```python
import torch

def _softmax(x, axis=-1):
    return torch.softmax(x, dim=axis)
def _gelu(x):
    return torch.nn.functional.gelu(x, approximate="tanh")
def _silu(x):
    return torch.nn.functional.silu(x)
def _sigmoid(x):
    return torch.sigmoid(x)
def _layernorm(x, axis=-1, eps=1e-5):
    m = x.mean(dim=axis, keepdim=True)
    v = x.var(dim=axis, keepdim=True, unbiased=False)
    return (x - m) / torch.sqrt(v + eps)
def _scatter_add(tgt, idx, src, axis=0):
    return tgt.clone().index_add_(axis, idx, src)
def _segment_sum(src, seg, n):
    out = torch.zeros((n,) + src.shape[1:], dtype=src.dtype)
    return out.index_add_(0, seg, src)
def _pad(x, pw):
    flat = tuple(v for pair in reversed(pw) for v in pair)
    return torch.nn.functional.pad(x, flat)

kernel_name = 'standalone__gqa_block_cached__near_ridge'

def setup_inputs():

    _w = (lambda a: (lambda t: t.to(torch.bfloat16) if t.is_floating_point() else t)(torch.as_tensor(a)))
    B, Sq, D = 16, 240, 640
    Hq, Hkv, Sk, Dh = 32, 32, 1024, 128
    assert Hq % Hkv == 0, (Hq, Hkv)
    return {
        "x": _w((lambda *s: torch.randn(*s, dtype=torch.bfloat16) * 0.8)(B, Sq, D)),
        "k_cache": _w((lambda *s: torch.randn(*s, dtype=torch.bfloat16) * 0.8)(B, Hkv, Sk, Dh)),
        "v_cache": _w((lambda *s: torch.randn(*s, dtype=torch.bfloat16) * 0.05)(B, Hkv, Sk, Dh)),
        "wq": _w((lambda *s: torch.randn(*s, dtype=torch.bfloat16) * 0.8)(D, Hq * Dh) * (D ** -0.5)),
        "wo": _w((lambda *s: torch.randn(*s, dtype=torch.bfloat16) * 0.05)(Hq * Dh, D) * ((Hq * Dh) ** -0.5)),
    }

def reference(x, k_cache, v_cache, wq, wo):
    B, Sq, D = x.shape
    Hq, Dh = 32, 128
    Hkv = k_cache.shape[1]
    rep = Hq // Hkv
    q = (lambda x, *d: x.permute(*d))((x @ wq).reshape(B, Sq, Hq, Dh), 0, 2, 1, 3)
    k = (lambda x, n, axis: torch.repeat_interleave(x, n, dim=axis))(k_cache, rep, axis=1)
    v = (lambda x, n, axis: torch.repeat_interleave(x, n, dim=axis))(v_cache, rep, axis=1)
    scores = torch.einsum("bhqd,bhkd->bhqk", q, k) * (Dh ** -0.5)
    p = _softmax(scores, axis=-1)
    o = torch.einsum("bhqk,bhkd->bhqd", p, v)
    return (lambda x, *d: x.permute(*d))(o, 0, 2, 1, 3).reshape(B, Sq, Hq * Dh) @ wo
```

```cpp
#include <hip/hip_runtime.h>
#include <hip/hip_bf16.h>

constexpr int LDS_ATT = 163840;

__global__ __launch_bounds__(256, 1) void qattn_kernel(const void* __restrict__ x, const void* __restrict__ kc, const void* __restrict__ vc,
                                                       const void* __restrict__ wq, void* __restrict__ ws, const void* __restrict__ wo,
                                                       void* __restrict__ out) {
  const unsigned blk = blockIdx.x;
  const unsigned wid = __builtin_amdgcn_readfirstlane(threadIdx.x >> 6);
  asm volatile(
R"A(s_mov_b32 s40,%6
s_mov_b32 s43,%5
s_and_b32 s44,s43,7
s_lshr_b32 s45,s43,3
s_lshr_b32 s41,s44,1
s_lshl_b32 s41,s41,2
s_lshr_b32 s46,s45,3
s_add_u32 s41,s41,s46
s_and_b32 s42,s44,1
s_lshl_b32 s42,s42,3
s_and_b32 s46,s45,7
s_add_u32 s42,s42,s46
s_lshr_b32 s70,s42,2
s_and_b32 s71,s42,3
s_lshl_b32 s44,s41,13
s_add_u32 s44,s44,104857600
s_mov_b64 s[64:65],%4
s_add_u32 s64,s64,s44
s_addc_u32 s65,s65,0
s_and_b32 s65,s65,0xffff
s_mov_b32 s66,8192
s_mov_b32 s67,0x20000
v_mov_b32 v14,4096
buffer_load_dword v13,v14,s[64:67],0 offen sc1
s_mul_i32 s44,s41,307200
s_mov_b64 s[28:29],%0
s_add_u32 s28,s28,s44
s_addc_u32 s29,s29,0
s_sub_u32 s28,s28,4096
s_subb_u32 s29,s29,0
s_and_b32 s29,s29,0xffff
s_mov_b32 s30,311296
s_mov_b32 s31,0x20000
s_mov_b64 s[32:33],%3
s_and_b32 s33,s33,0xffff
s_mov_b32 s34,5242880
s_mov_b32 s35,0x20000
s_lshl_b32 s56,s42,9
s_mov_b32 s57,0
s_lshl_b32 s59,s40,13
s_add_u32 s58,s59,65536
v_mbcnt_lo_u32_b32 v12,-1,0
v_mbcnt_hi_u32_b32 v12,-1,v12
s_getpc_b64 s[88:89]
L_pc_%=:
s_and_b32 s88,s88,0xffffff80
s_and_b32 s89,s89,0xffff
s_mov_b32 s90,L_fast_end_%=-L_pc_%=
s_mov_b32 s91,0x20000
s_lshl_b32 s44,s45,2
s_add_u32 s44,s44,s40
s_mul_i32 s44,s44,768
v_lshlrev_b32 v67,7,v12
s_mov_b64 exec,63
buffer_load_dword v19,v67,s[88:91],s44 offen
s_mov_b64 exec,-1
v_and_b32 v64,3,v12
v_bfe_u32 v65,v12,2,2
v_lshrrev_b32 v66,4,v12
v_lshrrev_b32 v75,3,v12
s_lshl_b32 s44,s40,6
v_add_u32 v75,s44,v75
v_mul_u32_u24 v75,1280,v75
v_and_b32 v76,7,v12
s_add_u32 s84,s70,1
s_and_b32 s84,s84,3
s_mul_i32 s84,s84,20480
s_add_u32 s84,s84,4096
s_add_u32 s85,s70,2
s_and_b32 s85,s85,3
s_mul_i32 s85,s85,20480
s_add_u32 s85,s85,2048
s_add_u32 s86,s70,3
s_and_b32 s86,s86,3
s_mul_i32 s86,s86,20480
s_add_u32 s86,s86,4096
s_add_u32 s87,s70,4
s_and_b32 s87,s87,3
s_mul_i32 s87,s87,20480
s_add_u32 s87,s87,2048
v_or_b32 v77,0,v66
v_xor_b32 v77,v77,v76
v_lshl_add_u32 v77,v77,4,v75
v_add_u32 v48,s84,v77
v_add_u32 v50,s85,v77
v_add_u32 v52,s86,v77
v_add_u32 v54,s87,v77
v_or_b32 v77,4,v66
v_xor_b32 v77,v77,v76
v_lshl_add_u32 v77,v77,4,v75
v_add_u32 v77,9216,v77
v_add_u32 v49,s84,v77
v_add_u32 v51,s85,v77
v_add_u32 v53,s86,v77
v_add_u32 v55,s87,v77
s_lshl_b32 s44,s40,17
v_lshlrev_b32 v75,13,v65
v_lshl_add_u32 v75,v66,6,v75
v_lshl_add_u32 v75,v64,4,v75
v_add_u32 v75,s44,v75
v_add_u32 v56,0,v75
v_add_u32 v57,256,v75
v_add_u32 v58,32768,v75
v_add_u32 v59,33024,v75
v_add_u32 v60,65536,v75
v_add_u32 v61,65792,v75
v_add_u32 v62,98304,v75
v_add_u32 v63,98560,v75
s_add_u32 m0,s59,0
s_nop 0
buffer_load_dwordx4 v48,s[28:31],s57 offen lds
buffer_load_dwordx4 v49,s[28:31],s57 offen offset:1024 lds
buffer_load_dwordx4 v50,s[28:31],s57 offen offset:2048 lds
buffer_load_dwordx4 v51,s[28:31],s57 offen offset:3072 lds
s_add_u32 m0,s59,4096
s_nop 0
buffer_load_dwordx4 v52,s[28:31],s57 offen lds
buffer_load_dwordx4 v53,s[28:31],s57 offen offset:1024 lds
buffer_load_dwordx4 v54,s[28:31],s57 offen offset:2048 lds
buffer_load_dwordx4 v55,s[28:31],s57 offen offset:3072 lds
s_add_u32 s57,s57,128
s_xor_b32 s59,s59,131072
s_add_u32 m0,s58,0
s_nop 0
buffer_load_dwordx4 v56,s[32:35],s56 offen lds
s_add_u32 m0,s58,1024
s_nop 0
buffer_load_dwordx4 v57,s[32:35],s56 offen lds
s_add_u32 m0,s58,2048
s_nop 0
buffer_load_dwordx4 v58,s[32:35],s56 offen lds
s_add_u32 m0,s58,3072
s_nop 0
buffer_load_dwordx4 v59,s[32:35],s56 offen lds
s_add_u32 m0,s58,4096
s_nop 0
buffer_load_dwordx4 v60,s[32:35],s56 offen lds
s_add_u32 m0,s58,5120
s_nop 0
buffer_load_dwordx4 v61,s[32:35],s56 offen lds
s_add_u32 m0,s58,6144
s_nop 0
buffer_load_dwordx4 v62,s[32:35],s56 offen lds
s_add_u32 m0,s58,7168
s_nop 0
buffer_load_dwordx4 v63,s[32:35],s56 offen lds
s_add_u32 s56,s56,524288
s_add_u32 s58,s58,32768
s_cmp_ge_u32 s58,131072
s_cselect_b32 s62,98304,0
s_sub_u32 s58,s58,s62
s_add_u32 m0,s58,0
s_nop 0
buffer_load_dwordx4 v56,s[32:35],s56 offen lds
s_add_u32 m0,s58,1024
s_nop 0
buffer_load_dwordx4 v57,s[32:35],s56 offen lds
s_add_u32 m0,s58,2048
s_nop 0
buffer_load_dwordx4 v58,s[32:35],s56 offen lds
s_add_u32 m0,s58,3072
s_nop 0
buffer_load_dwordx4 v59,s[32:35],s56 offen lds
s_add_u32 m0,s58,4096
s_nop 0
buffer_load_dwordx4 v60,s[32:35],s56 offen lds
s_add_u32 m0,s58,5120
s_nop 0
buffer_load_dwordx4 v61,s[32:35],s56 offen lds
s_add_u32 m0,s58,6144
s_nop 0
buffer_load_dwordx4 v62,s[32:35],s56 offen lds
s_add_u32 m0,s58,7168
s_nop 0
buffer_load_dwordx4 v63,s[32:35],s56 offen lds
s_add_u32 s56,s56,524288
s_add_u32 s58,s58,32768
s_cmp_ge_u32 s58,131072
s_cselect_b32 s62,98304,0
s_sub_u32 s58,s58,s62
s_add_u32 m0,s59,0
s_nop 0
buffer_load_dwordx4 v48,s[28:31],s57 offen lds
buffer_load_dwordx4 v49,s[28:31],s57 offen offset:1024 lds
buffer_load_dwordx4 v50,s[28:31],s57 offen offset:2048 lds
buffer_load_dwordx4 v51,s[28:31],s57 offen offset:3072 lds
s_add_u32 m0,s59,4096
s_nop 0
buffer_load_dwordx4 v52,s[28:31],s57 offen lds
buffer_load_dwordx4 v53,s[28:31],s57 offen offset:1024 lds
buffer_load_dwordx4 v54,s[28:31],s57 offen offset:2048 lds
buffer_load_dwordx4 v55,s[28:31],s57 offen offset:3072 lds
s_add_u32 s57,s57,128
s_xor_b32 s59,s59,131072
s_lshl_b32 s43,s41,5
s_lshl_b32 s44,s42,1
s_add_u32 s43,s43,s44
s_lshl_b32 s44,s43,18
s_mov_b64 s[20:21],%1
s_add_u32 s20,s20,s44
s_addc_u32 s21,s21,0
s_and_b32 s21,s21,0xffff
s_mov_b32 s22,524288
s_mov_b32 s23,0x20000
s_mov_b64 s[24:25],%2
s_add_u32 s24,s24,s44
s_addc_u32 s25,s25,0
s_and_b32 s25,s25,0xffff
s_mov_b32 s26,524288
s_mov_b32 s27,0x20000
s_lshl_b32 s44,s43,16
s_mov_b64 s[36:37],%4
s_add_u32 s36,s36,s44
s_addc_u32 s37,s37,0
s_and_b32 s37,s37,0xffff
s_mov_b32 s38,131072
s_mov_b32 s39,0x20000
s_lshl_b32 s51,s40,12
s_add_u32 s52,s51,16384
s_mul_i32 s47,s41,5
s_add_u32 s47,s47,s42
s_and_b32 s47,s47,15
s_lshl_b32 s47,s47,14
s_mov_b32 s54,0
s_mov_b32 s50,98304
s_lshl_b32 s55,s40,14
s_mov_b32 s60,65536
s_mov_b32 s61,0x3e0293ee
s_mov_b32 s63,0x5d800000
v_lshlrev_b32 v6,4,v12
s_lshl_b32 s44,s40,2
s_add_u32 s44,s44,s70
s_mov_b32 s46,0xff800000
s_cmp_eq_u32 s44,14
s_cselect_b32 s18,0x7149f2ca,s46
s_cmp_eq_u32 s44,13
s_cselect_b32 s19,0x7149f2ca,s46
s_cmp_eq_u32 s44,12
s_cselect_b32 s100,0x7149f2ca,s46
s_cmp_eq_u32 s44,15
s_cselect_b32 s101,0x7149f2ca,s46
v_lshlrev_b32 v70,8,v65
v_lshl_add_u32 v70,v66,6,v70
v_add_u32 v70,s51,v70
v_lshl_add_u32 v4,v64,4,v70
v_xor_b32 v71,2,v64
v_lshl_add_u32 v5,v71,4,v70
v_lshlrev_b32 v72,10,v65
v_lshl_add_u32 v72,v64,6,v72
v_and_b32 v73,2,v65
v_xor_b32 v73,v73,v66
v_lshl_add_u32 v72,v73,4,v72
v_add_u32 v1,98304,v72
v_lshlrev_b32 v72,10,v66
v_lshl_add_u32 v72,v65,6,v72
v_lshl_add_u32 v72,v64,3,v72
v_add_u32 v72,114688,v72
v_and_b32 v73,1,v66
v_lshl_add_u32 v2,v73,5,v72
v_xor_b32 v73,1,v73
v_lshl_add_u32 v3,v73,5,v72
v_lshlrev_b32 v72,12,v66
v_lshl_add_u32 v72,v65,6,v72
v_lshl_add_u32 v23,v64,4,v72
v_add_u32 v22,65536,v23
v_and_b32 v74,15,v12
v_lshrrev_b32 v76,1,v74
v_lshlrev_b32 v75,7,v74
s_lshl_b32 s44,s40,13
v_add_u32 v75,s44,v75
v_or_b32 v77,0,v66
v_xor_b32 v77,v77,v76
v_lshl_add_u32 v20,v77,4,v75
v_or_b32 v77,4,v66
v_xor_b32 v77,v77,v76
v_lshl_add_u32 v21,v77,4,v75
v_mov_b32 v28,0x3f803f80
v_mov_b32 v29,0x3f803f80
v_mov_b32 v30,0x3f803f80
v_mov_b32 v31,0x3f803f80
s_mov_b64 s[72:73],%7
s_and_b32 s73,s73,0xffff
s_mov_b32 s74,5242880
s_mov_b32 s75,0x20000
s_mul_i32 s81,s70,1310720
s_mul_i32 s44,s71,320
s_add_u32 s81,s81,s44
s_mul_i32 s82,s40,10240
v_lshlrev_b32 v15,3,v6
s_lshl_b32 s62,%5,2
s_add_u32 s62,s62,s40
s_mul_i32 s62,s62,5120
s_mov_b32 exec_hi,0xff
buffer_load_dword v16,v15,s[72:75],s62 offen
s_mov_b32 exec_hi,-1
s_mul_i32 s44,s40,40960
v_add_u32 v70,0,v12
v_mul_u32_u24 v71,3277,v70
v_lshrrev_b32 v71,16,v71
v_mul_u32_u24 v72,20,v71
v_sub_u32 v72,v70,v72
v_bfe_u32 v73,v71,2,1
v_lshlrev_b32 v73,1,v73
v_xor_b32 v72,v72,v73
v_mul_u32_u24 v71,1280,v71
v_lshl_add_u32 v71,v72,4,v71
v_add_u32 v71,s44,v71
v_subrev_u32 v7,0,v71
v_add_u32 v70,64,v12
v_mul_u32_u24 v71,3277,v70
v_lshrrev_b32 v71,16,v71
v_mul_u32_u24 v72,20,v71
v_sub_u32 v72,v70,v72
v_bfe_u32 v73,v71,2,1
v_lshlrev_b32 v73,1,v73
v_xor_b32 v72,v72,v73
v_mul_u32_u24 v71,1280,v71
v_lshl_add_u32 v71,v72,4,v71
v_add_u32 v71,s44,v71
v_subrev_u32 v8,1024,v71
v_add_u32 v70,128,v12
v_mul_u32_u24 v71,3277,v70
v_lshrrev_b32 v71,16,v71
v_mul_u32_u24 v72,20,v71
v_sub_u32 v72,v70,v72
v_bfe_u32 v73,v71,2,1
v_lshlrev_b32 v73,1,v73
v_xor_b32 v72,v72,v73
v_mul_u32_u24 v71,1280,v71
v_lshl_add_u32 v71,v72,4,v71
v_add_u32 v71,s44,v71
v_subrev_u32 v9,2048,v71
v_add_u32 v70,192,v12
v_mul_u32_u24 v71,3277,v70
v_lshrrev_b32 v71,16,v71
v_mul_u32_u24 v72,20,v71
v_sub_u32 v72,v70,v72
v_bfe_u32 v73,v71,2,1
v_lshlrev_b32 v73,1,v73
v_xor_b32 v72,v72,v73
v_mul_u32_u24 v71,1280,v71
v_lshl_add_u32 v71,v72,4,v71
v_add_u32 v71,s44,v71
v_subrev_u32 v10,3072,v71
v_add_u32 v70,256,v12
v_mul_u32_u24 v71,3277,v70
v_lshrrev_b32 v71,16,v71
v_mul_u32_u24 v72,20,v71
v_sub_u32 v72,v70,v72
v_bfe_u32 v73,v71,2,1
v_lshlrev_b32 v73,1,v73
v_xor_b32 v72,v72,v73
v_mul_u32_u24 v71,1280,v71
v_lshl_add_u32 v71,v72,4,v71
v_add_u32 v71,s44,v71
v_subrev_u32 v11,0,v71
v_mov_b32 v128,0
v_accvgpr_write_b32 a0,0
v_mov_b32 v129,0
v_accvgpr_write_b32 a1,0
v_mov_b32 v130,0
v_accvgpr_write_b32 a2,0
v_mov_b32 v131,0
v_accvgpr_write_b32 a3,0
v_mov_b32 v132,0
v_accvgpr_write_b32 a4,0
v_mov_b32 v133,0
v_accvgpr_write_b32 a5,0
v_mov_b32 v134,0
v_accvgpr_write_b32 a6,0
v_mov_b32 v135,0
v_accvgpr_write_b32 a7,0
v_mov_b32 v136,0
v_accvgpr_write_b32 a8,0
v_mov_b32 v137,0
v_accvgpr_write_b32 a9,0
v_mov_b32 v138,0
v_accvgpr_write_b32 a10,0
v_mov_b32 v139,0
v_accvgpr_write_b32 a11,0
v_mov_b32 v140,0
v_accvgpr_write_b32 a12,0
v_mov_b32 v141,0
v_accvgpr_write_b32 a13,0
v_mov_b32 v142,0
v_accvgpr_write_b32 a14,0
v_mov_b32 v143,0
v_accvgpr_write_b32 a15,0
v_mov_b32 v144,0
v_accvgpr_write_b32 a16,0
v_mov_b32 v145,0
v_accvgpr_write_b32 a17,0
v_mov_b32 v146,0
v_accvgpr_write_b32 a18,0
v_mov_b32 v147,0
v_accvgpr_write_b32 a19,0
v_mov_b32 v148,0
v_accvgpr_write_b32 a20,0
v_mov_b32 v149,0
v_accvgpr_write_b32 a21,0
v_mov_b32 v150,0
v_accvgpr_write_b32 a22,0
v_mov_b32 v151,0
v_accvgpr_write_b32 a23,0
v_mov_b32 v152,0
v_accvgpr_write_b32 a24,0
v_mov_b32 v153,0
v_accvgpr_write_b32 a25,0
v_mov_b32 v154,0
v_accvgpr_write_b32 a26,0
v_mov_b32 v155,0
v_accvgpr_write_b32 a27,0
v_mov_b32 v156,0
v_accvgpr_write_b32 a28,0
v_mov_b32 v157,0
v_accvgpr_write_b32 a29,0
v_mov_b32 v158,0
v_accvgpr_write_b32 a30,0
v_mov_b32 v159,0
v_accvgpr_write_b32 a31,0
v_mov_b32 v160,0
v_accvgpr_write_b32 a32,0
v_mov_b32 v161,0
v_accvgpr_write_b32 a33,0
v_mov_b32 v162,0
v_accvgpr_write_b32 a34,0
v_mov_b32 v163,0
v_accvgpr_write_b32 a35,0
v_mov_b32 v164,0
v_accvgpr_write_b32 a36,0
v_mov_b32 v165,0
v_accvgpr_write_b32 a37,0
v_mov_b32 v166,0
v_accvgpr_write_b32 a38,0
v_mov_b32 v167,0
v_accvgpr_write_b32 a39,0
v_mov_b32 v168,0
v_accvgpr_write_b32 a40,0
v_mov_b32 v169,0
v_accvgpr_write_b32 a41,0
v_mov_b32 v170,0
v_accvgpr_write_b32 a42,0
v_mov_b32 v171,0
v_accvgpr_write_b32 a43,0
v_mov_b32 v172,0
v_accvgpr_write_b32 a44,0
v_mov_b32 v173,0
v_accvgpr_write_b32 a45,0
v_mov_b32 v174,0
v_accvgpr_write_b32 a46,0
v_mov_b32 v175,0
v_accvgpr_write_b32 a47,0
v_mov_b32 v176,0
v_accvgpr_write_b32 a48,0
v_mov_b32 v177,0
v_accvgpr_write_b32 a49,0
v_mov_b32 v178,0
v_accvgpr_write_b32 a50,0
v_mov_b32 v179,0
v_accvgpr_write_b32 a51,0
v_mov_b32 v180,0
v_accvgpr_write_b32 a52,0
v_mov_b32 v181,0
v_accvgpr_write_b32 a53,0
v_mov_b32 v182,0
v_accvgpr_write_b32 a54,0
v_mov_b32 v183,0
v_accvgpr_write_b32 a55,0
v_mov_b32 v184,0
v_accvgpr_write_b32 a56,0
v_mov_b32 v185,0
v_accvgpr_write_b32 a57,0
v_mov_b32 v186,0
v_accvgpr_write_b32 a58,0
v_mov_b32 v187,0
v_accvgpr_write_b32 a59,0
v_mov_b32 v188,0
v_accvgpr_write_b32 a60,0
v_mov_b32 v189,0
v_accvgpr_write_b32 a61,0
v_mov_b32 v190,0
v_accvgpr_write_b32 a62,0
v_mov_b32 v191,0
v_accvgpr_write_b32 a63,0
v_mov_b32 v192,0
v_accvgpr_write_b32 a64,0
v_mov_b32 v193,0
v_accvgpr_write_b32 a65,0
v_mov_b32 v194,0
v_accvgpr_write_b32 a66,0
v_mov_b32 v195,0
v_accvgpr_write_b32 a67,0
v_mov_b32 v196,0
v_accvgpr_write_b32 a68,0
v_mov_b32 v197,0
v_accvgpr_write_b32 a69,0
v_mov_b32 v198,0
v_accvgpr_write_b32 a70,0
v_mov_b32 v199,0
v_accvgpr_write_b32 a71,0
v_mov_b32 v200,0
)A"
R"A(v_accvgpr_write_b32 a72,0
v_mov_b32 v201,0
v_accvgpr_write_b32 a73,0
v_mov_b32 v202,0
v_accvgpr_write_b32 a74,0
v_mov_b32 v203,0
v_accvgpr_write_b32 a75,0
v_mov_b32 v204,0
v_accvgpr_write_b32 a76,0
v_mov_b32 v205,0
v_accvgpr_write_b32 a77,0
v_mov_b32 v206,0
v_accvgpr_write_b32 a78,0
v_mov_b32 v207,0
v_accvgpr_write_b32 a79,0
v_mov_b32 v208,0
v_accvgpr_write_b32 a80,0
v_mov_b32 v209,0
v_accvgpr_write_b32 a81,0
v_mov_b32 v210,0
v_accvgpr_write_b32 a82,0
v_mov_b32 v211,0
v_accvgpr_write_b32 a83,0
v_mov_b32 v212,0
v_accvgpr_write_b32 a84,0
v_mov_b32 v213,0
v_accvgpr_write_b32 a85,0
v_mov_b32 v214,0
v_accvgpr_write_b32 a86,0
v_mov_b32 v215,0
v_accvgpr_write_b32 a87,0
v_mov_b32 v216,0
v_accvgpr_write_b32 a88,0
v_mov_b32 v217,0
v_accvgpr_write_b32 a89,0
v_mov_b32 v218,0
v_accvgpr_write_b32 a90,0
v_mov_b32 v219,0
v_accvgpr_write_b32 a91,0
v_mov_b32 v220,0
v_accvgpr_write_b32 a92,0
v_mov_b32 v221,0
v_accvgpr_write_b32 a93,0
v_mov_b32 v222,0
v_accvgpr_write_b32 a94,0
v_mov_b32 v223,0
v_accvgpr_write_b32 a95,0
v_mov_b32 v224,0
v_accvgpr_write_b32 a96,0
v_mov_b32 v225,0
v_accvgpr_write_b32 a97,0
v_mov_b32 v226,0
v_accvgpr_write_b32 a98,0
v_mov_b32 v227,0
v_accvgpr_write_b32 a99,0
v_mov_b32 v228,0
v_accvgpr_write_b32 a100,0
v_mov_b32 v229,0
v_accvgpr_write_b32 a101,0
v_mov_b32 v230,0
v_accvgpr_write_b32 a102,0
v_mov_b32 v231,0
v_accvgpr_write_b32 a103,0
v_mov_b32 v232,0
v_accvgpr_write_b32 a104,0
v_mov_b32 v233,0
v_accvgpr_write_b32 a105,0
v_mov_b32 v234,0
v_accvgpr_write_b32 a106,0
v_mov_b32 v235,0
v_accvgpr_write_b32 a107,0
v_mov_b32 v236,0
v_accvgpr_write_b32 a108,0
v_mov_b32 v237,0
v_accvgpr_write_b32 a109,0
v_mov_b32 v238,0
v_accvgpr_write_b32 a110,0
v_mov_b32 v239,0
v_accvgpr_write_b32 a111,0
v_mov_b32 v240,0
v_accvgpr_write_b32 a112,0
v_mov_b32 v241,0
v_accvgpr_write_b32 a113,0
v_mov_b32 v242,0
v_accvgpr_write_b32 a114,0
v_mov_b32 v243,0
v_accvgpr_write_b32 a115,0
v_mov_b32 v244,0
v_accvgpr_write_b32 a116,0
v_mov_b32 v245,0
v_accvgpr_write_b32 a117,0
v_mov_b32 v246,0
v_accvgpr_write_b32 a118,0
v_mov_b32 v247,0
v_accvgpr_write_b32 a119,0
v_mov_b32 v248,0
v_accvgpr_write_b32 a120,0
v_mov_b32 v249,0
v_accvgpr_write_b32 a121,0
v_mov_b32 v250,0
v_accvgpr_write_b32 a122,0
v_mov_b32 v251,0
v_accvgpr_write_b32 a123,0
v_mov_b32 v252,0
v_accvgpr_write_b32 a124,0
v_mov_b32 v253,0
v_accvgpr_write_b32 a125,0
v_mov_b32 v254,0
v_accvgpr_write_b32 a126,0
v_mov_b32 v255,0
v_accvgpr_write_b32 a127,0
s_waitcnt vmcnt(17)
s_barrier
v_readfirstlane_b32 s68,v13
s_nop 3
s_add_u32 s68,s68,1
s_xor_b32 s69,s68,0x9e3779b9
ds_read_b128 a[192:195],v20
ds_read_b128 a[196:199],v21
ds_read_b128 a[200:203],v20 offset:2048
ds_read_b128 a[204:207],v21 offset:2048
ds_read_b128 a[208:211],v20 offset:4096
ds_read_b128 a[212:215],v21 offset:4096
ds_read_b128 a[216:219],v20 offset:6144
ds_read_b128 a[220:223],v21 offset:6144
ds_read_b64_tr_b16 a[128:129],v22 offset:1024
ds_read_b64_tr_b16 a[130:131],v22 offset:3072
ds_read_b64_tr_b16 a[132:133],v22 offset:17408
ds_read_b64_tr_b16 a[134:135],v22 offset:19456
ds_read_b64_tr_b16 a[136:137],v22 offset:1032
ds_read_b64_tr_b16 a[138:139],v22 offset:3080
ds_read_b64_tr_b16 a[140:141],v22 offset:17416
ds_read_b64_tr_b16 a[142:143],v22 offset:19464
ds_read_b64_tr_b16 a[144:145],v22 offset:1280
ds_read_b64_tr_b16 a[146:147],v22 offset:3328
ds_read_b64_tr_b16 a[148:149],v22 offset:17664
ds_read_b64_tr_b16 a[150:151],v22 offset:19712
ds_read_b64_tr_b16 a[152:153],v22 offset:1288
ds_read_b64_tr_b16 a[154:155],v22 offset:3336
ds_read_b64_tr_b16 a[156:157],v22 offset:17672
ds_read_b64_tr_b16 a[158:159],v22 offset:19720
s_mov_b32 s53,4
L_qp_%=:
s_waitcnt lgkmcnt(0)
v_mfma_f32_16x16x32_bf16 v[128:131],a[128:131],a[192:195],v[128:131]
s_add_u32 m0,s58,0
s_add_u32 s60,s60,32768
ds_read_b64_tr_b16 a[160:161],v22 offset:1536
v_mfma_f32_16x16x32_bf16 v[128:131],a[132:135],a[196:199],v[128:131]
s_cmp_eq_u32 s60,131072
s_cselect_b32 s60,32768,s60
buffer_load_dwordx4 v56,s[32:35],s56 offen lds
v_mfma_f32_16x16x32_bf16 v[136:139],a[132:135],a[204:207],v[136:139]
ds_read_b64_tr_b16 a[162:163],v22 offset:3584
v_xor_b32 v20,131072,v20
v_mfma_f32_16x16x32_bf16 v[136:139],a[128:131],a[200:203],v[136:139]
s_add_u32 m0,s58,1024
v_xor_b32 v21,131072,v21
v_mfma_f32_16x16x32_bf16 v[144:147],a[128:131],a[208:211],v[144:147]
ds_read_b64_tr_b16 a[164:165],v22 offset:17920
v_mfma_f32_16x16x32_bf16 v[144:147],a[132:135],a[212:215],v[144:147]
v_mfma_f32_16x16x32_bf16 v[152:155],a[132:135],a[220:223],v[152:155]
buffer_load_dwordx4 v57,s[32:35],s56 offen lds
ds_read_b64_tr_b16 a[166:167],v22 offset:19968
v_mfma_f32_16x16x32_bf16 v[152:155],a[128:131],a[216:219],v[152:155]
v_mfma_f32_16x16x32_bf16 v[132:135],a[136:139],a[192:195],v[132:135]
ds_read_b64_tr_b16 a[168:169],v22 offset:1544
v_mfma_f32_16x16x32_bf16 v[132:135],a[140:143],a[196:199],v[132:135]
s_add_u32 m0,s58,2048
v_mfma_f32_16x16x32_bf16 v[140:143],a[140:143],a[204:207],v[140:143]
ds_read_b64_tr_b16 a[170:171],v22 offset:3592
v_mfma_f32_16x16x32_bf16 v[140:143],a[136:139],a[200:203],v[140:143]
v_mfma_f32_16x16x32_bf16 v[148:151],a[136:139],a[208:211],v[148:151]
buffer_load_dwordx4 v58,s[32:35],s56 offen lds
ds_read_b64_tr_b16 a[172:173],v22 offset:17928
v_mfma_f32_16x16x32_bf16 v[148:151],a[140:143],a[212:215],v[148:151]
v_mfma_f32_16x16x32_bf16 v[156:159],a[140:143],a[220:223],v[156:159]
s_add_u32 m0,s58,3072
ds_read_b64_tr_b16 a[174:175],v22 offset:19976
v_mfma_f32_16x16x32_bf16 v[156:159],a[136:139],a[216:219],v[156:159]
v_mfma_f32_16x16x32_bf16 v[160:163],a[144:147],a[192:195],v[160:163]
ds_read_b64_tr_b16 a[176:177],v22 offset:1792
v_mfma_f32_16x16x32_bf16 v[160:163],a[148:151],a[196:199],v[160:163]
buffer_load_dwordx4 v59,s[32:35],s56 offen lds
v_mfma_f32_16x16x32_bf16 v[168:171],a[148:151],a[204:207],v[168:171]
ds_read_b64_tr_b16 a[178:179],v22 offset:3840
v_mfma_f32_16x16x32_bf16 v[168:171],a[144:147],a[200:203],v[168:171]
v_mfma_f32_16x16x32_bf16 v[176:179],a[144:147],a[208:211],v[176:179]
s_add_u32 m0,s58,4096
ds_read_b64_tr_b16 a[180:181],v22 offset:18176
v_mfma_f32_16x16x32_bf16 v[176:179],a[148:151],a[212:215],v[176:179]
v_mfma_f32_16x16x32_bf16 v[184:187],a[148:151],a[220:223],v[184:187]
ds_read_b64_tr_b16 a[182:183],v22 offset:20224
v_mfma_f32_16x16x32_bf16 v[184:187],a[144:147],a[216:219],v[184:187]
buffer_load_dwordx4 v60,s[32:35],s56 offen lds
v_mfma_f32_16x16x32_bf16 v[164:167],a[152:155],a[192:195],v[164:167]
ds_read_b64_tr_b16 a[184:185],v22 offset:1800
v_mfma_f32_16x16x32_bf16 v[164:167],a[156:159],a[196:199],v[164:167]
s_add_u32 m0,s58,5120
v_mfma_f32_16x16x32_bf16 v[172:175],a[156:159],a[204:207],v[172:175]
ds_read_b64_tr_b16 a[186:187],v22 offset:3848
v_mfma_f32_16x16x32_bf16 v[172:175],a[152:155],a[200:203],v[172:175]
v_mfma_f32_16x16x32_bf16 v[180:183],a[152:155],a[208:211],v[180:183]
buffer_load_dwordx4 v61,s[32:35],s56 offen lds
ds_read_b64_tr_b16 a[188:189],v22 offset:18184
v_mfma_f32_16x16x32_bf16 v[180:183],a[156:159],a[212:215],v[180:183]
v_mfma_f32_16x16x32_bf16 v[188:191],a[156:159],a[220:223],v[188:191]
ds_read_b64_tr_b16 a[190:191],v22 offset:20232
v_mfma_f32_16x16x32_bf16 v[188:191],a[152:155],a[216:219],v[188:191]
s_add_u32 m0,s58,6144
s_waitcnt lgkmcnt(4)
v_mfma_f32_16x16x32_bf16 v[192:195],a[160:163],a[192:195],v[192:195]
ds_read_b64_tr_b16 a[128:129],v22
v_mfma_f32_16x16x32_bf16 v[192:195],a[164:167],a[196:199],v[192:195]
v_mfma_f32_16x16x32_bf16 v[200:203],a[164:167],a[204:207],v[200:203]
buffer_load_dwordx4 v62,s[32:35],s56 offen lds
ds_read_b64_tr_b16 a[130:131],v22 offset:2048
v_mfma_f32_16x16x32_bf16 v[200:203],a[160:163],a[200:203],v[200:203]
v_mfma_f32_16x16x32_bf16 v[208:211],a[160:163],a[208:211],v[208:211]
s_add_u32 m0,s58,7168
s_add_u32 s58,s58,32768
ds_read_b64_tr_b16 a[132:133],v22 offset:16384
v_mfma_f32_16x16x32_bf16 v[208:211],a[164:167],a[212:215],v[208:211]
s_cmp_ge_u32 s58,131072
s_cselect_b32 s62,98304,0
s_sub_u32 s58,s58,s62
v_mfma_f32_16x16x32_bf16 v[216:219],a[164:167],a[220:223],v[216:219]
ds_read_b64_tr_b16 a[134:135],v22 offset:18432
v_mfma_f32_16x16x32_bf16 v[216:219],a[160:163],a[216:219],v[216:219]
buffer_load_dwordx4 v63,s[32:35],s56 offen lds
v_mfma_f32_16x16x32_bf16 v[196:199],a[168:171],a[192:195],v[196:199]
s_add_u32 s56,s56,524288
ds_read_b64_tr_b16 a[136:137],v22 offset:8
v_mfma_f32_16x16x32_bf16 v[196:199],a[172:175],a[196:199],v[196:199]
v_mfma_f32_16x16x32_bf16 v[204:207],a[172:175],a[204:207],v[204:207]
s_add_u32 m0,s59,0
ds_read_b64_tr_b16 a[138:139],v22 offset:2056
v_mfma_f32_16x16x32_bf16 v[204:207],a[168:171],a[200:203],v[204:207]
v_mfma_f32_16x16x32_bf16 v[212:215],a[168:171],a[208:211],v[212:215]
ds_read_b64_tr_b16 a[140:141],v22 offset:16392
v_mfma_f32_16x16x32_bf16 v[212:215],a[172:175],a[212:215],v[212:215]
buffer_load_dwordx4 v48,s[28:31],s57 offen lds
v_mfma_f32_16x16x32_bf16 v[220:223],a[172:175],a[220:223],v[220:223]
ds_read_b64_tr_b16 a[142:143],v22 offset:18440
v_mfma_f32_16x16x32_bf16 v[220:223],a[168:171],a[216:219],v[220:223]
v_mfma_f32_16x16x32_bf16 v[224:227],a[176:179],a[192:195],v[224:227]
ds_read_b64_tr_b16 a[144:145],v22 offset:256
v_mfma_f32_16x16x32_bf16 v[224:227],a[180:183],a[196:199],v[224:227]
v_mfma_f32_16x16x32_bf16 v[232:235],a[180:183],a[204:207],v[232:235]
buffer_load_dwordx4 v49,s[28:31],s57 offen offset:1024 lds
ds_read_b64_tr_b16 a[146:147],v22 offset:2304
v_mfma_f32_16x16x32_bf16 v[232:235],a[176:179],a[200:203],v[232:235]
v_mfma_f32_16x16x32_bf16 v[240:243],a[176:179],a[208:211],v[240:243]
ds_read_b64_tr_b16 a[148:149],v22 offset:16640
v_mfma_f32_16x16x32_bf16 v[240:243],a[180:183],a[212:215],v[240:243]
v_mfma_f32_16x16x32_bf16 v[248:251],a[180:183],a[220:223],v[248:251]
ds_read_b64_tr_b16 a[150:151],v22 offset:18688
v_mfma_f32_16x16x32_bf16 v[248:251],a[176:179],a[216:219],v[248:251]
s_waitcnt lgkmcnt(4)
v_mfma_f32_16x16x32_bf16 v[228:231],a[184:187],a[192:195],v[228:231]
buffer_load_dwordx4 v50,s[28:31],s57 offen offset:2048 lds
ds_read_b64_tr_b16 a[152:153],v22 offset:264
v_mfma_f32_16x16x32_bf16 v[228:231],a[188:191],a[196:199],v[228:231]
v_mfma_f32_16x16x32_bf16 v[236:239],a[188:191],a[204:207],v[236:239]
ds_read_b64_tr_b16 a[154:155],v22 offset:2312
v_mfma_f32_16x16x32_bf16 v[236:239],a[184:187],a[200:203],v[236:239]
v_mfma_f32_16x16x32_bf16 v[244:247],a[184:187],a[208:211],v[244:247]
ds_read_b64_tr_b16 a[156:157],v22 offset:16648
v_mfma_f32_16x16x32_bf16 v[244:247],a[188:191],a[212:215],v[244:247]
buffer_load_dwordx4 v51,s[28:31],s57 offen offset:3072 lds
v_mfma_f32_16x16x32_bf16 v[252:255],a[188:191],a[220:223],v[252:255]
ds_read_b64_tr_b16 a[158:159],v22 offset:18696
v_mfma_f32_16x16x32_bf16 v[252:255],a[184:187],a[216:219],v[252:255]
v_mfma_f32_16x16x32_bf16 a[0:3],a[128:131],a[192:195],a[0:3]
s_add_u32 m0,s59,4096
s_xor_b32 s59,s59,131072
ds_read_b64_tr_b16 a[160:161],v22 offset:512
v_mfma_f32_16x16x32_bf16 a[0:3],a[132:135],a[196:199],a[0:3]
v_mfma_f32_16x16x32_bf16 a[32:35],a[132:135],a[204:207],a[32:35]
ds_read_b64_tr_b16 a[162:163],v22 offset:2560
v_mfma_f32_16x16x32_bf16 a[32:35],a[128:131],a[200:203],a[32:35]
buffer_load_dwordx4 v52,s[28:31],s57 offen lds
v_mfma_f32_16x16x32_bf16 a[64:67],a[128:131],a[208:211],a[64:67]
ds_read_b64_tr_b16 a[164:165],v22 offset:16896
v_mfma_f32_16x16x32_bf16 a[64:67],a[132:135],a[212:215],a[64:67]
v_mfma_f32_16x16x32_bf16 a[96:99],a[132:135],a[220:223],a[96:99]
ds_read_b64_tr_b16 a[166:167],v22 offset:18944
v_mfma_f32_16x16x32_bf16 a[96:99],a[128:131],a[216:219],a[96:99]
v_mfma_f32_16x16x32_bf16 a[4:7],a[136:139],a[192:195],a[4:7]
buffer_load_dwordx4 v53,s[28:31],s57 offen offset:1024 lds
ds_read_b64_tr_b16 a[168:169],v22 offset:520
v_mfma_f32_16x16x32_bf16 a[4:7],a[140:143],a[196:199],a[4:7]
v_mfma_f32_16x16x32_bf16 a[36:39],a[140:143],a[204:207],a[36:39]
ds_read_b64_tr_b16 a[170:171],v22 offset:2568
v_mfma_f32_16x16x32_bf16 a[36:39],a[136:139],a[200:203],a[36:39]
v_mfma_f32_16x16x32_bf16 a[68:71],a[136:139],a[208:211],a[68:71]
ds_read_b64_tr_b16 a[172:173],v22 offset:16904
v_mfma_f32_16x16x32_bf16 a[68:71],a[140:143],a[212:215],a[68:71]
v_mfma_f32_16x16x32_bf16 a[100:103],a[140:143],a[220:223],a[100:103]
)A"
R"A(buffer_load_dwordx4 v54,s[28:31],s57 offen offset:2048 lds
ds_read_b64_tr_b16 a[174:175],v22 offset:18952
v_mfma_f32_16x16x32_bf16 a[100:103],a[136:139],a[216:219],a[100:103]
s_waitcnt lgkmcnt(4)
v_mfma_f32_16x16x32_bf16 a[8:11],a[144:147],a[192:195],a[8:11]
ds_read_b64_tr_b16 a[176:177],v22 offset:768
v_mfma_f32_16x16x32_bf16 a[8:11],a[148:151],a[196:199],a[8:11]
v_mfma_f32_16x16x32_bf16 a[40:43],a[148:151],a[204:207],a[40:43]
ds_read_b64_tr_b16 a[178:179],v22 offset:2816
v_mfma_f32_16x16x32_bf16 a[40:43],a[144:147],a[200:203],a[40:43]
buffer_load_dwordx4 v55,s[28:31],s57 offen offset:3072 lds
v_mfma_f32_16x16x32_bf16 a[72:75],a[144:147],a[208:211],a[72:75]
s_add_u32 s57,s57,128
ds_read_b64_tr_b16 a[180:181],v22 offset:17152
v_mfma_f32_16x16x32_bf16 a[72:75],a[148:151],a[212:215],a[72:75]
v_mfma_f32_16x16x32_bf16 a[104:107],a[148:151],a[220:223],a[104:107]
ds_read_b64_tr_b16 a[182:183],v22 offset:19200
v_mfma_f32_16x16x32_bf16 a[104:107],a[144:147],a[216:219],a[104:107]
v_mfma_f32_16x16x32_bf16 a[12:15],a[152:155],a[192:195],a[12:15]
ds_read_b64_tr_b16 a[184:185],v22 offset:776
ds_read_b64_tr_b16 a[186:187],v22 offset:2824
ds_read_b64_tr_b16 a[188:189],v22 offset:17160
ds_read_b64_tr_b16 a[190:191],v22 offset:19208
v_mfma_f32_16x16x32_bf16 a[12:15],a[156:159],a[196:199],a[12:15]
v_mfma_f32_16x16x32_bf16 a[44:47],a[156:159],a[204:207],a[44:47]
v_mfma_f32_16x16x32_bf16 a[44:47],a[152:155],a[200:203],a[44:47]
v_mfma_f32_16x16x32_bf16 a[76:79],a[152:155],a[208:211],a[76:79]
v_mfma_f32_16x16x32_bf16 a[76:79],a[156:159],a[212:215],a[76:79]
v_add_u32 v22,s60,v23
v_mfma_f32_16x16x32_bf16 a[108:111],a[156:159],a[220:223],a[108:111]
s_add_u32 s60,s60,32768
s_cmp_eq_u32 s60,131072
s_cselect_b32 s60,32768,s60
v_mfma_f32_16x16x32_bf16 a[108:111],a[152:155],a[216:219],a[108:111]
s_waitcnt vmcnt(16) lgkmcnt(0)
s_barrier
v_mfma_f32_16x16x32_bf16 a[16:19],a[160:163],a[192:195],a[16:19]
ds_read_b128 a[224:227],v20
v_mfma_f32_16x16x32_bf16 a[16:19],a[164:167],a[196:199],a[16:19]
ds_read_b64_tr_b16 a[128:129],v22 offset:1024
v_mfma_f32_16x16x32_bf16 a[48:51],a[164:167],a[204:207],a[48:51]
ds_read_b64_tr_b16 a[130:131],v22 offset:3072
v_mfma_f32_16x16x32_bf16 a[48:51],a[160:163],a[200:203],a[48:51]
ds_read_b128 a[228:231],v21
v_mfma_f32_16x16x32_bf16 a[80:83],a[160:163],a[208:211],a[80:83]
ds_read_b128 a[232:235],v20 offset:2048
v_mfma_f32_16x16x32_bf16 a[80:83],a[164:167],a[212:215],a[80:83]
ds_read_b128 a[236:239],v21 offset:2048
v_mfma_f32_16x16x32_bf16 a[112:115],a[164:167],a[220:223],a[112:115]
ds_read_b128 a[240:243],v20 offset:4096
v_mfma_f32_16x16x32_bf16 a[112:115],a[160:163],a[216:219],a[112:115]
ds_read_b128 a[244:247],v21 offset:4096
v_mfma_f32_16x16x32_bf16 a[20:23],a[168:171],a[192:195],a[20:23]
ds_read_b128 a[248:251],v20 offset:6144
v_xor_b32 v20,131072,v20
v_mfma_f32_16x16x32_bf16 a[20:23],a[172:175],a[196:199],a[20:23]
ds_read_b128 a[252:255],v21 offset:6144
v_xor_b32 v21,131072,v21
v_mfma_f32_16x16x32_bf16 a[52:55],a[172:175],a[204:207],a[52:55]
ds_read_b64_tr_b16 a[132:133],v22 offset:17408
v_mfma_f32_16x16x32_bf16 a[52:55],a[168:171],a[200:203],a[52:55]
ds_read_b64_tr_b16 a[134:135],v22 offset:19456
v_mfma_f32_16x16x32_bf16 a[84:87],a[168:171],a[208:211],a[84:87]
ds_read_b64_tr_b16 a[136:137],v22 offset:1032
v_mfma_f32_16x16x32_bf16 a[84:87],a[172:175],a[212:215],a[84:87]
ds_read_b64_tr_b16 a[138:139],v22 offset:3080
v_mfma_f32_16x16x32_bf16 a[116:119],a[172:175],a[220:223],a[116:119]
ds_read_b64_tr_b16 a[140:141],v22 offset:17416
v_mfma_f32_16x16x32_bf16 a[116:119],a[168:171],a[216:219],a[116:119]
ds_read_b64_tr_b16 a[142:143],v22 offset:19464
v_mfma_f32_16x16x32_bf16 a[24:27],a[176:179],a[192:195],a[24:27]
ds_read_b64_tr_b16 a[144:145],v22 offset:1280
v_mfma_f32_16x16x32_bf16 a[24:27],a[180:183],a[196:199],a[24:27]
ds_read_b64_tr_b16 a[146:147],v22 offset:3328
v_mfma_f32_16x16x32_bf16 a[56:59],a[180:183],a[204:207],a[56:59]
ds_read_b64_tr_b16 a[148:149],v22 offset:17664
v_mfma_f32_16x16x32_bf16 a[56:59],a[176:179],a[200:203],a[56:59]
ds_read_b64_tr_b16 a[150:151],v22 offset:19712
v_mfma_f32_16x16x32_bf16 a[88:91],a[176:179],a[208:211],a[88:91]
ds_read_b64_tr_b16 a[152:153],v22 offset:1288
v_mfma_f32_16x16x32_bf16 a[88:91],a[180:183],a[212:215],a[88:91]
ds_read_b64_tr_b16 a[154:155],v22 offset:3336
v_mfma_f32_16x16x32_bf16 a[120:123],a[180:183],a[220:223],a[120:123]
ds_read_b64_tr_b16 a[156:157],v22 offset:17672
v_mfma_f32_16x16x32_bf16 a[120:123],a[176:179],a[216:219],a[120:123]
ds_read_b64_tr_b16 a[158:159],v22 offset:19720
v_mfma_f32_16x16x32_bf16 a[28:31],a[184:187],a[192:195],a[28:31]
ds_read_b64_tr_b16 a[160:161],v22 offset:1536
v_mfma_f32_16x16x32_bf16 a[28:31],a[188:191],a[196:199],a[28:31]
ds_read_b64_tr_b16 a[162:163],v22 offset:3584
v_mfma_f32_16x16x32_bf16 a[60:63],a[188:191],a[204:207],a[60:63]
ds_read_b64_tr_b16 a[164:165],v22 offset:17920
v_mfma_f32_16x16x32_bf16 a[60:63],a[184:187],a[200:203],a[60:63]
ds_read_b64_tr_b16 a[166:167],v22 offset:19968
v_mfma_f32_16x16x32_bf16 a[92:95],a[184:187],a[208:211],a[92:95]
ds_read_b64_tr_b16 a[168:169],v22 offset:1544
v_mfma_f32_16x16x32_bf16 a[92:95],a[188:191],a[212:215],a[92:95]
ds_read_b64_tr_b16 a[170:171],v22 offset:3592
v_mfma_f32_16x16x32_bf16 a[124:127],a[188:191],a[220:223],a[124:127]
s_add_u32 m0,s58,0
ds_read_b64_tr_b16 a[172:173],v22 offset:17928
v_mfma_f32_16x16x32_bf16 a[124:127],a[184:187],a[216:219],a[124:127]
s_waitcnt lgkmcnt(7)
ds_read_b64_tr_b16 a[174:175],v22 offset:19976
v_mfma_f32_16x16x32_bf16 v[128:131],a[128:131],a[224:227],v[128:131]
v_mfma_f32_16x16x32_bf16 v[128:131],a[132:135],a[228:231],v[128:131]
buffer_load_dwordx4 v56,s[32:35],s56 offen lds
ds_read_b64_tr_b16 a[176:177],v22 offset:1792
v_mfma_f32_16x16x32_bf16 v[136:139],a[132:135],a[236:239],v[136:139]
v_mfma_f32_16x16x32_bf16 v[136:139],a[128:131],a[232:235],v[136:139]
s_add_u32 m0,s58,1024
ds_read_b64_tr_b16 a[178:179],v22 offset:3840
v_mfma_f32_16x16x32_bf16 v[144:147],a[128:131],a[240:243],v[144:147]
v_mfma_f32_16x16x32_bf16 v[144:147],a[132:135],a[244:247],v[144:147]
ds_read_b64_tr_b16 a[180:181],v22 offset:18176
v_mfma_f32_16x16x32_bf16 v[152:155],a[132:135],a[252:255],v[152:155]
buffer_load_dwordx4 v57,s[32:35],s56 offen lds
v_mfma_f32_16x16x32_bf16 v[152:155],a[128:131],a[248:251],v[152:155]
ds_read_b64_tr_b16 a[182:183],v22 offset:20224
v_mfma_f32_16x16x32_bf16 v[132:135],a[136:139],a[224:227],v[132:135]
v_mfma_f32_16x16x32_bf16 v[132:135],a[140:143],a[228:231],v[132:135]
s_add_u32 m0,s58,2048
ds_read_b64_tr_b16 a[184:185],v22 offset:1800
v_mfma_f32_16x16x32_bf16 v[140:143],a[140:143],a[236:239],v[140:143]
v_mfma_f32_16x16x32_bf16 v[140:143],a[136:139],a[232:235],v[140:143]
ds_read_b64_tr_b16 a[186:187],v22 offset:3848
v_mfma_f32_16x16x32_bf16 v[148:151],a[136:139],a[240:243],v[148:151]
buffer_load_dwordx4 v58,s[32:35],s56 offen lds
v_mfma_f32_16x16x32_bf16 v[148:151],a[140:143],a[244:247],v[148:151]
ds_read_b64_tr_b16 a[188:189],v22 offset:18184
v_mfma_f32_16x16x32_bf16 v[156:159],a[140:143],a[252:255],v[156:159]
s_add_u32 m0,s58,3072
v_mfma_f32_16x16x32_bf16 v[156:159],a[136:139],a[248:251],v[156:159]
ds_read_b64_tr_b16 a[190:191],v22 offset:20232
v_mfma_f32_16x16x32_bf16 v[160:163],a[144:147],a[224:227],v[160:163]
v_mfma_f32_16x16x32_bf16 v[160:163],a[148:151],a[228:231],v[160:163]
buffer_load_dwordx4 v59,s[32:35],s56 offen lds
ds_read_b64_tr_b16 a[128:129],v22
v_mfma_f32_16x16x32_bf16 v[168:171],a[148:151],a[236:239],v[168:171]
v_mfma_f32_16x16x32_bf16 v[168:171],a[144:147],a[232:235],v[168:171]
ds_read_b64_tr_b16 a[130:131],v22 offset:2048
v_mfma_f32_16x16x32_bf16 v[176:179],a[144:147],a[240:243],v[176:179]
s_add_u32 m0,s58,4096
v_mfma_f32_16x16x32_bf16 v[176:179],a[148:151],a[244:247],v[176:179]
ds_read_b64_tr_b16 a[132:133],v22 offset:16384
v_mfma_f32_16x16x32_bf16 v[184:187],a[148:151],a[252:255],v[184:187]
v_mfma_f32_16x16x32_bf16 v[184:187],a[144:147],a[248:251],v[184:187]
buffer_load_dwordx4 v60,s[32:35],s56 offen lds
ds_read_b64_tr_b16 a[134:135],v22 offset:18432
v_mfma_f32_16x16x32_bf16 v[164:167],a[152:155],a[224:227],v[164:167]
v_mfma_f32_16x16x32_bf16 v[164:167],a[156:159],a[228:231],v[164:167]
s_add_u32 m0,s58,5120
ds_read_b64_tr_b16 a[136:137],v22 offset:8
v_mfma_f32_16x16x32_bf16 v[172:175],a[156:159],a[236:239],v[172:175]
v_mfma_f32_16x16x32_bf16 v[172:175],a[152:155],a[232:235],v[172:175]
ds_read_b64_tr_b16 a[138:139],v22 offset:2056
v_mfma_f32_16x16x32_bf16 v[180:183],a[152:155],a[240:243],v[180:183]
buffer_load_dwordx4 v61,s[32:35],s56 offen lds
v_mfma_f32_16x16x32_bf16 v[180:183],a[156:159],a[244:247],v[180:183]
ds_read_b64_tr_b16 a[140:141],v22 offset:16392
v_mfma_f32_16x16x32_bf16 v[188:191],a[156:159],a[252:255],v[188:191]
v_mfma_f32_16x16x32_bf16 v[188:191],a[152:155],a[248:251],v[188:191]
s_add_u32 m0,s58,6144
ds_read_b64_tr_b16 a[142:143],v22 offset:18440
s_waitcnt lgkmcnt(4)
v_mfma_f32_16x16x32_bf16 v[192:195],a[160:163],a[224:227],v[192:195]
v_mfma_f32_16x16x32_bf16 v[192:195],a[164:167],a[228:231],v[192:195]
ds_read_b64_tr_b16 a[144:145],v22 offset:256
v_mfma_f32_16x16x32_bf16 v[200:203],a[164:167],a[236:239],v[200:203]
buffer_load_dwordx4 v62,s[32:35],s56 offen lds
v_mfma_f32_16x16x32_bf16 v[200:203],a[160:163],a[232:235],v[200:203]
ds_read_b64_tr_b16 a[146:147],v22 offset:2304
v_mfma_f32_16x16x32_bf16 v[208:211],a[160:163],a[240:243],v[208:211]
s_add_u32 m0,s58,7168
s_add_u32 s58,s58,32768
v_mfma_f32_16x16x32_bf16 v[208:211],a[164:167],a[244:247],v[208:211]
s_cmp_ge_u32 s58,131072
s_cselect_b32 s62,98304,0
ds_read_b64_tr_b16 a[148:149],v22 offset:16640
v_mfma_f32_16x16x32_bf16 v[216:219],a[164:167],a[252:255],v[216:219]
s_sub_u32 s58,s58,s62
v_mfma_f32_16x16x32_bf16 v[216:219],a[160:163],a[248:251],v[216:219]
buffer_load_dwordx4 v63,s[32:35],s56 offen lds
ds_read_b64_tr_b16 a[150:151],v22 offset:18688
v_mfma_f32_16x16x32_bf16 v[196:199],a[168:171],a[224:227],v[196:199]
s_add_u32 s56,s56,524288
v_mfma_f32_16x16x32_bf16 v[196:199],a[172:175],a[228:231],v[196:199]
ds_read_b64_tr_b16 a[152:153],v22 offset:264
v_mfma_f32_16x16x32_bf16 v[204:207],a[172:175],a[236:239],v[204:207]
s_add_u32 m0,s59,0
v_mfma_f32_16x16x32_bf16 v[204:207],a[168:171],a[232:235],v[204:207]
ds_read_b64_tr_b16 a[154:155],v22 offset:2312
v_mfma_f32_16x16x32_bf16 v[212:215],a[168:171],a[240:243],v[212:215]
v_mfma_f32_16x16x32_bf16 v[212:215],a[172:175],a[244:247],v[212:215]
buffer_load_dwordx4 v48,s[28:31],s57 offen lds
ds_read_b64_tr_b16 a[156:157],v22 offset:16648
v_mfma_f32_16x16x32_bf16 v[220:223],a[172:175],a[252:255],v[220:223]
v_mfma_f32_16x16x32_bf16 v[220:223],a[168:171],a[248:251],v[220:223]
ds_read_b64_tr_b16 a[158:159],v22 offset:18696
v_mfma_f32_16x16x32_bf16 v[224:227],a[176:179],a[224:227],v[224:227]
v_mfma_f32_16x16x32_bf16 v[224:227],a[180:183],a[228:231],v[224:227]
ds_read_b64_tr_b16 a[160:161],v22 offset:512
v_mfma_f32_16x16x32_bf16 v[232:235],a[180:183],a[236:239],v[232:235]
buffer_load_dwordx4 v49,s[28:31],s57 offen offset:1024 lds
v_mfma_f32_16x16x32_bf16 v[232:235],a[176:179],a[232:235],v[232:235]
ds_read_b64_tr_b16 a[162:163],v22 offset:2560
v_mfma_f32_16x16x32_bf16 v[240:243],a[176:179],a[240:243],v[240:243]
v_mfma_f32_16x16x32_bf16 v[240:243],a[180:183],a[244:247],v[240:243]
ds_read_b64_tr_b16 a[164:165],v22 offset:16896
v_mfma_f32_16x16x32_bf16 v[248:251],a[180:183],a[252:255],v[248:251]
v_mfma_f32_16x16x32_bf16 v[248:251],a[176:179],a[248:251],v[248:251]
ds_read_b64_tr_b16 a[166:167],v22 offset:18944
v_mfma_f32_16x16x32_bf16 v[228:231],a[184:187],a[224:227],v[228:231]
buffer_load_dwordx4 v50,s[28:31],s57 offen offset:2048 lds
v_mfma_f32_16x16x32_bf16 v[228:231],a[188:191],a[228:231],v[228:231]
ds_read_b64_tr_b16 a[168:169],v22 offset:520
v_mfma_f32_16x16x32_bf16 v[236:239],a[188:191],a[236:239],v[236:239]
v_mfma_f32_16x16x32_bf16 v[236:239],a[184:187],a[232:235],v[236:239]
ds_read_b64_tr_b16 a[170:171],v22 offset:2568
v_mfma_f32_16x16x32_bf16 v[244:247],a[184:187],a[240:243],v[244:247]
v_mfma_f32_16x16x32_bf16 v[244:247],a[188:191],a[244:247],v[244:247]
buffer_load_dwordx4 v51,s[28:31],s57 offen offset:3072 lds
)A"
R"A(ds_read_b64_tr_b16 a[172:173],v22 offset:16904
v_mfma_f32_16x16x32_bf16 v[252:255],a[188:191],a[252:255],v[252:255]
v_mfma_f32_16x16x32_bf16 v[252:255],a[184:187],a[248:251],v[252:255]
ds_read_b64_tr_b16 a[174:175],v22 offset:18952
v_mfma_f32_16x16x32_bf16 a[0:3],a[128:131],a[224:227],a[0:3]
s_add_u32 m0,s59,4096
s_xor_b32 s59,s59,131072
v_mfma_f32_16x16x32_bf16 a[0:3],a[132:135],a[228:231],a[0:3]
ds_read_b64_tr_b16 a[176:177],v22 offset:768
v_mfma_f32_16x16x32_bf16 a[32:35],a[132:135],a[236:239],a[32:35]
v_mfma_f32_16x16x32_bf16 a[32:35],a[128:131],a[232:235],a[32:35]
buffer_load_dwordx4 v52,s[28:31],s57 offen lds
ds_read_b64_tr_b16 a[178:179],v22 offset:2816
v_mfma_f32_16x16x32_bf16 a[64:67],a[128:131],a[240:243],a[64:67]
v_mfma_f32_16x16x32_bf16 a[64:67],a[132:135],a[244:247],a[64:67]
ds_read_b64_tr_b16 a[180:181],v22 offset:17152
v_mfma_f32_16x16x32_bf16 a[96:99],a[132:135],a[252:255],a[96:99]
v_mfma_f32_16x16x32_bf16 a[96:99],a[128:131],a[248:251],a[96:99]
s_waitcnt lgkmcnt(3)
ds_read_b64_tr_b16 a[182:183],v22 offset:19200
v_mfma_f32_16x16x32_bf16 a[4:7],a[136:139],a[224:227],a[4:7]
buffer_load_dwordx4 v53,s[28:31],s57 offen offset:1024 lds
v_mfma_f32_16x16x32_bf16 a[4:7],a[140:143],a[228:231],a[4:7]
ds_read_b64_tr_b16 a[184:185],v22 offset:776
v_mfma_f32_16x16x32_bf16 a[36:39],a[140:143],a[236:239],a[36:39]
v_mfma_f32_16x16x32_bf16 a[36:39],a[136:139],a[232:235],a[36:39]
ds_read_b64_tr_b16 a[186:187],v22 offset:2824
v_mfma_f32_16x16x32_bf16 a[68:71],a[136:139],a[240:243],a[68:71]
v_mfma_f32_16x16x32_bf16 a[68:71],a[140:143],a[244:247],a[68:71]
ds_read_b64_tr_b16 a[188:189],v22 offset:17160
v_mfma_f32_16x16x32_bf16 a[100:103],a[140:143],a[252:255],a[100:103]
buffer_load_dwordx4 v54,s[28:31],s57 offen offset:2048 lds
v_mfma_f32_16x16x32_bf16 a[100:103],a[136:139],a[248:251],a[100:103]
ds_read_b64_tr_b16 a[190:191],v22 offset:19208
v_add_u32 v22,s60,v23
v_mfma_f32_16x16x32_bf16 a[8:11],a[144:147],a[224:227],a[8:11]
v_mfma_f32_16x16x32_bf16 a[8:11],a[148:151],a[228:231],a[8:11]
v_mfma_f32_16x16x32_bf16 a[40:43],a[148:151],a[236:239],a[40:43]
v_mfma_f32_16x16x32_bf16 a[40:43],a[144:147],a[232:235],a[40:43]
buffer_load_dwordx4 v55,s[28:31],s57 offen offset:3072 lds
v_mfma_f32_16x16x32_bf16 a[72:75],a[144:147],a[240:243],a[72:75]
s_add_u32 s57,s57,128
v_mfma_f32_16x16x32_bf16 a[72:75],a[148:151],a[244:247],a[72:75]
v_mfma_f32_16x16x32_bf16 a[104:107],a[148:151],a[252:255],a[104:107]
v_mfma_f32_16x16x32_bf16 a[104:107],a[144:147],a[248:251],a[104:107]
v_mfma_f32_16x16x32_bf16 a[12:15],a[152:155],a[224:227],a[12:15]
v_mfma_f32_16x16x32_bf16 a[12:15],a[156:159],a[228:231],a[12:15]
v_mfma_f32_16x16x32_bf16 a[44:47],a[156:159],a[236:239],a[44:47]
v_mfma_f32_16x16x32_bf16 a[44:47],a[152:155],a[232:235],a[44:47]
v_mfma_f32_16x16x32_bf16 a[76:79],a[152:155],a[240:243],a[76:79]
v_mfma_f32_16x16x32_bf16 a[76:79],a[156:159],a[244:247],a[76:79]
v_mfma_f32_16x16x32_bf16 a[108:111],a[156:159],a[252:255],a[108:111]
v_mfma_f32_16x16x32_bf16 a[108:111],a[152:155],a[248:251],a[108:111]
s_waitcnt vmcnt(16) lgkmcnt(0)
s_barrier
v_mfma_f32_16x16x32_bf16 a[16:19],a[160:163],a[224:227],a[16:19]
ds_read_b128 a[192:195],v20
v_mfma_f32_16x16x32_bf16 a[16:19],a[164:167],a[228:231],a[16:19]
ds_read_b128 a[196:199],v21
v_mfma_f32_16x16x32_bf16 a[48:51],a[164:167],a[236:239],a[48:51]
ds_read_b128 a[200:203],v20 offset:2048
v_mfma_f32_16x16x32_bf16 a[48:51],a[160:163],a[232:235],a[48:51]
ds_read_b128 a[204:207],v21 offset:2048
v_mfma_f32_16x16x32_bf16 a[80:83],a[160:163],a[240:243],a[80:83]
ds_read_b128 a[208:211],v20 offset:4096
v_mfma_f32_16x16x32_bf16 a[80:83],a[164:167],a[244:247],a[80:83]
ds_read_b128 a[212:215],v21 offset:4096
v_mfma_f32_16x16x32_bf16 a[112:115],a[164:167],a[252:255],a[112:115]
ds_read_b128 a[216:219],v20 offset:6144
v_mfma_f32_16x16x32_bf16 a[112:115],a[160:163],a[248:251],a[112:115]
ds_read_b128 a[220:223],v21 offset:6144
v_mfma_f32_16x16x32_bf16 a[20:23],a[168:171],a[224:227],a[20:23]
ds_read_b64_tr_b16 a[128:129],v22 offset:1024
v_mfma_f32_16x16x32_bf16 a[20:23],a[172:175],a[228:231],a[20:23]
ds_read_b64_tr_b16 a[130:131],v22 offset:3072
v_mfma_f32_16x16x32_bf16 a[52:55],a[172:175],a[236:239],a[52:55]
ds_read_b64_tr_b16 a[132:133],v22 offset:17408
v_mfma_f32_16x16x32_bf16 a[52:55],a[168:171],a[232:235],a[52:55]
ds_read_b64_tr_b16 a[134:135],v22 offset:19456
v_mfma_f32_16x16x32_bf16 a[84:87],a[168:171],a[240:243],a[84:87]
ds_read_b64_tr_b16 a[136:137],v22 offset:1032
v_mfma_f32_16x16x32_bf16 a[84:87],a[172:175],a[244:247],a[84:87]
ds_read_b64_tr_b16 a[138:139],v22 offset:3080
v_mfma_f32_16x16x32_bf16 a[116:119],a[172:175],a[252:255],a[116:119]
ds_read_b64_tr_b16 a[140:141],v22 offset:17416
v_mfma_f32_16x16x32_bf16 a[116:119],a[168:171],a[248:251],a[116:119]
ds_read_b64_tr_b16 a[142:143],v22 offset:19464
v_mfma_f32_16x16x32_bf16 a[24:27],a[176:179],a[224:227],a[24:27]
ds_read_b64_tr_b16 a[144:145],v22 offset:1280
v_mfma_f32_16x16x32_bf16 a[24:27],a[180:183],a[228:231],a[24:27]
ds_read_b64_tr_b16 a[146:147],v22 offset:3328
v_mfma_f32_16x16x32_bf16 a[56:59],a[180:183],a[236:239],a[56:59]
ds_read_b64_tr_b16 a[148:149],v22 offset:17664
v_mfma_f32_16x16x32_bf16 a[56:59],a[176:179],a[232:235],a[56:59]
ds_read_b64_tr_b16 a[150:151],v22 offset:19712
v_mfma_f32_16x16x32_bf16 a[88:91],a[176:179],a[240:243],a[88:91]
ds_read_b64_tr_b16 a[152:153],v22 offset:1288
v_mfma_f32_16x16x32_bf16 a[88:91],a[180:183],a[244:247],a[88:91]
ds_read_b64_tr_b16 a[154:155],v22 offset:3336
v_mfma_f32_16x16x32_bf16 a[120:123],a[180:183],a[252:255],a[120:123]
ds_read_b64_tr_b16 a[156:157],v22 offset:17672
v_mfma_f32_16x16x32_bf16 a[120:123],a[176:179],a[248:251],a[120:123]
ds_read_b64_tr_b16 a[158:159],v22 offset:19720
v_mfma_f32_16x16x32_bf16 a[28:31],a[184:187],a[224:227],a[28:31]
v_mfma_f32_16x16x32_bf16 a[28:31],a[188:191],a[228:231],a[28:31]
v_mfma_f32_16x16x32_bf16 a[60:63],a[188:191],a[236:239],a[60:63]
v_mfma_f32_16x16x32_bf16 a[60:63],a[184:187],a[232:235],a[60:63]
v_mfma_f32_16x16x32_bf16 a[92:95],a[184:187],a[240:243],a[92:95]
v_mfma_f32_16x16x32_bf16 a[92:95],a[188:191],a[244:247],a[92:95]
v_mfma_f32_16x16x32_bf16 a[124:127],a[188:191],a[252:255],a[124:127]
v_mfma_f32_16x16x32_bf16 a[124:127],a[184:187],a[248:251],a[124:127]
s_sub_u32 s53,s53,1
s_cmp_lg_u32 s53,0
s_cbranch_scc1 L_qp_%=
s_waitcnt lgkmcnt(0)
v_mfma_f32_16x16x32_bf16 v[128:131],a[128:131],a[192:195],v[128:131]
s_add_u32 m0,98304,s51
ds_read_b64_tr_b16 a[160:161],v22 offset:1536
v_xor_b32 v20,131072,v20
v_mfma_f32_16x16x32_bf16 v[128:131],a[132:135],a[196:199],v[128:131]
s_add_u32 s54,s54,16384
buffer_load_dwordx4 v4,s[20:23],s47 offen nt lds
v_xor_b32 v21,131072,v21
v_mfma_f32_16x16x32_bf16 v[136:139],a[132:135],a[204:207],v[136:139]
s_and_b32 s46,s54,0xfffc0000
s_add_u32 s54,s54,16384
ds_read_b64_tr_b16 a[162:163],v22 offset:3584
v_mfma_f32_16x16x32_bf16 v[136:139],a[128:131],a[200:203],v[136:139]
s_mov_b32 s49,s50
v_and_b32 v18,48,v6
v_lshrrev_b32 v19,4,v6
v_mfma_f32_16x16x32_bf16 v[144:147],a[128:131],a[208:211],v[144:147]
ds_read_b64_tr_b16 a[164:165],v22 offset:17920
v_lshlrev_b32 v18,2,v18
v_mfma_f32_16x16x32_bf16 v[144:147],a[132:135],a[212:215],v[144:147]
v_and_b32 v19,48,v19
v_add_u32 v18,v18,v19
v_mfma_f32_16x16x32_bf16 v[152:155],a[132:135],a[220:223],v[152:155]
buffer_load_dwordx4 v4,s[20:23],s47 offen offset:1024 nt lds
ds_read_b64_tr_b16 a[166:167],v22 offset:19968
v_mfma_f32_16x16x32_bf16 v[152:155],a[128:131],a[216:219],v[152:155]
s_cmp_eq_u32 s50,131072
s_cselect_b32 s62,163840,0
v_add_u32 v18,98304,v18
v_mfma_f32_16x16x32_bf16 v[132:135],a[136:139],a[192:195],v[132:135]
s_sub_u32 s48,32768,s62
s_add_u32 s50,s50,s48
ds_read_b64_tr_b16 a[168:169],v22 offset:1544
v_mfma_f32_16x16x32_bf16 v[132:135],a[140:143],a[196:199],v[132:135]
s_cmp_eq_u32 s50,131072
s_cselect_b32 s62,163840,0
v_mfma_f32_16x16x32_bf16 v[140:143],a[140:143],a[204:207],v[140:143]
ds_read_b64_tr_b16 a[170:171],v22 offset:3592
v_mfma_f32_16x16x32_bf16 v[140:143],a[136:139],a[200:203],v[140:143]
buffer_load_dwordx4 v5,s[20:23],s47 offen offset:2048 nt lds
v_mfma_f32_16x16x32_bf16 v[148:151],a[136:139],a[208:211],v[148:151]
ds_read_b64_tr_b16 a[172:173],v22 offset:17928
v_mfma_f32_16x16x32_bf16 v[148:151],a[140:143],a[212:215],v[148:151]
v_mfma_f32_16x16x32_bf16 v[156:159],a[140:143],a[220:223],v[156:159]
ds_read_b64_tr_b16 a[174:175],v22 offset:19976
v_mfma_f32_16x16x32_bf16 v[156:159],a[136:139],a[216:219],v[156:159]
v_mfma_f32_16x16x32_bf16 v[160:163],a[144:147],a[192:195],v[160:163]
buffer_load_dwordx4 v5,s[20:23],s47 offen offset:3072 nt lds
ds_read_b64_tr_b16 a[176:177],v22 offset:1792
v_mfma_f32_16x16x32_bf16 v[160:163],a[148:151],a[196:199],v[160:163]
v_mfma_f32_16x16x32_bf16 v[168:171],a[148:151],a[204:207],v[168:171]
s_add_u32 m0,98304,s52
s_waitcnt vmcnt(4)
ds_read_b128 a[224:227],v20
v_mfma_f32_16x16x32_bf16 v[168:171],a[144:147],a[200:203],v[168:171]
v_mfma_f32_16x16x32_bf16 v[176:179],a[144:147],a[208:211],v[176:179]
ds_read_b128 a[228:231],v21
v_mfma_f32_16x16x32_bf16 v[176:179],a[148:151],a[212:215],v[176:179]
buffer_load_dwordx4 v4,s[24:27],s47 offen nt lds
v_mfma_f32_16x16x32_bf16 v[184:187],a[148:151],a[220:223],v[184:187]
ds_read_b128 a[232:235],v20 offset:2048
v_mfma_f32_16x16x32_bf16 v[184:187],a[144:147],a[216:219],v[184:187]
v_mfma_f32_16x16x32_bf16 v[164:167],a[152:155],a[192:195],v[164:167]
ds_read_b128 a[236:239],v21 offset:2048
v_mfma_f32_16x16x32_bf16 v[164:167],a[156:159],a[196:199],v[164:167]
v_mfma_f32_16x16x32_bf16 v[172:175],a[156:159],a[204:207],v[172:175]
buffer_load_dwordx4 v5,s[24:27],s47 offen offset:1024 nt lds
ds_read_b128 a[240:243],v20 offset:4096
v_mfma_f32_16x16x32_bf16 v[172:175],a[152:155],a[200:203],v[172:175]
v_mfma_f32_16x16x32_bf16 v[180:183],a[152:155],a[208:211],v[180:183]
ds_read_b128 a[244:247],v21 offset:4096
v_mfma_f32_16x16x32_bf16 v[180:183],a[156:159],a[212:215],v[180:183]
v_mfma_f32_16x16x32_bf16 v[188:191],a[156:159],a[220:223],v[188:191]
ds_read_b128 a[248:251],v20 offset:6144
v_mfma_f32_16x16x32_bf16 v[188:191],a[152:155],a[216:219],v[188:191]
s_waitcnt lgkmcnt(4)
buffer_load_dwordx4 v4,s[24:27],s47 offen offset:2048 nt lds
v_mfma_f32_16x16x32_bf16 v[192:195],a[160:163],a[192:195],v[192:195]
ds_read_b128 a[252:255],v21 offset:6144
v_mfma_f32_16x16x32_bf16 v[192:195],a[164:167],a[196:199],v[192:195]
v_mfma_f32_16x16x32_bf16 v[200:203],a[164:167],a[204:207],v[200:203]
ds_read_b64_tr_b16 a[178:179],v22 offset:3840
v_mfma_f32_16x16x32_bf16 v[200:203],a[160:163],a[200:203],v[200:203]
v_mfma_f32_16x16x32_bf16 v[208:211],a[160:163],a[208:211],v[208:211]
buffer_load_dwordx4 v5,s[24:27],s47 offen offset:3072 nt lds
ds_read_b64_tr_b16 a[180:181],v22 offset:18176
v_mfma_f32_16x16x32_bf16 v[208:211],a[164:167],a[212:215],v[208:211]
s_add_u32 s47,s47,16384
s_and_b32 s47,s47,0x3ffff
s_or_b32 s47,s47,s46
v_mfma_f32_16x16x32_bf16 v[216:219],a[164:167],a[220:223],v[216:219]
s_and_b32 s46,s54,0xfffc0000
s_add_u32 s54,s54,16384
ds_read_b64_tr_b16 a[182:183],v22 offset:20224
v_mfma_f32_16x16x32_bf16 v[216:219],a[160:163],a[216:219],v[216:219]
s_waitcnt lgkmcnt(3)
s_barrier
v_mfma_f32_16x16x32_bf16 v[196:199],a[168:171],a[192:195],v[196:199]
s_add_u32 m0,131072,s51
ds_read_b64_tr_b16 a[184:185],v22 offset:1800
v_mfma_f32_16x16x32_bf16 v[196:199],a[172:175],a[196:199],v[196:199]
v_mfma_f32_16x16x32_bf16 v[204:207],a[172:175],a[204:207],v[204:207]
buffer_load_dwordx4 v4,s[20:23],s47 offen nt lds
ds_read_b64_tr_b16 a[186:187],v22 offset:3848
v_mfma_f32_16x16x32_bf16 v[204:207],a[168:171],a[200:203],v[204:207]
v_mfma_f32_16x16x32_bf16 v[212:215],a[168:171],a[208:211],v[212:215]
ds_read_b64_tr_b16 a[188:189],v22 offset:18184
v_mfma_f32_16x16x32_bf16 v[212:215],a[172:175],a[212:215],v[212:215]
v_mfma_f32_16x16x32_bf16 v[220:223],a[172:175],a[220:223],v[220:223]
ds_read_b64_tr_b16 a[190:191],v22 offset:20232
v_mfma_f32_16x16x32_bf16 v[220:223],a[168:171],a[216:219],v[220:223]
s_waitcnt lgkmcnt(4)
v_mfma_f32_16x16x32_bf16 v[224:227],a[176:179],a[192:195],v[224:227]
ds_read_b64_tr_b16 a[128:129],v22
v_mfma_f32_16x16x32_bf16 v[224:227],a[180:183],a[196:199],v[224:227]
)A"
R"A(v_mfma_f32_16x16x32_bf16 v[232:235],a[180:183],a[204:207],v[232:235]
ds_read_b64_tr_b16 a[130:131],v22 offset:2048
v_mfma_f32_16x16x32_bf16 v[232:235],a[176:179],a[200:203],v[232:235]
buffer_load_dwordx4 v4,s[20:23],s47 offen offset:1024 nt lds
v_mfma_f32_16x16x32_bf16 v[240:243],a[176:179],a[208:211],v[240:243]
ds_read_b64_tr_b16 a[132:133],v22 offset:16384
v_mfma_f32_16x16x32_bf16 v[240:243],a[180:183],a[212:215],v[240:243]
v_mfma_f32_16x16x32_bf16 v[248:251],a[180:183],a[220:223],v[248:251]
ds_read_b64_tr_b16 a[134:135],v22 offset:18432
v_mfma_f32_16x16x32_bf16 v[248:251],a[176:179],a[216:219],v[248:251]
s_waitcnt lgkmcnt(4)
v_mfma_f32_16x16x32_bf16 v[228:231],a[184:187],a[192:195],v[228:231]
ds_read_b64_tr_b16 a[136:137],v22 offset:32768
v_mfma_f32_16x16x32_bf16 v[228:231],a[188:191],a[196:199],v[228:231]
ds_read_b64_tr_b16 a[138:139],v22 offset:34816
v_mfma_f32_16x16x32_bf16 v[236:239],a[188:191],a[204:207],v[236:239]
ds_read_b64_tr_b16 a[140:141],v22 offset:49152
ds_read_b64_tr_b16 a[142:143],v22 offset:51200
v_mfma_f32_16x16x32_bf16 v[236:239],a[184:187],a[200:203],v[236:239]
v_mfma_f32_16x16x32_bf16 v[244:247],a[184:187],a[208:211],v[244:247]
v_mfma_f32_16x16x32_bf16 v[244:247],a[188:191],a[212:215],v[244:247]
buffer_load_dwordx4 v5,s[20:23],s47 offen offset:2048 nt lds
v_mfma_f32_16x16x32_bf16 v[252:255],a[188:191],a[220:223],v[252:255]
v_mfma_f32_16x16x32_bf16 v[252:255],a[184:187],a[216:219],v[252:255]
s_waitcnt lgkmcnt(4)
ds_read_b64_tr_b16 a[144:145],v22 offset:8
v_mfma_f32_16x16x32_bf16 a[0:3],a[128:131],a[192:195],a[0:3]
v_mfma_f32_16x16x32_bf16 a[0:3],a[132:135],a[196:199],a[0:3]
s_waitcnt lgkmcnt(3)
ds_read_b64_tr_b16 a[146:147],v22 offset:2056
v_mfma_f32_16x16x32_bf16 a[0:3],a[136:139],a[224:227],a[0:3]
s_waitcnt lgkmcnt(2)
v_mfma_f32_16x16x32_bf16 a[0:3],a[140:143],a[228:231],a[0:3]
ds_read_b64_tr_b16 a[148:149],v22 offset:16392
v_mfma_f32_16x16x32_bf16 a[32:35],a[140:143],a[236:239],a[32:35]
v_mfma_f32_16x16x32_bf16 a[32:35],a[136:139],a[232:235],a[32:35]
buffer_load_dwordx4 v5,s[20:23],s47 offen offset:3072 nt lds
ds_read_b64_tr_b16 a[150:151],v22 offset:18440
v_mfma_f32_16x16x32_bf16 a[32:35],a[132:135],a[204:207],a[32:35]
v_mfma_f32_16x16x32_bf16 a[32:35],a[128:131],a[200:203],a[32:35]
ds_read_b64_tr_b16 a[152:153],v22 offset:32776
v_mfma_f32_16x16x32_bf16 a[64:67],a[128:131],a[208:211],a[64:67]
v_mfma_f32_16x16x32_bf16 a[64:67],a[132:135],a[212:215],a[64:67]
ds_read_b64_tr_b16 a[154:155],v22 offset:34824
v_accvgpr_read_b32 v64,a0
v_mfma_f32_16x16x32_bf16 a[64:67],a[136:139],a[240:243],a[64:67]
ds_read_b64_tr_b16 a[156:157],v22 offset:49160
ds_read_b64_tr_b16 a[158:159],v22 offset:51208
v_mfma_f32_16x16x32_bf16 a[64:67],a[140:143],a[244:247],a[64:67]
v_mfma_f32_16x16x32_bf16 a[96:99],a[140:143],a[252:255],a[96:99]
v_mfma_f32_16x16x32_bf16 a[96:99],a[136:139],a[248:251],a[96:99]
s_add_u32 m0,131072,s52
v_accvgpr_read_b32 v65,a1
v_mul_f32 v64,s61,v64
v_mfma_f32_16x16x32_bf16 a[96:99],a[132:135],a[220:223],a[96:99]
buffer_load_dwordx4 v4,s[24:27],s47 offen nt lds
ds_read_b64_tr_b16 a[160:161],v22 offset:256
v_mfma_f32_16x16x32_bf16 a[96:99],a[128:131],a[216:219],a[96:99]
s_waitcnt lgkmcnt(4)
v_mul_f32 v65,s61,v65
v_accvgpr_read_b32 v66,a2
v_mfma_f32_16x16x32_bf16 a[4:7],a[144:147],a[192:195],a[4:7]
ds_read_b64_tr_b16 a[162:163],v22 offset:2304
v_accvgpr_read_b32 v67,a3
v_mfma_f32_16x16x32_bf16 a[4:7],a[148:151],a[196:199],a[4:7]
s_waitcnt lgkmcnt(4)
v_cvt_pk_bf16_f32 v64,v64,v65
v_mul_f32 v66,s61,v66
v_mfma_f32_16x16x32_bf16 a[4:7],a[152:155],a[224:227],a[4:7]
s_waitcnt lgkmcnt(2)
ds_read_b64_tr_b16 a[164:165],v22 offset:16640
v_mul_f32 v67,s61,v67
v_mfma_f32_16x16x32_bf16 a[4:7],a[156:159],a[228:231],a[4:7]
v_cvt_pk_bf16_f32 v65,v66,v67
v_accvgpr_read_b32 v96,a64
v_mfma_f32_16x16x32_bf16 a[36:39],a[156:159],a[236:239],a[36:39]
ds_read_b64_tr_b16 a[166:167],v22 offset:18688
v_accvgpr_read_b32 v97,a65
v_mfma_f32_16x16x32_bf16 a[36:39],a[152:155],a[232:235],a[36:39]
v_accvgpr_read_b32 v112,a96
v_accvgpr_read_b32 v113,a97
v_mfma_f32_16x16x32_bf16 a[36:39],a[148:151],a[204:207],a[36:39]
ds_read_b64_tr_b16 a[168:169],v22 offset:33024
v_mul_f32 v112,s61,v112
v_mfma_f32_16x16x32_bf16 a[36:39],a[144:147],a[200:203],a[36:39]
buffer_load_dwordx4 v5,s[24:27],s47 offen offset:1024 nt lds
v_mul_f32 v113,s61,v113
v_mfma_f32_16x16x32_bf16 a[68:71],a[144:147],a[208:211],a[68:71]
ds_read_b64_tr_b16 a[170:171],v22 offset:35072
v_accvgpr_read_b32 v114,a98
v_mfma_f32_16x16x32_bf16 a[68:71],a[148:151],a[212:215],a[68:71]
v_accvgpr_read_b32 v66,a4
v_accvgpr_read_b32 v67,a5
v_mfma_f32_16x16x32_bf16 a[68:71],a[152:155],a[240:243],a[68:71]
ds_read_b64_tr_b16 a[172:173],v22 offset:49408
ds_read_b64_tr_b16 a[174:175],v22 offset:51456
v_mfma_f32_16x16x32_bf16 a[68:71],a[156:159],a[244:247],a[68:71]
v_mfma_f32_16x16x32_bf16 a[100:103],a[156:159],a[252:255],a[100:103]
v_accvgpr_read_b32 v115,a99
v_accvgpr_read_b32 v12,a6
v_mfma_f32_16x16x32_bf16 a[100:103],a[152:155],a[248:251],a[100:103]
v_accvgpr_read_b32 v13,a7
v_mul_f32 v66,s61,v66
v_mfma_f32_16x16x32_bf16 a[100:103],a[148:151],a[220:223],a[100:103]
ds_read_b64_tr_b16 a[176:177],v22 offset:264
v_mul_f32 v67,s61,v67
v_mfma_f32_16x16x32_bf16 a[100:103],a[144:147],a[216:219],a[100:103]
s_waitcnt lgkmcnt(4)
v_accvgpr_read_b32 v80,a32
v_accvgpr_read_b32 v81,a33
v_mfma_f32_16x16x32_bf16 a[8:11],a[160:163],a[192:195],a[8:11]
buffer_load_dwordx4 v4,s[24:27],s47 offen offset:2048 nt lds
ds_read_b64_tr_b16 a[178:179],v22 offset:2312
v_mfma_f32_16x16x32_bf16 a[8:11],a[164:167],a[196:199],a[8:11]
s_waitcnt lgkmcnt(4)
v_mul_f32 v96,s61,v96
v_mul_f32 v97,s61,v97
v_mfma_f32_16x16x32_bf16 a[8:11],a[168:171],a[224:227],a[8:11]
s_waitcnt lgkmcnt(2)
ds_read_b64_tr_b16 a[180:181],v22 offset:16648
v_accvgpr_read_b32 v98,a66
v_mfma_f32_16x16x32_bf16 a[8:11],a[172:175],a[228:231],a[8:11]
v_accvgpr_read_b32 v99,a67
v_cvt_pk_bf16_f32 v112,v112,v113
v_mfma_f32_16x16x32_bf16 a[40:43],a[172:175],a[236:239],a[40:43]
ds_read_b64_tr_b16 a[182:183],v22 offset:18696
v_mul_f32 v114,s61,v114
v_mfma_f32_16x16x32_bf16 a[40:43],a[168:171],a[232:235],a[40:43]
v_mul_f32 v115,s61,v115
v_mul_f32 v12,s61,v12
v_mfma_f32_16x16x32_bf16 a[40:43],a[164:167],a[204:207],a[40:43]
ds_read_b64_tr_b16 a[184:185],v22 offset:33032
v_mul_f32 v13,s61,v13
v_mfma_f32_16x16x32_bf16 a[40:43],a[160:163],a[200:203],a[40:43]
v_cvt_pk_bf16_f32 v66,v66,v67
v_mul_f32 v80,s61,v80
v_mfma_f32_16x16x32_bf16 a[72:75],a[160:163],a[208:211],a[72:75]
ds_read_b64_tr_b16 a[186:187],v22 offset:35080
v_mul_f32 v81,s61,v81
v_mfma_f32_16x16x32_bf16 a[72:75],a[164:167],a[212:215],a[72:75]
buffer_load_dwordx4 v5,s[24:27],s47 offen offset:3072 nt lds
v_accvgpr_read_b32 v82,a34
v_mfma_f32_16x16x32_bf16 a[72:75],a[168:171],a[240:243],a[72:75]
ds_read_b64_tr_b16 a[188:189],v22 offset:49416
ds_read_b64_tr_b16 a[190:191],v22 offset:51464
v_mfma_f32_16x16x32_bf16 a[72:75],a[172:175],a[244:247],a[72:75]
v_mfma_f32_16x16x32_bf16 a[104:107],a[172:175],a[252:255],a[104:107]
s_add_u32 s47,s47,16384
s_and_b32 s47,s47,0x3ffff
s_or_b32 s47,s47,s46
v_mfma_f32_16x16x32_bf16 a[104:107],a[168:171],a[248:251],a[104:107]
s_and_b32 s46,s54,0xfffc0000
v_accvgpr_read_b32 v83,a35
v_cvt_pk_bf16_f32 v96,v96,v97
v_mfma_f32_16x16x32_bf16 a[104:107],a[164:167],a[220:223],a[104:107]
s_add_u32 s54,s54,16384
ds_read_b64_tr_b16 a[128:129],v22 offset:512
v_mul_f32 v98,s61,v98
v_mfma_f32_16x16x32_bf16 a[104:107],a[160:163],a[216:219],a[104:107]
s_add_u32 m0,0,s51
v_mul_f32 v99,s61,v99
v_cvt_pk_bf16_f32 v113,v114,v115
s_waitcnt lgkmcnt(4)
v_mfma_f32_16x16x32_bf16 a[12:15],a[176:179],a[192:195],a[12:15]
ds_read_b64_tr_b16 a[130:131],v22 offset:2560
v_cvt_pk_bf16_f32 v67,v12,v13
v_mfma_f32_16x16x32_bf16 a[12:15],a[180:183],a[196:199],a[12:15]
s_waitcnt lgkmcnt(4)
v_accvgpr_read_b32 v100,a72
v_accvgpr_read_b32 v101,a73
v_mfma_f32_16x16x32_bf16 a[12:15],a[184:187],a[224:227],a[12:15]
s_waitcnt lgkmcnt(2)
buffer_load_dwordx4 v4,s[20:23],s47 offen nt lds
ds_read_b64_tr_b16 a[132:133],v22 offset:16896
v_mfma_f32_16x16x32_bf16 a[12:15],a[188:191],a[228:231],a[12:15]
v_cvt_pk_bf16_f32 v80,v80,v81
v_mul_f32 v82,s61,v82
v_mfma_f32_16x16x32_bf16 a[44:47],a[188:191],a[236:239],a[44:47]
ds_read_b64_tr_b16 a[134:135],v22 offset:18944
v_mul_f32 v83,s61,v83
v_mfma_f32_16x16x32_bf16 a[44:47],a[184:187],a[232:235],a[44:47]
v_cvt_pk_bf16_f32 v97,v98,v99
v_accvgpr_read_b32 v114,a100
v_mfma_f32_16x16x32_bf16 a[44:47],a[180:183],a[204:207],a[44:47]
ds_read_b64_tr_b16 a[136:137],v22 offset:33280
v_accvgpr_read_b32 v115,a101
v_mfma_f32_16x16x32_bf16 a[44:47],a[176:179],a[200:203],a[44:47]
v_accvgpr_read_b32 v84,a40
v_accvgpr_read_b32 v85,a41
v_mfma_f32_16x16x32_bf16 a[76:79],a[176:179],a[208:211],a[76:79]
ds_read_b64_tr_b16 a[138:139],v22 offset:35328
v_mul_f32 v100,s61,v100
v_mfma_f32_16x16x32_bf16 a[76:79],a[180:183],a[212:215],a[76:79]
v_mul_f32 v101,s61,v101
v_accvgpr_read_b32 v102,a74
v_mfma_f32_16x16x32_bf16 a[76:79],a[184:187],a[240:243],a[76:79]
ds_read_b64_tr_b16 a[140:141],v22 offset:49664
ds_read_b64_tr_b16 a[142:143],v22 offset:51712
v_mfma_f32_16x16x32_bf16 a[76:79],a[188:191],a[244:247],a[76:79]
v_mfma_f32_16x16x32_bf16 a[108:111],a[188:191],a[252:255],a[108:111]
buffer_load_dwordx4 v4,s[20:23],s47 offen offset:1024 nt lds
v_accvgpr_read_b32 v103,a75
v_mfma_f32_16x16x32_bf16 a[108:111],a[184:187],a[248:251],a[108:111]
v_cvt_pk_bf16_f32 v81,v82,v83
v_accvgpr_read_b32 v98,a68
v_mfma_f32_16x16x32_bf16 a[108:111],a[180:183],a[220:223],a[108:111]
ds_read_b64_tr_b16 a[144:145],v22 offset:520
v_accvgpr_read_b32 v99,a69
v_mfma_f32_16x16x32_bf16 a[108:111],a[176:179],a[216:219],a[108:111]
s_waitcnt lgkmcnt(4)
v_accvgpr_read_b32 v12,a102
v_accvgpr_read_b32 v13,a103
v_mfma_f32_16x16x32_bf16 a[16:19],a[128:131],a[192:195],a[16:19]
ds_read_b64_tr_b16 a[146:147],v22 offset:2568
v_mul_f32 v114,s61,v114
v_mfma_f32_16x16x32_bf16 a[16:19],a[132:135],a[196:199],a[16:19]
s_waitcnt lgkmcnt(4)
v_mul_f32 v115,s61,v115
v_accvgpr_read_b32 v68,a8
v_mfma_f32_16x16x32_bf16 a[16:19],a[136:139],a[224:227],a[16:19]
s_waitcnt lgkmcnt(2)
ds_read_b64_tr_b16 a[148:149],v22 offset:16904
v_accvgpr_read_b32 v69,a9
v_mfma_f32_16x16x32_bf16 a[16:19],a[140:143],a[228:231],a[16:19]
v_mul_f32 v84,s61,v84
v_mul_f32 v85,s61,v85
v_mfma_f32_16x16x32_bf16 a[48:51],a[140:143],a[236:239],a[48:51]
buffer_load_dwordx4 v5,s[20:23],s47 offen offset:2048 nt lds
ds_read_b64_tr_b16 a[150:151],v22 offset:18952
v_mfma_f32_16x16x32_bf16 a[48:51],a[136:139],a[232:235],a[48:51]
v_accvgpr_read_b32 v86,a42
v_accvgpr_read_b32 v87,a43
v_mfma_f32_16x16x32_bf16 a[48:51],a[132:135],a[204:207],a[48:51]
ds_read_b64_tr_b16 a[152:153],v22 offset:33288
v_cvt_pk_bf16_f32 v100,v100,v101
v_mfma_f32_16x16x32_bf16 a[48:51],a[128:131],a[200:203],a[48:51]
v_mul_f32 v102,s61,v102
v_mul_f32 v103,s61,v103
v_mfma_f32_16x16x32_bf16 a[80:83],a[128:131],a[208:211],a[80:83]
ds_read_b64_tr_b16 a[154:155],v22 offset:35336
v_accvgpr_read_b32 v82,a36
v_mfma_f32_16x16x32_bf16 a[80:83],a[132:135],a[212:215],a[80:83]
v_accvgpr_read_b32 v83,a37
v_accvgpr_read_b32 v16,a70
v_mfma_f32_16x16x32_bf16 a[80:83],a[136:139],a[240:243],a[80:83]
ds_read_b64_tr_b16 a[156:157],v22 offset:49672
ds_read_b64_tr_b16 a[158:159],v22 offset:51720
v_mfma_f32_16x16x32_bf16 a[80:83],a[140:143],a[244:247],a[80:83]
v_mfma_f32_16x16x32_bf16 a[112:115],a[140:143],a[252:255],a[112:115]
v_accvgpr_read_b32 v17,a71
v_mul_f32 v98,s61,v98
v_mfma_f32_16x16x32_bf16 a[112:115],a[136:139],a[248:251],a[112:115]
buffer_load_dwordx4 v5,s[20:23],s47 offen offset:3072 nt lds
v_mul_f32 v99,s61,v99
v_mfma_f32_16x16x32_bf16 a[112:115],a[132:135],a[220:223],a[112:115]
ds_read_b64_tr_b16 a[160:161],v22 offset:768
v_mul_f32 v12,s61,v12
v_mfma_f32_16x16x32_bf16 a[112:115],a[128:131],a[216:219],a[112:115]
s_waitcnt lgkmcnt(4)
v_mul_f32 v13,s61,v13
v_cvt_pk_bf16_f32 v114,v114,v115
v_mfma_f32_16x16x32_bf16 a[20:23],a[144:147],a[192:195],a[20:23]
ds_read_b64_tr_b16 a[162:163],v22 offset:2816
v_mul_f32 v68,s61,v68
v_mfma_f32_16x16x32_bf16 a[20:23],a[148:151],a[196:199],a[20:23]
s_waitcnt lgkmcnt(4)
v_mul_f32 v69,s61,v69
)A"
R"A(v_accvgpr_read_b32 v70,a10
v_mfma_f32_16x16x32_bf16 a[20:23],a[152:155],a[224:227],a[20:23]
s_waitcnt lgkmcnt(2)
ds_read_b64_tr_b16 a[164:165],v22 offset:17152
v_accvgpr_read_b32 v71,a11
v_mfma_f32_16x16x32_bf16 a[20:23],a[156:159],a[228:231],a[20:23]
s_add_u32 m0,0,s52
v_cvt_pk_bf16_f32 v84,v84,v85
v_mul_f32 v86,s61,v86
v_mfma_f32_16x16x32_bf16 a[52:55],a[156:159],a[236:239],a[52:55]
ds_read_b64_tr_b16 a[166:167],v22 offset:19200
v_mul_f32 v87,s61,v87
v_mfma_f32_16x16x32_bf16 a[52:55],a[152:155],a[232:235],a[52:55]
v_cvt_pk_bf16_f32 v101,v102,v103
v_accvgpr_read_b32 v88,a48
v_mfma_f32_16x16x32_bf16 a[52:55],a[148:151],a[204:207],a[52:55]
buffer_load_dwordx4 v4,s[24:27],s47 offen nt lds
ds_read_b64_tr_b16 a[168:169],v22 offset:33536
v_mfma_f32_16x16x32_bf16 a[52:55],a[144:147],a[200:203],a[52:55]
v_accvgpr_read_b32 v89,a49
v_accvgpr_read_b32 v14,a38
v_mfma_f32_16x16x32_bf16 a[84:87],a[144:147],a[208:211],a[84:87]
ds_read_b64_tr_b16 a[170:171],v22 offset:35584
v_accvgpr_read_b32 v15,a39
v_mfma_f32_16x16x32_bf16 a[84:87],a[148:151],a[212:215],a[84:87]
v_mul_f32 v82,s61,v82
v_mul_f32 v83,s61,v83
v_mfma_f32_16x16x32_bf16 a[84:87],a[152:155],a[240:243],a[84:87]
ds_read_b64_tr_b16 a[172:173],v22 offset:49920
ds_read_b64_tr_b16 a[174:175],v22 offset:51968
v_mfma_f32_16x16x32_bf16 a[84:87],a[156:159],a[244:247],a[84:87]
v_mfma_f32_16x16x32_bf16 a[116:119],a[156:159],a[252:255],a[116:119]
v_mul_f32 v16,s61,v16
v_mul_f32 v17,s61,v17
v_mfma_f32_16x16x32_bf16 a[116:119],a[152:155],a[248:251],a[116:119]
v_cvt_pk_bf16_f32 v98,v98,v99
v_cvt_pk_bf16_f32 v115,v12,v13
v_mfma_f32_16x16x32_bf16 a[116:119],a[148:151],a[220:223],a[116:119]
ds_read_b64_tr_b16 a[176:177],v22 offset:776
v_cvt_pk_bf16_f32 v68,v68,v69
v_mfma_f32_16x16x32_bf16 a[116:119],a[144:147],a[216:219],a[116:119]
s_waitcnt lgkmcnt(4)
buffer_load_dwordx4 v5,s[24:27],s47 offen offset:1024 nt lds
v_mul_f32 v70,s61,v70
v_mfma_f32_16x16x32_bf16 a[24:27],a[160:163],a[192:195],a[24:27]
ds_read_b64_tr_b16 a[178:179],v22 offset:2824
v_mul_f32 v71,s61,v71
v_mfma_f32_16x16x32_bf16 a[24:27],a[164:167],a[196:199],a[24:27]
s_waitcnt lgkmcnt(4)
v_cvt_pk_bf16_f32 v85,v86,v87
v_accvgpr_read_b32 v102,a76
v_mfma_f32_16x16x32_bf16 a[24:27],a[168:171],a[224:227],a[24:27]
s_waitcnt lgkmcnt(2)
ds_read_b64_tr_b16 a[180:181],v22 offset:17160
v_accvgpr_read_b32 v103,a77
v_mfma_f32_16x16x32_bf16 a[24:27],a[172:175],a[228:231],a[24:27]
v_accvgpr_read_b32 v72,a16
v_accvgpr_read_b32 v73,a17
v_mfma_f32_16x16x32_bf16 a[56:59],a[172:175],a[236:239],a[56:59]
ds_read_b64_tr_b16 a[182:183],v22 offset:19208
v_mul_f32 v88,s61,v88
v_mfma_f32_16x16x32_bf16 a[56:59],a[168:171],a[232:235],a[56:59]
v_mul_f32 v89,s61,v89
v_accvgpr_read_b32 v90,a50
v_mfma_f32_16x16x32_bf16 a[56:59],a[164:167],a[204:207],a[56:59]
ds_read_b64_tr_b16 a[184:185],v22 offset:33544
v_accvgpr_read_b32 v91,a51
v_mfma_f32_16x16x32_bf16 a[56:59],a[160:163],a[200:203],a[56:59]
v_mul_f32 v14,s61,v14
v_mul_f32 v15,s61,v15
v_mfma_f32_16x16x32_bf16 a[88:91],a[160:163],a[208:211],a[88:91]
ds_read_b64_tr_b16 a[186:187],v22 offset:35592
buffer_load_dwordx4 v4,s[24:27],s47 offen offset:2048 nt lds
v_mfma_f32_16x16x32_bf16 a[88:91],a[164:167],a[212:215],a[88:91]
v_cvt_pk_bf16_f32 v82,v82,v83
v_cvt_pk_bf16_f32 v99,v16,v17
v_mfma_f32_16x16x32_bf16 a[88:91],a[168:171],a[240:243],a[88:91]
ds_read_b64_tr_b16 a[188:189],v22 offset:49928
ds_read_b64_tr_b16 a[190:191],v22 offset:51976
v_mfma_f32_16x16x32_bf16 a[88:91],a[172:175],a[244:247],a[88:91]
v_mfma_f32_16x16x32_bf16 a[120:123],a[172:175],a[252:255],a[120:123]
v_cvt_pk_bf16_f32 v69,v70,v71
v_accvgpr_read_b32 v86,a44
v_mfma_f32_16x16x32_bf16 a[120:123],a[168:171],a[248:251],a[120:123]
v_accvgpr_read_b32 v87,a45
v_accvgpr_read_b32 v12,a78
v_mfma_f32_16x16x32_bf16 a[120:123],a[164:167],a[220:223],a[120:123]
ds_read_b64_tr_b16 a[128:129],v22 offset:33792
v_accvgpr_read_b32 v13,a79
v_mfma_f32_16x16x32_bf16 a[120:123],a[160:163],a[216:219],a[120:123]
s_waitcnt lgkmcnt(4)
v_mul_f32 v102,s61,v102
v_mul_f32 v103,s61,v103
v_mfma_f32_16x16x32_bf16 a[28:31],a[176:179],a[192:195],a[28:31]
ds_read_b64_tr_b16 a[130:131],v22 offset:35840
v_mul_f32 v72,s61,v72
v_mfma_f32_16x16x32_bf16 a[28:31],a[180:183],a[196:199],a[28:31]
s_waitcnt lgkmcnt(4)
buffer_load_dwordx4 v5,s[24:27],s47 offen offset:3072 nt lds
v_mul_f32 v73,s61,v73
v_mfma_f32_16x16x32_bf16 a[28:31],a[184:187],a[224:227],a[28:31]
s_add_u32 s47,s47,16384
s_and_b32 s47,s47,0x3ffff
ds_read_b64_tr_b16 a[132:133],v22 offset:50176
s_waitcnt lgkmcnt(3)
v_mfma_f32_16x16x32_bf16 a[28:31],a[188:191],a[228:231],a[28:31]
s_or_b32 s47,s47,s46
v_accvgpr_read_b32 v74,a18
v_accvgpr_read_b32 v75,a19
v_mfma_f32_16x16x32_bf16 a[60:63],a[188:191],a[236:239],a[60:63]
s_and_b32 s46,s54,0xfffc0000
ds_read_b64_tr_b16 a[134:135],v22 offset:52224
v_cvt_pk_bf16_f32 v88,v88,v89
v_mfma_f32_16x16x32_bf16 a[60:63],a[184:187],a[232:235],a[60:63]
s_add_u32 s54,s54,16384
v_mul_f32 v90,s61,v90
v_mul_f32 v91,s61,v91
v_mfma_f32_16x16x32_bf16 a[60:63],a[180:183],a[204:207],a[60:63]
ds_read_b64_tr_b16 a[136:137],v22 offset:33800
v_cvt_pk_bf16_f32 v83,v14,v15
v_mfma_f32_16x16x32_bf16 a[60:63],a[176:179],a[200:203],a[60:63]
v_accvgpr_read_b32 v70,a12
v_accvgpr_read_b32 v71,a13
v_mfma_f32_16x16x32_bf16 a[92:95],a[176:179],a[208:211],a[92:95]
ds_read_b64_tr_b16 a[138:139],v22 offset:35848
v_accvgpr_read_b32 v16,a46
v_mfma_f32_16x16x32_bf16 a[92:95],a[180:183],a[212:215],a[92:95]
v_accvgpr_read_b32 v17,a47
v_mul_f32 v86,s61,v86
v_mfma_f32_16x16x32_bf16 a[92:95],a[184:187],a[240:243],a[92:95]
ds_read_b64_tr_b16 a[140:141],v22 offset:50184
v_mul_f32 v87,s61,v87
v_mfma_f32_16x16x32_bf16 a[92:95],a[188:191],a[244:247],a[92:95]
v_mul_f32 v12,s61,v12
v_mul_f32 v13,s61,v13
v_mfma_f32_16x16x32_bf16 a[124:127],a[188:191],a[252:255],a[124:127]
ds_read_b64_tr_b16 a[142:143],v22 offset:52232
v_cvt_pk_bf16_f32 v102,v102,v103
v_mfma_f32_16x16x32_bf16 a[124:127],a[184:187],a[248:251],a[124:127]
v_cvt_pk_bf16_f32 v72,v72,v73
v_mul_f32 v74,s61,v74
v_mfma_f32_16x16x32_bf16 a[124:127],a[180:183],a[220:223],a[124:127]
ds_read_b64_tr_b16 a[144:145],v22 offset:34048
v_mul_f32 v75,s61,v75
v_mfma_f32_16x16x32_bf16 a[124:127],a[176:179],a[216:219],a[124:127]
s_waitcnt lgkmcnt(4)
v_cvt_pk_bf16_f32 v89,v90,v91
v_accvgpr_read_b32 v76,a24
v_mfma_f32_16x16x32_bf16 v[128:131],a[128:131],a[224:227],v[128:131]
ds_read_b64_tr_b16 a[146:147],v22 offset:36096
v_accvgpr_read_b32 v77,a25
v_mfma_f32_16x16x32_bf16 v[128:131],a[132:135],a[228:231],v[128:131]
v_accvgpr_read_b32 v14,a14
v_accvgpr_read_b32 v15,a15
v_mfma_f32_16x16x32_bf16 v[136:139],a[132:135],a[236:239],v[136:139]
ds_read_b64_tr_b16 a[148:149],v22 offset:50432
v_mul_f32 v70,s61,v70
v_mfma_f32_16x16x32_bf16 v[136:139],a[128:131],a[232:235],v[136:139]
v_mul_f32 v71,s61,v71
v_mul_f32 v16,s61,v16
v_mfma_f32_16x16x32_bf16 v[144:147],a[128:131],a[240:243],v[144:147]
ds_read_b64_tr_b16 a[150:151],v22 offset:52480
v_mul_f32 v17,s61,v17
v_mfma_f32_16x16x32_bf16 v[144:147],a[132:135],a[244:247],v[144:147]
v_cvt_pk_bf16_f32 v86,v86,v87
v_cvt_pk_bf16_f32 v103,v12,v13
v_mfma_f32_16x16x32_bf16 v[152:155],a[132:135],a[252:255],v[152:155]
ds_read_b64_tr_b16 a[152:153],v22 offset:34056
v_cvt_pk_bf16_f32 v73,v74,v75
v_mfma_f32_16x16x32_bf16 v[152:155],a[128:131],a[248:251],v[152:155]
s_waitcnt lgkmcnt(4)
v_accvgpr_read_b32 v90,a52
v_accvgpr_read_b32 v91,a53
v_mfma_f32_16x16x32_bf16 v[132:135],a[136:139],a[224:227],v[132:135]
ds_read_b64_tr_b16 a[154:155],v22 offset:36104
ds_read_b64_tr_b16 a[156:157],v22 offset:50440
ds_read_b64_tr_b16 a[158:159],v22 offset:52488
ds_read_b64_tr_b16 a[160:161],v22 offset:34304
ds_read_b64_tr_b16 a[162:163],v22 offset:36352
ds_read_b64_tr_b16 a[164:165],v22 offset:50688
ds_read_b64_tr_b16 a[166:167],v22 offset:52736
ds_read_b64_tr_b16 a[168:169],v22 offset:34312
ds_read_b64_tr_b16 a[170:171],v22 offset:36360
ds_read_b64_tr_b16 a[172:173],v22 offset:50696
ds_read_b64_tr_b16 a[174:175],v22 offset:52744
ds_read_b64_tr_b16 a[176:177],v22 offset:34560
ds_read_b64_tr_b16 a[178:179],v22 offset:36608
ds_read_b64_tr_b16 a[180:181],v22 offset:50944
ds_read_b64_tr_b16 a[182:183],v22 offset:52992
ds_read_b64_tr_b16 a[184:185],v22 offset:34568
ds_read_b64_tr_b16 a[186:187],v22 offset:36616
ds_read_b64_tr_b16 a[188:189],v22 offset:50952
ds_read_b64_tr_b16 a[190:191],v22 offset:53000
v_mfma_f32_16x16x32_bf16 v[132:135],a[140:143],a[228:231],v[132:135]
v_mfma_f32_16x16x32_bf16 v[140:143],a[140:143],a[236:239],v[140:143]
v_mfma_f32_16x16x32_bf16 v[140:143],a[136:139],a[232:235],v[140:143]
v_mfma_f32_16x16x32_bf16 v[148:151],a[136:139],a[240:243],v[148:151]
v_mfma_f32_16x16x32_bf16 v[148:151],a[140:143],a[244:247],v[148:151]
v_mfma_f32_16x16x32_bf16 v[156:159],a[140:143],a[252:255],v[156:159]
v_mfma_f32_16x16x32_bf16 v[156:159],a[136:139],a[248:251],v[156:159]
s_waitcnt vmcnt(16) lgkmcnt(0)
s_barrier
v_mfma_f32_16x16x32_bf16 v[160:163],a[144:147],a[224:227],v[160:163]
v_mfma_f32_16x16x32_bf16 v[160:163],a[148:151],a[228:231],v[160:163]
s_add_u32 m0,32768,s51
v_mfma_f32_16x16x32_bf16 v[168:171],a[148:151],a[236:239],v[168:171]
v_mfma_f32_16x16x32_bf16 v[168:171],a[144:147],a[232:235],v[168:171]
v_mfma_f32_16x16x32_bf16 v[176:179],a[144:147],a[240:243],v[176:179]
v_mfma_f32_16x16x32_bf16 v[176:179],a[148:151],a[244:247],v[176:179]
buffer_load_dwordx4 v4,s[20:23],s47 offen nt lds
v_mfma_f32_16x16x32_bf16 v[184:187],a[148:151],a[252:255],v[184:187]
v_mfma_f32_16x16x32_bf16 v[184:187],a[144:147],a[248:251],v[184:187]
v_mfma_f32_16x16x32_bf16 v[164:167],a[152:155],a[224:227],v[164:167]
v_mfma_f32_16x16x32_bf16 v[164:167],a[156:159],a[228:231],v[164:167]
v_mfma_f32_16x16x32_bf16 v[172:175],a[156:159],a[236:239],v[172:175]
v_mfma_f32_16x16x32_bf16 v[172:175],a[152:155],a[232:235],v[172:175]
v_mfma_f32_16x16x32_bf16 v[180:183],a[152:155],a[240:243],v[180:183]
buffer_load_dwordx4 v4,s[20:23],s47 offen offset:1024 nt lds
v_mfma_f32_16x16x32_bf16 v[180:183],a[156:159],a[244:247],v[180:183]
v_mfma_f32_16x16x32_bf16 v[188:191],a[156:159],a[252:255],v[188:191]
v_mfma_f32_16x16x32_bf16 v[188:191],a[152:155],a[248:251],v[188:191]
v_mfma_f32_16x16x32_bf16 v[192:195],a[160:163],a[224:227],v[192:195]
v_mfma_f32_16x16x32_bf16 v[192:195],a[164:167],a[228:231],v[192:195]
v_mfma_f32_16x16x32_bf16 v[200:203],a[164:167],a[236:239],v[200:203]
v_mfma_f32_16x16x32_bf16 v[200:203],a[160:163],a[232:235],v[200:203]
buffer_load_dwordx4 v5,s[20:23],s47 offen offset:2048 nt lds
v_mfma_f32_16x16x32_bf16 v[208:211],a[160:163],a[240:243],v[208:211]
v_mul_f32 v76,s61,v76
v_mul_f32 v77,s61,v77
v_mfma_f32_16x16x32_bf16 v[208:211],a[164:167],a[244:247],v[208:211]
v_accvgpr_read_b32 v78,a26
v_accvgpr_read_b32 v79,a27
v_mfma_f32_16x16x32_bf16 v[216:219],a[164:167],a[252:255],v[216:219]
ds_read_b128 v[48:51],v18
v_mul_f32 v14,s61,v14
v_mfma_f32_16x16x32_bf16 v[216:219],a[160:163],a[248:251],v[216:219]
buffer_load_dwordx4 v5,s[20:23],s47 offen offset:3072 nt lds
v_mul_f32 v15,s61,v15
v_mfma_f32_16x16x32_bf16 v[196:199],a[168:171],a[224:227],v[196:199]
ds_read_b128 v[52:55],v18 offset:256
v_cvt_pk_bf16_f32 v70,v70,v71
v_mfma_f32_16x16x32_bf16 v[196:199],a[172:175],a[228:231],v[196:199]
v_cvt_pk_bf16_f32 v87,v16,v17
v_accvgpr_read_b32 v116,a104
v_mfma_f32_16x16x32_bf16 v[204:207],a[172:175],a[236:239],v[204:207]
ds_read_b128 v[56:59],v18 offset:512
v_accvgpr_read_b32 v117,a105
v_mfma_f32_16x16x32_bf16 v[204:207],a[168:171],a[232:235],v[204:207]
s_add_u32 m0,32768,s52
v_accvgpr_read_b32 v74,a20
v_accvgpr_read_b32 v75,a21
v_mfma_f32_16x16x32_bf16 v[212:215],a[168:171],a[240:243],v[212:215]
ds_read_b128 v[60:63],v18 offset:768
v_accvgpr_read_b32 v12,a54
v_mfma_f32_16x16x32_bf16 v[212:215],a[172:175],a[244:247],v[212:215]
v_accvgpr_read_b32 v13,a55
v_mul_f32 v90,s61,v90
v_mfma_f32_16x16x32_bf16 v[220:223],a[172:175],a[252:255],v[220:223]
buffer_load_dwordx4 v4,s[24:27],s47 offen nt lds
v_mul_f32 v91,s61,v91
v_mfma_f32_16x16x32_bf16 v[220:223],a[168:171],a[248:251],v[220:223]
v_cvt_pk_bf16_f32 v76,v76,v77
v_mul_f32 v78,s61,v78
v_mfma_f32_16x16x32_bf16 v[224:227],a[176:179],a[224:227],v[224:227]
)A"
R"A(v_mul_f32 v79,s61,v79
v_cvt_pk_bf16_f32 v71,v14,v15
v_mfma_f32_16x16x32_bf16 v[224:227],a[180:183],a[228:231],v[224:227]
v_mul_f32 v116,s61,v116
v_mul_f32 v117,s61,v117
v_mfma_f32_16x16x32_bf16 v[232:235],a[180:183],a[236:239],v[232:235]
v_accvgpr_read_b32 v118,a106
v_accvgpr_read_b32 v119,a107
v_mfma_f32_16x16x32_bf16 v[232:235],a[176:179],a[232:235],v[232:235]
v_accvgpr_read_b32 v16,a22
v_accvgpr_read_b32 v17,a23
v_mfma_f32_16x16x32_bf16 v[240:243],a[176:179],a[240:243],v[240:243]
v_mul_f32 v74,s61,v74
v_mul_f32 v75,s61,v75
v_mfma_f32_16x16x32_bf16 v[240:243],a[180:183],a[244:247],v[240:243]
buffer_load_dwordx4 v5,s[24:27],s47 offen offset:1024 nt lds
v_mul_f32 v12,s61,v12
v_mfma_f32_16x16x32_bf16 v[248:251],a[180:183],a[252:255],v[248:251]
v_mul_f32 v13,s61,v13
v_cvt_pk_bf16_f32 v90,v90,v91
v_mfma_f32_16x16x32_bf16 v[248:251],a[176:179],a[248:251],v[248:251]
v_cvt_pk_bf16_f32 v77,v78,v79
v_cvt_pk_bf16_f32 v116,v116,v117
v_mfma_f32_16x16x32_bf16 v[228:231],a[184:187],a[224:227],v[228:231]
v_mul_f32 v118,s61,v118
v_mul_f32 v119,s61,v119
v_mfma_f32_16x16x32_bf16 v[228:231],a[188:191],a[228:231],v[228:231]
v_mul_f32 v16,s61,v16
v_mul_f32 v17,s61,v17
v_mfma_f32_16x16x32_bf16 v[236:239],a[188:191],a[236:239],v[236:239]
v_cvt_pk_bf16_f32 v74,v74,v75
v_cvt_pk_bf16_f32 v91,v12,v13
v_mfma_f32_16x16x32_bf16 v[236:239],a[184:187],a[232:235],v[236:239]
v_accvgpr_read_b32 v78,a28
v_accvgpr_read_b32 v79,a29
v_mfma_f32_16x16x32_bf16 v[244:247],a[184:187],a[240:243],v[244:247]
v_cvt_pk_bf16_f32 v117,v118,v119
v_cvt_pk_bf16_f32 v75,v16,v17
v_mfma_f32_16x16x32_bf16 v[244:247],a[188:191],a[244:247],v[244:247]
v_accvgpr_read_b32 v104,a80
v_accvgpr_read_b32 v105,a81
v_mfma_f32_16x16x32_bf16 v[252:255],a[188:191],a[252:255],v[252:255]
v_accvgpr_read_b32 v12,a30
v_accvgpr_read_b32 v13,a31
v_mfma_f32_16x16x32_bf16 v[252:255],a[184:187],a[248:251],v[252:255]
s_waitcnt lgkmcnt(0)
buffer_load_dwordx4 v4,s[24:27],s47 offen offset:2048 nt lds
v_mul_f32 v78,s61,v78
v_mul_f32 v79,s61,v79
v_mfma_f32_16x16x32_bf16 v[32:35],v[48:51],v[64:67],0
v_accvgpr_read_b32 v118,a108
v_accvgpr_read_b32 v119,a109
v_mul_f32 v104,s61,v104
v_mul_f32 v105,s61,v105
v_accvgpr_read_b32 v106,a82
v_accvgpr_read_b32 v107,a83
v_mul_f32 v12,s61,v12
v_mul_f32 v13,s61,v13
v_cvt_pk_bf16_f32 v78,v78,v79
v_accvgpr_read_b32 v14,a110
v_accvgpr_read_b32 v15,a111
v_mul_f32 v118,s61,v118
v_mul_f32 v119,s61,v119
v_cvt_pk_bf16_f32 v104,v104,v105
v_mul_f32 v106,s61,v106
v_mul_f32 v107,s61,v107
v_cvt_pk_bf16_f32 v79,v12,v13
v_mfma_f32_16x16x32_bf16 v[32:35],v[52:55],v[68:71],v[32:35]
v_mul_f32 v14,s61,v14
v_mul_f32 v15,s61,v15
v_cvt_pk_bf16_f32 v118,v118,v119
v_cvt_pk_bf16_f32 v105,v106,v107
v_accvgpr_read_b32 v92,a56
v_accvgpr_read_b32 v93,a57
v_cvt_pk_bf16_f32 v119,v14,v15
v_accvgpr_read_b32 v106,a84
v_accvgpr_read_b32 v107,a85
v_mul_f32 v92,s61,v92
v_mul_f32 v93,s61,v93
v_accvgpr_read_b32 v94,a58
v_accvgpr_read_b32 v95,a59
v_mfma_f32_16x16x32_bf16 v[32:35],v[56:59],v[72:75],v[32:35]
v_accvgpr_read_b32 v14,a86
v_accvgpr_read_b32 v15,a87
v_mul_f32 v106,s61,v106
v_mul_f32 v107,s61,v107
v_cvt_pk_bf16_f32 v92,v92,v93
v_mul_f32 v94,s61,v94
v_mul_f32 v95,s61,v95
v_mul_f32 v14,s61,v14
v_mul_f32 v15,s61,v15
v_cvt_pk_bf16_f32 v106,v106,v107
v_cvt_pk_bf16_f32 v93,v94,v95
v_mfma_f32_16x16x32_bf16 v[32:35],v[60:63],v[76:79],v[32:35]
v_cvt_pk_bf16_f32 v107,v14,v15
v_accvgpr_read_b32 v94,a60
v_accvgpr_read_b32 v95,a61
v_accvgpr_read_b32 v120,a112
v_accvgpr_read_b32 v121,a113
v_accvgpr_read_b32 v14,a62
v_accvgpr_read_b32 v15,a63
v_mul_f32 v94,s61,v94
v_mul_f32 v95,s61,v95
v_mfma_f32_16x16x32_bf16 v[36:39],v[48:51],v[80:83],0
v_mul_f32 v120,s61,v120
v_mul_f32 v121,s61,v121
v_accvgpr_read_b32 v122,a114
v_accvgpr_read_b32 v123,a115
v_mul_f32 v14,s61,v14
v_mul_f32 v15,s61,v15
v_cvt_pk_bf16_f32 v94,v94,v95
v_cvt_pk_bf16_f32 v120,v120,v121
v_mul_f32 v122,s61,v122
v_mul_f32 v123,s61,v123
v_cvt_pk_bf16_f32 v95,v14,v15
v_mfma_f32_16x16x32_bf16 v[36:39],v[52:55],v[84:87],v[36:39]
v_cvt_pk_bf16_f32 v121,v122,v123
v_accvgpr_read_b32 v108,a88
v_accvgpr_read_b32 v109,a89
v_accvgpr_read_b32 v122,a116
v_accvgpr_read_b32 v123,a117
v_mul_f32 v108,s61,v108
v_mul_f32 v109,s61,v109
v_accvgpr_read_b32 v110,a90
v_accvgpr_read_b32 v111,a91
v_mfma_f32_16x16x32_bf16 v[36:39],v[56:59],v[88:91],v[36:39]
buffer_load_dwordx4 v5,s[24:27],s47 offen offset:3072 nt lds
v_accvgpr_read_b32 v16,a118
v_accvgpr_read_b32 v17,a119
v_mul_f32 v122,s61,v122
v_mul_f32 v123,s61,v123
v_cvt_pk_bf16_f32 v108,v108,v109
v_mul_f32 v110,s61,v110
v_mul_f32 v111,s61,v111
v_mul_f32 v16,s61,v16
v_mul_f32 v17,s61,v17
v_cvt_pk_bf16_f32 v122,v122,v123
v_cvt_pk_bf16_f32 v109,v110,v111
v_mfma_f32_16x16x32_bf16 v[36:39],v[60:63],v[92:95],v[36:39]
ds_read_b128 a[128:131],v1
v_cvt_pk_bf16_f32 v123,v16,v17
v_accvgpr_read_b32 v110,a92
v_accvgpr_read_b32 v111,a93
v_accvgpr_read_b32 v16,a94
v_accvgpr_read_b32 v17,a95
v_mul_f32 v110,s61,v110
v_mul_f32 v111,s61,v111
v_mfma_f32_16x16x32_bf16 v[40:43],v[48:51],v[96:99],0
ds_read_b128 a[132:135],v1 offset:256
v_mul_f32 v16,s61,v16
v_mul_f32 v17,s61,v17
v_cvt_pk_bf16_f32 v110,v110,v111
v_cvt_pk_bf16_f32 v111,v16,v17
v_mfma_f32_16x16x32_bf16 v[40:43],v[52:55],v[100:103],v[40:43]
s_add_u32 m0,65536,s51
ds_read_b128 a[136:139],v1 offset:512
v_accvgpr_read_b32 v124,a120
v_accvgpr_read_b32 v125,a121
v_mul_f32 v124,s61,v124
v_mul_f32 v125,s61,v125
v_accvgpr_read_b32 v126,a122
v_accvgpr_read_b32 v127,a123
v_mfma_f32_16x16x32_bf16 v[40:43],v[56:59],v[104:107],v[40:43]
ds_read_b128 a[140:143],v1 offset:768
v_cvt_pk_bf16_f32 v124,v124,v125
v_mul_f32 v126,s61,v126
v_mul_f32 v127,s61,v127
v_cvt_pk_bf16_f32 v125,v126,v127
v_mfma_f32_16x16x32_bf16 v[40:43],v[60:63],v[108:111],v[40:43]
ds_read_b128 a[156:159],v1 offset:4864
v_accvgpr_read_b32 v126,a124
v_accvgpr_read_b32 v127,a125
v_accvgpr_read_b32 v12,a126
v_accvgpr_read_b32 v13,a127
v_mul_f32 v126,s61,v126
v_mul_f32 v127,s61,v127
v_mfma_f32_16x16x32_bf16 v[44:47],v[48:51],v[112:115],0
s_add_u32 s47,s47,16384
s_and_b32 s47,s47,0x3ffff
ds_read_b128 a[152:155],v1 offset:4608
v_mul_f32 v12,s61,v12
v_mul_f32 v13,s61,v13
v_cvt_pk_bf16_f32 v126,v126,v127
v_max3_f32 v32,v32,v33,v34
v_cvt_pk_bf16_f32 v127,v12,v13
v_max3_f32 v32,v32,v35,s18
v_mfma_f32_16x16x32_bf16 v[44:47],v[52:55],v[116:119],v[44:47]
s_or_b32 s47,s47,s46
buffer_load_dwordx4 v4,s[20:23],s47 offen nt lds
ds_read_b128 a[148:151],v1 offset:4352
v_sub_f32 v35,0,v32
v_sub_f32 v34,0,v32
v_sub_f32 v33,0,v32
v_sub_f32 v32,0,v32
v_mfma_f32_16x16x32_bf16 v[44:47],v[56:59],v[120:123],v[44:47]
ds_read_b128 a[144:147],v1 offset:4096
v_mul_f32 v128,s61,v128
v_mul_f32 v129,s61,v129
v_cvt_pk_bf16_f32 v128,v128,v129
v_mul_f32 v130,s61,v130
v_mul_f32 v131,s61,v131
v_mfma_f32_16x16x32_bf16 v[44:47],v[60:63],v[124:127],v[44:47]
s_waitcnt lgkmcnt(7)
ds_read_b128 a[160:163],v1 offset:8192
v_cvt_pk_bf16_f32 v129,v130,v131
v_mul_f32 v132,s61,v132
v_mul_f32 v133,s61,v133
v_mul_f32 v134,s61,v134
v_mul_f32 v135,s61,v135
v_cvt_pk_bf16_f32 v130,v132,v133
v_cvt_pk_bf16_f32 v131,v134,v135
v_mfma_f32_16x16x32_bf16 v[132:135],a[128:131],v[64:67],v[32:35]
s_waitcnt lgkmcnt(7)
ds_read_b128 a[164:167],v1 offset:8448
v_mfma_f32_16x16x32_bf16 v[132:135],a[132:135],v[68:71],v[132:135]
s_waitcnt lgkmcnt(7)
ds_read_b128 a[168:171],v1 offset:8704
v_mfma_f32_16x16x32_bf16 v[132:135],a[136:139],v[72:75],v[132:135]
s_waitcnt lgkmcnt(7)
ds_read_b128 a[172:175],v1 offset:8960
v_mul_f32 v136,s61,v136
v_mul_f32 v137,s61,v137
v_cvt_pk_bf16_f32 v136,v136,v137
v_mul_f32 v138,s61,v138
v_mul_f32 v139,s61,v139
v_mfma_f32_16x16x32_bf16 v[132:135],a[140:143],v[76:79],v[132:135]
s_waitcnt lgkmcnt(7)
ds_read_b128 a[188:191],v1 offset:13056
v_cvt_pk_bf16_f32 v137,v138,v139
v_mul_f32 v140,s61,v140
v_mul_f32 v141,s61,v141
v_mul_f32 v142,s61,v142
v_mul_f32 v143,s61,v143
v_cvt_pk_bf16_f32 v138,v140,v141
v_cvt_pk_bf16_f32 v139,v142,v143
v_mfma_f32_16x16x32_bf16 v[140:143],a[156:159],v[76:79],v[32:35]
s_waitcnt lgkmcnt(7)
buffer_load_dwordx4 v4,s[20:23],s47 offen offset:1024 nt lds
ds_read_b128 a[184:187],v1 offset:12800
v_mfma_f32_16x16x32_bf16 v[140:143],a[152:155],v[72:75],v[140:143]
s_waitcnt lgkmcnt(7)
ds_read_b128 a[180:183],v1 offset:12544
v_mfma_f32_16x16x32_bf16 v[140:143],a[148:151],v[68:71],v[140:143]
s_waitcnt lgkmcnt(7)
ds_read_b128 a[176:179],v1 offset:12288
v_mul_f32 v144,s61,v144
v_mul_f32 v145,s61,v145
v_cvt_pk_bf16_f32 v144,v144,v145
v_mul_f32 v146,s61,v146
v_mul_f32 v147,s61,v147
v_mfma_f32_16x16x32_bf16 v[140:143],a[144:147],v[64:67],v[140:143]
s_waitcnt lgkmcnt(7)
v_cvt_pk_bf16_f32 v145,v146,v147
v_mul_f32 v148,s61,v148
v_mul_f32 v149,s61,v149
v_mul_f32 v150,s61,v150
v_mul_f32 v151,s61,v151
v_cvt_pk_bf16_f32 v146,v148,v149
v_cvt_pk_bf16_f32 v147,v150,v151
v_mfma_f32_16x16x32_bf16 v[148:151],a[160:163],v[64:67],v[32:35]
s_waitcnt lgkmcnt(6)
v_mfma_f32_16x16x32_bf16 v[148:151],a[164:167],v[68:71],v[148:151]
s_waitcnt lgkmcnt(5)
v_mfma_f32_16x16x32_bf16 v[148:151],a[168:171],v[72:75],v[148:151]
s_waitcnt lgkmcnt(4)
v_mul_f32 v152,s61,v152
v_mul_f32 v153,s61,v153
v_cvt_pk_bf16_f32 v152,v152,v153
v_mul_f32 v154,s61,v154
v_mul_f32 v155,s61,v155
v_mfma_f32_16x16x32_bf16 v[148:151],a[172:175],v[76:79],v[148:151]
s_waitcnt lgkmcnt(3)
buffer_load_dwordx4 v5,s[20:23],s47 offen offset:2048 nt lds
v_cvt_pk_bf16_f32 v153,v154,v155
v_mul_f32 v156,s61,v156
v_mul_f32 v157,s61,v157
v_mul_f32 v158,s61,v158
v_mul_f32 v159,s61,v159
v_cvt_pk_bf16_f32 v154,v156,v157
v_cvt_pk_bf16_f32 v155,v158,v159
v_mfma_f32_16x16x32_bf16 v[156:159],a[188:191],v[76:79],v[32:35]
s_waitcnt lgkmcnt(2)
v_max3_f32 v36,v36,v37,v38
v_max3_f32 v36,v36,v39,s19
v_mfma_f32_16x16x32_bf16 v[156:159],a[184:187],v[72:75],v[156:159]
s_waitcnt lgkmcnt(1)
v_sub_f32 v39,0,v36
v_sub_f32 v38,0,v36
v_sub_f32 v37,0,v36
v_sub_f32 v36,0,v36
v_mfma_f32_16x16x32_bf16 v[156:159],a[180:183],v[68:71],v[156:159]
s_waitcnt lgkmcnt(0)
v_mul_f32 v160,s61,v160
v_mul_f32 v161,s61,v161
v_cvt_pk_bf16_f32 v160,v160,v161
v_mul_f32 v162,s61,v162
v_mul_f32 v163,s61,v163
v_mfma_f32_16x16x32_bf16 v[156:159],a[176:179],v[64:67],v[156:159]
v_cvt_pk_bf16_f32 v161,v162,v163
v_mul_f32 v164,s61,v164
v_mul_f32 v165,s61,v165
v_mul_f32 v166,s61,v166
v_mul_f32 v167,s61,v167
v_cvt_pk_bf16_f32 v162,v164,v165
v_cvt_pk_bf16_f32 v163,v166,v167
v_mfma_f32_16x16x32_bf16 v[164:167],a[128:131],v[80:83],v[36:39]
s_and_b32 s46,s54,0xfffc0000
v_mul_f32 v168,s61,v168
v_mul_f32 v169,s61,v169
v_mfma_f32_16x16x32_bf16 v[164:167],a[132:135],v[84:87],v[164:167]
s_add_u32 s54,s54,16384
buffer_load_dwordx4 v5,s[20:23],s47 offen offset:3072 nt lds
v_cvt_pk_bf16_f32 v168,v168,v169
v_mfma_f32_16x16x32_bf16 v[164:167],a[136:139],v[88:91],v[164:167]
v_mul_f32 v170,s61,v170
v_mul_f32 v171,s61,v171
v_mfma_f32_16x16x32_bf16 v[164:167],a[140:143],v[92:95],v[164:167]
v_cvt_pk_bf16_f32 v169,v170,v171
v_mul_f32 v172,s61,v172
v_mul_f32 v173,s61,v173
v_mul_f32 v174,s61,v174
v_mul_f32 v175,s61,v175
v_cvt_pk_bf16_f32 v170,v172,v173
v_cvt_pk_bf16_f32 v171,v174,v175
v_mfma_f32_16x16x32_bf16 v[172:175],a[156:159],v[92:95],v[36:39]
s_add_u32 m0,65536,s52
v_mul_f32 v176,s61,v176
v_mul_f32 v177,s61,v177
v_mfma_f32_16x16x32_bf16 v[172:175],a[152:155],v[88:91],v[172:175]
v_cvt_pk_bf16_f32 v176,v176,v177
v_mul_f32 v178,s61,v178
v_mfma_f32_16x16x32_bf16 v[172:175],a[148:151],v[84:87],v[172:175]
v_mul_f32 v179,s61,v179
v_cvt_pk_bf16_f32 v177,v178,v179
v_mfma_f32_16x16x32_bf16 v[172:175],a[144:147],v[80:83],v[172:175]
v_mul_f32 v180,s61,v180
v_mul_f32 v181,s61,v181
v_mul_f32 v182,s61,v182
v_mul_f32 v183,s61,v183
v_cvt_pk_bf16_f32 v178,v180,v181
v_cvt_pk_bf16_f32 v179,v182,v183
v_mfma_f32_16x16x32_bf16 v[180:183],a[160:163],v[80:83],v[36:39]
buffer_load_dwordx4 v4,s[24:27],s47 offen nt lds
v_mul_f32 v184,s61,v184
v_mfma_f32_16x16x32_bf16 v[180:183],a[164:167],v[84:87],v[180:183]
v_mul_f32 v185,s61,v185
v_cvt_pk_bf16_f32 v184,v184,v185
v_mfma_f32_16x16x32_bf16 v[180:183],a[168:171],v[88:91],v[180:183]
v_mul_f32 v186,s61,v186
v_mul_f32 v187,s61,v187
v_mfma_f32_16x16x32_bf16 v[180:183],a[172:175],v[92:95],v[180:183]
v_cvt_pk_bf16_f32 v185,v186,v187
v_mul_f32 v188,s61,v188
)A"
R"A(v_mul_f32 v189,s61,v189
v_mul_f32 v190,s61,v190
v_mul_f32 v191,s61,v191
v_cvt_pk_bf16_f32 v186,v188,v189
v_cvt_pk_bf16_f32 v187,v190,v191
v_mfma_f32_16x16x32_bf16 v[188:191],a[188:191],v[92:95],v[36:39]
v_max3_f32 v40,v40,v41,v42
v_max3_f32 v40,v40,v43,s100
v_mfma_f32_16x16x32_bf16 v[188:191],a[184:187],v[88:91],v[188:191]
v_sub_f32 v43,0,v40
v_sub_f32 v42,0,v40
v_sub_f32 v41,0,v40
v_sub_f32 v40,0,v40
v_mfma_f32_16x16x32_bf16 v[188:191],a[180:183],v[84:87],v[188:191]
v_mul_f32 v192,s61,v192
v_mul_f32 v193,s61,v193
v_cvt_pk_bf16_f32 v192,v192,v193
v_mul_f32 v194,s61,v194
v_mul_f32 v195,s61,v195
v_mfma_f32_16x16x32_bf16 v[188:191],a[176:179],v[80:83],v[188:191]
v_cvt_pk_bf16_f32 v193,v194,v195
v_mul_f32 v196,s61,v196
v_mul_f32 v197,s61,v197
v_mul_f32 v198,s61,v198
v_mul_f32 v199,s61,v199
v_cvt_pk_bf16_f32 v194,v196,v197
v_cvt_pk_bf16_f32 v195,v198,v199
v_mfma_f32_16x16x32_bf16 v[196:199],a[128:131],v[96:99],v[40:43]
buffer_load_dwordx4 v5,s[24:27],s47 offen offset:1024 nt lds
v_mul_f32 v200,s61,v200
v_mfma_f32_16x16x32_bf16 v[196:199],a[132:135],v[100:103],v[196:199]
v_mul_f32 v201,s61,v201
v_cvt_pk_bf16_f32 v200,v200,v201
v_mfma_f32_16x16x32_bf16 v[196:199],a[136:139],v[104:107],v[196:199]
v_mul_f32 v202,s61,v202
v_mul_f32 v203,s61,v203
v_mfma_f32_16x16x32_bf16 v[196:199],a[140:143],v[108:111],v[196:199]
v_cvt_pk_bf16_f32 v201,v202,v203
v_mul_f32 v204,s61,v204
v_mul_f32 v205,s61,v205
v_mul_f32 v206,s61,v206
v_mul_f32 v207,s61,v207
v_cvt_pk_bf16_f32 v202,v204,v205
v_cvt_pk_bf16_f32 v203,v206,v207
v_mfma_f32_16x16x32_bf16 v[204:207],a[156:159],v[108:111],v[40:43]
v_mul_f32 v208,s61,v208
v_mul_f32 v209,s61,v209
v_mfma_f32_16x16x32_bf16 v[204:207],a[152:155],v[104:107],v[204:207]
v_cvt_pk_bf16_f32 v208,v208,v209
v_mul_f32 v210,s61,v210
v_mfma_f32_16x16x32_bf16 v[204:207],a[148:151],v[100:103],v[204:207]
v_mul_f32 v211,s61,v211
v_cvt_pk_bf16_f32 v209,v210,v211
v_mfma_f32_16x16x32_bf16 v[204:207],a[144:147],v[96:99],v[204:207]
buffer_load_dwordx4 v4,s[24:27],s47 offen offset:2048 nt lds
ds_read_b64_tr_b16 a[192:193],v2
ds_read_b64_tr_b16 a[194:195],v2 offset:4096
v_mul_f32 v212,s61,v212
v_mul_f32 v213,s61,v213
v_mul_f32 v214,s61,v214
v_mul_f32 v215,s61,v215
v_cvt_pk_bf16_f32 v210,v212,v213
v_cvt_pk_bf16_f32 v211,v214,v215
v_mfma_f32_16x16x32_bf16 v[212:215],a[160:163],v[96:99],v[40:43]
ds_read_b64_tr_b16 a[196:197],v2 offset:8192
ds_read_b64_tr_b16 a[198:199],v2 offset:12288
v_mfma_f32_16x16x32_bf16 v[212:215],a[164:167],v[100:103],v[212:215]
ds_read_b64_tr_b16 a[204:205],v3 offset:8192
ds_read_b64_tr_b16 a[206:207],v3 offset:12288
v_mfma_f32_16x16x32_bf16 v[212:215],a[168:171],v[104:107],v[212:215]
ds_read_b64_tr_b16 a[200:201],v3
ds_read_b64_tr_b16 a[202:203],v3 offset:4096
v_mul_f32 v216,s61,v216
v_mul_f32 v217,s61,v217
v_cvt_pk_bf16_f32 v216,v216,v217
v_mul_f32 v218,s61,v218
v_mul_f32 v219,s61,v219
v_mfma_f32_16x16x32_bf16 v[212:215],a[172:175],v[108:111],v[212:215]
ds_read_b64_tr_b16 a[208:209],v2 offset:256
ds_read_b64_tr_b16 a[210:211],v2 offset:4352
v_cvt_pk_bf16_f32 v217,v218,v219
v_mul_f32 v220,s61,v220
v_mul_f32 v221,s61,v221
v_mul_f32 v222,s61,v222
v_mul_f32 v223,s61,v223
v_cvt_pk_bf16_f32 v218,v220,v221
v_cvt_pk_bf16_f32 v219,v222,v223
v_exp_f32 v132,v132
v_exp_f32 v133,v133
v_mfma_f32_16x16x32_bf16 v[220:223],a[188:191],v[108:111],v[40:43]
ds_read_b64_tr_b16 a[212:213],v2 offset:8448
ds_read_b64_tr_b16 a[214:215],v2 offset:12544
v_exp_f32 v134,v134
v_exp_f32 v135,v135
v_cvt_pk_bf16_f32 v132,v132,v133
v_exp_f32 v140,v140
v_exp_f32 v141,v141
v_exp_f32 v142,v142
v_exp_f32 v143,v143
v_exp_f32 v148,v148
v_exp_f32 v149,v149
v_mfma_f32_16x16x32_bf16 v[220:223],a[184:187],v[104:107],v[220:223]
ds_read_b64_tr_b16 a[220:221],v3 offset:8448
ds_read_b64_tr_b16 a[222:223],v3 offset:12544
v_cvt_pk_bf16_f32 v133,v134,v135
v_exp_f32 v150,v150
v_exp_f32 v151,v151
v_cvt_pk_bf16_f32 v134,v140,v141
v_cvt_pk_bf16_f32 v135,v142,v143
v_cvt_pk_bf16_f32 v148,v148,v149
v_exp_f32 v156,v156
v_exp_f32 v157,v157
v_exp_f32 v158,v158
v_exp_f32 v159,v159
v_mfma_f32_16x16x32_bf16 v[220:223],a[180:183],v[100:103],v[220:223]
buffer_load_dwordx4 v5,s[24:27],s47 offen offset:3072 nt lds
ds_read_b64_tr_b16 a[216:217],v3 offset:256
ds_read_b64_tr_b16 a[218:219],v3 offset:4352
v_cvt_pk_bf16_f32 v149,v150,v151
v_cvt_pk_bf16_f32 v150,v156,v157
v_cvt_pk_bf16_f32 v151,v158,v159
v_mfma_f32_16x16x32_bf16 v[220:223],a[176:179],v[96:99],v[220:223]
s_waitcnt lgkmcnt(14)
ds_read_b64_tr_b16 a[224:225],v2 offset:512
ds_read_b64_tr_b16 a[226:227],v2 offset:4608
v_mfma_f32_16x16x32_bf16 a[0:3],a[192:195],v[132:135],0
s_waitcnt lgkmcnt(14)
ds_read_b64_tr_b16 a[228:229],v2 offset:8704
ds_read_b64_tr_b16 a[230:231],v2 offset:12800
v_mfma_f32_16x16x32_bf16 a[0:3],a[196:199],v[148:151],a[0:3]
s_waitcnt lgkmcnt(14)
ds_read_b64_tr_b16 a[236:237],v3 offset:8704
ds_read_b64_tr_b16 a[238:239],v3 offset:12800
v_mfma_f32_16x16x32_bf16 a[4:7],a[204:207],v[148:151],0
s_waitcnt lgkmcnt(14)
ds_read_b64_tr_b16 a[232:233],v3 offset:512
ds_read_b64_tr_b16 a[234:235],v3 offset:4608
v_mfma_f32_16x16x32_bf16 a[4:7],a[200:203],v[132:135],a[4:7]
s_waitcnt lgkmcnt(14)
ds_read_b64_tr_b16 a[240:241],v2 offset:768
ds_read_b64_tr_b16 a[242:243],v2 offset:4864
v_mfma_f32_16x16x32_bf16 a[8:11],a[208:211],v[132:135],0
s_waitcnt lgkmcnt(14)
ds_read_b64_tr_b16 a[244:245],v2 offset:8960
ds_read_b64_tr_b16 a[246:247],v2 offset:13056
v_mfma_f32_16x16x32_bf16 a[8:11],a[212:215],v[148:151],a[8:11]
s_waitcnt lgkmcnt(14)
ds_read_b64_tr_b16 a[252:253],v3 offset:8960
ds_read_b64_tr_b16 a[254:255],v3 offset:13056
v_mfma_f32_16x16x32_bf16 a[12:15],a[220:223],v[148:151],0
s_waitcnt lgkmcnt(14)
ds_read_b64_tr_b16 a[248:249],v3 offset:768
ds_read_b64_tr_b16 a[250:251],v3 offset:4864
v_mfma_f32_16x16x32_bf16 a[12:15],a[216:219],v[132:135],a[12:15]
s_waitcnt lgkmcnt(14)
v_mfma_f32_16x16x32_bf16 a[16:19],a[224:227],v[132:135],0
s_waitcnt lgkmcnt(12)
v_mfma_f32_16x16x32_bf16 a[16:19],a[228:231],v[148:151],a[16:19]
s_waitcnt lgkmcnt(10)
v_mfma_f32_16x16x32_bf16 a[20:23],a[236:239],v[148:151],0
s_waitcnt lgkmcnt(8)
v_mfma_f32_16x16x32_bf16 a[20:23],a[232:235],v[132:135],a[20:23]
s_waitcnt lgkmcnt(6)
v_mfma_f32_16x16x32_bf16 a[24:27],a[240:243],v[132:135],0
s_waitcnt lgkmcnt(4)
v_mfma_f32_16x16x32_bf16 a[24:27],a[244:247],v[148:151],a[24:27]
s_waitcnt lgkmcnt(2)
v_mfma_f32_16x16x32_bf16 a[28:31],a[252:255],v[148:151],0
s_waitcnt lgkmcnt(0)
v_mfma_f32_16x16x32_bf16 a[28:31],a[248:251],v[132:135],a[28:31]
s_add_u32 s47,s47,16384
v_max3_f32 v44,v44,v45,v46
v_max3_f32 v44,v44,v47,s101
v_mfma_f32_16x16x32_bf16 v[48:51],v[28:31],v[132:135],0
v_sub_f32 v47,0,v44
v_sub_f32 v46,0,v44
v_sub_f32 v45,0,v44
v_sub_f32 v44,0,v44
v_mfma_f32_16x16x32_bf16 v[48:51],v[28:31],v[148:151],v[48:51]
v_mul_f32 v224,s61,v224
v_mul_f32 v225,s61,v225
v_cvt_pk_bf16_f32 v224,v224,v225
v_mul_f32 v226,s61,v226
v_mul_f32 v227,s61,v227
s_waitcnt vmcnt(24)
s_barrier
v_cvt_pk_bf16_f32 v225,v226,v227
v_mul_f32 v228,s61,v228
v_mul_f32 v229,s61,v229
v_mul_f32 v230,s61,v230
v_mul_f32 v231,s61,v231
v_cvt_pk_bf16_f32 v226,v228,v229
v_cvt_pk_bf16_f32 v227,v230,v231
v_mfma_f32_16x16x32_bf16 v[228:231],a[128:131],v[112:115],v[44:47]
s_add_u32 m0,s49,s51
v_mul_f32 v232,s61,v232
v_mul_f32 v233,s61,v233
v_mfma_f32_16x16x32_bf16 v[228:231],a[132:135],v[116:119],v[228:231]
s_and_b32 s47,s47,0x3ffff
v_cvt_pk_bf16_f32 v232,v232,v233
v_mul_f32 v234,s61,v234
v_mfma_f32_16x16x32_bf16 v[228:231],a[136:139],v[120:123],v[228:231]
s_or_b32 s47,s47,s46
v_mul_f32 v235,s61,v235
v_cvt_pk_bf16_f32 v233,v234,v235
v_mfma_f32_16x16x32_bf16 v[228:231],a[140:143],v[124:127],v[228:231]
v_mul_f32 v236,s61,v236
v_mul_f32 v237,s61,v237
v_mul_f32 v238,s61,v238
v_mul_f32 v239,s61,v239
v_cvt_pk_bf16_f32 v234,v236,v237
v_cvt_pk_bf16_f32 v235,v238,v239
v_mfma_f32_16x16x32_bf16 v[236:239],a[156:159],v[124:127],v[44:47]
s_and_b32 s46,s54,0xfffc0000
buffer_load_dwordx4 v4,s[20:23],s47 offen nt lds
v_mul_f32 v240,s61,v240
v_mfma_f32_16x16x32_bf16 v[236:239],a[152:155],v[120:123],v[236:239]
v_mul_f32 v241,s61,v241
v_cvt_pk_bf16_f32 v240,v240,v241
v_mfma_f32_16x16x32_bf16 v[236:239],a[148:151],v[116:119],v[236:239]
v_mul_f32 v242,s61,v242
v_mul_f32 v243,s61,v243
v_mfma_f32_16x16x32_bf16 v[236:239],a[144:147],v[112:115],v[236:239]
v_cvt_pk_bf16_f32 v241,v242,v243
v_mul_f32 v244,s61,v244
v_mul_f32 v245,s61,v245
v_mul_f32 v246,s61,v246
v_mul_f32 v247,s61,v247
v_cvt_pk_bf16_f32 v242,v244,v245
v_cvt_pk_bf16_f32 v243,v246,v247
v_mfma_f32_16x16x32_bf16 v[244:247],a[160:163],v[112:115],v[44:47]
v_mul_f32 v248,s61,v248
v_mul_f32 v249,s61,v249
v_mfma_f32_16x16x32_bf16 v[244:247],a[164:167],v[116:119],v[244:247]
v_cvt_pk_bf16_f32 v248,v248,v249
v_mul_f32 v250,s61,v250
v_mfma_f32_16x16x32_bf16 v[244:247],a[168:171],v[120:123],v[244:247]
v_mul_f32 v251,s61,v251
v_cvt_pk_bf16_f32 v249,v250,v251
v_mfma_f32_16x16x32_bf16 v[244:247],a[172:175],v[124:127],v[244:247]
v_mul_f32 v252,s61,v252
v_mul_f32 v253,s61,v253
v_mul_f32 v254,s61,v254
v_mul_f32 v255,s61,v255
v_cvt_pk_bf16_f32 v250,v252,v253
v_cvt_pk_bf16_f32 v251,v254,v255
v_exp_f32 v164,v164
v_exp_f32 v165,v165
v_mfma_f32_16x16x32_bf16 v[252:255],a[188:191],v[124:127],v[44:47]
v_exp_f32 v166,v166
v_exp_f32 v167,v167
v_cvt_pk_bf16_f32 v164,v164,v165
v_exp_f32 v172,v172
v_exp_f32 v173,v173
v_exp_f32 v174,v174
v_exp_f32 v175,v175
v_exp_f32 v180,v180
v_exp_f32 v181,v181
v_mfma_f32_16x16x32_bf16 v[252:255],a[184:187],v[120:123],v[252:255]
v_cvt_pk_bf16_f32 v165,v166,v167
v_exp_f32 v182,v182
v_exp_f32 v183,v183
v_cvt_pk_bf16_f32 v166,v172,v173
v_cvt_pk_bf16_f32 v167,v174,v175
v_cvt_pk_bf16_f32 v180,v180,v181
v_exp_f32 v188,v188
v_exp_f32 v189,v189
v_exp_f32 v190,v190
v_exp_f32 v191,v191
v_mfma_f32_16x16x32_bf16 v[252:255],a[180:183],v[116:119],v[252:255]
v_cvt_pk_bf16_f32 v181,v182,v183
v_cvt_pk_bf16_f32 v182,v188,v189
v_cvt_pk_bf16_f32 v183,v190,v191
v_mfma_f32_16x16x32_bf16 v[252:255],a[176:179],v[112:115],v[252:255]
v_add_u32 v1,s48,v1
v_add_u32 v2,s48,v2
v_mfma_f32_16x16x32_bf16 a[32:35],a[192:195],v[164:167],0
buffer_load_dwordx4 v4,s[20:23],s47 offen offset:1024 nt lds
ds_read_b128 a[128:131],v1
v_mfma_f32_16x16x32_bf16 a[32:35],a[196:199],v[180:183],a[32:35]
v_exp_f32 v196,v196
v_mfma_f32_16x16x32_bf16 a[36:39],a[204:207],v[180:183],0
ds_read_b128 a[132:135],v1 offset:256
v_add_u32 v3,s48,v3
v_mfma_f32_16x16x32_bf16 a[36:39],a[200:203],v[164:167],a[36:39]
s_sub_u32 s48,32768,s62
v_exp_f32 v197,v197
v_mfma_f32_16x16x32_bf16 a[40:43],a[208:211],v[164:167],0
ds_read_b128 a[136:139],v1 offset:512
v_cvt_pk_bf16_f32 v196,v196,v197
v_mfma_f32_16x16x32_bf16 a[40:43],a[212:215],v[180:183],a[40:43]
v_exp_f32 v198,v198
v_mfma_f32_16x16x32_bf16 a[44:47],a[220:223],v[180:183],0
ds_read_b128 a[140:143],v1 offset:768
v_mfma_f32_16x16x32_bf16 a[44:47],a[216:219],v[164:167],a[44:47]
v_exp_f32 v199,v199
v_mfma_f32_16x16x32_bf16 a[48:51],a[224:227],v[164:167],0
ds_read_b128 a[156:159],v1 offset:4864
v_cvt_pk_bf16_f32 v197,v198,v199
v_mfma_f32_16x16x32_bf16 a[48:51],a[228:231],v[180:183],a[48:51]
v_exp_f32 v204,v204
v_mfma_f32_16x16x32_bf16 a[52:55],a[236:239],v[180:183],0
ds_read_b128 a[152:155],v1 offset:4608
v_mfma_f32_16x16x32_bf16 a[52:55],a[232:235],v[164:167],a[52:55]
v_exp_f32 v205,v205
v_mfma_f32_16x16x32_bf16 a[56:59],a[240:243],v[164:167],0
ds_read_b128 a[148:151],v1 offset:4352
v_cvt_pk_bf16_f32 v198,v204,v205
v_mfma_f32_16x16x32_bf16 a[56:59],a[244:247],v[180:183],a[56:59]
v_exp_f32 v206,v206
v_mfma_f32_16x16x32_bf16 a[60:63],a[252:255],v[180:183],0
ds_read_b128 a[144:147],v1 offset:4096
buffer_load_dwordx4 v5,s[20:23],s47 offen offset:2048 nt lds
v_mfma_f32_16x16x32_bf16 a[60:63],a[248:251],v[164:167],a[60:63]
v_exp_f32 v207,v207
v_mfma_f32_16x16x32_bf16 v[52:55],v[28:31],v[164:167],0
ds_read_b128 a[160:163],v1 offset:8192
v_cvt_pk_bf16_f32 v199,v206,v207
v_mfma_f32_16x16x32_bf16 v[52:55],v[28:31],v[180:183],v[52:55]
s_waitcnt lgkmcnt(4)
v_exp_f32 v212,v212
v_mfma_f32_16x16x32_bf16 v[132:135],a[128:131],v[64:67],v[32:35]
ds_read_b128 a[164:167],v1 offset:8448
v_mfma_f32_16x16x32_bf16 v[132:135],a[132:135],v[68:71],v[132:135]
)A"
R"A(ds_read_b128 a[168:171],v1 offset:8704
v_mfma_f32_16x16x32_bf16 v[132:135],a[136:139],v[72:75],v[132:135]
ds_read_b128 a[172:175],v1 offset:8960
v_mfma_f32_16x16x32_bf16 v[132:135],a[140:143],v[76:79],v[132:135]
ds_read_b128 a[188:191],v1 offset:13056
v_mfma_f32_16x16x32_bf16 v[140:143],a[156:159],v[76:79],v[32:35]
s_waitcnt lgkmcnt(5)
ds_read_b128 a[184:187],v1 offset:12800
v_mfma_f32_16x16x32_bf16 v[140:143],a[152:155],v[72:75],v[140:143]
ds_read_b128 a[180:183],v1 offset:12544
v_mfma_f32_16x16x32_bf16 v[140:143],a[148:151],v[68:71],v[140:143]
ds_read_b128 a[176:179],v1 offset:12288
v_mfma_f32_16x16x32_bf16 v[140:143],a[144:147],v[64:67],v[140:143]
s_waitcnt lgkmcnt(7)
v_mfma_f32_16x16x32_bf16 v[148:151],a[160:163],v[64:67],v[32:35]
s_waitcnt lgkmcnt(6)
v_mfma_f32_16x16x32_bf16 v[148:151],a[164:167],v[68:71],v[148:151]
s_waitcnt lgkmcnt(5)
v_mfma_f32_16x16x32_bf16 v[148:151],a[168:171],v[72:75],v[148:151]
s_waitcnt lgkmcnt(4)
v_exp_f32 v213,v213
v_mfma_f32_16x16x32_bf16 v[148:151],a[172:175],v[76:79],v[148:151]
s_waitcnt lgkmcnt(3)
v_exp_f32 v214,v214
v_mfma_f32_16x16x32_bf16 v[156:159],a[188:191],v[76:79],v[32:35]
s_waitcnt lgkmcnt(2)
v_exp_f32 v215,v215
v_mfma_f32_16x16x32_bf16 v[156:159],a[184:187],v[72:75],v[156:159]
s_waitcnt lgkmcnt(1)
buffer_load_dwordx4 v5,s[20:23],s47 offen offset:3072 nt lds
v_cvt_pk_bf16_f32 v212,v212,v213
v_exp_f32 v220,v220
v_exp_f32 v221,v221
v_exp_f32 v222,v222
v_exp_f32 v223,v223
v_mfma_f32_16x16x32_bf16 v[156:159],a[180:183],v[68:71],v[156:159]
s_waitcnt lgkmcnt(0)
v_cvt_pk_bf16_f32 v213,v214,v215
v_cvt_pk_bf16_f32 v214,v220,v221
v_cvt_pk_bf16_f32 v215,v222,v223
v_mfma_f32_16x16x32_bf16 v[156:159],a[176:179],v[64:67],v[156:159]
v_exp_f32 v228,v228
v_mfma_f32_16x16x32_bf16 a[64:67],a[192:195],v[196:199],0
v_exp_f32 v229,v229
v_mfma_f32_16x16x32_bf16 a[64:67],a[196:199],v[212:215],a[64:67]
v_exp_f32 v230,v230
v_mfma_f32_16x16x32_bf16 a[68:71],a[204:207],v[212:215],0
v_exp_f32 v231,v231
v_mfma_f32_16x16x32_bf16 a[68:71],a[200:203],v[196:199],a[68:71]
v_cvt_pk_bf16_f32 v228,v228,v229
v_cvt_pk_bf16_f32 v229,v230,v231
v_mfma_f32_16x16x32_bf16 a[72:75],a[208:211],v[196:199],0
v_exp_f32 v236,v236
v_mfma_f32_16x16x32_bf16 a[72:75],a[212:215],v[212:215],a[72:75]
s_add_u32 m0,s49,s52
v_exp_f32 v237,v237
v_mfma_f32_16x16x32_bf16 a[76:79],a[220:223],v[212:215],0
s_mov_b32 s49,s50
v_exp_f32 v238,v238
v_mfma_f32_16x16x32_bf16 a[76:79],a[216:219],v[196:199],a[76:79]
s_add_u32 s50,s50,s48
v_exp_f32 v239,v239
v_mfma_f32_16x16x32_bf16 a[80:83],a[224:227],v[196:199],0
buffer_load_dwordx4 v4,s[24:27],s47 offen nt lds
v_cvt_pk_bf16_f32 v230,v236,v237
v_mfma_f32_16x16x32_bf16 a[80:83],a[228:231],v[212:215],a[80:83]
v_exp_f32 v244,v244
v_mfma_f32_16x16x32_bf16 a[84:87],a[236:239],v[212:215],0
v_exp_f32 v245,v245
v_mfma_f32_16x16x32_bf16 a[84:87],a[232:235],v[196:199],a[84:87]
v_exp_f32 v246,v246
v_mfma_f32_16x16x32_bf16 a[88:91],a[240:243],v[196:199],0
v_exp_f32 v247,v247
v_mfma_f32_16x16x32_bf16 a[88:91],a[244:247],v[212:215],a[88:91]
v_cvt_pk_bf16_f32 v231,v238,v239
v_cvt_pk_bf16_f32 v244,v244,v245
v_mfma_f32_16x16x32_bf16 a[92:95],a[252:255],v[212:215],0
v_exp_f32 v252,v252
v_mfma_f32_16x16x32_bf16 a[92:95],a[248:251],v[196:199],a[92:95]
v_exp_f32 v253,v253
v_mfma_f32_16x16x32_bf16 v[56:59],v[28:31],v[196:199],0
v_exp_f32 v254,v254
v_mfma_f32_16x16x32_bf16 v[56:59],v[28:31],v[212:215],v[56:59]
v_exp_f32 v255,v255
v_mfma_f32_16x16x32_bf16 v[164:167],a[128:131],v[80:83],v[36:39]
v_cvt_pk_bf16_f32 v245,v246,v247
v_cvt_pk_bf16_f32 v246,v252,v253
v_mfma_f32_16x16x32_bf16 v[164:167],a[132:135],v[84:87],v[164:167]
v_cvt_pk_bf16_f32 v247,v254,v255
v_add_u32 v1,s48,v1
v_mfma_f32_16x16x32_bf16 v[164:167],a[136:139],v[88:91],v[164:167]
v_exp_f32 v132,v132
v_mfma_f32_16x16x32_bf16 v[164:167],a[140:143],v[92:95],v[164:167]
v_exp_f32 v133,v133
v_mfma_f32_16x16x32_bf16 v[172:175],a[156:159],v[92:95],v[36:39]
buffer_load_dwordx4 v5,s[24:27],s47 offen offset:1024 nt lds
v_cvt_pk_bf16_f32 v132,v132,v133
v_mfma_f32_16x16x32_bf16 v[172:175],a[152:155],v[88:91],v[172:175]
v_exp_f32 v134,v134
v_mfma_f32_16x16x32_bf16 v[172:175],a[148:151],v[84:87],v[172:175]
v_exp_f32 v135,v135
v_mfma_f32_16x16x32_bf16 v[172:175],a[144:147],v[80:83],v[172:175]
v_exp_f32 v140,v140
v_mfma_f32_16x16x32_bf16 v[180:183],a[160:163],v[80:83],v[36:39]
v_exp_f32 v141,v141
v_mfma_f32_16x16x32_bf16 v[180:183],a[164:167],v[84:87],v[180:183]
v_exp_f32 v142,v142
v_mfma_f32_16x16x32_bf16 v[180:183],a[168:171],v[88:91],v[180:183]
v_exp_f32 v143,v143
v_mfma_f32_16x16x32_bf16 v[180:183],a[172:175],v[92:95],v[180:183]
v_exp_f32 v148,v148
v_mfma_f32_16x16x32_bf16 v[188:191],a[188:191],v[92:95],v[36:39]
v_exp_f32 v149,v149
v_mfma_f32_16x16x32_bf16 v[188:191],a[184:187],v[88:91],v[188:191]
v_cvt_pk_bf16_f32 v133,v134,v135
v_cvt_pk_bf16_f32 v134,v140,v141
v_mfma_f32_16x16x32_bf16 v[188:191],a[180:183],v[84:87],v[188:191]
v_exp_f32 v150,v150
v_mfma_f32_16x16x32_bf16 v[188:191],a[176:179],v[80:83],v[188:191]
v_exp_f32 v151,v151
v_mfma_f32_16x16x32_bf16 a[96:99],a[192:195],v[228:231],0
ds_read_b64_tr_b16 a[192:193],v2
ds_read_b64_tr_b16 a[194:195],v2 offset:4096
v_mfma_f32_16x16x32_bf16 a[96:99],a[196:199],v[244:247],a[96:99]
ds_read_b64_tr_b16 a[196:197],v2 offset:8192
v_cvt_pk_bf16_f32 v135,v142,v143
v_mfma_f32_16x16x32_bf16 a[100:103],a[204:207],v[244:247],0
buffer_load_dwordx4 v4,s[24:27],s47 offen offset:2048 nt lds
ds_read_b64_tr_b16 a[198:199],v2 offset:12288
v_mfma_f32_16x16x32_bf16 a[100:103],a[200:203],v[228:231],a[100:103]
ds_read_b64_tr_b16 a[204:205],v3 offset:8192
v_cvt_pk_bf16_f32 v148,v148,v149
v_mfma_f32_16x16x32_bf16 a[104:107],a[208:211],v[228:231],0
ds_read_b64_tr_b16 a[206:207],v3 offset:12288
ds_read_b64_tr_b16 a[200:201],v3
v_mfma_f32_16x16x32_bf16 a[104:107],a[212:215],v[244:247],a[104:107]
ds_read_b64_tr_b16 a[202:203],v3 offset:4096
v_cvt_pk_bf16_f32 v149,v150,v151
v_mfma_f32_16x16x32_bf16 a[108:111],a[220:223],v[244:247],0
ds_read_b64_tr_b16 a[208:209],v2 offset:256
v_mfma_f32_16x16x32_bf16 a[108:111],a[216:219],v[228:231],a[108:111]
ds_read_b64_tr_b16 a[210:211],v2 offset:4352
v_mfma_f32_16x16x32_bf16 a[112:115],a[224:227],v[228:231],0
ds_read_b64_tr_b16 a[212:213],v2 offset:8448
ds_read_b64_tr_b16 a[214:215],v2 offset:12544
v_mfma_f32_16x16x32_bf16 a[112:115],a[228:231],v[244:247],a[112:115]
v_exp_f32 v156,v156
v_mfma_f32_16x16x32_bf16 a[116:119],a[236:239],v[244:247],0
v_exp_f32 v157,v157
v_mfma_f32_16x16x32_bf16 a[116:119],a[232:235],v[228:231],a[116:119]
v_exp_f32 v158,v158
v_mfma_f32_16x16x32_bf16 a[120:123],a[240:243],v[228:231],0
v_exp_f32 v159,v159
v_mfma_f32_16x16x32_bf16 a[120:123],a[244:247],v[244:247],a[120:123]
ds_read_b64_tr_b16 a[220:221],v3 offset:8448
ds_read_b64_tr_b16 a[222:223],v3 offset:12544
v_mfma_f32_16x16x32_bf16 a[124:127],a[252:255],v[244:247],0
v_cvt_pk_bf16_f32 v150,v156,v157
v_cvt_pk_bf16_f32 v151,v158,v159
v_mfma_f32_16x16x32_bf16 a[124:127],a[248:251],v[228:231],a[124:127]
ds_read_b64_tr_b16 a[216:217],v3 offset:256
ds_read_b64_tr_b16 a[218:219],v3 offset:4352
v_mfma_f32_16x16x32_bf16 v[60:63],v[28:31],v[228:231],0
buffer_load_dwordx4 v5,s[24:27],s47 offen offset:3072 nt lds
ds_read_b64_tr_b16 a[224:225],v2 offset:512
v_mfma_f32_16x16x32_bf16 v[60:63],v[28:31],v[244:247],v[60:63]
s_add_u32 s47,s47,16384
s_and_b32 s47,s47,0x3ffff
ds_read_b64_tr_b16 a[226:227],v2 offset:4608
v_mfma_f32_16x16x32_bf16 v[196:199],a[128:131],v[96:99],v[40:43]
s_or_b32 s47,s47,s46
ds_read_b64_tr_b16 a[228:229],v2 offset:8704
v_mfma_f32_16x16x32_bf16 v[196:199],a[132:135],v[100:103],v[196:199]
ds_read_b64_tr_b16 a[230:231],v2 offset:12800
ds_read_b64_tr_b16 a[236:237],v3 offset:8704
v_mfma_f32_16x16x32_bf16 v[196:199],a[136:139],v[104:107],v[196:199]
ds_read_b64_tr_b16 a[238:239],v3 offset:12800
v_mfma_f32_16x16x32_bf16 v[196:199],a[140:143],v[108:111],v[196:199]
ds_read_b64_tr_b16 a[232:233],v3 offset:512
v_mfma_f32_16x16x32_bf16 v[204:207],a[156:159],v[108:111],v[40:43]
ds_read_b64_tr_b16 a[234:235],v3 offset:4608
v_mfma_f32_16x16x32_bf16 v[204:207],a[152:155],v[104:107],v[204:207]
ds_read_b64_tr_b16 a[240:241],v2 offset:768
ds_read_b64_tr_b16 a[242:243],v2 offset:4864
v_mfma_f32_16x16x32_bf16 v[204:207],a[148:151],v[100:103],v[204:207]
ds_read_b64_tr_b16 a[244:245],v2 offset:8960
v_mfma_f32_16x16x32_bf16 v[204:207],a[144:147],v[96:99],v[204:207]
ds_read_b64_tr_b16 a[246:247],v2 offset:13056
v_mfma_f32_16x16x32_bf16 v[212:215],a[160:163],v[96:99],v[40:43]
ds_read_b64_tr_b16 a[252:253],v3 offset:8960
v_mfma_f32_16x16x32_bf16 v[212:215],a[164:167],v[100:103],v[212:215]
ds_read_b64_tr_b16 a[254:255],v3 offset:13056
ds_read_b64_tr_b16 a[248:249],v3 offset:768
v_mfma_f32_16x16x32_bf16 v[212:215],a[168:171],v[104:107],v[212:215]
ds_read_b64_tr_b16 a[250:251],v3 offset:4864
v_mfma_f32_16x16x32_bf16 v[212:215],a[172:175],v[108:111],v[212:215]
v_exp_f32 v164,v164
v_mfma_f32_16x16x32_bf16 v[220:223],a[188:191],v[108:111],v[40:43]
v_exp_f32 v165,v165
v_mfma_f32_16x16x32_bf16 v[220:223],a[184:187],v[104:107],v[220:223]
v_exp_f32 v180,v180
v_mfma_f32_16x16x32_bf16 v[220:223],a[180:183],v[100:103],v[220:223]
v_exp_f32 v181,v181
v_mfma_f32_16x16x32_bf16 v[220:223],a[176:179],v[96:99],v[220:223]
s_waitcnt lgkmcnt(4)
v_exp_f32 v166,v166
v_mfma_f32_16x16x32_bf16 a[0:3],a[192:195],v[132:135],a[0:3]
v_exp_f32 v167,v167
v_mfma_f32_16x16x32_bf16 a[0:3],a[196:199],v[148:151],a[0:3]
v_exp_f32 v182,v182
v_mfma_f32_16x16x32_bf16 a[4:7],a[204:207],v[148:151],a[4:7]
v_exp_f32 v183,v183
v_mfma_f32_16x16x32_bf16 a[4:7],a[200:203],v[132:135],a[4:7]
v_cvt_pk_bf16_f32 v164,v164,v165
v_cvt_pk_bf16_f32 v180,v180,v181
v_mfma_f32_16x16x32_bf16 a[8:11],a[208:211],v[132:135],a[8:11]
v_exp_f32 v172,v172
v_mfma_f32_16x16x32_bf16 a[8:11],a[212:215],v[148:151],a[8:11]
v_exp_f32 v173,v173
v_mfma_f32_16x16x32_bf16 a[12:15],a[220:223],v[148:151],a[12:15]
v_exp_f32 v174,v174
v_mfma_f32_16x16x32_bf16 a[12:15],a[216:219],v[132:135],a[12:15]
v_exp_f32 v175,v175
v_mfma_f32_16x16x32_bf16 a[16:19],a[224:227],v[132:135],a[16:19]
v_exp_f32 v188,v188
v_mfma_f32_16x16x32_bf16 a[16:19],a[228:231],v[148:151],a[16:19]
v_exp_f32 v189,v189
v_mfma_f32_16x16x32_bf16 a[20:23],a[236:239],v[148:151],a[20:23]
v_exp_f32 v190,v190
v_mfma_f32_16x16x32_bf16 a[20:23],a[232:235],v[132:135],a[20:23]
v_exp_f32 v191,v191
v_mfma_f32_16x16x32_bf16 a[24:27],a[240:243],v[132:135],a[24:27]
v_cvt_pk_bf16_f32 v165,v166,v167
v_cvt_pk_bf16_f32 v181,v182,v183
v_mfma_f32_16x16x32_bf16 a[24:27],a[244:247],v[148:151],a[24:27]
s_waitcnt lgkmcnt(0)
v_cvt_pk_bf16_f32 v166,v172,v173
v_cvt_pk_bf16_f32 v167,v174,v175
v_mfma_f32_16x16x32_bf16 a[28:31],a[252:255],v[148:151],a[28:31]
v_cvt_pk_bf16_f32 v182,v188,v189
v_cvt_pk_bf16_f32 v183,v190,v191
v_mfma_f32_16x16x32_bf16 a[28:31],a[248:251],v[132:135],a[28:31]
v_mfma_f32_16x16x32_bf16 v[48:51],v[28:31],v[132:135],v[48:51]
v_mfma_f32_16x16x32_bf16 v[48:51],v[28:31],v[148:151],v[48:51]
s_mov_b32 s53,13
L_att0_%=:
s_waitcnt vmcnt(24) lgkmcnt(0)
s_barrier
v_mfma_f32_16x16x32_bf16 v[228:231],a[128:131],v[112:115],v[44:47]
s_add_u32 m0,s49,s51
ds_read_b128 a[128:131],v1
v_add_u32 v2,s48,v2
v_mfma_f32_16x16x32_bf16 v[228:231],a[132:135],v[116:119],v[228:231]
s_add_u32 s54,s54,16384
v_exp_f32 v196,v196
v_mfma_f32_16x16x32_bf16 v[228:231],a[136:139],v[120:123],v[228:231]
s_and_b32 s46,s54,0xfffc0000
buffer_load_dwordx4 v4,s[20:23],s47 offen nt lds
ds_read_b128 a[132:135],v1 offset:256
v_mfma_f32_16x16x32_bf16 v[228:231],a[140:143],v[124:127],v[228:231]
v_exp_f32 v197,v197
v_mfma_f32_16x16x32_bf16 v[236:239],a[156:159],v[124:127],v[44:47]
ds_read_b128 a[136:139],v1 offset:512
v_cvt_pk_bf16_f32 v196,v196,v197
v_mfma_f32_16x16x32_bf16 v[236:239],a[152:155],v[120:123],v[236:239]
v_exp_f32 v198,v198
v_mfma_f32_16x16x32_bf16 v[236:239],a[148:151],v[116:119],v[236:239]
ds_read_b128 a[140:143],v1 offset:768
v_add_u32 v3,s48,v3
v_mfma_f32_16x16x32_bf16 v[236:239],a[144:147],v[112:115],v[236:239]
v_exp_f32 v199,v199
v_mfma_f32_16x16x32_bf16 v[244:247],a[160:163],v[112:115],v[44:47]
ds_read_b128 a[156:159],v1 offset:4864
v_cvt_pk_bf16_f32 v197,v198,v199
)A"
R"A(v_mfma_f32_16x16x32_bf16 v[244:247],a[164:167],v[116:119],v[244:247]
v_exp_f32 v204,v204
v_mfma_f32_16x16x32_bf16 v[244:247],a[168:171],v[120:123],v[244:247]
s_cmp_eq_u32 s50,131072
s_cselect_b32 s62,163840,0
ds_read_b128 a[152:155],v1 offset:4608
v_mfma_f32_16x16x32_bf16 v[244:247],a[172:175],v[124:127],v[244:247]
s_sub_u32 s48,32768,s62
v_exp_f32 v205,v205
v_mfma_f32_16x16x32_bf16 v[252:255],a[188:191],v[124:127],v[44:47]
ds_read_b128 a[148:151],v1 offset:4352
v_cvt_pk_bf16_f32 v198,v204,v205
v_mfma_f32_16x16x32_bf16 v[252:255],a[184:187],v[120:123],v[252:255]
v_exp_f32 v206,v206
v_mfma_f32_16x16x32_bf16 v[252:255],a[180:183],v[116:119],v[252:255]
ds_read_b128 a[144:147],v1 offset:4096
v_mfma_f32_16x16x32_bf16 v[252:255],a[176:179],v[112:115],v[252:255]
v_exp_f32 v207,v207
v_mfma_f32_16x16x32_bf16 a[32:35],a[192:195],v[164:167],a[32:35]
buffer_load_dwordx4 v4,s[20:23],s47 offen offset:1024 nt lds
ds_read_b128 a[160:163],v1 offset:8192
v_mfma_f32_16x16x32_bf16 a[32:35],a[196:199],v[180:183],a[32:35]
v_exp_f32 v212,v212
v_mfma_f32_16x16x32_bf16 a[36:39],a[204:207],v[180:183],a[36:39]
ds_read_b128 a[164:167],v1 offset:8448
v_cvt_pk_bf16_f32 v199,v206,v207
v_mfma_f32_16x16x32_bf16 a[36:39],a[200:203],v[164:167],a[36:39]
v_exp_f32 v213,v213
v_mfma_f32_16x16x32_bf16 a[40:43],a[208:211],v[164:167],a[40:43]
ds_read_b128 a[168:171],v1 offset:8704
v_cvt_pk_bf16_f32 v212,v212,v213
v_mfma_f32_16x16x32_bf16 a[40:43],a[212:215],v[180:183],a[40:43]
v_exp_f32 v214,v214
v_mfma_f32_16x16x32_bf16 a[44:47],a[220:223],v[180:183],a[44:47]
ds_read_b128 a[172:175],v1 offset:8960
v_mfma_f32_16x16x32_bf16 a[44:47],a[216:219],v[164:167],a[44:47]
v_exp_f32 v215,v215
v_mfma_f32_16x16x32_bf16 a[48:51],a[224:227],v[164:167],a[48:51]
ds_read_b128 a[188:191],v1 offset:13056
v_cvt_pk_bf16_f32 v213,v214,v215
v_mfma_f32_16x16x32_bf16 a[48:51],a[228:231],v[180:183],a[48:51]
v_exp_f32 v220,v220
v_mfma_f32_16x16x32_bf16 a[52:55],a[236:239],v[180:183],a[52:55]
ds_read_b128 a[184:187],v1 offset:12800
v_mfma_f32_16x16x32_bf16 a[52:55],a[232:235],v[164:167],a[52:55]
v_exp_f32 v221,v221
v_mfma_f32_16x16x32_bf16 a[56:59],a[240:243],v[164:167],a[56:59]
ds_read_b128 a[180:183],v1 offset:12544
v_cvt_pk_bf16_f32 v214,v220,v221
v_mfma_f32_16x16x32_bf16 a[56:59],a[244:247],v[180:183],a[56:59]
v_exp_f32 v222,v222
v_mfma_f32_16x16x32_bf16 a[60:63],a[252:255],v[180:183],a[60:63]
buffer_load_dwordx4 v5,s[20:23],s47 offen offset:2048 nt lds
ds_read_b128 a[176:179],v1 offset:12288
v_mfma_f32_16x16x32_bf16 a[60:63],a[248:251],v[164:167],a[60:63]
v_exp_f32 v223,v223
v_mfma_f32_16x16x32_bf16 v[52:55],v[28:31],v[164:167],v[52:55]
v_cvt_pk_bf16_f32 v215,v222,v223
v_add_u32 v1,s48,v1
v_mfma_f32_16x16x32_bf16 v[52:55],v[28:31],v[180:183],v[52:55]
s_waitcnt lgkmcnt(3)
v_exp_f32 v228,v228
v_mfma_f32_16x16x32_bf16 v[132:135],a[128:131],v[64:67],v[32:35]
v_exp_f32 v229,v229
v_mfma_f32_16x16x32_bf16 v[132:135],a[132:135],v[68:71],v[132:135]
v_exp_f32 v230,v230
v_mfma_f32_16x16x32_bf16 v[132:135],a[136:139],v[72:75],v[132:135]
v_exp_f32 v231,v231
v_mfma_f32_16x16x32_bf16 v[132:135],a[140:143],v[76:79],v[132:135]
v_cvt_pk_bf16_f32 v228,v228,v229
v_cvt_pk_bf16_f32 v229,v230,v231
v_mfma_f32_16x16x32_bf16 v[140:143],a[156:159],v[76:79],v[32:35]
v_exp_f32 v236,v236
v_mfma_f32_16x16x32_bf16 v[140:143],a[152:155],v[72:75],v[140:143]
v_exp_f32 v237,v237
v_mfma_f32_16x16x32_bf16 v[140:143],a[148:151],v[68:71],v[140:143]
v_exp_f32 v238,v238
v_mfma_f32_16x16x32_bf16 v[140:143],a[144:147],v[64:67],v[140:143]
v_exp_f32 v239,v239
v_mfma_f32_16x16x32_bf16 v[148:151],a[160:163],v[64:67],v[32:35]
v_exp_f32 v244,v244
v_mfma_f32_16x16x32_bf16 v[148:151],a[164:167],v[68:71],v[148:151]
v_exp_f32 v245,v245
v_mfma_f32_16x16x32_bf16 v[148:151],a[168:171],v[72:75],v[148:151]
buffer_load_dwordx4 v5,s[20:23],s47 offen offset:3072 nt lds
v_cvt_pk_bf16_f32 v230,v236,v237
v_mfma_f32_16x16x32_bf16 v[148:151],a[172:175],v[76:79],v[148:151]
v_exp_f32 v246,v246
v_mfma_f32_16x16x32_bf16 v[156:159],a[188:191],v[76:79],v[32:35]
s_waitcnt lgkmcnt(0)
v_exp_f32 v247,v247
v_mfma_f32_16x16x32_bf16 v[156:159],a[184:187],v[72:75],v[156:159]
v_cvt_pk_bf16_f32 v231,v238,v239
v_cvt_pk_bf16_f32 v244,v244,v245
v_mfma_f32_16x16x32_bf16 v[156:159],a[180:183],v[68:71],v[156:159]
v_exp_f32 v252,v252
v_mfma_f32_16x16x32_bf16 v[156:159],a[176:179],v[64:67],v[156:159]
v_exp_f32 v253,v253
v_mfma_f32_16x16x32_bf16 a[64:67],a[192:195],v[196:199],a[64:67]
v_exp_f32 v254,v254
v_mfma_f32_16x16x32_bf16 a[64:67],a[196:199],v[212:215],a[64:67]
v_exp_f32 v255,v255
v_mfma_f32_16x16x32_bf16 a[68:71],a[204:207],v[212:215],a[68:71]
v_cvt_pk_bf16_f32 v245,v246,v247
v_cvt_pk_bf16_f32 v246,v252,v253
v_mfma_f32_16x16x32_bf16 a[68:71],a[200:203],v[196:199],a[68:71]
v_cvt_pk_bf16_f32 v247,v254,v255
v_mfma_f32_16x16x32_bf16 a[72:75],a[208:211],v[196:199],a[72:75]
v_exp_f32 v132,v132
v_mfma_f32_16x16x32_bf16 a[72:75],a[212:215],v[212:215],a[72:75]
s_add_u32 m0,s49,s52
v_exp_f32 v133,v133
v_mfma_f32_16x16x32_bf16 a[76:79],a[220:223],v[212:215],a[76:79]
s_mov_b32 s49,s50
v_exp_f32 v134,v134
v_mfma_f32_16x16x32_bf16 a[76:79],a[216:219],v[196:199],a[76:79]
s_add_u32 s50,s50,s48
v_exp_f32 v135,v135
v_mfma_f32_16x16x32_bf16 a[80:83],a[224:227],v[196:199],a[80:83]
buffer_load_dwordx4 v4,s[24:27],s47 offen nt lds
v_cvt_pk_bf16_f32 v132,v132,v133
v_mfma_f32_16x16x32_bf16 a[80:83],a[228:231],v[212:215],a[80:83]
v_exp_f32 v140,v140
v_mfma_f32_16x16x32_bf16 a[84:87],a[236:239],v[212:215],a[84:87]
v_exp_f32 v141,v141
v_mfma_f32_16x16x32_bf16 a[84:87],a[232:235],v[196:199],a[84:87]
v_exp_f32 v142,v142
v_mfma_f32_16x16x32_bf16 a[88:91],a[240:243],v[196:199],a[88:91]
v_exp_f32 v143,v143
v_mfma_f32_16x16x32_bf16 a[88:91],a[244:247],v[212:215],a[88:91]
v_exp_f32 v148,v148
v_mfma_f32_16x16x32_bf16 a[92:95],a[252:255],v[212:215],a[92:95]
v_exp_f32 v149,v149
v_mfma_f32_16x16x32_bf16 a[92:95],a[248:251],v[196:199],a[92:95]
v_cvt_pk_bf16_f32 v133,v134,v135
v_cvt_pk_bf16_f32 v134,v140,v141
v_mfma_f32_16x16x32_bf16 v[56:59],v[28:31],v[196:199],v[56:59]
v_exp_f32 v150,v150
v_mfma_f32_16x16x32_bf16 v[56:59],v[28:31],v[212:215],v[56:59]
v_exp_f32 v151,v151
v_mfma_f32_16x16x32_bf16 v[164:167],a[128:131],v[80:83],v[36:39]
v_cvt_pk_bf16_f32 v135,v142,v143
v_cvt_pk_bf16_f32 v148,v148,v149
v_mfma_f32_16x16x32_bf16 v[164:167],a[132:135],v[84:87],v[164:167]
v_exp_f32 v156,v156
v_mfma_f32_16x16x32_bf16 v[164:167],a[136:139],v[88:91],v[164:167]
v_exp_f32 v157,v157
v_mfma_f32_16x16x32_bf16 v[164:167],a[140:143],v[92:95],v[164:167]
v_exp_f32 v158,v158
v_mfma_f32_16x16x32_bf16 v[172:175],a[156:159],v[92:95],v[36:39]
buffer_load_dwordx4 v5,s[24:27],s47 offen offset:1024 nt lds
v_cvt_pk_bf16_f32 v149,v150,v151
v_mfma_f32_16x16x32_bf16 v[172:175],a[152:155],v[88:91],v[172:175]
v_exp_f32 v159,v159
v_mfma_f32_16x16x32_bf16 v[172:175],a[148:151],v[84:87],v[172:175]
v_cvt_pk_bf16_f32 v150,v156,v157
v_cvt_pk_bf16_f32 v151,v158,v159
v_mfma_f32_16x16x32_bf16 v[172:175],a[144:147],v[80:83],v[172:175]
v_mfma_f32_16x16x32_bf16 v[180:183],a[160:163],v[80:83],v[36:39]
v_mfma_f32_16x16x32_bf16 v[180:183],a[164:167],v[84:87],v[180:183]
v_exp_f32 v164,v164
v_mfma_f32_16x16x32_bf16 v[180:183],a[168:171],v[88:91],v[180:183]
v_exp_f32 v165,v165
v_mfma_f32_16x16x32_bf16 v[180:183],a[172:175],v[92:95],v[180:183]
v_exp_f32 v166,v166
v_mfma_f32_16x16x32_bf16 v[188:191],a[188:191],v[92:95],v[36:39]
v_exp_f32 v167,v167
v_mfma_f32_16x16x32_bf16 v[188:191],a[184:187],v[88:91],v[188:191]
v_cvt_pk_bf16_f32 v164,v164,v165
v_cvt_pk_bf16_f32 v165,v166,v167
v_mfma_f32_16x16x32_bf16 v[188:191],a[180:183],v[84:87],v[188:191]
v_exp_f32 v172,v172
v_mfma_f32_16x16x32_bf16 v[188:191],a[176:179],v[80:83],v[188:191]
v_exp_f32 v173,v173
v_mfma_f32_16x16x32_bf16 a[96:99],a[192:195],v[228:231],a[96:99]
ds_read_b64_tr_b16 a[192:193],v2
ds_read_b64_tr_b16 a[194:195],v2 offset:4096
v_mfma_f32_16x16x32_bf16 a[96:99],a[196:199],v[244:247],a[96:99]
ds_read_b64_tr_b16 a[196:197],v2 offset:8192
v_cvt_pk_bf16_f32 v166,v172,v173
v_mfma_f32_16x16x32_bf16 a[100:103],a[204:207],v[244:247],a[100:103]
buffer_load_dwordx4 v4,s[24:27],s47 offen offset:2048 nt lds
ds_read_b64_tr_b16 a[198:199],v2 offset:12288
v_mfma_f32_16x16x32_bf16 a[100:103],a[200:203],v[228:231],a[100:103]
ds_read_b64_tr_b16 a[204:205],v3 offset:8192
v_mfma_f32_16x16x32_bf16 a[104:107],a[208:211],v[228:231],a[104:107]
ds_read_b64_tr_b16 a[206:207],v3 offset:12288
ds_read_b64_tr_b16 a[200:201],v3
v_mfma_f32_16x16x32_bf16 a[104:107],a[212:215],v[244:247],a[104:107]
ds_read_b64_tr_b16 a[202:203],v3 offset:4096
v_mfma_f32_16x16x32_bf16 a[108:111],a[220:223],v[244:247],a[108:111]
ds_read_b64_tr_b16 a[208:209],v2 offset:256
v_mfma_f32_16x16x32_bf16 a[108:111],a[216:219],v[228:231],a[108:111]
ds_read_b64_tr_b16 a[210:211],v2 offset:4352
v_mfma_f32_16x16x32_bf16 a[112:115],a[224:227],v[228:231],a[112:115]
ds_read_b64_tr_b16 a[212:213],v2 offset:8448
ds_read_b64_tr_b16 a[214:215],v2 offset:12544
v_mfma_f32_16x16x32_bf16 a[112:115],a[228:231],v[244:247],a[112:115]
ds_read_b64_tr_b16 a[220:221],v3 offset:8448
v_mfma_f32_16x16x32_bf16 a[116:119],a[236:239],v[244:247],a[116:119]
ds_read_b64_tr_b16 a[222:223],v3 offset:12544
v_mfma_f32_16x16x32_bf16 a[116:119],a[232:235],v[228:231],a[116:119]
ds_read_b64_tr_b16 a[216:217],v3 offset:256
v_mfma_f32_16x16x32_bf16 a[120:123],a[240:243],v[228:231],a[120:123]
ds_read_b64_tr_b16 a[218:219],v3 offset:4352
ds_read_b64_tr_b16 a[224:225],v2 offset:512
v_mfma_f32_16x16x32_bf16 a[120:123],a[244:247],v[244:247],a[120:123]
ds_read_b64_tr_b16 a[226:227],v2 offset:4608
v_mfma_f32_16x16x32_bf16 a[124:127],a[252:255],v[244:247],a[124:127]
ds_read_b64_tr_b16 a[228:229],v2 offset:8704
v_mfma_f32_16x16x32_bf16 a[124:127],a[248:251],v[228:231],a[124:127]
ds_read_b64_tr_b16 a[230:231],v2 offset:12800
v_mfma_f32_16x16x32_bf16 v[60:63],v[28:31],v[228:231],v[60:63]
buffer_load_dwordx4 v5,s[24:27],s47 offen offset:3072 nt lds
ds_read_b64_tr_b16 a[236:237],v3 offset:8704
v_mfma_f32_16x16x32_bf16 v[60:63],v[28:31],v[244:247],v[60:63]
s_add_u32 s47,s47,16384
ds_read_b64_tr_b16 a[238:239],v3 offset:12800
ds_read_b64_tr_b16 a[232:233],v3 offset:512
v_mfma_f32_16x16x32_bf16 v[196:199],a[128:131],v[96:99],v[40:43]
s_and_b32 s47,s47,0x3ffff
s_or_b32 s47,s47,s46
ds_read_b64_tr_b16 a[234:235],v3 offset:4608
v_mfma_f32_16x16x32_bf16 v[196:199],a[132:135],v[100:103],v[196:199]
ds_read_b64_tr_b16 a[240:241],v2 offset:768
v_mfma_f32_16x16x32_bf16 v[196:199],a[136:139],v[104:107],v[196:199]
ds_read_b64_tr_b16 a[242:243],v2 offset:4864
v_mfma_f32_16x16x32_bf16 v[196:199],a[140:143],v[108:111],v[196:199]
ds_read_b64_tr_b16 a[244:245],v2 offset:8960
ds_read_b64_tr_b16 a[246:247],v2 offset:13056
v_mfma_f32_16x16x32_bf16 v[204:207],a[156:159],v[108:111],v[40:43]
ds_read_b64_tr_b16 a[252:253],v3 offset:8960
v_mfma_f32_16x16x32_bf16 v[204:207],a[152:155],v[104:107],v[204:207]
ds_read_b64_tr_b16 a[254:255],v3 offset:13056
v_mfma_f32_16x16x32_bf16 v[204:207],a[148:151],v[100:103],v[204:207]
ds_read_b64_tr_b16 a[248:249],v3 offset:768
v_mfma_f32_16x16x32_bf16 v[204:207],a[144:147],v[96:99],v[204:207]
ds_read_b64_tr_b16 a[250:251],v3 offset:4864
v_mfma_f32_16x16x32_bf16 v[212:215],a[160:163],v[96:99],v[40:43]
v_exp_f32 v180,v180
v_mfma_f32_16x16x32_bf16 v[212:215],a[164:167],v[100:103],v[212:215]
v_exp_f32 v181,v181
v_mfma_f32_16x16x32_bf16 v[212:215],a[168:171],v[104:107],v[212:215]
v_exp_f32 v182,v182
v_mfma_f32_16x16x32_bf16 v[212:215],a[172:175],v[108:111],v[212:215]
v_exp_f32 v183,v183
v_mfma_f32_16x16x32_bf16 v[220:223],a[188:191],v[108:111],v[40:43]
v_exp_f32 v174,v174
v_mfma_f32_16x16x32_bf16 v[220:223],a[184:187],v[104:107],v[220:223]
v_exp_f32 v175,v175
v_mfma_f32_16x16x32_bf16 v[220:223],a[180:183],v[100:103],v[220:223]
v_cvt_pk_bf16_f32 v180,v180,v181
v_cvt_pk_bf16_f32 v181,v182,v183
v_mfma_f32_16x16x32_bf16 v[220:223],a[176:179],v[96:99],v[220:223]
s_waitcnt lgkmcnt(0)
v_exp_f32 v188,v188
v_mfma_f32_16x16x32_bf16 a[0:3],a[192:195],v[132:135],a[0:3]
v_exp_f32 v189,v189
v_mfma_f32_16x16x32_bf16 a[0:3],a[196:199],v[148:151],a[0:3]
v_exp_f32 v190,v190
v_mfma_f32_16x16x32_bf16 a[4:7],a[204:207],v[148:151],a[4:7]
)A"
R"A(v_exp_f32 v191,v191
v_mfma_f32_16x16x32_bf16 a[4:7],a[200:203],v[132:135],a[4:7]
v_cvt_pk_bf16_f32 v167,v174,v175
v_cvt_pk_bf16_f32 v182,v188,v189
v_mfma_f32_16x16x32_bf16 a[8:11],a[208:211],v[132:135],a[8:11]
v_cvt_pk_bf16_f32 v183,v190,v191
v_mfma_f32_16x16x32_bf16 a[8:11],a[212:215],v[148:151],a[8:11]
v_mfma_f32_16x16x32_bf16 a[12:15],a[220:223],v[148:151],a[12:15]
v_mfma_f32_16x16x32_bf16 a[12:15],a[216:219],v[132:135],a[12:15]
v_mfma_f32_16x16x32_bf16 a[16:19],a[224:227],v[132:135],a[16:19]
v_mfma_f32_16x16x32_bf16 a[16:19],a[228:231],v[148:151],a[16:19]
v_mfma_f32_16x16x32_bf16 a[20:23],a[236:239],v[148:151],a[20:23]
v_mfma_f32_16x16x32_bf16 a[20:23],a[232:235],v[132:135],a[20:23]
v_mfma_f32_16x16x32_bf16 a[24:27],a[240:243],v[132:135],a[24:27]
v_mfma_f32_16x16x32_bf16 a[24:27],a[244:247],v[148:151],a[24:27]
v_mfma_f32_16x16x32_bf16 a[28:31],a[252:255],v[148:151],a[28:31]
v_mfma_f32_16x16x32_bf16 a[28:31],a[248:251],v[132:135],a[28:31]
v_mfma_f32_16x16x32_bf16 v[48:51],v[28:31],v[132:135],v[48:51]
v_mfma_f32_16x16x32_bf16 v[48:51],v[28:31],v[148:151],v[48:51]
s_sleep 2
s_sub_u32 s53,s53,1
s_cmp_lg_u32 s53,0
s_cbranch_scc1 L_att0_%=
s_waitcnt vmcnt(16)
s_barrier
v_mfma_f32_16x16x32_bf16 v[228:231],a[128:131],v[112:115],v[44:47]
s_add_u32 m0,s49,s51
ds_read_b128 a[128:131],v1
v_and_b32 v18,48,v6
v_mfma_f32_16x16x32_bf16 v[228:231],a[132:135],v[116:119],v[228:231]
s_add_u32 s54,s54,16384
v_exp_f32 v196,v196
v_mfma_f32_16x16x32_bf16 v[228:231],a[136:139],v[120:123],v[228:231]
s_and_b32 s46,s54,0xfffc0000
buffer_load_dwordx4 v4,s[20:23],s47 offen nt lds
ds_read_b128 a[132:135],v1 offset:256
v_mfma_f32_16x16x32_bf16 v[228:231],a[140:143],v[124:127],v[228:231]
s_add_u32 s54,s54,16384
v_exp_f32 v197,v197
v_mfma_f32_16x16x32_bf16 v[236:239],a[156:159],v[124:127],v[44:47]
s_add_u32 s84,s55,0
ds_read_b128 a[136:139],v1 offset:512
v_cvt_pk_bf16_f32 v196,v196,v197
v_mfma_f32_16x16x32_bf16 v[236:239],a[152:155],v[120:123],v[236:239]
s_add_u32 s85,s55,4096
v_exp_f32 v198,v198
v_mfma_f32_16x16x32_bf16 v[236:239],a[148:151],v[116:119],v[236:239]
s_add_u32 s86,s55,8192
ds_read_b128 a[140:143],v1 offset:768
v_lshrrev_b32 v19,4,v6
v_mfma_f32_16x16x32_bf16 v[236:239],a[144:147],v[112:115],v[236:239]
s_add_u32 s87,s55,12288
v_exp_f32 v199,v199
v_mfma_f32_16x16x32_bf16 v[244:247],a[160:163],v[112:115],v[44:47]
ds_read_b128 a[156:159],v1 offset:4864
v_cvt_pk_bf16_f32 v197,v198,v199
v_mfma_f32_16x16x32_bf16 v[244:247],a[164:167],v[116:119],v[244:247]
v_exp_f32 v204,v204
v_mfma_f32_16x16x32_bf16 v[244:247],a[168:171],v[120:123],v[244:247]
ds_read_b128 a[152:155],v1 offset:4608
v_lshlrev_b32 v18,2,v18
v_mfma_f32_16x16x32_bf16 v[244:247],a[172:175],v[124:127],v[244:247]
v_exp_f32 v205,v205
v_mfma_f32_16x16x32_bf16 v[252:255],a[188:191],v[124:127],v[44:47]
ds_read_b128 a[148:151],v1 offset:4352
v_cvt_pk_bf16_f32 v198,v204,v205
v_mfma_f32_16x16x32_bf16 v[252:255],a[184:187],v[120:123],v[252:255]
v_exp_f32 v206,v206
v_mfma_f32_16x16x32_bf16 v[252:255],a[180:183],v[116:119],v[252:255]
ds_read_b128 a[144:147],v1 offset:4096
v_and_b32 v19,48,v19
v_mfma_f32_16x16x32_bf16 v[252:255],a[176:179],v[112:115],v[252:255]
v_exp_f32 v207,v207
v_mfma_f32_16x16x32_bf16 a[32:35],a[192:195],v[164:167],a[32:35]
buffer_load_dwordx4 v4,s[20:23],s47 offen offset:1024 nt lds
ds_read_b128 a[160:163],v1 offset:8192
v_mfma_f32_16x16x32_bf16 a[32:35],a[196:199],v[180:183],a[32:35]
v_exp_f32 v212,v212
v_mfma_f32_16x16x32_bf16 a[36:39],a[204:207],v[180:183],a[36:39]
ds_read_b128 a[164:167],v1 offset:8448
v_cvt_pk_bf16_f32 v199,v206,v207
v_mfma_f32_16x16x32_bf16 a[36:39],a[200:203],v[164:167],a[36:39]
v_exp_f32 v213,v213
v_mfma_f32_16x16x32_bf16 a[40:43],a[208:211],v[164:167],a[40:43]
ds_read_b128 a[168:171],v1 offset:8704
v_cvt_pk_bf16_f32 v212,v212,v213
v_mfma_f32_16x16x32_bf16 a[40:43],a[212:215],v[180:183],a[40:43]
v_exp_f32 v214,v214
v_mfma_f32_16x16x32_bf16 a[44:47],a[220:223],v[180:183],a[44:47]
ds_read_b128 a[172:175],v1 offset:8960
v_add_u32 v18,v18,v19
v_mfma_f32_16x16x32_bf16 a[44:47],a[216:219],v[164:167],a[44:47]
v_exp_f32 v215,v215
v_mfma_f32_16x16x32_bf16 a[48:51],a[224:227],v[164:167],a[48:51]
ds_read_b128 a[188:191],v1 offset:13056
v_cvt_pk_bf16_f32 v213,v214,v215
v_mfma_f32_16x16x32_bf16 a[48:51],a[228:231],v[180:183],a[48:51]
v_exp_f32 v220,v220
v_mfma_f32_16x16x32_bf16 a[52:55],a[236:239],v[180:183],a[52:55]
ds_read_b128 a[184:187],v1 offset:12800
v_add_u32 v18,131072,v18
v_mfma_f32_16x16x32_bf16 a[52:55],a[232:235],v[164:167],a[52:55]
v_exp_f32 v221,v221
v_mfma_f32_16x16x32_bf16 a[56:59],a[240:243],v[164:167],a[56:59]
ds_read_b128 a[180:183],v1 offset:12544
v_cvt_pk_bf16_f32 v214,v220,v221
v_mfma_f32_16x16x32_bf16 a[56:59],a[244:247],v[180:183],a[56:59]
v_exp_f32 v222,v222
v_mfma_f32_16x16x32_bf16 a[60:63],a[252:255],v[180:183],a[60:63]
buffer_load_dwordx4 v5,s[20:23],s47 offen offset:2048 nt lds
ds_read_b128 a[176:179],v1 offset:12288
v_mfma_f32_16x16x32_bf16 a[60:63],a[248:251],v[164:167],a[60:63]
v_exp_f32 v223,v223
v_mfma_f32_16x16x32_bf16 v[52:55],v[28:31],v[164:167],v[52:55]
v_cvt_pk_bf16_f32 v215,v222,v223
v_add_u32 v2,s48,v2
v_mfma_f32_16x16x32_bf16 v[52:55],v[28:31],v[180:183],v[52:55]
s_waitcnt lgkmcnt(3)
v_exp_f32 v228,v228
v_mfma_f32_16x16x32_bf16 v[132:135],a[128:131],v[64:67],v[32:35]
v_exp_f32 v229,v229
v_mfma_f32_16x16x32_bf16 v[132:135],a[132:135],v[68:71],v[132:135]
v_exp_f32 v230,v230
v_mfma_f32_16x16x32_bf16 v[132:135],a[136:139],v[72:75],v[132:135]
v_exp_f32 v231,v231
v_mfma_f32_16x16x32_bf16 v[132:135],a[140:143],v[76:79],v[132:135]
v_cvt_pk_bf16_f32 v228,v228,v229
v_cvt_pk_bf16_f32 v229,v230,v231
v_mfma_f32_16x16x32_bf16 v[140:143],a[156:159],v[76:79],v[32:35]
v_exp_f32 v236,v236
v_mfma_f32_16x16x32_bf16 v[140:143],a[152:155],v[72:75],v[140:143]
v_exp_f32 v237,v237
v_mfma_f32_16x16x32_bf16 v[140:143],a[148:151],v[68:71],v[140:143]
v_exp_f32 v238,v238
v_mfma_f32_16x16x32_bf16 v[140:143],a[144:147],v[64:67],v[140:143]
v_exp_f32 v239,v239
v_mfma_f32_16x16x32_bf16 v[148:151],a[160:163],v[64:67],v[32:35]
v_exp_f32 v244,v244
v_mfma_f32_16x16x32_bf16 v[148:151],a[164:167],v[68:71],v[148:151]
v_exp_f32 v245,v245
v_mfma_f32_16x16x32_bf16 v[148:151],a[168:171],v[72:75],v[148:151]
buffer_load_dwordx4 v5,s[20:23],s47 offen offset:3072 nt lds
v_cvt_pk_bf16_f32 v230,v236,v237
v_mfma_f32_16x16x32_bf16 v[148:151],a[172:175],v[76:79],v[148:151]
v_exp_f32 v246,v246
v_mfma_f32_16x16x32_bf16 v[156:159],a[188:191],v[76:79],v[32:35]
s_waitcnt lgkmcnt(0)
v_exp_f32 v247,v247
v_mfma_f32_16x16x32_bf16 v[156:159],a[184:187],v[72:75],v[156:159]
v_cvt_pk_bf16_f32 v231,v238,v239
v_cvt_pk_bf16_f32 v244,v244,v245
v_mfma_f32_16x16x32_bf16 v[156:159],a[180:183],v[68:71],v[156:159]
v_exp_f32 v252,v252
v_mfma_f32_16x16x32_bf16 v[156:159],a[176:179],v[64:67],v[156:159]
v_exp_f32 v253,v253
v_mfma_f32_16x16x32_bf16 a[64:67],a[192:195],v[196:199],a[64:67]
v_exp_f32 v254,v254
v_mfma_f32_16x16x32_bf16 a[64:67],a[196:199],v[212:215],a[64:67]
v_exp_f32 v255,v255
v_mfma_f32_16x16x32_bf16 a[68:71],a[204:207],v[212:215],a[68:71]
v_cvt_pk_bf16_f32 v245,v246,v247
v_cvt_pk_bf16_f32 v246,v252,v253
v_mfma_f32_16x16x32_bf16 a[68:71],a[200:203],v[196:199],a[68:71]
v_cvt_pk_bf16_f32 v247,v254,v255
v_add_u32 v3,s48,v3
v_mfma_f32_16x16x32_bf16 a[72:75],a[208:211],v[196:199],a[72:75]
v_exp_f32 v132,v132
v_mfma_f32_16x16x32_bf16 a[72:75],a[212:215],v[212:215],a[72:75]
s_add_u32 m0,s49,s52
v_exp_f32 v133,v133
v_mfma_f32_16x16x32_bf16 a[76:79],a[220:223],v[212:215],a[76:79]
s_mov_b32 s49,s50
v_exp_f32 v134,v134
v_mfma_f32_16x16x32_bf16 a[76:79],a[216:219],v[196:199],a[76:79]
v_exp_f32 v135,v135
v_mfma_f32_16x16x32_bf16 a[80:83],a[224:227],v[196:199],a[80:83]
buffer_load_dwordx4 v4,s[24:27],s47 offen nt lds
v_cvt_pk_bf16_f32 v132,v132,v133
v_mfma_f32_16x16x32_bf16 a[80:83],a[228:231],v[212:215],a[80:83]
v_exp_f32 v140,v140
v_mfma_f32_16x16x32_bf16 a[84:87],a[236:239],v[212:215],a[84:87]
v_exp_f32 v141,v141
v_mfma_f32_16x16x32_bf16 a[84:87],a[232:235],v[196:199],a[84:87]
v_exp_f32 v142,v142
v_mfma_f32_16x16x32_bf16 a[88:91],a[240:243],v[196:199],a[88:91]
v_exp_f32 v143,v143
v_mfma_f32_16x16x32_bf16 a[88:91],a[244:247],v[212:215],a[88:91]
v_exp_f32 v148,v148
v_mfma_f32_16x16x32_bf16 a[92:95],a[252:255],v[212:215],a[92:95]
v_exp_f32 v149,v149
v_mfma_f32_16x16x32_bf16 a[92:95],a[248:251],v[196:199],a[92:95]
v_cvt_pk_bf16_f32 v133,v134,v135
v_cvt_pk_bf16_f32 v134,v140,v141
v_mfma_f32_16x16x32_bf16 v[56:59],v[28:31],v[196:199],v[56:59]
v_exp_f32 v150,v150
v_mfma_f32_16x16x32_bf16 v[56:59],v[28:31],v[212:215],v[56:59]
v_exp_f32 v151,v151
v_mfma_f32_16x16x32_bf16 v[164:167],a[128:131],v[80:83],v[36:39]
v_cvt_pk_bf16_f32 v135,v142,v143
v_cvt_pk_bf16_f32 v148,v148,v149
v_mfma_f32_16x16x32_bf16 v[164:167],a[132:135],v[84:87],v[164:167]
v_exp_f32 v156,v156
v_mfma_f32_16x16x32_bf16 v[164:167],a[136:139],v[88:91],v[164:167]
v_exp_f32 v157,v157
v_mfma_f32_16x16x32_bf16 v[164:167],a[140:143],v[92:95],v[164:167]
v_exp_f32 v158,v158
v_mfma_f32_16x16x32_bf16 v[172:175],a[156:159],v[92:95],v[36:39]
buffer_load_dwordx4 v5,s[24:27],s47 offen offset:1024 nt lds
v_cvt_pk_bf16_f32 v149,v150,v151
v_mfma_f32_16x16x32_bf16 v[172:175],a[152:155],v[88:91],v[172:175]
v_exp_f32 v159,v159
v_mfma_f32_16x16x32_bf16 v[172:175],a[148:151],v[84:87],v[172:175]
v_cvt_pk_bf16_f32 v150,v156,v157
v_cvt_pk_bf16_f32 v151,v158,v159
v_mfma_f32_16x16x32_bf16 v[172:175],a[144:147],v[80:83],v[172:175]
s_cmp_eq_u32 s50,131072
s_cselect_b32 s62,163840,0
s_sub_u32 s48,32768,s62
v_mfma_f32_16x16x32_bf16 v[180:183],a[160:163],v[80:83],v[36:39]
s_add_u32 s50,s50,s48
v_add_u32 v1,s48,v1
v_mfma_f32_16x16x32_bf16 v[180:183],a[164:167],v[84:87],v[180:183]
v_exp_f32 v164,v164
v_mfma_f32_16x16x32_bf16 v[180:183],a[168:171],v[88:91],v[180:183]
v_exp_f32 v165,v165
v_mfma_f32_16x16x32_bf16 v[180:183],a[172:175],v[92:95],v[180:183]
v_exp_f32 v166,v166
v_mfma_f32_16x16x32_bf16 v[188:191],a[188:191],v[92:95],v[36:39]
ds_read_b128 v[92:95],v18 offset:768
v_cvt_pk_bf16_f32 v164,v164,v165
v_mfma_f32_16x16x32_bf16 v[188:191],a[184:187],v[88:91],v[188:191]
v_exp_f32 v167,v167
v_mfma_f32_16x16x32_bf16 v[188:191],a[180:183],v[84:87],v[188:191]
ds_read_b128 v[84:87],v18 offset:256
v_cvt_pk_bf16_f32 v165,v166,v167
v_mfma_f32_16x16x32_bf16 v[188:191],a[176:179],v[80:83],v[188:191]
v_exp_f32 v172,v172
v_mfma_f32_16x16x32_bf16 a[96:99],a[192:195],v[228:231],a[96:99]
s_cmp_eq_u32 s50,131072
s_cselect_b32 s62,163840,0
ds_read_b128 v[80:83],v18
v_mfma_f32_16x16x32_bf16 a[96:99],a[196:199],v[244:247],a[96:99]
ds_read_b64_tr_b16 a[192:193],v2
v_mfma_f32_16x16x32_bf16 a[100:103],a[204:207],v[244:247],a[100:103]
buffer_load_dwordx4 v4,s[24:27],s47 offen offset:2048 nt lds
ds_read_b64_tr_b16 a[194:195],v2 offset:4096
v_mfma_f32_16x16x32_bf16 a[100:103],a[200:203],v[228:231],a[100:103]
ds_read_b64_tr_b16 a[196:197],v2 offset:8192
v_mfma_f32_16x16x32_bf16 a[104:107],a[208:211],v[228:231],a[104:107]
ds_read_b128 v[88:91],v18 offset:512
v_mfma_f32_16x16x32_bf16 a[104:107],a[212:215],v[244:247],a[104:107]
ds_read_b64_tr_b16 a[198:199],v2 offset:12288
v_mfma_f32_16x16x32_bf16 a[108:111],a[220:223],v[244:247],a[108:111]
ds_read_b64_tr_b16 a[204:205],v3 offset:8192
v_mfma_f32_16x16x32_bf16 a[108:111],a[216:219],v[228:231],a[108:111]
ds_read_b64_tr_b16 a[206:207],v3 offset:12288
v_mfma_f32_16x16x32_bf16 a[112:115],a[224:227],v[228:231],a[112:115]
ds_read_b64_tr_b16 a[200:201],v3
ds_read_b64_tr_b16 a[202:203],v3 offset:4096
v_mfma_f32_16x16x32_bf16 a[112:115],a[228:231],v[244:247],a[112:115]
ds_read_b64_tr_b16 a[208:209],v2 offset:256
v_mfma_f32_16x16x32_bf16 a[116:119],a[236:239],v[244:247],a[116:119]
ds_read_b64_tr_b16 a[210:211],v2 offset:4352
v_mfma_f32_16x16x32_bf16 a[116:119],a[232:235],v[228:231],a[116:119]
ds_read_b64_tr_b16 a[212:213],v2 offset:8448
v_mfma_f32_16x16x32_bf16 a[120:123],a[240:243],v[228:231],a[120:123]
ds_read_b64_tr_b16 a[214:215],v2 offset:12544
ds_read_b64_tr_b16 a[220:221],v3 offset:8448
v_mfma_f32_16x16x32_bf16 a[120:123],a[244:247],v[244:247],a[120:123]
ds_read_b64_tr_b16 a[222:223],v3 offset:12544
v_mfma_f32_16x16x32_bf16 a[124:127],a[252:255],v[244:247],a[124:127]
)A"
R"A(ds_read_b64_tr_b16 a[216:217],v3 offset:256
v_mfma_f32_16x16x32_bf16 a[124:127],a[248:251],v[228:231],a[124:127]
ds_read_b64_tr_b16 a[218:219],v3 offset:4352
v_mfma_f32_16x16x32_bf16 v[60:63],v[28:31],v[228:231],v[60:63]
buffer_load_dwordx4 v5,s[24:27],s47 offen offset:3072 nt lds
ds_read_b64_tr_b16 a[224:225],v2 offset:512
v_mfma_f32_16x16x32_bf16 v[60:63],v[28:31],v[244:247],v[60:63]
s_add_u32 s47,s47,16384
ds_read_b64_tr_b16 a[226:227],v2 offset:4608
ds_read_b64_tr_b16 a[228:229],v2 offset:8704
v_mfma_f32_16x16x32_bf16 v[196:199],a[128:131],v[96:99],v[40:43]
s_and_b32 s47,s47,0x3ffff
s_or_b32 s47,s47,s46
ds_read_b64_tr_b16 a[230:231],v2 offset:12800
v_mfma_f32_16x16x32_bf16 v[196:199],a[132:135],v[100:103],v[196:199]
s_and_b32 s46,s54,0xfffc0000
s_add_u32 s54,s54,16384
ds_read_b64_tr_b16 a[236:237],v3 offset:8704
v_mfma_f32_16x16x32_bf16 v[196:199],a[136:139],v[104:107],v[196:199]
ds_read_b64_tr_b16 a[238:239],v3 offset:12800
v_mfma_f32_16x16x32_bf16 v[196:199],a[140:143],v[108:111],v[196:199]
ds_read_b64_tr_b16 a[232:233],v3 offset:512
ds_read_b64_tr_b16 a[234:235],v3 offset:4608
v_mfma_f32_16x16x32_bf16 v[204:207],a[156:159],v[108:111],v[40:43]
ds_read_b64_tr_b16 a[240:241],v2 offset:768
v_mfma_f32_16x16x32_bf16 v[204:207],a[152:155],v[104:107],v[204:207]
ds_read_b64_tr_b16 a[242:243],v2 offset:4864
v_mfma_f32_16x16x32_bf16 v[204:207],a[148:151],v[100:103],v[204:207]
ds_read_b64_tr_b16 a[244:245],v2 offset:8960
v_mfma_f32_16x16x32_bf16 v[204:207],a[144:147],v[96:99],v[204:207]
ds_read_b64_tr_b16 a[246:247],v2 offset:13056
ds_read_b64_tr_b16 a[252:253],v3 offset:8960
v_mfma_f32_16x16x32_bf16 v[212:215],a[160:163],v[96:99],v[40:43]
ds_read_b64_tr_b16 a[254:255],v3 offset:13056
v_add_u32 v2,s48,v2
v_mfma_f32_16x16x32_bf16 v[212:215],a[164:167],v[100:103],v[212:215]
ds_read_b64_tr_b16 a[248:249],v3 offset:768
v_mfma_f32_16x16x32_bf16 v[212:215],a[168:171],v[104:107],v[212:215]
ds_read_b64_tr_b16 a[250:251],v3 offset:4864
v_add_u32 v3,s48,v3
v_mfma_f32_16x16x32_bf16 v[212:215],a[172:175],v[108:111],v[212:215]
s_sub_u32 s48,32768,s62
v_exp_f32 v173,v173
v_mfma_f32_16x16x32_bf16 v[220:223],a[188:191],v[108:111],v[40:43]
v_exp_f32 v174,v174
v_mfma_f32_16x16x32_bf16 v[220:223],a[184:187],v[104:107],v[220:223]
v_exp_f32 v175,v175
v_mfma_f32_16x16x32_bf16 v[220:223],a[180:183],v[100:103],v[220:223]
v_exp_f32 v180,v180
v_mfma_f32_16x16x32_bf16 v[220:223],a[176:179],v[96:99],v[220:223]
s_waitcnt lgkmcnt(3)
v_exp_f32 v181,v181
v_mfma_f32_16x16x32_bf16 v[32:35],v[80:83],v[128:131],0
v_exp_f32 v182,v182
v_mfma_f32_16x16x32_bf16 v[32:35],v[84:87],v[160:163],v[32:35]
v_exp_f32 v183,v183
v_mfma_f32_16x16x32_bf16 v[32:35],v[88:91],v[192:195],v[32:35]
v_cvt_pk_bf16_f32 v166,v172,v173
v_cvt_pk_bf16_f32 v167,v174,v175
v_mfma_f32_16x16x32_bf16 v[32:35],v[92:95],v[224:227],v[32:35]
v_cvt_pk_bf16_f32 v180,v180,v181
v_cvt_pk_bf16_f32 v181,v182,v183
v_mfma_f32_16x16x32_bf16 v[36:39],v[80:83],v[136:139],0
v_exp_f32 v188,v188
v_mfma_f32_16x16x32_bf16 v[36:39],v[84:87],v[168:171],v[36:39]
v_exp_f32 v189,v189
v_mfma_f32_16x16x32_bf16 v[36:39],v[88:91],v[200:203],v[36:39]
v_exp_f32 v190,v190
v_mfma_f32_16x16x32_bf16 v[36:39],v[92:95],v[232:235],v[36:39]
v_exp_f32 v191,v191
v_mfma_f32_16x16x32_bf16 v[40:43],v[80:83],v[144:147],0
v_cvt_pk_bf16_f32 v182,v188,v189
v_cvt_pk_bf16_f32 v183,v190,v191
v_mfma_f32_16x16x32_bf16 v[40:43],v[84:87],v[176:179],v[40:43]
v_max3_f32 v32,v32,v33,v34
v_max3_f32 v32,v32,v35,s18
v_mfma_f32_16x16x32_bf16 v[40:43],v[88:91],v[208:211],v[40:43]
v_sub_f32 v35,0,v32
v_sub_f32 v34,0,v32
v_mfma_f32_16x16x32_bf16 v[40:43],v[92:95],v[240:243],v[40:43]
v_sub_f32 v33,0,v32
v_sub_f32 v32,0,v32
v_mfma_f32_16x16x32_bf16 a[0:3],a[192:195],v[132:135],a[0:3]
v_exp_f32 v196,v196
v_mfma_f32_16x16x32_bf16 a[0:3],a[196:199],v[148:151],a[0:3]
v_exp_f32 v197,v197
v_mfma_f32_16x16x32_bf16 a[4:7],a[204:207],v[148:151],a[4:7]
v_exp_f32 v198,v198
v_mfma_f32_16x16x32_bf16 a[4:7],a[200:203],v[132:135],a[4:7]
v_exp_f32 v199,v199
v_mfma_f32_16x16x32_bf16 a[8:11],a[208:211],v[132:135],a[8:11]
v_cvt_pk_bf16_f32 v196,v196,v197
v_cvt_pk_bf16_f32 v197,v198,v199
v_mfma_f32_16x16x32_bf16 a[8:11],a[212:215],v[148:151],a[8:11]
v_exp_f32 v204,v204
v_mfma_f32_16x16x32_bf16 a[12:15],a[220:223],v[148:151],a[12:15]
v_exp_f32 v205,v205
v_mfma_f32_16x16x32_bf16 a[12:15],a[216:219],v[132:135],a[12:15]
v_exp_f32 v206,v206
v_mfma_f32_16x16x32_bf16 a[16:19],a[224:227],v[132:135],a[16:19]
v_exp_f32 v207,v207
v_mfma_f32_16x16x32_bf16 a[16:19],a[228:231],v[148:151],a[16:19]
v_exp_f32 v212,v212
v_mfma_f32_16x16x32_bf16 a[20:23],a[236:239],v[148:151],a[20:23]
v_exp_f32 v213,v213
v_mfma_f32_16x16x32_bf16 a[20:23],a[232:235],v[132:135],a[20:23]
v_exp_f32 v214,v214
v_mfma_f32_16x16x32_bf16 a[24:27],a[240:243],v[132:135],a[24:27]
v_exp_f32 v215,v215
v_mfma_f32_16x16x32_bf16 a[24:27],a[244:247],v[148:151],a[24:27]
s_waitcnt lgkmcnt(0)
v_cvt_pk_bf16_f32 v198,v204,v205
v_cvt_pk_bf16_f32 v199,v206,v207
v_mfma_f32_16x16x32_bf16 a[28:31],a[252:255],v[148:151],a[28:31]
v_cvt_pk_bf16_f32 v212,v212,v213
v_cvt_pk_bf16_f32 v213,v214,v215
v_mfma_f32_16x16x32_bf16 a[28:31],a[248:251],v[132:135],a[28:31]
v_exp_f32 v220,v220
v_mfma_f32_16x16x32_bf16 v[48:51],v[28:31],v[132:135],v[48:51]
v_exp_f32 v221,v221
v_mfma_f32_16x16x32_bf16 v[48:51],v[28:31],v[148:151],v[48:51]
v_exp_f32 v222,v222
s_barrier
v_mfma_f32_16x16x32_bf16 v[228:231],a[128:131],v[112:115],v[44:47]
s_add_u32 m0,s49,s51
ds_read_b128 a[128:131],v1
v_cvt_pk_bf16_f32 v214,v220,v221
v_mfma_f32_16x16x32_bf16 v[228:231],a[132:135],v[116:119],v[228:231]
v_exp_f32 v223,v223
v_mfma_f32_16x16x32_bf16 v[228:231],a[136:139],v[120:123],v[228:231]
buffer_load_dwordx4 v4,s[20:23],s47 offen nt lds
ds_read_b128 a[132:135],v1 offset:256
v_mfma_f32_16x16x32_bf16 v[228:231],a[140:143],v[124:127],v[228:231]
v_cvt_pk_bf16_f32 v215,v222,v223
v_max3_f32 v36,v36,v37,v38
v_mfma_f32_16x16x32_bf16 v[236:239],a[156:159],v[124:127],v[44:47]
ds_read_b128 a[136:139],v1 offset:512
v_max3_f32 v36,v36,v39,s19
v_mfma_f32_16x16x32_bf16 v[236:239],a[152:155],v[120:123],v[236:239]
v_sub_f32 v39,0,v36
v_sub_f32 v38,0,v36
v_mfma_f32_16x16x32_bf16 v[236:239],a[148:151],v[116:119],v[236:239]
ds_read_b128 a[140:143],v1 offset:768
v_sub_f32 v37,0,v36
v_mfma_f32_16x16x32_bf16 v[236:239],a[144:147],v[112:115],v[236:239]
v_sub_f32 v36,0,v36
v_max3_f32 v40,v40,v41,v42
v_mfma_f32_16x16x32_bf16 v[244:247],a[160:163],v[112:115],v[44:47]
ds_read_b128 a[156:159],v1 offset:4864
v_max3_f32 v40,v40,v43,s100
v_mfma_f32_16x16x32_bf16 v[244:247],a[164:167],v[116:119],v[244:247]
v_exp_f32 v228,v228
v_mfma_f32_16x16x32_bf16 v[244:247],a[168:171],v[120:123],v[244:247]
ds_read_b128 a[152:155],v1 offset:4608
v_sub_f32 v43,0,v40
v_mfma_f32_16x16x32_bf16 v[244:247],a[172:175],v[124:127],v[244:247]
v_exp_f32 v229,v229
v_mfma_f32_16x16x32_bf16 v[252:255],a[188:191],v[124:127],v[44:47]
ds_read_b128 a[148:151],v1 offset:4352
v_cvt_pk_bf16_f32 v228,v228,v229
v_mfma_f32_16x16x32_bf16 v[252:255],a[184:187],v[120:123],v[252:255]
v_exp_f32 v230,v230
v_mfma_f32_16x16x32_bf16 v[252:255],a[180:183],v[116:119],v[252:255]
ds_read_b128 a[144:147],v1 offset:4096
v_sub_f32 v42,0,v40
v_mfma_f32_16x16x32_bf16 v[252:255],a[176:179],v[112:115],v[252:255]
v_exp_f32 v231,v231
v_mfma_f32_16x16x32_bf16 v[44:47],v[80:83],v[152:155],0
buffer_load_dwordx4 v4,s[20:23],s47 offen offset:1024 nt lds
ds_read_b128 a[160:163],v1 offset:8192
v_mfma_f32_16x16x32_bf16 v[44:47],v[84:87],v[184:187],v[44:47]
v_exp_f32 v236,v236
v_mfma_f32_16x16x32_bf16 v[44:47],v[88:91],v[216:219],v[44:47]
ds_read_b128 a[164:167],v1 offset:8448
v_cvt_pk_bf16_f32 v229,v230,v231
v_mfma_f32_16x16x32_bf16 v[44:47],v[92:95],v[248:251],v[44:47]
v_exp_f32 v237,v237
v_mfma_f32_16x16x32_bf16 a[32:35],a[192:195],v[164:167],a[32:35]
ds_read_b128 a[168:171],v1 offset:8704
v_cvt_pk_bf16_f32 v230,v236,v237
v_mfma_f32_16x16x32_bf16 a[32:35],a[196:199],v[180:183],a[32:35]
v_exp_f32 v238,v238
v_mfma_f32_16x16x32_bf16 a[36:39],a[204:207],v[180:183],a[36:39]
ds_read_b128 a[172:175],v1 offset:8960
v_sub_f32 v41,0,v40
v_mfma_f32_16x16x32_bf16 a[36:39],a[200:203],v[164:167],a[36:39]
v_exp_f32 v239,v239
v_mfma_f32_16x16x32_bf16 a[40:43],a[208:211],v[164:167],a[40:43]
ds_read_b128 a[188:191],v1 offset:13056
v_cvt_pk_bf16_f32 v231,v238,v239
v_mfma_f32_16x16x32_bf16 a[40:43],a[212:215],v[180:183],a[40:43]
v_exp_f32 v244,v244
v_mfma_f32_16x16x32_bf16 a[44:47],a[220:223],v[180:183],a[44:47]
ds_read_b128 a[184:187],v1 offset:12800
v_sub_f32 v40,0,v40
v_mfma_f32_16x16x32_bf16 a[44:47],a[216:219],v[164:167],a[44:47]
v_exp_f32 v245,v245
v_mfma_f32_16x16x32_bf16 a[48:51],a[224:227],v[164:167],a[48:51]
ds_read_b128 a[180:183],v1 offset:12544
v_cvt_pk_bf16_f32 v244,v244,v245
v_mfma_f32_16x16x32_bf16 a[48:51],a[228:231],v[180:183],a[48:51]
v_exp_f32 v246,v246
v_mfma_f32_16x16x32_bf16 a[52:55],a[236:239],v[180:183],a[52:55]
buffer_load_dwordx4 v5,s[20:23],s47 offen offset:2048 nt lds
ds_read_b128 a[176:179],v1 offset:12288
v_mfma_f32_16x16x32_bf16 a[52:55],a[232:235],v[164:167],a[52:55]
v_exp_f32 v247,v247
v_mfma_f32_16x16x32_bf16 a[56:59],a[240:243],v[164:167],a[56:59]
v_exp_f32 v252,v252
v_mfma_f32_16x16x32_bf16 a[56:59],a[244:247],v[180:183],a[56:59]
v_exp_f32 v253,v253
v_mfma_f32_16x16x32_bf16 a[60:63],a[252:255],v[180:183],a[60:63]
v_exp_f32 v254,v254
v_mfma_f32_16x16x32_bf16 a[60:63],a[248:251],v[164:167],a[60:63]
v_exp_f32 v255,v255
v_mfma_f32_16x16x32_bf16 v[52:55],v[28:31],v[164:167],v[52:55]
v_cvt_pk_bf16_f32 v245,v246,v247
v_cvt_pk_bf16_f32 v246,v252,v253
v_mfma_f32_16x16x32_bf16 v[52:55],v[28:31],v[180:183],v[52:55]
s_waitcnt lgkmcnt(1)
v_cvt_pk_bf16_f32 v247,v254,v255
v_max_f32 v20,0x00800000,v48
v_mfma_f32_16x16x32_bf16 v[132:135],a[128:131],v[128:131],v[32:35]
v_rcp_f32 v20,v20
v_mfma_f32_16x16x32_bf16 v[132:135],a[132:135],v[160:163],v[132:135]
v_accvgpr_read_b32 v64,a0
v_accvgpr_read_b32 v65,a1
v_mfma_f32_16x16x32_bf16 v[132:135],a[136:139],v[192:195],v[132:135]
v_accvgpr_read_b32 v66,a2
v_accvgpr_read_b32 v67,a3
v_mfma_f32_16x16x32_bf16 v[132:135],a[140:143],v[224:227],v[132:135]
v_mul_f32 v64,v20,v64
v_mul_f32 v65,v20,v65
v_mfma_f32_16x16x32_bf16 v[140:143],a[156:159],v[224:227],v[32:35]
v_cvt_pk_bf16_f32 v64,v64,v65
v_mul_f32 v66,v20,v66
v_mfma_f32_16x16x32_bf16 v[140:143],a[152:155],v[192:195],v[140:143]
v_mul_f32 v67,v20,v67
v_cvt_pk_bf16_f32 v65,v66,v67
v_mfma_f32_16x16x32_bf16 v[140:143],a[148:151],v[160:163],v[140:143]
buffer_load_dwordx4 v5,s[20:23],s47 offen offset:3072 nt lds
v_accvgpr_read_b32 v14,a6
v_mfma_f32_16x16x32_bf16 v[140:143],a[144:147],v[128:131],v[140:143]
v_accvgpr_read_b32 v15,a7
v_accvgpr_read_b32 v66,a4
v_mfma_f32_16x16x32_bf16 v[148:151],a[160:163],v[128:131],v[32:35]
v_accvgpr_read_b32 v67,a5
v_accvgpr_read_b32 v68,a8
v_mfma_f32_16x16x32_bf16 v[148:151],a[164:167],v[160:163],v[148:151]
v_exp_f32 v132,v132
v_mfma_f32_16x16x32_bf16 v[148:151],a[168:171],v[192:195],v[148:151]
v_exp_f32 v133,v133
v_mfma_f32_16x16x32_bf16 v[148:151],a[172:175],v[224:227],v[148:151]
v_exp_f32 v134,v134
v_mfma_f32_16x16x32_bf16 v[156:159],a[188:191],v[224:227],v[32:35]
v_exp_f32 v135,v135
v_mfma_f32_16x16x32_bf16 v[156:159],a[184:187],v[192:195],v[156:159]
v_cvt_pk_bf16_f32 v132,v132,v133
v_cvt_pk_bf16_f32 v133,v134,v135
v_mfma_f32_16x16x32_bf16 v[156:159],a[180:183],v[160:163],v[156:159]
s_waitcnt lgkmcnt(0)
v_exp_f32 v140,v140
v_mfma_f32_16x16x32_bf16 v[156:159],a[176:179],v[128:131],v[156:159]
v_exp_f32 v141,v141
v_mfma_f32_16x16x32_bf16 a[64:67],a[192:195],v[196:199],a[64:67]
v_exp_f32 v142,v142
v_mfma_f32_16x16x32_bf16 a[64:67],a[196:199],v[212:215],a[64:67]
s_add_u32 m0,s49,s52
v_exp_f32 v143,v143
v_mfma_f32_16x16x32_bf16 a[68:71],a[204:207],v[212:215],a[68:71]
s_mov_b32 s49,s50
v_exp_f32 v148,v148
v_mfma_f32_16x16x32_bf16 a[68:71],a[200:203],v[196:199],a[68:71]
s_add_u32 s50,s50,s48
v_exp_f32 v149,v149
v_mfma_f32_16x16x32_bf16 a[72:75],a[208:211],v[196:199],a[72:75]
buffer_load_dwordx4 v4,s[24:27],s47 offen nt lds
v_cvt_pk_bf16_f32 v134,v140,v141
v_mfma_f32_16x16x32_bf16 a[72:75],a[212:215],v[212:215],a[72:75]
)A"
R"A(v_exp_f32 v150,v150
v_mfma_f32_16x16x32_bf16 a[76:79],a[220:223],v[212:215],a[76:79]
v_exp_f32 v151,v151
v_mfma_f32_16x16x32_bf16 a[76:79],a[216:219],v[196:199],a[76:79]
v_cvt_pk_bf16_f32 v135,v142,v143
v_cvt_pk_bf16_f32 v148,v148,v149
v_mfma_f32_16x16x32_bf16 a[80:83],a[224:227],v[196:199],a[80:83]
v_exp_f32 v156,v156
v_mfma_f32_16x16x32_bf16 a[80:83],a[228:231],v[212:215],a[80:83]
v_exp_f32 v157,v157
v_mfma_f32_16x16x32_bf16 a[84:87],a[236:239],v[212:215],a[84:87]
v_exp_f32 v158,v158
v_mfma_f32_16x16x32_bf16 a[84:87],a[232:235],v[196:199],a[84:87]
v_exp_f32 v159,v159
v_mfma_f32_16x16x32_bf16 a[88:91],a[240:243],v[196:199],a[88:91]
v_cvt_pk_bf16_f32 v149,v150,v151
v_cvt_pk_bf16_f32 v150,v156,v157
v_mfma_f32_16x16x32_bf16 a[88:91],a[244:247],v[212:215],a[88:91]
v_cvt_pk_bf16_f32 v151,v158,v159
v_accvgpr_read_b32 v69,a9
v_mfma_f32_16x16x32_bf16 a[92:95],a[252:255],v[212:215],a[92:95]
v_accvgpr_read_b32 v70,a10
v_accvgpr_read_b32 v71,a11
v_mfma_f32_16x16x32_bf16 a[92:95],a[248:251],v[196:199],a[92:95]
v_mul_f32 v68,v20,v68
v_mul_f32 v69,v20,v69
v_mfma_f32_16x16x32_bf16 v[56:59],v[28:31],v[196:199],v[56:59]
v_cvt_pk_bf16_f32 v68,v68,v69
v_mul_f32 v70,v20,v70
v_mfma_f32_16x16x32_bf16 v[56:59],v[28:31],v[212:215],v[56:59]
v_mul_f32 v71,v20,v71
v_cvt_pk_bf16_f32 v69,v70,v71
v_mfma_f32_16x16x32_bf16 v[164:167],a[128:131],v[136:139],v[36:39]
buffer_load_dwordx4 v5,s[24:27],s47 offen offset:1024 nt lds
v_accvgpr_read_b32 v16,a14
v_mfma_f32_16x16x32_bf16 v[164:167],a[132:135],v[168:171],v[164:167]
v_accvgpr_read_b32 v17,a15
v_accvgpr_read_b32 v70,a12
v_mfma_f32_16x16x32_bf16 v[164:167],a[136:139],v[200:203],v[164:167]
v_accvgpr_read_b32 v71,a13
v_accvgpr_read_b32 v72,a16
v_mfma_f32_16x16x32_bf16 v[164:167],a[140:143],v[232:235],v[164:167]
v_accvgpr_read_b32 v73,a17
v_accvgpr_read_b32 v74,a18
v_mfma_f32_16x16x32_bf16 v[172:175],a[156:159],v[232:235],v[36:39]
v_accvgpr_read_b32 v75,a19
v_mul_f32 v72,v20,v72
v_mfma_f32_16x16x32_bf16 v[172:175],a[152:155],v[200:203],v[172:175]
v_mul_f32 v73,v20,v73
v_cvt_pk_bf16_f32 v72,v72,v73
v_mfma_f32_16x16x32_bf16 v[172:175],a[148:151],v[168:171],v[172:175]
v_mul_f32 v74,v20,v74
v_mul_f32 v75,v20,v75
v_mfma_f32_16x16x32_bf16 v[172:175],a[144:147],v[136:139],v[172:175]
v_cvt_pk_bf16_f32 v73,v74,v75
v_accvgpr_read_b32 v12,a22
v_mfma_f32_16x16x32_bf16 v[180:183],a[160:163],v[136:139],v[36:39]
v_accvgpr_read_b32 v13,a23
v_accvgpr_read_b32 v74,a20
v_mfma_f32_16x16x32_bf16 v[180:183],a[164:167],v[168:171],v[180:183]
v_accvgpr_read_b32 v75,a21
v_accvgpr_read_b32 v76,a24
v_mfma_f32_16x16x32_bf16 v[180:183],a[168:171],v[200:203],v[180:183]
v_accvgpr_read_b32 v77,a25
v_accvgpr_read_b32 v78,a26
v_mfma_f32_16x16x32_bf16 v[180:183],a[172:175],v[232:235],v[180:183]
v_accvgpr_read_b32 v79,a27
v_mul_f32 v66,v20,v66
v_mfma_f32_16x16x32_bf16 v[188:191],a[188:191],v[232:235],v[36:39]
v_mul_f32 v67,v20,v67
v_mul_f32 v76,v20,v76
v_mfma_f32_16x16x32_bf16 v[188:191],a[184:187],v[200:203],v[188:191]
v_mul_f32 v77,v20,v77
v_mul_f32 v14,v20,v14
v_mfma_f32_16x16x32_bf16 v[188:191],a[180:183],v[168:171],v[188:191]
buffer_load_dwordx4 v4,s[24:27],s47 offen offset:2048 nt lds
v_mul_f32 v15,v20,v15
v_mfma_f32_16x16x32_bf16 v[188:191],a[176:179],v[136:139],v[188:191]
v_cvt_pk_bf16_f32 v66,v66,v67
v_cvt_pk_bf16_f32 v76,v76,v77
v_mfma_f32_16x16x32_bf16 a[96:99],a[192:195],v[228:231],a[96:99]
ds_read_b64_tr_b16 a[192:193],v2
ds_read_b64_tr_b16 a[194:195],v2 offset:4096
v_mfma_f32_16x16x32_bf16 a[96:99],a[196:199],v[244:247],a[96:99]
ds_read_b64_tr_b16 a[196:197],v2 offset:8192
v_mul_f32 v78,v20,v78
v_mfma_f32_16x16x32_bf16 a[100:103],a[204:207],v[244:247],a[100:103]
ds_read_b64_tr_b16 a[198:199],v2 offset:12288
v_mul_f32 v79,v20,v79
v_mfma_f32_16x16x32_bf16 a[100:103],a[200:203],v[228:231],a[100:103]
ds_read_b64_tr_b16 a[204:205],v3 offset:8192
v_cvt_pk_bf16_f32 v67,v14,v15
v_mfma_f32_16x16x32_bf16 a[104:107],a[208:211],v[228:231],a[104:107]
ds_read_b64_tr_b16 a[206:207],v3 offset:12288
ds_read_b64_tr_b16 a[200:201],v3
v_mfma_f32_16x16x32_bf16 a[104:107],a[212:215],v[244:247],a[104:107]
ds_read_b64_tr_b16 a[202:203],v3 offset:4096
v_cvt_pk_bf16_f32 v77,v78,v79
v_mfma_f32_16x16x32_bf16 a[108:111],a[220:223],v[244:247],a[108:111]
ds_read_b64_tr_b16 a[208:209],v2 offset:256
v_accvgpr_read_b32 v14,a30
v_mfma_f32_16x16x32_bf16 a[108:111],a[216:219],v[228:231],a[108:111]
ds_read_b64_tr_b16 a[210:211],v2 offset:4352
v_accvgpr_read_b32 v15,a31
v_mfma_f32_16x16x32_bf16 a[112:115],a[224:227],v[228:231],a[112:115]
ds_read_b64_tr_b16 a[212:213],v2 offset:8448
ds_read_b64_tr_b16 a[214:215],v2 offset:12544
v_mfma_f32_16x16x32_bf16 a[112:115],a[228:231],v[244:247],a[112:115]
ds_read_b64_tr_b16 a[220:221],v3 offset:8448
v_accvgpr_read_b32 v78,a28
v_mfma_f32_16x16x32_bf16 a[116:119],a[236:239],v[244:247],a[116:119]
ds_read_b64_tr_b16 a[222:223],v3 offset:12544
v_accvgpr_read_b32 v79,a29
v_mfma_f32_16x16x32_bf16 a[116:119],a[232:235],v[228:231],a[116:119]
ds_read_b64_tr_b16 a[216:217],v3 offset:256
v_max3_f32 v44,v44,v45,v46
v_mfma_f32_16x16x32_bf16 a[120:123],a[240:243],v[228:231],a[120:123]
buffer_load_dwordx4 v5,s[24:27],s47 offen offset:3072 nt lds
ds_read_b64_tr_b16 a[218:219],v3 offset:4352
v_mfma_f32_16x16x32_bf16 a[120:123],a[244:247],v[244:247],a[120:123]
s_add_u32 s47,s47,16384
ds_read_b64_tr_b16 a[224:225],v2 offset:512
ds_read_b64_tr_b16 a[226:227],v2 offset:4608
v_mfma_f32_16x16x32_bf16 a[124:127],a[252:255],v[244:247],a[124:127]
s_and_b32 s47,s47,0x3ffff
ds_read_b64_tr_b16 a[228:229],v2 offset:8704
v_cmp_nlt_f32 s[56:57],v48,s63
v_mfma_f32_16x16x32_bf16 a[124:127],a[248:251],v[228:231],a[124:127]
s_or_b32 s47,s47,s46
ds_read_b64_tr_b16 a[230:231],v2 offset:12800
v_max3_f32 v44,v44,v47,s101
v_mfma_f32_16x16x32_bf16 v[60:63],v[28:31],v[228:231],v[60:63]
s_and_b32 s46,s54,0xfffc0000
ds_read_b64_tr_b16 a[236:237],v3 offset:8704
v_sub_f32 v47,0,v44
v_mfma_f32_16x16x32_bf16 v[60:63],v[28:31],v[244:247],v[60:63]
ds_read_b64_tr_b16 a[238:239],v3 offset:12800
ds_read_b64_tr_b16 a[232:233],v3 offset:512
v_mfma_f32_16x16x32_bf16 v[196:199],a[128:131],v[144:147],v[40:43]
ds_read_b64_tr_b16 a[234:235],v3 offset:4608
v_sub_f32 v46,0,v44
v_mfma_f32_16x16x32_bf16 v[196:199],a[132:135],v[176:179],v[196:199]
ds_read_b64_tr_b16 a[240:241],v2 offset:768
v_sub_f32 v45,0,v44
v_mfma_f32_16x16x32_bf16 v[196:199],a[136:139],v[208:211],v[196:199]
ds_read_b64_tr_b16 a[242:243],v2 offset:4864
v_sub_f32 v44,0,v44
v_mfma_f32_16x16x32_bf16 v[196:199],a[140:143],v[240:243],v[196:199]
ds_read_b64_tr_b16 a[244:245],v2 offset:8960
ds_read_b64_tr_b16 a[246:247],v2 offset:13056
v_mfma_f32_16x16x32_bf16 v[204:207],a[156:159],v[240:243],v[40:43]
ds_read_b64_tr_b16 a[252:253],v3 offset:8960
v_max_f32 v21,0x00800000,v52
v_mfma_f32_16x16x32_bf16 v[204:207],a[152:155],v[208:211],v[204:207]
ds_read_b64_tr_b16 a[254:255],v3 offset:13056
v_accvgpr_read_b32 v80,a32
v_mfma_f32_16x16x32_bf16 v[204:207],a[148:151],v[176:179],v[204:207]
ds_read_b64_tr_b16 a[248:249],v3 offset:768
v_accvgpr_read_b32 v81,a33
v_mfma_f32_16x16x32_bf16 v[204:207],a[144:147],v[144:147],v[204:207]
ds_read_b64_tr_b16 a[250:251],v3 offset:4864
v_accvgpr_read_b32 v82,a34
v_mfma_f32_16x16x32_bf16 v[212:215],a[160:163],v[144:147],v[40:43]
v_exp_f32 v164,v164
v_mfma_f32_16x16x32_bf16 v[212:215],a[164:167],v[176:179],v[212:215]
v_exp_f32 v165,v165
v_mfma_f32_16x16x32_bf16 v[212:215],a[168:171],v[208:211],v[212:215]
v_exp_f32 v166,v166
v_mfma_f32_16x16x32_bf16 v[212:215],a[172:175],v[240:243],v[212:215]
v_exp_f32 v167,v167
v_mfma_f32_16x16x32_bf16 v[220:223],a[188:191],v[240:243],v[40:43]
v_cvt_pk_bf16_f32 v164,v164,v165
v_cvt_pk_bf16_f32 v165,v166,v167
v_mfma_f32_16x16x32_bf16 v[220:223],a[184:187],v[208:211],v[220:223]
v_exp_f32 v172,v172
v_mfma_f32_16x16x32_bf16 v[220:223],a[180:183],v[176:179],v[220:223]
v_exp_f32 v173,v173
v_mfma_f32_16x16x32_bf16 v[220:223],a[176:179],v[144:147],v[220:223]
s_waitcnt lgkmcnt(0)
v_exp_f32 v174,v174
v_mfma_f32_16x16x32_bf16 a[0:3],a[192:195],v[132:135],0
v_exp_f32 v175,v175
v_mfma_f32_16x16x32_bf16 a[0:3],a[196:199],v[148:151],a[0:3]
v_exp_f32 v180,v180
v_mfma_f32_16x16x32_bf16 a[4:7],a[204:207],v[148:151],0
v_exp_f32 v181,v181
v_mfma_f32_16x16x32_bf16 a[4:7],a[200:203],v[132:135],a[4:7]
v_exp_f32 v182,v182
v_mfma_f32_16x16x32_bf16 a[8:11],a[208:211],v[132:135],0
v_exp_f32 v183,v183
v_mfma_f32_16x16x32_bf16 a[8:11],a[212:215],v[148:151],a[8:11]
v_cvt_pk_bf16_f32 v166,v172,v173
v_cvt_pk_bf16_f32 v167,v174,v175
v_mfma_f32_16x16x32_bf16 a[12:15],a[220:223],v[148:151],0
v_cvt_pk_bf16_f32 v180,v180,v181
v_cvt_pk_bf16_f32 v181,v182,v183
v_mfma_f32_16x16x32_bf16 a[12:15],a[216:219],v[132:135],a[12:15]
v_exp_f32 v188,v188
v_mfma_f32_16x16x32_bf16 a[16:19],a[224:227],v[132:135],0
v_exp_f32 v189,v189
v_mfma_f32_16x16x32_bf16 a[16:19],a[228:231],v[148:151],a[16:19]
v_exp_f32 v190,v190
v_mfma_f32_16x16x32_bf16 a[20:23],a[236:239],v[148:151],0
v_exp_f32 v191,v191
v_mfma_f32_16x16x32_bf16 a[20:23],a[232:235],v[132:135],a[20:23]
v_cvt_pk_bf16_f32 v182,v188,v189
v_cvt_pk_bf16_f32 v183,v190,v191
v_mfma_f32_16x16x32_bf16 a[24:27],a[240:243],v[132:135],0
v_rcp_f32 v21,v21
v_mfma_f32_16x16x32_bf16 a[24:27],a[244:247],v[148:151],a[24:27]
v_accvgpr_read_b32 v83,a35
v_mul_f32 v70,v20,v70
v_mfma_f32_16x16x32_bf16 a[28:31],a[252:255],v[148:151],0
v_mul_f32 v71,v20,v71
v_mul_f32 v80,v21,v80
v_mfma_f32_16x16x32_bf16 a[28:31],a[248:251],v[132:135],a[28:31]
v_mul_f32 v81,v21,v81
v_mul_f32 v16,v20,v16
v_mfma_f32_16x16x32_bf16 v[48:51],v[28:31],v[132:135],0
v_mul_f32 v17,v20,v17
v_cvt_pk_bf16_f32 v70,v70,v71
v_mfma_f32_16x16x32_bf16 v[48:51],v[28:31],v[148:151],v[48:51]
v_cvt_pk_bf16_f32 v80,v80,v81
v_mul_f32 v82,v21,v82
s_waitcnt vmcnt(24)
s_barrier
v_mfma_f32_16x16x32_bf16 v[228:231],a[128:131],v[152:155],v[44:47]
s_add_u32 m0,s49,s51
v_mul_f32 v83,v21,v83
v_cvt_pk_bf16_f32 v71,v16,v17
v_mfma_f32_16x16x32_bf16 v[228:231],a[132:135],v[184:187],v[228:231]
v_cvt_pk_bf16_f32 v81,v82,v83
v_accvgpr_read_b32 v16,a38
v_mfma_f32_16x16x32_bf16 v[228:231],a[136:139],v[216:219],v[228:231]
buffer_load_dwordx4 v4,s[20:23],s47 offen nt lds
v_accvgpr_read_b32 v17,a39
v_mfma_f32_16x16x32_bf16 v[228:231],a[140:143],v[248:251],v[228:231]
v_accvgpr_read_b32 v82,a36
v_accvgpr_read_b32 v83,a37
v_mfma_f32_16x16x32_bf16 v[236:239],a[156:159],v[248:251],v[44:47]
v_accvgpr_read_b32 v84,a40
v_accvgpr_read_b32 v85,a41
v_mfma_f32_16x16x32_bf16 v[236:239],a[152:155],v[216:219],v[236:239]
v_accvgpr_read_b32 v86,a42
v_accvgpr_read_b32 v87,a43
v_mfma_f32_16x16x32_bf16 v[236:239],a[148:151],v[184:187],v[236:239]
v_mul_f32 v74,v20,v74
v_mul_f32 v75,v20,v75
v_mfma_f32_16x16x32_bf16 v[236:239],a[144:147],v[152:155],v[236:239]
v_mul_f32 v84,v21,v84
v_mul_f32 v85,v21,v85
v_mfma_f32_16x16x32_bf16 v[244:247],a[160:163],v[152:155],v[44:47]
v_mul_f32 v12,v20,v12
v_mul_f32 v13,v20,v13
v_mfma_f32_16x16x32_bf16 v[244:247],a[164:167],v[184:187],v[244:247]
v_cvt_pk_bf16_f32 v74,v74,v75
v_cvt_pk_bf16_f32 v84,v84,v85
v_mfma_f32_16x16x32_bf16 v[244:247],a[168:171],v[216:219],v[244:247]
v_mul_f32 v86,v21,v86
v_mul_f32 v87,v21,v87
v_mfma_f32_16x16x32_bf16 v[244:247],a[172:175],v[248:251],v[244:247]
v_cvt_pk_bf16_f32 v75,v12,v13
v_cvt_pk_bf16_f32 v85,v86,v87
v_mfma_f32_16x16x32_bf16 v[252:255],a[188:191],v[248:251],v[44:47]
v_accvgpr_read_b32 v12,a46
v_accvgpr_read_b32 v13,a47
v_mfma_f32_16x16x32_bf16 v[252:255],a[184:187],v[216:219],v[252:255]
v_accvgpr_read_b32 v86,a44
v_accvgpr_read_b32 v87,a45
v_mfma_f32_16x16x32_bf16 v[252:255],a[180:183],v[184:187],v[252:255]
v_accvgpr_read_b32 v88,a48
v_accvgpr_read_b32 v89,a49
v_mfma_f32_16x16x32_bf16 v[252:255],a[176:179],v[152:155],v[252:255]
v_accvgpr_read_b32 v90,a50
v_accvgpr_read_b32 v91,a51
v_mfma_f32_16x16x32_bf16 a[32:35],a[192:195],v[164:167],0
buffer_load_dwordx4 v4,s[20:23],s47 offen offset:1024 nt lds
v_mul_f32 v78,v20,v78
v_mfma_f32_16x16x32_bf16 a[32:35],a[196:199],v[180:183],a[32:35]
v_mul_f32 v79,v20,v79
v_mul_f32 v88,v21,v88
v_mfma_f32_16x16x32_bf16 a[36:39],a[204:207],v[180:183],0
v_mul_f32 v89,v21,v89
v_mul_f32 v14,v20,v14
v_mfma_f32_16x16x32_bf16 a[36:39],a[200:203],v[164:167],a[36:39]
)A"
R"A(v_mul_f32 v15,v20,v15
v_cvt_pk_bf16_f32 v78,v78,v79
v_mfma_f32_16x16x32_bf16 a[40:43],a[208:211],v[164:167],0
v_add_u32 v1,s48,v1
v_cvt_pk_bf16_f32 v88,v88,v89
v_mfma_f32_16x16x32_bf16 a[40:43],a[212:215],v[180:183],a[40:43]
v_mul_f32 v90,v21,v90
v_mul_f32 v91,v21,v91
v_mfma_f32_16x16x32_bf16 a[44:47],a[220:223],v[180:183],0
v_cvt_pk_bf16_f32 v79,v14,v15
v_cvt_pk_bf16_f32 v89,v90,v91
v_mfma_f32_16x16x32_bf16 a[44:47],a[216:219],v[164:167],a[44:47]
v_accvgpr_read_b32 v14,a54
v_accvgpr_read_b32 v15,a55
v_mfma_f32_16x16x32_bf16 a[48:51],a[224:227],v[164:167],0
v_accvgpr_read_b32 v90,a52
v_accvgpr_read_b32 v91,a53
v_mfma_f32_16x16x32_bf16 a[48:51],a[228:231],v[180:183],a[48:51]
ds_read_b128 a[128:131],v1
v_accvgpr_read_b32 v92,a56
v_mfma_f32_16x16x32_bf16 a[52:55],a[236:239],v[180:183],0
ds_read_b128 a[132:135],v1 offset:256
v_mfma_f32_16x16x32_bf16 a[52:55],a[232:235],v[164:167],a[52:55]
ds_read_b128 a[136:139],v1 offset:512
v_accvgpr_read_b32 v93,a57
v_accvgpr_read_b32 v94,a58
v_accvgpr_read_b32 v95,a59
v_mfma_f32_16x16x32_bf16 a[56:59],a[240:243],v[164:167],0
ds_read_b128 a[140:143],v1 offset:768
v_mul_f32 v82,v21,v82
v_mul_f32 v83,v21,v83
v_mul_f32 v92,v21,v92
v_mul_f32 v93,v21,v93
v_mul_f32 v16,v21,v16
v_mul_f32 v17,v21,v17
v_cvt_pk_bf16_f32 v82,v82,v83
v_cvt_pk_bf16_f32 v92,v92,v93
v_mul_f32 v94,v21,v94
v_mul_f32 v95,v21,v95
v_mfma_f32_16x16x32_bf16 a[56:59],a[244:247],v[180:183],a[56:59]
ds_read_b128 a[156:159],v1 offset:4864
v_cvt_pk_bf16_f32 v83,v16,v17
v_cvt_pk_bf16_f32 v93,v94,v95
v_accvgpr_read_b32 v16,a62
v_accvgpr_read_b32 v17,a63
v_accvgpr_read_b32 v94,a60
v_accvgpr_read_b32 v95,a61
v_mfma_f32_16x16x32_bf16 a[60:63],a[252:255],v[180:183],0
ds_read_b128 a[152:155],v1 offset:4608
v_mfma_f32_16x16x32_bf16 a[60:63],a[248:251],v[164:167],a[60:63]
ds_read_b128 a[148:151],v1 offset:4352
v_cmp_nlt_f32 s[58:59],v52,s63
s_nop 1
s_or_b64 s[56:57],s[56:57],s[58:59]
v_mfma_f32_16x16x32_bf16 v[52:55],v[28:31],v[164:167],0
ds_read_b128 a[144:147],v1 offset:4096
v_mfma_f32_16x16x32_bf16 v[52:55],v[28:31],v[180:183],v[52:55]
s_waitcnt lgkmcnt(7)
buffer_load_dwordx4 v5,s[20:23],s47 offen offset:2048 nt lds
ds_read_b128 a[160:163],v1 offset:8192
v_mfma_f32_16x16x32_bf16 v[132:135],a[128:131],v[128:131],v[32:35]
s_waitcnt lgkmcnt(7)
ds_read_b128 a[164:167],v1 offset:8448
v_mfma_f32_16x16x32_bf16 v[132:135],a[132:135],v[160:163],v[132:135]
s_waitcnt lgkmcnt(7)
ds_read_b128 a[168:171],v1 offset:8704
v_mfma_f32_16x16x32_bf16 v[132:135],a[136:139],v[192:195],v[132:135]
s_waitcnt lgkmcnt(7)
ds_read_b128 a[172:175],v1 offset:8960
v_mfma_f32_16x16x32_bf16 v[132:135],a[140:143],v[224:227],v[132:135]
s_waitcnt lgkmcnt(7)
ds_read_b128 a[188:191],v1 offset:13056
v_mfma_f32_16x16x32_bf16 v[140:143],a[156:159],v[224:227],v[32:35]
s_waitcnt lgkmcnt(7)
ds_read_b128 a[184:187],v1 offset:12800
v_mfma_f32_16x16x32_bf16 v[140:143],a[152:155],v[192:195],v[140:143]
s_waitcnt lgkmcnt(7)
ds_read_b128 a[180:183],v1 offset:12544
v_mfma_f32_16x16x32_bf16 v[140:143],a[148:151],v[160:163],v[140:143]
s_waitcnt lgkmcnt(7)
ds_read_b128 a[176:179],v1 offset:12288
v_mfma_f32_16x16x32_bf16 v[140:143],a[144:147],v[128:131],v[140:143]
s_waitcnt lgkmcnt(7)
v_mfma_f32_16x16x32_bf16 v[148:151],a[160:163],v[128:131],v[32:35]
s_waitcnt lgkmcnt(6)
v_mfma_f32_16x16x32_bf16 v[148:151],a[164:167],v[160:163],v[148:151]
s_waitcnt lgkmcnt(5)
v_mfma_f32_16x16x32_bf16 v[148:151],a[168:171],v[192:195],v[148:151]
s_waitcnt lgkmcnt(4)
v_mfma_f32_16x16x32_bf16 v[148:151],a[172:175],v[224:227],v[148:151]
s_waitcnt lgkmcnt(3)
v_exp_f32 v196,v196
v_exp_f32 v197,v197
v_mfma_f32_16x16x32_bf16 v[156:159],a[188:191],v[224:227],v[32:35]
s_waitcnt lgkmcnt(2)
v_exp_f32 v198,v198
v_exp_f32 v199,v199
v_cvt_pk_bf16_f32 v196,v196,v197
v_exp_f32 v204,v204
v_exp_f32 v205,v205
v_exp_f32 v206,v206
v_exp_f32 v207,v207
v_exp_f32 v212,v212
v_exp_f32 v213,v213
v_mfma_f32_16x16x32_bf16 v[156:159],a[184:187],v[192:195],v[156:159]
s_waitcnt lgkmcnt(1)
buffer_load_dwordx4 v5,s[20:23],s47 offen offset:3072 nt lds
v_cvt_pk_bf16_f32 v197,v198,v199
v_exp_f32 v214,v214
v_exp_f32 v215,v215
v_cvt_pk_bf16_f32 v198,v204,v205
v_cvt_pk_bf16_f32 v199,v206,v207
v_cvt_pk_bf16_f32 v212,v212,v213
v_exp_f32 v220,v220
v_exp_f32 v221,v221
v_exp_f32 v222,v222
v_exp_f32 v223,v223
v_mfma_f32_16x16x32_bf16 v[156:159],a[180:183],v[160:163],v[156:159]
s_waitcnt lgkmcnt(0)
v_max_f32 v22,0x00800000,v56
v_cvt_pk_bf16_f32 v213,v214,v215
v_cvt_pk_bf16_f32 v214,v220,v221
v_cvt_pk_bf16_f32 v215,v222,v223
v_mfma_f32_16x16x32_bf16 v[156:159],a[176:179],v[128:131],v[156:159]
v_rcp_f32 v22,v22
v_accvgpr_read_b32 v96,a64
v_accvgpr_read_b32 v97,a65
v_accvgpr_read_b32 v98,a66
v_accvgpr_read_b32 v99,a67
v_mfma_f32_16x16x32_bf16 a[64:67],a[192:195],v[196:199],0
v_mul_f32 v86,v21,v86
v_mul_f32 v87,v21,v87
v_mul_f32 v96,v22,v96
v_mul_f32 v97,v22,v97
v_mul_f32 v12,v21,v12
v_mul_f32 v13,v21,v13
v_cvt_pk_bf16_f32 v86,v86,v87
v_cvt_pk_bf16_f32 v96,v96,v97
v_mul_f32 v98,v22,v98
v_mul_f32 v99,v22,v99
v_mfma_f32_16x16x32_bf16 a[64:67],a[196:199],v[212:215],a[64:67]
v_cvt_pk_bf16_f32 v87,v12,v13
v_cvt_pk_bf16_f32 v97,v98,v99
v_accvgpr_read_b32 v12,a70
v_accvgpr_read_b32 v13,a71
v_accvgpr_read_b32 v98,a68
v_accvgpr_read_b32 v99,a69
v_mfma_f32_16x16x32_bf16 a[68:71],a[204:207],v[212:215],0
v_accvgpr_read_b32 v100,a72
v_accvgpr_read_b32 v101,a73
v_mfma_f32_16x16x32_bf16 a[68:71],a[200:203],v[196:199],a[68:71]
v_accvgpr_read_b32 v102,a74
v_accvgpr_read_b32 v103,a75
v_mfma_f32_16x16x32_bf16 a[72:75],a[208:211],v[196:199],0
v_mul_f32 v90,v21,v90
v_mul_f32 v91,v21,v91
v_mul_f32 v100,v22,v100
v_mul_f32 v101,v22,v101
v_mul_f32 v14,v21,v14
v_mul_f32 v15,v21,v15
v_cvt_pk_bf16_f32 v90,v90,v91
v_cvt_pk_bf16_f32 v100,v100,v101
v_mul_f32 v102,v22,v102
v_mul_f32 v103,v22,v103
v_mfma_f32_16x16x32_bf16 a[72:75],a[212:215],v[212:215],a[72:75]
v_cvt_pk_bf16_f32 v91,v14,v15
v_cvt_pk_bf16_f32 v101,v102,v103
v_accvgpr_read_b32 v14,a78
v_accvgpr_read_b32 v15,a79
v_accvgpr_read_b32 v102,a76
v_accvgpr_read_b32 v103,a77
v_mfma_f32_16x16x32_bf16 a[76:79],a[220:223],v[212:215],0
s_add_u32 m0,s49,s52
v_accvgpr_read_b32 v104,a80
v_accvgpr_read_b32 v105,a81
v_mfma_f32_16x16x32_bf16 a[76:79],a[216:219],v[196:199],a[76:79]
s_mov_b32 s49,s50
v_accvgpr_read_b32 v106,a82
v_accvgpr_read_b32 v107,a83
v_mfma_f32_16x16x32_bf16 a[80:83],a[224:227],v[196:199],0
v_mul_f32 v94,v21,v94
v_mul_f32 v95,v21,v95
v_mul_f32 v104,v22,v104
v_mul_f32 v105,v22,v105
v_mul_f32 v16,v21,v16
v_mul_f32 v17,v21,v17
v_cvt_pk_bf16_f32 v94,v94,v95
v_cvt_pk_bf16_f32 v104,v104,v105
v_mul_f32 v106,v22,v106
v_mul_f32 v107,v22,v107
v_mfma_f32_16x16x32_bf16 a[80:83],a[228:231],v[212:215],a[80:83]
v_cvt_pk_bf16_f32 v95,v16,v17
v_cvt_pk_bf16_f32 v105,v106,v107
v_accvgpr_read_b32 v16,a86
v_accvgpr_read_b32 v17,a87
v_accvgpr_read_b32 v106,a84
v_accvgpr_read_b32 v107,a85
v_mfma_f32_16x16x32_bf16 a[84:87],a[236:239],v[212:215],0
v_accvgpr_read_b32 v108,a88
v_accvgpr_read_b32 v109,a89
v_mfma_f32_16x16x32_bf16 a[84:87],a[232:235],v[196:199],a[84:87]
buffer_load_dwordx4 v4,s[24:27],s47 offen nt lds
v_accvgpr_read_b32 v110,a90
v_accvgpr_read_b32 v111,a91
v_mfma_f32_16x16x32_bf16 a[88:91],a[240:243],v[196:199],0
v_mul_f32 v98,v22,v98
v_mul_f32 v99,v22,v99
v_mul_f32 v108,v22,v108
v_mul_f32 v109,v22,v109
v_mul_f32 v12,v22,v12
v_mul_f32 v13,v22,v13
v_cvt_pk_bf16_f32 v98,v98,v99
v_cvt_pk_bf16_f32 v108,v108,v109
v_mul_f32 v110,v22,v110
v_mul_f32 v111,v22,v111
v_mfma_f32_16x16x32_bf16 a[88:91],a[244:247],v[212:215],a[88:91]
v_cvt_pk_bf16_f32 v99,v12,v13
v_cvt_pk_bf16_f32 v109,v110,v111
v_accvgpr_read_b32 v12,a94
v_accvgpr_read_b32 v13,a95
v_accvgpr_read_b32 v110,a92
v_accvgpr_read_b32 v111,a93
v_mfma_f32_16x16x32_bf16 a[92:95],a[252:255],v[212:215],0
v_cmp_nlt_f32 s[58:59],v56,s63
s_nop 1
s_or_b64 s[56:57],s[56:57],s[58:59]
v_mfma_f32_16x16x32_bf16 a[92:95],a[248:251],v[196:199],a[92:95]
v_exp_f32 v228,v228
v_mfma_f32_16x16x32_bf16 v[56:59],v[28:31],v[196:199],0
v_exp_f32 v229,v229
v_mfma_f32_16x16x32_bf16 v[56:59],v[28:31],v[212:215],v[56:59]
v_exp_f32 v230,v230
v_mfma_f32_16x16x32_bf16 v[164:167],a[128:131],v[136:139],v[36:39]
v_exp_f32 v231,v231
v_mfma_f32_16x16x32_bf16 v[164:167],a[132:135],v[168:171],v[164:167]
v_cvt_pk_bf16_f32 v228,v228,v229
v_cvt_pk_bf16_f32 v229,v230,v231
v_mfma_f32_16x16x32_bf16 v[164:167],a[136:139],v[200:203],v[164:167]
v_exp_f32 v236,v236
v_mfma_f32_16x16x32_bf16 v[164:167],a[140:143],v[232:235],v[164:167]
v_exp_f32 v237,v237
v_mfma_f32_16x16x32_bf16 v[172:175],a[156:159],v[232:235],v[36:39]
buffer_load_dwordx4 v5,s[24:27],s47 offen offset:1024 nt lds
v_cvt_pk_bf16_f32 v230,v236,v237
v_mfma_f32_16x16x32_bf16 v[172:175],a[152:155],v[200:203],v[172:175]
v_exp_f32 v238,v238
v_mfma_f32_16x16x32_bf16 v[172:175],a[148:151],v[168:171],v[172:175]
v_exp_f32 v239,v239
v_mfma_f32_16x16x32_bf16 v[172:175],a[144:147],v[136:139],v[172:175]
v_exp_f32 v244,v244
v_mfma_f32_16x16x32_bf16 v[180:183],a[160:163],v[136:139],v[36:39]
v_exp_f32 v245,v245
v_mfma_f32_16x16x32_bf16 v[180:183],a[164:167],v[168:171],v[180:183]
v_exp_f32 v246,v246
v_mfma_f32_16x16x32_bf16 v[180:183],a[168:171],v[200:203],v[180:183]
v_exp_f32 v247,v247
v_mfma_f32_16x16x32_bf16 v[180:183],a[172:175],v[232:235],v[180:183]
v_cvt_pk_bf16_f32 v231,v238,v239
v_cvt_pk_bf16_f32 v244,v244,v245
v_mfma_f32_16x16x32_bf16 v[188:191],a[188:191],v[232:235],v[36:39]
v_exp_f32 v252,v252
v_mfma_f32_16x16x32_bf16 v[188:191],a[184:187],v[200:203],v[188:191]
v_exp_f32 v253,v253
v_exp_f32 v254,v254
v_exp_f32 v255,v255
v_mfma_f32_16x16x32_bf16 v[188:191],a[180:183],v[168:171],v[188:191]
v_max_f32 v23,0x00800000,v60
v_cvt_pk_bf16_f32 v245,v246,v247
v_cvt_pk_bf16_f32 v246,v252,v253
v_cvt_pk_bf16_f32 v247,v254,v255
v_mfma_f32_16x16x32_bf16 v[188:191],a[176:179],v[136:139],v[188:191]
v_rcp_f32 v23,v23
v_accvgpr_read_b32 v112,a96
v_accvgpr_read_b32 v113,a97
v_accvgpr_read_b32 v114,a98
v_accvgpr_read_b32 v115,a99
v_mfma_f32_16x16x32_bf16 a[96:99],a[192:195],v[228:231],0
v_mul_f32 v102,v22,v102
v_mul_f32 v103,v22,v103
v_mul_f32 v112,v23,v112
v_mul_f32 v113,v23,v113
v_mul_f32 v14,v22,v14
v_mul_f32 v15,v22,v15
v_cvt_pk_bf16_f32 v102,v102,v103
v_cvt_pk_bf16_f32 v112,v112,v113
v_mul_f32 v114,v23,v114
v_mul_f32 v115,v23,v115
v_mfma_f32_16x16x32_bf16 a[96:99],a[196:199],v[244:247],a[96:99]
v_cvt_pk_bf16_f32 v103,v14,v15
v_cvt_pk_bf16_f32 v113,v114,v115
v_accvgpr_read_b32 v14,a102
v_accvgpr_read_b32 v15,a103
v_accvgpr_read_b32 v114,a100
v_accvgpr_read_b32 v115,a101
v_mfma_f32_16x16x32_bf16 a[100:103],a[204:207],v[244:247],0
v_accvgpr_read_b32 v116,a104
v_accvgpr_read_b32 v117,a105
v_mfma_f32_16x16x32_bf16 a[100:103],a[200:203],v[228:231],a[100:103]
v_accvgpr_read_b32 v118,a106
v_accvgpr_read_b32 v119,a107
v_mfma_f32_16x16x32_bf16 a[104:107],a[208:211],v[228:231],0
v_mul_f32 v106,v22,v106
v_mul_f32 v107,v22,v107
v_mul_f32 v116,v23,v116
v_mul_f32 v117,v23,v117
v_mul_f32 v16,v22,v16
v_mul_f32 v17,v22,v17
v_cvt_pk_bf16_f32 v106,v106,v107
v_cvt_pk_bf16_f32 v116,v116,v117
v_mul_f32 v118,v23,v118
v_mul_f32 v119,v23,v119
v_mfma_f32_16x16x32_bf16 a[104:107],a[212:215],v[244:247],a[104:107]
buffer_load_dwordx4 v4,s[24:27],s47 offen offset:2048 nt lds
v_cvt_pk_bf16_f32 v107,v16,v17
v_cvt_pk_bf16_f32 v117,v118,v119
v_accvgpr_read_b32 v16,a110
v_accvgpr_read_b32 v17,a111
v_accvgpr_read_b32 v118,a108
v_accvgpr_read_b32 v119,a109
v_mfma_f32_16x16x32_bf16 a[108:111],a[220:223],v[244:247],0
v_accvgpr_read_b32 v120,a112
v_accvgpr_read_b32 v121,a113
v_mfma_f32_16x16x32_bf16 a[108:111],a[216:219],v[228:231],a[108:111]
v_accvgpr_read_b32 v122,a114
v_accvgpr_read_b32 v123,a115
v_mfma_f32_16x16x32_bf16 a[112:115],a[224:227],v[228:231],0
v_mul_f32 v110,v22,v110
v_mul_f32 v111,v22,v111
v_mul_f32 v120,v23,v120
v_mul_f32 v121,v23,v121
v_mul_f32 v12,v22,v12
v_mul_f32 v13,v22,v13
v_cvt_pk_bf16_f32 v110,v110,v111
v_cvt_pk_bf16_f32 v120,v120,v121
v_mul_f32 v122,v23,v122
v_mul_f32 v123,v23,v123
v_mfma_f32_16x16x32_bf16 a[112:115],a[228:231],v[244:247],a[112:115]
v_cvt_pk_bf16_f32 v111,v12,v13
v_cvt_pk_bf16_f32 v121,v122,v123
v_accvgpr_read_b32 v12,a118
v_accvgpr_read_b32 v13,a119
v_accvgpr_read_b32 v122,a116
)A"
R"A(v_accvgpr_read_b32 v123,a117
v_mfma_f32_16x16x32_bf16 a[116:119],a[236:239],v[244:247],0
v_accvgpr_read_b32 v124,a120
v_accvgpr_read_b32 v125,a121
v_mfma_f32_16x16x32_bf16 a[116:119],a[232:235],v[228:231],a[116:119]
v_accvgpr_read_b32 v126,a122
v_accvgpr_read_b32 v127,a123
v_mfma_f32_16x16x32_bf16 a[120:123],a[240:243],v[228:231],0
v_mul_f32 v114,v23,v114
v_mul_f32 v115,v23,v115
v_mul_f32 v124,v23,v124
v_mul_f32 v125,v23,v125
v_mul_f32 v14,v23,v14
v_mul_f32 v15,v23,v15
v_cvt_pk_bf16_f32 v114,v114,v115
v_cvt_pk_bf16_f32 v124,v124,v125
v_mul_f32 v126,v23,v126
v_mul_f32 v127,v23,v127
v_mfma_f32_16x16x32_bf16 a[120:123],a[244:247],v[244:247],a[120:123]
v_cvt_pk_bf16_f32 v115,v14,v15
v_cvt_pk_bf16_f32 v125,v126,v127
v_accvgpr_read_b32 v14,a126
v_accvgpr_read_b32 v15,a127
v_accvgpr_read_b32 v126,a124
v_accvgpr_read_b32 v127,a125
v_mfma_f32_16x16x32_bf16 a[124:127],a[252:255],v[244:247],0
v_cmp_nlt_f32 s[58:59],v60,s63
s_nop 1
s_or_b64 s[56:57],s[56:57],s[58:59]
v_mfma_f32_16x16x32_bf16 a[124:127],a[248:251],v[228:231],a[124:127]
v_add_u32 v2,s48,v2
v_add_u32 v3,s48,v3
v_mfma_f32_16x16x32_bf16 v[60:63],v[28:31],v[228:231],0
buffer_load_dwordx4 v5,s[24:27],s47 offen offset:3072 nt lds
ds_read_b64_tr_b16 a[192:193],v2
v_mfma_f32_16x16x32_bf16 v[60:63],v[28:31],v[244:247],v[60:63]
s_add_u32 s47,s47,16384
ds_read_b64_tr_b16 a[194:195],v2 offset:4096
ds_read_b64_tr_b16 a[196:197],v2 offset:8192
v_mfma_f32_16x16x32_bf16 v[196:199],a[128:131],v[144:147],v[40:43]
s_and_b32 s47,s47,0x3ffff
ds_read_b64_tr_b16 a[198:199],v2 offset:12288
v_mul_f32 v118,v23,v118
v_mfma_f32_16x16x32_bf16 v[196:199],a[132:135],v[176:179],v[196:199]
s_or_b32 s47,s47,s46
ds_read_b64_tr_b16 a[204:205],v3 offset:8192
v_mul_f32 v119,v23,v119
v_mfma_f32_16x16x32_bf16 v[196:199],a[136:139],v[208:211],v[196:199]
ds_read_b64_tr_b16 a[206:207],v3 offset:12288
v_mul_f32 v122,v23,v122
v_mfma_f32_16x16x32_bf16 v[196:199],a[140:143],v[240:243],v[196:199]
ds_read_b64_tr_b16 a[200:201],v3
ds_read_b64_tr_b16 a[202:203],v3 offset:4096
v_mfma_f32_16x16x32_bf16 v[204:207],a[156:159],v[240:243],v[40:43]
v_exp_f32 v132,v132
v_mfma_f32_16x16x32_bf16 v[204:207],a[152:155],v[208:211],v[204:207]
v_exp_f32 v133,v133
v_mfma_f32_16x16x32_bf16 v[204:207],a[148:151],v[176:179],v[204:207]
ds_read_b64_tr_b16 a[208:209],v2 offset:256
ds_read_b64_tr_b16 a[210:211],v2 offset:4352
v_mfma_f32_16x16x32_bf16 v[204:207],a[144:147],v[144:147],v[204:207]
v_exp_f32 v134,v134
v_mfma_f32_16x16x32_bf16 v[212:215],a[160:163],v[144:147],v[40:43]
v_exp_f32 v135,v135
v_mfma_f32_16x16x32_bf16 v[212:215],a[164:167],v[176:179],v[212:215]
ds_read_b64_tr_b16 a[212:213],v2 offset:8448
v_cvt_pk_bf16_f32 v132,v132,v133
v_mfma_f32_16x16x32_bf16 v[212:215],a[168:171],v[208:211],v[212:215]
v_exp_f32 v140,v140
v_mfma_f32_16x16x32_bf16 v[212:215],a[172:175],v[240:243],v[212:215]
v_exp_f32 v141,v141
v_mfma_f32_16x16x32_bf16 v[220:223],a[188:191],v[240:243],v[40:43]
ds_read_b64_tr_b16 a[214:215],v2 offset:12544
v_exp_f32 v142,v142
v_exp_f32 v143,v143
v_exp_f32 v148,v148
v_exp_f32 v149,v149
v_mfma_f32_16x16x32_bf16 v[220:223],a[184:187],v[208:211],v[220:223]
ds_read_b64_tr_b16 a[220:221],v3 offset:8448
ds_read_b64_tr_b16 a[222:223],v3 offset:12544
v_cvt_pk_bf16_f32 v133,v134,v135
v_exp_f32 v150,v150
v_exp_f32 v151,v151
v_cvt_pk_bf16_f32 v134,v140,v141
v_cvt_pk_bf16_f32 v135,v142,v143
v_cvt_pk_bf16_f32 v148,v148,v149
v_exp_f32 v156,v156
v_exp_f32 v157,v157
v_exp_f32 v158,v158
v_exp_f32 v159,v159
v_mfma_f32_16x16x32_bf16 v[220:223],a[180:183],v[176:179],v[220:223]
ds_read_b64_tr_b16 a[216:217],v3 offset:256
ds_read_b64_tr_b16 a[218:219],v3 offset:4352
v_cvt_pk_bf16_f32 v149,v150,v151
v_cvt_pk_bf16_f32 v150,v156,v157
v_cvt_pk_bf16_f32 v151,v158,v159
v_mfma_f32_16x16x32_bf16 v[220:223],a[176:179],v[144:147],v[220:223]
s_waitcnt lgkmcnt(6)
ds_read_b64_tr_b16 a[224:225],v2 offset:512
ds_read_b64_tr_b16 a[226:227],v2 offset:4608
v_mfma_f32_16x16x32_bf16 a[0:3],a[192:195],v[132:135],a[0:3]
ds_read_b64_tr_b16 a[228:229],v2 offset:8704
ds_read_b64_tr_b16 a[230:231],v2 offset:12800
v_mfma_f32_16x16x32_bf16 a[0:3],a[196:199],v[148:151],a[0:3]
ds_read_b64_tr_b16 a[236:237],v3 offset:8704
ds_read_b64_tr_b16 a[238:239],v3 offset:12800
v_mfma_f32_16x16x32_bf16 a[4:7],a[204:207],v[148:151],a[4:7]
ds_read_b64_tr_b16 a[232:233],v3 offset:512
ds_read_b64_tr_b16 a[234:235],v3 offset:4608
v_mfma_f32_16x16x32_bf16 a[4:7],a[200:203],v[132:135],a[4:7]
ds_read_b64_tr_b16 a[240:241],v2 offset:768
ds_read_b64_tr_b16 a[242:243],v2 offset:4864
v_mfma_f32_16x16x32_bf16 a[8:11],a[208:211],v[132:135],a[8:11]
s_waitcnt lgkmcnt(14)
ds_read_b64_tr_b16 a[244:245],v2 offset:8960
ds_read_b64_tr_b16 a[246:247],v2 offset:13056
v_mfma_f32_16x16x32_bf16 a[8:11],a[212:215],v[148:151],a[8:11]
s_waitcnt lgkmcnt(14)
ds_read_b64_tr_b16 a[252:253],v3 offset:8960
ds_read_b64_tr_b16 a[254:255],v3 offset:13056
v_mfma_f32_16x16x32_bf16 a[12:15],a[220:223],v[148:151],a[12:15]
s_waitcnt lgkmcnt(14)
ds_read_b64_tr_b16 a[248:249],v3 offset:768
ds_read_b64_tr_b16 a[250:251],v3 offset:4864
v_mfma_f32_16x16x32_bf16 a[12:15],a[216:219],v[132:135],a[12:15]
s_waitcnt lgkmcnt(14)
v_mfma_f32_16x16x32_bf16 a[16:19],a[224:227],v[132:135],a[16:19]
s_waitcnt lgkmcnt(12)
v_mfma_f32_16x16x32_bf16 a[16:19],a[228:231],v[148:151],a[16:19]
s_waitcnt lgkmcnt(10)
v_mfma_f32_16x16x32_bf16 a[20:23],a[236:239],v[148:151],a[20:23]
s_waitcnt lgkmcnt(8)
v_mfma_f32_16x16x32_bf16 a[20:23],a[232:235],v[132:135],a[20:23]
s_waitcnt lgkmcnt(6)
v_mfma_f32_16x16x32_bf16 a[24:27],a[240:243],v[132:135],a[24:27]
s_waitcnt lgkmcnt(4)
v_mul_f32 v123,v23,v123
v_mul_f32 v126,v23,v126
v_mfma_f32_16x16x32_bf16 a[24:27],a[244:247],v[148:151],a[24:27]
s_waitcnt lgkmcnt(2)
v_mul_f32 v127,v23,v127
v_mul_f32 v16,v23,v16
v_mfma_f32_16x16x32_bf16 a[28:31],a[252:255],v[148:151],a[28:31]
s_waitcnt lgkmcnt(0)
v_exp_f32 v164,v164
v_exp_f32 v165,v165
v_exp_f32 v180,v180
v_exp_f32 v181,v181
v_mfma_f32_16x16x32_bf16 a[28:31],a[248:251],v[132:135],a[28:31]
s_cmp_eq_u32 s50,131072
s_cselect_b32 s62,163840,0
v_mul_f32 v17,v23,v17
v_cvt_pk_bf16_f32 v118,v118,v119
v_mul_f32 v12,v23,v12
v_mul_f32 v13,v23,v13
v_cvt_pk_bf16_f32 v122,v122,v123
v_mul_f32 v14,v23,v14
v_mul_f32 v15,v23,v15
v_cvt_pk_bf16_f32 v126,v126,v127
v_exp_f32 v166,v166
v_exp_f32 v167,v167
v_exp_f32 v182,v182
v_exp_f32 v183,v183
v_cvt_pk_bf16_f32 v119,v16,v17
v_cvt_pk_bf16_f32 v123,v12,v13
v_cvt_pk_bf16_f32 v127,v14,v15
v_cvt_pk_bf16_f32 v164,v164,v165
v_exp_f32 v172,v172
v_exp_f32 v173,v173
v_exp_f32 v174,v174
v_exp_f32 v175,v175
v_cvt_pk_bf16_f32 v180,v180,v181
v_exp_f32 v188,v188
v_exp_f32 v189,v189
v_exp_f32 v190,v190
v_exp_f32 v191,v191
v_mfma_f32_16x16x32_bf16 v[48:51],v[28:31],v[132:135],v[48:51]
s_sub_u32 s48,32768,s62
s_cmp_lg_u64 s[56:57],0
s_cbranch_scc1 L_slow0_%=
L_sret0_%=:
s_add_u32 s50,s50,s48
v_cvt_pk_bf16_f32 v165,v166,v167
v_cvt_pk_bf16_f32 v181,v182,v183
v_add_u32 v1,s48,v1
v_cvt_pk_bf16_f32 v166,v172,v173
v_cvt_pk_bf16_f32 v167,v174,v175
v_cvt_pk_bf16_f32 v182,v188,v189
v_cvt_pk_bf16_f32 v183,v190,v191
v_mfma_f32_16x16x32_bf16 v[48:51],v[28:31],v[148:151],v[48:51]
s_mov_b32 s53,10
L_att1_%=:
s_waitcnt vmcnt(24) lgkmcnt(0)
s_barrier
v_mfma_f32_16x16x32_bf16 v[228:231],a[128:131],v[152:155],v[44:47]
s_add_u32 m0,s49,s51
ds_read_b128 a[128:131],v1
v_add_u32 v2,s48,v2
v_mfma_f32_16x16x32_bf16 v[228:231],a[132:135],v[184:187],v[228:231]
s_add_u32 s54,s54,16384
v_exp_f32 v196,v196
v_mfma_f32_16x16x32_bf16 v[228:231],a[136:139],v[216:219],v[228:231]
s_and_b32 s46,s54,0xfffc0000
buffer_load_dwordx4 v4,s[20:23],s47 offen nt lds
ds_read_b128 a[132:135],v1 offset:256
v_mfma_f32_16x16x32_bf16 v[228:231],a[140:143],v[248:251],v[228:231]
v_exp_f32 v197,v197
v_mfma_f32_16x16x32_bf16 v[236:239],a[156:159],v[248:251],v[44:47]
ds_read_b128 a[136:139],v1 offset:512
v_cvt_pk_bf16_f32 v196,v196,v197
v_mfma_f32_16x16x32_bf16 v[236:239],a[152:155],v[216:219],v[236:239]
v_exp_f32 v198,v198
v_mfma_f32_16x16x32_bf16 v[236:239],a[148:151],v[184:187],v[236:239]
ds_read_b128 a[140:143],v1 offset:768
v_add_u32 v3,s48,v3
v_mfma_f32_16x16x32_bf16 v[236:239],a[144:147],v[152:155],v[236:239]
v_exp_f32 v199,v199
v_mfma_f32_16x16x32_bf16 v[244:247],a[160:163],v[152:155],v[44:47]
ds_read_b128 a[156:159],v1 offset:4864
v_cvt_pk_bf16_f32 v197,v198,v199
v_mfma_f32_16x16x32_bf16 v[244:247],a[164:167],v[184:187],v[244:247]
v_exp_f32 v204,v204
v_mfma_f32_16x16x32_bf16 v[244:247],a[168:171],v[216:219],v[244:247]
s_cmp_eq_u32 s50,131072
s_cselect_b32 s62,163840,0
ds_read_b128 a[152:155],v1 offset:4608
v_mfma_f32_16x16x32_bf16 v[244:247],a[172:175],v[248:251],v[244:247]
s_sub_u32 s48,32768,s62
v_exp_f32 v205,v205
v_mfma_f32_16x16x32_bf16 v[252:255],a[188:191],v[248:251],v[44:47]
ds_read_b128 a[148:151],v1 offset:4352
v_cvt_pk_bf16_f32 v198,v204,v205
v_mfma_f32_16x16x32_bf16 v[252:255],a[184:187],v[216:219],v[252:255]
v_exp_f32 v206,v206
v_mfma_f32_16x16x32_bf16 v[252:255],a[180:183],v[184:187],v[252:255]
ds_read_b128 a[144:147],v1 offset:4096
v_mfma_f32_16x16x32_bf16 v[252:255],a[176:179],v[152:155],v[252:255]
v_exp_f32 v207,v207
v_mfma_f32_16x16x32_bf16 a[32:35],a[192:195],v[164:167],a[32:35]
buffer_load_dwordx4 v4,s[20:23],s47 offen offset:1024 nt lds
ds_read_b128 a[160:163],v1 offset:8192
v_mfma_f32_16x16x32_bf16 a[32:35],a[196:199],v[180:183],a[32:35]
v_exp_f32 v212,v212
v_mfma_f32_16x16x32_bf16 a[36:39],a[204:207],v[180:183],a[36:39]
ds_read_b128 a[164:167],v1 offset:8448
v_cvt_pk_bf16_f32 v199,v206,v207
v_mfma_f32_16x16x32_bf16 a[36:39],a[200:203],v[164:167],a[36:39]
v_exp_f32 v213,v213
v_mfma_f32_16x16x32_bf16 a[40:43],a[208:211],v[164:167],a[40:43]
ds_read_b128 a[168:171],v1 offset:8704
v_cvt_pk_bf16_f32 v212,v212,v213
v_mfma_f32_16x16x32_bf16 a[40:43],a[212:215],v[180:183],a[40:43]
v_exp_f32 v214,v214
v_mfma_f32_16x16x32_bf16 a[44:47],a[220:223],v[180:183],a[44:47]
ds_read_b128 a[172:175],v1 offset:8960
v_mfma_f32_16x16x32_bf16 a[44:47],a[216:219],v[164:167],a[44:47]
v_exp_f32 v215,v215
v_mfma_f32_16x16x32_bf16 a[48:51],a[224:227],v[164:167],a[48:51]
ds_read_b128 a[188:191],v1 offset:13056
v_cvt_pk_bf16_f32 v213,v214,v215
v_mfma_f32_16x16x32_bf16 a[48:51],a[228:231],v[180:183],a[48:51]
v_exp_f32 v220,v220
v_mfma_f32_16x16x32_bf16 a[52:55],a[236:239],v[180:183],a[52:55]
ds_read_b128 a[184:187],v1 offset:12800
v_mfma_f32_16x16x32_bf16 a[52:55],a[232:235],v[164:167],a[52:55]
v_exp_f32 v221,v221
v_mfma_f32_16x16x32_bf16 a[56:59],a[240:243],v[164:167],a[56:59]
ds_read_b128 a[180:183],v1 offset:12544
v_cvt_pk_bf16_f32 v214,v220,v221
v_mfma_f32_16x16x32_bf16 a[56:59],a[244:247],v[180:183],a[56:59]
v_exp_f32 v222,v222
v_mfma_f32_16x16x32_bf16 a[60:63],a[252:255],v[180:183],a[60:63]
buffer_load_dwordx4 v5,s[20:23],s47 offen offset:2048 nt lds
ds_read_b128 a[176:179],v1 offset:12288
v_mfma_f32_16x16x32_bf16 a[60:63],a[248:251],v[164:167],a[60:63]
v_exp_f32 v223,v223
v_mfma_f32_16x16x32_bf16 v[52:55],v[28:31],v[164:167],v[52:55]
v_cvt_pk_bf16_f32 v215,v222,v223
v_add_u32 v1,s48,v1
v_mfma_f32_16x16x32_bf16 v[52:55],v[28:31],v[180:183],v[52:55]
s_waitcnt lgkmcnt(3)
v_exp_f32 v228,v228
v_mfma_f32_16x16x32_bf16 v[132:135],a[128:131],v[128:131],v[32:35]
v_exp_f32 v229,v229
v_mfma_f32_16x16x32_bf16 v[132:135],a[132:135],v[160:163],v[132:135]
v_exp_f32 v230,v230
v_mfma_f32_16x16x32_bf16 v[132:135],a[136:139],v[192:195],v[132:135]
v_exp_f32 v231,v231
v_mfma_f32_16x16x32_bf16 v[132:135],a[140:143],v[224:227],v[132:135]
v_cvt_pk_bf16_f32 v228,v228,v229
v_cvt_pk_bf16_f32 v229,v230,v231
v_mfma_f32_16x16x32_bf16 v[140:143],a[156:159],v[224:227],v[32:35]
v_exp_f32 v236,v236
v_mfma_f32_16x16x32_bf16 v[140:143],a[152:155],v[192:195],v[140:143]
v_exp_f32 v237,v237
v_mfma_f32_16x16x32_bf16 v[140:143],a[148:151],v[160:163],v[140:143]
v_exp_f32 v238,v238
v_mfma_f32_16x16x32_bf16 v[140:143],a[144:147],v[128:131],v[140:143]
v_exp_f32 v239,v239
v_mfma_f32_16x16x32_bf16 v[148:151],a[160:163],v[128:131],v[32:35]
v_exp_f32 v244,v244
v_mfma_f32_16x16x32_bf16 v[148:151],a[164:167],v[160:163],v[148:151]
v_exp_f32 v245,v245
v_mfma_f32_16x16x32_bf16 v[148:151],a[168:171],v[192:195],v[148:151]
)A"
R"A(buffer_load_dwordx4 v5,s[20:23],s47 offen offset:3072 nt lds
v_cvt_pk_bf16_f32 v230,v236,v237
v_mfma_f32_16x16x32_bf16 v[148:151],a[172:175],v[224:227],v[148:151]
v_exp_f32 v246,v246
v_mfma_f32_16x16x32_bf16 v[156:159],a[188:191],v[224:227],v[32:35]
s_waitcnt lgkmcnt(0)
v_exp_f32 v247,v247
v_mfma_f32_16x16x32_bf16 v[156:159],a[184:187],v[192:195],v[156:159]
v_cvt_pk_bf16_f32 v231,v238,v239
v_cvt_pk_bf16_f32 v244,v244,v245
v_mfma_f32_16x16x32_bf16 v[156:159],a[180:183],v[160:163],v[156:159]
v_exp_f32 v252,v252
v_mfma_f32_16x16x32_bf16 v[156:159],a[176:179],v[128:131],v[156:159]
v_exp_f32 v253,v253
v_mfma_f32_16x16x32_bf16 a[64:67],a[192:195],v[196:199],a[64:67]
v_exp_f32 v254,v254
v_mfma_f32_16x16x32_bf16 a[64:67],a[196:199],v[212:215],a[64:67]
v_exp_f32 v255,v255
v_mfma_f32_16x16x32_bf16 a[68:71],a[204:207],v[212:215],a[68:71]
v_cvt_pk_bf16_f32 v245,v246,v247
v_cvt_pk_bf16_f32 v246,v252,v253
v_mfma_f32_16x16x32_bf16 a[68:71],a[200:203],v[196:199],a[68:71]
v_cvt_pk_bf16_f32 v247,v254,v255
v_mfma_f32_16x16x32_bf16 a[72:75],a[208:211],v[196:199],a[72:75]
v_exp_f32 v132,v132
v_mfma_f32_16x16x32_bf16 a[72:75],a[212:215],v[212:215],a[72:75]
s_add_u32 m0,s49,s52
v_exp_f32 v133,v133
v_mfma_f32_16x16x32_bf16 a[76:79],a[220:223],v[212:215],a[76:79]
s_mov_b32 s49,s50
v_exp_f32 v134,v134
v_mfma_f32_16x16x32_bf16 a[76:79],a[216:219],v[196:199],a[76:79]
s_add_u32 s50,s50,s48
v_exp_f32 v135,v135
v_mfma_f32_16x16x32_bf16 a[80:83],a[224:227],v[196:199],a[80:83]
buffer_load_dwordx4 v4,s[24:27],s47 offen nt lds
v_cvt_pk_bf16_f32 v132,v132,v133
v_mfma_f32_16x16x32_bf16 a[80:83],a[228:231],v[212:215],a[80:83]
v_exp_f32 v140,v140
v_mfma_f32_16x16x32_bf16 a[84:87],a[236:239],v[212:215],a[84:87]
v_exp_f32 v141,v141
v_mfma_f32_16x16x32_bf16 a[84:87],a[232:235],v[196:199],a[84:87]
v_exp_f32 v142,v142
v_mfma_f32_16x16x32_bf16 a[88:91],a[240:243],v[196:199],a[88:91]
v_exp_f32 v143,v143
v_mfma_f32_16x16x32_bf16 a[88:91],a[244:247],v[212:215],a[88:91]
v_exp_f32 v148,v148
v_mfma_f32_16x16x32_bf16 a[92:95],a[252:255],v[212:215],a[92:95]
v_exp_f32 v149,v149
v_mfma_f32_16x16x32_bf16 a[92:95],a[248:251],v[196:199],a[92:95]
v_cvt_pk_bf16_f32 v133,v134,v135
v_cvt_pk_bf16_f32 v134,v140,v141
v_mfma_f32_16x16x32_bf16 v[56:59],v[28:31],v[196:199],v[56:59]
v_exp_f32 v150,v150
v_mfma_f32_16x16x32_bf16 v[56:59],v[28:31],v[212:215],v[56:59]
v_exp_f32 v151,v151
v_mfma_f32_16x16x32_bf16 v[164:167],a[128:131],v[136:139],v[36:39]
v_cvt_pk_bf16_f32 v135,v142,v143
v_cvt_pk_bf16_f32 v148,v148,v149
v_mfma_f32_16x16x32_bf16 v[164:167],a[132:135],v[168:171],v[164:167]
v_exp_f32 v156,v156
v_mfma_f32_16x16x32_bf16 v[164:167],a[136:139],v[200:203],v[164:167]
v_exp_f32 v157,v157
v_mfma_f32_16x16x32_bf16 v[164:167],a[140:143],v[232:235],v[164:167]
v_exp_f32 v158,v158
v_mfma_f32_16x16x32_bf16 v[172:175],a[156:159],v[232:235],v[36:39]
buffer_load_dwordx4 v5,s[24:27],s47 offen offset:1024 nt lds
v_cvt_pk_bf16_f32 v149,v150,v151
v_mfma_f32_16x16x32_bf16 v[172:175],a[152:155],v[200:203],v[172:175]
v_exp_f32 v159,v159
v_mfma_f32_16x16x32_bf16 v[172:175],a[148:151],v[168:171],v[172:175]
v_cvt_pk_bf16_f32 v150,v156,v157
v_cvt_pk_bf16_f32 v151,v158,v159
v_mfma_f32_16x16x32_bf16 v[172:175],a[144:147],v[136:139],v[172:175]
v_mfma_f32_16x16x32_bf16 v[180:183],a[160:163],v[136:139],v[36:39]
v_mfma_f32_16x16x32_bf16 v[180:183],a[164:167],v[168:171],v[180:183]
v_exp_f32 v164,v164
v_mfma_f32_16x16x32_bf16 v[180:183],a[168:171],v[200:203],v[180:183]
v_exp_f32 v165,v165
v_mfma_f32_16x16x32_bf16 v[180:183],a[172:175],v[232:235],v[180:183]
v_exp_f32 v166,v166
v_mfma_f32_16x16x32_bf16 v[188:191],a[188:191],v[232:235],v[36:39]
v_exp_f32 v167,v167
v_mfma_f32_16x16x32_bf16 v[188:191],a[184:187],v[200:203],v[188:191]
v_cvt_pk_bf16_f32 v164,v164,v165
v_cvt_pk_bf16_f32 v165,v166,v167
v_mfma_f32_16x16x32_bf16 v[188:191],a[180:183],v[168:171],v[188:191]
v_exp_f32 v172,v172
v_mfma_f32_16x16x32_bf16 v[188:191],a[176:179],v[136:139],v[188:191]
v_exp_f32 v173,v173
v_mfma_f32_16x16x32_bf16 a[96:99],a[192:195],v[228:231],a[96:99]
ds_read_b64_tr_b16 a[192:193],v2
ds_read_b64_tr_b16 a[194:195],v2 offset:4096
v_mfma_f32_16x16x32_bf16 a[96:99],a[196:199],v[244:247],a[96:99]
ds_read_b64_tr_b16 a[196:197],v2 offset:8192
v_cvt_pk_bf16_f32 v166,v172,v173
v_mfma_f32_16x16x32_bf16 a[100:103],a[204:207],v[244:247],a[100:103]
buffer_load_dwordx4 v4,s[24:27],s47 offen offset:2048 nt lds
ds_read_b64_tr_b16 a[198:199],v2 offset:12288
v_mfma_f32_16x16x32_bf16 a[100:103],a[200:203],v[228:231],a[100:103]
ds_read_b64_tr_b16 a[204:205],v3 offset:8192
v_mfma_f32_16x16x32_bf16 a[104:107],a[208:211],v[228:231],a[104:107]
ds_read_b64_tr_b16 a[206:207],v3 offset:12288
ds_read_b64_tr_b16 a[200:201],v3
v_mfma_f32_16x16x32_bf16 a[104:107],a[212:215],v[244:247],a[104:107]
ds_read_b64_tr_b16 a[202:203],v3 offset:4096
v_mfma_f32_16x16x32_bf16 a[108:111],a[220:223],v[244:247],a[108:111]
ds_read_b64_tr_b16 a[208:209],v2 offset:256
v_mfma_f32_16x16x32_bf16 a[108:111],a[216:219],v[228:231],a[108:111]
ds_read_b64_tr_b16 a[210:211],v2 offset:4352
v_mfma_f32_16x16x32_bf16 a[112:115],a[224:227],v[228:231],a[112:115]
ds_read_b64_tr_b16 a[212:213],v2 offset:8448
ds_read_b64_tr_b16 a[214:215],v2 offset:12544
v_mfma_f32_16x16x32_bf16 a[112:115],a[228:231],v[244:247],a[112:115]
ds_read_b64_tr_b16 a[220:221],v3 offset:8448
v_mfma_f32_16x16x32_bf16 a[116:119],a[236:239],v[244:247],a[116:119]
ds_read_b64_tr_b16 a[222:223],v3 offset:12544
v_mfma_f32_16x16x32_bf16 a[116:119],a[232:235],v[228:231],a[116:119]
ds_read_b64_tr_b16 a[216:217],v3 offset:256
v_mfma_f32_16x16x32_bf16 a[120:123],a[240:243],v[228:231],a[120:123]
ds_read_b64_tr_b16 a[218:219],v3 offset:4352
ds_read_b64_tr_b16 a[224:225],v2 offset:512
v_mfma_f32_16x16x32_bf16 a[120:123],a[244:247],v[244:247],a[120:123]
ds_read_b64_tr_b16 a[226:227],v2 offset:4608
v_mfma_f32_16x16x32_bf16 a[124:127],a[252:255],v[244:247],a[124:127]
ds_read_b64_tr_b16 a[228:229],v2 offset:8704
v_mfma_f32_16x16x32_bf16 a[124:127],a[248:251],v[228:231],a[124:127]
ds_read_b64_tr_b16 a[230:231],v2 offset:12800
v_mfma_f32_16x16x32_bf16 v[60:63],v[28:31],v[228:231],v[60:63]
buffer_load_dwordx4 v5,s[24:27],s47 offen offset:3072 nt lds
ds_read_b64_tr_b16 a[236:237],v3 offset:8704
v_mfma_f32_16x16x32_bf16 v[60:63],v[28:31],v[244:247],v[60:63]
s_add_u32 s47,s47,16384
ds_read_b64_tr_b16 a[238:239],v3 offset:12800
ds_read_b64_tr_b16 a[232:233],v3 offset:512
v_mfma_f32_16x16x32_bf16 v[196:199],a[128:131],v[144:147],v[40:43]
s_and_b32 s47,s47,0x3ffff
s_or_b32 s47,s47,s46
ds_read_b64_tr_b16 a[234:235],v3 offset:4608
v_mfma_f32_16x16x32_bf16 v[196:199],a[132:135],v[176:179],v[196:199]
ds_read_b64_tr_b16 a[240:241],v2 offset:768
v_mfma_f32_16x16x32_bf16 v[196:199],a[136:139],v[208:211],v[196:199]
ds_read_b64_tr_b16 a[242:243],v2 offset:4864
v_mfma_f32_16x16x32_bf16 v[196:199],a[140:143],v[240:243],v[196:199]
ds_read_b64_tr_b16 a[244:245],v2 offset:8960
ds_read_b64_tr_b16 a[246:247],v2 offset:13056
v_mfma_f32_16x16x32_bf16 v[204:207],a[156:159],v[240:243],v[40:43]
ds_read_b64_tr_b16 a[252:253],v3 offset:8960
v_mfma_f32_16x16x32_bf16 v[204:207],a[152:155],v[208:211],v[204:207]
ds_read_b64_tr_b16 a[254:255],v3 offset:13056
v_mfma_f32_16x16x32_bf16 v[204:207],a[148:151],v[176:179],v[204:207]
ds_read_b64_tr_b16 a[248:249],v3 offset:768
v_mfma_f32_16x16x32_bf16 v[204:207],a[144:147],v[144:147],v[204:207]
ds_read_b64_tr_b16 a[250:251],v3 offset:4864
v_mfma_f32_16x16x32_bf16 v[212:215],a[160:163],v[144:147],v[40:43]
v_exp_f32 v180,v180
v_mfma_f32_16x16x32_bf16 v[212:215],a[164:167],v[176:179],v[212:215]
v_exp_f32 v181,v181
v_mfma_f32_16x16x32_bf16 v[212:215],a[168:171],v[208:211],v[212:215]
v_exp_f32 v182,v182
v_mfma_f32_16x16x32_bf16 v[212:215],a[172:175],v[240:243],v[212:215]
v_exp_f32 v183,v183
v_mfma_f32_16x16x32_bf16 v[220:223],a[188:191],v[240:243],v[40:43]
v_exp_f32 v174,v174
v_mfma_f32_16x16x32_bf16 v[220:223],a[184:187],v[208:211],v[220:223]
v_exp_f32 v175,v175
v_mfma_f32_16x16x32_bf16 v[220:223],a[180:183],v[176:179],v[220:223]
v_cvt_pk_bf16_f32 v180,v180,v181
v_cvt_pk_bf16_f32 v181,v182,v183
v_mfma_f32_16x16x32_bf16 v[220:223],a[176:179],v[144:147],v[220:223]
s_waitcnt lgkmcnt(0)
v_exp_f32 v188,v188
v_mfma_f32_16x16x32_bf16 a[0:3],a[192:195],v[132:135],a[0:3]
v_exp_f32 v189,v189
v_mfma_f32_16x16x32_bf16 a[0:3],a[196:199],v[148:151],a[0:3]
v_exp_f32 v190,v190
v_mfma_f32_16x16x32_bf16 a[4:7],a[204:207],v[148:151],a[4:7]
v_exp_f32 v191,v191
v_mfma_f32_16x16x32_bf16 a[4:7],a[200:203],v[132:135],a[4:7]
v_cvt_pk_bf16_f32 v167,v174,v175
v_cvt_pk_bf16_f32 v182,v188,v189
v_mfma_f32_16x16x32_bf16 a[8:11],a[208:211],v[132:135],a[8:11]
v_cvt_pk_bf16_f32 v183,v190,v191
v_mfma_f32_16x16x32_bf16 a[8:11],a[212:215],v[148:151],a[8:11]
v_mfma_f32_16x16x32_bf16 a[12:15],a[220:223],v[148:151],a[12:15]
v_mfma_f32_16x16x32_bf16 a[12:15],a[216:219],v[132:135],a[12:15]
v_mfma_f32_16x16x32_bf16 a[16:19],a[224:227],v[132:135],a[16:19]
v_mfma_f32_16x16x32_bf16 a[16:19],a[228:231],v[148:151],a[16:19]
v_mfma_f32_16x16x32_bf16 a[20:23],a[236:239],v[148:151],a[20:23]
v_mfma_f32_16x16x32_bf16 a[20:23],a[232:235],v[132:135],a[20:23]
v_mfma_f32_16x16x32_bf16 a[24:27],a[240:243],v[132:135],a[24:27]
v_mfma_f32_16x16x32_bf16 a[24:27],a[244:247],v[148:151],a[24:27]
v_mfma_f32_16x16x32_bf16 a[28:31],a[252:255],v[148:151],a[28:31]
v_mfma_f32_16x16x32_bf16 a[28:31],a[248:251],v[132:135],a[28:31]
v_mfma_f32_16x16x32_bf16 v[48:51],v[28:31],v[132:135],v[48:51]
v_mfma_f32_16x16x32_bf16 v[48:51],v[28:31],v[148:151],v[48:51]
s_sleep 2
s_sub_u32 s53,s53,1
s_cmp_lg_u32 s53,0
s_cbranch_scc1 L_att1_%=
s_waitcnt vmcnt(24)
s_barrier
v_mfma_f32_16x16x32_bf16 v[228:231],a[128:131],v[152:155],v[44:47]
s_mov_b32 s49,s50
ds_read_b128 a[128:131],v1
v_add_u32 v2,s48,v2
v_mfma_f32_16x16x32_bf16 v[228:231],a[132:135],v[184:187],v[228:231]
s_lshl_b32 s43,s41,2
v_exp_f32 v196,v196
v_mfma_f32_16x16x32_bf16 v[228:231],a[136:139],v[216:219],v[228:231]
s_add_u32 s43,s43,s70
ds_read_b128 a[132:135],v1 offset:256
v_add_u32 v3,s48,v3
v_mfma_f32_16x16x32_bf16 v[228:231],a[140:143],v[248:251],v[228:231]
s_lshl_b32 s43,s43,19
v_exp_f32 v197,v197
v_mfma_f32_16x16x32_bf16 v[236:239],a[156:159],v[248:251],v[44:47]
s_add_u32 s43,s43,s55
buffer_store_dwordx4 v[64:67],v6,s[36:39],s84 offen sc1
ds_read_b128 a[136:139],v1 offset:512
v_mfma_f32_16x16x32_bf16 v[236:239],a[152:155],v[216:219],v[236:239]
s_mov_b64 s[28:29],%4
v_exp_f32 v198,v198
v_mfma_f32_16x16x32_bf16 v[236:239],a[148:151],v[184:187],v[236:239]
s_mov_b32 s30,475136
ds_read_b128 a[140:143],v1 offset:768
v_cvt_pk_bf16_f32 v196,v196,v197
v_mfma_f32_16x16x32_bf16 v[236:239],a[144:147],v[152:155],v[236:239]
s_mov_b32 s31,0x20000
v_exp_f32 v199,v199
v_mfma_f32_16x16x32_bf16 v[244:247],a[160:163],v[152:155],v[44:47]
ds_read_b128 a[156:159],v1 offset:4864
v_cvt_pk_bf16_f32 v197,v198,v199
v_mfma_f32_16x16x32_bf16 v[244:247],a[164:167],v[184:187],v[244:247]
v_exp_f32 v204,v204
v_mfma_f32_16x16x32_bf16 v[244:247],a[168:171],v[216:219],v[244:247]
s_cmp_eq_u32 s50,131072
s_cselect_b32 s62,163840,0
ds_read_b128 a[152:155],v1 offset:4608
v_mfma_f32_16x16x32_bf16 v[244:247],a[172:175],v[248:251],v[244:247]
s_sub_u32 s48,32768,s62
v_exp_f32 v205,v205
v_mfma_f32_16x16x32_bf16 v[252:255],a[188:191],v[248:251],v[44:47]
s_add_u32 s50,s50,s48
ds_read_b128 a[148:151],v1 offset:4352
v_cvt_pk_bf16_f32 v198,v204,v205
v_mfma_f32_16x16x32_bf16 v[252:255],a[184:187],v[216:219],v[252:255]
s_mov_b32 s49,s50
v_exp_f32 v206,v206
v_mfma_f32_16x16x32_bf16 v[252:255],a[180:183],v[184:187],v[252:255]
s_cmp_eq_u32 s50,131072
s_cselect_b32 s62,163840,0
ds_read_b128 a[144:147],v1 offset:4096
v_mfma_f32_16x16x32_bf16 v[252:255],a[176:179],v[152:155],v[252:255]
v_exp_f32 v207,v207
v_mfma_f32_16x16x32_bf16 a[32:35],a[192:195],v[164:167],a[32:35]
ds_read_b128 a[160:163],v1 offset:8192
v_cvt_pk_bf16_f32 v199,v206,v207
v_mfma_f32_16x16x32_bf16 a[32:35],a[196:199],v[180:183],a[32:35]
v_exp_f32 v212,v212
v_mfma_f32_16x16x32_bf16 a[36:39],a[204:207],v[180:183],a[36:39]
buffer_store_dwordx4 v[68:71],v6,s[36:39],s85 offen sc1
ds_read_b128 a[164:167],v1 offset:8448
)A"
R"A(v_mfma_f32_16x16x32_bf16 a[36:39],a[200:203],v[164:167],a[36:39]
v_exp_f32 v213,v213
v_mfma_f32_16x16x32_bf16 a[40:43],a[208:211],v[164:167],a[40:43]
ds_read_b128 a[168:171],v1 offset:8704
v_cvt_pk_bf16_f32 v212,v212,v213
v_mfma_f32_16x16x32_bf16 a[40:43],a[212:215],v[180:183],a[40:43]
v_exp_f32 v214,v214
v_mfma_f32_16x16x32_bf16 a[44:47],a[220:223],v[180:183],a[44:47]
s_add_u32 s28,s28,s43
s_addc_u32 s29,s29,0
ds_read_b128 a[172:175],v1 offset:8960
v_mfma_f32_16x16x32_bf16 a[44:47],a[216:219],v[164:167],a[44:47]
s_and_b32 s29,s29,0xffff
v_exp_f32 v215,v215
v_mfma_f32_16x16x32_bf16 a[48:51],a[224:227],v[164:167],a[48:51]
ds_read_b128 a[188:191],v1 offset:13056
v_cvt_pk_bf16_f32 v213,v214,v215
v_mfma_f32_16x16x32_bf16 a[48:51],a[228:231],v[180:183],a[48:51]
v_exp_f32 v220,v220
v_mfma_f32_16x16x32_bf16 a[52:55],a[236:239],v[180:183],a[52:55]
ds_read_b128 a[184:187],v1 offset:12800
v_mfma_f32_16x16x32_bf16 a[52:55],a[232:235],v[164:167],a[52:55]
v_exp_f32 v221,v221
v_mfma_f32_16x16x32_bf16 a[56:59],a[240:243],v[164:167],a[56:59]
ds_read_b128 a[180:183],v1 offset:12544
v_cvt_pk_bf16_f32 v214,v220,v221
v_mfma_f32_16x16x32_bf16 a[56:59],a[244:247],v[180:183],a[56:59]
v_exp_f32 v222,v222
v_mfma_f32_16x16x32_bf16 a[60:63],a[252:255],v[180:183],a[60:63]
ds_read_b128 a[176:179],v1 offset:12288
v_add_u32 v1,s48,v1
v_mfma_f32_16x16x32_bf16 a[60:63],a[248:251],v[164:167],a[60:63]
v_exp_f32 v223,v223
v_mfma_f32_16x16x32_bf16 v[52:55],v[28:31],v[164:167],v[52:55]
buffer_store_dwordx4 v[72:75],v6,s[36:39],s86 offen sc1
v_cvt_pk_bf16_f32 v215,v222,v223
v_mfma_f32_16x16x32_bf16 v[52:55],v[28:31],v[180:183],v[52:55]
s_waitcnt lgkmcnt(3)
v_exp_f32 v228,v228
v_mfma_f32_16x16x32_bf16 v[132:135],a[128:131],v[128:131],v[32:35]
v_exp_f32 v229,v229
v_mfma_f32_16x16x32_bf16 v[132:135],a[132:135],v[160:163],v[132:135]
v_exp_f32 v230,v230
v_mfma_f32_16x16x32_bf16 v[132:135],a[136:139],v[192:195],v[132:135]
v_exp_f32 v231,v231
v_mfma_f32_16x16x32_bf16 v[132:135],a[140:143],v[224:227],v[132:135]
v_cvt_pk_bf16_f32 v228,v228,v229
v_cvt_pk_bf16_f32 v229,v230,v231
v_mfma_f32_16x16x32_bf16 v[140:143],a[156:159],v[224:227],v[32:35]
v_exp_f32 v236,v236
v_mfma_f32_16x16x32_bf16 v[140:143],a[152:155],v[192:195],v[140:143]
v_exp_f32 v237,v237
v_mfma_f32_16x16x32_bf16 v[140:143],a[148:151],v[160:163],v[140:143]
v_exp_f32 v238,v238
v_mfma_f32_16x16x32_bf16 v[140:143],a[144:147],v[128:131],v[140:143]
v_exp_f32 v239,v239
v_mfma_f32_16x16x32_bf16 v[148:151],a[160:163],v[128:131],v[32:35]
v_exp_f32 v244,v244
v_mfma_f32_16x16x32_bf16 v[148:151],a[164:167],v[160:163],v[148:151]
v_exp_f32 v245,v245
v_mfma_f32_16x16x32_bf16 v[148:151],a[168:171],v[192:195],v[148:151]
v_exp_f32 v246,v246
v_mfma_f32_16x16x32_bf16 v[148:151],a[172:175],v[224:227],v[148:151]
v_exp_f32 v247,v247
v_mfma_f32_16x16x32_bf16 v[156:159],a[188:191],v[224:227],v[32:35]
s_waitcnt lgkmcnt(0)
buffer_store_dwordx4 v[76:79],v6,s[36:39],s87 offen sc1
v_cvt_pk_bf16_f32 v230,v236,v237
v_mfma_f32_16x16x32_bf16 v[156:159],a[184:187],v[192:195],v[156:159]
v_cvt_pk_bf16_f32 v231,v238,v239
v_cvt_pk_bf16_f32 v244,v244,v245
v_mfma_f32_16x16x32_bf16 v[156:159],a[180:183],v[160:163],v[156:159]
v_exp_f32 v252,v252
v_mfma_f32_16x16x32_bf16 v[156:159],a[176:179],v[128:131],v[156:159]
v_exp_f32 v253,v253
v_mfma_f32_16x16x32_bf16 a[64:67],a[192:195],v[196:199],a[64:67]
v_exp_f32 v254,v254
v_mfma_f32_16x16x32_bf16 a[64:67],a[196:199],v[212:215],a[64:67]
v_exp_f32 v255,v255
v_mfma_f32_16x16x32_bf16 a[68:71],a[204:207],v[212:215],a[68:71]
v_cvt_pk_bf16_f32 v245,v246,v247
v_cvt_pk_bf16_f32 v246,v252,v253
v_mfma_f32_16x16x32_bf16 a[68:71],a[200:203],v[196:199],a[68:71]
v_cvt_pk_bf16_f32 v247,v254,v255
v_mfma_f32_16x16x32_bf16 a[72:75],a[208:211],v[196:199],a[72:75]
v_exp_f32 v132,v132
v_mfma_f32_16x16x32_bf16 a[72:75],a[212:215],v[212:215],a[72:75]
v_exp_f32 v133,v133
v_mfma_f32_16x16x32_bf16 a[76:79],a[220:223],v[212:215],a[76:79]
v_exp_f32 v134,v134
v_mfma_f32_16x16x32_bf16 a[76:79],a[216:219],v[196:199],a[76:79]
v_exp_f32 v135,v135
v_mfma_f32_16x16x32_bf16 a[80:83],a[224:227],v[196:199],a[80:83]
v_cvt_pk_bf16_f32 v132,v132,v133
v_cvt_pk_bf16_f32 v133,v134,v135
v_mfma_f32_16x16x32_bf16 a[80:83],a[228:231],v[212:215],a[80:83]
v_exp_f32 v140,v140
v_mfma_f32_16x16x32_bf16 a[84:87],a[236:239],v[212:215],a[84:87]
buffer_store_dwordx4 v[80:83],v6,s[36:39],s84 offen offset:1024 sc1
v_mfma_f32_16x16x32_bf16 a[84:87],a[232:235],v[196:199],a[84:87]
v_exp_f32 v141,v141
v_mfma_f32_16x16x32_bf16 a[88:91],a[240:243],v[196:199],a[88:91]
v_exp_f32 v142,v142
v_mfma_f32_16x16x32_bf16 a[88:91],a[244:247],v[212:215],a[88:91]
v_exp_f32 v143,v143
v_mfma_f32_16x16x32_bf16 a[92:95],a[252:255],v[212:215],a[92:95]
v_exp_f32 v148,v148
v_mfma_f32_16x16x32_bf16 a[92:95],a[248:251],v[196:199],a[92:95]
v_exp_f32 v149,v149
v_mfma_f32_16x16x32_bf16 v[56:59],v[28:31],v[196:199],v[56:59]
v_exp_f32 v150,v150
v_mfma_f32_16x16x32_bf16 v[56:59],v[28:31],v[212:215],v[56:59]
v_exp_f32 v151,v151
v_mfma_f32_16x16x32_bf16 v[164:167],a[128:131],v[136:139],v[36:39]
v_cvt_pk_bf16_f32 v134,v140,v141
v_cvt_pk_bf16_f32 v135,v142,v143
v_mfma_f32_16x16x32_bf16 v[164:167],a[132:135],v[168:171],v[164:167]
v_cvt_pk_bf16_f32 v148,v148,v149
v_cvt_pk_bf16_f32 v149,v150,v151
v_mfma_f32_16x16x32_bf16 v[164:167],a[136:139],v[200:203],v[164:167]
v_exp_f32 v156,v156
v_mfma_f32_16x16x32_bf16 v[164:167],a[140:143],v[232:235],v[164:167]
v_exp_f32 v157,v157
v_mfma_f32_16x16x32_bf16 v[172:175],a[156:159],v[232:235],v[36:39]
v_exp_f32 v158,v158
v_mfma_f32_16x16x32_bf16 v[172:175],a[152:155],v[200:203],v[172:175]
v_exp_f32 v159,v159
v_mfma_f32_16x16x32_bf16 v[172:175],a[148:151],v[168:171],v[172:175]
buffer_store_dwordx4 v[84:87],v6,s[36:39],s85 offen offset:1024 sc1
v_cvt_pk_bf16_f32 v150,v156,v157
v_mfma_f32_16x16x32_bf16 v[172:175],a[144:147],v[136:139],v[172:175]
v_cvt_pk_bf16_f32 v151,v158,v159
v_mfma_f32_16x16x32_bf16 v[180:183],a[160:163],v[136:139],v[36:39]
v_mfma_f32_16x16x32_bf16 v[180:183],a[164:167],v[168:171],v[180:183]
v_exp_f32 v164,v164
v_mfma_f32_16x16x32_bf16 v[180:183],a[168:171],v[200:203],v[180:183]
v_exp_f32 v165,v165
v_mfma_f32_16x16x32_bf16 v[180:183],a[172:175],v[232:235],v[180:183]
v_exp_f32 v166,v166
v_mfma_f32_16x16x32_bf16 v[188:191],a[188:191],v[232:235],v[36:39]
v_exp_f32 v167,v167
v_mfma_f32_16x16x32_bf16 v[188:191],a[184:187],v[200:203],v[188:191]
v_cvt_pk_bf16_f32 v164,v164,v165
v_cvt_pk_bf16_f32 v165,v166,v167
v_mfma_f32_16x16x32_bf16 v[188:191],a[180:183],v[168:171],v[188:191]
v_exp_f32 v172,v172
v_mfma_f32_16x16x32_bf16 v[188:191],a[176:179],v[136:139],v[188:191]
v_exp_f32 v173,v173
v_mfma_f32_16x16x32_bf16 a[96:99],a[192:195],v[228:231],a[96:99]
ds_read_b64_tr_b16 a[192:193],v2
ds_read_b64_tr_b16 a[194:195],v2 offset:4096
v_mfma_f32_16x16x32_bf16 a[96:99],a[196:199],v[244:247],a[96:99]
ds_read_b64_tr_b16 a[196:197],v2 offset:8192
v_cvt_pk_bf16_f32 v166,v172,v173
v_mfma_f32_16x16x32_bf16 a[100:103],a[204:207],v[244:247],a[100:103]
ds_read_b64_tr_b16 a[198:199],v2 offset:12288
v_mfma_f32_16x16x32_bf16 a[100:103],a[200:203],v[228:231],a[100:103]
ds_read_b64_tr_b16 a[204:205],v3 offset:8192
v_mfma_f32_16x16x32_bf16 a[104:107],a[208:211],v[228:231],a[104:107]
buffer_store_dwordx4 v[88:91],v6,s[36:39],s86 offen offset:1024 sc1
ds_read_b64_tr_b16 a[206:207],v3 offset:12288
v_mfma_f32_16x16x32_bf16 a[104:107],a[212:215],v[244:247],a[104:107]
ds_read_b64_tr_b16 a[200:201],v3
ds_read_b64_tr_b16 a[202:203],v3 offset:4096
v_mfma_f32_16x16x32_bf16 a[108:111],a[220:223],v[244:247],a[108:111]
ds_read_b64_tr_b16 a[208:209],v2 offset:256
v_mfma_f32_16x16x32_bf16 a[108:111],a[216:219],v[228:231],a[108:111]
ds_read_b64_tr_b16 a[210:211],v2 offset:4352
v_mfma_f32_16x16x32_bf16 a[112:115],a[224:227],v[228:231],a[112:115]
ds_read_b64_tr_b16 a[212:213],v2 offset:8448
v_mfma_f32_16x16x32_bf16 a[112:115],a[228:231],v[244:247],a[112:115]
ds_read_b64_tr_b16 a[214:215],v2 offset:12544
ds_read_b64_tr_b16 a[220:221],v3 offset:8448
v_mfma_f32_16x16x32_bf16 a[116:119],a[236:239],v[244:247],a[116:119]
ds_read_b64_tr_b16 a[222:223],v3 offset:12544
v_mfma_f32_16x16x32_bf16 a[116:119],a[232:235],v[228:231],a[116:119]
ds_read_b64_tr_b16 a[216:217],v3 offset:256
v_mfma_f32_16x16x32_bf16 a[120:123],a[240:243],v[228:231],a[120:123]
ds_read_b64_tr_b16 a[218:219],v3 offset:4352
v_mfma_f32_16x16x32_bf16 a[120:123],a[244:247],v[244:247],a[120:123]
ds_read_b64_tr_b16 a[224:225],v2 offset:512
ds_read_b64_tr_b16 a[226:227],v2 offset:4608
v_mfma_f32_16x16x32_bf16 a[124:127],a[252:255],v[244:247],a[124:127]
ds_read_b64_tr_b16 a[228:229],v2 offset:8704
v_mfma_f32_16x16x32_bf16 a[124:127],a[248:251],v[228:231],a[124:127]
ds_read_b64_tr_b16 a[230:231],v2 offset:12800
v_mfma_f32_16x16x32_bf16 v[60:63],v[28:31],v[228:231],v[60:63]
ds_read_b64_tr_b16 a[236:237],v3 offset:8704
v_mfma_f32_16x16x32_bf16 v[60:63],v[28:31],v[244:247],v[60:63]
ds_read_b64_tr_b16 a[238:239],v3 offset:12800
ds_read_b64_tr_b16 a[232:233],v3 offset:512
v_mfma_f32_16x16x32_bf16 v[196:199],a[128:131],v[144:147],v[40:43]
buffer_store_dwordx4 v[92:95],v6,s[36:39],s87 offen offset:1024 sc1
ds_read_b64_tr_b16 a[234:235],v3 offset:4608
v_mfma_f32_16x16x32_bf16 v[196:199],a[132:135],v[176:179],v[196:199]
ds_read_b64_tr_b16 a[240:241],v2 offset:768
v_mfma_f32_16x16x32_bf16 v[196:199],a[136:139],v[208:211],v[196:199]
ds_read_b64_tr_b16 a[242:243],v2 offset:4864
v_mfma_f32_16x16x32_bf16 v[196:199],a[140:143],v[240:243],v[196:199]
ds_read_b64_tr_b16 a[244:245],v2 offset:8960
ds_read_b64_tr_b16 a[246:247],v2 offset:13056
v_mfma_f32_16x16x32_bf16 v[204:207],a[156:159],v[240:243],v[40:43]
ds_read_b64_tr_b16 a[252:253],v3 offset:8960
v_add_u32 v2,s48,v2
v_mfma_f32_16x16x32_bf16 v[204:207],a[152:155],v[208:211],v[204:207]
ds_read_b64_tr_b16 a[254:255],v3 offset:13056
v_mfma_f32_16x16x32_bf16 v[204:207],a[148:151],v[176:179],v[204:207]
ds_read_b64_tr_b16 a[248:249],v3 offset:768
v_mfma_f32_16x16x32_bf16 v[204:207],a[144:147],v[144:147],v[204:207]
ds_read_b64_tr_b16 a[250:251],v3 offset:4864
v_add_u32 v3,s48,v3
v_mfma_f32_16x16x32_bf16 v[212:215],a[160:163],v[144:147],v[40:43]
s_sub_u32 s48,32768,s62
v_exp_f32 v174,v174
v_mfma_f32_16x16x32_bf16 v[212:215],a[164:167],v[176:179],v[212:215]
s_add_u32 s50,s50,s48
v_exp_f32 v175,v175
v_mfma_f32_16x16x32_bf16 v[212:215],a[168:171],v[208:211],v[212:215]
s_mov_b32 s49,s50
v_exp_f32 v180,v180
v_mfma_f32_16x16x32_bf16 v[212:215],a[172:175],v[240:243],v[212:215]
v_exp_f32 v181,v181
v_mfma_f32_16x16x32_bf16 v[220:223],a[188:191],v[240:243],v[40:43]
v_exp_f32 v182,v182
v_mfma_f32_16x16x32_bf16 v[220:223],a[184:187],v[208:211],v[220:223]
v_exp_f32 v183,v183
v_mfma_f32_16x16x32_bf16 v[220:223],a[180:183],v[176:179],v[220:223]
buffer_store_dwordx4 v[96:99],v6,s[36:39],s84 offen offset:2048 sc1
v_cvt_pk_bf16_f32 v167,v174,v175
v_mfma_f32_16x16x32_bf16 v[220:223],a[176:179],v[144:147],v[220:223]
s_waitcnt lgkmcnt(0)
v_cvt_pk_bf16_f32 v180,v180,v181
v_cvt_pk_bf16_f32 v181,v182,v183
v_mfma_f32_16x16x32_bf16 a[0:3],a[192:195],v[132:135],a[0:3]
v_exp_f32 v188,v188
v_mfma_f32_16x16x32_bf16 a[0:3],a[196:199],v[148:151],a[0:3]
v_exp_f32 v189,v189
v_mfma_f32_16x16x32_bf16 a[4:7],a[204:207],v[148:151],a[4:7]
v_exp_f32 v190,v190
v_mfma_f32_16x16x32_bf16 a[4:7],a[200:203],v[132:135],a[4:7]
v_exp_f32 v191,v191
v_mfma_f32_16x16x32_bf16 a[8:11],a[208:211],v[132:135],a[8:11]
v_cvt_pk_bf16_f32 v182,v188,v189
v_cvt_pk_bf16_f32 v183,v190,v191
v_mfma_f32_16x16x32_bf16 a[8:11],a[212:215],v[148:151],a[8:11]
v_exp_f32 v196,v196
v_mfma_f32_16x16x32_bf16 a[12:15],a[220:223],v[148:151],a[12:15]
v_exp_f32 v197,v197
v_mfma_f32_16x16x32_bf16 a[12:15],a[216:219],v[132:135],a[12:15]
s_add_u32 s80,s71,0
s_and_b32 s80,s80,3
s_lshl_b32 s80,s80,1
v_mfma_f32_16x16x32_bf16 a[16:19],a[224:227],v[132:135],a[16:19]
s_mul_i32 s80,s80,163840
s_add_u32 s80,s80,s81
v_cvt_pk_bf16_f32 v196,v196,v197
v_mfma_f32_16x16x32_bf16 a[16:19],a[228:231],v[148:151],a[16:19]
s_add_u32 s83,s80,20480
v_exp_f32 v198,v198
v_mfma_f32_16x16x32_bf16 a[20:23],a[236:239],v[148:151],a[20:23]
v_exp_f32 v199,v199
v_mfma_f32_16x16x32_bf16 a[20:23],a[232:235],v[132:135],a[20:23]
)A"
R"A(v_exp_f32 v204,v204
v_mfma_f32_16x16x32_bf16 a[24:27],a[240:243],v[132:135],a[24:27]
buffer_store_dwordx4 v[100:103],v6,s[36:39],s85 offen offset:2048 sc1
v_cvt_pk_bf16_f32 v197,v198,v199
v_mfma_f32_16x16x32_bf16 a[24:27],a[244:247],v[148:151],a[24:27]
v_exp_f32 v205,v205
v_mfma_f32_16x16x32_bf16 a[28:31],a[252:255],v[148:151],a[28:31]
v_exp_f32 v206,v206
v_mfma_f32_16x16x32_bf16 a[28:31],a[248:251],v[132:135],a[28:31]
v_exp_f32 v207,v207
v_mfma_f32_16x16x32_bf16 v[48:51],v[28:31],v[132:135],v[48:51]
v_exp_f32 v212,v212
v_mfma_f32_16x16x32_bf16 v[48:51],v[28:31],v[148:151],v[48:51]
v_exp_f32 v213,v213
s_waitcnt vmcnt(26)
s_barrier
v_mfma_f32_16x16x32_bf16 v[228:231],a[128:131],v[152:155],v[44:47]
ds_read_b128 a[128:131],v1
v_cvt_pk_bf16_f32 v198,v204,v205
v_mfma_f32_16x16x32_bf16 v[228:231],a[132:135],v[184:187],v[228:231]
v_exp_f32 v214,v214
v_mfma_f32_16x16x32_bf16 v[228:231],a[136:139],v[216:219],v[228:231]
ds_read_b128 a[132:135],v1 offset:256
v_cvt_pk_bf16_f32 v199,v206,v207
v_mfma_f32_16x16x32_bf16 v[228:231],a[140:143],v[248:251],v[228:231]
v_exp_f32 v215,v215
v_mfma_f32_16x16x32_bf16 v[236:239],a[156:159],v[248:251],v[44:47]
ds_read_b128 a[136:139],v1 offset:512
v_cvt_pk_bf16_f32 v212,v212,v213
v_mfma_f32_16x16x32_bf16 v[236:239],a[152:155],v[216:219],v[236:239]
v_exp_f32 v220,v220
v_mfma_f32_16x16x32_bf16 v[236:239],a[148:151],v[184:187],v[236:239]
ds_read_b128 a[140:143],v1 offset:768
v_cvt_pk_bf16_f32 v213,v214,v215
v_mfma_f32_16x16x32_bf16 v[236:239],a[144:147],v[152:155],v[236:239]
s_cmp_eq_u32 s50,131072
s_cselect_b32 s62,163840,0
buffer_store_dwordx4 v[104:107],v6,s[36:39],s86 offen offset:2048 sc1
v_mfma_f32_16x16x32_bf16 v[244:247],a[160:163],v[152:155],v[44:47]
ds_read_b128 a[156:159],v1 offset:4864
v_mfma_f32_16x16x32_bf16 v[244:247],a[164:167],v[184:187],v[244:247]
s_add_u32 m0,s82,0
v_exp_f32 v221,v221
v_mfma_f32_16x16x32_bf16 v[244:247],a[168:171],v[216:219],v[244:247]
ds_read_b128 a[152:155],v1 offset:4608
v_cvt_pk_bf16_f32 v214,v220,v221
v_mfma_f32_16x16x32_bf16 v[244:247],a[172:175],v[248:251],v[244:247]
v_exp_f32 v222,v222
v_mfma_f32_16x16x32_bf16 v[252:255],a[188:191],v[248:251],v[44:47]
buffer_load_dwordx4 v7,s[72:75],s80 offen lds
ds_read_b128 a[148:151],v1 offset:4352
v_mfma_f32_16x16x32_bf16 v[252:255],a[184:187],v[216:219],v[252:255]
v_exp_f32 v223,v223
v_mfma_f32_16x16x32_bf16 v[252:255],a[180:183],v[184:187],v[252:255]
ds_read_b128 a[144:147],v1 offset:4096
v_cvt_pk_bf16_f32 v215,v222,v223
v_mfma_f32_16x16x32_bf16 v[252:255],a[176:179],v[152:155],v[252:255]
v_exp_f32 v228,v228
v_mfma_f32_16x16x32_bf16 a[32:35],a[192:195],v[164:167],a[32:35]
ds_read_b128 a[160:163],v1 offset:8192
v_mfma_f32_16x16x32_bf16 a[32:35],a[196:199],v[180:183],a[32:35]
v_exp_f32 v229,v229
v_mfma_f32_16x16x32_bf16 a[36:39],a[204:207],v[180:183],a[36:39]
ds_read_b128 a[164:167],v1 offset:8448
v_cvt_pk_bf16_f32 v228,v228,v229
v_mfma_f32_16x16x32_bf16 a[36:39],a[200:203],v[164:167],a[36:39]
v_exp_f32 v230,v230
v_mfma_f32_16x16x32_bf16 a[40:43],a[208:211],v[164:167],a[40:43]
ds_read_b128 a[168:171],v1 offset:8704
v_mfma_f32_16x16x32_bf16 a[40:43],a[212:215],v[180:183],a[40:43]
buffer_store_dwordx4 v[108:111],v6,s[36:39],s87 offen offset:2048 sc1
v_mfma_f32_16x16x32_bf16 a[44:47],a[220:223],v[180:183],a[44:47]
ds_read_b128 a[172:175],v1 offset:8960
v_mfma_f32_16x16x32_bf16 a[44:47],a[216:219],v[164:167],a[44:47]
v_exp_f32 v231,v231
v_mfma_f32_16x16x32_bf16 a[48:51],a[224:227],v[164:167],a[48:51]
ds_read_b128 a[188:191],v1 offset:13056
v_cvt_pk_bf16_f32 v229,v230,v231
v_mfma_f32_16x16x32_bf16 a[48:51],a[228:231],v[180:183],a[48:51]
buffer_load_dwordx4 v8,s[72:75],s80 offen offset:1024 lds
v_mfma_f32_16x16x32_bf16 a[52:55],a[236:239],v[180:183],a[52:55]
ds_read_b128 a[184:187],v1 offset:12800
v_mfma_f32_16x16x32_bf16 a[52:55],a[232:235],v[164:167],a[52:55]
v_exp_f32 v236,v236
v_mfma_f32_16x16x32_bf16 a[56:59],a[240:243],v[164:167],a[56:59]
ds_read_b128 a[180:183],v1 offset:12544
v_mfma_f32_16x16x32_bf16 a[56:59],a[244:247],v[180:183],a[56:59]
v_exp_f32 v237,v237
v_mfma_f32_16x16x32_bf16 a[60:63],a[252:255],v[180:183],a[60:63]
ds_read_b128 a[176:179],v1 offset:12288
v_cvt_pk_bf16_f32 v230,v236,v237
v_mfma_f32_16x16x32_bf16 a[60:63],a[248:251],v[164:167],a[60:63]
v_exp_f32 v238,v238
v_mfma_f32_16x16x32_bf16 v[52:55],v[28:31],v[164:167],v[52:55]
v_exp_f32 v239,v239
v_mfma_f32_16x16x32_bf16 v[52:55],v[28:31],v[180:183],v[52:55]
s_waitcnt lgkmcnt(3)
v_exp_f32 v244,v244
v_mfma_f32_16x16x32_bf16 v[132:135],a[128:131],v[128:131],v[32:35]
v_exp_f32 v245,v245
v_mfma_f32_16x16x32_bf16 v[132:135],a[132:135],v[160:163],v[132:135]
buffer_store_dwordx4 v[112:115],v6,s[36:39],s84 offen offset:3072 sc1
v_cvt_pk_bf16_f32 v231,v238,v239
v_mfma_f32_16x16x32_bf16 v[132:135],a[136:139],v[192:195],v[132:135]
s_add_u32 s84,s55,65536
v_exp_f32 v246,v246
v_mfma_f32_16x16x32_bf16 v[132:135],a[140:143],v[224:227],v[132:135]
v_exp_f32 v247,v247
v_mfma_f32_16x16x32_bf16 v[140:143],a[156:159],v[224:227],v[32:35]
v_cvt_pk_bf16_f32 v244,v244,v245
v_cvt_pk_bf16_f32 v245,v246,v247
v_mfma_f32_16x16x32_bf16 v[140:143],a[152:155],v[192:195],v[140:143]
buffer_load_dwordx4 v9,s[72:75],s80 offen offset:2048 lds
v_add_u32 v1,s48,v1
v_mfma_f32_16x16x32_bf16 v[140:143],a[148:151],v[160:163],v[140:143]
v_exp_f32 v252,v252
v_mfma_f32_16x16x32_bf16 v[140:143],a[144:147],v[128:131],v[140:143]
v_exp_f32 v253,v253
v_mfma_f32_16x16x32_bf16 v[148:151],a[160:163],v[128:131],v[32:35]
v_exp_f32 v254,v254
v_mfma_f32_16x16x32_bf16 v[148:151],a[164:167],v[160:163],v[148:151]
v_exp_f32 v255,v255
v_mfma_f32_16x16x32_bf16 v[148:151],a[168:171],v[192:195],v[148:151]
v_cvt_pk_bf16_f32 v246,v252,v253
v_cvt_pk_bf16_f32 v247,v254,v255
v_mfma_f32_16x16x32_bf16 v[148:151],a[172:175],v[224:227],v[148:151]
v_exp_f32 v132,v132
v_mfma_f32_16x16x32_bf16 v[156:159],a[188:191],v[224:227],v[32:35]
s_waitcnt lgkmcnt(0)
v_exp_f32 v133,v133
v_mfma_f32_16x16x32_bf16 v[156:159],a[184:187],v[192:195],v[156:159]
v_exp_f32 v134,v134
v_mfma_f32_16x16x32_bf16 v[156:159],a[180:183],v[160:163],v[156:159]
buffer_load_dwordx4 v10,s[72:75],s80 offen offset:3072 lds
v_cvt_pk_bf16_f32 v132,v132,v133
v_mfma_f32_16x16x32_bf16 v[156:159],a[176:179],v[128:131],v[156:159]
v_exp_f32 v135,v135
v_mfma_f32_16x16x32_bf16 a[64:67],a[192:195],v[196:199],a[64:67]
v_exp_f32 v140,v140
v_mfma_f32_16x16x32_bf16 a[64:67],a[196:199],v[212:215],a[64:67]
v_exp_f32 v141,v141
v_mfma_f32_16x16x32_bf16 a[68:71],a[204:207],v[212:215],a[68:71]
buffer_store_dwordx4 v[116:119],v6,s[36:39],s85 offen offset:3072 sc1
v_cvt_pk_bf16_f32 v133,v134,v135
v_mfma_f32_16x16x32_bf16 a[68:71],a[200:203],v[196:199],a[68:71]
s_add_u32 s85,s55,69632
v_exp_f32 v142,v142
v_mfma_f32_16x16x32_bf16 a[72:75],a[208:211],v[196:199],a[72:75]
v_exp_f32 v143,v143
v_mfma_f32_16x16x32_bf16 a[72:75],a[212:215],v[212:215],a[72:75]
v_exp_f32 v148,v148
v_mfma_f32_16x16x32_bf16 a[76:79],a[220:223],v[212:215],a[76:79]
v_exp_f32 v149,v149
v_mfma_f32_16x16x32_bf16 a[76:79],a[216:219],v[196:199],a[76:79]
s_add_u32 m0,s82,4096
v_exp_f32 v150,v150
v_mfma_f32_16x16x32_bf16 a[80:83],a[224:227],v[196:199],a[80:83]
v_exp_f32 v151,v151
v_mfma_f32_16x16x32_bf16 a[80:83],a[228:231],v[212:215],a[80:83]
v_cvt_pk_bf16_f32 v134,v140,v141
v_cvt_pk_bf16_f32 v135,v142,v143
v_mfma_f32_16x16x32_bf16 a[84:87],a[236:239],v[212:215],a[84:87]
buffer_load_dwordx4 v11,s[72:75],s80 offen lds
v_cvt_pk_bf16_f32 v148,v148,v149
v_mfma_f32_16x16x32_bf16 a[84:87],a[232:235],v[196:199],a[84:87]
v_exp_f32 v156,v156
v_mfma_f32_16x16x32_bf16 a[88:91],a[240:243],v[196:199],a[88:91]
v_exp_f32 v157,v157
v_mfma_f32_16x16x32_bf16 a[88:91],a[244:247],v[212:215],a[88:91]
v_exp_f32 v158,v158
v_mfma_f32_16x16x32_bf16 a[92:95],a[252:255],v[212:215],a[92:95]
buffer_store_dwordx4 v[120:123],v6,s[36:39],s86 offen offset:3072 sc1
v_cvt_pk_bf16_f32 v149,v150,v151
v_mfma_f32_16x16x32_bf16 a[92:95],a[248:251],v[196:199],a[92:95]
s_add_u32 s86,s55,73728
v_exp_f32 v159,v159
v_mfma_f32_16x16x32_bf16 v[56:59],v[28:31],v[196:199],v[56:59]
v_cvt_pk_bf16_f32 v150,v156,v157
v_cvt_pk_bf16_f32 v151,v158,v159
v_mfma_f32_16x16x32_bf16 v[56:59],v[28:31],v[212:215],v[56:59]
v_mfma_f32_16x16x32_bf16 v[164:167],a[128:131],v[136:139],v[36:39]
v_mfma_f32_16x16x32_bf16 v[164:167],a[132:135],v[168:171],v[164:167]
s_add_u32 m0,s82,5120
v_mfma_f32_16x16x32_bf16 v[164:167],a[136:139],v[200:203],v[164:167]
v_mfma_f32_16x16x32_bf16 v[164:167],a[140:143],v[232:235],v[164:167]
v_mfma_f32_16x16x32_bf16 v[172:175],a[156:159],v[232:235],v[36:39]
buffer_load_dwordx4 v7,s[72:75],s83 offen lds
v_mfma_f32_16x16x32_bf16 v[172:175],a[152:155],v[200:203],v[172:175]
v_mfma_f32_16x16x32_bf16 v[172:175],a[148:151],v[168:171],v[172:175]
v_mfma_f32_16x16x32_bf16 v[172:175],a[144:147],v[136:139],v[172:175]
v_mfma_f32_16x16x32_bf16 v[180:183],a[160:163],v[136:139],v[36:39]
v_mfma_f32_16x16x32_bf16 v[180:183],a[164:167],v[168:171],v[180:183]
buffer_store_dwordx4 v[124:127],v6,s[36:39],s87 offen offset:3072 sc1
v_mfma_f32_16x16x32_bf16 v[180:183],a[168:171],v[200:203],v[180:183]
s_add_u32 s87,s55,77824
v_exp_f32 v164,v164
v_mfma_f32_16x16x32_bf16 v[180:183],a[172:175],v[232:235],v[180:183]
v_exp_f32 v165,v165
v_mfma_f32_16x16x32_bf16 v[188:191],a[188:191],v[232:235],v[36:39]
v_exp_f32 v166,v166
v_mfma_f32_16x16x32_bf16 v[188:191],a[184:187],v[200:203],v[188:191]
v_exp_f32 v167,v167
v_mfma_f32_16x16x32_bf16 v[188:191],a[180:183],v[168:171],v[188:191]
v_cvt_pk_bf16_f32 v164,v164,v165
v_cvt_pk_bf16_f32 v165,v166,v167
v_mfma_f32_16x16x32_bf16 v[188:191],a[176:179],v[136:139],v[188:191]
v_exp_f32 v172,v172
v_mfma_f32_16x16x32_bf16 a[96:99],a[192:195],v[228:231],a[96:99]
buffer_load_dwordx4 v8,s[72:75],s83 offen offset:1024 lds
ds_read_b64_tr_b16 a[192:193],v2
v_mfma_f32_16x16x32_bf16 a[96:99],a[196:199],v[244:247],a[96:99]
ds_read_b64_tr_b16 a[194:195],v2 offset:4096
ds_read_b64_tr_b16 a[196:197],v2 offset:8192
v_mfma_f32_16x16x32_bf16 a[100:103],a[204:207],v[244:247],a[100:103]
ds_read_b64_tr_b16 a[198:199],v2 offset:12288
v_mfma_f32_16x16x32_bf16 a[100:103],a[200:203],v[228:231],a[100:103]
ds_read_b64_tr_b16 a[204:205],v3 offset:8192
v_mfma_f32_16x16x32_bf16 a[104:107],a[208:211],v[228:231],a[104:107]
ds_read_b64_tr_b16 a[206:207],v3 offset:12288
v_mfma_f32_16x16x32_bf16 a[104:107],a[212:215],v[244:247],a[104:107]
ds_read_b64_tr_b16 a[200:201],v3
ds_read_b64_tr_b16 a[202:203],v3 offset:4096
v_mfma_f32_16x16x32_bf16 a[108:111],a[220:223],v[244:247],a[108:111]
ds_read_b64_tr_b16 a[208:209],v2 offset:256
v_mfma_f32_16x16x32_bf16 a[108:111],a[216:219],v[228:231],a[108:111]
ds_read_b64_tr_b16 a[210:211],v2 offset:4352
v_mfma_f32_16x16x32_bf16 a[112:115],a[224:227],v[228:231],a[112:115]
ds_read_b64_tr_b16 a[212:213],v2 offset:8448
v_mfma_f32_16x16x32_bf16 a[112:115],a[228:231],v[244:247],a[112:115]
ds_read_b64_tr_b16 a[214:215],v2 offset:12544
ds_read_b64_tr_b16 a[220:221],v3 offset:8448
v_mfma_f32_16x16x32_bf16 a[116:119],a[236:239],v[244:247],a[116:119]
ds_read_b64_tr_b16 a[222:223],v3 offset:12544
v_mfma_f32_16x16x32_bf16 a[116:119],a[232:235],v[228:231],a[116:119]
ds_read_b64_tr_b16 a[216:217],v3 offset:256
v_mfma_f32_16x16x32_bf16 a[120:123],a[240:243],v[228:231],a[120:123]
buffer_load_dwordx4 v9,s[72:75],s83 offen offset:2048 lds
ds_read_b64_tr_b16 a[218:219],v3 offset:4352
v_mfma_f32_16x16x32_bf16 a[120:123],a[244:247],v[244:247],a[120:123]
ds_read_b64_tr_b16 a[224:225],v2 offset:512
ds_read_b64_tr_b16 a[226:227],v2 offset:4608
v_mfma_f32_16x16x32_bf16 a[124:127],a[252:255],v[244:247],a[124:127]
ds_read_b64_tr_b16 a[228:229],v2 offset:8704
v_mfma_f32_16x16x32_bf16 a[124:127],a[248:251],v[228:231],a[124:127]
ds_read_b64_tr_b16 a[230:231],v2 offset:12800
v_mfma_f32_16x16x32_bf16 v[60:63],v[28:31],v[228:231],v[60:63]
ds_read_b64_tr_b16 a[236:237],v3 offset:8704
v_mfma_f32_16x16x32_bf16 v[60:63],v[28:31],v[244:247],v[60:63]
ds_read_b64_tr_b16 a[238:239],v3 offset:12800
ds_read_b64_tr_b16 a[232:233],v3 offset:512
v_mfma_f32_16x16x32_bf16 v[196:199],a[128:131],v[144:147],v[40:43]
ds_read_b64_tr_b16 a[234:235],v3 offset:4608
v_mfma_f32_16x16x32_bf16 v[196:199],a[132:135],v[176:179],v[196:199]
)A"
R"A(ds_read_b64_tr_b16 a[240:241],v2 offset:768
v_mfma_f32_16x16x32_bf16 v[196:199],a[136:139],v[208:211],v[196:199]
ds_read_b64_tr_b16 a[242:243],v2 offset:4864
v_mfma_f32_16x16x32_bf16 v[196:199],a[140:143],v[240:243],v[196:199]
ds_read_b64_tr_b16 a[244:245],v2 offset:8960
ds_read_b64_tr_b16 a[246:247],v2 offset:13056
v_mfma_f32_16x16x32_bf16 v[204:207],a[156:159],v[240:243],v[40:43]
ds_read_b64_tr_b16 a[252:253],v3 offset:8960
v_add_u32 v2,s48,v2
v_mfma_f32_16x16x32_bf16 v[204:207],a[152:155],v[208:211],v[204:207]
ds_read_b64_tr_b16 a[254:255],v3 offset:13056
v_mfma_f32_16x16x32_bf16 v[204:207],a[148:151],v[176:179],v[204:207]
buffer_load_dwordx4 v10,s[72:75],s83 offen offset:3072 lds
ds_read_b64_tr_b16 a[248:249],v3 offset:768
v_mfma_f32_16x16x32_bf16 v[204:207],a[144:147],v[144:147],v[204:207]
ds_read_b64_tr_b16 a[250:251],v3 offset:4864
v_add_u32 v3,s48,v3
v_mfma_f32_16x16x32_bf16 v[212:215],a[160:163],v[144:147],v[40:43]
s_sub_u32 s48,32768,s62
v_exp_f32 v173,v173
v_mfma_f32_16x16x32_bf16 v[212:215],a[164:167],v[176:179],v[212:215]
s_add_u32 s50,s50,s48
v_exp_f32 v174,v174
v_mfma_f32_16x16x32_bf16 v[212:215],a[168:171],v[208:211],v[212:215]
s_mov_b32 s49,s50
v_exp_f32 v175,v175
v_mfma_f32_16x16x32_bf16 v[212:215],a[172:175],v[240:243],v[212:215]
v_exp_f32 v180,v180
v_mfma_f32_16x16x32_bf16 v[220:223],a[188:191],v[240:243],v[40:43]
v_exp_f32 v181,v181
v_mfma_f32_16x16x32_bf16 v[220:223],a[184:187],v[208:211],v[220:223]
v_exp_f32 v182,v182
v_mfma_f32_16x16x32_bf16 v[220:223],a[180:183],v[176:179],v[220:223]
v_exp_f32 v183,v183
v_mfma_f32_16x16x32_bf16 v[220:223],a[176:179],v[144:147],v[220:223]
s_add_u32 m0,s82,9216
v_cvt_pk_bf16_f32 v166,v172,v173
v_cvt_pk_bf16_f32 v167,v174,v175
s_waitcnt lgkmcnt(0)
v_mfma_f32_16x16x32_bf16 a[0:3],a[192:195],v[132:135],a[0:3]
v_cvt_pk_bf16_f32 v180,v180,v181
v_cvt_pk_bf16_f32 v181,v182,v183
v_mfma_f32_16x16x32_bf16 a[0:3],a[196:199],v[148:151],a[0:3]
v_exp_f32 v188,v188
v_mfma_f32_16x16x32_bf16 a[4:7],a[204:207],v[148:151],a[4:7]
s_cmp_eq_u32 s50,131072
s_cselect_b32 s62,163840,0
buffer_load_dwordx4 v11,s[72:75],s83 offen lds
v_mfma_f32_16x16x32_bf16 a[4:7],a[200:203],v[132:135],a[4:7]
v_exp_f32 v189,v189
v_mfma_f32_16x16x32_bf16 a[8:11],a[208:211],v[132:135],a[8:11]
v_exp_f32 v190,v190
v_mfma_f32_16x16x32_bf16 a[8:11],a[212:215],v[148:151],a[8:11]
v_exp_f32 v191,v191
v_mfma_f32_16x16x32_bf16 a[12:15],a[220:223],v[148:151],a[12:15]
v_cvt_pk_bf16_f32 v182,v188,v189
v_cvt_pk_bf16_f32 v183,v190,v191
v_mfma_f32_16x16x32_bf16 a[12:15],a[216:219],v[132:135],a[12:15]
v_exp_f32 v196,v196
v_mfma_f32_16x16x32_bf16 a[16:19],a[224:227],v[132:135],a[16:19]
v_exp_f32 v197,v197
v_mfma_f32_16x16x32_bf16 a[16:19],a[228:231],v[148:151],a[16:19]
v_exp_f32 v198,v198
v_mfma_f32_16x16x32_bf16 a[20:23],a[236:239],v[148:151],a[20:23]
v_exp_f32 v199,v199
v_mfma_f32_16x16x32_bf16 a[20:23],a[232:235],v[132:135],a[20:23]
v_cvt_pk_bf16_f32 v196,v196,v197
v_cvt_pk_bf16_f32 v197,v198,v199
v_mfma_f32_16x16x32_bf16 a[24:27],a[240:243],v[132:135],a[24:27]
v_exp_f32 v204,v204
v_mfma_f32_16x16x32_bf16 a[24:27],a[244:247],v[148:151],a[24:27]
v_exp_f32 v205,v205
v_mfma_f32_16x16x32_bf16 a[28:31],a[252:255],v[148:151],a[28:31]
v_exp_f32 v206,v206
v_mfma_f32_16x16x32_bf16 a[28:31],a[248:251],v[132:135],a[28:31]
v_exp_f32 v207,v207
v_mfma_f32_16x16x32_bf16 v[48:51],v[28:31],v[132:135],v[48:51]
v_exp_f32 v212,v212
v_mfma_f32_16x16x32_bf16 v[48:51],v[28:31],v[148:151],v[48:51]
v_exp_f32 v213,v213
s_waitcnt vmcnt(34)
s_barrier
v_mfma_f32_16x16x32_bf16 v[228:231],a[128:131],v[152:155],v[44:47]
s_lshl_b32 s95,s42,2
s_add_u32 s95,s95,s40
s_lshl_b32 s95,s95,4
v_mfma_f32_16x16x32_bf16 v[228:231],a[132:135],v[184:187],v[228:231]
v_mov_b32 v18,s68
v_mov_b32 v19,s69
v_mfma_f32_16x16x32_bf16 v[228:231],a[136:139],v[216:219],v[228:231]
s_waitcnt vmcnt(4)
s_mov_b64 exec,1
buffer_store_dwordx2 v[18:19],v6,s[64:67],s95 offen sc1
s_mov_b64 exec,-1
v_mfma_f32_16x16x32_bf16 v[228:231],a[140:143],v[248:251],v[228:231]
s_cmp_ge_u32 0,s71
s_cselect_b32 s80,2,0
ds_read_b128 a[128:131],v1
v_mfma_f32_16x16x32_bf16 v[236:239],a[156:159],v[248:251],v[44:47]
s_mul_i32 s80,s80,163840
s_add_u32 s80,s80,s81
v_cvt_pk_bf16_f32 v198,v204,v205
v_mfma_f32_16x16x32_bf16 v[236:239],a[152:155],v[216:219],v[236:239]
s_add_u32 s83,s80,20480
ds_read_b128 a[132:135],v1 offset:256
v_cvt_pk_bf16_f32 v199,v206,v207
v_mfma_f32_16x16x32_bf16 v[236:239],a[148:151],v[184:187],v[236:239]
v_exp_f32 v214,v214
v_mfma_f32_16x16x32_bf16 v[236:239],a[144:147],v[152:155],v[236:239]
ds_read_b128 a[136:139],v1 offset:512
v_cvt_pk_bf16_f32 v212,v212,v213
v_mfma_f32_16x16x32_bf16 v[244:247],a[160:163],v[152:155],v[44:47]
v_exp_f32 v215,v215
v_mfma_f32_16x16x32_bf16 v[244:247],a[164:167],v[184:187],v[244:247]
ds_read_b128 a[140:143],v1 offset:768
v_cvt_pk_bf16_f32 v213,v214,v215
v_mfma_f32_16x16x32_bf16 v[244:247],a[168:171],v[216:219],v[244:247]
v_exp_f32 v220,v220
v_mfma_f32_16x16x32_bf16 v[244:247],a[172:175],v[248:251],v[244:247]
ds_read_b128 a[156:159],v1 offset:4864
v_lshrrev_b32 v18,6,v6
v_mfma_f32_16x16x32_bf16 v[252:255],a[188:191],v[248:251],v[44:47]
v_exp_f32 v221,v221
v_mfma_f32_16x16x32_bf16 v[252:255],a[184:187],v[216:219],v[252:255]
ds_read_b128 a[152:155],v1 offset:4608
v_cvt_pk_bf16_f32 v214,v220,v221
v_mfma_f32_16x16x32_bf16 v[252:255],a[180:183],v[184:187],v[252:255]
v_exp_f32 v222,v222
v_mfma_f32_16x16x32_bf16 v[252:255],a[176:179],v[152:155],v[252:255]
ds_read_b128 a[148:151],v1 offset:4352
v_mul_u32_u24 v18,320,v18
v_mfma_f32_16x16x32_bf16 a[32:35],a[192:195],v[164:167],a[32:35]
v_exp_f32 v223,v223
v_mfma_f32_16x16x32_bf16 a[32:35],a[196:199],v[180:183],a[32:35]
ds_read_b128 a[144:147],v1 offset:4096
v_cvt_pk_bf16_f32 v215,v222,v223
v_mfma_f32_16x16x32_bf16 a[36:39],a[204:207],v[180:183],a[36:39]
v_exp_f32 v228,v228
v_mfma_f32_16x16x32_bf16 a[36:39],a[200:203],v[164:167],a[36:39]
ds_read_b128 a[160:163],v1 offset:8192
v_bfe_u32 v19,v6,4,2
v_mfma_f32_16x16x32_bf16 a[40:43],a[208:211],v[164:167],a[40:43]
v_exp_f32 v229,v229
v_mfma_f32_16x16x32_bf16 a[40:43],a[212:215],v[180:183],a[40:43]
s_add_u32 m0,s82,40960
ds_read_b128 a[164:167],v1 offset:8448
v_cvt_pk_bf16_f32 v228,v228,v229
v_mfma_f32_16x16x32_bf16 a[44:47],a[220:223],v[180:183],a[44:47]
v_exp_f32 v230,v230
v_mfma_f32_16x16x32_bf16 a[44:47],a[216:219],v[164:167],a[44:47]
ds_read_b128 a[168:171],v1 offset:8704
v_lshl_add_u32 v18,v19,3,v18
v_mfma_f32_16x16x32_bf16 a[48:51],a[224:227],v[164:167],a[48:51]
buffer_load_dwordx4 v7,s[72:75],s80 offen lds
v_bfe_u32 v19,v6,8,1
v_mfma_f32_16x16x32_bf16 a[48:51],a[228:231],v[180:183],a[48:51]
ds_read_b128 a[172:175],v1 offset:8960
v_lshlrev_b32 v19,5,v19
v_mfma_f32_16x16x32_bf16 a[52:55],a[236:239],v[180:183],a[52:55]
v_exp_f32 v231,v231
v_mfma_f32_16x16x32_bf16 a[52:55],a[232:235],v[164:167],a[52:55]
ds_read_b128 a[188:191],v1 offset:13056
v_cvt_pk_bf16_f32 v229,v230,v231
v_mfma_f32_16x16x32_bf16 a[56:59],a[240:243],v[164:167],a[56:59]
v_exp_f32 v236,v236
v_mfma_f32_16x16x32_bf16 a[56:59],a[244:247],v[180:183],a[56:59]
ds_read_b128 a[184:187],v1 offset:12800
v_mfma_f32_16x16x32_bf16 a[60:63],a[252:255],v[180:183],a[60:63]
v_exp_f32 v237,v237
v_mfma_f32_16x16x32_bf16 a[60:63],a[248:251],v[164:167],a[60:63]
ds_read_b128 a[180:183],v1 offset:12544
v_cvt_pk_bf16_f32 v230,v236,v237
v_mfma_f32_16x16x32_bf16 v[52:55],v[28:31],v[164:167],v[52:55]
v_exp_f32 v238,v238
v_mfma_f32_16x16x32_bf16 v[52:55],v[28:31],v[180:183],v[52:55]
s_waitcnt lgkmcnt(3)
ds_read_b128 a[176:179],v1 offset:12288
v_add_u32 v1,s48,v1
v_mfma_f32_16x16x32_bf16 v[132:135],a[128:131],v[128:131],v[32:35]
v_exp_f32 v239,v239
v_mfma_f32_16x16x32_bf16 v[132:135],a[132:135],v[160:163],v[132:135]
v_exp_f32 v244,v244
v_mfma_f32_16x16x32_bf16 v[132:135],a[136:139],v[192:195],v[132:135]
buffer_load_dwordx4 v8,s[72:75],s80 offen offset:1024 lds
v_cvt_pk_bf16_f32 v231,v238,v239
v_mfma_f32_16x16x32_bf16 v[132:135],a[140:143],v[224:227],v[132:135]
v_exp_f32 v245,v245
v_mfma_f32_16x16x32_bf16 v[140:143],a[156:159],v[224:227],v[32:35]
v_exp_f32 v246,v246
v_mfma_f32_16x16x32_bf16 v[140:143],a[152:155],v[192:195],v[140:143]
v_exp_f32 v247,v247
v_mfma_f32_16x16x32_bf16 v[140:143],a[148:151],v[160:163],v[140:143]
v_cvt_pk_bf16_f32 v244,v244,v245
v_cvt_pk_bf16_f32 v245,v246,v247
v_mfma_f32_16x16x32_bf16 v[140:143],a[144:147],v[128:131],v[140:143]
v_exp_f32 v252,v252
v_mfma_f32_16x16x32_bf16 v[148:151],a[160:163],v[128:131],v[32:35]
v_exp_f32 v253,v253
v_mfma_f32_16x16x32_bf16 v[148:151],a[164:167],v[160:163],v[148:151]
v_exp_f32 v254,v254
v_mfma_f32_16x16x32_bf16 v[148:151],a[168:171],v[192:195],v[148:151]
v_exp_f32 v255,v255
v_mfma_f32_16x16x32_bf16 v[148:151],a[172:175],v[224:227],v[148:151]
s_waitcnt lgkmcnt(0)
v_cvt_pk_bf16_f32 v246,v252,v253
v_cvt_pk_bf16_f32 v247,v254,v255
v_mfma_f32_16x16x32_bf16 v[156:159],a[188:191],v[224:227],v[32:35]
v_exp_f32 v132,v132
v_mfma_f32_16x16x32_bf16 v[156:159],a[184:187],v[192:195],v[156:159]
v_exp_f32 v133,v133
v_mfma_f32_16x16x32_bf16 v[156:159],a[180:183],v[160:163],v[156:159]
buffer_load_dwordx4 v9,s[72:75],s80 offen offset:2048 lds
v_cvt_pk_bf16_f32 v132,v132,v133
v_mfma_f32_16x16x32_bf16 v[156:159],a[176:179],v[128:131],v[156:159]
v_exp_f32 v134,v134
v_mfma_f32_16x16x32_bf16 a[64:67],a[192:195],v[196:199],a[64:67]
v_exp_f32 v135,v135
v_mfma_f32_16x16x32_bf16 a[64:67],a[196:199],v[212:215],a[64:67]
v_exp_f32 v140,v140
v_mfma_f32_16x16x32_bf16 a[68:71],a[204:207],v[212:215],a[68:71]
v_exp_f32 v141,v141
v_mfma_f32_16x16x32_bf16 a[68:71],a[200:203],v[196:199],a[68:71]
v_exp_f32 v142,v142
v_mfma_f32_16x16x32_bf16 a[72:75],a[208:211],v[196:199],a[72:75]
v_exp_f32 v143,v143
v_mfma_f32_16x16x32_bf16 a[72:75],a[212:215],v[212:215],a[72:75]
v_exp_f32 v148,v148
v_mfma_f32_16x16x32_bf16 a[76:79],a[220:223],v[212:215],a[76:79]
v_exp_f32 v149,v149
v_mfma_f32_16x16x32_bf16 a[76:79],a[216:219],v[196:199],a[76:79]
v_cvt_pk_bf16_f32 v133,v134,v135
v_cvt_pk_bf16_f32 v134,v140,v141
v_mfma_f32_16x16x32_bf16 a[80:83],a[224:227],v[196:199],a[80:83]
v_exp_f32 v150,v150
v_mfma_f32_16x16x32_bf16 a[80:83],a[228:231],v[212:215],a[80:83]
v_exp_f32 v151,v151
v_mfma_f32_16x16x32_bf16 a[84:87],a[236:239],v[212:215],a[84:87]
buffer_load_dwordx4 v10,s[72:75],s80 offen offset:3072 lds
v_cvt_pk_bf16_f32 v135,v142,v143
v_mfma_f32_16x16x32_bf16 a[84:87],a[232:235],v[196:199],a[84:87]
v_cvt_pk_bf16_f32 v148,v148,v149
v_cvt_pk_bf16_f32 v149,v150,v151
v_mfma_f32_16x16x32_bf16 a[88:91],a[240:243],v[196:199],a[88:91]
v_exp_f32 v156,v156
v_mfma_f32_16x16x32_bf16 a[88:91],a[244:247],v[212:215],a[88:91]
v_exp_f32 v157,v157
v_mfma_f32_16x16x32_bf16 a[92:95],a[252:255],v[212:215],a[92:95]
v_exp_f32 v158,v158
v_mfma_f32_16x16x32_bf16 a[92:95],a[248:251],v[196:199],a[92:95]
v_exp_f32 v159,v159
v_mfma_f32_16x16x32_bf16 v[56:59],v[28:31],v[196:199],v[56:59]
v_cvt_pk_bf16_f32 v150,v156,v157
v_cvt_pk_bf16_f32 v151,v158,v159
v_mfma_f32_16x16x32_bf16 v[56:59],v[28:31],v[212:215],v[56:59]
v_mfma_f32_16x16x32_bf16 v[164:167],a[128:131],v[136:139],v[36:39]
v_mfma_f32_16x16x32_bf16 v[164:167],a[132:135],v[168:171],v[164:167]
s_add_u32 m0,s82,45056
v_mfma_f32_16x16x32_bf16 v[164:167],a[136:139],v[200:203],v[164:167]
v_mfma_f32_16x16x32_bf16 v[164:167],a[140:143],v[232:235],v[164:167]
v_mfma_f32_16x16x32_bf16 v[172:175],a[156:159],v[232:235],v[36:39]
buffer_load_dwordx4 v11,s[72:75],s80 offen lds
v_mfma_f32_16x16x32_bf16 v[172:175],a[152:155],v[200:203],v[172:175]
v_mfma_f32_16x16x32_bf16 v[172:175],a[148:151],v[168:171],v[172:175]
v_mfma_f32_16x16x32_bf16 v[172:175],a[144:147],v[136:139],v[172:175]
v_mfma_f32_16x16x32_bf16 v[180:183],a[160:163],v[136:139],v[36:39]
v_mfma_f32_16x16x32_bf16 v[180:183],a[164:167],v[168:171],v[180:183]
s_nop 0
v_exp_f32 v164,v164
v_mfma_f32_16x16x32_bf16 v[180:183],a[168:171],v[200:203],v[180:183]
v_exp_f32 v165,v165
v_mfma_f32_16x16x32_bf16 v[180:183],a[172:175],v[232:235],v[180:183]
v_exp_f32 v166,v166
v_mfma_f32_16x16x32_bf16 v[188:191],a[188:191],v[232:235],v[36:39]
v_exp_f32 v167,v167
v_mfma_f32_16x16x32_bf16 v[188:191],a[184:187],v[200:203],v[188:191]
s_add_u32 m0,s82,46080
)A"
R"A(v_cvt_pk_bf16_f32 v164,v164,v165
v_cvt_pk_bf16_f32 v165,v166,v167
v_mfma_f32_16x16x32_bf16 v[188:191],a[180:183],v[168:171],v[188:191]
v_exp_f32 v172,v172
v_mfma_f32_16x16x32_bf16 v[188:191],a[176:179],v[136:139],v[188:191]
v_exp_f32 v173,v173
v_mfma_f32_16x16x32_bf16 a[96:99],a[192:195],v[228:231],a[96:99]
buffer_load_dwordx4 v7,s[72:75],s83 offen lds
ds_read_b64_tr_b16 a[192:193],v2
v_mfma_f32_16x16x32_bf16 a[96:99],a[196:199],v[244:247],a[96:99]
ds_read_b64_tr_b16 a[194:195],v2 offset:4096
ds_read_b64_tr_b16 a[196:197],v2 offset:8192
v_mfma_f32_16x16x32_bf16 a[100:103],a[204:207],v[244:247],a[100:103]
ds_read_b64_tr_b16 a[198:199],v2 offset:12288
v_cvt_pk_bf16_f32 v166,v172,v173
v_mfma_f32_16x16x32_bf16 a[100:103],a[200:203],v[228:231],a[100:103]
ds_read_b64_tr_b16 a[204:205],v3 offset:8192
v_mfma_f32_16x16x32_bf16 a[104:107],a[208:211],v[228:231],a[104:107]
ds_read_b64_tr_b16 a[206:207],v3 offset:12288
v_mfma_f32_16x16x32_bf16 a[104:107],a[212:215],v[244:247],a[104:107]
ds_read_b64_tr_b16 a[200:201],v3
ds_read_b64_tr_b16 a[202:203],v3 offset:4096
v_mfma_f32_16x16x32_bf16 a[108:111],a[220:223],v[244:247],a[108:111]
ds_read_b64_tr_b16 a[208:209],v2 offset:256
v_mfma_f32_16x16x32_bf16 a[108:111],a[216:219],v[228:231],a[108:111]
ds_read_b64_tr_b16 a[210:211],v2 offset:4352
v_mfma_f32_16x16x32_bf16 a[112:115],a[224:227],v[228:231],a[112:115]
ds_read_b64_tr_b16 a[212:213],v2 offset:8448
v_mfma_f32_16x16x32_bf16 a[112:115],a[228:231],v[244:247],a[112:115]
ds_read_b64_tr_b16 a[214:215],v2 offset:12544
ds_read_b64_tr_b16 a[220:221],v3 offset:8448
v_mfma_f32_16x16x32_bf16 a[116:119],a[236:239],v[244:247],a[116:119]
ds_read_b64_tr_b16 a[222:223],v3 offset:12544
v_mfma_f32_16x16x32_bf16 a[116:119],a[232:235],v[228:231],a[116:119]
ds_read_b64_tr_b16 a[216:217],v3 offset:256
v_mfma_f32_16x16x32_bf16 a[120:123],a[240:243],v[228:231],a[120:123]
buffer_load_dwordx4 v8,s[72:75],s83 offen offset:1024 lds
ds_read_b64_tr_b16 a[218:219],v3 offset:4352
v_mfma_f32_16x16x32_bf16 a[120:123],a[244:247],v[244:247],a[120:123]
ds_read_b64_tr_b16 a[224:225],v2 offset:512
ds_read_b64_tr_b16 a[226:227],v2 offset:4608
v_mfma_f32_16x16x32_bf16 a[124:127],a[252:255],v[244:247],a[124:127]
ds_read_b64_tr_b16 a[228:229],v2 offset:8704
v_mfma_f32_16x16x32_bf16 a[124:127],a[248:251],v[228:231],a[124:127]
ds_read_b64_tr_b16 a[230:231],v2 offset:12800
v_mfma_f32_16x16x32_bf16 v[60:63],v[28:31],v[228:231],v[60:63]
ds_read_b64_tr_b16 a[236:237],v3 offset:8704
v_mfma_f32_16x16x32_bf16 v[60:63],v[28:31],v[244:247],v[60:63]
ds_read_b64_tr_b16 a[238:239],v3 offset:12800
ds_read_b64_tr_b16 a[232:233],v3 offset:512
v_mfma_f32_16x16x32_bf16 v[196:199],a[128:131],v[144:147],v[40:43]
ds_read_b64_tr_b16 a[234:235],v3 offset:4608
v_mfma_f32_16x16x32_bf16 v[196:199],a[132:135],v[176:179],v[196:199]
ds_read_b64_tr_b16 a[240:241],v2 offset:768
v_mfma_f32_16x16x32_bf16 v[196:199],a[136:139],v[208:211],v[196:199]
ds_read_b64_tr_b16 a[242:243],v2 offset:4864
v_mfma_f32_16x16x32_bf16 v[196:199],a[140:143],v[240:243],v[196:199]
ds_read_b64_tr_b16 a[244:245],v2 offset:8960
ds_read_b64_tr_b16 a[246:247],v2 offset:13056
v_mfma_f32_16x16x32_bf16 v[204:207],a[156:159],v[240:243],v[40:43]
ds_read_b64_tr_b16 a[252:253],v3 offset:8960
v_add_u32 v2,s48,v2
v_mfma_f32_16x16x32_bf16 v[204:207],a[152:155],v[208:211],v[204:207]
ds_read_b64_tr_b16 a[254:255],v3 offset:13056
v_mfma_f32_16x16x32_bf16 v[204:207],a[148:151],v[176:179],v[204:207]
buffer_load_dwordx4 v9,s[72:75],s83 offen offset:2048 lds
ds_read_b64_tr_b16 a[248:249],v3 offset:768
v_mfma_f32_16x16x32_bf16 v[204:207],a[144:147],v[144:147],v[204:207]
ds_read_b64_tr_b16 a[250:251],v3 offset:4864
v_add_u32 v3,s48,v3
v_mfma_f32_16x16x32_bf16 v[212:215],a[160:163],v[144:147],v[40:43]
s_sub_u32 s48,32768,s62
v_exp_f32 v174,v174
v_mfma_f32_16x16x32_bf16 v[212:215],a[164:167],v[176:179],v[212:215]
s_add_u32 s50,s50,s48
v_exp_f32 v175,v175
v_mfma_f32_16x16x32_bf16 v[212:215],a[168:171],v[208:211],v[212:215]
v_exp_f32 v180,v180
v_mfma_f32_16x16x32_bf16 v[212:215],a[172:175],v[240:243],v[212:215]
v_exp_f32 v181,v181
v_mfma_f32_16x16x32_bf16 v[220:223],a[188:191],v[240:243],v[40:43]
v_exp_f32 v182,v182
v_mfma_f32_16x16x32_bf16 v[220:223],a[184:187],v[208:211],v[220:223]
v_exp_f32 v183,v183
v_mfma_f32_16x16x32_bf16 v[220:223],a[180:183],v[176:179],v[220:223]
v_cvt_pk_bf16_f32 v167,v174,v175
v_cvt_pk_bf16_f32 v180,v180,v181
v_mfma_f32_16x16x32_bf16 v[220:223],a[176:179],v[144:147],v[220:223]
s_add_u32 s80,s71,0
s_and_b32 s80,s80,3
s_lshl_b32 s80,s80,1
s_waitcnt lgkmcnt(0)
v_mfma_f32_16x16x32_bf16 a[0:3],a[192:195],v[132:135],a[0:3]
s_or_b32 s80,s80,1
s_mul_i32 s80,s80,163840
s_add_u32 s80,s80,s81
v_mfma_f32_16x16x32_bf16 a[0:3],a[196:199],v[148:151],a[0:3]
v_exp_f32 v188,v188
v_mfma_f32_16x16x32_bf16 a[4:7],a[204:207],v[148:151],a[4:7]
buffer_load_dwordx4 v10,s[72:75],s83 offen offset:3072 lds
v_cvt_pk_bf16_f32 v181,v182,v183
v_mfma_f32_16x16x32_bf16 a[4:7],a[200:203],v[132:135],a[4:7]
v_exp_f32 v189,v189
v_mfma_f32_16x16x32_bf16 a[8:11],a[208:211],v[132:135],a[8:11]
v_exp_f32 v190,v190
v_mfma_f32_16x16x32_bf16 a[8:11],a[212:215],v[148:151],a[8:11]
v_exp_f32 v191,v191
v_mfma_f32_16x16x32_bf16 a[12:15],a[220:223],v[148:151],a[12:15]
v_cvt_pk_bf16_f32 v182,v188,v189
v_cvt_pk_bf16_f32 v183,v190,v191
v_mfma_f32_16x16x32_bf16 a[12:15],a[216:219],v[132:135],a[12:15]
v_exp_f32 v196,v196
v_mfma_f32_16x16x32_bf16 a[16:19],a[224:227],v[132:135],a[16:19]
v_exp_f32 v197,v197
v_mfma_f32_16x16x32_bf16 a[16:19],a[228:231],v[148:151],a[16:19]
v_exp_f32 v198,v198
v_mfma_f32_16x16x32_bf16 a[20:23],a[236:239],v[148:151],a[20:23]
v_exp_f32 v199,v199
v_mfma_f32_16x16x32_bf16 a[20:23],a[232:235],v[132:135],a[20:23]
s_add_u32 m0,s82,50176
v_cvt_pk_bf16_f32 v196,v196,v197
v_cvt_pk_bf16_f32 v197,v198,v199
v_mfma_f32_16x16x32_bf16 a[24:27],a[240:243],v[132:135],a[24:27]
v_exp_f32 v204,v204
v_mfma_f32_16x16x32_bf16 a[24:27],a[244:247],v[148:151],a[24:27]
v_exp_f32 v205,v205
v_mfma_f32_16x16x32_bf16 a[28:31],a[252:255],v[148:151],a[28:31]
buffer_load_dwordx4 v11,s[72:75],s83 offen lds
v_cvt_pk_bf16_f32 v198,v204,v205
v_mfma_f32_16x16x32_bf16 a[28:31],a[248:251],v[132:135],a[28:31]
s_add_u32 s83,s80,20480
v_exp_f32 v206,v206
v_mfma_f32_16x16x32_bf16 v[48:51],v[28:31],v[132:135],v[48:51]
v_exp_f32 v207,v207
v_mfma_f32_16x16x32_bf16 v[48:51],v[28:31],v[148:151],v[48:51]
v_exp_f32 v212,v212
s_barrier
v_mfma_f32_16x16x32_bf16 v[228:231],a[128:131],v[152:155],v[44:47]
ds_read_b128 a[128:131],v1
v_cvt_pk_bf16_f32 v199,v206,v207
v_mfma_f32_16x16x32_bf16 v[228:231],a[132:135],v[184:187],v[228:231]
s_add_u32 m0,s82,81920
v_exp_f32 v213,v213
v_mfma_f32_16x16x32_bf16 v[228:231],a[136:139],v[216:219],v[228:231]
ds_read_b128 a[132:135],v1 offset:256
v_cvt_pk_bf16_f32 v212,v212,v213
v_mfma_f32_16x16x32_bf16 v[228:231],a[140:143],v[248:251],v[228:231]
v_exp_f32 v214,v214
v_mfma_f32_16x16x32_bf16 v[236:239],a[156:159],v[248:251],v[44:47]
buffer_load_dwordx4 v7,s[72:75],s80 offen lds
ds_read_b128 a[136:139],v1 offset:512
v_mfma_f32_16x16x32_bf16 v[236:239],a[152:155],v[216:219],v[236:239]
v_exp_f32 v215,v215
v_mfma_f32_16x16x32_bf16 v[236:239],a[148:151],v[184:187],v[236:239]
ds_read_b128 a[140:143],v1 offset:768
v_cvt_pk_bf16_f32 v213,v214,v215
v_mfma_f32_16x16x32_bf16 v[236:239],a[144:147],v[152:155],v[236:239]
v_exp_f32 v220,v220
v_mfma_f32_16x16x32_bf16 v[244:247],a[160:163],v[152:155],v[44:47]
ds_read_b128 a[156:159],v1 offset:4864
v_mfma_f32_16x16x32_bf16 v[244:247],a[164:167],v[184:187],v[244:247]
v_exp_f32 v221,v221
v_mfma_f32_16x16x32_bf16 v[244:247],a[168:171],v[216:219],v[244:247]
ds_read_b128 a[152:155],v1 offset:4608
v_cvt_pk_bf16_f32 v214,v220,v221
v_mfma_f32_16x16x32_bf16 v[244:247],a[172:175],v[248:251],v[244:247]
v_exp_f32 v222,v222
v_mfma_f32_16x16x32_bf16 v[252:255],a[188:191],v[248:251],v[44:47]
buffer_load_dwordx4 v8,s[72:75],s80 offen offset:1024 lds
ds_read_b128 a[148:151],v1 offset:4352
v_mfma_f32_16x16x32_bf16 v[252:255],a[184:187],v[216:219],v[252:255]
v_exp_f32 v223,v223
v_mfma_f32_16x16x32_bf16 v[252:255],a[180:183],v[184:187],v[252:255]
ds_read_b128 a[144:147],v1 offset:4096
v_cvt_pk_bf16_f32 v215,v222,v223
v_mfma_f32_16x16x32_bf16 v[252:255],a[176:179],v[152:155],v[252:255]
v_exp_f32 v228,v228
v_mfma_f32_16x16x32_bf16 a[32:35],a[192:195],v[164:167],a[32:35]
ds_read_b128 a[160:163],v1 offset:8192
v_mfma_f32_16x16x32_bf16 a[32:35],a[196:199],v[180:183],a[32:35]
v_exp_f32 v229,v229
v_mfma_f32_16x16x32_bf16 a[36:39],a[204:207],v[180:183],a[36:39]
ds_read_b128 a[164:167],v1 offset:8448
v_cvt_pk_bf16_f32 v228,v228,v229
v_mfma_f32_16x16x32_bf16 a[36:39],a[200:203],v[164:167],a[36:39]
v_exp_f32 v230,v230
v_mfma_f32_16x16x32_bf16 a[40:43],a[208:211],v[164:167],a[40:43]
buffer_load_dwordx4 v9,s[72:75],s80 offen offset:2048 lds
ds_read_b128 a[168:171],v1 offset:8704
v_mfma_f32_16x16x32_bf16 a[40:43],a[212:215],v[180:183],a[40:43]
v_exp_f32 v231,v231
v_mfma_f32_16x16x32_bf16 a[44:47],a[220:223],v[180:183],a[44:47]
ds_read_b128 a[172:175],v1 offset:8960
v_cvt_pk_bf16_f32 v229,v230,v231
v_mfma_f32_16x16x32_bf16 a[44:47],a[216:219],v[164:167],a[44:47]
v_exp_f32 v236,v236
v_mfma_f32_16x16x32_bf16 a[48:51],a[224:227],v[164:167],a[48:51]
ds_read_b128 a[188:191],v1 offset:13056
v_mfma_f32_16x16x32_bf16 a[48:51],a[228:231],v[180:183],a[48:51]
v_exp_f32 v237,v237
v_mfma_f32_16x16x32_bf16 a[52:55],a[236:239],v[180:183],a[52:55]
ds_read_b128 a[184:187],v1 offset:12800
v_cvt_pk_bf16_f32 v230,v236,v237
v_mfma_f32_16x16x32_bf16 a[52:55],a[232:235],v[164:167],a[52:55]
v_exp_f32 v238,v238
v_mfma_f32_16x16x32_bf16 a[56:59],a[240:243],v[164:167],a[56:59]
buffer_load_dwordx4 v10,s[72:75],s80 offen offset:3072 lds
ds_read_b128 a[180:183],v1 offset:12544
v_mfma_f32_16x16x32_bf16 a[56:59],a[244:247],v[180:183],a[56:59]
v_exp_f32 v239,v239
v_mfma_f32_16x16x32_bf16 a[60:63],a[252:255],v[180:183],a[60:63]
ds_read_b128 a[176:179],v1 offset:12288
v_cvt_pk_bf16_f32 v231,v238,v239
v_mfma_f32_16x16x32_bf16 a[60:63],a[248:251],v[164:167],a[60:63]
v_exp_f32 v244,v244
v_mfma_f32_16x16x32_bf16 v[52:55],v[28:31],v[164:167],v[52:55]
v_exp_f32 v245,v245
v_mfma_f32_16x16x32_bf16 v[52:55],v[28:31],v[180:183],v[52:55]
s_add_u32 m0,s82,86016
v_exp_f32 v246,v246
s_waitcnt lgkmcnt(3)
v_mfma_f32_16x16x32_bf16 v[132:135],a[128:131],v[128:131],v[32:35]
v_exp_f32 v247,v247
v_mfma_f32_16x16x32_bf16 v[132:135],a[132:135],v[160:163],v[132:135]
v_cvt_pk_bf16_f32 v244,v244,v245
v_cvt_pk_bf16_f32 v245,v246,v247
v_mfma_f32_16x16x32_bf16 v[132:135],a[136:139],v[192:195],v[132:135]
buffer_load_dwordx4 v11,s[72:75],s80 offen lds
v_add_u32 v1,s48,v1
v_mfma_f32_16x16x32_bf16 v[132:135],a[140:143],v[224:227],v[132:135]
v_exp_f32 v252,v252
v_mfma_f32_16x16x32_bf16 v[140:143],a[156:159],v[224:227],v[32:35]
v_exp_f32 v253,v253
v_mfma_f32_16x16x32_bf16 v[140:143],a[152:155],v[192:195],v[140:143]
v_exp_f32 v254,v254
v_mfma_f32_16x16x32_bf16 v[140:143],a[148:151],v[160:163],v[140:143]
v_exp_f32 v255,v255
v_mfma_f32_16x16x32_bf16 v[140:143],a[144:147],v[128:131],v[140:143]
s_add_u32 m0,s82,87040
v_cvt_pk_bf16_f32 v246,v252,v253
v_cvt_pk_bf16_f32 v247,v254,v255
v_mfma_f32_16x16x32_bf16 v[148:151],a[160:163],v[128:131],v[32:35]
v_add_u32 v1,v18,v19
v_mfma_f32_16x16x32_bf16 v[148:151],a[164:167],v[160:163],v[148:151]
v_exp_f32 v132,v132
v_mfma_f32_16x16x32_bf16 v[148:151],a[168:171],v[192:195],v[148:151]
buffer_load_dwordx4 v7,s[72:75],s83 offen lds
v_mfma_f32_16x16x32_bf16 v[148:151],a[172:175],v[224:227],v[148:151]
v_exp_f32 v133,v133
v_mfma_f32_16x16x32_bf16 v[156:159],a[188:191],v[224:227],v[32:35]
s_waitcnt lgkmcnt(0)
v_exp_f32 v134,v134
v_mfma_f32_16x16x32_bf16 v[156:159],a[184:187],v[192:195],v[156:159]
v_exp_f32 v135,v135
v_mfma_f32_16x16x32_bf16 v[156:159],a[180:183],v[160:163],v[156:159]
v_cvt_pk_bf16_f32 v132,v132,v133
v_cvt_pk_bf16_f32 v133,v134,v135
v_mfma_f32_16x16x32_bf16 v[156:159],a[176:179],v[128:131],v[156:159]
v_exp_f32 v140,v140
v_mfma_f32_16x16x32_bf16 a[64:67],a[192:195],v[196:199],a[64:67]
s_lshl_b32 s94,s70,4
v_bfe_u32 v24,v6,4,2
v_min_u32 v24,2,v24
v_mfma_f32_16x16x32_bf16 a[64:67],a[196:199],v[212:215],a[64:67]
)A"
R"A(s_add_u32 s94,s94,s40
v_add_u32 v24,s71,v24
v_add_u32 v24,1,v24
v_mfma_f32_16x16x32_bf16 a[68:71],a[204:207],v[212:215],a[68:71]
s_lshl_b32 s94,s94,4
buffer_load_dwordx4 v8,s[72:75],s83 offen offset:1024 lds
v_and_b32 v24,3,v24
v_mfma_f32_16x16x32_bf16 a[68:71],a[200:203],v[196:199],a[68:71]
v_lshlrev_b32 v24,6,v24
v_mfma_f32_16x16x32_bf16 a[72:75],a[208:211],v[196:199],a[72:75]
v_exp_f32 v141,v141
v_mfma_f32_16x16x32_bf16 a[72:75],a[212:215],v[212:215],a[72:75]
v_exp_f32 v142,v142
v_mfma_f32_16x16x32_bf16 a[76:79],a[220:223],v[212:215],a[76:79]
v_exp_f32 v143,v143
v_mfma_f32_16x16x32_bf16 a[76:79],a[216:219],v[196:199],a[76:79]
v_exp_f32 v148,v148
v_mfma_f32_16x16x32_bf16 a[80:83],a[224:227],v[196:199],a[80:83]
v_exp_f32 v149,v149
v_mfma_f32_16x16x32_bf16 a[80:83],a[228:231],v[212:215],a[80:83]
v_exp_f32 v150,v150
v_mfma_f32_16x16x32_bf16 a[84:87],a[236:239],v[212:215],a[84:87]
buffer_load_dwordx4 v9,s[72:75],s83 offen offset:2048 lds
v_cvt_pk_bf16_f32 v134,v140,v141
v_mfma_f32_16x16x32_bf16 a[84:87],a[232:235],v[196:199],a[84:87]
v_exp_f32 v151,v151
v_mfma_f32_16x16x32_bf16 a[88:91],a[240:243],v[196:199],a[88:91]
v_cvt_pk_bf16_f32 v135,v142,v143
v_cvt_pk_bf16_f32 v148,v148,v149
v_mfma_f32_16x16x32_bf16 a[88:91],a[244:247],v[212:215],a[88:91]
v_exp_f32 v156,v156
v_mfma_f32_16x16x32_bf16 a[92:95],a[252:255],v[212:215],a[92:95]
v_exp_f32 v157,v157
v_mfma_f32_16x16x32_bf16 a[92:95],a[248:251],v[196:199],a[92:95]
v_exp_f32 v158,v158
v_mfma_f32_16x16x32_bf16 v[56:59],v[28:31],v[196:199],v[56:59]
v_exp_f32 v159,v159
v_mfma_f32_16x16x32_bf16 v[56:59],v[28:31],v[212:215],v[56:59]
v_cvt_pk_bf16_f32 v149,v150,v151
v_cvt_pk_bf16_f32 v150,v156,v157
v_mfma_f32_16x16x32_bf16 v[164:167],a[128:131],v[136:139],v[36:39]
buffer_load_dwordx4 v10,s[72:75],s83 offen offset:3072 lds
v_cvt_pk_bf16_f32 v151,v158,v159
v_mfma_f32_16x16x32_bf16 v[164:167],a[132:135],v[168:171],v[164:167]
v_mfma_f32_16x16x32_bf16 v[164:167],a[136:139],v[200:203],v[164:167]
v_mfma_f32_16x16x32_bf16 v[164:167],a[140:143],v[232:235],v[164:167]
v_mfma_f32_16x16x32_bf16 v[172:175],a[156:159],v[232:235],v[36:39]
v_mfma_f32_16x16x32_bf16 v[172:175],a[152:155],v[200:203],v[172:175]
s_add_u32 m0,s82,91136
v_mfma_f32_16x16x32_bf16 v[172:175],a[148:151],v[168:171],v[172:175]
v_mfma_f32_16x16x32_bf16 v[172:175],a[144:147],v[136:139],v[172:175]
v_mfma_f32_16x16x32_bf16 v[180:183],a[160:163],v[136:139],v[36:39]
buffer_load_dwordx4 v11,s[72:75],s83 offen lds
v_mfma_f32_16x16x32_bf16 v[180:183],a[164:167],v[168:171],v[180:183]
v_exp_f32 v164,v164
v_mfma_f32_16x16x32_bf16 v[180:183],a[168:171],v[200:203],v[180:183]
v_exp_f32 v165,v165
v_mfma_f32_16x16x32_bf16 v[180:183],a[172:175],v[232:235],v[180:183]
v_exp_f32 v166,v166
v_mfma_f32_16x16x32_bf16 v[188:191],a[188:191],v[232:235],v[36:39]
v_exp_f32 v167,v167
v_mfma_f32_16x16x32_bf16 v[188:191],a[184:187],v[200:203],v[188:191]
v_cvt_pk_bf16_f32 v164,v164,v165
v_cvt_pk_bf16_f32 v165,v166,v167
v_mfma_f32_16x16x32_bf16 v[188:191],a[180:183],v[168:171],v[188:191]
v_exp_f32 v172,v172
v_mfma_f32_16x16x32_bf16 v[188:191],a[176:179],v[136:139],v[188:191]
v_exp_f32 v173,v173
v_mfma_f32_16x16x32_bf16 a[96:99],a[192:195],v[228:231],a[96:99]
ds_read_b64_tr_b16 a[192:193],v2
ds_read_b64_tr_b16 a[194:195],v2 offset:4096
v_mfma_f32_16x16x32_bf16 a[96:99],a[196:199],v[244:247],a[96:99]
ds_read_b64_tr_b16 a[196:197],v2 offset:8192
v_cvt_pk_bf16_f32 v166,v172,v173
v_mfma_f32_16x16x32_bf16 a[100:103],a[204:207],v[244:247],a[100:103]
ds_read_b64_tr_b16 a[198:199],v2 offset:12288
v_mfma_f32_16x16x32_bf16 a[100:103],a[200:203],v[228:231],a[100:103]
ds_read_b64_tr_b16 a[204:205],v3 offset:8192
v_mfma_f32_16x16x32_bf16 a[104:107],a[208:211],v[228:231],a[104:107]
ds_read_b64_tr_b16 a[206:207],v3 offset:12288
ds_read_b64_tr_b16 a[200:201],v3
v_mfma_f32_16x16x32_bf16 a[104:107],a[212:215],v[244:247],a[104:107]
ds_read_b64_tr_b16 a[202:203],v3 offset:4096
v_mfma_f32_16x16x32_bf16 a[108:111],a[220:223],v[244:247],a[108:111]
buffer_load_dwordx2 v[26:27],v24,s[64:67],s94 offen sc1
ds_read_b64_tr_b16 a[208:209],v2 offset:256
v_mfma_f32_16x16x32_bf16 a[108:111],a[216:219],v[228:231],a[108:111]
ds_read_b64_tr_b16 a[210:211],v2 offset:4352
v_mfma_f32_16x16x32_bf16 a[112:115],a[224:227],v[228:231],a[112:115]
ds_read_b64_tr_b16 a[212:213],v2 offset:8448
ds_read_b64_tr_b16 a[214:215],v2 offset:12544
v_mfma_f32_16x16x32_bf16 a[112:115],a[228:231],v[244:247],a[112:115]
ds_read_b64_tr_b16 a[220:221],v3 offset:8448
v_mfma_f32_16x16x32_bf16 a[116:119],a[236:239],v[244:247],a[116:119]
ds_read_b64_tr_b16 a[222:223],v3 offset:12544
v_mfma_f32_16x16x32_bf16 a[116:119],a[232:235],v[228:231],a[116:119]
ds_read_b64_tr_b16 a[216:217],v3 offset:256
v_mfma_f32_16x16x32_bf16 a[120:123],a[240:243],v[228:231],a[120:123]
ds_read_b64_tr_b16 a[218:219],v3 offset:4352
ds_read_b64_tr_b16 a[224:225],v2 offset:512
v_mfma_f32_16x16x32_bf16 a[120:123],a[244:247],v[244:247],a[120:123]
ds_read_b64_tr_b16 a[226:227],v2 offset:4608
v_mfma_f32_16x16x32_bf16 a[124:127],a[252:255],v[244:247],a[124:127]
ds_read_b64_tr_b16 a[228:229],v2 offset:8704
v_mfma_f32_16x16x32_bf16 a[124:127],a[248:251],v[228:231],a[124:127]
ds_read_b64_tr_b16 a[230:231],v2 offset:12800
v_mfma_f32_16x16x32_bf16 v[60:63],v[28:31],v[228:231],v[60:63]
ds_read_b64_tr_b16 a[236:237],v3 offset:8704
ds_read_b64_tr_b16 a[238:239],v3 offset:12800
v_mfma_f32_16x16x32_bf16 v[60:63],v[28:31],v[244:247],v[60:63]
ds_read_b64_tr_b16 a[232:233],v3 offset:512
v_mfma_f32_16x16x32_bf16 v[196:199],a[128:131],v[144:147],v[40:43]
ds_read_b64_tr_b16 a[234:235],v3 offset:4608
v_mfma_f32_16x16x32_bf16 v[196:199],a[132:135],v[176:179],v[196:199]
ds_read_b64_tr_b16 a[240:241],v2 offset:768
v_mfma_f32_16x16x32_bf16 v[196:199],a[136:139],v[208:211],v[196:199]
ds_read_b64_tr_b16 a[242:243],v2 offset:4864
ds_read_b64_tr_b16 a[244:245],v2 offset:8960
v_mfma_f32_16x16x32_bf16 v[196:199],a[140:143],v[240:243],v[196:199]
ds_read_b64_tr_b16 a[246:247],v2 offset:13056
v_sub_u32 v2,v18,v19
v_mfma_f32_16x16x32_bf16 v[204:207],a[156:159],v[240:243],v[40:43]
ds_read_b64_tr_b16 a[252:253],v3 offset:8960
v_mfma_f32_16x16x32_bf16 v[204:207],a[152:155],v[208:211],v[204:207]
ds_read_b64_tr_b16 a[254:255],v3 offset:13056
v_mfma_f32_16x16x32_bf16 v[204:207],a[148:151],v[176:179],v[204:207]
ds_read_b64_tr_b16 a[248:249],v3 offset:768
ds_read_b64_tr_b16 a[250:251],v3 offset:4864
v_mfma_f32_16x16x32_bf16 v[204:207],a[144:147],v[144:147],v[204:207]
v_exp_f32 v174,v174
v_mfma_f32_16x16x32_bf16 v[212:215],a[160:163],v[144:147],v[40:43]
v_exp_f32 v175,v175
v_mfma_f32_16x16x32_bf16 v[212:215],a[164:167],v[176:179],v[212:215]
v_exp_f32 v180,v180
v_mfma_f32_16x16x32_bf16 v[212:215],a[168:171],v[208:211],v[212:215]
v_exp_f32 v181,v181
v_mfma_f32_16x16x32_bf16 v[212:215],a[172:175],v[240:243],v[212:215]
v_exp_f32 v182,v182
v_mfma_f32_16x16x32_bf16 v[220:223],a[188:191],v[240:243],v[40:43]
v_exp_f32 v183,v183
v_mfma_f32_16x16x32_bf16 v[220:223],a[184:187],v[208:211],v[220:223]
v_cvt_pk_bf16_f32 v167,v174,v175
v_cvt_pk_bf16_f32 v180,v180,v181
v_mfma_f32_16x16x32_bf16 v[220:223],a[180:183],v[176:179],v[220:223]
v_exp_f32 v188,v188
v_mfma_f32_16x16x32_bf16 v[220:223],a[176:179],v[144:147],v[220:223]
s_waitcnt lgkmcnt(0)
v_exp_f32 v189,v189
v_mfma_f32_16x16x32_bf16 a[0:3],a[192:195],v[132:135],a[0:3]
v_exp_f32 v190,v190
v_mfma_f32_16x16x32_bf16 a[0:3],a[196:199],v[148:151],a[0:3]
v_exp_f32 v191,v191
v_mfma_f32_16x16x32_bf16 a[4:7],a[204:207],v[148:151],a[4:7]
v_cvt_pk_bf16_f32 v181,v182,v183
v_cvt_pk_bf16_f32 v182,v188,v189
v_mfma_f32_16x16x32_bf16 a[4:7],a[200:203],v[132:135],a[4:7]
s_cmp_ge_u32 0,s71
s_cselect_b32 s76,2,0
s_lshl_b32 s76,s76,16
v_mfma_f32_16x16x32_bf16 a[8:11],a[208:211],v[132:135],a[8:11]
s_add_u32 s77,s76,4096
s_add_u32 s78,s76,8192
v_cvt_pk_bf16_f32 v183,v190,v191
v_mfma_f32_16x16x32_bf16 a[8:11],a[212:215],v[148:151],a[8:11]
s_add_u32 s79,s76,12288
v_exp_f32 v196,v196
v_mfma_f32_16x16x32_bf16 a[12:15],a[220:223],v[148:151],a[12:15]
v_exp_f32 v197,v197
v_mfma_f32_16x16x32_bf16 a[12:15],a[216:219],v[132:135],a[12:15]
v_exp_f32 v198,v198
v_mfma_f32_16x16x32_bf16 a[16:19],a[224:227],v[132:135],a[16:19]
v_exp_f32 v199,v199
v_mfma_f32_16x16x32_bf16 a[16:19],a[228:231],v[148:151],a[16:19]
v_cvt_pk_bf16_f32 v196,v196,v197
v_cvt_pk_bf16_f32 v197,v198,v199
v_mfma_f32_16x16x32_bf16 a[20:23],a[236:239],v[148:151],a[20:23]
v_exp_f32 v204,v204
v_mfma_f32_16x16x32_bf16 a[20:23],a[232:235],v[132:135],a[20:23]
v_exp_f32 v205,v205
v_mfma_f32_16x16x32_bf16 a[24:27],a[240:243],v[132:135],a[24:27]
v_exp_f32 v206,v206
v_mfma_f32_16x16x32_bf16 a[24:27],a[244:247],v[148:151],a[24:27]
v_exp_f32 v207,v207
v_mfma_f32_16x16x32_bf16 a[28:31],a[252:255],v[148:151],a[28:31]
v_exp_f32 v212,v212
v_mfma_f32_16x16x32_bf16 a[28:31],a[248:251],v[132:135],a[28:31]
v_exp_f32 v213,v213
v_mfma_f32_16x16x32_bf16 v[48:51],v[28:31],v[132:135],v[48:51]
v_exp_f32 v214,v214
v_mfma_f32_16x16x32_bf16 v[48:51],v[28:31],v[148:151],v[48:51]
v_exp_f32 v215,v215
s_waitcnt vmcnt(11)
s_barrier
v_mfma_f32_16x16x32_bf16 v[228:231],a[128:131],v[152:155],v[44:47]
s_waitcnt vmcnt(0)
v_cmp_ne_u32 s[90:91],s68,v26
v_cmp_ne_u32 s[92:93],s69,v27
s_nop 0
s_or_b64 s[90:91],s[90:91],s[92:93]
s_cbranch_scc1 L_fr1_%=
L_fo1_%=:
v_mfma_f32_16x16x32_bf16 v[228:231],a[132:135],v[184:187],v[228:231]
v_cvt_pk_bf16_f32 v198,v204,v205
v_cvt_pk_bf16_f32 v199,v206,v207
v_mfma_f32_16x16x32_bf16 v[228:231],a[136:139],v[216:219],v[228:231]
v_cvt_pk_bf16_f32 v212,v212,v213
v_cvt_pk_bf16_f32 v213,v214,v215
v_mfma_f32_16x16x32_bf16 v[228:231],a[140:143],v[248:251],v[228:231]
v_exp_f32 v220,v220
v_mfma_f32_16x16x32_bf16 v[236:239],a[156:159],v[248:251],v[44:47]
v_exp_f32 v221,v221
v_mfma_f32_16x16x32_bf16 v[236:239],a[152:155],v[216:219],v[236:239]
v_exp_f32 v222,v222
v_mfma_f32_16x16x32_bf16 v[236:239],a[148:151],v[184:187],v[236:239]
v_exp_f32 v223,v223
v_mfma_f32_16x16x32_bf16 v[236:239],a[144:147],v[152:155],v[236:239]
v_cvt_pk_bf16_f32 v214,v220,v221
v_cvt_pk_bf16_f32 v215,v222,v223
v_mfma_f32_16x16x32_bf16 v[244:247],a[160:163],v[152:155],v[44:47]
buffer_load_dwordx4 v[128:131],v6,s[28:31],s76 offen sc1
v_max_f32 v20,0x00800000,v48
v_mfma_f32_16x16x32_bf16 v[244:247],a[164:167],v[184:187],v[244:247]
v_rcp_f32 v20,v20
v_mfma_f32_16x16x32_bf16 v[244:247],a[168:171],v[216:219],v[244:247]
v_accvgpr_read_b32 v132,a0
v_accvgpr_read_b32 v133,a1
v_mfma_f32_16x16x32_bf16 v[244:247],a[172:175],v[248:251],v[244:247]
v_mul_f32 v132,v20,v132
v_mul_f32 v133,v20,v133
v_mfma_f32_16x16x32_bf16 v[252:255],a[188:191],v[248:251],v[44:47]
v_accvgpr_read_b32 v134,a2
v_accvgpr_read_b32 v135,a3
v_mfma_f32_16x16x32_bf16 v[252:255],a[184:187],v[216:219],v[252:255]
v_cvt_pk_bf16_f32 v132,v132,v133
v_mul_f32 v134,v20,v134
v_mfma_f32_16x16x32_bf16 v[252:255],a[180:183],v[184:187],v[252:255]
v_mul_f32 v135,v20,v135
v_cvt_pk_bf16_f32 v133,v134,v135
v_mfma_f32_16x16x32_bf16 v[252:255],a[176:179],v[152:155],v[252:255]
v_exp_f32 v228,v228
v_mfma_f32_16x16x32_bf16 a[32:35],a[192:195],v[164:167],a[32:35]
v_exp_f32 v229,v229
v_mfma_f32_16x16x32_bf16 a[32:35],a[196:199],v[180:183],a[32:35]
buffer_load_dwordx4 v[160:163],v6,s[28:31],s77 offen sc1
v_accvgpr_read_b32 v134,a4
v_mfma_f32_16x16x32_bf16 a[36:39],a[204:207],v[180:183],a[36:39]
v_accvgpr_read_b32 v135,a5
v_accvgpr_read_b32 v16,a6
v_mfma_f32_16x16x32_bf16 a[36:39],a[200:203],v[164:167],a[36:39]
v_exp_f32 v230,v230
v_mfma_f32_16x16x32_bf16 a[40:43],a[208:211],v[164:167],a[40:43]
v_exp_f32 v231,v231
v_mfma_f32_16x16x32_bf16 a[40:43],a[212:215],v[180:183],a[40:43]
v_accvgpr_read_b32 v17,a7
v_mul_f32 v134,v20,v134
v_mfma_f32_16x16x32_bf16 a[44:47],a[220:223],v[180:183],a[44:47]
v_mul_f32 v135,v20,v135
v_cvt_pk_bf16_f32 v228,v228,v229
v_mfma_f32_16x16x32_bf16 a[44:47],a[216:219],v[164:167],a[44:47]
v_exp_f32 v236,v236
v_mfma_f32_16x16x32_bf16 a[48:51],a[224:227],v[164:167],a[48:51]
v_exp_f32 v237,v237
v_mfma_f32_16x16x32_bf16 a[48:51],a[228:231],v[180:183],a[48:51]
v_exp_f32 v238,v238
v_mfma_f32_16x16x32_bf16 a[52:55],a[236:239],v[180:183],a[52:55]
buffer_load_dwordx4 v[192:195],v6,s[28:31],s78 offen sc1
)A"
R"A(v_mul_f32 v16,v20,v16
v_mfma_f32_16x16x32_bf16 a[52:55],a[232:235],v[164:167],a[52:55]
v_exp_f32 v239,v239
v_mfma_f32_16x16x32_bf16 a[56:59],a[240:243],v[164:167],a[56:59]
v_exp_f32 v244,v244
v_mfma_f32_16x16x32_bf16 a[56:59],a[244:247],v[180:183],a[56:59]
v_exp_f32 v245,v245
v_mfma_f32_16x16x32_bf16 a[60:63],a[252:255],v[180:183],a[60:63]
v_mul_f32 v17,v20,v17
v_cvt_pk_bf16_f32 v134,v134,v135
v_mfma_f32_16x16x32_bf16 a[60:63],a[248:251],v[164:167],a[60:63]
v_cvt_pk_bf16_f32 v229,v230,v231
v_cvt_pk_bf16_f32 v135,v16,v17
v_mfma_f32_16x16x32_bf16 v[52:55],v[28:31],v[164:167],v[52:55]
buffer_store_dwordx4 v[132:135],v6,s[36:39],s84 offen sc1
v_cvt_pk_bf16_f32 v230,v236,v237
v_mfma_f32_16x16x32_bf16 v[52:55],v[28:31],v[180:183],v[52:55]
v_exp_f32 v246,v246
v_mfma_f32_16x16x32_bf16 a[64:67],a[192:195],v[196:199],a[64:67]
v_exp_f32 v247,v247
v_mfma_f32_16x16x32_bf16 a[64:67],a[196:199],v[212:215],a[64:67]
v_cvt_pk_bf16_f32 v231,v238,v239
v_cvt_pk_bf16_f32 v244,v244,v245
v_mfma_f32_16x16x32_bf16 a[68:71],a[204:207],v[212:215],a[68:71]
buffer_load_dwordx4 v[224:227],v6,s[28:31],s79 offen sc1
v_cvt_pk_bf16_f32 v245,v246,v247
v_mfma_f32_16x16x32_bf16 a[68:71],a[200:203],v[196:199],a[68:71]
v_exp_f32 v252,v252
v_mfma_f32_16x16x32_bf16 a[72:75],a[208:211],v[196:199],a[72:75]
v_exp_f32 v253,v253
v_mfma_f32_16x16x32_bf16 a[72:75],a[212:215],v[212:215],a[72:75]
v_exp_f32 v254,v254
v_mfma_f32_16x16x32_bf16 a[76:79],a[220:223],v[212:215],a[76:79]
v_exp_f32 v255,v255
v_mfma_f32_16x16x32_bf16 a[76:79],a[216:219],v[196:199],a[76:79]
v_cvt_pk_bf16_f32 v246,v252,v253
v_cvt_pk_bf16_f32 v247,v254,v255
v_mfma_f32_16x16x32_bf16 a[80:83],a[224:227],v[196:199],a[80:83]
v_accvgpr_read_b32 v140,a8
v_accvgpr_read_b32 v141,a9
v_mfma_f32_16x16x32_bf16 a[80:83],a[228:231],v[212:215],a[80:83]
v_mul_f32 v140,v20,v140
v_mul_f32 v141,v20,v141
v_mfma_f32_16x16x32_bf16 a[84:87],a[236:239],v[212:215],a[84:87]
buffer_load_dwordx4 v[136:139],v6,s[28:31],s76 offen offset:1024 sc1
v_accvgpr_read_b32 v142,a10
v_mfma_f32_16x16x32_bf16 a[84:87],a[232:235],v[196:199],a[84:87]
v_accvgpr_read_b32 v143,a11
v_cvt_pk_bf16_f32 v140,v140,v141
v_mfma_f32_16x16x32_bf16 a[88:91],a[240:243],v[196:199],a[88:91]
v_mul_f32 v142,v20,v142
v_mul_f32 v143,v20,v143
v_mfma_f32_16x16x32_bf16 a[88:91],a[244:247],v[212:215],a[88:91]
v_cvt_pk_bf16_f32 v141,v142,v143
v_accvgpr_read_b32 v142,a12
v_mfma_f32_16x16x32_bf16 a[92:95],a[252:255],v[212:215],a[92:95]
v_accvgpr_read_b32 v143,a13
v_accvgpr_read_b32 v12,a14
v_mfma_f32_16x16x32_bf16 a[92:95],a[248:251],v[196:199],a[92:95]
v_accvgpr_read_b32 v13,a15
v_mul_f32 v142,v20,v142
v_mfma_f32_16x16x32_bf16 v[56:59],v[28:31],v[196:199],v[56:59]
v_mul_f32 v143,v20,v143
v_mul_f32 v12,v20,v12
v_mfma_f32_16x16x32_bf16 v[56:59],v[28:31],v[212:215],v[56:59]
v_mul_f32 v13,v20,v13
v_cvt_pk_bf16_f32 v142,v142,v143
v_mfma_f32_16x16x32_bf16 a[96:99],a[192:195],v[228:231],a[96:99]
ds_read_b64_tr_b16 a[128:129],v1
v_cvt_pk_bf16_f32 v143,v12,v13
v_mfma_f32_16x16x32_bf16 a[96:99],a[196:199],v[244:247],a[96:99]
buffer_store_dwordx4 v[140:143],v6,s[36:39],s85 offen sc1
ds_read_b64_tr_b16 a[130:131],v1 offset:5120
v_mfma_f32_16x16x32_bf16 a[100:103],a[204:207],v[244:247],a[100:103]
ds_read_b64_tr_b16 a[132:133],v1 offset:10240
ds_read_b64_tr_b16 a[134:135],v1 offset:15360
v_mfma_f32_16x16x32_bf16 a[100:103],a[200:203],v[228:231],a[100:103]
ds_read_b64_tr_b16 a[136:137],v1 offset:20480
v_accvgpr_read_b32 v148,a16
v_mfma_f32_16x16x32_bf16 a[104:107],a[208:211],v[228:231],a[104:107]
ds_read_b64_tr_b16 a[138:139],v1 offset:25600
v_accvgpr_read_b32 v149,a17
v_mfma_f32_16x16x32_bf16 a[104:107],a[212:215],v[244:247],a[104:107]
buffer_load_dwordx4 v[168:171],v6,s[28:31],s77 offen offset:1024 sc1
ds_read_b64_tr_b16 a[140:141],v1 offset:30720
v_mfma_f32_16x16x32_bf16 a[108:111],a[220:223],v[244:247],a[108:111]
ds_read_b64_tr_b16 a[142:143],v1 offset:35840
v_mul_f32 v148,v20,v148
v_mfma_f32_16x16x32_bf16 a[108:111],a[216:219],v[228:231],a[108:111]
v_mul_f32 v149,v20,v149
v_accvgpr_read_b32 v150,a18
v_mfma_f32_16x16x32_bf16 a[112:115],a[224:227],v[228:231],a[112:115]
v_accvgpr_read_b32 v151,a19
v_cvt_pk_bf16_f32 v148,v148,v149
v_mfma_f32_16x16x32_bf16 a[112:115],a[228:231],v[244:247],a[112:115]
v_mul_f32 v150,v20,v150
v_mul_f32 v151,v20,v151
v_mfma_f32_16x16x32_bf16 a[116:119],a[236:239],v[244:247],a[116:119]
buffer_load_dwordx4 v[200:203],v6,s[28:31],s78 offen offset:1024 sc1
v_cvt_pk_bf16_f32 v149,v150,v151
v_mfma_f32_16x16x32_bf16 a[116:119],a[232:235],v[228:231],a[116:119]
v_accvgpr_read_b32 v150,a20
v_accvgpr_read_b32 v151,a21
v_mfma_f32_16x16x32_bf16 a[120:123],a[240:243],v[228:231],a[120:123]
v_accvgpr_read_b32 v14,a22
v_accvgpr_read_b32 v15,a23
v_mfma_f32_16x16x32_bf16 a[120:123],a[244:247],v[244:247],a[120:123]
v_mul_f32 v150,v20,v150
v_mul_f32 v151,v20,v151
v_mfma_f32_16x16x32_bf16 a[124:127],a[252:255],v[244:247],a[124:127]
v_mul_f32 v14,v20,v14
v_mul_f32 v15,v20,v15
v_mfma_f32_16x16x32_bf16 a[124:127],a[248:251],v[228:231],a[124:127]
v_cvt_pk_bf16_f32 v150,v150,v151
v_cvt_pk_bf16_f32 v151,v14,v15
v_mfma_f32_16x16x32_bf16 v[60:63],v[28:31],v[228:231],v[60:63]
buffer_store_dwordx4 v[148:151],v6,s[36:39],s86 offen sc1
ds_read_b64_tr_b16 a[144:145],v2 offset:32
v_mfma_f32_16x16x32_bf16 v[60:63],v[28:31],v[244:247],v[60:63]
s_waitcnt lgkmcnt(1)
ds_read_b64_tr_b16 a[146:147],v2 offset:5152
ds_read_b64_tr_b16 a[148:149],v2 offset:10272
v_mfma_f32_16x16x32_bf16 a[192:195],a[128:131],v[64:67],0
ds_read_b64_tr_b16 a[150:151],v2 offset:15392
v_accvgpr_read_b32 v156,a24
v_mfma_f32_16x16x32_bf16 a[192:195],a[132:135],v[68:71],a[192:195]
ds_read_b64_tr_b16 a[152:153],v2 offset:20512
v_accvgpr_read_b32 v157,a25
v_mfma_f32_16x16x32_bf16 a[192:195],a[136:139],v[72:75],a[192:195]
buffer_load_dwordx4 v[232:235],v6,s[28:31],s79 offen offset:1024 sc1
ds_read_b64_tr_b16 a[154:155],v2 offset:25632
v_mfma_f32_16x16x32_bf16 a[192:195],a[140:143],v[76:79],a[192:195]
ds_read_b64_tr_b16 a[156:157],v2 offset:30752
ds_read_b64_tr_b16 a[158:159],v2 offset:35872
v_mfma_f32_16x16x32_bf16 a[196:199],a[140:143],v[92:95],0
v_mul_f32 v156,v20,v156
v_mul_f32 v157,v20,v157
v_mfma_f32_16x16x32_bf16 a[196:199],a[136:139],v[88:91],a[196:199]
v_accvgpr_read_b32 v158,a26
v_accvgpr_read_b32 v159,a27
v_mfma_f32_16x16x32_bf16 a[196:199],a[132:135],v[84:87],a[196:199]
v_cvt_pk_bf16_f32 v156,v156,v157
v_mul_f32 v158,v20,v158
v_mfma_f32_16x16x32_bf16 a[196:199],a[128:131],v[80:83],a[196:199]
v_mul_f32 v159,v20,v159
v_cvt_pk_bf16_f32 v157,v158,v159
v_mfma_f32_16x16x32_bf16 a[200:203],a[128:131],v[96:99],0
v_accvgpr_read_b32 v158,a28
v_accvgpr_read_b32 v159,a29
v_mfma_f32_16x16x32_bf16 a[200:203],a[132:135],v[100:103],a[200:203]
v_accvgpr_read_b32 v16,a30
v_accvgpr_read_b32 v17,a31
v_mfma_f32_16x16x32_bf16 a[200:203],a[136:139],v[104:107],a[200:203]
buffer_load_dwordx4 v[144:147],v6,s[28:31],s76 offen offset:2048 sc1
v_mul_f32 v158,v20,v158
v_mfma_f32_16x16x32_bf16 a[200:203],a[140:143],v[108:111],a[200:203]
v_mul_f32 v159,v20,v159
v_mul_f32 v16,v20,v16
v_mfma_f32_16x16x32_bf16 a[204:207],a[140:143],v[124:127],0
v_mul_f32 v17,v20,v17
v_cvt_pk_bf16_f32 v158,v158,v159
v_mfma_f32_16x16x32_bf16 a[204:207],a[136:139],v[120:123],a[204:207]
v_cvt_pk_bf16_f32 v159,v16,v17
v_max_f32 v21,0x00800000,v52
v_mfma_f32_16x16x32_bf16 a[204:207],a[132:135],v[116:119],a[204:207]
buffer_store_dwordx4 v[156:159],v6,s[36:39],s87 offen sc1
ds_read_b64_tr_b16 a[160:161],v1 offset:64
v_mfma_f32_16x16x32_bf16 a[204:207],a[128:131],v[112:115],a[204:207]
s_waitcnt lgkmcnt(1)
ds_read_b64_tr_b16 a[162:163],v1 offset:5184
ds_read_b64_tr_b16 a[164:165],v1 offset:10304
v_mfma_f32_16x16x32_bf16 a[208:211],a[144:147],v[64:67],0
v_rcp_f32 v21,v21
v_mfma_f32_16x16x32_bf16 a[208:211],a[148:151],v[68:71],a[208:211]
ds_read_b64_tr_b16 a[166:167],v1 offset:15424
v_accvgpr_read_b32 v164,a32
v_mfma_f32_16x16x32_bf16 a[208:211],a[152:155],v[72:75],a[208:211]
v_accvgpr_read_b32 v165,a33
v_mul_f32 v164,v21,v164
v_mfma_f32_16x16x32_bf16 a[208:211],a[156:159],v[76:79],a[208:211]
buffer_load_dwordx4 v[176:179],v6,s[28:31],s77 offen offset:2048 sc1
v_mul_f32 v165,v21,v165
v_mfma_f32_16x16x32_bf16 a[212:215],a[156:159],v[92:95],0
v_accvgpr_read_b32 v166,a34
v_accvgpr_read_b32 v167,a35
v_mfma_f32_16x16x32_bf16 a[212:215],a[152:155],v[88:91],a[212:215]
ds_read_b64_tr_b16 a[168:169],v1 offset:20544
ds_read_b64_tr_b16 a[170:171],v1 offset:25664
v_mfma_f32_16x16x32_bf16 a[212:215],a[148:151],v[84:87],a[212:215]
v_cvt_pk_bf16_f32 v164,v164,v165
v_mul_f32 v166,v21,v166
v_mfma_f32_16x16x32_bf16 a[212:215],a[144:147],v[80:83],a[212:215]
v_mul_f32 v167,v21,v167
v_cvt_pk_bf16_f32 v165,v166,v167
v_mfma_f32_16x16x32_bf16 a[216:219],a[144:147],v[96:99],0
ds_read_b64_tr_b16 a[172:173],v1 offset:30784
ds_read_b64_tr_b16 a[174:175],v1 offset:35904
v_mfma_f32_16x16x32_bf16 a[216:219],a[148:151],v[100:103],a[216:219]
v_accvgpr_read_b32 v166,a36
v_accvgpr_read_b32 v167,a37
v_mfma_f32_16x16x32_bf16 a[216:219],a[152:155],v[104:107],a[216:219]
v_accvgpr_read_b32 v12,a38
v_accvgpr_read_b32 v13,a39
v_mfma_f32_16x16x32_bf16 a[216:219],a[156:159],v[108:111],a[216:219]
v_mul_f32 v166,v21,v166
v_mul_f32 v167,v21,v167
v_mfma_f32_16x16x32_bf16 a[220:223],a[156:159],v[124:127],0
v_mul_f32 v12,v21,v12
v_mul_f32 v13,v21,v13
v_mfma_f32_16x16x32_bf16 a[220:223],a[152:155],v[120:123],a[220:223]
v_cvt_pk_bf16_f32 v166,v166,v167
v_cvt_pk_bf16_f32 v167,v12,v13
v_mfma_f32_16x16x32_bf16 a[220:223],a[148:151],v[116:119],a[220:223]
buffer_store_dwordx4 v[164:167],v6,s[36:39],s84 offen offset:1024 sc1
v_accvgpr_read_b32 v172,a40
v_mfma_f32_16x16x32_bf16 a[220:223],a[144:147],v[112:115],a[220:223]
s_waitcnt lgkmcnt(2)
v_accvgpr_read_b32 v173,a41
v_mul_f32 v172,v21,v172
v_mfma_f32_16x16x32_bf16 a[224:227],a[160:163],v[64:67],0
v_mul_f32 v173,v21,v173
v_accvgpr_read_b32 v174,a42
v_mfma_f32_16x16x32_bf16 a[224:227],a[164:167],v[68:71],a[224:227]
v_accvgpr_read_b32 v175,a43
v_cvt_pk_bf16_f32 v172,v172,v173
v_mfma_f32_16x16x32_bf16 a[224:227],a[168:171],v[72:75],a[224:227]
s_waitcnt lgkmcnt(0)
buffer_load_dwordx4 v[208:211],v6,s[28:31],s78 offen offset:2048 sc1
v_mul_f32 v174,v21,v174
v_mfma_f32_16x16x32_bf16 a[224:227],a[172:175],v[76:79],a[224:227]
v_mul_f32 v175,v21,v175
v_cvt_pk_bf16_f32 v173,v174,v175
v_mfma_f32_16x16x32_bf16 a[228:231],a[172:175],v[92:95],0
v_accvgpr_read_b32 v174,a44
v_accvgpr_read_b32 v175,a45
v_mfma_f32_16x16x32_bf16 a[228:231],a[168:171],v[88:91],a[228:231]
v_accvgpr_read_b32 v14,a46
v_accvgpr_read_b32 v15,a47
v_mfma_f32_16x16x32_bf16 a[228:231],a[164:167],v[84:87],a[228:231]
v_mul_f32 v174,v21,v174
v_mul_f32 v175,v21,v175
v_mfma_f32_16x16x32_bf16 a[228:231],a[160:163],v[80:83],a[228:231]
ds_read_b64_tr_b16 a[176:177],v2 offset:96
ds_read_b64_tr_b16 a[178:179],v2 offset:5216
v_mfma_f32_16x16x32_bf16 a[232:235],a[160:163],v[96:99],0
buffer_load_dwordx4 v[240:243],v6,s[28:31],s79 offen offset:2048 sc1
ds_read_b64_tr_b16 a[180:181],v2 offset:10336
ds_read_b64_tr_b16 a[182:183],v2 offset:15456
v_mfma_f32_16x16x32_bf16 a[232:235],a[164:167],v[100:103],a[232:235]
ds_read_b64_tr_b16 a[184:185],v2 offset:20576
ds_read_b64_tr_b16 a[186:187],v2 offset:25696
v_mfma_f32_16x16x32_bf16 a[232:235],a[168:171],v[104:107],a[232:235]
ds_read_b64_tr_b16 a[188:189],v2 offset:30816
ds_read_b64_tr_b16 a[190:191],v2 offset:35936
v_mfma_f32_16x16x32_bf16 a[232:235],a[172:175],v[108:111],a[232:235]
v_mfma_f32_16x16x32_bf16 a[236:239],a[172:175],v[124:127],0
v_mul_f32 v14,v21,v14
v_mul_f32 v15,v21,v15
v_mfma_f32_16x16x32_bf16 a[236:239],a[168:171],v[120:123],a[236:239]
v_cvt_pk_bf16_f32 v174,v174,v175
v_cvt_pk_bf16_f32 v175,v14,v15
v_mfma_f32_16x16x32_bf16 a[236:239],a[164:167],v[116:119],a[236:239]
buffer_store_dwordx4 v[172:175],v6,s[36:39],s85 offen offset:1024 sc1
v_accvgpr_read_b32 v180,a48
v_mfma_f32_16x16x32_bf16 a[236:239],a[160:163],v[112:115],a[236:239]
s_waitcnt lgkmcnt(6)
v_accvgpr_read_b32 v181,a49
v_mul_f32 v180,v21,v180
v_mfma_f32_16x16x32_bf16 a[240:243],a[176:179],v[64:67],0
s_waitcnt lgkmcnt(4)
v_mul_f32 v181,v21,v181
v_accvgpr_read_b32 v182,a50
v_mfma_f32_16x16x32_bf16 a[240:243],a[180:183],v[68:71],a[240:243]
)A"
R"A(s_waitcnt lgkmcnt(2)
v_accvgpr_read_b32 v183,a51
v_cvt_pk_bf16_f32 v180,v180,v181
v_mfma_f32_16x16x32_bf16 a[240:243],a[184:187],v[72:75],a[240:243]
s_waitcnt lgkmcnt(0)
buffer_load_dwordx4 v[152:155],v6,s[28:31],s76 offen offset:3072 sc1
v_mul_f32 v182,v21,v182
v_mfma_f32_16x16x32_bf16 a[240:243],a[188:191],v[76:79],a[240:243]
v_mul_f32 v183,v21,v183
v_cvt_pk_bf16_f32 v181,v182,v183
v_mfma_f32_16x16x32_bf16 a[244:247],a[188:191],v[92:95],0
v_accvgpr_read_b32 v182,a52
v_accvgpr_read_b32 v183,a53
v_mfma_f32_16x16x32_bf16 a[244:247],a[184:187],v[88:91],a[244:247]
v_accvgpr_read_b32 v16,a54
v_accvgpr_read_b32 v17,a55
v_mfma_f32_16x16x32_bf16 a[244:247],a[180:183],v[84:87],a[244:247]
v_mul_f32 v182,v21,v182
v_mul_f32 v183,v21,v183
v_mfma_f32_16x16x32_bf16 a[244:247],a[176:179],v[80:83],a[244:247]
ds_read_b64_tr_b16 a[128:129],v1 offset:128
ds_read_b64_tr_b16 a[130:131],v1 offset:5248
v_mfma_f32_16x16x32_bf16 a[248:251],a[176:179],v[96:99],0
ds_read_b64_tr_b16 a[132:133],v1 offset:10368
ds_read_b64_tr_b16 a[134:135],v1 offset:15488
v_mfma_f32_16x16x32_bf16 a[248:251],a[180:183],v[100:103],a[248:251]
ds_read_b64_tr_b16 a[136:137],v1 offset:20608
ds_read_b64_tr_b16 a[138:139],v1 offset:25728
v_mfma_f32_16x16x32_bf16 a[248:251],a[184:187],v[104:107],a[248:251]
buffer_load_dwordx4 v[184:187],v6,s[28:31],s77 offen offset:3072 sc1
ds_read_b64_tr_b16 a[140:141],v1 offset:30848
ds_read_b64_tr_b16 a[142:143],v1 offset:35968
v_mfma_f32_16x16x32_bf16 a[248:251],a[188:191],v[108:111],a[248:251]
v_mfma_f32_16x16x32_bf16 a[252:255],a[188:191],v[124:127],0
v_mul_f32 v16,v21,v16
v_mul_f32 v17,v21,v17
v_mfma_f32_16x16x32_bf16 a[252:255],a[184:187],v[120:123],a[252:255]
v_cvt_pk_bf16_f32 v182,v182,v183
v_cvt_pk_bf16_f32 v183,v16,v17
v_mfma_f32_16x16x32_bf16 a[252:255],a[180:183],v[116:119],a[252:255]
buffer_store_dwordx4 v[180:183],v6,s[36:39],s86 offen offset:1024 sc1
v_accvgpr_read_b32 v188,a56
v_mfma_f32_16x16x32_bf16 a[252:255],a[176:179],v[112:115],a[252:255]
s_waitcnt lgkmcnt(6)
v_accvgpr_read_b32 v189,a57
v_mul_f32 v188,v21,v188
v_mfma_f32_16x16x32_bf16 a[0:3],a[128:131],v[64:67],0
s_waitcnt lgkmcnt(4)
v_mul_f32 v189,v21,v189
v_accvgpr_read_b32 v190,a58
v_mfma_f32_16x16x32_bf16 a[0:3],a[132:135],v[68:71],a[0:3]
s_waitcnt lgkmcnt(2)
v_accvgpr_read_b32 v191,a59
v_cvt_pk_bf16_f32 v188,v188,v189
v_mfma_f32_16x16x32_bf16 a[0:3],a[136:139],v[72:75],a[0:3]
s_waitcnt lgkmcnt(0)
v_mul_f32 v190,v21,v190
v_mul_f32 v191,v21,v191
v_mfma_f32_16x16x32_bf16 a[0:3],a[140:143],v[76:79],a[0:3]
buffer_load_dwordx4 v[216:219],v6,s[28:31],s78 offen offset:3072 sc1
v_cvt_pk_bf16_f32 v189,v190,v191
v_mfma_f32_16x16x32_bf16 a[4:7],a[140:143],v[92:95],0
v_accvgpr_read_b32 v190,a60
v_accvgpr_read_b32 v191,a61
v_mfma_f32_16x16x32_bf16 a[4:7],a[136:139],v[88:91],a[4:7]
v_accvgpr_read_b32 v12,a62
v_accvgpr_read_b32 v13,a63
v_mfma_f32_16x16x32_bf16 a[4:7],a[132:135],v[84:87],a[4:7]
v_mul_f32 v190,v21,v190
v_mul_f32 v191,v21,v191
v_mfma_f32_16x16x32_bf16 a[4:7],a[128:131],v[80:83],a[4:7]
ds_read_b64_tr_b16 a[144:145],v2 offset:160
ds_read_b64_tr_b16 a[146:147],v2 offset:5280
v_mfma_f32_16x16x32_bf16 a[8:11],a[128:131],v[96:99],0
ds_read_b64_tr_b16 a[148:149],v2 offset:10400
ds_read_b64_tr_b16 a[150:151],v2 offset:15520
v_mfma_f32_16x16x32_bf16 a[8:11],a[132:135],v[100:103],a[8:11]
ds_read_b64_tr_b16 a[152:153],v2 offset:20640
ds_read_b64_tr_b16 a[154:155],v2 offset:25760
v_mfma_f32_16x16x32_bf16 a[8:11],a[136:139],v[104:107],a[8:11]
ds_read_b64_tr_b16 a[156:157],v2 offset:30880
ds_read_b64_tr_b16 a[158:159],v2 offset:36000
v_mfma_f32_16x16x32_bf16 a[8:11],a[140:143],v[108:111],a[8:11]
v_mfma_f32_16x16x32_bf16 a[12:15],a[140:143],v[124:127],0
buffer_load_dwordx4 v[248:251],v6,s[28:31],s79 offen offset:3072 sc1
v_mul_f32 v12,v21,v12
v_mfma_f32_16x16x32_bf16 a[12:15],a[136:139],v[120:123],a[12:15]
s_add_u32 m0,s82,122880
v_mul_f32 v13,v21,v13
v_cvt_pk_bf16_f32 v190,v190,v191
v_mfma_f32_16x16x32_bf16 a[12:15],a[132:135],v[116:119],a[12:15]
v_cvt_pk_bf16_f32 v191,v12,v13
v_max_f32 v22,0x00800000,v56
v_mfma_f32_16x16x32_bf16 a[12:15],a[128:131],v[112:115],a[12:15]
s_waitcnt lgkmcnt(6)
v_rcp_f32 v22,v22
v_mfma_f32_16x16x32_bf16 a[16:19],a[144:147],v[64:67],0
s_waitcnt lgkmcnt(4)
buffer_store_dwordx4 v[188:191],v6,s[36:39],s87 offen offset:1024 sc1
v_accvgpr_read_b32 v196,a64
v_mfma_f32_16x16x32_bf16 a[16:19],a[148:151],v[68:71],a[16:19]
s_waitcnt lgkmcnt(2)
v_accvgpr_read_b32 v197,a65
v_mul_f32 v196,v22,v196
v_mfma_f32_16x16x32_bf16 a[16:19],a[152:155],v[72:75],a[16:19]
s_cmp_ge_u32 1,s71
s_cselect_b32 s80,4,2
s_mul_i32 s80,s80,163840
s_waitcnt lgkmcnt(0)
v_mfma_f32_16x16x32_bf16 a[16:19],a[156:159],v[76:79],a[16:19]
s_add_u32 s80,s80,s81
v_mul_f32 v197,v22,v197
v_accvgpr_read_b32 v198,a66
v_mfma_f32_16x16x32_bf16 a[20:23],a[156:159],v[92:95],0
s_add_u32 s83,s80,20480
buffer_load_dwordx4 v7,s[72:75],s80 offen lds
v_accvgpr_read_b32 v199,a67
v_mfma_f32_16x16x32_bf16 a[20:23],a[152:155],v[88:91],a[20:23]
v_cvt_pk_bf16_f32 v196,v196,v197
v_mul_f32 v198,v22,v198
v_mfma_f32_16x16x32_bf16 a[20:23],a[148:151],v[84:87],a[20:23]
v_mul_f32 v199,v22,v199
v_cvt_pk_bf16_f32 v197,v198,v199
v_mfma_f32_16x16x32_bf16 a[20:23],a[144:147],v[80:83],a[20:23]
ds_read_b64_tr_b16 a[160:161],v1 offset:192
ds_read_b64_tr_b16 a[162:163],v1 offset:5312
v_mfma_f32_16x16x32_bf16 a[24:27],a[144:147],v[96:99],0
ds_read_b64_tr_b16 a[164:165],v1 offset:10432
ds_read_b64_tr_b16 a[166:167],v1 offset:15552
v_mfma_f32_16x16x32_bf16 a[24:27],a[148:151],v[100:103],a[24:27]
ds_read_b64_tr_b16 a[168:169],v1 offset:20672
ds_read_b64_tr_b16 a[170:171],v1 offset:25792
v_mfma_f32_16x16x32_bf16 a[24:27],a[152:155],v[104:107],a[24:27]
ds_read_b64_tr_b16 a[172:173],v1 offset:30912
ds_read_b64_tr_b16 a[174:175],v1 offset:36032
v_mfma_f32_16x16x32_bf16 a[24:27],a[156:159],v[108:111],a[24:27]
v_mfma_f32_16x16x32_bf16 a[28:31],a[156:159],v[124:127],0
buffer_load_dwordx4 v8,s[72:75],s80 offen offset:1024 lds
v_accvgpr_read_b32 v198,a68
v_mfma_f32_16x16x32_bf16 a[28:31],a[152:155],v[120:123],a[28:31]
v_accvgpr_read_b32 v199,a69
v_accvgpr_read_b32 v14,a70
v_mfma_f32_16x16x32_bf16 a[28:31],a[148:151],v[116:119],a[28:31]
v_accvgpr_read_b32 v15,a71
v_mul_f32 v198,v22,v198
v_mfma_f32_16x16x32_bf16 a[28:31],a[144:147],v[112:115],a[28:31]
s_waitcnt lgkmcnt(6)
v_mul_f32 v199,v22,v199
v_mul_f32 v14,v22,v14
v_mfma_f32_16x16x32_bf16 a[32:35],a[160:163],v[64:67],0
s_waitcnt lgkmcnt(4)
v_mul_f32 v15,v22,v15
v_cvt_pk_bf16_f32 v198,v198,v199
v_mfma_f32_16x16x32_bf16 a[32:35],a[164:167],v[68:71],a[32:35]
s_waitcnt lgkmcnt(2)
v_cvt_pk_bf16_f32 v199,v14,v15
v_accvgpr_read_b32 v204,a72
v_mfma_f32_16x16x32_bf16 a[32:35],a[168:171],v[72:75],a[32:35]
s_waitcnt lgkmcnt(0)
buffer_store_dwordx4 v[196:199],v6,s[36:39],s84 offen offset:2048 sc1
v_accvgpr_read_b32 v205,a73
v_mfma_f32_16x16x32_bf16 a[32:35],a[172:175],v[76:79],a[32:35]
v_mul_f32 v204,v22,v204
v_mul_f32 v205,v22,v205
v_mfma_f32_16x16x32_bf16 a[36:39],a[172:175],v[92:95],0
v_accvgpr_read_b32 v206,a74
v_accvgpr_read_b32 v207,a75
v_mfma_f32_16x16x32_bf16 a[36:39],a[168:171],v[88:91],a[36:39]
v_cvt_pk_bf16_f32 v204,v204,v205
v_mul_f32 v206,v22,v206
v_mfma_f32_16x16x32_bf16 a[36:39],a[164:167],v[84:87],a[36:39]
buffer_load_dwordx4 v9,s[72:75],s80 offen offset:2048 lds
v_mul_f32 v207,v22,v207
v_mfma_f32_16x16x32_bf16 a[36:39],a[160:163],v[80:83],a[36:39]
ds_read_b64_tr_b16 a[176:177],v2 offset:224
ds_read_b64_tr_b16 a[178:179],v2 offset:5344
v_mfma_f32_16x16x32_bf16 a[40:43],a[160:163],v[96:99],0
ds_read_b64_tr_b16 a[180:181],v2 offset:10464
ds_read_b64_tr_b16 a[182:183],v2 offset:15584
v_mfma_f32_16x16x32_bf16 a[40:43],a[164:167],v[100:103],a[40:43]
ds_read_b64_tr_b16 a[184:185],v2 offset:20704
ds_read_b64_tr_b16 a[186:187],v2 offset:25824
v_mfma_f32_16x16x32_bf16 a[40:43],a[168:171],v[104:107],a[40:43]
ds_read_b64_tr_b16 a[188:189],v2 offset:30944
ds_read_b64_tr_b16 a[190:191],v2 offset:36064
v_mfma_f32_16x16x32_bf16 a[40:43],a[172:175],v[108:111],a[40:43]
v_mfma_f32_16x16x32_bf16 a[44:47],a[172:175],v[124:127],0
v_cvt_pk_bf16_f32 v205,v206,v207
v_accvgpr_read_b32 v206,a76
v_mfma_f32_16x16x32_bf16 a[44:47],a[168:171],v[120:123],a[44:47]
v_accvgpr_read_b32 v207,a77
v_accvgpr_read_b32 v16,a78
v_mfma_f32_16x16x32_bf16 a[44:47],a[164:167],v[116:119],a[44:47]
buffer_load_dwordx4 v10,s[72:75],s80 offen offset:3072 lds
v_accvgpr_read_b32 v17,a79
v_mfma_f32_16x16x32_bf16 a[44:47],a[160:163],v[112:115],a[44:47]
s_waitcnt lgkmcnt(6)
v_mul_f32 v206,v22,v206
v_mul_f32 v207,v22,v207
v_mfma_f32_16x16x32_bf16 a[48:51],a[176:179],v[64:67],0
s_waitcnt lgkmcnt(4)
v_mul_f32 v16,v22,v16
v_mul_f32 v17,v22,v17
v_mfma_f32_16x16x32_bf16 a[48:51],a[180:183],v[68:71],a[48:51]
s_add_u32 m0,s82,126976
v_cvt_pk_bf16_f32 v206,v206,v207
v_cvt_pk_bf16_f32 v207,v16,v17
s_waitcnt lgkmcnt(2)
v_mfma_f32_16x16x32_bf16 a[48:51],a[184:187],v[72:75],a[48:51]
s_waitcnt lgkmcnt(0)
buffer_store_dwordx4 v[204:207],v6,s[36:39],s85 offen offset:2048 sc1
v_accvgpr_read_b32 v212,a80
v_mfma_f32_16x16x32_bf16 a[48:51],a[188:191],v[76:79],a[48:51]
v_accvgpr_read_b32 v213,a81
v_mul_f32 v212,v22,v212
v_mfma_f32_16x16x32_bf16 a[52:55],a[188:191],v[92:95],0
v_mul_f32 v213,v22,v213
v_accvgpr_read_b32 v214,a82
v_mfma_f32_16x16x32_bf16 a[52:55],a[184:187],v[88:91],a[52:55]
v_accvgpr_read_b32 v215,a83
v_cvt_pk_bf16_f32 v212,v212,v213
v_mfma_f32_16x16x32_bf16 a[52:55],a[180:183],v[84:87],a[52:55]
v_mul_f32 v214,v22,v214
v_mul_f32 v215,v22,v215
v_mfma_f32_16x16x32_bf16 a[52:55],a[176:179],v[80:83],a[52:55]
buffer_load_dwordx4 v11,s[72:75],s80 offen lds
ds_read_b64_tr_b16 a[128:129],v1 offset:256
ds_read_b64_tr_b16 a[130:131],v1 offset:5376
v_mfma_f32_16x16x32_bf16 a[56:59],a[176:179],v[96:99],0
ds_read_b64_tr_b16 a[132:133],v1 offset:10496
ds_read_b64_tr_b16 a[134:135],v1 offset:15616
v_mfma_f32_16x16x32_bf16 a[56:59],a[180:183],v[100:103],a[56:59]
ds_read_b64_tr_b16 a[136:137],v1 offset:20736
ds_read_b64_tr_b16 a[138:139],v1 offset:25856
v_mfma_f32_16x16x32_bf16 a[56:59],a[184:187],v[104:107],a[56:59]
ds_read_b64_tr_b16 a[140:141],v1 offset:30976
ds_read_b64_tr_b16 a[142:143],v1 offset:36096
v_mfma_f32_16x16x32_bf16 a[56:59],a[188:191],v[108:111],a[56:59]
v_mfma_f32_16x16x32_bf16 a[60:63],a[188:191],v[124:127],0
s_add_u32 m0,s82,128000
v_cvt_pk_bf16_f32 v213,v214,v215
v_accvgpr_read_b32 v214,a84
v_mfma_f32_16x16x32_bf16 a[60:63],a[184:187],v[120:123],a[60:63]
v_accvgpr_read_b32 v215,a85
v_accvgpr_read_b32 v12,a86
v_mfma_f32_16x16x32_bf16 a[60:63],a[180:183],v[116:119],a[60:63]
v_accvgpr_read_b32 v13,a87
v_mul_f32 v214,v22,v214
v_mfma_f32_16x16x32_bf16 a[60:63],a[176:179],v[112:115],a[60:63]
s_waitcnt lgkmcnt(6)
v_mul_f32 v215,v22,v215
v_mul_f32 v12,v22,v12
v_mfma_f32_16x16x32_bf16 a[64:67],a[128:131],v[64:67],0
s_waitcnt lgkmcnt(4)
buffer_load_dwordx4 v7,s[72:75],s83 offen lds
v_mul_f32 v13,v22,v13
v_mfma_f32_16x16x32_bf16 a[64:67],a[132:135],v[68:71],a[64:67]
s_waitcnt lgkmcnt(2)
v_cvt_pk_bf16_f32 v214,v214,v215
v_cvt_pk_bf16_f32 v215,v12,v13
v_mfma_f32_16x16x32_bf16 a[64:67],a[136:139],v[72:75],a[64:67]
s_waitcnt lgkmcnt(0)
v_accvgpr_read_b32 v220,a88
v_accvgpr_read_b32 v221,a89
v_mfma_f32_16x16x32_bf16 a[64:67],a[140:143],v[76:79],a[64:67]
v_mul_f32 v220,v22,v220
v_mul_f32 v221,v22,v221
v_mfma_f32_16x16x32_bf16 a[68:71],a[140:143],v[92:95],0
buffer_store_dwordx4 v[212:215],v6,s[36:39],s86 offen offset:2048 sc1
v_accvgpr_read_b32 v222,a90
v_mfma_f32_16x16x32_bf16 a[68:71],a[136:139],v[88:91],a[68:71]
v_accvgpr_read_b32 v223,a91
v_cvt_pk_bf16_f32 v220,v220,v221
v_mfma_f32_16x16x32_bf16 a[68:71],a[132:135],v[84:87],a[68:71]
v_mul_f32 v222,v22,v222
v_mul_f32 v223,v22,v223
v_mfma_f32_16x16x32_bf16 a[68:71],a[128:131],v[80:83],a[68:71]
ds_read_b64_tr_b16 a[144:145],v2 offset:288
ds_read_b64_tr_b16 a[146:147],v2 offset:5408
v_mfma_f32_16x16x32_bf16 a[72:75],a[128:131],v[96:99],0
ds_read_b64_tr_b16 a[148:149],v2 offset:10528
ds_read_b64_tr_b16 a[150:151],v2 offset:15648
v_mfma_f32_16x16x32_bf16 a[72:75],a[132:135],v[100:103],a[72:75]
buffer_load_dwordx4 v8,s[72:75],s83 offen offset:1024 lds
ds_read_b64_tr_b16 a[152:153],v2 offset:20768
)A"
R"A(ds_read_b64_tr_b16 a[154:155],v2 offset:25888
v_mfma_f32_16x16x32_bf16 a[72:75],a[136:139],v[104:107],a[72:75]
ds_read_b64_tr_b16 a[156:157],v2 offset:31008
ds_read_b64_tr_b16 a[158:159],v2 offset:36128
v_mfma_f32_16x16x32_bf16 a[72:75],a[140:143],v[108:111],a[72:75]
v_mfma_f32_16x16x32_bf16 a[76:79],a[140:143],v[124:127],0
v_cvt_pk_bf16_f32 v221,v222,v223
v_accvgpr_read_b32 v222,a92
v_mfma_f32_16x16x32_bf16 a[76:79],a[136:139],v[120:123],a[76:79]
v_accvgpr_read_b32 v223,a93
v_accvgpr_read_b32 v14,a94
v_mfma_f32_16x16x32_bf16 a[76:79],a[132:135],v[116:119],a[76:79]
v_accvgpr_read_b32 v15,a95
v_mul_f32 v222,v22,v222
v_mfma_f32_16x16x32_bf16 a[76:79],a[128:131],v[112:115],a[76:79]
s_waitcnt lgkmcnt(6)
v_mul_f32 v223,v22,v223
v_mul_f32 v14,v22,v14
v_mfma_f32_16x16x32_bf16 a[80:83],a[144:147],v[64:67],0
s_waitcnt lgkmcnt(4)
v_mul_f32 v15,v22,v15
v_cvt_pk_bf16_f32 v222,v222,v223
v_mfma_f32_16x16x32_bf16 a[80:83],a[148:151],v[68:71],a[80:83]
s_waitcnt lgkmcnt(2)
v_cvt_pk_bf16_f32 v223,v14,v15
v_max_f32 v23,0x00800000,v60
v_mfma_f32_16x16x32_bf16 a[80:83],a[152:155],v[72:75],a[80:83]
s_waitcnt lgkmcnt(0)
buffer_load_dwordx4 v9,s[72:75],s83 offen offset:2048 lds
v_accvgpr_read_b32 v228,a96
v_mfma_f32_16x16x32_bf16 a[80:83],a[156:159],v[76:79],a[80:83]
v_rcp_f32 v23,v23
v_mfma_f32_16x16x32_bf16 a[84:87],a[156:159],v[92:95],0
v_accvgpr_read_b32 v229,a97
v_mul_f32 v228,v23,v228
v_mfma_f32_16x16x32_bf16 a[84:87],a[152:155],v[88:91],a[84:87]
v_mul_f32 v229,v23,v229
v_accvgpr_read_b32 v230,a98
v_mfma_f32_16x16x32_bf16 a[84:87],a[148:151],v[84:87],a[84:87]
buffer_store_dwordx4 v[220:223],v6,s[36:39],s87 offen offset:2048 sc1
v_add_u32 v1,40960,v1
v_mfma_f32_16x16x32_bf16 a[84:87],a[144:147],v[80:83],a[84:87]
ds_read_b64_tr_b16 a[160:161],v1
ds_read_b64_tr_b16 a[162:163],v1 offset:5120
v_mfma_f32_16x16x32_bf16 a[88:91],a[144:147],v[96:99],0
ds_read_b64_tr_b16 a[164:165],v1 offset:10240
ds_read_b64_tr_b16 a[166:167],v1 offset:15360
v_mfma_f32_16x16x32_bf16 a[88:91],a[148:151],v[100:103],a[88:91]
ds_read_b64_tr_b16 a[168:169],v1 offset:20480
ds_read_b64_tr_b16 a[170:171],v1 offset:25600
v_mfma_f32_16x16x32_bf16 a[88:91],a[152:155],v[104:107],a[88:91]
ds_read_b64_tr_b16 a[172:173],v1 offset:30720
ds_read_b64_tr_b16 a[174:175],v1 offset:35840
v_mfma_f32_16x16x32_bf16 a[88:91],a[156:159],v[108:111],a[88:91]
buffer_load_dwordx4 v10,s[72:75],s83 offen offset:3072 lds
v_mfma_f32_16x16x32_bf16 a[92:95],a[156:159],v[124:127],0
v_accvgpr_read_b32 v231,a99
v_cvt_pk_bf16_f32 v228,v228,v229
v_mfma_f32_16x16x32_bf16 a[92:95],a[152:155],v[120:123],a[92:95]
v_mul_f32 v230,v23,v230
v_mul_f32 v231,v23,v231
v_mfma_f32_16x16x32_bf16 a[92:95],a[148:151],v[116:119],a[92:95]
s_add_u32 m0,s82,132096
v_cvt_pk_bf16_f32 v229,v230,v231
v_accvgpr_read_b32 v230,a100
v_mfma_f32_16x16x32_bf16 a[92:95],a[144:147],v[112:115],a[92:95]
s_waitcnt lgkmcnt(6)
v_accvgpr_read_b32 v231,a101
v_accvgpr_read_b32 v16,a102
s_waitcnt vmcnt(12)
v_mfma_f32_16x16x32_bf16 a[192:195],a[160:163],v[128:131],a[192:195]
s_waitcnt lgkmcnt(4)
v_accvgpr_read_b32 v17,a103
v_mul_f32 v230,v23,v230
v_mfma_f32_16x16x32_bf16 a[192:195],a[164:167],v[160:163],a[192:195]
s_waitcnt lgkmcnt(2)
v_mul_f32 v231,v23,v231
v_mul_f32 v16,v23,v16
v_mfma_f32_16x16x32_bf16 a[192:195],a[168:171],v[192:195],a[192:195]
s_waitcnt lgkmcnt(0)
v_mul_f32 v17,v23,v17
v_cvt_pk_bf16_f32 v230,v230,v231
v_mfma_f32_16x16x32_bf16 a[192:195],a[172:175],v[224:227],a[192:195]
v_cvt_pk_bf16_f32 v231,v16,v17
v_accvgpr_read_b32 v236,a104
v_mfma_f32_16x16x32_bf16 a[196:199],a[172:175],v[232:235],a[196:199]
buffer_load_dwordx4 v11,s[72:75],s83 offen lds
v_accvgpr_read_b32 v237,a105
v_mfma_f32_16x16x32_bf16 a[196:199],a[168:171],v[200:203],a[196:199]
v_mul_f32 v236,v23,v236
v_mul_f32 v237,v23,v237
v_mfma_f32_16x16x32_bf16 a[196:199],a[164:167],v[168:171],a[196:199]
v_accvgpr_read_b32 v238,a106
v_accvgpr_read_b32 v239,a107
v_mfma_f32_16x16x32_bf16 a[196:199],a[160:163],v[136:139],a[196:199]
v_add_u32 v2,40960,v2
v_cvt_pk_bf16_f32 v236,v236,v237
v_mfma_f32_16x16x32_bf16 a[200:203],a[160:163],v[144:147],a[200:203]
ds_read_b64_tr_b16 a[176:177],v2 offset:32
ds_read_b64_tr_b16 a[178:179],v2 offset:5152
v_mfma_f32_16x16x32_bf16 a[200:203],a[164:167],v[176:179],a[200:203]
ds_read_b64_tr_b16 a[180:181],v2 offset:10272
ds_read_b64_tr_b16 a[182:183],v2 offset:15392
v_mfma_f32_16x16x32_bf16 a[200:203],a[168:171],v[208:211],a[200:203]
ds_read_b64_tr_b16 a[184:185],v2 offset:20512
ds_read_b64_tr_b16 a[186:187],v2 offset:25632
v_mfma_f32_16x16x32_bf16 a[200:203],a[172:175],v[240:243],a[200:203]
ds_read_b64_tr_b16 a[188:189],v2 offset:30752
ds_read_b64_tr_b16 a[190:191],v2 offset:35872
v_mfma_f32_16x16x32_bf16 a[204:207],a[172:175],v[248:251],a[204:207]
v_mfma_f32_16x16x32_bf16 a[204:207],a[168:171],v[216:219],a[204:207]
buffer_store_dwordx4 v[228:231],v6,s[36:39],s84 offen offset:3072 sc1
v_mul_f32 v238,v23,v238
v_mfma_f32_16x16x32_bf16 a[204:207],a[164:167],v[184:187],a[204:207]
v_mul_f32 v239,v23,v239
v_cvt_pk_bf16_f32 v237,v238,v239
v_mfma_f32_16x16x32_bf16 a[204:207],a[160:163],v[152:155],a[204:207]
v_accvgpr_read_b32 v238,a108
v_accvgpr_read_b32 v239,a109
s_barrier
s_waitcnt lgkmcnt(6)
v_mfma_f32_16x16x32_bf16 a[208:211],a[176:179],v[128:131],a[208:211]
s_waitcnt lgkmcnt(4)
v_accvgpr_read_b32 v12,a110
v_accvgpr_read_b32 v13,a111
v_mfma_f32_16x16x32_bf16 a[208:211],a[180:183],v[160:163],a[208:211]
s_waitcnt lgkmcnt(2)
v_mul_f32 v238,v23,v238
v_mul_f32 v239,v23,v239
v_mfma_f32_16x16x32_bf16 a[208:211],a[184:187],v[192:195],a[208:211]
s_waitcnt lgkmcnt(0)
v_mul_f32 v12,v23,v12
v_mul_f32 v13,v23,v13
v_mfma_f32_16x16x32_bf16 a[208:211],a[188:191],v[224:227],a[208:211]
v_cvt_pk_bf16_f32 v238,v238,v239
v_cvt_pk_bf16_f32 v239,v12,v13
v_mfma_f32_16x16x32_bf16 a[212:215],a[188:191],v[232:235],a[212:215]
buffer_store_dwordx4 v[236:239],v6,s[36:39],s85 offen offset:3072 sc1
v_accvgpr_read_b32 v244,a112
v_mfma_f32_16x16x32_bf16 a[212:215],a[184:187],v[200:203],a[212:215]
v_accvgpr_read_b32 v245,a113
v_mul_f32 v244,v23,v244
v_mfma_f32_16x16x32_bf16 a[212:215],a[180:183],v[168:171],a[212:215]
v_mul_f32 v245,v23,v245
v_accvgpr_read_b32 v246,a114
v_mfma_f32_16x16x32_bf16 a[212:215],a[176:179],v[136:139],a[212:215]
ds_read_b64_tr_b16 a[128:129],v1 offset:64
ds_read_b64_tr_b16 a[130:131],v1 offset:5184
v_mfma_f32_16x16x32_bf16 a[216:219],a[176:179],v[144:147],a[216:219]
ds_read_b64_tr_b16 a[132:133],v1 offset:10304
ds_read_b64_tr_b16 a[134:135],v1 offset:15424
v_mfma_f32_16x16x32_bf16 a[216:219],a[180:183],v[176:179],a[216:219]
ds_read_b64_tr_b16 a[136:137],v1 offset:20544
ds_read_b64_tr_b16 a[138:139],v1 offset:25664
v_mfma_f32_16x16x32_bf16 a[216:219],a[184:187],v[208:211],a[216:219]
ds_read_b64_tr_b16 a[140:141],v1 offset:30784
ds_read_b64_tr_b16 a[142:143],v1 offset:35904
v_mfma_f32_16x16x32_bf16 a[216:219],a[188:191],v[240:243],a[216:219]
v_mfma_f32_16x16x32_bf16 a[220:223],a[188:191],v[248:251],a[220:223]
v_accvgpr_read_b32 v247,a115
v_cvt_pk_bf16_f32 v244,v244,v245
v_mfma_f32_16x16x32_bf16 a[220:223],a[184:187],v[216:219],a[220:223]
v_mul_f32 v246,v23,v246
v_mul_f32 v247,v23,v247
v_mfma_f32_16x16x32_bf16 a[220:223],a[180:183],v[184:187],a[220:223]
v_cvt_pk_bf16_f32 v245,v246,v247
v_accvgpr_read_b32 v246,a116
v_mfma_f32_16x16x32_bf16 a[220:223],a[176:179],v[152:155],a[220:223]
s_waitcnt lgkmcnt(6)
v_accvgpr_read_b32 v247,a117
v_accvgpr_read_b32 v14,a118
v_mfma_f32_16x16x32_bf16 a[224:227],a[128:131],v[128:131],a[224:227]
s_waitcnt lgkmcnt(4)
v_accvgpr_read_b32 v15,a119
v_mul_f32 v246,v23,v246
v_mfma_f32_16x16x32_bf16 a[224:227],a[132:135],v[160:163],a[224:227]
s_waitcnt lgkmcnt(2)
v_accvgpr_read_b32 v252,a120
v_accvgpr_read_b32 v253,a121
v_mul_f32 v252,v23,v252
v_mul_f32 v253,v23,v253
v_accvgpr_read_b32 v254,a122
v_accvgpr_read_b32 v255,a123
v_mfma_f32_16x16x32_bf16 a[224:227],a[136:139],v[192:195],a[224:227]
s_waitcnt lgkmcnt(0)
v_cvt_pk_bf16_f32 v252,v252,v253
v_mul_f32 v254,v23,v254
v_mul_f32 v255,v23,v255
v_cvt_pk_bf16_f32 v253,v254,v255
v_mfma_f32_16x16x32_bf16 a[224:227],a[140:143],v[224:227],a[224:227]
v_mul_f32 v247,v23,v247
v_accvgpr_read_b32 v254,a124
v_accvgpr_read_b32 v255,a125
v_accvgpr_read_b32 v16,a126
v_accvgpr_read_b32 v17,a127
v_mul_f32 v254,v23,v254
v_mul_f32 v255,v23,v255
v_mfma_f32_16x16x32_bf16 a[228:231],a[140:143],v[232:235],a[228:231]
v_mul_f32 v14,v23,v14
v_mul_f32 v15,v23,v15
v_cvt_pk_bf16_f32 v246,v246,v247
v_cvt_pk_bf16_f32 v247,v14,v15
v_mul_f32 v16,v23,v16
v_mul_f32 v17,v23,v17
v_cvt_pk_bf16_f32 v254,v254,v255
v_cvt_pk_bf16_f32 v255,v16,v17
v_mfma_f32_16x16x32_bf16 a[228:231],a[136:139],v[200:203],a[228:231]
buffer_store_dwordx4 v[244:247],v6,s[36:39],s86 offen offset:3072 sc1
ds_read_b64_tr_b16 a[144:145],v2 offset:96
v_mfma_f32_16x16x32_bf16 a[228:231],a[132:135],v[168:171],a[228:231]
ds_read_b64_tr_b16 a[146:147],v2 offset:5216
ds_read_b64_tr_b16 a[148:149],v2 offset:10336
v_mfma_f32_16x16x32_bf16 a[228:231],a[128:131],v[136:139],a[228:231]
s_cmp_ge_u32 1,s71
s_cselect_b32 s76,4,2
ds_read_b64_tr_b16 a[150:151],v2 offset:15456
v_mfma_f32_16x16x32_bf16 a[232:235],a[128:131],v[144:147],a[232:235]
s_lshl_b32 s76,s76,16
s_add_u32 s77,s76,4096
ds_read_b64_tr_b16 a[152:153],v2 offset:20576
v_mfma_f32_16x16x32_bf16 a[232:235],a[132:135],v[176:179],a[232:235]
s_add_u32 s78,s76,8192
buffer_store_dwordx4 v[252:255],v6,s[36:39],s87 offen offset:3072 sc1
ds_read_b64_tr_b16 a[154:155],v2 offset:25696
v_mfma_f32_16x16x32_bf16 a[232:235],a[136:139],v[208:211],a[232:235]
s_add_u32 s79,s76,12288
ds_read_b64_tr_b16 a[156:157],v2 offset:30816
ds_read_b64_tr_b16 a[158:159],v2 offset:35936
v_mfma_f32_16x16x32_bf16 a[232:235],a[140:143],v[240:243],a[232:235]
ds_read_b64_tr_b16 a[160:161],v1 offset:128
v_cmp_nlt_f32 s[56:57],v48,s63
v_mfma_f32_16x16x32_bf16 a[236:239],a[140:143],v[248:251],a[236:239]
ds_read_b64_tr_b16 a[162:163],v1 offset:5248
v_mfma_f32_16x16x32_bf16 a[236:239],a[136:139],v[216:219],a[236:239]
buffer_load_dwordx4 v[64:67],v6,s[28:31],s76 offen sc1
ds_read_b64_tr_b16 a[164:165],v1 offset:10368
v_mfma_f32_16x16x32_bf16 a[236:239],a[132:135],v[184:187],a[236:239]
ds_read_b64_tr_b16 a[166:167],v1 offset:15488
ds_read_b64_tr_b16 a[168:169],v1 offset:20608
v_mfma_f32_16x16x32_bf16 a[236:239],a[128:131],v[152:155],a[236:239]
s_waitcnt lgkmcnt(9)
ds_read_b64_tr_b16 a[170:171],v1 offset:25728
v_mfma_f32_16x16x32_bf16 a[240:243],a[144:147],v[128:131],a[240:243]
ds_read_b64_tr_b16 a[172:173],v1 offset:30848
v_mfma_f32_16x16x32_bf16 a[240:243],a[148:151],v[160:163],a[240:243]
s_waitcnt lgkmcnt(9)
buffer_load_dwordx4 v[68:71],v6,s[28:31],s77 offen sc1
ds_read_b64_tr_b16 a[174:175],v1 offset:35968
v_mfma_f32_16x16x32_bf16 a[240:243],a[152:155],v[192:195],a[240:243]
s_waitcnt lgkmcnt(8)
ds_read_b64_tr_b16 a[176:177],v2 offset:160
ds_read_b64_tr_b16 a[178:179],v2 offset:5280
v_mfma_f32_16x16x32_bf16 a[240:243],a[156:159],v[224:227],a[240:243]
ds_read_b64_tr_b16 a[180:181],v2 offset:10400
v_mfma_f32_16x16x32_bf16 a[244:247],a[156:159],v[232:235],a[244:247]
ds_read_b64_tr_b16 a[182:183],v2 offset:15520
v_mfma_f32_16x16x32_bf16 a[244:247],a[152:155],v[200:203],a[244:247]
buffer_load_dwordx4 v[72:75],v6,s[28:31],s78 offen sc1
ds_read_b64_tr_b16 a[184:185],v2 offset:20640
v_mfma_f32_16x16x32_bf16 a[244:247],a[148:151],v[168:171],a[244:247]
ds_read_b64_tr_b16 a[186:187],v2 offset:25760
ds_read_b64_tr_b16 a[188:189],v2 offset:30880
v_mfma_f32_16x16x32_bf16 a[244:247],a[144:147],v[136:139],a[244:247]
ds_read_b64_tr_b16 a[190:191],v2 offset:36000
v_mfma_f32_16x16x32_bf16 a[248:251],a[144:147],v[144:147],a[248:251]
ds_read_b64_tr_b16 a[128:129],v1 offset:192
v_mfma_f32_16x16x32_bf16 a[248:251],a[148:151],v[176:179],a[248:251]
buffer_load_dwordx4 v[76:79],v6,s[28:31],s79 offen sc1
ds_read_b64_tr_b16 a[130:131],v1 offset:5312
v_mfma_f32_16x16x32_bf16 a[248:251],a[152:155],v[208:211],a[248:251]
ds_read_b64_tr_b16 a[132:133],v1 offset:10432
ds_read_b64_tr_b16 a[134:135],v1 offset:15552
v_mfma_f32_16x16x32_bf16 a[248:251],a[156:159],v[240:243],a[248:251]
ds_read_b64_tr_b16 a[136:137],v1 offset:20672
v_mfma_f32_16x16x32_bf16 a[252:255],a[156:159],v[248:251],a[252:255]
)A"
R"A(ds_read_b64_tr_b16 a[138:139],v1 offset:25792
v_mfma_f32_16x16x32_bf16 a[252:255],a[152:155],v[216:219],a[252:255]
ds_read_b64_tr_b16 a[140:141],v1 offset:30912
v_mfma_f32_16x16x32_bf16 a[252:255],a[148:151],v[184:187],a[252:255]
ds_read_b64_tr_b16 a[142:143],v1 offset:36032
ds_read_b64_tr_b16 a[148:149],v2 offset:10464
v_mfma_f32_16x16x32_bf16 a[252:255],a[144:147],v[152:155],a[252:255]
s_waitcnt lgkmcnt(9)
buffer_load_dwordx4 v[80:83],v6,s[28:31],s76 offen offset:1024 sc1
ds_read_b64_tr_b16 a[144:145],v2 offset:224
v_mfma_f32_16x16x32_bf16 a[0:3],a[160:163],v[128:131],a[0:3]
v_cmp_nlt_f32 s[58:59],v52,s63
s_nop 1
s_or_b64 s[56:57],s[56:57],s[58:59]
v_mfma_f32_16x16x32_bf16 a[0:3],a[164:167],v[160:163],a[0:3]
v_cmp_nlt_f32 s[58:59],v56,s63
s_nop 1
s_or_b64 s[56:57],s[56:57],s[58:59]
v_mfma_f32_16x16x32_bf16 a[0:3],a[168:171],v[192:195],a[0:3]
v_cmp_nlt_f32 s[58:59],v60,s63
s_nop 1
s_or_b64 s[56:57],s[56:57],s[58:59]
v_mfma_f32_16x16x32_bf16 a[0:3],a[172:175],v[224:227],a[0:3]
s_cmp_lg_u64 s[56:57],0
s_cbranch_scc1 L_slow1_%=
L_sret1_%=:
ds_read_b64_tr_b16 a[146:147],v2 offset:5344
v_mfma_f32_16x16x32_bf16 a[4:7],a[172:175],v[232:235],a[4:7]
ds_read_b64_tr_b16 a[150:151],v2 offset:15584
ds_read_b64_tr_b16 a[152:153],v2 offset:20704
v_mfma_f32_16x16x32_bf16 a[4:7],a[168:171],v[200:203],a[4:7]
buffer_load_dwordx4 v[84:87],v6,s[28:31],s77 offen offset:1024 sc1
ds_read_b64_tr_b16 a[154:155],v2 offset:25824
v_mfma_f32_16x16x32_bf16 a[4:7],a[164:167],v[168:171],a[4:7]
ds_read_b64_tr_b16 a[156:157],v2 offset:30944
v_mfma_f32_16x16x32_bf16 a[4:7],a[160:163],v[136:139],a[4:7]
ds_read_b64_tr_b16 a[158:159],v2 offset:36064
v_mfma_f32_16x16x32_bf16 a[8:11],a[160:163],v[144:147],a[8:11]
v_mfma_f32_16x16x32_bf16 a[8:11],a[164:167],v[176:179],a[8:11]
v_mfma_f32_16x16x32_bf16 a[8:11],a[168:171],v[208:211],a[8:11]
v_mfma_f32_16x16x32_bf16 a[8:11],a[172:175],v[240:243],a[8:11]
buffer_load_dwordx4 v[88:91],v6,s[28:31],s78 offen offset:1024 sc1
v_mfma_f32_16x16x32_bf16 a[12:15],a[172:175],v[248:251],a[12:15]
ds_read_b64_tr_b16 a[172:173],v1 offset:30976
ds_read_b64_tr_b16 a[174:175],v1 offset:36096
v_mfma_f32_16x16x32_bf16 a[12:15],a[168:171],v[216:219],a[12:15]
ds_read_b64_tr_b16 a[168:169],v1 offset:20736
v_mfma_f32_16x16x32_bf16 a[12:15],a[164:167],v[184:187],a[12:15]
ds_read_b64_tr_b16 a[164:165],v1 offset:10496
v_mfma_f32_16x16x32_bf16 a[12:15],a[160:163],v[152:155],a[12:15]
ds_read_b64_tr_b16 a[160:161],v1 offset:256
v_mfma_f32_16x16x32_bf16 a[16:19],a[176:179],v[128:131],a[16:19]
ds_read_b64_tr_b16 a[162:163],v1 offset:5376
ds_read_b64_tr_b16 a[166:167],v1 offset:15616
v_mfma_f32_16x16x32_bf16 a[16:19],a[180:183],v[160:163],a[16:19]
buffer_load_dwordx4 v[92:95],v6,s[28:31],s79 offen offset:1024 sc1
ds_read_b64_tr_b16 a[170:171],v1 offset:25856
v_mfma_f32_16x16x32_bf16 a[16:19],a[184:187],v[192:195],a[16:19]
v_add_u32 v1,40960,v1
v_mfma_f32_16x16x32_bf16 a[16:19],a[188:191],v[224:227],a[16:19]
ds_read_b64_tr_b16 a[96:97],v1 offset:128
ds_read_b64_tr_b16 a[98:99],v1 offset:5248
v_mfma_f32_16x16x32_bf16 a[20:23],a[188:191],v[232:235],a[20:23]
ds_read_b64_tr_b16 a[100:101],v1 offset:10368
v_mfma_f32_16x16x32_bf16 a[20:23],a[184:187],v[200:203],a[20:23]
ds_read_b64_tr_b16 a[102:103],v1 offset:15488
v_mfma_f32_16x16x32_bf16 a[20:23],a[180:183],v[168:171],a[20:23]
ds_read_b64_tr_b16 a[104:105],v1 offset:20608
v_mfma_f32_16x16x32_bf16 a[20:23],a[176:179],v[136:139],a[20:23]
buffer_load_dwordx4 v[96:99],v6,s[28:31],s76 offen offset:2048 sc1
ds_read_b64_tr_b16 a[106:107],v1 offset:25728
v_mfma_f32_16x16x32_bf16 a[24:27],a[176:179],v[144:147],a[24:27]
ds_read_b64_tr_b16 a[108:109],v1 offset:30848
ds_read_b64_tr_b16 a[110:111],v1 offset:35968
v_mfma_f32_16x16x32_bf16 a[24:27],a[180:183],v[176:179],a[24:27]
s_lshl_b32 s95,s42,2
s_add_u32 s95,s95,s40
s_lshl_b32 s95,s95,4
v_mfma_f32_16x16x32_bf16 a[24:27],a[184:187],v[208:211],a[24:27]
v_mov_b32 v18,s68
v_mov_b32 v19,s69
v_mfma_f32_16x16x32_bf16 a[24:27],a[188:191],v[240:243],a[24:27]
v_mfma_f32_16x16x32_bf16 a[28:31],a[188:191],v[248:251],a[28:31]
ds_read_b64_tr_b16 a[188:189],v2 offset:31008
ds_read_b64_tr_b16 a[190:191],v2 offset:36128
v_mfma_f32_16x16x32_bf16 a[28:31],a[184:187],v[216:219],a[28:31]
buffer_load_dwordx4 v[100:103],v6,s[28:31],s77 offen offset:2048 sc1
ds_read_b64_tr_b16 a[184:185],v2 offset:20768
v_mfma_f32_16x16x32_bf16 a[28:31],a[180:183],v[184:187],a[28:31]
ds_read_b64_tr_b16 a[180:181],v2 offset:10528
v_mfma_f32_16x16x32_bf16 a[28:31],a[176:179],v[152:155],a[28:31]
s_waitcnt lgkmcnt(6)
ds_read_b64_tr_b16 a[176:177],v2 offset:288
v_mfma_f32_16x16x32_bf16 a[32:35],a[128:131],v[128:131],a[32:35]
ds_read_b64_tr_b16 a[178:179],v2 offset:5408
ds_read_b64_tr_b16 a[182:183],v2 offset:15648
v_mfma_f32_16x16x32_bf16 a[32:35],a[132:135],v[160:163],a[32:35]
s_waitcnt vmcnt(10)
s_mov_b64 exec,1
buffer_store_dwordx2 v[18:19],v6,s[64:67],s95 offen offset:1024 sc1
s_mov_b64 exec,-1
v_mfma_f32_16x16x32_bf16 a[32:35],a[136:139],v[192:195],a[32:35]
ds_read_b64_tr_b16 a[186:187],v2 offset:25888
v_add_u32 v2,40960,v2
v_mfma_f32_16x16x32_bf16 a[32:35],a[140:143],v[224:227],a[32:35]
ds_read_b64_tr_b16 a[112:113],v2 offset:160
ds_read_b64_tr_b16 a[114:115],v2 offset:5280
v_mfma_f32_16x16x32_bf16 a[36:39],a[140:143],v[232:235],a[36:39]
ds_read_b64_tr_b16 a[116:117],v2 offset:10400
v_mfma_f32_16x16x32_bf16 a[36:39],a[136:139],v[200:203],a[36:39]
buffer_load_dwordx4 v[104:107],v6,s[28:31],s78 offen offset:2048 sc1
ds_read_b64_tr_b16 a[118:119],v2 offset:15520
v_mfma_f32_16x16x32_bf16 a[36:39],a[132:135],v[168:171],a[36:39]
ds_read_b64_tr_b16 a[120:121],v2 offset:20640
v_mfma_f32_16x16x32_bf16 a[36:39],a[128:131],v[136:139],a[36:39]
ds_read_b64_tr_b16 a[122:123],v2 offset:25760
ds_read_b64_tr_b16 a[124:125],v2 offset:30880
v_mfma_f32_16x16x32_bf16 a[40:43],a[128:131],v[144:147],a[40:43]
ds_read_b64_tr_b16 a[126:127],v2 offset:36000
v_mfma_f32_16x16x32_bf16 a[40:43],a[132:135],v[176:179],a[40:43]
buffer_load_dwordx4 v[108:111],v6,s[28:31],s79 offen offset:2048 sc1
v_mfma_f32_16x16x32_bf16 a[40:43],a[136:139],v[208:211],a[40:43]
v_mfma_f32_16x16x32_bf16 a[40:43],a[140:143],v[240:243],a[40:43]
v_mfma_f32_16x16x32_bf16 a[44:47],a[140:143],v[248:251],a[44:47]
ds_read_b64_tr_b16 a[140:141],v1 offset:30720
ds_read_b64_tr_b16 a[142:143],v1 offset:35840
v_mfma_f32_16x16x32_bf16 a[44:47],a[136:139],v[216:219],a[44:47]
ds_read_b64_tr_b16 a[136:137],v1 offset:20480
v_mfma_f32_16x16x32_bf16 a[44:47],a[132:135],v[184:187],a[44:47]
ds_read_b64_tr_b16 a[132:133],v1 offset:10240
v_mfma_f32_16x16x32_bf16 a[44:47],a[128:131],v[152:155],a[44:47]
buffer_load_dwordx4 v[112:115],v6,s[28:31],s76 offen offset:3072 sc1
ds_read_b64_tr_b16 a[128:129],v1
v_mfma_f32_16x16x32_bf16 a[48:51],a[144:147],v[128:131],a[48:51]
ds_read_b64_tr_b16 a[130:131],v1 offset:5120
ds_read_b64_tr_b16 a[134:135],v1 offset:15360
v_mfma_f32_16x16x32_bf16 a[48:51],a[148:151],v[160:163],a[48:51]
s_cmp_ge_u32 2,s71
s_cselect_b32 s80,6,4
s_mul_i32 s80,s80,163840
v_mfma_f32_16x16x32_bf16 a[48:51],a[152:155],v[192:195],a[48:51]
s_add_u32 s80,s80,s81
s_add_u32 s83,s80,20480
ds_read_b64_tr_b16 a[138:139],v1 offset:25600
v_mfma_f32_16x16x32_bf16 a[48:51],a[156:159],v[224:227],a[48:51]
v_mfma_f32_16x16x32_bf16 a[52:55],a[156:159],v[232:235],a[52:55]
v_mfma_f32_16x16x32_bf16 a[52:55],a[152:155],v[200:203],a[52:55]
buffer_load_dwordx4 v[116:119],v6,s[28:31],s77 offen offset:3072 sc1
v_mfma_f32_16x16x32_bf16 a[52:55],a[148:151],v[168:171],a[52:55]
v_mfma_f32_16x16x32_bf16 a[52:55],a[144:147],v[136:139],a[52:55]
v_mfma_f32_16x16x32_bf16 a[56:59],a[144:147],v[144:147],a[56:59]
v_mfma_f32_16x16x32_bf16 a[56:59],a[148:151],v[176:179],a[56:59]
v_mfma_f32_16x16x32_bf16 a[56:59],a[152:155],v[208:211],a[56:59]
v_mfma_f32_16x16x32_bf16 a[56:59],a[156:159],v[240:243],a[56:59]
buffer_load_dwordx4 v[120:123],v6,s[28:31],s78 offen offset:3072 sc1
v_mfma_f32_16x16x32_bf16 a[60:63],a[156:159],v[248:251],a[60:63]
ds_read_b64_tr_b16 a[156:157],v2 offset:30752
ds_read_b64_tr_b16 a[158:159],v2 offset:35872
v_mfma_f32_16x16x32_bf16 a[60:63],a[152:155],v[216:219],a[60:63]
ds_read_b64_tr_b16 a[152:153],v2 offset:20512
v_mfma_f32_16x16x32_bf16 a[60:63],a[148:151],v[184:187],a[60:63]
ds_read_b64_tr_b16 a[148:149],v2 offset:10272
v_mfma_f32_16x16x32_bf16 a[60:63],a[144:147],v[152:155],a[60:63]
ds_read_b64_tr_b16 a[144:145],v2 offset:32
v_mfma_f32_16x16x32_bf16 a[64:67],a[160:163],v[128:131],a[64:67]
ds_read_b64_tr_b16 a[146:147],v2 offset:5152
ds_read_b64_tr_b16 a[150:151],v2 offset:15392
v_mfma_f32_16x16x32_bf16 a[64:67],a[164:167],v[160:163],a[64:67]
buffer_load_dwordx4 v[124:127],v6,s[28:31],s79 offen offset:3072 sc1
ds_read_b64_tr_b16 a[154:155],v2 offset:25632
v_mfma_f32_16x16x32_bf16 a[64:67],a[168:171],v[192:195],a[64:67]
v_mfma_f32_16x16x32_bf16 a[64:67],a[172:175],v[224:227],a[64:67]
v_mfma_f32_16x16x32_bf16 a[68:71],a[172:175],v[232:235],a[68:71]
v_mfma_f32_16x16x32_bf16 a[68:71],a[168:171],v[200:203],a[68:71]
v_mfma_f32_16x16x32_bf16 a[68:71],a[164:167],v[168:171],a[68:71]
s_add_u32 m0,s82,0
v_mfma_f32_16x16x32_bf16 a[68:71],a[160:163],v[136:139],a[68:71]
v_mfma_f32_16x16x32_bf16 a[72:75],a[160:163],v[144:147],a[72:75]
v_mfma_f32_16x16x32_bf16 a[72:75],a[164:167],v[176:179],a[72:75]
buffer_load_dwordx4 v7,s[72:75],s80 offen lds
v_mfma_f32_16x16x32_bf16 a[72:75],a[168:171],v[208:211],a[72:75]
v_mfma_f32_16x16x32_bf16 a[72:75],a[172:175],v[240:243],a[72:75]
v_mfma_f32_16x16x32_bf16 a[76:79],a[172:175],v[248:251],a[76:79]
ds_read_b64_tr_b16 a[172:173],v1 offset:30784
ds_read_b64_tr_b16 a[174:175],v1 offset:35904
v_mfma_f32_16x16x32_bf16 a[76:79],a[168:171],v[216:219],a[76:79]
ds_read_b64_tr_b16 a[168:169],v1 offset:20544
v_mfma_f32_16x16x32_bf16 a[76:79],a[164:167],v[184:187],a[76:79]
ds_read_b64_tr_b16 a[164:165],v1 offset:10304
v_mfma_f32_16x16x32_bf16 a[76:79],a[160:163],v[152:155],a[76:79]
s_waitcnt lgkmcnt(4)
buffer_load_dwordx4 v8,s[72:75],s80 offen offset:1024 lds
ds_read_b64_tr_b16 a[160:161],v1 offset:64
v_mfma_f32_16x16x32_bf16 a[80:83],a[176:179],v[128:131],a[80:83]
ds_read_b64_tr_b16 a[162:163],v1 offset:5184
ds_read_b64_tr_b16 a[166:167],v1 offset:15424
v_mfma_f32_16x16x32_bf16 a[80:83],a[180:183],v[160:163],a[80:83]
ds_read_b64_tr_b16 a[170:171],v1 offset:25664
v_mfma_f32_16x16x32_bf16 a[80:83],a[184:187],v[192:195],a[80:83]
v_mfma_f32_16x16x32_bf16 a[80:83],a[188:191],v[224:227],a[80:83]
v_mfma_f32_16x16x32_bf16 a[84:87],a[188:191],v[232:235],a[84:87]
v_mfma_f32_16x16x32_bf16 a[84:87],a[184:187],v[200:203],a[84:87]
buffer_load_dwordx4 v9,s[72:75],s80 offen offset:2048 lds
v_mfma_f32_16x16x32_bf16 a[84:87],a[180:183],v[168:171],a[84:87]
v_mfma_f32_16x16x32_bf16 a[84:87],a[176:179],v[136:139],a[84:87]
v_mfma_f32_16x16x32_bf16 a[88:91],a[176:179],v[144:147],a[88:91]
v_mfma_f32_16x16x32_bf16 a[88:91],a[180:183],v[176:179],a[88:91]
v_mfma_f32_16x16x32_bf16 a[88:91],a[184:187],v[208:211],a[88:91]
v_mfma_f32_16x16x32_bf16 a[88:91],a[188:191],v[240:243],a[88:91]
buffer_load_dwordx4 v10,s[72:75],s80 offen offset:3072 lds
v_mfma_f32_16x16x32_bf16 a[92:95],a[188:191],v[248:251],a[92:95]
ds_read_b64_tr_b16 a[188:189],v2 offset:30816
ds_read_b64_tr_b16 a[190:191],v2 offset:35936
v_mfma_f32_16x16x32_bf16 a[92:95],a[184:187],v[216:219],a[92:95]
ds_read_b64_tr_b16 a[184:185],v2 offset:20576
v_mfma_f32_16x16x32_bf16 a[92:95],a[180:183],v[184:187],a[92:95]
s_add_u32 m0,s82,4096
ds_read_b64_tr_b16 a[180:181],v2 offset:10336
v_mfma_f32_16x16x32_bf16 a[92:95],a[176:179],v[152:155],a[92:95]
ds_read_b64_tr_b16 a[176:177],v2 offset:96
v_mfma_f32_16x16x32_bf16 a[192:195],a[128:131],v[132:135],a[192:195]
s_lshl_b32 s43,s41,2
ds_read_b64_tr_b16 a[178:179],v2 offset:5216
ds_read_b64_tr_b16 a[182:183],v2 offset:15456
v_mfma_f32_16x16x32_bf16 a[192:195],a[132:135],v[140:143],a[192:195]
s_add_u32 s43,s43,s71
buffer_load_dwordx4 v11,s[72:75],s80 offen lds
ds_read_b64_tr_b16 a[186:187],v2 offset:25696
v_mfma_f32_16x16x32_bf16 a[192:195],a[136:139],v[148:151],a[192:195]
s_add_u32 s84,s70,1
s_add_u32 s85,s70,2
s_add_u32 s86,s70,3
v_mfma_f32_16x16x32_bf16 a[192:195],a[140:143],v[156:159],a[192:195]
)A"
R"A(s_mul_i32 s43,s43,245760
s_and_b32 s84,s84,3
s_and_b32 s85,s85,3
v_mfma_f32_16x16x32_bf16 a[196:199],a[140:143],v[188:191],a[196:199]
s_add_u32 m0,s82,5120
s_and_b32 s86,s86,3
s_add_u32 s43,s43,134217728
v_mfma_f32_16x16x32_bf16 a[196:199],a[136:139],v[180:183],a[196:199]
s_mov_b64 s[32:33],%4
s_mul_i32 s84,s84,12
s_mul_i32 s85,s85,12
v_mfma_f32_16x16x32_bf16 a[196:199],a[132:135],v[172:175],a[196:199]
s_mul_i32 s86,s86,12
s_add_u32 s84,s84,0
s_add_u32 s85,s85,4
v_mfma_f32_16x16x32_bf16 a[196:199],a[128:131],v[164:167],a[196:199]
s_add_u32 s86,s86,8
s_add_u32 s84,s84,s40
buffer_load_dwordx4 v7,s[72:75],s83 offen lds
v_mfma_f32_16x16x32_bf16 a[200:203],a[128:131],v[196:199],a[200:203]
s_add_u32 s32,s32,s43
s_addc_u32 s33,s33,0
s_add_u32 s85,s85,s40
v_mfma_f32_16x16x32_bf16 a[200:203],a[132:135],v[204:207],a[200:203]
s_add_u32 s86,s86,s40
s_and_b32 s33,s33,0xffff
s_mov_b32 s34,245760
v_mfma_f32_16x16x32_bf16 a[200:203],a[136:139],v[212:215],a[200:203]
s_mov_b32 s35,0x20000
s_mul_i32 s84,s84,5120
s_mul_i32 s85,s85,5120
v_mfma_f32_16x16x32_bf16 a[200:203],a[140:143],v[220:223],a[200:203]
s_mul_i32 s86,s86,5120
v_bfe_u32 v18,v6,6,2
v_lshrrev_b32 v19,8,v6
v_mfma_f32_16x16x32_bf16 a[204:207],a[140:143],v[252:255],a[204:207]
s_mul_i32 s44,s41,307200
ds_read_b64_tr_b16 a[140:141],v1 offset:30912
ds_read_b64_tr_b16 a[142:143],v1 offset:36032
v_mfma_f32_16x16x32_bf16 a[204:207],a[136:139],v[244:247],a[204:207]
s_mov_b64 s[20:21],%8
buffer_load_dwordx4 v8,s[72:75],s83 offen offset:1024 lds
ds_read_b64_tr_b16 a[136:137],v1 offset:20672
v_mfma_f32_16x16x32_bf16 a[204:207],a[132:135],v[236:239],a[204:207]
s_lshl_b32 s43,s40,2
ds_read_b64_tr_b16 a[132:133],v1 offset:10432
v_xor_b32 v18,v18,v19
v_mfma_f32_16x16x32_bf16 a[204:207],a[128:131],v[228:231],a[204:207]
s_add_u32 s43,s43,s70
ds_read_b64_tr_b16 a[128:129],v1 offset:192
v_lshlrev_b32 v18,2,v18
s_barrier
v_mfma_f32_16x16x32_bf16 a[208:211],a[144:147],v[132:135],a[208:211]
s_cmp_ge_u32 2,s71
s_cselect_b32 s76,6,4
s_lshl_b32 s76,s76,16
v_mfma_f32_16x16x32_bf16 a[208:211],a[148:151],v[140:143],a[208:211]
s_add_u32 s77,s76,4096
s_add_u32 s78,s76,8192
s_add_u32 s79,s76,12288
v_mfma_f32_16x16x32_bf16 a[208:211],a[152:155],v[148:151],a[208:211]
s_mul_i32 s43,s43,20480
buffer_load_dwordx4 v9,s[72:75],s83 offen offset:2048 lds
ds_read_b64_tr_b16 a[130:131],v1 offset:5312
v_mfma_f32_16x16x32_bf16 a[208:211],a[156:159],v[156:159],a[208:211]
s_mov_b32 s22,307200
ds_read_b64_tr_b16 a[134:135],v1 offset:15552
ds_read_b64_tr_b16 a[138:139],v1 offset:25792
v_mfma_f32_16x16x32_bf16 a[212:215],a[156:159],v[188:191],a[212:215]
s_mov_b32 s23,0x20000
v_bfe_u32 v19,v6,4,2
v_or_b32 v18,v18,v19
v_mfma_f32_16x16x32_bf16 a[212:215],a[152:155],v[180:183],a[212:215]
v_and_b32 v19,16,v6
v_mov_b32 v34,0x3f80
v_mfma_f32_16x16x32_bf16 a[212:215],a[148:151],v[172:175],a[212:215]
v_lshlrev_b32 v19,v19,v34
v_lshrrev_b32 v18,1,v18
v_mfma_f32_16x16x32_bf16 a[212:215],a[144:147],v[164:167],a[212:215]
v_cmp_eq_u32 vcc,0,v18
s_nop 1
v_cndmask_b32 v34,0,v19,vcc
v_mfma_f32_16x16x32_bf16 a[216:219],a[144:147],v[196:199],a[216:219]
buffer_load_dwordx4 v10,s[72:75],s83 offen offset:3072 lds
v_add_u32 v25,4096,v6
v_mfma_f32_16x16x32_bf16 a[216:219],a[148:151],v[204:207],a[216:219]
v_cmp_eq_u32 vcc,1,v18
s_nop 1
v_cndmask_b32 v35,0,v19,vcc
v_mfma_f32_16x16x32_bf16 a[216:219],a[152:155],v[212:215],a[216:219]
v_mov_b32 v36,0
v_mov_b32 v37,0
v_mfma_f32_16x16x32_bf16 a[216:219],a[156:159],v[220:223],a[216:219]
s_add_u32 m0,s82,9216
v_mov_b32 v32,0
v_mov_b32 v33,0
v_mfma_f32_16x16x32_bf16 a[220:223],a[156:159],v[252:255],a[220:223]
ds_read_b64_tr_b16 a[156:157],v2 offset:30944
ds_read_b64_tr_b16 a[158:159],v2 offset:36064
v_mfma_f32_16x16x32_bf16 a[220:223],a[152:155],v[244:247],a[220:223]
ds_read_b64_tr_b16 a[152:153],v2 offset:20704
v_bfe_u32 v40,v6,4,4
v_mfma_f32_16x16x32_bf16 a[220:223],a[148:151],v[236:239],a[220:223]
buffer_load_dwordx4 v11,s[72:75],s83 offen lds
ds_read_b64_tr_b16 a[148:149],v2 offset:10464
v_mfma_f32_16x16x32_bf16 a[220:223],a[144:147],v[228:231],a[220:223]
s_add_u32 s20,s20,s44
s_addc_u32 s21,s21,0
ds_read_b64_tr_b16 a[144:145],v2 offset:224
s_waitcnt lgkmcnt(5)
v_mfma_f32_16x16x32_bf16 a[224:227],a[160:163],v[132:135],a[224:227]
s_mul_i32 s44,s71,320
ds_read_b64_tr_b16 a[146:147],v2 offset:5344
ds_read_b64_tr_b16 a[150:151],v2 offset:15584
v_mfma_f32_16x16x32_bf16 a[224:227],a[164:167],v[140:143],a[224:227]
s_and_b32 s21,s21,0xffff
ds_read_b64_tr_b16 a[154:155],v2 offset:25824
v_mul_u32_u24 v40,1280,v40
v_mfma_f32_16x16x32_bf16 a[224:227],a[168:171],v[148:151],a[224:227]
s_add_u32 s43,s43,s44
v_lshrrev_b32 v41,8,v6
v_lshl_add_u32 v40,v41,3,v40
v_mfma_f32_16x16x32_bf16 a[224:227],a[172:175],v[156:159],a[224:227]
v_mfma_f32_16x16x32_bf16 a[228:231],a[172:175],v[188:191],a[228:231]
v_mfma_f32_16x16x32_bf16 a[228:231],a[168:171],v[180:183],a[228:231]
v_mfma_f32_16x16x32_bf16 a[228:231],a[164:167],v[172:175],a[228:231]
v_mfma_f32_16x16x32_bf16 a[228:231],a[160:163],v[164:167],a[228:231]
buffer_load_dwordx4 v[128:131],v6,s[28:31],s76 offen sc1
v_mfma_f32_16x16x32_bf16 a[232:235],a[160:163],v[196:199],a[232:235]
v_mfma_f32_16x16x32_bf16 a[232:235],a[164:167],v[204:207],a[232:235]
v_mfma_f32_16x16x32_bf16 a[232:235],a[168:171],v[212:215],a[232:235]
v_mfma_f32_16x16x32_bf16 a[232:235],a[172:175],v[220:223],a[232:235]
v_mfma_f32_16x16x32_bf16 a[236:239],a[172:175],v[252:255],a[236:239]
ds_read_b64_tr_b16 a[172:173],v1 offset:30976
ds_read_b64_tr_b16 a[174:175],v1 offset:36096
v_mfma_f32_16x16x32_bf16 a[236:239],a[168:171],v[244:247],a[236:239]
buffer_load_dwordx4 v[160:163],v6,s[28:31],s77 offen sc1
ds_read_b64_tr_b16 a[168:169],v1 offset:20736
v_mfma_f32_16x16x32_bf16 a[236:239],a[164:167],v[236:239],a[236:239]
ds_read_b64_tr_b16 a[164:165],v1 offset:10496
v_mfma_f32_16x16x32_bf16 a[236:239],a[160:163],v[228:231],a[236:239]
ds_read_b64_tr_b16 a[160:161],v1 offset:256
v_mfma_f32_16x16x32_bf16 a[240:243],a[176:179],v[132:135],a[240:243]
ds_read_b64_tr_b16 a[162:163],v1 offset:5376
ds_read_b64_tr_b16 a[166:167],v1 offset:15616
v_mfma_f32_16x16x32_bf16 a[240:243],a[180:183],v[140:143],a[240:243]
ds_read_b64_tr_b16 a[170:171],v1 offset:25856
v_add_u32 v1,40960,v1
v_mfma_f32_16x16x32_bf16 a[240:243],a[184:187],v[148:151],a[240:243]
v_mfma_f32_16x16x32_bf16 a[240:243],a[188:191],v[156:159],a[240:243]
buffer_load_dwordx4 v[192:195],v6,s[28:31],s78 offen sc1
v_mfma_f32_16x16x32_bf16 a[244:247],a[188:191],v[188:191],a[244:247]
v_mfma_f32_16x16x32_bf16 a[244:247],a[184:187],v[180:183],a[244:247]
v_mfma_f32_16x16x32_bf16 a[244:247],a[180:183],v[172:175],a[244:247]
v_mfma_f32_16x16x32_bf16 a[244:247],a[176:179],v[164:167],a[244:247]
v_mfma_f32_16x16x32_bf16 a[248:251],a[176:179],v[196:199],a[248:251]
v_mfma_f32_16x16x32_bf16 a[248:251],a[180:183],v[204:207],a[248:251]
buffer_load_dwordx4 v[224:227],v6,s[28:31],s79 offen sc1
v_mfma_f32_16x16x32_bf16 a[248:251],a[184:187],v[212:215],a[248:251]
v_mfma_f32_16x16x32_bf16 a[248:251],a[188:191],v[220:223],a[248:251]
v_mfma_f32_16x16x32_bf16 a[252:255],a[188:191],v[252:255],a[252:255]
ds_read_b64_tr_b16 a[188:189],v2 offset:31008
ds_read_b64_tr_b16 a[190:191],v2 offset:36128
v_mfma_f32_16x16x32_bf16 a[252:255],a[184:187],v[244:247],a[252:255]
ds_read_b64_tr_b16 a[184:185],v2 offset:20768
v_mfma_f32_16x16x32_bf16 a[252:255],a[180:183],v[236:239],a[252:255]
ds_read_b64_tr_b16 a[180:181],v2 offset:10528
v_mfma_f32_16x16x32_bf16 a[252:255],a[176:179],v[228:231],a[252:255]
buffer_load_dwordx4 v[136:139],v6,s[28:31],s76 offen offset:1024 sc1
ds_read_b64_tr_b16 a[176:177],v2 offset:288
v_mfma_f32_16x16x32_bf16 a[0:3],a[96:99],v[132:135],a[0:3]
ds_read_b64_tr_b16 a[178:179],v2 offset:5408
ds_read_b64_tr_b16 a[182:183],v2 offset:15648
v_mfma_f32_16x16x32_bf16 a[0:3],a[100:103],v[140:143],a[0:3]
ds_read_b64_tr_b16 a[186:187],v2 offset:25888
v_add_u32 v2,40960,v2
v_mfma_f32_16x16x32_bf16 a[0:3],a[104:107],v[148:151],a[0:3]
v_mfma_f32_16x16x32_bf16 a[0:3],a[108:111],v[156:159],a[0:3]
v_mfma_f32_16x16x32_bf16 a[4:7],a[108:111],v[188:191],a[4:7]
v_mfma_f32_16x16x32_bf16 a[4:7],a[104:107],v[180:183],a[4:7]
buffer_load_dwordx4 v[168:171],v6,s[28:31],s77 offen offset:1024 sc1
v_mfma_f32_16x16x32_bf16 a[4:7],a[100:103],v[172:175],a[4:7]
v_mfma_f32_16x16x32_bf16 a[4:7],a[96:99],v[164:167],a[4:7]
v_mfma_f32_16x16x32_bf16 a[8:11],a[96:99],v[196:199],a[8:11]
v_mfma_f32_16x16x32_bf16 a[8:11],a[100:103],v[204:207],a[8:11]
v_mfma_f32_16x16x32_bf16 a[8:11],a[104:107],v[212:215],a[8:11]
v_mfma_f32_16x16x32_bf16 a[8:11],a[108:111],v[220:223],a[8:11]
buffer_load_dwordx4 v[200:203],v6,s[28:31],s78 offen offset:1024 sc1
v_mfma_f32_16x16x32_bf16 a[12:15],a[108:111],v[252:255],a[12:15]
ds_read_b64_tr_b16 a[108:109],v1 offset:30720
ds_read_b64_tr_b16 a[110:111],v1 offset:35840
v_mfma_f32_16x16x32_bf16 a[12:15],a[104:107],v[244:247],a[12:15]
ds_read_b64_tr_b16 a[104:105],v1 offset:20480
v_mfma_f32_16x16x32_bf16 a[12:15],a[100:103],v[236:239],a[12:15]
ds_read_b64_tr_b16 a[100:101],v1 offset:10240
v_mfma_f32_16x16x32_bf16 a[12:15],a[96:99],v[228:231],a[12:15]
ds_read_b64_tr_b16 a[96:97],v1
v_mfma_f32_16x16x32_bf16 a[16:19],a[112:115],v[132:135],a[16:19]
ds_read_b64_tr_b16 a[98:99],v1 offset:5120
ds_read_b64_tr_b16 a[102:103],v1 offset:15360
v_mfma_f32_16x16x32_bf16 a[16:19],a[116:119],v[140:143],a[16:19]
buffer_load_dwordx4 v[232:235],v6,s[28:31],s79 offen offset:1024 sc1
ds_read_b64_tr_b16 a[106:107],v1 offset:25600
v_mfma_f32_16x16x32_bf16 a[16:19],a[120:123],v[148:151],a[16:19]
v_mfma_f32_16x16x32_bf16 a[16:19],a[124:127],v[156:159],a[16:19]
v_mfma_f32_16x16x32_bf16 a[20:23],a[124:127],v[188:191],a[20:23]
v_mfma_f32_16x16x32_bf16 a[20:23],a[120:123],v[180:183],a[20:23]
v_mfma_f32_16x16x32_bf16 a[20:23],a[116:119],v[172:175],a[20:23]
v_mfma_f32_16x16x32_bf16 a[20:23],a[112:115],v[164:167],a[20:23]
buffer_load_dwordx4 v[144:147],v6,s[28:31],s76 offen offset:2048 sc1
v_mfma_f32_16x16x32_bf16 a[24:27],a[112:115],v[196:199],a[24:27]
v_mfma_f32_16x16x32_bf16 a[24:27],a[116:119],v[204:207],a[24:27]
v_mfma_f32_16x16x32_bf16 a[24:27],a[120:123],v[212:215],a[24:27]
v_mfma_f32_16x16x32_bf16 a[24:27],a[124:127],v[220:223],a[24:27]
v_mfma_f32_16x16x32_bf16 a[28:31],a[124:127],v[252:255],a[28:31]
ds_read_b64_tr_b16 a[124:125],v2 offset:30752
ds_read_b64_tr_b16 a[126:127],v2 offset:35872
v_mfma_f32_16x16x32_bf16 a[28:31],a[120:123],v[244:247],a[28:31]
buffer_load_dwordx4 v[176:179],v6,s[28:31],s77 offen offset:2048 sc1
ds_read_b64_tr_b16 a[120:121],v2 offset:20512
v_mfma_f32_16x16x32_bf16 a[28:31],a[116:119],v[236:239],a[28:31]
ds_read_b64_tr_b16 a[116:117],v2 offset:10272
v_mfma_f32_16x16x32_bf16 a[28:31],a[112:115],v[228:231],a[28:31]
s_lshl_b32 s94,s70,4
v_bfe_u32 v24,v6,4,2
v_min_u32 v24,2,v24
v_mfma_f32_16x16x32_bf16 a[32:35],a[128:131],v[132:135],a[32:35]
s_add_u32 s94,s94,s40
v_add_u32 v24,s71,v24
v_add_u32 v24,1,v24
v_mfma_f32_16x16x32_bf16 a[32:35],a[132:135],v[140:143],a[32:35]
s_lshl_b32 s94,s94,4
v_and_b32 v24,3,v24
v_lshlrev_b32 v24,6,v24
v_mfma_f32_16x16x32_bf16 a[32:35],a[136:139],v[148:151],a[32:35]
ds_read_b64_tr_b16 a[112:113],v2 offset:32
ds_read_b64_tr_b16 a[114:115],v2 offset:5152
v_mfma_f32_16x16x32_bf16 a[32:35],a[140:143],v[156:159],a[32:35]
buffer_load_dwordx4 v[208:211],v6,s[28:31],s78 offen offset:2048 sc1
ds_read_b64_tr_b16 a[118:119],v2 offset:15392
v_mfma_f32_16x16x32_bf16 a[36:39],a[140:143],v[188:191],a[36:39]
ds_read_b64_tr_b16 a[122:123],v2 offset:25632
v_mfma_f32_16x16x32_bf16 a[36:39],a[136:139],v[180:183],a[36:39]
v_mfma_f32_16x16x32_bf16 a[36:39],a[132:135],v[172:175],a[36:39]
v_mfma_f32_16x16x32_bf16 a[36:39],a[128:131],v[164:167],a[36:39]
v_mfma_f32_16x16x32_bf16 a[40:43],a[128:131],v[196:199],a[40:43]
v_mfma_f32_16x16x32_bf16 a[40:43],a[132:135],v[204:207],a[40:43]
buffer_load_dwordx4 v[240:243],v6,s[28:31],s79 offen offset:2048 sc1
v_mfma_f32_16x16x32_bf16 a[40:43],a[136:139],v[212:215],a[40:43]
v_mfma_f32_16x16x32_bf16 a[40:43],a[140:143],v[220:223],a[40:43]
v_mfma_f32_16x16x32_bf16 a[44:47],a[140:143],v[252:255],a[44:47]
ds_read_b64_tr_b16 a[140:141],v1 offset:30784
)A"
R"A(ds_read_b64_tr_b16 a[142:143],v1 offset:35904
v_mfma_f32_16x16x32_bf16 a[44:47],a[136:139],v[244:247],a[44:47]
ds_read_b64_tr_b16 a[136:137],v1 offset:20544
v_mfma_f32_16x16x32_bf16 a[44:47],a[132:135],v[236:239],a[44:47]
ds_read_b64_tr_b16 a[132:133],v1 offset:10304
v_mfma_f32_16x16x32_bf16 a[44:47],a[128:131],v[228:231],a[44:47]
s_waitcnt lgkmcnt(4)
buffer_load_dwordx4 v[152:155],v6,s[28:31],s76 offen offset:3072 sc1
ds_read_b64_tr_b16 a[128:129],v1 offset:64
v_mfma_f32_16x16x32_bf16 a[48:51],a[144:147],v[132:135],a[48:51]
ds_read_b64_tr_b16 a[130:131],v1 offset:5184
ds_read_b64_tr_b16 a[134:135],v1 offset:15424
v_mfma_f32_16x16x32_bf16 a[48:51],a[148:151],v[140:143],a[48:51]
s_cmp_ge_u32 0,s71
s_cselect_b32 s80,3,1
s_mul_i32 s80,s80,163840
v_mfma_f32_16x16x32_bf16 a[48:51],a[152:155],v[148:151],a[48:51]
s_add_u32 s80,s80,s81
s_add_u32 s83,s80,20480
ds_read_b64_tr_b16 a[138:139],v1 offset:25664
v_mfma_f32_16x16x32_bf16 a[48:51],a[156:159],v[156:159],a[48:51]
v_mfma_f32_16x16x32_bf16 a[52:55],a[156:159],v[188:191],a[52:55]
v_mfma_f32_16x16x32_bf16 a[52:55],a[152:155],v[180:183],a[52:55]
buffer_load_dwordx4 v[184:187],v6,s[28:31],s77 offen offset:3072 sc1
v_mfma_f32_16x16x32_bf16 a[52:55],a[148:151],v[172:175],a[52:55]
v_mfma_f32_16x16x32_bf16 a[52:55],a[144:147],v[164:167],a[52:55]
v_mfma_f32_16x16x32_bf16 a[56:59],a[144:147],v[196:199],a[56:59]
v_mfma_f32_16x16x32_bf16 a[56:59],a[148:151],v[204:207],a[56:59]
v_mfma_f32_16x16x32_bf16 a[56:59],a[152:155],v[212:215],a[56:59]
v_mfma_f32_16x16x32_bf16 a[56:59],a[156:159],v[220:223],a[56:59]
buffer_load_dwordx4 v[216:219],v6,s[28:31],s78 offen offset:3072 sc1
v_mfma_f32_16x16x32_bf16 a[60:63],a[156:159],v[252:255],a[60:63]
ds_read_b64_tr_b16 a[156:157],v2 offset:30816
ds_read_b64_tr_b16 a[158:159],v2 offset:35936
v_mfma_f32_16x16x32_bf16 a[60:63],a[152:155],v[244:247],a[60:63]
ds_read_b64_tr_b16 a[152:153],v2 offset:20576
v_mfma_f32_16x16x32_bf16 a[60:63],a[148:151],v[236:239],a[60:63]
ds_read_b64_tr_b16 a[148:149],v2 offset:10336
v_mfma_f32_16x16x32_bf16 a[60:63],a[144:147],v[228:231],a[60:63]
ds_read_b64_tr_b16 a[144:145],v2 offset:96
v_mfma_f32_16x16x32_bf16 a[64:67],a[160:163],v[132:135],a[64:67]
ds_read_b64_tr_b16 a[146:147],v2 offset:5216
ds_read_b64_tr_b16 a[150:151],v2 offset:15456
v_mfma_f32_16x16x32_bf16 a[64:67],a[164:167],v[140:143],a[64:67]
buffer_load_dwordx4 v[248:251],v6,s[28:31],s79 offen offset:3072 sc1
ds_read_b64_tr_b16 a[154:155],v2 offset:25696
v_mfma_f32_16x16x32_bf16 a[64:67],a[168:171],v[148:151],a[64:67]
v_mfma_f32_16x16x32_bf16 a[64:67],a[172:175],v[156:159],a[64:67]
v_mfma_f32_16x16x32_bf16 a[68:71],a[172:175],v[188:191],a[68:71]
v_mfma_f32_16x16x32_bf16 a[68:71],a[168:171],v[180:183],a[68:71]
v_mfma_f32_16x16x32_bf16 a[68:71],a[164:167],v[172:175],a[68:71]
s_add_u32 m0,s82,40960
v_mfma_f32_16x16x32_bf16 a[68:71],a[160:163],v[164:167],a[68:71]
buffer_load_dwordx2 v[26:27],v24,s[64:67],s94 offen offset:1024 sc1
v_mfma_f32_16x16x32_bf16 a[72:75],a[160:163],v[196:199],a[72:75]
v_mfma_f32_16x16x32_bf16 a[72:75],a[164:167],v[204:207],a[72:75]
v_mfma_f32_16x16x32_bf16 a[72:75],a[168:171],v[212:215],a[72:75]
v_mfma_f32_16x16x32_bf16 a[72:75],a[172:175],v[220:223],a[72:75]
buffer_load_dwordx4 v7,s[72:75],s80 offen lds
v_mfma_f32_16x16x32_bf16 a[76:79],a[172:175],v[252:255],a[76:79]
ds_read_b64_tr_b16 a[172:173],v1 offset:30848
ds_read_b64_tr_b16 a[174:175],v1 offset:35968
v_mfma_f32_16x16x32_bf16 a[76:79],a[168:171],v[244:247],a[76:79]
ds_read_b64_tr_b16 a[168:169],v1 offset:20608
v_mfma_f32_16x16x32_bf16 a[76:79],a[164:167],v[236:239],a[76:79]
ds_read_b64_tr_b16 a[164:165],v1 offset:10368
v_mfma_f32_16x16x32_bf16 a[76:79],a[160:163],v[228:231],a[76:79]
buffer_load_dwordx4 v8,s[72:75],s80 offen offset:1024 lds
ds_read_b64_tr_b16 a[160:161],v1 offset:128
v_mfma_f32_16x16x32_bf16 a[80:83],a[176:179],v[132:135],a[80:83]
ds_read_b64_tr_b16 a[162:163],v1 offset:5248
ds_read_b64_tr_b16 a[166:167],v1 offset:15488
v_mfma_f32_16x16x32_bf16 a[80:83],a[180:183],v[140:143],a[80:83]
ds_read_b64_tr_b16 a[170:171],v1 offset:25728
v_mfma_f32_16x16x32_bf16 a[80:83],a[184:187],v[148:151],a[80:83]
v_mfma_f32_16x16x32_bf16 a[80:83],a[188:191],v[156:159],a[80:83]
v_mfma_f32_16x16x32_bf16 a[84:87],a[188:191],v[188:191],a[84:87]
v_mfma_f32_16x16x32_bf16 a[84:87],a[184:187],v[180:183],a[84:87]
buffer_load_dwordx4 v9,s[72:75],s80 offen offset:2048 lds
v_mfma_f32_16x16x32_bf16 a[84:87],a[180:183],v[172:175],a[84:87]
v_mfma_f32_16x16x32_bf16 a[84:87],a[176:179],v[164:167],a[84:87]
v_mfma_f32_16x16x32_bf16 a[88:91],a[176:179],v[196:199],a[88:91]
v_mfma_f32_16x16x32_bf16 a[88:91],a[180:183],v[204:207],a[88:91]
v_mfma_f32_16x16x32_bf16 a[88:91],a[184:187],v[212:215],a[88:91]
v_mfma_f32_16x16x32_bf16 a[88:91],a[188:191],v[220:223],a[88:91]
buffer_load_dwordx4 v10,s[72:75],s80 offen offset:3072 lds
v_mfma_f32_16x16x32_bf16 a[92:95],a[188:191],v[252:255],a[92:95]
ds_read_b64_tr_b16 a[188:189],v2 offset:30880
ds_read_b64_tr_b16 a[190:191],v2 offset:36000
v_mfma_f32_16x16x32_bf16 a[92:95],a[184:187],v[244:247],a[92:95]
ds_read_b64_tr_b16 a[184:185],v2 offset:20640
v_mfma_f32_16x16x32_bf16 a[92:95],a[180:183],v[236:239],a[92:95]
s_add_u32 m0,s82,45056
ds_read_b64_tr_b16 a[180:181],v2 offset:10400
v_mfma_f32_16x16x32_bf16 a[92:95],a[176:179],v[228:231],a[92:95]
s_waitcnt vmcnt(12)
ds_read_b64_tr_b16 a[176:177],v2 offset:160
v_mfma_f32_16x16x32_bf16 a[192:195],a[96:99],v[64:67],a[192:195]
ds_read_b64_tr_b16 a[178:179],v2 offset:5280
ds_read_b64_tr_b16 a[182:183],v2 offset:15520
v_mfma_f32_16x16x32_bf16 a[192:195],a[100:103],v[68:71],a[192:195]
buffer_load_dwordx4 v11,s[72:75],s80 offen lds
ds_read_b64_tr_b16 a[186:187],v2 offset:25760
v_mfma_f32_16x16x32_bf16 a[192:195],a[104:107],v[72:75],a[192:195]
v_mfma_f32_16x16x32_bf16 a[192:195],a[108:111],v[76:79],a[192:195]
v_mfma_f32_16x16x32_bf16 a[196:199],a[108:111],v[92:95],a[196:199]
s_add_u32 m0,s82,46080
v_mfma_f32_16x16x32_bf16 a[196:199],a[104:107],v[88:91],a[196:199]
v_mfma_f32_16x16x32_bf16 a[196:199],a[100:103],v[84:87],a[196:199]
v_mfma_f32_16x16x32_bf16 a[196:199],a[96:99],v[80:83],a[196:199]
buffer_load_dwordx4 v7,s[72:75],s83 offen lds
v_mfma_f32_16x16x32_bf16 a[200:203],a[96:99],v[96:99],a[200:203]
v_mfma_f32_16x16x32_bf16 a[200:203],a[100:103],v[100:103],a[200:203]
v_mfma_f32_16x16x32_bf16 a[200:203],a[104:107],v[104:107],a[200:203]
v_mfma_f32_16x16x32_bf16 a[200:203],a[108:111],v[108:111],a[200:203]
v_mfma_f32_16x16x32_bf16 a[204:207],a[108:111],v[124:127],a[204:207]
ds_read_b64_tr_b16 a[108:109],v1 offset:30912
ds_read_b64_tr_b16 a[110:111],v1 offset:36032
v_mfma_f32_16x16x32_bf16 a[204:207],a[104:107],v[120:123],a[204:207]
buffer_load_dwordx4 v8,s[72:75],s83 offen offset:1024 lds
ds_read_b64_tr_b16 a[104:105],v1 offset:20672
v_mfma_f32_16x16x32_bf16 a[204:207],a[100:103],v[116:119],a[204:207]
ds_read_b64_tr_b16 a[100:101],v1 offset:10432
v_mfma_f32_16x16x32_bf16 a[204:207],a[96:99],v[112:115],a[204:207]
ds_read_b64_tr_b16 a[96:97],v1 offset:192
s_barrier
v_mfma_f32_16x16x32_bf16 a[208:211],a[112:115],v[64:67],a[208:211]
s_cmp_ge_u32 0,s71
s_cselect_b32 s76,3,1
s_lshl_b32 s76,s76,16
v_mfma_f32_16x16x32_bf16 a[208:211],a[116:119],v[68:71],a[208:211]
s_add_u32 s77,s76,4096
s_add_u32 s78,s76,8192
s_add_u32 s79,s76,12288
v_mfma_f32_16x16x32_bf16 a[208:211],a[120:123],v[72:75],a[208:211]
buffer_load_dwordx4 v9,s[72:75],s83 offen offset:2048 lds
ds_read_b64_tr_b16 a[98:99],v1 offset:5312
v_mfma_f32_16x16x32_bf16 a[208:211],a[124:127],v[76:79],a[208:211]
ds_read_b64_tr_b16 a[102:103],v1 offset:15552
ds_read_b64_tr_b16 a[106:107],v1 offset:25792
v_mfma_f32_16x16x32_bf16 a[212:215],a[124:127],v[92:95],a[212:215]
v_mfma_f32_16x16x32_bf16 a[212:215],a[120:123],v[88:91],a[212:215]
v_mfma_f32_16x16x32_bf16 a[212:215],a[116:119],v[84:87],a[212:215]
v_mfma_f32_16x16x32_bf16 a[212:215],a[112:115],v[80:83],a[212:215]
v_mfma_f32_16x16x32_bf16 a[216:219],a[112:115],v[96:99],a[216:219]
buffer_load_dwordx4 v10,s[72:75],s83 offen offset:3072 lds
v_mfma_f32_16x16x32_bf16 a[216:219],a[116:119],v[100:103],a[216:219]
v_mfma_f32_16x16x32_bf16 a[216:219],a[120:123],v[104:107],a[216:219]
v_mfma_f32_16x16x32_bf16 a[216:219],a[124:127],v[108:111],a[216:219]
s_add_u32 m0,s82,50176
v_mfma_f32_16x16x32_bf16 a[220:223],a[124:127],v[124:127],a[220:223]
ds_read_b64_tr_b16 a[124:125],v2 offset:30944
ds_read_b64_tr_b16 a[126:127],v2 offset:36064
v_mfma_f32_16x16x32_bf16 a[220:223],a[120:123],v[120:123],a[220:223]
s_waitcnt vmcnt(9)
v_cmp_ne_u32 s[90:91],s68,v26
v_cmp_ne_u32 s[92:93],s69,v27
s_nop 0
s_or_b64 s[90:91],s[90:91],s[92:93]
s_cbranch_scc1 L_fr2_%=
L_fo2_%=:
v_mfma_f32_16x16x32_bf16 a[220:223],a[116:119],v[116:119],a[220:223]
buffer_load_dwordx4 v11,s[72:75],s83 offen lds
ds_read_b64_tr_b16 a[116:117],v2 offset:10464
v_mfma_f32_16x16x32_bf16 a[220:223],a[112:115],v[112:115],a[220:223]
s_waitcnt lgkmcnt(3)
ds_read_b64_tr_b16 a[112:113],v2 offset:224
ds_read_b64_tr_b16 a[114:115],v2 offset:5344
v_mfma_f32_16x16x32_bf16 a[224:227],a[128:131],v[64:67],a[224:227]
ds_read_b64_tr_b16 a[118:119],v2 offset:15584
v_mfma_f32_16x16x32_bf16 a[224:227],a[132:135],v[68:71],a[224:227]
ds_read_b64_tr_b16 a[120:121],v2 offset:20704
v_mfma_f32_16x16x32_bf16 a[224:227],a[136:139],v[72:75],a[224:227]
ds_read_b64_tr_b16 a[122:123],v2 offset:25824
v_mfma_f32_16x16x32_bf16 a[224:227],a[140:143],v[76:79],a[224:227]
v_mfma_f32_16x16x32_bf16 a[228:231],a[140:143],v[92:95],a[228:231]
v_mfma_f32_16x16x32_bf16 a[228:231],a[136:139],v[88:91],a[228:231]
v_mfma_f32_16x16x32_bf16 a[228:231],a[132:135],v[84:87],a[228:231]
v_mfma_f32_16x16x32_bf16 a[228:231],a[128:131],v[80:83],a[228:231]
buffer_load_dwordx4 v[132:135],v6,s[28:31],s76 offen sc1
v_mfma_f32_16x16x32_bf16 a[232:235],a[128:131],v[96:99],a[232:235]
v_mfma_f32_16x16x32_bf16 a[232:235],a[132:135],v[100:103],a[232:235]
v_mfma_f32_16x16x32_bf16 a[232:235],a[136:139],v[104:107],a[232:235]
v_mfma_f32_16x16x32_bf16 a[232:235],a[140:143],v[108:111],a[232:235]
v_mfma_f32_16x16x32_bf16 a[236:239],a[140:143],v[124:127],a[236:239]
ds_read_b64_tr_b16 a[140:141],v1 offset:30976
ds_read_b64_tr_b16 a[142:143],v1 offset:36096
v_mfma_f32_16x16x32_bf16 a[236:239],a[136:139],v[120:123],a[236:239]
buffer_load_dwordx4 v[140:143],v6,s[28:31],s77 offen sc1
ds_read_b64_tr_b16 a[136:137],v1 offset:20736
v_mfma_f32_16x16x32_bf16 a[236:239],a[132:135],v[116:119],a[236:239]
ds_read_b64_tr_b16 a[132:133],v1 offset:10496
v_mfma_f32_16x16x32_bf16 a[236:239],a[128:131],v[112:115],a[236:239]
ds_read_b64_tr_b16 a[128:129],v1 offset:256
v_mfma_f32_16x16x32_bf16 a[240:243],a[144:147],v[64:67],a[240:243]
ds_read_b64_tr_b16 a[130:131],v1 offset:5376
ds_read_b64_tr_b16 a[134:135],v1 offset:15616
v_mfma_f32_16x16x32_bf16 a[240:243],a[148:151],v[68:71],a[240:243]
ds_read_b64_tr_b16 a[138:139],v1 offset:25856
v_subrev_u32 v1,122880,v1
v_mfma_f32_16x16x32_bf16 a[240:243],a[152:155],v[72:75],a[240:243]
v_mfma_f32_16x16x32_bf16 a[240:243],a[156:159],v[76:79],a[240:243]
buffer_load_dwordx4 v[148:151],v6,s[28:31],s78 offen sc1
v_mfma_f32_16x16x32_bf16 a[244:247],a[156:159],v[92:95],a[244:247]
v_mfma_f32_16x16x32_bf16 a[244:247],a[152:155],v[88:91],a[244:247]
v_mfma_f32_16x16x32_bf16 a[244:247],a[148:151],v[84:87],a[244:247]
v_mfma_f32_16x16x32_bf16 a[244:247],a[144:147],v[80:83],a[244:247]
v_mfma_f32_16x16x32_bf16 a[248:251],a[144:147],v[96:99],a[248:251]
v_mfma_f32_16x16x32_bf16 a[248:251],a[148:151],v[100:103],a[248:251]
buffer_load_dwordx4 v[156:159],v6,s[28:31],s79 offen sc1
v_mfma_f32_16x16x32_bf16 a[248:251],a[152:155],v[104:107],a[248:251]
v_mfma_f32_16x16x32_bf16 a[248:251],a[156:159],v[108:111],a[248:251]
v_mfma_f32_16x16x32_bf16 a[252:255],a[156:159],v[124:127],a[252:255]
ds_read_b64_tr_b16 a[156:157],v2 offset:31008
ds_read_b64_tr_b16 a[158:159],v2 offset:36128
v_mfma_f32_16x16x32_bf16 a[252:255],a[152:155],v[120:123],a[252:255]
ds_read_b64_tr_b16 a[152:153],v2 offset:20768
v_mfma_f32_16x16x32_bf16 a[252:255],a[148:151],v[116:119],a[252:255]
ds_read_b64_tr_b16 a[148:149],v2 offset:10528
v_mfma_f32_16x16x32_bf16 a[252:255],a[144:147],v[112:115],a[252:255]
)A"
R"A(buffer_load_dwordx4 v[164:167],v6,s[28:31],s76 offen offset:1024 sc1
ds_read_b64_tr_b16 a[144:145],v2 offset:288
v_mfma_f32_16x16x32_bf16 a[0:3],a[160:163],v[64:67],a[0:3]
ds_read_b64_tr_b16 a[146:147],v2 offset:5408
ds_read_b64_tr_b16 a[150:151],v2 offset:15648
v_mfma_f32_16x16x32_bf16 a[0:3],a[164:167],v[68:71],a[0:3]
ds_read_b64_tr_b16 a[154:155],v2 offset:25888
v_subrev_u32 v2,122880,v2
v_mfma_f32_16x16x32_bf16 a[0:3],a[168:171],v[72:75],a[0:3]
v_mfma_f32_16x16x32_bf16 a[0:3],a[172:175],v[76:79],a[0:3]
v_mfma_f32_16x16x32_bf16 a[4:7],a[172:175],v[92:95],a[4:7]
v_mfma_f32_16x16x32_bf16 a[4:7],a[168:171],v[88:91],a[4:7]
buffer_load_dwordx4 v[172:175],v6,s[28:31],s77 offen offset:1024 sc1
v_mfma_f32_16x16x32_bf16 a[4:7],a[164:167],v[84:87],a[4:7]
v_mfma_f32_16x16x32_bf16 a[4:7],a[160:163],v[80:83],a[4:7]
v_mfma_f32_16x16x32_bf16 a[8:11],a[160:163],v[96:99],a[8:11]
v_mfma_f32_16x16x32_bf16 a[8:11],a[164:167],v[100:103],a[8:11]
v_mfma_f32_16x16x32_bf16 a[8:11],a[168:171],v[104:107],a[8:11]
v_mfma_f32_16x16x32_bf16 a[8:11],a[172:175],v[108:111],a[8:11]
buffer_load_dwordx4 v[180:183],v6,s[28:31],s78 offen offset:1024 sc1
v_mfma_f32_16x16x32_bf16 a[12:15],a[172:175],v[124:127],a[12:15]
ds_read_b64_tr_b16 a[172:173],v1 offset:30720
ds_read_b64_tr_b16 a[174:175],v1 offset:35840
v_mfma_f32_16x16x32_bf16 a[12:15],a[168:171],v[120:123],a[12:15]
ds_read_b64_tr_b16 a[168:169],v1 offset:20480
v_mfma_f32_16x16x32_bf16 a[12:15],a[164:167],v[116:119],a[12:15]
ds_read_b64_tr_b16 a[164:165],v1 offset:10240
v_mfma_f32_16x16x32_bf16 a[12:15],a[160:163],v[112:115],a[12:15]
ds_read_b64_tr_b16 a[160:161],v1
v_mfma_f32_16x16x32_bf16 a[16:19],a[176:179],v[64:67],a[16:19]
ds_read_b64_tr_b16 a[162:163],v1 offset:5120
ds_read_b64_tr_b16 a[166:167],v1 offset:15360
v_mfma_f32_16x16x32_bf16 a[16:19],a[180:183],v[68:71],a[16:19]
buffer_load_dwordx4 v[188:191],v6,s[28:31],s79 offen offset:1024 sc1
ds_read_b64_tr_b16 a[170:171],v1 offset:25600
v_mfma_f32_16x16x32_bf16 a[16:19],a[184:187],v[72:75],a[16:19]
v_mfma_f32_16x16x32_bf16 a[16:19],a[188:191],v[76:79],a[16:19]
v_mfma_f32_16x16x32_bf16 a[20:23],a[188:191],v[92:95],a[20:23]
v_mfma_f32_16x16x32_bf16 a[20:23],a[184:187],v[88:91],a[20:23]
v_mfma_f32_16x16x32_bf16 a[20:23],a[180:183],v[84:87],a[20:23]
v_mfma_f32_16x16x32_bf16 a[20:23],a[176:179],v[80:83],a[20:23]
buffer_load_dwordx4 v[196:199],v6,s[28:31],s76 offen offset:2048 sc1
v_mfma_f32_16x16x32_bf16 a[24:27],a[176:179],v[96:99],a[24:27]
v_mfma_f32_16x16x32_bf16 a[24:27],a[180:183],v[100:103],a[24:27]
v_mfma_f32_16x16x32_bf16 a[24:27],a[184:187],v[104:107],a[24:27]
v_mfma_f32_16x16x32_bf16 a[24:27],a[188:191],v[108:111],a[24:27]
v_mfma_f32_16x16x32_bf16 a[28:31],a[188:191],v[124:127],a[28:31]
ds_read_b64_tr_b16 a[188:189],v2 offset:30752
ds_read_b64_tr_b16 a[190:191],v2 offset:35872
v_mfma_f32_16x16x32_bf16 a[28:31],a[184:187],v[120:123],a[28:31]
buffer_load_dwordx4 v[204:207],v6,s[28:31],s77 offen offset:2048 sc1
ds_read_b64_tr_b16 a[184:185],v2 offset:20512
v_mfma_f32_16x16x32_bf16 a[28:31],a[180:183],v[116:119],a[28:31]
ds_read_b64_tr_b16 a[180:181],v2 offset:10272
v_mfma_f32_16x16x32_bf16 a[28:31],a[176:179],v[112:115],a[28:31]
ds_read_b64_tr_b16 a[176:177],v2 offset:32
v_mfma_f32_16x16x32_bf16 a[32:35],a[96:99],v[64:67],a[32:35]
ds_read_b64_tr_b16 a[178:179],v2 offset:5152
ds_read_b64_tr_b16 a[182:183],v2 offset:15392
v_mfma_f32_16x16x32_bf16 a[32:35],a[100:103],v[68:71],a[32:35]
ds_read_b64_tr_b16 a[186:187],v2 offset:25632
v_mfma_f32_16x16x32_bf16 a[32:35],a[104:107],v[72:75],a[32:35]
v_mfma_f32_16x16x32_bf16 a[32:35],a[108:111],v[76:79],a[32:35]
buffer_load_dwordx4 v[212:215],v6,s[28:31],s78 offen offset:2048 sc1
v_mfma_f32_16x16x32_bf16 a[36:39],a[108:111],v[92:95],a[36:39]
v_mfma_f32_16x16x32_bf16 a[36:39],a[104:107],v[88:91],a[36:39]
v_mfma_f32_16x16x32_bf16 a[36:39],a[100:103],v[84:87],a[36:39]
v_mfma_f32_16x16x32_bf16 a[36:39],a[96:99],v[80:83],a[36:39]
v_mfma_f32_16x16x32_bf16 a[40:43],a[96:99],v[96:99],a[40:43]
v_mfma_f32_16x16x32_bf16 a[40:43],a[100:103],v[100:103],a[40:43]
buffer_load_dwordx4 v[220:223],v6,s[28:31],s79 offen offset:2048 sc1
v_mfma_f32_16x16x32_bf16 a[40:43],a[104:107],v[104:107],a[40:43]
v_mfma_f32_16x16x32_bf16 a[40:43],a[108:111],v[108:111],a[40:43]
v_mfma_f32_16x16x32_bf16 a[44:47],a[108:111],v[124:127],a[44:47]
ds_read_b64_tr_b16 a[108:109],v1 offset:30784
ds_read_b64_tr_b16 a[110:111],v1 offset:35904
v_mfma_f32_16x16x32_bf16 a[44:47],a[104:107],v[120:123],a[44:47]
ds_read_b64_tr_b16 a[104:105],v1 offset:20544
v_mfma_f32_16x16x32_bf16 a[44:47],a[100:103],v[116:119],a[44:47]
ds_read_b64_tr_b16 a[100:101],v1 offset:10304
v_mfma_f32_16x16x32_bf16 a[44:47],a[96:99],v[112:115],a[44:47]
s_cmp_ge_u32 1,s71
s_cselect_b32 s80,5,3
buffer_load_dwordx4 v[228:231],v6,s[28:31],s76 offen offset:3072 sc1
s_waitcnt lgkmcnt(4)
v_mfma_f32_16x16x32_bf16 a[48:51],a[112:115],v[64:67],a[48:51]
s_mul_i32 s80,s80,163840
s_add_u32 s80,s80,s81
s_add_u32 s83,s80,20480
v_mfma_f32_16x16x32_bf16 a[48:51],a[116:119],v[68:71],a[48:51]
ds_read_b64_tr_b16 a[96:97],v1 offset:64
ds_read_b64_tr_b16 a[98:99],v1 offset:5184
v_mfma_f32_16x16x32_bf16 a[48:51],a[120:123],v[72:75],a[48:51]
ds_read_b64_tr_b16 a[102:103],v1 offset:15424
v_mfma_f32_16x16x32_bf16 a[48:51],a[124:127],v[76:79],a[48:51]
ds_read_b64_tr_b16 a[106:107],v1 offset:25664
v_mfma_f32_16x16x32_bf16 a[52:55],a[124:127],v[92:95],a[52:55]
v_mfma_f32_16x16x32_bf16 a[52:55],a[120:123],v[88:91],a[52:55]
buffer_load_dwordx4 v[236:239],v6,s[28:31],s77 offen offset:3072 sc1
v_mfma_f32_16x16x32_bf16 a[52:55],a[116:119],v[84:87],a[52:55]
v_mfma_f32_16x16x32_bf16 a[52:55],a[112:115],v[80:83],a[52:55]
v_mfma_f32_16x16x32_bf16 a[56:59],a[112:115],v[96:99],a[56:59]
v_mfma_f32_16x16x32_bf16 a[56:59],a[116:119],v[100:103],a[56:59]
v_mfma_f32_16x16x32_bf16 a[56:59],a[120:123],v[104:107],a[56:59]
v_mfma_f32_16x16x32_bf16 a[56:59],a[124:127],v[108:111],a[56:59]
buffer_load_dwordx4 v[244:247],v6,s[28:31],s78 offen offset:3072 sc1
v_mfma_f32_16x16x32_bf16 a[60:63],a[124:127],v[124:127],a[60:63]
ds_read_b64_tr_b16 a[124:125],v2 offset:30816
ds_read_b64_tr_b16 a[126:127],v2 offset:35936
v_mfma_f32_16x16x32_bf16 a[60:63],a[120:123],v[120:123],a[60:63]
ds_read_b64_tr_b16 a[120:121],v2 offset:20576
v_mfma_f32_16x16x32_bf16 a[60:63],a[116:119],v[116:119],a[60:63]
ds_read_b64_tr_b16 a[116:117],v2 offset:10336
v_mfma_f32_16x16x32_bf16 a[60:63],a[112:115],v[112:115],a[60:63]
ds_read_b64_tr_b16 a[112:113],v2 offset:96
v_mfma_f32_16x16x32_bf16 a[64:67],a[128:131],v[64:67],a[64:67]
ds_read_b64_tr_b16 a[114:115],v2 offset:5216
ds_read_b64_tr_b16 a[118:119],v2 offset:15456
v_mfma_f32_16x16x32_bf16 a[64:67],a[132:135],v[68:71],a[64:67]
buffer_load_dwordx4 v[252:255],v6,s[28:31],s79 offen offset:3072 sc1
ds_read_b64_tr_b16 a[122:123],v2 offset:25696
v_mfma_f32_16x16x32_bf16 a[64:67],a[136:139],v[72:75],a[64:67]
v_mfma_f32_16x16x32_bf16 a[64:67],a[140:143],v[76:79],a[64:67]
v_mfma_f32_16x16x32_bf16 a[68:71],a[140:143],v[92:95],a[68:71]
s_add_u32 m0,s82,81920
v_mfma_f32_16x16x32_bf16 a[68:71],a[136:139],v[88:91],a[68:71]
v_mfma_f32_16x16x32_bf16 a[68:71],a[132:135],v[84:87],a[68:71]
v_mfma_f32_16x16x32_bf16 a[68:71],a[128:131],v[80:83],a[68:71]
buffer_load_dwordx4 v7,s[72:75],s80 offen lds
v_mfma_f32_16x16x32_bf16 a[72:75],a[128:131],v[96:99],a[72:75]
v_mfma_f32_16x16x32_bf16 a[72:75],a[132:135],v[100:103],a[72:75]
v_mfma_f32_16x16x32_bf16 a[72:75],a[136:139],v[104:107],a[72:75]
v_mfma_f32_16x16x32_bf16 a[72:75],a[140:143],v[108:111],a[72:75]
v_mfma_f32_16x16x32_bf16 a[76:79],a[140:143],v[124:127],a[76:79]
buffer_load_dwordx4 v8,s[72:75],s80 offen offset:1024 lds
ds_read_b64_tr_b16 a[140:141],v1 offset:30848
v_mfma_f32_16x16x32_bf16 a[76:79],a[136:139],v[120:123],a[76:79]
ds_read_b64_tr_b16 a[136:137],v1 offset:20608
ds_read_b64_tr_b16 a[138:139],v1 offset:25728
v_mfma_f32_16x16x32_bf16 a[76:79],a[132:135],v[116:119],a[76:79]
ds_read_b64_tr_b16 a[132:133],v1 offset:10368
v_mfma_f32_16x16x32_bf16 a[76:79],a[128:131],v[112:115],a[76:79]
ds_read_b64_tr_b16 a[128:129],v1 offset:128
v_mfma_f32_16x16x32_bf16 a[80:83],a[144:147],v[64:67],a[80:83]
ds_read_b64_tr_b16 a[130:131],v1 offset:5248
v_mfma_f32_16x16x32_bf16 a[80:83],a[148:151],v[68:71],a[80:83]
buffer_load_dwordx4 v9,s[72:75],s80 offen offset:2048 lds
ds_read_b64_tr_b16 a[134:135],v1 offset:15488
v_mfma_f32_16x16x32_bf16 a[80:83],a[152:155],v[72:75],a[80:83]
ds_read_b64_tr_b16 a[142:143],v1 offset:35968
v_mfma_f32_16x16x32_bf16 a[80:83],a[156:159],v[76:79],a[80:83]
v_mfma_f32_16x16x32_bf16 a[84:87],a[156:159],v[92:95],a[84:87]
v_mfma_f32_16x16x32_bf16 a[84:87],a[152:155],v[88:91],a[84:87]
v_mfma_f32_16x16x32_bf16 a[84:87],a[148:151],v[84:87],a[84:87]
buffer_load_dwordx4 v10,s[72:75],s80 offen offset:3072 lds
v_mfma_f32_16x16x32_bf16 a[84:87],a[144:147],v[80:83],a[84:87]
v_mfma_f32_16x16x32_bf16 a[88:91],a[144:147],v[96:99],a[88:91]
s_add_u32 m0,s82,86016
v_mfma_f32_16x16x32_bf16 a[88:91],a[148:151],v[100:103],a[88:91]
v_mfma_f32_16x16x32_bf16 a[88:91],a[152:155],v[104:107],a[88:91]
v_mfma_f32_16x16x32_bf16 a[88:91],a[156:159],v[108:111],a[88:91]
buffer_load_dwordx4 v11,s[72:75],s80 offen lds
v_mfma_f32_16x16x32_bf16 a[92:95],a[156:159],v[124:127],a[92:95]
ds_read_b64_tr_b16 a[156:157],v2 offset:30880
ds_read_b64_tr_b16 a[158:159],v2 offset:36000
v_mfma_f32_16x16x32_bf16 a[92:95],a[152:155],v[120:123],a[92:95]
s_add_u32 m0,s82,87040
ds_read_b64_tr_b16 a[152:153],v2 offset:20640
v_mfma_f32_16x16x32_bf16 a[92:95],a[148:151],v[116:119],a[92:95]
ds_read_b64_tr_b16 a[148:149],v2 offset:10400
v_mfma_f32_16x16x32_bf16 a[92:95],a[144:147],v[112:115],a[92:95]
ds_read_b64_tr_b16 a[144:145],v2 offset:160
v_mfma_f32_16x16x32_bf16 a[192:195],a[160:163],v[128:131],a[192:195]
buffer_load_dwordx4 v7,s[72:75],s83 offen lds
ds_read_b64_tr_b16 a[146:147],v2 offset:5280
v_mfma_f32_16x16x32_bf16 a[192:195],a[164:167],v[160:163],a[192:195]
ds_read_b64_tr_b16 a[150:151],v2 offset:15520
ds_read_b64_tr_b16 a[154:155],v2 offset:25760
v_mfma_f32_16x16x32_bf16 a[192:195],a[168:171],v[192:195],a[192:195]
v_mfma_f32_16x16x32_bf16 a[192:195],a[172:175],v[224:227],a[192:195]
v_mfma_f32_16x16x32_bf16 a[196:199],a[172:175],v[232:235],a[196:199]
v_mfma_f32_16x16x32_bf16 a[196:199],a[168:171],v[200:203],a[196:199]
buffer_load_dwordx4 v8,s[72:75],s83 offen offset:1024 lds
v_mfma_f32_16x16x32_bf16 a[196:199],a[164:167],v[168:171],a[196:199]
s_cmp_ge_u32 1,s71
s_cselect_b32 s76,5,3
s_lshl_b32 s76,s76,16
v_mfma_f32_16x16x32_bf16 a[196:199],a[160:163],v[136:139],a[196:199]
s_add_u32 s77,s76,4096
s_add_u32 s78,s76,8192
s_add_u32 s79,s76,12288
v_mfma_f32_16x16x32_bf16 a[200:203],a[160:163],v[144:147],a[200:203]
v_mfma_f32_16x16x32_bf16 a[200:203],a[164:167],v[176:179],a[200:203]
v_mfma_f32_16x16x32_bf16 a[200:203],a[168:171],v[208:211],a[200:203]
buffer_load_dwordx4 v9,s[72:75],s83 offen offset:2048 lds
v_mfma_f32_16x16x32_bf16 a[200:203],a[172:175],v[240:243],a[200:203]
v_mfma_f32_16x16x32_bf16 a[204:207],a[172:175],v[248:251],a[204:207]
ds_read_b64_tr_b16 a[172:173],v1 offset:30912
ds_read_b64_tr_b16 a[174:175],v1 offset:36032
v_mfma_f32_16x16x32_bf16 a[204:207],a[168:171],v[216:219],a[204:207]
ds_read_b64_tr_b16 a[168:169],v1 offset:20672
v_mfma_f32_16x16x32_bf16 a[204:207],a[164:167],v[184:187],a[204:207]
ds_read_b64_tr_b16 a[164:165],v1 offset:10432
v_mfma_f32_16x16x32_bf16 a[204:207],a[160:163],v[152:155],a[204:207]
buffer_load_dwordx4 v10,s[72:75],s83 offen offset:3072 lds
ds_read_b64_tr_b16 a[160:161],v1 offset:192
s_waitcnt vmcnt(25)
s_barrier
v_mfma_f32_16x16x32_bf16 a[208:211],a[176:179],v[128:131],a[208:211]
s_add_u32 m0,s82,91136
ds_read_b64_tr_b16 a[162:163],v1 offset:5312
ds_read_b64_tr_b16 a[166:167],v1 offset:15552
v_mfma_f32_16x16x32_bf16 a[208:211],a[180:183],v[160:163],a[208:211]
ds_read_b64_tr_b16 a[170:171],v1 offset:25792
v_mfma_f32_16x16x32_bf16 a[208:211],a[184:187],v[192:195],a[208:211]
v_mfma_f32_16x16x32_bf16 a[208:211],a[188:191],v[224:227],a[208:211]
buffer_load_dwordx4 v11,s[72:75],s83 offen lds
v_mfma_f32_16x16x32_bf16 a[212:215],a[188:191],v[232:235],a[212:215]
)A"
R"A(v_mfma_f32_16x16x32_bf16 a[212:215],a[184:187],v[200:203],a[212:215]
v_mfma_f32_16x16x32_bf16 a[212:215],a[180:183],v[168:171],a[212:215]
v_mfma_f32_16x16x32_bf16 a[212:215],a[176:179],v[136:139],a[212:215]
v_mfma_f32_16x16x32_bf16 a[216:219],a[176:179],v[144:147],a[216:219]
v_mfma_f32_16x16x32_bf16 a[216:219],a[180:183],v[176:179],a[216:219]
v_mfma_f32_16x16x32_bf16 a[216:219],a[184:187],v[208:211],a[216:219]
v_mfma_f32_16x16x32_bf16 a[216:219],a[188:191],v[240:243],a[216:219]
v_mfma_f32_16x16x32_bf16 a[220:223],a[188:191],v[248:251],a[220:223]
ds_read_b64_tr_b16 a[188:189],v2 offset:30944
ds_read_b64_tr_b16 a[190:191],v2 offset:36064
v_mfma_f32_16x16x32_bf16 a[220:223],a[184:187],v[216:219],a[220:223]
buffer_load_dwordx4 v[64:67],v6,s[28:31],s76 offen sc1
ds_read_b64_tr_b16 a[184:185],v2 offset:20704
v_mfma_f32_16x16x32_bf16 a[220:223],a[180:183],v[184:187],a[220:223]
ds_read_b64_tr_b16 a[180:181],v2 offset:10464
v_mfma_f32_16x16x32_bf16 a[220:223],a[176:179],v[152:155],a[220:223]
s_waitcnt lgkmcnt(4)
ds_read_b64_tr_b16 a[176:177],v2 offset:224
v_mfma_f32_16x16x32_bf16 a[224:227],a[96:99],v[128:131],a[224:227]
ds_read_b64_tr_b16 a[178:179],v2 offset:5344
ds_read_b64_tr_b16 a[182:183],v2 offset:15584
v_mfma_f32_16x16x32_bf16 a[224:227],a[100:103],v[160:163],a[224:227]
ds_read_b64_tr_b16 a[186:187],v2 offset:25824
v_mfma_f32_16x16x32_bf16 a[224:227],a[104:107],v[192:195],a[224:227]
buffer_load_dwordx4 v[68:71],v6,s[28:31],s77 offen sc1
v_mfma_f32_16x16x32_bf16 a[224:227],a[108:111],v[224:227],a[224:227]
v_mfma_f32_16x16x32_bf16 a[228:231],a[108:111],v[232:235],a[228:231]
v_mfma_f32_16x16x32_bf16 a[228:231],a[104:107],v[200:203],a[228:231]
v_mfma_f32_16x16x32_bf16 a[228:231],a[100:103],v[168:171],a[228:231]
v_mfma_f32_16x16x32_bf16 a[228:231],a[96:99],v[136:139],a[228:231]
buffer_load_dwordx4 v[72:75],v6,s[28:31],s78 offen sc1
v_mfma_f32_16x16x32_bf16 a[232:235],a[96:99],v[144:147],a[232:235]
v_mfma_f32_16x16x32_bf16 a[232:235],a[100:103],v[176:179],a[232:235]
v_mfma_f32_16x16x32_bf16 a[232:235],a[104:107],v[208:211],a[232:235]
v_mfma_f32_16x16x32_bf16 a[232:235],a[108:111],v[240:243],a[232:235]
v_mfma_f32_16x16x32_bf16 a[236:239],a[108:111],v[248:251],a[236:239]
buffer_load_dwordx4 v[76:79],v6,s[28:31],s79 offen sc1
ds_read_b64_tr_b16 a[108:109],v1 offset:30976
v_mfma_f32_16x16x32_bf16 a[236:239],a[104:107],v[216:219],a[236:239]
ds_read_b64_tr_b16 a[104:105],v1 offset:20736
ds_read_b64_tr_b16 a[106:107],v1 offset:25856
v_mfma_f32_16x16x32_bf16 a[236:239],a[100:103],v[184:187],a[236:239]
ds_read_b64_tr_b16 a[100:101],v1 offset:10496
v_mfma_f32_16x16x32_bf16 a[236:239],a[96:99],v[152:155],a[236:239]
ds_read_b64_tr_b16 a[96:97],v1 offset:256
v_mfma_f32_16x16x32_bf16 a[240:243],a[112:115],v[128:131],a[240:243]
ds_read_b64_tr_b16 a[98:99],v1 offset:5376
v_mfma_f32_16x16x32_bf16 a[240:243],a[116:119],v[160:163],a[240:243]
buffer_load_dwordx4 v[80:83],v6,s[28:31],s76 offen offset:1024 sc1
ds_read_b64_tr_b16 a[102:103],v1 offset:15616
v_mfma_f32_16x16x32_bf16 a[240:243],a[120:123],v[192:195],a[240:243]
ds_read_b64_tr_b16 a[110:111],v1 offset:36096
v_add_u32 v1,40960,v1
v_mfma_f32_16x16x32_bf16 a[240:243],a[124:127],v[224:227],a[240:243]
v_add_u32 v42,40960,v1
v_add_u32 v44,81920,v1
v_mfma_f32_16x16x32_bf16 a[244:247],a[124:127],v[232:235],a[244:247]
v_mfma_f32_16x16x32_bf16 a[244:247],a[120:123],v[200:203],a[244:247]
v_mfma_f32_16x16x32_bf16 a[244:247],a[116:119],v[168:171],a[244:247]
buffer_load_dwordx4 v[84:87],v6,s[28:31],s77 offen offset:1024 sc1
v_mfma_f32_16x16x32_bf16 a[244:247],a[112:115],v[136:139],a[244:247]
v_mfma_f32_16x16x32_bf16 a[248:251],a[112:115],v[144:147],a[248:251]
v_mfma_f32_16x16x32_bf16 a[248:251],a[116:119],v[176:179],a[248:251]
v_mfma_f32_16x16x32_bf16 a[248:251],a[120:123],v[208:211],a[248:251]
v_mfma_f32_16x16x32_bf16 a[248:251],a[124:127],v[240:243],a[248:251]
buffer_load_dwordx4 v[88:91],v6,s[28:31],s78 offen offset:1024 sc1
v_mfma_f32_16x16x32_bf16 a[252:255],a[124:127],v[248:251],a[252:255]
ds_read_b64_tr_b16 a[124:125],v2 offset:31008
ds_read_b64_tr_b16 a[126:127],v2 offset:36128
v_mfma_f32_16x16x32_bf16 a[252:255],a[120:123],v[216:219],a[252:255]
ds_read_b64_tr_b16 a[120:121],v2 offset:20768
v_mfma_f32_16x16x32_bf16 a[252:255],a[116:119],v[184:187],a[252:255]
ds_read_b64_tr_b16 a[116:117],v2 offset:10528
v_mfma_f32_16x16x32_bf16 a[252:255],a[112:115],v[152:155],a[252:255]
ds_read_b64_tr_b16 a[112:113],v2 offset:288
v_mfma_f32_16x16x32_bf16 a[0:3],a[128:131],v[128:131],a[0:3]
buffer_load_dwordx4 v[92:95],v6,s[28:31],s79 offen offset:1024 sc1
ds_read_b64_tr_b16 a[114:115],v2 offset:5408
v_mfma_f32_16x16x32_bf16 a[0:3],a[132:135],v[160:163],a[0:3]
ds_read_b64_tr_b16 a[118:119],v2 offset:15648
ds_read_b64_tr_b16 a[122:123],v2 offset:25888
v_mfma_f32_16x16x32_bf16 a[0:3],a[136:139],v[192:195],a[0:3]
v_add_u32 v2,40960,v2
v_add_u32 v43,40960,v2
v_mfma_f32_16x16x32_bf16 a[0:3],a[140:143],v[224:227],a[0:3]
v_add_u32 v45,81920,v2
v_mfma_f32_16x16x32_bf16 a[4:7],a[140:143],v[232:235],a[4:7]
v_mfma_f32_16x16x32_bf16 a[4:7],a[136:139],v[200:203],a[4:7]
buffer_load_dwordx4 v[96:99],v6,s[28:31],s76 offen offset:2048 sc1
v_mfma_f32_16x16x32_bf16 a[4:7],a[132:135],v[168:171],a[4:7]
v_mfma_f32_16x16x32_bf16 a[4:7],a[128:131],v[136:139],a[4:7]
v_mfma_f32_16x16x32_bf16 a[8:11],a[128:131],v[144:147],a[8:11]
v_mfma_f32_16x16x32_bf16 a[8:11],a[132:135],v[176:179],a[8:11]
v_mfma_f32_16x16x32_bf16 a[8:11],a[136:139],v[208:211],a[8:11]
buffer_load_dwordx4 v[100:103],v6,s[28:31],s77 offen offset:2048 sc1
v_mfma_f32_16x16x32_bf16 a[8:11],a[140:143],v[240:243],a[8:11]
v_mfma_f32_16x16x32_bf16 a[12:15],a[140:143],v[248:251],a[12:15]
ds_read_b64_tr_b16 a[140:141],v1 offset:30720
ds_read_b64_tr_b16 a[142:143],v1 offset:35840
v_mfma_f32_16x16x32_bf16 a[12:15],a[136:139],v[216:219],a[12:15]
ds_read_b64_tr_b16 a[136:137],v1 offset:20480
v_mfma_f32_16x16x32_bf16 a[12:15],a[132:135],v[184:187],a[12:15]
ds_read_b64_tr_b16 a[132:133],v1 offset:10240
v_mfma_f32_16x16x32_bf16 a[12:15],a[128:131],v[152:155],a[12:15]
buffer_load_dwordx4 v[104:107],v6,s[28:31],s78 offen offset:2048 sc1
ds_read_b64_tr_b16 a[128:129],v1
v_mfma_f32_16x16x32_bf16 a[16:19],a[144:147],v[128:131],a[16:19]
ds_read_b64_tr_b16 a[130:131],v1 offset:5120
ds_read_b64_tr_b16 a[134:135],v1 offset:15360
v_mfma_f32_16x16x32_bf16 a[16:19],a[148:151],v[160:163],a[16:19]
ds_read_b64_tr_b16 a[138:139],v1 offset:25600
v_mfma_f32_16x16x32_bf16 a[16:19],a[152:155],v[192:195],a[16:19]
v_mfma_f32_16x16x32_bf16 a[16:19],a[156:159],v[224:227],a[16:19]
v_mfma_f32_16x16x32_bf16 a[20:23],a[156:159],v[232:235],a[20:23]
buffer_load_dwordx4 v[108:111],v6,s[28:31],s79 offen offset:2048 sc1
v_mfma_f32_16x16x32_bf16 a[20:23],a[152:155],v[200:203],a[20:23]
s_cmp_ge_u32 2,s71
s_cselect_b32 s80,7,5
s_mul_i32 s80,s80,163840
v_mfma_f32_16x16x32_bf16 a[20:23],a[148:151],v[168:171],a[20:23]
s_add_u32 s80,s80,s81
s_add_u32 s83,s80,20480
v_mfma_f32_16x16x32_bf16 a[20:23],a[144:147],v[136:139],a[20:23]
v_mfma_f32_16x16x32_bf16 a[24:27],a[144:147],v[144:147],a[24:27]
v_mfma_f32_16x16x32_bf16 a[24:27],a[148:151],v[176:179],a[24:27]
buffer_load_dwordx4 v[112:115],v6,s[28:31],s76 offen offset:3072 sc1
v_mfma_f32_16x16x32_bf16 a[24:27],a[152:155],v[208:211],a[24:27]
v_mfma_f32_16x16x32_bf16 a[24:27],a[156:159],v[240:243],a[24:27]
v_mfma_f32_16x16x32_bf16 a[28:31],a[156:159],v[248:251],a[28:31]
ds_read_b64_tr_b16 a[156:157],v2 offset:30752
ds_read_b64_tr_b16 a[158:159],v2 offset:35872
v_mfma_f32_16x16x32_bf16 a[28:31],a[152:155],v[216:219],a[28:31]
ds_read_b64_tr_b16 a[152:153],v2 offset:20512
v_mfma_f32_16x16x32_bf16 a[28:31],a[148:151],v[184:187],a[28:31]
buffer_load_dwordx4 v[116:119],v6,s[28:31],s77 offen offset:3072 sc1
ds_read_b64_tr_b16 a[148:149],v2 offset:10272
v_mfma_f32_16x16x32_bf16 a[28:31],a[144:147],v[152:155],a[28:31]
ds_read_b64_tr_b16 a[144:145],v2 offset:32
v_mfma_f32_16x16x32_bf16 a[32:35],a[160:163],v[128:131],a[32:35]
ds_read_b64_tr_b16 a[146:147],v2 offset:5152
ds_read_b64_tr_b16 a[150:151],v2 offset:15392
v_mfma_f32_16x16x32_bf16 a[32:35],a[164:167],v[160:163],a[32:35]
ds_read_b64_tr_b16 a[154:155],v2 offset:25632
v_mfma_f32_16x16x32_bf16 a[32:35],a[168:171],v[192:195],a[32:35]
v_mfma_f32_16x16x32_bf16 a[32:35],a[172:175],v[224:227],a[32:35]
buffer_load_dwordx4 v[120:123],v6,s[28:31],s78 offen offset:3072 sc1
v_mfma_f32_16x16x32_bf16 a[36:39],a[172:175],v[232:235],a[36:39]
v_mfma_f32_16x16x32_bf16 a[36:39],a[168:171],v[200:203],a[36:39]
v_mfma_f32_16x16x32_bf16 a[36:39],a[164:167],v[168:171],a[36:39]
v_mfma_f32_16x16x32_bf16 a[36:39],a[160:163],v[136:139],a[36:39]
v_mfma_f32_16x16x32_bf16 a[40:43],a[160:163],v[144:147],a[40:43]
buffer_load_dwordx4 v[124:127],v6,s[28:31],s79 offen offset:3072 sc1
v_mfma_f32_16x16x32_bf16 a[40:43],a[164:167],v[176:179],a[40:43]
v_mfma_f32_16x16x32_bf16 a[40:43],a[168:171],v[208:211],a[40:43]
s_add_u32 m0,s82,122880
v_mfma_f32_16x16x32_bf16 a[40:43],a[172:175],v[240:243],a[40:43]
v_mfma_f32_16x16x32_bf16 a[44:47],a[172:175],v[248:251],a[44:47]
ds_read_b64_tr_b16 a[172:173],v1 offset:30784
ds_read_b64_tr_b16 a[174:175],v1 offset:35904
v_mfma_f32_16x16x32_bf16 a[44:47],a[168:171],v[216:219],a[44:47]
buffer_load_dwordx4 v7,s[72:75],s80 offen lds
ds_read_b64_tr_b16 a[168:169],v1 offset:20544
v_mfma_f32_16x16x32_bf16 a[44:47],a[164:167],v[184:187],a[44:47]
ds_read_b64_tr_b16 a[164:165],v1 offset:10304
v_mfma_f32_16x16x32_bf16 a[44:47],a[160:163],v[152:155],a[44:47]
s_waitcnt lgkmcnt(4)
ds_read_b64_tr_b16 a[160:161],v1 offset:64
v_mfma_f32_16x16x32_bf16 a[48:51],a[176:179],v[128:131],a[48:51]
ds_read_b64_tr_b16 a[162:163],v1 offset:5184
ds_read_b64_tr_b16 a[166:167],v1 offset:15424
v_mfma_f32_16x16x32_bf16 a[48:51],a[180:183],v[160:163],a[48:51]
ds_read_b64_tr_b16 a[170:171],v1 offset:25664
v_mfma_f32_16x16x32_bf16 a[48:51],a[184:187],v[192:195],a[48:51]
buffer_load_dwordx4 v8,s[72:75],s80 offen offset:1024 lds
v_mfma_f32_16x16x32_bf16 a[48:51],a[188:191],v[224:227],a[48:51]
v_mfma_f32_16x16x32_bf16 a[52:55],a[188:191],v[232:235],a[52:55]
v_mfma_f32_16x16x32_bf16 a[52:55],a[184:187],v[200:203],a[52:55]
v_mfma_f32_16x16x32_bf16 a[52:55],a[180:183],v[168:171],a[52:55]
v_mfma_f32_16x16x32_bf16 a[52:55],a[176:179],v[136:139],a[52:55]
buffer_load_dwordx4 v9,s[72:75],s80 offen offset:2048 lds
v_mfma_f32_16x16x32_bf16 a[56:59],a[176:179],v[144:147],a[56:59]
v_mfma_f32_16x16x32_bf16 a[56:59],a[180:183],v[176:179],a[56:59]
v_mfma_f32_16x16x32_bf16 a[56:59],a[184:187],v[208:211],a[56:59]
v_mfma_f32_16x16x32_bf16 a[56:59],a[188:191],v[240:243],a[56:59]
v_mfma_f32_16x16x32_bf16 a[60:63],a[188:191],v[248:251],a[60:63]
buffer_load_dwordx4 v10,s[72:75],s80 offen offset:3072 lds
ds_read_b64_tr_b16 a[188:189],v2 offset:30816
v_mfma_f32_16x16x32_bf16 a[60:63],a[184:187],v[216:219],a[60:63]
ds_read_b64_tr_b16 a[184:185],v2 offset:20576
ds_read_b64_tr_b16 a[186:187],v2 offset:25696
v_mfma_f32_16x16x32_bf16 a[60:63],a[180:183],v[184:187],a[60:63]
s_add_u32 m0,s82,126976
ds_read_b64_tr_b16 a[180:181],v2 offset:10336
v_mfma_f32_16x16x32_bf16 a[60:63],a[176:179],v[152:155],a[60:63]
ds_read_b64_tr_b16 a[176:177],v2 offset:96
v_mfma_f32_16x16x32_bf16 a[64:67],a[96:99],v[128:131],a[64:67]
ds_read_b64_tr_b16 a[178:179],v2 offset:5216
v_mfma_f32_16x16x32_bf16 a[64:67],a[100:103],v[160:163],a[64:67]
buffer_load_dwordx4 v11,s[72:75],s80 offen lds
ds_read_b64_tr_b16 a[182:183],v2 offset:15456
v_mfma_f32_16x16x32_bf16 a[64:67],a[104:107],v[192:195],a[64:67]
ds_read_b64_tr_b16 a[190:191],v2 offset:35936
v_mfma_f32_16x16x32_bf16 a[64:67],a[108:111],v[224:227],a[64:67]
s_add_u32 m0,s82,128000
v_mfma_f32_16x16x32_bf16 a[68:71],a[108:111],v[232:235],a[68:71]
v_mfma_f32_16x16x32_bf16 a[68:71],a[104:107],v[200:203],a[68:71]
v_mfma_f32_16x16x32_bf16 a[68:71],a[100:103],v[168:171],a[68:71]
buffer_load_dwordx4 v7,s[72:75],s83 offen lds
v_mfma_f32_16x16x32_bf16 a[68:71],a[96:99],v[136:139],a[68:71]
v_mfma_f32_16x16x32_bf16 a[72:75],a[96:99],v[144:147],a[72:75]
v_mfma_f32_16x16x32_bf16 a[72:75],a[100:103],v[176:179],a[72:75]
v_mfma_f32_16x16x32_bf16 a[72:75],a[104:107],v[208:211],a[72:75]
)A"
R"A(s_cmp_ge_u32 2,s71
s_cselect_b32 s76,7,5
s_lshl_b32 s76,s76,16
v_mfma_f32_16x16x32_bf16 a[72:75],a[108:111],v[240:243],a[72:75]
s_add_u32 s77,s76,4096
s_add_u32 s78,s76,8192
buffer_load_dwordx4 v8,s[72:75],s83 offen offset:1024 lds
v_mfma_f32_16x16x32_bf16 a[76:79],a[108:111],v[248:251],a[76:79]
s_add_u32 s79,s76,12288
ds_read_b64_tr_b16 a[108:109],v1 offset:30848
ds_read_b64_tr_b16 a[110:111],v1 offset:35968
v_mfma_f32_16x16x32_bf16 a[76:79],a[104:107],v[216:219],a[76:79]
ds_read_b64_tr_b16 a[104:105],v1 offset:20608
v_mfma_f32_16x16x32_bf16 a[76:79],a[100:103],v[184:187],a[76:79]
ds_read_b64_tr_b16 a[100:101],v1 offset:10368
v_mfma_f32_16x16x32_bf16 a[76:79],a[96:99],v[152:155],a[76:79]
ds_read_b64_tr_b16 a[96:97],v1 offset:128
v_mfma_f32_16x16x32_bf16 a[80:83],a[112:115],v[128:131],a[80:83]
buffer_load_dwordx4 v9,s[72:75],s83 offen offset:2048 lds
ds_read_b64_tr_b16 a[98:99],v1 offset:5248
v_mfma_f32_16x16x32_bf16 a[80:83],a[116:119],v[160:163],a[80:83]
ds_read_b64_tr_b16 a[102:103],v1 offset:15488
ds_read_b64_tr_b16 a[106:107],v1 offset:25728
v_mfma_f32_16x16x32_bf16 a[80:83],a[120:123],v[192:195],a[80:83]
v_mfma_f32_16x16x32_bf16 a[80:83],a[124:127],v[224:227],a[80:83]
v_mfma_f32_16x16x32_bf16 a[84:87],a[124:127],v[232:235],a[84:87]
v_mfma_f32_16x16x32_bf16 a[84:87],a[120:123],v[200:203],a[84:87]
buffer_load_dwordx4 v10,s[72:75],s83 offen offset:3072 lds
v_mfma_f32_16x16x32_bf16 a[84:87],a[116:119],v[168:171],a[84:87]
v_mfma_f32_16x16x32_bf16 a[84:87],a[112:115],v[136:139],a[84:87]
s_add_u32 m0,s82,132096
v_mfma_f32_16x16x32_bf16 a[88:91],a[112:115],v[144:147],a[88:91]
v_mfma_f32_16x16x32_bf16 a[88:91],a[116:119],v[176:179],a[88:91]
v_mfma_f32_16x16x32_bf16 a[88:91],a[120:123],v[208:211],a[88:91]
buffer_load_dwordx4 v11,s[72:75],s83 offen lds
v_mfma_f32_16x16x32_bf16 a[88:91],a[124:127],v[240:243],a[88:91]
v_mfma_f32_16x16x32_bf16 a[92:95],a[124:127],v[248:251],a[92:95]
ds_read_b64_tr_b16 a[124:125],v2 offset:30880
ds_read_b64_tr_b16 a[126:127],v2 offset:36000
v_mfma_f32_16x16x32_bf16 a[92:95],a[120:123],v[216:219],a[92:95]
ds_read_b64_tr_b16 a[120:121],v2 offset:20640
v_mfma_f32_16x16x32_bf16 a[92:95],a[116:119],v[184:187],a[92:95]
ds_read_b64_tr_b16 a[116:117],v2 offset:10400
v_mfma_f32_16x16x32_bf16 a[92:95],a[112:115],v[152:155],a[92:95]
s_waitcnt vmcnt(13)
ds_read_b64_tr_b16 a[112:113],v2 offset:160
v_mfma_f32_16x16x32_bf16 a[192:195],a[128:131],v[132:135],a[192:195]
ds_read_b64_tr_b16 a[114:115],v2 offset:5280
ds_read_b64_tr_b16 a[118:119],v2 offset:15520
v_mfma_f32_16x16x32_bf16 a[192:195],a[132:135],v[140:143],a[192:195]
ds_read_b64_tr_b16 a[122:123],v2 offset:25760
v_mfma_f32_16x16x32_bf16 a[192:195],a[136:139],v[148:151],a[192:195]
buffer_load_dwordx4 v[128:131],v6,s[28:31],s76 offen sc1
v_mfma_f32_16x16x32_bf16 a[192:195],a[140:143],v[156:159],a[192:195]
v_mfma_f32_16x16x32_bf16 a[196:199],a[140:143],v[188:191],a[196:199]
v_mfma_f32_16x16x32_bf16 a[196:199],a[136:139],v[180:183],a[196:199]
v_mfma_f32_16x16x32_bf16 a[196:199],a[132:135],v[172:175],a[196:199]
v_mfma_f32_16x16x32_bf16 a[196:199],a[128:131],v[164:167],a[196:199]
buffer_load_dwordx4 v[160:163],v6,s[28:31],s77 offen sc1
v_mfma_f32_16x16x32_bf16 a[200:203],a[128:131],v[196:199],a[200:203]
ds_read_b64_tr_b16 a[128:129],v1 offset:192
ds_read_b64_tr_b16 a[130:131],v1 offset:5312
v_mfma_f32_16x16x32_bf16 a[200:203],a[132:135],v[204:207],a[200:203]
ds_read_b64_tr_b16 a[132:133],v1 offset:10432
v_mfma_f32_16x16x32_bf16 a[200:203],a[136:139],v[212:215],a[200:203]
ds_read_b64_tr_b16 a[134:135],v1 offset:15552
v_mfma_f32_16x16x32_bf16 a[200:203],a[140:143],v[220:223],a[200:203]
ds_read_b64_tr_b16 a[136:137],v1 offset:20672
v_mfma_f32_16x16x32_bf16 a[208:211],a[144:147],v[132:135],a[208:211]
buffer_load_dwordx4 v[192:195],v6,s[28:31],s78 offen sc1
ds_read_b64_tr_b16 a[138:139],v1 offset:25792
v_mfma_f32_16x16x32_bf16 a[208:211],a[148:151],v[140:143],a[208:211]
ds_read_b64_tr_b16 a[140:141],v1 offset:30912
ds_read_b64_tr_b16 a[142:143],v1 offset:36032
v_mfma_f32_16x16x32_bf16 a[208:211],a[152:155],v[148:151],a[208:211]
v_mfma_f32_16x16x32_bf16 a[208:211],a[156:159],v[156:159],a[208:211]
v_mfma_f32_16x16x32_bf16 a[212:215],a[156:159],v[188:191],a[212:215]
v_mfma_f32_16x16x32_bf16 a[212:215],a[152:155],v[180:183],a[212:215]
buffer_load_dwordx4 v[224:227],v6,s[28:31],s79 offen sc1
v_mfma_f32_16x16x32_bf16 a[212:215],a[148:151],v[172:175],a[212:215]
v_mfma_f32_16x16x32_bf16 a[212:215],a[144:147],v[164:167],a[212:215]
v_mfma_f32_16x16x32_bf16 a[216:219],a[144:147],v[196:199],a[216:219]
ds_read_b64_tr_b16 a[144:145],v2 offset:224
ds_read_b64_tr_b16 a[146:147],v2 offset:5344
v_mfma_f32_16x16x32_bf16 a[216:219],a[148:151],v[204:207],a[216:219]
ds_read_b64_tr_b16 a[148:149],v2 offset:10464
v_mfma_f32_16x16x32_bf16 a[216:219],a[152:155],v[212:215],a[216:219]
buffer_load_dwordx4 v[136:139],v6,s[28:31],s76 offen offset:1024 sc1
ds_read_b64_tr_b16 a[150:151],v2 offset:15584
v_mfma_f32_16x16x32_bf16 a[216:219],a[156:159],v[220:223],a[216:219]
s_waitcnt lgkmcnt(4)
ds_read_b64_tr_b16 a[152:153],v2 offset:20704
v_mfma_f32_16x16x32_bf16 a[224:227],a[160:163],v[132:135],a[224:227]
ds_read_b64_tr_b16 a[154:155],v2 offset:25824
ds_read_b64_tr_b16 a[156:157],v2 offset:30944
v_mfma_f32_16x16x32_bf16 a[224:227],a[164:167],v[140:143],a[224:227]
ds_read_b64_tr_b16 a[158:159],v2 offset:36064
v_mfma_f32_16x16x32_bf16 a[224:227],a[168:171],v[148:151],a[224:227]
v_mfma_f32_16x16x32_bf16 a[224:227],a[172:175],v[156:159],a[224:227]
buffer_load_dwordx4 v[168:171],v6,s[28:31],s77 offen offset:1024 sc1
v_mfma_f32_16x16x32_bf16 a[228:231],a[172:175],v[188:191],a[228:231]
v_mfma_f32_16x16x32_bf16 a[228:231],a[168:171],v[180:183],a[228:231]
v_mfma_f32_16x16x32_bf16 a[228:231],a[164:167],v[172:175],a[228:231]
v_mfma_f32_16x16x32_bf16 a[228:231],a[160:163],v[164:167],a[228:231]
v_mfma_f32_16x16x32_bf16 a[232:235],a[160:163],v[196:199],a[232:235]
buffer_load_dwordx4 v[200:203],v6,s[28:31],s78 offen offset:1024 sc1
ds_read_b64_tr_b16 a[160:161],v1 offset:256
v_mfma_f32_16x16x32_bf16 a[232:235],a[164:167],v[204:207],a[232:235]
ds_read_b64_tr_b16 a[162:163],v1 offset:5376
ds_read_b64_tr_b16 a[164:165],v1 offset:10496
v_mfma_f32_16x16x32_bf16 a[232:235],a[168:171],v[212:215],a[232:235]
ds_read_b64_tr_b16 a[166:167],v1 offset:15616
v_mfma_f32_16x16x32_bf16 a[232:235],a[172:175],v[220:223],a[232:235]
ds_read_b64_tr_b16 a[168:169],v1 offset:20736
v_mfma_f32_16x16x32_bf16 a[240:243],a[176:179],v[132:135],a[240:243]
ds_read_b64_tr_b16 a[170:171],v1 offset:25856
v_mfma_f32_16x16x32_bf16 a[240:243],a[180:183],v[140:143],a[240:243]
buffer_load_dwordx4 v[232:235],v6,s[28:31],s79 offen offset:1024 sc1
ds_read_b64_tr_b16 a[172:173],v1 offset:30976
v_mfma_f32_16x16x32_bf16 a[240:243],a[184:187],v[148:151],a[240:243]
ds_read_b64_tr_b16 a[174:175],v1 offset:36096
v_mfma_f32_16x16x32_bf16 a[240:243],a[188:191],v[156:159],a[240:243]
v_mfma_f32_16x16x32_bf16 a[244:247],a[188:191],v[188:191],a[244:247]
v_mfma_f32_16x16x32_bf16 a[244:247],a[184:187],v[180:183],a[244:247]
v_mfma_f32_16x16x32_bf16 a[244:247],a[180:183],v[172:175],a[244:247]
buffer_load_dwordx4 v[144:147],v6,s[28:31],s76 offen offset:2048 sc1
v_mfma_f32_16x16x32_bf16 a[244:247],a[176:179],v[164:167],a[244:247]
v_mfma_f32_16x16x32_bf16 a[248:251],a[176:179],v[196:199],a[248:251]
ds_read_b64_tr_b16 a[176:177],v2 offset:288
ds_read_b64_tr_b16 a[178:179],v2 offset:5408
v_mfma_f32_16x16x32_bf16 a[248:251],a[180:183],v[204:207],a[248:251]
ds_read_b64_tr_b16 a[180:181],v2 offset:10528
v_mfma_f32_16x16x32_bf16 a[248:251],a[184:187],v[212:215],a[248:251]
ds_read_b64_tr_b16 a[182:183],v2 offset:15648
v_mfma_f32_16x16x32_bf16 a[248:251],a[188:191],v[220:223],a[248:251]
buffer_load_dwordx4 v[176:179],v6,s[28:31],s77 offen offset:2048 sc1
ds_read_b64_tr_b16 a[184:185],v2 offset:20768
v_mfma_f32_16x16x32_bf16 a[0:3],a[96:99],v[132:135],a[0:3]
ds_read_b64_tr_b16 a[186:187],v2 offset:25888
ds_read_b64_tr_b16 a[188:189],v2 offset:31008
v_mfma_f32_16x16x32_bf16 a[0:3],a[100:103],v[140:143],a[0:3]
ds_read_b64_tr_b16 a[190:191],v2 offset:36128
v_mfma_f32_16x16x32_bf16 a[0:3],a[104:107],v[148:151],a[0:3]
v_mfma_f32_16x16x32_bf16 a[0:3],a[108:111],v[156:159],a[0:3]
v_mfma_f32_16x16x32_bf16 a[4:7],a[108:111],v[188:191],a[4:7]
buffer_load_dwordx4 v[208:211],v6,s[28:31],s78 offen offset:2048 sc1
v_mfma_f32_16x16x32_bf16 a[4:7],a[104:107],v[180:183],a[4:7]
v_mfma_f32_16x16x32_bf16 a[4:7],a[100:103],v[172:175],a[4:7]
v_mfma_f32_16x16x32_bf16 a[4:7],a[96:99],v[164:167],a[4:7]
v_mfma_f32_16x16x32_bf16 a[8:11],a[96:99],v[196:199],a[8:11]
v_mfma_f32_16x16x32_bf16 a[8:11],a[100:103],v[204:207],a[8:11]
buffer_load_dwordx4 v[240:243],v6,s[28:31],s79 offen offset:2048 sc1
v_mfma_f32_16x16x32_bf16 a[8:11],a[104:107],v[212:215],a[8:11]
v_mfma_f32_16x16x32_bf16 a[8:11],a[108:111],v[220:223],a[8:11]
v_mfma_f32_16x16x32_bf16 a[16:19],a[112:115],v[132:135],a[16:19]
v_mfma_f32_16x16x32_bf16 a[16:19],a[116:119],v[140:143],a[16:19]
v_mfma_f32_16x16x32_bf16 a[16:19],a[120:123],v[148:151],a[16:19]
buffer_load_dwordx4 v[152:155],v6,s[28:31],s76 offen offset:3072 sc1
v_mfma_f32_16x16x32_bf16 a[16:19],a[124:127],v[156:159],a[16:19]
s_mul_i32 s76,s70,12
s_add_u32 s76,s76,0
s_add_u32 s76,s76,s40
v_mfma_f32_16x16x32_bf16 a[20:23],a[124:127],v[188:191],a[20:23]
s_mul_i32 s76,s76,5120
v_mfma_f32_16x16x32_bf16 a[20:23],a[120:123],v[180:183],a[20:23]
v_mfma_f32_16x16x32_bf16 a[20:23],a[116:119],v[172:175],a[20:23]
v_mfma_f32_16x16x32_bf16 a[20:23],a[112:115],v[164:167],a[20:23]
buffer_load_dwordx4 v[184:187],v6,s[28:31],s77 offen offset:3072 sc1
v_mfma_f32_16x16x32_bf16 a[24:27],a[112:115],v[196:199],a[24:27]
s_mul_i32 s77,s70,12
s_add_u32 s77,s77,4
s_add_u32 s77,s77,s40
v_mfma_f32_16x16x32_bf16 a[24:27],a[116:119],v[204:207],a[24:27]
s_mul_i32 s77,s77,5120
v_mfma_f32_16x16x32_bf16 a[24:27],a[120:123],v[212:215],a[24:27]
v_mfma_f32_16x16x32_bf16 a[24:27],a[124:127],v[220:223],a[24:27]
v_mfma_f32_16x16x32_bf16 a[32:35],a[128:131],v[132:135],a[32:35]
buffer_load_dwordx4 v[216:219],v6,s[28:31],s78 offen offset:3072 sc1
v_mfma_f32_16x16x32_bf16 a[32:35],a[132:135],v[140:143],a[32:35]
s_mul_i32 s78,s70,12
s_add_u32 s78,s78,8
s_add_u32 s78,s78,s40
v_mfma_f32_16x16x32_bf16 a[32:35],a[136:139],v[148:151],a[32:35]
s_mul_i32 s78,s78,5120
v_mfma_f32_16x16x32_bf16 a[32:35],a[140:143],v[156:159],a[32:35]
v_mfma_f32_16x16x32_bf16 a[36:39],a[140:143],v[188:191],a[36:39]
v_mfma_f32_16x16x32_bf16 a[36:39],a[136:139],v[180:183],a[36:39]
buffer_load_dwordx4 v[248:251],v6,s[28:31],s79 offen offset:3072 sc1
v_mfma_f32_16x16x32_bf16 a[36:39],a[132:135],v[172:175],a[36:39]
v_mfma_f32_16x16x32_bf16 a[36:39],a[128:131],v[164:167],a[36:39]
v_mfma_f32_16x16x32_bf16 a[40:43],a[128:131],v[196:199],a[40:43]
v_mfma_f32_16x16x32_bf16 a[40:43],a[132:135],v[204:207],a[40:43]
v_mfma_f32_16x16x32_bf16 a[40:43],a[136:139],v[212:215],a[40:43]
v_mfma_f32_16x16x32_bf16 a[40:43],a[140:143],v[220:223],a[40:43]
s_waitcnt vmcnt(16)
s_barrier
s_waitcnt lgkmcnt(0)
v_mfma_f32_16x16x32_bf16 a[48:51],a[144:147],v[132:135],a[48:51]
ds_read_b64_tr_b16 a[96:97],v42
ds_read_b64_tr_b16 a[98:99],v42 offset:5120
v_mfma_f32_16x16x32_bf16 a[48:51],a[148:151],v[140:143],a[48:51]
ds_read_b64_tr_b16 a[100:101],v42 offset:10240
v_mfma_f32_16x16x32_bf16 a[48:51],a[152:155],v[148:151],a[48:51]
ds_read_b64_tr_b16 a[102:103],v42 offset:15360
v_mfma_f32_16x16x32_bf16 a[48:51],a[156:159],v[156:159],a[48:51]
ds_read_b64_tr_b16 a[104:105],v42 offset:20480
v_mfma_f32_16x16x32_bf16 a[52:55],a[156:159],v[188:191],a[52:55]
ds_read_b64_tr_b16 a[106:107],v42 offset:25600
ds_read_b64_tr_b16 a[108:109],v42 offset:30720
v_mfma_f32_16x16x32_bf16 a[52:55],a[152:155],v[180:183],a[52:55]
ds_read_b64_tr_b16 a[110:111],v42 offset:35840
v_mfma_f32_16x16x32_bf16 a[52:55],a[148:151],v[172:175],a[52:55]
ds_read_b64_tr_b16 a[112:113],v44
v_mfma_f32_16x16x32_bf16 a[52:55],a[144:147],v[164:167],a[52:55]
ds_read_b64_tr_b16 a[114:115],v44 offset:5120
v_mfma_f32_16x16x32_bf16 a[56:59],a[144:147],v[196:199],a[56:59]
ds_read_b64_tr_b16 a[116:117],v44 offset:10240
)A"
R"A(ds_read_b64_tr_b16 a[118:119],v44 offset:15360
v_mfma_f32_16x16x32_bf16 a[56:59],a[148:151],v[204:207],a[56:59]
ds_read_b64_tr_b16 a[120:121],v44 offset:20480
v_mfma_f32_16x16x32_bf16 a[56:59],a[152:155],v[212:215],a[56:59]
ds_read_b64_tr_b16 a[122:123],v44 offset:25600
v_mfma_f32_16x16x32_bf16 a[56:59],a[156:159],v[220:223],a[56:59]
ds_read_b64_tr_b16 a[124:125],v44 offset:30720
v_mfma_f32_16x16x32_bf16 a[64:67],a[160:163],v[132:135],a[64:67]
ds_read_b64_tr_b16 a[126:127],v44 offset:35840
ds_read_b64_tr_b16 a[128:129],v43 offset:32
v_mfma_f32_16x16x32_bf16 a[64:67],a[164:167],v[140:143],a[64:67]
ds_read_b64_tr_b16 a[130:131],v43 offset:5152
v_mfma_f32_16x16x32_bf16 a[64:67],a[168:171],v[148:151],a[64:67]
ds_read_b64_tr_b16 a[132:133],v43 offset:10272
v_mfma_f32_16x16x32_bf16 a[64:67],a[172:175],v[156:159],a[64:67]
ds_read_b64_tr_b16 a[134:135],v43 offset:15392
v_mfma_f32_16x16x32_bf16 a[68:71],a[172:175],v[188:191],a[68:71]
ds_read_b64_tr_b16 a[136:137],v43 offset:20512
ds_read_b64_tr_b16 a[138:139],v43 offset:25632
v_mfma_f32_16x16x32_bf16 a[68:71],a[168:171],v[180:183],a[68:71]
ds_read_b64_tr_b16 a[140:141],v43 offset:30752
v_mfma_f32_16x16x32_bf16 a[68:71],a[164:167],v[172:175],a[68:71]
ds_read_b64_tr_b16 a[142:143],v43 offset:35872
v_mfma_f32_16x16x32_bf16 a[68:71],a[160:163],v[164:167],a[68:71]
ds_read_b64_tr_b16 a[144:145],v45 offset:32
v_mfma_f32_16x16x32_bf16 a[72:75],a[160:163],v[196:199],a[72:75]
ds_read_b64_tr_b16 a[146:147],v45 offset:5152
ds_read_b64_tr_b16 a[148:149],v45 offset:10272
v_mfma_f32_16x16x32_bf16 a[72:75],a[164:167],v[204:207],a[72:75]
ds_read_b64_tr_b16 a[150:151],v45 offset:15392
v_mfma_f32_16x16x32_bf16 a[72:75],a[168:171],v[212:215],a[72:75]
ds_read_b64_tr_b16 a[152:153],v45 offset:20512
v_mfma_f32_16x16x32_bf16 a[72:75],a[172:175],v[220:223],a[72:75]
ds_read_b64_tr_b16 a[154:155],v45 offset:25632
v_mfma_f32_16x16x32_bf16 a[80:83],a[176:179],v[132:135],a[80:83]
ds_read_b64_tr_b16 a[156:157],v45 offset:30752
ds_read_b64_tr_b16 a[158:159],v45 offset:35872
v_mfma_f32_16x16x32_bf16 a[80:83],a[180:183],v[140:143],a[80:83]
ds_read_b64_tr_b16 a[160:161],v42 offset:64
v_mfma_f32_16x16x32_bf16 a[80:83],a[184:187],v[148:151],a[80:83]
ds_read_b64_tr_b16 a[162:163],v42 offset:5184
v_mfma_f32_16x16x32_bf16 a[80:83],a[188:191],v[156:159],a[80:83]
ds_read_b64_tr_b16 a[164:165],v42 offset:10304
v_mfma_f32_16x16x32_bf16 a[84:87],a[188:191],v[188:191],a[84:87]
ds_read_b64_tr_b16 a[166:167],v42 offset:15424
ds_read_b64_tr_b16 a[168:169],v42 offset:20544
v_mfma_f32_16x16x32_bf16 a[84:87],a[184:187],v[180:183],a[84:87]
ds_read_b64_tr_b16 a[170:171],v42 offset:25664
v_mfma_f32_16x16x32_bf16 a[84:87],a[180:183],v[172:175],a[84:87]
ds_read_b64_tr_b16 a[172:173],v42 offset:30784
v_mfma_f32_16x16x32_bf16 a[84:87],a[176:179],v[164:167],a[84:87]
ds_read_b64_tr_b16 a[174:175],v42 offset:35904
v_mfma_f32_16x16x32_bf16 a[88:91],a[176:179],v[196:199],a[88:91]
ds_read_b64_tr_b16 a[176:177],v44 offset:64
ds_read_b64_tr_b16 a[178:179],v44 offset:5184
v_mfma_f32_16x16x32_bf16 a[88:91],a[180:183],v[204:207],a[88:91]
ds_read_b64_tr_b16 a[180:181],v44 offset:10304
v_mfma_f32_16x16x32_bf16 a[88:91],a[184:187],v[212:215],a[88:91]
ds_read_b64_tr_b16 a[182:183],v44 offset:15424
v_mfma_f32_16x16x32_bf16 a[88:91],a[188:191],v[220:223],a[88:91]
s_waitcnt lgkmcnt(9)
ds_read_b64_tr_b16 a[184:185],v44 offset:20544
v_mfma_f32_16x16x32_bf16 a[192:195],a[96:99],v[64:67],a[192:195]
ds_read_b64_tr_b16 a[186:187],v44 offset:25664
ds_read_b64_tr_b16 a[188:189],v44 offset:30784
v_mfma_f32_16x16x32_bf16 a[192:195],a[100:103],v[68:71],a[192:195]
ds_read_b64_tr_b16 a[190:191],v44 offset:35904
v_mfma_f32_16x16x32_bf16 a[192:195],a[104:107],v[72:75],a[192:195]
v_mfma_f32_16x16x32_bf16 a[192:195],a[108:111],v[76:79],a[192:195]
v_mfma_f32_16x16x32_bf16 a[196:199],a[108:111],v[92:95],a[196:199]
v_mfma_f32_16x16x32_bf16 a[196:199],a[104:107],v[88:91],a[196:199]
v_mfma_f32_16x16x32_bf16 a[196:199],a[100:103],v[84:87],a[196:199]
v_mfma_f32_16x16x32_bf16 a[196:199],a[96:99],v[80:83],a[196:199]
v_mfma_f32_16x16x32_bf16 a[200:203],a[96:99],v[96:99],a[200:203]
ds_read_b64_tr_b16 a[96:97],v43 offset:96
ds_read_b64_tr_b16 a[98:99],v43 offset:5216
v_mfma_f32_16x16x32_bf16 a[200:203],a[100:103],v[100:103],a[200:203]
ds_read_b64_tr_b16 a[100:101],v43 offset:10336
v_mfma_f32_16x16x32_bf16 a[200:203],a[104:107],v[104:107],a[200:203]
ds_read_b64_tr_b16 a[102:103],v43 offset:15456
v_mfma_f32_16x16x32_bf16 a[200:203],a[108:111],v[108:111],a[200:203]
s_waitcnt vmcnt(4)
ds_read_b64_tr_b16 a[104:105],v43 offset:20576
v_mfma_f32_16x16x32_bf16 a[192:195],a[112:115],v[128:131],a[192:195]
ds_read_b64_tr_b16 a[106:107],v43 offset:25696
ds_read_b64_tr_b16 a[108:109],v43 offset:30816
v_mfma_f32_16x16x32_bf16 a[192:195],a[116:119],v[160:163],a[192:195]
ds_read_b64_tr_b16 a[110:111],v43 offset:35936
v_mfma_f32_16x16x32_bf16 a[192:195],a[120:123],v[192:195],a[192:195]
v_mfma_f32_16x16x32_bf16 a[192:195],a[124:127],v[224:227],a[192:195]
v_mfma_f32_16x16x32_bf16 a[196:199],a[124:127],v[232:235],a[196:199]
v_mfma_f32_16x16x32_bf16 a[196:199],a[120:123],v[200:203],a[196:199]
v_mfma_f32_16x16x32_bf16 a[196:199],a[116:119],v[168:171],a[196:199]
v_mfma_f32_16x16x32_bf16 a[196:199],a[112:115],v[136:139],a[196:199]
v_mfma_f32_16x16x32_bf16 a[200:203],a[112:115],v[144:147],a[200:203]
ds_read_b64_tr_b16 a[112:113],v45 offset:96
ds_read_b64_tr_b16 a[114:115],v45 offset:5216
v_mfma_f32_16x16x32_bf16 a[200:203],a[116:119],v[176:179],a[200:203]
v_accvgpr_read_b32 v132,a192
v_accvgpr_read_b32 v133,a193
v_mfma_f32_16x16x32_bf16 a[200:203],a[120:123],v[208:211],a[200:203]
v_cvt_pk_bf16_f32 v132,v132,v133
v_accvgpr_read_b32 v134,a194
v_mfma_f32_16x16x32_bf16 a[200:203],a[124:127],v[240:243],a[200:203]
v_accvgpr_read_b32 v135,a195
v_cvt_pk_bf16_f32 v133,v134,v135
v_mfma_f32_16x16x32_bf16 a[208:211],a[128:131],v[64:67],a[208:211]
ds_read_b64_tr_b16 a[116:117],v45 offset:10336
ds_read_b64_tr_b16 a[118:119],v45 offset:15456
v_mfma_f32_16x16x32_bf16 a[208:211],a[132:135],v[68:71],a[208:211]
v_accvgpr_read_b32 v164,a196
v_accvgpr_read_b32 v165,a197
v_mfma_f32_16x16x32_bf16 a[208:211],a[136:139],v[72:75],a[208:211]
v_cvt_pk_bf16_f32 v164,v164,v165
v_accvgpr_read_b32 v166,a198
v_mfma_f32_16x16x32_bf16 a[208:211],a[140:143],v[76:79],a[208:211]
v_accvgpr_read_b32 v167,a199
v_cvt_pk_bf16_f32 v165,v166,v167
v_mfma_f32_16x16x32_bf16 a[212:215],a[140:143],v[92:95],a[212:215]
ds_read_b64_tr_b16 a[120:121],v45 offset:20576
ds_read_b64_tr_b16 a[122:123],v45 offset:25696
v_mfma_f32_16x16x32_bf16 a[212:215],a[136:139],v[88:91],a[212:215]
ds_read_b64_tr_b16 a[124:125],v45 offset:30816
v_accvgpr_read_b32 v196,a200
v_mfma_f32_16x16x32_bf16 a[212:215],a[132:135],v[84:87],a[212:215]
ds_read_b64_tr_b16 a[126:127],v45 offset:35936
v_accvgpr_read_b32 v197,a201
v_mfma_f32_16x16x32_bf16 a[212:215],a[128:131],v[80:83],a[212:215]
v_cvt_pk_bf16_f32 v196,v196,v197
v_accvgpr_read_b32 v198,a202
v_mfma_f32_16x16x32_bf16 a[216:219],a[128:131],v[96:99],a[216:219]
v_accvgpr_read_b32 v199,a203
v_cvt_pk_bf16_f32 v197,v198,v199
v_mfma_f32_16x16x32_bf16 a[216:219],a[132:135],v[100:103],a[216:219]
ds_read_b64_tr_b16 a[128:129],v42 offset:128
ds_read_b64_tr_b16 a[130:131],v42 offset:5248
v_mfma_f32_16x16x32_bf16 a[216:219],a[136:139],v[104:107],a[216:219]
ds_read_b64_tr_b16 a[132:133],v42 offset:10368
v_mfma_f32_16x16x32_bf16 a[216:219],a[140:143],v[108:111],a[216:219]
ds_read_b64_tr_b16 a[134:135],v42 offset:15488
v_mfma_f32_16x16x32_bf16 a[208:211],a[144:147],v[128:131],a[208:211]
ds_read_b64_tr_b16 a[136:137],v42 offset:20608
v_mfma_f32_16x16x32_bf16 a[208:211],a[148:151],v[160:163],a[208:211]
ds_read_b64_tr_b16 a[138:139],v42 offset:25728
ds_read_b64_tr_b16 a[140:141],v42 offset:30848
v_mfma_f32_16x16x32_bf16 a[208:211],a[152:155],v[192:195],a[208:211]
ds_read_b64_tr_b16 a[142:143],v42 offset:35968
v_mfma_f32_16x16x32_bf16 a[208:211],a[156:159],v[224:227],a[208:211]
v_mfma_f32_16x16x32_bf16 a[212:215],a[156:159],v[232:235],a[212:215]
v_mfma_f32_16x16x32_bf16 a[212:215],a[152:155],v[200:203],a[212:215]
v_mfma_f32_16x16x32_bf16 a[212:215],a[148:151],v[168:171],a[212:215]
v_mfma_f32_16x16x32_bf16 a[212:215],a[144:147],v[136:139],a[212:215]
v_mfma_f32_16x16x32_bf16 a[216:219],a[144:147],v[144:147],a[216:219]
ds_read_b64_tr_b16 a[144:145],v44 offset:128
ds_read_b64_tr_b16 a[146:147],v44 offset:5248
v_mfma_f32_16x16x32_bf16 a[216:219],a[148:151],v[176:179],a[216:219]
v_accvgpr_read_b32 v134,a208
v_accvgpr_read_b32 v135,a209
v_mfma_f32_16x16x32_bf16 a[216:219],a[152:155],v[208:211],a[216:219]
v_accvgpr_read_b32 v12,a210
v_accvgpr_read_b32 v13,a211
v_mfma_f32_16x16x32_bf16 a[216:219],a[156:159],v[240:243],a[216:219]
v_cvt_pk_bf16_f32 v134,v134,v135
v_cvt_pk_bf16_f32 v135,v12,v13
v_mfma_f32_16x16x32_bf16 a[224:227],a[160:163],v[64:67],a[224:227]
s_waitcnt lgkmcnt(2)
buffer_store_dwordx4 v[132:135],v6,s[32:35],s84 offen sc1
ds_read_b64_tr_b16 a[148:149],v44 offset:10368
v_mfma_f32_16x16x32_bf16 a[224:227],a[164:167],v[68:71],a[224:227]
v_accvgpr_read_b32 v166,a212
v_accvgpr_read_b32 v167,a213
v_mfma_f32_16x16x32_bf16 a[224:227],a[168:171],v[72:75],a[224:227]
v_accvgpr_read_b32 v14,a214
v_accvgpr_read_b32 v15,a215
v_mfma_f32_16x16x32_bf16 a[224:227],a[172:175],v[76:79],a[224:227]
v_cvt_pk_bf16_f32 v166,v166,v167
v_cvt_pk_bf16_f32 v167,v14,v15
v_mfma_f32_16x16x32_bf16 a[228:231],a[172:175],v[92:95],a[228:231]
buffer_store_dwordx4 v[164:167],v6,s[32:35],s85 offen sc1
ds_read_b64_tr_b16 a[150:151],v44 offset:15488
v_mfma_f32_16x16x32_bf16 a[228:231],a[168:171],v[88:91],a[228:231]
v_accvgpr_read_b32 v198,a216
v_accvgpr_read_b32 v199,a217
v_mfma_f32_16x16x32_bf16 a[228:231],a[164:167],v[84:87],a[228:231]
v_accvgpr_read_b32 v16,a218
v_accvgpr_read_b32 v17,a219
v_mfma_f32_16x16x32_bf16 a[228:231],a[160:163],v[80:83],a[228:231]
v_cvt_pk_bf16_f32 v198,v198,v199
v_cvt_pk_bf16_f32 v199,v16,v17
v_mfma_f32_16x16x32_bf16 a[232:235],a[160:163],v[96:99],a[232:235]
buffer_store_dwordx4 v[196:199],v6,s[32:35],s86 offen sc1
ds_read_b64_tr_b16 a[152:153],v44 offset:20608
v_mfma_f32_16x16x32_bf16 a[232:235],a[164:167],v[100:103],a[232:235]
ds_read_b64_tr_b16 a[154:155],v44 offset:25728
ds_read_b64_tr_b16 a[156:157],v44 offset:30848
v_mfma_f32_16x16x32_bf16 a[232:235],a[168:171],v[104:107],a[232:235]
ds_read_b64_tr_b16 a[158:159],v44 offset:35968
v_mfma_f32_16x16x32_bf16 a[232:235],a[172:175],v[108:111],a[232:235]
ds_read_b64_tr_b16 a[160:161],v43 offset:160
v_mfma_f32_16x16x32_bf16 a[224:227],a[176:179],v[128:131],a[224:227]
ds_read_b64_tr_b16 a[162:163],v43 offset:5280
v_mfma_f32_16x16x32_bf16 a[224:227],a[180:183],v[160:163],a[224:227]
ds_read_b64_tr_b16 a[164:165],v43 offset:10400
ds_read_b64_tr_b16 a[166:167],v43 offset:15520
v_mfma_f32_16x16x32_bf16 a[224:227],a[184:187],v[192:195],a[224:227]
ds_read_b64_tr_b16 a[168:169],v43 offset:20640
v_mfma_f32_16x16x32_bf16 a[224:227],a[188:191],v[224:227],a[224:227]
ds_read_b64_tr_b16 a[170:171],v43 offset:25760
v_mfma_f32_16x16x32_bf16 a[228:231],a[188:191],v[232:235],a[228:231]
ds_read_b64_tr_b16 a[172:173],v43 offset:30880
v_mfma_f32_16x16x32_bf16 a[228:231],a[184:187],v[200:203],a[228:231]
ds_read_b64_tr_b16 a[174:175],v43 offset:36000
v_mfma_f32_16x16x32_bf16 a[228:231],a[180:183],v[168:171],a[228:231]
v_mfma_f32_16x16x32_bf16 a[228:231],a[176:179],v[136:139],a[228:231]
v_mfma_f32_16x16x32_bf16 a[232:235],a[176:179],v[144:147],a[232:235]
ds_read_b64_tr_b16 a[176:177],v45 offset:160
ds_read_b64_tr_b16 a[178:179],v45 offset:5280
v_mfma_f32_16x16x32_bf16 a[232:235],a[180:183],v[176:179],a[232:235]
v_accvgpr_read_b32 v140,a224
v_accvgpr_read_b32 v141,a225
v_mfma_f32_16x16x32_bf16 a[232:235],a[184:187],v[208:211],a[232:235]
v_cvt_pk_bf16_f32 v140,v140,v141
v_accvgpr_read_b32 v142,a226
v_mfma_f32_16x16x32_bf16 a[232:235],a[188:191],v[240:243],a[232:235]
v_accvgpr_read_b32 v143,a227
v_cvt_pk_bf16_f32 v141,v142,v143
v_mfma_f32_16x16x32_bf16 a[240:243],a[96:99],v[64:67],a[240:243]
ds_read_b64_tr_b16 a[180:181],v45 offset:10400
ds_read_b64_tr_b16 a[182:183],v45 offset:15520
v_mfma_f32_16x16x32_bf16 a[240:243],a[100:103],v[68:71],a[240:243]
)A"
R"A(v_accvgpr_read_b32 v172,a228
v_accvgpr_read_b32 v173,a229
v_mfma_f32_16x16x32_bf16 a[240:243],a[104:107],v[72:75],a[240:243]
v_cvt_pk_bf16_f32 v172,v172,v173
v_accvgpr_read_b32 v174,a230
v_mfma_f32_16x16x32_bf16 a[240:243],a[108:111],v[76:79],a[240:243]
v_accvgpr_read_b32 v175,a231
v_cvt_pk_bf16_f32 v173,v174,v175
v_mfma_f32_16x16x32_bf16 a[244:247],a[108:111],v[92:95],a[244:247]
ds_read_b64_tr_b16 a[184:185],v45 offset:20640
ds_read_b64_tr_b16 a[186:187],v45 offset:25760
v_mfma_f32_16x16x32_bf16 a[244:247],a[104:107],v[88:91],a[244:247]
ds_read_b64_tr_b16 a[188:189],v45 offset:30880
v_accvgpr_read_b32 v204,a232
v_mfma_f32_16x16x32_bf16 a[244:247],a[100:103],v[84:87],a[244:247]
ds_read_b64_tr_b16 a[190:191],v45 offset:36000
v_accvgpr_read_b32 v205,a233
v_mfma_f32_16x16x32_bf16 a[244:247],a[96:99],v[80:83],a[244:247]
v_cvt_pk_bf16_f32 v204,v204,v205
v_accvgpr_read_b32 v206,a234
v_mfma_f32_16x16x32_bf16 a[248:251],a[96:99],v[96:99],a[248:251]
v_accvgpr_read_b32 v207,a235
v_cvt_pk_bf16_f32 v205,v206,v207
v_mfma_f32_16x16x32_bf16 a[248:251],a[100:103],v[100:103],a[248:251]
ds_read_b64_tr_b16 a[96:97],v42 offset:192
ds_read_b64_tr_b16 a[98:99],v42 offset:5312
v_mfma_f32_16x16x32_bf16 a[248:251],a[104:107],v[104:107],a[248:251]
ds_read_b64_tr_b16 a[100:101],v42 offset:10432
v_mfma_f32_16x16x32_bf16 a[248:251],a[108:111],v[108:111],a[248:251]
ds_read_b64_tr_b16 a[102:103],v42 offset:15552
v_mfma_f32_16x16x32_bf16 a[240:243],a[112:115],v[128:131],a[240:243]
ds_read_b64_tr_b16 a[104:105],v42 offset:20672
v_mfma_f32_16x16x32_bf16 a[240:243],a[116:119],v[160:163],a[240:243]
ds_read_b64_tr_b16 a[106:107],v42 offset:25792
ds_read_b64_tr_b16 a[108:109],v42 offset:30912
v_mfma_f32_16x16x32_bf16 a[240:243],a[120:123],v[192:195],a[240:243]
ds_read_b64_tr_b16 a[110:111],v42 offset:36032
v_mfma_f32_16x16x32_bf16 a[240:243],a[124:127],v[224:227],a[240:243]
v_mfma_f32_16x16x32_bf16 a[244:247],a[124:127],v[232:235],a[244:247]
v_mfma_f32_16x16x32_bf16 a[244:247],a[120:123],v[200:203],a[244:247]
v_mfma_f32_16x16x32_bf16 a[244:247],a[116:119],v[168:171],a[244:247]
v_mfma_f32_16x16x32_bf16 a[244:247],a[112:115],v[136:139],a[244:247]
v_mfma_f32_16x16x32_bf16 a[248:251],a[112:115],v[144:147],a[248:251]
ds_read_b64_tr_b16 a[112:113],v44 offset:192
ds_read_b64_tr_b16 a[114:115],v44 offset:5312
v_mfma_f32_16x16x32_bf16 a[248:251],a[116:119],v[176:179],a[248:251]
v_accvgpr_read_b32 v142,a240
v_accvgpr_read_b32 v143,a241
v_mfma_f32_16x16x32_bf16 a[248:251],a[120:123],v[208:211],a[248:251]
v_accvgpr_read_b32 v12,a242
v_accvgpr_read_b32 v13,a243
v_mfma_f32_16x16x32_bf16 a[248:251],a[124:127],v[240:243],a[248:251]
v_cvt_pk_bf16_f32 v142,v142,v143
v_cvt_pk_bf16_f32 v143,v12,v13
v_mfma_f32_16x16x32_bf16 a[0:3],a[128:131],v[64:67],a[0:3]
buffer_store_dwordx4 v[140:143],v6,s[32:35],s84 offen offset:1024 sc1
ds_read_b64_tr_b16 a[116:117],v44 offset:10432
v_mfma_f32_16x16x32_bf16 a[0:3],a[132:135],v[68:71],a[0:3]
v_accvgpr_read_b32 v174,a244
v_accvgpr_read_b32 v175,a245
v_mfma_f32_16x16x32_bf16 a[0:3],a[136:139],v[72:75],a[0:3]
v_accvgpr_read_b32 v14,a246
v_accvgpr_read_b32 v15,a247
v_mfma_f32_16x16x32_bf16 a[0:3],a[140:143],v[76:79],a[0:3]
v_cvt_pk_bf16_f32 v174,v174,v175
v_cvt_pk_bf16_f32 v175,v14,v15
v_mfma_f32_16x16x32_bf16 a[4:7],a[140:143],v[92:95],a[4:7]
buffer_store_dwordx4 v[172:175],v6,s[32:35],s85 offen offset:1024 sc1
ds_read_b64_tr_b16 a[118:119],v44 offset:15552
v_mfma_f32_16x16x32_bf16 a[4:7],a[136:139],v[88:91],a[4:7]
v_accvgpr_read_b32 v206,a248
v_accvgpr_read_b32 v207,a249
v_mfma_f32_16x16x32_bf16 a[4:7],a[132:135],v[84:87],a[4:7]
v_accvgpr_read_b32 v16,a250
v_accvgpr_read_b32 v17,a251
v_mfma_f32_16x16x32_bf16 a[4:7],a[128:131],v[80:83],a[4:7]
v_cvt_pk_bf16_f32 v206,v206,v207
v_cvt_pk_bf16_f32 v207,v16,v17
v_mfma_f32_16x16x32_bf16 a[8:11],a[128:131],v[96:99],a[8:11]
buffer_store_dwordx4 v[204:207],v6,s[32:35],s86 offen offset:1024 sc1
ds_read_b64_tr_b16 a[120:121],v44 offset:20672
v_mfma_f32_16x16x32_bf16 a[8:11],a[132:135],v[100:103],a[8:11]
ds_read_b64_tr_b16 a[122:123],v44 offset:25792
ds_read_b64_tr_b16 a[124:125],v44 offset:30912
v_mfma_f32_16x16x32_bf16 a[8:11],a[136:139],v[104:107],a[8:11]
ds_read_b64_tr_b16 a[126:127],v44 offset:36032
v_mfma_f32_16x16x32_bf16 a[8:11],a[140:143],v[108:111],a[8:11]
s_waitcnt lgkmcnt(5)
ds_read_b64_tr_b16 a[128:129],v43 offset:224
v_mfma_f32_16x16x32_bf16 a[0:3],a[144:147],v[128:131],a[0:3]
ds_read_b64_tr_b16 a[130:131],v43 offset:5344
v_mfma_f32_16x16x32_bf16 a[0:3],a[148:151],v[160:163],a[0:3]
ds_read_b64_tr_b16 a[132:133],v43 offset:10464
ds_read_b64_tr_b16 a[134:135],v43 offset:15584
v_mfma_f32_16x16x32_bf16 a[0:3],a[152:155],v[192:195],a[0:3]
ds_read_b64_tr_b16 a[136:137],v43 offset:20704
v_mfma_f32_16x16x32_bf16 a[0:3],a[156:159],v[224:227],a[0:3]
ds_read_b64_tr_b16 a[138:139],v43 offset:25824
v_mfma_f32_16x16x32_bf16 a[4:7],a[156:159],v[232:235],a[4:7]
ds_read_b64_tr_b16 a[140:141],v43 offset:30944
v_mfma_f32_16x16x32_bf16 a[4:7],a[152:155],v[200:203],a[4:7]
ds_read_b64_tr_b16 a[142:143],v43 offset:36064
v_mfma_f32_16x16x32_bf16 a[4:7],a[148:151],v[168:171],a[4:7]
v_mfma_f32_16x16x32_bf16 a[4:7],a[144:147],v[136:139],a[4:7]
v_mfma_f32_16x16x32_bf16 a[8:11],a[144:147],v[144:147],a[8:11]
ds_read_b64_tr_b16 a[144:145],v45 offset:224
ds_read_b64_tr_b16 a[146:147],v45 offset:5344
v_mfma_f32_16x16x32_bf16 a[8:11],a[148:151],v[176:179],a[8:11]
v_accvgpr_read_b32 v148,a0
v_accvgpr_read_b32 v149,a1
v_mfma_f32_16x16x32_bf16 a[8:11],a[152:155],v[208:211],a[8:11]
v_cvt_pk_bf16_f32 v148,v148,v149
v_accvgpr_read_b32 v150,a2
v_mfma_f32_16x16x32_bf16 a[8:11],a[156:159],v[240:243],a[8:11]
v_accvgpr_read_b32 v151,a3
v_cvt_pk_bf16_f32 v149,v150,v151
v_mfma_f32_16x16x32_bf16 a[16:19],a[160:163],v[64:67],a[16:19]
ds_read_b64_tr_b16 a[148:149],v45 offset:10464
ds_read_b64_tr_b16 a[150:151],v45 offset:15584
v_mfma_f32_16x16x32_bf16 a[16:19],a[164:167],v[68:71],a[16:19]
v_accvgpr_read_b32 v180,a4
v_accvgpr_read_b32 v181,a5
v_mfma_f32_16x16x32_bf16 a[16:19],a[168:171],v[72:75],a[16:19]
v_cvt_pk_bf16_f32 v180,v180,v181
v_accvgpr_read_b32 v182,a6
v_mfma_f32_16x16x32_bf16 a[16:19],a[172:175],v[76:79],a[16:19]
v_accvgpr_read_b32 v183,a7
v_cvt_pk_bf16_f32 v181,v182,v183
v_mfma_f32_16x16x32_bf16 a[20:23],a[172:175],v[92:95],a[20:23]
ds_read_b64_tr_b16 a[152:153],v45 offset:20704
ds_read_b64_tr_b16 a[154:155],v45 offset:25824
v_mfma_f32_16x16x32_bf16 a[20:23],a[168:171],v[88:91],a[20:23]
ds_read_b64_tr_b16 a[156:157],v45 offset:30944
v_accvgpr_read_b32 v212,a8
v_mfma_f32_16x16x32_bf16 a[20:23],a[164:167],v[84:87],a[20:23]
ds_read_b64_tr_b16 a[158:159],v45 offset:36064
v_accvgpr_read_b32 v213,a9
v_mfma_f32_16x16x32_bf16 a[20:23],a[160:163],v[80:83],a[20:23]
v_cvt_pk_bf16_f32 v212,v212,v213
v_accvgpr_read_b32 v214,a10
v_mfma_f32_16x16x32_bf16 a[24:27],a[160:163],v[96:99],a[24:27]
v_accvgpr_read_b32 v215,a11
v_cvt_pk_bf16_f32 v213,v214,v215
v_mfma_f32_16x16x32_bf16 a[24:27],a[164:167],v[100:103],a[24:27]
ds_read_b64_tr_b16 a[160:161],v42 offset:256
ds_read_b64_tr_b16 a[162:163],v42 offset:5376
v_mfma_f32_16x16x32_bf16 a[24:27],a[168:171],v[104:107],a[24:27]
ds_read_b64_tr_b16 a[164:165],v42 offset:10496
v_mfma_f32_16x16x32_bf16 a[24:27],a[172:175],v[108:111],a[24:27]
ds_read_b64_tr_b16 a[166:167],v42 offset:15616
v_mfma_f32_16x16x32_bf16 a[16:19],a[176:179],v[128:131],a[16:19]
ds_read_b64_tr_b16 a[168:169],v42 offset:20736
v_mfma_f32_16x16x32_bf16 a[16:19],a[180:183],v[160:163],a[16:19]
ds_read_b64_tr_b16 a[170:171],v42 offset:25856
ds_read_b64_tr_b16 a[172:173],v42 offset:30976
v_mfma_f32_16x16x32_bf16 a[16:19],a[184:187],v[192:195],a[16:19]
ds_read_b64_tr_b16 a[174:175],v42 offset:36096
v_mfma_f32_16x16x32_bf16 a[16:19],a[188:191],v[224:227],a[16:19]
v_mfma_f32_16x16x32_bf16 a[20:23],a[188:191],v[232:235],a[20:23]
v_mfma_f32_16x16x32_bf16 a[20:23],a[184:187],v[200:203],a[20:23]
v_mfma_f32_16x16x32_bf16 a[20:23],a[180:183],v[168:171],a[20:23]
v_mfma_f32_16x16x32_bf16 a[20:23],a[176:179],v[136:139],a[20:23]
v_mfma_f32_16x16x32_bf16 a[24:27],a[176:179],v[144:147],a[24:27]
ds_read_b64_tr_b16 a[176:177],v44 offset:256
ds_read_b64_tr_b16 a[178:179],v44 offset:5376
v_mfma_f32_16x16x32_bf16 a[24:27],a[180:183],v[176:179],a[24:27]
v_accvgpr_read_b32 v150,a16
v_accvgpr_read_b32 v151,a17
v_mfma_f32_16x16x32_bf16 a[24:27],a[184:187],v[208:211],a[24:27]
v_accvgpr_read_b32 v12,a18
v_accvgpr_read_b32 v13,a19
v_mfma_f32_16x16x32_bf16 a[24:27],a[188:191],v[240:243],a[24:27]
v_cvt_pk_bf16_f32 v150,v150,v151
v_cvt_pk_bf16_f32 v151,v12,v13
v_mfma_f32_16x16x32_bf16 a[32:35],a[96:99],v[64:67],a[32:35]
buffer_store_dwordx4 v[148:151],v6,s[32:35],s84 offen offset:2048 sc1
ds_read_b64_tr_b16 a[180:181],v44 offset:10496
v_mfma_f32_16x16x32_bf16 a[32:35],a[100:103],v[68:71],a[32:35]
v_accvgpr_read_b32 v182,a20
v_accvgpr_read_b32 v183,a21
v_mfma_f32_16x16x32_bf16 a[32:35],a[104:107],v[72:75],a[32:35]
v_accvgpr_read_b32 v14,a22
v_accvgpr_read_b32 v15,a23
v_mfma_f32_16x16x32_bf16 a[32:35],a[108:111],v[76:79],a[32:35]
v_cvt_pk_bf16_f32 v182,v182,v183
v_cvt_pk_bf16_f32 v183,v14,v15
v_mfma_f32_16x16x32_bf16 a[36:39],a[108:111],v[92:95],a[36:39]
buffer_store_dwordx4 v[180:183],v6,s[32:35],s85 offen offset:2048 sc1
ds_read_b64_tr_b16 a[182:183],v44 offset:15616
v_mfma_f32_16x16x32_bf16 a[36:39],a[104:107],v[88:91],a[36:39]
v_accvgpr_read_b32 v214,a24
v_accvgpr_read_b32 v215,a25
v_mfma_f32_16x16x32_bf16 a[36:39],a[100:103],v[84:87],a[36:39]
v_accvgpr_read_b32 v16,a26
v_accvgpr_read_b32 v17,a27
v_mfma_f32_16x16x32_bf16 a[36:39],a[96:99],v[80:83],a[36:39]
v_cvt_pk_bf16_f32 v214,v214,v215
v_cvt_pk_bf16_f32 v215,v16,v17
v_mfma_f32_16x16x32_bf16 a[40:43],a[96:99],v[96:99],a[40:43]
buffer_store_dwordx4 v[212:215],v6,s[32:35],s86 offen offset:2048 sc1
ds_read_b64_tr_b16 a[184:185],v44 offset:20736
v_mfma_f32_16x16x32_bf16 a[40:43],a[100:103],v[100:103],a[40:43]
ds_read_b64_tr_b16 a[186:187],v44 offset:25856
ds_read_b64_tr_b16 a[188:189],v44 offset:30976
v_mfma_f32_16x16x32_bf16 a[40:43],a[104:107],v[104:107],a[40:43]
ds_read_b64_tr_b16 a[190:191],v44 offset:36096
v_mfma_f32_16x16x32_bf16 a[40:43],a[108:111],v[108:111],a[40:43]
ds_read_b64_tr_b16 a[96:97],v43 offset:288
v_mfma_f32_16x16x32_bf16 a[32:35],a[112:115],v[128:131],a[32:35]
s_waitcnt lgkmcnt(5)
ds_read_b64_tr_b16 a[98:99],v43 offset:5408
v_mfma_f32_16x16x32_bf16 a[32:35],a[116:119],v[160:163],a[32:35]
ds_read_b64_tr_b16 a[100:101],v43 offset:10528
ds_read_b64_tr_b16 a[102:103],v43 offset:15648
v_mfma_f32_16x16x32_bf16 a[32:35],a[120:123],v[192:195],a[32:35]
ds_read_b64_tr_b16 a[104:105],v43 offset:20768
v_mfma_f32_16x16x32_bf16 a[32:35],a[124:127],v[224:227],a[32:35]
ds_read_b64_tr_b16 a[106:107],v43 offset:25888
v_mfma_f32_16x16x32_bf16 a[36:39],a[124:127],v[232:235],a[36:39]
ds_read_b64_tr_b16 a[108:109],v43 offset:31008
v_mfma_f32_16x16x32_bf16 a[36:39],a[120:123],v[200:203],a[36:39]
ds_read_b64_tr_b16 a[110:111],v43 offset:36128
v_mfma_f32_16x16x32_bf16 a[36:39],a[116:119],v[168:171],a[36:39]
v_mfma_f32_16x16x32_bf16 a[36:39],a[112:115],v[136:139],a[36:39]
v_mfma_f32_16x16x32_bf16 a[40:43],a[112:115],v[144:147],a[40:43]
ds_read_b64_tr_b16 a[112:113],v45 offset:288
ds_read_b64_tr_b16 a[114:115],v45 offset:5408
v_mfma_f32_16x16x32_bf16 a[40:43],a[116:119],v[176:179],a[40:43]
v_accvgpr_read_b32 v156,a32
v_accvgpr_read_b32 v157,a33
v_mfma_f32_16x16x32_bf16 a[40:43],a[120:123],v[208:211],a[40:43]
v_cvt_pk_bf16_f32 v156,v156,v157
v_accvgpr_read_b32 v158,a34
v_mfma_f32_16x16x32_bf16 a[40:43],a[124:127],v[240:243],a[40:43]
v_accvgpr_read_b32 v159,a35
v_cvt_pk_bf16_f32 v157,v158,v159
v_mfma_f32_16x16x32_bf16 a[48:51],a[128:131],v[64:67],a[48:51]
ds_read_b64_tr_b16 a[116:117],v45 offset:10528
ds_read_b64_tr_b16 a[118:119],v45 offset:15648
v_mfma_f32_16x16x32_bf16 a[48:51],a[132:135],v[68:71],a[48:51]
v_accvgpr_read_b32 v188,a36
v_accvgpr_read_b32 v189,a37
v_mfma_f32_16x16x32_bf16 a[48:51],a[136:139],v[72:75],a[48:51]
v_cvt_pk_bf16_f32 v188,v188,v189
v_accvgpr_read_b32 v190,a38
v_mfma_f32_16x16x32_bf16 a[48:51],a[140:143],v[76:79],a[48:51]
v_accvgpr_read_b32 v191,a39
v_cvt_pk_bf16_f32 v189,v190,v191
)A"
R"A(v_mfma_f32_16x16x32_bf16 a[52:55],a[140:143],v[92:95],a[52:55]
ds_read_b64_tr_b16 a[120:121],v45 offset:20768
ds_read_b64_tr_b16 a[122:123],v45 offset:25888
v_mfma_f32_16x16x32_bf16 a[52:55],a[136:139],v[88:91],a[52:55]
v_accvgpr_read_b32 v220,a40
v_accvgpr_read_b32 v221,a41
v_mfma_f32_16x16x32_bf16 a[52:55],a[132:135],v[84:87],a[52:55]
v_cvt_pk_bf16_f32 v220,v220,v221
v_accvgpr_read_b32 v222,a42
v_mfma_f32_16x16x32_bf16 a[52:55],a[128:131],v[80:83],a[52:55]
v_accvgpr_read_b32 v223,a43
v_cvt_pk_bf16_f32 v221,v222,v223
v_mfma_f32_16x16x32_bf16 a[56:59],a[128:131],v[96:99],a[56:59]
ds_read_b64_tr_b16 a[124:125],v45 offset:31008
ds_read_b64_tr_b16 a[126:127],v45 offset:36128
v_mfma_f32_16x16x32_bf16 a[56:59],a[132:135],v[100:103],a[56:59]
ds_read_b64_tr_b16 a[128:129],v1
v_mfma_f32_16x16x32_bf16 a[56:59],a[136:139],v[104:107],a[56:59]
ds_read_b64_tr_b16 a[130:131],v1 offset:5120
v_mfma_f32_16x16x32_bf16 a[56:59],a[140:143],v[108:111],a[56:59]
ds_read_b64_tr_b16 a[132:133],v1 offset:10240
v_mfma_f32_16x16x32_bf16 a[48:51],a[144:147],v[128:131],a[48:51]
ds_read_b64_tr_b16 a[134:135],v1 offset:15360
ds_read_b64_tr_b16 a[136:137],v1 offset:20480
v_mfma_f32_16x16x32_bf16 a[48:51],a[148:151],v[160:163],a[48:51]
ds_read_b64_tr_b16 a[138:139],v1 offset:25600
v_mfma_f32_16x16x32_bf16 a[48:51],a[152:155],v[192:195],a[48:51]
ds_read_b64_tr_b16 a[140:141],v1 offset:30720
v_mfma_f32_16x16x32_bf16 a[48:51],a[156:159],v[224:227],a[48:51]
ds_read_b64_tr_b16 a[142:143],v1 offset:35840
v_mfma_f32_16x16x32_bf16 a[52:55],a[156:159],v[232:235],a[52:55]
v_mfma_f32_16x16x32_bf16 a[52:55],a[152:155],v[200:203],a[52:55]
v_mfma_f32_16x16x32_bf16 a[52:55],a[148:151],v[168:171],a[52:55]
v_mfma_f32_16x16x32_bf16 a[52:55],a[144:147],v[136:139],a[52:55]
v_mfma_f32_16x16x32_bf16 a[56:59],a[144:147],v[144:147],a[56:59]
ds_read_b64_tr_b16 a[144:145],v42
ds_read_b64_tr_b16 a[146:147],v42 offset:5120
v_mfma_f32_16x16x32_bf16 a[56:59],a[148:151],v[176:179],a[56:59]
v_accvgpr_read_b32 v158,a48
v_accvgpr_read_b32 v159,a49
v_mfma_f32_16x16x32_bf16 a[56:59],a[152:155],v[208:211],a[56:59]
v_accvgpr_read_b32 v12,a50
v_accvgpr_read_b32 v13,a51
v_mfma_f32_16x16x32_bf16 a[56:59],a[156:159],v[240:243],a[56:59]
v_cvt_pk_bf16_f32 v158,v158,v159
v_cvt_pk_bf16_f32 v159,v12,v13
v_mfma_f32_16x16x32_bf16 a[64:67],a[160:163],v[64:67],a[64:67]
buffer_store_dwordx4 v[156:159],v6,s[32:35],s84 offen offset:3072 sc1
ds_read_b64_tr_b16 a[148:149],v42 offset:10240
v_mfma_f32_16x16x32_bf16 a[64:67],a[164:167],v[68:71],a[64:67]
v_accvgpr_read_b32 v190,a52
v_accvgpr_read_b32 v191,a53
v_mfma_f32_16x16x32_bf16 a[64:67],a[168:171],v[72:75],a[64:67]
v_accvgpr_read_b32 v14,a54
v_accvgpr_read_b32 v15,a55
v_mfma_f32_16x16x32_bf16 a[64:67],a[172:175],v[76:79],a[64:67]
v_cvt_pk_bf16_f32 v190,v190,v191
v_cvt_pk_bf16_f32 v191,v14,v15
v_mfma_f32_16x16x32_bf16 a[68:71],a[172:175],v[92:95],a[68:71]
buffer_store_dwordx4 v[188:191],v6,s[32:35],s85 offen offset:3072 sc1
ds_read_b64_tr_b16 a[150:151],v42 offset:15360
v_mfma_f32_16x16x32_bf16 a[68:71],a[168:171],v[88:91],a[68:71]
v_accvgpr_read_b32 v222,a56
v_accvgpr_read_b32 v223,a57
v_mfma_f32_16x16x32_bf16 a[68:71],a[164:167],v[84:87],a[68:71]
v_accvgpr_read_b32 v16,a58
v_accvgpr_read_b32 v17,a59
v_mfma_f32_16x16x32_bf16 a[68:71],a[160:163],v[80:83],a[68:71]
v_cvt_pk_bf16_f32 v222,v222,v223
v_cvt_pk_bf16_f32 v223,v16,v17
v_mfma_f32_16x16x32_bf16 a[72:75],a[160:163],v[96:99],a[72:75]
buffer_store_dwordx4 v[220:223],v6,s[32:35],s86 offen offset:3072 sc1
ds_read_b64_tr_b16 a[152:153],v42 offset:20480
v_mfma_f32_16x16x32_bf16 a[72:75],a[164:167],v[100:103],a[72:75]
ds_read_b64_tr_b16 a[154:155],v42 offset:25600
ds_read_b64_tr_b16 a[156:157],v42 offset:30720
v_mfma_f32_16x16x32_bf16 a[72:75],a[168:171],v[104:107],a[72:75]
ds_read_b64_tr_b16 a[158:159],v42 offset:35840
v_mfma_f32_16x16x32_bf16 a[72:75],a[172:175],v[108:111],a[72:75]
ds_read_b64_tr_b16 a[160:161],v44
v_mfma_f32_16x16x32_bf16 a[64:67],a[176:179],v[128:131],a[64:67]
ds_read_b64_tr_b16 a[162:163],v44 offset:5120
v_mfma_f32_16x16x32_bf16 a[64:67],a[180:183],v[160:163],a[64:67]
s_waitcnt lgkmcnt(6)
ds_read_b64_tr_b16 a[164:165],v44 offset:10240
ds_read_b64_tr_b16 a[166:167],v44 offset:15360
v_mfma_f32_16x16x32_bf16 a[64:67],a[184:187],v[192:195],a[64:67]
ds_read_b64_tr_b16 a[168:169],v44 offset:20480
v_mfma_f32_16x16x32_bf16 a[64:67],a[188:191],v[224:227],a[64:67]
ds_read_b64_tr_b16 a[170:171],v44 offset:25600
v_mfma_f32_16x16x32_bf16 a[68:71],a[188:191],v[232:235],a[68:71]
ds_read_b64_tr_b16 a[172:173],v44 offset:30720
v_mfma_f32_16x16x32_bf16 a[68:71],a[184:187],v[200:203],a[68:71]
ds_read_b64_tr_b16 a[174:175],v44 offset:35840
v_mfma_f32_16x16x32_bf16 a[68:71],a[180:183],v[168:171],a[68:71]
v_mfma_f32_16x16x32_bf16 a[68:71],a[176:179],v[136:139],a[68:71]
v_mfma_f32_16x16x32_bf16 a[72:75],a[176:179],v[144:147],a[72:75]
ds_read_b64_tr_b16 a[176:177],v2 offset:32
ds_read_b64_tr_b16 a[178:179],v2 offset:5152
v_mfma_f32_16x16x32_bf16 a[72:75],a[180:183],v[176:179],a[72:75]
ds_read_b64_tr_b16 a[180:181],v2 offset:10272
v_accvgpr_read_b32 v132,a64
v_mfma_f32_16x16x32_bf16 a[72:75],a[184:187],v[208:211],a[72:75]
ds_read_b64_tr_b16 a[182:183],v2 offset:15392
v_accvgpr_read_b32 v133,a65
v_mfma_f32_16x16x32_bf16 a[72:75],a[188:191],v[240:243],a[72:75]
ds_read_b64_tr_b16 a[184:185],v2 offset:20512
v_cvt_pk_bf16_f32 v132,v132,v133
v_mfma_f32_16x16x32_bf16 a[80:83],a[96:99],v[64:67],a[80:83]
ds_read_b64_tr_b16 a[186:187],v2 offset:25632
ds_read_b64_tr_b16 a[188:189],v2 offset:30752
v_mfma_f32_16x16x32_bf16 a[80:83],a[100:103],v[68:71],a[80:83]
ds_read_b64_tr_b16 a[190:191],v2 offset:35872
v_accvgpr_read_b32 v134,a66
v_mfma_f32_16x16x32_bf16 a[80:83],a[104:107],v[72:75],a[80:83]
v_accvgpr_read_b32 v135,a67
v_cvt_pk_bf16_f32 v133,v134,v135
v_mfma_f32_16x16x32_bf16 a[80:83],a[108:111],v[76:79],a[80:83]
v_accvgpr_read_b32 v164,a68
v_accvgpr_read_b32 v165,a69
v_mfma_f32_16x16x32_bf16 a[84:87],a[108:111],v[92:95],a[84:87]
v_cvt_pk_bf16_f32 v164,v164,v165
v_accvgpr_read_b32 v166,a70
v_mfma_f32_16x16x32_bf16 a[84:87],a[104:107],v[88:91],a[84:87]
v_accvgpr_read_b32 v167,a71
v_cvt_pk_bf16_f32 v165,v166,v167
v_mfma_f32_16x16x32_bf16 a[84:87],a[100:103],v[84:87],a[84:87]
v_accvgpr_read_b32 v196,a72
v_accvgpr_read_b32 v197,a73
v_mfma_f32_16x16x32_bf16 a[84:87],a[96:99],v[80:83],a[84:87]
v_cvt_pk_bf16_f32 v196,v196,v197
v_accvgpr_read_b32 v198,a74
v_mfma_f32_16x16x32_bf16 a[88:91],a[96:99],v[96:99],a[88:91]
ds_read_b64_tr_b16 a[96:97],v43 offset:32
ds_read_b64_tr_b16 a[98:99],v43 offset:5152
v_mfma_f32_16x16x32_bf16 a[88:91],a[100:103],v[100:103],a[88:91]
ds_read_b64_tr_b16 a[100:101],v43 offset:10272
v_accvgpr_read_b32 v199,a75
v_mfma_f32_16x16x32_bf16 a[88:91],a[104:107],v[104:107],a[88:91]
ds_read_b64_tr_b16 a[102:103],v43 offset:15392
v_cvt_pk_bf16_f32 v197,v198,v199
v_mfma_f32_16x16x32_bf16 a[88:91],a[108:111],v[108:111],a[88:91]
ds_read_b64_tr_b16 a[104:105],v43 offset:20512
v_mfma_f32_16x16x32_bf16 a[80:83],a[112:115],v[128:131],a[80:83]
ds_read_b64_tr_b16 a[106:107],v43 offset:25632
ds_read_b64_tr_b16 a[108:109],v43 offset:30752
v_mfma_f32_16x16x32_bf16 a[80:83],a[116:119],v[160:163],a[80:83]
ds_read_b64_tr_b16 a[110:111],v43 offset:35872
v_mfma_f32_16x16x32_bf16 a[80:83],a[120:123],v[192:195],a[80:83]
v_mfma_f32_16x16x32_bf16 a[80:83],a[124:127],v[224:227],a[80:83]
v_mfma_f32_16x16x32_bf16 a[84:87],a[124:127],v[232:235],a[84:87]
v_mfma_f32_16x16x32_bf16 a[84:87],a[120:123],v[200:203],a[84:87]
v_mfma_f32_16x16x32_bf16 a[84:87],a[116:119],v[168:171],a[84:87]
v_mfma_f32_16x16x32_bf16 a[84:87],a[112:115],v[136:139],a[84:87]
v_mfma_f32_16x16x32_bf16 a[88:91],a[112:115],v[144:147],a[88:91]
ds_read_b64_tr_b16 a[112:113],v45 offset:32
ds_read_b64_tr_b16 a[114:115],v45 offset:5152
v_mfma_f32_16x16x32_bf16 a[88:91],a[116:119],v[176:179],a[88:91]
ds_read_b64_tr_b16 a[116:117],v45 offset:10272
v_accvgpr_read_b32 v134,a80
v_mfma_f32_16x16x32_bf16 a[88:91],a[120:123],v[208:211],a[88:91]
ds_read_b64_tr_b16 a[118:119],v45 offset:15392
v_accvgpr_read_b32 v135,a81
v_mfma_f32_16x16x32_bf16 a[88:91],a[124:127],v[240:243],a[88:91]
ds_read_b64_tr_b16 a[120:121],v45 offset:20512
v_accvgpr_read_b32 v12,a82
v_mfma_f32_16x16x32_bf16 a[204:207],a[128:131],v[228:231],a[204:207]
s_lshl_b32 s94,s71,2
ds_read_b64_tr_b16 a[122:123],v45 offset:25632
v_accvgpr_read_b32 v13,a83
v_mfma_f32_16x16x32_bf16 a[204:207],a[132:135],v[236:239],a[204:207]
s_add_u32 s94,s94,s40
v_cvt_pk_bf16_f32 v134,v134,v135
v_cvt_pk_bf16_f32 v135,v12,v13
v_mfma_f32_16x16x32_bf16 a[204:207],a[136:139],v[244:247],a[204:207]
s_lshl_b32 s95,s42,2
s_add_u32 s95,s95,s40
buffer_store_dwordx4 v[132:135],v25,s[32:35],s84 offen sc1
v_mfma_f32_16x16x32_bf16 a[204:207],a[140:143],v[252:255],a[204:207]
s_lshl_b32 s95,s95,4
v_mov_b32 v18,s68
v_mov_b32 v19,s69
v_mfma_f32_16x16x32_bf16 a[204:207],a[144:147],v[112:115],a[204:207]
s_lshl_b32 s94,s94,4
ds_read_b64_tr_b16 a[124:125],v45 offset:30752
ds_read_b64_tr_b16 a[126:127],v45 offset:35872
v_mfma_f32_16x16x32_bf16 a[204:207],a[148:151],v[116:119],a[204:207]
s_waitcnt lgkmcnt(5)
ds_read_b64_tr_b16 a[128:129],v1 offset:64
ds_read_b64_tr_b16 a[130:131],v1 offset:5184
v_mfma_f32_16x16x32_bf16 a[204:207],a[152:155],v[120:123],a[204:207]
s_waitcnt vmcnt(1)
s_mov_b64 exec,1
buffer_store_dwordx2 v[18:19],v6,s[64:67],s95 offen offset:2048 sc1
s_mov_b64 exec,-1
v_mfma_f32_16x16x32_bf16 a[204:207],a[156:159],v[124:127],a[204:207]
ds_read_b64_tr_b16 a[132:133],v1 offset:10304
ds_read_b64_tr_b16 a[134:135],v1 offset:15424
v_mfma_f32_16x16x32_bf16 a[204:207],a[160:163],v[152:155],a[204:207]
ds_read_b64_tr_b16 a[136:137],v1 offset:20544
ds_read_b64_tr_b16 a[138:139],v1 offset:25664
v_mfma_f32_16x16x32_bf16 a[204:207],a[164:167],v[184:187],a[204:207]
ds_read_b64_tr_b16 a[140:141],v1 offset:30784
ds_read_b64_tr_b16 a[142:143],v1 offset:35904
v_mfma_f32_16x16x32_bf16 a[204:207],a[168:171],v[216:219],a[204:207]
ds_read_b64_tr_b16 a[144:145],v42 offset:64
ds_read_b64_tr_b16 a[146:147],v42 offset:5184
v_mfma_f32_16x16x32_bf16 a[204:207],a[172:175],v[248:251],a[204:207]
ds_read_b64_tr_b16 a[148:149],v42 offset:10304
ds_read_b64_tr_b16 a[150:151],v42 offset:15424
v_mfma_f32_16x16x32_bf16 a[220:223],a[176:179],v[228:231],a[220:223]
ds_read_b64_tr_b16 a[152:153],v42 offset:20544
ds_read_b64_tr_b16 a[154:155],v42 offset:25664
v_mfma_f32_16x16x32_bf16 a[220:223],a[180:183],v[236:239],a[220:223]
ds_read_b64_tr_b16 a[156:157],v42 offset:30784
ds_read_b64_tr_b16 a[158:159],v42 offset:35904
v_mfma_f32_16x16x32_bf16 a[220:223],a[184:187],v[244:247],a[220:223]
v_accvgpr_read_b32 v166,a84
v_accvgpr_read_b32 v167,a85
v_mfma_f32_16x16x32_bf16 a[220:223],a[188:191],v[252:255],a[220:223]
ds_read_b64_tr_b16 a[160:161],v44 offset:64
ds_read_b64_tr_b16 a[162:163],v44 offset:5184
v_mfma_f32_16x16x32_bf16 a[220:223],a[96:99],v[112:115],a[220:223]
v_accvgpr_read_b32 v14,a86
v_accvgpr_read_b32 v15,a87
v_mfma_f32_16x16x32_bf16 a[220:223],a[100:103],v[116:119],a[220:223]
v_cvt_pk_bf16_f32 v166,v166,v167
v_cvt_pk_bf16_f32 v167,v14,v15
v_mfma_f32_16x16x32_bf16 a[220:223],a[104:107],v[120:123],a[220:223]
buffer_store_dwordx4 v[164:167],v25,s[32:35],s85 offen sc1
ds_read_b64_tr_b16 a[164:165],v44 offset:10304
v_mfma_f32_16x16x32_bf16 a[220:223],a[108:111],v[124:127],a[220:223]
ds_read_b64_tr_b16 a[166:167],v44 offset:15424
ds_read_b64_tr_b16 a[168:169],v44 offset:20544
v_mfma_f32_16x16x32_bf16 a[220:223],a[112:115],v[152:155],a[220:223]
s_waitcnt lgkmcnt(7)
ds_read_b64_tr_b16 a[170:171],v44 offset:25664
ds_read_b64_tr_b16 a[172:173],v44 offset:30784
v_mfma_f32_16x16x32_bf16 a[220:223],a[116:119],v[184:187],a[220:223]
ds_read_b64_tr_b16 a[174:175],v44 offset:35904
ds_read_b64_tr_b16 a[176:177],v2 offset:96
v_mfma_f32_16x16x32_bf16 a[220:223],a[120:123],v[216:219],a[220:223]
ds_read_b64_tr_b16 a[178:179],v2 offset:5216
v_bfe_u32 v24,v6,4,2
v_mfma_f32_16x16x32_bf16 a[220:223],a[124:127],v[248:251],a[220:223]
ds_read_b64_tr_b16 a[180:181],v2 offset:10336
ds_read_b64_tr_b16 a[182:183],v2 offset:15456
v_mfma_f32_16x16x32_bf16 a[236:239],a[128:131],v[228:231],a[236:239]
)A"
R"A(v_min_u32 v24,2,v24
v_add_u32 v24,s70,v24
v_mfma_f32_16x16x32_bf16 a[236:239],a[132:135],v[236:239],a[236:239]
ds_read_b64_tr_b16 a[184:185],v2 offset:20576
ds_read_b64_tr_b16 a[186:187],v2 offset:25696
v_mfma_f32_16x16x32_bf16 a[236:239],a[136:139],v[244:247],a[236:239]
v_add_u32 v24,1,v24
v_accvgpr_read_b32 v198,a88
v_mfma_f32_16x16x32_bf16 a[236:239],a[140:143],v[252:255],a[236:239]
v_accvgpr_read_b32 v199,a89
v_and_b32 v24,3,v24
v_mfma_f32_16x16x32_bf16 a[236:239],a[144:147],v[112:115],a[236:239]
ds_read_b64_tr_b16 a[188:189],v2 offset:30816
ds_read_b64_tr_b16 a[190:191],v2 offset:35936
v_mfma_f32_16x16x32_bf16 a[236:239],a[148:151],v[116:119],a[236:239]
v_accvgpr_read_b32 v16,a90
v_accvgpr_read_b32 v17,a91
v_mfma_f32_16x16x32_bf16 a[236:239],a[152:155],v[120:123],a[236:239]
s_waitcnt lgkmcnt(6)
v_cvt_pk_bf16_f32 v198,v198,v199
v_lshlrev_b32 v24,8,v24
v_mfma_f32_16x16x32_bf16 a[236:239],a[156:159],v[124:127],a[236:239]
ds_read_b64_tr_b16 a[96:97],v43 offset:96
ds_read_b64_tr_b16 a[98:99],v43 offset:5216
v_cvt_pk_bf16_f32 v199,v16,v17
v_mfma_f32_16x16x32_bf16 a[236:239],a[160:163],v[152:155],a[236:239]
buffer_store_dwordx4 v[196:199],v25,s[32:35],s86 offen sc1
ds_read_b64_tr_b16 a[100:101],v43 offset:10336
ds_read_b64_tr_b16 a[102:103],v43 offset:15456
v_mfma_f32_16x16x32_bf16 a[236:239],a[164:167],v[184:187],a[236:239]
ds_read_b64_tr_b16 a[104:105],v43 offset:20576
ds_read_b64_tr_b16 a[106:107],v43 offset:25696
v_mfma_f32_16x16x32_bf16 a[236:239],a[168:171],v[216:219],a[236:239]
ds_read_b64_tr_b16 a[108:109],v43 offset:30816
ds_read_b64_tr_b16 a[110:111],v43 offset:35936
v_mfma_f32_16x16x32_bf16 a[236:239],a[172:175],v[248:251],a[236:239]
ds_read_b64_tr_b16 a[112:113],v45 offset:96
ds_read_b64_tr_b16 a[114:115],v45 offset:5216
v_mfma_f32_16x16x32_bf16 a[252:255],a[176:179],v[228:231],a[252:255]
s_waitcnt lgkmcnt(10)
ds_read_b64_tr_b16 a[116:117],v45 offset:10336
ds_read_b64_tr_b16 a[118:119],v45 offset:15456
v_mfma_f32_16x16x32_bf16 a[252:255],a[180:183],v[236:239],a[252:255]
ds_read_b64_tr_b16 a[120:121],v45 offset:20576
ds_read_b64_tr_b16 a[122:123],v45 offset:25696
v_mfma_f32_16x16x32_bf16 a[252:255],a[184:187],v[244:247],a[252:255]
ds_read_b64_tr_b16 a[124:125],v45 offset:30816
ds_read_b64_tr_b16 a[126:127],v45 offset:35936
v_mfma_f32_16x16x32_bf16 a[252:255],a[188:191],v[252:255],a[252:255]
s_waitcnt lgkmcnt(14)
ds_read_b64_tr_b16 a[128:129],v1 offset:128
ds_read_b64_tr_b16 a[130:131],v1 offset:5248
v_mfma_f32_16x16x32_bf16 a[252:255],a[96:99],v[112:115],a[252:255]
s_waitcnt lgkmcnt(14)
buffer_load_dwordx2 v[26:27],v24,s[64:67],s94 offen offset:2048 sc1
ds_read_b64_tr_b16 a[132:133],v1 offset:10368
ds_read_b64_tr_b16 a[134:135],v1 offset:15488
v_mfma_f32_16x16x32_bf16 a[252:255],a[100:103],v[116:119],a[252:255]
s_waitcnt lgkmcnt(14)
ds_read_b64_tr_b16 a[136:137],v1 offset:20608
ds_read_b64_tr_b16 a[138:139],v1 offset:25728
v_mfma_f32_16x16x32_bf16 a[252:255],a[104:107],v[120:123],a[252:255]
s_waitcnt lgkmcnt(14)
ds_read_b64_tr_b16 a[140:141],v1 offset:30848
ds_read_b64_tr_b16 a[142:143],v1 offset:35968
v_mfma_f32_16x16x32_bf16 a[252:255],a[108:111],v[124:127],a[252:255]
s_waitcnt lgkmcnt(14)
ds_read_b64_tr_b16 a[144:145],v42 offset:128
ds_read_b64_tr_b16 a[146:147],v42 offset:5248
v_mfma_f32_16x16x32_bf16 a[252:255],a[112:115],v[152:155],a[252:255]
s_waitcnt lgkmcnt(14)
ds_read_b64_tr_b16 a[148:149],v42 offset:10368
ds_read_b64_tr_b16 a[150:151],v42 offset:15488
v_mfma_f32_16x16x32_bf16 a[252:255],a[116:119],v[184:187],a[252:255]
s_waitcnt lgkmcnt(14)
ds_read_b64_tr_b16 a[152:153],v42 offset:20608
ds_read_b64_tr_b16 a[154:155],v42 offset:25728
v_mfma_f32_16x16x32_bf16 a[252:255],a[120:123],v[216:219],a[252:255]
s_waitcnt lgkmcnt(14)
ds_read_b64_tr_b16 a[156:157],v42 offset:30848
ds_read_b64_tr_b16 a[158:159],v42 offset:35968
v_mfma_f32_16x16x32_bf16 a[252:255],a[124:127],v[248:251],a[252:255]
s_waitcnt lgkmcnt(14)
ds_read_b64_tr_b16 a[160:161],v44 offset:128
ds_read_b64_tr_b16 a[162:163],v44 offset:5248
v_mfma_f32_16x16x32_bf16 a[12:15],a[128:131],v[228:231],a[12:15]
s_waitcnt lgkmcnt(14)
ds_read_b64_tr_b16 a[164:165],v44 offset:10368
ds_read_b64_tr_b16 a[166:167],v44 offset:15488
v_mfma_f32_16x16x32_bf16 a[12:15],a[132:135],v[236:239],a[12:15]
s_waitcnt lgkmcnt(14)
ds_read_b64_tr_b16 a[168:169],v44 offset:20608
ds_read_b64_tr_b16 a[170:171],v44 offset:25728
v_mfma_f32_16x16x32_bf16 a[12:15],a[136:139],v[244:247],a[12:15]
s_waitcnt lgkmcnt(14)
ds_read_b64_tr_b16 a[172:173],v44 offset:30848
ds_read_b64_tr_b16 a[174:175],v44 offset:35968
v_mfma_f32_16x16x32_bf16 a[12:15],a[140:143],v[252:255],a[12:15]
s_waitcnt lgkmcnt(14)
ds_read_b64_tr_b16 a[176:177],v2 offset:160
ds_read_b64_tr_b16 a[178:179],v2 offset:5280
v_mfma_f32_16x16x32_bf16 a[12:15],a[144:147],v[112:115],a[12:15]
s_waitcnt lgkmcnt(14)
ds_read_b64_tr_b16 a[180:181],v2 offset:10400
ds_read_b64_tr_b16 a[182:183],v2 offset:15520
v_mfma_f32_16x16x32_bf16 a[12:15],a[148:151],v[116:119],a[12:15]
s_waitcnt lgkmcnt(14)
ds_read_b64_tr_b16 a[184:185],v2 offset:20640
ds_read_b64_tr_b16 a[186:187],v2 offset:25760
v_mfma_f32_16x16x32_bf16 a[12:15],a[152:155],v[120:123],a[12:15]
s_waitcnt lgkmcnt(14)
ds_read_b64_tr_b16 a[188:189],v2 offset:30880
ds_read_b64_tr_b16 a[190:191],v2 offset:36000
v_mfma_f32_16x16x32_bf16 a[12:15],a[156:159],v[124:127],a[12:15]
s_waitcnt lgkmcnt(14)
ds_read_b64_tr_b16 a[96:97],v43 offset:160
ds_read_b64_tr_b16 a[98:99],v43 offset:5280
v_mfma_f32_16x16x32_bf16 a[12:15],a[160:163],v[152:155],a[12:15]
s_waitcnt lgkmcnt(14)
ds_read_b64_tr_b16 a[100:101],v43 offset:10400
ds_read_b64_tr_b16 a[102:103],v43 offset:15520
v_mfma_f32_16x16x32_bf16 a[12:15],a[164:167],v[184:187],a[12:15]
s_waitcnt lgkmcnt(14)
ds_read_b64_tr_b16 a[104:105],v43 offset:20640
ds_read_b64_tr_b16 a[106:107],v43 offset:25760
v_mfma_f32_16x16x32_bf16 a[12:15],a[168:171],v[216:219],a[12:15]
s_waitcnt lgkmcnt(14)
ds_read_b64_tr_b16 a[108:109],v43 offset:30880
ds_read_b64_tr_b16 a[110:111],v43 offset:36000
v_mfma_f32_16x16x32_bf16 a[12:15],a[172:175],v[248:251],a[12:15]
s_waitcnt lgkmcnt(14)
ds_read_b64_tr_b16 a[112:113],v45 offset:160
ds_read_b64_tr_b16 a[114:115],v45 offset:5280
v_mfma_f32_16x16x32_bf16 a[28:31],a[176:179],v[228:231],a[28:31]
s_waitcnt lgkmcnt(14)
ds_read_b64_tr_b16 a[116:117],v45 offset:10400
ds_read_b64_tr_b16 a[118:119],v45 offset:15520
v_mfma_f32_16x16x32_bf16 a[28:31],a[180:183],v[236:239],a[28:31]
s_waitcnt lgkmcnt(14)
ds_read_b64_tr_b16 a[120:121],v45 offset:20640
ds_read_b64_tr_b16 a[122:123],v45 offset:25760
v_mfma_f32_16x16x32_bf16 a[28:31],a[184:187],v[244:247],a[28:31]
s_waitcnt lgkmcnt(14)
ds_read_b64_tr_b16 a[124:125],v45 offset:30880
ds_read_b64_tr_b16 a[126:127],v45 offset:36000
v_mfma_f32_16x16x32_bf16 a[28:31],a[188:191],v[252:255],a[28:31]
s_waitcnt lgkmcnt(14)
ds_read_b64_tr_b16 a[128:129],v1 offset:192
ds_read_b64_tr_b16 a[130:131],v1 offset:5312
v_mfma_f32_16x16x32_bf16 a[28:31],a[96:99],v[112:115],a[28:31]
s_waitcnt lgkmcnt(14)
ds_read_b64_tr_b16 a[132:133],v1 offset:10432
ds_read_b64_tr_b16 a[134:135],v1 offset:15552
v_mfma_f32_16x16x32_bf16 a[28:31],a[100:103],v[116:119],a[28:31]
s_waitcnt lgkmcnt(14)
ds_read_b64_tr_b16 a[136:137],v1 offset:20672
ds_read_b64_tr_b16 a[138:139],v1 offset:25792
v_mfma_f32_16x16x32_bf16 a[28:31],a[104:107],v[120:123],a[28:31]
s_waitcnt lgkmcnt(14)
ds_read_b64_tr_b16 a[140:141],v1 offset:30912
ds_read_b64_tr_b16 a[142:143],v1 offset:36032
v_mfma_f32_16x16x32_bf16 a[28:31],a[108:111],v[124:127],a[28:31]
s_waitcnt lgkmcnt(14)
ds_read_b64_tr_b16 a[144:145],v42 offset:192
ds_read_b64_tr_b16 a[146:147],v42 offset:5312
v_mfma_f32_16x16x32_bf16 a[28:31],a[112:115],v[152:155],a[28:31]
s_waitcnt lgkmcnt(14)
ds_read_b64_tr_b16 a[148:149],v42 offset:10432
ds_read_b64_tr_b16 a[150:151],v42 offset:15552
v_mfma_f32_16x16x32_bf16 a[28:31],a[116:119],v[184:187],a[28:31]
s_waitcnt lgkmcnt(14)
ds_read_b64_tr_b16 a[152:153],v42 offset:20672
ds_read_b64_tr_b16 a[154:155],v42 offset:25792
v_mfma_f32_16x16x32_bf16 a[28:31],a[120:123],v[216:219],a[28:31]
s_lshl_b32 s95,s42,2
ds_read_b64_tr_b16 a[156:157],v42 offset:30912
ds_read_b64_tr_b16 a[158:159],v42 offset:36032
s_waitcnt lgkmcnt(15)
v_mfma_f32_16x16x32_bf16 a[28:31],a[124:127],v[248:251],a[28:31]
s_add_u32 s95,s95,s40
s_lshl_b32 s95,s95,4
ds_read_b64_tr_b16 a[160:161],v44 offset:192
ds_read_b64_tr_b16 a[162:163],v44 offset:5312
v_mov_b32 v18,s68
v_mov_b32 v19,s69
s_waitcnt lgkmcnt(15)
v_mfma_f32_16x16x32_bf16 a[44:47],a[128:131],v[228:231],a[44:47]
s_waitcnt vmcnt(1)
s_mov_b64 exec,1
buffer_store_dwordx2 v[18:19],v6,s[64:67],s95 offen offset:3072 sc1
s_mov_b64 exec,-1
ds_read_b64_tr_b16 a[164:165],v44 offset:10432
ds_read_b64_tr_b16 a[166:167],v44 offset:15552
s_waitcnt vmcnt(1)
v_cmp_ne_u32 s[90:91],s68,v26
v_cmp_ne_u32 s[92:93],s69,v27
s_nop 0
s_or_b64 s[90:91],s[90:91],s[92:93]
s_cbranch_scc1 L_fr3_%=
L_fo3_%=:
s_waitcnt lgkmcnt(15)
v_mfma_f32_16x16x32_bf16 a[44:47],a[132:135],v[236:239],a[44:47]
s_lshl_b32 s94,s71,2
ds_read_b64_tr_b16 a[168:169],v44 offset:20672
ds_read_b64_tr_b16 a[170:171],v44 offset:25792
s_waitcnt lgkmcnt(15)
v_mfma_f32_16x16x32_bf16 a[44:47],a[136:139],v[244:247],a[44:47]
s_add_u32 s94,s94,s40
ds_read_b64_tr_b16 a[172:173],v44 offset:30912
ds_read_b64_tr_b16 a[174:175],v44 offset:36032
s_waitcnt lgkmcnt(15)
v_mfma_f32_16x16x32_bf16 a[44:47],a[140:143],v[252:255],a[44:47]
s_lshl_b32 s94,s94,4
ds_read_b64_tr_b16 a[176:177],v2 offset:224
ds_read_b64_tr_b16 a[178:179],v2 offset:5344
s_waitcnt lgkmcnt(15)
v_mfma_f32_16x16x32_bf16 a[44:47],a[144:147],v[112:115],a[44:47]
s_waitcnt lgkmcnt(14)
buffer_load_dwordx4 v[64:67],v6,s[32:35],s76 offen sc1
ds_read_b64_tr_b16 a[180:181],v2 offset:10464
ds_read_b64_tr_b16 a[182:183],v2 offset:15584
v_mfma_f32_16x16x32_bf16 a[44:47],a[148:151],v[116:119],a[44:47]
s_waitcnt lgkmcnt(14)
ds_read_b64_tr_b16 a[184:185],v2 offset:20704
ds_read_b64_tr_b16 a[186:187],v2 offset:25824
v_mfma_f32_16x16x32_bf16 a[44:47],a[152:155],v[120:123],a[44:47]
s_waitcnt lgkmcnt(14)
ds_read_b64_tr_b16 a[188:189],v2 offset:30944
ds_read_b64_tr_b16 a[190:191],v2 offset:36064
v_mfma_f32_16x16x32_bf16 a[44:47],a[156:159],v[124:127],a[44:47]
s_waitcnt lgkmcnt(14)
ds_read_b64_tr_b16 a[96:97],v43 offset:224
ds_read_b64_tr_b16 a[98:99],v43 offset:5344
v_mfma_f32_16x16x32_bf16 a[44:47],a[160:163],v[152:155],a[44:47]
s_waitcnt lgkmcnt(14)
buffer_load_dwordx4 v[80:83],v6,s[32:35],s77 offen sc1
ds_read_b64_tr_b16 a[100:101],v43 offset:10464
ds_read_b64_tr_b16 a[102:103],v43 offset:15584
v_mfma_f32_16x16x32_bf16 a[44:47],a[164:167],v[184:187],a[44:47]
s_waitcnt lgkmcnt(14)
ds_read_b64_tr_b16 a[104:105],v43 offset:20704
ds_read_b64_tr_b16 a[106:107],v43 offset:25824
v_mfma_f32_16x16x32_bf16 a[44:47],a[168:171],v[216:219],a[44:47]
s_waitcnt lgkmcnt(14)
ds_read_b64_tr_b16 a[108:109],v43 offset:30944
ds_read_b64_tr_b16 a[110:111],v43 offset:36064
v_mfma_f32_16x16x32_bf16 a[44:47],a[172:175],v[248:251],a[44:47]
s_waitcnt lgkmcnt(14)
ds_read_b64_tr_b16 a[112:113],v45 offset:224
ds_read_b64_tr_b16 a[114:115],v45 offset:5344
v_mfma_f32_16x16x32_bf16 a[60:63],a[176:179],v[228:231],a[60:63]
s_waitcnt lgkmcnt(14)
buffer_load_dwordx4 v[96:99],v6,s[32:35],s78 offen sc1
ds_read_b64_tr_b16 a[116:117],v45 offset:10464
ds_read_b64_tr_b16 a[118:119],v45 offset:15584
v_mfma_f32_16x16x32_bf16 a[60:63],a[180:183],v[236:239],a[60:63]
s_waitcnt lgkmcnt(14)
ds_read_b64_tr_b16 a[120:121],v45 offset:20704
ds_read_b64_tr_b16 a[122:123],v45 offset:25824
v_mfma_f32_16x16x32_bf16 a[60:63],a[184:187],v[244:247],a[60:63]
s_waitcnt lgkmcnt(14)
ds_read_b64_tr_b16 a[124:125],v45 offset:30944
ds_read_b64_tr_b16 a[126:127],v45 offset:36064
v_mfma_f32_16x16x32_bf16 a[60:63],a[188:191],v[252:255],a[60:63]
s_waitcnt lgkmcnt(14)
ds_read_b64_tr_b16 a[128:129],v1 offset:256
ds_read_b64_tr_b16 a[130:131],v1 offset:5376
v_mfma_f32_16x16x32_bf16 a[60:63],a[96:99],v[112:115],a[60:63]
s_waitcnt lgkmcnt(14)
buffer_load_dwordx4 v[68:71],v6,s[32:35],s76 offen offset:1024 sc1
ds_read_b64_tr_b16 a[132:133],v1 offset:10496
ds_read_b64_tr_b16 a[134:135],v1 offset:15616
)A"
R"A(v_mfma_f32_16x16x32_bf16 a[60:63],a[100:103],v[116:119],a[60:63]
s_waitcnt lgkmcnt(14)
ds_read_b64_tr_b16 a[136:137],v1 offset:20736
ds_read_b64_tr_b16 a[138:139],v1 offset:25856
v_mfma_f32_16x16x32_bf16 a[60:63],a[104:107],v[120:123],a[60:63]
s_waitcnt lgkmcnt(14)
ds_read_b64_tr_b16 a[140:141],v1 offset:30976
ds_read_b64_tr_b16 a[142:143],v1 offset:36096
v_mfma_f32_16x16x32_bf16 a[60:63],a[108:111],v[124:127],a[60:63]
s_waitcnt lgkmcnt(14)
ds_read_b64_tr_b16 a[144:145],v42 offset:256
ds_read_b64_tr_b16 a[146:147],v42 offset:5376
v_mfma_f32_16x16x32_bf16 a[60:63],a[112:115],v[152:155],a[60:63]
s_waitcnt lgkmcnt(14)
buffer_load_dwordx4 v[84:87],v6,s[32:35],s77 offen offset:1024 sc1
ds_read_b64_tr_b16 a[148:149],v42 offset:10496
ds_read_b64_tr_b16 a[150:151],v42 offset:15616
v_mfma_f32_16x16x32_bf16 a[60:63],a[116:119],v[184:187],a[60:63]
s_waitcnt lgkmcnt(14)
ds_read_b64_tr_b16 a[152:153],v42 offset:20736
ds_read_b64_tr_b16 a[154:155],v42 offset:25856
v_mfma_f32_16x16x32_bf16 a[60:63],a[120:123],v[216:219],a[60:63]
s_waitcnt lgkmcnt(14)
ds_read_b64_tr_b16 a[156:157],v42 offset:30976
ds_read_b64_tr_b16 a[158:159],v42 offset:36096
v_mfma_f32_16x16x32_bf16 a[60:63],a[124:127],v[248:251],a[60:63]
s_waitcnt lgkmcnt(14)
ds_read_b64_tr_b16 a[160:161],v44 offset:256
ds_read_b64_tr_b16 a[162:163],v44 offset:5376
v_mfma_f32_16x16x32_bf16 a[76:79],a[128:131],v[228:231],a[76:79]
s_waitcnt lgkmcnt(14)
buffer_load_dwordx4 v[100:103],v6,s[32:35],s78 offen offset:1024 sc1
ds_read_b64_tr_b16 a[164:165],v44 offset:10496
ds_read_b64_tr_b16 a[166:167],v44 offset:15616
v_mfma_f32_16x16x32_bf16 a[76:79],a[132:135],v[236:239],a[76:79]
s_waitcnt lgkmcnt(14)
ds_read_b64_tr_b16 a[168:169],v44 offset:20736
ds_read_b64_tr_b16 a[170:171],v44 offset:25856
v_bfe_u32 v24,v6,4,2
v_min_u32 v24,2,v24
v_mfma_f32_16x16x32_bf16 a[76:79],a[136:139],v[244:247],a[76:79]
s_waitcnt lgkmcnt(14)
ds_read_b64_tr_b16 a[172:173],v44 offset:30976
ds_read_b64_tr_b16 a[174:175],v44 offset:36096
v_add_u32 v24,s70,v24
v_add_u32 v24,1,v24
v_mfma_f32_16x16x32_bf16 a[76:79],a[140:143],v[252:255],a[76:79]
s_waitcnt lgkmcnt(14)
ds_read_b64_tr_b16 a[176:177],v2 offset:288
ds_read_b64_tr_b16 a[178:179],v2 offset:5408
v_and_b32 v24,3,v24
v_lshlrev_b32 v24,8,v24
v_mfma_f32_16x16x32_bf16 a[76:79],a[144:147],v[112:115],a[76:79]
s_waitcnt lgkmcnt(14)
buffer_load_dwordx4 v[72:75],v6,s[32:35],s76 offen offset:2048 sc1
ds_read_b64_tr_b16 a[180:181],v2 offset:10528
ds_read_b64_tr_b16 a[182:183],v2 offset:15648
v_mfma_f32_16x16x32_bf16 a[76:79],a[148:151],v[116:119],a[76:79]
s_waitcnt lgkmcnt(14)
ds_read_b64_tr_b16 a[184:185],v2 offset:20768
ds_read_b64_tr_b16 a[186:187],v2 offset:25888
v_mfma_f32_16x16x32_bf16 a[76:79],a[152:155],v[120:123],a[76:79]
s_waitcnt lgkmcnt(14)
ds_read_b64_tr_b16 a[188:189],v2 offset:31008
ds_read_b64_tr_b16 a[190:191],v2 offset:36128
v_mfma_f32_16x16x32_bf16 a[76:79],a[156:159],v[124:127],a[76:79]
s_waitcnt lgkmcnt(14)
ds_read_b64_tr_b16 a[96:97],v43 offset:288
ds_read_b64_tr_b16 a[98:99],v43 offset:5408
v_mfma_f32_16x16x32_bf16 a[76:79],a[160:163],v[152:155],a[76:79]
s_waitcnt lgkmcnt(14)
buffer_load_dwordx4 v[88:91],v6,s[32:35],s77 offen offset:2048 sc1
ds_read_b64_tr_b16 a[100:101],v43 offset:10528
ds_read_b64_tr_b16 a[102:103],v43 offset:15648
v_mfma_f32_16x16x32_bf16 a[76:79],a[164:167],v[184:187],a[76:79]
s_waitcnt lgkmcnt(14)
ds_read_b64_tr_b16 a[104:105],v43 offset:20768
ds_read_b64_tr_b16 a[106:107],v43 offset:25888
v_mfma_f32_16x16x32_bf16 a[76:79],a[168:171],v[216:219],a[76:79]
s_waitcnt lgkmcnt(14)
ds_read_b64_tr_b16 a[108:109],v43 offset:31008
ds_read_b64_tr_b16 a[110:111],v43 offset:36128
v_mfma_f32_16x16x32_bf16 a[76:79],a[172:175],v[248:251],a[76:79]
s_waitcnt lgkmcnt(14)
ds_read_b64_tr_b16 a[112:113],v45 offset:288
ds_read_b64_tr_b16 a[114:115],v45 offset:5408
v_mfma_f32_16x16x32_bf16 a[92:95],a[176:179],v[228:231],a[92:95]
s_waitcnt lgkmcnt(14)
buffer_load_dwordx4 v[104:107],v6,s[32:35],s78 offen offset:2048 sc1
ds_read_b64_tr_b16 a[116:117],v45 offset:10528
ds_read_b64_tr_b16 a[118:119],v45 offset:15648
v_mfma_f32_16x16x32_bf16 a[92:95],a[180:183],v[236:239],a[92:95]
s_waitcnt lgkmcnt(14)
ds_read_b64_tr_b16 a[120:121],v45 offset:20768
ds_read_b64_tr_b16 a[122:123],v45 offset:25888
v_mfma_f32_16x16x32_bf16 a[92:95],a[184:187],v[244:247],a[92:95]
s_waitcnt lgkmcnt(14)
ds_read_b64_tr_b16 a[124:125],v45 offset:31008
ds_read_b64_tr_b16 a[126:127],v45 offset:36128
v_mfma_f32_16x16x32_bf16 a[92:95],a[188:191],v[252:255],a[92:95]
s_waitcnt lgkmcnt(14)
v_mfma_f32_16x16x32_bf16 a[92:95],a[96:99],v[112:115],a[92:95]
s_waitcnt lgkmcnt(12)
buffer_load_dwordx4 v[76:79],v6,s[32:35],s76 offen offset:3072 sc1
v_mfma_f32_16x16x32_bf16 a[92:95],a[100:103],v[116:119],a[92:95]
s_waitcnt lgkmcnt(10)
v_mfma_f32_16x16x32_bf16 a[92:95],a[104:107],v[120:123],a[92:95]
s_waitcnt lgkmcnt(8)
v_mfma_f32_16x16x32_bf16 a[92:95],a[108:111],v[124:127],a[92:95]
s_waitcnt lgkmcnt(6)
v_mfma_f32_16x16x32_bf16 a[92:95],a[112:115],v[152:155],a[92:95]
s_waitcnt lgkmcnt(4)
buffer_load_dwordx4 v[92:95],v6,s[32:35],s77 offen offset:3072 sc1
v_mfma_f32_16x16x32_bf16 a[92:95],a[116:119],v[184:187],a[92:95]
s_waitcnt lgkmcnt(2)
v_mfma_f32_16x16x32_bf16 a[92:95],a[120:123],v[216:219],a[92:95]
s_waitcnt lgkmcnt(0)
v_mfma_f32_16x16x32_bf16 a[92:95],a[124:127],v[248:251],a[92:95]
buffer_load_dwordx2 v[26:27],v24,s[64:67],s94 offen offset:3072 sc1
s_waitcnt vmcnt(0)
v_cmp_ne_u32 s[90:91],s68,v26
v_cmp_ne_u32 s[92:93],s69,v27
s_nop 0
s_or_b64 s[90:91],s[90:91],s[92:93]
s_cbranch_scc1 L_fr4_%=
L_fo4_%=:
buffer_load_dwordx4 v[128:131],v25,s[32:35],s76 offen sc1
buffer_load_dwordx4 v[136:139],v25,s[32:35],s77 offen sc1
buffer_load_dwordx4 v[144:147],v25,s[32:35],s78 offen sc1
v_mfma_f32_16x16x32_bf16 a[204:207],v[34:37],v[64:67],a[204:207]
v_mfma_f32_16x16x32_bf16 a[204:207],v[34:37],v[80:83],a[204:207]
buffer_load_dwordx4 v[108:111],v6,s[32:35],s78 offen offset:3072 sc1
v_mfma_f32_16x16x32_bf16 a[204:207],v[34:37],v[96:99],a[204:207]
v_mfma_f32_16x16x32_bf16 a[220:223],v[32:35],v[64:67],a[220:223]
v_mfma_f32_16x16x32_bf16 a[220:223],v[32:35],v[80:83],a[220:223]
v_mfma_f32_16x16x32_bf16 a[220:223],v[32:35],v[96:99],a[220:223]
v_mfma_f32_16x16x32_bf16 a[236:239],v[34:37],v[68:71],a[236:239]
v_mfma_f32_16x16x32_bf16 a[236:239],v[34:37],v[84:87],a[236:239]
v_mfma_f32_16x16x32_bf16 a[236:239],v[34:37],v[100:103],a[236:239]
s_nop 1
v_accvgpr_read_b32 v132,a204
v_accvgpr_read_b32 v133,a205
v_mfma_f32_16x16x32_bf16 a[252:255],v[32:35],v[68:71],a[252:255]
v_cvt_pk_bf16_f32 v132,v132,v133
v_accvgpr_read_b32 v134,a206
v_mfma_f32_16x16x32_bf16 a[252:255],v[32:35],v[84:87],a[252:255]
v_accvgpr_read_b32 v135,a207
v_cvt_pk_bf16_f32 v133,v134,v135
v_mfma_f32_16x16x32_bf16 a[252:255],v[32:35],v[100:103],a[252:255]
buffer_store_dwordx2 v[132:133],v40,s[20:23],s43 offen
v_accvgpr_read_b32 v134,a220
v_mfma_f32_16x16x32_bf16 a[12:15],v[34:37],v[72:75],a[12:15]
v_accvgpr_read_b32 v135,a221
v_accvgpr_read_b32 v12,a222
v_mfma_f32_16x16x32_bf16 a[12:15],v[34:37],v[88:91],a[12:15]
v_accvgpr_read_b32 v13,a223
v_cvt_pk_bf16_f32 v134,v134,v135
v_mfma_f32_16x16x32_bf16 a[12:15],v[34:37],v[104:107],a[12:15]
v_cvt_pk_bf16_f32 v135,v12,v13
v_accvgpr_read_b32 v164,a236
v_mfma_f32_16x16x32_bf16 a[28:31],v[32:35],v[72:75],a[28:31]
buffer_store_dwordx2 v[134:135],v40,s[20:23],s43 offen offset:32
v_accvgpr_read_b32 v165,a237
v_mfma_f32_16x16x32_bf16 a[28:31],v[32:35],v[88:91],a[28:31]
v_cvt_pk_bf16_f32 v164,v164,v165
v_accvgpr_read_b32 v166,a238
v_mfma_f32_16x16x32_bf16 a[28:31],v[32:35],v[104:107],a[28:31]
v_accvgpr_read_b32 v167,a239
v_cvt_pk_bf16_f32 v165,v166,v167
v_mfma_f32_16x16x32_bf16 a[44:47],v[34:37],v[76:79],a[44:47]
buffer_store_dwordx2 v[164:165],v40,s[20:23],s43 offen offset:64
v_accvgpr_read_b32 v166,a252
v_mfma_f32_16x16x32_bf16 a[44:47],v[34:37],v[92:95],a[44:47]
s_waitcnt vmcnt(3)
v_accvgpr_read_b32 v167,a253
v_accvgpr_read_b32 v14,a254
v_mfma_f32_16x16x32_bf16 a[44:47],v[34:37],v[108:111],a[44:47]
v_accvgpr_read_b32 v196,a12
v_accvgpr_read_b32 v197,a13
v_cvt_pk_bf16_f32 v196,v196,v197
v_accvgpr_read_b32 v198,a14
v_accvgpr_read_b32 v199,a15
v_mfma_f32_16x16x32_bf16 a[60:63],v[32:35],v[76:79],a[60:63]
v_cvt_pk_bf16_f32 v197,v198,v199
v_accvgpr_read_b32 v15,a255
v_mfma_f32_16x16x32_bf16 a[60:63],v[32:35],v[92:95],a[60:63]
v_cvt_pk_bf16_f32 v166,v166,v167
v_cvt_pk_bf16_f32 v167,v14,v15
v_mfma_f32_16x16x32_bf16 a[60:63],v[32:35],v[108:111],a[60:63]
buffer_store_dwordx2 v[166:167],v40,s[20:23],s43 offen offset:96
buffer_store_dwordx2 v[196:197],v40,s[20:23],s43 offen offset:128
v_accvgpr_read_b32 v198,a28
v_accvgpr_read_b32 v199,a29
v_accvgpr_read_b32 v16,a30
v_accvgpr_read_b32 v17,a31
v_cvt_pk_bf16_f32 v198,v198,v199
v_cvt_pk_bf16_f32 v199,v16,v17
s_nop 3
buffer_store_dwordx2 v[198:199],v40,s[20:23],s43 offen offset:160
v_accvgpr_read_b32 v140,a44
v_accvgpr_read_b32 v141,a45
v_cvt_pk_bf16_f32 v140,v140,v141
v_accvgpr_read_b32 v142,a46
v_accvgpr_read_b32 v143,a47
v_cvt_pk_bf16_f32 v141,v142,v143
buffer_store_dwordx2 v[140:141],v40,s[20:23],s43 offen offset:192
v_accvgpr_read_b32 v142,a60
v_accvgpr_read_b32 v143,a61
v_accvgpr_read_b32 v12,a62
v_accvgpr_read_b32 v13,a63
v_cvt_pk_bf16_f32 v142,v142,v143
v_cvt_pk_bf16_f32 v143,v12,v13
buffer_store_dwordx2 v[142:143],v40,s[20:23],s43 offen offset:224
v_mfma_f32_16x16x32_bf16 a[76:79],v[34:37],v[128:131],a[76:79]
v_mfma_f32_16x16x32_bf16 a[76:79],v[34:37],v[136:139],a[76:79]
v_mfma_f32_16x16x32_bf16 a[76:79],v[34:37],v[144:147],a[76:79]
v_mfma_f32_16x16x32_bf16 a[92:95],v[32:35],v[128:131],a[92:95]
v_mfma_f32_16x16x32_bf16 a[92:95],v[32:35],v[136:139],a[92:95]
v_mfma_f32_16x16x32_bf16 a[92:95],v[32:35],v[144:147],a[92:95]
s_nop 3
s_nop 0
v_accvgpr_read_b32 v172,a76
v_accvgpr_read_b32 v173,a77
v_cvt_pk_bf16_f32 v172,v172,v173
v_accvgpr_read_b32 v174,a78
v_accvgpr_read_b32 v175,a79
v_cvt_pk_bf16_f32 v173,v174,v175
buffer_store_dwordx2 v[172:173],v40,s[20:23],s43 offen offset:256
v_accvgpr_read_b32 v174,a92
v_accvgpr_read_b32 v175,a93
v_accvgpr_read_b32 v14,a94
v_accvgpr_read_b32 v15,a95
v_cvt_pk_bf16_f32 v174,v174,v175
v_cvt_pk_bf16_f32 v175,v14,v15
buffer_store_dwordx2 v[174:175],v40,s[20:23],s43 offen offset:288
s_or_b32 s62,s42,s40
s_cmp_lg_u32 s62,0
s_cbranch_scc1 L_noep_%=
v_mov_b32 v12,s68
s_mov_b64 exec,1
buffer_store_dword v12,v25,s[64:67],0 offen sc1
s_mov_b64 exec,-1
L_noep_%=:
s_waitcnt vmcnt(0) lgkmcnt(0)
s_branch L_end_%=
L_fr1_%=:
s_mov_b32 s88,262144
L_fl1_%=:
buffer_load_dwordx2 v[26:27],v24,s[64:67],s94 offen sc1
s_waitcnt vmcnt(0)
s_sub_u32 s88,s88,1
s_cmp_eq_u32 s88,0
s_cbranch_scc1 L_fo1_%=
v_cmp_ne_u32 s[90:91],s68,v26
v_cmp_ne_u32 s[92:93],s69,v27
s_nop 0
s_or_b64 s[90:91],s[90:91],s[92:93]
s_cbranch_scc1 L_fl1_%=
s_branch L_fo1_%=
L_fr2_%=:
s_mov_b32 s88,262144
L_fl2_%=:
buffer_load_dwordx2 v[26:27],v24,s[64:67],s94 offen offset:1024 sc1
s_waitcnt vmcnt(0)
s_sub_u32 s88,s88,1
s_cmp_eq_u32 s88,0
s_cbranch_scc1 L_fo2_%=
v_cmp_ne_u32 s[90:91],s68,v26
v_cmp_ne_u32 s[92:93],s69,v27
s_nop 0
s_or_b64 s[90:91],s[90:91],s[92:93]
s_cbranch_scc1 L_fl2_%=
s_branch L_fo2_%=
L_fr3_%=:
s_mov_b32 s88,262144
L_fl3_%=:
buffer_load_dwordx2 v[26:27],v24,s[64:67],s94 offen offset:2048 sc1
s_waitcnt vmcnt(0)
s_sub_u32 s88,s88,1
s_cmp_eq_u32 s88,0
s_cbranch_scc1 L_fo3_%=
v_cmp_ne_u32 s[90:91],s68,v26
v_cmp_ne_u32 s[92:93],s69,v27
s_nop 0
s_or_b64 s[90:91],s[90:91],s[92:93]
s_cbranch_scc1 L_fl3_%=
s_branch L_fo3_%=
L_fr4_%=:
s_mov_b32 s88,262144
L_fl4_%=:
buffer_load_dwordx2 v[26:27],v24,s[64:67],s94 offen offset:3072 sc1
s_waitcnt vmcnt(0)
s_sub_u32 s88,s88,1
s_cmp_eq_u32 s88,0
s_cbranch_scc1 L_fo4_%=
v_cmp_ne_u32 s[90:91],s68,v26
v_cmp_ne_u32 s[92:93],s69,v27
s_nop 0
s_or_b64 s[90:91],s[90:91],s[92:93]
s_cbranch_scc1 L_fl4_%=
s_branch L_fo4_%=
L_fast_end_%=:
L_slow0_%=:
s_mov_b32 s88,0
s_branch L_slowc_%=
L_slow1_%=:
s_mov_b32 s88,1
L_slowc_%=:
s_nop 7
s_nop 7
s_waitcnt vmcnt(0) lgkmcnt(0)
s_lshl_b32 s90,%5,2
s_add_u32 s90,s90,s40
s_lshl_b32 s90,s90,15
s_add_u32 s90,s90,167772160
s_mov_b64 s[56:57],%4
s_add_u32 s56,s56,s90
s_addc_u32 s57,s57,0
)A"
R"A(s_and_b32 s57,s57,0xffff
s_mov_b32 s58,32768
s_mov_b32 s59,0x20000
s_mov_b32 s91,0
buffer_store_dwordx4 v[64:67],v6,s[56:59],s91 offen
buffer_store_dwordx4 v[68:71],v6,s[56:59],s91 offen offset:1024
buffer_store_dwordx4 v[72:75],v6,s[56:59],s91 offen offset:2048
buffer_store_dwordx4 v[76:79],v6,s[56:59],s91 offen offset:3072
s_mov_b32 s91,4096
buffer_store_dwordx4 v[80:83],v6,s[56:59],s91 offen
buffer_store_dwordx4 v[84:87],v6,s[56:59],s91 offen offset:1024
buffer_store_dwordx4 v[88:91],v6,s[56:59],s91 offen offset:2048
buffer_store_dwordx4 v[92:95],v6,s[56:59],s91 offen offset:3072
s_mov_b32 s91,8192
buffer_store_dwordx4 v[96:99],v6,s[56:59],s91 offen
buffer_store_dwordx4 v[100:103],v6,s[56:59],s91 offen offset:1024
buffer_store_dwordx4 v[104:107],v6,s[56:59],s91 offen offset:2048
buffer_store_dwordx4 v[108:111],v6,s[56:59],s91 offen offset:3072
s_mov_b32 s91,12288
buffer_store_dwordx4 v[112:115],v6,s[56:59],s91 offen
buffer_store_dwordx4 v[116:119],v6,s[56:59],s91 offen offset:1024
buffer_store_dwordx4 v[120:123],v6,s[56:59],s91 offen offset:2048
buffer_store_dwordx4 v[124:127],v6,s[56:59],s91 offen offset:3072
s_mov_b32 s91,16384
buffer_store_dwordx4 v[128:131],v6,s[56:59],s91 offen
buffer_store_dwordx4 v[132:135],v6,s[56:59],s91 offen offset:1024
buffer_store_dwordx4 v[136:139],v6,s[56:59],s91 offen offset:2048
buffer_store_dwordx4 v[140:143],v6,s[56:59],s91 offen offset:3072
s_mov_b32 s91,20480
buffer_store_dwordx4 v[144:147],v6,s[56:59],s91 offen
buffer_store_dwordx4 v[148:151],v6,s[56:59],s91 offen offset:1024
buffer_store_dwordx4 v[152:155],v6,s[56:59],s91 offen offset:2048
buffer_store_dwordx4 v[156:159],v6,s[56:59],s91 offen offset:3072
s_mov_b32 s91,24576
buffer_store_dwordx4 v[160:163],v6,s[56:59],s91 offen
buffer_store_dwordx4 v[164:167],v6,s[56:59],s91 offen offset:1024
buffer_store_dwordx4 v[168:171],v6,s[56:59],s91 offen offset:2048
buffer_store_dwordx4 v[172:175],v6,s[56:59],s91 offen offset:3072
s_mov_b32 s91,28672
buffer_store_dwordx4 v[176:179],v6,s[56:59],s91 offen
buffer_store_dwordx4 v[180:183],v6,s[56:59],s91 offen offset:1024
buffer_store_dwordx4 v[184:187],v6,s[56:59],s91 offen offset:2048
buffer_store_dwordx4 v[188:191],v6,s[56:59],s91 offen offset:3072
s_waitcnt vmcnt(0)
s_mul_i32 s90,s41,307200
s_mov_b64 s[92:93],%0
s_add_u32 s92,s92,s90
s_addc_u32 s93,s93,0
s_and_b32 s93,s93,0xffff
s_mov_b32 s94,307200
s_mov_b32 s95,0x20000
s_mov_b64 s[96:97],%3
s_and_b32 s97,s97,0xffff
s_mov_b32 s98,5242880
s_mov_b32 s99,0x20000
v_bfe_u32 v171,v6,4,4
v_lshrrev_b32 v172,8,v6
v_lshlrev_b32 v164,8,v171
v_lshl_add_u32 v164,v172,4,v164
v_lshlrev_b32 v165,10,v172
v_lshl_add_u32 v165,v171,1,v165
v_add_u32 v166,4096,v165
v_lshlrev_b32 v168,16,v172
v_lshrrev_b32 v170,2,v171
v_lshl_add_u32 v168,v170,4,v168
v_and_b32 v170,3,v171
v_lshl_add_u32 v168,v170,1,v168
s_lshl_b32 s90,s42,1
s_add_u32 s90,s90,s88
s_lshl_b32 s90,s90,8
v_add_u32 v168,s90,v168
s_mov_b32 s89,0
L_sjb_%=:
s_add_u32 s90,s89,s70
s_add_u32 s90,s90,1
s_and_b32 s90,s90,3
s_lshl_b32 s91,s40,2
s_add_u32 s90,s90,s91
s_lshl_b32 s90,s90,4
v_bfe_u32 v171,v6,4,4
v_lshrrev_b32 v172,8,v6
v_add_u32 v167,s90,v171
v_mul_u32_u24 v167,1280,v167
v_lshl_add_u32 v167,v172,4,v167
s_lshl_b32 s90,s89,10
v_add_u32 v169,s90,v6
v_mov_b32 v64,0
v_mov_b32 v65,0
v_mov_b32 v66,0
v_mov_b32 v67,0
v_mov_b32 v68,0
v_mov_b32 v69,0
v_mov_b32 v70,0
v_mov_b32 v71,0
v_mov_b32 v72,0
v_mov_b32 v73,0
v_mov_b32 v74,0
v_mov_b32 v75,0
v_mov_b32 v76,0
v_mov_b32 v77,0
v_mov_b32 v78,0
v_mov_b32 v79,0
v_mov_b32 v80,0
v_mov_b32 v81,0
v_mov_b32 v82,0
v_mov_b32 v83,0
v_mov_b32 v84,0
v_mov_b32 v85,0
v_mov_b32 v86,0
v_mov_b32 v87,0
v_mov_b32 v88,0
v_mov_b32 v89,0
v_mov_b32 v90,0
v_mov_b32 v91,0
v_mov_b32 v92,0
v_mov_b32 v93,0
v_mov_b32 v94,0
v_mov_b32 v95,0
s_mov_b32 s90,0
L_sq_%=:
s_lshl_b32 s91,s90,6
buffer_load_dwordx4 v[160:163],v167,s[92:95],s91 offen
s_lshl_b32 s91,s90,18
buffer_load_ushort v112,v168,s[96:99],s91 offen
buffer_load_ushort v120,v168,s[96:99],s91 offen offset:8
buffer_load_ushort v128,v168,s[96:99],s91 offen offset:64
buffer_load_ushort v136,v168,s[96:99],s91 offen offset:72
s_add_u32 s91,s91,8192
buffer_load_ushort v113,v168,s[96:99],s91 offen
buffer_load_ushort v121,v168,s[96:99],s91 offen offset:8
buffer_load_ushort v129,v168,s[96:99],s91 offen offset:64
buffer_load_ushort v137,v168,s[96:99],s91 offen offset:72
s_add_u32 s91,s91,8192
buffer_load_ushort v114,v168,s[96:99],s91 offen
buffer_load_ushort v122,v168,s[96:99],s91 offen offset:8
buffer_load_ushort v130,v168,s[96:99],s91 offen offset:64
buffer_load_ushort v138,v168,s[96:99],s91 offen offset:72
s_add_u32 s91,s91,8192
buffer_load_ushort v115,v168,s[96:99],s91 offen
buffer_load_ushort v123,v168,s[96:99],s91 offen offset:8
buffer_load_ushort v131,v168,s[96:99],s91 offen offset:64
buffer_load_ushort v139,v168,s[96:99],s91 offen offset:72
s_add_u32 s91,s91,8192
buffer_load_ushort v116,v168,s[96:99],s91 offen
buffer_load_ushort v124,v168,s[96:99],s91 offen offset:8
buffer_load_ushort v132,v168,s[96:99],s91 offen offset:64
buffer_load_ushort v140,v168,s[96:99],s91 offen offset:72
s_add_u32 s91,s91,8192
buffer_load_ushort v117,v168,s[96:99],s91 offen
buffer_load_ushort v125,v168,s[96:99],s91 offen offset:8
buffer_load_ushort v133,v168,s[96:99],s91 offen offset:64
buffer_load_ushort v141,v168,s[96:99],s91 offen offset:72
s_add_u32 s91,s91,8192
buffer_load_ushort v118,v168,s[96:99],s91 offen
buffer_load_ushort v126,v168,s[96:99],s91 offen offset:8
buffer_load_ushort v134,v168,s[96:99],s91 offen offset:64
buffer_load_ushort v142,v168,s[96:99],s91 offen offset:72
s_add_u32 s91,s91,8192
buffer_load_ushort v119,v168,s[96:99],s91 offen
buffer_load_ushort v127,v168,s[96:99],s91 offen offset:8
buffer_load_ushort v135,v168,s[96:99],s91 offen offset:64
buffer_load_ushort v143,v168,s[96:99],s91 offen offset:72
s_add_u32 s91,s91,8192
s_waitcnt vmcnt(0)
v_lshl_or_b32 v112,v113,16,v112
v_lshl_or_b32 v113,v115,16,v114
v_lshl_or_b32 v114,v117,16,v116
v_lshl_or_b32 v115,v119,16,v118
v_lshl_or_b32 v120,v121,16,v120
v_lshl_or_b32 v121,v123,16,v122
v_lshl_or_b32 v122,v125,16,v124
v_lshl_or_b32 v123,v127,16,v126
v_lshl_or_b32 v128,v129,16,v128
v_lshl_or_b32 v129,v131,16,v130
v_lshl_or_b32 v130,v133,16,v132
v_lshl_or_b32 v131,v135,16,v134
v_lshl_or_b32 v136,v137,16,v136
v_lshl_or_b32 v137,v139,16,v138
v_lshl_or_b32 v138,v141,16,v140
v_lshl_or_b32 v139,v143,16,v142
s_nop 1
v_mfma_f32_16x16x32_bf16 v[64:67],v[112:115],v[160:163],v[64:67]
v_mfma_f32_16x16x32_bf16 v[68:71],v[120:123],v[160:163],v[68:71]
v_mfma_f32_16x16x32_bf16 v[72:75],v[128:131],v[160:163],v[72:75]
v_mfma_f32_16x16x32_bf16 v[76:79],v[136:139],v[160:163],v[76:79]
s_nop 7
s_lshl_b32 s91,s90,18
buffer_load_ushort v112,v168,s[96:99],s91 offen offset:128
buffer_load_ushort v120,v168,s[96:99],s91 offen offset:136
buffer_load_ushort v128,v168,s[96:99],s91 offen offset:192
buffer_load_ushort v136,v168,s[96:99],s91 offen offset:200
s_add_u32 s91,s91,8192
buffer_load_ushort v113,v168,s[96:99],s91 offen offset:128
buffer_load_ushort v121,v168,s[96:99],s91 offen offset:136
buffer_load_ushort v129,v168,s[96:99],s91 offen offset:192
buffer_load_ushort v137,v168,s[96:99],s91 offen offset:200
s_add_u32 s91,s91,8192
buffer_load_ushort v114,v168,s[96:99],s91 offen offset:128
buffer_load_ushort v122,v168,s[96:99],s91 offen offset:136
buffer_load_ushort v130,v168,s[96:99],s91 offen offset:192
buffer_load_ushort v138,v168,s[96:99],s91 offen offset:200
s_add_u32 s91,s91,8192
buffer_load_ushort v115,v168,s[96:99],s91 offen offset:128
buffer_load_ushort v123,v168,s[96:99],s91 offen offset:136
buffer_load_ushort v131,v168,s[96:99],s91 offen offset:192
buffer_load_ushort v139,v168,s[96:99],s91 offen offset:200
s_add_u32 s91,s91,8192
buffer_load_ushort v116,v168,s[96:99],s91 offen offset:128
buffer_load_ushort v124,v168,s[96:99],s91 offen offset:136
buffer_load_ushort v132,v168,s[96:99],s91 offen offset:192
buffer_load_ushort v140,v168,s[96:99],s91 offen offset:200
s_add_u32 s91,s91,8192
buffer_load_ushort v117,v168,s[96:99],s91 offen offset:128
buffer_load_ushort v125,v168,s[96:99],s91 offen offset:136
buffer_load_ushort v133,v168,s[96:99],s91 offen offset:192
buffer_load_ushort v141,v168,s[96:99],s91 offen offset:200
s_add_u32 s91,s91,8192
buffer_load_ushort v118,v168,s[96:99],s91 offen offset:128
buffer_load_ushort v126,v168,s[96:99],s91 offen offset:136
buffer_load_ushort v134,v168,s[96:99],s91 offen offset:192
buffer_load_ushort v142,v168,s[96:99],s91 offen offset:200
s_add_u32 s91,s91,8192
buffer_load_ushort v119,v168,s[96:99],s91 offen offset:128
buffer_load_ushort v127,v168,s[96:99],s91 offen offset:136
buffer_load_ushort v135,v168,s[96:99],s91 offen offset:192
buffer_load_ushort v143,v168,s[96:99],s91 offen offset:200
s_add_u32 s91,s91,8192
s_waitcnt vmcnt(0)
v_lshl_or_b32 v112,v113,16,v112
v_lshl_or_b32 v113,v115,16,v114
v_lshl_or_b32 v114,v117,16,v116
v_lshl_or_b32 v115,v119,16,v118
v_lshl_or_b32 v120,v121,16,v120
v_lshl_or_b32 v121,v123,16,v122
v_lshl_or_b32 v122,v125,16,v124
v_lshl_or_b32 v123,v127,16,v126
v_lshl_or_b32 v128,v129,16,v128
v_lshl_or_b32 v129,v131,16,v130
v_lshl_or_b32 v130,v133,16,v132
v_lshl_or_b32 v131,v135,16,v134
v_lshl_or_b32 v136,v137,16,v136
v_lshl_or_b32 v137,v139,16,v138
v_lshl_or_b32 v138,v141,16,v140
v_lshl_or_b32 v139,v143,16,v142
s_nop 1
v_mfma_f32_16x16x32_bf16 v[80:83],v[112:115],v[160:163],v[80:83]
v_mfma_f32_16x16x32_bf16 v[84:87],v[120:123],v[160:163],v[84:87]
v_mfma_f32_16x16x32_bf16 v[88:91],v[128:131],v[160:163],v[88:91]
v_mfma_f32_16x16x32_bf16 v[92:95],v[136:139],v[160:163],v[92:95]
s_nop 7
s_add_u32 s90,s90,1
s_cmp_lg_u32 s90,20
s_cbranch_scc1 L_sq_%=
s_nop 7
v_mul_f32 v64,s61,v64
v_mul_f32 v65,s61,v65
v_mul_f32 v66,s61,v66
v_mul_f32 v67,s61,v67
v_mul_f32 v68,s61,v68
v_mul_f32 v69,s61,v69
v_mul_f32 v70,s61,v70
v_mul_f32 v71,s61,v71
v_mul_f32 v72,s61,v72
v_mul_f32 v73,s61,v73
v_mul_f32 v74,s61,v74
v_mul_f32 v75,s61,v75
v_mul_f32 v76,s61,v76
v_mul_f32 v77,s61,v77
v_mul_f32 v78,s61,v78
v_mul_f32 v79,s61,v79
v_mul_f32 v80,s61,v80
v_mul_f32 v81,s61,v81
v_mul_f32 v82,s61,v82
v_mul_f32 v83,s61,v83
v_mul_f32 v84,s61,v84
v_mul_f32 v85,s61,v85
v_mul_f32 v86,s61,v86
v_mul_f32 v87,s61,v87
v_mul_f32 v88,s61,v88
v_mul_f32 v89,s61,v89
v_mul_f32 v90,s61,v90
v_mul_f32 v91,s61,v91
v_mul_f32 v92,s61,v92
v_mul_f32 v93,s61,v93
v_mul_f32 v94,s61,v94
v_mul_f32 v95,s61,v95
v_cvt_pk_bf16_f32 v96,v64,v65
v_cvt_pk_bf16_f32 v97,v66,v67
v_cvt_pk_bf16_f32 v98,v68,v69
v_cvt_pk_bf16_f32 v99,v70,v71
v_cvt_pk_bf16_f32 v100,v72,v73
v_cvt_pk_bf16_f32 v101,v74,v75
v_cvt_pk_bf16_f32 v102,v76,v77
v_cvt_pk_bf16_f32 v103,v78,v79
v_cvt_pk_bf16_f32 v104,v80,v81
v_cvt_pk_bf16_f32 v105,v82,v83
v_cvt_pk_bf16_f32 v106,v84,v85
v_cvt_pk_bf16_f32 v107,v86,v87
v_cvt_pk_bf16_f32 v108,v88,v89
v_cvt_pk_bf16_f32 v109,v90,v91
v_cvt_pk_bf16_f32 v110,v92,v93
v_cvt_pk_bf16_f32 v111,v94,v95
v_mov_b32 v170,0xff800000
s_lshl_b32 s91,s88,18
s_mov_b32 s90,64
L_s1_%=:
buffer_load_dwordx4 v[112:115],v164,s[20:23],s91 offen
buffer_load_dwordx4 v[116:119],v164,s[20:23],s91 offen offset:64
buffer_load_dwordx4 v[120:123],v164,s[20:23],s91 offen offset:128
buffer_load_dwordx4 v[124:127],v164,s[20:23],s91 offen offset:192
s_waitcnt vmcnt(0)
v_mfma_f32_16x16x32_bf16 v[144:147],v[112:115],v[96:99],0
v_mfma_f32_16x16x32_bf16 v[144:147],v[116:119],v[100:103],v[144:147]
v_mfma_f32_16x16x32_bf16 v[144:147],v[120:123],v[104:107],v[144:147]
v_mfma_f32_16x16x32_bf16 v[144:147],v[124:127],v[108:111],v[144:147]
s_nop 7
v_max3_f32 v170,v170,v144,v145
v_max3_f32 v170,v170,v146,v147
s_add_u32 s91,s91,4096
s_sub_u32 s90,s90,1
s_cmp_lg_u32 s90,0
s_cbranch_scc1 L_s1_%=
v_mov_b32 v171,v170
s_nop 1
v_permlane32_swap_b32 v170,v171
v_max_f32 v170,v170,v171
v_mov_b32 v171,v170
s_nop 1
v_permlane16_swap_b32 v170,v171
v_max_f32 v170,v170,v171
v_sub_f32 v152,0,v170
v_mov_b32 v156,0
v_sub_f32 v153,0,v170
v_mov_b32 v157,0
v_sub_f32 v154,0,v170
v_mov_b32 v158,0
v_sub_f32 v155,0,v170
v_mov_b32 v159,0
v_mov_b32 v64,0
v_mov_b32 v65,0
v_mov_b32 v66,0
v_mov_b32 v67,0
v_mov_b32 v68,0
v_mov_b32 v69,0
v_mov_b32 v70,0
v_mov_b32 v71,0
)A"
R"A(v_mov_b32 v72,0
v_mov_b32 v73,0
v_mov_b32 v74,0
v_mov_b32 v75,0
v_mov_b32 v76,0
v_mov_b32 v77,0
v_mov_b32 v78,0
v_mov_b32 v79,0
v_mov_b32 v80,0
v_mov_b32 v81,0
v_mov_b32 v82,0
v_mov_b32 v83,0
v_mov_b32 v84,0
v_mov_b32 v85,0
v_mov_b32 v86,0
v_mov_b32 v87,0
v_mov_b32 v88,0
v_mov_b32 v89,0
v_mov_b32 v90,0
v_mov_b32 v91,0
v_mov_b32 v92,0
v_mov_b32 v93,0
v_mov_b32 v94,0
v_mov_b32 v95,0
s_lshl_b32 s91,s88,18
s_mov_b32 s90,32
v_add_u32 v172,4096,v164
L_s2_%=:
buffer_load_dwordx4 v[112:115],v164,s[20:23],s91 offen
buffer_load_dwordx4 v[116:119],v164,s[20:23],s91 offen offset:64
buffer_load_dwordx4 v[120:123],v164,s[20:23],s91 offen offset:128
buffer_load_dwordx4 v[124:127],v164,s[20:23],s91 offen offset:192
buffer_load_dwordx4 v[128:131],v172,s[20:23],s91 offen
buffer_load_dwordx4 v[132:135],v172,s[20:23],s91 offen offset:64
buffer_load_dwordx4 v[136:139],v172,s[20:23],s91 offen offset:128
buffer_load_dwordx4 v[140:143],v172,s[20:23],s91 offen offset:192
s_waitcnt vmcnt(0)
v_mfma_f32_16x16x32_bf16 v[144:147],v[112:115],v[96:99],v[152:155]
v_mfma_f32_16x16x32_bf16 v[144:147],v[116:119],v[100:103],v[144:147]
v_mfma_f32_16x16x32_bf16 v[144:147],v[120:123],v[104:107],v[144:147]
v_mfma_f32_16x16x32_bf16 v[144:147],v[124:127],v[108:111],v[144:147]
v_mfma_f32_16x16x32_bf16 v[148:151],v[128:131],v[96:99],v[152:155]
v_mfma_f32_16x16x32_bf16 v[148:151],v[132:135],v[100:103],v[148:151]
v_mfma_f32_16x16x32_bf16 v[148:151],v[136:139],v[104:107],v[148:151]
v_mfma_f32_16x16x32_bf16 v[148:151],v[140:143],v[108:111],v[148:151]
s_nop 7
v_exp_f32 v144,v144
v_exp_f32 v145,v145
v_exp_f32 v146,v146
v_exp_f32 v147,v147
v_exp_f32 v148,v148
v_exp_f32 v149,v149
v_exp_f32 v150,v150
v_exp_f32 v151,v151
s_nop 0
v_cvt_pk_bf16_f32 v144,v144,v145
v_cvt_pk_bf16_f32 v145,v146,v147
v_cvt_pk_bf16_f32 v146,v148,v149
v_cvt_pk_bf16_f32 v147,v150,v151
buffer_load_ushort v112,v165,s[24:27],s91 offen
buffer_load_ushort v113,v165,s[24:27],s91 offen offset:256
buffer_load_ushort v114,v165,s[24:27],s91 offen offset:512
buffer_load_ushort v115,v165,s[24:27],s91 offen offset:768
buffer_load_ushort v116,v166,s[24:27],s91 offen
buffer_load_ushort v117,v166,s[24:27],s91 offen offset:256
buffer_load_ushort v118,v166,s[24:27],s91 offen offset:512
buffer_load_ushort v119,v166,s[24:27],s91 offen offset:768
buffer_load_ushort v120,v165,s[24:27],s91 offen offset:32
buffer_load_ushort v121,v165,s[24:27],s91 offen offset:288
buffer_load_ushort v122,v165,s[24:27],s91 offen offset:544
buffer_load_ushort v123,v165,s[24:27],s91 offen offset:800
buffer_load_ushort v124,v166,s[24:27],s91 offen offset:32
buffer_load_ushort v125,v166,s[24:27],s91 offen offset:288
buffer_load_ushort v126,v166,s[24:27],s91 offen offset:544
buffer_load_ushort v127,v166,s[24:27],s91 offen offset:800
buffer_load_ushort v128,v165,s[24:27],s91 offen offset:64
buffer_load_ushort v129,v165,s[24:27],s91 offen offset:320
buffer_load_ushort v130,v165,s[24:27],s91 offen offset:576
buffer_load_ushort v131,v165,s[24:27],s91 offen offset:832
buffer_load_ushort v132,v166,s[24:27],s91 offen offset:64
buffer_load_ushort v133,v166,s[24:27],s91 offen offset:320
buffer_load_ushort v134,v166,s[24:27],s91 offen offset:576
buffer_load_ushort v135,v166,s[24:27],s91 offen offset:832
buffer_load_ushort v136,v165,s[24:27],s91 offen offset:96
buffer_load_ushort v137,v165,s[24:27],s91 offen offset:352
buffer_load_ushort v138,v165,s[24:27],s91 offen offset:608
buffer_load_ushort v139,v165,s[24:27],s91 offen offset:864
buffer_load_ushort v140,v166,s[24:27],s91 offen offset:96
buffer_load_ushort v141,v166,s[24:27],s91 offen offset:352
buffer_load_ushort v142,v166,s[24:27],s91 offen offset:608
buffer_load_ushort v143,v166,s[24:27],s91 offen offset:864
s_waitcnt vmcnt(0)
v_lshl_or_b32 v112,v113,16,v112
v_lshl_or_b32 v113,v115,16,v114
v_lshl_or_b32 v114,v117,16,v116
v_lshl_or_b32 v115,v119,16,v118
v_lshl_or_b32 v120,v121,16,v120
v_lshl_or_b32 v121,v123,16,v122
v_lshl_or_b32 v122,v125,16,v124
v_lshl_or_b32 v123,v127,16,v126
v_lshl_or_b32 v128,v129,16,v128
v_lshl_or_b32 v129,v131,16,v130
v_lshl_or_b32 v130,v133,16,v132
v_lshl_or_b32 v131,v135,16,v134
v_lshl_or_b32 v136,v137,16,v136
v_lshl_or_b32 v137,v139,16,v138
v_lshl_or_b32 v138,v141,16,v140
v_lshl_or_b32 v139,v143,16,v142
s_nop 1
v_mfma_f32_16x16x32_bf16 v[64:67],v[112:115],v[144:147],v[64:67]
v_mfma_f32_16x16x32_bf16 v[68:71],v[120:123],v[144:147],v[68:71]
v_mfma_f32_16x16x32_bf16 v[72:75],v[128:131],v[144:147],v[72:75]
v_mfma_f32_16x16x32_bf16 v[76:79],v[136:139],v[144:147],v[76:79]
s_nop 7
buffer_load_ushort v112,v165,s[24:27],s91 offen offset:128
buffer_load_ushort v113,v165,s[24:27],s91 offen offset:384
buffer_load_ushort v114,v165,s[24:27],s91 offen offset:640
buffer_load_ushort v115,v165,s[24:27],s91 offen offset:896
buffer_load_ushort v116,v166,s[24:27],s91 offen offset:128
buffer_load_ushort v117,v166,s[24:27],s91 offen offset:384
buffer_load_ushort v118,v166,s[24:27],s91 offen offset:640
buffer_load_ushort v119,v166,s[24:27],s91 offen offset:896
buffer_load_ushort v120,v165,s[24:27],s91 offen offset:160
buffer_load_ushort v121,v165,s[24:27],s91 offen offset:416
buffer_load_ushort v122,v165,s[24:27],s91 offen offset:672
buffer_load_ushort v123,v165,s[24:27],s91 offen offset:928
buffer_load_ushort v124,v166,s[24:27],s91 offen offset:160
buffer_load_ushort v125,v166,s[24:27],s91 offen offset:416
buffer_load_ushort v126,v166,s[24:27],s91 offen offset:672
buffer_load_ushort v127,v166,s[24:27],s91 offen offset:928
buffer_load_ushort v128,v165,s[24:27],s91 offen offset:192
buffer_load_ushort v129,v165,s[24:27],s91 offen offset:448
buffer_load_ushort v130,v165,s[24:27],s91 offen offset:704
buffer_load_ushort v131,v165,s[24:27],s91 offen offset:960
buffer_load_ushort v132,v166,s[24:27],s91 offen offset:192
buffer_load_ushort v133,v166,s[24:27],s91 offen offset:448
buffer_load_ushort v134,v166,s[24:27],s91 offen offset:704
buffer_load_ushort v135,v166,s[24:27],s91 offen offset:960
buffer_load_ushort v136,v165,s[24:27],s91 offen offset:224
buffer_load_ushort v137,v165,s[24:27],s91 offen offset:480
buffer_load_ushort v138,v165,s[24:27],s91 offen offset:736
buffer_load_ushort v139,v165,s[24:27],s91 offen offset:992
buffer_load_ushort v140,v166,s[24:27],s91 offen offset:224
buffer_load_ushort v141,v166,s[24:27],s91 offen offset:480
buffer_load_ushort v142,v166,s[24:27],s91 offen offset:736
buffer_load_ushort v143,v166,s[24:27],s91 offen offset:992
s_waitcnt vmcnt(0)
v_lshl_or_b32 v112,v113,16,v112
v_lshl_or_b32 v113,v115,16,v114
v_lshl_or_b32 v114,v117,16,v116
v_lshl_or_b32 v115,v119,16,v118
v_lshl_or_b32 v120,v121,16,v120
v_lshl_or_b32 v121,v123,16,v122
v_lshl_or_b32 v122,v125,16,v124
v_lshl_or_b32 v123,v127,16,v126
v_lshl_or_b32 v128,v129,16,v128
v_lshl_or_b32 v129,v131,16,v130
v_lshl_or_b32 v130,v133,16,v132
v_lshl_or_b32 v131,v135,16,v134
v_lshl_or_b32 v136,v137,16,v136
v_lshl_or_b32 v137,v139,16,v138
v_lshl_or_b32 v138,v141,16,v140
v_lshl_or_b32 v139,v143,16,v142
s_nop 1
v_mfma_f32_16x16x32_bf16 v[80:83],v[112:115],v[144:147],v[80:83]
v_mfma_f32_16x16x32_bf16 v[84:87],v[120:123],v[144:147],v[84:87]
v_mfma_f32_16x16x32_bf16 v[88:91],v[128:131],v[144:147],v[88:91]
v_mfma_f32_16x16x32_bf16 v[92:95],v[136:139],v[144:147],v[92:95]
s_nop 7
v_mfma_f32_16x16x32_bf16 v[156:159],v[28:31],v[144:147],v[156:159]
s_nop 7
s_add_u32 s91,s91,8192
s_sub_u32 s90,s90,1
s_cmp_lg_u32 s90,0
s_cbranch_scc1 L_s2_%=
v_rcp_f32 v170,v156
s_nop 0
v_mul_f32 v64,v170,v64
v_mul_f32 v65,v170,v65
v_mul_f32 v66,v170,v66
v_mul_f32 v67,v170,v67
v_mul_f32 v68,v170,v68
v_mul_f32 v69,v170,v69
v_mul_f32 v70,v170,v70
v_mul_f32 v71,v170,v71
v_mul_f32 v72,v170,v72
v_mul_f32 v73,v170,v73
v_mul_f32 v74,v170,v74
v_mul_f32 v75,v170,v75
v_mul_f32 v76,v170,v76
v_mul_f32 v77,v170,v77
v_mul_f32 v78,v170,v78
v_mul_f32 v79,v170,v79
v_mul_f32 v80,v170,v80
v_mul_f32 v81,v170,v81
v_mul_f32 v82,v170,v82
v_mul_f32 v83,v170,v83
v_mul_f32 v84,v170,v84
v_mul_f32 v85,v170,v85
v_mul_f32 v86,v170,v86
v_mul_f32 v87,v170,v87
v_mul_f32 v88,v170,v88
v_mul_f32 v89,v170,v89
v_mul_f32 v90,v170,v90
v_mul_f32 v91,v170,v91
v_mul_f32 v92,v170,v92
v_mul_f32 v93,v170,v93
v_mul_f32 v94,v170,v94
v_mul_f32 v95,v170,v95
v_cvt_pk_bf16_f32 v96,v64,v65
v_cvt_pk_bf16_f32 v97,v66,v67
v_cvt_pk_bf16_f32 v98,v68,v69
v_cvt_pk_bf16_f32 v99,v70,v71
v_cvt_pk_bf16_f32 v100,v72,v73
v_cvt_pk_bf16_f32 v101,v74,v75
v_cvt_pk_bf16_f32 v102,v76,v77
v_cvt_pk_bf16_f32 v103,v78,v79
v_cvt_pk_bf16_f32 v104,v80,v81
v_cvt_pk_bf16_f32 v105,v82,v83
v_cvt_pk_bf16_f32 v106,v84,v85
v_cvt_pk_bf16_f32 v107,v86,v87
v_cvt_pk_bf16_f32 v108,v88,v89
v_cvt_pk_bf16_f32 v109,v90,v91
v_cvt_pk_bf16_f32 v110,v92,v93
v_cvt_pk_bf16_f32 v111,v94,v95
buffer_store_dwordx4 v[96:99],v169,s[36:39],s84 offen sc1
buffer_store_dwordx4 v[100:103],v169,s[36:39],s85 offen sc1
buffer_store_dwordx4 v[104:107],v169,s[36:39],s86 offen sc1
buffer_store_dwordx4 v[108:111],v169,s[36:39],s87 offen sc1
s_waitcnt vmcnt(0)
s_add_u32 s89,s89,1
s_cmp_lg_u32 s89,4
s_cbranch_scc1 L_sjb_%=
s_mov_b32 s91,0
buffer_load_dwordx4 v[64:67],v6,s[56:59],s91 offen sc1
buffer_load_dwordx4 v[68:71],v6,s[56:59],s91 offen offset:1024 sc1
buffer_load_dwordx4 v[72:75],v6,s[56:59],s91 offen offset:2048 sc1
buffer_load_dwordx4 v[76:79],v6,s[56:59],s91 offen offset:3072 sc1
s_mov_b32 s91,4096
buffer_load_dwordx4 v[80:83],v6,s[56:59],s91 offen sc1
buffer_load_dwordx4 v[84:87],v6,s[56:59],s91 offen offset:1024 sc1
buffer_load_dwordx4 v[88:91],v6,s[56:59],s91 offen offset:2048 sc1
buffer_load_dwordx4 v[92:95],v6,s[56:59],s91 offen offset:3072 sc1
s_mov_b32 s91,8192
buffer_load_dwordx4 v[96:99],v6,s[56:59],s91 offen sc1
buffer_load_dwordx4 v[100:103],v6,s[56:59],s91 offen offset:1024 sc1
buffer_load_dwordx4 v[104:107],v6,s[56:59],s91 offen offset:2048 sc1
buffer_load_dwordx4 v[108:111],v6,s[56:59],s91 offen offset:3072 sc1
s_mov_b32 s91,12288
buffer_load_dwordx4 v[112:115],v6,s[56:59],s91 offen sc1
buffer_load_dwordx4 v[116:119],v6,s[56:59],s91 offen offset:1024 sc1
buffer_load_dwordx4 v[120:123],v6,s[56:59],s91 offen offset:2048 sc1
buffer_load_dwordx4 v[124:127],v6,s[56:59],s91 offen offset:3072 sc1
s_mov_b32 s91,16384
buffer_load_dwordx4 v[128:131],v6,s[56:59],s91 offen sc1
buffer_load_dwordx4 v[132:135],v6,s[56:59],s91 offen offset:1024 sc1
buffer_load_dwordx4 v[136:139],v6,s[56:59],s91 offen offset:2048 sc1
buffer_load_dwordx4 v[140:143],v6,s[56:59],s91 offen offset:3072 sc1
s_mov_b32 s91,20480
buffer_load_dwordx4 v[144:147],v6,s[56:59],s91 offen sc1
buffer_load_dwordx4 v[148:151],v6,s[56:59],s91 offen offset:1024 sc1
buffer_load_dwordx4 v[152:155],v6,s[56:59],s91 offen offset:2048 sc1
buffer_load_dwordx4 v[156:159],v6,s[56:59],s91 offen offset:3072 sc1
s_mov_b32 s91,24576
buffer_load_dwordx4 v[160:163],v6,s[56:59],s91 offen sc1
buffer_load_dwordx4 v[164:167],v6,s[56:59],s91 offen offset:1024 sc1
buffer_load_dwordx4 v[168:171],v6,s[56:59],s91 offen offset:2048 sc1
buffer_load_dwordx4 v[172:175],v6,s[56:59],s91 offen offset:3072 sc1
s_mov_b32 s91,28672
buffer_load_dwordx4 v[176:179],v6,s[56:59],s91 offen sc1
buffer_load_dwordx4 v[180:183],v6,s[56:59],s91 offen offset:1024 sc1
buffer_load_dwordx4 v[184:187],v6,s[56:59],s91 offen offset:2048 sc1
buffer_load_dwordx4 v[188:191],v6,s[56:59],s91 offen offset:3072 sc1
s_waitcnt vmcnt(0)
s_cmp_lg_u32 s88,0
s_cbranch_scc1 L_sr1_%=
buffer_load_dwordx4 v[64:67],v6,s[36:39],s84 offen sc1
buffer_load_dwordx4 v[68:71],v6,s[36:39],s85 offen sc1
buffer_load_dwordx4 v[72:75],v6,s[36:39],s86 offen sc1
buffer_load_dwordx4 v[76:79],v6,s[36:39],s87 offen sc1
buffer_load_dwordx4 v[80:83],v6,s[36:39],s84 offen offset:1024 sc1
buffer_load_dwordx4 v[84:87],v6,s[36:39],s85 offen offset:1024 sc1
buffer_load_dwordx4 v[88:91],v6,s[36:39],s86 offen offset:1024 sc1
buffer_load_dwordx4 v[92:95],v6,s[36:39],s87 offen offset:1024 sc1
buffer_load_dwordx4 v[96:99],v6,s[36:39],s84 offen offset:2048 sc1
buffer_load_dwordx4 v[100:103],v6,s[36:39],s85 offen offset:2048 sc1
buffer_load_dwordx4 v[104:107],v6,s[36:39],s86 offen offset:2048 sc1
buffer_load_dwordx4 v[108:111],v6,s[36:39],s87 offen offset:2048 sc1
)A"
R"A(buffer_load_dwordx4 v[112:115],v6,s[36:39],s84 offen offset:3072 sc1
buffer_load_dwordx4 v[116:119],v6,s[36:39],s85 offen offset:3072 sc1
buffer_load_dwordx4 v[120:123],v6,s[36:39],s86 offen offset:3072 sc1
buffer_load_dwordx4 v[124:127],v6,s[36:39],s87 offen offset:3072 sc1
s_waitcnt vmcnt(0)
s_nop 7
s_branch L_sret0_%=
L_sr1_%=:
buffer_load_dwordx4 v[132:135],v6,s[36:39],s84 offen sc1
buffer_load_dwordx4 v[140:143],v6,s[36:39],s85 offen sc1
buffer_load_dwordx4 v[148:151],v6,s[36:39],s86 offen sc1
buffer_load_dwordx4 v[156:159],v6,s[36:39],s87 offen sc1
buffer_load_dwordx4 v[164:167],v6,s[36:39],s84 offen offset:1024 sc1
buffer_load_dwordx4 v[172:175],v6,s[36:39],s85 offen offset:1024 sc1
buffer_load_dwordx4 v[180:183],v6,s[36:39],s86 offen offset:1024 sc1
buffer_load_dwordx4 v[188:191],v6,s[36:39],s87 offen offset:1024 sc1
buffer_load_dwordx4 v[196:199],v6,s[36:39],s84 offen offset:2048 sc1
buffer_load_dwordx4 v[204:207],v6,s[36:39],s85 offen offset:2048 sc1
buffer_load_dwordx4 v[212:215],v6,s[36:39],s86 offen offset:2048 sc1
buffer_load_dwordx4 v[220:223],v6,s[36:39],s87 offen offset:2048 sc1
buffer_load_dwordx4 v[228:231],v6,s[36:39],s84 offen offset:3072 sc1
buffer_load_dwordx4 v[236:239],v6,s[36:39],s85 offen offset:3072 sc1
buffer_load_dwordx4 v[244:247],v6,s[36:39],s86 offen offset:3072 sc1
buffer_load_dwordx4 v[252:255],v6,s[36:39],s87 offen offset:3072 sc1
s_waitcnt vmcnt(0)
s_nop 7
s_branch L_sret1_%=
L_end_%=:
)A"
    :: "s"(x), "s"(kc), "s"(vc), "s"(wq), "s"(ws), "s"(blk), "s"(wid), "s"(wo), "s"(out)
    : "v0", "v1", "v2", "v3", "v4", "v5", "v6", "v7", "v8", "v9", "v10", "v11", "v12", "v13", "v14", "v15", "v16", "v17", "v18", "v19", "v20", "v21", "v22", "v23", "v24", "v25", "v26", "v27", "v28", "v29", "v30", "v31", "v32", "v33", "v34", "v35", "v36", "v37", "v38", "v39", "v40", "v41", "v42", "v43", "v44", "v45", "v46", "v47", "v48", "v49", "v50", "v51", "v52", "v53", "v54", "v55", "v56", "v57", "v58", "v59", "v60", "v61", "v62", "v63", "v64", "v65", "v66", "v67", "v68", "v69", "v70", "v71", "v72", "v73", "v74", "v75", "v76", "v77", "v78", "v79", "v80", "v81", "v82", "v83", "v84", "v85", "v86", "v87", "v88", "v89", "v90", "v91", "v92", "v93", "v94", "v95", "v96", "v97", "v98", "v99", "v100", "v101", "v102", "v103", "v104", "v105", "v106", "v107", "v108", "v109", "v110", "v111", "v112", "v113", "v114", "v115", "v116", "v117", "v118", "v119", "v120", "v121", "v122", "v123", "v124", "v125", "v126", "v127", "v128", "v129", "v130", "v131", "v132", "v133", "v134", "v135", "v136", "v137", "v138", "v139", "v140", "v141", "v142", "v143", "v144", "v145", "v146", "v147", "v148", "v149", "v150", "v151", "v152", "v153", "v154", "v155", "v156", "v157", "v158", "v159", "v160", "v161", "v162", "v163", "v164", "v165", "v166", "v167", "v168", "v169", "v170", "v171", "v172", "v173", "v174", "v175", "v176", "v177", "v178", "v179", "v180", "v181", "v182", "v183", "v184", "v185", "v186", "v187", "v188", "v189", "v190", "v191", "v192", "v193", "v194", "v195", "v196", "v197", "v198", "v199", "v200", "v201", "v202", "v203", "v204", "v205", "v206", "v207", "v208", "v209", "v210", "v211", "v212", "v213", "v214", "v215", "v216", "v217", "v218", "v219", "v220", "v221", "v222", "v223", "v224", "v225", "v226", "v227", "v228", "v229", "v230", "v231", "v232", "v233", "v234", "v235", "v236", "v237", "v238", "v239", "v240", "v241", "v242", "v243", "v244", "v245", "v246", "v247", "v248", "v249", "v250", "v251", "v252", "v253", "v254", "v255", "a0", "a1", "a2", "a3", "a4", "a5", "a6", "a7", "a8", "a9", "a10", "a11", "a12", "a13", "a14", "a15", "a16", "a17", "a18", "a19", "a20", "a21", "a22", "a23", "a24", "a25", "a26", "a27", "a28", "a29", "a30", "a31", "a32", "a33", "a34", "a35", "a36", "a37", "a38", "a39", "a40", "a41", "a42", "a43", "a44", "a45", "a46", "a47", "a48", "a49", "a50", "a51", "a52", "a53", "a54", "a55", "a56", "a57", "a58", "a59", "a60", "a61", "a62", "a63", "a64", "a65", "a66", "a67", "a68", "a69", "a70", "a71", "a72", "a73", "a74", "a75", "a76", "a77", "a78", "a79", "a80", "a81", "a82", "a83", "a84", "a85", "a86", "a87", "a88", "a89", "a90", "a91", "a92", "a93", "a94", "a95", "a96", "a97", "a98", "a99", "a100", "a101", "a102", "a103", "a104", "a105", "a106", "a107", "a108", "a109", "a110", "a111", "a112", "a113", "a114", "a115", "a116", "a117", "a118", "a119", "a120", "a121", "a122", "a123", "a124", "a125", "a126", "a127", "a128", "a129", "a130", "a131", "a132", "a133", "a134", "a135", "a136", "a137", "a138", "a139", "a140", "a141", "a142", "a143", "a144", "a145", "a146", "a147", "a148", "a149", "a150", "a151", "a152", "a153", "a154", "a155", "a156", "a157", "a158", "a159", "a160", "a161", "a162", "a163", "a164", "a165", "a166", "a167", "a168", "a169", "a170", "a171", "a172", "a173", "a174", "a175", "a176", "a177", "a178", "a179", "a180", "a181", "a182", "a183", "a184", "a185", "a186", "a187", "a188", "a189", "a190", "a191", "a192", "a193", "a194", "a195", "a196", "a197", "a198", "a199", "a200", "a201", "a202", "a203", "a204", "a205", "a206", "a207", "a208", "a209", "a210", "a211", "a212", "a213", "a214", "a215", "a216", "a217", "a218", "a219", "a220", "a221", "a222", "a223", "a224", "a225", "a226", "a227", "a228", "a229", "a230", "a231", "a232", "a233", "a234", "a235", "a236", "a237", "a238", "a239", "a240", "a241", "a242", "a243", "a244", "a245", "a246", "a247", "a248", "a249", "a250", "a251", "a252", "a253", "a254", "a255", "s18", "s19", "s20", "s21", "s22", "s23", "s24", "s25", "s26", "s27", "s28", "s29", "s30", "s31", "s32", "s33", "s34", "s35", "s36", "s37", "s38", "s39", "s40", "s41", "s42", "s43", "s44", "s45", "s46", "s47", "s48", "s49", "s50", "s51", "s52", "s53", "s54", "s55", "s56", "s57", "s58", "s59", "s60", "s61", "s62", "s63", "s64", "s65", "s66", "s67", "s68", "s69", "s70", "s71", "s72", "s73", "s74", "s75", "s76", "s77", "s78", "s79", "s80", "s81", "s82", "s83", "s84", "s85", "s86", "s87", "s88", "s89", "s90", "s91", "s92", "s93", "s94", "s95", "s96", "s97", "s98", "s99", "s100", "s101", "vcc", "scc", "memory");
}

extern "C" void kernel_launch(void* const* d_in, const int* in_sizes, int n_in,
                              void* d_out, int out_size, void* d_ws, size_t ws_size,
                              hipStream_t stream) {
  (void)hipFuncSetAttribute((const void*)qattn_kernel, hipFuncAttributeMaxDynamicSharedMemorySize, LDS_ATT);
  qattn_kernel<<<256, 256, LDS_ATT, stream>>>(d_in[0], d_in[1], d_in[2], d_in[3], d_ws, d_in[4], d_out);
}
```

```cpp
#include <hip/hip_runtime.h>
#include <hip/hip_bf16.h>

constexpr int LDS_ATT = 163840;

__global__ __launch_bounds__(256, 1) void qattn_kernel(const void* __restrict__ x, const void* __restrict__ kc, const void* __restrict__ vc,
                                                       const void* __restrict__ wq, void* __restrict__ ws, const void* __restrict__ wo,
                                                       void* __restrict__ out) {
#if defined(__HIP_DEVICE_COMPILE__)
  const auto karg = __builtin_amdgcn_kernarg_segment_ptr();
  const unsigned blk = blockIdx.x;
  const unsigned wid = __builtin_amdgcn_readfirstlane(threadIdx.x >> 6);
  asm volatile(
R"A(s_load_dwordx8 s[4:11],%0,0x0
s_load_dwordx4 s[12:15],%0,0x20
s_load_dwordx2 s[16:17],%0,0x30
s_mov_b32 s40,%2
s_mov_b32 s43,%1
s_lshr_b32 s45,s43,3
v_mbcnt_lo_u32_b32 v12,-1,0
v_mbcnt_hi_u32_b32 v12,-1,v12
s_getpc_b64 s[88:89]
L_pc_%=:
s_and_b32 s88,s88,0xffffff80
s_and_b32 s89,s89,0xffff
s_mov_b32 s90,L_fast_end_%=-L_pc_%=
s_mov_b32 s91,0x20000
s_lshl_b32 s44,s45,2
s_add_u32 s44,s44,s40
s_mul_i32 s44,s44,768
v_lshlrev_b32 v67,7,v12
s_mov_b64 exec,63
buffer_load_dword v19,v67,s[88:91],s44 offen
s_mov_b64 exec,-1
s_and_b32 s70,s43,3
s_and_b32 s71,s45,3
s_lshl_b32 s42,s70,2
s_add_u32 s42,s42,s71
s_and_b32 s41,s43,4
s_lshl_b32 s41,s41,1
s_lshr_b32 s46,s45,2
s_add_u32 s41,s41,s46
s_lshl_b32 s56,s42,9
s_mov_b32 s57,0
s_lshl_b32 s59,s40,13
s_add_u32 s58,s59,65536
v_and_b32 v64,3,v12
v_bfe_u32 v65,v12,2,2
v_lshrrev_b32 v66,4,v12
v_lshrrev_b32 v75,3,v12
s_lshl_b32 s44,s40,6
v_add_u32 v75,s44,v75
v_mul_u32_u24 v75,1280,v75
v_and_b32 v76,7,v12
s_add_u32 s84,s70,1
s_and_b32 s84,s84,3
s_mul_i32 s84,s84,20480
s_add_u32 s84,s84,4096
s_add_u32 s85,s70,2
s_and_b32 s85,s85,3
s_mul_i32 s85,s85,20480
s_add_u32 s85,s85,2048
s_add_u32 s86,s70,3
s_and_b32 s86,s86,3
s_mul_i32 s86,s86,20480
s_add_u32 s86,s86,4096
s_add_u32 s87,s70,4
s_and_b32 s87,s87,3
s_mul_i32 s87,s87,20480
s_add_u32 s87,s87,2048
v_or_b32 v77,0,v66
v_xor_b32 v77,v77,v76
v_lshl_add_u32 v77,v77,4,v75
v_add_u32 v48,s84,v77
v_add_u32 v50,s85,v77
v_add_u32 v52,s86,v77
v_add_u32 v54,s87,v77
v_or_b32 v77,4,v66
v_xor_b32 v77,v77,v76
v_lshl_add_u32 v77,v77,4,v75
v_add_u32 v77,9216,v77
v_add_u32 v49,s84,v77
v_add_u32 v51,s85,v77
v_add_u32 v53,s86,v77
v_add_u32 v55,s87,v77
s_lshl_b32 s44,s40,17
v_lshlrev_b32 v75,13,v65
v_lshl_add_u32 v75,v66,6,v75
v_lshl_add_u32 v75,v64,4,v75
v_add_u32 v75,s44,v75
v_add_u32 v56,0,v75
v_add_u32 v57,256,v75
v_add_u32 v58,32768,v75
v_add_u32 v59,33024,v75
v_add_u32 v60,65536,v75
v_add_u32 v61,65792,v75
v_add_u32 v62,98304,v75
v_add_u32 v63,98560,v75
s_mul_i32 s44,s41,307200
s_waitcnt lgkmcnt(0)
s_mov_b64 s[28:29],s[4:5]
s_add_u32 s28,s28,s44
s_addc_u32 s29,s29,0
s_sub_u32 s28,s28,4096
s_subb_u32 s29,s29,0
s_and_b32 s29,s29,0xffff
s_mov_b32 s30,311296
s_mov_b32 s31,0x20000
s_mov_b64 s[32:33],s[10:11]
s_and_b32 s33,s33,0xffff
s_mov_b32 s34,5242880
s_mov_b32 s35,0x20000
s_add_u32 m0,s58,0
s_nop 0
buffer_load_dwordx4 v56,s[32:35],s56 offen lds
s_add_u32 m0,s58,1024
s_nop 0
buffer_load_dwordx4 v57,s[32:35],s56 offen lds
s_add_u32 m0,s58,2048
s_nop 0
buffer_load_dwordx4 v58,s[32:35],s56 offen lds
s_add_u32 m0,s58,3072
s_nop 0
buffer_load_dwordx4 v59,s[32:35],s56 offen lds
s_add_u32 m0,s58,4096
s_nop 0
buffer_load_dwordx4 v60,s[32:35],s56 offen lds
s_add_u32 m0,s58,5120
s_nop 0
buffer_load_dwordx4 v61,s[32:35],s56 offen lds
s_add_u32 m0,s58,6144
s_nop 0
buffer_load_dwordx4 v62,s[32:35],s56 offen lds
s_add_u32 m0,s58,7168
s_nop 0
buffer_load_dwordx4 v63,s[32:35],s56 offen lds
s_add_u32 s56,s56,524288
s_add_u32 s58,s58,32768
s_cmp_ge_u32 s58,131072
s_cselect_b32 s62,98304,0
s_sub_u32 s58,s58,s62
s_add_u32 m0,s59,0
s_nop 0
buffer_load_dwordx4 v48,s[28:31],s57 offen lds
buffer_load_dwordx4 v49,s[28:31],s57 offen offset:1024 lds
buffer_load_dwordx4 v50,s[28:31],s57 offen offset:2048 lds
buffer_load_dwordx4 v51,s[28:31],s57 offen offset:3072 lds
s_add_u32 m0,s59,4096
s_nop 0
buffer_load_dwordx4 v52,s[28:31],s57 offen lds
buffer_load_dwordx4 v53,s[28:31],s57 offen offset:1024 lds
buffer_load_dwordx4 v54,s[28:31],s57 offen offset:2048 lds
buffer_load_dwordx4 v55,s[28:31],s57 offen offset:3072 lds
s_add_u32 s57,s57,128
s_xor_b32 s59,s59,131072
s_add_u32 m0,s58,0
s_nop 0
buffer_load_dwordx4 v56,s[32:35],s56 offen lds
s_add_u32 m0,s58,1024
s_nop 0
buffer_load_dwordx4 v57,s[32:35],s56 offen lds
s_add_u32 m0,s58,2048
s_nop 0
buffer_load_dwordx4 v58,s[32:35],s56 offen lds
s_add_u32 m0,s58,3072
s_nop 0
buffer_load_dwordx4 v59,s[32:35],s56 offen lds
s_add_u32 m0,s58,4096
s_nop 0
buffer_load_dwordx4 v60,s[32:35],s56 offen lds
s_add_u32 m0,s58,5120
s_nop 0
buffer_load_dwordx4 v61,s[32:35],s56 offen lds
s_add_u32 m0,s58,6144
s_nop 0
buffer_load_dwordx4 v62,s[32:35],s56 offen lds
s_add_u32 m0,s58,7168
s_nop 0
buffer_load_dwordx4 v63,s[32:35],s56 offen lds
s_add_u32 s56,s56,524288
s_add_u32 s58,s58,32768
s_cmp_ge_u32 s58,131072
s_cselect_b32 s62,98304,0
s_sub_u32 s58,s58,s62
s_add_u32 m0,s59,0
s_nop 0
buffer_load_dwordx4 v48,s[28:31],s57 offen lds
buffer_load_dwordx4 v49,s[28:31],s57 offen offset:1024 lds
buffer_load_dwordx4 v50,s[28:31],s57 offen offset:2048 lds
buffer_load_dwordx4 v51,s[28:31],s57 offen offset:3072 lds
s_add_u32 m0,s59,4096
s_nop 0
buffer_load_dwordx4 v52,s[28:31],s57 offen lds
buffer_load_dwordx4 v53,s[28:31],s57 offen offset:1024 lds
buffer_load_dwordx4 v54,s[28:31],s57 offen offset:2048 lds
buffer_load_dwordx4 v55,s[28:31],s57 offen offset:3072 lds
s_add_u32 s57,s57,128
s_xor_b32 s59,s59,131072
s_mov_b32 s60,65536
v_lshlrev_b32 v6,4,v12
v_lshlrev_b32 v72,12,v66
v_lshl_add_u32 v72,v65,6,v72
v_lshl_add_u32 v23,v64,4,v72
v_add_u32 v22,65536,v23
v_and_b32 v74,15,v12
v_lshrrev_b32 v76,1,v74
v_lshlrev_b32 v75,7,v74
s_lshl_b32 s44,s40,13
v_add_u32 v75,s44,v75
v_or_b32 v77,0,v66
v_xor_b32 v77,v77,v76
v_lshl_add_u32 v20,v77,4,v75
v_or_b32 v77,4,v66
v_xor_b32 v77,v77,v76
v_lshl_add_u32 v21,v77,4,v75
s_waitcnt vmcnt(16)
s_barrier
ds_read_b128 a[192:195],v20
ds_read_b64_tr_b16 a[128:129],v22 offset:1024
ds_read_b64_tr_b16 a[130:131],v22 offset:3072
ds_read_b128 a[196:199],v21
ds_read_b64_tr_b16 a[132:133],v22 offset:17408
ds_read_b64_tr_b16 a[134:135],v22 offset:19456
ds_read_b128 a[204:207],v21 offset:2048
ds_read_b128 a[200:203],v20 offset:2048
ds_read_b128 a[208:211],v20 offset:4096
ds_read_b128 a[212:215],v21 offset:4096
ds_read_b128 a[220:223],v21 offset:6144
ds_read_b128 a[216:219],v20 offset:6144
ds_read_b64_tr_b16 a[136:137],v22 offset:1032
ds_read_b64_tr_b16 a[138:139],v22 offset:3080
s_waitcnt lgkmcnt(11)
v_mfma_f32_16x16x32_bf16 v[128:131],a[128:131],a[192:195],0
s_add_u32 m0,s58,0
ds_read_b64_tr_b16 a[140:141],v22 offset:17416
ds_read_b64_tr_b16 a[142:143],v22 offset:19464
s_waitcnt lgkmcnt(10)
v_mfma_f32_16x16x32_bf16 v[128:131],a[132:135],a[196:199],v[128:131]
s_waitcnt lgkmcnt(9)
v_mfma_f32_16x16x32_bf16 v[136:139],a[132:135],a[204:207],0
s_waitcnt lgkmcnt(8)
v_mfma_f32_16x16x32_bf16 v[136:139],a[128:131],a[200:203],v[136:139]
s_waitcnt lgkmcnt(7)
v_mfma_f32_16x16x32_bf16 v[144:147],a[128:131],a[208:211],0
s_waitcnt lgkmcnt(6)
buffer_load_dwordx4 v56,s[32:35],s56 offen lds
v_mfma_f32_16x16x32_bf16 v[144:147],a[132:135],a[212:215],v[144:147]
s_add_u32 m0,s58,1024
s_waitcnt lgkmcnt(5)
v_mfma_f32_16x16x32_bf16 v[152:155],a[132:135],a[220:223],0
s_waitcnt lgkmcnt(4)
v_mfma_f32_16x16x32_bf16 v[152:155],a[128:131],a[216:219],v[152:155]
s_waitcnt lgkmcnt(2)
ds_read_b64_tr_b16 a[144:145],v22 offset:1280
ds_read_b64_tr_b16 a[146:147],v22 offset:3328
v_mfma_f32_16x16x32_bf16 v[132:135],a[136:139],a[192:195],0
s_waitcnt lgkmcnt(2)
ds_read_b64_tr_b16 a[148:149],v22 offset:17664
ds_read_b64_tr_b16 a[150:151],v22 offset:19712
v_mfma_f32_16x16x32_bf16 v[132:135],a[140:143],a[196:199],v[132:135]
buffer_load_dwordx4 v57,s[32:35],s56 offen lds
v_mfma_f32_16x16x32_bf16 v[140:143],a[140:143],a[204:207],0
v_mfma_f32_16x16x32_bf16 v[140:143],a[136:139],a[200:203],v[140:143]
s_add_u32 m0,s58,2048
v_mfma_f32_16x16x32_bf16 v[148:151],a[136:139],a[208:211],0
v_mfma_f32_16x16x32_bf16 v[148:151],a[140:143],a[212:215],v[148:151]
v_mfma_f32_16x16x32_bf16 v[156:159],a[140:143],a[220:223],0
v_mfma_f32_16x16x32_bf16 v[156:159],a[136:139],a[216:219],v[156:159]
s_waitcnt lgkmcnt(2)
ds_read_b64_tr_b16 a[152:153],v22 offset:1288
ds_read_b64_tr_b16 a[154:155],v22 offset:3336
buffer_load_dwordx4 v58,s[32:35],s56 offen lds
v_mfma_f32_16x16x32_bf16 v[160:163],a[144:147],a[192:195],0
s_add_u32 m0,s58,3072
ds_read_b64_tr_b16 a[156:157],v22 offset:17672
ds_read_b64_tr_b16 a[158:159],v22 offset:19720
s_waitcnt lgkmcnt(4)
v_mfma_f32_16x16x32_bf16 v[160:163],a[148:151],a[196:199],v[160:163]
v_mfma_f32_16x16x32_bf16 v[168:171],a[148:151],a[204:207],0
v_mfma_f32_16x16x32_bf16 v[168:171],a[144:147],a[200:203],v[168:171]
v_mfma_f32_16x16x32_bf16 v[176:179],a[144:147],a[208:211],0
buffer_load_dwordx4 v59,s[32:35],s56 offen lds
v_mfma_f32_16x16x32_bf16 v[176:179],a[148:151],a[212:215],v[176:179]
v_mfma_f32_16x16x32_bf16 v[184:187],a[148:151],a[220:223],0
s_add_u32 m0,s58,4096
v_mfma_f32_16x16x32_bf16 v[184:187],a[144:147],a[216:219],v[184:187]
s_waitcnt lgkmcnt(2)
ds_read_b64_tr_b16 a[160:161],v22 offset:1536
ds_read_b64_tr_b16 a[162:163],v22 offset:3584
v_mfma_f32_16x16x32_bf16 v[164:167],a[152:155],a[192:195],0
s_waitcnt lgkmcnt(2)
ds_read_b64_tr_b16 a[164:165],v22 offset:17920
ds_read_b64_tr_b16 a[166:167],v22 offset:19968
v_mfma_f32_16x16x32_bf16 v[164:167],a[156:159],a[196:199],v[164:167]
v_mfma_f32_16x16x32_bf16 v[172:175],a[156:159],a[204:207],0
buffer_load_dwordx4 v60,s[32:35],s56 offen lds
v_mfma_f32_16x16x32_bf16 v[172:175],a[152:155],a[200:203],v[172:175]
s_add_u32 m0,s58,5120
v_mfma_f32_16x16x32_bf16 v[180:183],a[152:155],a[208:211],0
v_mfma_f32_16x16x32_bf16 v[180:183],a[156:159],a[212:215],v[180:183]
v_mfma_f32_16x16x32_bf16 v[188:191],a[156:159],a[220:223],0
v_mfma_f32_16x16x32_bf16 v[188:191],a[152:155],a[216:219],v[188:191]
s_waitcnt lgkmcnt(2)
buffer_load_dwordx4 v61,s[32:35],s56 offen lds
ds_read_b64_tr_b16 a[168:169],v22 offset:1544
ds_read_b64_tr_b16 a[170:171],v22 offset:3592
v_mfma_f32_16x16x32_bf16 v[192:195],a[160:163],a[192:195],0
s_waitcnt lgkmcnt(2)
ds_read_b64_tr_b16 a[172:173],v22 offset:17928
ds_read_b64_tr_b16 a[174:175],v22 offset:19976
v_mfma_f32_16x16x32_bf16 v[192:195],a[164:167],a[196:199],v[192:195]
s_add_u32 m0,s58,6144
v_mfma_f32_16x16x32_bf16 v[200:203],a[164:167],a[204:207],0
v_mfma_f32_16x16x32_bf16 v[200:203],a[160:163],a[200:203],v[200:203]
s_lshl_b32 s44,s41,13
s_add_u32 s44,s44,104857600
s_mov_b64 s[64:65],s[12:13]
v_mfma_f32_16x16x32_bf16 v[208:211],a[160:163],a[208:211],0
s_add_u32 s64,s64,s44
s_addc_u32 s65,s65,0
s_and_b32 s65,s65,0xffff
v_mfma_f32_16x16x32_bf16 v[208:211],a[164:167],a[212:215],v[208:211]
s_mov_b32 s66,8192
buffer_load_dwordx4 v62,s[32:35],s56 offen lds
v_mfma_f32_16x16x32_bf16 v[216:219],a[164:167],a[220:223],0
s_add_u32 m0,s58,7168
s_mov_b32 s67,0x20000
v_mov_b32 v14,4096
v_mfma_f32_16x16x32_bf16 v[216:219],a[160:163],a[216:219],v[216:219]
s_waitcnt lgkmcnt(2)
ds_read_b64_tr_b16 a[176:177],v22 offset:1792
ds_read_b64_tr_b16 a[178:179],v22 offset:3840
v_mfma_f32_16x16x32_bf16 v[196:199],a[168:171],a[192:195],0
s_waitcnt lgkmcnt(2)
ds_read_b64_tr_b16 a[180:181],v22 offset:18176
ds_read_b64_tr_b16 a[182:183],v22 offset:20224
v_mfma_f32_16x16x32_bf16 v[196:199],a[172:175],a[196:199],v[196:199]
v_mfma_f32_16x16x32_bf16 v[204:207],a[172:175],a[204:207],0
s_mov_b64 s[72:73],s[14:15]
buffer_load_dwordx4 v63,s[32:35],s56 offen lds
v_mfma_f32_16x16x32_bf16 v[204:207],a[168:171],a[200:203],v[204:207]
s_and_b32 s73,s73,0xffff
s_mov_b32 s74,5242880
s_add_u32 m0,s59,0
v_mfma_f32_16x16x32_bf16 v[212:215],a[168:171],a[208:211],0
s_mov_b32 s75,0x20000
s_lshl_b32 s45,%1,2
v_lshlrev_b32 v15,3,v6
v_mfma_f32_16x16x32_bf16 v[212:215],a[172:175],a[212:215],v[212:215]
s_add_u32 s45,s45,s40
s_mul_i32 s45,s45,5120
ds_read_b64_tr_b16 a[184:185],v22 offset:1800
v_mfma_f32_16x16x32_bf16 v[220:223],a[172:175],a[220:223],0
s_lshl_b32 s43,s41,5
s_lshl_b32 s44,s42,1
ds_read_b64_tr_b16 a[186:187],v22 offset:3848
v_mfma_f32_16x16x32_bf16 v[220:223],a[168:171],a[216:219],v[220:223]
s_add_u32 s43,s43,s44
buffer_load_dwordx4 v48,s[28:31],s57 offen lds
s_waitcnt lgkmcnt(4)
v_mfma_f32_16x16x32_bf16 v[224:227],a[176:179],a[192:195],0
s_waitcnt lgkmcnt(2)
ds_read_b64_tr_b16 a[188:189],v22 offset:18184
ds_read_b64_tr_b16 a[190:191],v22 offset:20232
v_mfma_f32_16x16x32_bf16 v[224:227],a[180:183],a[196:199],v[224:227]
s_lshl_b32 s44,s43,18
s_mov_b64 s[20:21],s[6:7]
s_mov_b32 s22,524288
v_mfma_f32_16x16x32_bf16 v[232:235],a[180:183],a[204:207],0
s_add_u32 s20,s20,s44
s_addc_u32 s21,s21,0
ds_read_b64_tr_b16 a[128:129],v22
v_mfma_f32_16x16x32_bf16 v[232:235],a[176:179],a[200:203],v[232:235]
)A"
R"A(s_and_b32 s21,s21,0xffff
buffer_load_dwordx4 v49,s[28:31],s57 offen offset:1024 lds
v_mfma_f32_16x16x32_bf16 v[240:243],a[176:179],a[208:211],0
s_mov_b32 s23,0x20000
s_mov_b64 s[24:25],s[8:9]
ds_read_b64_tr_b16 a[130:131],v22 offset:2048
v_mfma_f32_16x16x32_bf16 v[240:243],a[180:183],a[212:215],v[240:243]
s_add_u32 s24,s24,s44
s_addc_u32 s25,s25,0
ds_read_b64_tr_b16 a[132:133],v22 offset:16384
v_mfma_f32_16x16x32_bf16 v[248:251],a[180:183],a[220:223],0
s_and_b32 s25,s25,0xffff
s_mov_b32 s26,524288
ds_read_b64_tr_b16 a[134:135],v22 offset:18432
v_mfma_f32_16x16x32_bf16 v[248:251],a[176:179],a[216:219],v[248:251]
s_mov_b32 s27,0x20000
s_lshl_b32 s44,s43,16
ds_read_b64_tr_b16 a[136:137],v22 offset:8
s_waitcnt lgkmcnt(7)
v_mfma_f32_16x16x32_bf16 v[228:231],a[184:187],a[192:195],0
s_mov_b64 s[36:37],s[12:13]
buffer_load_dwordx4 v50,s[28:31],s57 offen offset:2048 lds
s_waitcnt lgkmcnt(5)
v_mfma_f32_16x16x32_bf16 v[228:231],a[188:191],a[196:199],v[228:231]
s_add_u32 s36,s36,s44
s_addc_u32 s37,s37,0
ds_read_b64_tr_b16 a[138:139],v22 offset:2056
v_mfma_f32_16x16x32_bf16 v[236:239],a[188:191],a[204:207],0
s_and_b32 s37,s37,0xffff
s_mov_b32 s38,131072
ds_read_b64_tr_b16 a[140:141],v22 offset:16392
v_mfma_f32_16x16x32_bf16 v[236:239],a[184:187],a[200:203],v[236:239]
s_mov_b32 s39,0x20000
s_lshl_b32 s51,s40,12
ds_read_b64_tr_b16 a[142:143],v22 offset:18440
v_mfma_f32_16x16x32_bf16 v[244:247],a[184:187],a[208:211],0
s_add_u32 s52,s51,16384
s_mul_i32 s47,s41,5
ds_read_b64_tr_b16 a[144:145],v22 offset:256
v_mfma_f32_16x16x32_bf16 v[244:247],a[188:191],a[212:215],v[244:247]
s_add_u32 s47,s47,s42
buffer_load_dwordx4 v51,s[28:31],s57 offen offset:3072 lds
v_mfma_f32_16x16x32_bf16 v[252:255],a[188:191],a[220:223],0
s_and_b32 s47,s47,15
s_lshl_b32 s47,s47,14
ds_read_b64_tr_b16 a[146:147],v22 offset:2304
v_mfma_f32_16x16x32_bf16 v[252:255],a[184:187],a[216:219],v[252:255]
s_mov_b32 s54,0
s_mov_b32 s50,98304
ds_read_b64_tr_b16 a[148:149],v22 offset:16640
s_waitcnt lgkmcnt(6)
v_mfma_f32_16x16x32_bf16 a[0:3],a[128:131],a[192:195],0
s_add_u32 m0,s59,4096
s_mov_b32 s49,32768
ds_read_b64_tr_b16 a[150:151],v22 offset:18688
v_mfma_f32_16x16x32_bf16 a[0:3],a[132:135],a[196:199],a[0:3]
s_lshl_b32 s55,s40,14
s_mov_b32 s61,0x3e0293ee
ds_read_b64_tr_b16 a[152:153],v22 offset:264
v_mfma_f32_16x16x32_bf16 a[32:35],a[132:135],a[204:207],0
s_mov_b32 s63,0x5d800000
s_lshl_b32 s44,s40,2
ds_read_b64_tr_b16 a[154:155],v22 offset:2312
v_mfma_f32_16x16x32_bf16 a[32:35],a[128:131],a[200:203],a[32:35]
s_add_u32 s44,s44,s70
buffer_load_dwordx4 v52,s[28:31],s57 offen lds
v_mfma_f32_16x16x32_bf16 a[64:67],a[128:131],a[208:211],0
s_mov_b32 s46,0xff800000
ds_read_b64_tr_b16 a[156:157],v22 offset:16648
v_lshlrev_b32 v70,8,v65
v_mfma_f32_16x16x32_bf16 a[64:67],a[132:135],a[212:215],a[64:67]
s_cmp_eq_u32 s44,14
s_cselect_b32 s18,0x7149f2ca,s46
ds_read_b64_tr_b16 a[158:159],v22 offset:18696
v_mfma_f32_16x16x32_bf16 a[96:99],a[132:135],a[220:223],0
s_cmp_eq_u32 s44,13
s_cselect_b32 s19,0x7149f2ca,s46
ds_read_b64_tr_b16 a[160:161],v22 offset:512
v_mfma_f32_16x16x32_bf16 a[96:99],a[128:131],a[216:219],a[96:99]
s_cmp_eq_u32 s44,12
s_cselect_b32 s100,0x7149f2ca,s46
ds_read_b64_tr_b16 a[162:163],v22 offset:2560
s_waitcnt lgkmcnt(7)
v_mfma_f32_16x16x32_bf16 a[4:7],a[136:139],a[192:195],0
s_add_u32 s60,s60,32768
buffer_load_dwordx4 v53,s[28:31],s57 offen offset:1024 lds
v_mfma_f32_16x16x32_bf16 a[4:7],a[140:143],a[196:199],a[4:7]
s_cmp_eq_u32 s44,15
s_cselect_b32 s101,0x7149f2ca,s46
ds_read_b64_tr_b16 a[164:165],v22 offset:16896
v_mfma_f32_16x16x32_bf16 a[36:39],a[140:143],a[204:207],0
s_add_u32 s58,s58,32768
ds_read_b64_tr_b16 a[166:167],v22 offset:18944
v_lshl_add_u32 v70,v66,6,v70
v_mfma_f32_16x16x32_bf16 a[36:39],a[136:139],a[200:203],a[36:39]
s_mul_i32 s81,s70,1310720
ds_read_b64_tr_b16 a[168:169],v22 offset:520
v_add_u32 v70,s51,v70
v_mfma_f32_16x16x32_bf16 a[68:71],a[136:139],a[208:211],0
s_mul_i32 s44,s71,320
ds_read_b64_tr_b16 a[170:171],v22 offset:2568
v_lshl_add_u32 v4,v64,4,v70
v_mfma_f32_16x16x32_bf16 a[68:71],a[140:143],a[212:215],a[68:71]
s_add_u32 s81,s81,s44
ds_read_b64_tr_b16 a[172:173],v22 offset:16904
v_xor_b32 v71,2,v64
v_mfma_f32_16x16x32_bf16 a[100:103],a[140:143],a[220:223],0
s_mul_i32 s82,s40,10240
buffer_load_dwordx4 v54,s[28:31],s57 offen offset:2048 lds
v_mfma_f32_16x16x32_bf16 a[100:103],a[136:139],a[216:219],a[100:103]
s_add_u32 s56,s56,524288
ds_read_b64_tr_b16 a[174:175],v22 offset:18952
v_lshl_add_u32 v5,v71,4,v70
v_mfma_f32_16x16x32_bf16 a[8:11],a[144:147],a[192:195],0
s_mul_i32 s44,s40,40960
ds_read_b64_tr_b16 a[176:177],v22 offset:768
v_lshlrev_b32 v72,10,v65
s_waitcnt lgkmcnt(7)
v_mfma_f32_16x16x32_bf16 a[8:11],a[148:151],a[196:199],a[8:11]
s_xor_b32 s59,s59,131072
ds_read_b64_tr_b16 a[178:179],v22 offset:2816
v_lshl_add_u32 v72,v64,6,v72
v_mfma_f32_16x16x32_bf16 a[40:43],a[148:151],a[204:207],0
s_lshr_b32 s92,%1,3
ds_read_b64_tr_b16 a[180:181],v22 offset:17152
v_and_b32 v73,2,v65
v_mfma_f32_16x16x32_bf16 a[40:43],a[144:147],a[200:203],a[40:43]
s_lshl_b32 s92,s92,2
buffer_load_dwordx4 v55,s[28:31],s57 offen offset:3072 lds
v_mfma_f32_16x16x32_bf16 a[72:75],a[144:147],a[208:211],0
s_add_u32 s57,s57,128
ds_read_b64_tr_b16 a[182:183],v22 offset:19200
v_xor_b32 v73,v73,v66
v_mfma_f32_16x16x32_bf16 a[72:75],a[148:151],a[212:215],a[72:75]
s_add_u32 s92,s92,s40
ds_read_b64_tr_b16 a[184:185],v22 offset:776
v_lshl_add_u32 v72,v73,4,v72
v_mfma_f32_16x16x32_bf16 a[104:107],a[148:151],a[220:223],0
s_lshl_b32 s92,s92,7
ds_read_b64_tr_b16 a[186:187],v22 offset:2824
v_add_u32 v1,98304,v72
v_mfma_f32_16x16x32_bf16 a[104:107],a[144:147],a[216:219],a[104:107]
s_add_u32 s93,s92,L_tailseg_%=-L_pc_%=
ds_read_b64_tr_b16 a[188:189],v22 offset:17160
v_lshlrev_b32 v72,10,v66
v_mfma_f32_16x16x32_bf16 a[12:15],a[152:155],a[192:195],0
ds_read_b64_tr_b16 a[190:191],v22 offset:19208
v_lshl_add_u32 v72,v65,6,v72
v_mfma_f32_16x16x32_bf16 a[12:15],a[156:159],a[196:199],a[12:15]
v_lshl_add_u32 v72,v64,3,v72
v_add_u32 v72,114688,v72
v_mfma_f32_16x16x32_bf16 a[44:47],a[156:159],a[204:207],0
s_cmp_eq_u32 s60,131072
s_cselect_b32 s60,32768,s60
v_and_b32 v73,1,v66
v_mfma_f32_16x16x32_bf16 a[44:47],a[152:155],a[200:203],a[44:47]
v_lshl_add_u32 v2,v73,5,v72
v_add_u32 v22,s60,v23
v_mfma_f32_16x16x32_bf16 a[76:79],a[152:155],a[208:211],0
s_add_u32 s60,s60,32768
buffer_load_dword v13,v14,s[64:67],0 offen sc1
v_mfma_f32_16x16x32_bf16 a[76:79],a[156:159],a[212:215],a[76:79]
v_xor_b32 v20,131072,v20
v_xor_b32 v73,1,v73
v_mfma_f32_16x16x32_bf16 a[108:111],a[156:159],a[220:223],0
v_xor_b32 v21,131072,v21
v_lshl_add_u32 v3,v73,5,v72
v_mfma_f32_16x16x32_bf16 a[108:111],a[152:155],a[216:219],a[108:111]
s_waitcnt vmcnt(17) lgkmcnt(0)
v_mov_b32 v28,0x3f803f80
v_mov_b32 v29,0x3f803f80
v_mfma_f32_16x16x32_bf16 a[16:19],a[160:163],a[192:195],0
s_barrier
ds_read_b128 a[224:227],v20
v_mov_b32 v30,0x3f803f80
v_mfma_f32_16x16x32_bf16 a[16:19],a[164:167],a[196:199],a[16:19]
ds_read_b64_tr_b16 a[128:129],v22 offset:1024
v_mov_b32 v31,0x3f803f80
v_mfma_f32_16x16x32_bf16 a[48:51],a[164:167],a[204:207],0
s_cmp_ge_u32 s58,131072
s_cselect_b32 s62,98304,0
ds_read_b64_tr_b16 a[130:131],v22 offset:3072
v_mfma_f32_16x16x32_bf16 a[48:51],a[160:163],a[200:203],a[48:51]
s_sub_u32 s58,s58,s62
s_mov_b32 exec_hi,0xff
buffer_load_dword v16,v15,s[72:75],s45 offen
s_mov_b32 exec_hi,-1
v_mfma_f32_16x16x32_bf16 a[80:83],a[160:163],a[208:211],0
ds_read_b128 a[228:231],v21
v_add_u32 v70,0,v12
v_mfma_f32_16x16x32_bf16 a[80:83],a[164:167],a[212:215],a[80:83]
ds_read_b64_tr_b16 a[132:133],v22 offset:17408
v_mul_u32_u24 v71,3277,v70
v_mfma_f32_16x16x32_bf16 a[112:115],a[164:167],a[220:223],0
ds_read_b64_tr_b16 a[134:135],v22 offset:19456
v_lshrrev_b32 v71,16,v71
v_mfma_f32_16x16x32_bf16 a[112:115],a[160:163],a[216:219],a[112:115]
ds_read_b128 a[232:235],v20 offset:2048
v_mul_u32_u24 v72,20,v71
v_mfma_f32_16x16x32_bf16 a[20:23],a[168:171],a[192:195],0
ds_read_b128 a[236:239],v21 offset:2048
v_sub_u32 v72,v70,v72
v_mfma_f32_16x16x32_bf16 a[20:23],a[172:175],a[196:199],a[20:23]
ds_read_b128 a[240:243],v20 offset:4096
v_bfe_u32 v73,v71,2,1
v_mfma_f32_16x16x32_bf16 a[52:55],a[172:175],a[204:207],0
ds_read_b128 a[244:247],v21 offset:4096
v_lshlrev_b32 v73,1,v73
v_mfma_f32_16x16x32_bf16 a[52:55],a[168:171],a[200:203],a[52:55]
ds_read_b128 a[248:251],v20 offset:6144
v_xor_b32 v72,v72,v73
v_mfma_f32_16x16x32_bf16 a[84:87],a[168:171],a[208:211],0
ds_read_b128 a[252:255],v21 offset:6144
v_mul_u32_u24 v71,1280,v71
v_mfma_f32_16x16x32_bf16 a[84:87],a[172:175],a[212:215],a[84:87]
ds_read_b64_tr_b16 a[136:137],v22 offset:1032
v_lshl_add_u32 v71,v72,4,v71
v_mfma_f32_16x16x32_bf16 a[116:119],a[172:175],a[220:223],0
ds_read_b64_tr_b16 a[138:139],v22 offset:3080
v_add_u32 v71,s44,v71
v_mfma_f32_16x16x32_bf16 a[116:119],a[168:171],a[216:219],a[116:119]
ds_read_b64_tr_b16 a[140:141],v22 offset:17416
v_subrev_u32 v7,0,v71
v_mfma_f32_16x16x32_bf16 a[24:27],a[176:179],a[192:195],0
ds_read_b64_tr_b16 a[142:143],v22 offset:19464
v_add_u32 v70,64,v12
v_mfma_f32_16x16x32_bf16 a[24:27],a[180:183],a[196:199],a[24:27]
ds_read_b64_tr_b16 a[144:145],v22 offset:1280
v_mul_u32_u24 v71,3277,v70
v_mfma_f32_16x16x32_bf16 a[56:59],a[180:183],a[204:207],0
ds_read_b64_tr_b16 a[146:147],v22 offset:3328
v_lshrrev_b32 v71,16,v71
v_mfma_f32_16x16x32_bf16 a[56:59],a[176:179],a[200:203],a[56:59]
ds_read_b64_tr_b16 a[148:149],v22 offset:17664
v_mul_u32_u24 v72,20,v71
v_mfma_f32_16x16x32_bf16 a[88:91],a[176:179],a[208:211],0
ds_read_b64_tr_b16 a[150:151],v22 offset:19712
v_sub_u32 v72,v70,v72
v_mfma_f32_16x16x32_bf16 a[88:91],a[180:183],a[212:215],a[88:91]
ds_read_b64_tr_b16 a[152:153],v22 offset:1288
v_bfe_u32 v73,v71,2,1
v_mfma_f32_16x16x32_bf16 a[120:123],a[180:183],a[220:223],0
ds_read_b64_tr_b16 a[154:155],v22 offset:3336
v_lshlrev_b32 v73,1,v73
v_mfma_f32_16x16x32_bf16 a[120:123],a[176:179],a[216:219],a[120:123]
ds_read_b64_tr_b16 a[156:157],v22 offset:17672
v_xor_b32 v72,v72,v73
v_mfma_f32_16x16x32_bf16 a[28:31],a[184:187],a[192:195],0
ds_read_b64_tr_b16 a[158:159],v22 offset:19720
v_mul_u32_u24 v71,1280,v71
v_mfma_f32_16x16x32_bf16 a[28:31],a[188:191],a[196:199],a[28:31]
ds_read_b64_tr_b16 a[160:161],v22 offset:1536
v_lshl_add_u32 v71,v72,4,v71
v_mfma_f32_16x16x32_bf16 a[60:63],a[188:191],a[204:207],0
ds_read_b64_tr_b16 a[162:163],v22 offset:3584
v_add_u32 v71,s44,v71
v_mfma_f32_16x16x32_bf16 a[60:63],a[184:187],a[200:203],a[60:63]
ds_read_b64_tr_b16 a[164:165],v22 offset:17920
v_subrev_u32 v8,1024,v71
v_mfma_f32_16x16x32_bf16 a[92:95],a[184:187],a[208:211],0
ds_read_b64_tr_b16 a[166:167],v22 offset:19968
v_add_u32 v70,128,v12
v_mfma_f32_16x16x32_bf16 a[92:95],a[188:191],a[212:215],a[92:95]
ds_read_b64_tr_b16 a[168:169],v22 offset:1544
v_mul_u32_u24 v71,3277,v70
v_mfma_f32_16x16x32_bf16 a[124:127],a[188:191],a[220:223],0
s_add_u32 m0,s58,0
ds_read_b64_tr_b16 a[170:171],v22 offset:3592
v_lshrrev_b32 v71,16,v71
v_mfma_f32_16x16x32_bf16 a[124:127],a[184:187],a[216:219],a[124:127]
s_waitcnt lgkmcnt(7)
ds_read_b64_tr_b16 a[172:173],v22 offset:17928
v_mul_u32_u24 v72,20,v71
v_mfma_f32_16x16x32_bf16 v[128:131],a[128:131],a[224:227],v[128:131]
ds_read_b64_tr_b16 a[174:175],v22 offset:19976
v_sub_u32 v72,v70,v72
v_mfma_f32_16x16x32_bf16 v[128:131],a[132:135],a[228:231],v[128:131]
buffer_load_dwordx4 v56,s[32:35],s56 offen lds
v_mfma_f32_16x16x32_bf16 v[136:139],a[132:135],a[236:239],v[136:139]
ds_read_b64_tr_b16 a[176:177],v22 offset:1792
v_bfe_u32 v73,v71,2,1
v_mfma_f32_16x16x32_bf16 v[136:139],a[128:131],a[232:235],v[136:139]
s_add_u32 m0,s58,1024
ds_read_b64_tr_b16 a[178:179],v22 offset:3840
v_lshlrev_b32 v73,1,v73
v_mfma_f32_16x16x32_bf16 v[144:147],a[128:131],a[240:243],v[144:147]
ds_read_b64_tr_b16 a[180:181],v22 offset:18176
v_xor_b32 v72,v72,v73
v_mfma_f32_16x16x32_bf16 v[144:147],a[132:135],a[244:247],v[144:147]
ds_read_b64_tr_b16 a[182:183],v22 offset:20224
v_mul_u32_u24 v71,1280,v71
v_mfma_f32_16x16x32_bf16 v[152:155],a[132:135],a[252:255],v[152:155]
buffer_load_dwordx4 v57,s[32:35],s56 offen lds
v_mfma_f32_16x16x32_bf16 v[152:155],a[128:131],a[248:251],v[152:155]
)A"
R"A(v_lshl_add_u32 v71,v72,4,v71
v_add_u32 v71,s44,v71
v_mfma_f32_16x16x32_bf16 v[132:135],a[136:139],a[224:227],v[132:135]
ds_read_b64_tr_b16 a[184:185],v22 offset:1800
v_subrev_u32 v9,2048,v71
v_mfma_f32_16x16x32_bf16 v[132:135],a[140:143],a[228:231],v[132:135]
s_add_u32 m0,s58,2048
ds_read_b64_tr_b16 a[186:187],v22 offset:3848
v_add_u32 v70,192,v12
v_mfma_f32_16x16x32_bf16 v[140:143],a[140:143],a[236:239],v[140:143]
ds_read_b64_tr_b16 a[188:189],v22 offset:18184
v_mul_u32_u24 v71,3277,v70
v_mfma_f32_16x16x32_bf16 v[140:143],a[136:139],a[232:235],v[140:143]
ds_read_b64_tr_b16 a[190:191],v22 offset:20232
v_lshrrev_b32 v71,16,v71
v_mfma_f32_16x16x32_bf16 v[148:151],a[136:139],a[240:243],v[148:151]
buffer_load_dwordx4 v58,s[32:35],s56 offen lds
v_mfma_f32_16x16x32_bf16 v[148:151],a[140:143],a[244:247],v[148:151]
v_mul_u32_u24 v72,20,v71
v_sub_u32 v72,v70,v72
v_mfma_f32_16x16x32_bf16 v[156:159],a[140:143],a[252:255],v[156:159]
s_add_u32 m0,s58,3072
v_bfe_u32 v73,v71,2,1
v_lshlrev_b32 v73,1,v73
v_mfma_f32_16x16x32_bf16 v[156:159],a[136:139],a[248:251],v[156:159]
ds_read_b64_tr_b16 a[128:129],v22
v_xor_b32 v72,v72,v73
v_mfma_f32_16x16x32_bf16 v[160:163],a[144:147],a[224:227],v[160:163]
ds_read_b64_tr_b16 a[130:131],v22 offset:2048
v_mul_u32_u24 v71,1280,v71
v_mfma_f32_16x16x32_bf16 v[160:163],a[148:151],a[228:231],v[160:163]
buffer_load_dwordx4 v59,s[32:35],s56 offen lds
v_mfma_f32_16x16x32_bf16 v[168:171],a[148:151],a[236:239],v[168:171]
ds_read_b64_tr_b16 a[132:133],v22 offset:16384
v_lshl_add_u32 v71,v72,4,v71
v_mfma_f32_16x16x32_bf16 v[168:171],a[144:147],a[232:235],v[168:171]
ds_read_b64_tr_b16 a[134:135],v22 offset:18432
v_add_u32 v71,s44,v71
v_mfma_f32_16x16x32_bf16 v[176:179],a[144:147],a[240:243],v[176:179]
s_add_u32 m0,s58,4096
v_subrev_u32 v10,3072,v71
v_add_u32 v70,256,v12
v_mfma_f32_16x16x32_bf16 v[176:179],a[148:151],a[244:247],v[176:179]
ds_read_b64_tr_b16 a[136:137],v22 offset:8
v_mul_u32_u24 v71,3277,v70
v_mfma_f32_16x16x32_bf16 v[184:187],a[148:151],a[252:255],v[184:187]
ds_read_b64_tr_b16 a[138:139],v22 offset:2056
v_lshrrev_b32 v71,16,v71
v_mfma_f32_16x16x32_bf16 v[184:187],a[144:147],a[248:251],v[184:187]
buffer_load_dwordx4 v60,s[32:35],s56 offen lds
v_mfma_f32_16x16x32_bf16 v[164:167],a[152:155],a[224:227],v[164:167]
s_waitcnt lgkmcnt(4)
ds_read_b64_tr_b16 a[140:141],v22 offset:16392
v_mul_u32_u24 v72,20,v71
v_mfma_f32_16x16x32_bf16 v[164:167],a[156:159],a[228:231],v[164:167]
s_add_u32 m0,s58,5120
ds_read_b64_tr_b16 a[142:143],v22 offset:18440
v_sub_u32 v72,v70,v72
v_mfma_f32_16x16x32_bf16 v[172:175],a[156:159],a[236:239],v[172:175]
v_bfe_u32 v73,v71,2,1
v_lshlrev_b32 v73,1,v73
v_mfma_f32_16x16x32_bf16 v[172:175],a[152:155],a[232:235],v[172:175]
ds_read_b64_tr_b16 a[144:145],v22 offset:256
v_xor_b32 v72,v72,v73
v_mfma_f32_16x16x32_bf16 v[180:183],a[152:155],a[240:243],v[180:183]
buffer_load_dwordx4 v61,s[32:35],s56 offen lds
v_mfma_f32_16x16x32_bf16 v[180:183],a[156:159],a[244:247],v[180:183]
ds_read_b64_tr_b16 a[146:147],v22 offset:2304
v_mul_u32_u24 v71,1280,v71
v_mfma_f32_16x16x32_bf16 v[188:191],a[156:159],a[252:255],v[188:191]
ds_read_b64_tr_b16 a[148:149],v22 offset:16640
v_lshl_add_u32 v71,v72,4,v71
v_mfma_f32_16x16x32_bf16 v[188:191],a[152:155],a[248:251],v[188:191]
s_add_u32 m0,s58,6144
ds_read_b64_tr_b16 a[150:151],v22 offset:18688
v_add_u32 v71,s44,v71
v_mfma_f32_16x16x32_bf16 v[192:195],a[160:163],a[224:227],v[192:195]
ds_read_b64_tr_b16 a[152:153],v22 offset:264
v_subrev_u32 v11,0,v71
v_mfma_f32_16x16x32_bf16 v[192:195],a[164:167],a[228:231],v[192:195]
ds_read_b64_tr_b16 a[154:155],v22 offset:2312
v_lshlrev_b32 v24,10,v6
v_mfma_f32_16x16x32_bf16 v[200:203],a[164:167],a[236:239],v[200:203]
buffer_load_dwordx4 v62,s[32:35],s56 offen lds
v_mfma_f32_16x16x32_bf16 v[200:203],a[160:163],a[232:235],v[200:203]
s_cmp_eq_u32 s60,131072
s_cselect_b32 s60,32768,s60
ds_read_b64_tr_b16 a[156:157],v22 offset:16648
v_mfma_f32_16x16x32_bf16 v[208:211],a[160:163],a[240:243],v[208:211]
s_add_u32 m0,s58,7168
ds_read_b64_tr_b16 a[158:159],v22 offset:18696
v_xor_b32 v20,131072,v20
v_mfma_f32_16x16x32_bf16 v[208:211],a[164:167],a[244:247],v[208:211]
s_add_u32 s58,s58,32768
v_xor_b32 v21,131072,v21
v_mfma_f32_16x16x32_bf16 v[216:219],a[164:167],a[252:255],v[216:219]
s_cmp_ge_u32 s58,131072
s_cselect_b32 s62,98304,0
ds_read_b64_tr_b16 a[164:165],v22 offset:16896
v_mfma_f32_16x16x32_bf16 v[216:219],a[160:163],a[248:251],v[216:219]
s_sub_u32 s58,s58,s62
buffer_load_dwordx4 v63,s[32:35],s56 offen lds
v_mfma_f32_16x16x32_bf16 v[196:199],a[168:171],a[224:227],v[196:199]
s_add_u32 s56,s56,524288
ds_read_b64_tr_b16 a[160:161],v22 offset:512
v_mfma_f32_16x16x32_bf16 v[196:199],a[172:175],a[228:231],v[196:199]
ds_read_b64_tr_b16 a[162:163],v22 offset:2560
v_mfma_f32_16x16x32_bf16 v[204:207],a[172:175],a[236:239],v[204:207]
s_add_u32 m0,s59,0
ds_read_b64_tr_b16 a[166:167],v22 offset:18944
v_mfma_f32_16x16x32_bf16 v[204:207],a[168:171],a[232:235],v[204:207]
v_mfma_f32_16x16x32_bf16 v[212:215],a[168:171],a[240:243],v[212:215]
v_mfma_f32_16x16x32_bf16 v[212:215],a[172:175],a[244:247],v[212:215]
buffer_load_dwordx4 v48,s[28:31],s57 offen lds
v_mfma_f32_16x16x32_bf16 v[220:223],a[172:175],a[252:255],v[220:223]
ds_read_b64_tr_b16 a[172:173],v22 offset:16904
v_mfma_f32_16x16x32_bf16 v[220:223],a[168:171],a[248:251],v[220:223]
ds_read_b64_tr_b16 a[168:169],v22 offset:520
v_mfma_f32_16x16x32_bf16 v[224:227],a[176:179],a[224:227],v[224:227]
ds_read_b64_tr_b16 a[170:171],v22 offset:2568
v_mfma_f32_16x16x32_bf16 v[224:227],a[180:183],a[228:231],v[224:227]
ds_read_b64_tr_b16 a[174:175],v22 offset:18952
v_mfma_f32_16x16x32_bf16 v[232:235],a[180:183],a[236:239],v[232:235]
buffer_load_dwordx4 v49,s[28:31],s57 offen offset:1024 lds
v_mfma_f32_16x16x32_bf16 v[232:235],a[176:179],a[232:235],v[232:235]
v_mfma_f32_16x16x32_bf16 v[240:243],a[176:179],a[240:243],v[240:243]
v_mfma_f32_16x16x32_bf16 v[240:243],a[180:183],a[244:247],v[240:243]
v_mfma_f32_16x16x32_bf16 v[248:251],a[180:183],a[252:255],v[248:251]
ds_read_b64_tr_b16 a[180:181],v22 offset:17152
v_mfma_f32_16x16x32_bf16 v[248:251],a[176:179],a[248:251],v[248:251]
ds_read_b64_tr_b16 a[176:177],v22 offset:768
v_mfma_f32_16x16x32_bf16 v[228:231],a[184:187],a[224:227],v[228:231]
buffer_load_dwordx4 v50,s[28:31],s57 offen offset:2048 lds
v_mfma_f32_16x16x32_bf16 v[228:231],a[188:191],a[228:231],v[228:231]
ds_read_b64_tr_b16 a[178:179],v22 offset:2816
v_mfma_f32_16x16x32_bf16 v[236:239],a[188:191],a[236:239],v[236:239]
ds_read_b64_tr_b16 a[182:183],v22 offset:19200
v_mfma_f32_16x16x32_bf16 v[236:239],a[184:187],a[232:235],v[236:239]
v_mfma_f32_16x16x32_bf16 v[244:247],a[184:187],a[240:243],v[244:247]
v_mfma_f32_16x16x32_bf16 v[244:247],a[188:191],a[244:247],v[244:247]
buffer_load_dwordx4 v51,s[28:31],s57 offen offset:3072 lds
v_mfma_f32_16x16x32_bf16 v[252:255],a[188:191],a[252:255],v[252:255]
ds_read_b64_tr_b16 a[188:189],v22 offset:17160
v_mfma_f32_16x16x32_bf16 v[252:255],a[184:187],a[248:251],v[252:255]
ds_read_b64_tr_b16 a[184:185],v22 offset:776
v_mfma_f32_16x16x32_bf16 a[0:3],a[128:131],a[224:227],a[0:3]
s_add_u32 m0,s59,4096
s_xor_b32 s59,s59,131072
ds_read_b64_tr_b16 a[186:187],v22 offset:2824
s_waitcnt lgkmcnt(5)
v_mfma_f32_16x16x32_bf16 a[0:3],a[132:135],a[228:231],a[0:3]
ds_read_b64_tr_b16 a[190:191],v22 offset:19208
v_add_u32 v22,s60,v23
v_mfma_f32_16x16x32_bf16 a[32:35],a[132:135],a[236:239],a[32:35]
v_mfma_f32_16x16x32_bf16 a[32:35],a[128:131],a[232:235],a[32:35]
buffer_load_dwordx4 v52,s[28:31],s57 offen lds
v_mfma_f32_16x16x32_bf16 a[64:67],a[128:131],a[240:243],a[64:67]
v_mfma_f32_16x16x32_bf16 a[64:67],a[132:135],a[244:247],a[64:67]
v_mfma_f32_16x16x32_bf16 a[96:99],a[132:135],a[252:255],a[96:99]
v_mfma_f32_16x16x32_bf16 a[96:99],a[128:131],a[248:251],a[96:99]
v_mfma_f32_16x16x32_bf16 a[4:7],a[136:139],a[224:227],a[4:7]
buffer_load_dwordx4 v53,s[28:31],s57 offen offset:1024 lds
v_mfma_f32_16x16x32_bf16 a[4:7],a[140:143],a[228:231],a[4:7]
v_mfma_f32_16x16x32_bf16 a[36:39],a[140:143],a[236:239],a[36:39]
v_mfma_f32_16x16x32_bf16 a[36:39],a[136:139],a[232:235],a[36:39]
v_mfma_f32_16x16x32_bf16 a[68:71],a[136:139],a[240:243],a[68:71]
v_mfma_f32_16x16x32_bf16 a[68:71],a[140:143],a[244:247],a[68:71]
v_mfma_f32_16x16x32_bf16 a[100:103],a[140:143],a[252:255],a[100:103]
buffer_load_dwordx4 v54,s[28:31],s57 offen offset:2048 lds
v_mfma_f32_16x16x32_bf16 a[100:103],a[136:139],a[248:251],a[100:103]
v_mfma_f32_16x16x32_bf16 a[8:11],a[144:147],a[224:227],a[8:11]
v_mfma_f32_16x16x32_bf16 a[8:11],a[148:151],a[228:231],a[8:11]
v_mfma_f32_16x16x32_bf16 a[40:43],a[148:151],a[236:239],a[40:43]
v_mfma_f32_16x16x32_bf16 a[40:43],a[144:147],a[232:235],a[40:43]
buffer_load_dwordx4 v55,s[28:31],s57 offen offset:3072 lds
v_mfma_f32_16x16x32_bf16 a[72:75],a[144:147],a[240:243],a[72:75]
s_add_u32 s57,s57,128
v_mfma_f32_16x16x32_bf16 a[72:75],a[148:151],a[244:247],a[72:75]
v_mfma_f32_16x16x32_bf16 a[104:107],a[148:151],a[252:255],a[104:107]
v_mfma_f32_16x16x32_bf16 a[104:107],a[144:147],a[248:251],a[104:107]
v_mfma_f32_16x16x32_bf16 a[12:15],a[152:155],a[224:227],a[12:15]
v_mfma_f32_16x16x32_bf16 a[12:15],a[156:159],a[228:231],a[12:15]
v_mfma_f32_16x16x32_bf16 a[44:47],a[156:159],a[236:239],a[44:47]
v_mfma_f32_16x16x32_bf16 a[44:47],a[152:155],a[232:235],a[44:47]
v_mfma_f32_16x16x32_bf16 a[76:79],a[152:155],a[240:243],a[76:79]
v_mfma_f32_16x16x32_bf16 a[76:79],a[156:159],a[244:247],a[76:79]
v_mfma_f32_16x16x32_bf16 a[108:111],a[156:159],a[252:255],a[108:111]
v_mfma_f32_16x16x32_bf16 a[108:111],a[152:155],a[248:251],a[108:111]
s_waitcnt vmcnt(18) lgkmcnt(0)
v_mfma_f32_16x16x32_bf16 a[16:19],a[160:163],a[224:227],a[16:19]
s_barrier
ds_read_b128 a[192:195],v20
v_mfma_f32_16x16x32_bf16 a[16:19],a[164:167],a[228:231],a[16:19]
ds_read_b128 a[196:199],v21
v_mfma_f32_16x16x32_bf16 a[48:51],a[164:167],a[236:239],a[48:51]
ds_read_b128 a[200:203],v20 offset:2048
v_mfma_f32_16x16x32_bf16 a[48:51],a[160:163],a[232:235],a[48:51]
ds_read_b128 a[204:207],v21 offset:2048
v_mfma_f32_16x16x32_bf16 a[80:83],a[160:163],a[240:243],a[80:83]
ds_read_b128 a[208:211],v20 offset:4096
v_mfma_f32_16x16x32_bf16 a[80:83],a[164:167],a[244:247],a[80:83]
ds_read_b128 a[212:215],v21 offset:4096
v_mfma_f32_16x16x32_bf16 a[112:115],a[164:167],a[252:255],a[112:115]
ds_read_b128 a[216:219],v20 offset:6144
v_mfma_f32_16x16x32_bf16 a[112:115],a[160:163],a[248:251],a[112:115]
ds_read_b128 a[220:223],v21 offset:6144
v_mfma_f32_16x16x32_bf16 a[20:23],a[168:171],a[224:227],a[20:23]
ds_read_b64_tr_b16 a[128:129],v22 offset:1024
v_mfma_f32_16x16x32_bf16 a[20:23],a[172:175],a[228:231],a[20:23]
ds_read_b64_tr_b16 a[130:131],v22 offset:3072
v_mfma_f32_16x16x32_bf16 a[52:55],a[172:175],a[236:239],a[52:55]
ds_read_b64_tr_b16 a[132:133],v22 offset:17408
v_mfma_f32_16x16x32_bf16 a[52:55],a[168:171],a[232:235],a[52:55]
ds_read_b64_tr_b16 a[134:135],v22 offset:19456
v_mfma_f32_16x16x32_bf16 a[84:87],a[168:171],a[240:243],a[84:87]
ds_read_b64_tr_b16 a[136:137],v22 offset:1032
v_mfma_f32_16x16x32_bf16 a[84:87],a[172:175],a[244:247],a[84:87]
ds_read_b64_tr_b16 a[138:139],v22 offset:3080
v_mfma_f32_16x16x32_bf16 a[116:119],a[172:175],a[252:255],a[116:119]
ds_read_b64_tr_b16 a[140:141],v22 offset:17416
v_mfma_f32_16x16x32_bf16 a[116:119],a[168:171],a[248:251],a[116:119]
ds_read_b64_tr_b16 a[142:143],v22 offset:19464
v_mfma_f32_16x16x32_bf16 a[24:27],a[176:179],a[224:227],a[24:27]
ds_read_b64_tr_b16 a[144:145],v22 offset:1280
v_mfma_f32_16x16x32_bf16 a[24:27],a[180:183],a[228:231],a[24:27]
ds_read_b64_tr_b16 a[146:147],v22 offset:3328
v_mfma_f32_16x16x32_bf16 a[56:59],a[180:183],a[236:239],a[56:59]
ds_read_b64_tr_b16 a[148:149],v22 offset:17664
v_mfma_f32_16x16x32_bf16 a[56:59],a[176:179],a[232:235],a[56:59]
ds_read_b64_tr_b16 a[150:151],v22 offset:19712
v_mfma_f32_16x16x32_bf16 a[88:91],a[176:179],a[240:243],a[88:91]
ds_read_b64_tr_b16 a[152:153],v22 offset:1288
v_mfma_f32_16x16x32_bf16 a[88:91],a[180:183],a[244:247],a[88:91]
ds_read_b64_tr_b16 a[154:155],v22 offset:3336
v_mfma_f32_16x16x32_bf16 a[120:123],a[180:183],a[252:255],a[120:123]
)A"
R"A(ds_read_b64_tr_b16 a[156:157],v22 offset:17672
v_mfma_f32_16x16x32_bf16 a[120:123],a[176:179],a[248:251],a[120:123]
ds_read_b64_tr_b16 a[158:159],v22 offset:19720
v_mfma_f32_16x16x32_bf16 a[28:31],a[184:187],a[224:227],a[28:31]
v_mfma_f32_16x16x32_bf16 a[28:31],a[188:191],a[228:231],a[28:31]
v_mfma_f32_16x16x32_bf16 a[60:63],a[188:191],a[236:239],a[60:63]
v_mfma_f32_16x16x32_bf16 a[60:63],a[184:187],a[232:235],a[60:63]
v_mfma_f32_16x16x32_bf16 a[92:95],a[184:187],a[240:243],a[92:95]
v_mfma_f32_16x16x32_bf16 a[92:95],a[188:191],a[244:247],a[92:95]
v_mfma_f32_16x16x32_bf16 a[124:127],a[188:191],a[252:255],a[124:127]
v_mfma_f32_16x16x32_bf16 a[124:127],a[184:187],a[248:251],a[124:127]
s_mov_b32 s53,3
L_qp_%=:
s_waitcnt lgkmcnt(0)
v_mfma_f32_16x16x32_bf16 v[128:131],a[128:131],a[192:195],v[128:131]
s_add_u32 m0,s58,0
s_add_u32 s60,s60,32768
ds_read_b64_tr_b16 a[160:161],v22 offset:1536
v_mfma_f32_16x16x32_bf16 v[128:131],a[132:135],a[196:199],v[128:131]
s_mov_b32 s90,L_att0_%=-L_pc_%=+128
buffer_load_dwordx4 v56,s[32:35],s56 offen lds
v_mfma_f32_16x16x32_bf16 v[136:139],a[132:135],a[204:207],v[136:139]
s_cmp_eq_u32 s60,131072
s_cselect_b32 s60,32768,s60
ds_read_b64_tr_b16 a[162:163],v22 offset:3584
v_mfma_f32_16x16x32_bf16 v[136:139],a[128:131],a[200:203],v[136:139]
s_add_u32 m0,s58,1024
ds_read_b64_tr_b16 a[164:165],v22 offset:17920
v_xor_b32 v20,131072,v20
v_mfma_f32_16x16x32_bf16 v[144:147],a[128:131],a[208:211],v[144:147]
s_sub_u32 s53,s53,1
ds_read_b64_tr_b16 a[166:167],v22 offset:19968
v_xor_b32 v21,131072,v21
v_mfma_f32_16x16x32_bf16 v[144:147],a[132:135],a[212:215],v[144:147]
ds_read_b64_tr_b16 a[168:169],v22 offset:1544
v_mfma_f32_16x16x32_bf16 v[152:155],a[132:135],a[220:223],v[152:155]
buffer_load_dwordx4 v57,s[32:35],s56 offen lds
v_mfma_f32_16x16x32_bf16 v[152:155],a[128:131],a[216:219],v[152:155]
ds_read_b64_tr_b16 a[170:171],v22 offset:3592
v_mfma_f32_16x16x32_bf16 v[132:135],a[136:139],a[192:195],v[132:135]
ds_read_b64_tr_b16 a[172:173],v22 offset:17928
v_mfma_f32_16x16x32_bf16 v[132:135],a[140:143],a[196:199],v[132:135]
s_add_u32 m0,s58,2048
ds_read_b64_tr_b16 a[174:175],v22 offset:19976
v_mfma_f32_16x16x32_bf16 v[140:143],a[140:143],a[204:207],v[140:143]
ds_read_b64_tr_b16 a[176:177],v22 offset:1792
v_mfma_f32_16x16x32_bf16 v[140:143],a[136:139],a[200:203],v[140:143]
ds_read_b64_tr_b16 a[178:179],v22 offset:3840
v_mfma_f32_16x16x32_bf16 v[148:151],a[136:139],a[208:211],v[148:151]
buffer_load_dwordx4 v58,s[32:35],s56 offen lds
v_mfma_f32_16x16x32_bf16 v[148:151],a[140:143],a[212:215],v[148:151]
ds_read_b64_tr_b16 a[180:181],v22 offset:18176
v_mfma_f32_16x16x32_bf16 v[156:159],a[140:143],a[220:223],v[156:159]
s_add_u32 m0,s58,3072
ds_read_b64_tr_b16 a[182:183],v22 offset:20224
v_mfma_f32_16x16x32_bf16 v[156:159],a[136:139],a[216:219],v[156:159]
ds_read_b64_tr_b16 a[184:185],v22 offset:1800
v_mfma_f32_16x16x32_bf16 v[160:163],a[144:147],a[192:195],v[160:163]
ds_read_b64_tr_b16 a[186:187],v22 offset:3848
v_mfma_f32_16x16x32_bf16 v[160:163],a[148:151],a[196:199],v[160:163]
buffer_load_dwordx4 v59,s[32:35],s56 offen lds
v_mfma_f32_16x16x32_bf16 v[168:171],a[148:151],a[204:207],v[168:171]
ds_read_b64_tr_b16 a[188:189],v22 offset:18184
v_mfma_f32_16x16x32_bf16 v[168:171],a[144:147],a[200:203],v[168:171]
ds_read_b64_tr_b16 a[190:191],v22 offset:20232
v_mfma_f32_16x16x32_bf16 v[176:179],a[144:147],a[208:211],v[176:179]
s_add_u32 m0,s58,4096
ds_read_b64_tr_b16 a[128:129],v22
v_mfma_f32_16x16x32_bf16 v[176:179],a[148:151],a[212:215],v[176:179]
ds_read_b64_tr_b16 a[130:131],v22 offset:2048
v_mfma_f32_16x16x32_bf16 v[184:187],a[148:151],a[220:223],v[184:187]
ds_read_b64_tr_b16 a[132:133],v22 offset:16384
v_mfma_f32_16x16x32_bf16 v[184:187],a[144:147],a[216:219],v[184:187]
buffer_load_dwordx4 v60,s[32:35],s56 offen lds
v_mfma_f32_16x16x32_bf16 v[164:167],a[152:155],a[192:195],v[164:167]
ds_read_b64_tr_b16 a[134:135],v22 offset:18432
v_mfma_f32_16x16x32_bf16 v[164:167],a[156:159],a[196:199],v[164:167]
s_add_u32 m0,s58,5120
ds_read_b64_tr_b16 a[136:137],v22 offset:8
v_mfma_f32_16x16x32_bf16 v[172:175],a[156:159],a[204:207],v[172:175]
ds_read_b64_tr_b16 a[138:139],v22 offset:2056
v_mfma_f32_16x16x32_bf16 v[172:175],a[152:155],a[200:203],v[172:175]
ds_read_b64_tr_b16 a[140:141],v22 offset:16392
v_mfma_f32_16x16x32_bf16 v[180:183],a[152:155],a[208:211],v[180:183]
buffer_load_dwordx4 v61,s[32:35],s56 offen lds
v_mfma_f32_16x16x32_bf16 v[180:183],a[156:159],a[212:215],v[180:183]
ds_read_b64_tr_b16 a[142:143],v22 offset:18440
v_mfma_f32_16x16x32_bf16 v[188:191],a[156:159],a[220:223],v[188:191]
ds_read_b64_tr_b16 a[144:145],v22 offset:256
v_mfma_f32_16x16x32_bf16 v[188:191],a[152:155],a[216:219],v[188:191]
s_add_u32 m0,s58,6144
ds_read_b64_tr_b16 a[146:147],v22 offset:2304
s_waitcnt lgkmcnt(7)
v_mfma_f32_16x16x32_bf16 v[192:195],a[160:163],a[192:195],v[192:195]
ds_read_b64_tr_b16 a[148:149],v22 offset:16640
v_mfma_f32_16x16x32_bf16 v[192:195],a[164:167],a[196:199],v[192:195]
ds_read_b64_tr_b16 a[150:151],v22 offset:18688
v_mfma_f32_16x16x32_bf16 v[200:203],a[164:167],a[204:207],v[200:203]
buffer_load_dwordx4 v62,s[32:35],s56 offen lds
v_mfma_f32_16x16x32_bf16 v[200:203],a[160:163],a[200:203],v[200:203]
ds_read_b64_tr_b16 a[152:153],v22 offset:264
v_mfma_f32_16x16x32_bf16 v[208:211],a[160:163],a[208:211],v[208:211]
s_add_u32 m0,s58,7168
s_add_u32 s58,s58,32768
ds_read_b64_tr_b16 a[154:155],v22 offset:2312
v_mfma_f32_16x16x32_bf16 v[208:211],a[164:167],a[212:215],v[208:211]
s_cmp_ge_u32 s58,131072
s_cselect_b32 s62,98304,0
ds_read_b64_tr_b16 a[156:157],v22 offset:16648
v_mfma_f32_16x16x32_bf16 v[216:219],a[164:167],a[220:223],v[216:219]
s_sub_u32 s58,s58,s62
ds_read_b64_tr_b16 a[158:159],v22 offset:18696
v_mfma_f32_16x16x32_bf16 v[216:219],a[160:163],a[216:219],v[216:219]
buffer_load_dwordx4 v63,s[32:35],s56 offen lds
v_mfma_f32_16x16x32_bf16 v[196:199],a[168:171],a[192:195],v[196:199]
s_add_u32 s56,s56,524288
ds_read_b64_tr_b16 a[160:161],v22 offset:512
v_mfma_f32_16x16x32_bf16 v[196:199],a[172:175],a[196:199],v[196:199]
ds_read_b64_tr_b16 a[162:163],v22 offset:2560
v_mfma_f32_16x16x32_bf16 v[204:207],a[172:175],a[204:207],v[204:207]
s_add_u32 m0,s59,0
ds_read_b64_tr_b16 a[164:165],v22 offset:16896
v_mfma_f32_16x16x32_bf16 v[204:207],a[168:171],a[200:203],v[204:207]
ds_read_b64_tr_b16 a[166:167],v22 offset:18944
v_mfma_f32_16x16x32_bf16 v[212:215],a[168:171],a[208:211],v[212:215]
v_mfma_f32_16x16x32_bf16 v[212:215],a[172:175],a[212:215],v[212:215]
buffer_load_dwordx4 v48,s[28:31],s57 offen lds
v_mfma_f32_16x16x32_bf16 v[220:223],a[172:175],a[220:223],v[220:223]
ds_read_b64_tr_b16 a[172:173],v22 offset:16904
v_mfma_f32_16x16x32_bf16 v[220:223],a[168:171],a[216:219],v[220:223]
ds_read_b64_tr_b16 a[168:169],v22 offset:520
v_mfma_f32_16x16x32_bf16 v[224:227],a[176:179],a[192:195],v[224:227]
ds_read_b64_tr_b16 a[170:171],v22 offset:2568
v_mfma_f32_16x16x32_bf16 v[224:227],a[180:183],a[196:199],v[224:227]
ds_read_b64_tr_b16 a[174:175],v22 offset:18952
v_mfma_f32_16x16x32_bf16 v[232:235],a[180:183],a[204:207],v[232:235]
buffer_load_dwordx4 v49,s[28:31],s57 offen offset:1024 lds
v_mfma_f32_16x16x32_bf16 v[232:235],a[176:179],a[200:203],v[232:235]
v_mfma_f32_16x16x32_bf16 v[240:243],a[176:179],a[208:211],v[240:243]
v_mfma_f32_16x16x32_bf16 v[240:243],a[180:183],a[212:215],v[240:243]
v_mfma_f32_16x16x32_bf16 v[248:251],a[180:183],a[220:223],v[248:251]
ds_read_b64_tr_b16 a[180:181],v22 offset:17152
v_mfma_f32_16x16x32_bf16 v[248:251],a[176:179],a[216:219],v[248:251]
ds_read_b64_tr_b16 a[176:177],v22 offset:768
v_mfma_f32_16x16x32_bf16 v[228:231],a[184:187],a[192:195],v[228:231]
buffer_load_dwordx4 v50,s[28:31],s57 offen offset:2048 lds
v_mfma_f32_16x16x32_bf16 v[228:231],a[188:191],a[196:199],v[228:231]
ds_read_b64_tr_b16 a[178:179],v22 offset:2816
v_mfma_f32_16x16x32_bf16 v[236:239],a[188:191],a[204:207],v[236:239]
ds_read_b64_tr_b16 a[182:183],v22 offset:19200
v_mfma_f32_16x16x32_bf16 v[236:239],a[184:187],a[200:203],v[236:239]
v_mfma_f32_16x16x32_bf16 v[244:247],a[184:187],a[208:211],v[244:247]
v_mfma_f32_16x16x32_bf16 v[244:247],a[188:191],a[212:215],v[244:247]
buffer_load_dwordx4 v51,s[28:31],s57 offen offset:3072 lds
v_mfma_f32_16x16x32_bf16 v[252:255],a[188:191],a[220:223],v[252:255]
ds_read_b64_tr_b16 a[188:189],v22 offset:17160
v_mfma_f32_16x16x32_bf16 v[252:255],a[184:187],a[216:219],v[252:255]
ds_read_b64_tr_b16 a[184:185],v22 offset:776
v_mfma_f32_16x16x32_bf16 a[0:3],a[128:131],a[192:195],a[0:3]
s_add_u32 m0,s59,4096
s_xor_b32 s59,s59,131072
ds_read_b64_tr_b16 a[186:187],v22 offset:2824
s_waitcnt lgkmcnt(5)
v_mfma_f32_16x16x32_bf16 a[0:3],a[132:135],a[196:199],a[0:3]
ds_read_b64_tr_b16 a[190:191],v22 offset:19208
v_add_u32 v22,s60,v23
v_mfma_f32_16x16x32_bf16 a[32:35],a[132:135],a[204:207],a[32:35]
s_add_u32 s60,s60,32768
s_cmp_eq_u32 s60,131072
s_cselect_b32 s60,32768,s60
v_mfma_f32_16x16x32_bf16 a[32:35],a[128:131],a[200:203],a[32:35]
buffer_load_dwordx4 v52,s[28:31],s57 offen lds
v_mfma_f32_16x16x32_bf16 a[64:67],a[128:131],a[208:211],a[64:67]
v_mfma_f32_16x16x32_bf16 a[64:67],a[132:135],a[212:215],a[64:67]
v_mfma_f32_16x16x32_bf16 a[96:99],a[132:135],a[220:223],a[96:99]
v_mfma_f32_16x16x32_bf16 a[96:99],a[128:131],a[216:219],a[96:99]
v_mfma_f32_16x16x32_bf16 a[4:7],a[136:139],a[192:195],a[4:7]
buffer_load_dwordx4 v53,s[28:31],s57 offen offset:1024 lds
v_mfma_f32_16x16x32_bf16 a[4:7],a[140:143],a[196:199],a[4:7]
v_mfma_f32_16x16x32_bf16 a[36:39],a[140:143],a[204:207],a[36:39]
v_mfma_f32_16x16x32_bf16 a[36:39],a[136:139],a[200:203],a[36:39]
v_mfma_f32_16x16x32_bf16 a[68:71],a[136:139],a[208:211],a[68:71]
v_mfma_f32_16x16x32_bf16 a[68:71],a[140:143],a[212:215],a[68:71]
v_mfma_f32_16x16x32_bf16 a[100:103],a[140:143],a[220:223],a[100:103]
buffer_load_dwordx4 v54,s[28:31],s57 offen offset:2048 lds
v_mfma_f32_16x16x32_bf16 a[100:103],a[136:139],a[216:219],a[100:103]
v_mfma_f32_16x16x32_bf16 a[8:11],a[144:147],a[192:195],a[8:11]
v_mfma_f32_16x16x32_bf16 a[8:11],a[148:151],a[196:199],a[8:11]
v_mfma_f32_16x16x32_bf16 a[40:43],a[148:151],a[204:207],a[40:43]
v_mfma_f32_16x16x32_bf16 a[40:43],a[144:147],a[200:203],a[40:43]
buffer_load_dwordx4 v55,s[28:31],s57 offen offset:3072 lds
v_mfma_f32_16x16x32_bf16 a[72:75],a[144:147],a[208:211],a[72:75]
s_add_u32 s57,s57,128
v_mfma_f32_16x16x32_bf16 a[72:75],a[148:151],a[212:215],a[72:75]
v_mfma_f32_16x16x32_bf16 a[104:107],a[148:151],a[220:223],a[104:107]
v_mfma_f32_16x16x32_bf16 a[104:107],a[144:147],a[216:219],a[104:107]
v_mfma_f32_16x16x32_bf16 a[12:15],a[152:155],a[192:195],a[12:15]
v_mfma_f32_16x16x32_bf16 a[12:15],a[156:159],a[196:199],a[12:15]
v_mfma_f32_16x16x32_bf16 a[44:47],a[156:159],a[204:207],a[44:47]
v_mfma_f32_16x16x32_bf16 a[44:47],a[152:155],a[200:203],a[44:47]
v_mfma_f32_16x16x32_bf16 a[76:79],a[152:155],a[208:211],a[76:79]
v_mfma_f32_16x16x32_bf16 a[76:79],a[156:159],a[212:215],a[76:79]
v_mfma_f32_16x16x32_bf16 a[108:111],a[156:159],a[220:223],a[108:111]
v_mfma_f32_16x16x32_bf16 a[108:111],a[152:155],a[216:219],a[108:111]
s_waitcnt vmcnt(16) lgkmcnt(0)
v_mfma_f32_16x16x32_bf16 a[16:19],a[160:163],a[192:195],a[16:19]
s_barrier
ds_read_b128 a[224:227],v20
v_mfma_f32_16x16x32_bf16 a[16:19],a[164:167],a[196:199],a[16:19]
ds_read_b64_tr_b16 a[128:129],v22 offset:1024
v_mfma_f32_16x16x32_bf16 a[48:51],a[164:167],a[204:207],a[48:51]
ds_read_b64_tr_b16 a[130:131],v22 offset:3072
v_mfma_f32_16x16x32_bf16 a[48:51],a[160:163],a[200:203],a[48:51]
ds_read_b128 a[228:231],v21
v_mfma_f32_16x16x32_bf16 a[80:83],a[160:163],a[208:211],a[80:83]
ds_read_b64_tr_b16 a[132:133],v22 offset:17408
v_mfma_f32_16x16x32_bf16 a[80:83],a[164:167],a[212:215],a[80:83]
ds_read_b64_tr_b16 a[134:135],v22 offset:19456
v_mfma_f32_16x16x32_bf16 a[112:115],a[164:167],a[220:223],a[112:115]
ds_read_b128 a[232:235],v20 offset:2048
v_mfma_f32_16x16x32_bf16 a[112:115],a[160:163],a[216:219],a[112:115]
ds_read_b128 a[236:239],v21 offset:2048
v_mfma_f32_16x16x32_bf16 a[20:23],a[168:171],a[192:195],a[20:23]
ds_read_b128 a[240:243],v20 offset:4096
v_mfma_f32_16x16x32_bf16 a[20:23],a[172:175],a[196:199],a[20:23]
)A"
R"A(ds_read_b128 a[244:247],v21 offset:4096
v_mfma_f32_16x16x32_bf16 a[52:55],a[172:175],a[204:207],a[52:55]
ds_read_b128 a[248:251],v20 offset:6144
v_xor_b32 v20,131072,v20
v_mfma_f32_16x16x32_bf16 a[52:55],a[168:171],a[200:203],a[52:55]
ds_read_b128 a[252:255],v21 offset:6144
v_xor_b32 v21,131072,v21
v_mfma_f32_16x16x32_bf16 a[84:87],a[168:171],a[208:211],a[84:87]
ds_read_b64_tr_b16 a[136:137],v22 offset:1032
v_mfma_f32_16x16x32_bf16 a[84:87],a[172:175],a[212:215],a[84:87]
ds_read_b64_tr_b16 a[138:139],v22 offset:3080
v_mfma_f32_16x16x32_bf16 a[116:119],a[172:175],a[220:223],a[116:119]
ds_read_b64_tr_b16 a[140:141],v22 offset:17416
v_mfma_f32_16x16x32_bf16 a[116:119],a[168:171],a[216:219],a[116:119]
ds_read_b64_tr_b16 a[142:143],v22 offset:19464
v_mfma_f32_16x16x32_bf16 a[24:27],a[176:179],a[192:195],a[24:27]
ds_read_b64_tr_b16 a[144:145],v22 offset:1280
v_mfma_f32_16x16x32_bf16 a[24:27],a[180:183],a[196:199],a[24:27]
ds_read_b64_tr_b16 a[146:147],v22 offset:3328
v_mfma_f32_16x16x32_bf16 a[56:59],a[180:183],a[204:207],a[56:59]
ds_read_b64_tr_b16 a[148:149],v22 offset:17664
v_mfma_f32_16x16x32_bf16 a[56:59],a[176:179],a[200:203],a[56:59]
ds_read_b64_tr_b16 a[150:151],v22 offset:19712
v_mfma_f32_16x16x32_bf16 a[88:91],a[176:179],a[208:211],a[88:91]
ds_read_b64_tr_b16 a[152:153],v22 offset:1288
v_mfma_f32_16x16x32_bf16 a[88:91],a[180:183],a[212:215],a[88:91]
ds_read_b64_tr_b16 a[154:155],v22 offset:3336
v_mfma_f32_16x16x32_bf16 a[120:123],a[180:183],a[220:223],a[120:123]
ds_read_b64_tr_b16 a[156:157],v22 offset:17672
v_mfma_f32_16x16x32_bf16 a[120:123],a[176:179],a[216:219],a[120:123]
ds_read_b64_tr_b16 a[158:159],v22 offset:19720
v_mfma_f32_16x16x32_bf16 a[28:31],a[184:187],a[192:195],a[28:31]
ds_read_b64_tr_b16 a[160:161],v22 offset:1536
v_mfma_f32_16x16x32_bf16 a[28:31],a[188:191],a[196:199],a[28:31]
ds_read_b64_tr_b16 a[162:163],v22 offset:3584
v_mfma_f32_16x16x32_bf16 a[60:63],a[188:191],a[204:207],a[60:63]
ds_read_b64_tr_b16 a[164:165],v22 offset:17920
v_mfma_f32_16x16x32_bf16 a[60:63],a[184:187],a[200:203],a[60:63]
ds_read_b64_tr_b16 a[166:167],v22 offset:19968
v_mfma_f32_16x16x32_bf16 a[92:95],a[184:187],a[208:211],a[92:95]
ds_read_b64_tr_b16 a[168:169],v22 offset:1544
v_mfma_f32_16x16x32_bf16 a[92:95],a[188:191],a[212:215],a[92:95]
ds_read_b64_tr_b16 a[170:171],v22 offset:3592
v_mfma_f32_16x16x32_bf16 a[124:127],a[188:191],a[220:223],a[124:127]
s_add_u32 m0,s58,0
ds_read_b64_tr_b16 a[172:173],v22 offset:17928
v_mfma_f32_16x16x32_bf16 a[124:127],a[184:187],a[216:219],a[124:127]
s_waitcnt lgkmcnt(7)
ds_read_b64_tr_b16 a[174:175],v22 offset:19976
v_mfma_f32_16x16x32_bf16 v[128:131],a[128:131],a[224:227],v[128:131]
ds_read_b64_tr_b16 a[176:177],v22 offset:1792
v_mfma_f32_16x16x32_bf16 v[128:131],a[132:135],a[228:231],v[128:131]
buffer_load_dwordx4 v56,s[32:35],s56 offen lds
v_mfma_f32_16x16x32_bf16 v[136:139],a[132:135],a[236:239],v[136:139]
ds_read_b64_tr_b16 a[178:179],v22 offset:3840
v_mfma_f32_16x16x32_bf16 v[136:139],a[128:131],a[232:235],v[136:139]
s_add_u32 m0,s58,1024
ds_read_b64_tr_b16 a[180:181],v22 offset:18176
v_mfma_f32_16x16x32_bf16 v[144:147],a[128:131],a[240:243],v[144:147]
ds_read_b64_tr_b16 a[182:183],v22 offset:20224
v_mfma_f32_16x16x32_bf16 v[144:147],a[132:135],a[244:247],v[144:147]
ds_read_b64_tr_b16 a[184:185],v22 offset:1800
v_mfma_f32_16x16x32_bf16 v[152:155],a[132:135],a[252:255],v[152:155]
buffer_load_dwordx4 v57,s[32:35],s56 offen lds
v_mfma_f32_16x16x32_bf16 v[152:155],a[128:131],a[248:251],v[152:155]
ds_read_b64_tr_b16 a[186:187],v22 offset:3848
v_mfma_f32_16x16x32_bf16 v[132:135],a[136:139],a[224:227],v[132:135]
ds_read_b64_tr_b16 a[188:189],v22 offset:18184
v_mfma_f32_16x16x32_bf16 v[132:135],a[140:143],a[228:231],v[132:135]
s_add_u32 m0,s58,2048
ds_read_b64_tr_b16 a[190:191],v22 offset:20232
v_mfma_f32_16x16x32_bf16 v[140:143],a[140:143],a[236:239],v[140:143]
ds_read_b64_tr_b16 a[128:129],v22
v_mfma_f32_16x16x32_bf16 v[140:143],a[136:139],a[232:235],v[140:143]
ds_read_b64_tr_b16 a[130:131],v22 offset:2048
v_mfma_f32_16x16x32_bf16 v[148:151],a[136:139],a[240:243],v[148:151]
buffer_load_dwordx4 v58,s[32:35],s56 offen lds
v_mfma_f32_16x16x32_bf16 v[148:151],a[140:143],a[244:247],v[148:151]
ds_read_b64_tr_b16 a[132:133],v22 offset:16384
v_mfma_f32_16x16x32_bf16 v[156:159],a[140:143],a[252:255],v[156:159]
s_add_u32 m0,s58,3072
ds_read_b64_tr_b16 a[134:135],v22 offset:18432
v_mfma_f32_16x16x32_bf16 v[156:159],a[136:139],a[248:251],v[156:159]
ds_read_b64_tr_b16 a[136:137],v22 offset:8
v_mfma_f32_16x16x32_bf16 v[160:163],a[144:147],a[224:227],v[160:163]
ds_read_b64_tr_b16 a[138:139],v22 offset:2056
v_mfma_f32_16x16x32_bf16 v[160:163],a[148:151],a[228:231],v[160:163]
buffer_load_dwordx4 v59,s[32:35],s56 offen lds
v_mfma_f32_16x16x32_bf16 v[168:171],a[148:151],a[236:239],v[168:171]
ds_read_b64_tr_b16 a[140:141],v22 offset:16392
v_mfma_f32_16x16x32_bf16 v[168:171],a[144:147],a[232:235],v[168:171]
ds_read_b64_tr_b16 a[142:143],v22 offset:18440
v_mfma_f32_16x16x32_bf16 v[176:179],a[144:147],a[240:243],v[176:179]
s_add_u32 m0,s58,4096
v_mfma_f32_16x16x32_bf16 v[176:179],a[148:151],a[244:247],v[176:179]
v_mfma_f32_16x16x32_bf16 v[184:187],a[148:151],a[252:255],v[184:187]
ds_read_b64_tr_b16 a[148:149],v22 offset:16640
v_mfma_f32_16x16x32_bf16 v[184:187],a[144:147],a[248:251],v[184:187]
buffer_load_dwordx4 v60,s[32:35],s56 offen lds
v_mfma_f32_16x16x32_bf16 v[164:167],a[152:155],a[224:227],v[164:167]
ds_read_b64_tr_b16 a[144:145],v22 offset:256
v_mfma_f32_16x16x32_bf16 v[164:167],a[156:159],a[228:231],v[164:167]
s_add_u32 m0,s58,5120
ds_read_b64_tr_b16 a[146:147],v22 offset:2304
v_mfma_f32_16x16x32_bf16 v[172:175],a[156:159],a[236:239],v[172:175]
ds_read_b64_tr_b16 a[150:151],v22 offset:18688
v_mfma_f32_16x16x32_bf16 v[172:175],a[152:155],a[232:235],v[172:175]
v_mfma_f32_16x16x32_bf16 v[180:183],a[152:155],a[240:243],v[180:183]
buffer_load_dwordx4 v61,s[32:35],s56 offen lds
v_mfma_f32_16x16x32_bf16 v[180:183],a[156:159],a[244:247],v[180:183]
v_mfma_f32_16x16x32_bf16 v[188:191],a[156:159],a[252:255],v[188:191]
ds_read_b64_tr_b16 a[156:157],v22 offset:16648
v_mfma_f32_16x16x32_bf16 v[188:191],a[152:155],a[248:251],v[188:191]
s_add_u32 m0,s58,6144
ds_read_b64_tr_b16 a[152:153],v22 offset:264
s_waitcnt lgkmcnt(5)
v_mfma_f32_16x16x32_bf16 v[192:195],a[160:163],a[224:227],v[192:195]
ds_read_b64_tr_b16 a[154:155],v22 offset:2312
v_mfma_f32_16x16x32_bf16 v[192:195],a[164:167],a[228:231],v[192:195]
ds_read_b64_tr_b16 a[158:159],v22 offset:18696
v_mfma_f32_16x16x32_bf16 v[200:203],a[164:167],a[236:239],v[200:203]
buffer_load_dwordx4 v62,s[32:35],s56 offen lds
v_mfma_f32_16x16x32_bf16 v[200:203],a[160:163],a[232:235],v[200:203]
v_mfma_f32_16x16x32_bf16 v[208:211],a[160:163],a[240:243],v[208:211]
s_add_u32 m0,s58,7168
s_add_u32 s58,s58,32768
v_mfma_f32_16x16x32_bf16 v[208:211],a[164:167],a[244:247],v[208:211]
s_cmp_ge_u32 s58,131072
s_cselect_b32 s62,98304,0
s_sub_u32 s58,s58,s62
v_mfma_f32_16x16x32_bf16 v[216:219],a[164:167],a[252:255],v[216:219]
ds_read_b64_tr_b16 a[164:165],v22 offset:16896
v_mfma_f32_16x16x32_bf16 v[216:219],a[160:163],a[248:251],v[216:219]
buffer_load_dwordx4 v63,s[32:35],s56 offen lds
v_mfma_f32_16x16x32_bf16 v[196:199],a[168:171],a[224:227],v[196:199]
s_add_u32 s56,s56,524288
ds_read_b64_tr_b16 a[160:161],v22 offset:512
v_mfma_f32_16x16x32_bf16 v[196:199],a[172:175],a[228:231],v[196:199]
ds_read_b64_tr_b16 a[162:163],v22 offset:2560
v_mfma_f32_16x16x32_bf16 v[204:207],a[172:175],a[236:239],v[204:207]
s_add_u32 m0,s59,0
ds_read_b64_tr_b16 a[166:167],v22 offset:18944
v_mfma_f32_16x16x32_bf16 v[204:207],a[168:171],a[232:235],v[204:207]
v_mfma_f32_16x16x32_bf16 v[212:215],a[168:171],a[240:243],v[212:215]
v_mfma_f32_16x16x32_bf16 v[212:215],a[172:175],a[244:247],v[212:215]
buffer_load_dwordx4 v48,s[28:31],s57 offen lds
v_mfma_f32_16x16x32_bf16 v[220:223],a[172:175],a[252:255],v[220:223]
ds_read_b64_tr_b16 a[172:173],v22 offset:16904
v_mfma_f32_16x16x32_bf16 v[220:223],a[168:171],a[248:251],v[220:223]
ds_read_b64_tr_b16 a[168:169],v22 offset:520
v_mfma_f32_16x16x32_bf16 v[224:227],a[176:179],a[224:227],v[224:227]
ds_read_b64_tr_b16 a[170:171],v22 offset:2568
v_mfma_f32_16x16x32_bf16 v[224:227],a[180:183],a[228:231],v[224:227]
ds_read_b64_tr_b16 a[174:175],v22 offset:18952
v_mfma_f32_16x16x32_bf16 v[232:235],a[180:183],a[236:239],v[232:235]
buffer_load_dwordx4 v49,s[28:31],s57 offen offset:1024 lds
v_mfma_f32_16x16x32_bf16 v[232:235],a[176:179],a[232:235],v[232:235]
v_mfma_f32_16x16x32_bf16 v[240:243],a[176:179],a[240:243],v[240:243]
v_mfma_f32_16x16x32_bf16 v[240:243],a[180:183],a[244:247],v[240:243]
v_mfma_f32_16x16x32_bf16 v[248:251],a[180:183],a[252:255],v[248:251]
ds_read_b64_tr_b16 a[180:181],v22 offset:17152
v_mfma_f32_16x16x32_bf16 v[248:251],a[176:179],a[248:251],v[248:251]
ds_read_b64_tr_b16 a[176:177],v22 offset:768
v_mfma_f32_16x16x32_bf16 v[228:231],a[184:187],a[224:227],v[228:231]
buffer_load_dwordx4 v50,s[28:31],s57 offen offset:2048 lds
v_mfma_f32_16x16x32_bf16 v[228:231],a[188:191],a[228:231],v[228:231]
ds_read_b64_tr_b16 a[178:179],v22 offset:2816
v_mfma_f32_16x16x32_bf16 v[236:239],a[188:191],a[236:239],v[236:239]
ds_read_b64_tr_b16 a[182:183],v22 offset:19200
v_mfma_f32_16x16x32_bf16 v[236:239],a[184:187],a[232:235],v[236:239]
v_mfma_f32_16x16x32_bf16 v[244:247],a[184:187],a[240:243],v[244:247]
v_mfma_f32_16x16x32_bf16 v[244:247],a[188:191],a[244:247],v[244:247]
buffer_load_dwordx4 v51,s[28:31],s57 offen offset:3072 lds
v_mfma_f32_16x16x32_bf16 v[252:255],a[188:191],a[252:255],v[252:255]
ds_read_b64_tr_b16 a[188:189],v22 offset:17160
v_mfma_f32_16x16x32_bf16 v[252:255],a[184:187],a[248:251],v[252:255]
ds_read_b64_tr_b16 a[184:185],v22 offset:776
v_mfma_f32_16x16x32_bf16 a[0:3],a[128:131],a[224:227],a[0:3]
s_add_u32 m0,s59,4096
s_xor_b32 s59,s59,131072
ds_read_b64_tr_b16 a[186:187],v22 offset:2824
v_mfma_f32_16x16x32_bf16 a[0:3],a[132:135],a[228:231],a[0:3]
ds_read_b64_tr_b16 a[190:191],v22 offset:19208
v_add_u32 v22,s60,v23
v_mfma_f32_16x16x32_bf16 a[32:35],a[132:135],a[236:239],a[32:35]
v_mfma_f32_16x16x32_bf16 a[32:35],a[128:131],a[232:235],a[32:35]
buffer_load_dwordx4 v52,s[28:31],s57 offen lds
v_mfma_f32_16x16x32_bf16 a[64:67],a[128:131],a[240:243],a[64:67]
v_mfma_f32_16x16x32_bf16 a[64:67],a[132:135],a[244:247],a[64:67]
v_mfma_f32_16x16x32_bf16 a[96:99],a[132:135],a[252:255],a[96:99]
v_mfma_f32_16x16x32_bf16 a[96:99],a[128:131],a[248:251],a[96:99]
v_mfma_f32_16x16x32_bf16 a[4:7],a[136:139],a[224:227],a[4:7]
buffer_load_dwordx4 v53,s[28:31],s57 offen offset:1024 lds
v_mfma_f32_16x16x32_bf16 a[4:7],a[140:143],a[228:231],a[4:7]
v_mfma_f32_16x16x32_bf16 a[36:39],a[140:143],a[236:239],a[36:39]
v_mfma_f32_16x16x32_bf16 a[36:39],a[136:139],a[232:235],a[36:39]
v_mfma_f32_16x16x32_bf16 a[68:71],a[136:139],a[240:243],a[68:71]
v_mfma_f32_16x16x32_bf16 a[68:71],a[140:143],a[244:247],a[68:71]
v_mfma_f32_16x16x32_bf16 a[100:103],a[140:143],a[252:255],a[100:103]
buffer_load_dwordx4 v54,s[28:31],s57 offen offset:2048 lds
v_mfma_f32_16x16x32_bf16 a[100:103],a[136:139],a[248:251],a[100:103]
s_waitcnt lgkmcnt(0)
v_mfma_f32_16x16x32_bf16 a[8:11],a[144:147],a[224:227],a[8:11]
v_mfma_f32_16x16x32_bf16 a[8:11],a[148:151],a[228:231],a[8:11]
v_mfma_f32_16x16x32_bf16 a[40:43],a[148:151],a[236:239],a[40:43]
v_mfma_f32_16x16x32_bf16 a[40:43],a[144:147],a[232:235],a[40:43]
buffer_load_dwordx4 v55,s[28:31],s57 offen offset:3072 lds
v_mfma_f32_16x16x32_bf16 a[72:75],a[144:147],a[240:243],a[72:75]
s_add_u32 s57,s57,128
v_mfma_f32_16x16x32_bf16 a[72:75],a[148:151],a[244:247],a[72:75]
v_mfma_f32_16x16x32_bf16 a[104:107],a[148:151],a[252:255],a[104:107]
v_mfma_f32_16x16x32_bf16 a[104:107],a[144:147],a[248:251],a[104:107]
v_mfma_f32_16x16x32_bf16 a[12:15],a[152:155],a[224:227],a[12:15]
v_mfma_f32_16x16x32_bf16 a[12:15],a[156:159],a[228:231],a[12:15]
v_mfma_f32_16x16x32_bf16 a[44:47],a[156:159],a[236:239],a[44:47]
v_mfma_f32_16x16x32_bf16 a[44:47],a[152:155],a[232:235],a[44:47]
v_mfma_f32_16x16x32_bf16 a[76:79],a[152:155],a[240:243],a[76:79]
buffer_load_dword v26,v24,s[88:91],s93 offen
)A"
R"A(v_mfma_f32_16x16x32_bf16 a[76:79],a[156:159],a[244:247],a[76:79]
v_mfma_f32_16x16x32_bf16 a[108:111],a[156:159],a[252:255],a[108:111]
v_mfma_f32_16x16x32_bf16 a[108:111],a[152:155],a[248:251],a[108:111]
s_waitcnt vmcnt(16) lgkmcnt(0)
v_mfma_f32_16x16x32_bf16 a[16:19],a[160:163],a[224:227],a[16:19]
s_barrier
ds_read_b128 a[192:195],v20
v_mfma_f32_16x16x32_bf16 a[16:19],a[164:167],a[228:231],a[16:19]
ds_read_b128 a[196:199],v21
v_mfma_f32_16x16x32_bf16 a[48:51],a[164:167],a[236:239],a[48:51]
ds_read_b128 a[200:203],v20 offset:2048
v_mfma_f32_16x16x32_bf16 a[48:51],a[160:163],a[232:235],a[48:51]
ds_read_b128 a[204:207],v21 offset:2048
v_mfma_f32_16x16x32_bf16 a[80:83],a[160:163],a[240:243],a[80:83]
ds_read_b128 a[208:211],v20 offset:4096
v_mfma_f32_16x16x32_bf16 a[80:83],a[164:167],a[244:247],a[80:83]
ds_read_b128 a[212:215],v21 offset:4096
v_mfma_f32_16x16x32_bf16 a[112:115],a[164:167],a[252:255],a[112:115]
ds_read_b128 a[216:219],v20 offset:6144
v_mfma_f32_16x16x32_bf16 a[112:115],a[160:163],a[248:251],a[112:115]
ds_read_b128 a[220:223],v21 offset:6144
v_mfma_f32_16x16x32_bf16 a[20:23],a[168:171],a[224:227],a[20:23]
ds_read_b64_tr_b16 a[128:129],v22 offset:1024
v_mfma_f32_16x16x32_bf16 a[20:23],a[172:175],a[228:231],a[20:23]
ds_read_b64_tr_b16 a[130:131],v22 offset:3072
v_mfma_f32_16x16x32_bf16 a[52:55],a[172:175],a[236:239],a[52:55]
ds_read_b64_tr_b16 a[132:133],v22 offset:17408
v_mfma_f32_16x16x32_bf16 a[52:55],a[168:171],a[232:235],a[52:55]
ds_read_b64_tr_b16 a[134:135],v22 offset:19456
v_mfma_f32_16x16x32_bf16 a[84:87],a[168:171],a[240:243],a[84:87]
ds_read_b64_tr_b16 a[136:137],v22 offset:1032
v_mfma_f32_16x16x32_bf16 a[84:87],a[172:175],a[244:247],a[84:87]
ds_read_b64_tr_b16 a[138:139],v22 offset:3080
v_mfma_f32_16x16x32_bf16 a[116:119],a[172:175],a[252:255],a[116:119]
ds_read_b64_tr_b16 a[140:141],v22 offset:17416
v_mfma_f32_16x16x32_bf16 a[116:119],a[168:171],a[248:251],a[116:119]
ds_read_b64_tr_b16 a[142:143],v22 offset:19464
v_mfma_f32_16x16x32_bf16 a[24:27],a[176:179],a[224:227],a[24:27]
ds_read_b64_tr_b16 a[144:145],v22 offset:1280
v_mfma_f32_16x16x32_bf16 a[24:27],a[180:183],a[228:231],a[24:27]
ds_read_b64_tr_b16 a[146:147],v22 offset:3328
v_mfma_f32_16x16x32_bf16 a[56:59],a[180:183],a[236:239],a[56:59]
ds_read_b64_tr_b16 a[148:149],v22 offset:17664
v_mfma_f32_16x16x32_bf16 a[56:59],a[176:179],a[232:235],a[56:59]
ds_read_b64_tr_b16 a[150:151],v22 offset:19712
v_mfma_f32_16x16x32_bf16 a[88:91],a[176:179],a[240:243],a[88:91]
ds_read_b64_tr_b16 a[152:153],v22 offset:1288
v_mfma_f32_16x16x32_bf16 a[88:91],a[180:183],a[244:247],a[88:91]
ds_read_b64_tr_b16 a[154:155],v22 offset:3336
v_mfma_f32_16x16x32_bf16 a[120:123],a[180:183],a[252:255],a[120:123]
ds_read_b64_tr_b16 a[156:157],v22 offset:17672
v_mfma_f32_16x16x32_bf16 a[120:123],a[176:179],a[248:251],a[120:123]
ds_read_b64_tr_b16 a[158:159],v22 offset:19720
v_mfma_f32_16x16x32_bf16 a[28:31],a[184:187],a[224:227],a[28:31]
v_mfma_f32_16x16x32_bf16 a[28:31],a[188:191],a[228:231],a[28:31]
v_mfma_f32_16x16x32_bf16 a[60:63],a[188:191],a[236:239],a[60:63]
v_mfma_f32_16x16x32_bf16 a[60:63],a[184:187],a[232:235],a[60:63]
v_mfma_f32_16x16x32_bf16 a[92:95],a[184:187],a[240:243],a[92:95]
v_mfma_f32_16x16x32_bf16 a[92:95],a[188:191],a[244:247],a[92:95]
v_mfma_f32_16x16x32_bf16 a[124:127],a[188:191],a[252:255],a[124:127]
s_cmp_lg_u32 s53,0
v_mfma_f32_16x16x32_bf16 a[124:127],a[184:187],a[248:251],a[124:127]
s_cbranch_scc1 L_qp_%=
L_tailseg_%=:
s_waitcnt lgkmcnt(0)
v_mfma_f32_16x16x32_bf16 v[128:131],a[128:131],a[192:195],v[128:131]
s_add_u32 m0,98304,s51
ds_read_b64_tr_b16 a[160:161],v22 offset:1536
v_xor_b32 v20,131072,v20
v_mfma_f32_16x16x32_bf16 v[128:131],a[132:135],a[196:199],v[128:131]
s_add_u32 s54,s54,16384
buffer_load_dwordx4 v4,s[20:23],s47 offen nt lds
v_mfma_f32_16x16x32_bf16 v[136:139],a[132:135],a[204:207],v[136:139]
s_and_b32 s46,s54,0xfffc0000
ds_read_b64_tr_b16 a[162:163],v22 offset:3584
v_xor_b32 v21,131072,v21
v_mfma_f32_16x16x32_bf16 v[136:139],a[128:131],a[200:203],v[136:139]
s_add_u32 s54,s54,16384
ds_read_b64_tr_b16 a[164:165],v22 offset:17920
v_and_b32 v18,48,v6
v_mfma_f32_16x16x32_bf16 v[144:147],a[128:131],a[208:211],v[144:147]
s_add_u32 s49,s49,32768
ds_read_b64_tr_b16 a[166:167],v22 offset:19968
v_lshrrev_b32 v19,4,v6
v_mfma_f32_16x16x32_bf16 v[144:147],a[132:135],a[212:215],v[144:147]
s_mov_b32 s60,138
ds_read_b64_tr_b16 a[168:169],v22 offset:1544
v_lshlrev_b32 v18,2,v18
v_mfma_f32_16x16x32_bf16 v[152:155],a[132:135],a[220:223],v[152:155]
s_add_u32 s93,s92,L_att0_%=-L_pc_%=
ds_read_b64_tr_b16 a[170:171],v22 offset:3592
v_and_b32 v19,48,v19
v_mfma_f32_16x16x32_bf16 v[152:155],a[128:131],a[216:219],v[152:155]
ds_read_b64_tr_b16 a[172:173],v22 offset:17928
v_add_u32 v18,v18,v19
v_mfma_f32_16x16x32_bf16 v[132:135],a[136:139],a[192:195],v[132:135]
ds_read_b64_tr_b16 a[174:175],v22 offset:19976
v_add_u32 v18,98304,v18
v_mfma_f32_16x16x32_bf16 v[132:135],a[140:143],a[196:199],v[132:135]
buffer_load_dwordx4 v4,s[20:23],s47 offen offset:1024 nt lds
v_mfma_f32_16x16x32_bf16 v[140:143],a[140:143],a[204:207],v[140:143]
s_cmp_eq_u32 s49,163840
s_cselect_b32 s49,0,s49
ds_read_b64_tr_b16 a[176:177],v22 offset:1792
v_mfma_f32_16x16x32_bf16 v[140:143],a[136:139],a[200:203],v[140:143]
s_cmp_eq_u32 s50,131072
s_cselect_b32 s62,163840,0
ds_read_b64_tr_b16 a[178:179],v22 offset:3840
v_mfma_f32_16x16x32_bf16 v[148:151],a[136:139],a[208:211],v[148:151]
s_sub_u32 s48,32768,s62
s_add_u32 s50,s50,s48
ds_read_b64_tr_b16 a[180:181],v22 offset:18176
v_mfma_f32_16x16x32_bf16 v[148:151],a[140:143],a[212:215],v[148:151]
s_cmp_eq_u32 s50,131072
s_cselect_b32 s62,163840,0
ds_read_b64_tr_b16 a[182:183],v22 offset:20224
v_mfma_f32_16x16x32_bf16 v[156:159],a[140:143],a[220:223],v[156:159]
ds_read_b64_tr_b16 a[184:185],v22 offset:1800
v_mfma_f32_16x16x32_bf16 v[156:159],a[136:139],a[216:219],v[156:159]
ds_read_b64_tr_b16 a[186:187],v22 offset:3848
v_mfma_f32_16x16x32_bf16 v[160:163],a[144:147],a[192:195],v[160:163]
ds_read_b64_tr_b16 a[188:189],v22 offset:18184
v_mfma_f32_16x16x32_bf16 v[160:163],a[148:151],a[196:199],v[160:163]
buffer_load_dwordx4 v5,s[20:23],s47 offen offset:2048 nt lds
v_mfma_f32_16x16x32_bf16 v[168:171],a[148:151],a[204:207],v[168:171]
s_waitcnt vmcnt(4)
ds_read_b128 a[224:227],v20
v_mfma_f32_16x16x32_bf16 v[168:171],a[144:147],a[200:203],v[168:171]
ds_read_b64_tr_b16 a[190:191],v22 offset:20232
v_mfma_f32_16x16x32_bf16 v[176:179],a[144:147],a[208:211],v[176:179]
ds_read_b64_tr_b16 a[128:129],v22
v_mfma_f32_16x16x32_bf16 v[176:179],a[148:151],a[212:215],v[176:179]
ds_read_b64_tr_b16 a[130:131],v22 offset:2048
v_mfma_f32_16x16x32_bf16 v[184:187],a[148:151],a[220:223],v[184:187]
ds_read_b64_tr_b16 a[132:133],v22 offset:16384
v_mfma_f32_16x16x32_bf16 v[184:187],a[144:147],a[216:219],v[184:187]
ds_read_b64_tr_b16 a[134:135],v22 offset:18432
v_mfma_f32_16x16x32_bf16 v[164:167],a[152:155],a[192:195],v[164:167]
ds_read_b64_tr_b16 a[144:145],v22 offset:8
v_mfma_f32_16x16x32_bf16 v[164:167],a[156:159],a[196:199],v[164:167]
buffer_load_dwordx4 v5,s[20:23],s47 offen offset:3072 nt lds
v_mfma_f32_16x16x32_bf16 v[172:175],a[156:159],a[204:207],v[172:175]
ds_read_b128 a[228:231],v21
v_mfma_f32_16x16x32_bf16 v[172:175],a[152:155],a[200:203],v[172:175]
ds_read_b64_tr_b16 a[146:147],v22 offset:2056
v_mfma_f32_16x16x32_bf16 v[180:183],a[152:155],a[208:211],v[180:183]
ds_read_b64_tr_b16 a[148:149],v22 offset:16392
v_mfma_f32_16x16x32_bf16 v[180:183],a[156:159],a[212:215],v[180:183]
ds_read_b64_tr_b16 a[150:151],v22 offset:18440
v_mfma_f32_16x16x32_bf16 v[188:191],a[156:159],a[220:223],v[188:191]
s_add_u32 m0,98304,s52
v_readfirstlane_b32 s68,v13
s_nop 1
s_add_u32 s68,s68,1
v_mfma_f32_16x16x32_bf16 v[188:191],a[152:155],a[216:219],v[188:191]
s_xor_b32 s69,s68,0x9e3779b9
ds_read_b128 a[232:235],v20 offset:2048
s_waitcnt lgkmcnt(6)
v_mfma_f32_16x16x32_bf16 v[192:195],a[160:163],a[192:195],v[192:195]
ds_read_b128 a[236:239],v21 offset:2048
ds_read_b128 a[240:243],v20 offset:4096
ds_read_b128 a[244:247],v21 offset:4096
ds_read_b128 a[248:251],v20 offset:6144
ds_read_b128 a[252:255],v21 offset:6144
v_mfma_f32_16x16x32_bf16 v[192:195],a[164:167],a[196:199],v[192:195]
v_mfma_f32_16x16x32_bf16 v[200:203],a[164:167],a[204:207],v[200:203]
v_mfma_f32_16x16x32_bf16 v[200:203],a[160:163],a[200:203],v[200:203]
v_mfma_f32_16x16x32_bf16 v[208:211],a[160:163],a[208:211],v[208:211]
buffer_load_dwordx4 v4,s[24:27],s47 offen nt lds
v_mfma_f32_16x16x32_bf16 v[208:211],a[164:167],a[212:215],v[208:211]
v_mfma_f32_16x16x32_bf16 v[216:219],a[164:167],a[220:223],v[216:219]
s_waitcnt lgkmcnt(0)
v_mfma_f32_16x16x32_bf16 v[216:219],a[160:163],a[216:219],v[216:219]
s_barrier
ds_read_b64_tr_b16 a[136:137],v22 offset:32768
v_mfma_f32_16x16x32_bf16 v[196:199],a[168:171],a[192:195],v[196:199]
buffer_load_dwordx4 v5,s[24:27],s47 offen offset:1024 nt lds
v_mfma_f32_16x16x32_bf16 v[196:199],a[172:175],a[196:199],v[196:199]
ds_read_b64_tr_b16 a[138:139],v22 offset:34816
v_mfma_f32_16x16x32_bf16 v[204:207],a[172:175],a[204:207],v[204:207]
ds_read_b64_tr_b16 a[140:141],v22 offset:49152
v_mfma_f32_16x16x32_bf16 v[204:207],a[168:171],a[200:203],v[204:207]
ds_read_b64_tr_b16 a[142:143],v22 offset:51200
v_mfma_f32_16x16x32_bf16 v[212:215],a[168:171],a[208:211],v[212:215]
ds_read_b64_tr_b16 a[152:153],v22 offset:32776
v_mfma_f32_16x16x32_bf16 v[212:215],a[172:175],a[212:215],v[212:215]
ds_read_b64_tr_b16 a[154:155],v22 offset:34824
v_mfma_f32_16x16x32_bf16 v[220:223],a[172:175],a[220:223],v[220:223]
ds_read_b64_tr_b16 a[156:157],v22 offset:49160
v_mfma_f32_16x16x32_bf16 v[220:223],a[168:171],a[216:219],v[220:223]
ds_read_b64_tr_b16 a[158:159],v22 offset:51208
v_mfma_f32_16x16x32_bf16 v[224:227],a[176:179],a[192:195],v[224:227]
buffer_load_dwordx4 v4,s[24:27],s47 offen offset:2048 nt lds
v_mfma_f32_16x16x32_bf16 v[224:227],a[180:183],a[196:199],v[224:227]
ds_read_b64_tr_b16 a[160:161],v22 offset:256
v_mfma_f32_16x16x32_bf16 v[232:235],a[180:183],a[204:207],v[232:235]
ds_read_b64_tr_b16 a[162:163],v22 offset:2304
v_mfma_f32_16x16x32_bf16 v[232:235],a[176:179],a[200:203],v[232:235]
ds_read_b64_tr_b16 a[164:165],v22 offset:16640
v_mfma_f32_16x16x32_bf16 v[240:243],a[176:179],a[208:211],v[240:243]
ds_read_b64_tr_b16 a[166:167],v22 offset:18688
v_mfma_f32_16x16x32_bf16 v[240:243],a[180:183],a[212:215],v[240:243]
ds_read_b64_tr_b16 a[168:169],v22 offset:33024
v_mfma_f32_16x16x32_bf16 v[248:251],a[180:183],a[220:223],v[248:251]
ds_read_b64_tr_b16 a[170:171],v22 offset:35072
v_mfma_f32_16x16x32_bf16 v[248:251],a[176:179],a[216:219],v[248:251]
ds_read_b64_tr_b16 a[172:173],v22 offset:49408
v_mfma_f32_16x16x32_bf16 v[228:231],a[184:187],a[192:195],v[228:231]
buffer_load_dwordx4 v5,s[24:27],s47 offen offset:3072 nt lds
v_mfma_f32_16x16x32_bf16 v[228:231],a[188:191],a[196:199],v[228:231]
s_add_u32 s47,s47,16384
s_and_b32 s47,s47,0x3ffff
ds_read_b64_tr_b16 a[174:175],v22 offset:51456
v_mfma_f32_16x16x32_bf16 v[236:239],a[188:191],a[204:207],v[236:239]
s_or_b32 s47,s47,s46
s_and_b32 s46,s54,0xfffc0000
ds_read_b64_tr_b16 a[176:177],v22 offset:264
v_mfma_f32_16x16x32_bf16 v[236:239],a[184:187],a[200:203],v[236:239]
s_add_u32 s54,s54,16384
ds_read_b64_tr_b16 a[178:179],v22 offset:2312
v_mfma_f32_16x16x32_bf16 v[244:247],a[184:187],a[208:211],v[244:247]
ds_read_b64_tr_b16 a[180:181],v22 offset:16648
v_mfma_f32_16x16x32_bf16 v[244:247],a[188:191],a[212:215],v[244:247]
ds_read_b64_tr_b16 a[182:183],v22 offset:18696
v_mfma_f32_16x16x32_bf16 v[252:255],a[188:191],a[220:223],v[252:255]
ds_read_b64_tr_b16 a[188:189],v22 offset:49416
v_mfma_f32_16x16x32_bf16 v[252:255],a[184:187],a[216:219],v[252:255]
ds_read_b64_tr_b16 a[184:185],v22 offset:33032
v_mfma_f32_16x16x32_bf16 a[0:3],a[128:131],a[192:195],a[0:3]
ds_read_b64_tr_b16 a[186:187],v22 offset:35080
v_mfma_f32_16x16x32_bf16 a[0:3],a[132:135],a[196:199],a[0:3]
s_waitcnt lgkmcnt(7)
ds_read_b64_tr_b16 a[190:191],v22 offset:51464
v_mfma_f32_16x16x32_bf16 a[0:3],a[136:139],a[224:227],a[0:3]
v_mfma_f32_16x16x32_bf16 a[0:3],a[140:143],a[228:231],a[0:3]
v_mfma_f32_16x16x32_bf16 a[32:35],a[140:143],a[236:239],a[32:35]
)A"
R"A(v_mfma_f32_16x16x32_bf16 a[32:35],a[136:139],a[232:235],a[32:35]
v_mfma_f32_16x16x32_bf16 a[32:35],a[132:135],a[204:207],a[32:35]
v_mfma_f32_16x16x32_bf16 a[32:35],a[128:131],a[200:203],a[32:35]
v_mfma_f32_16x16x32_bf16 a[64:67],a[128:131],a[208:211],a[64:67]
v_mfma_f32_16x16x32_bf16 a[64:67],a[132:135],a[212:215],a[64:67]
s_nop 1
v_accvgpr_read_b32 v64,a0
v_accvgpr_read_b32 v65,a1
v_mfma_f32_16x16x32_bf16 a[64:67],a[136:139],a[240:243],a[64:67]
v_mul_f32 v64,s61,v64
v_mul_f32 v65,s61,v65
v_mfma_f32_16x16x32_bf16 a[64:67],a[140:143],a[244:247],a[64:67]
v_accvgpr_read_b32 v66,a2
v_accvgpr_read_b32 v67,a3
v_mfma_f32_16x16x32_bf16 a[96:99],a[140:143],a[252:255],a[96:99]
ds_read_b64_tr_b16 a[140:141],v22 offset:49664
v_cvt_pk_bf16_f32 v64,v64,v65
v_mfma_f32_16x16x32_bf16 a[96:99],a[136:139],a[248:251],a[96:99]
ds_read_b64_tr_b16 a[136:137],v22 offset:33280
v_mul_f32 v66,s61,v66
v_mfma_f32_16x16x32_bf16 a[96:99],a[132:135],a[220:223],a[96:99]
ds_read_b64_tr_b16 a[132:133],v22 offset:16896
v_mul_f32 v67,s61,v67
v_mfma_f32_16x16x32_bf16 a[96:99],a[128:131],a[216:219],a[96:99]
ds_read_b64_tr_b16 a[128:129],v22 offset:512
v_cvt_pk_bf16_f32 v65,v66,v67
v_mfma_f32_16x16x32_bf16 a[4:7],a[144:147],a[192:195],a[4:7]
ds_read_b64_tr_b16 a[130:131],v22 offset:2560
v_accvgpr_read_b32 v80,a32
v_mfma_f32_16x16x32_bf16 a[4:7],a[148:151],a[196:199],a[4:7]
ds_read_b64_tr_b16 a[134:135],v22 offset:18944
v_accvgpr_read_b32 v96,a64
v_mfma_f32_16x16x32_bf16 a[4:7],a[152:155],a[224:227],a[4:7]
ds_read_b64_tr_b16 a[138:139],v22 offset:35328
v_accvgpr_read_b32 v97,a65
v_mfma_f32_16x16x32_bf16 a[4:7],a[156:159],a[228:231],a[4:7]
ds_read_b64_tr_b16 a[142:143],v22 offset:51712
v_accvgpr_read_b32 v81,a33
v_mfma_f32_16x16x32_bf16 a[36:39],a[156:159],a[236:239],a[36:39]
v_mul_f32 v96,s61,v96
v_mul_f32 v97,s61,v97
v_mfma_f32_16x16x32_bf16 a[36:39],a[152:155],a[232:235],a[36:39]
v_accvgpr_read_b32 v112,a96
v_accvgpr_read_b32 v113,a97
v_mfma_f32_16x16x32_bf16 a[36:39],a[148:151],a[204:207],a[36:39]
s_add_u32 m0,131072,s51
v_mul_f32 v112,s61,v112
v_mul_f32 v113,s61,v113
v_mfma_f32_16x16x32_bf16 a[36:39],a[144:147],a[200:203],a[36:39]
v_accvgpr_read_b32 v114,a98
v_accvgpr_read_b32 v115,a99
v_mfma_f32_16x16x32_bf16 a[68:71],a[144:147],a[208:211],a[68:71]
v_accvgpr_read_b32 v98,a66
v_accvgpr_read_b32 v99,a67
v_mfma_f32_16x16x32_bf16 a[68:71],a[148:151],a[212:215],a[68:71]
buffer_load_dwordx4 v4,s[20:23],s47 offen nt lds
v_mfma_f32_16x16x32_bf16 a[68:71],a[152:155],a[240:243],a[68:71]
v_accvgpr_read_b32 v66,a4
v_accvgpr_read_b32 v67,a5
v_mfma_f32_16x16x32_bf16 a[68:71],a[156:159],a[244:247],a[68:71]
v_accvgpr_read_b32 v12,a6
v_accvgpr_read_b32 v13,a7
v_mfma_f32_16x16x32_bf16 a[100:103],a[156:159],a[252:255],a[100:103]
ds_read_b64_tr_b16 a[156:157],v22 offset:49672
v_mul_f32 v66,s61,v66
v_mfma_f32_16x16x32_bf16 a[100:103],a[152:155],a[248:251],a[100:103]
ds_read_b64_tr_b16 a[152:153],v22 offset:33288
v_mul_f32 v67,s61,v67
v_mfma_f32_16x16x32_bf16 a[100:103],a[148:151],a[220:223],a[100:103]
ds_read_b64_tr_b16 a[148:149],v22 offset:16904
v_cvt_pk_bf16_f32 v112,v112,v113
v_mfma_f32_16x16x32_bf16 a[100:103],a[144:147],a[216:219],a[100:103]
ds_read_b64_tr_b16 a[144:145],v22 offset:520
v_mul_f32 v114,s61,v114
v_mfma_f32_16x16x32_bf16 a[8:11],a[160:163],a[192:195],a[8:11]
ds_read_b64_tr_b16 a[146:147],v22 offset:2568
v_mul_f32 v115,s61,v115
v_mfma_f32_16x16x32_bf16 a[8:11],a[164:167],a[196:199],a[8:11]
ds_read_b64_tr_b16 a[150:151],v22 offset:18952
v_mul_f32 v12,s61,v12
v_mfma_f32_16x16x32_bf16 a[8:11],a[168:171],a[224:227],a[8:11]
ds_read_b64_tr_b16 a[154:155],v22 offset:35336
v_mul_f32 v13,s61,v13
v_mfma_f32_16x16x32_bf16 a[8:11],a[172:175],a[228:231],a[8:11]
ds_read_b64_tr_b16 a[158:159],v22 offset:51720
v_cvt_pk_bf16_f32 v66,v66,v67
v_mfma_f32_16x16x32_bf16 a[40:43],a[172:175],a[236:239],a[40:43]
buffer_load_dwordx4 v4,s[20:23],s47 offen offset:1024 nt lds
v_mfma_f32_16x16x32_bf16 a[40:43],a[168:171],a[232:235],a[40:43]
v_mul_f32 v80,s61,v80
v_mul_f32 v81,s61,v81
v_mfma_f32_16x16x32_bf16 a[40:43],a[164:167],a[204:207],a[40:43]
v_accvgpr_read_b32 v82,a34
v_accvgpr_read_b32 v83,a35
v_mfma_f32_16x16x32_bf16 a[40:43],a[160:163],a[200:203],a[40:43]
v_cvt_pk_bf16_f32 v96,v96,v97
v_mul_f32 v98,s61,v98
v_mfma_f32_16x16x32_bf16 a[72:75],a[160:163],a[208:211],a[72:75]
v_mul_f32 v99,s61,v99
v_cvt_pk_bf16_f32 v113,v114,v115
v_mfma_f32_16x16x32_bf16 a[72:75],a[164:167],a[212:215],a[72:75]
v_cvt_pk_bf16_f32 v67,v12,v13
v_cvt_pk_bf16_f32 v80,v80,v81
v_mfma_f32_16x16x32_bf16 a[72:75],a[168:171],a[240:243],a[72:75]
v_mul_f32 v82,s61,v82
v_mul_f32 v83,s61,v83
v_mfma_f32_16x16x32_bf16 a[72:75],a[172:175],a[244:247],a[72:75]
v_cvt_pk_bf16_f32 v97,v98,v99
v_accvgpr_read_b32 v114,a100
v_mfma_f32_16x16x32_bf16 a[104:107],a[172:175],a[252:255],a[104:107]
ds_read_b64_tr_b16 a[172:173],v22 offset:49920
v_accvgpr_read_b32 v115,a101
v_mfma_f32_16x16x32_bf16 a[104:107],a[168:171],a[248:251],a[104:107]
ds_read_b64_tr_b16 a[168:169],v22 offset:33536
v_accvgpr_read_b32 v84,a40
v_mfma_f32_16x16x32_bf16 a[104:107],a[164:167],a[220:223],a[104:107]
ds_read_b64_tr_b16 a[164:165],v22 offset:17152
v_accvgpr_read_b32 v85,a41
v_mfma_f32_16x16x32_bf16 a[104:107],a[160:163],a[216:219],a[104:107]
s_waitcnt lgkmcnt(3)
buffer_load_dwordx4 v5,s[20:23],s47 offen offset:2048 nt lds
v_mfma_f32_16x16x32_bf16 a[12:15],a[176:179],a[192:195],a[12:15]
ds_read_b64_tr_b16 a[160:161],v22 offset:768
v_cvt_pk_bf16_f32 v81,v82,v83
v_mfma_f32_16x16x32_bf16 a[12:15],a[180:183],a[196:199],a[12:15]
ds_read_b64_tr_b16 a[162:163],v22 offset:2816
v_accvgpr_read_b32 v100,a72
v_mfma_f32_16x16x32_bf16 a[12:15],a[184:187],a[224:227],a[12:15]
ds_read_b64_tr_b16 a[166:167],v22 offset:19200
v_accvgpr_read_b32 v101,a73
v_mfma_f32_16x16x32_bf16 a[12:15],a[188:191],a[228:231],a[12:15]
ds_read_b64_tr_b16 a[170:171],v22 offset:35584
v_mul_f32 v100,s61,v100
v_mfma_f32_16x16x32_bf16 a[44:47],a[188:191],a[236:239],a[44:47]
ds_read_b64_tr_b16 a[174:175],v22 offset:51968
v_mul_f32 v101,s61,v101
v_mfma_f32_16x16x32_bf16 a[44:47],a[184:187],a[232:235],a[44:47]
v_accvgpr_read_b32 v102,a74
v_accvgpr_read_b32 v103,a75
v_mfma_f32_16x16x32_bf16 a[44:47],a[180:183],a[204:207],a[44:47]
v_accvgpr_read_b32 v98,a68
v_accvgpr_read_b32 v99,a69
v_mfma_f32_16x16x32_bf16 a[44:47],a[176:179],a[200:203],a[44:47]
v_accvgpr_read_b32 v12,a102
v_accvgpr_read_b32 v13,a103
v_mfma_f32_16x16x32_bf16 a[76:79],a[176:179],a[208:211],a[76:79]
v_mul_f32 v114,s61,v114
v_mul_f32 v115,s61,v115
v_mfma_f32_16x16x32_bf16 a[76:79],a[180:183],a[212:215],a[76:79]
v_accvgpr_read_b32 v68,a8
v_accvgpr_read_b32 v69,a9
v_mfma_f32_16x16x32_bf16 a[76:79],a[184:187],a[240:243],a[76:79]
buffer_load_dwordx4 v5,s[20:23],s47 offen offset:3072 nt lds
v_mfma_f32_16x16x32_bf16 a[76:79],a[188:191],a[244:247],a[76:79]
v_mul_f32 v84,s61,v84
v_mul_f32 v85,s61,v85
v_mfma_f32_16x16x32_bf16 a[108:111],a[188:191],a[252:255],a[108:111]
ds_read_b64_tr_b16 a[188:189],v22 offset:49928
v_accvgpr_read_b32 v86,a42
v_mfma_f32_16x16x32_bf16 a[108:111],a[184:187],a[248:251],a[108:111]
ds_read_b64_tr_b16 a[184:185],v22 offset:33544
v_accvgpr_read_b32 v87,a43
v_mfma_f32_16x16x32_bf16 a[108:111],a[180:183],a[220:223],a[108:111]
ds_read_b64_tr_b16 a[180:181],v22 offset:17160
v_cvt_pk_bf16_f32 v100,v100,v101
v_mfma_f32_16x16x32_bf16 a[108:111],a[176:179],a[216:219],a[108:111]
ds_read_b64_tr_b16 a[176:177],v22 offset:776
v_mul_f32 v102,s61,v102
v_mfma_f32_16x16x32_bf16 a[16:19],a[128:131],a[192:195],a[16:19]
ds_read_b64_tr_b16 a[178:179],v22 offset:2824
v_mul_f32 v103,s61,v103
v_mfma_f32_16x16x32_bf16 a[16:19],a[132:135],a[196:199],a[16:19]
ds_read_b64_tr_b16 a[182:183],v22 offset:19208
v_accvgpr_read_b32 v82,a36
v_mfma_f32_16x16x32_bf16 a[16:19],a[136:139],a[224:227],a[16:19]
s_add_u32 m0,131072,s52
ds_read_b64_tr_b16 a[186:187],v22 offset:35592
v_accvgpr_read_b32 v83,a37
v_mfma_f32_16x16x32_bf16 a[16:19],a[140:143],a[228:231],a[16:19]
ds_read_b64_tr_b16 a[190:191],v22 offset:51976
v_accvgpr_read_b32 v16,a70
v_mfma_f32_16x16x32_bf16 a[48:51],a[140:143],a[236:239],a[48:51]
v_accvgpr_read_b32 v17,a71
v_mul_f32 v98,s61,v98
v_mfma_f32_16x16x32_bf16 a[48:51],a[136:139],a[232:235],a[48:51]
buffer_load_dwordx4 v4,s[24:27],s47 offen nt lds
v_mfma_f32_16x16x32_bf16 a[48:51],a[132:135],a[204:207],a[48:51]
v_mul_f32 v99,s61,v99
v_mul_f32 v12,s61,v12
v_mfma_f32_16x16x32_bf16 a[48:51],a[128:131],a[200:203],a[48:51]
v_mul_f32 v13,s61,v13
v_cvt_pk_bf16_f32 v114,v114,v115
v_mfma_f32_16x16x32_bf16 a[80:83],a[128:131],a[208:211],a[80:83]
v_mul_f32 v68,s61,v68
v_mul_f32 v69,s61,v69
v_mfma_f32_16x16x32_bf16 a[80:83],a[132:135],a[212:215],a[80:83]
v_accvgpr_read_b32 v70,a10
v_accvgpr_read_b32 v71,a11
v_mfma_f32_16x16x32_bf16 a[80:83],a[136:139],a[240:243],a[80:83]
v_cvt_pk_bf16_f32 v84,v84,v85
v_mul_f32 v86,s61,v86
v_mfma_f32_16x16x32_bf16 a[80:83],a[140:143],a[244:247],a[80:83]
v_mul_f32 v87,s61,v87
v_cvt_pk_bf16_f32 v101,v102,v103
v_mfma_f32_16x16x32_bf16 a[112:115],a[140:143],a[252:255],a[112:115]
ds_read_b64_tr_b16 a[140:141],v22 offset:50184
v_accvgpr_read_b32 v14,a38
v_mfma_f32_16x16x32_bf16 a[112:115],a[136:139],a[248:251],a[112:115]
ds_read_b64_tr_b16 a[136:137],v22 offset:33800
v_accvgpr_read_b32 v88,a48
v_mfma_f32_16x16x32_bf16 a[112:115],a[132:135],a[220:223],a[112:115]
ds_read_b64_tr_b16 a[132:133],v22 offset:50176
v_accvgpr_read_b32 v89,a49
v_mfma_f32_16x16x32_bf16 a[112:115],a[128:131],a[216:219],a[112:115]
ds_read_b64_tr_b16 a[128:129],v22 offset:33792
v_accvgpr_read_b32 v15,a39
v_mfma_f32_16x16x32_bf16 a[20:23],a[144:147],a[192:195],a[20:23]
buffer_load_dwordx4 v5,s[24:27],s47 offen offset:1024 nt lds
v_mfma_f32_16x16x32_bf16 a[20:23],a[148:151],a[196:199],a[20:23]
ds_read_b64_tr_b16 a[130:131],v22 offset:35840
v_mul_f32 v82,s61,v82
v_mfma_f32_16x16x32_bf16 a[20:23],a[152:155],a[224:227],a[20:23]
ds_read_b64_tr_b16 a[134:135],v22 offset:52224
v_mul_f32 v83,s61,v83
v_mfma_f32_16x16x32_bf16 a[20:23],a[156:159],a[228:231],a[20:23]
ds_read_b64_tr_b16 a[138:139],v22 offset:35848
v_mul_f32 v16,s61,v16
v_mfma_f32_16x16x32_bf16 a[52:55],a[156:159],a[236:239],a[52:55]
ds_read_b64_tr_b16 a[142:143],v22 offset:52232
v_mul_f32 v17,s61,v17
v_mfma_f32_16x16x32_bf16 a[52:55],a[152:155],a[232:235],a[52:55]
v_cvt_pk_bf16_f32 v98,v98,v99
v_cvt_pk_bf16_f32 v115,v12,v13
v_mfma_f32_16x16x32_bf16 a[52:55],a[148:151],a[204:207],a[52:55]
v_cvt_pk_bf16_f32 v68,v68,v69
v_mul_f32 v70,s61,v70
v_mfma_f32_16x16x32_bf16 a[52:55],a[144:147],a[200:203],a[52:55]
v_mul_f32 v71,s61,v71
v_cvt_pk_bf16_f32 v85,v86,v87
v_mfma_f32_16x16x32_bf16 a[84:87],a[144:147],a[208:211],a[84:87]
v_accvgpr_read_b32 v102,a76
v_accvgpr_read_b32 v103,a77
v_mfma_f32_16x16x32_bf16 a[84:87],a[148:151],a[212:215],a[84:87]
v_accvgpr_read_b32 v72,a16
v_accvgpr_read_b32 v73,a17
v_mfma_f32_16x16x32_bf16 a[84:87],a[152:155],a[240:243],a[84:87]
v_mul_f32 v88,s61,v88
v_mul_f32 v89,s61,v89
v_mfma_f32_16x16x32_bf16 a[84:87],a[156:159],a[244:247],a[84:87]
buffer_load_dwordx4 v4,s[24:27],s47 offen offset:2048 nt lds
v_mfma_f32_16x16x32_bf16 a[116:119],a[156:159],a[252:255],a[116:119]
ds_read_b64_tr_b16 a[156:157],v22 offset:50440
v_accvgpr_read_b32 v90,a50
v_mfma_f32_16x16x32_bf16 a[116:119],a[152:155],a[248:251],a[116:119]
ds_read_b64_tr_b16 a[152:153],v22 offset:34056
v_accvgpr_read_b32 v91,a51
v_mfma_f32_16x16x32_bf16 a[116:119],a[148:151],a[220:223],a[116:119]
ds_read_b64_tr_b16 a[148:149],v22 offset:50432
v_mul_f32 v14,s61,v14
v_mfma_f32_16x16x32_bf16 a[116:119],a[144:147],a[216:219],a[116:119]
s_waitcnt lgkmcnt(3)
ds_read_b64_tr_b16 a[144:145],v22 offset:34048
v_mul_f32 v15,s61,v15
v_mfma_f32_16x16x32_bf16 a[24:27],a[160:163],a[192:195],a[24:27]
ds_read_b64_tr_b16 a[146:147],v22 offset:36096
v_cvt_pk_bf16_f32 v82,v82,v83
v_mfma_f32_16x16x32_bf16 a[24:27],a[164:167],a[196:199],a[24:27]
ds_read_b64_tr_b16 a[150:151],v22 offset:52480
v_cvt_pk_bf16_f32 v99,v16,v17
v_mfma_f32_16x16x32_bf16 a[24:27],a[168:171],a[224:227],a[24:27]
ds_read_b64_tr_b16 a[154:155],v22 offset:36104
v_cvt_pk_bf16_f32 v69,v70,v71
v_mfma_f32_16x16x32_bf16 a[24:27],a[172:175],a[228:231],a[24:27]
)A"
R"A(ds_read_b64_tr_b16 a[158:159],v22 offset:52488
v_accvgpr_read_b32 v86,a44
v_mfma_f32_16x16x32_bf16 a[56:59],a[172:175],a[236:239],a[56:59]
v_accvgpr_read_b32 v87,a45
v_accvgpr_read_b32 v12,a78
v_mfma_f32_16x16x32_bf16 a[56:59],a[168:171],a[232:235],a[56:59]
v_accvgpr_read_b32 v13,a79
v_mul_f32 v102,s61,v102
v_mfma_f32_16x16x32_bf16 a[56:59],a[164:167],a[204:207],a[56:59]
buffer_load_dwordx4 v5,s[24:27],s47 offen offset:3072 nt lds
v_mfma_f32_16x16x32_bf16 a[56:59],a[160:163],a[200:203],a[56:59]
s_add_u32 s47,s47,16384
s_and_b32 s47,s47,0x3ffff
s_or_b32 s47,s47,s46
v_mfma_f32_16x16x32_bf16 a[88:91],a[160:163],a[208:211],a[88:91]
s_and_b32 s46,s54,0xfffc0000
v_mul_f32 v103,s61,v103
v_mul_f32 v72,s61,v72
v_mfma_f32_16x16x32_bf16 a[88:91],a[164:167],a[212:215],a[88:91]
s_add_u32 s54,s54,16384
v_mul_f32 v73,s61,v73
v_accvgpr_read_b32 v74,a18
v_mfma_f32_16x16x32_bf16 a[88:91],a[168:171],a[240:243],a[88:91]
v_accvgpr_read_b32 v75,a19
v_cvt_pk_bf16_f32 v88,v88,v89
v_mfma_f32_16x16x32_bf16 a[88:91],a[172:175],a[244:247],a[88:91]
v_mul_f32 v90,s61,v90
v_mul_f32 v91,s61,v91
v_mfma_f32_16x16x32_bf16 a[120:123],a[172:175],a[252:255],a[120:123]
ds_read_b64_tr_b16 a[172:173],v22 offset:50696
v_cvt_pk_bf16_f32 v83,v14,v15
v_mfma_f32_16x16x32_bf16 a[120:123],a[168:171],a[248:251],a[120:123]
ds_read_b64_tr_b16 a[168:169],v22 offset:34312
v_accvgpr_read_b32 v70,a12
v_mfma_f32_16x16x32_bf16 a[120:123],a[164:167],a[220:223],a[120:123]
ds_read_b64_tr_b16 a[164:165],v22 offset:50688
v_accvgpr_read_b32 v71,a13
v_mfma_f32_16x16x32_bf16 a[120:123],a[160:163],a[216:219],a[120:123]
ds_read_b64_tr_b16 a[160:161],v22 offset:34304
v_accvgpr_read_b32 v16,a46
v_mfma_f32_16x16x32_bf16 a[28:31],a[176:179],a[192:195],a[28:31]
s_add_u32 m0,0,s51
ds_read_b64_tr_b16 a[162:163],v22 offset:36352
v_accvgpr_read_b32 v17,a47
v_mfma_f32_16x16x32_bf16 a[28:31],a[180:183],a[196:199],a[28:31]
ds_read_b64_tr_b16 a[166:167],v22 offset:52736
v_mul_f32 v86,s61,v86
v_mfma_f32_16x16x32_bf16 a[28:31],a[184:187],a[224:227],a[28:31]
ds_read_b64_tr_b16 a[170:171],v22 offset:36360
v_mul_f32 v87,s61,v87
v_mfma_f32_16x16x32_bf16 a[28:31],a[188:191],a[228:231],a[28:31]
buffer_load_dwordx4 v4,s[20:23],s47 offen nt lds
v_mfma_f32_16x16x32_bf16 a[60:63],a[188:191],a[236:239],a[60:63]
ds_read_b64_tr_b16 a[174:175],v22 offset:52744
v_mul_f32 v12,s61,v12
v_mfma_f32_16x16x32_bf16 a[60:63],a[184:187],a[232:235],a[60:63]
v_mul_f32 v13,s61,v13
v_cvt_pk_bf16_f32 v102,v102,v103
v_mfma_f32_16x16x32_bf16 a[60:63],a[180:183],a[204:207],a[60:63]
v_cvt_pk_bf16_f32 v72,v72,v73
v_mul_f32 v74,s61,v74
v_mfma_f32_16x16x32_bf16 a[60:63],a[176:179],a[200:203],a[60:63]
v_mul_f32 v75,s61,v75
v_cvt_pk_bf16_f32 v89,v90,v91
v_mfma_f32_16x16x32_bf16 a[92:95],a[176:179],a[208:211],a[92:95]
v_accvgpr_read_b32 v76,a24
v_accvgpr_read_b32 v77,a25
v_mfma_f32_16x16x32_bf16 a[92:95],a[180:183],a[212:215],a[92:95]
v_accvgpr_read_b32 v14,a14
v_accvgpr_read_b32 v15,a15
v_mfma_f32_16x16x32_bf16 a[92:95],a[184:187],a[240:243],a[92:95]
v_mul_f32 v70,s61,v70
v_mul_f32 v71,s61,v71
v_mfma_f32_16x16x32_bf16 a[92:95],a[188:191],a[244:247],a[92:95]
v_mul_f32 v16,s61,v16
v_mul_f32 v17,s61,v17
v_mfma_f32_16x16x32_bf16 a[124:127],a[188:191],a[252:255],a[124:127]
ds_read_b64_tr_b16 a[188:189],v22 offset:50952
v_cvt_pk_bf16_f32 v86,v86,v87
v_mfma_f32_16x16x32_bf16 a[124:127],a[184:187],a[248:251],a[124:127]
buffer_load_dwordx4 v4,s[20:23],s47 offen offset:1024 nt lds
v_mfma_f32_16x16x32_bf16 a[124:127],a[180:183],a[220:223],a[124:127]
ds_read_b64_tr_b16 a[180:181],v22 offset:50944
v_cvt_pk_bf16_f32 v103,v12,v13
v_mfma_f32_16x16x32_bf16 a[124:127],a[176:179],a[216:219],a[124:127]
ds_read_b64_tr_b16 a[176:177],v22 offset:34560
v_cvt_pk_bf16_f32 v73,v74,v75
v_mfma_f32_16x16x32_bf16 v[128:131],a[128:131],a[224:227],v[128:131]
ds_read_b64_tr_b16 a[178:179],v22 offset:36608
v_accvgpr_read_b32 v90,a52
v_mfma_f32_16x16x32_bf16 v[128:131],a[132:135],a[228:231],v[128:131]
ds_read_b64_tr_b16 a[182:183],v22 offset:52992
v_accvgpr_read_b32 v91,a53
v_mfma_f32_16x16x32_bf16 v[136:139],a[132:135],a[236:239],v[136:139]
ds_read_b64_tr_b16 a[184:185],v22 offset:34568
v_mul_f32 v76,s61,v76
v_mfma_f32_16x16x32_bf16 v[136:139],a[128:131],a[232:235],v[136:139]
ds_read_b64_tr_b16 a[186:187],v22 offset:36616
v_mul_f32 v77,s61,v77
v_mfma_f32_16x16x32_bf16 v[144:147],a[128:131],a[240:243],v[144:147]
ds_read_b64_tr_b16 a[190:191],v22 offset:53000
v_accvgpr_read_b32 v78,a26
v_mfma_f32_16x16x32_bf16 v[144:147],a[132:135],a[244:247],v[144:147]
v_accvgpr_read_b32 v79,a27
v_mul_f32 v14,s61,v14
v_mfma_f32_16x16x32_bf16 v[152:155],a[132:135],a[252:255],v[152:155]
v_mul_f32 v15,s61,v15
v_cvt_pk_bf16_f32 v70,v70,v71
v_mfma_f32_16x16x32_bf16 v[152:155],a[128:131],a[248:251],v[152:155]
buffer_load_dwordx4 v5,s[20:23],s47 offen offset:2048 nt lds
v_mfma_f32_16x16x32_bf16 v[132:135],a[136:139],a[224:227],v[132:135]
v_cvt_pk_bf16_f32 v87,v16,v17
v_accvgpr_read_b32 v116,a104
v_mfma_f32_16x16x32_bf16 v[132:135],a[140:143],a[228:231],v[132:135]
v_accvgpr_read_b32 v117,a105
v_accvgpr_read_b32 v74,a20
v_mfma_f32_16x16x32_bf16 v[140:143],a[140:143],a[236:239],v[140:143]
v_accvgpr_read_b32 v75,a21
v_accvgpr_read_b32 v12,a54
v_mfma_f32_16x16x32_bf16 v[140:143],a[136:139],a[232:235],v[140:143]
v_accvgpr_read_b32 v13,a55
v_mul_f32 v90,s61,v90
v_mfma_f32_16x16x32_bf16 v[148:151],a[136:139],a[240:243],v[148:151]
v_mul_f32 v91,s61,v91
v_cvt_pk_bf16_f32 v76,v76,v77
v_mfma_f32_16x16x32_bf16 v[148:151],a[140:143],a[244:247],v[148:151]
v_mul_f32 v78,s61,v78
v_mul_f32 v79,s61,v79
v_mfma_f32_16x16x32_bf16 v[156:159],a[140:143],a[252:255],v[156:159]
s_waitcnt vmcnt(11) lgkmcnt(0)
v_cvt_pk_bf16_f32 v71,v14,v15
v_mul_f32 v116,s61,v116
v_mfma_f32_16x16x32_bf16 v[156:159],a[136:139],a[248:251],v[156:159]
s_barrier
ds_read_b128 v[48:51],v18
v_mul_f32 v117,s61,v117
v_mfma_f32_16x16x32_bf16 v[160:163],a[144:147],a[224:227],v[160:163]
buffer_load_dwordx4 v5,s[20:23],s47 offen offset:3072 nt lds
v_mfma_f32_16x16x32_bf16 v[160:163],a[148:151],a[228:231],v[160:163]
ds_read_b128 v[52:55],v18 offset:256
v_accvgpr_read_b32 v118,a106
v_mfma_f32_16x16x32_bf16 v[168:171],a[148:151],a[236:239],v[168:171]
v_accvgpr_read_b32 v119,a107
v_accvgpr_read_b32 v16,a22
v_mfma_f32_16x16x32_bf16 v[168:171],a[144:147],a[232:235],v[168:171]
ds_read_b128 v[56:59],v18 offset:512
v_accvgpr_read_b32 v17,a23
v_mfma_f32_16x16x32_bf16 v[176:179],a[144:147],a[240:243],v[176:179]
v_mul_f32 v74,s61,v74
v_mul_f32 v75,s61,v75
v_mfma_f32_16x16x32_bf16 v[176:179],a[148:151],a[244:247],v[176:179]
ds_read_b128 v[60:63],v18 offset:768
v_mul_f32 v12,s61,v12
v_mfma_f32_16x16x32_bf16 v[184:187],a[148:151],a[252:255],v[184:187]
v_mul_f32 v13,s61,v13
v_cvt_pk_bf16_f32 v90,v90,v91
v_mfma_f32_16x16x32_bf16 v[184:187],a[144:147],a[248:251],v[184:187]
s_add_u32 m0,0,s52
v_cvt_pk_bf16_f32 v77,v78,v79
v_cvt_pk_bf16_f32 v116,v116,v117
v_mfma_f32_16x16x32_bf16 v[164:167],a[152:155],a[224:227],v[164:167]
v_mul_f32 v118,s61,v118
v_mul_f32 v119,s61,v119
v_mfma_f32_16x16x32_bf16 v[164:167],a[156:159],a[228:231],v[164:167]
v_mul_f32 v16,s61,v16
v_mul_f32 v17,s61,v17
v_mfma_f32_16x16x32_bf16 v[172:175],a[156:159],a[236:239],v[172:175]
buffer_load_dwordx4 v4,s[24:27],s47 offen nt lds
v_mfma_f32_16x16x32_bf16 v[172:175],a[152:155],a[232:235],v[172:175]
v_cvt_pk_bf16_f32 v74,v74,v75
v_cvt_pk_bf16_f32 v91,v12,v13
v_mfma_f32_16x16x32_bf16 v[180:183],a[152:155],a[240:243],v[180:183]
v_accvgpr_read_b32 v78,a28
v_accvgpr_read_b32 v79,a29
v_mfma_f32_16x16x32_bf16 v[180:183],a[156:159],a[244:247],v[180:183]
v_cvt_pk_bf16_f32 v117,v118,v119
v_cvt_pk_bf16_f32 v75,v16,v17
v_mfma_f32_16x16x32_bf16 v[188:191],a[156:159],a[252:255],v[188:191]
v_accvgpr_read_b32 v104,a80
v_accvgpr_read_b32 v105,a81
v_mfma_f32_16x16x32_bf16 v[188:191],a[152:155],a[248:251],v[188:191]
v_accvgpr_read_b32 v12,a30
v_accvgpr_read_b32 v13,a31
v_mfma_f32_16x16x32_bf16 v[192:195],a[160:163],a[224:227],v[192:195]
v_mul_f32 v78,s61,v78
v_mul_f32 v79,s61,v79
v_mfma_f32_16x16x32_bf16 v[192:195],a[164:167],a[228:231],v[192:195]
v_accvgpr_read_b32 v118,a108
v_accvgpr_read_b32 v119,a109
v_mfma_f32_16x16x32_bf16 v[200:203],a[164:167],a[236:239],v[200:203]
v_mul_f32 v104,s61,v104
v_mul_f32 v105,s61,v105
v_mfma_f32_16x16x32_bf16 v[200:203],a[160:163],a[232:235],v[200:203]
v_accvgpr_read_b32 v106,a82
v_accvgpr_read_b32 v107,a83
v_mfma_f32_16x16x32_bf16 v[208:211],a[160:163],a[240:243],v[208:211]
buffer_load_dwordx4 v5,s[24:27],s47 offen offset:1024 nt lds
v_mfma_f32_16x16x32_bf16 v[208:211],a[164:167],a[244:247],v[208:211]
v_mul_f32 v12,s61,v12
v_mul_f32 v13,s61,v13
v_mfma_f32_16x16x32_bf16 v[216:219],a[164:167],a[252:255],v[216:219]
v_cvt_pk_bf16_f32 v78,v78,v79
v_accvgpr_read_b32 v14,a110
v_mfma_f32_16x16x32_bf16 v[216:219],a[160:163],a[248:251],v[216:219]
v_accvgpr_read_b32 v15,a111
v_mul_f32 v118,s61,v118
v_mfma_f32_16x16x32_bf16 v[196:199],a[168:171],a[224:227],v[196:199]
v_mul_f32 v119,s61,v119
v_cvt_pk_bf16_f32 v104,v104,v105
v_mfma_f32_16x16x32_bf16 v[196:199],a[172:175],a[228:231],v[196:199]
v_mul_f32 v106,s61,v106
v_mul_f32 v107,s61,v107
v_mfma_f32_16x16x32_bf16 v[204:207],a[172:175],a[236:239],v[204:207]
v_cvt_pk_bf16_f32 v79,v12,v13
v_mul_f32 v14,s61,v14
v_mfma_f32_16x16x32_bf16 v[204:207],a[168:171],a[232:235],v[204:207]
v_mul_f32 v15,s61,v15
v_cvt_pk_bf16_f32 v118,v118,v119
v_mfma_f32_16x16x32_bf16 v[212:215],a[168:171],a[240:243],v[212:215]
v_cvt_pk_bf16_f32 v105,v106,v107
v_accvgpr_read_b32 v92,a56
v_mfma_f32_16x16x32_bf16 v[212:215],a[172:175],a[244:247],v[212:215]
v_accvgpr_read_b32 v93,a57
v_cvt_pk_bf16_f32 v119,v14,v15
v_mfma_f32_16x16x32_bf16 v[220:223],a[172:175],a[252:255],v[220:223]
buffer_load_dwordx4 v4,s[24:27],s47 offen offset:2048 nt lds
v_mfma_f32_16x16x32_bf16 v[220:223],a[168:171],a[248:251],v[220:223]
v_accvgpr_read_b32 v106,a84
v_accvgpr_read_b32 v107,a85
v_mfma_f32_16x16x32_bf16 v[224:227],a[176:179],a[224:227],v[224:227]
v_mul_f32 v92,s61,v92
v_mul_f32 v93,s61,v93
v_mfma_f32_16x16x32_bf16 v[224:227],a[180:183],a[228:231],v[224:227]
v_accvgpr_read_b32 v94,a58
v_accvgpr_read_b32 v95,a59
v_mfma_f32_16x16x32_bf16 v[232:235],a[180:183],a[236:239],v[232:235]
v_accvgpr_read_b32 v14,a86
v_accvgpr_read_b32 v15,a87
v_mfma_f32_16x16x32_bf16 v[232:235],a[176:179],a[232:235],v[232:235]
v_mul_f32 v106,s61,v106
v_mul_f32 v107,s61,v107
v_mfma_f32_16x16x32_bf16 v[240:243],a[176:179],a[240:243],v[240:243]
v_cvt_pk_bf16_f32 v92,v92,v93
v_mul_f32 v94,s61,v94
v_mfma_f32_16x16x32_bf16 v[240:243],a[180:183],a[244:247],v[240:243]
v_mul_f32 v95,s61,v95
v_mul_f32 v14,s61,v14
v_mfma_f32_16x16x32_bf16 v[248:251],a[180:183],a[252:255],v[248:251]
v_mul_f32 v15,s61,v15
v_cvt_pk_bf16_f32 v106,v106,v107
v_mfma_f32_16x16x32_bf16 v[248:251],a[176:179],a[248:251],v[248:251]
v_cvt_pk_bf16_f32 v93,v94,v95
v_cvt_pk_bf16_f32 v107,v14,v15
v_mfma_f32_16x16x32_bf16 v[228:231],a[184:187],a[224:227],v[228:231]
buffer_load_dwordx4 v5,s[24:27],s47 offen offset:3072 nt lds
v_mfma_f32_16x16x32_bf16 v[228:231],a[188:191],a[228:231],v[228:231]
s_add_u32 s47,s47,16384
v_accvgpr_read_b32 v94,a60
v_accvgpr_read_b32 v95,a61
v_mfma_f32_16x16x32_bf16 v[236:239],a[188:191],a[236:239],v[236:239]
s_and_b32 s47,s47,0x3ffff
v_accvgpr_read_b32 v120,a112
v_accvgpr_read_b32 v121,a113
v_mfma_f32_16x16x32_bf16 v[236:239],a[184:187],a[232:235],v[236:239]
s_or_b32 s47,s47,s46
v_accvgpr_read_b32 v14,a62
v_accvgpr_read_b32 v15,a63
v_mfma_f32_16x16x32_bf16 v[244:247],a[184:187],a[240:243],v[244:247]
s_and_b32 s46,s54,0xfffc0000
v_mul_f32 v94,s61,v94
v_mul_f32 v95,s61,v95
v_mfma_f32_16x16x32_bf16 v[244:247],a[188:191],a[244:247],v[244:247]
s_add_u32 s54,s54,16384
v_mul_f32 v120,s61,v120
v_mul_f32 v121,s61,v121
v_mfma_f32_16x16x32_bf16 v[252:255],a[188:191],a[252:255],v[252:255]
v_accvgpr_read_b32 v122,a114
v_accvgpr_read_b32 v123,a115
v_mfma_f32_16x16x32_bf16 v[252:255],a[184:187],a[248:251],v[252:255]
s_waitcnt lgkmcnt(0)
v_mul_f32 v14,s61,v14
v_mul_f32 v15,s61,v15
v_mfma_f32_16x16x32_bf16 v[32:35],v[48:51],v[64:67],0
v_cvt_pk_bf16_f32 v94,v94,v95
v_cvt_pk_bf16_f32 v120,v120,v121
v_mfma_f32_16x16x32_bf16 v[32:35],v[52:55],v[68:71],v[32:35]
)A"
R"A(v_mul_f32 v122,s61,v122
v_mul_f32 v123,s61,v123
v_mfma_f32_16x16x32_bf16 v[32:35],v[56:59],v[72:75],v[32:35]
v_cvt_pk_bf16_f32 v95,v14,v15
v_cvt_pk_bf16_f32 v121,v122,v123
v_mfma_f32_16x16x32_bf16 v[32:35],v[60:63],v[76:79],v[32:35]
v_accvgpr_read_b32 v108,a88
v_accvgpr_read_b32 v109,a89
v_mfma_f32_16x16x32_bf16 v[36:39],v[48:51],v[80:83],0
s_add_u32 m0,32768,s51
v_accvgpr_read_b32 v122,a116
v_accvgpr_read_b32 v123,a117
v_mfma_f32_16x16x32_bf16 v[36:39],v[52:55],v[84:87],v[36:39]
v_mul_f32 v108,s61,v108
v_mul_f32 v109,s61,v109
v_accvgpr_read_b32 v110,a90
v_accvgpr_read_b32 v111,a91
v_mfma_f32_16x16x32_bf16 v[36:39],v[56:59],v[88:91],v[36:39]
v_accvgpr_read_b32 v16,a118
v_accvgpr_read_b32 v17,a119
v_mul_f32 v122,s61,v122
v_mul_f32 v123,s61,v123
v_cvt_pk_bf16_f32 v108,v108,v109
v_mul_f32 v110,s61,v110
v_mul_f32 v111,s61,v111
v_mul_f32 v16,s61,v16
v_mul_f32 v17,s61,v17
v_cvt_pk_bf16_f32 v122,v122,v123
v_cvt_pk_bf16_f32 v109,v110,v111
v_mfma_f32_16x16x32_bf16 v[36:39],v[60:63],v[92:95],v[36:39]
ds_read_b128 a[128:131],v1
v_cvt_pk_bf16_f32 v123,v16,v17
v_accvgpr_read_b32 v110,a92
v_accvgpr_read_b32 v111,a93
v_accvgpr_read_b32 v16,a94
v_accvgpr_read_b32 v17,a95
v_mul_f32 v110,s61,v110
v_mul_f32 v111,s61,v111
v_mfma_f32_16x16x32_bf16 v[40:43],v[48:51],v[96:99],0
ds_read_b128 a[132:135],v1 offset:256
v_mul_f32 v16,s61,v16
v_mul_f32 v17,s61,v17
v_cvt_pk_bf16_f32 v110,v110,v111
v_cvt_pk_bf16_f32 v111,v16,v17
v_mfma_f32_16x16x32_bf16 v[40:43],v[52:55],v[100:103],v[40:43]
ds_read_b128 a[136:139],v1 offset:512
v_accvgpr_read_b32 v124,a120
v_accvgpr_read_b32 v125,a121
v_mul_f32 v124,s61,v124
v_mul_f32 v125,s61,v125
v_accvgpr_read_b32 v126,a122
v_accvgpr_read_b32 v127,a123
v_mfma_f32_16x16x32_bf16 v[40:43],v[56:59],v[104:107],v[40:43]
buffer_load_dwordx4 v4,s[20:23],s47 offen nt lds
ds_read_b128 a[140:143],v1 offset:768
v_cvt_pk_bf16_f32 v124,v124,v125
v_mul_f32 v126,s61,v126
v_mul_f32 v127,s61,v127
v_cvt_pk_bf16_f32 v125,v126,v127
v_mfma_f32_16x16x32_bf16 v[40:43],v[60:63],v[108:111],v[40:43]
ds_read_b128 a[156:159],v1 offset:4864
v_accvgpr_read_b32 v126,a124
v_accvgpr_read_b32 v127,a125
v_accvgpr_read_b32 v12,a126
v_accvgpr_read_b32 v13,a127
v_mul_f32 v126,s61,v126
v_mul_f32 v127,s61,v127
v_mfma_f32_16x16x32_bf16 v[44:47],v[48:51],v[112:115],0
ds_read_b128 a[152:155],v1 offset:4608
v_mul_f32 v12,s61,v12
v_mul_f32 v13,s61,v13
v_cvt_pk_bf16_f32 v126,v126,v127
v_max3_f32 v32,v32,v33,v34
v_cvt_pk_bf16_f32 v127,v12,v13
v_max3_f32 v32,v32,v35,s18
v_mfma_f32_16x16x32_bf16 v[44:47],v[52:55],v[116:119],v[44:47]
ds_read_b128 a[148:151],v1 offset:4352
v_sub_f32 v35,0,v32
v_sub_f32 v34,0,v32
v_sub_f32 v33,0,v32
v_sub_f32 v32,0,v32
v_mfma_f32_16x16x32_bf16 v[44:47],v[56:59],v[120:123],v[44:47]
ds_read_b128 a[144:147],v1 offset:4096
v_mul_f32 v128,s61,v128
v_mul_f32 v129,s61,v129
v_cvt_pk_bf16_f32 v128,v128,v129
v_mul_f32 v130,s61,v130
v_mul_f32 v131,s61,v131
v_mfma_f32_16x16x32_bf16 v[44:47],v[60:63],v[124:127],v[44:47]
s_waitcnt lgkmcnt(7)
ds_read_b128 a[160:163],v1 offset:8192
v_cvt_pk_bf16_f32 v129,v130,v131
v_mul_f32 v132,s61,v132
v_mul_f32 v133,s61,v133
v_mul_f32 v134,s61,v134
v_mul_f32 v135,s61,v135
v_cvt_pk_bf16_f32 v130,v132,v133
v_cvt_pk_bf16_f32 v131,v134,v135
v_mfma_f32_16x16x32_bf16 v[132:135],a[128:131],v[64:67],v[32:35]
s_waitcnt lgkmcnt(7)
ds_read_b128 a[164:167],v1 offset:8448
v_mfma_f32_16x16x32_bf16 v[132:135],a[132:135],v[68:71],v[132:135]
s_waitcnt lgkmcnt(7)
ds_read_b128 a[168:171],v1 offset:8704
v_mfma_f32_16x16x32_bf16 v[132:135],a[136:139],v[72:75],v[132:135]
s_waitcnt lgkmcnt(7)
ds_read_b128 a[172:175],v1 offset:8960
v_mul_f32 v136,s61,v136
v_mul_f32 v137,s61,v137
v_cvt_pk_bf16_f32 v136,v136,v137
v_mul_f32 v138,s61,v138
v_mul_f32 v139,s61,v139
v_mfma_f32_16x16x32_bf16 v[132:135],a[140:143],v[76:79],v[132:135]
s_waitcnt lgkmcnt(7)
buffer_load_dwordx4 v4,s[20:23],s47 offen offset:1024 nt lds
ds_read_b128 a[188:191],v1 offset:13056
v_cvt_pk_bf16_f32 v137,v138,v139
v_mul_f32 v140,s61,v140
v_mul_f32 v141,s61,v141
v_mul_f32 v142,s61,v142
v_mul_f32 v143,s61,v143
v_cvt_pk_bf16_f32 v138,v140,v141
v_cvt_pk_bf16_f32 v139,v142,v143
v_mfma_f32_16x16x32_bf16 v[140:143],a[156:159],v[76:79],v[32:35]
s_waitcnt lgkmcnt(7)
ds_read_b128 a[184:187],v1 offset:12800
v_mfma_f32_16x16x32_bf16 v[140:143],a[152:155],v[72:75],v[140:143]
s_waitcnt lgkmcnt(7)
ds_read_b128 a[180:183],v1 offset:12544
v_mfma_f32_16x16x32_bf16 v[140:143],a[148:151],v[68:71],v[140:143]
s_waitcnt lgkmcnt(7)
ds_read_b128 a[176:179],v1 offset:12288
v_mul_f32 v144,s61,v144
v_mul_f32 v145,s61,v145
v_cvt_pk_bf16_f32 v144,v144,v145
v_mul_f32 v146,s61,v146
v_mul_f32 v147,s61,v147
v_mfma_f32_16x16x32_bf16 v[140:143],a[144:147],v[64:67],v[140:143]
s_waitcnt lgkmcnt(7)
v_cvt_pk_bf16_f32 v145,v146,v147
v_mul_f32 v148,s61,v148
v_mul_f32 v149,s61,v149
v_mul_f32 v150,s61,v150
v_mul_f32 v151,s61,v151
v_cvt_pk_bf16_f32 v146,v148,v149
v_cvt_pk_bf16_f32 v147,v150,v151
v_mfma_f32_16x16x32_bf16 v[148:151],a[160:163],v[64:67],v[32:35]
s_waitcnt lgkmcnt(6)
v_mfma_f32_16x16x32_bf16 v[148:151],a[164:167],v[68:71],v[148:151]
s_waitcnt lgkmcnt(5)
v_mfma_f32_16x16x32_bf16 v[148:151],a[168:171],v[72:75],v[148:151]
s_waitcnt lgkmcnt(4)
v_mul_f32 v152,s61,v152
v_mul_f32 v153,s61,v153
v_cvt_pk_bf16_f32 v152,v152,v153
v_mul_f32 v154,s61,v154
v_mul_f32 v155,s61,v155
v_mfma_f32_16x16x32_bf16 v[148:151],a[172:175],v[76:79],v[148:151]
s_waitcnt lgkmcnt(3)
v_cvt_pk_bf16_f32 v153,v154,v155
v_mul_f32 v156,s61,v156
v_mul_f32 v157,s61,v157
v_mul_f32 v158,s61,v158
v_mul_f32 v159,s61,v159
v_cvt_pk_bf16_f32 v154,v156,v157
v_cvt_pk_bf16_f32 v155,v158,v159
v_mfma_f32_16x16x32_bf16 v[156:159],a[188:191],v[76:79],v[32:35]
s_waitcnt lgkmcnt(2)
buffer_load_dwordx4 v5,s[20:23],s47 offen offset:2048 nt lds
v_max3_f32 v36,v36,v37,v38
v_max3_f32 v36,v36,v39,s19
v_mfma_f32_16x16x32_bf16 v[156:159],a[184:187],v[72:75],v[156:159]
s_waitcnt lgkmcnt(1)
v_sub_f32 v39,0,v36
v_sub_f32 v38,0,v36
v_sub_f32 v37,0,v36
v_sub_f32 v36,0,v36
v_mfma_f32_16x16x32_bf16 v[156:159],a[180:183],v[68:71],v[156:159]
s_waitcnt lgkmcnt(0)
v_mul_f32 v160,s61,v160
v_mul_f32 v161,s61,v161
v_cvt_pk_bf16_f32 v160,v160,v161
v_mul_f32 v162,s61,v162
v_mul_f32 v163,s61,v163
v_mfma_f32_16x16x32_bf16 v[156:159],a[176:179],v[64:67],v[156:159]
v_cvt_pk_bf16_f32 v161,v162,v163
v_mul_f32 v164,s61,v164
v_mul_f32 v165,s61,v165
v_mul_f32 v166,s61,v166
v_mul_f32 v167,s61,v167
v_cvt_pk_bf16_f32 v162,v164,v165
v_cvt_pk_bf16_f32 v163,v166,v167
v_mfma_f32_16x16x32_bf16 v[164:167],a[128:131],v[80:83],v[36:39]
v_mul_f32 v168,s61,v168
v_mul_f32 v169,s61,v169
v_mfma_f32_16x16x32_bf16 v[164:167],a[132:135],v[84:87],v[164:167]
v_cvt_pk_bf16_f32 v168,v168,v169
v_mul_f32 v170,s61,v170
v_mfma_f32_16x16x32_bf16 v[164:167],a[136:139],v[88:91],v[164:167]
v_mul_f32 v171,s61,v171
v_cvt_pk_bf16_f32 v169,v170,v171
v_mfma_f32_16x16x32_bf16 v[164:167],a[140:143],v[92:95],v[164:167]
v_mul_f32 v172,s61,v172
v_mul_f32 v173,s61,v173
v_mul_f32 v174,s61,v174
v_mul_f32 v175,s61,v175
v_cvt_pk_bf16_f32 v170,v172,v173
v_cvt_pk_bf16_f32 v171,v174,v175
v_mfma_f32_16x16x32_bf16 v[172:175],a[156:159],v[92:95],v[36:39]
buffer_load_dwordx4 v5,s[20:23],s47 offen offset:3072 nt lds
v_mfma_f32_16x16x32_bf16 v[172:175],a[152:155],v[88:91],v[172:175]
v_mul_f32 v176,s61,v176
v_mul_f32 v177,s61,v177
v_mfma_f32_16x16x32_bf16 v[172:175],a[148:151],v[84:87],v[172:175]
v_cvt_pk_bf16_f32 v176,v176,v177
v_mul_f32 v178,s61,v178
v_mul_f32 v179,s61,v179
v_mfma_f32_16x16x32_bf16 v[172:175],a[144:147],v[80:83],v[172:175]
v_cvt_pk_bf16_f32 v177,v178,v179
v_mul_f32 v180,s61,v180
v_mul_f32 v181,s61,v181
v_mul_f32 v182,s61,v182
v_mul_f32 v183,s61,v183
v_cvt_pk_bf16_f32 v178,v180,v181
v_cvt_pk_bf16_f32 v179,v182,v183
v_mfma_f32_16x16x32_bf16 v[180:183],a[160:163],v[80:83],v[36:39]
s_add_u32 m0,32768,s52
v_mul_f32 v184,s61,v184
v_mul_f32 v185,s61,v185
v_mfma_f32_16x16x32_bf16 v[180:183],a[164:167],v[84:87],v[180:183]
v_cvt_pk_bf16_f32 v184,v184,v185
v_mul_f32 v186,s61,v186
v_mfma_f32_16x16x32_bf16 v[180:183],a[168:171],v[88:91],v[180:183]
v_mul_f32 v187,s61,v187
v_cvt_pk_bf16_f32 v185,v186,v187
v_mfma_f32_16x16x32_bf16 v[180:183],a[172:175],v[92:95],v[180:183]
v_mul_f32 v188,s61,v188
v_mul_f32 v189,s61,v189
v_mul_f32 v190,s61,v190
v_mul_f32 v191,s61,v191
v_cvt_pk_bf16_f32 v186,v188,v189
v_cvt_pk_bf16_f32 v187,v190,v191
v_mfma_f32_16x16x32_bf16 v[188:191],a[188:191],v[92:95],v[36:39]
v_max3_f32 v40,v40,v41,v42
v_max3_f32 v40,v40,v43,s100
v_mfma_f32_16x16x32_bf16 v[188:191],a[184:187],v[88:91],v[188:191]
v_sub_f32 v43,0,v40
v_sub_f32 v42,0,v40
v_sub_f32 v41,0,v40
v_sub_f32 v40,0,v40
v_mfma_f32_16x16x32_bf16 v[188:191],a[180:183],v[84:87],v[188:191]
buffer_load_dwordx4 v4,s[24:27],s47 offen nt lds
v_mul_f32 v192,s61,v192
v_mul_f32 v193,s61,v193
v_cvt_pk_bf16_f32 v192,v192,v193
v_mul_f32 v194,s61,v194
v_mul_f32 v195,s61,v195
v_mfma_f32_16x16x32_bf16 v[188:191],a[176:179],v[80:83],v[188:191]
v_cvt_pk_bf16_f32 v193,v194,v195
v_mul_f32 v196,s61,v196
v_mul_f32 v197,s61,v197
v_mul_f32 v198,s61,v198
v_mul_f32 v199,s61,v199
v_cvt_pk_bf16_f32 v194,v196,v197
v_cvt_pk_bf16_f32 v195,v198,v199
v_mfma_f32_16x16x32_bf16 v[196:199],a[128:131],v[96:99],v[40:43]
v_mul_f32 v200,s61,v200
v_mul_f32 v201,s61,v201
v_mfma_f32_16x16x32_bf16 v[196:199],a[132:135],v[100:103],v[196:199]
v_cvt_pk_bf16_f32 v200,v200,v201
v_mul_f32 v202,s61,v202
v_mfma_f32_16x16x32_bf16 v[196:199],a[136:139],v[104:107],v[196:199]
v_mul_f32 v203,s61,v203
v_cvt_pk_bf16_f32 v201,v202,v203
v_mfma_f32_16x16x32_bf16 v[196:199],a[140:143],v[108:111],v[196:199]
v_mul_f32 v204,s61,v204
v_mul_f32 v205,s61,v205
v_mul_f32 v206,s61,v206
v_mul_f32 v207,s61,v207
v_cvt_pk_bf16_f32 v202,v204,v205
v_cvt_pk_bf16_f32 v203,v206,v207
v_mfma_f32_16x16x32_bf16 v[204:207],a[156:159],v[108:111],v[40:43]
v_mul_f32 v208,s61,v208
v_mul_f32 v209,s61,v209
v_mfma_f32_16x16x32_bf16 v[204:207],a[152:155],v[104:107],v[204:207]
v_cvt_pk_bf16_f32 v208,v208,v209
v_mul_f32 v210,s61,v210
v_mfma_f32_16x16x32_bf16 v[204:207],a[148:151],v[100:103],v[204:207]
v_mul_f32 v211,s61,v211
v_cvt_pk_bf16_f32 v209,v210,v211
v_mfma_f32_16x16x32_bf16 v[204:207],a[144:147],v[96:99],v[204:207]
buffer_load_dwordx4 v5,s[24:27],s47 offen offset:1024 nt lds
ds_read_b64_tr_b16 a[192:193],v2
ds_read_b64_tr_b16 a[194:195],v2 offset:4096
v_mul_f32 v212,s61,v212
v_mul_f32 v213,s61,v213
v_mul_f32 v214,s61,v214
v_mul_f32 v215,s61,v215
v_cvt_pk_bf16_f32 v210,v212,v213
v_cvt_pk_bf16_f32 v211,v214,v215
v_mfma_f32_16x16x32_bf16 v[212:215],a[160:163],v[96:99],v[40:43]
ds_read_b64_tr_b16 a[196:197],v2 offset:8192
ds_read_b64_tr_b16 a[198:199],v2 offset:12288
v_mfma_f32_16x16x32_bf16 v[212:215],a[164:167],v[100:103],v[212:215]
ds_read_b64_tr_b16 a[204:205],v3 offset:8192
ds_read_b64_tr_b16 a[206:207],v3 offset:12288
v_mfma_f32_16x16x32_bf16 v[212:215],a[168:171],v[104:107],v[212:215]
ds_read_b64_tr_b16 a[200:201],v3
ds_read_b64_tr_b16 a[202:203],v3 offset:4096
v_mul_f32 v216,s61,v216
v_mul_f32 v217,s61,v217
v_cvt_pk_bf16_f32 v216,v216,v217
v_mul_f32 v218,s61,v218
v_mul_f32 v219,s61,v219
v_mfma_f32_16x16x32_bf16 v[212:215],a[172:175],v[108:111],v[212:215]
ds_read_b64_tr_b16 a[208:209],v2 offset:256
ds_read_b64_tr_b16 a[210:211],v2 offset:4352
v_cvt_pk_bf16_f32 v217,v218,v219
v_mul_f32 v220,s61,v220
v_mul_f32 v221,s61,v221
v_mul_f32 v222,s61,v222
v_mul_f32 v223,s61,v223
v_cvt_pk_bf16_f32 v218,v220,v221
v_cvt_pk_bf16_f32 v219,v222,v223
v_exp_f32 v132,v132
v_exp_f32 v133,v133
v_mfma_f32_16x16x32_bf16 v[220:223],a[188:191],v[108:111],v[40:43]
ds_read_b64_tr_b16 a[212:213],v2 offset:8448
ds_read_b64_tr_b16 a[214:215],v2 offset:12544
v_exp_f32 v134,v134
v_exp_f32 v135,v135
v_cvt_pk_bf16_f32 v132,v132,v133
v_exp_f32 v140,v140
v_exp_f32 v141,v141
v_exp_f32 v142,v142
v_exp_f32 v143,v143
v_exp_f32 v148,v148
v_exp_f32 v149,v149
v_mfma_f32_16x16x32_bf16 v[220:223],a[184:187],v[104:107],v[220:223]
ds_read_b64_tr_b16 a[220:221],v3 offset:8448
ds_read_b64_tr_b16 a[222:223],v3 offset:12544
v_cvt_pk_bf16_f32 v133,v134,v135
v_exp_f32 v150,v150
v_exp_f32 v151,v151
v_cvt_pk_bf16_f32 v134,v140,v141
v_cvt_pk_bf16_f32 v135,v142,v143
)A"
R"A(v_cvt_pk_bf16_f32 v148,v148,v149
v_exp_f32 v156,v156
v_exp_f32 v157,v157
v_exp_f32 v158,v158
v_exp_f32 v159,v159
v_mfma_f32_16x16x32_bf16 v[220:223],a[180:183],v[100:103],v[220:223]
ds_read_b64_tr_b16 a[216:217],v3 offset:256
ds_read_b64_tr_b16 a[218:219],v3 offset:4352
v_cvt_pk_bf16_f32 v149,v150,v151
v_cvt_pk_bf16_f32 v150,v156,v157
v_cvt_pk_bf16_f32 v151,v158,v159
v_mfma_f32_16x16x32_bf16 v[220:223],a[176:179],v[96:99],v[220:223]
s_waitcnt lgkmcnt(14)
ds_read_b64_tr_b16 a[224:225],v2 offset:512
ds_read_b64_tr_b16 a[226:227],v2 offset:4608
v_mfma_f32_16x16x32_bf16 a[0:3],a[192:195],v[132:135],0
s_waitcnt lgkmcnt(14)
buffer_load_dwordx4 v4,s[24:27],s47 offen offset:2048 nt lds
ds_read_b64_tr_b16 a[228:229],v2 offset:8704
ds_read_b64_tr_b16 a[230:231],v2 offset:12800
v_mfma_f32_16x16x32_bf16 a[0:3],a[196:199],v[148:151],a[0:3]
s_waitcnt lgkmcnt(14)
ds_read_b64_tr_b16 a[236:237],v3 offset:8704
ds_read_b64_tr_b16 a[238:239],v3 offset:12800
v_mfma_f32_16x16x32_bf16 a[4:7],a[204:207],v[148:151],0
s_waitcnt lgkmcnt(14)
ds_read_b64_tr_b16 a[232:233],v3 offset:512
ds_read_b64_tr_b16 a[234:235],v3 offset:4608
v_mfma_f32_16x16x32_bf16 a[4:7],a[200:203],v[132:135],a[4:7]
s_waitcnt lgkmcnt(14)
ds_read_b64_tr_b16 a[240:241],v2 offset:768
ds_read_b64_tr_b16 a[242:243],v2 offset:4864
v_mfma_f32_16x16x32_bf16 a[8:11],a[208:211],v[132:135],0
s_waitcnt lgkmcnt(14)
ds_read_b64_tr_b16 a[244:245],v2 offset:8960
ds_read_b64_tr_b16 a[246:247],v2 offset:13056
v_mfma_f32_16x16x32_bf16 a[8:11],a[212:215],v[148:151],a[8:11]
s_memrealtime s[76:77]
ds_read_b64_tr_b16 a[252:253],v3 offset:8960
ds_read_b64_tr_b16 a[254:255],v3 offset:13056
s_waitcnt lgkmcnt(15)
v_mfma_f32_16x16x32_bf16 a[12:15],a[220:223],v[148:151],0
s_waitcnt lgkmcnt(14)
ds_read_b64_tr_b16 a[248:249],v3 offset:768
ds_read_b64_tr_b16 a[250:251],v3 offset:4864
v_mfma_f32_16x16x32_bf16 a[12:15],a[216:219],v[132:135],a[12:15]
s_waitcnt lgkmcnt(14)
v_mfma_f32_16x16x32_bf16 a[16:19],a[224:227],v[132:135],0
s_waitcnt lgkmcnt(12)
v_mfma_f32_16x16x32_bf16 a[16:19],a[228:231],v[148:151],a[16:19]
s_waitcnt lgkmcnt(10)
buffer_load_dwordx4 v5,s[24:27],s47 offen offset:3072 nt lds
v_mfma_f32_16x16x32_bf16 a[20:23],a[236:239],v[148:151],0
s_waitcnt lgkmcnt(8)
v_mfma_f32_16x16x32_bf16 a[20:23],a[232:235],v[132:135],a[20:23]
s_waitcnt lgkmcnt(0)
v_mfma_f32_16x16x32_bf16 a[24:27],a[240:243],v[132:135],0
v_mfma_f32_16x16x32_bf16 a[24:27],a[244:247],v[148:151],a[24:27]
v_mfma_f32_16x16x32_bf16 a[28:31],a[252:255],v[148:151],0
v_max3_f32 v44,v44,v45,v46
v_max3_f32 v44,v44,v47,s101
v_mfma_f32_16x16x32_bf16 a[28:31],a[248:251],v[132:135],a[28:31]
v_sub_f32 v47,0,v44
v_sub_f32 v46,0,v44
v_sub_f32 v45,0,v44
v_sub_f32 v44,0,v44
v_mfma_f32_16x16x32_bf16 v[48:51],v[28:31],v[132:135],0
v_mul_f32 v224,s61,v224
v_mul_f32 v225,s61,v225
v_cvt_pk_bf16_f32 v224,v224,v225
v_mul_f32 v226,s61,v226
v_mul_f32 v227,s61,v227
v_mfma_f32_16x16x32_bf16 v[48:51],v[28:31],v[148:151],v[48:51]
v_cvt_pk_bf16_f32 v225,v226,v227
v_mul_f32 v228,s61,v228
v_mul_f32 v229,s61,v229
v_mul_f32 v230,s61,v230
v_mul_f32 v231,s61,v231
v_cvt_pk_bf16_f32 v226,v228,v229
v_cvt_pk_bf16_f32 v227,v230,v231
v_mfma_f32_16x16x32_bf16 v[228:231],a[128:131],v[112:115],v[44:47]
s_waitcnt vmcnt(16)
s_add_u32 s47,s47,16384
v_mul_f32 v232,s61,v232
v_mul_f32 v233,s61,v233
v_mfma_f32_16x16x32_bf16 v[228:231],a[132:135],v[116:119],v[228:231]
s_barrier
v_cvt_pk_bf16_f32 v232,v232,v233
v_mul_f32 v234,s61,v234
v_mfma_f32_16x16x32_bf16 v[228:231],a[136:139],v[120:123],v[228:231]
s_add_u32 m0,s49,s51
v_mul_f32 v235,s61,v235
v_cvt_pk_bf16_f32 v233,v234,v235
v_mfma_f32_16x16x32_bf16 v[228:231],a[140:143],v[124:127],v[228:231]
v_mul_f32 v236,s61,v236
v_mul_f32 v237,s61,v237
v_mul_f32 v238,s61,v238
v_mul_f32 v239,s61,v239
v_cvt_pk_bf16_f32 v234,v236,v237
v_cvt_pk_bf16_f32 v235,v238,v239
v_mfma_f32_16x16x32_bf16 v[236:239],a[156:159],v[124:127],v[44:47]
s_and_b32 s47,s47,0x3ffff
v_mul_f32 v240,s61,v240
v_mul_f32 v241,s61,v241
v_mfma_f32_16x16x32_bf16 v[236:239],a[152:155],v[120:123],v[236:239]
s_or_b32 s47,s47,s46
v_cvt_pk_bf16_f32 v240,v240,v241
v_mul_f32 v242,s61,v242
v_mfma_f32_16x16x32_bf16 v[236:239],a[148:151],v[116:119],v[236:239]
s_add_u32 s78,s76,138
v_mul_f32 v243,s61,v243
v_cvt_pk_bf16_f32 v241,v242,v243
v_mfma_f32_16x16x32_bf16 v[236:239],a[144:147],v[112:115],v[236:239]
buffer_load_dwordx4 v4,s[20:23],s47 offen nt lds
v_mul_f32 v244,s61,v244
v_mul_f32 v245,s61,v245
v_mul_f32 v246,s61,v246
v_mul_f32 v247,s61,v247
v_cvt_pk_bf16_f32 v242,v244,v245
v_cvt_pk_bf16_f32 v243,v246,v247
v_mfma_f32_16x16x32_bf16 v[244:247],a[160:163],v[112:115],v[44:47]
s_and_b32 s46,s54,0xfffc0000
v_mul_f32 v248,s61,v248
v_mul_f32 v249,s61,v249
v_mfma_f32_16x16x32_bf16 v[244:247],a[164:167],v[116:119],v[244:247]
v_cvt_pk_bf16_f32 v248,v248,v249
v_mul_f32 v250,s61,v250
v_mfma_f32_16x16x32_bf16 v[244:247],a[168:171],v[120:123],v[244:247]
v_mul_f32 v251,s61,v251
v_cvt_pk_bf16_f32 v249,v250,v251
v_mfma_f32_16x16x32_bf16 v[244:247],a[172:175],v[124:127],v[244:247]
v_mul_f32 v252,s61,v252
v_mul_f32 v253,s61,v253
v_mul_f32 v254,s61,v254
v_mul_f32 v255,s61,v255
v_cvt_pk_bf16_f32 v250,v252,v253
v_cvt_pk_bf16_f32 v251,v254,v255
v_exp_f32 v164,v164
v_exp_f32 v165,v165
v_mfma_f32_16x16x32_bf16 v[252:255],a[188:191],v[124:127],v[44:47]
v_exp_f32 v166,v166
v_exp_f32 v167,v167
v_cvt_pk_bf16_f32 v164,v164,v165
v_exp_f32 v172,v172
v_exp_f32 v173,v173
v_exp_f32 v174,v174
v_exp_f32 v175,v175
v_exp_f32 v180,v180
v_exp_f32 v181,v181
v_mfma_f32_16x16x32_bf16 v[252:255],a[184:187],v[120:123],v[252:255]
v_cvt_pk_bf16_f32 v165,v166,v167
v_exp_f32 v182,v182
v_exp_f32 v183,v183
v_cvt_pk_bf16_f32 v166,v172,v173
v_cvt_pk_bf16_f32 v167,v174,v175
v_cvt_pk_bf16_f32 v180,v180,v181
v_exp_f32 v188,v188
v_exp_f32 v189,v189
v_exp_f32 v190,v190
v_exp_f32 v191,v191
v_mfma_f32_16x16x32_bf16 v[252:255],a[180:183],v[116:119],v[252:255]
v_cvt_pk_bf16_f32 v181,v182,v183
v_cvt_pk_bf16_f32 v182,v188,v189
v_cvt_pk_bf16_f32 v183,v190,v191
v_mfma_f32_16x16x32_bf16 v[252:255],a[176:179],v[112:115],v[252:255]
v_add_u32 v1,s48,v1
v_add_u32 v2,s48,v2
v_mfma_f32_16x16x32_bf16 a[32:35],a[192:195],v[164:167],0
ds_read_b128 a[128:131],v1
v_add_u32 v3,s48,v3
v_mfma_f32_16x16x32_bf16 a[32:35],a[196:199],v[180:183],a[32:35]
s_sub_u32 s48,32768,s62
v_exp_f32 v196,v196
v_mfma_f32_16x16x32_bf16 a[36:39],a[204:207],v[180:183],0
s_add_u32 s50,s50,s48
buffer_load_dwordx4 v4,s[20:23],s47 offen offset:1024 nt lds
v_mfma_f32_16x16x32_bf16 a[36:39],a[200:203],v[164:167],a[36:39]
ds_read_b128 a[132:135],v1 offset:256
v_mfma_f32_16x16x32_bf16 a[40:43],a[208:211],v[164:167],0
v_exp_f32 v197,v197
v_mfma_f32_16x16x32_bf16 a[40:43],a[212:215],v[180:183],a[40:43]
ds_read_b128 a[136:139],v1 offset:512
v_cvt_pk_bf16_f32 v196,v196,v197
v_mfma_f32_16x16x32_bf16 a[44:47],a[220:223],v[180:183],0
v_exp_f32 v198,v198
v_mfma_f32_16x16x32_bf16 a[44:47],a[216:219],v[164:167],a[44:47]
ds_read_b128 a[140:143],v1 offset:768
v_mfma_f32_16x16x32_bf16 a[48:51],a[224:227],v[164:167],0
v_exp_f32 v199,v199
v_mfma_f32_16x16x32_bf16 a[48:51],a[228:231],v[180:183],a[48:51]
ds_read_b128 a[156:159],v1 offset:4864
v_cvt_pk_bf16_f32 v197,v198,v199
v_mfma_f32_16x16x32_bf16 a[52:55],a[236:239],v[180:183],0
v_exp_f32 v204,v204
v_mfma_f32_16x16x32_bf16 a[52:55],a[232:235],v[164:167],a[52:55]
ds_read_b128 a[152:155],v1 offset:4608
v_mfma_f32_16x16x32_bf16 a[56:59],a[240:243],v[164:167],0
v_exp_f32 v205,v205
v_mfma_f32_16x16x32_bf16 a[56:59],a[244:247],v[180:183],a[56:59]
ds_read_b128 a[148:151],v1 offset:4352
v_cvt_pk_bf16_f32 v198,v204,v205
v_mfma_f32_16x16x32_bf16 a[60:63],a[252:255],v[180:183],0
v_exp_f32 v206,v206
v_mfma_f32_16x16x32_bf16 a[60:63],a[248:251],v[164:167],a[60:63]
ds_read_b128 a[144:147],v1 offset:4096
v_mfma_f32_16x16x32_bf16 v[52:55],v[28:31],v[164:167],0
buffer_load_dwordx4 v5,s[20:23],s47 offen offset:2048 nt lds
v_mfma_f32_16x16x32_bf16 v[52:55],v[28:31],v[180:183],v[52:55]
s_waitcnt lgkmcnt(3)
ds_read_b128 a[160:163],v1 offset:8192
v_mfma_f32_16x16x32_bf16 v[132:135],a[128:131],v[64:67],v[32:35]
ds_read_b128 a[164:167],v1 offset:8448
v_mfma_f32_16x16x32_bf16 v[132:135],a[132:135],v[68:71],v[132:135]
ds_read_b128 a[168:171],v1 offset:8704
v_mfma_f32_16x16x32_bf16 v[132:135],a[136:139],v[72:75],v[132:135]
ds_read_b128 a[172:175],v1 offset:8960
v_mfma_f32_16x16x32_bf16 v[132:135],a[140:143],v[76:79],v[132:135]
ds_read_b128 a[188:191],v1 offset:13056
v_mfma_f32_16x16x32_bf16 v[140:143],a[156:159],v[76:79],v[32:35]
s_waitcnt lgkmcnt(6)
ds_read_b128 a[184:187],v1 offset:12800
v_mfma_f32_16x16x32_bf16 v[140:143],a[152:155],v[72:75],v[140:143]
ds_read_b128 a[180:183],v1 offset:12544
v_mfma_f32_16x16x32_bf16 v[140:143],a[148:151],v[68:71],v[140:143]
s_waitcnt lgkmcnt(7)
ds_read_b128 a[176:179],v1 offset:12288
v_mfma_f32_16x16x32_bf16 v[140:143],a[144:147],v[64:67],v[140:143]
s_waitcnt lgkmcnt(7)
v_mfma_f32_16x16x32_bf16 v[148:151],a[160:163],v[64:67],v[32:35]
s_waitcnt lgkmcnt(6)
v_mfma_f32_16x16x32_bf16 v[148:151],a[164:167],v[68:71],v[148:151]
s_waitcnt lgkmcnt(5)
v_mfma_f32_16x16x32_bf16 v[148:151],a[168:171],v[72:75],v[148:151]
s_waitcnt lgkmcnt(4)
v_mfma_f32_16x16x32_bf16 v[148:151],a[172:175],v[76:79],v[148:151]
s_waitcnt lgkmcnt(3)
v_exp_f32 v207,v207
v_mfma_f32_16x16x32_bf16 v[156:159],a[188:191],v[76:79],v[32:35]
s_waitcnt lgkmcnt(2)
v_exp_f32 v212,v212
v_exp_f32 v213,v213
v_mfma_f32_16x16x32_bf16 v[156:159],a[184:187],v[72:75],v[156:159]
s_waitcnt lgkmcnt(1)
v_exp_f32 v214,v214
v_exp_f32 v215,v215
v_cvt_pk_bf16_f32 v199,v206,v207
v_cvt_pk_bf16_f32 v212,v212,v213
v_exp_f32 v220,v220
v_exp_f32 v221,v221
v_exp_f32 v222,v222
v_exp_f32 v223,v223
v_mfma_f32_16x16x32_bf16 v[156:159],a[180:183],v[68:71],v[156:159]
s_waitcnt lgkmcnt(0)
v_cvt_pk_bf16_f32 v213,v214,v215
v_cvt_pk_bf16_f32 v214,v220,v221
v_cvt_pk_bf16_f32 v215,v222,v223
v_mfma_f32_16x16x32_bf16 v[156:159],a[176:179],v[64:67],v[156:159]
buffer_load_dwordx4 v5,s[20:23],s47 offen offset:3072 nt lds
v_mfma_f32_16x16x32_bf16 a[64:67],a[192:195],v[196:199],0
v_exp_f32 v228,v228
v_mfma_f32_16x16x32_bf16 a[64:67],a[196:199],v[212:215],a[64:67]
v_exp_f32 v229,v229
v_mfma_f32_16x16x32_bf16 a[68:71],a[204:207],v[212:215],0
v_exp_f32 v230,v230
v_mfma_f32_16x16x32_bf16 a[68:71],a[200:203],v[196:199],a[68:71]
v_exp_f32 v231,v231
v_mfma_f32_16x16x32_bf16 a[72:75],a[208:211],v[196:199],0
v_cvt_pk_bf16_f32 v228,v228,v229
v_cvt_pk_bf16_f32 v229,v230,v231
v_mfma_f32_16x16x32_bf16 a[72:75],a[212:215],v[212:215],a[72:75]
v_exp_f32 v236,v236
v_mfma_f32_16x16x32_bf16 a[76:79],a[220:223],v[212:215],0
v_exp_f32 v237,v237
v_mfma_f32_16x16x32_bf16 a[76:79],a[216:219],v[196:199],a[76:79]
s_add_u32 m0,s49,s52
v_exp_f32 v238,v238
v_mfma_f32_16x16x32_bf16 a[80:83],a[224:227],v[196:199],0
s_add_u32 s49,s49,32768
v_exp_f32 v239,v239
v_mfma_f32_16x16x32_bf16 a[80:83],a[228:231],v[212:215],a[80:83]
v_exp_f32 v244,v244
v_mfma_f32_16x16x32_bf16 a[84:87],a[236:239],v[212:215],0
buffer_load_dwordx4 v4,s[24:27],s47 offen nt lds
v_mfma_f32_16x16x32_bf16 a[84:87],a[232:235],v[196:199],a[84:87]
v_exp_f32 v245,v245
v_mfma_f32_16x16x32_bf16 a[88:91],a[240:243],v[196:199],0
v_exp_f32 v246,v246
v_mfma_f32_16x16x32_bf16 a[88:91],a[244:247],v[212:215],a[88:91]
v_exp_f32 v247,v247
v_mfma_f32_16x16x32_bf16 a[92:95],a[252:255],v[212:215],0
v_cvt_pk_bf16_f32 v230,v236,v237
v_cvt_pk_bf16_f32 v231,v238,v239
v_mfma_f32_16x16x32_bf16 a[92:95],a[248:251],v[196:199],a[92:95]
v_cvt_pk_bf16_f32 v244,v244,v245
v_cvt_pk_bf16_f32 v245,v246,v247
v_mfma_f32_16x16x32_bf16 v[56:59],v[28:31],v[196:199],0
v_exp_f32 v252,v252
v_mfma_f32_16x16x32_bf16 v[56:59],v[28:31],v[212:215],v[56:59]
v_exp_f32 v253,v253
v_mfma_f32_16x16x32_bf16 v[164:167],a[128:131],v[80:83],v[36:39]
v_exp_f32 v254,v254
v_mfma_f32_16x16x32_bf16 v[164:167],a[132:135],v[84:87],v[164:167]
v_exp_f32 v255,v255
v_mfma_f32_16x16x32_bf16 v[164:167],a[136:139],v[88:91],v[164:167]
v_cvt_pk_bf16_f32 v246,v252,v253
v_cvt_pk_bf16_f32 v247,v254,v255
v_mfma_f32_16x16x32_bf16 v[164:167],a[140:143],v[92:95],v[164:167]
v_exp_f32 v132,v132
v_mfma_f32_16x16x32_bf16 v[172:175],a[156:159],v[92:95],v[36:39]
)A"
R"A(v_exp_f32 v133,v133
v_mfma_f32_16x16x32_bf16 v[172:175],a[152:155],v[88:91],v[172:175]
v_exp_f32 v134,v134
v_mfma_f32_16x16x32_bf16 v[172:175],a[148:151],v[84:87],v[172:175]
buffer_load_dwordx4 v5,s[24:27],s47 offen offset:1024 nt lds
v_mfma_f32_16x16x32_bf16 v[172:175],a[144:147],v[80:83],v[172:175]
v_exp_f32 v135,v135
v_mfma_f32_16x16x32_bf16 v[180:183],a[160:163],v[80:83],v[36:39]
v_cvt_pk_bf16_f32 v132,v132,v133
v_cvt_pk_bf16_f32 v133,v134,v135
v_mfma_f32_16x16x32_bf16 v[180:183],a[164:167],v[84:87],v[180:183]
v_exp_f32 v140,v140
v_mfma_f32_16x16x32_bf16 v[180:183],a[168:171],v[88:91],v[180:183]
v_exp_f32 v141,v141
v_mfma_f32_16x16x32_bf16 v[180:183],a[172:175],v[92:95],v[180:183]
v_exp_f32 v142,v142
v_mfma_f32_16x16x32_bf16 v[188:191],a[188:191],v[92:95],v[36:39]
v_exp_f32 v143,v143
v_mfma_f32_16x16x32_bf16 v[188:191],a[184:187],v[88:91],v[188:191]
v_exp_f32 v148,v148
v_mfma_f32_16x16x32_bf16 v[188:191],a[180:183],v[84:87],v[188:191]
v_exp_f32 v149,v149
v_mfma_f32_16x16x32_bf16 v[188:191],a[176:179],v[80:83],v[188:191]
v_exp_f32 v150,v150
v_mfma_f32_16x16x32_bf16 a[96:99],a[192:195],v[228:231],0
ds_read_b64_tr_b16 a[192:193],v2
ds_read_b64_tr_b16 a[194:195],v2 offset:4096
v_mfma_f32_16x16x32_bf16 a[96:99],a[196:199],v[244:247],a[96:99]
ds_read_b64_tr_b16 a[196:197],v2 offset:8192
v_cvt_pk_bf16_f32 v134,v140,v141
v_mfma_f32_16x16x32_bf16 a[100:103],a[204:207],v[244:247],0
ds_read_b64_tr_b16 a[198:199],v2 offset:12288
v_cvt_pk_bf16_f32 v135,v142,v143
v_mfma_f32_16x16x32_bf16 a[100:103],a[200:203],v[228:231],a[100:103]
ds_read_b64_tr_b16 a[204:205],v3 offset:8192
v_cvt_pk_bf16_f32 v148,v148,v149
v_mfma_f32_16x16x32_bf16 a[104:107],a[208:211],v[228:231],0
buffer_load_dwordx4 v4,s[24:27],s47 offen offset:2048 nt lds
v_mfma_f32_16x16x32_bf16 a[104:107],a[212:215],v[244:247],a[104:107]
ds_read_b64_tr_b16 a[206:207],v3 offset:12288
ds_read_b64_tr_b16 a[200:201],v3
v_mfma_f32_16x16x32_bf16 a[108:111],a[220:223],v[244:247],0
ds_read_b64_tr_b16 a[202:203],v3 offset:4096
v_add_u32 v1,s48,v1
v_mfma_f32_16x16x32_bf16 a[108:111],a[216:219],v[228:231],a[108:111]
s_cmp_eq_u32 s49,163840
s_cselect_b32 s49,0,s49
ds_read_b64_tr_b16 a[208:209],v2 offset:256
v_mfma_f32_16x16x32_bf16 a[112:115],a[224:227],v[228:231],0
ds_read_b64_tr_b16 a[210:211],v2 offset:4352
v_mfma_f32_16x16x32_bf16 a[112:115],a[228:231],v[244:247],a[112:115]
ds_read_b64_tr_b16 a[212:213],v2 offset:8448
ds_read_b64_tr_b16 a[214:215],v2 offset:12544
v_mfma_f32_16x16x32_bf16 a[116:119],a[236:239],v[244:247],0
v_exp_f32 v151,v151
v_mfma_f32_16x16x32_bf16 a[116:119],a[232:235],v[228:231],a[116:119]
v_exp_f32 v156,v156
v_mfma_f32_16x16x32_bf16 a[120:123],a[240:243],v[228:231],0
v_exp_f32 v157,v157
v_mfma_f32_16x16x32_bf16 a[120:123],a[244:247],v[244:247],a[120:123]
v_exp_f32 v158,v158
v_mfma_f32_16x16x32_bf16 a[124:127],a[252:255],v[244:247],0
v_exp_f32 v159,v159
v_mfma_f32_16x16x32_bf16 a[124:127],a[248:251],v[228:231],a[124:127]
ds_read_b64_tr_b16 a[220:221],v3 offset:8448
ds_read_b64_tr_b16 a[222:223],v3 offset:12544
v_mfma_f32_16x16x32_bf16 v[60:63],v[28:31],v[228:231],0
v_cvt_pk_bf16_f32 v149,v150,v151
v_cvt_pk_bf16_f32 v150,v156,v157
v_mfma_f32_16x16x32_bf16 v[60:63],v[28:31],v[244:247],v[60:63]
ds_read_b64_tr_b16 a[216:217],v3 offset:256
v_cvt_pk_bf16_f32 v151,v158,v159
v_mfma_f32_16x16x32_bf16 v[196:199],a[128:131],v[96:99],v[40:43]
buffer_load_dwordx4 v5,s[24:27],s47 offen offset:3072 nt lds
v_mfma_f32_16x16x32_bf16 v[196:199],a[132:135],v[100:103],v[196:199]
s_add_u32 s47,s47,16384
ds_read_b64_tr_b16 a[218:219],v3 offset:4352
ds_read_b64_tr_b16 a[224:225],v2 offset:512
v_mfma_f32_16x16x32_bf16 v[196:199],a[136:139],v[104:107],v[196:199]
s_and_b32 s47,s47,0x3ffff
s_or_b32 s47,s47,s46
ds_read_b64_tr_b16 a[226:227],v2 offset:4608
v_mfma_f32_16x16x32_bf16 v[196:199],a[140:143],v[108:111],v[196:199]
ds_read_b64_tr_b16 a[228:229],v2 offset:8704
v_mfma_f32_16x16x32_bf16 v[204:207],a[156:159],v[108:111],v[40:43]
ds_read_b64_tr_b16 a[230:231],v2 offset:12800
v_mfma_f32_16x16x32_bf16 v[204:207],a[152:155],v[104:107],v[204:207]
ds_read_b64_tr_b16 a[236:237],v3 offset:8704
ds_read_b64_tr_b16 a[238:239],v3 offset:12800
v_mfma_f32_16x16x32_bf16 v[204:207],a[148:151],v[100:103],v[204:207]
ds_read_b64_tr_b16 a[232:233],v3 offset:512
v_mfma_f32_16x16x32_bf16 v[204:207],a[144:147],v[96:99],v[204:207]
ds_read_b64_tr_b16 a[234:235],v3 offset:4608
v_mfma_f32_16x16x32_bf16 v[212:215],a[160:163],v[96:99],v[40:43]
ds_read_b64_tr_b16 a[240:241],v2 offset:768
v_mfma_f32_16x16x32_bf16 v[212:215],a[164:167],v[100:103],v[212:215]
ds_read_b64_tr_b16 a[242:243],v2 offset:4864
ds_read_b64_tr_b16 a[244:245],v2 offset:8960
v_mfma_f32_16x16x32_bf16 v[212:215],a[168:171],v[104:107],v[212:215]
ds_read_b64_tr_b16 a[246:247],v2 offset:13056
v_mfma_f32_16x16x32_bf16 v[212:215],a[172:175],v[108:111],v[212:215]
ds_read_b64_tr_b16 a[252:253],v3 offset:8960
v_mfma_f32_16x16x32_bf16 v[220:223],a[188:191],v[108:111],v[40:43]
ds_read_b64_tr_b16 a[254:255],v3 offset:13056
v_mfma_f32_16x16x32_bf16 v[220:223],a[184:187],v[104:107],v[220:223]
ds_read_b64_tr_b16 a[248:249],v3 offset:768
ds_read_b64_tr_b16 a[250:251],v3 offset:4864
v_mfma_f32_16x16x32_bf16 v[220:223],a[180:183],v[100:103],v[220:223]
v_exp_f32 v164,v164
v_mfma_f32_16x16x32_bf16 v[220:223],a[176:179],v[96:99],v[220:223]
s_waitcnt lgkmcnt(8)
v_exp_f32 v165,v165
v_mfma_f32_16x16x32_bf16 a[0:3],a[192:195],v[132:135],a[0:3]
v_exp_f32 v180,v180
v_mfma_f32_16x16x32_bf16 a[0:3],a[196:199],v[148:151],a[0:3]
v_exp_f32 v181,v181
v_mfma_f32_16x16x32_bf16 a[4:7],a[204:207],v[148:151],a[4:7]
v_exp_f32 v166,v166
v_mfma_f32_16x16x32_bf16 a[4:7],a[200:203],v[132:135],a[4:7]
v_exp_f32 v167,v167
v_mfma_f32_16x16x32_bf16 a[8:11],a[208:211],v[132:135],a[8:11]
s_memrealtime s[76:77]
v_exp_f32 v182,v182
v_mfma_f32_16x16x32_bf16 a[8:11],a[212:215],v[148:151],a[8:11]
v_exp_f32 v183,v183
v_mfma_f32_16x16x32_bf16 a[12:15],a[220:223],v[148:151],a[12:15]
v_cvt_pk_bf16_f32 v164,v164,v165
v_cvt_pk_bf16_f32 v180,v180,v181
v_mfma_f32_16x16x32_bf16 a[12:15],a[216:219],v[132:135],a[12:15]
v_exp_f32 v172,v172
v_mfma_f32_16x16x32_bf16 a[16:19],a[224:227],v[132:135],a[16:19]
v_exp_f32 v173,v173
v_mfma_f32_16x16x32_bf16 a[16:19],a[228:231],v[148:151],a[16:19]
v_exp_f32 v174,v174
v_mfma_f32_16x16x32_bf16 a[20:23],a[236:239],v[148:151],a[20:23]
v_exp_f32 v175,v175
v_mfma_f32_16x16x32_bf16 a[20:23],a[232:235],v[132:135],a[20:23]
s_waitcnt lgkmcnt(0)
v_exp_f32 v188,v188
v_mfma_f32_16x16x32_bf16 a[24:27],a[240:243],v[132:135],a[24:27]
s_sub_u32 s79,s78,s76
v_exp_f32 v189,v189
v_mfma_f32_16x16x32_bf16 a[24:27],a[244:247],v[148:151],a[24:27]
s_add_u32 s78,s76,s60
v_exp_f32 v190,v190
v_mfma_f32_16x16x32_bf16 a[28:31],a[252:255],v[148:151],a[28:31]
s_cmp_gt_i32 s79,0
s_cbranch_scc1 L_ps1_%=
L_pr1_%=:
v_cvt_pk_bf16_f32 v165,v166,v167
v_mfma_f32_16x16x32_bf16 a[28:31],a[248:251],v[132:135],a[28:31]
v_exp_f32 v191,v191
v_mfma_f32_16x16x32_bf16 v[48:51],v[28:31],v[132:135],v[48:51]
v_cvt_pk_bf16_f32 v181,v182,v183
v_cvt_pk_bf16_f32 v166,v172,v173
v_cvt_pk_bf16_f32 v167,v174,v175
v_cvt_pk_bf16_f32 v182,v188,v189
v_cvt_pk_bf16_f32 v183,v190,v191
v_mfma_f32_16x16x32_bf16 v[48:51],v[28:31],v[148:151],v[48:51]
s_mov_b32 s53,13
L_att0_%=:
v_mfma_f32_16x16x32_bf16 v[228:231],a[128:131],v[112:115],v[44:47]
s_waitcnt vmcnt(16) lgkmcnt(0)
v_exp_f32 v196,v196
v_mfma_f32_16x16x32_bf16 v[228:231],a[132:135],v[116:119],v[228:231]
s_barrier
s_add_u32 m0,s49,s51
ds_read_b128 a[128:131],v1
v_mfma_f32_16x16x32_bf16 v[228:231],a[136:139],v[120:123],v[228:231]
s_mov_b32 s90,L_endseg_%=-L_pc_%=+128
v_exp_f32 v197,v197
v_mfma_f32_16x16x32_bf16 v[228:231],a[140:143],v[124:127],v[228:231]
s_add_u32 s54,s54,16384
ds_read_b128 a[132:135],v1 offset:256
v_cvt_pk_bf16_f32 v196,v196,v197
v_mfma_f32_16x16x32_bf16 v[236:239],a[156:159],v[124:127],v[44:47]
s_sub_u32 s53,s53,1
buffer_load_dwordx4 v4,s[20:23],s47 offen nt lds
v_mfma_f32_16x16x32_bf16 v[236:239],a[152:155],v[120:123],v[236:239]
s_and_b32 s46,s54,0xfffc0000
ds_read_b128 a[136:139],v1 offset:512
v_add_u32 v2,s48,v2
v_mfma_f32_16x16x32_bf16 v[236:239],a[148:151],v[116:119],v[236:239]
v_exp_f32 v198,v198
v_mfma_f32_16x16x32_bf16 v[236:239],a[144:147],v[112:115],v[236:239]
ds_read_b128 a[140:143],v1 offset:768
v_add_u32 v3,s48,v3
v_mfma_f32_16x16x32_bf16 v[244:247],a[160:163],v[112:115],v[44:47]
v_exp_f32 v199,v199
v_mfma_f32_16x16x32_bf16 v[244:247],a[164:167],v[116:119],v[244:247]
ds_read_b128 a[156:159],v1 offset:4864
v_cvt_pk_bf16_f32 v197,v198,v199
v_mfma_f32_16x16x32_bf16 v[244:247],a[168:171],v[120:123],v[244:247]
v_exp_f32 v204,v204
v_mfma_f32_16x16x32_bf16 v[244:247],a[172:175],v[124:127],v[244:247]
s_cmp_eq_u32 s50,131072
s_cselect_b32 s62,163840,0
ds_read_b128 a[152:155],v1 offset:4608
v_mfma_f32_16x16x32_bf16 v[252:255],a[188:191],v[124:127],v[44:47]
s_sub_u32 s48,32768,s62
v_exp_f32 v205,v205
v_mfma_f32_16x16x32_bf16 v[252:255],a[184:187],v[120:123],v[252:255]
s_add_u32 s50,s50,s48
ds_read_b128 a[148:151],v1 offset:4352
v_cvt_pk_bf16_f32 v198,v204,v205
v_mfma_f32_16x16x32_bf16 v[252:255],a[180:183],v[116:119],v[252:255]
v_exp_f32 v206,v206
v_mfma_f32_16x16x32_bf16 v[252:255],a[176:179],v[112:115],v[252:255]
ds_read_b128 a[144:147],v1 offset:4096
v_mfma_f32_16x16x32_bf16 a[32:35],a[192:195],v[164:167],a[32:35]
v_exp_f32 v207,v207
v_mfma_f32_16x16x32_bf16 a[32:35],a[196:199],v[180:183],a[32:35]
ds_read_b128 a[160:163],v1 offset:8192
v_cvt_pk_bf16_f32 v199,v206,v207
v_mfma_f32_16x16x32_bf16 a[36:39],a[204:207],v[180:183],a[36:39]
buffer_load_dwordx4 v4,s[20:23],s47 offen offset:1024 nt lds
v_mfma_f32_16x16x32_bf16 a[36:39],a[200:203],v[164:167],a[36:39]
ds_read_b128 a[164:167],v1 offset:8448
v_mfma_f32_16x16x32_bf16 a[40:43],a[208:211],v[164:167],a[40:43]
v_exp_f32 v212,v212
v_mfma_f32_16x16x32_bf16 a[40:43],a[212:215],v[180:183],a[40:43]
ds_read_b128 a[168:171],v1 offset:8704
v_mfma_f32_16x16x32_bf16 a[44:47],a[220:223],v[180:183],a[44:47]
v_exp_f32 v213,v213
v_mfma_f32_16x16x32_bf16 a[44:47],a[216:219],v[164:167],a[44:47]
ds_read_b128 a[172:175],v1 offset:8960
v_cvt_pk_bf16_f32 v212,v212,v213
v_mfma_f32_16x16x32_bf16 a[48:51],a[224:227],v[164:167],a[48:51]
v_exp_f32 v214,v214
v_mfma_f32_16x16x32_bf16 a[48:51],a[228:231],v[180:183],a[48:51]
ds_read_b128 a[188:191],v1 offset:13056
v_mfma_f32_16x16x32_bf16 a[52:55],a[236:239],v[180:183],a[52:55]
v_exp_f32 v215,v215
v_mfma_f32_16x16x32_bf16 a[52:55],a[232:235],v[164:167],a[52:55]
ds_read_b128 a[184:187],v1 offset:12800
v_cvt_pk_bf16_f32 v213,v214,v215
v_mfma_f32_16x16x32_bf16 a[56:59],a[240:243],v[164:167],a[56:59]
v_exp_f32 v220,v220
v_mfma_f32_16x16x32_bf16 a[56:59],a[244:247],v[180:183],a[56:59]
ds_read_b128 a[180:183],v1 offset:12544
v_mfma_f32_16x16x32_bf16 a[60:63],a[252:255],v[180:183],a[60:63]
v_exp_f32 v221,v221
v_mfma_f32_16x16x32_bf16 a[60:63],a[248:251],v[164:167],a[60:63]
ds_read_b128 a[176:179],v1 offset:12288
v_cvt_pk_bf16_f32 v214,v220,v221
v_mfma_f32_16x16x32_bf16 v[52:55],v[28:31],v[164:167],v[52:55]
buffer_load_dwordx4 v5,s[20:23],s47 offen offset:2048 nt lds
v_mfma_f32_16x16x32_bf16 v[52:55],v[28:31],v[180:183],v[52:55]
s_waitcnt lgkmcnt(3)
v_exp_f32 v222,v222
v_mfma_f32_16x16x32_bf16 v[132:135],a[128:131],v[64:67],v[32:35]
v_exp_f32 v223,v223
v_mfma_f32_16x16x32_bf16 v[132:135],a[132:135],v[68:71],v[132:135]
v_cvt_pk_bf16_f32 v215,v222,v223
v_add_u32 v1,s48,v1
v_mfma_f32_16x16x32_bf16 v[132:135],a[136:139],v[72:75],v[132:135]
v_exp_f32 v228,v228
v_mfma_f32_16x16x32_bf16 v[132:135],a[140:143],v[76:79],v[132:135]
v_exp_f32 v229,v229
v_mfma_f32_16x16x32_bf16 v[140:143],a[156:159],v[76:79],v[32:35]
v_exp_f32 v230,v230
v_mfma_f32_16x16x32_bf16 v[140:143],a[152:155],v[72:75],v[140:143]
v_exp_f32 v231,v231
v_mfma_f32_16x16x32_bf16 v[140:143],a[148:151],v[68:71],v[140:143]
v_cvt_pk_bf16_f32 v228,v228,v229
v_cvt_pk_bf16_f32 v229,v230,v231
v_mfma_f32_16x16x32_bf16 v[140:143],a[144:147],v[64:67],v[140:143]
v_exp_f32 v236,v236
v_mfma_f32_16x16x32_bf16 v[148:151],a[160:163],v[64:67],v[32:35]
v_exp_f32 v237,v237
v_mfma_f32_16x16x32_bf16 v[148:151],a[164:167],v[68:71],v[148:151]
)A"
R"A(v_exp_f32 v238,v238
v_mfma_f32_16x16x32_bf16 v[148:151],a[168:171],v[72:75],v[148:151]
v_exp_f32 v239,v239
v_mfma_f32_16x16x32_bf16 v[148:151],a[172:175],v[76:79],v[148:151]
v_exp_f32 v244,v244
v_mfma_f32_16x16x32_bf16 v[156:159],a[188:191],v[76:79],v[32:35]
s_waitcnt lgkmcnt(0)
buffer_load_dwordx4 v5,s[20:23],s47 offen offset:3072 nt lds
v_mfma_f32_16x16x32_bf16 v[156:159],a[184:187],v[72:75],v[156:159]
v_exp_f32 v245,v245
v_mfma_f32_16x16x32_bf16 v[156:159],a[180:183],v[68:71],v[156:159]
v_exp_f32 v246,v246
v_mfma_f32_16x16x32_bf16 v[156:159],a[176:179],v[64:67],v[156:159]
v_exp_f32 v247,v247
v_mfma_f32_16x16x32_bf16 a[64:67],a[192:195],v[196:199],a[64:67]
v_cvt_pk_bf16_f32 v230,v236,v237
v_cvt_pk_bf16_f32 v231,v238,v239
v_mfma_f32_16x16x32_bf16 a[64:67],a[196:199],v[212:215],a[64:67]
v_cvt_pk_bf16_f32 v244,v244,v245
v_cvt_pk_bf16_f32 v245,v246,v247
v_mfma_f32_16x16x32_bf16 a[68:71],a[204:207],v[212:215],a[68:71]
v_exp_f32 v252,v252
v_mfma_f32_16x16x32_bf16 a[68:71],a[200:203],v[196:199],a[68:71]
v_exp_f32 v253,v253
v_mfma_f32_16x16x32_bf16 a[72:75],a[208:211],v[196:199],a[72:75]
v_exp_f32 v254,v254
v_mfma_f32_16x16x32_bf16 a[72:75],a[212:215],v[212:215],a[72:75]
v_exp_f32 v255,v255
v_mfma_f32_16x16x32_bf16 a[76:79],a[220:223],v[212:215],a[76:79]
v_cvt_pk_bf16_f32 v246,v252,v253
v_cvt_pk_bf16_f32 v247,v254,v255
v_mfma_f32_16x16x32_bf16 a[76:79],a[216:219],v[196:199],a[76:79]
s_add_u32 m0,s49,s52
v_exp_f32 v132,v132
v_mfma_f32_16x16x32_bf16 a[80:83],a[224:227],v[196:199],a[80:83]
s_add_u32 s49,s49,32768
v_exp_f32 v133,v133
v_mfma_f32_16x16x32_bf16 a[80:83],a[228:231],v[212:215],a[80:83]
v_exp_f32 v134,v134
v_mfma_f32_16x16x32_bf16 a[84:87],a[236:239],v[212:215],a[84:87]
buffer_load_dwordx4 v4,s[24:27],s47 offen nt lds
v_mfma_f32_16x16x32_bf16 a[84:87],a[232:235],v[196:199],a[84:87]
v_exp_f32 v135,v135
v_mfma_f32_16x16x32_bf16 a[88:91],a[240:243],v[196:199],a[88:91]
v_cvt_pk_bf16_f32 v132,v132,v133
v_cvt_pk_bf16_f32 v133,v134,v135
v_mfma_f32_16x16x32_bf16 a[88:91],a[244:247],v[212:215],a[88:91]
v_exp_f32 v140,v140
v_mfma_f32_16x16x32_bf16 a[92:95],a[252:255],v[212:215],a[92:95]
v_exp_f32 v141,v141
v_mfma_f32_16x16x32_bf16 a[92:95],a[248:251],v[196:199],a[92:95]
v_exp_f32 v142,v142
v_mfma_f32_16x16x32_bf16 v[56:59],v[28:31],v[196:199],v[56:59]
v_exp_f32 v143,v143
v_mfma_f32_16x16x32_bf16 v[56:59],v[28:31],v[212:215],v[56:59]
v_exp_f32 v148,v148
v_mfma_f32_16x16x32_bf16 v[164:167],a[128:131],v[80:83],v[36:39]
v_exp_f32 v149,v149
v_mfma_f32_16x16x32_bf16 v[164:167],a[132:135],v[84:87],v[164:167]
v_exp_f32 v150,v150
v_mfma_f32_16x16x32_bf16 v[164:167],a[136:139],v[88:91],v[164:167]
v_exp_f32 v151,v151
v_mfma_f32_16x16x32_bf16 v[164:167],a[140:143],v[92:95],v[164:167]
v_cvt_pk_bf16_f32 v134,v140,v141
v_cvt_pk_bf16_f32 v135,v142,v143
v_mfma_f32_16x16x32_bf16 v[172:175],a[156:159],v[92:95],v[36:39]
v_cvt_pk_bf16_f32 v148,v148,v149
v_cvt_pk_bf16_f32 v149,v150,v151
v_mfma_f32_16x16x32_bf16 v[172:175],a[152:155],v[88:91],v[172:175]
v_exp_f32 v156,v156
v_mfma_f32_16x16x32_bf16 v[172:175],a[148:151],v[84:87],v[172:175]
buffer_load_dwordx4 v5,s[24:27],s47 offen offset:1024 nt lds
v_mfma_f32_16x16x32_bf16 v[172:175],a[144:147],v[80:83],v[172:175]
v_exp_f32 v157,v157
v_mfma_f32_16x16x32_bf16 v[180:183],a[160:163],v[80:83],v[36:39]
v_exp_f32 v158,v158
v_mfma_f32_16x16x32_bf16 v[180:183],a[164:167],v[84:87],v[180:183]
v_exp_f32 v159,v159
v_mfma_f32_16x16x32_bf16 v[180:183],a[168:171],v[88:91],v[180:183]
v_cvt_pk_bf16_f32 v150,v156,v157
v_cvt_pk_bf16_f32 v151,v158,v159
v_mfma_f32_16x16x32_bf16 v[180:183],a[172:175],v[92:95],v[180:183]
v_exp_f32 v164,v164
v_mfma_f32_16x16x32_bf16 v[188:191],a[188:191],v[92:95],v[36:39]
v_exp_f32 v165,v165
v_mfma_f32_16x16x32_bf16 v[188:191],a[184:187],v[88:91],v[188:191]
v_exp_f32 v166,v166
v_mfma_f32_16x16x32_bf16 v[188:191],a[180:183],v[84:87],v[188:191]
v_exp_f32 v167,v167
v_mfma_f32_16x16x32_bf16 v[188:191],a[176:179],v[80:83],v[188:191]
v_cvt_pk_bf16_f32 v164,v164,v165
v_cvt_pk_bf16_f32 v165,v166,v167
v_mfma_f32_16x16x32_bf16 a[96:99],a[192:195],v[228:231],a[96:99]
s_cmp_eq_u32 s49,163840
s_cselect_b32 s49,0,s49
ds_read_b64_tr_b16 a[192:193],v2
v_mfma_f32_16x16x32_bf16 a[96:99],a[196:199],v[244:247],a[96:99]
ds_read_b64_tr_b16 a[194:195],v2 offset:4096
v_mfma_f32_16x16x32_bf16 a[100:103],a[204:207],v[244:247],a[100:103]
ds_read_b64_tr_b16 a[196:197],v2 offset:8192
v_mfma_f32_16x16x32_bf16 a[100:103],a[200:203],v[228:231],a[100:103]
ds_read_b64_tr_b16 a[198:199],v2 offset:12288
v_mfma_f32_16x16x32_bf16 a[104:107],a[208:211],v[228:231],a[104:107]
buffer_load_dwordx4 v4,s[24:27],s47 offen offset:2048 nt lds
v_mfma_f32_16x16x32_bf16 a[104:107],a[212:215],v[244:247],a[104:107]
ds_read_b64_tr_b16 a[204:205],v3 offset:8192
v_mfma_f32_16x16x32_bf16 a[108:111],a[220:223],v[244:247],a[108:111]
ds_read_b64_tr_b16 a[206:207],v3 offset:12288
v_mfma_f32_16x16x32_bf16 a[108:111],a[216:219],v[228:231],a[108:111]
ds_read_b64_tr_b16 a[200:201],v3
v_mfma_f32_16x16x32_bf16 a[112:115],a[224:227],v[228:231],a[112:115]
ds_read_b64_tr_b16 a[202:203],v3 offset:4096
v_mfma_f32_16x16x32_bf16 a[112:115],a[228:231],v[244:247],a[112:115]
ds_read_b64_tr_b16 a[208:209],v2 offset:256
v_mfma_f32_16x16x32_bf16 a[116:119],a[236:239],v[244:247],a[116:119]
ds_read_b64_tr_b16 a[210:211],v2 offset:4352
v_mfma_f32_16x16x32_bf16 a[116:119],a[232:235],v[228:231],a[116:119]
ds_read_b64_tr_b16 a[212:213],v2 offset:8448
v_mfma_f32_16x16x32_bf16 a[120:123],a[240:243],v[228:231],a[120:123]
ds_read_b64_tr_b16 a[214:215],v2 offset:12544
v_mfma_f32_16x16x32_bf16 a[120:123],a[244:247],v[244:247],a[120:123]
ds_read_b64_tr_b16 a[220:221],v3 offset:8448
v_mfma_f32_16x16x32_bf16 a[124:127],a[252:255],v[244:247],a[124:127]
ds_read_b64_tr_b16 a[222:223],v3 offset:12544
v_mfma_f32_16x16x32_bf16 a[124:127],a[248:251],v[228:231],a[124:127]
ds_read_b64_tr_b16 a[216:217],v3 offset:256
v_mfma_f32_16x16x32_bf16 v[60:63],v[28:31],v[228:231],v[60:63]
ds_read_b64_tr_b16 a[218:219],v3 offset:4352
v_mfma_f32_16x16x32_bf16 v[60:63],v[28:31],v[244:247],v[60:63]
ds_read_b64_tr_b16 a[224:225],v2 offset:512
v_mfma_f32_16x16x32_bf16 v[196:199],a[128:131],v[96:99],v[40:43]
buffer_load_dwordx4 v5,s[24:27],s47 offen offset:3072 nt lds
v_mfma_f32_16x16x32_bf16 v[196:199],a[132:135],v[100:103],v[196:199]
s_add_u32 s47,s47,16384
s_and_b32 s47,s47,0x3ffff
ds_read_b64_tr_b16 a[226:227],v2 offset:4608
v_mfma_f32_16x16x32_bf16 v[196:199],a[136:139],v[104:107],v[196:199]
s_or_b32 s47,s47,s46
ds_read_b64_tr_b16 a[228:229],v2 offset:8704
v_mfma_f32_16x16x32_bf16 v[196:199],a[140:143],v[108:111],v[196:199]
ds_read_b64_tr_b16 a[230:231],v2 offset:12800
v_mfma_f32_16x16x32_bf16 v[204:207],a[156:159],v[108:111],v[40:43]
ds_read_b64_tr_b16 a[236:237],v3 offset:8704
v_mfma_f32_16x16x32_bf16 v[204:207],a[152:155],v[104:107],v[204:207]
ds_read_b64_tr_b16 a[238:239],v3 offset:12800
v_mfma_f32_16x16x32_bf16 v[204:207],a[148:151],v[100:103],v[204:207]
ds_read_b64_tr_b16 a[232:233],v3 offset:512
v_mfma_f32_16x16x32_bf16 v[204:207],a[144:147],v[96:99],v[204:207]
ds_read_b64_tr_b16 a[234:235],v3 offset:4608
v_mfma_f32_16x16x32_bf16 v[212:215],a[160:163],v[96:99],v[40:43]
ds_read_b64_tr_b16 a[240:241],v2 offset:768
v_mfma_f32_16x16x32_bf16 v[212:215],a[164:167],v[100:103],v[212:215]
ds_read_b64_tr_b16 a[242:243],v2 offset:4864
v_mfma_f32_16x16x32_bf16 v[212:215],a[168:171],v[104:107],v[212:215]
ds_read_b64_tr_b16 a[244:245],v2 offset:8960
v_mfma_f32_16x16x32_bf16 v[212:215],a[172:175],v[108:111],v[212:215]
ds_read_b64_tr_b16 a[246:247],v2 offset:13056
v_mfma_f32_16x16x32_bf16 v[220:223],a[188:191],v[108:111],v[40:43]
ds_read_b64_tr_b16 a[252:253],v3 offset:8960
v_mfma_f32_16x16x32_bf16 v[220:223],a[184:187],v[104:107],v[220:223]
ds_read_b64_tr_b16 a[254:255],v3 offset:13056
v_mfma_f32_16x16x32_bf16 v[220:223],a[180:183],v[100:103],v[220:223]
ds_read_b64_tr_b16 a[248:249],v3 offset:768
v_mfma_f32_16x16x32_bf16 v[220:223],a[176:179],v[96:99],v[220:223]
s_waitcnt lgkmcnt(7)
ds_read_b64_tr_b16 a[250:251],v3 offset:4864
v_mfma_f32_16x16x32_bf16 a[0:3],a[192:195],v[132:135],a[0:3]
buffer_load_dword v26,v24,s[88:91],s93 offen
v_mfma_f32_16x16x32_bf16 a[0:3],a[196:199],v[148:151],a[0:3]
v_exp_f32 v180,v180
v_mfma_f32_16x16x32_bf16 a[4:7],a[204:207],v[148:151],a[4:7]
v_exp_f32 v181,v181
v_mfma_f32_16x16x32_bf16 a[4:7],a[200:203],v[132:135],a[4:7]
v_exp_f32 v182,v182
v_mfma_f32_16x16x32_bf16 a[8:11],a[208:211],v[132:135],a[8:11]
s_memrealtime s[76:77]
v_exp_f32 v183,v183
v_mfma_f32_16x16x32_bf16 a[8:11],a[212:215],v[148:151],a[8:11]
v_exp_f32 v172,v172
v_mfma_f32_16x16x32_bf16 a[12:15],a[220:223],v[148:151],a[12:15]
v_exp_f32 v173,v173
v_mfma_f32_16x16x32_bf16 a[12:15],a[216:219],v[132:135],a[12:15]
v_exp_f32 v174,v174
v_mfma_f32_16x16x32_bf16 a[16:19],a[224:227],v[132:135],a[16:19]
v_exp_f32 v175,v175
v_mfma_f32_16x16x32_bf16 a[16:19],a[228:231],v[148:151],a[16:19]
v_cvt_pk_bf16_f32 v180,v180,v181
v_cvt_pk_bf16_f32 v181,v182,v183
v_mfma_f32_16x16x32_bf16 a[20:23],a[236:239],v[148:151],a[20:23]
v_exp_f32 v188,v188
v_mfma_f32_16x16x32_bf16 a[20:23],a[232:235],v[132:135],a[20:23]
s_waitcnt lgkmcnt(0)
v_exp_f32 v189,v189
v_mfma_f32_16x16x32_bf16 a[24:27],a[240:243],v[132:135],a[24:27]
s_sub_u32 s79,s78,s76
v_exp_f32 v190,v190
v_mfma_f32_16x16x32_bf16 a[24:27],a[244:247],v[148:151],a[24:27]
s_add_u32 s78,s76,s60
v_exp_f32 v191,v191
v_mfma_f32_16x16x32_bf16 a[28:31],a[252:255],v[148:151],a[28:31]
s_cmp_gt_i32 s79,0
s_cbranch_scc1 L_ps2_%=
L_pr2_%=:
v_cvt_pk_bf16_f32 v166,v172,v173
v_mfma_f32_16x16x32_bf16 a[28:31],a[248:251],v[132:135],a[28:31]
v_cvt_pk_bf16_f32 v167,v174,v175
v_cvt_pk_bf16_f32 v182,v188,v189
v_mfma_f32_16x16x32_bf16 v[48:51],v[28:31],v[132:135],v[48:51]
v_cvt_pk_bf16_f32 v183,v190,v191
s_cmp_lg_u32 s53,0
v_mfma_f32_16x16x32_bf16 v[48:51],v[28:31],v[148:151],v[48:51]
s_cbranch_scc1 L_att0_%=
v_mfma_f32_16x16x32_bf16 v[228:231],a[128:131],v[112:115],v[44:47]
s_waitcnt vmcnt(19)
s_add_u32 s54,s54,16384
v_exp_f32 v196,v196
v_mfma_f32_16x16x32_bf16 v[228:231],a[132:135],v[116:119],v[228:231]
s_barrier
s_add_u32 m0,s49,s51
ds_read_b128 a[128:131],v1
v_mfma_f32_16x16x32_bf16 v[228:231],a[136:139],v[120:123],v[228:231]
s_and_b32 s46,s54,0xfffc0000
ds_read_b128 a[132:135],v1 offset:256
v_and_b32 v18,48,v6
v_mfma_f32_16x16x32_bf16 v[228:231],a[140:143],v[124:127],v[228:231]
s_add_u32 s54,s54,16384
ds_read_b128 a[136:139],v1 offset:512
v_lshrrev_b32 v19,4,v6
v_mfma_f32_16x16x32_bf16 v[236:239],a[156:159],v[124:127],v[44:47]
s_add_u32 s84,s55,0
buffer_load_dwordx4 v4,s[20:23],s47 offen nt lds
v_mfma_f32_16x16x32_bf16 v[236:239],a[152:155],v[120:123],v[236:239]
s_add_u32 s85,s55,4096
ds_read_b128 a[140:143],v1 offset:768
v_lshlrev_b32 v18,2,v18
v_mfma_f32_16x16x32_bf16 v[236:239],a[148:151],v[116:119],v[236:239]
s_add_u32 s86,s55,8192
ds_read_b128 a[156:159],v1 offset:4864
v_and_b32 v19,48,v19
v_mfma_f32_16x16x32_bf16 v[236:239],a[144:147],v[112:115],v[236:239]
s_add_u32 s87,s55,12288
ds_read_b128 a[152:155],v1 offset:4608
v_add_u32 v18,v18,v19
v_mfma_f32_16x16x32_bf16 v[244:247],a[160:163],v[112:115],v[44:47]
s_max_i32 s84,s84,s18
ds_read_b128 a[148:151],v1 offset:4352
v_add_u32 v18,131072,v18
v_mfma_f32_16x16x32_bf16 v[244:247],a[164:167],v[116:119],v[244:247]
s_max_i32 s85,s85,s19
ds_read_b128 a[144:147],v1 offset:4096
v_add_u32 v2,s48,v2
v_mfma_f32_16x16x32_bf16 v[244:247],a[168:171],v[120:123],v[244:247]
s_max_i32 s86,s86,s100
ds_read_b128 a[160:163],v1 offset:8192
v_add_u32 v3,s48,v3
v_mfma_f32_16x16x32_bf16 v[244:247],a[172:175],v[124:127],v[244:247]
s_cmp_eq_u32 s50,131072
s_cselect_b32 s62,163840,0
ds_read_b128 a[164:167],v1 offset:8448
v_mfma_f32_16x16x32_bf16 v[252:255],a[188:191],v[124:127],v[44:47]
s_sub_u32 s48,32768,s62
s_add_u32 s50,s50,s48
ds_read_b128 a[168:171],v1 offset:8704
v_mfma_f32_16x16x32_bf16 v[252:255],a[184:187],v[120:123],v[252:255]
s_cmp_eq_u32 s50,131072
s_cselect_b32 s62,163840,0
ds_read_b128 a[172:175],v1 offset:8960
v_mfma_f32_16x16x32_bf16 v[252:255],a[180:183],v[116:119],v[252:255]
s_max_i32 s87,s87,s101
s_add_u32 s93,s92,L_endseg_%=-L_pc_%=
)A"
R"A(ds_read_b128 a[188:191],v1 offset:13056
v_mfma_f32_16x16x32_bf16 v[252:255],a[176:179],v[112:115],v[252:255]
s_mov_b32 s80,0
s_mov_b32 s83,s55
ds_read_b128 a[184:187],v1 offset:12800
v_mfma_f32_16x16x32_bf16 a[32:35],a[192:195],v[164:167],a[32:35]
ds_read_b128 a[180:183],v1 offset:12544
v_mfma_f32_16x16x32_bf16 a[32:35],a[196:199],v[180:183],a[32:35]
ds_read_b128 a[176:179],v1 offset:12288
v_add_u32 v1,s48,v1
v_mfma_f32_16x16x32_bf16 a[36:39],a[204:207],v[180:183],a[36:39]
buffer_load_dwordx4 v4,s[20:23],s47 offen offset:1024 nt lds
v_mfma_f32_16x16x32_bf16 a[36:39],a[200:203],v[164:167],a[36:39]
v_exp_f32 v197,v197
v_mfma_f32_16x16x32_bf16 a[40:43],a[208:211],v[164:167],a[40:43]
v_exp_f32 v198,v198
v_mfma_f32_16x16x32_bf16 a[40:43],a[212:215],v[180:183],a[40:43]
v_exp_f32 v199,v199
v_mfma_f32_16x16x32_bf16 a[44:47],a[220:223],v[180:183],a[44:47]
v_cvt_pk_bf16_f32 v196,v196,v197
v_cvt_pk_bf16_f32 v197,v198,v199
v_mfma_f32_16x16x32_bf16 a[44:47],a[216:219],v[164:167],a[44:47]
v_exp_f32 v204,v204
v_mfma_f32_16x16x32_bf16 a[48:51],a[224:227],v[164:167],a[48:51]
v_exp_f32 v205,v205
v_mfma_f32_16x16x32_bf16 a[48:51],a[228:231],v[180:183],a[48:51]
v_exp_f32 v206,v206
v_mfma_f32_16x16x32_bf16 a[52:55],a[236:239],v[180:183],a[52:55]
v_exp_f32 v207,v207
v_mfma_f32_16x16x32_bf16 a[52:55],a[232:235],v[164:167],a[52:55]
v_exp_f32 v212,v212
v_mfma_f32_16x16x32_bf16 a[56:59],a[240:243],v[164:167],a[56:59]
v_exp_f32 v213,v213
v_mfma_f32_16x16x32_bf16 a[56:59],a[244:247],v[180:183],a[56:59]
v_exp_f32 v214,v214
v_mfma_f32_16x16x32_bf16 a[60:63],a[252:255],v[180:183],a[60:63]
v_exp_f32 v215,v215
v_mfma_f32_16x16x32_bf16 a[60:63],a[248:251],v[164:167],a[60:63]
v_cvt_pk_bf16_f32 v198,v204,v205
v_cvt_pk_bf16_f32 v199,v206,v207
v_mfma_f32_16x16x32_bf16 v[52:55],v[28:31],v[164:167],v[52:55]
buffer_load_dwordx4 v5,s[20:23],s47 offen offset:2048 nt lds
v_mfma_f32_16x16x32_bf16 v[52:55],v[28:31],v[180:183],v[52:55]
s_waitcnt lgkmcnt(0)
v_cvt_pk_bf16_f32 v212,v212,v213
v_cvt_pk_bf16_f32 v213,v214,v215
v_mfma_f32_16x16x32_bf16 v[132:135],a[128:131],v[64:67],v[32:35]
v_exp_f32 v220,v220
v_mfma_f32_16x16x32_bf16 v[132:135],a[132:135],v[68:71],v[132:135]
v_exp_f32 v221,v221
v_mfma_f32_16x16x32_bf16 v[132:135],a[136:139],v[72:75],v[132:135]
v_exp_f32 v222,v222
v_mfma_f32_16x16x32_bf16 v[132:135],a[140:143],v[76:79],v[132:135]
v_exp_f32 v223,v223
v_mfma_f32_16x16x32_bf16 v[140:143],a[156:159],v[76:79],v[32:35]
v_cvt_pk_bf16_f32 v214,v220,v221
v_cvt_pk_bf16_f32 v215,v222,v223
v_mfma_f32_16x16x32_bf16 v[140:143],a[152:155],v[72:75],v[140:143]
v_exp_f32 v228,v228
v_mfma_f32_16x16x32_bf16 v[140:143],a[148:151],v[68:71],v[140:143]
v_exp_f32 v229,v229
v_mfma_f32_16x16x32_bf16 v[140:143],a[144:147],v[64:67],v[140:143]
v_exp_f32 v230,v230
v_mfma_f32_16x16x32_bf16 v[148:151],a[160:163],v[64:67],v[32:35]
v_exp_f32 v231,v231
v_mfma_f32_16x16x32_bf16 v[148:151],a[164:167],v[68:71],v[148:151]
v_cvt_pk_bf16_f32 v228,v228,v229
v_cvt_pk_bf16_f32 v229,v230,v231
v_mfma_f32_16x16x32_bf16 v[148:151],a[168:171],v[72:75],v[148:151]
v_exp_f32 v236,v236
v_mfma_f32_16x16x32_bf16 v[148:151],a[172:175],v[76:79],v[148:151]
v_exp_f32 v237,v237
v_mfma_f32_16x16x32_bf16 v[156:159],a[188:191],v[76:79],v[32:35]
buffer_load_dwordx4 v5,s[20:23],s47 offen offset:3072 nt lds
v_mfma_f32_16x16x32_bf16 v[156:159],a[184:187],v[72:75],v[156:159]
v_exp_f32 v238,v238
v_mfma_f32_16x16x32_bf16 v[156:159],a[180:183],v[68:71],v[156:159]
v_exp_f32 v239,v239
v_mfma_f32_16x16x32_bf16 v[156:159],a[176:179],v[64:67],v[156:159]
v_exp_f32 v244,v244
v_mfma_f32_16x16x32_bf16 a[64:67],a[192:195],v[196:199],a[64:67]
v_exp_f32 v245,v245
v_mfma_f32_16x16x32_bf16 a[64:67],a[196:199],v[212:215],a[64:67]
v_exp_f32 v246,v246
v_mfma_f32_16x16x32_bf16 a[68:71],a[204:207],v[212:215],a[68:71]
v_exp_f32 v247,v247
v_mfma_f32_16x16x32_bf16 a[68:71],a[200:203],v[196:199],a[68:71]
v_cvt_pk_bf16_f32 v230,v236,v237
v_cvt_pk_bf16_f32 v231,v238,v239
v_mfma_f32_16x16x32_bf16 a[72:75],a[208:211],v[196:199],a[72:75]
v_cvt_pk_bf16_f32 v244,v244,v245
v_cvt_pk_bf16_f32 v245,v246,v247
v_mfma_f32_16x16x32_bf16 a[72:75],a[212:215],v[212:215],a[72:75]
v_exp_f32 v252,v252
v_mfma_f32_16x16x32_bf16 a[76:79],a[220:223],v[212:215],a[76:79]
v_exp_f32 v253,v253
v_mfma_f32_16x16x32_bf16 a[76:79],a[216:219],v[196:199],a[76:79]
s_add_u32 m0,s49,s52
v_exp_f32 v254,v254
v_mfma_f32_16x16x32_bf16 a[80:83],a[224:227],v[196:199],a[80:83]
s_add_u32 s49,s49,32768
v_exp_f32 v255,v255
v_mfma_f32_16x16x32_bf16 a[80:83],a[228:231],v[212:215],a[80:83]
v_cvt_pk_bf16_f32 v246,v252,v253
v_cvt_pk_bf16_f32 v247,v254,v255
v_mfma_f32_16x16x32_bf16 a[84:87],a[236:239],v[212:215],a[84:87]
buffer_load_dwordx4 v4,s[24:27],s47 offen nt lds
v_mfma_f32_16x16x32_bf16 a[84:87],a[232:235],v[196:199],a[84:87]
v_exp_f32 v132,v132
v_mfma_f32_16x16x32_bf16 a[88:91],a[240:243],v[196:199],a[88:91]
v_exp_f32 v133,v133
v_mfma_f32_16x16x32_bf16 a[88:91],a[244:247],v[212:215],a[88:91]
v_exp_f32 v134,v134
v_mfma_f32_16x16x32_bf16 a[92:95],a[252:255],v[212:215],a[92:95]
v_exp_f32 v135,v135
v_mfma_f32_16x16x32_bf16 a[92:95],a[248:251],v[196:199],a[92:95]
v_cvt_pk_bf16_f32 v132,v132,v133
v_cvt_pk_bf16_f32 v133,v134,v135
v_mfma_f32_16x16x32_bf16 v[56:59],v[28:31],v[196:199],v[56:59]
v_exp_f32 v140,v140
v_mfma_f32_16x16x32_bf16 v[56:59],v[28:31],v[212:215],v[56:59]
v_exp_f32 v141,v141
v_mfma_f32_16x16x32_bf16 v[164:167],a[128:131],v[80:83],v[36:39]
v_exp_f32 v142,v142
v_mfma_f32_16x16x32_bf16 v[164:167],a[132:135],v[84:87],v[164:167]
v_exp_f32 v143,v143
v_mfma_f32_16x16x32_bf16 v[164:167],a[136:139],v[88:91],v[164:167]
v_exp_f32 v148,v148
v_mfma_f32_16x16x32_bf16 v[164:167],a[140:143],v[92:95],v[164:167]
v_exp_f32 v149,v149
v_mfma_f32_16x16x32_bf16 v[172:175],a[156:159],v[92:95],v[36:39]
v_exp_f32 v150,v150
v_mfma_f32_16x16x32_bf16 v[172:175],a[152:155],v[88:91],v[172:175]
v_exp_f32 v151,v151
v_mfma_f32_16x16x32_bf16 v[172:175],a[148:151],v[84:87],v[172:175]
buffer_load_dwordx4 v5,s[24:27],s47 offen offset:1024 nt lds
v_mfma_f32_16x16x32_bf16 v[172:175],a[144:147],v[80:83],v[172:175]
v_cvt_pk_bf16_f32 v134,v140,v141
v_cvt_pk_bf16_f32 v135,v142,v143
v_mfma_f32_16x16x32_bf16 v[180:183],a[160:163],v[80:83],v[36:39]
v_cvt_pk_bf16_f32 v148,v148,v149
v_cvt_pk_bf16_f32 v149,v150,v151
v_mfma_f32_16x16x32_bf16 v[180:183],a[164:167],v[84:87],v[180:183]
v_exp_f32 v156,v156
v_mfma_f32_16x16x32_bf16 v[180:183],a[168:171],v[88:91],v[180:183]
v_exp_f32 v157,v157
v_mfma_f32_16x16x32_bf16 v[180:183],a[172:175],v[92:95],v[180:183]
v_exp_f32 v158,v158
v_mfma_f32_16x16x32_bf16 v[188:191],a[188:191],v[92:95],v[36:39]
v_exp_f32 v159,v159
v_mfma_f32_16x16x32_bf16 v[188:191],a[184:187],v[88:91],v[188:191]
v_cvt_pk_bf16_f32 v150,v156,v157
v_cvt_pk_bf16_f32 v151,v158,v159
v_mfma_f32_16x16x32_bf16 v[188:191],a[180:183],v[84:87],v[188:191]
s_waitcnt vmcnt(23)
s_cmp_eq_u32 s49,163840
s_cselect_b32 s49,0,s49
v_mfma_f32_16x16x32_bf16 v[188:191],a[176:179],v[80:83],v[188:191]
s_barrier
ds_read_b128 v[80:83],v18
v_mfma_f32_16x16x32_bf16 a[96:99],a[192:195],v[228:231],a[96:99]
ds_read_b128 v[84:87],v18 offset:256
v_mfma_f32_16x16x32_bf16 a[96:99],a[196:199],v[244:247],a[96:99]
ds_read_b128 v[88:91],v18 offset:512
v_mfma_f32_16x16x32_bf16 a[100:103],a[204:207],v[244:247],a[100:103]
ds_read_b128 v[92:95],v18 offset:768
v_mfma_f32_16x16x32_bf16 a[100:103],a[200:203],v[228:231],a[100:103]
buffer_load_dwordx4 v4,s[24:27],s47 offen offset:2048 nt lds
v_mfma_f32_16x16x32_bf16 a[104:107],a[208:211],v[228:231],a[104:107]
ds_read_b64_tr_b16 a[192:193],v2
ds_read_b64_tr_b16 a[194:195],v2 offset:4096
v_mfma_f32_16x16x32_bf16 a[104:107],a[212:215],v[244:247],a[104:107]
ds_read_b64_tr_b16 a[196:197],v2 offset:8192
ds_read_b64_tr_b16 a[198:199],v2 offset:12288
v_mfma_f32_16x16x32_bf16 a[108:111],a[220:223],v[244:247],a[108:111]
ds_read_b64_tr_b16 a[204:205],v3 offset:8192
ds_read_b64_tr_b16 a[206:207],v3 offset:12288
v_mfma_f32_16x16x32_bf16 a[108:111],a[216:219],v[228:231],a[108:111]
ds_read_b64_tr_b16 a[200:201],v3
ds_read_b64_tr_b16 a[202:203],v3 offset:4096
v_mfma_f32_16x16x32_bf16 a[112:115],a[224:227],v[228:231],a[112:115]
ds_read_b64_tr_b16 a[208:209],v2 offset:256
ds_read_b64_tr_b16 a[210:211],v2 offset:4352
v_mfma_f32_16x16x32_bf16 a[112:115],a[228:231],v[244:247],a[112:115]
ds_read_b64_tr_b16 a[212:213],v2 offset:8448
ds_read_b64_tr_b16 a[214:215],v2 offset:12544
v_mfma_f32_16x16x32_bf16 a[116:119],a[236:239],v[244:247],a[116:119]
ds_read_b64_tr_b16 a[220:221],v3 offset:8448
ds_read_b64_tr_b16 a[222:223],v3 offset:12544
v_mfma_f32_16x16x32_bf16 a[116:119],a[232:235],v[228:231],a[116:119]
ds_read_b64_tr_b16 a[216:217],v3 offset:256
ds_read_b64_tr_b16 a[218:219],v3 offset:4352
v_mfma_f32_16x16x32_bf16 a[120:123],a[240:243],v[228:231],a[120:123]
ds_read_b64_tr_b16 a[224:225],v2 offset:512
ds_read_b64_tr_b16 a[226:227],v2 offset:4608
v_mfma_f32_16x16x32_bf16 a[120:123],a[244:247],v[244:247],a[120:123]
ds_read_b64_tr_b16 a[228:229],v2 offset:8704
ds_read_b64_tr_b16 a[230:231],v2 offset:12800
v_mfma_f32_16x16x32_bf16 a[124:127],a[252:255],v[244:247],a[124:127]
ds_read_b64_tr_b16 a[236:237],v3 offset:8704
ds_read_b64_tr_b16 a[238:239],v3 offset:12800
v_mfma_f32_16x16x32_bf16 a[124:127],a[248:251],v[228:231],a[124:127]
ds_read_b64_tr_b16 a[232:233],v3 offset:512
ds_read_b64_tr_b16 a[234:235],v3 offset:4608
v_mfma_f32_16x16x32_bf16 v[60:63],v[28:31],v[228:231],v[60:63]
ds_read_b64_tr_b16 a[240:241],v2 offset:768
ds_read_b64_tr_b16 a[242:243],v2 offset:4864
v_mfma_f32_16x16x32_bf16 v[60:63],v[28:31],v[244:247],v[60:63]
buffer_load_dwordx4 v5,s[24:27],s47 offen offset:3072 nt lds
v_mfma_f32_16x16x32_bf16 v[196:199],a[128:131],v[96:99],v[40:43]
s_add_u32 s47,s47,16384
ds_read_b64_tr_b16 a[244:245],v2 offset:8960
ds_read_b64_tr_b16 a[246:247],v2 offset:13056
v_mfma_f32_16x16x32_bf16 v[196:199],a[132:135],v[100:103],v[196:199]
s_and_b32 s47,s47,0x3ffff
ds_read_b64_tr_b16 a[252:253],v3 offset:8960
ds_read_b64_tr_b16 a[254:255],v3 offset:13056
v_mfma_f32_16x16x32_bf16 v[196:199],a[136:139],v[104:107],v[196:199]
s_or_b32 s47,s47,s46
ds_read_b64_tr_b16 a[248:249],v3 offset:768
ds_read_b64_tr_b16 a[250:251],v3 offset:4864
v_mfma_f32_16x16x32_bf16 v[196:199],a[140:143],v[108:111],v[196:199]
s_and_b32 s46,s54,0xfffc0000
v_exp_f32 v164,v164
v_mfma_f32_16x16x32_bf16 v[204:207],a[156:159],v[108:111],v[40:43]
s_add_u32 s54,s54,16384
v_exp_f32 v165,v165
v_mfma_f32_16x16x32_bf16 v[204:207],a[152:155],v[104:107],v[204:207]
v_exp_f32 v166,v166
v_mfma_f32_16x16x32_bf16 v[204:207],a[148:151],v[100:103],v[204:207]
v_exp_f32 v167,v167
v_mfma_f32_16x16x32_bf16 v[204:207],a[144:147],v[96:99],v[204:207]
v_cvt_pk_bf16_f32 v164,v164,v165
v_cvt_pk_bf16_f32 v165,v166,v167
v_mfma_f32_16x16x32_bf16 v[212:215],a[160:163],v[96:99],v[40:43]
v_exp_f32 v172,v172
v_mfma_f32_16x16x32_bf16 v[212:215],a[164:167],v[100:103],v[212:215]
v_exp_f32 v173,v173
v_mfma_f32_16x16x32_bf16 v[212:215],a[168:171],v[104:107],v[212:215]
v_exp_f32 v174,v174
v_mfma_f32_16x16x32_bf16 v[212:215],a[172:175],v[108:111],v[212:215]
v_exp_f32 v175,v175
v_mfma_f32_16x16x32_bf16 v[220:223],a[188:191],v[108:111],v[40:43]
v_exp_f32 v180,v180
v_mfma_f32_16x16x32_bf16 v[220:223],a[184:187],v[104:107],v[220:223]
v_exp_f32 v181,v181
v_mfma_f32_16x16x32_bf16 v[220:223],a[180:183],v[100:103],v[220:223]
v_exp_f32 v182,v182
v_mfma_f32_16x16x32_bf16 v[220:223],a[176:179],v[96:99],v[220:223]
s_waitcnt lgkmcnt(0)
v_exp_f32 v183,v183
v_mfma_f32_16x16x32_bf16 v[32:35],v[80:83],v[128:131],0
v_cvt_pk_bf16_f32 v166,v172,v173
v_cvt_pk_bf16_f32 v167,v174,v175
v_mfma_f32_16x16x32_bf16 v[32:35],v[84:87],v[160:163],v[32:35]
v_cvt_pk_bf16_f32 v180,v180,v181
v_cvt_pk_bf16_f32 v181,v182,v183
v_mfma_f32_16x16x32_bf16 v[32:35],v[88:91],v[192:195],v[32:35]
v_exp_f32 v188,v188
v_mfma_f32_16x16x32_bf16 v[32:35],v[92:95],v[224:227],v[32:35]
v_exp_f32 v189,v189
v_mfma_f32_16x16x32_bf16 v[36:39],v[80:83],v[136:139],0
v_exp_f32 v190,v190
v_mfma_f32_16x16x32_bf16 v[36:39],v[84:87],v[168:171],v[36:39]
v_exp_f32 v191,v191
v_mfma_f32_16x16x32_bf16 v[36:39],v[88:91],v[200:203],v[36:39]
v_cvt_pk_bf16_f32 v182,v188,v189
)A"
R"A(v_cvt_pk_bf16_f32 v183,v190,v191
v_mfma_f32_16x16x32_bf16 v[36:39],v[92:95],v[232:235],v[36:39]
v_exp_f32 v196,v196
v_mfma_f32_16x16x32_bf16 v[40:43],v[80:83],v[144:147],0
v_exp_f32 v197,v197
v_mfma_f32_16x16x32_bf16 v[40:43],v[84:87],v[176:179],v[40:43]
v_max3_f32 v32,v32,v33,v34
v_max3_f32 v32,v32,v35,s18
v_mfma_f32_16x16x32_bf16 v[40:43],v[88:91],v[208:211],v[40:43]
v_sub_f32 v35,0,v32
v_sub_f32 v34,0,v32
v_mfma_f32_16x16x32_bf16 v[40:43],v[92:95],v[240:243],v[40:43]
v_sub_f32 v33,0,v32
v_sub_f32 v32,0,v32
v_mfma_f32_16x16x32_bf16 a[0:3],a[192:195],v[132:135],a[0:3]
v_exp_f32 v198,v198
v_mfma_f32_16x16x32_bf16 a[0:3],a[196:199],v[148:151],a[0:3]
v_exp_f32 v199,v199
v_mfma_f32_16x16x32_bf16 a[4:7],a[204:207],v[148:151],a[4:7]
v_cvt_pk_bf16_f32 v196,v196,v197
v_cvt_pk_bf16_f32 v197,v198,v199
v_mfma_f32_16x16x32_bf16 a[4:7],a[200:203],v[132:135],a[4:7]
v_exp_f32 v204,v204
v_mfma_f32_16x16x32_bf16 a[8:11],a[208:211],v[132:135],a[8:11]
s_memrealtime s[76:77]
v_exp_f32 v205,v205
v_mfma_f32_16x16x32_bf16 a[8:11],a[212:215],v[148:151],a[8:11]
v_exp_f32 v206,v206
v_mfma_f32_16x16x32_bf16 a[12:15],a[220:223],v[148:151],a[12:15]
v_exp_f32 v207,v207
v_mfma_f32_16x16x32_bf16 a[12:15],a[216:219],v[132:135],a[12:15]
v_exp_f32 v212,v212
v_mfma_f32_16x16x32_bf16 a[16:19],a[224:227],v[132:135],a[16:19]
v_exp_f32 v213,v213
v_mfma_f32_16x16x32_bf16 a[16:19],a[228:231],v[148:151],a[16:19]
v_exp_f32 v214,v214
v_mfma_f32_16x16x32_bf16 a[20:23],a[236:239],v[148:151],a[20:23]
v_exp_f32 v215,v215
v_mfma_f32_16x16x32_bf16 a[20:23],a[232:235],v[132:135],a[20:23]
s_waitcnt lgkmcnt(0)
v_cvt_pk_bf16_f32 v198,v204,v205
v_cvt_pk_bf16_f32 v199,v206,v207
v_mfma_f32_16x16x32_bf16 a[24:27],a[240:243],v[132:135],a[24:27]
s_sub_u32 s79,s78,s76
v_cvt_pk_bf16_f32 v212,v212,v213
v_cvt_pk_bf16_f32 v213,v214,v215
v_mfma_f32_16x16x32_bf16 a[24:27],a[244:247],v[148:151],a[24:27]
s_add_u32 s78,s76,s60
v_exp_f32 v220,v220
v_mfma_f32_16x16x32_bf16 a[28:31],a[252:255],v[148:151],a[28:31]
s_cmp_gt_i32 s79,0
s_cbranch_scc1 L_ps3_%=
L_pr3_%=:
v_max3_f32 v36,v36,v37,v38
v_mfma_f32_16x16x32_bf16 a[28:31],a[248:251],v[132:135],a[28:31]
v_exp_f32 v221,v221
v_mfma_f32_16x16x32_bf16 v[48:51],v[28:31],v[132:135],v[48:51]
v_exp_f32 v222,v222
v_mfma_f32_16x16x32_bf16 v[48:51],v[28:31],v[148:151],v[48:51]
v_exp_f32 v223,v223
v_mfma_f32_16x16x32_bf16 v[228:231],a[128:131],v[112:115],v[44:47]
s_waitcnt vmcnt(18)
v_cvt_pk_bf16_f32 v214,v220,v221
v_cvt_pk_bf16_f32 v215,v222,v223
v_mfma_f32_16x16x32_bf16 v[228:231],a[132:135],v[116:119],v[228:231]
s_barrier
s_add_u32 m0,s49,s51
ds_read_b128 a[128:131],v1
v_mfma_f32_16x16x32_bf16 v[228:231],a[136:139],v[120:123],v[228:231]
ds_read_b128 a[132:135],v1 offset:256
v_max3_f32 v36,v36,v39,s19
v_mfma_f32_16x16x32_bf16 v[228:231],a[140:143],v[124:127],v[228:231]
ds_read_b128 a[136:139],v1 offset:512
v_sub_f32 v39,0,v36
v_mfma_f32_16x16x32_bf16 v[236:239],a[156:159],v[124:127],v[44:47]
buffer_load_dwordx4 v4,s[20:23],s47 offen nt lds
v_mfma_f32_16x16x32_bf16 v[236:239],a[152:155],v[120:123],v[236:239]
ds_read_b128 a[140:143],v1 offset:768
v_sub_f32 v38,0,v36
v_mfma_f32_16x16x32_bf16 v[236:239],a[148:151],v[116:119],v[236:239]
ds_read_b128 a[156:159],v1 offset:4864
v_sub_f32 v37,0,v36
v_mfma_f32_16x16x32_bf16 v[236:239],a[144:147],v[112:115],v[236:239]
ds_read_b128 a[152:155],v1 offset:4608
v_sub_f32 v36,0,v36
v_mfma_f32_16x16x32_bf16 v[244:247],a[160:163],v[112:115],v[44:47]
ds_read_b128 a[148:151],v1 offset:4352
v_max3_f32 v40,v40,v41,v42
v_mfma_f32_16x16x32_bf16 v[244:247],a[164:167],v[116:119],v[244:247]
ds_read_b128 a[144:147],v1 offset:4096
v_max3_f32 v40,v40,v43,s100
v_mfma_f32_16x16x32_bf16 v[244:247],a[168:171],v[120:123],v[244:247]
ds_read_b128 a[160:163],v1 offset:8192
v_sub_f32 v43,0,v40
v_mfma_f32_16x16x32_bf16 v[244:247],a[172:175],v[124:127],v[244:247]
ds_read_b128 a[164:167],v1 offset:8448
v_sub_f32 v42,0,v40
v_mfma_f32_16x16x32_bf16 v[252:255],a[188:191],v[124:127],v[44:47]
ds_read_b128 a[168:171],v1 offset:8704
v_sub_f32 v41,0,v40
v_mfma_f32_16x16x32_bf16 v[252:255],a[184:187],v[120:123],v[252:255]
ds_read_b128 a[172:175],v1 offset:8960
v_sub_f32 v40,0,v40
v_mfma_f32_16x16x32_bf16 v[252:255],a[180:183],v[116:119],v[252:255]
ds_read_b128 a[188:191],v1 offset:13056
v_add_u32 v2,s48,v2
v_mfma_f32_16x16x32_bf16 v[252:255],a[176:179],v[112:115],v[252:255]
ds_read_b128 a[184:187],v1 offset:12800
v_add_u32 v3,s48,v3
v_mfma_f32_16x16x32_bf16 v[44:47],v[80:83],v[152:155],0
s_sub_u32 s48,32768,s62
ds_read_b128 a[180:183],v1 offset:12544
v_max_f32 v20,0x00800000,v48
v_mfma_f32_16x16x32_bf16 v[44:47],v[84:87],v[184:187],v[44:47]
s_add_u32 s50,s50,s48
ds_read_b128 a[176:179],v1 offset:12288
v_accvgpr_read_b32 v64,a0
v_mfma_f32_16x16x32_bf16 v[44:47],v[88:91],v[216:219],v[44:47]
buffer_load_dwordx4 v4,s[20:23],s47 offen offset:1024 nt lds
v_mfma_f32_16x16x32_bf16 v[44:47],v[92:95],v[248:251],v[44:47]
v_exp_f32 v228,v228
v_mfma_f32_16x16x32_bf16 a[32:35],a[192:195],v[164:167],a[32:35]
v_exp_f32 v229,v229
v_mfma_f32_16x16x32_bf16 a[32:35],a[196:199],v[180:183],a[32:35]
v_exp_f32 v230,v230
v_mfma_f32_16x16x32_bf16 a[36:39],a[204:207],v[180:183],a[36:39]
v_exp_f32 v231,v231
v_mfma_f32_16x16x32_bf16 a[36:39],a[200:203],v[164:167],a[36:39]
v_cvt_pk_bf16_f32 v228,v228,v229
v_cvt_pk_bf16_f32 v229,v230,v231
v_mfma_f32_16x16x32_bf16 a[40:43],a[208:211],v[164:167],a[40:43]
v_exp_f32 v236,v236
v_mfma_f32_16x16x32_bf16 a[40:43],a[212:215],v[180:183],a[40:43]
v_exp_f32 v237,v237
v_mfma_f32_16x16x32_bf16 a[44:47],a[220:223],v[180:183],a[44:47]
v_exp_f32 v238,v238
v_mfma_f32_16x16x32_bf16 a[44:47],a[216:219],v[164:167],a[44:47]
v_exp_f32 v239,v239
v_mfma_f32_16x16x32_bf16 a[48:51],a[224:227],v[164:167],a[48:51]
v_exp_f32 v244,v244
v_mfma_f32_16x16x32_bf16 a[48:51],a[228:231],v[180:183],a[48:51]
v_exp_f32 v245,v245
v_mfma_f32_16x16x32_bf16 a[52:55],a[236:239],v[180:183],a[52:55]
v_exp_f32 v246,v246
v_mfma_f32_16x16x32_bf16 a[52:55],a[232:235],v[164:167],a[52:55]
v_exp_f32 v247,v247
v_mfma_f32_16x16x32_bf16 a[56:59],a[240:243],v[164:167],a[56:59]
buffer_load_dwordx4 v5,s[20:23],s47 offen offset:2048 nt lds
v_mfma_f32_16x16x32_bf16 a[56:59],a[244:247],v[180:183],a[56:59]
v_cvt_pk_bf16_f32 v230,v236,v237
v_cvt_pk_bf16_f32 v231,v238,v239
v_mfma_f32_16x16x32_bf16 a[60:63],a[252:255],v[180:183],a[60:63]
v_cvt_pk_bf16_f32 v244,v244,v245
v_cvt_pk_bf16_f32 v245,v246,v247
v_mfma_f32_16x16x32_bf16 a[60:63],a[248:251],v[164:167],a[60:63]
v_exp_f32 v252,v252
v_mfma_f32_16x16x32_bf16 v[52:55],v[28:31],v[164:167],v[52:55]
v_exp_f32 v253,v253
v_mfma_f32_16x16x32_bf16 v[52:55],v[28:31],v[180:183],v[52:55]
s_waitcnt lgkmcnt(0)
v_exp_f32 v254,v254
v_mfma_f32_16x16x32_bf16 v[132:135],a[128:131],v[128:131],v[32:35]
v_exp_f32 v255,v255
v_mfma_f32_16x16x32_bf16 v[132:135],a[132:135],v[160:163],v[132:135]
v_cvt_pk_bf16_f32 v246,v252,v253
v_cvt_pk_bf16_f32 v247,v254,v255
v_mfma_f32_16x16x32_bf16 v[132:135],a[136:139],v[192:195],v[132:135]
v_rcp_f32 v20,v20
v_mfma_f32_16x16x32_bf16 v[132:135],a[140:143],v[224:227],v[132:135]
v_accvgpr_read_b32 v65,a1
v_accvgpr_read_b32 v66,a2
v_mfma_f32_16x16x32_bf16 v[140:143],a[156:159],v[224:227],v[32:35]
v_accvgpr_read_b32 v67,a3
v_mul_f32 v64,v20,v64
v_mfma_f32_16x16x32_bf16 v[140:143],a[152:155],v[192:195],v[140:143]
v_mul_f32 v65,v20,v65
v_cvt_pk_bf16_f32 v64,v64,v65
v_mfma_f32_16x16x32_bf16 v[140:143],a[148:151],v[160:163],v[140:143]
v_mul_f32 v66,v20,v66
v_mul_f32 v67,v20,v67
v_mfma_f32_16x16x32_bf16 v[140:143],a[144:147],v[128:131],v[140:143]
v_cvt_pk_bf16_f32 v65,v66,v67
v_accvgpr_read_b32 v14,a6
v_mfma_f32_16x16x32_bf16 v[148:151],a[160:163],v[128:131],v[32:35]
buffer_load_dwordx4 v5,s[20:23],s47 offen offset:3072 nt lds
v_mfma_f32_16x16x32_bf16 v[148:151],a[164:167],v[160:163],v[148:151]
v_exp_f32 v132,v132
v_mfma_f32_16x16x32_bf16 v[148:151],a[168:171],v[192:195],v[148:151]
v_exp_f32 v133,v133
v_mfma_f32_16x16x32_bf16 v[148:151],a[172:175],v[224:227],v[148:151]
v_exp_f32 v134,v134
v_mfma_f32_16x16x32_bf16 v[156:159],a[188:191],v[224:227],v[32:35]
v_exp_f32 v135,v135
v_mfma_f32_16x16x32_bf16 v[156:159],a[184:187],v[192:195],v[156:159]
v_cvt_pk_bf16_f32 v132,v132,v133
v_cvt_pk_bf16_f32 v133,v134,v135
v_mfma_f32_16x16x32_bf16 v[156:159],a[180:183],v[160:163],v[156:159]
v_exp_f32 v140,v140
v_mfma_f32_16x16x32_bf16 v[156:159],a[176:179],v[128:131],v[156:159]
v_exp_f32 v141,v141
v_mfma_f32_16x16x32_bf16 a[64:67],a[192:195],v[196:199],a[64:67]
v_exp_f32 v142,v142
v_mfma_f32_16x16x32_bf16 a[64:67],a[196:199],v[212:215],a[64:67]
v_exp_f32 v143,v143
v_mfma_f32_16x16x32_bf16 a[68:71],a[204:207],v[212:215],a[68:71]
v_exp_f32 v148,v148
v_mfma_f32_16x16x32_bf16 a[68:71],a[200:203],v[196:199],a[68:71]
s_add_u32 m0,s49,s52
v_exp_f32 v149,v149
v_mfma_f32_16x16x32_bf16 a[72:75],a[208:211],v[196:199],a[72:75]
s_add_u32 s49,s49,32768
v_exp_f32 v150,v150
v_mfma_f32_16x16x32_bf16 a[72:75],a[212:215],v[212:215],a[72:75]
v_exp_f32 v151,v151
v_mfma_f32_16x16x32_bf16 a[76:79],a[220:223],v[212:215],a[76:79]
buffer_load_dwordx4 v4,s[24:27],s47 offen nt lds
v_mfma_f32_16x16x32_bf16 a[76:79],a[216:219],v[196:199],a[76:79]
v_cvt_pk_bf16_f32 v134,v140,v141
v_cvt_pk_bf16_f32 v135,v142,v143
v_mfma_f32_16x16x32_bf16 a[80:83],a[224:227],v[196:199],a[80:83]
v_cvt_pk_bf16_f32 v148,v148,v149
v_cvt_pk_bf16_f32 v149,v150,v151
v_mfma_f32_16x16x32_bf16 a[80:83],a[228:231],v[212:215],a[80:83]
v_exp_f32 v156,v156
v_mfma_f32_16x16x32_bf16 a[84:87],a[236:239],v[212:215],a[84:87]
v_exp_f32 v157,v157
v_mfma_f32_16x16x32_bf16 a[84:87],a[232:235],v[196:199],a[84:87]
v_exp_f32 v158,v158
v_mfma_f32_16x16x32_bf16 a[88:91],a[240:243],v[196:199],a[88:91]
v_exp_f32 v159,v159
v_mfma_f32_16x16x32_bf16 a[88:91],a[244:247],v[212:215],a[88:91]
v_cvt_pk_bf16_f32 v150,v156,v157
v_cvt_pk_bf16_f32 v151,v158,v159
v_mfma_f32_16x16x32_bf16 a[92:95],a[252:255],v[212:215],a[92:95]
v_accvgpr_read_b32 v15,a7
v_accvgpr_read_b32 v66,a4
v_mfma_f32_16x16x32_bf16 a[92:95],a[248:251],v[196:199],a[92:95]
v_accvgpr_read_b32 v67,a5
v_accvgpr_read_b32 v68,a8
v_mfma_f32_16x16x32_bf16 v[56:59],v[28:31],v[196:199],v[56:59]
v_accvgpr_read_b32 v69,a9
v_accvgpr_read_b32 v70,a10
v_mfma_f32_16x16x32_bf16 v[56:59],v[28:31],v[212:215],v[56:59]
v_accvgpr_read_b32 v71,a11
v_mul_f32 v68,v20,v68
v_mfma_f32_16x16x32_bf16 v[164:167],a[128:131],v[136:139],v[36:39]
v_mul_f32 v69,v20,v69
v_cvt_pk_bf16_f32 v68,v68,v69
v_mfma_f32_16x16x32_bf16 v[164:167],a[132:135],v[168:171],v[164:167]
v_mul_f32 v70,v20,v70
v_mul_f32 v71,v20,v71
v_mfma_f32_16x16x32_bf16 v[164:167],a[136:139],v[200:203],v[164:167]
buffer_load_dwordx4 v5,s[24:27],s47 offen offset:1024 nt lds
v_mfma_f32_16x16x32_bf16 v[164:167],a[140:143],v[232:235],v[164:167]
v_cvt_pk_bf16_f32 v69,v70,v71
v_accvgpr_read_b32 v16,a14
v_mfma_f32_16x16x32_bf16 v[172:175],a[156:159],v[232:235],v[36:39]
v_accvgpr_read_b32 v17,a15
v_accvgpr_read_b32 v70,a12
v_mfma_f32_16x16x32_bf16 v[172:175],a[152:155],v[200:203],v[172:175]
v_accvgpr_read_b32 v71,a13
v_accvgpr_read_b32 v72,a16
v_mfma_f32_16x16x32_bf16 v[172:175],a[148:151],v[168:171],v[172:175]
v_accvgpr_read_b32 v73,a17
v_accvgpr_read_b32 v74,a18
v_mfma_f32_16x16x32_bf16 v[172:175],a[144:147],v[136:139],v[172:175]
v_accvgpr_read_b32 v75,a19
v_mul_f32 v72,v20,v72
v_mfma_f32_16x16x32_bf16 v[180:183],a[160:163],v[136:139],v[36:39]
v_mul_f32 v73,v20,v73
v_cvt_pk_bf16_f32 v72,v72,v73
v_mfma_f32_16x16x32_bf16 v[180:183],a[164:167],v[168:171],v[180:183]
v_mul_f32 v74,v20,v74
v_mul_f32 v75,v20,v75
v_mfma_f32_16x16x32_bf16 v[180:183],a[168:171],v[200:203],v[180:183]
v_cvt_pk_bf16_f32 v73,v74,v75
v_accvgpr_read_b32 v12,a22
v_mfma_f32_16x16x32_bf16 v[180:183],a[172:175],v[232:235],v[180:183]
v_accvgpr_read_b32 v13,a23
v_accvgpr_read_b32 v74,a20
v_mfma_f32_16x16x32_bf16 v[188:191],a[188:191],v[232:235],v[36:39]
v_accvgpr_read_b32 v75,a21
v_accvgpr_read_b32 v76,a24
v_mfma_f32_16x16x32_bf16 v[188:191],a[184:187],v[200:203],v[188:191]
v_accvgpr_read_b32 v77,a25
v_accvgpr_read_b32 v78,a26
v_mfma_f32_16x16x32_bf16 v[188:191],a[180:183],v[168:171],v[188:191]
v_accvgpr_read_b32 v79,a27
v_mul_f32 v66,v20,v66
v_mfma_f32_16x16x32_bf16 v[188:191],a[176:179],v[136:139],v[188:191]
)A"
R"A(v_mul_f32 v67,v20,v67
v_mul_f32 v76,v20,v76
v_mfma_f32_16x16x32_bf16 a[96:99],a[192:195],v[228:231],a[96:99]
buffer_load_dwordx4 v4,s[24:27],s47 offen offset:2048 nt lds
v_mfma_f32_16x16x32_bf16 a[96:99],a[196:199],v[244:247],a[96:99]
ds_read_b64_tr_b16 a[192:193],v2
ds_read_b64_tr_b16 a[194:195],v2 offset:4096
v_mfma_f32_16x16x32_bf16 a[100:103],a[204:207],v[244:247],a[100:103]
ds_read_b64_tr_b16 a[196:197],v2 offset:8192
ds_read_b64_tr_b16 a[198:199],v2 offset:12288
v_mfma_f32_16x16x32_bf16 a[100:103],a[200:203],v[228:231],a[100:103]
ds_read_b64_tr_b16 a[204:205],v3 offset:8192
ds_read_b64_tr_b16 a[206:207],v3 offset:12288
v_mfma_f32_16x16x32_bf16 a[104:107],a[208:211],v[228:231],a[104:107]
ds_read_b64_tr_b16 a[200:201],v3
ds_read_b64_tr_b16 a[202:203],v3 offset:4096
v_mfma_f32_16x16x32_bf16 a[104:107],a[212:215],v[244:247],a[104:107]
ds_read_b64_tr_b16 a[208:209],v2 offset:256
ds_read_b64_tr_b16 a[210:211],v2 offset:4352
v_mfma_f32_16x16x32_bf16 a[108:111],a[220:223],v[244:247],a[108:111]
ds_read_b64_tr_b16 a[212:213],v2 offset:8448
ds_read_b64_tr_b16 a[214:215],v2 offset:12544
v_mfma_f32_16x16x32_bf16 a[108:111],a[216:219],v[228:231],a[108:111]
ds_read_b64_tr_b16 a[220:221],v3 offset:8448
ds_read_b64_tr_b16 a[222:223],v3 offset:12544
v_mfma_f32_16x16x32_bf16 a[112:115],a[224:227],v[228:231],a[112:115]
ds_read_b64_tr_b16 a[216:217],v3 offset:256
ds_read_b64_tr_b16 a[218:219],v3 offset:4352
v_mfma_f32_16x16x32_bf16 a[112:115],a[228:231],v[244:247],a[112:115]
ds_read_b64_tr_b16 a[224:225],v2 offset:512
ds_read_b64_tr_b16 a[226:227],v2 offset:4608
v_mfma_f32_16x16x32_bf16 a[116:119],a[236:239],v[244:247],a[116:119]
ds_read_b64_tr_b16 a[228:229],v2 offset:8704
ds_read_b64_tr_b16 a[230:231],v2 offset:12800
v_mfma_f32_16x16x32_bf16 a[116:119],a[232:235],v[228:231],a[116:119]
ds_read_b64_tr_b16 a[236:237],v3 offset:8704
ds_read_b64_tr_b16 a[238:239],v3 offset:12800
v_mfma_f32_16x16x32_bf16 a[120:123],a[240:243],v[228:231],a[120:123]
ds_read_b64_tr_b16 a[232:233],v3 offset:512
ds_read_b64_tr_b16 a[234:235],v3 offset:4608
v_mfma_f32_16x16x32_bf16 a[120:123],a[244:247],v[244:247],a[120:123]
ds_read_b64_tr_b16 a[240:241],v2 offset:768
ds_read_b64_tr_b16 a[242:243],v2 offset:4864
v_mfma_f32_16x16x32_bf16 a[124:127],a[252:255],v[244:247],a[124:127]
buffer_load_dwordx4 v5,s[24:27],s47 offen offset:3072 nt lds
v_mfma_f32_16x16x32_bf16 a[124:127],a[248:251],v[228:231],a[124:127]
s_add_u32 s47,s47,16384
ds_read_b64_tr_b16 a[244:245],v2 offset:8960
ds_read_b64_tr_b16 a[246:247],v2 offset:13056
v_mfma_f32_16x16x32_bf16 v[60:63],v[28:31],v[228:231],v[60:63]
s_and_b32 s47,s47,0x3ffff
ds_read_b64_tr_b16 a[252:253],v3 offset:8960
ds_read_b64_tr_b16 a[254:255],v3 offset:13056
v_mfma_f32_16x16x32_bf16 v[60:63],v[28:31],v[244:247],v[60:63]
s_or_b32 s47,s47,s46
ds_read_b64_tr_b16 a[248:249],v3 offset:768
ds_read_b64_tr_b16 a[250:251],v3 offset:4864
v_mfma_f32_16x16x32_bf16 v[196:199],a[128:131],v[144:147],v[40:43]
s_and_b32 s46,s54,0xfffc0000
v_mul_f32 v77,v20,v77
v_mul_f32 v14,v20,v14
v_mfma_f32_16x16x32_bf16 v[196:199],a[132:135],v[176:179],v[196:199]
v_mul_f32 v15,v20,v15
v_cvt_pk_bf16_f32 v66,v66,v67
v_mfma_f32_16x16x32_bf16 v[196:199],a[136:139],v[208:211],v[196:199]
v_cvt_pk_bf16_f32 v76,v76,v77
v_mul_f32 v78,v20,v78
v_mfma_f32_16x16x32_bf16 v[196:199],a[140:143],v[240:243],v[196:199]
v_mul_f32 v79,v20,v79
v_cvt_pk_bf16_f32 v67,v14,v15
v_mfma_f32_16x16x32_bf16 v[204:207],a[156:159],v[240:243],v[40:43]
v_cvt_pk_bf16_f32 v77,v78,v79
v_accvgpr_read_b32 v14,a30
v_mfma_f32_16x16x32_bf16 v[204:207],a[152:155],v[208:211],v[204:207]
v_accvgpr_read_b32 v15,a31
v_accvgpr_read_b32 v78,a28
v_mfma_f32_16x16x32_bf16 v[204:207],a[148:151],v[176:179],v[204:207]
v_accvgpr_read_b32 v79,a29
v_max3_f32 v44,v44,v45,v46
v_mfma_f32_16x16x32_bf16 v[204:207],a[144:147],v[144:147],v[204:207]
v_max3_f32 v44,v44,v47,s101
v_sub_f32 v47,0,v44
v_mfma_f32_16x16x32_bf16 v[212:215],a[160:163],v[144:147],v[40:43]
v_sub_f32 v46,0,v44
v_sub_f32 v45,0,v44
v_mfma_f32_16x16x32_bf16 v[212:215],a[164:167],v[176:179],v[212:215]
v_cmp_nlt_f32 s[56:57],v48,s63
v_sub_f32 v44,0,v44
v_mfma_f32_16x16x32_bf16 v[212:215],a[168:171],v[208:211],v[212:215]
s_cmp_eq_u32 s49,163840
s_cselect_b32 s49,0,s49
v_max_f32 v21,0x00800000,v52
v_mfma_f32_16x16x32_bf16 v[212:215],a[172:175],v[240:243],v[212:215]
v_exp_f32 v164,v164
v_mfma_f32_16x16x32_bf16 v[220:223],a[188:191],v[240:243],v[40:43]
v_exp_f32 v165,v165
v_mfma_f32_16x16x32_bf16 v[220:223],a[184:187],v[208:211],v[220:223]
v_exp_f32 v166,v166
v_mfma_f32_16x16x32_bf16 v[220:223],a[180:183],v[176:179],v[220:223]
v_exp_f32 v167,v167
v_mfma_f32_16x16x32_bf16 v[220:223],a[176:179],v[144:147],v[220:223]
s_waitcnt lgkmcnt(0)
v_cvt_pk_bf16_f32 v164,v164,v165
v_cvt_pk_bf16_f32 v165,v166,v167
v_mfma_f32_16x16x32_bf16 a[0:3],a[192:195],v[132:135],0
v_exp_f32 v172,v172
v_mfma_f32_16x16x32_bf16 a[0:3],a[196:199],v[148:151],a[0:3]
v_exp_f32 v173,v173
v_mfma_f32_16x16x32_bf16 a[4:7],a[204:207],v[148:151],0
v_exp_f32 v174,v174
v_mfma_f32_16x16x32_bf16 a[4:7],a[200:203],v[132:135],a[4:7]
v_exp_f32 v175,v175
v_mfma_f32_16x16x32_bf16 a[8:11],a[208:211],v[132:135],0
s_memrealtime s[76:77]
v_exp_f32 v180,v180
v_mfma_f32_16x16x32_bf16 a[8:11],a[212:215],v[148:151],a[8:11]
v_exp_f32 v181,v181
v_mfma_f32_16x16x32_bf16 a[12:15],a[220:223],v[148:151],0
v_exp_f32 v182,v182
v_mfma_f32_16x16x32_bf16 a[12:15],a[216:219],v[132:135],a[12:15]
v_exp_f32 v183,v183
v_mfma_f32_16x16x32_bf16 a[16:19],a[224:227],v[132:135],0
v_cvt_pk_bf16_f32 v166,v172,v173
v_cvt_pk_bf16_f32 v167,v174,v175
v_mfma_f32_16x16x32_bf16 a[16:19],a[228:231],v[148:151],a[16:19]
v_cvt_pk_bf16_f32 v180,v180,v181
v_cvt_pk_bf16_f32 v181,v182,v183
v_mfma_f32_16x16x32_bf16 a[20:23],a[236:239],v[148:151],0
v_exp_f32 v188,v188
v_mfma_f32_16x16x32_bf16 a[20:23],a[232:235],v[132:135],a[20:23]
s_waitcnt lgkmcnt(0)
v_exp_f32 v189,v189
v_mfma_f32_16x16x32_bf16 a[24:27],a[240:243],v[132:135],0
s_sub_u32 s79,s78,s76
v_exp_f32 v190,v190
v_mfma_f32_16x16x32_bf16 a[24:27],a[244:247],v[148:151],a[24:27]
s_add_u32 s78,s76,s60
v_exp_f32 v191,v191
v_mfma_f32_16x16x32_bf16 a[28:31],a[252:255],v[148:151],0
s_cmp_gt_i32 s79,0
s_cbranch_scc1 L_ps4_%=
L_pr4_%=:
v_cvt_pk_bf16_f32 v182,v188,v189
v_mfma_f32_16x16x32_bf16 a[28:31],a[248:251],v[132:135],a[28:31]
v_cvt_pk_bf16_f32 v183,v190,v191
v_accvgpr_read_b32 v80,a32
v_mfma_f32_16x16x32_bf16 v[48:51],v[28:31],v[132:135],0
v_rcp_f32 v21,v21
v_mfma_f32_16x16x32_bf16 v[48:51],v[28:31],v[148:151],v[48:51]
v_accvgpr_read_b32 v81,a33
v_accvgpr_read_b32 v82,a34
v_mfma_f32_16x16x32_bf16 v[228:231],a[128:131],v[152:155],v[44:47]
s_waitcnt vmcnt(17)
v_accvgpr_read_b32 v83,a35
v_mul_f32 v70,v20,v70
v_mfma_f32_16x16x32_bf16 v[228:231],a[132:135],v[184:187],v[228:231]
s_barrier
s_add_u32 m0,s49,s51
v_mul_f32 v71,v20,v71
v_mfma_f32_16x16x32_bf16 v[228:231],a[136:139],v[216:219],v[228:231]
v_mul_f32 v80,v21,v80
v_mul_f32 v81,v21,v81
v_mfma_f32_16x16x32_bf16 v[228:231],a[140:143],v[248:251],v[228:231]
v_mul_f32 v16,v20,v16
v_mul_f32 v17,v20,v17
v_mfma_f32_16x16x32_bf16 v[236:239],a[156:159],v[248:251],v[44:47]
buffer_load_dwordx4 v4,s[20:23],s47 offen nt lds
v_mfma_f32_16x16x32_bf16 v[236:239],a[152:155],v[216:219],v[236:239]
v_cvt_pk_bf16_f32 v70,v70,v71
v_cvt_pk_bf16_f32 v80,v80,v81
v_mfma_f32_16x16x32_bf16 v[236:239],a[148:151],v[184:187],v[236:239]
v_mul_f32 v82,v21,v82
v_mul_f32 v83,v21,v83
v_mfma_f32_16x16x32_bf16 v[236:239],a[144:147],v[152:155],v[236:239]
v_cvt_pk_bf16_f32 v71,v16,v17
v_cvt_pk_bf16_f32 v81,v82,v83
v_mfma_f32_16x16x32_bf16 v[244:247],a[160:163],v[152:155],v[44:47]
v_accvgpr_read_b32 v16,a38
v_accvgpr_read_b32 v17,a39
v_mfma_f32_16x16x32_bf16 v[244:247],a[164:167],v[184:187],v[244:247]
v_accvgpr_read_b32 v82,a36
v_accvgpr_read_b32 v83,a37
v_mfma_f32_16x16x32_bf16 v[244:247],a[168:171],v[216:219],v[244:247]
v_accvgpr_read_b32 v84,a40
v_accvgpr_read_b32 v85,a41
v_mfma_f32_16x16x32_bf16 v[244:247],a[172:175],v[248:251],v[244:247]
v_accvgpr_read_b32 v86,a42
v_accvgpr_read_b32 v87,a43
v_mfma_f32_16x16x32_bf16 v[252:255],a[188:191],v[248:251],v[44:47]
v_mul_f32 v74,v20,v74
v_mul_f32 v75,v20,v75
v_mfma_f32_16x16x32_bf16 v[252:255],a[184:187],v[216:219],v[252:255]
v_mul_f32 v84,v21,v84
v_mul_f32 v85,v21,v85
v_mfma_f32_16x16x32_bf16 v[252:255],a[180:183],v[184:187],v[252:255]
v_mul_f32 v12,v20,v12
v_mul_f32 v13,v20,v13
v_mfma_f32_16x16x32_bf16 v[252:255],a[176:179],v[152:155],v[252:255]
v_cvt_pk_bf16_f32 v74,v74,v75
v_cvt_pk_bf16_f32 v84,v84,v85
v_mfma_f32_16x16x32_bf16 a[32:35],a[192:195],v[164:167],0
v_mul_f32 v86,v21,v86
v_mul_f32 v87,v21,v87
v_mfma_f32_16x16x32_bf16 a[32:35],a[196:199],v[180:183],a[32:35]
v_cvt_pk_bf16_f32 v75,v12,v13
v_cvt_pk_bf16_f32 v85,v86,v87
v_mfma_f32_16x16x32_bf16 a[36:39],a[204:207],v[180:183],0
v_accvgpr_read_b32 v12,a46
v_accvgpr_read_b32 v13,a47
v_mfma_f32_16x16x32_bf16 a[36:39],a[200:203],v[164:167],a[36:39]
v_accvgpr_read_b32 v86,a44
v_accvgpr_read_b32 v87,a45
v_mfma_f32_16x16x32_bf16 a[40:43],a[208:211],v[164:167],0
buffer_load_dwordx4 v4,s[20:23],s47 offen offset:1024 nt lds
v_mfma_f32_16x16x32_bf16 a[40:43],a[212:215],v[180:183],a[40:43]
v_accvgpr_read_b32 v88,a48
v_accvgpr_read_b32 v89,a49
v_mfma_f32_16x16x32_bf16 a[44:47],a[220:223],v[180:183],0
v_accvgpr_read_b32 v90,a50
v_accvgpr_read_b32 v91,a51
v_mfma_f32_16x16x32_bf16 a[44:47],a[216:219],v[164:167],a[44:47]
v_mul_f32 v78,v20,v78
v_mul_f32 v79,v20,v79
v_mfma_f32_16x16x32_bf16 a[48:51],a[224:227],v[164:167],0
v_mul_f32 v88,v21,v88
v_mul_f32 v89,v21,v89
v_mul_f32 v14,v20,v14
v_mul_f32 v15,v20,v15
v_cvt_pk_bf16_f32 v78,v78,v79
v_add_u32 v1,s48,v1
v_cvt_pk_bf16_f32 v88,v88,v89
v_mul_f32 v90,v21,v90
v_mul_f32 v91,v21,v91
v_mfma_f32_16x16x32_bf16 a[48:51],a[228:231],v[180:183],a[48:51]
ds_read_b128 a[128:131],v1
v_cvt_pk_bf16_f32 v79,v14,v15
v_cvt_pk_bf16_f32 v89,v90,v91
v_accvgpr_read_b32 v14,a54
v_accvgpr_read_b32 v15,a55
v_accvgpr_read_b32 v90,a52
v_accvgpr_read_b32 v91,a53
v_mfma_f32_16x16x32_bf16 a[52:55],a[236:239],v[180:183],0
ds_read_b128 a[132:135],v1 offset:256
v_accvgpr_read_b32 v92,a56
v_mfma_f32_16x16x32_bf16 a[52:55],a[232:235],v[164:167],a[52:55]
ds_read_b128 a[136:139],v1 offset:512
v_accvgpr_read_b32 v93,a57
v_accvgpr_read_b32 v94,a58
v_accvgpr_read_b32 v95,a59
v_mfma_f32_16x16x32_bf16 a[56:59],a[240:243],v[164:167],0
ds_read_b128 a[140:143],v1 offset:768
v_mul_f32 v82,v21,v82
v_mul_f32 v83,v21,v83
v_mul_f32 v92,v21,v92
v_mul_f32 v93,v21,v93
v_mul_f32 v16,v21,v16
v_mul_f32 v17,v21,v17
v_cvt_pk_bf16_f32 v82,v82,v83
v_cvt_pk_bf16_f32 v92,v92,v93
v_mul_f32 v94,v21,v94
v_mul_f32 v95,v21,v95
v_mfma_f32_16x16x32_bf16 a[56:59],a[244:247],v[180:183],a[56:59]
ds_read_b128 a[156:159],v1 offset:4864
v_cvt_pk_bf16_f32 v83,v16,v17
v_cvt_pk_bf16_f32 v93,v94,v95
v_accvgpr_read_b32 v16,a62
v_accvgpr_read_b32 v17,a63
v_accvgpr_read_b32 v94,a60
v_accvgpr_read_b32 v95,a61
v_mfma_f32_16x16x32_bf16 a[60:63],a[252:255],v[180:183],0
ds_read_b128 a[152:155],v1 offset:4608
v_max_f32 v22,0x00800000,v56
v_mfma_f32_16x16x32_bf16 a[60:63],a[248:251],v[164:167],a[60:63]
ds_read_b128 a[148:151],v1 offset:4352
v_cmp_nlt_f32 s[58:59],v52,s63
s_nop 1
s_or_b64 s[56:57],s[56:57],s[58:59]
v_mfma_f32_16x16x32_bf16 v[52:55],v[28:31],v[164:167],0
ds_read_b128 a[144:147],v1 offset:4096
v_accvgpr_read_b32 v96,a64
v_mfma_f32_16x16x32_bf16 v[52:55],v[28:31],v[180:183],v[52:55]
s_waitcnt lgkmcnt(7)
ds_read_b128 a[160:163],v1 offset:8192
v_accvgpr_read_b32 v97,a65
v_mfma_f32_16x16x32_bf16 v[132:135],a[128:131],v[128:131],v[32:35]
s_waitcnt lgkmcnt(7)
ds_read_b128 a[164:167],v1 offset:8448
v_accvgpr_read_b32 v98,a66
v_mfma_f32_16x16x32_bf16 v[132:135],a[132:135],v[160:163],v[132:135]
s_waitcnt lgkmcnt(7)
buffer_load_dwordx4 v5,s[20:23],s47 offen offset:2048 nt lds
ds_read_b128 a[168:171],v1 offset:8704
v_mfma_f32_16x16x32_bf16 v[132:135],a[136:139],v[192:195],v[132:135]
s_waitcnt lgkmcnt(7)
ds_read_b128 a[172:175],v1 offset:8960
v_accvgpr_read_b32 v99,a67
v_mfma_f32_16x16x32_bf16 v[132:135],a[140:143],v[224:227],v[132:135]
s_waitcnt lgkmcnt(7)
ds_read_b128 a[188:191],v1 offset:13056
)A"
R"A(v_mul_f32 v86,v21,v86
v_mfma_f32_16x16x32_bf16 v[140:143],a[156:159],v[224:227],v[32:35]
s_waitcnt lgkmcnt(7)
ds_read_b128 a[184:187],v1 offset:12800
v_mul_f32 v87,v21,v87
v_mfma_f32_16x16x32_bf16 v[140:143],a[152:155],v[192:195],v[140:143]
s_waitcnt lgkmcnt(7)
ds_read_b128 a[180:183],v1 offset:12544
v_mul_f32 v12,v21,v12
v_mfma_f32_16x16x32_bf16 v[140:143],a[148:151],v[160:163],v[140:143]
s_waitcnt lgkmcnt(7)
ds_read_b128 a[176:179],v1 offset:12288
v_mul_f32 v13,v21,v13
v_mfma_f32_16x16x32_bf16 v[140:143],a[144:147],v[128:131],v[140:143]
s_waitcnt lgkmcnt(7)
v_exp_f32 v196,v196
v_mfma_f32_16x16x32_bf16 v[148:151],a[160:163],v[128:131],v[32:35]
s_waitcnt lgkmcnt(6)
v_exp_f32 v197,v197
v_mfma_f32_16x16x32_bf16 v[148:151],a[164:167],v[160:163],v[148:151]
s_waitcnt lgkmcnt(5)
v_exp_f32 v198,v198
v_mfma_f32_16x16x32_bf16 v[148:151],a[168:171],v[192:195],v[148:151]
s_waitcnt lgkmcnt(4)
v_exp_f32 v199,v199
v_mfma_f32_16x16x32_bf16 v[148:151],a[172:175],v[224:227],v[148:151]
s_waitcnt lgkmcnt(3)
v_cvt_pk_bf16_f32 v196,v196,v197
v_cvt_pk_bf16_f32 v197,v198,v199
v_mfma_f32_16x16x32_bf16 v[156:159],a[188:191],v[224:227],v[32:35]
s_waitcnt lgkmcnt(2)
v_exp_f32 v204,v204
v_exp_f32 v205,v205
v_exp_f32 v206,v206
v_exp_f32 v207,v207
v_exp_f32 v212,v212
v_exp_f32 v213,v213
v_mfma_f32_16x16x32_bf16 v[156:159],a[184:187],v[192:195],v[156:159]
s_waitcnt lgkmcnt(1)
v_exp_f32 v214,v214
v_exp_f32 v215,v215
v_cvt_pk_bf16_f32 v198,v204,v205
v_cvt_pk_bf16_f32 v199,v206,v207
v_cvt_pk_bf16_f32 v212,v212,v213
v_exp_f32 v220,v220
v_exp_f32 v221,v221
v_exp_f32 v222,v222
v_exp_f32 v223,v223
v_mfma_f32_16x16x32_bf16 v[156:159],a[180:183],v[160:163],v[156:159]
s_waitcnt lgkmcnt(0)
v_cvt_pk_bf16_f32 v213,v214,v215
v_cvt_pk_bf16_f32 v214,v220,v221
v_cvt_pk_bf16_f32 v215,v222,v223
v_mfma_f32_16x16x32_bf16 v[156:159],a[176:179],v[128:131],v[156:159]
buffer_load_dwordx4 v5,s[20:23],s47 offen offset:3072 nt lds
v_rcp_f32 v22,v22
v_mfma_f32_16x16x32_bf16 a[64:67],a[192:195],v[196:199],0
v_mul_f32 v96,v22,v96
v_mul_f32 v97,v22,v97
v_cvt_pk_bf16_f32 v86,v86,v87
v_cvt_pk_bf16_f32 v96,v96,v97
v_mul_f32 v98,v22,v98
v_mul_f32 v99,v22,v99
v_mfma_f32_16x16x32_bf16 a[64:67],a[196:199],v[212:215],a[64:67]
v_cvt_pk_bf16_f32 v87,v12,v13
v_cvt_pk_bf16_f32 v97,v98,v99
v_accvgpr_read_b32 v12,a70
v_accvgpr_read_b32 v13,a71
v_accvgpr_read_b32 v98,a68
v_accvgpr_read_b32 v99,a69
v_mfma_f32_16x16x32_bf16 a[68:71],a[204:207],v[212:215],0
v_accvgpr_read_b32 v100,a72
v_accvgpr_read_b32 v101,a73
v_mfma_f32_16x16x32_bf16 a[68:71],a[200:203],v[196:199],a[68:71]
v_accvgpr_read_b32 v102,a74
v_accvgpr_read_b32 v103,a75
v_mfma_f32_16x16x32_bf16 a[72:75],a[208:211],v[196:199],0
v_mul_f32 v90,v21,v90
v_mul_f32 v91,v21,v91
v_mul_f32 v100,v22,v100
v_mul_f32 v101,v22,v101
v_mul_f32 v14,v21,v14
v_mul_f32 v15,v21,v15
v_cvt_pk_bf16_f32 v90,v90,v91
v_cvt_pk_bf16_f32 v100,v100,v101
v_mul_f32 v102,v22,v102
v_mul_f32 v103,v22,v103
v_mfma_f32_16x16x32_bf16 a[72:75],a[212:215],v[212:215],a[72:75]
v_cvt_pk_bf16_f32 v91,v14,v15
v_cvt_pk_bf16_f32 v101,v102,v103
v_accvgpr_read_b32 v14,a78
v_accvgpr_read_b32 v15,a79
v_accvgpr_read_b32 v102,a76
v_accvgpr_read_b32 v103,a77
v_mfma_f32_16x16x32_bf16 a[76:79],a[220:223],v[212:215],0
v_accvgpr_read_b32 v104,a80
v_accvgpr_read_b32 v105,a81
v_mfma_f32_16x16x32_bf16 a[76:79],a[216:219],v[196:199],a[76:79]
s_add_u32 m0,s49,s52
v_accvgpr_read_b32 v106,a82
v_accvgpr_read_b32 v107,a83
v_mfma_f32_16x16x32_bf16 a[80:83],a[224:227],v[196:199],0
v_mul_f32 v94,v21,v94
v_mul_f32 v95,v21,v95
v_mul_f32 v104,v22,v104
v_mul_f32 v105,v22,v105
v_mul_f32 v16,v21,v16
v_mul_f32 v17,v21,v17
v_cvt_pk_bf16_f32 v94,v94,v95
v_cvt_pk_bf16_f32 v104,v104,v105
v_mul_f32 v106,v22,v106
v_mul_f32 v107,v22,v107
v_mfma_f32_16x16x32_bf16 a[80:83],a[228:231],v[212:215],a[80:83]
v_cvt_pk_bf16_f32 v95,v16,v17
v_cvt_pk_bf16_f32 v105,v106,v107
v_accvgpr_read_b32 v16,a86
v_accvgpr_read_b32 v17,a87
v_accvgpr_read_b32 v106,a84
v_accvgpr_read_b32 v107,a85
v_mfma_f32_16x16x32_bf16 a[84:87],a[236:239],v[212:215],0
s_add_u32 s49,s49,32768
v_accvgpr_read_b32 v108,a88
v_accvgpr_read_b32 v109,a89
v_mfma_f32_16x16x32_bf16 a[84:87],a[232:235],v[196:199],a[84:87]
v_accvgpr_read_b32 v110,a90
v_accvgpr_read_b32 v111,a91
v_mfma_f32_16x16x32_bf16 a[88:91],a[240:243],v[196:199],0
v_mul_f32 v98,v22,v98
v_mul_f32 v99,v22,v99
v_mul_f32 v108,v22,v108
v_mul_f32 v109,v22,v109
v_mul_f32 v12,v22,v12
v_mul_f32 v13,v22,v13
v_cvt_pk_bf16_f32 v98,v98,v99
v_cvt_pk_bf16_f32 v108,v108,v109
v_mul_f32 v110,v22,v110
v_mul_f32 v111,v22,v111
v_mfma_f32_16x16x32_bf16 a[88:91],a[244:247],v[212:215],a[88:91]
buffer_load_dwordx4 v4,s[24:27],s47 offen nt lds
v_cvt_pk_bf16_f32 v99,v12,v13
v_cvt_pk_bf16_f32 v109,v110,v111
v_accvgpr_read_b32 v12,a94
v_accvgpr_read_b32 v13,a95
v_accvgpr_read_b32 v110,a92
v_accvgpr_read_b32 v111,a93
v_mfma_f32_16x16x32_bf16 a[92:95],a[252:255],v[212:215],0
v_cmp_nlt_f32 s[58:59],v56,s63
s_nop 1
s_or_b64 s[56:57],s[56:57],s[58:59]
v_mfma_f32_16x16x32_bf16 a[92:95],a[248:251],v[196:199],a[92:95]
v_exp_f32 v228,v228
v_mfma_f32_16x16x32_bf16 v[56:59],v[28:31],v[196:199],0
v_exp_f32 v229,v229
v_mfma_f32_16x16x32_bf16 v[56:59],v[28:31],v[212:215],v[56:59]
v_exp_f32 v230,v230
v_mfma_f32_16x16x32_bf16 v[164:167],a[128:131],v[136:139],v[36:39]
v_exp_f32 v231,v231
v_mfma_f32_16x16x32_bf16 v[164:167],a[132:135],v[168:171],v[164:167]
v_cvt_pk_bf16_f32 v228,v228,v229
v_cvt_pk_bf16_f32 v229,v230,v231
v_mfma_f32_16x16x32_bf16 v[164:167],a[136:139],v[200:203],v[164:167]
v_exp_f32 v236,v236
v_mfma_f32_16x16x32_bf16 v[164:167],a[140:143],v[232:235],v[164:167]
v_exp_f32 v237,v237
v_mfma_f32_16x16x32_bf16 v[172:175],a[156:159],v[232:235],v[36:39]
v_exp_f32 v238,v238
v_mfma_f32_16x16x32_bf16 v[172:175],a[152:155],v[200:203],v[172:175]
v_exp_f32 v239,v239
v_mfma_f32_16x16x32_bf16 v[172:175],a[148:151],v[168:171],v[172:175]
buffer_load_dwordx4 v5,s[24:27],s47 offen offset:1024 nt lds
v_mfma_f32_16x16x32_bf16 v[172:175],a[144:147],v[136:139],v[172:175]
v_exp_f32 v244,v244
v_mfma_f32_16x16x32_bf16 v[180:183],a[160:163],v[136:139],v[36:39]
v_exp_f32 v245,v245
v_mfma_f32_16x16x32_bf16 v[180:183],a[164:167],v[168:171],v[180:183]
v_exp_f32 v246,v246
v_mfma_f32_16x16x32_bf16 v[180:183],a[168:171],v[200:203],v[180:183]
v_exp_f32 v247,v247
v_mfma_f32_16x16x32_bf16 v[180:183],a[172:175],v[232:235],v[180:183]
v_cvt_pk_bf16_f32 v230,v236,v237
v_cvt_pk_bf16_f32 v231,v238,v239
v_mfma_f32_16x16x32_bf16 v[188:191],a[188:191],v[232:235],v[36:39]
v_cvt_pk_bf16_f32 v244,v244,v245
v_max_f32 v23,0x00800000,v60
v_mfma_f32_16x16x32_bf16 v[188:191],a[184:187],v[200:203],v[188:191]
v_exp_f32 v252,v252
v_exp_f32 v253,v253
v_exp_f32 v254,v254
v_exp_f32 v255,v255
v_mfma_f32_16x16x32_bf16 v[188:191],a[180:183],v[168:171],v[188:191]
v_cvt_pk_bf16_f32 v245,v246,v247
v_cvt_pk_bf16_f32 v246,v252,v253
v_cvt_pk_bf16_f32 v247,v254,v255
v_mfma_f32_16x16x32_bf16 v[188:191],a[176:179],v[136:139],v[188:191]
v_rcp_f32 v23,v23
v_accvgpr_read_b32 v112,a96
v_accvgpr_read_b32 v113,a97
v_accvgpr_read_b32 v114,a98
v_accvgpr_read_b32 v115,a99
v_mfma_f32_16x16x32_bf16 a[96:99],a[192:195],v[228:231],0
v_mul_f32 v102,v22,v102
v_mul_f32 v103,v22,v103
v_mul_f32 v112,v23,v112
v_mul_f32 v113,v23,v113
v_mul_f32 v14,v22,v14
v_mul_f32 v15,v22,v15
v_cvt_pk_bf16_f32 v102,v102,v103
v_cvt_pk_bf16_f32 v112,v112,v113
v_mul_f32 v114,v23,v114
v_mul_f32 v115,v23,v115
v_mfma_f32_16x16x32_bf16 a[96:99],a[196:199],v[244:247],a[96:99]
v_cvt_pk_bf16_f32 v103,v14,v15
v_cvt_pk_bf16_f32 v113,v114,v115
v_accvgpr_read_b32 v14,a102
v_accvgpr_read_b32 v15,a103
v_accvgpr_read_b32 v114,a100
v_accvgpr_read_b32 v115,a101
v_mfma_f32_16x16x32_bf16 a[100:103],a[204:207],v[244:247],0
v_accvgpr_read_b32 v116,a104
v_accvgpr_read_b32 v117,a105
v_mfma_f32_16x16x32_bf16 a[100:103],a[200:203],v[228:231],a[100:103]
v_accvgpr_read_b32 v118,a106
v_accvgpr_read_b32 v119,a107
v_mfma_f32_16x16x32_bf16 a[104:107],a[208:211],v[228:231],0
v_mul_f32 v106,v22,v106
v_mul_f32 v107,v22,v107
v_mul_f32 v116,v23,v116
v_mul_f32 v117,v23,v117
v_mul_f32 v16,v22,v16
v_mul_f32 v17,v22,v17
v_cvt_pk_bf16_f32 v106,v106,v107
v_cvt_pk_bf16_f32 v116,v116,v117
v_mul_f32 v118,v23,v118
v_mul_f32 v119,v23,v119
v_mfma_f32_16x16x32_bf16 a[104:107],a[212:215],v[244:247],a[104:107]
v_cvt_pk_bf16_f32 v107,v16,v17
v_cvt_pk_bf16_f32 v117,v118,v119
v_accvgpr_read_b32 v16,a110
v_accvgpr_read_b32 v17,a111
v_accvgpr_read_b32 v118,a108
v_accvgpr_read_b32 v119,a109
v_mfma_f32_16x16x32_bf16 a[108:111],a[220:223],v[244:247],0
v_accvgpr_read_b32 v120,a112
v_accvgpr_read_b32 v121,a113
v_mfma_f32_16x16x32_bf16 a[108:111],a[216:219],v[228:231],a[108:111]
buffer_load_dwordx4 v4,s[24:27],s47 offen offset:2048 nt lds
v_accvgpr_read_b32 v122,a114
v_accvgpr_read_b32 v123,a115
v_mfma_f32_16x16x32_bf16 a[112:115],a[224:227],v[228:231],0
v_mul_f32 v110,v22,v110
v_mul_f32 v111,v22,v111
v_mul_f32 v120,v23,v120
v_mul_f32 v121,v23,v121
v_mul_f32 v12,v22,v12
v_mul_f32 v13,v22,v13
v_cvt_pk_bf16_f32 v110,v110,v111
v_cvt_pk_bf16_f32 v120,v120,v121
v_mul_f32 v122,v23,v122
v_mul_f32 v123,v23,v123
v_mfma_f32_16x16x32_bf16 a[112:115],a[228:231],v[244:247],a[112:115]
v_cvt_pk_bf16_f32 v111,v12,v13
v_cvt_pk_bf16_f32 v121,v122,v123
v_accvgpr_read_b32 v12,a118
v_accvgpr_read_b32 v13,a119
v_accvgpr_read_b32 v122,a116
v_accvgpr_read_b32 v123,a117
v_mfma_f32_16x16x32_bf16 a[116:119],a[236:239],v[244:247],0
v_accvgpr_read_b32 v124,a120
v_accvgpr_read_b32 v125,a121
v_mfma_f32_16x16x32_bf16 a[116:119],a[232:235],v[228:231],a[116:119]
v_accvgpr_read_b32 v126,a122
v_accvgpr_read_b32 v127,a123
v_mfma_f32_16x16x32_bf16 a[120:123],a[240:243],v[228:231],0
v_mul_f32 v114,v23,v114
v_mul_f32 v115,v23,v115
v_mul_f32 v124,v23,v124
v_mul_f32 v125,v23,v125
v_mul_f32 v14,v23,v14
v_mul_f32 v15,v23,v15
v_cvt_pk_bf16_f32 v114,v114,v115
v_cvt_pk_bf16_f32 v124,v124,v125
v_mul_f32 v126,v23,v126
v_mul_f32 v127,v23,v127
v_mfma_f32_16x16x32_bf16 a[120:123],a[244:247],v[244:247],a[120:123]
v_cvt_pk_bf16_f32 v115,v14,v15
v_cvt_pk_bf16_f32 v125,v126,v127
v_accvgpr_read_b32 v14,a126
v_accvgpr_read_b32 v15,a127
v_accvgpr_read_b32 v126,a124
v_accvgpr_read_b32 v127,a125
v_mfma_f32_16x16x32_bf16 a[124:127],a[252:255],v[244:247],0
v_cmp_nlt_f32 s[58:59],v60,s63
s_nop 1
s_or_b64 s[56:57],s[56:57],s[58:59]
v_mfma_f32_16x16x32_bf16 a[124:127],a[248:251],v[228:231],a[124:127]
v_add_u32 v2,s48,v2
v_add_u32 v3,s48,v3
v_mfma_f32_16x16x32_bf16 v[60:63],v[28:31],v[228:231],0
ds_read_b64_tr_b16 a[192:193],v2
ds_read_b64_tr_b16 a[194:195],v2 offset:4096
v_mfma_f32_16x16x32_bf16 v[60:63],v[28:31],v[244:247],v[60:63]
ds_read_b64_tr_b16 a[196:197],v2 offset:8192
ds_read_b64_tr_b16 a[198:199],v2 offset:12288
v_mfma_f32_16x16x32_bf16 v[196:199],a[128:131],v[144:147],v[40:43]
buffer_load_dwordx4 v5,s[24:27],s47 offen offset:3072 nt lds
v_mfma_f32_16x16x32_bf16 v[196:199],a[132:135],v[176:179],v[196:199]
s_add_u32 s47,s47,16384
ds_read_b64_tr_b16 a[204:205],v3 offset:8192
ds_read_b64_tr_b16 a[206:207],v3 offset:12288
v_mfma_f32_16x16x32_bf16 v[196:199],a[136:139],v[208:211],v[196:199]
s_and_b32 s47,s47,0x3ffff
ds_read_b64_tr_b16 a[200:201],v3
ds_read_b64_tr_b16 a[202:203],v3 offset:4096
v_mfma_f32_16x16x32_bf16 v[196:199],a[140:143],v[240:243],v[196:199]
s_or_b32 s47,s47,s46
v_exp_f32 v132,v132
v_mfma_f32_16x16x32_bf16 v[204:207],a[156:159],v[240:243],v[40:43]
v_exp_f32 v133,v133
v_mfma_f32_16x16x32_bf16 v[204:207],a[152:155],v[208:211],v[204:207]
ds_read_b64_tr_b16 a[208:209],v2 offset:256
ds_read_b64_tr_b16 a[210:211],v2 offset:4352
v_mfma_f32_16x16x32_bf16 v[204:207],a[148:151],v[176:179],v[204:207]
v_exp_f32 v134,v134
v_mfma_f32_16x16x32_bf16 v[204:207],a[144:147],v[144:147],v[204:207]
v_exp_f32 v135,v135
v_mfma_f32_16x16x32_bf16 v[212:215],a[160:163],v[144:147],v[40:43]
ds_read_b64_tr_b16 a[212:213],v2 offset:8448
v_cvt_pk_bf16_f32 v132,v132,v133
v_mfma_f32_16x16x32_bf16 v[212:215],a[164:167],v[176:179],v[212:215]
v_exp_f32 v140,v140
v_mfma_f32_16x16x32_bf16 v[212:215],a[168:171],v[208:211],v[212:215]
v_exp_f32 v141,v141
v_mfma_f32_16x16x32_bf16 v[212:215],a[172:175],v[240:243],v[212:215]
v_exp_f32 v142,v142
)A"
R"A(v_mfma_f32_16x16x32_bf16 v[220:223],a[188:191],v[240:243],v[40:43]
ds_read_b64_tr_b16 a[214:215],v2 offset:12544
v_exp_f32 v143,v143
v_exp_f32 v148,v148
v_exp_f32 v149,v149
v_mfma_f32_16x16x32_bf16 v[220:223],a[184:187],v[208:211],v[220:223]
ds_read_b64_tr_b16 a[220:221],v3 offset:8448
ds_read_b64_tr_b16 a[222:223],v3 offset:12544
v_cvt_pk_bf16_f32 v133,v134,v135
v_exp_f32 v150,v150
v_exp_f32 v151,v151
v_cvt_pk_bf16_f32 v134,v140,v141
v_cvt_pk_bf16_f32 v135,v142,v143
v_cvt_pk_bf16_f32 v148,v148,v149
v_exp_f32 v156,v156
v_exp_f32 v157,v157
v_exp_f32 v158,v158
v_exp_f32 v159,v159
v_mfma_f32_16x16x32_bf16 v[220:223],a[180:183],v[176:179],v[220:223]
ds_read_b64_tr_b16 a[216:217],v3 offset:256
ds_read_b64_tr_b16 a[218:219],v3 offset:4352
v_cvt_pk_bf16_f32 v149,v150,v151
v_cvt_pk_bf16_f32 v150,v156,v157
v_cvt_pk_bf16_f32 v151,v158,v159
v_mfma_f32_16x16x32_bf16 v[220:223],a[176:179],v[144:147],v[220:223]
s_waitcnt lgkmcnt(6)
ds_read_b64_tr_b16 a[224:225],v2 offset:512
ds_read_b64_tr_b16 a[226:227],v2 offset:4608
v_mfma_f32_16x16x32_bf16 a[0:3],a[192:195],v[132:135],a[0:3]
ds_read_b64_tr_b16 a[228:229],v2 offset:8704
ds_read_b64_tr_b16 a[230:231],v2 offset:12800
v_mfma_f32_16x16x32_bf16 a[0:3],a[196:199],v[148:151],a[0:3]
ds_read_b64_tr_b16 a[236:237],v3 offset:8704
ds_read_b64_tr_b16 a[238:239],v3 offset:12800
v_mfma_f32_16x16x32_bf16 a[4:7],a[204:207],v[148:151],a[4:7]
ds_read_b64_tr_b16 a[232:233],v3 offset:512
ds_read_b64_tr_b16 a[234:235],v3 offset:4608
v_mfma_f32_16x16x32_bf16 a[4:7],a[200:203],v[132:135],a[4:7]
ds_read_b64_tr_b16 a[240:241],v2 offset:768
ds_read_b64_tr_b16 a[242:243],v2 offset:4864
v_mfma_f32_16x16x32_bf16 a[8:11],a[208:211],v[132:135],a[8:11]
s_memrealtime s[76:77]
ds_read_b64_tr_b16 a[244:245],v2 offset:8960
ds_read_b64_tr_b16 a[246:247],v2 offset:13056
s_waitcnt lgkmcnt(15)
v_mfma_f32_16x16x32_bf16 a[8:11],a[212:215],v[148:151],a[8:11]
s_waitcnt lgkmcnt(14)
ds_read_b64_tr_b16 a[252:253],v3 offset:8960
ds_read_b64_tr_b16 a[254:255],v3 offset:13056
v_mfma_f32_16x16x32_bf16 a[12:15],a[220:223],v[148:151],a[12:15]
s_waitcnt lgkmcnt(14)
ds_read_b64_tr_b16 a[248:249],v3 offset:768
ds_read_b64_tr_b16 a[250:251],v3 offset:4864
v_mfma_f32_16x16x32_bf16 a[12:15],a[216:219],v[132:135],a[12:15]
s_waitcnt lgkmcnt(14)
v_mul_f32 v118,v23,v118
v_mul_f32 v119,v23,v119
v_mfma_f32_16x16x32_bf16 a[16:19],a[224:227],v[132:135],a[16:19]
s_waitcnt lgkmcnt(12)
v_mul_f32 v122,v23,v122
v_mul_f32 v123,v23,v123
v_mfma_f32_16x16x32_bf16 a[16:19],a[228:231],v[148:151],a[16:19]
s_waitcnt lgkmcnt(10)
v_mul_f32 v126,v23,v126
v_mul_f32 v127,v23,v127
v_mfma_f32_16x16x32_bf16 a[20:23],a[236:239],v[148:151],a[20:23]
s_waitcnt lgkmcnt(8)
v_exp_f32 v164,v164
v_mfma_f32_16x16x32_bf16 a[20:23],a[232:235],v[132:135],a[20:23]
s_waitcnt lgkmcnt(0)
v_exp_f32 v165,v165
v_mfma_f32_16x16x32_bf16 a[24:27],a[240:243],v[132:135],a[24:27]
s_sub_u32 s79,s78,s76
v_exp_f32 v180,v180
v_mfma_f32_16x16x32_bf16 a[24:27],a[244:247],v[148:151],a[24:27]
s_add_u32 s78,s76,s60
v_exp_f32 v181,v181
v_mfma_f32_16x16x32_bf16 a[28:31],a[252:255],v[148:151],a[28:31]
s_cmp_gt_i32 s79,0
s_cbranch_scc1 L_ps5_%=
L_pr5_%=:
v_mul_f32 v16,v23,v16
v_mfma_f32_16x16x32_bf16 a[28:31],a[248:251],v[132:135],a[28:31]
s_cmp_eq_u32 s50,131072
s_cselect_b32 s62,163840,0
v_mul_f32 v17,v23,v17
v_cvt_pk_bf16_f32 v118,v118,v119
v_mul_f32 v12,v23,v12
v_mul_f32 v13,v23,v13
v_cvt_pk_bf16_f32 v122,v122,v123
v_mul_f32 v14,v23,v14
v_mul_f32 v15,v23,v15
v_cvt_pk_bf16_f32 v126,v126,v127
v_exp_f32 v166,v166
v_exp_f32 v167,v167
v_exp_f32 v182,v182
v_exp_f32 v183,v183
v_cvt_pk_bf16_f32 v119,v16,v17
v_cvt_pk_bf16_f32 v123,v12,v13
v_cvt_pk_bf16_f32 v127,v14,v15
v_cvt_pk_bf16_f32 v164,v164,v165
v_exp_f32 v172,v172
v_exp_f32 v173,v173
v_exp_f32 v174,v174
v_exp_f32 v175,v175
v_cvt_pk_bf16_f32 v180,v180,v181
v_exp_f32 v188,v188
v_exp_f32 v189,v189
v_exp_f32 v190,v190
v_exp_f32 v191,v191
v_mfma_f32_16x16x32_bf16 v[48:51],v[28:31],v[132:135],v[48:51]
s_sub_u32 s48,32768,s62
s_cmp_lg_u64 s[56:57],0
s_cbranch_scc1 L_slow0_%=
L_sret0_%=:
s_cmp_eq_u32 s49,163840
s_cselect_b32 s49,0,s49
s_add_u32 s50,s50,s48
v_cvt_pk_bf16_f32 v165,v166,v167
v_cvt_pk_bf16_f32 v181,v182,v183
v_add_u32 v1,s48,v1
v_cvt_pk_bf16_f32 v166,v172,v173
v_cvt_pk_bf16_f32 v167,v174,v175
v_cvt_pk_bf16_f32 v182,v188,v189
v_cvt_pk_bf16_f32 v183,v190,v191
v_mfma_f32_16x16x32_bf16 v[48:51],v[28:31],v[148:151],v[48:51]
s_mov_b32 s53,11
L_att1_%=:
v_mfma_f32_16x16x32_bf16 v[228:231],a[128:131],v[152:155],v[44:47]
s_waitcnt vmcnt(16) lgkmcnt(0)
v_exp_f32 v196,v196
v_mfma_f32_16x16x32_bf16 v[228:231],a[132:135],v[184:187],v[228:231]
s_barrier
s_add_u32 m0,s49,s51
ds_read_b128 a[128:131],v1
v_mfma_f32_16x16x32_bf16 v[228:231],a[136:139],v[216:219],v[228:231]
s_mov_b32 s90,L_fast_end_%=-L_pc_%=+128
v_exp_f32 v197,v197
v_mfma_f32_16x16x32_bf16 v[228:231],a[140:143],v[248:251],v[228:231]
s_add_u32 s54,s54,16384
ds_read_b128 a[132:135],v1 offset:256
v_cvt_pk_bf16_f32 v196,v196,v197
v_mfma_f32_16x16x32_bf16 v[236:239],a[156:159],v[248:251],v[44:47]
s_sub_u32 s53,s53,1
buffer_load_dwordx4 v4,s[20:23],s47 offen nt lds
v_mfma_f32_16x16x32_bf16 v[236:239],a[152:155],v[216:219],v[236:239]
s_and_b32 s46,s54,0xfffc0000
ds_read_b128 a[136:139],v1 offset:512
v_add_u32 v2,s48,v2
v_mfma_f32_16x16x32_bf16 v[236:239],a[148:151],v[184:187],v[236:239]
v_exp_f32 v198,v198
v_mfma_f32_16x16x32_bf16 v[236:239],a[144:147],v[152:155],v[236:239]
ds_read_b128 a[140:143],v1 offset:768
v_add_u32 v3,s48,v3
v_mfma_f32_16x16x32_bf16 v[244:247],a[160:163],v[152:155],v[44:47]
v_exp_f32 v199,v199
v_mfma_f32_16x16x32_bf16 v[244:247],a[164:167],v[184:187],v[244:247]
ds_read_b128 a[156:159],v1 offset:4864
v_cvt_pk_bf16_f32 v197,v198,v199
v_mfma_f32_16x16x32_bf16 v[244:247],a[168:171],v[216:219],v[244:247]
v_exp_f32 v204,v204
v_mfma_f32_16x16x32_bf16 v[244:247],a[172:175],v[248:251],v[244:247]
s_cmp_eq_u32 s50,131072
s_cselect_b32 s62,163840,0
ds_read_b128 a[152:155],v1 offset:4608
v_mfma_f32_16x16x32_bf16 v[252:255],a[188:191],v[248:251],v[44:47]
s_sub_u32 s48,32768,s62
v_exp_f32 v205,v205
v_mfma_f32_16x16x32_bf16 v[252:255],a[184:187],v[216:219],v[252:255]
s_add_u32 s50,s50,s48
ds_read_b128 a[148:151],v1 offset:4352
v_cvt_pk_bf16_f32 v198,v204,v205
v_mfma_f32_16x16x32_bf16 v[252:255],a[180:183],v[184:187],v[252:255]
v_exp_f32 v206,v206
v_mfma_f32_16x16x32_bf16 v[252:255],a[176:179],v[152:155],v[252:255]
ds_read_b128 a[144:147],v1 offset:4096
v_mfma_f32_16x16x32_bf16 a[32:35],a[192:195],v[164:167],a[32:35]
v_exp_f32 v207,v207
v_mfma_f32_16x16x32_bf16 a[32:35],a[196:199],v[180:183],a[32:35]
ds_read_b128 a[160:163],v1 offset:8192
v_cvt_pk_bf16_f32 v199,v206,v207
v_mfma_f32_16x16x32_bf16 a[36:39],a[204:207],v[180:183],a[36:39]
buffer_load_dwordx4 v4,s[20:23],s47 offen offset:1024 nt lds
v_mfma_f32_16x16x32_bf16 a[36:39],a[200:203],v[164:167],a[36:39]
ds_read_b128 a[164:167],v1 offset:8448
v_mfma_f32_16x16x32_bf16 a[40:43],a[208:211],v[164:167],a[40:43]
v_exp_f32 v212,v212
v_mfma_f32_16x16x32_bf16 a[40:43],a[212:215],v[180:183],a[40:43]
ds_read_b128 a[168:171],v1 offset:8704
v_mfma_f32_16x16x32_bf16 a[44:47],a[220:223],v[180:183],a[44:47]
v_exp_f32 v213,v213
v_mfma_f32_16x16x32_bf16 a[44:47],a[216:219],v[164:167],a[44:47]
ds_read_b128 a[172:175],v1 offset:8960
v_cvt_pk_bf16_f32 v212,v212,v213
v_mfma_f32_16x16x32_bf16 a[48:51],a[224:227],v[164:167],a[48:51]
v_exp_f32 v214,v214
v_mfma_f32_16x16x32_bf16 a[48:51],a[228:231],v[180:183],a[48:51]
ds_read_b128 a[188:191],v1 offset:13056
v_mfma_f32_16x16x32_bf16 a[52:55],a[236:239],v[180:183],a[52:55]
v_exp_f32 v215,v215
v_mfma_f32_16x16x32_bf16 a[52:55],a[232:235],v[164:167],a[52:55]
ds_read_b128 a[184:187],v1 offset:12800
v_cvt_pk_bf16_f32 v213,v214,v215
v_mfma_f32_16x16x32_bf16 a[56:59],a[240:243],v[164:167],a[56:59]
v_exp_f32 v220,v220
v_mfma_f32_16x16x32_bf16 a[56:59],a[244:247],v[180:183],a[56:59]
ds_read_b128 a[180:183],v1 offset:12544
v_mfma_f32_16x16x32_bf16 a[60:63],a[252:255],v[180:183],a[60:63]
v_exp_f32 v221,v221
v_mfma_f32_16x16x32_bf16 a[60:63],a[248:251],v[164:167],a[60:63]
ds_read_b128 a[176:179],v1 offset:12288
v_cvt_pk_bf16_f32 v214,v220,v221
v_mfma_f32_16x16x32_bf16 v[52:55],v[28:31],v[164:167],v[52:55]
buffer_load_dwordx4 v5,s[20:23],s47 offen offset:2048 nt lds
v_mfma_f32_16x16x32_bf16 v[52:55],v[28:31],v[180:183],v[52:55]
s_waitcnt lgkmcnt(3)
v_exp_f32 v222,v222
v_mfma_f32_16x16x32_bf16 v[132:135],a[128:131],v[128:131],v[32:35]
v_exp_f32 v223,v223
v_mfma_f32_16x16x32_bf16 v[132:135],a[132:135],v[160:163],v[132:135]
v_cvt_pk_bf16_f32 v215,v222,v223
v_add_u32 v1,s48,v1
v_mfma_f32_16x16x32_bf16 v[132:135],a[136:139],v[192:195],v[132:135]
v_exp_f32 v228,v228
v_mfma_f32_16x16x32_bf16 v[132:135],a[140:143],v[224:227],v[132:135]
v_exp_f32 v229,v229
v_mfma_f32_16x16x32_bf16 v[140:143],a[156:159],v[224:227],v[32:35]
v_exp_f32 v230,v230
v_mfma_f32_16x16x32_bf16 v[140:143],a[152:155],v[192:195],v[140:143]
v_exp_f32 v231,v231
v_mfma_f32_16x16x32_bf16 v[140:143],a[148:151],v[160:163],v[140:143]
v_cvt_pk_bf16_f32 v228,v228,v229
v_cvt_pk_bf16_f32 v229,v230,v231
v_mfma_f32_16x16x32_bf16 v[140:143],a[144:147],v[128:131],v[140:143]
v_exp_f32 v236,v236
v_mfma_f32_16x16x32_bf16 v[148:151],a[160:163],v[128:131],v[32:35]
v_exp_f32 v237,v237
v_mfma_f32_16x16x32_bf16 v[148:151],a[164:167],v[160:163],v[148:151]
v_exp_f32 v238,v238
v_mfma_f32_16x16x32_bf16 v[148:151],a[168:171],v[192:195],v[148:151]
v_exp_f32 v239,v239
v_mfma_f32_16x16x32_bf16 v[148:151],a[172:175],v[224:227],v[148:151]
v_exp_f32 v244,v244
v_mfma_f32_16x16x32_bf16 v[156:159],a[188:191],v[224:227],v[32:35]
s_waitcnt lgkmcnt(0)
buffer_load_dwordx4 v5,s[20:23],s47 offen offset:3072 nt lds
v_mfma_f32_16x16x32_bf16 v[156:159],a[184:187],v[192:195],v[156:159]
v_exp_f32 v245,v245
v_mfma_f32_16x16x32_bf16 v[156:159],a[180:183],v[160:163],v[156:159]
v_exp_f32 v246,v246
v_mfma_f32_16x16x32_bf16 v[156:159],a[176:179],v[128:131],v[156:159]
v_exp_f32 v247,v247
v_mfma_f32_16x16x32_bf16 a[64:67],a[192:195],v[196:199],a[64:67]
v_cvt_pk_bf16_f32 v230,v236,v237
v_cvt_pk_bf16_f32 v231,v238,v239
v_mfma_f32_16x16x32_bf16 a[64:67],a[196:199],v[212:215],a[64:67]
v_cvt_pk_bf16_f32 v244,v244,v245
v_cvt_pk_bf16_f32 v245,v246,v247
v_mfma_f32_16x16x32_bf16 a[68:71],a[204:207],v[212:215],a[68:71]
v_exp_f32 v252,v252
v_mfma_f32_16x16x32_bf16 a[68:71],a[200:203],v[196:199],a[68:71]
v_exp_f32 v253,v253
v_mfma_f32_16x16x32_bf16 a[72:75],a[208:211],v[196:199],a[72:75]
v_exp_f32 v254,v254
v_mfma_f32_16x16x32_bf16 a[72:75],a[212:215],v[212:215],a[72:75]
v_exp_f32 v255,v255
v_mfma_f32_16x16x32_bf16 a[76:79],a[220:223],v[212:215],a[76:79]
v_cvt_pk_bf16_f32 v246,v252,v253
v_cvt_pk_bf16_f32 v247,v254,v255
v_mfma_f32_16x16x32_bf16 a[76:79],a[216:219],v[196:199],a[76:79]
s_add_u32 m0,s49,s52
v_exp_f32 v132,v132
v_mfma_f32_16x16x32_bf16 a[80:83],a[224:227],v[196:199],a[80:83]
s_add_u32 s49,s49,32768
v_exp_f32 v133,v133
v_mfma_f32_16x16x32_bf16 a[80:83],a[228:231],v[212:215],a[80:83]
v_exp_f32 v134,v134
v_mfma_f32_16x16x32_bf16 a[84:87],a[236:239],v[212:215],a[84:87]
buffer_load_dwordx4 v4,s[24:27],s47 offen nt lds
v_mfma_f32_16x16x32_bf16 a[84:87],a[232:235],v[196:199],a[84:87]
v_exp_f32 v135,v135
v_mfma_f32_16x16x32_bf16 a[88:91],a[240:243],v[196:199],a[88:91]
v_cvt_pk_bf16_f32 v132,v132,v133
v_cvt_pk_bf16_f32 v133,v134,v135
v_mfma_f32_16x16x32_bf16 a[88:91],a[244:247],v[212:215],a[88:91]
v_exp_f32 v140,v140
v_mfma_f32_16x16x32_bf16 a[92:95],a[252:255],v[212:215],a[92:95]
v_exp_f32 v141,v141
v_mfma_f32_16x16x32_bf16 a[92:95],a[248:251],v[196:199],a[92:95]
v_exp_f32 v142,v142
v_mfma_f32_16x16x32_bf16 v[56:59],v[28:31],v[196:199],v[56:59]
v_exp_f32 v143,v143
v_mfma_f32_16x16x32_bf16 v[56:59],v[28:31],v[212:215],v[56:59]
v_exp_f32 v148,v148
v_mfma_f32_16x16x32_bf16 v[164:167],a[128:131],v[136:139],v[36:39]
v_exp_f32 v149,v149
v_mfma_f32_16x16x32_bf16 v[164:167],a[132:135],v[168:171],v[164:167]
v_exp_f32 v150,v150
v_mfma_f32_16x16x32_bf16 v[164:167],a[136:139],v[200:203],v[164:167]
v_exp_f32 v151,v151
v_mfma_f32_16x16x32_bf16 v[164:167],a[140:143],v[232:235],v[164:167]
v_cvt_pk_bf16_f32 v134,v140,v141
)A"
R"A(v_cvt_pk_bf16_f32 v135,v142,v143
v_mfma_f32_16x16x32_bf16 v[172:175],a[156:159],v[232:235],v[36:39]
v_cvt_pk_bf16_f32 v148,v148,v149
v_cvt_pk_bf16_f32 v149,v150,v151
v_mfma_f32_16x16x32_bf16 v[172:175],a[152:155],v[200:203],v[172:175]
v_exp_f32 v156,v156
v_mfma_f32_16x16x32_bf16 v[172:175],a[148:151],v[168:171],v[172:175]
buffer_load_dwordx4 v5,s[24:27],s47 offen offset:1024 nt lds
v_mfma_f32_16x16x32_bf16 v[172:175],a[144:147],v[136:139],v[172:175]
v_exp_f32 v157,v157
v_mfma_f32_16x16x32_bf16 v[180:183],a[160:163],v[136:139],v[36:39]
v_exp_f32 v158,v158
v_mfma_f32_16x16x32_bf16 v[180:183],a[164:167],v[168:171],v[180:183]
v_exp_f32 v159,v159
v_mfma_f32_16x16x32_bf16 v[180:183],a[168:171],v[200:203],v[180:183]
v_cvt_pk_bf16_f32 v150,v156,v157
v_cvt_pk_bf16_f32 v151,v158,v159
v_mfma_f32_16x16x32_bf16 v[180:183],a[172:175],v[232:235],v[180:183]
v_exp_f32 v164,v164
v_mfma_f32_16x16x32_bf16 v[188:191],a[188:191],v[232:235],v[36:39]
v_exp_f32 v165,v165
v_mfma_f32_16x16x32_bf16 v[188:191],a[184:187],v[200:203],v[188:191]
v_exp_f32 v166,v166
v_mfma_f32_16x16x32_bf16 v[188:191],a[180:183],v[168:171],v[188:191]
v_exp_f32 v167,v167
v_mfma_f32_16x16x32_bf16 v[188:191],a[176:179],v[136:139],v[188:191]
v_cvt_pk_bf16_f32 v164,v164,v165
v_cvt_pk_bf16_f32 v165,v166,v167
v_mfma_f32_16x16x32_bf16 a[96:99],a[192:195],v[228:231],a[96:99]
s_cmp_eq_u32 s49,163840
s_cselect_b32 s49,0,s49
ds_read_b64_tr_b16 a[192:193],v2
v_mfma_f32_16x16x32_bf16 a[96:99],a[196:199],v[244:247],a[96:99]
ds_read_b64_tr_b16 a[194:195],v2 offset:4096
v_mfma_f32_16x16x32_bf16 a[100:103],a[204:207],v[244:247],a[100:103]
ds_read_b64_tr_b16 a[196:197],v2 offset:8192
v_mfma_f32_16x16x32_bf16 a[100:103],a[200:203],v[228:231],a[100:103]
ds_read_b64_tr_b16 a[198:199],v2 offset:12288
v_mfma_f32_16x16x32_bf16 a[104:107],a[208:211],v[228:231],a[104:107]
buffer_load_dwordx4 v4,s[24:27],s47 offen offset:2048 nt lds
v_mfma_f32_16x16x32_bf16 a[104:107],a[212:215],v[244:247],a[104:107]
ds_read_b64_tr_b16 a[204:205],v3 offset:8192
v_mfma_f32_16x16x32_bf16 a[108:111],a[220:223],v[244:247],a[108:111]
ds_read_b64_tr_b16 a[206:207],v3 offset:12288
v_mfma_f32_16x16x32_bf16 a[108:111],a[216:219],v[228:231],a[108:111]
ds_read_b64_tr_b16 a[200:201],v3
v_mfma_f32_16x16x32_bf16 a[112:115],a[224:227],v[228:231],a[112:115]
ds_read_b64_tr_b16 a[202:203],v3 offset:4096
v_mfma_f32_16x16x32_bf16 a[112:115],a[228:231],v[244:247],a[112:115]
ds_read_b64_tr_b16 a[208:209],v2 offset:256
v_mfma_f32_16x16x32_bf16 a[116:119],a[236:239],v[244:247],a[116:119]
ds_read_b64_tr_b16 a[210:211],v2 offset:4352
v_mfma_f32_16x16x32_bf16 a[116:119],a[232:235],v[228:231],a[116:119]
ds_read_b64_tr_b16 a[212:213],v2 offset:8448
v_mfma_f32_16x16x32_bf16 a[120:123],a[240:243],v[228:231],a[120:123]
ds_read_b64_tr_b16 a[214:215],v2 offset:12544
v_mfma_f32_16x16x32_bf16 a[120:123],a[244:247],v[244:247],a[120:123]
ds_read_b64_tr_b16 a[220:221],v3 offset:8448
v_mfma_f32_16x16x32_bf16 a[124:127],a[252:255],v[244:247],a[124:127]
ds_read_b64_tr_b16 a[222:223],v3 offset:12544
v_mfma_f32_16x16x32_bf16 a[124:127],a[248:251],v[228:231],a[124:127]
ds_read_b64_tr_b16 a[216:217],v3 offset:256
v_mfma_f32_16x16x32_bf16 v[60:63],v[28:31],v[228:231],v[60:63]
ds_read_b64_tr_b16 a[218:219],v3 offset:4352
v_mfma_f32_16x16x32_bf16 v[60:63],v[28:31],v[244:247],v[60:63]
ds_read_b64_tr_b16 a[224:225],v2 offset:512
v_mfma_f32_16x16x32_bf16 v[196:199],a[128:131],v[144:147],v[40:43]
buffer_load_dwordx4 v5,s[24:27],s47 offen offset:3072 nt lds
v_mfma_f32_16x16x32_bf16 v[196:199],a[132:135],v[176:179],v[196:199]
s_add_u32 s47,s47,16384
s_and_b32 s47,s47,0x3ffff
ds_read_b64_tr_b16 a[226:227],v2 offset:4608
v_mfma_f32_16x16x32_bf16 v[196:199],a[136:139],v[208:211],v[196:199]
s_or_b32 s47,s47,s46
ds_read_b64_tr_b16 a[228:229],v2 offset:8704
v_mfma_f32_16x16x32_bf16 v[196:199],a[140:143],v[240:243],v[196:199]
ds_read_b64_tr_b16 a[230:231],v2 offset:12800
v_mfma_f32_16x16x32_bf16 v[204:207],a[156:159],v[240:243],v[40:43]
ds_read_b64_tr_b16 a[236:237],v3 offset:8704
v_mfma_f32_16x16x32_bf16 v[204:207],a[152:155],v[208:211],v[204:207]
ds_read_b64_tr_b16 a[238:239],v3 offset:12800
v_mfma_f32_16x16x32_bf16 v[204:207],a[148:151],v[176:179],v[204:207]
ds_read_b64_tr_b16 a[232:233],v3 offset:512
v_mfma_f32_16x16x32_bf16 v[204:207],a[144:147],v[144:147],v[204:207]
ds_read_b64_tr_b16 a[234:235],v3 offset:4608
v_mfma_f32_16x16x32_bf16 v[212:215],a[160:163],v[144:147],v[40:43]
ds_read_b64_tr_b16 a[240:241],v2 offset:768
v_mfma_f32_16x16x32_bf16 v[212:215],a[164:167],v[176:179],v[212:215]
ds_read_b64_tr_b16 a[242:243],v2 offset:4864
v_mfma_f32_16x16x32_bf16 v[212:215],a[168:171],v[208:211],v[212:215]
ds_read_b64_tr_b16 a[244:245],v2 offset:8960
v_mfma_f32_16x16x32_bf16 v[212:215],a[172:175],v[240:243],v[212:215]
ds_read_b64_tr_b16 a[246:247],v2 offset:13056
v_mfma_f32_16x16x32_bf16 v[220:223],a[188:191],v[240:243],v[40:43]
ds_read_b64_tr_b16 a[252:253],v3 offset:8960
v_mfma_f32_16x16x32_bf16 v[220:223],a[184:187],v[208:211],v[220:223]
ds_read_b64_tr_b16 a[254:255],v3 offset:13056
v_mfma_f32_16x16x32_bf16 v[220:223],a[180:183],v[176:179],v[220:223]
ds_read_b64_tr_b16 a[248:249],v3 offset:768
v_mfma_f32_16x16x32_bf16 v[220:223],a[176:179],v[144:147],v[220:223]
s_waitcnt lgkmcnt(7)
ds_read_b64_tr_b16 a[250:251],v3 offset:4864
v_mfma_f32_16x16x32_bf16 a[0:3],a[192:195],v[132:135],a[0:3]
buffer_load_dword v26,v24,s[88:91],s93 offen
v_mfma_f32_16x16x32_bf16 a[0:3],a[196:199],v[148:151],a[0:3]
v_exp_f32 v180,v180
v_mfma_f32_16x16x32_bf16 a[4:7],a[204:207],v[148:151],a[4:7]
v_exp_f32 v181,v181
v_mfma_f32_16x16x32_bf16 a[4:7],a[200:203],v[132:135],a[4:7]
v_exp_f32 v182,v182
v_mfma_f32_16x16x32_bf16 a[8:11],a[208:211],v[132:135],a[8:11]
s_memrealtime s[76:77]
v_exp_f32 v183,v183
v_mfma_f32_16x16x32_bf16 a[8:11],a[212:215],v[148:151],a[8:11]
v_exp_f32 v172,v172
v_mfma_f32_16x16x32_bf16 a[12:15],a[220:223],v[148:151],a[12:15]
v_exp_f32 v173,v173
v_mfma_f32_16x16x32_bf16 a[12:15],a[216:219],v[132:135],a[12:15]
v_exp_f32 v174,v174
v_mfma_f32_16x16x32_bf16 a[16:19],a[224:227],v[132:135],a[16:19]
v_exp_f32 v175,v175
v_mfma_f32_16x16x32_bf16 a[16:19],a[228:231],v[148:151],a[16:19]
v_cvt_pk_bf16_f32 v180,v180,v181
v_cvt_pk_bf16_f32 v181,v182,v183
v_mfma_f32_16x16x32_bf16 a[20:23],a[236:239],v[148:151],a[20:23]
v_exp_f32 v188,v188
v_mfma_f32_16x16x32_bf16 a[20:23],a[232:235],v[132:135],a[20:23]
s_waitcnt lgkmcnt(0)
v_exp_f32 v189,v189
v_mfma_f32_16x16x32_bf16 a[24:27],a[240:243],v[132:135],a[24:27]
s_sub_u32 s79,s78,s76
v_exp_f32 v190,v190
v_mfma_f32_16x16x32_bf16 a[24:27],a[244:247],v[148:151],a[24:27]
s_add_u32 s78,s76,s60
v_exp_f32 v191,v191
v_mfma_f32_16x16x32_bf16 a[28:31],a[252:255],v[148:151],a[28:31]
s_cmp_gt_i32 s79,0
s_cbranch_scc1 L_ps6_%=
L_pr6_%=:
v_cvt_pk_bf16_f32 v166,v172,v173
v_mfma_f32_16x16x32_bf16 a[28:31],a[248:251],v[132:135],a[28:31]
v_cvt_pk_bf16_f32 v167,v174,v175
v_cvt_pk_bf16_f32 v182,v188,v189
v_mfma_f32_16x16x32_bf16 v[48:51],v[28:31],v[132:135],v[48:51]
v_cvt_pk_bf16_f32 v183,v190,v191
s_cmp_lg_u32 s53,0
v_mfma_f32_16x16x32_bf16 v[48:51],v[28:31],v[148:151],v[48:51]
s_cbranch_scc1 L_att1_%=
L_endseg_%=:
v_mfma_f32_16x16x32_bf16 v[228:231],a[128:131],v[152:155],v[44:47]
s_waitcnt vmcnt(19)
s_add_u32 s80,s71,0
s_and_b32 s80,s80,3
s_lshl_b32 s80,s80,1
v_mfma_f32_16x16x32_bf16 v[228:231],a[132:135],v[184:187],v[228:231]
s_barrier
s_mul_i32 s80,s80,163840
s_add_u32 s80,s80,s81
v_mfma_f32_16x16x32_bf16 v[228:231],a[136:139],v[216:219],v[228:231]
s_add_u32 s83,s80,20480
ds_read_b128 a[128:131],v1
v_add_u32 v2,s48,v2
v_mfma_f32_16x16x32_bf16 v[228:231],a[140:143],v[248:251],v[228:231]
s_lshl_b32 s43,s41,2
v_exp_f32 v196,v196
v_mfma_f32_16x16x32_bf16 v[236:239],a[156:159],v[248:251],v[44:47]
s_add_u32 s43,s43,s70
ds_read_b128 a[132:135],v1 offset:256
v_add_u32 v3,s48,v3
v_mfma_f32_16x16x32_bf16 v[236:239],a[152:155],v[216:219],v[236:239]
s_lshl_b32 s43,s43,19
v_exp_f32 v197,v197
v_mfma_f32_16x16x32_bf16 v[236:239],a[148:151],v[184:187],v[236:239]
s_add_u32 s43,s43,s55
ds_read_b128 a[136:139],v1 offset:512
v_cvt_pk_bf16_f32 v196,v196,v197
v_mfma_f32_16x16x32_bf16 v[236:239],a[144:147],v[152:155],v[236:239]
s_mov_b64 s[28:29],s[12:13]
v_exp_f32 v198,v198
v_mfma_f32_16x16x32_bf16 v[244:247],a[160:163],v[152:155],v[44:47]
s_add_u32 m0,s82,0
ds_read_b128 a[140:143],v1 offset:768
v_lshrrev_b32 v4,6,v6
v_mfma_f32_16x16x32_bf16 v[244:247],a[164:167],v[184:187],v[244:247]
s_mov_b32 s30,475136
v_exp_f32 v199,v199
v_mfma_f32_16x16x32_bf16 v[244:247],a[168:171],v[216:219],v[244:247]
s_mov_b32 s31,0x20000
ds_read_b128 a[156:159],v1 offset:4864
v_cvt_pk_bf16_f32 v197,v198,v199
v_mfma_f32_16x16x32_bf16 v[244:247],a[172:175],v[248:251],v[244:247]
s_add_u32 s49,s49,32768
buffer_load_dwordx4 v7,s[72:75],s80 offen lds
v_mfma_f32_16x16x32_bf16 v[252:255],a[188:191],v[248:251],v[44:47]
s_cmp_eq_u32 s50,131072
s_cselect_b32 s62,163840,0
ds_read_b128 a[152:155],v1 offset:4608
v_mfma_f32_16x16x32_bf16 v[252:255],a[184:187],v[216:219],v[252:255]
s_sub_u32 s48,32768,s62
v_exp_f32 v204,v204
v_mfma_f32_16x16x32_bf16 v[252:255],a[180:183],v[184:187],v[252:255]
s_add_u32 s50,s50,s48
ds_read_b128 a[148:151],v1 offset:4352
v_mul_u32_u24 v4,320,v4
v_mfma_f32_16x16x32_bf16 v[252:255],a[176:179],v[152:155],v[252:255]
v_exp_f32 v205,v205
v_mfma_f32_16x16x32_bf16 a[32:35],a[192:195],v[164:167],a[32:35]
ds_read_b128 a[144:147],v1 offset:4096
v_cvt_pk_bf16_f32 v198,v204,v205
v_mfma_f32_16x16x32_bf16 a[32:35],a[196:199],v[180:183],a[32:35]
v_exp_f32 v206,v206
v_mfma_f32_16x16x32_bf16 a[36:39],a[204:207],v[180:183],a[36:39]
s_cmp_eq_u32 s50,131072
s_cselect_b32 s62,163840,0
ds_read_b128 a[160:163],v1 offset:8192
v_mfma_f32_16x16x32_bf16 a[36:39],a[200:203],v[164:167],a[36:39]
v_exp_f32 v207,v207
v_mfma_f32_16x16x32_bf16 a[40:43],a[208:211],v[164:167],a[40:43]
ds_read_b128 a[164:167],v1 offset:8448
v_cvt_pk_bf16_f32 v199,v206,v207
v_mfma_f32_16x16x32_bf16 a[40:43],a[212:215],v[180:183],a[40:43]
v_exp_f32 v212,v212
v_mfma_f32_16x16x32_bf16 a[44:47],a[220:223],v[180:183],a[44:47]
ds_read_b128 a[168:171],v1 offset:8704
v_bfe_u32 v5,v6,4,2
v_mfma_f32_16x16x32_bf16 a[44:47],a[216:219],v[164:167],a[44:47]
v_exp_f32 v213,v213
v_mfma_f32_16x16x32_bf16 a[48:51],a[224:227],v[164:167],a[48:51]
buffer_load_dwordx4 v8,s[72:75],s80 offen offset:1024 lds
v_mfma_f32_16x16x32_bf16 a[48:51],a[228:231],v[180:183],a[48:51]
ds_read_b128 a[172:175],v1 offset:8960
v_cvt_pk_bf16_f32 v212,v212,v213
v_mfma_f32_16x16x32_bf16 a[52:55],a[236:239],v[180:183],a[52:55]
v_exp_f32 v214,v214
v_mfma_f32_16x16x32_bf16 a[52:55],a[232:235],v[164:167],a[52:55]
ds_read_b128 a[188:191],v1 offset:13056
v_lshl_add_u32 v4,v5,3,v4
v_mfma_f32_16x16x32_bf16 a[56:59],a[240:243],v[164:167],a[56:59]
v_exp_f32 v215,v215
v_mfma_f32_16x16x32_bf16 a[56:59],a[244:247],v[180:183],a[56:59]
ds_read_b128 a[184:187],v1 offset:12800
v_cvt_pk_bf16_f32 v213,v214,v215
v_mfma_f32_16x16x32_bf16 a[60:63],a[252:255],v[180:183],a[60:63]
v_exp_f32 v220,v220
v_mfma_f32_16x16x32_bf16 a[60:63],a[248:251],v[164:167],a[60:63]
ds_read_b128 a[180:183],v1 offset:12544
v_bfe_u32 v5,v6,8,1
v_mfma_f32_16x16x32_bf16 v[52:55],v[28:31],v[164:167],v[52:55]
v_exp_f32 v221,v221
v_mfma_f32_16x16x32_bf16 v[52:55],v[28:31],v[180:183],v[52:55]
s_waitcnt lgkmcnt(3)
ds_read_b128 a[176:179],v1 offset:12288
v_cvt_pk_bf16_f32 v214,v220,v221
v_mfma_f32_16x16x32_bf16 v[132:135],a[128:131],v[128:131],v[32:35]
v_exp_f32 v222,v222
v_mfma_f32_16x16x32_bf16 v[132:135],a[132:135],v[160:163],v[132:135]
v_exp_f32 v223,v223
v_mfma_f32_16x16x32_bf16 v[132:135],a[136:139],v[192:195],v[132:135]
v_cvt_pk_bf16_f32 v215,v222,v223
v_add_u32 v1,s48,v1
v_mfma_f32_16x16x32_bf16 v[132:135],a[140:143],v[224:227],v[132:135]
buffer_load_dwordx4 v9,s[72:75],s80 offen offset:2048 lds
v_mfma_f32_16x16x32_bf16 v[140:143],a[156:159],v[224:227],v[32:35]
v_exp_f32 v228,v228
v_mfma_f32_16x16x32_bf16 v[140:143],a[152:155],v[192:195],v[140:143]
v_exp_f32 v229,v229
v_mfma_f32_16x16x32_bf16 v[140:143],a[148:151],v[160:163],v[140:143]
v_exp_f32 v230,v230
v_mfma_f32_16x16x32_bf16 v[140:143],a[144:147],v[128:131],v[140:143]
)A"
R"A(v_exp_f32 v231,v231
v_mfma_f32_16x16x32_bf16 v[148:151],a[160:163],v[128:131],v[32:35]
buffer_store_dwordx4 v[64:67],v6,s[36:39],s84 offen sc1
v_mfma_f32_16x16x32_bf16 v[148:151],a[164:167],v[160:163],v[148:151]
v_cvt_pk_bf16_f32 v228,v228,v229
v_cvt_pk_bf16_f32 v229,v230,v231
v_mfma_f32_16x16x32_bf16 v[148:151],a[168:171],v[192:195],v[148:151]
v_exp_f32 v236,v236
v_mfma_f32_16x16x32_bf16 v[148:151],a[172:175],v[224:227],v[148:151]
s_waitcnt lgkmcnt(0)
v_exp_f32 v237,v237
v_mfma_f32_16x16x32_bf16 v[156:159],a[188:191],v[224:227],v[32:35]
v_exp_f32 v238,v238
v_mfma_f32_16x16x32_bf16 v[156:159],a[184:187],v[192:195],v[156:159]
v_exp_f32 v239,v239
v_mfma_f32_16x16x32_bf16 v[156:159],a[180:183],v[160:163],v[156:159]
v_exp_f32 v244,v244
v_mfma_f32_16x16x32_bf16 v[156:159],a[176:179],v[128:131],v[156:159]
v_exp_f32 v245,v245
v_mfma_f32_16x16x32_bf16 a[64:67],a[192:195],v[196:199],a[64:67]
buffer_load_dwordx4 v10,s[72:75],s80 offen offset:3072 lds
v_mfma_f32_16x16x32_bf16 a[64:67],a[196:199],v[212:215],a[64:67]
v_exp_f32 v246,v246
v_mfma_f32_16x16x32_bf16 a[68:71],a[204:207],v[212:215],a[68:71]
v_exp_f32 v247,v247
v_mfma_f32_16x16x32_bf16 a[68:71],a[200:203],v[196:199],a[68:71]
v_cvt_pk_bf16_f32 v230,v236,v237
v_cvt_pk_bf16_f32 v231,v238,v239
v_mfma_f32_16x16x32_bf16 a[72:75],a[208:211],v[196:199],a[72:75]
v_cvt_pk_bf16_f32 v244,v244,v245
v_cvt_pk_bf16_f32 v245,v246,v247
v_mfma_f32_16x16x32_bf16 a[72:75],a[212:215],v[212:215],a[72:75]
v_exp_f32 v252,v252
v_mfma_f32_16x16x32_bf16 a[76:79],a[220:223],v[212:215],a[76:79]
v_exp_f32 v253,v253
v_mfma_f32_16x16x32_bf16 a[76:79],a[216:219],v[196:199],a[76:79]
v_exp_f32 v254,v254
v_mfma_f32_16x16x32_bf16 a[80:83],a[224:227],v[196:199],a[80:83]
v_exp_f32 v255,v255
v_mfma_f32_16x16x32_bf16 a[80:83],a[228:231],v[212:215],a[80:83]
v_cvt_pk_bf16_f32 v246,v252,v253
v_cvt_pk_bf16_f32 v247,v254,v255
v_mfma_f32_16x16x32_bf16 a[84:87],a[236:239],v[212:215],a[84:87]
s_add_u32 m0,s82,4096
v_exp_f32 v132,v132
v_mfma_f32_16x16x32_bf16 a[84:87],a[232:235],v[196:199],a[84:87]
v_exp_f32 v133,v133
v_mfma_f32_16x16x32_bf16 a[88:91],a[240:243],v[196:199],a[88:91]
v_exp_f32 v134,v134
v_mfma_f32_16x16x32_bf16 a[88:91],a[244:247],v[212:215],a[88:91]
buffer_load_dwordx4 v11,s[72:75],s80 offen lds
v_mfma_f32_16x16x32_bf16 a[92:95],a[252:255],v[212:215],a[92:95]
v_exp_f32 v135,v135
v_mfma_f32_16x16x32_bf16 a[92:95],a[248:251],v[196:199],a[92:95]
v_cvt_pk_bf16_f32 v132,v132,v133
v_cvt_pk_bf16_f32 v133,v134,v135
v_mfma_f32_16x16x32_bf16 v[56:59],v[28:31],v[196:199],v[56:59]
v_exp_f32 v140,v140
v_mfma_f32_16x16x32_bf16 v[56:59],v[28:31],v[212:215],v[56:59]
buffer_store_dwordx4 v[68:71],v6,s[36:39],s84 offen offset:1024 sc1
v_mfma_f32_16x16x32_bf16 v[164:167],a[128:131],v[136:139],v[36:39]
v_exp_f32 v141,v141
v_mfma_f32_16x16x32_bf16 v[164:167],a[132:135],v[168:171],v[164:167]
v_exp_f32 v142,v142
v_mfma_f32_16x16x32_bf16 v[164:167],a[136:139],v[200:203],v[164:167]
v_exp_f32 v143,v143
v_mfma_f32_16x16x32_bf16 v[164:167],a[140:143],v[232:235],v[164:167]
v_exp_f32 v148,v148
v_mfma_f32_16x16x32_bf16 v[172:175],a[156:159],v[232:235],v[36:39]
v_exp_f32 v149,v149
v_mfma_f32_16x16x32_bf16 v[172:175],a[152:155],v[200:203],v[172:175]
s_add_u32 m0,s82,5120
v_exp_f32 v150,v150
v_mfma_f32_16x16x32_bf16 v[172:175],a[148:151],v[168:171],v[172:175]
v_exp_f32 v151,v151
v_mfma_f32_16x16x32_bf16 v[172:175],a[144:147],v[136:139],v[172:175]
v_cvt_pk_bf16_f32 v134,v140,v141
v_cvt_pk_bf16_f32 v135,v142,v143
v_mfma_f32_16x16x32_bf16 v[180:183],a[160:163],v[136:139],v[36:39]
buffer_load_dwordx4 v7,s[72:75],s83 offen lds
v_mfma_f32_16x16x32_bf16 v[180:183],a[164:167],v[168:171],v[180:183]
v_cvt_pk_bf16_f32 v148,v148,v149
v_cvt_pk_bf16_f32 v149,v150,v151
v_mfma_f32_16x16x32_bf16 v[180:183],a[168:171],v[200:203],v[180:183]
v_exp_f32 v156,v156
v_mfma_f32_16x16x32_bf16 v[180:183],a[172:175],v[232:235],v[180:183]
v_exp_f32 v157,v157
v_mfma_f32_16x16x32_bf16 v[188:191],a[188:191],v[232:235],v[36:39]
v_exp_f32 v158,v158
v_mfma_f32_16x16x32_bf16 v[188:191],a[184:187],v[200:203],v[188:191]
v_exp_f32 v159,v159
v_mfma_f32_16x16x32_bf16 v[188:191],a[180:183],v[168:171],v[188:191]
v_cvt_pk_bf16_f32 v150,v156,v157
v_cvt_pk_bf16_f32 v151,v158,v159
v_mfma_f32_16x16x32_bf16 v[188:191],a[176:179],v[136:139],v[188:191]
v_exp_f32 v164,v164
v_mfma_f32_16x16x32_bf16 a[96:99],a[192:195],v[228:231],a[96:99]
ds_read_b64_tr_b16 a[192:193],v2
ds_read_b64_tr_b16 a[194:195],v2 offset:4096
v_mfma_f32_16x16x32_bf16 a[96:99],a[196:199],v[244:247],a[96:99]
ds_read_b64_tr_b16 a[196:197],v2 offset:8192
v_lshlrev_b32 v5,5,v5
v_mfma_f32_16x16x32_bf16 a[100:103],a[204:207],v[244:247],a[100:103]
buffer_store_dwordx4 v[72:75],v6,s[36:39],s84 offen offset:2048 sc1
v_mfma_f32_16x16x32_bf16 a[100:103],a[200:203],v[228:231],a[100:103]
ds_read_b64_tr_b16 a[198:199],v2 offset:12288
ds_read_b64_tr_b16 a[204:205],v3 offset:8192
v_mfma_f32_16x16x32_bf16 a[104:107],a[208:211],v[228:231],a[104:107]
s_add_u32 s28,s28,s43
s_addc_u32 s29,s29,0
ds_read_b64_tr_b16 a[206:207],v3 offset:12288
v_mfma_f32_16x16x32_bf16 a[104:107],a[212:215],v[244:247],a[104:107]
s_and_b32 s29,s29,0xffff
ds_read_b64_tr_b16 a[200:201],v3
v_mfma_f32_16x16x32_bf16 a[108:111],a[220:223],v[244:247],a[108:111]
buffer_load_dwordx4 v8,s[72:75],s83 offen offset:1024 lds
v_mfma_f32_16x16x32_bf16 a[108:111],a[216:219],v[228:231],a[108:111]
ds_read_b64_tr_b16 a[202:203],v3 offset:4096
ds_read_b64_tr_b16 a[208:209],v2 offset:256
v_mfma_f32_16x16x32_bf16 a[112:115],a[224:227],v[228:231],a[112:115]
s_cmp_eq_u32 s49,163840
s_cselect_b32 s49,0,s49
ds_read_b64_tr_b16 a[210:211],v2 offset:4352
v_mfma_f32_16x16x32_bf16 a[112:115],a[228:231],v[244:247],a[112:115]
s_add_u32 s49,s49,32768
ds_read_b64_tr_b16 a[212:213],v2 offset:8448
v_mfma_f32_16x16x32_bf16 a[116:119],a[236:239],v[244:247],a[116:119]
s_cmp_eq_u32 s49,163840
s_cselect_b32 s49,0,s49
ds_read_b64_tr_b16 a[214:215],v2 offset:12544
v_mfma_f32_16x16x32_bf16 a[116:119],a[232:235],v[228:231],a[116:119]
s_add_u32 s49,s49,32768
ds_read_b64_tr_b16 a[220:221],v3 offset:8448
ds_read_b64_tr_b16 a[222:223],v3 offset:12544
v_mfma_f32_16x16x32_bf16 a[120:123],a[240:243],v[228:231],a[120:123]
s_cmp_eq_u32 s49,163840
s_cselect_b32 s49,0,s49
ds_read_b64_tr_b16 a[216:217],v3 offset:256
v_mfma_f32_16x16x32_bf16 a[120:123],a[244:247],v[244:247],a[120:123]
ds_read_b64_tr_b16 a[218:219],v3 offset:4352
v_mfma_f32_16x16x32_bf16 a[124:127],a[252:255],v[244:247],a[124:127]
ds_read_b64_tr_b16 a[224:225],v2 offset:512
v_mfma_f32_16x16x32_bf16 a[124:127],a[248:251],v[228:231],a[124:127]
ds_read_b64_tr_b16 a[226:227],v2 offset:4608
ds_read_b64_tr_b16 a[228:229],v2 offset:8704
v_mfma_f32_16x16x32_bf16 v[60:63],v[28:31],v[228:231],v[60:63]
ds_read_b64_tr_b16 a[230:231],v2 offset:12800
v_mfma_f32_16x16x32_bf16 v[60:63],v[28:31],v[244:247],v[60:63]
ds_read_b64_tr_b16 a[236:237],v3 offset:8704
v_mfma_f32_16x16x32_bf16 v[196:199],a[128:131],v[144:147],v[40:43]
buffer_load_dwordx4 v9,s[72:75],s83 offen offset:2048 lds
v_mfma_f32_16x16x32_bf16 v[196:199],a[132:135],v[176:179],v[196:199]
ds_read_b64_tr_b16 a[238:239],v3 offset:12800
ds_read_b64_tr_b16 a[232:233],v3 offset:512
v_mfma_f32_16x16x32_bf16 v[196:199],a[136:139],v[208:211],v[196:199]
ds_read_b64_tr_b16 a[234:235],v3 offset:4608
v_mfma_f32_16x16x32_bf16 v[196:199],a[140:143],v[240:243],v[196:199]
ds_read_b64_tr_b16 a[240:241],v2 offset:768
v_mfma_f32_16x16x32_bf16 v[204:207],a[156:159],v[240:243],v[40:43]
ds_read_b64_tr_b16 a[242:243],v2 offset:4864
v_mfma_f32_16x16x32_bf16 v[204:207],a[152:155],v[208:211],v[204:207]
ds_read_b64_tr_b16 a[244:245],v2 offset:8960
ds_read_b64_tr_b16 a[246:247],v2 offset:13056
v_mfma_f32_16x16x32_bf16 v[204:207],a[148:151],v[176:179],v[204:207]
buffer_store_dwordx4 v[76:79],v6,s[36:39],s84 offen offset:3072 sc1
v_mfma_f32_16x16x32_bf16 v[204:207],a[144:147],v[144:147],v[204:207]
s_add_u32 s84,s55,65536
ds_read_b64_tr_b16 a[252:253],v3 offset:8960
ds_read_b64_tr_b16 a[254:255],v3 offset:13056
v_mfma_f32_16x16x32_bf16 v[212:215],a[160:163],v[144:147],v[40:43]
s_max_i32 s84,s84,s18
ds_read_b64_tr_b16 a[248:249],v3 offset:768
v_add_u32 v2,s48,v2
v_mfma_f32_16x16x32_bf16 v[212:215],a[164:167],v[176:179],v[212:215]
ds_read_b64_tr_b16 a[250:251],v3 offset:4864
v_add_u32 v3,s48,v3
v_mfma_f32_16x16x32_bf16 v[212:215],a[168:171],v[208:211],v[212:215]
s_sub_u32 s48,32768,s62
v_exp_f32 v165,v165
v_mfma_f32_16x16x32_bf16 v[212:215],a[172:175],v[240:243],v[212:215]
s_add_u32 s50,s50,s48
v_exp_f32 v166,v166
v_mfma_f32_16x16x32_bf16 v[220:223],a[188:191],v[240:243],v[40:43]
v_exp_f32 v167,v167
v_mfma_f32_16x16x32_bf16 v[220:223],a[184:187],v[208:211],v[220:223]
buffer_load_dwordx4 v10,s[72:75],s83 offen offset:3072 lds
v_mfma_f32_16x16x32_bf16 v[220:223],a[180:183],v[176:179],v[220:223]
v_cvt_pk_bf16_f32 v164,v164,v165
v_cvt_pk_bf16_f32 v165,v166,v167
v_mfma_f32_16x16x32_bf16 v[220:223],a[176:179],v[144:147],v[220:223]
s_waitcnt lgkmcnt(2)
v_exp_f32 v172,v172
v_mfma_f32_16x16x32_bf16 a[0:3],a[192:195],v[132:135],a[0:3]
v_exp_f32 v173,v173
v_mfma_f32_16x16x32_bf16 a[0:3],a[196:199],v[148:151],a[0:3]
v_exp_f32 v174,v174
v_mfma_f32_16x16x32_bf16 a[4:7],a[204:207],v[148:151],a[4:7]
v_exp_f32 v175,v175
v_mfma_f32_16x16x32_bf16 a[4:7],a[200:203],v[132:135],a[4:7]
v_exp_f32 v180,v180
v_mfma_f32_16x16x32_bf16 a[8:11],a[208:211],v[132:135],a[8:11]
v_exp_f32 v181,v181
v_mfma_f32_16x16x32_bf16 a[8:11],a[212:215],v[148:151],a[8:11]
v_exp_f32 v182,v182
v_mfma_f32_16x16x32_bf16 a[12:15],a[220:223],v[148:151],a[12:15]
s_add_u32 s80,s71,0
s_and_b32 s80,s80,3
s_lshl_b32 s80,s80,1
v_mfma_f32_16x16x32_bf16 a[12:15],a[216:219],v[132:135],a[12:15]
s_add_u32 m0,s82,9216
s_or_b32 s80,s80,1
s_mul_i32 s80,s80,163840
v_mfma_f32_16x16x32_bf16 a[16:19],a[224:227],v[132:135],a[16:19]
s_add_u32 s80,s80,s81
v_exp_f32 v183,v183
v_mfma_f32_16x16x32_bf16 a[16:19],a[228:231],v[148:151],a[16:19]
v_cvt_pk_bf16_f32 v166,v172,v173
v_cvt_pk_bf16_f32 v167,v174,v175
v_mfma_f32_16x16x32_bf16 a[20:23],a[236:239],v[148:151],a[20:23]
buffer_load_dwordx4 v11,s[72:75],s83 offen lds
v_mfma_f32_16x16x32_bf16 a[20:23],a[232:235],v[132:135],a[20:23]
s_add_u32 s83,s80,20480
v_cvt_pk_bf16_f32 v180,v180,v181
v_cvt_pk_bf16_f32 v181,v182,v183
v_mfma_f32_16x16x32_bf16 a[24:27],a[240:243],v[132:135],a[24:27]
v_exp_f32 v188,v188
v_mfma_f32_16x16x32_bf16 a[24:27],a[244:247],v[148:151],a[24:27]
v_exp_f32 v189,v189
v_mfma_f32_16x16x32_bf16 a[28:31],a[252:255],v[148:151],a[28:31]
s_waitcnt lgkmcnt(0)
buffer_store_dwordx4 v[80:83],v6,s[36:39],s85 offen sc1
v_mfma_f32_16x16x32_bf16 a[28:31],a[248:251],v[132:135],a[28:31]
v_exp_f32 v190,v190
v_mfma_f32_16x16x32_bf16 v[48:51],v[28:31],v[132:135],v[48:51]
v_exp_f32 v191,v191
v_mfma_f32_16x16x32_bf16 v[48:51],v[28:31],v[148:151],v[48:51]
v_cvt_pk_bf16_f32 v182,v188,v189
v_cvt_pk_bf16_f32 v183,v190,v191
v_mfma_f32_16x16x32_bf16 v[228:231],a[128:131],v[152:155],v[44:47]
s_waitcnt vmcnt(25)
v_exp_f32 v196,v196
v_mfma_f32_16x16x32_bf16 v[228:231],a[132:135],v[184:187],v[228:231]
s_barrier
ds_read_b128 a[128:131],v1
v_mfma_f32_16x16x32_bf16 v[228:231],a[136:139],v[216:219],v[228:231]
v_exp_f32 v197,v197
v_mfma_f32_16x16x32_bf16 v[228:231],a[140:143],v[248:251],v[228:231]
ds_read_b128 a[132:135],v1 offset:256
v_cvt_pk_bf16_f32 v196,v196,v197
v_mfma_f32_16x16x32_bf16 v[236:239],a[156:159],v[248:251],v[44:47]
v_exp_f32 v198,v198
v_mfma_f32_16x16x32_bf16 v[236:239],a[152:155],v[216:219],v[236:239]
s_cmp_eq_u32 s50,131072
s_cselect_b32 s62,163840,0
ds_read_b128 a[136:139],v1 offset:512
v_mfma_f32_16x16x32_bf16 v[236:239],a[148:151],v[184:187],v[236:239]
v_exp_f32 v199,v199
v_mfma_f32_16x16x32_bf16 v[236:239],a[144:147],v[152:155],v[236:239]
ds_read_b128 a[140:143],v1 offset:768
v_cvt_pk_bf16_f32 v197,v198,v199
v_mfma_f32_16x16x32_bf16 v[244:247],a[160:163],v[152:155],v[44:47]
s_add_u32 m0,s82,40960
v_exp_f32 v204,v204
v_mfma_f32_16x16x32_bf16 v[244:247],a[164:167],v[184:187],v[244:247]
ds_read_b128 a[156:159],v1 offset:4864
v_mfma_f32_16x16x32_bf16 v[244:247],a[168:171],v[216:219],v[244:247]
v_exp_f32 v205,v205
v_mfma_f32_16x16x32_bf16 v[244:247],a[172:175],v[248:251],v[244:247]
)A"
R"A(buffer_load_dwordx4 v7,s[72:75],s80 offen lds
v_mfma_f32_16x16x32_bf16 v[252:255],a[188:191],v[248:251],v[44:47]
ds_read_b128 a[152:155],v1 offset:4608
v_cvt_pk_bf16_f32 v198,v204,v205
v_mfma_f32_16x16x32_bf16 v[252:255],a[184:187],v[216:219],v[252:255]
v_exp_f32 v206,v206
v_mfma_f32_16x16x32_bf16 v[252:255],a[180:183],v[184:187],v[252:255]
ds_read_b128 a[148:151],v1 offset:4352
v_mfma_f32_16x16x32_bf16 v[252:255],a[176:179],v[152:155],v[252:255]
buffer_store_dwordx4 v[84:87],v6,s[36:39],s85 offen offset:1024 sc1
v_mfma_f32_16x16x32_bf16 a[32:35],a[192:195],v[164:167],a[32:35]
ds_read_b128 a[144:147],v1 offset:4096
v_mfma_f32_16x16x32_bf16 a[32:35],a[196:199],v[180:183],a[32:35]
v_exp_f32 v207,v207
v_mfma_f32_16x16x32_bf16 a[36:39],a[204:207],v[180:183],a[36:39]
ds_read_b128 a[160:163],v1 offset:8192
v_cvt_pk_bf16_f32 v199,v206,v207
v_mfma_f32_16x16x32_bf16 a[36:39],a[200:203],v[164:167],a[36:39]
v_exp_f32 v212,v212
v_mfma_f32_16x16x32_bf16 a[40:43],a[208:211],v[164:167],a[40:43]
ds_read_b128 a[164:167],v1 offset:8448
v_mfma_f32_16x16x32_bf16 a[40:43],a[212:215],v[180:183],a[40:43]
v_exp_f32 v213,v213
v_mfma_f32_16x16x32_bf16 a[44:47],a[220:223],v[180:183],a[44:47]
ds_read_b128 a[168:171],v1 offset:8704
v_cvt_pk_bf16_f32 v212,v212,v213
v_mfma_f32_16x16x32_bf16 a[44:47],a[216:219],v[164:167],a[44:47]
v_exp_f32 v214,v214
v_mfma_f32_16x16x32_bf16 a[48:51],a[224:227],v[164:167],a[48:51]
buffer_load_dwordx4 v8,s[72:75],s80 offen offset:1024 lds
v_mfma_f32_16x16x32_bf16 a[48:51],a[228:231],v[180:183],a[48:51]
ds_read_b128 a[172:175],v1 offset:8960
v_mfma_f32_16x16x32_bf16 a[52:55],a[236:239],v[180:183],a[52:55]
v_exp_f32 v215,v215
v_mfma_f32_16x16x32_bf16 a[52:55],a[232:235],v[164:167],a[52:55]
ds_read_b128 a[188:191],v1 offset:13056
v_cvt_pk_bf16_f32 v213,v214,v215
v_mfma_f32_16x16x32_bf16 a[56:59],a[240:243],v[164:167],a[56:59]
v_exp_f32 v220,v220
v_mfma_f32_16x16x32_bf16 a[56:59],a[244:247],v[180:183],a[56:59]
ds_read_b128 a[184:187],v1 offset:12800
v_mfma_f32_16x16x32_bf16 a[60:63],a[252:255],v[180:183],a[60:63]
v_exp_f32 v221,v221
v_mfma_f32_16x16x32_bf16 a[60:63],a[248:251],v[164:167],a[60:63]
ds_read_b128 a[180:183],v1 offset:12544
v_cvt_pk_bf16_f32 v214,v220,v221
v_mfma_f32_16x16x32_bf16 v[52:55],v[28:31],v[164:167],v[52:55]
v_exp_f32 v222,v222
v_mfma_f32_16x16x32_bf16 v[52:55],v[28:31],v[180:183],v[52:55]
s_waitcnt lgkmcnt(3)
ds_read_b128 a[176:179],v1 offset:12288
v_add_u32 v1,s48,v1
v_mfma_f32_16x16x32_bf16 v[132:135],a[128:131],v[128:131],v[32:35]
v_exp_f32 v223,v223
v_mfma_f32_16x16x32_bf16 v[132:135],a[132:135],v[160:163],v[132:135]
v_cvt_pk_bf16_f32 v215,v222,v223
v_mfma_f32_16x16x32_bf16 v[132:135],a[136:139],v[192:195],v[132:135]
v_exp_f32 v228,v228
v_mfma_f32_16x16x32_bf16 v[132:135],a[140:143],v[224:227],v[132:135]
buffer_store_dwordx4 v[88:91],v6,s[36:39],s85 offen offset:2048 sc1
v_mfma_f32_16x16x32_bf16 v[140:143],a[156:159],v[224:227],v[32:35]
v_exp_f32 v229,v229
v_mfma_f32_16x16x32_bf16 v[140:143],a[152:155],v[192:195],v[140:143]
v_exp_f32 v230,v230
v_mfma_f32_16x16x32_bf16 v[140:143],a[148:151],v[160:163],v[140:143]
v_exp_f32 v231,v231
v_mfma_f32_16x16x32_bf16 v[140:143],a[144:147],v[128:131],v[140:143]
buffer_load_dwordx4 v9,s[72:75],s80 offen offset:2048 lds
v_mfma_f32_16x16x32_bf16 v[148:151],a[160:163],v[128:131],v[32:35]
v_cvt_pk_bf16_f32 v228,v228,v229
v_cvt_pk_bf16_f32 v229,v230,v231
v_mfma_f32_16x16x32_bf16 v[148:151],a[164:167],v[160:163],v[148:151]
v_exp_f32 v236,v236
v_mfma_f32_16x16x32_bf16 v[148:151],a[168:171],v[192:195],v[148:151]
v_exp_f32 v237,v237
v_mfma_f32_16x16x32_bf16 v[148:151],a[172:175],v[224:227],v[148:151]
s_waitcnt lgkmcnt(0)
v_exp_f32 v238,v238
v_mfma_f32_16x16x32_bf16 v[156:159],a[188:191],v[224:227],v[32:35]
v_exp_f32 v239,v239
v_mfma_f32_16x16x32_bf16 v[156:159],a[184:187],v[192:195],v[156:159]
v_exp_f32 v244,v244
v_mfma_f32_16x16x32_bf16 v[156:159],a[180:183],v[160:163],v[156:159]
v_exp_f32 v245,v245
v_mfma_f32_16x16x32_bf16 v[156:159],a[176:179],v[128:131],v[156:159]
v_exp_f32 v246,v246
v_mfma_f32_16x16x32_bf16 a[64:67],a[192:195],v[196:199],a[64:67]
buffer_load_dwordx4 v10,s[72:75],s80 offen offset:3072 lds
v_mfma_f32_16x16x32_bf16 a[64:67],a[196:199],v[212:215],a[64:67]
v_exp_f32 v247,v247
v_mfma_f32_16x16x32_bf16 a[68:71],a[204:207],v[212:215],a[68:71]
v_cvt_pk_bf16_f32 v230,v236,v237
v_cvt_pk_bf16_f32 v231,v238,v239
v_mfma_f32_16x16x32_bf16 a[68:71],a[200:203],v[196:199],a[68:71]
v_cvt_pk_bf16_f32 v244,v244,v245
v_cvt_pk_bf16_f32 v245,v246,v247
v_mfma_f32_16x16x32_bf16 a[72:75],a[208:211],v[196:199],a[72:75]
v_exp_f32 v252,v252
v_mfma_f32_16x16x32_bf16 a[72:75],a[212:215],v[212:215],a[72:75]
v_exp_f32 v253,v253
v_mfma_f32_16x16x32_bf16 a[76:79],a[220:223],v[212:215],a[76:79]
v_exp_f32 v254,v254
v_mfma_f32_16x16x32_bf16 a[76:79],a[216:219],v[196:199],a[76:79]
v_exp_f32 v255,v255
v_mfma_f32_16x16x32_bf16 a[80:83],a[224:227],v[196:199],a[80:83]
v_cvt_pk_bf16_f32 v246,v252,v253
v_cvt_pk_bf16_f32 v247,v254,v255
v_mfma_f32_16x16x32_bf16 a[80:83],a[228:231],v[212:215],a[80:83]
buffer_store_dwordx4 v[92:95],v6,s[36:39],s85 offen offset:3072 sc1
v_mfma_f32_16x16x32_bf16 a[84:87],a[236:239],v[212:215],a[84:87]
s_add_u32 m0,s82,45056
v_exp_f32 v132,v132
v_mfma_f32_16x16x32_bf16 a[84:87],a[232:235],v[196:199],a[84:87]
s_add_u32 s85,s55,69632
v_exp_f32 v133,v133
v_mfma_f32_16x16x32_bf16 a[88:91],a[240:243],v[196:199],a[88:91]
s_max_i32 s85,s85,s19
v_exp_f32 v134,v134
v_mfma_f32_16x16x32_bf16 a[88:91],a[244:247],v[212:215],a[88:91]
buffer_load_dwordx4 v11,s[72:75],s80 offen lds
v_mfma_f32_16x16x32_bf16 a[92:95],a[252:255],v[212:215],a[92:95]
v_exp_f32 v135,v135
v_mfma_f32_16x16x32_bf16 a[92:95],a[248:251],v[196:199],a[92:95]
v_cvt_pk_bf16_f32 v132,v132,v133
v_cvt_pk_bf16_f32 v133,v134,v135
v_mfma_f32_16x16x32_bf16 v[56:59],v[28:31],v[196:199],v[56:59]
v_exp_f32 v140,v140
v_mfma_f32_16x16x32_bf16 v[56:59],v[28:31],v[212:215],v[56:59]
v_exp_f32 v141,v141
v_mfma_f32_16x16x32_bf16 v[164:167],a[128:131],v[136:139],v[36:39]
v_exp_f32 v142,v142
v_mfma_f32_16x16x32_bf16 v[164:167],a[132:135],v[168:171],v[164:167]
v_exp_f32 v143,v143
v_mfma_f32_16x16x32_bf16 v[164:167],a[136:139],v[200:203],v[164:167]
v_exp_f32 v148,v148
v_mfma_f32_16x16x32_bf16 v[164:167],a[140:143],v[232:235],v[164:167]
v_exp_f32 v149,v149
v_mfma_f32_16x16x32_bf16 v[172:175],a[156:159],v[232:235],v[36:39]
v_exp_f32 v150,v150
v_mfma_f32_16x16x32_bf16 v[172:175],a[152:155],v[200:203],v[172:175]
s_add_u32 m0,s82,46080
v_exp_f32 v151,v151
v_mfma_f32_16x16x32_bf16 v[172:175],a[148:151],v[168:171],v[172:175]
v_cvt_pk_bf16_f32 v134,v140,v141
v_cvt_pk_bf16_f32 v135,v142,v143
v_mfma_f32_16x16x32_bf16 v[172:175],a[144:147],v[136:139],v[172:175]
v_cvt_pk_bf16_f32 v148,v148,v149
v_cvt_pk_bf16_f32 v149,v150,v151
v_mfma_f32_16x16x32_bf16 v[180:183],a[160:163],v[136:139],v[36:39]
buffer_load_dwordx4 v7,s[72:75],s83 offen lds
v_mfma_f32_16x16x32_bf16 v[180:183],a[164:167],v[168:171],v[180:183]
v_exp_f32 v156,v156
v_mfma_f32_16x16x32_bf16 v[180:183],a[168:171],v[200:203],v[180:183]
v_exp_f32 v157,v157
v_mfma_f32_16x16x32_bf16 v[180:183],a[172:175],v[232:235],v[180:183]
v_exp_f32 v158,v158
v_mfma_f32_16x16x32_bf16 v[188:191],a[188:191],v[232:235],v[36:39]
v_exp_f32 v159,v159
v_mfma_f32_16x16x32_bf16 v[188:191],a[184:187],v[200:203],v[188:191]
buffer_store_dwordx4 v[96:99],v6,s[36:39],s86 offen sc1
v_mfma_f32_16x16x32_bf16 v[188:191],a[180:183],v[168:171],v[188:191]
v_cvt_pk_bf16_f32 v150,v156,v157
v_cvt_pk_bf16_f32 v151,v158,v159
v_mfma_f32_16x16x32_bf16 v[188:191],a[176:179],v[136:139],v[188:191]
v_exp_f32 v164,v164
v_mfma_f32_16x16x32_bf16 a[96:99],a[192:195],v[228:231],a[96:99]
ds_read_b64_tr_b16 a[192:193],v2
ds_read_b64_tr_b16 a[194:195],v2 offset:4096
v_mfma_f32_16x16x32_bf16 a[96:99],a[196:199],v[244:247],a[96:99]
ds_read_b64_tr_b16 a[196:197],v2 offset:8192
v_mfma_f32_16x16x32_bf16 a[100:103],a[204:207],v[244:247],a[100:103]
ds_read_b64_tr_b16 a[198:199],v2 offset:12288
v_mfma_f32_16x16x32_bf16 a[100:103],a[200:203],v[228:231],a[100:103]
ds_read_b64_tr_b16 a[204:205],v3 offset:8192
v_mfma_f32_16x16x32_bf16 a[104:107],a[208:211],v[228:231],a[104:107]
ds_read_b64_tr_b16 a[206:207],v3 offset:12288
ds_read_b64_tr_b16 a[200:201],v3
v_mfma_f32_16x16x32_bf16 a[104:107],a[212:215],v[244:247],a[104:107]
buffer_load_dwordx4 v8,s[72:75],s83 offen offset:1024 lds
v_mfma_f32_16x16x32_bf16 a[108:111],a[220:223],v[244:247],a[108:111]
ds_read_b64_tr_b16 a[202:203],v3 offset:4096
ds_read_b64_tr_b16 a[208:209],v2 offset:256
v_mfma_f32_16x16x32_bf16 a[108:111],a[216:219],v[228:231],a[108:111]
ds_read_b64_tr_b16 a[210:211],v2 offset:4352
v_mfma_f32_16x16x32_bf16 a[112:115],a[224:227],v[228:231],a[112:115]
ds_read_b64_tr_b16 a[212:213],v2 offset:8448
v_mfma_f32_16x16x32_bf16 a[112:115],a[228:231],v[244:247],a[112:115]
ds_read_b64_tr_b16 a[214:215],v2 offset:12544
v_mfma_f32_16x16x32_bf16 a[116:119],a[236:239],v[244:247],a[116:119]
ds_read_b64_tr_b16 a[220:221],v3 offset:8448
ds_read_b64_tr_b16 a[222:223],v3 offset:12544
v_mfma_f32_16x16x32_bf16 a[116:119],a[232:235],v[228:231],a[116:119]
ds_read_b64_tr_b16 a[216:217],v3 offset:256
v_mfma_f32_16x16x32_bf16 a[120:123],a[240:243],v[228:231],a[120:123]
ds_read_b64_tr_b16 a[218:219],v3 offset:4352
v_mfma_f32_16x16x32_bf16 a[120:123],a[244:247],v[244:247],a[120:123]
ds_read_b64_tr_b16 a[224:225],v2 offset:512
v_mfma_f32_16x16x32_bf16 a[124:127],a[252:255],v[244:247],a[124:127]
ds_read_b64_tr_b16 a[226:227],v2 offset:4608
ds_read_b64_tr_b16 a[228:229],v2 offset:8704
v_mfma_f32_16x16x32_bf16 a[124:127],a[248:251],v[228:231],a[124:127]
ds_read_b64_tr_b16 a[230:231],v2 offset:12800
v_mfma_f32_16x16x32_bf16 v[60:63],v[28:31],v[228:231],v[60:63]
ds_read_b64_tr_b16 a[236:237],v3 offset:8704
v_mfma_f32_16x16x32_bf16 v[60:63],v[28:31],v[244:247],v[60:63]
ds_read_b64_tr_b16 a[238:239],v3 offset:12800
v_mfma_f32_16x16x32_bf16 v[196:199],a[128:131],v[144:147],v[40:43]
buffer_load_dwordx4 v9,s[72:75],s83 offen offset:2048 lds
v_mfma_f32_16x16x32_bf16 v[196:199],a[132:135],v[176:179],v[196:199]
ds_read_b64_tr_b16 a[232:233],v3 offset:512
ds_read_b64_tr_b16 a[234:235],v3 offset:4608
v_mfma_f32_16x16x32_bf16 v[196:199],a[136:139],v[208:211],v[196:199]
ds_read_b64_tr_b16 a[240:241],v2 offset:768
v_mfma_f32_16x16x32_bf16 v[196:199],a[140:143],v[240:243],v[196:199]
ds_read_b64_tr_b16 a[242:243],v2 offset:4864
v_mfma_f32_16x16x32_bf16 v[204:207],a[156:159],v[240:243],v[40:43]
buffer_store_dwordx4 v[100:103],v6,s[36:39],s86 offen offset:1024 sc1
v_mfma_f32_16x16x32_bf16 v[204:207],a[152:155],v[208:211],v[204:207]
ds_read_b64_tr_b16 a[244:245],v2 offset:8960
ds_read_b64_tr_b16 a[246:247],v2 offset:13056
v_mfma_f32_16x16x32_bf16 v[204:207],a[148:151],v[176:179],v[204:207]
ds_read_b64_tr_b16 a[252:253],v3 offset:8960
v_add_u32 v2,s48,v2
v_mfma_f32_16x16x32_bf16 v[204:207],a[144:147],v[144:147],v[204:207]
ds_read_b64_tr_b16 a[254:255],v3 offset:13056
v_mfma_f32_16x16x32_bf16 v[212:215],a[160:163],v[144:147],v[40:43]
ds_read_b64_tr_b16 a[248:249],v3 offset:768
v_mfma_f32_16x16x32_bf16 v[212:215],a[164:167],v[176:179],v[212:215]
ds_read_b64_tr_b16 a[250:251],v3 offset:4864
v_add_u32 v3,s48,v3
v_mfma_f32_16x16x32_bf16 v[212:215],a[168:171],v[208:211],v[212:215]
s_sub_u32 s48,32768,s62
v_exp_f32 v165,v165
v_mfma_f32_16x16x32_bf16 v[212:215],a[172:175],v[240:243],v[212:215]
s_add_u32 s50,s50,s48
v_exp_f32 v166,v166
v_mfma_f32_16x16x32_bf16 v[220:223],a[188:191],v[240:243],v[40:43]
v_exp_f32 v167,v167
v_mfma_f32_16x16x32_bf16 v[220:223],a[184:187],v[208:211],v[220:223]
buffer_load_dwordx4 v10,s[72:75],s83 offen offset:3072 lds
v_mfma_f32_16x16x32_bf16 v[220:223],a[180:183],v[176:179],v[220:223]
v_cvt_pk_bf16_f32 v164,v164,v165
v_cvt_pk_bf16_f32 v165,v166,v167
v_mfma_f32_16x16x32_bf16 v[220:223],a[176:179],v[144:147],v[220:223]
s_waitcnt lgkmcnt(2)
v_exp_f32 v172,v172
v_mfma_f32_16x16x32_bf16 a[0:3],a[192:195],v[132:135],a[0:3]
v_exp_f32 v173,v173
v_mfma_f32_16x16x32_bf16 a[0:3],a[196:199],v[148:151],a[0:3]
v_exp_f32 v174,v174
v_mfma_f32_16x16x32_bf16 a[4:7],a[204:207],v[148:151],a[4:7]
)A"
R"A(v_exp_f32 v175,v175
v_mfma_f32_16x16x32_bf16 a[4:7],a[200:203],v[132:135],a[4:7]
v_exp_f32 v180,v180
v_mfma_f32_16x16x32_bf16 a[8:11],a[208:211],v[132:135],a[8:11]
v_exp_f32 v181,v181
v_mfma_f32_16x16x32_bf16 a[8:11],a[212:215],v[148:151],a[8:11]
v_exp_f32 v182,v182
v_mfma_f32_16x16x32_bf16 a[12:15],a[220:223],v[148:151],a[12:15]
s_cmp_ge_u32 0,s71
s_cselect_b32 s80,2,0
s_mul_i32 s80,s80,163840
v_mfma_f32_16x16x32_bf16 a[12:15],a[216:219],v[132:135],a[12:15]
s_add_u32 m0,s82,50176
buffer_store_dwordx4 v[104:107],v6,s[36:39],s86 offen offset:2048 sc1
v_mfma_f32_16x16x32_bf16 a[16:19],a[224:227],v[132:135],a[16:19]
s_add_u32 s80,s80,s81
v_exp_f32 v183,v183
v_mfma_f32_16x16x32_bf16 a[16:19],a[228:231],v[148:151],a[16:19]
v_cvt_pk_bf16_f32 v166,v172,v173
v_cvt_pk_bf16_f32 v167,v174,v175
v_mfma_f32_16x16x32_bf16 a[20:23],a[236:239],v[148:151],a[20:23]
v_cvt_pk_bf16_f32 v180,v180,v181
v_cvt_pk_bf16_f32 v181,v182,v183
v_mfma_f32_16x16x32_bf16 a[20:23],a[232:235],v[132:135],a[20:23]
buffer_load_dwordx4 v11,s[72:75],s83 offen lds
v_mfma_f32_16x16x32_bf16 a[24:27],a[240:243],v[132:135],a[24:27]
s_add_u32 s83,s80,20480
v_exp_f32 v188,v188
v_mfma_f32_16x16x32_bf16 a[24:27],a[244:247],v[148:151],a[24:27]
v_exp_f32 v189,v189
v_mfma_f32_16x16x32_bf16 a[28:31],a[252:255],v[148:151],a[28:31]
s_waitcnt lgkmcnt(0)
v_exp_f32 v190,v190
v_mfma_f32_16x16x32_bf16 a[28:31],a[248:251],v[132:135],a[28:31]
v_exp_f32 v191,v191
v_mfma_f32_16x16x32_bf16 v[48:51],v[28:31],v[132:135],v[48:51]
v_cvt_pk_bf16_f32 v182,v188,v189
v_cvt_pk_bf16_f32 v183,v190,v191
v_mfma_f32_16x16x32_bf16 v[48:51],v[28:31],v[148:151],v[48:51]
v_exp_f32 v196,v196
v_mfma_f32_16x16x32_bf16 v[228:231],a[128:131],v[152:155],v[44:47]
s_waitcnt vmcnt(32)
v_exp_f32 v197,v197
v_mfma_f32_16x16x32_bf16 v[228:231],a[132:135],v[184:187],v[228:231]
s_barrier
ds_read_b128 a[128:131],v1
v_cvt_pk_bf16_f32 v196,v196,v197
v_mfma_f32_16x16x32_bf16 v[228:231],a[136:139],v[216:219],v[228:231]
v_exp_f32 v198,v198
v_mfma_f32_16x16x32_bf16 v[228:231],a[140:143],v[248:251],v[228:231]
ds_read_b128 a[132:135],v1 offset:256
v_mfma_f32_16x16x32_bf16 v[236:239],a[156:159],v[248:251],v[44:47]
v_exp_f32 v199,v199
v_mfma_f32_16x16x32_bf16 v[236:239],a[152:155],v[216:219],v[236:239]
ds_read_b128 a[136:139],v1 offset:512
v_cvt_pk_bf16_f32 v197,v198,v199
v_mfma_f32_16x16x32_bf16 v[236:239],a[148:151],v[184:187],v[236:239]
v_exp_f32 v204,v204
v_mfma_f32_16x16x32_bf16 v[236:239],a[144:147],v[152:155],v[236:239]
ds_read_b128 a[140:143],v1 offset:768
v_mfma_f32_16x16x32_bf16 v[244:247],a[160:163],v[152:155],v[44:47]
s_add_u32 m0,s82,81920
v_exp_f32 v205,v205
v_mfma_f32_16x16x32_bf16 v[244:247],a[164:167],v[184:187],v[244:247]
ds_read_b128 a[156:159],v1 offset:4864
v_cvt_pk_bf16_f32 v198,v204,v205
v_mfma_f32_16x16x32_bf16 v[244:247],a[168:171],v[216:219],v[244:247]
buffer_store_dwordx4 v[108:111],v6,s[36:39],s86 offen offset:3072 sc1
v_mfma_f32_16x16x32_bf16 v[244:247],a[172:175],v[248:251],v[244:247]
s_add_u32 s86,s55,73728
s_max_i32 s86,s86,s100
ds_read_b128 a[152:155],v1 offset:4608
v_mfma_f32_16x16x32_bf16 v[252:255],a[188:191],v[248:251],v[44:47]
v_exp_f32 v206,v206
v_mfma_f32_16x16x32_bf16 v[252:255],a[184:187],v[216:219],v[252:255]
ds_read_b128 a[148:151],v1 offset:4352
v_mfma_f32_16x16x32_bf16 v[252:255],a[180:183],v[184:187],v[252:255]
buffer_load_dwordx4 v7,s[72:75],s80 offen lds
v_mfma_f32_16x16x32_bf16 v[252:255],a[176:179],v[152:155],v[252:255]
ds_read_b128 a[144:147],v1 offset:4096
v_mfma_f32_16x16x32_bf16 a[32:35],a[192:195],v[164:167],a[32:35]
v_exp_f32 v207,v207
v_mfma_f32_16x16x32_bf16 a[32:35],a[196:199],v[180:183],a[32:35]
ds_read_b128 a[160:163],v1 offset:8192
v_cvt_pk_bf16_f32 v199,v206,v207
v_mfma_f32_16x16x32_bf16 a[36:39],a[204:207],v[180:183],a[36:39]
v_exp_f32 v212,v212
v_mfma_f32_16x16x32_bf16 a[36:39],a[200:203],v[164:167],a[36:39]
ds_read_b128 a[164:167],v1 offset:8448
v_mfma_f32_16x16x32_bf16 a[40:43],a[208:211],v[164:167],a[40:43]
v_exp_f32 v213,v213
v_mfma_f32_16x16x32_bf16 a[40:43],a[212:215],v[180:183],a[40:43]
ds_read_b128 a[168:171],v1 offset:8704
v_cvt_pk_bf16_f32 v212,v212,v213
v_mfma_f32_16x16x32_bf16 a[44:47],a[220:223],v[180:183],a[44:47]
v_exp_f32 v214,v214
v_mfma_f32_16x16x32_bf16 a[44:47],a[216:219],v[164:167],a[44:47]
ds_read_b128 a[172:175],v1 offset:8960
v_mfma_f32_16x16x32_bf16 a[48:51],a[224:227],v[164:167],a[48:51]
buffer_load_dwordx4 v8,s[72:75],s80 offen offset:1024 lds
v_mfma_f32_16x16x32_bf16 a[48:51],a[228:231],v[180:183],a[48:51]
ds_read_b128 a[188:191],v1 offset:13056
v_mfma_f32_16x16x32_bf16 a[52:55],a[236:239],v[180:183],a[52:55]
v_exp_f32 v215,v215
v_mfma_f32_16x16x32_bf16 a[52:55],a[232:235],v[164:167],a[52:55]
ds_read_b128 a[184:187],v1 offset:12800
v_cvt_pk_bf16_f32 v213,v214,v215
v_mfma_f32_16x16x32_bf16 a[56:59],a[240:243],v[164:167],a[56:59]
v_exp_f32 v220,v220
v_mfma_f32_16x16x32_bf16 a[56:59],a[244:247],v[180:183],a[56:59]
ds_read_b128 a[180:183],v1 offset:12544
v_mfma_f32_16x16x32_bf16 a[60:63],a[252:255],v[180:183],a[60:63]
v_exp_f32 v221,v221
v_mfma_f32_16x16x32_bf16 a[60:63],a[248:251],v[164:167],a[60:63]
ds_read_b128 a[176:179],v1 offset:12288
v_cvt_pk_bf16_f32 v214,v220,v221
v_mfma_f32_16x16x32_bf16 v[52:55],v[28:31],v[164:167],v[52:55]
buffer_store_dwordx4 v[112:115],v6,s[36:39],s87 offen sc1
v_mfma_f32_16x16x32_bf16 v[52:55],v[28:31],v[180:183],v[52:55]
s_waitcnt lgkmcnt(3)
v_exp_f32 v222,v222
v_mfma_f32_16x16x32_bf16 v[132:135],a[128:131],v[128:131],v[32:35]
v_exp_f32 v223,v223
v_mfma_f32_16x16x32_bf16 v[132:135],a[132:135],v[160:163],v[132:135]
v_cvt_pk_bf16_f32 v215,v222,v223
v_add_u32 v1,s48,v1
v_mfma_f32_16x16x32_bf16 v[132:135],a[136:139],v[192:195],v[132:135]
v_exp_f32 v228,v228
v_mfma_f32_16x16x32_bf16 v[132:135],a[140:143],v[224:227],v[132:135]
buffer_load_dwordx4 v9,s[72:75],s80 offen offset:2048 lds
v_mfma_f32_16x16x32_bf16 v[140:143],a[156:159],v[224:227],v[32:35]
v_exp_f32 v229,v229
v_mfma_f32_16x16x32_bf16 v[140:143],a[152:155],v[192:195],v[140:143]
v_exp_f32 v230,v230
v_mfma_f32_16x16x32_bf16 v[140:143],a[148:151],v[160:163],v[140:143]
v_exp_f32 v231,v231
v_mfma_f32_16x16x32_bf16 v[140:143],a[144:147],v[128:131],v[140:143]
v_cvt_pk_bf16_f32 v228,v228,v229
v_cvt_pk_bf16_f32 v229,v230,v231
v_mfma_f32_16x16x32_bf16 v[148:151],a[160:163],v[128:131],v[32:35]
v_exp_f32 v236,v236
v_mfma_f32_16x16x32_bf16 v[148:151],a[164:167],v[160:163],v[148:151]
v_exp_f32 v237,v237
v_mfma_f32_16x16x32_bf16 v[148:151],a[168:171],v[192:195],v[148:151]
v_exp_f32 v238,v238
v_mfma_f32_16x16x32_bf16 v[148:151],a[172:175],v[224:227],v[148:151]
v_exp_f32 v239,v239
v_mfma_f32_16x16x32_bf16 v[156:159],a[188:191],v[224:227],v[32:35]
s_waitcnt lgkmcnt(0)
v_exp_f32 v244,v244
v_mfma_f32_16x16x32_bf16 v[156:159],a[184:187],v[192:195],v[156:159]
v_exp_f32 v245,v245
v_mfma_f32_16x16x32_bf16 v[156:159],a[180:183],v[160:163],v[156:159]
v_exp_f32 v246,v246
v_mfma_f32_16x16x32_bf16 v[156:159],a[176:179],v[128:131],v[156:159]
v_exp_f32 v247,v247
v_mfma_f32_16x16x32_bf16 a[64:67],a[192:195],v[196:199],a[64:67]
buffer_load_dwordx4 v10,s[72:75],s80 offen offset:3072 lds
v_mfma_f32_16x16x32_bf16 a[64:67],a[196:199],v[212:215],a[64:67]
v_cvt_pk_bf16_f32 v230,v236,v237
v_cvt_pk_bf16_f32 v231,v238,v239
v_mfma_f32_16x16x32_bf16 a[68:71],a[204:207],v[212:215],a[68:71]
v_cvt_pk_bf16_f32 v244,v244,v245
v_cvt_pk_bf16_f32 v245,v246,v247
v_mfma_f32_16x16x32_bf16 a[68:71],a[200:203],v[196:199],a[68:71]
v_exp_f32 v252,v252
v_mfma_f32_16x16x32_bf16 a[72:75],a[208:211],v[196:199],a[72:75]
buffer_store_dwordx4 v[116:119],v6,s[36:39],s87 offen offset:1024 sc1
v_mfma_f32_16x16x32_bf16 a[72:75],a[212:215],v[212:215],a[72:75]
v_exp_f32 v253,v253
v_mfma_f32_16x16x32_bf16 a[76:79],a[220:223],v[212:215],a[76:79]
v_exp_f32 v254,v254
v_mfma_f32_16x16x32_bf16 a[76:79],a[216:219],v[196:199],a[76:79]
v_exp_f32 v255,v255
v_mfma_f32_16x16x32_bf16 a[80:83],a[224:227],v[196:199],a[80:83]
v_cvt_pk_bf16_f32 v246,v252,v253
v_cvt_pk_bf16_f32 v247,v254,v255
v_mfma_f32_16x16x32_bf16 a[80:83],a[228:231],v[212:215],a[80:83]
v_exp_f32 v132,v132
v_mfma_f32_16x16x32_bf16 a[84:87],a[236:239],v[212:215],a[84:87]
s_add_u32 m0,s82,86016
v_exp_f32 v133,v133
v_mfma_f32_16x16x32_bf16 a[84:87],a[232:235],v[196:199],a[84:87]
v_exp_f32 v134,v134
v_mfma_f32_16x16x32_bf16 a[88:91],a[240:243],v[196:199],a[88:91]
v_exp_f32 v135,v135
v_mfma_f32_16x16x32_bf16 a[88:91],a[244:247],v[212:215],a[88:91]
buffer_load_dwordx4 v11,s[72:75],s80 offen lds
v_mfma_f32_16x16x32_bf16 a[92:95],a[252:255],v[212:215],a[92:95]
v_cvt_pk_bf16_f32 v132,v132,v133
v_cvt_pk_bf16_f32 v133,v134,v135
v_mfma_f32_16x16x32_bf16 a[92:95],a[248:251],v[196:199],a[92:95]
v_exp_f32 v140,v140
v_mfma_f32_16x16x32_bf16 v[56:59],v[28:31],v[196:199],v[56:59]
v_exp_f32 v141,v141
v_mfma_f32_16x16x32_bf16 v[56:59],v[28:31],v[212:215],v[56:59]
v_exp_f32 v142,v142
v_mfma_f32_16x16x32_bf16 v[164:167],a[128:131],v[136:139],v[36:39]
v_exp_f32 v143,v143
v_mfma_f32_16x16x32_bf16 v[164:167],a[132:135],v[168:171],v[164:167]
v_exp_f32 v148,v148
v_mfma_f32_16x16x32_bf16 v[164:167],a[136:139],v[200:203],v[164:167]
v_exp_f32 v149,v149
v_mfma_f32_16x16x32_bf16 v[164:167],a[140:143],v[232:235],v[164:167]
v_exp_f32 v150,v150
v_mfma_f32_16x16x32_bf16 v[172:175],a[156:159],v[232:235],v[36:39]
v_exp_f32 v151,v151
v_mfma_f32_16x16x32_bf16 v[172:175],a[152:155],v[200:203],v[172:175]
s_add_u32 m0,s82,87040
v_cvt_pk_bf16_f32 v134,v140,v141
v_cvt_pk_bf16_f32 v135,v142,v143
v_mfma_f32_16x16x32_bf16 v[172:175],a[148:151],v[168:171],v[172:175]
v_cvt_pk_bf16_f32 v148,v148,v149
v_cvt_pk_bf16_f32 v149,v150,v151
v_mfma_f32_16x16x32_bf16 v[172:175],a[144:147],v[136:139],v[172:175]
v_exp_f32 v156,v156
v_mfma_f32_16x16x32_bf16 v[180:183],a[160:163],v[136:139],v[36:39]
buffer_store_dwordx4 v[120:123],v6,s[36:39],s87 offen offset:2048 sc1
v_mfma_f32_16x16x32_bf16 v[180:183],a[164:167],v[168:171],v[180:183]
v_exp_f32 v157,v157
v_mfma_f32_16x16x32_bf16 v[180:183],a[168:171],v[200:203],v[180:183]
v_exp_f32 v158,v158
v_mfma_f32_16x16x32_bf16 v[180:183],a[172:175],v[232:235],v[180:183]
v_exp_f32 v159,v159
v_mfma_f32_16x16x32_bf16 v[188:191],a[188:191],v[232:235],v[36:39]
buffer_load_dwordx4 v7,s[72:75],s83 offen lds
v_mfma_f32_16x16x32_bf16 v[188:191],a[184:187],v[200:203],v[188:191]
v_cvt_pk_bf16_f32 v150,v156,v157
v_cvt_pk_bf16_f32 v151,v158,v159
v_mfma_f32_16x16x32_bf16 v[188:191],a[180:183],v[168:171],v[188:191]
v_exp_f32 v164,v164
v_mfma_f32_16x16x32_bf16 v[188:191],a[176:179],v[136:139],v[188:191]
v_exp_f32 v165,v165
v_mfma_f32_16x16x32_bf16 a[96:99],a[192:195],v[228:231],a[96:99]
ds_read_b64_tr_b16 a[192:193],v2
ds_read_b64_tr_b16 a[194:195],v2 offset:4096
v_mfma_f32_16x16x32_bf16 a[96:99],a[196:199],v[244:247],a[96:99]
ds_read_b64_tr_b16 a[196:197],v2 offset:8192
v_cvt_pk_bf16_f32 v164,v164,v165
v_mfma_f32_16x16x32_bf16 a[100:103],a[204:207],v[244:247],a[100:103]
ds_read_b64_tr_b16 a[198:199],v2 offset:12288
v_add_u32 v1,v4,v5
v_mfma_f32_16x16x32_bf16 a[100:103],a[200:203],v[228:231],a[100:103]
ds_read_b64_tr_b16 a[204:205],v3 offset:8192
v_mfma_f32_16x16x32_bf16 a[104:107],a[208:211],v[228:231],a[104:107]
ds_read_b64_tr_b16 a[206:207],v3 offset:12288
ds_read_b64_tr_b16 a[200:201],v3
v_mfma_f32_16x16x32_bf16 a[104:107],a[212:215],v[244:247],a[104:107]
buffer_load_dwordx4 v8,s[72:75],s83 offen offset:1024 lds
v_mfma_f32_16x16x32_bf16 a[108:111],a[220:223],v[244:247],a[108:111]
ds_read_b64_tr_b16 a[202:203],v3 offset:4096
ds_read_b64_tr_b16 a[208:209],v2 offset:256
v_mfma_f32_16x16x32_bf16 a[108:111],a[216:219],v[228:231],a[108:111]
ds_read_b64_tr_b16 a[210:211],v2 offset:4352
v_mfma_f32_16x16x32_bf16 a[112:115],a[224:227],v[228:231],a[112:115]
ds_read_b64_tr_b16 a[212:213],v2 offset:8448
v_mfma_f32_16x16x32_bf16 a[112:115],a[228:231],v[244:247],a[112:115]
ds_read_b64_tr_b16 a[214:215],v2 offset:12544
v_mfma_f32_16x16x32_bf16 a[116:119],a[236:239],v[244:247],a[116:119]
ds_read_b64_tr_b16 a[220:221],v3 offset:8448
ds_read_b64_tr_b16 a[222:223],v3 offset:12544
v_mfma_f32_16x16x32_bf16 a[116:119],a[232:235],v[228:231],a[116:119]
ds_read_b64_tr_b16 a[216:217],v3 offset:256
)A"
R"A(v_mfma_f32_16x16x32_bf16 a[120:123],a[240:243],v[228:231],a[120:123]
ds_read_b64_tr_b16 a[218:219],v3 offset:4352
v_mfma_f32_16x16x32_bf16 a[120:123],a[244:247],v[244:247],a[120:123]
ds_read_b64_tr_b16 a[224:225],v2 offset:512
v_mfma_f32_16x16x32_bf16 a[124:127],a[252:255],v[244:247],a[124:127]
buffer_store_dwordx4 v[124:127],v6,s[36:39],s87 offen offset:3072 sc1
v_mfma_f32_16x16x32_bf16 a[124:127],a[248:251],v[228:231],a[124:127]
s_add_u32 s87,s55,77824
ds_read_b64_tr_b16 a[226:227],v2 offset:4608
ds_read_b64_tr_b16 a[228:229],v2 offset:8704
v_mfma_f32_16x16x32_bf16 v[60:63],v[28:31],v[228:231],v[60:63]
s_max_i32 s87,s87,s101
ds_read_b64_tr_b16 a[230:231],v2 offset:12800
v_mfma_f32_16x16x32_bf16 v[60:63],v[28:31],v[244:247],v[60:63]
ds_read_b64_tr_b16 a[236:237],v3 offset:8704
v_mfma_f32_16x16x32_bf16 v[196:199],a[128:131],v[144:147],v[40:43]
buffer_load_dwordx4 v9,s[72:75],s83 offen offset:2048 lds
v_mfma_f32_16x16x32_bf16 v[196:199],a[132:135],v[176:179],v[196:199]
ds_read_b64_tr_b16 a[238:239],v3 offset:12800
ds_read_b64_tr_b16 a[232:233],v3 offset:512
v_mfma_f32_16x16x32_bf16 v[196:199],a[136:139],v[208:211],v[196:199]
ds_read_b64_tr_b16 a[234:235],v3 offset:4608
v_mfma_f32_16x16x32_bf16 v[196:199],a[140:143],v[240:243],v[196:199]
ds_read_b64_tr_b16 a[240:241],v2 offset:768
v_mfma_f32_16x16x32_bf16 v[204:207],a[156:159],v[240:243],v[40:43]
ds_read_b64_tr_b16 a[242:243],v2 offset:4864
v_mfma_f32_16x16x32_bf16 v[204:207],a[152:155],v[208:211],v[204:207]
ds_read_b64_tr_b16 a[244:245],v2 offset:8960
ds_read_b64_tr_b16 a[246:247],v2 offset:13056
v_mfma_f32_16x16x32_bf16 v[204:207],a[148:151],v[176:179],v[204:207]
ds_read_b64_tr_b16 a[252:253],v3 offset:8960
v_sub_u32 v2,v4,v5
v_mfma_f32_16x16x32_bf16 v[204:207],a[144:147],v[144:147],v[204:207]
ds_read_b64_tr_b16 a[254:255],v3 offset:13056
v_mfma_f32_16x16x32_bf16 v[212:215],a[160:163],v[144:147],v[40:43]
ds_read_b64_tr_b16 a[248:249],v3 offset:768
v_mfma_f32_16x16x32_bf16 v[212:215],a[164:167],v[176:179],v[212:215]
ds_read_b64_tr_b16 a[250:251],v3 offset:4864
v_mfma_f32_16x16x32_bf16 v[212:215],a[168:171],v[208:211],v[212:215]
v_exp_f32 v166,v166
v_mfma_f32_16x16x32_bf16 v[212:215],a[172:175],v[240:243],v[212:215]
v_exp_f32 v167,v167
v_mfma_f32_16x16x32_bf16 v[220:223],a[188:191],v[240:243],v[40:43]
v_exp_f32 v172,v172
v_mfma_f32_16x16x32_bf16 v[220:223],a[184:187],v[208:211],v[220:223]
buffer_load_dwordx4 v10,s[72:75],s83 offen offset:3072 lds
v_mfma_f32_16x16x32_bf16 v[220:223],a[180:183],v[176:179],v[220:223]
v_exp_f32 v173,v173
v_mfma_f32_16x16x32_bf16 v[220:223],a[176:179],v[144:147],v[220:223]
s_waitcnt lgkmcnt(2)
v_exp_f32 v174,v174
v_mfma_f32_16x16x32_bf16 a[0:3],a[192:195],v[132:135],a[0:3]
v_exp_f32 v175,v175
v_mfma_f32_16x16x32_bf16 a[0:3],a[196:199],v[148:151],a[0:3]
v_exp_f32 v180,v180
v_mfma_f32_16x16x32_bf16 a[4:7],a[204:207],v[148:151],a[4:7]
v_exp_f32 v181,v181
v_mfma_f32_16x16x32_bf16 a[4:7],a[200:203],v[132:135],a[4:7]
v_cvt_pk_bf16_f32 v165,v166,v167
v_cvt_pk_bf16_f32 v166,v172,v173
v_mfma_f32_16x16x32_bf16 a[8:11],a[208:211],v[132:135],a[8:11]
v_exp_f32 v182,v182
v_mfma_f32_16x16x32_bf16 a[8:11],a[212:215],v[148:151],a[8:11]
v_exp_f32 v183,v183
v_mfma_f32_16x16x32_bf16 a[12:15],a[220:223],v[148:151],a[12:15]
v_cvt_pk_bf16_f32 v167,v174,v175
v_cvt_pk_bf16_f32 v180,v180,v181
v_mfma_f32_16x16x32_bf16 a[12:15],a[216:219],v[132:135],a[12:15]
s_add_u32 m0,s82,91136
v_exp_f32 v188,v188
v_mfma_f32_16x16x32_bf16 a[16:19],a[224:227],v[132:135],a[16:19]
v_exp_f32 v189,v189
v_mfma_f32_16x16x32_bf16 a[16:19],a[228:231],v[148:151],a[16:19]
v_exp_f32 v190,v190
v_mfma_f32_16x16x32_bf16 a[20:23],a[236:239],v[148:151],a[20:23]
buffer_load_dwordx4 v11,s[72:75],s83 offen lds
v_mfma_f32_16x16x32_bf16 a[20:23],a[232:235],v[132:135],a[20:23]
v_exp_f32 v191,v191
v_mfma_f32_16x16x32_bf16 a[24:27],a[240:243],v[132:135],a[24:27]
v_cvt_pk_bf16_f32 v181,v182,v183
v_cvt_pk_bf16_f32 v182,v188,v189
v_mfma_f32_16x16x32_bf16 a[24:27],a[244:247],v[148:151],a[24:27]
v_cvt_pk_bf16_f32 v183,v190,v191
v_accvgpr_read_b32 v16,a6
v_mfma_f32_16x16x32_bf16 a[28:31],a[252:255],v[148:151],a[28:31]
s_waitcnt lgkmcnt(0)
v_exp_f32 v196,v196
v_mfma_f32_16x16x32_bf16 a[28:31],a[248:251],v[132:135],a[28:31]
s_cmp_ge_u32 1,s71
s_cselect_b32 s80,4,2
s_mul_i32 s80,s80,163840
v_mfma_f32_16x16x32_bf16 v[48:51],v[28:31],v[132:135],v[48:51]
s_waitcnt vmcnt(15)
s_add_u32 s80,s80,s81
v_exp_f32 v197,v197
v_mfma_f32_16x16x32_bf16 v[48:51],v[28:31],v[148:151],v[48:51]
s_barrier
v_exp_f32 v198,v198
v_mfma_f32_16x16x32_bf16 v[228:231],a[128:131],v[152:155],v[44:47]
s_add_u32 s83,s80,20480
v_exp_f32 v199,v199
v_mfma_f32_16x16x32_bf16 v[228:231],a[132:135],v[184:187],v[228:231]
v_cvt_pk_bf16_f32 v196,v196,v197
v_cvt_pk_bf16_f32 v197,v198,v199
v_mfma_f32_16x16x32_bf16 v[228:231],a[136:139],v[216:219],v[228:231]
v_exp_f32 v204,v204
v_mfma_f32_16x16x32_bf16 v[228:231],a[140:143],v[248:251],v[228:231]
v_exp_f32 v205,v205
v_mfma_f32_16x16x32_bf16 v[236:239],a[156:159],v[248:251],v[44:47]
v_exp_f32 v206,v206
v_mfma_f32_16x16x32_bf16 v[236:239],a[152:155],v[216:219],v[236:239]
v_exp_f32 v207,v207
v_mfma_f32_16x16x32_bf16 v[236:239],a[148:151],v[184:187],v[236:239]
v_exp_f32 v212,v212
v_mfma_f32_16x16x32_bf16 v[236:239],a[144:147],v[152:155],v[236:239]
v_exp_f32 v213,v213
v_mfma_f32_16x16x32_bf16 v[244:247],a[160:163],v[152:155],v[44:47]
v_exp_f32 v214,v214
v_mfma_f32_16x16x32_bf16 v[244:247],a[164:167],v[184:187],v[244:247]
v_exp_f32 v215,v215
v_mfma_f32_16x16x32_bf16 v[244:247],a[168:171],v[216:219],v[244:247]
v_cvt_pk_bf16_f32 v198,v204,v205
v_cvt_pk_bf16_f32 v199,v206,v207
v_mfma_f32_16x16x32_bf16 v[244:247],a[172:175],v[248:251],v[244:247]
s_lshl_b32 s95,s42,2
s_add_u32 s95,s95,s40
s_lshl_b32 s95,s95,4
v_mfma_f32_16x16x32_bf16 v[252:255],a[188:191],v[248:251],v[44:47]
v_mov_b32 v18,s68
v_mov_b32 v19,s69
v_mfma_f32_16x16x32_bf16 v[252:255],a[184:187],v[216:219],v[252:255]
v_cvt_pk_bf16_f32 v212,v212,v213
v_cvt_pk_bf16_f32 v213,v214,v215
v_mfma_f32_16x16x32_bf16 v[252:255],a[180:183],v[184:187],v[252:255]
v_exp_f32 v220,v220
v_mfma_f32_16x16x32_bf16 v[252:255],a[176:179],v[152:155],v[252:255]
v_exp_f32 v221,v221
v_mfma_f32_16x16x32_bf16 a[32:35],a[192:195],v[164:167],a[32:35]
v_exp_f32 v222,v222
v_mfma_f32_16x16x32_bf16 a[32:35],a[196:199],v[180:183],a[32:35]
s_add_u32 m0,s82,122880
v_exp_f32 v223,v223
v_mfma_f32_16x16x32_bf16 a[36:39],a[204:207],v[180:183],a[36:39]
v_cvt_pk_bf16_f32 v214,v220,v221
v_cvt_pk_bf16_f32 v215,v222,v223
v_mfma_f32_16x16x32_bf16 a[36:39],a[200:203],v[164:167],a[36:39]
s_waitcnt vmcnt(3)
s_mov_b64 exec,1
buffer_store_dwordx2 v[18:19],v6,s[64:67],s95 offen sc1
s_mov_b64 exec,-1
v_mfma_f32_16x16x32_bf16 a[40:43],a[208:211],v[164:167],a[40:43]
v_max_f32 v20,0x00800000,v48
v_accvgpr_read_b32 v132,a0
v_mfma_f32_16x16x32_bf16 a[40:43],a[212:215],v[180:183],a[40:43]
v_rcp_f32 v20,v20
v_mfma_f32_16x16x32_bf16 a[44:47],a[220:223],v[180:183],a[44:47]
v_accvgpr_read_b32 v133,a1
v_mul_f32 v132,v20,v132
v_mfma_f32_16x16x32_bf16 a[44:47],a[216:219],v[164:167],a[44:47]
buffer_load_dwordx4 v7,s[72:75],s80 offen lds
v_mfma_f32_16x16x32_bf16 a[48:51],a[224:227],v[164:167],a[48:51]
v_mul_f32 v133,v20,v133
v_accvgpr_read_b32 v134,a2
v_mfma_f32_16x16x32_bf16 a[48:51],a[228:231],v[180:183],a[48:51]
v_accvgpr_read_b32 v135,a3
v_cvt_pk_bf16_f32 v132,v132,v133
v_mfma_f32_16x16x32_bf16 a[52:55],a[236:239],v[180:183],a[52:55]
v_mul_f32 v134,v20,v134
v_mul_f32 v135,v20,v135
v_mfma_f32_16x16x32_bf16 a[52:55],a[232:235],v[164:167],a[52:55]
v_cvt_pk_bf16_f32 v133,v134,v135
v_accvgpr_read_b32 v134,a4
v_mfma_f32_16x16x32_bf16 a[56:59],a[240:243],v[164:167],a[56:59]
v_exp_f32 v228,v228
v_mfma_f32_16x16x32_bf16 a[56:59],a[244:247],v[180:183],a[56:59]
v_exp_f32 v229,v229
v_mfma_f32_16x16x32_bf16 a[60:63],a[252:255],v[180:183],a[60:63]
v_accvgpr_read_b32 v135,a5
v_accvgpr_read_b32 v17,a7
v_mfma_f32_16x16x32_bf16 a[60:63],a[248:251],v[164:167],a[60:63]
v_exp_f32 v230,v230
v_mfma_f32_16x16x32_bf16 v[52:55],v[28:31],v[164:167],v[52:55]
v_exp_f32 v231,v231
v_mfma_f32_16x16x32_bf16 v[52:55],v[28:31],v[180:183],v[52:55]
v_mul_f32 v134,v20,v134
v_mul_f32 v135,v20,v135
v_mfma_f32_16x16x32_bf16 a[64:67],a[192:195],v[196:199],a[64:67]
v_cvt_pk_bf16_f32 v228,v228,v229
v_mul_f32 v16,v20,v16
v_mfma_f32_16x16x32_bf16 a[64:67],a[196:199],v[212:215],a[64:67]
v_exp_f32 v236,v236
v_mfma_f32_16x16x32_bf16 a[68:71],a[204:207],v[212:215],a[68:71]
buffer_load_dwordx4 v8,s[72:75],s80 offen offset:1024 lds
v_mfma_f32_16x16x32_bf16 a[68:71],a[200:203],v[196:199],a[68:71]
v_exp_f32 v237,v237
v_mfma_f32_16x16x32_bf16 a[72:75],a[208:211],v[196:199],a[72:75]
v_exp_f32 v238,v238
v_mfma_f32_16x16x32_bf16 a[72:75],a[212:215],v[212:215],a[72:75]
v_exp_f32 v239,v239
v_mfma_f32_16x16x32_bf16 a[76:79],a[220:223],v[212:215],a[76:79]
v_exp_f32 v244,v244
v_mfma_f32_16x16x32_bf16 a[76:79],a[216:219],v[196:199],a[76:79]
v_exp_f32 v245,v245
v_mfma_f32_16x16x32_bf16 a[80:83],a[224:227],v[196:199],a[80:83]
v_mul_f32 v17,v20,v17
v_cvt_pk_bf16_f32 v134,v134,v135
v_mfma_f32_16x16x32_bf16 a[80:83],a[228:231],v[212:215],a[80:83]
v_cvt_pk_bf16_f32 v229,v230,v231
v_cvt_pk_bf16_f32 v135,v16,v17
v_mfma_f32_16x16x32_bf16 a[84:87],a[236:239],v[212:215],a[84:87]
v_exp_f32 v246,v246
v_mfma_f32_16x16x32_bf16 a[84:87],a[232:235],v[196:199],a[84:87]
v_exp_f32 v247,v247
v_mfma_f32_16x16x32_bf16 a[88:91],a[240:243],v[196:199],a[88:91]
v_cvt_pk_bf16_f32 v230,v236,v237
v_cvt_pk_bf16_f32 v231,v238,v239
v_mfma_f32_16x16x32_bf16 a[88:91],a[244:247],v[212:215],a[88:91]
v_cvt_pk_bf16_f32 v244,v244,v245
v_cvt_pk_bf16_f32 v245,v246,v247
v_mfma_f32_16x16x32_bf16 a[92:95],a[252:255],v[212:215],a[92:95]
v_exp_f32 v252,v252
v_mfma_f32_16x16x32_bf16 a[92:95],a[248:251],v[196:199],a[92:95]
v_exp_f32 v253,v253
v_exp_f32 v254,v254
v_exp_f32 v255,v255
v_mfma_f32_16x16x32_bf16 v[56:59],v[28:31],v[196:199],v[56:59]
v_cvt_pk_bf16_f32 v246,v252,v253
v_cvt_pk_bf16_f32 v247,v254,v255
v_mfma_f32_16x16x32_bf16 v[56:59],v[28:31],v[212:215],v[56:59]
buffer_store_dwordx4 v[132:135],v6,s[36:39],s84 offen sc1
v_mfma_f32_16x16x32_bf16 a[96:99],a[192:195],v[228:231],a[96:99]
v_accvgpr_read_b32 v140,a8
v_accvgpr_read_b32 v141,a9
v_mfma_f32_16x16x32_bf16 a[96:99],a[196:199],v[244:247],a[96:99]
v_mul_f32 v140,v20,v140
v_mul_f32 v141,v20,v141
v_mfma_f32_16x16x32_bf16 a[100:103],a[204:207],v[244:247],a[100:103]
v_accvgpr_read_b32 v142,a10
v_accvgpr_read_b32 v143,a11
v_mfma_f32_16x16x32_bf16 a[100:103],a[200:203],v[228:231],a[100:103]
buffer_load_dwordx4 v9,s[72:75],s80 offen offset:2048 lds
v_mfma_f32_16x16x32_bf16 a[104:107],a[208:211],v[228:231],a[104:107]
v_cvt_pk_bf16_f32 v140,v140,v141
v_mul_f32 v142,v20,v142
v_mfma_f32_16x16x32_bf16 a[104:107],a[212:215],v[244:247],a[104:107]
v_mul_f32 v143,v20,v143
v_cvt_pk_bf16_f32 v141,v142,v143
v_mfma_f32_16x16x32_bf16 a[108:111],a[220:223],v[244:247],a[108:111]
v_accvgpr_read_b32 v142,a12
v_accvgpr_read_b32 v143,a13
v_mfma_f32_16x16x32_bf16 a[108:111],a[216:219],v[228:231],a[108:111]
v_accvgpr_read_b32 v12,a14
v_accvgpr_read_b32 v13,a15
v_mfma_f32_16x16x32_bf16 a[112:115],a[224:227],v[228:231],a[112:115]
v_mul_f32 v142,v20,v142
v_mul_f32 v143,v20,v143
v_mfma_f32_16x16x32_bf16 a[112:115],a[228:231],v[244:247],a[112:115]
ds_read_b64_tr_b16 a[128:129],v1
ds_read_b64_tr_b16 a[130:131],v1 offset:5120
v_mfma_f32_16x16x32_bf16 a[116:119],a[236:239],v[244:247],a[116:119]
ds_read_b64_tr_b16 a[132:133],v1 offset:10240
ds_read_b64_tr_b16 a[134:135],v1 offset:15360
v_mfma_f32_16x16x32_bf16 a[116:119],a[232:235],v[228:231],a[116:119]
ds_read_b64_tr_b16 a[136:137],v1 offset:20480
ds_read_b64_tr_b16 a[138:139],v1 offset:25600
v_mfma_f32_16x16x32_bf16 a[120:123],a[240:243],v[228:231],a[120:123]
ds_read_b64_tr_b16 a[140:141],v1 offset:30720
ds_read_b64_tr_b16 a[142:143],v1 offset:35840
v_mfma_f32_16x16x32_bf16 a[120:123],a[244:247],v[244:247],a[120:123]
v_mfma_f32_16x16x32_bf16 a[124:127],a[252:255],v[244:247],a[124:127]
v_mul_f32 v12,v20,v12
v_mul_f32 v13,v20,v13
v_mfma_f32_16x16x32_bf16 a[124:127],a[248:251],v[228:231],a[124:127]
v_cvt_pk_bf16_f32 v142,v142,v143
)A"
R"A(v_cvt_pk_bf16_f32 v143,v12,v13
v_mfma_f32_16x16x32_bf16 v[60:63],v[28:31],v[228:231],v[60:63]
v_accvgpr_read_b32 v148,a16
v_accvgpr_read_b32 v149,a17
v_mfma_f32_16x16x32_bf16 v[60:63],v[28:31],v[244:247],v[60:63]
s_waitcnt lgkmcnt(6)
buffer_store_dwordx4 v[140:143],v6,s[36:39],s84 offen offset:1024 sc1
v_mfma_f32_16x16x32_bf16 a[192:195],a[128:131],v[64:67],0
s_waitcnt lgkmcnt(4)
v_mul_f32 v148,v20,v148
v_mul_f32 v149,v20,v149
v_mfma_f32_16x16x32_bf16 a[192:195],a[132:135],v[68:71],a[192:195]
s_waitcnt lgkmcnt(2)
v_accvgpr_read_b32 v150,a18
v_accvgpr_read_b32 v151,a19
v_mfma_f32_16x16x32_bf16 a[192:195],a[136:139],v[72:75],a[192:195]
s_waitcnt lgkmcnt(0)
v_cvt_pk_bf16_f32 v148,v148,v149
v_mul_f32 v150,v20,v150
v_mfma_f32_16x16x32_bf16 a[192:195],a[140:143],v[76:79],a[192:195]
buffer_load_dwordx4 v10,s[72:75],s80 offen offset:3072 lds
v_mfma_f32_16x16x32_bf16 a[196:199],a[140:143],v[92:95],0
v_mul_f32 v151,v20,v151
v_cvt_pk_bf16_f32 v149,v150,v151
v_mfma_f32_16x16x32_bf16 a[196:199],a[136:139],v[88:91],a[196:199]
v_accvgpr_read_b32 v150,a20
v_accvgpr_read_b32 v151,a21
v_mfma_f32_16x16x32_bf16 a[196:199],a[132:135],v[84:87],a[196:199]
v_accvgpr_read_b32 v14,a22
v_accvgpr_read_b32 v15,a23
v_mfma_f32_16x16x32_bf16 a[196:199],a[128:131],v[80:83],a[196:199]
ds_read_b64_tr_b16 a[144:145],v2 offset:32
ds_read_b64_tr_b16 a[146:147],v2 offset:5152
v_mfma_f32_16x16x32_bf16 a[200:203],a[128:131],v[96:99],0
ds_read_b64_tr_b16 a[148:149],v2 offset:10272
ds_read_b64_tr_b16 a[150:151],v2 offset:15392
v_mfma_f32_16x16x32_bf16 a[200:203],a[132:135],v[100:103],a[200:203]
ds_read_b64_tr_b16 a[152:153],v2 offset:20512
ds_read_b64_tr_b16 a[154:155],v2 offset:25632
v_mfma_f32_16x16x32_bf16 a[200:203],a[136:139],v[104:107],a[200:203]
ds_read_b64_tr_b16 a[156:157],v2 offset:30752
ds_read_b64_tr_b16 a[158:159],v2 offset:35872
v_mfma_f32_16x16x32_bf16 a[200:203],a[140:143],v[108:111],a[200:203]
v_mfma_f32_16x16x32_bf16 a[204:207],a[140:143],v[124:127],0
s_add_u32 m0,s82,126976
v_mul_f32 v150,v20,v150
v_mul_f32 v151,v20,v151
v_mfma_f32_16x16x32_bf16 a[204:207],a[136:139],v[120:123],a[204:207]
v_mul_f32 v14,v20,v14
v_mul_f32 v15,v20,v15
v_mfma_f32_16x16x32_bf16 a[204:207],a[132:135],v[116:119],a[204:207]
v_cvt_pk_bf16_f32 v150,v150,v151
v_cvt_pk_bf16_f32 v151,v14,v15
v_mfma_f32_16x16x32_bf16 a[204:207],a[128:131],v[112:115],a[204:207]
s_waitcnt lgkmcnt(6)
v_accvgpr_read_b32 v156,a24
v_accvgpr_read_b32 v157,a25
v_mfma_f32_16x16x32_bf16 a[208:211],a[144:147],v[64:67],0
s_waitcnt lgkmcnt(4)
v_mul_f32 v156,v20,v156
v_mul_f32 v157,v20,v157
v_mfma_f32_16x16x32_bf16 a[208:211],a[148:151],v[68:71],a[208:211]
s_waitcnt lgkmcnt(2)
buffer_load_dwordx4 v11,s[72:75],s80 offen lds
v_mfma_f32_16x16x32_bf16 a[208:211],a[152:155],v[72:75],a[208:211]
s_waitcnt lgkmcnt(0)
v_accvgpr_read_b32 v158,a26
v_accvgpr_read_b32 v159,a27
v_mfma_f32_16x16x32_bf16 a[208:211],a[156:159],v[76:79],a[208:211]
v_cvt_pk_bf16_f32 v156,v156,v157
v_mul_f32 v158,v20,v158
v_mfma_f32_16x16x32_bf16 a[212:215],a[156:159],v[92:95],0
v_mul_f32 v159,v20,v159
v_cvt_pk_bf16_f32 v157,v158,v159
v_mfma_f32_16x16x32_bf16 a[212:215],a[152:155],v[88:91],a[212:215]
buffer_store_dwordx4 v[148:151],v6,s[36:39],s84 offen offset:2048 sc1
v_mfma_f32_16x16x32_bf16 a[212:215],a[148:151],v[84:87],a[212:215]
v_accvgpr_read_b32 v158,a28
v_accvgpr_read_b32 v159,a29
v_mfma_f32_16x16x32_bf16 a[212:215],a[144:147],v[80:83],a[212:215]
ds_read_b64_tr_b16 a[160:161],v1 offset:64
ds_read_b64_tr_b16 a[162:163],v1 offset:5184
v_mfma_f32_16x16x32_bf16 a[216:219],a[144:147],v[96:99],0
ds_read_b64_tr_b16 a[164:165],v1 offset:10304
ds_read_b64_tr_b16 a[166:167],v1 offset:15424
v_mfma_f32_16x16x32_bf16 a[216:219],a[148:151],v[100:103],a[216:219]
ds_read_b64_tr_b16 a[168:169],v1 offset:20544
ds_read_b64_tr_b16 a[170:171],v1 offset:25664
v_mfma_f32_16x16x32_bf16 a[216:219],a[152:155],v[104:107],a[216:219]
ds_read_b64_tr_b16 a[172:173],v1 offset:30784
ds_read_b64_tr_b16 a[174:175],v1 offset:35904
v_mfma_f32_16x16x32_bf16 a[216:219],a[156:159],v[108:111],a[216:219]
v_mfma_f32_16x16x32_bf16 a[220:223],a[156:159],v[124:127],0
s_add_u32 m0,s82,128000
v_accvgpr_read_b32 v16,a30
v_accvgpr_read_b32 v17,a31
v_mfma_f32_16x16x32_bf16 a[220:223],a[152:155],v[120:123],a[220:223]
v_mul_f32 v158,v20,v158
v_mul_f32 v159,v20,v159
v_mfma_f32_16x16x32_bf16 a[220:223],a[148:151],v[116:119],a[220:223]
v_mul_f32 v16,v20,v16
v_mul_f32 v17,v20,v17
v_mfma_f32_16x16x32_bf16 a[220:223],a[144:147],v[112:115],a[220:223]
s_waitcnt lgkmcnt(6)
v_cvt_pk_bf16_f32 v158,v158,v159
v_cvt_pk_bf16_f32 v159,v16,v17
v_mfma_f32_16x16x32_bf16 a[224:227],a[160:163],v[64:67],0
s_waitcnt lgkmcnt(4)
v_max_f32 v21,0x00800000,v52
v_accvgpr_read_b32 v164,a32
v_mfma_f32_16x16x32_bf16 a[224:227],a[164:167],v[68:71],a[224:227]
s_waitcnt lgkmcnt(2)
buffer_load_dwordx4 v7,s[72:75],s83 offen lds
v_mfma_f32_16x16x32_bf16 a[224:227],a[168:171],v[72:75],a[224:227]
s_waitcnt lgkmcnt(0)
v_rcp_f32 v21,v21
v_mfma_f32_16x16x32_bf16 a[224:227],a[172:175],v[76:79],a[224:227]
v_accvgpr_read_b32 v165,a33
v_mul_f32 v164,v21,v164
v_mfma_f32_16x16x32_bf16 a[228:231],a[172:175],v[92:95],0
v_mul_f32 v165,v21,v165
v_accvgpr_read_b32 v166,a34
v_mfma_f32_16x16x32_bf16 a[228:231],a[168:171],v[88:91],a[228:231]
v_accvgpr_read_b32 v167,a35
v_cvt_pk_bf16_f32 v164,v164,v165
v_mfma_f32_16x16x32_bf16 a[228:231],a[164:167],v[84:87],a[228:231]
v_mul_f32 v166,v21,v166
v_mul_f32 v167,v21,v167
v_mfma_f32_16x16x32_bf16 a[228:231],a[160:163],v[80:83],a[228:231]
ds_read_b64_tr_b16 a[176:177],v2 offset:96
ds_read_b64_tr_b16 a[178:179],v2 offset:5216
v_mfma_f32_16x16x32_bf16 a[232:235],a[160:163],v[96:99],0
ds_read_b64_tr_b16 a[180:181],v2 offset:10336
ds_read_b64_tr_b16 a[182:183],v2 offset:15456
v_mfma_f32_16x16x32_bf16 a[232:235],a[164:167],v[100:103],a[232:235]
ds_read_b64_tr_b16 a[184:185],v2 offset:20576
ds_read_b64_tr_b16 a[186:187],v2 offset:25696
v_mfma_f32_16x16x32_bf16 a[232:235],a[168:171],v[104:107],a[232:235]
ds_read_b64_tr_b16 a[188:189],v2 offset:30816
ds_read_b64_tr_b16 a[190:191],v2 offset:35936
v_mfma_f32_16x16x32_bf16 a[232:235],a[172:175],v[108:111],a[232:235]
v_mfma_f32_16x16x32_bf16 a[236:239],a[172:175],v[124:127],0
buffer_store_dwordx4 v[156:159],v6,s[36:39],s84 offen offset:3072 sc1
v_mfma_f32_16x16x32_bf16 a[236:239],a[168:171],v[120:123],a[236:239]
v_cvt_pk_bf16_f32 v165,v166,v167
v_accvgpr_read_b32 v166,a36
v_mfma_f32_16x16x32_bf16 a[236:239],a[164:167],v[116:119],a[236:239]
v_accvgpr_read_b32 v167,a37
v_accvgpr_read_b32 v12,a38
v_mfma_f32_16x16x32_bf16 a[236:239],a[160:163],v[112:115],a[236:239]
s_waitcnt lgkmcnt(6)
v_accvgpr_read_b32 v13,a39
v_mul_f32 v166,v21,v166
v_mfma_f32_16x16x32_bf16 a[240:243],a[176:179],v[64:67],0
s_waitcnt lgkmcnt(4)
v_mul_f32 v167,v21,v167
v_mul_f32 v12,v21,v12
v_mfma_f32_16x16x32_bf16 a[240:243],a[180:183],v[68:71],a[240:243]
s_waitcnt lgkmcnt(2)
buffer_load_dwordx4 v8,s[72:75],s83 offen offset:1024 lds
v_mfma_f32_16x16x32_bf16 a[240:243],a[184:187],v[72:75],a[240:243]
s_waitcnt lgkmcnt(0)
v_mul_f32 v13,v21,v13
v_cvt_pk_bf16_f32 v166,v166,v167
v_mfma_f32_16x16x32_bf16 a[240:243],a[188:191],v[76:79],a[240:243]
v_cvt_pk_bf16_f32 v167,v12,v13
v_accvgpr_read_b32 v172,a40
v_mfma_f32_16x16x32_bf16 a[244:247],a[188:191],v[92:95],0
v_accvgpr_read_b32 v173,a41
v_mul_f32 v172,v21,v172
v_mfma_f32_16x16x32_bf16 a[244:247],a[184:187],v[88:91],a[244:247]
v_mul_f32 v173,v21,v173
v_accvgpr_read_b32 v174,a42
v_mfma_f32_16x16x32_bf16 a[244:247],a[180:183],v[84:87],a[244:247]
v_accvgpr_read_b32 v175,a43
v_cvt_pk_bf16_f32 v172,v172,v173
v_mfma_f32_16x16x32_bf16 a[244:247],a[176:179],v[80:83],a[244:247]
ds_read_b64_tr_b16 a[128:129],v1 offset:128
ds_read_b64_tr_b16 a[130:131],v1 offset:5248
v_mfma_f32_16x16x32_bf16 a[248:251],a[176:179],v[96:99],0
ds_read_b64_tr_b16 a[132:133],v1 offset:10368
ds_read_b64_tr_b16 a[134:135],v1 offset:15488
v_mfma_f32_16x16x32_bf16 a[248:251],a[180:183],v[100:103],a[248:251]
ds_read_b64_tr_b16 a[136:137],v1 offset:20608
ds_read_b64_tr_b16 a[138:139],v1 offset:25728
v_mfma_f32_16x16x32_bf16 a[248:251],a[184:187],v[104:107],a[248:251]
ds_read_b64_tr_b16 a[140:141],v1 offset:30848
ds_read_b64_tr_b16 a[142:143],v1 offset:35968
v_mfma_f32_16x16x32_bf16 a[248:251],a[188:191],v[108:111],a[248:251]
v_mfma_f32_16x16x32_bf16 a[252:255],a[188:191],v[124:127],0
buffer_store_dwordx4 v[164:167],v6,s[36:39],s85 offen sc1
v_mfma_f32_16x16x32_bf16 a[252:255],a[184:187],v[120:123],a[252:255]
v_mul_f32 v174,v21,v174
v_mul_f32 v175,v21,v175
v_mfma_f32_16x16x32_bf16 a[252:255],a[180:183],v[116:119],a[252:255]
v_cvt_pk_bf16_f32 v173,v174,v175
v_accvgpr_read_b32 v174,a44
v_mfma_f32_16x16x32_bf16 a[252:255],a[176:179],v[112:115],a[252:255]
s_waitcnt lgkmcnt(6)
v_accvgpr_read_b32 v175,a45
v_accvgpr_read_b32 v14,a46
v_mfma_f32_16x16x32_bf16 a[0:3],a[128:131],v[64:67],0
s_waitcnt lgkmcnt(4)
v_accvgpr_read_b32 v15,a47
v_mul_f32 v174,v21,v174
v_mfma_f32_16x16x32_bf16 a[0:3],a[132:135],v[68:71],a[0:3]
s_waitcnt lgkmcnt(2)
buffer_load_dwordx4 v9,s[72:75],s83 offen offset:2048 lds
v_mfma_f32_16x16x32_bf16 a[0:3],a[136:139],v[72:75],a[0:3]
s_waitcnt lgkmcnt(0)
v_mul_f32 v175,v21,v175
v_mul_f32 v14,v21,v14
v_mfma_f32_16x16x32_bf16 a[0:3],a[140:143],v[76:79],a[0:3]
v_mul_f32 v15,v21,v15
v_cvt_pk_bf16_f32 v174,v174,v175
v_mfma_f32_16x16x32_bf16 a[4:7],a[140:143],v[92:95],0
v_cvt_pk_bf16_f32 v175,v14,v15
v_accvgpr_read_b32 v180,a48
v_mfma_f32_16x16x32_bf16 a[4:7],a[136:139],v[88:91],a[4:7]
v_accvgpr_read_b32 v181,a49
v_mul_f32 v180,v21,v180
v_mfma_f32_16x16x32_bf16 a[4:7],a[132:135],v[84:87],a[4:7]
s_lshl_b32 s94,s70,4
v_mul_f32 v181,v21,v181
v_accvgpr_read_b32 v182,a50
v_mfma_f32_16x16x32_bf16 a[4:7],a[128:131],v[80:83],a[4:7]
s_add_u32 s94,s94,s40
ds_read_b64_tr_b16 a[144:145],v2 offset:160
ds_read_b64_tr_b16 a[146:147],v2 offset:5280
v_mfma_f32_16x16x32_bf16 a[8:11],a[128:131],v[96:99],0
ds_read_b64_tr_b16 a[148:149],v2 offset:10400
ds_read_b64_tr_b16 a[150:151],v2 offset:15520
v_mfma_f32_16x16x32_bf16 a[8:11],a[132:135],v[100:103],a[8:11]
ds_read_b64_tr_b16 a[152:153],v2 offset:20640
ds_read_b64_tr_b16 a[154:155],v2 offset:25760
v_mfma_f32_16x16x32_bf16 a[8:11],a[136:139],v[104:107],a[8:11]
ds_read_b64_tr_b16 a[156:157],v2 offset:30880
ds_read_b64_tr_b16 a[158:159],v2 offset:36000
v_mfma_f32_16x16x32_bf16 a[8:11],a[140:143],v[108:111],a[8:11]
v_mfma_f32_16x16x32_bf16 a[12:15],a[140:143],v[124:127],0
s_lshl_b32 s94,s94,4
v_accvgpr_read_b32 v183,a51
v_cvt_pk_bf16_f32 v180,v180,v181
v_mfma_f32_16x16x32_bf16 a[12:15],a[136:139],v[120:123],a[12:15]
v_mul_f32 v182,v21,v182
v_mul_f32 v183,v21,v183
v_mfma_f32_16x16x32_bf16 a[12:15],a[132:135],v[116:119],a[12:15]
v_cvt_pk_bf16_f32 v181,v182,v183
v_accvgpr_read_b32 v182,a52
v_mfma_f32_16x16x32_bf16 a[12:15],a[128:131],v[112:115],a[12:15]
s_waitcnt lgkmcnt(6)
v_accvgpr_read_b32 v183,a53
v_accvgpr_read_b32 v16,a54
v_mfma_f32_16x16x32_bf16 a[16:19],a[144:147],v[64:67],0
s_waitcnt lgkmcnt(4)
buffer_store_dwordx4 v[172:175],v6,s[36:39],s85 offen offset:1024 sc1
v_mfma_f32_16x16x32_bf16 a[16:19],a[148:151],v[68:71],a[16:19]
s_waitcnt lgkmcnt(2)
v_accvgpr_read_b32 v17,a55
v_mul_f32 v182,v21,v182
v_mfma_f32_16x16x32_bf16 a[16:19],a[152:155],v[72:75],a[16:19]
s_waitcnt lgkmcnt(0)
v_mul_f32 v183,v21,v183
v_mul_f32 v16,v21,v16
v_mfma_f32_16x16x32_bf16 a[16:19],a[156:159],v[76:79],a[16:19]
v_mul_f32 v17,v21,v17
v_cvt_pk_bf16_f32 v182,v182,v183
v_mfma_f32_16x16x32_bf16 a[20:23],a[156:159],v[92:95],0
buffer_load_dwordx4 v10,s[72:75],s83 offen offset:3072 lds
v_mfma_f32_16x16x32_bf16 a[20:23],a[152:155],v[88:91],a[20:23]
v_cvt_pk_bf16_f32 v183,v16,v17
v_accvgpr_read_b32 v188,a56
v_mfma_f32_16x16x32_bf16 a[20:23],a[148:151],v[84:87],a[20:23]
v_accvgpr_read_b32 v189,a57
v_mul_f32 v188,v21,v188
v_mfma_f32_16x16x32_bf16 a[20:23],a[144:147],v[80:83],a[20:23]
ds_read_b64_tr_b16 a[160:161],v1 offset:192
ds_read_b64_tr_b16 a[162:163],v1 offset:5312
v_mfma_f32_16x16x32_bf16 a[24:27],a[144:147],v[96:99],0
ds_read_b64_tr_b16 a[164:165],v1 offset:10432
ds_read_b64_tr_b16 a[166:167],v1 offset:15552
v_mfma_f32_16x16x32_bf16 a[24:27],a[148:151],v[100:103],a[24:27]
)A"
R"A(ds_read_b64_tr_b16 a[168:169],v1 offset:20672
ds_read_b64_tr_b16 a[170:171],v1 offset:25792
v_mfma_f32_16x16x32_bf16 a[24:27],a[152:155],v[104:107],a[24:27]
ds_read_b64_tr_b16 a[172:173],v1 offset:30912
ds_read_b64_tr_b16 a[174:175],v1 offset:36032
v_mfma_f32_16x16x32_bf16 a[24:27],a[156:159],v[108:111],a[24:27]
v_mfma_f32_16x16x32_bf16 a[28:31],a[156:159],v[124:127],0
s_add_u32 m0,s82,132096
v_mul_f32 v189,v21,v189
v_accvgpr_read_b32 v190,a58
v_mfma_f32_16x16x32_bf16 a[28:31],a[152:155],v[120:123],a[28:31]
v_accvgpr_read_b32 v191,a59
v_cvt_pk_bf16_f32 v188,v188,v189
v_mfma_f32_16x16x32_bf16 a[28:31],a[148:151],v[116:119],a[28:31]
v_mul_f32 v190,v21,v190
v_mul_f32 v191,v21,v191
v_mfma_f32_16x16x32_bf16 a[28:31],a[144:147],v[112:115],a[28:31]
s_waitcnt lgkmcnt(6)
v_cvt_pk_bf16_f32 v189,v190,v191
v_accvgpr_read_b32 v190,a60
v_mfma_f32_16x16x32_bf16 a[32:35],a[160:163],v[64:67],0
s_waitcnt lgkmcnt(4)
v_accvgpr_read_b32 v191,a61
v_accvgpr_read_b32 v12,a62
v_mfma_f32_16x16x32_bf16 a[32:35],a[164:167],v[68:71],a[32:35]
s_waitcnt lgkmcnt(2)
buffer_load_dwordx4 v11,s[72:75],s83 offen lds
v_mfma_f32_16x16x32_bf16 a[32:35],a[168:171],v[72:75],a[32:35]
s_waitcnt lgkmcnt(0)
v_accvgpr_read_b32 v13,a63
v_mul_f32 v190,v21,v190
v_mfma_f32_16x16x32_bf16 a[32:35],a[172:175],v[76:79],a[32:35]
v_mul_f32 v191,v21,v191
v_mul_f32 v12,v21,v12
v_mfma_f32_16x16x32_bf16 a[36:39],a[172:175],v[92:95],0
v_mul_f32 v13,v21,v13
v_cvt_pk_bf16_f32 v190,v190,v191
v_mfma_f32_16x16x32_bf16 a[36:39],a[168:171],v[88:91],a[36:39]
buffer_store_dwordx4 v[180:183],v6,s[36:39],s85 offen offset:2048 sc1
v_mfma_f32_16x16x32_bf16 a[36:39],a[164:167],v[84:87],a[36:39]
v_cvt_pk_bf16_f32 v191,v12,v13
v_max_f32 v22,0x00800000,v56
v_mfma_f32_16x16x32_bf16 a[36:39],a[160:163],v[80:83],a[36:39]
ds_read_b64_tr_b16 a[176:177],v2 offset:224
ds_read_b64_tr_b16 a[178:179],v2 offset:5344
v_mfma_f32_16x16x32_bf16 a[40:43],a[160:163],v[96:99],0
ds_read_b64_tr_b16 a[180:181],v2 offset:10464
ds_read_b64_tr_b16 a[182:183],v2 offset:15584
v_mfma_f32_16x16x32_bf16 a[40:43],a[164:167],v[100:103],a[40:43]
ds_read_b64_tr_b16 a[184:185],v2 offset:20704
ds_read_b64_tr_b16 a[186:187],v2 offset:25824
v_mfma_f32_16x16x32_bf16 a[40:43],a[168:171],v[104:107],a[40:43]
ds_read_b64_tr_b16 a[188:189],v2 offset:30944
ds_read_b64_tr_b16 a[190:191],v2 offset:36064
v_mfma_f32_16x16x32_bf16 a[40:43],a[172:175],v[108:111],a[40:43]
v_mfma_f32_16x16x32_bf16 a[44:47],a[172:175],v[124:127],0
v_rcp_f32 v22,v22
v_mfma_f32_16x16x32_bf16 a[44:47],a[168:171],v[120:123],a[44:47]
v_bfe_u32 v24,v6,4,2
v_min_u32 v24,2,v24
v_mfma_f32_16x16x32_bf16 a[44:47],a[164:167],v[116:119],a[44:47]
v_add_u32 v24,s71,v24
v_add_u32 v24,1,v24
v_mfma_f32_16x16x32_bf16 a[44:47],a[160:163],v[112:115],a[44:47]
s_waitcnt lgkmcnt(6)
v_and_b32 v24,3,v24
v_lshlrev_b32 v24,6,v24
v_mfma_f32_16x16x32_bf16 a[48:51],a[176:179],v[64:67],0
s_waitcnt lgkmcnt(4)
buffer_load_dwordx2 v[26:27],v24,s[64:67],s94 offen sc1
v_mfma_f32_16x16x32_bf16 a[48:51],a[180:183],v[68:71],a[48:51]
s_waitcnt lgkmcnt(2)
v_accvgpr_read_b32 v196,a64
v_accvgpr_read_b32 v197,a65
v_mfma_f32_16x16x32_bf16 a[48:51],a[184:187],v[72:75],a[48:51]
s_waitcnt lgkmcnt(0)
v_mul_f32 v196,v22,v196
v_mul_f32 v197,v22,v197
v_mfma_f32_16x16x32_bf16 a[48:51],a[188:191],v[76:79],a[48:51]
v_accvgpr_read_b32 v198,a66
v_accvgpr_read_b32 v199,a67
v_mfma_f32_16x16x32_bf16 a[52:55],a[188:191],v[92:95],0
v_cvt_pk_bf16_f32 v196,v196,v197
v_mul_f32 v198,v22,v198
v_mfma_f32_16x16x32_bf16 a[52:55],a[184:187],v[88:91],a[52:55]
v_mul_f32 v199,v22,v199
v_cvt_pk_bf16_f32 v197,v198,v199
v_mfma_f32_16x16x32_bf16 a[52:55],a[180:183],v[84:87],a[52:55]
v_accvgpr_read_b32 v198,a68
v_accvgpr_read_b32 v199,a69
v_mfma_f32_16x16x32_bf16 a[52:55],a[176:179],v[80:83],a[52:55]
ds_read_b64_tr_b16 a[128:129],v1 offset:256
ds_read_b64_tr_b16 a[130:131],v1 offset:5376
v_mfma_f32_16x16x32_bf16 a[56:59],a[176:179],v[96:99],0
ds_read_b64_tr_b16 a[132:133],v1 offset:10496
ds_read_b64_tr_b16 a[134:135],v1 offset:15616
v_mfma_f32_16x16x32_bf16 a[56:59],a[180:183],v[100:103],a[56:59]
ds_read_b64_tr_b16 a[136:137],v1 offset:20736
ds_read_b64_tr_b16 a[138:139],v1 offset:25856
v_mfma_f32_16x16x32_bf16 a[56:59],a[184:187],v[104:107],a[56:59]
ds_read_b64_tr_b16 a[140:141],v1 offset:30976
ds_read_b64_tr_b16 a[142:143],v1 offset:36096
v_mfma_f32_16x16x32_bf16 a[56:59],a[188:191],v[108:111],a[56:59]
v_mfma_f32_16x16x32_bf16 a[60:63],a[188:191],v[124:127],0
buffer_store_dwordx4 v[188:191],v6,s[36:39],s85 offen offset:3072 sc1
v_mfma_f32_16x16x32_bf16 a[60:63],a[184:187],v[120:123],a[60:63]
v_accvgpr_read_b32 v14,a70
v_accvgpr_read_b32 v15,a71
v_mfma_f32_16x16x32_bf16 a[60:63],a[180:183],v[116:119],a[60:63]
v_mul_f32 v198,v22,v198
v_mul_f32 v199,v22,v199
v_mfma_f32_16x16x32_bf16 a[60:63],a[176:179],v[112:115],a[60:63]
s_waitcnt lgkmcnt(6)
v_mul_f32 v14,v22,v14
v_mul_f32 v15,v22,v15
v_mfma_f32_16x16x32_bf16 a[64:67],a[128:131],v[64:67],0
s_waitcnt lgkmcnt(4)
v_cvt_pk_bf16_f32 v198,v198,v199
v_cvt_pk_bf16_f32 v199,v14,v15
v_mfma_f32_16x16x32_bf16 a[64:67],a[132:135],v[68:71],a[64:67]
s_waitcnt lgkmcnt(2)
v_accvgpr_read_b32 v204,a72
v_accvgpr_read_b32 v205,a73
v_mfma_f32_16x16x32_bf16 a[64:67],a[136:139],v[72:75],a[64:67]
s_waitcnt lgkmcnt(0)
v_mul_f32 v204,v22,v204
v_mul_f32 v205,v22,v205
v_mfma_f32_16x16x32_bf16 a[64:67],a[140:143],v[76:79],a[64:67]
v_accvgpr_read_b32 v206,a74
v_accvgpr_read_b32 v207,a75
v_mfma_f32_16x16x32_bf16 a[68:71],a[140:143],v[92:95],0
v_cvt_pk_bf16_f32 v204,v204,v205
v_mul_f32 v206,v22,v206
v_mfma_f32_16x16x32_bf16 a[68:71],a[136:139],v[88:91],a[68:71]
v_mul_f32 v207,v22,v207
v_cvt_pk_bf16_f32 v205,v206,v207
v_mfma_f32_16x16x32_bf16 a[68:71],a[132:135],v[84:87],a[68:71]
v_accvgpr_read_b32 v206,a76
v_accvgpr_read_b32 v207,a77
v_mfma_f32_16x16x32_bf16 a[68:71],a[128:131],v[80:83],a[68:71]
ds_read_b64_tr_b16 a[144:145],v2 offset:288
ds_read_b64_tr_b16 a[146:147],v2 offset:5408
v_mfma_f32_16x16x32_bf16 a[72:75],a[128:131],v[96:99],0
ds_read_b64_tr_b16 a[148:149],v2 offset:10528
ds_read_b64_tr_b16 a[150:151],v2 offset:15648
v_mfma_f32_16x16x32_bf16 a[72:75],a[132:135],v[100:103],a[72:75]
ds_read_b64_tr_b16 a[152:153],v2 offset:20768
ds_read_b64_tr_b16 a[154:155],v2 offset:25888
v_mfma_f32_16x16x32_bf16 a[72:75],a[136:139],v[104:107],a[72:75]
ds_read_b64_tr_b16 a[156:157],v2 offset:31008
ds_read_b64_tr_b16 a[158:159],v2 offset:36128
v_mfma_f32_16x16x32_bf16 a[72:75],a[140:143],v[108:111],a[72:75]
v_accvgpr_read_b32 v16,a78
v_accvgpr_read_b32 v17,a79
v_mfma_f32_16x16x32_bf16 a[76:79],a[140:143],v[124:127],0
v_mul_f32 v206,v22,v206
v_mul_f32 v207,v22,v207
v_mfma_f32_16x16x32_bf16 a[76:79],a[136:139],v[120:123],a[76:79]
v_mul_f32 v16,v22,v16
v_mul_f32 v17,v22,v17
v_mfma_f32_16x16x32_bf16 a[76:79],a[132:135],v[116:119],a[76:79]
v_cvt_pk_bf16_f32 v206,v206,v207
v_cvt_pk_bf16_f32 v207,v16,v17
v_mfma_f32_16x16x32_bf16 a[76:79],a[128:131],v[112:115],a[76:79]
s_waitcnt lgkmcnt(6)
v_accvgpr_read_b32 v212,a80
v_accvgpr_read_b32 v213,a81
v_accvgpr_read_b32 v214,a82
v_accvgpr_read_b32 v215,a83
v_mfma_f32_16x16x32_bf16 a[80:83],a[144:147],v[64:67],0
s_waitcnt lgkmcnt(4)
v_mul_f32 v212,v22,v212
v_mul_f32 v213,v22,v213
v_mfma_f32_16x16x32_bf16 a[80:83],a[148:151],v[68:71],a[80:83]
s_waitcnt lgkmcnt(2)
v_cvt_pk_bf16_f32 v212,v212,v213
v_mul_f32 v214,v22,v214
v_mfma_f32_16x16x32_bf16 a[80:83],a[152:155],v[72:75],a[80:83]
s_waitcnt lgkmcnt(0)
v_mul_f32 v215,v22,v215
v_cvt_pk_bf16_f32 v213,v214,v215
v_mfma_f32_16x16x32_bf16 a[80:83],a[156:159],v[76:79],a[80:83]
v_accvgpr_read_b32 v214,a84
v_accvgpr_read_b32 v215,a85
v_accvgpr_read_b32 v12,a86
v_accvgpr_read_b32 v13,a87
v_mfma_f32_16x16x32_bf16 a[84:87],a[156:159],v[92:95],0
v_mul_f32 v214,v22,v214
v_mul_f32 v215,v22,v215
v_mfma_f32_16x16x32_bf16 a[84:87],a[152:155],v[88:91],a[84:87]
v_mul_f32 v12,v22,v12
v_mul_f32 v13,v22,v13
v_mfma_f32_16x16x32_bf16 a[84:87],a[148:151],v[84:87],a[84:87]
v_add_u32 v1,40960,v1
v_cvt_pk_bf16_f32 v214,v214,v215
v_mfma_f32_16x16x32_bf16 a[84:87],a[144:147],v[80:83],a[84:87]
ds_read_b64_tr_b16 a[160:161],v1
ds_read_b64_tr_b16 a[162:163],v1 offset:5120
v_accvgpr_read_b32 v220,a88
v_accvgpr_read_b32 v221,a89
v_accvgpr_read_b32 v222,a90
v_accvgpr_read_b32 v223,a91
v_mfma_f32_16x16x32_bf16 a[88:91],a[144:147],v[96:99],0
ds_read_b64_tr_b16 a[164:165],v1 offset:10240
ds_read_b64_tr_b16 a[166:167],v1 offset:15360
v_max_f32 v23,0x00800000,v60
v_mfma_f32_16x16x32_bf16 a[88:91],a[148:151],v[100:103],a[88:91]
ds_read_b64_tr_b16 a[168:169],v1 offset:20480
ds_read_b64_tr_b16 a[170:171],v1 offset:25600
v_rcp_f32 v23,v23
v_accvgpr_read_b32 v228,a96
v_accvgpr_read_b32 v229,a97
v_mfma_f32_16x16x32_bf16 a[88:91],a[152:155],v[104:107],a[88:91]
ds_read_b64_tr_b16 a[172:173],v1 offset:30720
ds_read_b64_tr_b16 a[174:175],v1 offset:35840
v_mul_f32 v220,v22,v220
v_mul_f32 v221,v22,v221
v_cvt_pk_bf16_f32 v220,v220,v221
v_mul_f32 v222,v22,v222
v_mul_f32 v223,v22,v223
v_mul_f32 v228,v23,v228
v_mul_f32 v229,v23,v229
v_accvgpr_read_b32 v230,a98
v_accvgpr_read_b32 v231,a99
v_cvt_pk_bf16_f32 v228,v228,v229
v_mul_f32 v230,v23,v230
v_mul_f32 v231,v23,v231
v_mfma_f32_16x16x32_bf16 a[88:91],a[156:159],v[108:111],a[88:91]
v_cvt_pk_bf16_f32 v221,v222,v223
v_accvgpr_read_b32 v222,a92
v_accvgpr_read_b32 v223,a93
v_accvgpr_read_b32 v14,a94
v_accvgpr_read_b32 v15,a95
v_cvt_pk_bf16_f32 v229,v230,v231
v_accvgpr_read_b32 v230,a100
v_accvgpr_read_b32 v231,a101
v_accvgpr_read_b32 v236,a104
v_accvgpr_read_b32 v237,a105
v_mul_f32 v236,v23,v236
v_mul_f32 v237,v23,v237
v_accvgpr_read_b32 v238,a106
v_accvgpr_read_b32 v239,a107
v_accvgpr_read_b32 v244,a112
v_accvgpr_read_b32 v245,a113
v_mul_f32 v244,v23,v244
v_mul_f32 v245,v23,v245
v_accvgpr_read_b32 v246,a114
v_accvgpr_read_b32 v247,a115
v_accvgpr_read_b32 v252,a120
v_accvgpr_read_b32 v253,a121
v_cmp_nlt_f32 s[56:57],v48,s63
v_mul_f32 v252,v23,v252
v_mul_f32 v253,v23,v253
v_accvgpr_read_b32 v254,a122
v_accvgpr_read_b32 v255,a123
v_mfma_f32_16x16x32_bf16 a[92:95],a[156:159],v[124:127],0
v_mul_f32 v222,v22,v222
v_mul_f32 v223,v22,v223
v_mul_f32 v14,v22,v14
v_mul_f32 v15,v22,v15
v_cvt_pk_bf16_f32 v222,v222,v223
v_accvgpr_read_b32 v16,a102
v_accvgpr_read_b32 v17,a103
v_mul_f32 v230,v23,v230
v_mul_f32 v231,v23,v231
v_mul_f32 v16,v23,v16
v_mul_f32 v17,v23,v17
v_cvt_pk_bf16_f32 v230,v230,v231
v_cvt_pk_bf16_f32 v236,v236,v237
v_mul_f32 v238,v23,v238
v_mul_f32 v239,v23,v239
v_cvt_pk_bf16_f32 v237,v238,v239
v_cvt_pk_bf16_f32 v244,v244,v245
v_mul_f32 v246,v23,v246
v_mul_f32 v247,v23,v247
v_cvt_pk_bf16_f32 v245,v246,v247
v_cvt_pk_bf16_f32 v252,v252,v253
v_mul_f32 v254,v23,v254
v_mul_f32 v255,v23,v255
v_cmp_nlt_f32 s[58:59],v52,s63
s_nop 1
s_or_b64 s[56:57],s[56:57],s[58:59]
v_cvt_pk_bf16_f32 v253,v254,v255
v_mfma_f32_16x16x32_bf16 a[92:95],a[152:155],v[120:123],a[92:95]
v_cvt_pk_bf16_f32 v215,v12,v13
v_cvt_pk_bf16_f32 v223,v14,v15
v_cvt_pk_bf16_f32 v231,v16,v17
v_accvgpr_read_b32 v238,a108
v_accvgpr_read_b32 v239,a109
v_accvgpr_read_b32 v12,a110
v_accvgpr_read_b32 v13,a111
v_mul_f32 v238,v23,v238
v_mul_f32 v239,v23,v239
v_accvgpr_read_b32 v246,a116
v_accvgpr_read_b32 v247,a117
v_accvgpr_read_b32 v14,a118
v_accvgpr_read_b32 v15,a119
v_mul_f32 v246,v23,v246
v_mul_f32 v247,v23,v247
v_accvgpr_read_b32 v254,a124
v_accvgpr_read_b32 v255,a125
v_cmp_nlt_f32 s[58:59],v56,s63
s_nop 1
s_or_b64 s[56:57],s[56:57],s[58:59]
v_accvgpr_read_b32 v16,a126
v_accvgpr_read_b32 v17,a127
v_mul_f32 v254,v23,v254
v_mul_f32 v255,v23,v255
v_mfma_f32_16x16x32_bf16 a[92:95],a[148:151],v[116:119],a[92:95]
v_mul_f32 v12,v23,v12
v_mul_f32 v13,v23,v13
v_cvt_pk_bf16_f32 v238,v238,v239
v_cvt_pk_bf16_f32 v239,v12,v13
v_mul_f32 v14,v23,v14
v_mul_f32 v15,v23,v15
v_cvt_pk_bf16_f32 v246,v246,v247
v_cvt_pk_bf16_f32 v247,v14,v15
v_mul_f32 v16,v23,v16
v_mul_f32 v17,v23,v17
v_cvt_pk_bf16_f32 v254,v254,v255
v_cmp_nlt_f32 s[58:59],v60,s63
s_nop 1
s_or_b64 s[56:57],s[56:57],s[58:59]
v_cvt_pk_bf16_f32 v255,v16,v17
s_waitcnt vmcnt(1)
v_cmp_ne_u32 s[90:91],s68,v26
v_cmp_ne_u32 s[92:93],s69,v27
s_nop 0
s_or_b64 s[90:91],s[90:91],s[92:93]
s_cbranch_scc1 L_fr7_%=
L_fo7_%=:
v_mfma_f32_16x16x32_bf16 a[92:95],a[144:147],v[112:115],a[92:95]
)A"
R"A(s_cmp_lg_u64 s[56:57],0
s_cbranch_scc1 L_slow1_%=
L_sret1_%=:
v_add_u32 v2,40960,v2
s_waitcnt lgkmcnt(6)
v_mfma_f32_16x16x32_bf16 a[192:195],a[160:163],v[132:135],a[192:195]
s_waitcnt lgkmcnt(4)
buffer_store_dwordx4 v[196:199],v6,s[36:39],s86 offen sc1
v_mfma_f32_16x16x32_bf16 a[192:195],a[164:167],v[140:143],a[192:195]
s_cmp_ge_u32 0,s71
s_cselect_b32 s76,2,0
s_lshl_b32 s76,s76,16
s_waitcnt lgkmcnt(2)
v_mfma_f32_16x16x32_bf16 a[192:195],a[168:171],v[148:151],a[192:195]
s_add_u32 s77,s76,4096
s_add_u32 s78,s76,8192
s_add_u32 s79,s76,12288
s_max_i32 s76,s76,s18
s_waitcnt lgkmcnt(0)
v_mfma_f32_16x16x32_bf16 a[192:195],a[172:175],v[156:159],a[192:195]
s_max_i32 s77,s77,s19
ds_read_b64_tr_b16 a[176:177],v2 offset:32
ds_read_b64_tr_b16 a[178:179],v2 offset:5152
v_mfma_f32_16x16x32_bf16 a[196:199],a[172:175],v[188:191],a[196:199]
s_max_i32 s78,s78,s100
buffer_load_dwordx4 v[128:131],v6,s[28:31],s76 offen sc1
v_mfma_f32_16x16x32_bf16 a[196:199],a[168:171],v[180:183],a[196:199]
s_max_i32 s79,s79,s101
ds_read_b64_tr_b16 a[180:181],v2 offset:10272
ds_read_b64_tr_b16 a[182:183],v2 offset:15392
v_mfma_f32_16x16x32_bf16 a[196:199],a[164:167],v[172:175],a[196:199]
s_cmp_ge_u32 2,s71
s_cselect_b32 s80,6,4
ds_read_b64_tr_b16 a[184:185],v2 offset:20512
v_mfma_f32_16x16x32_bf16 a[196:199],a[160:163],v[164:167],a[196:199]
s_mul_i32 s80,s80,163840
s_add_u32 s80,s80,s81
ds_read_b64_tr_b16 a[186:187],v2 offset:25632
v_mfma_f32_16x16x32_bf16 a[200:203],a[160:163],v[196:199],a[200:203]
s_add_u32 s83,s80,20480
buffer_store_dwordx4 v[204:207],v6,s[36:39],s86 offen offset:1024 sc1
v_mfma_f32_16x16x32_bf16 a[200:203],a[164:167],v[204:207],a[200:203]
ds_read_b64_tr_b16 a[188:189],v2 offset:30752
ds_read_b64_tr_b16 a[190:191],v2 offset:35872
v_mfma_f32_16x16x32_bf16 a[200:203],a[168:171],v[212:215],a[200:203]
ds_read_b64_tr_b16 a[128:129],v1 offset:64
v_mfma_f32_16x16x32_bf16 a[200:203],a[172:175],v[220:223],a[200:203]
ds_read_b64_tr_b16 a[130:131],v1 offset:5184
v_mfma_f32_16x16x32_bf16 a[204:207],a[172:175],v[252:255],a[204:207]
buffer_load_dwordx4 v[160:163],v6,s[28:31],s76 offen offset:1024 sc1
v_mfma_f32_16x16x32_bf16 a[204:207],a[168:171],v[244:247],a[204:207]
ds_read_b64_tr_b16 a[132:133],v1 offset:10304
ds_read_b64_tr_b16 a[134:135],v1 offset:15424
v_mfma_f32_16x16x32_bf16 a[204:207],a[164:167],v[236:239],a[204:207]
ds_read_b64_tr_b16 a[136:137],v1 offset:20544
v_mfma_f32_16x16x32_bf16 a[204:207],a[160:163],v[228:231],a[204:207]
s_barrier
buffer_load_dwordx4 v[192:195],v6,s[28:31],s76 offen offset:2048 sc1
s_waitcnt lgkmcnt(7)
v_mfma_f32_16x16x32_bf16 a[208:211],a[176:179],v[132:135],a[208:211]
ds_read_b64_tr_b16 a[138:139],v1 offset:25664
ds_read_b64_tr_b16 a[140:141],v1 offset:30784
v_mfma_f32_16x16x32_bf16 a[208:211],a[180:183],v[140:143],a[208:211]
ds_read_b64_tr_b16 a[142:143],v1 offset:35904
v_mfma_f32_16x16x32_bf16 a[208:211],a[184:187],v[148:151],a[208:211]
s_add_u32 m0,s82,0
ds_read_b64_tr_b16 a[144:145],v2 offset:96
s_waitcnt lgkmcnt(8)
v_mfma_f32_16x16x32_bf16 a[208:211],a[188:191],v[156:159],a[208:211]
buffer_store_dwordx4 v[212:215],v6,s[36:39],s86 offen offset:2048 sc1
v_mfma_f32_16x16x32_bf16 a[212:215],a[188:191],v[188:191],a[212:215]
ds_read_b64_tr_b16 a[146:147],v2 offset:5216
ds_read_b64_tr_b16 a[148:149],v2 offset:10336
v_mfma_f32_16x16x32_bf16 a[212:215],a[184:187],v[180:183],a[212:215]
ds_read_b64_tr_b16 a[150:151],v2 offset:15456
v_mfma_f32_16x16x32_bf16 a[212:215],a[180:183],v[172:175],a[212:215]
ds_read_b64_tr_b16 a[152:153],v2 offset:20576
v_mfma_f32_16x16x32_bf16 a[212:215],a[176:179],v[164:167],a[212:215]
buffer_load_dwordx4 v[224:227],v6,s[28:31],s76 offen offset:3072 sc1
v_mfma_f32_16x16x32_bf16 a[216:219],a[176:179],v[196:199],a[216:219]
ds_read_b64_tr_b16 a[154:155],v2 offset:25696
ds_read_b64_tr_b16 a[156:157],v2 offset:30816
v_mfma_f32_16x16x32_bf16 a[216:219],a[180:183],v[204:207],a[216:219]
ds_read_b64_tr_b16 a[158:159],v2 offset:35936
v_mfma_f32_16x16x32_bf16 a[216:219],a[184:187],v[212:215],a[216:219]
ds_read_b64_tr_b16 a[160:161],v1 offset:128
v_mfma_f32_16x16x32_bf16 a[216:219],a[188:191],v[220:223],a[216:219]
buffer_load_dwordx4 v7,s[72:75],s80 offen lds
v_mfma_f32_16x16x32_bf16 a[220:223],a[188:191],v[252:255],a[220:223]
ds_read_b64_tr_b16 a[162:163],v1 offset:5248
ds_read_b64_tr_b16 a[164:165],v1 offset:10368
v_mfma_f32_16x16x32_bf16 a[220:223],a[184:187],v[244:247],a[220:223]
ds_read_b64_tr_b16 a[166:167],v1 offset:15488
v_mfma_f32_16x16x32_bf16 a[220:223],a[180:183],v[236:239],a[220:223]
ds_read_b64_tr_b16 a[168:169],v1 offset:20608
v_mfma_f32_16x16x32_bf16 a[220:223],a[176:179],v[228:231],a[220:223]
s_waitcnt lgkmcnt(8)
buffer_load_dwordx4 v[136:139],v6,s[28:31],s77 offen sc1
v_mfma_f32_16x16x32_bf16 a[224:227],a[128:131],v[132:135],a[224:227]
ds_read_b64_tr_b16 a[170:171],v1 offset:25728
ds_read_b64_tr_b16 a[172:173],v1 offset:30848
v_mfma_f32_16x16x32_bf16 a[224:227],a[132:135],v[140:143],a[224:227]
ds_read_b64_tr_b16 a[174:175],v1 offset:35968
v_mfma_f32_16x16x32_bf16 a[224:227],a[136:139],v[148:151],a[224:227]
ds_read_b64_tr_b16 a[176:177],v2 offset:160
v_mfma_f32_16x16x32_bf16 a[224:227],a[140:143],v[156:159],a[224:227]
buffer_load_dwordx4 v[168:171],v6,s[28:31],s77 offen offset:1024 sc1
v_mfma_f32_16x16x32_bf16 a[228:231],a[140:143],v[188:191],a[228:231]
ds_read_b64_tr_b16 a[178:179],v2 offset:5280
ds_read_b64_tr_b16 a[180:181],v2 offset:10400
v_mfma_f32_16x16x32_bf16 a[228:231],a[136:139],v[180:183],a[228:231]
ds_read_b64_tr_b16 a[182:183],v2 offset:15520
v_mfma_f32_16x16x32_bf16 a[228:231],a[132:135],v[172:175],a[228:231]
ds_read_b64_tr_b16 a[184:185],v2 offset:20640
v_mfma_f32_16x16x32_bf16 a[228:231],a[128:131],v[164:167],a[228:231]
buffer_load_dwordx4 v8,s[72:75],s80 offen offset:1024 lds
v_mfma_f32_16x16x32_bf16 a[232:235],a[128:131],v[196:199],a[232:235]
ds_read_b64_tr_b16 a[186:187],v2 offset:25760
ds_read_b64_tr_b16 a[188:189],v2 offset:30880
v_mfma_f32_16x16x32_bf16 a[232:235],a[132:135],v[204:207],a[232:235]
ds_read_b64_tr_b16 a[190:191],v2 offset:36000
v_mfma_f32_16x16x32_bf16 a[232:235],a[136:139],v[212:215],a[232:235]
v_mfma_f32_16x16x32_bf16 a[232:235],a[140:143],v[220:223],a[232:235]
buffer_store_dwordx4 v[220:223],v6,s[36:39],s86 offen offset:3072 sc1
v_mfma_f32_16x16x32_bf16 a[236:239],a[140:143],v[252:255],a[236:239]
ds_read_b64_tr_b16 a[140:141],v1 offset:30912
ds_read_b64_tr_b16 a[142:143],v1 offset:36032
v_mfma_f32_16x16x32_bf16 a[236:239],a[136:139],v[244:247],a[236:239]
ds_read_b64_tr_b16 a[136:137],v1 offset:20672
v_mfma_f32_16x16x32_bf16 a[236:239],a[132:135],v[236:239],a[236:239]
ds_read_b64_tr_b16 a[132:133],v1 offset:10432
v_mfma_f32_16x16x32_bf16 a[236:239],a[128:131],v[228:231],a[236:239]
buffer_load_dwordx4 v[200:203],v6,s[28:31],s77 offen offset:2048 sc1
v_mfma_f32_16x16x32_bf16 a[240:243],a[144:147],v[132:135],a[240:243]
ds_read_b64_tr_b16 a[128:129],v1 offset:192
ds_read_b64_tr_b16 a[130:131],v1 offset:5312
v_mfma_f32_16x16x32_bf16 a[240:243],a[148:151],v[140:143],a[240:243]
s_waitcnt lgkmcnt(6)
ds_read_b64_tr_b16 a[134:135],v1 offset:15552
v_mfma_f32_16x16x32_bf16 a[240:243],a[152:155],v[148:151],a[240:243]
ds_read_b64_tr_b16 a[138:139],v1 offset:25792
v_mfma_f32_16x16x32_bf16 a[240:243],a[156:159],v[156:159],a[240:243]
buffer_load_dwordx4 v[232:235],v6,s[28:31],s77 offen offset:3072 sc1
v_mfma_f32_16x16x32_bf16 a[244:247],a[156:159],v[188:191],a[244:247]
v_mfma_f32_16x16x32_bf16 a[244:247],a[152:155],v[180:183],a[244:247]
v_mfma_f32_16x16x32_bf16 a[244:247],a[148:151],v[172:175],a[244:247]
v_mfma_f32_16x16x32_bf16 a[244:247],a[144:147],v[164:167],a[244:247]
buffer_load_dwordx4 v9,s[72:75],s80 offen offset:2048 lds
v_mfma_f32_16x16x32_bf16 a[248:251],a[144:147],v[196:199],a[248:251]
v_mfma_f32_16x16x32_bf16 a[248:251],a[148:151],v[204:207],a[248:251]
v_mfma_f32_16x16x32_bf16 a[248:251],a[152:155],v[212:215],a[248:251]
v_mfma_f32_16x16x32_bf16 a[248:251],a[156:159],v[220:223],a[248:251]
buffer_load_dwordx4 v[144:147],v6,s[28:31],s78 offen sc1
v_mfma_f32_16x16x32_bf16 a[252:255],a[156:159],v[252:255],a[252:255]
ds_read_b64_tr_b16 a[156:157],v2 offset:30944
ds_read_b64_tr_b16 a[158:159],v2 offset:36064
v_mfma_f32_16x16x32_bf16 a[252:255],a[152:155],v[244:247],a[252:255]
ds_read_b64_tr_b16 a[152:153],v2 offset:20704
v_mfma_f32_16x16x32_bf16 a[252:255],a[148:151],v[236:239],a[252:255]
ds_read_b64_tr_b16 a[148:149],v2 offset:10464
v_mfma_f32_16x16x32_bf16 a[252:255],a[144:147],v[228:231],a[252:255]
buffer_store_dwordx4 v[228:231],v6,s[36:39],s87 offen sc1
v_mfma_f32_16x16x32_bf16 a[0:3],a[160:163],v[132:135],a[0:3]
ds_read_b64_tr_b16 a[144:145],v2 offset:224
ds_read_b64_tr_b16 a[146:147],v2 offset:5344
v_mfma_f32_16x16x32_bf16 a[0:3],a[164:167],v[140:143],a[0:3]
ds_read_b64_tr_b16 a[150:151],v2 offset:15584
v_mfma_f32_16x16x32_bf16 a[0:3],a[168:171],v[148:151],a[0:3]
ds_read_b64_tr_b16 a[154:155],v2 offset:25824
v_mfma_f32_16x16x32_bf16 a[0:3],a[172:175],v[156:159],a[0:3]
buffer_load_dwordx4 v[176:179],v6,s[28:31],s78 offen offset:1024 sc1
v_mfma_f32_16x16x32_bf16 a[4:7],a[172:175],v[188:191],a[4:7]
v_mfma_f32_16x16x32_bf16 a[4:7],a[168:171],v[180:183],a[4:7]
v_mfma_f32_16x16x32_bf16 a[4:7],a[164:167],v[172:175],a[4:7]
v_mfma_f32_16x16x32_bf16 a[4:7],a[160:163],v[164:167],a[4:7]
buffer_load_dwordx4 v10,s[72:75],s80 offen offset:3072 lds
v_mfma_f32_16x16x32_bf16 a[8:11],a[160:163],v[196:199],a[8:11]
v_mfma_f32_16x16x32_bf16 a[8:11],a[164:167],v[204:207],a[8:11]
v_mfma_f32_16x16x32_bf16 a[8:11],a[168:171],v[212:215],a[8:11]
v_mfma_f32_16x16x32_bf16 a[8:11],a[172:175],v[220:223],a[8:11]
buffer_load_dwordx4 v[208:211],v6,s[28:31],s78 offen offset:2048 sc1
v_mfma_f32_16x16x32_bf16 a[12:15],a[172:175],v[252:255],a[12:15]
s_cmp_ge_u32 1,s71
s_cselect_b32 s76,4,2
s_lshl_b32 s76,s76,16
v_mfma_f32_16x16x32_bf16 a[12:15],a[168:171],v[244:247],a[12:15]
s_add_u32 s77,s76,4096
ds_read_b64_tr_b16 a[168:169],v1 offset:20736
ds_read_b64_tr_b16 a[170:171],v1 offset:25856
v_mfma_f32_16x16x32_bf16 a[12:15],a[164:167],v[236:239],a[12:15]
s_add_u32 m0,s82,4096
s_max_i32 s77,s77,s19
ds_read_b64_tr_b16 a[164:165],v1 offset:10496
v_mfma_f32_16x16x32_bf16 a[12:15],a[160:163],v[228:231],a[12:15]
buffer_load_dwordx4 v[240:243],v6,s[28:31],s78 offen offset:3072 sc1
v_mfma_f32_16x16x32_bf16 a[16:19],a[176:179],v[132:135],a[16:19]
s_add_u32 s78,s76,8192
ds_read_b64_tr_b16 a[160:161],v1 offset:256
ds_read_b64_tr_b16 a[162:163],v1 offset:5376
v_mfma_f32_16x16x32_bf16 a[16:19],a[180:183],v[140:143],a[16:19]
s_max_i32 s78,s78,s100
ds_read_b64_tr_b16 a[166:167],v1 offset:15616
v_mfma_f32_16x16x32_bf16 a[16:19],a[184:187],v[148:151],a[16:19]
ds_read_b64_tr_b16 a[172:173],v1 offset:30976
v_mfma_f32_16x16x32_bf16 a[16:19],a[188:191],v[156:159],a[16:19]
buffer_store_dwordx4 v[236:239],v6,s[36:39],s87 offen offset:1024 sc1
v_mfma_f32_16x16x32_bf16 a[20:23],a[188:191],v[188:191],a[20:23]
ds_read_b64_tr_b16 a[174:175],v1 offset:36096
v_add_u32 v1,40960,v1
v_mfma_f32_16x16x32_bf16 a[20:23],a[184:187],v[180:183],a[20:23]
ds_read_b64_tr_b16 a[96:97],v1 offset:128
ds_read_b64_tr_b16 a[98:99],v1 offset:5248
v_mfma_f32_16x16x32_bf16 a[20:23],a[180:183],v[172:175],a[20:23]
ds_read_b64_tr_b16 a[100:101],v1 offset:10368
v_mfma_f32_16x16x32_bf16 a[20:23],a[176:179],v[164:167],a[20:23]
buffer_load_dwordx4 v11,s[72:75],s80 offen lds
v_mfma_f32_16x16x32_bf16 a[24:27],a[176:179],v[196:199],a[24:27]
ds_read_b64_tr_b16 a[102:103],v1 offset:15488
ds_read_b64_tr_b16 a[104:105],v1 offset:20608
v_mfma_f32_16x16x32_bf16 a[24:27],a[180:183],v[204:207],a[24:27]
ds_read_b64_tr_b16 a[106:107],v1 offset:25728
v_mfma_f32_16x16x32_bf16 a[24:27],a[184:187],v[212:215],a[24:27]
ds_read_b64_tr_b16 a[108:109],v1 offset:30848
v_mfma_f32_16x16x32_bf16 a[24:27],a[188:191],v[220:223],a[24:27]
buffer_load_dwordx4 v[152:155],v6,s[28:31],s79 offen sc1
v_mfma_f32_16x16x32_bf16 a[28:31],a[188:191],v[252:255],a[28:31]
ds_read_b64_tr_b16 a[188:189],v2 offset:31008
ds_read_b64_tr_b16 a[190:191],v2 offset:36128
v_mfma_f32_16x16x32_bf16 a[28:31],a[184:187],v[244:247],a[28:31]
s_add_u32 m0,s82,5120
ds_read_b64_tr_b16 a[184:185],v2 offset:20768
)A"
R"A(v_mfma_f32_16x16x32_bf16 a[28:31],a[180:183],v[236:239],a[28:31]
ds_read_b64_tr_b16 a[180:181],v2 offset:10528
v_mfma_f32_16x16x32_bf16 a[28:31],a[176:179],v[228:231],a[28:31]
s_waitcnt lgkmcnt(8)
buffer_load_dwordx4 v[184:187],v6,s[28:31],s79 offen offset:1024 sc1
v_mfma_f32_16x16x32_bf16 a[32:35],a[128:131],v[132:135],a[32:35]
ds_read_b64_tr_b16 a[176:177],v2 offset:288
ds_read_b64_tr_b16 a[178:179],v2 offset:5408
v_mfma_f32_16x16x32_bf16 a[32:35],a[132:135],v[140:143],a[32:35]
ds_read_b64_tr_b16 a[182:183],v2 offset:15648
v_mfma_f32_16x16x32_bf16 a[32:35],a[136:139],v[148:151],a[32:35]
ds_read_b64_tr_b16 a[186:187],v2 offset:25888
v_add_u32 v2,40960,v2
v_mfma_f32_16x16x32_bf16 a[32:35],a[140:143],v[156:159],a[32:35]
buffer_load_dwordx4 v7,s[72:75],s83 offen lds
v_mfma_f32_16x16x32_bf16 a[36:39],a[140:143],v[188:191],a[36:39]
ds_read_b64_tr_b16 a[110:111],v1 offset:35968
ds_read_b64_tr_b16 a[112:113],v2 offset:160
v_mfma_f32_16x16x32_bf16 a[36:39],a[136:139],v[180:183],a[36:39]
ds_read_b64_tr_b16 a[114:115],v2 offset:5280
v_mfma_f32_16x16x32_bf16 a[36:39],a[132:135],v[172:175],a[36:39]
ds_read_b64_tr_b16 a[116:117],v2 offset:10400
v_mfma_f32_16x16x32_bf16 a[36:39],a[128:131],v[164:167],a[36:39]
buffer_load_dwordx4 v[216:219],v6,s[28:31],s79 offen offset:2048 sc1
v_mfma_f32_16x16x32_bf16 a[40:43],a[128:131],v[196:199],a[40:43]
ds_read_b64_tr_b16 a[118:119],v2 offset:15520
ds_read_b64_tr_b16 a[120:121],v2 offset:20640
v_mfma_f32_16x16x32_bf16 a[40:43],a[132:135],v[204:207],a[40:43]
ds_read_b64_tr_b16 a[122:123],v2 offset:25760
v_mfma_f32_16x16x32_bf16 a[40:43],a[136:139],v[212:215],a[40:43]
ds_read_b64_tr_b16 a[124:125],v2 offset:30880
v_mfma_f32_16x16x32_bf16 a[40:43],a[140:143],v[220:223],a[40:43]
buffer_load_dwordx4 v[248:251],v6,s[28:31],s79 offen offset:3072 sc1
v_mfma_f32_16x16x32_bf16 a[44:47],a[140:143],v[252:255],a[44:47]
s_add_u32 s79,s76,12288
s_max_i32 s76,s76,s18
ds_read_b64_tr_b16 a[140:141],v1 offset:30720
v_mfma_f32_16x16x32_bf16 a[44:47],a[136:139],v[244:247],a[44:47]
s_max_i32 s79,s79,s101
ds_read_b64_tr_b16 a[136:137],v1 offset:20480
ds_read_b64_tr_b16 a[138:139],v1 offset:25600
v_mfma_f32_16x16x32_bf16 a[44:47],a[132:135],v[236:239],a[44:47]
ds_read_b64_tr_b16 a[132:133],v1 offset:10240
v_mfma_f32_16x16x32_bf16 a[44:47],a[128:131],v[228:231],a[44:47]
buffer_load_dwordx4 v[64:67],v6,s[28:31],s76 offen sc1
v_mfma_f32_16x16x32_bf16 a[48:51],a[144:147],v[132:135],a[48:51]
ds_read_b64_tr_b16 a[128:129],v1
ds_read_b64_tr_b16 a[130:131],v1 offset:5120
v_mfma_f32_16x16x32_bf16 a[48:51],a[148:151],v[140:143],a[48:51]
ds_read_b64_tr_b16 a[134:135],v1 offset:15360
v_mfma_f32_16x16x32_bf16 a[48:51],a[152:155],v[148:151],a[48:51]
ds_read_b64_tr_b16 a[142:143],v1 offset:35840
v_mfma_f32_16x16x32_bf16 a[48:51],a[156:159],v[156:159],a[48:51]
buffer_load_dwordx4 v[68:71],v6,s[28:31],s76 offen offset:1024 sc1
v_mfma_f32_16x16x32_bf16 a[52:55],a[156:159],v[188:191],a[52:55]
ds_read_b64_tr_b16 a[126:127],v2 offset:36000
v_mfma_f32_16x16x32_bf16 a[52:55],a[152:155],v[180:183],a[52:55]
v_mfma_f32_16x16x32_bf16 a[52:55],a[148:151],v[172:175],a[52:55]
v_mfma_f32_16x16x32_bf16 a[52:55],a[144:147],v[164:167],a[52:55]
buffer_store_dwordx4 v[244:247],v6,s[36:39],s87 offen offset:2048 sc1
v_mfma_f32_16x16x32_bf16 a[56:59],a[144:147],v[196:199],a[56:59]
v_mfma_f32_16x16x32_bf16 a[56:59],a[148:151],v[204:207],a[56:59]
v_mfma_f32_16x16x32_bf16 a[56:59],a[152:155],v[212:215],a[56:59]
v_mfma_f32_16x16x32_bf16 a[56:59],a[156:159],v[220:223],a[56:59]
buffer_store_dwordx4 v[252:255],v6,s[36:39],s87 offen offset:3072 sc1
v_mfma_f32_16x16x32_bf16 a[60:63],a[156:159],v[252:255],a[60:63]
ds_read_b64_tr_b16 a[156:157],v2 offset:30752
ds_read_b64_tr_b16 a[158:159],v2 offset:35872
v_mfma_f32_16x16x32_bf16 a[60:63],a[152:155],v[244:247],a[60:63]
ds_read_b64_tr_b16 a[152:153],v2 offset:20512
v_mfma_f32_16x16x32_bf16 a[60:63],a[148:151],v[236:239],a[60:63]
ds_read_b64_tr_b16 a[148:149],v2 offset:10272
v_mfma_f32_16x16x32_bf16 a[60:63],a[144:147],v[228:231],a[60:63]
buffer_load_dwordx4 v8,s[72:75],s83 offen offset:1024 lds
v_mfma_f32_16x16x32_bf16 a[64:67],a[160:163],v[132:135],a[64:67]
ds_read_b64_tr_b16 a[144:145],v2 offset:32
ds_read_b64_tr_b16 a[146:147],v2 offset:5152
v_mfma_f32_16x16x32_bf16 a[64:67],a[164:167],v[140:143],a[64:67]
ds_read_b64_tr_b16 a[150:151],v2 offset:15392
v_mfma_f32_16x16x32_bf16 a[64:67],a[168:171],v[148:151],a[64:67]
ds_read_b64_tr_b16 a[154:155],v2 offset:25632
v_mfma_f32_16x16x32_bf16 a[64:67],a[172:175],v[156:159],a[64:67]
buffer_load_dwordx4 v[72:75],v6,s[28:31],s76 offen offset:2048 sc1
v_mfma_f32_16x16x32_bf16 a[68:71],a[172:175],v[188:191],a[68:71]
v_mfma_f32_16x16x32_bf16 a[68:71],a[168:171],v[180:183],a[68:71]
v_mfma_f32_16x16x32_bf16 a[68:71],a[164:167],v[172:175],a[68:71]
v_mfma_f32_16x16x32_bf16 a[68:71],a[160:163],v[164:167],a[68:71]
buffer_load_dwordx4 v[76:79],v6,s[28:31],s76 offen offset:3072 sc1
v_mfma_f32_16x16x32_bf16 a[72:75],a[160:163],v[196:199],a[72:75]
v_mfma_f32_16x16x32_bf16 a[72:75],a[164:167],v[204:207],a[72:75]
v_mfma_f32_16x16x32_bf16 a[72:75],a[168:171],v[212:215],a[72:75]
v_mfma_f32_16x16x32_bf16 a[72:75],a[172:175],v[220:223],a[72:75]
buffer_load_dwordx4 v9,s[72:75],s83 offen offset:2048 lds
v_mfma_f32_16x16x32_bf16 a[76:79],a[172:175],v[252:255],a[76:79]
ds_read_b64_tr_b16 a[172:173],v1 offset:30784
ds_read_b64_tr_b16 a[174:175],v1 offset:35904
v_mfma_f32_16x16x32_bf16 a[76:79],a[168:171],v[244:247],a[76:79]
ds_read_b64_tr_b16 a[168:169],v1 offset:20544
v_mfma_f32_16x16x32_bf16 a[76:79],a[164:167],v[236:239],a[76:79]
ds_read_b64_tr_b16 a[164:165],v1 offset:10304
v_mfma_f32_16x16x32_bf16 a[76:79],a[160:163],v[228:231],a[76:79]
s_waitcnt lgkmcnt(4)
buffer_load_dwordx4 v[80:83],v6,s[28:31],s77 offen sc1
v_mfma_f32_16x16x32_bf16 a[80:83],a[176:179],v[132:135],a[80:83]
ds_read_b64_tr_b16 a[160:161],v1 offset:64
ds_read_b64_tr_b16 a[162:163],v1 offset:5184
v_mfma_f32_16x16x32_bf16 a[80:83],a[180:183],v[140:143],a[80:83]
ds_read_b64_tr_b16 a[166:167],v1 offset:15424
v_mfma_f32_16x16x32_bf16 a[80:83],a[184:187],v[148:151],a[80:83]
ds_read_b64_tr_b16 a[170:171],v1 offset:25664
v_mfma_f32_16x16x32_bf16 a[80:83],a[188:191],v[156:159],a[80:83]
buffer_load_dwordx4 v[84:87],v6,s[28:31],s77 offen offset:1024 sc1
v_mfma_f32_16x16x32_bf16 a[84:87],a[188:191],v[188:191],a[84:87]
v_mfma_f32_16x16x32_bf16 a[84:87],a[184:187],v[180:183],a[84:87]
v_mfma_f32_16x16x32_bf16 a[84:87],a[180:183],v[172:175],a[84:87]
v_mfma_f32_16x16x32_bf16 a[84:87],a[176:179],v[164:167],a[84:87]
buffer_load_dwordx4 v[88:91],v6,s[28:31],s77 offen offset:2048 sc1
v_mfma_f32_16x16x32_bf16 a[88:91],a[176:179],v[196:199],a[88:91]
v_mfma_f32_16x16x32_bf16 a[88:91],a[180:183],v[204:207],a[88:91]
v_mfma_f32_16x16x32_bf16 a[88:91],a[184:187],v[212:215],a[88:91]
v_mfma_f32_16x16x32_bf16 a[88:91],a[188:191],v[220:223],a[88:91]
buffer_load_dwordx4 v10,s[72:75],s83 offen offset:3072 lds
v_mfma_f32_16x16x32_bf16 a[92:95],a[188:191],v[252:255],a[92:95]
s_add_u32 m0,s82,9216
ds_read_b64_tr_b16 a[188:189],v2 offset:30816
ds_read_b64_tr_b16 a[190:191],v2 offset:35936
v_mfma_f32_16x16x32_bf16 a[92:95],a[184:187],v[244:247],a[92:95]
ds_read_b64_tr_b16 a[184:185],v2 offset:20576
v_mfma_f32_16x16x32_bf16 a[92:95],a[180:183],v[236:239],a[92:95]
s_cmp_ge_u32 0,s71
s_cselect_b32 s80,3,1
s_mul_i32 s80,s80,163840
v_mfma_f32_16x16x32_bf16 a[92:95],a[176:179],v[228:231],a[92:95]
s_add_u32 s80,s80,s81
buffer_load_dwordx4 v[92:95],v6,s[28:31],s77 offen offset:3072 sc1
s_waitcnt vmcnt(18)
v_mfma_f32_16x16x32_bf16 a[192:195],a[128:131],v[128:131],a[192:195]
s_lshl_b32 s43,s41,2
ds_read_b64_tr_b16 a[176:177],v2 offset:96
ds_read_b64_tr_b16 a[178:179],v2 offset:5216
v_mfma_f32_16x16x32_bf16 a[192:195],a[132:135],v[160:163],a[192:195]
s_add_u32 s84,s70,1
s_add_u32 s85,s70,2
ds_read_b64_tr_b16 a[180:181],v2 offset:10336
v_mfma_f32_16x16x32_bf16 a[192:195],a[136:139],v[192:195],a[192:195]
s_add_u32 s86,s70,3
s_add_u32 s43,s43,s71
ds_read_b64_tr_b16 a[182:183],v2 offset:15456
v_mfma_f32_16x16x32_bf16 a[192:195],a[140:143],v[224:227],a[192:195]
s_and_b32 s84,s84,3
buffer_load_dwordx4 v[96:99],v6,s[28:31],s78 offen sc1
v_mfma_f32_16x16x32_bf16 a[196:199],a[140:143],v[232:235],a[196:199]
s_and_b32 s85,s85,3
s_and_b32 s86,s86,3
ds_read_b64_tr_b16 a[186:187],v2 offset:25696
v_mfma_f32_16x16x32_bf16 a[196:199],a[136:139],v[200:203],a[196:199]
s_mul_i32 s43,s43,245760
s_mul_i32 s84,s84,12
s_mul_i32 s85,s85,12
v_mfma_f32_16x16x32_bf16 a[196:199],a[132:135],v[168:171],a[196:199]
s_mul_i32 s86,s86,12
s_add_u32 s43,s43,134217728
s_mov_b64 s[32:33],s[12:13]
v_mfma_f32_16x16x32_bf16 a[196:199],a[128:131],v[136:139],a[196:199]
s_add_u32 s84,s84,0
buffer_load_dwordx4 v11,s[72:75],s83 offen lds
v_mfma_f32_16x16x32_bf16 a[200:203],a[128:131],v[144:147],a[200:203]
s_add_u32 s83,s80,20480
s_add_u32 s85,s85,4
s_add_u32 s86,s86,8
v_mfma_f32_16x16x32_bf16 a[200:203],a[132:135],v[176:179],a[200:203]
s_lshl_b32 s95,s42,2
s_add_u32 s95,s95,s40
s_lshl_b32 s95,s95,4
v_mfma_f32_16x16x32_bf16 a[200:203],a[136:139],v[208:211],a[200:203]
s_add_u32 s84,s84,s40
v_mov_b32 v18,s68
v_mov_b32 v19,s69
v_mfma_f32_16x16x32_bf16 a[200:203],a[140:143],v[240:243],a[200:203]
s_add_u32 s85,s85,s40
buffer_load_dwordx4 v[100:103],v6,s[28:31],s78 offen offset:1024 sc1
s_waitcnt vmcnt(16)
v_mfma_f32_16x16x32_bf16 a[204:207],a[140:143],v[248:251],a[204:207]
s_add_u32 s86,s86,s40
ds_read_b64_tr_b16 a[140:141],v1 offset:30912
ds_read_b64_tr_b16 a[142:143],v1 offset:36032
v_mfma_f32_16x16x32_bf16 a[204:207],a[136:139],v[216:219],a[204:207]
s_add_u32 s32,s32,s43
s_addc_u32 s33,s33,0
ds_read_b64_tr_b16 a[136:137],v1 offset:20672
v_mfma_f32_16x16x32_bf16 a[204:207],a[132:135],v[184:187],a[204:207]
s_mul_i32 s84,s84,5120
s_mul_i32 s85,s85,5120
ds_read_b64_tr_b16 a[132:133],v1 offset:10432
v_mfma_f32_16x16x32_bf16 a[204:207],a[128:131],v[152:155],a[204:207]
buffer_load_dwordx4 v[104:107],v6,s[28:31],s78 offen offset:2048 sc1
s_barrier
ds_read_b64_tr_b16 a[128:129],v1 offset:192
ds_read_b64_tr_b16 a[130:131],v1 offset:5312
v_mfma_f32_16x16x32_bf16 a[208:211],a[144:147],v[128:131],a[208:211]
s_mul_i32 s86,s86,5120
s_and_b32 s33,s33,0xffff
ds_read_b64_tr_b16 a[134:135],v1 offset:15552
v_mfma_f32_16x16x32_bf16 a[208:211],a[148:151],v[160:163],a[208:211]
s_mov_b32 s34,245760
s_mov_b32 s35,0x20000
ds_read_b64_tr_b16 a[138:139],v1 offset:25792
v_mfma_f32_16x16x32_bf16 a[208:211],a[152:155],v[192:195],a[208:211]
s_add_u32 m0,s82,40960
s_waitcnt vmcnt(13)
s_mov_b64 exec,1
buffer_store_dwordx2 v[18:19],v6,s[64:67],s95 offen offset:1024 sc1
s_mov_b64 exec,-1
v_mfma_f32_16x16x32_bf16 a[208:211],a[156:159],v[224:227],a[208:211]
s_max_i32 s84,s84,s18
s_max_i32 s85,s85,s19
s_max_i32 s86,s86,s100
v_mfma_f32_16x16x32_bf16 a[212:215],a[156:159],v[232:235],a[212:215]
s_mul_i32 s44,s41,307200
v_bfe_u32 v18,v6,6,2
v_lshrrev_b32 v19,8,v6
v_mfma_f32_16x16x32_bf16 a[212:215],a[152:155],v[200:203],a[212:215]
s_mov_b64 s[20:21],s[16:17]
v_xor_b32 v18,v18,v19
v_lshlrev_b32 v18,2,v18
v_mfma_f32_16x16x32_bf16 a[212:215],a[148:151],v[168:171],a[212:215]
s_lshl_b32 s43,s40,2
buffer_load_dwordx4 v[108:111],v6,s[28:31],s78 offen offset:3072 sc1
v_mfma_f32_16x16x32_bf16 a[212:215],a[144:147],v[136:139],a[212:215]
s_add_u32 s43,s43,s70
v_bfe_u32 v19,v6,4,2
v_or_b32 v18,v18,v19
v_mfma_f32_16x16x32_bf16 a[216:219],a[144:147],v[144:147],a[216:219]
s_mul_i32 s43,s43,20480
v_and_b32 v19,16,v6
v_mov_b32 v34,0x3f80
v_mfma_f32_16x16x32_bf16 a[216:219],a[148:151],v[176:179],a[216:219]
s_mov_b32 s22,307200
v_lshlrev_b32 v19,v19,v34
v_lshrrev_b32 v18,1,v18
v_mfma_f32_16x16x32_bf16 a[216:219],a[152:155],v[208:211],a[216:219]
s_mov_b32 s23,0x20000
buffer_load_dwordx4 v7,s[72:75],s80 offen lds
v_mfma_f32_16x16x32_bf16 a[216:219],a[156:159],v[240:243],a[216:219]
v_cmp_eq_u32 vcc,0,v18
s_nop 1
v_cndmask_b32 v34,0,v19,vcc
v_mfma_f32_16x16x32_bf16 a[220:223],a[156:159],v[248:251],a[220:223]
ds_read_b64_tr_b16 a[156:157],v2 offset:30944
ds_read_b64_tr_b16 a[158:159],v2 offset:36064
v_mfma_f32_16x16x32_bf16 a[220:223],a[152:155],v[216:219],a[220:223]
)A"
R"A(ds_read_b64_tr_b16 a[152:153],v2 offset:20704
v_add_u32 v25,4096,v6
v_mfma_f32_16x16x32_bf16 a[220:223],a[148:151],v[184:187],a[220:223]
buffer_load_dwordx4 v[112:115],v6,s[28:31],s79 offen sc1
v_mfma_f32_16x16x32_bf16 a[220:223],a[144:147],v[152:155],a[220:223]
s_waitcnt lgkmcnt(3)
ds_read_b64_tr_b16 a[144:145],v2 offset:224
ds_read_b64_tr_b16 a[146:147],v2 offset:5344
v_mfma_f32_16x16x32_bf16 a[224:227],a[160:163],v[128:131],a[224:227]
ds_read_b64_tr_b16 a[148:149],v2 offset:10464
v_mov_b32 v36,0
v_mfma_f32_16x16x32_bf16 a[224:227],a[164:167],v[160:163],a[224:227]
ds_read_b64_tr_b16 a[150:151],v2 offset:15584
v_mov_b32 v37,0
v_mfma_f32_16x16x32_bf16 a[224:227],a[168:171],v[192:195],a[224:227]
buffer_load_dwordx4 v[116:119],v6,s[28:31],s79 offen offset:1024 sc1
v_mfma_f32_16x16x32_bf16 a[224:227],a[172:175],v[224:227],a[224:227]
s_cmp_ge_u32 2,s71
s_cselect_b32 s76,6,4
s_lshl_b32 s76,s76,16
v_mfma_f32_16x16x32_bf16 a[228:231],a[172:175],v[232:235],a[228:231]
s_add_u32 s77,s76,4096
s_add_u32 s78,s76,8192
s_max_i32 s77,s77,s19
v_mfma_f32_16x16x32_bf16 a[228:231],a[168:171],v[200:203],a[228:231]
s_max_i32 s78,s78,s100
ds_read_b64_tr_b16 a[154:155],v2 offset:25824
v_mov_b32 v32,0
v_mfma_f32_16x16x32_bf16 a[228:231],a[164:167],v[168:171],a[228:231]
buffer_load_dwordx4 v[120:123],v6,s[28:31],s79 offen offset:2048 sc1
v_mfma_f32_16x16x32_bf16 a[228:231],a[160:163],v[136:139],a[228:231]
v_cmp_eq_u32 vcc,1,v18
s_nop 1
v_cndmask_b32 v35,0,v19,vcc
v_mfma_f32_16x16x32_bf16 a[232:235],a[160:163],v[144:147],a[232:235]
s_add_u32 s20,s20,s44
s_addc_u32 s21,s21,0
v_mov_b32 v33,0
v_mfma_f32_16x16x32_bf16 a[232:235],a[164:167],v[176:179],a[232:235]
s_mul_i32 s44,s71,320
v_bfe_u32 v40,v6,4,4
v_mul_u32_u24 v40,1280,v40
v_mfma_f32_16x16x32_bf16 a[232:235],a[168:171],v[208:211],a[232:235]
s_and_b32 s21,s21,0xffff
buffer_load_dwordx4 v8,s[72:75],s80 offen offset:1024 lds
v_mfma_f32_16x16x32_bf16 a[232:235],a[172:175],v[240:243],a[232:235]
s_add_u32 s43,s43,s44
v_lshrrev_b32 v41,8,v6
v_lshl_add_u32 v40,v41,3,v40
v_mfma_f32_16x16x32_bf16 a[236:239],a[172:175],v[248:251],a[236:239]
ds_read_b64_tr_b16 a[172:173],v1 offset:30976
ds_read_b64_tr_b16 a[174:175],v1 offset:36096
v_mfma_f32_16x16x32_bf16 a[236:239],a[168:171],v[216:219],a[236:239]
ds_read_b64_tr_b16 a[168:169],v1 offset:20736
v_mfma_f32_16x16x32_bf16 a[236:239],a[164:167],v[184:187],a[236:239]
buffer_load_dwordx4 v[124:127],v6,s[28:31],s79 offen offset:3072 sc1
v_mfma_f32_16x16x32_bf16 a[236:239],a[160:163],v[152:155],a[236:239]
s_add_u32 s79,s76,12288
s_max_i32 s76,s76,s18
ds_read_b64_tr_b16 a[160:161],v1 offset:256
v_mfma_f32_16x16x32_bf16 a[240:243],a[176:179],v[128:131],a[240:243]
s_max_i32 s79,s79,s101
ds_read_b64_tr_b16 a[162:163],v1 offset:5376
ds_read_b64_tr_b16 a[164:165],v1 offset:10496
v_mfma_f32_16x16x32_bf16 a[240:243],a[180:183],v[160:163],a[240:243]
ds_read_b64_tr_b16 a[166:167],v1 offset:15616
v_mfma_f32_16x16x32_bf16 a[240:243],a[184:187],v[192:195],a[240:243]
ds_read_b64_tr_b16 a[170:171],v1 offset:25856
v_add_u32 v1,40960,v1
v_mfma_f32_16x16x32_bf16 a[240:243],a[188:191],v[224:227],a[240:243]
buffer_load_dwordx4 v9,s[72:75],s80 offen offset:2048 lds
v_mfma_f32_16x16x32_bf16 a[244:247],a[188:191],v[232:235],a[244:247]
v_mfma_f32_16x16x32_bf16 a[244:247],a[184:187],v[200:203],a[244:247]
v_mfma_f32_16x16x32_bf16 a[244:247],a[180:183],v[168:171],a[244:247]
v_mfma_f32_16x16x32_bf16 a[244:247],a[176:179],v[136:139],a[244:247]
v_mfma_f32_16x16x32_bf16 a[248:251],a[176:179],v[144:147],a[248:251]
v_mfma_f32_16x16x32_bf16 a[248:251],a[180:183],v[176:179],a[248:251]
v_mfma_f32_16x16x32_bf16 a[248:251],a[184:187],v[208:211],a[248:251]
v_mfma_f32_16x16x32_bf16 a[248:251],a[188:191],v[240:243],a[248:251]
buffer_load_dwordx4 v[132:135],v6,s[28:31],s76 offen sc1
v_mfma_f32_16x16x32_bf16 a[252:255],a[188:191],v[248:251],a[252:255]
ds_read_b64_tr_b16 a[188:189],v2 offset:31008
ds_read_b64_tr_b16 a[190:191],v2 offset:36128
v_mfma_f32_16x16x32_bf16 a[252:255],a[184:187],v[216:219],a[252:255]
ds_read_b64_tr_b16 a[184:185],v2 offset:20768
v_mfma_f32_16x16x32_bf16 a[252:255],a[180:183],v[184:187],a[252:255]
ds_read_b64_tr_b16 a[180:181],v2 offset:10528
v_mfma_f32_16x16x32_bf16 a[252:255],a[176:179],v[152:155],a[252:255]
ds_read_b64_tr_b16 a[176:177],v2 offset:288
v_mfma_f32_16x16x32_bf16 a[0:3],a[96:99],v[128:131],a[0:3]
ds_read_b64_tr_b16 a[178:179],v2 offset:5408
ds_read_b64_tr_b16 a[182:183],v2 offset:15648
v_mfma_f32_16x16x32_bf16 a[0:3],a[100:103],v[160:163],a[0:3]
buffer_load_dwordx4 v[140:143],v6,s[28:31],s76 offen offset:1024 sc1
v_mfma_f32_16x16x32_bf16 a[0:3],a[104:107],v[192:195],a[0:3]
ds_read_b64_tr_b16 a[186:187],v2 offset:25888
v_add_u32 v2,40960,v2
v_mfma_f32_16x16x32_bf16 a[0:3],a[108:111],v[224:227],a[0:3]
v_mfma_f32_16x16x32_bf16 a[4:7],a[108:111],v[232:235],a[4:7]
v_mfma_f32_16x16x32_bf16 a[4:7],a[104:107],v[200:203],a[4:7]
buffer_load_dwordx4 v10,s[72:75],s80 offen offset:3072 lds
v_mfma_f32_16x16x32_bf16 a[4:7],a[100:103],v[168:171],a[4:7]
v_mfma_f32_16x16x32_bf16 a[4:7],a[96:99],v[136:139],a[4:7]
v_mfma_f32_16x16x32_bf16 a[8:11],a[96:99],v[144:147],a[8:11]
v_mfma_f32_16x16x32_bf16 a[8:11],a[100:103],v[176:179],a[8:11]
buffer_load_dwordx4 v[148:151],v6,s[28:31],s76 offen offset:2048 sc1
v_mfma_f32_16x16x32_bf16 a[8:11],a[104:107],v[208:211],a[8:11]
v_mfma_f32_16x16x32_bf16 a[8:11],a[108:111],v[240:243],a[8:11]
v_mfma_f32_16x16x32_bf16 a[12:15],a[108:111],v[248:251],a[12:15]
ds_read_b64_tr_b16 a[108:109],v1 offset:30720
ds_read_b64_tr_b16 a[110:111],v1 offset:35840
v_mfma_f32_16x16x32_bf16 a[12:15],a[104:107],v[216:219],a[12:15]
buffer_load_dwordx4 v[156:159],v6,s[28:31],s76 offen offset:3072 sc1
v_mfma_f32_16x16x32_bf16 a[12:15],a[100:103],v[184:187],a[12:15]
s_add_u32 m0,s82,45056
ds_read_b64_tr_b16 a[100:101],v1 offset:10240
ds_read_b64_tr_b16 a[102:103],v1 offset:15360
v_mfma_f32_16x16x32_bf16 a[12:15],a[96:99],v[152:155],a[12:15]
ds_read_b64_tr_b16 a[96:97],v1
v_mfma_f32_16x16x32_bf16 a[16:19],a[112:115],v[128:131],a[16:19]
ds_read_b64_tr_b16 a[98:99],v1 offset:5120
v_mfma_f32_16x16x32_bf16 a[16:19],a[116:119],v[160:163],a[16:19]
buffer_load_dwordx4 v11,s[72:75],s80 offen lds
v_mfma_f32_16x16x32_bf16 a[16:19],a[120:123],v[192:195],a[16:19]
ds_read_b64_tr_b16 a[104:105],v1 offset:20480
ds_read_b64_tr_b16 a[106:107],v1 offset:25600
v_mfma_f32_16x16x32_bf16 a[16:19],a[124:127],v[224:227],a[16:19]
v_mfma_f32_16x16x32_bf16 a[20:23],a[124:127],v[232:235],a[20:23]
v_mfma_f32_16x16x32_bf16 a[20:23],a[120:123],v[200:203],a[20:23]
buffer_load_dwordx4 v[164:167],v6,s[28:31],s77 offen sc1
v_mfma_f32_16x16x32_bf16 a[20:23],a[116:119],v[168:171],a[20:23]
v_mfma_f32_16x16x32_bf16 a[20:23],a[112:115],v[136:139],a[20:23]
v_mfma_f32_16x16x32_bf16 a[24:27],a[112:115],v[144:147],a[24:27]
v_mfma_f32_16x16x32_bf16 a[24:27],a[116:119],v[176:179],a[24:27]
buffer_load_dwordx4 v[172:175],v6,s[28:31],s77 offen offset:1024 sc1
v_mfma_f32_16x16x32_bf16 a[24:27],a[120:123],v[208:211],a[24:27]
v_mfma_f32_16x16x32_bf16 a[24:27],a[124:127],v[240:243],a[24:27]
v_mfma_f32_16x16x32_bf16 a[28:31],a[124:127],v[248:251],a[28:31]
ds_read_b64_tr_b16 a[124:125],v2 offset:30752
ds_read_b64_tr_b16 a[126:127],v2 offset:35872
v_mfma_f32_16x16x32_bf16 a[28:31],a[120:123],v[216:219],a[28:31]
s_add_u32 m0,s82,46080
ds_read_b64_tr_b16 a[120:121],v2 offset:20512
v_mfma_f32_16x16x32_bf16 a[28:31],a[116:119],v[184:187],a[28:31]
ds_read_b64_tr_b16 a[116:117],v2 offset:10272
v_mfma_f32_16x16x32_bf16 a[28:31],a[112:115],v[152:155],a[28:31]
buffer_load_dwordx4 v[180:183],v6,s[28:31],s77 offen offset:2048 sc1
v_mfma_f32_16x16x32_bf16 a[32:35],a[128:131],v[128:131],a[32:35]
ds_read_b64_tr_b16 a[112:113],v2 offset:32
ds_read_b64_tr_b16 a[114:115],v2 offset:5152
v_mfma_f32_16x16x32_bf16 a[32:35],a[132:135],v[160:163],a[32:35]
ds_read_b64_tr_b16 a[118:119],v2 offset:15392
v_mfma_f32_16x16x32_bf16 a[32:35],a[136:139],v[192:195],a[32:35]
ds_read_b64_tr_b16 a[122:123],v2 offset:25632
v_mfma_f32_16x16x32_bf16 a[32:35],a[140:143],v[224:227],a[32:35]
buffer_load_dwordx4 v7,s[72:75],s83 offen lds
v_mfma_f32_16x16x32_bf16 a[36:39],a[140:143],v[232:235],a[36:39]
v_mfma_f32_16x16x32_bf16 a[36:39],a[136:139],v[200:203],a[36:39]
v_mfma_f32_16x16x32_bf16 a[36:39],a[132:135],v[168:171],a[36:39]
v_mfma_f32_16x16x32_bf16 a[36:39],a[128:131],v[136:139],a[36:39]
buffer_load_dwordx4 v[188:191],v6,s[28:31],s77 offen offset:3072 sc1
v_mfma_f32_16x16x32_bf16 a[40:43],a[128:131],v[144:147],a[40:43]
v_mfma_f32_16x16x32_bf16 a[40:43],a[132:135],v[176:179],a[40:43]
v_mfma_f32_16x16x32_bf16 a[40:43],a[136:139],v[208:211],a[40:43]
v_mfma_f32_16x16x32_bf16 a[40:43],a[140:143],v[240:243],a[40:43]
buffer_load_dwordx4 v[196:199],v6,s[28:31],s78 offen sc1
v_mfma_f32_16x16x32_bf16 a[44:47],a[140:143],v[248:251],a[44:47]
ds_read_b64_tr_b16 a[140:141],v1 offset:30784
ds_read_b64_tr_b16 a[142:143],v1 offset:35904
v_mfma_f32_16x16x32_bf16 a[44:47],a[136:139],v[216:219],a[44:47]
ds_read_b64_tr_b16 a[136:137],v1 offset:20544
v_mfma_f32_16x16x32_bf16 a[44:47],a[132:135],v[184:187],a[44:47]
ds_read_b64_tr_b16 a[132:133],v1 offset:10304
v_mfma_f32_16x16x32_bf16 a[44:47],a[128:131],v[152:155],a[44:47]
s_waitcnt lgkmcnt(4)
buffer_load_dwordx4 v8,s[72:75],s83 offen offset:1024 lds
v_mfma_f32_16x16x32_bf16 a[48:51],a[144:147],v[128:131],a[48:51]
ds_read_b64_tr_b16 a[128:129],v1 offset:64
ds_read_b64_tr_b16 a[130:131],v1 offset:5184
v_mfma_f32_16x16x32_bf16 a[48:51],a[148:151],v[160:163],a[48:51]
ds_read_b64_tr_b16 a[134:135],v1 offset:15424
v_mfma_f32_16x16x32_bf16 a[48:51],a[152:155],v[192:195],a[48:51]
ds_read_b64_tr_b16 a[138:139],v1 offset:25664
v_mfma_f32_16x16x32_bf16 a[48:51],a[156:159],v[224:227],a[48:51]
s_lshl_b32 s94,s70,4
buffer_load_dwordx4 v[204:207],v6,s[28:31],s78 offen offset:1024 sc1
v_mfma_f32_16x16x32_bf16 a[52:55],a[156:159],v[232:235],a[52:55]
s_add_u32 s94,s94,s40
v_bfe_u32 v24,v6,4,2
v_min_u32 v24,2,v24
v_mfma_f32_16x16x32_bf16 a[52:55],a[152:155],v[200:203],a[52:55]
s_lshl_b32 s94,s94,4
v_add_u32 v24,s71,v24
v_add_u32 v24,1,v24
v_mfma_f32_16x16x32_bf16 a[52:55],a[148:151],v[168:171],a[52:55]
v_and_b32 v24,3,v24
v_lshlrev_b32 v24,6,v24
v_mfma_f32_16x16x32_bf16 a[52:55],a[144:147],v[136:139],a[52:55]
buffer_load_dwordx4 v[212:215],v6,s[28:31],s78 offen offset:2048 sc1
v_mfma_f32_16x16x32_bf16 a[56:59],a[144:147],v[144:147],a[56:59]
v_mfma_f32_16x16x32_bf16 a[56:59],a[148:151],v[176:179],a[56:59]
v_mfma_f32_16x16x32_bf16 a[56:59],a[152:155],v[208:211],a[56:59]
v_mfma_f32_16x16x32_bf16 a[56:59],a[156:159],v[240:243],a[56:59]
v_mfma_f32_16x16x32_bf16 a[60:63],a[156:159],v[248:251],a[60:63]
ds_read_b64_tr_b16 a[156:157],v2 offset:30816
ds_read_b64_tr_b16 a[158:159],v2 offset:35936
v_mfma_f32_16x16x32_bf16 a[60:63],a[152:155],v[216:219],a[60:63]
buffer_load_dwordx4 v[220:223],v6,s[28:31],s78 offen offset:3072 sc1
v_mfma_f32_16x16x32_bf16 a[60:63],a[148:151],v[184:187],a[60:63]
ds_read_b64_tr_b16 a[148:149],v2 offset:10336
ds_read_b64_tr_b16 a[150:151],v2 offset:15456
v_mfma_f32_16x16x32_bf16 a[60:63],a[144:147],v[152:155],a[60:63]
ds_read_b64_tr_b16 a[144:145],v2 offset:96
v_mfma_f32_16x16x32_bf16 a[64:67],a[160:163],v[128:131],a[64:67]
ds_read_b64_tr_b16 a[146:147],v2 offset:5216
v_mfma_f32_16x16x32_bf16 a[64:67],a[164:167],v[160:163],a[64:67]
buffer_load_dwordx4 v9,s[72:75],s83 offen offset:2048 lds
v_mfma_f32_16x16x32_bf16 a[64:67],a[168:171],v[192:195],a[64:67]
ds_read_b64_tr_b16 a[152:153],v2 offset:20576
ds_read_b64_tr_b16 a[154:155],v2 offset:25696
v_mfma_f32_16x16x32_bf16 a[64:67],a[172:175],v[224:227],a[64:67]
v_mfma_f32_16x16x32_bf16 a[68:71],a[172:175],v[232:235],a[68:71]
v_mfma_f32_16x16x32_bf16 a[68:71],a[168:171],v[200:203],a[68:71]
buffer_load_dwordx4 v[228:231],v6,s[28:31],s79 offen sc1
v_mfma_f32_16x16x32_bf16 a[68:71],a[164:167],v[168:171],a[68:71]
v_mfma_f32_16x16x32_bf16 a[68:71],a[160:163],v[136:139],a[68:71]
v_mfma_f32_16x16x32_bf16 a[72:75],a[160:163],v[144:147],a[72:75]
v_mfma_f32_16x16x32_bf16 a[72:75],a[164:167],v[176:179],a[72:75]
buffer_load_dwordx4 v[236:239],v6,s[28:31],s79 offen offset:1024 sc1
)A"
R"A(v_mfma_f32_16x16x32_bf16 a[72:75],a[168:171],v[208:211],a[72:75]
v_mfma_f32_16x16x32_bf16 a[72:75],a[172:175],v[240:243],a[72:75]
v_mfma_f32_16x16x32_bf16 a[76:79],a[172:175],v[248:251],a[76:79]
ds_read_b64_tr_b16 a[172:173],v1 offset:30848
ds_read_b64_tr_b16 a[174:175],v1 offset:35968
v_mfma_f32_16x16x32_bf16 a[76:79],a[168:171],v[216:219],a[76:79]
buffer_load_dwordx4 v10,s[72:75],s83 offen offset:3072 lds
v_mfma_f32_16x16x32_bf16 a[76:79],a[164:167],v[184:187],a[76:79]
ds_read_b64_tr_b16 a[164:165],v1 offset:10368
ds_read_b64_tr_b16 a[166:167],v1 offset:15488
v_mfma_f32_16x16x32_bf16 a[76:79],a[160:163],v[152:155],a[76:79]
ds_read_b64_tr_b16 a[160:161],v1 offset:128
v_mfma_f32_16x16x32_bf16 a[80:83],a[176:179],v[128:131],a[80:83]
ds_read_b64_tr_b16 a[162:163],v1 offset:5248
v_mfma_f32_16x16x32_bf16 a[80:83],a[180:183],v[160:163],a[80:83]
buffer_load_dwordx4 v[244:247],v6,s[28:31],s79 offen offset:2048 sc1
v_mfma_f32_16x16x32_bf16 a[80:83],a[184:187],v[192:195],a[80:83]
ds_read_b64_tr_b16 a[168:169],v1 offset:20608
ds_read_b64_tr_b16 a[170:171],v1 offset:25728
v_mfma_f32_16x16x32_bf16 a[80:83],a[188:191],v[224:227],a[80:83]
v_mfma_f32_16x16x32_bf16 a[84:87],a[188:191],v[232:235],a[84:87]
v_mfma_f32_16x16x32_bf16 a[84:87],a[184:187],v[200:203],a[84:87]
buffer_load_dwordx4 v[252:255],v6,s[28:31],s79 offen offset:3072 sc1
v_mfma_f32_16x16x32_bf16 a[84:87],a[180:183],v[168:171],a[84:87]
v_mfma_f32_16x16x32_bf16 a[84:87],a[176:179],v[136:139],a[84:87]
v_mfma_f32_16x16x32_bf16 a[88:91],a[176:179],v[144:147],a[88:91]
v_mfma_f32_16x16x32_bf16 a[88:91],a[180:183],v[176:179],a[88:91]
s_add_u32 m0,s82,50176
v_mfma_f32_16x16x32_bf16 a[88:91],a[184:187],v[208:211],a[88:91]
v_mfma_f32_16x16x32_bf16 a[88:91],a[188:191],v[240:243],a[88:91]
buffer_load_dwordx2 v[26:27],v24,s[64:67],s94 offen offset:1024 sc1
v_mfma_f32_16x16x32_bf16 a[92:95],a[188:191],v[248:251],a[92:95]
ds_read_b64_tr_b16 a[188:189],v2 offset:30880
ds_read_b64_tr_b16 a[190:191],v2 offset:36000
v_mfma_f32_16x16x32_bf16 a[92:95],a[184:187],v[216:219],a[92:95]
ds_read_b64_tr_b16 a[184:185],v2 offset:20640
v_mfma_f32_16x16x32_bf16 a[92:95],a[180:183],v[184:187],a[92:95]
ds_read_b64_tr_b16 a[180:181],v2 offset:10400
v_mfma_f32_16x16x32_bf16 a[92:95],a[176:179],v[152:155],a[92:95]
buffer_load_dwordx4 v11,s[72:75],s83 offen lds
v_mfma_f32_16x16x32_bf16 a[192:195],a[96:99],v[64:67],a[192:195]
ds_read_b64_tr_b16 a[176:177],v2 offset:160
ds_read_b64_tr_b16 a[178:179],v2 offset:5280
v_mfma_f32_16x16x32_bf16 a[192:195],a[100:103],v[68:71],a[192:195]
s_waitcnt vmcnt(16)
ds_read_b64_tr_b16 a[182:183],v2 offset:15520
v_mfma_f32_16x16x32_bf16 a[192:195],a[104:107],v[72:75],a[192:195]
ds_read_b64_tr_b16 a[186:187],v2 offset:25760
v_mfma_f32_16x16x32_bf16 a[192:195],a[108:111],v[76:79],a[192:195]
v_mfma_f32_16x16x32_bf16 a[196:199],a[108:111],v[92:95],a[196:199]
v_mfma_f32_16x16x32_bf16 a[196:199],a[104:107],v[88:91],a[196:199]
v_mfma_f32_16x16x32_bf16 a[196:199],a[100:103],v[84:87],a[196:199]
v_mfma_f32_16x16x32_bf16 a[196:199],a[96:99],v[80:83],a[196:199]
v_mfma_f32_16x16x32_bf16 a[200:203],a[96:99],v[96:99],a[200:203]
v_mfma_f32_16x16x32_bf16 a[200:203],a[100:103],v[100:103],a[200:203]
v_mfma_f32_16x16x32_bf16 a[200:203],a[104:107],v[104:107],a[200:203]
v_mfma_f32_16x16x32_bf16 a[200:203],a[108:111],v[108:111],a[200:203]
v_mfma_f32_16x16x32_bf16 a[204:207],a[108:111],v[124:127],a[204:207]
ds_read_b64_tr_b16 a[108:109],v1 offset:30912
ds_read_b64_tr_b16 a[110:111],v1 offset:36032
v_mfma_f32_16x16x32_bf16 a[204:207],a[104:107],v[120:123],a[204:207]
ds_read_b64_tr_b16 a[104:105],v1 offset:20672
v_mfma_f32_16x16x32_bf16 a[204:207],a[100:103],v[116:119],a[204:207]
ds_read_b64_tr_b16 a[100:101],v1 offset:10432
v_mfma_f32_16x16x32_bf16 a[204:207],a[96:99],v[112:115],a[204:207]
s_barrier
ds_read_b64_tr_b16 a[96:97],v1 offset:192
ds_read_b64_tr_b16 a[98:99],v1 offset:5312
v_mfma_f32_16x16x32_bf16 a[208:211],a[112:115],v[64:67],a[208:211]
ds_read_b64_tr_b16 a[102:103],v1 offset:15552
v_mfma_f32_16x16x32_bf16 a[208:211],a[116:119],v[68:71],a[208:211]
ds_read_b64_tr_b16 a[106:107],v1 offset:25792
v_mfma_f32_16x16x32_bf16 a[208:211],a[120:123],v[72:75],a[208:211]
v_mfma_f32_16x16x32_bf16 a[208:211],a[124:127],v[76:79],a[208:211]
v_mfma_f32_16x16x32_bf16 a[212:215],a[124:127],v[92:95],a[212:215]
v_mfma_f32_16x16x32_bf16 a[212:215],a[120:123],v[88:91],a[212:215]
v_mfma_f32_16x16x32_bf16 a[212:215],a[116:119],v[84:87],a[212:215]
v_mfma_f32_16x16x32_bf16 a[212:215],a[112:115],v[80:83],a[212:215]
v_mfma_f32_16x16x32_bf16 a[216:219],a[112:115],v[96:99],a[216:219]
v_mfma_f32_16x16x32_bf16 a[216:219],a[116:119],v[100:103],a[216:219]
s_cmp_ge_u32 0,s71
s_cselect_b32 s76,3,1
s_lshl_b32 s76,s76,16
v_mfma_f32_16x16x32_bf16 a[216:219],a[120:123],v[104:107],a[216:219]
s_add_u32 s77,s76,4096
s_add_u32 s78,s76,8192
s_add_u32 s79,s76,12288
v_mfma_f32_16x16x32_bf16 a[216:219],a[124:127],v[108:111],a[216:219]
s_max_i32 s76,s76,s18
s_max_i32 s77,s77,s19
s_max_i32 s78,s78,s100
v_mfma_f32_16x16x32_bf16 a[220:223],a[124:127],v[124:127],a[220:223]
s_max_i32 s79,s79,s101
ds_read_b64_tr_b16 a[124:125],v2 offset:30944
ds_read_b64_tr_b16 a[126:127],v2 offset:36064
v_mfma_f32_16x16x32_bf16 a[220:223],a[120:123],v[120:123],a[220:223]
ds_read_b64_tr_b16 a[120:121],v2 offset:20704
v_mfma_f32_16x16x32_bf16 a[220:223],a[116:119],v[116:119],a[220:223]
ds_read_b64_tr_b16 a[116:117],v2 offset:10464
v_mfma_f32_16x16x32_bf16 a[220:223],a[112:115],v[112:115],a[220:223]
s_waitcnt lgkmcnt(4)
ds_read_b64_tr_b16 a[112:113],v2 offset:224
v_mfma_f32_16x16x32_bf16 a[224:227],a[128:131],v[64:67],a[224:227]
ds_read_b64_tr_b16 a[114:115],v2 offset:5344
ds_read_b64_tr_b16 a[118:119],v2 offset:15584
v_mfma_f32_16x16x32_bf16 a[224:227],a[132:135],v[68:71],a[224:227]
ds_read_b64_tr_b16 a[122:123],v2 offset:25824
v_mfma_f32_16x16x32_bf16 a[224:227],a[136:139],v[72:75],a[224:227]
v_mfma_f32_16x16x32_bf16 a[224:227],a[140:143],v[76:79],a[224:227]
v_mfma_f32_16x16x32_bf16 a[228:231],a[140:143],v[92:95],a[228:231]
v_mfma_f32_16x16x32_bf16 a[228:231],a[136:139],v[88:91],a[228:231]
v_mfma_f32_16x16x32_bf16 a[228:231],a[132:135],v[84:87],a[228:231]
v_mfma_f32_16x16x32_bf16 a[228:231],a[128:131],v[80:83],a[228:231]
s_waitcnt vmcnt(1)
v_cmp_ne_u32 s[90:91],s68,v26
v_cmp_ne_u32 s[92:93],s69,v27
s_nop 0
s_or_b64 s[90:91],s[90:91],s[92:93]
s_cbranch_scc1 L_fr8_%=
L_fo8_%=:
v_mfma_f32_16x16x32_bf16 a[232:235],a[128:131],v[96:99],a[232:235]
v_mfma_f32_16x16x32_bf16 a[232:235],a[132:135],v[100:103],a[232:235]
v_mfma_f32_16x16x32_bf16 a[232:235],a[136:139],v[104:107],a[232:235]
v_mfma_f32_16x16x32_bf16 a[232:235],a[140:143],v[108:111],a[232:235]
v_mfma_f32_16x16x32_bf16 a[236:239],a[140:143],v[124:127],a[236:239]
ds_read_b64_tr_b16 a[140:141],v1 offset:30976
ds_read_b64_tr_b16 a[142:143],v1 offset:36096
v_mfma_f32_16x16x32_bf16 a[236:239],a[136:139],v[120:123],a[236:239]
ds_read_b64_tr_b16 a[136:137],v1 offset:20736
v_mfma_f32_16x16x32_bf16 a[236:239],a[132:135],v[116:119],a[236:239]
ds_read_b64_tr_b16 a[132:133],v1 offset:10496
v_mfma_f32_16x16x32_bf16 a[236:239],a[128:131],v[112:115],a[236:239]
ds_read_b64_tr_b16 a[128:129],v1 offset:256
v_mfma_f32_16x16x32_bf16 a[240:243],a[144:147],v[64:67],a[240:243]
ds_read_b64_tr_b16 a[130:131],v1 offset:5376
ds_read_b64_tr_b16 a[134:135],v1 offset:15616
v_mfma_f32_16x16x32_bf16 a[240:243],a[148:151],v[68:71],a[240:243]
buffer_load_dwordx4 v[128:131],v6,s[28:31],s76 offen sc1
v_mfma_f32_16x16x32_bf16 a[240:243],a[152:155],v[72:75],a[240:243]
ds_read_b64_tr_b16 a[138:139],v1 offset:25856
v_subrev_u32 v1,122880,v1
v_mfma_f32_16x16x32_bf16 a[240:243],a[156:159],v[76:79],a[240:243]
v_mfma_f32_16x16x32_bf16 a[244:247],a[156:159],v[92:95],a[244:247]
v_mfma_f32_16x16x32_bf16 a[244:247],a[152:155],v[88:91],a[244:247]
v_mfma_f32_16x16x32_bf16 a[244:247],a[148:151],v[84:87],a[244:247]
v_mfma_f32_16x16x32_bf16 a[244:247],a[144:147],v[80:83],a[244:247]
buffer_load_dwordx4 v[160:163],v6,s[28:31],s76 offen offset:1024 sc1
v_mfma_f32_16x16x32_bf16 a[248:251],a[144:147],v[96:99],a[248:251]
v_mfma_f32_16x16x32_bf16 a[248:251],a[148:151],v[100:103],a[248:251]
v_mfma_f32_16x16x32_bf16 a[248:251],a[152:155],v[104:107],a[248:251]
v_mfma_f32_16x16x32_bf16 a[248:251],a[156:159],v[108:111],a[248:251]
v_mfma_f32_16x16x32_bf16 a[252:255],a[156:159],v[124:127],a[252:255]
ds_read_b64_tr_b16 a[156:157],v2 offset:31008
ds_read_b64_tr_b16 a[158:159],v2 offset:36128
v_mfma_f32_16x16x32_bf16 a[252:255],a[152:155],v[120:123],a[252:255]
buffer_load_dwordx4 v[192:195],v6,s[28:31],s76 offen offset:2048 sc1
v_mfma_f32_16x16x32_bf16 a[252:255],a[148:151],v[116:119],a[252:255]
ds_read_b64_tr_b16 a[148:149],v2 offset:10528
ds_read_b64_tr_b16 a[150:151],v2 offset:15648
v_mfma_f32_16x16x32_bf16 a[252:255],a[144:147],v[112:115],a[252:255]
ds_read_b64_tr_b16 a[144:145],v2 offset:288
v_mfma_f32_16x16x32_bf16 a[0:3],a[160:163],v[64:67],a[0:3]
ds_read_b64_tr_b16 a[146:147],v2 offset:5408
v_mfma_f32_16x16x32_bf16 a[0:3],a[164:167],v[68:71],a[0:3]
ds_read_b64_tr_b16 a[152:153],v2 offset:20768
v_mfma_f32_16x16x32_bf16 a[0:3],a[168:171],v[72:75],a[0:3]
ds_read_b64_tr_b16 a[154:155],v2 offset:25888
v_subrev_u32 v2,122880,v2
v_mfma_f32_16x16x32_bf16 a[0:3],a[172:175],v[76:79],a[0:3]
buffer_load_dwordx4 v[224:227],v6,s[28:31],s76 offen offset:3072 sc1
v_mfma_f32_16x16x32_bf16 a[4:7],a[172:175],v[92:95],a[4:7]
v_mfma_f32_16x16x32_bf16 a[4:7],a[168:171],v[88:91],a[4:7]
v_mfma_f32_16x16x32_bf16 a[4:7],a[164:167],v[84:87],a[4:7]
v_mfma_f32_16x16x32_bf16 a[4:7],a[160:163],v[80:83],a[4:7]
v_mfma_f32_16x16x32_bf16 a[8:11],a[160:163],v[96:99],a[8:11]
v_mfma_f32_16x16x32_bf16 a[8:11],a[164:167],v[100:103],a[8:11]
buffer_load_dwordx4 v[136:139],v6,s[28:31],s77 offen sc1
v_mfma_f32_16x16x32_bf16 a[8:11],a[168:171],v[104:107],a[8:11]
v_mfma_f32_16x16x32_bf16 a[8:11],a[172:175],v[108:111],a[8:11]
v_mfma_f32_16x16x32_bf16 a[12:15],a[172:175],v[124:127],a[12:15]
ds_read_b64_tr_b16 a[172:173],v1 offset:30720
ds_read_b64_tr_b16 a[174:175],v1 offset:35840
v_mfma_f32_16x16x32_bf16 a[12:15],a[168:171],v[120:123],a[12:15]
ds_read_b64_tr_b16 a[168:169],v1 offset:20480
v_mfma_f32_16x16x32_bf16 a[12:15],a[164:167],v[116:119],a[12:15]
ds_read_b64_tr_b16 a[164:165],v1 offset:10240
v_mfma_f32_16x16x32_bf16 a[12:15],a[160:163],v[112:115],a[12:15]
buffer_load_dwordx4 v[168:171],v6,s[28:31],s77 offen offset:1024 sc1
v_mfma_f32_16x16x32_bf16 a[16:19],a[176:179],v[64:67],a[16:19]
ds_read_b64_tr_b16 a[160:161],v1
ds_read_b64_tr_b16 a[162:163],v1 offset:5120
v_mfma_f32_16x16x32_bf16 a[16:19],a[180:183],v[68:71],a[16:19]
ds_read_b64_tr_b16 a[166:167],v1 offset:15360
v_mfma_f32_16x16x32_bf16 a[16:19],a[184:187],v[72:75],a[16:19]
ds_read_b64_tr_b16 a[170:171],v1 offset:25600
v_mfma_f32_16x16x32_bf16 a[16:19],a[188:191],v[76:79],a[16:19]
v_mfma_f32_16x16x32_bf16 a[20:23],a[188:191],v[92:95],a[20:23]
v_mfma_f32_16x16x32_bf16 a[20:23],a[184:187],v[88:91],a[20:23]
buffer_load_dwordx4 v[200:203],v6,s[28:31],s77 offen offset:2048 sc1
v_mfma_f32_16x16x32_bf16 a[20:23],a[180:183],v[84:87],a[20:23]
v_mfma_f32_16x16x32_bf16 a[20:23],a[176:179],v[80:83],a[20:23]
v_mfma_f32_16x16x32_bf16 a[24:27],a[176:179],v[96:99],a[24:27]
v_mfma_f32_16x16x32_bf16 a[24:27],a[180:183],v[100:103],a[24:27]
v_mfma_f32_16x16x32_bf16 a[24:27],a[184:187],v[104:107],a[24:27]
v_mfma_f32_16x16x32_bf16 a[24:27],a[188:191],v[108:111],a[24:27]
buffer_load_dwordx4 v[232:235],v6,s[28:31],s77 offen offset:3072 sc1
v_mfma_f32_16x16x32_bf16 a[28:31],a[188:191],v[124:127],a[28:31]
ds_read_b64_tr_b16 a[188:189],v2 offset:30752
ds_read_b64_tr_b16 a[190:191],v2 offset:35872
v_mfma_f32_16x16x32_bf16 a[28:31],a[184:187],v[120:123],a[28:31]
ds_read_b64_tr_b16 a[184:185],v2 offset:20512
v_mfma_f32_16x16x32_bf16 a[28:31],a[180:183],v[116:119],a[28:31]
ds_read_b64_tr_b16 a[180:181],v2 offset:10272
v_mfma_f32_16x16x32_bf16 a[28:31],a[176:179],v[112:115],a[28:31]
ds_read_b64_tr_b16 a[176:177],v2 offset:32
v_mfma_f32_16x16x32_bf16 a[32:35],a[96:99],v[64:67],a[32:35]
ds_read_b64_tr_b16 a[178:179],v2 offset:5152
ds_read_b64_tr_b16 a[182:183],v2 offset:15392
)A"
R"A(v_mfma_f32_16x16x32_bf16 a[32:35],a[100:103],v[68:71],a[32:35]
buffer_load_dwordx4 v[144:147],v6,s[28:31],s78 offen sc1
v_mfma_f32_16x16x32_bf16 a[32:35],a[104:107],v[72:75],a[32:35]
ds_read_b64_tr_b16 a[186:187],v2 offset:25632
v_mfma_f32_16x16x32_bf16 a[32:35],a[108:111],v[76:79],a[32:35]
v_mfma_f32_16x16x32_bf16 a[36:39],a[108:111],v[92:95],a[36:39]
v_mfma_f32_16x16x32_bf16 a[36:39],a[104:107],v[88:91],a[36:39]
v_mfma_f32_16x16x32_bf16 a[36:39],a[100:103],v[84:87],a[36:39]
v_mfma_f32_16x16x32_bf16 a[36:39],a[96:99],v[80:83],a[36:39]
buffer_load_dwordx4 v[176:179],v6,s[28:31],s78 offen offset:1024 sc1
v_mfma_f32_16x16x32_bf16 a[40:43],a[96:99],v[96:99],a[40:43]
v_mfma_f32_16x16x32_bf16 a[40:43],a[100:103],v[100:103],a[40:43]
v_mfma_f32_16x16x32_bf16 a[40:43],a[104:107],v[104:107],a[40:43]
v_mfma_f32_16x16x32_bf16 a[40:43],a[108:111],v[108:111],a[40:43]
v_mfma_f32_16x16x32_bf16 a[44:47],a[108:111],v[124:127],a[44:47]
ds_read_b64_tr_b16 a[108:109],v1 offset:30784
ds_read_b64_tr_b16 a[110:111],v1 offset:35904
v_mfma_f32_16x16x32_bf16 a[44:47],a[104:107],v[120:123],a[44:47]
buffer_load_dwordx4 v[208:211],v6,s[28:31],s78 offen offset:2048 sc1
v_mfma_f32_16x16x32_bf16 a[44:47],a[100:103],v[116:119],a[44:47]
ds_read_b64_tr_b16 a[100:101],v1 offset:10304
ds_read_b64_tr_b16 a[102:103],v1 offset:15424
v_mfma_f32_16x16x32_bf16 a[44:47],a[96:99],v[112:115],a[44:47]
s_waitcnt lgkmcnt(4)
ds_read_b64_tr_b16 a[96:97],v1 offset:64
v_mfma_f32_16x16x32_bf16 a[48:51],a[112:115],v[64:67],a[48:51]
ds_read_b64_tr_b16 a[98:99],v1 offset:5184
v_mfma_f32_16x16x32_bf16 a[48:51],a[116:119],v[68:71],a[48:51]
ds_read_b64_tr_b16 a[104:105],v1 offset:20544
v_mfma_f32_16x16x32_bf16 a[48:51],a[120:123],v[72:75],a[48:51]
ds_read_b64_tr_b16 a[106:107],v1 offset:25664
v_mfma_f32_16x16x32_bf16 a[48:51],a[124:127],v[76:79],a[48:51]
buffer_load_dwordx4 v[240:243],v6,s[28:31],s78 offen offset:3072 sc1
v_mfma_f32_16x16x32_bf16 a[52:55],a[124:127],v[92:95],a[52:55]
v_mfma_f32_16x16x32_bf16 a[52:55],a[120:123],v[88:91],a[52:55]
v_mfma_f32_16x16x32_bf16 a[52:55],a[116:119],v[84:87],a[52:55]
v_mfma_f32_16x16x32_bf16 a[52:55],a[112:115],v[80:83],a[52:55]
v_mfma_f32_16x16x32_bf16 a[56:59],a[112:115],v[96:99],a[56:59]
v_mfma_f32_16x16x32_bf16 a[56:59],a[116:119],v[100:103],a[56:59]
buffer_load_dwordx4 v[152:155],v6,s[28:31],s79 offen sc1
v_mfma_f32_16x16x32_bf16 a[56:59],a[120:123],v[104:107],a[56:59]
s_cmp_ge_u32 1,s71
s_cselect_b32 s80,5,3
s_mul_i32 s80,s80,163840
v_mfma_f32_16x16x32_bf16 a[56:59],a[124:127],v[108:111],a[56:59]
s_add_u32 s80,s80,s81
s_add_u32 s83,s80,20480
v_mfma_f32_16x16x32_bf16 a[60:63],a[124:127],v[124:127],a[60:63]
ds_read_b64_tr_b16 a[124:125],v2 offset:30816
ds_read_b64_tr_b16 a[126:127],v2 offset:35936
v_mfma_f32_16x16x32_bf16 a[60:63],a[120:123],v[120:123],a[60:63]
ds_read_b64_tr_b16 a[120:121],v2 offset:20576
v_mfma_f32_16x16x32_bf16 a[60:63],a[116:119],v[116:119],a[60:63]
ds_read_b64_tr_b16 a[116:117],v2 offset:10336
v_mfma_f32_16x16x32_bf16 a[60:63],a[112:115],v[112:115],a[60:63]
buffer_load_dwordx4 v[184:187],v6,s[28:31],s79 offen offset:1024 sc1
v_mfma_f32_16x16x32_bf16 a[64:67],a[128:131],v[64:67],a[64:67]
ds_read_b64_tr_b16 a[112:113],v2 offset:96
ds_read_b64_tr_b16 a[114:115],v2 offset:5216
v_mfma_f32_16x16x32_bf16 a[64:67],a[132:135],v[68:71],a[64:67]
ds_read_b64_tr_b16 a[118:119],v2 offset:15456
v_mfma_f32_16x16x32_bf16 a[64:67],a[136:139],v[72:75],a[64:67]
ds_read_b64_tr_b16 a[122:123],v2 offset:25696
v_mfma_f32_16x16x32_bf16 a[64:67],a[140:143],v[76:79],a[64:67]
v_mfma_f32_16x16x32_bf16 a[68:71],a[140:143],v[92:95],a[68:71]
v_mfma_f32_16x16x32_bf16 a[68:71],a[136:139],v[88:91],a[68:71]
buffer_load_dwordx4 v[216:219],v6,s[28:31],s79 offen offset:2048 sc1
v_mfma_f32_16x16x32_bf16 a[68:71],a[132:135],v[84:87],a[68:71]
v_mfma_f32_16x16x32_bf16 a[68:71],a[128:131],v[80:83],a[68:71]
v_mfma_f32_16x16x32_bf16 a[72:75],a[128:131],v[96:99],a[72:75]
v_mfma_f32_16x16x32_bf16 a[72:75],a[132:135],v[100:103],a[72:75]
v_mfma_f32_16x16x32_bf16 a[72:75],a[136:139],v[104:107],a[72:75]
v_mfma_f32_16x16x32_bf16 a[72:75],a[140:143],v[108:111],a[72:75]
buffer_load_dwordx4 v[248:251],v6,s[28:31],s79 offen offset:3072 sc1
v_mfma_f32_16x16x32_bf16 a[76:79],a[140:143],v[124:127],a[76:79]
ds_read_b64_tr_b16 a[140:141],v1 offset:30848
ds_read_b64_tr_b16 a[142:143],v1 offset:35968
v_mfma_f32_16x16x32_bf16 a[76:79],a[136:139],v[120:123],a[76:79]
ds_read_b64_tr_b16 a[136:137],v1 offset:20608
v_mfma_f32_16x16x32_bf16 a[76:79],a[132:135],v[116:119],a[76:79]
s_add_u32 m0,s82,81920
ds_read_b64_tr_b16 a[132:133],v1 offset:10368
v_mfma_f32_16x16x32_bf16 a[76:79],a[128:131],v[112:115],a[76:79]
ds_read_b64_tr_b16 a[128:129],v1 offset:128
v_mfma_f32_16x16x32_bf16 a[80:83],a[144:147],v[64:67],a[80:83]
ds_read_b64_tr_b16 a[130:131],v1 offset:5248
ds_read_b64_tr_b16 a[134:135],v1 offset:15488
v_mfma_f32_16x16x32_bf16 a[80:83],a[148:151],v[68:71],a[80:83]
buffer_load_dwordx4 v7,s[72:75],s80 offen lds
v_mfma_f32_16x16x32_bf16 a[80:83],a[152:155],v[72:75],a[80:83]
ds_read_b64_tr_b16 a[138:139],v1 offset:25728
v_mfma_f32_16x16x32_bf16 a[80:83],a[156:159],v[76:79],a[80:83]
v_mfma_f32_16x16x32_bf16 a[84:87],a[156:159],v[92:95],a[84:87]
v_mfma_f32_16x16x32_bf16 a[84:87],a[152:155],v[88:91],a[84:87]
v_mfma_f32_16x16x32_bf16 a[84:87],a[148:151],v[84:87],a[84:87]
buffer_load_dwordx4 v8,s[72:75],s80 offen offset:1024 lds
v_mfma_f32_16x16x32_bf16 a[84:87],a[144:147],v[80:83],a[84:87]
v_mfma_f32_16x16x32_bf16 a[88:91],a[144:147],v[96:99],a[88:91]
v_mfma_f32_16x16x32_bf16 a[88:91],a[148:151],v[100:103],a[88:91]
v_mfma_f32_16x16x32_bf16 a[88:91],a[152:155],v[104:107],a[88:91]
v_mfma_f32_16x16x32_bf16 a[88:91],a[156:159],v[108:111],a[88:91]
buffer_load_dwordx4 v9,s[72:75],s80 offen offset:2048 lds
v_mfma_f32_16x16x32_bf16 a[92:95],a[156:159],v[124:127],a[92:95]
ds_read_b64_tr_b16 a[156:157],v2 offset:30880
ds_read_b64_tr_b16 a[158:159],v2 offset:36000
v_mfma_f32_16x16x32_bf16 a[92:95],a[152:155],v[120:123],a[92:95]
ds_read_b64_tr_b16 a[152:153],v2 offset:20640
v_mfma_f32_16x16x32_bf16 a[92:95],a[148:151],v[116:119],a[92:95]
ds_read_b64_tr_b16 a[148:149],v2 offset:10400
v_mfma_f32_16x16x32_bf16 a[92:95],a[144:147],v[112:115],a[92:95]
ds_read_b64_tr_b16 a[144:145],v2 offset:160
v_mfma_f32_16x16x32_bf16 a[192:195],a[160:163],v[132:135],a[192:195]
buffer_load_dwordx4 v10,s[72:75],s80 offen offset:3072 lds
v_mfma_f32_16x16x32_bf16 a[192:195],a[164:167],v[140:143],a[192:195]
ds_read_b64_tr_b16 a[146:147],v2 offset:5280
ds_read_b64_tr_b16 a[150:151],v2 offset:15520
v_mfma_f32_16x16x32_bf16 a[192:195],a[168:171],v[148:151],a[192:195]
s_add_u32 m0,s82,86016
ds_read_b64_tr_b16 a[154:155],v2 offset:25760
v_mfma_f32_16x16x32_bf16 a[192:195],a[172:175],v[156:159],a[192:195]
v_mfma_f32_16x16x32_bf16 a[196:199],a[172:175],v[188:191],a[196:199]
v_mfma_f32_16x16x32_bf16 a[196:199],a[168:171],v[180:183],a[196:199]
buffer_load_dwordx4 v11,s[72:75],s80 offen lds
v_mfma_f32_16x16x32_bf16 a[196:199],a[164:167],v[172:175],a[196:199]
v_mfma_f32_16x16x32_bf16 a[196:199],a[160:163],v[164:167],a[196:199]
s_add_u32 m0,s82,87040
v_mfma_f32_16x16x32_bf16 a[200:203],a[160:163],v[196:199],a[200:203]
v_mfma_f32_16x16x32_bf16 a[200:203],a[164:167],v[204:207],a[200:203]
v_mfma_f32_16x16x32_bf16 a[200:203],a[168:171],v[212:215],a[200:203]
buffer_load_dwordx4 v7,s[72:75],s83 offen lds
v_mfma_f32_16x16x32_bf16 a[200:203],a[172:175],v[220:223],a[200:203]
v_mfma_f32_16x16x32_bf16 a[204:207],a[172:175],v[252:255],a[204:207]
ds_read_b64_tr_b16 a[172:173],v1 offset:30912
ds_read_b64_tr_b16 a[174:175],v1 offset:36032
v_mfma_f32_16x16x32_bf16 a[204:207],a[168:171],v[244:247],a[204:207]
ds_read_b64_tr_b16 a[168:169],v1 offset:20672
v_mfma_f32_16x16x32_bf16 a[204:207],a[164:167],v[236:239],a[204:207]
s_waitcnt vmcnt(22)
ds_read_b64_tr_b16 a[164:165],v1 offset:10432
v_mfma_f32_16x16x32_bf16 a[204:207],a[160:163],v[228:231],a[204:207]
s_barrier
buffer_load_dwordx4 v8,s[72:75],s83 offen offset:1024 lds
v_mfma_f32_16x16x32_bf16 a[208:211],a[176:179],v[132:135],a[208:211]
s_cmp_ge_u32 1,s71
s_cselect_b32 s76,5,3
s_lshl_b32 s76,s76,16
v_mfma_f32_16x16x32_bf16 a[208:211],a[180:183],v[140:143],a[208:211]
s_add_u32 s77,s76,4096
s_add_u32 s78,s76,8192
s_add_u32 s79,s76,12288
v_mfma_f32_16x16x32_bf16 a[208:211],a[184:187],v[148:151],a[208:211]
s_max_i32 s76,s76,s18
s_max_i32 s77,s77,s19
s_max_i32 s78,s78,s100
v_mfma_f32_16x16x32_bf16 a[208:211],a[188:191],v[156:159],a[208:211]
s_max_i32 s79,s79,s101
buffer_load_dwordx4 v9,s[72:75],s83 offen offset:2048 lds
v_mfma_f32_16x16x32_bf16 a[212:215],a[188:191],v[188:191],a[212:215]
ds_read_b64_tr_b16 a[160:161],v1 offset:192
ds_read_b64_tr_b16 a[162:163],v1 offset:5312
v_mfma_f32_16x16x32_bf16 a[212:215],a[184:187],v[180:183],a[212:215]
ds_read_b64_tr_b16 a[166:167],v1 offset:15552
v_mfma_f32_16x16x32_bf16 a[212:215],a[180:183],v[172:175],a[212:215]
ds_read_b64_tr_b16 a[170:171],v1 offset:25792
v_mfma_f32_16x16x32_bf16 a[212:215],a[176:179],v[164:167],a[212:215]
v_mfma_f32_16x16x32_bf16 a[216:219],a[176:179],v[196:199],a[216:219]
buffer_load_dwordx4 v10,s[72:75],s83 offen offset:3072 lds
v_mfma_f32_16x16x32_bf16 a[216:219],a[180:183],v[204:207],a[216:219]
v_mfma_f32_16x16x32_bf16 a[216:219],a[184:187],v[212:215],a[216:219]
s_add_u32 m0,s82,91136
v_mfma_f32_16x16x32_bf16 a[216:219],a[188:191],v[220:223],a[216:219]
v_mfma_f32_16x16x32_bf16 a[220:223],a[188:191],v[252:255],a[220:223]
ds_read_b64_tr_b16 a[188:189],v2 offset:30944
ds_read_b64_tr_b16 a[190:191],v2 offset:36064
v_mfma_f32_16x16x32_bf16 a[220:223],a[184:187],v[244:247],a[220:223]
buffer_load_dwordx4 v11,s[72:75],s83 offen lds
v_mfma_f32_16x16x32_bf16 a[220:223],a[180:183],v[236:239],a[220:223]
ds_read_b64_tr_b16 a[180:181],v2 offset:10464
ds_read_b64_tr_b16 a[182:183],v2 offset:15584
v_mfma_f32_16x16x32_bf16 a[220:223],a[176:179],v[228:231],a[220:223]
s_waitcnt lgkmcnt(4)
ds_read_b64_tr_b16 a[176:177],v2 offset:224
v_mfma_f32_16x16x32_bf16 a[224:227],a[96:99],v[132:135],a[224:227]
ds_read_b64_tr_b16 a[178:179],v2 offset:5344
v_mfma_f32_16x16x32_bf16 a[224:227],a[100:103],v[140:143],a[224:227]
ds_read_b64_tr_b16 a[184:185],v2 offset:20704
v_mfma_f32_16x16x32_bf16 a[224:227],a[104:107],v[148:151],a[224:227]
ds_read_b64_tr_b16 a[186:187],v2 offset:25824
v_mfma_f32_16x16x32_bf16 a[224:227],a[108:111],v[156:159],a[224:227]
v_mfma_f32_16x16x32_bf16 a[228:231],a[108:111],v[188:191],a[228:231]
v_mfma_f32_16x16x32_bf16 a[228:231],a[104:107],v[180:183],a[228:231]
v_mfma_f32_16x16x32_bf16 a[228:231],a[100:103],v[172:175],a[228:231]
v_mfma_f32_16x16x32_bf16 a[228:231],a[96:99],v[164:167],a[228:231]
buffer_load_dwordx4 v[64:67],v6,s[28:31],s76 offen sc1
v_mfma_f32_16x16x32_bf16 a[232:235],a[96:99],v[196:199],a[232:235]
v_mfma_f32_16x16x32_bf16 a[232:235],a[100:103],v[204:207],a[232:235]
v_mfma_f32_16x16x32_bf16 a[232:235],a[104:107],v[212:215],a[232:235]
v_mfma_f32_16x16x32_bf16 a[232:235],a[108:111],v[220:223],a[232:235]
v_mfma_f32_16x16x32_bf16 a[236:239],a[108:111],v[252:255],a[236:239]
buffer_load_dwordx4 v[68:71],v6,s[28:31],s76 offen offset:1024 sc1
v_mfma_f32_16x16x32_bf16 a[236:239],a[104:107],v[244:247],a[236:239]
ds_read_b64_tr_b16 a[104:105],v1 offset:20736
ds_read_b64_tr_b16 a[106:107],v1 offset:25856
v_mfma_f32_16x16x32_bf16 a[236:239],a[100:103],v[236:239],a[236:239]
ds_read_b64_tr_b16 a[100:101],v1 offset:10496
v_mfma_f32_16x16x32_bf16 a[236:239],a[96:99],v[228:231],a[236:239]
ds_read_b64_tr_b16 a[96:97],v1 offset:256
v_mfma_f32_16x16x32_bf16 a[240:243],a[112:115],v[132:135],a[240:243]
ds_read_b64_tr_b16 a[98:99],v1 offset:5376
v_mfma_f32_16x16x32_bf16 a[240:243],a[116:119],v[140:143],a[240:243]
buffer_load_dwordx4 v[72:75],v6,s[28:31],s76 offen offset:2048 sc1
v_mfma_f32_16x16x32_bf16 a[240:243],a[120:123],v[148:151],a[240:243]
ds_read_b64_tr_b16 a[102:103],v1 offset:15616
ds_read_b64_tr_b16 a[108:109],v1 offset:30976
v_mfma_f32_16x16x32_bf16 a[240:243],a[124:127],v[156:159],a[240:243]
ds_read_b64_tr_b16 a[110:111],v1 offset:36096
v_add_u32 v1,40960,v1
v_mfma_f32_16x16x32_bf16 a[244:247],a[124:127],v[188:191],a[244:247]
)A"
R"A(v_add_u32 v42,40960,v1
v_add_u32 v44,81920,v1
v_mfma_f32_16x16x32_bf16 a[244:247],a[120:123],v[180:183],a[244:247]
v_mfma_f32_16x16x32_bf16 a[244:247],a[116:119],v[172:175],a[244:247]
buffer_load_dwordx4 v[76:79],v6,s[28:31],s76 offen offset:3072 sc1
v_mfma_f32_16x16x32_bf16 a[244:247],a[112:115],v[164:167],a[244:247]
v_mfma_f32_16x16x32_bf16 a[248:251],a[112:115],v[196:199],a[248:251]
v_mfma_f32_16x16x32_bf16 a[248:251],a[116:119],v[204:207],a[248:251]
v_mfma_f32_16x16x32_bf16 a[248:251],a[120:123],v[212:215],a[248:251]
v_mfma_f32_16x16x32_bf16 a[248:251],a[124:127],v[220:223],a[248:251]
buffer_load_dwordx4 v[80:83],v6,s[28:31],s77 offen sc1
v_mfma_f32_16x16x32_bf16 a[252:255],a[124:127],v[252:255],a[252:255]
ds_read_b64_tr_b16 a[124:125],v2 offset:31008
ds_read_b64_tr_b16 a[126:127],v2 offset:36128
v_mfma_f32_16x16x32_bf16 a[252:255],a[120:123],v[244:247],a[252:255]
ds_read_b64_tr_b16 a[120:121],v2 offset:20768
v_mfma_f32_16x16x32_bf16 a[252:255],a[116:119],v[236:239],a[252:255]
ds_read_b64_tr_b16 a[116:117],v2 offset:10528
v_mfma_f32_16x16x32_bf16 a[252:255],a[112:115],v[228:231],a[252:255]
ds_read_b64_tr_b16 a[112:113],v2 offset:288
v_mfma_f32_16x16x32_bf16 a[0:3],a[128:131],v[132:135],a[0:3]
buffer_load_dwordx4 v[84:87],v6,s[28:31],s77 offen offset:1024 sc1
v_mfma_f32_16x16x32_bf16 a[0:3],a[132:135],v[140:143],a[0:3]
ds_read_b64_tr_b16 a[114:115],v2 offset:5408
ds_read_b64_tr_b16 a[118:119],v2 offset:15648
v_mfma_f32_16x16x32_bf16 a[0:3],a[136:139],v[148:151],a[0:3]
ds_read_b64_tr_b16 a[122:123],v2 offset:25888
v_add_u32 v2,40960,v2
v_mfma_f32_16x16x32_bf16 a[0:3],a[140:143],v[156:159],a[0:3]
v_add_u32 v43,40960,v2
v_add_u32 v45,81920,v2
v_mfma_f32_16x16x32_bf16 a[4:7],a[140:143],v[188:191],a[4:7]
v_mfma_f32_16x16x32_bf16 a[4:7],a[136:139],v[180:183],a[4:7]
buffer_load_dwordx4 v[88:91],v6,s[28:31],s77 offen offset:2048 sc1
v_mfma_f32_16x16x32_bf16 a[4:7],a[132:135],v[172:175],a[4:7]
v_mfma_f32_16x16x32_bf16 a[4:7],a[128:131],v[164:167],a[4:7]
v_mfma_f32_16x16x32_bf16 a[8:11],a[128:131],v[196:199],a[8:11]
v_mfma_f32_16x16x32_bf16 a[8:11],a[132:135],v[204:207],a[8:11]
v_mfma_f32_16x16x32_bf16 a[8:11],a[136:139],v[212:215],a[8:11]
buffer_load_dwordx4 v[92:95],v6,s[28:31],s77 offen offset:3072 sc1
v_mfma_f32_16x16x32_bf16 a[8:11],a[140:143],v[220:223],a[8:11]
v_mfma_f32_16x16x32_bf16 a[12:15],a[140:143],v[252:255],a[12:15]
ds_read_b64_tr_b16 a[140:141],v1 offset:30720
ds_read_b64_tr_b16 a[142:143],v1 offset:35840
v_mfma_f32_16x16x32_bf16 a[12:15],a[136:139],v[244:247],a[12:15]
ds_read_b64_tr_b16 a[136:137],v1 offset:20480
v_mfma_f32_16x16x32_bf16 a[12:15],a[132:135],v[236:239],a[12:15]
ds_read_b64_tr_b16 a[132:133],v1 offset:10240
v_mfma_f32_16x16x32_bf16 a[12:15],a[128:131],v[228:231],a[12:15]
buffer_load_dwordx4 v[96:99],v6,s[28:31],s78 offen sc1
v_mfma_f32_16x16x32_bf16 a[16:19],a[144:147],v[132:135],a[16:19]
ds_read_b64_tr_b16 a[128:129],v1
ds_read_b64_tr_b16 a[130:131],v1 offset:5120
v_mfma_f32_16x16x32_bf16 a[16:19],a[148:151],v[140:143],a[16:19]
ds_read_b64_tr_b16 a[134:135],v1 offset:15360
v_mfma_f32_16x16x32_bf16 a[16:19],a[152:155],v[148:151],a[16:19]
ds_read_b64_tr_b16 a[138:139],v1 offset:25600
v_mfma_f32_16x16x32_bf16 a[16:19],a[156:159],v[156:159],a[16:19]
v_mfma_f32_16x16x32_bf16 a[20:23],a[156:159],v[188:191],a[20:23]
buffer_load_dwordx4 v[100:103],v6,s[28:31],s78 offen offset:1024 sc1
v_mfma_f32_16x16x32_bf16 a[20:23],a[152:155],v[180:183],a[20:23]
v_mfma_f32_16x16x32_bf16 a[20:23],a[148:151],v[172:175],a[20:23]
v_mfma_f32_16x16x32_bf16 a[20:23],a[144:147],v[164:167],a[20:23]
v_mfma_f32_16x16x32_bf16 a[24:27],a[144:147],v[196:199],a[24:27]
v_mfma_f32_16x16x32_bf16 a[24:27],a[148:151],v[204:207],a[24:27]
buffer_load_dwordx4 v[104:107],v6,s[28:31],s78 offen offset:2048 sc1
v_mfma_f32_16x16x32_bf16 a[24:27],a[152:155],v[212:215],a[24:27]
v_mfma_f32_16x16x32_bf16 a[24:27],a[156:159],v[220:223],a[24:27]
v_mfma_f32_16x16x32_bf16 a[28:31],a[156:159],v[252:255],a[28:31]
ds_read_b64_tr_b16 a[156:157],v2 offset:30752
ds_read_b64_tr_b16 a[158:159],v2 offset:35872
v_mfma_f32_16x16x32_bf16 a[28:31],a[152:155],v[244:247],a[28:31]
ds_read_b64_tr_b16 a[152:153],v2 offset:20512
v_mfma_f32_16x16x32_bf16 a[28:31],a[148:151],v[236:239],a[28:31]
buffer_load_dwordx4 v[108:111],v6,s[28:31],s78 offen offset:3072 sc1
v_mfma_f32_16x16x32_bf16 a[28:31],a[144:147],v[228:231],a[28:31]
s_cmp_ge_u32 2,s71
s_cselect_b32 s80,7,5
s_mul_i32 s80,s80,163840
v_mfma_f32_16x16x32_bf16 a[32:35],a[160:163],v[132:135],a[32:35]
s_add_u32 s80,s80,s81
s_add_u32 s83,s80,20480
ds_read_b64_tr_b16 a[144:145],v2 offset:32
v_mfma_f32_16x16x32_bf16 a[32:35],a[164:167],v[140:143],a[32:35]
ds_read_b64_tr_b16 a[146:147],v2 offset:5152
ds_read_b64_tr_b16 a[148:149],v2 offset:10272
v_mfma_f32_16x16x32_bf16 a[32:35],a[168:171],v[148:151],a[32:35]
ds_read_b64_tr_b16 a[150:151],v2 offset:15392
v_mfma_f32_16x16x32_bf16 a[32:35],a[172:175],v[156:159],a[32:35]
buffer_load_dwordx4 v[112:115],v6,s[28:31],s79 offen sc1
v_mfma_f32_16x16x32_bf16 a[36:39],a[172:175],v[188:191],a[36:39]
ds_read_b64_tr_b16 a[154:155],v2 offset:25632
v_mfma_f32_16x16x32_bf16 a[36:39],a[168:171],v[180:183],a[36:39]
v_mfma_f32_16x16x32_bf16 a[36:39],a[164:167],v[172:175],a[36:39]
v_mfma_f32_16x16x32_bf16 a[36:39],a[160:163],v[164:167],a[36:39]
v_mfma_f32_16x16x32_bf16 a[40:43],a[160:163],v[196:199],a[40:43]
buffer_load_dwordx4 v[116:119],v6,s[28:31],s79 offen offset:1024 sc1
v_mfma_f32_16x16x32_bf16 a[40:43],a[164:167],v[204:207],a[40:43]
v_mfma_f32_16x16x32_bf16 a[40:43],a[168:171],v[212:215],a[40:43]
v_mfma_f32_16x16x32_bf16 a[40:43],a[172:175],v[220:223],a[40:43]
v_mfma_f32_16x16x32_bf16 a[44:47],a[172:175],v[252:255],a[44:47]
ds_read_b64_tr_b16 a[172:173],v1 offset:30784
ds_read_b64_tr_b16 a[174:175],v1 offset:35904
v_mfma_f32_16x16x32_bf16 a[44:47],a[168:171],v[244:247],a[44:47]
buffer_load_dwordx4 v[120:123],v6,s[28:31],s79 offen offset:2048 sc1
v_mfma_f32_16x16x32_bf16 a[44:47],a[164:167],v[236:239],a[44:47]
ds_read_b64_tr_b16 a[164:165],v1 offset:10304
ds_read_b64_tr_b16 a[166:167],v1 offset:15424
v_mfma_f32_16x16x32_bf16 a[44:47],a[160:163],v[228:231],a[44:47]
s_waitcnt lgkmcnt(4)
ds_read_b64_tr_b16 a[160:161],v1 offset:64
v_mfma_f32_16x16x32_bf16 a[48:51],a[176:179],v[132:135],a[48:51]
ds_read_b64_tr_b16 a[162:163],v1 offset:5184
v_mfma_f32_16x16x32_bf16 a[48:51],a[180:183],v[140:143],a[48:51]
ds_read_b64_tr_b16 a[168:169],v1 offset:20544
v_mfma_f32_16x16x32_bf16 a[48:51],a[184:187],v[148:151],a[48:51]
buffer_load_dwordx4 v[124:127],v6,s[28:31],s79 offen offset:3072 sc1
v_mfma_f32_16x16x32_bf16 a[48:51],a[188:191],v[156:159],a[48:51]
ds_read_b64_tr_b16 a[170:171],v1 offset:25664
v_mfma_f32_16x16x32_bf16 a[52:55],a[188:191],v[188:191],a[52:55]
s_add_u32 m0,s82,122880
v_mfma_f32_16x16x32_bf16 a[52:55],a[184:187],v[180:183],a[52:55]
v_mfma_f32_16x16x32_bf16 a[52:55],a[180:183],v[172:175],a[52:55]
v_mfma_f32_16x16x32_bf16 a[52:55],a[176:179],v[164:167],a[52:55]
buffer_load_dwordx4 v7,s[72:75],s80 offen lds
v_mfma_f32_16x16x32_bf16 a[56:59],a[176:179],v[196:199],a[56:59]
v_mfma_f32_16x16x32_bf16 a[56:59],a[180:183],v[204:207],a[56:59]
v_mfma_f32_16x16x32_bf16 a[56:59],a[184:187],v[212:215],a[56:59]
v_mfma_f32_16x16x32_bf16 a[56:59],a[188:191],v[220:223],a[56:59]
v_mfma_f32_16x16x32_bf16 a[60:63],a[188:191],v[252:255],a[60:63]
buffer_load_dwordx4 v8,s[72:75],s80 offen offset:1024 lds
v_mfma_f32_16x16x32_bf16 a[60:63],a[184:187],v[244:247],a[60:63]
ds_read_b64_tr_b16 a[184:185],v2 offset:20576
ds_read_b64_tr_b16 a[186:187],v2 offset:25696
v_mfma_f32_16x16x32_bf16 a[60:63],a[180:183],v[236:239],a[60:63]
ds_read_b64_tr_b16 a[180:181],v2 offset:10336
v_mfma_f32_16x16x32_bf16 a[60:63],a[176:179],v[228:231],a[60:63]
ds_read_b64_tr_b16 a[176:177],v2 offset:96
v_mfma_f32_16x16x32_bf16 a[64:67],a[96:99],v[132:135],a[64:67]
ds_read_b64_tr_b16 a[178:179],v2 offset:5216
v_mfma_f32_16x16x32_bf16 a[64:67],a[100:103],v[140:143],a[64:67]
buffer_load_dwordx4 v9,s[72:75],s80 offen offset:2048 lds
v_mfma_f32_16x16x32_bf16 a[64:67],a[104:107],v[148:151],a[64:67]
ds_read_b64_tr_b16 a[182:183],v2 offset:15456
ds_read_b64_tr_b16 a[188:189],v2 offset:30816
v_mfma_f32_16x16x32_bf16 a[64:67],a[108:111],v[156:159],a[64:67]
ds_read_b64_tr_b16 a[190:191],v2 offset:35936
v_mfma_f32_16x16x32_bf16 a[68:71],a[108:111],v[188:191],a[68:71]
v_mfma_f32_16x16x32_bf16 a[68:71],a[104:107],v[180:183],a[68:71]
v_mfma_f32_16x16x32_bf16 a[68:71],a[100:103],v[172:175],a[68:71]
buffer_load_dwordx4 v10,s[72:75],s80 offen offset:3072 lds
v_mfma_f32_16x16x32_bf16 a[68:71],a[96:99],v[164:167],a[68:71]
v_mfma_f32_16x16x32_bf16 a[72:75],a[96:99],v[196:199],a[72:75]
s_add_u32 m0,s82,126976
v_mfma_f32_16x16x32_bf16 a[72:75],a[100:103],v[204:207],a[72:75]
v_mfma_f32_16x16x32_bf16 a[72:75],a[104:107],v[212:215],a[72:75]
s_cmp_ge_u32 2,s71
s_cselect_b32 s76,7,5
s_lshl_b32 s76,s76,16
v_mfma_f32_16x16x32_bf16 a[72:75],a[108:111],v[220:223],a[72:75]
s_add_u32 s77,s76,4096
buffer_load_dwordx4 v11,s[72:75],s80 offen lds
v_mfma_f32_16x16x32_bf16 a[76:79],a[108:111],v[252:255],a[76:79]
s_add_u32 s78,s76,8192
s_add_u32 s79,s76,12288
s_max_i32 s76,s76,s18
v_mfma_f32_16x16x32_bf16 a[76:79],a[104:107],v[244:247],a[76:79]
s_add_u32 m0,s82,128000
s_max_i32 s77,s77,s19
ds_read_b64_tr_b16 a[104:105],v1 offset:20608
v_mfma_f32_16x16x32_bf16 a[76:79],a[100:103],v[236:239],a[76:79]
s_max_i32 s78,s78,s100
ds_read_b64_tr_b16 a[100:101],v1 offset:10368
ds_read_b64_tr_b16 a[102:103],v1 offset:15488
v_mfma_f32_16x16x32_bf16 a[76:79],a[96:99],v[228:231],a[76:79]
s_max_i32 s79,s79,s101
ds_read_b64_tr_b16 a[96:97],v1 offset:128
v_mfma_f32_16x16x32_bf16 a[80:83],a[112:115],v[132:135],a[80:83]
buffer_load_dwordx4 v7,s[72:75],s83 offen lds
v_mfma_f32_16x16x32_bf16 a[80:83],a[116:119],v[140:143],a[80:83]
ds_read_b64_tr_b16 a[98:99],v1 offset:5248
ds_read_b64_tr_b16 a[106:107],v1 offset:25728
v_mfma_f32_16x16x32_bf16 a[80:83],a[120:123],v[148:151],a[80:83]
ds_read_b64_tr_b16 a[108:109],v1 offset:30848
v_mfma_f32_16x16x32_bf16 a[80:83],a[124:127],v[156:159],a[80:83]
ds_read_b64_tr_b16 a[110:111],v1 offset:35968
v_mfma_f32_16x16x32_bf16 a[84:87],a[124:127],v[188:191],a[84:87]
v_mfma_f32_16x16x32_bf16 a[84:87],a[120:123],v[180:183],a[84:87]
buffer_load_dwordx4 v8,s[72:75],s83 offen offset:1024 lds
v_mfma_f32_16x16x32_bf16 a[84:87],a[116:119],v[172:175],a[84:87]
v_mfma_f32_16x16x32_bf16 a[84:87],a[112:115],v[164:167],a[84:87]
v_mfma_f32_16x16x32_bf16 a[88:91],a[112:115],v[196:199],a[88:91]
v_mfma_f32_16x16x32_bf16 a[88:91],a[116:119],v[204:207],a[88:91]
v_mfma_f32_16x16x32_bf16 a[88:91],a[120:123],v[212:215],a[88:91]
buffer_load_dwordx4 v9,s[72:75],s83 offen offset:2048 lds
v_mfma_f32_16x16x32_bf16 a[88:91],a[124:127],v[220:223],a[88:91]
v_mfma_f32_16x16x32_bf16 a[92:95],a[124:127],v[252:255],a[92:95]
ds_read_b64_tr_b16 a[124:125],v2 offset:30880
ds_read_b64_tr_b16 a[126:127],v2 offset:36000
v_mfma_f32_16x16x32_bf16 a[92:95],a[120:123],v[244:247],a[92:95]
ds_read_b64_tr_b16 a[120:121],v2 offset:20640
v_mfma_f32_16x16x32_bf16 a[92:95],a[116:119],v[236:239],a[92:95]
ds_read_b64_tr_b16 a[116:117],v2 offset:10400
v_mfma_f32_16x16x32_bf16 a[92:95],a[112:115],v[228:231],a[92:95]
s_waitcnt vmcnt(13)
buffer_load_dwordx4 v10,s[72:75],s83 offen offset:3072 lds
v_mfma_f32_16x16x32_bf16 a[192:195],a[128:131],v[128:131],a[192:195]
ds_read_b64_tr_b16 a[112:113],v2 offset:160
ds_read_b64_tr_b16 a[114:115],v2 offset:5280
v_mfma_f32_16x16x32_bf16 a[192:195],a[132:135],v[160:163],a[192:195]
s_add_u32 m0,s82,132096
ds_read_b64_tr_b16 a[118:119],v2 offset:15520
v_mfma_f32_16x16x32_bf16 a[192:195],a[136:139],v[192:195],a[192:195]
ds_read_b64_tr_b16 a[122:123],v2 offset:25760
v_mfma_f32_16x16x32_bf16 a[192:195],a[140:143],v[224:227],a[192:195]
buffer_load_dwordx4 v[132:135],v6,s[28:31],s76 offen sc1
v_mfma_f32_16x16x32_bf16 a[196:199],a[140:143],v[232:235],a[196:199]
v_mfma_f32_16x16x32_bf16 a[196:199],a[136:139],v[200:203],a[196:199]
v_mfma_f32_16x16x32_bf16 a[196:199],a[132:135],v[168:171],a[196:199]
v_mfma_f32_16x16x32_bf16 a[196:199],a[128:131],v[136:139],a[196:199]
)A"
R"A(buffer_load_dwordx4 v11,s[72:75],s83 offen lds
v_mfma_f32_16x16x32_bf16 a[200:203],a[128:131],v[144:147],a[200:203]
ds_read_b64_tr_b16 a[128:129],v1 offset:192
ds_read_b64_tr_b16 a[130:131],v1 offset:5312
v_mfma_f32_16x16x32_bf16 a[200:203],a[132:135],v[176:179],a[200:203]
ds_read_b64_tr_b16 a[132:133],v1 offset:10432
v_mfma_f32_16x16x32_bf16 a[200:203],a[136:139],v[208:211],a[200:203]
ds_read_b64_tr_b16 a[134:135],v1 offset:15552
v_mfma_f32_16x16x32_bf16 a[200:203],a[140:143],v[240:243],a[200:203]
buffer_load_dwordx4 v[140:143],v6,s[28:31],s76 offen offset:1024 sc1
v_mfma_f32_16x16x32_bf16 a[208:211],a[144:147],v[128:131],a[208:211]
ds_read_b64_tr_b16 a[136:137],v1 offset:20672
ds_read_b64_tr_b16 a[138:139],v1 offset:25792
v_mfma_f32_16x16x32_bf16 a[208:211],a[148:151],v[160:163],a[208:211]
ds_read_b64_tr_b16 a[140:141],v1 offset:30912
v_mfma_f32_16x16x32_bf16 a[208:211],a[152:155],v[192:195],a[208:211]
ds_read_b64_tr_b16 a[142:143],v1 offset:36032
v_mfma_f32_16x16x32_bf16 a[208:211],a[156:159],v[224:227],a[208:211]
buffer_load_dwordx4 v[148:151],v6,s[28:31],s76 offen offset:2048 sc1
v_mfma_f32_16x16x32_bf16 a[212:215],a[156:159],v[232:235],a[212:215]
v_mfma_f32_16x16x32_bf16 a[212:215],a[152:155],v[200:203],a[212:215]
v_mfma_f32_16x16x32_bf16 a[212:215],a[148:151],v[168:171],a[212:215]
v_mfma_f32_16x16x32_bf16 a[212:215],a[144:147],v[136:139],a[212:215]
buffer_load_dwordx4 v[156:159],v6,s[28:31],s76 offen offset:3072 sc1
v_mfma_f32_16x16x32_bf16 a[216:219],a[144:147],v[144:147],a[216:219]
s_mul_i32 s76,s70,12
ds_read_b64_tr_b16 a[144:145],v2 offset:224
ds_read_b64_tr_b16 a[146:147],v2 offset:5344
v_mfma_f32_16x16x32_bf16 a[216:219],a[148:151],v[176:179],a[216:219]
s_add_u32 s76,s76,0
s_add_u32 s76,s76,s40
ds_read_b64_tr_b16 a[148:149],v2 offset:10464
v_mfma_f32_16x16x32_bf16 a[216:219],a[152:155],v[208:211],a[216:219]
s_mul_i32 s76,s76,5120
s_max_i32 s76,s76,s101
ds_read_b64_tr_b16 a[150:151],v2 offset:15584
v_mfma_f32_16x16x32_bf16 a[216:219],a[156:159],v[240:243],a[216:219]
s_waitcnt lgkmcnt(4)
buffer_load_dwordx4 v[164:167],v6,s[28:31],s77 offen sc1
v_mfma_f32_16x16x32_bf16 a[224:227],a[160:163],v[128:131],a[224:227]
ds_read_b64_tr_b16 a[152:153],v2 offset:20704
ds_read_b64_tr_b16 a[154:155],v2 offset:25824
v_mfma_f32_16x16x32_bf16 a[224:227],a[164:167],v[160:163],a[224:227]
ds_read_b64_tr_b16 a[156:157],v2 offset:30944
v_mfma_f32_16x16x32_bf16 a[224:227],a[168:171],v[192:195],a[224:227]
ds_read_b64_tr_b16 a[158:159],v2 offset:36064
v_mfma_f32_16x16x32_bf16 a[224:227],a[172:175],v[224:227],a[224:227]
buffer_load_dwordx4 v[172:175],v6,s[28:31],s77 offen offset:1024 sc1
v_mfma_f32_16x16x32_bf16 a[228:231],a[172:175],v[232:235],a[228:231]
v_mfma_f32_16x16x32_bf16 a[228:231],a[168:171],v[200:203],a[228:231]
v_mfma_f32_16x16x32_bf16 a[228:231],a[164:167],v[168:171],a[228:231]
v_mfma_f32_16x16x32_bf16 a[228:231],a[160:163],v[136:139],a[228:231]
v_mfma_f32_16x16x32_bf16 a[232:235],a[160:163],v[144:147],a[232:235]
buffer_load_dwordx4 v[180:183],v6,s[28:31],s77 offen offset:2048 sc1
v_mfma_f32_16x16x32_bf16 a[232:235],a[164:167],v[176:179],a[232:235]
ds_read_b64_tr_b16 a[160:161],v1 offset:256
ds_read_b64_tr_b16 a[162:163],v1 offset:5376
v_mfma_f32_16x16x32_bf16 a[232:235],a[168:171],v[208:211],a[232:235]
ds_read_b64_tr_b16 a[164:165],v1 offset:10496
v_mfma_f32_16x16x32_bf16 a[232:235],a[172:175],v[240:243],a[232:235]
ds_read_b64_tr_b16 a[166:167],v1 offset:15616
v_mfma_f32_16x16x32_bf16 a[240:243],a[176:179],v[128:131],a[240:243]
ds_read_b64_tr_b16 a[168:169],v1 offset:20736
v_mfma_f32_16x16x32_bf16 a[240:243],a[180:183],v[160:163],a[240:243]
buffer_load_dwordx4 v[188:191],v6,s[28:31],s77 offen offset:3072 sc1
v_mfma_f32_16x16x32_bf16 a[240:243],a[184:187],v[192:195],a[240:243]
s_mul_i32 s77,s70,12
ds_read_b64_tr_b16 a[170:171],v1 offset:25856
ds_read_b64_tr_b16 a[172:173],v1 offset:30976
v_mfma_f32_16x16x32_bf16 a[240:243],a[188:191],v[224:227],a[240:243]
s_add_u32 s77,s77,4
s_add_u32 s77,s77,s40
ds_read_b64_tr_b16 a[174:175],v1 offset:36096
v_mfma_f32_16x16x32_bf16 a[244:247],a[188:191],v[232:235],a[244:247]
s_mul_i32 s77,s77,5120
s_max_i32 s77,s77,s101
v_mfma_f32_16x16x32_bf16 a[244:247],a[184:187],v[200:203],a[244:247]
v_mfma_f32_16x16x32_bf16 a[244:247],a[180:183],v[168:171],a[244:247]
buffer_load_dwordx4 v[196:199],v6,s[28:31],s78 offen sc1
v_mfma_f32_16x16x32_bf16 a[244:247],a[176:179],v[136:139],a[244:247]
v_mfma_f32_16x16x32_bf16 a[248:251],a[176:179],v[144:147],a[248:251]
ds_read_b64_tr_b16 a[176:177],v2 offset:288
ds_read_b64_tr_b16 a[178:179],v2 offset:5408
v_mfma_f32_16x16x32_bf16 a[248:251],a[180:183],v[176:179],a[248:251]
ds_read_b64_tr_b16 a[180:181],v2 offset:10528
v_mfma_f32_16x16x32_bf16 a[248:251],a[184:187],v[208:211],a[248:251]
ds_read_b64_tr_b16 a[182:183],v2 offset:15648
v_mfma_f32_16x16x32_bf16 a[248:251],a[188:191],v[240:243],a[248:251]
buffer_load_dwordx4 v[204:207],v6,s[28:31],s78 offen offset:1024 sc1
v_mfma_f32_16x16x32_bf16 a[0:3],a[96:99],v[128:131],a[0:3]
ds_read_b64_tr_b16 a[184:185],v2 offset:20768
ds_read_b64_tr_b16 a[186:187],v2 offset:25888
v_mfma_f32_16x16x32_bf16 a[0:3],a[100:103],v[160:163],a[0:3]
ds_read_b64_tr_b16 a[188:189],v2 offset:31008
v_mfma_f32_16x16x32_bf16 a[0:3],a[104:107],v[192:195],a[0:3]
ds_read_b64_tr_b16 a[190:191],v2 offset:36128
v_mfma_f32_16x16x32_bf16 a[0:3],a[108:111],v[224:227],a[0:3]
v_mfma_f32_16x16x32_bf16 a[4:7],a[108:111],v[232:235],a[4:7]
buffer_load_dwordx4 v[212:215],v6,s[28:31],s78 offen offset:2048 sc1
v_mfma_f32_16x16x32_bf16 a[4:7],a[104:107],v[200:203],a[4:7]
v_mfma_f32_16x16x32_bf16 a[4:7],a[100:103],v[168:171],a[4:7]
v_mfma_f32_16x16x32_bf16 a[4:7],a[96:99],v[136:139],a[4:7]
v_mfma_f32_16x16x32_bf16 a[8:11],a[96:99],v[144:147],a[8:11]
v_mfma_f32_16x16x32_bf16 a[8:11],a[100:103],v[176:179],a[8:11]
buffer_load_dwordx4 v[220:223],v6,s[28:31],s78 offen offset:3072 sc1
v_mfma_f32_16x16x32_bf16 a[8:11],a[104:107],v[208:211],a[8:11]
s_mul_i32 s78,s70,12
s_add_u32 s78,s78,8
s_add_u32 s78,s78,s40
v_mfma_f32_16x16x32_bf16 a[8:11],a[108:111],v[240:243],a[8:11]
s_mul_i32 s78,s78,5120
s_max_i32 s78,s78,s101
v_mfma_f32_16x16x32_bf16 a[16:19],a[112:115],v[128:131],a[16:19]
v_mfma_f32_16x16x32_bf16 a[16:19],a[116:119],v[160:163],a[16:19]
v_mfma_f32_16x16x32_bf16 a[16:19],a[120:123],v[192:195],a[16:19]
buffer_load_dwordx4 v[228:231],v6,s[28:31],s79 offen sc1
v_mfma_f32_16x16x32_bf16 a[16:19],a[124:127],v[224:227],a[16:19]
v_mfma_f32_16x16x32_bf16 a[20:23],a[124:127],v[232:235],a[20:23]
v_mfma_f32_16x16x32_bf16 a[20:23],a[120:123],v[200:203],a[20:23]
v_mfma_f32_16x16x32_bf16 a[20:23],a[116:119],v[168:171],a[20:23]
v_mfma_f32_16x16x32_bf16 a[20:23],a[112:115],v[136:139],a[20:23]
buffer_load_dwordx4 v[236:239],v6,s[28:31],s79 offen offset:1024 sc1
v_mfma_f32_16x16x32_bf16 a[24:27],a[112:115],v[144:147],a[24:27]
v_mfma_f32_16x16x32_bf16 a[24:27],a[116:119],v[176:179],a[24:27]
v_mfma_f32_16x16x32_bf16 a[24:27],a[120:123],v[208:211],a[24:27]
v_mfma_f32_16x16x32_bf16 a[24:27],a[124:127],v[240:243],a[24:27]
v_mfma_f32_16x16x32_bf16 a[32:35],a[128:131],v[128:131],a[32:35]
buffer_load_dwordx4 v[244:247],v6,s[28:31],s79 offen offset:2048 sc1
v_mfma_f32_16x16x32_bf16 a[32:35],a[132:135],v[160:163],a[32:35]
v_mfma_f32_16x16x32_bf16 a[32:35],a[136:139],v[192:195],a[32:35]
v_mfma_f32_16x16x32_bf16 a[32:35],a[140:143],v[224:227],a[32:35]
v_mfma_f32_16x16x32_bf16 a[36:39],a[140:143],v[232:235],a[36:39]
v_mfma_f32_16x16x32_bf16 a[36:39],a[136:139],v[200:203],a[36:39]
buffer_load_dwordx4 v[252:255],v6,s[28:31],s79 offen offset:3072 sc1
v_mfma_f32_16x16x32_bf16 a[36:39],a[132:135],v[168:171],a[36:39]
v_mfma_f32_16x16x32_bf16 a[36:39],a[128:131],v[136:139],a[36:39]
v_mfma_f32_16x16x32_bf16 a[40:43],a[128:131],v[144:147],a[40:43]
v_mfma_f32_16x16x32_bf16 a[40:43],a[132:135],v[176:179],a[40:43]
v_mfma_f32_16x16x32_bf16 a[40:43],a[136:139],v[208:211],a[40:43]
s_waitcnt vmcnt(15)
v_mfma_f32_16x16x32_bf16 a[40:43],a[140:143],v[240:243],a[40:43]
s_barrier
ds_read_b64_tr_b16 a[96:97],v42
ds_read_b64_tr_b16 a[98:99],v42 offset:5120
s_waitcnt lgkmcnt(2)
v_mfma_f32_16x16x32_bf16 a[48:51],a[144:147],v[128:131],a[48:51]
ds_read_b64_tr_b16 a[100:101],v42 offset:10240
v_mfma_f32_16x16x32_bf16 a[48:51],a[148:151],v[160:163],a[48:51]
ds_read_b64_tr_b16 a[102:103],v42 offset:15360
v_mfma_f32_16x16x32_bf16 a[48:51],a[152:155],v[192:195],a[48:51]
ds_read_b64_tr_b16 a[104:105],v42 offset:20480
v_mfma_f32_16x16x32_bf16 a[48:51],a[156:159],v[224:227],a[48:51]
ds_read_b64_tr_b16 a[106:107],v42 offset:25600
ds_read_b64_tr_b16 a[108:109],v42 offset:30720
v_mfma_f32_16x16x32_bf16 a[52:55],a[156:159],v[232:235],a[52:55]
ds_read_b64_tr_b16 a[110:111],v42 offset:35840
v_mfma_f32_16x16x32_bf16 a[52:55],a[152:155],v[200:203],a[52:55]
ds_read_b64_tr_b16 a[112:113],v44
v_mfma_f32_16x16x32_bf16 a[52:55],a[148:151],v[168:171],a[52:55]
ds_read_b64_tr_b16 a[114:115],v44 offset:5120
v_mfma_f32_16x16x32_bf16 a[52:55],a[144:147],v[136:139],a[52:55]
ds_read_b64_tr_b16 a[116:117],v44 offset:10240
ds_read_b64_tr_b16 a[118:119],v44 offset:15360
v_mfma_f32_16x16x32_bf16 a[56:59],a[144:147],v[144:147],a[56:59]
ds_read_b64_tr_b16 a[120:121],v44 offset:20480
v_mfma_f32_16x16x32_bf16 a[56:59],a[148:151],v[176:179],a[56:59]
ds_read_b64_tr_b16 a[122:123],v44 offset:25600
v_mfma_f32_16x16x32_bf16 a[56:59],a[152:155],v[208:211],a[56:59]
ds_read_b64_tr_b16 a[124:125],v44 offset:30720
v_mfma_f32_16x16x32_bf16 a[56:59],a[156:159],v[240:243],a[56:59]
ds_read_b64_tr_b16 a[126:127],v44 offset:35840
ds_read_b64_tr_b16 a[128:129],v43 offset:32
v_mfma_f32_16x16x32_bf16 a[64:67],a[160:163],v[128:131],a[64:67]
ds_read_b64_tr_b16 a[130:131],v43 offset:5152
v_mfma_f32_16x16x32_bf16 a[64:67],a[164:167],v[160:163],a[64:67]
ds_read_b64_tr_b16 a[132:133],v43 offset:10272
v_mfma_f32_16x16x32_bf16 a[64:67],a[168:171],v[192:195],a[64:67]
ds_read_b64_tr_b16 a[134:135],v43 offset:15392
v_mfma_f32_16x16x32_bf16 a[64:67],a[172:175],v[224:227],a[64:67]
ds_read_b64_tr_b16 a[136:137],v43 offset:20512
ds_read_b64_tr_b16 a[138:139],v43 offset:25632
v_mfma_f32_16x16x32_bf16 a[68:71],a[172:175],v[232:235],a[68:71]
ds_read_b64_tr_b16 a[140:141],v43 offset:30752
v_mfma_f32_16x16x32_bf16 a[68:71],a[168:171],v[200:203],a[68:71]
ds_read_b64_tr_b16 a[142:143],v43 offset:35872
v_mfma_f32_16x16x32_bf16 a[68:71],a[164:167],v[168:171],a[68:71]
ds_read_b64_tr_b16 a[144:145],v45 offset:32
v_mfma_f32_16x16x32_bf16 a[68:71],a[160:163],v[136:139],a[68:71]
ds_read_b64_tr_b16 a[146:147],v45 offset:5152
ds_read_b64_tr_b16 a[148:149],v45 offset:10272
v_mfma_f32_16x16x32_bf16 a[72:75],a[160:163],v[144:147],a[72:75]
ds_read_b64_tr_b16 a[150:151],v45 offset:15392
v_mfma_f32_16x16x32_bf16 a[72:75],a[164:167],v[176:179],a[72:75]
ds_read_b64_tr_b16 a[152:153],v45 offset:20512
v_mfma_f32_16x16x32_bf16 a[72:75],a[168:171],v[208:211],a[72:75]
ds_read_b64_tr_b16 a[154:155],v45 offset:25632
v_mfma_f32_16x16x32_bf16 a[72:75],a[172:175],v[240:243],a[72:75]
ds_read_b64_tr_b16 a[156:157],v45 offset:30752
ds_read_b64_tr_b16 a[158:159],v45 offset:35872
v_mfma_f32_16x16x32_bf16 a[80:83],a[176:179],v[128:131],a[80:83]
ds_read_b64_tr_b16 a[160:161],v42 offset:64
v_mfma_f32_16x16x32_bf16 a[80:83],a[180:183],v[160:163],a[80:83]
ds_read_b64_tr_b16 a[162:163],v42 offset:5184
v_mfma_f32_16x16x32_bf16 a[80:83],a[184:187],v[192:195],a[80:83]
ds_read_b64_tr_b16 a[164:165],v42 offset:10304
v_mfma_f32_16x16x32_bf16 a[80:83],a[188:191],v[224:227],a[80:83]
ds_read_b64_tr_b16 a[166:167],v42 offset:15424
ds_read_b64_tr_b16 a[168:169],v42 offset:20544
v_mfma_f32_16x16x32_bf16 a[84:87],a[188:191],v[232:235],a[84:87]
ds_read_b64_tr_b16 a[170:171],v42 offset:25664
v_mfma_f32_16x16x32_bf16 a[84:87],a[184:187],v[200:203],a[84:87]
ds_read_b64_tr_b16 a[172:173],v42 offset:30784
v_mfma_f32_16x16x32_bf16 a[84:87],a[180:183],v[168:171],a[84:87]
ds_read_b64_tr_b16 a[174:175],v42 offset:35904
v_mfma_f32_16x16x32_bf16 a[84:87],a[176:179],v[136:139],a[84:87]
v_mfma_f32_16x16x32_bf16 a[88:91],a[176:179],v[144:147],a[88:91]
ds_read_b64_tr_b16 a[176:177],v44 offset:64
ds_read_b64_tr_b16 a[178:179],v44 offset:5184
)A"
R"A(v_mfma_f32_16x16x32_bf16 a[88:91],a[180:183],v[176:179],a[88:91]
ds_read_b64_tr_b16 a[180:181],v44 offset:10304
v_mfma_f32_16x16x32_bf16 a[88:91],a[184:187],v[208:211],a[88:91]
ds_read_b64_tr_b16 a[182:183],v44 offset:15424
v_mfma_f32_16x16x32_bf16 a[88:91],a[188:191],v[240:243],a[88:91]
s_waitcnt lgkmcnt(7)
ds_read_b64_tr_b16 a[184:185],v44 offset:20544
v_mfma_f32_16x16x32_bf16 a[192:195],a[96:99],v[64:67],a[192:195]
ds_read_b64_tr_b16 a[186:187],v44 offset:25664
ds_read_b64_tr_b16 a[188:189],v44 offset:30784
v_mfma_f32_16x16x32_bf16 a[192:195],a[100:103],v[68:71],a[192:195]
ds_read_b64_tr_b16 a[190:191],v44 offset:35904
v_mfma_f32_16x16x32_bf16 a[192:195],a[104:107],v[72:75],a[192:195]
v_mfma_f32_16x16x32_bf16 a[192:195],a[108:111],v[76:79],a[192:195]
v_mfma_f32_16x16x32_bf16 a[196:199],a[108:111],v[92:95],a[196:199]
v_mfma_f32_16x16x32_bf16 a[196:199],a[104:107],v[88:91],a[196:199]
v_mfma_f32_16x16x32_bf16 a[196:199],a[100:103],v[84:87],a[196:199]
v_mfma_f32_16x16x32_bf16 a[196:199],a[96:99],v[80:83],a[196:199]
v_mfma_f32_16x16x32_bf16 a[200:203],a[96:99],v[96:99],a[200:203]
ds_read_b64_tr_b16 a[96:97],v43 offset:96
ds_read_b64_tr_b16 a[98:99],v43 offset:5216
v_mfma_f32_16x16x32_bf16 a[200:203],a[100:103],v[100:103],a[200:203]
ds_read_b64_tr_b16 a[100:101],v43 offset:10336
v_mfma_f32_16x16x32_bf16 a[200:203],a[104:107],v[104:107],a[200:203]
ds_read_b64_tr_b16 a[102:103],v43 offset:15456
v_mfma_f32_16x16x32_bf16 a[200:203],a[108:111],v[108:111],a[200:203]
ds_read_b64_tr_b16 a[104:105],v43 offset:20576
v_mfma_f32_16x16x32_bf16 a[192:195],a[112:115],v[132:135],a[192:195]
s_waitcnt vmcnt(3)
ds_read_b64_tr_b16 a[106:107],v43 offset:25696
ds_read_b64_tr_b16 a[108:109],v43 offset:30816
v_mfma_f32_16x16x32_bf16 a[192:195],a[116:119],v[140:143],a[192:195]
ds_read_b64_tr_b16 a[110:111],v43 offset:35936
v_mfma_f32_16x16x32_bf16 a[192:195],a[120:123],v[148:151],a[192:195]
v_mfma_f32_16x16x32_bf16 a[192:195],a[124:127],v[156:159],a[192:195]
v_mfma_f32_16x16x32_bf16 a[196:199],a[124:127],v[188:191],a[196:199]
v_mfma_f32_16x16x32_bf16 a[196:199],a[120:123],v[180:183],a[196:199]
v_mfma_f32_16x16x32_bf16 a[196:199],a[116:119],v[172:175],a[196:199]
v_mfma_f32_16x16x32_bf16 a[196:199],a[112:115],v[164:167],a[196:199]
v_mfma_f32_16x16x32_bf16 a[200:203],a[112:115],v[196:199],a[200:203]
ds_read_b64_tr_b16 a[112:113],v45 offset:96
ds_read_b64_tr_b16 a[114:115],v45 offset:5216
v_mfma_f32_16x16x32_bf16 a[200:203],a[116:119],v[204:207],a[200:203]
v_accvgpr_read_b32 v128,a192
v_accvgpr_read_b32 v129,a193
v_mfma_f32_16x16x32_bf16 a[200:203],a[120:123],v[212:215],a[200:203]
v_cvt_pk_bf16_f32 v128,v128,v129
v_accvgpr_read_b32 v130,a194
v_mfma_f32_16x16x32_bf16 a[200:203],a[124:127],v[220:223],a[200:203]
v_accvgpr_read_b32 v131,a195
v_cvt_pk_bf16_f32 v129,v130,v131
v_mfma_f32_16x16x32_bf16 a[208:211],a[128:131],v[64:67],a[208:211]
ds_read_b64_tr_b16 a[116:117],v45 offset:10336
ds_read_b64_tr_b16 a[118:119],v45 offset:15456
v_mfma_f32_16x16x32_bf16 a[208:211],a[132:135],v[68:71],a[208:211]
v_accvgpr_read_b32 v136,a196
v_accvgpr_read_b32 v137,a197
v_mfma_f32_16x16x32_bf16 a[208:211],a[136:139],v[72:75],a[208:211]
v_cvt_pk_bf16_f32 v136,v136,v137
v_accvgpr_read_b32 v138,a198
v_mfma_f32_16x16x32_bf16 a[208:211],a[140:143],v[76:79],a[208:211]
v_accvgpr_read_b32 v139,a199
v_cvt_pk_bf16_f32 v137,v138,v139
v_mfma_f32_16x16x32_bf16 a[212:215],a[140:143],v[92:95],a[212:215]
ds_read_b64_tr_b16 a[120:121],v45 offset:20576
ds_read_b64_tr_b16 a[122:123],v45 offset:25696
v_mfma_f32_16x16x32_bf16 a[212:215],a[136:139],v[88:91],a[212:215]
ds_read_b64_tr_b16 a[124:125],v45 offset:30816
v_accvgpr_read_b32 v144,a200
v_mfma_f32_16x16x32_bf16 a[212:215],a[132:135],v[84:87],a[212:215]
ds_read_b64_tr_b16 a[126:127],v45 offset:35936
v_accvgpr_read_b32 v145,a201
v_mfma_f32_16x16x32_bf16 a[212:215],a[128:131],v[80:83],a[212:215]
v_cvt_pk_bf16_f32 v144,v144,v145
v_accvgpr_read_b32 v146,a202
v_mfma_f32_16x16x32_bf16 a[216:219],a[128:131],v[96:99],a[216:219]
v_accvgpr_read_b32 v147,a203
v_cvt_pk_bf16_f32 v145,v146,v147
v_mfma_f32_16x16x32_bf16 a[216:219],a[132:135],v[100:103],a[216:219]
ds_read_b64_tr_b16 a[128:129],v42 offset:128
ds_read_b64_tr_b16 a[130:131],v42 offset:5248
v_mfma_f32_16x16x32_bf16 a[216:219],a[136:139],v[104:107],a[216:219]
ds_read_b64_tr_b16 a[132:133],v42 offset:10368
v_mfma_f32_16x16x32_bf16 a[216:219],a[140:143],v[108:111],a[216:219]
ds_read_b64_tr_b16 a[134:135],v42 offset:15488
v_mfma_f32_16x16x32_bf16 a[208:211],a[144:147],v[132:135],a[208:211]
ds_read_b64_tr_b16 a[136:137],v42 offset:20608
v_mfma_f32_16x16x32_bf16 a[208:211],a[148:151],v[140:143],a[208:211]
ds_read_b64_tr_b16 a[138:139],v42 offset:25728
ds_read_b64_tr_b16 a[140:141],v42 offset:30848
v_mfma_f32_16x16x32_bf16 a[208:211],a[152:155],v[148:151],a[208:211]
ds_read_b64_tr_b16 a[142:143],v42 offset:35968
v_mfma_f32_16x16x32_bf16 a[208:211],a[156:159],v[156:159],a[208:211]
v_mfma_f32_16x16x32_bf16 a[212:215],a[156:159],v[188:191],a[212:215]
v_mfma_f32_16x16x32_bf16 a[212:215],a[152:155],v[180:183],a[212:215]
v_mfma_f32_16x16x32_bf16 a[212:215],a[148:151],v[172:175],a[212:215]
v_mfma_f32_16x16x32_bf16 a[212:215],a[144:147],v[164:167],a[212:215]
v_mfma_f32_16x16x32_bf16 a[216:219],a[144:147],v[196:199],a[216:219]
ds_read_b64_tr_b16 a[144:145],v44 offset:128
ds_read_b64_tr_b16 a[146:147],v44 offset:5248
v_mfma_f32_16x16x32_bf16 a[216:219],a[148:151],v[204:207],a[216:219]
v_accvgpr_read_b32 v130,a208
v_accvgpr_read_b32 v131,a209
v_mfma_f32_16x16x32_bf16 a[216:219],a[152:155],v[212:215],a[216:219]
v_accvgpr_read_b32 v12,a210
v_accvgpr_read_b32 v13,a211
v_mfma_f32_16x16x32_bf16 a[216:219],a[156:159],v[220:223],a[216:219]
v_cvt_pk_bf16_f32 v130,v130,v131
v_cvt_pk_bf16_f32 v131,v12,v13
v_mfma_f32_16x16x32_bf16 a[224:227],a[160:163],v[64:67],a[224:227]
buffer_store_dwordx4 v[128:131],v6,s[32:35],s84 offen sc1
v_mfma_f32_16x16x32_bf16 a[224:227],a[164:167],v[68:71],a[224:227]
s_waitcnt lgkmcnt(2)
v_accvgpr_read_b32 v138,a212
v_accvgpr_read_b32 v139,a213
v_mfma_f32_16x16x32_bf16 a[224:227],a[168:171],v[72:75],a[224:227]
v_accvgpr_read_b32 v14,a214
v_accvgpr_read_b32 v15,a215
v_mfma_f32_16x16x32_bf16 a[224:227],a[172:175],v[76:79],a[224:227]
v_cvt_pk_bf16_f32 v138,v138,v139
v_cvt_pk_bf16_f32 v139,v14,v15
v_mfma_f32_16x16x32_bf16 a[228:231],a[172:175],v[92:95],a[228:231]
buffer_store_dwordx4 v[136:139],v6,s[32:35],s85 offen sc1
v_mfma_f32_16x16x32_bf16 a[228:231],a[168:171],v[88:91],a[228:231]
v_accvgpr_read_b32 v146,a216
v_accvgpr_read_b32 v147,a217
v_mfma_f32_16x16x32_bf16 a[228:231],a[164:167],v[84:87],a[228:231]
v_accvgpr_read_b32 v16,a218
v_accvgpr_read_b32 v17,a219
v_mfma_f32_16x16x32_bf16 a[228:231],a[160:163],v[80:83],a[228:231]
v_cvt_pk_bf16_f32 v146,v146,v147
v_cvt_pk_bf16_f32 v147,v16,v17
v_mfma_f32_16x16x32_bf16 a[232:235],a[160:163],v[96:99],a[232:235]
buffer_store_dwordx4 v[144:147],v6,s[32:35],s86 offen sc1
v_mfma_f32_16x16x32_bf16 a[232:235],a[164:167],v[100:103],a[232:235]
ds_read_b64_tr_b16 a[148:149],v44 offset:10368
ds_read_b64_tr_b16 a[150:151],v44 offset:15488
v_mfma_f32_16x16x32_bf16 a[232:235],a[168:171],v[104:107],a[232:235]
ds_read_b64_tr_b16 a[152:153],v44 offset:20608
v_mfma_f32_16x16x32_bf16 a[232:235],a[172:175],v[108:111],a[232:235]
ds_read_b64_tr_b16 a[154:155],v44 offset:25728
v_mfma_f32_16x16x32_bf16 a[224:227],a[176:179],v[132:135],a[224:227]
ds_read_b64_tr_b16 a[156:157],v44 offset:30848
v_mfma_f32_16x16x32_bf16 a[224:227],a[180:183],v[140:143],a[224:227]
ds_read_b64_tr_b16 a[158:159],v44 offset:35968
ds_read_b64_tr_b16 a[160:161],v43 offset:160
v_mfma_f32_16x16x32_bf16 a[224:227],a[184:187],v[148:151],a[224:227]
ds_read_b64_tr_b16 a[162:163],v43 offset:5280
v_mfma_f32_16x16x32_bf16 a[224:227],a[188:191],v[156:159],a[224:227]
ds_read_b64_tr_b16 a[164:165],v43 offset:10400
v_mfma_f32_16x16x32_bf16 a[228:231],a[188:191],v[188:191],a[228:231]
ds_read_b64_tr_b16 a[166:167],v43 offset:15520
v_mfma_f32_16x16x32_bf16 a[228:231],a[184:187],v[180:183],a[228:231]
ds_read_b64_tr_b16 a[168:169],v43 offset:20640
ds_read_b64_tr_b16 a[170:171],v43 offset:25760
v_mfma_f32_16x16x32_bf16 a[228:231],a[180:183],v[172:175],a[228:231]
ds_read_b64_tr_b16 a[172:173],v43 offset:30880
v_mfma_f32_16x16x32_bf16 a[228:231],a[176:179],v[164:167],a[228:231]
ds_read_b64_tr_b16 a[174:175],v43 offset:36000
v_mfma_f32_16x16x32_bf16 a[232:235],a[176:179],v[196:199],a[232:235]
ds_read_b64_tr_b16 a[176:177],v45 offset:160
v_mfma_f32_16x16x32_bf16 a[232:235],a[180:183],v[204:207],a[232:235]
v_accvgpr_read_b32 v160,a224
v_accvgpr_read_b32 v161,a225
v_mfma_f32_16x16x32_bf16 a[232:235],a[184:187],v[212:215],a[232:235]
v_cvt_pk_bf16_f32 v160,v160,v161
v_accvgpr_read_b32 v162,a226
v_mfma_f32_16x16x32_bf16 a[232:235],a[188:191],v[220:223],a[232:235]
v_accvgpr_read_b32 v163,a227
v_cvt_pk_bf16_f32 v161,v162,v163
v_mfma_f32_16x16x32_bf16 a[240:243],a[96:99],v[64:67],a[240:243]
ds_read_b64_tr_b16 a[178:179],v45 offset:5280
ds_read_b64_tr_b16 a[180:181],v45 offset:10400
v_mfma_f32_16x16x32_bf16 a[240:243],a[100:103],v[68:71],a[240:243]
v_accvgpr_read_b32 v168,a228
v_accvgpr_read_b32 v169,a229
v_mfma_f32_16x16x32_bf16 a[240:243],a[104:107],v[72:75],a[240:243]
v_cvt_pk_bf16_f32 v168,v168,v169
v_accvgpr_read_b32 v170,a230
v_mfma_f32_16x16x32_bf16 a[240:243],a[108:111],v[76:79],a[240:243]
v_accvgpr_read_b32 v171,a231
v_cvt_pk_bf16_f32 v169,v170,v171
v_mfma_f32_16x16x32_bf16 a[244:247],a[108:111],v[92:95],a[244:247]
ds_read_b64_tr_b16 a[182:183],v45 offset:15520
ds_read_b64_tr_b16 a[184:185],v45 offset:20640
v_mfma_f32_16x16x32_bf16 a[244:247],a[104:107],v[88:91],a[244:247]
ds_read_b64_tr_b16 a[186:187],v45 offset:25760
v_accvgpr_read_b32 v176,a232
v_mfma_f32_16x16x32_bf16 a[244:247],a[100:103],v[84:87],a[244:247]
ds_read_b64_tr_b16 a[188:189],v45 offset:30880
v_accvgpr_read_b32 v177,a233
v_mfma_f32_16x16x32_bf16 a[244:247],a[96:99],v[80:83],a[244:247]
ds_read_b64_tr_b16 a[190:191],v45 offset:36000
v_cvt_pk_bf16_f32 v176,v176,v177
v_mfma_f32_16x16x32_bf16 a[248:251],a[96:99],v[96:99],a[248:251]
v_accvgpr_read_b32 v178,a234
v_accvgpr_read_b32 v179,a235
v_mfma_f32_16x16x32_bf16 a[248:251],a[100:103],v[100:103],a[248:251]
ds_read_b64_tr_b16 a[96:97],v42 offset:192
v_cvt_pk_bf16_f32 v177,v178,v179
v_mfma_f32_16x16x32_bf16 a[248:251],a[104:107],v[104:107],a[248:251]
ds_read_b64_tr_b16 a[98:99],v42 offset:5312
ds_read_b64_tr_b16 a[100:101],v42 offset:10432
v_mfma_f32_16x16x32_bf16 a[248:251],a[108:111],v[108:111],a[248:251]
ds_read_b64_tr_b16 a[102:103],v42 offset:15552
v_mfma_f32_16x16x32_bf16 a[240:243],a[112:115],v[132:135],a[240:243]
ds_read_b64_tr_b16 a[104:105],v42 offset:20672
v_mfma_f32_16x16x32_bf16 a[240:243],a[116:119],v[140:143],a[240:243]
ds_read_b64_tr_b16 a[106:107],v42 offset:25792
v_mfma_f32_16x16x32_bf16 a[240:243],a[120:123],v[148:151],a[240:243]
ds_read_b64_tr_b16 a[108:109],v42 offset:30912
ds_read_b64_tr_b16 a[110:111],v42 offset:36032
v_mfma_f32_16x16x32_bf16 a[240:243],a[124:127],v[156:159],a[240:243]
v_mfma_f32_16x16x32_bf16 a[244:247],a[124:127],v[188:191],a[244:247]
v_mfma_f32_16x16x32_bf16 a[244:247],a[120:123],v[180:183],a[244:247]
v_mfma_f32_16x16x32_bf16 a[244:247],a[116:119],v[172:175],a[244:247]
v_mfma_f32_16x16x32_bf16 a[244:247],a[112:115],v[164:167],a[244:247]
v_mfma_f32_16x16x32_bf16 a[248:251],a[112:115],v[196:199],a[248:251]
ds_read_b64_tr_b16 a[112:113],v44 offset:192
ds_read_b64_tr_b16 a[114:115],v44 offset:5312
v_mfma_f32_16x16x32_bf16 a[248:251],a[116:119],v[204:207],a[248:251]
v_accvgpr_read_b32 v162,a240
v_accvgpr_read_b32 v163,a241
v_mfma_f32_16x16x32_bf16 a[248:251],a[120:123],v[212:215],a[248:251]
v_accvgpr_read_b32 v12,a242
v_accvgpr_read_b32 v13,a243
v_mfma_f32_16x16x32_bf16 a[248:251],a[124:127],v[220:223],a[248:251]
v_cvt_pk_bf16_f32 v162,v162,v163
v_cvt_pk_bf16_f32 v163,v12,v13
v_mfma_f32_16x16x32_bf16 a[0:3],a[128:131],v[64:67],a[0:3]
buffer_store_dwordx4 v[160:163],v6,s[32:35],s84 offen offset:1024 sc1
v_mfma_f32_16x16x32_bf16 a[0:3],a[132:135],v[68:71],a[0:3]
v_accvgpr_read_b32 v170,a244
v_accvgpr_read_b32 v171,a245
v_mfma_f32_16x16x32_bf16 a[0:3],a[136:139],v[72:75],a[0:3]
)A"
R"A(v_accvgpr_read_b32 v14,a246
v_accvgpr_read_b32 v15,a247
v_mfma_f32_16x16x32_bf16 a[0:3],a[140:143],v[76:79],a[0:3]
v_cvt_pk_bf16_f32 v170,v170,v171
v_cvt_pk_bf16_f32 v171,v14,v15
v_mfma_f32_16x16x32_bf16 a[4:7],a[140:143],v[92:95],a[4:7]
buffer_store_dwordx4 v[168:171],v6,s[32:35],s85 offen offset:1024 sc1
v_mfma_f32_16x16x32_bf16 a[4:7],a[136:139],v[88:91],a[4:7]
v_accvgpr_read_b32 v178,a248
v_accvgpr_read_b32 v179,a249
v_mfma_f32_16x16x32_bf16 a[4:7],a[132:135],v[84:87],a[4:7]
v_accvgpr_read_b32 v16,a250
v_accvgpr_read_b32 v17,a251
v_mfma_f32_16x16x32_bf16 a[4:7],a[128:131],v[80:83],a[4:7]
v_cvt_pk_bf16_f32 v178,v178,v179
v_cvt_pk_bf16_f32 v179,v16,v17
v_mfma_f32_16x16x32_bf16 a[8:11],a[128:131],v[96:99],a[8:11]
buffer_store_dwordx4 v[176:179],v6,s[32:35],s86 offen offset:1024 sc1
v_mfma_f32_16x16x32_bf16 a[8:11],a[132:135],v[100:103],a[8:11]
ds_read_b64_tr_b16 a[116:117],v44 offset:10432
ds_read_b64_tr_b16 a[118:119],v44 offset:15552
v_mfma_f32_16x16x32_bf16 a[8:11],a[136:139],v[104:107],a[8:11]
ds_read_b64_tr_b16 a[120:121],v44 offset:20672
v_mfma_f32_16x16x32_bf16 a[8:11],a[140:143],v[108:111],a[8:11]
s_waitcnt lgkmcnt(3)
ds_read_b64_tr_b16 a[122:123],v44 offset:25792
v_mfma_f32_16x16x32_bf16 a[0:3],a[144:147],v[132:135],a[0:3]
ds_read_b64_tr_b16 a[124:125],v44 offset:30912
v_mfma_f32_16x16x32_bf16 a[0:3],a[148:151],v[140:143],a[0:3]
ds_read_b64_tr_b16 a[126:127],v44 offset:36032
ds_read_b64_tr_b16 a[128:129],v43 offset:224
v_mfma_f32_16x16x32_bf16 a[0:3],a[152:155],v[148:151],a[0:3]
ds_read_b64_tr_b16 a[130:131],v43 offset:5344
v_mfma_f32_16x16x32_bf16 a[0:3],a[156:159],v[156:159],a[0:3]
ds_read_b64_tr_b16 a[132:133],v43 offset:10464
v_mfma_f32_16x16x32_bf16 a[4:7],a[156:159],v[188:191],a[4:7]
ds_read_b64_tr_b16 a[134:135],v43 offset:15584
v_mfma_f32_16x16x32_bf16 a[4:7],a[152:155],v[180:183],a[4:7]
ds_read_b64_tr_b16 a[136:137],v43 offset:20704
ds_read_b64_tr_b16 a[138:139],v43 offset:25824
v_mfma_f32_16x16x32_bf16 a[4:7],a[148:151],v[172:175],a[4:7]
ds_read_b64_tr_b16 a[140:141],v43 offset:30944
v_mfma_f32_16x16x32_bf16 a[4:7],a[144:147],v[164:167],a[4:7]
ds_read_b64_tr_b16 a[142:143],v43 offset:36064
v_mfma_f32_16x16x32_bf16 a[8:11],a[144:147],v[196:199],a[8:11]
ds_read_b64_tr_b16 a[144:145],v45 offset:224
v_mfma_f32_16x16x32_bf16 a[8:11],a[148:151],v[204:207],a[8:11]
v_accvgpr_read_b32 v192,a0
v_accvgpr_read_b32 v193,a1
v_mfma_f32_16x16x32_bf16 a[8:11],a[152:155],v[212:215],a[8:11]
v_cvt_pk_bf16_f32 v192,v192,v193
v_accvgpr_read_b32 v194,a2
v_mfma_f32_16x16x32_bf16 a[8:11],a[156:159],v[220:223],a[8:11]
v_accvgpr_read_b32 v195,a3
v_cvt_pk_bf16_f32 v193,v194,v195
v_mfma_f32_16x16x32_bf16 a[16:19],a[160:163],v[64:67],a[16:19]
ds_read_b64_tr_b16 a[146:147],v45 offset:5344
ds_read_b64_tr_b16 a[148:149],v45 offset:10464
v_mfma_f32_16x16x32_bf16 a[16:19],a[164:167],v[68:71],a[16:19]
v_accvgpr_read_b32 v200,a4
v_accvgpr_read_b32 v201,a5
v_mfma_f32_16x16x32_bf16 a[16:19],a[168:171],v[72:75],a[16:19]
v_cvt_pk_bf16_f32 v200,v200,v201
v_accvgpr_read_b32 v202,a6
v_mfma_f32_16x16x32_bf16 a[16:19],a[172:175],v[76:79],a[16:19]
v_accvgpr_read_b32 v203,a7
v_cvt_pk_bf16_f32 v201,v202,v203
v_mfma_f32_16x16x32_bf16 a[20:23],a[172:175],v[92:95],a[20:23]
ds_read_b64_tr_b16 a[150:151],v45 offset:15584
ds_read_b64_tr_b16 a[152:153],v45 offset:20704
v_mfma_f32_16x16x32_bf16 a[20:23],a[168:171],v[88:91],a[20:23]
ds_read_b64_tr_b16 a[154:155],v45 offset:25824
v_accvgpr_read_b32 v208,a8
v_mfma_f32_16x16x32_bf16 a[20:23],a[164:167],v[84:87],a[20:23]
ds_read_b64_tr_b16 a[156:157],v45 offset:30944
v_accvgpr_read_b32 v209,a9
v_mfma_f32_16x16x32_bf16 a[20:23],a[160:163],v[80:83],a[20:23]
ds_read_b64_tr_b16 a[158:159],v45 offset:36064
v_cvt_pk_bf16_f32 v208,v208,v209
v_mfma_f32_16x16x32_bf16 a[24:27],a[160:163],v[96:99],a[24:27]
v_accvgpr_read_b32 v210,a10
v_accvgpr_read_b32 v211,a11
v_mfma_f32_16x16x32_bf16 a[24:27],a[164:167],v[100:103],a[24:27]
ds_read_b64_tr_b16 a[160:161],v42 offset:256
v_cvt_pk_bf16_f32 v209,v210,v211
v_mfma_f32_16x16x32_bf16 a[24:27],a[168:171],v[104:107],a[24:27]
ds_read_b64_tr_b16 a[162:163],v42 offset:5376
ds_read_b64_tr_b16 a[164:165],v42 offset:10496
v_mfma_f32_16x16x32_bf16 a[24:27],a[172:175],v[108:111],a[24:27]
ds_read_b64_tr_b16 a[166:167],v42 offset:15616
v_mfma_f32_16x16x32_bf16 a[16:19],a[176:179],v[132:135],a[16:19]
ds_read_b64_tr_b16 a[168:169],v42 offset:20736
v_mfma_f32_16x16x32_bf16 a[16:19],a[180:183],v[140:143],a[16:19]
ds_read_b64_tr_b16 a[170:171],v42 offset:25856
v_mfma_f32_16x16x32_bf16 a[16:19],a[184:187],v[148:151],a[16:19]
ds_read_b64_tr_b16 a[172:173],v42 offset:30976
ds_read_b64_tr_b16 a[174:175],v42 offset:36096
v_mfma_f32_16x16x32_bf16 a[16:19],a[188:191],v[156:159],a[16:19]
v_mfma_f32_16x16x32_bf16 a[20:23],a[188:191],v[188:191],a[20:23]
v_mfma_f32_16x16x32_bf16 a[20:23],a[184:187],v[180:183],a[20:23]
v_mfma_f32_16x16x32_bf16 a[20:23],a[180:183],v[172:175],a[20:23]
v_mfma_f32_16x16x32_bf16 a[20:23],a[176:179],v[164:167],a[20:23]
v_mfma_f32_16x16x32_bf16 a[24:27],a[176:179],v[196:199],a[24:27]
ds_read_b64_tr_b16 a[176:177],v44 offset:256
ds_read_b64_tr_b16 a[178:179],v44 offset:5376
v_mfma_f32_16x16x32_bf16 a[24:27],a[180:183],v[204:207],a[24:27]
v_accvgpr_read_b32 v194,a16
v_accvgpr_read_b32 v195,a17
v_mfma_f32_16x16x32_bf16 a[24:27],a[184:187],v[212:215],a[24:27]
v_accvgpr_read_b32 v12,a18
v_accvgpr_read_b32 v13,a19
v_mfma_f32_16x16x32_bf16 a[24:27],a[188:191],v[220:223],a[24:27]
v_cvt_pk_bf16_f32 v194,v194,v195
v_cvt_pk_bf16_f32 v195,v12,v13
v_mfma_f32_16x16x32_bf16 a[32:35],a[96:99],v[64:67],a[32:35]
buffer_store_dwordx4 v[192:195],v6,s[32:35],s84 offen offset:2048 sc1
v_mfma_f32_16x16x32_bf16 a[32:35],a[100:103],v[68:71],a[32:35]
v_accvgpr_read_b32 v202,a20
v_accvgpr_read_b32 v203,a21
v_mfma_f32_16x16x32_bf16 a[32:35],a[104:107],v[72:75],a[32:35]
v_accvgpr_read_b32 v14,a22
v_accvgpr_read_b32 v15,a23
v_mfma_f32_16x16x32_bf16 a[32:35],a[108:111],v[76:79],a[32:35]
v_cvt_pk_bf16_f32 v202,v202,v203
v_cvt_pk_bf16_f32 v203,v14,v15
v_mfma_f32_16x16x32_bf16 a[36:39],a[108:111],v[92:95],a[36:39]
buffer_store_dwordx4 v[200:203],v6,s[32:35],s85 offen offset:2048 sc1
v_mfma_f32_16x16x32_bf16 a[36:39],a[104:107],v[88:91],a[36:39]
v_accvgpr_read_b32 v210,a24
v_accvgpr_read_b32 v211,a25
v_mfma_f32_16x16x32_bf16 a[36:39],a[100:103],v[84:87],a[36:39]
v_accvgpr_read_b32 v16,a26
v_accvgpr_read_b32 v17,a27
v_mfma_f32_16x16x32_bf16 a[36:39],a[96:99],v[80:83],a[36:39]
v_cvt_pk_bf16_f32 v210,v210,v211
v_cvt_pk_bf16_f32 v211,v16,v17
v_mfma_f32_16x16x32_bf16 a[40:43],a[96:99],v[96:99],a[40:43]
buffer_store_dwordx4 v[208:211],v6,s[32:35],s86 offen offset:2048 sc1
v_mfma_f32_16x16x32_bf16 a[40:43],a[100:103],v[100:103],a[40:43]
ds_read_b64_tr_b16 a[180:181],v44 offset:10496
ds_read_b64_tr_b16 a[182:183],v44 offset:15616
v_mfma_f32_16x16x32_bf16 a[40:43],a[104:107],v[104:107],a[40:43]
ds_read_b64_tr_b16 a[184:185],v44 offset:20736
v_mfma_f32_16x16x32_bf16 a[40:43],a[108:111],v[108:111],a[40:43]
ds_read_b64_tr_b16 a[186:187],v44 offset:25856
v_mfma_f32_16x16x32_bf16 a[32:35],a[112:115],v[132:135],a[32:35]
s_waitcnt lgkmcnt(4)
ds_read_b64_tr_b16 a[188:189],v44 offset:30976
v_mfma_f32_16x16x32_bf16 a[32:35],a[116:119],v[140:143],a[32:35]
ds_read_b64_tr_b16 a[190:191],v44 offset:36096
ds_read_b64_tr_b16 a[96:97],v43 offset:288
v_mfma_f32_16x16x32_bf16 a[32:35],a[120:123],v[148:151],a[32:35]
ds_read_b64_tr_b16 a[98:99],v43 offset:5408
v_mfma_f32_16x16x32_bf16 a[32:35],a[124:127],v[156:159],a[32:35]
ds_read_b64_tr_b16 a[100:101],v43 offset:10528
v_mfma_f32_16x16x32_bf16 a[36:39],a[124:127],v[188:191],a[36:39]
ds_read_b64_tr_b16 a[102:103],v43 offset:15648
v_mfma_f32_16x16x32_bf16 a[36:39],a[120:123],v[180:183],a[36:39]
ds_read_b64_tr_b16 a[104:105],v43 offset:20768
ds_read_b64_tr_b16 a[106:107],v43 offset:25888
v_mfma_f32_16x16x32_bf16 a[36:39],a[116:119],v[172:175],a[36:39]
ds_read_b64_tr_b16 a[108:109],v43 offset:31008
v_mfma_f32_16x16x32_bf16 a[36:39],a[112:115],v[164:167],a[36:39]
ds_read_b64_tr_b16 a[110:111],v43 offset:36128
v_mfma_f32_16x16x32_bf16 a[40:43],a[112:115],v[196:199],a[40:43]
ds_read_b64_tr_b16 a[112:113],v45 offset:288
v_mfma_f32_16x16x32_bf16 a[40:43],a[116:119],v[204:207],a[40:43]
v_accvgpr_read_b32 v224,a32
v_accvgpr_read_b32 v225,a33
v_mfma_f32_16x16x32_bf16 a[40:43],a[120:123],v[212:215],a[40:43]
v_cvt_pk_bf16_f32 v224,v224,v225
v_accvgpr_read_b32 v226,a34
v_mfma_f32_16x16x32_bf16 a[40:43],a[124:127],v[220:223],a[40:43]
v_accvgpr_read_b32 v227,a35
v_cvt_pk_bf16_f32 v225,v226,v227
v_mfma_f32_16x16x32_bf16 a[48:51],a[128:131],v[64:67],a[48:51]
ds_read_b64_tr_b16 a[114:115],v45 offset:5408
ds_read_b64_tr_b16 a[116:117],v45 offset:10528
v_mfma_f32_16x16x32_bf16 a[48:51],a[132:135],v[68:71],a[48:51]
v_accvgpr_read_b32 v232,a36
v_accvgpr_read_b32 v233,a37
v_mfma_f32_16x16x32_bf16 a[48:51],a[136:139],v[72:75],a[48:51]
v_cvt_pk_bf16_f32 v232,v232,v233
v_accvgpr_read_b32 v234,a38
v_mfma_f32_16x16x32_bf16 a[48:51],a[140:143],v[76:79],a[48:51]
v_accvgpr_read_b32 v235,a39
v_cvt_pk_bf16_f32 v233,v234,v235
v_mfma_f32_16x16x32_bf16 a[52:55],a[140:143],v[92:95],a[52:55]
ds_read_b64_tr_b16 a[118:119],v45 offset:15648
ds_read_b64_tr_b16 a[120:121],v45 offset:20768
v_mfma_f32_16x16x32_bf16 a[52:55],a[136:139],v[88:91],a[52:55]
v_accvgpr_read_b32 v240,a40
v_accvgpr_read_b32 v241,a41
v_mfma_f32_16x16x32_bf16 a[52:55],a[132:135],v[84:87],a[52:55]
v_cvt_pk_bf16_f32 v240,v240,v241
v_accvgpr_read_b32 v242,a42
v_mfma_f32_16x16x32_bf16 a[52:55],a[128:131],v[80:83],a[52:55]
v_accvgpr_read_b32 v243,a43
v_cvt_pk_bf16_f32 v241,v242,v243
v_mfma_f32_16x16x32_bf16 a[56:59],a[128:131],v[96:99],a[56:59]
ds_read_b64_tr_b16 a[122:123],v45 offset:25888
ds_read_b64_tr_b16 a[124:125],v45 offset:31008
v_mfma_f32_16x16x32_bf16 a[56:59],a[132:135],v[100:103],a[56:59]
ds_read_b64_tr_b16 a[126:127],v45 offset:36128
v_mfma_f32_16x16x32_bf16 a[56:59],a[136:139],v[104:107],a[56:59]
ds_read_b64_tr_b16 a[128:129],v1
v_mfma_f32_16x16x32_bf16 a[56:59],a[140:143],v[108:111],a[56:59]
ds_read_b64_tr_b16 a[130:131],v1 offset:5120
v_mfma_f32_16x16x32_bf16 a[48:51],a[144:147],v[132:135],a[48:51]
ds_read_b64_tr_b16 a[132:133],v1 offset:10240
ds_read_b64_tr_b16 a[134:135],v1 offset:15360
v_mfma_f32_16x16x32_bf16 a[48:51],a[148:151],v[140:143],a[48:51]
ds_read_b64_tr_b16 a[136:137],v1 offset:20480
v_mfma_f32_16x16x32_bf16 a[48:51],a[152:155],v[148:151],a[48:51]
ds_read_b64_tr_b16 a[138:139],v1 offset:25600
v_mfma_f32_16x16x32_bf16 a[48:51],a[156:159],v[156:159],a[48:51]
ds_read_b64_tr_b16 a[140:141],v1 offset:30720
v_mfma_f32_16x16x32_bf16 a[52:55],a[156:159],v[188:191],a[52:55]
ds_read_b64_tr_b16 a[142:143],v1 offset:35840
v_mfma_f32_16x16x32_bf16 a[52:55],a[152:155],v[180:183],a[52:55]
v_mfma_f32_16x16x32_bf16 a[52:55],a[148:151],v[172:175],a[52:55]
v_mfma_f32_16x16x32_bf16 a[52:55],a[144:147],v[164:167],a[52:55]
v_mfma_f32_16x16x32_bf16 a[56:59],a[144:147],v[196:199],a[56:59]
ds_read_b64_tr_b16 a[144:145],v42
ds_read_b64_tr_b16 a[146:147],v42 offset:5120
v_mfma_f32_16x16x32_bf16 a[56:59],a[148:151],v[204:207],a[56:59]
v_accvgpr_read_b32 v226,a48
v_accvgpr_read_b32 v227,a49
v_mfma_f32_16x16x32_bf16 a[56:59],a[152:155],v[212:215],a[56:59]
v_accvgpr_read_b32 v12,a50
v_accvgpr_read_b32 v13,a51
v_mfma_f32_16x16x32_bf16 a[56:59],a[156:159],v[220:223],a[56:59]
v_cvt_pk_bf16_f32 v226,v226,v227
v_cvt_pk_bf16_f32 v227,v12,v13
v_mfma_f32_16x16x32_bf16 a[64:67],a[160:163],v[64:67],a[64:67]
buffer_store_dwordx4 v[224:227],v6,s[32:35],s84 offen offset:3072 sc1
v_mfma_f32_16x16x32_bf16 a[64:67],a[164:167],v[68:71],a[64:67]
v_accvgpr_read_b32 v234,a52
v_accvgpr_read_b32 v235,a53
v_mfma_f32_16x16x32_bf16 a[64:67],a[168:171],v[72:75],a[64:67]
v_accvgpr_read_b32 v14,a54
v_accvgpr_read_b32 v15,a55
v_mfma_f32_16x16x32_bf16 a[64:67],a[172:175],v[76:79],a[64:67]
v_cvt_pk_bf16_f32 v234,v234,v235
v_cvt_pk_bf16_f32 v235,v14,v15
v_mfma_f32_16x16x32_bf16 a[68:71],a[172:175],v[92:95],a[68:71]
buffer_store_dwordx4 v[232:235],v6,s[32:35],s85 offen offset:3072 sc1
v_mfma_f32_16x16x32_bf16 a[68:71],a[168:171],v[88:91],a[68:71]
v_accvgpr_read_b32 v242,a56
)A"
R"A(v_accvgpr_read_b32 v243,a57
v_mfma_f32_16x16x32_bf16 a[68:71],a[164:167],v[84:87],a[68:71]
v_accvgpr_read_b32 v16,a58
v_accvgpr_read_b32 v17,a59
v_mfma_f32_16x16x32_bf16 a[68:71],a[160:163],v[80:83],a[68:71]
v_cvt_pk_bf16_f32 v242,v242,v243
v_cvt_pk_bf16_f32 v243,v16,v17
v_mfma_f32_16x16x32_bf16 a[72:75],a[160:163],v[96:99],a[72:75]
buffer_store_dwordx4 v[240:243],v6,s[32:35],s86 offen offset:3072 sc1
v_mfma_f32_16x16x32_bf16 a[72:75],a[164:167],v[100:103],a[72:75]
ds_read_b64_tr_b16 a[148:149],v42 offset:10240
ds_read_b64_tr_b16 a[150:151],v42 offset:15360
v_mfma_f32_16x16x32_bf16 a[72:75],a[168:171],v[104:107],a[72:75]
ds_read_b64_tr_b16 a[152:153],v42 offset:20480
v_mfma_f32_16x16x32_bf16 a[72:75],a[172:175],v[108:111],a[72:75]
ds_read_b64_tr_b16 a[154:155],v42 offset:25600
v_mfma_f32_16x16x32_bf16 a[64:67],a[176:179],v[132:135],a[64:67]
s_waitcnt lgkmcnt(4)
ds_read_b64_tr_b16 a[156:157],v42 offset:30720
v_mfma_f32_16x16x32_bf16 a[64:67],a[180:183],v[140:143],a[64:67]
ds_read_b64_tr_b16 a[158:159],v42 offset:35840
ds_read_b64_tr_b16 a[160:161],v44
v_mfma_f32_16x16x32_bf16 a[64:67],a[184:187],v[148:151],a[64:67]
ds_read_b64_tr_b16 a[162:163],v44 offset:5120
v_mfma_f32_16x16x32_bf16 a[64:67],a[188:191],v[156:159],a[64:67]
ds_read_b64_tr_b16 a[164:165],v44 offset:10240
v_mfma_f32_16x16x32_bf16 a[68:71],a[188:191],v[188:191],a[68:71]
ds_read_b64_tr_b16 a[166:167],v44 offset:15360
v_mfma_f32_16x16x32_bf16 a[68:71],a[184:187],v[180:183],a[68:71]
ds_read_b64_tr_b16 a[168:169],v44 offset:20480
ds_read_b64_tr_b16 a[170:171],v44 offset:25600
v_mfma_f32_16x16x32_bf16 a[68:71],a[180:183],v[172:175],a[68:71]
ds_read_b64_tr_b16 a[172:173],v44 offset:30720
v_mfma_f32_16x16x32_bf16 a[68:71],a[176:179],v[164:167],a[68:71]
ds_read_b64_tr_b16 a[174:175],v44 offset:35840
v_mfma_f32_16x16x32_bf16 a[72:75],a[176:179],v[196:199],a[72:75]
ds_read_b64_tr_b16 a[176:177],v2 offset:32
v_mfma_f32_16x16x32_bf16 a[72:75],a[180:183],v[204:207],a[72:75]
ds_read_b64_tr_b16 a[178:179],v2 offset:5152
ds_read_b64_tr_b16 a[180:181],v2 offset:10272
v_mfma_f32_16x16x32_bf16 a[72:75],a[184:187],v[212:215],a[72:75]
ds_read_b64_tr_b16 a[182:183],v2 offset:15392
v_accvgpr_read_b32 v128,a64
v_mfma_f32_16x16x32_bf16 a[72:75],a[188:191],v[220:223],a[72:75]
ds_read_b64_tr_b16 a[184:185],v2 offset:20512
v_accvgpr_read_b32 v129,a65
v_mfma_f32_16x16x32_bf16 a[80:83],a[96:99],v[64:67],a[80:83]
ds_read_b64_tr_b16 a[186:187],v2 offset:25632
v_cvt_pk_bf16_f32 v128,v128,v129
v_mfma_f32_16x16x32_bf16 a[80:83],a[100:103],v[68:71],a[80:83]
ds_read_b64_tr_b16 a[188:189],v2 offset:30752
ds_read_b64_tr_b16 a[190:191],v2 offset:35872
v_mfma_f32_16x16x32_bf16 a[80:83],a[104:107],v[72:75],a[80:83]
v_accvgpr_read_b32 v130,a66
v_accvgpr_read_b32 v131,a67
v_mfma_f32_16x16x32_bf16 a[80:83],a[108:111],v[76:79],a[80:83]
v_cvt_pk_bf16_f32 v129,v130,v131
v_accvgpr_read_b32 v136,a68
v_mfma_f32_16x16x32_bf16 a[84:87],a[108:111],v[92:95],a[84:87]
v_accvgpr_read_b32 v137,a69
v_cvt_pk_bf16_f32 v136,v136,v137
v_mfma_f32_16x16x32_bf16 a[84:87],a[104:107],v[88:91],a[84:87]
v_accvgpr_read_b32 v138,a70
v_accvgpr_read_b32 v139,a71
v_mfma_f32_16x16x32_bf16 a[84:87],a[100:103],v[84:87],a[84:87]
v_cvt_pk_bf16_f32 v137,v138,v139
v_accvgpr_read_b32 v144,a72
v_mfma_f32_16x16x32_bf16 a[84:87],a[96:99],v[80:83],a[84:87]
v_accvgpr_read_b32 v145,a73
v_cvt_pk_bf16_f32 v144,v144,v145
v_mfma_f32_16x16x32_bf16 a[88:91],a[96:99],v[96:99],a[88:91]
ds_read_b64_tr_b16 a[96:97],v43 offset:32
ds_read_b64_tr_b16 a[98:99],v43 offset:5152
v_mfma_f32_16x16x32_bf16 a[88:91],a[100:103],v[100:103],a[88:91]
ds_read_b64_tr_b16 a[100:101],v43 offset:10272
v_accvgpr_read_b32 v146,a74
v_mfma_f32_16x16x32_bf16 a[88:91],a[104:107],v[104:107],a[88:91]
ds_read_b64_tr_b16 a[102:103],v43 offset:15392
v_accvgpr_read_b32 v147,a75
v_mfma_f32_16x16x32_bf16 a[88:91],a[108:111],v[108:111],a[88:91]
ds_read_b64_tr_b16 a[104:105],v43 offset:20512
v_cvt_pk_bf16_f32 v145,v146,v147
v_mfma_f32_16x16x32_bf16 a[80:83],a[112:115],v[132:135],a[80:83]
ds_read_b64_tr_b16 a[106:107],v43 offset:25632
ds_read_b64_tr_b16 a[108:109],v43 offset:30752
v_mfma_f32_16x16x32_bf16 a[80:83],a[116:119],v[140:143],a[80:83]
ds_read_b64_tr_b16 a[110:111],v43 offset:35872
v_mfma_f32_16x16x32_bf16 a[80:83],a[120:123],v[148:151],a[80:83]
v_mfma_f32_16x16x32_bf16 a[80:83],a[124:127],v[156:159],a[80:83]
v_mfma_f32_16x16x32_bf16 a[84:87],a[124:127],v[188:191],a[84:87]
v_mfma_f32_16x16x32_bf16 a[84:87],a[120:123],v[180:183],a[84:87]
v_mfma_f32_16x16x32_bf16 a[84:87],a[116:119],v[172:175],a[84:87]
v_mfma_f32_16x16x32_bf16 a[84:87],a[112:115],v[164:167],a[84:87]
v_mfma_f32_16x16x32_bf16 a[88:91],a[112:115],v[196:199],a[88:91]
ds_read_b64_tr_b16 a[112:113],v45 offset:32
ds_read_b64_tr_b16 a[114:115],v45 offset:5152
v_mfma_f32_16x16x32_bf16 a[88:91],a[116:119],v[204:207],a[88:91]
ds_read_b64_tr_b16 a[116:117],v45 offset:10272
v_accvgpr_read_b32 v130,a80
v_mfma_f32_16x16x32_bf16 a[88:91],a[120:123],v[212:215],a[88:91]
ds_read_b64_tr_b16 a[118:119],v45 offset:15392
v_accvgpr_read_b32 v131,a81
v_mfma_f32_16x16x32_bf16 a[88:91],a[124:127],v[220:223],a[88:91]
ds_read_b64_tr_b16 a[120:121],v45 offset:20512
v_accvgpr_read_b32 v12,a82
v_mfma_f32_16x16x32_bf16 a[204:207],a[128:131],v[152:155],a[204:207]
s_lshl_b32 s94,s71,2
ds_read_b64_tr_b16 a[122:123],v45 offset:25632
v_accvgpr_read_b32 v13,a83
v_mfma_f32_16x16x32_bf16 a[204:207],a[132:135],v[184:187],a[204:207]
s_add_u32 s94,s94,s40
v_cvt_pk_bf16_f32 v130,v130,v131
v_cvt_pk_bf16_f32 v131,v12,v13
v_mfma_f32_16x16x32_bf16 a[204:207],a[136:139],v[216:219],a[204:207]
s_lshl_b32 s95,s42,2
buffer_store_dwordx4 v[128:131],v25,s[32:35],s84 offen sc1
v_mfma_f32_16x16x32_bf16 a[204:207],a[140:143],v[248:251],a[204:207]
s_add_u32 s95,s95,s40
s_lshl_b32 s95,s95,4
v_mov_b32 v18,s68
v_mov_b32 v19,s69
v_mfma_f32_16x16x32_bf16 a[204:207],a[144:147],v[112:115],a[204:207]
s_lshl_b32 s94,s94,4
ds_read_b64_tr_b16 a[124:125],v45 offset:30752
ds_read_b64_tr_b16 a[126:127],v45 offset:35872
s_waitcnt lgkmcnt(6)
v_mfma_f32_16x16x32_bf16 a[204:207],a[148:151],v[116:119],a[204:207]
ds_read_b64_tr_b16 a[128:129],v1 offset:64
ds_read_b64_tr_b16 a[130:131],v1 offset:5184
v_mfma_f32_16x16x32_bf16 a[204:207],a[152:155],v[120:123],a[204:207]
s_waitcnt vmcnt(1)
s_mov_b64 exec,1
buffer_store_dwordx2 v[18:19],v6,s[64:67],s95 offen offset:2048 sc1
s_mov_b64 exec,-1
v_mfma_f32_16x16x32_bf16 a[204:207],a[156:159],v[124:127],a[204:207]
ds_read_b64_tr_b16 a[132:133],v1 offset:10304
ds_read_b64_tr_b16 a[134:135],v1 offset:15424
v_mfma_f32_16x16x32_bf16 a[204:207],a[160:163],v[228:231],a[204:207]
ds_read_b64_tr_b16 a[136:137],v1 offset:20544
ds_read_b64_tr_b16 a[138:139],v1 offset:25664
v_mfma_f32_16x16x32_bf16 a[204:207],a[164:167],v[236:239],a[204:207]
ds_read_b64_tr_b16 a[140:141],v1 offset:30784
ds_read_b64_tr_b16 a[142:143],v1 offset:35904
v_mfma_f32_16x16x32_bf16 a[204:207],a[168:171],v[244:247],a[204:207]
ds_read_b64_tr_b16 a[144:145],v42 offset:64
ds_read_b64_tr_b16 a[146:147],v42 offset:5184
v_mfma_f32_16x16x32_bf16 a[204:207],a[172:175],v[252:255],a[204:207]
ds_read_b64_tr_b16 a[148:149],v42 offset:10304
ds_read_b64_tr_b16 a[150:151],v42 offset:15424
v_mfma_f32_16x16x32_bf16 a[220:223],a[176:179],v[152:155],a[220:223]
ds_read_b64_tr_b16 a[152:153],v42 offset:20544
ds_read_b64_tr_b16 a[154:155],v42 offset:25664
v_mfma_f32_16x16x32_bf16 a[220:223],a[180:183],v[184:187],a[220:223]
ds_read_b64_tr_b16 a[156:157],v42 offset:30784
ds_read_b64_tr_b16 a[158:159],v42 offset:35904
v_mfma_f32_16x16x32_bf16 a[220:223],a[184:187],v[216:219],a[220:223]
v_accvgpr_read_b32 v138,a84
v_accvgpr_read_b32 v139,a85
v_mfma_f32_16x16x32_bf16 a[220:223],a[188:191],v[248:251],a[220:223]
ds_read_b64_tr_b16 a[160:161],v44 offset:64
ds_read_b64_tr_b16 a[162:163],v44 offset:5184
v_mfma_f32_16x16x32_bf16 a[220:223],a[96:99],v[112:115],a[220:223]
v_accvgpr_read_b32 v14,a86
v_accvgpr_read_b32 v15,a87
v_mfma_f32_16x16x32_bf16 a[220:223],a[100:103],v[116:119],a[220:223]
v_cvt_pk_bf16_f32 v138,v138,v139
v_cvt_pk_bf16_f32 v139,v14,v15
v_mfma_f32_16x16x32_bf16 a[220:223],a[104:107],v[120:123],a[220:223]
buffer_store_dwordx4 v[136:139],v25,s[32:35],s85 offen sc1
v_mfma_f32_16x16x32_bf16 a[220:223],a[108:111],v[124:127],a[220:223]
ds_read_b64_tr_b16 a[164:165],v44 offset:10304
ds_read_b64_tr_b16 a[166:167],v44 offset:15424
v_mfma_f32_16x16x32_bf16 a[220:223],a[112:115],v[228:231],a[220:223]
s_waitcnt lgkmcnt(6)
ds_read_b64_tr_b16 a[168:169],v44 offset:20544
ds_read_b64_tr_b16 a[170:171],v44 offset:25664
v_mfma_f32_16x16x32_bf16 a[220:223],a[116:119],v[236:239],a[220:223]
ds_read_b64_tr_b16 a[172:173],v44 offset:30784
ds_read_b64_tr_b16 a[174:175],v44 offset:35904
v_mfma_f32_16x16x32_bf16 a[220:223],a[120:123],v[244:247],a[220:223]
ds_read_b64_tr_b16 a[176:177],v2 offset:96
ds_read_b64_tr_b16 a[178:179],v2 offset:5216
v_mfma_f32_16x16x32_bf16 a[220:223],a[124:127],v[252:255],a[220:223]
v_bfe_u32 v24,v6,4,2
v_min_u32 v24,2,v24
v_mfma_f32_16x16x32_bf16 a[236:239],a[128:131],v[152:155],a[236:239]
ds_read_b64_tr_b16 a[180:181],v2 offset:10336
ds_read_b64_tr_b16 a[182:183],v2 offset:15456
v_mfma_f32_16x16x32_bf16 a[236:239],a[132:135],v[184:187],a[236:239]
v_add_u32 v24,s70,v24
v_add_u32 v24,1,v24
v_mfma_f32_16x16x32_bf16 a[236:239],a[136:139],v[216:219],a[236:239]
ds_read_b64_tr_b16 a[184:185],v2 offset:20576
ds_read_b64_tr_b16 a[186:187],v2 offset:25696
v_mfma_f32_16x16x32_bf16 a[236:239],a[140:143],v[248:251],a[236:239]
v_and_b32 v24,3,v24
v_accvgpr_read_b32 v146,a88
v_mfma_f32_16x16x32_bf16 a[236:239],a[144:147],v[112:115],a[236:239]
v_accvgpr_read_b32 v147,a89
v_lshlrev_b32 v24,8,v24
v_mfma_f32_16x16x32_bf16 a[236:239],a[148:151],v[116:119],a[236:239]
ds_read_b64_tr_b16 a[188:189],v2 offset:30816
ds_read_b64_tr_b16 a[190:191],v2 offset:35936
v_mfma_f32_16x16x32_bf16 a[236:239],a[152:155],v[120:123],a[236:239]
s_waitcnt lgkmcnt(6)
v_accvgpr_read_b32 v16,a90
v_accvgpr_read_b32 v17,a91
v_mfma_f32_16x16x32_bf16 a[236:239],a[156:159],v[124:127],a[236:239]
ds_read_b64_tr_b16 a[96:97],v43 offset:96
ds_read_b64_tr_b16 a[98:99],v43 offset:5216
v_cvt_pk_bf16_f32 v146,v146,v147
v_cvt_pk_bf16_f32 v147,v16,v17
v_mfma_f32_16x16x32_bf16 a[236:239],a[160:163],v[228:231],a[236:239]
buffer_store_dwordx4 v[144:147],v25,s[32:35],s86 offen sc1
ds_read_b64_tr_b16 a[100:101],v43 offset:10336
ds_read_b64_tr_b16 a[102:103],v43 offset:15456
v_mfma_f32_16x16x32_bf16 a[236:239],a[164:167],v[236:239],a[236:239]
ds_read_b64_tr_b16 a[104:105],v43 offset:20576
ds_read_b64_tr_b16 a[106:107],v43 offset:25696
v_mfma_f32_16x16x32_bf16 a[236:239],a[168:171],v[244:247],a[236:239]
ds_read_b64_tr_b16 a[108:109],v43 offset:30816
ds_read_b64_tr_b16 a[110:111],v43 offset:35936
v_mfma_f32_16x16x32_bf16 a[236:239],a[172:175],v[252:255],a[236:239]
ds_read_b64_tr_b16 a[112:113],v45 offset:96
ds_read_b64_tr_b16 a[114:115],v45 offset:5216
v_mfma_f32_16x16x32_bf16 a[252:255],a[176:179],v[152:155],a[252:255]
s_waitcnt lgkmcnt(12)
ds_read_b64_tr_b16 a[116:117],v45 offset:10336
ds_read_b64_tr_b16 a[118:119],v45 offset:15456
v_mfma_f32_16x16x32_bf16 a[252:255],a[180:183],v[184:187],a[252:255]
ds_read_b64_tr_b16 a[120:121],v45 offset:20576
ds_read_b64_tr_b16 a[122:123],v45 offset:25696
v_mfma_f32_16x16x32_bf16 a[252:255],a[184:187],v[216:219],a[252:255]
s_waitcnt lgkmcnt(14)
ds_read_b64_tr_b16 a[124:125],v45 offset:30816
ds_read_b64_tr_b16 a[126:127],v45 offset:35936
v_mfma_f32_16x16x32_bf16 a[252:255],a[188:191],v[248:251],a[252:255]
s_waitcnt lgkmcnt(14)
ds_read_b64_tr_b16 a[128:129],v1 offset:128
ds_read_b64_tr_b16 a[130:131],v1 offset:5248
v_mfma_f32_16x16x32_bf16 a[252:255],a[96:99],v[112:115],a[252:255]
s_waitcnt lgkmcnt(14)
buffer_load_dwordx2 v[26:27],v24,s[64:67],s94 offen offset:2048 sc1
ds_read_b64_tr_b16 a[132:133],v1 offset:10368
ds_read_b64_tr_b16 a[134:135],v1 offset:15488
v_mfma_f32_16x16x32_bf16 a[252:255],a[100:103],v[116:119],a[252:255]
s_waitcnt lgkmcnt(14)
ds_read_b64_tr_b16 a[136:137],v1 offset:20608
ds_read_b64_tr_b16 a[138:139],v1 offset:25728
v_mfma_f32_16x16x32_bf16 a[252:255],a[104:107],v[120:123],a[252:255]
)A"
R"A(s_waitcnt lgkmcnt(14)
ds_read_b64_tr_b16 a[140:141],v1 offset:30848
ds_read_b64_tr_b16 a[142:143],v1 offset:35968
v_mfma_f32_16x16x32_bf16 a[252:255],a[108:111],v[124:127],a[252:255]
s_waitcnt lgkmcnt(14)
ds_read_b64_tr_b16 a[144:145],v42 offset:128
ds_read_b64_tr_b16 a[146:147],v42 offset:5248
v_mfma_f32_16x16x32_bf16 a[252:255],a[112:115],v[228:231],a[252:255]
s_waitcnt lgkmcnt(14)
ds_read_b64_tr_b16 a[148:149],v42 offset:10368
ds_read_b64_tr_b16 a[150:151],v42 offset:15488
v_mfma_f32_16x16x32_bf16 a[252:255],a[116:119],v[236:239],a[252:255]
s_waitcnt lgkmcnt(14)
ds_read_b64_tr_b16 a[152:153],v42 offset:20608
ds_read_b64_tr_b16 a[154:155],v42 offset:25728
v_mfma_f32_16x16x32_bf16 a[252:255],a[120:123],v[244:247],a[252:255]
s_waitcnt lgkmcnt(14)
ds_read_b64_tr_b16 a[156:157],v42 offset:30848
ds_read_b64_tr_b16 a[158:159],v42 offset:35968
v_mfma_f32_16x16x32_bf16 a[252:255],a[124:127],v[252:255],a[252:255]
s_waitcnt lgkmcnt(14)
ds_read_b64_tr_b16 a[160:161],v44 offset:128
ds_read_b64_tr_b16 a[162:163],v44 offset:5248
v_mfma_f32_16x16x32_bf16 a[12:15],a[128:131],v[152:155],a[12:15]
s_waitcnt lgkmcnt(14)
ds_read_b64_tr_b16 a[164:165],v44 offset:10368
ds_read_b64_tr_b16 a[166:167],v44 offset:15488
v_mfma_f32_16x16x32_bf16 a[12:15],a[132:135],v[184:187],a[12:15]
s_waitcnt lgkmcnt(14)
ds_read_b64_tr_b16 a[168:169],v44 offset:20608
ds_read_b64_tr_b16 a[170:171],v44 offset:25728
v_mfma_f32_16x16x32_bf16 a[12:15],a[136:139],v[216:219],a[12:15]
s_waitcnt lgkmcnt(14)
ds_read_b64_tr_b16 a[172:173],v44 offset:30848
ds_read_b64_tr_b16 a[174:175],v44 offset:35968
v_mfma_f32_16x16x32_bf16 a[12:15],a[140:143],v[248:251],a[12:15]
s_waitcnt lgkmcnt(14)
ds_read_b64_tr_b16 a[176:177],v2 offset:160
ds_read_b64_tr_b16 a[178:179],v2 offset:5280
v_mfma_f32_16x16x32_bf16 a[12:15],a[144:147],v[112:115],a[12:15]
s_waitcnt lgkmcnt(14)
ds_read_b64_tr_b16 a[180:181],v2 offset:10400
ds_read_b64_tr_b16 a[182:183],v2 offset:15520
v_mfma_f32_16x16x32_bf16 a[12:15],a[148:151],v[116:119],a[12:15]
s_waitcnt lgkmcnt(14)
ds_read_b64_tr_b16 a[184:185],v2 offset:20640
ds_read_b64_tr_b16 a[186:187],v2 offset:25760
v_mfma_f32_16x16x32_bf16 a[12:15],a[152:155],v[120:123],a[12:15]
s_waitcnt lgkmcnt(14)
ds_read_b64_tr_b16 a[188:189],v2 offset:30880
ds_read_b64_tr_b16 a[190:191],v2 offset:36000
v_mfma_f32_16x16x32_bf16 a[12:15],a[156:159],v[124:127],a[12:15]
s_waitcnt lgkmcnt(14)
ds_read_b64_tr_b16 a[96:97],v43 offset:160
ds_read_b64_tr_b16 a[98:99],v43 offset:5280
v_mfma_f32_16x16x32_bf16 a[12:15],a[160:163],v[228:231],a[12:15]
s_waitcnt lgkmcnt(14)
ds_read_b64_tr_b16 a[100:101],v43 offset:10400
ds_read_b64_tr_b16 a[102:103],v43 offset:15520
v_mfma_f32_16x16x32_bf16 a[12:15],a[164:167],v[236:239],a[12:15]
s_waitcnt lgkmcnt(14)
ds_read_b64_tr_b16 a[104:105],v43 offset:20640
ds_read_b64_tr_b16 a[106:107],v43 offset:25760
v_mfma_f32_16x16x32_bf16 a[12:15],a[168:171],v[244:247],a[12:15]
s_waitcnt lgkmcnt(14)
ds_read_b64_tr_b16 a[108:109],v43 offset:30880
ds_read_b64_tr_b16 a[110:111],v43 offset:36000
v_mfma_f32_16x16x32_bf16 a[12:15],a[172:175],v[252:255],a[12:15]
s_waitcnt lgkmcnt(14)
ds_read_b64_tr_b16 a[112:113],v45 offset:160
ds_read_b64_tr_b16 a[114:115],v45 offset:5280
v_mfma_f32_16x16x32_bf16 a[28:31],a[176:179],v[152:155],a[28:31]
s_waitcnt lgkmcnt(14)
ds_read_b64_tr_b16 a[116:117],v45 offset:10400
ds_read_b64_tr_b16 a[118:119],v45 offset:15520
v_mfma_f32_16x16x32_bf16 a[28:31],a[180:183],v[184:187],a[28:31]
s_waitcnt lgkmcnt(14)
ds_read_b64_tr_b16 a[120:121],v45 offset:20640
ds_read_b64_tr_b16 a[122:123],v45 offset:25760
v_mfma_f32_16x16x32_bf16 a[28:31],a[184:187],v[216:219],a[28:31]
s_waitcnt lgkmcnt(14)
ds_read_b64_tr_b16 a[124:125],v45 offset:30880
ds_read_b64_tr_b16 a[126:127],v45 offset:36000
v_mfma_f32_16x16x32_bf16 a[28:31],a[188:191],v[248:251],a[28:31]
s_waitcnt lgkmcnt(14)
ds_read_b64_tr_b16 a[128:129],v1 offset:192
ds_read_b64_tr_b16 a[130:131],v1 offset:5312
v_mfma_f32_16x16x32_bf16 a[28:31],a[96:99],v[112:115],a[28:31]
s_waitcnt lgkmcnt(14)
ds_read_b64_tr_b16 a[132:133],v1 offset:10432
ds_read_b64_tr_b16 a[134:135],v1 offset:15552
v_mfma_f32_16x16x32_bf16 a[28:31],a[100:103],v[116:119],a[28:31]
s_waitcnt lgkmcnt(14)
ds_read_b64_tr_b16 a[136:137],v1 offset:20672
ds_read_b64_tr_b16 a[138:139],v1 offset:25792
v_mfma_f32_16x16x32_bf16 a[28:31],a[104:107],v[120:123],a[28:31]
s_waitcnt lgkmcnt(14)
ds_read_b64_tr_b16 a[140:141],v1 offset:30912
ds_read_b64_tr_b16 a[142:143],v1 offset:36032
v_mfma_f32_16x16x32_bf16 a[28:31],a[108:111],v[124:127],a[28:31]
s_waitcnt lgkmcnt(14)
ds_read_b64_tr_b16 a[144:145],v42 offset:192
ds_read_b64_tr_b16 a[146:147],v42 offset:5312
v_mfma_f32_16x16x32_bf16 a[28:31],a[112:115],v[228:231],a[28:31]
s_waitcnt lgkmcnt(14)
ds_read_b64_tr_b16 a[148:149],v42 offset:10432
ds_read_b64_tr_b16 a[150:151],v42 offset:15552
v_mfma_f32_16x16x32_bf16 a[28:31],a[116:119],v[236:239],a[28:31]
s_waitcnt lgkmcnt(14)
ds_read_b64_tr_b16 a[152:153],v42 offset:20672
ds_read_b64_tr_b16 a[154:155],v42 offset:25792
v_mfma_f32_16x16x32_bf16 a[28:31],a[120:123],v[244:247],a[28:31]
s_lshl_b32 s95,s42,2
ds_read_b64_tr_b16 a[156:157],v42 offset:30912
ds_read_b64_tr_b16 a[158:159],v42 offset:36032
s_waitcnt lgkmcnt(15)
v_mfma_f32_16x16x32_bf16 a[28:31],a[124:127],v[252:255],a[28:31]
s_add_u32 s95,s95,s40
s_lshl_b32 s95,s95,4
ds_read_b64_tr_b16 a[160:161],v44 offset:192
ds_read_b64_tr_b16 a[162:163],v44 offset:5312
v_mov_b32 v18,s68
v_mov_b32 v19,s69
s_waitcnt lgkmcnt(15)
v_mfma_f32_16x16x32_bf16 a[44:47],a[128:131],v[152:155],a[44:47]
s_waitcnt vmcnt(1)
s_mov_b64 exec,1
buffer_store_dwordx2 v[18:19],v6,s[64:67],s95 offen offset:3072 sc1
s_mov_b64 exec,-1
ds_read_b64_tr_b16 a[164:165],v44 offset:10432
ds_read_b64_tr_b16 a[166:167],v44 offset:15552
s_waitcnt vmcnt(1)
v_cmp_ne_u32 s[90:91],s68,v26
v_cmp_ne_u32 s[92:93],s69,v27
s_nop 0
s_or_b64 s[90:91],s[90:91],s[92:93]
s_cbranch_scc1 L_fr9_%=
L_fo9_%=:
s_waitcnt lgkmcnt(15)
v_mfma_f32_16x16x32_bf16 a[44:47],a[132:135],v[184:187],a[44:47]
s_lshl_b32 s94,s71,2
ds_read_b64_tr_b16 a[168:169],v44 offset:20672
ds_read_b64_tr_b16 a[170:171],v44 offset:25792
s_waitcnt lgkmcnt(15)
v_mfma_f32_16x16x32_bf16 a[44:47],a[136:139],v[216:219],a[44:47]
s_add_u32 s94,s94,s40
ds_read_b64_tr_b16 a[172:173],v44 offset:30912
ds_read_b64_tr_b16 a[174:175],v44 offset:36032
s_waitcnt lgkmcnt(15)
v_mfma_f32_16x16x32_bf16 a[44:47],a[140:143],v[248:251],a[44:47]
s_lshl_b32 s94,s94,4
ds_read_b64_tr_b16 a[176:177],v2 offset:224
ds_read_b64_tr_b16 a[178:179],v2 offset:5344
s_waitcnt lgkmcnt(15)
v_mfma_f32_16x16x32_bf16 a[44:47],a[144:147],v[112:115],a[44:47]
s_waitcnt lgkmcnt(14)
buffer_load_dwordx4 v[64:67],v6,s[32:35],s76 offen sc1
ds_read_b64_tr_b16 a[180:181],v2 offset:10464
ds_read_b64_tr_b16 a[182:183],v2 offset:15584
v_mfma_f32_16x16x32_bf16 a[44:47],a[148:151],v[116:119],a[44:47]
s_waitcnt lgkmcnt(14)
ds_read_b64_tr_b16 a[184:185],v2 offset:20704
ds_read_b64_tr_b16 a[186:187],v2 offset:25824
v_mfma_f32_16x16x32_bf16 a[44:47],a[152:155],v[120:123],a[44:47]
s_waitcnt lgkmcnt(14)
ds_read_b64_tr_b16 a[188:189],v2 offset:30944
ds_read_b64_tr_b16 a[190:191],v2 offset:36064
v_mfma_f32_16x16x32_bf16 a[44:47],a[156:159],v[124:127],a[44:47]
s_waitcnt lgkmcnt(14)
ds_read_b64_tr_b16 a[96:97],v43 offset:224
ds_read_b64_tr_b16 a[98:99],v43 offset:5344
v_mfma_f32_16x16x32_bf16 a[44:47],a[160:163],v[228:231],a[44:47]
s_waitcnt lgkmcnt(14)
buffer_load_dwordx4 v[80:83],v6,s[32:35],s77 offen sc1
ds_read_b64_tr_b16 a[100:101],v43 offset:10464
ds_read_b64_tr_b16 a[102:103],v43 offset:15584
v_mfma_f32_16x16x32_bf16 a[44:47],a[164:167],v[236:239],a[44:47]
s_waitcnt lgkmcnt(14)
ds_read_b64_tr_b16 a[104:105],v43 offset:20704
ds_read_b64_tr_b16 a[106:107],v43 offset:25824
v_mfma_f32_16x16x32_bf16 a[44:47],a[168:171],v[244:247],a[44:47]
s_waitcnt lgkmcnt(14)
ds_read_b64_tr_b16 a[108:109],v43 offset:30944
ds_read_b64_tr_b16 a[110:111],v43 offset:36064
v_mfma_f32_16x16x32_bf16 a[44:47],a[172:175],v[252:255],a[44:47]
s_waitcnt lgkmcnt(14)
ds_read_b64_tr_b16 a[112:113],v45 offset:224
ds_read_b64_tr_b16 a[114:115],v45 offset:5344
v_mfma_f32_16x16x32_bf16 a[60:63],a[176:179],v[152:155],a[60:63]
s_waitcnt lgkmcnt(14)
buffer_load_dwordx4 v[96:99],v6,s[32:35],s78 offen sc1
ds_read_b64_tr_b16 a[116:117],v45 offset:10464
ds_read_b64_tr_b16 a[118:119],v45 offset:15584
v_mfma_f32_16x16x32_bf16 a[60:63],a[180:183],v[184:187],a[60:63]
s_waitcnt lgkmcnt(14)
ds_read_b64_tr_b16 a[120:121],v45 offset:20704
ds_read_b64_tr_b16 a[122:123],v45 offset:25824
v_mfma_f32_16x16x32_bf16 a[60:63],a[184:187],v[216:219],a[60:63]
s_waitcnt lgkmcnt(14)
ds_read_b64_tr_b16 a[124:125],v45 offset:30944
ds_read_b64_tr_b16 a[126:127],v45 offset:36064
v_mfma_f32_16x16x32_bf16 a[60:63],a[188:191],v[248:251],a[60:63]
s_waitcnt lgkmcnt(14)
ds_read_b64_tr_b16 a[128:129],v1 offset:256
ds_read_b64_tr_b16 a[130:131],v1 offset:5376
v_mfma_f32_16x16x32_bf16 a[60:63],a[96:99],v[112:115],a[60:63]
s_waitcnt lgkmcnt(14)
buffer_load_dwordx4 v[68:71],v6,s[32:35],s76 offen offset:1024 sc1
ds_read_b64_tr_b16 a[132:133],v1 offset:10496
ds_read_b64_tr_b16 a[134:135],v1 offset:15616
v_mfma_f32_16x16x32_bf16 a[60:63],a[100:103],v[116:119],a[60:63]
s_waitcnt lgkmcnt(14)
ds_read_b64_tr_b16 a[136:137],v1 offset:20736
ds_read_b64_tr_b16 a[138:139],v1 offset:25856
v_mfma_f32_16x16x32_bf16 a[60:63],a[104:107],v[120:123],a[60:63]
s_waitcnt lgkmcnt(14)
ds_read_b64_tr_b16 a[140:141],v1 offset:30976
ds_read_b64_tr_b16 a[142:143],v1 offset:36096
v_mfma_f32_16x16x32_bf16 a[60:63],a[108:111],v[124:127],a[60:63]
s_waitcnt lgkmcnt(14)
ds_read_b64_tr_b16 a[144:145],v42 offset:256
ds_read_b64_tr_b16 a[146:147],v42 offset:5376
v_mfma_f32_16x16x32_bf16 a[60:63],a[112:115],v[228:231],a[60:63]
s_waitcnt lgkmcnt(14)
buffer_load_dwordx4 v[84:87],v6,s[32:35],s77 offen offset:1024 sc1
ds_read_b64_tr_b16 a[148:149],v42 offset:10496
ds_read_b64_tr_b16 a[150:151],v42 offset:15616
v_mfma_f32_16x16x32_bf16 a[60:63],a[116:119],v[236:239],a[60:63]
s_waitcnt lgkmcnt(14)
ds_read_b64_tr_b16 a[152:153],v42 offset:20736
ds_read_b64_tr_b16 a[154:155],v42 offset:25856
v_mfma_f32_16x16x32_bf16 a[60:63],a[120:123],v[244:247],a[60:63]
s_waitcnt lgkmcnt(14)
ds_read_b64_tr_b16 a[156:157],v42 offset:30976
ds_read_b64_tr_b16 a[158:159],v42 offset:36096
v_mfma_f32_16x16x32_bf16 a[60:63],a[124:127],v[252:255],a[60:63]
s_waitcnt lgkmcnt(14)
ds_read_b64_tr_b16 a[160:161],v44 offset:256
ds_read_b64_tr_b16 a[162:163],v44 offset:5376
v_mfma_f32_16x16x32_bf16 a[76:79],a[128:131],v[152:155],a[76:79]
s_waitcnt lgkmcnt(14)
buffer_load_dwordx4 v[100:103],v6,s[32:35],s78 offen offset:1024 sc1
ds_read_b64_tr_b16 a[164:165],v44 offset:10496
ds_read_b64_tr_b16 a[166:167],v44 offset:15616
v_mfma_f32_16x16x32_bf16 a[76:79],a[132:135],v[184:187],a[76:79]
s_waitcnt lgkmcnt(14)
ds_read_b64_tr_b16 a[168:169],v44 offset:20736
ds_read_b64_tr_b16 a[170:171],v44 offset:25856
v_bfe_u32 v24,v6,4,2
v_min_u32 v24,2,v24
v_mfma_f32_16x16x32_bf16 a[76:79],a[136:139],v[216:219],a[76:79]
s_waitcnt lgkmcnt(14)
ds_read_b64_tr_b16 a[172:173],v44 offset:30976
ds_read_b64_tr_b16 a[174:175],v44 offset:36096
v_add_u32 v24,s70,v24
v_add_u32 v24,1,v24
v_mfma_f32_16x16x32_bf16 a[76:79],a[140:143],v[248:251],a[76:79]
s_waitcnt lgkmcnt(14)
ds_read_b64_tr_b16 a[176:177],v2 offset:288
ds_read_b64_tr_b16 a[178:179],v2 offset:5408
v_and_b32 v24,3,v24
v_lshlrev_b32 v24,8,v24
v_mfma_f32_16x16x32_bf16 a[76:79],a[144:147],v[112:115],a[76:79]
s_waitcnt lgkmcnt(14)
buffer_load_dwordx4 v[72:75],v6,s[32:35],s76 offen offset:2048 sc1
ds_read_b64_tr_b16 a[180:181],v2 offset:10528
ds_read_b64_tr_b16 a[182:183],v2 offset:15648
v_mfma_f32_16x16x32_bf16 a[76:79],a[148:151],v[116:119],a[76:79]
s_waitcnt lgkmcnt(14)
ds_read_b64_tr_b16 a[184:185],v2 offset:20768
ds_read_b64_tr_b16 a[186:187],v2 offset:25888
v_mfma_f32_16x16x32_bf16 a[76:79],a[152:155],v[120:123],a[76:79]
s_waitcnt lgkmcnt(14)
ds_read_b64_tr_b16 a[188:189],v2 offset:31008
ds_read_b64_tr_b16 a[190:191],v2 offset:36128
v_mfma_f32_16x16x32_bf16 a[76:79],a[156:159],v[124:127],a[76:79]
s_waitcnt lgkmcnt(14)
ds_read_b64_tr_b16 a[96:97],v43 offset:288
)A"
R"A(ds_read_b64_tr_b16 a[98:99],v43 offset:5408
v_mfma_f32_16x16x32_bf16 a[76:79],a[160:163],v[228:231],a[76:79]
s_waitcnt lgkmcnt(14)
buffer_load_dwordx4 v[88:91],v6,s[32:35],s77 offen offset:2048 sc1
ds_read_b64_tr_b16 a[100:101],v43 offset:10528
ds_read_b64_tr_b16 a[102:103],v43 offset:15648
v_mfma_f32_16x16x32_bf16 a[76:79],a[164:167],v[236:239],a[76:79]
s_waitcnt lgkmcnt(14)
ds_read_b64_tr_b16 a[104:105],v43 offset:20768
ds_read_b64_tr_b16 a[106:107],v43 offset:25888
v_mfma_f32_16x16x32_bf16 a[76:79],a[168:171],v[244:247],a[76:79]
s_waitcnt lgkmcnt(14)
ds_read_b64_tr_b16 a[108:109],v43 offset:31008
ds_read_b64_tr_b16 a[110:111],v43 offset:36128
v_mfma_f32_16x16x32_bf16 a[76:79],a[172:175],v[252:255],a[76:79]
s_waitcnt lgkmcnt(14)
ds_read_b64_tr_b16 a[112:113],v45 offset:288
ds_read_b64_tr_b16 a[114:115],v45 offset:5408
v_mfma_f32_16x16x32_bf16 a[92:95],a[176:179],v[152:155],a[92:95]
s_waitcnt lgkmcnt(14)
buffer_load_dwordx4 v[104:107],v6,s[32:35],s78 offen offset:2048 sc1
ds_read_b64_tr_b16 a[116:117],v45 offset:10528
ds_read_b64_tr_b16 a[118:119],v45 offset:15648
v_mfma_f32_16x16x32_bf16 a[92:95],a[180:183],v[184:187],a[92:95]
s_waitcnt lgkmcnt(14)
ds_read_b64_tr_b16 a[120:121],v45 offset:20768
ds_read_b64_tr_b16 a[122:123],v45 offset:25888
v_mfma_f32_16x16x32_bf16 a[92:95],a[184:187],v[216:219],a[92:95]
s_waitcnt lgkmcnt(14)
ds_read_b64_tr_b16 a[124:125],v45 offset:31008
ds_read_b64_tr_b16 a[126:127],v45 offset:36128
v_mfma_f32_16x16x32_bf16 a[92:95],a[188:191],v[248:251],a[92:95]
s_waitcnt lgkmcnt(14)
v_mfma_f32_16x16x32_bf16 a[92:95],a[96:99],v[112:115],a[92:95]
s_waitcnt lgkmcnt(12)
buffer_load_dwordx4 v[76:79],v6,s[32:35],s76 offen offset:3072 sc1
v_mfma_f32_16x16x32_bf16 a[92:95],a[100:103],v[116:119],a[92:95]
s_waitcnt lgkmcnt(10)
v_mfma_f32_16x16x32_bf16 a[92:95],a[104:107],v[120:123],a[92:95]
s_waitcnt lgkmcnt(8)
v_mfma_f32_16x16x32_bf16 a[92:95],a[108:111],v[124:127],a[92:95]
s_waitcnt lgkmcnt(6)
v_mfma_f32_16x16x32_bf16 a[92:95],a[112:115],v[228:231],a[92:95]
s_waitcnt lgkmcnt(4)
buffer_load_dwordx4 v[92:95],v6,s[32:35],s77 offen offset:3072 sc1
v_mfma_f32_16x16x32_bf16 a[92:95],a[116:119],v[236:239],a[92:95]
s_waitcnt lgkmcnt(2)
v_mfma_f32_16x16x32_bf16 a[92:95],a[120:123],v[244:247],a[92:95]
s_waitcnt lgkmcnt(0)
v_mfma_f32_16x16x32_bf16 a[92:95],a[124:127],v[252:255],a[92:95]
buffer_load_dwordx2 v[26:27],v24,s[64:67],s94 offen offset:3072 sc1
s_waitcnt vmcnt(0)
v_cmp_ne_u32 s[90:91],s68,v26
v_cmp_ne_u32 s[92:93],s69,v27
s_nop 0
s_or_b64 s[90:91],s[90:91],s[92:93]
s_cbranch_scc1 L_fr10_%=
L_fo10_%=:
buffer_load_dwordx4 v[132:135],v25,s[32:35],s76 offen sc1
buffer_load_dwordx4 v[164:167],v25,s[32:35],s77 offen sc1
buffer_load_dwordx4 v[196:199],v25,s[32:35],s78 offen sc1
v_mfma_f32_16x16x32_bf16 a[204:207],v[34:37],v[64:67],a[204:207]
v_mfma_f32_16x16x32_bf16 a[204:207],v[34:37],v[80:83],a[204:207]
buffer_load_dwordx4 v[108:111],v6,s[32:35],s78 offen offset:3072 sc1
v_mfma_f32_16x16x32_bf16 a[204:207],v[34:37],v[96:99],a[204:207]
v_mfma_f32_16x16x32_bf16 a[220:223],v[32:35],v[64:67],a[220:223]
v_mfma_f32_16x16x32_bf16 a[220:223],v[32:35],v[80:83],a[220:223]
v_mfma_f32_16x16x32_bf16 a[220:223],v[32:35],v[96:99],a[220:223]
v_mfma_f32_16x16x32_bf16 a[236:239],v[34:37],v[68:71],a[236:239]
v_mfma_f32_16x16x32_bf16 a[236:239],v[34:37],v[84:87],a[236:239]
v_mfma_f32_16x16x32_bf16 a[236:239],v[34:37],v[100:103],a[236:239]
s_nop 1
v_accvgpr_read_b32 v128,a204
v_accvgpr_read_b32 v129,a205
v_mfma_f32_16x16x32_bf16 a[252:255],v[32:35],v[68:71],a[252:255]
v_cvt_pk_bf16_f32 v128,v128,v129
v_accvgpr_read_b32 v130,a206
v_mfma_f32_16x16x32_bf16 a[252:255],v[32:35],v[84:87],a[252:255]
v_accvgpr_read_b32 v131,a207
v_cvt_pk_bf16_f32 v129,v130,v131
v_mfma_f32_16x16x32_bf16 a[252:255],v[32:35],v[100:103],a[252:255]
buffer_store_dwordx2 v[128:129],v40,s[20:23],s43 offen
v_mfma_f32_16x16x32_bf16 a[12:15],v[34:37],v[72:75],a[12:15]
v_accvgpr_read_b32 v130,a220
v_accvgpr_read_b32 v131,a221
v_mfma_f32_16x16x32_bf16 a[12:15],v[34:37],v[88:91],a[12:15]
v_accvgpr_read_b32 v12,a222
v_accvgpr_read_b32 v13,a223
v_mfma_f32_16x16x32_bf16 a[12:15],v[34:37],v[104:107],a[12:15]
v_cvt_pk_bf16_f32 v130,v130,v131
v_cvt_pk_bf16_f32 v131,v12,v13
v_mfma_f32_16x16x32_bf16 a[28:31],v[32:35],v[72:75],a[28:31]
buffer_store_dwordx2 v[130:131],v40,s[20:23],s43 offen offset:32
v_mfma_f32_16x16x32_bf16 a[28:31],v[32:35],v[88:91],a[28:31]
v_accvgpr_read_b32 v136,a236
v_accvgpr_read_b32 v137,a237
v_mfma_f32_16x16x32_bf16 a[28:31],v[32:35],v[104:107],a[28:31]
v_cvt_pk_bf16_f32 v136,v136,v137
v_accvgpr_read_b32 v138,a238
v_mfma_f32_16x16x32_bf16 a[44:47],v[34:37],v[76:79],a[44:47]
v_accvgpr_read_b32 v139,a239
v_cvt_pk_bf16_f32 v137,v138,v139
v_mfma_f32_16x16x32_bf16 a[44:47],v[34:37],v[92:95],a[44:47]
s_waitcnt vmcnt(2)
buffer_store_dwordx2 v[136:137],v40,s[20:23],s43 offen offset:64
v_mfma_f32_16x16x32_bf16 a[44:47],v[34:37],v[108:111],a[44:47]
v_accvgpr_read_b32 v144,a12
v_accvgpr_read_b32 v145,a13
v_cvt_pk_bf16_f32 v144,v144,v145
v_accvgpr_read_b32 v146,a14
v_accvgpr_read_b32 v147,a15
v_mfma_f32_16x16x32_bf16 a[60:63],v[32:35],v[76:79],a[60:63]
v_accvgpr_read_b32 v138,a252
v_accvgpr_read_b32 v139,a253
v_cvt_pk_bf16_f32 v145,v146,v147
v_mfma_f32_16x16x32_bf16 a[60:63],v[32:35],v[92:95],a[60:63]
v_accvgpr_read_b32 v14,a254
v_accvgpr_read_b32 v15,a255
v_cvt_pk_bf16_f32 v138,v138,v139
v_cvt_pk_bf16_f32 v139,v14,v15
v_mfma_f32_16x16x32_bf16 a[60:63],v[32:35],v[108:111],a[60:63]
buffer_store_dwordx2 v[138:139],v40,s[20:23],s43 offen offset:96
buffer_store_dwordx2 v[144:145],v40,s[20:23],s43 offen offset:128
v_accvgpr_read_b32 v146,a28
v_accvgpr_read_b32 v147,a29
v_accvgpr_read_b32 v16,a30
v_accvgpr_read_b32 v17,a31
v_cvt_pk_bf16_f32 v146,v146,v147
v_cvt_pk_bf16_f32 v147,v16,v17
s_nop 3
buffer_store_dwordx2 v[146:147],v40,s[20:23],s43 offen offset:160
v_accvgpr_read_b32 v160,a44
v_accvgpr_read_b32 v161,a45
v_cvt_pk_bf16_f32 v160,v160,v161
v_accvgpr_read_b32 v162,a46
v_accvgpr_read_b32 v163,a47
v_cvt_pk_bf16_f32 v161,v162,v163
buffer_store_dwordx2 v[160:161],v40,s[20:23],s43 offen offset:192
v_accvgpr_read_b32 v162,a60
v_accvgpr_read_b32 v163,a61
v_accvgpr_read_b32 v12,a62
v_accvgpr_read_b32 v13,a63
v_cvt_pk_bf16_f32 v162,v162,v163
v_cvt_pk_bf16_f32 v163,v12,v13
buffer_store_dwordx2 v[162:163],v40,s[20:23],s43 offen offset:224
v_mfma_f32_16x16x32_bf16 a[76:79],v[34:37],v[132:135],a[76:79]
v_mfma_f32_16x16x32_bf16 a[76:79],v[34:37],v[164:167],a[76:79]
v_mfma_f32_16x16x32_bf16 a[76:79],v[34:37],v[196:199],a[76:79]
v_mfma_f32_16x16x32_bf16 a[92:95],v[32:35],v[132:135],a[92:95]
v_mfma_f32_16x16x32_bf16 a[92:95],v[32:35],v[164:167],a[92:95]
v_mfma_f32_16x16x32_bf16 a[92:95],v[32:35],v[196:199],a[92:95]
s_nop 3
s_nop 0
v_accvgpr_read_b32 v168,a76
v_accvgpr_read_b32 v169,a77
v_cvt_pk_bf16_f32 v168,v168,v169
v_accvgpr_read_b32 v170,a78
v_accvgpr_read_b32 v171,a79
v_cvt_pk_bf16_f32 v169,v170,v171
buffer_store_dwordx2 v[168:169],v40,s[20:23],s43 offen offset:256
v_accvgpr_read_b32 v170,a92
v_accvgpr_read_b32 v171,a93
v_accvgpr_read_b32 v14,a94
v_accvgpr_read_b32 v15,a95
v_cvt_pk_bf16_f32 v170,v170,v171
v_cvt_pk_bf16_f32 v171,v14,v15
buffer_store_dwordx2 v[170:171],v40,s[20:23],s43 offen offset:288
s_or_b32 s62,s42,s40
s_cmp_lg_u32 s62,0
s_cbranch_scc1 L_noep_%=
v_mov_b32 v12,s68
s_mov_b64 exec,1
buffer_store_dword v12,v25,s[64:67],0 offen sc1
s_mov_b64 exec,-1
L_noep_%=:
s_waitcnt vmcnt(0) lgkmcnt(0)
s_branch L_end_%=
L_ps1_%=:
s_min_i32 s79,s79,40
s_add_u32 s78,s78,s79
L_pl1_%=:
s_sleep 1
s_sub_u32 s79,s79,4
s_cmp_gt_i32 s79,1
s_cbranch_scc1 L_pl1_%=
s_branch L_pr1_%=
L_ps2_%=:
s_min_i32 s79,s79,40
s_add_u32 s78,s78,s79
L_pl2_%=:
s_sleep 1
s_sub_u32 s79,s79,4
s_cmp_gt_i32 s79,1
s_cbranch_scc1 L_pl2_%=
s_branch L_pr2_%=
L_ps3_%=:
s_min_i32 s79,s79,40
s_add_u32 s78,s78,s79
L_pl3_%=:
s_sleep 1
s_sub_u32 s79,s79,4
s_cmp_gt_i32 s79,1
s_cbranch_scc1 L_pl3_%=
s_branch L_pr3_%=
L_ps4_%=:
s_min_i32 s79,s79,40
s_add_u32 s78,s78,s79
L_pl4_%=:
s_sleep 1
s_sub_u32 s79,s79,4
s_cmp_gt_i32 s79,1
s_cbranch_scc1 L_pl4_%=
s_branch L_pr4_%=
L_ps5_%=:
s_min_i32 s79,s79,40
s_add_u32 s78,s78,s79
L_pl5_%=:
s_sleep 1
s_sub_u32 s79,s79,4
s_cmp_gt_i32 s79,1
s_cbranch_scc1 L_pl5_%=
s_branch L_pr5_%=
L_ps6_%=:
s_min_i32 s79,s79,40
s_add_u32 s78,s78,s79
L_pl6_%=:
s_sleep 1
s_sub_u32 s79,s79,4
s_cmp_gt_i32 s79,1
s_cbranch_scc1 L_pl6_%=
s_branch L_pr6_%=
L_fr7_%=:
s_mov_b32 s88,262144
L_fl7_%=:
buffer_load_dwordx2 v[26:27],v24,s[64:67],s94 offen sc1
s_waitcnt vmcnt(0)
s_sub_u32 s88,s88,1
s_cmp_eq_u32 s88,0
s_cbranch_scc1 L_fo7_%=
v_cmp_ne_u32 s[90:91],s68,v26
v_cmp_ne_u32 s[92:93],s69,v27
s_nop 0
s_or_b64 s[90:91],s[90:91],s[92:93]
s_cbranch_scc1 L_fl7_%=
s_branch L_fo7_%=
L_fr8_%=:
s_mov_b32 s88,262144
L_fl8_%=:
buffer_load_dwordx2 v[26:27],v24,s[64:67],s94 offen offset:1024 sc1
s_waitcnt vmcnt(0)
s_sub_u32 s88,s88,1
s_cmp_eq_u32 s88,0
s_cbranch_scc1 L_fo8_%=
v_cmp_ne_u32 s[90:91],s68,v26
v_cmp_ne_u32 s[92:93],s69,v27
s_nop 0
s_or_b64 s[90:91],s[90:91],s[92:93]
s_cbranch_scc1 L_fl8_%=
s_branch L_fo8_%=
L_fr9_%=:
s_mov_b32 s88,262144
L_fl9_%=:
buffer_load_dwordx2 v[26:27],v24,s[64:67],s94 offen offset:2048 sc1
s_waitcnt vmcnt(0)
s_sub_u32 s88,s88,1
s_cmp_eq_u32 s88,0
s_cbranch_scc1 L_fo9_%=
v_cmp_ne_u32 s[90:91],s68,v26
v_cmp_ne_u32 s[92:93],s69,v27
s_nop 0
s_or_b64 s[90:91],s[90:91],s[92:93]
s_cbranch_scc1 L_fl9_%=
s_branch L_fo9_%=
L_fr10_%=:
s_mov_b32 s88,262144
L_fl10_%=:
buffer_load_dwordx2 v[26:27],v24,s[64:67],s94 offen offset:3072 sc1
s_waitcnt vmcnt(0)
s_sub_u32 s88,s88,1
s_cmp_eq_u32 s88,0
s_cbranch_scc1 L_fo10_%=
v_cmp_ne_u32 s[90:91],s68,v26
v_cmp_ne_u32 s[92:93],s69,v27
s_nop 0
s_or_b64 s[90:91],s[90:91],s[92:93]
s_cbranch_scc1 L_fl10_%=
s_branch L_fo10_%=
L_fast_end_%=:
L_slow0_%=:
s_mov_b32 s98,0
s_branch L_slowc_%=
L_slow1_%=:
s_mov_b32 s98,1
L_slowc_%=:
s_nop 7
s_nop 7
s_waitcnt vmcnt(0) lgkmcnt(0)
s_lshl_b32 s96,%1,2
s_add_u32 s96,s96,s40
s_lshl_b32 s96,s96,15
s_add_u32 s96,s96,167772160
s_mov_b64 s[56:57],s[12:13]
s_add_u32 s56,s56,s96
s_addc_u32 s57,s57,0
s_and_b32 s57,s57,0xffff
s_mov_b32 s58,32768
s_mov_b32 s59,0x20000
s_mov_b32 s96,0
buffer_store_dwordx4 v[64:67],v6,s[56:59],s96 offen
buffer_store_dwordx4 v[68:71],v6,s[56:59],s96 offen offset:1024
buffer_store_dwordx4 v[72:75],v6,s[56:59],s96 offen offset:2048
buffer_store_dwordx4 v[76:79],v6,s[56:59],s96 offen offset:3072
s_mov_b32 s96,4096
buffer_store_dwordx4 v[80:83],v6,s[56:59],s96 offen
buffer_store_dwordx4 v[84:87],v6,s[56:59],s96 offen offset:1024
buffer_store_dwordx4 v[88:91],v6,s[56:59],s96 offen offset:2048
buffer_store_dwordx4 v[92:95],v6,s[56:59],s96 offen offset:3072
s_mov_b32 s96,8192
buffer_store_dwordx4 v[96:99],v6,s[56:59],s96 offen
buffer_store_dwordx4 v[100:103],v6,s[56:59],s96 offen offset:1024
buffer_store_dwordx4 v[104:107],v6,s[56:59],s96 offen offset:2048
buffer_store_dwordx4 v[108:111],v6,s[56:59],s96 offen offset:3072
s_mov_b32 s96,12288
buffer_store_dwordx4 v[112:115],v6,s[56:59],s96 offen
buffer_store_dwordx4 v[116:119],v6,s[56:59],s96 offen offset:1024
buffer_store_dwordx4 v[120:123],v6,s[56:59],s96 offen offset:2048
buffer_store_dwordx4 v[124:127],v6,s[56:59],s96 offen offset:3072
s_mov_b32 s96,16384
buffer_store_dwordx4 v[128:131],v6,s[56:59],s96 offen
buffer_store_dwordx4 v[132:135],v6,s[56:59],s96 offen offset:1024
buffer_store_dwordx4 v[136:139],v6,s[56:59],s96 offen offset:2048
buffer_store_dwordx4 v[140:143],v6,s[56:59],s96 offen offset:3072
s_mov_b32 s96,20480
buffer_store_dwordx4 v[144:147],v6,s[56:59],s96 offen
buffer_store_dwordx4 v[148:151],v6,s[56:59],s96 offen offset:1024
buffer_store_dwordx4 v[152:155],v6,s[56:59],s96 offen offset:2048
buffer_store_dwordx4 v[156:159],v6,s[56:59],s96 offen offset:3072
s_mov_b32 s96,24576
buffer_store_dwordx4 v[160:163],v6,s[56:59],s96 offen
buffer_store_dwordx4 v[164:167],v6,s[56:59],s96 offen offset:1024
buffer_store_dwordx4 v[168:171],v6,s[56:59],s96 offen offset:2048
buffer_store_dwordx4 v[172:175],v6,s[56:59],s96 offen offset:3072
s_mov_b32 s96,28672
buffer_store_dwordx4 v[176:179],v6,s[56:59],s96 offen
buffer_store_dwordx4 v[180:183],v6,s[56:59],s96 offen offset:1024
)A"
R"A(buffer_store_dwordx4 v[184:187],v6,s[56:59],s96 offen offset:2048
buffer_store_dwordx4 v[188:191],v6,s[56:59],s96 offen offset:3072
s_waitcnt vmcnt(0)
v_writelane_b32 v173,s88,0
v_writelane_b32 v173,s89,1
v_writelane_b32 v173,s90,2
v_writelane_b32 v173,s91,3
v_writelane_b32 v173,s92,4
v_writelane_b32 v173,s93,5
v_writelane_b32 v173,s94,6
v_writelane_b32 v173,s95,7
s_mov_b32 s88,s98
s_mul_i32 s90,s41,307200
s_mov_b64 s[92:93],s[4:5]
s_add_u32 s92,s92,s90
s_addc_u32 s93,s93,0
s_and_b32 s93,s93,0xffff
s_mov_b32 s94,307200
s_mov_b32 s95,0x20000
s_mov_b64 s[96:97],s[10:11]
s_and_b32 s97,s97,0xffff
s_mov_b32 s98,5242880
s_mov_b32 s99,0x20000
v_bfe_u32 v171,v6,4,4
v_lshrrev_b32 v172,8,v6
v_lshlrev_b32 v164,8,v171
v_lshl_add_u32 v164,v172,4,v164
v_lshlrev_b32 v165,10,v172
v_lshl_add_u32 v165,v171,1,v165
v_add_u32 v166,4096,v165
v_lshlrev_b32 v168,16,v172
v_lshrrev_b32 v170,2,v171
v_lshl_add_u32 v168,v170,4,v168
v_and_b32 v170,3,v171
v_lshl_add_u32 v168,v170,1,v168
s_lshl_b32 s90,s42,1
s_add_u32 s90,s90,s88
s_lshl_b32 s90,s90,8
v_add_u32 v168,s90,v168
s_mov_b32 s89,0
L_sjb_%=:
s_add_u32 s90,s89,s70
s_add_u32 s90,s90,1
s_and_b32 s90,s90,3
s_lshl_b32 s91,s40,2
s_add_u32 s90,s90,s91
s_lshl_b32 s90,s90,4
v_bfe_u32 v171,v6,4,4
v_lshrrev_b32 v172,8,v6
v_add_u32 v167,s90,v171
v_mul_u32_u24 v167,1280,v167
v_lshl_add_u32 v167,v172,4,v167
s_lshl_b32 s90,s88,4
s_add_u32 s90,s90,s89
s_lshl_b32 s90,s90,12
v_add_u32 v169,s90,v6
v_mov_b32 v64,0
v_mov_b32 v65,0
v_mov_b32 v66,0
v_mov_b32 v67,0
v_mov_b32 v68,0
v_mov_b32 v69,0
v_mov_b32 v70,0
v_mov_b32 v71,0
v_mov_b32 v72,0
v_mov_b32 v73,0
v_mov_b32 v74,0
v_mov_b32 v75,0
v_mov_b32 v76,0
v_mov_b32 v77,0
v_mov_b32 v78,0
v_mov_b32 v79,0
v_mov_b32 v80,0
v_mov_b32 v81,0
v_mov_b32 v82,0
v_mov_b32 v83,0
v_mov_b32 v84,0
v_mov_b32 v85,0
v_mov_b32 v86,0
v_mov_b32 v87,0
v_mov_b32 v88,0
v_mov_b32 v89,0
v_mov_b32 v90,0
v_mov_b32 v91,0
v_mov_b32 v92,0
v_mov_b32 v93,0
v_mov_b32 v94,0
v_mov_b32 v95,0
s_mov_b32 s90,0
L_sq_%=:
s_lshl_b32 s91,s90,6
buffer_load_dwordx4 v[160:163],v167,s[92:95],s91 offen
s_lshl_b32 s91,s90,18
buffer_load_ushort v112,v168,s[96:99],s91 offen
buffer_load_ushort v120,v168,s[96:99],s91 offen offset:8
buffer_load_ushort v128,v168,s[96:99],s91 offen offset:64
buffer_load_ushort v136,v168,s[96:99],s91 offen offset:72
s_add_u32 s91,s91,8192
buffer_load_ushort v113,v168,s[96:99],s91 offen
buffer_load_ushort v121,v168,s[96:99],s91 offen offset:8
buffer_load_ushort v129,v168,s[96:99],s91 offen offset:64
buffer_load_ushort v137,v168,s[96:99],s91 offen offset:72
s_add_u32 s91,s91,8192
buffer_load_ushort v114,v168,s[96:99],s91 offen
buffer_load_ushort v122,v168,s[96:99],s91 offen offset:8
buffer_load_ushort v130,v168,s[96:99],s91 offen offset:64
buffer_load_ushort v138,v168,s[96:99],s91 offen offset:72
s_add_u32 s91,s91,8192
buffer_load_ushort v115,v168,s[96:99],s91 offen
buffer_load_ushort v123,v168,s[96:99],s91 offen offset:8
buffer_load_ushort v131,v168,s[96:99],s91 offen offset:64
buffer_load_ushort v139,v168,s[96:99],s91 offen offset:72
s_add_u32 s91,s91,8192
buffer_load_ushort v116,v168,s[96:99],s91 offen
buffer_load_ushort v124,v168,s[96:99],s91 offen offset:8
buffer_load_ushort v132,v168,s[96:99],s91 offen offset:64
buffer_load_ushort v140,v168,s[96:99],s91 offen offset:72
s_add_u32 s91,s91,8192
buffer_load_ushort v117,v168,s[96:99],s91 offen
buffer_load_ushort v125,v168,s[96:99],s91 offen offset:8
buffer_load_ushort v133,v168,s[96:99],s91 offen offset:64
buffer_load_ushort v141,v168,s[96:99],s91 offen offset:72
s_add_u32 s91,s91,8192
buffer_load_ushort v118,v168,s[96:99],s91 offen
buffer_load_ushort v126,v168,s[96:99],s91 offen offset:8
buffer_load_ushort v134,v168,s[96:99],s91 offen offset:64
buffer_load_ushort v142,v168,s[96:99],s91 offen offset:72
s_add_u32 s91,s91,8192
buffer_load_ushort v119,v168,s[96:99],s91 offen
buffer_load_ushort v127,v168,s[96:99],s91 offen offset:8
buffer_load_ushort v135,v168,s[96:99],s91 offen offset:64
buffer_load_ushort v143,v168,s[96:99],s91 offen offset:72
s_add_u32 s91,s91,8192
s_waitcnt vmcnt(0)
v_lshl_or_b32 v112,v113,16,v112
v_lshl_or_b32 v113,v115,16,v114
v_lshl_or_b32 v114,v117,16,v116
v_lshl_or_b32 v115,v119,16,v118
v_lshl_or_b32 v120,v121,16,v120
v_lshl_or_b32 v121,v123,16,v122
v_lshl_or_b32 v122,v125,16,v124
v_lshl_or_b32 v123,v127,16,v126
v_lshl_or_b32 v128,v129,16,v128
v_lshl_or_b32 v129,v131,16,v130
v_lshl_or_b32 v130,v133,16,v132
v_lshl_or_b32 v131,v135,16,v134
v_lshl_or_b32 v136,v137,16,v136
v_lshl_or_b32 v137,v139,16,v138
v_lshl_or_b32 v138,v141,16,v140
v_lshl_or_b32 v139,v143,16,v142
s_nop 1
v_mfma_f32_16x16x32_bf16 v[64:67],v[112:115],v[160:163],v[64:67]
v_mfma_f32_16x16x32_bf16 v[68:71],v[120:123],v[160:163],v[68:71]
v_mfma_f32_16x16x32_bf16 v[72:75],v[128:131],v[160:163],v[72:75]
v_mfma_f32_16x16x32_bf16 v[76:79],v[136:139],v[160:163],v[76:79]
s_nop 7
s_lshl_b32 s91,s90,18
buffer_load_ushort v112,v168,s[96:99],s91 offen offset:128
buffer_load_ushort v120,v168,s[96:99],s91 offen offset:136
buffer_load_ushort v128,v168,s[96:99],s91 offen offset:192
buffer_load_ushort v136,v168,s[96:99],s91 offen offset:200
s_add_u32 s91,s91,8192
buffer_load_ushort v113,v168,s[96:99],s91 offen offset:128
buffer_load_ushort v121,v168,s[96:99],s91 offen offset:136
buffer_load_ushort v129,v168,s[96:99],s91 offen offset:192
buffer_load_ushort v137,v168,s[96:99],s91 offen offset:200
s_add_u32 s91,s91,8192
buffer_load_ushort v114,v168,s[96:99],s91 offen offset:128
buffer_load_ushort v122,v168,s[96:99],s91 offen offset:136
buffer_load_ushort v130,v168,s[96:99],s91 offen offset:192
buffer_load_ushort v138,v168,s[96:99],s91 offen offset:200
s_add_u32 s91,s91,8192
buffer_load_ushort v115,v168,s[96:99],s91 offen offset:128
buffer_load_ushort v123,v168,s[96:99],s91 offen offset:136
buffer_load_ushort v131,v168,s[96:99],s91 offen offset:192
buffer_load_ushort v139,v168,s[96:99],s91 offen offset:200
s_add_u32 s91,s91,8192
buffer_load_ushort v116,v168,s[96:99],s91 offen offset:128
buffer_load_ushort v124,v168,s[96:99],s91 offen offset:136
buffer_load_ushort v132,v168,s[96:99],s91 offen offset:192
buffer_load_ushort v140,v168,s[96:99],s91 offen offset:200
s_add_u32 s91,s91,8192
buffer_load_ushort v117,v168,s[96:99],s91 offen offset:128
buffer_load_ushort v125,v168,s[96:99],s91 offen offset:136
buffer_load_ushort v133,v168,s[96:99],s91 offen offset:192
buffer_load_ushort v141,v168,s[96:99],s91 offen offset:200
s_add_u32 s91,s91,8192
buffer_load_ushort v118,v168,s[96:99],s91 offen offset:128
buffer_load_ushort v126,v168,s[96:99],s91 offen offset:136
buffer_load_ushort v134,v168,s[96:99],s91 offen offset:192
buffer_load_ushort v142,v168,s[96:99],s91 offen offset:200
s_add_u32 s91,s91,8192
buffer_load_ushort v119,v168,s[96:99],s91 offen offset:128
buffer_load_ushort v127,v168,s[96:99],s91 offen offset:136
buffer_load_ushort v135,v168,s[96:99],s91 offen offset:192
buffer_load_ushort v143,v168,s[96:99],s91 offen offset:200
s_add_u32 s91,s91,8192
s_waitcnt vmcnt(0)
v_lshl_or_b32 v112,v113,16,v112
v_lshl_or_b32 v113,v115,16,v114
v_lshl_or_b32 v114,v117,16,v116
v_lshl_or_b32 v115,v119,16,v118
v_lshl_or_b32 v120,v121,16,v120
v_lshl_or_b32 v121,v123,16,v122
v_lshl_or_b32 v122,v125,16,v124
v_lshl_or_b32 v123,v127,16,v126
v_lshl_or_b32 v128,v129,16,v128
v_lshl_or_b32 v129,v131,16,v130
v_lshl_or_b32 v130,v133,16,v132
v_lshl_or_b32 v131,v135,16,v134
v_lshl_or_b32 v136,v137,16,v136
v_lshl_or_b32 v137,v139,16,v138
v_lshl_or_b32 v138,v141,16,v140
v_lshl_or_b32 v139,v143,16,v142
s_nop 1
v_mfma_f32_16x16x32_bf16 v[80:83],v[112:115],v[160:163],v[80:83]
v_mfma_f32_16x16x32_bf16 v[84:87],v[120:123],v[160:163],v[84:87]
v_mfma_f32_16x16x32_bf16 v[88:91],v[128:131],v[160:163],v[88:91]
v_mfma_f32_16x16x32_bf16 v[92:95],v[136:139],v[160:163],v[92:95]
s_nop 7
s_add_u32 s90,s90,1
s_cmp_lg_u32 s90,20
s_cbranch_scc1 L_sq_%=
s_nop 7
v_mul_f32 v64,s61,v64
v_mul_f32 v65,s61,v65
v_mul_f32 v66,s61,v66
v_mul_f32 v67,s61,v67
v_mul_f32 v68,s61,v68
v_mul_f32 v69,s61,v69
v_mul_f32 v70,s61,v70
v_mul_f32 v71,s61,v71
v_mul_f32 v72,s61,v72
v_mul_f32 v73,s61,v73
v_mul_f32 v74,s61,v74
v_mul_f32 v75,s61,v75
v_mul_f32 v76,s61,v76
v_mul_f32 v77,s61,v77
v_mul_f32 v78,s61,v78
v_mul_f32 v79,s61,v79
v_mul_f32 v80,s61,v80
v_mul_f32 v81,s61,v81
v_mul_f32 v82,s61,v82
v_mul_f32 v83,s61,v83
v_mul_f32 v84,s61,v84
v_mul_f32 v85,s61,v85
v_mul_f32 v86,s61,v86
v_mul_f32 v87,s61,v87
v_mul_f32 v88,s61,v88
v_mul_f32 v89,s61,v89
v_mul_f32 v90,s61,v90
v_mul_f32 v91,s61,v91
v_mul_f32 v92,s61,v92
v_mul_f32 v93,s61,v93
v_mul_f32 v94,s61,v94
v_mul_f32 v95,s61,v95
v_cvt_pk_bf16_f32 v96,v64,v65
v_cvt_pk_bf16_f32 v97,v66,v67
v_cvt_pk_bf16_f32 v98,v68,v69
v_cvt_pk_bf16_f32 v99,v70,v71
v_cvt_pk_bf16_f32 v100,v72,v73
v_cvt_pk_bf16_f32 v101,v74,v75
v_cvt_pk_bf16_f32 v102,v76,v77
v_cvt_pk_bf16_f32 v103,v78,v79
v_cvt_pk_bf16_f32 v104,v80,v81
v_cvt_pk_bf16_f32 v105,v82,v83
v_cvt_pk_bf16_f32 v106,v84,v85
v_cvt_pk_bf16_f32 v107,v86,v87
v_cvt_pk_bf16_f32 v108,v88,v89
v_cvt_pk_bf16_f32 v109,v90,v91
v_cvt_pk_bf16_f32 v110,v92,v93
v_cvt_pk_bf16_f32 v111,v94,v95
v_mov_b32 v170,0xff800000
s_lshl_b32 s91,s88,18
s_mov_b32 s90,64
L_s1_%=:
buffer_load_dwordx4 v[112:115],v164,s[20:23],s91 offen
buffer_load_dwordx4 v[116:119],v164,s[20:23],s91 offen offset:64
buffer_load_dwordx4 v[120:123],v164,s[20:23],s91 offen offset:128
buffer_load_dwordx4 v[124:127],v164,s[20:23],s91 offen offset:192
s_waitcnt vmcnt(0)
v_mfma_f32_16x16x32_bf16 v[144:147],v[112:115],v[96:99],0
v_mfma_f32_16x16x32_bf16 v[144:147],v[116:119],v[100:103],v[144:147]
v_mfma_f32_16x16x32_bf16 v[144:147],v[120:123],v[104:107],v[144:147]
v_mfma_f32_16x16x32_bf16 v[144:147],v[124:127],v[108:111],v[144:147]
s_nop 7
v_max3_f32 v170,v170,v144,v145
v_max3_f32 v170,v170,v146,v147
s_add_u32 s91,s91,4096
s_sub_u32 s90,s90,1
s_cmp_lg_u32 s90,0
s_cbranch_scc1 L_s1_%=
v_mov_b32 v171,v170
s_nop 1
v_permlane32_swap_b32 v170,v171
v_max_f32 v170,v170,v171
v_mov_b32 v171,v170
s_nop 1
v_permlane16_swap_b32 v170,v171
v_max_f32 v170,v170,v171
v_sub_f32 v152,0,v170
v_mov_b32 v156,0
v_sub_f32 v153,0,v170
v_mov_b32 v157,0
v_sub_f32 v154,0,v170
v_mov_b32 v158,0
v_sub_f32 v155,0,v170
v_mov_b32 v159,0
v_mov_b32 v64,0
v_mov_b32 v65,0
v_mov_b32 v66,0
v_mov_b32 v67,0
v_mov_b32 v68,0
v_mov_b32 v69,0
v_mov_b32 v70,0
v_mov_b32 v71,0
v_mov_b32 v72,0
v_mov_b32 v73,0
v_mov_b32 v74,0
v_mov_b32 v75,0
v_mov_b32 v76,0
v_mov_b32 v77,0
v_mov_b32 v78,0
v_mov_b32 v79,0
v_mov_b32 v80,0
v_mov_b32 v81,0
v_mov_b32 v82,0
v_mov_b32 v83,0
v_mov_b32 v84,0
v_mov_b32 v85,0
v_mov_b32 v86,0
v_mov_b32 v87,0
v_mov_b32 v88,0
v_mov_b32 v89,0
v_mov_b32 v90,0
v_mov_b32 v91,0
v_mov_b32 v92,0
v_mov_b32 v93,0
v_mov_b32 v94,0
v_mov_b32 v95,0
s_lshl_b32 s91,s88,18
s_mov_b32 s90,32
v_add_u32 v172,4096,v164
L_s2_%=:
buffer_load_dwordx4 v[112:115],v164,s[20:23],s91 offen
buffer_load_dwordx4 v[116:119],v164,s[20:23],s91 offen offset:64
buffer_load_dwordx4 v[120:123],v164,s[20:23],s91 offen offset:128
buffer_load_dwordx4 v[124:127],v164,s[20:23],s91 offen offset:192
buffer_load_dwordx4 v[128:131],v172,s[20:23],s91 offen
buffer_load_dwordx4 v[132:135],v172,s[20:23],s91 offen offset:64
buffer_load_dwordx4 v[136:139],v172,s[20:23],s91 offen offset:128
buffer_load_dwordx4 v[140:143],v172,s[20:23],s91 offen offset:192
s_waitcnt vmcnt(0)
v_mfma_f32_16x16x32_bf16 v[144:147],v[112:115],v[96:99],v[152:155]
v_mfma_f32_16x16x32_bf16 v[144:147],v[116:119],v[100:103],v[144:147]
v_mfma_f32_16x16x32_bf16 v[144:147],v[120:123],v[104:107],v[144:147]
v_mfma_f32_16x16x32_bf16 v[144:147],v[124:127],v[108:111],v[144:147]
v_mfma_f32_16x16x32_bf16 v[148:151],v[128:131],v[96:99],v[152:155]
v_mfma_f32_16x16x32_bf16 v[148:151],v[132:135],v[100:103],v[148:151]
v_mfma_f32_16x16x32_bf16 v[148:151],v[136:139],v[104:107],v[148:151]
v_mfma_f32_16x16x32_bf16 v[148:151],v[140:143],v[108:111],v[148:151]
s_nop 7
v_exp_f32 v144,v144
v_exp_f32 v145,v145
v_exp_f32 v146,v146
v_exp_f32 v147,v147
v_exp_f32 v148,v148
v_exp_f32 v149,v149
v_exp_f32 v150,v150
v_exp_f32 v151,v151
s_nop 0
v_cvt_pk_bf16_f32 v144,v144,v145
v_cvt_pk_bf16_f32 v145,v146,v147
v_cvt_pk_bf16_f32 v146,v148,v149
)A"
R"A(v_cvt_pk_bf16_f32 v147,v150,v151
buffer_load_ushort v112,v165,s[24:27],s91 offen
buffer_load_ushort v113,v165,s[24:27],s91 offen offset:256
buffer_load_ushort v114,v165,s[24:27],s91 offen offset:512
buffer_load_ushort v115,v165,s[24:27],s91 offen offset:768
buffer_load_ushort v116,v166,s[24:27],s91 offen
buffer_load_ushort v117,v166,s[24:27],s91 offen offset:256
buffer_load_ushort v118,v166,s[24:27],s91 offen offset:512
buffer_load_ushort v119,v166,s[24:27],s91 offen offset:768
buffer_load_ushort v120,v165,s[24:27],s91 offen offset:32
buffer_load_ushort v121,v165,s[24:27],s91 offen offset:288
buffer_load_ushort v122,v165,s[24:27],s91 offen offset:544
buffer_load_ushort v123,v165,s[24:27],s91 offen offset:800
buffer_load_ushort v124,v166,s[24:27],s91 offen offset:32
buffer_load_ushort v125,v166,s[24:27],s91 offen offset:288
buffer_load_ushort v126,v166,s[24:27],s91 offen offset:544
buffer_load_ushort v127,v166,s[24:27],s91 offen offset:800
buffer_load_ushort v128,v165,s[24:27],s91 offen offset:64
buffer_load_ushort v129,v165,s[24:27],s91 offen offset:320
buffer_load_ushort v130,v165,s[24:27],s91 offen offset:576
buffer_load_ushort v131,v165,s[24:27],s91 offen offset:832
buffer_load_ushort v132,v166,s[24:27],s91 offen offset:64
buffer_load_ushort v133,v166,s[24:27],s91 offen offset:320
buffer_load_ushort v134,v166,s[24:27],s91 offen offset:576
buffer_load_ushort v135,v166,s[24:27],s91 offen offset:832
buffer_load_ushort v136,v165,s[24:27],s91 offen offset:96
buffer_load_ushort v137,v165,s[24:27],s91 offen offset:352
buffer_load_ushort v138,v165,s[24:27],s91 offen offset:608
buffer_load_ushort v139,v165,s[24:27],s91 offen offset:864
buffer_load_ushort v140,v166,s[24:27],s91 offen offset:96
buffer_load_ushort v141,v166,s[24:27],s91 offen offset:352
buffer_load_ushort v142,v166,s[24:27],s91 offen offset:608
buffer_load_ushort v143,v166,s[24:27],s91 offen offset:864
s_waitcnt vmcnt(0)
v_lshl_or_b32 v112,v113,16,v112
v_lshl_or_b32 v113,v115,16,v114
v_lshl_or_b32 v114,v117,16,v116
v_lshl_or_b32 v115,v119,16,v118
v_lshl_or_b32 v120,v121,16,v120
v_lshl_or_b32 v121,v123,16,v122
v_lshl_or_b32 v122,v125,16,v124
v_lshl_or_b32 v123,v127,16,v126
v_lshl_or_b32 v128,v129,16,v128
v_lshl_or_b32 v129,v131,16,v130
v_lshl_or_b32 v130,v133,16,v132
v_lshl_or_b32 v131,v135,16,v134
v_lshl_or_b32 v136,v137,16,v136
v_lshl_or_b32 v137,v139,16,v138
v_lshl_or_b32 v138,v141,16,v140
v_lshl_or_b32 v139,v143,16,v142
s_nop 1
v_mfma_f32_16x16x32_bf16 v[64:67],v[112:115],v[144:147],v[64:67]
v_mfma_f32_16x16x32_bf16 v[68:71],v[120:123],v[144:147],v[68:71]
v_mfma_f32_16x16x32_bf16 v[72:75],v[128:131],v[144:147],v[72:75]
v_mfma_f32_16x16x32_bf16 v[76:79],v[136:139],v[144:147],v[76:79]
s_nop 7
buffer_load_ushort v112,v165,s[24:27],s91 offen offset:128
buffer_load_ushort v113,v165,s[24:27],s91 offen offset:384
buffer_load_ushort v114,v165,s[24:27],s91 offen offset:640
buffer_load_ushort v115,v165,s[24:27],s91 offen offset:896
buffer_load_ushort v116,v166,s[24:27],s91 offen offset:128
buffer_load_ushort v117,v166,s[24:27],s91 offen offset:384
buffer_load_ushort v118,v166,s[24:27],s91 offen offset:640
buffer_load_ushort v119,v166,s[24:27],s91 offen offset:896
buffer_load_ushort v120,v165,s[24:27],s91 offen offset:160
buffer_load_ushort v121,v165,s[24:27],s91 offen offset:416
buffer_load_ushort v122,v165,s[24:27],s91 offen offset:672
buffer_load_ushort v123,v165,s[24:27],s91 offen offset:928
buffer_load_ushort v124,v166,s[24:27],s91 offen offset:160
buffer_load_ushort v125,v166,s[24:27],s91 offen offset:416
buffer_load_ushort v126,v166,s[24:27],s91 offen offset:672
buffer_load_ushort v127,v166,s[24:27],s91 offen offset:928
buffer_load_ushort v128,v165,s[24:27],s91 offen offset:192
buffer_load_ushort v129,v165,s[24:27],s91 offen offset:448
buffer_load_ushort v130,v165,s[24:27],s91 offen offset:704
buffer_load_ushort v131,v165,s[24:27],s91 offen offset:960
buffer_load_ushort v132,v166,s[24:27],s91 offen offset:192
buffer_load_ushort v133,v166,s[24:27],s91 offen offset:448
buffer_load_ushort v134,v166,s[24:27],s91 offen offset:704
buffer_load_ushort v135,v166,s[24:27],s91 offen offset:960
buffer_load_ushort v136,v165,s[24:27],s91 offen offset:224
buffer_load_ushort v137,v165,s[24:27],s91 offen offset:480
buffer_load_ushort v138,v165,s[24:27],s91 offen offset:736
buffer_load_ushort v139,v165,s[24:27],s91 offen offset:992
buffer_load_ushort v140,v166,s[24:27],s91 offen offset:224
buffer_load_ushort v141,v166,s[24:27],s91 offen offset:480
buffer_load_ushort v142,v166,s[24:27],s91 offen offset:736
buffer_load_ushort v143,v166,s[24:27],s91 offen offset:992
s_waitcnt vmcnt(0)
v_lshl_or_b32 v112,v113,16,v112
v_lshl_or_b32 v113,v115,16,v114
v_lshl_or_b32 v114,v117,16,v116
v_lshl_or_b32 v115,v119,16,v118
v_lshl_or_b32 v120,v121,16,v120
v_lshl_or_b32 v121,v123,16,v122
v_lshl_or_b32 v122,v125,16,v124
v_lshl_or_b32 v123,v127,16,v126
v_lshl_or_b32 v128,v129,16,v128
v_lshl_or_b32 v129,v131,16,v130
v_lshl_or_b32 v130,v133,16,v132
v_lshl_or_b32 v131,v135,16,v134
v_lshl_or_b32 v136,v137,16,v136
v_lshl_or_b32 v137,v139,16,v138
v_lshl_or_b32 v138,v141,16,v140
v_lshl_or_b32 v139,v143,16,v142
s_nop 1
v_mfma_f32_16x16x32_bf16 v[80:83],v[112:115],v[144:147],v[80:83]
v_mfma_f32_16x16x32_bf16 v[84:87],v[120:123],v[144:147],v[84:87]
v_mfma_f32_16x16x32_bf16 v[88:91],v[128:131],v[144:147],v[88:91]
v_mfma_f32_16x16x32_bf16 v[92:95],v[136:139],v[144:147],v[92:95]
s_nop 7
v_mfma_f32_16x16x32_bf16 v[156:159],v[28:31],v[144:147],v[156:159]
s_nop 7
s_add_u32 s91,s91,8192
s_sub_u32 s90,s90,1
s_cmp_lg_u32 s90,0
s_cbranch_scc1 L_s2_%=
v_rcp_f32 v170,v156
s_nop 0
v_mul_f32 v64,v170,v64
v_mul_f32 v65,v170,v65
v_mul_f32 v66,v170,v66
v_mul_f32 v67,v170,v67
v_mul_f32 v68,v170,v68
v_mul_f32 v69,v170,v69
v_mul_f32 v70,v170,v70
v_mul_f32 v71,v170,v71
v_mul_f32 v72,v170,v72
v_mul_f32 v73,v170,v73
v_mul_f32 v74,v170,v74
v_mul_f32 v75,v170,v75
v_mul_f32 v76,v170,v76
v_mul_f32 v77,v170,v77
v_mul_f32 v78,v170,v78
v_mul_f32 v79,v170,v79
v_mul_f32 v80,v170,v80
v_mul_f32 v81,v170,v81
v_mul_f32 v82,v170,v82
v_mul_f32 v83,v170,v83
v_mul_f32 v84,v170,v84
v_mul_f32 v85,v170,v85
v_mul_f32 v86,v170,v86
v_mul_f32 v87,v170,v87
v_mul_f32 v88,v170,v88
v_mul_f32 v89,v170,v89
v_mul_f32 v90,v170,v90
v_mul_f32 v91,v170,v91
v_mul_f32 v92,v170,v92
v_mul_f32 v93,v170,v93
v_mul_f32 v94,v170,v94
v_mul_f32 v95,v170,v95
v_cvt_pk_bf16_f32 v96,v64,v65
v_cvt_pk_bf16_f32 v97,v66,v67
v_cvt_pk_bf16_f32 v98,v68,v69
v_cvt_pk_bf16_f32 v99,v70,v71
v_cvt_pk_bf16_f32 v100,v72,v73
v_cvt_pk_bf16_f32 v101,v74,v75
v_cvt_pk_bf16_f32 v102,v76,v77
v_cvt_pk_bf16_f32 v103,v78,v79
v_cvt_pk_bf16_f32 v104,v80,v81
v_cvt_pk_bf16_f32 v105,v82,v83
v_cvt_pk_bf16_f32 v106,v84,v85
v_cvt_pk_bf16_f32 v107,v86,v87
v_cvt_pk_bf16_f32 v108,v88,v89
v_cvt_pk_bf16_f32 v109,v90,v91
v_cvt_pk_bf16_f32 v110,v92,v93
v_cvt_pk_bf16_f32 v111,v94,v95
buffer_store_dwordx4 v[96:99],v169,s[36:39],s55 offen sc1
buffer_store_dwordx4 v[100:103],v169,s[36:39],s55 offen offset:1024 sc1
buffer_store_dwordx4 v[104:107],v169,s[36:39],s55 offen offset:2048 sc1
buffer_store_dwordx4 v[108:111],v169,s[36:39],s55 offen offset:3072 sc1
s_waitcnt vmcnt(0)
s_add_u32 s89,s89,1
s_cmp_lg_u32 s89,4
s_cbranch_scc1 L_sjb_%=
s_mov_b32 s98,s88
v_readlane_b32 s88,v173,0
v_readlane_b32 s89,v173,1
v_readlane_b32 s90,v173,2
v_readlane_b32 s91,v173,3
v_readlane_b32 s92,v173,4
v_readlane_b32 s93,v173,5
v_readlane_b32 s94,v173,6
v_readlane_b32 s95,v173,7
s_nop 4
s_mov_b32 s96,0
buffer_load_dwordx4 v[64:67],v6,s[56:59],s96 offen sc1
buffer_load_dwordx4 v[68:71],v6,s[56:59],s96 offen offset:1024 sc1
buffer_load_dwordx4 v[72:75],v6,s[56:59],s96 offen offset:2048 sc1
buffer_load_dwordx4 v[76:79],v6,s[56:59],s96 offen offset:3072 sc1
s_mov_b32 s96,4096
buffer_load_dwordx4 v[80:83],v6,s[56:59],s96 offen sc1
buffer_load_dwordx4 v[84:87],v6,s[56:59],s96 offen offset:1024 sc1
buffer_load_dwordx4 v[88:91],v6,s[56:59],s96 offen offset:2048 sc1
buffer_load_dwordx4 v[92:95],v6,s[56:59],s96 offen offset:3072 sc1
s_mov_b32 s96,8192
buffer_load_dwordx4 v[96:99],v6,s[56:59],s96 offen sc1
buffer_load_dwordx4 v[100:103],v6,s[56:59],s96 offen offset:1024 sc1
buffer_load_dwordx4 v[104:107],v6,s[56:59],s96 offen offset:2048 sc1
buffer_load_dwordx4 v[108:111],v6,s[56:59],s96 offen offset:3072 sc1
s_mov_b32 s96,12288
buffer_load_dwordx4 v[112:115],v6,s[56:59],s96 offen sc1
buffer_load_dwordx4 v[116:119],v6,s[56:59],s96 offen offset:1024 sc1
buffer_load_dwordx4 v[120:123],v6,s[56:59],s96 offen offset:2048 sc1
buffer_load_dwordx4 v[124:127],v6,s[56:59],s96 offen offset:3072 sc1
s_mov_b32 s96,16384
buffer_load_dwordx4 v[128:131],v6,s[56:59],s96 offen sc1
buffer_load_dwordx4 v[132:135],v6,s[56:59],s96 offen offset:1024 sc1
buffer_load_dwordx4 v[136:139],v6,s[56:59],s96 offen offset:2048 sc1
buffer_load_dwordx4 v[140:143],v6,s[56:59],s96 offen offset:3072 sc1
s_mov_b32 s96,20480
buffer_load_dwordx4 v[144:147],v6,s[56:59],s96 offen sc1
buffer_load_dwordx4 v[148:151],v6,s[56:59],s96 offen offset:1024 sc1
buffer_load_dwordx4 v[152:155],v6,s[56:59],s96 offen offset:2048 sc1
buffer_load_dwordx4 v[156:159],v6,s[56:59],s96 offen offset:3072 sc1
s_mov_b32 s96,24576
buffer_load_dwordx4 v[160:163],v6,s[56:59],s96 offen sc1
buffer_load_dwordx4 v[164:167],v6,s[56:59],s96 offen offset:1024 sc1
buffer_load_dwordx4 v[168:171],v6,s[56:59],s96 offen offset:2048 sc1
buffer_load_dwordx4 v[172:175],v6,s[56:59],s96 offen offset:3072 sc1
s_mov_b32 s96,28672
buffer_load_dwordx4 v[176:179],v6,s[56:59],s96 offen sc1
buffer_load_dwordx4 v[180:183],v6,s[56:59],s96 offen offset:1024 sc1
buffer_load_dwordx4 v[184:187],v6,s[56:59],s96 offen offset:2048 sc1
buffer_load_dwordx4 v[188:191],v6,s[56:59],s96 offen offset:3072 sc1
s_waitcnt vmcnt(0)
s_cmp_lg_u32 s98,0
s_cbranch_scc1 L_sr1_%=
buffer_load_dwordx4 v[64:67],v6,s[36:39],s84 offen sc1
buffer_load_dwordx4 v[68:71],v6,s[36:39],s84 offen offset:1024 sc1
buffer_load_dwordx4 v[72:75],v6,s[36:39],s84 offen offset:2048 sc1
buffer_load_dwordx4 v[76:79],v6,s[36:39],s84 offen offset:3072 sc1
buffer_load_dwordx4 v[80:83],v6,s[36:39],s85 offen sc1
buffer_load_dwordx4 v[84:87],v6,s[36:39],s85 offen offset:1024 sc1
buffer_load_dwordx4 v[88:91],v6,s[36:39],s85 offen offset:2048 sc1
buffer_load_dwordx4 v[92:95],v6,s[36:39],s85 offen offset:3072 sc1
buffer_load_dwordx4 v[96:99],v6,s[36:39],s86 offen sc1
buffer_load_dwordx4 v[100:103],v6,s[36:39],s86 offen offset:1024 sc1
buffer_load_dwordx4 v[104:107],v6,s[36:39],s86 offen offset:2048 sc1
buffer_load_dwordx4 v[108:111],v6,s[36:39],s86 offen offset:3072 sc1
buffer_load_dwordx4 v[112:115],v6,s[36:39],s87 offen sc1
buffer_load_dwordx4 v[116:119],v6,s[36:39],s87 offen offset:1024 sc1
buffer_load_dwordx4 v[120:123],v6,s[36:39],s87 offen offset:2048 sc1
buffer_load_dwordx4 v[124:127],v6,s[36:39],s87 offen offset:3072 sc1
s_waitcnt vmcnt(0)
s_nop 7
s_branch L_sret0_%=
L_sr1_%=:
buffer_load_dwordx4 v[132:135],v6,s[36:39],s84 offen sc1
buffer_load_dwordx4 v[140:143],v6,s[36:39],s84 offen offset:1024 sc1
buffer_load_dwordx4 v[148:151],v6,s[36:39],s84 offen offset:2048 sc1
buffer_load_dwordx4 v[156:159],v6,s[36:39],s84 offen offset:3072 sc1
buffer_load_dwordx4 v[164:167],v6,s[36:39],s85 offen sc1
buffer_load_dwordx4 v[172:175],v6,s[36:39],s85 offen offset:1024 sc1
buffer_load_dwordx4 v[180:183],v6,s[36:39],s85 offen offset:2048 sc1
buffer_load_dwordx4 v[188:191],v6,s[36:39],s85 offen offset:3072 sc1
buffer_load_dwordx4 v[196:199],v6,s[36:39],s86 offen sc1
buffer_load_dwordx4 v[204:207],v6,s[36:39],s86 offen offset:1024 sc1
buffer_load_dwordx4 v[212:215],v6,s[36:39],s86 offen offset:2048 sc1
buffer_load_dwordx4 v[220:223],v6,s[36:39],s86 offen offset:3072 sc1
buffer_load_dwordx4 v[228:231],v6,s[36:39],s87 offen sc1
buffer_load_dwordx4 v[236:239],v6,s[36:39],s87 offen offset:1024 sc1
buffer_load_dwordx4 v[244:247],v6,s[36:39],s87 offen offset:2048 sc1
buffer_load_dwordx4 v[252:255],v6,s[36:39],s87 offen offset:3072 sc1
s_waitcnt vmcnt(0)
s_nop 7
s_branch L_sret1_%=
L_end_%=:
)A"
    :: "s"(karg), "s"(blk), "s"(wid)
    : "v0", "v1", "v2", "v3", "v4", "v5", "v6", "v7", "v8", "v9", "v10", "v11", "v12", "v13", "v14", "v15", "v16", "v17", "v18", "v19", "v20", "v21", "v22", "v23", "v24", "v25", "v26", "v27", "v28", "v29", "v30", "v31", "v32", "v33", "v34", "v35", "v36", "v37", "v38", "v39", "v40", "v41", "v42", "v43", "v44", "v45", "v46", "v47", "v48", "v49", "v50", "v51", "v52", "v53", "v54", "v55", "v56", "v57", "v58", "v59", "v60", "v61", "v62", "v63", "v64", "v65", "v66", "v67", "v68", "v69", "v70", "v71", "v72", "v73", "v74", "v75", "v76", "v77", "v78", "v79", "v80", "v81", "v82", "v83", "v84", "v85", "v86", "v87", "v88", "v89", "v90", "v91", "v92", "v93", "v94", "v95", "v96", "v97", "v98", "v99", "v100", "v101", "v102", "v103", "v104", "v105", "v106", "v107", "v108", "v109", "v110", "v111", "v112", "v113", "v114", "v115", "v116", "v117", "v118", "v119", "v120", "v121", "v122", "v123", "v124", "v125", "v126", "v127", "v128", "v129", "v130", "v131", "v132", "v133", "v134", "v135", "v136", "v137", "v138", "v139", "v140", "v141", "v142", "v143", "v144", "v145", "v146", "v147", "v148", "v149", "v150", "v151", "v152", "v153", "v154", "v155", "v156", "v157", "v158", "v159", "v160", "v161", "v162", "v163", "v164", "v165", "v166", "v167", "v168", "v169", "v170", "v171", "v172", "v173", "v174", "v175", "v176", "v177", "v178", "v179", "v180", "v181", "v182", "v183", "v184", "v185", "v186", "v187", "v188", "v189", "v190", "v191", "v192", "v193", "v194", "v195", "v196", "v197", "v198", "v199", "v200", "v201", "v202", "v203", "v204", "v205", "v206", "v207", "v208", "v209", "v210", "v211", "v212", "v213", "v214", "v215", "v216", "v217", "v218", "v219", "v220", "v221", "v222", "v223", "v224", "v225", "v226", "v227", "v228", "v229", "v230", "v231", "v232", "v233", "v234", "v235", "v236", "v237", "v238", "v239", "v240", "v241", "v242", "v243", "v244", "v245", "v246", "v247", "v248", "v249", "v250", "v251", "v252", "v253", "v254", "v255", "a0", "a1", "a2", "a3", "a4", "a5", "a6", "a7", "a8", "a9", "a10", "a11", "a12", "a13", "a14", "a15", "a16", "a17", "a18", "a19", "a20", "a21", "a22", "a23", "a24", "a25", "a26", "a27", "a28", "a29", "a30", "a31", "a32", "a33", "a34", "a35", "a36", "a37", "a38", "a39", "a40", "a41", "a42", "a43", "a44", "a45", "a46", "a47", "a48", "a49", "a50", "a51", "a52", "a53", "a54", "a55", "a56", "a57", "a58", "a59", "a60", "a61", "a62", "a63", "a64", "a65", "a66", "a67", "a68", "a69", "a70", "a71", "a72", "a73", "a74", "a75", "a76", "a77", "a78", "a79", "a80", "a81", "a82", "a83", "a84", "a85", "a86", "a87", "a88", "a89", "a90", "a91", "a92", "a93", "a94", "a95", "a96", "a97", "a98", "a99", "a100", "a101", "a102", "a103", "a104", "a105", "a106", "a107", "a108", "a109", "a110", "a111", "a112", "a113", "a114", "a115", "a116", "a117", "a118", "a119", "a120", "a121", "a122", "a123", "a124", "a125", "a126", "a127", "a128", "a129", "a130", "a131", "a132", "a133", "a134", "a135", "a136", "a137", "a138", "a139", "a140", "a141", "a142", "a143", "a144", "a145", "a146", "a147", "a148", "a149", "a150", "a151", "a152", "a153", "a154", "a155", "a156", "a157", "a158", "a159", "a160", "a161", "a162", "a163", "a164", "a165", "a166", "a167", "a168", "a169", "a170", "a171", "a172", "a173", "a174", "a175", "a176", "a177", "a178", "a179", "a180", "a181", "a182", "a183", "a184", "a185", "a186", "a187", "a188", "a189", "a190", "a191", "a192", "a193", "a194", "a195", "a196", "a197", "a198", "a199", "a200", "a201", "a202", "a203", "a204", "a205", "a206", "a207", "a208", "a209", "a210", "a211", "a212", "a213", "a214", "a215", "a216", "a217", "a218", "a219", "a220", "a221", "a222", "a223", "a224", "a225", "a226", "a227", "a228", "a229", "a230", "a231", "a232", "a233", "a234", "a235", "a236", "a237", "a238", "a239", "a240", "a241", "a242", "a243", "a244", "a245", "a246", "a247", "a248", "a249", "a250", "a251", "a252", "a253", "a254", "a255", "s4", "s5", "s6", "s7", "s8", "s9", "s10", "s11", "s12", "s13", "s14", "s15", "s16", "s17", "s18", "s19", "s20", "s21", "s22", "s23", "s24", "s25", "s26", "s27", "s28", "s29", "s30", "s31", "s32", "s33", "s34", "s35", "s36", "s37", "s38", "s39", "s40", "s41", "s42", "s43", "s44", "s45", "s46", "s47", "s48", "s49", "s50", "s51", "s52", "s53", "s54", "s55", "s56", "s57", "s58", "s59", "s60", "s61", "s62", "s63", "s64", "s65", "s66", "s67", "s68", "s69", "s70", "s71", "s72", "s73", "s74", "s75", "s76", "s77", "s78", "s79", "s80", "s81", "s82", "s83", "s84", "s85", "s86", "s87", "s88", "s89", "s90", "s91", "s92", "s93", "s94", "s95", "s96", "s97", "s98", "s99", "s100", "s101", "vcc", "scc", "memory");
#endif
}

extern "C" void kernel_launch(void* const* d_in, const int* in_sizes, int n_in,
                              void* d_out, int out_size, void* d_ws, size_t ws_size,
                              hipStream_t stream) {
  (void)hipFuncSetAttribute((const void*)qattn_kernel, hipFuncAttributeMaxDynamicSharedMemorySize, LDS_ATT);
  qattn_kernel<<<256, 256, LDS_ATT, stream>>>(d_in[0], d_in[1], d_in[2], d_in[3], d_ws, d_in[4], d_out);
}
```

```cpp
#include <hip/hip_runtime.h>
#include <hip/hip_bf16.h>

constexpr int LDS_ATT = 163840;

__global__ __launch_bounds__(256, 1) void qattn_kernel(const void* __restrict__ x, const void* __restrict__ kc, const void* __restrict__ vc,
                                                       const void* __restrict__ wq, void* __restrict__ ws, const void* __restrict__ wo,
                                                       void* __restrict__ out) {
#if defined(__HIP_DEVICE_COMPILE__)
  const auto karg = __builtin_amdgcn_kernarg_segment_ptr();
  const unsigned blk = blockIdx.x;
  const unsigned wid = __builtin_amdgcn_readfirstlane(threadIdx.x >> 6);
  asm volatile(
R"A(s_load_dwordx8 s[4:11],%0,0x0
s_load_dwordx4 s[12:15],%0,0x20
s_load_dwordx2 s[16:17],%0,0x30
s_mov_b32 s40,%2
s_mov_b32 s43,%1
s_lshr_b32 s45,s43,3
v_mbcnt_lo_u32_b32 v12,-1,0
v_mbcnt_hi_u32_b32 v12,-1,v12
s_getpc_b64 s[88:89]
L_pc_%=:
s_and_b32 s88,s88,0xffffff80
s_and_b32 s89,s89,0xffff
s_mov_b32 s90,L_fast_end_%=-L_pc_%=
s_mov_b32 s91,0x20000
s_lshl_b32 s44,s45,2
s_add_u32 s44,s44,s40
s_mul_i32 s44,s44,768
v_lshlrev_b32 v67,7,v12
s_mov_b64 exec,63
buffer_load_dword v19,v67,s[88:91],s44 offen
s_mov_b64 exec,-1
s_and_b32 s70,s43,3
s_and_b32 s71,s45,3
s_lshl_b32 s42,s70,2
s_add_u32 s42,s42,s71
s_and_b32 s41,s43,4
s_lshl_b32 s41,s41,1
s_lshr_b32 s46,s45,2
s_add_u32 s41,s41,s46
s_lshl_b32 s56,s42,9
s_mov_b32 s57,0
s_lshl_b32 s59,s40,13
s_add_u32 s58,s59,65536
v_and_b32 v64,3,v12
v_bfe_u32 v65,v12,2,2
v_lshrrev_b32 v66,4,v12
v_lshrrev_b32 v75,3,v12
s_lshl_b32 s44,s40,6
v_add_u32 v75,s44,v75
v_mul_u32_u24 v75,1280,v75
v_and_b32 v76,7,v12
s_add_u32 s84,s70,1
s_and_b32 s84,s84,3
s_mul_i32 s84,s84,20480
s_add_u32 s84,s84,4096
s_add_u32 s85,s70,2
s_and_b32 s85,s85,3
s_mul_i32 s85,s85,20480
s_add_u32 s85,s85,2048
s_add_u32 s86,s70,3
s_and_b32 s86,s86,3
s_mul_i32 s86,s86,20480
s_add_u32 s86,s86,4096
s_add_u32 s87,s70,4
s_and_b32 s87,s87,3
s_mul_i32 s87,s87,20480
s_add_u32 s87,s87,2048
v_or_b32 v77,0,v66
v_xor_b32 v77,v77,v76
v_lshl_add_u32 v77,v77,4,v75
v_add_u32 v48,s84,v77
v_add_u32 v50,s85,v77
v_add_u32 v52,s86,v77
v_add_u32 v54,s87,v77
v_or_b32 v77,4,v66
v_xor_b32 v77,v77,v76
v_lshl_add_u32 v77,v77,4,v75
v_add_u32 v77,9216,v77
v_add_u32 v49,s84,v77
v_add_u32 v51,s85,v77
v_add_u32 v53,s86,v77
v_add_u32 v55,s87,v77
s_lshl_b32 s44,s40,17
v_lshlrev_b32 v75,13,v65
v_lshl_add_u32 v75,v66,6,v75
v_lshl_add_u32 v75,v64,4,v75
v_add_u32 v75,s44,v75
v_add_u32 v56,0,v75
v_add_u32 v57,256,v75
v_add_u32 v58,32768,v75
v_add_u32 v59,33024,v75
v_add_u32 v60,65536,v75
v_add_u32 v61,65792,v75
v_add_u32 v62,98304,v75
v_add_u32 v63,98560,v75
s_mul_i32 s44,s41,307200
s_waitcnt lgkmcnt(0)
s_mov_b64 s[28:29],s[4:5]
s_add_u32 s28,s28,s44
s_addc_u32 s29,s29,0
s_sub_u32 s28,s28,4096
s_subb_u32 s29,s29,0
s_and_b32 s29,s29,0xffff
s_mov_b32 s30,311296
s_mov_b32 s31,0x20000
s_mov_b64 s[32:33],s[10:11]
s_and_b32 s33,s33,0xffff
s_mov_b32 s34,5242880
s_mov_b32 s35,0x20000
s_add_u32 m0,s58,0
s_nop 0
buffer_load_dwordx4 v56,s[32:35],s56 offen lds
s_add_u32 m0,s58,1024
s_nop 0
buffer_load_dwordx4 v57,s[32:35],s56 offen lds
s_add_u32 m0,s58,2048
s_nop 0
buffer_load_dwordx4 v58,s[32:35],s56 offen lds
s_add_u32 m0,s58,3072
s_nop 0
buffer_load_dwordx4 v59,s[32:35],s56 offen lds
s_add_u32 m0,s58,4096
s_nop 0
buffer_load_dwordx4 v60,s[32:35],s56 offen lds
s_add_u32 m0,s58,5120
s_nop 0
buffer_load_dwordx4 v61,s[32:35],s56 offen lds
s_add_u32 m0,s58,6144
s_nop 0
buffer_load_dwordx4 v62,s[32:35],s56 offen lds
s_add_u32 m0,s58,7168
s_nop 0
buffer_load_dwordx4 v63,s[32:35],s56 offen lds
s_add_u32 s56,s56,524288
s_add_u32 s58,s58,32768
s_cmp_ge_u32 s58,131072
s_cselect_b32 s62,98304,0
s_sub_u32 s58,s58,s62
s_add_u32 m0,s59,0
s_nop 0
buffer_load_dwordx4 v48,s[28:31],s57 offen lds
buffer_load_dwordx4 v49,s[28:31],s57 offen offset:1024 lds
buffer_load_dwordx4 v50,s[28:31],s57 offen offset:2048 lds
buffer_load_dwordx4 v51,s[28:31],s57 offen offset:3072 lds
s_add_u32 m0,s59,4096
s_nop 0
buffer_load_dwordx4 v52,s[28:31],s57 offen lds
buffer_load_dwordx4 v53,s[28:31],s57 offen offset:1024 lds
buffer_load_dwordx4 v54,s[28:31],s57 offen offset:2048 lds
buffer_load_dwordx4 v55,s[28:31],s57 offen offset:3072 lds
s_add_u32 s57,s57,128
s_xor_b32 s59,s59,131072
s_add_u32 m0,s58,0
s_nop 0
buffer_load_dwordx4 v56,s[32:35],s56 offen lds
s_add_u32 m0,s58,1024
s_nop 0
buffer_load_dwordx4 v57,s[32:35],s56 offen lds
s_add_u32 m0,s58,2048
s_nop 0
buffer_load_dwordx4 v58,s[32:35],s56 offen lds
s_add_u32 m0,s58,3072
s_nop 0
buffer_load_dwordx4 v59,s[32:35],s56 offen lds
s_add_u32 m0,s58,4096
s_nop 0
buffer_load_dwordx4 v60,s[32:35],s56 offen lds
s_add_u32 m0,s58,5120
s_nop 0
buffer_load_dwordx4 v61,s[32:35],s56 offen lds
s_add_u32 m0,s58,6144
s_nop 0
buffer_load_dwordx4 v62,s[32:35],s56 offen lds
s_add_u32 m0,s58,7168
s_nop 0
buffer_load_dwordx4 v63,s[32:35],s56 offen lds
s_add_u32 s56,s56,524288
s_add_u32 s58,s58,32768
s_cmp_ge_u32 s58,131072
s_cselect_b32 s62,98304,0
s_sub_u32 s58,s58,s62
s_add_u32 m0,s59,0
s_nop 0
buffer_load_dwordx4 v48,s[28:31],s57 offen lds
buffer_load_dwordx4 v49,s[28:31],s57 offen offset:1024 lds
buffer_load_dwordx4 v50,s[28:31],s57 offen offset:2048 lds
buffer_load_dwordx4 v51,s[28:31],s57 offen offset:3072 lds
s_add_u32 m0,s59,4096
s_nop 0
buffer_load_dwordx4 v52,s[28:31],s57 offen lds
buffer_load_dwordx4 v53,s[28:31],s57 offen offset:1024 lds
buffer_load_dwordx4 v54,s[28:31],s57 offen offset:2048 lds
buffer_load_dwordx4 v55,s[28:31],s57 offen offset:3072 lds
s_add_u32 s57,s57,128
s_xor_b32 s59,s59,131072
s_mov_b32 s60,65536
v_lshlrev_b32 v6,4,v12
v_lshlrev_b32 v72,12,v66
v_lshl_add_u32 v72,v65,6,v72
v_lshl_add_u32 v23,v64,4,v72
v_add_u32 v22,65536,v23
v_and_b32 v74,15,v12
v_lshrrev_b32 v76,1,v74
v_lshlrev_b32 v75,7,v74
s_lshl_b32 s44,s40,13
v_add_u32 v75,s44,v75
v_or_b32 v77,0,v66
v_xor_b32 v77,v77,v76
v_lshl_add_u32 v20,v77,4,v75
v_or_b32 v77,4,v66
v_xor_b32 v77,v77,v76
v_lshl_add_u32 v21,v77,4,v75
s_waitcnt vmcnt(16)
s_barrier
ds_read_b128 a[192:195],v20
ds_read_b64_tr_b16 a[128:129],v22 offset:1024
ds_read_b64_tr_b16 a[130:131],v22 offset:3072
ds_read_b128 a[196:199],v21
ds_read_b64_tr_b16 a[132:133],v22 offset:17408
ds_read_b64_tr_b16 a[134:135],v22 offset:19456
ds_read_b128 a[204:207],v21 offset:2048
ds_read_b128 a[200:203],v20 offset:2048
ds_read_b128 a[208:211],v20 offset:4096
ds_read_b128 a[212:215],v21 offset:4096
ds_read_b128 a[220:223],v21 offset:6144
ds_read_b128 a[216:219],v20 offset:6144
ds_read_b64_tr_b16 a[136:137],v22 offset:1032
ds_read_b64_tr_b16 a[138:139],v22 offset:3080
s_waitcnt lgkmcnt(11)
v_mfma_f32_16x16x32_bf16 v[128:131],a[128:131],a[192:195],0
s_add_u32 m0,s58,0
ds_read_b64_tr_b16 a[140:141],v22 offset:17416
ds_read_b64_tr_b16 a[142:143],v22 offset:19464
s_waitcnt lgkmcnt(10)
v_mfma_f32_16x16x32_bf16 v[128:131],a[132:135],a[196:199],v[128:131]
s_waitcnt lgkmcnt(9)
v_mfma_f32_16x16x32_bf16 v[136:139],a[132:135],a[204:207],0
s_waitcnt lgkmcnt(8)
v_mfma_f32_16x16x32_bf16 v[136:139],a[128:131],a[200:203],v[136:139]
s_waitcnt lgkmcnt(7)
v_mfma_f32_16x16x32_bf16 v[144:147],a[128:131],a[208:211],0
s_waitcnt lgkmcnt(6)
buffer_load_dwordx4 v56,s[32:35],s56 offen lds
v_mfma_f32_16x16x32_bf16 v[144:147],a[132:135],a[212:215],v[144:147]
s_add_u32 m0,s58,1024
s_waitcnt lgkmcnt(5)
v_mfma_f32_16x16x32_bf16 v[152:155],a[132:135],a[220:223],0
s_waitcnt lgkmcnt(4)
v_mfma_f32_16x16x32_bf16 v[152:155],a[128:131],a[216:219],v[152:155]
s_waitcnt lgkmcnt(2)
ds_read_b64_tr_b16 a[144:145],v22 offset:1280
ds_read_b64_tr_b16 a[146:147],v22 offset:3328
v_mfma_f32_16x16x32_bf16 v[132:135],a[136:139],a[192:195],0
s_waitcnt lgkmcnt(2)
ds_read_b64_tr_b16 a[148:149],v22 offset:17664
ds_read_b64_tr_b16 a[150:151],v22 offset:19712
v_mfma_f32_16x16x32_bf16 v[132:135],a[140:143],a[196:199],v[132:135]
buffer_load_dwordx4 v57,s[32:35],s56 offen lds
v_mfma_f32_16x16x32_bf16 v[140:143],a[140:143],a[204:207],0
v_mfma_f32_16x16x32_bf16 v[140:143],a[136:139],a[200:203],v[140:143]
s_add_u32 m0,s58,2048
v_mfma_f32_16x16x32_bf16 v[148:151],a[136:139],a[208:211],0
v_mfma_f32_16x16x32_bf16 v[148:151],a[140:143],a[212:215],v[148:151]
v_mfma_f32_16x16x32_bf16 v[156:159],a[140:143],a[220:223],0
v_mfma_f32_16x16x32_bf16 v[156:159],a[136:139],a[216:219],v[156:159]
s_waitcnt lgkmcnt(2)
ds_read_b64_tr_b16 a[152:153],v22 offset:1288
ds_read_b64_tr_b16 a[154:155],v22 offset:3336
buffer_load_dwordx4 v58,s[32:35],s56 offen lds
v_mfma_f32_16x16x32_bf16 v[160:163],a[144:147],a[192:195],0
s_add_u32 m0,s58,3072
ds_read_b64_tr_b16 a[156:157],v22 offset:17672
ds_read_b64_tr_b16 a[158:159],v22 offset:19720
s_waitcnt lgkmcnt(4)
v_mfma_f32_16x16x32_bf16 v[160:163],a[148:151],a[196:199],v[160:163]
v_mfma_f32_16x16x32_bf16 v[168:171],a[148:151],a[204:207],0
v_mfma_f32_16x16x32_bf16 v[168:171],a[144:147],a[200:203],v[168:171]
v_mfma_f32_16x16x32_bf16 v[176:179],a[144:147],a[208:211],0
buffer_load_dwordx4 v59,s[32:35],s56 offen lds
v_mfma_f32_16x16x32_bf16 v[176:179],a[148:151],a[212:215],v[176:179]
v_mfma_f32_16x16x32_bf16 v[184:187],a[148:151],a[220:223],0
s_add_u32 m0,s58,4096
v_mfma_f32_16x16x32_bf16 v[184:187],a[144:147],a[216:219],v[184:187]
s_waitcnt lgkmcnt(2)
ds_read_b64_tr_b16 a[160:161],v22 offset:1536
ds_read_b64_tr_b16 a[162:163],v22 offset:3584
v_mfma_f32_16x16x32_bf16 v[164:167],a[152:155],a[192:195],0
s_waitcnt lgkmcnt(2)
ds_read_b64_tr_b16 a[164:165],v22 offset:17920
ds_read_b64_tr_b16 a[166:167],v22 offset:19968
v_mfma_f32_16x16x32_bf16 v[164:167],a[156:159],a[196:199],v[164:167]
v_mfma_f32_16x16x32_bf16 v[172:175],a[156:159],a[204:207],0
buffer_load_dwordx4 v60,s[32:35],s56 offen lds
v_mfma_f32_16x16x32_bf16 v[172:175],a[152:155],a[200:203],v[172:175]
s_add_u32 m0,s58,5120
v_mfma_f32_16x16x32_bf16 v[180:183],a[152:155],a[208:211],0
v_mfma_f32_16x16x32_bf16 v[180:183],a[156:159],a[212:215],v[180:183]
v_mfma_f32_16x16x32_bf16 v[188:191],a[156:159],a[220:223],0
v_mfma_f32_16x16x32_bf16 v[188:191],a[152:155],a[216:219],v[188:191]
s_waitcnt lgkmcnt(2)
buffer_load_dwordx4 v61,s[32:35],s56 offen lds
ds_read_b64_tr_b16 a[168:169],v22 offset:1544
ds_read_b64_tr_b16 a[170:171],v22 offset:3592
v_mfma_f32_16x16x32_bf16 v[192:195],a[160:163],a[192:195],0
s_waitcnt lgkmcnt(2)
ds_read_b64_tr_b16 a[172:173],v22 offset:17928
ds_read_b64_tr_b16 a[174:175],v22 offset:19976
v_mfma_f32_16x16x32_bf16 v[192:195],a[164:167],a[196:199],v[192:195]
s_add_u32 m0,s58,6144
v_mfma_f32_16x16x32_bf16 v[200:203],a[164:167],a[204:207],0
v_mfma_f32_16x16x32_bf16 v[200:203],a[160:163],a[200:203],v[200:203]
s_lshl_b32 s44,s41,13
s_add_u32 s44,s44,104857600
s_mov_b64 s[64:65],s[12:13]
v_mfma_f32_16x16x32_bf16 v[208:211],a[160:163],a[208:211],0
s_add_u32 s64,s64,s44
s_addc_u32 s65,s65,0
s_and_b32 s65,s65,0xffff
v_mfma_f32_16x16x32_bf16 v[208:211],a[164:167],a[212:215],v[208:211]
s_mov_b32 s66,8192
buffer_load_dwordx4 v62,s[32:35],s56 offen lds
v_mfma_f32_16x16x32_bf16 v[216:219],a[164:167],a[220:223],0
s_add_u32 m0,s58,7168
s_mov_b32 s67,0x20000
v_mov_b32 v14,4096
v_mfma_f32_16x16x32_bf16 v[216:219],a[160:163],a[216:219],v[216:219]
s_waitcnt lgkmcnt(2)
ds_read_b64_tr_b16 a[176:177],v22 offset:1792
ds_read_b64_tr_b16 a[178:179],v22 offset:3840
v_mfma_f32_16x16x32_bf16 v[196:199],a[168:171],a[192:195],0
s_waitcnt lgkmcnt(2)
ds_read_b64_tr_b16 a[180:181],v22 offset:18176
ds_read_b64_tr_b16 a[182:183],v22 offset:20224
v_mfma_f32_16x16x32_bf16 v[196:199],a[172:175],a[196:199],v[196:199]
v_mfma_f32_16x16x32_bf16 v[204:207],a[172:175],a[204:207],0
s_mov_b64 s[72:73],s[14:15]
buffer_load_dwordx4 v63,s[32:35],s56 offen lds
v_mfma_f32_16x16x32_bf16 v[204:207],a[168:171],a[200:203],v[204:207]
s_and_b32 s73,s73,0xffff
s_mov_b32 s74,5242880
s_add_u32 m0,s59,0
v_mfma_f32_16x16x32_bf16 v[212:215],a[168:171],a[208:211],0
s_mov_b32 s75,0x20000
s_lshl_b32 s45,%1,2
v_lshlrev_b32 v15,3,v6
v_mfma_f32_16x16x32_bf16 v[212:215],a[172:175],a[212:215],v[212:215]
s_add_u32 s45,s45,s40
s_mul_i32 s45,s45,5120
ds_read_b64_tr_b16 a[184:185],v22 offset:1800
v_mfma_f32_16x16x32_bf16 v[220:223],a[172:175],a[220:223],0
s_lshl_b32 s43,s41,5
s_lshl_b32 s44,s42,1
ds_read_b64_tr_b16 a[186:187],v22 offset:3848
v_mfma_f32_16x16x32_bf16 v[220:223],a[168:171],a[216:219],v[220:223]
s_add_u32 s43,s43,s44
buffer_load_dwordx4 v48,s[28:31],s57 offen lds
s_waitcnt lgkmcnt(4)
v_mfma_f32_16x16x32_bf16 v[224:227],a[176:179],a[192:195],0
s_waitcnt lgkmcnt(2)
ds_read_b64_tr_b16 a[188:189],v22 offset:18184
ds_read_b64_tr_b16 a[190:191],v22 offset:20232
v_mfma_f32_16x16x32_bf16 v[224:227],a[180:183],a[196:199],v[224:227]
s_lshl_b32 s44,s43,18
s_mov_b64 s[20:21],s[6:7]
s_mov_b32 s22,524288
v_mfma_f32_16x16x32_bf16 v[232:235],a[180:183],a[204:207],0
s_add_u32 s20,s20,s44
s_addc_u32 s21,s21,0
ds_read_b64_tr_b16 a[128:129],v22
v_mfma_f32_16x16x32_bf16 v[232:235],a[176:179],a[200:203],v[232:235]
)A"
R"A(s_and_b32 s21,s21,0xffff
buffer_load_dwordx4 v49,s[28:31],s57 offen offset:1024 lds
v_mfma_f32_16x16x32_bf16 v[240:243],a[176:179],a[208:211],0
s_mov_b32 s23,0x20000
s_mov_b64 s[24:25],s[8:9]
ds_read_b64_tr_b16 a[130:131],v22 offset:2048
v_mfma_f32_16x16x32_bf16 v[240:243],a[180:183],a[212:215],v[240:243]
s_add_u32 s24,s24,s44
s_addc_u32 s25,s25,0
ds_read_b64_tr_b16 a[132:133],v22 offset:16384
v_mfma_f32_16x16x32_bf16 v[248:251],a[180:183],a[220:223],0
s_and_b32 s25,s25,0xffff
s_mov_b32 s26,524288
ds_read_b64_tr_b16 a[134:135],v22 offset:18432
v_mfma_f32_16x16x32_bf16 v[248:251],a[176:179],a[216:219],v[248:251]
s_mov_b32 s27,0x20000
s_lshl_b32 s44,s43,16
ds_read_b64_tr_b16 a[136:137],v22 offset:8
s_waitcnt lgkmcnt(7)
v_mfma_f32_16x16x32_bf16 v[228:231],a[184:187],a[192:195],0
s_mov_b64 s[36:37],s[12:13]
buffer_load_dwordx4 v50,s[28:31],s57 offen offset:2048 lds
s_waitcnt lgkmcnt(5)
v_mfma_f32_16x16x32_bf16 v[228:231],a[188:191],a[196:199],v[228:231]
s_add_u32 s36,s36,s44
s_addc_u32 s37,s37,0
ds_read_b64_tr_b16 a[138:139],v22 offset:2056
v_mfma_f32_16x16x32_bf16 v[236:239],a[188:191],a[204:207],0
s_and_b32 s37,s37,0xffff
s_mov_b32 s38,131072
ds_read_b64_tr_b16 a[140:141],v22 offset:16392
v_mfma_f32_16x16x32_bf16 v[236:239],a[184:187],a[200:203],v[236:239]
s_mov_b32 s39,0x20000
s_lshl_b32 s51,s40,12
ds_read_b64_tr_b16 a[142:143],v22 offset:18440
v_mfma_f32_16x16x32_bf16 v[244:247],a[184:187],a[208:211],0
s_add_u32 s52,s51,16384
s_mul_i32 s47,s41,5
ds_read_b64_tr_b16 a[144:145],v22 offset:256
v_mfma_f32_16x16x32_bf16 v[244:247],a[188:191],a[212:215],v[244:247]
s_add_u32 s47,s47,s42
buffer_load_dwordx4 v51,s[28:31],s57 offen offset:3072 lds
v_mfma_f32_16x16x32_bf16 v[252:255],a[188:191],a[220:223],0
s_and_b32 s47,s47,15
s_lshl_b32 s47,s47,14
ds_read_b64_tr_b16 a[146:147],v22 offset:2304
v_mfma_f32_16x16x32_bf16 v[252:255],a[184:187],a[216:219],v[252:255]
s_mov_b32 s54,0
s_mov_b32 s50,98304
ds_read_b64_tr_b16 a[148:149],v22 offset:16640
s_waitcnt lgkmcnt(6)
v_mfma_f32_16x16x32_bf16 a[0:3],a[128:131],a[192:195],0
s_add_u32 m0,s59,4096
s_mov_b32 s49,0
ds_read_b64_tr_b16 a[150:151],v22 offset:18688
v_mfma_f32_16x16x32_bf16 a[0:3],a[132:135],a[196:199],a[0:3]
s_lshl_b32 s55,s40,14
s_mov_b32 s61,0x3e0293ee
ds_read_b64_tr_b16 a[152:153],v22 offset:264
v_mfma_f32_16x16x32_bf16 a[32:35],a[132:135],a[204:207],0
s_mov_b32 s63,0x5d800000
s_lshl_b32 s44,s40,2
ds_read_b64_tr_b16 a[154:155],v22 offset:2312
v_mfma_f32_16x16x32_bf16 a[32:35],a[128:131],a[200:203],a[32:35]
s_add_u32 s44,s44,s70
buffer_load_dwordx4 v52,s[28:31],s57 offen lds
v_mfma_f32_16x16x32_bf16 a[64:67],a[128:131],a[208:211],0
s_mov_b32 s46,0xff800000
ds_read_b64_tr_b16 a[156:157],v22 offset:16648
v_lshlrev_b32 v70,8,v65
v_mfma_f32_16x16x32_bf16 a[64:67],a[132:135],a[212:215],a[64:67]
s_cmp_eq_u32 s44,14
s_cselect_b32 s18,0x7149f2ca,s46
ds_read_b64_tr_b16 a[158:159],v22 offset:18696
v_mfma_f32_16x16x32_bf16 a[96:99],a[132:135],a[220:223],0
s_cmp_eq_u32 s44,13
s_cselect_b32 s19,0x7149f2ca,s46
ds_read_b64_tr_b16 a[160:161],v22 offset:512
v_mfma_f32_16x16x32_bf16 a[96:99],a[128:131],a[216:219],a[96:99]
s_cmp_eq_u32 s44,12
s_cselect_b32 s100,0x7149f2ca,s46
ds_read_b64_tr_b16 a[162:163],v22 offset:2560
s_waitcnt lgkmcnt(7)
v_mfma_f32_16x16x32_bf16 a[4:7],a[136:139],a[192:195],0
s_add_u32 s60,s60,32768
buffer_load_dwordx4 v53,s[28:31],s57 offen offset:1024 lds
v_mfma_f32_16x16x32_bf16 a[4:7],a[140:143],a[196:199],a[4:7]
s_cmp_eq_u32 s44,15
s_cselect_b32 s101,0x7149f2ca,s46
ds_read_b64_tr_b16 a[164:165],v22 offset:16896
v_mfma_f32_16x16x32_bf16 a[36:39],a[140:143],a[204:207],0
s_add_u32 s58,s58,32768
ds_read_b64_tr_b16 a[166:167],v22 offset:18944
v_lshl_add_u32 v70,v66,6,v70
v_mfma_f32_16x16x32_bf16 a[36:39],a[136:139],a[200:203],a[36:39]
s_mul_i32 s81,s70,1310720
ds_read_b64_tr_b16 a[168:169],v22 offset:520
v_add_u32 v70,s51,v70
v_mfma_f32_16x16x32_bf16 a[68:71],a[136:139],a[208:211],0
s_mul_i32 s44,s71,320
ds_read_b64_tr_b16 a[170:171],v22 offset:2568
v_lshl_add_u32 v4,v64,4,v70
v_mfma_f32_16x16x32_bf16 a[68:71],a[140:143],a[212:215],a[68:71]
s_add_u32 s81,s81,s44
ds_read_b64_tr_b16 a[172:173],v22 offset:16904
v_xor_b32 v71,2,v64
v_mfma_f32_16x16x32_bf16 a[100:103],a[140:143],a[220:223],0
s_mul_i32 s82,s40,10240
buffer_load_dwordx4 v54,s[28:31],s57 offen offset:2048 lds
v_mfma_f32_16x16x32_bf16 a[100:103],a[136:139],a[216:219],a[100:103]
s_add_u32 s56,s56,524288
ds_read_b64_tr_b16 a[174:175],v22 offset:18952
v_lshl_add_u32 v5,v71,4,v70
v_mfma_f32_16x16x32_bf16 a[8:11],a[144:147],a[192:195],0
s_mul_i32 s44,s40,40960
ds_read_b64_tr_b16 a[176:177],v22 offset:768
v_lshlrev_b32 v72,10,v65
s_waitcnt lgkmcnt(7)
v_mfma_f32_16x16x32_bf16 a[8:11],a[148:151],a[196:199],a[8:11]
s_xor_b32 s59,s59,131072
ds_read_b64_tr_b16 a[178:179],v22 offset:2816
v_lshl_add_u32 v72,v64,6,v72
v_mfma_f32_16x16x32_bf16 a[40:43],a[148:151],a[204:207],0
s_lshr_b32 s92,%1,3
ds_read_b64_tr_b16 a[180:181],v22 offset:17152
v_and_b32 v73,2,v65
v_mfma_f32_16x16x32_bf16 a[40:43],a[144:147],a[200:203],a[40:43]
s_lshl_b32 s92,s92,2
buffer_load_dwordx4 v55,s[28:31],s57 offen offset:3072 lds
v_mfma_f32_16x16x32_bf16 a[72:75],a[144:147],a[208:211],0
s_add_u32 s57,s57,128
ds_read_b64_tr_b16 a[182:183],v22 offset:19200
v_xor_b32 v73,v73,v66
v_mfma_f32_16x16x32_bf16 a[72:75],a[148:151],a[212:215],a[72:75]
s_add_u32 s92,s92,s40
ds_read_b64_tr_b16 a[184:185],v22 offset:776
v_lshl_add_u32 v72,v73,4,v72
v_mfma_f32_16x16x32_bf16 a[104:107],a[148:151],a[220:223],0
s_lshl_b32 s92,s92,7
ds_read_b64_tr_b16 a[186:187],v22 offset:2824
v_add_u32 v1,98304,v72
v_mfma_f32_16x16x32_bf16 a[104:107],a[144:147],a[216:219],a[104:107]
s_add_u32 s93,s92,L_tailseg_%=-L_pc_%=
ds_read_b64_tr_b16 a[188:189],v22 offset:17160
v_lshlrev_b32 v72,10,v66
v_mfma_f32_16x16x32_bf16 a[12:15],a[152:155],a[192:195],0
ds_read_b64_tr_b16 a[190:191],v22 offset:19208
v_lshl_add_u32 v72,v65,6,v72
v_mfma_f32_16x16x32_bf16 a[12:15],a[156:159],a[196:199],a[12:15]
v_lshl_add_u32 v72,v64,3,v72
v_add_u32 v72,114688,v72
v_mfma_f32_16x16x32_bf16 a[44:47],a[156:159],a[204:207],0
s_cmp_eq_u32 s60,131072
s_cselect_b32 s60,32768,s60
v_and_b32 v73,1,v66
v_mfma_f32_16x16x32_bf16 a[44:47],a[152:155],a[200:203],a[44:47]
v_lshl_add_u32 v2,v73,5,v72
v_add_u32 v22,s60,v23
v_mfma_f32_16x16x32_bf16 a[76:79],a[152:155],a[208:211],0
s_add_u32 s60,s60,32768
buffer_load_dword v13,v14,s[64:67],0 offen sc1
v_mfma_f32_16x16x32_bf16 a[76:79],a[156:159],a[212:215],a[76:79]
v_xor_b32 v20,131072,v20
v_xor_b32 v73,1,v73
v_mfma_f32_16x16x32_bf16 a[108:111],a[156:159],a[220:223],0
v_xor_b32 v21,131072,v21
v_lshl_add_u32 v3,v73,5,v72
v_mfma_f32_16x16x32_bf16 a[108:111],a[152:155],a[216:219],a[108:111]
s_waitcnt vmcnt(17) lgkmcnt(0)
v_mov_b32 v28,0x3f803f80
v_mov_b32 v29,0x3f803f80
v_mfma_f32_16x16x32_bf16 a[16:19],a[160:163],a[192:195],0
s_barrier
ds_read_b128 a[224:227],v20
v_mov_b32 v30,0x3f803f80
v_mfma_f32_16x16x32_bf16 a[16:19],a[164:167],a[196:199],a[16:19]
ds_read_b64_tr_b16 a[128:129],v22 offset:1024
v_mov_b32 v31,0x3f803f80
v_mfma_f32_16x16x32_bf16 a[48:51],a[164:167],a[204:207],0
s_cmp_ge_u32 s58,131072
s_cselect_b32 s62,98304,0
ds_read_b64_tr_b16 a[130:131],v22 offset:3072
v_mfma_f32_16x16x32_bf16 a[48:51],a[160:163],a[200:203],a[48:51]
s_sub_u32 s58,s58,s62
s_mov_b32 exec_hi,0xff
buffer_load_dword v16,v15,s[72:75],s45 offen
s_mov_b32 exec_hi,-1
v_mfma_f32_16x16x32_bf16 a[80:83],a[160:163],a[208:211],0
ds_read_b128 a[228:231],v21
v_add_u32 v70,0,v12
v_mfma_f32_16x16x32_bf16 a[80:83],a[164:167],a[212:215],a[80:83]
ds_read_b64_tr_b16 a[132:133],v22 offset:17408
v_mul_u32_u24 v71,3277,v70
v_mfma_f32_16x16x32_bf16 a[112:115],a[164:167],a[220:223],0
ds_read_b64_tr_b16 a[134:135],v22 offset:19456
v_lshrrev_b32 v71,16,v71
v_mfma_f32_16x16x32_bf16 a[112:115],a[160:163],a[216:219],a[112:115]
ds_read_b128 a[232:235],v20 offset:2048
v_mul_u32_u24 v72,20,v71
v_mfma_f32_16x16x32_bf16 a[20:23],a[168:171],a[192:195],0
ds_read_b128 a[236:239],v21 offset:2048
v_sub_u32 v72,v70,v72
v_mfma_f32_16x16x32_bf16 a[20:23],a[172:175],a[196:199],a[20:23]
ds_read_b128 a[240:243],v20 offset:4096
v_bfe_u32 v73,v71,2,1
v_mfma_f32_16x16x32_bf16 a[52:55],a[172:175],a[204:207],0
ds_read_b128 a[244:247],v21 offset:4096
v_lshlrev_b32 v73,1,v73
v_mfma_f32_16x16x32_bf16 a[52:55],a[168:171],a[200:203],a[52:55]
ds_read_b128 a[248:251],v20 offset:6144
v_xor_b32 v72,v72,v73
v_mfma_f32_16x16x32_bf16 a[84:87],a[168:171],a[208:211],0
ds_read_b128 a[252:255],v21 offset:6144
v_mul_u32_u24 v71,1280,v71
v_mfma_f32_16x16x32_bf16 a[84:87],a[172:175],a[212:215],a[84:87]
ds_read_b64_tr_b16 a[136:137],v22 offset:1032
v_lshl_add_u32 v71,v72,4,v71
v_mfma_f32_16x16x32_bf16 a[116:119],a[172:175],a[220:223],0
ds_read_b64_tr_b16 a[138:139],v22 offset:3080
v_add_u32 v71,s44,v71
v_mfma_f32_16x16x32_bf16 a[116:119],a[168:171],a[216:219],a[116:119]
ds_read_b64_tr_b16 a[140:141],v22 offset:17416
v_subrev_u32 v7,0,v71
v_mfma_f32_16x16x32_bf16 a[24:27],a[176:179],a[192:195],0
ds_read_b64_tr_b16 a[142:143],v22 offset:19464
v_add_u32 v70,64,v12
v_mfma_f32_16x16x32_bf16 a[24:27],a[180:183],a[196:199],a[24:27]
ds_read_b64_tr_b16 a[144:145],v22 offset:1280
v_mul_u32_u24 v71,3277,v70
v_mfma_f32_16x16x32_bf16 a[56:59],a[180:183],a[204:207],0
ds_read_b64_tr_b16 a[146:147],v22 offset:3328
v_lshrrev_b32 v71,16,v71
v_mfma_f32_16x16x32_bf16 a[56:59],a[176:179],a[200:203],a[56:59]
ds_read_b64_tr_b16 a[148:149],v22 offset:17664
v_mul_u32_u24 v72,20,v71
v_mfma_f32_16x16x32_bf16 a[88:91],a[176:179],a[208:211],0
ds_read_b64_tr_b16 a[150:151],v22 offset:19712
v_sub_u32 v72,v70,v72
v_mfma_f32_16x16x32_bf16 a[88:91],a[180:183],a[212:215],a[88:91]
ds_read_b64_tr_b16 a[152:153],v22 offset:1288
v_bfe_u32 v73,v71,2,1
v_mfma_f32_16x16x32_bf16 a[120:123],a[180:183],a[220:223],0
ds_read_b64_tr_b16 a[154:155],v22 offset:3336
v_lshlrev_b32 v73,1,v73
v_mfma_f32_16x16x32_bf16 a[120:123],a[176:179],a[216:219],a[120:123]
ds_read_b64_tr_b16 a[156:157],v22 offset:17672
v_xor_b32 v72,v72,v73
v_mfma_f32_16x16x32_bf16 a[28:31],a[184:187],a[192:195],0
ds_read_b64_tr_b16 a[158:159],v22 offset:19720
v_mul_u32_u24 v71,1280,v71
v_mfma_f32_16x16x32_bf16 a[28:31],a[188:191],a[196:199],a[28:31]
ds_read_b64_tr_b16 a[160:161],v22 offset:1536
v_lshl_add_u32 v71,v72,4,v71
v_mfma_f32_16x16x32_bf16 a[60:63],a[188:191],a[204:207],0
ds_read_b64_tr_b16 a[162:163],v22 offset:3584
v_add_u32 v71,s44,v71
v_mfma_f32_16x16x32_bf16 a[60:63],a[184:187],a[200:203],a[60:63]
ds_read_b64_tr_b16 a[164:165],v22 offset:17920
v_subrev_u32 v8,1024,v71
v_mfma_f32_16x16x32_bf16 a[92:95],a[184:187],a[208:211],0
ds_read_b64_tr_b16 a[166:167],v22 offset:19968
v_add_u32 v70,128,v12
v_mfma_f32_16x16x32_bf16 a[92:95],a[188:191],a[212:215],a[92:95]
ds_read_b64_tr_b16 a[168:169],v22 offset:1544
v_mul_u32_u24 v71,3277,v70
v_mfma_f32_16x16x32_bf16 a[124:127],a[188:191],a[220:223],0
s_add_u32 m0,s58,0
ds_read_b64_tr_b16 a[170:171],v22 offset:3592
v_lshrrev_b32 v71,16,v71
v_mfma_f32_16x16x32_bf16 a[124:127],a[184:187],a[216:219],a[124:127]
s_waitcnt lgkmcnt(7)
ds_read_b64_tr_b16 a[172:173],v22 offset:17928
v_mul_u32_u24 v72,20,v71
v_mfma_f32_16x16x32_bf16 v[128:131],a[128:131],a[224:227],v[128:131]
ds_read_b64_tr_b16 a[174:175],v22 offset:19976
v_sub_u32 v72,v70,v72
v_mfma_f32_16x16x32_bf16 v[128:131],a[132:135],a[228:231],v[128:131]
buffer_load_dwordx4 v56,s[32:35],s56 offen lds
v_mfma_f32_16x16x32_bf16 v[136:139],a[132:135],a[236:239],v[136:139]
ds_read_b64_tr_b16 a[176:177],v22 offset:1792
v_bfe_u32 v73,v71,2,1
v_mfma_f32_16x16x32_bf16 v[136:139],a[128:131],a[232:235],v[136:139]
s_add_u32 m0,s58,1024
ds_read_b64_tr_b16 a[178:179],v22 offset:3840
v_lshlrev_b32 v73,1,v73
v_mfma_f32_16x16x32_bf16 v[144:147],a[128:131],a[240:243],v[144:147]
ds_read_b64_tr_b16 a[180:181],v22 offset:18176
v_xor_b32 v72,v72,v73
v_mfma_f32_16x16x32_bf16 v[144:147],a[132:135],a[244:247],v[144:147]
ds_read_b64_tr_b16 a[182:183],v22 offset:20224
v_mul_u32_u24 v71,1280,v71
v_mfma_f32_16x16x32_bf16 v[152:155],a[132:135],a[252:255],v[152:155]
buffer_load_dwordx4 v57,s[32:35],s56 offen lds
v_mfma_f32_16x16x32_bf16 v[152:155],a[128:131],a[248:251],v[152:155]
)A"
R"A(v_lshl_add_u32 v71,v72,4,v71
v_add_u32 v71,s44,v71
v_mfma_f32_16x16x32_bf16 v[132:135],a[136:139],a[224:227],v[132:135]
ds_read_b64_tr_b16 a[184:185],v22 offset:1800
v_subrev_u32 v9,2048,v71
v_mfma_f32_16x16x32_bf16 v[132:135],a[140:143],a[228:231],v[132:135]
s_add_u32 m0,s58,2048
ds_read_b64_tr_b16 a[186:187],v22 offset:3848
v_add_u32 v70,192,v12
v_mfma_f32_16x16x32_bf16 v[140:143],a[140:143],a[236:239],v[140:143]
ds_read_b64_tr_b16 a[188:189],v22 offset:18184
v_mul_u32_u24 v71,3277,v70
v_mfma_f32_16x16x32_bf16 v[140:143],a[136:139],a[232:235],v[140:143]
ds_read_b64_tr_b16 a[190:191],v22 offset:20232
v_lshrrev_b32 v71,16,v71
v_mfma_f32_16x16x32_bf16 v[148:151],a[136:139],a[240:243],v[148:151]
buffer_load_dwordx4 v58,s[32:35],s56 offen lds
v_mfma_f32_16x16x32_bf16 v[148:151],a[140:143],a[244:247],v[148:151]
v_mul_u32_u24 v72,20,v71
v_sub_u32 v72,v70,v72
v_mfma_f32_16x16x32_bf16 v[156:159],a[140:143],a[252:255],v[156:159]
s_add_u32 m0,s58,3072
v_bfe_u32 v73,v71,2,1
v_lshlrev_b32 v73,1,v73
v_mfma_f32_16x16x32_bf16 v[156:159],a[136:139],a[248:251],v[156:159]
ds_read_b64_tr_b16 a[128:129],v22
v_xor_b32 v72,v72,v73
v_mfma_f32_16x16x32_bf16 v[160:163],a[144:147],a[224:227],v[160:163]
ds_read_b64_tr_b16 a[130:131],v22 offset:2048
v_mul_u32_u24 v71,1280,v71
v_mfma_f32_16x16x32_bf16 v[160:163],a[148:151],a[228:231],v[160:163]
buffer_load_dwordx4 v59,s[32:35],s56 offen lds
v_mfma_f32_16x16x32_bf16 v[168:171],a[148:151],a[236:239],v[168:171]
ds_read_b64_tr_b16 a[132:133],v22 offset:16384
v_lshl_add_u32 v71,v72,4,v71
v_mfma_f32_16x16x32_bf16 v[168:171],a[144:147],a[232:235],v[168:171]
ds_read_b64_tr_b16 a[134:135],v22 offset:18432
v_add_u32 v71,s44,v71
v_mfma_f32_16x16x32_bf16 v[176:179],a[144:147],a[240:243],v[176:179]
s_add_u32 m0,s58,4096
v_subrev_u32 v10,3072,v71
v_add_u32 v70,256,v12
v_mfma_f32_16x16x32_bf16 v[176:179],a[148:151],a[244:247],v[176:179]
ds_read_b64_tr_b16 a[136:137],v22 offset:8
v_mul_u32_u24 v71,3277,v70
v_mfma_f32_16x16x32_bf16 v[184:187],a[148:151],a[252:255],v[184:187]
ds_read_b64_tr_b16 a[138:139],v22 offset:2056
v_lshrrev_b32 v71,16,v71
v_mfma_f32_16x16x32_bf16 v[184:187],a[144:147],a[248:251],v[184:187]
buffer_load_dwordx4 v60,s[32:35],s56 offen lds
v_mfma_f32_16x16x32_bf16 v[164:167],a[152:155],a[224:227],v[164:167]
s_waitcnt lgkmcnt(4)
ds_read_b64_tr_b16 a[140:141],v22 offset:16392
v_mul_u32_u24 v72,20,v71
v_mfma_f32_16x16x32_bf16 v[164:167],a[156:159],a[228:231],v[164:167]
s_add_u32 m0,s58,5120
ds_read_b64_tr_b16 a[142:143],v22 offset:18440
v_sub_u32 v72,v70,v72
v_mfma_f32_16x16x32_bf16 v[172:175],a[156:159],a[236:239],v[172:175]
v_bfe_u32 v73,v71,2,1
v_lshlrev_b32 v73,1,v73
v_mfma_f32_16x16x32_bf16 v[172:175],a[152:155],a[232:235],v[172:175]
ds_read_b64_tr_b16 a[144:145],v22 offset:256
v_xor_b32 v72,v72,v73
v_mfma_f32_16x16x32_bf16 v[180:183],a[152:155],a[240:243],v[180:183]
buffer_load_dwordx4 v61,s[32:35],s56 offen lds
v_mfma_f32_16x16x32_bf16 v[180:183],a[156:159],a[244:247],v[180:183]
ds_read_b64_tr_b16 a[146:147],v22 offset:2304
v_mul_u32_u24 v71,1280,v71
v_mfma_f32_16x16x32_bf16 v[188:191],a[156:159],a[252:255],v[188:191]
ds_read_b64_tr_b16 a[148:149],v22 offset:16640
v_lshl_add_u32 v71,v72,4,v71
v_mfma_f32_16x16x32_bf16 v[188:191],a[152:155],a[248:251],v[188:191]
s_add_u32 m0,s58,6144
ds_read_b64_tr_b16 a[150:151],v22 offset:18688
v_add_u32 v71,s44,v71
v_mfma_f32_16x16x32_bf16 v[192:195],a[160:163],a[224:227],v[192:195]
ds_read_b64_tr_b16 a[152:153],v22 offset:264
v_subrev_u32 v11,0,v71
v_mfma_f32_16x16x32_bf16 v[192:195],a[164:167],a[228:231],v[192:195]
ds_read_b64_tr_b16 a[154:155],v22 offset:2312
v_lshlrev_b32 v24,10,v6
v_mfma_f32_16x16x32_bf16 v[200:203],a[164:167],a[236:239],v[200:203]
buffer_load_dwordx4 v62,s[32:35],s56 offen lds
v_mfma_f32_16x16x32_bf16 v[200:203],a[160:163],a[232:235],v[200:203]
s_cmp_eq_u32 s60,131072
s_cselect_b32 s60,32768,s60
ds_read_b64_tr_b16 a[156:157],v22 offset:16648
v_mfma_f32_16x16x32_bf16 v[208:211],a[160:163],a[240:243],v[208:211]
s_add_u32 m0,s58,7168
ds_read_b64_tr_b16 a[158:159],v22 offset:18696
v_xor_b32 v20,131072,v20
v_mfma_f32_16x16x32_bf16 v[208:211],a[164:167],a[244:247],v[208:211]
s_add_u32 s58,s58,32768
v_xor_b32 v21,131072,v21
v_mfma_f32_16x16x32_bf16 v[216:219],a[164:167],a[252:255],v[216:219]
s_cmp_ge_u32 s58,131072
s_cselect_b32 s62,98304,0
ds_read_b64_tr_b16 a[164:165],v22 offset:16896
v_mfma_f32_16x16x32_bf16 v[216:219],a[160:163],a[248:251],v[216:219]
s_sub_u32 s58,s58,s62
buffer_load_dwordx4 v63,s[32:35],s56 offen lds
v_mfma_f32_16x16x32_bf16 v[196:199],a[168:171],a[224:227],v[196:199]
s_add_u32 s56,s56,524288
ds_read_b64_tr_b16 a[160:161],v22 offset:512
v_mfma_f32_16x16x32_bf16 v[196:199],a[172:175],a[228:231],v[196:199]
ds_read_b64_tr_b16 a[162:163],v22 offset:2560
v_mfma_f32_16x16x32_bf16 v[204:207],a[172:175],a[236:239],v[204:207]
s_add_u32 m0,s59,0
ds_read_b64_tr_b16 a[166:167],v22 offset:18944
v_mfma_f32_16x16x32_bf16 v[204:207],a[168:171],a[232:235],v[204:207]
v_mfma_f32_16x16x32_bf16 v[212:215],a[168:171],a[240:243],v[212:215]
v_mfma_f32_16x16x32_bf16 v[212:215],a[172:175],a[244:247],v[212:215]
buffer_load_dwordx4 v48,s[28:31],s57 offen lds
v_mfma_f32_16x16x32_bf16 v[220:223],a[172:175],a[252:255],v[220:223]
ds_read_b64_tr_b16 a[172:173],v22 offset:16904
v_mfma_f32_16x16x32_bf16 v[220:223],a[168:171],a[248:251],v[220:223]
ds_read_b64_tr_b16 a[168:169],v22 offset:520
v_mfma_f32_16x16x32_bf16 v[224:227],a[176:179],a[224:227],v[224:227]
ds_read_b64_tr_b16 a[170:171],v22 offset:2568
v_mfma_f32_16x16x32_bf16 v[224:227],a[180:183],a[228:231],v[224:227]
ds_read_b64_tr_b16 a[174:175],v22 offset:18952
v_mfma_f32_16x16x32_bf16 v[232:235],a[180:183],a[236:239],v[232:235]
buffer_load_dwordx4 v49,s[28:31],s57 offen offset:1024 lds
v_mfma_f32_16x16x32_bf16 v[232:235],a[176:179],a[232:235],v[232:235]
v_mfma_f32_16x16x32_bf16 v[240:243],a[176:179],a[240:243],v[240:243]
v_mfma_f32_16x16x32_bf16 v[240:243],a[180:183],a[244:247],v[240:243]
v_mfma_f32_16x16x32_bf16 v[248:251],a[180:183],a[252:255],v[248:251]
ds_read_b64_tr_b16 a[180:181],v22 offset:17152
v_mfma_f32_16x16x32_bf16 v[248:251],a[176:179],a[248:251],v[248:251]
ds_read_b64_tr_b16 a[176:177],v22 offset:768
v_mfma_f32_16x16x32_bf16 v[228:231],a[184:187],a[224:227],v[228:231]
buffer_load_dwordx4 v50,s[28:31],s57 offen offset:2048 lds
v_mfma_f32_16x16x32_bf16 v[228:231],a[188:191],a[228:231],v[228:231]
ds_read_b64_tr_b16 a[178:179],v22 offset:2816
v_mfma_f32_16x16x32_bf16 v[236:239],a[188:191],a[236:239],v[236:239]
ds_read_b64_tr_b16 a[182:183],v22 offset:19200
v_mfma_f32_16x16x32_bf16 v[236:239],a[184:187],a[232:235],v[236:239]
v_mfma_f32_16x16x32_bf16 v[244:247],a[184:187],a[240:243],v[244:247]
v_mfma_f32_16x16x32_bf16 v[244:247],a[188:191],a[244:247],v[244:247]
buffer_load_dwordx4 v51,s[28:31],s57 offen offset:3072 lds
v_mfma_f32_16x16x32_bf16 v[252:255],a[188:191],a[252:255],v[252:255]
ds_read_b64_tr_b16 a[188:189],v22 offset:17160
v_mfma_f32_16x16x32_bf16 v[252:255],a[184:187],a[248:251],v[252:255]
ds_read_b64_tr_b16 a[184:185],v22 offset:776
v_mfma_f32_16x16x32_bf16 a[0:3],a[128:131],a[224:227],a[0:3]
s_add_u32 m0,s59,4096
s_xor_b32 s59,s59,131072
ds_read_b64_tr_b16 a[186:187],v22 offset:2824
s_waitcnt lgkmcnt(5)
v_mfma_f32_16x16x32_bf16 a[0:3],a[132:135],a[228:231],a[0:3]
ds_read_b64_tr_b16 a[190:191],v22 offset:19208
v_add_u32 v22,s60,v23
v_mfma_f32_16x16x32_bf16 a[32:35],a[132:135],a[236:239],a[32:35]
v_mfma_f32_16x16x32_bf16 a[32:35],a[128:131],a[232:235],a[32:35]
buffer_load_dwordx4 v52,s[28:31],s57 offen lds
v_mfma_f32_16x16x32_bf16 a[64:67],a[128:131],a[240:243],a[64:67]
v_mfma_f32_16x16x32_bf16 a[64:67],a[132:135],a[244:247],a[64:67]
v_mfma_f32_16x16x32_bf16 a[96:99],a[132:135],a[252:255],a[96:99]
v_mfma_f32_16x16x32_bf16 a[96:99],a[128:131],a[248:251],a[96:99]
v_mfma_f32_16x16x32_bf16 a[4:7],a[136:139],a[224:227],a[4:7]
buffer_load_dwordx4 v53,s[28:31],s57 offen offset:1024 lds
v_mfma_f32_16x16x32_bf16 a[4:7],a[140:143],a[228:231],a[4:7]
v_mfma_f32_16x16x32_bf16 a[36:39],a[140:143],a[236:239],a[36:39]
v_mfma_f32_16x16x32_bf16 a[36:39],a[136:139],a[232:235],a[36:39]
v_mfma_f32_16x16x32_bf16 a[68:71],a[136:139],a[240:243],a[68:71]
v_mfma_f32_16x16x32_bf16 a[68:71],a[140:143],a[244:247],a[68:71]
v_mfma_f32_16x16x32_bf16 a[100:103],a[140:143],a[252:255],a[100:103]
buffer_load_dwordx4 v54,s[28:31],s57 offen offset:2048 lds
v_mfma_f32_16x16x32_bf16 a[100:103],a[136:139],a[248:251],a[100:103]
v_mfma_f32_16x16x32_bf16 a[8:11],a[144:147],a[224:227],a[8:11]
v_mfma_f32_16x16x32_bf16 a[8:11],a[148:151],a[228:231],a[8:11]
v_mfma_f32_16x16x32_bf16 a[40:43],a[148:151],a[236:239],a[40:43]
v_mfma_f32_16x16x32_bf16 a[40:43],a[144:147],a[232:235],a[40:43]
buffer_load_dwordx4 v55,s[28:31],s57 offen offset:3072 lds
v_mfma_f32_16x16x32_bf16 a[72:75],a[144:147],a[240:243],a[72:75]
s_add_u32 s57,s57,128
v_mfma_f32_16x16x32_bf16 a[72:75],a[148:151],a[244:247],a[72:75]
v_mfma_f32_16x16x32_bf16 a[104:107],a[148:151],a[252:255],a[104:107]
v_mfma_f32_16x16x32_bf16 a[104:107],a[144:147],a[248:251],a[104:107]
v_mfma_f32_16x16x32_bf16 a[12:15],a[152:155],a[224:227],a[12:15]
v_mfma_f32_16x16x32_bf16 a[12:15],a[156:159],a[228:231],a[12:15]
v_mfma_f32_16x16x32_bf16 a[44:47],a[156:159],a[236:239],a[44:47]
v_mfma_f32_16x16x32_bf16 a[44:47],a[152:155],a[232:235],a[44:47]
v_mfma_f32_16x16x32_bf16 a[76:79],a[152:155],a[240:243],a[76:79]
v_mfma_f32_16x16x32_bf16 a[76:79],a[156:159],a[244:247],a[76:79]
v_mfma_f32_16x16x32_bf16 a[108:111],a[156:159],a[252:255],a[108:111]
v_mfma_f32_16x16x32_bf16 a[108:111],a[152:155],a[248:251],a[108:111]
s_waitcnt vmcnt(18) lgkmcnt(0)
v_mfma_f32_16x16x32_bf16 a[16:19],a[160:163],a[224:227],a[16:19]
s_barrier
ds_read_b128 a[192:195],v20
v_mfma_f32_16x16x32_bf16 a[16:19],a[164:167],a[228:231],a[16:19]
ds_read_b128 a[196:199],v21
v_mfma_f32_16x16x32_bf16 a[48:51],a[164:167],a[236:239],a[48:51]
ds_read_b128 a[200:203],v20 offset:2048
v_mfma_f32_16x16x32_bf16 a[48:51],a[160:163],a[232:235],a[48:51]
ds_read_b128 a[204:207],v21 offset:2048
v_mfma_f32_16x16x32_bf16 a[80:83],a[160:163],a[240:243],a[80:83]
ds_read_b128 a[208:211],v20 offset:4096
v_mfma_f32_16x16x32_bf16 a[80:83],a[164:167],a[244:247],a[80:83]
ds_read_b128 a[212:215],v21 offset:4096
v_mfma_f32_16x16x32_bf16 a[112:115],a[164:167],a[252:255],a[112:115]
ds_read_b128 a[216:219],v20 offset:6144
v_mfma_f32_16x16x32_bf16 a[112:115],a[160:163],a[248:251],a[112:115]
ds_read_b128 a[220:223],v21 offset:6144
v_mfma_f32_16x16x32_bf16 a[20:23],a[168:171],a[224:227],a[20:23]
ds_read_b64_tr_b16 a[128:129],v22 offset:1024
v_mfma_f32_16x16x32_bf16 a[20:23],a[172:175],a[228:231],a[20:23]
ds_read_b64_tr_b16 a[130:131],v22 offset:3072
v_mfma_f32_16x16x32_bf16 a[52:55],a[172:175],a[236:239],a[52:55]
ds_read_b64_tr_b16 a[132:133],v22 offset:17408
v_mfma_f32_16x16x32_bf16 a[52:55],a[168:171],a[232:235],a[52:55]
ds_read_b64_tr_b16 a[134:135],v22 offset:19456
v_mfma_f32_16x16x32_bf16 a[84:87],a[168:171],a[240:243],a[84:87]
ds_read_b64_tr_b16 a[136:137],v22 offset:1032
v_mfma_f32_16x16x32_bf16 a[84:87],a[172:175],a[244:247],a[84:87]
ds_read_b64_tr_b16 a[138:139],v22 offset:3080
v_mfma_f32_16x16x32_bf16 a[116:119],a[172:175],a[252:255],a[116:119]
ds_read_b64_tr_b16 a[140:141],v22 offset:17416
v_mfma_f32_16x16x32_bf16 a[116:119],a[168:171],a[248:251],a[116:119]
ds_read_b64_tr_b16 a[142:143],v22 offset:19464
v_mfma_f32_16x16x32_bf16 a[24:27],a[176:179],a[224:227],a[24:27]
ds_read_b64_tr_b16 a[144:145],v22 offset:1280
v_mfma_f32_16x16x32_bf16 a[24:27],a[180:183],a[228:231],a[24:27]
ds_read_b64_tr_b16 a[146:147],v22 offset:3328
v_mfma_f32_16x16x32_bf16 a[56:59],a[180:183],a[236:239],a[56:59]
ds_read_b64_tr_b16 a[148:149],v22 offset:17664
v_mfma_f32_16x16x32_bf16 a[56:59],a[176:179],a[232:235],a[56:59]
ds_read_b64_tr_b16 a[150:151],v22 offset:19712
v_mfma_f32_16x16x32_bf16 a[88:91],a[176:179],a[240:243],a[88:91]
ds_read_b64_tr_b16 a[152:153],v22 offset:1288
v_mfma_f32_16x16x32_bf16 a[88:91],a[180:183],a[244:247],a[88:91]
ds_read_b64_tr_b16 a[154:155],v22 offset:3336
v_mfma_f32_16x16x32_bf16 a[120:123],a[180:183],a[252:255],a[120:123]
)A"
R"A(ds_read_b64_tr_b16 a[156:157],v22 offset:17672
v_mfma_f32_16x16x32_bf16 a[120:123],a[176:179],a[248:251],a[120:123]
ds_read_b64_tr_b16 a[158:159],v22 offset:19720
v_mfma_f32_16x16x32_bf16 a[28:31],a[184:187],a[224:227],a[28:31]
v_mfma_f32_16x16x32_bf16 a[28:31],a[188:191],a[228:231],a[28:31]
v_mfma_f32_16x16x32_bf16 a[60:63],a[188:191],a[236:239],a[60:63]
v_mfma_f32_16x16x32_bf16 a[60:63],a[184:187],a[232:235],a[60:63]
v_mfma_f32_16x16x32_bf16 a[92:95],a[184:187],a[240:243],a[92:95]
v_mfma_f32_16x16x32_bf16 a[92:95],a[188:191],a[244:247],a[92:95]
v_mfma_f32_16x16x32_bf16 a[124:127],a[188:191],a[252:255],a[124:127]
v_mfma_f32_16x16x32_bf16 a[124:127],a[184:187],a[248:251],a[124:127]
s_mov_b32 s53,3
L_qp_%=:
s_waitcnt lgkmcnt(0)
v_mfma_f32_16x16x32_bf16 v[128:131],a[128:131],a[192:195],v[128:131]
s_add_u32 m0,s58,0
s_add_u32 s60,s60,32768
ds_read_b64_tr_b16 a[160:161],v22 offset:1536
v_mfma_f32_16x16x32_bf16 v[128:131],a[132:135],a[196:199],v[128:131]
s_mov_b32 s90,L_att0_%=-L_pc_%=+128
buffer_load_dwordx4 v56,s[32:35],s56 offen lds
v_mfma_f32_16x16x32_bf16 v[136:139],a[132:135],a[204:207],v[136:139]
s_cmp_eq_u32 s60,131072
s_cselect_b32 s60,32768,s60
ds_read_b64_tr_b16 a[162:163],v22 offset:3584
v_mfma_f32_16x16x32_bf16 v[136:139],a[128:131],a[200:203],v[136:139]
s_add_u32 m0,s58,1024
ds_read_b64_tr_b16 a[164:165],v22 offset:17920
v_xor_b32 v20,131072,v20
v_mfma_f32_16x16x32_bf16 v[144:147],a[128:131],a[208:211],v[144:147]
s_sub_u32 s53,s53,1
ds_read_b64_tr_b16 a[166:167],v22 offset:19968
v_xor_b32 v21,131072,v21
v_mfma_f32_16x16x32_bf16 v[144:147],a[132:135],a[212:215],v[144:147]
ds_read_b64_tr_b16 a[168:169],v22 offset:1544
v_mfma_f32_16x16x32_bf16 v[152:155],a[132:135],a[220:223],v[152:155]
buffer_load_dwordx4 v57,s[32:35],s56 offen lds
v_mfma_f32_16x16x32_bf16 v[152:155],a[128:131],a[216:219],v[152:155]
ds_read_b64_tr_b16 a[170:171],v22 offset:3592
v_mfma_f32_16x16x32_bf16 v[132:135],a[136:139],a[192:195],v[132:135]
ds_read_b64_tr_b16 a[172:173],v22 offset:17928
v_mfma_f32_16x16x32_bf16 v[132:135],a[140:143],a[196:199],v[132:135]
s_add_u32 m0,s58,2048
ds_read_b64_tr_b16 a[174:175],v22 offset:19976
v_mfma_f32_16x16x32_bf16 v[140:143],a[140:143],a[204:207],v[140:143]
ds_read_b64_tr_b16 a[176:177],v22 offset:1792
v_mfma_f32_16x16x32_bf16 v[140:143],a[136:139],a[200:203],v[140:143]
ds_read_b64_tr_b16 a[178:179],v22 offset:3840
v_mfma_f32_16x16x32_bf16 v[148:151],a[136:139],a[208:211],v[148:151]
buffer_load_dwordx4 v58,s[32:35],s56 offen lds
v_mfma_f32_16x16x32_bf16 v[148:151],a[140:143],a[212:215],v[148:151]
ds_read_b64_tr_b16 a[180:181],v22 offset:18176
v_mfma_f32_16x16x32_bf16 v[156:159],a[140:143],a[220:223],v[156:159]
s_add_u32 m0,s58,3072
ds_read_b64_tr_b16 a[182:183],v22 offset:20224
v_mfma_f32_16x16x32_bf16 v[156:159],a[136:139],a[216:219],v[156:159]
ds_read_b64_tr_b16 a[184:185],v22 offset:1800
v_mfma_f32_16x16x32_bf16 v[160:163],a[144:147],a[192:195],v[160:163]
ds_read_b64_tr_b16 a[186:187],v22 offset:3848
v_mfma_f32_16x16x32_bf16 v[160:163],a[148:151],a[196:199],v[160:163]
buffer_load_dwordx4 v59,s[32:35],s56 offen lds
v_mfma_f32_16x16x32_bf16 v[168:171],a[148:151],a[204:207],v[168:171]
ds_read_b64_tr_b16 a[188:189],v22 offset:18184
v_mfma_f32_16x16x32_bf16 v[168:171],a[144:147],a[200:203],v[168:171]
ds_read_b64_tr_b16 a[190:191],v22 offset:20232
v_mfma_f32_16x16x32_bf16 v[176:179],a[144:147],a[208:211],v[176:179]
s_add_u32 m0,s58,4096
ds_read_b64_tr_b16 a[128:129],v22
v_mfma_f32_16x16x32_bf16 v[176:179],a[148:151],a[212:215],v[176:179]
ds_read_b64_tr_b16 a[130:131],v22 offset:2048
v_mfma_f32_16x16x32_bf16 v[184:187],a[148:151],a[220:223],v[184:187]
ds_read_b64_tr_b16 a[132:133],v22 offset:16384
v_mfma_f32_16x16x32_bf16 v[184:187],a[144:147],a[216:219],v[184:187]
buffer_load_dwordx4 v60,s[32:35],s56 offen lds
v_mfma_f32_16x16x32_bf16 v[164:167],a[152:155],a[192:195],v[164:167]
ds_read_b64_tr_b16 a[134:135],v22 offset:18432
v_mfma_f32_16x16x32_bf16 v[164:167],a[156:159],a[196:199],v[164:167]
s_add_u32 m0,s58,5120
ds_read_b64_tr_b16 a[136:137],v22 offset:8
v_mfma_f32_16x16x32_bf16 v[172:175],a[156:159],a[204:207],v[172:175]
ds_read_b64_tr_b16 a[138:139],v22 offset:2056
v_mfma_f32_16x16x32_bf16 v[172:175],a[152:155],a[200:203],v[172:175]
ds_read_b64_tr_b16 a[140:141],v22 offset:16392
v_mfma_f32_16x16x32_bf16 v[180:183],a[152:155],a[208:211],v[180:183]
buffer_load_dwordx4 v61,s[32:35],s56 offen lds
v_mfma_f32_16x16x32_bf16 v[180:183],a[156:159],a[212:215],v[180:183]
ds_read_b64_tr_b16 a[142:143],v22 offset:18440
v_mfma_f32_16x16x32_bf16 v[188:191],a[156:159],a[220:223],v[188:191]
ds_read_b64_tr_b16 a[144:145],v22 offset:256
v_mfma_f32_16x16x32_bf16 v[188:191],a[152:155],a[216:219],v[188:191]
s_add_u32 m0,s58,6144
ds_read_b64_tr_b16 a[146:147],v22 offset:2304
s_waitcnt lgkmcnt(7)
v_mfma_f32_16x16x32_bf16 v[192:195],a[160:163],a[192:195],v[192:195]
ds_read_b64_tr_b16 a[148:149],v22 offset:16640
v_mfma_f32_16x16x32_bf16 v[192:195],a[164:167],a[196:199],v[192:195]
ds_read_b64_tr_b16 a[150:151],v22 offset:18688
v_mfma_f32_16x16x32_bf16 v[200:203],a[164:167],a[204:207],v[200:203]
buffer_load_dwordx4 v62,s[32:35],s56 offen lds
v_mfma_f32_16x16x32_bf16 v[200:203],a[160:163],a[200:203],v[200:203]
ds_read_b64_tr_b16 a[152:153],v22 offset:264
v_mfma_f32_16x16x32_bf16 v[208:211],a[160:163],a[208:211],v[208:211]
s_add_u32 m0,s58,7168
s_add_u32 s58,s58,32768
ds_read_b64_tr_b16 a[154:155],v22 offset:2312
v_mfma_f32_16x16x32_bf16 v[208:211],a[164:167],a[212:215],v[208:211]
s_cmp_ge_u32 s58,131072
s_cselect_b32 s62,98304,0
ds_read_b64_tr_b16 a[156:157],v22 offset:16648
v_mfma_f32_16x16x32_bf16 v[216:219],a[164:167],a[220:223],v[216:219]
s_sub_u32 s58,s58,s62
ds_read_b64_tr_b16 a[158:159],v22 offset:18696
v_mfma_f32_16x16x32_bf16 v[216:219],a[160:163],a[216:219],v[216:219]
buffer_load_dwordx4 v63,s[32:35],s56 offen lds
v_mfma_f32_16x16x32_bf16 v[196:199],a[168:171],a[192:195],v[196:199]
s_add_u32 s56,s56,524288
ds_read_b64_tr_b16 a[160:161],v22 offset:512
v_mfma_f32_16x16x32_bf16 v[196:199],a[172:175],a[196:199],v[196:199]
ds_read_b64_tr_b16 a[162:163],v22 offset:2560
v_mfma_f32_16x16x32_bf16 v[204:207],a[172:175],a[204:207],v[204:207]
s_add_u32 m0,s59,0
ds_read_b64_tr_b16 a[164:165],v22 offset:16896
v_mfma_f32_16x16x32_bf16 v[204:207],a[168:171],a[200:203],v[204:207]
ds_read_b64_tr_b16 a[166:167],v22 offset:18944
v_mfma_f32_16x16x32_bf16 v[212:215],a[168:171],a[208:211],v[212:215]
v_mfma_f32_16x16x32_bf16 v[212:215],a[172:175],a[212:215],v[212:215]
buffer_load_dwordx4 v48,s[28:31],s57 offen lds
v_mfma_f32_16x16x32_bf16 v[220:223],a[172:175],a[220:223],v[220:223]
ds_read_b64_tr_b16 a[172:173],v22 offset:16904
v_mfma_f32_16x16x32_bf16 v[220:223],a[168:171],a[216:219],v[220:223]
ds_read_b64_tr_b16 a[168:169],v22 offset:520
v_mfma_f32_16x16x32_bf16 v[224:227],a[176:179],a[192:195],v[224:227]
ds_read_b64_tr_b16 a[170:171],v22 offset:2568
v_mfma_f32_16x16x32_bf16 v[224:227],a[180:183],a[196:199],v[224:227]
ds_read_b64_tr_b16 a[174:175],v22 offset:18952
v_mfma_f32_16x16x32_bf16 v[232:235],a[180:183],a[204:207],v[232:235]
buffer_load_dwordx4 v49,s[28:31],s57 offen offset:1024 lds
v_mfma_f32_16x16x32_bf16 v[232:235],a[176:179],a[200:203],v[232:235]
v_mfma_f32_16x16x32_bf16 v[240:243],a[176:179],a[208:211],v[240:243]
v_mfma_f32_16x16x32_bf16 v[240:243],a[180:183],a[212:215],v[240:243]
v_mfma_f32_16x16x32_bf16 v[248:251],a[180:183],a[220:223],v[248:251]
ds_read_b64_tr_b16 a[180:181],v22 offset:17152
v_mfma_f32_16x16x32_bf16 v[248:251],a[176:179],a[216:219],v[248:251]
ds_read_b64_tr_b16 a[176:177],v22 offset:768
v_mfma_f32_16x16x32_bf16 v[228:231],a[184:187],a[192:195],v[228:231]
buffer_load_dwordx4 v50,s[28:31],s57 offen offset:2048 lds
v_mfma_f32_16x16x32_bf16 v[228:231],a[188:191],a[196:199],v[228:231]
ds_read_b64_tr_b16 a[178:179],v22 offset:2816
v_mfma_f32_16x16x32_bf16 v[236:239],a[188:191],a[204:207],v[236:239]
ds_read_b64_tr_b16 a[182:183],v22 offset:19200
v_mfma_f32_16x16x32_bf16 v[236:239],a[184:187],a[200:203],v[236:239]
v_mfma_f32_16x16x32_bf16 v[244:247],a[184:187],a[208:211],v[244:247]
v_mfma_f32_16x16x32_bf16 v[244:247],a[188:191],a[212:215],v[244:247]
buffer_load_dwordx4 v51,s[28:31],s57 offen offset:3072 lds
v_mfma_f32_16x16x32_bf16 v[252:255],a[188:191],a[220:223],v[252:255]
ds_read_b64_tr_b16 a[188:189],v22 offset:17160
v_mfma_f32_16x16x32_bf16 v[252:255],a[184:187],a[216:219],v[252:255]
ds_read_b64_tr_b16 a[184:185],v22 offset:776
v_mfma_f32_16x16x32_bf16 a[0:3],a[128:131],a[192:195],a[0:3]
s_add_u32 m0,s59,4096
s_xor_b32 s59,s59,131072
ds_read_b64_tr_b16 a[186:187],v22 offset:2824
s_waitcnt lgkmcnt(5)
v_mfma_f32_16x16x32_bf16 a[0:3],a[132:135],a[196:199],a[0:3]
ds_read_b64_tr_b16 a[190:191],v22 offset:19208
v_add_u32 v22,s60,v23
v_mfma_f32_16x16x32_bf16 a[32:35],a[132:135],a[204:207],a[32:35]
s_add_u32 s60,s60,32768
s_cmp_eq_u32 s60,131072
s_cselect_b32 s60,32768,s60
v_mfma_f32_16x16x32_bf16 a[32:35],a[128:131],a[200:203],a[32:35]
buffer_load_dwordx4 v52,s[28:31],s57 offen lds
v_mfma_f32_16x16x32_bf16 a[64:67],a[128:131],a[208:211],a[64:67]
v_mfma_f32_16x16x32_bf16 a[64:67],a[132:135],a[212:215],a[64:67]
v_mfma_f32_16x16x32_bf16 a[96:99],a[132:135],a[220:223],a[96:99]
v_mfma_f32_16x16x32_bf16 a[96:99],a[128:131],a[216:219],a[96:99]
v_mfma_f32_16x16x32_bf16 a[4:7],a[136:139],a[192:195],a[4:7]
buffer_load_dwordx4 v53,s[28:31],s57 offen offset:1024 lds
v_mfma_f32_16x16x32_bf16 a[4:7],a[140:143],a[196:199],a[4:7]
v_mfma_f32_16x16x32_bf16 a[36:39],a[140:143],a[204:207],a[36:39]
v_mfma_f32_16x16x32_bf16 a[36:39],a[136:139],a[200:203],a[36:39]
v_mfma_f32_16x16x32_bf16 a[68:71],a[136:139],a[208:211],a[68:71]
v_mfma_f32_16x16x32_bf16 a[68:71],a[140:143],a[212:215],a[68:71]
v_mfma_f32_16x16x32_bf16 a[100:103],a[140:143],a[220:223],a[100:103]
buffer_load_dwordx4 v54,s[28:31],s57 offen offset:2048 lds
v_mfma_f32_16x16x32_bf16 a[100:103],a[136:139],a[216:219],a[100:103]
v_mfma_f32_16x16x32_bf16 a[8:11],a[144:147],a[192:195],a[8:11]
v_mfma_f32_16x16x32_bf16 a[8:11],a[148:151],a[196:199],a[8:11]
v_mfma_f32_16x16x32_bf16 a[40:43],a[148:151],a[204:207],a[40:43]
v_mfma_f32_16x16x32_bf16 a[40:43],a[144:147],a[200:203],a[40:43]
buffer_load_dwordx4 v55,s[28:31],s57 offen offset:3072 lds
v_mfma_f32_16x16x32_bf16 a[72:75],a[144:147],a[208:211],a[72:75]
s_add_u32 s57,s57,128
v_mfma_f32_16x16x32_bf16 a[72:75],a[148:151],a[212:215],a[72:75]
v_mfma_f32_16x16x32_bf16 a[104:107],a[148:151],a[220:223],a[104:107]
v_mfma_f32_16x16x32_bf16 a[104:107],a[144:147],a[216:219],a[104:107]
v_mfma_f32_16x16x32_bf16 a[12:15],a[152:155],a[192:195],a[12:15]
v_mfma_f32_16x16x32_bf16 a[12:15],a[156:159],a[196:199],a[12:15]
v_mfma_f32_16x16x32_bf16 a[44:47],a[156:159],a[204:207],a[44:47]
v_mfma_f32_16x16x32_bf16 a[44:47],a[152:155],a[200:203],a[44:47]
v_mfma_f32_16x16x32_bf16 a[76:79],a[152:155],a[208:211],a[76:79]
v_mfma_f32_16x16x32_bf16 a[76:79],a[156:159],a[212:215],a[76:79]
v_mfma_f32_16x16x32_bf16 a[108:111],a[156:159],a[220:223],a[108:111]
v_mfma_f32_16x16x32_bf16 a[108:111],a[152:155],a[216:219],a[108:111]
s_waitcnt vmcnt(16) lgkmcnt(0)
v_mfma_f32_16x16x32_bf16 a[16:19],a[160:163],a[192:195],a[16:19]
s_barrier
ds_read_b128 a[224:227],v20
v_mfma_f32_16x16x32_bf16 a[16:19],a[164:167],a[196:199],a[16:19]
ds_read_b64_tr_b16 a[128:129],v22 offset:1024
v_mfma_f32_16x16x32_bf16 a[48:51],a[164:167],a[204:207],a[48:51]
ds_read_b64_tr_b16 a[130:131],v22 offset:3072
v_mfma_f32_16x16x32_bf16 a[48:51],a[160:163],a[200:203],a[48:51]
ds_read_b128 a[228:231],v21
v_mfma_f32_16x16x32_bf16 a[80:83],a[160:163],a[208:211],a[80:83]
ds_read_b64_tr_b16 a[132:133],v22 offset:17408
v_mfma_f32_16x16x32_bf16 a[80:83],a[164:167],a[212:215],a[80:83]
ds_read_b64_tr_b16 a[134:135],v22 offset:19456
v_mfma_f32_16x16x32_bf16 a[112:115],a[164:167],a[220:223],a[112:115]
ds_read_b128 a[232:235],v20 offset:2048
v_mfma_f32_16x16x32_bf16 a[112:115],a[160:163],a[216:219],a[112:115]
ds_read_b128 a[236:239],v21 offset:2048
v_mfma_f32_16x16x32_bf16 a[20:23],a[168:171],a[192:195],a[20:23]
ds_read_b128 a[240:243],v20 offset:4096
v_mfma_f32_16x16x32_bf16 a[20:23],a[172:175],a[196:199],a[20:23]
)A"
R"A(ds_read_b128 a[244:247],v21 offset:4096
v_mfma_f32_16x16x32_bf16 a[52:55],a[172:175],a[204:207],a[52:55]
ds_read_b128 a[248:251],v20 offset:6144
v_xor_b32 v20,131072,v20
v_mfma_f32_16x16x32_bf16 a[52:55],a[168:171],a[200:203],a[52:55]
ds_read_b128 a[252:255],v21 offset:6144
v_xor_b32 v21,131072,v21
v_mfma_f32_16x16x32_bf16 a[84:87],a[168:171],a[208:211],a[84:87]
ds_read_b64_tr_b16 a[136:137],v22 offset:1032
v_mfma_f32_16x16x32_bf16 a[84:87],a[172:175],a[212:215],a[84:87]
ds_read_b64_tr_b16 a[138:139],v22 offset:3080
v_mfma_f32_16x16x32_bf16 a[116:119],a[172:175],a[220:223],a[116:119]
ds_read_b64_tr_b16 a[140:141],v22 offset:17416
v_mfma_f32_16x16x32_bf16 a[116:119],a[168:171],a[216:219],a[116:119]
ds_read_b64_tr_b16 a[142:143],v22 offset:19464
v_mfma_f32_16x16x32_bf16 a[24:27],a[176:179],a[192:195],a[24:27]
ds_read_b64_tr_b16 a[144:145],v22 offset:1280
v_mfma_f32_16x16x32_bf16 a[24:27],a[180:183],a[196:199],a[24:27]
ds_read_b64_tr_b16 a[146:147],v22 offset:3328
v_mfma_f32_16x16x32_bf16 a[56:59],a[180:183],a[204:207],a[56:59]
ds_read_b64_tr_b16 a[148:149],v22 offset:17664
v_mfma_f32_16x16x32_bf16 a[56:59],a[176:179],a[200:203],a[56:59]
ds_read_b64_tr_b16 a[150:151],v22 offset:19712
v_mfma_f32_16x16x32_bf16 a[88:91],a[176:179],a[208:211],a[88:91]
ds_read_b64_tr_b16 a[152:153],v22 offset:1288
v_mfma_f32_16x16x32_bf16 a[88:91],a[180:183],a[212:215],a[88:91]
ds_read_b64_tr_b16 a[154:155],v22 offset:3336
v_mfma_f32_16x16x32_bf16 a[120:123],a[180:183],a[220:223],a[120:123]
ds_read_b64_tr_b16 a[156:157],v22 offset:17672
v_mfma_f32_16x16x32_bf16 a[120:123],a[176:179],a[216:219],a[120:123]
ds_read_b64_tr_b16 a[158:159],v22 offset:19720
v_mfma_f32_16x16x32_bf16 a[28:31],a[184:187],a[192:195],a[28:31]
ds_read_b64_tr_b16 a[160:161],v22 offset:1536
v_mfma_f32_16x16x32_bf16 a[28:31],a[188:191],a[196:199],a[28:31]
ds_read_b64_tr_b16 a[162:163],v22 offset:3584
v_mfma_f32_16x16x32_bf16 a[60:63],a[188:191],a[204:207],a[60:63]
ds_read_b64_tr_b16 a[164:165],v22 offset:17920
v_mfma_f32_16x16x32_bf16 a[60:63],a[184:187],a[200:203],a[60:63]
ds_read_b64_tr_b16 a[166:167],v22 offset:19968
v_mfma_f32_16x16x32_bf16 a[92:95],a[184:187],a[208:211],a[92:95]
ds_read_b64_tr_b16 a[168:169],v22 offset:1544
v_mfma_f32_16x16x32_bf16 a[92:95],a[188:191],a[212:215],a[92:95]
ds_read_b64_tr_b16 a[170:171],v22 offset:3592
v_mfma_f32_16x16x32_bf16 a[124:127],a[188:191],a[220:223],a[124:127]
s_add_u32 m0,s58,0
ds_read_b64_tr_b16 a[172:173],v22 offset:17928
v_mfma_f32_16x16x32_bf16 a[124:127],a[184:187],a[216:219],a[124:127]
s_waitcnt lgkmcnt(7)
ds_read_b64_tr_b16 a[174:175],v22 offset:19976
v_mfma_f32_16x16x32_bf16 v[128:131],a[128:131],a[224:227],v[128:131]
ds_read_b64_tr_b16 a[176:177],v22 offset:1792
v_mfma_f32_16x16x32_bf16 v[128:131],a[132:135],a[228:231],v[128:131]
buffer_load_dwordx4 v56,s[32:35],s56 offen lds
v_mfma_f32_16x16x32_bf16 v[136:139],a[132:135],a[236:239],v[136:139]
ds_read_b64_tr_b16 a[178:179],v22 offset:3840
v_mfma_f32_16x16x32_bf16 v[136:139],a[128:131],a[232:235],v[136:139]
s_add_u32 m0,s58,1024
ds_read_b64_tr_b16 a[180:181],v22 offset:18176
v_mfma_f32_16x16x32_bf16 v[144:147],a[128:131],a[240:243],v[144:147]
ds_read_b64_tr_b16 a[182:183],v22 offset:20224
v_mfma_f32_16x16x32_bf16 v[144:147],a[132:135],a[244:247],v[144:147]
ds_read_b64_tr_b16 a[184:185],v22 offset:1800
v_mfma_f32_16x16x32_bf16 v[152:155],a[132:135],a[252:255],v[152:155]
buffer_load_dwordx4 v57,s[32:35],s56 offen lds
v_mfma_f32_16x16x32_bf16 v[152:155],a[128:131],a[248:251],v[152:155]
ds_read_b64_tr_b16 a[186:187],v22 offset:3848
v_mfma_f32_16x16x32_bf16 v[132:135],a[136:139],a[224:227],v[132:135]
ds_read_b64_tr_b16 a[188:189],v22 offset:18184
v_mfma_f32_16x16x32_bf16 v[132:135],a[140:143],a[228:231],v[132:135]
s_add_u32 m0,s58,2048
ds_read_b64_tr_b16 a[190:191],v22 offset:20232
v_mfma_f32_16x16x32_bf16 v[140:143],a[140:143],a[236:239],v[140:143]
ds_read_b64_tr_b16 a[128:129],v22
v_mfma_f32_16x16x32_bf16 v[140:143],a[136:139],a[232:235],v[140:143]
ds_read_b64_tr_b16 a[130:131],v22 offset:2048
v_mfma_f32_16x16x32_bf16 v[148:151],a[136:139],a[240:243],v[148:151]
buffer_load_dwordx4 v58,s[32:35],s56 offen lds
v_mfma_f32_16x16x32_bf16 v[148:151],a[140:143],a[244:247],v[148:151]
ds_read_b64_tr_b16 a[132:133],v22 offset:16384
v_mfma_f32_16x16x32_bf16 v[156:159],a[140:143],a[252:255],v[156:159]
s_add_u32 m0,s58,3072
ds_read_b64_tr_b16 a[134:135],v22 offset:18432
v_mfma_f32_16x16x32_bf16 v[156:159],a[136:139],a[248:251],v[156:159]
ds_read_b64_tr_b16 a[136:137],v22 offset:8
v_mfma_f32_16x16x32_bf16 v[160:163],a[144:147],a[224:227],v[160:163]
ds_read_b64_tr_b16 a[138:139],v22 offset:2056
v_mfma_f32_16x16x32_bf16 v[160:163],a[148:151],a[228:231],v[160:163]
buffer_load_dwordx4 v59,s[32:35],s56 offen lds
v_mfma_f32_16x16x32_bf16 v[168:171],a[148:151],a[236:239],v[168:171]
ds_read_b64_tr_b16 a[140:141],v22 offset:16392
v_mfma_f32_16x16x32_bf16 v[168:171],a[144:147],a[232:235],v[168:171]
ds_read_b64_tr_b16 a[142:143],v22 offset:18440
v_mfma_f32_16x16x32_bf16 v[176:179],a[144:147],a[240:243],v[176:179]
s_add_u32 m0,s58,4096
v_mfma_f32_16x16x32_bf16 v[176:179],a[148:151],a[244:247],v[176:179]
v_mfma_f32_16x16x32_bf16 v[184:187],a[148:151],a[252:255],v[184:187]
ds_read_b64_tr_b16 a[148:149],v22 offset:16640
v_mfma_f32_16x16x32_bf16 v[184:187],a[144:147],a[248:251],v[184:187]
buffer_load_dwordx4 v60,s[32:35],s56 offen lds
v_mfma_f32_16x16x32_bf16 v[164:167],a[152:155],a[224:227],v[164:167]
ds_read_b64_tr_b16 a[144:145],v22 offset:256
v_mfma_f32_16x16x32_bf16 v[164:167],a[156:159],a[228:231],v[164:167]
s_add_u32 m0,s58,5120
ds_read_b64_tr_b16 a[146:147],v22 offset:2304
v_mfma_f32_16x16x32_bf16 v[172:175],a[156:159],a[236:239],v[172:175]
ds_read_b64_tr_b16 a[150:151],v22 offset:18688
v_mfma_f32_16x16x32_bf16 v[172:175],a[152:155],a[232:235],v[172:175]
v_mfma_f32_16x16x32_bf16 v[180:183],a[152:155],a[240:243],v[180:183]
buffer_load_dwordx4 v61,s[32:35],s56 offen lds
v_mfma_f32_16x16x32_bf16 v[180:183],a[156:159],a[244:247],v[180:183]
v_mfma_f32_16x16x32_bf16 v[188:191],a[156:159],a[252:255],v[188:191]
ds_read_b64_tr_b16 a[156:157],v22 offset:16648
v_mfma_f32_16x16x32_bf16 v[188:191],a[152:155],a[248:251],v[188:191]
s_add_u32 m0,s58,6144
ds_read_b64_tr_b16 a[152:153],v22 offset:264
s_waitcnt lgkmcnt(5)
v_mfma_f32_16x16x32_bf16 v[192:195],a[160:163],a[224:227],v[192:195]
ds_read_b64_tr_b16 a[154:155],v22 offset:2312
v_mfma_f32_16x16x32_bf16 v[192:195],a[164:167],a[228:231],v[192:195]
ds_read_b64_tr_b16 a[158:159],v22 offset:18696
v_mfma_f32_16x16x32_bf16 v[200:203],a[164:167],a[236:239],v[200:203]
buffer_load_dwordx4 v62,s[32:35],s56 offen lds
v_mfma_f32_16x16x32_bf16 v[200:203],a[160:163],a[232:235],v[200:203]
v_mfma_f32_16x16x32_bf16 v[208:211],a[160:163],a[240:243],v[208:211]
s_add_u32 m0,s58,7168
s_add_u32 s58,s58,32768
v_mfma_f32_16x16x32_bf16 v[208:211],a[164:167],a[244:247],v[208:211]
s_cmp_ge_u32 s58,131072
s_cselect_b32 s62,98304,0
s_sub_u32 s58,s58,s62
v_mfma_f32_16x16x32_bf16 v[216:219],a[164:167],a[252:255],v[216:219]
ds_read_b64_tr_b16 a[164:165],v22 offset:16896
v_mfma_f32_16x16x32_bf16 v[216:219],a[160:163],a[248:251],v[216:219]
buffer_load_dwordx4 v63,s[32:35],s56 offen lds
v_mfma_f32_16x16x32_bf16 v[196:199],a[168:171],a[224:227],v[196:199]
s_add_u32 s56,s56,524288
ds_read_b64_tr_b16 a[160:161],v22 offset:512
v_mfma_f32_16x16x32_bf16 v[196:199],a[172:175],a[228:231],v[196:199]
ds_read_b64_tr_b16 a[162:163],v22 offset:2560
v_mfma_f32_16x16x32_bf16 v[204:207],a[172:175],a[236:239],v[204:207]
s_add_u32 m0,s59,0
ds_read_b64_tr_b16 a[166:167],v22 offset:18944
v_mfma_f32_16x16x32_bf16 v[204:207],a[168:171],a[232:235],v[204:207]
v_mfma_f32_16x16x32_bf16 v[212:215],a[168:171],a[240:243],v[212:215]
v_mfma_f32_16x16x32_bf16 v[212:215],a[172:175],a[244:247],v[212:215]
buffer_load_dwordx4 v48,s[28:31],s57 offen lds
v_mfma_f32_16x16x32_bf16 v[220:223],a[172:175],a[252:255],v[220:223]
ds_read_b64_tr_b16 a[172:173],v22 offset:16904
v_mfma_f32_16x16x32_bf16 v[220:223],a[168:171],a[248:251],v[220:223]
ds_read_b64_tr_b16 a[168:169],v22 offset:520
v_mfma_f32_16x16x32_bf16 v[224:227],a[176:179],a[224:227],v[224:227]
ds_read_b64_tr_b16 a[170:171],v22 offset:2568
v_mfma_f32_16x16x32_bf16 v[224:227],a[180:183],a[228:231],v[224:227]
ds_read_b64_tr_b16 a[174:175],v22 offset:18952
v_mfma_f32_16x16x32_bf16 v[232:235],a[180:183],a[236:239],v[232:235]
buffer_load_dwordx4 v49,s[28:31],s57 offen offset:1024 lds
v_mfma_f32_16x16x32_bf16 v[232:235],a[176:179],a[232:235],v[232:235]
v_mfma_f32_16x16x32_bf16 v[240:243],a[176:179],a[240:243],v[240:243]
v_mfma_f32_16x16x32_bf16 v[240:243],a[180:183],a[244:247],v[240:243]
v_mfma_f32_16x16x32_bf16 v[248:251],a[180:183],a[252:255],v[248:251]
ds_read_b64_tr_b16 a[180:181],v22 offset:17152
v_mfma_f32_16x16x32_bf16 v[248:251],a[176:179],a[248:251],v[248:251]
ds_read_b64_tr_b16 a[176:177],v22 offset:768
v_mfma_f32_16x16x32_bf16 v[228:231],a[184:187],a[224:227],v[228:231]
buffer_load_dwordx4 v50,s[28:31],s57 offen offset:2048 lds
v_mfma_f32_16x16x32_bf16 v[228:231],a[188:191],a[228:231],v[228:231]
ds_read_b64_tr_b16 a[178:179],v22 offset:2816
v_mfma_f32_16x16x32_bf16 v[236:239],a[188:191],a[236:239],v[236:239]
ds_read_b64_tr_b16 a[182:183],v22 offset:19200
v_mfma_f32_16x16x32_bf16 v[236:239],a[184:187],a[232:235],v[236:239]
v_mfma_f32_16x16x32_bf16 v[244:247],a[184:187],a[240:243],v[244:247]
v_mfma_f32_16x16x32_bf16 v[244:247],a[188:191],a[244:247],v[244:247]
buffer_load_dwordx4 v51,s[28:31],s57 offen offset:3072 lds
v_mfma_f32_16x16x32_bf16 v[252:255],a[188:191],a[252:255],v[252:255]
ds_read_b64_tr_b16 a[188:189],v22 offset:17160
v_mfma_f32_16x16x32_bf16 v[252:255],a[184:187],a[248:251],v[252:255]
ds_read_b64_tr_b16 a[184:185],v22 offset:776
v_mfma_f32_16x16x32_bf16 a[0:3],a[128:131],a[224:227],a[0:3]
s_add_u32 m0,s59,4096
s_xor_b32 s59,s59,131072
ds_read_b64_tr_b16 a[186:187],v22 offset:2824
v_mfma_f32_16x16x32_bf16 a[0:3],a[132:135],a[228:231],a[0:3]
ds_read_b64_tr_b16 a[190:191],v22 offset:19208
v_add_u32 v22,s60,v23
v_mfma_f32_16x16x32_bf16 a[32:35],a[132:135],a[236:239],a[32:35]
v_mfma_f32_16x16x32_bf16 a[32:35],a[128:131],a[232:235],a[32:35]
buffer_load_dwordx4 v52,s[28:31],s57 offen lds
v_mfma_f32_16x16x32_bf16 a[64:67],a[128:131],a[240:243],a[64:67]
v_mfma_f32_16x16x32_bf16 a[64:67],a[132:135],a[244:247],a[64:67]
v_mfma_f32_16x16x32_bf16 a[96:99],a[132:135],a[252:255],a[96:99]
v_mfma_f32_16x16x32_bf16 a[96:99],a[128:131],a[248:251],a[96:99]
v_mfma_f32_16x16x32_bf16 a[4:7],a[136:139],a[224:227],a[4:7]
buffer_load_dwordx4 v53,s[28:31],s57 offen offset:1024 lds
v_mfma_f32_16x16x32_bf16 a[4:7],a[140:143],a[228:231],a[4:7]
v_mfma_f32_16x16x32_bf16 a[36:39],a[140:143],a[236:239],a[36:39]
v_mfma_f32_16x16x32_bf16 a[36:39],a[136:139],a[232:235],a[36:39]
v_mfma_f32_16x16x32_bf16 a[68:71],a[136:139],a[240:243],a[68:71]
v_mfma_f32_16x16x32_bf16 a[68:71],a[140:143],a[244:247],a[68:71]
v_mfma_f32_16x16x32_bf16 a[100:103],a[140:143],a[252:255],a[100:103]
buffer_load_dwordx4 v54,s[28:31],s57 offen offset:2048 lds
v_mfma_f32_16x16x32_bf16 a[100:103],a[136:139],a[248:251],a[100:103]
s_waitcnt lgkmcnt(0)
v_mfma_f32_16x16x32_bf16 a[8:11],a[144:147],a[224:227],a[8:11]
v_mfma_f32_16x16x32_bf16 a[8:11],a[148:151],a[228:231],a[8:11]
v_mfma_f32_16x16x32_bf16 a[40:43],a[148:151],a[236:239],a[40:43]
v_mfma_f32_16x16x32_bf16 a[40:43],a[144:147],a[232:235],a[40:43]
buffer_load_dwordx4 v55,s[28:31],s57 offen offset:3072 lds
v_mfma_f32_16x16x32_bf16 a[72:75],a[144:147],a[240:243],a[72:75]
s_add_u32 s57,s57,128
v_mfma_f32_16x16x32_bf16 a[72:75],a[148:151],a[244:247],a[72:75]
v_mfma_f32_16x16x32_bf16 a[104:107],a[148:151],a[252:255],a[104:107]
v_mfma_f32_16x16x32_bf16 a[104:107],a[144:147],a[248:251],a[104:107]
v_mfma_f32_16x16x32_bf16 a[12:15],a[152:155],a[224:227],a[12:15]
v_mfma_f32_16x16x32_bf16 a[12:15],a[156:159],a[228:231],a[12:15]
v_mfma_f32_16x16x32_bf16 a[44:47],a[156:159],a[236:239],a[44:47]
v_mfma_f32_16x16x32_bf16 a[44:47],a[152:155],a[232:235],a[44:47]
v_mfma_f32_16x16x32_bf16 a[76:79],a[152:155],a[240:243],a[76:79]
buffer_load_dword v26,v24,s[88:91],s93 offen
)A"
R"A(v_mfma_f32_16x16x32_bf16 a[76:79],a[156:159],a[244:247],a[76:79]
v_mfma_f32_16x16x32_bf16 a[108:111],a[156:159],a[252:255],a[108:111]
v_mfma_f32_16x16x32_bf16 a[108:111],a[152:155],a[248:251],a[108:111]
s_waitcnt vmcnt(16) lgkmcnt(0)
v_mfma_f32_16x16x32_bf16 a[16:19],a[160:163],a[224:227],a[16:19]
s_barrier
ds_read_b128 a[192:195],v20
v_mfma_f32_16x16x32_bf16 a[16:19],a[164:167],a[228:231],a[16:19]
ds_read_b128 a[196:199],v21
v_mfma_f32_16x16x32_bf16 a[48:51],a[164:167],a[236:239],a[48:51]
ds_read_b128 a[200:203],v20 offset:2048
v_mfma_f32_16x16x32_bf16 a[48:51],a[160:163],a[232:235],a[48:51]
ds_read_b128 a[204:207],v21 offset:2048
v_mfma_f32_16x16x32_bf16 a[80:83],a[160:163],a[240:243],a[80:83]
ds_read_b128 a[208:211],v20 offset:4096
v_mfma_f32_16x16x32_bf16 a[80:83],a[164:167],a[244:247],a[80:83]
ds_read_b128 a[212:215],v21 offset:4096
v_mfma_f32_16x16x32_bf16 a[112:115],a[164:167],a[252:255],a[112:115]
ds_read_b128 a[216:219],v20 offset:6144
v_mfma_f32_16x16x32_bf16 a[112:115],a[160:163],a[248:251],a[112:115]
ds_read_b128 a[220:223],v21 offset:6144
v_mfma_f32_16x16x32_bf16 a[20:23],a[168:171],a[224:227],a[20:23]
ds_read_b64_tr_b16 a[128:129],v22 offset:1024
v_mfma_f32_16x16x32_bf16 a[20:23],a[172:175],a[228:231],a[20:23]
ds_read_b64_tr_b16 a[130:131],v22 offset:3072
v_mfma_f32_16x16x32_bf16 a[52:55],a[172:175],a[236:239],a[52:55]
ds_read_b64_tr_b16 a[132:133],v22 offset:17408
v_mfma_f32_16x16x32_bf16 a[52:55],a[168:171],a[232:235],a[52:55]
ds_read_b64_tr_b16 a[134:135],v22 offset:19456
v_mfma_f32_16x16x32_bf16 a[84:87],a[168:171],a[240:243],a[84:87]
ds_read_b64_tr_b16 a[136:137],v22 offset:1032
v_mfma_f32_16x16x32_bf16 a[84:87],a[172:175],a[244:247],a[84:87]
ds_read_b64_tr_b16 a[138:139],v22 offset:3080
v_mfma_f32_16x16x32_bf16 a[116:119],a[172:175],a[252:255],a[116:119]
ds_read_b64_tr_b16 a[140:141],v22 offset:17416
v_mfma_f32_16x16x32_bf16 a[116:119],a[168:171],a[248:251],a[116:119]
ds_read_b64_tr_b16 a[142:143],v22 offset:19464
v_mfma_f32_16x16x32_bf16 a[24:27],a[176:179],a[224:227],a[24:27]
ds_read_b64_tr_b16 a[144:145],v22 offset:1280
v_mfma_f32_16x16x32_bf16 a[24:27],a[180:183],a[228:231],a[24:27]
ds_read_b64_tr_b16 a[146:147],v22 offset:3328
v_mfma_f32_16x16x32_bf16 a[56:59],a[180:183],a[236:239],a[56:59]
ds_read_b64_tr_b16 a[148:149],v22 offset:17664
v_mfma_f32_16x16x32_bf16 a[56:59],a[176:179],a[232:235],a[56:59]
ds_read_b64_tr_b16 a[150:151],v22 offset:19712
v_mfma_f32_16x16x32_bf16 a[88:91],a[176:179],a[240:243],a[88:91]
ds_read_b64_tr_b16 a[152:153],v22 offset:1288
v_mfma_f32_16x16x32_bf16 a[88:91],a[180:183],a[244:247],a[88:91]
ds_read_b64_tr_b16 a[154:155],v22 offset:3336
v_mfma_f32_16x16x32_bf16 a[120:123],a[180:183],a[252:255],a[120:123]
ds_read_b64_tr_b16 a[156:157],v22 offset:17672
v_mfma_f32_16x16x32_bf16 a[120:123],a[176:179],a[248:251],a[120:123]
ds_read_b64_tr_b16 a[158:159],v22 offset:19720
v_mfma_f32_16x16x32_bf16 a[28:31],a[184:187],a[224:227],a[28:31]
v_mfma_f32_16x16x32_bf16 a[28:31],a[188:191],a[228:231],a[28:31]
v_mfma_f32_16x16x32_bf16 a[60:63],a[188:191],a[236:239],a[60:63]
v_mfma_f32_16x16x32_bf16 a[60:63],a[184:187],a[232:235],a[60:63]
v_mfma_f32_16x16x32_bf16 a[92:95],a[184:187],a[240:243],a[92:95]
v_mfma_f32_16x16x32_bf16 a[92:95],a[188:191],a[244:247],a[92:95]
v_mfma_f32_16x16x32_bf16 a[124:127],a[188:191],a[252:255],a[124:127]
s_cmp_lg_u32 s53,0
v_mfma_f32_16x16x32_bf16 a[124:127],a[184:187],a[248:251],a[124:127]
s_cbranch_scc1 L_qp_%=
L_tailseg_%=:
s_waitcnt lgkmcnt(0)
v_mfma_f32_16x16x32_bf16 v[128:131],a[128:131],a[192:195],v[128:131]
s_add_u32 m0,98304,s51
ds_read_b64_tr_b16 a[160:161],v22 offset:1536
v_xor_b32 v20,131072,v20
v_mfma_f32_16x16x32_bf16 v[128:131],a[132:135],a[196:199],v[128:131]
s_add_u32 s54,s54,16384
buffer_load_dwordx4 v4,s[20:23],s47 offen nt lds
v_mfma_f32_16x16x32_bf16 v[136:139],a[132:135],a[204:207],v[136:139]
s_and_b32 s46,s54,0xfffc0000
ds_read_b64_tr_b16 a[162:163],v22 offset:3584
v_xor_b32 v21,131072,v21
v_mfma_f32_16x16x32_bf16 v[136:139],a[128:131],a[200:203],v[136:139]
s_add_u32 s54,s54,16384
ds_read_b64_tr_b16 a[164:165],v22 offset:17920
v_and_b32 v18,48,v6
v_mfma_f32_16x16x32_bf16 v[144:147],a[128:131],a[208:211],v[144:147]
s_add_u32 s49,s49,32768
ds_read_b64_tr_b16 a[166:167],v22 offset:19968
v_lshrrev_b32 v19,4,v6
v_mfma_f32_16x16x32_bf16 v[144:147],a[132:135],a[212:215],v[144:147]
s_mov_b32 s60,138
ds_read_b64_tr_b16 a[168:169],v22 offset:1544
v_lshlrev_b32 v18,2,v18
v_mfma_f32_16x16x32_bf16 v[152:155],a[132:135],a[220:223],v[152:155]
s_add_u32 s93,s92,L_att0_%=-L_pc_%=
ds_read_b64_tr_b16 a[170:171],v22 offset:3592
v_and_b32 v19,48,v19
v_mfma_f32_16x16x32_bf16 v[152:155],a[128:131],a[216:219],v[152:155]
ds_read_b64_tr_b16 a[172:173],v22 offset:17928
v_add_u32 v18,v18,v19
v_mfma_f32_16x16x32_bf16 v[132:135],a[136:139],a[192:195],v[132:135]
ds_read_b64_tr_b16 a[174:175],v22 offset:19976
v_add_u32 v18,98304,v18
v_mfma_f32_16x16x32_bf16 v[132:135],a[140:143],a[196:199],v[132:135]
s_cmp_eq_u32 s49,163840
s_cselect_b32 s49,0,s49
ds_read_b64_tr_b16 a[176:177],v22 offset:1792
v_mfma_f32_16x16x32_bf16 v[140:143],a[140:143],a[204:207],v[140:143]
s_cmp_eq_u32 s50,131072
s_cselect_b32 s62,163840,0
ds_read_b64_tr_b16 a[178:179],v22 offset:3840
v_mfma_f32_16x16x32_bf16 v[140:143],a[136:139],a[200:203],v[140:143]
s_sub_u32 s48,32768,s62
s_add_u32 s50,s50,s48
ds_read_b64_tr_b16 a[180:181],v22 offset:18176
v_mfma_f32_16x16x32_bf16 v[148:151],a[136:139],a[208:211],v[148:151]
s_cmp_eq_u32 s50,131072
s_cselect_b32 s62,163840,0
ds_read_b64_tr_b16 a[182:183],v22 offset:20224
v_mfma_f32_16x16x32_bf16 v[148:151],a[140:143],a[212:215],v[148:151]
ds_read_b64_tr_b16 a[184:185],v22 offset:1800
v_mfma_f32_16x16x32_bf16 v[156:159],a[140:143],a[220:223],v[156:159]
ds_read_b64_tr_b16 a[186:187],v22 offset:3848
v_mfma_f32_16x16x32_bf16 v[156:159],a[136:139],a[216:219],v[156:159]
ds_read_b64_tr_b16 a[188:189],v22 offset:18184
v_mfma_f32_16x16x32_bf16 v[160:163],a[144:147],a[192:195],v[160:163]
ds_read_b64_tr_b16 a[190:191],v22 offset:20232
v_mfma_f32_16x16x32_bf16 v[160:163],a[148:151],a[196:199],v[160:163]
buffer_load_dwordx4 v4,s[20:23],s47 offen offset:1024 nt lds
v_mfma_f32_16x16x32_bf16 v[168:171],a[148:151],a[204:207],v[168:171]
s_waitcnt vmcnt(3)
ds_read_b128 a[224:227],v20
v_mfma_f32_16x16x32_bf16 v[168:171],a[144:147],a[200:203],v[168:171]
ds_read_b64_tr_b16 a[128:129],v22
v_mfma_f32_16x16x32_bf16 v[176:179],a[144:147],a[208:211],v[176:179]
ds_read_b64_tr_b16 a[130:131],v22 offset:2048
v_mfma_f32_16x16x32_bf16 v[176:179],a[148:151],a[212:215],v[176:179]
ds_read_b64_tr_b16 a[132:133],v22 offset:16384
v_mfma_f32_16x16x32_bf16 v[184:187],a[148:151],a[220:223],v[184:187]
ds_read_b64_tr_b16 a[134:135],v22 offset:18432
v_mfma_f32_16x16x32_bf16 v[184:187],a[144:147],a[216:219],v[184:187]
ds_read_b64_tr_b16 a[144:145],v22 offset:8
v_mfma_f32_16x16x32_bf16 v[164:167],a[152:155],a[192:195],v[164:167]
ds_read_b64_tr_b16 a[146:147],v22 offset:2056
v_mfma_f32_16x16x32_bf16 v[164:167],a[156:159],a[196:199],v[164:167]
ds_read_b64_tr_b16 a[148:149],v22 offset:16392
v_mfma_f32_16x16x32_bf16 v[172:175],a[156:159],a[204:207],v[172:175]
ds_read_b64_tr_b16 a[150:151],v22 offset:18440
v_mfma_f32_16x16x32_bf16 v[172:175],a[152:155],a[200:203],v[172:175]
v_readfirstlane_b32 s68,v13
s_nop 1
s_add_u32 s68,s68,1
v_mfma_f32_16x16x32_bf16 v[180:183],a[152:155],a[208:211],v[180:183]
s_xor_b32 s69,s68,0x9e3779b9
ds_read_b128 a[228:231],v21
v_mfma_f32_16x16x32_bf16 v[180:183],a[156:159],a[212:215],v[180:183]
v_mfma_f32_16x16x32_bf16 v[188:191],a[156:159],a[220:223],v[188:191]
ds_read_b128 a[232:235],v20 offset:2048
v_mfma_f32_16x16x32_bf16 v[188:191],a[152:155],a[216:219],v[188:191]
s_waitcnt lgkmcnt(5)
v_mfma_f32_16x16x32_bf16 v[192:195],a[160:163],a[192:195],v[192:195]
ds_read_b128 a[236:239],v21 offset:2048
ds_read_b128 a[240:243],v20 offset:4096
ds_read_b128 a[244:247],v21 offset:4096
ds_read_b128 a[248:251],v20 offset:6144
ds_read_b128 a[252:255],v21 offset:6144
v_mfma_f32_16x16x32_bf16 v[192:195],a[164:167],a[196:199],v[192:195]
v_mfma_f32_16x16x32_bf16 v[200:203],a[164:167],a[204:207],v[200:203]
v_mfma_f32_16x16x32_bf16 v[200:203],a[160:163],a[200:203],v[200:203]
v_mfma_f32_16x16x32_bf16 v[208:211],a[160:163],a[208:211],v[208:211]
buffer_load_dwordx4 v5,s[20:23],s47 offen offset:2048 nt lds
v_mfma_f32_16x16x32_bf16 v[208:211],a[164:167],a[212:215],v[208:211]
v_mfma_f32_16x16x32_bf16 v[216:219],a[164:167],a[220:223],v[216:219]
s_waitcnt lgkmcnt(0)
v_mfma_f32_16x16x32_bf16 v[216:219],a[160:163],a[216:219],v[216:219]
s_barrier
ds_read_b64_tr_b16 a[136:137],v22 offset:32768
v_mfma_f32_16x16x32_bf16 v[196:199],a[168:171],a[192:195],v[196:199]
ds_read_b64_tr_b16 a[138:139],v22 offset:34816
v_mfma_f32_16x16x32_bf16 v[196:199],a[172:175],a[196:199],v[196:199]
ds_read_b64_tr_b16 a[140:141],v22 offset:49152
v_mfma_f32_16x16x32_bf16 v[204:207],a[172:175],a[204:207],v[204:207]
ds_read_b64_tr_b16 a[142:143],v22 offset:51200
v_mfma_f32_16x16x32_bf16 v[204:207],a[168:171],a[200:203],v[204:207]
ds_read_b64_tr_b16 a[152:153],v22 offset:32776
v_mfma_f32_16x16x32_bf16 v[212:215],a[168:171],a[208:211],v[212:215]
ds_read_b64_tr_b16 a[154:155],v22 offset:34824
v_mfma_f32_16x16x32_bf16 v[212:215],a[172:175],a[212:215],v[212:215]
ds_read_b64_tr_b16 a[156:157],v22 offset:49160
v_mfma_f32_16x16x32_bf16 v[220:223],a[172:175],a[220:223],v[220:223]
ds_read_b64_tr_b16 a[158:159],v22 offset:51208
v_mfma_f32_16x16x32_bf16 v[220:223],a[168:171],a[216:219],v[220:223]
ds_read_b64_tr_b16 a[160:161],v22 offset:256
v_mfma_f32_16x16x32_bf16 v[224:227],a[176:179],a[192:195],v[224:227]
buffer_load_dwordx4 v5,s[20:23],s47 offen offset:3072 nt lds
v_mfma_f32_16x16x32_bf16 v[224:227],a[180:183],a[196:199],v[224:227]
ds_read_b64_tr_b16 a[162:163],v22 offset:2304
v_mfma_f32_16x16x32_bf16 v[232:235],a[180:183],a[204:207],v[232:235]
ds_read_b64_tr_b16 a[164:165],v22 offset:16640
v_mfma_f32_16x16x32_bf16 v[232:235],a[176:179],a[200:203],v[232:235]
ds_read_b64_tr_b16 a[166:167],v22 offset:18688
v_mfma_f32_16x16x32_bf16 v[240:243],a[176:179],a[208:211],v[240:243]
ds_read_b64_tr_b16 a[168:169],v22 offset:33024
v_mfma_f32_16x16x32_bf16 v[240:243],a[180:183],a[212:215],v[240:243]
ds_read_b64_tr_b16 a[170:171],v22 offset:35072
v_mfma_f32_16x16x32_bf16 v[248:251],a[180:183],a[220:223],v[248:251]
ds_read_b64_tr_b16 a[172:173],v22 offset:49408
v_mfma_f32_16x16x32_bf16 v[248:251],a[176:179],a[216:219],v[248:251]
ds_read_b64_tr_b16 a[174:175],v22 offset:51456
v_mfma_f32_16x16x32_bf16 v[228:231],a[184:187],a[192:195],v[228:231]
ds_read_b64_tr_b16 a[176:177],v22 offset:264
v_mfma_f32_16x16x32_bf16 v[228:231],a[188:191],a[196:199],v[228:231]
ds_read_b64_tr_b16 a[178:179],v22 offset:2312
v_mfma_f32_16x16x32_bf16 v[236:239],a[188:191],a[204:207],v[236:239]
ds_read_b64_tr_b16 a[180:181],v22 offset:16648
v_mfma_f32_16x16x32_bf16 v[236:239],a[184:187],a[200:203],v[236:239]
ds_read_b64_tr_b16 a[182:183],v22 offset:18696
v_mfma_f32_16x16x32_bf16 v[244:247],a[184:187],a[208:211],v[244:247]
v_mfma_f32_16x16x32_bf16 v[244:247],a[188:191],a[212:215],v[244:247]
s_add_u32 m0,98304,s52
v_mfma_f32_16x16x32_bf16 v[252:255],a[188:191],a[220:223],v[252:255]
ds_read_b64_tr_b16 a[188:189],v22 offset:49416
v_mfma_f32_16x16x32_bf16 v[252:255],a[184:187],a[216:219],v[252:255]
ds_read_b64_tr_b16 a[184:185],v22 offset:33032
v_mfma_f32_16x16x32_bf16 a[0:3],a[128:131],a[192:195],a[0:3]
buffer_load_dwordx4 v4,s[24:27],s47 offen nt lds
v_mfma_f32_16x16x32_bf16 a[0:3],a[132:135],a[196:199],a[0:3]
s_waitcnt lgkmcnt(4)
ds_read_b64_tr_b16 a[186:187],v22 offset:35080
v_mfma_f32_16x16x32_bf16 a[0:3],a[136:139],a[224:227],a[0:3]
ds_read_b64_tr_b16 a[190:191],v22 offset:51464
v_mfma_f32_16x16x32_bf16 a[0:3],a[140:143],a[228:231],a[0:3]
v_mfma_f32_16x16x32_bf16 a[32:35],a[140:143],a[236:239],a[32:35]
v_mfma_f32_16x16x32_bf16 a[32:35],a[136:139],a[232:235],a[32:35]
v_mfma_f32_16x16x32_bf16 a[32:35],a[132:135],a[204:207],a[32:35]
v_mfma_f32_16x16x32_bf16 a[32:35],a[128:131],a[200:203],a[32:35]
v_mfma_f32_16x16x32_bf16 a[64:67],a[128:131],a[208:211],a[64:67]
)A"
R"A(v_mfma_f32_16x16x32_bf16 a[64:67],a[132:135],a[212:215],a[64:67]
s_nop 1
v_accvgpr_read_b32 v64,a0
v_accvgpr_read_b32 v65,a1
v_mfma_f32_16x16x32_bf16 a[64:67],a[136:139],a[240:243],a[64:67]
v_mul_f32 v64,s61,v64
v_mul_f32 v65,s61,v65
v_mfma_f32_16x16x32_bf16 a[64:67],a[140:143],a[244:247],a[64:67]
v_accvgpr_read_b32 v66,a2
v_accvgpr_read_b32 v67,a3
v_mfma_f32_16x16x32_bf16 a[96:99],a[140:143],a[252:255],a[96:99]
ds_read_b64_tr_b16 a[140:141],v22 offset:49664
v_cvt_pk_bf16_f32 v64,v64,v65
v_mfma_f32_16x16x32_bf16 a[96:99],a[136:139],a[248:251],a[96:99]
ds_read_b64_tr_b16 a[136:137],v22 offset:33280
v_mul_f32 v66,s61,v66
v_mfma_f32_16x16x32_bf16 a[96:99],a[132:135],a[220:223],a[96:99]
ds_read_b64_tr_b16 a[132:133],v22 offset:16896
v_mul_f32 v67,s61,v67
v_mfma_f32_16x16x32_bf16 a[96:99],a[128:131],a[216:219],a[96:99]
ds_read_b64_tr_b16 a[128:129],v22 offset:512
v_cvt_pk_bf16_f32 v65,v66,v67
v_mfma_f32_16x16x32_bf16 a[4:7],a[144:147],a[192:195],a[4:7]
buffer_load_dwordx4 v5,s[24:27],s47 offen offset:1024 nt lds
v_mfma_f32_16x16x32_bf16 a[4:7],a[148:151],a[196:199],a[4:7]
ds_read_b64_tr_b16 a[130:131],v22 offset:2560
v_accvgpr_read_b32 v96,a64
v_mfma_f32_16x16x32_bf16 a[4:7],a[152:155],a[224:227],a[4:7]
ds_read_b64_tr_b16 a[134:135],v22 offset:18944
v_accvgpr_read_b32 v97,a65
v_mfma_f32_16x16x32_bf16 a[4:7],a[156:159],a[228:231],a[4:7]
ds_read_b64_tr_b16 a[138:139],v22 offset:35328
v_accvgpr_read_b32 v80,a32
v_mfma_f32_16x16x32_bf16 a[36:39],a[156:159],a[236:239],a[36:39]
ds_read_b64_tr_b16 a[142:143],v22 offset:51712
v_accvgpr_read_b32 v81,a33
v_mfma_f32_16x16x32_bf16 a[36:39],a[152:155],a[232:235],a[36:39]
v_accvgpr_read_b32 v112,a96
v_accvgpr_read_b32 v113,a97
v_mfma_f32_16x16x32_bf16 a[36:39],a[148:151],a[204:207],a[36:39]
v_mul_f32 v112,s61,v112
v_mul_f32 v113,s61,v113
v_mfma_f32_16x16x32_bf16 a[36:39],a[144:147],a[200:203],a[36:39]
v_accvgpr_read_b32 v114,a98
v_accvgpr_read_b32 v115,a99
v_mfma_f32_16x16x32_bf16 a[68:71],a[144:147],a[208:211],a[68:71]
v_mul_f32 v96,s61,v96
v_mul_f32 v97,s61,v97
v_mfma_f32_16x16x32_bf16 a[68:71],a[148:151],a[212:215],a[68:71]
v_accvgpr_read_b32 v66,a4
v_accvgpr_read_b32 v67,a5
v_mfma_f32_16x16x32_bf16 a[68:71],a[152:155],a[240:243],a[68:71]
v_accvgpr_read_b32 v12,a6
v_accvgpr_read_b32 v13,a7
v_mfma_f32_16x16x32_bf16 a[68:71],a[156:159],a[244:247],a[68:71]
v_mul_f32 v66,s61,v66
v_mul_f32 v67,s61,v67
v_mfma_f32_16x16x32_bf16 a[100:103],a[156:159],a[252:255],a[100:103]
ds_read_b64_tr_b16 a[156:157],v22 offset:49672
v_accvgpr_read_b32 v98,a66
v_mfma_f32_16x16x32_bf16 a[100:103],a[152:155],a[248:251],a[100:103]
ds_read_b64_tr_b16 a[152:153],v22 offset:33288
v_accvgpr_read_b32 v99,a67
v_mfma_f32_16x16x32_bf16 a[100:103],a[148:151],a[220:223],a[100:103]
ds_read_b64_tr_b16 a[148:149],v22 offset:16904
v_cvt_pk_bf16_f32 v112,v112,v113
v_mfma_f32_16x16x32_bf16 a[100:103],a[144:147],a[216:219],a[100:103]
ds_read_b64_tr_b16 a[144:145],v22 offset:520
v_mul_f32 v114,s61,v114
v_mfma_f32_16x16x32_bf16 a[8:11],a[160:163],a[192:195],a[8:11]
buffer_load_dwordx4 v4,s[24:27],s47 offen offset:2048 nt lds
v_mfma_f32_16x16x32_bf16 a[8:11],a[164:167],a[196:199],a[8:11]
ds_read_b64_tr_b16 a[146:147],v22 offset:2568
v_mul_f32 v115,s61,v115
v_mfma_f32_16x16x32_bf16 a[8:11],a[168:171],a[224:227],a[8:11]
ds_read_b64_tr_b16 a[150:151],v22 offset:18952
v_mul_f32 v12,s61,v12
v_mfma_f32_16x16x32_bf16 a[8:11],a[172:175],a[228:231],a[8:11]
ds_read_b64_tr_b16 a[154:155],v22 offset:35336
v_mul_f32 v13,s61,v13
v_mfma_f32_16x16x32_bf16 a[40:43],a[172:175],a[236:239],a[40:43]
ds_read_b64_tr_b16 a[158:159],v22 offset:51720
v_cvt_pk_bf16_f32 v66,v66,v67
v_mfma_f32_16x16x32_bf16 a[40:43],a[168:171],a[232:235],a[40:43]
v_mul_f32 v80,s61,v80
v_mul_f32 v81,s61,v81
v_mfma_f32_16x16x32_bf16 a[40:43],a[164:167],a[204:207],a[40:43]
v_accvgpr_read_b32 v82,a34
v_accvgpr_read_b32 v83,a35
v_mfma_f32_16x16x32_bf16 a[40:43],a[160:163],a[200:203],a[40:43]
v_cvt_pk_bf16_f32 v96,v96,v97
v_mul_f32 v98,s61,v98
v_mfma_f32_16x16x32_bf16 a[72:75],a[160:163],a[208:211],a[72:75]
v_mul_f32 v99,s61,v99
v_cvt_pk_bf16_f32 v113,v114,v115
v_mfma_f32_16x16x32_bf16 a[72:75],a[164:167],a[212:215],a[72:75]
v_cvt_pk_bf16_f32 v67,v12,v13
v_cvt_pk_bf16_f32 v80,v80,v81
v_mfma_f32_16x16x32_bf16 a[72:75],a[168:171],a[240:243],a[72:75]
v_mul_f32 v82,s61,v82
v_mul_f32 v83,s61,v83
v_mfma_f32_16x16x32_bf16 a[72:75],a[172:175],a[244:247],a[72:75]
v_cvt_pk_bf16_f32 v97,v98,v99
v_accvgpr_read_b32 v114,a100
v_mfma_f32_16x16x32_bf16 a[104:107],a[172:175],a[252:255],a[104:107]
ds_read_b64_tr_b16 a[172:173],v22 offset:49920
v_accvgpr_read_b32 v115,a101
v_mfma_f32_16x16x32_bf16 a[104:107],a[168:171],a[248:251],a[104:107]
ds_read_b64_tr_b16 a[168:169],v22 offset:33536
v_accvgpr_read_b32 v84,a40
v_mfma_f32_16x16x32_bf16 a[104:107],a[164:167],a[220:223],a[104:107]
ds_read_b64_tr_b16 a[164:165],v22 offset:17152
v_accvgpr_read_b32 v85,a41
v_mfma_f32_16x16x32_bf16 a[104:107],a[160:163],a[216:219],a[104:107]
ds_read_b64_tr_b16 a[160:161],v22 offset:768
v_cvt_pk_bf16_f32 v81,v82,v83
v_mfma_f32_16x16x32_bf16 a[12:15],a[176:179],a[192:195],a[12:15]
s_waitcnt lgkmcnt(4)
buffer_load_dwordx4 v5,s[24:27],s47 offen offset:3072 nt lds
v_mfma_f32_16x16x32_bf16 a[12:15],a[180:183],a[196:199],a[12:15]
s_add_u32 s47,s47,16384
s_and_b32 s47,s47,0x3ffff
s_or_b32 s47,s47,s46
v_mfma_f32_16x16x32_bf16 a[12:15],a[184:187],a[224:227],a[12:15]
s_and_b32 s46,s54,0xfffc0000
ds_read_b64_tr_b16 a[162:163],v22 offset:2816
v_accvgpr_read_b32 v100,a72
v_mfma_f32_16x16x32_bf16 a[12:15],a[188:191],a[228:231],a[12:15]
s_add_u32 s54,s54,16384
ds_read_b64_tr_b16 a[166:167],v22 offset:19200
v_accvgpr_read_b32 v101,a73
v_mfma_f32_16x16x32_bf16 a[44:47],a[188:191],a[236:239],a[44:47]
ds_read_b64_tr_b16 a[170:171],v22 offset:35584
v_mul_f32 v100,s61,v100
v_mfma_f32_16x16x32_bf16 a[44:47],a[184:187],a[232:235],a[44:47]
ds_read_b64_tr_b16 a[174:175],v22 offset:51968
v_mul_f32 v101,s61,v101
v_mfma_f32_16x16x32_bf16 a[44:47],a[180:183],a[204:207],a[44:47]
v_accvgpr_read_b32 v102,a74
v_accvgpr_read_b32 v103,a75
v_mfma_f32_16x16x32_bf16 a[44:47],a[176:179],a[200:203],a[44:47]
v_accvgpr_read_b32 v98,a68
v_accvgpr_read_b32 v99,a69
v_mfma_f32_16x16x32_bf16 a[76:79],a[176:179],a[208:211],a[76:79]
v_accvgpr_read_b32 v12,a102
v_accvgpr_read_b32 v13,a103
v_mfma_f32_16x16x32_bf16 a[76:79],a[180:183],a[212:215],a[76:79]
v_mul_f32 v114,s61,v114
v_mul_f32 v115,s61,v115
v_mfma_f32_16x16x32_bf16 a[76:79],a[184:187],a[240:243],a[76:79]
v_accvgpr_read_b32 v68,a8
v_accvgpr_read_b32 v69,a9
v_mfma_f32_16x16x32_bf16 a[76:79],a[188:191],a[244:247],a[76:79]
v_mul_f32 v84,s61,v84
v_mul_f32 v85,s61,v85
v_mfma_f32_16x16x32_bf16 a[108:111],a[188:191],a[252:255],a[108:111]
ds_read_b64_tr_b16 a[188:189],v22 offset:49928
v_accvgpr_read_b32 v86,a42
v_mfma_f32_16x16x32_bf16 a[108:111],a[184:187],a[248:251],a[108:111]
ds_read_b64_tr_b16 a[184:185],v22 offset:33544
v_accvgpr_read_b32 v87,a43
v_mfma_f32_16x16x32_bf16 a[108:111],a[180:183],a[220:223],a[108:111]
ds_read_b64_tr_b16 a[180:181],v22 offset:17160
v_cvt_pk_bf16_f32 v100,v100,v101
v_mfma_f32_16x16x32_bf16 a[108:111],a[176:179],a[216:219],a[108:111]
ds_read_b64_tr_b16 a[176:177],v22 offset:776
v_mul_f32 v102,s61,v102
v_mfma_f32_16x16x32_bf16 a[16:19],a[128:131],a[192:195],a[16:19]
ds_read_b64_tr_b16 a[178:179],v22 offset:2824
v_mul_f32 v103,s61,v103
v_mfma_f32_16x16x32_bf16 a[16:19],a[132:135],a[196:199],a[16:19]
ds_read_b64_tr_b16 a[182:183],v22 offset:19208
v_accvgpr_read_b32 v82,a36
v_mfma_f32_16x16x32_bf16 a[16:19],a[136:139],a[224:227],a[16:19]
ds_read_b64_tr_b16 a[186:187],v22 offset:35592
v_accvgpr_read_b32 v83,a37
v_mfma_f32_16x16x32_bf16 a[16:19],a[140:143],a[228:231],a[16:19]
s_add_u32 m0,131072,s51
ds_read_b64_tr_b16 a[190:191],v22 offset:51976
v_accvgpr_read_b32 v16,a70
v_mfma_f32_16x16x32_bf16 a[48:51],a[140:143],a[236:239],a[48:51]
v_accvgpr_read_b32 v17,a71
v_mul_f32 v98,s61,v98
v_mfma_f32_16x16x32_bf16 a[48:51],a[136:139],a[232:235],a[48:51]
v_mul_f32 v99,s61,v99
v_mul_f32 v12,s61,v12
v_mfma_f32_16x16x32_bf16 a[48:51],a[132:135],a[204:207],a[48:51]
buffer_load_dwordx4 v4,s[20:23],s47 offen nt lds
v_mfma_f32_16x16x32_bf16 a[48:51],a[128:131],a[200:203],a[48:51]
v_mul_f32 v13,s61,v13
v_cvt_pk_bf16_f32 v114,v114,v115
v_mfma_f32_16x16x32_bf16 a[80:83],a[128:131],a[208:211],a[80:83]
v_mul_f32 v68,s61,v68
v_mul_f32 v69,s61,v69
v_mfma_f32_16x16x32_bf16 a[80:83],a[132:135],a[212:215],a[80:83]
v_accvgpr_read_b32 v70,a10
v_accvgpr_read_b32 v71,a11
v_mfma_f32_16x16x32_bf16 a[80:83],a[136:139],a[240:243],a[80:83]
v_cvt_pk_bf16_f32 v84,v84,v85
v_mul_f32 v86,s61,v86
v_mfma_f32_16x16x32_bf16 a[80:83],a[140:143],a[244:247],a[80:83]
v_mul_f32 v87,s61,v87
v_cvt_pk_bf16_f32 v101,v102,v103
v_mfma_f32_16x16x32_bf16 a[112:115],a[140:143],a[252:255],a[112:115]
ds_read_b64_tr_b16 a[140:141],v22 offset:50184
v_accvgpr_read_b32 v14,a38
v_mfma_f32_16x16x32_bf16 a[112:115],a[136:139],a[248:251],a[112:115]
ds_read_b64_tr_b16 a[136:137],v22 offset:33800
v_accvgpr_read_b32 v88,a48
v_mfma_f32_16x16x32_bf16 a[112:115],a[132:135],a[220:223],a[112:115]
ds_read_b64_tr_b16 a[132:133],v22 offset:50176
v_accvgpr_read_b32 v89,a49
v_mfma_f32_16x16x32_bf16 a[112:115],a[128:131],a[216:219],a[112:115]
ds_read_b64_tr_b16 a[128:129],v22 offset:33792
v_accvgpr_read_b32 v15,a39
v_mfma_f32_16x16x32_bf16 a[20:23],a[144:147],a[192:195],a[20:23]
ds_read_b64_tr_b16 a[130:131],v22 offset:35840
v_mul_f32 v82,s61,v82
v_mfma_f32_16x16x32_bf16 a[20:23],a[148:151],a[196:199],a[20:23]
ds_read_b64_tr_b16 a[134:135],v22 offset:52224
v_mul_f32 v83,s61,v83
v_mfma_f32_16x16x32_bf16 a[20:23],a[152:155],a[224:227],a[20:23]
ds_read_b64_tr_b16 a[138:139],v22 offset:35848
v_mul_f32 v16,s61,v16
v_mfma_f32_16x16x32_bf16 a[20:23],a[156:159],a[228:231],a[20:23]
ds_read_b64_tr_b16 a[142:143],v22 offset:52232
v_mul_f32 v17,s61,v17
v_mfma_f32_16x16x32_bf16 a[52:55],a[156:159],a[236:239],a[52:55]
v_cvt_pk_bf16_f32 v98,v98,v99
v_cvt_pk_bf16_f32 v115,v12,v13
v_mfma_f32_16x16x32_bf16 a[52:55],a[152:155],a[232:235],a[52:55]
v_cvt_pk_bf16_f32 v68,v68,v69
v_mul_f32 v70,s61,v70
v_mfma_f32_16x16x32_bf16 a[52:55],a[148:151],a[204:207],a[52:55]
buffer_load_dwordx4 v4,s[20:23],s47 offen offset:1024 nt lds
v_mfma_f32_16x16x32_bf16 a[52:55],a[144:147],a[200:203],a[52:55]
v_mul_f32 v71,s61,v71
v_cvt_pk_bf16_f32 v85,v86,v87
v_mfma_f32_16x16x32_bf16 a[84:87],a[144:147],a[208:211],a[84:87]
v_accvgpr_read_b32 v102,a76
v_accvgpr_read_b32 v103,a77
v_mfma_f32_16x16x32_bf16 a[84:87],a[148:151],a[212:215],a[84:87]
v_accvgpr_read_b32 v72,a16
v_accvgpr_read_b32 v73,a17
v_mfma_f32_16x16x32_bf16 a[84:87],a[152:155],a[240:243],a[84:87]
v_mul_f32 v88,s61,v88
v_mul_f32 v89,s61,v89
v_mfma_f32_16x16x32_bf16 a[84:87],a[156:159],a[244:247],a[84:87]
v_accvgpr_read_b32 v90,a50
v_accvgpr_read_b32 v91,a51
v_mfma_f32_16x16x32_bf16 a[116:119],a[156:159],a[252:255],a[116:119]
ds_read_b64_tr_b16 a[156:157],v22 offset:50440
v_mul_f32 v14,s61,v14
v_mfma_f32_16x16x32_bf16 a[116:119],a[152:155],a[248:251],a[116:119]
ds_read_b64_tr_b16 a[152:153],v22 offset:34056
v_mul_f32 v15,s61,v15
v_mfma_f32_16x16x32_bf16 a[116:119],a[148:151],a[220:223],a[116:119]
ds_read_b64_tr_b16 a[148:149],v22 offset:50432
v_cvt_pk_bf16_f32 v82,v82,v83
v_mfma_f32_16x16x32_bf16 a[116:119],a[144:147],a[216:219],a[116:119]
s_waitcnt lgkmcnt(3)
ds_read_b64_tr_b16 a[144:145],v22 offset:34048
v_cvt_pk_bf16_f32 v99,v16,v17
v_mfma_f32_16x16x32_bf16 a[24:27],a[160:163],a[192:195],a[24:27]
ds_read_b64_tr_b16 a[146:147],v22 offset:36096
v_cvt_pk_bf16_f32 v69,v70,v71
v_mfma_f32_16x16x32_bf16 a[24:27],a[164:167],a[196:199],a[24:27]
ds_read_b64_tr_b16 a[150:151],v22 offset:52480
v_accvgpr_read_b32 v86,a44
v_mfma_f32_16x16x32_bf16 a[24:27],a[168:171],a[224:227],a[24:27]
ds_read_b64_tr_b16 a[154:155],v22 offset:36104
v_accvgpr_read_b32 v87,a45
v_mfma_f32_16x16x32_bf16 a[24:27],a[172:175],a[228:231],a[24:27]
ds_read_b64_tr_b16 a[158:159],v22 offset:52488
v_accvgpr_read_b32 v12,a78
v_mfma_f32_16x16x32_bf16 a[56:59],a[172:175],a[236:239],a[56:59]
v_accvgpr_read_b32 v13,a79
v_mul_f32 v102,s61,v102
v_mfma_f32_16x16x32_bf16 a[56:59],a[168:171],a[232:235],a[56:59]
)A"
R"A(v_mul_f32 v103,s61,v103
v_mul_f32 v72,s61,v72
v_mfma_f32_16x16x32_bf16 a[56:59],a[164:167],a[204:207],a[56:59]
buffer_load_dwordx4 v5,s[20:23],s47 offen offset:2048 nt lds
v_mfma_f32_16x16x32_bf16 a[56:59],a[160:163],a[200:203],a[56:59]
v_mul_f32 v73,s61,v73
v_accvgpr_read_b32 v74,a18
v_mfma_f32_16x16x32_bf16 a[88:91],a[160:163],a[208:211],a[88:91]
v_accvgpr_read_b32 v75,a19
v_cvt_pk_bf16_f32 v88,v88,v89
v_mfma_f32_16x16x32_bf16 a[88:91],a[164:167],a[212:215],a[88:91]
v_mul_f32 v90,s61,v90
v_mul_f32 v91,s61,v91
v_mfma_f32_16x16x32_bf16 a[88:91],a[168:171],a[240:243],a[88:91]
v_cvt_pk_bf16_f32 v83,v14,v15
v_accvgpr_read_b32 v70,a12
v_mfma_f32_16x16x32_bf16 a[88:91],a[172:175],a[244:247],a[88:91]
v_accvgpr_read_b32 v71,a13
v_accvgpr_read_b32 v16,a46
v_mfma_f32_16x16x32_bf16 a[120:123],a[172:175],a[252:255],a[120:123]
ds_read_b64_tr_b16 a[172:173],v22 offset:50696
v_accvgpr_read_b32 v17,a47
v_mfma_f32_16x16x32_bf16 a[120:123],a[168:171],a[248:251],a[120:123]
ds_read_b64_tr_b16 a[168:169],v22 offset:34312
v_mul_f32 v86,s61,v86
v_mfma_f32_16x16x32_bf16 a[120:123],a[164:167],a[220:223],a[120:123]
ds_read_b64_tr_b16 a[164:165],v22 offset:50688
v_mul_f32 v87,s61,v87
v_mfma_f32_16x16x32_bf16 a[120:123],a[160:163],a[216:219],a[120:123]
ds_read_b64_tr_b16 a[160:161],v22 offset:34304
v_mul_f32 v12,s61,v12
v_mfma_f32_16x16x32_bf16 a[28:31],a[176:179],a[192:195],a[28:31]
ds_read_b64_tr_b16 a[162:163],v22 offset:36352
v_mul_f32 v13,s61,v13
v_mfma_f32_16x16x32_bf16 a[28:31],a[180:183],a[196:199],a[28:31]
ds_read_b64_tr_b16 a[166:167],v22 offset:52736
v_cvt_pk_bf16_f32 v102,v102,v103
v_mfma_f32_16x16x32_bf16 a[28:31],a[184:187],a[224:227],a[28:31]
ds_read_b64_tr_b16 a[170:171],v22 offset:36360
v_cvt_pk_bf16_f32 v72,v72,v73
v_mfma_f32_16x16x32_bf16 a[28:31],a[188:191],a[228:231],a[28:31]
ds_read_b64_tr_b16 a[174:175],v22 offset:52744
v_mul_f32 v74,s61,v74
v_mfma_f32_16x16x32_bf16 a[60:63],a[188:191],a[236:239],a[60:63]
v_mul_f32 v75,s61,v75
v_cvt_pk_bf16_f32 v89,v90,v91
v_mfma_f32_16x16x32_bf16 a[60:63],a[184:187],a[232:235],a[60:63]
v_accvgpr_read_b32 v76,a24
v_accvgpr_read_b32 v77,a25
v_mfma_f32_16x16x32_bf16 a[60:63],a[180:183],a[204:207],a[60:63]
buffer_load_dwordx4 v5,s[20:23],s47 offen offset:3072 nt lds
v_mfma_f32_16x16x32_bf16 a[60:63],a[176:179],a[200:203],a[60:63]
v_accvgpr_read_b32 v14,a14
v_accvgpr_read_b32 v15,a15
v_mfma_f32_16x16x32_bf16 a[92:95],a[176:179],a[208:211],a[92:95]
v_mul_f32 v70,s61,v70
v_mul_f32 v71,s61,v71
v_mfma_f32_16x16x32_bf16 a[92:95],a[180:183],a[212:215],a[92:95]
v_mul_f32 v16,s61,v16
v_mul_f32 v17,s61,v17
v_mfma_f32_16x16x32_bf16 a[92:95],a[184:187],a[240:243],a[92:95]
v_cvt_pk_bf16_f32 v86,v86,v87
v_cvt_pk_bf16_f32 v103,v12,v13
v_mfma_f32_16x16x32_bf16 a[92:95],a[188:191],a[244:247],a[92:95]
v_cvt_pk_bf16_f32 v73,v74,v75
v_accvgpr_read_b32 v90,a52
v_mfma_f32_16x16x32_bf16 a[124:127],a[188:191],a[252:255],a[124:127]
ds_read_b64_tr_b16 a[188:189],v22 offset:50952
v_accvgpr_read_b32 v91,a53
v_mfma_f32_16x16x32_bf16 a[124:127],a[184:187],a[248:251],a[124:127]
ds_read_b64_tr_b16 a[184:185],v22 offset:34568
v_mul_f32 v76,s61,v76
v_mfma_f32_16x16x32_bf16 a[124:127],a[180:183],a[220:223],a[124:127]
ds_read_b64_tr_b16 a[180:181],v22 offset:50944
v_mul_f32 v77,s61,v77
v_mfma_f32_16x16x32_bf16 a[124:127],a[176:179],a[216:219],a[124:127]
ds_read_b64_tr_b16 a[176:177],v22 offset:34560
v_accvgpr_read_b32 v78,a26
v_mfma_f32_16x16x32_bf16 v[128:131],a[128:131],a[224:227],v[128:131]
ds_read_b64_tr_b16 a[178:179],v22 offset:36608
v_accvgpr_read_b32 v79,a27
v_mfma_f32_16x16x32_bf16 v[128:131],a[132:135],a[228:231],v[128:131]
ds_read_b64_tr_b16 a[182:183],v22 offset:52992
v_mul_f32 v14,s61,v14
v_mfma_f32_16x16x32_bf16 v[136:139],a[132:135],a[236:239],v[136:139]
ds_read_b64_tr_b16 a[186:187],v22 offset:36616
v_mul_f32 v15,s61,v15
v_mfma_f32_16x16x32_bf16 v[136:139],a[128:131],a[232:235],v[136:139]
s_add_u32 m0,131072,s52
ds_read_b64_tr_b16 a[190:191],v22 offset:53000
v_cvt_pk_bf16_f32 v70,v70,v71
v_mfma_f32_16x16x32_bf16 v[144:147],a[128:131],a[240:243],v[144:147]
v_cvt_pk_bf16_f32 v87,v16,v17
v_accvgpr_read_b32 v116,a104
v_mfma_f32_16x16x32_bf16 v[144:147],a[132:135],a[244:247],v[144:147]
v_accvgpr_read_b32 v117,a105
v_accvgpr_read_b32 v74,a20
v_mfma_f32_16x16x32_bf16 v[152:155],a[132:135],a[252:255],v[152:155]
buffer_load_dwordx4 v4,s[24:27],s47 offen nt lds
v_mfma_f32_16x16x32_bf16 v[152:155],a[128:131],a[248:251],v[152:155]
v_accvgpr_read_b32 v75,a21
v_accvgpr_read_b32 v12,a54
v_mfma_f32_16x16x32_bf16 v[132:135],a[136:139],a[224:227],v[132:135]
v_accvgpr_read_b32 v13,a55
v_mul_f32 v90,s61,v90
v_mfma_f32_16x16x32_bf16 v[132:135],a[140:143],a[228:231],v[132:135]
v_mul_f32 v91,s61,v91
v_cvt_pk_bf16_f32 v76,v76,v77
v_mfma_f32_16x16x32_bf16 v[140:143],a[140:143],a[236:239],v[140:143]
v_mul_f32 v78,s61,v78
v_mul_f32 v79,s61,v79
v_mfma_f32_16x16x32_bf16 v[140:143],a[136:139],a[232:235],v[140:143]
v_cvt_pk_bf16_f32 v71,v14,v15
v_mul_f32 v116,s61,v116
v_mfma_f32_16x16x32_bf16 v[148:151],a[136:139],a[240:243],v[148:151]
v_mul_f32 v117,s61,v117
v_accvgpr_read_b32 v118,a106
v_mfma_f32_16x16x32_bf16 v[148:151],a[140:143],a[244:247],v[148:151]
v_accvgpr_read_b32 v119,a107
v_accvgpr_read_b32 v16,a22
v_mfma_f32_16x16x32_bf16 v[156:159],a[140:143],a[252:255],v[156:159]
s_waitcnt vmcnt(5) lgkmcnt(0)
v_accvgpr_read_b32 v17,a23
v_mul_f32 v74,s61,v74
v_mfma_f32_16x16x32_bf16 v[156:159],a[136:139],a[248:251],v[156:159]
s_barrier
ds_read_b128 v[48:51],v18
v_mul_f32 v75,s61,v75
v_mfma_f32_16x16x32_bf16 v[160:163],a[144:147],a[224:227],v[160:163]
v_mul_f32 v12,s61,v12
v_mul_f32 v13,s61,v13
v_mfma_f32_16x16x32_bf16 v[160:163],a[148:151],a[228:231],v[160:163]
ds_read_b128 v[52:55],v18 offset:256
v_cvt_pk_bf16_f32 v90,v90,v91
v_mfma_f32_16x16x32_bf16 v[168:171],a[148:151],a[236:239],v[168:171]
v_cvt_pk_bf16_f32 v77,v78,v79
v_cvt_pk_bf16_f32 v116,v116,v117
v_mfma_f32_16x16x32_bf16 v[168:171],a[144:147],a[232:235],v[168:171]
ds_read_b128 v[56:59],v18 offset:512
v_mul_f32 v118,s61,v118
v_mfma_f32_16x16x32_bf16 v[176:179],a[144:147],a[240:243],v[176:179]
v_mul_f32 v119,s61,v119
v_mul_f32 v16,s61,v16
v_mfma_f32_16x16x32_bf16 v[176:179],a[148:151],a[244:247],v[176:179]
buffer_load_dwordx4 v5,s[24:27],s47 offen offset:1024 nt lds
v_mfma_f32_16x16x32_bf16 v[184:187],a[148:151],a[252:255],v[184:187]
ds_read_b128 v[60:63],v18 offset:768
v_mul_f32 v17,s61,v17
v_mfma_f32_16x16x32_bf16 v[184:187],a[144:147],a[248:251],v[184:187]
v_cvt_pk_bf16_f32 v74,v74,v75
v_cvt_pk_bf16_f32 v91,v12,v13
v_mfma_f32_16x16x32_bf16 v[164:167],a[152:155],a[224:227],v[164:167]
v_accvgpr_read_b32 v78,a28
v_accvgpr_read_b32 v79,a29
v_mfma_f32_16x16x32_bf16 v[164:167],a[156:159],a[228:231],v[164:167]
v_cvt_pk_bf16_f32 v117,v118,v119
v_cvt_pk_bf16_f32 v75,v16,v17
v_mfma_f32_16x16x32_bf16 v[172:175],a[156:159],a[236:239],v[172:175]
v_accvgpr_read_b32 v104,a80
v_accvgpr_read_b32 v105,a81
v_mfma_f32_16x16x32_bf16 v[172:175],a[152:155],a[232:235],v[172:175]
v_accvgpr_read_b32 v12,a30
v_accvgpr_read_b32 v13,a31
v_mfma_f32_16x16x32_bf16 v[180:183],a[152:155],a[240:243],v[180:183]
v_mul_f32 v78,s61,v78
v_mul_f32 v79,s61,v79
v_mfma_f32_16x16x32_bf16 v[180:183],a[156:159],a[244:247],v[180:183]
v_accvgpr_read_b32 v118,a108
v_accvgpr_read_b32 v119,a109
v_mfma_f32_16x16x32_bf16 v[188:191],a[156:159],a[252:255],v[188:191]
v_mul_f32 v104,s61,v104
v_mul_f32 v105,s61,v105
v_mfma_f32_16x16x32_bf16 v[188:191],a[152:155],a[248:251],v[188:191]
v_accvgpr_read_b32 v106,a82
v_accvgpr_read_b32 v107,a83
v_mfma_f32_16x16x32_bf16 v[192:195],a[160:163],a[224:227],v[192:195]
v_mul_f32 v12,s61,v12
v_mul_f32 v13,s61,v13
v_mfma_f32_16x16x32_bf16 v[192:195],a[164:167],a[228:231],v[192:195]
v_cvt_pk_bf16_f32 v78,v78,v79
v_accvgpr_read_b32 v14,a110
v_mfma_f32_16x16x32_bf16 v[200:203],a[164:167],a[236:239],v[200:203]
v_accvgpr_read_b32 v15,a111
v_mul_f32 v118,s61,v118
v_mfma_f32_16x16x32_bf16 v[200:203],a[160:163],a[232:235],v[200:203]
v_mul_f32 v119,s61,v119
v_cvt_pk_bf16_f32 v104,v104,v105
v_mfma_f32_16x16x32_bf16 v[208:211],a[160:163],a[240:243],v[208:211]
v_mul_f32 v106,s61,v106
v_mul_f32 v107,s61,v107
v_mfma_f32_16x16x32_bf16 v[208:211],a[164:167],a[244:247],v[208:211]
buffer_load_dwordx4 v4,s[24:27],s47 offen offset:2048 nt lds
v_mfma_f32_16x16x32_bf16 v[216:219],a[164:167],a[252:255],v[216:219]
v_cvt_pk_bf16_f32 v79,v12,v13
v_mul_f32 v14,s61,v14
v_mfma_f32_16x16x32_bf16 v[216:219],a[160:163],a[248:251],v[216:219]
v_mul_f32 v15,s61,v15
v_cvt_pk_bf16_f32 v118,v118,v119
v_mfma_f32_16x16x32_bf16 v[196:199],a[168:171],a[224:227],v[196:199]
v_cvt_pk_bf16_f32 v105,v106,v107
v_accvgpr_read_b32 v92,a56
v_mfma_f32_16x16x32_bf16 v[196:199],a[172:175],a[228:231],v[196:199]
v_accvgpr_read_b32 v93,a57
v_cvt_pk_bf16_f32 v119,v14,v15
v_mfma_f32_16x16x32_bf16 v[204:207],a[172:175],a[236:239],v[204:207]
v_accvgpr_read_b32 v106,a84
v_accvgpr_read_b32 v107,a85
v_mfma_f32_16x16x32_bf16 v[204:207],a[168:171],a[232:235],v[204:207]
v_mul_f32 v92,s61,v92
v_mul_f32 v93,s61,v93
v_mfma_f32_16x16x32_bf16 v[212:215],a[168:171],a[240:243],v[212:215]
v_accvgpr_read_b32 v94,a58
v_accvgpr_read_b32 v95,a59
v_mfma_f32_16x16x32_bf16 v[212:215],a[172:175],a[244:247],v[212:215]
v_accvgpr_read_b32 v14,a86
v_accvgpr_read_b32 v15,a87
v_mfma_f32_16x16x32_bf16 v[220:223],a[172:175],a[252:255],v[220:223]
v_mul_f32 v106,s61,v106
v_mul_f32 v107,s61,v107
v_mfma_f32_16x16x32_bf16 v[220:223],a[168:171],a[248:251],v[220:223]
v_cvt_pk_bf16_f32 v92,v92,v93
v_mul_f32 v94,s61,v94
v_mfma_f32_16x16x32_bf16 v[224:227],a[176:179],a[224:227],v[224:227]
v_mul_f32 v95,s61,v95
v_mul_f32 v14,s61,v14
v_mfma_f32_16x16x32_bf16 v[224:227],a[180:183],a[228:231],v[224:227]
v_mul_f32 v15,s61,v15
v_cvt_pk_bf16_f32 v106,v106,v107
v_mfma_f32_16x16x32_bf16 v[232:235],a[180:183],a[236:239],v[232:235]
v_cvt_pk_bf16_f32 v93,v94,v95
v_cvt_pk_bf16_f32 v107,v14,v15
v_mfma_f32_16x16x32_bf16 v[232:235],a[176:179],a[232:235],v[232:235]
v_accvgpr_read_b32 v94,a60
v_accvgpr_read_b32 v95,a61
v_mfma_f32_16x16x32_bf16 v[240:243],a[176:179],a[240:243],v[240:243]
v_accvgpr_read_b32 v120,a112
v_accvgpr_read_b32 v121,a113
v_mfma_f32_16x16x32_bf16 v[240:243],a[180:183],a[244:247],v[240:243]
buffer_load_dwordx4 v5,s[24:27],s47 offen offset:3072 nt lds
v_mfma_f32_16x16x32_bf16 v[248:251],a[180:183],a[252:255],v[248:251]
s_add_u32 s47,s47,16384
v_accvgpr_read_b32 v14,a62
v_accvgpr_read_b32 v15,a63
v_mfma_f32_16x16x32_bf16 v[248:251],a[176:179],a[248:251],v[248:251]
s_and_b32 s47,s47,0x3ffff
v_mul_f32 v94,s61,v94
v_mul_f32 v95,s61,v95
v_mfma_f32_16x16x32_bf16 v[228:231],a[184:187],a[224:227],v[228:231]
s_or_b32 s47,s47,s46
v_mul_f32 v120,s61,v120
v_mul_f32 v121,s61,v121
v_mfma_f32_16x16x32_bf16 v[228:231],a[188:191],a[228:231],v[228:231]
s_and_b32 s46,s54,0xfffc0000
v_accvgpr_read_b32 v122,a114
v_accvgpr_read_b32 v123,a115
v_mfma_f32_16x16x32_bf16 v[236:239],a[188:191],a[236:239],v[236:239]
s_add_u32 s54,s54,16384
v_mul_f32 v14,s61,v14
v_mul_f32 v15,s61,v15
v_mfma_f32_16x16x32_bf16 v[236:239],a[184:187],a[232:235],v[236:239]
v_cvt_pk_bf16_f32 v94,v94,v95
v_cvt_pk_bf16_f32 v120,v120,v121
v_mfma_f32_16x16x32_bf16 v[244:247],a[184:187],a[240:243],v[244:247]
v_mul_f32 v122,s61,v122
v_mul_f32 v123,s61,v123
v_mfma_f32_16x16x32_bf16 v[244:247],a[188:191],a[244:247],v[244:247]
v_cvt_pk_bf16_f32 v95,v14,v15
v_cvt_pk_bf16_f32 v121,v122,v123
v_mfma_f32_16x16x32_bf16 v[252:255],a[188:191],a[252:255],v[252:255]
v_accvgpr_read_b32 v108,a88
v_accvgpr_read_b32 v109,a89
v_mfma_f32_16x16x32_bf16 v[252:255],a[184:187],a[248:251],v[252:255]
s_waitcnt lgkmcnt(0)
v_accvgpr_read_b32 v122,a116
v_accvgpr_read_b32 v123,a117
v_mfma_f32_16x16x32_bf16 v[32:35],v[48:51],v[64:67],0
v_mul_f32 v108,s61,v108
v_mul_f32 v109,s61,v109
v_mfma_f32_16x16x32_bf16 v[32:35],v[52:55],v[68:71],v[32:35]
v_accvgpr_read_b32 v110,a90
v_accvgpr_read_b32 v111,a91
v_mfma_f32_16x16x32_bf16 v[32:35],v[56:59],v[72:75],v[32:35]
v_accvgpr_read_b32 v16,a118
v_accvgpr_read_b32 v17,a119
v_mfma_f32_16x16x32_bf16 v[32:35],v[60:63],v[76:79],v[32:35]
v_mul_f32 v122,s61,v122
v_mul_f32 v123,s61,v123
v_mfma_f32_16x16x32_bf16 v[36:39],v[48:51],v[80:83],0
)A"
R"A(v_cvt_pk_bf16_f32 v108,v108,v109
v_mul_f32 v110,s61,v110
v_mfma_f32_16x16x32_bf16 v[36:39],v[52:55],v[84:87],v[36:39]
v_mul_f32 v111,s61,v111
v_mul_f32 v16,s61,v16
v_mfma_f32_16x16x32_bf16 v[36:39],v[56:59],v[88:91],v[36:39]
v_mul_f32 v17,s61,v17
v_cvt_pk_bf16_f32 v122,v122,v123
v_cvt_pk_bf16_f32 v109,v110,v111
v_mfma_f32_16x16x32_bf16 v[36:39],v[60:63],v[92:95],v[36:39]
ds_read_b128 a[128:131],v1
v_cvt_pk_bf16_f32 v123,v16,v17
v_accvgpr_read_b32 v110,a92
v_accvgpr_read_b32 v111,a93
v_accvgpr_read_b32 v16,a94
v_accvgpr_read_b32 v17,a95
v_mul_f32 v110,s61,v110
v_mul_f32 v111,s61,v111
v_mfma_f32_16x16x32_bf16 v[40:43],v[48:51],v[96:99],0
ds_read_b128 a[132:135],v1 offset:256
v_mul_f32 v16,s61,v16
v_mul_f32 v17,s61,v17
v_cvt_pk_bf16_f32 v110,v110,v111
v_cvt_pk_bf16_f32 v111,v16,v17
v_mfma_f32_16x16x32_bf16 v[40:43],v[52:55],v[100:103],v[40:43]
ds_read_b128 a[136:139],v1 offset:512
v_accvgpr_read_b32 v124,a120
v_accvgpr_read_b32 v125,a121
v_mul_f32 v124,s61,v124
v_mul_f32 v125,s61,v125
v_accvgpr_read_b32 v126,a122
v_accvgpr_read_b32 v127,a123
v_mfma_f32_16x16x32_bf16 v[40:43],v[56:59],v[104:107],v[40:43]
ds_read_b128 a[140:143],v1 offset:768
v_cvt_pk_bf16_f32 v124,v124,v125
v_mul_f32 v126,s61,v126
v_mul_f32 v127,s61,v127
v_cvt_pk_bf16_f32 v125,v126,v127
v_mfma_f32_16x16x32_bf16 v[40:43],v[60:63],v[108:111],v[40:43]
s_add_u32 m0,0,s51
ds_read_b128 a[156:159],v1 offset:4864
v_accvgpr_read_b32 v126,a124
v_accvgpr_read_b32 v127,a125
v_accvgpr_read_b32 v12,a126
v_accvgpr_read_b32 v13,a127
v_mul_f32 v126,s61,v126
v_mul_f32 v127,s61,v127
v_mfma_f32_16x16x32_bf16 v[44:47],v[48:51],v[112:115],0
ds_read_b128 a[152:155],v1 offset:4608
v_mul_f32 v12,s61,v12
v_mul_f32 v13,s61,v13
v_cvt_pk_bf16_f32 v126,v126,v127
v_max3_f32 v32,v32,v33,v34
v_cvt_pk_bf16_f32 v127,v12,v13
v_max3_f32 v32,v32,v35,s18
v_mfma_f32_16x16x32_bf16 v[44:47],v[52:55],v[116:119],v[44:47]
ds_read_b128 a[148:151],v1 offset:4352
v_sub_f32 v35,0,v32
v_sub_f32 v34,0,v32
v_sub_f32 v33,0,v32
v_sub_f32 v32,0,v32
v_mfma_f32_16x16x32_bf16 v[44:47],v[56:59],v[120:123],v[44:47]
ds_read_b128 a[144:147],v1 offset:4096
v_mul_f32 v128,s61,v128
v_mul_f32 v129,s61,v129
v_cvt_pk_bf16_f32 v128,v128,v129
v_mul_f32 v130,s61,v130
v_mul_f32 v131,s61,v131
v_mfma_f32_16x16x32_bf16 v[44:47],v[60:63],v[124:127],v[44:47]
s_waitcnt lgkmcnt(7)
buffer_load_dwordx4 v4,s[20:23],s47 offen nt lds
ds_read_b128 a[160:163],v1 offset:8192
v_cvt_pk_bf16_f32 v129,v130,v131
v_mul_f32 v132,s61,v132
v_mul_f32 v133,s61,v133
v_mul_f32 v134,s61,v134
v_mul_f32 v135,s61,v135
v_cvt_pk_bf16_f32 v130,v132,v133
v_cvt_pk_bf16_f32 v131,v134,v135
v_mfma_f32_16x16x32_bf16 v[132:135],a[128:131],v[64:67],v[32:35]
s_waitcnt lgkmcnt(7)
ds_read_b128 a[164:167],v1 offset:8448
v_mfma_f32_16x16x32_bf16 v[132:135],a[132:135],v[68:71],v[132:135]
s_waitcnt lgkmcnt(7)
ds_read_b128 a[168:171],v1 offset:8704
v_mfma_f32_16x16x32_bf16 v[132:135],a[136:139],v[72:75],v[132:135]
s_waitcnt lgkmcnt(7)
ds_read_b128 a[172:175],v1 offset:8960
v_mul_f32 v136,s61,v136
v_mul_f32 v137,s61,v137
v_cvt_pk_bf16_f32 v136,v136,v137
v_mul_f32 v138,s61,v138
v_mul_f32 v139,s61,v139
v_mfma_f32_16x16x32_bf16 v[132:135],a[140:143],v[76:79],v[132:135]
s_waitcnt lgkmcnt(7)
ds_read_b128 a[188:191],v1 offset:13056
v_cvt_pk_bf16_f32 v137,v138,v139
v_mul_f32 v140,s61,v140
v_mul_f32 v141,s61,v141
v_mul_f32 v142,s61,v142
v_mul_f32 v143,s61,v143
v_cvt_pk_bf16_f32 v138,v140,v141
v_cvt_pk_bf16_f32 v139,v142,v143
v_mfma_f32_16x16x32_bf16 v[140:143],a[156:159],v[76:79],v[32:35]
s_waitcnt lgkmcnt(7)
ds_read_b128 a[184:187],v1 offset:12800
v_mfma_f32_16x16x32_bf16 v[140:143],a[152:155],v[72:75],v[140:143]
s_waitcnt lgkmcnt(7)
ds_read_b128 a[180:183],v1 offset:12544
v_mfma_f32_16x16x32_bf16 v[140:143],a[148:151],v[68:71],v[140:143]
s_waitcnt lgkmcnt(7)
ds_read_b128 a[176:179],v1 offset:12288
v_mul_f32 v144,s61,v144
v_mul_f32 v145,s61,v145
v_cvt_pk_bf16_f32 v144,v144,v145
v_mul_f32 v146,s61,v146
v_mul_f32 v147,s61,v147
v_mfma_f32_16x16x32_bf16 v[140:143],a[144:147],v[64:67],v[140:143]
s_waitcnt lgkmcnt(7)
v_cvt_pk_bf16_f32 v145,v146,v147
v_mul_f32 v148,s61,v148
v_mul_f32 v149,s61,v149
v_mul_f32 v150,s61,v150
v_mul_f32 v151,s61,v151
v_cvt_pk_bf16_f32 v146,v148,v149
v_cvt_pk_bf16_f32 v147,v150,v151
v_mfma_f32_16x16x32_bf16 v[148:151],a[160:163],v[64:67],v[32:35]
s_waitcnt lgkmcnt(6)
buffer_load_dwordx4 v4,s[20:23],s47 offen offset:1024 nt lds
v_mfma_f32_16x16x32_bf16 v[148:151],a[164:167],v[68:71],v[148:151]
s_waitcnt lgkmcnt(5)
v_mfma_f32_16x16x32_bf16 v[148:151],a[168:171],v[72:75],v[148:151]
s_waitcnt lgkmcnt(4)
v_mul_f32 v152,s61,v152
v_mul_f32 v153,s61,v153
v_cvt_pk_bf16_f32 v152,v152,v153
v_mul_f32 v154,s61,v154
v_mul_f32 v155,s61,v155
v_mfma_f32_16x16x32_bf16 v[148:151],a[172:175],v[76:79],v[148:151]
s_waitcnt lgkmcnt(3)
v_cvt_pk_bf16_f32 v153,v154,v155
v_mul_f32 v156,s61,v156
v_mul_f32 v157,s61,v157
v_mul_f32 v158,s61,v158
v_mul_f32 v159,s61,v159
v_cvt_pk_bf16_f32 v154,v156,v157
v_cvt_pk_bf16_f32 v155,v158,v159
v_mfma_f32_16x16x32_bf16 v[156:159],a[188:191],v[76:79],v[32:35]
s_waitcnt lgkmcnt(2)
v_max3_f32 v36,v36,v37,v38
v_max3_f32 v36,v36,v39,s19
v_mfma_f32_16x16x32_bf16 v[156:159],a[184:187],v[72:75],v[156:159]
s_waitcnt lgkmcnt(1)
v_sub_f32 v39,0,v36
v_sub_f32 v38,0,v36
v_sub_f32 v37,0,v36
v_sub_f32 v36,0,v36
v_mfma_f32_16x16x32_bf16 v[156:159],a[180:183],v[68:71],v[156:159]
s_waitcnt lgkmcnt(0)
v_mul_f32 v160,s61,v160
v_mul_f32 v161,s61,v161
v_cvt_pk_bf16_f32 v160,v160,v161
v_mul_f32 v162,s61,v162
v_mul_f32 v163,s61,v163
v_mfma_f32_16x16x32_bf16 v[156:159],a[176:179],v[64:67],v[156:159]
v_cvt_pk_bf16_f32 v161,v162,v163
v_mul_f32 v164,s61,v164
v_mul_f32 v165,s61,v165
v_mul_f32 v166,s61,v166
v_mul_f32 v167,s61,v167
v_cvt_pk_bf16_f32 v162,v164,v165
v_cvt_pk_bf16_f32 v163,v166,v167
v_mfma_f32_16x16x32_bf16 v[164:167],a[128:131],v[80:83],v[36:39]
buffer_load_dwordx4 v5,s[20:23],s47 offen offset:2048 nt lds
v_mfma_f32_16x16x32_bf16 v[164:167],a[132:135],v[84:87],v[164:167]
v_mul_f32 v168,s61,v168
v_mul_f32 v169,s61,v169
v_mfma_f32_16x16x32_bf16 v[164:167],a[136:139],v[88:91],v[164:167]
v_cvt_pk_bf16_f32 v168,v168,v169
v_mul_f32 v170,s61,v170
v_mul_f32 v171,s61,v171
v_mfma_f32_16x16x32_bf16 v[164:167],a[140:143],v[92:95],v[164:167]
v_cvt_pk_bf16_f32 v169,v170,v171
v_mul_f32 v172,s61,v172
v_mul_f32 v173,s61,v173
v_mul_f32 v174,s61,v174
v_mul_f32 v175,s61,v175
v_cvt_pk_bf16_f32 v170,v172,v173
v_cvt_pk_bf16_f32 v171,v174,v175
v_mfma_f32_16x16x32_bf16 v[172:175],a[156:159],v[92:95],v[36:39]
v_mul_f32 v176,s61,v176
v_mul_f32 v177,s61,v177
v_mfma_f32_16x16x32_bf16 v[172:175],a[152:155],v[88:91],v[172:175]
v_cvt_pk_bf16_f32 v176,v176,v177
v_mul_f32 v178,s61,v178
v_mfma_f32_16x16x32_bf16 v[172:175],a[148:151],v[84:87],v[172:175]
v_mul_f32 v179,s61,v179
v_cvt_pk_bf16_f32 v177,v178,v179
v_mfma_f32_16x16x32_bf16 v[172:175],a[144:147],v[80:83],v[172:175]
v_mul_f32 v180,s61,v180
v_mul_f32 v181,s61,v181
v_mul_f32 v182,s61,v182
v_mul_f32 v183,s61,v183
v_cvt_pk_bf16_f32 v178,v180,v181
v_cvt_pk_bf16_f32 v179,v182,v183
v_mfma_f32_16x16x32_bf16 v[180:183],a[160:163],v[80:83],v[36:39]
v_mul_f32 v184,s61,v184
v_mul_f32 v185,s61,v185
v_mfma_f32_16x16x32_bf16 v[180:183],a[164:167],v[84:87],v[180:183]
buffer_load_dwordx4 v5,s[20:23],s47 offen offset:3072 nt lds
v_mfma_f32_16x16x32_bf16 v[180:183],a[168:171],v[88:91],v[180:183]
v_cvt_pk_bf16_f32 v184,v184,v185
v_mul_f32 v186,s61,v186
v_mul_f32 v187,s61,v187
v_mfma_f32_16x16x32_bf16 v[180:183],a[172:175],v[92:95],v[180:183]
v_cvt_pk_bf16_f32 v185,v186,v187
v_mul_f32 v188,s61,v188
v_mul_f32 v189,s61,v189
v_mul_f32 v190,s61,v190
v_mul_f32 v191,s61,v191
v_cvt_pk_bf16_f32 v186,v188,v189
v_cvt_pk_bf16_f32 v187,v190,v191
v_mfma_f32_16x16x32_bf16 v[188:191],a[188:191],v[92:95],v[36:39]
v_max3_f32 v40,v40,v41,v42
v_max3_f32 v40,v40,v43,s100
v_mfma_f32_16x16x32_bf16 v[188:191],a[184:187],v[88:91],v[188:191]
v_sub_f32 v43,0,v40
v_sub_f32 v42,0,v40
v_sub_f32 v41,0,v40
v_sub_f32 v40,0,v40
v_mfma_f32_16x16x32_bf16 v[188:191],a[180:183],v[84:87],v[188:191]
v_mul_f32 v192,s61,v192
v_mul_f32 v193,s61,v193
v_cvt_pk_bf16_f32 v192,v192,v193
v_mul_f32 v194,s61,v194
v_mul_f32 v195,s61,v195
v_mfma_f32_16x16x32_bf16 v[188:191],a[176:179],v[80:83],v[188:191]
s_add_u32 m0,0,s52
v_cvt_pk_bf16_f32 v193,v194,v195
v_mul_f32 v196,s61,v196
v_mul_f32 v197,s61,v197
v_mul_f32 v198,s61,v198
v_mul_f32 v199,s61,v199
v_cvt_pk_bf16_f32 v194,v196,v197
v_cvt_pk_bf16_f32 v195,v198,v199
v_mfma_f32_16x16x32_bf16 v[196:199],a[128:131],v[96:99],v[40:43]
v_mul_f32 v200,s61,v200
v_mul_f32 v201,s61,v201
v_mfma_f32_16x16x32_bf16 v[196:199],a[132:135],v[100:103],v[196:199]
v_cvt_pk_bf16_f32 v200,v200,v201
v_mul_f32 v202,s61,v202
v_mfma_f32_16x16x32_bf16 v[196:199],a[136:139],v[104:107],v[196:199]
v_mul_f32 v203,s61,v203
v_cvt_pk_bf16_f32 v201,v202,v203
v_mfma_f32_16x16x32_bf16 v[196:199],a[140:143],v[108:111],v[196:199]
buffer_load_dwordx4 v4,s[24:27],s47 offen nt lds
v_mul_f32 v204,s61,v204
v_mul_f32 v205,s61,v205
v_mul_f32 v206,s61,v206
v_mul_f32 v207,s61,v207
v_cvt_pk_bf16_f32 v202,v204,v205
v_cvt_pk_bf16_f32 v203,v206,v207
v_mfma_f32_16x16x32_bf16 v[204:207],a[156:159],v[108:111],v[40:43]
v_mul_f32 v208,s61,v208
v_mul_f32 v209,s61,v209
v_mfma_f32_16x16x32_bf16 v[204:207],a[152:155],v[104:107],v[204:207]
v_cvt_pk_bf16_f32 v208,v208,v209
v_mul_f32 v210,s61,v210
v_mfma_f32_16x16x32_bf16 v[204:207],a[148:151],v[100:103],v[204:207]
v_mul_f32 v211,s61,v211
v_cvt_pk_bf16_f32 v209,v210,v211
v_mfma_f32_16x16x32_bf16 v[204:207],a[144:147],v[96:99],v[204:207]
ds_read_b64_tr_b16 a[192:193],v2
ds_read_b64_tr_b16 a[194:195],v2 offset:4096
v_mul_f32 v212,s61,v212
v_mul_f32 v213,s61,v213
v_mul_f32 v214,s61,v214
v_mul_f32 v215,s61,v215
v_cvt_pk_bf16_f32 v210,v212,v213
v_cvt_pk_bf16_f32 v211,v214,v215
v_mfma_f32_16x16x32_bf16 v[212:215],a[160:163],v[96:99],v[40:43]
ds_read_b64_tr_b16 a[196:197],v2 offset:8192
ds_read_b64_tr_b16 a[198:199],v2 offset:12288
v_mfma_f32_16x16x32_bf16 v[212:215],a[164:167],v[100:103],v[212:215]
ds_read_b64_tr_b16 a[204:205],v3 offset:8192
ds_read_b64_tr_b16 a[206:207],v3 offset:12288
v_mfma_f32_16x16x32_bf16 v[212:215],a[168:171],v[104:107],v[212:215]
ds_read_b64_tr_b16 a[200:201],v3
ds_read_b64_tr_b16 a[202:203],v3 offset:4096
v_mul_f32 v216,s61,v216
v_mul_f32 v217,s61,v217
v_cvt_pk_bf16_f32 v216,v216,v217
v_mul_f32 v218,s61,v218
v_mul_f32 v219,s61,v219
v_mfma_f32_16x16x32_bf16 v[212:215],a[172:175],v[108:111],v[212:215]
ds_read_b64_tr_b16 a[208:209],v2 offset:256
ds_read_b64_tr_b16 a[210:211],v2 offset:4352
v_cvt_pk_bf16_f32 v217,v218,v219
v_mul_f32 v220,s61,v220
v_mul_f32 v221,s61,v221
v_mul_f32 v222,s61,v222
v_mul_f32 v223,s61,v223
v_cvt_pk_bf16_f32 v218,v220,v221
v_cvt_pk_bf16_f32 v219,v222,v223
v_exp_f32 v132,v132
v_exp_f32 v133,v133
v_mfma_f32_16x16x32_bf16 v[220:223],a[188:191],v[108:111],v[40:43]
buffer_load_dwordx4 v5,s[24:27],s47 offen offset:1024 nt lds
ds_read_b64_tr_b16 a[212:213],v2 offset:8448
ds_read_b64_tr_b16 a[214:215],v2 offset:12544
v_exp_f32 v134,v134
v_exp_f32 v135,v135
v_cvt_pk_bf16_f32 v132,v132,v133
v_exp_f32 v140,v140
v_exp_f32 v141,v141
v_exp_f32 v142,v142
v_exp_f32 v143,v143
v_exp_f32 v148,v148
v_exp_f32 v149,v149
v_mfma_f32_16x16x32_bf16 v[220:223],a[184:187],v[104:107],v[220:223]
ds_read_b64_tr_b16 a[220:221],v3 offset:8448
ds_read_b64_tr_b16 a[222:223],v3 offset:12544
v_cvt_pk_bf16_f32 v133,v134,v135
v_exp_f32 v150,v150
v_exp_f32 v151,v151
v_cvt_pk_bf16_f32 v134,v140,v141
v_cvt_pk_bf16_f32 v135,v142,v143
v_cvt_pk_bf16_f32 v148,v148,v149
v_exp_f32 v156,v156
v_exp_f32 v157,v157
v_exp_f32 v158,v158
v_exp_f32 v159,v159
v_mfma_f32_16x16x32_bf16 v[220:223],a[180:183],v[100:103],v[220:223]
ds_read_b64_tr_b16 a[216:217],v3 offset:256
ds_read_b64_tr_b16 a[218:219],v3 offset:4352
v_cvt_pk_bf16_f32 v149,v150,v151
v_cvt_pk_bf16_f32 v150,v156,v157
v_cvt_pk_bf16_f32 v151,v158,v159
v_mfma_f32_16x16x32_bf16 v[220:223],a[176:179],v[96:99],v[220:223]
s_waitcnt lgkmcnt(14)
ds_read_b64_tr_b16 a[224:225],v2 offset:512
ds_read_b64_tr_b16 a[226:227],v2 offset:4608
v_mfma_f32_16x16x32_bf16 a[0:3],a[192:195],v[132:135],0
s_waitcnt lgkmcnt(14)
)A"
R"A(ds_read_b64_tr_b16 a[228:229],v2 offset:8704
ds_read_b64_tr_b16 a[230:231],v2 offset:12800
v_mfma_f32_16x16x32_bf16 a[0:3],a[196:199],v[148:151],a[0:3]
s_waitcnt lgkmcnt(14)
ds_read_b64_tr_b16 a[236:237],v3 offset:8704
ds_read_b64_tr_b16 a[238:239],v3 offset:12800
v_mfma_f32_16x16x32_bf16 a[4:7],a[204:207],v[148:151],0
s_waitcnt lgkmcnt(14)
ds_read_b64_tr_b16 a[232:233],v3 offset:512
ds_read_b64_tr_b16 a[234:235],v3 offset:4608
v_mfma_f32_16x16x32_bf16 a[4:7],a[200:203],v[132:135],a[4:7]
s_waitcnt lgkmcnt(14)
ds_read_b64_tr_b16 a[240:241],v2 offset:768
ds_read_b64_tr_b16 a[242:243],v2 offset:4864
v_mfma_f32_16x16x32_bf16 a[8:11],a[208:211],v[132:135],0
s_waitcnt lgkmcnt(14)
ds_read_b64_tr_b16 a[244:245],v2 offset:8960
ds_read_b64_tr_b16 a[246:247],v2 offset:13056
v_mfma_f32_16x16x32_bf16 a[8:11],a[212:215],v[148:151],a[8:11]
s_memrealtime s[76:77]
buffer_load_dwordx4 v4,s[24:27],s47 offen offset:2048 nt lds
ds_read_b64_tr_b16 a[252:253],v3 offset:8960
ds_read_b64_tr_b16 a[254:255],v3 offset:13056
s_waitcnt lgkmcnt(15)
v_mfma_f32_16x16x32_bf16 a[12:15],a[220:223],v[148:151],0
s_waitcnt lgkmcnt(14)
ds_read_b64_tr_b16 a[248:249],v3 offset:768
ds_read_b64_tr_b16 a[250:251],v3 offset:4864
v_mfma_f32_16x16x32_bf16 a[12:15],a[216:219],v[132:135],a[12:15]
s_waitcnt lgkmcnt(14)
v_mfma_f32_16x16x32_bf16 a[16:19],a[224:227],v[132:135],0
s_waitcnt lgkmcnt(12)
v_mfma_f32_16x16x32_bf16 a[16:19],a[228:231],v[148:151],a[16:19]
s_waitcnt lgkmcnt(10)
v_mfma_f32_16x16x32_bf16 a[20:23],a[236:239],v[148:151],0
s_waitcnt lgkmcnt(8)
v_mfma_f32_16x16x32_bf16 a[20:23],a[232:235],v[132:135],a[20:23]
s_waitcnt lgkmcnt(0)
v_mfma_f32_16x16x32_bf16 a[24:27],a[240:243],v[132:135],0
v_mfma_f32_16x16x32_bf16 a[24:27],a[244:247],v[148:151],a[24:27]
v_mfma_f32_16x16x32_bf16 a[28:31],a[252:255],v[148:151],0
buffer_load_dwordx4 v5,s[24:27],s47 offen offset:3072 nt lds
v_max3_f32 v44,v44,v45,v46
v_max3_f32 v44,v44,v47,s101
v_mfma_f32_16x16x32_bf16 a[28:31],a[248:251],v[132:135],a[28:31]
v_sub_f32 v47,0,v44
v_sub_f32 v46,0,v44
v_sub_f32 v45,0,v44
v_sub_f32 v44,0,v44
v_mfma_f32_16x16x32_bf16 v[48:51],v[28:31],v[132:135],0
v_mul_f32 v224,s61,v224
v_mul_f32 v225,s61,v225
v_cvt_pk_bf16_f32 v224,v224,v225
v_mul_f32 v226,s61,v226
v_mul_f32 v227,s61,v227
v_mfma_f32_16x16x32_bf16 v[48:51],v[28:31],v[148:151],v[48:51]
v_cvt_pk_bf16_f32 v225,v226,v227
v_mul_f32 v228,s61,v228
v_mul_f32 v229,s61,v229
v_mul_f32 v230,s61,v230
v_mul_f32 v231,s61,v231
v_cvt_pk_bf16_f32 v226,v228,v229
v_cvt_pk_bf16_f32 v227,v230,v231
v_mfma_f32_16x16x32_bf16 v[228:231],a[128:131],v[112:115],v[44:47]
s_waitcnt vmcnt(8)
s_add_u32 s47,s47,16384
v_mul_f32 v232,s61,v232
v_mul_f32 v233,s61,v233
v_mfma_f32_16x16x32_bf16 v[228:231],a[132:135],v[116:119],v[228:231]
s_barrier
v_cvt_pk_bf16_f32 v232,v232,v233
v_mul_f32 v234,s61,v234
v_mfma_f32_16x16x32_bf16 v[228:231],a[136:139],v[120:123],v[228:231]
s_add_u32 m0,s49,s51
v_mul_f32 v235,s61,v235
v_cvt_pk_bf16_f32 v233,v234,v235
v_mfma_f32_16x16x32_bf16 v[228:231],a[140:143],v[124:127],v[228:231]
v_mul_f32 v236,s61,v236
v_mul_f32 v237,s61,v237
v_mul_f32 v238,s61,v238
v_mul_f32 v239,s61,v239
v_cvt_pk_bf16_f32 v234,v236,v237
v_cvt_pk_bf16_f32 v235,v238,v239
v_mfma_f32_16x16x32_bf16 v[236:239],a[156:159],v[124:127],v[44:47]
s_and_b32 s47,s47,0x3ffff
v_mul_f32 v240,s61,v240
v_mul_f32 v241,s61,v241
v_mfma_f32_16x16x32_bf16 v[236:239],a[152:155],v[120:123],v[236:239]
s_or_b32 s47,s47,s46
v_cvt_pk_bf16_f32 v240,v240,v241
v_mul_f32 v242,s61,v242
v_mfma_f32_16x16x32_bf16 v[236:239],a[148:151],v[116:119],v[236:239]
s_add_u32 s78,s76,s60
v_mul_f32 v243,s61,v243
v_cvt_pk_bf16_f32 v241,v242,v243
v_mfma_f32_16x16x32_bf16 v[236:239],a[144:147],v[112:115],v[236:239]
buffer_load_dwordx4 v4,s[20:23],s47 offen nt lds
v_mul_f32 v244,s61,v244
v_mul_f32 v245,s61,v245
v_mul_f32 v246,s61,v246
v_mul_f32 v247,s61,v247
v_cvt_pk_bf16_f32 v242,v244,v245
v_cvt_pk_bf16_f32 v243,v246,v247
v_mfma_f32_16x16x32_bf16 v[244:247],a[160:163],v[112:115],v[44:47]
s_and_b32 s46,s54,0xfffc0000
v_mul_f32 v248,s61,v248
v_mul_f32 v249,s61,v249
v_mfma_f32_16x16x32_bf16 v[244:247],a[164:167],v[116:119],v[244:247]
v_cvt_pk_bf16_f32 v248,v248,v249
v_mul_f32 v250,s61,v250
v_mfma_f32_16x16x32_bf16 v[244:247],a[168:171],v[120:123],v[244:247]
v_mul_f32 v251,s61,v251
v_cvt_pk_bf16_f32 v249,v250,v251
v_mfma_f32_16x16x32_bf16 v[244:247],a[172:175],v[124:127],v[244:247]
v_mul_f32 v252,s61,v252
v_mul_f32 v253,s61,v253
v_mul_f32 v254,s61,v254
v_mul_f32 v255,s61,v255
v_cvt_pk_bf16_f32 v250,v252,v253
v_cvt_pk_bf16_f32 v251,v254,v255
v_exp_f32 v164,v164
v_exp_f32 v165,v165
v_mfma_f32_16x16x32_bf16 v[252:255],a[188:191],v[124:127],v[44:47]
v_exp_f32 v166,v166
v_exp_f32 v167,v167
v_cvt_pk_bf16_f32 v164,v164,v165
v_exp_f32 v172,v172
v_exp_f32 v173,v173
v_exp_f32 v174,v174
v_exp_f32 v175,v175
v_exp_f32 v180,v180
v_exp_f32 v181,v181
v_mfma_f32_16x16x32_bf16 v[252:255],a[184:187],v[120:123],v[252:255]
v_cvt_pk_bf16_f32 v165,v166,v167
v_exp_f32 v182,v182
v_exp_f32 v183,v183
v_cvt_pk_bf16_f32 v166,v172,v173
v_cvt_pk_bf16_f32 v167,v174,v175
v_cvt_pk_bf16_f32 v180,v180,v181
v_exp_f32 v188,v188
v_exp_f32 v189,v189
v_exp_f32 v190,v190
v_exp_f32 v191,v191
v_mfma_f32_16x16x32_bf16 v[252:255],a[180:183],v[116:119],v[252:255]
v_cvt_pk_bf16_f32 v181,v182,v183
v_cvt_pk_bf16_f32 v182,v188,v189
v_cvt_pk_bf16_f32 v183,v190,v191
v_mfma_f32_16x16x32_bf16 v[252:255],a[176:179],v[112:115],v[252:255]
v_add_u32 v1,s48,v1
v_add_u32 v2,s48,v2
v_mfma_f32_16x16x32_bf16 a[32:35],a[192:195],v[164:167],0
ds_read_b128 a[128:131],v1
v_add_u32 v3,s48,v3
v_mfma_f32_16x16x32_bf16 a[32:35],a[196:199],v[180:183],a[32:35]
s_sub_u32 s48,32768,s62
v_exp_f32 v196,v196
v_mfma_f32_16x16x32_bf16 a[36:39],a[204:207],v[180:183],0
s_add_u32 s50,s50,s48
buffer_load_dwordx4 v4,s[20:23],s47 offen offset:1024 nt lds
v_mfma_f32_16x16x32_bf16 a[36:39],a[200:203],v[164:167],a[36:39]
ds_read_b128 a[132:135],v1 offset:256
v_mfma_f32_16x16x32_bf16 a[40:43],a[208:211],v[164:167],0
v_exp_f32 v197,v197
v_mfma_f32_16x16x32_bf16 a[40:43],a[212:215],v[180:183],a[40:43]
ds_read_b128 a[136:139],v1 offset:512
v_cvt_pk_bf16_f32 v196,v196,v197
v_mfma_f32_16x16x32_bf16 a[44:47],a[220:223],v[180:183],0
v_exp_f32 v198,v198
v_mfma_f32_16x16x32_bf16 a[44:47],a[216:219],v[164:167],a[44:47]
ds_read_b128 a[140:143],v1 offset:768
v_mfma_f32_16x16x32_bf16 a[48:51],a[224:227],v[164:167],0
v_exp_f32 v199,v199
v_mfma_f32_16x16x32_bf16 a[48:51],a[228:231],v[180:183],a[48:51]
ds_read_b128 a[156:159],v1 offset:4864
v_cvt_pk_bf16_f32 v197,v198,v199
v_mfma_f32_16x16x32_bf16 a[52:55],a[236:239],v[180:183],0
v_exp_f32 v204,v204
v_mfma_f32_16x16x32_bf16 a[52:55],a[232:235],v[164:167],a[52:55]
ds_read_b128 a[152:155],v1 offset:4608
v_mfma_f32_16x16x32_bf16 a[56:59],a[240:243],v[164:167],0
v_exp_f32 v205,v205
v_mfma_f32_16x16x32_bf16 a[56:59],a[244:247],v[180:183],a[56:59]
ds_read_b128 a[148:151],v1 offset:4352
v_cvt_pk_bf16_f32 v198,v204,v205
v_mfma_f32_16x16x32_bf16 a[60:63],a[252:255],v[180:183],0
v_exp_f32 v206,v206
v_mfma_f32_16x16x32_bf16 a[60:63],a[248:251],v[164:167],a[60:63]
ds_read_b128 a[144:147],v1 offset:4096
v_mfma_f32_16x16x32_bf16 v[52:55],v[28:31],v[164:167],0
buffer_load_dwordx4 v5,s[20:23],s47 offen offset:2048 nt lds
v_mfma_f32_16x16x32_bf16 v[52:55],v[28:31],v[180:183],v[52:55]
s_waitcnt lgkmcnt(3)
ds_read_b128 a[160:163],v1 offset:8192
v_mfma_f32_16x16x32_bf16 v[132:135],a[128:131],v[64:67],v[32:35]
ds_read_b128 a[164:167],v1 offset:8448
v_mfma_f32_16x16x32_bf16 v[132:135],a[132:135],v[68:71],v[132:135]
ds_read_b128 a[168:171],v1 offset:8704
v_mfma_f32_16x16x32_bf16 v[132:135],a[136:139],v[72:75],v[132:135]
ds_read_b128 a[172:175],v1 offset:8960
v_mfma_f32_16x16x32_bf16 v[132:135],a[140:143],v[76:79],v[132:135]
ds_read_b128 a[188:191],v1 offset:13056
v_mfma_f32_16x16x32_bf16 v[140:143],a[156:159],v[76:79],v[32:35]
s_waitcnt lgkmcnt(6)
ds_read_b128 a[184:187],v1 offset:12800
v_mfma_f32_16x16x32_bf16 v[140:143],a[152:155],v[72:75],v[140:143]
ds_read_b128 a[180:183],v1 offset:12544
v_mfma_f32_16x16x32_bf16 v[140:143],a[148:151],v[68:71],v[140:143]
s_waitcnt lgkmcnt(7)
ds_read_b128 a[176:179],v1 offset:12288
v_mfma_f32_16x16x32_bf16 v[140:143],a[144:147],v[64:67],v[140:143]
s_waitcnt lgkmcnt(7)
v_mfma_f32_16x16x32_bf16 v[148:151],a[160:163],v[64:67],v[32:35]
s_waitcnt lgkmcnt(6)
v_mfma_f32_16x16x32_bf16 v[148:151],a[164:167],v[68:71],v[148:151]
s_waitcnt lgkmcnt(5)
v_mfma_f32_16x16x32_bf16 v[148:151],a[168:171],v[72:75],v[148:151]
s_waitcnt lgkmcnt(4)
v_mfma_f32_16x16x32_bf16 v[148:151],a[172:175],v[76:79],v[148:151]
s_waitcnt lgkmcnt(3)
v_exp_f32 v207,v207
v_mfma_f32_16x16x32_bf16 v[156:159],a[188:191],v[76:79],v[32:35]
s_waitcnt lgkmcnt(2)
v_exp_f32 v212,v212
v_exp_f32 v213,v213
v_mfma_f32_16x16x32_bf16 v[156:159],a[184:187],v[72:75],v[156:159]
s_waitcnt lgkmcnt(1)
v_exp_f32 v214,v214
v_exp_f32 v215,v215
v_cvt_pk_bf16_f32 v199,v206,v207
v_cvt_pk_bf16_f32 v212,v212,v213
v_exp_f32 v220,v220
v_exp_f32 v221,v221
v_exp_f32 v222,v222
v_exp_f32 v223,v223
v_mfma_f32_16x16x32_bf16 v[156:159],a[180:183],v[68:71],v[156:159]
s_waitcnt lgkmcnt(0)
v_cvt_pk_bf16_f32 v213,v214,v215
v_cvt_pk_bf16_f32 v214,v220,v221
v_cvt_pk_bf16_f32 v215,v222,v223
v_mfma_f32_16x16x32_bf16 v[156:159],a[176:179],v[64:67],v[156:159]
buffer_load_dwordx4 v5,s[20:23],s47 offen offset:3072 nt lds
v_mfma_f32_16x16x32_bf16 a[64:67],a[192:195],v[196:199],0
v_exp_f32 v228,v228
v_mfma_f32_16x16x32_bf16 a[64:67],a[196:199],v[212:215],a[64:67]
v_exp_f32 v229,v229
v_mfma_f32_16x16x32_bf16 a[68:71],a[204:207],v[212:215],0
v_exp_f32 v230,v230
v_mfma_f32_16x16x32_bf16 a[68:71],a[200:203],v[196:199],a[68:71]
v_exp_f32 v231,v231
v_mfma_f32_16x16x32_bf16 a[72:75],a[208:211],v[196:199],0
v_cvt_pk_bf16_f32 v228,v228,v229
v_cvt_pk_bf16_f32 v229,v230,v231
v_mfma_f32_16x16x32_bf16 a[72:75],a[212:215],v[212:215],a[72:75]
v_exp_f32 v236,v236
v_mfma_f32_16x16x32_bf16 a[76:79],a[220:223],v[212:215],0
v_exp_f32 v237,v237
v_mfma_f32_16x16x32_bf16 a[76:79],a[216:219],v[196:199],a[76:79]
s_add_u32 m0,s49,s52
v_exp_f32 v238,v238
v_mfma_f32_16x16x32_bf16 a[80:83],a[224:227],v[196:199],0
s_add_u32 s49,s49,32768
v_exp_f32 v239,v239
v_mfma_f32_16x16x32_bf16 a[80:83],a[228:231],v[212:215],a[80:83]
v_exp_f32 v244,v244
v_mfma_f32_16x16x32_bf16 a[84:87],a[236:239],v[212:215],0
buffer_load_dwordx4 v4,s[24:27],s47 offen nt lds
v_mfma_f32_16x16x32_bf16 a[84:87],a[232:235],v[196:199],a[84:87]
v_exp_f32 v245,v245
v_mfma_f32_16x16x32_bf16 a[88:91],a[240:243],v[196:199],0
v_exp_f32 v246,v246
v_mfma_f32_16x16x32_bf16 a[88:91],a[244:247],v[212:215],a[88:91]
v_exp_f32 v247,v247
v_mfma_f32_16x16x32_bf16 a[92:95],a[252:255],v[212:215],0
v_cvt_pk_bf16_f32 v230,v236,v237
v_cvt_pk_bf16_f32 v231,v238,v239
v_mfma_f32_16x16x32_bf16 a[92:95],a[248:251],v[196:199],a[92:95]
v_cvt_pk_bf16_f32 v244,v244,v245
v_cvt_pk_bf16_f32 v245,v246,v247
v_mfma_f32_16x16x32_bf16 v[56:59],v[28:31],v[196:199],0
v_exp_f32 v252,v252
v_mfma_f32_16x16x32_bf16 v[56:59],v[28:31],v[212:215],v[56:59]
v_exp_f32 v253,v253
v_mfma_f32_16x16x32_bf16 v[164:167],a[128:131],v[80:83],v[36:39]
v_exp_f32 v254,v254
v_mfma_f32_16x16x32_bf16 v[164:167],a[132:135],v[84:87],v[164:167]
v_exp_f32 v255,v255
v_mfma_f32_16x16x32_bf16 v[164:167],a[136:139],v[88:91],v[164:167]
v_cvt_pk_bf16_f32 v246,v252,v253
v_cvt_pk_bf16_f32 v247,v254,v255
v_mfma_f32_16x16x32_bf16 v[164:167],a[140:143],v[92:95],v[164:167]
v_exp_f32 v132,v132
v_mfma_f32_16x16x32_bf16 v[172:175],a[156:159],v[92:95],v[36:39]
v_exp_f32 v133,v133
v_mfma_f32_16x16x32_bf16 v[172:175],a[152:155],v[88:91],v[172:175]
v_exp_f32 v134,v134
v_mfma_f32_16x16x32_bf16 v[172:175],a[148:151],v[84:87],v[172:175]
buffer_load_dwordx4 v5,s[24:27],s47 offen offset:1024 nt lds
v_mfma_f32_16x16x32_bf16 v[172:175],a[144:147],v[80:83],v[172:175]
v_exp_f32 v135,v135
v_mfma_f32_16x16x32_bf16 v[180:183],a[160:163],v[80:83],v[36:39]
v_cvt_pk_bf16_f32 v132,v132,v133
v_cvt_pk_bf16_f32 v133,v134,v135
v_mfma_f32_16x16x32_bf16 v[180:183],a[164:167],v[84:87],v[180:183]
v_exp_f32 v140,v140
v_mfma_f32_16x16x32_bf16 v[180:183],a[168:171],v[88:91],v[180:183]
)A"
R"A(v_exp_f32 v141,v141
v_mfma_f32_16x16x32_bf16 v[180:183],a[172:175],v[92:95],v[180:183]
v_exp_f32 v142,v142
v_mfma_f32_16x16x32_bf16 v[188:191],a[188:191],v[92:95],v[36:39]
v_exp_f32 v143,v143
v_mfma_f32_16x16x32_bf16 v[188:191],a[184:187],v[88:91],v[188:191]
v_exp_f32 v148,v148
v_mfma_f32_16x16x32_bf16 v[188:191],a[180:183],v[84:87],v[188:191]
v_exp_f32 v149,v149
v_mfma_f32_16x16x32_bf16 v[188:191],a[176:179],v[80:83],v[188:191]
v_exp_f32 v150,v150
v_mfma_f32_16x16x32_bf16 a[96:99],a[192:195],v[228:231],0
ds_read_b64_tr_b16 a[192:193],v2
ds_read_b64_tr_b16 a[194:195],v2 offset:4096
v_mfma_f32_16x16x32_bf16 a[96:99],a[196:199],v[244:247],a[96:99]
ds_read_b64_tr_b16 a[196:197],v2 offset:8192
v_cvt_pk_bf16_f32 v134,v140,v141
v_mfma_f32_16x16x32_bf16 a[100:103],a[204:207],v[244:247],0
ds_read_b64_tr_b16 a[198:199],v2 offset:12288
v_cvt_pk_bf16_f32 v135,v142,v143
v_mfma_f32_16x16x32_bf16 a[100:103],a[200:203],v[228:231],a[100:103]
ds_read_b64_tr_b16 a[204:205],v3 offset:8192
v_cvt_pk_bf16_f32 v148,v148,v149
v_mfma_f32_16x16x32_bf16 a[104:107],a[208:211],v[228:231],0
buffer_load_dwordx4 v4,s[24:27],s47 offen offset:2048 nt lds
v_mfma_f32_16x16x32_bf16 a[104:107],a[212:215],v[244:247],a[104:107]
ds_read_b64_tr_b16 a[206:207],v3 offset:12288
ds_read_b64_tr_b16 a[200:201],v3
v_mfma_f32_16x16x32_bf16 a[108:111],a[220:223],v[244:247],0
ds_read_b64_tr_b16 a[202:203],v3 offset:4096
v_add_u32 v1,s48,v1
v_mfma_f32_16x16x32_bf16 a[108:111],a[216:219],v[228:231],a[108:111]
s_cmp_eq_u32 s49,163840
s_cselect_b32 s49,0,s49
ds_read_b64_tr_b16 a[208:209],v2 offset:256
v_mfma_f32_16x16x32_bf16 a[112:115],a[224:227],v[228:231],0
ds_read_b64_tr_b16 a[210:211],v2 offset:4352
v_mfma_f32_16x16x32_bf16 a[112:115],a[228:231],v[244:247],a[112:115]
ds_read_b64_tr_b16 a[212:213],v2 offset:8448
ds_read_b64_tr_b16 a[214:215],v2 offset:12544
v_mfma_f32_16x16x32_bf16 a[116:119],a[236:239],v[244:247],0
v_exp_f32 v151,v151
v_mfma_f32_16x16x32_bf16 a[116:119],a[232:235],v[228:231],a[116:119]
v_exp_f32 v156,v156
v_mfma_f32_16x16x32_bf16 a[120:123],a[240:243],v[228:231],0
v_exp_f32 v157,v157
v_mfma_f32_16x16x32_bf16 a[120:123],a[244:247],v[244:247],a[120:123]
v_exp_f32 v158,v158
v_mfma_f32_16x16x32_bf16 a[124:127],a[252:255],v[244:247],0
v_exp_f32 v159,v159
v_mfma_f32_16x16x32_bf16 a[124:127],a[248:251],v[228:231],a[124:127]
ds_read_b64_tr_b16 a[220:221],v3 offset:8448
ds_read_b64_tr_b16 a[222:223],v3 offset:12544
v_mfma_f32_16x16x32_bf16 v[60:63],v[28:31],v[228:231],0
v_cvt_pk_bf16_f32 v149,v150,v151
v_cvt_pk_bf16_f32 v150,v156,v157
v_mfma_f32_16x16x32_bf16 v[60:63],v[28:31],v[244:247],v[60:63]
ds_read_b64_tr_b16 a[216:217],v3 offset:256
v_cvt_pk_bf16_f32 v151,v158,v159
v_mfma_f32_16x16x32_bf16 v[196:199],a[128:131],v[96:99],v[40:43]
buffer_load_dwordx4 v5,s[24:27],s47 offen offset:3072 nt lds
v_mfma_f32_16x16x32_bf16 v[196:199],a[132:135],v[100:103],v[196:199]
s_add_u32 s47,s47,16384
ds_read_b64_tr_b16 a[218:219],v3 offset:4352
ds_read_b64_tr_b16 a[224:225],v2 offset:512
v_mfma_f32_16x16x32_bf16 v[196:199],a[136:139],v[104:107],v[196:199]
s_and_b32 s47,s47,0x3ffff
s_or_b32 s47,s47,s46
ds_read_b64_tr_b16 a[226:227],v2 offset:4608
v_mfma_f32_16x16x32_bf16 v[196:199],a[140:143],v[108:111],v[196:199]
ds_read_b64_tr_b16 a[228:229],v2 offset:8704
v_mfma_f32_16x16x32_bf16 v[204:207],a[156:159],v[108:111],v[40:43]
ds_read_b64_tr_b16 a[230:231],v2 offset:12800
v_mfma_f32_16x16x32_bf16 v[204:207],a[152:155],v[104:107],v[204:207]
ds_read_b64_tr_b16 a[236:237],v3 offset:8704
ds_read_b64_tr_b16 a[238:239],v3 offset:12800
v_mfma_f32_16x16x32_bf16 v[204:207],a[148:151],v[100:103],v[204:207]
ds_read_b64_tr_b16 a[232:233],v3 offset:512
v_mfma_f32_16x16x32_bf16 v[204:207],a[144:147],v[96:99],v[204:207]
ds_read_b64_tr_b16 a[234:235],v3 offset:4608
v_mfma_f32_16x16x32_bf16 v[212:215],a[160:163],v[96:99],v[40:43]
ds_read_b64_tr_b16 a[240:241],v2 offset:768
v_mfma_f32_16x16x32_bf16 v[212:215],a[164:167],v[100:103],v[212:215]
ds_read_b64_tr_b16 a[242:243],v2 offset:4864
ds_read_b64_tr_b16 a[244:245],v2 offset:8960
v_mfma_f32_16x16x32_bf16 v[212:215],a[168:171],v[104:107],v[212:215]
ds_read_b64_tr_b16 a[246:247],v2 offset:13056
v_mfma_f32_16x16x32_bf16 v[212:215],a[172:175],v[108:111],v[212:215]
ds_read_b64_tr_b16 a[252:253],v3 offset:8960
v_mfma_f32_16x16x32_bf16 v[220:223],a[188:191],v[108:111],v[40:43]
ds_read_b64_tr_b16 a[254:255],v3 offset:13056
v_mfma_f32_16x16x32_bf16 v[220:223],a[184:187],v[104:107],v[220:223]
ds_read_b64_tr_b16 a[248:249],v3 offset:768
ds_read_b64_tr_b16 a[250:251],v3 offset:4864
v_mfma_f32_16x16x32_bf16 v[220:223],a[180:183],v[100:103],v[220:223]
v_exp_f32 v164,v164
v_mfma_f32_16x16x32_bf16 v[220:223],a[176:179],v[96:99],v[220:223]
s_waitcnt lgkmcnt(8)
v_exp_f32 v165,v165
v_mfma_f32_16x16x32_bf16 a[0:3],a[192:195],v[132:135],a[0:3]
v_exp_f32 v180,v180
v_mfma_f32_16x16x32_bf16 a[0:3],a[196:199],v[148:151],a[0:3]
v_exp_f32 v181,v181
v_mfma_f32_16x16x32_bf16 a[4:7],a[204:207],v[148:151],a[4:7]
v_exp_f32 v166,v166
v_mfma_f32_16x16x32_bf16 a[4:7],a[200:203],v[132:135],a[4:7]
v_exp_f32 v167,v167
v_mfma_f32_16x16x32_bf16 a[8:11],a[208:211],v[132:135],a[8:11]
s_memrealtime s[76:77]
v_exp_f32 v182,v182
v_mfma_f32_16x16x32_bf16 a[8:11],a[212:215],v[148:151],a[8:11]
v_exp_f32 v183,v183
v_mfma_f32_16x16x32_bf16 a[12:15],a[220:223],v[148:151],a[12:15]
v_cvt_pk_bf16_f32 v164,v164,v165
v_cvt_pk_bf16_f32 v180,v180,v181
v_mfma_f32_16x16x32_bf16 a[12:15],a[216:219],v[132:135],a[12:15]
v_exp_f32 v172,v172
v_mfma_f32_16x16x32_bf16 a[16:19],a[224:227],v[132:135],a[16:19]
v_exp_f32 v173,v173
v_mfma_f32_16x16x32_bf16 a[16:19],a[228:231],v[148:151],a[16:19]
v_exp_f32 v174,v174
v_mfma_f32_16x16x32_bf16 a[20:23],a[236:239],v[148:151],a[20:23]
v_exp_f32 v175,v175
v_mfma_f32_16x16x32_bf16 a[20:23],a[232:235],v[132:135],a[20:23]
s_waitcnt lgkmcnt(0)
v_exp_f32 v188,v188
v_mfma_f32_16x16x32_bf16 a[24:27],a[240:243],v[132:135],a[24:27]
s_sub_u32 s79,s78,s76
v_exp_f32 v189,v189
v_mfma_f32_16x16x32_bf16 a[24:27],a[244:247],v[148:151],a[24:27]
s_add_u32 s78,s76,s60
v_exp_f32 v190,v190
v_mfma_f32_16x16x32_bf16 a[28:31],a[252:255],v[148:151],a[28:31]
s_cmp_gt_i32 s79,0
s_cbranch_scc1 L_ps1_%=
L_pr1_%=:
v_cvt_pk_bf16_f32 v165,v166,v167
v_mfma_f32_16x16x32_bf16 a[28:31],a[248:251],v[132:135],a[28:31]
v_exp_f32 v191,v191
v_mfma_f32_16x16x32_bf16 v[48:51],v[28:31],v[132:135],v[48:51]
v_cvt_pk_bf16_f32 v181,v182,v183
v_cvt_pk_bf16_f32 v166,v172,v173
v_cvt_pk_bf16_f32 v167,v174,v175
v_cvt_pk_bf16_f32 v182,v188,v189
v_cvt_pk_bf16_f32 v183,v190,v191
v_mfma_f32_16x16x32_bf16 v[48:51],v[28:31],v[148:151],v[48:51]
s_mov_b32 s53,13
L_att0_%=:
v_mfma_f32_16x16x32_bf16 v[228:231],a[128:131],v[112:115],v[44:47]
s_waitcnt vmcnt(8) lgkmcnt(0)
v_exp_f32 v196,v196
v_mfma_f32_16x16x32_bf16 v[228:231],a[132:135],v[116:119],v[228:231]
s_barrier
s_add_u32 m0,s49,s51
ds_read_b128 a[128:131],v1
v_mfma_f32_16x16x32_bf16 v[228:231],a[136:139],v[120:123],v[228:231]
s_mov_b32 s90,L_endseg_%=-L_pc_%=+128
v_exp_f32 v197,v197
v_mfma_f32_16x16x32_bf16 v[228:231],a[140:143],v[124:127],v[228:231]
s_add_u32 s54,s54,16384
ds_read_b128 a[132:135],v1 offset:256
v_cvt_pk_bf16_f32 v196,v196,v197
v_mfma_f32_16x16x32_bf16 v[236:239],a[156:159],v[124:127],v[44:47]
s_sub_u32 s53,s53,1
buffer_load_dwordx4 v4,s[20:23],s47 offen nt lds
v_mfma_f32_16x16x32_bf16 v[236:239],a[152:155],v[120:123],v[236:239]
s_and_b32 s46,s54,0xfffc0000
ds_read_b128 a[136:139],v1 offset:512
v_add_u32 v2,s48,v2
v_mfma_f32_16x16x32_bf16 v[236:239],a[148:151],v[116:119],v[236:239]
v_exp_f32 v198,v198
v_mfma_f32_16x16x32_bf16 v[236:239],a[144:147],v[112:115],v[236:239]
ds_read_b128 a[140:143],v1 offset:768
v_add_u32 v3,s48,v3
v_mfma_f32_16x16x32_bf16 v[244:247],a[160:163],v[112:115],v[44:47]
v_exp_f32 v199,v199
v_mfma_f32_16x16x32_bf16 v[244:247],a[164:167],v[116:119],v[244:247]
ds_read_b128 a[156:159],v1 offset:4864
v_cvt_pk_bf16_f32 v197,v198,v199
v_mfma_f32_16x16x32_bf16 v[244:247],a[168:171],v[120:123],v[244:247]
v_exp_f32 v204,v204
v_mfma_f32_16x16x32_bf16 v[244:247],a[172:175],v[124:127],v[244:247]
s_cmp_eq_u32 s50,131072
s_cselect_b32 s62,163840,0
ds_read_b128 a[152:155],v1 offset:4608
v_mfma_f32_16x16x32_bf16 v[252:255],a[188:191],v[124:127],v[44:47]
s_sub_u32 s48,32768,s62
v_exp_f32 v205,v205
v_mfma_f32_16x16x32_bf16 v[252:255],a[184:187],v[120:123],v[252:255]
s_add_u32 s50,s50,s48
ds_read_b128 a[148:151],v1 offset:4352
v_cvt_pk_bf16_f32 v198,v204,v205
v_mfma_f32_16x16x32_bf16 v[252:255],a[180:183],v[116:119],v[252:255]
v_exp_f32 v206,v206
v_mfma_f32_16x16x32_bf16 v[252:255],a[176:179],v[112:115],v[252:255]
ds_read_b128 a[144:147],v1 offset:4096
v_mfma_f32_16x16x32_bf16 a[32:35],a[192:195],v[164:167],a[32:35]
v_exp_f32 v207,v207
v_mfma_f32_16x16x32_bf16 a[32:35],a[196:199],v[180:183],a[32:35]
ds_read_b128 a[160:163],v1 offset:8192
v_cvt_pk_bf16_f32 v199,v206,v207
v_mfma_f32_16x16x32_bf16 a[36:39],a[204:207],v[180:183],a[36:39]
buffer_load_dwordx4 v4,s[20:23],s47 offen offset:1024 nt lds
v_mfma_f32_16x16x32_bf16 a[36:39],a[200:203],v[164:167],a[36:39]
ds_read_b128 a[164:167],v1 offset:8448
v_mfma_f32_16x16x32_bf16 a[40:43],a[208:211],v[164:167],a[40:43]
v_exp_f32 v212,v212
v_mfma_f32_16x16x32_bf16 a[40:43],a[212:215],v[180:183],a[40:43]
ds_read_b128 a[168:171],v1 offset:8704
v_mfma_f32_16x16x32_bf16 a[44:47],a[220:223],v[180:183],a[44:47]
v_exp_f32 v213,v213
v_mfma_f32_16x16x32_bf16 a[44:47],a[216:219],v[164:167],a[44:47]
ds_read_b128 a[172:175],v1 offset:8960
v_cvt_pk_bf16_f32 v212,v212,v213
v_mfma_f32_16x16x32_bf16 a[48:51],a[224:227],v[164:167],a[48:51]
v_exp_f32 v214,v214
v_mfma_f32_16x16x32_bf16 a[48:51],a[228:231],v[180:183],a[48:51]
ds_read_b128 a[188:191],v1 offset:13056
v_mfma_f32_16x16x32_bf16 a[52:55],a[236:239],v[180:183],a[52:55]
v_exp_f32 v215,v215
v_mfma_f32_16x16x32_bf16 a[52:55],a[232:235],v[164:167],a[52:55]
ds_read_b128 a[184:187],v1 offset:12800
v_cvt_pk_bf16_f32 v213,v214,v215
v_mfma_f32_16x16x32_bf16 a[56:59],a[240:243],v[164:167],a[56:59]
v_exp_f32 v220,v220
v_mfma_f32_16x16x32_bf16 a[56:59],a[244:247],v[180:183],a[56:59]
ds_read_b128 a[180:183],v1 offset:12544
v_mfma_f32_16x16x32_bf16 a[60:63],a[252:255],v[180:183],a[60:63]
v_exp_f32 v221,v221
v_mfma_f32_16x16x32_bf16 a[60:63],a[248:251],v[164:167],a[60:63]
ds_read_b128 a[176:179],v1 offset:12288
v_cvt_pk_bf16_f32 v214,v220,v221
v_mfma_f32_16x16x32_bf16 v[52:55],v[28:31],v[164:167],v[52:55]
buffer_load_dwordx4 v5,s[20:23],s47 offen offset:2048 nt lds
v_mfma_f32_16x16x32_bf16 v[52:55],v[28:31],v[180:183],v[52:55]
s_waitcnt lgkmcnt(3)
v_exp_f32 v222,v222
v_mfma_f32_16x16x32_bf16 v[132:135],a[128:131],v[64:67],v[32:35]
v_exp_f32 v223,v223
v_mfma_f32_16x16x32_bf16 v[132:135],a[132:135],v[68:71],v[132:135]
v_cvt_pk_bf16_f32 v215,v222,v223
v_add_u32 v1,s48,v1
v_mfma_f32_16x16x32_bf16 v[132:135],a[136:139],v[72:75],v[132:135]
v_exp_f32 v228,v228
v_mfma_f32_16x16x32_bf16 v[132:135],a[140:143],v[76:79],v[132:135]
v_exp_f32 v229,v229
v_mfma_f32_16x16x32_bf16 v[140:143],a[156:159],v[76:79],v[32:35]
v_exp_f32 v230,v230
v_mfma_f32_16x16x32_bf16 v[140:143],a[152:155],v[72:75],v[140:143]
v_exp_f32 v231,v231
v_mfma_f32_16x16x32_bf16 v[140:143],a[148:151],v[68:71],v[140:143]
v_cvt_pk_bf16_f32 v228,v228,v229
v_cvt_pk_bf16_f32 v229,v230,v231
v_mfma_f32_16x16x32_bf16 v[140:143],a[144:147],v[64:67],v[140:143]
v_exp_f32 v236,v236
v_mfma_f32_16x16x32_bf16 v[148:151],a[160:163],v[64:67],v[32:35]
v_exp_f32 v237,v237
v_mfma_f32_16x16x32_bf16 v[148:151],a[164:167],v[68:71],v[148:151]
v_exp_f32 v238,v238
v_mfma_f32_16x16x32_bf16 v[148:151],a[168:171],v[72:75],v[148:151]
v_exp_f32 v239,v239
v_mfma_f32_16x16x32_bf16 v[148:151],a[172:175],v[76:79],v[148:151]
v_exp_f32 v244,v244
v_mfma_f32_16x16x32_bf16 v[156:159],a[188:191],v[76:79],v[32:35]
s_waitcnt lgkmcnt(0)
buffer_load_dwordx4 v5,s[20:23],s47 offen offset:3072 nt lds
v_mfma_f32_16x16x32_bf16 v[156:159],a[184:187],v[72:75],v[156:159]
v_exp_f32 v245,v245
v_mfma_f32_16x16x32_bf16 v[156:159],a[180:183],v[68:71],v[156:159]
v_exp_f32 v246,v246
v_mfma_f32_16x16x32_bf16 v[156:159],a[176:179],v[64:67],v[156:159]
v_exp_f32 v247,v247
)A"
R"A(v_mfma_f32_16x16x32_bf16 a[64:67],a[192:195],v[196:199],a[64:67]
v_cvt_pk_bf16_f32 v230,v236,v237
v_cvt_pk_bf16_f32 v231,v238,v239
v_mfma_f32_16x16x32_bf16 a[64:67],a[196:199],v[212:215],a[64:67]
v_cvt_pk_bf16_f32 v244,v244,v245
v_cvt_pk_bf16_f32 v245,v246,v247
v_mfma_f32_16x16x32_bf16 a[68:71],a[204:207],v[212:215],a[68:71]
v_exp_f32 v252,v252
v_mfma_f32_16x16x32_bf16 a[68:71],a[200:203],v[196:199],a[68:71]
v_exp_f32 v253,v253
v_mfma_f32_16x16x32_bf16 a[72:75],a[208:211],v[196:199],a[72:75]
v_exp_f32 v254,v254
v_mfma_f32_16x16x32_bf16 a[72:75],a[212:215],v[212:215],a[72:75]
v_exp_f32 v255,v255
v_mfma_f32_16x16x32_bf16 a[76:79],a[220:223],v[212:215],a[76:79]
v_cvt_pk_bf16_f32 v246,v252,v253
v_cvt_pk_bf16_f32 v247,v254,v255
v_mfma_f32_16x16x32_bf16 a[76:79],a[216:219],v[196:199],a[76:79]
s_add_u32 m0,s49,s52
v_exp_f32 v132,v132
v_mfma_f32_16x16x32_bf16 a[80:83],a[224:227],v[196:199],a[80:83]
s_add_u32 s49,s49,32768
v_exp_f32 v133,v133
v_mfma_f32_16x16x32_bf16 a[80:83],a[228:231],v[212:215],a[80:83]
v_exp_f32 v134,v134
v_mfma_f32_16x16x32_bf16 a[84:87],a[236:239],v[212:215],a[84:87]
buffer_load_dwordx4 v4,s[24:27],s47 offen nt lds
v_mfma_f32_16x16x32_bf16 a[84:87],a[232:235],v[196:199],a[84:87]
v_exp_f32 v135,v135
v_mfma_f32_16x16x32_bf16 a[88:91],a[240:243],v[196:199],a[88:91]
v_cvt_pk_bf16_f32 v132,v132,v133
v_cvt_pk_bf16_f32 v133,v134,v135
v_mfma_f32_16x16x32_bf16 a[88:91],a[244:247],v[212:215],a[88:91]
v_exp_f32 v140,v140
v_mfma_f32_16x16x32_bf16 a[92:95],a[252:255],v[212:215],a[92:95]
v_exp_f32 v141,v141
v_mfma_f32_16x16x32_bf16 a[92:95],a[248:251],v[196:199],a[92:95]
v_exp_f32 v142,v142
v_mfma_f32_16x16x32_bf16 v[56:59],v[28:31],v[196:199],v[56:59]
v_exp_f32 v143,v143
v_mfma_f32_16x16x32_bf16 v[56:59],v[28:31],v[212:215],v[56:59]
v_exp_f32 v148,v148
v_mfma_f32_16x16x32_bf16 v[164:167],a[128:131],v[80:83],v[36:39]
v_exp_f32 v149,v149
v_mfma_f32_16x16x32_bf16 v[164:167],a[132:135],v[84:87],v[164:167]
v_exp_f32 v150,v150
v_mfma_f32_16x16x32_bf16 v[164:167],a[136:139],v[88:91],v[164:167]
v_exp_f32 v151,v151
v_mfma_f32_16x16x32_bf16 v[164:167],a[140:143],v[92:95],v[164:167]
v_cvt_pk_bf16_f32 v134,v140,v141
v_cvt_pk_bf16_f32 v135,v142,v143
v_mfma_f32_16x16x32_bf16 v[172:175],a[156:159],v[92:95],v[36:39]
v_cvt_pk_bf16_f32 v148,v148,v149
v_cvt_pk_bf16_f32 v149,v150,v151
v_mfma_f32_16x16x32_bf16 v[172:175],a[152:155],v[88:91],v[172:175]
v_exp_f32 v156,v156
v_mfma_f32_16x16x32_bf16 v[172:175],a[148:151],v[84:87],v[172:175]
buffer_load_dwordx4 v5,s[24:27],s47 offen offset:1024 nt lds
v_mfma_f32_16x16x32_bf16 v[172:175],a[144:147],v[80:83],v[172:175]
v_exp_f32 v157,v157
v_mfma_f32_16x16x32_bf16 v[180:183],a[160:163],v[80:83],v[36:39]
v_exp_f32 v158,v158
v_mfma_f32_16x16x32_bf16 v[180:183],a[164:167],v[84:87],v[180:183]
v_exp_f32 v159,v159
v_mfma_f32_16x16x32_bf16 v[180:183],a[168:171],v[88:91],v[180:183]
v_cvt_pk_bf16_f32 v150,v156,v157
v_cvt_pk_bf16_f32 v151,v158,v159
v_mfma_f32_16x16x32_bf16 v[180:183],a[172:175],v[92:95],v[180:183]
v_exp_f32 v164,v164
v_mfma_f32_16x16x32_bf16 v[188:191],a[188:191],v[92:95],v[36:39]
v_exp_f32 v165,v165
v_mfma_f32_16x16x32_bf16 v[188:191],a[184:187],v[88:91],v[188:191]
v_exp_f32 v166,v166
v_mfma_f32_16x16x32_bf16 v[188:191],a[180:183],v[84:87],v[188:191]
v_exp_f32 v167,v167
v_mfma_f32_16x16x32_bf16 v[188:191],a[176:179],v[80:83],v[188:191]
v_cvt_pk_bf16_f32 v164,v164,v165
v_cvt_pk_bf16_f32 v165,v166,v167
v_mfma_f32_16x16x32_bf16 a[96:99],a[192:195],v[228:231],a[96:99]
s_cmp_eq_u32 s49,163840
s_cselect_b32 s49,0,s49
ds_read_b64_tr_b16 a[192:193],v2
v_mfma_f32_16x16x32_bf16 a[96:99],a[196:199],v[244:247],a[96:99]
ds_read_b64_tr_b16 a[194:195],v2 offset:4096
v_mfma_f32_16x16x32_bf16 a[100:103],a[204:207],v[244:247],a[100:103]
ds_read_b64_tr_b16 a[196:197],v2 offset:8192
v_mfma_f32_16x16x32_bf16 a[100:103],a[200:203],v[228:231],a[100:103]
ds_read_b64_tr_b16 a[198:199],v2 offset:12288
v_mfma_f32_16x16x32_bf16 a[104:107],a[208:211],v[228:231],a[104:107]
buffer_load_dwordx4 v4,s[24:27],s47 offen offset:2048 nt lds
v_mfma_f32_16x16x32_bf16 a[104:107],a[212:215],v[244:247],a[104:107]
ds_read_b64_tr_b16 a[204:205],v3 offset:8192
v_mfma_f32_16x16x32_bf16 a[108:111],a[220:223],v[244:247],a[108:111]
ds_read_b64_tr_b16 a[206:207],v3 offset:12288
v_mfma_f32_16x16x32_bf16 a[108:111],a[216:219],v[228:231],a[108:111]
ds_read_b64_tr_b16 a[200:201],v3
v_mfma_f32_16x16x32_bf16 a[112:115],a[224:227],v[228:231],a[112:115]
ds_read_b64_tr_b16 a[202:203],v3 offset:4096
v_mfma_f32_16x16x32_bf16 a[112:115],a[228:231],v[244:247],a[112:115]
ds_read_b64_tr_b16 a[208:209],v2 offset:256
v_mfma_f32_16x16x32_bf16 a[116:119],a[236:239],v[244:247],a[116:119]
ds_read_b64_tr_b16 a[210:211],v2 offset:4352
v_mfma_f32_16x16x32_bf16 a[116:119],a[232:235],v[228:231],a[116:119]
ds_read_b64_tr_b16 a[212:213],v2 offset:8448
v_mfma_f32_16x16x32_bf16 a[120:123],a[240:243],v[228:231],a[120:123]
ds_read_b64_tr_b16 a[214:215],v2 offset:12544
v_mfma_f32_16x16x32_bf16 a[120:123],a[244:247],v[244:247],a[120:123]
ds_read_b64_tr_b16 a[220:221],v3 offset:8448
v_mfma_f32_16x16x32_bf16 a[124:127],a[252:255],v[244:247],a[124:127]
ds_read_b64_tr_b16 a[222:223],v3 offset:12544
v_mfma_f32_16x16x32_bf16 a[124:127],a[248:251],v[228:231],a[124:127]
ds_read_b64_tr_b16 a[216:217],v3 offset:256
v_mfma_f32_16x16x32_bf16 v[60:63],v[28:31],v[228:231],v[60:63]
ds_read_b64_tr_b16 a[218:219],v3 offset:4352
v_mfma_f32_16x16x32_bf16 v[60:63],v[28:31],v[244:247],v[60:63]
ds_read_b64_tr_b16 a[224:225],v2 offset:512
v_mfma_f32_16x16x32_bf16 v[196:199],a[128:131],v[96:99],v[40:43]
buffer_load_dwordx4 v5,s[24:27],s47 offen offset:3072 nt lds
v_mfma_f32_16x16x32_bf16 v[196:199],a[132:135],v[100:103],v[196:199]
s_add_u32 s47,s47,16384
s_and_b32 s47,s47,0x3ffff
ds_read_b64_tr_b16 a[226:227],v2 offset:4608
v_mfma_f32_16x16x32_bf16 v[196:199],a[136:139],v[104:107],v[196:199]
s_or_b32 s47,s47,s46
ds_read_b64_tr_b16 a[228:229],v2 offset:8704
v_mfma_f32_16x16x32_bf16 v[196:199],a[140:143],v[108:111],v[196:199]
ds_read_b64_tr_b16 a[230:231],v2 offset:12800
v_mfma_f32_16x16x32_bf16 v[204:207],a[156:159],v[108:111],v[40:43]
ds_read_b64_tr_b16 a[236:237],v3 offset:8704
v_mfma_f32_16x16x32_bf16 v[204:207],a[152:155],v[104:107],v[204:207]
ds_read_b64_tr_b16 a[238:239],v3 offset:12800
v_mfma_f32_16x16x32_bf16 v[204:207],a[148:151],v[100:103],v[204:207]
ds_read_b64_tr_b16 a[232:233],v3 offset:512
v_mfma_f32_16x16x32_bf16 v[204:207],a[144:147],v[96:99],v[204:207]
ds_read_b64_tr_b16 a[234:235],v3 offset:4608
v_mfma_f32_16x16x32_bf16 v[212:215],a[160:163],v[96:99],v[40:43]
ds_read_b64_tr_b16 a[240:241],v2 offset:768
v_mfma_f32_16x16x32_bf16 v[212:215],a[164:167],v[100:103],v[212:215]
ds_read_b64_tr_b16 a[242:243],v2 offset:4864
v_mfma_f32_16x16x32_bf16 v[212:215],a[168:171],v[104:107],v[212:215]
ds_read_b64_tr_b16 a[244:245],v2 offset:8960
v_mfma_f32_16x16x32_bf16 v[212:215],a[172:175],v[108:111],v[212:215]
ds_read_b64_tr_b16 a[246:247],v2 offset:13056
v_mfma_f32_16x16x32_bf16 v[220:223],a[188:191],v[108:111],v[40:43]
ds_read_b64_tr_b16 a[252:253],v3 offset:8960
v_mfma_f32_16x16x32_bf16 v[220:223],a[184:187],v[104:107],v[220:223]
ds_read_b64_tr_b16 a[254:255],v3 offset:13056
v_mfma_f32_16x16x32_bf16 v[220:223],a[180:183],v[100:103],v[220:223]
ds_read_b64_tr_b16 a[248:249],v3 offset:768
v_mfma_f32_16x16x32_bf16 v[220:223],a[176:179],v[96:99],v[220:223]
s_waitcnt lgkmcnt(7)
ds_read_b64_tr_b16 a[250:251],v3 offset:4864
v_mfma_f32_16x16x32_bf16 a[0:3],a[192:195],v[132:135],a[0:3]
buffer_load_dword v26,v24,s[88:91],s93 offen
v_mfma_f32_16x16x32_bf16 a[0:3],a[196:199],v[148:151],a[0:3]
v_exp_f32 v180,v180
v_mfma_f32_16x16x32_bf16 a[4:7],a[204:207],v[148:151],a[4:7]
v_exp_f32 v181,v181
v_mfma_f32_16x16x32_bf16 a[4:7],a[200:203],v[132:135],a[4:7]
v_exp_f32 v182,v182
v_mfma_f32_16x16x32_bf16 a[8:11],a[208:211],v[132:135],a[8:11]
s_memrealtime s[76:77]
v_exp_f32 v183,v183
v_mfma_f32_16x16x32_bf16 a[8:11],a[212:215],v[148:151],a[8:11]
v_exp_f32 v172,v172
v_mfma_f32_16x16x32_bf16 a[12:15],a[220:223],v[148:151],a[12:15]
v_exp_f32 v173,v173
v_mfma_f32_16x16x32_bf16 a[12:15],a[216:219],v[132:135],a[12:15]
v_exp_f32 v174,v174
v_mfma_f32_16x16x32_bf16 a[16:19],a[224:227],v[132:135],a[16:19]
v_exp_f32 v175,v175
v_mfma_f32_16x16x32_bf16 a[16:19],a[228:231],v[148:151],a[16:19]
v_cvt_pk_bf16_f32 v180,v180,v181
v_cvt_pk_bf16_f32 v181,v182,v183
v_mfma_f32_16x16x32_bf16 a[20:23],a[236:239],v[148:151],a[20:23]
v_exp_f32 v188,v188
v_mfma_f32_16x16x32_bf16 a[20:23],a[232:235],v[132:135],a[20:23]
s_waitcnt lgkmcnt(0)
v_exp_f32 v189,v189
v_mfma_f32_16x16x32_bf16 a[24:27],a[240:243],v[132:135],a[24:27]
s_sub_u32 s79,s78,s76
v_exp_f32 v190,v190
v_mfma_f32_16x16x32_bf16 a[24:27],a[244:247],v[148:151],a[24:27]
s_add_u32 s78,s76,s60
v_exp_f32 v191,v191
v_mfma_f32_16x16x32_bf16 a[28:31],a[252:255],v[148:151],a[28:31]
s_cmp_gt_i32 s79,0
s_cbranch_scc1 L_ps2_%=
L_pr2_%=:
v_cvt_pk_bf16_f32 v166,v172,v173
v_mfma_f32_16x16x32_bf16 a[28:31],a[248:251],v[132:135],a[28:31]
v_cvt_pk_bf16_f32 v167,v174,v175
v_cvt_pk_bf16_f32 v182,v188,v189
v_mfma_f32_16x16x32_bf16 v[48:51],v[28:31],v[132:135],v[48:51]
v_cvt_pk_bf16_f32 v183,v190,v191
s_cmp_lg_u32 s53,0
v_mfma_f32_16x16x32_bf16 v[48:51],v[28:31],v[148:151],v[48:51]
s_cbranch_scc1 L_att0_%=
v_mfma_f32_16x16x32_bf16 v[228:231],a[128:131],v[112:115],v[44:47]
s_waitcnt vmcnt(10)
s_add_u32 s54,s54,16384
v_exp_f32 v196,v196
v_mfma_f32_16x16x32_bf16 v[228:231],a[132:135],v[116:119],v[228:231]
s_barrier
s_add_u32 m0,s49,s51
ds_read_b128 a[128:131],v1
v_mfma_f32_16x16x32_bf16 v[228:231],a[136:139],v[120:123],v[228:231]
s_and_b32 s46,s54,0xfffc0000
ds_read_b128 a[132:135],v1 offset:256
v_and_b32 v18,48,v6
v_mfma_f32_16x16x32_bf16 v[228:231],a[140:143],v[124:127],v[228:231]
s_add_u32 s54,s54,16384
ds_read_b128 a[136:139],v1 offset:512
v_lshrrev_b32 v19,4,v6
v_mfma_f32_16x16x32_bf16 v[236:239],a[156:159],v[124:127],v[44:47]
s_add_u32 s84,s55,0
buffer_load_dwordx4 v4,s[20:23],s47 offen nt lds
v_mfma_f32_16x16x32_bf16 v[236:239],a[152:155],v[120:123],v[236:239]
s_add_u32 s85,s55,4096
ds_read_b128 a[140:143],v1 offset:768
v_lshlrev_b32 v18,2,v18
v_mfma_f32_16x16x32_bf16 v[236:239],a[148:151],v[116:119],v[236:239]
s_add_u32 s86,s55,8192
ds_read_b128 a[156:159],v1 offset:4864
v_and_b32 v19,48,v19
v_mfma_f32_16x16x32_bf16 v[236:239],a[144:147],v[112:115],v[236:239]
s_add_u32 s87,s55,12288
ds_read_b128 a[152:155],v1 offset:4608
v_add_u32 v18,v18,v19
v_mfma_f32_16x16x32_bf16 v[244:247],a[160:163],v[112:115],v[44:47]
s_max_i32 s84,s84,s18
ds_read_b128 a[148:151],v1 offset:4352
v_add_u32 v18,131072,v18
v_mfma_f32_16x16x32_bf16 v[244:247],a[164:167],v[116:119],v[244:247]
s_max_i32 s85,s85,s19
ds_read_b128 a[144:147],v1 offset:4096
v_add_u32 v2,s48,v2
v_mfma_f32_16x16x32_bf16 v[244:247],a[168:171],v[120:123],v[244:247]
s_max_i32 s86,s86,s100
ds_read_b128 a[160:163],v1 offset:8192
v_add_u32 v3,s48,v3
v_mfma_f32_16x16x32_bf16 v[244:247],a[172:175],v[124:127],v[244:247]
s_cmp_eq_u32 s50,131072
s_cselect_b32 s62,163840,0
ds_read_b128 a[164:167],v1 offset:8448
v_mfma_f32_16x16x32_bf16 v[252:255],a[188:191],v[124:127],v[44:47]
s_sub_u32 s48,32768,s62
s_add_u32 s50,s50,s48
ds_read_b128 a[168:171],v1 offset:8704
v_mfma_f32_16x16x32_bf16 v[252:255],a[184:187],v[120:123],v[252:255]
s_cmp_eq_u32 s50,131072
s_cselect_b32 s62,163840,0
ds_read_b128 a[172:175],v1 offset:8960
v_mfma_f32_16x16x32_bf16 v[252:255],a[180:183],v[116:119],v[252:255]
s_max_i32 s87,s87,s101
s_add_u32 s93,s92,L_endseg_%=-L_pc_%=
ds_read_b128 a[188:191],v1 offset:13056
v_mfma_f32_16x16x32_bf16 v[252:255],a[176:179],v[112:115],v[252:255]
s_mov_b32 s80,0
s_mov_b32 s83,s55
ds_read_b128 a[184:187],v1 offset:12800
v_mfma_f32_16x16x32_bf16 a[32:35],a[192:195],v[164:167],a[32:35]
ds_read_b128 a[180:183],v1 offset:12544
v_mfma_f32_16x16x32_bf16 a[32:35],a[196:199],v[180:183],a[32:35]
ds_read_b128 a[176:179],v1 offset:12288
v_add_u32 v1,s48,v1
v_mfma_f32_16x16x32_bf16 a[36:39],a[204:207],v[180:183],a[36:39]
buffer_load_dwordx4 v4,s[20:23],s47 offen offset:1024 nt lds
v_mfma_f32_16x16x32_bf16 a[36:39],a[200:203],v[164:167],a[36:39]
)A"
R"A(v_exp_f32 v197,v197
v_mfma_f32_16x16x32_bf16 a[40:43],a[208:211],v[164:167],a[40:43]
v_exp_f32 v198,v198
v_mfma_f32_16x16x32_bf16 a[40:43],a[212:215],v[180:183],a[40:43]
v_exp_f32 v199,v199
v_mfma_f32_16x16x32_bf16 a[44:47],a[220:223],v[180:183],a[44:47]
v_cvt_pk_bf16_f32 v196,v196,v197
v_cvt_pk_bf16_f32 v197,v198,v199
v_mfma_f32_16x16x32_bf16 a[44:47],a[216:219],v[164:167],a[44:47]
v_exp_f32 v204,v204
v_mfma_f32_16x16x32_bf16 a[48:51],a[224:227],v[164:167],a[48:51]
v_exp_f32 v205,v205
v_mfma_f32_16x16x32_bf16 a[48:51],a[228:231],v[180:183],a[48:51]
v_exp_f32 v206,v206
v_mfma_f32_16x16x32_bf16 a[52:55],a[236:239],v[180:183],a[52:55]
v_exp_f32 v207,v207
v_mfma_f32_16x16x32_bf16 a[52:55],a[232:235],v[164:167],a[52:55]
v_exp_f32 v212,v212
v_mfma_f32_16x16x32_bf16 a[56:59],a[240:243],v[164:167],a[56:59]
v_exp_f32 v213,v213
v_mfma_f32_16x16x32_bf16 a[56:59],a[244:247],v[180:183],a[56:59]
v_exp_f32 v214,v214
v_mfma_f32_16x16x32_bf16 a[60:63],a[252:255],v[180:183],a[60:63]
v_exp_f32 v215,v215
v_mfma_f32_16x16x32_bf16 a[60:63],a[248:251],v[164:167],a[60:63]
v_cvt_pk_bf16_f32 v198,v204,v205
v_cvt_pk_bf16_f32 v199,v206,v207
v_mfma_f32_16x16x32_bf16 v[52:55],v[28:31],v[164:167],v[52:55]
buffer_load_dwordx4 v5,s[20:23],s47 offen offset:2048 nt lds
v_mfma_f32_16x16x32_bf16 v[52:55],v[28:31],v[180:183],v[52:55]
s_waitcnt lgkmcnt(0)
v_cvt_pk_bf16_f32 v212,v212,v213
v_cvt_pk_bf16_f32 v213,v214,v215
v_mfma_f32_16x16x32_bf16 v[132:135],a[128:131],v[64:67],v[32:35]
v_exp_f32 v220,v220
v_mfma_f32_16x16x32_bf16 v[132:135],a[132:135],v[68:71],v[132:135]
v_exp_f32 v221,v221
v_mfma_f32_16x16x32_bf16 v[132:135],a[136:139],v[72:75],v[132:135]
v_exp_f32 v222,v222
v_mfma_f32_16x16x32_bf16 v[132:135],a[140:143],v[76:79],v[132:135]
v_exp_f32 v223,v223
v_mfma_f32_16x16x32_bf16 v[140:143],a[156:159],v[76:79],v[32:35]
v_cvt_pk_bf16_f32 v214,v220,v221
v_cvt_pk_bf16_f32 v215,v222,v223
v_mfma_f32_16x16x32_bf16 v[140:143],a[152:155],v[72:75],v[140:143]
v_exp_f32 v228,v228
v_mfma_f32_16x16x32_bf16 v[140:143],a[148:151],v[68:71],v[140:143]
v_exp_f32 v229,v229
v_mfma_f32_16x16x32_bf16 v[140:143],a[144:147],v[64:67],v[140:143]
v_exp_f32 v230,v230
v_mfma_f32_16x16x32_bf16 v[148:151],a[160:163],v[64:67],v[32:35]
v_exp_f32 v231,v231
v_mfma_f32_16x16x32_bf16 v[148:151],a[164:167],v[68:71],v[148:151]
v_cvt_pk_bf16_f32 v228,v228,v229
v_cvt_pk_bf16_f32 v229,v230,v231
v_mfma_f32_16x16x32_bf16 v[148:151],a[168:171],v[72:75],v[148:151]
v_exp_f32 v236,v236
v_mfma_f32_16x16x32_bf16 v[148:151],a[172:175],v[76:79],v[148:151]
v_exp_f32 v237,v237
v_mfma_f32_16x16x32_bf16 v[156:159],a[188:191],v[76:79],v[32:35]
buffer_load_dwordx4 v5,s[20:23],s47 offen offset:3072 nt lds
v_mfma_f32_16x16x32_bf16 v[156:159],a[184:187],v[72:75],v[156:159]
v_exp_f32 v238,v238
v_mfma_f32_16x16x32_bf16 v[156:159],a[180:183],v[68:71],v[156:159]
v_exp_f32 v239,v239
v_mfma_f32_16x16x32_bf16 v[156:159],a[176:179],v[64:67],v[156:159]
v_exp_f32 v244,v244
v_mfma_f32_16x16x32_bf16 a[64:67],a[192:195],v[196:199],a[64:67]
v_exp_f32 v245,v245
v_mfma_f32_16x16x32_bf16 a[64:67],a[196:199],v[212:215],a[64:67]
v_exp_f32 v246,v246
v_mfma_f32_16x16x32_bf16 a[68:71],a[204:207],v[212:215],a[68:71]
v_exp_f32 v247,v247
v_mfma_f32_16x16x32_bf16 a[68:71],a[200:203],v[196:199],a[68:71]
v_cvt_pk_bf16_f32 v230,v236,v237
v_cvt_pk_bf16_f32 v231,v238,v239
v_mfma_f32_16x16x32_bf16 a[72:75],a[208:211],v[196:199],a[72:75]
v_cvt_pk_bf16_f32 v244,v244,v245
v_cvt_pk_bf16_f32 v245,v246,v247
v_mfma_f32_16x16x32_bf16 a[72:75],a[212:215],v[212:215],a[72:75]
v_exp_f32 v252,v252
v_mfma_f32_16x16x32_bf16 a[76:79],a[220:223],v[212:215],a[76:79]
v_exp_f32 v253,v253
v_mfma_f32_16x16x32_bf16 a[76:79],a[216:219],v[196:199],a[76:79]
s_add_u32 m0,s49,s52
v_exp_f32 v254,v254
v_mfma_f32_16x16x32_bf16 a[80:83],a[224:227],v[196:199],a[80:83]
s_add_u32 s49,s49,32768
v_exp_f32 v255,v255
v_mfma_f32_16x16x32_bf16 a[80:83],a[228:231],v[212:215],a[80:83]
v_cvt_pk_bf16_f32 v246,v252,v253
v_cvt_pk_bf16_f32 v247,v254,v255
v_mfma_f32_16x16x32_bf16 a[84:87],a[236:239],v[212:215],a[84:87]
buffer_load_dwordx4 v4,s[24:27],s47 offen nt lds
v_mfma_f32_16x16x32_bf16 a[84:87],a[232:235],v[196:199],a[84:87]
v_exp_f32 v132,v132
v_mfma_f32_16x16x32_bf16 a[88:91],a[240:243],v[196:199],a[88:91]
v_exp_f32 v133,v133
v_mfma_f32_16x16x32_bf16 a[88:91],a[244:247],v[212:215],a[88:91]
v_exp_f32 v134,v134
v_mfma_f32_16x16x32_bf16 a[92:95],a[252:255],v[212:215],a[92:95]
v_exp_f32 v135,v135
v_mfma_f32_16x16x32_bf16 a[92:95],a[248:251],v[196:199],a[92:95]
v_cvt_pk_bf16_f32 v132,v132,v133
v_cvt_pk_bf16_f32 v133,v134,v135
v_mfma_f32_16x16x32_bf16 v[56:59],v[28:31],v[196:199],v[56:59]
v_exp_f32 v140,v140
v_mfma_f32_16x16x32_bf16 v[56:59],v[28:31],v[212:215],v[56:59]
v_exp_f32 v141,v141
v_mfma_f32_16x16x32_bf16 v[164:167],a[128:131],v[80:83],v[36:39]
v_exp_f32 v142,v142
v_mfma_f32_16x16x32_bf16 v[164:167],a[132:135],v[84:87],v[164:167]
v_exp_f32 v143,v143
v_mfma_f32_16x16x32_bf16 v[164:167],a[136:139],v[88:91],v[164:167]
v_exp_f32 v148,v148
v_mfma_f32_16x16x32_bf16 v[164:167],a[140:143],v[92:95],v[164:167]
v_exp_f32 v149,v149
v_mfma_f32_16x16x32_bf16 v[172:175],a[156:159],v[92:95],v[36:39]
v_exp_f32 v150,v150
v_mfma_f32_16x16x32_bf16 v[172:175],a[152:155],v[88:91],v[172:175]
v_exp_f32 v151,v151
v_mfma_f32_16x16x32_bf16 v[172:175],a[148:151],v[84:87],v[172:175]
buffer_load_dwordx4 v5,s[24:27],s47 offen offset:1024 nt lds
v_mfma_f32_16x16x32_bf16 v[172:175],a[144:147],v[80:83],v[172:175]
v_cvt_pk_bf16_f32 v134,v140,v141
v_cvt_pk_bf16_f32 v135,v142,v143
v_mfma_f32_16x16x32_bf16 v[180:183],a[160:163],v[80:83],v[36:39]
v_cvt_pk_bf16_f32 v148,v148,v149
v_cvt_pk_bf16_f32 v149,v150,v151
v_mfma_f32_16x16x32_bf16 v[180:183],a[164:167],v[84:87],v[180:183]
v_exp_f32 v156,v156
v_mfma_f32_16x16x32_bf16 v[180:183],a[168:171],v[88:91],v[180:183]
v_exp_f32 v157,v157
v_mfma_f32_16x16x32_bf16 v[180:183],a[172:175],v[92:95],v[180:183]
v_exp_f32 v158,v158
v_mfma_f32_16x16x32_bf16 v[188:191],a[188:191],v[92:95],v[36:39]
v_exp_f32 v159,v159
v_mfma_f32_16x16x32_bf16 v[188:191],a[184:187],v[88:91],v[188:191]
v_cvt_pk_bf16_f32 v150,v156,v157
v_cvt_pk_bf16_f32 v151,v158,v159
v_mfma_f32_16x16x32_bf16 v[188:191],a[180:183],v[84:87],v[188:191]
s_waitcnt vmcnt(14)
s_cmp_eq_u32 s49,163840
s_cselect_b32 s49,0,s49
v_mfma_f32_16x16x32_bf16 v[188:191],a[176:179],v[80:83],v[188:191]
s_barrier
ds_read_b128 v[80:83],v18
v_mfma_f32_16x16x32_bf16 a[96:99],a[192:195],v[228:231],a[96:99]
ds_read_b128 v[84:87],v18 offset:256
v_mfma_f32_16x16x32_bf16 a[96:99],a[196:199],v[244:247],a[96:99]
ds_read_b128 v[88:91],v18 offset:512
v_mfma_f32_16x16x32_bf16 a[100:103],a[204:207],v[244:247],a[100:103]
ds_read_b128 v[92:95],v18 offset:768
v_mfma_f32_16x16x32_bf16 a[100:103],a[200:203],v[228:231],a[100:103]
buffer_load_dwordx4 v4,s[24:27],s47 offen offset:2048 nt lds
v_mfma_f32_16x16x32_bf16 a[104:107],a[208:211],v[228:231],a[104:107]
ds_read_b64_tr_b16 a[192:193],v2
ds_read_b64_tr_b16 a[194:195],v2 offset:4096
v_mfma_f32_16x16x32_bf16 a[104:107],a[212:215],v[244:247],a[104:107]
ds_read_b64_tr_b16 a[196:197],v2 offset:8192
ds_read_b64_tr_b16 a[198:199],v2 offset:12288
v_mfma_f32_16x16x32_bf16 a[108:111],a[220:223],v[244:247],a[108:111]
ds_read_b64_tr_b16 a[204:205],v3 offset:8192
ds_read_b64_tr_b16 a[206:207],v3 offset:12288
v_mfma_f32_16x16x32_bf16 a[108:111],a[216:219],v[228:231],a[108:111]
ds_read_b64_tr_b16 a[200:201],v3
ds_read_b64_tr_b16 a[202:203],v3 offset:4096
v_mfma_f32_16x16x32_bf16 a[112:115],a[224:227],v[228:231],a[112:115]
ds_read_b64_tr_b16 a[208:209],v2 offset:256
ds_read_b64_tr_b16 a[210:211],v2 offset:4352
v_mfma_f32_16x16x32_bf16 a[112:115],a[228:231],v[244:247],a[112:115]
ds_read_b64_tr_b16 a[212:213],v2 offset:8448
ds_read_b64_tr_b16 a[214:215],v2 offset:12544
v_mfma_f32_16x16x32_bf16 a[116:119],a[236:239],v[244:247],a[116:119]
ds_read_b64_tr_b16 a[220:221],v3 offset:8448
ds_read_b64_tr_b16 a[222:223],v3 offset:12544
v_mfma_f32_16x16x32_bf16 a[116:119],a[232:235],v[228:231],a[116:119]
ds_read_b64_tr_b16 a[216:217],v3 offset:256
ds_read_b64_tr_b16 a[218:219],v3 offset:4352
v_mfma_f32_16x16x32_bf16 a[120:123],a[240:243],v[228:231],a[120:123]
ds_read_b64_tr_b16 a[224:225],v2 offset:512
ds_read_b64_tr_b16 a[226:227],v2 offset:4608
v_mfma_f32_16x16x32_bf16 a[120:123],a[244:247],v[244:247],a[120:123]
ds_read_b64_tr_b16 a[228:229],v2 offset:8704
ds_read_b64_tr_b16 a[230:231],v2 offset:12800
v_mfma_f32_16x16x32_bf16 a[124:127],a[252:255],v[244:247],a[124:127]
ds_read_b64_tr_b16 a[236:237],v3 offset:8704
ds_read_b64_tr_b16 a[238:239],v3 offset:12800
v_mfma_f32_16x16x32_bf16 a[124:127],a[248:251],v[228:231],a[124:127]
ds_read_b64_tr_b16 a[232:233],v3 offset:512
ds_read_b64_tr_b16 a[234:235],v3 offset:4608
v_mfma_f32_16x16x32_bf16 v[60:63],v[28:31],v[228:231],v[60:63]
ds_read_b64_tr_b16 a[240:241],v2 offset:768
ds_read_b64_tr_b16 a[242:243],v2 offset:4864
v_mfma_f32_16x16x32_bf16 v[60:63],v[28:31],v[244:247],v[60:63]
buffer_load_dwordx4 v5,s[24:27],s47 offen offset:3072 nt lds
v_mfma_f32_16x16x32_bf16 v[196:199],a[128:131],v[96:99],v[40:43]
s_add_u32 s47,s47,16384
ds_read_b64_tr_b16 a[244:245],v2 offset:8960
ds_read_b64_tr_b16 a[246:247],v2 offset:13056
v_mfma_f32_16x16x32_bf16 v[196:199],a[132:135],v[100:103],v[196:199]
s_and_b32 s47,s47,0x3ffff
ds_read_b64_tr_b16 a[252:253],v3 offset:8960
ds_read_b64_tr_b16 a[254:255],v3 offset:13056
v_mfma_f32_16x16x32_bf16 v[196:199],a[136:139],v[104:107],v[196:199]
s_or_b32 s47,s47,s46
ds_read_b64_tr_b16 a[248:249],v3 offset:768
ds_read_b64_tr_b16 a[250:251],v3 offset:4864
v_mfma_f32_16x16x32_bf16 v[196:199],a[140:143],v[108:111],v[196:199]
s_and_b32 s46,s54,0xfffc0000
v_exp_f32 v164,v164
v_mfma_f32_16x16x32_bf16 v[204:207],a[156:159],v[108:111],v[40:43]
s_add_u32 s54,s54,16384
v_exp_f32 v165,v165
v_mfma_f32_16x16x32_bf16 v[204:207],a[152:155],v[104:107],v[204:207]
v_exp_f32 v166,v166
v_mfma_f32_16x16x32_bf16 v[204:207],a[148:151],v[100:103],v[204:207]
v_exp_f32 v167,v167
v_mfma_f32_16x16x32_bf16 v[204:207],a[144:147],v[96:99],v[204:207]
v_cvt_pk_bf16_f32 v164,v164,v165
v_cvt_pk_bf16_f32 v165,v166,v167
v_mfma_f32_16x16x32_bf16 v[212:215],a[160:163],v[96:99],v[40:43]
v_exp_f32 v172,v172
v_mfma_f32_16x16x32_bf16 v[212:215],a[164:167],v[100:103],v[212:215]
v_exp_f32 v173,v173
v_mfma_f32_16x16x32_bf16 v[212:215],a[168:171],v[104:107],v[212:215]
v_exp_f32 v174,v174
v_mfma_f32_16x16x32_bf16 v[212:215],a[172:175],v[108:111],v[212:215]
v_exp_f32 v175,v175
v_mfma_f32_16x16x32_bf16 v[220:223],a[188:191],v[108:111],v[40:43]
v_exp_f32 v180,v180
v_mfma_f32_16x16x32_bf16 v[220:223],a[184:187],v[104:107],v[220:223]
v_exp_f32 v181,v181
v_mfma_f32_16x16x32_bf16 v[220:223],a[180:183],v[100:103],v[220:223]
v_exp_f32 v182,v182
v_mfma_f32_16x16x32_bf16 v[220:223],a[176:179],v[96:99],v[220:223]
s_waitcnt lgkmcnt(0)
v_exp_f32 v183,v183
v_mfma_f32_16x16x32_bf16 v[32:35],v[80:83],v[128:131],0
v_cvt_pk_bf16_f32 v166,v172,v173
v_cvt_pk_bf16_f32 v167,v174,v175
v_mfma_f32_16x16x32_bf16 v[32:35],v[84:87],v[160:163],v[32:35]
v_cvt_pk_bf16_f32 v180,v180,v181
v_cvt_pk_bf16_f32 v181,v182,v183
v_mfma_f32_16x16x32_bf16 v[32:35],v[88:91],v[192:195],v[32:35]
v_exp_f32 v188,v188
v_mfma_f32_16x16x32_bf16 v[32:35],v[92:95],v[224:227],v[32:35]
v_exp_f32 v189,v189
v_mfma_f32_16x16x32_bf16 v[36:39],v[80:83],v[136:139],0
v_exp_f32 v190,v190
v_mfma_f32_16x16x32_bf16 v[36:39],v[84:87],v[168:171],v[36:39]
v_exp_f32 v191,v191
v_mfma_f32_16x16x32_bf16 v[36:39],v[88:91],v[200:203],v[36:39]
v_cvt_pk_bf16_f32 v182,v188,v189
v_cvt_pk_bf16_f32 v183,v190,v191
v_mfma_f32_16x16x32_bf16 v[36:39],v[92:95],v[232:235],v[36:39]
v_exp_f32 v196,v196
v_mfma_f32_16x16x32_bf16 v[40:43],v[80:83],v[144:147],0
v_exp_f32 v197,v197
v_mfma_f32_16x16x32_bf16 v[40:43],v[84:87],v[176:179],v[40:43]
v_max3_f32 v32,v32,v33,v34
v_max3_f32 v32,v32,v35,s18
v_mfma_f32_16x16x32_bf16 v[40:43],v[88:91],v[208:211],v[40:43]
v_sub_f32 v35,0,v32
v_sub_f32 v34,0,v32
v_mfma_f32_16x16x32_bf16 v[40:43],v[92:95],v[240:243],v[40:43]
v_sub_f32 v33,0,v32
v_sub_f32 v32,0,v32
v_mfma_f32_16x16x32_bf16 a[0:3],a[192:195],v[132:135],a[0:3]
v_exp_f32 v198,v198
)A"
R"A(v_mfma_f32_16x16x32_bf16 a[0:3],a[196:199],v[148:151],a[0:3]
v_exp_f32 v199,v199
v_mfma_f32_16x16x32_bf16 a[4:7],a[204:207],v[148:151],a[4:7]
v_cvt_pk_bf16_f32 v196,v196,v197
v_cvt_pk_bf16_f32 v197,v198,v199
v_mfma_f32_16x16x32_bf16 a[4:7],a[200:203],v[132:135],a[4:7]
v_exp_f32 v204,v204
v_mfma_f32_16x16x32_bf16 a[8:11],a[208:211],v[132:135],a[8:11]
s_memrealtime s[76:77]
v_exp_f32 v205,v205
v_mfma_f32_16x16x32_bf16 a[8:11],a[212:215],v[148:151],a[8:11]
v_exp_f32 v206,v206
v_mfma_f32_16x16x32_bf16 a[12:15],a[220:223],v[148:151],a[12:15]
v_exp_f32 v207,v207
v_mfma_f32_16x16x32_bf16 a[12:15],a[216:219],v[132:135],a[12:15]
v_exp_f32 v212,v212
v_mfma_f32_16x16x32_bf16 a[16:19],a[224:227],v[132:135],a[16:19]
v_exp_f32 v213,v213
v_mfma_f32_16x16x32_bf16 a[16:19],a[228:231],v[148:151],a[16:19]
v_exp_f32 v214,v214
v_mfma_f32_16x16x32_bf16 a[20:23],a[236:239],v[148:151],a[20:23]
v_exp_f32 v215,v215
v_mfma_f32_16x16x32_bf16 a[20:23],a[232:235],v[132:135],a[20:23]
s_waitcnt lgkmcnt(0)
v_cvt_pk_bf16_f32 v198,v204,v205
v_cvt_pk_bf16_f32 v199,v206,v207
v_mfma_f32_16x16x32_bf16 a[24:27],a[240:243],v[132:135],a[24:27]
s_sub_u32 s79,s78,s76
v_cvt_pk_bf16_f32 v212,v212,v213
v_cvt_pk_bf16_f32 v213,v214,v215
v_mfma_f32_16x16x32_bf16 a[24:27],a[244:247],v[148:151],a[24:27]
s_add_u32 s78,s76,s60
v_exp_f32 v220,v220
v_mfma_f32_16x16x32_bf16 a[28:31],a[252:255],v[148:151],a[28:31]
s_cmp_gt_i32 s79,0
s_cbranch_scc1 L_ps3_%=
L_pr3_%=:
v_max3_f32 v36,v36,v37,v38
v_mfma_f32_16x16x32_bf16 a[28:31],a[248:251],v[132:135],a[28:31]
v_exp_f32 v221,v221
v_mfma_f32_16x16x32_bf16 v[48:51],v[28:31],v[132:135],v[48:51]
v_exp_f32 v222,v222
v_mfma_f32_16x16x32_bf16 v[48:51],v[28:31],v[148:151],v[48:51]
v_exp_f32 v223,v223
v_mfma_f32_16x16x32_bf16 v[228:231],a[128:131],v[112:115],v[44:47]
s_waitcnt vmcnt(9)
v_cvt_pk_bf16_f32 v214,v220,v221
v_cvt_pk_bf16_f32 v215,v222,v223
v_mfma_f32_16x16x32_bf16 v[228:231],a[132:135],v[116:119],v[228:231]
s_barrier
s_add_u32 m0,s49,s51
ds_read_b128 a[128:131],v1
v_mfma_f32_16x16x32_bf16 v[228:231],a[136:139],v[120:123],v[228:231]
ds_read_b128 a[132:135],v1 offset:256
v_max3_f32 v36,v36,v39,s19
v_mfma_f32_16x16x32_bf16 v[228:231],a[140:143],v[124:127],v[228:231]
ds_read_b128 a[136:139],v1 offset:512
v_sub_f32 v39,0,v36
v_mfma_f32_16x16x32_bf16 v[236:239],a[156:159],v[124:127],v[44:47]
buffer_load_dwordx4 v4,s[20:23],s47 offen nt lds
v_mfma_f32_16x16x32_bf16 v[236:239],a[152:155],v[120:123],v[236:239]
ds_read_b128 a[140:143],v1 offset:768
v_sub_f32 v38,0,v36
v_mfma_f32_16x16x32_bf16 v[236:239],a[148:151],v[116:119],v[236:239]
ds_read_b128 a[156:159],v1 offset:4864
v_sub_f32 v37,0,v36
v_mfma_f32_16x16x32_bf16 v[236:239],a[144:147],v[112:115],v[236:239]
ds_read_b128 a[152:155],v1 offset:4608
v_sub_f32 v36,0,v36
v_mfma_f32_16x16x32_bf16 v[244:247],a[160:163],v[112:115],v[44:47]
ds_read_b128 a[148:151],v1 offset:4352
v_max3_f32 v40,v40,v41,v42
v_mfma_f32_16x16x32_bf16 v[244:247],a[164:167],v[116:119],v[244:247]
ds_read_b128 a[144:147],v1 offset:4096
v_max3_f32 v40,v40,v43,s100
v_mfma_f32_16x16x32_bf16 v[244:247],a[168:171],v[120:123],v[244:247]
ds_read_b128 a[160:163],v1 offset:8192
v_sub_f32 v43,0,v40
v_mfma_f32_16x16x32_bf16 v[244:247],a[172:175],v[124:127],v[244:247]
ds_read_b128 a[164:167],v1 offset:8448
v_sub_f32 v42,0,v40
v_mfma_f32_16x16x32_bf16 v[252:255],a[188:191],v[124:127],v[44:47]
ds_read_b128 a[168:171],v1 offset:8704
v_sub_f32 v41,0,v40
v_mfma_f32_16x16x32_bf16 v[252:255],a[184:187],v[120:123],v[252:255]
ds_read_b128 a[172:175],v1 offset:8960
v_sub_f32 v40,0,v40
v_mfma_f32_16x16x32_bf16 v[252:255],a[180:183],v[116:119],v[252:255]
ds_read_b128 a[188:191],v1 offset:13056
v_add_u32 v2,s48,v2
v_mfma_f32_16x16x32_bf16 v[252:255],a[176:179],v[112:115],v[252:255]
ds_read_b128 a[184:187],v1 offset:12800
v_add_u32 v3,s48,v3
v_mfma_f32_16x16x32_bf16 v[44:47],v[80:83],v[152:155],0
s_sub_u32 s48,32768,s62
ds_read_b128 a[180:183],v1 offset:12544
v_max_f32 v20,0x00800000,v48
v_mfma_f32_16x16x32_bf16 v[44:47],v[84:87],v[184:187],v[44:47]
s_add_u32 s50,s50,s48
ds_read_b128 a[176:179],v1 offset:12288
v_accvgpr_read_b32 v64,a0
v_mfma_f32_16x16x32_bf16 v[44:47],v[88:91],v[216:219],v[44:47]
buffer_load_dwordx4 v4,s[20:23],s47 offen offset:1024 nt lds
v_mfma_f32_16x16x32_bf16 v[44:47],v[92:95],v[248:251],v[44:47]
v_exp_f32 v228,v228
v_mfma_f32_16x16x32_bf16 a[32:35],a[192:195],v[164:167],a[32:35]
v_exp_f32 v229,v229
v_mfma_f32_16x16x32_bf16 a[32:35],a[196:199],v[180:183],a[32:35]
v_exp_f32 v230,v230
v_mfma_f32_16x16x32_bf16 a[36:39],a[204:207],v[180:183],a[36:39]
v_exp_f32 v231,v231
v_mfma_f32_16x16x32_bf16 a[36:39],a[200:203],v[164:167],a[36:39]
v_cvt_pk_bf16_f32 v228,v228,v229
v_cvt_pk_bf16_f32 v229,v230,v231
v_mfma_f32_16x16x32_bf16 a[40:43],a[208:211],v[164:167],a[40:43]
v_exp_f32 v236,v236
v_mfma_f32_16x16x32_bf16 a[40:43],a[212:215],v[180:183],a[40:43]
v_exp_f32 v237,v237
v_mfma_f32_16x16x32_bf16 a[44:47],a[220:223],v[180:183],a[44:47]
v_exp_f32 v238,v238
v_mfma_f32_16x16x32_bf16 a[44:47],a[216:219],v[164:167],a[44:47]
v_exp_f32 v239,v239
v_mfma_f32_16x16x32_bf16 a[48:51],a[224:227],v[164:167],a[48:51]
v_exp_f32 v244,v244
v_mfma_f32_16x16x32_bf16 a[48:51],a[228:231],v[180:183],a[48:51]
v_exp_f32 v245,v245
v_mfma_f32_16x16x32_bf16 a[52:55],a[236:239],v[180:183],a[52:55]
v_exp_f32 v246,v246
v_mfma_f32_16x16x32_bf16 a[52:55],a[232:235],v[164:167],a[52:55]
v_exp_f32 v247,v247
v_mfma_f32_16x16x32_bf16 a[56:59],a[240:243],v[164:167],a[56:59]
buffer_load_dwordx4 v5,s[20:23],s47 offen offset:2048 nt lds
v_mfma_f32_16x16x32_bf16 a[56:59],a[244:247],v[180:183],a[56:59]
v_cvt_pk_bf16_f32 v230,v236,v237
v_cvt_pk_bf16_f32 v231,v238,v239
v_mfma_f32_16x16x32_bf16 a[60:63],a[252:255],v[180:183],a[60:63]
v_cvt_pk_bf16_f32 v244,v244,v245
v_cvt_pk_bf16_f32 v245,v246,v247
v_mfma_f32_16x16x32_bf16 a[60:63],a[248:251],v[164:167],a[60:63]
v_exp_f32 v252,v252
v_mfma_f32_16x16x32_bf16 v[52:55],v[28:31],v[164:167],v[52:55]
v_exp_f32 v253,v253
v_mfma_f32_16x16x32_bf16 v[52:55],v[28:31],v[180:183],v[52:55]
s_waitcnt lgkmcnt(0)
v_exp_f32 v254,v254
v_mfma_f32_16x16x32_bf16 v[132:135],a[128:131],v[128:131],v[32:35]
v_exp_f32 v255,v255
v_mfma_f32_16x16x32_bf16 v[132:135],a[132:135],v[160:163],v[132:135]
v_cvt_pk_bf16_f32 v246,v252,v253
v_cvt_pk_bf16_f32 v247,v254,v255
v_mfma_f32_16x16x32_bf16 v[132:135],a[136:139],v[192:195],v[132:135]
v_rcp_f32 v20,v20
v_mfma_f32_16x16x32_bf16 v[132:135],a[140:143],v[224:227],v[132:135]
v_accvgpr_read_b32 v65,a1
v_accvgpr_read_b32 v66,a2
v_mfma_f32_16x16x32_bf16 v[140:143],a[156:159],v[224:227],v[32:35]
v_accvgpr_read_b32 v67,a3
v_mul_f32 v64,v20,v64
v_mfma_f32_16x16x32_bf16 v[140:143],a[152:155],v[192:195],v[140:143]
v_mul_f32 v65,v20,v65
v_cvt_pk_bf16_f32 v64,v64,v65
v_mfma_f32_16x16x32_bf16 v[140:143],a[148:151],v[160:163],v[140:143]
v_mul_f32 v66,v20,v66
v_mul_f32 v67,v20,v67
v_mfma_f32_16x16x32_bf16 v[140:143],a[144:147],v[128:131],v[140:143]
v_cvt_pk_bf16_f32 v65,v66,v67
v_accvgpr_read_b32 v14,a6
v_mfma_f32_16x16x32_bf16 v[148:151],a[160:163],v[128:131],v[32:35]
buffer_load_dwordx4 v5,s[20:23],s47 offen offset:3072 nt lds
v_mfma_f32_16x16x32_bf16 v[148:151],a[164:167],v[160:163],v[148:151]
v_exp_f32 v132,v132
v_mfma_f32_16x16x32_bf16 v[148:151],a[168:171],v[192:195],v[148:151]
v_exp_f32 v133,v133
v_mfma_f32_16x16x32_bf16 v[148:151],a[172:175],v[224:227],v[148:151]
v_exp_f32 v134,v134
v_mfma_f32_16x16x32_bf16 v[156:159],a[188:191],v[224:227],v[32:35]
v_exp_f32 v135,v135
v_mfma_f32_16x16x32_bf16 v[156:159],a[184:187],v[192:195],v[156:159]
v_cvt_pk_bf16_f32 v132,v132,v133
v_cvt_pk_bf16_f32 v133,v134,v135
v_mfma_f32_16x16x32_bf16 v[156:159],a[180:183],v[160:163],v[156:159]
v_exp_f32 v140,v140
v_mfma_f32_16x16x32_bf16 v[156:159],a[176:179],v[128:131],v[156:159]
v_exp_f32 v141,v141
v_mfma_f32_16x16x32_bf16 a[64:67],a[192:195],v[196:199],a[64:67]
v_exp_f32 v142,v142
v_mfma_f32_16x16x32_bf16 a[64:67],a[196:199],v[212:215],a[64:67]
v_exp_f32 v143,v143
v_mfma_f32_16x16x32_bf16 a[68:71],a[204:207],v[212:215],a[68:71]
v_exp_f32 v148,v148
v_mfma_f32_16x16x32_bf16 a[68:71],a[200:203],v[196:199],a[68:71]
s_add_u32 m0,s49,s52
v_exp_f32 v149,v149
v_mfma_f32_16x16x32_bf16 a[72:75],a[208:211],v[196:199],a[72:75]
s_add_u32 s49,s49,32768
v_exp_f32 v150,v150
v_mfma_f32_16x16x32_bf16 a[72:75],a[212:215],v[212:215],a[72:75]
v_exp_f32 v151,v151
v_mfma_f32_16x16x32_bf16 a[76:79],a[220:223],v[212:215],a[76:79]
buffer_load_dwordx4 v4,s[24:27],s47 offen nt lds
v_mfma_f32_16x16x32_bf16 a[76:79],a[216:219],v[196:199],a[76:79]
v_cvt_pk_bf16_f32 v134,v140,v141
v_cvt_pk_bf16_f32 v135,v142,v143
v_mfma_f32_16x16x32_bf16 a[80:83],a[224:227],v[196:199],a[80:83]
v_cvt_pk_bf16_f32 v148,v148,v149
v_cvt_pk_bf16_f32 v149,v150,v151
v_mfma_f32_16x16x32_bf16 a[80:83],a[228:231],v[212:215],a[80:83]
v_exp_f32 v156,v156
v_mfma_f32_16x16x32_bf16 a[84:87],a[236:239],v[212:215],a[84:87]
v_exp_f32 v157,v157
v_mfma_f32_16x16x32_bf16 a[84:87],a[232:235],v[196:199],a[84:87]
v_exp_f32 v158,v158
v_mfma_f32_16x16x32_bf16 a[88:91],a[240:243],v[196:199],a[88:91]
v_exp_f32 v159,v159
v_mfma_f32_16x16x32_bf16 a[88:91],a[244:247],v[212:215],a[88:91]
v_cvt_pk_bf16_f32 v150,v156,v157
v_cvt_pk_bf16_f32 v151,v158,v159
v_mfma_f32_16x16x32_bf16 a[92:95],a[252:255],v[212:215],a[92:95]
v_accvgpr_read_b32 v15,a7
v_accvgpr_read_b32 v66,a4
v_mfma_f32_16x16x32_bf16 a[92:95],a[248:251],v[196:199],a[92:95]
v_accvgpr_read_b32 v67,a5
v_accvgpr_read_b32 v68,a8
v_mfma_f32_16x16x32_bf16 v[56:59],v[28:31],v[196:199],v[56:59]
v_accvgpr_read_b32 v69,a9
v_accvgpr_read_b32 v70,a10
v_mfma_f32_16x16x32_bf16 v[56:59],v[28:31],v[212:215],v[56:59]
v_accvgpr_read_b32 v71,a11
v_mul_f32 v68,v20,v68
v_mfma_f32_16x16x32_bf16 v[164:167],a[128:131],v[136:139],v[36:39]
v_mul_f32 v69,v20,v69
v_cvt_pk_bf16_f32 v68,v68,v69
v_mfma_f32_16x16x32_bf16 v[164:167],a[132:135],v[168:171],v[164:167]
v_mul_f32 v70,v20,v70
v_mul_f32 v71,v20,v71
v_mfma_f32_16x16x32_bf16 v[164:167],a[136:139],v[200:203],v[164:167]
buffer_load_dwordx4 v5,s[24:27],s47 offen offset:1024 nt lds
v_mfma_f32_16x16x32_bf16 v[164:167],a[140:143],v[232:235],v[164:167]
v_cvt_pk_bf16_f32 v69,v70,v71
v_accvgpr_read_b32 v16,a14
v_mfma_f32_16x16x32_bf16 v[172:175],a[156:159],v[232:235],v[36:39]
v_accvgpr_read_b32 v17,a15
v_accvgpr_read_b32 v70,a12
v_mfma_f32_16x16x32_bf16 v[172:175],a[152:155],v[200:203],v[172:175]
v_accvgpr_read_b32 v71,a13
v_accvgpr_read_b32 v72,a16
v_mfma_f32_16x16x32_bf16 v[172:175],a[148:151],v[168:171],v[172:175]
v_accvgpr_read_b32 v73,a17
v_accvgpr_read_b32 v74,a18
v_mfma_f32_16x16x32_bf16 v[172:175],a[144:147],v[136:139],v[172:175]
v_accvgpr_read_b32 v75,a19
v_mul_f32 v72,v20,v72
v_mfma_f32_16x16x32_bf16 v[180:183],a[160:163],v[136:139],v[36:39]
v_mul_f32 v73,v20,v73
v_cvt_pk_bf16_f32 v72,v72,v73
v_mfma_f32_16x16x32_bf16 v[180:183],a[164:167],v[168:171],v[180:183]
v_mul_f32 v74,v20,v74
v_mul_f32 v75,v20,v75
v_mfma_f32_16x16x32_bf16 v[180:183],a[168:171],v[200:203],v[180:183]
v_cvt_pk_bf16_f32 v73,v74,v75
v_accvgpr_read_b32 v12,a22
v_mfma_f32_16x16x32_bf16 v[180:183],a[172:175],v[232:235],v[180:183]
v_accvgpr_read_b32 v13,a23
v_accvgpr_read_b32 v74,a20
v_mfma_f32_16x16x32_bf16 v[188:191],a[188:191],v[232:235],v[36:39]
v_accvgpr_read_b32 v75,a21
v_accvgpr_read_b32 v76,a24
v_mfma_f32_16x16x32_bf16 v[188:191],a[184:187],v[200:203],v[188:191]
v_accvgpr_read_b32 v77,a25
v_accvgpr_read_b32 v78,a26
v_mfma_f32_16x16x32_bf16 v[188:191],a[180:183],v[168:171],v[188:191]
v_accvgpr_read_b32 v79,a27
v_mul_f32 v66,v20,v66
v_mfma_f32_16x16x32_bf16 v[188:191],a[176:179],v[136:139],v[188:191]
v_mul_f32 v67,v20,v67
v_mul_f32 v76,v20,v76
v_mfma_f32_16x16x32_bf16 a[96:99],a[192:195],v[228:231],a[96:99]
buffer_load_dwordx4 v4,s[24:27],s47 offen offset:2048 nt lds
v_mfma_f32_16x16x32_bf16 a[96:99],a[196:199],v[244:247],a[96:99]
ds_read_b64_tr_b16 a[192:193],v2
ds_read_b64_tr_b16 a[194:195],v2 offset:4096
v_mfma_f32_16x16x32_bf16 a[100:103],a[204:207],v[244:247],a[100:103]
ds_read_b64_tr_b16 a[196:197],v2 offset:8192
ds_read_b64_tr_b16 a[198:199],v2 offset:12288
v_mfma_f32_16x16x32_bf16 a[100:103],a[200:203],v[228:231],a[100:103]
)A"
R"A(ds_read_b64_tr_b16 a[204:205],v3 offset:8192
ds_read_b64_tr_b16 a[206:207],v3 offset:12288
v_mfma_f32_16x16x32_bf16 a[104:107],a[208:211],v[228:231],a[104:107]
ds_read_b64_tr_b16 a[200:201],v3
ds_read_b64_tr_b16 a[202:203],v3 offset:4096
v_mfma_f32_16x16x32_bf16 a[104:107],a[212:215],v[244:247],a[104:107]
ds_read_b64_tr_b16 a[208:209],v2 offset:256
ds_read_b64_tr_b16 a[210:211],v2 offset:4352
v_mfma_f32_16x16x32_bf16 a[108:111],a[220:223],v[244:247],a[108:111]
ds_read_b64_tr_b16 a[212:213],v2 offset:8448
ds_read_b64_tr_b16 a[214:215],v2 offset:12544
v_mfma_f32_16x16x32_bf16 a[108:111],a[216:219],v[228:231],a[108:111]
ds_read_b64_tr_b16 a[220:221],v3 offset:8448
ds_read_b64_tr_b16 a[222:223],v3 offset:12544
v_mfma_f32_16x16x32_bf16 a[112:115],a[224:227],v[228:231],a[112:115]
ds_read_b64_tr_b16 a[216:217],v3 offset:256
ds_read_b64_tr_b16 a[218:219],v3 offset:4352
v_mfma_f32_16x16x32_bf16 a[112:115],a[228:231],v[244:247],a[112:115]
ds_read_b64_tr_b16 a[224:225],v2 offset:512
ds_read_b64_tr_b16 a[226:227],v2 offset:4608
v_mfma_f32_16x16x32_bf16 a[116:119],a[236:239],v[244:247],a[116:119]
ds_read_b64_tr_b16 a[228:229],v2 offset:8704
ds_read_b64_tr_b16 a[230:231],v2 offset:12800
v_mfma_f32_16x16x32_bf16 a[116:119],a[232:235],v[228:231],a[116:119]
ds_read_b64_tr_b16 a[236:237],v3 offset:8704
ds_read_b64_tr_b16 a[238:239],v3 offset:12800
v_mfma_f32_16x16x32_bf16 a[120:123],a[240:243],v[228:231],a[120:123]
ds_read_b64_tr_b16 a[232:233],v3 offset:512
ds_read_b64_tr_b16 a[234:235],v3 offset:4608
v_mfma_f32_16x16x32_bf16 a[120:123],a[244:247],v[244:247],a[120:123]
ds_read_b64_tr_b16 a[240:241],v2 offset:768
ds_read_b64_tr_b16 a[242:243],v2 offset:4864
v_mfma_f32_16x16x32_bf16 a[124:127],a[252:255],v[244:247],a[124:127]
buffer_load_dwordx4 v5,s[24:27],s47 offen offset:3072 nt lds
v_mfma_f32_16x16x32_bf16 a[124:127],a[248:251],v[228:231],a[124:127]
s_add_u32 s47,s47,16384
ds_read_b64_tr_b16 a[244:245],v2 offset:8960
ds_read_b64_tr_b16 a[246:247],v2 offset:13056
v_mfma_f32_16x16x32_bf16 v[60:63],v[28:31],v[228:231],v[60:63]
s_and_b32 s47,s47,0x3ffff
ds_read_b64_tr_b16 a[252:253],v3 offset:8960
ds_read_b64_tr_b16 a[254:255],v3 offset:13056
v_mfma_f32_16x16x32_bf16 v[60:63],v[28:31],v[244:247],v[60:63]
s_or_b32 s47,s47,s46
ds_read_b64_tr_b16 a[248:249],v3 offset:768
ds_read_b64_tr_b16 a[250:251],v3 offset:4864
v_mfma_f32_16x16x32_bf16 v[196:199],a[128:131],v[144:147],v[40:43]
s_and_b32 s46,s54,0xfffc0000
v_mul_f32 v77,v20,v77
v_mul_f32 v14,v20,v14
v_mfma_f32_16x16x32_bf16 v[196:199],a[132:135],v[176:179],v[196:199]
v_mul_f32 v15,v20,v15
v_cvt_pk_bf16_f32 v66,v66,v67
v_mfma_f32_16x16x32_bf16 v[196:199],a[136:139],v[208:211],v[196:199]
v_cvt_pk_bf16_f32 v76,v76,v77
v_mul_f32 v78,v20,v78
v_mfma_f32_16x16x32_bf16 v[196:199],a[140:143],v[240:243],v[196:199]
v_mul_f32 v79,v20,v79
v_cvt_pk_bf16_f32 v67,v14,v15
v_mfma_f32_16x16x32_bf16 v[204:207],a[156:159],v[240:243],v[40:43]
v_cvt_pk_bf16_f32 v77,v78,v79
v_accvgpr_read_b32 v14,a30
v_mfma_f32_16x16x32_bf16 v[204:207],a[152:155],v[208:211],v[204:207]
v_accvgpr_read_b32 v15,a31
v_accvgpr_read_b32 v78,a28
v_mfma_f32_16x16x32_bf16 v[204:207],a[148:151],v[176:179],v[204:207]
v_accvgpr_read_b32 v79,a29
v_max3_f32 v44,v44,v45,v46
v_mfma_f32_16x16x32_bf16 v[204:207],a[144:147],v[144:147],v[204:207]
v_max3_f32 v44,v44,v47,s101
v_sub_f32 v47,0,v44
v_mfma_f32_16x16x32_bf16 v[212:215],a[160:163],v[144:147],v[40:43]
v_sub_f32 v46,0,v44
v_sub_f32 v45,0,v44
v_mfma_f32_16x16x32_bf16 v[212:215],a[164:167],v[176:179],v[212:215]
v_cmp_nlt_f32 s[56:57],v48,s63
v_sub_f32 v44,0,v44
v_mfma_f32_16x16x32_bf16 v[212:215],a[168:171],v[208:211],v[212:215]
s_cmp_eq_u32 s49,163840
s_cselect_b32 s49,0,s49
v_max_f32 v21,0x00800000,v52
v_mfma_f32_16x16x32_bf16 v[212:215],a[172:175],v[240:243],v[212:215]
v_exp_f32 v164,v164
v_mfma_f32_16x16x32_bf16 v[220:223],a[188:191],v[240:243],v[40:43]
v_exp_f32 v165,v165
v_mfma_f32_16x16x32_bf16 v[220:223],a[184:187],v[208:211],v[220:223]
v_exp_f32 v166,v166
v_mfma_f32_16x16x32_bf16 v[220:223],a[180:183],v[176:179],v[220:223]
v_exp_f32 v167,v167
v_mfma_f32_16x16x32_bf16 v[220:223],a[176:179],v[144:147],v[220:223]
s_waitcnt lgkmcnt(0)
v_cvt_pk_bf16_f32 v164,v164,v165
v_cvt_pk_bf16_f32 v165,v166,v167
v_mfma_f32_16x16x32_bf16 a[0:3],a[192:195],v[132:135],0
v_exp_f32 v172,v172
v_mfma_f32_16x16x32_bf16 a[0:3],a[196:199],v[148:151],a[0:3]
v_exp_f32 v173,v173
v_mfma_f32_16x16x32_bf16 a[4:7],a[204:207],v[148:151],0
v_exp_f32 v174,v174
v_mfma_f32_16x16x32_bf16 a[4:7],a[200:203],v[132:135],a[4:7]
v_exp_f32 v175,v175
v_mfma_f32_16x16x32_bf16 a[8:11],a[208:211],v[132:135],0
s_memrealtime s[76:77]
v_exp_f32 v180,v180
v_mfma_f32_16x16x32_bf16 a[8:11],a[212:215],v[148:151],a[8:11]
v_exp_f32 v181,v181
v_mfma_f32_16x16x32_bf16 a[12:15],a[220:223],v[148:151],0
v_exp_f32 v182,v182
v_mfma_f32_16x16x32_bf16 a[12:15],a[216:219],v[132:135],a[12:15]
v_exp_f32 v183,v183
v_mfma_f32_16x16x32_bf16 a[16:19],a[224:227],v[132:135],0
v_cvt_pk_bf16_f32 v166,v172,v173
v_cvt_pk_bf16_f32 v167,v174,v175
v_mfma_f32_16x16x32_bf16 a[16:19],a[228:231],v[148:151],a[16:19]
v_cvt_pk_bf16_f32 v180,v180,v181
v_cvt_pk_bf16_f32 v181,v182,v183
v_mfma_f32_16x16x32_bf16 a[20:23],a[236:239],v[148:151],0
v_exp_f32 v188,v188
v_mfma_f32_16x16x32_bf16 a[20:23],a[232:235],v[132:135],a[20:23]
s_waitcnt lgkmcnt(0)
v_exp_f32 v189,v189
v_mfma_f32_16x16x32_bf16 a[24:27],a[240:243],v[132:135],0
s_sub_u32 s79,s78,s76
v_exp_f32 v190,v190
v_mfma_f32_16x16x32_bf16 a[24:27],a[244:247],v[148:151],a[24:27]
s_add_u32 s78,s76,s60
v_exp_f32 v191,v191
v_mfma_f32_16x16x32_bf16 a[28:31],a[252:255],v[148:151],0
s_cmp_gt_i32 s79,0
s_cbranch_scc1 L_ps4_%=
L_pr4_%=:
v_cvt_pk_bf16_f32 v182,v188,v189
v_mfma_f32_16x16x32_bf16 a[28:31],a[248:251],v[132:135],a[28:31]
v_cvt_pk_bf16_f32 v183,v190,v191
v_accvgpr_read_b32 v80,a32
v_mfma_f32_16x16x32_bf16 v[48:51],v[28:31],v[132:135],0
v_rcp_f32 v21,v21
v_mfma_f32_16x16x32_bf16 v[48:51],v[28:31],v[148:151],v[48:51]
v_accvgpr_read_b32 v81,a33
v_accvgpr_read_b32 v82,a34
v_mfma_f32_16x16x32_bf16 v[228:231],a[128:131],v[152:155],v[44:47]
s_waitcnt vmcnt(8)
v_accvgpr_read_b32 v83,a35
v_mul_f32 v70,v20,v70
v_mfma_f32_16x16x32_bf16 v[228:231],a[132:135],v[184:187],v[228:231]
s_barrier
s_add_u32 m0,s49,s51
v_mul_f32 v71,v20,v71
v_mfma_f32_16x16x32_bf16 v[228:231],a[136:139],v[216:219],v[228:231]
v_mul_f32 v80,v21,v80
v_mul_f32 v81,v21,v81
v_mfma_f32_16x16x32_bf16 v[228:231],a[140:143],v[248:251],v[228:231]
v_mul_f32 v16,v20,v16
v_mul_f32 v17,v20,v17
v_mfma_f32_16x16x32_bf16 v[236:239],a[156:159],v[248:251],v[44:47]
buffer_load_dwordx4 v4,s[20:23],s47 offen nt lds
v_mfma_f32_16x16x32_bf16 v[236:239],a[152:155],v[216:219],v[236:239]
v_cvt_pk_bf16_f32 v70,v70,v71
v_cvt_pk_bf16_f32 v80,v80,v81
v_mfma_f32_16x16x32_bf16 v[236:239],a[148:151],v[184:187],v[236:239]
v_mul_f32 v82,v21,v82
v_mul_f32 v83,v21,v83
v_mfma_f32_16x16x32_bf16 v[236:239],a[144:147],v[152:155],v[236:239]
v_cvt_pk_bf16_f32 v71,v16,v17
v_cvt_pk_bf16_f32 v81,v82,v83
v_mfma_f32_16x16x32_bf16 v[244:247],a[160:163],v[152:155],v[44:47]
v_accvgpr_read_b32 v16,a38
v_accvgpr_read_b32 v17,a39
v_mfma_f32_16x16x32_bf16 v[244:247],a[164:167],v[184:187],v[244:247]
v_accvgpr_read_b32 v82,a36
v_accvgpr_read_b32 v83,a37
v_mfma_f32_16x16x32_bf16 v[244:247],a[168:171],v[216:219],v[244:247]
v_accvgpr_read_b32 v84,a40
v_accvgpr_read_b32 v85,a41
v_mfma_f32_16x16x32_bf16 v[244:247],a[172:175],v[248:251],v[244:247]
v_accvgpr_read_b32 v86,a42
v_accvgpr_read_b32 v87,a43
v_mfma_f32_16x16x32_bf16 v[252:255],a[188:191],v[248:251],v[44:47]
v_mul_f32 v74,v20,v74
v_mul_f32 v75,v20,v75
v_mfma_f32_16x16x32_bf16 v[252:255],a[184:187],v[216:219],v[252:255]
v_mul_f32 v84,v21,v84
v_mul_f32 v85,v21,v85
v_mfma_f32_16x16x32_bf16 v[252:255],a[180:183],v[184:187],v[252:255]
v_mul_f32 v12,v20,v12
v_mul_f32 v13,v20,v13
v_mfma_f32_16x16x32_bf16 v[252:255],a[176:179],v[152:155],v[252:255]
v_cvt_pk_bf16_f32 v74,v74,v75
v_cvt_pk_bf16_f32 v84,v84,v85
v_mfma_f32_16x16x32_bf16 a[32:35],a[192:195],v[164:167],0
v_mul_f32 v86,v21,v86
v_mul_f32 v87,v21,v87
v_mfma_f32_16x16x32_bf16 a[32:35],a[196:199],v[180:183],a[32:35]
v_cvt_pk_bf16_f32 v75,v12,v13
v_cvt_pk_bf16_f32 v85,v86,v87
v_mfma_f32_16x16x32_bf16 a[36:39],a[204:207],v[180:183],0
v_accvgpr_read_b32 v12,a46
v_accvgpr_read_b32 v13,a47
v_mfma_f32_16x16x32_bf16 a[36:39],a[200:203],v[164:167],a[36:39]
v_accvgpr_read_b32 v86,a44
v_accvgpr_read_b32 v87,a45
v_mfma_f32_16x16x32_bf16 a[40:43],a[208:211],v[164:167],0
buffer_load_dwordx4 v4,s[20:23],s47 offen offset:1024 nt lds
v_mfma_f32_16x16x32_bf16 a[40:43],a[212:215],v[180:183],a[40:43]
v_accvgpr_read_b32 v88,a48
v_accvgpr_read_b32 v89,a49
v_mfma_f32_16x16x32_bf16 a[44:47],a[220:223],v[180:183],0
v_accvgpr_read_b32 v90,a50
v_accvgpr_read_b32 v91,a51
v_mfma_f32_16x16x32_bf16 a[44:47],a[216:219],v[164:167],a[44:47]
v_mul_f32 v78,v20,v78
v_mul_f32 v79,v20,v79
v_mfma_f32_16x16x32_bf16 a[48:51],a[224:227],v[164:167],0
v_mul_f32 v88,v21,v88
v_mul_f32 v89,v21,v89
v_mul_f32 v14,v20,v14
v_mul_f32 v15,v20,v15
v_cvt_pk_bf16_f32 v78,v78,v79
v_add_u32 v1,s48,v1
v_cvt_pk_bf16_f32 v88,v88,v89
v_mul_f32 v90,v21,v90
v_mul_f32 v91,v21,v91
v_mfma_f32_16x16x32_bf16 a[48:51],a[228:231],v[180:183],a[48:51]
ds_read_b128 a[128:131],v1
v_cvt_pk_bf16_f32 v79,v14,v15
v_cvt_pk_bf16_f32 v89,v90,v91
v_accvgpr_read_b32 v14,a54
v_accvgpr_read_b32 v15,a55
v_accvgpr_read_b32 v90,a52
v_accvgpr_read_b32 v91,a53
v_mfma_f32_16x16x32_bf16 a[52:55],a[236:239],v[180:183],0
ds_read_b128 a[132:135],v1 offset:256
v_accvgpr_read_b32 v92,a56
v_mfma_f32_16x16x32_bf16 a[52:55],a[232:235],v[164:167],a[52:55]
ds_read_b128 a[136:139],v1 offset:512
v_accvgpr_read_b32 v93,a57
v_accvgpr_read_b32 v94,a58
v_accvgpr_read_b32 v95,a59
v_mfma_f32_16x16x32_bf16 a[56:59],a[240:243],v[164:167],0
ds_read_b128 a[140:143],v1 offset:768
v_mul_f32 v82,v21,v82
v_mul_f32 v83,v21,v83
v_mul_f32 v92,v21,v92
v_mul_f32 v93,v21,v93
v_mul_f32 v16,v21,v16
v_mul_f32 v17,v21,v17
v_cvt_pk_bf16_f32 v82,v82,v83
v_cvt_pk_bf16_f32 v92,v92,v93
v_mul_f32 v94,v21,v94
v_mul_f32 v95,v21,v95
v_mfma_f32_16x16x32_bf16 a[56:59],a[244:247],v[180:183],a[56:59]
ds_read_b128 a[156:159],v1 offset:4864
v_cvt_pk_bf16_f32 v83,v16,v17
v_cvt_pk_bf16_f32 v93,v94,v95
v_accvgpr_read_b32 v16,a62
v_accvgpr_read_b32 v17,a63
v_accvgpr_read_b32 v94,a60
v_accvgpr_read_b32 v95,a61
v_mfma_f32_16x16x32_bf16 a[60:63],a[252:255],v[180:183],0
ds_read_b128 a[152:155],v1 offset:4608
v_max_f32 v22,0x00800000,v56
v_mfma_f32_16x16x32_bf16 a[60:63],a[248:251],v[164:167],a[60:63]
ds_read_b128 a[148:151],v1 offset:4352
v_cmp_nlt_f32 s[58:59],v52,s63
s_nop 1
s_or_b64 s[56:57],s[56:57],s[58:59]
v_mfma_f32_16x16x32_bf16 v[52:55],v[28:31],v[164:167],0
ds_read_b128 a[144:147],v1 offset:4096
v_accvgpr_read_b32 v96,a64
v_mfma_f32_16x16x32_bf16 v[52:55],v[28:31],v[180:183],v[52:55]
s_waitcnt lgkmcnt(7)
ds_read_b128 a[160:163],v1 offset:8192
v_accvgpr_read_b32 v97,a65
v_mfma_f32_16x16x32_bf16 v[132:135],a[128:131],v[128:131],v[32:35]
s_waitcnt lgkmcnt(7)
ds_read_b128 a[164:167],v1 offset:8448
v_accvgpr_read_b32 v98,a66
v_mfma_f32_16x16x32_bf16 v[132:135],a[132:135],v[160:163],v[132:135]
s_waitcnt lgkmcnt(7)
buffer_load_dwordx4 v5,s[20:23],s47 offen offset:2048 nt lds
ds_read_b128 a[168:171],v1 offset:8704
v_mfma_f32_16x16x32_bf16 v[132:135],a[136:139],v[192:195],v[132:135]
s_waitcnt lgkmcnt(7)
ds_read_b128 a[172:175],v1 offset:8960
v_accvgpr_read_b32 v99,a67
v_mfma_f32_16x16x32_bf16 v[132:135],a[140:143],v[224:227],v[132:135]
s_waitcnt lgkmcnt(7)
ds_read_b128 a[188:191],v1 offset:13056
v_mul_f32 v86,v21,v86
v_mfma_f32_16x16x32_bf16 v[140:143],a[156:159],v[224:227],v[32:35]
s_waitcnt lgkmcnt(7)
ds_read_b128 a[184:187],v1 offset:12800
v_mul_f32 v87,v21,v87
v_mfma_f32_16x16x32_bf16 v[140:143],a[152:155],v[192:195],v[140:143]
s_waitcnt lgkmcnt(7)
ds_read_b128 a[180:183],v1 offset:12544
v_mul_f32 v12,v21,v12
v_mfma_f32_16x16x32_bf16 v[140:143],a[148:151],v[160:163],v[140:143]
s_waitcnt lgkmcnt(7)
ds_read_b128 a[176:179],v1 offset:12288
v_mul_f32 v13,v21,v13
v_mfma_f32_16x16x32_bf16 v[140:143],a[144:147],v[128:131],v[140:143]
)A"
R"A(s_waitcnt lgkmcnt(7)
v_exp_f32 v196,v196
v_mfma_f32_16x16x32_bf16 v[148:151],a[160:163],v[128:131],v[32:35]
s_waitcnt lgkmcnt(6)
v_exp_f32 v197,v197
v_mfma_f32_16x16x32_bf16 v[148:151],a[164:167],v[160:163],v[148:151]
s_waitcnt lgkmcnt(5)
v_exp_f32 v198,v198
v_mfma_f32_16x16x32_bf16 v[148:151],a[168:171],v[192:195],v[148:151]
s_waitcnt lgkmcnt(4)
v_exp_f32 v199,v199
v_mfma_f32_16x16x32_bf16 v[148:151],a[172:175],v[224:227],v[148:151]
s_waitcnt lgkmcnt(3)
v_cvt_pk_bf16_f32 v196,v196,v197
v_cvt_pk_bf16_f32 v197,v198,v199
v_mfma_f32_16x16x32_bf16 v[156:159],a[188:191],v[224:227],v[32:35]
s_waitcnt lgkmcnt(2)
v_exp_f32 v204,v204
v_exp_f32 v205,v205
v_exp_f32 v206,v206
v_exp_f32 v207,v207
v_exp_f32 v212,v212
v_exp_f32 v213,v213
v_mfma_f32_16x16x32_bf16 v[156:159],a[184:187],v[192:195],v[156:159]
s_waitcnt lgkmcnt(1)
v_exp_f32 v214,v214
v_exp_f32 v215,v215
v_cvt_pk_bf16_f32 v198,v204,v205
v_cvt_pk_bf16_f32 v199,v206,v207
v_cvt_pk_bf16_f32 v212,v212,v213
v_exp_f32 v220,v220
v_exp_f32 v221,v221
v_exp_f32 v222,v222
v_exp_f32 v223,v223
v_mfma_f32_16x16x32_bf16 v[156:159],a[180:183],v[160:163],v[156:159]
s_waitcnt lgkmcnt(0)
v_cvt_pk_bf16_f32 v213,v214,v215
v_cvt_pk_bf16_f32 v214,v220,v221
v_cvt_pk_bf16_f32 v215,v222,v223
v_mfma_f32_16x16x32_bf16 v[156:159],a[176:179],v[128:131],v[156:159]
buffer_load_dwordx4 v5,s[20:23],s47 offen offset:3072 nt lds
v_rcp_f32 v22,v22
v_mfma_f32_16x16x32_bf16 a[64:67],a[192:195],v[196:199],0
v_mul_f32 v96,v22,v96
v_mul_f32 v97,v22,v97
v_cvt_pk_bf16_f32 v86,v86,v87
v_cvt_pk_bf16_f32 v96,v96,v97
v_mul_f32 v98,v22,v98
v_mul_f32 v99,v22,v99
v_mfma_f32_16x16x32_bf16 a[64:67],a[196:199],v[212:215],a[64:67]
v_cvt_pk_bf16_f32 v87,v12,v13
v_cvt_pk_bf16_f32 v97,v98,v99
v_accvgpr_read_b32 v12,a70
v_accvgpr_read_b32 v13,a71
v_accvgpr_read_b32 v98,a68
v_accvgpr_read_b32 v99,a69
v_mfma_f32_16x16x32_bf16 a[68:71],a[204:207],v[212:215],0
v_accvgpr_read_b32 v100,a72
v_accvgpr_read_b32 v101,a73
v_mfma_f32_16x16x32_bf16 a[68:71],a[200:203],v[196:199],a[68:71]
v_accvgpr_read_b32 v102,a74
v_accvgpr_read_b32 v103,a75
v_mfma_f32_16x16x32_bf16 a[72:75],a[208:211],v[196:199],0
v_mul_f32 v90,v21,v90
v_mul_f32 v91,v21,v91
v_mul_f32 v100,v22,v100
v_mul_f32 v101,v22,v101
v_mul_f32 v14,v21,v14
v_mul_f32 v15,v21,v15
v_cvt_pk_bf16_f32 v90,v90,v91
v_cvt_pk_bf16_f32 v100,v100,v101
v_mul_f32 v102,v22,v102
v_mul_f32 v103,v22,v103
v_mfma_f32_16x16x32_bf16 a[72:75],a[212:215],v[212:215],a[72:75]
v_cvt_pk_bf16_f32 v91,v14,v15
v_cvt_pk_bf16_f32 v101,v102,v103
v_accvgpr_read_b32 v14,a78
v_accvgpr_read_b32 v15,a79
v_accvgpr_read_b32 v102,a76
v_accvgpr_read_b32 v103,a77
v_mfma_f32_16x16x32_bf16 a[76:79],a[220:223],v[212:215],0
v_accvgpr_read_b32 v104,a80
v_accvgpr_read_b32 v105,a81
v_mfma_f32_16x16x32_bf16 a[76:79],a[216:219],v[196:199],a[76:79]
s_add_u32 m0,s49,s52
v_accvgpr_read_b32 v106,a82
v_accvgpr_read_b32 v107,a83
v_mfma_f32_16x16x32_bf16 a[80:83],a[224:227],v[196:199],0
v_mul_f32 v94,v21,v94
v_mul_f32 v95,v21,v95
v_mul_f32 v104,v22,v104
v_mul_f32 v105,v22,v105
v_mul_f32 v16,v21,v16
v_mul_f32 v17,v21,v17
v_cvt_pk_bf16_f32 v94,v94,v95
v_cvt_pk_bf16_f32 v104,v104,v105
v_mul_f32 v106,v22,v106
v_mul_f32 v107,v22,v107
v_mfma_f32_16x16x32_bf16 a[80:83],a[228:231],v[212:215],a[80:83]
v_cvt_pk_bf16_f32 v95,v16,v17
v_cvt_pk_bf16_f32 v105,v106,v107
v_accvgpr_read_b32 v16,a86
v_accvgpr_read_b32 v17,a87
v_accvgpr_read_b32 v106,a84
v_accvgpr_read_b32 v107,a85
v_mfma_f32_16x16x32_bf16 a[84:87],a[236:239],v[212:215],0
s_add_u32 s49,s49,32768
v_accvgpr_read_b32 v108,a88
v_accvgpr_read_b32 v109,a89
v_mfma_f32_16x16x32_bf16 a[84:87],a[232:235],v[196:199],a[84:87]
v_accvgpr_read_b32 v110,a90
v_accvgpr_read_b32 v111,a91
v_mfma_f32_16x16x32_bf16 a[88:91],a[240:243],v[196:199],0
v_mul_f32 v98,v22,v98
v_mul_f32 v99,v22,v99
v_mul_f32 v108,v22,v108
v_mul_f32 v109,v22,v109
v_mul_f32 v12,v22,v12
v_mul_f32 v13,v22,v13
v_cvt_pk_bf16_f32 v98,v98,v99
v_cvt_pk_bf16_f32 v108,v108,v109
v_mul_f32 v110,v22,v110
v_mul_f32 v111,v22,v111
v_mfma_f32_16x16x32_bf16 a[88:91],a[244:247],v[212:215],a[88:91]
buffer_load_dwordx4 v4,s[24:27],s47 offen nt lds
v_cvt_pk_bf16_f32 v99,v12,v13
v_cvt_pk_bf16_f32 v109,v110,v111
v_accvgpr_read_b32 v12,a94
v_accvgpr_read_b32 v13,a95
v_accvgpr_read_b32 v110,a92
v_accvgpr_read_b32 v111,a93
v_mfma_f32_16x16x32_bf16 a[92:95],a[252:255],v[212:215],0
v_cmp_nlt_f32 s[58:59],v56,s63
s_nop 1
s_or_b64 s[56:57],s[56:57],s[58:59]
v_mfma_f32_16x16x32_bf16 a[92:95],a[248:251],v[196:199],a[92:95]
v_exp_f32 v228,v228
v_mfma_f32_16x16x32_bf16 v[56:59],v[28:31],v[196:199],0
v_exp_f32 v229,v229
v_mfma_f32_16x16x32_bf16 v[56:59],v[28:31],v[212:215],v[56:59]
v_exp_f32 v230,v230
v_mfma_f32_16x16x32_bf16 v[164:167],a[128:131],v[136:139],v[36:39]
v_exp_f32 v231,v231
v_mfma_f32_16x16x32_bf16 v[164:167],a[132:135],v[168:171],v[164:167]
v_cvt_pk_bf16_f32 v228,v228,v229
v_cvt_pk_bf16_f32 v229,v230,v231
v_mfma_f32_16x16x32_bf16 v[164:167],a[136:139],v[200:203],v[164:167]
v_exp_f32 v236,v236
v_mfma_f32_16x16x32_bf16 v[164:167],a[140:143],v[232:235],v[164:167]
v_exp_f32 v237,v237
v_mfma_f32_16x16x32_bf16 v[172:175],a[156:159],v[232:235],v[36:39]
v_exp_f32 v238,v238
v_mfma_f32_16x16x32_bf16 v[172:175],a[152:155],v[200:203],v[172:175]
v_exp_f32 v239,v239
v_mfma_f32_16x16x32_bf16 v[172:175],a[148:151],v[168:171],v[172:175]
buffer_load_dwordx4 v5,s[24:27],s47 offen offset:1024 nt lds
v_mfma_f32_16x16x32_bf16 v[172:175],a[144:147],v[136:139],v[172:175]
v_exp_f32 v244,v244
v_mfma_f32_16x16x32_bf16 v[180:183],a[160:163],v[136:139],v[36:39]
v_exp_f32 v245,v245
v_mfma_f32_16x16x32_bf16 v[180:183],a[164:167],v[168:171],v[180:183]
v_exp_f32 v246,v246
v_mfma_f32_16x16x32_bf16 v[180:183],a[168:171],v[200:203],v[180:183]
v_exp_f32 v247,v247
v_mfma_f32_16x16x32_bf16 v[180:183],a[172:175],v[232:235],v[180:183]
v_cvt_pk_bf16_f32 v230,v236,v237
v_cvt_pk_bf16_f32 v231,v238,v239
v_mfma_f32_16x16x32_bf16 v[188:191],a[188:191],v[232:235],v[36:39]
v_cvt_pk_bf16_f32 v244,v244,v245
v_max_f32 v23,0x00800000,v60
v_mfma_f32_16x16x32_bf16 v[188:191],a[184:187],v[200:203],v[188:191]
v_exp_f32 v252,v252
v_exp_f32 v253,v253
v_exp_f32 v254,v254
v_exp_f32 v255,v255
v_mfma_f32_16x16x32_bf16 v[188:191],a[180:183],v[168:171],v[188:191]
v_cvt_pk_bf16_f32 v245,v246,v247
v_cvt_pk_bf16_f32 v246,v252,v253
v_cvt_pk_bf16_f32 v247,v254,v255
v_mfma_f32_16x16x32_bf16 v[188:191],a[176:179],v[136:139],v[188:191]
v_rcp_f32 v23,v23
v_accvgpr_read_b32 v112,a96
v_accvgpr_read_b32 v113,a97
v_accvgpr_read_b32 v114,a98
v_accvgpr_read_b32 v115,a99
v_mfma_f32_16x16x32_bf16 a[96:99],a[192:195],v[228:231],0
v_mul_f32 v102,v22,v102
v_mul_f32 v103,v22,v103
v_mul_f32 v112,v23,v112
v_mul_f32 v113,v23,v113
v_mul_f32 v14,v22,v14
v_mul_f32 v15,v22,v15
v_cvt_pk_bf16_f32 v102,v102,v103
v_cvt_pk_bf16_f32 v112,v112,v113
v_mul_f32 v114,v23,v114
v_mul_f32 v115,v23,v115
v_mfma_f32_16x16x32_bf16 a[96:99],a[196:199],v[244:247],a[96:99]
v_cvt_pk_bf16_f32 v103,v14,v15
v_cvt_pk_bf16_f32 v113,v114,v115
v_accvgpr_read_b32 v14,a102
v_accvgpr_read_b32 v15,a103
v_accvgpr_read_b32 v114,a100
v_accvgpr_read_b32 v115,a101
v_mfma_f32_16x16x32_bf16 a[100:103],a[204:207],v[244:247],0
v_accvgpr_read_b32 v116,a104
v_accvgpr_read_b32 v117,a105
v_mfma_f32_16x16x32_bf16 a[100:103],a[200:203],v[228:231],a[100:103]
v_accvgpr_read_b32 v118,a106
v_accvgpr_read_b32 v119,a107
v_mfma_f32_16x16x32_bf16 a[104:107],a[208:211],v[228:231],0
v_mul_f32 v106,v22,v106
v_mul_f32 v107,v22,v107
v_mul_f32 v116,v23,v116
v_mul_f32 v117,v23,v117
v_mul_f32 v16,v22,v16
v_mul_f32 v17,v22,v17
v_cvt_pk_bf16_f32 v106,v106,v107
v_cvt_pk_bf16_f32 v116,v116,v117
v_mul_f32 v118,v23,v118
v_mul_f32 v119,v23,v119
v_mfma_f32_16x16x32_bf16 a[104:107],a[212:215],v[244:247],a[104:107]
v_cvt_pk_bf16_f32 v107,v16,v17
v_cvt_pk_bf16_f32 v117,v118,v119
v_accvgpr_read_b32 v16,a110
v_accvgpr_read_b32 v17,a111
v_accvgpr_read_b32 v118,a108
v_accvgpr_read_b32 v119,a109
v_mfma_f32_16x16x32_bf16 a[108:111],a[220:223],v[244:247],0
v_accvgpr_read_b32 v120,a112
v_accvgpr_read_b32 v121,a113
v_mfma_f32_16x16x32_bf16 a[108:111],a[216:219],v[228:231],a[108:111]
buffer_load_dwordx4 v4,s[24:27],s47 offen offset:2048 nt lds
v_accvgpr_read_b32 v122,a114
v_accvgpr_read_b32 v123,a115
v_mfma_f32_16x16x32_bf16 a[112:115],a[224:227],v[228:231],0
v_mul_f32 v110,v22,v110
v_mul_f32 v111,v22,v111
v_mul_f32 v120,v23,v120
v_mul_f32 v121,v23,v121
v_mul_f32 v12,v22,v12
v_mul_f32 v13,v22,v13
v_cvt_pk_bf16_f32 v110,v110,v111
v_cvt_pk_bf16_f32 v120,v120,v121
v_mul_f32 v122,v23,v122
v_mul_f32 v123,v23,v123
v_mfma_f32_16x16x32_bf16 a[112:115],a[228:231],v[244:247],a[112:115]
v_cvt_pk_bf16_f32 v111,v12,v13
v_cvt_pk_bf16_f32 v121,v122,v123
v_accvgpr_read_b32 v12,a118
v_accvgpr_read_b32 v13,a119
v_accvgpr_read_b32 v122,a116
v_accvgpr_read_b32 v123,a117
v_mfma_f32_16x16x32_bf16 a[116:119],a[236:239],v[244:247],0
v_accvgpr_read_b32 v124,a120
v_accvgpr_read_b32 v125,a121
v_mfma_f32_16x16x32_bf16 a[116:119],a[232:235],v[228:231],a[116:119]
v_accvgpr_read_b32 v126,a122
v_accvgpr_read_b32 v127,a123
v_mfma_f32_16x16x32_bf16 a[120:123],a[240:243],v[228:231],0
v_mul_f32 v114,v23,v114
v_mul_f32 v115,v23,v115
v_mul_f32 v124,v23,v124
v_mul_f32 v125,v23,v125
v_mul_f32 v14,v23,v14
v_mul_f32 v15,v23,v15
v_cvt_pk_bf16_f32 v114,v114,v115
v_cvt_pk_bf16_f32 v124,v124,v125
v_mul_f32 v126,v23,v126
v_mul_f32 v127,v23,v127
v_mfma_f32_16x16x32_bf16 a[120:123],a[244:247],v[244:247],a[120:123]
v_cvt_pk_bf16_f32 v115,v14,v15
v_cvt_pk_bf16_f32 v125,v126,v127
v_accvgpr_read_b32 v14,a126
v_accvgpr_read_b32 v15,a127
v_accvgpr_read_b32 v126,a124
v_accvgpr_read_b32 v127,a125
v_mfma_f32_16x16x32_bf16 a[124:127],a[252:255],v[244:247],0
v_cmp_nlt_f32 s[58:59],v60,s63
s_nop 1
s_or_b64 s[56:57],s[56:57],s[58:59]
v_mfma_f32_16x16x32_bf16 a[124:127],a[248:251],v[228:231],a[124:127]
v_add_u32 v2,s48,v2
v_add_u32 v3,s48,v3
v_mfma_f32_16x16x32_bf16 v[60:63],v[28:31],v[228:231],0
ds_read_b64_tr_b16 a[192:193],v2
ds_read_b64_tr_b16 a[194:195],v2 offset:4096
v_mfma_f32_16x16x32_bf16 v[60:63],v[28:31],v[244:247],v[60:63]
ds_read_b64_tr_b16 a[196:197],v2 offset:8192
ds_read_b64_tr_b16 a[198:199],v2 offset:12288
v_mfma_f32_16x16x32_bf16 v[196:199],a[128:131],v[144:147],v[40:43]
buffer_load_dwordx4 v5,s[24:27],s47 offen offset:3072 nt lds
v_mfma_f32_16x16x32_bf16 v[196:199],a[132:135],v[176:179],v[196:199]
s_add_u32 s47,s47,16384
ds_read_b64_tr_b16 a[204:205],v3 offset:8192
ds_read_b64_tr_b16 a[206:207],v3 offset:12288
v_mfma_f32_16x16x32_bf16 v[196:199],a[136:139],v[208:211],v[196:199]
s_and_b32 s47,s47,0x3ffff
ds_read_b64_tr_b16 a[200:201],v3
ds_read_b64_tr_b16 a[202:203],v3 offset:4096
v_mfma_f32_16x16x32_bf16 v[196:199],a[140:143],v[240:243],v[196:199]
s_or_b32 s47,s47,s46
v_exp_f32 v132,v132
v_mfma_f32_16x16x32_bf16 v[204:207],a[156:159],v[240:243],v[40:43]
v_exp_f32 v133,v133
v_mfma_f32_16x16x32_bf16 v[204:207],a[152:155],v[208:211],v[204:207]
ds_read_b64_tr_b16 a[208:209],v2 offset:256
ds_read_b64_tr_b16 a[210:211],v2 offset:4352
v_mfma_f32_16x16x32_bf16 v[204:207],a[148:151],v[176:179],v[204:207]
v_exp_f32 v134,v134
v_mfma_f32_16x16x32_bf16 v[204:207],a[144:147],v[144:147],v[204:207]
v_exp_f32 v135,v135
v_mfma_f32_16x16x32_bf16 v[212:215],a[160:163],v[144:147],v[40:43]
ds_read_b64_tr_b16 a[212:213],v2 offset:8448
v_cvt_pk_bf16_f32 v132,v132,v133
v_mfma_f32_16x16x32_bf16 v[212:215],a[164:167],v[176:179],v[212:215]
v_exp_f32 v140,v140
v_mfma_f32_16x16x32_bf16 v[212:215],a[168:171],v[208:211],v[212:215]
v_exp_f32 v141,v141
v_mfma_f32_16x16x32_bf16 v[212:215],a[172:175],v[240:243],v[212:215]
v_exp_f32 v142,v142
v_mfma_f32_16x16x32_bf16 v[220:223],a[188:191],v[240:243],v[40:43]
ds_read_b64_tr_b16 a[214:215],v2 offset:12544
v_exp_f32 v143,v143
v_exp_f32 v148,v148
v_exp_f32 v149,v149
v_mfma_f32_16x16x32_bf16 v[220:223],a[184:187],v[208:211],v[220:223]
ds_read_b64_tr_b16 a[220:221],v3 offset:8448
ds_read_b64_tr_b16 a[222:223],v3 offset:12544
v_cvt_pk_bf16_f32 v133,v134,v135
v_exp_f32 v150,v150
v_exp_f32 v151,v151
v_cvt_pk_bf16_f32 v134,v140,v141
v_cvt_pk_bf16_f32 v135,v142,v143
v_cvt_pk_bf16_f32 v148,v148,v149
v_exp_f32 v156,v156
v_exp_f32 v157,v157
)A"
R"A(v_exp_f32 v158,v158
v_exp_f32 v159,v159
v_mfma_f32_16x16x32_bf16 v[220:223],a[180:183],v[176:179],v[220:223]
ds_read_b64_tr_b16 a[216:217],v3 offset:256
ds_read_b64_tr_b16 a[218:219],v3 offset:4352
v_cvt_pk_bf16_f32 v149,v150,v151
v_cvt_pk_bf16_f32 v150,v156,v157
v_cvt_pk_bf16_f32 v151,v158,v159
v_mfma_f32_16x16x32_bf16 v[220:223],a[176:179],v[144:147],v[220:223]
s_waitcnt lgkmcnt(6)
ds_read_b64_tr_b16 a[224:225],v2 offset:512
ds_read_b64_tr_b16 a[226:227],v2 offset:4608
v_mfma_f32_16x16x32_bf16 a[0:3],a[192:195],v[132:135],a[0:3]
ds_read_b64_tr_b16 a[228:229],v2 offset:8704
ds_read_b64_tr_b16 a[230:231],v2 offset:12800
v_mfma_f32_16x16x32_bf16 a[0:3],a[196:199],v[148:151],a[0:3]
ds_read_b64_tr_b16 a[236:237],v3 offset:8704
ds_read_b64_tr_b16 a[238:239],v3 offset:12800
v_mfma_f32_16x16x32_bf16 a[4:7],a[204:207],v[148:151],a[4:7]
ds_read_b64_tr_b16 a[232:233],v3 offset:512
ds_read_b64_tr_b16 a[234:235],v3 offset:4608
v_mfma_f32_16x16x32_bf16 a[4:7],a[200:203],v[132:135],a[4:7]
ds_read_b64_tr_b16 a[240:241],v2 offset:768
ds_read_b64_tr_b16 a[242:243],v2 offset:4864
v_mfma_f32_16x16x32_bf16 a[8:11],a[208:211],v[132:135],a[8:11]
s_memrealtime s[76:77]
ds_read_b64_tr_b16 a[244:245],v2 offset:8960
ds_read_b64_tr_b16 a[246:247],v2 offset:13056
s_waitcnt lgkmcnt(15)
v_mfma_f32_16x16x32_bf16 a[8:11],a[212:215],v[148:151],a[8:11]
s_waitcnt lgkmcnt(14)
ds_read_b64_tr_b16 a[252:253],v3 offset:8960
ds_read_b64_tr_b16 a[254:255],v3 offset:13056
v_mfma_f32_16x16x32_bf16 a[12:15],a[220:223],v[148:151],a[12:15]
s_waitcnt lgkmcnt(14)
ds_read_b64_tr_b16 a[248:249],v3 offset:768
ds_read_b64_tr_b16 a[250:251],v3 offset:4864
v_mfma_f32_16x16x32_bf16 a[12:15],a[216:219],v[132:135],a[12:15]
s_waitcnt lgkmcnt(14)
v_mul_f32 v118,v23,v118
v_mul_f32 v119,v23,v119
v_mfma_f32_16x16x32_bf16 a[16:19],a[224:227],v[132:135],a[16:19]
s_waitcnt lgkmcnt(12)
v_mul_f32 v122,v23,v122
v_mul_f32 v123,v23,v123
v_mfma_f32_16x16x32_bf16 a[16:19],a[228:231],v[148:151],a[16:19]
s_waitcnt lgkmcnt(10)
v_mul_f32 v126,v23,v126
v_mul_f32 v127,v23,v127
v_mfma_f32_16x16x32_bf16 a[20:23],a[236:239],v[148:151],a[20:23]
s_waitcnt lgkmcnt(8)
v_exp_f32 v164,v164
v_mfma_f32_16x16x32_bf16 a[20:23],a[232:235],v[132:135],a[20:23]
s_waitcnt lgkmcnt(0)
v_exp_f32 v165,v165
v_mfma_f32_16x16x32_bf16 a[24:27],a[240:243],v[132:135],a[24:27]
s_sub_u32 s79,s78,s76
v_exp_f32 v180,v180
v_mfma_f32_16x16x32_bf16 a[24:27],a[244:247],v[148:151],a[24:27]
s_add_u32 s78,s76,s60
v_exp_f32 v181,v181
v_mfma_f32_16x16x32_bf16 a[28:31],a[252:255],v[148:151],a[28:31]
s_cmp_gt_i32 s79,0
s_cbranch_scc1 L_ps5_%=
L_pr5_%=:
v_mul_f32 v16,v23,v16
v_mfma_f32_16x16x32_bf16 a[28:31],a[248:251],v[132:135],a[28:31]
s_cmp_eq_u32 s50,131072
s_cselect_b32 s62,163840,0
v_mul_f32 v17,v23,v17
v_cvt_pk_bf16_f32 v118,v118,v119
v_mul_f32 v12,v23,v12
v_mul_f32 v13,v23,v13
v_cvt_pk_bf16_f32 v122,v122,v123
v_mul_f32 v14,v23,v14
v_mul_f32 v15,v23,v15
v_cvt_pk_bf16_f32 v126,v126,v127
v_exp_f32 v166,v166
v_exp_f32 v167,v167
v_exp_f32 v182,v182
v_exp_f32 v183,v183
v_cvt_pk_bf16_f32 v119,v16,v17
v_cvt_pk_bf16_f32 v123,v12,v13
v_cvt_pk_bf16_f32 v127,v14,v15
v_cvt_pk_bf16_f32 v164,v164,v165
v_exp_f32 v172,v172
v_exp_f32 v173,v173
v_exp_f32 v174,v174
v_exp_f32 v175,v175
v_cvt_pk_bf16_f32 v180,v180,v181
v_exp_f32 v188,v188
v_exp_f32 v189,v189
v_exp_f32 v190,v190
v_exp_f32 v191,v191
v_mfma_f32_16x16x32_bf16 v[48:51],v[28:31],v[132:135],v[48:51]
s_sub_u32 s48,32768,s62
s_cmp_lg_u64 s[56:57],0
s_cbranch_scc1 L_slow0_%=
L_sret0_%=:
s_cmp_eq_u32 s49,163840
s_cselect_b32 s49,0,s49
s_add_u32 s50,s50,s48
v_cvt_pk_bf16_f32 v165,v166,v167
v_cvt_pk_bf16_f32 v181,v182,v183
v_add_u32 v1,s48,v1
v_cvt_pk_bf16_f32 v166,v172,v173
v_cvt_pk_bf16_f32 v167,v174,v175
v_cvt_pk_bf16_f32 v182,v188,v189
v_cvt_pk_bf16_f32 v183,v190,v191
v_mfma_f32_16x16x32_bf16 v[48:51],v[28:31],v[148:151],v[48:51]
s_mov_b32 s53,11
L_att1_%=:
v_mfma_f32_16x16x32_bf16 v[228:231],a[128:131],v[152:155],v[44:47]
s_waitcnt vmcnt(8) lgkmcnt(0)
v_exp_f32 v196,v196
v_mfma_f32_16x16x32_bf16 v[228:231],a[132:135],v[184:187],v[228:231]
s_barrier
s_add_u32 m0,s49,s51
ds_read_b128 a[128:131],v1
v_mfma_f32_16x16x32_bf16 v[228:231],a[136:139],v[216:219],v[228:231]
s_mov_b32 s90,L_fast_end_%=-L_pc_%=+128
v_exp_f32 v197,v197
v_mfma_f32_16x16x32_bf16 v[228:231],a[140:143],v[248:251],v[228:231]
s_add_u32 s54,s54,16384
ds_read_b128 a[132:135],v1 offset:256
v_cvt_pk_bf16_f32 v196,v196,v197
v_mfma_f32_16x16x32_bf16 v[236:239],a[156:159],v[248:251],v[44:47]
s_sub_u32 s53,s53,1
buffer_load_dwordx4 v4,s[20:23],s47 offen nt lds
v_mfma_f32_16x16x32_bf16 v[236:239],a[152:155],v[216:219],v[236:239]
s_and_b32 s46,s54,0xfffc0000
ds_read_b128 a[136:139],v1 offset:512
v_add_u32 v2,s48,v2
v_mfma_f32_16x16x32_bf16 v[236:239],a[148:151],v[184:187],v[236:239]
v_exp_f32 v198,v198
v_mfma_f32_16x16x32_bf16 v[236:239],a[144:147],v[152:155],v[236:239]
ds_read_b128 a[140:143],v1 offset:768
v_add_u32 v3,s48,v3
v_mfma_f32_16x16x32_bf16 v[244:247],a[160:163],v[152:155],v[44:47]
v_exp_f32 v199,v199
v_mfma_f32_16x16x32_bf16 v[244:247],a[164:167],v[184:187],v[244:247]
ds_read_b128 a[156:159],v1 offset:4864
v_cvt_pk_bf16_f32 v197,v198,v199
v_mfma_f32_16x16x32_bf16 v[244:247],a[168:171],v[216:219],v[244:247]
v_exp_f32 v204,v204
v_mfma_f32_16x16x32_bf16 v[244:247],a[172:175],v[248:251],v[244:247]
s_cmp_eq_u32 s50,131072
s_cselect_b32 s62,163840,0
ds_read_b128 a[152:155],v1 offset:4608
v_mfma_f32_16x16x32_bf16 v[252:255],a[188:191],v[248:251],v[44:47]
s_sub_u32 s48,32768,s62
v_exp_f32 v205,v205
v_mfma_f32_16x16x32_bf16 v[252:255],a[184:187],v[216:219],v[252:255]
s_add_u32 s50,s50,s48
ds_read_b128 a[148:151],v1 offset:4352
v_cvt_pk_bf16_f32 v198,v204,v205
v_mfma_f32_16x16x32_bf16 v[252:255],a[180:183],v[184:187],v[252:255]
v_exp_f32 v206,v206
v_mfma_f32_16x16x32_bf16 v[252:255],a[176:179],v[152:155],v[252:255]
ds_read_b128 a[144:147],v1 offset:4096
v_mfma_f32_16x16x32_bf16 a[32:35],a[192:195],v[164:167],a[32:35]
v_exp_f32 v207,v207
v_mfma_f32_16x16x32_bf16 a[32:35],a[196:199],v[180:183],a[32:35]
ds_read_b128 a[160:163],v1 offset:8192
v_cvt_pk_bf16_f32 v199,v206,v207
v_mfma_f32_16x16x32_bf16 a[36:39],a[204:207],v[180:183],a[36:39]
buffer_load_dwordx4 v4,s[20:23],s47 offen offset:1024 nt lds
v_mfma_f32_16x16x32_bf16 a[36:39],a[200:203],v[164:167],a[36:39]
ds_read_b128 a[164:167],v1 offset:8448
v_mfma_f32_16x16x32_bf16 a[40:43],a[208:211],v[164:167],a[40:43]
v_exp_f32 v212,v212
v_mfma_f32_16x16x32_bf16 a[40:43],a[212:215],v[180:183],a[40:43]
ds_read_b128 a[168:171],v1 offset:8704
v_mfma_f32_16x16x32_bf16 a[44:47],a[220:223],v[180:183],a[44:47]
v_exp_f32 v213,v213
v_mfma_f32_16x16x32_bf16 a[44:47],a[216:219],v[164:167],a[44:47]
ds_read_b128 a[172:175],v1 offset:8960
v_cvt_pk_bf16_f32 v212,v212,v213
v_mfma_f32_16x16x32_bf16 a[48:51],a[224:227],v[164:167],a[48:51]
v_exp_f32 v214,v214
v_mfma_f32_16x16x32_bf16 a[48:51],a[228:231],v[180:183],a[48:51]
ds_read_b128 a[188:191],v1 offset:13056
v_mfma_f32_16x16x32_bf16 a[52:55],a[236:239],v[180:183],a[52:55]
v_exp_f32 v215,v215
v_mfma_f32_16x16x32_bf16 a[52:55],a[232:235],v[164:167],a[52:55]
ds_read_b128 a[184:187],v1 offset:12800
v_cvt_pk_bf16_f32 v213,v214,v215
v_mfma_f32_16x16x32_bf16 a[56:59],a[240:243],v[164:167],a[56:59]
v_exp_f32 v220,v220
v_mfma_f32_16x16x32_bf16 a[56:59],a[244:247],v[180:183],a[56:59]
ds_read_b128 a[180:183],v1 offset:12544
v_mfma_f32_16x16x32_bf16 a[60:63],a[252:255],v[180:183],a[60:63]
v_exp_f32 v221,v221
v_mfma_f32_16x16x32_bf16 a[60:63],a[248:251],v[164:167],a[60:63]
ds_read_b128 a[176:179],v1 offset:12288
v_cvt_pk_bf16_f32 v214,v220,v221
v_mfma_f32_16x16x32_bf16 v[52:55],v[28:31],v[164:167],v[52:55]
buffer_load_dwordx4 v5,s[20:23],s47 offen offset:2048 nt lds
v_mfma_f32_16x16x32_bf16 v[52:55],v[28:31],v[180:183],v[52:55]
s_waitcnt lgkmcnt(3)
v_exp_f32 v222,v222
v_mfma_f32_16x16x32_bf16 v[132:135],a[128:131],v[128:131],v[32:35]
v_exp_f32 v223,v223
v_mfma_f32_16x16x32_bf16 v[132:135],a[132:135],v[160:163],v[132:135]
v_cvt_pk_bf16_f32 v215,v222,v223
v_add_u32 v1,s48,v1
v_mfma_f32_16x16x32_bf16 v[132:135],a[136:139],v[192:195],v[132:135]
v_exp_f32 v228,v228
v_mfma_f32_16x16x32_bf16 v[132:135],a[140:143],v[224:227],v[132:135]
v_exp_f32 v229,v229
v_mfma_f32_16x16x32_bf16 v[140:143],a[156:159],v[224:227],v[32:35]
v_exp_f32 v230,v230
v_mfma_f32_16x16x32_bf16 v[140:143],a[152:155],v[192:195],v[140:143]
v_exp_f32 v231,v231
v_mfma_f32_16x16x32_bf16 v[140:143],a[148:151],v[160:163],v[140:143]
v_cvt_pk_bf16_f32 v228,v228,v229
v_cvt_pk_bf16_f32 v229,v230,v231
v_mfma_f32_16x16x32_bf16 v[140:143],a[144:147],v[128:131],v[140:143]
v_exp_f32 v236,v236
v_mfma_f32_16x16x32_bf16 v[148:151],a[160:163],v[128:131],v[32:35]
v_exp_f32 v237,v237
v_mfma_f32_16x16x32_bf16 v[148:151],a[164:167],v[160:163],v[148:151]
v_exp_f32 v238,v238
v_mfma_f32_16x16x32_bf16 v[148:151],a[168:171],v[192:195],v[148:151]
v_exp_f32 v239,v239
v_mfma_f32_16x16x32_bf16 v[148:151],a[172:175],v[224:227],v[148:151]
v_exp_f32 v244,v244
v_mfma_f32_16x16x32_bf16 v[156:159],a[188:191],v[224:227],v[32:35]
s_waitcnt lgkmcnt(0)
buffer_load_dwordx4 v5,s[20:23],s47 offen offset:3072 nt lds
v_mfma_f32_16x16x32_bf16 v[156:159],a[184:187],v[192:195],v[156:159]
v_exp_f32 v245,v245
v_mfma_f32_16x16x32_bf16 v[156:159],a[180:183],v[160:163],v[156:159]
v_exp_f32 v246,v246
v_mfma_f32_16x16x32_bf16 v[156:159],a[176:179],v[128:131],v[156:159]
v_exp_f32 v247,v247
v_mfma_f32_16x16x32_bf16 a[64:67],a[192:195],v[196:199],a[64:67]
v_cvt_pk_bf16_f32 v230,v236,v237
v_cvt_pk_bf16_f32 v231,v238,v239
v_mfma_f32_16x16x32_bf16 a[64:67],a[196:199],v[212:215],a[64:67]
v_cvt_pk_bf16_f32 v244,v244,v245
v_cvt_pk_bf16_f32 v245,v246,v247
v_mfma_f32_16x16x32_bf16 a[68:71],a[204:207],v[212:215],a[68:71]
v_exp_f32 v252,v252
v_mfma_f32_16x16x32_bf16 a[68:71],a[200:203],v[196:199],a[68:71]
v_exp_f32 v253,v253
v_mfma_f32_16x16x32_bf16 a[72:75],a[208:211],v[196:199],a[72:75]
v_exp_f32 v254,v254
v_mfma_f32_16x16x32_bf16 a[72:75],a[212:215],v[212:215],a[72:75]
v_exp_f32 v255,v255
v_mfma_f32_16x16x32_bf16 a[76:79],a[220:223],v[212:215],a[76:79]
v_cvt_pk_bf16_f32 v246,v252,v253
v_cvt_pk_bf16_f32 v247,v254,v255
v_mfma_f32_16x16x32_bf16 a[76:79],a[216:219],v[196:199],a[76:79]
s_add_u32 m0,s49,s52
v_exp_f32 v132,v132
v_mfma_f32_16x16x32_bf16 a[80:83],a[224:227],v[196:199],a[80:83]
s_add_u32 s49,s49,32768
v_exp_f32 v133,v133
v_mfma_f32_16x16x32_bf16 a[80:83],a[228:231],v[212:215],a[80:83]
v_exp_f32 v134,v134
v_mfma_f32_16x16x32_bf16 a[84:87],a[236:239],v[212:215],a[84:87]
buffer_load_dwordx4 v4,s[24:27],s47 offen nt lds
v_mfma_f32_16x16x32_bf16 a[84:87],a[232:235],v[196:199],a[84:87]
v_exp_f32 v135,v135
v_mfma_f32_16x16x32_bf16 a[88:91],a[240:243],v[196:199],a[88:91]
v_cvt_pk_bf16_f32 v132,v132,v133
v_cvt_pk_bf16_f32 v133,v134,v135
v_mfma_f32_16x16x32_bf16 a[88:91],a[244:247],v[212:215],a[88:91]
v_exp_f32 v140,v140
v_mfma_f32_16x16x32_bf16 a[92:95],a[252:255],v[212:215],a[92:95]
v_exp_f32 v141,v141
v_mfma_f32_16x16x32_bf16 a[92:95],a[248:251],v[196:199],a[92:95]
v_exp_f32 v142,v142
v_mfma_f32_16x16x32_bf16 v[56:59],v[28:31],v[196:199],v[56:59]
v_exp_f32 v143,v143
v_mfma_f32_16x16x32_bf16 v[56:59],v[28:31],v[212:215],v[56:59]
v_exp_f32 v148,v148
v_mfma_f32_16x16x32_bf16 v[164:167],a[128:131],v[136:139],v[36:39]
v_exp_f32 v149,v149
v_mfma_f32_16x16x32_bf16 v[164:167],a[132:135],v[168:171],v[164:167]
v_exp_f32 v150,v150
v_mfma_f32_16x16x32_bf16 v[164:167],a[136:139],v[200:203],v[164:167]
v_exp_f32 v151,v151
v_mfma_f32_16x16x32_bf16 v[164:167],a[140:143],v[232:235],v[164:167]
v_cvt_pk_bf16_f32 v134,v140,v141
v_cvt_pk_bf16_f32 v135,v142,v143
v_mfma_f32_16x16x32_bf16 v[172:175],a[156:159],v[232:235],v[36:39]
v_cvt_pk_bf16_f32 v148,v148,v149
v_cvt_pk_bf16_f32 v149,v150,v151
v_mfma_f32_16x16x32_bf16 v[172:175],a[152:155],v[200:203],v[172:175]
v_exp_f32 v156,v156
v_mfma_f32_16x16x32_bf16 v[172:175],a[148:151],v[168:171],v[172:175]
buffer_load_dwordx4 v5,s[24:27],s47 offen offset:1024 nt lds
v_mfma_f32_16x16x32_bf16 v[172:175],a[144:147],v[136:139],v[172:175]
v_exp_f32 v157,v157
v_mfma_f32_16x16x32_bf16 v[180:183],a[160:163],v[136:139],v[36:39]
)A"
R"A(v_exp_f32 v158,v158
v_mfma_f32_16x16x32_bf16 v[180:183],a[164:167],v[168:171],v[180:183]
v_exp_f32 v159,v159
v_mfma_f32_16x16x32_bf16 v[180:183],a[168:171],v[200:203],v[180:183]
v_cvt_pk_bf16_f32 v150,v156,v157
v_cvt_pk_bf16_f32 v151,v158,v159
v_mfma_f32_16x16x32_bf16 v[180:183],a[172:175],v[232:235],v[180:183]
v_exp_f32 v164,v164
v_mfma_f32_16x16x32_bf16 v[188:191],a[188:191],v[232:235],v[36:39]
v_exp_f32 v165,v165
v_mfma_f32_16x16x32_bf16 v[188:191],a[184:187],v[200:203],v[188:191]
v_exp_f32 v166,v166
v_mfma_f32_16x16x32_bf16 v[188:191],a[180:183],v[168:171],v[188:191]
v_exp_f32 v167,v167
v_mfma_f32_16x16x32_bf16 v[188:191],a[176:179],v[136:139],v[188:191]
v_cvt_pk_bf16_f32 v164,v164,v165
v_cvt_pk_bf16_f32 v165,v166,v167
v_mfma_f32_16x16x32_bf16 a[96:99],a[192:195],v[228:231],a[96:99]
s_cmp_eq_u32 s49,163840
s_cselect_b32 s49,0,s49
ds_read_b64_tr_b16 a[192:193],v2
v_mfma_f32_16x16x32_bf16 a[96:99],a[196:199],v[244:247],a[96:99]
ds_read_b64_tr_b16 a[194:195],v2 offset:4096
v_mfma_f32_16x16x32_bf16 a[100:103],a[204:207],v[244:247],a[100:103]
ds_read_b64_tr_b16 a[196:197],v2 offset:8192
v_mfma_f32_16x16x32_bf16 a[100:103],a[200:203],v[228:231],a[100:103]
ds_read_b64_tr_b16 a[198:199],v2 offset:12288
v_mfma_f32_16x16x32_bf16 a[104:107],a[208:211],v[228:231],a[104:107]
buffer_load_dwordx4 v4,s[24:27],s47 offen offset:2048 nt lds
v_mfma_f32_16x16x32_bf16 a[104:107],a[212:215],v[244:247],a[104:107]
ds_read_b64_tr_b16 a[204:205],v3 offset:8192
v_mfma_f32_16x16x32_bf16 a[108:111],a[220:223],v[244:247],a[108:111]
ds_read_b64_tr_b16 a[206:207],v3 offset:12288
v_mfma_f32_16x16x32_bf16 a[108:111],a[216:219],v[228:231],a[108:111]
ds_read_b64_tr_b16 a[200:201],v3
v_mfma_f32_16x16x32_bf16 a[112:115],a[224:227],v[228:231],a[112:115]
ds_read_b64_tr_b16 a[202:203],v3 offset:4096
v_mfma_f32_16x16x32_bf16 a[112:115],a[228:231],v[244:247],a[112:115]
ds_read_b64_tr_b16 a[208:209],v2 offset:256
v_mfma_f32_16x16x32_bf16 a[116:119],a[236:239],v[244:247],a[116:119]
ds_read_b64_tr_b16 a[210:211],v2 offset:4352
v_mfma_f32_16x16x32_bf16 a[116:119],a[232:235],v[228:231],a[116:119]
ds_read_b64_tr_b16 a[212:213],v2 offset:8448
v_mfma_f32_16x16x32_bf16 a[120:123],a[240:243],v[228:231],a[120:123]
ds_read_b64_tr_b16 a[214:215],v2 offset:12544
v_mfma_f32_16x16x32_bf16 a[120:123],a[244:247],v[244:247],a[120:123]
ds_read_b64_tr_b16 a[220:221],v3 offset:8448
v_mfma_f32_16x16x32_bf16 a[124:127],a[252:255],v[244:247],a[124:127]
ds_read_b64_tr_b16 a[222:223],v3 offset:12544
v_mfma_f32_16x16x32_bf16 a[124:127],a[248:251],v[228:231],a[124:127]
ds_read_b64_tr_b16 a[216:217],v3 offset:256
v_mfma_f32_16x16x32_bf16 v[60:63],v[28:31],v[228:231],v[60:63]
ds_read_b64_tr_b16 a[218:219],v3 offset:4352
v_mfma_f32_16x16x32_bf16 v[60:63],v[28:31],v[244:247],v[60:63]
ds_read_b64_tr_b16 a[224:225],v2 offset:512
v_mfma_f32_16x16x32_bf16 v[196:199],a[128:131],v[144:147],v[40:43]
buffer_load_dwordx4 v5,s[24:27],s47 offen offset:3072 nt lds
v_mfma_f32_16x16x32_bf16 v[196:199],a[132:135],v[176:179],v[196:199]
s_add_u32 s47,s47,16384
s_and_b32 s47,s47,0x3ffff
ds_read_b64_tr_b16 a[226:227],v2 offset:4608
v_mfma_f32_16x16x32_bf16 v[196:199],a[136:139],v[208:211],v[196:199]
s_or_b32 s47,s47,s46
ds_read_b64_tr_b16 a[228:229],v2 offset:8704
v_mfma_f32_16x16x32_bf16 v[196:199],a[140:143],v[240:243],v[196:199]
ds_read_b64_tr_b16 a[230:231],v2 offset:12800
v_mfma_f32_16x16x32_bf16 v[204:207],a[156:159],v[240:243],v[40:43]
ds_read_b64_tr_b16 a[236:237],v3 offset:8704
v_mfma_f32_16x16x32_bf16 v[204:207],a[152:155],v[208:211],v[204:207]
ds_read_b64_tr_b16 a[238:239],v3 offset:12800
v_mfma_f32_16x16x32_bf16 v[204:207],a[148:151],v[176:179],v[204:207]
ds_read_b64_tr_b16 a[232:233],v3 offset:512
v_mfma_f32_16x16x32_bf16 v[204:207],a[144:147],v[144:147],v[204:207]
ds_read_b64_tr_b16 a[234:235],v3 offset:4608
v_mfma_f32_16x16x32_bf16 v[212:215],a[160:163],v[144:147],v[40:43]
ds_read_b64_tr_b16 a[240:241],v2 offset:768
v_mfma_f32_16x16x32_bf16 v[212:215],a[164:167],v[176:179],v[212:215]
ds_read_b64_tr_b16 a[242:243],v2 offset:4864
v_mfma_f32_16x16x32_bf16 v[212:215],a[168:171],v[208:211],v[212:215]
ds_read_b64_tr_b16 a[244:245],v2 offset:8960
v_mfma_f32_16x16x32_bf16 v[212:215],a[172:175],v[240:243],v[212:215]
ds_read_b64_tr_b16 a[246:247],v2 offset:13056
v_mfma_f32_16x16x32_bf16 v[220:223],a[188:191],v[240:243],v[40:43]
ds_read_b64_tr_b16 a[252:253],v3 offset:8960
v_mfma_f32_16x16x32_bf16 v[220:223],a[184:187],v[208:211],v[220:223]
ds_read_b64_tr_b16 a[254:255],v3 offset:13056
v_mfma_f32_16x16x32_bf16 v[220:223],a[180:183],v[176:179],v[220:223]
ds_read_b64_tr_b16 a[248:249],v3 offset:768
v_mfma_f32_16x16x32_bf16 v[220:223],a[176:179],v[144:147],v[220:223]
s_waitcnt lgkmcnt(7)
ds_read_b64_tr_b16 a[250:251],v3 offset:4864
v_mfma_f32_16x16x32_bf16 a[0:3],a[192:195],v[132:135],a[0:3]
buffer_load_dword v26,v24,s[88:91],s93 offen
v_mfma_f32_16x16x32_bf16 a[0:3],a[196:199],v[148:151],a[0:3]
v_exp_f32 v180,v180
v_mfma_f32_16x16x32_bf16 a[4:7],a[204:207],v[148:151],a[4:7]
v_exp_f32 v181,v181
v_mfma_f32_16x16x32_bf16 a[4:7],a[200:203],v[132:135],a[4:7]
v_exp_f32 v182,v182
v_mfma_f32_16x16x32_bf16 a[8:11],a[208:211],v[132:135],a[8:11]
s_memrealtime s[76:77]
v_exp_f32 v183,v183
v_mfma_f32_16x16x32_bf16 a[8:11],a[212:215],v[148:151],a[8:11]
v_exp_f32 v172,v172
v_mfma_f32_16x16x32_bf16 a[12:15],a[220:223],v[148:151],a[12:15]
v_exp_f32 v173,v173
v_mfma_f32_16x16x32_bf16 a[12:15],a[216:219],v[132:135],a[12:15]
v_exp_f32 v174,v174
v_mfma_f32_16x16x32_bf16 a[16:19],a[224:227],v[132:135],a[16:19]
v_exp_f32 v175,v175
v_mfma_f32_16x16x32_bf16 a[16:19],a[228:231],v[148:151],a[16:19]
v_cvt_pk_bf16_f32 v180,v180,v181
v_cvt_pk_bf16_f32 v181,v182,v183
v_mfma_f32_16x16x32_bf16 a[20:23],a[236:239],v[148:151],a[20:23]
v_exp_f32 v188,v188
v_mfma_f32_16x16x32_bf16 a[20:23],a[232:235],v[132:135],a[20:23]
s_waitcnt lgkmcnt(0)
v_exp_f32 v189,v189
v_mfma_f32_16x16x32_bf16 a[24:27],a[240:243],v[132:135],a[24:27]
s_sub_u32 s79,s78,s76
v_exp_f32 v190,v190
v_mfma_f32_16x16x32_bf16 a[24:27],a[244:247],v[148:151],a[24:27]
s_add_u32 s78,s76,s60
v_exp_f32 v191,v191
v_mfma_f32_16x16x32_bf16 a[28:31],a[252:255],v[148:151],a[28:31]
s_cmp_gt_i32 s79,0
s_cbranch_scc1 L_ps6_%=
L_pr6_%=:
v_cvt_pk_bf16_f32 v166,v172,v173
v_mfma_f32_16x16x32_bf16 a[28:31],a[248:251],v[132:135],a[28:31]
v_cvt_pk_bf16_f32 v167,v174,v175
v_cvt_pk_bf16_f32 v182,v188,v189
v_mfma_f32_16x16x32_bf16 v[48:51],v[28:31],v[132:135],v[48:51]
v_cvt_pk_bf16_f32 v183,v190,v191
s_cmp_lg_u32 s53,0
v_mfma_f32_16x16x32_bf16 v[48:51],v[28:31],v[148:151],v[48:51]
s_cbranch_scc1 L_att1_%=
L_endseg_%=:
v_mfma_f32_16x16x32_bf16 v[228:231],a[128:131],v[152:155],v[44:47]
s_waitcnt vmcnt(10)
s_add_u32 s80,s71,0
s_and_b32 s80,s80,3
s_lshl_b32 s80,s80,1
v_mfma_f32_16x16x32_bf16 v[228:231],a[132:135],v[184:187],v[228:231]
s_barrier
s_add_u32 m0,s49,s51
s_mul_i32 s80,s80,163840
v_mfma_f32_16x16x32_bf16 v[228:231],a[136:139],v[216:219],v[228:231]
s_add_u32 s80,s80,s81
s_add_u32 s83,s80,20480
ds_read_b128 a[128:131],v1
v_mfma_f32_16x16x32_bf16 v[228:231],a[140:143],v[248:251],v[228:231]
s_lshl_b32 s43,s41,2
v_exp_f32 v196,v196
v_mfma_f32_16x16x32_bf16 v[236:239],a[156:159],v[248:251],v[44:47]
s_add_u32 s43,s43,s70
buffer_load_dwordx4 v4,s[20:23],s47 offen nt lds
v_mfma_f32_16x16x32_bf16 v[236:239],a[152:155],v[216:219],v[236:239]
s_lshl_b32 s43,s43,19
ds_read_b128 a[132:135],v1 offset:256
v_add_u32 v2,s48,v2
v_mfma_f32_16x16x32_bf16 v[236:239],a[148:151],v[184:187],v[236:239]
s_add_u32 s43,s43,s55
v_exp_f32 v197,v197
v_mfma_f32_16x16x32_bf16 v[236:239],a[144:147],v[152:155],v[236:239]
s_mov_b64 s[28:29],s[12:13]
ds_read_b128 a[136:139],v1 offset:512
v_cvt_pk_bf16_f32 v196,v196,v197
v_mfma_f32_16x16x32_bf16 v[244:247],a[160:163],v[152:155],v[44:47]
s_mov_b32 s30,475136
v_exp_f32 v198,v198
v_mfma_f32_16x16x32_bf16 v[244:247],a[164:167],v[184:187],v[244:247]
s_mov_b32 s31,0x20000
ds_read_b128 a[140:143],v1 offset:768
v_add_u32 v3,s48,v3
v_mfma_f32_16x16x32_bf16 v[244:247],a[168:171],v[216:219],v[244:247]
s_add_u32 s54,s54,16384
v_exp_f32 v199,v199
v_mfma_f32_16x16x32_bf16 v[244:247],a[172:175],v[248:251],v[244:247]
s_and_b32 s46,s54,0xfffc0000
ds_read_b128 a[156:159],v1 offset:4864
v_cvt_pk_bf16_f32 v197,v198,v199
v_mfma_f32_16x16x32_bf16 v[252:255],a[188:191],v[248:251],v[44:47]
v_exp_f32 v204,v204
v_mfma_f32_16x16x32_bf16 v[252:255],a[184:187],v[216:219],v[252:255]
s_cmp_eq_u32 s50,131072
s_cselect_b32 s62,163840,0
ds_read_b128 a[152:155],v1 offset:4608
v_mfma_f32_16x16x32_bf16 v[252:255],a[180:183],v[184:187],v[252:255]
s_sub_u32 s48,32768,s62
v_exp_f32 v205,v205
v_mfma_f32_16x16x32_bf16 v[252:255],a[176:179],v[152:155],v[252:255]
s_add_u32 s50,s50,s48
ds_read_b128 a[148:151],v1 offset:4352
v_cvt_pk_bf16_f32 v198,v204,v205
v_mfma_f32_16x16x32_bf16 a[32:35],a[192:195],v[164:167],a[32:35]
v_exp_f32 v206,v206
v_mfma_f32_16x16x32_bf16 a[32:35],a[196:199],v[180:183],a[32:35]
s_cmp_eq_u32 s50,131072
s_cselect_b32 s62,163840,0
ds_read_b128 a[144:147],v1 offset:4096
v_mfma_f32_16x16x32_bf16 a[36:39],a[204:207],v[180:183],a[36:39]
buffer_load_dwordx4 v4,s[20:23],s47 offen offset:1024 nt lds
v_mfma_f32_16x16x32_bf16 a[36:39],a[200:203],v[164:167],a[36:39]
s_add_u32 s28,s28,s43
s_addc_u32 s29,s29,0
ds_read_b128 a[160:163],v1 offset:8192
v_mfma_f32_16x16x32_bf16 a[40:43],a[208:211],v[164:167],a[40:43]
s_and_b32 s29,s29,0xffff
v_exp_f32 v207,v207
v_mfma_f32_16x16x32_bf16 a[40:43],a[212:215],v[180:183],a[40:43]
ds_read_b128 a[164:167],v1 offset:8448
v_cvt_pk_bf16_f32 v199,v206,v207
v_mfma_f32_16x16x32_bf16 a[44:47],a[220:223],v[180:183],a[44:47]
v_exp_f32 v212,v212
v_mfma_f32_16x16x32_bf16 a[44:47],a[216:219],v[164:167],a[44:47]
ds_read_b128 a[168:171],v1 offset:8704
v_mfma_f32_16x16x32_bf16 a[48:51],a[224:227],v[164:167],a[48:51]
v_exp_f32 v213,v213
v_mfma_f32_16x16x32_bf16 a[48:51],a[228:231],v[180:183],a[48:51]
ds_read_b128 a[172:175],v1 offset:8960
v_cvt_pk_bf16_f32 v212,v212,v213
v_mfma_f32_16x16x32_bf16 a[52:55],a[236:239],v[180:183],a[52:55]
v_exp_f32 v214,v214
v_mfma_f32_16x16x32_bf16 a[52:55],a[232:235],v[164:167],a[52:55]
ds_read_b128 a[188:191],v1 offset:13056
v_mfma_f32_16x16x32_bf16 a[56:59],a[240:243],v[164:167],a[56:59]
v_exp_f32 v215,v215
v_mfma_f32_16x16x32_bf16 a[56:59],a[244:247],v[180:183],a[56:59]
ds_read_b128 a[184:187],v1 offset:12800
v_cvt_pk_bf16_f32 v213,v214,v215
v_mfma_f32_16x16x32_bf16 a[60:63],a[252:255],v[180:183],a[60:63]
v_exp_f32 v220,v220
v_mfma_f32_16x16x32_bf16 a[60:63],a[248:251],v[164:167],a[60:63]
ds_read_b128 a[180:183],v1 offset:12544
v_mfma_f32_16x16x32_bf16 v[52:55],v[28:31],v[164:167],v[52:55]
buffer_load_dwordx4 v5,s[20:23],s47 offen offset:2048 nt lds
v_mfma_f32_16x16x32_bf16 v[52:55],v[28:31],v[180:183],v[52:55]
s_waitcnt lgkmcnt(3)
ds_read_b128 a[176:179],v1 offset:12288
v_add_u32 v1,s48,v1
v_mfma_f32_16x16x32_bf16 v[132:135],a[128:131],v[128:131],v[32:35]
v_exp_f32 v221,v221
v_mfma_f32_16x16x32_bf16 v[132:135],a[132:135],v[160:163],v[132:135]
v_exp_f32 v222,v222
v_mfma_f32_16x16x32_bf16 v[132:135],a[136:139],v[192:195],v[132:135]
v_exp_f32 v223,v223
v_mfma_f32_16x16x32_bf16 v[132:135],a[140:143],v[224:227],v[132:135]
v_cvt_pk_bf16_f32 v214,v220,v221
v_cvt_pk_bf16_f32 v215,v222,v223
v_mfma_f32_16x16x32_bf16 v[140:143],a[156:159],v[224:227],v[32:35]
v_exp_f32 v228,v228
v_mfma_f32_16x16x32_bf16 v[140:143],a[152:155],v[192:195],v[140:143]
v_exp_f32 v229,v229
v_mfma_f32_16x16x32_bf16 v[140:143],a[148:151],v[160:163],v[140:143]
v_exp_f32 v230,v230
v_mfma_f32_16x16x32_bf16 v[140:143],a[144:147],v[128:131],v[140:143]
v_exp_f32 v231,v231
v_mfma_f32_16x16x32_bf16 v[148:151],a[160:163],v[128:131],v[32:35]
v_cvt_pk_bf16_f32 v228,v228,v229
v_cvt_pk_bf16_f32 v229,v230,v231
v_mfma_f32_16x16x32_bf16 v[148:151],a[164:167],v[160:163],v[148:151]
v_exp_f32 v236,v236
v_mfma_f32_16x16x32_bf16 v[148:151],a[168:171],v[192:195],v[148:151]
v_exp_f32 v237,v237
v_mfma_f32_16x16x32_bf16 v[148:151],a[172:175],v[224:227],v[148:151]
s_waitcnt lgkmcnt(0)
v_exp_f32 v238,v238
v_mfma_f32_16x16x32_bf16 v[156:159],a[188:191],v[224:227],v[32:35]
buffer_load_dwordx4 v5,s[20:23],s47 offen offset:3072 nt lds
)A"
R"A(v_mfma_f32_16x16x32_bf16 v[156:159],a[184:187],v[192:195],v[156:159]
v_exp_f32 v239,v239
v_mfma_f32_16x16x32_bf16 v[156:159],a[180:183],v[160:163],v[156:159]
v_exp_f32 v244,v244
v_mfma_f32_16x16x32_bf16 v[156:159],a[176:179],v[128:131],v[156:159]
v_exp_f32 v245,v245
v_mfma_f32_16x16x32_bf16 a[64:67],a[192:195],v[196:199],a[64:67]
v_exp_f32 v246,v246
v_mfma_f32_16x16x32_bf16 a[64:67],a[196:199],v[212:215],a[64:67]
v_exp_f32 v247,v247
v_mfma_f32_16x16x32_bf16 a[68:71],a[204:207],v[212:215],a[68:71]
v_cvt_pk_bf16_f32 v230,v236,v237
v_cvt_pk_bf16_f32 v231,v238,v239
v_mfma_f32_16x16x32_bf16 a[68:71],a[200:203],v[196:199],a[68:71]
v_cvt_pk_bf16_f32 v244,v244,v245
v_cvt_pk_bf16_f32 v245,v246,v247
v_mfma_f32_16x16x32_bf16 a[72:75],a[208:211],v[196:199],a[72:75]
v_exp_f32 v252,v252
v_mfma_f32_16x16x32_bf16 a[72:75],a[212:215],v[212:215],a[72:75]
v_exp_f32 v253,v253
v_mfma_f32_16x16x32_bf16 a[76:79],a[220:223],v[212:215],a[76:79]
v_exp_f32 v254,v254
v_mfma_f32_16x16x32_bf16 a[76:79],a[216:219],v[196:199],a[76:79]
s_add_u32 m0,s49,s52
v_exp_f32 v255,v255
v_mfma_f32_16x16x32_bf16 a[80:83],a[224:227],v[196:199],a[80:83]
s_add_u32 s49,s49,32768
v_cvt_pk_bf16_f32 v246,v252,v253
v_cvt_pk_bf16_f32 v247,v254,v255
v_mfma_f32_16x16x32_bf16 a[80:83],a[228:231],v[212:215],a[80:83]
v_exp_f32 v132,v132
v_mfma_f32_16x16x32_bf16 a[84:87],a[236:239],v[212:215],a[84:87]
buffer_load_dwordx4 v4,s[24:27],s47 offen nt lds
v_mfma_f32_16x16x32_bf16 a[84:87],a[232:235],v[196:199],a[84:87]
v_exp_f32 v133,v133
v_mfma_f32_16x16x32_bf16 a[88:91],a[240:243],v[196:199],a[88:91]
v_exp_f32 v134,v134
v_mfma_f32_16x16x32_bf16 a[88:91],a[244:247],v[212:215],a[88:91]
v_exp_f32 v135,v135
v_mfma_f32_16x16x32_bf16 a[92:95],a[252:255],v[212:215],a[92:95]
v_cvt_pk_bf16_f32 v132,v132,v133
v_cvt_pk_bf16_f32 v133,v134,v135
v_mfma_f32_16x16x32_bf16 a[92:95],a[248:251],v[196:199],a[92:95]
v_exp_f32 v140,v140
v_mfma_f32_16x16x32_bf16 v[56:59],v[28:31],v[196:199],v[56:59]
v_exp_f32 v141,v141
v_mfma_f32_16x16x32_bf16 v[56:59],v[28:31],v[212:215],v[56:59]
v_exp_f32 v142,v142
v_mfma_f32_16x16x32_bf16 v[164:167],a[128:131],v[136:139],v[36:39]
v_exp_f32 v143,v143
v_mfma_f32_16x16x32_bf16 v[164:167],a[132:135],v[168:171],v[164:167]
v_exp_f32 v148,v148
v_mfma_f32_16x16x32_bf16 v[164:167],a[136:139],v[200:203],v[164:167]
v_exp_f32 v149,v149
v_mfma_f32_16x16x32_bf16 v[164:167],a[140:143],v[232:235],v[164:167]
v_exp_f32 v150,v150
v_mfma_f32_16x16x32_bf16 v[172:175],a[156:159],v[232:235],v[36:39]
v_exp_f32 v151,v151
v_mfma_f32_16x16x32_bf16 v[172:175],a[152:155],v[200:203],v[172:175]
v_cvt_pk_bf16_f32 v134,v140,v141
v_cvt_pk_bf16_f32 v135,v142,v143
v_mfma_f32_16x16x32_bf16 v[172:175],a[148:151],v[168:171],v[172:175]
buffer_load_dwordx4 v5,s[24:27],s47 offen offset:1024 nt lds
v_mfma_f32_16x16x32_bf16 v[172:175],a[144:147],v[136:139],v[172:175]
v_cvt_pk_bf16_f32 v148,v148,v149
v_cvt_pk_bf16_f32 v149,v150,v151
v_mfma_f32_16x16x32_bf16 v[180:183],a[160:163],v[136:139],v[36:39]
v_exp_f32 v156,v156
v_mfma_f32_16x16x32_bf16 v[180:183],a[164:167],v[168:171],v[180:183]
v_exp_f32 v157,v157
v_mfma_f32_16x16x32_bf16 v[180:183],a[168:171],v[200:203],v[180:183]
v_exp_f32 v158,v158
v_mfma_f32_16x16x32_bf16 v[180:183],a[172:175],v[232:235],v[180:183]
v_exp_f32 v159,v159
v_mfma_f32_16x16x32_bf16 v[188:191],a[188:191],v[232:235],v[36:39]
v_cvt_pk_bf16_f32 v150,v156,v157
v_cvt_pk_bf16_f32 v151,v158,v159
v_mfma_f32_16x16x32_bf16 v[188:191],a[184:187],v[200:203],v[188:191]
v_exp_f32 v164,v164
v_mfma_f32_16x16x32_bf16 v[188:191],a[180:183],v[168:171],v[188:191]
v_exp_f32 v165,v165
v_mfma_f32_16x16x32_bf16 v[188:191],a[176:179],v[136:139],v[188:191]
v_exp_f32 v166,v166
v_mfma_f32_16x16x32_bf16 a[96:99],a[192:195],v[228:231],a[96:99]
ds_read_b64_tr_b16 a[192:193],v2
ds_read_b64_tr_b16 a[194:195],v2 offset:4096
v_mfma_f32_16x16x32_bf16 a[96:99],a[196:199],v[244:247],a[96:99]
ds_read_b64_tr_b16 a[196:197],v2 offset:8192
v_cvt_pk_bf16_f32 v164,v164,v165
v_mfma_f32_16x16x32_bf16 a[100:103],a[204:207],v[244:247],a[100:103]
s_cmp_eq_u32 s49,163840
s_cselect_b32 s49,0,s49
ds_read_b64_tr_b16 a[198:199],v2 offset:12288
v_mfma_f32_16x16x32_bf16 a[100:103],a[200:203],v[228:231],a[100:103]
s_add_u32 s49,s49,32768
ds_read_b64_tr_b16 a[204:205],v3 offset:8192
v_mfma_f32_16x16x32_bf16 a[104:107],a[208:211],v[228:231],a[104:107]
buffer_load_dwordx4 v4,s[24:27],s47 offen offset:2048 nt lds
v_mfma_f32_16x16x32_bf16 a[104:107],a[212:215],v[244:247],a[104:107]
ds_read_b64_tr_b16 a[206:207],v3 offset:12288
ds_read_b64_tr_b16 a[200:201],v3
v_mfma_f32_16x16x32_bf16 a[108:111],a[220:223],v[244:247],a[108:111]
ds_read_b64_tr_b16 a[202:203],v3 offset:4096
v_lshrrev_b32 v4,6,v6
v_mfma_f32_16x16x32_bf16 a[108:111],a[216:219],v[228:231],a[108:111]
ds_read_b64_tr_b16 a[208:209],v2 offset:256
v_mul_u32_u24 v4,320,v4
v_mfma_f32_16x16x32_bf16 a[112:115],a[224:227],v[228:231],a[112:115]
s_cmp_eq_u32 s49,163840
s_cselect_b32 s49,0,s49
ds_read_b64_tr_b16 a[210:211],v2 offset:4352
v_mfma_f32_16x16x32_bf16 a[112:115],a[228:231],v[244:247],a[112:115]
s_add_u32 s49,s49,32768
ds_read_b64_tr_b16 a[212:213],v2 offset:8448
ds_read_b64_tr_b16 a[214:215],v2 offset:12544
v_mfma_f32_16x16x32_bf16 a[116:119],a[236:239],v[244:247],a[116:119]
s_cmp_eq_u32 s49,163840
s_cselect_b32 s49,0,s49
ds_read_b64_tr_b16 a[220:221],v3 offset:8448
v_mfma_f32_16x16x32_bf16 a[116:119],a[232:235],v[228:231],a[116:119]
ds_read_b64_tr_b16 a[222:223],v3 offset:12544
v_mfma_f32_16x16x32_bf16 a[120:123],a[240:243],v[228:231],a[120:123]
ds_read_b64_tr_b16 a[216:217],v3 offset:256
v_mfma_f32_16x16x32_bf16 a[120:123],a[244:247],v[244:247],a[120:123]
ds_read_b64_tr_b16 a[218:219],v3 offset:4352
ds_read_b64_tr_b16 a[224:225],v2 offset:512
v_mfma_f32_16x16x32_bf16 a[124:127],a[252:255],v[244:247],a[124:127]
ds_read_b64_tr_b16 a[226:227],v2 offset:4608
v_mfma_f32_16x16x32_bf16 a[124:127],a[248:251],v[228:231],a[124:127]
ds_read_b64_tr_b16 a[228:229],v2 offset:8704
v_mfma_f32_16x16x32_bf16 v[60:63],v[28:31],v[228:231],v[60:63]
ds_read_b64_tr_b16 a[230:231],v2 offset:12800
v_mfma_f32_16x16x32_bf16 v[60:63],v[28:31],v[244:247],v[60:63]
ds_read_b64_tr_b16 a[236:237],v3 offset:8704
ds_read_b64_tr_b16 a[238:239],v3 offset:12800
v_mfma_f32_16x16x32_bf16 v[196:199],a[128:131],v[144:147],v[40:43]
buffer_load_dwordx4 v5,s[24:27],s47 offen offset:3072 nt lds
v_mfma_f32_16x16x32_bf16 v[196:199],a[132:135],v[176:179],v[196:199]
s_add_u32 m0,s82,0
ds_read_b64_tr_b16 a[232:233],v3 offset:512
ds_read_b64_tr_b16 a[234:235],v3 offset:4608
v_mfma_f32_16x16x32_bf16 v[196:199],a[136:139],v[208:211],v[196:199]
s_add_u32 s47,s47,16384
ds_read_b64_tr_b16 a[240:241],v2 offset:768
v_bfe_u32 v5,v6,4,2
v_mfma_f32_16x16x32_bf16 v[196:199],a[140:143],v[240:243],v[196:199]
s_and_b32 s47,s47,0x3ffff
ds_read_b64_tr_b16 a[242:243],v2 offset:4864
v_lshl_add_u32 v4,v5,3,v4
v_mfma_f32_16x16x32_bf16 v[204:207],a[156:159],v[240:243],v[40:43]
s_or_b32 s47,s47,s46
buffer_load_dwordx4 v7,s[72:75],s80 offen lds
v_mfma_f32_16x16x32_bf16 v[204:207],a[152:155],v[208:211],v[204:207]
ds_read_b64_tr_b16 a[244:245],v2 offset:8960
ds_read_b64_tr_b16 a[246:247],v2 offset:13056
v_mfma_f32_16x16x32_bf16 v[204:207],a[148:151],v[176:179],v[204:207]
ds_read_b64_tr_b16 a[252:253],v3 offset:8960
v_add_u32 v2,s48,v2
v_mfma_f32_16x16x32_bf16 v[204:207],a[144:147],v[144:147],v[204:207]
ds_read_b64_tr_b16 a[254:255],v3 offset:13056
v_bfe_u32 v5,v6,8,1
v_mfma_f32_16x16x32_bf16 v[212:215],a[160:163],v[144:147],v[40:43]
buffer_load_dwordx4 v8,s[72:75],s80 offen offset:1024 lds
v_mfma_f32_16x16x32_bf16 v[212:215],a[164:167],v[176:179],v[212:215]
ds_read_b64_tr_b16 a[248:249],v3 offset:768
ds_read_b64_tr_b16 a[250:251],v3 offset:4864
v_mfma_f32_16x16x32_bf16 v[212:215],a[168:171],v[208:211],v[212:215]
v_exp_f32 v167,v167
v_mfma_f32_16x16x32_bf16 v[212:215],a[172:175],v[240:243],v[212:215]
v_exp_f32 v172,v172
v_mfma_f32_16x16x32_bf16 v[220:223],a[188:191],v[240:243],v[40:43]
buffer_load_dwordx4 v9,s[72:75],s80 offen offset:2048 lds
v_mfma_f32_16x16x32_bf16 v[220:223],a[184:187],v[208:211],v[220:223]
v_exp_f32 v173,v173
v_mfma_f32_16x16x32_bf16 v[220:223],a[180:183],v[176:179],v[220:223]
v_exp_f32 v174,v174
v_mfma_f32_16x16x32_bf16 v[220:223],a[176:179],v[144:147],v[220:223]
s_waitcnt lgkmcnt(2)
v_exp_f32 v175,v175
v_mfma_f32_16x16x32_bf16 a[0:3],a[192:195],v[132:135],a[0:3]
buffer_load_dwordx4 v10,s[72:75],s80 offen offset:3072 lds
v_mfma_f32_16x16x32_bf16 a[0:3],a[196:199],v[148:151],a[0:3]
s_add_u32 m0,s82,4096
v_exp_f32 v180,v180
v_mfma_f32_16x16x32_bf16 a[4:7],a[204:207],v[148:151],a[4:7]
v_exp_f32 v181,v181
v_mfma_f32_16x16x32_bf16 a[4:7],a[200:203],v[132:135],a[4:7]
v_cvt_pk_bf16_f32 v165,v166,v167
v_cvt_pk_bf16_f32 v166,v172,v173
v_mfma_f32_16x16x32_bf16 a[8:11],a[208:211],v[132:135],a[8:11]
buffer_load_dwordx4 v11,s[72:75],s80 offen lds
v_mfma_f32_16x16x32_bf16 a[8:11],a[212:215],v[148:151],a[8:11]
s_add_u32 m0,s82,5120
v_exp_f32 v182,v182
v_mfma_f32_16x16x32_bf16 a[12:15],a[220:223],v[148:151],a[12:15]
s_add_u32 s80,s71,0
s_and_b32 s80,s80,3
s_lshl_b32 s80,s80,1
v_mfma_f32_16x16x32_bf16 a[12:15],a[216:219],v[132:135],a[12:15]
s_or_b32 s80,s80,1
v_exp_f32 v183,v183
v_mfma_f32_16x16x32_bf16 a[16:19],a[224:227],v[132:135],a[16:19]
s_mul_i32 s80,s80,163840
buffer_load_dwordx4 v7,s[72:75],s83 offen lds
v_mfma_f32_16x16x32_bf16 a[16:19],a[228:231],v[148:151],a[16:19]
s_add_u32 s80,s80,s81
v_cvt_pk_bf16_f32 v167,v174,v175
v_cvt_pk_bf16_f32 v180,v180,v181
v_mfma_f32_16x16x32_bf16 a[20:23],a[236:239],v[148:151],a[20:23]
v_exp_f32 v188,v188
v_mfma_f32_16x16x32_bf16 a[20:23],a[232:235],v[132:135],a[20:23]
v_exp_f32 v189,v189
v_mfma_f32_16x16x32_bf16 a[24:27],a[240:243],v[132:135],a[24:27]
buffer_load_dwordx4 v8,s[72:75],s83 offen offset:1024 lds
v_mfma_f32_16x16x32_bf16 a[24:27],a[244:247],v[148:151],a[24:27]
v_exp_f32 v190,v190
v_mfma_f32_16x16x32_bf16 a[28:31],a[252:255],v[148:151],a[28:31]
s_waitcnt lgkmcnt(0)
v_exp_f32 v191,v191
v_mfma_f32_16x16x32_bf16 a[28:31],a[248:251],v[132:135],a[28:31]
v_cvt_pk_bf16_f32 v181,v182,v183
v_cvt_pk_bf16_f32 v182,v188,v189
v_mfma_f32_16x16x32_bf16 v[48:51],v[28:31],v[132:135],v[48:51]
buffer_load_dwordx4 v9,s[72:75],s83 offen offset:2048 lds
v_mfma_f32_16x16x32_bf16 v[48:51],v[28:31],v[148:151],v[48:51]
v_cvt_pk_bf16_f32 v183,v190,v191
v_add_u32 v3,s48,v3
v_mfma_f32_16x16x32_bf16 v[228:231],a[128:131],v[152:155],v[44:47]
s_waitcnt vmcnt(17)
s_sub_u32 s48,32768,s62
v_exp_f32 v196,v196
v_mfma_f32_16x16x32_bf16 v[228:231],a[132:135],v[184:187],v[228:231]
s_barrier
buffer_load_dwordx4 v10,s[72:75],s83 offen offset:3072 lds
v_mfma_f32_16x16x32_bf16 v[228:231],a[136:139],v[216:219],v[228:231]
s_add_u32 m0,s82,9216
s_add_u32 s50,s50,s48
ds_read_b128 a[128:131],v1
v_mfma_f32_16x16x32_bf16 v[228:231],a[140:143],v[248:251],v[228:231]
v_exp_f32 v197,v197
v_mfma_f32_16x16x32_bf16 v[236:239],a[156:159],v[248:251],v[44:47]
ds_read_b128 a[132:135],v1 offset:256
v_cvt_pk_bf16_f32 v196,v196,v197
v_mfma_f32_16x16x32_bf16 v[236:239],a[152:155],v[216:219],v[236:239]
buffer_load_dwordx4 v11,s[72:75],s83 offen lds
v_mfma_f32_16x16x32_bf16 v[236:239],a[148:151],v[184:187],v[236:239]
s_add_u32 s83,s80,20480
ds_read_b128 a[136:139],v1 offset:512
v_lshlrev_b32 v5,5,v5
v_mfma_f32_16x16x32_bf16 v[236:239],a[144:147],v[152:155],v[236:239]
v_exp_f32 v198,v198
v_mfma_f32_16x16x32_bf16 v[244:247],a[160:163],v[152:155],v[44:47]
s_add_u32 m0,s82,40960
ds_read_b128 a[140:143],v1 offset:768
v_mfma_f32_16x16x32_bf16 v[244:247],a[164:167],v[184:187],v[244:247]
v_exp_f32 v199,v199
v_mfma_f32_16x16x32_bf16 v[244:247],a[168:171],v[216:219],v[244:247]
ds_read_b128 a[156:159],v1 offset:4864
v_cvt_pk_bf16_f32 v197,v198,v199
v_mfma_f32_16x16x32_bf16 v[244:247],a[172:175],v[248:251],v[244:247]
buffer_load_dwordx4 v7,s[72:75],s80 offen lds
v_mfma_f32_16x16x32_bf16 v[252:255],a[188:191],v[248:251],v[44:47]
s_cmp_eq_u32 s50,131072
s_cselect_b32 s62,163840,0
ds_read_b128 a[152:155],v1 offset:4608
v_mfma_f32_16x16x32_bf16 v[252:255],a[184:187],v[216:219],v[252:255]
v_exp_f32 v204,v204
v_mfma_f32_16x16x32_bf16 v[252:255],a[180:183],v[184:187],v[252:255]
)A"
R"A(ds_read_b128 a[148:151],v1 offset:4352
v_mfma_f32_16x16x32_bf16 v[252:255],a[176:179],v[152:155],v[252:255]
v_exp_f32 v205,v205
v_mfma_f32_16x16x32_bf16 a[32:35],a[192:195],v[164:167],a[32:35]
ds_read_b128 a[144:147],v1 offset:4096
v_cvt_pk_bf16_f32 v198,v204,v205
v_mfma_f32_16x16x32_bf16 a[32:35],a[196:199],v[180:183],a[32:35]
v_exp_f32 v206,v206
v_mfma_f32_16x16x32_bf16 a[36:39],a[204:207],v[180:183],a[36:39]
ds_read_b128 a[160:163],v1 offset:8192
v_mfma_f32_16x16x32_bf16 a[36:39],a[200:203],v[164:167],a[36:39]
v_exp_f32 v207,v207
v_mfma_f32_16x16x32_bf16 a[40:43],a[208:211],v[164:167],a[40:43]
ds_read_b128 a[164:167],v1 offset:8448
v_cvt_pk_bf16_f32 v199,v206,v207
v_mfma_f32_16x16x32_bf16 a[40:43],a[212:215],v[180:183],a[40:43]
v_exp_f32 v212,v212
v_mfma_f32_16x16x32_bf16 a[44:47],a[220:223],v[180:183],a[44:47]
buffer_store_dwordx4 v[64:67],v6,s[36:39],s84 offen sc1
v_mfma_f32_16x16x32_bf16 a[44:47],a[216:219],v[164:167],a[44:47]
ds_read_b128 a[168:171],v1 offset:8704
v_mfma_f32_16x16x32_bf16 a[48:51],a[224:227],v[164:167],a[48:51]
v_exp_f32 v213,v213
v_mfma_f32_16x16x32_bf16 a[48:51],a[228:231],v[180:183],a[48:51]
ds_read_b128 a[172:175],v1 offset:8960
v_cvt_pk_bf16_f32 v212,v212,v213
v_mfma_f32_16x16x32_bf16 a[52:55],a[236:239],v[180:183],a[52:55]
buffer_load_dwordx4 v8,s[72:75],s80 offen offset:1024 lds
v_mfma_f32_16x16x32_bf16 a[52:55],a[232:235],v[164:167],a[52:55]
ds_read_b128 a[188:191],v1 offset:13056
v_mfma_f32_16x16x32_bf16 a[56:59],a[240:243],v[164:167],a[56:59]
v_exp_f32 v214,v214
v_mfma_f32_16x16x32_bf16 a[56:59],a[244:247],v[180:183],a[56:59]
ds_read_b128 a[184:187],v1 offset:12800
v_mfma_f32_16x16x32_bf16 a[60:63],a[252:255],v[180:183],a[60:63]
v_exp_f32 v215,v215
v_mfma_f32_16x16x32_bf16 a[60:63],a[248:251],v[164:167],a[60:63]
ds_read_b128 a[180:183],v1 offset:12544
v_cvt_pk_bf16_f32 v213,v214,v215
v_mfma_f32_16x16x32_bf16 v[52:55],v[28:31],v[164:167],v[52:55]
v_exp_f32 v220,v220
v_mfma_f32_16x16x32_bf16 v[52:55],v[28:31],v[180:183],v[52:55]
s_waitcnt lgkmcnt(3)
ds_read_b128 a[176:179],v1 offset:12288
v_add_u32 v1,s48,v1
v_mfma_f32_16x16x32_bf16 v[132:135],a[128:131],v[128:131],v[32:35]
v_exp_f32 v221,v221
v_mfma_f32_16x16x32_bf16 v[132:135],a[132:135],v[160:163],v[132:135]
v_exp_f32 v222,v222
v_mfma_f32_16x16x32_bf16 v[132:135],a[136:139],v[192:195],v[132:135]
v_exp_f32 v223,v223
v_mfma_f32_16x16x32_bf16 v[132:135],a[140:143],v[224:227],v[132:135]
buffer_load_dwordx4 v9,s[72:75],s80 offen offset:2048 lds
v_mfma_f32_16x16x32_bf16 v[140:143],a[156:159],v[224:227],v[32:35]
v_cvt_pk_bf16_f32 v214,v220,v221
v_cvt_pk_bf16_f32 v215,v222,v223
v_mfma_f32_16x16x32_bf16 v[140:143],a[152:155],v[192:195],v[140:143]
v_exp_f32 v228,v228
v_mfma_f32_16x16x32_bf16 v[140:143],a[148:151],v[160:163],v[140:143]
v_exp_f32 v229,v229
v_mfma_f32_16x16x32_bf16 v[140:143],a[144:147],v[128:131],v[140:143]
buffer_store_dwordx4 v[68:71],v6,s[36:39],s84 offen offset:1024 sc1
v_mfma_f32_16x16x32_bf16 v[148:151],a[160:163],v[128:131],v[32:35]
v_exp_f32 v230,v230
v_mfma_f32_16x16x32_bf16 v[148:151],a[164:167],v[160:163],v[148:151]
v_exp_f32 v231,v231
v_mfma_f32_16x16x32_bf16 v[148:151],a[168:171],v[192:195],v[148:151]
v_cvt_pk_bf16_f32 v228,v228,v229
v_cvt_pk_bf16_f32 v229,v230,v231
v_mfma_f32_16x16x32_bf16 v[148:151],a[172:175],v[224:227],v[148:151]
s_waitcnt lgkmcnt(0)
v_exp_f32 v236,v236
v_mfma_f32_16x16x32_bf16 v[156:159],a[188:191],v[224:227],v[32:35]
v_exp_f32 v237,v237
v_mfma_f32_16x16x32_bf16 v[156:159],a[184:187],v[192:195],v[156:159]
v_exp_f32 v238,v238
v_mfma_f32_16x16x32_bf16 v[156:159],a[180:183],v[160:163],v[156:159]
v_exp_f32 v239,v239
v_mfma_f32_16x16x32_bf16 v[156:159],a[176:179],v[128:131],v[156:159]
v_exp_f32 v244,v244
v_mfma_f32_16x16x32_bf16 a[64:67],a[192:195],v[196:199],a[64:67]
buffer_load_dwordx4 v10,s[72:75],s80 offen offset:3072 lds
v_mfma_f32_16x16x32_bf16 a[64:67],a[196:199],v[212:215],a[64:67]
v_exp_f32 v245,v245
v_mfma_f32_16x16x32_bf16 a[68:71],a[204:207],v[212:215],a[68:71]
v_exp_f32 v246,v246
v_mfma_f32_16x16x32_bf16 a[68:71],a[200:203],v[196:199],a[68:71]
v_exp_f32 v247,v247
v_mfma_f32_16x16x32_bf16 a[72:75],a[208:211],v[196:199],a[72:75]
buffer_store_dwordx4 v[72:75],v6,s[36:39],s84 offen offset:2048 sc1
v_mfma_f32_16x16x32_bf16 a[72:75],a[212:215],v[212:215],a[72:75]
v_cvt_pk_bf16_f32 v230,v236,v237
v_cvt_pk_bf16_f32 v231,v238,v239
v_mfma_f32_16x16x32_bf16 a[76:79],a[220:223],v[212:215],a[76:79]
v_cvt_pk_bf16_f32 v244,v244,v245
v_cvt_pk_bf16_f32 v245,v246,v247
v_mfma_f32_16x16x32_bf16 a[76:79],a[216:219],v[196:199],a[76:79]
v_exp_f32 v252,v252
v_mfma_f32_16x16x32_bf16 a[80:83],a[224:227],v[196:199],a[80:83]
v_exp_f32 v253,v253
v_mfma_f32_16x16x32_bf16 a[80:83],a[228:231],v[212:215],a[80:83]
v_exp_f32 v254,v254
v_mfma_f32_16x16x32_bf16 a[84:87],a[236:239],v[212:215],a[84:87]
s_add_u32 m0,s82,45056
v_exp_f32 v255,v255
v_mfma_f32_16x16x32_bf16 a[84:87],a[232:235],v[196:199],a[84:87]
v_cvt_pk_bf16_f32 v246,v252,v253
v_cvt_pk_bf16_f32 v247,v254,v255
v_mfma_f32_16x16x32_bf16 a[88:91],a[240:243],v[196:199],a[88:91]
v_exp_f32 v132,v132
v_mfma_f32_16x16x32_bf16 a[88:91],a[244:247],v[212:215],a[88:91]
buffer_load_dwordx4 v11,s[72:75],s80 offen lds
v_mfma_f32_16x16x32_bf16 a[92:95],a[252:255],v[212:215],a[92:95]
v_exp_f32 v133,v133
v_mfma_f32_16x16x32_bf16 a[92:95],a[248:251],v[196:199],a[92:95]
v_exp_f32 v134,v134
v_mfma_f32_16x16x32_bf16 v[56:59],v[28:31],v[196:199],v[56:59]
v_exp_f32 v135,v135
v_mfma_f32_16x16x32_bf16 v[56:59],v[28:31],v[212:215],v[56:59]
v_cvt_pk_bf16_f32 v132,v132,v133
v_cvt_pk_bf16_f32 v133,v134,v135
v_mfma_f32_16x16x32_bf16 v[164:167],a[128:131],v[136:139],v[36:39]
v_exp_f32 v140,v140
v_mfma_f32_16x16x32_bf16 v[164:167],a[132:135],v[168:171],v[164:167]
v_exp_f32 v141,v141
v_mfma_f32_16x16x32_bf16 v[164:167],a[136:139],v[200:203],v[164:167]
buffer_store_dwordx4 v[76:79],v6,s[36:39],s84 offen offset:3072 sc1
v_mfma_f32_16x16x32_bf16 v[164:167],a[140:143],v[232:235],v[164:167]
s_add_u32 s84,s55,65536
v_exp_f32 v142,v142
v_mfma_f32_16x16x32_bf16 v[172:175],a[156:159],v[232:235],v[36:39]
s_max_i32 s84,s84,s18
v_exp_f32 v143,v143
v_mfma_f32_16x16x32_bf16 v[172:175],a[152:155],v[200:203],v[172:175]
s_add_u32 m0,s82,46080
v_exp_f32 v148,v148
v_mfma_f32_16x16x32_bf16 v[172:175],a[148:151],v[168:171],v[172:175]
v_exp_f32 v149,v149
v_mfma_f32_16x16x32_bf16 v[172:175],a[144:147],v[136:139],v[172:175]
v_exp_f32 v150,v150
v_mfma_f32_16x16x32_bf16 v[180:183],a[160:163],v[136:139],v[36:39]
buffer_load_dwordx4 v7,s[72:75],s83 offen lds
v_mfma_f32_16x16x32_bf16 v[180:183],a[164:167],v[168:171],v[180:183]
v_exp_f32 v151,v151
v_mfma_f32_16x16x32_bf16 v[180:183],a[168:171],v[200:203],v[180:183]
v_cvt_pk_bf16_f32 v134,v140,v141
v_cvt_pk_bf16_f32 v135,v142,v143
v_mfma_f32_16x16x32_bf16 v[180:183],a[172:175],v[232:235],v[180:183]
v_cvt_pk_bf16_f32 v148,v148,v149
v_cvt_pk_bf16_f32 v149,v150,v151
v_mfma_f32_16x16x32_bf16 v[188:191],a[188:191],v[232:235],v[36:39]
v_exp_f32 v156,v156
v_mfma_f32_16x16x32_bf16 v[188:191],a[184:187],v[200:203],v[188:191]
v_exp_f32 v157,v157
v_mfma_f32_16x16x32_bf16 v[188:191],a[180:183],v[168:171],v[188:191]
v_exp_f32 v158,v158
v_mfma_f32_16x16x32_bf16 v[188:191],a[176:179],v[136:139],v[188:191]
v_exp_f32 v159,v159
v_mfma_f32_16x16x32_bf16 a[96:99],a[192:195],v[228:231],a[96:99]
ds_read_b64_tr_b16 a[192:193],v2
ds_read_b64_tr_b16 a[194:195],v2 offset:4096
v_mfma_f32_16x16x32_bf16 a[96:99],a[196:199],v[244:247],a[96:99]
ds_read_b64_tr_b16 a[196:197],v2 offset:8192
v_cvt_pk_bf16_f32 v150,v156,v157
v_mfma_f32_16x16x32_bf16 a[100:103],a[204:207],v[244:247],a[100:103]
buffer_store_dwordx4 v[80:83],v6,s[36:39],s85 offen sc1
v_mfma_f32_16x16x32_bf16 a[100:103],a[200:203],v[228:231],a[100:103]
ds_read_b64_tr_b16 a[198:199],v2 offset:12288
ds_read_b64_tr_b16 a[204:205],v3 offset:8192
v_mfma_f32_16x16x32_bf16 a[104:107],a[208:211],v[228:231],a[104:107]
ds_read_b64_tr_b16 a[206:207],v3 offset:12288
v_cvt_pk_bf16_f32 v151,v158,v159
v_mfma_f32_16x16x32_bf16 a[104:107],a[212:215],v[244:247],a[104:107]
ds_read_b64_tr_b16 a[200:201],v3
v_mfma_f32_16x16x32_bf16 a[108:111],a[220:223],v[244:247],a[108:111]
buffer_load_dwordx4 v8,s[72:75],s83 offen offset:1024 lds
v_mfma_f32_16x16x32_bf16 a[108:111],a[216:219],v[228:231],a[108:111]
ds_read_b64_tr_b16 a[202:203],v3 offset:4096
ds_read_b64_tr_b16 a[208:209],v2 offset:256
v_mfma_f32_16x16x32_bf16 a[112:115],a[224:227],v[228:231],a[112:115]
ds_read_b64_tr_b16 a[210:211],v2 offset:4352
v_mfma_f32_16x16x32_bf16 a[112:115],a[228:231],v[244:247],a[112:115]
ds_read_b64_tr_b16 a[212:213],v2 offset:8448
v_mfma_f32_16x16x32_bf16 a[116:119],a[236:239],v[244:247],a[116:119]
ds_read_b64_tr_b16 a[214:215],v2 offset:12544
v_mfma_f32_16x16x32_bf16 a[116:119],a[232:235],v[228:231],a[116:119]
ds_read_b64_tr_b16 a[220:221],v3 offset:8448
ds_read_b64_tr_b16 a[222:223],v3 offset:12544
v_mfma_f32_16x16x32_bf16 a[120:123],a[240:243],v[228:231],a[120:123]
ds_read_b64_tr_b16 a[216:217],v3 offset:256
v_mfma_f32_16x16x32_bf16 a[120:123],a[244:247],v[244:247],a[120:123]
ds_read_b64_tr_b16 a[218:219],v3 offset:4352
v_mfma_f32_16x16x32_bf16 a[124:127],a[252:255],v[244:247],a[124:127]
ds_read_b64_tr_b16 a[224:225],v2 offset:512
v_mfma_f32_16x16x32_bf16 a[124:127],a[248:251],v[228:231],a[124:127]
ds_read_b64_tr_b16 a[226:227],v2 offset:4608
ds_read_b64_tr_b16 a[228:229],v2 offset:8704
v_mfma_f32_16x16x32_bf16 v[60:63],v[28:31],v[228:231],v[60:63]
ds_read_b64_tr_b16 a[230:231],v2 offset:12800
v_mfma_f32_16x16x32_bf16 v[60:63],v[28:31],v[244:247],v[60:63]
ds_read_b64_tr_b16 a[236:237],v3 offset:8704
v_mfma_f32_16x16x32_bf16 v[196:199],a[128:131],v[144:147],v[40:43]
buffer_store_dwordx4 v[84:87],v6,s[36:39],s85 offen offset:1024 sc1
v_mfma_f32_16x16x32_bf16 v[196:199],a[132:135],v[176:179],v[196:199]
ds_read_b64_tr_b16 a[238:239],v3 offset:12800
ds_read_b64_tr_b16 a[232:233],v3 offset:512
v_mfma_f32_16x16x32_bf16 v[196:199],a[136:139],v[208:211],v[196:199]
ds_read_b64_tr_b16 a[234:235],v3 offset:4608
v_mfma_f32_16x16x32_bf16 v[196:199],a[140:143],v[240:243],v[196:199]
ds_read_b64_tr_b16 a[240:241],v2 offset:768
v_mfma_f32_16x16x32_bf16 v[204:207],a[156:159],v[240:243],v[40:43]
buffer_load_dwordx4 v9,s[72:75],s83 offen offset:2048 lds
v_mfma_f32_16x16x32_bf16 v[204:207],a[152:155],v[208:211],v[204:207]
ds_read_b64_tr_b16 a[242:243],v2 offset:4864
ds_read_b64_tr_b16 a[244:245],v2 offset:8960
v_mfma_f32_16x16x32_bf16 v[204:207],a[148:151],v[176:179],v[204:207]
ds_read_b64_tr_b16 a[246:247],v2 offset:13056
v_add_u32 v2,s48,v2
v_mfma_f32_16x16x32_bf16 v[204:207],a[144:147],v[144:147],v[204:207]
ds_read_b64_tr_b16 a[252:253],v3 offset:8960
v_mfma_f32_16x16x32_bf16 v[212:215],a[160:163],v[144:147],v[40:43]
ds_read_b64_tr_b16 a[254:255],v3 offset:13056
v_mfma_f32_16x16x32_bf16 v[212:215],a[164:167],v[176:179],v[212:215]
ds_read_b64_tr_b16 a[248:249],v3 offset:768
ds_read_b64_tr_b16 a[250:251],v3 offset:4864
v_mfma_f32_16x16x32_bf16 v[212:215],a[168:171],v[208:211],v[212:215]
v_exp_f32 v164,v164
v_mfma_f32_16x16x32_bf16 v[212:215],a[172:175],v[240:243],v[212:215]
v_exp_f32 v165,v165
v_mfma_f32_16x16x32_bf16 v[220:223],a[188:191],v[240:243],v[40:43]
v_exp_f32 v166,v166
v_mfma_f32_16x16x32_bf16 v[220:223],a[184:187],v[208:211],v[220:223]
buffer_load_dwordx4 v10,s[72:75],s83 offen offset:3072 lds
v_mfma_f32_16x16x32_bf16 v[220:223],a[180:183],v[176:179],v[220:223]
v_exp_f32 v167,v167
v_mfma_f32_16x16x32_bf16 v[220:223],a[176:179],v[144:147],v[220:223]
s_waitcnt lgkmcnt(3)
v_cvt_pk_bf16_f32 v164,v164,v165
v_cvt_pk_bf16_f32 v165,v166,v167
v_mfma_f32_16x16x32_bf16 a[0:3],a[192:195],v[132:135],a[0:3]
v_exp_f32 v172,v172
v_mfma_f32_16x16x32_bf16 a[0:3],a[196:199],v[148:151],a[0:3]
buffer_store_dwordx4 v[88:91],v6,s[36:39],s85 offen offset:2048 sc1
v_mfma_f32_16x16x32_bf16 a[4:7],a[204:207],v[148:151],a[4:7]
v_exp_f32 v173,v173
v_mfma_f32_16x16x32_bf16 a[4:7],a[200:203],v[132:135],a[4:7]
v_exp_f32 v174,v174
v_mfma_f32_16x16x32_bf16 a[8:11],a[208:211],v[132:135],a[8:11]
v_exp_f32 v175,v175
v_mfma_f32_16x16x32_bf16 a[8:11],a[212:215],v[148:151],a[8:11]
v_exp_f32 v180,v180
)A"
R"A(v_mfma_f32_16x16x32_bf16 a[12:15],a[220:223],v[148:151],a[12:15]
v_exp_f32 v181,v181
v_mfma_f32_16x16x32_bf16 a[12:15],a[216:219],v[132:135],a[12:15]
s_add_u32 m0,s82,50176
s_cmp_ge_u32 0,s71
s_cselect_b32 s80,2,0
v_mfma_f32_16x16x32_bf16 a[16:19],a[224:227],v[132:135],a[16:19]
s_mul_i32 s80,s80,163840
v_exp_f32 v182,v182
v_mfma_f32_16x16x32_bf16 a[16:19],a[228:231],v[148:151],a[16:19]
s_add_u32 s80,s80,s81
v_exp_f32 v183,v183
v_mfma_f32_16x16x32_bf16 a[20:23],a[236:239],v[148:151],a[20:23]
buffer_load_dwordx4 v11,s[72:75],s83 offen lds
v_mfma_f32_16x16x32_bf16 a[20:23],a[232:235],v[132:135],a[20:23]
s_add_u32 s83,s80,20480
v_cvt_pk_bf16_f32 v166,v172,v173
v_cvt_pk_bf16_f32 v167,v174,v175
v_mfma_f32_16x16x32_bf16 a[24:27],a[240:243],v[132:135],a[24:27]
v_cvt_pk_bf16_f32 v180,v180,v181
v_cvt_pk_bf16_f32 v181,v182,v183
v_mfma_f32_16x16x32_bf16 a[24:27],a[244:247],v[148:151],a[24:27]
s_waitcnt lgkmcnt(0)
v_exp_f32 v188,v188
v_mfma_f32_16x16x32_bf16 a[28:31],a[252:255],v[148:151],a[28:31]
v_exp_f32 v189,v189
v_mfma_f32_16x16x32_bf16 a[28:31],a[248:251],v[132:135],a[28:31]
v_exp_f32 v190,v190
v_mfma_f32_16x16x32_bf16 v[48:51],v[28:31],v[132:135],v[48:51]
buffer_store_dwordx4 v[92:95],v6,s[36:39],s85 offen offset:3072 sc1
v_mfma_f32_16x16x32_bf16 v[48:51],v[28:31],v[148:151],v[48:51]
s_add_u32 s85,s55,69632
v_exp_f32 v191,v191
v_mfma_f32_16x16x32_bf16 v[228:231],a[128:131],v[152:155],v[44:47]
s_waitcnt vmcnt(28)
s_max_i32 s85,s85,s19
v_cvt_pk_bf16_f32 v182,v188,v189
v_cvt_pk_bf16_f32 v183,v190,v191
v_mfma_f32_16x16x32_bf16 v[228:231],a[132:135],v[184:187],v[228:231]
s_barrier
ds_read_b128 a[128:131],v1
v_add_u32 v3,s48,v3
v_mfma_f32_16x16x32_bf16 v[228:231],a[136:139],v[216:219],v[228:231]
s_sub_u32 s48,32768,s62
v_exp_f32 v196,v196
v_mfma_f32_16x16x32_bf16 v[228:231],a[140:143],v[248:251],v[228:231]
s_add_u32 s50,s50,s48
ds_read_b128 a[132:135],v1 offset:256
v_mfma_f32_16x16x32_bf16 v[236:239],a[156:159],v[248:251],v[44:47]
v_exp_f32 v197,v197
v_mfma_f32_16x16x32_bf16 v[236:239],a[152:155],v[216:219],v[236:239]
ds_read_b128 a[136:139],v1 offset:512
v_cvt_pk_bf16_f32 v196,v196,v197
v_mfma_f32_16x16x32_bf16 v[236:239],a[148:151],v[184:187],v[236:239]
v_exp_f32 v198,v198
v_mfma_f32_16x16x32_bf16 v[236:239],a[144:147],v[152:155],v[236:239]
ds_read_b128 a[140:143],v1 offset:768
v_mfma_f32_16x16x32_bf16 v[244:247],a[160:163],v[152:155],v[44:47]
s_add_u32 m0,s82,81920
v_exp_f32 v199,v199
v_mfma_f32_16x16x32_bf16 v[244:247],a[164:167],v[184:187],v[244:247]
ds_read_b128 a[156:159],v1 offset:4864
v_cvt_pk_bf16_f32 v197,v198,v199
v_mfma_f32_16x16x32_bf16 v[244:247],a[168:171],v[216:219],v[244:247]
v_exp_f32 v204,v204
v_mfma_f32_16x16x32_bf16 v[244:247],a[172:175],v[248:251],v[244:247]
buffer_load_dwordx4 v7,s[72:75],s80 offen lds
v_mfma_f32_16x16x32_bf16 v[252:255],a[188:191],v[248:251],v[44:47]
ds_read_b128 a[152:155],v1 offset:4608
v_mfma_f32_16x16x32_bf16 v[252:255],a[184:187],v[216:219],v[252:255]
v_exp_f32 v205,v205
v_mfma_f32_16x16x32_bf16 v[252:255],a[180:183],v[184:187],v[252:255]
ds_read_b128 a[148:151],v1 offset:4352
v_cvt_pk_bf16_f32 v198,v204,v205
v_mfma_f32_16x16x32_bf16 v[252:255],a[176:179],v[152:155],v[252:255]
buffer_store_dwordx4 v[96:99],v6,s[36:39],s86 offen sc1
v_mfma_f32_16x16x32_bf16 a[32:35],a[192:195],v[164:167],a[32:35]
ds_read_b128 a[144:147],v1 offset:4096
v_mfma_f32_16x16x32_bf16 a[32:35],a[196:199],v[180:183],a[32:35]
v_exp_f32 v206,v206
v_mfma_f32_16x16x32_bf16 a[36:39],a[204:207],v[180:183],a[36:39]
ds_read_b128 a[160:163],v1 offset:8192
v_mfma_f32_16x16x32_bf16 a[36:39],a[200:203],v[164:167],a[36:39]
v_exp_f32 v207,v207
v_mfma_f32_16x16x32_bf16 a[40:43],a[208:211],v[164:167],a[40:43]
ds_read_b128 a[164:167],v1 offset:8448
v_cvt_pk_bf16_f32 v199,v206,v207
v_mfma_f32_16x16x32_bf16 a[40:43],a[212:215],v[180:183],a[40:43]
v_exp_f32 v212,v212
v_mfma_f32_16x16x32_bf16 a[44:47],a[220:223],v[180:183],a[44:47]
ds_read_b128 a[168:171],v1 offset:8704
v_mfma_f32_16x16x32_bf16 a[44:47],a[216:219],v[164:167],a[44:47]
v_exp_f32 v213,v213
v_mfma_f32_16x16x32_bf16 a[48:51],a[224:227],v[164:167],a[48:51]
buffer_load_dwordx4 v8,s[72:75],s80 offen offset:1024 lds
v_mfma_f32_16x16x32_bf16 a[48:51],a[228:231],v[180:183],a[48:51]
ds_read_b128 a[172:175],v1 offset:8960
v_cvt_pk_bf16_f32 v212,v212,v213
v_mfma_f32_16x16x32_bf16 a[52:55],a[236:239],v[180:183],a[52:55]
v_exp_f32 v214,v214
v_mfma_f32_16x16x32_bf16 a[52:55],a[232:235],v[164:167],a[52:55]
ds_read_b128 a[188:191],v1 offset:13056
v_mfma_f32_16x16x32_bf16 a[56:59],a[240:243],v[164:167],a[56:59]
v_exp_f32 v215,v215
v_mfma_f32_16x16x32_bf16 a[56:59],a[244:247],v[180:183],a[56:59]
buffer_store_dwordx4 v[100:103],v6,s[36:39],s86 offen offset:1024 sc1
v_mfma_f32_16x16x32_bf16 a[60:63],a[252:255],v[180:183],a[60:63]
ds_read_b128 a[184:187],v1 offset:12800
v_cvt_pk_bf16_f32 v213,v214,v215
v_mfma_f32_16x16x32_bf16 a[60:63],a[248:251],v[164:167],a[60:63]
v_exp_f32 v220,v220
v_mfma_f32_16x16x32_bf16 v[52:55],v[28:31],v[164:167],v[52:55]
ds_read_b128 a[180:183],v1 offset:12544
v_mfma_f32_16x16x32_bf16 v[52:55],v[28:31],v[180:183],v[52:55]
s_waitcnt lgkmcnt(3)
v_exp_f32 v221,v221
v_mfma_f32_16x16x32_bf16 v[132:135],a[128:131],v[128:131],v[32:35]
ds_read_b128 a[176:179],v1 offset:12288
v_cvt_pk_bf16_f32 v214,v220,v221
v_mfma_f32_16x16x32_bf16 v[132:135],a[132:135],v[160:163],v[132:135]
v_exp_f32 v222,v222
v_mfma_f32_16x16x32_bf16 v[132:135],a[136:139],v[192:195],v[132:135]
v_exp_f32 v223,v223
v_mfma_f32_16x16x32_bf16 v[132:135],a[140:143],v[224:227],v[132:135]
buffer_load_dwordx4 v9,s[72:75],s80 offen offset:2048 lds
v_mfma_f32_16x16x32_bf16 v[140:143],a[156:159],v[224:227],v[32:35]
v_cvt_pk_bf16_f32 v215,v222,v223
v_add_u32 v1,s48,v1
v_mfma_f32_16x16x32_bf16 v[140:143],a[152:155],v[192:195],v[140:143]
v_exp_f32 v228,v228
v_mfma_f32_16x16x32_bf16 v[140:143],a[148:151],v[160:163],v[140:143]
v_exp_f32 v229,v229
v_mfma_f32_16x16x32_bf16 v[140:143],a[144:147],v[128:131],v[140:143]
v_exp_f32 v230,v230
v_mfma_f32_16x16x32_bf16 v[148:151],a[160:163],v[128:131],v[32:35]
v_exp_f32 v231,v231
v_mfma_f32_16x16x32_bf16 v[148:151],a[164:167],v[160:163],v[148:151]
v_cvt_pk_bf16_f32 v228,v228,v229
v_cvt_pk_bf16_f32 v229,v230,v231
v_mfma_f32_16x16x32_bf16 v[148:151],a[168:171],v[192:195],v[148:151]
v_exp_f32 v236,v236
v_mfma_f32_16x16x32_bf16 v[148:151],a[172:175],v[224:227],v[148:151]
s_waitcnt lgkmcnt(0)
buffer_store_dwordx4 v[104:107],v6,s[36:39],s86 offen offset:2048 sc1
v_mfma_f32_16x16x32_bf16 v[156:159],a[188:191],v[224:227],v[32:35]
v_exp_f32 v237,v237
v_mfma_f32_16x16x32_bf16 v[156:159],a[184:187],v[192:195],v[156:159]
v_exp_f32 v238,v238
v_mfma_f32_16x16x32_bf16 v[156:159],a[180:183],v[160:163],v[156:159]
v_exp_f32 v239,v239
v_mfma_f32_16x16x32_bf16 v[156:159],a[176:179],v[128:131],v[156:159]
v_exp_f32 v244,v244
v_mfma_f32_16x16x32_bf16 a[64:67],a[192:195],v[196:199],a[64:67]
buffer_load_dwordx4 v10,s[72:75],s80 offen offset:3072 lds
v_mfma_f32_16x16x32_bf16 a[64:67],a[196:199],v[212:215],a[64:67]
v_exp_f32 v245,v245
v_mfma_f32_16x16x32_bf16 a[68:71],a[204:207],v[212:215],a[68:71]
v_exp_f32 v246,v246
v_mfma_f32_16x16x32_bf16 a[68:71],a[200:203],v[196:199],a[68:71]
v_exp_f32 v247,v247
v_mfma_f32_16x16x32_bf16 a[72:75],a[208:211],v[196:199],a[72:75]
v_cvt_pk_bf16_f32 v230,v236,v237
v_cvt_pk_bf16_f32 v231,v238,v239
v_mfma_f32_16x16x32_bf16 a[72:75],a[212:215],v[212:215],a[72:75]
v_cvt_pk_bf16_f32 v244,v244,v245
v_cvt_pk_bf16_f32 v245,v246,v247
v_mfma_f32_16x16x32_bf16 a[76:79],a[220:223],v[212:215],a[76:79]
v_exp_f32 v252,v252
v_mfma_f32_16x16x32_bf16 a[76:79],a[216:219],v[196:199],a[76:79]
v_exp_f32 v253,v253
v_mfma_f32_16x16x32_bf16 a[80:83],a[224:227],v[196:199],a[80:83]
v_exp_f32 v254,v254
v_mfma_f32_16x16x32_bf16 a[80:83],a[228:231],v[212:215],a[80:83]
v_exp_f32 v255,v255
v_mfma_f32_16x16x32_bf16 a[84:87],a[236:239],v[212:215],a[84:87]
s_add_u32 m0,s82,86016
v_cvt_pk_bf16_f32 v246,v252,v253
v_cvt_pk_bf16_f32 v247,v254,v255
v_mfma_f32_16x16x32_bf16 a[84:87],a[232:235],v[196:199],a[84:87]
buffer_store_dwordx4 v[108:111],v6,s[36:39],s86 offen offset:3072 sc1
v_mfma_f32_16x16x32_bf16 a[88:91],a[240:243],v[196:199],a[88:91]
s_add_u32 s86,s55,73728
v_exp_f32 v132,v132
v_mfma_f32_16x16x32_bf16 a[88:91],a[244:247],v[212:215],a[88:91]
s_max_i32 s86,s86,s100
v_exp_f32 v133,v133
v_mfma_f32_16x16x32_bf16 a[92:95],a[252:255],v[212:215],a[92:95]
v_exp_f32 v134,v134
v_mfma_f32_16x16x32_bf16 a[92:95],a[248:251],v[196:199],a[92:95]
buffer_load_dwordx4 v11,s[72:75],s80 offen lds
v_mfma_f32_16x16x32_bf16 v[56:59],v[28:31],v[196:199],v[56:59]
v_exp_f32 v135,v135
v_mfma_f32_16x16x32_bf16 v[56:59],v[28:31],v[212:215],v[56:59]
v_cvt_pk_bf16_f32 v132,v132,v133
v_cvt_pk_bf16_f32 v133,v134,v135
v_mfma_f32_16x16x32_bf16 v[164:167],a[128:131],v[136:139],v[36:39]
v_exp_f32 v140,v140
v_mfma_f32_16x16x32_bf16 v[164:167],a[132:135],v[168:171],v[164:167]
v_exp_f32 v141,v141
v_mfma_f32_16x16x32_bf16 v[164:167],a[136:139],v[200:203],v[164:167]
v_exp_f32 v142,v142
v_mfma_f32_16x16x32_bf16 v[164:167],a[140:143],v[232:235],v[164:167]
v_exp_f32 v143,v143
v_mfma_f32_16x16x32_bf16 v[172:175],a[156:159],v[232:235],v[36:39]
v_exp_f32 v148,v148
v_mfma_f32_16x16x32_bf16 v[172:175],a[152:155],v[200:203],v[172:175]
s_add_u32 m0,s82,87040
v_exp_f32 v149,v149
v_mfma_f32_16x16x32_bf16 v[172:175],a[148:151],v[168:171],v[172:175]
v_exp_f32 v150,v150
v_mfma_f32_16x16x32_bf16 v[172:175],a[144:147],v[136:139],v[172:175]
v_exp_f32 v151,v151
v_mfma_f32_16x16x32_bf16 v[180:183],a[160:163],v[136:139],v[36:39]
buffer_load_dwordx4 v7,s[72:75],s83 offen lds
v_mfma_f32_16x16x32_bf16 v[180:183],a[164:167],v[168:171],v[180:183]
v_cvt_pk_bf16_f32 v134,v140,v141
v_cvt_pk_bf16_f32 v135,v142,v143
v_mfma_f32_16x16x32_bf16 v[180:183],a[168:171],v[200:203],v[180:183]
v_cvt_pk_bf16_f32 v148,v148,v149
v_cvt_pk_bf16_f32 v149,v150,v151
v_mfma_f32_16x16x32_bf16 v[180:183],a[172:175],v[232:235],v[180:183]
v_exp_f32 v156,v156
v_mfma_f32_16x16x32_bf16 v[188:191],a[188:191],v[232:235],v[36:39]
buffer_store_dwordx4 v[112:115],v6,s[36:39],s87 offen sc1
v_mfma_f32_16x16x32_bf16 v[188:191],a[184:187],v[200:203],v[188:191]
v_exp_f32 v157,v157
v_mfma_f32_16x16x32_bf16 v[188:191],a[180:183],v[168:171],v[188:191]
v_exp_f32 v158,v158
v_mfma_f32_16x16x32_bf16 v[188:191],a[176:179],v[136:139],v[188:191]
v_exp_f32 v159,v159
v_mfma_f32_16x16x32_bf16 a[96:99],a[192:195],v[228:231],a[96:99]
ds_read_b64_tr_b16 a[192:193],v2
ds_read_b64_tr_b16 a[194:195],v2 offset:4096
v_mfma_f32_16x16x32_bf16 a[96:99],a[196:199],v[244:247],a[96:99]
ds_read_b64_tr_b16 a[196:197],v2 offset:8192
v_cvt_pk_bf16_f32 v150,v156,v157
v_mfma_f32_16x16x32_bf16 a[100:103],a[204:207],v[244:247],a[100:103]
ds_read_b64_tr_b16 a[198:199],v2 offset:12288
v_cvt_pk_bf16_f32 v151,v158,v159
v_mfma_f32_16x16x32_bf16 a[100:103],a[200:203],v[228:231],a[100:103]
ds_read_b64_tr_b16 a[204:205],v3 offset:8192
v_add_u32 v1,v4,v5
v_mfma_f32_16x16x32_bf16 a[104:107],a[208:211],v[228:231],a[104:107]
ds_read_b64_tr_b16 a[206:207],v3 offset:12288
ds_read_b64_tr_b16 a[200:201],v3
v_mfma_f32_16x16x32_bf16 a[104:107],a[212:215],v[244:247],a[104:107]
buffer_load_dwordx4 v8,s[72:75],s83 offen offset:1024 lds
v_mfma_f32_16x16x32_bf16 a[108:111],a[220:223],v[244:247],a[108:111]
ds_read_b64_tr_b16 a[202:203],v3 offset:4096
ds_read_b64_tr_b16 a[208:209],v2 offset:256
v_mfma_f32_16x16x32_bf16 a[108:111],a[216:219],v[228:231],a[108:111]
ds_read_b64_tr_b16 a[210:211],v2 offset:4352
v_mfma_f32_16x16x32_bf16 a[112:115],a[224:227],v[228:231],a[112:115]
ds_read_b64_tr_b16 a[212:213],v2 offset:8448
v_mfma_f32_16x16x32_bf16 a[112:115],a[228:231],v[244:247],a[112:115]
buffer_store_dwordx4 v[116:119],v6,s[36:39],s87 offen offset:1024 sc1
v_mfma_f32_16x16x32_bf16 a[116:119],a[236:239],v[244:247],a[116:119]
ds_read_b64_tr_b16 a[214:215],v2 offset:12544
ds_read_b64_tr_b16 a[220:221],v3 offset:8448
v_mfma_f32_16x16x32_bf16 a[116:119],a[232:235],v[228:231],a[116:119]
ds_read_b64_tr_b16 a[222:223],v3 offset:12544
v_mfma_f32_16x16x32_bf16 a[120:123],a[240:243],v[228:231],a[120:123]
ds_read_b64_tr_b16 a[216:217],v3 offset:256
)A"
R"A(v_mfma_f32_16x16x32_bf16 a[120:123],a[244:247],v[244:247],a[120:123]
ds_read_b64_tr_b16 a[218:219],v3 offset:4352
v_mfma_f32_16x16x32_bf16 a[124:127],a[252:255],v[244:247],a[124:127]
ds_read_b64_tr_b16 a[224:225],v2 offset:512
ds_read_b64_tr_b16 a[226:227],v2 offset:4608
v_mfma_f32_16x16x32_bf16 a[124:127],a[248:251],v[228:231],a[124:127]
ds_read_b64_tr_b16 a[228:229],v2 offset:8704
v_mfma_f32_16x16x32_bf16 v[60:63],v[28:31],v[228:231],v[60:63]
ds_read_b64_tr_b16 a[230:231],v2 offset:12800
v_mfma_f32_16x16x32_bf16 v[60:63],v[28:31],v[244:247],v[60:63]
ds_read_b64_tr_b16 a[236:237],v3 offset:8704
v_mfma_f32_16x16x32_bf16 v[196:199],a[128:131],v[144:147],v[40:43]
buffer_load_dwordx4 v9,s[72:75],s83 offen offset:2048 lds
v_mfma_f32_16x16x32_bf16 v[196:199],a[132:135],v[176:179],v[196:199]
ds_read_b64_tr_b16 a[238:239],v3 offset:12800
ds_read_b64_tr_b16 a[232:233],v3 offset:512
v_mfma_f32_16x16x32_bf16 v[196:199],a[136:139],v[208:211],v[196:199]
ds_read_b64_tr_b16 a[234:235],v3 offset:4608
v_mfma_f32_16x16x32_bf16 v[196:199],a[140:143],v[240:243],v[196:199]
ds_read_b64_tr_b16 a[240:241],v2 offset:768
v_mfma_f32_16x16x32_bf16 v[204:207],a[156:159],v[240:243],v[40:43]
ds_read_b64_tr_b16 a[242:243],v2 offset:4864
v_mfma_f32_16x16x32_bf16 v[204:207],a[152:155],v[208:211],v[204:207]
ds_read_b64_tr_b16 a[244:245],v2 offset:8960
ds_read_b64_tr_b16 a[246:247],v2 offset:13056
v_mfma_f32_16x16x32_bf16 v[204:207],a[148:151],v[176:179],v[204:207]
ds_read_b64_tr_b16 a[252:253],v3 offset:8960
v_sub_u32 v2,v4,v5
v_mfma_f32_16x16x32_bf16 v[204:207],a[144:147],v[144:147],v[204:207]
buffer_store_dwordx4 v[120:123],v6,s[36:39],s87 offen offset:2048 sc1
v_mfma_f32_16x16x32_bf16 v[212:215],a[160:163],v[144:147],v[40:43]
ds_read_b64_tr_b16 a[254:255],v3 offset:13056
ds_read_b64_tr_b16 a[248:249],v3 offset:768
v_mfma_f32_16x16x32_bf16 v[212:215],a[164:167],v[176:179],v[212:215]
ds_read_b64_tr_b16 a[250:251],v3 offset:4864
v_mfma_f32_16x16x32_bf16 v[212:215],a[168:171],v[208:211],v[212:215]
v_exp_f32 v164,v164
v_mfma_f32_16x16x32_bf16 v[212:215],a[172:175],v[240:243],v[212:215]
v_exp_f32 v165,v165
v_mfma_f32_16x16x32_bf16 v[220:223],a[188:191],v[240:243],v[40:43]
v_exp_f32 v166,v166
v_mfma_f32_16x16x32_bf16 v[220:223],a[184:187],v[208:211],v[220:223]
buffer_load_dwordx4 v10,s[72:75],s83 offen offset:3072 lds
v_mfma_f32_16x16x32_bf16 v[220:223],a[180:183],v[176:179],v[220:223]
v_exp_f32 v167,v167
v_mfma_f32_16x16x32_bf16 v[220:223],a[176:179],v[144:147],v[220:223]
s_waitcnt lgkmcnt(3)
v_cvt_pk_bf16_f32 v164,v164,v165
v_cvt_pk_bf16_f32 v165,v166,v167
v_mfma_f32_16x16x32_bf16 a[0:3],a[192:195],v[132:135],a[0:3]
v_exp_f32 v172,v172
v_mfma_f32_16x16x32_bf16 a[0:3],a[196:199],v[148:151],a[0:3]
v_exp_f32 v173,v173
v_mfma_f32_16x16x32_bf16 a[4:7],a[204:207],v[148:151],a[4:7]
v_exp_f32 v174,v174
v_mfma_f32_16x16x32_bf16 a[4:7],a[200:203],v[132:135],a[4:7]
v_exp_f32 v175,v175
v_mfma_f32_16x16x32_bf16 a[8:11],a[208:211],v[132:135],a[8:11]
v_exp_f32 v180,v180
v_mfma_f32_16x16x32_bf16 a[8:11],a[212:215],v[148:151],a[8:11]
v_exp_f32 v181,v181
v_mfma_f32_16x16x32_bf16 a[12:15],a[220:223],v[148:151],a[12:15]
v_exp_f32 v182,v182
v_mfma_f32_16x16x32_bf16 a[12:15],a[216:219],v[132:135],a[12:15]
s_add_u32 m0,s82,91136
buffer_store_dwordx4 v[124:127],v6,s[36:39],s87 offen offset:3072 sc1
v_mfma_f32_16x16x32_bf16 a[16:19],a[224:227],v[132:135],a[16:19]
s_add_u32 s87,s55,77824
v_exp_f32 v183,v183
v_mfma_f32_16x16x32_bf16 a[16:19],a[228:231],v[148:151],a[16:19]
s_max_i32 s87,s87,s101
v_cvt_pk_bf16_f32 v166,v172,v173
v_cvt_pk_bf16_f32 v167,v174,v175
v_mfma_f32_16x16x32_bf16 a[20:23],a[236:239],v[148:151],a[20:23]
v_cvt_pk_bf16_f32 v180,v180,v181
v_cvt_pk_bf16_f32 v181,v182,v183
v_mfma_f32_16x16x32_bf16 a[20:23],a[232:235],v[132:135],a[20:23]
buffer_load_dwordx4 v11,s[72:75],s83 offen lds
v_mfma_f32_16x16x32_bf16 a[24:27],a[240:243],v[132:135],a[24:27]
v_exp_f32 v188,v188
v_mfma_f32_16x16x32_bf16 a[24:27],a[244:247],v[148:151],a[24:27]
s_waitcnt lgkmcnt(0)
v_exp_f32 v189,v189
v_mfma_f32_16x16x32_bf16 a[28:31],a[252:255],v[148:151],a[28:31]
v_exp_f32 v190,v190
v_mfma_f32_16x16x32_bf16 a[28:31],a[248:251],v[132:135],a[28:31]
v_exp_f32 v191,v191
v_mfma_f32_16x16x32_bf16 v[48:51],v[28:31],v[132:135],v[48:51]
s_waitcnt vmcnt(19)
v_cvt_pk_bf16_f32 v182,v188,v189
v_cvt_pk_bf16_f32 v183,v190,v191
v_mfma_f32_16x16x32_bf16 v[48:51],v[28:31],v[148:151],v[48:51]
s_barrier
s_cmp_ge_u32 1,s71
s_cselect_b32 s80,4,2
v_mfma_f32_16x16x32_bf16 v[228:231],a[128:131],v[152:155],v[44:47]
s_mul_i32 s80,s80,163840
s_add_u32 s80,s80,s81
v_accvgpr_read_b32 v132,a0
v_mfma_f32_16x16x32_bf16 v[228:231],a[132:135],v[184:187],v[228:231]
s_add_u32 s83,s80,20480
v_exp_f32 v196,v196
v_mfma_f32_16x16x32_bf16 v[228:231],a[136:139],v[216:219],v[228:231]
v_exp_f32 v197,v197
v_mfma_f32_16x16x32_bf16 v[228:231],a[140:143],v[248:251],v[228:231]
v_exp_f32 v198,v198
v_mfma_f32_16x16x32_bf16 v[236:239],a[156:159],v[248:251],v[44:47]
v_exp_f32 v199,v199
v_mfma_f32_16x16x32_bf16 v[236:239],a[152:155],v[216:219],v[236:239]
v_cvt_pk_bf16_f32 v196,v196,v197
v_cvt_pk_bf16_f32 v197,v198,v199
v_mfma_f32_16x16x32_bf16 v[236:239],a[148:151],v[184:187],v[236:239]
v_exp_f32 v204,v204
v_mfma_f32_16x16x32_bf16 v[236:239],a[144:147],v[152:155],v[236:239]
v_exp_f32 v205,v205
v_mfma_f32_16x16x32_bf16 v[244:247],a[160:163],v[152:155],v[44:47]
v_exp_f32 v206,v206
v_mfma_f32_16x16x32_bf16 v[244:247],a[164:167],v[184:187],v[244:247]
v_exp_f32 v207,v207
v_mfma_f32_16x16x32_bf16 v[244:247],a[168:171],v[216:219],v[244:247]
v_exp_f32 v212,v212
v_mfma_f32_16x16x32_bf16 v[244:247],a[172:175],v[248:251],v[244:247]
v_exp_f32 v213,v213
v_mfma_f32_16x16x32_bf16 v[252:255],a[188:191],v[248:251],v[44:47]
v_exp_f32 v214,v214
v_mfma_f32_16x16x32_bf16 v[252:255],a[184:187],v[216:219],v[252:255]
v_exp_f32 v215,v215
v_mfma_f32_16x16x32_bf16 v[252:255],a[180:183],v[184:187],v[252:255]
v_cvt_pk_bf16_f32 v198,v204,v205
v_cvt_pk_bf16_f32 v199,v206,v207
v_mfma_f32_16x16x32_bf16 v[252:255],a[176:179],v[152:155],v[252:255]
v_cvt_pk_bf16_f32 v212,v212,v213
v_cvt_pk_bf16_f32 v213,v214,v215
v_mfma_f32_16x16x32_bf16 a[32:35],a[192:195],v[164:167],a[32:35]
v_exp_f32 v220,v220
v_mfma_f32_16x16x32_bf16 a[32:35],a[196:199],v[180:183],a[32:35]
s_add_u32 m0,s82,122880
v_exp_f32 v221,v221
v_mfma_f32_16x16x32_bf16 a[36:39],a[204:207],v[180:183],a[36:39]
v_exp_f32 v222,v222
v_mfma_f32_16x16x32_bf16 a[36:39],a[200:203],v[164:167],a[36:39]
v_exp_f32 v223,v223
v_mfma_f32_16x16x32_bf16 a[40:43],a[208:211],v[164:167],a[40:43]
buffer_load_dwordx4 v7,s[72:75],s80 offen lds
v_mfma_f32_16x16x32_bf16 a[40:43],a[212:215],v[180:183],a[40:43]
v_cvt_pk_bf16_f32 v214,v220,v221
v_cvt_pk_bf16_f32 v215,v222,v223
v_mfma_f32_16x16x32_bf16 a[44:47],a[220:223],v[180:183],a[44:47]
v_max_f32 v20,0x00800000,v48
v_accvgpr_read_b32 v133,a1
v_mfma_f32_16x16x32_bf16 a[44:47],a[216:219],v[164:167],a[44:47]
v_rcp_f32 v20,v20
v_mfma_f32_16x16x32_bf16 a[48:51],a[224:227],v[164:167],a[48:51]
v_mul_f32 v132,v20,v132
v_mul_f32 v133,v20,v133
v_mfma_f32_16x16x32_bf16 a[48:51],a[228:231],v[180:183],a[48:51]
v_accvgpr_read_b32 v134,a2
v_accvgpr_read_b32 v135,a3
v_mfma_f32_16x16x32_bf16 a[52:55],a[236:239],v[180:183],a[52:55]
v_cvt_pk_bf16_f32 v132,v132,v133
v_mul_f32 v134,v20,v134
v_mfma_f32_16x16x32_bf16 a[52:55],a[232:235],v[164:167],a[52:55]
v_mul_f32 v135,v20,v135
v_cvt_pk_bf16_f32 v133,v134,v135
v_mfma_f32_16x16x32_bf16 a[56:59],a[240:243],v[164:167],a[56:59]
v_exp_f32 v228,v228
v_mfma_f32_16x16x32_bf16 a[56:59],a[244:247],v[180:183],a[56:59]
v_exp_f32 v229,v229
v_mfma_f32_16x16x32_bf16 a[60:63],a[252:255],v[180:183],a[60:63]
v_accvgpr_read_b32 v134,a4
v_accvgpr_read_b32 v135,a5
v_mfma_f32_16x16x32_bf16 a[60:63],a[248:251],v[164:167],a[60:63]
v_exp_f32 v230,v230
v_mfma_f32_16x16x32_bf16 v[52:55],v[28:31],v[164:167],v[52:55]
v_exp_f32 v231,v231
v_mfma_f32_16x16x32_bf16 v[52:55],v[28:31],v[180:183],v[52:55]
v_accvgpr_read_b32 v16,a6
v_accvgpr_read_b32 v17,a7
v_mfma_f32_16x16x32_bf16 a[64:67],a[192:195],v[196:199],a[64:67]
v_mul_f32 v134,v20,v134
v_mul_f32 v135,v20,v135
v_mfma_f32_16x16x32_bf16 a[64:67],a[196:199],v[212:215],a[64:67]
v_cvt_pk_bf16_f32 v228,v228,v229
v_mul_f32 v16,v20,v16
v_mfma_f32_16x16x32_bf16 a[68:71],a[204:207],v[212:215],a[68:71]
buffer_load_dwordx4 v8,s[72:75],s80 offen offset:1024 lds
v_mfma_f32_16x16x32_bf16 a[68:71],a[200:203],v[196:199],a[68:71]
v_exp_f32 v236,v236
v_mfma_f32_16x16x32_bf16 a[72:75],a[208:211],v[196:199],a[72:75]
s_lshl_b32 s95,s42,2
s_add_u32 s95,s95,s40
s_lshl_b32 s95,s95,4
v_mfma_f32_16x16x32_bf16 a[72:75],a[212:215],v[212:215],a[72:75]
v_mov_b32 v18,s68
v_mov_b32 v19,s69
v_mfma_f32_16x16x32_bf16 a[76:79],a[220:223],v[212:215],a[76:79]
v_exp_f32 v237,v237
v_mfma_f32_16x16x32_bf16 a[76:79],a[216:219],v[196:199],a[76:79]
v_exp_f32 v238,v238
v_mfma_f32_16x16x32_bf16 a[80:83],a[224:227],v[196:199],a[80:83]
v_exp_f32 v239,v239
v_mfma_f32_16x16x32_bf16 a[80:83],a[228:231],v[212:215],a[80:83]
v_exp_f32 v244,v244
v_mfma_f32_16x16x32_bf16 a[84:87],a[236:239],v[212:215],a[84:87]
v_exp_f32 v245,v245
v_mfma_f32_16x16x32_bf16 a[84:87],a[232:235],v[196:199],a[84:87]
v_mul_f32 v17,v20,v17
v_cvt_pk_bf16_f32 v134,v134,v135
v_mfma_f32_16x16x32_bf16 a[88:91],a[240:243],v[196:199],a[88:91]
s_waitcnt vmcnt(3)
s_mov_b64 exec,1
buffer_store_dwordx2 v[18:19],v6,s[64:67],s95 offen sc1
s_mov_b64 exec,-1
v_mfma_f32_16x16x32_bf16 a[88:91],a[244:247],v[212:215],a[88:91]
v_cvt_pk_bf16_f32 v229,v230,v231
v_cvt_pk_bf16_f32 v135,v16,v17
v_mfma_f32_16x16x32_bf16 a[92:95],a[252:255],v[212:215],a[92:95]
v_exp_f32 v246,v246
v_mfma_f32_16x16x32_bf16 a[92:95],a[248:251],v[196:199],a[92:95]
v_exp_f32 v247,v247
v_cvt_pk_bf16_f32 v230,v236,v237
v_cvt_pk_bf16_f32 v231,v238,v239
v_cvt_pk_bf16_f32 v244,v244,v245
v_exp_f32 v252,v252
v_exp_f32 v253,v253
v_exp_f32 v254,v254
v_exp_f32 v255,v255
v_mfma_f32_16x16x32_bf16 v[56:59],v[28:31],v[196:199],v[56:59]
v_cvt_pk_bf16_f32 v245,v246,v247
v_cvt_pk_bf16_f32 v246,v252,v253
v_cvt_pk_bf16_f32 v247,v254,v255
v_mfma_f32_16x16x32_bf16 v[56:59],v[28:31],v[212:215],v[56:59]
buffer_store_dwordx4 v[132:135],v6,s[36:39],s84 offen sc1
v_mfma_f32_16x16x32_bf16 a[96:99],a[192:195],v[228:231],a[96:99]
v_accvgpr_read_b32 v140,a8
v_accvgpr_read_b32 v141,a9
v_mfma_f32_16x16x32_bf16 a[96:99],a[196:199],v[244:247],a[96:99]
v_mul_f32 v140,v20,v140
v_mul_f32 v141,v20,v141
v_mfma_f32_16x16x32_bf16 a[100:103],a[204:207],v[244:247],a[100:103]
v_accvgpr_read_b32 v142,a10
v_accvgpr_read_b32 v143,a11
v_mfma_f32_16x16x32_bf16 a[100:103],a[200:203],v[228:231],a[100:103]
buffer_load_dwordx4 v9,s[72:75],s80 offen offset:2048 lds
v_mfma_f32_16x16x32_bf16 a[104:107],a[208:211],v[228:231],a[104:107]
v_cvt_pk_bf16_f32 v140,v140,v141
v_mul_f32 v142,v20,v142
v_mfma_f32_16x16x32_bf16 a[104:107],a[212:215],v[244:247],a[104:107]
v_mul_f32 v143,v20,v143
v_cvt_pk_bf16_f32 v141,v142,v143
v_mfma_f32_16x16x32_bf16 a[108:111],a[220:223],v[244:247],a[108:111]
v_accvgpr_read_b32 v142,a12
v_accvgpr_read_b32 v143,a13
v_mfma_f32_16x16x32_bf16 a[108:111],a[216:219],v[228:231],a[108:111]
v_accvgpr_read_b32 v12,a14
v_accvgpr_read_b32 v13,a15
v_mfma_f32_16x16x32_bf16 a[112:115],a[224:227],v[228:231],a[112:115]
v_mul_f32 v142,v20,v142
v_mul_f32 v143,v20,v143
v_mfma_f32_16x16x32_bf16 a[112:115],a[228:231],v[244:247],a[112:115]
ds_read_b64_tr_b16 a[128:129],v1
ds_read_b64_tr_b16 a[130:131],v1 offset:5120
v_mfma_f32_16x16x32_bf16 a[116:119],a[236:239],v[244:247],a[116:119]
ds_read_b64_tr_b16 a[132:133],v1 offset:10240
ds_read_b64_tr_b16 a[134:135],v1 offset:15360
v_mfma_f32_16x16x32_bf16 a[116:119],a[232:235],v[228:231],a[116:119]
ds_read_b64_tr_b16 a[136:137],v1 offset:20480
ds_read_b64_tr_b16 a[138:139],v1 offset:25600
v_mfma_f32_16x16x32_bf16 a[120:123],a[240:243],v[228:231],a[120:123]
ds_read_b64_tr_b16 a[140:141],v1 offset:30720
ds_read_b64_tr_b16 a[142:143],v1 offset:35840
v_mfma_f32_16x16x32_bf16 a[120:123],a[244:247],v[244:247],a[120:123]
v_mfma_f32_16x16x32_bf16 a[124:127],a[252:255],v[244:247],a[124:127]
v_mul_f32 v12,v20,v12
v_mul_f32 v13,v20,v13
v_mfma_f32_16x16x32_bf16 a[124:127],a[248:251],v[228:231],a[124:127]
)A"
R"A(v_cvt_pk_bf16_f32 v142,v142,v143
v_cvt_pk_bf16_f32 v143,v12,v13
v_mfma_f32_16x16x32_bf16 v[60:63],v[28:31],v[228:231],v[60:63]
v_accvgpr_read_b32 v148,a16
v_accvgpr_read_b32 v149,a17
v_mfma_f32_16x16x32_bf16 v[60:63],v[28:31],v[244:247],v[60:63]
s_waitcnt lgkmcnt(6)
buffer_store_dwordx4 v[140:143],v6,s[36:39],s84 offen offset:1024 sc1
v_mfma_f32_16x16x32_bf16 a[192:195],a[128:131],v[64:67],0
s_waitcnt lgkmcnt(4)
v_mul_f32 v148,v20,v148
v_mul_f32 v149,v20,v149
v_mfma_f32_16x16x32_bf16 a[192:195],a[132:135],v[68:71],a[192:195]
s_waitcnt lgkmcnt(2)
v_accvgpr_read_b32 v150,a18
v_accvgpr_read_b32 v151,a19
v_mfma_f32_16x16x32_bf16 a[192:195],a[136:139],v[72:75],a[192:195]
s_waitcnt lgkmcnt(0)
v_cvt_pk_bf16_f32 v148,v148,v149
v_mul_f32 v150,v20,v150
v_mfma_f32_16x16x32_bf16 a[192:195],a[140:143],v[76:79],a[192:195]
buffer_load_dwordx4 v10,s[72:75],s80 offen offset:3072 lds
v_mfma_f32_16x16x32_bf16 a[196:199],a[140:143],v[92:95],0
v_mul_f32 v151,v20,v151
v_cvt_pk_bf16_f32 v149,v150,v151
v_mfma_f32_16x16x32_bf16 a[196:199],a[136:139],v[88:91],a[196:199]
v_accvgpr_read_b32 v150,a20
v_accvgpr_read_b32 v151,a21
v_mfma_f32_16x16x32_bf16 a[196:199],a[132:135],v[84:87],a[196:199]
v_accvgpr_read_b32 v14,a22
v_accvgpr_read_b32 v15,a23
v_mfma_f32_16x16x32_bf16 a[196:199],a[128:131],v[80:83],a[196:199]
ds_read_b64_tr_b16 a[144:145],v2 offset:32
ds_read_b64_tr_b16 a[146:147],v2 offset:5152
v_mfma_f32_16x16x32_bf16 a[200:203],a[128:131],v[96:99],0
ds_read_b64_tr_b16 a[148:149],v2 offset:10272
ds_read_b64_tr_b16 a[150:151],v2 offset:15392
v_mfma_f32_16x16x32_bf16 a[200:203],a[132:135],v[100:103],a[200:203]
ds_read_b64_tr_b16 a[152:153],v2 offset:20512
ds_read_b64_tr_b16 a[154:155],v2 offset:25632
v_mfma_f32_16x16x32_bf16 a[200:203],a[136:139],v[104:107],a[200:203]
ds_read_b64_tr_b16 a[156:157],v2 offset:30752
ds_read_b64_tr_b16 a[158:159],v2 offset:35872
v_mfma_f32_16x16x32_bf16 a[200:203],a[140:143],v[108:111],a[200:203]
v_mfma_f32_16x16x32_bf16 a[204:207],a[140:143],v[124:127],0
s_add_u32 m0,s82,126976
v_mul_f32 v150,v20,v150
v_mul_f32 v151,v20,v151
v_mfma_f32_16x16x32_bf16 a[204:207],a[136:139],v[120:123],a[204:207]
v_mul_f32 v14,v20,v14
v_mul_f32 v15,v20,v15
v_mfma_f32_16x16x32_bf16 a[204:207],a[132:135],v[116:119],a[204:207]
v_cvt_pk_bf16_f32 v150,v150,v151
v_cvt_pk_bf16_f32 v151,v14,v15
v_mfma_f32_16x16x32_bf16 a[204:207],a[128:131],v[112:115],a[204:207]
s_waitcnt lgkmcnt(6)
v_accvgpr_read_b32 v156,a24
v_accvgpr_read_b32 v157,a25
v_mfma_f32_16x16x32_bf16 a[208:211],a[144:147],v[64:67],0
s_waitcnt lgkmcnt(4)
v_mul_f32 v156,v20,v156
v_mul_f32 v157,v20,v157
v_mfma_f32_16x16x32_bf16 a[208:211],a[148:151],v[68:71],a[208:211]
s_waitcnt lgkmcnt(2)
buffer_load_dwordx4 v11,s[72:75],s80 offen lds
v_mfma_f32_16x16x32_bf16 a[208:211],a[152:155],v[72:75],a[208:211]
s_waitcnt lgkmcnt(0)
v_accvgpr_read_b32 v158,a26
v_accvgpr_read_b32 v159,a27
v_mfma_f32_16x16x32_bf16 a[208:211],a[156:159],v[76:79],a[208:211]
v_cvt_pk_bf16_f32 v156,v156,v157
v_mul_f32 v158,v20,v158
v_mfma_f32_16x16x32_bf16 a[212:215],a[156:159],v[92:95],0
v_mul_f32 v159,v20,v159
v_cvt_pk_bf16_f32 v157,v158,v159
v_mfma_f32_16x16x32_bf16 a[212:215],a[152:155],v[88:91],a[212:215]
buffer_store_dwordx4 v[148:151],v6,s[36:39],s84 offen offset:2048 sc1
v_mfma_f32_16x16x32_bf16 a[212:215],a[148:151],v[84:87],a[212:215]
v_accvgpr_read_b32 v158,a28
v_accvgpr_read_b32 v159,a29
v_mfma_f32_16x16x32_bf16 a[212:215],a[144:147],v[80:83],a[212:215]
ds_read_b64_tr_b16 a[160:161],v1 offset:64
ds_read_b64_tr_b16 a[162:163],v1 offset:5184
v_mfma_f32_16x16x32_bf16 a[216:219],a[144:147],v[96:99],0
ds_read_b64_tr_b16 a[164:165],v1 offset:10304
ds_read_b64_tr_b16 a[166:167],v1 offset:15424
v_mfma_f32_16x16x32_bf16 a[216:219],a[148:151],v[100:103],a[216:219]
ds_read_b64_tr_b16 a[168:169],v1 offset:20544
ds_read_b64_tr_b16 a[170:171],v1 offset:25664
v_mfma_f32_16x16x32_bf16 a[216:219],a[152:155],v[104:107],a[216:219]
ds_read_b64_tr_b16 a[172:173],v1 offset:30784
ds_read_b64_tr_b16 a[174:175],v1 offset:35904
v_mfma_f32_16x16x32_bf16 a[216:219],a[156:159],v[108:111],a[216:219]
v_mfma_f32_16x16x32_bf16 a[220:223],a[156:159],v[124:127],0
s_add_u32 m0,s82,128000
v_accvgpr_read_b32 v16,a30
v_accvgpr_read_b32 v17,a31
v_mfma_f32_16x16x32_bf16 a[220:223],a[152:155],v[120:123],a[220:223]
v_mul_f32 v158,v20,v158
v_mul_f32 v159,v20,v159
v_mfma_f32_16x16x32_bf16 a[220:223],a[148:151],v[116:119],a[220:223]
v_mul_f32 v16,v20,v16
v_mul_f32 v17,v20,v17
v_mfma_f32_16x16x32_bf16 a[220:223],a[144:147],v[112:115],a[220:223]
s_waitcnt lgkmcnt(6)
v_cvt_pk_bf16_f32 v158,v158,v159
v_cvt_pk_bf16_f32 v159,v16,v17
v_mfma_f32_16x16x32_bf16 a[224:227],a[160:163],v[64:67],0
s_waitcnt lgkmcnt(4)
v_max_f32 v21,0x00800000,v52
v_accvgpr_read_b32 v164,a32
v_mfma_f32_16x16x32_bf16 a[224:227],a[164:167],v[68:71],a[224:227]
s_waitcnt lgkmcnt(2)
buffer_load_dwordx4 v7,s[72:75],s83 offen lds
v_mfma_f32_16x16x32_bf16 a[224:227],a[168:171],v[72:75],a[224:227]
s_waitcnt lgkmcnt(0)
v_rcp_f32 v21,v21
v_mfma_f32_16x16x32_bf16 a[224:227],a[172:175],v[76:79],a[224:227]
v_accvgpr_read_b32 v165,a33
v_mul_f32 v164,v21,v164
v_mfma_f32_16x16x32_bf16 a[228:231],a[172:175],v[92:95],0
v_mul_f32 v165,v21,v165
v_accvgpr_read_b32 v166,a34
v_mfma_f32_16x16x32_bf16 a[228:231],a[168:171],v[88:91],a[228:231]
v_accvgpr_read_b32 v167,a35
v_cvt_pk_bf16_f32 v164,v164,v165
v_mfma_f32_16x16x32_bf16 a[228:231],a[164:167],v[84:87],a[228:231]
v_mul_f32 v166,v21,v166
v_mul_f32 v167,v21,v167
v_mfma_f32_16x16x32_bf16 a[228:231],a[160:163],v[80:83],a[228:231]
ds_read_b64_tr_b16 a[176:177],v2 offset:96
ds_read_b64_tr_b16 a[178:179],v2 offset:5216
v_mfma_f32_16x16x32_bf16 a[232:235],a[160:163],v[96:99],0
ds_read_b64_tr_b16 a[180:181],v2 offset:10336
ds_read_b64_tr_b16 a[182:183],v2 offset:15456
v_mfma_f32_16x16x32_bf16 a[232:235],a[164:167],v[100:103],a[232:235]
ds_read_b64_tr_b16 a[184:185],v2 offset:20576
ds_read_b64_tr_b16 a[186:187],v2 offset:25696
v_mfma_f32_16x16x32_bf16 a[232:235],a[168:171],v[104:107],a[232:235]
ds_read_b64_tr_b16 a[188:189],v2 offset:30816
ds_read_b64_tr_b16 a[190:191],v2 offset:35936
v_mfma_f32_16x16x32_bf16 a[232:235],a[172:175],v[108:111],a[232:235]
v_mfma_f32_16x16x32_bf16 a[236:239],a[172:175],v[124:127],0
buffer_store_dwordx4 v[156:159],v6,s[36:39],s84 offen offset:3072 sc1
v_mfma_f32_16x16x32_bf16 a[236:239],a[168:171],v[120:123],a[236:239]
v_cvt_pk_bf16_f32 v165,v166,v167
v_accvgpr_read_b32 v166,a36
v_mfma_f32_16x16x32_bf16 a[236:239],a[164:167],v[116:119],a[236:239]
v_accvgpr_read_b32 v167,a37
v_accvgpr_read_b32 v12,a38
v_mfma_f32_16x16x32_bf16 a[236:239],a[160:163],v[112:115],a[236:239]
s_waitcnt lgkmcnt(6)
v_accvgpr_read_b32 v13,a39
v_mul_f32 v166,v21,v166
v_mfma_f32_16x16x32_bf16 a[240:243],a[176:179],v[64:67],0
s_waitcnt lgkmcnt(4)
v_mul_f32 v167,v21,v167
v_mul_f32 v12,v21,v12
v_mfma_f32_16x16x32_bf16 a[240:243],a[180:183],v[68:71],a[240:243]
s_waitcnt lgkmcnt(2)
buffer_load_dwordx4 v8,s[72:75],s83 offen offset:1024 lds
v_mfma_f32_16x16x32_bf16 a[240:243],a[184:187],v[72:75],a[240:243]
s_waitcnt lgkmcnt(0)
v_mul_f32 v13,v21,v13
v_cvt_pk_bf16_f32 v166,v166,v167
v_mfma_f32_16x16x32_bf16 a[240:243],a[188:191],v[76:79],a[240:243]
v_cvt_pk_bf16_f32 v167,v12,v13
v_accvgpr_read_b32 v172,a40
v_mfma_f32_16x16x32_bf16 a[244:247],a[188:191],v[92:95],0
v_accvgpr_read_b32 v173,a41
v_mul_f32 v172,v21,v172
v_mfma_f32_16x16x32_bf16 a[244:247],a[184:187],v[88:91],a[244:247]
v_mul_f32 v173,v21,v173
v_accvgpr_read_b32 v174,a42
v_mfma_f32_16x16x32_bf16 a[244:247],a[180:183],v[84:87],a[244:247]
v_accvgpr_read_b32 v175,a43
v_cvt_pk_bf16_f32 v172,v172,v173
v_mfma_f32_16x16x32_bf16 a[244:247],a[176:179],v[80:83],a[244:247]
ds_read_b64_tr_b16 a[128:129],v1 offset:128
ds_read_b64_tr_b16 a[130:131],v1 offset:5248
v_mfma_f32_16x16x32_bf16 a[248:251],a[176:179],v[96:99],0
ds_read_b64_tr_b16 a[132:133],v1 offset:10368
ds_read_b64_tr_b16 a[134:135],v1 offset:15488
v_mfma_f32_16x16x32_bf16 a[248:251],a[180:183],v[100:103],a[248:251]
ds_read_b64_tr_b16 a[136:137],v1 offset:20608
ds_read_b64_tr_b16 a[138:139],v1 offset:25728
v_mfma_f32_16x16x32_bf16 a[248:251],a[184:187],v[104:107],a[248:251]
ds_read_b64_tr_b16 a[140:141],v1 offset:30848
ds_read_b64_tr_b16 a[142:143],v1 offset:35968
v_mfma_f32_16x16x32_bf16 a[248:251],a[188:191],v[108:111],a[248:251]
v_mfma_f32_16x16x32_bf16 a[252:255],a[188:191],v[124:127],0
buffer_store_dwordx4 v[164:167],v6,s[36:39],s85 offen sc1
v_mfma_f32_16x16x32_bf16 a[252:255],a[184:187],v[120:123],a[252:255]
v_mul_f32 v174,v21,v174
v_mul_f32 v175,v21,v175
v_mfma_f32_16x16x32_bf16 a[252:255],a[180:183],v[116:119],a[252:255]
v_cvt_pk_bf16_f32 v173,v174,v175
v_accvgpr_read_b32 v174,a44
v_mfma_f32_16x16x32_bf16 a[252:255],a[176:179],v[112:115],a[252:255]
s_waitcnt lgkmcnt(6)
v_accvgpr_read_b32 v175,a45
v_accvgpr_read_b32 v14,a46
v_mfma_f32_16x16x32_bf16 a[0:3],a[128:131],v[64:67],0
s_waitcnt lgkmcnt(4)
v_accvgpr_read_b32 v15,a47
v_mul_f32 v174,v21,v174
v_mfma_f32_16x16x32_bf16 a[0:3],a[132:135],v[68:71],a[0:3]
s_waitcnt lgkmcnt(2)
buffer_load_dwordx4 v9,s[72:75],s83 offen offset:2048 lds
v_mfma_f32_16x16x32_bf16 a[0:3],a[136:139],v[72:75],a[0:3]
s_waitcnt lgkmcnt(0)
v_mul_f32 v175,v21,v175
v_mul_f32 v14,v21,v14
v_mfma_f32_16x16x32_bf16 a[0:3],a[140:143],v[76:79],a[0:3]
v_mul_f32 v15,v21,v15
v_cvt_pk_bf16_f32 v174,v174,v175
v_mfma_f32_16x16x32_bf16 a[4:7],a[140:143],v[92:95],0
v_cvt_pk_bf16_f32 v175,v14,v15
v_accvgpr_read_b32 v180,a48
v_mfma_f32_16x16x32_bf16 a[4:7],a[136:139],v[88:91],a[4:7]
v_accvgpr_read_b32 v181,a49
v_mul_f32 v180,v21,v180
v_mfma_f32_16x16x32_bf16 a[4:7],a[132:135],v[84:87],a[4:7]
s_lshl_b32 s94,s70,4
v_mul_f32 v181,v21,v181
v_accvgpr_read_b32 v182,a50
v_mfma_f32_16x16x32_bf16 a[4:7],a[128:131],v[80:83],a[4:7]
s_add_u32 s94,s94,s40
ds_read_b64_tr_b16 a[144:145],v2 offset:160
ds_read_b64_tr_b16 a[146:147],v2 offset:5280
v_mfma_f32_16x16x32_bf16 a[8:11],a[128:131],v[96:99],0
ds_read_b64_tr_b16 a[148:149],v2 offset:10400
ds_read_b64_tr_b16 a[150:151],v2 offset:15520
v_mfma_f32_16x16x32_bf16 a[8:11],a[132:135],v[100:103],a[8:11]
ds_read_b64_tr_b16 a[152:153],v2 offset:20640
ds_read_b64_tr_b16 a[154:155],v2 offset:25760
v_mfma_f32_16x16x32_bf16 a[8:11],a[136:139],v[104:107],a[8:11]
ds_read_b64_tr_b16 a[156:157],v2 offset:30880
ds_read_b64_tr_b16 a[158:159],v2 offset:36000
v_mfma_f32_16x16x32_bf16 a[8:11],a[140:143],v[108:111],a[8:11]
v_mfma_f32_16x16x32_bf16 a[12:15],a[140:143],v[124:127],0
s_lshl_b32 s94,s94,4
v_accvgpr_read_b32 v183,a51
v_cvt_pk_bf16_f32 v180,v180,v181
v_mfma_f32_16x16x32_bf16 a[12:15],a[136:139],v[120:123],a[12:15]
v_mul_f32 v182,v21,v182
v_mul_f32 v183,v21,v183
v_mfma_f32_16x16x32_bf16 a[12:15],a[132:135],v[116:119],a[12:15]
v_cvt_pk_bf16_f32 v181,v182,v183
v_accvgpr_read_b32 v182,a52
v_mfma_f32_16x16x32_bf16 a[12:15],a[128:131],v[112:115],a[12:15]
s_waitcnt lgkmcnt(6)
v_accvgpr_read_b32 v183,a53
v_accvgpr_read_b32 v16,a54
v_mfma_f32_16x16x32_bf16 a[16:19],a[144:147],v[64:67],0
s_waitcnt lgkmcnt(4)
buffer_store_dwordx4 v[172:175],v6,s[36:39],s85 offen offset:1024 sc1
v_mfma_f32_16x16x32_bf16 a[16:19],a[148:151],v[68:71],a[16:19]
s_waitcnt lgkmcnt(2)
v_accvgpr_read_b32 v17,a55
v_mul_f32 v182,v21,v182
v_mfma_f32_16x16x32_bf16 a[16:19],a[152:155],v[72:75],a[16:19]
s_waitcnt lgkmcnt(0)
v_mul_f32 v183,v21,v183
v_mul_f32 v16,v21,v16
v_mfma_f32_16x16x32_bf16 a[16:19],a[156:159],v[76:79],a[16:19]
v_mul_f32 v17,v21,v17
v_cvt_pk_bf16_f32 v182,v182,v183
v_mfma_f32_16x16x32_bf16 a[20:23],a[156:159],v[92:95],0
buffer_load_dwordx4 v10,s[72:75],s83 offen offset:3072 lds
v_mfma_f32_16x16x32_bf16 a[20:23],a[152:155],v[88:91],a[20:23]
v_cvt_pk_bf16_f32 v183,v16,v17
v_accvgpr_read_b32 v188,a56
v_mfma_f32_16x16x32_bf16 a[20:23],a[148:151],v[84:87],a[20:23]
v_accvgpr_read_b32 v189,a57
v_mul_f32 v188,v21,v188
v_mfma_f32_16x16x32_bf16 a[20:23],a[144:147],v[80:83],a[20:23]
ds_read_b64_tr_b16 a[160:161],v1 offset:192
ds_read_b64_tr_b16 a[162:163],v1 offset:5312
v_mfma_f32_16x16x32_bf16 a[24:27],a[144:147],v[96:99],0
ds_read_b64_tr_b16 a[164:165],v1 offset:10432
ds_read_b64_tr_b16 a[166:167],v1 offset:15552
)A"
R"A(v_mfma_f32_16x16x32_bf16 a[24:27],a[148:151],v[100:103],a[24:27]
ds_read_b64_tr_b16 a[168:169],v1 offset:20672
ds_read_b64_tr_b16 a[170:171],v1 offset:25792
v_mfma_f32_16x16x32_bf16 a[24:27],a[152:155],v[104:107],a[24:27]
ds_read_b64_tr_b16 a[172:173],v1 offset:30912
ds_read_b64_tr_b16 a[174:175],v1 offset:36032
v_mfma_f32_16x16x32_bf16 a[24:27],a[156:159],v[108:111],a[24:27]
v_mfma_f32_16x16x32_bf16 a[28:31],a[156:159],v[124:127],0
s_add_u32 m0,s82,132096
v_mul_f32 v189,v21,v189
v_accvgpr_read_b32 v190,a58
v_mfma_f32_16x16x32_bf16 a[28:31],a[152:155],v[120:123],a[28:31]
v_accvgpr_read_b32 v191,a59
v_cvt_pk_bf16_f32 v188,v188,v189
v_mfma_f32_16x16x32_bf16 a[28:31],a[148:151],v[116:119],a[28:31]
v_mul_f32 v190,v21,v190
v_mul_f32 v191,v21,v191
v_mfma_f32_16x16x32_bf16 a[28:31],a[144:147],v[112:115],a[28:31]
s_waitcnt lgkmcnt(6)
v_cvt_pk_bf16_f32 v189,v190,v191
v_accvgpr_read_b32 v190,a60
v_mfma_f32_16x16x32_bf16 a[32:35],a[160:163],v[64:67],0
s_waitcnt lgkmcnt(4)
v_accvgpr_read_b32 v191,a61
v_accvgpr_read_b32 v12,a62
v_mfma_f32_16x16x32_bf16 a[32:35],a[164:167],v[68:71],a[32:35]
s_waitcnt lgkmcnt(2)
buffer_load_dwordx4 v11,s[72:75],s83 offen lds
v_mfma_f32_16x16x32_bf16 a[32:35],a[168:171],v[72:75],a[32:35]
s_waitcnt lgkmcnt(0)
v_accvgpr_read_b32 v13,a63
v_mul_f32 v190,v21,v190
v_mfma_f32_16x16x32_bf16 a[32:35],a[172:175],v[76:79],a[32:35]
v_mul_f32 v191,v21,v191
v_mul_f32 v12,v21,v12
v_mfma_f32_16x16x32_bf16 a[36:39],a[172:175],v[92:95],0
v_mul_f32 v13,v21,v13
v_cvt_pk_bf16_f32 v190,v190,v191
v_mfma_f32_16x16x32_bf16 a[36:39],a[168:171],v[88:91],a[36:39]
buffer_store_dwordx4 v[180:183],v6,s[36:39],s85 offen offset:2048 sc1
v_mfma_f32_16x16x32_bf16 a[36:39],a[164:167],v[84:87],a[36:39]
v_cvt_pk_bf16_f32 v191,v12,v13
v_max_f32 v22,0x00800000,v56
v_mfma_f32_16x16x32_bf16 a[36:39],a[160:163],v[80:83],a[36:39]
ds_read_b64_tr_b16 a[176:177],v2 offset:224
ds_read_b64_tr_b16 a[178:179],v2 offset:5344
v_mfma_f32_16x16x32_bf16 a[40:43],a[160:163],v[96:99],0
ds_read_b64_tr_b16 a[180:181],v2 offset:10464
ds_read_b64_tr_b16 a[182:183],v2 offset:15584
v_mfma_f32_16x16x32_bf16 a[40:43],a[164:167],v[100:103],a[40:43]
ds_read_b64_tr_b16 a[184:185],v2 offset:20704
ds_read_b64_tr_b16 a[186:187],v2 offset:25824
v_mfma_f32_16x16x32_bf16 a[40:43],a[168:171],v[104:107],a[40:43]
ds_read_b64_tr_b16 a[188:189],v2 offset:30944
ds_read_b64_tr_b16 a[190:191],v2 offset:36064
v_mfma_f32_16x16x32_bf16 a[40:43],a[172:175],v[108:111],a[40:43]
v_mfma_f32_16x16x32_bf16 a[44:47],a[172:175],v[124:127],0
v_rcp_f32 v22,v22
v_mfma_f32_16x16x32_bf16 a[44:47],a[168:171],v[120:123],a[44:47]
v_bfe_u32 v24,v6,4,2
v_min_u32 v24,2,v24
v_mfma_f32_16x16x32_bf16 a[44:47],a[164:167],v[116:119],a[44:47]
v_add_u32 v24,s71,v24
v_add_u32 v24,1,v24
v_mfma_f32_16x16x32_bf16 a[44:47],a[160:163],v[112:115],a[44:47]
s_waitcnt lgkmcnt(6)
v_and_b32 v24,3,v24
v_lshlrev_b32 v24,6,v24
v_mfma_f32_16x16x32_bf16 a[48:51],a[176:179],v[64:67],0
s_waitcnt lgkmcnt(4)
buffer_load_dwordx2 v[26:27],v24,s[64:67],s94 offen sc1
v_mfma_f32_16x16x32_bf16 a[48:51],a[180:183],v[68:71],a[48:51]
s_waitcnt lgkmcnt(2)
v_accvgpr_read_b32 v196,a64
v_accvgpr_read_b32 v197,a65
v_mfma_f32_16x16x32_bf16 a[48:51],a[184:187],v[72:75],a[48:51]
s_waitcnt lgkmcnt(0)
v_mul_f32 v196,v22,v196
v_mul_f32 v197,v22,v197
v_mfma_f32_16x16x32_bf16 a[48:51],a[188:191],v[76:79],a[48:51]
v_accvgpr_read_b32 v198,a66
v_accvgpr_read_b32 v199,a67
v_mfma_f32_16x16x32_bf16 a[52:55],a[188:191],v[92:95],0
v_cvt_pk_bf16_f32 v196,v196,v197
v_mul_f32 v198,v22,v198
v_mfma_f32_16x16x32_bf16 a[52:55],a[184:187],v[88:91],a[52:55]
v_mul_f32 v199,v22,v199
v_cvt_pk_bf16_f32 v197,v198,v199
v_mfma_f32_16x16x32_bf16 a[52:55],a[180:183],v[84:87],a[52:55]
v_accvgpr_read_b32 v198,a68
v_accvgpr_read_b32 v199,a69
v_mfma_f32_16x16x32_bf16 a[52:55],a[176:179],v[80:83],a[52:55]
ds_read_b64_tr_b16 a[128:129],v1 offset:256
ds_read_b64_tr_b16 a[130:131],v1 offset:5376
v_mfma_f32_16x16x32_bf16 a[56:59],a[176:179],v[96:99],0
ds_read_b64_tr_b16 a[132:133],v1 offset:10496
ds_read_b64_tr_b16 a[134:135],v1 offset:15616
v_mfma_f32_16x16x32_bf16 a[56:59],a[180:183],v[100:103],a[56:59]
ds_read_b64_tr_b16 a[136:137],v1 offset:20736
ds_read_b64_tr_b16 a[138:139],v1 offset:25856
v_mfma_f32_16x16x32_bf16 a[56:59],a[184:187],v[104:107],a[56:59]
ds_read_b64_tr_b16 a[140:141],v1 offset:30976
ds_read_b64_tr_b16 a[142:143],v1 offset:36096
v_mfma_f32_16x16x32_bf16 a[56:59],a[188:191],v[108:111],a[56:59]
v_mfma_f32_16x16x32_bf16 a[60:63],a[188:191],v[124:127],0
buffer_store_dwordx4 v[188:191],v6,s[36:39],s85 offen offset:3072 sc1
v_mfma_f32_16x16x32_bf16 a[60:63],a[184:187],v[120:123],a[60:63]
v_accvgpr_read_b32 v14,a70
v_accvgpr_read_b32 v15,a71
v_mfma_f32_16x16x32_bf16 a[60:63],a[180:183],v[116:119],a[60:63]
v_mul_f32 v198,v22,v198
v_mul_f32 v199,v22,v199
v_mfma_f32_16x16x32_bf16 a[60:63],a[176:179],v[112:115],a[60:63]
s_waitcnt lgkmcnt(6)
v_mul_f32 v14,v22,v14
v_mul_f32 v15,v22,v15
v_mfma_f32_16x16x32_bf16 a[64:67],a[128:131],v[64:67],0
s_waitcnt lgkmcnt(4)
v_cvt_pk_bf16_f32 v198,v198,v199
v_cvt_pk_bf16_f32 v199,v14,v15
v_mfma_f32_16x16x32_bf16 a[64:67],a[132:135],v[68:71],a[64:67]
s_waitcnt lgkmcnt(2)
v_accvgpr_read_b32 v204,a72
v_accvgpr_read_b32 v205,a73
v_mfma_f32_16x16x32_bf16 a[64:67],a[136:139],v[72:75],a[64:67]
s_waitcnt lgkmcnt(0)
v_mul_f32 v204,v22,v204
v_mul_f32 v205,v22,v205
v_mfma_f32_16x16x32_bf16 a[64:67],a[140:143],v[76:79],a[64:67]
v_accvgpr_read_b32 v206,a74
v_accvgpr_read_b32 v207,a75
v_mfma_f32_16x16x32_bf16 a[68:71],a[140:143],v[92:95],0
v_cvt_pk_bf16_f32 v204,v204,v205
v_mul_f32 v206,v22,v206
v_mfma_f32_16x16x32_bf16 a[68:71],a[136:139],v[88:91],a[68:71]
v_mul_f32 v207,v22,v207
v_cvt_pk_bf16_f32 v205,v206,v207
v_mfma_f32_16x16x32_bf16 a[68:71],a[132:135],v[84:87],a[68:71]
v_accvgpr_read_b32 v206,a76
v_accvgpr_read_b32 v207,a77
v_mfma_f32_16x16x32_bf16 a[68:71],a[128:131],v[80:83],a[68:71]
ds_read_b64_tr_b16 a[144:145],v2 offset:288
ds_read_b64_tr_b16 a[146:147],v2 offset:5408
v_mfma_f32_16x16x32_bf16 a[72:75],a[128:131],v[96:99],0
ds_read_b64_tr_b16 a[148:149],v2 offset:10528
ds_read_b64_tr_b16 a[150:151],v2 offset:15648
v_mfma_f32_16x16x32_bf16 a[72:75],a[132:135],v[100:103],a[72:75]
ds_read_b64_tr_b16 a[152:153],v2 offset:20768
ds_read_b64_tr_b16 a[154:155],v2 offset:25888
v_mfma_f32_16x16x32_bf16 a[72:75],a[136:139],v[104:107],a[72:75]
ds_read_b64_tr_b16 a[156:157],v2 offset:31008
ds_read_b64_tr_b16 a[158:159],v2 offset:36128
v_mfma_f32_16x16x32_bf16 a[72:75],a[140:143],v[108:111],a[72:75]
v_accvgpr_read_b32 v16,a78
v_accvgpr_read_b32 v17,a79
v_mfma_f32_16x16x32_bf16 a[76:79],a[140:143],v[124:127],0
v_mul_f32 v206,v22,v206
v_mul_f32 v207,v22,v207
v_mfma_f32_16x16x32_bf16 a[76:79],a[136:139],v[120:123],a[76:79]
v_mul_f32 v16,v22,v16
v_mul_f32 v17,v22,v17
v_mfma_f32_16x16x32_bf16 a[76:79],a[132:135],v[116:119],a[76:79]
v_cvt_pk_bf16_f32 v206,v206,v207
v_cvt_pk_bf16_f32 v207,v16,v17
v_mfma_f32_16x16x32_bf16 a[76:79],a[128:131],v[112:115],a[76:79]
s_waitcnt lgkmcnt(6)
v_accvgpr_read_b32 v212,a80
v_accvgpr_read_b32 v213,a81
v_accvgpr_read_b32 v214,a82
v_accvgpr_read_b32 v215,a83
v_mfma_f32_16x16x32_bf16 a[80:83],a[144:147],v[64:67],0
s_waitcnt lgkmcnt(4)
v_mul_f32 v212,v22,v212
v_mul_f32 v213,v22,v213
v_mfma_f32_16x16x32_bf16 a[80:83],a[148:151],v[68:71],a[80:83]
s_waitcnt lgkmcnt(2)
v_cvt_pk_bf16_f32 v212,v212,v213
v_mul_f32 v214,v22,v214
v_mfma_f32_16x16x32_bf16 a[80:83],a[152:155],v[72:75],a[80:83]
s_waitcnt lgkmcnt(0)
v_mul_f32 v215,v22,v215
v_cvt_pk_bf16_f32 v213,v214,v215
v_mfma_f32_16x16x32_bf16 a[80:83],a[156:159],v[76:79],a[80:83]
v_accvgpr_read_b32 v214,a84
v_accvgpr_read_b32 v215,a85
v_accvgpr_read_b32 v12,a86
v_accvgpr_read_b32 v13,a87
v_mfma_f32_16x16x32_bf16 a[84:87],a[156:159],v[92:95],0
v_mul_f32 v214,v22,v214
v_mul_f32 v215,v22,v215
v_mfma_f32_16x16x32_bf16 a[84:87],a[152:155],v[88:91],a[84:87]
v_mul_f32 v12,v22,v12
v_mul_f32 v13,v22,v13
v_mfma_f32_16x16x32_bf16 a[84:87],a[148:151],v[84:87],a[84:87]
v_add_u32 v1,40960,v1
v_cvt_pk_bf16_f32 v214,v214,v215
v_mfma_f32_16x16x32_bf16 a[84:87],a[144:147],v[80:83],a[84:87]
ds_read_b64_tr_b16 a[160:161],v1
ds_read_b64_tr_b16 a[162:163],v1 offset:5120
v_accvgpr_read_b32 v220,a88
v_accvgpr_read_b32 v221,a89
v_accvgpr_read_b32 v222,a90
v_accvgpr_read_b32 v223,a91
v_mfma_f32_16x16x32_bf16 a[88:91],a[144:147],v[96:99],0
ds_read_b64_tr_b16 a[164:165],v1 offset:10240
ds_read_b64_tr_b16 a[166:167],v1 offset:15360
v_max_f32 v23,0x00800000,v60
v_mfma_f32_16x16x32_bf16 a[88:91],a[148:151],v[100:103],a[88:91]
ds_read_b64_tr_b16 a[168:169],v1 offset:20480
ds_read_b64_tr_b16 a[170:171],v1 offset:25600
v_rcp_f32 v23,v23
v_accvgpr_read_b32 v228,a96
v_accvgpr_read_b32 v229,a97
v_mfma_f32_16x16x32_bf16 a[88:91],a[152:155],v[104:107],a[88:91]
ds_read_b64_tr_b16 a[172:173],v1 offset:30720
ds_read_b64_tr_b16 a[174:175],v1 offset:35840
v_mul_f32 v220,v22,v220
v_mul_f32 v221,v22,v221
v_cvt_pk_bf16_f32 v220,v220,v221
v_mul_f32 v222,v22,v222
v_mul_f32 v223,v22,v223
v_mul_f32 v228,v23,v228
v_mul_f32 v229,v23,v229
v_accvgpr_read_b32 v230,a98
v_accvgpr_read_b32 v231,a99
v_cvt_pk_bf16_f32 v228,v228,v229
v_mul_f32 v230,v23,v230
v_mul_f32 v231,v23,v231
v_mfma_f32_16x16x32_bf16 a[88:91],a[156:159],v[108:111],a[88:91]
v_cvt_pk_bf16_f32 v221,v222,v223
v_accvgpr_read_b32 v222,a92
v_accvgpr_read_b32 v223,a93
v_accvgpr_read_b32 v14,a94
v_accvgpr_read_b32 v15,a95
v_cvt_pk_bf16_f32 v229,v230,v231
v_accvgpr_read_b32 v230,a100
v_accvgpr_read_b32 v231,a101
v_accvgpr_read_b32 v236,a104
v_accvgpr_read_b32 v237,a105
v_mul_f32 v236,v23,v236
v_mul_f32 v237,v23,v237
v_accvgpr_read_b32 v238,a106
v_accvgpr_read_b32 v239,a107
v_accvgpr_read_b32 v244,a112
v_accvgpr_read_b32 v245,a113
v_mul_f32 v244,v23,v244
v_mul_f32 v245,v23,v245
v_accvgpr_read_b32 v246,a114
v_accvgpr_read_b32 v247,a115
v_accvgpr_read_b32 v252,a120
v_accvgpr_read_b32 v253,a121
v_cmp_nlt_f32 s[56:57],v48,s63
v_mul_f32 v252,v23,v252
v_mul_f32 v253,v23,v253
v_accvgpr_read_b32 v254,a122
v_accvgpr_read_b32 v255,a123
v_mfma_f32_16x16x32_bf16 a[92:95],a[156:159],v[124:127],0
v_mul_f32 v222,v22,v222
v_mul_f32 v223,v22,v223
v_mul_f32 v14,v22,v14
v_mul_f32 v15,v22,v15
v_cvt_pk_bf16_f32 v222,v222,v223
v_accvgpr_read_b32 v16,a102
v_accvgpr_read_b32 v17,a103
v_mul_f32 v230,v23,v230
v_mul_f32 v231,v23,v231
v_mul_f32 v16,v23,v16
v_mul_f32 v17,v23,v17
v_cvt_pk_bf16_f32 v230,v230,v231
v_cvt_pk_bf16_f32 v236,v236,v237
v_mul_f32 v238,v23,v238
v_mul_f32 v239,v23,v239
v_cvt_pk_bf16_f32 v237,v238,v239
v_cvt_pk_bf16_f32 v244,v244,v245
v_mul_f32 v246,v23,v246
v_mul_f32 v247,v23,v247
v_cvt_pk_bf16_f32 v245,v246,v247
v_cvt_pk_bf16_f32 v252,v252,v253
v_mul_f32 v254,v23,v254
v_mul_f32 v255,v23,v255
v_cmp_nlt_f32 s[58:59],v52,s63
s_nop 1
s_or_b64 s[56:57],s[56:57],s[58:59]
v_cvt_pk_bf16_f32 v253,v254,v255
v_mfma_f32_16x16x32_bf16 a[92:95],a[152:155],v[120:123],a[92:95]
v_cvt_pk_bf16_f32 v215,v12,v13
v_cvt_pk_bf16_f32 v223,v14,v15
v_cvt_pk_bf16_f32 v231,v16,v17
v_accvgpr_read_b32 v238,a108
v_accvgpr_read_b32 v239,a109
v_accvgpr_read_b32 v12,a110
v_accvgpr_read_b32 v13,a111
v_mul_f32 v238,v23,v238
v_mul_f32 v239,v23,v239
v_accvgpr_read_b32 v246,a116
v_accvgpr_read_b32 v247,a117
v_accvgpr_read_b32 v14,a118
v_accvgpr_read_b32 v15,a119
v_mul_f32 v246,v23,v246
v_mul_f32 v247,v23,v247
v_accvgpr_read_b32 v254,a124
v_accvgpr_read_b32 v255,a125
v_cmp_nlt_f32 s[58:59],v56,s63
s_nop 1
s_or_b64 s[56:57],s[56:57],s[58:59]
v_accvgpr_read_b32 v16,a126
v_accvgpr_read_b32 v17,a127
v_mul_f32 v254,v23,v254
v_mul_f32 v255,v23,v255
v_mfma_f32_16x16x32_bf16 a[92:95],a[148:151],v[116:119],a[92:95]
v_mul_f32 v12,v23,v12
v_mul_f32 v13,v23,v13
v_cvt_pk_bf16_f32 v238,v238,v239
v_cvt_pk_bf16_f32 v239,v12,v13
v_mul_f32 v14,v23,v14
v_mul_f32 v15,v23,v15
v_cvt_pk_bf16_f32 v246,v246,v247
v_cvt_pk_bf16_f32 v247,v14,v15
v_mul_f32 v16,v23,v16
v_mul_f32 v17,v23,v17
v_cvt_pk_bf16_f32 v254,v254,v255
v_cmp_nlt_f32 s[58:59],v60,s63
s_nop 1
s_or_b64 s[56:57],s[56:57],s[58:59]
v_cvt_pk_bf16_f32 v255,v16,v17
s_waitcnt vmcnt(1)
v_cmp_ne_u32 s[90:91],s68,v26
v_cmp_ne_u32 s[92:93],s69,v27
s_nop 0
s_or_b64 s[90:91],s[90:91],s[92:93]
)A"
R"A(s_cbranch_scc1 L_fr7_%=
L_fo7_%=:
v_mfma_f32_16x16x32_bf16 a[92:95],a[144:147],v[112:115],a[92:95]
s_cmp_lg_u64 s[56:57],0
s_cbranch_scc1 L_slow1_%=
L_sret1_%=:
v_add_u32 v2,40960,v2
s_waitcnt lgkmcnt(6)
v_mfma_f32_16x16x32_bf16 a[192:195],a[160:163],v[132:135],a[192:195]
s_waitcnt lgkmcnt(4)
buffer_store_dwordx4 v[196:199],v6,s[36:39],s86 offen sc1
v_mfma_f32_16x16x32_bf16 a[192:195],a[164:167],v[140:143],a[192:195]
s_cmp_ge_u32 0,s71
s_cselect_b32 s76,2,0
s_lshl_b32 s76,s76,16
s_waitcnt lgkmcnt(2)
v_mfma_f32_16x16x32_bf16 a[192:195],a[168:171],v[148:151],a[192:195]
s_add_u32 s77,s76,4096
s_add_u32 s78,s76,8192
s_add_u32 s79,s76,12288
s_max_i32 s76,s76,s18
s_waitcnt lgkmcnt(0)
v_mfma_f32_16x16x32_bf16 a[192:195],a[172:175],v[156:159],a[192:195]
s_max_i32 s77,s77,s19
ds_read_b64_tr_b16 a[176:177],v2 offset:32
ds_read_b64_tr_b16 a[178:179],v2 offset:5152
v_mfma_f32_16x16x32_bf16 a[196:199],a[172:175],v[188:191],a[196:199]
s_max_i32 s78,s78,s100
buffer_load_dwordx4 v[128:131],v6,s[28:31],s76 offen sc1
v_mfma_f32_16x16x32_bf16 a[196:199],a[168:171],v[180:183],a[196:199]
s_max_i32 s79,s79,s101
ds_read_b64_tr_b16 a[180:181],v2 offset:10272
ds_read_b64_tr_b16 a[182:183],v2 offset:15392
v_mfma_f32_16x16x32_bf16 a[196:199],a[164:167],v[172:175],a[196:199]
s_cmp_ge_u32 2,s71
s_cselect_b32 s80,6,4
ds_read_b64_tr_b16 a[184:185],v2 offset:20512
v_mfma_f32_16x16x32_bf16 a[196:199],a[160:163],v[164:167],a[196:199]
s_mul_i32 s80,s80,163840
s_add_u32 s80,s80,s81
ds_read_b64_tr_b16 a[186:187],v2 offset:25632
v_mfma_f32_16x16x32_bf16 a[200:203],a[160:163],v[196:199],a[200:203]
s_add_u32 s83,s80,20480
buffer_store_dwordx4 v[204:207],v6,s[36:39],s86 offen offset:1024 sc1
v_mfma_f32_16x16x32_bf16 a[200:203],a[164:167],v[204:207],a[200:203]
ds_read_b64_tr_b16 a[188:189],v2 offset:30752
ds_read_b64_tr_b16 a[190:191],v2 offset:35872
v_mfma_f32_16x16x32_bf16 a[200:203],a[168:171],v[212:215],a[200:203]
ds_read_b64_tr_b16 a[128:129],v1 offset:64
v_mfma_f32_16x16x32_bf16 a[200:203],a[172:175],v[220:223],a[200:203]
ds_read_b64_tr_b16 a[130:131],v1 offset:5184
v_mfma_f32_16x16x32_bf16 a[204:207],a[172:175],v[252:255],a[204:207]
buffer_load_dwordx4 v[160:163],v6,s[28:31],s76 offen offset:1024 sc1
v_mfma_f32_16x16x32_bf16 a[204:207],a[168:171],v[244:247],a[204:207]
ds_read_b64_tr_b16 a[132:133],v1 offset:10304
ds_read_b64_tr_b16 a[134:135],v1 offset:15424
v_mfma_f32_16x16x32_bf16 a[204:207],a[164:167],v[236:239],a[204:207]
ds_read_b64_tr_b16 a[136:137],v1 offset:20544
v_mfma_f32_16x16x32_bf16 a[204:207],a[160:163],v[228:231],a[204:207]
s_barrier
buffer_load_dwordx4 v[192:195],v6,s[28:31],s76 offen offset:2048 sc1
s_waitcnt lgkmcnt(7)
v_mfma_f32_16x16x32_bf16 a[208:211],a[176:179],v[132:135],a[208:211]
ds_read_b64_tr_b16 a[138:139],v1 offset:25664
ds_read_b64_tr_b16 a[140:141],v1 offset:30784
v_mfma_f32_16x16x32_bf16 a[208:211],a[180:183],v[140:143],a[208:211]
ds_read_b64_tr_b16 a[142:143],v1 offset:35904
v_mfma_f32_16x16x32_bf16 a[208:211],a[184:187],v[148:151],a[208:211]
s_add_u32 m0,s82,0
ds_read_b64_tr_b16 a[144:145],v2 offset:96
s_waitcnt lgkmcnt(8)
v_mfma_f32_16x16x32_bf16 a[208:211],a[188:191],v[156:159],a[208:211]
buffer_store_dwordx4 v[212:215],v6,s[36:39],s86 offen offset:2048 sc1
v_mfma_f32_16x16x32_bf16 a[212:215],a[188:191],v[188:191],a[212:215]
ds_read_b64_tr_b16 a[146:147],v2 offset:5216
ds_read_b64_tr_b16 a[148:149],v2 offset:10336
v_mfma_f32_16x16x32_bf16 a[212:215],a[184:187],v[180:183],a[212:215]
ds_read_b64_tr_b16 a[150:151],v2 offset:15456
v_mfma_f32_16x16x32_bf16 a[212:215],a[180:183],v[172:175],a[212:215]
ds_read_b64_tr_b16 a[152:153],v2 offset:20576
v_mfma_f32_16x16x32_bf16 a[212:215],a[176:179],v[164:167],a[212:215]
buffer_load_dwordx4 v[224:227],v6,s[28:31],s76 offen offset:3072 sc1
v_mfma_f32_16x16x32_bf16 a[216:219],a[176:179],v[196:199],a[216:219]
ds_read_b64_tr_b16 a[154:155],v2 offset:25696
ds_read_b64_tr_b16 a[156:157],v2 offset:30816
v_mfma_f32_16x16x32_bf16 a[216:219],a[180:183],v[204:207],a[216:219]
ds_read_b64_tr_b16 a[158:159],v2 offset:35936
v_mfma_f32_16x16x32_bf16 a[216:219],a[184:187],v[212:215],a[216:219]
ds_read_b64_tr_b16 a[160:161],v1 offset:128
v_mfma_f32_16x16x32_bf16 a[216:219],a[188:191],v[220:223],a[216:219]
buffer_load_dwordx4 v7,s[72:75],s80 offen lds
v_mfma_f32_16x16x32_bf16 a[220:223],a[188:191],v[252:255],a[220:223]
ds_read_b64_tr_b16 a[162:163],v1 offset:5248
ds_read_b64_tr_b16 a[164:165],v1 offset:10368
v_mfma_f32_16x16x32_bf16 a[220:223],a[184:187],v[244:247],a[220:223]
ds_read_b64_tr_b16 a[166:167],v1 offset:15488
v_mfma_f32_16x16x32_bf16 a[220:223],a[180:183],v[236:239],a[220:223]
ds_read_b64_tr_b16 a[168:169],v1 offset:20608
v_mfma_f32_16x16x32_bf16 a[220:223],a[176:179],v[228:231],a[220:223]
s_waitcnt lgkmcnt(8)
buffer_load_dwordx4 v[136:139],v6,s[28:31],s77 offen sc1
v_mfma_f32_16x16x32_bf16 a[224:227],a[128:131],v[132:135],a[224:227]
ds_read_b64_tr_b16 a[170:171],v1 offset:25728
ds_read_b64_tr_b16 a[172:173],v1 offset:30848
v_mfma_f32_16x16x32_bf16 a[224:227],a[132:135],v[140:143],a[224:227]
ds_read_b64_tr_b16 a[174:175],v1 offset:35968
v_mfma_f32_16x16x32_bf16 a[224:227],a[136:139],v[148:151],a[224:227]
ds_read_b64_tr_b16 a[176:177],v2 offset:160
v_mfma_f32_16x16x32_bf16 a[224:227],a[140:143],v[156:159],a[224:227]
buffer_load_dwordx4 v[168:171],v6,s[28:31],s77 offen offset:1024 sc1
v_mfma_f32_16x16x32_bf16 a[228:231],a[140:143],v[188:191],a[228:231]
ds_read_b64_tr_b16 a[178:179],v2 offset:5280
ds_read_b64_tr_b16 a[180:181],v2 offset:10400
v_mfma_f32_16x16x32_bf16 a[228:231],a[136:139],v[180:183],a[228:231]
ds_read_b64_tr_b16 a[182:183],v2 offset:15520
v_mfma_f32_16x16x32_bf16 a[228:231],a[132:135],v[172:175],a[228:231]
ds_read_b64_tr_b16 a[184:185],v2 offset:20640
v_mfma_f32_16x16x32_bf16 a[228:231],a[128:131],v[164:167],a[228:231]
buffer_load_dwordx4 v8,s[72:75],s80 offen offset:1024 lds
v_mfma_f32_16x16x32_bf16 a[232:235],a[128:131],v[196:199],a[232:235]
ds_read_b64_tr_b16 a[186:187],v2 offset:25760
ds_read_b64_tr_b16 a[188:189],v2 offset:30880
v_mfma_f32_16x16x32_bf16 a[232:235],a[132:135],v[204:207],a[232:235]
ds_read_b64_tr_b16 a[190:191],v2 offset:36000
v_mfma_f32_16x16x32_bf16 a[232:235],a[136:139],v[212:215],a[232:235]
v_mfma_f32_16x16x32_bf16 a[232:235],a[140:143],v[220:223],a[232:235]
buffer_store_dwordx4 v[220:223],v6,s[36:39],s86 offen offset:3072 sc1
v_mfma_f32_16x16x32_bf16 a[236:239],a[140:143],v[252:255],a[236:239]
ds_read_b64_tr_b16 a[140:141],v1 offset:30912
ds_read_b64_tr_b16 a[142:143],v1 offset:36032
v_mfma_f32_16x16x32_bf16 a[236:239],a[136:139],v[244:247],a[236:239]
ds_read_b64_tr_b16 a[136:137],v1 offset:20672
v_mfma_f32_16x16x32_bf16 a[236:239],a[132:135],v[236:239],a[236:239]
ds_read_b64_tr_b16 a[132:133],v1 offset:10432
v_mfma_f32_16x16x32_bf16 a[236:239],a[128:131],v[228:231],a[236:239]
buffer_load_dwordx4 v[200:203],v6,s[28:31],s77 offen offset:2048 sc1
v_mfma_f32_16x16x32_bf16 a[240:243],a[144:147],v[132:135],a[240:243]
ds_read_b64_tr_b16 a[128:129],v1 offset:192
ds_read_b64_tr_b16 a[130:131],v1 offset:5312
v_mfma_f32_16x16x32_bf16 a[240:243],a[148:151],v[140:143],a[240:243]
s_waitcnt lgkmcnt(6)
ds_read_b64_tr_b16 a[134:135],v1 offset:15552
v_mfma_f32_16x16x32_bf16 a[240:243],a[152:155],v[148:151],a[240:243]
ds_read_b64_tr_b16 a[138:139],v1 offset:25792
v_mfma_f32_16x16x32_bf16 a[240:243],a[156:159],v[156:159],a[240:243]
buffer_load_dwordx4 v[232:235],v6,s[28:31],s77 offen offset:3072 sc1
v_mfma_f32_16x16x32_bf16 a[244:247],a[156:159],v[188:191],a[244:247]
v_mfma_f32_16x16x32_bf16 a[244:247],a[152:155],v[180:183],a[244:247]
v_mfma_f32_16x16x32_bf16 a[244:247],a[148:151],v[172:175],a[244:247]
v_mfma_f32_16x16x32_bf16 a[244:247],a[144:147],v[164:167],a[244:247]
buffer_load_dwordx4 v9,s[72:75],s80 offen offset:2048 lds
v_mfma_f32_16x16x32_bf16 a[248:251],a[144:147],v[196:199],a[248:251]
v_mfma_f32_16x16x32_bf16 a[248:251],a[148:151],v[204:207],a[248:251]
v_mfma_f32_16x16x32_bf16 a[248:251],a[152:155],v[212:215],a[248:251]
v_mfma_f32_16x16x32_bf16 a[248:251],a[156:159],v[220:223],a[248:251]
buffer_load_dwordx4 v[144:147],v6,s[28:31],s78 offen sc1
v_mfma_f32_16x16x32_bf16 a[252:255],a[156:159],v[252:255],a[252:255]
ds_read_b64_tr_b16 a[156:157],v2 offset:30944
ds_read_b64_tr_b16 a[158:159],v2 offset:36064
v_mfma_f32_16x16x32_bf16 a[252:255],a[152:155],v[244:247],a[252:255]
ds_read_b64_tr_b16 a[152:153],v2 offset:20704
v_mfma_f32_16x16x32_bf16 a[252:255],a[148:151],v[236:239],a[252:255]
ds_read_b64_tr_b16 a[148:149],v2 offset:10464
v_mfma_f32_16x16x32_bf16 a[252:255],a[144:147],v[228:231],a[252:255]
buffer_store_dwordx4 v[228:231],v6,s[36:39],s87 offen sc1
v_mfma_f32_16x16x32_bf16 a[0:3],a[160:163],v[132:135],a[0:3]
ds_read_b64_tr_b16 a[144:145],v2 offset:224
ds_read_b64_tr_b16 a[146:147],v2 offset:5344
v_mfma_f32_16x16x32_bf16 a[0:3],a[164:167],v[140:143],a[0:3]
ds_read_b64_tr_b16 a[150:151],v2 offset:15584
v_mfma_f32_16x16x32_bf16 a[0:3],a[168:171],v[148:151],a[0:3]
ds_read_b64_tr_b16 a[154:155],v2 offset:25824
v_mfma_f32_16x16x32_bf16 a[0:3],a[172:175],v[156:159],a[0:3]
buffer_load_dwordx4 v[176:179],v6,s[28:31],s78 offen offset:1024 sc1
v_mfma_f32_16x16x32_bf16 a[4:7],a[172:175],v[188:191],a[4:7]
v_mfma_f32_16x16x32_bf16 a[4:7],a[168:171],v[180:183],a[4:7]
v_mfma_f32_16x16x32_bf16 a[4:7],a[164:167],v[172:175],a[4:7]
v_mfma_f32_16x16x32_bf16 a[4:7],a[160:163],v[164:167],a[4:7]
buffer_load_dwordx4 v10,s[72:75],s80 offen offset:3072 lds
v_mfma_f32_16x16x32_bf16 a[8:11],a[160:163],v[196:199],a[8:11]
v_mfma_f32_16x16x32_bf16 a[8:11],a[164:167],v[204:207],a[8:11]
v_mfma_f32_16x16x32_bf16 a[8:11],a[168:171],v[212:215],a[8:11]
v_mfma_f32_16x16x32_bf16 a[8:11],a[172:175],v[220:223],a[8:11]
buffer_load_dwordx4 v[208:211],v6,s[28:31],s78 offen offset:2048 sc1
v_mfma_f32_16x16x32_bf16 a[12:15],a[172:175],v[252:255],a[12:15]
s_cmp_ge_u32 1,s71
s_cselect_b32 s76,4,2
s_lshl_b32 s76,s76,16
v_mfma_f32_16x16x32_bf16 a[12:15],a[168:171],v[244:247],a[12:15]
s_add_u32 s77,s76,4096
ds_read_b64_tr_b16 a[168:169],v1 offset:20736
ds_read_b64_tr_b16 a[170:171],v1 offset:25856
v_mfma_f32_16x16x32_bf16 a[12:15],a[164:167],v[236:239],a[12:15]
s_add_u32 m0,s82,4096
s_max_i32 s77,s77,s19
ds_read_b64_tr_b16 a[164:165],v1 offset:10496
v_mfma_f32_16x16x32_bf16 a[12:15],a[160:163],v[228:231],a[12:15]
buffer_load_dwordx4 v[240:243],v6,s[28:31],s78 offen offset:3072 sc1
v_mfma_f32_16x16x32_bf16 a[16:19],a[176:179],v[132:135],a[16:19]
s_add_u32 s78,s76,8192
ds_read_b64_tr_b16 a[160:161],v1 offset:256
ds_read_b64_tr_b16 a[162:163],v1 offset:5376
v_mfma_f32_16x16x32_bf16 a[16:19],a[180:183],v[140:143],a[16:19]
s_max_i32 s78,s78,s100
ds_read_b64_tr_b16 a[166:167],v1 offset:15616
v_mfma_f32_16x16x32_bf16 a[16:19],a[184:187],v[148:151],a[16:19]
ds_read_b64_tr_b16 a[172:173],v1 offset:30976
v_mfma_f32_16x16x32_bf16 a[16:19],a[188:191],v[156:159],a[16:19]
buffer_store_dwordx4 v[236:239],v6,s[36:39],s87 offen offset:1024 sc1
v_mfma_f32_16x16x32_bf16 a[20:23],a[188:191],v[188:191],a[20:23]
ds_read_b64_tr_b16 a[174:175],v1 offset:36096
v_add_u32 v1,40960,v1
v_mfma_f32_16x16x32_bf16 a[20:23],a[184:187],v[180:183],a[20:23]
ds_read_b64_tr_b16 a[96:97],v1 offset:128
ds_read_b64_tr_b16 a[98:99],v1 offset:5248
v_mfma_f32_16x16x32_bf16 a[20:23],a[180:183],v[172:175],a[20:23]
ds_read_b64_tr_b16 a[100:101],v1 offset:10368
v_mfma_f32_16x16x32_bf16 a[20:23],a[176:179],v[164:167],a[20:23]
buffer_load_dwordx4 v11,s[72:75],s80 offen lds
v_mfma_f32_16x16x32_bf16 a[24:27],a[176:179],v[196:199],a[24:27]
ds_read_b64_tr_b16 a[102:103],v1 offset:15488
ds_read_b64_tr_b16 a[104:105],v1 offset:20608
v_mfma_f32_16x16x32_bf16 a[24:27],a[180:183],v[204:207],a[24:27]
ds_read_b64_tr_b16 a[106:107],v1 offset:25728
v_mfma_f32_16x16x32_bf16 a[24:27],a[184:187],v[212:215],a[24:27]
ds_read_b64_tr_b16 a[108:109],v1 offset:30848
v_mfma_f32_16x16x32_bf16 a[24:27],a[188:191],v[220:223],a[24:27]
buffer_load_dwordx4 v[152:155],v6,s[28:31],s79 offen sc1
v_mfma_f32_16x16x32_bf16 a[28:31],a[188:191],v[252:255],a[28:31]
ds_read_b64_tr_b16 a[188:189],v2 offset:31008
ds_read_b64_tr_b16 a[190:191],v2 offset:36128
)A"
R"A(v_mfma_f32_16x16x32_bf16 a[28:31],a[184:187],v[244:247],a[28:31]
s_add_u32 m0,s82,5120
ds_read_b64_tr_b16 a[184:185],v2 offset:20768
v_mfma_f32_16x16x32_bf16 a[28:31],a[180:183],v[236:239],a[28:31]
ds_read_b64_tr_b16 a[180:181],v2 offset:10528
v_mfma_f32_16x16x32_bf16 a[28:31],a[176:179],v[228:231],a[28:31]
s_waitcnt lgkmcnt(8)
buffer_load_dwordx4 v[184:187],v6,s[28:31],s79 offen offset:1024 sc1
v_mfma_f32_16x16x32_bf16 a[32:35],a[128:131],v[132:135],a[32:35]
ds_read_b64_tr_b16 a[176:177],v2 offset:288
ds_read_b64_tr_b16 a[178:179],v2 offset:5408
v_mfma_f32_16x16x32_bf16 a[32:35],a[132:135],v[140:143],a[32:35]
ds_read_b64_tr_b16 a[182:183],v2 offset:15648
v_mfma_f32_16x16x32_bf16 a[32:35],a[136:139],v[148:151],a[32:35]
ds_read_b64_tr_b16 a[186:187],v2 offset:25888
v_add_u32 v2,40960,v2
v_mfma_f32_16x16x32_bf16 a[32:35],a[140:143],v[156:159],a[32:35]
buffer_load_dwordx4 v7,s[72:75],s83 offen lds
v_mfma_f32_16x16x32_bf16 a[36:39],a[140:143],v[188:191],a[36:39]
ds_read_b64_tr_b16 a[110:111],v1 offset:35968
ds_read_b64_tr_b16 a[112:113],v2 offset:160
v_mfma_f32_16x16x32_bf16 a[36:39],a[136:139],v[180:183],a[36:39]
ds_read_b64_tr_b16 a[114:115],v2 offset:5280
v_mfma_f32_16x16x32_bf16 a[36:39],a[132:135],v[172:175],a[36:39]
ds_read_b64_tr_b16 a[116:117],v2 offset:10400
v_mfma_f32_16x16x32_bf16 a[36:39],a[128:131],v[164:167],a[36:39]
buffer_load_dwordx4 v[216:219],v6,s[28:31],s79 offen offset:2048 sc1
v_mfma_f32_16x16x32_bf16 a[40:43],a[128:131],v[196:199],a[40:43]
ds_read_b64_tr_b16 a[118:119],v2 offset:15520
ds_read_b64_tr_b16 a[120:121],v2 offset:20640
v_mfma_f32_16x16x32_bf16 a[40:43],a[132:135],v[204:207],a[40:43]
ds_read_b64_tr_b16 a[122:123],v2 offset:25760
v_mfma_f32_16x16x32_bf16 a[40:43],a[136:139],v[212:215],a[40:43]
ds_read_b64_tr_b16 a[124:125],v2 offset:30880
v_mfma_f32_16x16x32_bf16 a[40:43],a[140:143],v[220:223],a[40:43]
buffer_load_dwordx4 v[248:251],v6,s[28:31],s79 offen offset:3072 sc1
v_mfma_f32_16x16x32_bf16 a[44:47],a[140:143],v[252:255],a[44:47]
s_add_u32 s79,s76,12288
s_max_i32 s76,s76,s18
ds_read_b64_tr_b16 a[140:141],v1 offset:30720
v_mfma_f32_16x16x32_bf16 a[44:47],a[136:139],v[244:247],a[44:47]
s_max_i32 s79,s79,s101
ds_read_b64_tr_b16 a[136:137],v1 offset:20480
ds_read_b64_tr_b16 a[138:139],v1 offset:25600
v_mfma_f32_16x16x32_bf16 a[44:47],a[132:135],v[236:239],a[44:47]
ds_read_b64_tr_b16 a[132:133],v1 offset:10240
v_mfma_f32_16x16x32_bf16 a[44:47],a[128:131],v[228:231],a[44:47]
buffer_load_dwordx4 v[64:67],v6,s[28:31],s76 offen sc1
v_mfma_f32_16x16x32_bf16 a[48:51],a[144:147],v[132:135],a[48:51]
ds_read_b64_tr_b16 a[128:129],v1
ds_read_b64_tr_b16 a[130:131],v1 offset:5120
v_mfma_f32_16x16x32_bf16 a[48:51],a[148:151],v[140:143],a[48:51]
ds_read_b64_tr_b16 a[134:135],v1 offset:15360
v_mfma_f32_16x16x32_bf16 a[48:51],a[152:155],v[148:151],a[48:51]
ds_read_b64_tr_b16 a[142:143],v1 offset:35840
v_mfma_f32_16x16x32_bf16 a[48:51],a[156:159],v[156:159],a[48:51]
buffer_load_dwordx4 v[68:71],v6,s[28:31],s76 offen offset:1024 sc1
v_mfma_f32_16x16x32_bf16 a[52:55],a[156:159],v[188:191],a[52:55]
ds_read_b64_tr_b16 a[126:127],v2 offset:36000
v_mfma_f32_16x16x32_bf16 a[52:55],a[152:155],v[180:183],a[52:55]
v_mfma_f32_16x16x32_bf16 a[52:55],a[148:151],v[172:175],a[52:55]
v_mfma_f32_16x16x32_bf16 a[52:55],a[144:147],v[164:167],a[52:55]
buffer_store_dwordx4 v[244:247],v6,s[36:39],s87 offen offset:2048 sc1
v_mfma_f32_16x16x32_bf16 a[56:59],a[144:147],v[196:199],a[56:59]
v_mfma_f32_16x16x32_bf16 a[56:59],a[148:151],v[204:207],a[56:59]
v_mfma_f32_16x16x32_bf16 a[56:59],a[152:155],v[212:215],a[56:59]
v_mfma_f32_16x16x32_bf16 a[56:59],a[156:159],v[220:223],a[56:59]
buffer_store_dwordx4 v[252:255],v6,s[36:39],s87 offen offset:3072 sc1
v_mfma_f32_16x16x32_bf16 a[60:63],a[156:159],v[252:255],a[60:63]
ds_read_b64_tr_b16 a[156:157],v2 offset:30752
ds_read_b64_tr_b16 a[158:159],v2 offset:35872
v_mfma_f32_16x16x32_bf16 a[60:63],a[152:155],v[244:247],a[60:63]
ds_read_b64_tr_b16 a[152:153],v2 offset:20512
v_mfma_f32_16x16x32_bf16 a[60:63],a[148:151],v[236:239],a[60:63]
ds_read_b64_tr_b16 a[148:149],v2 offset:10272
v_mfma_f32_16x16x32_bf16 a[60:63],a[144:147],v[228:231],a[60:63]
buffer_load_dwordx4 v8,s[72:75],s83 offen offset:1024 lds
v_mfma_f32_16x16x32_bf16 a[64:67],a[160:163],v[132:135],a[64:67]
ds_read_b64_tr_b16 a[144:145],v2 offset:32
ds_read_b64_tr_b16 a[146:147],v2 offset:5152
v_mfma_f32_16x16x32_bf16 a[64:67],a[164:167],v[140:143],a[64:67]
ds_read_b64_tr_b16 a[150:151],v2 offset:15392
v_mfma_f32_16x16x32_bf16 a[64:67],a[168:171],v[148:151],a[64:67]
ds_read_b64_tr_b16 a[154:155],v2 offset:25632
v_mfma_f32_16x16x32_bf16 a[64:67],a[172:175],v[156:159],a[64:67]
buffer_load_dwordx4 v[72:75],v6,s[28:31],s76 offen offset:2048 sc1
v_mfma_f32_16x16x32_bf16 a[68:71],a[172:175],v[188:191],a[68:71]
v_mfma_f32_16x16x32_bf16 a[68:71],a[168:171],v[180:183],a[68:71]
v_mfma_f32_16x16x32_bf16 a[68:71],a[164:167],v[172:175],a[68:71]
v_mfma_f32_16x16x32_bf16 a[68:71],a[160:163],v[164:167],a[68:71]
buffer_load_dwordx4 v[76:79],v6,s[28:31],s76 offen offset:3072 sc1
v_mfma_f32_16x16x32_bf16 a[72:75],a[160:163],v[196:199],a[72:75]
v_mfma_f32_16x16x32_bf16 a[72:75],a[164:167],v[204:207],a[72:75]
v_mfma_f32_16x16x32_bf16 a[72:75],a[168:171],v[212:215],a[72:75]
v_mfma_f32_16x16x32_bf16 a[72:75],a[172:175],v[220:223],a[72:75]
buffer_load_dwordx4 v9,s[72:75],s83 offen offset:2048 lds
v_mfma_f32_16x16x32_bf16 a[76:79],a[172:175],v[252:255],a[76:79]
ds_read_b64_tr_b16 a[172:173],v1 offset:30784
ds_read_b64_tr_b16 a[174:175],v1 offset:35904
v_mfma_f32_16x16x32_bf16 a[76:79],a[168:171],v[244:247],a[76:79]
ds_read_b64_tr_b16 a[168:169],v1 offset:20544
v_mfma_f32_16x16x32_bf16 a[76:79],a[164:167],v[236:239],a[76:79]
ds_read_b64_tr_b16 a[164:165],v1 offset:10304
v_mfma_f32_16x16x32_bf16 a[76:79],a[160:163],v[228:231],a[76:79]
s_waitcnt lgkmcnt(4)
buffer_load_dwordx4 v[80:83],v6,s[28:31],s77 offen sc1
v_mfma_f32_16x16x32_bf16 a[80:83],a[176:179],v[132:135],a[80:83]
ds_read_b64_tr_b16 a[160:161],v1 offset:64
ds_read_b64_tr_b16 a[162:163],v1 offset:5184
v_mfma_f32_16x16x32_bf16 a[80:83],a[180:183],v[140:143],a[80:83]
ds_read_b64_tr_b16 a[166:167],v1 offset:15424
v_mfma_f32_16x16x32_bf16 a[80:83],a[184:187],v[148:151],a[80:83]
ds_read_b64_tr_b16 a[170:171],v1 offset:25664
v_mfma_f32_16x16x32_bf16 a[80:83],a[188:191],v[156:159],a[80:83]
buffer_load_dwordx4 v[84:87],v6,s[28:31],s77 offen offset:1024 sc1
v_mfma_f32_16x16x32_bf16 a[84:87],a[188:191],v[188:191],a[84:87]
v_mfma_f32_16x16x32_bf16 a[84:87],a[184:187],v[180:183],a[84:87]
v_mfma_f32_16x16x32_bf16 a[84:87],a[180:183],v[172:175],a[84:87]
v_mfma_f32_16x16x32_bf16 a[84:87],a[176:179],v[164:167],a[84:87]
buffer_load_dwordx4 v[88:91],v6,s[28:31],s77 offen offset:2048 sc1
v_mfma_f32_16x16x32_bf16 a[88:91],a[176:179],v[196:199],a[88:91]
v_mfma_f32_16x16x32_bf16 a[88:91],a[180:183],v[204:207],a[88:91]
v_mfma_f32_16x16x32_bf16 a[88:91],a[184:187],v[212:215],a[88:91]
v_mfma_f32_16x16x32_bf16 a[88:91],a[188:191],v[220:223],a[88:91]
buffer_load_dwordx4 v10,s[72:75],s83 offen offset:3072 lds
v_mfma_f32_16x16x32_bf16 a[92:95],a[188:191],v[252:255],a[92:95]
s_add_u32 m0,s82,9216
ds_read_b64_tr_b16 a[188:189],v2 offset:30816
ds_read_b64_tr_b16 a[190:191],v2 offset:35936
v_mfma_f32_16x16x32_bf16 a[92:95],a[184:187],v[244:247],a[92:95]
ds_read_b64_tr_b16 a[184:185],v2 offset:20576
v_mfma_f32_16x16x32_bf16 a[92:95],a[180:183],v[236:239],a[92:95]
s_cmp_ge_u32 0,s71
s_cselect_b32 s80,3,1
s_mul_i32 s80,s80,163840
v_mfma_f32_16x16x32_bf16 a[92:95],a[176:179],v[228:231],a[92:95]
s_add_u32 s80,s80,s81
buffer_load_dwordx4 v[92:95],v6,s[28:31],s77 offen offset:3072 sc1
s_waitcnt vmcnt(18)
v_mfma_f32_16x16x32_bf16 a[192:195],a[128:131],v[128:131],a[192:195]
s_lshl_b32 s43,s41,2
ds_read_b64_tr_b16 a[176:177],v2 offset:96
ds_read_b64_tr_b16 a[178:179],v2 offset:5216
v_mfma_f32_16x16x32_bf16 a[192:195],a[132:135],v[160:163],a[192:195]
s_add_u32 s84,s70,1
s_add_u32 s85,s70,2
ds_read_b64_tr_b16 a[180:181],v2 offset:10336
v_mfma_f32_16x16x32_bf16 a[192:195],a[136:139],v[192:195],a[192:195]
s_add_u32 s86,s70,3
s_add_u32 s43,s43,s71
ds_read_b64_tr_b16 a[182:183],v2 offset:15456
v_mfma_f32_16x16x32_bf16 a[192:195],a[140:143],v[224:227],a[192:195]
s_and_b32 s84,s84,3
buffer_load_dwordx4 v[96:99],v6,s[28:31],s78 offen sc1
v_mfma_f32_16x16x32_bf16 a[196:199],a[140:143],v[232:235],a[196:199]
s_and_b32 s85,s85,3
s_and_b32 s86,s86,3
ds_read_b64_tr_b16 a[186:187],v2 offset:25696
v_mfma_f32_16x16x32_bf16 a[196:199],a[136:139],v[200:203],a[196:199]
s_mul_i32 s43,s43,245760
s_mul_i32 s84,s84,12
s_mul_i32 s85,s85,12
v_mfma_f32_16x16x32_bf16 a[196:199],a[132:135],v[168:171],a[196:199]
s_mul_i32 s86,s86,12
s_add_u32 s43,s43,134217728
s_mov_b64 s[32:33],s[12:13]
v_mfma_f32_16x16x32_bf16 a[196:199],a[128:131],v[136:139],a[196:199]
s_add_u32 s84,s84,0
buffer_load_dwordx4 v11,s[72:75],s83 offen lds
v_mfma_f32_16x16x32_bf16 a[200:203],a[128:131],v[144:147],a[200:203]
s_add_u32 s83,s80,20480
s_add_u32 s85,s85,4
s_add_u32 s86,s86,8
v_mfma_f32_16x16x32_bf16 a[200:203],a[132:135],v[176:179],a[200:203]
s_lshl_b32 s95,s42,2
s_add_u32 s95,s95,s40
s_lshl_b32 s95,s95,4
v_mfma_f32_16x16x32_bf16 a[200:203],a[136:139],v[208:211],a[200:203]
s_add_u32 s84,s84,s40
v_mov_b32 v18,s68
v_mov_b32 v19,s69
v_mfma_f32_16x16x32_bf16 a[200:203],a[140:143],v[240:243],a[200:203]
s_add_u32 s85,s85,s40
buffer_load_dwordx4 v[100:103],v6,s[28:31],s78 offen offset:1024 sc1
s_waitcnt vmcnt(16)
v_mfma_f32_16x16x32_bf16 a[204:207],a[140:143],v[248:251],a[204:207]
s_add_u32 s86,s86,s40
ds_read_b64_tr_b16 a[140:141],v1 offset:30912
ds_read_b64_tr_b16 a[142:143],v1 offset:36032
v_mfma_f32_16x16x32_bf16 a[204:207],a[136:139],v[216:219],a[204:207]
s_add_u32 s32,s32,s43
s_addc_u32 s33,s33,0
ds_read_b64_tr_b16 a[136:137],v1 offset:20672
v_mfma_f32_16x16x32_bf16 a[204:207],a[132:135],v[184:187],a[204:207]
s_mul_i32 s84,s84,5120
s_mul_i32 s85,s85,5120
ds_read_b64_tr_b16 a[132:133],v1 offset:10432
v_mfma_f32_16x16x32_bf16 a[204:207],a[128:131],v[152:155],a[204:207]
buffer_load_dwordx4 v[104:107],v6,s[28:31],s78 offen offset:2048 sc1
s_barrier
ds_read_b64_tr_b16 a[128:129],v1 offset:192
ds_read_b64_tr_b16 a[130:131],v1 offset:5312
v_mfma_f32_16x16x32_bf16 a[208:211],a[144:147],v[128:131],a[208:211]
s_mul_i32 s86,s86,5120
s_and_b32 s33,s33,0xffff
ds_read_b64_tr_b16 a[134:135],v1 offset:15552
v_mfma_f32_16x16x32_bf16 a[208:211],a[148:151],v[160:163],a[208:211]
s_mov_b32 s34,245760
s_mov_b32 s35,0x20000
ds_read_b64_tr_b16 a[138:139],v1 offset:25792
v_mfma_f32_16x16x32_bf16 a[208:211],a[152:155],v[192:195],a[208:211]
s_add_u32 m0,s82,40960
s_waitcnt vmcnt(13)
s_mov_b64 exec,1
buffer_store_dwordx2 v[18:19],v6,s[64:67],s95 offen offset:1024 sc1
s_mov_b64 exec,-1
v_mfma_f32_16x16x32_bf16 a[208:211],a[156:159],v[224:227],a[208:211]
s_max_i32 s84,s84,s18
s_max_i32 s85,s85,s19
s_max_i32 s86,s86,s100
v_mfma_f32_16x16x32_bf16 a[212:215],a[156:159],v[232:235],a[212:215]
s_mul_i32 s44,s41,307200
v_bfe_u32 v18,v6,6,2
v_lshrrev_b32 v19,8,v6
v_mfma_f32_16x16x32_bf16 a[212:215],a[152:155],v[200:203],a[212:215]
s_mov_b64 s[20:21],s[16:17]
v_xor_b32 v18,v18,v19
v_lshlrev_b32 v18,2,v18
v_mfma_f32_16x16x32_bf16 a[212:215],a[148:151],v[168:171],a[212:215]
s_lshl_b32 s43,s40,2
buffer_load_dwordx4 v[108:111],v6,s[28:31],s78 offen offset:3072 sc1
v_mfma_f32_16x16x32_bf16 a[212:215],a[144:147],v[136:139],a[212:215]
s_add_u32 s43,s43,s70
v_bfe_u32 v19,v6,4,2
v_or_b32 v18,v18,v19
v_mfma_f32_16x16x32_bf16 a[216:219],a[144:147],v[144:147],a[216:219]
s_mul_i32 s43,s43,20480
v_and_b32 v19,16,v6
v_mov_b32 v34,0x3f80
v_mfma_f32_16x16x32_bf16 a[216:219],a[148:151],v[176:179],a[216:219]
s_mov_b32 s22,307200
v_lshlrev_b32 v19,v19,v34
v_lshrrev_b32 v18,1,v18
v_mfma_f32_16x16x32_bf16 a[216:219],a[152:155],v[208:211],a[216:219]
s_mov_b32 s23,0x20000
buffer_load_dwordx4 v7,s[72:75],s80 offen lds
v_mfma_f32_16x16x32_bf16 a[216:219],a[156:159],v[240:243],a[216:219]
v_cmp_eq_u32 vcc,0,v18
s_nop 1
v_cndmask_b32 v34,0,v19,vcc
v_mfma_f32_16x16x32_bf16 a[220:223],a[156:159],v[248:251],a[220:223]
ds_read_b64_tr_b16 a[156:157],v2 offset:30944
)A"
R"A(ds_read_b64_tr_b16 a[158:159],v2 offset:36064
v_mfma_f32_16x16x32_bf16 a[220:223],a[152:155],v[216:219],a[220:223]
ds_read_b64_tr_b16 a[152:153],v2 offset:20704
v_add_u32 v25,4096,v6
v_mfma_f32_16x16x32_bf16 a[220:223],a[148:151],v[184:187],a[220:223]
buffer_load_dwordx4 v[112:115],v6,s[28:31],s79 offen sc1
v_mfma_f32_16x16x32_bf16 a[220:223],a[144:147],v[152:155],a[220:223]
s_waitcnt lgkmcnt(3)
ds_read_b64_tr_b16 a[144:145],v2 offset:224
ds_read_b64_tr_b16 a[146:147],v2 offset:5344
v_mfma_f32_16x16x32_bf16 a[224:227],a[160:163],v[128:131],a[224:227]
ds_read_b64_tr_b16 a[148:149],v2 offset:10464
v_mov_b32 v36,0
v_mfma_f32_16x16x32_bf16 a[224:227],a[164:167],v[160:163],a[224:227]
ds_read_b64_tr_b16 a[150:151],v2 offset:15584
v_mov_b32 v37,0
v_mfma_f32_16x16x32_bf16 a[224:227],a[168:171],v[192:195],a[224:227]
buffer_load_dwordx4 v[116:119],v6,s[28:31],s79 offen offset:1024 sc1
v_mfma_f32_16x16x32_bf16 a[224:227],a[172:175],v[224:227],a[224:227]
s_cmp_ge_u32 2,s71
s_cselect_b32 s76,6,4
s_lshl_b32 s76,s76,16
v_mfma_f32_16x16x32_bf16 a[228:231],a[172:175],v[232:235],a[228:231]
s_add_u32 s77,s76,4096
s_add_u32 s78,s76,8192
s_max_i32 s77,s77,s19
v_mfma_f32_16x16x32_bf16 a[228:231],a[168:171],v[200:203],a[228:231]
s_max_i32 s78,s78,s100
ds_read_b64_tr_b16 a[154:155],v2 offset:25824
v_mov_b32 v32,0
v_mfma_f32_16x16x32_bf16 a[228:231],a[164:167],v[168:171],a[228:231]
buffer_load_dwordx4 v[120:123],v6,s[28:31],s79 offen offset:2048 sc1
v_mfma_f32_16x16x32_bf16 a[228:231],a[160:163],v[136:139],a[228:231]
v_cmp_eq_u32 vcc,1,v18
s_nop 1
v_cndmask_b32 v35,0,v19,vcc
v_mfma_f32_16x16x32_bf16 a[232:235],a[160:163],v[144:147],a[232:235]
s_add_u32 s20,s20,s44
s_addc_u32 s21,s21,0
v_mov_b32 v33,0
v_mfma_f32_16x16x32_bf16 a[232:235],a[164:167],v[176:179],a[232:235]
s_mul_i32 s44,s71,320
v_bfe_u32 v40,v6,4,4
v_mul_u32_u24 v40,1280,v40
v_mfma_f32_16x16x32_bf16 a[232:235],a[168:171],v[208:211],a[232:235]
s_and_b32 s21,s21,0xffff
buffer_load_dwordx4 v8,s[72:75],s80 offen offset:1024 lds
v_mfma_f32_16x16x32_bf16 a[232:235],a[172:175],v[240:243],a[232:235]
s_add_u32 s43,s43,s44
v_lshrrev_b32 v41,8,v6
v_lshl_add_u32 v40,v41,3,v40
v_mfma_f32_16x16x32_bf16 a[236:239],a[172:175],v[248:251],a[236:239]
ds_read_b64_tr_b16 a[172:173],v1 offset:30976
ds_read_b64_tr_b16 a[174:175],v1 offset:36096
v_mfma_f32_16x16x32_bf16 a[236:239],a[168:171],v[216:219],a[236:239]
ds_read_b64_tr_b16 a[168:169],v1 offset:20736
v_mfma_f32_16x16x32_bf16 a[236:239],a[164:167],v[184:187],a[236:239]
buffer_load_dwordx4 v[124:127],v6,s[28:31],s79 offen offset:3072 sc1
v_mfma_f32_16x16x32_bf16 a[236:239],a[160:163],v[152:155],a[236:239]
s_add_u32 s79,s76,12288
s_max_i32 s76,s76,s18
ds_read_b64_tr_b16 a[160:161],v1 offset:256
v_mfma_f32_16x16x32_bf16 a[240:243],a[176:179],v[128:131],a[240:243]
s_max_i32 s79,s79,s101
ds_read_b64_tr_b16 a[162:163],v1 offset:5376
ds_read_b64_tr_b16 a[164:165],v1 offset:10496
v_mfma_f32_16x16x32_bf16 a[240:243],a[180:183],v[160:163],a[240:243]
ds_read_b64_tr_b16 a[166:167],v1 offset:15616
v_mfma_f32_16x16x32_bf16 a[240:243],a[184:187],v[192:195],a[240:243]
ds_read_b64_tr_b16 a[170:171],v1 offset:25856
v_add_u32 v1,40960,v1
v_mfma_f32_16x16x32_bf16 a[240:243],a[188:191],v[224:227],a[240:243]
buffer_load_dwordx4 v9,s[72:75],s80 offen offset:2048 lds
v_mfma_f32_16x16x32_bf16 a[244:247],a[188:191],v[232:235],a[244:247]
v_mfma_f32_16x16x32_bf16 a[244:247],a[184:187],v[200:203],a[244:247]
v_mfma_f32_16x16x32_bf16 a[244:247],a[180:183],v[168:171],a[244:247]
v_mfma_f32_16x16x32_bf16 a[244:247],a[176:179],v[136:139],a[244:247]
v_mfma_f32_16x16x32_bf16 a[248:251],a[176:179],v[144:147],a[248:251]
v_mfma_f32_16x16x32_bf16 a[248:251],a[180:183],v[176:179],a[248:251]
v_mfma_f32_16x16x32_bf16 a[248:251],a[184:187],v[208:211],a[248:251]
v_mfma_f32_16x16x32_bf16 a[248:251],a[188:191],v[240:243],a[248:251]
buffer_load_dwordx4 v[132:135],v6,s[28:31],s76 offen sc1
v_mfma_f32_16x16x32_bf16 a[252:255],a[188:191],v[248:251],a[252:255]
ds_read_b64_tr_b16 a[188:189],v2 offset:31008
ds_read_b64_tr_b16 a[190:191],v2 offset:36128
v_mfma_f32_16x16x32_bf16 a[252:255],a[184:187],v[216:219],a[252:255]
ds_read_b64_tr_b16 a[184:185],v2 offset:20768
v_mfma_f32_16x16x32_bf16 a[252:255],a[180:183],v[184:187],a[252:255]
ds_read_b64_tr_b16 a[180:181],v2 offset:10528
v_mfma_f32_16x16x32_bf16 a[252:255],a[176:179],v[152:155],a[252:255]
ds_read_b64_tr_b16 a[176:177],v2 offset:288
v_mfma_f32_16x16x32_bf16 a[0:3],a[96:99],v[128:131],a[0:3]
ds_read_b64_tr_b16 a[178:179],v2 offset:5408
ds_read_b64_tr_b16 a[182:183],v2 offset:15648
v_mfma_f32_16x16x32_bf16 a[0:3],a[100:103],v[160:163],a[0:3]
buffer_load_dwordx4 v[140:143],v6,s[28:31],s76 offen offset:1024 sc1
v_mfma_f32_16x16x32_bf16 a[0:3],a[104:107],v[192:195],a[0:3]
ds_read_b64_tr_b16 a[186:187],v2 offset:25888
v_add_u32 v2,40960,v2
v_mfma_f32_16x16x32_bf16 a[0:3],a[108:111],v[224:227],a[0:3]
v_mfma_f32_16x16x32_bf16 a[4:7],a[108:111],v[232:235],a[4:7]
v_mfma_f32_16x16x32_bf16 a[4:7],a[104:107],v[200:203],a[4:7]
buffer_load_dwordx4 v10,s[72:75],s80 offen offset:3072 lds
v_mfma_f32_16x16x32_bf16 a[4:7],a[100:103],v[168:171],a[4:7]
v_mfma_f32_16x16x32_bf16 a[4:7],a[96:99],v[136:139],a[4:7]
v_mfma_f32_16x16x32_bf16 a[8:11],a[96:99],v[144:147],a[8:11]
v_mfma_f32_16x16x32_bf16 a[8:11],a[100:103],v[176:179],a[8:11]
buffer_load_dwordx4 v[148:151],v6,s[28:31],s76 offen offset:2048 sc1
v_mfma_f32_16x16x32_bf16 a[8:11],a[104:107],v[208:211],a[8:11]
v_mfma_f32_16x16x32_bf16 a[8:11],a[108:111],v[240:243],a[8:11]
v_mfma_f32_16x16x32_bf16 a[12:15],a[108:111],v[248:251],a[12:15]
ds_read_b64_tr_b16 a[108:109],v1 offset:30720
ds_read_b64_tr_b16 a[110:111],v1 offset:35840
v_mfma_f32_16x16x32_bf16 a[12:15],a[104:107],v[216:219],a[12:15]
buffer_load_dwordx4 v[156:159],v6,s[28:31],s76 offen offset:3072 sc1
v_mfma_f32_16x16x32_bf16 a[12:15],a[100:103],v[184:187],a[12:15]
s_add_u32 m0,s82,45056
ds_read_b64_tr_b16 a[100:101],v1 offset:10240
ds_read_b64_tr_b16 a[102:103],v1 offset:15360
v_mfma_f32_16x16x32_bf16 a[12:15],a[96:99],v[152:155],a[12:15]
ds_read_b64_tr_b16 a[96:97],v1
v_mfma_f32_16x16x32_bf16 a[16:19],a[112:115],v[128:131],a[16:19]
ds_read_b64_tr_b16 a[98:99],v1 offset:5120
v_mfma_f32_16x16x32_bf16 a[16:19],a[116:119],v[160:163],a[16:19]
buffer_load_dwordx4 v11,s[72:75],s80 offen lds
v_mfma_f32_16x16x32_bf16 a[16:19],a[120:123],v[192:195],a[16:19]
ds_read_b64_tr_b16 a[104:105],v1 offset:20480
ds_read_b64_tr_b16 a[106:107],v1 offset:25600
v_mfma_f32_16x16x32_bf16 a[16:19],a[124:127],v[224:227],a[16:19]
v_mfma_f32_16x16x32_bf16 a[20:23],a[124:127],v[232:235],a[20:23]
v_mfma_f32_16x16x32_bf16 a[20:23],a[120:123],v[200:203],a[20:23]
buffer_load_dwordx4 v[164:167],v6,s[28:31],s77 offen sc1
v_mfma_f32_16x16x32_bf16 a[20:23],a[116:119],v[168:171],a[20:23]
v_mfma_f32_16x16x32_bf16 a[20:23],a[112:115],v[136:139],a[20:23]
v_mfma_f32_16x16x32_bf16 a[24:27],a[112:115],v[144:147],a[24:27]
v_mfma_f32_16x16x32_bf16 a[24:27],a[116:119],v[176:179],a[24:27]
buffer_load_dwordx4 v[172:175],v6,s[28:31],s77 offen offset:1024 sc1
v_mfma_f32_16x16x32_bf16 a[24:27],a[120:123],v[208:211],a[24:27]
v_mfma_f32_16x16x32_bf16 a[24:27],a[124:127],v[240:243],a[24:27]
v_mfma_f32_16x16x32_bf16 a[28:31],a[124:127],v[248:251],a[28:31]
ds_read_b64_tr_b16 a[124:125],v2 offset:30752
ds_read_b64_tr_b16 a[126:127],v2 offset:35872
v_mfma_f32_16x16x32_bf16 a[28:31],a[120:123],v[216:219],a[28:31]
s_add_u32 m0,s82,46080
ds_read_b64_tr_b16 a[120:121],v2 offset:20512
v_mfma_f32_16x16x32_bf16 a[28:31],a[116:119],v[184:187],a[28:31]
ds_read_b64_tr_b16 a[116:117],v2 offset:10272
v_mfma_f32_16x16x32_bf16 a[28:31],a[112:115],v[152:155],a[28:31]
buffer_load_dwordx4 v[180:183],v6,s[28:31],s77 offen offset:2048 sc1
v_mfma_f32_16x16x32_bf16 a[32:35],a[128:131],v[128:131],a[32:35]
ds_read_b64_tr_b16 a[112:113],v2 offset:32
ds_read_b64_tr_b16 a[114:115],v2 offset:5152
v_mfma_f32_16x16x32_bf16 a[32:35],a[132:135],v[160:163],a[32:35]
ds_read_b64_tr_b16 a[118:119],v2 offset:15392
v_mfma_f32_16x16x32_bf16 a[32:35],a[136:139],v[192:195],a[32:35]
ds_read_b64_tr_b16 a[122:123],v2 offset:25632
v_mfma_f32_16x16x32_bf16 a[32:35],a[140:143],v[224:227],a[32:35]
buffer_load_dwordx4 v7,s[72:75],s83 offen lds
v_mfma_f32_16x16x32_bf16 a[36:39],a[140:143],v[232:235],a[36:39]
v_mfma_f32_16x16x32_bf16 a[36:39],a[136:139],v[200:203],a[36:39]
v_mfma_f32_16x16x32_bf16 a[36:39],a[132:135],v[168:171],a[36:39]
v_mfma_f32_16x16x32_bf16 a[36:39],a[128:131],v[136:139],a[36:39]
buffer_load_dwordx4 v[188:191],v6,s[28:31],s77 offen offset:3072 sc1
v_mfma_f32_16x16x32_bf16 a[40:43],a[128:131],v[144:147],a[40:43]
v_mfma_f32_16x16x32_bf16 a[40:43],a[132:135],v[176:179],a[40:43]
v_mfma_f32_16x16x32_bf16 a[40:43],a[136:139],v[208:211],a[40:43]
v_mfma_f32_16x16x32_bf16 a[40:43],a[140:143],v[240:243],a[40:43]
buffer_load_dwordx4 v[196:199],v6,s[28:31],s78 offen sc1
v_mfma_f32_16x16x32_bf16 a[44:47],a[140:143],v[248:251],a[44:47]
ds_read_b64_tr_b16 a[140:141],v1 offset:30784
ds_read_b64_tr_b16 a[142:143],v1 offset:35904
v_mfma_f32_16x16x32_bf16 a[44:47],a[136:139],v[216:219],a[44:47]
ds_read_b64_tr_b16 a[136:137],v1 offset:20544
v_mfma_f32_16x16x32_bf16 a[44:47],a[132:135],v[184:187],a[44:47]
ds_read_b64_tr_b16 a[132:133],v1 offset:10304
v_mfma_f32_16x16x32_bf16 a[44:47],a[128:131],v[152:155],a[44:47]
s_waitcnt lgkmcnt(4)
buffer_load_dwordx4 v8,s[72:75],s83 offen offset:1024 lds
v_mfma_f32_16x16x32_bf16 a[48:51],a[144:147],v[128:131],a[48:51]
ds_read_b64_tr_b16 a[128:129],v1 offset:64
ds_read_b64_tr_b16 a[130:131],v1 offset:5184
v_mfma_f32_16x16x32_bf16 a[48:51],a[148:151],v[160:163],a[48:51]
ds_read_b64_tr_b16 a[134:135],v1 offset:15424
v_mfma_f32_16x16x32_bf16 a[48:51],a[152:155],v[192:195],a[48:51]
ds_read_b64_tr_b16 a[138:139],v1 offset:25664
v_mfma_f32_16x16x32_bf16 a[48:51],a[156:159],v[224:227],a[48:51]
s_lshl_b32 s94,s70,4
buffer_load_dwordx4 v[204:207],v6,s[28:31],s78 offen offset:1024 sc1
v_mfma_f32_16x16x32_bf16 a[52:55],a[156:159],v[232:235],a[52:55]
s_add_u32 s94,s94,s40
v_bfe_u32 v24,v6,4,2
v_min_u32 v24,2,v24
v_mfma_f32_16x16x32_bf16 a[52:55],a[152:155],v[200:203],a[52:55]
s_lshl_b32 s94,s94,4
v_add_u32 v24,s71,v24
v_add_u32 v24,1,v24
v_mfma_f32_16x16x32_bf16 a[52:55],a[148:151],v[168:171],a[52:55]
v_and_b32 v24,3,v24
v_lshlrev_b32 v24,6,v24
v_mfma_f32_16x16x32_bf16 a[52:55],a[144:147],v[136:139],a[52:55]
buffer_load_dwordx4 v[212:215],v6,s[28:31],s78 offen offset:2048 sc1
v_mfma_f32_16x16x32_bf16 a[56:59],a[144:147],v[144:147],a[56:59]
v_mfma_f32_16x16x32_bf16 a[56:59],a[148:151],v[176:179],a[56:59]
v_mfma_f32_16x16x32_bf16 a[56:59],a[152:155],v[208:211],a[56:59]
v_mfma_f32_16x16x32_bf16 a[56:59],a[156:159],v[240:243],a[56:59]
v_mfma_f32_16x16x32_bf16 a[60:63],a[156:159],v[248:251],a[60:63]
ds_read_b64_tr_b16 a[156:157],v2 offset:30816
ds_read_b64_tr_b16 a[158:159],v2 offset:35936
v_mfma_f32_16x16x32_bf16 a[60:63],a[152:155],v[216:219],a[60:63]
buffer_load_dwordx4 v[220:223],v6,s[28:31],s78 offen offset:3072 sc1
v_mfma_f32_16x16x32_bf16 a[60:63],a[148:151],v[184:187],a[60:63]
ds_read_b64_tr_b16 a[148:149],v2 offset:10336
ds_read_b64_tr_b16 a[150:151],v2 offset:15456
v_mfma_f32_16x16x32_bf16 a[60:63],a[144:147],v[152:155],a[60:63]
ds_read_b64_tr_b16 a[144:145],v2 offset:96
v_mfma_f32_16x16x32_bf16 a[64:67],a[160:163],v[128:131],a[64:67]
ds_read_b64_tr_b16 a[146:147],v2 offset:5216
v_mfma_f32_16x16x32_bf16 a[64:67],a[164:167],v[160:163],a[64:67]
buffer_load_dwordx4 v9,s[72:75],s83 offen offset:2048 lds
v_mfma_f32_16x16x32_bf16 a[64:67],a[168:171],v[192:195],a[64:67]
ds_read_b64_tr_b16 a[152:153],v2 offset:20576
ds_read_b64_tr_b16 a[154:155],v2 offset:25696
v_mfma_f32_16x16x32_bf16 a[64:67],a[172:175],v[224:227],a[64:67]
v_mfma_f32_16x16x32_bf16 a[68:71],a[172:175],v[232:235],a[68:71]
v_mfma_f32_16x16x32_bf16 a[68:71],a[168:171],v[200:203],a[68:71]
buffer_load_dwordx4 v[228:231],v6,s[28:31],s79 offen sc1
v_mfma_f32_16x16x32_bf16 a[68:71],a[164:167],v[168:171],a[68:71]
v_mfma_f32_16x16x32_bf16 a[68:71],a[160:163],v[136:139],a[68:71]
v_mfma_f32_16x16x32_bf16 a[72:75],a[160:163],v[144:147],a[72:75]
)A"
R"A(v_mfma_f32_16x16x32_bf16 a[72:75],a[164:167],v[176:179],a[72:75]
buffer_load_dwordx4 v[236:239],v6,s[28:31],s79 offen offset:1024 sc1
v_mfma_f32_16x16x32_bf16 a[72:75],a[168:171],v[208:211],a[72:75]
v_mfma_f32_16x16x32_bf16 a[72:75],a[172:175],v[240:243],a[72:75]
v_mfma_f32_16x16x32_bf16 a[76:79],a[172:175],v[248:251],a[76:79]
ds_read_b64_tr_b16 a[172:173],v1 offset:30848
ds_read_b64_tr_b16 a[174:175],v1 offset:35968
v_mfma_f32_16x16x32_bf16 a[76:79],a[168:171],v[216:219],a[76:79]
buffer_load_dwordx4 v10,s[72:75],s83 offen offset:3072 lds
v_mfma_f32_16x16x32_bf16 a[76:79],a[164:167],v[184:187],a[76:79]
ds_read_b64_tr_b16 a[164:165],v1 offset:10368
ds_read_b64_tr_b16 a[166:167],v1 offset:15488
v_mfma_f32_16x16x32_bf16 a[76:79],a[160:163],v[152:155],a[76:79]
ds_read_b64_tr_b16 a[160:161],v1 offset:128
v_mfma_f32_16x16x32_bf16 a[80:83],a[176:179],v[128:131],a[80:83]
ds_read_b64_tr_b16 a[162:163],v1 offset:5248
v_mfma_f32_16x16x32_bf16 a[80:83],a[180:183],v[160:163],a[80:83]
buffer_load_dwordx4 v[244:247],v6,s[28:31],s79 offen offset:2048 sc1
v_mfma_f32_16x16x32_bf16 a[80:83],a[184:187],v[192:195],a[80:83]
ds_read_b64_tr_b16 a[168:169],v1 offset:20608
ds_read_b64_tr_b16 a[170:171],v1 offset:25728
v_mfma_f32_16x16x32_bf16 a[80:83],a[188:191],v[224:227],a[80:83]
v_mfma_f32_16x16x32_bf16 a[84:87],a[188:191],v[232:235],a[84:87]
v_mfma_f32_16x16x32_bf16 a[84:87],a[184:187],v[200:203],a[84:87]
buffer_load_dwordx4 v[252:255],v6,s[28:31],s79 offen offset:3072 sc1
v_mfma_f32_16x16x32_bf16 a[84:87],a[180:183],v[168:171],a[84:87]
v_mfma_f32_16x16x32_bf16 a[84:87],a[176:179],v[136:139],a[84:87]
v_mfma_f32_16x16x32_bf16 a[88:91],a[176:179],v[144:147],a[88:91]
v_mfma_f32_16x16x32_bf16 a[88:91],a[180:183],v[176:179],a[88:91]
s_add_u32 m0,s82,50176
v_mfma_f32_16x16x32_bf16 a[88:91],a[184:187],v[208:211],a[88:91]
v_mfma_f32_16x16x32_bf16 a[88:91],a[188:191],v[240:243],a[88:91]
buffer_load_dwordx2 v[26:27],v24,s[64:67],s94 offen offset:1024 sc1
v_mfma_f32_16x16x32_bf16 a[92:95],a[188:191],v[248:251],a[92:95]
ds_read_b64_tr_b16 a[188:189],v2 offset:30880
ds_read_b64_tr_b16 a[190:191],v2 offset:36000
v_mfma_f32_16x16x32_bf16 a[92:95],a[184:187],v[216:219],a[92:95]
ds_read_b64_tr_b16 a[184:185],v2 offset:20640
v_mfma_f32_16x16x32_bf16 a[92:95],a[180:183],v[184:187],a[92:95]
ds_read_b64_tr_b16 a[180:181],v2 offset:10400
v_mfma_f32_16x16x32_bf16 a[92:95],a[176:179],v[152:155],a[92:95]
buffer_load_dwordx4 v11,s[72:75],s83 offen lds
v_mfma_f32_16x16x32_bf16 a[192:195],a[96:99],v[64:67],a[192:195]
ds_read_b64_tr_b16 a[176:177],v2 offset:160
ds_read_b64_tr_b16 a[178:179],v2 offset:5280
v_mfma_f32_16x16x32_bf16 a[192:195],a[100:103],v[68:71],a[192:195]
s_waitcnt vmcnt(16)
ds_read_b64_tr_b16 a[182:183],v2 offset:15520
v_mfma_f32_16x16x32_bf16 a[192:195],a[104:107],v[72:75],a[192:195]
ds_read_b64_tr_b16 a[186:187],v2 offset:25760
v_mfma_f32_16x16x32_bf16 a[192:195],a[108:111],v[76:79],a[192:195]
v_mfma_f32_16x16x32_bf16 a[196:199],a[108:111],v[92:95],a[196:199]
v_mfma_f32_16x16x32_bf16 a[196:199],a[104:107],v[88:91],a[196:199]
v_mfma_f32_16x16x32_bf16 a[196:199],a[100:103],v[84:87],a[196:199]
v_mfma_f32_16x16x32_bf16 a[196:199],a[96:99],v[80:83],a[196:199]
v_mfma_f32_16x16x32_bf16 a[200:203],a[96:99],v[96:99],a[200:203]
v_mfma_f32_16x16x32_bf16 a[200:203],a[100:103],v[100:103],a[200:203]
v_mfma_f32_16x16x32_bf16 a[200:203],a[104:107],v[104:107],a[200:203]
v_mfma_f32_16x16x32_bf16 a[200:203],a[108:111],v[108:111],a[200:203]
v_mfma_f32_16x16x32_bf16 a[204:207],a[108:111],v[124:127],a[204:207]
ds_read_b64_tr_b16 a[108:109],v1 offset:30912
ds_read_b64_tr_b16 a[110:111],v1 offset:36032
v_mfma_f32_16x16x32_bf16 a[204:207],a[104:107],v[120:123],a[204:207]
ds_read_b64_tr_b16 a[104:105],v1 offset:20672
v_mfma_f32_16x16x32_bf16 a[204:207],a[100:103],v[116:119],a[204:207]
ds_read_b64_tr_b16 a[100:101],v1 offset:10432
v_mfma_f32_16x16x32_bf16 a[204:207],a[96:99],v[112:115],a[204:207]
s_barrier
ds_read_b64_tr_b16 a[96:97],v1 offset:192
ds_read_b64_tr_b16 a[98:99],v1 offset:5312
v_mfma_f32_16x16x32_bf16 a[208:211],a[112:115],v[64:67],a[208:211]
ds_read_b64_tr_b16 a[102:103],v1 offset:15552
v_mfma_f32_16x16x32_bf16 a[208:211],a[116:119],v[68:71],a[208:211]
ds_read_b64_tr_b16 a[106:107],v1 offset:25792
v_mfma_f32_16x16x32_bf16 a[208:211],a[120:123],v[72:75],a[208:211]
v_mfma_f32_16x16x32_bf16 a[208:211],a[124:127],v[76:79],a[208:211]
v_mfma_f32_16x16x32_bf16 a[212:215],a[124:127],v[92:95],a[212:215]
v_mfma_f32_16x16x32_bf16 a[212:215],a[120:123],v[88:91],a[212:215]
v_mfma_f32_16x16x32_bf16 a[212:215],a[116:119],v[84:87],a[212:215]
v_mfma_f32_16x16x32_bf16 a[212:215],a[112:115],v[80:83],a[212:215]
v_mfma_f32_16x16x32_bf16 a[216:219],a[112:115],v[96:99],a[216:219]
v_mfma_f32_16x16x32_bf16 a[216:219],a[116:119],v[100:103],a[216:219]
s_cmp_ge_u32 0,s71
s_cselect_b32 s76,3,1
s_lshl_b32 s76,s76,16
v_mfma_f32_16x16x32_bf16 a[216:219],a[120:123],v[104:107],a[216:219]
s_add_u32 s77,s76,4096
s_add_u32 s78,s76,8192
s_add_u32 s79,s76,12288
v_mfma_f32_16x16x32_bf16 a[216:219],a[124:127],v[108:111],a[216:219]
s_max_i32 s76,s76,s18
s_max_i32 s77,s77,s19
s_max_i32 s78,s78,s100
v_mfma_f32_16x16x32_bf16 a[220:223],a[124:127],v[124:127],a[220:223]
s_max_i32 s79,s79,s101
ds_read_b64_tr_b16 a[124:125],v2 offset:30944
ds_read_b64_tr_b16 a[126:127],v2 offset:36064
v_mfma_f32_16x16x32_bf16 a[220:223],a[120:123],v[120:123],a[220:223]
ds_read_b64_tr_b16 a[120:121],v2 offset:20704
v_mfma_f32_16x16x32_bf16 a[220:223],a[116:119],v[116:119],a[220:223]
ds_read_b64_tr_b16 a[116:117],v2 offset:10464
v_mfma_f32_16x16x32_bf16 a[220:223],a[112:115],v[112:115],a[220:223]
s_waitcnt lgkmcnt(4)
ds_read_b64_tr_b16 a[112:113],v2 offset:224
v_mfma_f32_16x16x32_bf16 a[224:227],a[128:131],v[64:67],a[224:227]
ds_read_b64_tr_b16 a[114:115],v2 offset:5344
ds_read_b64_tr_b16 a[118:119],v2 offset:15584
v_mfma_f32_16x16x32_bf16 a[224:227],a[132:135],v[68:71],a[224:227]
ds_read_b64_tr_b16 a[122:123],v2 offset:25824
v_mfma_f32_16x16x32_bf16 a[224:227],a[136:139],v[72:75],a[224:227]
v_mfma_f32_16x16x32_bf16 a[224:227],a[140:143],v[76:79],a[224:227]
v_mfma_f32_16x16x32_bf16 a[228:231],a[140:143],v[92:95],a[228:231]
v_mfma_f32_16x16x32_bf16 a[228:231],a[136:139],v[88:91],a[228:231]
v_mfma_f32_16x16x32_bf16 a[228:231],a[132:135],v[84:87],a[228:231]
v_mfma_f32_16x16x32_bf16 a[228:231],a[128:131],v[80:83],a[228:231]
s_waitcnt vmcnt(1)
v_cmp_ne_u32 s[90:91],s68,v26
v_cmp_ne_u32 s[92:93],s69,v27
s_nop 0
s_or_b64 s[90:91],s[90:91],s[92:93]
s_cbranch_scc1 L_fr8_%=
L_fo8_%=:
v_mfma_f32_16x16x32_bf16 a[232:235],a[128:131],v[96:99],a[232:235]
v_mfma_f32_16x16x32_bf16 a[232:235],a[132:135],v[100:103],a[232:235]
v_mfma_f32_16x16x32_bf16 a[232:235],a[136:139],v[104:107],a[232:235]
v_mfma_f32_16x16x32_bf16 a[232:235],a[140:143],v[108:111],a[232:235]
v_mfma_f32_16x16x32_bf16 a[236:239],a[140:143],v[124:127],a[236:239]
ds_read_b64_tr_b16 a[140:141],v1 offset:30976
ds_read_b64_tr_b16 a[142:143],v1 offset:36096
v_mfma_f32_16x16x32_bf16 a[236:239],a[136:139],v[120:123],a[236:239]
ds_read_b64_tr_b16 a[136:137],v1 offset:20736
v_mfma_f32_16x16x32_bf16 a[236:239],a[132:135],v[116:119],a[236:239]
ds_read_b64_tr_b16 a[132:133],v1 offset:10496
v_mfma_f32_16x16x32_bf16 a[236:239],a[128:131],v[112:115],a[236:239]
ds_read_b64_tr_b16 a[128:129],v1 offset:256
v_mfma_f32_16x16x32_bf16 a[240:243],a[144:147],v[64:67],a[240:243]
ds_read_b64_tr_b16 a[130:131],v1 offset:5376
ds_read_b64_tr_b16 a[134:135],v1 offset:15616
v_mfma_f32_16x16x32_bf16 a[240:243],a[148:151],v[68:71],a[240:243]
buffer_load_dwordx4 v[128:131],v6,s[28:31],s76 offen sc1
v_mfma_f32_16x16x32_bf16 a[240:243],a[152:155],v[72:75],a[240:243]
ds_read_b64_tr_b16 a[138:139],v1 offset:25856
v_subrev_u32 v1,122880,v1
v_mfma_f32_16x16x32_bf16 a[240:243],a[156:159],v[76:79],a[240:243]
v_mfma_f32_16x16x32_bf16 a[244:247],a[156:159],v[92:95],a[244:247]
v_mfma_f32_16x16x32_bf16 a[244:247],a[152:155],v[88:91],a[244:247]
v_mfma_f32_16x16x32_bf16 a[244:247],a[148:151],v[84:87],a[244:247]
v_mfma_f32_16x16x32_bf16 a[244:247],a[144:147],v[80:83],a[244:247]
buffer_load_dwordx4 v[160:163],v6,s[28:31],s76 offen offset:1024 sc1
v_mfma_f32_16x16x32_bf16 a[248:251],a[144:147],v[96:99],a[248:251]
v_mfma_f32_16x16x32_bf16 a[248:251],a[148:151],v[100:103],a[248:251]
v_mfma_f32_16x16x32_bf16 a[248:251],a[152:155],v[104:107],a[248:251]
v_mfma_f32_16x16x32_bf16 a[248:251],a[156:159],v[108:111],a[248:251]
v_mfma_f32_16x16x32_bf16 a[252:255],a[156:159],v[124:127],a[252:255]
ds_read_b64_tr_b16 a[156:157],v2 offset:31008
ds_read_b64_tr_b16 a[158:159],v2 offset:36128
v_mfma_f32_16x16x32_bf16 a[252:255],a[152:155],v[120:123],a[252:255]
buffer_load_dwordx4 v[192:195],v6,s[28:31],s76 offen offset:2048 sc1
v_mfma_f32_16x16x32_bf16 a[252:255],a[148:151],v[116:119],a[252:255]
ds_read_b64_tr_b16 a[148:149],v2 offset:10528
ds_read_b64_tr_b16 a[150:151],v2 offset:15648
v_mfma_f32_16x16x32_bf16 a[252:255],a[144:147],v[112:115],a[252:255]
ds_read_b64_tr_b16 a[144:145],v2 offset:288
v_mfma_f32_16x16x32_bf16 a[0:3],a[160:163],v[64:67],a[0:3]
ds_read_b64_tr_b16 a[146:147],v2 offset:5408
v_mfma_f32_16x16x32_bf16 a[0:3],a[164:167],v[68:71],a[0:3]
ds_read_b64_tr_b16 a[152:153],v2 offset:20768
v_mfma_f32_16x16x32_bf16 a[0:3],a[168:171],v[72:75],a[0:3]
ds_read_b64_tr_b16 a[154:155],v2 offset:25888
v_subrev_u32 v2,122880,v2
v_mfma_f32_16x16x32_bf16 a[0:3],a[172:175],v[76:79],a[0:3]
buffer_load_dwordx4 v[224:227],v6,s[28:31],s76 offen offset:3072 sc1
v_mfma_f32_16x16x32_bf16 a[4:7],a[172:175],v[92:95],a[4:7]
v_mfma_f32_16x16x32_bf16 a[4:7],a[168:171],v[88:91],a[4:7]
v_mfma_f32_16x16x32_bf16 a[4:7],a[164:167],v[84:87],a[4:7]
v_mfma_f32_16x16x32_bf16 a[4:7],a[160:163],v[80:83],a[4:7]
v_mfma_f32_16x16x32_bf16 a[8:11],a[160:163],v[96:99],a[8:11]
v_mfma_f32_16x16x32_bf16 a[8:11],a[164:167],v[100:103],a[8:11]
buffer_load_dwordx4 v[136:139],v6,s[28:31],s77 offen sc1
v_mfma_f32_16x16x32_bf16 a[8:11],a[168:171],v[104:107],a[8:11]
v_mfma_f32_16x16x32_bf16 a[8:11],a[172:175],v[108:111],a[8:11]
v_mfma_f32_16x16x32_bf16 a[12:15],a[172:175],v[124:127],a[12:15]
ds_read_b64_tr_b16 a[172:173],v1 offset:30720
ds_read_b64_tr_b16 a[174:175],v1 offset:35840
v_mfma_f32_16x16x32_bf16 a[12:15],a[168:171],v[120:123],a[12:15]
ds_read_b64_tr_b16 a[168:169],v1 offset:20480
v_mfma_f32_16x16x32_bf16 a[12:15],a[164:167],v[116:119],a[12:15]
ds_read_b64_tr_b16 a[164:165],v1 offset:10240
v_mfma_f32_16x16x32_bf16 a[12:15],a[160:163],v[112:115],a[12:15]
buffer_load_dwordx4 v[168:171],v6,s[28:31],s77 offen offset:1024 sc1
v_mfma_f32_16x16x32_bf16 a[16:19],a[176:179],v[64:67],a[16:19]
ds_read_b64_tr_b16 a[160:161],v1
ds_read_b64_tr_b16 a[162:163],v1 offset:5120
v_mfma_f32_16x16x32_bf16 a[16:19],a[180:183],v[68:71],a[16:19]
ds_read_b64_tr_b16 a[166:167],v1 offset:15360
v_mfma_f32_16x16x32_bf16 a[16:19],a[184:187],v[72:75],a[16:19]
ds_read_b64_tr_b16 a[170:171],v1 offset:25600
v_mfma_f32_16x16x32_bf16 a[16:19],a[188:191],v[76:79],a[16:19]
v_mfma_f32_16x16x32_bf16 a[20:23],a[188:191],v[92:95],a[20:23]
v_mfma_f32_16x16x32_bf16 a[20:23],a[184:187],v[88:91],a[20:23]
buffer_load_dwordx4 v[200:203],v6,s[28:31],s77 offen offset:2048 sc1
v_mfma_f32_16x16x32_bf16 a[20:23],a[180:183],v[84:87],a[20:23]
v_mfma_f32_16x16x32_bf16 a[20:23],a[176:179],v[80:83],a[20:23]
v_mfma_f32_16x16x32_bf16 a[24:27],a[176:179],v[96:99],a[24:27]
v_mfma_f32_16x16x32_bf16 a[24:27],a[180:183],v[100:103],a[24:27]
v_mfma_f32_16x16x32_bf16 a[24:27],a[184:187],v[104:107],a[24:27]
v_mfma_f32_16x16x32_bf16 a[24:27],a[188:191],v[108:111],a[24:27]
buffer_load_dwordx4 v[232:235],v6,s[28:31],s77 offen offset:3072 sc1
v_mfma_f32_16x16x32_bf16 a[28:31],a[188:191],v[124:127],a[28:31]
ds_read_b64_tr_b16 a[188:189],v2 offset:30752
ds_read_b64_tr_b16 a[190:191],v2 offset:35872
v_mfma_f32_16x16x32_bf16 a[28:31],a[184:187],v[120:123],a[28:31]
ds_read_b64_tr_b16 a[184:185],v2 offset:20512
v_mfma_f32_16x16x32_bf16 a[28:31],a[180:183],v[116:119],a[28:31]
ds_read_b64_tr_b16 a[180:181],v2 offset:10272
v_mfma_f32_16x16x32_bf16 a[28:31],a[176:179],v[112:115],a[28:31]
ds_read_b64_tr_b16 a[176:177],v2 offset:32
)A"
R"A(v_mfma_f32_16x16x32_bf16 a[32:35],a[96:99],v[64:67],a[32:35]
ds_read_b64_tr_b16 a[178:179],v2 offset:5152
ds_read_b64_tr_b16 a[182:183],v2 offset:15392
v_mfma_f32_16x16x32_bf16 a[32:35],a[100:103],v[68:71],a[32:35]
buffer_load_dwordx4 v[144:147],v6,s[28:31],s78 offen sc1
v_mfma_f32_16x16x32_bf16 a[32:35],a[104:107],v[72:75],a[32:35]
ds_read_b64_tr_b16 a[186:187],v2 offset:25632
v_mfma_f32_16x16x32_bf16 a[32:35],a[108:111],v[76:79],a[32:35]
v_mfma_f32_16x16x32_bf16 a[36:39],a[108:111],v[92:95],a[36:39]
v_mfma_f32_16x16x32_bf16 a[36:39],a[104:107],v[88:91],a[36:39]
v_mfma_f32_16x16x32_bf16 a[36:39],a[100:103],v[84:87],a[36:39]
v_mfma_f32_16x16x32_bf16 a[36:39],a[96:99],v[80:83],a[36:39]
buffer_load_dwordx4 v[176:179],v6,s[28:31],s78 offen offset:1024 sc1
v_mfma_f32_16x16x32_bf16 a[40:43],a[96:99],v[96:99],a[40:43]
v_mfma_f32_16x16x32_bf16 a[40:43],a[100:103],v[100:103],a[40:43]
v_mfma_f32_16x16x32_bf16 a[40:43],a[104:107],v[104:107],a[40:43]
v_mfma_f32_16x16x32_bf16 a[40:43],a[108:111],v[108:111],a[40:43]
v_mfma_f32_16x16x32_bf16 a[44:47],a[108:111],v[124:127],a[44:47]
ds_read_b64_tr_b16 a[108:109],v1 offset:30784
ds_read_b64_tr_b16 a[110:111],v1 offset:35904
v_mfma_f32_16x16x32_bf16 a[44:47],a[104:107],v[120:123],a[44:47]
buffer_load_dwordx4 v[208:211],v6,s[28:31],s78 offen offset:2048 sc1
v_mfma_f32_16x16x32_bf16 a[44:47],a[100:103],v[116:119],a[44:47]
ds_read_b64_tr_b16 a[100:101],v1 offset:10304
ds_read_b64_tr_b16 a[102:103],v1 offset:15424
v_mfma_f32_16x16x32_bf16 a[44:47],a[96:99],v[112:115],a[44:47]
s_waitcnt lgkmcnt(4)
ds_read_b64_tr_b16 a[96:97],v1 offset:64
v_mfma_f32_16x16x32_bf16 a[48:51],a[112:115],v[64:67],a[48:51]
ds_read_b64_tr_b16 a[98:99],v1 offset:5184
v_mfma_f32_16x16x32_bf16 a[48:51],a[116:119],v[68:71],a[48:51]
ds_read_b64_tr_b16 a[104:105],v1 offset:20544
v_mfma_f32_16x16x32_bf16 a[48:51],a[120:123],v[72:75],a[48:51]
ds_read_b64_tr_b16 a[106:107],v1 offset:25664
v_mfma_f32_16x16x32_bf16 a[48:51],a[124:127],v[76:79],a[48:51]
buffer_load_dwordx4 v[240:243],v6,s[28:31],s78 offen offset:3072 sc1
v_mfma_f32_16x16x32_bf16 a[52:55],a[124:127],v[92:95],a[52:55]
v_mfma_f32_16x16x32_bf16 a[52:55],a[120:123],v[88:91],a[52:55]
v_mfma_f32_16x16x32_bf16 a[52:55],a[116:119],v[84:87],a[52:55]
v_mfma_f32_16x16x32_bf16 a[52:55],a[112:115],v[80:83],a[52:55]
v_mfma_f32_16x16x32_bf16 a[56:59],a[112:115],v[96:99],a[56:59]
v_mfma_f32_16x16x32_bf16 a[56:59],a[116:119],v[100:103],a[56:59]
buffer_load_dwordx4 v[152:155],v6,s[28:31],s79 offen sc1
v_mfma_f32_16x16x32_bf16 a[56:59],a[120:123],v[104:107],a[56:59]
s_cmp_ge_u32 1,s71
s_cselect_b32 s80,5,3
s_mul_i32 s80,s80,163840
v_mfma_f32_16x16x32_bf16 a[56:59],a[124:127],v[108:111],a[56:59]
s_add_u32 s80,s80,s81
s_add_u32 s83,s80,20480
v_mfma_f32_16x16x32_bf16 a[60:63],a[124:127],v[124:127],a[60:63]
ds_read_b64_tr_b16 a[124:125],v2 offset:30816
ds_read_b64_tr_b16 a[126:127],v2 offset:35936
v_mfma_f32_16x16x32_bf16 a[60:63],a[120:123],v[120:123],a[60:63]
ds_read_b64_tr_b16 a[120:121],v2 offset:20576
v_mfma_f32_16x16x32_bf16 a[60:63],a[116:119],v[116:119],a[60:63]
ds_read_b64_tr_b16 a[116:117],v2 offset:10336
v_mfma_f32_16x16x32_bf16 a[60:63],a[112:115],v[112:115],a[60:63]
buffer_load_dwordx4 v[184:187],v6,s[28:31],s79 offen offset:1024 sc1
v_mfma_f32_16x16x32_bf16 a[64:67],a[128:131],v[64:67],a[64:67]
ds_read_b64_tr_b16 a[112:113],v2 offset:96
ds_read_b64_tr_b16 a[114:115],v2 offset:5216
v_mfma_f32_16x16x32_bf16 a[64:67],a[132:135],v[68:71],a[64:67]
ds_read_b64_tr_b16 a[118:119],v2 offset:15456
v_mfma_f32_16x16x32_bf16 a[64:67],a[136:139],v[72:75],a[64:67]
ds_read_b64_tr_b16 a[122:123],v2 offset:25696
v_mfma_f32_16x16x32_bf16 a[64:67],a[140:143],v[76:79],a[64:67]
v_mfma_f32_16x16x32_bf16 a[68:71],a[140:143],v[92:95],a[68:71]
v_mfma_f32_16x16x32_bf16 a[68:71],a[136:139],v[88:91],a[68:71]
buffer_load_dwordx4 v[216:219],v6,s[28:31],s79 offen offset:2048 sc1
v_mfma_f32_16x16x32_bf16 a[68:71],a[132:135],v[84:87],a[68:71]
v_mfma_f32_16x16x32_bf16 a[68:71],a[128:131],v[80:83],a[68:71]
v_mfma_f32_16x16x32_bf16 a[72:75],a[128:131],v[96:99],a[72:75]
v_mfma_f32_16x16x32_bf16 a[72:75],a[132:135],v[100:103],a[72:75]
v_mfma_f32_16x16x32_bf16 a[72:75],a[136:139],v[104:107],a[72:75]
v_mfma_f32_16x16x32_bf16 a[72:75],a[140:143],v[108:111],a[72:75]
buffer_load_dwordx4 v[248:251],v6,s[28:31],s79 offen offset:3072 sc1
v_mfma_f32_16x16x32_bf16 a[76:79],a[140:143],v[124:127],a[76:79]
ds_read_b64_tr_b16 a[140:141],v1 offset:30848
ds_read_b64_tr_b16 a[142:143],v1 offset:35968
v_mfma_f32_16x16x32_bf16 a[76:79],a[136:139],v[120:123],a[76:79]
ds_read_b64_tr_b16 a[136:137],v1 offset:20608
v_mfma_f32_16x16x32_bf16 a[76:79],a[132:135],v[116:119],a[76:79]
s_add_u32 m0,s82,81920
ds_read_b64_tr_b16 a[132:133],v1 offset:10368
v_mfma_f32_16x16x32_bf16 a[76:79],a[128:131],v[112:115],a[76:79]
ds_read_b64_tr_b16 a[128:129],v1 offset:128
v_mfma_f32_16x16x32_bf16 a[80:83],a[144:147],v[64:67],a[80:83]
ds_read_b64_tr_b16 a[130:131],v1 offset:5248
ds_read_b64_tr_b16 a[134:135],v1 offset:15488
v_mfma_f32_16x16x32_bf16 a[80:83],a[148:151],v[68:71],a[80:83]
buffer_load_dwordx4 v7,s[72:75],s80 offen lds
v_mfma_f32_16x16x32_bf16 a[80:83],a[152:155],v[72:75],a[80:83]
ds_read_b64_tr_b16 a[138:139],v1 offset:25728
v_mfma_f32_16x16x32_bf16 a[80:83],a[156:159],v[76:79],a[80:83]
v_mfma_f32_16x16x32_bf16 a[84:87],a[156:159],v[92:95],a[84:87]
v_mfma_f32_16x16x32_bf16 a[84:87],a[152:155],v[88:91],a[84:87]
v_mfma_f32_16x16x32_bf16 a[84:87],a[148:151],v[84:87],a[84:87]
buffer_load_dwordx4 v8,s[72:75],s80 offen offset:1024 lds
v_mfma_f32_16x16x32_bf16 a[84:87],a[144:147],v[80:83],a[84:87]
v_mfma_f32_16x16x32_bf16 a[88:91],a[144:147],v[96:99],a[88:91]
v_mfma_f32_16x16x32_bf16 a[88:91],a[148:151],v[100:103],a[88:91]
v_mfma_f32_16x16x32_bf16 a[88:91],a[152:155],v[104:107],a[88:91]
v_mfma_f32_16x16x32_bf16 a[88:91],a[156:159],v[108:111],a[88:91]
buffer_load_dwordx4 v9,s[72:75],s80 offen offset:2048 lds
v_mfma_f32_16x16x32_bf16 a[92:95],a[156:159],v[124:127],a[92:95]
ds_read_b64_tr_b16 a[156:157],v2 offset:30880
ds_read_b64_tr_b16 a[158:159],v2 offset:36000
v_mfma_f32_16x16x32_bf16 a[92:95],a[152:155],v[120:123],a[92:95]
ds_read_b64_tr_b16 a[152:153],v2 offset:20640
v_mfma_f32_16x16x32_bf16 a[92:95],a[148:151],v[116:119],a[92:95]
ds_read_b64_tr_b16 a[148:149],v2 offset:10400
v_mfma_f32_16x16x32_bf16 a[92:95],a[144:147],v[112:115],a[92:95]
ds_read_b64_tr_b16 a[144:145],v2 offset:160
v_mfma_f32_16x16x32_bf16 a[192:195],a[160:163],v[132:135],a[192:195]
buffer_load_dwordx4 v10,s[72:75],s80 offen offset:3072 lds
v_mfma_f32_16x16x32_bf16 a[192:195],a[164:167],v[140:143],a[192:195]
ds_read_b64_tr_b16 a[146:147],v2 offset:5280
ds_read_b64_tr_b16 a[150:151],v2 offset:15520
v_mfma_f32_16x16x32_bf16 a[192:195],a[168:171],v[148:151],a[192:195]
s_add_u32 m0,s82,86016
ds_read_b64_tr_b16 a[154:155],v2 offset:25760
v_mfma_f32_16x16x32_bf16 a[192:195],a[172:175],v[156:159],a[192:195]
v_mfma_f32_16x16x32_bf16 a[196:199],a[172:175],v[188:191],a[196:199]
v_mfma_f32_16x16x32_bf16 a[196:199],a[168:171],v[180:183],a[196:199]
buffer_load_dwordx4 v11,s[72:75],s80 offen lds
v_mfma_f32_16x16x32_bf16 a[196:199],a[164:167],v[172:175],a[196:199]
v_mfma_f32_16x16x32_bf16 a[196:199],a[160:163],v[164:167],a[196:199]
s_add_u32 m0,s82,87040
v_mfma_f32_16x16x32_bf16 a[200:203],a[160:163],v[196:199],a[200:203]
v_mfma_f32_16x16x32_bf16 a[200:203],a[164:167],v[204:207],a[200:203]
v_mfma_f32_16x16x32_bf16 a[200:203],a[168:171],v[212:215],a[200:203]
buffer_load_dwordx4 v7,s[72:75],s83 offen lds
v_mfma_f32_16x16x32_bf16 a[200:203],a[172:175],v[220:223],a[200:203]
v_mfma_f32_16x16x32_bf16 a[204:207],a[172:175],v[252:255],a[204:207]
ds_read_b64_tr_b16 a[172:173],v1 offset:30912
ds_read_b64_tr_b16 a[174:175],v1 offset:36032
v_mfma_f32_16x16x32_bf16 a[204:207],a[168:171],v[244:247],a[204:207]
ds_read_b64_tr_b16 a[168:169],v1 offset:20672
v_mfma_f32_16x16x32_bf16 a[204:207],a[164:167],v[236:239],a[204:207]
s_waitcnt vmcnt(22)
ds_read_b64_tr_b16 a[164:165],v1 offset:10432
v_mfma_f32_16x16x32_bf16 a[204:207],a[160:163],v[228:231],a[204:207]
s_barrier
buffer_load_dwordx4 v8,s[72:75],s83 offen offset:1024 lds
v_mfma_f32_16x16x32_bf16 a[208:211],a[176:179],v[132:135],a[208:211]
s_cmp_ge_u32 1,s71
s_cselect_b32 s76,5,3
s_lshl_b32 s76,s76,16
v_mfma_f32_16x16x32_bf16 a[208:211],a[180:183],v[140:143],a[208:211]
s_add_u32 s77,s76,4096
s_add_u32 s78,s76,8192
s_add_u32 s79,s76,12288
v_mfma_f32_16x16x32_bf16 a[208:211],a[184:187],v[148:151],a[208:211]
s_max_i32 s76,s76,s18
s_max_i32 s77,s77,s19
s_max_i32 s78,s78,s100
v_mfma_f32_16x16x32_bf16 a[208:211],a[188:191],v[156:159],a[208:211]
s_max_i32 s79,s79,s101
buffer_load_dwordx4 v9,s[72:75],s83 offen offset:2048 lds
v_mfma_f32_16x16x32_bf16 a[212:215],a[188:191],v[188:191],a[212:215]
ds_read_b64_tr_b16 a[160:161],v1 offset:192
ds_read_b64_tr_b16 a[162:163],v1 offset:5312
v_mfma_f32_16x16x32_bf16 a[212:215],a[184:187],v[180:183],a[212:215]
ds_read_b64_tr_b16 a[166:167],v1 offset:15552
v_mfma_f32_16x16x32_bf16 a[212:215],a[180:183],v[172:175],a[212:215]
ds_read_b64_tr_b16 a[170:171],v1 offset:25792
v_mfma_f32_16x16x32_bf16 a[212:215],a[176:179],v[164:167],a[212:215]
v_mfma_f32_16x16x32_bf16 a[216:219],a[176:179],v[196:199],a[216:219]
buffer_load_dwordx4 v10,s[72:75],s83 offen offset:3072 lds
v_mfma_f32_16x16x32_bf16 a[216:219],a[180:183],v[204:207],a[216:219]
v_mfma_f32_16x16x32_bf16 a[216:219],a[184:187],v[212:215],a[216:219]
s_add_u32 m0,s82,91136
v_mfma_f32_16x16x32_bf16 a[216:219],a[188:191],v[220:223],a[216:219]
v_mfma_f32_16x16x32_bf16 a[220:223],a[188:191],v[252:255],a[220:223]
ds_read_b64_tr_b16 a[188:189],v2 offset:30944
ds_read_b64_tr_b16 a[190:191],v2 offset:36064
v_mfma_f32_16x16x32_bf16 a[220:223],a[184:187],v[244:247],a[220:223]
buffer_load_dwordx4 v11,s[72:75],s83 offen lds
v_mfma_f32_16x16x32_bf16 a[220:223],a[180:183],v[236:239],a[220:223]
ds_read_b64_tr_b16 a[180:181],v2 offset:10464
ds_read_b64_tr_b16 a[182:183],v2 offset:15584
v_mfma_f32_16x16x32_bf16 a[220:223],a[176:179],v[228:231],a[220:223]
s_waitcnt lgkmcnt(4)
ds_read_b64_tr_b16 a[176:177],v2 offset:224
v_mfma_f32_16x16x32_bf16 a[224:227],a[96:99],v[132:135],a[224:227]
ds_read_b64_tr_b16 a[178:179],v2 offset:5344
v_mfma_f32_16x16x32_bf16 a[224:227],a[100:103],v[140:143],a[224:227]
ds_read_b64_tr_b16 a[184:185],v2 offset:20704
v_mfma_f32_16x16x32_bf16 a[224:227],a[104:107],v[148:151],a[224:227]
ds_read_b64_tr_b16 a[186:187],v2 offset:25824
v_mfma_f32_16x16x32_bf16 a[224:227],a[108:111],v[156:159],a[224:227]
v_mfma_f32_16x16x32_bf16 a[228:231],a[108:111],v[188:191],a[228:231]
v_mfma_f32_16x16x32_bf16 a[228:231],a[104:107],v[180:183],a[228:231]
v_mfma_f32_16x16x32_bf16 a[228:231],a[100:103],v[172:175],a[228:231]
v_mfma_f32_16x16x32_bf16 a[228:231],a[96:99],v[164:167],a[228:231]
buffer_load_dwordx4 v[64:67],v6,s[28:31],s76 offen sc1
v_mfma_f32_16x16x32_bf16 a[232:235],a[96:99],v[196:199],a[232:235]
v_mfma_f32_16x16x32_bf16 a[232:235],a[100:103],v[204:207],a[232:235]
v_mfma_f32_16x16x32_bf16 a[232:235],a[104:107],v[212:215],a[232:235]
v_mfma_f32_16x16x32_bf16 a[232:235],a[108:111],v[220:223],a[232:235]
v_mfma_f32_16x16x32_bf16 a[236:239],a[108:111],v[252:255],a[236:239]
buffer_load_dwordx4 v[68:71],v6,s[28:31],s76 offen offset:1024 sc1
v_mfma_f32_16x16x32_bf16 a[236:239],a[104:107],v[244:247],a[236:239]
ds_read_b64_tr_b16 a[104:105],v1 offset:20736
ds_read_b64_tr_b16 a[106:107],v1 offset:25856
v_mfma_f32_16x16x32_bf16 a[236:239],a[100:103],v[236:239],a[236:239]
ds_read_b64_tr_b16 a[100:101],v1 offset:10496
v_mfma_f32_16x16x32_bf16 a[236:239],a[96:99],v[228:231],a[236:239]
ds_read_b64_tr_b16 a[96:97],v1 offset:256
v_mfma_f32_16x16x32_bf16 a[240:243],a[112:115],v[132:135],a[240:243]
ds_read_b64_tr_b16 a[98:99],v1 offset:5376
v_mfma_f32_16x16x32_bf16 a[240:243],a[116:119],v[140:143],a[240:243]
buffer_load_dwordx4 v[72:75],v6,s[28:31],s76 offen offset:2048 sc1
v_mfma_f32_16x16x32_bf16 a[240:243],a[120:123],v[148:151],a[240:243]
ds_read_b64_tr_b16 a[102:103],v1 offset:15616
ds_read_b64_tr_b16 a[108:109],v1 offset:30976
v_mfma_f32_16x16x32_bf16 a[240:243],a[124:127],v[156:159],a[240:243]
)A"
R"A(ds_read_b64_tr_b16 a[110:111],v1 offset:36096
v_add_u32 v1,40960,v1
v_mfma_f32_16x16x32_bf16 a[244:247],a[124:127],v[188:191],a[244:247]
v_add_u32 v42,40960,v1
v_add_u32 v44,81920,v1
v_mfma_f32_16x16x32_bf16 a[244:247],a[120:123],v[180:183],a[244:247]
v_mfma_f32_16x16x32_bf16 a[244:247],a[116:119],v[172:175],a[244:247]
buffer_load_dwordx4 v[76:79],v6,s[28:31],s76 offen offset:3072 sc1
v_mfma_f32_16x16x32_bf16 a[244:247],a[112:115],v[164:167],a[244:247]
v_mfma_f32_16x16x32_bf16 a[248:251],a[112:115],v[196:199],a[248:251]
v_mfma_f32_16x16x32_bf16 a[248:251],a[116:119],v[204:207],a[248:251]
v_mfma_f32_16x16x32_bf16 a[248:251],a[120:123],v[212:215],a[248:251]
v_mfma_f32_16x16x32_bf16 a[248:251],a[124:127],v[220:223],a[248:251]
buffer_load_dwordx4 v[80:83],v6,s[28:31],s77 offen sc1
v_mfma_f32_16x16x32_bf16 a[252:255],a[124:127],v[252:255],a[252:255]
ds_read_b64_tr_b16 a[124:125],v2 offset:31008
ds_read_b64_tr_b16 a[126:127],v2 offset:36128
v_mfma_f32_16x16x32_bf16 a[252:255],a[120:123],v[244:247],a[252:255]
ds_read_b64_tr_b16 a[120:121],v2 offset:20768
v_mfma_f32_16x16x32_bf16 a[252:255],a[116:119],v[236:239],a[252:255]
ds_read_b64_tr_b16 a[116:117],v2 offset:10528
v_mfma_f32_16x16x32_bf16 a[252:255],a[112:115],v[228:231],a[252:255]
ds_read_b64_tr_b16 a[112:113],v2 offset:288
v_mfma_f32_16x16x32_bf16 a[0:3],a[128:131],v[132:135],a[0:3]
buffer_load_dwordx4 v[84:87],v6,s[28:31],s77 offen offset:1024 sc1
v_mfma_f32_16x16x32_bf16 a[0:3],a[132:135],v[140:143],a[0:3]
ds_read_b64_tr_b16 a[114:115],v2 offset:5408
ds_read_b64_tr_b16 a[118:119],v2 offset:15648
v_mfma_f32_16x16x32_bf16 a[0:3],a[136:139],v[148:151],a[0:3]
ds_read_b64_tr_b16 a[122:123],v2 offset:25888
v_add_u32 v2,40960,v2
v_mfma_f32_16x16x32_bf16 a[0:3],a[140:143],v[156:159],a[0:3]
v_add_u32 v43,40960,v2
v_add_u32 v45,81920,v2
v_mfma_f32_16x16x32_bf16 a[4:7],a[140:143],v[188:191],a[4:7]
v_mfma_f32_16x16x32_bf16 a[4:7],a[136:139],v[180:183],a[4:7]
buffer_load_dwordx4 v[88:91],v6,s[28:31],s77 offen offset:2048 sc1
v_mfma_f32_16x16x32_bf16 a[4:7],a[132:135],v[172:175],a[4:7]
v_mfma_f32_16x16x32_bf16 a[4:7],a[128:131],v[164:167],a[4:7]
v_mfma_f32_16x16x32_bf16 a[8:11],a[128:131],v[196:199],a[8:11]
v_mfma_f32_16x16x32_bf16 a[8:11],a[132:135],v[204:207],a[8:11]
v_mfma_f32_16x16x32_bf16 a[8:11],a[136:139],v[212:215],a[8:11]
buffer_load_dwordx4 v[92:95],v6,s[28:31],s77 offen offset:3072 sc1
v_mfma_f32_16x16x32_bf16 a[8:11],a[140:143],v[220:223],a[8:11]
v_mfma_f32_16x16x32_bf16 a[12:15],a[140:143],v[252:255],a[12:15]
ds_read_b64_tr_b16 a[140:141],v1 offset:30720
ds_read_b64_tr_b16 a[142:143],v1 offset:35840
v_mfma_f32_16x16x32_bf16 a[12:15],a[136:139],v[244:247],a[12:15]
ds_read_b64_tr_b16 a[136:137],v1 offset:20480
v_mfma_f32_16x16x32_bf16 a[12:15],a[132:135],v[236:239],a[12:15]
ds_read_b64_tr_b16 a[132:133],v1 offset:10240
v_mfma_f32_16x16x32_bf16 a[12:15],a[128:131],v[228:231],a[12:15]
buffer_load_dwordx4 v[96:99],v6,s[28:31],s78 offen sc1
v_mfma_f32_16x16x32_bf16 a[16:19],a[144:147],v[132:135],a[16:19]
ds_read_b64_tr_b16 a[128:129],v1
ds_read_b64_tr_b16 a[130:131],v1 offset:5120
v_mfma_f32_16x16x32_bf16 a[16:19],a[148:151],v[140:143],a[16:19]
ds_read_b64_tr_b16 a[134:135],v1 offset:15360
v_mfma_f32_16x16x32_bf16 a[16:19],a[152:155],v[148:151],a[16:19]
ds_read_b64_tr_b16 a[138:139],v1 offset:25600
v_mfma_f32_16x16x32_bf16 a[16:19],a[156:159],v[156:159],a[16:19]
v_mfma_f32_16x16x32_bf16 a[20:23],a[156:159],v[188:191],a[20:23]
buffer_load_dwordx4 v[100:103],v6,s[28:31],s78 offen offset:1024 sc1
v_mfma_f32_16x16x32_bf16 a[20:23],a[152:155],v[180:183],a[20:23]
v_mfma_f32_16x16x32_bf16 a[20:23],a[148:151],v[172:175],a[20:23]
v_mfma_f32_16x16x32_bf16 a[20:23],a[144:147],v[164:167],a[20:23]
v_mfma_f32_16x16x32_bf16 a[24:27],a[144:147],v[196:199],a[24:27]
v_mfma_f32_16x16x32_bf16 a[24:27],a[148:151],v[204:207],a[24:27]
buffer_load_dwordx4 v[104:107],v6,s[28:31],s78 offen offset:2048 sc1
v_mfma_f32_16x16x32_bf16 a[24:27],a[152:155],v[212:215],a[24:27]
v_mfma_f32_16x16x32_bf16 a[24:27],a[156:159],v[220:223],a[24:27]
v_mfma_f32_16x16x32_bf16 a[28:31],a[156:159],v[252:255],a[28:31]
ds_read_b64_tr_b16 a[156:157],v2 offset:30752
ds_read_b64_tr_b16 a[158:159],v2 offset:35872
v_mfma_f32_16x16x32_bf16 a[28:31],a[152:155],v[244:247],a[28:31]
ds_read_b64_tr_b16 a[152:153],v2 offset:20512
v_mfma_f32_16x16x32_bf16 a[28:31],a[148:151],v[236:239],a[28:31]
buffer_load_dwordx4 v[108:111],v6,s[28:31],s78 offen offset:3072 sc1
v_mfma_f32_16x16x32_bf16 a[28:31],a[144:147],v[228:231],a[28:31]
s_cmp_ge_u32 2,s71
s_cselect_b32 s80,7,5
s_mul_i32 s80,s80,163840
v_mfma_f32_16x16x32_bf16 a[32:35],a[160:163],v[132:135],a[32:35]
s_add_u32 s80,s80,s81
s_add_u32 s83,s80,20480
ds_read_b64_tr_b16 a[144:145],v2 offset:32
v_mfma_f32_16x16x32_bf16 a[32:35],a[164:167],v[140:143],a[32:35]
ds_read_b64_tr_b16 a[146:147],v2 offset:5152
ds_read_b64_tr_b16 a[148:149],v2 offset:10272
v_mfma_f32_16x16x32_bf16 a[32:35],a[168:171],v[148:151],a[32:35]
ds_read_b64_tr_b16 a[150:151],v2 offset:15392
v_mfma_f32_16x16x32_bf16 a[32:35],a[172:175],v[156:159],a[32:35]
buffer_load_dwordx4 v[112:115],v6,s[28:31],s79 offen sc1
v_mfma_f32_16x16x32_bf16 a[36:39],a[172:175],v[188:191],a[36:39]
ds_read_b64_tr_b16 a[154:155],v2 offset:25632
v_mfma_f32_16x16x32_bf16 a[36:39],a[168:171],v[180:183],a[36:39]
v_mfma_f32_16x16x32_bf16 a[36:39],a[164:167],v[172:175],a[36:39]
v_mfma_f32_16x16x32_bf16 a[36:39],a[160:163],v[164:167],a[36:39]
v_mfma_f32_16x16x32_bf16 a[40:43],a[160:163],v[196:199],a[40:43]
buffer_load_dwordx4 v[116:119],v6,s[28:31],s79 offen offset:1024 sc1
v_mfma_f32_16x16x32_bf16 a[40:43],a[164:167],v[204:207],a[40:43]
v_mfma_f32_16x16x32_bf16 a[40:43],a[168:171],v[212:215],a[40:43]
v_mfma_f32_16x16x32_bf16 a[40:43],a[172:175],v[220:223],a[40:43]
v_mfma_f32_16x16x32_bf16 a[44:47],a[172:175],v[252:255],a[44:47]
ds_read_b64_tr_b16 a[172:173],v1 offset:30784
ds_read_b64_tr_b16 a[174:175],v1 offset:35904
v_mfma_f32_16x16x32_bf16 a[44:47],a[168:171],v[244:247],a[44:47]
buffer_load_dwordx4 v[120:123],v6,s[28:31],s79 offen offset:2048 sc1
v_mfma_f32_16x16x32_bf16 a[44:47],a[164:167],v[236:239],a[44:47]
ds_read_b64_tr_b16 a[164:165],v1 offset:10304
ds_read_b64_tr_b16 a[166:167],v1 offset:15424
v_mfma_f32_16x16x32_bf16 a[44:47],a[160:163],v[228:231],a[44:47]
s_waitcnt lgkmcnt(4)
ds_read_b64_tr_b16 a[160:161],v1 offset:64
v_mfma_f32_16x16x32_bf16 a[48:51],a[176:179],v[132:135],a[48:51]
ds_read_b64_tr_b16 a[162:163],v1 offset:5184
v_mfma_f32_16x16x32_bf16 a[48:51],a[180:183],v[140:143],a[48:51]
ds_read_b64_tr_b16 a[168:169],v1 offset:20544
v_mfma_f32_16x16x32_bf16 a[48:51],a[184:187],v[148:151],a[48:51]
buffer_load_dwordx4 v[124:127],v6,s[28:31],s79 offen offset:3072 sc1
v_mfma_f32_16x16x32_bf16 a[48:51],a[188:191],v[156:159],a[48:51]
ds_read_b64_tr_b16 a[170:171],v1 offset:25664
v_mfma_f32_16x16x32_bf16 a[52:55],a[188:191],v[188:191],a[52:55]
s_add_u32 m0,s82,122880
v_mfma_f32_16x16x32_bf16 a[52:55],a[184:187],v[180:183],a[52:55]
v_mfma_f32_16x16x32_bf16 a[52:55],a[180:183],v[172:175],a[52:55]
v_mfma_f32_16x16x32_bf16 a[52:55],a[176:179],v[164:167],a[52:55]
buffer_load_dwordx4 v7,s[72:75],s80 offen lds
v_mfma_f32_16x16x32_bf16 a[56:59],a[176:179],v[196:199],a[56:59]
v_mfma_f32_16x16x32_bf16 a[56:59],a[180:183],v[204:207],a[56:59]
v_mfma_f32_16x16x32_bf16 a[56:59],a[184:187],v[212:215],a[56:59]
v_mfma_f32_16x16x32_bf16 a[56:59],a[188:191],v[220:223],a[56:59]
v_mfma_f32_16x16x32_bf16 a[60:63],a[188:191],v[252:255],a[60:63]
buffer_load_dwordx4 v8,s[72:75],s80 offen offset:1024 lds
v_mfma_f32_16x16x32_bf16 a[60:63],a[184:187],v[244:247],a[60:63]
ds_read_b64_tr_b16 a[184:185],v2 offset:20576
ds_read_b64_tr_b16 a[186:187],v2 offset:25696
v_mfma_f32_16x16x32_bf16 a[60:63],a[180:183],v[236:239],a[60:63]
ds_read_b64_tr_b16 a[180:181],v2 offset:10336
v_mfma_f32_16x16x32_bf16 a[60:63],a[176:179],v[228:231],a[60:63]
ds_read_b64_tr_b16 a[176:177],v2 offset:96
v_mfma_f32_16x16x32_bf16 a[64:67],a[96:99],v[132:135],a[64:67]
ds_read_b64_tr_b16 a[178:179],v2 offset:5216
v_mfma_f32_16x16x32_bf16 a[64:67],a[100:103],v[140:143],a[64:67]
buffer_load_dwordx4 v9,s[72:75],s80 offen offset:2048 lds
v_mfma_f32_16x16x32_bf16 a[64:67],a[104:107],v[148:151],a[64:67]
ds_read_b64_tr_b16 a[182:183],v2 offset:15456
ds_read_b64_tr_b16 a[188:189],v2 offset:30816
v_mfma_f32_16x16x32_bf16 a[64:67],a[108:111],v[156:159],a[64:67]
ds_read_b64_tr_b16 a[190:191],v2 offset:35936
v_mfma_f32_16x16x32_bf16 a[68:71],a[108:111],v[188:191],a[68:71]
v_mfma_f32_16x16x32_bf16 a[68:71],a[104:107],v[180:183],a[68:71]
v_mfma_f32_16x16x32_bf16 a[68:71],a[100:103],v[172:175],a[68:71]
buffer_load_dwordx4 v10,s[72:75],s80 offen offset:3072 lds
v_mfma_f32_16x16x32_bf16 a[68:71],a[96:99],v[164:167],a[68:71]
v_mfma_f32_16x16x32_bf16 a[72:75],a[96:99],v[196:199],a[72:75]
s_add_u32 m0,s82,126976
v_mfma_f32_16x16x32_bf16 a[72:75],a[100:103],v[204:207],a[72:75]
v_mfma_f32_16x16x32_bf16 a[72:75],a[104:107],v[212:215],a[72:75]
s_cmp_ge_u32 2,s71
s_cselect_b32 s76,7,5
s_lshl_b32 s76,s76,16
v_mfma_f32_16x16x32_bf16 a[72:75],a[108:111],v[220:223],a[72:75]
s_add_u32 s77,s76,4096
buffer_load_dwordx4 v11,s[72:75],s80 offen lds
v_mfma_f32_16x16x32_bf16 a[76:79],a[108:111],v[252:255],a[76:79]
s_add_u32 s78,s76,8192
s_add_u32 s79,s76,12288
s_max_i32 s76,s76,s18
v_mfma_f32_16x16x32_bf16 a[76:79],a[104:107],v[244:247],a[76:79]
s_add_u32 m0,s82,128000
s_max_i32 s77,s77,s19
ds_read_b64_tr_b16 a[104:105],v1 offset:20608
v_mfma_f32_16x16x32_bf16 a[76:79],a[100:103],v[236:239],a[76:79]
s_max_i32 s78,s78,s100
ds_read_b64_tr_b16 a[100:101],v1 offset:10368
ds_read_b64_tr_b16 a[102:103],v1 offset:15488
v_mfma_f32_16x16x32_bf16 a[76:79],a[96:99],v[228:231],a[76:79]
s_max_i32 s79,s79,s101
ds_read_b64_tr_b16 a[96:97],v1 offset:128
v_mfma_f32_16x16x32_bf16 a[80:83],a[112:115],v[132:135],a[80:83]
buffer_load_dwordx4 v7,s[72:75],s83 offen lds
v_mfma_f32_16x16x32_bf16 a[80:83],a[116:119],v[140:143],a[80:83]
ds_read_b64_tr_b16 a[98:99],v1 offset:5248
ds_read_b64_tr_b16 a[106:107],v1 offset:25728
v_mfma_f32_16x16x32_bf16 a[80:83],a[120:123],v[148:151],a[80:83]
ds_read_b64_tr_b16 a[108:109],v1 offset:30848
v_mfma_f32_16x16x32_bf16 a[80:83],a[124:127],v[156:159],a[80:83]
ds_read_b64_tr_b16 a[110:111],v1 offset:35968
v_mfma_f32_16x16x32_bf16 a[84:87],a[124:127],v[188:191],a[84:87]
v_mfma_f32_16x16x32_bf16 a[84:87],a[120:123],v[180:183],a[84:87]
buffer_load_dwordx4 v8,s[72:75],s83 offen offset:1024 lds
v_mfma_f32_16x16x32_bf16 a[84:87],a[116:119],v[172:175],a[84:87]
v_mfma_f32_16x16x32_bf16 a[84:87],a[112:115],v[164:167],a[84:87]
v_mfma_f32_16x16x32_bf16 a[88:91],a[112:115],v[196:199],a[88:91]
v_mfma_f32_16x16x32_bf16 a[88:91],a[116:119],v[204:207],a[88:91]
v_mfma_f32_16x16x32_bf16 a[88:91],a[120:123],v[212:215],a[88:91]
buffer_load_dwordx4 v9,s[72:75],s83 offen offset:2048 lds
v_mfma_f32_16x16x32_bf16 a[88:91],a[124:127],v[220:223],a[88:91]
v_mfma_f32_16x16x32_bf16 a[92:95],a[124:127],v[252:255],a[92:95]
ds_read_b64_tr_b16 a[124:125],v2 offset:30880
ds_read_b64_tr_b16 a[126:127],v2 offset:36000
v_mfma_f32_16x16x32_bf16 a[92:95],a[120:123],v[244:247],a[92:95]
ds_read_b64_tr_b16 a[120:121],v2 offset:20640
v_mfma_f32_16x16x32_bf16 a[92:95],a[116:119],v[236:239],a[92:95]
ds_read_b64_tr_b16 a[116:117],v2 offset:10400
v_mfma_f32_16x16x32_bf16 a[92:95],a[112:115],v[228:231],a[92:95]
s_waitcnt vmcnt(13)
buffer_load_dwordx4 v10,s[72:75],s83 offen offset:3072 lds
v_mfma_f32_16x16x32_bf16 a[192:195],a[128:131],v[128:131],a[192:195]
ds_read_b64_tr_b16 a[112:113],v2 offset:160
ds_read_b64_tr_b16 a[114:115],v2 offset:5280
v_mfma_f32_16x16x32_bf16 a[192:195],a[132:135],v[160:163],a[192:195]
s_add_u32 m0,s82,132096
ds_read_b64_tr_b16 a[118:119],v2 offset:15520
v_mfma_f32_16x16x32_bf16 a[192:195],a[136:139],v[192:195],a[192:195]
ds_read_b64_tr_b16 a[122:123],v2 offset:25760
v_mfma_f32_16x16x32_bf16 a[192:195],a[140:143],v[224:227],a[192:195]
buffer_load_dwordx4 v[132:135],v6,s[28:31],s76 offen sc1
v_mfma_f32_16x16x32_bf16 a[196:199],a[140:143],v[232:235],a[196:199]
v_mfma_f32_16x16x32_bf16 a[196:199],a[136:139],v[200:203],a[196:199]
)A"
R"A(v_mfma_f32_16x16x32_bf16 a[196:199],a[132:135],v[168:171],a[196:199]
v_mfma_f32_16x16x32_bf16 a[196:199],a[128:131],v[136:139],a[196:199]
buffer_load_dwordx4 v11,s[72:75],s83 offen lds
v_mfma_f32_16x16x32_bf16 a[200:203],a[128:131],v[144:147],a[200:203]
ds_read_b64_tr_b16 a[128:129],v1 offset:192
ds_read_b64_tr_b16 a[130:131],v1 offset:5312
v_mfma_f32_16x16x32_bf16 a[200:203],a[132:135],v[176:179],a[200:203]
ds_read_b64_tr_b16 a[132:133],v1 offset:10432
v_mfma_f32_16x16x32_bf16 a[200:203],a[136:139],v[208:211],a[200:203]
ds_read_b64_tr_b16 a[134:135],v1 offset:15552
v_mfma_f32_16x16x32_bf16 a[200:203],a[140:143],v[240:243],a[200:203]
buffer_load_dwordx4 v[140:143],v6,s[28:31],s76 offen offset:1024 sc1
v_mfma_f32_16x16x32_bf16 a[208:211],a[144:147],v[128:131],a[208:211]
ds_read_b64_tr_b16 a[136:137],v1 offset:20672
ds_read_b64_tr_b16 a[138:139],v1 offset:25792
v_mfma_f32_16x16x32_bf16 a[208:211],a[148:151],v[160:163],a[208:211]
ds_read_b64_tr_b16 a[140:141],v1 offset:30912
v_mfma_f32_16x16x32_bf16 a[208:211],a[152:155],v[192:195],a[208:211]
ds_read_b64_tr_b16 a[142:143],v1 offset:36032
v_mfma_f32_16x16x32_bf16 a[208:211],a[156:159],v[224:227],a[208:211]
buffer_load_dwordx4 v[148:151],v6,s[28:31],s76 offen offset:2048 sc1
v_mfma_f32_16x16x32_bf16 a[212:215],a[156:159],v[232:235],a[212:215]
v_mfma_f32_16x16x32_bf16 a[212:215],a[152:155],v[200:203],a[212:215]
v_mfma_f32_16x16x32_bf16 a[212:215],a[148:151],v[168:171],a[212:215]
v_mfma_f32_16x16x32_bf16 a[212:215],a[144:147],v[136:139],a[212:215]
buffer_load_dwordx4 v[156:159],v6,s[28:31],s76 offen offset:3072 sc1
v_mfma_f32_16x16x32_bf16 a[216:219],a[144:147],v[144:147],a[216:219]
s_mul_i32 s76,s70,12
ds_read_b64_tr_b16 a[144:145],v2 offset:224
ds_read_b64_tr_b16 a[146:147],v2 offset:5344
v_mfma_f32_16x16x32_bf16 a[216:219],a[148:151],v[176:179],a[216:219]
s_add_u32 s76,s76,0
s_add_u32 s76,s76,s40
ds_read_b64_tr_b16 a[148:149],v2 offset:10464
v_mfma_f32_16x16x32_bf16 a[216:219],a[152:155],v[208:211],a[216:219]
s_mul_i32 s76,s76,5120
s_max_i32 s76,s76,s101
ds_read_b64_tr_b16 a[150:151],v2 offset:15584
v_mfma_f32_16x16x32_bf16 a[216:219],a[156:159],v[240:243],a[216:219]
s_waitcnt lgkmcnt(4)
buffer_load_dwordx4 v[164:167],v6,s[28:31],s77 offen sc1
v_mfma_f32_16x16x32_bf16 a[224:227],a[160:163],v[128:131],a[224:227]
ds_read_b64_tr_b16 a[152:153],v2 offset:20704
ds_read_b64_tr_b16 a[154:155],v2 offset:25824
v_mfma_f32_16x16x32_bf16 a[224:227],a[164:167],v[160:163],a[224:227]
ds_read_b64_tr_b16 a[156:157],v2 offset:30944
v_mfma_f32_16x16x32_bf16 a[224:227],a[168:171],v[192:195],a[224:227]
ds_read_b64_tr_b16 a[158:159],v2 offset:36064
v_mfma_f32_16x16x32_bf16 a[224:227],a[172:175],v[224:227],a[224:227]
buffer_load_dwordx4 v[172:175],v6,s[28:31],s77 offen offset:1024 sc1
v_mfma_f32_16x16x32_bf16 a[228:231],a[172:175],v[232:235],a[228:231]
v_mfma_f32_16x16x32_bf16 a[228:231],a[168:171],v[200:203],a[228:231]
v_mfma_f32_16x16x32_bf16 a[228:231],a[164:167],v[168:171],a[228:231]
v_mfma_f32_16x16x32_bf16 a[228:231],a[160:163],v[136:139],a[228:231]
v_mfma_f32_16x16x32_bf16 a[232:235],a[160:163],v[144:147],a[232:235]
buffer_load_dwordx4 v[180:183],v6,s[28:31],s77 offen offset:2048 sc1
v_mfma_f32_16x16x32_bf16 a[232:235],a[164:167],v[176:179],a[232:235]
ds_read_b64_tr_b16 a[160:161],v1 offset:256
ds_read_b64_tr_b16 a[162:163],v1 offset:5376
v_mfma_f32_16x16x32_bf16 a[232:235],a[168:171],v[208:211],a[232:235]
ds_read_b64_tr_b16 a[164:165],v1 offset:10496
v_mfma_f32_16x16x32_bf16 a[232:235],a[172:175],v[240:243],a[232:235]
ds_read_b64_tr_b16 a[166:167],v1 offset:15616
v_mfma_f32_16x16x32_bf16 a[240:243],a[176:179],v[128:131],a[240:243]
ds_read_b64_tr_b16 a[168:169],v1 offset:20736
v_mfma_f32_16x16x32_bf16 a[240:243],a[180:183],v[160:163],a[240:243]
buffer_load_dwordx4 v[188:191],v6,s[28:31],s77 offen offset:3072 sc1
v_mfma_f32_16x16x32_bf16 a[240:243],a[184:187],v[192:195],a[240:243]
s_mul_i32 s77,s70,12
ds_read_b64_tr_b16 a[170:171],v1 offset:25856
ds_read_b64_tr_b16 a[172:173],v1 offset:30976
v_mfma_f32_16x16x32_bf16 a[240:243],a[188:191],v[224:227],a[240:243]
s_add_u32 s77,s77,4
s_add_u32 s77,s77,s40
ds_read_b64_tr_b16 a[174:175],v1 offset:36096
v_mfma_f32_16x16x32_bf16 a[244:247],a[188:191],v[232:235],a[244:247]
s_mul_i32 s77,s77,5120
s_max_i32 s77,s77,s101
v_mfma_f32_16x16x32_bf16 a[244:247],a[184:187],v[200:203],a[244:247]
v_mfma_f32_16x16x32_bf16 a[244:247],a[180:183],v[168:171],a[244:247]
buffer_load_dwordx4 v[196:199],v6,s[28:31],s78 offen sc1
v_mfma_f32_16x16x32_bf16 a[244:247],a[176:179],v[136:139],a[244:247]
v_mfma_f32_16x16x32_bf16 a[248:251],a[176:179],v[144:147],a[248:251]
ds_read_b64_tr_b16 a[176:177],v2 offset:288
ds_read_b64_tr_b16 a[178:179],v2 offset:5408
v_mfma_f32_16x16x32_bf16 a[248:251],a[180:183],v[176:179],a[248:251]
ds_read_b64_tr_b16 a[180:181],v2 offset:10528
v_mfma_f32_16x16x32_bf16 a[248:251],a[184:187],v[208:211],a[248:251]
ds_read_b64_tr_b16 a[182:183],v2 offset:15648
v_mfma_f32_16x16x32_bf16 a[248:251],a[188:191],v[240:243],a[248:251]
buffer_load_dwordx4 v[204:207],v6,s[28:31],s78 offen offset:1024 sc1
v_mfma_f32_16x16x32_bf16 a[0:3],a[96:99],v[128:131],a[0:3]
ds_read_b64_tr_b16 a[184:185],v2 offset:20768
ds_read_b64_tr_b16 a[186:187],v2 offset:25888
v_mfma_f32_16x16x32_bf16 a[0:3],a[100:103],v[160:163],a[0:3]
ds_read_b64_tr_b16 a[188:189],v2 offset:31008
v_mfma_f32_16x16x32_bf16 a[0:3],a[104:107],v[192:195],a[0:3]
ds_read_b64_tr_b16 a[190:191],v2 offset:36128
v_mfma_f32_16x16x32_bf16 a[0:3],a[108:111],v[224:227],a[0:3]
v_mfma_f32_16x16x32_bf16 a[4:7],a[108:111],v[232:235],a[4:7]
buffer_load_dwordx4 v[212:215],v6,s[28:31],s78 offen offset:2048 sc1
v_mfma_f32_16x16x32_bf16 a[4:7],a[104:107],v[200:203],a[4:7]
v_mfma_f32_16x16x32_bf16 a[4:7],a[100:103],v[168:171],a[4:7]
v_mfma_f32_16x16x32_bf16 a[4:7],a[96:99],v[136:139],a[4:7]
v_mfma_f32_16x16x32_bf16 a[8:11],a[96:99],v[144:147],a[8:11]
v_mfma_f32_16x16x32_bf16 a[8:11],a[100:103],v[176:179],a[8:11]
buffer_load_dwordx4 v[220:223],v6,s[28:31],s78 offen offset:3072 sc1
v_mfma_f32_16x16x32_bf16 a[8:11],a[104:107],v[208:211],a[8:11]
s_mul_i32 s78,s70,12
s_add_u32 s78,s78,8
s_add_u32 s78,s78,s40
v_mfma_f32_16x16x32_bf16 a[8:11],a[108:111],v[240:243],a[8:11]
s_mul_i32 s78,s78,5120
s_max_i32 s78,s78,s101
v_mfma_f32_16x16x32_bf16 a[16:19],a[112:115],v[128:131],a[16:19]
v_mfma_f32_16x16x32_bf16 a[16:19],a[116:119],v[160:163],a[16:19]
v_mfma_f32_16x16x32_bf16 a[16:19],a[120:123],v[192:195],a[16:19]
buffer_load_dwordx4 v[228:231],v6,s[28:31],s79 offen sc1
v_mfma_f32_16x16x32_bf16 a[16:19],a[124:127],v[224:227],a[16:19]
v_mfma_f32_16x16x32_bf16 a[20:23],a[124:127],v[232:235],a[20:23]
v_mfma_f32_16x16x32_bf16 a[20:23],a[120:123],v[200:203],a[20:23]
v_mfma_f32_16x16x32_bf16 a[20:23],a[116:119],v[168:171],a[20:23]
v_mfma_f32_16x16x32_bf16 a[20:23],a[112:115],v[136:139],a[20:23]
buffer_load_dwordx4 v[236:239],v6,s[28:31],s79 offen offset:1024 sc1
v_mfma_f32_16x16x32_bf16 a[24:27],a[112:115],v[144:147],a[24:27]
v_mfma_f32_16x16x32_bf16 a[24:27],a[116:119],v[176:179],a[24:27]
v_mfma_f32_16x16x32_bf16 a[24:27],a[120:123],v[208:211],a[24:27]
v_mfma_f32_16x16x32_bf16 a[24:27],a[124:127],v[240:243],a[24:27]
v_mfma_f32_16x16x32_bf16 a[32:35],a[128:131],v[128:131],a[32:35]
buffer_load_dwordx4 v[244:247],v6,s[28:31],s79 offen offset:2048 sc1
v_mfma_f32_16x16x32_bf16 a[32:35],a[132:135],v[160:163],a[32:35]
v_mfma_f32_16x16x32_bf16 a[32:35],a[136:139],v[192:195],a[32:35]
v_mfma_f32_16x16x32_bf16 a[32:35],a[140:143],v[224:227],a[32:35]
v_mfma_f32_16x16x32_bf16 a[36:39],a[140:143],v[232:235],a[36:39]
v_mfma_f32_16x16x32_bf16 a[36:39],a[136:139],v[200:203],a[36:39]
buffer_load_dwordx4 v[252:255],v6,s[28:31],s79 offen offset:3072 sc1
v_mfma_f32_16x16x32_bf16 a[36:39],a[132:135],v[168:171],a[36:39]
v_mfma_f32_16x16x32_bf16 a[36:39],a[128:131],v[136:139],a[36:39]
v_mfma_f32_16x16x32_bf16 a[40:43],a[128:131],v[144:147],a[40:43]
v_mfma_f32_16x16x32_bf16 a[40:43],a[132:135],v[176:179],a[40:43]
v_mfma_f32_16x16x32_bf16 a[40:43],a[136:139],v[208:211],a[40:43]
s_waitcnt vmcnt(15)
v_mfma_f32_16x16x32_bf16 a[40:43],a[140:143],v[240:243],a[40:43]
s_barrier
ds_read_b64_tr_b16 a[96:97],v42
ds_read_b64_tr_b16 a[98:99],v42 offset:5120
s_waitcnt lgkmcnt(2)
v_mfma_f32_16x16x32_bf16 a[48:51],a[144:147],v[128:131],a[48:51]
ds_read_b64_tr_b16 a[100:101],v42 offset:10240
v_mfma_f32_16x16x32_bf16 a[48:51],a[148:151],v[160:163],a[48:51]
ds_read_b64_tr_b16 a[102:103],v42 offset:15360
v_mfma_f32_16x16x32_bf16 a[48:51],a[152:155],v[192:195],a[48:51]
ds_read_b64_tr_b16 a[104:105],v42 offset:20480
v_mfma_f32_16x16x32_bf16 a[48:51],a[156:159],v[224:227],a[48:51]
ds_read_b64_tr_b16 a[106:107],v42 offset:25600
ds_read_b64_tr_b16 a[108:109],v42 offset:30720
v_mfma_f32_16x16x32_bf16 a[52:55],a[156:159],v[232:235],a[52:55]
ds_read_b64_tr_b16 a[110:111],v42 offset:35840
v_mfma_f32_16x16x32_bf16 a[52:55],a[152:155],v[200:203],a[52:55]
ds_read_b64_tr_b16 a[112:113],v44
v_mfma_f32_16x16x32_bf16 a[52:55],a[148:151],v[168:171],a[52:55]
ds_read_b64_tr_b16 a[114:115],v44 offset:5120
v_mfma_f32_16x16x32_bf16 a[52:55],a[144:147],v[136:139],a[52:55]
ds_read_b64_tr_b16 a[116:117],v44 offset:10240
ds_read_b64_tr_b16 a[118:119],v44 offset:15360
v_mfma_f32_16x16x32_bf16 a[56:59],a[144:147],v[144:147],a[56:59]
ds_read_b64_tr_b16 a[120:121],v44 offset:20480
v_mfma_f32_16x16x32_bf16 a[56:59],a[148:151],v[176:179],a[56:59]
ds_read_b64_tr_b16 a[122:123],v44 offset:25600
v_mfma_f32_16x16x32_bf16 a[56:59],a[152:155],v[208:211],a[56:59]
ds_read_b64_tr_b16 a[124:125],v44 offset:30720
v_mfma_f32_16x16x32_bf16 a[56:59],a[156:159],v[240:243],a[56:59]
ds_read_b64_tr_b16 a[126:127],v44 offset:35840
ds_read_b64_tr_b16 a[128:129],v43 offset:32
v_mfma_f32_16x16x32_bf16 a[64:67],a[160:163],v[128:131],a[64:67]
ds_read_b64_tr_b16 a[130:131],v43 offset:5152
v_mfma_f32_16x16x32_bf16 a[64:67],a[164:167],v[160:163],a[64:67]
ds_read_b64_tr_b16 a[132:133],v43 offset:10272
v_mfma_f32_16x16x32_bf16 a[64:67],a[168:171],v[192:195],a[64:67]
ds_read_b64_tr_b16 a[134:135],v43 offset:15392
v_mfma_f32_16x16x32_bf16 a[64:67],a[172:175],v[224:227],a[64:67]
ds_read_b64_tr_b16 a[136:137],v43 offset:20512
ds_read_b64_tr_b16 a[138:139],v43 offset:25632
v_mfma_f32_16x16x32_bf16 a[68:71],a[172:175],v[232:235],a[68:71]
ds_read_b64_tr_b16 a[140:141],v43 offset:30752
v_mfma_f32_16x16x32_bf16 a[68:71],a[168:171],v[200:203],a[68:71]
ds_read_b64_tr_b16 a[142:143],v43 offset:35872
v_mfma_f32_16x16x32_bf16 a[68:71],a[164:167],v[168:171],a[68:71]
ds_read_b64_tr_b16 a[144:145],v45 offset:32
v_mfma_f32_16x16x32_bf16 a[68:71],a[160:163],v[136:139],a[68:71]
ds_read_b64_tr_b16 a[146:147],v45 offset:5152
ds_read_b64_tr_b16 a[148:149],v45 offset:10272
v_mfma_f32_16x16x32_bf16 a[72:75],a[160:163],v[144:147],a[72:75]
ds_read_b64_tr_b16 a[150:151],v45 offset:15392
v_mfma_f32_16x16x32_bf16 a[72:75],a[164:167],v[176:179],a[72:75]
ds_read_b64_tr_b16 a[152:153],v45 offset:20512
v_mfma_f32_16x16x32_bf16 a[72:75],a[168:171],v[208:211],a[72:75]
ds_read_b64_tr_b16 a[154:155],v45 offset:25632
v_mfma_f32_16x16x32_bf16 a[72:75],a[172:175],v[240:243],a[72:75]
ds_read_b64_tr_b16 a[156:157],v45 offset:30752
ds_read_b64_tr_b16 a[158:159],v45 offset:35872
v_mfma_f32_16x16x32_bf16 a[80:83],a[176:179],v[128:131],a[80:83]
ds_read_b64_tr_b16 a[160:161],v42 offset:64
v_mfma_f32_16x16x32_bf16 a[80:83],a[180:183],v[160:163],a[80:83]
ds_read_b64_tr_b16 a[162:163],v42 offset:5184
v_mfma_f32_16x16x32_bf16 a[80:83],a[184:187],v[192:195],a[80:83]
ds_read_b64_tr_b16 a[164:165],v42 offset:10304
v_mfma_f32_16x16x32_bf16 a[80:83],a[188:191],v[224:227],a[80:83]
ds_read_b64_tr_b16 a[166:167],v42 offset:15424
ds_read_b64_tr_b16 a[168:169],v42 offset:20544
v_mfma_f32_16x16x32_bf16 a[84:87],a[188:191],v[232:235],a[84:87]
ds_read_b64_tr_b16 a[170:171],v42 offset:25664
v_mfma_f32_16x16x32_bf16 a[84:87],a[184:187],v[200:203],a[84:87]
ds_read_b64_tr_b16 a[172:173],v42 offset:30784
v_mfma_f32_16x16x32_bf16 a[84:87],a[180:183],v[168:171],a[84:87]
ds_read_b64_tr_b16 a[174:175],v42 offset:35904
v_mfma_f32_16x16x32_bf16 a[84:87],a[176:179],v[136:139],a[84:87]
)A"
R"A(v_mfma_f32_16x16x32_bf16 a[88:91],a[176:179],v[144:147],a[88:91]
ds_read_b64_tr_b16 a[176:177],v44 offset:64
ds_read_b64_tr_b16 a[178:179],v44 offset:5184
v_mfma_f32_16x16x32_bf16 a[88:91],a[180:183],v[176:179],a[88:91]
ds_read_b64_tr_b16 a[180:181],v44 offset:10304
v_mfma_f32_16x16x32_bf16 a[88:91],a[184:187],v[208:211],a[88:91]
ds_read_b64_tr_b16 a[182:183],v44 offset:15424
v_mfma_f32_16x16x32_bf16 a[88:91],a[188:191],v[240:243],a[88:91]
s_waitcnt lgkmcnt(7)
ds_read_b64_tr_b16 a[184:185],v44 offset:20544
v_mfma_f32_16x16x32_bf16 a[192:195],a[96:99],v[64:67],a[192:195]
ds_read_b64_tr_b16 a[186:187],v44 offset:25664
ds_read_b64_tr_b16 a[188:189],v44 offset:30784
v_mfma_f32_16x16x32_bf16 a[192:195],a[100:103],v[68:71],a[192:195]
ds_read_b64_tr_b16 a[190:191],v44 offset:35904
v_mfma_f32_16x16x32_bf16 a[192:195],a[104:107],v[72:75],a[192:195]
v_mfma_f32_16x16x32_bf16 a[192:195],a[108:111],v[76:79],a[192:195]
v_mfma_f32_16x16x32_bf16 a[196:199],a[108:111],v[92:95],a[196:199]
v_mfma_f32_16x16x32_bf16 a[196:199],a[104:107],v[88:91],a[196:199]
v_mfma_f32_16x16x32_bf16 a[196:199],a[100:103],v[84:87],a[196:199]
v_mfma_f32_16x16x32_bf16 a[196:199],a[96:99],v[80:83],a[196:199]
v_mfma_f32_16x16x32_bf16 a[200:203],a[96:99],v[96:99],a[200:203]
ds_read_b64_tr_b16 a[96:97],v43 offset:96
ds_read_b64_tr_b16 a[98:99],v43 offset:5216
v_mfma_f32_16x16x32_bf16 a[200:203],a[100:103],v[100:103],a[200:203]
ds_read_b64_tr_b16 a[100:101],v43 offset:10336
v_mfma_f32_16x16x32_bf16 a[200:203],a[104:107],v[104:107],a[200:203]
ds_read_b64_tr_b16 a[102:103],v43 offset:15456
v_mfma_f32_16x16x32_bf16 a[200:203],a[108:111],v[108:111],a[200:203]
ds_read_b64_tr_b16 a[104:105],v43 offset:20576
v_mfma_f32_16x16x32_bf16 a[192:195],a[112:115],v[132:135],a[192:195]
s_waitcnt vmcnt(3)
ds_read_b64_tr_b16 a[106:107],v43 offset:25696
ds_read_b64_tr_b16 a[108:109],v43 offset:30816
v_mfma_f32_16x16x32_bf16 a[192:195],a[116:119],v[140:143],a[192:195]
ds_read_b64_tr_b16 a[110:111],v43 offset:35936
v_mfma_f32_16x16x32_bf16 a[192:195],a[120:123],v[148:151],a[192:195]
v_mfma_f32_16x16x32_bf16 a[192:195],a[124:127],v[156:159],a[192:195]
v_mfma_f32_16x16x32_bf16 a[196:199],a[124:127],v[188:191],a[196:199]
v_mfma_f32_16x16x32_bf16 a[196:199],a[120:123],v[180:183],a[196:199]
v_mfma_f32_16x16x32_bf16 a[196:199],a[116:119],v[172:175],a[196:199]
v_mfma_f32_16x16x32_bf16 a[196:199],a[112:115],v[164:167],a[196:199]
v_mfma_f32_16x16x32_bf16 a[200:203],a[112:115],v[196:199],a[200:203]
ds_read_b64_tr_b16 a[112:113],v45 offset:96
ds_read_b64_tr_b16 a[114:115],v45 offset:5216
v_mfma_f32_16x16x32_bf16 a[200:203],a[116:119],v[204:207],a[200:203]
v_accvgpr_read_b32 v128,a192
v_accvgpr_read_b32 v129,a193
v_mfma_f32_16x16x32_bf16 a[200:203],a[120:123],v[212:215],a[200:203]
v_cvt_pk_bf16_f32 v128,v128,v129
v_accvgpr_read_b32 v130,a194
v_mfma_f32_16x16x32_bf16 a[200:203],a[124:127],v[220:223],a[200:203]
v_accvgpr_read_b32 v131,a195
v_cvt_pk_bf16_f32 v129,v130,v131
v_mfma_f32_16x16x32_bf16 a[208:211],a[128:131],v[64:67],a[208:211]
ds_read_b64_tr_b16 a[116:117],v45 offset:10336
ds_read_b64_tr_b16 a[118:119],v45 offset:15456
v_mfma_f32_16x16x32_bf16 a[208:211],a[132:135],v[68:71],a[208:211]
v_accvgpr_read_b32 v136,a196
v_accvgpr_read_b32 v137,a197
v_mfma_f32_16x16x32_bf16 a[208:211],a[136:139],v[72:75],a[208:211]
v_cvt_pk_bf16_f32 v136,v136,v137
v_accvgpr_read_b32 v138,a198
v_mfma_f32_16x16x32_bf16 a[208:211],a[140:143],v[76:79],a[208:211]
v_accvgpr_read_b32 v139,a199
v_cvt_pk_bf16_f32 v137,v138,v139
v_mfma_f32_16x16x32_bf16 a[212:215],a[140:143],v[92:95],a[212:215]
ds_read_b64_tr_b16 a[120:121],v45 offset:20576
ds_read_b64_tr_b16 a[122:123],v45 offset:25696
v_mfma_f32_16x16x32_bf16 a[212:215],a[136:139],v[88:91],a[212:215]
ds_read_b64_tr_b16 a[124:125],v45 offset:30816
v_accvgpr_read_b32 v144,a200
v_mfma_f32_16x16x32_bf16 a[212:215],a[132:135],v[84:87],a[212:215]
ds_read_b64_tr_b16 a[126:127],v45 offset:35936
v_accvgpr_read_b32 v145,a201
v_mfma_f32_16x16x32_bf16 a[212:215],a[128:131],v[80:83],a[212:215]
v_cvt_pk_bf16_f32 v144,v144,v145
v_accvgpr_read_b32 v146,a202
v_mfma_f32_16x16x32_bf16 a[216:219],a[128:131],v[96:99],a[216:219]
v_accvgpr_read_b32 v147,a203
v_cvt_pk_bf16_f32 v145,v146,v147
v_mfma_f32_16x16x32_bf16 a[216:219],a[132:135],v[100:103],a[216:219]
ds_read_b64_tr_b16 a[128:129],v42 offset:128
ds_read_b64_tr_b16 a[130:131],v42 offset:5248
v_mfma_f32_16x16x32_bf16 a[216:219],a[136:139],v[104:107],a[216:219]
ds_read_b64_tr_b16 a[132:133],v42 offset:10368
v_mfma_f32_16x16x32_bf16 a[216:219],a[140:143],v[108:111],a[216:219]
ds_read_b64_tr_b16 a[134:135],v42 offset:15488
v_mfma_f32_16x16x32_bf16 a[208:211],a[144:147],v[132:135],a[208:211]
ds_read_b64_tr_b16 a[136:137],v42 offset:20608
v_mfma_f32_16x16x32_bf16 a[208:211],a[148:151],v[140:143],a[208:211]
ds_read_b64_tr_b16 a[138:139],v42 offset:25728
ds_read_b64_tr_b16 a[140:141],v42 offset:30848
v_mfma_f32_16x16x32_bf16 a[208:211],a[152:155],v[148:151],a[208:211]
ds_read_b64_tr_b16 a[142:143],v42 offset:35968
v_mfma_f32_16x16x32_bf16 a[208:211],a[156:159],v[156:159],a[208:211]
v_mfma_f32_16x16x32_bf16 a[212:215],a[156:159],v[188:191],a[212:215]
v_mfma_f32_16x16x32_bf16 a[212:215],a[152:155],v[180:183],a[212:215]
v_mfma_f32_16x16x32_bf16 a[212:215],a[148:151],v[172:175],a[212:215]
v_mfma_f32_16x16x32_bf16 a[212:215],a[144:147],v[164:167],a[212:215]
v_mfma_f32_16x16x32_bf16 a[216:219],a[144:147],v[196:199],a[216:219]
ds_read_b64_tr_b16 a[144:145],v44 offset:128
ds_read_b64_tr_b16 a[146:147],v44 offset:5248
v_mfma_f32_16x16x32_bf16 a[216:219],a[148:151],v[204:207],a[216:219]
v_accvgpr_read_b32 v130,a208
v_accvgpr_read_b32 v131,a209
v_mfma_f32_16x16x32_bf16 a[216:219],a[152:155],v[212:215],a[216:219]
v_accvgpr_read_b32 v12,a210
v_accvgpr_read_b32 v13,a211
v_mfma_f32_16x16x32_bf16 a[216:219],a[156:159],v[220:223],a[216:219]
v_cvt_pk_bf16_f32 v130,v130,v131
v_cvt_pk_bf16_f32 v131,v12,v13
v_mfma_f32_16x16x32_bf16 a[224:227],a[160:163],v[64:67],a[224:227]
buffer_store_dwordx4 v[128:131],v6,s[32:35],s84 offen sc1
v_mfma_f32_16x16x32_bf16 a[224:227],a[164:167],v[68:71],a[224:227]
s_waitcnt lgkmcnt(2)
v_accvgpr_read_b32 v138,a212
v_accvgpr_read_b32 v139,a213
v_mfma_f32_16x16x32_bf16 a[224:227],a[168:171],v[72:75],a[224:227]
v_accvgpr_read_b32 v14,a214
v_accvgpr_read_b32 v15,a215
v_mfma_f32_16x16x32_bf16 a[224:227],a[172:175],v[76:79],a[224:227]
v_cvt_pk_bf16_f32 v138,v138,v139
v_cvt_pk_bf16_f32 v139,v14,v15
v_mfma_f32_16x16x32_bf16 a[228:231],a[172:175],v[92:95],a[228:231]
buffer_store_dwordx4 v[136:139],v6,s[32:35],s85 offen sc1
v_mfma_f32_16x16x32_bf16 a[228:231],a[168:171],v[88:91],a[228:231]
v_accvgpr_read_b32 v146,a216
v_accvgpr_read_b32 v147,a217
v_mfma_f32_16x16x32_bf16 a[228:231],a[164:167],v[84:87],a[228:231]
v_accvgpr_read_b32 v16,a218
v_accvgpr_read_b32 v17,a219
v_mfma_f32_16x16x32_bf16 a[228:231],a[160:163],v[80:83],a[228:231]
v_cvt_pk_bf16_f32 v146,v146,v147
v_cvt_pk_bf16_f32 v147,v16,v17
v_mfma_f32_16x16x32_bf16 a[232:235],a[160:163],v[96:99],a[232:235]
buffer_store_dwordx4 v[144:147],v6,s[32:35],s86 offen sc1
v_mfma_f32_16x16x32_bf16 a[232:235],a[164:167],v[100:103],a[232:235]
ds_read_b64_tr_b16 a[148:149],v44 offset:10368
ds_read_b64_tr_b16 a[150:151],v44 offset:15488
v_mfma_f32_16x16x32_bf16 a[232:235],a[168:171],v[104:107],a[232:235]
ds_read_b64_tr_b16 a[152:153],v44 offset:20608
v_mfma_f32_16x16x32_bf16 a[232:235],a[172:175],v[108:111],a[232:235]
ds_read_b64_tr_b16 a[154:155],v44 offset:25728
v_mfma_f32_16x16x32_bf16 a[224:227],a[176:179],v[132:135],a[224:227]
ds_read_b64_tr_b16 a[156:157],v44 offset:30848
v_mfma_f32_16x16x32_bf16 a[224:227],a[180:183],v[140:143],a[224:227]
ds_read_b64_tr_b16 a[158:159],v44 offset:35968
ds_read_b64_tr_b16 a[160:161],v43 offset:160
v_mfma_f32_16x16x32_bf16 a[224:227],a[184:187],v[148:151],a[224:227]
ds_read_b64_tr_b16 a[162:163],v43 offset:5280
v_mfma_f32_16x16x32_bf16 a[224:227],a[188:191],v[156:159],a[224:227]
ds_read_b64_tr_b16 a[164:165],v43 offset:10400
v_mfma_f32_16x16x32_bf16 a[228:231],a[188:191],v[188:191],a[228:231]
ds_read_b64_tr_b16 a[166:167],v43 offset:15520
v_mfma_f32_16x16x32_bf16 a[228:231],a[184:187],v[180:183],a[228:231]
ds_read_b64_tr_b16 a[168:169],v43 offset:20640
ds_read_b64_tr_b16 a[170:171],v43 offset:25760
v_mfma_f32_16x16x32_bf16 a[228:231],a[180:183],v[172:175],a[228:231]
ds_read_b64_tr_b16 a[172:173],v43 offset:30880
v_mfma_f32_16x16x32_bf16 a[228:231],a[176:179],v[164:167],a[228:231]
ds_read_b64_tr_b16 a[174:175],v43 offset:36000
v_mfma_f32_16x16x32_bf16 a[232:235],a[176:179],v[196:199],a[232:235]
ds_read_b64_tr_b16 a[176:177],v45 offset:160
v_mfma_f32_16x16x32_bf16 a[232:235],a[180:183],v[204:207],a[232:235]
v_accvgpr_read_b32 v160,a224
v_accvgpr_read_b32 v161,a225
v_mfma_f32_16x16x32_bf16 a[232:235],a[184:187],v[212:215],a[232:235]
v_cvt_pk_bf16_f32 v160,v160,v161
v_accvgpr_read_b32 v162,a226
v_mfma_f32_16x16x32_bf16 a[232:235],a[188:191],v[220:223],a[232:235]
v_accvgpr_read_b32 v163,a227
v_cvt_pk_bf16_f32 v161,v162,v163
v_mfma_f32_16x16x32_bf16 a[240:243],a[96:99],v[64:67],a[240:243]
ds_read_b64_tr_b16 a[178:179],v45 offset:5280
ds_read_b64_tr_b16 a[180:181],v45 offset:10400
v_mfma_f32_16x16x32_bf16 a[240:243],a[100:103],v[68:71],a[240:243]
v_accvgpr_read_b32 v168,a228
v_accvgpr_read_b32 v169,a229
v_mfma_f32_16x16x32_bf16 a[240:243],a[104:107],v[72:75],a[240:243]
v_cvt_pk_bf16_f32 v168,v168,v169
v_accvgpr_read_b32 v170,a230
v_mfma_f32_16x16x32_bf16 a[240:243],a[108:111],v[76:79],a[240:243]
v_accvgpr_read_b32 v171,a231
v_cvt_pk_bf16_f32 v169,v170,v171
v_mfma_f32_16x16x32_bf16 a[244:247],a[108:111],v[92:95],a[244:247]
ds_read_b64_tr_b16 a[182:183],v45 offset:15520
ds_read_b64_tr_b16 a[184:185],v45 offset:20640
v_mfma_f32_16x16x32_bf16 a[244:247],a[104:107],v[88:91],a[244:247]
ds_read_b64_tr_b16 a[186:187],v45 offset:25760
v_accvgpr_read_b32 v176,a232
v_mfma_f32_16x16x32_bf16 a[244:247],a[100:103],v[84:87],a[244:247]
ds_read_b64_tr_b16 a[188:189],v45 offset:30880
v_accvgpr_read_b32 v177,a233
v_mfma_f32_16x16x32_bf16 a[244:247],a[96:99],v[80:83],a[244:247]
ds_read_b64_tr_b16 a[190:191],v45 offset:36000
v_cvt_pk_bf16_f32 v176,v176,v177
v_mfma_f32_16x16x32_bf16 a[248:251],a[96:99],v[96:99],a[248:251]
v_accvgpr_read_b32 v178,a234
v_accvgpr_read_b32 v179,a235
v_mfma_f32_16x16x32_bf16 a[248:251],a[100:103],v[100:103],a[248:251]
ds_read_b64_tr_b16 a[96:97],v42 offset:192
v_cvt_pk_bf16_f32 v177,v178,v179
v_mfma_f32_16x16x32_bf16 a[248:251],a[104:107],v[104:107],a[248:251]
ds_read_b64_tr_b16 a[98:99],v42 offset:5312
ds_read_b64_tr_b16 a[100:101],v42 offset:10432
v_mfma_f32_16x16x32_bf16 a[248:251],a[108:111],v[108:111],a[248:251]
ds_read_b64_tr_b16 a[102:103],v42 offset:15552
v_mfma_f32_16x16x32_bf16 a[240:243],a[112:115],v[132:135],a[240:243]
ds_read_b64_tr_b16 a[104:105],v42 offset:20672
v_mfma_f32_16x16x32_bf16 a[240:243],a[116:119],v[140:143],a[240:243]
ds_read_b64_tr_b16 a[106:107],v42 offset:25792
v_mfma_f32_16x16x32_bf16 a[240:243],a[120:123],v[148:151],a[240:243]
ds_read_b64_tr_b16 a[108:109],v42 offset:30912
ds_read_b64_tr_b16 a[110:111],v42 offset:36032
v_mfma_f32_16x16x32_bf16 a[240:243],a[124:127],v[156:159],a[240:243]
v_mfma_f32_16x16x32_bf16 a[244:247],a[124:127],v[188:191],a[244:247]
v_mfma_f32_16x16x32_bf16 a[244:247],a[120:123],v[180:183],a[244:247]
v_mfma_f32_16x16x32_bf16 a[244:247],a[116:119],v[172:175],a[244:247]
v_mfma_f32_16x16x32_bf16 a[244:247],a[112:115],v[164:167],a[244:247]
v_mfma_f32_16x16x32_bf16 a[248:251],a[112:115],v[196:199],a[248:251]
ds_read_b64_tr_b16 a[112:113],v44 offset:192
ds_read_b64_tr_b16 a[114:115],v44 offset:5312
v_mfma_f32_16x16x32_bf16 a[248:251],a[116:119],v[204:207],a[248:251]
v_accvgpr_read_b32 v162,a240
v_accvgpr_read_b32 v163,a241
v_mfma_f32_16x16x32_bf16 a[248:251],a[120:123],v[212:215],a[248:251]
v_accvgpr_read_b32 v12,a242
v_accvgpr_read_b32 v13,a243
v_mfma_f32_16x16x32_bf16 a[248:251],a[124:127],v[220:223],a[248:251]
v_cvt_pk_bf16_f32 v162,v162,v163
v_cvt_pk_bf16_f32 v163,v12,v13
v_mfma_f32_16x16x32_bf16 a[0:3],a[128:131],v[64:67],a[0:3]
buffer_store_dwordx4 v[160:163],v6,s[32:35],s84 offen offset:1024 sc1
)A"
R"A(v_mfma_f32_16x16x32_bf16 a[0:3],a[132:135],v[68:71],a[0:3]
v_accvgpr_read_b32 v170,a244
v_accvgpr_read_b32 v171,a245
v_mfma_f32_16x16x32_bf16 a[0:3],a[136:139],v[72:75],a[0:3]
v_accvgpr_read_b32 v14,a246
v_accvgpr_read_b32 v15,a247
v_mfma_f32_16x16x32_bf16 a[0:3],a[140:143],v[76:79],a[0:3]
v_cvt_pk_bf16_f32 v170,v170,v171
v_cvt_pk_bf16_f32 v171,v14,v15
v_mfma_f32_16x16x32_bf16 a[4:7],a[140:143],v[92:95],a[4:7]
buffer_store_dwordx4 v[168:171],v6,s[32:35],s85 offen offset:1024 sc1
v_mfma_f32_16x16x32_bf16 a[4:7],a[136:139],v[88:91],a[4:7]
v_accvgpr_read_b32 v178,a248
v_accvgpr_read_b32 v179,a249
v_mfma_f32_16x16x32_bf16 a[4:7],a[132:135],v[84:87],a[4:7]
v_accvgpr_read_b32 v16,a250
v_accvgpr_read_b32 v17,a251
v_mfma_f32_16x16x32_bf16 a[4:7],a[128:131],v[80:83],a[4:7]
v_cvt_pk_bf16_f32 v178,v178,v179
v_cvt_pk_bf16_f32 v179,v16,v17
v_mfma_f32_16x16x32_bf16 a[8:11],a[128:131],v[96:99],a[8:11]
buffer_store_dwordx4 v[176:179],v6,s[32:35],s86 offen offset:1024 sc1
v_mfma_f32_16x16x32_bf16 a[8:11],a[132:135],v[100:103],a[8:11]
ds_read_b64_tr_b16 a[116:117],v44 offset:10432
ds_read_b64_tr_b16 a[118:119],v44 offset:15552
v_mfma_f32_16x16x32_bf16 a[8:11],a[136:139],v[104:107],a[8:11]
ds_read_b64_tr_b16 a[120:121],v44 offset:20672
v_mfma_f32_16x16x32_bf16 a[8:11],a[140:143],v[108:111],a[8:11]
s_waitcnt lgkmcnt(3)
ds_read_b64_tr_b16 a[122:123],v44 offset:25792
v_mfma_f32_16x16x32_bf16 a[0:3],a[144:147],v[132:135],a[0:3]
ds_read_b64_tr_b16 a[124:125],v44 offset:30912
v_mfma_f32_16x16x32_bf16 a[0:3],a[148:151],v[140:143],a[0:3]
ds_read_b64_tr_b16 a[126:127],v44 offset:36032
ds_read_b64_tr_b16 a[128:129],v43 offset:224
v_mfma_f32_16x16x32_bf16 a[0:3],a[152:155],v[148:151],a[0:3]
ds_read_b64_tr_b16 a[130:131],v43 offset:5344
v_mfma_f32_16x16x32_bf16 a[0:3],a[156:159],v[156:159],a[0:3]
ds_read_b64_tr_b16 a[132:133],v43 offset:10464
v_mfma_f32_16x16x32_bf16 a[4:7],a[156:159],v[188:191],a[4:7]
ds_read_b64_tr_b16 a[134:135],v43 offset:15584
v_mfma_f32_16x16x32_bf16 a[4:7],a[152:155],v[180:183],a[4:7]
ds_read_b64_tr_b16 a[136:137],v43 offset:20704
ds_read_b64_tr_b16 a[138:139],v43 offset:25824
v_mfma_f32_16x16x32_bf16 a[4:7],a[148:151],v[172:175],a[4:7]
ds_read_b64_tr_b16 a[140:141],v43 offset:30944
v_mfma_f32_16x16x32_bf16 a[4:7],a[144:147],v[164:167],a[4:7]
ds_read_b64_tr_b16 a[142:143],v43 offset:36064
v_mfma_f32_16x16x32_bf16 a[8:11],a[144:147],v[196:199],a[8:11]
ds_read_b64_tr_b16 a[144:145],v45 offset:224
v_mfma_f32_16x16x32_bf16 a[8:11],a[148:151],v[204:207],a[8:11]
v_accvgpr_read_b32 v192,a0
v_accvgpr_read_b32 v193,a1
v_mfma_f32_16x16x32_bf16 a[8:11],a[152:155],v[212:215],a[8:11]
v_cvt_pk_bf16_f32 v192,v192,v193
v_accvgpr_read_b32 v194,a2
v_mfma_f32_16x16x32_bf16 a[8:11],a[156:159],v[220:223],a[8:11]
v_accvgpr_read_b32 v195,a3
v_cvt_pk_bf16_f32 v193,v194,v195
v_mfma_f32_16x16x32_bf16 a[16:19],a[160:163],v[64:67],a[16:19]
ds_read_b64_tr_b16 a[146:147],v45 offset:5344
ds_read_b64_tr_b16 a[148:149],v45 offset:10464
v_mfma_f32_16x16x32_bf16 a[16:19],a[164:167],v[68:71],a[16:19]
v_accvgpr_read_b32 v200,a4
v_accvgpr_read_b32 v201,a5
v_mfma_f32_16x16x32_bf16 a[16:19],a[168:171],v[72:75],a[16:19]
v_cvt_pk_bf16_f32 v200,v200,v201
v_accvgpr_read_b32 v202,a6
v_mfma_f32_16x16x32_bf16 a[16:19],a[172:175],v[76:79],a[16:19]
v_accvgpr_read_b32 v203,a7
v_cvt_pk_bf16_f32 v201,v202,v203
v_mfma_f32_16x16x32_bf16 a[20:23],a[172:175],v[92:95],a[20:23]
ds_read_b64_tr_b16 a[150:151],v45 offset:15584
ds_read_b64_tr_b16 a[152:153],v45 offset:20704
v_mfma_f32_16x16x32_bf16 a[20:23],a[168:171],v[88:91],a[20:23]
ds_read_b64_tr_b16 a[154:155],v45 offset:25824
v_accvgpr_read_b32 v208,a8
v_mfma_f32_16x16x32_bf16 a[20:23],a[164:167],v[84:87],a[20:23]
ds_read_b64_tr_b16 a[156:157],v45 offset:30944
v_accvgpr_read_b32 v209,a9
v_mfma_f32_16x16x32_bf16 a[20:23],a[160:163],v[80:83],a[20:23]
ds_read_b64_tr_b16 a[158:159],v45 offset:36064
v_cvt_pk_bf16_f32 v208,v208,v209
v_mfma_f32_16x16x32_bf16 a[24:27],a[160:163],v[96:99],a[24:27]
v_accvgpr_read_b32 v210,a10
v_accvgpr_read_b32 v211,a11
v_mfma_f32_16x16x32_bf16 a[24:27],a[164:167],v[100:103],a[24:27]
ds_read_b64_tr_b16 a[160:161],v42 offset:256
v_cvt_pk_bf16_f32 v209,v210,v211
v_mfma_f32_16x16x32_bf16 a[24:27],a[168:171],v[104:107],a[24:27]
ds_read_b64_tr_b16 a[162:163],v42 offset:5376
ds_read_b64_tr_b16 a[164:165],v42 offset:10496
v_mfma_f32_16x16x32_bf16 a[24:27],a[172:175],v[108:111],a[24:27]
ds_read_b64_tr_b16 a[166:167],v42 offset:15616
v_mfma_f32_16x16x32_bf16 a[16:19],a[176:179],v[132:135],a[16:19]
ds_read_b64_tr_b16 a[168:169],v42 offset:20736
v_mfma_f32_16x16x32_bf16 a[16:19],a[180:183],v[140:143],a[16:19]
ds_read_b64_tr_b16 a[170:171],v42 offset:25856
v_mfma_f32_16x16x32_bf16 a[16:19],a[184:187],v[148:151],a[16:19]
ds_read_b64_tr_b16 a[172:173],v42 offset:30976
ds_read_b64_tr_b16 a[174:175],v42 offset:36096
v_mfma_f32_16x16x32_bf16 a[16:19],a[188:191],v[156:159],a[16:19]
v_mfma_f32_16x16x32_bf16 a[20:23],a[188:191],v[188:191],a[20:23]
v_mfma_f32_16x16x32_bf16 a[20:23],a[184:187],v[180:183],a[20:23]
v_mfma_f32_16x16x32_bf16 a[20:23],a[180:183],v[172:175],a[20:23]
v_mfma_f32_16x16x32_bf16 a[20:23],a[176:179],v[164:167],a[20:23]
v_mfma_f32_16x16x32_bf16 a[24:27],a[176:179],v[196:199],a[24:27]
ds_read_b64_tr_b16 a[176:177],v44 offset:256
ds_read_b64_tr_b16 a[178:179],v44 offset:5376
v_mfma_f32_16x16x32_bf16 a[24:27],a[180:183],v[204:207],a[24:27]
v_accvgpr_read_b32 v194,a16
v_accvgpr_read_b32 v195,a17
v_mfma_f32_16x16x32_bf16 a[24:27],a[184:187],v[212:215],a[24:27]
v_accvgpr_read_b32 v12,a18
v_accvgpr_read_b32 v13,a19
v_mfma_f32_16x16x32_bf16 a[24:27],a[188:191],v[220:223],a[24:27]
v_cvt_pk_bf16_f32 v194,v194,v195
v_cvt_pk_bf16_f32 v195,v12,v13
v_mfma_f32_16x16x32_bf16 a[32:35],a[96:99],v[64:67],a[32:35]
buffer_store_dwordx4 v[192:195],v6,s[32:35],s84 offen offset:2048 sc1
v_mfma_f32_16x16x32_bf16 a[32:35],a[100:103],v[68:71],a[32:35]
v_accvgpr_read_b32 v202,a20
v_accvgpr_read_b32 v203,a21
v_mfma_f32_16x16x32_bf16 a[32:35],a[104:107],v[72:75],a[32:35]
v_accvgpr_read_b32 v14,a22
v_accvgpr_read_b32 v15,a23
v_mfma_f32_16x16x32_bf16 a[32:35],a[108:111],v[76:79],a[32:35]
v_cvt_pk_bf16_f32 v202,v202,v203
v_cvt_pk_bf16_f32 v203,v14,v15
v_mfma_f32_16x16x32_bf16 a[36:39],a[108:111],v[92:95],a[36:39]
buffer_store_dwordx4 v[200:203],v6,s[32:35],s85 offen offset:2048 sc1
v_mfma_f32_16x16x32_bf16 a[36:39],a[104:107],v[88:91],a[36:39]
v_accvgpr_read_b32 v210,a24
v_accvgpr_read_b32 v211,a25
v_mfma_f32_16x16x32_bf16 a[36:39],a[100:103],v[84:87],a[36:39]
v_accvgpr_read_b32 v16,a26
v_accvgpr_read_b32 v17,a27
v_mfma_f32_16x16x32_bf16 a[36:39],a[96:99],v[80:83],a[36:39]
v_cvt_pk_bf16_f32 v210,v210,v211
v_cvt_pk_bf16_f32 v211,v16,v17
v_mfma_f32_16x16x32_bf16 a[40:43],a[96:99],v[96:99],a[40:43]
buffer_store_dwordx4 v[208:211],v6,s[32:35],s86 offen offset:2048 sc1
v_mfma_f32_16x16x32_bf16 a[40:43],a[100:103],v[100:103],a[40:43]
ds_read_b64_tr_b16 a[180:181],v44 offset:10496
ds_read_b64_tr_b16 a[182:183],v44 offset:15616
v_mfma_f32_16x16x32_bf16 a[40:43],a[104:107],v[104:107],a[40:43]
ds_read_b64_tr_b16 a[184:185],v44 offset:20736
v_mfma_f32_16x16x32_bf16 a[40:43],a[108:111],v[108:111],a[40:43]
ds_read_b64_tr_b16 a[186:187],v44 offset:25856
v_mfma_f32_16x16x32_bf16 a[32:35],a[112:115],v[132:135],a[32:35]
s_waitcnt lgkmcnt(4)
ds_read_b64_tr_b16 a[188:189],v44 offset:30976
v_mfma_f32_16x16x32_bf16 a[32:35],a[116:119],v[140:143],a[32:35]
ds_read_b64_tr_b16 a[190:191],v44 offset:36096
ds_read_b64_tr_b16 a[96:97],v43 offset:288
v_mfma_f32_16x16x32_bf16 a[32:35],a[120:123],v[148:151],a[32:35]
ds_read_b64_tr_b16 a[98:99],v43 offset:5408
v_mfma_f32_16x16x32_bf16 a[32:35],a[124:127],v[156:159],a[32:35]
ds_read_b64_tr_b16 a[100:101],v43 offset:10528
v_mfma_f32_16x16x32_bf16 a[36:39],a[124:127],v[188:191],a[36:39]
ds_read_b64_tr_b16 a[102:103],v43 offset:15648
v_mfma_f32_16x16x32_bf16 a[36:39],a[120:123],v[180:183],a[36:39]
ds_read_b64_tr_b16 a[104:105],v43 offset:20768
ds_read_b64_tr_b16 a[106:107],v43 offset:25888
v_mfma_f32_16x16x32_bf16 a[36:39],a[116:119],v[172:175],a[36:39]
ds_read_b64_tr_b16 a[108:109],v43 offset:31008
v_mfma_f32_16x16x32_bf16 a[36:39],a[112:115],v[164:167],a[36:39]
ds_read_b64_tr_b16 a[110:111],v43 offset:36128
v_mfma_f32_16x16x32_bf16 a[40:43],a[112:115],v[196:199],a[40:43]
ds_read_b64_tr_b16 a[112:113],v45 offset:288
v_mfma_f32_16x16x32_bf16 a[40:43],a[116:119],v[204:207],a[40:43]
v_accvgpr_read_b32 v224,a32
v_accvgpr_read_b32 v225,a33
v_mfma_f32_16x16x32_bf16 a[40:43],a[120:123],v[212:215],a[40:43]
v_cvt_pk_bf16_f32 v224,v224,v225
v_accvgpr_read_b32 v226,a34
v_mfma_f32_16x16x32_bf16 a[40:43],a[124:127],v[220:223],a[40:43]
v_accvgpr_read_b32 v227,a35
v_cvt_pk_bf16_f32 v225,v226,v227
v_mfma_f32_16x16x32_bf16 a[48:51],a[128:131],v[64:67],a[48:51]
ds_read_b64_tr_b16 a[114:115],v45 offset:5408
ds_read_b64_tr_b16 a[116:117],v45 offset:10528
v_mfma_f32_16x16x32_bf16 a[48:51],a[132:135],v[68:71],a[48:51]
v_accvgpr_read_b32 v232,a36
v_accvgpr_read_b32 v233,a37
v_mfma_f32_16x16x32_bf16 a[48:51],a[136:139],v[72:75],a[48:51]
v_cvt_pk_bf16_f32 v232,v232,v233
v_accvgpr_read_b32 v234,a38
v_mfma_f32_16x16x32_bf16 a[48:51],a[140:143],v[76:79],a[48:51]
v_accvgpr_read_b32 v235,a39
v_cvt_pk_bf16_f32 v233,v234,v235
v_mfma_f32_16x16x32_bf16 a[52:55],a[140:143],v[92:95],a[52:55]
ds_read_b64_tr_b16 a[118:119],v45 offset:15648
ds_read_b64_tr_b16 a[120:121],v45 offset:20768
v_mfma_f32_16x16x32_bf16 a[52:55],a[136:139],v[88:91],a[52:55]
v_accvgpr_read_b32 v240,a40
v_accvgpr_read_b32 v241,a41
v_mfma_f32_16x16x32_bf16 a[52:55],a[132:135],v[84:87],a[52:55]
v_cvt_pk_bf16_f32 v240,v240,v241
v_accvgpr_read_b32 v242,a42
v_mfma_f32_16x16x32_bf16 a[52:55],a[128:131],v[80:83],a[52:55]
v_accvgpr_read_b32 v243,a43
v_cvt_pk_bf16_f32 v241,v242,v243
v_mfma_f32_16x16x32_bf16 a[56:59],a[128:131],v[96:99],a[56:59]
ds_read_b64_tr_b16 a[122:123],v45 offset:25888
ds_read_b64_tr_b16 a[124:125],v45 offset:31008
v_mfma_f32_16x16x32_bf16 a[56:59],a[132:135],v[100:103],a[56:59]
ds_read_b64_tr_b16 a[126:127],v45 offset:36128
v_mfma_f32_16x16x32_bf16 a[56:59],a[136:139],v[104:107],a[56:59]
ds_read_b64_tr_b16 a[128:129],v1
v_mfma_f32_16x16x32_bf16 a[56:59],a[140:143],v[108:111],a[56:59]
ds_read_b64_tr_b16 a[130:131],v1 offset:5120
v_mfma_f32_16x16x32_bf16 a[48:51],a[144:147],v[132:135],a[48:51]
ds_read_b64_tr_b16 a[132:133],v1 offset:10240
ds_read_b64_tr_b16 a[134:135],v1 offset:15360
v_mfma_f32_16x16x32_bf16 a[48:51],a[148:151],v[140:143],a[48:51]
ds_read_b64_tr_b16 a[136:137],v1 offset:20480
v_mfma_f32_16x16x32_bf16 a[48:51],a[152:155],v[148:151],a[48:51]
ds_read_b64_tr_b16 a[138:139],v1 offset:25600
v_mfma_f32_16x16x32_bf16 a[48:51],a[156:159],v[156:159],a[48:51]
ds_read_b64_tr_b16 a[140:141],v1 offset:30720
v_mfma_f32_16x16x32_bf16 a[52:55],a[156:159],v[188:191],a[52:55]
ds_read_b64_tr_b16 a[142:143],v1 offset:35840
v_mfma_f32_16x16x32_bf16 a[52:55],a[152:155],v[180:183],a[52:55]
v_mfma_f32_16x16x32_bf16 a[52:55],a[148:151],v[172:175],a[52:55]
v_mfma_f32_16x16x32_bf16 a[52:55],a[144:147],v[164:167],a[52:55]
v_mfma_f32_16x16x32_bf16 a[56:59],a[144:147],v[196:199],a[56:59]
ds_read_b64_tr_b16 a[144:145],v42
ds_read_b64_tr_b16 a[146:147],v42 offset:5120
v_mfma_f32_16x16x32_bf16 a[56:59],a[148:151],v[204:207],a[56:59]
v_accvgpr_read_b32 v226,a48
v_accvgpr_read_b32 v227,a49
v_mfma_f32_16x16x32_bf16 a[56:59],a[152:155],v[212:215],a[56:59]
v_accvgpr_read_b32 v12,a50
v_accvgpr_read_b32 v13,a51
v_mfma_f32_16x16x32_bf16 a[56:59],a[156:159],v[220:223],a[56:59]
v_cvt_pk_bf16_f32 v226,v226,v227
v_cvt_pk_bf16_f32 v227,v12,v13
v_mfma_f32_16x16x32_bf16 a[64:67],a[160:163],v[64:67],a[64:67]
buffer_store_dwordx4 v[224:227],v6,s[32:35],s84 offen offset:3072 sc1
v_mfma_f32_16x16x32_bf16 a[64:67],a[164:167],v[68:71],a[64:67]
v_accvgpr_read_b32 v234,a52
v_accvgpr_read_b32 v235,a53
v_mfma_f32_16x16x32_bf16 a[64:67],a[168:171],v[72:75],a[64:67]
v_accvgpr_read_b32 v14,a54
v_accvgpr_read_b32 v15,a55
v_mfma_f32_16x16x32_bf16 a[64:67],a[172:175],v[76:79],a[64:67]
v_cvt_pk_bf16_f32 v234,v234,v235
v_cvt_pk_bf16_f32 v235,v14,v15
v_mfma_f32_16x16x32_bf16 a[68:71],a[172:175],v[92:95],a[68:71]
)A"
R"A(buffer_store_dwordx4 v[232:235],v6,s[32:35],s85 offen offset:3072 sc1
v_mfma_f32_16x16x32_bf16 a[68:71],a[168:171],v[88:91],a[68:71]
v_accvgpr_read_b32 v242,a56
v_accvgpr_read_b32 v243,a57
v_mfma_f32_16x16x32_bf16 a[68:71],a[164:167],v[84:87],a[68:71]
v_accvgpr_read_b32 v16,a58
v_accvgpr_read_b32 v17,a59
v_mfma_f32_16x16x32_bf16 a[68:71],a[160:163],v[80:83],a[68:71]
v_cvt_pk_bf16_f32 v242,v242,v243
v_cvt_pk_bf16_f32 v243,v16,v17
v_mfma_f32_16x16x32_bf16 a[72:75],a[160:163],v[96:99],a[72:75]
buffer_store_dwordx4 v[240:243],v6,s[32:35],s86 offen offset:3072 sc1
v_mfma_f32_16x16x32_bf16 a[72:75],a[164:167],v[100:103],a[72:75]
ds_read_b64_tr_b16 a[148:149],v42 offset:10240
ds_read_b64_tr_b16 a[150:151],v42 offset:15360
v_mfma_f32_16x16x32_bf16 a[72:75],a[168:171],v[104:107],a[72:75]
ds_read_b64_tr_b16 a[152:153],v42 offset:20480
v_mfma_f32_16x16x32_bf16 a[72:75],a[172:175],v[108:111],a[72:75]
ds_read_b64_tr_b16 a[154:155],v42 offset:25600
v_mfma_f32_16x16x32_bf16 a[64:67],a[176:179],v[132:135],a[64:67]
s_waitcnt lgkmcnt(4)
ds_read_b64_tr_b16 a[156:157],v42 offset:30720
v_mfma_f32_16x16x32_bf16 a[64:67],a[180:183],v[140:143],a[64:67]
ds_read_b64_tr_b16 a[158:159],v42 offset:35840
ds_read_b64_tr_b16 a[160:161],v44
v_mfma_f32_16x16x32_bf16 a[64:67],a[184:187],v[148:151],a[64:67]
ds_read_b64_tr_b16 a[162:163],v44 offset:5120
v_mfma_f32_16x16x32_bf16 a[64:67],a[188:191],v[156:159],a[64:67]
ds_read_b64_tr_b16 a[164:165],v44 offset:10240
v_mfma_f32_16x16x32_bf16 a[68:71],a[188:191],v[188:191],a[68:71]
ds_read_b64_tr_b16 a[166:167],v44 offset:15360
v_mfma_f32_16x16x32_bf16 a[68:71],a[184:187],v[180:183],a[68:71]
ds_read_b64_tr_b16 a[168:169],v44 offset:20480
ds_read_b64_tr_b16 a[170:171],v44 offset:25600
v_mfma_f32_16x16x32_bf16 a[68:71],a[180:183],v[172:175],a[68:71]
ds_read_b64_tr_b16 a[172:173],v44 offset:30720
v_mfma_f32_16x16x32_bf16 a[68:71],a[176:179],v[164:167],a[68:71]
ds_read_b64_tr_b16 a[174:175],v44 offset:35840
v_mfma_f32_16x16x32_bf16 a[72:75],a[176:179],v[196:199],a[72:75]
ds_read_b64_tr_b16 a[176:177],v2 offset:32
v_mfma_f32_16x16x32_bf16 a[72:75],a[180:183],v[204:207],a[72:75]
ds_read_b64_tr_b16 a[178:179],v2 offset:5152
ds_read_b64_tr_b16 a[180:181],v2 offset:10272
v_mfma_f32_16x16x32_bf16 a[72:75],a[184:187],v[212:215],a[72:75]
ds_read_b64_tr_b16 a[182:183],v2 offset:15392
v_accvgpr_read_b32 v128,a64
v_mfma_f32_16x16x32_bf16 a[72:75],a[188:191],v[220:223],a[72:75]
ds_read_b64_tr_b16 a[184:185],v2 offset:20512
v_accvgpr_read_b32 v129,a65
v_mfma_f32_16x16x32_bf16 a[80:83],a[96:99],v[64:67],a[80:83]
ds_read_b64_tr_b16 a[186:187],v2 offset:25632
v_cvt_pk_bf16_f32 v128,v128,v129
v_mfma_f32_16x16x32_bf16 a[80:83],a[100:103],v[68:71],a[80:83]
ds_read_b64_tr_b16 a[188:189],v2 offset:30752
ds_read_b64_tr_b16 a[190:191],v2 offset:35872
v_mfma_f32_16x16x32_bf16 a[80:83],a[104:107],v[72:75],a[80:83]
v_accvgpr_read_b32 v130,a66
v_accvgpr_read_b32 v131,a67
v_mfma_f32_16x16x32_bf16 a[80:83],a[108:111],v[76:79],a[80:83]
v_cvt_pk_bf16_f32 v129,v130,v131
v_accvgpr_read_b32 v136,a68
v_mfma_f32_16x16x32_bf16 a[84:87],a[108:111],v[92:95],a[84:87]
v_accvgpr_read_b32 v137,a69
v_cvt_pk_bf16_f32 v136,v136,v137
v_mfma_f32_16x16x32_bf16 a[84:87],a[104:107],v[88:91],a[84:87]
v_accvgpr_read_b32 v138,a70
v_accvgpr_read_b32 v139,a71
v_mfma_f32_16x16x32_bf16 a[84:87],a[100:103],v[84:87],a[84:87]
v_cvt_pk_bf16_f32 v137,v138,v139
v_accvgpr_read_b32 v144,a72
v_mfma_f32_16x16x32_bf16 a[84:87],a[96:99],v[80:83],a[84:87]
v_accvgpr_read_b32 v145,a73
v_cvt_pk_bf16_f32 v144,v144,v145
v_mfma_f32_16x16x32_bf16 a[88:91],a[96:99],v[96:99],a[88:91]
ds_read_b64_tr_b16 a[96:97],v43 offset:32
ds_read_b64_tr_b16 a[98:99],v43 offset:5152
v_mfma_f32_16x16x32_bf16 a[88:91],a[100:103],v[100:103],a[88:91]
ds_read_b64_tr_b16 a[100:101],v43 offset:10272
v_accvgpr_read_b32 v146,a74
v_mfma_f32_16x16x32_bf16 a[88:91],a[104:107],v[104:107],a[88:91]
ds_read_b64_tr_b16 a[102:103],v43 offset:15392
v_accvgpr_read_b32 v147,a75
v_mfma_f32_16x16x32_bf16 a[88:91],a[108:111],v[108:111],a[88:91]
ds_read_b64_tr_b16 a[104:105],v43 offset:20512
v_cvt_pk_bf16_f32 v145,v146,v147
v_mfma_f32_16x16x32_bf16 a[80:83],a[112:115],v[132:135],a[80:83]
ds_read_b64_tr_b16 a[106:107],v43 offset:25632
ds_read_b64_tr_b16 a[108:109],v43 offset:30752
v_mfma_f32_16x16x32_bf16 a[80:83],a[116:119],v[140:143],a[80:83]
ds_read_b64_tr_b16 a[110:111],v43 offset:35872
v_mfma_f32_16x16x32_bf16 a[80:83],a[120:123],v[148:151],a[80:83]
v_mfma_f32_16x16x32_bf16 a[80:83],a[124:127],v[156:159],a[80:83]
v_mfma_f32_16x16x32_bf16 a[84:87],a[124:127],v[188:191],a[84:87]
v_mfma_f32_16x16x32_bf16 a[84:87],a[120:123],v[180:183],a[84:87]
v_mfma_f32_16x16x32_bf16 a[84:87],a[116:119],v[172:175],a[84:87]
v_mfma_f32_16x16x32_bf16 a[84:87],a[112:115],v[164:167],a[84:87]
v_mfma_f32_16x16x32_bf16 a[88:91],a[112:115],v[196:199],a[88:91]
ds_read_b64_tr_b16 a[112:113],v45 offset:32
ds_read_b64_tr_b16 a[114:115],v45 offset:5152
v_mfma_f32_16x16x32_bf16 a[88:91],a[116:119],v[204:207],a[88:91]
ds_read_b64_tr_b16 a[116:117],v45 offset:10272
v_accvgpr_read_b32 v130,a80
v_mfma_f32_16x16x32_bf16 a[88:91],a[120:123],v[212:215],a[88:91]
ds_read_b64_tr_b16 a[118:119],v45 offset:15392
v_accvgpr_read_b32 v131,a81
v_mfma_f32_16x16x32_bf16 a[88:91],a[124:127],v[220:223],a[88:91]
ds_read_b64_tr_b16 a[120:121],v45 offset:20512
v_accvgpr_read_b32 v12,a82
v_mfma_f32_16x16x32_bf16 a[204:207],a[128:131],v[152:155],a[204:207]
s_lshl_b32 s94,s71,2
ds_read_b64_tr_b16 a[122:123],v45 offset:25632
v_accvgpr_read_b32 v13,a83
v_mfma_f32_16x16x32_bf16 a[204:207],a[132:135],v[184:187],a[204:207]
s_add_u32 s94,s94,s40
v_cvt_pk_bf16_f32 v130,v130,v131
v_cvt_pk_bf16_f32 v131,v12,v13
v_mfma_f32_16x16x32_bf16 a[204:207],a[136:139],v[216:219],a[204:207]
s_lshl_b32 s95,s42,2
buffer_store_dwordx4 v[128:131],v25,s[32:35],s84 offen sc1
v_mfma_f32_16x16x32_bf16 a[204:207],a[140:143],v[248:251],a[204:207]
s_add_u32 s95,s95,s40
s_lshl_b32 s95,s95,4
v_mov_b32 v18,s68
v_mov_b32 v19,s69
v_mfma_f32_16x16x32_bf16 a[204:207],a[144:147],v[112:115],a[204:207]
s_lshl_b32 s94,s94,4
ds_read_b64_tr_b16 a[124:125],v45 offset:30752
ds_read_b64_tr_b16 a[126:127],v45 offset:35872
s_waitcnt lgkmcnt(6)
v_mfma_f32_16x16x32_bf16 a[204:207],a[148:151],v[116:119],a[204:207]
ds_read_b64_tr_b16 a[128:129],v1 offset:64
ds_read_b64_tr_b16 a[130:131],v1 offset:5184
v_mfma_f32_16x16x32_bf16 a[204:207],a[152:155],v[120:123],a[204:207]
s_waitcnt vmcnt(1)
s_mov_b64 exec,1
buffer_store_dwordx2 v[18:19],v6,s[64:67],s95 offen offset:2048 sc1
s_mov_b64 exec,-1
v_mfma_f32_16x16x32_bf16 a[204:207],a[156:159],v[124:127],a[204:207]
ds_read_b64_tr_b16 a[132:133],v1 offset:10304
ds_read_b64_tr_b16 a[134:135],v1 offset:15424
v_mfma_f32_16x16x32_bf16 a[204:207],a[160:163],v[228:231],a[204:207]
ds_read_b64_tr_b16 a[136:137],v1 offset:20544
ds_read_b64_tr_b16 a[138:139],v1 offset:25664
v_mfma_f32_16x16x32_bf16 a[204:207],a[164:167],v[236:239],a[204:207]
ds_read_b64_tr_b16 a[140:141],v1 offset:30784
ds_read_b64_tr_b16 a[142:143],v1 offset:35904
v_mfma_f32_16x16x32_bf16 a[204:207],a[168:171],v[244:247],a[204:207]
ds_read_b64_tr_b16 a[144:145],v42 offset:64
ds_read_b64_tr_b16 a[146:147],v42 offset:5184
v_mfma_f32_16x16x32_bf16 a[204:207],a[172:175],v[252:255],a[204:207]
ds_read_b64_tr_b16 a[148:149],v42 offset:10304
ds_read_b64_tr_b16 a[150:151],v42 offset:15424
v_mfma_f32_16x16x32_bf16 a[220:223],a[176:179],v[152:155],a[220:223]
ds_read_b64_tr_b16 a[152:153],v42 offset:20544
ds_read_b64_tr_b16 a[154:155],v42 offset:25664
v_mfma_f32_16x16x32_bf16 a[220:223],a[180:183],v[184:187],a[220:223]
ds_read_b64_tr_b16 a[156:157],v42 offset:30784
ds_read_b64_tr_b16 a[158:159],v42 offset:35904
v_mfma_f32_16x16x32_bf16 a[220:223],a[184:187],v[216:219],a[220:223]
v_accvgpr_read_b32 v138,a84
v_accvgpr_read_b32 v139,a85
v_mfma_f32_16x16x32_bf16 a[220:223],a[188:191],v[248:251],a[220:223]
ds_read_b64_tr_b16 a[160:161],v44 offset:64
ds_read_b64_tr_b16 a[162:163],v44 offset:5184
v_mfma_f32_16x16x32_bf16 a[220:223],a[96:99],v[112:115],a[220:223]
v_accvgpr_read_b32 v14,a86
v_accvgpr_read_b32 v15,a87
v_mfma_f32_16x16x32_bf16 a[220:223],a[100:103],v[116:119],a[220:223]
v_cvt_pk_bf16_f32 v138,v138,v139
v_cvt_pk_bf16_f32 v139,v14,v15
v_mfma_f32_16x16x32_bf16 a[220:223],a[104:107],v[120:123],a[220:223]
buffer_store_dwordx4 v[136:139],v25,s[32:35],s85 offen sc1
v_mfma_f32_16x16x32_bf16 a[220:223],a[108:111],v[124:127],a[220:223]
ds_read_b64_tr_b16 a[164:165],v44 offset:10304
ds_read_b64_tr_b16 a[166:167],v44 offset:15424
v_mfma_f32_16x16x32_bf16 a[220:223],a[112:115],v[228:231],a[220:223]
s_waitcnt lgkmcnt(6)
ds_read_b64_tr_b16 a[168:169],v44 offset:20544
ds_read_b64_tr_b16 a[170:171],v44 offset:25664
v_mfma_f32_16x16x32_bf16 a[220:223],a[116:119],v[236:239],a[220:223]
ds_read_b64_tr_b16 a[172:173],v44 offset:30784
ds_read_b64_tr_b16 a[174:175],v44 offset:35904
v_mfma_f32_16x16x32_bf16 a[220:223],a[120:123],v[244:247],a[220:223]
ds_read_b64_tr_b16 a[176:177],v2 offset:96
ds_read_b64_tr_b16 a[178:179],v2 offset:5216
v_mfma_f32_16x16x32_bf16 a[220:223],a[124:127],v[252:255],a[220:223]
v_bfe_u32 v24,v6,4,2
v_min_u32 v24,2,v24
v_mfma_f32_16x16x32_bf16 a[236:239],a[128:131],v[152:155],a[236:239]
ds_read_b64_tr_b16 a[180:181],v2 offset:10336
ds_read_b64_tr_b16 a[182:183],v2 offset:15456
v_mfma_f32_16x16x32_bf16 a[236:239],a[132:135],v[184:187],a[236:239]
v_add_u32 v24,s70,v24
v_add_u32 v24,1,v24
v_mfma_f32_16x16x32_bf16 a[236:239],a[136:139],v[216:219],a[236:239]
ds_read_b64_tr_b16 a[184:185],v2 offset:20576
ds_read_b64_tr_b16 a[186:187],v2 offset:25696
v_mfma_f32_16x16x32_bf16 a[236:239],a[140:143],v[248:251],a[236:239]
v_and_b32 v24,3,v24
v_accvgpr_read_b32 v146,a88
v_mfma_f32_16x16x32_bf16 a[236:239],a[144:147],v[112:115],a[236:239]
v_accvgpr_read_b32 v147,a89
v_lshlrev_b32 v24,8,v24
v_mfma_f32_16x16x32_bf16 a[236:239],a[148:151],v[116:119],a[236:239]
ds_read_b64_tr_b16 a[188:189],v2 offset:30816
ds_read_b64_tr_b16 a[190:191],v2 offset:35936
v_mfma_f32_16x16x32_bf16 a[236:239],a[152:155],v[120:123],a[236:239]
s_waitcnt lgkmcnt(6)
v_accvgpr_read_b32 v16,a90
v_accvgpr_read_b32 v17,a91
v_mfma_f32_16x16x32_bf16 a[236:239],a[156:159],v[124:127],a[236:239]
ds_read_b64_tr_b16 a[96:97],v43 offset:96
ds_read_b64_tr_b16 a[98:99],v43 offset:5216
v_cvt_pk_bf16_f32 v146,v146,v147
v_cvt_pk_bf16_f32 v147,v16,v17
v_mfma_f32_16x16x32_bf16 a[236:239],a[160:163],v[228:231],a[236:239]
buffer_store_dwordx4 v[144:147],v25,s[32:35],s86 offen sc1
ds_read_b64_tr_b16 a[100:101],v43 offset:10336
ds_read_b64_tr_b16 a[102:103],v43 offset:15456
v_mfma_f32_16x16x32_bf16 a[236:239],a[164:167],v[236:239],a[236:239]
ds_read_b64_tr_b16 a[104:105],v43 offset:20576
ds_read_b64_tr_b16 a[106:107],v43 offset:25696
v_mfma_f32_16x16x32_bf16 a[236:239],a[168:171],v[244:247],a[236:239]
ds_read_b64_tr_b16 a[108:109],v43 offset:30816
ds_read_b64_tr_b16 a[110:111],v43 offset:35936
v_mfma_f32_16x16x32_bf16 a[236:239],a[172:175],v[252:255],a[236:239]
ds_read_b64_tr_b16 a[112:113],v45 offset:96
ds_read_b64_tr_b16 a[114:115],v45 offset:5216
v_mfma_f32_16x16x32_bf16 a[252:255],a[176:179],v[152:155],a[252:255]
s_waitcnt lgkmcnt(12)
ds_read_b64_tr_b16 a[116:117],v45 offset:10336
ds_read_b64_tr_b16 a[118:119],v45 offset:15456
v_mfma_f32_16x16x32_bf16 a[252:255],a[180:183],v[184:187],a[252:255]
ds_read_b64_tr_b16 a[120:121],v45 offset:20576
ds_read_b64_tr_b16 a[122:123],v45 offset:25696
v_mfma_f32_16x16x32_bf16 a[252:255],a[184:187],v[216:219],a[252:255]
s_waitcnt lgkmcnt(14)
ds_read_b64_tr_b16 a[124:125],v45 offset:30816
ds_read_b64_tr_b16 a[126:127],v45 offset:35936
v_mfma_f32_16x16x32_bf16 a[252:255],a[188:191],v[248:251],a[252:255]
s_waitcnt lgkmcnt(14)
ds_read_b64_tr_b16 a[128:129],v1 offset:128
ds_read_b64_tr_b16 a[130:131],v1 offset:5248
v_mfma_f32_16x16x32_bf16 a[252:255],a[96:99],v[112:115],a[252:255]
s_waitcnt lgkmcnt(14)
buffer_load_dwordx2 v[26:27],v24,s[64:67],s94 offen offset:2048 sc1
ds_read_b64_tr_b16 a[132:133],v1 offset:10368
ds_read_b64_tr_b16 a[134:135],v1 offset:15488
v_mfma_f32_16x16x32_bf16 a[252:255],a[100:103],v[116:119],a[252:255]
s_waitcnt lgkmcnt(14)
)A"
R"A(ds_read_b64_tr_b16 a[136:137],v1 offset:20608
ds_read_b64_tr_b16 a[138:139],v1 offset:25728
v_mfma_f32_16x16x32_bf16 a[252:255],a[104:107],v[120:123],a[252:255]
s_waitcnt lgkmcnt(14)
ds_read_b64_tr_b16 a[140:141],v1 offset:30848
ds_read_b64_tr_b16 a[142:143],v1 offset:35968
v_mfma_f32_16x16x32_bf16 a[252:255],a[108:111],v[124:127],a[252:255]
s_waitcnt lgkmcnt(14)
ds_read_b64_tr_b16 a[144:145],v42 offset:128
ds_read_b64_tr_b16 a[146:147],v42 offset:5248
v_mfma_f32_16x16x32_bf16 a[252:255],a[112:115],v[228:231],a[252:255]
s_waitcnt lgkmcnt(14)
ds_read_b64_tr_b16 a[148:149],v42 offset:10368
ds_read_b64_tr_b16 a[150:151],v42 offset:15488
v_mfma_f32_16x16x32_bf16 a[252:255],a[116:119],v[236:239],a[252:255]
s_waitcnt lgkmcnt(14)
ds_read_b64_tr_b16 a[152:153],v42 offset:20608
ds_read_b64_tr_b16 a[154:155],v42 offset:25728
v_mfma_f32_16x16x32_bf16 a[252:255],a[120:123],v[244:247],a[252:255]
s_waitcnt lgkmcnt(14)
ds_read_b64_tr_b16 a[156:157],v42 offset:30848
ds_read_b64_tr_b16 a[158:159],v42 offset:35968
v_mfma_f32_16x16x32_bf16 a[252:255],a[124:127],v[252:255],a[252:255]
s_waitcnt lgkmcnt(14)
ds_read_b64_tr_b16 a[160:161],v44 offset:128
ds_read_b64_tr_b16 a[162:163],v44 offset:5248
v_mfma_f32_16x16x32_bf16 a[12:15],a[128:131],v[152:155],a[12:15]
s_waitcnt lgkmcnt(14)
ds_read_b64_tr_b16 a[164:165],v44 offset:10368
ds_read_b64_tr_b16 a[166:167],v44 offset:15488
v_mfma_f32_16x16x32_bf16 a[12:15],a[132:135],v[184:187],a[12:15]
s_waitcnt lgkmcnt(14)
ds_read_b64_tr_b16 a[168:169],v44 offset:20608
ds_read_b64_tr_b16 a[170:171],v44 offset:25728
v_mfma_f32_16x16x32_bf16 a[12:15],a[136:139],v[216:219],a[12:15]
s_waitcnt lgkmcnt(14)
ds_read_b64_tr_b16 a[172:173],v44 offset:30848
ds_read_b64_tr_b16 a[174:175],v44 offset:35968
v_mfma_f32_16x16x32_bf16 a[12:15],a[140:143],v[248:251],a[12:15]
s_waitcnt lgkmcnt(14)
ds_read_b64_tr_b16 a[176:177],v2 offset:160
ds_read_b64_tr_b16 a[178:179],v2 offset:5280
v_mfma_f32_16x16x32_bf16 a[12:15],a[144:147],v[112:115],a[12:15]
s_waitcnt lgkmcnt(14)
ds_read_b64_tr_b16 a[180:181],v2 offset:10400
ds_read_b64_tr_b16 a[182:183],v2 offset:15520
v_mfma_f32_16x16x32_bf16 a[12:15],a[148:151],v[116:119],a[12:15]
s_waitcnt lgkmcnt(14)
ds_read_b64_tr_b16 a[184:185],v2 offset:20640
ds_read_b64_tr_b16 a[186:187],v2 offset:25760
v_mfma_f32_16x16x32_bf16 a[12:15],a[152:155],v[120:123],a[12:15]
s_waitcnt lgkmcnt(14)
ds_read_b64_tr_b16 a[188:189],v2 offset:30880
ds_read_b64_tr_b16 a[190:191],v2 offset:36000
v_mfma_f32_16x16x32_bf16 a[12:15],a[156:159],v[124:127],a[12:15]
s_waitcnt lgkmcnt(14)
ds_read_b64_tr_b16 a[96:97],v43 offset:160
ds_read_b64_tr_b16 a[98:99],v43 offset:5280
v_mfma_f32_16x16x32_bf16 a[12:15],a[160:163],v[228:231],a[12:15]
s_waitcnt lgkmcnt(14)
ds_read_b64_tr_b16 a[100:101],v43 offset:10400
ds_read_b64_tr_b16 a[102:103],v43 offset:15520
v_mfma_f32_16x16x32_bf16 a[12:15],a[164:167],v[236:239],a[12:15]
s_waitcnt lgkmcnt(14)
ds_read_b64_tr_b16 a[104:105],v43 offset:20640
ds_read_b64_tr_b16 a[106:107],v43 offset:25760
v_mfma_f32_16x16x32_bf16 a[12:15],a[168:171],v[244:247],a[12:15]
s_waitcnt lgkmcnt(14)
ds_read_b64_tr_b16 a[108:109],v43 offset:30880
ds_read_b64_tr_b16 a[110:111],v43 offset:36000
v_mfma_f32_16x16x32_bf16 a[12:15],a[172:175],v[252:255],a[12:15]
s_waitcnt lgkmcnt(14)
ds_read_b64_tr_b16 a[112:113],v45 offset:160
ds_read_b64_tr_b16 a[114:115],v45 offset:5280
v_mfma_f32_16x16x32_bf16 a[28:31],a[176:179],v[152:155],a[28:31]
s_waitcnt lgkmcnt(14)
ds_read_b64_tr_b16 a[116:117],v45 offset:10400
ds_read_b64_tr_b16 a[118:119],v45 offset:15520
v_mfma_f32_16x16x32_bf16 a[28:31],a[180:183],v[184:187],a[28:31]
s_waitcnt lgkmcnt(14)
ds_read_b64_tr_b16 a[120:121],v45 offset:20640
ds_read_b64_tr_b16 a[122:123],v45 offset:25760
v_mfma_f32_16x16x32_bf16 a[28:31],a[184:187],v[216:219],a[28:31]
s_waitcnt lgkmcnt(14)
ds_read_b64_tr_b16 a[124:125],v45 offset:30880
ds_read_b64_tr_b16 a[126:127],v45 offset:36000
v_mfma_f32_16x16x32_bf16 a[28:31],a[188:191],v[248:251],a[28:31]
s_waitcnt lgkmcnt(14)
ds_read_b64_tr_b16 a[128:129],v1 offset:192
ds_read_b64_tr_b16 a[130:131],v1 offset:5312
v_mfma_f32_16x16x32_bf16 a[28:31],a[96:99],v[112:115],a[28:31]
s_waitcnt lgkmcnt(14)
ds_read_b64_tr_b16 a[132:133],v1 offset:10432
ds_read_b64_tr_b16 a[134:135],v1 offset:15552
v_mfma_f32_16x16x32_bf16 a[28:31],a[100:103],v[116:119],a[28:31]
s_waitcnt lgkmcnt(14)
ds_read_b64_tr_b16 a[136:137],v1 offset:20672
ds_read_b64_tr_b16 a[138:139],v1 offset:25792
v_mfma_f32_16x16x32_bf16 a[28:31],a[104:107],v[120:123],a[28:31]
s_waitcnt lgkmcnt(14)
ds_read_b64_tr_b16 a[140:141],v1 offset:30912
ds_read_b64_tr_b16 a[142:143],v1 offset:36032
v_mfma_f32_16x16x32_bf16 a[28:31],a[108:111],v[124:127],a[28:31]
s_waitcnt lgkmcnt(14)
ds_read_b64_tr_b16 a[144:145],v42 offset:192
ds_read_b64_tr_b16 a[146:147],v42 offset:5312
v_mfma_f32_16x16x32_bf16 a[28:31],a[112:115],v[228:231],a[28:31]
s_waitcnt lgkmcnt(14)
ds_read_b64_tr_b16 a[148:149],v42 offset:10432
ds_read_b64_tr_b16 a[150:151],v42 offset:15552
v_mfma_f32_16x16x32_bf16 a[28:31],a[116:119],v[236:239],a[28:31]
s_waitcnt lgkmcnt(14)
ds_read_b64_tr_b16 a[152:153],v42 offset:20672
ds_read_b64_tr_b16 a[154:155],v42 offset:25792
v_mfma_f32_16x16x32_bf16 a[28:31],a[120:123],v[244:247],a[28:31]
s_lshl_b32 s95,s42,2
ds_read_b64_tr_b16 a[156:157],v42 offset:30912
ds_read_b64_tr_b16 a[158:159],v42 offset:36032
s_waitcnt lgkmcnt(15)
v_mfma_f32_16x16x32_bf16 a[28:31],a[124:127],v[252:255],a[28:31]
s_add_u32 s95,s95,s40
s_lshl_b32 s95,s95,4
ds_read_b64_tr_b16 a[160:161],v44 offset:192
ds_read_b64_tr_b16 a[162:163],v44 offset:5312
v_mov_b32 v18,s68
v_mov_b32 v19,s69
s_waitcnt lgkmcnt(15)
v_mfma_f32_16x16x32_bf16 a[44:47],a[128:131],v[152:155],a[44:47]
s_waitcnt vmcnt(1)
s_mov_b64 exec,1
buffer_store_dwordx2 v[18:19],v6,s[64:67],s95 offen offset:3072 sc1
s_mov_b64 exec,-1
ds_read_b64_tr_b16 a[164:165],v44 offset:10432
ds_read_b64_tr_b16 a[166:167],v44 offset:15552
s_waitcnt vmcnt(1)
v_cmp_ne_u32 s[90:91],s68,v26
v_cmp_ne_u32 s[92:93],s69,v27
s_nop 0
s_or_b64 s[90:91],s[90:91],s[92:93]
s_cbranch_scc1 L_fr9_%=
L_fo9_%=:
s_waitcnt lgkmcnt(15)
v_mfma_f32_16x16x32_bf16 a[44:47],a[132:135],v[184:187],a[44:47]
s_lshl_b32 s94,s71,2
ds_read_b64_tr_b16 a[168:169],v44 offset:20672
ds_read_b64_tr_b16 a[170:171],v44 offset:25792
s_waitcnt lgkmcnt(15)
v_mfma_f32_16x16x32_bf16 a[44:47],a[136:139],v[216:219],a[44:47]
s_add_u32 s94,s94,s40
ds_read_b64_tr_b16 a[172:173],v44 offset:30912
ds_read_b64_tr_b16 a[174:175],v44 offset:36032
s_waitcnt lgkmcnt(15)
v_mfma_f32_16x16x32_bf16 a[44:47],a[140:143],v[248:251],a[44:47]
s_lshl_b32 s94,s94,4
ds_read_b64_tr_b16 a[176:177],v2 offset:224
ds_read_b64_tr_b16 a[178:179],v2 offset:5344
s_waitcnt lgkmcnt(15)
v_mfma_f32_16x16x32_bf16 a[44:47],a[144:147],v[112:115],a[44:47]
s_waitcnt lgkmcnt(14)
buffer_load_dwordx4 v[64:67],v6,s[32:35],s76 offen sc1
ds_read_b64_tr_b16 a[180:181],v2 offset:10464
ds_read_b64_tr_b16 a[182:183],v2 offset:15584
v_mfma_f32_16x16x32_bf16 a[44:47],a[148:151],v[116:119],a[44:47]
s_waitcnt lgkmcnt(14)
ds_read_b64_tr_b16 a[184:185],v2 offset:20704
ds_read_b64_tr_b16 a[186:187],v2 offset:25824
v_mfma_f32_16x16x32_bf16 a[44:47],a[152:155],v[120:123],a[44:47]
s_waitcnt lgkmcnt(14)
ds_read_b64_tr_b16 a[188:189],v2 offset:30944
ds_read_b64_tr_b16 a[190:191],v2 offset:36064
v_mfma_f32_16x16x32_bf16 a[44:47],a[156:159],v[124:127],a[44:47]
s_waitcnt lgkmcnt(14)
ds_read_b64_tr_b16 a[96:97],v43 offset:224
ds_read_b64_tr_b16 a[98:99],v43 offset:5344
v_mfma_f32_16x16x32_bf16 a[44:47],a[160:163],v[228:231],a[44:47]
s_waitcnt lgkmcnt(14)
buffer_load_dwordx4 v[80:83],v6,s[32:35],s77 offen sc1
ds_read_b64_tr_b16 a[100:101],v43 offset:10464
ds_read_b64_tr_b16 a[102:103],v43 offset:15584
v_mfma_f32_16x16x32_bf16 a[44:47],a[164:167],v[236:239],a[44:47]
s_waitcnt lgkmcnt(14)
ds_read_b64_tr_b16 a[104:105],v43 offset:20704
ds_read_b64_tr_b16 a[106:107],v43 offset:25824
v_mfma_f32_16x16x32_bf16 a[44:47],a[168:171],v[244:247],a[44:47]
s_waitcnt lgkmcnt(14)
ds_read_b64_tr_b16 a[108:109],v43 offset:30944
ds_read_b64_tr_b16 a[110:111],v43 offset:36064
v_mfma_f32_16x16x32_bf16 a[44:47],a[172:175],v[252:255],a[44:47]
s_waitcnt lgkmcnt(14)
ds_read_b64_tr_b16 a[112:113],v45 offset:224
ds_read_b64_tr_b16 a[114:115],v45 offset:5344
v_mfma_f32_16x16x32_bf16 a[60:63],a[176:179],v[152:155],a[60:63]
s_waitcnt lgkmcnt(14)
buffer_load_dwordx4 v[96:99],v6,s[32:35],s78 offen sc1
ds_read_b64_tr_b16 a[116:117],v45 offset:10464
ds_read_b64_tr_b16 a[118:119],v45 offset:15584
v_mfma_f32_16x16x32_bf16 a[60:63],a[180:183],v[184:187],a[60:63]
s_waitcnt lgkmcnt(14)
ds_read_b64_tr_b16 a[120:121],v45 offset:20704
ds_read_b64_tr_b16 a[122:123],v45 offset:25824
v_mfma_f32_16x16x32_bf16 a[60:63],a[184:187],v[216:219],a[60:63]
s_waitcnt lgkmcnt(14)
ds_read_b64_tr_b16 a[124:125],v45 offset:30944
ds_read_b64_tr_b16 a[126:127],v45 offset:36064
v_mfma_f32_16x16x32_bf16 a[60:63],a[188:191],v[248:251],a[60:63]
s_waitcnt lgkmcnt(14)
ds_read_b64_tr_b16 a[128:129],v1 offset:256
ds_read_b64_tr_b16 a[130:131],v1 offset:5376
v_mfma_f32_16x16x32_bf16 a[60:63],a[96:99],v[112:115],a[60:63]
s_waitcnt lgkmcnt(14)
buffer_load_dwordx4 v[68:71],v6,s[32:35],s76 offen offset:1024 sc1
ds_read_b64_tr_b16 a[132:133],v1 offset:10496
ds_read_b64_tr_b16 a[134:135],v1 offset:15616
v_mfma_f32_16x16x32_bf16 a[60:63],a[100:103],v[116:119],a[60:63]
s_waitcnt lgkmcnt(14)
ds_read_b64_tr_b16 a[136:137],v1 offset:20736
ds_read_b64_tr_b16 a[138:139],v1 offset:25856
v_mfma_f32_16x16x32_bf16 a[60:63],a[104:107],v[120:123],a[60:63]
s_waitcnt lgkmcnt(14)
ds_read_b64_tr_b16 a[140:141],v1 offset:30976
ds_read_b64_tr_b16 a[142:143],v1 offset:36096
v_mfma_f32_16x16x32_bf16 a[60:63],a[108:111],v[124:127],a[60:63]
s_waitcnt lgkmcnt(14)
ds_read_b64_tr_b16 a[144:145],v42 offset:256
ds_read_b64_tr_b16 a[146:147],v42 offset:5376
v_mfma_f32_16x16x32_bf16 a[60:63],a[112:115],v[228:231],a[60:63]
s_waitcnt lgkmcnt(14)
buffer_load_dwordx4 v[84:87],v6,s[32:35],s77 offen offset:1024 sc1
ds_read_b64_tr_b16 a[148:149],v42 offset:10496
ds_read_b64_tr_b16 a[150:151],v42 offset:15616
v_mfma_f32_16x16x32_bf16 a[60:63],a[116:119],v[236:239],a[60:63]
s_waitcnt lgkmcnt(14)
ds_read_b64_tr_b16 a[152:153],v42 offset:20736
ds_read_b64_tr_b16 a[154:155],v42 offset:25856
v_mfma_f32_16x16x32_bf16 a[60:63],a[120:123],v[244:247],a[60:63]
s_waitcnt lgkmcnt(14)
ds_read_b64_tr_b16 a[156:157],v42 offset:30976
ds_read_b64_tr_b16 a[158:159],v42 offset:36096
v_mfma_f32_16x16x32_bf16 a[60:63],a[124:127],v[252:255],a[60:63]
s_waitcnt lgkmcnt(14)
ds_read_b64_tr_b16 a[160:161],v44 offset:256
ds_read_b64_tr_b16 a[162:163],v44 offset:5376
v_mfma_f32_16x16x32_bf16 a[76:79],a[128:131],v[152:155],a[76:79]
s_waitcnt lgkmcnt(14)
buffer_load_dwordx4 v[100:103],v6,s[32:35],s78 offen offset:1024 sc1
ds_read_b64_tr_b16 a[164:165],v44 offset:10496
ds_read_b64_tr_b16 a[166:167],v44 offset:15616
v_mfma_f32_16x16x32_bf16 a[76:79],a[132:135],v[184:187],a[76:79]
s_waitcnt lgkmcnt(14)
ds_read_b64_tr_b16 a[168:169],v44 offset:20736
ds_read_b64_tr_b16 a[170:171],v44 offset:25856
v_bfe_u32 v24,v6,4,2
v_min_u32 v24,2,v24
v_mfma_f32_16x16x32_bf16 a[76:79],a[136:139],v[216:219],a[76:79]
s_waitcnt lgkmcnt(14)
ds_read_b64_tr_b16 a[172:173],v44 offset:30976
ds_read_b64_tr_b16 a[174:175],v44 offset:36096
v_add_u32 v24,s70,v24
v_add_u32 v24,1,v24
v_mfma_f32_16x16x32_bf16 a[76:79],a[140:143],v[248:251],a[76:79]
s_waitcnt lgkmcnt(14)
ds_read_b64_tr_b16 a[176:177],v2 offset:288
ds_read_b64_tr_b16 a[178:179],v2 offset:5408
v_and_b32 v24,3,v24
v_lshlrev_b32 v24,8,v24
v_mfma_f32_16x16x32_bf16 a[76:79],a[144:147],v[112:115],a[76:79]
s_waitcnt lgkmcnt(14)
buffer_load_dwordx4 v[72:75],v6,s[32:35],s76 offen offset:2048 sc1
ds_read_b64_tr_b16 a[180:181],v2 offset:10528
ds_read_b64_tr_b16 a[182:183],v2 offset:15648
v_mfma_f32_16x16x32_bf16 a[76:79],a[148:151],v[116:119],a[76:79]
s_waitcnt lgkmcnt(14)
ds_read_b64_tr_b16 a[184:185],v2 offset:20768
ds_read_b64_tr_b16 a[186:187],v2 offset:25888
v_mfma_f32_16x16x32_bf16 a[76:79],a[152:155],v[120:123],a[76:79]
s_waitcnt lgkmcnt(14)
ds_read_b64_tr_b16 a[188:189],v2 offset:31008
)A"
R"A(ds_read_b64_tr_b16 a[190:191],v2 offset:36128
v_mfma_f32_16x16x32_bf16 a[76:79],a[156:159],v[124:127],a[76:79]
s_waitcnt lgkmcnt(14)
ds_read_b64_tr_b16 a[96:97],v43 offset:288
ds_read_b64_tr_b16 a[98:99],v43 offset:5408
v_mfma_f32_16x16x32_bf16 a[76:79],a[160:163],v[228:231],a[76:79]
s_waitcnt lgkmcnt(14)
buffer_load_dwordx4 v[88:91],v6,s[32:35],s77 offen offset:2048 sc1
ds_read_b64_tr_b16 a[100:101],v43 offset:10528
ds_read_b64_tr_b16 a[102:103],v43 offset:15648
v_mfma_f32_16x16x32_bf16 a[76:79],a[164:167],v[236:239],a[76:79]
s_waitcnt lgkmcnt(14)
ds_read_b64_tr_b16 a[104:105],v43 offset:20768
ds_read_b64_tr_b16 a[106:107],v43 offset:25888
v_mfma_f32_16x16x32_bf16 a[76:79],a[168:171],v[244:247],a[76:79]
s_waitcnt lgkmcnt(14)
ds_read_b64_tr_b16 a[108:109],v43 offset:31008
ds_read_b64_tr_b16 a[110:111],v43 offset:36128
v_mfma_f32_16x16x32_bf16 a[76:79],a[172:175],v[252:255],a[76:79]
s_waitcnt lgkmcnt(14)
ds_read_b64_tr_b16 a[112:113],v45 offset:288
ds_read_b64_tr_b16 a[114:115],v45 offset:5408
v_mfma_f32_16x16x32_bf16 a[92:95],a[176:179],v[152:155],a[92:95]
s_waitcnt lgkmcnt(14)
buffer_load_dwordx4 v[104:107],v6,s[32:35],s78 offen offset:2048 sc1
ds_read_b64_tr_b16 a[116:117],v45 offset:10528
ds_read_b64_tr_b16 a[118:119],v45 offset:15648
v_mfma_f32_16x16x32_bf16 a[92:95],a[180:183],v[184:187],a[92:95]
s_waitcnt lgkmcnt(14)
ds_read_b64_tr_b16 a[120:121],v45 offset:20768
ds_read_b64_tr_b16 a[122:123],v45 offset:25888
v_mfma_f32_16x16x32_bf16 a[92:95],a[184:187],v[216:219],a[92:95]
s_waitcnt lgkmcnt(14)
ds_read_b64_tr_b16 a[124:125],v45 offset:31008
ds_read_b64_tr_b16 a[126:127],v45 offset:36128
v_mfma_f32_16x16x32_bf16 a[92:95],a[188:191],v[248:251],a[92:95]
s_waitcnt lgkmcnt(14)
v_mfma_f32_16x16x32_bf16 a[92:95],a[96:99],v[112:115],a[92:95]
s_waitcnt lgkmcnt(12)
buffer_load_dwordx4 v[76:79],v6,s[32:35],s76 offen offset:3072 sc1
v_mfma_f32_16x16x32_bf16 a[92:95],a[100:103],v[116:119],a[92:95]
s_waitcnt lgkmcnt(10)
v_mfma_f32_16x16x32_bf16 a[92:95],a[104:107],v[120:123],a[92:95]
s_waitcnt lgkmcnt(8)
v_mfma_f32_16x16x32_bf16 a[92:95],a[108:111],v[124:127],a[92:95]
s_waitcnt lgkmcnt(6)
v_mfma_f32_16x16x32_bf16 a[92:95],a[112:115],v[228:231],a[92:95]
s_waitcnt lgkmcnt(4)
buffer_load_dwordx4 v[92:95],v6,s[32:35],s77 offen offset:3072 sc1
v_mfma_f32_16x16x32_bf16 a[92:95],a[116:119],v[236:239],a[92:95]
s_waitcnt lgkmcnt(2)
v_mfma_f32_16x16x32_bf16 a[92:95],a[120:123],v[244:247],a[92:95]
s_waitcnt lgkmcnt(0)
v_mfma_f32_16x16x32_bf16 a[92:95],a[124:127],v[252:255],a[92:95]
buffer_load_dwordx2 v[26:27],v24,s[64:67],s94 offen offset:3072 sc1
s_waitcnt vmcnt(0)
v_cmp_ne_u32 s[90:91],s68,v26
v_cmp_ne_u32 s[92:93],s69,v27
s_nop 0
s_or_b64 s[90:91],s[90:91],s[92:93]
s_cbranch_scc1 L_fr10_%=
L_fo10_%=:
buffer_load_dwordx4 v[132:135],v25,s[32:35],s76 offen sc1
buffer_load_dwordx4 v[164:167],v25,s[32:35],s77 offen sc1
buffer_load_dwordx4 v[196:199],v25,s[32:35],s78 offen sc1
v_mfma_f32_16x16x32_bf16 a[204:207],v[34:37],v[64:67],a[204:207]
v_mfma_f32_16x16x32_bf16 a[204:207],v[34:37],v[80:83],a[204:207]
buffer_load_dwordx4 v[108:111],v6,s[32:35],s78 offen offset:3072 sc1
v_mfma_f32_16x16x32_bf16 a[204:207],v[34:37],v[96:99],a[204:207]
v_mfma_f32_16x16x32_bf16 a[220:223],v[32:35],v[64:67],a[220:223]
v_mfma_f32_16x16x32_bf16 a[220:223],v[32:35],v[80:83],a[220:223]
v_mfma_f32_16x16x32_bf16 a[220:223],v[32:35],v[96:99],a[220:223]
v_mfma_f32_16x16x32_bf16 a[236:239],v[34:37],v[68:71],a[236:239]
v_mfma_f32_16x16x32_bf16 a[236:239],v[34:37],v[84:87],a[236:239]
v_mfma_f32_16x16x32_bf16 a[236:239],v[34:37],v[100:103],a[236:239]
s_nop 1
v_accvgpr_read_b32 v128,a204
v_accvgpr_read_b32 v129,a205
v_mfma_f32_16x16x32_bf16 a[252:255],v[32:35],v[68:71],a[252:255]
v_cvt_pk_bf16_f32 v128,v128,v129
v_accvgpr_read_b32 v130,a206
v_mfma_f32_16x16x32_bf16 a[252:255],v[32:35],v[84:87],a[252:255]
v_accvgpr_read_b32 v131,a207
v_cvt_pk_bf16_f32 v129,v130,v131
v_mfma_f32_16x16x32_bf16 a[252:255],v[32:35],v[100:103],a[252:255]
buffer_store_dwordx2 v[128:129],v40,s[20:23],s43 offen
v_mfma_f32_16x16x32_bf16 a[12:15],v[34:37],v[72:75],a[12:15]
v_accvgpr_read_b32 v130,a220
v_accvgpr_read_b32 v131,a221
v_mfma_f32_16x16x32_bf16 a[12:15],v[34:37],v[88:91],a[12:15]
v_accvgpr_read_b32 v12,a222
v_accvgpr_read_b32 v13,a223
v_mfma_f32_16x16x32_bf16 a[12:15],v[34:37],v[104:107],a[12:15]
v_cvt_pk_bf16_f32 v130,v130,v131
v_cvt_pk_bf16_f32 v131,v12,v13
v_mfma_f32_16x16x32_bf16 a[28:31],v[32:35],v[72:75],a[28:31]
buffer_store_dwordx2 v[130:131],v40,s[20:23],s43 offen offset:32
v_mfma_f32_16x16x32_bf16 a[28:31],v[32:35],v[88:91],a[28:31]
v_accvgpr_read_b32 v136,a236
v_accvgpr_read_b32 v137,a237
v_mfma_f32_16x16x32_bf16 a[28:31],v[32:35],v[104:107],a[28:31]
v_cvt_pk_bf16_f32 v136,v136,v137
v_accvgpr_read_b32 v138,a238
v_mfma_f32_16x16x32_bf16 a[44:47],v[34:37],v[76:79],a[44:47]
v_accvgpr_read_b32 v139,a239
v_cvt_pk_bf16_f32 v137,v138,v139
v_mfma_f32_16x16x32_bf16 a[44:47],v[34:37],v[92:95],a[44:47]
s_waitcnt vmcnt(2)
buffer_store_dwordx2 v[136:137],v40,s[20:23],s43 offen offset:64
v_mfma_f32_16x16x32_bf16 a[44:47],v[34:37],v[108:111],a[44:47]
v_accvgpr_read_b32 v144,a12
v_accvgpr_read_b32 v145,a13
v_cvt_pk_bf16_f32 v144,v144,v145
v_accvgpr_read_b32 v146,a14
v_accvgpr_read_b32 v147,a15
v_mfma_f32_16x16x32_bf16 a[60:63],v[32:35],v[76:79],a[60:63]
v_accvgpr_read_b32 v138,a252
v_accvgpr_read_b32 v139,a253
v_cvt_pk_bf16_f32 v145,v146,v147
v_mfma_f32_16x16x32_bf16 a[60:63],v[32:35],v[92:95],a[60:63]
v_accvgpr_read_b32 v14,a254
v_accvgpr_read_b32 v15,a255
v_cvt_pk_bf16_f32 v138,v138,v139
v_cvt_pk_bf16_f32 v139,v14,v15
v_mfma_f32_16x16x32_bf16 a[60:63],v[32:35],v[108:111],a[60:63]
buffer_store_dwordx2 v[138:139],v40,s[20:23],s43 offen offset:96
buffer_store_dwordx2 v[144:145],v40,s[20:23],s43 offen offset:128
v_accvgpr_read_b32 v146,a28
v_accvgpr_read_b32 v147,a29
v_accvgpr_read_b32 v16,a30
v_accvgpr_read_b32 v17,a31
v_cvt_pk_bf16_f32 v146,v146,v147
v_cvt_pk_bf16_f32 v147,v16,v17
s_nop 3
buffer_store_dwordx2 v[146:147],v40,s[20:23],s43 offen offset:160
v_accvgpr_read_b32 v160,a44
v_accvgpr_read_b32 v161,a45
v_cvt_pk_bf16_f32 v160,v160,v161
v_accvgpr_read_b32 v162,a46
v_accvgpr_read_b32 v163,a47
v_cvt_pk_bf16_f32 v161,v162,v163
buffer_store_dwordx2 v[160:161],v40,s[20:23],s43 offen offset:192
v_accvgpr_read_b32 v162,a60
v_accvgpr_read_b32 v163,a61
v_accvgpr_read_b32 v12,a62
v_accvgpr_read_b32 v13,a63
v_cvt_pk_bf16_f32 v162,v162,v163
v_cvt_pk_bf16_f32 v163,v12,v13
buffer_store_dwordx2 v[162:163],v40,s[20:23],s43 offen offset:224
v_mfma_f32_16x16x32_bf16 a[76:79],v[34:37],v[132:135],a[76:79]
v_mfma_f32_16x16x32_bf16 a[76:79],v[34:37],v[164:167],a[76:79]
v_mfma_f32_16x16x32_bf16 a[76:79],v[34:37],v[196:199],a[76:79]
v_mfma_f32_16x16x32_bf16 a[92:95],v[32:35],v[132:135],a[92:95]
v_mfma_f32_16x16x32_bf16 a[92:95],v[32:35],v[164:167],a[92:95]
v_mfma_f32_16x16x32_bf16 a[92:95],v[32:35],v[196:199],a[92:95]
s_nop 3
s_nop 0
v_accvgpr_read_b32 v168,a76
v_accvgpr_read_b32 v169,a77
v_cvt_pk_bf16_f32 v168,v168,v169
v_accvgpr_read_b32 v170,a78
v_accvgpr_read_b32 v171,a79
v_cvt_pk_bf16_f32 v169,v170,v171
buffer_store_dwordx2 v[168:169],v40,s[20:23],s43 offen offset:256
v_accvgpr_read_b32 v170,a92
v_accvgpr_read_b32 v171,a93
v_accvgpr_read_b32 v14,a94
v_accvgpr_read_b32 v15,a95
v_cvt_pk_bf16_f32 v170,v170,v171
v_cvt_pk_bf16_f32 v171,v14,v15
buffer_store_dwordx2 v[170:171],v40,s[20:23],s43 offen offset:288
s_or_b32 s62,s42,s40
s_cmp_lg_u32 s62,0
s_cbranch_scc1 L_noep_%=
v_mov_b32 v12,s68
s_mov_b64 exec,1
buffer_store_dword v12,v25,s[64:67],0 offen sc1
s_mov_b64 exec,-1
L_noep_%=:
s_waitcnt vmcnt(0) lgkmcnt(0)
s_branch L_end_%=
L_ps1_%=:
s_min_i32 s79,s79,100
s_add_u32 s78,s78,s79
L_pl1_%=:
s_sleep 1
s_sub_u32 s79,s79,4
s_cmp_gt_i32 s79,1
s_cbranch_scc1 L_pl1_%=
s_branch L_pr1_%=
L_ps2_%=:
s_min_i32 s79,s79,100
s_add_u32 s78,s78,s79
L_pl2_%=:
s_sleep 1
s_sub_u32 s79,s79,4
s_cmp_gt_i32 s79,1
s_cbranch_scc1 L_pl2_%=
s_branch L_pr2_%=
L_ps3_%=:
s_min_i32 s79,s79,100
s_add_u32 s78,s78,s79
L_pl3_%=:
s_sleep 1
s_sub_u32 s79,s79,4
s_cmp_gt_i32 s79,1
s_cbranch_scc1 L_pl3_%=
s_branch L_pr3_%=
L_ps4_%=:
s_min_i32 s79,s79,100
s_add_u32 s78,s78,s79
L_pl4_%=:
s_sleep 1
s_sub_u32 s79,s79,4
s_cmp_gt_i32 s79,1
s_cbranch_scc1 L_pl4_%=
s_branch L_pr4_%=
L_ps5_%=:
s_min_i32 s79,s79,100
s_add_u32 s78,s78,s79
L_pl5_%=:
s_sleep 1
s_sub_u32 s79,s79,4
s_cmp_gt_i32 s79,1
s_cbranch_scc1 L_pl5_%=
s_branch L_pr5_%=
L_ps6_%=:
s_min_i32 s79,s79,100
s_add_u32 s78,s78,s79
L_pl6_%=:
s_sleep 1
s_sub_u32 s79,s79,4
s_cmp_gt_i32 s79,1
s_cbranch_scc1 L_pl6_%=
s_branch L_pr6_%=
L_fr7_%=:
s_mov_b32 s88,262144
L_fl7_%=:
buffer_load_dwordx2 v[26:27],v24,s[64:67],s94 offen sc1
s_waitcnt vmcnt(0)
s_sub_u32 s88,s88,1
s_cmp_eq_u32 s88,0
s_cbranch_scc1 L_fo7_%=
v_cmp_ne_u32 s[90:91],s68,v26
v_cmp_ne_u32 s[92:93],s69,v27
s_nop 0
s_or_b64 s[90:91],s[90:91],s[92:93]
s_cbranch_scc1 L_fl7_%=
s_branch L_fo7_%=
L_fr8_%=:
s_mov_b32 s88,262144
L_fl8_%=:
buffer_load_dwordx2 v[26:27],v24,s[64:67],s94 offen offset:1024 sc1
s_waitcnt vmcnt(0)
s_sub_u32 s88,s88,1
s_cmp_eq_u32 s88,0
s_cbranch_scc1 L_fo8_%=
v_cmp_ne_u32 s[90:91],s68,v26
v_cmp_ne_u32 s[92:93],s69,v27
s_nop 0
s_or_b64 s[90:91],s[90:91],s[92:93]
s_cbranch_scc1 L_fl8_%=
s_branch L_fo8_%=
L_fr9_%=:
s_mov_b32 s88,262144
L_fl9_%=:
buffer_load_dwordx2 v[26:27],v24,s[64:67],s94 offen offset:2048 sc1
s_waitcnt vmcnt(0)
s_sub_u32 s88,s88,1
s_cmp_eq_u32 s88,0
s_cbranch_scc1 L_fo9_%=
v_cmp_ne_u32 s[90:91],s68,v26
v_cmp_ne_u32 s[92:93],s69,v27
s_nop 0
s_or_b64 s[90:91],s[90:91],s[92:93]
s_cbranch_scc1 L_fl9_%=
s_branch L_fo9_%=
L_fr10_%=:
s_mov_b32 s88,262144
L_fl10_%=:
buffer_load_dwordx2 v[26:27],v24,s[64:67],s94 offen offset:3072 sc1
s_waitcnt vmcnt(0)
s_sub_u32 s88,s88,1
s_cmp_eq_u32 s88,0
s_cbranch_scc1 L_fo10_%=
v_cmp_ne_u32 s[90:91],s68,v26
v_cmp_ne_u32 s[92:93],s69,v27
s_nop 0
s_or_b64 s[90:91],s[90:91],s[92:93]
s_cbranch_scc1 L_fl10_%=
s_branch L_fo10_%=
L_fast_end_%=:
L_slow0_%=:
s_mov_b32 s98,0
s_branch L_slowc_%=
L_slow1_%=:
s_mov_b32 s98,1
L_slowc_%=:
s_nop 7
s_nop 7
s_waitcnt vmcnt(0) lgkmcnt(0)
s_lshl_b32 s96,%1,2
s_add_u32 s96,s96,s40
s_lshl_b32 s96,s96,15
s_add_u32 s96,s96,167772160
s_mov_b64 s[56:57],s[12:13]
s_add_u32 s56,s56,s96
s_addc_u32 s57,s57,0
s_and_b32 s57,s57,0xffff
s_mov_b32 s58,32768
s_mov_b32 s59,0x20000
s_mov_b32 s96,0
buffer_store_dwordx4 v[64:67],v6,s[56:59],s96 offen
buffer_store_dwordx4 v[68:71],v6,s[56:59],s96 offen offset:1024
buffer_store_dwordx4 v[72:75],v6,s[56:59],s96 offen offset:2048
buffer_store_dwordx4 v[76:79],v6,s[56:59],s96 offen offset:3072
s_mov_b32 s96,4096
buffer_store_dwordx4 v[80:83],v6,s[56:59],s96 offen
buffer_store_dwordx4 v[84:87],v6,s[56:59],s96 offen offset:1024
buffer_store_dwordx4 v[88:91],v6,s[56:59],s96 offen offset:2048
buffer_store_dwordx4 v[92:95],v6,s[56:59],s96 offen offset:3072
s_mov_b32 s96,8192
buffer_store_dwordx4 v[96:99],v6,s[56:59],s96 offen
buffer_store_dwordx4 v[100:103],v6,s[56:59],s96 offen offset:1024
buffer_store_dwordx4 v[104:107],v6,s[56:59],s96 offen offset:2048
buffer_store_dwordx4 v[108:111],v6,s[56:59],s96 offen offset:3072
s_mov_b32 s96,12288
buffer_store_dwordx4 v[112:115],v6,s[56:59],s96 offen
buffer_store_dwordx4 v[116:119],v6,s[56:59],s96 offen offset:1024
buffer_store_dwordx4 v[120:123],v6,s[56:59],s96 offen offset:2048
buffer_store_dwordx4 v[124:127],v6,s[56:59],s96 offen offset:3072
s_mov_b32 s96,16384
buffer_store_dwordx4 v[128:131],v6,s[56:59],s96 offen
buffer_store_dwordx4 v[132:135],v6,s[56:59],s96 offen offset:1024
buffer_store_dwordx4 v[136:139],v6,s[56:59],s96 offen offset:2048
buffer_store_dwordx4 v[140:143],v6,s[56:59],s96 offen offset:3072
s_mov_b32 s96,20480
buffer_store_dwordx4 v[144:147],v6,s[56:59],s96 offen
buffer_store_dwordx4 v[148:151],v6,s[56:59],s96 offen offset:1024
buffer_store_dwordx4 v[152:155],v6,s[56:59],s96 offen offset:2048
buffer_store_dwordx4 v[156:159],v6,s[56:59],s96 offen offset:3072
s_mov_b32 s96,24576
buffer_store_dwordx4 v[160:163],v6,s[56:59],s96 offen
buffer_store_dwordx4 v[164:167],v6,s[56:59],s96 offen offset:1024
buffer_store_dwordx4 v[168:171],v6,s[56:59],s96 offen offset:2048
)A"
R"A(buffer_store_dwordx4 v[172:175],v6,s[56:59],s96 offen offset:3072
s_mov_b32 s96,28672
buffer_store_dwordx4 v[176:179],v6,s[56:59],s96 offen
buffer_store_dwordx4 v[180:183],v6,s[56:59],s96 offen offset:1024
buffer_store_dwordx4 v[184:187],v6,s[56:59],s96 offen offset:2048
buffer_store_dwordx4 v[188:191],v6,s[56:59],s96 offen offset:3072
s_waitcnt vmcnt(0)
v_writelane_b32 v173,s88,0
v_writelane_b32 v173,s89,1
v_writelane_b32 v173,s90,2
v_writelane_b32 v173,s91,3
v_writelane_b32 v173,s92,4
v_writelane_b32 v173,s93,5
v_writelane_b32 v173,s94,6
v_writelane_b32 v173,s95,7
s_mov_b32 s88,s98
s_mul_i32 s90,s41,307200
s_mov_b64 s[92:93],s[4:5]
s_add_u32 s92,s92,s90
s_addc_u32 s93,s93,0
s_and_b32 s93,s93,0xffff
s_mov_b32 s94,307200
s_mov_b32 s95,0x20000
s_mov_b64 s[96:97],s[10:11]
s_and_b32 s97,s97,0xffff
s_mov_b32 s98,5242880
s_mov_b32 s99,0x20000
v_bfe_u32 v171,v6,4,4
v_lshrrev_b32 v172,8,v6
v_lshlrev_b32 v164,8,v171
v_lshl_add_u32 v164,v172,4,v164
v_lshlrev_b32 v165,10,v172
v_lshl_add_u32 v165,v171,1,v165
v_add_u32 v166,4096,v165
v_lshlrev_b32 v168,16,v172
v_lshrrev_b32 v170,2,v171
v_lshl_add_u32 v168,v170,4,v168
v_and_b32 v170,3,v171
v_lshl_add_u32 v168,v170,1,v168
s_lshl_b32 s90,s42,1
s_add_u32 s90,s90,s88
s_lshl_b32 s90,s90,8
v_add_u32 v168,s90,v168
s_mov_b32 s89,0
L_sjb_%=:
s_add_u32 s90,s89,s70
s_add_u32 s90,s90,1
s_and_b32 s90,s90,3
s_lshl_b32 s91,s40,2
s_add_u32 s90,s90,s91
s_lshl_b32 s90,s90,4
v_bfe_u32 v171,v6,4,4
v_lshrrev_b32 v172,8,v6
v_add_u32 v167,s90,v171
v_mul_u32_u24 v167,1280,v167
v_lshl_add_u32 v167,v172,4,v167
s_lshl_b32 s90,s88,4
s_add_u32 s90,s90,s89
s_lshl_b32 s90,s90,12
v_add_u32 v169,s90,v6
v_mov_b32 v64,0
v_mov_b32 v65,0
v_mov_b32 v66,0
v_mov_b32 v67,0
v_mov_b32 v68,0
v_mov_b32 v69,0
v_mov_b32 v70,0
v_mov_b32 v71,0
v_mov_b32 v72,0
v_mov_b32 v73,0
v_mov_b32 v74,0
v_mov_b32 v75,0
v_mov_b32 v76,0
v_mov_b32 v77,0
v_mov_b32 v78,0
v_mov_b32 v79,0
v_mov_b32 v80,0
v_mov_b32 v81,0
v_mov_b32 v82,0
v_mov_b32 v83,0
v_mov_b32 v84,0
v_mov_b32 v85,0
v_mov_b32 v86,0
v_mov_b32 v87,0
v_mov_b32 v88,0
v_mov_b32 v89,0
v_mov_b32 v90,0
v_mov_b32 v91,0
v_mov_b32 v92,0
v_mov_b32 v93,0
v_mov_b32 v94,0
v_mov_b32 v95,0
s_mov_b32 s90,0
L_sq_%=:
s_lshl_b32 s91,s90,6
buffer_load_dwordx4 v[160:163],v167,s[92:95],s91 offen
s_lshl_b32 s91,s90,18
buffer_load_ushort v112,v168,s[96:99],s91 offen
buffer_load_ushort v120,v168,s[96:99],s91 offen offset:8
buffer_load_ushort v128,v168,s[96:99],s91 offen offset:64
buffer_load_ushort v136,v168,s[96:99],s91 offen offset:72
s_add_u32 s91,s91,8192
buffer_load_ushort v113,v168,s[96:99],s91 offen
buffer_load_ushort v121,v168,s[96:99],s91 offen offset:8
buffer_load_ushort v129,v168,s[96:99],s91 offen offset:64
buffer_load_ushort v137,v168,s[96:99],s91 offen offset:72
s_add_u32 s91,s91,8192
buffer_load_ushort v114,v168,s[96:99],s91 offen
buffer_load_ushort v122,v168,s[96:99],s91 offen offset:8
buffer_load_ushort v130,v168,s[96:99],s91 offen offset:64
buffer_load_ushort v138,v168,s[96:99],s91 offen offset:72
s_add_u32 s91,s91,8192
buffer_load_ushort v115,v168,s[96:99],s91 offen
buffer_load_ushort v123,v168,s[96:99],s91 offen offset:8
buffer_load_ushort v131,v168,s[96:99],s91 offen offset:64
buffer_load_ushort v139,v168,s[96:99],s91 offen offset:72
s_add_u32 s91,s91,8192
buffer_load_ushort v116,v168,s[96:99],s91 offen
buffer_load_ushort v124,v168,s[96:99],s91 offen offset:8
buffer_load_ushort v132,v168,s[96:99],s91 offen offset:64
buffer_load_ushort v140,v168,s[96:99],s91 offen offset:72
s_add_u32 s91,s91,8192
buffer_load_ushort v117,v168,s[96:99],s91 offen
buffer_load_ushort v125,v168,s[96:99],s91 offen offset:8
buffer_load_ushort v133,v168,s[96:99],s91 offen offset:64
buffer_load_ushort v141,v168,s[96:99],s91 offen offset:72
s_add_u32 s91,s91,8192
buffer_load_ushort v118,v168,s[96:99],s91 offen
buffer_load_ushort v126,v168,s[96:99],s91 offen offset:8
buffer_load_ushort v134,v168,s[96:99],s91 offen offset:64
buffer_load_ushort v142,v168,s[96:99],s91 offen offset:72
s_add_u32 s91,s91,8192
buffer_load_ushort v119,v168,s[96:99],s91 offen
buffer_load_ushort v127,v168,s[96:99],s91 offen offset:8
buffer_load_ushort v135,v168,s[96:99],s91 offen offset:64
buffer_load_ushort v143,v168,s[96:99],s91 offen offset:72
s_add_u32 s91,s91,8192
s_waitcnt vmcnt(0)
v_lshl_or_b32 v112,v113,16,v112
v_lshl_or_b32 v113,v115,16,v114
v_lshl_or_b32 v114,v117,16,v116
v_lshl_or_b32 v115,v119,16,v118
v_lshl_or_b32 v120,v121,16,v120
v_lshl_or_b32 v121,v123,16,v122
v_lshl_or_b32 v122,v125,16,v124
v_lshl_or_b32 v123,v127,16,v126
v_lshl_or_b32 v128,v129,16,v128
v_lshl_or_b32 v129,v131,16,v130
v_lshl_or_b32 v130,v133,16,v132
v_lshl_or_b32 v131,v135,16,v134
v_lshl_or_b32 v136,v137,16,v136
v_lshl_or_b32 v137,v139,16,v138
v_lshl_or_b32 v138,v141,16,v140
v_lshl_or_b32 v139,v143,16,v142
s_nop 1
v_mfma_f32_16x16x32_bf16 v[64:67],v[112:115],v[160:163],v[64:67]
v_mfma_f32_16x16x32_bf16 v[68:71],v[120:123],v[160:163],v[68:71]
v_mfma_f32_16x16x32_bf16 v[72:75],v[128:131],v[160:163],v[72:75]
v_mfma_f32_16x16x32_bf16 v[76:79],v[136:139],v[160:163],v[76:79]
s_nop 7
s_lshl_b32 s91,s90,18
buffer_load_ushort v112,v168,s[96:99],s91 offen offset:128
buffer_load_ushort v120,v168,s[96:99],s91 offen offset:136
buffer_load_ushort v128,v168,s[96:99],s91 offen offset:192
buffer_load_ushort v136,v168,s[96:99],s91 offen offset:200
s_add_u32 s91,s91,8192
buffer_load_ushort v113,v168,s[96:99],s91 offen offset:128
buffer_load_ushort v121,v168,s[96:99],s91 offen offset:136
buffer_load_ushort v129,v168,s[96:99],s91 offen offset:192
buffer_load_ushort v137,v168,s[96:99],s91 offen offset:200
s_add_u32 s91,s91,8192
buffer_load_ushort v114,v168,s[96:99],s91 offen offset:128
buffer_load_ushort v122,v168,s[96:99],s91 offen offset:136
buffer_load_ushort v130,v168,s[96:99],s91 offen offset:192
buffer_load_ushort v138,v168,s[96:99],s91 offen offset:200
s_add_u32 s91,s91,8192
buffer_load_ushort v115,v168,s[96:99],s91 offen offset:128
buffer_load_ushort v123,v168,s[96:99],s91 offen offset:136
buffer_load_ushort v131,v168,s[96:99],s91 offen offset:192
buffer_load_ushort v139,v168,s[96:99],s91 offen offset:200
s_add_u32 s91,s91,8192
buffer_load_ushort v116,v168,s[96:99],s91 offen offset:128
buffer_load_ushort v124,v168,s[96:99],s91 offen offset:136
buffer_load_ushort v132,v168,s[96:99],s91 offen offset:192
buffer_load_ushort v140,v168,s[96:99],s91 offen offset:200
s_add_u32 s91,s91,8192
buffer_load_ushort v117,v168,s[96:99],s91 offen offset:128
buffer_load_ushort v125,v168,s[96:99],s91 offen offset:136
buffer_load_ushort v133,v168,s[96:99],s91 offen offset:192
buffer_load_ushort v141,v168,s[96:99],s91 offen offset:200
s_add_u32 s91,s91,8192
buffer_load_ushort v118,v168,s[96:99],s91 offen offset:128
buffer_load_ushort v126,v168,s[96:99],s91 offen offset:136
buffer_load_ushort v134,v168,s[96:99],s91 offen offset:192
buffer_load_ushort v142,v168,s[96:99],s91 offen offset:200
s_add_u32 s91,s91,8192
buffer_load_ushort v119,v168,s[96:99],s91 offen offset:128
buffer_load_ushort v127,v168,s[96:99],s91 offen offset:136
buffer_load_ushort v135,v168,s[96:99],s91 offen offset:192
buffer_load_ushort v143,v168,s[96:99],s91 offen offset:200
s_add_u32 s91,s91,8192
s_waitcnt vmcnt(0)
v_lshl_or_b32 v112,v113,16,v112
v_lshl_or_b32 v113,v115,16,v114
v_lshl_or_b32 v114,v117,16,v116
v_lshl_or_b32 v115,v119,16,v118
v_lshl_or_b32 v120,v121,16,v120
v_lshl_or_b32 v121,v123,16,v122
v_lshl_or_b32 v122,v125,16,v124
v_lshl_or_b32 v123,v127,16,v126
v_lshl_or_b32 v128,v129,16,v128
v_lshl_or_b32 v129,v131,16,v130
v_lshl_or_b32 v130,v133,16,v132
v_lshl_or_b32 v131,v135,16,v134
v_lshl_or_b32 v136,v137,16,v136
v_lshl_or_b32 v137,v139,16,v138
v_lshl_or_b32 v138,v141,16,v140
v_lshl_or_b32 v139,v143,16,v142
s_nop 1
v_mfma_f32_16x16x32_bf16 v[80:83],v[112:115],v[160:163],v[80:83]
v_mfma_f32_16x16x32_bf16 v[84:87],v[120:123],v[160:163],v[84:87]
v_mfma_f32_16x16x32_bf16 v[88:91],v[128:131],v[160:163],v[88:91]
v_mfma_f32_16x16x32_bf16 v[92:95],v[136:139],v[160:163],v[92:95]
s_nop 7
s_add_u32 s90,s90,1
s_cmp_lg_u32 s90,20
s_cbranch_scc1 L_sq_%=
s_nop 7
v_mul_f32 v64,s61,v64
v_mul_f32 v65,s61,v65
v_mul_f32 v66,s61,v66
v_mul_f32 v67,s61,v67
v_mul_f32 v68,s61,v68
v_mul_f32 v69,s61,v69
v_mul_f32 v70,s61,v70
v_mul_f32 v71,s61,v71
v_mul_f32 v72,s61,v72
v_mul_f32 v73,s61,v73
v_mul_f32 v74,s61,v74
v_mul_f32 v75,s61,v75
v_mul_f32 v76,s61,v76
v_mul_f32 v77,s61,v77
v_mul_f32 v78,s61,v78
v_mul_f32 v79,s61,v79
v_mul_f32 v80,s61,v80
v_mul_f32 v81,s61,v81
v_mul_f32 v82,s61,v82
v_mul_f32 v83,s61,v83
v_mul_f32 v84,s61,v84
v_mul_f32 v85,s61,v85
v_mul_f32 v86,s61,v86
v_mul_f32 v87,s61,v87
v_mul_f32 v88,s61,v88
v_mul_f32 v89,s61,v89
v_mul_f32 v90,s61,v90
v_mul_f32 v91,s61,v91
v_mul_f32 v92,s61,v92
v_mul_f32 v93,s61,v93
v_mul_f32 v94,s61,v94
v_mul_f32 v95,s61,v95
v_cvt_pk_bf16_f32 v96,v64,v65
v_cvt_pk_bf16_f32 v97,v66,v67
v_cvt_pk_bf16_f32 v98,v68,v69
v_cvt_pk_bf16_f32 v99,v70,v71
v_cvt_pk_bf16_f32 v100,v72,v73
v_cvt_pk_bf16_f32 v101,v74,v75
v_cvt_pk_bf16_f32 v102,v76,v77
v_cvt_pk_bf16_f32 v103,v78,v79
v_cvt_pk_bf16_f32 v104,v80,v81
v_cvt_pk_bf16_f32 v105,v82,v83
v_cvt_pk_bf16_f32 v106,v84,v85
v_cvt_pk_bf16_f32 v107,v86,v87
v_cvt_pk_bf16_f32 v108,v88,v89
v_cvt_pk_bf16_f32 v109,v90,v91
v_cvt_pk_bf16_f32 v110,v92,v93
v_cvt_pk_bf16_f32 v111,v94,v95
v_mov_b32 v170,0xff800000
s_lshl_b32 s91,s88,18
s_mov_b32 s90,64
L_s1_%=:
buffer_load_dwordx4 v[112:115],v164,s[20:23],s91 offen
buffer_load_dwordx4 v[116:119],v164,s[20:23],s91 offen offset:64
buffer_load_dwordx4 v[120:123],v164,s[20:23],s91 offen offset:128
buffer_load_dwordx4 v[124:127],v164,s[20:23],s91 offen offset:192
s_waitcnt vmcnt(0)
v_mfma_f32_16x16x32_bf16 v[144:147],v[112:115],v[96:99],0
v_mfma_f32_16x16x32_bf16 v[144:147],v[116:119],v[100:103],v[144:147]
v_mfma_f32_16x16x32_bf16 v[144:147],v[120:123],v[104:107],v[144:147]
v_mfma_f32_16x16x32_bf16 v[144:147],v[124:127],v[108:111],v[144:147]
s_nop 7
v_max3_f32 v170,v170,v144,v145
v_max3_f32 v170,v170,v146,v147
s_add_u32 s91,s91,4096
s_sub_u32 s90,s90,1
s_cmp_lg_u32 s90,0
s_cbranch_scc1 L_s1_%=
v_mov_b32 v171,v170
s_nop 1
v_permlane32_swap_b32 v170,v171
v_max_f32 v170,v170,v171
v_mov_b32 v171,v170
s_nop 1
v_permlane16_swap_b32 v170,v171
v_max_f32 v170,v170,v171
v_sub_f32 v152,0,v170
v_mov_b32 v156,0
v_sub_f32 v153,0,v170
v_mov_b32 v157,0
v_sub_f32 v154,0,v170
v_mov_b32 v158,0
v_sub_f32 v155,0,v170
v_mov_b32 v159,0
v_mov_b32 v64,0
v_mov_b32 v65,0
v_mov_b32 v66,0
v_mov_b32 v67,0
v_mov_b32 v68,0
v_mov_b32 v69,0
v_mov_b32 v70,0
v_mov_b32 v71,0
v_mov_b32 v72,0
v_mov_b32 v73,0
v_mov_b32 v74,0
v_mov_b32 v75,0
v_mov_b32 v76,0
v_mov_b32 v77,0
v_mov_b32 v78,0
v_mov_b32 v79,0
v_mov_b32 v80,0
v_mov_b32 v81,0
v_mov_b32 v82,0
v_mov_b32 v83,0
v_mov_b32 v84,0
v_mov_b32 v85,0
v_mov_b32 v86,0
v_mov_b32 v87,0
v_mov_b32 v88,0
v_mov_b32 v89,0
v_mov_b32 v90,0
v_mov_b32 v91,0
v_mov_b32 v92,0
v_mov_b32 v93,0
v_mov_b32 v94,0
v_mov_b32 v95,0
s_lshl_b32 s91,s88,18
s_mov_b32 s90,32
v_add_u32 v172,4096,v164
L_s2_%=:
buffer_load_dwordx4 v[112:115],v164,s[20:23],s91 offen
buffer_load_dwordx4 v[116:119],v164,s[20:23],s91 offen offset:64
buffer_load_dwordx4 v[120:123],v164,s[20:23],s91 offen offset:128
buffer_load_dwordx4 v[124:127],v164,s[20:23],s91 offen offset:192
buffer_load_dwordx4 v[128:131],v172,s[20:23],s91 offen
buffer_load_dwordx4 v[132:135],v172,s[20:23],s91 offen offset:64
buffer_load_dwordx4 v[136:139],v172,s[20:23],s91 offen offset:128
buffer_load_dwordx4 v[140:143],v172,s[20:23],s91 offen offset:192
s_waitcnt vmcnt(0)
v_mfma_f32_16x16x32_bf16 v[144:147],v[112:115],v[96:99],v[152:155]
v_mfma_f32_16x16x32_bf16 v[144:147],v[116:119],v[100:103],v[144:147]
v_mfma_f32_16x16x32_bf16 v[144:147],v[120:123],v[104:107],v[144:147]
v_mfma_f32_16x16x32_bf16 v[144:147],v[124:127],v[108:111],v[144:147]
v_mfma_f32_16x16x32_bf16 v[148:151],v[128:131],v[96:99],v[152:155]
v_mfma_f32_16x16x32_bf16 v[148:151],v[132:135],v[100:103],v[148:151]
v_mfma_f32_16x16x32_bf16 v[148:151],v[136:139],v[104:107],v[148:151]
v_mfma_f32_16x16x32_bf16 v[148:151],v[140:143],v[108:111],v[148:151]
s_nop 7
v_exp_f32 v144,v144
v_exp_f32 v145,v145
v_exp_f32 v146,v146
)A"
R"A(v_exp_f32 v147,v147
v_exp_f32 v148,v148
v_exp_f32 v149,v149
v_exp_f32 v150,v150
v_exp_f32 v151,v151
s_nop 0
v_cvt_pk_bf16_f32 v144,v144,v145
v_cvt_pk_bf16_f32 v145,v146,v147
v_cvt_pk_bf16_f32 v146,v148,v149
v_cvt_pk_bf16_f32 v147,v150,v151
buffer_load_ushort v112,v165,s[24:27],s91 offen
buffer_load_ushort v113,v165,s[24:27],s91 offen offset:256
buffer_load_ushort v114,v165,s[24:27],s91 offen offset:512
buffer_load_ushort v115,v165,s[24:27],s91 offen offset:768
buffer_load_ushort v116,v166,s[24:27],s91 offen
buffer_load_ushort v117,v166,s[24:27],s91 offen offset:256
buffer_load_ushort v118,v166,s[24:27],s91 offen offset:512
buffer_load_ushort v119,v166,s[24:27],s91 offen offset:768
buffer_load_ushort v120,v165,s[24:27],s91 offen offset:32
buffer_load_ushort v121,v165,s[24:27],s91 offen offset:288
buffer_load_ushort v122,v165,s[24:27],s91 offen offset:544
buffer_load_ushort v123,v165,s[24:27],s91 offen offset:800
buffer_load_ushort v124,v166,s[24:27],s91 offen offset:32
buffer_load_ushort v125,v166,s[24:27],s91 offen offset:288
buffer_load_ushort v126,v166,s[24:27],s91 offen offset:544
buffer_load_ushort v127,v166,s[24:27],s91 offen offset:800
buffer_load_ushort v128,v165,s[24:27],s91 offen offset:64
buffer_load_ushort v129,v165,s[24:27],s91 offen offset:320
buffer_load_ushort v130,v165,s[24:27],s91 offen offset:576
buffer_load_ushort v131,v165,s[24:27],s91 offen offset:832
buffer_load_ushort v132,v166,s[24:27],s91 offen offset:64
buffer_load_ushort v133,v166,s[24:27],s91 offen offset:320
buffer_load_ushort v134,v166,s[24:27],s91 offen offset:576
buffer_load_ushort v135,v166,s[24:27],s91 offen offset:832
buffer_load_ushort v136,v165,s[24:27],s91 offen offset:96
buffer_load_ushort v137,v165,s[24:27],s91 offen offset:352
buffer_load_ushort v138,v165,s[24:27],s91 offen offset:608
buffer_load_ushort v139,v165,s[24:27],s91 offen offset:864
buffer_load_ushort v140,v166,s[24:27],s91 offen offset:96
buffer_load_ushort v141,v166,s[24:27],s91 offen offset:352
buffer_load_ushort v142,v166,s[24:27],s91 offen offset:608
buffer_load_ushort v143,v166,s[24:27],s91 offen offset:864
s_waitcnt vmcnt(0)
v_lshl_or_b32 v112,v113,16,v112
v_lshl_or_b32 v113,v115,16,v114
v_lshl_or_b32 v114,v117,16,v116
v_lshl_or_b32 v115,v119,16,v118
v_lshl_or_b32 v120,v121,16,v120
v_lshl_or_b32 v121,v123,16,v122
v_lshl_or_b32 v122,v125,16,v124
v_lshl_or_b32 v123,v127,16,v126
v_lshl_or_b32 v128,v129,16,v128
v_lshl_or_b32 v129,v131,16,v130
v_lshl_or_b32 v130,v133,16,v132
v_lshl_or_b32 v131,v135,16,v134
v_lshl_or_b32 v136,v137,16,v136
v_lshl_or_b32 v137,v139,16,v138
v_lshl_or_b32 v138,v141,16,v140
v_lshl_or_b32 v139,v143,16,v142
s_nop 1
v_mfma_f32_16x16x32_bf16 v[64:67],v[112:115],v[144:147],v[64:67]
v_mfma_f32_16x16x32_bf16 v[68:71],v[120:123],v[144:147],v[68:71]
v_mfma_f32_16x16x32_bf16 v[72:75],v[128:131],v[144:147],v[72:75]
v_mfma_f32_16x16x32_bf16 v[76:79],v[136:139],v[144:147],v[76:79]
s_nop 7
buffer_load_ushort v112,v165,s[24:27],s91 offen offset:128
buffer_load_ushort v113,v165,s[24:27],s91 offen offset:384
buffer_load_ushort v114,v165,s[24:27],s91 offen offset:640
buffer_load_ushort v115,v165,s[24:27],s91 offen offset:896
buffer_load_ushort v116,v166,s[24:27],s91 offen offset:128
buffer_load_ushort v117,v166,s[24:27],s91 offen offset:384
buffer_load_ushort v118,v166,s[24:27],s91 offen offset:640
buffer_load_ushort v119,v166,s[24:27],s91 offen offset:896
buffer_load_ushort v120,v165,s[24:27],s91 offen offset:160
buffer_load_ushort v121,v165,s[24:27],s91 offen offset:416
buffer_load_ushort v122,v165,s[24:27],s91 offen offset:672
buffer_load_ushort v123,v165,s[24:27],s91 offen offset:928
buffer_load_ushort v124,v166,s[24:27],s91 offen offset:160
buffer_load_ushort v125,v166,s[24:27],s91 offen offset:416
buffer_load_ushort v126,v166,s[24:27],s91 offen offset:672
buffer_load_ushort v127,v166,s[24:27],s91 offen offset:928
buffer_load_ushort v128,v165,s[24:27],s91 offen offset:192
buffer_load_ushort v129,v165,s[24:27],s91 offen offset:448
buffer_load_ushort v130,v165,s[24:27],s91 offen offset:704
buffer_load_ushort v131,v165,s[24:27],s91 offen offset:960
buffer_load_ushort v132,v166,s[24:27],s91 offen offset:192
buffer_load_ushort v133,v166,s[24:27],s91 offen offset:448
buffer_load_ushort v134,v166,s[24:27],s91 offen offset:704
buffer_load_ushort v135,v166,s[24:27],s91 offen offset:960
buffer_load_ushort v136,v165,s[24:27],s91 offen offset:224
buffer_load_ushort v137,v165,s[24:27],s91 offen offset:480
buffer_load_ushort v138,v165,s[24:27],s91 offen offset:736
buffer_load_ushort v139,v165,s[24:27],s91 offen offset:992
buffer_load_ushort v140,v166,s[24:27],s91 offen offset:224
buffer_load_ushort v141,v166,s[24:27],s91 offen offset:480
buffer_load_ushort v142,v166,s[24:27],s91 offen offset:736
buffer_load_ushort v143,v166,s[24:27],s91 offen offset:992
s_waitcnt vmcnt(0)
v_lshl_or_b32 v112,v113,16,v112
v_lshl_or_b32 v113,v115,16,v114
v_lshl_or_b32 v114,v117,16,v116
v_lshl_or_b32 v115,v119,16,v118
v_lshl_or_b32 v120,v121,16,v120
v_lshl_or_b32 v121,v123,16,v122
v_lshl_or_b32 v122,v125,16,v124
v_lshl_or_b32 v123,v127,16,v126
v_lshl_or_b32 v128,v129,16,v128
v_lshl_or_b32 v129,v131,16,v130
v_lshl_or_b32 v130,v133,16,v132
v_lshl_or_b32 v131,v135,16,v134
v_lshl_or_b32 v136,v137,16,v136
v_lshl_or_b32 v137,v139,16,v138
v_lshl_or_b32 v138,v141,16,v140
v_lshl_or_b32 v139,v143,16,v142
s_nop 1
v_mfma_f32_16x16x32_bf16 v[80:83],v[112:115],v[144:147],v[80:83]
v_mfma_f32_16x16x32_bf16 v[84:87],v[120:123],v[144:147],v[84:87]
v_mfma_f32_16x16x32_bf16 v[88:91],v[128:131],v[144:147],v[88:91]
v_mfma_f32_16x16x32_bf16 v[92:95],v[136:139],v[144:147],v[92:95]
s_nop 7
v_mfma_f32_16x16x32_bf16 v[156:159],v[28:31],v[144:147],v[156:159]
s_nop 7
s_add_u32 s91,s91,8192
s_sub_u32 s90,s90,1
s_cmp_lg_u32 s90,0
s_cbranch_scc1 L_s2_%=
v_rcp_f32 v170,v156
s_nop 0
v_mul_f32 v64,v170,v64
v_mul_f32 v65,v170,v65
v_mul_f32 v66,v170,v66
v_mul_f32 v67,v170,v67
v_mul_f32 v68,v170,v68
v_mul_f32 v69,v170,v69
v_mul_f32 v70,v170,v70
v_mul_f32 v71,v170,v71
v_mul_f32 v72,v170,v72
v_mul_f32 v73,v170,v73
v_mul_f32 v74,v170,v74
v_mul_f32 v75,v170,v75
v_mul_f32 v76,v170,v76
v_mul_f32 v77,v170,v77
v_mul_f32 v78,v170,v78
v_mul_f32 v79,v170,v79
v_mul_f32 v80,v170,v80
v_mul_f32 v81,v170,v81
v_mul_f32 v82,v170,v82
v_mul_f32 v83,v170,v83
v_mul_f32 v84,v170,v84
v_mul_f32 v85,v170,v85
v_mul_f32 v86,v170,v86
v_mul_f32 v87,v170,v87
v_mul_f32 v88,v170,v88
v_mul_f32 v89,v170,v89
v_mul_f32 v90,v170,v90
v_mul_f32 v91,v170,v91
v_mul_f32 v92,v170,v92
v_mul_f32 v93,v170,v93
v_mul_f32 v94,v170,v94
v_mul_f32 v95,v170,v95
v_cvt_pk_bf16_f32 v96,v64,v65
v_cvt_pk_bf16_f32 v97,v66,v67
v_cvt_pk_bf16_f32 v98,v68,v69
v_cvt_pk_bf16_f32 v99,v70,v71
v_cvt_pk_bf16_f32 v100,v72,v73
v_cvt_pk_bf16_f32 v101,v74,v75
v_cvt_pk_bf16_f32 v102,v76,v77
v_cvt_pk_bf16_f32 v103,v78,v79
v_cvt_pk_bf16_f32 v104,v80,v81
v_cvt_pk_bf16_f32 v105,v82,v83
v_cvt_pk_bf16_f32 v106,v84,v85
v_cvt_pk_bf16_f32 v107,v86,v87
v_cvt_pk_bf16_f32 v108,v88,v89
v_cvt_pk_bf16_f32 v109,v90,v91
v_cvt_pk_bf16_f32 v110,v92,v93
v_cvt_pk_bf16_f32 v111,v94,v95
buffer_store_dwordx4 v[96:99],v169,s[36:39],s55 offen sc1
buffer_store_dwordx4 v[100:103],v169,s[36:39],s55 offen offset:1024 sc1
buffer_store_dwordx4 v[104:107],v169,s[36:39],s55 offen offset:2048 sc1
buffer_store_dwordx4 v[108:111],v169,s[36:39],s55 offen offset:3072 sc1
s_waitcnt vmcnt(0)
s_add_u32 s89,s89,1
s_cmp_lg_u32 s89,4
s_cbranch_scc1 L_sjb_%=
s_mov_b32 s98,s88
v_readlane_b32 s88,v173,0
v_readlane_b32 s89,v173,1
v_readlane_b32 s90,v173,2
v_readlane_b32 s91,v173,3
v_readlane_b32 s92,v173,4
v_readlane_b32 s93,v173,5
v_readlane_b32 s94,v173,6
v_readlane_b32 s95,v173,7
s_nop 4
s_mov_b32 s96,0
buffer_load_dwordx4 v[64:67],v6,s[56:59],s96 offen sc1
buffer_load_dwordx4 v[68:71],v6,s[56:59],s96 offen offset:1024 sc1
buffer_load_dwordx4 v[72:75],v6,s[56:59],s96 offen offset:2048 sc1
buffer_load_dwordx4 v[76:79],v6,s[56:59],s96 offen offset:3072 sc1
s_mov_b32 s96,4096
buffer_load_dwordx4 v[80:83],v6,s[56:59],s96 offen sc1
buffer_load_dwordx4 v[84:87],v6,s[56:59],s96 offen offset:1024 sc1
buffer_load_dwordx4 v[88:91],v6,s[56:59],s96 offen offset:2048 sc1
buffer_load_dwordx4 v[92:95],v6,s[56:59],s96 offen offset:3072 sc1
s_mov_b32 s96,8192
buffer_load_dwordx4 v[96:99],v6,s[56:59],s96 offen sc1
buffer_load_dwordx4 v[100:103],v6,s[56:59],s96 offen offset:1024 sc1
buffer_load_dwordx4 v[104:107],v6,s[56:59],s96 offen offset:2048 sc1
buffer_load_dwordx4 v[108:111],v6,s[56:59],s96 offen offset:3072 sc1
s_mov_b32 s96,12288
buffer_load_dwordx4 v[112:115],v6,s[56:59],s96 offen sc1
buffer_load_dwordx4 v[116:119],v6,s[56:59],s96 offen offset:1024 sc1
buffer_load_dwordx4 v[120:123],v6,s[56:59],s96 offen offset:2048 sc1
buffer_load_dwordx4 v[124:127],v6,s[56:59],s96 offen offset:3072 sc1
s_mov_b32 s96,16384
buffer_load_dwordx4 v[128:131],v6,s[56:59],s96 offen sc1
buffer_load_dwordx4 v[132:135],v6,s[56:59],s96 offen offset:1024 sc1
buffer_load_dwordx4 v[136:139],v6,s[56:59],s96 offen offset:2048 sc1
buffer_load_dwordx4 v[140:143],v6,s[56:59],s96 offen offset:3072 sc1
s_mov_b32 s96,20480
buffer_load_dwordx4 v[144:147],v6,s[56:59],s96 offen sc1
buffer_load_dwordx4 v[148:151],v6,s[56:59],s96 offen offset:1024 sc1
buffer_load_dwordx4 v[152:155],v6,s[56:59],s96 offen offset:2048 sc1
buffer_load_dwordx4 v[156:159],v6,s[56:59],s96 offen offset:3072 sc1
s_mov_b32 s96,24576
buffer_load_dwordx4 v[160:163],v6,s[56:59],s96 offen sc1
buffer_load_dwordx4 v[164:167],v6,s[56:59],s96 offen offset:1024 sc1
buffer_load_dwordx4 v[168:171],v6,s[56:59],s96 offen offset:2048 sc1
buffer_load_dwordx4 v[172:175],v6,s[56:59],s96 offen offset:3072 sc1
s_mov_b32 s96,28672
buffer_load_dwordx4 v[176:179],v6,s[56:59],s96 offen sc1
buffer_load_dwordx4 v[180:183],v6,s[56:59],s96 offen offset:1024 sc1
buffer_load_dwordx4 v[184:187],v6,s[56:59],s96 offen offset:2048 sc1
buffer_load_dwordx4 v[188:191],v6,s[56:59],s96 offen offset:3072 sc1
s_waitcnt vmcnt(0)
s_cmp_lg_u32 s98,0
s_cbranch_scc1 L_sr1_%=
buffer_load_dwordx4 v[64:67],v6,s[36:39],s84 offen sc1
buffer_load_dwordx4 v[68:71],v6,s[36:39],s84 offen offset:1024 sc1
buffer_load_dwordx4 v[72:75],v6,s[36:39],s84 offen offset:2048 sc1
buffer_load_dwordx4 v[76:79],v6,s[36:39],s84 offen offset:3072 sc1
buffer_load_dwordx4 v[80:83],v6,s[36:39],s85 offen sc1
buffer_load_dwordx4 v[84:87],v6,s[36:39],s85 offen offset:1024 sc1
buffer_load_dwordx4 v[88:91],v6,s[36:39],s85 offen offset:2048 sc1
buffer_load_dwordx4 v[92:95],v6,s[36:39],s85 offen offset:3072 sc1
buffer_load_dwordx4 v[96:99],v6,s[36:39],s86 offen sc1
buffer_load_dwordx4 v[100:103],v6,s[36:39],s86 offen offset:1024 sc1
buffer_load_dwordx4 v[104:107],v6,s[36:39],s86 offen offset:2048 sc1
buffer_load_dwordx4 v[108:111],v6,s[36:39],s86 offen offset:3072 sc1
buffer_load_dwordx4 v[112:115],v6,s[36:39],s87 offen sc1
buffer_load_dwordx4 v[116:119],v6,s[36:39],s87 offen offset:1024 sc1
buffer_load_dwordx4 v[120:123],v6,s[36:39],s87 offen offset:2048 sc1
buffer_load_dwordx4 v[124:127],v6,s[36:39],s87 offen offset:3072 sc1
s_waitcnt vmcnt(0)
s_nop 7
s_branch L_sret0_%=
L_sr1_%=:
buffer_load_dwordx4 v[132:135],v6,s[36:39],s84 offen sc1
buffer_load_dwordx4 v[140:143],v6,s[36:39],s84 offen offset:1024 sc1
buffer_load_dwordx4 v[148:151],v6,s[36:39],s84 offen offset:2048 sc1
buffer_load_dwordx4 v[156:159],v6,s[36:39],s84 offen offset:3072 sc1
buffer_load_dwordx4 v[164:167],v6,s[36:39],s85 offen sc1
buffer_load_dwordx4 v[172:175],v6,s[36:39],s85 offen offset:1024 sc1
buffer_load_dwordx4 v[180:183],v6,s[36:39],s85 offen offset:2048 sc1
buffer_load_dwordx4 v[188:191],v6,s[36:39],s85 offen offset:3072 sc1
buffer_load_dwordx4 v[196:199],v6,s[36:39],s86 offen sc1
buffer_load_dwordx4 v[204:207],v6,s[36:39],s86 offen offset:1024 sc1
buffer_load_dwordx4 v[212:215],v6,s[36:39],s86 offen offset:2048 sc1
buffer_load_dwordx4 v[220:223],v6,s[36:39],s86 offen offset:3072 sc1
buffer_load_dwordx4 v[228:231],v6,s[36:39],s87 offen sc1
buffer_load_dwordx4 v[236:239],v6,s[36:39],s87 offen offset:1024 sc1
buffer_load_dwordx4 v[244:247],v6,s[36:39],s87 offen offset:2048 sc1
buffer_load_dwordx4 v[252:255],v6,s[36:39],s87 offen offset:3072 sc1
)A"
R"A(s_waitcnt vmcnt(0)
s_nop 7
s_branch L_sret1_%=
L_end_%=:
)A"
    :: "s"(karg), "s"(blk), "s"(wid)
    : "v0", "v1", "v2", "v3", "v4", "v5", "v6", "v7", "v8", "v9", "v10", "v11", "v12", "v13", "v14", "v15", "v16", "v17", "v18", "v19", "v20", "v21", "v22", "v23", "v24", "v25", "v26", "v27", "v28", "v29", "v30", "v31", "v32", "v33", "v34", "v35", "v36", "v37", "v38", "v39", "v40", "v41", "v42", "v43", "v44", "v45", "v46", "v47", "v48", "v49", "v50", "v51", "v52", "v53", "v54", "v55", "v56", "v57", "v58", "v59", "v60", "v61", "v62", "v63", "v64", "v65", "v66", "v67", "v68", "v69", "v70", "v71", "v72", "v73", "v74", "v75", "v76", "v77", "v78", "v79", "v80", "v81", "v82", "v83", "v84", "v85", "v86", "v87", "v88", "v89", "v90", "v91", "v92", "v93", "v94", "v95", "v96", "v97", "v98", "v99", "v100", "v101", "v102", "v103", "v104", "v105", "v106", "v107", "v108", "v109", "v110", "v111", "v112", "v113", "v114", "v115", "v116", "v117", "v118", "v119", "v120", "v121", "v122", "v123", "v124", "v125", "v126", "v127", "v128", "v129", "v130", "v131", "v132", "v133", "v134", "v135", "v136", "v137", "v138", "v139", "v140", "v141", "v142", "v143", "v144", "v145", "v146", "v147", "v148", "v149", "v150", "v151", "v152", "v153", "v154", "v155", "v156", "v157", "v158", "v159", "v160", "v161", "v162", "v163", "v164", "v165", "v166", "v167", "v168", "v169", "v170", "v171", "v172", "v173", "v174", "v175", "v176", "v177", "v178", "v179", "v180", "v181", "v182", "v183", "v184", "v185", "v186", "v187", "v188", "v189", "v190", "v191", "v192", "v193", "v194", "v195", "v196", "v197", "v198", "v199", "v200", "v201", "v202", "v203", "v204", "v205", "v206", "v207", "v208", "v209", "v210", "v211", "v212", "v213", "v214", "v215", "v216", "v217", "v218", "v219", "v220", "v221", "v222", "v223", "v224", "v225", "v226", "v227", "v228", "v229", "v230", "v231", "v232", "v233", "v234", "v235", "v236", "v237", "v238", "v239", "v240", "v241", "v242", "v243", "v244", "v245", "v246", "v247", "v248", "v249", "v250", "v251", "v252", "v253", "v254", "v255", "a0", "a1", "a2", "a3", "a4", "a5", "a6", "a7", "a8", "a9", "a10", "a11", "a12", "a13", "a14", "a15", "a16", "a17", "a18", "a19", "a20", "a21", "a22", "a23", "a24", "a25", "a26", "a27", "a28", "a29", "a30", "a31", "a32", "a33", "a34", "a35", "a36", "a37", "a38", "a39", "a40", "a41", "a42", "a43", "a44", "a45", "a46", "a47", "a48", "a49", "a50", "a51", "a52", "a53", "a54", "a55", "a56", "a57", "a58", "a59", "a60", "a61", "a62", "a63", "a64", "a65", "a66", "a67", "a68", "a69", "a70", "a71", "a72", "a73", "a74", "a75", "a76", "a77", "a78", "a79", "a80", "a81", "a82", "a83", "a84", "a85", "a86", "a87", "a88", "a89", "a90", "a91", "a92", "a93", "a94", "a95", "a96", "a97", "a98", "a99", "a100", "a101", "a102", "a103", "a104", "a105", "a106", "a107", "a108", "a109", "a110", "a111", "a112", "a113", "a114", "a115", "a116", "a117", "a118", "a119", "a120", "a121", "a122", "a123", "a124", "a125", "a126", "a127", "a128", "a129", "a130", "a131", "a132", "a133", "a134", "a135", "a136", "a137", "a138", "a139", "a140", "a141", "a142", "a143", "a144", "a145", "a146", "a147", "a148", "a149", "a150", "a151", "a152", "a153", "a154", "a155", "a156", "a157", "a158", "a159", "a160", "a161", "a162", "a163", "a164", "a165", "a166", "a167", "a168", "a169", "a170", "a171", "a172", "a173", "a174", "a175", "a176", "a177", "a178", "a179", "a180", "a181", "a182", "a183", "a184", "a185", "a186", "a187", "a188", "a189", "a190", "a191", "a192", "a193", "a194", "a195", "a196", "a197", "a198", "a199", "a200", "a201", "a202", "a203", "a204", "a205", "a206", "a207", "a208", "a209", "a210", "a211", "a212", "a213", "a214", "a215", "a216", "a217", "a218", "a219", "a220", "a221", "a222", "a223", "a224", "a225", "a226", "a227", "a228", "a229", "a230", "a231", "a232", "a233", "a234", "a235", "a236", "a237", "a238", "a239", "a240", "a241", "a242", "a243", "a244", "a245", "a246", "a247", "a248", "a249", "a250", "a251", "a252", "a253", "a254", "a255", "s4", "s5", "s6", "s7", "s8", "s9", "s10", "s11", "s12", "s13", "s14", "s15", "s16", "s17", "s18", "s19", "s20", "s21", "s22", "s23", "s24", "s25", "s26", "s27", "s28", "s29", "s30", "s31", "s32", "s33", "s34", "s35", "s36", "s37", "s38", "s39", "s40", "s41", "s42", "s43", "s44", "s45", "s46", "s47", "s48", "s49", "s50", "s51", "s52", "s53", "s54", "s55", "s56", "s57", "s58", "s59", "s60", "s61", "s62", "s63", "s64", "s65", "s66", "s67", "s68", "s69", "s70", "s71", "s72", "s73", "s74", "s75", "s76", "s77", "s78", "s79", "s80", "s81", "s82", "s83", "s84", "s85", "s86", "s87", "s88", "s89", "s90", "s91", "s92", "s93", "s94", "s95", "s96", "s97", "s98", "s99", "s100", "s101", "vcc", "scc", "memory");
#endif
}

extern "C" void kernel_launch(void* const* d_in, const int* in_sizes, int n_in,
                              void* d_out, int out_size, void* d_ws, size_t ws_size,
                              hipStream_t stream) {
  (void)hipFuncSetAttribute((const void*)qattn_kernel, hipFuncAttributeMaxDynamicSharedMemorySize, LDS_ATT);
  qattn_kernel<<<256, 256, LDS_ATT, stream>>>(d_in[0], d_in[1], d_in[2], d_in[3], d_ws, d_in[4], d_out);
}
```

```cpp
#include <hip/hip_runtime.h>
#include <hip/hip_bf16.h>

constexpr int LDS_ATT = 163840;

__global__ __launch_bounds__(256, 1) void qattn_kernel(const void* __restrict__ x, const void* __restrict__ kc, const void* __restrict__ vc,
                                                       const void* __restrict__ wq, void* __restrict__ ws, const void* __restrict__ wo,
                                                       void* __restrict__ out) {
#if defined(__HIP_DEVICE_COMPILE__)
  const auto karg = __builtin_amdgcn_kernarg_segment_ptr();
  const unsigned blk = blockIdx.x;
  const unsigned wid = __builtin_amdgcn_readfirstlane(threadIdx.x >> 6);
  asm volatile(
R"A(s_load_dwordx8 s[4:11],%0,0x0
s_load_dwordx4 s[12:15],%0,0x20
s_load_dwordx2 s[16:17],%0,0x30
s_mov_b32 s40,%2
s_mov_b32 s43,%1
s_lshr_b32 s45,s43,3
v_mbcnt_lo_u32_b32 v12,-1,0
v_mbcnt_hi_u32_b32 v12,-1,v12
s_getpc_b64 s[88:89]
L_pc_%=:
s_and_b32 s88,s88,0xffffff80
s_and_b32 s89,s89,0xffff
s_mov_b32 s90,L_fast_end_%=-L_pc_%=
s_mov_b32 s91,0x20000
s_lshl_b32 s44,s45,2
s_add_u32 s44,s44,s40
s_mul_i32 s44,s44,768
v_lshlrev_b32 v67,7,v12
s_mov_b64 exec,63
buffer_load_dword v19,v67,s[88:91],s44 offen
s_mov_b64 exec,-1
s_and_b32 s70,s43,3
s_and_b32 s71,s45,3
s_lshl_b32 s42,s70,2
s_add_u32 s42,s42,s71
s_and_b32 s41,s43,4
s_lshl_b32 s41,s41,1
s_lshr_b32 s46,s45,2
s_add_u32 s41,s41,s46
s_lshl_b32 s56,s42,9
s_mov_b32 s57,0
s_lshl_b32 s59,s40,13
s_add_u32 s58,s59,65536
v_and_b32 v64,3,v12
v_bfe_u32 v65,v12,2,2
v_lshrrev_b32 v66,4,v12
v_lshrrev_b32 v75,3,v12
s_lshl_b32 s44,s40,6
v_add_u32 v75,s44,v75
v_mul_u32_u24 v75,1280,v75
v_and_b32 v76,7,v12
s_add_u32 s84,s70,1
s_and_b32 s84,s84,3
s_mul_i32 s84,s84,20480
s_add_u32 s84,s84,4096
s_add_u32 s85,s70,2
s_and_b32 s85,s85,3
s_mul_i32 s85,s85,20480
s_add_u32 s85,s85,2048
s_add_u32 s86,s70,3
s_and_b32 s86,s86,3
s_mul_i32 s86,s86,20480
s_add_u32 s86,s86,4096
s_add_u32 s87,s70,4
s_and_b32 s87,s87,3
s_mul_i32 s87,s87,20480
s_add_u32 s87,s87,2048
v_or_b32 v77,0,v66
v_xor_b32 v77,v77,v76
v_lshl_add_u32 v77,v77,4,v75
v_add_u32 v48,s84,v77
v_add_u32 v50,s85,v77
v_add_u32 v52,s86,v77
v_add_u32 v54,s87,v77
v_or_b32 v77,4,v66
v_xor_b32 v77,v77,v76
v_lshl_add_u32 v77,v77,4,v75
v_add_u32 v77,9216,v77
v_add_u32 v49,s84,v77
v_add_u32 v51,s85,v77
v_add_u32 v53,s86,v77
v_add_u32 v55,s87,v77
s_lshl_b32 s44,s40,17
v_lshlrev_b32 v75,13,v65
v_lshl_add_u32 v75,v66,6,v75
v_lshl_add_u32 v75,v64,4,v75
v_add_u32 v75,s44,v75
v_add_u32 v56,0,v75
v_add_u32 v57,256,v75
v_add_u32 v58,32768,v75
v_add_u32 v59,33024,v75
v_add_u32 v60,65536,v75
v_add_u32 v61,65792,v75
v_add_u32 v62,98304,v75
v_add_u32 v63,98560,v75
s_mul_i32 s44,s41,307200
s_waitcnt lgkmcnt(0)
s_mov_b64 s[28:29],s[4:5]
s_add_u32 s28,s28,s44
s_addc_u32 s29,s29,0
s_sub_u32 s28,s28,4096
s_subb_u32 s29,s29,0
s_and_b32 s29,s29,0xffff
s_mov_b32 s30,311296
s_mov_b32 s31,0x20000
s_mov_b64 s[32:33],s[10:11]
s_and_b32 s33,s33,0xffff
s_mov_b32 s34,5242880
s_mov_b32 s35,0x20000
s_add_u32 m0,s58,1024
s_nop 0
buffer_load_dwordx4 v57,s[32:35],s56 offen lds
s_add_u32 m0,s58,3072
s_nop 0
buffer_load_dwordx4 v59,s[32:35],s56 offen lds
s_add_u32 m0,s58,5120
s_nop 0
buffer_load_dwordx4 v61,s[32:35],s56 offen lds
s_add_u32 m0,s58,7168
s_nop 0
buffer_load_dwordx4 v63,s[32:35],s56 offen lds
s_add_u32 m0,s59,0
s_nop 0
buffer_load_dwordx4 v48,s[28:31],s57 offen lds
buffer_load_dwordx4 v49,s[28:31],s57 offen offset:1024 lds
buffer_load_dwordx4 v50,s[28:31],s57 offen offset:2048 lds
buffer_load_dwordx4 v51,s[28:31],s57 offen offset:3072 lds
s_add_u32 m0,s59,4096
s_nop 0
buffer_load_dwordx4 v52,s[28:31],s57 offen lds
buffer_load_dwordx4 v53,s[28:31],s57 offen offset:1024 lds
buffer_load_dwordx4 v54,s[28:31],s57 offen offset:2048 lds
buffer_load_dwordx4 v55,s[28:31],s57 offen offset:3072 lds
s_add_u32 s57,s57,128
s_xor_b32 s59,s59,131072
s_add_u32 m0,s58,0
s_nop 0
buffer_load_dwordx4 v56,s[32:35],s56 offen lds
s_add_u32 m0,s58,2048
s_nop 0
buffer_load_dwordx4 v58,s[32:35],s56 offen lds
s_add_u32 m0,s58,4096
s_nop 0
buffer_load_dwordx4 v60,s[32:35],s56 offen lds
s_add_u32 m0,s58,6144
s_nop 0
buffer_load_dwordx4 v62,s[32:35],s56 offen lds
s_add_u32 s56,s56,524288
s_add_u32 s58,s58,32768
s_cmp_ge_u32 s58,131072
s_cselect_b32 s62,98304,0
s_sub_u32 s58,s58,s62
s_add_u32 m0,s58,0
s_nop 0
buffer_load_dwordx4 v56,s[32:35],s56 offen lds
s_add_u32 m0,s58,1024
s_nop 0
buffer_load_dwordx4 v57,s[32:35],s56 offen lds
s_add_u32 m0,s58,2048
s_nop 0
buffer_load_dwordx4 v58,s[32:35],s56 offen lds
s_add_u32 m0,s58,3072
s_nop 0
buffer_load_dwordx4 v59,s[32:35],s56 offen lds
s_add_u32 m0,s58,4096
s_nop 0
buffer_load_dwordx4 v60,s[32:35],s56 offen lds
s_add_u32 m0,s58,5120
s_nop 0
buffer_load_dwordx4 v61,s[32:35],s56 offen lds
s_add_u32 m0,s58,6144
s_nop 0
buffer_load_dwordx4 v62,s[32:35],s56 offen lds
s_add_u32 m0,s58,7168
s_nop 0
buffer_load_dwordx4 v63,s[32:35],s56 offen lds
s_add_u32 s56,s56,524288
s_add_u32 s58,s58,32768
s_cmp_ge_u32 s58,131072
s_cselect_b32 s62,98304,0
s_sub_u32 s58,s58,s62
s_mov_b32 s60,65536
v_lshlrev_b32 v6,4,v12
v_lshlrev_b32 v72,12,v66
v_lshl_add_u32 v72,v65,6,v72
v_lshl_add_u32 v23,v64,4,v72
v_add_u32 v22,65536,v23
v_and_b32 v74,15,v12
v_lshrrev_b32 v76,1,v74
v_lshlrev_b32 v75,7,v74
s_lshl_b32 s44,s40,13
v_add_u32 v75,s44,v75
v_or_b32 v77,0,v66
v_xor_b32 v77,v77,v76
v_lshl_add_u32 v20,v77,4,v75
v_or_b32 v77,4,v66
v_xor_b32 v77,v77,v76
v_lshl_add_u32 v21,v77,4,v75
s_waitcnt vmcnt(12)
s_barrier
ds_read_b128 a[192:195],v20
ds_read_b64_tr_b16 a[128:129],v22 offset:1024
ds_read_b64_tr_b16 a[130:131],v22 offset:3072
ds_read_b128 a[196:199],v21
ds_read_b64_tr_b16 a[132:133],v22 offset:17408
ds_read_b64_tr_b16 a[134:135],v22 offset:19456
ds_read_b128 a[204:207],v21 offset:2048
ds_read_b128 a[200:203],v20 offset:2048
ds_read_b128 a[208:211],v20 offset:4096
ds_read_b128 a[212:215],v21 offset:4096
ds_read_b128 a[220:223],v21 offset:6144
ds_read_b128 a[216:219],v20 offset:6144
ds_read_b64_tr_b16 a[136:137],v22 offset:1032
ds_read_b64_tr_b16 a[138:139],v22 offset:3080
s_waitcnt lgkmcnt(11)
v_mfma_f32_16x16x32_bf16 v[128:131],a[128:131],a[192:195],0
s_add_u32 m0,s59,0
ds_read_b64_tr_b16 a[140:141],v22 offset:17416
ds_read_b64_tr_b16 a[142:143],v22 offset:19464
s_waitcnt lgkmcnt(10)
v_mfma_f32_16x16x32_bf16 v[128:131],a[132:135],a[196:199],v[128:131]
s_waitcnt lgkmcnt(9)
v_mfma_f32_16x16x32_bf16 v[136:139],a[132:135],a[204:207],0
s_waitcnt lgkmcnt(8)
v_mfma_f32_16x16x32_bf16 v[136:139],a[128:131],a[200:203],v[136:139]
s_waitcnt lgkmcnt(7)
v_mfma_f32_16x16x32_bf16 v[144:147],a[128:131],a[208:211],0
s_waitcnt lgkmcnt(6)
buffer_load_dwordx4 v48,s[28:31],s57 offen lds
v_mfma_f32_16x16x32_bf16 v[144:147],a[132:135],a[212:215],v[144:147]
s_waitcnt lgkmcnt(5)
v_mfma_f32_16x16x32_bf16 v[152:155],a[132:135],a[220:223],0
s_waitcnt lgkmcnt(4)
v_mfma_f32_16x16x32_bf16 v[152:155],a[128:131],a[216:219],v[152:155]
s_waitcnt lgkmcnt(2)
ds_read_b64_tr_b16 a[144:145],v22 offset:1280
ds_read_b64_tr_b16 a[146:147],v22 offset:3328
v_mfma_f32_16x16x32_bf16 v[132:135],a[136:139],a[192:195],0
s_waitcnt lgkmcnt(2)
ds_read_b64_tr_b16 a[148:149],v22 offset:17664
ds_read_b64_tr_b16 a[150:151],v22 offset:19712
buffer_load_dwordx4 v49,s[28:31],s57 offen offset:1024 lds
v_mfma_f32_16x16x32_bf16 v[132:135],a[140:143],a[196:199],v[132:135]
v_mfma_f32_16x16x32_bf16 v[140:143],a[140:143],a[204:207],0
v_mfma_f32_16x16x32_bf16 v[140:143],a[136:139],a[200:203],v[140:143]
v_mfma_f32_16x16x32_bf16 v[148:151],a[136:139],a[208:211],0
buffer_load_dwordx4 v50,s[28:31],s57 offen offset:2048 lds
v_mfma_f32_16x16x32_bf16 v[148:151],a[140:143],a[212:215],v[148:151]
v_mfma_f32_16x16x32_bf16 v[156:159],a[140:143],a[220:223],0
v_mfma_f32_16x16x32_bf16 v[156:159],a[136:139],a[216:219],v[156:159]
s_waitcnt lgkmcnt(2)
ds_read_b64_tr_b16 a[152:153],v22 offset:1288
ds_read_b64_tr_b16 a[154:155],v22 offset:3336
v_mfma_f32_16x16x32_bf16 v[160:163],a[144:147],a[192:195],0
s_waitcnt lgkmcnt(2)
buffer_load_dwordx4 v51,s[28:31],s57 offen offset:3072 lds
ds_read_b64_tr_b16 a[156:157],v22 offset:17672
ds_read_b64_tr_b16 a[158:159],v22 offset:19720
v_mfma_f32_16x16x32_bf16 v[160:163],a[148:151],a[196:199],v[160:163]
s_add_u32 m0,s59,4096
v_mfma_f32_16x16x32_bf16 v[168:171],a[148:151],a[204:207],0
v_mfma_f32_16x16x32_bf16 v[168:171],a[144:147],a[200:203],v[168:171]
v_mfma_f32_16x16x32_bf16 v[176:179],a[144:147],a[208:211],0
buffer_load_dwordx4 v52,s[28:31],s57 offen lds
v_mfma_f32_16x16x32_bf16 v[176:179],a[148:151],a[212:215],v[176:179]
v_mfma_f32_16x16x32_bf16 v[184:187],a[148:151],a[220:223],0
v_mfma_f32_16x16x32_bf16 v[184:187],a[144:147],a[216:219],v[184:187]
s_waitcnt lgkmcnt(2)
v_mfma_f32_16x16x32_bf16 v[164:167],a[152:155],a[192:195],0
s_waitcnt lgkmcnt(0)
buffer_load_dwordx4 v53,s[28:31],s57 offen offset:1024 lds
ds_read_b64_tr_b16 a[160:161],v22 offset:1536
ds_read_b64_tr_b16 a[162:163],v22 offset:3584
v_mfma_f32_16x16x32_bf16 v[164:167],a[156:159],a[196:199],v[164:167]
ds_read_b64_tr_b16 a[164:165],v22 offset:17920
ds_read_b64_tr_b16 a[166:167],v22 offset:19968
v_mfma_f32_16x16x32_bf16 v[172:175],a[156:159],a[204:207],0
v_mfma_f32_16x16x32_bf16 v[172:175],a[152:155],a[200:203],v[172:175]
v_mfma_f32_16x16x32_bf16 v[180:183],a[152:155],a[208:211],0
buffer_load_dwordx4 v54,s[28:31],s57 offen offset:2048 lds
v_mfma_f32_16x16x32_bf16 v[180:183],a[156:159],a[212:215],v[180:183]
v_mfma_f32_16x16x32_bf16 v[188:191],a[156:159],a[220:223],0
s_waitcnt vmcnt(15)
v_mfma_f32_16x16x32_bf16 v[188:191],a[152:155],a[216:219],v[188:191]
s_barrier
buffer_load_dwordx4 v55,s[28:31],s57 offen offset:3072 lds
ds_read_b64_tr_b16 a[168:169],v22 offset:1544
ds_read_b64_tr_b16 a[170:171],v22 offset:3592
s_waitcnt lgkmcnt(4)
v_mfma_f32_16x16x32_bf16 v[192:195],a[160:163],a[192:195],0
s_add_u32 m0,s58,0
ds_read_b64_tr_b16 a[172:173],v22 offset:17928
ds_read_b64_tr_b16 a[174:175],v22 offset:19976
s_waitcnt lgkmcnt(4)
v_mfma_f32_16x16x32_bf16 v[192:195],a[164:167],a[196:199],v[192:195]
v_mfma_f32_16x16x32_bf16 v[200:203],a[164:167],a[204:207],0
s_lshl_b32 s44,s41,13
s_add_u32 s44,s44,104857600
s_mov_b64 s[64:65],s[12:13]
v_mfma_f32_16x16x32_bf16 v[200:203],a[160:163],a[200:203],v[200:203]
s_mov_b32 s66,8192
buffer_load_dwordx4 v56,s[32:35],s56 offen lds
v_mfma_f32_16x16x32_bf16 v[208:211],a[160:163],a[208:211],0
s_add_u32 m0,s58,1024
s_add_u32 s64,s64,s44
s_addc_u32 s65,s65,0
v_mfma_f32_16x16x32_bf16 v[208:211],a[164:167],a[212:215],v[208:211]
s_and_b32 s65,s65,0xffff
s_mov_b32 s67,0x20000
v_mov_b32 v14,4096
v_mfma_f32_16x16x32_bf16 v[216:219],a[164:167],a[220:223],0
s_mov_b64 s[72:73],s[14:15]
s_and_b32 s73,s73,0xffff
ds_read_b64_tr_b16 a[176:177],v22 offset:1792
v_mfma_f32_16x16x32_bf16 v[216:219],a[160:163],a[216:219],v[216:219]
s_mov_b32 s74,5242880
s_mov_b32 s75,0x20000
ds_read_b64_tr_b16 a[178:179],v22 offset:3840
s_waitcnt lgkmcnt(4)
v_mfma_f32_16x16x32_bf16 v[196:199],a[168:171],a[192:195],0
s_waitcnt lgkmcnt(2)
buffer_load_dwordx4 v57,s[32:35],s56 offen lds
ds_read_b64_tr_b16 a[180:181],v22 offset:18176
ds_read_b64_tr_b16 a[182:183],v22 offset:20224
v_mfma_f32_16x16x32_bf16 v[196:199],a[172:175],a[196:199],v[196:199]
s_add_u32 m0,s58,2048
v_lshlrev_b32 v15,3,v6
v_add_u32 v16,128,v15
v_mfma_f32_16x16x32_bf16 v[204:207],a[172:175],a[204:207],0
s_lshl_b32 s83,%1,2
ds_read_b64_tr_b16 a[184:185],v22 offset:1800
v_lshrrev_b32 v16,10,v16
v_mfma_f32_16x16x32_bf16 v[204:207],a[168:171],a[200:203],v[204:207]
s_add_u32 s83,s83,s40
ds_read_b64_tr_b16 a[186:187],v22 offset:3848
v_lshl_add_u32 v15,v16,24,v15
v_mfma_f32_16x16x32_bf16 v[212:215],a[168:171],a[208:211],0
s_mul_i32 s83,s83,5120
buffer_load_dwordx4 v58,s[32:35],s56 offen lds
v_mfma_f32_16x16x32_bf16 v[212:215],a[172:175],a[212:215],v[212:215]
s_add_u32 m0,s58,3072
s_lshl_b32 s43,s41,5
ds_read_b64_tr_b16 a[188:189],v22 offset:18184
v_mfma_f32_16x16x32_bf16 v[220:223],a[172:175],a[220:223],0
s_lshl_b32 s44,s42,1
s_add_u32 s43,s43,s44
ds_read_b64_tr_b16 a[190:191],v22 offset:20232
v_mfma_f32_16x16x32_bf16 v[220:223],a[168:171],a[216:219],v[220:223]
s_lshl_b32 s44,s43,18
s_mov_b64 s[20:21],s[6:7]
ds_read_b64_tr_b16 a[128:129],v22
s_waitcnt lgkmcnt(7)
v_mfma_f32_16x16x32_bf16 v[224:227],a[176:179],a[192:195],0
s_mov_b32 s22,524288
buffer_load_dwordx4 v59,s[32:35],s56 offen lds
s_waitcnt lgkmcnt(5)
v_mfma_f32_16x16x32_bf16 v[224:227],a[180:183],a[196:199],v[224:227]
s_add_u32 m0,s58,4096
s_mov_b32 s23,0x20000
ds_read_b64_tr_b16 a[130:131],v22 offset:2048
v_mfma_f32_16x16x32_bf16 v[232:235],a[180:183],a[204:207],0
s_add_u32 s20,s20,s44
s_addc_u32 s21,s21,0
ds_read_b64_tr_b16 a[132:133],v22 offset:16384
v_mfma_f32_16x16x32_bf16 v[232:235],a[176:179],a[200:203],v[232:235]
s_and_b32 s21,s21,0xffff
s_mov_b64 s[24:25],s[8:9]
ds_read_b64_tr_b16 a[134:135],v22 offset:18432
v_mfma_f32_16x16x32_bf16 v[240:243],a[176:179],a[208:211],0
)A"
R"A(s_mov_b32 s26,524288
buffer_load_dwordx4 v60,s[32:35],s56 offen lds
v_mfma_f32_16x16x32_bf16 v[240:243],a[180:183],a[212:215],v[240:243]
s_add_u32 s24,s24,s44
s_addc_u32 s25,s25,0
ds_read_b64_tr_b16 a[136:137],v22 offset:8
v_mfma_f32_16x16x32_bf16 v[248:251],a[180:183],a[220:223],0
s_add_u32 m0,s58,5120
s_and_b32 s25,s25,0xffff
ds_read_b64_tr_b16 a[138:139],v22 offset:2056
v_mfma_f32_16x16x32_bf16 v[248:251],a[176:179],a[216:219],v[248:251]
s_mov_b32 s27,0x20000
s_lshl_b32 s44,s43,16
ds_read_b64_tr_b16 a[140:141],v22 offset:16392
s_waitcnt lgkmcnt(6)
v_mfma_f32_16x16x32_bf16 v[228:231],a[184:187],a[192:195],0
s_xor_b32 s59,s59,131072
s_mov_b64 s[36:37],s[12:13]
ds_read_b64_tr_b16 a[142:143],v22 offset:18440
v_mfma_f32_16x16x32_bf16 v[228:231],a[188:191],a[196:199],v[228:231]
s_add_u32 s57,s57,128
buffer_load_dwordx4 v61,s[32:35],s56 offen lds
v_mfma_f32_16x16x32_bf16 v[236:239],a[188:191],a[204:207],0
s_add_u32 s36,s36,s44
s_addc_u32 s37,s37,0
ds_read_b64_tr_b16 a[144:145],v22 offset:256
v_mfma_f32_16x16x32_bf16 v[236:239],a[184:187],a[200:203],v[236:239]
s_add_u32 m0,s58,6144
s_and_b32 s37,s37,0xffff
ds_read_b64_tr_b16 a[146:147],v22 offset:2304
v_mfma_f32_16x16x32_bf16 v[244:247],a[184:187],a[208:211],0
s_mov_b32 s38,131072
s_mov_b32 s39,0x20000
ds_read_b64_tr_b16 a[148:149],v22 offset:16640
v_mfma_f32_16x16x32_bf16 v[244:247],a[188:191],a[212:215],v[244:247]
s_lshl_b32 s51,s40,12
s_add_u32 s52,s51,16384
ds_read_b64_tr_b16 a[150:151],v22 offset:18688
v_mfma_f32_16x16x32_bf16 v[252:255],a[188:191],a[220:223],0
s_mul_i32 s47,s41,5
buffer_load_dwordx4 v62,s[32:35],s56 offen lds
v_mfma_f32_16x16x32_bf16 v[252:255],a[184:187],a[216:219],v[252:255]
s_add_u32 s47,s47,s42
s_and_b32 s47,s47,15
ds_read_b64_tr_b16 a[152:153],v22 offset:264
s_waitcnt lgkmcnt(6)
v_mfma_f32_16x16x32_bf16 a[0:3],a[128:131],a[192:195],0
s_add_u32 m0,s58,7168
s_lshl_b32 s47,s47,14
ds_read_b64_tr_b16 a[154:155],v22 offset:2312
v_mfma_f32_16x16x32_bf16 a[0:3],a[132:135],a[196:199],a[0:3]
s_mov_b32 s54,0
s_mov_b32 s50,98304
ds_read_b64_tr_b16 a[156:157],v22 offset:16648
v_mfma_f32_16x16x32_bf16 a[32:35],a[132:135],a[204:207],0
s_mov_b32 s49,0
s_lshl_b32 s55,s40,14
ds_read_b64_tr_b16 a[158:159],v22 offset:18696
v_mfma_f32_16x16x32_bf16 a[32:35],a[128:131],a[200:203],a[32:35]
s_mov_b32 s61,0x3e0293ee
buffer_load_dwordx4 v63,s[32:35],s56 offen lds
v_mfma_f32_16x16x32_bf16 a[64:67],a[128:131],a[208:211],0
s_mov_b32 s63,0x5d800000
s_lshl_b32 s44,s40,2
ds_read_b64_tr_b16 a[160:161],v22 offset:512
v_mfma_f32_16x16x32_bf16 a[64:67],a[132:135],a[212:215],a[64:67]
s_add_u32 m0,s59,0
s_add_u32 s44,s44,s70
ds_read_b64_tr_b16 a[162:163],v22 offset:2560
v_mfma_f32_16x16x32_bf16 a[96:99],a[132:135],a[220:223],0
s_mov_b32 s46,0xff800000
ds_read_b64_tr_b16 a[164:165],v22 offset:16896
v_lshlrev_b32 v70,8,v65
v_mfma_f32_16x16x32_bf16 a[96:99],a[128:131],a[216:219],a[96:99]
s_cmp_eq_u32 s44,14
s_cselect_b32 s18,0x7149f2ca,s46
ds_read_b64_tr_b16 a[166:167],v22 offset:18944
v_mfma_f32_16x16x32_bf16 a[4:7],a[136:139],a[192:195],0
s_add_u32 s60,s60,32768
buffer_load_dwordx4 v48,s[28:31],s57 offen lds
s_waitcnt lgkmcnt(6)
v_mfma_f32_16x16x32_bf16 a[4:7],a[140:143],a[196:199],a[4:7]
s_cmp_eq_u32 s44,13
s_cselect_b32 s19,0x7149f2ca,s46
ds_read_b64_tr_b16 a[168:169],v22 offset:520
v_mfma_f32_16x16x32_bf16 a[36:39],a[140:143],a[204:207],0
s_cmp_eq_u32 s44,12
s_cselect_b32 s100,0x7149f2ca,s46
ds_read_b64_tr_b16 a[170:171],v22 offset:2568
v_mfma_f32_16x16x32_bf16 a[36:39],a[136:139],a[200:203],a[36:39]
s_cmp_eq_u32 s44,15
s_cselect_b32 s101,0x7149f2ca,s46
ds_read_b64_tr_b16 a[172:173],v22 offset:16904
v_mfma_f32_16x16x32_bf16 a[68:71],a[136:139],a[208:211],0
s_add_u32 s58,s58,32768
ds_read_b64_tr_b16 a[174:175],v22 offset:18952
v_lshl_add_u32 v70,v66,6,v70
v_mfma_f32_16x16x32_bf16 a[68:71],a[140:143],a[212:215],a[68:71]
s_mul_i32 s81,s70,1310720
buffer_load_dwordx4 v49,s[28:31],s57 offen offset:1024 lds
v_mfma_f32_16x16x32_bf16 a[100:103],a[140:143],a[220:223],0
s_mul_i32 s44,s71,320
ds_read_b64_tr_b16 a[176:177],v22 offset:768
v_add_u32 v70,s51,v70
v_mfma_f32_16x16x32_bf16 a[100:103],a[136:139],a[216:219],a[100:103]
s_add_u32 s81,s81,s44
ds_read_b64_tr_b16 a[178:179],v22 offset:2816
v_lshl_add_u32 v4,v64,4,v70
v_mfma_f32_16x16x32_bf16 a[8:11],a[144:147],a[192:195],0
s_add_u32 s56,s56,524288
ds_read_b64_tr_b16 a[180:181],v22 offset:17152
v_xor_b32 v71,2,v64
v_mfma_f32_16x16x32_bf16 a[8:11],a[148:151],a[196:199],a[8:11]
s_mul_i32 s82,s40,10240
ds_read_b64_tr_b16 a[182:183],v22 offset:19200
v_lshl_add_u32 v5,v71,4,v70
v_mfma_f32_16x16x32_bf16 a[40:43],a[148:151],a[204:207],0
s_mul_i32 s44,s40,40960
buffer_load_dwordx4 v50,s[28:31],s57 offen offset:2048 lds
v_mfma_f32_16x16x32_bf16 a[40:43],a[144:147],a[200:203],a[40:43]
s_lshr_b32 s92,%1,3
ds_read_b64_tr_b16 a[184:185],v22 offset:776
v_lshlrev_b32 v72,10,v65
v_mfma_f32_16x16x32_bf16 a[72:75],a[144:147],a[208:211],0
s_lshl_b32 s92,s92,2
ds_read_b64_tr_b16 a[186:187],v22 offset:2824
v_lshl_add_u32 v72,v64,6,v72
v_mfma_f32_16x16x32_bf16 a[72:75],a[148:151],a[212:215],a[72:75]
s_add_u32 s92,s92,s40
ds_read_b64_tr_b16 a[188:189],v22 offset:17160
v_and_b32 v73,2,v65
v_mfma_f32_16x16x32_bf16 a[104:107],a[148:151],a[220:223],0
s_lshl_b32 s92,s92,7
ds_read_b64_tr_b16 a[190:191],v22 offset:19208
v_xor_b32 v73,v73,v66
v_mfma_f32_16x16x32_bf16 a[104:107],a[144:147],a[216:219],a[104:107]
s_add_u32 s93,s92,L_tailseg_%=-L_pc_%=
buffer_load_dwordx4 v51,s[28:31],s57 offen offset:3072 lds
v_mfma_f32_16x16x32_bf16 a[12:15],a[152:155],a[192:195],0
s_waitcnt lgkmcnt(5)
v_lshl_add_u32 v72,v73,4,v72
v_add_u32 v1,98304,v72
v_mfma_f32_16x16x32_bf16 a[12:15],a[156:159],a[196:199],a[12:15]
s_add_u32 m0,s59,4096
v_lshlrev_b32 v72,10,v66
v_lshl_add_u32 v72,v65,6,v72
v_mfma_f32_16x16x32_bf16 a[44:47],a[156:159],a[204:207],0
s_xor_b32 s59,s59,131072
v_lshl_add_u32 v72,v64,3,v72
v_add_u32 v72,114688,v72
v_mfma_f32_16x16x32_bf16 a[44:47],a[152:155],a[200:203],a[44:47]
s_cmp_eq_u32 s60,131072
s_cselect_b32 s60,32768,s60
v_and_b32 v73,1,v66
v_mfma_f32_16x16x32_bf16 a[76:79],a[152:155],a[208:211],0
buffer_load_dwordx4 v52,s[28:31],s57 offen lds
v_mfma_f32_16x16x32_bf16 a[76:79],a[156:159],a[212:215],a[76:79]
v_add_u32 v22,s60,v23
v_xor_b32 v20,131072,v20
v_mfma_f32_16x16x32_bf16 a[108:111],a[156:159],a[220:223],0
s_add_u32 s60,s60,32768
v_xor_b32 v21,131072,v21
v_lshl_add_u32 v2,v73,5,v72
v_mfma_f32_16x16x32_bf16 a[108:111],a[152:155],a[216:219],a[108:111]
s_waitcnt vmcnt(13) lgkmcnt(0)
v_xor_b32 v73,1,v73
v_lshl_add_u32 v3,v73,5,v72
v_mfma_f32_16x16x32_bf16 a[16:19],a[160:163],a[192:195],0
s_barrier
buffer_load_dwordx4 v53,s[28:31],s57 offen offset:1024 lds
v_mfma_f32_16x16x32_bf16 a[16:19],a[164:167],a[196:199],a[16:19]
ds_read_b128 a[224:227],v20
v_mov_b32 v28,0x3f803f80
v_mfma_f32_16x16x32_bf16 a[48:51],a[164:167],a[204:207],0
ds_read_b64_tr_b16 a[128:129],v22 offset:1024
v_mov_b32 v29,0x3f803f80
v_mfma_f32_16x16x32_bf16 a[48:51],a[160:163],a[200:203],a[48:51]
ds_read_b64_tr_b16 a[130:131],v22 offset:3072
v_mov_b32 v30,0x3f803f80
v_mfma_f32_16x16x32_bf16 a[80:83],a[160:163],a[208:211],0
ds_read_b128 a[228:231],v21
v_mov_b32 v31,0x3f803f80
v_mfma_f32_16x16x32_bf16 a[80:83],a[164:167],a[212:215],a[80:83]
buffer_load_dwordx4 v54,s[28:31],s57 offen offset:2048 lds
v_mfma_f32_16x16x32_bf16 a[112:115],a[164:167],a[220:223],0
s_cmp_ge_u32 s58,131072
s_cselect_b32 s62,98304,0
ds_read_b64_tr_b16 a[132:133],v22 offset:17408
v_mfma_f32_16x16x32_bf16 a[112:115],a[160:163],a[216:219],a[112:115]
s_sub_u32 s58,s58,s62
ds_read_b64_tr_b16 a[134:135],v22 offset:19456
v_add_u32 v70,0,v12
v_mfma_f32_16x16x32_bf16 a[20:23],a[168:171],a[192:195],0
ds_read_b128 a[232:235],v20 offset:2048
v_mul_u32_u24 v71,3277,v70
v_mfma_f32_16x16x32_bf16 a[20:23],a[172:175],a[196:199],a[20:23]
ds_read_b128 a[236:239],v21 offset:2048
v_lshrrev_b32 v71,16,v71
v_mfma_f32_16x16x32_bf16 a[52:55],a[172:175],a[204:207],0
buffer_load_dwordx4 v55,s[28:31],s57 offen offset:3072 lds
v_mfma_f32_16x16x32_bf16 a[52:55],a[168:171],a[200:203],a[52:55]
s_add_u32 s57,s57,128
ds_read_b128 a[240:243],v20 offset:4096
v_mul_u32_u24 v72,20,v71
v_mfma_f32_16x16x32_bf16 a[84:87],a[168:171],a[208:211],0
ds_read_b128 a[244:247],v21 offset:4096
v_sub_u32 v72,v70,v72
v_mfma_f32_16x16x32_bf16 a[84:87],a[172:175],a[212:215],a[84:87]
ds_read_b128 a[248:251],v20 offset:6144
v_bfe_u32 v73,v71,2,1
v_mfma_f32_16x16x32_bf16 a[116:119],a[172:175],a[220:223],0
ds_read_b128 a[252:255],v21 offset:6144
v_lshlrev_b32 v73,1,v73
v_mfma_f32_16x16x32_bf16 a[116:119],a[168:171],a[216:219],a[116:119]
ds_read_b64_tr_b16 a[136:137],v22 offset:1032
v_xor_b32 v72,v72,v73
v_mfma_f32_16x16x32_bf16 a[24:27],a[176:179],a[192:195],0
ds_read_b64_tr_b16 a[138:139],v22 offset:3080
v_mul_u32_u24 v71,1280,v71
v_mfma_f32_16x16x32_bf16 a[24:27],a[180:183],a[196:199],a[24:27]
ds_read_b64_tr_b16 a[140:141],v22 offset:17416
v_lshl_add_u32 v71,v72,4,v71
v_mfma_f32_16x16x32_bf16 a[56:59],a[180:183],a[204:207],0
ds_read_b64_tr_b16 a[142:143],v22 offset:19464
v_add_u32 v71,s44,v71
v_mfma_f32_16x16x32_bf16 a[56:59],a[176:179],a[200:203],a[56:59]
ds_read_b64_tr_b16 a[144:145],v22 offset:1280
v_subrev_u32 v7,0,v71
v_mfma_f32_16x16x32_bf16 a[88:91],a[176:179],a[208:211],0
ds_read_b64_tr_b16 a[146:147],v22 offset:3328
v_add_u32 v70,64,v12
v_mfma_f32_16x16x32_bf16 a[88:91],a[180:183],a[212:215],a[88:91]
ds_read_b64_tr_b16 a[148:149],v22 offset:17664
v_mul_u32_u24 v71,3277,v70
v_mfma_f32_16x16x32_bf16 a[120:123],a[180:183],a[220:223],0
ds_read_b64_tr_b16 a[150:151],v22 offset:19712
v_lshrrev_b32 v71,16,v71
v_mfma_f32_16x16x32_bf16 a[120:123],a[176:179],a[216:219],a[120:123]
ds_read_b64_tr_b16 a[152:153],v22 offset:1288
v_mul_u32_u24 v72,20,v71
v_mfma_f32_16x16x32_bf16 a[28:31],a[184:187],a[192:195],0
ds_read_b64_tr_b16 a[154:155],v22 offset:3336
v_sub_u32 v72,v70,v72
v_mfma_f32_16x16x32_bf16 a[28:31],a[188:191],a[196:199],a[28:31]
ds_read_b64_tr_b16 a[156:157],v22 offset:17672
ds_read_b64_tr_b16 a[158:159],v22 offset:19720
v_mfma_f32_16x16x32_bf16 a[60:63],a[188:191],a[204:207],0
ds_read_b64_tr_b16 a[160:161],v22 offset:1536
v_bfe_u32 v73,v71,2,1
v_mfma_f32_16x16x32_bf16 a[60:63],a[184:187],a[200:203],a[60:63]
ds_read_b64_tr_b16 a[162:163],v22 offset:3584
v_lshlrev_b32 v73,1,v73
v_mfma_f32_16x16x32_bf16 a[92:95],a[184:187],a[208:211],0
ds_read_b64_tr_b16 a[164:165],v22 offset:17920
v_xor_b32 v72,v72,v73
v_mfma_f32_16x16x32_bf16 a[92:95],a[188:191],a[212:215],a[92:95]
ds_read_b64_tr_b16 a[166:167],v22 offset:19968
v_mul_u32_u24 v71,1280,v71
v_mfma_f32_16x16x32_bf16 a[124:127],a[188:191],a[220:223],0
s_add_u32 m0,s58,0
ds_read_b64_tr_b16 a[168:169],v22 offset:1544
v_lshl_add_u32 v71,v72,4,v71
v_mfma_f32_16x16x32_bf16 a[124:127],a[184:187],a[216:219],a[124:127]
s_waitcnt lgkmcnt(8)
ds_read_b64_tr_b16 a[170:171],v22 offset:3592
v_add_u32 v71,s44,v71
v_mfma_f32_16x16x32_bf16 v[128:131],a[128:131],a[224:227],v[128:131]
ds_read_b64_tr_b16 a[172:173],v22 offset:17928
v_subrev_u32 v8,1024,v71
v_mfma_f32_16x16x32_bf16 v[128:131],a[132:135],a[228:231],v[128:131]
buffer_load_dwordx4 v56,s[32:35],s56 offen lds
v_mfma_f32_16x16x32_bf16 v[136:139],a[132:135],a[236:239],v[136:139]
ds_read_b64_tr_b16 a[174:175],v22 offset:19976
v_add_u32 v70,128,v12
v_mfma_f32_16x16x32_bf16 v[136:139],a[128:131],a[232:235],v[136:139]
s_add_u32 m0,s58,1024
v_mul_u32_u24 v71,3277,v70
v_lshrrev_b32 v71,16,v71
v_mfma_f32_16x16x32_bf16 v[144:147],a[128:131],a[240:243],v[144:147]
ds_read_b64_tr_b16 a[176:177],v22 offset:1792
v_mul_u32_u24 v72,20,v71
v_mfma_f32_16x16x32_bf16 v[144:147],a[132:135],a[244:247],v[144:147]
ds_read_b64_tr_b16 a[178:179],v22 offset:3840
v_sub_u32 v72,v70,v72
v_mfma_f32_16x16x32_bf16 v[152:155],a[132:135],a[252:255],v[152:155]
buffer_load_dwordx4 v57,s[32:35],s56 offen lds
v_mfma_f32_16x16x32_bf16 v[152:155],a[128:131],a[248:251],v[152:155]
ds_read_b64_tr_b16 a[180:181],v22 offset:18176
v_bfe_u32 v73,v71,2,1
v_mfma_f32_16x16x32_bf16 v[132:135],a[136:139],a[224:227],v[132:135]
ds_read_b64_tr_b16 a[182:183],v22 offset:20224
v_lshlrev_b32 v73,1,v73
v_mfma_f32_16x16x32_bf16 v[132:135],a[140:143],a[228:231],v[132:135]
)A"
R"A(s_add_u32 m0,s58,2048
v_xor_b32 v72,v72,v73
v_mul_u32_u24 v71,1280,v71
v_mfma_f32_16x16x32_bf16 v[140:143],a[140:143],a[236:239],v[140:143]
v_lshl_add_u32 v71,v72,4,v71
v_add_u32 v71,s44,v71
v_mfma_f32_16x16x32_bf16 v[140:143],a[136:139],a[232:235],v[140:143]
ds_read_b64_tr_b16 a[184:185],v22 offset:1800
v_subrev_u32 v9,2048,v71
v_mfma_f32_16x16x32_bf16 v[148:151],a[136:139],a[240:243],v[148:151]
buffer_load_dwordx4 v58,s[32:35],s56 offen lds
v_mfma_f32_16x16x32_bf16 v[148:151],a[140:143],a[244:247],v[148:151]
ds_read_b64_tr_b16 a[186:187],v22 offset:3848
v_add_u32 v70,192,v12
v_mfma_f32_16x16x32_bf16 v[156:159],a[140:143],a[252:255],v[156:159]
ds_read_b64_tr_b16 a[188:189],v22 offset:18184
v_mul_u32_u24 v71,3277,v70
v_mfma_f32_16x16x32_bf16 v[156:159],a[136:139],a[248:251],v[156:159]
s_add_u32 m0,s58,3072
ds_read_b64_tr_b16 a[190:191],v22 offset:20232
v_lshrrev_b32 v71,16,v71
v_mfma_f32_16x16x32_bf16 v[160:163],a[144:147],a[224:227],v[160:163]
v_mul_u32_u24 v72,20,v71
v_sub_u32 v72,v70,v72
v_mfma_f32_16x16x32_bf16 v[160:163],a[148:151],a[228:231],v[160:163]
ds_read_b64_tr_b16 a[128:129],v22
v_bfe_u32 v73,v71,2,1
v_mfma_f32_16x16x32_bf16 v[168:171],a[148:151],a[236:239],v[168:171]
buffer_load_dwordx4 v59,s[32:35],s56 offen lds
v_mfma_f32_16x16x32_bf16 v[168:171],a[144:147],a[232:235],v[168:171]
ds_read_b64_tr_b16 a[130:131],v22 offset:2048
v_lshlrev_b32 v73,1,v73
v_mfma_f32_16x16x32_bf16 v[176:179],a[144:147],a[240:243],v[176:179]
s_add_u32 m0,s58,4096
ds_read_b64_tr_b16 a[132:133],v22 offset:16384
v_xor_b32 v72,v72,v73
v_mfma_f32_16x16x32_bf16 v[176:179],a[148:151],a[244:247],v[176:179]
ds_read_b64_tr_b16 a[134:135],v22 offset:18432
v_mul_u32_u24 v71,1280,v71
v_mfma_f32_16x16x32_bf16 v[184:187],a[148:151],a[252:255],v[184:187]
buffer_load_dword v13,v14,s[64:67],0 offen sc1
v_mfma_f32_16x16x32_bf16 v[184:187],a[144:147],a[248:251],v[184:187]
s_waitcnt lgkmcnt(4)
v_lshl_add_u32 v71,v72,4,v71
v_add_u32 v71,s44,v71
v_mfma_f32_16x16x32_bf16 v[164:167],a[152:155],a[224:227],v[164:167]
ds_read_b64_tr_b16 a[136:137],v22 offset:8
v_subrev_u32 v10,3072,v71
v_mfma_f32_16x16x32_bf16 v[164:167],a[156:159],a[228:231],v[164:167]
ds_read_b64_tr_b16 a[138:139],v22 offset:2056
v_add_u32 v70,256,v12
v_mfma_f32_16x16x32_bf16 v[172:175],a[156:159],a[236:239],v[172:175]
buffer_load_dwordx4 v60,s[32:35],s56 offen lds
v_mfma_f32_16x16x32_bf16 v[172:175],a[152:155],a[232:235],v[172:175]
s_add_u32 m0,s58,5120
ds_read_b64_tr_b16 a[140:141],v22 offset:16392
v_mul_u32_u24 v71,3277,v70
v_mfma_f32_16x16x32_bf16 v[180:183],a[152:155],a[240:243],v[180:183]
ds_read_b64_tr_b16 a[142:143],v22 offset:18440
v_lshrrev_b32 v71,16,v71
v_mfma_f32_16x16x32_bf16 v[180:183],a[156:159],a[244:247],v[180:183]
v_mul_u32_u24 v72,20,v71
v_sub_u32 v72,v70,v72
v_mfma_f32_16x16x32_bf16 v[188:191],a[156:159],a[252:255],v[188:191]
buffer_load_dwordx4 v61,s[32:35],s56 offen lds
v_mfma_f32_16x16x32_bf16 v[188:191],a[152:155],a[248:251],v[188:191]
v_bfe_u32 v73,v71,2,1
v_lshlrev_b32 v73,1,v73
v_mfma_f32_16x16x32_bf16 v[192:195],a[160:163],a[224:227],v[192:195]
s_add_u32 m0,s58,6144
ds_read_b64_tr_b16 a[144:145],v22 offset:256
v_xor_b32 v72,v72,v73
v_mfma_f32_16x16x32_bf16 v[192:195],a[164:167],a[228:231],v[192:195]
ds_read_b64_tr_b16 a[146:147],v22 offset:2304
v_mul_u32_u24 v71,1280,v71
v_mfma_f32_16x16x32_bf16 v[200:203],a[164:167],a[236:239],v[200:203]
ds_read_b64_tr_b16 a[148:149],v22 offset:16640
v_lshl_add_u32 v71,v72,4,v71
v_mfma_f32_16x16x32_bf16 v[200:203],a[160:163],a[232:235],v[200:203]
buffer_load_dwordx4 v62,s[32:35],s56 offen lds
v_mfma_f32_16x16x32_bf16 v[208:211],a[160:163],a[240:243],v[208:211]
ds_read_b64_tr_b16 a[150:151],v22 offset:18688
v_add_u32 v71,s44,v71
v_mfma_f32_16x16x32_bf16 v[208:211],a[164:167],a[244:247],v[208:211]
s_add_u32 m0,s58,7168
ds_read_b64_tr_b16 a[152:153],v22 offset:264
v_subrev_u32 v11,0,v71
v_mfma_f32_16x16x32_bf16 v[216:219],a[164:167],a[252:255],v[216:219]
s_add_u32 s58,s58,32768
ds_read_b64_tr_b16 a[154:155],v22 offset:2312
v_lshlrev_b32 v24,10,v6
v_mfma_f32_16x16x32_bf16 v[216:219],a[160:163],a[248:251],v[216:219]
s_cmp_eq_u32 s60,131072
s_cselect_b32 s60,32768,s60
ds_read_b64_tr_b16 a[156:157],v22 offset:16648
v_mfma_f32_16x16x32_bf16 v[196:199],a[168:171],a[224:227],v[196:199]
buffer_load_dwordx4 v63,s[32:35],s56 offen lds
v_mfma_f32_16x16x32_bf16 v[196:199],a[172:175],a[228:231],v[196:199]
s_add_u32 s56,s56,524288
ds_read_b64_tr_b16 a[158:159],v22 offset:18696
v_xor_b32 v20,131072,v20
v_mfma_f32_16x16x32_bf16 v[204:207],a[172:175],a[236:239],v[204:207]
ds_read_b64_tr_b16 a[160:161],v22 offset:512
v_xor_b32 v21,131072,v21
v_mfma_f32_16x16x32_bf16 v[204:207],a[168:171],a[232:235],v[204:207]
s_add_u32 m0,s59,0
ds_read_b64_tr_b16 a[162:163],v22 offset:2560
v_mfma_f32_16x16x32_bf16 v[212:215],a[168:171],a[240:243],v[212:215]
s_cmp_ge_u32 s58,131072
s_cselect_b32 s62,98304,0
ds_read_b64_tr_b16 a[164:165],v22 offset:16896
v_mfma_f32_16x16x32_bf16 v[212:215],a[172:175],a[244:247],v[212:215]
s_sub_u32 s58,s58,s62
ds_read_b64_tr_b16 a[166:167],v22 offset:18944
v_mfma_f32_16x16x32_bf16 v[220:223],a[172:175],a[252:255],v[220:223]
buffer_load_dwordx4 v48,s[28:31],s57 offen lds
v_mfma_f32_16x16x32_bf16 v[220:223],a[168:171],a[248:251],v[220:223]
ds_read_b64_tr_b16 a[168:169],v22 offset:520
v_mfma_f32_16x16x32_bf16 v[224:227],a[176:179],a[224:227],v[224:227]
ds_read_b64_tr_b16 a[170:171],v22 offset:2568
v_mfma_f32_16x16x32_bf16 v[224:227],a[180:183],a[228:231],v[224:227]
ds_read_b64_tr_b16 a[172:173],v22 offset:16904
v_mfma_f32_16x16x32_bf16 v[232:235],a[180:183],a[236:239],v[232:235]
ds_read_b64_tr_b16 a[174:175],v22 offset:18952
v_mfma_f32_16x16x32_bf16 v[232:235],a[176:179],a[232:235],v[232:235]
v_mfma_f32_16x16x32_bf16 v[240:243],a[176:179],a[240:243],v[240:243]
buffer_load_dwordx4 v49,s[28:31],s57 offen offset:1024 lds
v_mfma_f32_16x16x32_bf16 v[240:243],a[180:183],a[244:247],v[240:243]
v_mfma_f32_16x16x32_bf16 v[248:251],a[180:183],a[252:255],v[248:251]
ds_read_b64_tr_b16 a[180:181],v22 offset:17152
v_mfma_f32_16x16x32_bf16 v[248:251],a[176:179],a[248:251],v[248:251]
ds_read_b64_tr_b16 a[176:177],v22 offset:768
v_mfma_f32_16x16x32_bf16 v[228:231],a[184:187],a[224:227],v[228:231]
ds_read_b64_tr_b16 a[178:179],v22 offset:2816
v_mfma_f32_16x16x32_bf16 v[228:231],a[188:191],a[228:231],v[228:231]
buffer_load_dwordx4 v50,s[28:31],s57 offen offset:2048 lds
v_mfma_f32_16x16x32_bf16 v[236:239],a[188:191],a[236:239],v[236:239]
ds_read_b64_tr_b16 a[182:183],v22 offset:19200
v_mfma_f32_16x16x32_bf16 v[236:239],a[184:187],a[232:235],v[236:239]
v_mfma_f32_16x16x32_bf16 v[244:247],a[184:187],a[240:243],v[244:247]
v_mfma_f32_16x16x32_bf16 v[244:247],a[188:191],a[244:247],v[244:247]
v_mfma_f32_16x16x32_bf16 v[252:255],a[188:191],a[252:255],v[252:255]
ds_read_b64_tr_b16 a[188:189],v22 offset:17160
v_mfma_f32_16x16x32_bf16 v[252:255],a[184:187],a[248:251],v[252:255]
s_waitcnt lgkmcnt(3)
buffer_load_dwordx4 v51,s[28:31],s57 offen offset:3072 lds
v_mfma_f32_16x16x32_bf16 a[0:3],a[128:131],a[224:227],a[0:3]
ds_read_b64_tr_b16 a[184:185],v22 offset:776
v_mfma_f32_16x16x32_bf16 a[0:3],a[132:135],a[228:231],a[0:3]
ds_read_b64_tr_b16 a[186:187],v22 offset:2824
v_mfma_f32_16x16x32_bf16 a[32:35],a[132:135],a[236:239],a[32:35]
s_add_u32 m0,s59,4096
ds_read_b64_tr_b16 a[190:191],v22 offset:19208
v_add_u32 v22,s60,v23
v_mfma_f32_16x16x32_bf16 a[32:35],a[128:131],a[232:235],a[32:35]
s_xor_b32 s59,s59,131072
v_mfma_f32_16x16x32_bf16 a[64:67],a[128:131],a[240:243],a[64:67]
v_mfma_f32_16x16x32_bf16 a[64:67],a[132:135],a[244:247],a[64:67]
buffer_load_dwordx4 v52,s[28:31],s57 offen lds
v_mfma_f32_16x16x32_bf16 a[96:99],a[132:135],a[252:255],a[96:99]
v_mfma_f32_16x16x32_bf16 a[96:99],a[128:131],a[248:251],a[96:99]
v_mfma_f32_16x16x32_bf16 a[4:7],a[136:139],a[224:227],a[4:7]
v_mfma_f32_16x16x32_bf16 a[4:7],a[140:143],a[228:231],a[4:7]
v_mfma_f32_16x16x32_bf16 a[36:39],a[140:143],a[236:239],a[36:39]
buffer_load_dwordx4 v53,s[28:31],s57 offen offset:1024 lds
v_mfma_f32_16x16x32_bf16 a[36:39],a[136:139],a[232:235],a[36:39]
v_mfma_f32_16x16x32_bf16 a[68:71],a[136:139],a[240:243],a[68:71]
v_mfma_f32_16x16x32_bf16 a[68:71],a[140:143],a[244:247],a[68:71]
v_mfma_f32_16x16x32_bf16 a[100:103],a[140:143],a[252:255],a[100:103]
v_mfma_f32_16x16x32_bf16 a[100:103],a[136:139],a[248:251],a[100:103]
v_mfma_f32_16x16x32_bf16 a[8:11],a[144:147],a[224:227],a[8:11]
buffer_load_dwordx4 v54,s[28:31],s57 offen offset:2048 lds
v_mfma_f32_16x16x32_bf16 a[8:11],a[148:151],a[228:231],a[8:11]
v_mfma_f32_16x16x32_bf16 a[40:43],a[148:151],a[236:239],a[40:43]
v_mfma_f32_16x16x32_bf16 a[40:43],a[144:147],a[232:235],a[40:43]
v_mfma_f32_16x16x32_bf16 a[72:75],a[144:147],a[240:243],a[72:75]
v_mfma_f32_16x16x32_bf16 a[72:75],a[148:151],a[244:247],a[72:75]
v_mfma_f32_16x16x32_bf16 a[104:107],a[148:151],a[252:255],a[104:107]
buffer_load_dwordx4 v55,s[28:31],s57 offen offset:3072 lds
v_mfma_f32_16x16x32_bf16 a[104:107],a[144:147],a[248:251],a[104:107]
s_add_u32 s57,s57,128
v_mfma_f32_16x16x32_bf16 a[12:15],a[152:155],a[224:227],a[12:15]
v_mfma_f32_16x16x32_bf16 a[12:15],a[156:159],a[228:231],a[12:15]
v_mfma_f32_16x16x32_bf16 a[44:47],a[156:159],a[236:239],a[44:47]
v_mfma_f32_16x16x32_bf16 a[44:47],a[152:155],a[232:235],a[44:47]
v_mfma_f32_16x16x32_bf16 a[76:79],a[152:155],a[240:243],a[76:79]
v_mfma_f32_16x16x32_bf16 a[76:79],a[156:159],a[244:247],a[76:79]
v_mfma_f32_16x16x32_bf16 a[108:111],a[156:159],a[252:255],a[108:111]
v_mfma_f32_16x16x32_bf16 a[108:111],a[152:155],a[248:251],a[108:111]
s_waitcnt vmcnt(17) lgkmcnt(0)
v_mfma_f32_16x16x32_bf16 a[16:19],a[160:163],a[224:227],a[16:19]
s_barrier
ds_read_b128 a[192:195],v20
v_mfma_f32_16x16x32_bf16 a[16:19],a[164:167],a[228:231],a[16:19]
ds_read_b128 a[196:199],v21
v_mfma_f32_16x16x32_bf16 a[48:51],a[164:167],a[236:239],a[48:51]
ds_read_b128 a[200:203],v20 offset:2048
v_mfma_f32_16x16x32_bf16 a[48:51],a[160:163],a[232:235],a[48:51]
ds_read_b128 a[204:207],v21 offset:2048
v_mfma_f32_16x16x32_bf16 a[80:83],a[160:163],a[240:243],a[80:83]
ds_read_b128 a[208:211],v20 offset:4096
v_mfma_f32_16x16x32_bf16 a[80:83],a[164:167],a[244:247],a[80:83]
ds_read_b128 a[212:215],v21 offset:4096
v_mfma_f32_16x16x32_bf16 a[112:115],a[164:167],a[252:255],a[112:115]
ds_read_b128 a[216:219],v20 offset:6144
v_mfma_f32_16x16x32_bf16 a[112:115],a[160:163],a[248:251],a[112:115]
ds_read_b128 a[220:223],v21 offset:6144
v_mfma_f32_16x16x32_bf16 a[20:23],a[168:171],a[224:227],a[20:23]
ds_read_b64_tr_b16 a[128:129],v22 offset:1024
v_mfma_f32_16x16x32_bf16 a[20:23],a[172:175],a[228:231],a[20:23]
ds_read_b64_tr_b16 a[130:131],v22 offset:3072
v_mfma_f32_16x16x32_bf16 a[52:55],a[172:175],a[236:239],a[52:55]
ds_read_b64_tr_b16 a[132:133],v22 offset:17408
v_mfma_f32_16x16x32_bf16 a[52:55],a[168:171],a[232:235],a[52:55]
ds_read_b64_tr_b16 a[134:135],v22 offset:19456
v_mfma_f32_16x16x32_bf16 a[84:87],a[168:171],a[240:243],a[84:87]
ds_read_b64_tr_b16 a[136:137],v22 offset:1032
v_mfma_f32_16x16x32_bf16 a[84:87],a[172:175],a[244:247],a[84:87]
ds_read_b64_tr_b16 a[138:139],v22 offset:3080
v_mfma_f32_16x16x32_bf16 a[116:119],a[172:175],a[252:255],a[116:119]
ds_read_b64_tr_b16 a[140:141],v22 offset:17416
v_mfma_f32_16x16x32_bf16 a[116:119],a[168:171],a[248:251],a[116:119]
ds_read_b64_tr_b16 a[142:143],v22 offset:19464
v_mfma_f32_16x16x32_bf16 a[24:27],a[176:179],a[224:227],a[24:27]
ds_read_b64_tr_b16 a[144:145],v22 offset:1280
v_mfma_f32_16x16x32_bf16 a[24:27],a[180:183],a[228:231],a[24:27]
ds_read_b64_tr_b16 a[146:147],v22 offset:3328
v_mfma_f32_16x16x32_bf16 a[56:59],a[180:183],a[236:239],a[56:59]
ds_read_b64_tr_b16 a[148:149],v22 offset:17664
v_mfma_f32_16x16x32_bf16 a[56:59],a[176:179],a[232:235],a[56:59]
ds_read_b64_tr_b16 a[150:151],v22 offset:19712
v_mfma_f32_16x16x32_bf16 a[88:91],a[176:179],a[240:243],a[88:91]
ds_read_b64_tr_b16 a[152:153],v22 offset:1288
v_mfma_f32_16x16x32_bf16 a[88:91],a[180:183],a[244:247],a[88:91]
ds_read_b64_tr_b16 a[154:155],v22 offset:3336
v_mfma_f32_16x16x32_bf16 a[120:123],a[180:183],a[252:255],a[120:123]
ds_read_b64_tr_b16 a[156:157],v22 offset:17672
)A"
R"A(v_mfma_f32_16x16x32_bf16 a[120:123],a[176:179],a[248:251],a[120:123]
ds_read_b64_tr_b16 a[158:159],v22 offset:19720
v_mfma_f32_16x16x32_bf16 a[28:31],a[184:187],a[224:227],a[28:31]
v_mfma_f32_16x16x32_bf16 a[28:31],a[188:191],a[228:231],a[28:31]
v_mfma_f32_16x16x32_bf16 a[60:63],a[188:191],a[236:239],a[60:63]
v_mfma_f32_16x16x32_bf16 a[60:63],a[184:187],a[232:235],a[60:63]
v_mfma_f32_16x16x32_bf16 a[92:95],a[184:187],a[240:243],a[92:95]
v_mfma_f32_16x16x32_bf16 a[92:95],a[188:191],a[244:247],a[92:95]
v_mfma_f32_16x16x32_bf16 a[124:127],a[188:191],a[252:255],a[124:127]
v_mfma_f32_16x16x32_bf16 a[124:127],a[184:187],a[248:251],a[124:127]
s_mov_b32 s53,3
L_qp_%=:
s_waitcnt lgkmcnt(1)
v_mfma_f32_16x16x32_bf16 v[128:131],a[128:131],a[192:195],v[128:131]
s_add_u32 m0,s58,0
s_add_u32 s60,s60,32768
ds_read_b64_tr_b16 a[160:161],v22 offset:1536
v_mfma_f32_16x16x32_bf16 v[128:131],a[132:135],a[196:199],v[128:131]
s_mov_b32 s90,L_att0_%=-L_pc_%=+128
buffer_load_dwordx4 v56,s[32:35],s56 offen lds
v_mfma_f32_16x16x32_bf16 v[136:139],a[132:135],a[204:207],v[136:139]
s_cmp_eq_u32 s60,131072
s_cselect_b32 s60,32768,s60
ds_read_b64_tr_b16 a[162:163],v22 offset:3584
v_mfma_f32_16x16x32_bf16 v[136:139],a[128:131],a[200:203],v[136:139]
s_add_u32 m0,s58,1024
ds_read_b64_tr_b16 a[164:165],v22 offset:17920
v_xor_b32 v20,131072,v20
v_mfma_f32_16x16x32_bf16 v[144:147],a[128:131],a[208:211],v[144:147]
s_sub_u32 s53,s53,1
ds_read_b64_tr_b16 a[166:167],v22 offset:19968
v_xor_b32 v21,131072,v21
v_mfma_f32_16x16x32_bf16 v[144:147],a[132:135],a[212:215],v[144:147]
ds_read_b64_tr_b16 a[168:169],v22 offset:1544
v_mfma_f32_16x16x32_bf16 v[152:155],a[132:135],a[220:223],v[152:155]
buffer_load_dwordx4 v57,s[32:35],s56 offen lds
v_mfma_f32_16x16x32_bf16 v[152:155],a[128:131],a[216:219],v[152:155]
ds_read_b64_tr_b16 a[170:171],v22 offset:3592
v_mfma_f32_16x16x32_bf16 v[132:135],a[136:139],a[192:195],v[132:135]
ds_read_b64_tr_b16 a[172:173],v22 offset:17928
v_mfma_f32_16x16x32_bf16 v[132:135],a[140:143],a[196:199],v[132:135]
s_add_u32 m0,s58,2048
ds_read_b64_tr_b16 a[174:175],v22 offset:19976
v_mfma_f32_16x16x32_bf16 v[140:143],a[140:143],a[204:207],v[140:143]
ds_read_b64_tr_b16 a[176:177],v22 offset:1792
v_mfma_f32_16x16x32_bf16 v[140:143],a[136:139],a[200:203],v[140:143]
ds_read_b64_tr_b16 a[178:179],v22 offset:3840
v_mfma_f32_16x16x32_bf16 v[148:151],a[136:139],a[208:211],v[148:151]
buffer_load_dwordx4 v58,s[32:35],s56 offen lds
v_mfma_f32_16x16x32_bf16 v[148:151],a[140:143],a[212:215],v[148:151]
ds_read_b64_tr_b16 a[180:181],v22 offset:18176
v_mfma_f32_16x16x32_bf16 v[156:159],a[140:143],a[220:223],v[156:159]
s_add_u32 m0,s58,3072
ds_read_b64_tr_b16 a[182:183],v22 offset:20224
v_mfma_f32_16x16x32_bf16 v[156:159],a[136:139],a[216:219],v[156:159]
ds_read_b64_tr_b16 a[184:185],v22 offset:1800
v_mfma_f32_16x16x32_bf16 v[160:163],a[144:147],a[192:195],v[160:163]
ds_read_b64_tr_b16 a[186:187],v22 offset:3848
v_mfma_f32_16x16x32_bf16 v[160:163],a[148:151],a[196:199],v[160:163]
buffer_load_dwordx4 v59,s[32:35],s56 offen lds
v_mfma_f32_16x16x32_bf16 v[168:171],a[148:151],a[204:207],v[168:171]
ds_read_b64_tr_b16 a[188:189],v22 offset:18184
v_mfma_f32_16x16x32_bf16 v[168:171],a[144:147],a[200:203],v[168:171]
ds_read_b64_tr_b16 a[190:191],v22 offset:20232
v_mfma_f32_16x16x32_bf16 v[176:179],a[144:147],a[208:211],v[176:179]
s_add_u32 m0,s58,4096
ds_read_b64_tr_b16 a[128:129],v22
v_mfma_f32_16x16x32_bf16 v[176:179],a[148:151],a[212:215],v[176:179]
ds_read_b64_tr_b16 a[130:131],v22 offset:2048
v_mfma_f32_16x16x32_bf16 v[184:187],a[148:151],a[220:223],v[184:187]
ds_read_b64_tr_b16 a[132:133],v22 offset:16384
v_mfma_f32_16x16x32_bf16 v[184:187],a[144:147],a[216:219],v[184:187]
buffer_load_dwordx4 v60,s[32:35],s56 offen lds
v_mfma_f32_16x16x32_bf16 v[164:167],a[152:155],a[192:195],v[164:167]
s_waitcnt lgkmcnt(5)
ds_read_b64_tr_b16 a[134:135],v22 offset:18432
v_mfma_f32_16x16x32_bf16 v[164:167],a[156:159],a[196:199],v[164:167]
s_add_u32 m0,s58,5120
ds_read_b64_tr_b16 a[136:137],v22 offset:8
v_mfma_f32_16x16x32_bf16 v[172:175],a[156:159],a[204:207],v[172:175]
ds_read_b64_tr_b16 a[138:139],v22 offset:2056
v_mfma_f32_16x16x32_bf16 v[172:175],a[152:155],a[200:203],v[172:175]
ds_read_b64_tr_b16 a[140:141],v22 offset:16392
v_mfma_f32_16x16x32_bf16 v[180:183],a[152:155],a[208:211],v[180:183]
buffer_load_dwordx4 v61,s[32:35],s56 offen lds
v_mfma_f32_16x16x32_bf16 v[180:183],a[156:159],a[212:215],v[180:183]
ds_read_b64_tr_b16 a[142:143],v22 offset:18440
v_mfma_f32_16x16x32_bf16 v[188:191],a[156:159],a[220:223],v[188:191]
ds_read_b64_tr_b16 a[144:145],v22 offset:256
v_mfma_f32_16x16x32_bf16 v[188:191],a[152:155],a[216:219],v[188:191]
s_add_u32 m0,s58,6144
ds_read_b64_tr_b16 a[146:147],v22 offset:2304
v_mfma_f32_16x16x32_bf16 v[192:195],a[160:163],a[192:195],v[192:195]
ds_read_b64_tr_b16 a[148:149],v22 offset:16640
v_mfma_f32_16x16x32_bf16 v[192:195],a[164:167],a[196:199],v[192:195]
ds_read_b64_tr_b16 a[150:151],v22 offset:18688
v_mfma_f32_16x16x32_bf16 v[200:203],a[164:167],a[204:207],v[200:203]
buffer_load_dwordx4 v62,s[32:35],s56 offen lds
v_mfma_f32_16x16x32_bf16 v[200:203],a[160:163],a[200:203],v[200:203]
ds_read_b64_tr_b16 a[152:153],v22 offset:264
v_mfma_f32_16x16x32_bf16 v[208:211],a[160:163],a[208:211],v[208:211]
s_add_u32 m0,s58,7168
s_add_u32 s58,s58,32768
ds_read_b64_tr_b16 a[154:155],v22 offset:2312
v_mfma_f32_16x16x32_bf16 v[208:211],a[164:167],a[212:215],v[208:211]
s_cmp_ge_u32 s58,131072
s_cselect_b32 s62,98304,0
ds_read_b64_tr_b16 a[156:157],v22 offset:16648
v_mfma_f32_16x16x32_bf16 v[216:219],a[164:167],a[220:223],v[216:219]
s_sub_u32 s58,s58,s62
ds_read_b64_tr_b16 a[158:159],v22 offset:18696
v_mfma_f32_16x16x32_bf16 v[216:219],a[160:163],a[216:219],v[216:219]
buffer_load_dwordx4 v63,s[32:35],s56 offen lds
v_mfma_f32_16x16x32_bf16 v[196:199],a[168:171],a[192:195],v[196:199]
s_add_u32 s56,s56,524288
ds_read_b64_tr_b16 a[160:161],v22 offset:512
v_mfma_f32_16x16x32_bf16 v[196:199],a[172:175],a[196:199],v[196:199]
ds_read_b64_tr_b16 a[162:163],v22 offset:2560
v_mfma_f32_16x16x32_bf16 v[204:207],a[172:175],a[204:207],v[204:207]
s_add_u32 m0,s59,0
ds_read_b64_tr_b16 a[164:165],v22 offset:16896
v_mfma_f32_16x16x32_bf16 v[204:207],a[168:171],a[200:203],v[204:207]
ds_read_b64_tr_b16 a[166:167],v22 offset:18944
v_mfma_f32_16x16x32_bf16 v[212:215],a[168:171],a[208:211],v[212:215]
v_mfma_f32_16x16x32_bf16 v[212:215],a[172:175],a[212:215],v[212:215]
buffer_load_dwordx4 v48,s[28:31],s57 offen lds
v_mfma_f32_16x16x32_bf16 v[220:223],a[172:175],a[220:223],v[220:223]
ds_read_b64_tr_b16 a[172:173],v22 offset:16904
v_mfma_f32_16x16x32_bf16 v[220:223],a[168:171],a[216:219],v[220:223]
ds_read_b64_tr_b16 a[168:169],v22 offset:520
v_mfma_f32_16x16x32_bf16 v[224:227],a[176:179],a[192:195],v[224:227]
ds_read_b64_tr_b16 a[170:171],v22 offset:2568
v_mfma_f32_16x16x32_bf16 v[224:227],a[180:183],a[196:199],v[224:227]
ds_read_b64_tr_b16 a[174:175],v22 offset:18952
v_mfma_f32_16x16x32_bf16 v[232:235],a[180:183],a[204:207],v[232:235]
buffer_load_dwordx4 v49,s[28:31],s57 offen offset:1024 lds
v_mfma_f32_16x16x32_bf16 v[232:235],a[176:179],a[200:203],v[232:235]
v_mfma_f32_16x16x32_bf16 v[240:243],a[176:179],a[208:211],v[240:243]
v_mfma_f32_16x16x32_bf16 v[240:243],a[180:183],a[212:215],v[240:243]
v_mfma_f32_16x16x32_bf16 v[248:251],a[180:183],a[220:223],v[248:251]
ds_read_b64_tr_b16 a[180:181],v22 offset:17152
v_mfma_f32_16x16x32_bf16 v[248:251],a[176:179],a[216:219],v[248:251]
ds_read_b64_tr_b16 a[176:177],v22 offset:768
v_mfma_f32_16x16x32_bf16 v[228:231],a[184:187],a[192:195],v[228:231]
s_waitcnt lgkmcnt(3)
buffer_load_dwordx4 v50,s[28:31],s57 offen offset:2048 lds
v_mfma_f32_16x16x32_bf16 v[228:231],a[188:191],a[196:199],v[228:231]
ds_read_b64_tr_b16 a[178:179],v22 offset:2816
v_mfma_f32_16x16x32_bf16 v[236:239],a[188:191],a[204:207],v[236:239]
ds_read_b64_tr_b16 a[182:183],v22 offset:19200
v_mfma_f32_16x16x32_bf16 v[236:239],a[184:187],a[200:203],v[236:239]
v_mfma_f32_16x16x32_bf16 v[244:247],a[184:187],a[208:211],v[244:247]
v_mfma_f32_16x16x32_bf16 v[244:247],a[188:191],a[212:215],v[244:247]
buffer_load_dwordx4 v51,s[28:31],s57 offen offset:3072 lds
v_mfma_f32_16x16x32_bf16 v[252:255],a[188:191],a[220:223],v[252:255]
ds_read_b64_tr_b16 a[188:189],v22 offset:17160
v_mfma_f32_16x16x32_bf16 v[252:255],a[184:187],a[216:219],v[252:255]
ds_read_b64_tr_b16 a[184:185],v22 offset:776
v_mfma_f32_16x16x32_bf16 a[0:3],a[128:131],a[192:195],a[0:3]
s_add_u32 m0,s59,4096
s_xor_b32 s59,s59,131072
ds_read_b64_tr_b16 a[186:187],v22 offset:2824
v_mfma_f32_16x16x32_bf16 a[0:3],a[132:135],a[196:199],a[0:3]
ds_read_b64_tr_b16 a[190:191],v22 offset:19208
v_add_u32 v22,s60,v23
v_mfma_f32_16x16x32_bf16 a[32:35],a[132:135],a[204:207],a[32:35]
s_add_u32 s60,s60,32768
s_cmp_eq_u32 s60,131072
s_cselect_b32 s60,32768,s60
v_mfma_f32_16x16x32_bf16 a[32:35],a[128:131],a[200:203],a[32:35]
buffer_load_dwordx4 v52,s[28:31],s57 offen lds
v_mfma_f32_16x16x32_bf16 a[64:67],a[128:131],a[208:211],a[64:67]
v_mfma_f32_16x16x32_bf16 a[64:67],a[132:135],a[212:215],a[64:67]
v_mfma_f32_16x16x32_bf16 a[96:99],a[132:135],a[220:223],a[96:99]
v_mfma_f32_16x16x32_bf16 a[96:99],a[128:131],a[216:219],a[96:99]
v_mfma_f32_16x16x32_bf16 a[4:7],a[136:139],a[192:195],a[4:7]
buffer_load_dwordx4 v53,s[28:31],s57 offen offset:1024 lds
v_mfma_f32_16x16x32_bf16 a[4:7],a[140:143],a[196:199],a[4:7]
v_mfma_f32_16x16x32_bf16 a[36:39],a[140:143],a[204:207],a[36:39]
v_mfma_f32_16x16x32_bf16 a[36:39],a[136:139],a[200:203],a[36:39]
v_mfma_f32_16x16x32_bf16 a[68:71],a[136:139],a[208:211],a[68:71]
v_mfma_f32_16x16x32_bf16 a[68:71],a[140:143],a[212:215],a[68:71]
v_mfma_f32_16x16x32_bf16 a[100:103],a[140:143],a[220:223],a[100:103]
buffer_load_dwordx4 v54,s[28:31],s57 offen offset:2048 lds
v_mfma_f32_16x16x32_bf16 a[100:103],a[136:139],a[216:219],a[100:103]
v_mfma_f32_16x16x32_bf16 a[8:11],a[144:147],a[192:195],a[8:11]
v_mfma_f32_16x16x32_bf16 a[8:11],a[148:151],a[196:199],a[8:11]
v_mfma_f32_16x16x32_bf16 a[40:43],a[148:151],a[204:207],a[40:43]
v_mfma_f32_16x16x32_bf16 a[40:43],a[144:147],a[200:203],a[40:43]
buffer_load_dwordx4 v55,s[28:31],s57 offen offset:3072 lds
v_mfma_f32_16x16x32_bf16 a[72:75],a[144:147],a[208:211],a[72:75]
s_add_u32 s57,s57,128
v_mfma_f32_16x16x32_bf16 a[72:75],a[148:151],a[212:215],a[72:75]
v_mfma_f32_16x16x32_bf16 a[104:107],a[148:151],a[220:223],a[104:107]
v_mfma_f32_16x16x32_bf16 a[104:107],a[144:147],a[216:219],a[104:107]
v_mfma_f32_16x16x32_bf16 a[12:15],a[152:155],a[192:195],a[12:15]
v_mfma_f32_16x16x32_bf16 a[12:15],a[156:159],a[196:199],a[12:15]
v_mfma_f32_16x16x32_bf16 a[44:47],a[156:159],a[204:207],a[44:47]
buffer_load_dword v16,v15,s[72:75],s83 offen
v_mfma_f32_16x16x32_bf16 a[44:47],a[152:155],a[200:203],a[44:47]
s_add_u32 s83,s83,896
v_mfma_f32_16x16x32_bf16 a[76:79],a[152:155],a[208:211],a[76:79]
v_mfma_f32_16x16x32_bf16 a[76:79],a[156:159],a[212:215],a[76:79]
v_mfma_f32_16x16x32_bf16 a[108:111],a[156:159],a[220:223],a[108:111]
v_mfma_f32_16x16x32_bf16 a[108:111],a[152:155],a[216:219],a[108:111]
s_waitcnt vmcnt(17) lgkmcnt(0)
v_mfma_f32_16x16x32_bf16 a[16:19],a[160:163],a[192:195],a[16:19]
s_barrier
ds_read_b128 a[224:227],v20
v_mfma_f32_16x16x32_bf16 a[16:19],a[164:167],a[196:199],a[16:19]
ds_read_b64_tr_b16 a[128:129],v22 offset:1024
v_mfma_f32_16x16x32_bf16 a[48:51],a[164:167],a[204:207],a[48:51]
ds_read_b64_tr_b16 a[130:131],v22 offset:3072
v_mfma_f32_16x16x32_bf16 a[48:51],a[160:163],a[200:203],a[48:51]
ds_read_b128 a[228:231],v21
v_mfma_f32_16x16x32_bf16 a[80:83],a[160:163],a[208:211],a[80:83]
ds_read_b64_tr_b16 a[132:133],v22 offset:17408
v_mfma_f32_16x16x32_bf16 a[80:83],a[164:167],a[212:215],a[80:83]
ds_read_b64_tr_b16 a[134:135],v22 offset:19456
v_mfma_f32_16x16x32_bf16 a[112:115],a[164:167],a[220:223],a[112:115]
ds_read_b128 a[232:235],v20 offset:2048
v_mfma_f32_16x16x32_bf16 a[112:115],a[160:163],a[216:219],a[112:115]
ds_read_b128 a[236:239],v21 offset:2048
v_mfma_f32_16x16x32_bf16 a[20:23],a[168:171],a[192:195],a[20:23]
ds_read_b128 a[240:243],v20 offset:4096
v_mfma_f32_16x16x32_bf16 a[20:23],a[172:175],a[196:199],a[20:23]
)A"
R"A(ds_read_b128 a[244:247],v21 offset:4096
v_mfma_f32_16x16x32_bf16 a[52:55],a[172:175],a[204:207],a[52:55]
ds_read_b128 a[248:251],v20 offset:6144
v_xor_b32 v20,131072,v20
v_mfma_f32_16x16x32_bf16 a[52:55],a[168:171],a[200:203],a[52:55]
ds_read_b128 a[252:255],v21 offset:6144
v_xor_b32 v21,131072,v21
v_mfma_f32_16x16x32_bf16 a[84:87],a[168:171],a[208:211],a[84:87]
ds_read_b64_tr_b16 a[136:137],v22 offset:1032
v_mfma_f32_16x16x32_bf16 a[84:87],a[172:175],a[212:215],a[84:87]
ds_read_b64_tr_b16 a[138:139],v22 offset:3080
v_mfma_f32_16x16x32_bf16 a[116:119],a[172:175],a[220:223],a[116:119]
ds_read_b64_tr_b16 a[140:141],v22 offset:17416
v_mfma_f32_16x16x32_bf16 a[116:119],a[168:171],a[216:219],a[116:119]
ds_read_b64_tr_b16 a[142:143],v22 offset:19464
v_mfma_f32_16x16x32_bf16 a[24:27],a[176:179],a[192:195],a[24:27]
ds_read_b64_tr_b16 a[144:145],v22 offset:1280
v_mfma_f32_16x16x32_bf16 a[24:27],a[180:183],a[196:199],a[24:27]
ds_read_b64_tr_b16 a[146:147],v22 offset:3328
v_mfma_f32_16x16x32_bf16 a[56:59],a[180:183],a[204:207],a[56:59]
ds_read_b64_tr_b16 a[148:149],v22 offset:17664
v_mfma_f32_16x16x32_bf16 a[56:59],a[176:179],a[200:203],a[56:59]
ds_read_b64_tr_b16 a[150:151],v22 offset:19712
v_mfma_f32_16x16x32_bf16 a[88:91],a[176:179],a[208:211],a[88:91]
ds_read_b64_tr_b16 a[152:153],v22 offset:1288
v_mfma_f32_16x16x32_bf16 a[88:91],a[180:183],a[212:215],a[88:91]
ds_read_b64_tr_b16 a[154:155],v22 offset:3336
v_mfma_f32_16x16x32_bf16 a[120:123],a[180:183],a[220:223],a[120:123]
ds_read_b64_tr_b16 a[156:157],v22 offset:17672
v_mfma_f32_16x16x32_bf16 a[120:123],a[176:179],a[216:219],a[120:123]
ds_read_b64_tr_b16 a[158:159],v22 offset:19720
v_mfma_f32_16x16x32_bf16 a[28:31],a[184:187],a[192:195],a[28:31]
ds_read_b64_tr_b16 a[160:161],v22 offset:1536
v_mfma_f32_16x16x32_bf16 a[28:31],a[188:191],a[196:199],a[28:31]
ds_read_b64_tr_b16 a[162:163],v22 offset:3584
v_mfma_f32_16x16x32_bf16 a[60:63],a[188:191],a[204:207],a[60:63]
ds_read_b64_tr_b16 a[164:165],v22 offset:17920
v_mfma_f32_16x16x32_bf16 a[60:63],a[184:187],a[200:203],a[60:63]
ds_read_b64_tr_b16 a[166:167],v22 offset:19968
v_mfma_f32_16x16x32_bf16 a[92:95],a[184:187],a[208:211],a[92:95]
ds_read_b64_tr_b16 a[168:169],v22 offset:1544
v_mfma_f32_16x16x32_bf16 a[92:95],a[188:191],a[212:215],a[92:95]
ds_read_b64_tr_b16 a[170:171],v22 offset:3592
v_mfma_f32_16x16x32_bf16 a[124:127],a[188:191],a[220:223],a[124:127]
s_add_u32 m0,s58,0
ds_read_b64_tr_b16 a[172:173],v22 offset:17928
v_mfma_f32_16x16x32_bf16 a[124:127],a[184:187],a[216:219],a[124:127]
s_waitcnt lgkmcnt(7)
ds_read_b64_tr_b16 a[174:175],v22 offset:19976
v_mfma_f32_16x16x32_bf16 v[128:131],a[128:131],a[224:227],v[128:131]
ds_read_b64_tr_b16 a[176:177],v22 offset:1792
v_mfma_f32_16x16x32_bf16 v[128:131],a[132:135],a[228:231],v[128:131]
buffer_load_dwordx4 v56,s[32:35],s56 offen lds
v_mfma_f32_16x16x32_bf16 v[136:139],a[132:135],a[236:239],v[136:139]
ds_read_b64_tr_b16 a[178:179],v22 offset:3840
v_mfma_f32_16x16x32_bf16 v[136:139],a[128:131],a[232:235],v[136:139]
s_add_u32 m0,s58,1024
ds_read_b64_tr_b16 a[180:181],v22 offset:18176
v_mfma_f32_16x16x32_bf16 v[144:147],a[128:131],a[240:243],v[144:147]
ds_read_b64_tr_b16 a[182:183],v22 offset:20224
v_mfma_f32_16x16x32_bf16 v[144:147],a[132:135],a[244:247],v[144:147]
ds_read_b64_tr_b16 a[184:185],v22 offset:1800
v_mfma_f32_16x16x32_bf16 v[152:155],a[132:135],a[252:255],v[152:155]
buffer_load_dwordx4 v57,s[32:35],s56 offen lds
v_mfma_f32_16x16x32_bf16 v[152:155],a[128:131],a[248:251],v[152:155]
ds_read_b64_tr_b16 a[186:187],v22 offset:3848
v_mfma_f32_16x16x32_bf16 v[132:135],a[136:139],a[224:227],v[132:135]
ds_read_b64_tr_b16 a[188:189],v22 offset:18184
v_mfma_f32_16x16x32_bf16 v[132:135],a[140:143],a[228:231],v[132:135]
s_add_u32 m0,s58,2048
ds_read_b64_tr_b16 a[190:191],v22 offset:20232
v_mfma_f32_16x16x32_bf16 v[140:143],a[140:143],a[236:239],v[140:143]
ds_read_b64_tr_b16 a[128:129],v22
v_mfma_f32_16x16x32_bf16 v[140:143],a[136:139],a[232:235],v[140:143]
ds_read_b64_tr_b16 a[130:131],v22 offset:2048
v_mfma_f32_16x16x32_bf16 v[148:151],a[136:139],a[240:243],v[148:151]
buffer_load_dwordx4 v58,s[32:35],s56 offen lds
v_mfma_f32_16x16x32_bf16 v[148:151],a[140:143],a[244:247],v[148:151]
ds_read_b64_tr_b16 a[132:133],v22 offset:16384
v_mfma_f32_16x16x32_bf16 v[156:159],a[140:143],a[252:255],v[156:159]
s_add_u32 m0,s58,3072
ds_read_b64_tr_b16 a[134:135],v22 offset:18432
v_mfma_f32_16x16x32_bf16 v[156:159],a[136:139],a[248:251],v[156:159]
ds_read_b64_tr_b16 a[136:137],v22 offset:8
v_mfma_f32_16x16x32_bf16 v[160:163],a[144:147],a[224:227],v[160:163]
ds_read_b64_tr_b16 a[138:139],v22 offset:2056
v_mfma_f32_16x16x32_bf16 v[160:163],a[148:151],a[228:231],v[160:163]
buffer_load_dwordx4 v59,s[32:35],s56 offen lds
v_mfma_f32_16x16x32_bf16 v[168:171],a[148:151],a[236:239],v[168:171]
ds_read_b64_tr_b16 a[140:141],v22 offset:16392
v_mfma_f32_16x16x32_bf16 v[168:171],a[144:147],a[232:235],v[168:171]
ds_read_b64_tr_b16 a[142:143],v22 offset:18440
v_mfma_f32_16x16x32_bf16 v[176:179],a[144:147],a[240:243],v[176:179]
s_add_u32 m0,s58,4096
v_mfma_f32_16x16x32_bf16 v[176:179],a[148:151],a[244:247],v[176:179]
v_mfma_f32_16x16x32_bf16 v[184:187],a[148:151],a[252:255],v[184:187]
ds_read_b64_tr_b16 a[148:149],v22 offset:16640
v_mfma_f32_16x16x32_bf16 v[184:187],a[144:147],a[248:251],v[184:187]
buffer_load_dwordx4 v60,s[32:35],s56 offen lds
v_mfma_f32_16x16x32_bf16 v[164:167],a[152:155],a[224:227],v[164:167]
ds_read_b64_tr_b16 a[144:145],v22 offset:256
v_mfma_f32_16x16x32_bf16 v[164:167],a[156:159],a[228:231],v[164:167]
s_add_u32 m0,s58,5120
ds_read_b64_tr_b16 a[146:147],v22 offset:2304
v_mfma_f32_16x16x32_bf16 v[172:175],a[156:159],a[236:239],v[172:175]
ds_read_b64_tr_b16 a[150:151],v22 offset:18688
v_mfma_f32_16x16x32_bf16 v[172:175],a[152:155],a[232:235],v[172:175]
v_mfma_f32_16x16x32_bf16 v[180:183],a[152:155],a[240:243],v[180:183]
buffer_load_dwordx4 v61,s[32:35],s56 offen lds
v_mfma_f32_16x16x32_bf16 v[180:183],a[156:159],a[244:247],v[180:183]
v_mfma_f32_16x16x32_bf16 v[188:191],a[156:159],a[252:255],v[188:191]
ds_read_b64_tr_b16 a[156:157],v22 offset:16648
v_mfma_f32_16x16x32_bf16 v[188:191],a[152:155],a[248:251],v[188:191]
s_add_u32 m0,s58,6144
ds_read_b64_tr_b16 a[152:153],v22 offset:264
s_waitcnt lgkmcnt(5)
v_mfma_f32_16x16x32_bf16 v[192:195],a[160:163],a[224:227],v[192:195]
ds_read_b64_tr_b16 a[154:155],v22 offset:2312
v_mfma_f32_16x16x32_bf16 v[192:195],a[164:167],a[228:231],v[192:195]
ds_read_b64_tr_b16 a[158:159],v22 offset:18696
v_mfma_f32_16x16x32_bf16 v[200:203],a[164:167],a[236:239],v[200:203]
buffer_load_dwordx4 v62,s[32:35],s56 offen lds
v_mfma_f32_16x16x32_bf16 v[200:203],a[160:163],a[232:235],v[200:203]
v_mfma_f32_16x16x32_bf16 v[208:211],a[160:163],a[240:243],v[208:211]
s_add_u32 m0,s58,7168
s_add_u32 s58,s58,32768
v_mfma_f32_16x16x32_bf16 v[208:211],a[164:167],a[244:247],v[208:211]
s_cmp_ge_u32 s58,131072
s_cselect_b32 s62,98304,0
s_sub_u32 s58,s58,s62
v_mfma_f32_16x16x32_bf16 v[216:219],a[164:167],a[252:255],v[216:219]
ds_read_b64_tr_b16 a[164:165],v22 offset:16896
v_mfma_f32_16x16x32_bf16 v[216:219],a[160:163],a[248:251],v[216:219]
buffer_load_dwordx4 v63,s[32:35],s56 offen lds
v_mfma_f32_16x16x32_bf16 v[196:199],a[168:171],a[224:227],v[196:199]
s_add_u32 s56,s56,524288
ds_read_b64_tr_b16 a[160:161],v22 offset:512
v_mfma_f32_16x16x32_bf16 v[196:199],a[172:175],a[228:231],v[196:199]
ds_read_b64_tr_b16 a[162:163],v22 offset:2560
v_mfma_f32_16x16x32_bf16 v[204:207],a[172:175],a[236:239],v[204:207]
s_add_u32 m0,s59,0
ds_read_b64_tr_b16 a[166:167],v22 offset:18944
v_mfma_f32_16x16x32_bf16 v[204:207],a[168:171],a[232:235],v[204:207]
v_mfma_f32_16x16x32_bf16 v[212:215],a[168:171],a[240:243],v[212:215]
v_mfma_f32_16x16x32_bf16 v[212:215],a[172:175],a[244:247],v[212:215]
buffer_load_dwordx4 v48,s[28:31],s57 offen lds
v_mfma_f32_16x16x32_bf16 v[220:223],a[172:175],a[252:255],v[220:223]
ds_read_b64_tr_b16 a[172:173],v22 offset:16904
v_mfma_f32_16x16x32_bf16 v[220:223],a[168:171],a[248:251],v[220:223]
ds_read_b64_tr_b16 a[168:169],v22 offset:520
v_mfma_f32_16x16x32_bf16 v[224:227],a[176:179],a[224:227],v[224:227]
ds_read_b64_tr_b16 a[170:171],v22 offset:2568
v_mfma_f32_16x16x32_bf16 v[224:227],a[180:183],a[228:231],v[224:227]
ds_read_b64_tr_b16 a[174:175],v22 offset:18952
v_mfma_f32_16x16x32_bf16 v[232:235],a[180:183],a[236:239],v[232:235]
buffer_load_dwordx4 v49,s[28:31],s57 offen offset:1024 lds
v_mfma_f32_16x16x32_bf16 v[232:235],a[176:179],a[232:235],v[232:235]
v_mfma_f32_16x16x32_bf16 v[240:243],a[176:179],a[240:243],v[240:243]
v_mfma_f32_16x16x32_bf16 v[240:243],a[180:183],a[244:247],v[240:243]
v_mfma_f32_16x16x32_bf16 v[248:251],a[180:183],a[252:255],v[248:251]
ds_read_b64_tr_b16 a[180:181],v22 offset:17152
v_mfma_f32_16x16x32_bf16 v[248:251],a[176:179],a[248:251],v[248:251]
ds_read_b64_tr_b16 a[176:177],v22 offset:768
v_mfma_f32_16x16x32_bf16 v[228:231],a[184:187],a[224:227],v[228:231]
buffer_load_dwordx4 v50,s[28:31],s57 offen offset:2048 lds
v_mfma_f32_16x16x32_bf16 v[228:231],a[188:191],a[228:231],v[228:231]
ds_read_b64_tr_b16 a[178:179],v22 offset:2816
v_mfma_f32_16x16x32_bf16 v[236:239],a[188:191],a[236:239],v[236:239]
ds_read_b64_tr_b16 a[182:183],v22 offset:19200
v_mfma_f32_16x16x32_bf16 v[236:239],a[184:187],a[232:235],v[236:239]
v_mfma_f32_16x16x32_bf16 v[244:247],a[184:187],a[240:243],v[244:247]
v_mfma_f32_16x16x32_bf16 v[244:247],a[188:191],a[244:247],v[244:247]
buffer_load_dwordx4 v51,s[28:31],s57 offen offset:3072 lds
v_mfma_f32_16x16x32_bf16 v[252:255],a[188:191],a[252:255],v[252:255]
ds_read_b64_tr_b16 a[188:189],v22 offset:17160
v_mfma_f32_16x16x32_bf16 v[252:255],a[184:187],a[248:251],v[252:255]
ds_read_b64_tr_b16 a[184:185],v22 offset:776
v_mfma_f32_16x16x32_bf16 a[0:3],a[128:131],a[224:227],a[0:3]
s_add_u32 m0,s59,4096
s_xor_b32 s59,s59,131072
ds_read_b64_tr_b16 a[186:187],v22 offset:2824
v_mfma_f32_16x16x32_bf16 a[0:3],a[132:135],a[228:231],a[0:3]
ds_read_b64_tr_b16 a[190:191],v22 offset:19208
v_add_u32 v22,s60,v23
v_mfma_f32_16x16x32_bf16 a[32:35],a[132:135],a[236:239],a[32:35]
v_mfma_f32_16x16x32_bf16 a[32:35],a[128:131],a[232:235],a[32:35]
buffer_load_dwordx4 v52,s[28:31],s57 offen lds
v_mfma_f32_16x16x32_bf16 a[64:67],a[128:131],a[240:243],a[64:67]
v_mfma_f32_16x16x32_bf16 a[64:67],a[132:135],a[244:247],a[64:67]
v_mfma_f32_16x16x32_bf16 a[96:99],a[132:135],a[252:255],a[96:99]
v_mfma_f32_16x16x32_bf16 a[96:99],a[128:131],a[248:251],a[96:99]
v_mfma_f32_16x16x32_bf16 a[4:7],a[136:139],a[224:227],a[4:7]
buffer_load_dwordx4 v53,s[28:31],s57 offen offset:1024 lds
v_mfma_f32_16x16x32_bf16 a[4:7],a[140:143],a[228:231],a[4:7]
v_mfma_f32_16x16x32_bf16 a[36:39],a[140:143],a[236:239],a[36:39]
v_mfma_f32_16x16x32_bf16 a[36:39],a[136:139],a[232:235],a[36:39]
v_mfma_f32_16x16x32_bf16 a[68:71],a[136:139],a[240:243],a[68:71]
v_mfma_f32_16x16x32_bf16 a[68:71],a[140:143],a[244:247],a[68:71]
v_mfma_f32_16x16x32_bf16 a[100:103],a[140:143],a[252:255],a[100:103]
buffer_load_dwordx4 v54,s[28:31],s57 offen offset:2048 lds
v_mfma_f32_16x16x32_bf16 a[100:103],a[136:139],a[248:251],a[100:103]
s_waitcnt lgkmcnt(0)
v_mfma_f32_16x16x32_bf16 a[8:11],a[144:147],a[224:227],a[8:11]
v_mfma_f32_16x16x32_bf16 a[8:11],a[148:151],a[228:231],a[8:11]
v_mfma_f32_16x16x32_bf16 a[40:43],a[148:151],a[236:239],a[40:43]
v_mfma_f32_16x16x32_bf16 a[40:43],a[144:147],a[232:235],a[40:43]
buffer_load_dwordx4 v55,s[28:31],s57 offen offset:3072 lds
v_mfma_f32_16x16x32_bf16 a[72:75],a[144:147],a[240:243],a[72:75]
s_add_u32 s57,s57,128
v_mfma_f32_16x16x32_bf16 a[72:75],a[148:151],a[244:247],a[72:75]
v_mfma_f32_16x16x32_bf16 a[104:107],a[148:151],a[252:255],a[104:107]
v_mfma_f32_16x16x32_bf16 a[104:107],a[144:147],a[248:251],a[104:107]
v_mfma_f32_16x16x32_bf16 a[12:15],a[152:155],a[224:227],a[12:15]
v_mfma_f32_16x16x32_bf16 a[12:15],a[156:159],a[228:231],a[12:15]
v_mfma_f32_16x16x32_bf16 a[44:47],a[156:159],a[236:239],a[44:47]
buffer_load_dword v16,v15,s[72:75],s83 offen
v_mfma_f32_16x16x32_bf16 a[44:47],a[152:155],a[232:235],a[44:47]
s_add_u32 s83,s83,896
v_mfma_f32_16x16x32_bf16 a[76:79],a[152:155],a[240:243],a[76:79]
)A"
R"A(v_mfma_f32_16x16x32_bf16 a[76:79],a[156:159],a[244:247],a[76:79]
v_mfma_f32_16x16x32_bf16 a[108:111],a[156:159],a[252:255],a[108:111]
v_mfma_f32_16x16x32_bf16 a[108:111],a[152:155],a[248:251],a[108:111]
s_waitcnt vmcnt(17) lgkmcnt(0)
v_mfma_f32_16x16x32_bf16 a[16:19],a[160:163],a[224:227],a[16:19]
s_barrier
ds_read_b128 a[192:195],v20
v_mfma_f32_16x16x32_bf16 a[16:19],a[164:167],a[228:231],a[16:19]
ds_read_b128 a[196:199],v21
v_mfma_f32_16x16x32_bf16 a[48:51],a[164:167],a[236:239],a[48:51]
ds_read_b128 a[200:203],v20 offset:2048
v_mfma_f32_16x16x32_bf16 a[48:51],a[160:163],a[232:235],a[48:51]
ds_read_b128 a[204:207],v21 offset:2048
v_mfma_f32_16x16x32_bf16 a[80:83],a[160:163],a[240:243],a[80:83]
ds_read_b128 a[208:211],v20 offset:4096
v_mfma_f32_16x16x32_bf16 a[80:83],a[164:167],a[244:247],a[80:83]
ds_read_b128 a[212:215],v21 offset:4096
v_mfma_f32_16x16x32_bf16 a[112:115],a[164:167],a[252:255],a[112:115]
buffer_load_dword v26,v24,s[88:91],s93 offen
v_mfma_f32_16x16x32_bf16 a[112:115],a[160:163],a[248:251],a[112:115]
ds_read_b128 a[216:219],v20 offset:6144
v_mfma_f32_16x16x32_bf16 a[20:23],a[168:171],a[224:227],a[20:23]
ds_read_b128 a[220:223],v21 offset:6144
v_mfma_f32_16x16x32_bf16 a[20:23],a[172:175],a[228:231],a[20:23]
ds_read_b64_tr_b16 a[128:129],v22 offset:1024
v_mfma_f32_16x16x32_bf16 a[52:55],a[172:175],a[236:239],a[52:55]
ds_read_b64_tr_b16 a[130:131],v22 offset:3072
v_mfma_f32_16x16x32_bf16 a[52:55],a[168:171],a[232:235],a[52:55]
ds_read_b64_tr_b16 a[132:133],v22 offset:17408
v_mfma_f32_16x16x32_bf16 a[84:87],a[168:171],a[240:243],a[84:87]
ds_read_b64_tr_b16 a[134:135],v22 offset:19456
v_mfma_f32_16x16x32_bf16 a[84:87],a[172:175],a[244:247],a[84:87]
ds_read_b64_tr_b16 a[136:137],v22 offset:1032
v_mfma_f32_16x16x32_bf16 a[116:119],a[172:175],a[252:255],a[116:119]
ds_read_b64_tr_b16 a[138:139],v22 offset:3080
v_mfma_f32_16x16x32_bf16 a[116:119],a[168:171],a[248:251],a[116:119]
ds_read_b64_tr_b16 a[140:141],v22 offset:17416
v_mfma_f32_16x16x32_bf16 a[24:27],a[176:179],a[224:227],a[24:27]
ds_read_b64_tr_b16 a[142:143],v22 offset:19464
v_mfma_f32_16x16x32_bf16 a[24:27],a[180:183],a[228:231],a[24:27]
ds_read_b64_tr_b16 a[144:145],v22 offset:1280
v_mfma_f32_16x16x32_bf16 a[56:59],a[180:183],a[236:239],a[56:59]
ds_read_b64_tr_b16 a[146:147],v22 offset:3328
v_mfma_f32_16x16x32_bf16 a[56:59],a[176:179],a[232:235],a[56:59]
ds_read_b64_tr_b16 a[148:149],v22 offset:17664
v_mfma_f32_16x16x32_bf16 a[88:91],a[176:179],a[240:243],a[88:91]
ds_read_b64_tr_b16 a[150:151],v22 offset:19712
v_mfma_f32_16x16x32_bf16 a[88:91],a[180:183],a[244:247],a[88:91]
ds_read_b64_tr_b16 a[152:153],v22 offset:1288
v_mfma_f32_16x16x32_bf16 a[120:123],a[180:183],a[252:255],a[120:123]
ds_read_b64_tr_b16 a[154:155],v22 offset:3336
v_mfma_f32_16x16x32_bf16 a[120:123],a[176:179],a[248:251],a[120:123]
ds_read_b64_tr_b16 a[156:157],v22 offset:17672
v_mfma_f32_16x16x32_bf16 a[28:31],a[184:187],a[224:227],a[28:31]
ds_read_b64_tr_b16 a[158:159],v22 offset:19720
v_mfma_f32_16x16x32_bf16 a[28:31],a[188:191],a[228:231],a[28:31]
v_mfma_f32_16x16x32_bf16 a[60:63],a[188:191],a[236:239],a[60:63]
v_mfma_f32_16x16x32_bf16 a[60:63],a[184:187],a[232:235],a[60:63]
v_mfma_f32_16x16x32_bf16 a[92:95],a[184:187],a[240:243],a[92:95]
v_mfma_f32_16x16x32_bf16 a[92:95],a[188:191],a[244:247],a[92:95]
v_mfma_f32_16x16x32_bf16 a[124:127],a[188:191],a[252:255],a[124:127]
s_cmp_lg_u32 s53,0
v_mfma_f32_16x16x32_bf16 a[124:127],a[184:187],a[248:251],a[124:127]
s_cbranch_scc1 L_qp_%=
L_tailseg_%=:
s_waitcnt lgkmcnt(1)
v_mfma_f32_16x16x32_bf16 v[128:131],a[128:131],a[192:195],v[128:131]
s_add_u32 m0,98304,s51
ds_read_b64_tr_b16 a[160:161],v22 offset:1536
v_xor_b32 v20,131072,v20
v_mfma_f32_16x16x32_bf16 v[128:131],a[132:135],a[196:199],v[128:131]
s_add_u32 s54,s54,16384
buffer_load_dwordx4 v4,s[20:23],s47 offen nt lds
v_mfma_f32_16x16x32_bf16 v[136:139],a[132:135],a[204:207],v[136:139]
s_and_b32 s46,s54,0xfffc0000
ds_read_b64_tr_b16 a[162:163],v22 offset:3584
v_xor_b32 v21,131072,v21
v_mfma_f32_16x16x32_bf16 v[136:139],a[128:131],a[200:203],v[136:139]
s_add_u32 s54,s54,16384
ds_read_b64_tr_b16 a[164:165],v22 offset:17920
v_and_b32 v18,48,v6
v_mfma_f32_16x16x32_bf16 v[144:147],a[128:131],a[208:211],v[144:147]
s_add_u32 s49,s49,32768
ds_read_b64_tr_b16 a[166:167],v22 offset:19968
v_lshrrev_b32 v19,4,v6
v_mfma_f32_16x16x32_bf16 v[144:147],a[132:135],a[212:215],v[144:147]
s_mov_b32 s60,138
ds_read_b64_tr_b16 a[168:169],v22 offset:1544
v_lshlrev_b32 v18,2,v18
v_mfma_f32_16x16x32_bf16 v[152:155],a[132:135],a[220:223],v[152:155]
s_add_u32 s93,s92,L_att0_%=-L_pc_%=
ds_read_b64_tr_b16 a[170:171],v22 offset:3592
v_and_b32 v19,48,v19
v_mfma_f32_16x16x32_bf16 v[152:155],a[128:131],a[216:219],v[152:155]
ds_read_b64_tr_b16 a[172:173],v22 offset:17928
v_add_u32 v18,v18,v19
v_mfma_f32_16x16x32_bf16 v[132:135],a[136:139],a[192:195],v[132:135]
ds_read_b64_tr_b16 a[174:175],v22 offset:19976
v_add_u32 v18,98304,v18
v_mfma_f32_16x16x32_bf16 v[132:135],a[140:143],a[196:199],v[132:135]
s_cmp_eq_u32 s49,163840
s_cselect_b32 s49,0,s49
ds_read_b64_tr_b16 a[176:177],v22 offset:1792
v_mfma_f32_16x16x32_bf16 v[140:143],a[140:143],a[204:207],v[140:143]
s_cmp_eq_u32 s50,131072
s_cselect_b32 s62,163840,0
ds_read_b64_tr_b16 a[178:179],v22 offset:3840
v_mfma_f32_16x16x32_bf16 v[140:143],a[136:139],a[200:203],v[140:143]
s_sub_u32 s48,32768,s62
s_add_u32 s50,s50,s48
ds_read_b64_tr_b16 a[180:181],v22 offset:18176
v_mfma_f32_16x16x32_bf16 v[148:151],a[136:139],a[208:211],v[148:151]
s_cmp_eq_u32 s50,131072
s_cselect_b32 s62,163840,0
ds_read_b64_tr_b16 a[182:183],v22 offset:20224
v_mfma_f32_16x16x32_bf16 v[148:151],a[140:143],a[212:215],v[148:151]
ds_read_b64_tr_b16 a[184:185],v22 offset:1800
v_mfma_f32_16x16x32_bf16 v[156:159],a[140:143],a[220:223],v[156:159]
ds_read_b64_tr_b16 a[186:187],v22 offset:3848
v_mfma_f32_16x16x32_bf16 v[156:159],a[136:139],a[216:219],v[156:159]
ds_read_b64_tr_b16 a[188:189],v22 offset:18184
v_mfma_f32_16x16x32_bf16 v[160:163],a[144:147],a[192:195],v[160:163]
ds_read_b64_tr_b16 a[190:191],v22 offset:20232
v_mfma_f32_16x16x32_bf16 v[160:163],a[148:151],a[196:199],v[160:163]
buffer_load_dwordx4 v4,s[20:23],s47 offen offset:1024 nt lds
v_mfma_f32_16x16x32_bf16 v[168:171],a[148:151],a[204:207],v[168:171]
s_waitcnt vmcnt(4)
ds_read_b128 a[224:227],v20
v_mfma_f32_16x16x32_bf16 v[168:171],a[144:147],a[200:203],v[168:171]
ds_read_b64_tr_b16 a[128:129],v22
v_mfma_f32_16x16x32_bf16 v[176:179],a[144:147],a[208:211],v[176:179]
ds_read_b64_tr_b16 a[130:131],v22 offset:2048
v_mfma_f32_16x16x32_bf16 v[176:179],a[148:151],a[212:215],v[176:179]
ds_read_b64_tr_b16 a[132:133],v22 offset:16384
v_mfma_f32_16x16x32_bf16 v[184:187],a[148:151],a[220:223],v[184:187]
ds_read_b64_tr_b16 a[134:135],v22 offset:18432
v_mfma_f32_16x16x32_bf16 v[184:187],a[144:147],a[216:219],v[184:187]
ds_read_b64_tr_b16 a[144:145],v22 offset:8
v_mfma_f32_16x16x32_bf16 v[164:167],a[152:155],a[192:195],v[164:167]
s_waitcnt lgkmcnt(6)
ds_read_b64_tr_b16 a[146:147],v22 offset:2056
v_mfma_f32_16x16x32_bf16 v[164:167],a[156:159],a[196:199],v[164:167]
ds_read_b64_tr_b16 a[148:149],v22 offset:16392
v_mfma_f32_16x16x32_bf16 v[172:175],a[156:159],a[204:207],v[172:175]
ds_read_b64_tr_b16 a[150:151],v22 offset:18440
v_mfma_f32_16x16x32_bf16 v[172:175],a[152:155],a[200:203],v[172:175]
v_readfirstlane_b32 s68,v13
s_nop 1
s_add_u32 s68,s68,1
v_mfma_f32_16x16x32_bf16 v[180:183],a[152:155],a[208:211],v[180:183]
s_xor_b32 s69,s68,0x9e3779b9
ds_read_b128 a[228:231],v21
v_mfma_f32_16x16x32_bf16 v[180:183],a[156:159],a[212:215],v[180:183]
v_mfma_f32_16x16x32_bf16 v[188:191],a[156:159],a[220:223],v[188:191]
ds_read_b128 a[232:235],v20 offset:2048
v_mfma_f32_16x16x32_bf16 v[188:191],a[152:155],a[216:219],v[188:191]
v_mfma_f32_16x16x32_bf16 v[192:195],a[160:163],a[192:195],v[192:195]
ds_read_b128 a[236:239],v21 offset:2048
ds_read_b128 a[240:243],v20 offset:4096
ds_read_b128 a[244:247],v21 offset:4096
ds_read_b128 a[248:251],v20 offset:6144
ds_read_b128 a[252:255],v21 offset:6144
v_mfma_f32_16x16x32_bf16 v[192:195],a[164:167],a[196:199],v[192:195]
v_mfma_f32_16x16x32_bf16 v[200:203],a[164:167],a[204:207],v[200:203]
v_mfma_f32_16x16x32_bf16 v[200:203],a[160:163],a[200:203],v[200:203]
v_mfma_f32_16x16x32_bf16 v[208:211],a[160:163],a[208:211],v[208:211]
buffer_load_dwordx4 v5,s[20:23],s47 offen offset:2048 nt lds
v_mfma_f32_16x16x32_bf16 v[208:211],a[164:167],a[212:215],v[208:211]
v_mfma_f32_16x16x32_bf16 v[216:219],a[164:167],a[220:223],v[216:219]
s_waitcnt vmcnt(4) lgkmcnt(0)
v_mfma_f32_16x16x32_bf16 v[216:219],a[160:163],a[216:219],v[216:219]
s_barrier
ds_read_b64_tr_b16 a[136:137],v22 offset:32768
v_mfma_f32_16x16x32_bf16 v[196:199],a[168:171],a[192:195],v[196:199]
ds_read_b64_tr_b16 a[138:139],v22 offset:34816
v_mfma_f32_16x16x32_bf16 v[196:199],a[172:175],a[196:199],v[196:199]
ds_read_b64_tr_b16 a[140:141],v22 offset:49152
v_mfma_f32_16x16x32_bf16 v[204:207],a[172:175],a[204:207],v[204:207]
ds_read_b64_tr_b16 a[142:143],v22 offset:51200
v_mfma_f32_16x16x32_bf16 v[204:207],a[168:171],a[200:203],v[204:207]
ds_read_b64_tr_b16 a[152:153],v22 offset:32776
v_mfma_f32_16x16x32_bf16 v[212:215],a[168:171],a[208:211],v[212:215]
ds_read_b64_tr_b16 a[154:155],v22 offset:34824
v_mfma_f32_16x16x32_bf16 v[212:215],a[172:175],a[212:215],v[212:215]
ds_read_b64_tr_b16 a[156:157],v22 offset:49160
v_mfma_f32_16x16x32_bf16 v[220:223],a[172:175],a[220:223],v[220:223]
ds_read_b64_tr_b16 a[158:159],v22 offset:51208
v_mfma_f32_16x16x32_bf16 v[220:223],a[168:171],a[216:219],v[220:223]
ds_read_b64_tr_b16 a[160:161],v22 offset:256
v_mfma_f32_16x16x32_bf16 v[224:227],a[176:179],a[192:195],v[224:227]
buffer_load_dwordx4 v5,s[20:23],s47 offen offset:3072 nt lds
v_mfma_f32_16x16x32_bf16 v[224:227],a[180:183],a[196:199],v[224:227]
ds_read_b64_tr_b16 a[162:163],v22 offset:2304
v_mfma_f32_16x16x32_bf16 v[232:235],a[180:183],a[204:207],v[232:235]
ds_read_b64_tr_b16 a[164:165],v22 offset:16640
v_mfma_f32_16x16x32_bf16 v[232:235],a[176:179],a[200:203],v[232:235]
ds_read_b64_tr_b16 a[166:167],v22 offset:18688
v_mfma_f32_16x16x32_bf16 v[240:243],a[176:179],a[208:211],v[240:243]
ds_read_b64_tr_b16 a[168:169],v22 offset:33024
v_mfma_f32_16x16x32_bf16 v[240:243],a[180:183],a[212:215],v[240:243]
ds_read_b64_tr_b16 a[170:171],v22 offset:35072
v_mfma_f32_16x16x32_bf16 v[248:251],a[180:183],a[220:223],v[248:251]
ds_read_b64_tr_b16 a[172:173],v22 offset:49408
v_mfma_f32_16x16x32_bf16 v[248:251],a[176:179],a[216:219],v[248:251]
ds_read_b64_tr_b16 a[174:175],v22 offset:51456
v_mfma_f32_16x16x32_bf16 v[228:231],a[184:187],a[192:195],v[228:231]
ds_read_b64_tr_b16 a[176:177],v22 offset:264
v_mfma_f32_16x16x32_bf16 v[228:231],a[188:191],a[196:199],v[228:231]
ds_read_b64_tr_b16 a[178:179],v22 offset:2312
v_mfma_f32_16x16x32_bf16 v[236:239],a[188:191],a[204:207],v[236:239]
ds_read_b64_tr_b16 a[180:181],v22 offset:16648
v_mfma_f32_16x16x32_bf16 v[236:239],a[184:187],a[200:203],v[236:239]
ds_read_b64_tr_b16 a[182:183],v22 offset:18696
v_mfma_f32_16x16x32_bf16 v[244:247],a[184:187],a[208:211],v[244:247]
v_mfma_f32_16x16x32_bf16 v[244:247],a[188:191],a[212:215],v[244:247]
s_add_u32 m0,98304,s52
v_mfma_f32_16x16x32_bf16 v[252:255],a[188:191],a[220:223],v[252:255]
ds_read_b64_tr_b16 a[188:189],v22 offset:49416
v_mfma_f32_16x16x32_bf16 v[252:255],a[184:187],a[216:219],v[252:255]
ds_read_b64_tr_b16 a[184:185],v22 offset:33032
v_mfma_f32_16x16x32_bf16 a[0:3],a[128:131],a[192:195],a[0:3]
buffer_load_dwordx4 v4,s[24:27],s47 offen nt lds
v_mfma_f32_16x16x32_bf16 a[0:3],a[132:135],a[196:199],a[0:3]
s_waitcnt lgkmcnt(4)
ds_read_b64_tr_b16 a[186:187],v22 offset:35080
v_mfma_f32_16x16x32_bf16 a[0:3],a[136:139],a[224:227],a[0:3]
ds_read_b64_tr_b16 a[190:191],v22 offset:51464
v_mfma_f32_16x16x32_bf16 a[0:3],a[140:143],a[228:231],a[0:3]
v_mfma_f32_16x16x32_bf16 a[32:35],a[140:143],a[236:239],a[32:35]
v_mfma_f32_16x16x32_bf16 a[32:35],a[136:139],a[232:235],a[32:35]
v_mfma_f32_16x16x32_bf16 a[32:35],a[132:135],a[204:207],a[32:35]
v_mfma_f32_16x16x32_bf16 a[32:35],a[128:131],a[200:203],a[32:35]
)A"
R"A(v_mfma_f32_16x16x32_bf16 a[64:67],a[128:131],a[208:211],a[64:67]
v_mfma_f32_16x16x32_bf16 a[64:67],a[132:135],a[212:215],a[64:67]
s_nop 1
v_accvgpr_read_b32 v64,a0
v_accvgpr_read_b32 v65,a1
v_mfma_f32_16x16x32_bf16 a[64:67],a[136:139],a[240:243],a[64:67]
v_mul_f32 v64,s61,v64
v_mul_f32 v65,s61,v65
v_mfma_f32_16x16x32_bf16 a[64:67],a[140:143],a[244:247],a[64:67]
v_accvgpr_read_b32 v66,a2
v_accvgpr_read_b32 v67,a3
v_mfma_f32_16x16x32_bf16 a[96:99],a[140:143],a[252:255],a[96:99]
ds_read_b64_tr_b16 a[140:141],v22 offset:49664
v_cvt_pk_bf16_f32 v64,v64,v65
v_mfma_f32_16x16x32_bf16 a[96:99],a[136:139],a[248:251],a[96:99]
ds_read_b64_tr_b16 a[136:137],v22 offset:33280
v_mul_f32 v66,s61,v66
v_mfma_f32_16x16x32_bf16 a[96:99],a[132:135],a[220:223],a[96:99]
ds_read_b64_tr_b16 a[132:133],v22 offset:16896
v_mul_f32 v67,s61,v67
v_mfma_f32_16x16x32_bf16 a[96:99],a[128:131],a[216:219],a[96:99]
ds_read_b64_tr_b16 a[128:129],v22 offset:512
v_cvt_pk_bf16_f32 v65,v66,v67
v_mfma_f32_16x16x32_bf16 a[4:7],a[144:147],a[192:195],a[4:7]
buffer_load_dwordx4 v5,s[24:27],s47 offen offset:1024 nt lds
v_mfma_f32_16x16x32_bf16 a[4:7],a[148:151],a[196:199],a[4:7]
ds_read_b64_tr_b16 a[130:131],v22 offset:2560
v_accvgpr_read_b32 v96,a64
v_mfma_f32_16x16x32_bf16 a[4:7],a[152:155],a[224:227],a[4:7]
ds_read_b64_tr_b16 a[134:135],v22 offset:18944
v_accvgpr_read_b32 v97,a65
v_mfma_f32_16x16x32_bf16 a[4:7],a[156:159],a[228:231],a[4:7]
ds_read_b64_tr_b16 a[138:139],v22 offset:35328
v_accvgpr_read_b32 v80,a32
v_mfma_f32_16x16x32_bf16 a[36:39],a[156:159],a[236:239],a[36:39]
ds_read_b64_tr_b16 a[142:143],v22 offset:51712
v_accvgpr_read_b32 v81,a33
v_mfma_f32_16x16x32_bf16 a[36:39],a[152:155],a[232:235],a[36:39]
v_accvgpr_read_b32 v112,a96
v_accvgpr_read_b32 v113,a97
v_mfma_f32_16x16x32_bf16 a[36:39],a[148:151],a[204:207],a[36:39]
v_mul_f32 v112,s61,v112
v_mul_f32 v113,s61,v113
v_mfma_f32_16x16x32_bf16 a[36:39],a[144:147],a[200:203],a[36:39]
v_accvgpr_read_b32 v114,a98
v_accvgpr_read_b32 v115,a99
v_mfma_f32_16x16x32_bf16 a[68:71],a[144:147],a[208:211],a[68:71]
v_mul_f32 v96,s61,v96
v_mul_f32 v97,s61,v97
v_mfma_f32_16x16x32_bf16 a[68:71],a[148:151],a[212:215],a[68:71]
v_accvgpr_read_b32 v66,a4
v_accvgpr_read_b32 v67,a5
v_mfma_f32_16x16x32_bf16 a[68:71],a[152:155],a[240:243],a[68:71]
v_accvgpr_read_b32 v12,a6
v_accvgpr_read_b32 v13,a7
v_mfma_f32_16x16x32_bf16 a[68:71],a[156:159],a[244:247],a[68:71]
v_mul_f32 v66,s61,v66
v_mul_f32 v67,s61,v67
v_mfma_f32_16x16x32_bf16 a[100:103],a[156:159],a[252:255],a[100:103]
ds_read_b64_tr_b16 a[156:157],v22 offset:49672
v_accvgpr_read_b32 v98,a66
v_mfma_f32_16x16x32_bf16 a[100:103],a[152:155],a[248:251],a[100:103]
ds_read_b64_tr_b16 a[152:153],v22 offset:33288
v_accvgpr_read_b32 v99,a67
v_mfma_f32_16x16x32_bf16 a[100:103],a[148:151],a[220:223],a[100:103]
ds_read_b64_tr_b16 a[148:149],v22 offset:16904
v_cvt_pk_bf16_f32 v112,v112,v113
v_mfma_f32_16x16x32_bf16 a[100:103],a[144:147],a[216:219],a[100:103]
ds_read_b64_tr_b16 a[144:145],v22 offset:520
v_mul_f32 v114,s61,v114
v_mfma_f32_16x16x32_bf16 a[8:11],a[160:163],a[192:195],a[8:11]
buffer_load_dwordx4 v4,s[24:27],s47 offen offset:2048 nt lds
v_mfma_f32_16x16x32_bf16 a[8:11],a[164:167],a[196:199],a[8:11]
ds_read_b64_tr_b16 a[146:147],v22 offset:2568
v_mul_f32 v115,s61,v115
v_mfma_f32_16x16x32_bf16 a[8:11],a[168:171],a[224:227],a[8:11]
ds_read_b64_tr_b16 a[150:151],v22 offset:18952
v_mul_f32 v12,s61,v12
v_mfma_f32_16x16x32_bf16 a[8:11],a[172:175],a[228:231],a[8:11]
ds_read_b64_tr_b16 a[154:155],v22 offset:35336
v_mul_f32 v13,s61,v13
v_mfma_f32_16x16x32_bf16 a[40:43],a[172:175],a[236:239],a[40:43]
ds_read_b64_tr_b16 a[158:159],v22 offset:51720
v_cvt_pk_bf16_f32 v66,v66,v67
v_mfma_f32_16x16x32_bf16 a[40:43],a[168:171],a[232:235],a[40:43]
v_mul_f32 v80,s61,v80
v_mul_f32 v81,s61,v81
v_mfma_f32_16x16x32_bf16 a[40:43],a[164:167],a[204:207],a[40:43]
v_accvgpr_read_b32 v82,a34
v_accvgpr_read_b32 v83,a35
v_mfma_f32_16x16x32_bf16 a[40:43],a[160:163],a[200:203],a[40:43]
v_cvt_pk_bf16_f32 v96,v96,v97
v_mul_f32 v98,s61,v98
v_mfma_f32_16x16x32_bf16 a[72:75],a[160:163],a[208:211],a[72:75]
v_mul_f32 v99,s61,v99
v_cvt_pk_bf16_f32 v113,v114,v115
v_mfma_f32_16x16x32_bf16 a[72:75],a[164:167],a[212:215],a[72:75]
v_cvt_pk_bf16_f32 v67,v12,v13
v_cvt_pk_bf16_f32 v80,v80,v81
v_mfma_f32_16x16x32_bf16 a[72:75],a[168:171],a[240:243],a[72:75]
v_mul_f32 v82,s61,v82
v_mul_f32 v83,s61,v83
v_mfma_f32_16x16x32_bf16 a[72:75],a[172:175],a[244:247],a[72:75]
v_cvt_pk_bf16_f32 v97,v98,v99
v_accvgpr_read_b32 v114,a100
v_mfma_f32_16x16x32_bf16 a[104:107],a[172:175],a[252:255],a[104:107]
ds_read_b64_tr_b16 a[172:173],v22 offset:49920
v_accvgpr_read_b32 v115,a101
v_mfma_f32_16x16x32_bf16 a[104:107],a[168:171],a[248:251],a[104:107]
ds_read_b64_tr_b16 a[168:169],v22 offset:33536
v_accvgpr_read_b32 v84,a40
v_mfma_f32_16x16x32_bf16 a[104:107],a[164:167],a[220:223],a[104:107]
ds_read_b64_tr_b16 a[164:165],v22 offset:17152
v_accvgpr_read_b32 v85,a41
v_mfma_f32_16x16x32_bf16 a[104:107],a[160:163],a[216:219],a[104:107]
ds_read_b64_tr_b16 a[160:161],v22 offset:768
v_cvt_pk_bf16_f32 v81,v82,v83
v_mfma_f32_16x16x32_bf16 a[12:15],a[176:179],a[192:195],a[12:15]
s_waitcnt lgkmcnt(4)
buffer_load_dwordx4 v5,s[24:27],s47 offen offset:3072 nt lds
v_mfma_f32_16x16x32_bf16 a[12:15],a[180:183],a[196:199],a[12:15]
s_add_u32 s47,s47,16384
s_and_b32 s47,s47,0x3ffff
s_or_b32 s47,s47,s46
v_mfma_f32_16x16x32_bf16 a[12:15],a[184:187],a[224:227],a[12:15]
s_and_b32 s46,s54,0xfffc0000
ds_read_b64_tr_b16 a[162:163],v22 offset:2816
v_accvgpr_read_b32 v100,a72
v_mfma_f32_16x16x32_bf16 a[12:15],a[188:191],a[228:231],a[12:15]
s_add_u32 s54,s54,16384
ds_read_b64_tr_b16 a[166:167],v22 offset:19200
v_accvgpr_read_b32 v101,a73
v_mfma_f32_16x16x32_bf16 a[44:47],a[188:191],a[236:239],a[44:47]
ds_read_b64_tr_b16 a[170:171],v22 offset:35584
v_mul_f32 v100,s61,v100
v_mfma_f32_16x16x32_bf16 a[44:47],a[184:187],a[232:235],a[44:47]
ds_read_b64_tr_b16 a[174:175],v22 offset:51968
v_mul_f32 v101,s61,v101
v_mfma_f32_16x16x32_bf16 a[44:47],a[180:183],a[204:207],a[44:47]
v_accvgpr_read_b32 v102,a74
v_accvgpr_read_b32 v103,a75
v_mfma_f32_16x16x32_bf16 a[44:47],a[176:179],a[200:203],a[44:47]
v_accvgpr_read_b32 v98,a68
v_accvgpr_read_b32 v99,a69
v_mfma_f32_16x16x32_bf16 a[76:79],a[176:179],a[208:211],a[76:79]
v_accvgpr_read_b32 v12,a102
v_accvgpr_read_b32 v13,a103
v_mfma_f32_16x16x32_bf16 a[76:79],a[180:183],a[212:215],a[76:79]
v_mul_f32 v114,s61,v114
v_mul_f32 v115,s61,v115
v_mfma_f32_16x16x32_bf16 a[76:79],a[184:187],a[240:243],a[76:79]
v_accvgpr_read_b32 v68,a8
v_accvgpr_read_b32 v69,a9
v_mfma_f32_16x16x32_bf16 a[76:79],a[188:191],a[244:247],a[76:79]
v_mul_f32 v84,s61,v84
v_mul_f32 v85,s61,v85
v_mfma_f32_16x16x32_bf16 a[108:111],a[188:191],a[252:255],a[108:111]
ds_read_b64_tr_b16 a[188:189],v22 offset:49928
v_accvgpr_read_b32 v86,a42
v_mfma_f32_16x16x32_bf16 a[108:111],a[184:187],a[248:251],a[108:111]
ds_read_b64_tr_b16 a[184:185],v22 offset:33544
v_accvgpr_read_b32 v87,a43
v_mfma_f32_16x16x32_bf16 a[108:111],a[180:183],a[220:223],a[108:111]
ds_read_b64_tr_b16 a[180:181],v22 offset:17160
v_cvt_pk_bf16_f32 v100,v100,v101
v_mfma_f32_16x16x32_bf16 a[108:111],a[176:179],a[216:219],a[108:111]
ds_read_b64_tr_b16 a[176:177],v22 offset:776
v_mul_f32 v102,s61,v102
v_mfma_f32_16x16x32_bf16 a[16:19],a[128:131],a[192:195],a[16:19]
ds_read_b64_tr_b16 a[178:179],v22 offset:2824
v_mul_f32 v103,s61,v103
v_mfma_f32_16x16x32_bf16 a[16:19],a[132:135],a[196:199],a[16:19]
ds_read_b64_tr_b16 a[182:183],v22 offset:19208
v_accvgpr_read_b32 v82,a36
v_mfma_f32_16x16x32_bf16 a[16:19],a[136:139],a[224:227],a[16:19]
ds_read_b64_tr_b16 a[186:187],v22 offset:35592
v_accvgpr_read_b32 v83,a37
v_mfma_f32_16x16x32_bf16 a[16:19],a[140:143],a[228:231],a[16:19]
s_add_u32 m0,131072,s51
ds_read_b64_tr_b16 a[190:191],v22 offset:51976
v_accvgpr_read_b32 v16,a70
v_mfma_f32_16x16x32_bf16 a[48:51],a[140:143],a[236:239],a[48:51]
v_accvgpr_read_b32 v17,a71
v_mul_f32 v98,s61,v98
v_mfma_f32_16x16x32_bf16 a[48:51],a[136:139],a[232:235],a[48:51]
v_mul_f32 v99,s61,v99
v_mul_f32 v12,s61,v12
v_mfma_f32_16x16x32_bf16 a[48:51],a[132:135],a[204:207],a[48:51]
buffer_load_dwordx4 v4,s[20:23],s47 offen nt lds
v_mfma_f32_16x16x32_bf16 a[48:51],a[128:131],a[200:203],a[48:51]
v_mul_f32 v13,s61,v13
v_cvt_pk_bf16_f32 v114,v114,v115
v_mfma_f32_16x16x32_bf16 a[80:83],a[128:131],a[208:211],a[80:83]
v_mul_f32 v68,s61,v68
v_mul_f32 v69,s61,v69
v_mfma_f32_16x16x32_bf16 a[80:83],a[132:135],a[212:215],a[80:83]
v_accvgpr_read_b32 v70,a10
v_accvgpr_read_b32 v71,a11
v_mfma_f32_16x16x32_bf16 a[80:83],a[136:139],a[240:243],a[80:83]
v_cvt_pk_bf16_f32 v84,v84,v85
v_mul_f32 v86,s61,v86
v_mfma_f32_16x16x32_bf16 a[80:83],a[140:143],a[244:247],a[80:83]
v_mul_f32 v87,s61,v87
v_cvt_pk_bf16_f32 v101,v102,v103
v_mfma_f32_16x16x32_bf16 a[112:115],a[140:143],a[252:255],a[112:115]
ds_read_b64_tr_b16 a[140:141],v22 offset:50184
v_accvgpr_read_b32 v14,a38
v_mfma_f32_16x16x32_bf16 a[112:115],a[136:139],a[248:251],a[112:115]
ds_read_b64_tr_b16 a[136:137],v22 offset:33800
v_accvgpr_read_b32 v88,a48
v_mfma_f32_16x16x32_bf16 a[112:115],a[132:135],a[220:223],a[112:115]
ds_read_b64_tr_b16 a[132:133],v22 offset:50176
v_accvgpr_read_b32 v89,a49
v_mfma_f32_16x16x32_bf16 a[112:115],a[128:131],a[216:219],a[112:115]
ds_read_b64_tr_b16 a[128:129],v22 offset:33792
v_accvgpr_read_b32 v15,a39
v_mfma_f32_16x16x32_bf16 a[20:23],a[144:147],a[192:195],a[20:23]
ds_read_b64_tr_b16 a[130:131],v22 offset:35840
v_mul_f32 v82,s61,v82
v_mfma_f32_16x16x32_bf16 a[20:23],a[148:151],a[196:199],a[20:23]
ds_read_b64_tr_b16 a[134:135],v22 offset:52224
v_mul_f32 v83,s61,v83
v_mfma_f32_16x16x32_bf16 a[20:23],a[152:155],a[224:227],a[20:23]
ds_read_b64_tr_b16 a[138:139],v22 offset:35848
v_mul_f32 v16,s61,v16
v_mfma_f32_16x16x32_bf16 a[20:23],a[156:159],a[228:231],a[20:23]
ds_read_b64_tr_b16 a[142:143],v22 offset:52232
v_mul_f32 v17,s61,v17
v_mfma_f32_16x16x32_bf16 a[52:55],a[156:159],a[236:239],a[52:55]
v_cvt_pk_bf16_f32 v98,v98,v99
v_cvt_pk_bf16_f32 v115,v12,v13
v_mfma_f32_16x16x32_bf16 a[52:55],a[152:155],a[232:235],a[52:55]
v_cvt_pk_bf16_f32 v68,v68,v69
v_mul_f32 v70,s61,v70
v_mfma_f32_16x16x32_bf16 a[52:55],a[148:151],a[204:207],a[52:55]
buffer_load_dwordx4 v4,s[20:23],s47 offen offset:1024 nt lds
v_mfma_f32_16x16x32_bf16 a[52:55],a[144:147],a[200:203],a[52:55]
v_mul_f32 v71,s61,v71
v_cvt_pk_bf16_f32 v85,v86,v87
v_mfma_f32_16x16x32_bf16 a[84:87],a[144:147],a[208:211],a[84:87]
v_accvgpr_read_b32 v102,a76
v_accvgpr_read_b32 v103,a77
v_mfma_f32_16x16x32_bf16 a[84:87],a[148:151],a[212:215],a[84:87]
v_accvgpr_read_b32 v72,a16
v_accvgpr_read_b32 v73,a17
v_mfma_f32_16x16x32_bf16 a[84:87],a[152:155],a[240:243],a[84:87]
v_mul_f32 v88,s61,v88
v_mul_f32 v89,s61,v89
v_mfma_f32_16x16x32_bf16 a[84:87],a[156:159],a[244:247],a[84:87]
v_accvgpr_read_b32 v90,a50
v_accvgpr_read_b32 v91,a51
v_mfma_f32_16x16x32_bf16 a[116:119],a[156:159],a[252:255],a[116:119]
ds_read_b64_tr_b16 a[156:157],v22 offset:50440
v_mul_f32 v14,s61,v14
v_mfma_f32_16x16x32_bf16 a[116:119],a[152:155],a[248:251],a[116:119]
ds_read_b64_tr_b16 a[152:153],v22 offset:34056
v_mul_f32 v15,s61,v15
v_mfma_f32_16x16x32_bf16 a[116:119],a[148:151],a[220:223],a[116:119]
ds_read_b64_tr_b16 a[148:149],v22 offset:50432
v_cvt_pk_bf16_f32 v82,v82,v83
v_mfma_f32_16x16x32_bf16 a[116:119],a[144:147],a[216:219],a[116:119]
s_waitcnt lgkmcnt(3)
ds_read_b64_tr_b16 a[144:145],v22 offset:34048
v_cvt_pk_bf16_f32 v99,v16,v17
v_mfma_f32_16x16x32_bf16 a[24:27],a[160:163],a[192:195],a[24:27]
ds_read_b64_tr_b16 a[146:147],v22 offset:36096
v_cvt_pk_bf16_f32 v69,v70,v71
v_mfma_f32_16x16x32_bf16 a[24:27],a[164:167],a[196:199],a[24:27]
ds_read_b64_tr_b16 a[150:151],v22 offset:52480
v_accvgpr_read_b32 v86,a44
v_mfma_f32_16x16x32_bf16 a[24:27],a[168:171],a[224:227],a[24:27]
ds_read_b64_tr_b16 a[154:155],v22 offset:36104
v_accvgpr_read_b32 v87,a45
v_mfma_f32_16x16x32_bf16 a[24:27],a[172:175],a[228:231],a[24:27]
ds_read_b64_tr_b16 a[158:159],v22 offset:52488
v_accvgpr_read_b32 v12,a78
v_mfma_f32_16x16x32_bf16 a[56:59],a[172:175],a[236:239],a[56:59]
)A"
R"A(v_accvgpr_read_b32 v13,a79
v_mul_f32 v102,s61,v102
v_mfma_f32_16x16x32_bf16 a[56:59],a[168:171],a[232:235],a[56:59]
v_mul_f32 v103,s61,v103
v_mul_f32 v72,s61,v72
v_mfma_f32_16x16x32_bf16 a[56:59],a[164:167],a[204:207],a[56:59]
buffer_load_dwordx4 v5,s[20:23],s47 offen offset:2048 nt lds
v_mfma_f32_16x16x32_bf16 a[56:59],a[160:163],a[200:203],a[56:59]
v_mul_f32 v73,s61,v73
v_accvgpr_read_b32 v74,a18
v_mfma_f32_16x16x32_bf16 a[88:91],a[160:163],a[208:211],a[88:91]
v_accvgpr_read_b32 v75,a19
v_cvt_pk_bf16_f32 v88,v88,v89
v_mfma_f32_16x16x32_bf16 a[88:91],a[164:167],a[212:215],a[88:91]
v_mul_f32 v90,s61,v90
v_mul_f32 v91,s61,v91
v_mfma_f32_16x16x32_bf16 a[88:91],a[168:171],a[240:243],a[88:91]
v_cvt_pk_bf16_f32 v83,v14,v15
v_accvgpr_read_b32 v70,a12
v_mfma_f32_16x16x32_bf16 a[88:91],a[172:175],a[244:247],a[88:91]
v_accvgpr_read_b32 v71,a13
v_accvgpr_read_b32 v16,a46
v_mfma_f32_16x16x32_bf16 a[120:123],a[172:175],a[252:255],a[120:123]
ds_read_b64_tr_b16 a[172:173],v22 offset:50696
v_accvgpr_read_b32 v17,a47
v_mfma_f32_16x16x32_bf16 a[120:123],a[168:171],a[248:251],a[120:123]
ds_read_b64_tr_b16 a[168:169],v22 offset:34312
v_mul_f32 v86,s61,v86
v_mfma_f32_16x16x32_bf16 a[120:123],a[164:167],a[220:223],a[120:123]
ds_read_b64_tr_b16 a[164:165],v22 offset:50688
v_mul_f32 v87,s61,v87
v_mfma_f32_16x16x32_bf16 a[120:123],a[160:163],a[216:219],a[120:123]
ds_read_b64_tr_b16 a[160:161],v22 offset:34304
v_mul_f32 v12,s61,v12
v_mfma_f32_16x16x32_bf16 a[28:31],a[176:179],a[192:195],a[28:31]
ds_read_b64_tr_b16 a[162:163],v22 offset:36352
v_mul_f32 v13,s61,v13
v_mfma_f32_16x16x32_bf16 a[28:31],a[180:183],a[196:199],a[28:31]
ds_read_b64_tr_b16 a[166:167],v22 offset:52736
v_cvt_pk_bf16_f32 v102,v102,v103
v_mfma_f32_16x16x32_bf16 a[28:31],a[184:187],a[224:227],a[28:31]
ds_read_b64_tr_b16 a[170:171],v22 offset:36360
v_cvt_pk_bf16_f32 v72,v72,v73
v_mfma_f32_16x16x32_bf16 a[28:31],a[188:191],a[228:231],a[28:31]
ds_read_b64_tr_b16 a[174:175],v22 offset:52744
v_mul_f32 v74,s61,v74
v_mfma_f32_16x16x32_bf16 a[60:63],a[188:191],a[236:239],a[60:63]
v_mul_f32 v75,s61,v75
v_cvt_pk_bf16_f32 v89,v90,v91
v_mfma_f32_16x16x32_bf16 a[60:63],a[184:187],a[232:235],a[60:63]
v_accvgpr_read_b32 v76,a24
v_accvgpr_read_b32 v77,a25
v_mfma_f32_16x16x32_bf16 a[60:63],a[180:183],a[204:207],a[60:63]
buffer_load_dwordx4 v5,s[20:23],s47 offen offset:3072 nt lds
v_mfma_f32_16x16x32_bf16 a[60:63],a[176:179],a[200:203],a[60:63]
v_accvgpr_read_b32 v14,a14
v_accvgpr_read_b32 v15,a15
v_mfma_f32_16x16x32_bf16 a[92:95],a[176:179],a[208:211],a[92:95]
v_mul_f32 v70,s61,v70
v_mul_f32 v71,s61,v71
v_mfma_f32_16x16x32_bf16 a[92:95],a[180:183],a[212:215],a[92:95]
v_mul_f32 v16,s61,v16
v_mul_f32 v17,s61,v17
v_mfma_f32_16x16x32_bf16 a[92:95],a[184:187],a[240:243],a[92:95]
v_cvt_pk_bf16_f32 v86,v86,v87
v_cvt_pk_bf16_f32 v103,v12,v13
v_mfma_f32_16x16x32_bf16 a[92:95],a[188:191],a[244:247],a[92:95]
v_cvt_pk_bf16_f32 v73,v74,v75
v_accvgpr_read_b32 v90,a52
v_mfma_f32_16x16x32_bf16 a[124:127],a[188:191],a[252:255],a[124:127]
ds_read_b64_tr_b16 a[188:189],v22 offset:50952
v_accvgpr_read_b32 v91,a53
v_mfma_f32_16x16x32_bf16 a[124:127],a[184:187],a[248:251],a[124:127]
ds_read_b64_tr_b16 a[184:185],v22 offset:34568
v_mul_f32 v76,s61,v76
v_mfma_f32_16x16x32_bf16 a[124:127],a[180:183],a[220:223],a[124:127]
ds_read_b64_tr_b16 a[180:181],v22 offset:50944
v_mul_f32 v77,s61,v77
v_mfma_f32_16x16x32_bf16 a[124:127],a[176:179],a[216:219],a[124:127]
ds_read_b64_tr_b16 a[176:177],v22 offset:34560
v_accvgpr_read_b32 v78,a26
v_mfma_f32_16x16x32_bf16 v[128:131],a[128:131],a[224:227],v[128:131]
ds_read_b64_tr_b16 a[178:179],v22 offset:36608
v_accvgpr_read_b32 v79,a27
v_mfma_f32_16x16x32_bf16 v[128:131],a[132:135],a[228:231],v[128:131]
ds_read_b64_tr_b16 a[182:183],v22 offset:52992
v_mul_f32 v14,s61,v14
v_mfma_f32_16x16x32_bf16 v[136:139],a[132:135],a[236:239],v[136:139]
ds_read_b64_tr_b16 a[186:187],v22 offset:36616
v_mul_f32 v15,s61,v15
v_mfma_f32_16x16x32_bf16 v[136:139],a[128:131],a[232:235],v[136:139]
s_add_u32 m0,131072,s52
ds_read_b64_tr_b16 a[190:191],v22 offset:53000
v_cvt_pk_bf16_f32 v70,v70,v71
v_mfma_f32_16x16x32_bf16 v[144:147],a[128:131],a[240:243],v[144:147]
v_cvt_pk_bf16_f32 v87,v16,v17
v_accvgpr_read_b32 v116,a104
v_mfma_f32_16x16x32_bf16 v[144:147],a[132:135],a[244:247],v[144:147]
v_accvgpr_read_b32 v117,a105
v_accvgpr_read_b32 v74,a20
v_mfma_f32_16x16x32_bf16 v[152:155],a[132:135],a[252:255],v[152:155]
buffer_load_dwordx4 v4,s[24:27],s47 offen nt lds
v_mfma_f32_16x16x32_bf16 v[152:155],a[128:131],a[248:251],v[152:155]
v_accvgpr_read_b32 v75,a21
v_accvgpr_read_b32 v12,a54
v_mfma_f32_16x16x32_bf16 v[132:135],a[136:139],a[224:227],v[132:135]
v_accvgpr_read_b32 v13,a55
v_mul_f32 v90,s61,v90
v_mfma_f32_16x16x32_bf16 v[132:135],a[140:143],a[228:231],v[132:135]
v_mul_f32 v91,s61,v91
v_cvt_pk_bf16_f32 v76,v76,v77
v_mfma_f32_16x16x32_bf16 v[140:143],a[140:143],a[236:239],v[140:143]
v_mul_f32 v78,s61,v78
v_mul_f32 v79,s61,v79
v_mfma_f32_16x16x32_bf16 v[140:143],a[136:139],a[232:235],v[140:143]
v_cvt_pk_bf16_f32 v71,v14,v15
v_mul_f32 v116,s61,v116
v_mfma_f32_16x16x32_bf16 v[148:151],a[136:139],a[240:243],v[148:151]
v_mul_f32 v117,s61,v117
v_accvgpr_read_b32 v118,a106
v_mfma_f32_16x16x32_bf16 v[148:151],a[140:143],a[244:247],v[148:151]
v_accvgpr_read_b32 v119,a107
v_accvgpr_read_b32 v16,a22
v_mfma_f32_16x16x32_bf16 v[156:159],a[140:143],a[252:255],v[156:159]
s_waitcnt vmcnt(5) lgkmcnt(0)
v_accvgpr_read_b32 v17,a23
v_mul_f32 v74,s61,v74
v_mfma_f32_16x16x32_bf16 v[156:159],a[136:139],a[248:251],v[156:159]
s_barrier
ds_read_b128 v[48:51],v18
v_mul_f32 v75,s61,v75
v_mfma_f32_16x16x32_bf16 v[160:163],a[144:147],a[224:227],v[160:163]
v_mul_f32 v12,s61,v12
v_mul_f32 v13,s61,v13
v_mfma_f32_16x16x32_bf16 v[160:163],a[148:151],a[228:231],v[160:163]
ds_read_b128 v[52:55],v18 offset:256
v_cvt_pk_bf16_f32 v90,v90,v91
v_mfma_f32_16x16x32_bf16 v[168:171],a[148:151],a[236:239],v[168:171]
v_cvt_pk_bf16_f32 v77,v78,v79
v_cvt_pk_bf16_f32 v116,v116,v117
v_mfma_f32_16x16x32_bf16 v[168:171],a[144:147],a[232:235],v[168:171]
ds_read_b128 v[56:59],v18 offset:512
v_mul_f32 v118,s61,v118
v_mfma_f32_16x16x32_bf16 v[176:179],a[144:147],a[240:243],v[176:179]
v_mul_f32 v119,s61,v119
v_mul_f32 v16,s61,v16
v_mfma_f32_16x16x32_bf16 v[176:179],a[148:151],a[244:247],v[176:179]
buffer_load_dwordx4 v5,s[24:27],s47 offen offset:1024 nt lds
v_mfma_f32_16x16x32_bf16 v[184:187],a[148:151],a[252:255],v[184:187]
ds_read_b128 v[60:63],v18 offset:768
v_mul_f32 v17,s61,v17
v_mfma_f32_16x16x32_bf16 v[184:187],a[144:147],a[248:251],v[184:187]
v_cvt_pk_bf16_f32 v74,v74,v75
v_cvt_pk_bf16_f32 v91,v12,v13
v_mfma_f32_16x16x32_bf16 v[164:167],a[152:155],a[224:227],v[164:167]
v_accvgpr_read_b32 v78,a28
v_accvgpr_read_b32 v79,a29
v_mfma_f32_16x16x32_bf16 v[164:167],a[156:159],a[228:231],v[164:167]
v_cvt_pk_bf16_f32 v117,v118,v119
v_cvt_pk_bf16_f32 v75,v16,v17
v_mfma_f32_16x16x32_bf16 v[172:175],a[156:159],a[236:239],v[172:175]
v_accvgpr_read_b32 v104,a80
v_accvgpr_read_b32 v105,a81
v_mfma_f32_16x16x32_bf16 v[172:175],a[152:155],a[232:235],v[172:175]
v_accvgpr_read_b32 v12,a30
v_accvgpr_read_b32 v13,a31
v_mfma_f32_16x16x32_bf16 v[180:183],a[152:155],a[240:243],v[180:183]
v_mul_f32 v78,s61,v78
v_mul_f32 v79,s61,v79
v_mfma_f32_16x16x32_bf16 v[180:183],a[156:159],a[244:247],v[180:183]
v_accvgpr_read_b32 v118,a108
v_accvgpr_read_b32 v119,a109
v_mfma_f32_16x16x32_bf16 v[188:191],a[156:159],a[252:255],v[188:191]
v_mul_f32 v104,s61,v104
v_mul_f32 v105,s61,v105
v_mfma_f32_16x16x32_bf16 v[188:191],a[152:155],a[248:251],v[188:191]
v_accvgpr_read_b32 v106,a82
v_accvgpr_read_b32 v107,a83
v_mfma_f32_16x16x32_bf16 v[192:195],a[160:163],a[224:227],v[192:195]
v_mul_f32 v12,s61,v12
v_mul_f32 v13,s61,v13
v_mfma_f32_16x16x32_bf16 v[192:195],a[164:167],a[228:231],v[192:195]
v_cvt_pk_bf16_f32 v78,v78,v79
v_accvgpr_read_b32 v14,a110
v_mfma_f32_16x16x32_bf16 v[200:203],a[164:167],a[236:239],v[200:203]
v_accvgpr_read_b32 v15,a111
v_mul_f32 v118,s61,v118
v_mfma_f32_16x16x32_bf16 v[200:203],a[160:163],a[232:235],v[200:203]
v_mul_f32 v119,s61,v119
v_cvt_pk_bf16_f32 v104,v104,v105
v_mfma_f32_16x16x32_bf16 v[208:211],a[160:163],a[240:243],v[208:211]
v_mul_f32 v106,s61,v106
v_mul_f32 v107,s61,v107
v_mfma_f32_16x16x32_bf16 v[208:211],a[164:167],a[244:247],v[208:211]
buffer_load_dwordx4 v4,s[24:27],s47 offen offset:2048 nt lds
v_mfma_f32_16x16x32_bf16 v[216:219],a[164:167],a[252:255],v[216:219]
v_cvt_pk_bf16_f32 v79,v12,v13
v_mul_f32 v14,s61,v14
v_mfma_f32_16x16x32_bf16 v[216:219],a[160:163],a[248:251],v[216:219]
v_mul_f32 v15,s61,v15
v_cvt_pk_bf16_f32 v118,v118,v119
v_mfma_f32_16x16x32_bf16 v[196:199],a[168:171],a[224:227],v[196:199]
v_cvt_pk_bf16_f32 v105,v106,v107
v_accvgpr_read_b32 v92,a56
v_mfma_f32_16x16x32_bf16 v[196:199],a[172:175],a[228:231],v[196:199]
v_accvgpr_read_b32 v93,a57
v_cvt_pk_bf16_f32 v119,v14,v15
v_mfma_f32_16x16x32_bf16 v[204:207],a[172:175],a[236:239],v[204:207]
v_accvgpr_read_b32 v106,a84
v_accvgpr_read_b32 v107,a85
v_mfma_f32_16x16x32_bf16 v[204:207],a[168:171],a[232:235],v[204:207]
v_mul_f32 v92,s61,v92
v_mul_f32 v93,s61,v93
v_mfma_f32_16x16x32_bf16 v[212:215],a[168:171],a[240:243],v[212:215]
v_accvgpr_read_b32 v94,a58
v_accvgpr_read_b32 v95,a59
v_mfma_f32_16x16x32_bf16 v[212:215],a[172:175],a[244:247],v[212:215]
v_accvgpr_read_b32 v14,a86
v_accvgpr_read_b32 v15,a87
v_mfma_f32_16x16x32_bf16 v[220:223],a[172:175],a[252:255],v[220:223]
v_mul_f32 v106,s61,v106
v_mul_f32 v107,s61,v107
v_mfma_f32_16x16x32_bf16 v[220:223],a[168:171],a[248:251],v[220:223]
v_cvt_pk_bf16_f32 v92,v92,v93
v_mul_f32 v94,s61,v94
v_mfma_f32_16x16x32_bf16 v[224:227],a[176:179],a[224:227],v[224:227]
v_mul_f32 v95,s61,v95
v_mul_f32 v14,s61,v14
v_mfma_f32_16x16x32_bf16 v[224:227],a[180:183],a[228:231],v[224:227]
v_mul_f32 v15,s61,v15
v_cvt_pk_bf16_f32 v106,v106,v107
v_mfma_f32_16x16x32_bf16 v[232:235],a[180:183],a[236:239],v[232:235]
v_cvt_pk_bf16_f32 v93,v94,v95
v_cvt_pk_bf16_f32 v107,v14,v15
v_mfma_f32_16x16x32_bf16 v[232:235],a[176:179],a[232:235],v[232:235]
v_accvgpr_read_b32 v94,a60
v_accvgpr_read_b32 v95,a61
v_mfma_f32_16x16x32_bf16 v[240:243],a[176:179],a[240:243],v[240:243]
v_accvgpr_read_b32 v120,a112
v_accvgpr_read_b32 v121,a113
v_mfma_f32_16x16x32_bf16 v[240:243],a[180:183],a[244:247],v[240:243]
buffer_load_dwordx4 v5,s[24:27],s47 offen offset:3072 nt lds
v_mfma_f32_16x16x32_bf16 v[248:251],a[180:183],a[252:255],v[248:251]
s_add_u32 s47,s47,16384
v_accvgpr_read_b32 v14,a62
v_accvgpr_read_b32 v15,a63
v_mfma_f32_16x16x32_bf16 v[248:251],a[176:179],a[248:251],v[248:251]
s_and_b32 s47,s47,0x3ffff
v_mul_f32 v94,s61,v94
v_mul_f32 v95,s61,v95
v_mfma_f32_16x16x32_bf16 v[228:231],a[184:187],a[224:227],v[228:231]
s_or_b32 s47,s47,s46
v_mul_f32 v120,s61,v120
v_mul_f32 v121,s61,v121
v_mfma_f32_16x16x32_bf16 v[228:231],a[188:191],a[228:231],v[228:231]
s_and_b32 s46,s54,0xfffc0000
v_accvgpr_read_b32 v122,a114
v_accvgpr_read_b32 v123,a115
v_mfma_f32_16x16x32_bf16 v[236:239],a[188:191],a[236:239],v[236:239]
s_add_u32 s54,s54,16384
v_mul_f32 v14,s61,v14
v_mul_f32 v15,s61,v15
v_mfma_f32_16x16x32_bf16 v[236:239],a[184:187],a[232:235],v[236:239]
v_cvt_pk_bf16_f32 v94,v94,v95
v_cvt_pk_bf16_f32 v120,v120,v121
v_mfma_f32_16x16x32_bf16 v[244:247],a[184:187],a[240:243],v[244:247]
v_mul_f32 v122,s61,v122
v_mul_f32 v123,s61,v123
v_mfma_f32_16x16x32_bf16 v[244:247],a[188:191],a[244:247],v[244:247]
v_cvt_pk_bf16_f32 v95,v14,v15
v_cvt_pk_bf16_f32 v121,v122,v123
v_mfma_f32_16x16x32_bf16 v[252:255],a[188:191],a[252:255],v[252:255]
v_accvgpr_read_b32 v108,a88
v_accvgpr_read_b32 v109,a89
v_mfma_f32_16x16x32_bf16 v[252:255],a[184:187],a[248:251],v[252:255]
s_waitcnt lgkmcnt(0)
v_accvgpr_read_b32 v122,a116
v_accvgpr_read_b32 v123,a117
v_mfma_f32_16x16x32_bf16 v[32:35],v[48:51],v[64:67],0
v_mul_f32 v108,s61,v108
v_mul_f32 v109,s61,v109
v_mfma_f32_16x16x32_bf16 v[32:35],v[52:55],v[68:71],v[32:35]
v_accvgpr_read_b32 v110,a90
v_accvgpr_read_b32 v111,a91
v_mfma_f32_16x16x32_bf16 v[32:35],v[56:59],v[72:75],v[32:35]
v_accvgpr_read_b32 v16,a118
v_accvgpr_read_b32 v17,a119
v_mfma_f32_16x16x32_bf16 v[32:35],v[60:63],v[76:79],v[32:35]
)A"
R"A(v_mul_f32 v122,s61,v122
v_mul_f32 v123,s61,v123
v_mfma_f32_16x16x32_bf16 v[36:39],v[48:51],v[80:83],0
v_cvt_pk_bf16_f32 v108,v108,v109
v_mul_f32 v110,s61,v110
v_mfma_f32_16x16x32_bf16 v[36:39],v[52:55],v[84:87],v[36:39]
v_mul_f32 v111,s61,v111
v_mul_f32 v16,s61,v16
v_mfma_f32_16x16x32_bf16 v[36:39],v[56:59],v[88:91],v[36:39]
v_mul_f32 v17,s61,v17
v_cvt_pk_bf16_f32 v122,v122,v123
v_cvt_pk_bf16_f32 v109,v110,v111
v_mfma_f32_16x16x32_bf16 v[36:39],v[60:63],v[92:95],v[36:39]
ds_read_b128 a[128:131],v1
v_cvt_pk_bf16_f32 v123,v16,v17
v_accvgpr_read_b32 v110,a92
v_accvgpr_read_b32 v111,a93
v_accvgpr_read_b32 v16,a94
v_accvgpr_read_b32 v17,a95
v_mul_f32 v110,s61,v110
v_mul_f32 v111,s61,v111
v_mfma_f32_16x16x32_bf16 v[40:43],v[48:51],v[96:99],0
ds_read_b128 a[132:135],v1 offset:256
v_mul_f32 v16,s61,v16
v_mul_f32 v17,s61,v17
v_cvt_pk_bf16_f32 v110,v110,v111
v_cvt_pk_bf16_f32 v111,v16,v17
v_mfma_f32_16x16x32_bf16 v[40:43],v[52:55],v[100:103],v[40:43]
ds_read_b128 a[136:139],v1 offset:512
v_accvgpr_read_b32 v124,a120
v_accvgpr_read_b32 v125,a121
v_mul_f32 v124,s61,v124
v_mul_f32 v125,s61,v125
v_accvgpr_read_b32 v126,a122
v_accvgpr_read_b32 v127,a123
v_mfma_f32_16x16x32_bf16 v[40:43],v[56:59],v[104:107],v[40:43]
ds_read_b128 a[140:143],v1 offset:768
v_cvt_pk_bf16_f32 v124,v124,v125
v_mul_f32 v126,s61,v126
v_mul_f32 v127,s61,v127
v_cvt_pk_bf16_f32 v125,v126,v127
v_mfma_f32_16x16x32_bf16 v[40:43],v[60:63],v[108:111],v[40:43]
s_add_u32 m0,0,s51
ds_read_b128 a[156:159],v1 offset:4864
v_accvgpr_read_b32 v126,a124
v_accvgpr_read_b32 v127,a125
v_accvgpr_read_b32 v12,a126
v_accvgpr_read_b32 v13,a127
v_mul_f32 v126,s61,v126
v_mul_f32 v127,s61,v127
v_mfma_f32_16x16x32_bf16 v[44:47],v[48:51],v[112:115],0
ds_read_b128 a[152:155],v1 offset:4608
v_mul_f32 v12,s61,v12
v_mul_f32 v13,s61,v13
v_cvt_pk_bf16_f32 v126,v126,v127
v_max3_f32 v32,v32,v33,v34
v_cvt_pk_bf16_f32 v127,v12,v13
v_max3_f32 v32,v32,v35,s18
v_mfma_f32_16x16x32_bf16 v[44:47],v[52:55],v[116:119],v[44:47]
ds_read_b128 a[148:151],v1 offset:4352
v_sub_f32 v35,0,v32
v_sub_f32 v34,0,v32
v_sub_f32 v33,0,v32
v_sub_f32 v32,0,v32
v_mfma_f32_16x16x32_bf16 v[44:47],v[56:59],v[120:123],v[44:47]
ds_read_b128 a[144:147],v1 offset:4096
v_mul_f32 v128,s61,v128
v_mul_f32 v129,s61,v129
v_cvt_pk_bf16_f32 v128,v128,v129
v_mul_f32 v130,s61,v130
v_mul_f32 v131,s61,v131
v_mfma_f32_16x16x32_bf16 v[44:47],v[60:63],v[124:127],v[44:47]
s_waitcnt lgkmcnt(7)
buffer_load_dwordx4 v4,s[20:23],s47 offen nt lds
ds_read_b128 a[160:163],v1 offset:8192
v_cvt_pk_bf16_f32 v129,v130,v131
v_mul_f32 v132,s61,v132
v_mul_f32 v133,s61,v133
v_mul_f32 v134,s61,v134
v_mul_f32 v135,s61,v135
v_cvt_pk_bf16_f32 v130,v132,v133
v_cvt_pk_bf16_f32 v131,v134,v135
v_mfma_f32_16x16x32_bf16 v[132:135],a[128:131],v[64:67],v[32:35]
s_waitcnt lgkmcnt(7)
ds_read_b128 a[164:167],v1 offset:8448
v_mfma_f32_16x16x32_bf16 v[132:135],a[132:135],v[68:71],v[132:135]
s_waitcnt lgkmcnt(7)
ds_read_b128 a[168:171],v1 offset:8704
v_mfma_f32_16x16x32_bf16 v[132:135],a[136:139],v[72:75],v[132:135]
s_waitcnt lgkmcnt(7)
ds_read_b128 a[172:175],v1 offset:8960
v_mul_f32 v136,s61,v136
v_mul_f32 v137,s61,v137
v_cvt_pk_bf16_f32 v136,v136,v137
v_mul_f32 v138,s61,v138
v_mul_f32 v139,s61,v139
v_mfma_f32_16x16x32_bf16 v[132:135],a[140:143],v[76:79],v[132:135]
s_waitcnt lgkmcnt(7)
ds_read_b128 a[188:191],v1 offset:13056
v_cvt_pk_bf16_f32 v137,v138,v139
v_mul_f32 v140,s61,v140
v_mul_f32 v141,s61,v141
v_mul_f32 v142,s61,v142
v_mul_f32 v143,s61,v143
v_cvt_pk_bf16_f32 v138,v140,v141
v_cvt_pk_bf16_f32 v139,v142,v143
v_mfma_f32_16x16x32_bf16 v[140:143],a[156:159],v[76:79],v[32:35]
s_waitcnt lgkmcnt(7)
ds_read_b128 a[184:187],v1 offset:12800
v_mfma_f32_16x16x32_bf16 v[140:143],a[152:155],v[72:75],v[140:143]
s_waitcnt lgkmcnt(7)
ds_read_b128 a[180:183],v1 offset:12544
v_mfma_f32_16x16x32_bf16 v[140:143],a[148:151],v[68:71],v[140:143]
s_waitcnt lgkmcnt(7)
ds_read_b128 a[176:179],v1 offset:12288
v_mul_f32 v144,s61,v144
v_mul_f32 v145,s61,v145
v_cvt_pk_bf16_f32 v144,v144,v145
v_mul_f32 v146,s61,v146
v_mul_f32 v147,s61,v147
v_mfma_f32_16x16x32_bf16 v[140:143],a[144:147],v[64:67],v[140:143]
s_waitcnt lgkmcnt(7)
v_cvt_pk_bf16_f32 v145,v146,v147
v_mul_f32 v148,s61,v148
v_mul_f32 v149,s61,v149
v_mul_f32 v150,s61,v150
v_mul_f32 v151,s61,v151
v_cvt_pk_bf16_f32 v146,v148,v149
v_cvt_pk_bf16_f32 v147,v150,v151
v_mfma_f32_16x16x32_bf16 v[148:151],a[160:163],v[64:67],v[32:35]
s_waitcnt lgkmcnt(6)
buffer_load_dwordx4 v4,s[20:23],s47 offen offset:1024 nt lds
v_mfma_f32_16x16x32_bf16 v[148:151],a[164:167],v[68:71],v[148:151]
s_waitcnt lgkmcnt(5)
v_mfma_f32_16x16x32_bf16 v[148:151],a[168:171],v[72:75],v[148:151]
s_waitcnt lgkmcnt(4)
v_mul_f32 v152,s61,v152
v_mul_f32 v153,s61,v153
v_cvt_pk_bf16_f32 v152,v152,v153
v_mul_f32 v154,s61,v154
v_mul_f32 v155,s61,v155
v_mfma_f32_16x16x32_bf16 v[148:151],a[172:175],v[76:79],v[148:151]
s_waitcnt lgkmcnt(3)
v_cvt_pk_bf16_f32 v153,v154,v155
v_mul_f32 v156,s61,v156
v_mul_f32 v157,s61,v157
v_mul_f32 v158,s61,v158
v_mul_f32 v159,s61,v159
v_cvt_pk_bf16_f32 v154,v156,v157
v_cvt_pk_bf16_f32 v155,v158,v159
v_mfma_f32_16x16x32_bf16 v[156:159],a[188:191],v[76:79],v[32:35]
s_waitcnt lgkmcnt(2)
v_max3_f32 v36,v36,v37,v38
v_max3_f32 v36,v36,v39,s19
v_mfma_f32_16x16x32_bf16 v[156:159],a[184:187],v[72:75],v[156:159]
s_waitcnt lgkmcnt(1)
v_sub_f32 v39,0,v36
v_sub_f32 v38,0,v36
v_sub_f32 v37,0,v36
v_sub_f32 v36,0,v36
v_mfma_f32_16x16x32_bf16 v[156:159],a[180:183],v[68:71],v[156:159]
s_waitcnt lgkmcnt(0)
v_mul_f32 v160,s61,v160
v_mul_f32 v161,s61,v161
v_cvt_pk_bf16_f32 v160,v160,v161
v_mul_f32 v162,s61,v162
v_mul_f32 v163,s61,v163
v_mfma_f32_16x16x32_bf16 v[156:159],a[176:179],v[64:67],v[156:159]
v_cvt_pk_bf16_f32 v161,v162,v163
v_mul_f32 v164,s61,v164
v_mul_f32 v165,s61,v165
v_mul_f32 v166,s61,v166
v_mul_f32 v167,s61,v167
v_cvt_pk_bf16_f32 v162,v164,v165
v_cvt_pk_bf16_f32 v163,v166,v167
v_mfma_f32_16x16x32_bf16 v[164:167],a[128:131],v[80:83],v[36:39]
buffer_load_dwordx4 v5,s[20:23],s47 offen offset:2048 nt lds
v_mfma_f32_16x16x32_bf16 v[164:167],a[132:135],v[84:87],v[164:167]
v_mul_f32 v168,s61,v168
v_mul_f32 v169,s61,v169
v_mfma_f32_16x16x32_bf16 v[164:167],a[136:139],v[88:91],v[164:167]
v_cvt_pk_bf16_f32 v168,v168,v169
v_mul_f32 v170,s61,v170
v_mul_f32 v171,s61,v171
v_mfma_f32_16x16x32_bf16 v[164:167],a[140:143],v[92:95],v[164:167]
v_cvt_pk_bf16_f32 v169,v170,v171
v_mul_f32 v172,s61,v172
v_mul_f32 v173,s61,v173
v_mul_f32 v174,s61,v174
v_mul_f32 v175,s61,v175
v_cvt_pk_bf16_f32 v170,v172,v173
v_cvt_pk_bf16_f32 v171,v174,v175
v_mfma_f32_16x16x32_bf16 v[172:175],a[156:159],v[92:95],v[36:39]
v_mul_f32 v176,s61,v176
v_mul_f32 v177,s61,v177
v_mfma_f32_16x16x32_bf16 v[172:175],a[152:155],v[88:91],v[172:175]
v_cvt_pk_bf16_f32 v176,v176,v177
v_mul_f32 v178,s61,v178
v_mfma_f32_16x16x32_bf16 v[172:175],a[148:151],v[84:87],v[172:175]
v_mul_f32 v179,s61,v179
v_cvt_pk_bf16_f32 v177,v178,v179
v_mfma_f32_16x16x32_bf16 v[172:175],a[144:147],v[80:83],v[172:175]
v_mul_f32 v180,s61,v180
v_mul_f32 v181,s61,v181
v_mul_f32 v182,s61,v182
v_mul_f32 v183,s61,v183
v_cvt_pk_bf16_f32 v178,v180,v181
v_cvt_pk_bf16_f32 v179,v182,v183
v_mfma_f32_16x16x32_bf16 v[180:183],a[160:163],v[80:83],v[36:39]
v_mul_f32 v184,s61,v184
v_mul_f32 v185,s61,v185
v_mfma_f32_16x16x32_bf16 v[180:183],a[164:167],v[84:87],v[180:183]
buffer_load_dwordx4 v5,s[20:23],s47 offen offset:3072 nt lds
v_mfma_f32_16x16x32_bf16 v[180:183],a[168:171],v[88:91],v[180:183]
v_cvt_pk_bf16_f32 v184,v184,v185
v_mul_f32 v186,s61,v186
v_mul_f32 v187,s61,v187
v_mfma_f32_16x16x32_bf16 v[180:183],a[172:175],v[92:95],v[180:183]
v_cvt_pk_bf16_f32 v185,v186,v187
v_mul_f32 v188,s61,v188
v_mul_f32 v189,s61,v189
v_mul_f32 v190,s61,v190
v_mul_f32 v191,s61,v191
v_cvt_pk_bf16_f32 v186,v188,v189
v_cvt_pk_bf16_f32 v187,v190,v191
v_mfma_f32_16x16x32_bf16 v[188:191],a[188:191],v[92:95],v[36:39]
v_max3_f32 v40,v40,v41,v42
v_max3_f32 v40,v40,v43,s100
v_mfma_f32_16x16x32_bf16 v[188:191],a[184:187],v[88:91],v[188:191]
v_sub_f32 v43,0,v40
v_sub_f32 v42,0,v40
v_sub_f32 v41,0,v40
v_sub_f32 v40,0,v40
v_mfma_f32_16x16x32_bf16 v[188:191],a[180:183],v[84:87],v[188:191]
v_mul_f32 v192,s61,v192
v_mul_f32 v193,s61,v193
v_cvt_pk_bf16_f32 v192,v192,v193
v_mul_f32 v194,s61,v194
v_mul_f32 v195,s61,v195
v_mfma_f32_16x16x32_bf16 v[188:191],a[176:179],v[80:83],v[188:191]
s_add_u32 m0,0,s52
v_cvt_pk_bf16_f32 v193,v194,v195
v_mul_f32 v196,s61,v196
v_mul_f32 v197,s61,v197
v_mul_f32 v198,s61,v198
v_mul_f32 v199,s61,v199
v_cvt_pk_bf16_f32 v194,v196,v197
v_cvt_pk_bf16_f32 v195,v198,v199
v_mfma_f32_16x16x32_bf16 v[196:199],a[128:131],v[96:99],v[40:43]
v_mul_f32 v200,s61,v200
v_mul_f32 v201,s61,v201
v_mfma_f32_16x16x32_bf16 v[196:199],a[132:135],v[100:103],v[196:199]
v_cvt_pk_bf16_f32 v200,v200,v201
v_mul_f32 v202,s61,v202
v_mfma_f32_16x16x32_bf16 v[196:199],a[136:139],v[104:107],v[196:199]
v_mul_f32 v203,s61,v203
v_cvt_pk_bf16_f32 v201,v202,v203
v_mfma_f32_16x16x32_bf16 v[196:199],a[140:143],v[108:111],v[196:199]
buffer_load_dwordx4 v4,s[24:27],s47 offen nt lds
v_mul_f32 v204,s61,v204
v_mul_f32 v205,s61,v205
v_mul_f32 v206,s61,v206
v_mul_f32 v207,s61,v207
v_cvt_pk_bf16_f32 v202,v204,v205
v_cvt_pk_bf16_f32 v203,v206,v207
v_mfma_f32_16x16x32_bf16 v[204:207],a[156:159],v[108:111],v[40:43]
v_mul_f32 v208,s61,v208
v_mul_f32 v209,s61,v209
v_mfma_f32_16x16x32_bf16 v[204:207],a[152:155],v[104:107],v[204:207]
v_cvt_pk_bf16_f32 v208,v208,v209
v_mul_f32 v210,s61,v210
v_mfma_f32_16x16x32_bf16 v[204:207],a[148:151],v[100:103],v[204:207]
v_mul_f32 v211,s61,v211
v_cvt_pk_bf16_f32 v209,v210,v211
v_mfma_f32_16x16x32_bf16 v[204:207],a[144:147],v[96:99],v[204:207]
ds_read_b64_tr_b16 a[192:193],v2
ds_read_b64_tr_b16 a[194:195],v2 offset:4096
v_mul_f32 v212,s61,v212
v_mul_f32 v213,s61,v213
v_mul_f32 v214,s61,v214
v_mul_f32 v215,s61,v215
v_cvt_pk_bf16_f32 v210,v212,v213
v_cvt_pk_bf16_f32 v211,v214,v215
v_mfma_f32_16x16x32_bf16 v[212:215],a[160:163],v[96:99],v[40:43]
ds_read_b64_tr_b16 a[196:197],v2 offset:8192
ds_read_b64_tr_b16 a[198:199],v2 offset:12288
v_mfma_f32_16x16x32_bf16 v[212:215],a[164:167],v[100:103],v[212:215]
ds_read_b64_tr_b16 a[204:205],v3 offset:8192
ds_read_b64_tr_b16 a[206:207],v3 offset:12288
v_mfma_f32_16x16x32_bf16 v[212:215],a[168:171],v[104:107],v[212:215]
ds_read_b64_tr_b16 a[200:201],v3
ds_read_b64_tr_b16 a[202:203],v3 offset:4096
v_mul_f32 v216,s61,v216
v_mul_f32 v217,s61,v217
v_cvt_pk_bf16_f32 v216,v216,v217
v_mul_f32 v218,s61,v218
v_mul_f32 v219,s61,v219
v_mfma_f32_16x16x32_bf16 v[212:215],a[172:175],v[108:111],v[212:215]
ds_read_b64_tr_b16 a[208:209],v2 offset:256
ds_read_b64_tr_b16 a[210:211],v2 offset:4352
v_cvt_pk_bf16_f32 v217,v218,v219
v_mul_f32 v220,s61,v220
v_mul_f32 v221,s61,v221
v_mul_f32 v222,s61,v222
v_mul_f32 v223,s61,v223
v_cvt_pk_bf16_f32 v218,v220,v221
v_cvt_pk_bf16_f32 v219,v222,v223
v_exp_f32 v132,v132
v_exp_f32 v133,v133
v_mfma_f32_16x16x32_bf16 v[220:223],a[188:191],v[108:111],v[40:43]
buffer_load_dwordx4 v5,s[24:27],s47 offen offset:1024 nt lds
ds_read_b64_tr_b16 a[212:213],v2 offset:8448
ds_read_b64_tr_b16 a[214:215],v2 offset:12544
v_exp_f32 v134,v134
v_exp_f32 v135,v135
v_cvt_pk_bf16_f32 v132,v132,v133
v_exp_f32 v140,v140
v_exp_f32 v141,v141
v_exp_f32 v142,v142
v_exp_f32 v143,v143
v_exp_f32 v148,v148
v_exp_f32 v149,v149
v_mfma_f32_16x16x32_bf16 v[220:223],a[184:187],v[104:107],v[220:223]
ds_read_b64_tr_b16 a[220:221],v3 offset:8448
ds_read_b64_tr_b16 a[222:223],v3 offset:12544
v_cvt_pk_bf16_f32 v133,v134,v135
v_exp_f32 v150,v150
v_exp_f32 v151,v151
v_cvt_pk_bf16_f32 v134,v140,v141
v_cvt_pk_bf16_f32 v135,v142,v143
v_cvt_pk_bf16_f32 v148,v148,v149
v_exp_f32 v156,v156
v_exp_f32 v157,v157
v_exp_f32 v158,v158
v_exp_f32 v159,v159
v_mfma_f32_16x16x32_bf16 v[220:223],a[180:183],v[100:103],v[220:223]
ds_read_b64_tr_b16 a[216:217],v3 offset:256
ds_read_b64_tr_b16 a[218:219],v3 offset:4352
v_cvt_pk_bf16_f32 v149,v150,v151
v_cvt_pk_bf16_f32 v150,v156,v157
v_cvt_pk_bf16_f32 v151,v158,v159
v_mfma_f32_16x16x32_bf16 v[220:223],a[176:179],v[96:99],v[220:223]
s_waitcnt lgkmcnt(14)
ds_read_b64_tr_b16 a[224:225],v2 offset:512
ds_read_b64_tr_b16 a[226:227],v2 offset:4608
)A"
R"A(v_mfma_f32_16x16x32_bf16 a[0:3],a[192:195],v[132:135],0
s_waitcnt lgkmcnt(14)
ds_read_b64_tr_b16 a[228:229],v2 offset:8704
ds_read_b64_tr_b16 a[230:231],v2 offset:12800
v_mfma_f32_16x16x32_bf16 a[0:3],a[196:199],v[148:151],a[0:3]
s_waitcnt lgkmcnt(14)
ds_read_b64_tr_b16 a[236:237],v3 offset:8704
ds_read_b64_tr_b16 a[238:239],v3 offset:12800
v_mfma_f32_16x16x32_bf16 a[4:7],a[204:207],v[148:151],0
s_waitcnt lgkmcnt(14)
ds_read_b64_tr_b16 a[232:233],v3 offset:512
ds_read_b64_tr_b16 a[234:235],v3 offset:4608
v_mfma_f32_16x16x32_bf16 a[4:7],a[200:203],v[132:135],a[4:7]
s_waitcnt lgkmcnt(14)
ds_read_b64_tr_b16 a[240:241],v2 offset:768
ds_read_b64_tr_b16 a[242:243],v2 offset:4864
v_mfma_f32_16x16x32_bf16 a[8:11],a[208:211],v[132:135],0
s_waitcnt lgkmcnt(14)
ds_read_b64_tr_b16 a[244:245],v2 offset:8960
ds_read_b64_tr_b16 a[246:247],v2 offset:13056
v_mfma_f32_16x16x32_bf16 a[8:11],a[212:215],v[148:151],a[8:11]
s_memrealtime s[76:77]
buffer_load_dwordx4 v4,s[24:27],s47 offen offset:2048 nt lds
ds_read_b64_tr_b16 a[252:253],v3 offset:8960
ds_read_b64_tr_b16 a[254:255],v3 offset:13056
s_waitcnt lgkmcnt(15)
v_mfma_f32_16x16x32_bf16 a[12:15],a[220:223],v[148:151],0
s_waitcnt lgkmcnt(14)
ds_read_b64_tr_b16 a[248:249],v3 offset:768
ds_read_b64_tr_b16 a[250:251],v3 offset:4864
v_mfma_f32_16x16x32_bf16 a[12:15],a[216:219],v[132:135],a[12:15]
s_waitcnt lgkmcnt(14)
v_mfma_f32_16x16x32_bf16 a[16:19],a[224:227],v[132:135],0
s_waitcnt lgkmcnt(12)
v_mfma_f32_16x16x32_bf16 a[16:19],a[228:231],v[148:151],a[16:19]
s_waitcnt lgkmcnt(10)
v_mfma_f32_16x16x32_bf16 a[20:23],a[236:239],v[148:151],0
s_waitcnt lgkmcnt(8)
v_mfma_f32_16x16x32_bf16 a[20:23],a[232:235],v[132:135],a[20:23]
s_waitcnt lgkmcnt(0)
v_mfma_f32_16x16x32_bf16 a[24:27],a[240:243],v[132:135],0
v_mfma_f32_16x16x32_bf16 a[24:27],a[244:247],v[148:151],a[24:27]
v_mfma_f32_16x16x32_bf16 a[28:31],a[252:255],v[148:151],0
buffer_load_dwordx4 v5,s[24:27],s47 offen offset:3072 nt lds
v_max3_f32 v44,v44,v45,v46
v_max3_f32 v44,v44,v47,s101
v_mfma_f32_16x16x32_bf16 a[28:31],a[248:251],v[132:135],a[28:31]
v_sub_f32 v47,0,v44
v_sub_f32 v46,0,v44
v_sub_f32 v45,0,v44
v_sub_f32 v44,0,v44
v_mfma_f32_16x16x32_bf16 v[48:51],v[28:31],v[132:135],0
v_mul_f32 v224,s61,v224
v_mul_f32 v225,s61,v225
v_cvt_pk_bf16_f32 v224,v224,v225
v_mul_f32 v226,s61,v226
v_mul_f32 v227,s61,v227
v_mfma_f32_16x16x32_bf16 v[48:51],v[28:31],v[148:151],v[48:51]
v_cvt_pk_bf16_f32 v225,v226,v227
v_mul_f32 v228,s61,v228
v_mul_f32 v229,s61,v229
v_mul_f32 v230,s61,v230
v_mul_f32 v231,s61,v231
v_cvt_pk_bf16_f32 v226,v228,v229
v_cvt_pk_bf16_f32 v227,v230,v231
v_mfma_f32_16x16x32_bf16 v[228:231],a[128:131],v[112:115],v[44:47]
s_waitcnt vmcnt(8)
s_add_u32 s47,s47,16384
v_mul_f32 v232,s61,v232
v_mul_f32 v233,s61,v233
v_mfma_f32_16x16x32_bf16 v[228:231],a[132:135],v[116:119],v[228:231]
s_barrier
v_cvt_pk_bf16_f32 v232,v232,v233
v_mul_f32 v234,s61,v234
v_mfma_f32_16x16x32_bf16 v[228:231],a[136:139],v[120:123],v[228:231]
s_add_u32 m0,s49,s51
v_mul_f32 v235,s61,v235
v_cvt_pk_bf16_f32 v233,v234,v235
v_mfma_f32_16x16x32_bf16 v[228:231],a[140:143],v[124:127],v[228:231]
v_mul_f32 v236,s61,v236
v_mul_f32 v237,s61,v237
v_mul_f32 v238,s61,v238
v_mul_f32 v239,s61,v239
v_cvt_pk_bf16_f32 v234,v236,v237
v_cvt_pk_bf16_f32 v235,v238,v239
v_mfma_f32_16x16x32_bf16 v[236:239],a[156:159],v[124:127],v[44:47]
s_and_b32 s47,s47,0x3ffff
v_mul_f32 v240,s61,v240
v_mul_f32 v241,s61,v241
v_mfma_f32_16x16x32_bf16 v[236:239],a[152:155],v[120:123],v[236:239]
s_or_b32 s47,s47,s46
v_cvt_pk_bf16_f32 v240,v240,v241
v_mul_f32 v242,s61,v242
v_mfma_f32_16x16x32_bf16 v[236:239],a[148:151],v[116:119],v[236:239]
s_add_u32 s78,s76,s60
v_mul_f32 v243,s61,v243
v_cvt_pk_bf16_f32 v241,v242,v243
v_mfma_f32_16x16x32_bf16 v[236:239],a[144:147],v[112:115],v[236:239]
buffer_load_dwordx4 v4,s[20:23],s47 offen nt lds
v_mul_f32 v244,s61,v244
v_mul_f32 v245,s61,v245
v_mul_f32 v246,s61,v246
v_mul_f32 v247,s61,v247
v_cvt_pk_bf16_f32 v242,v244,v245
v_cvt_pk_bf16_f32 v243,v246,v247
v_mfma_f32_16x16x32_bf16 v[244:247],a[160:163],v[112:115],v[44:47]
s_and_b32 s46,s54,0xfffc0000
v_mul_f32 v248,s61,v248
v_mul_f32 v249,s61,v249
v_mfma_f32_16x16x32_bf16 v[244:247],a[164:167],v[116:119],v[244:247]
v_cvt_pk_bf16_f32 v248,v248,v249
v_mul_f32 v250,s61,v250
v_mfma_f32_16x16x32_bf16 v[244:247],a[168:171],v[120:123],v[244:247]
v_mul_f32 v251,s61,v251
v_cvt_pk_bf16_f32 v249,v250,v251
v_mfma_f32_16x16x32_bf16 v[244:247],a[172:175],v[124:127],v[244:247]
v_mul_f32 v252,s61,v252
v_mul_f32 v253,s61,v253
v_mul_f32 v254,s61,v254
v_mul_f32 v255,s61,v255
v_cvt_pk_bf16_f32 v250,v252,v253
v_cvt_pk_bf16_f32 v251,v254,v255
v_exp_f32 v164,v164
v_exp_f32 v165,v165
v_mfma_f32_16x16x32_bf16 v[252:255],a[188:191],v[124:127],v[44:47]
v_exp_f32 v166,v166
v_exp_f32 v167,v167
v_cvt_pk_bf16_f32 v164,v164,v165
v_exp_f32 v172,v172
v_exp_f32 v173,v173
v_exp_f32 v174,v174
v_exp_f32 v175,v175
v_exp_f32 v180,v180
v_exp_f32 v181,v181
v_mfma_f32_16x16x32_bf16 v[252:255],a[184:187],v[120:123],v[252:255]
v_cvt_pk_bf16_f32 v165,v166,v167
v_exp_f32 v182,v182
v_exp_f32 v183,v183
v_cvt_pk_bf16_f32 v166,v172,v173
v_cvt_pk_bf16_f32 v167,v174,v175
v_cvt_pk_bf16_f32 v180,v180,v181
v_exp_f32 v188,v188
v_exp_f32 v189,v189
v_exp_f32 v190,v190
v_exp_f32 v191,v191
v_mfma_f32_16x16x32_bf16 v[252:255],a[180:183],v[116:119],v[252:255]
v_cvt_pk_bf16_f32 v181,v182,v183
v_cvt_pk_bf16_f32 v182,v188,v189
v_cvt_pk_bf16_f32 v183,v190,v191
v_mfma_f32_16x16x32_bf16 v[252:255],a[176:179],v[112:115],v[252:255]
v_add_u32 v1,s48,v1
v_add_u32 v2,s48,v2
v_mfma_f32_16x16x32_bf16 a[32:35],a[192:195],v[164:167],0
ds_read_b128 a[128:131],v1
v_add_u32 v3,s48,v3
v_mfma_f32_16x16x32_bf16 a[32:35],a[196:199],v[180:183],a[32:35]
s_sub_u32 s48,32768,s62
v_exp_f32 v196,v196
v_mfma_f32_16x16x32_bf16 a[36:39],a[204:207],v[180:183],0
s_add_u32 s50,s50,s48
buffer_load_dwordx4 v4,s[20:23],s47 offen offset:1024 nt lds
v_mfma_f32_16x16x32_bf16 a[36:39],a[200:203],v[164:167],a[36:39]
ds_read_b128 a[132:135],v1 offset:256
v_mfma_f32_16x16x32_bf16 a[40:43],a[208:211],v[164:167],0
v_exp_f32 v197,v197
v_mfma_f32_16x16x32_bf16 a[40:43],a[212:215],v[180:183],a[40:43]
ds_read_b128 a[136:139],v1 offset:512
v_cvt_pk_bf16_f32 v196,v196,v197
v_mfma_f32_16x16x32_bf16 a[44:47],a[220:223],v[180:183],0
v_exp_f32 v198,v198
v_mfma_f32_16x16x32_bf16 a[44:47],a[216:219],v[164:167],a[44:47]
ds_read_b128 a[140:143],v1 offset:768
v_mfma_f32_16x16x32_bf16 a[48:51],a[224:227],v[164:167],0
v_exp_f32 v199,v199
v_mfma_f32_16x16x32_bf16 a[48:51],a[228:231],v[180:183],a[48:51]
ds_read_b128 a[156:159],v1 offset:4864
v_cvt_pk_bf16_f32 v197,v198,v199
v_mfma_f32_16x16x32_bf16 a[52:55],a[236:239],v[180:183],0
v_exp_f32 v204,v204
v_mfma_f32_16x16x32_bf16 a[52:55],a[232:235],v[164:167],a[52:55]
ds_read_b128 a[152:155],v1 offset:4608
v_mfma_f32_16x16x32_bf16 a[56:59],a[240:243],v[164:167],0
v_exp_f32 v205,v205
v_mfma_f32_16x16x32_bf16 a[56:59],a[244:247],v[180:183],a[56:59]
ds_read_b128 a[148:151],v1 offset:4352
v_cvt_pk_bf16_f32 v198,v204,v205
v_mfma_f32_16x16x32_bf16 a[60:63],a[252:255],v[180:183],0
v_exp_f32 v206,v206
v_mfma_f32_16x16x32_bf16 a[60:63],a[248:251],v[164:167],a[60:63]
ds_read_b128 a[144:147],v1 offset:4096
v_mfma_f32_16x16x32_bf16 v[52:55],v[28:31],v[164:167],0
buffer_load_dwordx4 v5,s[20:23],s47 offen offset:2048 nt lds
v_mfma_f32_16x16x32_bf16 v[52:55],v[28:31],v[180:183],v[52:55]
s_waitcnt lgkmcnt(3)
ds_read_b128 a[160:163],v1 offset:8192
v_mfma_f32_16x16x32_bf16 v[132:135],a[128:131],v[64:67],v[32:35]
ds_read_b128 a[164:167],v1 offset:8448
v_mfma_f32_16x16x32_bf16 v[132:135],a[132:135],v[68:71],v[132:135]
ds_read_b128 a[168:171],v1 offset:8704
v_mfma_f32_16x16x32_bf16 v[132:135],a[136:139],v[72:75],v[132:135]
ds_read_b128 a[172:175],v1 offset:8960
v_mfma_f32_16x16x32_bf16 v[132:135],a[140:143],v[76:79],v[132:135]
ds_read_b128 a[188:191],v1 offset:13056
v_mfma_f32_16x16x32_bf16 v[140:143],a[156:159],v[76:79],v[32:35]
s_waitcnt lgkmcnt(6)
ds_read_b128 a[184:187],v1 offset:12800
v_mfma_f32_16x16x32_bf16 v[140:143],a[152:155],v[72:75],v[140:143]
ds_read_b128 a[180:183],v1 offset:12544
v_mfma_f32_16x16x32_bf16 v[140:143],a[148:151],v[68:71],v[140:143]
s_waitcnt lgkmcnt(7)
ds_read_b128 a[176:179],v1 offset:12288
v_mfma_f32_16x16x32_bf16 v[140:143],a[144:147],v[64:67],v[140:143]
s_waitcnt lgkmcnt(7)
v_mfma_f32_16x16x32_bf16 v[148:151],a[160:163],v[64:67],v[32:35]
s_waitcnt lgkmcnt(6)
v_mfma_f32_16x16x32_bf16 v[148:151],a[164:167],v[68:71],v[148:151]
s_waitcnt lgkmcnt(5)
v_mfma_f32_16x16x32_bf16 v[148:151],a[168:171],v[72:75],v[148:151]
s_waitcnt lgkmcnt(4)
v_mfma_f32_16x16x32_bf16 v[148:151],a[172:175],v[76:79],v[148:151]
s_waitcnt lgkmcnt(3)
v_exp_f32 v207,v207
v_mfma_f32_16x16x32_bf16 v[156:159],a[188:191],v[76:79],v[32:35]
s_waitcnt lgkmcnt(2)
v_exp_f32 v212,v212
v_exp_f32 v213,v213
v_mfma_f32_16x16x32_bf16 v[156:159],a[184:187],v[72:75],v[156:159]
s_waitcnt lgkmcnt(1)
v_exp_f32 v214,v214
v_exp_f32 v215,v215
v_cvt_pk_bf16_f32 v199,v206,v207
v_cvt_pk_bf16_f32 v212,v212,v213
v_exp_f32 v220,v220
v_exp_f32 v221,v221
v_exp_f32 v222,v222
v_exp_f32 v223,v223
v_mfma_f32_16x16x32_bf16 v[156:159],a[180:183],v[68:71],v[156:159]
s_waitcnt lgkmcnt(0)
v_cvt_pk_bf16_f32 v213,v214,v215
v_cvt_pk_bf16_f32 v214,v220,v221
v_cvt_pk_bf16_f32 v215,v222,v223
v_mfma_f32_16x16x32_bf16 v[156:159],a[176:179],v[64:67],v[156:159]
buffer_load_dwordx4 v5,s[20:23],s47 offen offset:3072 nt lds
v_mfma_f32_16x16x32_bf16 a[64:67],a[192:195],v[196:199],0
v_exp_f32 v228,v228
v_mfma_f32_16x16x32_bf16 a[64:67],a[196:199],v[212:215],a[64:67]
v_exp_f32 v229,v229
v_mfma_f32_16x16x32_bf16 a[68:71],a[204:207],v[212:215],0
v_exp_f32 v230,v230
v_mfma_f32_16x16x32_bf16 a[68:71],a[200:203],v[196:199],a[68:71]
v_exp_f32 v231,v231
v_mfma_f32_16x16x32_bf16 a[72:75],a[208:211],v[196:199],0
v_cvt_pk_bf16_f32 v228,v228,v229
v_cvt_pk_bf16_f32 v229,v230,v231
v_mfma_f32_16x16x32_bf16 a[72:75],a[212:215],v[212:215],a[72:75]
v_exp_f32 v236,v236
v_mfma_f32_16x16x32_bf16 a[76:79],a[220:223],v[212:215],0
v_exp_f32 v237,v237
v_mfma_f32_16x16x32_bf16 a[76:79],a[216:219],v[196:199],a[76:79]
s_add_u32 m0,s49,s52
v_exp_f32 v238,v238
v_mfma_f32_16x16x32_bf16 a[80:83],a[224:227],v[196:199],0
s_add_u32 s49,s49,32768
v_exp_f32 v239,v239
v_mfma_f32_16x16x32_bf16 a[80:83],a[228:231],v[212:215],a[80:83]
v_exp_f32 v244,v244
v_mfma_f32_16x16x32_bf16 a[84:87],a[236:239],v[212:215],0
buffer_load_dwordx4 v4,s[24:27],s47 offen nt lds
v_mfma_f32_16x16x32_bf16 a[84:87],a[232:235],v[196:199],a[84:87]
v_exp_f32 v245,v245
v_mfma_f32_16x16x32_bf16 a[88:91],a[240:243],v[196:199],0
v_exp_f32 v246,v246
v_mfma_f32_16x16x32_bf16 a[88:91],a[244:247],v[212:215],a[88:91]
v_exp_f32 v247,v247
v_mfma_f32_16x16x32_bf16 a[92:95],a[252:255],v[212:215],0
v_cvt_pk_bf16_f32 v230,v236,v237
v_cvt_pk_bf16_f32 v231,v238,v239
v_mfma_f32_16x16x32_bf16 a[92:95],a[248:251],v[196:199],a[92:95]
v_cvt_pk_bf16_f32 v244,v244,v245
v_cvt_pk_bf16_f32 v245,v246,v247
v_mfma_f32_16x16x32_bf16 v[56:59],v[28:31],v[196:199],0
v_exp_f32 v252,v252
v_mfma_f32_16x16x32_bf16 v[56:59],v[28:31],v[212:215],v[56:59]
v_exp_f32 v253,v253
v_mfma_f32_16x16x32_bf16 v[164:167],a[128:131],v[80:83],v[36:39]
v_exp_f32 v254,v254
v_mfma_f32_16x16x32_bf16 v[164:167],a[132:135],v[84:87],v[164:167]
v_exp_f32 v255,v255
v_mfma_f32_16x16x32_bf16 v[164:167],a[136:139],v[88:91],v[164:167]
v_cvt_pk_bf16_f32 v246,v252,v253
v_cvt_pk_bf16_f32 v247,v254,v255
v_mfma_f32_16x16x32_bf16 v[164:167],a[140:143],v[92:95],v[164:167]
v_exp_f32 v132,v132
v_mfma_f32_16x16x32_bf16 v[172:175],a[156:159],v[92:95],v[36:39]
v_exp_f32 v133,v133
v_mfma_f32_16x16x32_bf16 v[172:175],a[152:155],v[88:91],v[172:175]
v_exp_f32 v134,v134
v_mfma_f32_16x16x32_bf16 v[172:175],a[148:151],v[84:87],v[172:175]
buffer_load_dwordx4 v5,s[24:27],s47 offen offset:1024 nt lds
v_mfma_f32_16x16x32_bf16 v[172:175],a[144:147],v[80:83],v[172:175]
v_exp_f32 v135,v135
v_mfma_f32_16x16x32_bf16 v[180:183],a[160:163],v[80:83],v[36:39]
v_cvt_pk_bf16_f32 v132,v132,v133
v_cvt_pk_bf16_f32 v133,v134,v135
v_mfma_f32_16x16x32_bf16 v[180:183],a[164:167],v[84:87],v[180:183]
v_exp_f32 v140,v140
)A"
R"A(v_mfma_f32_16x16x32_bf16 v[180:183],a[168:171],v[88:91],v[180:183]
v_exp_f32 v141,v141
v_mfma_f32_16x16x32_bf16 v[180:183],a[172:175],v[92:95],v[180:183]
v_exp_f32 v142,v142
v_mfma_f32_16x16x32_bf16 v[188:191],a[188:191],v[92:95],v[36:39]
v_exp_f32 v143,v143
v_mfma_f32_16x16x32_bf16 v[188:191],a[184:187],v[88:91],v[188:191]
v_exp_f32 v148,v148
v_mfma_f32_16x16x32_bf16 v[188:191],a[180:183],v[84:87],v[188:191]
v_exp_f32 v149,v149
v_mfma_f32_16x16x32_bf16 v[188:191],a[176:179],v[80:83],v[188:191]
v_exp_f32 v150,v150
v_mfma_f32_16x16x32_bf16 a[96:99],a[192:195],v[228:231],0
ds_read_b64_tr_b16 a[192:193],v2
ds_read_b64_tr_b16 a[194:195],v2 offset:4096
v_mfma_f32_16x16x32_bf16 a[96:99],a[196:199],v[244:247],a[96:99]
ds_read_b64_tr_b16 a[196:197],v2 offset:8192
v_cvt_pk_bf16_f32 v134,v140,v141
v_mfma_f32_16x16x32_bf16 a[100:103],a[204:207],v[244:247],0
ds_read_b64_tr_b16 a[198:199],v2 offset:12288
v_cvt_pk_bf16_f32 v135,v142,v143
v_mfma_f32_16x16x32_bf16 a[100:103],a[200:203],v[228:231],a[100:103]
ds_read_b64_tr_b16 a[204:205],v3 offset:8192
v_cvt_pk_bf16_f32 v148,v148,v149
v_mfma_f32_16x16x32_bf16 a[104:107],a[208:211],v[228:231],0
buffer_load_dwordx4 v4,s[24:27],s47 offen offset:2048 nt lds
v_mfma_f32_16x16x32_bf16 a[104:107],a[212:215],v[244:247],a[104:107]
ds_read_b64_tr_b16 a[206:207],v3 offset:12288
ds_read_b64_tr_b16 a[200:201],v3
v_mfma_f32_16x16x32_bf16 a[108:111],a[220:223],v[244:247],0
ds_read_b64_tr_b16 a[202:203],v3 offset:4096
v_add_u32 v1,s48,v1
v_mfma_f32_16x16x32_bf16 a[108:111],a[216:219],v[228:231],a[108:111]
s_cmp_eq_u32 s49,163840
s_cselect_b32 s49,0,s49
ds_read_b64_tr_b16 a[208:209],v2 offset:256
v_mfma_f32_16x16x32_bf16 a[112:115],a[224:227],v[228:231],0
ds_read_b64_tr_b16 a[210:211],v2 offset:4352
v_mfma_f32_16x16x32_bf16 a[112:115],a[228:231],v[244:247],a[112:115]
ds_read_b64_tr_b16 a[212:213],v2 offset:8448
ds_read_b64_tr_b16 a[214:215],v2 offset:12544
v_mfma_f32_16x16x32_bf16 a[116:119],a[236:239],v[244:247],0
v_exp_f32 v151,v151
v_mfma_f32_16x16x32_bf16 a[116:119],a[232:235],v[228:231],a[116:119]
v_exp_f32 v156,v156
v_mfma_f32_16x16x32_bf16 a[120:123],a[240:243],v[228:231],0
v_exp_f32 v157,v157
v_mfma_f32_16x16x32_bf16 a[120:123],a[244:247],v[244:247],a[120:123]
v_exp_f32 v158,v158
v_mfma_f32_16x16x32_bf16 a[124:127],a[252:255],v[244:247],0
v_exp_f32 v159,v159
v_mfma_f32_16x16x32_bf16 a[124:127],a[248:251],v[228:231],a[124:127]
ds_read_b64_tr_b16 a[220:221],v3 offset:8448
ds_read_b64_tr_b16 a[222:223],v3 offset:12544
v_mfma_f32_16x16x32_bf16 v[60:63],v[28:31],v[228:231],0
v_cvt_pk_bf16_f32 v149,v150,v151
v_cvt_pk_bf16_f32 v150,v156,v157
v_mfma_f32_16x16x32_bf16 v[60:63],v[28:31],v[244:247],v[60:63]
ds_read_b64_tr_b16 a[216:217],v3 offset:256
v_cvt_pk_bf16_f32 v151,v158,v159
v_mfma_f32_16x16x32_bf16 v[196:199],a[128:131],v[96:99],v[40:43]
buffer_load_dwordx4 v5,s[24:27],s47 offen offset:3072 nt lds
v_mfma_f32_16x16x32_bf16 v[196:199],a[132:135],v[100:103],v[196:199]
s_add_u32 s47,s47,16384
ds_read_b64_tr_b16 a[218:219],v3 offset:4352
ds_read_b64_tr_b16 a[224:225],v2 offset:512
v_mfma_f32_16x16x32_bf16 v[196:199],a[136:139],v[104:107],v[196:199]
s_and_b32 s47,s47,0x3ffff
s_or_b32 s47,s47,s46
ds_read_b64_tr_b16 a[226:227],v2 offset:4608
v_mfma_f32_16x16x32_bf16 v[196:199],a[140:143],v[108:111],v[196:199]
ds_read_b64_tr_b16 a[228:229],v2 offset:8704
v_mfma_f32_16x16x32_bf16 v[204:207],a[156:159],v[108:111],v[40:43]
ds_read_b64_tr_b16 a[230:231],v2 offset:12800
v_mfma_f32_16x16x32_bf16 v[204:207],a[152:155],v[104:107],v[204:207]
ds_read_b64_tr_b16 a[236:237],v3 offset:8704
ds_read_b64_tr_b16 a[238:239],v3 offset:12800
v_mfma_f32_16x16x32_bf16 v[204:207],a[148:151],v[100:103],v[204:207]
ds_read_b64_tr_b16 a[232:233],v3 offset:512
v_mfma_f32_16x16x32_bf16 v[204:207],a[144:147],v[96:99],v[204:207]
ds_read_b64_tr_b16 a[234:235],v3 offset:4608
v_mfma_f32_16x16x32_bf16 v[212:215],a[160:163],v[96:99],v[40:43]
ds_read_b64_tr_b16 a[240:241],v2 offset:768
v_mfma_f32_16x16x32_bf16 v[212:215],a[164:167],v[100:103],v[212:215]
ds_read_b64_tr_b16 a[242:243],v2 offset:4864
ds_read_b64_tr_b16 a[244:245],v2 offset:8960
v_mfma_f32_16x16x32_bf16 v[212:215],a[168:171],v[104:107],v[212:215]
ds_read_b64_tr_b16 a[246:247],v2 offset:13056
v_mfma_f32_16x16x32_bf16 v[212:215],a[172:175],v[108:111],v[212:215]
ds_read_b64_tr_b16 a[252:253],v3 offset:8960
v_mfma_f32_16x16x32_bf16 v[220:223],a[188:191],v[108:111],v[40:43]
ds_read_b64_tr_b16 a[254:255],v3 offset:13056
v_mfma_f32_16x16x32_bf16 v[220:223],a[184:187],v[104:107],v[220:223]
ds_read_b64_tr_b16 a[248:249],v3 offset:768
ds_read_b64_tr_b16 a[250:251],v3 offset:4864
v_mfma_f32_16x16x32_bf16 v[220:223],a[180:183],v[100:103],v[220:223]
v_exp_f32 v164,v164
v_mfma_f32_16x16x32_bf16 v[220:223],a[176:179],v[96:99],v[220:223]
s_waitcnt lgkmcnt(8)
v_exp_f32 v165,v165
v_mfma_f32_16x16x32_bf16 a[0:3],a[192:195],v[132:135],a[0:3]
v_exp_f32 v180,v180
v_mfma_f32_16x16x32_bf16 a[0:3],a[196:199],v[148:151],a[0:3]
v_exp_f32 v181,v181
v_mfma_f32_16x16x32_bf16 a[4:7],a[204:207],v[148:151],a[4:7]
v_exp_f32 v166,v166
v_mfma_f32_16x16x32_bf16 a[4:7],a[200:203],v[132:135],a[4:7]
v_exp_f32 v167,v167
v_mfma_f32_16x16x32_bf16 a[8:11],a[208:211],v[132:135],a[8:11]
s_memrealtime s[76:77]
v_exp_f32 v182,v182
v_mfma_f32_16x16x32_bf16 a[8:11],a[212:215],v[148:151],a[8:11]
v_exp_f32 v183,v183
v_mfma_f32_16x16x32_bf16 a[12:15],a[220:223],v[148:151],a[12:15]
v_cvt_pk_bf16_f32 v164,v164,v165
v_cvt_pk_bf16_f32 v180,v180,v181
v_mfma_f32_16x16x32_bf16 a[12:15],a[216:219],v[132:135],a[12:15]
v_exp_f32 v172,v172
v_mfma_f32_16x16x32_bf16 a[16:19],a[224:227],v[132:135],a[16:19]
v_exp_f32 v173,v173
v_mfma_f32_16x16x32_bf16 a[16:19],a[228:231],v[148:151],a[16:19]
v_exp_f32 v174,v174
v_mfma_f32_16x16x32_bf16 a[20:23],a[236:239],v[148:151],a[20:23]
v_exp_f32 v175,v175
v_mfma_f32_16x16x32_bf16 a[20:23],a[232:235],v[132:135],a[20:23]
s_waitcnt lgkmcnt(0)
v_exp_f32 v188,v188
v_mfma_f32_16x16x32_bf16 a[24:27],a[240:243],v[132:135],a[24:27]
s_sub_u32 s79,s78,s76
v_exp_f32 v189,v189
v_mfma_f32_16x16x32_bf16 a[24:27],a[244:247],v[148:151],a[24:27]
s_add_u32 s78,s76,s60
v_exp_f32 v190,v190
v_mfma_f32_16x16x32_bf16 a[28:31],a[252:255],v[148:151],a[28:31]
s_cmp_gt_i32 s79,0
s_cbranch_scc1 L_ps1_%=
L_pr1_%=:
v_cvt_pk_bf16_f32 v165,v166,v167
v_mfma_f32_16x16x32_bf16 a[28:31],a[248:251],v[132:135],a[28:31]
v_exp_f32 v191,v191
v_mfma_f32_16x16x32_bf16 v[48:51],v[28:31],v[132:135],v[48:51]
v_cvt_pk_bf16_f32 v181,v182,v183
v_cvt_pk_bf16_f32 v166,v172,v173
v_cvt_pk_bf16_f32 v167,v174,v175
v_cvt_pk_bf16_f32 v182,v188,v189
v_cvt_pk_bf16_f32 v183,v190,v191
v_mfma_f32_16x16x32_bf16 v[48:51],v[28:31],v[148:151],v[48:51]
s_mov_b32 s53,13
L_att0_%=:
v_mfma_f32_16x16x32_bf16 v[228:231],a[128:131],v[112:115],v[44:47]
s_waitcnt vmcnt(8) lgkmcnt(0)
v_exp_f32 v196,v196
v_mfma_f32_16x16x32_bf16 v[228:231],a[132:135],v[116:119],v[228:231]
s_barrier
s_add_u32 m0,s49,s51
ds_read_b128 a[128:131],v1
v_mfma_f32_16x16x32_bf16 v[228:231],a[136:139],v[120:123],v[228:231]
s_mov_b32 s90,L_endseg_%=-L_pc_%=+128
v_exp_f32 v197,v197
v_mfma_f32_16x16x32_bf16 v[228:231],a[140:143],v[124:127],v[228:231]
s_add_u32 s54,s54,16384
ds_read_b128 a[132:135],v1 offset:256
v_cvt_pk_bf16_f32 v196,v196,v197
v_mfma_f32_16x16x32_bf16 v[236:239],a[156:159],v[124:127],v[44:47]
s_sub_u32 s53,s53,1
buffer_load_dwordx4 v4,s[20:23],s47 offen nt lds
v_mfma_f32_16x16x32_bf16 v[236:239],a[152:155],v[120:123],v[236:239]
s_and_b32 s46,s54,0xfffc0000
ds_read_b128 a[136:139],v1 offset:512
v_add_u32 v2,s48,v2
v_mfma_f32_16x16x32_bf16 v[236:239],a[148:151],v[116:119],v[236:239]
v_exp_f32 v198,v198
v_mfma_f32_16x16x32_bf16 v[236:239],a[144:147],v[112:115],v[236:239]
ds_read_b128 a[140:143],v1 offset:768
v_add_u32 v3,s48,v3
v_mfma_f32_16x16x32_bf16 v[244:247],a[160:163],v[112:115],v[44:47]
v_exp_f32 v199,v199
v_mfma_f32_16x16x32_bf16 v[244:247],a[164:167],v[116:119],v[244:247]
ds_read_b128 a[156:159],v1 offset:4864
v_cvt_pk_bf16_f32 v197,v198,v199
v_mfma_f32_16x16x32_bf16 v[244:247],a[168:171],v[120:123],v[244:247]
v_exp_f32 v204,v204
v_mfma_f32_16x16x32_bf16 v[244:247],a[172:175],v[124:127],v[244:247]
s_cmp_eq_u32 s50,131072
s_cselect_b32 s62,163840,0
ds_read_b128 a[152:155],v1 offset:4608
v_mfma_f32_16x16x32_bf16 v[252:255],a[188:191],v[124:127],v[44:47]
s_sub_u32 s48,32768,s62
v_exp_f32 v205,v205
v_mfma_f32_16x16x32_bf16 v[252:255],a[184:187],v[120:123],v[252:255]
s_add_u32 s50,s50,s48
ds_read_b128 a[148:151],v1 offset:4352
v_cvt_pk_bf16_f32 v198,v204,v205
v_mfma_f32_16x16x32_bf16 v[252:255],a[180:183],v[116:119],v[252:255]
v_exp_f32 v206,v206
v_mfma_f32_16x16x32_bf16 v[252:255],a[176:179],v[112:115],v[252:255]
ds_read_b128 a[144:147],v1 offset:4096
v_mfma_f32_16x16x32_bf16 a[32:35],a[192:195],v[164:167],a[32:35]
v_exp_f32 v207,v207
v_mfma_f32_16x16x32_bf16 a[32:35],a[196:199],v[180:183],a[32:35]
ds_read_b128 a[160:163],v1 offset:8192
v_cvt_pk_bf16_f32 v199,v206,v207
v_mfma_f32_16x16x32_bf16 a[36:39],a[204:207],v[180:183],a[36:39]
buffer_load_dwordx4 v4,s[20:23],s47 offen offset:1024 nt lds
v_mfma_f32_16x16x32_bf16 a[36:39],a[200:203],v[164:167],a[36:39]
ds_read_b128 a[164:167],v1 offset:8448
v_mfma_f32_16x16x32_bf16 a[40:43],a[208:211],v[164:167],a[40:43]
v_exp_f32 v212,v212
v_mfma_f32_16x16x32_bf16 a[40:43],a[212:215],v[180:183],a[40:43]
ds_read_b128 a[168:171],v1 offset:8704
v_mfma_f32_16x16x32_bf16 a[44:47],a[220:223],v[180:183],a[44:47]
v_exp_f32 v213,v213
v_mfma_f32_16x16x32_bf16 a[44:47],a[216:219],v[164:167],a[44:47]
ds_read_b128 a[172:175],v1 offset:8960
v_cvt_pk_bf16_f32 v212,v212,v213
v_mfma_f32_16x16x32_bf16 a[48:51],a[224:227],v[164:167],a[48:51]
v_exp_f32 v214,v214
v_mfma_f32_16x16x32_bf16 a[48:51],a[228:231],v[180:183],a[48:51]
ds_read_b128 a[188:191],v1 offset:13056
v_mfma_f32_16x16x32_bf16 a[52:55],a[236:239],v[180:183],a[52:55]
v_exp_f32 v215,v215
v_mfma_f32_16x16x32_bf16 a[52:55],a[232:235],v[164:167],a[52:55]
ds_read_b128 a[184:187],v1 offset:12800
v_cvt_pk_bf16_f32 v213,v214,v215
v_mfma_f32_16x16x32_bf16 a[56:59],a[240:243],v[164:167],a[56:59]
v_exp_f32 v220,v220
v_mfma_f32_16x16x32_bf16 a[56:59],a[244:247],v[180:183],a[56:59]
ds_read_b128 a[180:183],v1 offset:12544
v_mfma_f32_16x16x32_bf16 a[60:63],a[252:255],v[180:183],a[60:63]
v_exp_f32 v221,v221
v_mfma_f32_16x16x32_bf16 a[60:63],a[248:251],v[164:167],a[60:63]
ds_read_b128 a[176:179],v1 offset:12288
v_cvt_pk_bf16_f32 v214,v220,v221
v_mfma_f32_16x16x32_bf16 v[52:55],v[28:31],v[164:167],v[52:55]
buffer_load_dwordx4 v5,s[20:23],s47 offen offset:2048 nt lds
v_mfma_f32_16x16x32_bf16 v[52:55],v[28:31],v[180:183],v[52:55]
s_waitcnt lgkmcnt(3)
v_exp_f32 v222,v222
v_mfma_f32_16x16x32_bf16 v[132:135],a[128:131],v[64:67],v[32:35]
v_exp_f32 v223,v223
v_mfma_f32_16x16x32_bf16 v[132:135],a[132:135],v[68:71],v[132:135]
v_cvt_pk_bf16_f32 v215,v222,v223
v_add_u32 v1,s48,v1
v_mfma_f32_16x16x32_bf16 v[132:135],a[136:139],v[72:75],v[132:135]
v_exp_f32 v228,v228
v_mfma_f32_16x16x32_bf16 v[132:135],a[140:143],v[76:79],v[132:135]
v_exp_f32 v229,v229
v_mfma_f32_16x16x32_bf16 v[140:143],a[156:159],v[76:79],v[32:35]
v_exp_f32 v230,v230
v_mfma_f32_16x16x32_bf16 v[140:143],a[152:155],v[72:75],v[140:143]
v_exp_f32 v231,v231
v_mfma_f32_16x16x32_bf16 v[140:143],a[148:151],v[68:71],v[140:143]
v_cvt_pk_bf16_f32 v228,v228,v229
v_cvt_pk_bf16_f32 v229,v230,v231
v_mfma_f32_16x16x32_bf16 v[140:143],a[144:147],v[64:67],v[140:143]
v_exp_f32 v236,v236
v_mfma_f32_16x16x32_bf16 v[148:151],a[160:163],v[64:67],v[32:35]
v_exp_f32 v237,v237
v_mfma_f32_16x16x32_bf16 v[148:151],a[164:167],v[68:71],v[148:151]
v_exp_f32 v238,v238
v_mfma_f32_16x16x32_bf16 v[148:151],a[168:171],v[72:75],v[148:151]
v_exp_f32 v239,v239
v_mfma_f32_16x16x32_bf16 v[148:151],a[172:175],v[76:79],v[148:151]
v_exp_f32 v244,v244
v_mfma_f32_16x16x32_bf16 v[156:159],a[188:191],v[76:79],v[32:35]
s_waitcnt lgkmcnt(0)
buffer_load_dwordx4 v5,s[20:23],s47 offen offset:3072 nt lds
v_mfma_f32_16x16x32_bf16 v[156:159],a[184:187],v[72:75],v[156:159]
v_exp_f32 v245,v245
v_mfma_f32_16x16x32_bf16 v[156:159],a[180:183],v[68:71],v[156:159]
v_exp_f32 v246,v246
v_mfma_f32_16x16x32_bf16 v[156:159],a[176:179],v[64:67],v[156:159]
)A"
R"A(v_exp_f32 v247,v247
v_mfma_f32_16x16x32_bf16 a[64:67],a[192:195],v[196:199],a[64:67]
v_cvt_pk_bf16_f32 v230,v236,v237
v_cvt_pk_bf16_f32 v231,v238,v239
v_mfma_f32_16x16x32_bf16 a[64:67],a[196:199],v[212:215],a[64:67]
v_cvt_pk_bf16_f32 v244,v244,v245
v_cvt_pk_bf16_f32 v245,v246,v247
v_mfma_f32_16x16x32_bf16 a[68:71],a[204:207],v[212:215],a[68:71]
v_exp_f32 v252,v252
v_mfma_f32_16x16x32_bf16 a[68:71],a[200:203],v[196:199],a[68:71]
v_exp_f32 v253,v253
v_mfma_f32_16x16x32_bf16 a[72:75],a[208:211],v[196:199],a[72:75]
v_exp_f32 v254,v254
v_mfma_f32_16x16x32_bf16 a[72:75],a[212:215],v[212:215],a[72:75]
v_exp_f32 v255,v255
v_mfma_f32_16x16x32_bf16 a[76:79],a[220:223],v[212:215],a[76:79]
v_cvt_pk_bf16_f32 v246,v252,v253
v_cvt_pk_bf16_f32 v247,v254,v255
v_mfma_f32_16x16x32_bf16 a[76:79],a[216:219],v[196:199],a[76:79]
s_add_u32 m0,s49,s52
v_exp_f32 v132,v132
v_mfma_f32_16x16x32_bf16 a[80:83],a[224:227],v[196:199],a[80:83]
s_add_u32 s49,s49,32768
v_exp_f32 v133,v133
v_mfma_f32_16x16x32_bf16 a[80:83],a[228:231],v[212:215],a[80:83]
v_exp_f32 v134,v134
v_mfma_f32_16x16x32_bf16 a[84:87],a[236:239],v[212:215],a[84:87]
buffer_load_dwordx4 v4,s[24:27],s47 offen nt lds
v_mfma_f32_16x16x32_bf16 a[84:87],a[232:235],v[196:199],a[84:87]
v_exp_f32 v135,v135
v_mfma_f32_16x16x32_bf16 a[88:91],a[240:243],v[196:199],a[88:91]
v_cvt_pk_bf16_f32 v132,v132,v133
v_cvt_pk_bf16_f32 v133,v134,v135
v_mfma_f32_16x16x32_bf16 a[88:91],a[244:247],v[212:215],a[88:91]
v_exp_f32 v140,v140
v_mfma_f32_16x16x32_bf16 a[92:95],a[252:255],v[212:215],a[92:95]
v_exp_f32 v141,v141
v_mfma_f32_16x16x32_bf16 a[92:95],a[248:251],v[196:199],a[92:95]
v_exp_f32 v142,v142
v_mfma_f32_16x16x32_bf16 v[56:59],v[28:31],v[196:199],v[56:59]
v_exp_f32 v143,v143
v_mfma_f32_16x16x32_bf16 v[56:59],v[28:31],v[212:215],v[56:59]
v_exp_f32 v148,v148
v_mfma_f32_16x16x32_bf16 v[164:167],a[128:131],v[80:83],v[36:39]
v_exp_f32 v149,v149
v_mfma_f32_16x16x32_bf16 v[164:167],a[132:135],v[84:87],v[164:167]
v_exp_f32 v150,v150
v_mfma_f32_16x16x32_bf16 v[164:167],a[136:139],v[88:91],v[164:167]
v_exp_f32 v151,v151
v_mfma_f32_16x16x32_bf16 v[164:167],a[140:143],v[92:95],v[164:167]
v_cvt_pk_bf16_f32 v134,v140,v141
v_cvt_pk_bf16_f32 v135,v142,v143
v_mfma_f32_16x16x32_bf16 v[172:175],a[156:159],v[92:95],v[36:39]
v_cvt_pk_bf16_f32 v148,v148,v149
v_cvt_pk_bf16_f32 v149,v150,v151
v_mfma_f32_16x16x32_bf16 v[172:175],a[152:155],v[88:91],v[172:175]
v_exp_f32 v156,v156
v_mfma_f32_16x16x32_bf16 v[172:175],a[148:151],v[84:87],v[172:175]
buffer_load_dwordx4 v5,s[24:27],s47 offen offset:1024 nt lds
v_mfma_f32_16x16x32_bf16 v[172:175],a[144:147],v[80:83],v[172:175]
v_exp_f32 v157,v157
v_mfma_f32_16x16x32_bf16 v[180:183],a[160:163],v[80:83],v[36:39]
v_exp_f32 v158,v158
v_mfma_f32_16x16x32_bf16 v[180:183],a[164:167],v[84:87],v[180:183]
v_exp_f32 v159,v159
v_mfma_f32_16x16x32_bf16 v[180:183],a[168:171],v[88:91],v[180:183]
v_cvt_pk_bf16_f32 v150,v156,v157
v_cvt_pk_bf16_f32 v151,v158,v159
v_mfma_f32_16x16x32_bf16 v[180:183],a[172:175],v[92:95],v[180:183]
v_exp_f32 v164,v164
v_mfma_f32_16x16x32_bf16 v[188:191],a[188:191],v[92:95],v[36:39]
v_exp_f32 v165,v165
v_mfma_f32_16x16x32_bf16 v[188:191],a[184:187],v[88:91],v[188:191]
v_exp_f32 v166,v166
v_mfma_f32_16x16x32_bf16 v[188:191],a[180:183],v[84:87],v[188:191]
v_exp_f32 v167,v167
v_mfma_f32_16x16x32_bf16 v[188:191],a[176:179],v[80:83],v[188:191]
v_cvt_pk_bf16_f32 v164,v164,v165
v_cvt_pk_bf16_f32 v165,v166,v167
v_mfma_f32_16x16x32_bf16 a[96:99],a[192:195],v[228:231],a[96:99]
s_cmp_eq_u32 s49,163840
s_cselect_b32 s49,0,s49
ds_read_b64_tr_b16 a[192:193],v2
v_mfma_f32_16x16x32_bf16 a[96:99],a[196:199],v[244:247],a[96:99]
ds_read_b64_tr_b16 a[194:195],v2 offset:4096
v_mfma_f32_16x16x32_bf16 a[100:103],a[204:207],v[244:247],a[100:103]
ds_read_b64_tr_b16 a[196:197],v2 offset:8192
v_mfma_f32_16x16x32_bf16 a[100:103],a[200:203],v[228:231],a[100:103]
ds_read_b64_tr_b16 a[198:199],v2 offset:12288
v_mfma_f32_16x16x32_bf16 a[104:107],a[208:211],v[228:231],a[104:107]
buffer_load_dwordx4 v4,s[24:27],s47 offen offset:2048 nt lds
v_mfma_f32_16x16x32_bf16 a[104:107],a[212:215],v[244:247],a[104:107]
ds_read_b64_tr_b16 a[204:205],v3 offset:8192
v_mfma_f32_16x16x32_bf16 a[108:111],a[220:223],v[244:247],a[108:111]
ds_read_b64_tr_b16 a[206:207],v3 offset:12288
v_mfma_f32_16x16x32_bf16 a[108:111],a[216:219],v[228:231],a[108:111]
ds_read_b64_tr_b16 a[200:201],v3
v_mfma_f32_16x16x32_bf16 a[112:115],a[224:227],v[228:231],a[112:115]
ds_read_b64_tr_b16 a[202:203],v3 offset:4096
v_mfma_f32_16x16x32_bf16 a[112:115],a[228:231],v[244:247],a[112:115]
ds_read_b64_tr_b16 a[208:209],v2 offset:256
v_mfma_f32_16x16x32_bf16 a[116:119],a[236:239],v[244:247],a[116:119]
ds_read_b64_tr_b16 a[210:211],v2 offset:4352
v_mfma_f32_16x16x32_bf16 a[116:119],a[232:235],v[228:231],a[116:119]
ds_read_b64_tr_b16 a[212:213],v2 offset:8448
v_mfma_f32_16x16x32_bf16 a[120:123],a[240:243],v[228:231],a[120:123]
ds_read_b64_tr_b16 a[214:215],v2 offset:12544
v_mfma_f32_16x16x32_bf16 a[120:123],a[244:247],v[244:247],a[120:123]
ds_read_b64_tr_b16 a[220:221],v3 offset:8448
v_mfma_f32_16x16x32_bf16 a[124:127],a[252:255],v[244:247],a[124:127]
ds_read_b64_tr_b16 a[222:223],v3 offset:12544
v_mfma_f32_16x16x32_bf16 a[124:127],a[248:251],v[228:231],a[124:127]
ds_read_b64_tr_b16 a[216:217],v3 offset:256
v_mfma_f32_16x16x32_bf16 v[60:63],v[28:31],v[228:231],v[60:63]
ds_read_b64_tr_b16 a[218:219],v3 offset:4352
v_mfma_f32_16x16x32_bf16 v[60:63],v[28:31],v[244:247],v[60:63]
ds_read_b64_tr_b16 a[224:225],v2 offset:512
v_mfma_f32_16x16x32_bf16 v[196:199],a[128:131],v[96:99],v[40:43]
buffer_load_dwordx4 v5,s[24:27],s47 offen offset:3072 nt lds
v_mfma_f32_16x16x32_bf16 v[196:199],a[132:135],v[100:103],v[196:199]
s_add_u32 s47,s47,16384
s_and_b32 s47,s47,0x3ffff
ds_read_b64_tr_b16 a[226:227],v2 offset:4608
v_mfma_f32_16x16x32_bf16 v[196:199],a[136:139],v[104:107],v[196:199]
s_or_b32 s47,s47,s46
ds_read_b64_tr_b16 a[228:229],v2 offset:8704
v_mfma_f32_16x16x32_bf16 v[196:199],a[140:143],v[108:111],v[196:199]
ds_read_b64_tr_b16 a[230:231],v2 offset:12800
v_mfma_f32_16x16x32_bf16 v[204:207],a[156:159],v[108:111],v[40:43]
ds_read_b64_tr_b16 a[236:237],v3 offset:8704
v_mfma_f32_16x16x32_bf16 v[204:207],a[152:155],v[104:107],v[204:207]
ds_read_b64_tr_b16 a[238:239],v3 offset:12800
v_mfma_f32_16x16x32_bf16 v[204:207],a[148:151],v[100:103],v[204:207]
ds_read_b64_tr_b16 a[232:233],v3 offset:512
v_mfma_f32_16x16x32_bf16 v[204:207],a[144:147],v[96:99],v[204:207]
ds_read_b64_tr_b16 a[234:235],v3 offset:4608
v_mfma_f32_16x16x32_bf16 v[212:215],a[160:163],v[96:99],v[40:43]
ds_read_b64_tr_b16 a[240:241],v2 offset:768
v_mfma_f32_16x16x32_bf16 v[212:215],a[164:167],v[100:103],v[212:215]
ds_read_b64_tr_b16 a[242:243],v2 offset:4864
v_mfma_f32_16x16x32_bf16 v[212:215],a[168:171],v[104:107],v[212:215]
ds_read_b64_tr_b16 a[244:245],v2 offset:8960
v_mfma_f32_16x16x32_bf16 v[212:215],a[172:175],v[108:111],v[212:215]
ds_read_b64_tr_b16 a[246:247],v2 offset:13056
v_mfma_f32_16x16x32_bf16 v[220:223],a[188:191],v[108:111],v[40:43]
ds_read_b64_tr_b16 a[252:253],v3 offset:8960
v_mfma_f32_16x16x32_bf16 v[220:223],a[184:187],v[104:107],v[220:223]
ds_read_b64_tr_b16 a[254:255],v3 offset:13056
v_mfma_f32_16x16x32_bf16 v[220:223],a[180:183],v[100:103],v[220:223]
ds_read_b64_tr_b16 a[248:249],v3 offset:768
v_mfma_f32_16x16x32_bf16 v[220:223],a[176:179],v[96:99],v[220:223]
s_waitcnt lgkmcnt(7)
ds_read_b64_tr_b16 a[250:251],v3 offset:4864
v_mfma_f32_16x16x32_bf16 a[0:3],a[192:195],v[132:135],a[0:3]
buffer_load_dword v26,v24,s[88:91],s93 offen
v_mfma_f32_16x16x32_bf16 a[0:3],a[196:199],v[148:151],a[0:3]
v_exp_f32 v180,v180
v_mfma_f32_16x16x32_bf16 a[4:7],a[204:207],v[148:151],a[4:7]
v_exp_f32 v181,v181
v_mfma_f32_16x16x32_bf16 a[4:7],a[200:203],v[132:135],a[4:7]
v_exp_f32 v182,v182
v_mfma_f32_16x16x32_bf16 a[8:11],a[208:211],v[132:135],a[8:11]
s_memrealtime s[76:77]
v_exp_f32 v183,v183
v_mfma_f32_16x16x32_bf16 a[8:11],a[212:215],v[148:151],a[8:11]
v_exp_f32 v172,v172
v_mfma_f32_16x16x32_bf16 a[12:15],a[220:223],v[148:151],a[12:15]
v_exp_f32 v173,v173
v_mfma_f32_16x16x32_bf16 a[12:15],a[216:219],v[132:135],a[12:15]
v_exp_f32 v174,v174
v_mfma_f32_16x16x32_bf16 a[16:19],a[224:227],v[132:135],a[16:19]
v_exp_f32 v175,v175
v_mfma_f32_16x16x32_bf16 a[16:19],a[228:231],v[148:151],a[16:19]
v_cvt_pk_bf16_f32 v180,v180,v181
v_cvt_pk_bf16_f32 v181,v182,v183
v_mfma_f32_16x16x32_bf16 a[20:23],a[236:239],v[148:151],a[20:23]
v_exp_f32 v188,v188
v_mfma_f32_16x16x32_bf16 a[20:23],a[232:235],v[132:135],a[20:23]
s_waitcnt lgkmcnt(0)
v_exp_f32 v189,v189
v_mfma_f32_16x16x32_bf16 a[24:27],a[240:243],v[132:135],a[24:27]
s_sub_u32 s79,s78,s76
v_exp_f32 v190,v190
v_mfma_f32_16x16x32_bf16 a[24:27],a[244:247],v[148:151],a[24:27]
s_add_u32 s78,s76,s60
v_exp_f32 v191,v191
v_mfma_f32_16x16x32_bf16 a[28:31],a[252:255],v[148:151],a[28:31]
s_cmp_gt_i32 s79,0
s_cbranch_scc1 L_ps2_%=
L_pr2_%=:
v_cvt_pk_bf16_f32 v166,v172,v173
v_mfma_f32_16x16x32_bf16 a[28:31],a[248:251],v[132:135],a[28:31]
v_cvt_pk_bf16_f32 v167,v174,v175
v_cvt_pk_bf16_f32 v182,v188,v189
v_mfma_f32_16x16x32_bf16 v[48:51],v[28:31],v[132:135],v[48:51]
v_cvt_pk_bf16_f32 v183,v190,v191
s_cmp_lg_u32 s53,0
v_mfma_f32_16x16x32_bf16 v[48:51],v[28:31],v[148:151],v[48:51]
s_cbranch_scc1 L_att0_%=
v_mfma_f32_16x16x32_bf16 v[228:231],a[128:131],v[112:115],v[44:47]
s_waitcnt vmcnt(10)
s_add_u32 s54,s54,16384
v_exp_f32 v196,v196
v_mfma_f32_16x16x32_bf16 v[228:231],a[132:135],v[116:119],v[228:231]
s_barrier
s_add_u32 m0,s49,s51
ds_read_b128 a[128:131],v1
v_mfma_f32_16x16x32_bf16 v[228:231],a[136:139],v[120:123],v[228:231]
s_and_b32 s46,s54,0xfffc0000
ds_read_b128 a[132:135],v1 offset:256
v_and_b32 v18,48,v6
v_mfma_f32_16x16x32_bf16 v[228:231],a[140:143],v[124:127],v[228:231]
s_add_u32 s54,s54,16384
ds_read_b128 a[136:139],v1 offset:512
v_lshrrev_b32 v19,4,v6
v_mfma_f32_16x16x32_bf16 v[236:239],a[156:159],v[124:127],v[44:47]
s_add_u32 s84,s55,0
buffer_load_dwordx4 v4,s[20:23],s47 offen nt lds
v_mfma_f32_16x16x32_bf16 v[236:239],a[152:155],v[120:123],v[236:239]
s_add_u32 s85,s55,4096
ds_read_b128 a[140:143],v1 offset:768
v_lshlrev_b32 v18,2,v18
v_mfma_f32_16x16x32_bf16 v[236:239],a[148:151],v[116:119],v[236:239]
s_add_u32 s86,s55,8192
ds_read_b128 a[156:159],v1 offset:4864
v_and_b32 v19,48,v19
v_mfma_f32_16x16x32_bf16 v[236:239],a[144:147],v[112:115],v[236:239]
s_add_u32 s87,s55,12288
ds_read_b128 a[152:155],v1 offset:4608
v_add_u32 v18,v18,v19
v_mfma_f32_16x16x32_bf16 v[244:247],a[160:163],v[112:115],v[44:47]
s_max_i32 s84,s84,s18
ds_read_b128 a[148:151],v1 offset:4352
v_add_u32 v18,131072,v18
v_mfma_f32_16x16x32_bf16 v[244:247],a[164:167],v[116:119],v[244:247]
s_max_i32 s85,s85,s19
ds_read_b128 a[144:147],v1 offset:4096
v_add_u32 v2,s48,v2
v_mfma_f32_16x16x32_bf16 v[244:247],a[168:171],v[120:123],v[244:247]
s_max_i32 s86,s86,s100
ds_read_b128 a[160:163],v1 offset:8192
v_add_u32 v3,s48,v3
v_mfma_f32_16x16x32_bf16 v[244:247],a[172:175],v[124:127],v[244:247]
s_cmp_eq_u32 s50,131072
s_cselect_b32 s62,163840,0
ds_read_b128 a[164:167],v1 offset:8448
v_mfma_f32_16x16x32_bf16 v[252:255],a[188:191],v[124:127],v[44:47]
s_sub_u32 s48,32768,s62
s_add_u32 s50,s50,s48
ds_read_b128 a[168:171],v1 offset:8704
v_mfma_f32_16x16x32_bf16 v[252:255],a[184:187],v[120:123],v[252:255]
s_cmp_eq_u32 s50,131072
s_cselect_b32 s62,163840,0
ds_read_b128 a[172:175],v1 offset:8960
v_mfma_f32_16x16x32_bf16 v[252:255],a[180:183],v[116:119],v[252:255]
s_max_i32 s87,s87,s101
s_add_u32 s93,s92,L_endseg_%=-L_pc_%=
ds_read_b128 a[188:191],v1 offset:13056
v_mfma_f32_16x16x32_bf16 v[252:255],a[176:179],v[112:115],v[252:255]
s_mov_b32 s80,0
s_mov_b32 s83,s55
ds_read_b128 a[184:187],v1 offset:12800
v_mfma_f32_16x16x32_bf16 a[32:35],a[192:195],v[164:167],a[32:35]
ds_read_b128 a[180:183],v1 offset:12544
v_mfma_f32_16x16x32_bf16 a[32:35],a[196:199],v[180:183],a[32:35]
ds_read_b128 a[176:179],v1 offset:12288
v_add_u32 v1,s48,v1
v_mfma_f32_16x16x32_bf16 a[36:39],a[204:207],v[180:183],a[36:39]
buffer_load_dwordx4 v4,s[20:23],s47 offen offset:1024 nt lds
v_mfma_f32_16x16x32_bf16 a[36:39],a[200:203],v[164:167],a[36:39]
)A"
R"A(v_exp_f32 v197,v197
v_mfma_f32_16x16x32_bf16 a[40:43],a[208:211],v[164:167],a[40:43]
v_exp_f32 v198,v198
v_mfma_f32_16x16x32_bf16 a[40:43],a[212:215],v[180:183],a[40:43]
v_exp_f32 v199,v199
v_mfma_f32_16x16x32_bf16 a[44:47],a[220:223],v[180:183],a[44:47]
v_cvt_pk_bf16_f32 v196,v196,v197
v_cvt_pk_bf16_f32 v197,v198,v199
v_mfma_f32_16x16x32_bf16 a[44:47],a[216:219],v[164:167],a[44:47]
v_exp_f32 v204,v204
v_mfma_f32_16x16x32_bf16 a[48:51],a[224:227],v[164:167],a[48:51]
v_exp_f32 v205,v205
v_mfma_f32_16x16x32_bf16 a[48:51],a[228:231],v[180:183],a[48:51]
v_exp_f32 v206,v206
v_mfma_f32_16x16x32_bf16 a[52:55],a[236:239],v[180:183],a[52:55]
v_exp_f32 v207,v207
v_mfma_f32_16x16x32_bf16 a[52:55],a[232:235],v[164:167],a[52:55]
v_exp_f32 v212,v212
v_mfma_f32_16x16x32_bf16 a[56:59],a[240:243],v[164:167],a[56:59]
v_exp_f32 v213,v213
v_mfma_f32_16x16x32_bf16 a[56:59],a[244:247],v[180:183],a[56:59]
v_exp_f32 v214,v214
v_mfma_f32_16x16x32_bf16 a[60:63],a[252:255],v[180:183],a[60:63]
v_exp_f32 v215,v215
v_mfma_f32_16x16x32_bf16 a[60:63],a[248:251],v[164:167],a[60:63]
v_cvt_pk_bf16_f32 v198,v204,v205
v_cvt_pk_bf16_f32 v199,v206,v207
v_mfma_f32_16x16x32_bf16 v[52:55],v[28:31],v[164:167],v[52:55]
buffer_load_dwordx4 v5,s[20:23],s47 offen offset:2048 nt lds
v_mfma_f32_16x16x32_bf16 v[52:55],v[28:31],v[180:183],v[52:55]
s_waitcnt lgkmcnt(0)
v_cvt_pk_bf16_f32 v212,v212,v213
v_cvt_pk_bf16_f32 v213,v214,v215
v_mfma_f32_16x16x32_bf16 v[132:135],a[128:131],v[64:67],v[32:35]
v_exp_f32 v220,v220
v_mfma_f32_16x16x32_bf16 v[132:135],a[132:135],v[68:71],v[132:135]
v_exp_f32 v221,v221
v_mfma_f32_16x16x32_bf16 v[132:135],a[136:139],v[72:75],v[132:135]
v_exp_f32 v222,v222
v_mfma_f32_16x16x32_bf16 v[132:135],a[140:143],v[76:79],v[132:135]
v_exp_f32 v223,v223
v_mfma_f32_16x16x32_bf16 v[140:143],a[156:159],v[76:79],v[32:35]
v_cvt_pk_bf16_f32 v214,v220,v221
v_cvt_pk_bf16_f32 v215,v222,v223
v_mfma_f32_16x16x32_bf16 v[140:143],a[152:155],v[72:75],v[140:143]
v_exp_f32 v228,v228
v_mfma_f32_16x16x32_bf16 v[140:143],a[148:151],v[68:71],v[140:143]
v_exp_f32 v229,v229
v_mfma_f32_16x16x32_bf16 v[140:143],a[144:147],v[64:67],v[140:143]
v_exp_f32 v230,v230
v_mfma_f32_16x16x32_bf16 v[148:151],a[160:163],v[64:67],v[32:35]
v_exp_f32 v231,v231
v_mfma_f32_16x16x32_bf16 v[148:151],a[164:167],v[68:71],v[148:151]
v_cvt_pk_bf16_f32 v228,v228,v229
v_cvt_pk_bf16_f32 v229,v230,v231
v_mfma_f32_16x16x32_bf16 v[148:151],a[168:171],v[72:75],v[148:151]
v_exp_f32 v236,v236
v_mfma_f32_16x16x32_bf16 v[148:151],a[172:175],v[76:79],v[148:151]
v_exp_f32 v237,v237
v_mfma_f32_16x16x32_bf16 v[156:159],a[188:191],v[76:79],v[32:35]
buffer_load_dwordx4 v5,s[20:23],s47 offen offset:3072 nt lds
v_mfma_f32_16x16x32_bf16 v[156:159],a[184:187],v[72:75],v[156:159]
v_exp_f32 v238,v238
v_mfma_f32_16x16x32_bf16 v[156:159],a[180:183],v[68:71],v[156:159]
v_exp_f32 v239,v239
v_mfma_f32_16x16x32_bf16 v[156:159],a[176:179],v[64:67],v[156:159]
v_exp_f32 v244,v244
v_mfma_f32_16x16x32_bf16 a[64:67],a[192:195],v[196:199],a[64:67]
v_exp_f32 v245,v245
v_mfma_f32_16x16x32_bf16 a[64:67],a[196:199],v[212:215],a[64:67]
v_exp_f32 v246,v246
v_mfma_f32_16x16x32_bf16 a[68:71],a[204:207],v[212:215],a[68:71]
v_exp_f32 v247,v247
v_mfma_f32_16x16x32_bf16 a[68:71],a[200:203],v[196:199],a[68:71]
v_cvt_pk_bf16_f32 v230,v236,v237
v_cvt_pk_bf16_f32 v231,v238,v239
v_mfma_f32_16x16x32_bf16 a[72:75],a[208:211],v[196:199],a[72:75]
v_cvt_pk_bf16_f32 v244,v244,v245
v_cvt_pk_bf16_f32 v245,v246,v247
v_mfma_f32_16x16x32_bf16 a[72:75],a[212:215],v[212:215],a[72:75]
v_exp_f32 v252,v252
v_mfma_f32_16x16x32_bf16 a[76:79],a[220:223],v[212:215],a[76:79]
v_exp_f32 v253,v253
v_mfma_f32_16x16x32_bf16 a[76:79],a[216:219],v[196:199],a[76:79]
s_add_u32 m0,s49,s52
v_exp_f32 v254,v254
v_mfma_f32_16x16x32_bf16 a[80:83],a[224:227],v[196:199],a[80:83]
s_add_u32 s49,s49,32768
v_exp_f32 v255,v255
v_mfma_f32_16x16x32_bf16 a[80:83],a[228:231],v[212:215],a[80:83]
v_cvt_pk_bf16_f32 v246,v252,v253
v_cvt_pk_bf16_f32 v247,v254,v255
v_mfma_f32_16x16x32_bf16 a[84:87],a[236:239],v[212:215],a[84:87]
buffer_load_dwordx4 v4,s[24:27],s47 offen nt lds
v_mfma_f32_16x16x32_bf16 a[84:87],a[232:235],v[196:199],a[84:87]
v_exp_f32 v132,v132
v_mfma_f32_16x16x32_bf16 a[88:91],a[240:243],v[196:199],a[88:91]
v_exp_f32 v133,v133
v_mfma_f32_16x16x32_bf16 a[88:91],a[244:247],v[212:215],a[88:91]
v_exp_f32 v134,v134
v_mfma_f32_16x16x32_bf16 a[92:95],a[252:255],v[212:215],a[92:95]
v_exp_f32 v135,v135
v_mfma_f32_16x16x32_bf16 a[92:95],a[248:251],v[196:199],a[92:95]
v_cvt_pk_bf16_f32 v132,v132,v133
v_cvt_pk_bf16_f32 v133,v134,v135
v_mfma_f32_16x16x32_bf16 v[56:59],v[28:31],v[196:199],v[56:59]
v_exp_f32 v140,v140
v_mfma_f32_16x16x32_bf16 v[56:59],v[28:31],v[212:215],v[56:59]
v_exp_f32 v141,v141
v_mfma_f32_16x16x32_bf16 v[164:167],a[128:131],v[80:83],v[36:39]
v_exp_f32 v142,v142
v_mfma_f32_16x16x32_bf16 v[164:167],a[132:135],v[84:87],v[164:167]
v_exp_f32 v143,v143
v_mfma_f32_16x16x32_bf16 v[164:167],a[136:139],v[88:91],v[164:167]
v_exp_f32 v148,v148
v_mfma_f32_16x16x32_bf16 v[164:167],a[140:143],v[92:95],v[164:167]
v_exp_f32 v149,v149
v_mfma_f32_16x16x32_bf16 v[172:175],a[156:159],v[92:95],v[36:39]
v_exp_f32 v150,v150
v_mfma_f32_16x16x32_bf16 v[172:175],a[152:155],v[88:91],v[172:175]
v_exp_f32 v151,v151
v_mfma_f32_16x16x32_bf16 v[172:175],a[148:151],v[84:87],v[172:175]
buffer_load_dwordx4 v5,s[24:27],s47 offen offset:1024 nt lds
v_mfma_f32_16x16x32_bf16 v[172:175],a[144:147],v[80:83],v[172:175]
v_cvt_pk_bf16_f32 v134,v140,v141
v_cvt_pk_bf16_f32 v135,v142,v143
v_mfma_f32_16x16x32_bf16 v[180:183],a[160:163],v[80:83],v[36:39]
v_cvt_pk_bf16_f32 v148,v148,v149
v_cvt_pk_bf16_f32 v149,v150,v151
v_mfma_f32_16x16x32_bf16 v[180:183],a[164:167],v[84:87],v[180:183]
v_exp_f32 v156,v156
v_mfma_f32_16x16x32_bf16 v[180:183],a[168:171],v[88:91],v[180:183]
v_exp_f32 v157,v157
v_mfma_f32_16x16x32_bf16 v[180:183],a[172:175],v[92:95],v[180:183]
v_exp_f32 v158,v158
v_mfma_f32_16x16x32_bf16 v[188:191],a[188:191],v[92:95],v[36:39]
v_exp_f32 v159,v159
v_mfma_f32_16x16x32_bf16 v[188:191],a[184:187],v[88:91],v[188:191]
v_cvt_pk_bf16_f32 v150,v156,v157
v_cvt_pk_bf16_f32 v151,v158,v159
v_mfma_f32_16x16x32_bf16 v[188:191],a[180:183],v[84:87],v[188:191]
s_waitcnt vmcnt(14)
s_cmp_eq_u32 s49,163840
s_cselect_b32 s49,0,s49
v_mfma_f32_16x16x32_bf16 v[188:191],a[176:179],v[80:83],v[188:191]
s_barrier
ds_read_b128 v[80:83],v18
v_mfma_f32_16x16x32_bf16 a[96:99],a[192:195],v[228:231],a[96:99]
ds_read_b128 v[84:87],v18 offset:256
v_mfma_f32_16x16x32_bf16 a[96:99],a[196:199],v[244:247],a[96:99]
ds_read_b128 v[88:91],v18 offset:512
v_mfma_f32_16x16x32_bf16 a[100:103],a[204:207],v[244:247],a[100:103]
ds_read_b128 v[92:95],v18 offset:768
v_mfma_f32_16x16x32_bf16 a[100:103],a[200:203],v[228:231],a[100:103]
buffer_load_dwordx4 v4,s[24:27],s47 offen offset:2048 nt lds
v_mfma_f32_16x16x32_bf16 a[104:107],a[208:211],v[228:231],a[104:107]
ds_read_b64_tr_b16 a[192:193],v2
ds_read_b64_tr_b16 a[194:195],v2 offset:4096
v_mfma_f32_16x16x32_bf16 a[104:107],a[212:215],v[244:247],a[104:107]
ds_read_b64_tr_b16 a[196:197],v2 offset:8192
ds_read_b64_tr_b16 a[198:199],v2 offset:12288
v_mfma_f32_16x16x32_bf16 a[108:111],a[220:223],v[244:247],a[108:111]
ds_read_b64_tr_b16 a[204:205],v3 offset:8192
ds_read_b64_tr_b16 a[206:207],v3 offset:12288
v_mfma_f32_16x16x32_bf16 a[108:111],a[216:219],v[228:231],a[108:111]
ds_read_b64_tr_b16 a[200:201],v3
ds_read_b64_tr_b16 a[202:203],v3 offset:4096
v_mfma_f32_16x16x32_bf16 a[112:115],a[224:227],v[228:231],a[112:115]
ds_read_b64_tr_b16 a[208:209],v2 offset:256
ds_read_b64_tr_b16 a[210:211],v2 offset:4352
v_mfma_f32_16x16x32_bf16 a[112:115],a[228:231],v[244:247],a[112:115]
ds_read_b64_tr_b16 a[212:213],v2 offset:8448
ds_read_b64_tr_b16 a[214:215],v2 offset:12544
v_mfma_f32_16x16x32_bf16 a[116:119],a[236:239],v[244:247],a[116:119]
ds_read_b64_tr_b16 a[220:221],v3 offset:8448
ds_read_b64_tr_b16 a[222:223],v3 offset:12544
v_mfma_f32_16x16x32_bf16 a[116:119],a[232:235],v[228:231],a[116:119]
ds_read_b64_tr_b16 a[216:217],v3 offset:256
ds_read_b64_tr_b16 a[218:219],v3 offset:4352
v_mfma_f32_16x16x32_bf16 a[120:123],a[240:243],v[228:231],a[120:123]
ds_read_b64_tr_b16 a[224:225],v2 offset:512
ds_read_b64_tr_b16 a[226:227],v2 offset:4608
v_mfma_f32_16x16x32_bf16 a[120:123],a[244:247],v[244:247],a[120:123]
ds_read_b64_tr_b16 a[228:229],v2 offset:8704
ds_read_b64_tr_b16 a[230:231],v2 offset:12800
v_mfma_f32_16x16x32_bf16 a[124:127],a[252:255],v[244:247],a[124:127]
ds_read_b64_tr_b16 a[236:237],v3 offset:8704
ds_read_b64_tr_b16 a[238:239],v3 offset:12800
v_mfma_f32_16x16x32_bf16 a[124:127],a[248:251],v[228:231],a[124:127]
ds_read_b64_tr_b16 a[232:233],v3 offset:512
ds_read_b64_tr_b16 a[234:235],v3 offset:4608
v_mfma_f32_16x16x32_bf16 v[60:63],v[28:31],v[228:231],v[60:63]
ds_read_b64_tr_b16 a[240:241],v2 offset:768
ds_read_b64_tr_b16 a[242:243],v2 offset:4864
v_mfma_f32_16x16x32_bf16 v[60:63],v[28:31],v[244:247],v[60:63]
buffer_load_dwordx4 v5,s[24:27],s47 offen offset:3072 nt lds
v_mfma_f32_16x16x32_bf16 v[196:199],a[128:131],v[96:99],v[40:43]
s_add_u32 s47,s47,16384
ds_read_b64_tr_b16 a[244:245],v2 offset:8960
ds_read_b64_tr_b16 a[246:247],v2 offset:13056
v_mfma_f32_16x16x32_bf16 v[196:199],a[132:135],v[100:103],v[196:199]
s_and_b32 s47,s47,0x3ffff
ds_read_b64_tr_b16 a[252:253],v3 offset:8960
ds_read_b64_tr_b16 a[254:255],v3 offset:13056
v_mfma_f32_16x16x32_bf16 v[196:199],a[136:139],v[104:107],v[196:199]
s_or_b32 s47,s47,s46
ds_read_b64_tr_b16 a[248:249],v3 offset:768
ds_read_b64_tr_b16 a[250:251],v3 offset:4864
v_mfma_f32_16x16x32_bf16 v[196:199],a[140:143],v[108:111],v[196:199]
s_and_b32 s46,s54,0xfffc0000
v_exp_f32 v164,v164
v_mfma_f32_16x16x32_bf16 v[204:207],a[156:159],v[108:111],v[40:43]
s_add_u32 s54,s54,16384
v_exp_f32 v165,v165
v_mfma_f32_16x16x32_bf16 v[204:207],a[152:155],v[104:107],v[204:207]
v_exp_f32 v166,v166
v_mfma_f32_16x16x32_bf16 v[204:207],a[148:151],v[100:103],v[204:207]
v_exp_f32 v167,v167
v_mfma_f32_16x16x32_bf16 v[204:207],a[144:147],v[96:99],v[204:207]
v_cvt_pk_bf16_f32 v164,v164,v165
v_cvt_pk_bf16_f32 v165,v166,v167
v_mfma_f32_16x16x32_bf16 v[212:215],a[160:163],v[96:99],v[40:43]
v_exp_f32 v172,v172
v_mfma_f32_16x16x32_bf16 v[212:215],a[164:167],v[100:103],v[212:215]
v_exp_f32 v173,v173
v_mfma_f32_16x16x32_bf16 v[212:215],a[168:171],v[104:107],v[212:215]
v_exp_f32 v174,v174
v_mfma_f32_16x16x32_bf16 v[212:215],a[172:175],v[108:111],v[212:215]
v_exp_f32 v175,v175
v_mfma_f32_16x16x32_bf16 v[220:223],a[188:191],v[108:111],v[40:43]
v_exp_f32 v180,v180
v_mfma_f32_16x16x32_bf16 v[220:223],a[184:187],v[104:107],v[220:223]
v_exp_f32 v181,v181
v_mfma_f32_16x16x32_bf16 v[220:223],a[180:183],v[100:103],v[220:223]
v_exp_f32 v182,v182
v_mfma_f32_16x16x32_bf16 v[220:223],a[176:179],v[96:99],v[220:223]
s_waitcnt lgkmcnt(0)
v_exp_f32 v183,v183
v_mfma_f32_16x16x32_bf16 v[32:35],v[80:83],v[128:131],0
v_cvt_pk_bf16_f32 v166,v172,v173
v_cvt_pk_bf16_f32 v167,v174,v175
v_mfma_f32_16x16x32_bf16 v[32:35],v[84:87],v[160:163],v[32:35]
v_cvt_pk_bf16_f32 v180,v180,v181
v_cvt_pk_bf16_f32 v181,v182,v183
v_mfma_f32_16x16x32_bf16 v[32:35],v[88:91],v[192:195],v[32:35]
v_exp_f32 v188,v188
v_mfma_f32_16x16x32_bf16 v[32:35],v[92:95],v[224:227],v[32:35]
v_exp_f32 v189,v189
v_mfma_f32_16x16x32_bf16 v[36:39],v[80:83],v[136:139],0
v_exp_f32 v190,v190
v_mfma_f32_16x16x32_bf16 v[36:39],v[84:87],v[168:171],v[36:39]
v_exp_f32 v191,v191
v_mfma_f32_16x16x32_bf16 v[36:39],v[88:91],v[200:203],v[36:39]
v_cvt_pk_bf16_f32 v182,v188,v189
v_cvt_pk_bf16_f32 v183,v190,v191
v_mfma_f32_16x16x32_bf16 v[36:39],v[92:95],v[232:235],v[36:39]
v_exp_f32 v196,v196
v_mfma_f32_16x16x32_bf16 v[40:43],v[80:83],v[144:147],0
v_exp_f32 v197,v197
v_mfma_f32_16x16x32_bf16 v[40:43],v[84:87],v[176:179],v[40:43]
v_max3_f32 v32,v32,v33,v34
v_max3_f32 v32,v32,v35,s18
v_mfma_f32_16x16x32_bf16 v[40:43],v[88:91],v[208:211],v[40:43]
v_sub_f32 v35,0,v32
v_sub_f32 v34,0,v32
v_mfma_f32_16x16x32_bf16 v[40:43],v[92:95],v[240:243],v[40:43]
v_sub_f32 v33,0,v32
v_sub_f32 v32,0,v32
v_mfma_f32_16x16x32_bf16 a[0:3],a[192:195],v[132:135],a[0:3]
v_exp_f32 v198,v198
)A"
R"A(v_mfma_f32_16x16x32_bf16 a[0:3],a[196:199],v[148:151],a[0:3]
v_exp_f32 v199,v199
v_mfma_f32_16x16x32_bf16 a[4:7],a[204:207],v[148:151],a[4:7]
v_cvt_pk_bf16_f32 v196,v196,v197
v_cvt_pk_bf16_f32 v197,v198,v199
v_mfma_f32_16x16x32_bf16 a[4:7],a[200:203],v[132:135],a[4:7]
v_exp_f32 v204,v204
v_mfma_f32_16x16x32_bf16 a[8:11],a[208:211],v[132:135],a[8:11]
s_memrealtime s[76:77]
v_exp_f32 v205,v205
v_mfma_f32_16x16x32_bf16 a[8:11],a[212:215],v[148:151],a[8:11]
v_exp_f32 v206,v206
v_mfma_f32_16x16x32_bf16 a[12:15],a[220:223],v[148:151],a[12:15]
v_exp_f32 v207,v207
v_mfma_f32_16x16x32_bf16 a[12:15],a[216:219],v[132:135],a[12:15]
v_exp_f32 v212,v212
v_mfma_f32_16x16x32_bf16 a[16:19],a[224:227],v[132:135],a[16:19]
v_exp_f32 v213,v213
v_mfma_f32_16x16x32_bf16 a[16:19],a[228:231],v[148:151],a[16:19]
v_exp_f32 v214,v214
v_mfma_f32_16x16x32_bf16 a[20:23],a[236:239],v[148:151],a[20:23]
v_exp_f32 v215,v215
v_mfma_f32_16x16x32_bf16 a[20:23],a[232:235],v[132:135],a[20:23]
s_waitcnt lgkmcnt(0)
v_cvt_pk_bf16_f32 v198,v204,v205
v_cvt_pk_bf16_f32 v199,v206,v207
v_mfma_f32_16x16x32_bf16 a[24:27],a[240:243],v[132:135],a[24:27]
s_sub_u32 s79,s78,s76
v_cvt_pk_bf16_f32 v212,v212,v213
v_cvt_pk_bf16_f32 v213,v214,v215
v_mfma_f32_16x16x32_bf16 a[24:27],a[244:247],v[148:151],a[24:27]
s_add_u32 s78,s76,s60
v_exp_f32 v220,v220
v_mfma_f32_16x16x32_bf16 a[28:31],a[252:255],v[148:151],a[28:31]
s_cmp_gt_i32 s79,0
s_cbranch_scc1 L_ps3_%=
L_pr3_%=:
v_max3_f32 v36,v36,v37,v38
v_mfma_f32_16x16x32_bf16 a[28:31],a[248:251],v[132:135],a[28:31]
v_exp_f32 v221,v221
v_mfma_f32_16x16x32_bf16 v[48:51],v[28:31],v[132:135],v[48:51]
v_exp_f32 v222,v222
v_mfma_f32_16x16x32_bf16 v[48:51],v[28:31],v[148:151],v[48:51]
v_exp_f32 v223,v223
v_mfma_f32_16x16x32_bf16 v[228:231],a[128:131],v[112:115],v[44:47]
s_waitcnt vmcnt(9)
v_cvt_pk_bf16_f32 v214,v220,v221
v_cvt_pk_bf16_f32 v215,v222,v223
v_mfma_f32_16x16x32_bf16 v[228:231],a[132:135],v[116:119],v[228:231]
s_barrier
s_add_u32 m0,s49,s51
ds_read_b128 a[128:131],v1
v_mfma_f32_16x16x32_bf16 v[228:231],a[136:139],v[120:123],v[228:231]
ds_read_b128 a[132:135],v1 offset:256
v_max3_f32 v36,v36,v39,s19
v_mfma_f32_16x16x32_bf16 v[228:231],a[140:143],v[124:127],v[228:231]
ds_read_b128 a[136:139],v1 offset:512
v_sub_f32 v39,0,v36
v_mfma_f32_16x16x32_bf16 v[236:239],a[156:159],v[124:127],v[44:47]
buffer_load_dwordx4 v4,s[20:23],s47 offen nt lds
v_mfma_f32_16x16x32_bf16 v[236:239],a[152:155],v[120:123],v[236:239]
ds_read_b128 a[140:143],v1 offset:768
v_sub_f32 v38,0,v36
v_mfma_f32_16x16x32_bf16 v[236:239],a[148:151],v[116:119],v[236:239]
ds_read_b128 a[156:159],v1 offset:4864
v_sub_f32 v37,0,v36
v_mfma_f32_16x16x32_bf16 v[236:239],a[144:147],v[112:115],v[236:239]
ds_read_b128 a[152:155],v1 offset:4608
v_sub_f32 v36,0,v36
v_mfma_f32_16x16x32_bf16 v[244:247],a[160:163],v[112:115],v[44:47]
ds_read_b128 a[148:151],v1 offset:4352
v_max3_f32 v40,v40,v41,v42
v_mfma_f32_16x16x32_bf16 v[244:247],a[164:167],v[116:119],v[244:247]
ds_read_b128 a[144:147],v1 offset:4096
v_max3_f32 v40,v40,v43,s100
v_mfma_f32_16x16x32_bf16 v[244:247],a[168:171],v[120:123],v[244:247]
ds_read_b128 a[160:163],v1 offset:8192
v_sub_f32 v43,0,v40
v_mfma_f32_16x16x32_bf16 v[244:247],a[172:175],v[124:127],v[244:247]
ds_read_b128 a[164:167],v1 offset:8448
v_sub_f32 v42,0,v40
v_mfma_f32_16x16x32_bf16 v[252:255],a[188:191],v[124:127],v[44:47]
ds_read_b128 a[168:171],v1 offset:8704
v_sub_f32 v41,0,v40
v_mfma_f32_16x16x32_bf16 v[252:255],a[184:187],v[120:123],v[252:255]
ds_read_b128 a[172:175],v1 offset:8960
v_sub_f32 v40,0,v40
v_mfma_f32_16x16x32_bf16 v[252:255],a[180:183],v[116:119],v[252:255]
ds_read_b128 a[188:191],v1 offset:13056
v_add_u32 v2,s48,v2
v_mfma_f32_16x16x32_bf16 v[252:255],a[176:179],v[112:115],v[252:255]
ds_read_b128 a[184:187],v1 offset:12800
v_add_u32 v3,s48,v3
v_mfma_f32_16x16x32_bf16 v[44:47],v[80:83],v[152:155],0
s_sub_u32 s48,32768,s62
ds_read_b128 a[180:183],v1 offset:12544
v_max_f32 v20,0x00800000,v48
v_mfma_f32_16x16x32_bf16 v[44:47],v[84:87],v[184:187],v[44:47]
s_add_u32 s50,s50,s48
ds_read_b128 a[176:179],v1 offset:12288
v_accvgpr_read_b32 v64,a0
v_mfma_f32_16x16x32_bf16 v[44:47],v[88:91],v[216:219],v[44:47]
buffer_load_dwordx4 v4,s[20:23],s47 offen offset:1024 nt lds
v_mfma_f32_16x16x32_bf16 v[44:47],v[92:95],v[248:251],v[44:47]
v_exp_f32 v228,v228
v_mfma_f32_16x16x32_bf16 a[32:35],a[192:195],v[164:167],a[32:35]
v_exp_f32 v229,v229
v_mfma_f32_16x16x32_bf16 a[32:35],a[196:199],v[180:183],a[32:35]
v_exp_f32 v230,v230
v_mfma_f32_16x16x32_bf16 a[36:39],a[204:207],v[180:183],a[36:39]
v_exp_f32 v231,v231
v_mfma_f32_16x16x32_bf16 a[36:39],a[200:203],v[164:167],a[36:39]
v_cvt_pk_bf16_f32 v228,v228,v229
v_cvt_pk_bf16_f32 v229,v230,v231
v_mfma_f32_16x16x32_bf16 a[40:43],a[208:211],v[164:167],a[40:43]
v_exp_f32 v236,v236
v_mfma_f32_16x16x32_bf16 a[40:43],a[212:215],v[180:183],a[40:43]
v_exp_f32 v237,v237
v_mfma_f32_16x16x32_bf16 a[44:47],a[220:223],v[180:183],a[44:47]
v_exp_f32 v238,v238
v_mfma_f32_16x16x32_bf16 a[44:47],a[216:219],v[164:167],a[44:47]
v_exp_f32 v239,v239
v_mfma_f32_16x16x32_bf16 a[48:51],a[224:227],v[164:167],a[48:51]
v_exp_f32 v244,v244
v_mfma_f32_16x16x32_bf16 a[48:51],a[228:231],v[180:183],a[48:51]
v_exp_f32 v245,v245
v_mfma_f32_16x16x32_bf16 a[52:55],a[236:239],v[180:183],a[52:55]
v_exp_f32 v246,v246
v_mfma_f32_16x16x32_bf16 a[52:55],a[232:235],v[164:167],a[52:55]
v_exp_f32 v247,v247
v_mfma_f32_16x16x32_bf16 a[56:59],a[240:243],v[164:167],a[56:59]
buffer_load_dwordx4 v5,s[20:23],s47 offen offset:2048 nt lds
v_mfma_f32_16x16x32_bf16 a[56:59],a[244:247],v[180:183],a[56:59]
v_cvt_pk_bf16_f32 v230,v236,v237
v_cvt_pk_bf16_f32 v231,v238,v239
v_mfma_f32_16x16x32_bf16 a[60:63],a[252:255],v[180:183],a[60:63]
v_cvt_pk_bf16_f32 v244,v244,v245
v_cvt_pk_bf16_f32 v245,v246,v247
v_mfma_f32_16x16x32_bf16 a[60:63],a[248:251],v[164:167],a[60:63]
v_exp_f32 v252,v252
v_mfma_f32_16x16x32_bf16 v[52:55],v[28:31],v[164:167],v[52:55]
v_exp_f32 v253,v253
v_mfma_f32_16x16x32_bf16 v[52:55],v[28:31],v[180:183],v[52:55]
s_waitcnt lgkmcnt(0)
v_exp_f32 v254,v254
v_mfma_f32_16x16x32_bf16 v[132:135],a[128:131],v[128:131],v[32:35]
v_exp_f32 v255,v255
v_mfma_f32_16x16x32_bf16 v[132:135],a[132:135],v[160:163],v[132:135]
v_cvt_pk_bf16_f32 v246,v252,v253
v_cvt_pk_bf16_f32 v247,v254,v255
v_mfma_f32_16x16x32_bf16 v[132:135],a[136:139],v[192:195],v[132:135]
v_rcp_f32 v20,v20
v_mfma_f32_16x16x32_bf16 v[132:135],a[140:143],v[224:227],v[132:135]
v_accvgpr_read_b32 v65,a1
v_accvgpr_read_b32 v66,a2
v_mfma_f32_16x16x32_bf16 v[140:143],a[156:159],v[224:227],v[32:35]
v_accvgpr_read_b32 v67,a3
v_mul_f32 v64,v20,v64
v_mfma_f32_16x16x32_bf16 v[140:143],a[152:155],v[192:195],v[140:143]
v_mul_f32 v65,v20,v65
v_cvt_pk_bf16_f32 v64,v64,v65
v_mfma_f32_16x16x32_bf16 v[140:143],a[148:151],v[160:163],v[140:143]
v_mul_f32 v66,v20,v66
v_mul_f32 v67,v20,v67
v_mfma_f32_16x16x32_bf16 v[140:143],a[144:147],v[128:131],v[140:143]
v_cvt_pk_bf16_f32 v65,v66,v67
v_accvgpr_read_b32 v14,a6
v_mfma_f32_16x16x32_bf16 v[148:151],a[160:163],v[128:131],v[32:35]
buffer_load_dwordx4 v5,s[20:23],s47 offen offset:3072 nt lds
v_mfma_f32_16x16x32_bf16 v[148:151],a[164:167],v[160:163],v[148:151]
v_exp_f32 v132,v132
v_mfma_f32_16x16x32_bf16 v[148:151],a[168:171],v[192:195],v[148:151]
v_exp_f32 v133,v133
v_mfma_f32_16x16x32_bf16 v[148:151],a[172:175],v[224:227],v[148:151]
v_exp_f32 v134,v134
v_mfma_f32_16x16x32_bf16 v[156:159],a[188:191],v[224:227],v[32:35]
v_exp_f32 v135,v135
v_mfma_f32_16x16x32_bf16 v[156:159],a[184:187],v[192:195],v[156:159]
v_cvt_pk_bf16_f32 v132,v132,v133
v_cvt_pk_bf16_f32 v133,v134,v135
v_mfma_f32_16x16x32_bf16 v[156:159],a[180:183],v[160:163],v[156:159]
v_exp_f32 v140,v140
v_mfma_f32_16x16x32_bf16 v[156:159],a[176:179],v[128:131],v[156:159]
v_exp_f32 v141,v141
v_mfma_f32_16x16x32_bf16 a[64:67],a[192:195],v[196:199],a[64:67]
v_exp_f32 v142,v142
v_mfma_f32_16x16x32_bf16 a[64:67],a[196:199],v[212:215],a[64:67]
v_exp_f32 v143,v143
v_mfma_f32_16x16x32_bf16 a[68:71],a[204:207],v[212:215],a[68:71]
v_exp_f32 v148,v148
v_mfma_f32_16x16x32_bf16 a[68:71],a[200:203],v[196:199],a[68:71]
s_add_u32 m0,s49,s52
v_exp_f32 v149,v149
v_mfma_f32_16x16x32_bf16 a[72:75],a[208:211],v[196:199],a[72:75]
s_add_u32 s49,s49,32768
v_exp_f32 v150,v150
v_mfma_f32_16x16x32_bf16 a[72:75],a[212:215],v[212:215],a[72:75]
v_exp_f32 v151,v151
v_mfma_f32_16x16x32_bf16 a[76:79],a[220:223],v[212:215],a[76:79]
buffer_load_dwordx4 v4,s[24:27],s47 offen nt lds
v_mfma_f32_16x16x32_bf16 a[76:79],a[216:219],v[196:199],a[76:79]
v_cvt_pk_bf16_f32 v134,v140,v141
v_cvt_pk_bf16_f32 v135,v142,v143
v_mfma_f32_16x16x32_bf16 a[80:83],a[224:227],v[196:199],a[80:83]
v_cvt_pk_bf16_f32 v148,v148,v149
v_cvt_pk_bf16_f32 v149,v150,v151
v_mfma_f32_16x16x32_bf16 a[80:83],a[228:231],v[212:215],a[80:83]
v_exp_f32 v156,v156
v_mfma_f32_16x16x32_bf16 a[84:87],a[236:239],v[212:215],a[84:87]
v_exp_f32 v157,v157
v_mfma_f32_16x16x32_bf16 a[84:87],a[232:235],v[196:199],a[84:87]
v_exp_f32 v158,v158
v_mfma_f32_16x16x32_bf16 a[88:91],a[240:243],v[196:199],a[88:91]
v_exp_f32 v159,v159
v_mfma_f32_16x16x32_bf16 a[88:91],a[244:247],v[212:215],a[88:91]
v_cvt_pk_bf16_f32 v150,v156,v157
v_cvt_pk_bf16_f32 v151,v158,v159
v_mfma_f32_16x16x32_bf16 a[92:95],a[252:255],v[212:215],a[92:95]
v_accvgpr_read_b32 v15,a7
v_accvgpr_read_b32 v66,a4
v_mfma_f32_16x16x32_bf16 a[92:95],a[248:251],v[196:199],a[92:95]
v_accvgpr_read_b32 v67,a5
v_accvgpr_read_b32 v68,a8
v_mfma_f32_16x16x32_bf16 v[56:59],v[28:31],v[196:199],v[56:59]
v_accvgpr_read_b32 v69,a9
v_accvgpr_read_b32 v70,a10
v_mfma_f32_16x16x32_bf16 v[56:59],v[28:31],v[212:215],v[56:59]
v_accvgpr_read_b32 v71,a11
v_mul_f32 v68,v20,v68
v_mfma_f32_16x16x32_bf16 v[164:167],a[128:131],v[136:139],v[36:39]
v_mul_f32 v69,v20,v69
v_cvt_pk_bf16_f32 v68,v68,v69
v_mfma_f32_16x16x32_bf16 v[164:167],a[132:135],v[168:171],v[164:167]
v_mul_f32 v70,v20,v70
v_mul_f32 v71,v20,v71
v_mfma_f32_16x16x32_bf16 v[164:167],a[136:139],v[200:203],v[164:167]
buffer_load_dwordx4 v5,s[24:27],s47 offen offset:1024 nt lds
v_mfma_f32_16x16x32_bf16 v[164:167],a[140:143],v[232:235],v[164:167]
v_cvt_pk_bf16_f32 v69,v70,v71
v_accvgpr_read_b32 v16,a14
v_mfma_f32_16x16x32_bf16 v[172:175],a[156:159],v[232:235],v[36:39]
v_accvgpr_read_b32 v17,a15
v_accvgpr_read_b32 v70,a12
v_mfma_f32_16x16x32_bf16 v[172:175],a[152:155],v[200:203],v[172:175]
v_accvgpr_read_b32 v71,a13
v_accvgpr_read_b32 v72,a16
v_mfma_f32_16x16x32_bf16 v[172:175],a[148:151],v[168:171],v[172:175]
v_accvgpr_read_b32 v73,a17
v_accvgpr_read_b32 v74,a18
v_mfma_f32_16x16x32_bf16 v[172:175],a[144:147],v[136:139],v[172:175]
v_accvgpr_read_b32 v75,a19
v_mul_f32 v72,v20,v72
v_mfma_f32_16x16x32_bf16 v[180:183],a[160:163],v[136:139],v[36:39]
v_mul_f32 v73,v20,v73
v_cvt_pk_bf16_f32 v72,v72,v73
v_mfma_f32_16x16x32_bf16 v[180:183],a[164:167],v[168:171],v[180:183]
v_mul_f32 v74,v20,v74
v_mul_f32 v75,v20,v75
v_mfma_f32_16x16x32_bf16 v[180:183],a[168:171],v[200:203],v[180:183]
v_cvt_pk_bf16_f32 v73,v74,v75
v_accvgpr_read_b32 v12,a22
v_mfma_f32_16x16x32_bf16 v[180:183],a[172:175],v[232:235],v[180:183]
v_accvgpr_read_b32 v13,a23
v_accvgpr_read_b32 v74,a20
v_mfma_f32_16x16x32_bf16 v[188:191],a[188:191],v[232:235],v[36:39]
v_accvgpr_read_b32 v75,a21
v_accvgpr_read_b32 v76,a24
v_mfma_f32_16x16x32_bf16 v[188:191],a[184:187],v[200:203],v[188:191]
v_accvgpr_read_b32 v77,a25
v_accvgpr_read_b32 v78,a26
v_mfma_f32_16x16x32_bf16 v[188:191],a[180:183],v[168:171],v[188:191]
v_accvgpr_read_b32 v79,a27
v_mul_f32 v66,v20,v66
v_mfma_f32_16x16x32_bf16 v[188:191],a[176:179],v[136:139],v[188:191]
v_mul_f32 v67,v20,v67
v_mul_f32 v76,v20,v76
v_mfma_f32_16x16x32_bf16 a[96:99],a[192:195],v[228:231],a[96:99]
buffer_load_dwordx4 v4,s[24:27],s47 offen offset:2048 nt lds
v_mfma_f32_16x16x32_bf16 a[96:99],a[196:199],v[244:247],a[96:99]
ds_read_b64_tr_b16 a[192:193],v2
ds_read_b64_tr_b16 a[194:195],v2 offset:4096
v_mfma_f32_16x16x32_bf16 a[100:103],a[204:207],v[244:247],a[100:103]
ds_read_b64_tr_b16 a[196:197],v2 offset:8192
ds_read_b64_tr_b16 a[198:199],v2 offset:12288
v_mfma_f32_16x16x32_bf16 a[100:103],a[200:203],v[228:231],a[100:103]
)A"
R"A(ds_read_b64_tr_b16 a[204:205],v3 offset:8192
ds_read_b64_tr_b16 a[206:207],v3 offset:12288
v_mfma_f32_16x16x32_bf16 a[104:107],a[208:211],v[228:231],a[104:107]
ds_read_b64_tr_b16 a[200:201],v3
ds_read_b64_tr_b16 a[202:203],v3 offset:4096
v_mfma_f32_16x16x32_bf16 a[104:107],a[212:215],v[244:247],a[104:107]
ds_read_b64_tr_b16 a[208:209],v2 offset:256
ds_read_b64_tr_b16 a[210:211],v2 offset:4352
v_mfma_f32_16x16x32_bf16 a[108:111],a[220:223],v[244:247],a[108:111]
ds_read_b64_tr_b16 a[212:213],v2 offset:8448
ds_read_b64_tr_b16 a[214:215],v2 offset:12544
v_mfma_f32_16x16x32_bf16 a[108:111],a[216:219],v[228:231],a[108:111]
ds_read_b64_tr_b16 a[220:221],v3 offset:8448
ds_read_b64_tr_b16 a[222:223],v3 offset:12544
v_mfma_f32_16x16x32_bf16 a[112:115],a[224:227],v[228:231],a[112:115]
ds_read_b64_tr_b16 a[216:217],v3 offset:256
ds_read_b64_tr_b16 a[218:219],v3 offset:4352
v_mfma_f32_16x16x32_bf16 a[112:115],a[228:231],v[244:247],a[112:115]
ds_read_b64_tr_b16 a[224:225],v2 offset:512
ds_read_b64_tr_b16 a[226:227],v2 offset:4608
v_mfma_f32_16x16x32_bf16 a[116:119],a[236:239],v[244:247],a[116:119]
ds_read_b64_tr_b16 a[228:229],v2 offset:8704
ds_read_b64_tr_b16 a[230:231],v2 offset:12800
v_mfma_f32_16x16x32_bf16 a[116:119],a[232:235],v[228:231],a[116:119]
ds_read_b64_tr_b16 a[236:237],v3 offset:8704
ds_read_b64_tr_b16 a[238:239],v3 offset:12800
v_mfma_f32_16x16x32_bf16 a[120:123],a[240:243],v[228:231],a[120:123]
ds_read_b64_tr_b16 a[232:233],v3 offset:512
ds_read_b64_tr_b16 a[234:235],v3 offset:4608
v_mfma_f32_16x16x32_bf16 a[120:123],a[244:247],v[244:247],a[120:123]
ds_read_b64_tr_b16 a[240:241],v2 offset:768
ds_read_b64_tr_b16 a[242:243],v2 offset:4864
v_mfma_f32_16x16x32_bf16 a[124:127],a[252:255],v[244:247],a[124:127]
buffer_load_dwordx4 v5,s[24:27],s47 offen offset:3072 nt lds
v_mfma_f32_16x16x32_bf16 a[124:127],a[248:251],v[228:231],a[124:127]
s_add_u32 s47,s47,16384
ds_read_b64_tr_b16 a[244:245],v2 offset:8960
ds_read_b64_tr_b16 a[246:247],v2 offset:13056
v_mfma_f32_16x16x32_bf16 v[60:63],v[28:31],v[228:231],v[60:63]
s_and_b32 s47,s47,0x3ffff
ds_read_b64_tr_b16 a[252:253],v3 offset:8960
ds_read_b64_tr_b16 a[254:255],v3 offset:13056
v_mfma_f32_16x16x32_bf16 v[60:63],v[28:31],v[244:247],v[60:63]
s_or_b32 s47,s47,s46
ds_read_b64_tr_b16 a[248:249],v3 offset:768
ds_read_b64_tr_b16 a[250:251],v3 offset:4864
v_mfma_f32_16x16x32_bf16 v[196:199],a[128:131],v[144:147],v[40:43]
s_and_b32 s46,s54,0xfffc0000
v_mul_f32 v77,v20,v77
v_mul_f32 v14,v20,v14
v_mfma_f32_16x16x32_bf16 v[196:199],a[132:135],v[176:179],v[196:199]
v_mul_f32 v15,v20,v15
v_cvt_pk_bf16_f32 v66,v66,v67
v_mfma_f32_16x16x32_bf16 v[196:199],a[136:139],v[208:211],v[196:199]
v_cvt_pk_bf16_f32 v76,v76,v77
v_mul_f32 v78,v20,v78
v_mfma_f32_16x16x32_bf16 v[196:199],a[140:143],v[240:243],v[196:199]
v_mul_f32 v79,v20,v79
v_cvt_pk_bf16_f32 v67,v14,v15
v_mfma_f32_16x16x32_bf16 v[204:207],a[156:159],v[240:243],v[40:43]
v_cvt_pk_bf16_f32 v77,v78,v79
v_accvgpr_read_b32 v14,a30
v_mfma_f32_16x16x32_bf16 v[204:207],a[152:155],v[208:211],v[204:207]
v_accvgpr_read_b32 v15,a31
v_accvgpr_read_b32 v78,a28
v_mfma_f32_16x16x32_bf16 v[204:207],a[148:151],v[176:179],v[204:207]
v_accvgpr_read_b32 v79,a29
v_max3_f32 v44,v44,v45,v46
v_mfma_f32_16x16x32_bf16 v[204:207],a[144:147],v[144:147],v[204:207]
v_max3_f32 v44,v44,v47,s101
v_sub_f32 v47,0,v44
v_mfma_f32_16x16x32_bf16 v[212:215],a[160:163],v[144:147],v[40:43]
v_sub_f32 v46,0,v44
v_sub_f32 v45,0,v44
v_mfma_f32_16x16x32_bf16 v[212:215],a[164:167],v[176:179],v[212:215]
v_cmp_nlt_f32 s[56:57],v48,s63
v_sub_f32 v44,0,v44
v_mfma_f32_16x16x32_bf16 v[212:215],a[168:171],v[208:211],v[212:215]
s_cmp_eq_u32 s49,163840
s_cselect_b32 s49,0,s49
v_max_f32 v21,0x00800000,v52
v_mfma_f32_16x16x32_bf16 v[212:215],a[172:175],v[240:243],v[212:215]
v_exp_f32 v164,v164
v_mfma_f32_16x16x32_bf16 v[220:223],a[188:191],v[240:243],v[40:43]
v_exp_f32 v165,v165
v_mfma_f32_16x16x32_bf16 v[220:223],a[184:187],v[208:211],v[220:223]
v_exp_f32 v166,v166
v_mfma_f32_16x16x32_bf16 v[220:223],a[180:183],v[176:179],v[220:223]
v_exp_f32 v167,v167
v_mfma_f32_16x16x32_bf16 v[220:223],a[176:179],v[144:147],v[220:223]
s_waitcnt lgkmcnt(0)
v_cvt_pk_bf16_f32 v164,v164,v165
v_cvt_pk_bf16_f32 v165,v166,v167
v_mfma_f32_16x16x32_bf16 a[0:3],a[192:195],v[132:135],0
v_exp_f32 v172,v172
v_mfma_f32_16x16x32_bf16 a[0:3],a[196:199],v[148:151],a[0:3]
v_exp_f32 v173,v173
v_mfma_f32_16x16x32_bf16 a[4:7],a[204:207],v[148:151],0
v_exp_f32 v174,v174
v_mfma_f32_16x16x32_bf16 a[4:7],a[200:203],v[132:135],a[4:7]
v_exp_f32 v175,v175
v_mfma_f32_16x16x32_bf16 a[8:11],a[208:211],v[132:135],0
s_memrealtime s[76:77]
v_exp_f32 v180,v180
v_mfma_f32_16x16x32_bf16 a[8:11],a[212:215],v[148:151],a[8:11]
v_exp_f32 v181,v181
v_mfma_f32_16x16x32_bf16 a[12:15],a[220:223],v[148:151],0
v_exp_f32 v182,v182
v_mfma_f32_16x16x32_bf16 a[12:15],a[216:219],v[132:135],a[12:15]
v_exp_f32 v183,v183
v_mfma_f32_16x16x32_bf16 a[16:19],a[224:227],v[132:135],0
v_cvt_pk_bf16_f32 v166,v172,v173
v_cvt_pk_bf16_f32 v167,v174,v175
v_mfma_f32_16x16x32_bf16 a[16:19],a[228:231],v[148:151],a[16:19]
v_cvt_pk_bf16_f32 v180,v180,v181
v_cvt_pk_bf16_f32 v181,v182,v183
v_mfma_f32_16x16x32_bf16 a[20:23],a[236:239],v[148:151],0
v_exp_f32 v188,v188
v_mfma_f32_16x16x32_bf16 a[20:23],a[232:235],v[132:135],a[20:23]
s_waitcnt lgkmcnt(0)
v_exp_f32 v189,v189
v_mfma_f32_16x16x32_bf16 a[24:27],a[240:243],v[132:135],0
s_sub_u32 s79,s78,s76
v_exp_f32 v190,v190
v_mfma_f32_16x16x32_bf16 a[24:27],a[244:247],v[148:151],a[24:27]
s_add_u32 s78,s76,s60
v_exp_f32 v191,v191
v_mfma_f32_16x16x32_bf16 a[28:31],a[252:255],v[148:151],0
s_cmp_gt_i32 s79,0
s_cbranch_scc1 L_ps4_%=
L_pr4_%=:
v_cvt_pk_bf16_f32 v182,v188,v189
v_mfma_f32_16x16x32_bf16 a[28:31],a[248:251],v[132:135],a[28:31]
v_cvt_pk_bf16_f32 v183,v190,v191
v_accvgpr_read_b32 v80,a32
v_mfma_f32_16x16x32_bf16 v[48:51],v[28:31],v[132:135],0
v_rcp_f32 v21,v21
v_mfma_f32_16x16x32_bf16 v[48:51],v[28:31],v[148:151],v[48:51]
v_accvgpr_read_b32 v81,a33
v_accvgpr_read_b32 v82,a34
v_mfma_f32_16x16x32_bf16 v[228:231],a[128:131],v[152:155],v[44:47]
s_waitcnt vmcnt(8)
v_accvgpr_read_b32 v83,a35
v_mul_f32 v70,v20,v70
v_mfma_f32_16x16x32_bf16 v[228:231],a[132:135],v[184:187],v[228:231]
s_barrier
s_add_u32 m0,s49,s51
v_mul_f32 v71,v20,v71
v_mfma_f32_16x16x32_bf16 v[228:231],a[136:139],v[216:219],v[228:231]
v_mul_f32 v80,v21,v80
v_mul_f32 v81,v21,v81
v_mfma_f32_16x16x32_bf16 v[228:231],a[140:143],v[248:251],v[228:231]
v_mul_f32 v16,v20,v16
v_mul_f32 v17,v20,v17
v_mfma_f32_16x16x32_bf16 v[236:239],a[156:159],v[248:251],v[44:47]
buffer_load_dwordx4 v4,s[20:23],s47 offen nt lds
v_mfma_f32_16x16x32_bf16 v[236:239],a[152:155],v[216:219],v[236:239]
v_cvt_pk_bf16_f32 v70,v70,v71
v_cvt_pk_bf16_f32 v80,v80,v81
v_mfma_f32_16x16x32_bf16 v[236:239],a[148:151],v[184:187],v[236:239]
v_mul_f32 v82,v21,v82
v_mul_f32 v83,v21,v83
v_mfma_f32_16x16x32_bf16 v[236:239],a[144:147],v[152:155],v[236:239]
v_cvt_pk_bf16_f32 v71,v16,v17
v_cvt_pk_bf16_f32 v81,v82,v83
v_mfma_f32_16x16x32_bf16 v[244:247],a[160:163],v[152:155],v[44:47]
v_accvgpr_read_b32 v16,a38
v_accvgpr_read_b32 v17,a39
v_mfma_f32_16x16x32_bf16 v[244:247],a[164:167],v[184:187],v[244:247]
v_accvgpr_read_b32 v82,a36
v_accvgpr_read_b32 v83,a37
v_mfma_f32_16x16x32_bf16 v[244:247],a[168:171],v[216:219],v[244:247]
v_accvgpr_read_b32 v84,a40
v_accvgpr_read_b32 v85,a41
v_mfma_f32_16x16x32_bf16 v[244:247],a[172:175],v[248:251],v[244:247]
v_accvgpr_read_b32 v86,a42
v_accvgpr_read_b32 v87,a43
v_mfma_f32_16x16x32_bf16 v[252:255],a[188:191],v[248:251],v[44:47]
v_mul_f32 v74,v20,v74
v_mul_f32 v75,v20,v75
v_mfma_f32_16x16x32_bf16 v[252:255],a[184:187],v[216:219],v[252:255]
v_mul_f32 v84,v21,v84
v_mul_f32 v85,v21,v85
v_mfma_f32_16x16x32_bf16 v[252:255],a[180:183],v[184:187],v[252:255]
v_mul_f32 v12,v20,v12
v_mul_f32 v13,v20,v13
v_mfma_f32_16x16x32_bf16 v[252:255],a[176:179],v[152:155],v[252:255]
v_cvt_pk_bf16_f32 v74,v74,v75
v_cvt_pk_bf16_f32 v84,v84,v85
v_mfma_f32_16x16x32_bf16 a[32:35],a[192:195],v[164:167],0
v_mul_f32 v86,v21,v86
v_mul_f32 v87,v21,v87
v_mfma_f32_16x16x32_bf16 a[32:35],a[196:199],v[180:183],a[32:35]
v_cvt_pk_bf16_f32 v75,v12,v13
v_cvt_pk_bf16_f32 v85,v86,v87
v_mfma_f32_16x16x32_bf16 a[36:39],a[204:207],v[180:183],0
v_accvgpr_read_b32 v12,a46
v_accvgpr_read_b32 v13,a47
v_mfma_f32_16x16x32_bf16 a[36:39],a[200:203],v[164:167],a[36:39]
v_accvgpr_read_b32 v86,a44
v_accvgpr_read_b32 v87,a45
v_mfma_f32_16x16x32_bf16 a[40:43],a[208:211],v[164:167],0
buffer_load_dwordx4 v4,s[20:23],s47 offen offset:1024 nt lds
v_mfma_f32_16x16x32_bf16 a[40:43],a[212:215],v[180:183],a[40:43]
v_accvgpr_read_b32 v88,a48
v_accvgpr_read_b32 v89,a49
v_mfma_f32_16x16x32_bf16 a[44:47],a[220:223],v[180:183],0
v_accvgpr_read_b32 v90,a50
v_accvgpr_read_b32 v91,a51
v_mfma_f32_16x16x32_bf16 a[44:47],a[216:219],v[164:167],a[44:47]
v_mul_f32 v78,v20,v78
v_mul_f32 v79,v20,v79
v_mfma_f32_16x16x32_bf16 a[48:51],a[224:227],v[164:167],0
v_mul_f32 v88,v21,v88
v_mul_f32 v89,v21,v89
v_mul_f32 v14,v20,v14
v_mul_f32 v15,v20,v15
v_cvt_pk_bf16_f32 v78,v78,v79
v_add_u32 v1,s48,v1
v_cvt_pk_bf16_f32 v88,v88,v89
v_mul_f32 v90,v21,v90
v_mul_f32 v91,v21,v91
v_mfma_f32_16x16x32_bf16 a[48:51],a[228:231],v[180:183],a[48:51]
ds_read_b128 a[128:131],v1
v_cvt_pk_bf16_f32 v79,v14,v15
v_cvt_pk_bf16_f32 v89,v90,v91
v_accvgpr_read_b32 v14,a54
v_accvgpr_read_b32 v15,a55
v_accvgpr_read_b32 v90,a52
v_accvgpr_read_b32 v91,a53
v_mfma_f32_16x16x32_bf16 a[52:55],a[236:239],v[180:183],0
ds_read_b128 a[132:135],v1 offset:256
v_accvgpr_read_b32 v92,a56
v_mfma_f32_16x16x32_bf16 a[52:55],a[232:235],v[164:167],a[52:55]
ds_read_b128 a[136:139],v1 offset:512
v_accvgpr_read_b32 v93,a57
v_accvgpr_read_b32 v94,a58
v_accvgpr_read_b32 v95,a59
v_mfma_f32_16x16x32_bf16 a[56:59],a[240:243],v[164:167],0
ds_read_b128 a[140:143],v1 offset:768
v_mul_f32 v82,v21,v82
v_mul_f32 v83,v21,v83
v_mul_f32 v92,v21,v92
v_mul_f32 v93,v21,v93
v_mul_f32 v16,v21,v16
v_mul_f32 v17,v21,v17
v_cvt_pk_bf16_f32 v82,v82,v83
v_cvt_pk_bf16_f32 v92,v92,v93
v_mul_f32 v94,v21,v94
v_mul_f32 v95,v21,v95
v_mfma_f32_16x16x32_bf16 a[56:59],a[244:247],v[180:183],a[56:59]
ds_read_b128 a[156:159],v1 offset:4864
v_cvt_pk_bf16_f32 v83,v16,v17
v_cvt_pk_bf16_f32 v93,v94,v95
v_accvgpr_read_b32 v16,a62
v_accvgpr_read_b32 v17,a63
v_accvgpr_read_b32 v94,a60
v_accvgpr_read_b32 v95,a61
v_mfma_f32_16x16x32_bf16 a[60:63],a[252:255],v[180:183],0
ds_read_b128 a[152:155],v1 offset:4608
v_max_f32 v22,0x00800000,v56
v_mfma_f32_16x16x32_bf16 a[60:63],a[248:251],v[164:167],a[60:63]
ds_read_b128 a[148:151],v1 offset:4352
v_cmp_nlt_f32 s[58:59],v52,s63
s_nop 1
s_or_b64 s[56:57],s[56:57],s[58:59]
v_mfma_f32_16x16x32_bf16 v[52:55],v[28:31],v[164:167],0
ds_read_b128 a[144:147],v1 offset:4096
v_accvgpr_read_b32 v96,a64
v_mfma_f32_16x16x32_bf16 v[52:55],v[28:31],v[180:183],v[52:55]
s_waitcnt lgkmcnt(7)
ds_read_b128 a[160:163],v1 offset:8192
v_accvgpr_read_b32 v97,a65
v_mfma_f32_16x16x32_bf16 v[132:135],a[128:131],v[128:131],v[32:35]
s_waitcnt lgkmcnt(7)
ds_read_b128 a[164:167],v1 offset:8448
v_accvgpr_read_b32 v98,a66
v_mfma_f32_16x16x32_bf16 v[132:135],a[132:135],v[160:163],v[132:135]
s_waitcnt lgkmcnt(7)
buffer_load_dwordx4 v5,s[20:23],s47 offen offset:2048 nt lds
ds_read_b128 a[168:171],v1 offset:8704
v_mfma_f32_16x16x32_bf16 v[132:135],a[136:139],v[192:195],v[132:135]
s_waitcnt lgkmcnt(7)
ds_read_b128 a[172:175],v1 offset:8960
v_accvgpr_read_b32 v99,a67
v_mfma_f32_16x16x32_bf16 v[132:135],a[140:143],v[224:227],v[132:135]
s_waitcnt lgkmcnt(7)
ds_read_b128 a[188:191],v1 offset:13056
v_mul_f32 v86,v21,v86
v_mfma_f32_16x16x32_bf16 v[140:143],a[156:159],v[224:227],v[32:35]
s_waitcnt lgkmcnt(7)
ds_read_b128 a[184:187],v1 offset:12800
v_mul_f32 v87,v21,v87
v_mfma_f32_16x16x32_bf16 v[140:143],a[152:155],v[192:195],v[140:143]
s_waitcnt lgkmcnt(7)
ds_read_b128 a[180:183],v1 offset:12544
v_mul_f32 v12,v21,v12
v_mfma_f32_16x16x32_bf16 v[140:143],a[148:151],v[160:163],v[140:143]
s_waitcnt lgkmcnt(7)
ds_read_b128 a[176:179],v1 offset:12288
v_mul_f32 v13,v21,v13
v_mfma_f32_16x16x32_bf16 v[140:143],a[144:147],v[128:131],v[140:143]
)A"
R"A(s_waitcnt lgkmcnt(7)
v_exp_f32 v196,v196
v_mfma_f32_16x16x32_bf16 v[148:151],a[160:163],v[128:131],v[32:35]
s_waitcnt lgkmcnt(6)
v_exp_f32 v197,v197
v_mfma_f32_16x16x32_bf16 v[148:151],a[164:167],v[160:163],v[148:151]
s_waitcnt lgkmcnt(5)
v_exp_f32 v198,v198
v_mfma_f32_16x16x32_bf16 v[148:151],a[168:171],v[192:195],v[148:151]
s_waitcnt lgkmcnt(4)
v_exp_f32 v199,v199
v_mfma_f32_16x16x32_bf16 v[148:151],a[172:175],v[224:227],v[148:151]
s_waitcnt lgkmcnt(3)
v_cvt_pk_bf16_f32 v196,v196,v197
v_cvt_pk_bf16_f32 v197,v198,v199
v_mfma_f32_16x16x32_bf16 v[156:159],a[188:191],v[224:227],v[32:35]
s_waitcnt lgkmcnt(2)
v_exp_f32 v204,v204
v_exp_f32 v205,v205
v_exp_f32 v206,v206
v_exp_f32 v207,v207
v_exp_f32 v212,v212
v_exp_f32 v213,v213
v_mfma_f32_16x16x32_bf16 v[156:159],a[184:187],v[192:195],v[156:159]
s_waitcnt lgkmcnt(1)
v_exp_f32 v214,v214
v_exp_f32 v215,v215
v_cvt_pk_bf16_f32 v198,v204,v205
v_cvt_pk_bf16_f32 v199,v206,v207
v_cvt_pk_bf16_f32 v212,v212,v213
v_exp_f32 v220,v220
v_exp_f32 v221,v221
v_exp_f32 v222,v222
v_exp_f32 v223,v223
v_mfma_f32_16x16x32_bf16 v[156:159],a[180:183],v[160:163],v[156:159]
s_waitcnt lgkmcnt(0)
v_cvt_pk_bf16_f32 v213,v214,v215
v_cvt_pk_bf16_f32 v214,v220,v221
v_cvt_pk_bf16_f32 v215,v222,v223
v_mfma_f32_16x16x32_bf16 v[156:159],a[176:179],v[128:131],v[156:159]
buffer_load_dwordx4 v5,s[20:23],s47 offen offset:3072 nt lds
v_rcp_f32 v22,v22
v_mfma_f32_16x16x32_bf16 a[64:67],a[192:195],v[196:199],0
v_mul_f32 v96,v22,v96
v_mul_f32 v97,v22,v97
v_cvt_pk_bf16_f32 v86,v86,v87
v_cvt_pk_bf16_f32 v96,v96,v97
v_mul_f32 v98,v22,v98
v_mul_f32 v99,v22,v99
v_mfma_f32_16x16x32_bf16 a[64:67],a[196:199],v[212:215],a[64:67]
v_cvt_pk_bf16_f32 v87,v12,v13
v_cvt_pk_bf16_f32 v97,v98,v99
v_accvgpr_read_b32 v12,a70
v_accvgpr_read_b32 v13,a71
v_accvgpr_read_b32 v98,a68
v_accvgpr_read_b32 v99,a69
v_mfma_f32_16x16x32_bf16 a[68:71],a[204:207],v[212:215],0
v_accvgpr_read_b32 v100,a72
v_accvgpr_read_b32 v101,a73
v_mfma_f32_16x16x32_bf16 a[68:71],a[200:203],v[196:199],a[68:71]
v_accvgpr_read_b32 v102,a74
v_accvgpr_read_b32 v103,a75
v_mfma_f32_16x16x32_bf16 a[72:75],a[208:211],v[196:199],0
v_mul_f32 v90,v21,v90
v_mul_f32 v91,v21,v91
v_mul_f32 v100,v22,v100
v_mul_f32 v101,v22,v101
v_mul_f32 v14,v21,v14
v_mul_f32 v15,v21,v15
v_cvt_pk_bf16_f32 v90,v90,v91
v_cvt_pk_bf16_f32 v100,v100,v101
v_mul_f32 v102,v22,v102
v_mul_f32 v103,v22,v103
v_mfma_f32_16x16x32_bf16 a[72:75],a[212:215],v[212:215],a[72:75]
v_cvt_pk_bf16_f32 v91,v14,v15
v_cvt_pk_bf16_f32 v101,v102,v103
v_accvgpr_read_b32 v14,a78
v_accvgpr_read_b32 v15,a79
v_accvgpr_read_b32 v102,a76
v_accvgpr_read_b32 v103,a77
v_mfma_f32_16x16x32_bf16 a[76:79],a[220:223],v[212:215],0
v_accvgpr_read_b32 v104,a80
v_accvgpr_read_b32 v105,a81
v_mfma_f32_16x16x32_bf16 a[76:79],a[216:219],v[196:199],a[76:79]
s_add_u32 m0,s49,s52
v_accvgpr_read_b32 v106,a82
v_accvgpr_read_b32 v107,a83
v_mfma_f32_16x16x32_bf16 a[80:83],a[224:227],v[196:199],0
v_mul_f32 v94,v21,v94
v_mul_f32 v95,v21,v95
v_mul_f32 v104,v22,v104
v_mul_f32 v105,v22,v105
v_mul_f32 v16,v21,v16
v_mul_f32 v17,v21,v17
v_cvt_pk_bf16_f32 v94,v94,v95
v_cvt_pk_bf16_f32 v104,v104,v105
v_mul_f32 v106,v22,v106
v_mul_f32 v107,v22,v107
v_mfma_f32_16x16x32_bf16 a[80:83],a[228:231],v[212:215],a[80:83]
v_cvt_pk_bf16_f32 v95,v16,v17
v_cvt_pk_bf16_f32 v105,v106,v107
v_accvgpr_read_b32 v16,a86
v_accvgpr_read_b32 v17,a87
v_accvgpr_read_b32 v106,a84
v_accvgpr_read_b32 v107,a85
v_mfma_f32_16x16x32_bf16 a[84:87],a[236:239],v[212:215],0
s_add_u32 s49,s49,32768
v_accvgpr_read_b32 v108,a88
v_accvgpr_read_b32 v109,a89
v_mfma_f32_16x16x32_bf16 a[84:87],a[232:235],v[196:199],a[84:87]
v_accvgpr_read_b32 v110,a90
v_accvgpr_read_b32 v111,a91
v_mfma_f32_16x16x32_bf16 a[88:91],a[240:243],v[196:199],0
v_mul_f32 v98,v22,v98
v_mul_f32 v99,v22,v99
v_mul_f32 v108,v22,v108
v_mul_f32 v109,v22,v109
v_mul_f32 v12,v22,v12
v_mul_f32 v13,v22,v13
v_cvt_pk_bf16_f32 v98,v98,v99
v_cvt_pk_bf16_f32 v108,v108,v109
v_mul_f32 v110,v22,v110
v_mul_f32 v111,v22,v111
v_mfma_f32_16x16x32_bf16 a[88:91],a[244:247],v[212:215],a[88:91]
buffer_load_dwordx4 v4,s[24:27],s47 offen nt lds
v_cvt_pk_bf16_f32 v99,v12,v13
v_cvt_pk_bf16_f32 v109,v110,v111
v_accvgpr_read_b32 v12,a94
v_accvgpr_read_b32 v13,a95
v_accvgpr_read_b32 v110,a92
v_accvgpr_read_b32 v111,a93
v_mfma_f32_16x16x32_bf16 a[92:95],a[252:255],v[212:215],0
v_cmp_nlt_f32 s[58:59],v56,s63
s_nop 1
s_or_b64 s[56:57],s[56:57],s[58:59]
v_mfma_f32_16x16x32_bf16 a[92:95],a[248:251],v[196:199],a[92:95]
v_exp_f32 v228,v228
v_mfma_f32_16x16x32_bf16 v[56:59],v[28:31],v[196:199],0
v_exp_f32 v229,v229
v_mfma_f32_16x16x32_bf16 v[56:59],v[28:31],v[212:215],v[56:59]
v_exp_f32 v230,v230
v_mfma_f32_16x16x32_bf16 v[164:167],a[128:131],v[136:139],v[36:39]
v_exp_f32 v231,v231
v_mfma_f32_16x16x32_bf16 v[164:167],a[132:135],v[168:171],v[164:167]
v_cvt_pk_bf16_f32 v228,v228,v229
v_cvt_pk_bf16_f32 v229,v230,v231
v_mfma_f32_16x16x32_bf16 v[164:167],a[136:139],v[200:203],v[164:167]
v_exp_f32 v236,v236
v_mfma_f32_16x16x32_bf16 v[164:167],a[140:143],v[232:235],v[164:167]
v_exp_f32 v237,v237
v_mfma_f32_16x16x32_bf16 v[172:175],a[156:159],v[232:235],v[36:39]
v_exp_f32 v238,v238
v_mfma_f32_16x16x32_bf16 v[172:175],a[152:155],v[200:203],v[172:175]
v_exp_f32 v239,v239
v_mfma_f32_16x16x32_bf16 v[172:175],a[148:151],v[168:171],v[172:175]
buffer_load_dwordx4 v5,s[24:27],s47 offen offset:1024 nt lds
v_mfma_f32_16x16x32_bf16 v[172:175],a[144:147],v[136:139],v[172:175]
v_exp_f32 v244,v244
v_mfma_f32_16x16x32_bf16 v[180:183],a[160:163],v[136:139],v[36:39]
v_exp_f32 v245,v245
v_mfma_f32_16x16x32_bf16 v[180:183],a[164:167],v[168:171],v[180:183]
v_exp_f32 v246,v246
v_mfma_f32_16x16x32_bf16 v[180:183],a[168:171],v[200:203],v[180:183]
v_exp_f32 v247,v247
v_mfma_f32_16x16x32_bf16 v[180:183],a[172:175],v[232:235],v[180:183]
v_cvt_pk_bf16_f32 v230,v236,v237
v_cvt_pk_bf16_f32 v231,v238,v239
v_mfma_f32_16x16x32_bf16 v[188:191],a[188:191],v[232:235],v[36:39]
v_cvt_pk_bf16_f32 v244,v244,v245
v_max_f32 v23,0x00800000,v60
v_mfma_f32_16x16x32_bf16 v[188:191],a[184:187],v[200:203],v[188:191]
v_exp_f32 v252,v252
v_exp_f32 v253,v253
v_exp_f32 v254,v254
v_exp_f32 v255,v255
v_mfma_f32_16x16x32_bf16 v[188:191],a[180:183],v[168:171],v[188:191]
v_cvt_pk_bf16_f32 v245,v246,v247
v_cvt_pk_bf16_f32 v246,v252,v253
v_cvt_pk_bf16_f32 v247,v254,v255
v_mfma_f32_16x16x32_bf16 v[188:191],a[176:179],v[136:139],v[188:191]
v_rcp_f32 v23,v23
v_accvgpr_read_b32 v112,a96
v_accvgpr_read_b32 v113,a97
v_accvgpr_read_b32 v114,a98
v_accvgpr_read_b32 v115,a99
v_mfma_f32_16x16x32_bf16 a[96:99],a[192:195],v[228:231],0
v_mul_f32 v102,v22,v102
v_mul_f32 v103,v22,v103
v_mul_f32 v112,v23,v112
v_mul_f32 v113,v23,v113
v_mul_f32 v14,v22,v14
v_mul_f32 v15,v22,v15
v_cvt_pk_bf16_f32 v102,v102,v103
v_cvt_pk_bf16_f32 v112,v112,v113
v_mul_f32 v114,v23,v114
v_mul_f32 v115,v23,v115
v_mfma_f32_16x16x32_bf16 a[96:99],a[196:199],v[244:247],a[96:99]
v_cvt_pk_bf16_f32 v103,v14,v15
v_cvt_pk_bf16_f32 v113,v114,v115
v_accvgpr_read_b32 v14,a102
v_accvgpr_read_b32 v15,a103
v_accvgpr_read_b32 v114,a100
v_accvgpr_read_b32 v115,a101
v_mfma_f32_16x16x32_bf16 a[100:103],a[204:207],v[244:247],0
v_accvgpr_read_b32 v116,a104
v_accvgpr_read_b32 v117,a105
v_mfma_f32_16x16x32_bf16 a[100:103],a[200:203],v[228:231],a[100:103]
v_accvgpr_read_b32 v118,a106
v_accvgpr_read_b32 v119,a107
v_mfma_f32_16x16x32_bf16 a[104:107],a[208:211],v[228:231],0
v_mul_f32 v106,v22,v106
v_mul_f32 v107,v22,v107
v_mul_f32 v116,v23,v116
v_mul_f32 v117,v23,v117
v_mul_f32 v16,v22,v16
v_mul_f32 v17,v22,v17
v_cvt_pk_bf16_f32 v106,v106,v107
v_cvt_pk_bf16_f32 v116,v116,v117
v_mul_f32 v118,v23,v118
v_mul_f32 v119,v23,v119
v_mfma_f32_16x16x32_bf16 a[104:107],a[212:215],v[244:247],a[104:107]
v_cvt_pk_bf16_f32 v107,v16,v17
v_cvt_pk_bf16_f32 v117,v118,v119
v_accvgpr_read_b32 v16,a110
v_accvgpr_read_b32 v17,a111
v_accvgpr_read_b32 v118,a108
v_accvgpr_read_b32 v119,a109
v_mfma_f32_16x16x32_bf16 a[108:111],a[220:223],v[244:247],0
v_accvgpr_read_b32 v120,a112
v_accvgpr_read_b32 v121,a113
v_mfma_f32_16x16x32_bf16 a[108:111],a[216:219],v[228:231],a[108:111]
buffer_load_dwordx4 v4,s[24:27],s47 offen offset:2048 nt lds
v_accvgpr_read_b32 v122,a114
v_accvgpr_read_b32 v123,a115
v_mfma_f32_16x16x32_bf16 a[112:115],a[224:227],v[228:231],0
v_mul_f32 v110,v22,v110
v_mul_f32 v111,v22,v111
v_mul_f32 v120,v23,v120
v_mul_f32 v121,v23,v121
v_mul_f32 v12,v22,v12
v_mul_f32 v13,v22,v13
v_cvt_pk_bf16_f32 v110,v110,v111
v_cvt_pk_bf16_f32 v120,v120,v121
v_mul_f32 v122,v23,v122
v_mul_f32 v123,v23,v123
v_mfma_f32_16x16x32_bf16 a[112:115],a[228:231],v[244:247],a[112:115]
v_cvt_pk_bf16_f32 v111,v12,v13
v_cvt_pk_bf16_f32 v121,v122,v123
v_accvgpr_read_b32 v12,a118
v_accvgpr_read_b32 v13,a119
v_accvgpr_read_b32 v122,a116
v_accvgpr_read_b32 v123,a117
v_mfma_f32_16x16x32_bf16 a[116:119],a[236:239],v[244:247],0
v_accvgpr_read_b32 v124,a120
v_accvgpr_read_b32 v125,a121
v_mfma_f32_16x16x32_bf16 a[116:119],a[232:235],v[228:231],a[116:119]
v_accvgpr_read_b32 v126,a122
v_accvgpr_read_b32 v127,a123
v_mfma_f32_16x16x32_bf16 a[120:123],a[240:243],v[228:231],0
v_mul_f32 v114,v23,v114
v_mul_f32 v115,v23,v115
v_mul_f32 v124,v23,v124
v_mul_f32 v125,v23,v125
v_mul_f32 v14,v23,v14
v_mul_f32 v15,v23,v15
v_cvt_pk_bf16_f32 v114,v114,v115
v_cvt_pk_bf16_f32 v124,v124,v125
v_mul_f32 v126,v23,v126
v_mul_f32 v127,v23,v127
v_mfma_f32_16x16x32_bf16 a[120:123],a[244:247],v[244:247],a[120:123]
v_cvt_pk_bf16_f32 v115,v14,v15
v_cvt_pk_bf16_f32 v125,v126,v127
v_accvgpr_read_b32 v14,a126
v_accvgpr_read_b32 v15,a127
v_accvgpr_read_b32 v126,a124
v_accvgpr_read_b32 v127,a125
v_mfma_f32_16x16x32_bf16 a[124:127],a[252:255],v[244:247],0
v_cmp_nlt_f32 s[58:59],v60,s63
s_nop 1
s_or_b64 s[56:57],s[56:57],s[58:59]
v_mfma_f32_16x16x32_bf16 a[124:127],a[248:251],v[228:231],a[124:127]
v_add_u32 v2,s48,v2
v_add_u32 v3,s48,v3
v_mfma_f32_16x16x32_bf16 v[60:63],v[28:31],v[228:231],0
ds_read_b64_tr_b16 a[192:193],v2
ds_read_b64_tr_b16 a[194:195],v2 offset:4096
v_mfma_f32_16x16x32_bf16 v[60:63],v[28:31],v[244:247],v[60:63]
ds_read_b64_tr_b16 a[196:197],v2 offset:8192
ds_read_b64_tr_b16 a[198:199],v2 offset:12288
v_mfma_f32_16x16x32_bf16 v[196:199],a[128:131],v[144:147],v[40:43]
buffer_load_dwordx4 v5,s[24:27],s47 offen offset:3072 nt lds
v_mfma_f32_16x16x32_bf16 v[196:199],a[132:135],v[176:179],v[196:199]
s_add_u32 s47,s47,16384
ds_read_b64_tr_b16 a[204:205],v3 offset:8192
ds_read_b64_tr_b16 a[206:207],v3 offset:12288
v_mfma_f32_16x16x32_bf16 v[196:199],a[136:139],v[208:211],v[196:199]
s_and_b32 s47,s47,0x3ffff
ds_read_b64_tr_b16 a[200:201],v3
ds_read_b64_tr_b16 a[202:203],v3 offset:4096
v_mfma_f32_16x16x32_bf16 v[196:199],a[140:143],v[240:243],v[196:199]
s_or_b32 s47,s47,s46
v_exp_f32 v132,v132
v_mfma_f32_16x16x32_bf16 v[204:207],a[156:159],v[240:243],v[40:43]
v_exp_f32 v133,v133
v_mfma_f32_16x16x32_bf16 v[204:207],a[152:155],v[208:211],v[204:207]
ds_read_b64_tr_b16 a[208:209],v2 offset:256
ds_read_b64_tr_b16 a[210:211],v2 offset:4352
v_mfma_f32_16x16x32_bf16 v[204:207],a[148:151],v[176:179],v[204:207]
v_exp_f32 v134,v134
v_mfma_f32_16x16x32_bf16 v[204:207],a[144:147],v[144:147],v[204:207]
v_exp_f32 v135,v135
v_mfma_f32_16x16x32_bf16 v[212:215],a[160:163],v[144:147],v[40:43]
ds_read_b64_tr_b16 a[212:213],v2 offset:8448
v_cvt_pk_bf16_f32 v132,v132,v133
v_mfma_f32_16x16x32_bf16 v[212:215],a[164:167],v[176:179],v[212:215]
v_exp_f32 v140,v140
v_mfma_f32_16x16x32_bf16 v[212:215],a[168:171],v[208:211],v[212:215]
v_exp_f32 v141,v141
v_mfma_f32_16x16x32_bf16 v[212:215],a[172:175],v[240:243],v[212:215]
v_exp_f32 v142,v142
v_mfma_f32_16x16x32_bf16 v[220:223],a[188:191],v[240:243],v[40:43]
ds_read_b64_tr_b16 a[214:215],v2 offset:12544
v_exp_f32 v143,v143
v_exp_f32 v148,v148
v_exp_f32 v149,v149
v_mfma_f32_16x16x32_bf16 v[220:223],a[184:187],v[208:211],v[220:223]
ds_read_b64_tr_b16 a[220:221],v3 offset:8448
ds_read_b64_tr_b16 a[222:223],v3 offset:12544
v_cvt_pk_bf16_f32 v133,v134,v135
v_exp_f32 v150,v150
v_exp_f32 v151,v151
v_cvt_pk_bf16_f32 v134,v140,v141
v_cvt_pk_bf16_f32 v135,v142,v143
v_cvt_pk_bf16_f32 v148,v148,v149
v_exp_f32 v156,v156
v_exp_f32 v157,v157
)A"
R"A(v_exp_f32 v158,v158
v_exp_f32 v159,v159
v_mfma_f32_16x16x32_bf16 v[220:223],a[180:183],v[176:179],v[220:223]
ds_read_b64_tr_b16 a[216:217],v3 offset:256
ds_read_b64_tr_b16 a[218:219],v3 offset:4352
v_cvt_pk_bf16_f32 v149,v150,v151
v_cvt_pk_bf16_f32 v150,v156,v157
v_cvt_pk_bf16_f32 v151,v158,v159
v_mfma_f32_16x16x32_bf16 v[220:223],a[176:179],v[144:147],v[220:223]
s_waitcnt lgkmcnt(6)
ds_read_b64_tr_b16 a[224:225],v2 offset:512
ds_read_b64_tr_b16 a[226:227],v2 offset:4608
v_mfma_f32_16x16x32_bf16 a[0:3],a[192:195],v[132:135],a[0:3]
ds_read_b64_tr_b16 a[228:229],v2 offset:8704
ds_read_b64_tr_b16 a[230:231],v2 offset:12800
v_mfma_f32_16x16x32_bf16 a[0:3],a[196:199],v[148:151],a[0:3]
ds_read_b64_tr_b16 a[236:237],v3 offset:8704
ds_read_b64_tr_b16 a[238:239],v3 offset:12800
v_mfma_f32_16x16x32_bf16 a[4:7],a[204:207],v[148:151],a[4:7]
ds_read_b64_tr_b16 a[232:233],v3 offset:512
ds_read_b64_tr_b16 a[234:235],v3 offset:4608
v_mfma_f32_16x16x32_bf16 a[4:7],a[200:203],v[132:135],a[4:7]
ds_read_b64_tr_b16 a[240:241],v2 offset:768
ds_read_b64_tr_b16 a[242:243],v2 offset:4864
v_mfma_f32_16x16x32_bf16 a[8:11],a[208:211],v[132:135],a[8:11]
s_memrealtime s[76:77]
ds_read_b64_tr_b16 a[244:245],v2 offset:8960
ds_read_b64_tr_b16 a[246:247],v2 offset:13056
s_waitcnt lgkmcnt(15)
v_mfma_f32_16x16x32_bf16 a[8:11],a[212:215],v[148:151],a[8:11]
s_waitcnt lgkmcnt(14)
ds_read_b64_tr_b16 a[252:253],v3 offset:8960
ds_read_b64_tr_b16 a[254:255],v3 offset:13056
v_mfma_f32_16x16x32_bf16 a[12:15],a[220:223],v[148:151],a[12:15]
s_waitcnt lgkmcnt(14)
ds_read_b64_tr_b16 a[248:249],v3 offset:768
ds_read_b64_tr_b16 a[250:251],v3 offset:4864
v_mfma_f32_16x16x32_bf16 a[12:15],a[216:219],v[132:135],a[12:15]
s_waitcnt lgkmcnt(14)
v_mul_f32 v118,v23,v118
v_mul_f32 v119,v23,v119
v_mfma_f32_16x16x32_bf16 a[16:19],a[224:227],v[132:135],a[16:19]
s_waitcnt lgkmcnt(12)
v_mul_f32 v122,v23,v122
v_mul_f32 v123,v23,v123
v_mfma_f32_16x16x32_bf16 a[16:19],a[228:231],v[148:151],a[16:19]
s_waitcnt lgkmcnt(10)
v_mul_f32 v126,v23,v126
v_mul_f32 v127,v23,v127
v_mfma_f32_16x16x32_bf16 a[20:23],a[236:239],v[148:151],a[20:23]
s_waitcnt lgkmcnt(8)
v_exp_f32 v164,v164
v_mfma_f32_16x16x32_bf16 a[20:23],a[232:235],v[132:135],a[20:23]
s_waitcnt lgkmcnt(0)
v_exp_f32 v165,v165
v_mfma_f32_16x16x32_bf16 a[24:27],a[240:243],v[132:135],a[24:27]
s_sub_u32 s79,s78,s76
v_exp_f32 v180,v180
v_mfma_f32_16x16x32_bf16 a[24:27],a[244:247],v[148:151],a[24:27]
s_add_u32 s78,s76,s60
v_exp_f32 v181,v181
v_mfma_f32_16x16x32_bf16 a[28:31],a[252:255],v[148:151],a[28:31]
s_cmp_gt_i32 s79,0
s_cbranch_scc1 L_ps5_%=
L_pr5_%=:
v_mul_f32 v16,v23,v16
v_mfma_f32_16x16x32_bf16 a[28:31],a[248:251],v[132:135],a[28:31]
s_cmp_eq_u32 s50,131072
s_cselect_b32 s62,163840,0
v_mul_f32 v17,v23,v17
v_cvt_pk_bf16_f32 v118,v118,v119
v_mul_f32 v12,v23,v12
v_mul_f32 v13,v23,v13
v_cvt_pk_bf16_f32 v122,v122,v123
v_mul_f32 v14,v23,v14
v_mul_f32 v15,v23,v15
v_cvt_pk_bf16_f32 v126,v126,v127
v_exp_f32 v166,v166
v_exp_f32 v167,v167
v_exp_f32 v182,v182
v_exp_f32 v183,v183
v_cvt_pk_bf16_f32 v119,v16,v17
v_cvt_pk_bf16_f32 v123,v12,v13
v_cvt_pk_bf16_f32 v127,v14,v15
v_cvt_pk_bf16_f32 v164,v164,v165
v_exp_f32 v172,v172
v_exp_f32 v173,v173
v_exp_f32 v174,v174
v_exp_f32 v175,v175
v_cvt_pk_bf16_f32 v180,v180,v181
v_exp_f32 v188,v188
v_exp_f32 v189,v189
v_exp_f32 v190,v190
v_exp_f32 v191,v191
v_mfma_f32_16x16x32_bf16 v[48:51],v[28:31],v[132:135],v[48:51]
s_sub_u32 s48,32768,s62
s_cmp_lg_u64 s[56:57],0
s_cbranch_scc1 L_slow0_%=
L_sret0_%=:
s_cmp_eq_u32 s49,163840
s_cselect_b32 s49,0,s49
s_add_u32 s50,s50,s48
v_cvt_pk_bf16_f32 v165,v166,v167
v_cvt_pk_bf16_f32 v181,v182,v183
v_add_u32 v1,s48,v1
v_cvt_pk_bf16_f32 v166,v172,v173
v_cvt_pk_bf16_f32 v167,v174,v175
v_cvt_pk_bf16_f32 v182,v188,v189
v_cvt_pk_bf16_f32 v183,v190,v191
v_mfma_f32_16x16x32_bf16 v[48:51],v[28:31],v[148:151],v[48:51]
s_mov_b32 s53,11
L_att1_%=:
v_mfma_f32_16x16x32_bf16 v[228:231],a[128:131],v[152:155],v[44:47]
s_waitcnt vmcnt(8) lgkmcnt(0)
v_exp_f32 v196,v196
v_mfma_f32_16x16x32_bf16 v[228:231],a[132:135],v[184:187],v[228:231]
s_barrier
s_add_u32 m0,s49,s51
ds_read_b128 a[128:131],v1
v_mfma_f32_16x16x32_bf16 v[228:231],a[136:139],v[216:219],v[228:231]
s_mov_b32 s90,L_fast_end_%=-L_pc_%=+128
v_exp_f32 v197,v197
v_mfma_f32_16x16x32_bf16 v[228:231],a[140:143],v[248:251],v[228:231]
s_add_u32 s54,s54,16384
ds_read_b128 a[132:135],v1 offset:256
v_cvt_pk_bf16_f32 v196,v196,v197
v_mfma_f32_16x16x32_bf16 v[236:239],a[156:159],v[248:251],v[44:47]
s_sub_u32 s53,s53,1
buffer_load_dwordx4 v4,s[20:23],s47 offen nt lds
v_mfma_f32_16x16x32_bf16 v[236:239],a[152:155],v[216:219],v[236:239]
s_and_b32 s46,s54,0xfffc0000
ds_read_b128 a[136:139],v1 offset:512
v_add_u32 v2,s48,v2
v_mfma_f32_16x16x32_bf16 v[236:239],a[148:151],v[184:187],v[236:239]
v_exp_f32 v198,v198
v_mfma_f32_16x16x32_bf16 v[236:239],a[144:147],v[152:155],v[236:239]
ds_read_b128 a[140:143],v1 offset:768
v_add_u32 v3,s48,v3
v_mfma_f32_16x16x32_bf16 v[244:247],a[160:163],v[152:155],v[44:47]
v_exp_f32 v199,v199
v_mfma_f32_16x16x32_bf16 v[244:247],a[164:167],v[184:187],v[244:247]
ds_read_b128 a[156:159],v1 offset:4864
v_cvt_pk_bf16_f32 v197,v198,v199
v_mfma_f32_16x16x32_bf16 v[244:247],a[168:171],v[216:219],v[244:247]
v_exp_f32 v204,v204
v_mfma_f32_16x16x32_bf16 v[244:247],a[172:175],v[248:251],v[244:247]
s_cmp_eq_u32 s50,131072
s_cselect_b32 s62,163840,0
ds_read_b128 a[152:155],v1 offset:4608
v_mfma_f32_16x16x32_bf16 v[252:255],a[188:191],v[248:251],v[44:47]
s_sub_u32 s48,32768,s62
v_exp_f32 v205,v205
v_mfma_f32_16x16x32_bf16 v[252:255],a[184:187],v[216:219],v[252:255]
s_add_u32 s50,s50,s48
ds_read_b128 a[148:151],v1 offset:4352
v_cvt_pk_bf16_f32 v198,v204,v205
v_mfma_f32_16x16x32_bf16 v[252:255],a[180:183],v[184:187],v[252:255]
v_exp_f32 v206,v206
v_mfma_f32_16x16x32_bf16 v[252:255],a[176:179],v[152:155],v[252:255]
ds_read_b128 a[144:147],v1 offset:4096
v_mfma_f32_16x16x32_bf16 a[32:35],a[192:195],v[164:167],a[32:35]
v_exp_f32 v207,v207
v_mfma_f32_16x16x32_bf16 a[32:35],a[196:199],v[180:183],a[32:35]
ds_read_b128 a[160:163],v1 offset:8192
v_cvt_pk_bf16_f32 v199,v206,v207
v_mfma_f32_16x16x32_bf16 a[36:39],a[204:207],v[180:183],a[36:39]
buffer_load_dwordx4 v4,s[20:23],s47 offen offset:1024 nt lds
v_mfma_f32_16x16x32_bf16 a[36:39],a[200:203],v[164:167],a[36:39]
ds_read_b128 a[164:167],v1 offset:8448
v_mfma_f32_16x16x32_bf16 a[40:43],a[208:211],v[164:167],a[40:43]
v_exp_f32 v212,v212
v_mfma_f32_16x16x32_bf16 a[40:43],a[212:215],v[180:183],a[40:43]
ds_read_b128 a[168:171],v1 offset:8704
v_mfma_f32_16x16x32_bf16 a[44:47],a[220:223],v[180:183],a[44:47]
v_exp_f32 v213,v213
v_mfma_f32_16x16x32_bf16 a[44:47],a[216:219],v[164:167],a[44:47]
ds_read_b128 a[172:175],v1 offset:8960
v_cvt_pk_bf16_f32 v212,v212,v213
v_mfma_f32_16x16x32_bf16 a[48:51],a[224:227],v[164:167],a[48:51]
v_exp_f32 v214,v214
v_mfma_f32_16x16x32_bf16 a[48:51],a[228:231],v[180:183],a[48:51]
ds_read_b128 a[188:191],v1 offset:13056
v_mfma_f32_16x16x32_bf16 a[52:55],a[236:239],v[180:183],a[52:55]
v_exp_f32 v215,v215
v_mfma_f32_16x16x32_bf16 a[52:55],a[232:235],v[164:167],a[52:55]
ds_read_b128 a[184:187],v1 offset:12800
v_cvt_pk_bf16_f32 v213,v214,v215
v_mfma_f32_16x16x32_bf16 a[56:59],a[240:243],v[164:167],a[56:59]
v_exp_f32 v220,v220
v_mfma_f32_16x16x32_bf16 a[56:59],a[244:247],v[180:183],a[56:59]
ds_read_b128 a[180:183],v1 offset:12544
v_mfma_f32_16x16x32_bf16 a[60:63],a[252:255],v[180:183],a[60:63]
v_exp_f32 v221,v221
v_mfma_f32_16x16x32_bf16 a[60:63],a[248:251],v[164:167],a[60:63]
ds_read_b128 a[176:179],v1 offset:12288
v_cvt_pk_bf16_f32 v214,v220,v221
v_mfma_f32_16x16x32_bf16 v[52:55],v[28:31],v[164:167],v[52:55]
buffer_load_dwordx4 v5,s[20:23],s47 offen offset:2048 nt lds
v_mfma_f32_16x16x32_bf16 v[52:55],v[28:31],v[180:183],v[52:55]
s_waitcnt lgkmcnt(3)
v_exp_f32 v222,v222
v_mfma_f32_16x16x32_bf16 v[132:135],a[128:131],v[128:131],v[32:35]
v_exp_f32 v223,v223
v_mfma_f32_16x16x32_bf16 v[132:135],a[132:135],v[160:163],v[132:135]
v_cvt_pk_bf16_f32 v215,v222,v223
v_add_u32 v1,s48,v1
v_mfma_f32_16x16x32_bf16 v[132:135],a[136:139],v[192:195],v[132:135]
v_exp_f32 v228,v228
v_mfma_f32_16x16x32_bf16 v[132:135],a[140:143],v[224:227],v[132:135]
v_exp_f32 v229,v229
v_mfma_f32_16x16x32_bf16 v[140:143],a[156:159],v[224:227],v[32:35]
v_exp_f32 v230,v230
v_mfma_f32_16x16x32_bf16 v[140:143],a[152:155],v[192:195],v[140:143]
v_exp_f32 v231,v231
v_mfma_f32_16x16x32_bf16 v[140:143],a[148:151],v[160:163],v[140:143]
v_cvt_pk_bf16_f32 v228,v228,v229
v_cvt_pk_bf16_f32 v229,v230,v231
v_mfma_f32_16x16x32_bf16 v[140:143],a[144:147],v[128:131],v[140:143]
v_exp_f32 v236,v236
v_mfma_f32_16x16x32_bf16 v[148:151],a[160:163],v[128:131],v[32:35]
v_exp_f32 v237,v237
v_mfma_f32_16x16x32_bf16 v[148:151],a[164:167],v[160:163],v[148:151]
v_exp_f32 v238,v238
v_mfma_f32_16x16x32_bf16 v[148:151],a[168:171],v[192:195],v[148:151]
v_exp_f32 v239,v239
v_mfma_f32_16x16x32_bf16 v[148:151],a[172:175],v[224:227],v[148:151]
v_exp_f32 v244,v244
v_mfma_f32_16x16x32_bf16 v[156:159],a[188:191],v[224:227],v[32:35]
s_waitcnt lgkmcnt(0)
buffer_load_dwordx4 v5,s[20:23],s47 offen offset:3072 nt lds
v_mfma_f32_16x16x32_bf16 v[156:159],a[184:187],v[192:195],v[156:159]
v_exp_f32 v245,v245
v_mfma_f32_16x16x32_bf16 v[156:159],a[180:183],v[160:163],v[156:159]
v_exp_f32 v246,v246
v_mfma_f32_16x16x32_bf16 v[156:159],a[176:179],v[128:131],v[156:159]
v_exp_f32 v247,v247
v_mfma_f32_16x16x32_bf16 a[64:67],a[192:195],v[196:199],a[64:67]
v_cvt_pk_bf16_f32 v230,v236,v237
v_cvt_pk_bf16_f32 v231,v238,v239
v_mfma_f32_16x16x32_bf16 a[64:67],a[196:199],v[212:215],a[64:67]
v_cvt_pk_bf16_f32 v244,v244,v245
v_cvt_pk_bf16_f32 v245,v246,v247
v_mfma_f32_16x16x32_bf16 a[68:71],a[204:207],v[212:215],a[68:71]
v_exp_f32 v252,v252
v_mfma_f32_16x16x32_bf16 a[68:71],a[200:203],v[196:199],a[68:71]
v_exp_f32 v253,v253
v_mfma_f32_16x16x32_bf16 a[72:75],a[208:211],v[196:199],a[72:75]
v_exp_f32 v254,v254
v_mfma_f32_16x16x32_bf16 a[72:75],a[212:215],v[212:215],a[72:75]
v_exp_f32 v255,v255
v_mfma_f32_16x16x32_bf16 a[76:79],a[220:223],v[212:215],a[76:79]
v_cvt_pk_bf16_f32 v246,v252,v253
v_cvt_pk_bf16_f32 v247,v254,v255
v_mfma_f32_16x16x32_bf16 a[76:79],a[216:219],v[196:199],a[76:79]
s_add_u32 m0,s49,s52
v_exp_f32 v132,v132
v_mfma_f32_16x16x32_bf16 a[80:83],a[224:227],v[196:199],a[80:83]
s_add_u32 s49,s49,32768
v_exp_f32 v133,v133
v_mfma_f32_16x16x32_bf16 a[80:83],a[228:231],v[212:215],a[80:83]
v_exp_f32 v134,v134
v_mfma_f32_16x16x32_bf16 a[84:87],a[236:239],v[212:215],a[84:87]
buffer_load_dwordx4 v4,s[24:27],s47 offen nt lds
v_mfma_f32_16x16x32_bf16 a[84:87],a[232:235],v[196:199],a[84:87]
v_exp_f32 v135,v135
v_mfma_f32_16x16x32_bf16 a[88:91],a[240:243],v[196:199],a[88:91]
v_cvt_pk_bf16_f32 v132,v132,v133
v_cvt_pk_bf16_f32 v133,v134,v135
v_mfma_f32_16x16x32_bf16 a[88:91],a[244:247],v[212:215],a[88:91]
v_exp_f32 v140,v140
v_mfma_f32_16x16x32_bf16 a[92:95],a[252:255],v[212:215],a[92:95]
v_exp_f32 v141,v141
v_mfma_f32_16x16x32_bf16 a[92:95],a[248:251],v[196:199],a[92:95]
v_exp_f32 v142,v142
v_mfma_f32_16x16x32_bf16 v[56:59],v[28:31],v[196:199],v[56:59]
v_exp_f32 v143,v143
v_mfma_f32_16x16x32_bf16 v[56:59],v[28:31],v[212:215],v[56:59]
v_exp_f32 v148,v148
v_mfma_f32_16x16x32_bf16 v[164:167],a[128:131],v[136:139],v[36:39]
v_exp_f32 v149,v149
v_mfma_f32_16x16x32_bf16 v[164:167],a[132:135],v[168:171],v[164:167]
v_exp_f32 v150,v150
v_mfma_f32_16x16x32_bf16 v[164:167],a[136:139],v[200:203],v[164:167]
v_exp_f32 v151,v151
v_mfma_f32_16x16x32_bf16 v[164:167],a[140:143],v[232:235],v[164:167]
v_cvt_pk_bf16_f32 v134,v140,v141
v_cvt_pk_bf16_f32 v135,v142,v143
v_mfma_f32_16x16x32_bf16 v[172:175],a[156:159],v[232:235],v[36:39]
v_cvt_pk_bf16_f32 v148,v148,v149
v_cvt_pk_bf16_f32 v149,v150,v151
v_mfma_f32_16x16x32_bf16 v[172:175],a[152:155],v[200:203],v[172:175]
v_exp_f32 v156,v156
v_mfma_f32_16x16x32_bf16 v[172:175],a[148:151],v[168:171],v[172:175]
buffer_load_dwordx4 v5,s[24:27],s47 offen offset:1024 nt lds
v_mfma_f32_16x16x32_bf16 v[172:175],a[144:147],v[136:139],v[172:175]
v_exp_f32 v157,v157
v_mfma_f32_16x16x32_bf16 v[180:183],a[160:163],v[136:139],v[36:39]
)A"
R"A(v_exp_f32 v158,v158
v_mfma_f32_16x16x32_bf16 v[180:183],a[164:167],v[168:171],v[180:183]
v_exp_f32 v159,v159
v_mfma_f32_16x16x32_bf16 v[180:183],a[168:171],v[200:203],v[180:183]
v_cvt_pk_bf16_f32 v150,v156,v157
v_cvt_pk_bf16_f32 v151,v158,v159
v_mfma_f32_16x16x32_bf16 v[180:183],a[172:175],v[232:235],v[180:183]
v_exp_f32 v164,v164
v_mfma_f32_16x16x32_bf16 v[188:191],a[188:191],v[232:235],v[36:39]
v_exp_f32 v165,v165
v_mfma_f32_16x16x32_bf16 v[188:191],a[184:187],v[200:203],v[188:191]
v_exp_f32 v166,v166
v_mfma_f32_16x16x32_bf16 v[188:191],a[180:183],v[168:171],v[188:191]
v_exp_f32 v167,v167
v_mfma_f32_16x16x32_bf16 v[188:191],a[176:179],v[136:139],v[188:191]
v_cvt_pk_bf16_f32 v164,v164,v165
v_cvt_pk_bf16_f32 v165,v166,v167
v_mfma_f32_16x16x32_bf16 a[96:99],a[192:195],v[228:231],a[96:99]
s_cmp_eq_u32 s49,163840
s_cselect_b32 s49,0,s49
ds_read_b64_tr_b16 a[192:193],v2
v_mfma_f32_16x16x32_bf16 a[96:99],a[196:199],v[244:247],a[96:99]
ds_read_b64_tr_b16 a[194:195],v2 offset:4096
v_mfma_f32_16x16x32_bf16 a[100:103],a[204:207],v[244:247],a[100:103]
ds_read_b64_tr_b16 a[196:197],v2 offset:8192
v_mfma_f32_16x16x32_bf16 a[100:103],a[200:203],v[228:231],a[100:103]
ds_read_b64_tr_b16 a[198:199],v2 offset:12288
v_mfma_f32_16x16x32_bf16 a[104:107],a[208:211],v[228:231],a[104:107]
buffer_load_dwordx4 v4,s[24:27],s47 offen offset:2048 nt lds
v_mfma_f32_16x16x32_bf16 a[104:107],a[212:215],v[244:247],a[104:107]
ds_read_b64_tr_b16 a[204:205],v3 offset:8192
v_mfma_f32_16x16x32_bf16 a[108:111],a[220:223],v[244:247],a[108:111]
ds_read_b64_tr_b16 a[206:207],v3 offset:12288
v_mfma_f32_16x16x32_bf16 a[108:111],a[216:219],v[228:231],a[108:111]
ds_read_b64_tr_b16 a[200:201],v3
v_mfma_f32_16x16x32_bf16 a[112:115],a[224:227],v[228:231],a[112:115]
ds_read_b64_tr_b16 a[202:203],v3 offset:4096
v_mfma_f32_16x16x32_bf16 a[112:115],a[228:231],v[244:247],a[112:115]
ds_read_b64_tr_b16 a[208:209],v2 offset:256
v_mfma_f32_16x16x32_bf16 a[116:119],a[236:239],v[244:247],a[116:119]
ds_read_b64_tr_b16 a[210:211],v2 offset:4352
v_mfma_f32_16x16x32_bf16 a[116:119],a[232:235],v[228:231],a[116:119]
ds_read_b64_tr_b16 a[212:213],v2 offset:8448
v_mfma_f32_16x16x32_bf16 a[120:123],a[240:243],v[228:231],a[120:123]
ds_read_b64_tr_b16 a[214:215],v2 offset:12544
v_mfma_f32_16x16x32_bf16 a[120:123],a[244:247],v[244:247],a[120:123]
ds_read_b64_tr_b16 a[220:221],v3 offset:8448
v_mfma_f32_16x16x32_bf16 a[124:127],a[252:255],v[244:247],a[124:127]
ds_read_b64_tr_b16 a[222:223],v3 offset:12544
v_mfma_f32_16x16x32_bf16 a[124:127],a[248:251],v[228:231],a[124:127]
ds_read_b64_tr_b16 a[216:217],v3 offset:256
v_mfma_f32_16x16x32_bf16 v[60:63],v[28:31],v[228:231],v[60:63]
ds_read_b64_tr_b16 a[218:219],v3 offset:4352
v_mfma_f32_16x16x32_bf16 v[60:63],v[28:31],v[244:247],v[60:63]
ds_read_b64_tr_b16 a[224:225],v2 offset:512
v_mfma_f32_16x16x32_bf16 v[196:199],a[128:131],v[144:147],v[40:43]
buffer_load_dwordx4 v5,s[24:27],s47 offen offset:3072 nt lds
v_mfma_f32_16x16x32_bf16 v[196:199],a[132:135],v[176:179],v[196:199]
s_add_u32 s47,s47,16384
s_and_b32 s47,s47,0x3ffff
ds_read_b64_tr_b16 a[226:227],v2 offset:4608
v_mfma_f32_16x16x32_bf16 v[196:199],a[136:139],v[208:211],v[196:199]
s_or_b32 s47,s47,s46
ds_read_b64_tr_b16 a[228:229],v2 offset:8704
v_mfma_f32_16x16x32_bf16 v[196:199],a[140:143],v[240:243],v[196:199]
ds_read_b64_tr_b16 a[230:231],v2 offset:12800
v_mfma_f32_16x16x32_bf16 v[204:207],a[156:159],v[240:243],v[40:43]
ds_read_b64_tr_b16 a[236:237],v3 offset:8704
v_mfma_f32_16x16x32_bf16 v[204:207],a[152:155],v[208:211],v[204:207]
ds_read_b64_tr_b16 a[238:239],v3 offset:12800
v_mfma_f32_16x16x32_bf16 v[204:207],a[148:151],v[176:179],v[204:207]
ds_read_b64_tr_b16 a[232:233],v3 offset:512
v_mfma_f32_16x16x32_bf16 v[204:207],a[144:147],v[144:147],v[204:207]
ds_read_b64_tr_b16 a[234:235],v3 offset:4608
v_mfma_f32_16x16x32_bf16 v[212:215],a[160:163],v[144:147],v[40:43]
ds_read_b64_tr_b16 a[240:241],v2 offset:768
v_mfma_f32_16x16x32_bf16 v[212:215],a[164:167],v[176:179],v[212:215]
ds_read_b64_tr_b16 a[242:243],v2 offset:4864
v_mfma_f32_16x16x32_bf16 v[212:215],a[168:171],v[208:211],v[212:215]
ds_read_b64_tr_b16 a[244:245],v2 offset:8960
v_mfma_f32_16x16x32_bf16 v[212:215],a[172:175],v[240:243],v[212:215]
ds_read_b64_tr_b16 a[246:247],v2 offset:13056
v_mfma_f32_16x16x32_bf16 v[220:223],a[188:191],v[240:243],v[40:43]
ds_read_b64_tr_b16 a[252:253],v3 offset:8960
v_mfma_f32_16x16x32_bf16 v[220:223],a[184:187],v[208:211],v[220:223]
ds_read_b64_tr_b16 a[254:255],v3 offset:13056
v_mfma_f32_16x16x32_bf16 v[220:223],a[180:183],v[176:179],v[220:223]
ds_read_b64_tr_b16 a[248:249],v3 offset:768
v_mfma_f32_16x16x32_bf16 v[220:223],a[176:179],v[144:147],v[220:223]
s_waitcnt lgkmcnt(7)
ds_read_b64_tr_b16 a[250:251],v3 offset:4864
v_mfma_f32_16x16x32_bf16 a[0:3],a[192:195],v[132:135],a[0:3]
buffer_load_dword v26,v24,s[88:91],s93 offen
v_mfma_f32_16x16x32_bf16 a[0:3],a[196:199],v[148:151],a[0:3]
v_exp_f32 v180,v180
v_mfma_f32_16x16x32_bf16 a[4:7],a[204:207],v[148:151],a[4:7]
v_exp_f32 v181,v181
v_mfma_f32_16x16x32_bf16 a[4:7],a[200:203],v[132:135],a[4:7]
v_exp_f32 v182,v182
v_mfma_f32_16x16x32_bf16 a[8:11],a[208:211],v[132:135],a[8:11]
s_memrealtime s[76:77]
v_exp_f32 v183,v183
v_mfma_f32_16x16x32_bf16 a[8:11],a[212:215],v[148:151],a[8:11]
v_exp_f32 v172,v172
v_mfma_f32_16x16x32_bf16 a[12:15],a[220:223],v[148:151],a[12:15]
v_exp_f32 v173,v173
v_mfma_f32_16x16x32_bf16 a[12:15],a[216:219],v[132:135],a[12:15]
v_exp_f32 v174,v174
v_mfma_f32_16x16x32_bf16 a[16:19],a[224:227],v[132:135],a[16:19]
v_exp_f32 v175,v175
v_mfma_f32_16x16x32_bf16 a[16:19],a[228:231],v[148:151],a[16:19]
v_cvt_pk_bf16_f32 v180,v180,v181
v_cvt_pk_bf16_f32 v181,v182,v183
v_mfma_f32_16x16x32_bf16 a[20:23],a[236:239],v[148:151],a[20:23]
v_exp_f32 v188,v188
v_mfma_f32_16x16x32_bf16 a[20:23],a[232:235],v[132:135],a[20:23]
s_waitcnt lgkmcnt(0)
v_exp_f32 v189,v189
v_mfma_f32_16x16x32_bf16 a[24:27],a[240:243],v[132:135],a[24:27]
s_sub_u32 s79,s78,s76
v_exp_f32 v190,v190
v_mfma_f32_16x16x32_bf16 a[24:27],a[244:247],v[148:151],a[24:27]
s_add_u32 s78,s76,s60
v_exp_f32 v191,v191
v_mfma_f32_16x16x32_bf16 a[28:31],a[252:255],v[148:151],a[28:31]
s_cmp_gt_i32 s79,0
s_cbranch_scc1 L_ps6_%=
L_pr6_%=:
v_cvt_pk_bf16_f32 v166,v172,v173
v_mfma_f32_16x16x32_bf16 a[28:31],a[248:251],v[132:135],a[28:31]
v_cvt_pk_bf16_f32 v167,v174,v175
v_cvt_pk_bf16_f32 v182,v188,v189
v_mfma_f32_16x16x32_bf16 v[48:51],v[28:31],v[132:135],v[48:51]
v_cvt_pk_bf16_f32 v183,v190,v191
s_cmp_lg_u32 s53,0
v_mfma_f32_16x16x32_bf16 v[48:51],v[28:31],v[148:151],v[48:51]
s_cbranch_scc1 L_att1_%=
L_endseg_%=:
v_mfma_f32_16x16x32_bf16 v[228:231],a[128:131],v[152:155],v[44:47]
s_waitcnt vmcnt(10)
s_add_u32 s80,s71,0
s_and_b32 s80,s80,3
s_lshl_b32 s80,s80,1
v_mfma_f32_16x16x32_bf16 v[228:231],a[132:135],v[184:187],v[228:231]
s_barrier
s_add_u32 m0,s49,s51
s_mul_i32 s80,s80,163840
v_mfma_f32_16x16x32_bf16 v[228:231],a[136:139],v[216:219],v[228:231]
s_add_u32 s80,s80,s81
s_add_u32 s83,s80,20480
ds_read_b128 a[128:131],v1
v_mfma_f32_16x16x32_bf16 v[228:231],a[140:143],v[248:251],v[228:231]
s_lshl_b32 s43,s41,2
v_exp_f32 v196,v196
v_mfma_f32_16x16x32_bf16 v[236:239],a[156:159],v[248:251],v[44:47]
s_add_u32 s43,s43,s70
buffer_load_dwordx4 v4,s[20:23],s47 offen nt lds
v_mfma_f32_16x16x32_bf16 v[236:239],a[152:155],v[216:219],v[236:239]
s_lshl_b32 s43,s43,19
ds_read_b128 a[132:135],v1 offset:256
v_add_u32 v2,s48,v2
v_mfma_f32_16x16x32_bf16 v[236:239],a[148:151],v[184:187],v[236:239]
s_add_u32 s43,s43,s55
v_exp_f32 v197,v197
v_mfma_f32_16x16x32_bf16 v[236:239],a[144:147],v[152:155],v[236:239]
s_mov_b64 s[28:29],s[12:13]
ds_read_b128 a[136:139],v1 offset:512
v_cvt_pk_bf16_f32 v196,v196,v197
v_mfma_f32_16x16x32_bf16 v[244:247],a[160:163],v[152:155],v[44:47]
s_mov_b32 s30,475136
v_exp_f32 v198,v198
v_mfma_f32_16x16x32_bf16 v[244:247],a[164:167],v[184:187],v[244:247]
s_mov_b32 s31,0x20000
ds_read_b128 a[140:143],v1 offset:768
v_add_u32 v3,s48,v3
v_mfma_f32_16x16x32_bf16 v[244:247],a[168:171],v[216:219],v[244:247]
s_add_u32 s54,s54,16384
v_exp_f32 v199,v199
v_mfma_f32_16x16x32_bf16 v[244:247],a[172:175],v[248:251],v[244:247]
s_and_b32 s46,s54,0xfffc0000
ds_read_b128 a[156:159],v1 offset:4864
v_cvt_pk_bf16_f32 v197,v198,v199
v_mfma_f32_16x16x32_bf16 v[252:255],a[188:191],v[248:251],v[44:47]
v_exp_f32 v204,v204
v_mfma_f32_16x16x32_bf16 v[252:255],a[184:187],v[216:219],v[252:255]
s_cmp_eq_u32 s50,131072
s_cselect_b32 s62,163840,0
ds_read_b128 a[152:155],v1 offset:4608
v_mfma_f32_16x16x32_bf16 v[252:255],a[180:183],v[184:187],v[252:255]
s_sub_u32 s48,32768,s62
v_exp_f32 v205,v205
v_mfma_f32_16x16x32_bf16 v[252:255],a[176:179],v[152:155],v[252:255]
s_add_u32 s50,s50,s48
ds_read_b128 a[148:151],v1 offset:4352
v_cvt_pk_bf16_f32 v198,v204,v205
v_mfma_f32_16x16x32_bf16 a[32:35],a[192:195],v[164:167],a[32:35]
v_exp_f32 v206,v206
v_mfma_f32_16x16x32_bf16 a[32:35],a[196:199],v[180:183],a[32:35]
s_cmp_eq_u32 s50,131072
s_cselect_b32 s62,163840,0
ds_read_b128 a[144:147],v1 offset:4096
v_mfma_f32_16x16x32_bf16 a[36:39],a[204:207],v[180:183],a[36:39]
buffer_load_dwordx4 v4,s[20:23],s47 offen offset:1024 nt lds
v_mfma_f32_16x16x32_bf16 a[36:39],a[200:203],v[164:167],a[36:39]
s_add_u32 s28,s28,s43
s_addc_u32 s29,s29,0
ds_read_b128 a[160:163],v1 offset:8192
v_mfma_f32_16x16x32_bf16 a[40:43],a[208:211],v[164:167],a[40:43]
s_and_b32 s29,s29,0xffff
v_exp_f32 v207,v207
v_mfma_f32_16x16x32_bf16 a[40:43],a[212:215],v[180:183],a[40:43]
ds_read_b128 a[164:167],v1 offset:8448
v_cvt_pk_bf16_f32 v199,v206,v207
v_mfma_f32_16x16x32_bf16 a[44:47],a[220:223],v[180:183],a[44:47]
v_exp_f32 v212,v212
v_mfma_f32_16x16x32_bf16 a[44:47],a[216:219],v[164:167],a[44:47]
ds_read_b128 a[168:171],v1 offset:8704
v_mfma_f32_16x16x32_bf16 a[48:51],a[224:227],v[164:167],a[48:51]
v_exp_f32 v213,v213
v_mfma_f32_16x16x32_bf16 a[48:51],a[228:231],v[180:183],a[48:51]
ds_read_b128 a[172:175],v1 offset:8960
v_cvt_pk_bf16_f32 v212,v212,v213
v_mfma_f32_16x16x32_bf16 a[52:55],a[236:239],v[180:183],a[52:55]
v_exp_f32 v214,v214
v_mfma_f32_16x16x32_bf16 a[52:55],a[232:235],v[164:167],a[52:55]
ds_read_b128 a[188:191],v1 offset:13056
v_mfma_f32_16x16x32_bf16 a[56:59],a[240:243],v[164:167],a[56:59]
v_exp_f32 v215,v215
v_mfma_f32_16x16x32_bf16 a[56:59],a[244:247],v[180:183],a[56:59]
ds_read_b128 a[184:187],v1 offset:12800
v_cvt_pk_bf16_f32 v213,v214,v215
v_mfma_f32_16x16x32_bf16 a[60:63],a[252:255],v[180:183],a[60:63]
v_exp_f32 v220,v220
v_mfma_f32_16x16x32_bf16 a[60:63],a[248:251],v[164:167],a[60:63]
ds_read_b128 a[180:183],v1 offset:12544
v_mfma_f32_16x16x32_bf16 v[52:55],v[28:31],v[164:167],v[52:55]
buffer_load_dwordx4 v5,s[20:23],s47 offen offset:2048 nt lds
v_mfma_f32_16x16x32_bf16 v[52:55],v[28:31],v[180:183],v[52:55]
s_waitcnt lgkmcnt(3)
ds_read_b128 a[176:179],v1 offset:12288
v_add_u32 v1,s48,v1
v_mfma_f32_16x16x32_bf16 v[132:135],a[128:131],v[128:131],v[32:35]
v_exp_f32 v221,v221
v_mfma_f32_16x16x32_bf16 v[132:135],a[132:135],v[160:163],v[132:135]
v_exp_f32 v222,v222
v_mfma_f32_16x16x32_bf16 v[132:135],a[136:139],v[192:195],v[132:135]
v_exp_f32 v223,v223
v_mfma_f32_16x16x32_bf16 v[132:135],a[140:143],v[224:227],v[132:135]
v_cvt_pk_bf16_f32 v214,v220,v221
v_cvt_pk_bf16_f32 v215,v222,v223
v_mfma_f32_16x16x32_bf16 v[140:143],a[156:159],v[224:227],v[32:35]
v_exp_f32 v228,v228
v_mfma_f32_16x16x32_bf16 v[140:143],a[152:155],v[192:195],v[140:143]
v_exp_f32 v229,v229
v_mfma_f32_16x16x32_bf16 v[140:143],a[148:151],v[160:163],v[140:143]
v_exp_f32 v230,v230
v_mfma_f32_16x16x32_bf16 v[140:143],a[144:147],v[128:131],v[140:143]
v_exp_f32 v231,v231
v_mfma_f32_16x16x32_bf16 v[148:151],a[160:163],v[128:131],v[32:35]
v_cvt_pk_bf16_f32 v228,v228,v229
v_cvt_pk_bf16_f32 v229,v230,v231
v_mfma_f32_16x16x32_bf16 v[148:151],a[164:167],v[160:163],v[148:151]
v_exp_f32 v236,v236
v_mfma_f32_16x16x32_bf16 v[148:151],a[168:171],v[192:195],v[148:151]
v_exp_f32 v237,v237
v_mfma_f32_16x16x32_bf16 v[148:151],a[172:175],v[224:227],v[148:151]
s_waitcnt lgkmcnt(0)
v_exp_f32 v238,v238
v_mfma_f32_16x16x32_bf16 v[156:159],a[188:191],v[224:227],v[32:35]
buffer_load_dwordx4 v5,s[20:23],s47 offen offset:3072 nt lds
)A"
R"A(v_mfma_f32_16x16x32_bf16 v[156:159],a[184:187],v[192:195],v[156:159]
v_exp_f32 v239,v239
v_mfma_f32_16x16x32_bf16 v[156:159],a[180:183],v[160:163],v[156:159]
v_exp_f32 v244,v244
v_mfma_f32_16x16x32_bf16 v[156:159],a[176:179],v[128:131],v[156:159]
v_exp_f32 v245,v245
v_mfma_f32_16x16x32_bf16 a[64:67],a[192:195],v[196:199],a[64:67]
v_exp_f32 v246,v246
v_mfma_f32_16x16x32_bf16 a[64:67],a[196:199],v[212:215],a[64:67]
v_exp_f32 v247,v247
v_mfma_f32_16x16x32_bf16 a[68:71],a[204:207],v[212:215],a[68:71]
v_cvt_pk_bf16_f32 v230,v236,v237
v_cvt_pk_bf16_f32 v231,v238,v239
v_mfma_f32_16x16x32_bf16 a[68:71],a[200:203],v[196:199],a[68:71]
v_cvt_pk_bf16_f32 v244,v244,v245
v_cvt_pk_bf16_f32 v245,v246,v247
v_mfma_f32_16x16x32_bf16 a[72:75],a[208:211],v[196:199],a[72:75]
v_exp_f32 v252,v252
v_mfma_f32_16x16x32_bf16 a[72:75],a[212:215],v[212:215],a[72:75]
v_exp_f32 v253,v253
v_mfma_f32_16x16x32_bf16 a[76:79],a[220:223],v[212:215],a[76:79]
v_exp_f32 v254,v254
v_mfma_f32_16x16x32_bf16 a[76:79],a[216:219],v[196:199],a[76:79]
s_add_u32 m0,s49,s52
v_exp_f32 v255,v255
v_mfma_f32_16x16x32_bf16 a[80:83],a[224:227],v[196:199],a[80:83]
s_add_u32 s49,s49,32768
v_cvt_pk_bf16_f32 v246,v252,v253
v_cvt_pk_bf16_f32 v247,v254,v255
v_mfma_f32_16x16x32_bf16 a[80:83],a[228:231],v[212:215],a[80:83]
v_exp_f32 v132,v132
v_mfma_f32_16x16x32_bf16 a[84:87],a[236:239],v[212:215],a[84:87]
buffer_load_dwordx4 v4,s[24:27],s47 offen nt lds
v_mfma_f32_16x16x32_bf16 a[84:87],a[232:235],v[196:199],a[84:87]
v_exp_f32 v133,v133
v_mfma_f32_16x16x32_bf16 a[88:91],a[240:243],v[196:199],a[88:91]
v_exp_f32 v134,v134
v_mfma_f32_16x16x32_bf16 a[88:91],a[244:247],v[212:215],a[88:91]
v_exp_f32 v135,v135
v_mfma_f32_16x16x32_bf16 a[92:95],a[252:255],v[212:215],a[92:95]
v_cvt_pk_bf16_f32 v132,v132,v133
v_cvt_pk_bf16_f32 v133,v134,v135
v_mfma_f32_16x16x32_bf16 a[92:95],a[248:251],v[196:199],a[92:95]
v_exp_f32 v140,v140
v_mfma_f32_16x16x32_bf16 v[56:59],v[28:31],v[196:199],v[56:59]
v_exp_f32 v141,v141
v_mfma_f32_16x16x32_bf16 v[56:59],v[28:31],v[212:215],v[56:59]
v_exp_f32 v142,v142
v_mfma_f32_16x16x32_bf16 v[164:167],a[128:131],v[136:139],v[36:39]
v_exp_f32 v143,v143
v_mfma_f32_16x16x32_bf16 v[164:167],a[132:135],v[168:171],v[164:167]
v_exp_f32 v148,v148
v_mfma_f32_16x16x32_bf16 v[164:167],a[136:139],v[200:203],v[164:167]
v_exp_f32 v149,v149
v_mfma_f32_16x16x32_bf16 v[164:167],a[140:143],v[232:235],v[164:167]
v_exp_f32 v150,v150
v_mfma_f32_16x16x32_bf16 v[172:175],a[156:159],v[232:235],v[36:39]
v_exp_f32 v151,v151
v_mfma_f32_16x16x32_bf16 v[172:175],a[152:155],v[200:203],v[172:175]
v_cvt_pk_bf16_f32 v134,v140,v141
v_cvt_pk_bf16_f32 v135,v142,v143
v_mfma_f32_16x16x32_bf16 v[172:175],a[148:151],v[168:171],v[172:175]
buffer_load_dwordx4 v5,s[24:27],s47 offen offset:1024 nt lds
v_mfma_f32_16x16x32_bf16 v[172:175],a[144:147],v[136:139],v[172:175]
v_cvt_pk_bf16_f32 v148,v148,v149
v_cvt_pk_bf16_f32 v149,v150,v151
v_mfma_f32_16x16x32_bf16 v[180:183],a[160:163],v[136:139],v[36:39]
v_exp_f32 v156,v156
v_mfma_f32_16x16x32_bf16 v[180:183],a[164:167],v[168:171],v[180:183]
v_exp_f32 v157,v157
v_mfma_f32_16x16x32_bf16 v[180:183],a[168:171],v[200:203],v[180:183]
v_exp_f32 v158,v158
v_mfma_f32_16x16x32_bf16 v[180:183],a[172:175],v[232:235],v[180:183]
v_exp_f32 v159,v159
v_mfma_f32_16x16x32_bf16 v[188:191],a[188:191],v[232:235],v[36:39]
v_cvt_pk_bf16_f32 v150,v156,v157
v_cvt_pk_bf16_f32 v151,v158,v159
v_mfma_f32_16x16x32_bf16 v[188:191],a[184:187],v[200:203],v[188:191]
v_exp_f32 v164,v164
v_mfma_f32_16x16x32_bf16 v[188:191],a[180:183],v[168:171],v[188:191]
v_exp_f32 v165,v165
v_mfma_f32_16x16x32_bf16 v[188:191],a[176:179],v[136:139],v[188:191]
v_exp_f32 v166,v166
v_mfma_f32_16x16x32_bf16 a[96:99],a[192:195],v[228:231],a[96:99]
ds_read_b64_tr_b16 a[192:193],v2
ds_read_b64_tr_b16 a[194:195],v2 offset:4096
v_mfma_f32_16x16x32_bf16 a[96:99],a[196:199],v[244:247],a[96:99]
ds_read_b64_tr_b16 a[196:197],v2 offset:8192
v_cvt_pk_bf16_f32 v164,v164,v165
v_mfma_f32_16x16x32_bf16 a[100:103],a[204:207],v[244:247],a[100:103]
s_cmp_eq_u32 s49,163840
s_cselect_b32 s49,0,s49
ds_read_b64_tr_b16 a[198:199],v2 offset:12288
v_mfma_f32_16x16x32_bf16 a[100:103],a[200:203],v[228:231],a[100:103]
s_add_u32 s49,s49,32768
ds_read_b64_tr_b16 a[204:205],v3 offset:8192
v_mfma_f32_16x16x32_bf16 a[104:107],a[208:211],v[228:231],a[104:107]
buffer_load_dwordx4 v4,s[24:27],s47 offen offset:2048 nt lds
v_mfma_f32_16x16x32_bf16 a[104:107],a[212:215],v[244:247],a[104:107]
ds_read_b64_tr_b16 a[206:207],v3 offset:12288
ds_read_b64_tr_b16 a[200:201],v3
v_mfma_f32_16x16x32_bf16 a[108:111],a[220:223],v[244:247],a[108:111]
ds_read_b64_tr_b16 a[202:203],v3 offset:4096
v_lshrrev_b32 v4,6,v6
v_mfma_f32_16x16x32_bf16 a[108:111],a[216:219],v[228:231],a[108:111]
ds_read_b64_tr_b16 a[208:209],v2 offset:256
v_mul_u32_u24 v4,320,v4
v_mfma_f32_16x16x32_bf16 a[112:115],a[224:227],v[228:231],a[112:115]
s_cmp_eq_u32 s49,163840
s_cselect_b32 s49,0,s49
ds_read_b64_tr_b16 a[210:211],v2 offset:4352
v_mfma_f32_16x16x32_bf16 a[112:115],a[228:231],v[244:247],a[112:115]
s_add_u32 s49,s49,32768
ds_read_b64_tr_b16 a[212:213],v2 offset:8448
ds_read_b64_tr_b16 a[214:215],v2 offset:12544
v_mfma_f32_16x16x32_bf16 a[116:119],a[236:239],v[244:247],a[116:119]
s_cmp_eq_u32 s49,163840
s_cselect_b32 s49,0,s49
ds_read_b64_tr_b16 a[220:221],v3 offset:8448
v_mfma_f32_16x16x32_bf16 a[116:119],a[232:235],v[228:231],a[116:119]
ds_read_b64_tr_b16 a[222:223],v3 offset:12544
v_mfma_f32_16x16x32_bf16 a[120:123],a[240:243],v[228:231],a[120:123]
ds_read_b64_tr_b16 a[216:217],v3 offset:256
v_mfma_f32_16x16x32_bf16 a[120:123],a[244:247],v[244:247],a[120:123]
ds_read_b64_tr_b16 a[218:219],v3 offset:4352
ds_read_b64_tr_b16 a[224:225],v2 offset:512
v_mfma_f32_16x16x32_bf16 a[124:127],a[252:255],v[244:247],a[124:127]
ds_read_b64_tr_b16 a[226:227],v2 offset:4608
v_mfma_f32_16x16x32_bf16 a[124:127],a[248:251],v[228:231],a[124:127]
ds_read_b64_tr_b16 a[228:229],v2 offset:8704
v_mfma_f32_16x16x32_bf16 v[60:63],v[28:31],v[228:231],v[60:63]
ds_read_b64_tr_b16 a[230:231],v2 offset:12800
v_mfma_f32_16x16x32_bf16 v[60:63],v[28:31],v[244:247],v[60:63]
ds_read_b64_tr_b16 a[236:237],v3 offset:8704
ds_read_b64_tr_b16 a[238:239],v3 offset:12800
v_mfma_f32_16x16x32_bf16 v[196:199],a[128:131],v[144:147],v[40:43]
buffer_load_dwordx4 v5,s[24:27],s47 offen offset:3072 nt lds
v_mfma_f32_16x16x32_bf16 v[196:199],a[132:135],v[176:179],v[196:199]
s_add_u32 m0,s82,0
ds_read_b64_tr_b16 a[232:233],v3 offset:512
ds_read_b64_tr_b16 a[234:235],v3 offset:4608
v_mfma_f32_16x16x32_bf16 v[196:199],a[136:139],v[208:211],v[196:199]
s_add_u32 s47,s47,16384
ds_read_b64_tr_b16 a[240:241],v2 offset:768
v_bfe_u32 v5,v6,4,2
v_mfma_f32_16x16x32_bf16 v[196:199],a[140:143],v[240:243],v[196:199]
s_and_b32 s47,s47,0x3ffff
ds_read_b64_tr_b16 a[242:243],v2 offset:4864
v_lshl_add_u32 v4,v5,3,v4
v_mfma_f32_16x16x32_bf16 v[204:207],a[156:159],v[240:243],v[40:43]
s_or_b32 s47,s47,s46
buffer_load_dwordx4 v7,s[72:75],s80 offen lds
v_mfma_f32_16x16x32_bf16 v[204:207],a[152:155],v[208:211],v[204:207]
ds_read_b64_tr_b16 a[244:245],v2 offset:8960
ds_read_b64_tr_b16 a[246:247],v2 offset:13056
v_mfma_f32_16x16x32_bf16 v[204:207],a[148:151],v[176:179],v[204:207]
ds_read_b64_tr_b16 a[252:253],v3 offset:8960
v_add_u32 v2,s48,v2
v_mfma_f32_16x16x32_bf16 v[204:207],a[144:147],v[144:147],v[204:207]
ds_read_b64_tr_b16 a[254:255],v3 offset:13056
v_bfe_u32 v5,v6,8,1
v_mfma_f32_16x16x32_bf16 v[212:215],a[160:163],v[144:147],v[40:43]
buffer_load_dwordx4 v8,s[72:75],s80 offen offset:1024 lds
v_mfma_f32_16x16x32_bf16 v[212:215],a[164:167],v[176:179],v[212:215]
ds_read_b64_tr_b16 a[248:249],v3 offset:768
ds_read_b64_tr_b16 a[250:251],v3 offset:4864
v_mfma_f32_16x16x32_bf16 v[212:215],a[168:171],v[208:211],v[212:215]
v_exp_f32 v167,v167
v_mfma_f32_16x16x32_bf16 v[212:215],a[172:175],v[240:243],v[212:215]
v_exp_f32 v172,v172
v_mfma_f32_16x16x32_bf16 v[220:223],a[188:191],v[240:243],v[40:43]
buffer_load_dwordx4 v9,s[72:75],s80 offen offset:2048 lds
v_mfma_f32_16x16x32_bf16 v[220:223],a[184:187],v[208:211],v[220:223]
v_exp_f32 v173,v173
v_mfma_f32_16x16x32_bf16 v[220:223],a[180:183],v[176:179],v[220:223]
v_exp_f32 v174,v174
v_mfma_f32_16x16x32_bf16 v[220:223],a[176:179],v[144:147],v[220:223]
s_waitcnt lgkmcnt(2)
v_exp_f32 v175,v175
v_mfma_f32_16x16x32_bf16 a[0:3],a[192:195],v[132:135],a[0:3]
buffer_load_dwordx4 v10,s[72:75],s80 offen offset:3072 lds
v_mfma_f32_16x16x32_bf16 a[0:3],a[196:199],v[148:151],a[0:3]
s_add_u32 m0,s82,4096
v_exp_f32 v180,v180
v_mfma_f32_16x16x32_bf16 a[4:7],a[204:207],v[148:151],a[4:7]
v_exp_f32 v181,v181
v_mfma_f32_16x16x32_bf16 a[4:7],a[200:203],v[132:135],a[4:7]
v_cvt_pk_bf16_f32 v165,v166,v167
v_cvt_pk_bf16_f32 v166,v172,v173
v_mfma_f32_16x16x32_bf16 a[8:11],a[208:211],v[132:135],a[8:11]
buffer_load_dwordx4 v11,s[72:75],s80 offen lds
v_mfma_f32_16x16x32_bf16 a[8:11],a[212:215],v[148:151],a[8:11]
s_add_u32 m0,s82,5120
v_exp_f32 v182,v182
v_mfma_f32_16x16x32_bf16 a[12:15],a[220:223],v[148:151],a[12:15]
s_add_u32 s80,s71,0
s_and_b32 s80,s80,3
s_lshl_b32 s80,s80,1
v_mfma_f32_16x16x32_bf16 a[12:15],a[216:219],v[132:135],a[12:15]
s_or_b32 s80,s80,1
v_exp_f32 v183,v183
v_mfma_f32_16x16x32_bf16 a[16:19],a[224:227],v[132:135],a[16:19]
s_mul_i32 s80,s80,163840
buffer_load_dwordx4 v7,s[72:75],s83 offen lds
v_mfma_f32_16x16x32_bf16 a[16:19],a[228:231],v[148:151],a[16:19]
s_add_u32 s80,s80,s81
v_cvt_pk_bf16_f32 v167,v174,v175
v_cvt_pk_bf16_f32 v180,v180,v181
v_mfma_f32_16x16x32_bf16 a[20:23],a[236:239],v[148:151],a[20:23]
v_exp_f32 v188,v188
v_mfma_f32_16x16x32_bf16 a[20:23],a[232:235],v[132:135],a[20:23]
v_exp_f32 v189,v189
v_mfma_f32_16x16x32_bf16 a[24:27],a[240:243],v[132:135],a[24:27]
buffer_load_dwordx4 v8,s[72:75],s83 offen offset:1024 lds
v_mfma_f32_16x16x32_bf16 a[24:27],a[244:247],v[148:151],a[24:27]
v_exp_f32 v190,v190
v_mfma_f32_16x16x32_bf16 a[28:31],a[252:255],v[148:151],a[28:31]
s_waitcnt lgkmcnt(0)
v_exp_f32 v191,v191
v_mfma_f32_16x16x32_bf16 a[28:31],a[248:251],v[132:135],a[28:31]
v_cvt_pk_bf16_f32 v181,v182,v183
v_cvt_pk_bf16_f32 v182,v188,v189
v_mfma_f32_16x16x32_bf16 v[48:51],v[28:31],v[132:135],v[48:51]
buffer_load_dwordx4 v9,s[72:75],s83 offen offset:2048 lds
v_mfma_f32_16x16x32_bf16 v[48:51],v[28:31],v[148:151],v[48:51]
v_cvt_pk_bf16_f32 v183,v190,v191
v_add_u32 v3,s48,v3
v_mfma_f32_16x16x32_bf16 v[228:231],a[128:131],v[152:155],v[44:47]
s_waitcnt vmcnt(17)
s_sub_u32 s48,32768,s62
v_exp_f32 v196,v196
v_mfma_f32_16x16x32_bf16 v[228:231],a[132:135],v[184:187],v[228:231]
s_barrier
buffer_load_dwordx4 v10,s[72:75],s83 offen offset:3072 lds
v_mfma_f32_16x16x32_bf16 v[228:231],a[136:139],v[216:219],v[228:231]
s_add_u32 m0,s82,9216
s_add_u32 s50,s50,s48
ds_read_b128 a[128:131],v1
v_mfma_f32_16x16x32_bf16 v[228:231],a[140:143],v[248:251],v[228:231]
v_exp_f32 v197,v197
v_mfma_f32_16x16x32_bf16 v[236:239],a[156:159],v[248:251],v[44:47]
ds_read_b128 a[132:135],v1 offset:256
v_cvt_pk_bf16_f32 v196,v196,v197
v_mfma_f32_16x16x32_bf16 v[236:239],a[152:155],v[216:219],v[236:239]
buffer_load_dwordx4 v11,s[72:75],s83 offen lds
v_mfma_f32_16x16x32_bf16 v[236:239],a[148:151],v[184:187],v[236:239]
s_add_u32 s83,s80,20480
ds_read_b128 a[136:139],v1 offset:512
v_lshlrev_b32 v5,5,v5
v_mfma_f32_16x16x32_bf16 v[236:239],a[144:147],v[152:155],v[236:239]
v_exp_f32 v198,v198
v_mfma_f32_16x16x32_bf16 v[244:247],a[160:163],v[152:155],v[44:47]
s_add_u32 m0,s82,40960
ds_read_b128 a[140:143],v1 offset:768
v_mfma_f32_16x16x32_bf16 v[244:247],a[164:167],v[184:187],v[244:247]
v_exp_f32 v199,v199
v_mfma_f32_16x16x32_bf16 v[244:247],a[168:171],v[216:219],v[244:247]
ds_read_b128 a[156:159],v1 offset:4864
v_cvt_pk_bf16_f32 v197,v198,v199
v_mfma_f32_16x16x32_bf16 v[244:247],a[172:175],v[248:251],v[244:247]
buffer_load_dwordx4 v7,s[72:75],s80 offen lds
v_mfma_f32_16x16x32_bf16 v[252:255],a[188:191],v[248:251],v[44:47]
s_cmp_eq_u32 s50,131072
s_cselect_b32 s62,163840,0
ds_read_b128 a[152:155],v1 offset:4608
v_mfma_f32_16x16x32_bf16 v[252:255],a[184:187],v[216:219],v[252:255]
v_exp_f32 v204,v204
v_mfma_f32_16x16x32_bf16 v[252:255],a[180:183],v[184:187],v[252:255]
)A"
R"A(ds_read_b128 a[148:151],v1 offset:4352
v_mfma_f32_16x16x32_bf16 v[252:255],a[176:179],v[152:155],v[252:255]
v_exp_f32 v205,v205
v_mfma_f32_16x16x32_bf16 a[32:35],a[192:195],v[164:167],a[32:35]
ds_read_b128 a[144:147],v1 offset:4096
v_cvt_pk_bf16_f32 v198,v204,v205
v_mfma_f32_16x16x32_bf16 a[32:35],a[196:199],v[180:183],a[32:35]
v_exp_f32 v206,v206
v_mfma_f32_16x16x32_bf16 a[36:39],a[204:207],v[180:183],a[36:39]
ds_read_b128 a[160:163],v1 offset:8192
v_mfma_f32_16x16x32_bf16 a[36:39],a[200:203],v[164:167],a[36:39]
v_exp_f32 v207,v207
v_mfma_f32_16x16x32_bf16 a[40:43],a[208:211],v[164:167],a[40:43]
ds_read_b128 a[164:167],v1 offset:8448
v_cvt_pk_bf16_f32 v199,v206,v207
v_mfma_f32_16x16x32_bf16 a[40:43],a[212:215],v[180:183],a[40:43]
v_exp_f32 v212,v212
v_mfma_f32_16x16x32_bf16 a[44:47],a[220:223],v[180:183],a[44:47]
buffer_store_dwordx4 v[64:67],v6,s[36:39],s84 offen sc1
v_mfma_f32_16x16x32_bf16 a[44:47],a[216:219],v[164:167],a[44:47]
ds_read_b128 a[168:171],v1 offset:8704
v_mfma_f32_16x16x32_bf16 a[48:51],a[224:227],v[164:167],a[48:51]
v_exp_f32 v213,v213
v_mfma_f32_16x16x32_bf16 a[48:51],a[228:231],v[180:183],a[48:51]
ds_read_b128 a[172:175],v1 offset:8960
v_cvt_pk_bf16_f32 v212,v212,v213
v_mfma_f32_16x16x32_bf16 a[52:55],a[236:239],v[180:183],a[52:55]
buffer_load_dwordx4 v8,s[72:75],s80 offen offset:1024 lds
v_mfma_f32_16x16x32_bf16 a[52:55],a[232:235],v[164:167],a[52:55]
ds_read_b128 a[188:191],v1 offset:13056
v_mfma_f32_16x16x32_bf16 a[56:59],a[240:243],v[164:167],a[56:59]
v_exp_f32 v214,v214
v_mfma_f32_16x16x32_bf16 a[56:59],a[244:247],v[180:183],a[56:59]
ds_read_b128 a[184:187],v1 offset:12800
v_mfma_f32_16x16x32_bf16 a[60:63],a[252:255],v[180:183],a[60:63]
v_exp_f32 v215,v215
v_mfma_f32_16x16x32_bf16 a[60:63],a[248:251],v[164:167],a[60:63]
ds_read_b128 a[180:183],v1 offset:12544
v_cvt_pk_bf16_f32 v213,v214,v215
v_mfma_f32_16x16x32_bf16 v[52:55],v[28:31],v[164:167],v[52:55]
v_exp_f32 v220,v220
v_mfma_f32_16x16x32_bf16 v[52:55],v[28:31],v[180:183],v[52:55]
s_waitcnt lgkmcnt(3)
ds_read_b128 a[176:179],v1 offset:12288
v_add_u32 v1,s48,v1
v_mfma_f32_16x16x32_bf16 v[132:135],a[128:131],v[128:131],v[32:35]
v_exp_f32 v221,v221
v_mfma_f32_16x16x32_bf16 v[132:135],a[132:135],v[160:163],v[132:135]
v_exp_f32 v222,v222
v_mfma_f32_16x16x32_bf16 v[132:135],a[136:139],v[192:195],v[132:135]
v_exp_f32 v223,v223
v_mfma_f32_16x16x32_bf16 v[132:135],a[140:143],v[224:227],v[132:135]
buffer_load_dwordx4 v9,s[72:75],s80 offen offset:2048 lds
v_mfma_f32_16x16x32_bf16 v[140:143],a[156:159],v[224:227],v[32:35]
v_cvt_pk_bf16_f32 v214,v220,v221
v_cvt_pk_bf16_f32 v215,v222,v223
v_mfma_f32_16x16x32_bf16 v[140:143],a[152:155],v[192:195],v[140:143]
v_exp_f32 v228,v228
v_mfma_f32_16x16x32_bf16 v[140:143],a[148:151],v[160:163],v[140:143]
v_exp_f32 v229,v229
v_mfma_f32_16x16x32_bf16 v[140:143],a[144:147],v[128:131],v[140:143]
buffer_store_dwordx4 v[68:71],v6,s[36:39],s84 offen offset:1024 sc1
v_mfma_f32_16x16x32_bf16 v[148:151],a[160:163],v[128:131],v[32:35]
v_exp_f32 v230,v230
v_mfma_f32_16x16x32_bf16 v[148:151],a[164:167],v[160:163],v[148:151]
v_exp_f32 v231,v231
v_mfma_f32_16x16x32_bf16 v[148:151],a[168:171],v[192:195],v[148:151]
v_cvt_pk_bf16_f32 v228,v228,v229
v_cvt_pk_bf16_f32 v229,v230,v231
v_mfma_f32_16x16x32_bf16 v[148:151],a[172:175],v[224:227],v[148:151]
s_waitcnt lgkmcnt(0)
v_exp_f32 v236,v236
v_mfma_f32_16x16x32_bf16 v[156:159],a[188:191],v[224:227],v[32:35]
v_exp_f32 v237,v237
v_mfma_f32_16x16x32_bf16 v[156:159],a[184:187],v[192:195],v[156:159]
v_exp_f32 v238,v238
v_mfma_f32_16x16x32_bf16 v[156:159],a[180:183],v[160:163],v[156:159]
v_exp_f32 v239,v239
v_mfma_f32_16x16x32_bf16 v[156:159],a[176:179],v[128:131],v[156:159]
v_exp_f32 v244,v244
v_mfma_f32_16x16x32_bf16 a[64:67],a[192:195],v[196:199],a[64:67]
buffer_load_dwordx4 v10,s[72:75],s80 offen offset:3072 lds
v_mfma_f32_16x16x32_bf16 a[64:67],a[196:199],v[212:215],a[64:67]
v_exp_f32 v245,v245
v_mfma_f32_16x16x32_bf16 a[68:71],a[204:207],v[212:215],a[68:71]
v_exp_f32 v246,v246
v_mfma_f32_16x16x32_bf16 a[68:71],a[200:203],v[196:199],a[68:71]
v_exp_f32 v247,v247
v_mfma_f32_16x16x32_bf16 a[72:75],a[208:211],v[196:199],a[72:75]
buffer_store_dwordx4 v[72:75],v6,s[36:39],s84 offen offset:2048 sc1
v_mfma_f32_16x16x32_bf16 a[72:75],a[212:215],v[212:215],a[72:75]
v_cvt_pk_bf16_f32 v230,v236,v237
v_cvt_pk_bf16_f32 v231,v238,v239
v_mfma_f32_16x16x32_bf16 a[76:79],a[220:223],v[212:215],a[76:79]
v_cvt_pk_bf16_f32 v244,v244,v245
v_cvt_pk_bf16_f32 v245,v246,v247
v_mfma_f32_16x16x32_bf16 a[76:79],a[216:219],v[196:199],a[76:79]
v_exp_f32 v252,v252
v_mfma_f32_16x16x32_bf16 a[80:83],a[224:227],v[196:199],a[80:83]
v_exp_f32 v253,v253
v_mfma_f32_16x16x32_bf16 a[80:83],a[228:231],v[212:215],a[80:83]
v_exp_f32 v254,v254
v_mfma_f32_16x16x32_bf16 a[84:87],a[236:239],v[212:215],a[84:87]
s_add_u32 m0,s82,45056
v_exp_f32 v255,v255
v_mfma_f32_16x16x32_bf16 a[84:87],a[232:235],v[196:199],a[84:87]
v_cvt_pk_bf16_f32 v246,v252,v253
v_cvt_pk_bf16_f32 v247,v254,v255
v_mfma_f32_16x16x32_bf16 a[88:91],a[240:243],v[196:199],a[88:91]
v_exp_f32 v132,v132
v_mfma_f32_16x16x32_bf16 a[88:91],a[244:247],v[212:215],a[88:91]
buffer_load_dwordx4 v11,s[72:75],s80 offen lds
v_mfma_f32_16x16x32_bf16 a[92:95],a[252:255],v[212:215],a[92:95]
v_exp_f32 v133,v133
v_mfma_f32_16x16x32_bf16 a[92:95],a[248:251],v[196:199],a[92:95]
v_exp_f32 v134,v134
v_mfma_f32_16x16x32_bf16 v[56:59],v[28:31],v[196:199],v[56:59]
v_exp_f32 v135,v135
v_mfma_f32_16x16x32_bf16 v[56:59],v[28:31],v[212:215],v[56:59]
v_cvt_pk_bf16_f32 v132,v132,v133
v_cvt_pk_bf16_f32 v133,v134,v135
v_mfma_f32_16x16x32_bf16 v[164:167],a[128:131],v[136:139],v[36:39]
v_exp_f32 v140,v140
v_mfma_f32_16x16x32_bf16 v[164:167],a[132:135],v[168:171],v[164:167]
v_exp_f32 v141,v141
v_mfma_f32_16x16x32_bf16 v[164:167],a[136:139],v[200:203],v[164:167]
buffer_store_dwordx4 v[76:79],v6,s[36:39],s84 offen offset:3072 sc1
v_mfma_f32_16x16x32_bf16 v[164:167],a[140:143],v[232:235],v[164:167]
s_add_u32 s84,s55,65536
v_exp_f32 v142,v142
v_mfma_f32_16x16x32_bf16 v[172:175],a[156:159],v[232:235],v[36:39]
s_max_i32 s84,s84,s18
v_exp_f32 v143,v143
v_mfma_f32_16x16x32_bf16 v[172:175],a[152:155],v[200:203],v[172:175]
s_add_u32 m0,s82,46080
v_exp_f32 v148,v148
v_mfma_f32_16x16x32_bf16 v[172:175],a[148:151],v[168:171],v[172:175]
v_exp_f32 v149,v149
v_mfma_f32_16x16x32_bf16 v[172:175],a[144:147],v[136:139],v[172:175]
v_exp_f32 v150,v150
v_mfma_f32_16x16x32_bf16 v[180:183],a[160:163],v[136:139],v[36:39]
buffer_load_dwordx4 v7,s[72:75],s83 offen lds
v_mfma_f32_16x16x32_bf16 v[180:183],a[164:167],v[168:171],v[180:183]
v_exp_f32 v151,v151
v_mfma_f32_16x16x32_bf16 v[180:183],a[168:171],v[200:203],v[180:183]
v_cvt_pk_bf16_f32 v134,v140,v141
v_cvt_pk_bf16_f32 v135,v142,v143
v_mfma_f32_16x16x32_bf16 v[180:183],a[172:175],v[232:235],v[180:183]
v_cvt_pk_bf16_f32 v148,v148,v149
v_cvt_pk_bf16_f32 v149,v150,v151
v_mfma_f32_16x16x32_bf16 v[188:191],a[188:191],v[232:235],v[36:39]
v_exp_f32 v156,v156
v_mfma_f32_16x16x32_bf16 v[188:191],a[184:187],v[200:203],v[188:191]
v_exp_f32 v157,v157
v_mfma_f32_16x16x32_bf16 v[188:191],a[180:183],v[168:171],v[188:191]
v_exp_f32 v158,v158
v_mfma_f32_16x16x32_bf16 v[188:191],a[176:179],v[136:139],v[188:191]
v_exp_f32 v159,v159
v_mfma_f32_16x16x32_bf16 a[96:99],a[192:195],v[228:231],a[96:99]
ds_read_b64_tr_b16 a[192:193],v2
ds_read_b64_tr_b16 a[194:195],v2 offset:4096
v_mfma_f32_16x16x32_bf16 a[96:99],a[196:199],v[244:247],a[96:99]
ds_read_b64_tr_b16 a[196:197],v2 offset:8192
v_cvt_pk_bf16_f32 v150,v156,v157
v_mfma_f32_16x16x32_bf16 a[100:103],a[204:207],v[244:247],a[100:103]
buffer_store_dwordx4 v[80:83],v6,s[36:39],s85 offen sc1
v_mfma_f32_16x16x32_bf16 a[100:103],a[200:203],v[228:231],a[100:103]
ds_read_b64_tr_b16 a[198:199],v2 offset:12288
ds_read_b64_tr_b16 a[204:205],v3 offset:8192
v_mfma_f32_16x16x32_bf16 a[104:107],a[208:211],v[228:231],a[104:107]
ds_read_b64_tr_b16 a[206:207],v3 offset:12288
v_cvt_pk_bf16_f32 v151,v158,v159
v_mfma_f32_16x16x32_bf16 a[104:107],a[212:215],v[244:247],a[104:107]
ds_read_b64_tr_b16 a[200:201],v3
v_mfma_f32_16x16x32_bf16 a[108:111],a[220:223],v[244:247],a[108:111]
buffer_load_dwordx4 v8,s[72:75],s83 offen offset:1024 lds
v_mfma_f32_16x16x32_bf16 a[108:111],a[216:219],v[228:231],a[108:111]
ds_read_b64_tr_b16 a[202:203],v3 offset:4096
ds_read_b64_tr_b16 a[208:209],v2 offset:256
v_mfma_f32_16x16x32_bf16 a[112:115],a[224:227],v[228:231],a[112:115]
ds_read_b64_tr_b16 a[210:211],v2 offset:4352
v_mfma_f32_16x16x32_bf16 a[112:115],a[228:231],v[244:247],a[112:115]
ds_read_b64_tr_b16 a[212:213],v2 offset:8448
v_mfma_f32_16x16x32_bf16 a[116:119],a[236:239],v[244:247],a[116:119]
ds_read_b64_tr_b16 a[214:215],v2 offset:12544
v_mfma_f32_16x16x32_bf16 a[116:119],a[232:235],v[228:231],a[116:119]
ds_read_b64_tr_b16 a[220:221],v3 offset:8448
ds_read_b64_tr_b16 a[222:223],v3 offset:12544
v_mfma_f32_16x16x32_bf16 a[120:123],a[240:243],v[228:231],a[120:123]
ds_read_b64_tr_b16 a[216:217],v3 offset:256
v_mfma_f32_16x16x32_bf16 a[120:123],a[244:247],v[244:247],a[120:123]
ds_read_b64_tr_b16 a[218:219],v3 offset:4352
v_mfma_f32_16x16x32_bf16 a[124:127],a[252:255],v[244:247],a[124:127]
ds_read_b64_tr_b16 a[224:225],v2 offset:512
v_mfma_f32_16x16x32_bf16 a[124:127],a[248:251],v[228:231],a[124:127]
ds_read_b64_tr_b16 a[226:227],v2 offset:4608
ds_read_b64_tr_b16 a[228:229],v2 offset:8704
v_mfma_f32_16x16x32_bf16 v[60:63],v[28:31],v[228:231],v[60:63]
ds_read_b64_tr_b16 a[230:231],v2 offset:12800
v_mfma_f32_16x16x32_bf16 v[60:63],v[28:31],v[244:247],v[60:63]
ds_read_b64_tr_b16 a[236:237],v3 offset:8704
v_mfma_f32_16x16x32_bf16 v[196:199],a[128:131],v[144:147],v[40:43]
buffer_store_dwordx4 v[84:87],v6,s[36:39],s85 offen offset:1024 sc1
v_mfma_f32_16x16x32_bf16 v[196:199],a[132:135],v[176:179],v[196:199]
ds_read_b64_tr_b16 a[238:239],v3 offset:12800
ds_read_b64_tr_b16 a[232:233],v3 offset:512
v_mfma_f32_16x16x32_bf16 v[196:199],a[136:139],v[208:211],v[196:199]
ds_read_b64_tr_b16 a[234:235],v3 offset:4608
v_mfma_f32_16x16x32_bf16 v[196:199],a[140:143],v[240:243],v[196:199]
ds_read_b64_tr_b16 a[240:241],v2 offset:768
v_mfma_f32_16x16x32_bf16 v[204:207],a[156:159],v[240:243],v[40:43]
buffer_load_dwordx4 v9,s[72:75],s83 offen offset:2048 lds
v_mfma_f32_16x16x32_bf16 v[204:207],a[152:155],v[208:211],v[204:207]
ds_read_b64_tr_b16 a[242:243],v2 offset:4864
ds_read_b64_tr_b16 a[244:245],v2 offset:8960
v_mfma_f32_16x16x32_bf16 v[204:207],a[148:151],v[176:179],v[204:207]
ds_read_b64_tr_b16 a[246:247],v2 offset:13056
v_add_u32 v2,s48,v2
v_mfma_f32_16x16x32_bf16 v[204:207],a[144:147],v[144:147],v[204:207]
ds_read_b64_tr_b16 a[252:253],v3 offset:8960
v_mfma_f32_16x16x32_bf16 v[212:215],a[160:163],v[144:147],v[40:43]
ds_read_b64_tr_b16 a[254:255],v3 offset:13056
v_mfma_f32_16x16x32_bf16 v[212:215],a[164:167],v[176:179],v[212:215]
ds_read_b64_tr_b16 a[248:249],v3 offset:768
ds_read_b64_tr_b16 a[250:251],v3 offset:4864
v_mfma_f32_16x16x32_bf16 v[212:215],a[168:171],v[208:211],v[212:215]
v_exp_f32 v164,v164
v_mfma_f32_16x16x32_bf16 v[212:215],a[172:175],v[240:243],v[212:215]
v_exp_f32 v165,v165
v_mfma_f32_16x16x32_bf16 v[220:223],a[188:191],v[240:243],v[40:43]
v_exp_f32 v166,v166
v_mfma_f32_16x16x32_bf16 v[220:223],a[184:187],v[208:211],v[220:223]
buffer_load_dwordx4 v10,s[72:75],s83 offen offset:3072 lds
v_mfma_f32_16x16x32_bf16 v[220:223],a[180:183],v[176:179],v[220:223]
v_exp_f32 v167,v167
v_mfma_f32_16x16x32_bf16 v[220:223],a[176:179],v[144:147],v[220:223]
s_waitcnt lgkmcnt(3)
v_cvt_pk_bf16_f32 v164,v164,v165
v_cvt_pk_bf16_f32 v165,v166,v167
v_mfma_f32_16x16x32_bf16 a[0:3],a[192:195],v[132:135],a[0:3]
v_exp_f32 v172,v172
v_mfma_f32_16x16x32_bf16 a[0:3],a[196:199],v[148:151],a[0:3]
buffer_store_dwordx4 v[88:91],v6,s[36:39],s85 offen offset:2048 sc1
v_mfma_f32_16x16x32_bf16 a[4:7],a[204:207],v[148:151],a[4:7]
v_exp_f32 v173,v173
v_mfma_f32_16x16x32_bf16 a[4:7],a[200:203],v[132:135],a[4:7]
v_exp_f32 v174,v174
v_mfma_f32_16x16x32_bf16 a[8:11],a[208:211],v[132:135],a[8:11]
v_exp_f32 v175,v175
v_mfma_f32_16x16x32_bf16 a[8:11],a[212:215],v[148:151],a[8:11]
v_exp_f32 v180,v180
)A"
R"A(v_mfma_f32_16x16x32_bf16 a[12:15],a[220:223],v[148:151],a[12:15]
v_exp_f32 v181,v181
v_mfma_f32_16x16x32_bf16 a[12:15],a[216:219],v[132:135],a[12:15]
s_add_u32 m0,s82,50176
s_cmp_ge_u32 0,s71
s_cselect_b32 s80,2,0
v_mfma_f32_16x16x32_bf16 a[16:19],a[224:227],v[132:135],a[16:19]
s_mul_i32 s80,s80,163840
v_exp_f32 v182,v182
v_mfma_f32_16x16x32_bf16 a[16:19],a[228:231],v[148:151],a[16:19]
s_add_u32 s80,s80,s81
v_exp_f32 v183,v183
v_mfma_f32_16x16x32_bf16 a[20:23],a[236:239],v[148:151],a[20:23]
buffer_load_dwordx4 v11,s[72:75],s83 offen lds
v_mfma_f32_16x16x32_bf16 a[20:23],a[232:235],v[132:135],a[20:23]
s_add_u32 s83,s80,20480
v_cvt_pk_bf16_f32 v166,v172,v173
v_cvt_pk_bf16_f32 v167,v174,v175
v_mfma_f32_16x16x32_bf16 a[24:27],a[240:243],v[132:135],a[24:27]
v_cvt_pk_bf16_f32 v180,v180,v181
v_cvt_pk_bf16_f32 v181,v182,v183
v_mfma_f32_16x16x32_bf16 a[24:27],a[244:247],v[148:151],a[24:27]
s_waitcnt lgkmcnt(0)
v_exp_f32 v188,v188
v_mfma_f32_16x16x32_bf16 a[28:31],a[252:255],v[148:151],a[28:31]
v_exp_f32 v189,v189
v_mfma_f32_16x16x32_bf16 a[28:31],a[248:251],v[132:135],a[28:31]
v_exp_f32 v190,v190
v_mfma_f32_16x16x32_bf16 v[48:51],v[28:31],v[132:135],v[48:51]
buffer_store_dwordx4 v[92:95],v6,s[36:39],s85 offen offset:3072 sc1
v_mfma_f32_16x16x32_bf16 v[48:51],v[28:31],v[148:151],v[48:51]
s_add_u32 s85,s55,69632
v_exp_f32 v191,v191
v_mfma_f32_16x16x32_bf16 v[228:231],a[128:131],v[152:155],v[44:47]
s_waitcnt vmcnt(28)
s_max_i32 s85,s85,s19
v_cvt_pk_bf16_f32 v182,v188,v189
v_cvt_pk_bf16_f32 v183,v190,v191
v_mfma_f32_16x16x32_bf16 v[228:231],a[132:135],v[184:187],v[228:231]
s_barrier
ds_read_b128 a[128:131],v1
v_add_u32 v3,s48,v3
v_mfma_f32_16x16x32_bf16 v[228:231],a[136:139],v[216:219],v[228:231]
s_sub_u32 s48,32768,s62
v_exp_f32 v196,v196
v_mfma_f32_16x16x32_bf16 v[228:231],a[140:143],v[248:251],v[228:231]
s_add_u32 s50,s50,s48
ds_read_b128 a[132:135],v1 offset:256
v_mfma_f32_16x16x32_bf16 v[236:239],a[156:159],v[248:251],v[44:47]
v_exp_f32 v197,v197
v_mfma_f32_16x16x32_bf16 v[236:239],a[152:155],v[216:219],v[236:239]
ds_read_b128 a[136:139],v1 offset:512
v_cvt_pk_bf16_f32 v196,v196,v197
v_mfma_f32_16x16x32_bf16 v[236:239],a[148:151],v[184:187],v[236:239]
v_exp_f32 v198,v198
v_mfma_f32_16x16x32_bf16 v[236:239],a[144:147],v[152:155],v[236:239]
ds_read_b128 a[140:143],v1 offset:768
v_mfma_f32_16x16x32_bf16 v[244:247],a[160:163],v[152:155],v[44:47]
s_add_u32 m0,s82,81920
v_exp_f32 v199,v199
v_mfma_f32_16x16x32_bf16 v[244:247],a[164:167],v[184:187],v[244:247]
ds_read_b128 a[156:159],v1 offset:4864
v_cvt_pk_bf16_f32 v197,v198,v199
v_mfma_f32_16x16x32_bf16 v[244:247],a[168:171],v[216:219],v[244:247]
v_exp_f32 v204,v204
v_mfma_f32_16x16x32_bf16 v[244:247],a[172:175],v[248:251],v[244:247]
buffer_load_dwordx4 v7,s[72:75],s80 offen lds
v_mfma_f32_16x16x32_bf16 v[252:255],a[188:191],v[248:251],v[44:47]
ds_read_b128 a[152:155],v1 offset:4608
v_mfma_f32_16x16x32_bf16 v[252:255],a[184:187],v[216:219],v[252:255]
v_exp_f32 v205,v205
v_mfma_f32_16x16x32_bf16 v[252:255],a[180:183],v[184:187],v[252:255]
ds_read_b128 a[148:151],v1 offset:4352
v_cvt_pk_bf16_f32 v198,v204,v205
v_mfma_f32_16x16x32_bf16 v[252:255],a[176:179],v[152:155],v[252:255]
buffer_store_dwordx4 v[96:99],v6,s[36:39],s86 offen sc1
v_mfma_f32_16x16x32_bf16 a[32:35],a[192:195],v[164:167],a[32:35]
ds_read_b128 a[144:147],v1 offset:4096
v_mfma_f32_16x16x32_bf16 a[32:35],a[196:199],v[180:183],a[32:35]
v_exp_f32 v206,v206
v_mfma_f32_16x16x32_bf16 a[36:39],a[204:207],v[180:183],a[36:39]
ds_read_b128 a[160:163],v1 offset:8192
v_mfma_f32_16x16x32_bf16 a[36:39],a[200:203],v[164:167],a[36:39]
v_exp_f32 v207,v207
v_mfma_f32_16x16x32_bf16 a[40:43],a[208:211],v[164:167],a[40:43]
ds_read_b128 a[164:167],v1 offset:8448
v_cvt_pk_bf16_f32 v199,v206,v207
v_mfma_f32_16x16x32_bf16 a[40:43],a[212:215],v[180:183],a[40:43]
v_exp_f32 v212,v212
v_mfma_f32_16x16x32_bf16 a[44:47],a[220:223],v[180:183],a[44:47]
ds_read_b128 a[168:171],v1 offset:8704
v_mfma_f32_16x16x32_bf16 a[44:47],a[216:219],v[164:167],a[44:47]
v_exp_f32 v213,v213
v_mfma_f32_16x16x32_bf16 a[48:51],a[224:227],v[164:167],a[48:51]
buffer_load_dwordx4 v8,s[72:75],s80 offen offset:1024 lds
v_mfma_f32_16x16x32_bf16 a[48:51],a[228:231],v[180:183],a[48:51]
ds_read_b128 a[172:175],v1 offset:8960
v_cvt_pk_bf16_f32 v212,v212,v213
v_mfma_f32_16x16x32_bf16 a[52:55],a[236:239],v[180:183],a[52:55]
v_exp_f32 v214,v214
v_mfma_f32_16x16x32_bf16 a[52:55],a[232:235],v[164:167],a[52:55]
ds_read_b128 a[188:191],v1 offset:13056
v_mfma_f32_16x16x32_bf16 a[56:59],a[240:243],v[164:167],a[56:59]
v_exp_f32 v215,v215
v_mfma_f32_16x16x32_bf16 a[56:59],a[244:247],v[180:183],a[56:59]
buffer_store_dwordx4 v[100:103],v6,s[36:39],s86 offen offset:1024 sc1
v_mfma_f32_16x16x32_bf16 a[60:63],a[252:255],v[180:183],a[60:63]
ds_read_b128 a[184:187],v1 offset:12800
v_cvt_pk_bf16_f32 v213,v214,v215
v_mfma_f32_16x16x32_bf16 a[60:63],a[248:251],v[164:167],a[60:63]
v_exp_f32 v220,v220
v_mfma_f32_16x16x32_bf16 v[52:55],v[28:31],v[164:167],v[52:55]
ds_read_b128 a[180:183],v1 offset:12544
v_mfma_f32_16x16x32_bf16 v[52:55],v[28:31],v[180:183],v[52:55]
s_waitcnt lgkmcnt(3)
v_exp_f32 v221,v221
v_mfma_f32_16x16x32_bf16 v[132:135],a[128:131],v[128:131],v[32:35]
ds_read_b128 a[176:179],v1 offset:12288
v_cvt_pk_bf16_f32 v214,v220,v221
v_mfma_f32_16x16x32_bf16 v[132:135],a[132:135],v[160:163],v[132:135]
v_exp_f32 v222,v222
v_mfma_f32_16x16x32_bf16 v[132:135],a[136:139],v[192:195],v[132:135]
v_exp_f32 v223,v223
v_mfma_f32_16x16x32_bf16 v[132:135],a[140:143],v[224:227],v[132:135]
buffer_load_dwordx4 v9,s[72:75],s80 offen offset:2048 lds
v_mfma_f32_16x16x32_bf16 v[140:143],a[156:159],v[224:227],v[32:35]
v_cvt_pk_bf16_f32 v215,v222,v223
v_add_u32 v1,s48,v1
v_mfma_f32_16x16x32_bf16 v[140:143],a[152:155],v[192:195],v[140:143]
v_exp_f32 v228,v228
v_mfma_f32_16x16x32_bf16 v[140:143],a[148:151],v[160:163],v[140:143]
v_exp_f32 v229,v229
v_mfma_f32_16x16x32_bf16 v[140:143],a[144:147],v[128:131],v[140:143]
v_exp_f32 v230,v230
v_mfma_f32_16x16x32_bf16 v[148:151],a[160:163],v[128:131],v[32:35]
v_exp_f32 v231,v231
v_mfma_f32_16x16x32_bf16 v[148:151],a[164:167],v[160:163],v[148:151]
v_cvt_pk_bf16_f32 v228,v228,v229
v_cvt_pk_bf16_f32 v229,v230,v231
v_mfma_f32_16x16x32_bf16 v[148:151],a[168:171],v[192:195],v[148:151]
v_exp_f32 v236,v236
v_mfma_f32_16x16x32_bf16 v[148:151],a[172:175],v[224:227],v[148:151]
s_waitcnt lgkmcnt(0)
buffer_store_dwordx4 v[104:107],v6,s[36:39],s86 offen offset:2048 sc1
v_mfma_f32_16x16x32_bf16 v[156:159],a[188:191],v[224:227],v[32:35]
v_exp_f32 v237,v237
v_mfma_f32_16x16x32_bf16 v[156:159],a[184:187],v[192:195],v[156:159]
v_exp_f32 v238,v238
v_mfma_f32_16x16x32_bf16 v[156:159],a[180:183],v[160:163],v[156:159]
v_exp_f32 v239,v239
v_mfma_f32_16x16x32_bf16 v[156:159],a[176:179],v[128:131],v[156:159]
v_exp_f32 v244,v244
v_mfma_f32_16x16x32_bf16 a[64:67],a[192:195],v[196:199],a[64:67]
buffer_load_dwordx4 v10,s[72:75],s80 offen offset:3072 lds
v_mfma_f32_16x16x32_bf16 a[64:67],a[196:199],v[212:215],a[64:67]
v_exp_f32 v245,v245
v_mfma_f32_16x16x32_bf16 a[68:71],a[204:207],v[212:215],a[68:71]
v_exp_f32 v246,v246
v_mfma_f32_16x16x32_bf16 a[68:71],a[200:203],v[196:199],a[68:71]
v_exp_f32 v247,v247
v_mfma_f32_16x16x32_bf16 a[72:75],a[208:211],v[196:199],a[72:75]
v_cvt_pk_bf16_f32 v230,v236,v237
v_cvt_pk_bf16_f32 v231,v238,v239
v_mfma_f32_16x16x32_bf16 a[72:75],a[212:215],v[212:215],a[72:75]
v_cvt_pk_bf16_f32 v244,v244,v245
v_cvt_pk_bf16_f32 v245,v246,v247
v_mfma_f32_16x16x32_bf16 a[76:79],a[220:223],v[212:215],a[76:79]
v_exp_f32 v252,v252
v_mfma_f32_16x16x32_bf16 a[76:79],a[216:219],v[196:199],a[76:79]
v_exp_f32 v253,v253
v_mfma_f32_16x16x32_bf16 a[80:83],a[224:227],v[196:199],a[80:83]
v_exp_f32 v254,v254
v_mfma_f32_16x16x32_bf16 a[80:83],a[228:231],v[212:215],a[80:83]
v_exp_f32 v255,v255
v_mfma_f32_16x16x32_bf16 a[84:87],a[236:239],v[212:215],a[84:87]
s_add_u32 m0,s82,86016
v_cvt_pk_bf16_f32 v246,v252,v253
v_cvt_pk_bf16_f32 v247,v254,v255
v_mfma_f32_16x16x32_bf16 a[84:87],a[232:235],v[196:199],a[84:87]
buffer_store_dwordx4 v[108:111],v6,s[36:39],s86 offen offset:3072 sc1
v_mfma_f32_16x16x32_bf16 a[88:91],a[240:243],v[196:199],a[88:91]
s_add_u32 s86,s55,73728
v_exp_f32 v132,v132
v_mfma_f32_16x16x32_bf16 a[88:91],a[244:247],v[212:215],a[88:91]
s_max_i32 s86,s86,s100
v_exp_f32 v133,v133
v_mfma_f32_16x16x32_bf16 a[92:95],a[252:255],v[212:215],a[92:95]
v_exp_f32 v134,v134
v_mfma_f32_16x16x32_bf16 a[92:95],a[248:251],v[196:199],a[92:95]
buffer_load_dwordx4 v11,s[72:75],s80 offen lds
v_mfma_f32_16x16x32_bf16 v[56:59],v[28:31],v[196:199],v[56:59]
v_exp_f32 v135,v135
v_mfma_f32_16x16x32_bf16 v[56:59],v[28:31],v[212:215],v[56:59]
v_cvt_pk_bf16_f32 v132,v132,v133
v_cvt_pk_bf16_f32 v133,v134,v135
v_mfma_f32_16x16x32_bf16 v[164:167],a[128:131],v[136:139],v[36:39]
v_exp_f32 v140,v140
v_mfma_f32_16x16x32_bf16 v[164:167],a[132:135],v[168:171],v[164:167]
v_exp_f32 v141,v141
v_mfma_f32_16x16x32_bf16 v[164:167],a[136:139],v[200:203],v[164:167]
v_exp_f32 v142,v142
v_mfma_f32_16x16x32_bf16 v[164:167],a[140:143],v[232:235],v[164:167]
v_exp_f32 v143,v143
v_mfma_f32_16x16x32_bf16 v[172:175],a[156:159],v[232:235],v[36:39]
v_exp_f32 v148,v148
v_mfma_f32_16x16x32_bf16 v[172:175],a[152:155],v[200:203],v[172:175]
s_add_u32 m0,s82,87040
v_exp_f32 v149,v149
v_mfma_f32_16x16x32_bf16 v[172:175],a[148:151],v[168:171],v[172:175]
v_exp_f32 v150,v150
v_mfma_f32_16x16x32_bf16 v[172:175],a[144:147],v[136:139],v[172:175]
v_exp_f32 v151,v151
v_mfma_f32_16x16x32_bf16 v[180:183],a[160:163],v[136:139],v[36:39]
buffer_load_dwordx4 v7,s[72:75],s83 offen lds
v_mfma_f32_16x16x32_bf16 v[180:183],a[164:167],v[168:171],v[180:183]
v_cvt_pk_bf16_f32 v134,v140,v141
v_cvt_pk_bf16_f32 v135,v142,v143
v_mfma_f32_16x16x32_bf16 v[180:183],a[168:171],v[200:203],v[180:183]
v_cvt_pk_bf16_f32 v148,v148,v149
v_cvt_pk_bf16_f32 v149,v150,v151
v_mfma_f32_16x16x32_bf16 v[180:183],a[172:175],v[232:235],v[180:183]
v_exp_f32 v156,v156
v_mfma_f32_16x16x32_bf16 v[188:191],a[188:191],v[232:235],v[36:39]
buffer_store_dwordx4 v[112:115],v6,s[36:39],s87 offen sc1
v_mfma_f32_16x16x32_bf16 v[188:191],a[184:187],v[200:203],v[188:191]
v_exp_f32 v157,v157
v_mfma_f32_16x16x32_bf16 v[188:191],a[180:183],v[168:171],v[188:191]
v_exp_f32 v158,v158
v_mfma_f32_16x16x32_bf16 v[188:191],a[176:179],v[136:139],v[188:191]
v_exp_f32 v159,v159
v_mfma_f32_16x16x32_bf16 a[96:99],a[192:195],v[228:231],a[96:99]
ds_read_b64_tr_b16 a[192:193],v2
ds_read_b64_tr_b16 a[194:195],v2 offset:4096
v_mfma_f32_16x16x32_bf16 a[96:99],a[196:199],v[244:247],a[96:99]
ds_read_b64_tr_b16 a[196:197],v2 offset:8192
v_cvt_pk_bf16_f32 v150,v156,v157
v_mfma_f32_16x16x32_bf16 a[100:103],a[204:207],v[244:247],a[100:103]
ds_read_b64_tr_b16 a[198:199],v2 offset:12288
v_cvt_pk_bf16_f32 v151,v158,v159
v_mfma_f32_16x16x32_bf16 a[100:103],a[200:203],v[228:231],a[100:103]
ds_read_b64_tr_b16 a[204:205],v3 offset:8192
v_add_u32 v1,v4,v5
v_mfma_f32_16x16x32_bf16 a[104:107],a[208:211],v[228:231],a[104:107]
ds_read_b64_tr_b16 a[206:207],v3 offset:12288
ds_read_b64_tr_b16 a[200:201],v3
v_mfma_f32_16x16x32_bf16 a[104:107],a[212:215],v[244:247],a[104:107]
buffer_load_dwordx4 v8,s[72:75],s83 offen offset:1024 lds
v_mfma_f32_16x16x32_bf16 a[108:111],a[220:223],v[244:247],a[108:111]
ds_read_b64_tr_b16 a[202:203],v3 offset:4096
ds_read_b64_tr_b16 a[208:209],v2 offset:256
v_mfma_f32_16x16x32_bf16 a[108:111],a[216:219],v[228:231],a[108:111]
ds_read_b64_tr_b16 a[210:211],v2 offset:4352
v_mfma_f32_16x16x32_bf16 a[112:115],a[224:227],v[228:231],a[112:115]
ds_read_b64_tr_b16 a[212:213],v2 offset:8448
v_mfma_f32_16x16x32_bf16 a[112:115],a[228:231],v[244:247],a[112:115]
buffer_store_dwordx4 v[116:119],v6,s[36:39],s87 offen offset:1024 sc1
v_mfma_f32_16x16x32_bf16 a[116:119],a[236:239],v[244:247],a[116:119]
ds_read_b64_tr_b16 a[214:215],v2 offset:12544
ds_read_b64_tr_b16 a[220:221],v3 offset:8448
v_mfma_f32_16x16x32_bf16 a[116:119],a[232:235],v[228:231],a[116:119]
ds_read_b64_tr_b16 a[222:223],v3 offset:12544
v_mfma_f32_16x16x32_bf16 a[120:123],a[240:243],v[228:231],a[120:123]
ds_read_b64_tr_b16 a[216:217],v3 offset:256
)A"
R"A(v_mfma_f32_16x16x32_bf16 a[120:123],a[244:247],v[244:247],a[120:123]
ds_read_b64_tr_b16 a[218:219],v3 offset:4352
v_mfma_f32_16x16x32_bf16 a[124:127],a[252:255],v[244:247],a[124:127]
ds_read_b64_tr_b16 a[224:225],v2 offset:512
ds_read_b64_tr_b16 a[226:227],v2 offset:4608
v_mfma_f32_16x16x32_bf16 a[124:127],a[248:251],v[228:231],a[124:127]
ds_read_b64_tr_b16 a[228:229],v2 offset:8704
v_mfma_f32_16x16x32_bf16 v[60:63],v[28:31],v[228:231],v[60:63]
ds_read_b64_tr_b16 a[230:231],v2 offset:12800
v_mfma_f32_16x16x32_bf16 v[60:63],v[28:31],v[244:247],v[60:63]
ds_read_b64_tr_b16 a[236:237],v3 offset:8704
v_mfma_f32_16x16x32_bf16 v[196:199],a[128:131],v[144:147],v[40:43]
buffer_load_dwordx4 v9,s[72:75],s83 offen offset:2048 lds
v_mfma_f32_16x16x32_bf16 v[196:199],a[132:135],v[176:179],v[196:199]
ds_read_b64_tr_b16 a[238:239],v3 offset:12800
ds_read_b64_tr_b16 a[232:233],v3 offset:512
v_mfma_f32_16x16x32_bf16 v[196:199],a[136:139],v[208:211],v[196:199]
ds_read_b64_tr_b16 a[234:235],v3 offset:4608
v_mfma_f32_16x16x32_bf16 v[196:199],a[140:143],v[240:243],v[196:199]
ds_read_b64_tr_b16 a[240:241],v2 offset:768
v_mfma_f32_16x16x32_bf16 v[204:207],a[156:159],v[240:243],v[40:43]
ds_read_b64_tr_b16 a[242:243],v2 offset:4864
v_mfma_f32_16x16x32_bf16 v[204:207],a[152:155],v[208:211],v[204:207]
ds_read_b64_tr_b16 a[244:245],v2 offset:8960
ds_read_b64_tr_b16 a[246:247],v2 offset:13056
v_mfma_f32_16x16x32_bf16 v[204:207],a[148:151],v[176:179],v[204:207]
ds_read_b64_tr_b16 a[252:253],v3 offset:8960
v_sub_u32 v2,v4,v5
v_mfma_f32_16x16x32_bf16 v[204:207],a[144:147],v[144:147],v[204:207]
buffer_store_dwordx4 v[120:123],v6,s[36:39],s87 offen offset:2048 sc1
v_mfma_f32_16x16x32_bf16 v[212:215],a[160:163],v[144:147],v[40:43]
ds_read_b64_tr_b16 a[254:255],v3 offset:13056
ds_read_b64_tr_b16 a[248:249],v3 offset:768
v_mfma_f32_16x16x32_bf16 v[212:215],a[164:167],v[176:179],v[212:215]
ds_read_b64_tr_b16 a[250:251],v3 offset:4864
v_mfma_f32_16x16x32_bf16 v[212:215],a[168:171],v[208:211],v[212:215]
v_exp_f32 v164,v164
v_mfma_f32_16x16x32_bf16 v[212:215],a[172:175],v[240:243],v[212:215]
v_exp_f32 v165,v165
v_mfma_f32_16x16x32_bf16 v[220:223],a[188:191],v[240:243],v[40:43]
v_exp_f32 v166,v166
v_mfma_f32_16x16x32_bf16 v[220:223],a[184:187],v[208:211],v[220:223]
buffer_load_dwordx4 v10,s[72:75],s83 offen offset:3072 lds
v_mfma_f32_16x16x32_bf16 v[220:223],a[180:183],v[176:179],v[220:223]
v_exp_f32 v167,v167
v_mfma_f32_16x16x32_bf16 v[220:223],a[176:179],v[144:147],v[220:223]
s_waitcnt lgkmcnt(3)
v_cvt_pk_bf16_f32 v164,v164,v165
v_cvt_pk_bf16_f32 v165,v166,v167
v_mfma_f32_16x16x32_bf16 a[0:3],a[192:195],v[132:135],a[0:3]
v_exp_f32 v172,v172
v_mfma_f32_16x16x32_bf16 a[0:3],a[196:199],v[148:151],a[0:3]
v_exp_f32 v173,v173
v_mfma_f32_16x16x32_bf16 a[4:7],a[204:207],v[148:151],a[4:7]
v_exp_f32 v174,v174
v_mfma_f32_16x16x32_bf16 a[4:7],a[200:203],v[132:135],a[4:7]
v_exp_f32 v175,v175
v_mfma_f32_16x16x32_bf16 a[8:11],a[208:211],v[132:135],a[8:11]
v_exp_f32 v180,v180
v_mfma_f32_16x16x32_bf16 a[8:11],a[212:215],v[148:151],a[8:11]
v_exp_f32 v181,v181
v_mfma_f32_16x16x32_bf16 a[12:15],a[220:223],v[148:151],a[12:15]
v_exp_f32 v182,v182
v_mfma_f32_16x16x32_bf16 a[12:15],a[216:219],v[132:135],a[12:15]
s_add_u32 m0,s82,91136
buffer_store_dwordx4 v[124:127],v6,s[36:39],s87 offen offset:3072 sc1
v_mfma_f32_16x16x32_bf16 a[16:19],a[224:227],v[132:135],a[16:19]
s_add_u32 s87,s55,77824
v_exp_f32 v183,v183
v_mfma_f32_16x16x32_bf16 a[16:19],a[228:231],v[148:151],a[16:19]
s_max_i32 s87,s87,s101
v_cvt_pk_bf16_f32 v166,v172,v173
v_cvt_pk_bf16_f32 v167,v174,v175
v_mfma_f32_16x16x32_bf16 a[20:23],a[236:239],v[148:151],a[20:23]
v_cvt_pk_bf16_f32 v180,v180,v181
v_cvt_pk_bf16_f32 v181,v182,v183
v_mfma_f32_16x16x32_bf16 a[20:23],a[232:235],v[132:135],a[20:23]
buffer_load_dwordx4 v11,s[72:75],s83 offen lds
v_mfma_f32_16x16x32_bf16 a[24:27],a[240:243],v[132:135],a[24:27]
v_exp_f32 v188,v188
v_mfma_f32_16x16x32_bf16 a[24:27],a[244:247],v[148:151],a[24:27]
s_waitcnt lgkmcnt(0)
v_exp_f32 v189,v189
v_mfma_f32_16x16x32_bf16 a[28:31],a[252:255],v[148:151],a[28:31]
v_exp_f32 v190,v190
v_mfma_f32_16x16x32_bf16 a[28:31],a[248:251],v[132:135],a[28:31]
v_exp_f32 v191,v191
v_mfma_f32_16x16x32_bf16 v[48:51],v[28:31],v[132:135],v[48:51]
s_waitcnt vmcnt(19)
v_cvt_pk_bf16_f32 v182,v188,v189
v_cvt_pk_bf16_f32 v183,v190,v191
v_mfma_f32_16x16x32_bf16 v[48:51],v[28:31],v[148:151],v[48:51]
s_barrier
s_cmp_ge_u32 1,s71
s_cselect_b32 s80,4,2
v_mfma_f32_16x16x32_bf16 v[228:231],a[128:131],v[152:155],v[44:47]
s_mul_i32 s80,s80,163840
s_add_u32 s80,s80,s81
v_accvgpr_read_b32 v132,a0
v_mfma_f32_16x16x32_bf16 v[228:231],a[132:135],v[184:187],v[228:231]
s_add_u32 s83,s80,20480
v_exp_f32 v196,v196
v_mfma_f32_16x16x32_bf16 v[228:231],a[136:139],v[216:219],v[228:231]
v_exp_f32 v197,v197
v_mfma_f32_16x16x32_bf16 v[228:231],a[140:143],v[248:251],v[228:231]
v_exp_f32 v198,v198
v_mfma_f32_16x16x32_bf16 v[236:239],a[156:159],v[248:251],v[44:47]
v_exp_f32 v199,v199
v_mfma_f32_16x16x32_bf16 v[236:239],a[152:155],v[216:219],v[236:239]
v_cvt_pk_bf16_f32 v196,v196,v197
v_cvt_pk_bf16_f32 v197,v198,v199
v_mfma_f32_16x16x32_bf16 v[236:239],a[148:151],v[184:187],v[236:239]
v_exp_f32 v204,v204
v_mfma_f32_16x16x32_bf16 v[236:239],a[144:147],v[152:155],v[236:239]
v_exp_f32 v205,v205
v_mfma_f32_16x16x32_bf16 v[244:247],a[160:163],v[152:155],v[44:47]
v_exp_f32 v206,v206
v_mfma_f32_16x16x32_bf16 v[244:247],a[164:167],v[184:187],v[244:247]
v_exp_f32 v207,v207
v_mfma_f32_16x16x32_bf16 v[244:247],a[168:171],v[216:219],v[244:247]
v_exp_f32 v212,v212
v_mfma_f32_16x16x32_bf16 v[244:247],a[172:175],v[248:251],v[244:247]
v_exp_f32 v213,v213
v_mfma_f32_16x16x32_bf16 v[252:255],a[188:191],v[248:251],v[44:47]
v_exp_f32 v214,v214
v_mfma_f32_16x16x32_bf16 v[252:255],a[184:187],v[216:219],v[252:255]
v_exp_f32 v215,v215
v_mfma_f32_16x16x32_bf16 v[252:255],a[180:183],v[184:187],v[252:255]
v_cvt_pk_bf16_f32 v198,v204,v205
v_cvt_pk_bf16_f32 v199,v206,v207
v_mfma_f32_16x16x32_bf16 v[252:255],a[176:179],v[152:155],v[252:255]
v_cvt_pk_bf16_f32 v212,v212,v213
v_cvt_pk_bf16_f32 v213,v214,v215
v_mfma_f32_16x16x32_bf16 a[32:35],a[192:195],v[164:167],a[32:35]
v_exp_f32 v220,v220
v_mfma_f32_16x16x32_bf16 a[32:35],a[196:199],v[180:183],a[32:35]
s_add_u32 m0,s82,122880
v_exp_f32 v221,v221
v_mfma_f32_16x16x32_bf16 a[36:39],a[204:207],v[180:183],a[36:39]
v_exp_f32 v222,v222
v_mfma_f32_16x16x32_bf16 a[36:39],a[200:203],v[164:167],a[36:39]
v_exp_f32 v223,v223
v_mfma_f32_16x16x32_bf16 a[40:43],a[208:211],v[164:167],a[40:43]
buffer_load_dwordx4 v7,s[72:75],s80 offen lds
v_mfma_f32_16x16x32_bf16 a[40:43],a[212:215],v[180:183],a[40:43]
v_cvt_pk_bf16_f32 v214,v220,v221
v_cvt_pk_bf16_f32 v215,v222,v223
v_mfma_f32_16x16x32_bf16 a[44:47],a[220:223],v[180:183],a[44:47]
v_max_f32 v20,0x00800000,v48
v_accvgpr_read_b32 v133,a1
v_mfma_f32_16x16x32_bf16 a[44:47],a[216:219],v[164:167],a[44:47]
v_rcp_f32 v20,v20
v_mfma_f32_16x16x32_bf16 a[48:51],a[224:227],v[164:167],a[48:51]
v_mul_f32 v132,v20,v132
v_mul_f32 v133,v20,v133
v_mfma_f32_16x16x32_bf16 a[48:51],a[228:231],v[180:183],a[48:51]
v_accvgpr_read_b32 v134,a2
v_accvgpr_read_b32 v135,a3
v_mfma_f32_16x16x32_bf16 a[52:55],a[236:239],v[180:183],a[52:55]
v_cvt_pk_bf16_f32 v132,v132,v133
v_mul_f32 v134,v20,v134
v_mfma_f32_16x16x32_bf16 a[52:55],a[232:235],v[164:167],a[52:55]
v_mul_f32 v135,v20,v135
v_cvt_pk_bf16_f32 v133,v134,v135
v_mfma_f32_16x16x32_bf16 a[56:59],a[240:243],v[164:167],a[56:59]
v_exp_f32 v228,v228
v_mfma_f32_16x16x32_bf16 a[56:59],a[244:247],v[180:183],a[56:59]
v_exp_f32 v229,v229
v_mfma_f32_16x16x32_bf16 a[60:63],a[252:255],v[180:183],a[60:63]
v_accvgpr_read_b32 v134,a4
v_accvgpr_read_b32 v135,a5
v_mfma_f32_16x16x32_bf16 a[60:63],a[248:251],v[164:167],a[60:63]
v_exp_f32 v230,v230
v_mfma_f32_16x16x32_bf16 v[52:55],v[28:31],v[164:167],v[52:55]
v_exp_f32 v231,v231
v_mfma_f32_16x16x32_bf16 v[52:55],v[28:31],v[180:183],v[52:55]
v_accvgpr_read_b32 v16,a6
v_accvgpr_read_b32 v17,a7
v_mfma_f32_16x16x32_bf16 a[64:67],a[192:195],v[196:199],a[64:67]
v_mul_f32 v134,v20,v134
v_mul_f32 v135,v20,v135
v_mfma_f32_16x16x32_bf16 a[64:67],a[196:199],v[212:215],a[64:67]
v_cvt_pk_bf16_f32 v228,v228,v229
v_mul_f32 v16,v20,v16
v_mfma_f32_16x16x32_bf16 a[68:71],a[204:207],v[212:215],a[68:71]
buffer_load_dwordx4 v8,s[72:75],s80 offen offset:1024 lds
v_mfma_f32_16x16x32_bf16 a[68:71],a[200:203],v[196:199],a[68:71]
v_exp_f32 v236,v236
v_mfma_f32_16x16x32_bf16 a[72:75],a[208:211],v[196:199],a[72:75]
s_lshl_b32 s95,s42,2
s_add_u32 s95,s95,s40
s_lshl_b32 s95,s95,4
v_mfma_f32_16x16x32_bf16 a[72:75],a[212:215],v[212:215],a[72:75]
v_mov_b32 v18,s68
v_mov_b32 v19,s69
v_mfma_f32_16x16x32_bf16 a[76:79],a[220:223],v[212:215],a[76:79]
v_exp_f32 v237,v237
v_mfma_f32_16x16x32_bf16 a[76:79],a[216:219],v[196:199],a[76:79]
v_exp_f32 v238,v238
v_mfma_f32_16x16x32_bf16 a[80:83],a[224:227],v[196:199],a[80:83]
v_exp_f32 v239,v239
v_mfma_f32_16x16x32_bf16 a[80:83],a[228:231],v[212:215],a[80:83]
v_exp_f32 v244,v244
v_mfma_f32_16x16x32_bf16 a[84:87],a[236:239],v[212:215],a[84:87]
v_exp_f32 v245,v245
v_mfma_f32_16x16x32_bf16 a[84:87],a[232:235],v[196:199],a[84:87]
v_mul_f32 v17,v20,v17
v_cvt_pk_bf16_f32 v134,v134,v135
v_mfma_f32_16x16x32_bf16 a[88:91],a[240:243],v[196:199],a[88:91]
s_waitcnt vmcnt(3)
s_mov_b64 exec,1
buffer_store_dwordx2 v[18:19],v6,s[64:67],s95 offen sc1
s_mov_b64 exec,-1
v_mfma_f32_16x16x32_bf16 a[88:91],a[244:247],v[212:215],a[88:91]
v_cvt_pk_bf16_f32 v229,v230,v231
v_cvt_pk_bf16_f32 v135,v16,v17
v_mfma_f32_16x16x32_bf16 a[92:95],a[252:255],v[212:215],a[92:95]
v_exp_f32 v246,v246
v_mfma_f32_16x16x32_bf16 a[92:95],a[248:251],v[196:199],a[92:95]
v_exp_f32 v247,v247
v_cvt_pk_bf16_f32 v230,v236,v237
v_cvt_pk_bf16_f32 v231,v238,v239
v_cvt_pk_bf16_f32 v244,v244,v245
v_exp_f32 v252,v252
v_exp_f32 v253,v253
v_exp_f32 v254,v254
v_exp_f32 v255,v255
v_mfma_f32_16x16x32_bf16 v[56:59],v[28:31],v[196:199],v[56:59]
v_cvt_pk_bf16_f32 v245,v246,v247
v_cvt_pk_bf16_f32 v246,v252,v253
v_cvt_pk_bf16_f32 v247,v254,v255
v_mfma_f32_16x16x32_bf16 v[56:59],v[28:31],v[212:215],v[56:59]
buffer_store_dwordx4 v[132:135],v6,s[36:39],s84 offen sc1
v_mfma_f32_16x16x32_bf16 a[96:99],a[192:195],v[228:231],a[96:99]
v_accvgpr_read_b32 v140,a8
v_accvgpr_read_b32 v141,a9
v_mfma_f32_16x16x32_bf16 a[96:99],a[196:199],v[244:247],a[96:99]
v_mul_f32 v140,v20,v140
v_mul_f32 v141,v20,v141
v_mfma_f32_16x16x32_bf16 a[100:103],a[204:207],v[244:247],a[100:103]
v_accvgpr_read_b32 v142,a10
v_accvgpr_read_b32 v143,a11
v_mfma_f32_16x16x32_bf16 a[100:103],a[200:203],v[228:231],a[100:103]
buffer_load_dwordx4 v9,s[72:75],s80 offen offset:2048 lds
v_mfma_f32_16x16x32_bf16 a[104:107],a[208:211],v[228:231],a[104:107]
v_cvt_pk_bf16_f32 v140,v140,v141
v_mul_f32 v142,v20,v142
v_mfma_f32_16x16x32_bf16 a[104:107],a[212:215],v[244:247],a[104:107]
v_mul_f32 v143,v20,v143
v_cvt_pk_bf16_f32 v141,v142,v143
v_mfma_f32_16x16x32_bf16 a[108:111],a[220:223],v[244:247],a[108:111]
v_accvgpr_read_b32 v142,a12
v_accvgpr_read_b32 v143,a13
v_mfma_f32_16x16x32_bf16 a[108:111],a[216:219],v[228:231],a[108:111]
v_accvgpr_read_b32 v12,a14
v_accvgpr_read_b32 v13,a15
v_mfma_f32_16x16x32_bf16 a[112:115],a[224:227],v[228:231],a[112:115]
v_mul_f32 v142,v20,v142
v_mul_f32 v143,v20,v143
v_mfma_f32_16x16x32_bf16 a[112:115],a[228:231],v[244:247],a[112:115]
ds_read_b64_tr_b16 a[128:129],v1
ds_read_b64_tr_b16 a[130:131],v1 offset:5120
v_mfma_f32_16x16x32_bf16 a[116:119],a[236:239],v[244:247],a[116:119]
ds_read_b64_tr_b16 a[132:133],v1 offset:10240
ds_read_b64_tr_b16 a[134:135],v1 offset:15360
v_mfma_f32_16x16x32_bf16 a[116:119],a[232:235],v[228:231],a[116:119]
ds_read_b64_tr_b16 a[136:137],v1 offset:20480
ds_read_b64_tr_b16 a[138:139],v1 offset:25600
v_mfma_f32_16x16x32_bf16 a[120:123],a[240:243],v[228:231],a[120:123]
ds_read_b64_tr_b16 a[140:141],v1 offset:30720
ds_read_b64_tr_b16 a[142:143],v1 offset:35840
v_mfma_f32_16x16x32_bf16 a[120:123],a[244:247],v[244:247],a[120:123]
v_mfma_f32_16x16x32_bf16 a[124:127],a[252:255],v[244:247],a[124:127]
v_mul_f32 v12,v20,v12
v_mul_f32 v13,v20,v13
v_mfma_f32_16x16x32_bf16 a[124:127],a[248:251],v[228:231],a[124:127]
)A"
R"A(v_cvt_pk_bf16_f32 v142,v142,v143
v_cvt_pk_bf16_f32 v143,v12,v13
v_mfma_f32_16x16x32_bf16 v[60:63],v[28:31],v[228:231],v[60:63]
v_accvgpr_read_b32 v148,a16
v_accvgpr_read_b32 v149,a17
v_mfma_f32_16x16x32_bf16 v[60:63],v[28:31],v[244:247],v[60:63]
s_waitcnt lgkmcnt(6)
buffer_store_dwordx4 v[140:143],v6,s[36:39],s84 offen offset:1024 sc1
v_mfma_f32_16x16x32_bf16 a[192:195],a[128:131],v[64:67],0
s_waitcnt lgkmcnt(4)
v_mul_f32 v148,v20,v148
v_mul_f32 v149,v20,v149
v_mfma_f32_16x16x32_bf16 a[192:195],a[132:135],v[68:71],a[192:195]
s_waitcnt lgkmcnt(2)
v_accvgpr_read_b32 v150,a18
v_accvgpr_read_b32 v151,a19
v_mfma_f32_16x16x32_bf16 a[192:195],a[136:139],v[72:75],a[192:195]
s_waitcnt lgkmcnt(0)
v_cvt_pk_bf16_f32 v148,v148,v149
v_mul_f32 v150,v20,v150
v_mfma_f32_16x16x32_bf16 a[192:195],a[140:143],v[76:79],a[192:195]
buffer_load_dwordx4 v10,s[72:75],s80 offen offset:3072 lds
v_mfma_f32_16x16x32_bf16 a[196:199],a[140:143],v[92:95],0
v_mul_f32 v151,v20,v151
v_cvt_pk_bf16_f32 v149,v150,v151
v_mfma_f32_16x16x32_bf16 a[196:199],a[136:139],v[88:91],a[196:199]
v_accvgpr_read_b32 v150,a20
v_accvgpr_read_b32 v151,a21
v_mfma_f32_16x16x32_bf16 a[196:199],a[132:135],v[84:87],a[196:199]
v_accvgpr_read_b32 v14,a22
v_accvgpr_read_b32 v15,a23
v_mfma_f32_16x16x32_bf16 a[196:199],a[128:131],v[80:83],a[196:199]
ds_read_b64_tr_b16 a[144:145],v2 offset:32
ds_read_b64_tr_b16 a[146:147],v2 offset:5152
v_mfma_f32_16x16x32_bf16 a[200:203],a[128:131],v[96:99],0
ds_read_b64_tr_b16 a[148:149],v2 offset:10272
ds_read_b64_tr_b16 a[150:151],v2 offset:15392
v_mfma_f32_16x16x32_bf16 a[200:203],a[132:135],v[100:103],a[200:203]
ds_read_b64_tr_b16 a[152:153],v2 offset:20512
ds_read_b64_tr_b16 a[154:155],v2 offset:25632
v_mfma_f32_16x16x32_bf16 a[200:203],a[136:139],v[104:107],a[200:203]
ds_read_b64_tr_b16 a[156:157],v2 offset:30752
ds_read_b64_tr_b16 a[158:159],v2 offset:35872
v_mfma_f32_16x16x32_bf16 a[200:203],a[140:143],v[108:111],a[200:203]
v_mfma_f32_16x16x32_bf16 a[204:207],a[140:143],v[124:127],0
s_add_u32 m0,s82,126976
v_mul_f32 v150,v20,v150
v_mul_f32 v151,v20,v151
v_mfma_f32_16x16x32_bf16 a[204:207],a[136:139],v[120:123],a[204:207]
v_mul_f32 v14,v20,v14
v_mul_f32 v15,v20,v15
v_mfma_f32_16x16x32_bf16 a[204:207],a[132:135],v[116:119],a[204:207]
v_cvt_pk_bf16_f32 v150,v150,v151
v_cvt_pk_bf16_f32 v151,v14,v15
v_mfma_f32_16x16x32_bf16 a[204:207],a[128:131],v[112:115],a[204:207]
s_waitcnt lgkmcnt(6)
v_accvgpr_read_b32 v156,a24
v_accvgpr_read_b32 v157,a25
v_mfma_f32_16x16x32_bf16 a[208:211],a[144:147],v[64:67],0
s_waitcnt lgkmcnt(4)
v_mul_f32 v156,v20,v156
v_mul_f32 v157,v20,v157
v_mfma_f32_16x16x32_bf16 a[208:211],a[148:151],v[68:71],a[208:211]
s_waitcnt lgkmcnt(2)
buffer_load_dwordx4 v11,s[72:75],s80 offen lds
v_mfma_f32_16x16x32_bf16 a[208:211],a[152:155],v[72:75],a[208:211]
s_waitcnt lgkmcnt(0)
v_accvgpr_read_b32 v158,a26
v_accvgpr_read_b32 v159,a27
v_mfma_f32_16x16x32_bf16 a[208:211],a[156:159],v[76:79],a[208:211]
v_cvt_pk_bf16_f32 v156,v156,v157
v_mul_f32 v158,v20,v158
v_mfma_f32_16x16x32_bf16 a[212:215],a[156:159],v[92:95],0
v_mul_f32 v159,v20,v159
v_cvt_pk_bf16_f32 v157,v158,v159
v_mfma_f32_16x16x32_bf16 a[212:215],a[152:155],v[88:91],a[212:215]
buffer_store_dwordx4 v[148:151],v6,s[36:39],s84 offen offset:2048 sc1
v_mfma_f32_16x16x32_bf16 a[212:215],a[148:151],v[84:87],a[212:215]
v_accvgpr_read_b32 v158,a28
v_accvgpr_read_b32 v159,a29
v_mfma_f32_16x16x32_bf16 a[212:215],a[144:147],v[80:83],a[212:215]
ds_read_b64_tr_b16 a[160:161],v1 offset:64
ds_read_b64_tr_b16 a[162:163],v1 offset:5184
v_mfma_f32_16x16x32_bf16 a[216:219],a[144:147],v[96:99],0
ds_read_b64_tr_b16 a[164:165],v1 offset:10304
ds_read_b64_tr_b16 a[166:167],v1 offset:15424
v_mfma_f32_16x16x32_bf16 a[216:219],a[148:151],v[100:103],a[216:219]
ds_read_b64_tr_b16 a[168:169],v1 offset:20544
ds_read_b64_tr_b16 a[170:171],v1 offset:25664
v_mfma_f32_16x16x32_bf16 a[216:219],a[152:155],v[104:107],a[216:219]
ds_read_b64_tr_b16 a[172:173],v1 offset:30784
ds_read_b64_tr_b16 a[174:175],v1 offset:35904
v_mfma_f32_16x16x32_bf16 a[216:219],a[156:159],v[108:111],a[216:219]
v_mfma_f32_16x16x32_bf16 a[220:223],a[156:159],v[124:127],0
s_add_u32 m0,s82,128000
v_accvgpr_read_b32 v16,a30
v_accvgpr_read_b32 v17,a31
v_mfma_f32_16x16x32_bf16 a[220:223],a[152:155],v[120:123],a[220:223]
v_mul_f32 v158,v20,v158
v_mul_f32 v159,v20,v159
v_mfma_f32_16x16x32_bf16 a[220:223],a[148:151],v[116:119],a[220:223]
v_mul_f32 v16,v20,v16
v_mul_f32 v17,v20,v17
v_mfma_f32_16x16x32_bf16 a[220:223],a[144:147],v[112:115],a[220:223]
s_waitcnt lgkmcnt(6)
v_cvt_pk_bf16_f32 v158,v158,v159
v_cvt_pk_bf16_f32 v159,v16,v17
v_mfma_f32_16x16x32_bf16 a[224:227],a[160:163],v[64:67],0
s_waitcnt lgkmcnt(4)
v_max_f32 v21,0x00800000,v52
v_accvgpr_read_b32 v164,a32
v_mfma_f32_16x16x32_bf16 a[224:227],a[164:167],v[68:71],a[224:227]
s_waitcnt lgkmcnt(2)
buffer_load_dwordx4 v7,s[72:75],s83 offen lds
v_mfma_f32_16x16x32_bf16 a[224:227],a[168:171],v[72:75],a[224:227]
s_waitcnt lgkmcnt(0)
v_rcp_f32 v21,v21
v_mfma_f32_16x16x32_bf16 a[224:227],a[172:175],v[76:79],a[224:227]
v_accvgpr_read_b32 v165,a33
v_mul_f32 v164,v21,v164
v_mfma_f32_16x16x32_bf16 a[228:231],a[172:175],v[92:95],0
v_mul_f32 v165,v21,v165
v_accvgpr_read_b32 v166,a34
v_mfma_f32_16x16x32_bf16 a[228:231],a[168:171],v[88:91],a[228:231]
v_accvgpr_read_b32 v167,a35
v_cvt_pk_bf16_f32 v164,v164,v165
v_mfma_f32_16x16x32_bf16 a[228:231],a[164:167],v[84:87],a[228:231]
v_mul_f32 v166,v21,v166
v_mul_f32 v167,v21,v167
v_mfma_f32_16x16x32_bf16 a[228:231],a[160:163],v[80:83],a[228:231]
ds_read_b64_tr_b16 a[176:177],v2 offset:96
ds_read_b64_tr_b16 a[178:179],v2 offset:5216
v_mfma_f32_16x16x32_bf16 a[232:235],a[160:163],v[96:99],0
ds_read_b64_tr_b16 a[180:181],v2 offset:10336
ds_read_b64_tr_b16 a[182:183],v2 offset:15456
v_mfma_f32_16x16x32_bf16 a[232:235],a[164:167],v[100:103],a[232:235]
ds_read_b64_tr_b16 a[184:185],v2 offset:20576
ds_read_b64_tr_b16 a[186:187],v2 offset:25696
v_mfma_f32_16x16x32_bf16 a[232:235],a[168:171],v[104:107],a[232:235]
ds_read_b64_tr_b16 a[188:189],v2 offset:30816
ds_read_b64_tr_b16 a[190:191],v2 offset:35936
v_mfma_f32_16x16x32_bf16 a[232:235],a[172:175],v[108:111],a[232:235]
v_mfma_f32_16x16x32_bf16 a[236:239],a[172:175],v[124:127],0
buffer_store_dwordx4 v[156:159],v6,s[36:39],s84 offen offset:3072 sc1
v_mfma_f32_16x16x32_bf16 a[236:239],a[168:171],v[120:123],a[236:239]
v_cvt_pk_bf16_f32 v165,v166,v167
v_accvgpr_read_b32 v166,a36
v_mfma_f32_16x16x32_bf16 a[236:239],a[164:167],v[116:119],a[236:239]
v_accvgpr_read_b32 v167,a37
v_accvgpr_read_b32 v12,a38
v_mfma_f32_16x16x32_bf16 a[236:239],a[160:163],v[112:115],a[236:239]
s_waitcnt lgkmcnt(6)
v_accvgpr_read_b32 v13,a39
v_mul_f32 v166,v21,v166
v_mfma_f32_16x16x32_bf16 a[240:243],a[176:179],v[64:67],0
s_waitcnt lgkmcnt(4)
v_mul_f32 v167,v21,v167
v_mul_f32 v12,v21,v12
v_mfma_f32_16x16x32_bf16 a[240:243],a[180:183],v[68:71],a[240:243]
s_waitcnt lgkmcnt(2)
buffer_load_dwordx4 v8,s[72:75],s83 offen offset:1024 lds
v_mfma_f32_16x16x32_bf16 a[240:243],a[184:187],v[72:75],a[240:243]
s_waitcnt lgkmcnt(0)
v_mul_f32 v13,v21,v13
v_cvt_pk_bf16_f32 v166,v166,v167
v_mfma_f32_16x16x32_bf16 a[240:243],a[188:191],v[76:79],a[240:243]
v_cvt_pk_bf16_f32 v167,v12,v13
v_accvgpr_read_b32 v172,a40
v_mfma_f32_16x16x32_bf16 a[244:247],a[188:191],v[92:95],0
v_accvgpr_read_b32 v173,a41
v_mul_f32 v172,v21,v172
v_mfma_f32_16x16x32_bf16 a[244:247],a[184:187],v[88:91],a[244:247]
v_mul_f32 v173,v21,v173
v_accvgpr_read_b32 v174,a42
v_mfma_f32_16x16x32_bf16 a[244:247],a[180:183],v[84:87],a[244:247]
v_accvgpr_read_b32 v175,a43
v_cvt_pk_bf16_f32 v172,v172,v173
v_mfma_f32_16x16x32_bf16 a[244:247],a[176:179],v[80:83],a[244:247]
ds_read_b64_tr_b16 a[128:129],v1 offset:128
ds_read_b64_tr_b16 a[130:131],v1 offset:5248
v_mfma_f32_16x16x32_bf16 a[248:251],a[176:179],v[96:99],0
ds_read_b64_tr_b16 a[132:133],v1 offset:10368
ds_read_b64_tr_b16 a[134:135],v1 offset:15488
v_mfma_f32_16x16x32_bf16 a[248:251],a[180:183],v[100:103],a[248:251]
ds_read_b64_tr_b16 a[136:137],v1 offset:20608
ds_read_b64_tr_b16 a[138:139],v1 offset:25728
v_mfma_f32_16x16x32_bf16 a[248:251],a[184:187],v[104:107],a[248:251]
ds_read_b64_tr_b16 a[140:141],v1 offset:30848
ds_read_b64_tr_b16 a[142:143],v1 offset:35968
v_mfma_f32_16x16x32_bf16 a[248:251],a[188:191],v[108:111],a[248:251]
v_mfma_f32_16x16x32_bf16 a[252:255],a[188:191],v[124:127],0
buffer_store_dwordx4 v[164:167],v6,s[36:39],s85 offen sc1
v_mfma_f32_16x16x32_bf16 a[252:255],a[184:187],v[120:123],a[252:255]
v_mul_f32 v174,v21,v174
v_mul_f32 v175,v21,v175
v_mfma_f32_16x16x32_bf16 a[252:255],a[180:183],v[116:119],a[252:255]
v_cvt_pk_bf16_f32 v173,v174,v175
v_accvgpr_read_b32 v174,a44
v_mfma_f32_16x16x32_bf16 a[252:255],a[176:179],v[112:115],a[252:255]
s_waitcnt lgkmcnt(6)
v_accvgpr_read_b32 v175,a45
v_accvgpr_read_b32 v14,a46
v_mfma_f32_16x16x32_bf16 a[0:3],a[128:131],v[64:67],0
s_waitcnt lgkmcnt(4)
v_accvgpr_read_b32 v15,a47
v_mul_f32 v174,v21,v174
v_mfma_f32_16x16x32_bf16 a[0:3],a[132:135],v[68:71],a[0:3]
s_waitcnt lgkmcnt(2)
buffer_load_dwordx4 v9,s[72:75],s83 offen offset:2048 lds
v_mfma_f32_16x16x32_bf16 a[0:3],a[136:139],v[72:75],a[0:3]
s_waitcnt lgkmcnt(0)
v_mul_f32 v175,v21,v175
v_mul_f32 v14,v21,v14
v_mfma_f32_16x16x32_bf16 a[0:3],a[140:143],v[76:79],a[0:3]
v_mul_f32 v15,v21,v15
v_cvt_pk_bf16_f32 v174,v174,v175
v_mfma_f32_16x16x32_bf16 a[4:7],a[140:143],v[92:95],0
v_cvt_pk_bf16_f32 v175,v14,v15
v_accvgpr_read_b32 v180,a48
v_mfma_f32_16x16x32_bf16 a[4:7],a[136:139],v[88:91],a[4:7]
v_accvgpr_read_b32 v181,a49
v_mul_f32 v180,v21,v180
v_mfma_f32_16x16x32_bf16 a[4:7],a[132:135],v[84:87],a[4:7]
s_lshl_b32 s94,s70,4
v_mul_f32 v181,v21,v181
v_accvgpr_read_b32 v182,a50
v_mfma_f32_16x16x32_bf16 a[4:7],a[128:131],v[80:83],a[4:7]
s_add_u32 s94,s94,s40
ds_read_b64_tr_b16 a[144:145],v2 offset:160
ds_read_b64_tr_b16 a[146:147],v2 offset:5280
v_mfma_f32_16x16x32_bf16 a[8:11],a[128:131],v[96:99],0
ds_read_b64_tr_b16 a[148:149],v2 offset:10400
ds_read_b64_tr_b16 a[150:151],v2 offset:15520
v_mfma_f32_16x16x32_bf16 a[8:11],a[132:135],v[100:103],a[8:11]
ds_read_b64_tr_b16 a[152:153],v2 offset:20640
ds_read_b64_tr_b16 a[154:155],v2 offset:25760
v_mfma_f32_16x16x32_bf16 a[8:11],a[136:139],v[104:107],a[8:11]
ds_read_b64_tr_b16 a[156:157],v2 offset:30880
ds_read_b64_tr_b16 a[158:159],v2 offset:36000
v_mfma_f32_16x16x32_bf16 a[8:11],a[140:143],v[108:111],a[8:11]
v_mfma_f32_16x16x32_bf16 a[12:15],a[140:143],v[124:127],0
s_lshl_b32 s94,s94,4
v_accvgpr_read_b32 v183,a51
v_cvt_pk_bf16_f32 v180,v180,v181
v_mfma_f32_16x16x32_bf16 a[12:15],a[136:139],v[120:123],a[12:15]
v_mul_f32 v182,v21,v182
v_mul_f32 v183,v21,v183
v_mfma_f32_16x16x32_bf16 a[12:15],a[132:135],v[116:119],a[12:15]
v_cvt_pk_bf16_f32 v181,v182,v183
v_accvgpr_read_b32 v182,a52
v_mfma_f32_16x16x32_bf16 a[12:15],a[128:131],v[112:115],a[12:15]
s_waitcnt lgkmcnt(6)
v_accvgpr_read_b32 v183,a53
v_accvgpr_read_b32 v16,a54
v_mfma_f32_16x16x32_bf16 a[16:19],a[144:147],v[64:67],0
s_waitcnt lgkmcnt(4)
buffer_store_dwordx4 v[172:175],v6,s[36:39],s85 offen offset:1024 sc1
v_mfma_f32_16x16x32_bf16 a[16:19],a[148:151],v[68:71],a[16:19]
s_waitcnt lgkmcnt(2)
v_accvgpr_read_b32 v17,a55
v_mul_f32 v182,v21,v182
v_mfma_f32_16x16x32_bf16 a[16:19],a[152:155],v[72:75],a[16:19]
s_waitcnt lgkmcnt(0)
v_mul_f32 v183,v21,v183
v_mul_f32 v16,v21,v16
v_mfma_f32_16x16x32_bf16 a[16:19],a[156:159],v[76:79],a[16:19]
v_mul_f32 v17,v21,v17
v_cvt_pk_bf16_f32 v182,v182,v183
v_mfma_f32_16x16x32_bf16 a[20:23],a[156:159],v[92:95],0
buffer_load_dwordx4 v10,s[72:75],s83 offen offset:3072 lds
v_mfma_f32_16x16x32_bf16 a[20:23],a[152:155],v[88:91],a[20:23]
v_cvt_pk_bf16_f32 v183,v16,v17
v_accvgpr_read_b32 v188,a56
v_mfma_f32_16x16x32_bf16 a[20:23],a[148:151],v[84:87],a[20:23]
v_accvgpr_read_b32 v189,a57
v_mul_f32 v188,v21,v188
v_mfma_f32_16x16x32_bf16 a[20:23],a[144:147],v[80:83],a[20:23]
ds_read_b64_tr_b16 a[160:161],v1 offset:192
ds_read_b64_tr_b16 a[162:163],v1 offset:5312
v_mfma_f32_16x16x32_bf16 a[24:27],a[144:147],v[96:99],0
ds_read_b64_tr_b16 a[164:165],v1 offset:10432
ds_read_b64_tr_b16 a[166:167],v1 offset:15552
)A"
R"A(v_mfma_f32_16x16x32_bf16 a[24:27],a[148:151],v[100:103],a[24:27]
ds_read_b64_tr_b16 a[168:169],v1 offset:20672
ds_read_b64_tr_b16 a[170:171],v1 offset:25792
v_mfma_f32_16x16x32_bf16 a[24:27],a[152:155],v[104:107],a[24:27]
ds_read_b64_tr_b16 a[172:173],v1 offset:30912
ds_read_b64_tr_b16 a[174:175],v1 offset:36032
v_mfma_f32_16x16x32_bf16 a[24:27],a[156:159],v[108:111],a[24:27]
v_mfma_f32_16x16x32_bf16 a[28:31],a[156:159],v[124:127],0
s_add_u32 m0,s82,132096
v_mul_f32 v189,v21,v189
v_accvgpr_read_b32 v190,a58
v_mfma_f32_16x16x32_bf16 a[28:31],a[152:155],v[120:123],a[28:31]
v_accvgpr_read_b32 v191,a59
v_cvt_pk_bf16_f32 v188,v188,v189
v_mfma_f32_16x16x32_bf16 a[28:31],a[148:151],v[116:119],a[28:31]
v_mul_f32 v190,v21,v190
v_mul_f32 v191,v21,v191
v_mfma_f32_16x16x32_bf16 a[28:31],a[144:147],v[112:115],a[28:31]
s_waitcnt lgkmcnt(6)
v_cvt_pk_bf16_f32 v189,v190,v191
v_accvgpr_read_b32 v190,a60
v_mfma_f32_16x16x32_bf16 a[32:35],a[160:163],v[64:67],0
s_waitcnt lgkmcnt(4)
v_accvgpr_read_b32 v191,a61
v_accvgpr_read_b32 v12,a62
v_mfma_f32_16x16x32_bf16 a[32:35],a[164:167],v[68:71],a[32:35]
s_waitcnt lgkmcnt(2)
buffer_load_dwordx4 v11,s[72:75],s83 offen lds
v_mfma_f32_16x16x32_bf16 a[32:35],a[168:171],v[72:75],a[32:35]
s_waitcnt lgkmcnt(0)
v_accvgpr_read_b32 v13,a63
v_mul_f32 v190,v21,v190
v_mfma_f32_16x16x32_bf16 a[32:35],a[172:175],v[76:79],a[32:35]
v_mul_f32 v191,v21,v191
v_mul_f32 v12,v21,v12
v_mfma_f32_16x16x32_bf16 a[36:39],a[172:175],v[92:95],0
v_mul_f32 v13,v21,v13
v_cvt_pk_bf16_f32 v190,v190,v191
v_mfma_f32_16x16x32_bf16 a[36:39],a[168:171],v[88:91],a[36:39]
buffer_store_dwordx4 v[180:183],v6,s[36:39],s85 offen offset:2048 sc1
v_mfma_f32_16x16x32_bf16 a[36:39],a[164:167],v[84:87],a[36:39]
v_cvt_pk_bf16_f32 v191,v12,v13
v_max_f32 v22,0x00800000,v56
v_mfma_f32_16x16x32_bf16 a[36:39],a[160:163],v[80:83],a[36:39]
ds_read_b64_tr_b16 a[176:177],v2 offset:224
ds_read_b64_tr_b16 a[178:179],v2 offset:5344
v_mfma_f32_16x16x32_bf16 a[40:43],a[160:163],v[96:99],0
ds_read_b64_tr_b16 a[180:181],v2 offset:10464
ds_read_b64_tr_b16 a[182:183],v2 offset:15584
v_mfma_f32_16x16x32_bf16 a[40:43],a[164:167],v[100:103],a[40:43]
ds_read_b64_tr_b16 a[184:185],v2 offset:20704
ds_read_b64_tr_b16 a[186:187],v2 offset:25824
v_mfma_f32_16x16x32_bf16 a[40:43],a[168:171],v[104:107],a[40:43]
ds_read_b64_tr_b16 a[188:189],v2 offset:30944
ds_read_b64_tr_b16 a[190:191],v2 offset:36064
v_mfma_f32_16x16x32_bf16 a[40:43],a[172:175],v[108:111],a[40:43]
v_mfma_f32_16x16x32_bf16 a[44:47],a[172:175],v[124:127],0
v_rcp_f32 v22,v22
v_mfma_f32_16x16x32_bf16 a[44:47],a[168:171],v[120:123],a[44:47]
v_bfe_u32 v24,v6,4,2
v_min_u32 v24,2,v24
v_mfma_f32_16x16x32_bf16 a[44:47],a[164:167],v[116:119],a[44:47]
v_add_u32 v24,s71,v24
v_add_u32 v24,1,v24
v_mfma_f32_16x16x32_bf16 a[44:47],a[160:163],v[112:115],a[44:47]
s_waitcnt lgkmcnt(6)
v_and_b32 v24,3,v24
v_lshlrev_b32 v24,6,v24
v_mfma_f32_16x16x32_bf16 a[48:51],a[176:179],v[64:67],0
s_waitcnt lgkmcnt(4)
buffer_load_dwordx2 v[26:27],v24,s[64:67],s94 offen sc1
v_mfma_f32_16x16x32_bf16 a[48:51],a[180:183],v[68:71],a[48:51]
s_waitcnt lgkmcnt(2)
v_accvgpr_read_b32 v196,a64
v_accvgpr_read_b32 v197,a65
v_mfma_f32_16x16x32_bf16 a[48:51],a[184:187],v[72:75],a[48:51]
s_waitcnt lgkmcnt(0)
v_mul_f32 v196,v22,v196
v_mul_f32 v197,v22,v197
v_mfma_f32_16x16x32_bf16 a[48:51],a[188:191],v[76:79],a[48:51]
v_accvgpr_read_b32 v198,a66
v_accvgpr_read_b32 v199,a67
v_mfma_f32_16x16x32_bf16 a[52:55],a[188:191],v[92:95],0
v_cvt_pk_bf16_f32 v196,v196,v197
v_mul_f32 v198,v22,v198
v_mfma_f32_16x16x32_bf16 a[52:55],a[184:187],v[88:91],a[52:55]
v_mul_f32 v199,v22,v199
v_cvt_pk_bf16_f32 v197,v198,v199
v_mfma_f32_16x16x32_bf16 a[52:55],a[180:183],v[84:87],a[52:55]
v_accvgpr_read_b32 v198,a68
v_accvgpr_read_b32 v199,a69
v_mfma_f32_16x16x32_bf16 a[52:55],a[176:179],v[80:83],a[52:55]
ds_read_b64_tr_b16 a[128:129],v1 offset:256
ds_read_b64_tr_b16 a[130:131],v1 offset:5376
v_mfma_f32_16x16x32_bf16 a[56:59],a[176:179],v[96:99],0
ds_read_b64_tr_b16 a[132:133],v1 offset:10496
ds_read_b64_tr_b16 a[134:135],v1 offset:15616
v_mfma_f32_16x16x32_bf16 a[56:59],a[180:183],v[100:103],a[56:59]
ds_read_b64_tr_b16 a[136:137],v1 offset:20736
ds_read_b64_tr_b16 a[138:139],v1 offset:25856
v_mfma_f32_16x16x32_bf16 a[56:59],a[184:187],v[104:107],a[56:59]
ds_read_b64_tr_b16 a[140:141],v1 offset:30976
ds_read_b64_tr_b16 a[142:143],v1 offset:36096
v_mfma_f32_16x16x32_bf16 a[56:59],a[188:191],v[108:111],a[56:59]
v_mfma_f32_16x16x32_bf16 a[60:63],a[188:191],v[124:127],0
buffer_store_dwordx4 v[188:191],v6,s[36:39],s85 offen offset:3072 sc1
v_mfma_f32_16x16x32_bf16 a[60:63],a[184:187],v[120:123],a[60:63]
v_accvgpr_read_b32 v14,a70
v_accvgpr_read_b32 v15,a71
v_mfma_f32_16x16x32_bf16 a[60:63],a[180:183],v[116:119],a[60:63]
v_mul_f32 v198,v22,v198
v_mul_f32 v199,v22,v199
v_mfma_f32_16x16x32_bf16 a[60:63],a[176:179],v[112:115],a[60:63]
s_waitcnt lgkmcnt(6)
v_mul_f32 v14,v22,v14
v_mul_f32 v15,v22,v15
v_mfma_f32_16x16x32_bf16 a[64:67],a[128:131],v[64:67],0
s_waitcnt lgkmcnt(4)
v_cvt_pk_bf16_f32 v198,v198,v199
v_cvt_pk_bf16_f32 v199,v14,v15
v_mfma_f32_16x16x32_bf16 a[64:67],a[132:135],v[68:71],a[64:67]
s_waitcnt lgkmcnt(2)
v_accvgpr_read_b32 v204,a72
v_accvgpr_read_b32 v205,a73
v_mfma_f32_16x16x32_bf16 a[64:67],a[136:139],v[72:75],a[64:67]
s_waitcnt lgkmcnt(0)
v_mul_f32 v204,v22,v204
v_mul_f32 v205,v22,v205
v_mfma_f32_16x16x32_bf16 a[64:67],a[140:143],v[76:79],a[64:67]
v_accvgpr_read_b32 v206,a74
v_accvgpr_read_b32 v207,a75
v_mfma_f32_16x16x32_bf16 a[68:71],a[140:143],v[92:95],0
v_cvt_pk_bf16_f32 v204,v204,v205
v_mul_f32 v206,v22,v206
v_mfma_f32_16x16x32_bf16 a[68:71],a[136:139],v[88:91],a[68:71]
v_mul_f32 v207,v22,v207
v_cvt_pk_bf16_f32 v205,v206,v207
v_mfma_f32_16x16x32_bf16 a[68:71],a[132:135],v[84:87],a[68:71]
v_accvgpr_read_b32 v206,a76
v_accvgpr_read_b32 v207,a77
v_mfma_f32_16x16x32_bf16 a[68:71],a[128:131],v[80:83],a[68:71]
ds_read_b64_tr_b16 a[144:145],v2 offset:288
ds_read_b64_tr_b16 a[146:147],v2 offset:5408
v_mfma_f32_16x16x32_bf16 a[72:75],a[128:131],v[96:99],0
ds_read_b64_tr_b16 a[148:149],v2 offset:10528
ds_read_b64_tr_b16 a[150:151],v2 offset:15648
v_mfma_f32_16x16x32_bf16 a[72:75],a[132:135],v[100:103],a[72:75]
ds_read_b64_tr_b16 a[152:153],v2 offset:20768
ds_read_b64_tr_b16 a[154:155],v2 offset:25888
v_mfma_f32_16x16x32_bf16 a[72:75],a[136:139],v[104:107],a[72:75]
ds_read_b64_tr_b16 a[156:157],v2 offset:31008
ds_read_b64_tr_b16 a[158:159],v2 offset:36128
v_mfma_f32_16x16x32_bf16 a[72:75],a[140:143],v[108:111],a[72:75]
v_accvgpr_read_b32 v16,a78
v_accvgpr_read_b32 v17,a79
v_mfma_f32_16x16x32_bf16 a[76:79],a[140:143],v[124:127],0
v_mul_f32 v206,v22,v206
v_mul_f32 v207,v22,v207
v_mfma_f32_16x16x32_bf16 a[76:79],a[136:139],v[120:123],a[76:79]
v_mul_f32 v16,v22,v16
v_mul_f32 v17,v22,v17
v_mfma_f32_16x16x32_bf16 a[76:79],a[132:135],v[116:119],a[76:79]
v_cvt_pk_bf16_f32 v206,v206,v207
v_cvt_pk_bf16_f32 v207,v16,v17
v_mfma_f32_16x16x32_bf16 a[76:79],a[128:131],v[112:115],a[76:79]
s_waitcnt lgkmcnt(6)
v_accvgpr_read_b32 v212,a80
v_accvgpr_read_b32 v213,a81
v_accvgpr_read_b32 v214,a82
v_accvgpr_read_b32 v215,a83
v_mfma_f32_16x16x32_bf16 a[80:83],a[144:147],v[64:67],0
s_waitcnt lgkmcnt(4)
v_mul_f32 v212,v22,v212
v_mul_f32 v213,v22,v213
v_mfma_f32_16x16x32_bf16 a[80:83],a[148:151],v[68:71],a[80:83]
s_waitcnt lgkmcnt(2)
v_cvt_pk_bf16_f32 v212,v212,v213
v_mul_f32 v214,v22,v214
v_mfma_f32_16x16x32_bf16 a[80:83],a[152:155],v[72:75],a[80:83]
s_waitcnt lgkmcnt(0)
v_mul_f32 v215,v22,v215
v_cvt_pk_bf16_f32 v213,v214,v215
v_mfma_f32_16x16x32_bf16 a[80:83],a[156:159],v[76:79],a[80:83]
v_accvgpr_read_b32 v214,a84
v_accvgpr_read_b32 v215,a85
v_accvgpr_read_b32 v12,a86
v_accvgpr_read_b32 v13,a87
v_mfma_f32_16x16x32_bf16 a[84:87],a[156:159],v[92:95],0
v_mul_f32 v214,v22,v214
v_mul_f32 v215,v22,v215
v_mfma_f32_16x16x32_bf16 a[84:87],a[152:155],v[88:91],a[84:87]
v_mul_f32 v12,v22,v12
v_mul_f32 v13,v22,v13
v_mfma_f32_16x16x32_bf16 a[84:87],a[148:151],v[84:87],a[84:87]
v_add_u32 v1,40960,v1
v_cvt_pk_bf16_f32 v214,v214,v215
v_mfma_f32_16x16x32_bf16 a[84:87],a[144:147],v[80:83],a[84:87]
ds_read_b64_tr_b16 a[160:161],v1
ds_read_b64_tr_b16 a[162:163],v1 offset:5120
v_accvgpr_read_b32 v220,a88
v_accvgpr_read_b32 v221,a89
v_accvgpr_read_b32 v222,a90
v_accvgpr_read_b32 v223,a91
v_mfma_f32_16x16x32_bf16 a[88:91],a[144:147],v[96:99],0
ds_read_b64_tr_b16 a[164:165],v1 offset:10240
ds_read_b64_tr_b16 a[166:167],v1 offset:15360
v_max_f32 v23,0x00800000,v60
v_mfma_f32_16x16x32_bf16 a[88:91],a[148:151],v[100:103],a[88:91]
ds_read_b64_tr_b16 a[168:169],v1 offset:20480
ds_read_b64_tr_b16 a[170:171],v1 offset:25600
v_rcp_f32 v23,v23
v_accvgpr_read_b32 v228,a96
v_accvgpr_read_b32 v229,a97
v_mfma_f32_16x16x32_bf16 a[88:91],a[152:155],v[104:107],a[88:91]
ds_read_b64_tr_b16 a[172:173],v1 offset:30720
ds_read_b64_tr_b16 a[174:175],v1 offset:35840
v_mul_f32 v220,v22,v220
v_mul_f32 v221,v22,v221
v_cvt_pk_bf16_f32 v220,v220,v221
v_mul_f32 v222,v22,v222
v_mul_f32 v223,v22,v223
v_mul_f32 v228,v23,v228
v_mul_f32 v229,v23,v229
v_accvgpr_read_b32 v230,a98
v_accvgpr_read_b32 v231,a99
v_cvt_pk_bf16_f32 v228,v228,v229
v_mul_f32 v230,v23,v230
v_mul_f32 v231,v23,v231
v_mfma_f32_16x16x32_bf16 a[88:91],a[156:159],v[108:111],a[88:91]
v_cvt_pk_bf16_f32 v221,v222,v223
v_accvgpr_read_b32 v222,a92
v_accvgpr_read_b32 v223,a93
v_accvgpr_read_b32 v14,a94
v_accvgpr_read_b32 v15,a95
v_cvt_pk_bf16_f32 v229,v230,v231
v_accvgpr_read_b32 v230,a100
v_accvgpr_read_b32 v231,a101
v_accvgpr_read_b32 v236,a104
v_accvgpr_read_b32 v237,a105
v_mul_f32 v236,v23,v236
v_mul_f32 v237,v23,v237
v_accvgpr_read_b32 v238,a106
v_accvgpr_read_b32 v239,a107
v_accvgpr_read_b32 v244,a112
v_accvgpr_read_b32 v245,a113
v_mul_f32 v244,v23,v244
v_mul_f32 v245,v23,v245
v_accvgpr_read_b32 v246,a114
v_accvgpr_read_b32 v247,a115
v_accvgpr_read_b32 v252,a120
v_accvgpr_read_b32 v253,a121
v_cmp_nlt_f32 s[56:57],v48,s63
v_mul_f32 v252,v23,v252
v_mul_f32 v253,v23,v253
v_accvgpr_read_b32 v254,a122
v_accvgpr_read_b32 v255,a123
v_mfma_f32_16x16x32_bf16 a[92:95],a[156:159],v[124:127],0
v_mul_f32 v222,v22,v222
v_mul_f32 v223,v22,v223
v_mul_f32 v14,v22,v14
v_mul_f32 v15,v22,v15
v_cvt_pk_bf16_f32 v222,v222,v223
v_accvgpr_read_b32 v16,a102
v_accvgpr_read_b32 v17,a103
v_mul_f32 v230,v23,v230
v_mul_f32 v231,v23,v231
v_mul_f32 v16,v23,v16
v_mul_f32 v17,v23,v17
v_cvt_pk_bf16_f32 v230,v230,v231
v_cvt_pk_bf16_f32 v236,v236,v237
v_mul_f32 v238,v23,v238
v_mul_f32 v239,v23,v239
v_cvt_pk_bf16_f32 v237,v238,v239
v_cvt_pk_bf16_f32 v244,v244,v245
v_mul_f32 v246,v23,v246
v_mul_f32 v247,v23,v247
v_cvt_pk_bf16_f32 v245,v246,v247
v_cvt_pk_bf16_f32 v252,v252,v253
v_mul_f32 v254,v23,v254
v_mul_f32 v255,v23,v255
v_cmp_nlt_f32 s[58:59],v52,s63
s_nop 1
s_or_b64 s[56:57],s[56:57],s[58:59]
v_cvt_pk_bf16_f32 v253,v254,v255
v_mfma_f32_16x16x32_bf16 a[92:95],a[152:155],v[120:123],a[92:95]
v_cvt_pk_bf16_f32 v215,v12,v13
v_cvt_pk_bf16_f32 v223,v14,v15
v_cvt_pk_bf16_f32 v231,v16,v17
v_accvgpr_read_b32 v238,a108
v_accvgpr_read_b32 v239,a109
v_accvgpr_read_b32 v12,a110
v_accvgpr_read_b32 v13,a111
v_mul_f32 v238,v23,v238
v_mul_f32 v239,v23,v239
v_accvgpr_read_b32 v246,a116
v_accvgpr_read_b32 v247,a117
v_accvgpr_read_b32 v14,a118
v_accvgpr_read_b32 v15,a119
v_mul_f32 v246,v23,v246
v_mul_f32 v247,v23,v247
v_accvgpr_read_b32 v254,a124
v_accvgpr_read_b32 v255,a125
v_cmp_nlt_f32 s[58:59],v56,s63
s_nop 1
s_or_b64 s[56:57],s[56:57],s[58:59]
v_accvgpr_read_b32 v16,a126
v_accvgpr_read_b32 v17,a127
v_mul_f32 v254,v23,v254
v_mul_f32 v255,v23,v255
v_mfma_f32_16x16x32_bf16 a[92:95],a[148:151],v[116:119],a[92:95]
v_mul_f32 v12,v23,v12
v_mul_f32 v13,v23,v13
v_cvt_pk_bf16_f32 v238,v238,v239
v_cvt_pk_bf16_f32 v239,v12,v13
v_mul_f32 v14,v23,v14
v_mul_f32 v15,v23,v15
v_cvt_pk_bf16_f32 v246,v246,v247
v_cvt_pk_bf16_f32 v247,v14,v15
v_mul_f32 v16,v23,v16
v_mul_f32 v17,v23,v17
v_cvt_pk_bf16_f32 v254,v254,v255
v_cmp_nlt_f32 s[58:59],v60,s63
s_nop 1
s_or_b64 s[56:57],s[56:57],s[58:59]
v_cvt_pk_bf16_f32 v255,v16,v17
s_waitcnt vmcnt(1)
v_cmp_ne_u32 s[90:91],s68,v26
v_cmp_ne_u32 s[92:93],s69,v27
s_nop 0
s_or_b64 s[90:91],s[90:91],s[92:93]
)A"
R"A(s_cbranch_scc1 L_fr7_%=
L_fo7_%=:
v_mfma_f32_16x16x32_bf16 a[92:95],a[144:147],v[112:115],a[92:95]
s_cmp_lg_u64 s[56:57],0
s_cbranch_scc1 L_slow1_%=
L_sret1_%=:
v_add_u32 v2,40960,v2
s_waitcnt lgkmcnt(6)
v_mfma_f32_16x16x32_bf16 a[192:195],a[160:163],v[132:135],a[192:195]
s_waitcnt lgkmcnt(4)
buffer_store_dwordx4 v[196:199],v6,s[36:39],s86 offen sc1
v_mfma_f32_16x16x32_bf16 a[192:195],a[164:167],v[140:143],a[192:195]
s_cmp_ge_u32 0,s71
s_cselect_b32 s76,2,0
s_lshl_b32 s76,s76,16
s_waitcnt lgkmcnt(2)
v_mfma_f32_16x16x32_bf16 a[192:195],a[168:171],v[148:151],a[192:195]
s_add_u32 s77,s76,4096
s_add_u32 s78,s76,8192
s_add_u32 s79,s76,12288
s_max_i32 s76,s76,s18
s_waitcnt lgkmcnt(0)
v_mfma_f32_16x16x32_bf16 a[192:195],a[172:175],v[156:159],a[192:195]
s_max_i32 s77,s77,s19
ds_read_b64_tr_b16 a[176:177],v2 offset:32
ds_read_b64_tr_b16 a[178:179],v2 offset:5152
v_mfma_f32_16x16x32_bf16 a[196:199],a[172:175],v[188:191],a[196:199]
s_max_i32 s78,s78,s100
buffer_load_dwordx4 v[128:131],v6,s[28:31],s76 offen sc1
v_mfma_f32_16x16x32_bf16 a[196:199],a[168:171],v[180:183],a[196:199]
s_max_i32 s79,s79,s101
ds_read_b64_tr_b16 a[180:181],v2 offset:10272
ds_read_b64_tr_b16 a[182:183],v2 offset:15392
v_mfma_f32_16x16x32_bf16 a[196:199],a[164:167],v[172:175],a[196:199]
s_cmp_ge_u32 2,s71
s_cselect_b32 s80,6,4
ds_read_b64_tr_b16 a[184:185],v2 offset:20512
v_mfma_f32_16x16x32_bf16 a[196:199],a[160:163],v[164:167],a[196:199]
s_mul_i32 s80,s80,163840
s_add_u32 s80,s80,s81
ds_read_b64_tr_b16 a[186:187],v2 offset:25632
v_mfma_f32_16x16x32_bf16 a[200:203],a[160:163],v[196:199],a[200:203]
s_add_u32 s83,s80,20480
buffer_store_dwordx4 v[204:207],v6,s[36:39],s86 offen offset:1024 sc1
v_mfma_f32_16x16x32_bf16 a[200:203],a[164:167],v[204:207],a[200:203]
ds_read_b64_tr_b16 a[188:189],v2 offset:30752
ds_read_b64_tr_b16 a[190:191],v2 offset:35872
v_mfma_f32_16x16x32_bf16 a[200:203],a[168:171],v[212:215],a[200:203]
ds_read_b64_tr_b16 a[128:129],v1 offset:64
v_mfma_f32_16x16x32_bf16 a[200:203],a[172:175],v[220:223],a[200:203]
ds_read_b64_tr_b16 a[130:131],v1 offset:5184
v_mfma_f32_16x16x32_bf16 a[204:207],a[172:175],v[252:255],a[204:207]
buffer_load_dwordx4 v[160:163],v6,s[28:31],s76 offen offset:1024 sc1
v_mfma_f32_16x16x32_bf16 a[204:207],a[168:171],v[244:247],a[204:207]
ds_read_b64_tr_b16 a[132:133],v1 offset:10304
ds_read_b64_tr_b16 a[134:135],v1 offset:15424
v_mfma_f32_16x16x32_bf16 a[204:207],a[164:167],v[236:239],a[204:207]
ds_read_b64_tr_b16 a[136:137],v1 offset:20544
v_mfma_f32_16x16x32_bf16 a[204:207],a[160:163],v[228:231],a[204:207]
s_barrier
buffer_load_dwordx4 v[192:195],v6,s[28:31],s76 offen offset:2048 sc1
s_waitcnt lgkmcnt(7)
v_mfma_f32_16x16x32_bf16 a[208:211],a[176:179],v[132:135],a[208:211]
ds_read_b64_tr_b16 a[138:139],v1 offset:25664
ds_read_b64_tr_b16 a[140:141],v1 offset:30784
v_mfma_f32_16x16x32_bf16 a[208:211],a[180:183],v[140:143],a[208:211]
ds_read_b64_tr_b16 a[142:143],v1 offset:35904
v_mfma_f32_16x16x32_bf16 a[208:211],a[184:187],v[148:151],a[208:211]
s_add_u32 m0,s82,0
ds_read_b64_tr_b16 a[144:145],v2 offset:96
s_waitcnt lgkmcnt(8)
v_mfma_f32_16x16x32_bf16 a[208:211],a[188:191],v[156:159],a[208:211]
buffer_store_dwordx4 v[212:215],v6,s[36:39],s86 offen offset:2048 sc1
v_mfma_f32_16x16x32_bf16 a[212:215],a[188:191],v[188:191],a[212:215]
ds_read_b64_tr_b16 a[146:147],v2 offset:5216
ds_read_b64_tr_b16 a[148:149],v2 offset:10336
v_mfma_f32_16x16x32_bf16 a[212:215],a[184:187],v[180:183],a[212:215]
ds_read_b64_tr_b16 a[150:151],v2 offset:15456
v_mfma_f32_16x16x32_bf16 a[212:215],a[180:183],v[172:175],a[212:215]
ds_read_b64_tr_b16 a[152:153],v2 offset:20576
v_mfma_f32_16x16x32_bf16 a[212:215],a[176:179],v[164:167],a[212:215]
buffer_load_dwordx4 v[224:227],v6,s[28:31],s76 offen offset:3072 sc1
v_mfma_f32_16x16x32_bf16 a[216:219],a[176:179],v[196:199],a[216:219]
ds_read_b64_tr_b16 a[154:155],v2 offset:25696
ds_read_b64_tr_b16 a[156:157],v2 offset:30816
v_mfma_f32_16x16x32_bf16 a[216:219],a[180:183],v[204:207],a[216:219]
ds_read_b64_tr_b16 a[158:159],v2 offset:35936
v_mfma_f32_16x16x32_bf16 a[216:219],a[184:187],v[212:215],a[216:219]
ds_read_b64_tr_b16 a[160:161],v1 offset:128
v_mfma_f32_16x16x32_bf16 a[216:219],a[188:191],v[220:223],a[216:219]
buffer_load_dwordx4 v7,s[72:75],s80 offen lds
v_mfma_f32_16x16x32_bf16 a[220:223],a[188:191],v[252:255],a[220:223]
ds_read_b64_tr_b16 a[162:163],v1 offset:5248
ds_read_b64_tr_b16 a[164:165],v1 offset:10368
v_mfma_f32_16x16x32_bf16 a[220:223],a[184:187],v[244:247],a[220:223]
ds_read_b64_tr_b16 a[166:167],v1 offset:15488
v_mfma_f32_16x16x32_bf16 a[220:223],a[180:183],v[236:239],a[220:223]
ds_read_b64_tr_b16 a[168:169],v1 offset:20608
v_mfma_f32_16x16x32_bf16 a[220:223],a[176:179],v[228:231],a[220:223]
s_waitcnt lgkmcnt(8)
buffer_load_dwordx4 v[136:139],v6,s[28:31],s77 offen sc1
v_mfma_f32_16x16x32_bf16 a[224:227],a[128:131],v[132:135],a[224:227]
ds_read_b64_tr_b16 a[170:171],v1 offset:25728
ds_read_b64_tr_b16 a[172:173],v1 offset:30848
v_mfma_f32_16x16x32_bf16 a[224:227],a[132:135],v[140:143],a[224:227]
ds_read_b64_tr_b16 a[174:175],v1 offset:35968
v_mfma_f32_16x16x32_bf16 a[224:227],a[136:139],v[148:151],a[224:227]
ds_read_b64_tr_b16 a[176:177],v2 offset:160
v_mfma_f32_16x16x32_bf16 a[224:227],a[140:143],v[156:159],a[224:227]
buffer_load_dwordx4 v[168:171],v6,s[28:31],s77 offen offset:1024 sc1
v_mfma_f32_16x16x32_bf16 a[228:231],a[140:143],v[188:191],a[228:231]
ds_read_b64_tr_b16 a[178:179],v2 offset:5280
ds_read_b64_tr_b16 a[180:181],v2 offset:10400
v_mfma_f32_16x16x32_bf16 a[228:231],a[136:139],v[180:183],a[228:231]
ds_read_b64_tr_b16 a[182:183],v2 offset:15520
v_mfma_f32_16x16x32_bf16 a[228:231],a[132:135],v[172:175],a[228:231]
ds_read_b64_tr_b16 a[184:185],v2 offset:20640
v_mfma_f32_16x16x32_bf16 a[228:231],a[128:131],v[164:167],a[228:231]
buffer_load_dwordx4 v8,s[72:75],s80 offen offset:1024 lds
v_mfma_f32_16x16x32_bf16 a[232:235],a[128:131],v[196:199],a[232:235]
ds_read_b64_tr_b16 a[186:187],v2 offset:25760
ds_read_b64_tr_b16 a[188:189],v2 offset:30880
v_mfma_f32_16x16x32_bf16 a[232:235],a[132:135],v[204:207],a[232:235]
ds_read_b64_tr_b16 a[190:191],v2 offset:36000
v_mfma_f32_16x16x32_bf16 a[232:235],a[136:139],v[212:215],a[232:235]
v_mfma_f32_16x16x32_bf16 a[232:235],a[140:143],v[220:223],a[232:235]
buffer_store_dwordx4 v[220:223],v6,s[36:39],s86 offen offset:3072 sc1
v_mfma_f32_16x16x32_bf16 a[236:239],a[140:143],v[252:255],a[236:239]
ds_read_b64_tr_b16 a[140:141],v1 offset:30912
ds_read_b64_tr_b16 a[142:143],v1 offset:36032
v_mfma_f32_16x16x32_bf16 a[236:239],a[136:139],v[244:247],a[236:239]
ds_read_b64_tr_b16 a[136:137],v1 offset:20672
v_mfma_f32_16x16x32_bf16 a[236:239],a[132:135],v[236:239],a[236:239]
ds_read_b64_tr_b16 a[132:133],v1 offset:10432
v_mfma_f32_16x16x32_bf16 a[236:239],a[128:131],v[228:231],a[236:239]
buffer_load_dwordx4 v[200:203],v6,s[28:31],s77 offen offset:2048 sc1
v_mfma_f32_16x16x32_bf16 a[240:243],a[144:147],v[132:135],a[240:243]
ds_read_b64_tr_b16 a[128:129],v1 offset:192
ds_read_b64_tr_b16 a[130:131],v1 offset:5312
v_mfma_f32_16x16x32_bf16 a[240:243],a[148:151],v[140:143],a[240:243]
s_waitcnt lgkmcnt(6)
ds_read_b64_tr_b16 a[134:135],v1 offset:15552
v_mfma_f32_16x16x32_bf16 a[240:243],a[152:155],v[148:151],a[240:243]
ds_read_b64_tr_b16 a[138:139],v1 offset:25792
v_mfma_f32_16x16x32_bf16 a[240:243],a[156:159],v[156:159],a[240:243]
buffer_load_dwordx4 v[232:235],v6,s[28:31],s77 offen offset:3072 sc1
v_mfma_f32_16x16x32_bf16 a[244:247],a[156:159],v[188:191],a[244:247]
v_mfma_f32_16x16x32_bf16 a[244:247],a[152:155],v[180:183],a[244:247]
v_mfma_f32_16x16x32_bf16 a[244:247],a[148:151],v[172:175],a[244:247]
v_mfma_f32_16x16x32_bf16 a[244:247],a[144:147],v[164:167],a[244:247]
buffer_load_dwordx4 v9,s[72:75],s80 offen offset:2048 lds
v_mfma_f32_16x16x32_bf16 a[248:251],a[144:147],v[196:199],a[248:251]
v_mfma_f32_16x16x32_bf16 a[248:251],a[148:151],v[204:207],a[248:251]
v_mfma_f32_16x16x32_bf16 a[248:251],a[152:155],v[212:215],a[248:251]
v_mfma_f32_16x16x32_bf16 a[248:251],a[156:159],v[220:223],a[248:251]
buffer_load_dwordx4 v[144:147],v6,s[28:31],s78 offen sc1
v_mfma_f32_16x16x32_bf16 a[252:255],a[156:159],v[252:255],a[252:255]
ds_read_b64_tr_b16 a[156:157],v2 offset:30944
ds_read_b64_tr_b16 a[158:159],v2 offset:36064
v_mfma_f32_16x16x32_bf16 a[252:255],a[152:155],v[244:247],a[252:255]
ds_read_b64_tr_b16 a[152:153],v2 offset:20704
v_mfma_f32_16x16x32_bf16 a[252:255],a[148:151],v[236:239],a[252:255]
ds_read_b64_tr_b16 a[148:149],v2 offset:10464
v_mfma_f32_16x16x32_bf16 a[252:255],a[144:147],v[228:231],a[252:255]
buffer_store_dwordx4 v[228:231],v6,s[36:39],s87 offen sc1
v_mfma_f32_16x16x32_bf16 a[0:3],a[160:163],v[132:135],a[0:3]
ds_read_b64_tr_b16 a[144:145],v2 offset:224
ds_read_b64_tr_b16 a[146:147],v2 offset:5344
v_mfma_f32_16x16x32_bf16 a[0:3],a[164:167],v[140:143],a[0:3]
ds_read_b64_tr_b16 a[150:151],v2 offset:15584
v_mfma_f32_16x16x32_bf16 a[0:3],a[168:171],v[148:151],a[0:3]
ds_read_b64_tr_b16 a[154:155],v2 offset:25824
v_mfma_f32_16x16x32_bf16 a[0:3],a[172:175],v[156:159],a[0:3]
buffer_load_dwordx4 v[176:179],v6,s[28:31],s78 offen offset:1024 sc1
v_mfma_f32_16x16x32_bf16 a[4:7],a[172:175],v[188:191],a[4:7]
v_mfma_f32_16x16x32_bf16 a[4:7],a[168:171],v[180:183],a[4:7]
v_mfma_f32_16x16x32_bf16 a[4:7],a[164:167],v[172:175],a[4:7]
v_mfma_f32_16x16x32_bf16 a[4:7],a[160:163],v[164:167],a[4:7]
buffer_load_dwordx4 v10,s[72:75],s80 offen offset:3072 lds
v_mfma_f32_16x16x32_bf16 a[8:11],a[160:163],v[196:199],a[8:11]
v_mfma_f32_16x16x32_bf16 a[8:11],a[164:167],v[204:207],a[8:11]
v_mfma_f32_16x16x32_bf16 a[8:11],a[168:171],v[212:215],a[8:11]
v_mfma_f32_16x16x32_bf16 a[8:11],a[172:175],v[220:223],a[8:11]
buffer_load_dwordx4 v[208:211],v6,s[28:31],s78 offen offset:2048 sc1
v_mfma_f32_16x16x32_bf16 a[12:15],a[172:175],v[252:255],a[12:15]
ds_read_b64_tr_b16 a[172:173],v1 offset:30976
ds_read_b64_tr_b16 a[174:175],v1 offset:36096
v_mfma_f32_16x16x32_bf16 a[12:15],a[168:171],v[244:247],a[12:15]
ds_read_b64_tr_b16 a[168:169],v1 offset:20736
v_mfma_f32_16x16x32_bf16 a[12:15],a[164:167],v[236:239],a[12:15]
s_add_u32 m0,s82,4096
ds_read_b64_tr_b16 a[164:165],v1 offset:10496
v_mfma_f32_16x16x32_bf16 a[12:15],a[160:163],v[228:231],a[12:15]
buffer_load_dwordx4 v[240:243],v6,s[28:31],s78 offen offset:3072 sc1
v_mfma_f32_16x16x32_bf16 a[16:19],a[176:179],v[132:135],a[16:19]
ds_read_b64_tr_b16 a[160:161],v1 offset:256
ds_read_b64_tr_b16 a[162:163],v1 offset:5376
v_mfma_f32_16x16x32_bf16 a[16:19],a[180:183],v[140:143],a[16:19]
ds_read_b64_tr_b16 a[166:167],v1 offset:15616
v_mfma_f32_16x16x32_bf16 a[16:19],a[184:187],v[148:151],a[16:19]
ds_read_b64_tr_b16 a[170:171],v1 offset:25856
v_add_u32 v1,40960,v1
v_mfma_f32_16x16x32_bf16 a[16:19],a[188:191],v[156:159],a[16:19]
buffer_store_dwordx4 v[236:239],v6,s[36:39],s87 offen offset:1024 sc1
v_mfma_f32_16x16x32_bf16 a[20:23],a[188:191],v[188:191],a[20:23]
ds_read_b64_tr_b16 a[96:97],v1 offset:128
ds_read_b64_tr_b16 a[98:99],v1 offset:5248
v_mfma_f32_16x16x32_bf16 a[20:23],a[184:187],v[180:183],a[20:23]
ds_read_b64_tr_b16 a[100:101],v1 offset:10368
v_mfma_f32_16x16x32_bf16 a[20:23],a[180:183],v[172:175],a[20:23]
ds_read_b64_tr_b16 a[102:103],v1 offset:15488
v_mfma_f32_16x16x32_bf16 a[20:23],a[176:179],v[164:167],a[20:23]
buffer_load_dwordx4 v11,s[72:75],s80 offen lds
v_mfma_f32_16x16x32_bf16 a[24:27],a[176:179],v[196:199],a[24:27]
ds_read_b64_tr_b16 a[104:105],v1 offset:20608
ds_read_b64_tr_b16 a[106:107],v1 offset:25728
v_mfma_f32_16x16x32_bf16 a[24:27],a[180:183],v[204:207],a[24:27]
ds_read_b64_tr_b16 a[108:109],v1 offset:30848
v_mfma_f32_16x16x32_bf16 a[24:27],a[184:187],v[212:215],a[24:27]
ds_read_b64_tr_b16 a[110:111],v1 offset:35968
v_mfma_f32_16x16x32_bf16 a[24:27],a[188:191],v[220:223],a[24:27]
buffer_load_dwordx4 v[152:155],v6,s[28:31],s79 offen sc1
v_mfma_f32_16x16x32_bf16 a[28:31],a[188:191],v[252:255],a[28:31]
ds_read_b64_tr_b16 a[188:189],v2 offset:31008
ds_read_b64_tr_b16 a[190:191],v2 offset:36128
v_mfma_f32_16x16x32_bf16 a[28:31],a[184:187],v[244:247],a[28:31]
s_add_u32 m0,s82,5120
ds_read_b64_tr_b16 a[184:185],v2 offset:20768
)A"
R"A(v_mfma_f32_16x16x32_bf16 a[28:31],a[180:183],v[236:239],a[28:31]
ds_read_b64_tr_b16 a[180:181],v2 offset:10528
v_mfma_f32_16x16x32_bf16 a[28:31],a[176:179],v[228:231],a[28:31]
s_waitcnt lgkmcnt(8)
buffer_load_dwordx4 v[184:187],v6,s[28:31],s79 offen offset:1024 sc1
v_mfma_f32_16x16x32_bf16 a[32:35],a[128:131],v[132:135],a[32:35]
ds_read_b64_tr_b16 a[176:177],v2 offset:288
ds_read_b64_tr_b16 a[178:179],v2 offset:5408
v_mfma_f32_16x16x32_bf16 a[32:35],a[132:135],v[140:143],a[32:35]
ds_read_b64_tr_b16 a[182:183],v2 offset:15648
v_mfma_f32_16x16x32_bf16 a[32:35],a[136:139],v[148:151],a[32:35]
ds_read_b64_tr_b16 a[186:187],v2 offset:25888
v_add_u32 v2,40960,v2
v_mfma_f32_16x16x32_bf16 a[32:35],a[140:143],v[156:159],a[32:35]
buffer_load_dwordx4 v7,s[72:75],s83 offen lds
v_mfma_f32_16x16x32_bf16 a[36:39],a[140:143],v[188:191],a[36:39]
ds_read_b64_tr_b16 a[112:113],v2 offset:160
ds_read_b64_tr_b16 a[114:115],v2 offset:5280
v_mfma_f32_16x16x32_bf16 a[36:39],a[136:139],v[180:183],a[36:39]
ds_read_b64_tr_b16 a[116:117],v2 offset:10400
v_mfma_f32_16x16x32_bf16 a[36:39],a[132:135],v[172:175],a[36:39]
ds_read_b64_tr_b16 a[118:119],v2 offset:15520
v_mfma_f32_16x16x32_bf16 a[36:39],a[128:131],v[164:167],a[36:39]
buffer_load_dwordx4 v[216:219],v6,s[28:31],s79 offen offset:2048 sc1
v_mfma_f32_16x16x32_bf16 a[40:43],a[128:131],v[196:199],a[40:43]
ds_read_b64_tr_b16 a[120:121],v2 offset:20640
ds_read_b64_tr_b16 a[122:123],v2 offset:25760
v_mfma_f32_16x16x32_bf16 a[40:43],a[132:135],v[204:207],a[40:43]
ds_read_b64_tr_b16 a[124:125],v2 offset:30880
v_mfma_f32_16x16x32_bf16 a[40:43],a[136:139],v[212:215],a[40:43]
ds_read_b64_tr_b16 a[126:127],v2 offset:36000
v_mfma_f32_16x16x32_bf16 a[40:43],a[140:143],v[220:223],a[40:43]
buffer_load_dwordx4 v[248:251],v6,s[28:31],s79 offen offset:3072 sc1
v_mfma_f32_16x16x32_bf16 a[44:47],a[140:143],v[252:255],a[44:47]
ds_read_b64_tr_b16 a[140:141],v1 offset:30720
ds_read_b64_tr_b16 a[142:143],v1 offset:35840
v_mfma_f32_16x16x32_bf16 a[44:47],a[136:139],v[244:247],a[44:47]
ds_read_b64_tr_b16 a[136:137],v1 offset:20480
v_mfma_f32_16x16x32_bf16 a[44:47],a[132:135],v[236:239],a[44:47]
ds_read_b64_tr_b16 a[132:133],v1 offset:10240
v_mfma_f32_16x16x32_bf16 a[44:47],a[128:131],v[228:231],a[44:47]
buffer_store_dwordx4 v[244:247],v6,s[36:39],s87 offen offset:2048 sc1
v_mfma_f32_16x16x32_bf16 a[48:51],a[144:147],v[132:135],a[48:51]
ds_read_b64_tr_b16 a[128:129],v1
ds_read_b64_tr_b16 a[130:131],v1 offset:5120
v_mfma_f32_16x16x32_bf16 a[48:51],a[148:151],v[140:143],a[48:51]
ds_read_b64_tr_b16 a[134:135],v1 offset:15360
v_mfma_f32_16x16x32_bf16 a[48:51],a[152:155],v[148:151],a[48:51]
ds_read_b64_tr_b16 a[138:139],v1 offset:25600
v_mfma_f32_16x16x32_bf16 a[48:51],a[156:159],v[156:159],a[48:51]
buffer_load_dwordx4 v8,s[72:75],s83 offen offset:1024 lds
v_mfma_f32_16x16x32_bf16 a[52:55],a[156:159],v[188:191],a[52:55]
s_cmp_ge_u32 1,s71
s_cselect_b32 s76,4,2
s_lshl_b32 s76,s76,16
v_mfma_f32_16x16x32_bf16 a[52:55],a[152:155],v[180:183],a[52:55]
s_add_u32 s77,s76,4096
s_add_u32 s78,s76,8192
s_add_u32 s79,s76,12288
v_mfma_f32_16x16x32_bf16 a[52:55],a[148:151],v[172:175],a[52:55]
s_max_i32 s76,s76,s18
s_max_i32 s77,s77,s19
s_max_i32 s78,s78,s100
v_mfma_f32_16x16x32_bf16 a[52:55],a[144:147],v[164:167],a[52:55]
s_max_i32 s79,s79,s101
buffer_store_dwordx4 v[252:255],v6,s[36:39],s87 offen offset:3072 sc1
v_mfma_f32_16x16x32_bf16 a[56:59],a[144:147],v[196:199],a[56:59]
v_mfma_f32_16x16x32_bf16 a[56:59],a[148:151],v[204:207],a[56:59]
v_mfma_f32_16x16x32_bf16 a[56:59],a[152:155],v[212:215],a[56:59]
v_mfma_f32_16x16x32_bf16 a[56:59],a[156:159],v[220:223],a[56:59]
v_mfma_f32_16x16x32_bf16 a[60:63],a[156:159],v[252:255],a[60:63]
ds_read_b64_tr_b16 a[156:157],v2 offset:30752
ds_read_b64_tr_b16 a[158:159],v2 offset:35872
v_mfma_f32_16x16x32_bf16 a[60:63],a[152:155],v[244:247],a[60:63]
ds_read_b64_tr_b16 a[152:153],v2 offset:20512
v_mfma_f32_16x16x32_bf16 a[60:63],a[148:151],v[236:239],a[60:63]
buffer_load_dwordx4 v9,s[72:75],s83 offen offset:2048 lds
v_mfma_f32_16x16x32_bf16 a[60:63],a[144:147],v[228:231],a[60:63]
ds_read_b64_tr_b16 a[144:145],v2 offset:32
ds_read_b64_tr_b16 a[146:147],v2 offset:5152
v_mfma_f32_16x16x32_bf16 a[64:67],a[160:163],v[132:135],a[64:67]
ds_read_b64_tr_b16 a[148:149],v2 offset:10272
v_mfma_f32_16x16x32_bf16 a[64:67],a[164:167],v[140:143],a[64:67]
ds_read_b64_tr_b16 a[150:151],v2 offset:15392
v_mfma_f32_16x16x32_bf16 a[64:67],a[168:171],v[148:151],a[64:67]
ds_read_b64_tr_b16 a[154:155],v2 offset:25632
v_mfma_f32_16x16x32_bf16 a[64:67],a[172:175],v[156:159],a[64:67]
v_mfma_f32_16x16x32_bf16 a[68:71],a[172:175],v[188:191],a[68:71]
v_mfma_f32_16x16x32_bf16 a[68:71],a[168:171],v[180:183],a[68:71]
v_mfma_f32_16x16x32_bf16 a[68:71],a[164:167],v[172:175],a[68:71]
v_mfma_f32_16x16x32_bf16 a[68:71],a[160:163],v[164:167],a[68:71]
v_mfma_f32_16x16x32_bf16 a[72:75],a[160:163],v[196:199],a[72:75]
v_mfma_f32_16x16x32_bf16 a[72:75],a[164:167],v[204:207],a[72:75]
v_mfma_f32_16x16x32_bf16 a[72:75],a[168:171],v[212:215],a[72:75]
v_mfma_f32_16x16x32_bf16 a[72:75],a[172:175],v[220:223],a[72:75]
buffer_load_dwordx4 v[64:67],v6,s[28:31],s76 offen sc1
v_mfma_f32_16x16x32_bf16 a[76:79],a[172:175],v[252:255],a[76:79]
ds_read_b64_tr_b16 a[172:173],v1 offset:30784
ds_read_b64_tr_b16 a[174:175],v1 offset:35904
v_mfma_f32_16x16x32_bf16 a[76:79],a[168:171],v[244:247],a[76:79]
ds_read_b64_tr_b16 a[168:169],v1 offset:20544
v_mfma_f32_16x16x32_bf16 a[76:79],a[164:167],v[236:239],a[76:79]
ds_read_b64_tr_b16 a[164:165],v1 offset:10304
v_mfma_f32_16x16x32_bf16 a[76:79],a[160:163],v[228:231],a[76:79]
s_waitcnt lgkmcnt(4)
buffer_load_dwordx4 v10,s[72:75],s83 offen offset:3072 lds
v_mfma_f32_16x16x32_bf16 a[80:83],a[176:179],v[132:135],a[80:83]
ds_read_b64_tr_b16 a[160:161],v1 offset:64
ds_read_b64_tr_b16 a[162:163],v1 offset:5184
v_mfma_f32_16x16x32_bf16 a[80:83],a[180:183],v[140:143],a[80:83]
ds_read_b64_tr_b16 a[166:167],v1 offset:15424
v_mfma_f32_16x16x32_bf16 a[80:83],a[184:187],v[148:151],a[80:83]
ds_read_b64_tr_b16 a[170:171],v1 offset:25664
v_mfma_f32_16x16x32_bf16 a[80:83],a[188:191],v[156:159],a[80:83]
buffer_load_dwordx4 v[68:71],v6,s[28:31],s76 offen offset:1024 sc1
v_mfma_f32_16x16x32_bf16 a[84:87],a[188:191],v[188:191],a[84:87]
v_mfma_f32_16x16x32_bf16 a[84:87],a[184:187],v[180:183],a[84:87]
v_mfma_f32_16x16x32_bf16 a[84:87],a[180:183],v[172:175],a[84:87]
v_mfma_f32_16x16x32_bf16 a[84:87],a[176:179],v[164:167],a[84:87]
v_mfma_f32_16x16x32_bf16 a[88:91],a[176:179],v[196:199],a[88:91]
v_mfma_f32_16x16x32_bf16 a[88:91],a[180:183],v[204:207],a[88:91]
s_add_u32 m0,s82,9216
v_mfma_f32_16x16x32_bf16 a[88:91],a[184:187],v[212:215],a[88:91]
v_mfma_f32_16x16x32_bf16 a[88:91],a[188:191],v[220:223],a[88:91]
buffer_load_dwordx4 v[72:75],v6,s[28:31],s76 offen offset:2048 sc1
v_mfma_f32_16x16x32_bf16 a[92:95],a[188:191],v[252:255],a[92:95]
ds_read_b64_tr_b16 a[188:189],v2 offset:30816
ds_read_b64_tr_b16 a[190:191],v2 offset:35936
v_mfma_f32_16x16x32_bf16 a[92:95],a[184:187],v[244:247],a[92:95]
ds_read_b64_tr_b16 a[184:185],v2 offset:20576
v_mfma_f32_16x16x32_bf16 a[92:95],a[180:183],v[236:239],a[92:95]
s_cmp_ge_u32 0,s71
s_cselect_b32 s80,3,1
s_mul_i32 s80,s80,163840
v_mfma_f32_16x16x32_bf16 a[92:95],a[176:179],v[228:231],a[92:95]
s_add_u32 s80,s80,s81
buffer_load_dwordx4 v11,s[72:75],s83 offen lds
s_waitcnt vmcnt(14)
v_mfma_f32_16x16x32_bf16 a[192:195],a[128:131],v[128:131],a[192:195]
s_add_u32 s83,s80,20480
ds_read_b64_tr_b16 a[176:177],v2 offset:96
ds_read_b64_tr_b16 a[178:179],v2 offset:5216
v_mfma_f32_16x16x32_bf16 a[192:195],a[132:135],v[160:163],a[192:195]
ds_read_b64_tr_b16 a[180:181],v2 offset:10336
v_mfma_f32_16x16x32_bf16 a[192:195],a[136:139],v[192:195],a[192:195]
ds_read_b64_tr_b16 a[182:183],v2 offset:15456
v_mfma_f32_16x16x32_bf16 a[192:195],a[140:143],v[224:227],a[192:195]
buffer_load_dwordx4 v[76:79],v6,s[28:31],s76 offen offset:3072 sc1
v_mfma_f32_16x16x32_bf16 a[196:199],a[140:143],v[232:235],a[196:199]
ds_read_b64_tr_b16 a[186:187],v2 offset:25696
v_mfma_f32_16x16x32_bf16 a[196:199],a[136:139],v[200:203],a[196:199]
v_mfma_f32_16x16x32_bf16 a[196:199],a[132:135],v[168:171],a[196:199]
v_mfma_f32_16x16x32_bf16 a[196:199],a[128:131],v[136:139],a[196:199]
v_mfma_f32_16x16x32_bf16 a[200:203],a[128:131],v[144:147],a[200:203]
v_mfma_f32_16x16x32_bf16 a[200:203],a[132:135],v[176:179],a[200:203]
s_lshl_b32 s95,s42,2
s_add_u32 s95,s95,s40
s_lshl_b32 s95,s95,4
v_mfma_f32_16x16x32_bf16 a[200:203],a[136:139],v[208:211],a[200:203]
v_mov_b32 v18,s68
v_mov_b32 v19,s69
v_mfma_f32_16x16x32_bf16 a[200:203],a[140:143],v[240:243],a[200:203]
s_waitcnt vmcnt(10)
buffer_load_dwordx4 v[80:83],v6,s[28:31],s77 offen sc1
v_mfma_f32_16x16x32_bf16 a[204:207],a[140:143],v[248:251],a[204:207]
ds_read_b64_tr_b16 a[140:141],v1 offset:30912
ds_read_b64_tr_b16 a[142:143],v1 offset:36032
v_mfma_f32_16x16x32_bf16 a[204:207],a[136:139],v[216:219],a[204:207]
ds_read_b64_tr_b16 a[136:137],v1 offset:20672
v_mfma_f32_16x16x32_bf16 a[204:207],a[132:135],v[184:187],a[204:207]
ds_read_b64_tr_b16 a[132:133],v1 offset:10432
v_mfma_f32_16x16x32_bf16 a[204:207],a[128:131],v[152:155],a[204:207]
s_barrier
ds_read_b64_tr_b16 a[128:129],v1 offset:192
ds_read_b64_tr_b16 a[130:131],v1 offset:5312
v_mfma_f32_16x16x32_bf16 a[208:211],a[144:147],v[128:131],a[208:211]
s_waitcnt vmcnt(8)
s_mov_b64 exec,1
buffer_store_dwordx2 v[18:19],v6,s[64:67],s95 offen offset:1024 sc1
s_mov_b64 exec,-1
v_mfma_f32_16x16x32_bf16 a[208:211],a[148:151],v[160:163],a[208:211]
ds_read_b64_tr_b16 a[134:135],v1 offset:15552
ds_read_b64_tr_b16 a[138:139],v1 offset:25792
v_mfma_f32_16x16x32_bf16 a[208:211],a[152:155],v[192:195],a[208:211]
s_add_u32 m0,s82,40960
v_mfma_f32_16x16x32_bf16 a[208:211],a[156:159],v[224:227],a[208:211]
v_mfma_f32_16x16x32_bf16 a[212:215],a[156:159],v[232:235],a[212:215]
buffer_load_dwordx4 v[84:87],v6,s[28:31],s77 offen offset:1024 sc1
v_mfma_f32_16x16x32_bf16 a[212:215],a[152:155],v[200:203],a[212:215]
v_mfma_f32_16x16x32_bf16 a[212:215],a[148:151],v[168:171],a[212:215]
v_mfma_f32_16x16x32_bf16 a[212:215],a[144:147],v[136:139],a[212:215]
v_mfma_f32_16x16x32_bf16 a[216:219],a[144:147],v[144:147],a[216:219]
buffer_load_dwordx4 v7,s[72:75],s80 offen lds
v_mfma_f32_16x16x32_bf16 a[216:219],a[148:151],v[176:179],a[216:219]
v_mfma_f32_16x16x32_bf16 a[216:219],a[152:155],v[208:211],a[216:219]
v_mfma_f32_16x16x32_bf16 a[216:219],a[156:159],v[240:243],a[216:219]
v_mfma_f32_16x16x32_bf16 a[220:223],a[156:159],v[248:251],a[220:223]
buffer_load_dwordx4 v[88:91],v6,s[28:31],s77 offen offset:2048 sc1
v_mfma_f32_16x16x32_bf16 a[220:223],a[152:155],v[216:219],a[220:223]
ds_read_b64_tr_b16 a[152:153],v2 offset:20704
ds_read_b64_tr_b16 a[154:155],v2 offset:25824
v_mfma_f32_16x16x32_bf16 a[220:223],a[148:151],v[184:187],a[220:223]
ds_read_b64_tr_b16 a[148:149],v2 offset:10464
v_mfma_f32_16x16x32_bf16 a[220:223],a[144:147],v[152:155],a[220:223]
s_waitcnt lgkmcnt(3)
ds_read_b64_tr_b16 a[144:145],v2 offset:224
v_mfma_f32_16x16x32_bf16 a[224:227],a[160:163],v[128:131],a[224:227]
ds_read_b64_tr_b16 a[146:147],v2 offset:5344
v_mfma_f32_16x16x32_bf16 a[224:227],a[164:167],v[160:163],a[224:227]
ds_read_b64_tr_b16 a[150:151],v2 offset:15584
ds_read_b64_tr_b16 a[156:157],v2 offset:30944
v_mfma_f32_16x16x32_bf16 a[224:227],a[168:171],v[192:195],a[224:227]
buffer_load_dwordx4 v[92:95],v6,s[28:31],s77 offen offset:3072 sc1
v_mfma_f32_16x16x32_bf16 a[224:227],a[172:175],v[224:227],a[224:227]
ds_read_b64_tr_b16 a[158:159],v2 offset:36064
v_mfma_f32_16x16x32_bf16 a[228:231],a[172:175],v[232:235],a[228:231]
v_mfma_f32_16x16x32_bf16 a[228:231],a[168:171],v[200:203],a[228:231]
v_mfma_f32_16x16x32_bf16 a[228:231],a[164:167],v[168:171],a[228:231]
buffer_load_dwordx4 v8,s[72:75],s80 offen offset:1024 lds
v_mfma_f32_16x16x32_bf16 a[228:231],a[160:163],v[136:139],a[228:231]
v_mfma_f32_16x16x32_bf16 a[232:235],a[160:163],v[144:147],a[232:235]
v_mfma_f32_16x16x32_bf16 a[232:235],a[164:167],v[176:179],a[232:235]
v_mfma_f32_16x16x32_bf16 a[232:235],a[168:171],v[208:211],a[232:235]
buffer_load_dwordx4 v[96:99],v6,s[28:31],s78 offen sc1
)A"
R"A(v_mfma_f32_16x16x32_bf16 a[232:235],a[172:175],v[240:243],a[232:235]
v_mfma_f32_16x16x32_bf16 a[236:239],a[172:175],v[248:251],a[236:239]
ds_read_b64_tr_b16 a[172:173],v1 offset:30976
ds_read_b64_tr_b16 a[174:175],v1 offset:36096
v_mfma_f32_16x16x32_bf16 a[236:239],a[168:171],v[216:219],a[236:239]
ds_read_b64_tr_b16 a[168:169],v1 offset:20736
v_mfma_f32_16x16x32_bf16 a[236:239],a[164:167],v[184:187],a[236:239]
ds_read_b64_tr_b16 a[164:165],v1 offset:10496
v_mfma_f32_16x16x32_bf16 a[236:239],a[160:163],v[152:155],a[236:239]
ds_read_b64_tr_b16 a[160:161],v1 offset:256
v_mfma_f32_16x16x32_bf16 a[240:243],a[176:179],v[128:131],a[240:243]
ds_read_b64_tr_b16 a[162:163],v1 offset:5376
ds_read_b64_tr_b16 a[166:167],v1 offset:15616
v_mfma_f32_16x16x32_bf16 a[240:243],a[180:183],v[160:163],a[240:243]
ds_read_b64_tr_b16 a[170:171],v1 offset:25856
v_add_u32 v1,40960,v1
v_mfma_f32_16x16x32_bf16 a[240:243],a[184:187],v[192:195],a[240:243]
buffer_load_dwordx4 v[100:103],v6,s[28:31],s78 offen offset:1024 sc1
v_mfma_f32_16x16x32_bf16 a[240:243],a[188:191],v[224:227],a[240:243]
v_mfma_f32_16x16x32_bf16 a[244:247],a[188:191],v[232:235],a[244:247]
v_mfma_f32_16x16x32_bf16 a[244:247],a[184:187],v[200:203],a[244:247]
v_mfma_f32_16x16x32_bf16 a[244:247],a[180:183],v[168:171],a[244:247]
buffer_load_dwordx4 v9,s[72:75],s80 offen offset:2048 lds
v_mfma_f32_16x16x32_bf16 a[244:247],a[176:179],v[136:139],a[244:247]
v_mfma_f32_16x16x32_bf16 a[248:251],a[176:179],v[144:147],a[248:251]
v_mfma_f32_16x16x32_bf16 a[248:251],a[180:183],v[176:179],a[248:251]
v_mfma_f32_16x16x32_bf16 a[248:251],a[184:187],v[208:211],a[248:251]
buffer_load_dwordx4 v[104:107],v6,s[28:31],s78 offen offset:2048 sc1
v_mfma_f32_16x16x32_bf16 a[248:251],a[188:191],v[240:243],a[248:251]
v_mfma_f32_16x16x32_bf16 a[252:255],a[188:191],v[248:251],a[252:255]
ds_read_b64_tr_b16 a[188:189],v2 offset:31008
ds_read_b64_tr_b16 a[190:191],v2 offset:36128
v_mfma_f32_16x16x32_bf16 a[252:255],a[184:187],v[216:219],a[252:255]
ds_read_b64_tr_b16 a[184:185],v2 offset:20768
v_mfma_f32_16x16x32_bf16 a[252:255],a[180:183],v[184:187],a[252:255]
ds_read_b64_tr_b16 a[180:181],v2 offset:10528
v_mfma_f32_16x16x32_bf16 a[252:255],a[176:179],v[152:155],a[252:255]
ds_read_b64_tr_b16 a[176:177],v2 offset:288
v_mfma_f32_16x16x32_bf16 a[0:3],a[96:99],v[128:131],a[0:3]
ds_read_b64_tr_b16 a[178:179],v2 offset:5408
ds_read_b64_tr_b16 a[182:183],v2 offset:15648
v_mfma_f32_16x16x32_bf16 a[0:3],a[100:103],v[160:163],a[0:3]
ds_read_b64_tr_b16 a[186:187],v2 offset:25888
v_add_u32 v2,40960,v2
v_mfma_f32_16x16x32_bf16 a[0:3],a[104:107],v[192:195],a[0:3]
buffer_load_dwordx4 v[108:111],v6,s[28:31],s78 offen offset:3072 sc1
v_mfma_f32_16x16x32_bf16 a[0:3],a[108:111],v[224:227],a[0:3]
v_mfma_f32_16x16x32_bf16 a[4:7],a[108:111],v[232:235],a[4:7]
v_mfma_f32_16x16x32_bf16 a[4:7],a[104:107],v[200:203],a[4:7]
v_mfma_f32_16x16x32_bf16 a[4:7],a[100:103],v[168:171],a[4:7]
buffer_load_dwordx4 v10,s[72:75],s80 offen offset:3072 lds
v_mfma_f32_16x16x32_bf16 a[4:7],a[96:99],v[136:139],a[4:7]
v_mfma_f32_16x16x32_bf16 a[8:11],a[96:99],v[144:147],a[8:11]
v_mfma_f32_16x16x32_bf16 a[8:11],a[100:103],v[176:179],a[8:11]
v_mfma_f32_16x16x32_bf16 a[8:11],a[104:107],v[208:211],a[8:11]
buffer_load_dwordx4 v[112:115],v6,s[28:31],s79 offen sc1
v_mfma_f32_16x16x32_bf16 a[8:11],a[108:111],v[240:243],a[8:11]
v_mfma_f32_16x16x32_bf16 a[12:15],a[108:111],v[248:251],a[12:15]
ds_read_b64_tr_b16 a[108:109],v1 offset:30720
ds_read_b64_tr_b16 a[110:111],v1 offset:35840
v_mfma_f32_16x16x32_bf16 a[12:15],a[104:107],v[216:219],a[12:15]
ds_read_b64_tr_b16 a[104:105],v1 offset:20480
v_mfma_f32_16x16x32_bf16 a[12:15],a[100:103],v[184:187],a[12:15]
s_add_u32 m0,s82,45056
ds_read_b64_tr_b16 a[100:101],v1 offset:10240
v_mfma_f32_16x16x32_bf16 a[12:15],a[96:99],v[152:155],a[12:15]
ds_read_b64_tr_b16 a[96:97],v1
v_mfma_f32_16x16x32_bf16 a[16:19],a[112:115],v[128:131],a[16:19]
ds_read_b64_tr_b16 a[98:99],v1 offset:5120
ds_read_b64_tr_b16 a[102:103],v1 offset:15360
v_mfma_f32_16x16x32_bf16 a[16:19],a[116:119],v[160:163],a[16:19]
buffer_load_dwordx4 v11,s[72:75],s80 offen lds
v_mfma_f32_16x16x32_bf16 a[16:19],a[120:123],v[192:195],a[16:19]
ds_read_b64_tr_b16 a[106:107],v1 offset:25600
v_mfma_f32_16x16x32_bf16 a[16:19],a[124:127],v[224:227],a[16:19]
v_mfma_f32_16x16x32_bf16 a[20:23],a[124:127],v[232:235],a[20:23]
v_mfma_f32_16x16x32_bf16 a[20:23],a[120:123],v[200:203],a[20:23]
buffer_load_dwordx4 v[116:119],v6,s[28:31],s79 offen offset:1024 sc1
v_mfma_f32_16x16x32_bf16 a[20:23],a[116:119],v[168:171],a[20:23]
v_mfma_f32_16x16x32_bf16 a[20:23],a[112:115],v[136:139],a[20:23]
v_mfma_f32_16x16x32_bf16 a[24:27],a[112:115],v[144:147],a[24:27]
v_mfma_f32_16x16x32_bf16 a[24:27],a[116:119],v[176:179],a[24:27]
v_mfma_f32_16x16x32_bf16 a[24:27],a[120:123],v[208:211],a[24:27]
buffer_load_dwordx4 v[120:123],v6,s[28:31],s79 offen offset:2048 sc1
v_mfma_f32_16x16x32_bf16 a[24:27],a[124:127],v[240:243],a[24:27]
v_mfma_f32_16x16x32_bf16 a[28:31],a[124:127],v[248:251],a[28:31]
ds_read_b64_tr_b16 a[124:125],v2 offset:30752
ds_read_b64_tr_b16 a[126:127],v2 offset:35872
v_mfma_f32_16x16x32_bf16 a[28:31],a[120:123],v[216:219],a[28:31]
s_add_u32 m0,s82,46080
ds_read_b64_tr_b16 a[120:121],v2 offset:20512
v_mfma_f32_16x16x32_bf16 a[28:31],a[116:119],v[184:187],a[28:31]
ds_read_b64_tr_b16 a[116:117],v2 offset:10272
v_mfma_f32_16x16x32_bf16 a[28:31],a[112:115],v[152:155],a[28:31]
s_cmp_ge_u32 2,s71
s_cselect_b32 s76,6,4
s_lshl_b32 s76,s76,16
v_mfma_f32_16x16x32_bf16 a[32:35],a[128:131],v[128:131],a[32:35]
s_add_u32 s77,s76,4096
buffer_load_dwordx4 v7,s[72:75],s83 offen lds
v_mfma_f32_16x16x32_bf16 a[32:35],a[132:135],v[160:163],a[32:35]
s_add_u32 s78,s76,8192
s_max_i32 s77,s77,s19
ds_read_b64_tr_b16 a[112:113],v2 offset:32
v_mfma_f32_16x16x32_bf16 a[32:35],a[136:139],v[192:195],a[32:35]
s_max_i32 s78,s78,s100
ds_read_b64_tr_b16 a[114:115],v2 offset:5152
ds_read_b64_tr_b16 a[118:119],v2 offset:15392
v_mfma_f32_16x16x32_bf16 a[32:35],a[140:143],v[224:227],a[32:35]
ds_read_b64_tr_b16 a[122:123],v2 offset:25632
v_mfma_f32_16x16x32_bf16 a[36:39],a[140:143],v[232:235],a[36:39]
buffer_load_dwordx4 v[124:127],v6,s[28:31],s79 offen offset:3072 sc1
v_mfma_f32_16x16x32_bf16 a[36:39],a[136:139],v[200:203],a[36:39]
s_add_u32 s79,s76,12288
s_max_i32 s76,s76,s18
s_max_i32 s79,s79,s101
v_mfma_f32_16x16x32_bf16 a[36:39],a[132:135],v[168:171],a[36:39]
v_mfma_f32_16x16x32_bf16 a[36:39],a[128:131],v[136:139],a[36:39]
v_mfma_f32_16x16x32_bf16 a[40:43],a[128:131],v[144:147],a[40:43]
v_mfma_f32_16x16x32_bf16 a[40:43],a[132:135],v[176:179],a[40:43]
v_mfma_f32_16x16x32_bf16 a[40:43],a[136:139],v[208:211],a[40:43]
v_mfma_f32_16x16x32_bf16 a[40:43],a[140:143],v[240:243],a[40:43]
v_mfma_f32_16x16x32_bf16 a[44:47],a[140:143],v[248:251],a[44:47]
ds_read_b64_tr_b16 a[140:141],v1 offset:30784
ds_read_b64_tr_b16 a[142:143],v1 offset:35904
v_mfma_f32_16x16x32_bf16 a[44:47],a[136:139],v[216:219],a[44:47]
ds_read_b64_tr_b16 a[136:137],v1 offset:20544
v_mfma_f32_16x16x32_bf16 a[44:47],a[132:135],v[184:187],a[44:47]
ds_read_b64_tr_b16 a[132:133],v1 offset:10304
v_mfma_f32_16x16x32_bf16 a[44:47],a[128:131],v[152:155],a[44:47]
s_waitcnt lgkmcnt(4)
buffer_load_dwordx4 v8,s[72:75],s83 offen offset:1024 lds
v_mfma_f32_16x16x32_bf16 a[48:51],a[144:147],v[128:131],a[48:51]
ds_read_b64_tr_b16 a[128:129],v1 offset:64
ds_read_b64_tr_b16 a[130:131],v1 offset:5184
v_mfma_f32_16x16x32_bf16 a[48:51],a[148:151],v[160:163],a[48:51]
ds_read_b64_tr_b16 a[134:135],v1 offset:15424
v_mfma_f32_16x16x32_bf16 a[48:51],a[152:155],v[192:195],a[48:51]
ds_read_b64_tr_b16 a[138:139],v1 offset:25664
v_mfma_f32_16x16x32_bf16 a[48:51],a[156:159],v[224:227],a[48:51]
s_lshl_b32 s94,s70,4
v_bfe_u32 v24,v6,4,2
v_min_u32 v24,2,v24
v_mfma_f32_16x16x32_bf16 a[52:55],a[156:159],v[232:235],a[52:55]
s_add_u32 s94,s94,s40
v_add_u32 v24,s71,v24
v_add_u32 v24,1,v24
v_mfma_f32_16x16x32_bf16 a[52:55],a[152:155],v[200:203],a[52:55]
s_lshl_b32 s94,s94,4
v_and_b32 v24,3,v24
v_lshlrev_b32 v24,6,v24
v_mfma_f32_16x16x32_bf16 a[52:55],a[148:151],v[168:171],a[52:55]
v_mfma_f32_16x16x32_bf16 a[52:55],a[144:147],v[136:139],a[52:55]
buffer_load_dwordx4 v[132:135],v6,s[28:31],s76 offen sc1
v_mfma_f32_16x16x32_bf16 a[56:59],a[144:147],v[144:147],a[56:59]
v_mfma_f32_16x16x32_bf16 a[56:59],a[148:151],v[176:179],a[56:59]
v_mfma_f32_16x16x32_bf16 a[56:59],a[152:155],v[208:211],a[56:59]
v_mfma_f32_16x16x32_bf16 a[56:59],a[156:159],v[240:243],a[56:59]
v_mfma_f32_16x16x32_bf16 a[60:63],a[156:159],v[248:251],a[60:63]
ds_read_b64_tr_b16 a[156:157],v2 offset:30816
ds_read_b64_tr_b16 a[158:159],v2 offset:35936
v_mfma_f32_16x16x32_bf16 a[60:63],a[152:155],v[216:219],a[60:63]
buffer_load_dwordx4 v[140:143],v6,s[28:31],s76 offen offset:1024 sc1
v_mfma_f32_16x16x32_bf16 a[60:63],a[148:151],v[184:187],a[60:63]
ds_read_b64_tr_b16 a[148:149],v2 offset:10336
ds_read_b64_tr_b16 a[150:151],v2 offset:15456
v_mfma_f32_16x16x32_bf16 a[60:63],a[144:147],v[152:155],a[60:63]
ds_read_b64_tr_b16 a[144:145],v2 offset:96
v_mfma_f32_16x16x32_bf16 a[64:67],a[160:163],v[128:131],a[64:67]
ds_read_b64_tr_b16 a[146:147],v2 offset:5216
v_mfma_f32_16x16x32_bf16 a[64:67],a[164:167],v[160:163],a[64:67]
buffer_load_dwordx4 v9,s[72:75],s83 offen offset:2048 lds
v_mfma_f32_16x16x32_bf16 a[64:67],a[168:171],v[192:195],a[64:67]
ds_read_b64_tr_b16 a[152:153],v2 offset:20576
ds_read_b64_tr_b16 a[154:155],v2 offset:25696
v_mfma_f32_16x16x32_bf16 a[64:67],a[172:175],v[224:227],a[64:67]
v_mfma_f32_16x16x32_bf16 a[68:71],a[172:175],v[232:235],a[68:71]
v_mfma_f32_16x16x32_bf16 a[68:71],a[168:171],v[200:203],a[68:71]
buffer_load_dwordx4 v[148:151],v6,s[28:31],s76 offen offset:2048 sc1
v_mfma_f32_16x16x32_bf16 a[68:71],a[164:167],v[168:171],a[68:71]
v_mfma_f32_16x16x32_bf16 a[68:71],a[160:163],v[136:139],a[68:71]
v_mfma_f32_16x16x32_bf16 a[72:75],a[160:163],v[144:147],a[72:75]
v_mfma_f32_16x16x32_bf16 a[72:75],a[164:167],v[176:179],a[72:75]
buffer_load_dwordx4 v[156:159],v6,s[28:31],s76 offen offset:3072 sc1
v_mfma_f32_16x16x32_bf16 a[72:75],a[168:171],v[208:211],a[72:75]
v_mfma_f32_16x16x32_bf16 a[72:75],a[172:175],v[240:243],a[72:75]
v_mfma_f32_16x16x32_bf16 a[76:79],a[172:175],v[248:251],a[76:79]
ds_read_b64_tr_b16 a[172:173],v1 offset:30848
ds_read_b64_tr_b16 a[174:175],v1 offset:35968
v_mfma_f32_16x16x32_bf16 a[76:79],a[168:171],v[216:219],a[76:79]
buffer_load_dwordx4 v10,s[72:75],s83 offen offset:3072 lds
v_mfma_f32_16x16x32_bf16 a[76:79],a[164:167],v[184:187],a[76:79]
ds_read_b64_tr_b16 a[164:165],v1 offset:10368
ds_read_b64_tr_b16 a[166:167],v1 offset:15488
v_mfma_f32_16x16x32_bf16 a[76:79],a[160:163],v[152:155],a[76:79]
ds_read_b64_tr_b16 a[160:161],v1 offset:128
v_mfma_f32_16x16x32_bf16 a[80:83],a[176:179],v[128:131],a[80:83]
ds_read_b64_tr_b16 a[162:163],v1 offset:5248
v_mfma_f32_16x16x32_bf16 a[80:83],a[180:183],v[160:163],a[80:83]
buffer_load_dwordx4 v[164:167],v6,s[28:31],s77 offen sc1
v_mfma_f32_16x16x32_bf16 a[80:83],a[184:187],v[192:195],a[80:83]
ds_read_b64_tr_b16 a[168:169],v1 offset:20608
ds_read_b64_tr_b16 a[170:171],v1 offset:25728
v_mfma_f32_16x16x32_bf16 a[80:83],a[188:191],v[224:227],a[80:83]
v_mfma_f32_16x16x32_bf16 a[84:87],a[188:191],v[232:235],a[84:87]
v_mfma_f32_16x16x32_bf16 a[84:87],a[184:187],v[200:203],a[84:87]
buffer_load_dwordx4 v[172:175],v6,s[28:31],s77 offen offset:1024 sc1
v_mfma_f32_16x16x32_bf16 a[84:87],a[180:183],v[168:171],a[84:87]
v_mfma_f32_16x16x32_bf16 a[84:87],a[176:179],v[136:139],a[84:87]
v_mfma_f32_16x16x32_bf16 a[88:91],a[176:179],v[144:147],a[88:91]
v_mfma_f32_16x16x32_bf16 a[88:91],a[180:183],v[176:179],a[88:91]
s_add_u32 m0,s82,50176
v_mfma_f32_16x16x32_bf16 a[88:91],a[184:187],v[208:211],a[88:91]
v_mfma_f32_16x16x32_bf16 a[88:91],a[188:191],v[240:243],a[88:91]
buffer_load_dwordx2 v[26:27],v24,s[64:67],s94 offen offset:1024 sc1
v_mfma_f32_16x16x32_bf16 a[92:95],a[188:191],v[248:251],a[92:95]
ds_read_b64_tr_b16 a[188:189],v2 offset:30880
ds_read_b64_tr_b16 a[190:191],v2 offset:36000
v_mfma_f32_16x16x32_bf16 a[92:95],a[184:187],v[216:219],a[92:95]
)A"
R"A(ds_read_b64_tr_b16 a[184:185],v2 offset:20640
v_mfma_f32_16x16x32_bf16 a[92:95],a[180:183],v[184:187],a[92:95]
ds_read_b64_tr_b16 a[180:181],v2 offset:10400
v_mfma_f32_16x16x32_bf16 a[92:95],a[176:179],v[152:155],a[92:95]
s_waitcnt vmcnt(13)
buffer_load_dwordx4 v[180:183],v6,s[28:31],s77 offen offset:2048 sc1
v_mfma_f32_16x16x32_bf16 a[192:195],a[96:99],v[64:67],a[192:195]
s_lshl_b32 s43,s41,2
ds_read_b64_tr_b16 a[176:177],v2 offset:160
ds_read_b64_tr_b16 a[178:179],v2 offset:5280
v_mfma_f32_16x16x32_bf16 a[192:195],a[100:103],v[68:71],a[192:195]
s_add_u32 s84,s70,1
s_add_u32 s85,s70,2
ds_read_b64_tr_b16 a[182:183],v2 offset:15520
v_mfma_f32_16x16x32_bf16 a[192:195],a[104:107],v[72:75],a[192:195]
s_add_u32 s86,s70,3
s_add_u32 s43,s43,s71
ds_read_b64_tr_b16 a[186:187],v2 offset:25760
v_mfma_f32_16x16x32_bf16 a[192:195],a[108:111],v[76:79],a[192:195]
s_and_b32 s84,s84,3
buffer_load_dwordx4 v11,s[72:75],s83 offen lds
v_mfma_f32_16x16x32_bf16 a[196:199],a[108:111],v[92:95],a[196:199]
s_and_b32 s85,s85,3
s_and_b32 s86,s86,3
s_mul_i32 s43,s43,245760
v_mfma_f32_16x16x32_bf16 a[196:199],a[104:107],v[88:91],a[196:199]
s_mul_i32 s84,s84,12
s_mul_i32 s85,s85,12
s_mul_i32 s86,s86,12
v_mfma_f32_16x16x32_bf16 a[196:199],a[100:103],v[84:87],a[196:199]
s_add_u32 s43,s43,134217728
s_mov_b64 s[32:33],s[12:13]
s_add_u32 s84,s84,0
v_mfma_f32_16x16x32_bf16 a[196:199],a[96:99],v[80:83],a[196:199]
s_add_u32 s85,s85,4
buffer_load_dwordx4 v[188:191],v6,s[28:31],s77 offen offset:3072 sc1
v_mfma_f32_16x16x32_bf16 a[200:203],a[96:99],v[96:99],a[200:203]
s_add_u32 s86,s86,8
s_add_u32 s84,s84,s40
s_add_u32 s85,s85,s40
v_mfma_f32_16x16x32_bf16 a[200:203],a[100:103],v[100:103],a[200:203]
s_add_u32 s86,s86,s40
s_add_u32 s32,s32,s43
s_addc_u32 s33,s33,0
v_mfma_f32_16x16x32_bf16 a[200:203],a[104:107],v[104:107],a[200:203]
s_mul_i32 s84,s84,5120
s_mul_i32 s85,s85,5120
s_mul_i32 s86,s86,5120
v_mfma_f32_16x16x32_bf16 a[200:203],a[108:111],v[108:111],a[200:203]
s_and_b32 s33,s33,0xffff
buffer_load_dwordx4 v[196:199],v6,s[28:31],s78 offen sc1
s_waitcnt vmcnt(14)
v_mfma_f32_16x16x32_bf16 a[204:207],a[108:111],v[124:127],a[204:207]
s_mov_b32 s34,245760
ds_read_b64_tr_b16 a[108:109],v1 offset:30912
ds_read_b64_tr_b16 a[110:111],v1 offset:36032
v_mfma_f32_16x16x32_bf16 a[204:207],a[104:107],v[120:123],a[204:207]
s_mov_b32 s35,0x20000
s_max_i32 s84,s84,s18
ds_read_b64_tr_b16 a[104:105],v1 offset:20672
v_mfma_f32_16x16x32_bf16 a[204:207],a[100:103],v[116:119],a[204:207]
s_max_i32 s85,s85,s19
s_max_i32 s86,s86,s100
ds_read_b64_tr_b16 a[100:101],v1 offset:10432
v_mfma_f32_16x16x32_bf16 a[204:207],a[96:99],v[112:115],a[204:207]
s_barrier
buffer_load_dwordx4 v[204:207],v6,s[28:31],s78 offen offset:1024 sc1
v_mfma_f32_16x16x32_bf16 a[208:211],a[112:115],v[64:67],a[208:211]
s_mul_i32 s44,s41,307200
ds_read_b64_tr_b16 a[96:97],v1 offset:192
ds_read_b64_tr_b16 a[98:99],v1 offset:5312
v_mfma_f32_16x16x32_bf16 a[208:211],a[116:119],v[68:71],a[208:211]
s_mov_b64 s[20:21],s[16:17]
ds_read_b64_tr_b16 a[102:103],v1 offset:15552
v_bfe_u32 v18,v6,6,2
v_mfma_f32_16x16x32_bf16 a[208:211],a[120:123],v[72:75],a[208:211]
s_lshl_b32 s43,s40,2
ds_read_b64_tr_b16 a[106:107],v1 offset:25792
v_lshrrev_b32 v19,8,v6
v_mfma_f32_16x16x32_bf16 a[208:211],a[124:127],v[76:79],a[208:211]
s_add_u32 s43,s43,s70
buffer_load_dwordx4 v[212:215],v6,s[28:31],s78 offen offset:2048 sc1
v_mfma_f32_16x16x32_bf16 a[212:215],a[124:127],v[92:95],a[212:215]
s_mul_i32 s43,s43,20480
v_xor_b32 v18,v18,v19
v_lshlrev_b32 v18,2,v18
v_mfma_f32_16x16x32_bf16 a[212:215],a[120:123],v[88:91],a[212:215]
s_mov_b32 s22,307200
v_bfe_u32 v19,v6,4,2
v_or_b32 v18,v18,v19
v_mfma_f32_16x16x32_bf16 a[212:215],a[116:119],v[84:87],a[212:215]
s_mov_b32 s23,0x20000
v_and_b32 v19,16,v6
v_mov_b32 v34,0x3f80
v_mfma_f32_16x16x32_bf16 a[212:215],a[112:115],v[80:83],a[212:215]
s_cmp_ge_u32 0,s71
s_cselect_b32 s76,3,1
s_lshl_b32 s76,s76,16
v_mfma_f32_16x16x32_bf16 a[216:219],a[112:115],v[96:99],a[216:219]
s_add_u32 s77,s76,4096
buffer_load_dwordx4 v[220:223],v6,s[28:31],s78 offen offset:3072 sc1
v_mfma_f32_16x16x32_bf16 a[216:219],a[116:119],v[100:103],a[216:219]
s_add_u32 s78,s76,8192
s_max_i32 s77,s77,s19
s_max_i32 s78,s78,s100
v_mfma_f32_16x16x32_bf16 a[216:219],a[120:123],v[104:107],a[216:219]
v_lshlrev_b32 v19,v19,v34
v_lshrrev_b32 v18,1,v18
v_mfma_f32_16x16x32_bf16 a[216:219],a[124:127],v[108:111],a[216:219]
v_cmp_eq_u32 vcc,0,v18
s_nop 1
v_cndmask_b32 v34,0,v19,vcc
v_mfma_f32_16x16x32_bf16 a[220:223],a[124:127],v[124:127],a[220:223]
ds_read_b64_tr_b16 a[124:125],v2 offset:30944
ds_read_b64_tr_b16 a[126:127],v2 offset:36064
v_mfma_f32_16x16x32_bf16 a[220:223],a[120:123],v[120:123],a[220:223]
ds_read_b64_tr_b16 a[120:121],v2 offset:20704
v_add_u32 v25,4096,v6
v_mfma_f32_16x16x32_bf16 a[220:223],a[116:119],v[116:119],a[220:223]
buffer_load_dwordx4 v[228:231],v6,s[28:31],s79 offen sc1
v_mfma_f32_16x16x32_bf16 a[220:223],a[112:115],v[112:115],a[220:223]
s_waitcnt lgkmcnt(3)
ds_read_b64_tr_b16 a[112:113],v2 offset:224
ds_read_b64_tr_b16 a[114:115],v2 offset:5344
v_mfma_f32_16x16x32_bf16 a[224:227],a[128:131],v[64:67],a[224:227]
ds_read_b64_tr_b16 a[116:117],v2 offset:10464
v_mov_b32 v36,0
v_mfma_f32_16x16x32_bf16 a[224:227],a[132:135],v[68:71],a[224:227]
ds_read_b64_tr_b16 a[118:119],v2 offset:15584
v_mov_b32 v37,0
v_mfma_f32_16x16x32_bf16 a[224:227],a[136:139],v[72:75],a[224:227]
ds_read_b64_tr_b16 a[122:123],v2 offset:25824
v_mov_b32 v32,0
v_mfma_f32_16x16x32_bf16 a[224:227],a[140:143],v[76:79],a[224:227]
v_cmp_eq_u32 vcc,1,v18
s_nop 1
v_cndmask_b32 v35,0,v19,vcc
v_mfma_f32_16x16x32_bf16 a[228:231],a[140:143],v[92:95],a[228:231]
buffer_load_dwordx4 v[236:239],v6,s[28:31],s79 offen offset:1024 sc1
v_mfma_f32_16x16x32_bf16 a[228:231],a[136:139],v[88:91],a[228:231]
s_add_u32 s20,s20,s44
s_addc_u32 s21,s21,0
v_mov_b32 v33,0
v_mfma_f32_16x16x32_bf16 a[228:231],a[132:135],v[84:87],a[228:231]
s_mul_i32 s44,s71,320
v_bfe_u32 v40,v6,4,4
v_mul_u32_u24 v40,1280,v40
v_mfma_f32_16x16x32_bf16 a[228:231],a[128:131],v[80:83],a[228:231]
s_waitcnt vmcnt(9)
v_cmp_ne_u32 s[90:91],s68,v26
v_cmp_ne_u32 s[92:93],s69,v27
s_nop 0
s_or_b64 s[90:91],s[90:91],s[92:93]
s_cbranch_scc1 L_fr8_%=
L_fo8_%=:
v_mfma_f32_16x16x32_bf16 a[232:235],a[128:131],v[96:99],a[232:235]
s_and_b32 s21,s21,0xffff
v_lshrrev_b32 v41,8,v6
v_lshl_add_u32 v40,v41,3,v40
v_mfma_f32_16x16x32_bf16 a[232:235],a[132:135],v[100:103],a[232:235]
s_add_u32 s43,s43,s44
v_mfma_f32_16x16x32_bf16 a[232:235],a[136:139],v[104:107],a[232:235]
buffer_load_dwordx4 v[244:247],v6,s[28:31],s79 offen offset:2048 sc1
v_mfma_f32_16x16x32_bf16 a[232:235],a[140:143],v[108:111],a[232:235]
v_mfma_f32_16x16x32_bf16 a[236:239],a[140:143],v[124:127],a[236:239]
ds_read_b64_tr_b16 a[140:141],v1 offset:30976
ds_read_b64_tr_b16 a[142:143],v1 offset:36096
v_mfma_f32_16x16x32_bf16 a[236:239],a[136:139],v[120:123],a[236:239]
ds_read_b64_tr_b16 a[136:137],v1 offset:20736
v_mfma_f32_16x16x32_bf16 a[236:239],a[132:135],v[116:119],a[236:239]
ds_read_b64_tr_b16 a[132:133],v1 offset:10496
v_mfma_f32_16x16x32_bf16 a[236:239],a[128:131],v[112:115],a[236:239]
ds_read_b64_tr_b16 a[128:129],v1 offset:256
v_mfma_f32_16x16x32_bf16 a[240:243],a[144:147],v[64:67],a[240:243]
buffer_load_dwordx4 v[252:255],v6,s[28:31],s79 offen offset:3072 sc1
v_mfma_f32_16x16x32_bf16 a[240:243],a[148:151],v[68:71],a[240:243]
s_add_u32 s79,s76,12288
s_max_i32 s76,s76,s18
ds_read_b64_tr_b16 a[130:131],v1 offset:5376
v_mfma_f32_16x16x32_bf16 a[240:243],a[152:155],v[72:75],a[240:243]
s_max_i32 s79,s79,s101
ds_read_b64_tr_b16 a[134:135],v1 offset:15616
ds_read_b64_tr_b16 a[138:139],v1 offset:25856
v_mfma_f32_16x16x32_bf16 a[240:243],a[156:159],v[76:79],a[240:243]
v_subrev_u32 v1,122880,v1
v_mfma_f32_16x16x32_bf16 a[244:247],a[156:159],v[92:95],a[244:247]
buffer_load_dwordx4 v[128:131],v6,s[28:31],s76 offen sc1
v_mfma_f32_16x16x32_bf16 a[244:247],a[152:155],v[88:91],a[244:247]
v_mfma_f32_16x16x32_bf16 a[244:247],a[148:151],v[84:87],a[244:247]
v_mfma_f32_16x16x32_bf16 a[244:247],a[144:147],v[80:83],a[244:247]
v_mfma_f32_16x16x32_bf16 a[248:251],a[144:147],v[96:99],a[248:251]
buffer_load_dwordx4 v[160:163],v6,s[28:31],s76 offen offset:1024 sc1
v_mfma_f32_16x16x32_bf16 a[248:251],a[148:151],v[100:103],a[248:251]
v_mfma_f32_16x16x32_bf16 a[248:251],a[152:155],v[104:107],a[248:251]
v_mfma_f32_16x16x32_bf16 a[248:251],a[156:159],v[108:111],a[248:251]
v_mfma_f32_16x16x32_bf16 a[252:255],a[156:159],v[124:127],a[252:255]
buffer_load_dwordx4 v[192:195],v6,s[28:31],s76 offen offset:2048 sc1
v_mfma_f32_16x16x32_bf16 a[252:255],a[152:155],v[120:123],a[252:255]
ds_read_b64_tr_b16 a[152:153],v2 offset:20768
ds_read_b64_tr_b16 a[154:155],v2 offset:25888
v_mfma_f32_16x16x32_bf16 a[252:255],a[148:151],v[116:119],a[252:255]
ds_read_b64_tr_b16 a[148:149],v2 offset:10528
v_mfma_f32_16x16x32_bf16 a[252:255],a[144:147],v[112:115],a[252:255]
ds_read_b64_tr_b16 a[144:145],v2 offset:288
v_mfma_f32_16x16x32_bf16 a[0:3],a[160:163],v[64:67],a[0:3]
ds_read_b64_tr_b16 a[146:147],v2 offset:5408
v_mfma_f32_16x16x32_bf16 a[0:3],a[164:167],v[68:71],a[0:3]
buffer_load_dwordx4 v[224:227],v6,s[28:31],s76 offen offset:3072 sc1
v_mfma_f32_16x16x32_bf16 a[0:3],a[168:171],v[72:75],a[0:3]
ds_read_b64_tr_b16 a[150:151],v2 offset:15648
ds_read_b64_tr_b16 a[156:157],v2 offset:31008
v_mfma_f32_16x16x32_bf16 a[0:3],a[172:175],v[76:79],a[0:3]
ds_read_b64_tr_b16 a[158:159],v2 offset:36128
v_subrev_u32 v2,122880,v2
v_mfma_f32_16x16x32_bf16 a[4:7],a[172:175],v[92:95],a[4:7]
v_mfma_f32_16x16x32_bf16 a[4:7],a[168:171],v[88:91],a[4:7]
v_mfma_f32_16x16x32_bf16 a[4:7],a[164:167],v[84:87],a[4:7]
v_mfma_f32_16x16x32_bf16 a[4:7],a[160:163],v[80:83],a[4:7]
buffer_load_dwordx4 v[136:139],v6,s[28:31],s77 offen sc1
v_mfma_f32_16x16x32_bf16 a[8:11],a[160:163],v[96:99],a[8:11]
v_mfma_f32_16x16x32_bf16 a[8:11],a[164:167],v[100:103],a[8:11]
v_mfma_f32_16x16x32_bf16 a[8:11],a[168:171],v[104:107],a[8:11]
v_mfma_f32_16x16x32_bf16 a[8:11],a[172:175],v[108:111],a[8:11]
v_mfma_f32_16x16x32_bf16 a[12:15],a[172:175],v[124:127],a[12:15]
ds_read_b64_tr_b16 a[172:173],v1 offset:30720
ds_read_b64_tr_b16 a[174:175],v1 offset:35840
v_mfma_f32_16x16x32_bf16 a[12:15],a[168:171],v[120:123],a[12:15]
buffer_load_dwordx4 v[168:171],v6,s[28:31],s77 offen offset:1024 sc1
v_mfma_f32_16x16x32_bf16 a[12:15],a[164:167],v[116:119],a[12:15]
ds_read_b64_tr_b16 a[164:165],v1 offset:10240
ds_read_b64_tr_b16 a[166:167],v1 offset:15360
v_mfma_f32_16x16x32_bf16 a[12:15],a[160:163],v[112:115],a[12:15]
ds_read_b64_tr_b16 a[160:161],v1
v_mfma_f32_16x16x32_bf16 a[16:19],a[176:179],v[64:67],a[16:19]
ds_read_b64_tr_b16 a[162:163],v1 offset:5120
v_mfma_f32_16x16x32_bf16 a[16:19],a[180:183],v[68:71],a[16:19]
ds_read_b64_tr_b16 a[168:169],v1 offset:20480
v_mfma_f32_16x16x32_bf16 a[16:19],a[184:187],v[72:75],a[16:19]
ds_read_b64_tr_b16 a[170:171],v1 offset:25600
v_mfma_f32_16x16x32_bf16 a[16:19],a[188:191],v[76:79],a[16:19]
buffer_load_dwordx4 v[200:203],v6,s[28:31],s77 offen offset:2048 sc1
v_mfma_f32_16x16x32_bf16 a[20:23],a[188:191],v[92:95],a[20:23]
v_mfma_f32_16x16x32_bf16 a[20:23],a[184:187],v[88:91],a[20:23]
v_mfma_f32_16x16x32_bf16 a[20:23],a[180:183],v[84:87],a[20:23]
v_mfma_f32_16x16x32_bf16 a[20:23],a[176:179],v[80:83],a[20:23]
v_mfma_f32_16x16x32_bf16 a[24:27],a[176:179],v[96:99],a[24:27]
v_mfma_f32_16x16x32_bf16 a[24:27],a[180:183],v[100:103],a[24:27]
buffer_load_dwordx4 v[232:235],v6,s[28:31],s77 offen offset:3072 sc1
v_mfma_f32_16x16x32_bf16 a[24:27],a[184:187],v[104:107],a[24:27]
v_mfma_f32_16x16x32_bf16 a[24:27],a[188:191],v[108:111],a[24:27]
v_mfma_f32_16x16x32_bf16 a[28:31],a[188:191],v[124:127],a[28:31]
ds_read_b64_tr_b16 a[188:189],v2 offset:30752
ds_read_b64_tr_b16 a[190:191],v2 offset:35872
v_mfma_f32_16x16x32_bf16 a[28:31],a[184:187],v[120:123],a[28:31]
ds_read_b64_tr_b16 a[184:185],v2 offset:20512
v_mfma_f32_16x16x32_bf16 a[28:31],a[180:183],v[116:119],a[28:31]
ds_read_b64_tr_b16 a[180:181],v2 offset:10272
v_mfma_f32_16x16x32_bf16 a[28:31],a[176:179],v[112:115],a[28:31]
buffer_load_dwordx4 v[144:147],v6,s[28:31],s78 offen sc1
)A"
R"A(v_mfma_f32_16x16x32_bf16 a[32:35],a[96:99],v[64:67],a[32:35]
ds_read_b64_tr_b16 a[176:177],v2 offset:32
ds_read_b64_tr_b16 a[178:179],v2 offset:5152
v_mfma_f32_16x16x32_bf16 a[32:35],a[100:103],v[68:71],a[32:35]
ds_read_b64_tr_b16 a[182:183],v2 offset:15392
v_mfma_f32_16x16x32_bf16 a[32:35],a[104:107],v[72:75],a[32:35]
ds_read_b64_tr_b16 a[186:187],v2 offset:25632
v_mfma_f32_16x16x32_bf16 a[32:35],a[108:111],v[76:79],a[32:35]
v_mfma_f32_16x16x32_bf16 a[36:39],a[108:111],v[92:95],a[36:39]
v_mfma_f32_16x16x32_bf16 a[36:39],a[104:107],v[88:91],a[36:39]
buffer_load_dwordx4 v[176:179],v6,s[28:31],s78 offen offset:1024 sc1
v_mfma_f32_16x16x32_bf16 a[36:39],a[100:103],v[84:87],a[36:39]
v_mfma_f32_16x16x32_bf16 a[36:39],a[96:99],v[80:83],a[36:39]
v_mfma_f32_16x16x32_bf16 a[40:43],a[96:99],v[96:99],a[40:43]
v_mfma_f32_16x16x32_bf16 a[40:43],a[100:103],v[100:103],a[40:43]
v_mfma_f32_16x16x32_bf16 a[40:43],a[104:107],v[104:107],a[40:43]
v_mfma_f32_16x16x32_bf16 a[40:43],a[108:111],v[108:111],a[40:43]
buffer_load_dwordx4 v[208:211],v6,s[28:31],s78 offen offset:2048 sc1
v_mfma_f32_16x16x32_bf16 a[44:47],a[108:111],v[124:127],a[44:47]
ds_read_b64_tr_b16 a[108:109],v1 offset:30784
ds_read_b64_tr_b16 a[110:111],v1 offset:35904
v_mfma_f32_16x16x32_bf16 a[44:47],a[104:107],v[120:123],a[44:47]
ds_read_b64_tr_b16 a[104:105],v1 offset:20544
v_mfma_f32_16x16x32_bf16 a[44:47],a[100:103],v[116:119],a[44:47]
ds_read_b64_tr_b16 a[100:101],v1 offset:10304
v_mfma_f32_16x16x32_bf16 a[44:47],a[96:99],v[112:115],a[44:47]
s_waitcnt lgkmcnt(4)
ds_read_b64_tr_b16 a[96:97],v1 offset:64
v_mfma_f32_16x16x32_bf16 a[48:51],a[112:115],v[64:67],a[48:51]
ds_read_b64_tr_b16 a[98:99],v1 offset:5184
ds_read_b64_tr_b16 a[102:103],v1 offset:15424
v_mfma_f32_16x16x32_bf16 a[48:51],a[116:119],v[68:71],a[48:51]
buffer_load_dwordx4 v[240:243],v6,s[28:31],s78 offen offset:3072 sc1
v_mfma_f32_16x16x32_bf16 a[48:51],a[120:123],v[72:75],a[48:51]
ds_read_b64_tr_b16 a[106:107],v1 offset:25664
v_mfma_f32_16x16x32_bf16 a[48:51],a[124:127],v[76:79],a[48:51]
v_mfma_f32_16x16x32_bf16 a[52:55],a[124:127],v[92:95],a[52:55]
v_mfma_f32_16x16x32_bf16 a[52:55],a[120:123],v[88:91],a[52:55]
v_mfma_f32_16x16x32_bf16 a[52:55],a[116:119],v[84:87],a[52:55]
v_mfma_f32_16x16x32_bf16 a[52:55],a[112:115],v[80:83],a[52:55]
buffer_load_dwordx4 v[152:155],v6,s[28:31],s79 offen sc1
v_mfma_f32_16x16x32_bf16 a[56:59],a[112:115],v[96:99],a[56:59]
v_mfma_f32_16x16x32_bf16 a[56:59],a[116:119],v[100:103],a[56:59]
s_cmp_ge_u32 1,s71
s_cselect_b32 s80,5,3
s_mul_i32 s80,s80,163840
v_mfma_f32_16x16x32_bf16 a[56:59],a[120:123],v[104:107],a[56:59]
s_add_u32 s80,s80,s81
s_add_u32 s83,s80,20480
v_mfma_f32_16x16x32_bf16 a[56:59],a[124:127],v[108:111],a[56:59]
v_mfma_f32_16x16x32_bf16 a[60:63],a[124:127],v[124:127],a[60:63]
ds_read_b64_tr_b16 a[124:125],v2 offset:30816
ds_read_b64_tr_b16 a[126:127],v2 offset:35936
v_mfma_f32_16x16x32_bf16 a[60:63],a[120:123],v[120:123],a[60:63]
buffer_load_dwordx4 v[184:187],v6,s[28:31],s79 offen offset:1024 sc1
v_mfma_f32_16x16x32_bf16 a[60:63],a[116:119],v[116:119],a[60:63]
ds_read_b64_tr_b16 a[116:117],v2 offset:10336
ds_read_b64_tr_b16 a[118:119],v2 offset:15456
v_mfma_f32_16x16x32_bf16 a[60:63],a[112:115],v[112:115],a[60:63]
ds_read_b64_tr_b16 a[112:113],v2 offset:96
v_mfma_f32_16x16x32_bf16 a[64:67],a[128:131],v[64:67],a[64:67]
ds_read_b64_tr_b16 a[114:115],v2 offset:5216
v_mfma_f32_16x16x32_bf16 a[64:67],a[132:135],v[68:71],a[64:67]
ds_read_b64_tr_b16 a[120:121],v2 offset:20576
v_mfma_f32_16x16x32_bf16 a[64:67],a[136:139],v[72:75],a[64:67]
ds_read_b64_tr_b16 a[122:123],v2 offset:25696
v_mfma_f32_16x16x32_bf16 a[64:67],a[140:143],v[76:79],a[64:67]
buffer_load_dwordx4 v[216:219],v6,s[28:31],s79 offen offset:2048 sc1
v_mfma_f32_16x16x32_bf16 a[68:71],a[140:143],v[92:95],a[68:71]
v_mfma_f32_16x16x32_bf16 a[68:71],a[136:139],v[88:91],a[68:71]
v_mfma_f32_16x16x32_bf16 a[68:71],a[132:135],v[84:87],a[68:71]
v_mfma_f32_16x16x32_bf16 a[68:71],a[128:131],v[80:83],a[68:71]
v_mfma_f32_16x16x32_bf16 a[72:75],a[128:131],v[96:99],a[72:75]
v_mfma_f32_16x16x32_bf16 a[72:75],a[132:135],v[100:103],a[72:75]
buffer_load_dwordx4 v[248:251],v6,s[28:31],s79 offen offset:3072 sc1
v_mfma_f32_16x16x32_bf16 a[72:75],a[136:139],v[104:107],a[72:75]
v_mfma_f32_16x16x32_bf16 a[72:75],a[140:143],v[108:111],a[72:75]
v_mfma_f32_16x16x32_bf16 a[76:79],a[140:143],v[124:127],a[76:79]
ds_read_b64_tr_b16 a[140:141],v1 offset:30848
ds_read_b64_tr_b16 a[142:143],v1 offset:35968
v_mfma_f32_16x16x32_bf16 a[76:79],a[136:139],v[120:123],a[76:79]
ds_read_b64_tr_b16 a[136:137],v1 offset:20608
v_mfma_f32_16x16x32_bf16 a[76:79],a[132:135],v[116:119],a[76:79]
s_add_u32 m0,s82,81920
ds_read_b64_tr_b16 a[132:133],v1 offset:10368
v_mfma_f32_16x16x32_bf16 a[76:79],a[128:131],v[112:115],a[76:79]
ds_read_b64_tr_b16 a[128:129],v1 offset:128
v_mfma_f32_16x16x32_bf16 a[80:83],a[144:147],v[64:67],a[80:83]
ds_read_b64_tr_b16 a[130:131],v1 offset:5248
ds_read_b64_tr_b16 a[134:135],v1 offset:15488
v_mfma_f32_16x16x32_bf16 a[80:83],a[148:151],v[68:71],a[80:83]
buffer_load_dwordx4 v7,s[72:75],s80 offen lds
v_mfma_f32_16x16x32_bf16 a[80:83],a[152:155],v[72:75],a[80:83]
ds_read_b64_tr_b16 a[138:139],v1 offset:25728
v_mfma_f32_16x16x32_bf16 a[80:83],a[156:159],v[76:79],a[80:83]
v_mfma_f32_16x16x32_bf16 a[84:87],a[156:159],v[92:95],a[84:87]
v_mfma_f32_16x16x32_bf16 a[84:87],a[152:155],v[88:91],a[84:87]
v_mfma_f32_16x16x32_bf16 a[84:87],a[148:151],v[84:87],a[84:87]
buffer_load_dwordx4 v8,s[72:75],s80 offen offset:1024 lds
v_mfma_f32_16x16x32_bf16 a[84:87],a[144:147],v[80:83],a[84:87]
v_mfma_f32_16x16x32_bf16 a[88:91],a[144:147],v[96:99],a[88:91]
v_mfma_f32_16x16x32_bf16 a[88:91],a[148:151],v[100:103],a[88:91]
v_mfma_f32_16x16x32_bf16 a[88:91],a[152:155],v[104:107],a[88:91]
v_mfma_f32_16x16x32_bf16 a[88:91],a[156:159],v[108:111],a[88:91]
buffer_load_dwordx4 v9,s[72:75],s80 offen offset:2048 lds
v_mfma_f32_16x16x32_bf16 a[92:95],a[156:159],v[124:127],a[92:95]
ds_read_b64_tr_b16 a[156:157],v2 offset:30880
ds_read_b64_tr_b16 a[158:159],v2 offset:36000
v_mfma_f32_16x16x32_bf16 a[92:95],a[152:155],v[120:123],a[92:95]
ds_read_b64_tr_b16 a[152:153],v2 offset:20640
v_mfma_f32_16x16x32_bf16 a[92:95],a[148:151],v[116:119],a[92:95]
ds_read_b64_tr_b16 a[148:149],v2 offset:10400
v_mfma_f32_16x16x32_bf16 a[92:95],a[144:147],v[112:115],a[92:95]
ds_read_b64_tr_b16 a[144:145],v2 offset:160
v_mfma_f32_16x16x32_bf16 a[192:195],a[160:163],v[132:135],a[192:195]
buffer_load_dwordx4 v10,s[72:75],s80 offen offset:3072 lds
v_mfma_f32_16x16x32_bf16 a[192:195],a[164:167],v[140:143],a[192:195]
ds_read_b64_tr_b16 a[146:147],v2 offset:5280
ds_read_b64_tr_b16 a[150:151],v2 offset:15520
v_mfma_f32_16x16x32_bf16 a[192:195],a[168:171],v[148:151],a[192:195]
s_add_u32 m0,s82,86016
ds_read_b64_tr_b16 a[154:155],v2 offset:25760
v_mfma_f32_16x16x32_bf16 a[192:195],a[172:175],v[156:159],a[192:195]
s_waitcnt vmcnt(12)
v_mfma_f32_16x16x32_bf16 a[196:199],a[172:175],v[188:191],a[196:199]
v_mfma_f32_16x16x32_bf16 a[196:199],a[168:171],v[180:183],a[196:199]
buffer_load_dwordx4 v11,s[72:75],s80 offen lds
v_mfma_f32_16x16x32_bf16 a[196:199],a[164:167],v[172:175],a[196:199]
v_mfma_f32_16x16x32_bf16 a[196:199],a[160:163],v[164:167],a[196:199]
s_add_u32 m0,s82,87040
v_mfma_f32_16x16x32_bf16 a[200:203],a[160:163],v[196:199],a[200:203]
v_mfma_f32_16x16x32_bf16 a[200:203],a[164:167],v[204:207],a[200:203]
v_mfma_f32_16x16x32_bf16 a[200:203],a[168:171],v[212:215],a[200:203]
buffer_load_dwordx4 v7,s[72:75],s83 offen lds
v_mfma_f32_16x16x32_bf16 a[200:203],a[172:175],v[220:223],a[200:203]
v_mfma_f32_16x16x32_bf16 a[204:207],a[172:175],v[252:255],a[204:207]
ds_read_b64_tr_b16 a[172:173],v1 offset:30912
ds_read_b64_tr_b16 a[174:175],v1 offset:36032
v_mfma_f32_16x16x32_bf16 a[204:207],a[168:171],v[244:247],a[204:207]
ds_read_b64_tr_b16 a[168:169],v1 offset:20672
v_mfma_f32_16x16x32_bf16 a[204:207],a[164:167],v[236:239],a[204:207]
ds_read_b64_tr_b16 a[164:165],v1 offset:10432
v_mfma_f32_16x16x32_bf16 a[204:207],a[160:163],v[228:231],a[204:207]
s_barrier
buffer_load_dwordx4 v8,s[72:75],s83 offen offset:1024 lds
v_mfma_f32_16x16x32_bf16 a[208:211],a[176:179],v[132:135],a[208:211]
s_cmp_ge_u32 1,s71
s_cselect_b32 s76,5,3
s_lshl_b32 s76,s76,16
v_mfma_f32_16x16x32_bf16 a[208:211],a[180:183],v[140:143],a[208:211]
s_add_u32 s77,s76,4096
s_add_u32 s78,s76,8192
s_add_u32 s79,s76,12288
v_mfma_f32_16x16x32_bf16 a[208:211],a[184:187],v[148:151],a[208:211]
s_max_i32 s76,s76,s18
s_max_i32 s77,s77,s19
s_max_i32 s78,s78,s100
v_mfma_f32_16x16x32_bf16 a[208:211],a[188:191],v[156:159],a[208:211]
s_max_i32 s79,s79,s101
buffer_load_dwordx4 v9,s[72:75],s83 offen offset:2048 lds
v_mfma_f32_16x16x32_bf16 a[212:215],a[188:191],v[188:191],a[212:215]
ds_read_b64_tr_b16 a[160:161],v1 offset:192
ds_read_b64_tr_b16 a[162:163],v1 offset:5312
v_mfma_f32_16x16x32_bf16 a[212:215],a[184:187],v[180:183],a[212:215]
ds_read_b64_tr_b16 a[166:167],v1 offset:15552
v_mfma_f32_16x16x32_bf16 a[212:215],a[180:183],v[172:175],a[212:215]
ds_read_b64_tr_b16 a[170:171],v1 offset:25792
v_mfma_f32_16x16x32_bf16 a[212:215],a[176:179],v[164:167],a[212:215]
v_mfma_f32_16x16x32_bf16 a[216:219],a[176:179],v[196:199],a[216:219]
buffer_load_dwordx4 v10,s[72:75],s83 offen offset:3072 lds
v_mfma_f32_16x16x32_bf16 a[216:219],a[180:183],v[204:207],a[216:219]
v_mfma_f32_16x16x32_bf16 a[216:219],a[184:187],v[212:215],a[216:219]
s_add_u32 m0,s82,91136
v_mfma_f32_16x16x32_bf16 a[216:219],a[188:191],v[220:223],a[216:219]
v_mfma_f32_16x16x32_bf16 a[220:223],a[188:191],v[252:255],a[220:223]
ds_read_b64_tr_b16 a[188:189],v2 offset:30944
ds_read_b64_tr_b16 a[190:191],v2 offset:36064
v_mfma_f32_16x16x32_bf16 a[220:223],a[184:187],v[244:247],a[220:223]
buffer_load_dwordx4 v11,s[72:75],s83 offen lds
v_mfma_f32_16x16x32_bf16 a[220:223],a[180:183],v[236:239],a[220:223]
ds_read_b64_tr_b16 a[180:181],v2 offset:10464
ds_read_b64_tr_b16 a[182:183],v2 offset:15584
v_mfma_f32_16x16x32_bf16 a[220:223],a[176:179],v[228:231],a[220:223]
s_waitcnt lgkmcnt(4)
ds_read_b64_tr_b16 a[176:177],v2 offset:224
v_mfma_f32_16x16x32_bf16 a[224:227],a[96:99],v[132:135],a[224:227]
ds_read_b64_tr_b16 a[178:179],v2 offset:5344
v_mfma_f32_16x16x32_bf16 a[224:227],a[100:103],v[140:143],a[224:227]
ds_read_b64_tr_b16 a[184:185],v2 offset:20704
v_mfma_f32_16x16x32_bf16 a[224:227],a[104:107],v[148:151],a[224:227]
ds_read_b64_tr_b16 a[186:187],v2 offset:25824
v_mfma_f32_16x16x32_bf16 a[224:227],a[108:111],v[156:159],a[224:227]
v_mfma_f32_16x16x32_bf16 a[228:231],a[108:111],v[188:191],a[228:231]
v_mfma_f32_16x16x32_bf16 a[228:231],a[104:107],v[180:183],a[228:231]
v_mfma_f32_16x16x32_bf16 a[228:231],a[100:103],v[172:175],a[228:231]
v_mfma_f32_16x16x32_bf16 a[228:231],a[96:99],v[164:167],a[228:231]
buffer_load_dwordx4 v[64:67],v6,s[28:31],s76 offen sc1
v_mfma_f32_16x16x32_bf16 a[232:235],a[96:99],v[196:199],a[232:235]
v_mfma_f32_16x16x32_bf16 a[232:235],a[100:103],v[204:207],a[232:235]
v_mfma_f32_16x16x32_bf16 a[232:235],a[104:107],v[212:215],a[232:235]
v_mfma_f32_16x16x32_bf16 a[232:235],a[108:111],v[220:223],a[232:235]
v_mfma_f32_16x16x32_bf16 a[236:239],a[108:111],v[252:255],a[236:239]
buffer_load_dwordx4 v[68:71],v6,s[28:31],s76 offen offset:1024 sc1
v_mfma_f32_16x16x32_bf16 a[236:239],a[104:107],v[244:247],a[236:239]
ds_read_b64_tr_b16 a[104:105],v1 offset:20736
ds_read_b64_tr_b16 a[106:107],v1 offset:25856
v_mfma_f32_16x16x32_bf16 a[236:239],a[100:103],v[236:239],a[236:239]
ds_read_b64_tr_b16 a[100:101],v1 offset:10496
v_mfma_f32_16x16x32_bf16 a[236:239],a[96:99],v[228:231],a[236:239]
ds_read_b64_tr_b16 a[96:97],v1 offset:256
v_mfma_f32_16x16x32_bf16 a[240:243],a[112:115],v[132:135],a[240:243]
ds_read_b64_tr_b16 a[98:99],v1 offset:5376
v_mfma_f32_16x16x32_bf16 a[240:243],a[116:119],v[140:143],a[240:243]
buffer_load_dwordx4 v[72:75],v6,s[28:31],s76 offen offset:2048 sc1
v_mfma_f32_16x16x32_bf16 a[240:243],a[120:123],v[148:151],a[240:243]
ds_read_b64_tr_b16 a[102:103],v1 offset:15616
ds_read_b64_tr_b16 a[108:109],v1 offset:30976
v_mfma_f32_16x16x32_bf16 a[240:243],a[124:127],v[156:159],a[240:243]
)A"
R"A(ds_read_b64_tr_b16 a[110:111],v1 offset:36096
v_add_u32 v1,40960,v1
v_mfma_f32_16x16x32_bf16 a[244:247],a[124:127],v[188:191],a[244:247]
v_add_u32 v42,40960,v1
v_add_u32 v44,81920,v1
v_mfma_f32_16x16x32_bf16 a[244:247],a[120:123],v[180:183],a[244:247]
v_mfma_f32_16x16x32_bf16 a[244:247],a[116:119],v[172:175],a[244:247]
buffer_load_dwordx4 v[76:79],v6,s[28:31],s76 offen offset:3072 sc1
v_mfma_f32_16x16x32_bf16 a[244:247],a[112:115],v[164:167],a[244:247]
v_mfma_f32_16x16x32_bf16 a[248:251],a[112:115],v[196:199],a[248:251]
v_mfma_f32_16x16x32_bf16 a[248:251],a[116:119],v[204:207],a[248:251]
v_mfma_f32_16x16x32_bf16 a[248:251],a[120:123],v[212:215],a[248:251]
v_mfma_f32_16x16x32_bf16 a[248:251],a[124:127],v[220:223],a[248:251]
buffer_load_dwordx4 v[80:83],v6,s[28:31],s77 offen sc1
v_mfma_f32_16x16x32_bf16 a[252:255],a[124:127],v[252:255],a[252:255]
ds_read_b64_tr_b16 a[124:125],v2 offset:31008
ds_read_b64_tr_b16 a[126:127],v2 offset:36128
v_mfma_f32_16x16x32_bf16 a[252:255],a[120:123],v[244:247],a[252:255]
ds_read_b64_tr_b16 a[120:121],v2 offset:20768
v_mfma_f32_16x16x32_bf16 a[252:255],a[116:119],v[236:239],a[252:255]
ds_read_b64_tr_b16 a[116:117],v2 offset:10528
v_mfma_f32_16x16x32_bf16 a[252:255],a[112:115],v[228:231],a[252:255]
ds_read_b64_tr_b16 a[112:113],v2 offset:288
v_mfma_f32_16x16x32_bf16 a[0:3],a[128:131],v[132:135],a[0:3]
buffer_load_dwordx4 v[84:87],v6,s[28:31],s77 offen offset:1024 sc1
v_mfma_f32_16x16x32_bf16 a[0:3],a[132:135],v[140:143],a[0:3]
ds_read_b64_tr_b16 a[114:115],v2 offset:5408
ds_read_b64_tr_b16 a[118:119],v2 offset:15648
v_mfma_f32_16x16x32_bf16 a[0:3],a[136:139],v[148:151],a[0:3]
ds_read_b64_tr_b16 a[122:123],v2 offset:25888
v_add_u32 v2,40960,v2
v_mfma_f32_16x16x32_bf16 a[0:3],a[140:143],v[156:159],a[0:3]
v_add_u32 v43,40960,v2
v_add_u32 v45,81920,v2
v_mfma_f32_16x16x32_bf16 a[4:7],a[140:143],v[188:191],a[4:7]
v_mfma_f32_16x16x32_bf16 a[4:7],a[136:139],v[180:183],a[4:7]
buffer_load_dwordx4 v[88:91],v6,s[28:31],s77 offen offset:2048 sc1
v_mfma_f32_16x16x32_bf16 a[4:7],a[132:135],v[172:175],a[4:7]
v_mfma_f32_16x16x32_bf16 a[4:7],a[128:131],v[164:167],a[4:7]
v_mfma_f32_16x16x32_bf16 a[8:11],a[128:131],v[196:199],a[8:11]
v_mfma_f32_16x16x32_bf16 a[8:11],a[132:135],v[204:207],a[8:11]
v_mfma_f32_16x16x32_bf16 a[8:11],a[136:139],v[212:215],a[8:11]
buffer_load_dwordx4 v[92:95],v6,s[28:31],s77 offen offset:3072 sc1
v_mfma_f32_16x16x32_bf16 a[8:11],a[140:143],v[220:223],a[8:11]
v_mfma_f32_16x16x32_bf16 a[12:15],a[140:143],v[252:255],a[12:15]
ds_read_b64_tr_b16 a[140:141],v1 offset:30720
ds_read_b64_tr_b16 a[142:143],v1 offset:35840
v_mfma_f32_16x16x32_bf16 a[12:15],a[136:139],v[244:247],a[12:15]
ds_read_b64_tr_b16 a[136:137],v1 offset:20480
v_mfma_f32_16x16x32_bf16 a[12:15],a[132:135],v[236:239],a[12:15]
ds_read_b64_tr_b16 a[132:133],v1 offset:10240
v_mfma_f32_16x16x32_bf16 a[12:15],a[128:131],v[228:231],a[12:15]
buffer_load_dwordx4 v[96:99],v6,s[28:31],s78 offen sc1
v_mfma_f32_16x16x32_bf16 a[16:19],a[144:147],v[132:135],a[16:19]
ds_read_b64_tr_b16 a[128:129],v1
ds_read_b64_tr_b16 a[130:131],v1 offset:5120
v_mfma_f32_16x16x32_bf16 a[16:19],a[148:151],v[140:143],a[16:19]
ds_read_b64_tr_b16 a[134:135],v1 offset:15360
v_mfma_f32_16x16x32_bf16 a[16:19],a[152:155],v[148:151],a[16:19]
ds_read_b64_tr_b16 a[138:139],v1 offset:25600
v_mfma_f32_16x16x32_bf16 a[16:19],a[156:159],v[156:159],a[16:19]
v_mfma_f32_16x16x32_bf16 a[20:23],a[156:159],v[188:191],a[20:23]
buffer_load_dwordx4 v[100:103],v6,s[28:31],s78 offen offset:1024 sc1
v_mfma_f32_16x16x32_bf16 a[20:23],a[152:155],v[180:183],a[20:23]
v_mfma_f32_16x16x32_bf16 a[20:23],a[148:151],v[172:175],a[20:23]
v_mfma_f32_16x16x32_bf16 a[20:23],a[144:147],v[164:167],a[20:23]
v_mfma_f32_16x16x32_bf16 a[24:27],a[144:147],v[196:199],a[24:27]
v_mfma_f32_16x16x32_bf16 a[24:27],a[148:151],v[204:207],a[24:27]
buffer_load_dwordx4 v[104:107],v6,s[28:31],s78 offen offset:2048 sc1
v_mfma_f32_16x16x32_bf16 a[24:27],a[152:155],v[212:215],a[24:27]
v_mfma_f32_16x16x32_bf16 a[24:27],a[156:159],v[220:223],a[24:27]
v_mfma_f32_16x16x32_bf16 a[28:31],a[156:159],v[252:255],a[28:31]
ds_read_b64_tr_b16 a[156:157],v2 offset:30752
ds_read_b64_tr_b16 a[158:159],v2 offset:35872
v_mfma_f32_16x16x32_bf16 a[28:31],a[152:155],v[244:247],a[28:31]
ds_read_b64_tr_b16 a[152:153],v2 offset:20512
v_mfma_f32_16x16x32_bf16 a[28:31],a[148:151],v[236:239],a[28:31]
buffer_load_dwordx4 v[108:111],v6,s[28:31],s78 offen offset:3072 sc1
v_mfma_f32_16x16x32_bf16 a[28:31],a[144:147],v[228:231],a[28:31]
s_cmp_ge_u32 2,s71
s_cselect_b32 s80,7,5
s_mul_i32 s80,s80,163840
v_mfma_f32_16x16x32_bf16 a[32:35],a[160:163],v[132:135],a[32:35]
s_add_u32 s80,s80,s81
s_add_u32 s83,s80,20480
ds_read_b64_tr_b16 a[144:145],v2 offset:32
v_mfma_f32_16x16x32_bf16 a[32:35],a[164:167],v[140:143],a[32:35]
ds_read_b64_tr_b16 a[146:147],v2 offset:5152
ds_read_b64_tr_b16 a[148:149],v2 offset:10272
v_mfma_f32_16x16x32_bf16 a[32:35],a[168:171],v[148:151],a[32:35]
ds_read_b64_tr_b16 a[150:151],v2 offset:15392
v_mfma_f32_16x16x32_bf16 a[32:35],a[172:175],v[156:159],a[32:35]
buffer_load_dwordx4 v[112:115],v6,s[28:31],s79 offen sc1
v_mfma_f32_16x16x32_bf16 a[36:39],a[172:175],v[188:191],a[36:39]
ds_read_b64_tr_b16 a[154:155],v2 offset:25632
v_mfma_f32_16x16x32_bf16 a[36:39],a[168:171],v[180:183],a[36:39]
v_mfma_f32_16x16x32_bf16 a[36:39],a[164:167],v[172:175],a[36:39]
v_mfma_f32_16x16x32_bf16 a[36:39],a[160:163],v[164:167],a[36:39]
v_mfma_f32_16x16x32_bf16 a[40:43],a[160:163],v[196:199],a[40:43]
buffer_load_dwordx4 v[116:119],v6,s[28:31],s79 offen offset:1024 sc1
v_mfma_f32_16x16x32_bf16 a[40:43],a[164:167],v[204:207],a[40:43]
v_mfma_f32_16x16x32_bf16 a[40:43],a[168:171],v[212:215],a[40:43]
v_mfma_f32_16x16x32_bf16 a[40:43],a[172:175],v[220:223],a[40:43]
v_mfma_f32_16x16x32_bf16 a[44:47],a[172:175],v[252:255],a[44:47]
ds_read_b64_tr_b16 a[172:173],v1 offset:30784
ds_read_b64_tr_b16 a[174:175],v1 offset:35904
v_mfma_f32_16x16x32_bf16 a[44:47],a[168:171],v[244:247],a[44:47]
buffer_load_dwordx4 v[120:123],v6,s[28:31],s79 offen offset:2048 sc1
v_mfma_f32_16x16x32_bf16 a[44:47],a[164:167],v[236:239],a[44:47]
ds_read_b64_tr_b16 a[164:165],v1 offset:10304
ds_read_b64_tr_b16 a[166:167],v1 offset:15424
v_mfma_f32_16x16x32_bf16 a[44:47],a[160:163],v[228:231],a[44:47]
s_waitcnt lgkmcnt(4)
ds_read_b64_tr_b16 a[160:161],v1 offset:64
v_mfma_f32_16x16x32_bf16 a[48:51],a[176:179],v[132:135],a[48:51]
ds_read_b64_tr_b16 a[162:163],v1 offset:5184
v_mfma_f32_16x16x32_bf16 a[48:51],a[180:183],v[140:143],a[48:51]
ds_read_b64_tr_b16 a[168:169],v1 offset:20544
v_mfma_f32_16x16x32_bf16 a[48:51],a[184:187],v[148:151],a[48:51]
buffer_load_dwordx4 v[124:127],v6,s[28:31],s79 offen offset:3072 sc1
v_mfma_f32_16x16x32_bf16 a[48:51],a[188:191],v[156:159],a[48:51]
ds_read_b64_tr_b16 a[170:171],v1 offset:25664
v_mfma_f32_16x16x32_bf16 a[52:55],a[188:191],v[188:191],a[52:55]
s_add_u32 m0,s82,122880
v_mfma_f32_16x16x32_bf16 a[52:55],a[184:187],v[180:183],a[52:55]
v_mfma_f32_16x16x32_bf16 a[52:55],a[180:183],v[172:175],a[52:55]
v_mfma_f32_16x16x32_bf16 a[52:55],a[176:179],v[164:167],a[52:55]
buffer_load_dwordx4 v7,s[72:75],s80 offen lds
v_mfma_f32_16x16x32_bf16 a[56:59],a[176:179],v[196:199],a[56:59]
v_mfma_f32_16x16x32_bf16 a[56:59],a[180:183],v[204:207],a[56:59]
v_mfma_f32_16x16x32_bf16 a[56:59],a[184:187],v[212:215],a[56:59]
v_mfma_f32_16x16x32_bf16 a[56:59],a[188:191],v[220:223],a[56:59]
v_mfma_f32_16x16x32_bf16 a[60:63],a[188:191],v[252:255],a[60:63]
buffer_load_dwordx4 v8,s[72:75],s80 offen offset:1024 lds
v_mfma_f32_16x16x32_bf16 a[60:63],a[184:187],v[244:247],a[60:63]
ds_read_b64_tr_b16 a[184:185],v2 offset:20576
ds_read_b64_tr_b16 a[186:187],v2 offset:25696
v_mfma_f32_16x16x32_bf16 a[60:63],a[180:183],v[236:239],a[60:63]
ds_read_b64_tr_b16 a[180:181],v2 offset:10336
v_mfma_f32_16x16x32_bf16 a[60:63],a[176:179],v[228:231],a[60:63]
ds_read_b64_tr_b16 a[176:177],v2 offset:96
v_mfma_f32_16x16x32_bf16 a[64:67],a[96:99],v[132:135],a[64:67]
ds_read_b64_tr_b16 a[178:179],v2 offset:5216
v_mfma_f32_16x16x32_bf16 a[64:67],a[100:103],v[140:143],a[64:67]
buffer_load_dwordx4 v9,s[72:75],s80 offen offset:2048 lds
v_mfma_f32_16x16x32_bf16 a[64:67],a[104:107],v[148:151],a[64:67]
ds_read_b64_tr_b16 a[182:183],v2 offset:15456
ds_read_b64_tr_b16 a[188:189],v2 offset:30816
v_mfma_f32_16x16x32_bf16 a[64:67],a[108:111],v[156:159],a[64:67]
ds_read_b64_tr_b16 a[190:191],v2 offset:35936
v_mfma_f32_16x16x32_bf16 a[68:71],a[108:111],v[188:191],a[68:71]
v_mfma_f32_16x16x32_bf16 a[68:71],a[104:107],v[180:183],a[68:71]
v_mfma_f32_16x16x32_bf16 a[68:71],a[100:103],v[172:175],a[68:71]
buffer_load_dwordx4 v10,s[72:75],s80 offen offset:3072 lds
v_mfma_f32_16x16x32_bf16 a[68:71],a[96:99],v[164:167],a[68:71]
v_mfma_f32_16x16x32_bf16 a[72:75],a[96:99],v[196:199],a[72:75]
s_add_u32 m0,s82,126976
v_mfma_f32_16x16x32_bf16 a[72:75],a[100:103],v[204:207],a[72:75]
v_mfma_f32_16x16x32_bf16 a[72:75],a[104:107],v[212:215],a[72:75]
s_cmp_ge_u32 2,s71
s_cselect_b32 s76,7,5
s_lshl_b32 s76,s76,16
v_mfma_f32_16x16x32_bf16 a[72:75],a[108:111],v[220:223],a[72:75]
s_add_u32 s77,s76,4096
buffer_load_dwordx4 v11,s[72:75],s80 offen lds
v_mfma_f32_16x16x32_bf16 a[76:79],a[108:111],v[252:255],a[76:79]
s_add_u32 s78,s76,8192
s_add_u32 s79,s76,12288
s_max_i32 s76,s76,s18
v_mfma_f32_16x16x32_bf16 a[76:79],a[104:107],v[244:247],a[76:79]
s_add_u32 m0,s82,128000
s_max_i32 s77,s77,s19
ds_read_b64_tr_b16 a[104:105],v1 offset:20608
v_mfma_f32_16x16x32_bf16 a[76:79],a[100:103],v[236:239],a[76:79]
s_max_i32 s78,s78,s100
ds_read_b64_tr_b16 a[100:101],v1 offset:10368
ds_read_b64_tr_b16 a[102:103],v1 offset:15488
v_mfma_f32_16x16x32_bf16 a[76:79],a[96:99],v[228:231],a[76:79]
s_max_i32 s79,s79,s101
ds_read_b64_tr_b16 a[96:97],v1 offset:128
v_mfma_f32_16x16x32_bf16 a[80:83],a[112:115],v[132:135],a[80:83]
buffer_load_dwordx4 v7,s[72:75],s83 offen lds
v_mfma_f32_16x16x32_bf16 a[80:83],a[116:119],v[140:143],a[80:83]
ds_read_b64_tr_b16 a[98:99],v1 offset:5248
ds_read_b64_tr_b16 a[106:107],v1 offset:25728
v_mfma_f32_16x16x32_bf16 a[80:83],a[120:123],v[148:151],a[80:83]
ds_read_b64_tr_b16 a[108:109],v1 offset:30848
v_mfma_f32_16x16x32_bf16 a[80:83],a[124:127],v[156:159],a[80:83]
ds_read_b64_tr_b16 a[110:111],v1 offset:35968
v_mfma_f32_16x16x32_bf16 a[84:87],a[124:127],v[188:191],a[84:87]
v_mfma_f32_16x16x32_bf16 a[84:87],a[120:123],v[180:183],a[84:87]
buffer_load_dwordx4 v8,s[72:75],s83 offen offset:1024 lds
v_mfma_f32_16x16x32_bf16 a[84:87],a[116:119],v[172:175],a[84:87]
v_mfma_f32_16x16x32_bf16 a[84:87],a[112:115],v[164:167],a[84:87]
v_mfma_f32_16x16x32_bf16 a[88:91],a[112:115],v[196:199],a[88:91]
v_mfma_f32_16x16x32_bf16 a[88:91],a[116:119],v[204:207],a[88:91]
v_mfma_f32_16x16x32_bf16 a[88:91],a[120:123],v[212:215],a[88:91]
buffer_load_dwordx4 v9,s[72:75],s83 offen offset:2048 lds
v_mfma_f32_16x16x32_bf16 a[88:91],a[124:127],v[220:223],a[88:91]
v_mfma_f32_16x16x32_bf16 a[92:95],a[124:127],v[252:255],a[92:95]
ds_read_b64_tr_b16 a[124:125],v2 offset:30880
ds_read_b64_tr_b16 a[126:127],v2 offset:36000
v_mfma_f32_16x16x32_bf16 a[92:95],a[120:123],v[244:247],a[92:95]
ds_read_b64_tr_b16 a[120:121],v2 offset:20640
v_mfma_f32_16x16x32_bf16 a[92:95],a[116:119],v[236:239],a[92:95]
ds_read_b64_tr_b16 a[116:117],v2 offset:10400
v_mfma_f32_16x16x32_bf16 a[92:95],a[112:115],v[228:231],a[92:95]
buffer_load_dwordx4 v10,s[72:75],s83 offen offset:3072 lds
v_mfma_f32_16x16x32_bf16 a[192:195],a[128:131],v[128:131],a[192:195]
ds_read_b64_tr_b16 a[112:113],v2 offset:160
ds_read_b64_tr_b16 a[114:115],v2 offset:5280
v_mfma_f32_16x16x32_bf16 a[192:195],a[132:135],v[160:163],a[192:195]
s_add_u32 m0,s82,132096
ds_read_b64_tr_b16 a[118:119],v2 offset:15520
v_mfma_f32_16x16x32_bf16 a[192:195],a[136:139],v[192:195],a[192:195]
ds_read_b64_tr_b16 a[122:123],v2 offset:25760
v_mfma_f32_16x16x32_bf16 a[192:195],a[140:143],v[224:227],a[192:195]
buffer_load_dwordx4 v[132:135],v6,s[28:31],s76 offen sc1
v_mfma_f32_16x16x32_bf16 a[196:199],a[140:143],v[232:235],a[196:199]
v_mfma_f32_16x16x32_bf16 a[196:199],a[136:139],v[200:203],a[196:199]
v_mfma_f32_16x16x32_bf16 a[196:199],a[132:135],v[168:171],a[196:199]
)A"
R"A(v_mfma_f32_16x16x32_bf16 a[196:199],a[128:131],v[136:139],a[196:199]
s_waitcnt vmcnt(14)
buffer_load_dwordx4 v11,s[72:75],s83 offen lds
v_mfma_f32_16x16x32_bf16 a[200:203],a[128:131],v[144:147],a[200:203]
ds_read_b64_tr_b16 a[128:129],v1 offset:192
ds_read_b64_tr_b16 a[130:131],v1 offset:5312
v_mfma_f32_16x16x32_bf16 a[200:203],a[132:135],v[176:179],a[200:203]
ds_read_b64_tr_b16 a[132:133],v1 offset:10432
v_mfma_f32_16x16x32_bf16 a[200:203],a[136:139],v[208:211],a[200:203]
ds_read_b64_tr_b16 a[134:135],v1 offset:15552
v_mfma_f32_16x16x32_bf16 a[200:203],a[140:143],v[240:243],a[200:203]
buffer_load_dwordx4 v[140:143],v6,s[28:31],s76 offen offset:1024 sc1
v_mfma_f32_16x16x32_bf16 a[208:211],a[144:147],v[128:131],a[208:211]
ds_read_b64_tr_b16 a[136:137],v1 offset:20672
ds_read_b64_tr_b16 a[138:139],v1 offset:25792
v_mfma_f32_16x16x32_bf16 a[208:211],a[148:151],v[160:163],a[208:211]
ds_read_b64_tr_b16 a[140:141],v1 offset:30912
v_mfma_f32_16x16x32_bf16 a[208:211],a[152:155],v[192:195],a[208:211]
ds_read_b64_tr_b16 a[142:143],v1 offset:36032
v_mfma_f32_16x16x32_bf16 a[208:211],a[156:159],v[224:227],a[208:211]
buffer_load_dwordx4 v[148:151],v6,s[28:31],s76 offen offset:2048 sc1
v_mfma_f32_16x16x32_bf16 a[212:215],a[156:159],v[232:235],a[212:215]
v_mfma_f32_16x16x32_bf16 a[212:215],a[152:155],v[200:203],a[212:215]
v_mfma_f32_16x16x32_bf16 a[212:215],a[148:151],v[168:171],a[212:215]
v_mfma_f32_16x16x32_bf16 a[212:215],a[144:147],v[136:139],a[212:215]
buffer_load_dwordx4 v[156:159],v6,s[28:31],s76 offen offset:3072 sc1
v_mfma_f32_16x16x32_bf16 a[216:219],a[144:147],v[144:147],a[216:219]
s_mul_i32 s76,s70,12
ds_read_b64_tr_b16 a[144:145],v2 offset:224
ds_read_b64_tr_b16 a[146:147],v2 offset:5344
v_mfma_f32_16x16x32_bf16 a[216:219],a[148:151],v[176:179],a[216:219]
s_add_u32 s76,s76,0
s_add_u32 s76,s76,s40
ds_read_b64_tr_b16 a[148:149],v2 offset:10464
v_mfma_f32_16x16x32_bf16 a[216:219],a[152:155],v[208:211],a[216:219]
s_mul_i32 s76,s76,5120
s_max_i32 s76,s76,s101
ds_read_b64_tr_b16 a[150:151],v2 offset:15584
v_mfma_f32_16x16x32_bf16 a[216:219],a[156:159],v[240:243],a[216:219]
s_waitcnt lgkmcnt(4)
buffer_load_dwordx4 v[164:167],v6,s[28:31],s77 offen sc1
v_mfma_f32_16x16x32_bf16 a[224:227],a[160:163],v[128:131],a[224:227]
ds_read_b64_tr_b16 a[152:153],v2 offset:20704
ds_read_b64_tr_b16 a[154:155],v2 offset:25824
v_mfma_f32_16x16x32_bf16 a[224:227],a[164:167],v[160:163],a[224:227]
ds_read_b64_tr_b16 a[156:157],v2 offset:30944
v_mfma_f32_16x16x32_bf16 a[224:227],a[168:171],v[192:195],a[224:227]
ds_read_b64_tr_b16 a[158:159],v2 offset:36064
v_mfma_f32_16x16x32_bf16 a[224:227],a[172:175],v[224:227],a[224:227]
buffer_load_dwordx4 v[172:175],v6,s[28:31],s77 offen offset:1024 sc1
v_mfma_f32_16x16x32_bf16 a[228:231],a[172:175],v[232:235],a[228:231]
v_mfma_f32_16x16x32_bf16 a[228:231],a[168:171],v[200:203],a[228:231]
v_mfma_f32_16x16x32_bf16 a[228:231],a[164:167],v[168:171],a[228:231]
v_mfma_f32_16x16x32_bf16 a[228:231],a[160:163],v[136:139],a[228:231]
v_mfma_f32_16x16x32_bf16 a[232:235],a[160:163],v[144:147],a[232:235]
buffer_load_dwordx4 v[180:183],v6,s[28:31],s77 offen offset:2048 sc1
v_mfma_f32_16x16x32_bf16 a[232:235],a[164:167],v[176:179],a[232:235]
ds_read_b64_tr_b16 a[160:161],v1 offset:256
ds_read_b64_tr_b16 a[162:163],v1 offset:5376
v_mfma_f32_16x16x32_bf16 a[232:235],a[168:171],v[208:211],a[232:235]
ds_read_b64_tr_b16 a[164:165],v1 offset:10496
v_mfma_f32_16x16x32_bf16 a[232:235],a[172:175],v[240:243],a[232:235]
ds_read_b64_tr_b16 a[166:167],v1 offset:15616
v_mfma_f32_16x16x32_bf16 a[240:243],a[176:179],v[128:131],a[240:243]
ds_read_b64_tr_b16 a[168:169],v1 offset:20736
v_mfma_f32_16x16x32_bf16 a[240:243],a[180:183],v[160:163],a[240:243]
buffer_load_dwordx4 v[188:191],v6,s[28:31],s77 offen offset:3072 sc1
v_mfma_f32_16x16x32_bf16 a[240:243],a[184:187],v[192:195],a[240:243]
s_mul_i32 s77,s70,12
ds_read_b64_tr_b16 a[170:171],v1 offset:25856
ds_read_b64_tr_b16 a[172:173],v1 offset:30976
v_mfma_f32_16x16x32_bf16 a[240:243],a[188:191],v[224:227],a[240:243]
s_add_u32 s77,s77,4
s_add_u32 s77,s77,s40
ds_read_b64_tr_b16 a[174:175],v1 offset:36096
v_mfma_f32_16x16x32_bf16 a[244:247],a[188:191],v[232:235],a[244:247]
s_mul_i32 s77,s77,5120
s_max_i32 s77,s77,s101
v_mfma_f32_16x16x32_bf16 a[244:247],a[184:187],v[200:203],a[244:247]
v_mfma_f32_16x16x32_bf16 a[244:247],a[180:183],v[168:171],a[244:247]
buffer_load_dwordx4 v[196:199],v6,s[28:31],s78 offen sc1
v_mfma_f32_16x16x32_bf16 a[244:247],a[176:179],v[136:139],a[244:247]
v_mfma_f32_16x16x32_bf16 a[248:251],a[176:179],v[144:147],a[248:251]
ds_read_b64_tr_b16 a[176:177],v2 offset:288
ds_read_b64_tr_b16 a[178:179],v2 offset:5408
v_mfma_f32_16x16x32_bf16 a[248:251],a[180:183],v[176:179],a[248:251]
ds_read_b64_tr_b16 a[180:181],v2 offset:10528
v_mfma_f32_16x16x32_bf16 a[248:251],a[184:187],v[208:211],a[248:251]
ds_read_b64_tr_b16 a[182:183],v2 offset:15648
v_mfma_f32_16x16x32_bf16 a[248:251],a[188:191],v[240:243],a[248:251]
buffer_load_dwordx4 v[204:207],v6,s[28:31],s78 offen offset:1024 sc1
v_mfma_f32_16x16x32_bf16 a[0:3],a[96:99],v[128:131],a[0:3]
ds_read_b64_tr_b16 a[184:185],v2 offset:20768
ds_read_b64_tr_b16 a[186:187],v2 offset:25888
v_mfma_f32_16x16x32_bf16 a[0:3],a[100:103],v[160:163],a[0:3]
ds_read_b64_tr_b16 a[188:189],v2 offset:31008
v_mfma_f32_16x16x32_bf16 a[0:3],a[104:107],v[192:195],a[0:3]
ds_read_b64_tr_b16 a[190:191],v2 offset:36128
v_mfma_f32_16x16x32_bf16 a[0:3],a[108:111],v[224:227],a[0:3]
v_mfma_f32_16x16x32_bf16 a[4:7],a[108:111],v[232:235],a[4:7]
buffer_load_dwordx4 v[212:215],v6,s[28:31],s78 offen offset:2048 sc1
v_mfma_f32_16x16x32_bf16 a[4:7],a[104:107],v[200:203],a[4:7]
v_mfma_f32_16x16x32_bf16 a[4:7],a[100:103],v[168:171],a[4:7]
v_mfma_f32_16x16x32_bf16 a[4:7],a[96:99],v[136:139],a[4:7]
v_mfma_f32_16x16x32_bf16 a[8:11],a[96:99],v[144:147],a[8:11]
v_mfma_f32_16x16x32_bf16 a[8:11],a[100:103],v[176:179],a[8:11]
buffer_load_dwordx4 v[220:223],v6,s[28:31],s78 offen offset:3072 sc1
v_mfma_f32_16x16x32_bf16 a[8:11],a[104:107],v[208:211],a[8:11]
s_mul_i32 s78,s70,12
s_add_u32 s78,s78,8
s_add_u32 s78,s78,s40
v_mfma_f32_16x16x32_bf16 a[8:11],a[108:111],v[240:243],a[8:11]
s_mul_i32 s78,s78,5120
s_max_i32 s78,s78,s101
v_mfma_f32_16x16x32_bf16 a[16:19],a[112:115],v[128:131],a[16:19]
v_mfma_f32_16x16x32_bf16 a[16:19],a[116:119],v[160:163],a[16:19]
v_mfma_f32_16x16x32_bf16 a[16:19],a[120:123],v[192:195],a[16:19]
buffer_load_dwordx4 v[228:231],v6,s[28:31],s79 offen sc1
v_mfma_f32_16x16x32_bf16 a[16:19],a[124:127],v[224:227],a[16:19]
v_mfma_f32_16x16x32_bf16 a[20:23],a[124:127],v[232:235],a[20:23]
v_mfma_f32_16x16x32_bf16 a[20:23],a[120:123],v[200:203],a[20:23]
v_mfma_f32_16x16x32_bf16 a[20:23],a[116:119],v[168:171],a[20:23]
v_mfma_f32_16x16x32_bf16 a[20:23],a[112:115],v[136:139],a[20:23]
buffer_load_dwordx4 v[236:239],v6,s[28:31],s79 offen offset:1024 sc1
v_mfma_f32_16x16x32_bf16 a[24:27],a[112:115],v[144:147],a[24:27]
v_mfma_f32_16x16x32_bf16 a[24:27],a[116:119],v[176:179],a[24:27]
v_mfma_f32_16x16x32_bf16 a[24:27],a[120:123],v[208:211],a[24:27]
v_mfma_f32_16x16x32_bf16 a[24:27],a[124:127],v[240:243],a[24:27]
v_mfma_f32_16x16x32_bf16 a[32:35],a[128:131],v[128:131],a[32:35]
buffer_load_dwordx4 v[244:247],v6,s[28:31],s79 offen offset:2048 sc1
v_mfma_f32_16x16x32_bf16 a[32:35],a[132:135],v[160:163],a[32:35]
v_mfma_f32_16x16x32_bf16 a[32:35],a[136:139],v[192:195],a[32:35]
v_mfma_f32_16x16x32_bf16 a[32:35],a[140:143],v[224:227],a[32:35]
v_mfma_f32_16x16x32_bf16 a[36:39],a[140:143],v[232:235],a[36:39]
v_mfma_f32_16x16x32_bf16 a[36:39],a[136:139],v[200:203],a[36:39]
buffer_load_dwordx4 v[252:255],v6,s[28:31],s79 offen offset:3072 sc1
v_mfma_f32_16x16x32_bf16 a[36:39],a[132:135],v[168:171],a[36:39]
v_mfma_f32_16x16x32_bf16 a[36:39],a[128:131],v[136:139],a[36:39]
v_mfma_f32_16x16x32_bf16 a[40:43],a[128:131],v[144:147],a[40:43]
v_mfma_f32_16x16x32_bf16 a[40:43],a[132:135],v[176:179],a[40:43]
v_mfma_f32_16x16x32_bf16 a[40:43],a[136:139],v[208:211],a[40:43]
s_waitcnt vmcnt(15)
v_mfma_f32_16x16x32_bf16 a[40:43],a[140:143],v[240:243],a[40:43]
s_barrier
ds_read_b64_tr_b16 a[96:97],v42
ds_read_b64_tr_b16 a[98:99],v42 offset:5120
s_waitcnt lgkmcnt(2)
v_mfma_f32_16x16x32_bf16 a[48:51],a[144:147],v[128:131],a[48:51]
ds_read_b64_tr_b16 a[100:101],v42 offset:10240
v_mfma_f32_16x16x32_bf16 a[48:51],a[148:151],v[160:163],a[48:51]
ds_read_b64_tr_b16 a[102:103],v42 offset:15360
v_mfma_f32_16x16x32_bf16 a[48:51],a[152:155],v[192:195],a[48:51]
ds_read_b64_tr_b16 a[104:105],v42 offset:20480
v_mfma_f32_16x16x32_bf16 a[48:51],a[156:159],v[224:227],a[48:51]
ds_read_b64_tr_b16 a[106:107],v42 offset:25600
ds_read_b64_tr_b16 a[108:109],v42 offset:30720
v_mfma_f32_16x16x32_bf16 a[52:55],a[156:159],v[232:235],a[52:55]
ds_read_b64_tr_b16 a[110:111],v42 offset:35840
v_mfma_f32_16x16x32_bf16 a[52:55],a[152:155],v[200:203],a[52:55]
ds_read_b64_tr_b16 a[112:113],v44
v_mfma_f32_16x16x32_bf16 a[52:55],a[148:151],v[168:171],a[52:55]
ds_read_b64_tr_b16 a[114:115],v44 offset:5120
v_mfma_f32_16x16x32_bf16 a[52:55],a[144:147],v[136:139],a[52:55]
ds_read_b64_tr_b16 a[116:117],v44 offset:10240
ds_read_b64_tr_b16 a[118:119],v44 offset:15360
v_mfma_f32_16x16x32_bf16 a[56:59],a[144:147],v[144:147],a[56:59]
ds_read_b64_tr_b16 a[120:121],v44 offset:20480
v_mfma_f32_16x16x32_bf16 a[56:59],a[148:151],v[176:179],a[56:59]
ds_read_b64_tr_b16 a[122:123],v44 offset:25600
v_mfma_f32_16x16x32_bf16 a[56:59],a[152:155],v[208:211],a[56:59]
ds_read_b64_tr_b16 a[124:125],v44 offset:30720
v_mfma_f32_16x16x32_bf16 a[56:59],a[156:159],v[240:243],a[56:59]
ds_read_b64_tr_b16 a[126:127],v44 offset:35840
ds_read_b64_tr_b16 a[128:129],v43 offset:32
v_mfma_f32_16x16x32_bf16 a[64:67],a[160:163],v[128:131],a[64:67]
ds_read_b64_tr_b16 a[130:131],v43 offset:5152
v_mfma_f32_16x16x32_bf16 a[64:67],a[164:167],v[160:163],a[64:67]
ds_read_b64_tr_b16 a[132:133],v43 offset:10272
v_mfma_f32_16x16x32_bf16 a[64:67],a[168:171],v[192:195],a[64:67]
ds_read_b64_tr_b16 a[134:135],v43 offset:15392
v_mfma_f32_16x16x32_bf16 a[64:67],a[172:175],v[224:227],a[64:67]
ds_read_b64_tr_b16 a[136:137],v43 offset:20512
ds_read_b64_tr_b16 a[138:139],v43 offset:25632
v_mfma_f32_16x16x32_bf16 a[68:71],a[172:175],v[232:235],a[68:71]
ds_read_b64_tr_b16 a[140:141],v43 offset:30752
v_mfma_f32_16x16x32_bf16 a[68:71],a[168:171],v[200:203],a[68:71]
ds_read_b64_tr_b16 a[142:143],v43 offset:35872
v_mfma_f32_16x16x32_bf16 a[68:71],a[164:167],v[168:171],a[68:71]
ds_read_b64_tr_b16 a[144:145],v45 offset:32
v_mfma_f32_16x16x32_bf16 a[68:71],a[160:163],v[136:139],a[68:71]
ds_read_b64_tr_b16 a[146:147],v45 offset:5152
ds_read_b64_tr_b16 a[148:149],v45 offset:10272
v_mfma_f32_16x16x32_bf16 a[72:75],a[160:163],v[144:147],a[72:75]
ds_read_b64_tr_b16 a[150:151],v45 offset:15392
v_mfma_f32_16x16x32_bf16 a[72:75],a[164:167],v[176:179],a[72:75]
ds_read_b64_tr_b16 a[152:153],v45 offset:20512
v_mfma_f32_16x16x32_bf16 a[72:75],a[168:171],v[208:211],a[72:75]
ds_read_b64_tr_b16 a[154:155],v45 offset:25632
v_mfma_f32_16x16x32_bf16 a[72:75],a[172:175],v[240:243],a[72:75]
ds_read_b64_tr_b16 a[156:157],v45 offset:30752
ds_read_b64_tr_b16 a[158:159],v45 offset:35872
v_mfma_f32_16x16x32_bf16 a[80:83],a[176:179],v[128:131],a[80:83]
ds_read_b64_tr_b16 a[160:161],v42 offset:64
v_mfma_f32_16x16x32_bf16 a[80:83],a[180:183],v[160:163],a[80:83]
ds_read_b64_tr_b16 a[162:163],v42 offset:5184
v_mfma_f32_16x16x32_bf16 a[80:83],a[184:187],v[192:195],a[80:83]
ds_read_b64_tr_b16 a[164:165],v42 offset:10304
v_mfma_f32_16x16x32_bf16 a[80:83],a[188:191],v[224:227],a[80:83]
ds_read_b64_tr_b16 a[166:167],v42 offset:15424
ds_read_b64_tr_b16 a[168:169],v42 offset:20544
v_mfma_f32_16x16x32_bf16 a[84:87],a[188:191],v[232:235],a[84:87]
ds_read_b64_tr_b16 a[170:171],v42 offset:25664
v_mfma_f32_16x16x32_bf16 a[84:87],a[184:187],v[200:203],a[84:87]
ds_read_b64_tr_b16 a[172:173],v42 offset:30784
v_mfma_f32_16x16x32_bf16 a[84:87],a[180:183],v[168:171],a[84:87]
ds_read_b64_tr_b16 a[174:175],v42 offset:35904
v_mfma_f32_16x16x32_bf16 a[84:87],a[176:179],v[136:139],a[84:87]
v_mfma_f32_16x16x32_bf16 a[88:91],a[176:179],v[144:147],a[88:91]
)A"
R"A(ds_read_b64_tr_b16 a[176:177],v44 offset:64
ds_read_b64_tr_b16 a[178:179],v44 offset:5184
v_mfma_f32_16x16x32_bf16 a[88:91],a[180:183],v[176:179],a[88:91]
ds_read_b64_tr_b16 a[180:181],v44 offset:10304
v_mfma_f32_16x16x32_bf16 a[88:91],a[184:187],v[208:211],a[88:91]
ds_read_b64_tr_b16 a[182:183],v44 offset:15424
v_mfma_f32_16x16x32_bf16 a[88:91],a[188:191],v[240:243],a[88:91]
s_waitcnt lgkmcnt(7)
ds_read_b64_tr_b16 a[184:185],v44 offset:20544
v_mfma_f32_16x16x32_bf16 a[192:195],a[96:99],v[64:67],a[192:195]
ds_read_b64_tr_b16 a[186:187],v44 offset:25664
ds_read_b64_tr_b16 a[188:189],v44 offset:30784
v_mfma_f32_16x16x32_bf16 a[192:195],a[100:103],v[68:71],a[192:195]
ds_read_b64_tr_b16 a[190:191],v44 offset:35904
v_mfma_f32_16x16x32_bf16 a[192:195],a[104:107],v[72:75],a[192:195]
v_mfma_f32_16x16x32_bf16 a[192:195],a[108:111],v[76:79],a[192:195]
v_mfma_f32_16x16x32_bf16 a[196:199],a[108:111],v[92:95],a[196:199]
v_mfma_f32_16x16x32_bf16 a[196:199],a[104:107],v[88:91],a[196:199]
v_mfma_f32_16x16x32_bf16 a[196:199],a[100:103],v[84:87],a[196:199]
v_mfma_f32_16x16x32_bf16 a[196:199],a[96:99],v[80:83],a[196:199]
v_mfma_f32_16x16x32_bf16 a[200:203],a[96:99],v[96:99],a[200:203]
ds_read_b64_tr_b16 a[96:97],v43 offset:96
ds_read_b64_tr_b16 a[98:99],v43 offset:5216
v_mfma_f32_16x16x32_bf16 a[200:203],a[100:103],v[100:103],a[200:203]
ds_read_b64_tr_b16 a[100:101],v43 offset:10336
v_mfma_f32_16x16x32_bf16 a[200:203],a[104:107],v[104:107],a[200:203]
ds_read_b64_tr_b16 a[102:103],v43 offset:15456
v_mfma_f32_16x16x32_bf16 a[200:203],a[108:111],v[108:111],a[200:203]
ds_read_b64_tr_b16 a[104:105],v43 offset:20576
v_mfma_f32_16x16x32_bf16 a[192:195],a[112:115],v[132:135],a[192:195]
s_waitcnt vmcnt(3)
ds_read_b64_tr_b16 a[106:107],v43 offset:25696
ds_read_b64_tr_b16 a[108:109],v43 offset:30816
v_mfma_f32_16x16x32_bf16 a[192:195],a[116:119],v[140:143],a[192:195]
ds_read_b64_tr_b16 a[110:111],v43 offset:35936
v_mfma_f32_16x16x32_bf16 a[192:195],a[120:123],v[148:151],a[192:195]
v_mfma_f32_16x16x32_bf16 a[192:195],a[124:127],v[156:159],a[192:195]
v_mfma_f32_16x16x32_bf16 a[196:199],a[124:127],v[188:191],a[196:199]
v_mfma_f32_16x16x32_bf16 a[196:199],a[120:123],v[180:183],a[196:199]
v_mfma_f32_16x16x32_bf16 a[196:199],a[116:119],v[172:175],a[196:199]
v_mfma_f32_16x16x32_bf16 a[196:199],a[112:115],v[164:167],a[196:199]
v_mfma_f32_16x16x32_bf16 a[200:203],a[112:115],v[196:199],a[200:203]
ds_read_b64_tr_b16 a[112:113],v45 offset:96
ds_read_b64_tr_b16 a[114:115],v45 offset:5216
v_mfma_f32_16x16x32_bf16 a[200:203],a[116:119],v[204:207],a[200:203]
v_accvgpr_read_b32 v128,a192
v_accvgpr_read_b32 v129,a193
v_mfma_f32_16x16x32_bf16 a[200:203],a[120:123],v[212:215],a[200:203]
v_cvt_pk_bf16_f32 v128,v128,v129
v_accvgpr_read_b32 v130,a194
v_mfma_f32_16x16x32_bf16 a[200:203],a[124:127],v[220:223],a[200:203]
v_accvgpr_read_b32 v131,a195
v_cvt_pk_bf16_f32 v129,v130,v131
v_mfma_f32_16x16x32_bf16 a[208:211],a[128:131],v[64:67],a[208:211]
ds_read_b64_tr_b16 a[116:117],v45 offset:10336
ds_read_b64_tr_b16 a[118:119],v45 offset:15456
v_mfma_f32_16x16x32_bf16 a[208:211],a[132:135],v[68:71],a[208:211]
v_accvgpr_read_b32 v136,a196
v_accvgpr_read_b32 v137,a197
v_mfma_f32_16x16x32_bf16 a[208:211],a[136:139],v[72:75],a[208:211]
v_cvt_pk_bf16_f32 v136,v136,v137
v_accvgpr_read_b32 v138,a198
v_mfma_f32_16x16x32_bf16 a[208:211],a[140:143],v[76:79],a[208:211]
v_accvgpr_read_b32 v139,a199
v_cvt_pk_bf16_f32 v137,v138,v139
v_mfma_f32_16x16x32_bf16 a[212:215],a[140:143],v[92:95],a[212:215]
ds_read_b64_tr_b16 a[120:121],v45 offset:20576
ds_read_b64_tr_b16 a[122:123],v45 offset:25696
v_mfma_f32_16x16x32_bf16 a[212:215],a[136:139],v[88:91],a[212:215]
ds_read_b64_tr_b16 a[124:125],v45 offset:30816
v_accvgpr_read_b32 v144,a200
v_mfma_f32_16x16x32_bf16 a[212:215],a[132:135],v[84:87],a[212:215]
ds_read_b64_tr_b16 a[126:127],v45 offset:35936
v_accvgpr_read_b32 v145,a201
v_mfma_f32_16x16x32_bf16 a[212:215],a[128:131],v[80:83],a[212:215]
v_cvt_pk_bf16_f32 v144,v144,v145
v_accvgpr_read_b32 v146,a202
v_mfma_f32_16x16x32_bf16 a[216:219],a[128:131],v[96:99],a[216:219]
v_accvgpr_read_b32 v147,a203
v_cvt_pk_bf16_f32 v145,v146,v147
v_mfma_f32_16x16x32_bf16 a[216:219],a[132:135],v[100:103],a[216:219]
ds_read_b64_tr_b16 a[128:129],v42 offset:128
ds_read_b64_tr_b16 a[130:131],v42 offset:5248
v_mfma_f32_16x16x32_bf16 a[216:219],a[136:139],v[104:107],a[216:219]
ds_read_b64_tr_b16 a[132:133],v42 offset:10368
v_mfma_f32_16x16x32_bf16 a[216:219],a[140:143],v[108:111],a[216:219]
ds_read_b64_tr_b16 a[134:135],v42 offset:15488
v_mfma_f32_16x16x32_bf16 a[208:211],a[144:147],v[132:135],a[208:211]
ds_read_b64_tr_b16 a[136:137],v42 offset:20608
v_mfma_f32_16x16x32_bf16 a[208:211],a[148:151],v[140:143],a[208:211]
ds_read_b64_tr_b16 a[138:139],v42 offset:25728
ds_read_b64_tr_b16 a[140:141],v42 offset:30848
v_mfma_f32_16x16x32_bf16 a[208:211],a[152:155],v[148:151],a[208:211]
ds_read_b64_tr_b16 a[142:143],v42 offset:35968
v_mfma_f32_16x16x32_bf16 a[208:211],a[156:159],v[156:159],a[208:211]
v_mfma_f32_16x16x32_bf16 a[212:215],a[156:159],v[188:191],a[212:215]
v_mfma_f32_16x16x32_bf16 a[212:215],a[152:155],v[180:183],a[212:215]
v_mfma_f32_16x16x32_bf16 a[212:215],a[148:151],v[172:175],a[212:215]
v_mfma_f32_16x16x32_bf16 a[212:215],a[144:147],v[164:167],a[212:215]
v_mfma_f32_16x16x32_bf16 a[216:219],a[144:147],v[196:199],a[216:219]
ds_read_b64_tr_b16 a[144:145],v44 offset:128
ds_read_b64_tr_b16 a[146:147],v44 offset:5248
v_mfma_f32_16x16x32_bf16 a[216:219],a[148:151],v[204:207],a[216:219]
v_accvgpr_read_b32 v130,a208
v_accvgpr_read_b32 v131,a209
v_mfma_f32_16x16x32_bf16 a[216:219],a[152:155],v[212:215],a[216:219]
v_accvgpr_read_b32 v12,a210
v_accvgpr_read_b32 v13,a211
v_mfma_f32_16x16x32_bf16 a[216:219],a[156:159],v[220:223],a[216:219]
v_cvt_pk_bf16_f32 v130,v130,v131
v_cvt_pk_bf16_f32 v131,v12,v13
v_mfma_f32_16x16x32_bf16 a[224:227],a[160:163],v[64:67],a[224:227]
buffer_store_dwordx4 v[128:131],v6,s[32:35],s84 offen sc1
v_mfma_f32_16x16x32_bf16 a[224:227],a[164:167],v[68:71],a[224:227]
s_waitcnt lgkmcnt(2)
v_accvgpr_read_b32 v138,a212
v_accvgpr_read_b32 v139,a213
v_mfma_f32_16x16x32_bf16 a[224:227],a[168:171],v[72:75],a[224:227]
v_accvgpr_read_b32 v14,a214
v_accvgpr_read_b32 v15,a215
v_mfma_f32_16x16x32_bf16 a[224:227],a[172:175],v[76:79],a[224:227]
v_cvt_pk_bf16_f32 v138,v138,v139
v_cvt_pk_bf16_f32 v139,v14,v15
v_mfma_f32_16x16x32_bf16 a[228:231],a[172:175],v[92:95],a[228:231]
buffer_store_dwordx4 v[136:139],v6,s[32:35],s85 offen sc1
v_mfma_f32_16x16x32_bf16 a[228:231],a[168:171],v[88:91],a[228:231]
v_accvgpr_read_b32 v146,a216
v_accvgpr_read_b32 v147,a217
v_mfma_f32_16x16x32_bf16 a[228:231],a[164:167],v[84:87],a[228:231]
v_accvgpr_read_b32 v16,a218
v_accvgpr_read_b32 v17,a219
v_mfma_f32_16x16x32_bf16 a[228:231],a[160:163],v[80:83],a[228:231]
v_cvt_pk_bf16_f32 v146,v146,v147
v_cvt_pk_bf16_f32 v147,v16,v17
v_mfma_f32_16x16x32_bf16 a[232:235],a[160:163],v[96:99],a[232:235]
buffer_store_dwordx4 v[144:147],v6,s[32:35],s86 offen sc1
v_mfma_f32_16x16x32_bf16 a[232:235],a[164:167],v[100:103],a[232:235]
ds_read_b64_tr_b16 a[148:149],v44 offset:10368
ds_read_b64_tr_b16 a[150:151],v44 offset:15488
v_mfma_f32_16x16x32_bf16 a[232:235],a[168:171],v[104:107],a[232:235]
ds_read_b64_tr_b16 a[152:153],v44 offset:20608
v_mfma_f32_16x16x32_bf16 a[232:235],a[172:175],v[108:111],a[232:235]
ds_read_b64_tr_b16 a[154:155],v44 offset:25728
v_mfma_f32_16x16x32_bf16 a[224:227],a[176:179],v[132:135],a[224:227]
ds_read_b64_tr_b16 a[156:157],v44 offset:30848
v_mfma_f32_16x16x32_bf16 a[224:227],a[180:183],v[140:143],a[224:227]
ds_read_b64_tr_b16 a[158:159],v44 offset:35968
ds_read_b64_tr_b16 a[160:161],v43 offset:160
v_mfma_f32_16x16x32_bf16 a[224:227],a[184:187],v[148:151],a[224:227]
ds_read_b64_tr_b16 a[162:163],v43 offset:5280
v_mfma_f32_16x16x32_bf16 a[224:227],a[188:191],v[156:159],a[224:227]
ds_read_b64_tr_b16 a[164:165],v43 offset:10400
v_mfma_f32_16x16x32_bf16 a[228:231],a[188:191],v[188:191],a[228:231]
ds_read_b64_tr_b16 a[166:167],v43 offset:15520
v_mfma_f32_16x16x32_bf16 a[228:231],a[184:187],v[180:183],a[228:231]
ds_read_b64_tr_b16 a[168:169],v43 offset:20640
ds_read_b64_tr_b16 a[170:171],v43 offset:25760
v_mfma_f32_16x16x32_bf16 a[228:231],a[180:183],v[172:175],a[228:231]
ds_read_b64_tr_b16 a[172:173],v43 offset:30880
v_mfma_f32_16x16x32_bf16 a[228:231],a[176:179],v[164:167],a[228:231]
ds_read_b64_tr_b16 a[174:175],v43 offset:36000
v_mfma_f32_16x16x32_bf16 a[232:235],a[176:179],v[196:199],a[232:235]
ds_read_b64_tr_b16 a[176:177],v45 offset:160
v_mfma_f32_16x16x32_bf16 a[232:235],a[180:183],v[204:207],a[232:235]
v_accvgpr_read_b32 v160,a224
v_accvgpr_read_b32 v161,a225
v_mfma_f32_16x16x32_bf16 a[232:235],a[184:187],v[212:215],a[232:235]
v_cvt_pk_bf16_f32 v160,v160,v161
v_accvgpr_read_b32 v162,a226
v_mfma_f32_16x16x32_bf16 a[232:235],a[188:191],v[220:223],a[232:235]
v_accvgpr_read_b32 v163,a227
v_cvt_pk_bf16_f32 v161,v162,v163
v_mfma_f32_16x16x32_bf16 a[240:243],a[96:99],v[64:67],a[240:243]
ds_read_b64_tr_b16 a[178:179],v45 offset:5280
ds_read_b64_tr_b16 a[180:181],v45 offset:10400
v_mfma_f32_16x16x32_bf16 a[240:243],a[100:103],v[68:71],a[240:243]
v_accvgpr_read_b32 v168,a228
v_accvgpr_read_b32 v169,a229
v_mfma_f32_16x16x32_bf16 a[240:243],a[104:107],v[72:75],a[240:243]
v_cvt_pk_bf16_f32 v168,v168,v169
v_accvgpr_read_b32 v170,a230
v_mfma_f32_16x16x32_bf16 a[240:243],a[108:111],v[76:79],a[240:243]
v_accvgpr_read_b32 v171,a231
v_cvt_pk_bf16_f32 v169,v170,v171
v_mfma_f32_16x16x32_bf16 a[244:247],a[108:111],v[92:95],a[244:247]
ds_read_b64_tr_b16 a[182:183],v45 offset:15520
ds_read_b64_tr_b16 a[184:185],v45 offset:20640
v_mfma_f32_16x16x32_bf16 a[244:247],a[104:107],v[88:91],a[244:247]
ds_read_b64_tr_b16 a[186:187],v45 offset:25760
v_accvgpr_read_b32 v176,a232
v_mfma_f32_16x16x32_bf16 a[244:247],a[100:103],v[84:87],a[244:247]
ds_read_b64_tr_b16 a[188:189],v45 offset:30880
v_accvgpr_read_b32 v177,a233
v_mfma_f32_16x16x32_bf16 a[244:247],a[96:99],v[80:83],a[244:247]
ds_read_b64_tr_b16 a[190:191],v45 offset:36000
v_cvt_pk_bf16_f32 v176,v176,v177
v_mfma_f32_16x16x32_bf16 a[248:251],a[96:99],v[96:99],a[248:251]
v_accvgpr_read_b32 v178,a234
v_accvgpr_read_b32 v179,a235
v_mfma_f32_16x16x32_bf16 a[248:251],a[100:103],v[100:103],a[248:251]
ds_read_b64_tr_b16 a[96:97],v42 offset:192
v_cvt_pk_bf16_f32 v177,v178,v179
v_mfma_f32_16x16x32_bf16 a[248:251],a[104:107],v[104:107],a[248:251]
ds_read_b64_tr_b16 a[98:99],v42 offset:5312
ds_read_b64_tr_b16 a[100:101],v42 offset:10432
v_mfma_f32_16x16x32_bf16 a[248:251],a[108:111],v[108:111],a[248:251]
ds_read_b64_tr_b16 a[102:103],v42 offset:15552
v_mfma_f32_16x16x32_bf16 a[240:243],a[112:115],v[132:135],a[240:243]
ds_read_b64_tr_b16 a[104:105],v42 offset:20672
v_mfma_f32_16x16x32_bf16 a[240:243],a[116:119],v[140:143],a[240:243]
ds_read_b64_tr_b16 a[106:107],v42 offset:25792
v_mfma_f32_16x16x32_bf16 a[240:243],a[120:123],v[148:151],a[240:243]
ds_read_b64_tr_b16 a[108:109],v42 offset:30912
ds_read_b64_tr_b16 a[110:111],v42 offset:36032
v_mfma_f32_16x16x32_bf16 a[240:243],a[124:127],v[156:159],a[240:243]
v_mfma_f32_16x16x32_bf16 a[244:247],a[124:127],v[188:191],a[244:247]
v_mfma_f32_16x16x32_bf16 a[244:247],a[120:123],v[180:183],a[244:247]
v_mfma_f32_16x16x32_bf16 a[244:247],a[116:119],v[172:175],a[244:247]
v_mfma_f32_16x16x32_bf16 a[244:247],a[112:115],v[164:167],a[244:247]
v_mfma_f32_16x16x32_bf16 a[248:251],a[112:115],v[196:199],a[248:251]
ds_read_b64_tr_b16 a[112:113],v44 offset:192
ds_read_b64_tr_b16 a[114:115],v44 offset:5312
v_mfma_f32_16x16x32_bf16 a[248:251],a[116:119],v[204:207],a[248:251]
v_accvgpr_read_b32 v162,a240
v_accvgpr_read_b32 v163,a241
v_mfma_f32_16x16x32_bf16 a[248:251],a[120:123],v[212:215],a[248:251]
v_accvgpr_read_b32 v12,a242
v_accvgpr_read_b32 v13,a243
v_mfma_f32_16x16x32_bf16 a[248:251],a[124:127],v[220:223],a[248:251]
v_cvt_pk_bf16_f32 v162,v162,v163
v_cvt_pk_bf16_f32 v163,v12,v13
v_mfma_f32_16x16x32_bf16 a[0:3],a[128:131],v[64:67],a[0:3]
buffer_store_dwordx4 v[160:163],v6,s[32:35],s84 offen offset:1024 sc1
v_mfma_f32_16x16x32_bf16 a[0:3],a[132:135],v[68:71],a[0:3]
)A"
R"A(v_accvgpr_read_b32 v170,a244
v_accvgpr_read_b32 v171,a245
v_mfma_f32_16x16x32_bf16 a[0:3],a[136:139],v[72:75],a[0:3]
v_accvgpr_read_b32 v14,a246
v_accvgpr_read_b32 v15,a247
v_mfma_f32_16x16x32_bf16 a[0:3],a[140:143],v[76:79],a[0:3]
v_cvt_pk_bf16_f32 v170,v170,v171
v_cvt_pk_bf16_f32 v171,v14,v15
v_mfma_f32_16x16x32_bf16 a[4:7],a[140:143],v[92:95],a[4:7]
buffer_store_dwordx4 v[168:171],v6,s[32:35],s85 offen offset:1024 sc1
v_mfma_f32_16x16x32_bf16 a[4:7],a[136:139],v[88:91],a[4:7]
v_accvgpr_read_b32 v178,a248
v_accvgpr_read_b32 v179,a249
v_mfma_f32_16x16x32_bf16 a[4:7],a[132:135],v[84:87],a[4:7]
v_accvgpr_read_b32 v16,a250
v_accvgpr_read_b32 v17,a251
v_mfma_f32_16x16x32_bf16 a[4:7],a[128:131],v[80:83],a[4:7]
v_cvt_pk_bf16_f32 v178,v178,v179
v_cvt_pk_bf16_f32 v179,v16,v17
v_mfma_f32_16x16x32_bf16 a[8:11],a[128:131],v[96:99],a[8:11]
buffer_store_dwordx4 v[176:179],v6,s[32:35],s86 offen offset:1024 sc1
v_mfma_f32_16x16x32_bf16 a[8:11],a[132:135],v[100:103],a[8:11]
ds_read_b64_tr_b16 a[116:117],v44 offset:10432
ds_read_b64_tr_b16 a[118:119],v44 offset:15552
v_mfma_f32_16x16x32_bf16 a[8:11],a[136:139],v[104:107],a[8:11]
ds_read_b64_tr_b16 a[120:121],v44 offset:20672
v_mfma_f32_16x16x32_bf16 a[8:11],a[140:143],v[108:111],a[8:11]
s_waitcnt lgkmcnt(3)
ds_read_b64_tr_b16 a[122:123],v44 offset:25792
v_mfma_f32_16x16x32_bf16 a[0:3],a[144:147],v[132:135],a[0:3]
ds_read_b64_tr_b16 a[124:125],v44 offset:30912
v_mfma_f32_16x16x32_bf16 a[0:3],a[148:151],v[140:143],a[0:3]
ds_read_b64_tr_b16 a[126:127],v44 offset:36032
ds_read_b64_tr_b16 a[128:129],v43 offset:224
v_mfma_f32_16x16x32_bf16 a[0:3],a[152:155],v[148:151],a[0:3]
ds_read_b64_tr_b16 a[130:131],v43 offset:5344
v_mfma_f32_16x16x32_bf16 a[0:3],a[156:159],v[156:159],a[0:3]
ds_read_b64_tr_b16 a[132:133],v43 offset:10464
v_mfma_f32_16x16x32_bf16 a[4:7],a[156:159],v[188:191],a[4:7]
ds_read_b64_tr_b16 a[134:135],v43 offset:15584
v_mfma_f32_16x16x32_bf16 a[4:7],a[152:155],v[180:183],a[4:7]
ds_read_b64_tr_b16 a[136:137],v43 offset:20704
ds_read_b64_tr_b16 a[138:139],v43 offset:25824
v_mfma_f32_16x16x32_bf16 a[4:7],a[148:151],v[172:175],a[4:7]
ds_read_b64_tr_b16 a[140:141],v43 offset:30944
v_mfma_f32_16x16x32_bf16 a[4:7],a[144:147],v[164:167],a[4:7]
ds_read_b64_tr_b16 a[142:143],v43 offset:36064
v_mfma_f32_16x16x32_bf16 a[8:11],a[144:147],v[196:199],a[8:11]
ds_read_b64_tr_b16 a[144:145],v45 offset:224
v_mfma_f32_16x16x32_bf16 a[8:11],a[148:151],v[204:207],a[8:11]
v_accvgpr_read_b32 v192,a0
v_accvgpr_read_b32 v193,a1
v_mfma_f32_16x16x32_bf16 a[8:11],a[152:155],v[212:215],a[8:11]
v_cvt_pk_bf16_f32 v192,v192,v193
v_accvgpr_read_b32 v194,a2
v_mfma_f32_16x16x32_bf16 a[8:11],a[156:159],v[220:223],a[8:11]
v_accvgpr_read_b32 v195,a3
v_cvt_pk_bf16_f32 v193,v194,v195
v_mfma_f32_16x16x32_bf16 a[16:19],a[160:163],v[64:67],a[16:19]
ds_read_b64_tr_b16 a[146:147],v45 offset:5344
ds_read_b64_tr_b16 a[148:149],v45 offset:10464
v_mfma_f32_16x16x32_bf16 a[16:19],a[164:167],v[68:71],a[16:19]
v_accvgpr_read_b32 v200,a4
v_accvgpr_read_b32 v201,a5
v_mfma_f32_16x16x32_bf16 a[16:19],a[168:171],v[72:75],a[16:19]
v_cvt_pk_bf16_f32 v200,v200,v201
v_accvgpr_read_b32 v202,a6
v_mfma_f32_16x16x32_bf16 a[16:19],a[172:175],v[76:79],a[16:19]
v_accvgpr_read_b32 v203,a7
v_cvt_pk_bf16_f32 v201,v202,v203
v_mfma_f32_16x16x32_bf16 a[20:23],a[172:175],v[92:95],a[20:23]
ds_read_b64_tr_b16 a[150:151],v45 offset:15584
ds_read_b64_tr_b16 a[152:153],v45 offset:20704
v_mfma_f32_16x16x32_bf16 a[20:23],a[168:171],v[88:91],a[20:23]
ds_read_b64_tr_b16 a[154:155],v45 offset:25824
v_accvgpr_read_b32 v208,a8
v_mfma_f32_16x16x32_bf16 a[20:23],a[164:167],v[84:87],a[20:23]
ds_read_b64_tr_b16 a[156:157],v45 offset:30944
v_accvgpr_read_b32 v209,a9
v_mfma_f32_16x16x32_bf16 a[20:23],a[160:163],v[80:83],a[20:23]
ds_read_b64_tr_b16 a[158:159],v45 offset:36064
v_cvt_pk_bf16_f32 v208,v208,v209
v_mfma_f32_16x16x32_bf16 a[24:27],a[160:163],v[96:99],a[24:27]
v_accvgpr_read_b32 v210,a10
v_accvgpr_read_b32 v211,a11
v_mfma_f32_16x16x32_bf16 a[24:27],a[164:167],v[100:103],a[24:27]
ds_read_b64_tr_b16 a[160:161],v42 offset:256
v_cvt_pk_bf16_f32 v209,v210,v211
v_mfma_f32_16x16x32_bf16 a[24:27],a[168:171],v[104:107],a[24:27]
ds_read_b64_tr_b16 a[162:163],v42 offset:5376
ds_read_b64_tr_b16 a[164:165],v42 offset:10496
v_mfma_f32_16x16x32_bf16 a[24:27],a[172:175],v[108:111],a[24:27]
ds_read_b64_tr_b16 a[166:167],v42 offset:15616
v_mfma_f32_16x16x32_bf16 a[16:19],a[176:179],v[132:135],a[16:19]
ds_read_b64_tr_b16 a[168:169],v42 offset:20736
v_mfma_f32_16x16x32_bf16 a[16:19],a[180:183],v[140:143],a[16:19]
ds_read_b64_tr_b16 a[170:171],v42 offset:25856
v_mfma_f32_16x16x32_bf16 a[16:19],a[184:187],v[148:151],a[16:19]
ds_read_b64_tr_b16 a[172:173],v42 offset:30976
ds_read_b64_tr_b16 a[174:175],v42 offset:36096
v_mfma_f32_16x16x32_bf16 a[16:19],a[188:191],v[156:159],a[16:19]
v_mfma_f32_16x16x32_bf16 a[20:23],a[188:191],v[188:191],a[20:23]
v_mfma_f32_16x16x32_bf16 a[20:23],a[184:187],v[180:183],a[20:23]
v_mfma_f32_16x16x32_bf16 a[20:23],a[180:183],v[172:175],a[20:23]
v_mfma_f32_16x16x32_bf16 a[20:23],a[176:179],v[164:167],a[20:23]
v_mfma_f32_16x16x32_bf16 a[24:27],a[176:179],v[196:199],a[24:27]
ds_read_b64_tr_b16 a[176:177],v44 offset:256
ds_read_b64_tr_b16 a[178:179],v44 offset:5376
v_mfma_f32_16x16x32_bf16 a[24:27],a[180:183],v[204:207],a[24:27]
v_accvgpr_read_b32 v194,a16
v_accvgpr_read_b32 v195,a17
v_mfma_f32_16x16x32_bf16 a[24:27],a[184:187],v[212:215],a[24:27]
v_accvgpr_read_b32 v12,a18
v_accvgpr_read_b32 v13,a19
v_mfma_f32_16x16x32_bf16 a[24:27],a[188:191],v[220:223],a[24:27]
v_cvt_pk_bf16_f32 v194,v194,v195
v_cvt_pk_bf16_f32 v195,v12,v13
v_mfma_f32_16x16x32_bf16 a[32:35],a[96:99],v[64:67],a[32:35]
buffer_store_dwordx4 v[192:195],v6,s[32:35],s84 offen offset:2048 sc1
v_mfma_f32_16x16x32_bf16 a[32:35],a[100:103],v[68:71],a[32:35]
v_accvgpr_read_b32 v202,a20
v_accvgpr_read_b32 v203,a21
v_mfma_f32_16x16x32_bf16 a[32:35],a[104:107],v[72:75],a[32:35]
v_accvgpr_read_b32 v14,a22
v_accvgpr_read_b32 v15,a23
v_mfma_f32_16x16x32_bf16 a[32:35],a[108:111],v[76:79],a[32:35]
v_cvt_pk_bf16_f32 v202,v202,v203
v_cvt_pk_bf16_f32 v203,v14,v15
v_mfma_f32_16x16x32_bf16 a[36:39],a[108:111],v[92:95],a[36:39]
buffer_store_dwordx4 v[200:203],v6,s[32:35],s85 offen offset:2048 sc1
v_mfma_f32_16x16x32_bf16 a[36:39],a[104:107],v[88:91],a[36:39]
v_accvgpr_read_b32 v210,a24
v_accvgpr_read_b32 v211,a25
v_mfma_f32_16x16x32_bf16 a[36:39],a[100:103],v[84:87],a[36:39]
v_accvgpr_read_b32 v16,a26
v_accvgpr_read_b32 v17,a27
v_mfma_f32_16x16x32_bf16 a[36:39],a[96:99],v[80:83],a[36:39]
v_cvt_pk_bf16_f32 v210,v210,v211
v_cvt_pk_bf16_f32 v211,v16,v17
v_mfma_f32_16x16x32_bf16 a[40:43],a[96:99],v[96:99],a[40:43]
buffer_store_dwordx4 v[208:211],v6,s[32:35],s86 offen offset:2048 sc1
v_mfma_f32_16x16x32_bf16 a[40:43],a[100:103],v[100:103],a[40:43]
ds_read_b64_tr_b16 a[180:181],v44 offset:10496
ds_read_b64_tr_b16 a[182:183],v44 offset:15616
v_mfma_f32_16x16x32_bf16 a[40:43],a[104:107],v[104:107],a[40:43]
ds_read_b64_tr_b16 a[184:185],v44 offset:20736
v_mfma_f32_16x16x32_bf16 a[40:43],a[108:111],v[108:111],a[40:43]
ds_read_b64_tr_b16 a[186:187],v44 offset:25856
v_mfma_f32_16x16x32_bf16 a[32:35],a[112:115],v[132:135],a[32:35]
s_waitcnt lgkmcnt(4)
ds_read_b64_tr_b16 a[188:189],v44 offset:30976
v_mfma_f32_16x16x32_bf16 a[32:35],a[116:119],v[140:143],a[32:35]
ds_read_b64_tr_b16 a[190:191],v44 offset:36096
ds_read_b64_tr_b16 a[96:97],v43 offset:288
v_mfma_f32_16x16x32_bf16 a[32:35],a[120:123],v[148:151],a[32:35]
ds_read_b64_tr_b16 a[98:99],v43 offset:5408
v_mfma_f32_16x16x32_bf16 a[32:35],a[124:127],v[156:159],a[32:35]
ds_read_b64_tr_b16 a[100:101],v43 offset:10528
v_mfma_f32_16x16x32_bf16 a[36:39],a[124:127],v[188:191],a[36:39]
ds_read_b64_tr_b16 a[102:103],v43 offset:15648
v_mfma_f32_16x16x32_bf16 a[36:39],a[120:123],v[180:183],a[36:39]
ds_read_b64_tr_b16 a[104:105],v43 offset:20768
ds_read_b64_tr_b16 a[106:107],v43 offset:25888
v_mfma_f32_16x16x32_bf16 a[36:39],a[116:119],v[172:175],a[36:39]
ds_read_b64_tr_b16 a[108:109],v43 offset:31008
v_mfma_f32_16x16x32_bf16 a[36:39],a[112:115],v[164:167],a[36:39]
ds_read_b64_tr_b16 a[110:111],v43 offset:36128
v_mfma_f32_16x16x32_bf16 a[40:43],a[112:115],v[196:199],a[40:43]
ds_read_b64_tr_b16 a[112:113],v45 offset:288
v_mfma_f32_16x16x32_bf16 a[40:43],a[116:119],v[204:207],a[40:43]
v_accvgpr_read_b32 v224,a32
v_accvgpr_read_b32 v225,a33
v_mfma_f32_16x16x32_bf16 a[40:43],a[120:123],v[212:215],a[40:43]
v_cvt_pk_bf16_f32 v224,v224,v225
v_accvgpr_read_b32 v226,a34
v_mfma_f32_16x16x32_bf16 a[40:43],a[124:127],v[220:223],a[40:43]
v_accvgpr_read_b32 v227,a35
v_cvt_pk_bf16_f32 v225,v226,v227
v_mfma_f32_16x16x32_bf16 a[48:51],a[128:131],v[64:67],a[48:51]
ds_read_b64_tr_b16 a[114:115],v45 offset:5408
ds_read_b64_tr_b16 a[116:117],v45 offset:10528
v_mfma_f32_16x16x32_bf16 a[48:51],a[132:135],v[68:71],a[48:51]
v_accvgpr_read_b32 v232,a36
v_accvgpr_read_b32 v233,a37
v_mfma_f32_16x16x32_bf16 a[48:51],a[136:139],v[72:75],a[48:51]
v_cvt_pk_bf16_f32 v232,v232,v233
v_accvgpr_read_b32 v234,a38
v_mfma_f32_16x16x32_bf16 a[48:51],a[140:143],v[76:79],a[48:51]
v_accvgpr_read_b32 v235,a39
v_cvt_pk_bf16_f32 v233,v234,v235
v_mfma_f32_16x16x32_bf16 a[52:55],a[140:143],v[92:95],a[52:55]
ds_read_b64_tr_b16 a[118:119],v45 offset:15648
ds_read_b64_tr_b16 a[120:121],v45 offset:20768
v_mfma_f32_16x16x32_bf16 a[52:55],a[136:139],v[88:91],a[52:55]
v_accvgpr_read_b32 v240,a40
v_accvgpr_read_b32 v241,a41
v_mfma_f32_16x16x32_bf16 a[52:55],a[132:135],v[84:87],a[52:55]
v_cvt_pk_bf16_f32 v240,v240,v241
v_accvgpr_read_b32 v242,a42
v_mfma_f32_16x16x32_bf16 a[52:55],a[128:131],v[80:83],a[52:55]
v_accvgpr_read_b32 v243,a43
v_cvt_pk_bf16_f32 v241,v242,v243
v_mfma_f32_16x16x32_bf16 a[56:59],a[128:131],v[96:99],a[56:59]
ds_read_b64_tr_b16 a[122:123],v45 offset:25888
ds_read_b64_tr_b16 a[124:125],v45 offset:31008
v_mfma_f32_16x16x32_bf16 a[56:59],a[132:135],v[100:103],a[56:59]
ds_read_b64_tr_b16 a[126:127],v45 offset:36128
v_mfma_f32_16x16x32_bf16 a[56:59],a[136:139],v[104:107],a[56:59]
ds_read_b64_tr_b16 a[128:129],v1
v_mfma_f32_16x16x32_bf16 a[56:59],a[140:143],v[108:111],a[56:59]
ds_read_b64_tr_b16 a[130:131],v1 offset:5120
v_mfma_f32_16x16x32_bf16 a[48:51],a[144:147],v[132:135],a[48:51]
ds_read_b64_tr_b16 a[132:133],v1 offset:10240
ds_read_b64_tr_b16 a[134:135],v1 offset:15360
v_mfma_f32_16x16x32_bf16 a[48:51],a[148:151],v[140:143],a[48:51]
ds_read_b64_tr_b16 a[136:137],v1 offset:20480
v_mfma_f32_16x16x32_bf16 a[48:51],a[152:155],v[148:151],a[48:51]
ds_read_b64_tr_b16 a[138:139],v1 offset:25600
v_mfma_f32_16x16x32_bf16 a[48:51],a[156:159],v[156:159],a[48:51]
ds_read_b64_tr_b16 a[140:141],v1 offset:30720
v_mfma_f32_16x16x32_bf16 a[52:55],a[156:159],v[188:191],a[52:55]
ds_read_b64_tr_b16 a[142:143],v1 offset:35840
v_mfma_f32_16x16x32_bf16 a[52:55],a[152:155],v[180:183],a[52:55]
v_mfma_f32_16x16x32_bf16 a[52:55],a[148:151],v[172:175],a[52:55]
v_mfma_f32_16x16x32_bf16 a[52:55],a[144:147],v[164:167],a[52:55]
v_mfma_f32_16x16x32_bf16 a[56:59],a[144:147],v[196:199],a[56:59]
ds_read_b64_tr_b16 a[144:145],v42
ds_read_b64_tr_b16 a[146:147],v42 offset:5120
v_mfma_f32_16x16x32_bf16 a[56:59],a[148:151],v[204:207],a[56:59]
v_accvgpr_read_b32 v226,a48
v_accvgpr_read_b32 v227,a49
v_mfma_f32_16x16x32_bf16 a[56:59],a[152:155],v[212:215],a[56:59]
v_accvgpr_read_b32 v12,a50
v_accvgpr_read_b32 v13,a51
v_mfma_f32_16x16x32_bf16 a[56:59],a[156:159],v[220:223],a[56:59]
v_cvt_pk_bf16_f32 v226,v226,v227
v_cvt_pk_bf16_f32 v227,v12,v13
v_mfma_f32_16x16x32_bf16 a[64:67],a[160:163],v[64:67],a[64:67]
buffer_store_dwordx4 v[224:227],v6,s[32:35],s84 offen offset:3072 sc1
v_mfma_f32_16x16x32_bf16 a[64:67],a[164:167],v[68:71],a[64:67]
v_accvgpr_read_b32 v234,a52
v_accvgpr_read_b32 v235,a53
v_mfma_f32_16x16x32_bf16 a[64:67],a[168:171],v[72:75],a[64:67]
v_accvgpr_read_b32 v14,a54
v_accvgpr_read_b32 v15,a55
v_mfma_f32_16x16x32_bf16 a[64:67],a[172:175],v[76:79],a[64:67]
v_cvt_pk_bf16_f32 v234,v234,v235
v_cvt_pk_bf16_f32 v235,v14,v15
v_mfma_f32_16x16x32_bf16 a[68:71],a[172:175],v[92:95],a[68:71]
buffer_store_dwordx4 v[232:235],v6,s[32:35],s85 offen offset:3072 sc1
)A"
R"A(v_mfma_f32_16x16x32_bf16 a[68:71],a[168:171],v[88:91],a[68:71]
v_accvgpr_read_b32 v242,a56
v_accvgpr_read_b32 v243,a57
v_mfma_f32_16x16x32_bf16 a[68:71],a[164:167],v[84:87],a[68:71]
v_accvgpr_read_b32 v16,a58
v_accvgpr_read_b32 v17,a59
v_mfma_f32_16x16x32_bf16 a[68:71],a[160:163],v[80:83],a[68:71]
v_cvt_pk_bf16_f32 v242,v242,v243
v_cvt_pk_bf16_f32 v243,v16,v17
v_mfma_f32_16x16x32_bf16 a[72:75],a[160:163],v[96:99],a[72:75]
buffer_store_dwordx4 v[240:243],v6,s[32:35],s86 offen offset:3072 sc1
v_mfma_f32_16x16x32_bf16 a[72:75],a[164:167],v[100:103],a[72:75]
ds_read_b64_tr_b16 a[148:149],v42 offset:10240
ds_read_b64_tr_b16 a[150:151],v42 offset:15360
v_mfma_f32_16x16x32_bf16 a[72:75],a[168:171],v[104:107],a[72:75]
ds_read_b64_tr_b16 a[152:153],v42 offset:20480
v_mfma_f32_16x16x32_bf16 a[72:75],a[172:175],v[108:111],a[72:75]
ds_read_b64_tr_b16 a[154:155],v42 offset:25600
v_mfma_f32_16x16x32_bf16 a[64:67],a[176:179],v[132:135],a[64:67]
s_waitcnt lgkmcnt(4)
ds_read_b64_tr_b16 a[156:157],v42 offset:30720
v_mfma_f32_16x16x32_bf16 a[64:67],a[180:183],v[140:143],a[64:67]
ds_read_b64_tr_b16 a[158:159],v42 offset:35840
ds_read_b64_tr_b16 a[160:161],v44
v_mfma_f32_16x16x32_bf16 a[64:67],a[184:187],v[148:151],a[64:67]
ds_read_b64_tr_b16 a[162:163],v44 offset:5120
v_mfma_f32_16x16x32_bf16 a[64:67],a[188:191],v[156:159],a[64:67]
ds_read_b64_tr_b16 a[164:165],v44 offset:10240
v_mfma_f32_16x16x32_bf16 a[68:71],a[188:191],v[188:191],a[68:71]
ds_read_b64_tr_b16 a[166:167],v44 offset:15360
v_mfma_f32_16x16x32_bf16 a[68:71],a[184:187],v[180:183],a[68:71]
ds_read_b64_tr_b16 a[168:169],v44 offset:20480
ds_read_b64_tr_b16 a[170:171],v44 offset:25600
v_mfma_f32_16x16x32_bf16 a[68:71],a[180:183],v[172:175],a[68:71]
ds_read_b64_tr_b16 a[172:173],v44 offset:30720
v_mfma_f32_16x16x32_bf16 a[68:71],a[176:179],v[164:167],a[68:71]
ds_read_b64_tr_b16 a[174:175],v44 offset:35840
v_mfma_f32_16x16x32_bf16 a[72:75],a[176:179],v[196:199],a[72:75]
ds_read_b64_tr_b16 a[176:177],v2 offset:32
v_mfma_f32_16x16x32_bf16 a[72:75],a[180:183],v[204:207],a[72:75]
v_accvgpr_read_b32 v128,a64
v_accvgpr_read_b32 v129,a65
v_mfma_f32_16x16x32_bf16 a[72:75],a[184:187],v[212:215],a[72:75]
v_cvt_pk_bf16_f32 v128,v128,v129
v_accvgpr_read_b32 v130,a66
v_mfma_f32_16x16x32_bf16 a[72:75],a[188:191],v[220:223],a[72:75]
v_accvgpr_read_b32 v131,a67
v_cvt_pk_bf16_f32 v129,v130,v131
v_mfma_f32_16x16x32_bf16 a[80:83],a[96:99],v[64:67],a[80:83]
ds_read_b64_tr_b16 a[178:179],v2 offset:5152
ds_read_b64_tr_b16 a[180:181],v2 offset:10272
v_mfma_f32_16x16x32_bf16 a[80:83],a[100:103],v[68:71],a[80:83]
v_accvgpr_read_b32 v136,a68
v_accvgpr_read_b32 v137,a69
v_mfma_f32_16x16x32_bf16 a[80:83],a[104:107],v[72:75],a[80:83]
v_cvt_pk_bf16_f32 v136,v136,v137
v_accvgpr_read_b32 v138,a70
v_mfma_f32_16x16x32_bf16 a[80:83],a[108:111],v[76:79],a[80:83]
ds_read_b64_tr_b16 a[182:183],v2 offset:15392
v_accvgpr_read_b32 v139,a71
v_mfma_f32_16x16x32_bf16 a[84:87],a[108:111],v[92:95],a[84:87]
ds_read_b64_tr_b16 a[184:185],v2 offset:20512
v_cvt_pk_bf16_f32 v137,v138,v139
v_mfma_f32_16x16x32_bf16 a[84:87],a[104:107],v[88:91],a[84:87]
v_accvgpr_read_b32 v144,a72
v_accvgpr_read_b32 v145,a73
v_mfma_f32_16x16x32_bf16 a[84:87],a[100:103],v[84:87],a[84:87]
v_cvt_pk_bf16_f32 v144,v144,v145
v_accvgpr_read_b32 v146,a74
v_mfma_f32_16x16x32_bf16 a[84:87],a[96:99],v[80:83],a[84:87]
v_accvgpr_read_b32 v147,a75
v_cvt_pk_bf16_f32 v145,v146,v147
v_mfma_f32_16x16x32_bf16 a[88:91],a[96:99],v[96:99],a[88:91]
ds_read_b64_tr_b16 a[186:187],v2 offset:25632
ds_read_b64_tr_b16 a[188:189],v2 offset:30752
v_mfma_f32_16x16x32_bf16 a[88:91],a[100:103],v[100:103],a[88:91]
ds_read_b64_tr_b16 a[190:191],v2 offset:35872
v_mfma_f32_16x16x32_bf16 a[88:91],a[104:107],v[104:107],a[88:91]
ds_read_b64_tr_b16 a[96:97],v43 offset:32
v_mfma_f32_16x16x32_bf16 a[88:91],a[108:111],v[108:111],a[88:91]
ds_read_b64_tr_b16 a[98:99],v43 offset:5152
v_mfma_f32_16x16x32_bf16 a[80:83],a[112:115],v[132:135],a[80:83]
ds_read_b64_tr_b16 a[100:101],v43 offset:10272
ds_read_b64_tr_b16 a[102:103],v43 offset:15392
v_mfma_f32_16x16x32_bf16 a[80:83],a[116:119],v[140:143],a[80:83]
ds_read_b64_tr_b16 a[104:105],v43 offset:20512
v_mfma_f32_16x16x32_bf16 a[80:83],a[120:123],v[148:151],a[80:83]
ds_read_b64_tr_b16 a[106:107],v43 offset:25632
v_mfma_f32_16x16x32_bf16 a[80:83],a[124:127],v[156:159],a[80:83]
ds_read_b64_tr_b16 a[108:109],v43 offset:30752
v_mfma_f32_16x16x32_bf16 a[84:87],a[124:127],v[188:191],a[84:87]
ds_read_b64_tr_b16 a[110:111],v43 offset:35872
v_mfma_f32_16x16x32_bf16 a[84:87],a[120:123],v[180:183],a[84:87]
v_mfma_f32_16x16x32_bf16 a[84:87],a[116:119],v[172:175],a[84:87]
v_mfma_f32_16x16x32_bf16 a[84:87],a[112:115],v[164:167],a[84:87]
v_mfma_f32_16x16x32_bf16 a[88:91],a[112:115],v[196:199],a[88:91]
ds_read_b64_tr_b16 a[112:113],v45 offset:32
ds_read_b64_tr_b16 a[114:115],v45 offset:5152
v_mfma_f32_16x16x32_bf16 a[88:91],a[116:119],v[204:207],a[88:91]
v_accvgpr_read_b32 v130,a80
v_accvgpr_read_b32 v131,a81
v_mfma_f32_16x16x32_bf16 a[88:91],a[120:123],v[212:215],a[88:91]
v_accvgpr_read_b32 v12,a82
v_accvgpr_read_b32 v13,a83
v_mfma_f32_16x16x32_bf16 a[88:91],a[124:127],v[220:223],a[88:91]
v_cvt_pk_bf16_f32 v130,v130,v131
v_cvt_pk_bf16_f32 v131,v12,v13
v_mfma_f32_16x16x32_bf16 a[204:207],a[128:131],v[152:155],a[204:207]
s_lshl_b32 s94,s71,2
buffer_store_dwordx4 v[128:131],v25,s[32:35],s84 offen sc1
v_mfma_f32_16x16x32_bf16 a[204:207],a[132:135],v[184:187],a[204:207]
s_add_u32 s94,s94,s40
v_accvgpr_read_b32 v138,a84
v_accvgpr_read_b32 v139,a85
v_mfma_f32_16x16x32_bf16 a[204:207],a[136:139],v[216:219],a[204:207]
s_lshl_b32 s95,s42,2
s_add_u32 s95,s95,s40
s_lshl_b32 s95,s95,4
v_mfma_f32_16x16x32_bf16 a[204:207],a[140:143],v[248:251],a[204:207]
s_lshl_b32 s94,s94,4
v_mov_b32 v18,s68
v_mov_b32 v19,s69
v_mfma_f32_16x16x32_bf16 a[204:207],a[144:147],v[112:115],a[204:207]
s_waitcnt vmcnt(1)
s_mov_b64 exec,1
buffer_store_dwordx2 v[18:19],v6,s[64:67],s95 offen offset:2048 sc1
s_mov_b64 exec,-1
s_waitcnt lgkmcnt(0)
v_mfma_f32_16x16x32_bf16 a[204:207],a[148:151],v[116:119],a[204:207]
v_accvgpr_read_b32 v146,a88
v_accvgpr_read_b32 v147,a89
v_mfma_f32_16x16x32_bf16 a[204:207],a[152:155],v[120:123],a[204:207]
v_accvgpr_read_b32 v14,a86
v_accvgpr_read_b32 v15,a87
v_cvt_pk_bf16_f32 v138,v138,v139
v_accvgpr_read_b32 v16,a90
v_accvgpr_read_b32 v17,a91
v_cvt_pk_bf16_f32 v146,v146,v147
v_cvt_pk_bf16_f32 v139,v14,v15
v_cvt_pk_bf16_f32 v147,v16,v17
v_mfma_f32_16x16x32_bf16 a[204:207],a[156:159],v[124:127],a[204:207]
ds_read_b64_tr_b16 a[116:117],v45 offset:10272
ds_read_b64_tr_b16 a[118:119],v45 offset:15392
v_mfma_f32_16x16x32_bf16 a[204:207],a[160:163],v[228:231],a[204:207]
buffer_store_dwordx4 v[136:139],v25,s[32:35],s85 offen sc1
v_mfma_f32_16x16x32_bf16 a[204:207],a[164:167],v[236:239],a[204:207]
ds_read_b64_tr_b16 a[120:121],v45 offset:20512
ds_read_b64_tr_b16 a[122:123],v45 offset:25632
v_mfma_f32_16x16x32_bf16 a[204:207],a[168:171],v[244:247],a[204:207]
ds_read_b64_tr_b16 a[124:125],v45 offset:30752
ds_read_b64_tr_b16 a[126:127],v45 offset:35872
v_mfma_f32_16x16x32_bf16 a[204:207],a[172:175],v[252:255],a[204:207]
ds_read_b64_tr_b16 a[128:129],v1 offset:64
ds_read_b64_tr_b16 a[130:131],v1 offset:5184
v_mfma_f32_16x16x32_bf16 a[220:223],a[176:179],v[152:155],a[220:223]
buffer_store_dwordx4 v[144:147],v25,s[32:35],s86 offen sc1
v_mfma_f32_16x16x32_bf16 a[220:223],a[180:183],v[184:187],a[220:223]
ds_read_b64_tr_b16 a[132:133],v1 offset:10304
ds_read_b64_tr_b16 a[134:135],v1 offset:15424
v_mfma_f32_16x16x32_bf16 a[220:223],a[184:187],v[216:219],a[220:223]
ds_read_b64_tr_b16 a[136:137],v1 offset:20544
ds_read_b64_tr_b16 a[138:139],v1 offset:25664
v_mfma_f32_16x16x32_bf16 a[220:223],a[188:191],v[248:251],a[220:223]
ds_read_b64_tr_b16 a[140:141],v1 offset:30784
ds_read_b64_tr_b16 a[142:143],v1 offset:35904
v_mfma_f32_16x16x32_bf16 a[220:223],a[96:99],v[112:115],a[220:223]
ds_read_b64_tr_b16 a[144:145],v42 offset:64
ds_read_b64_tr_b16 a[146:147],v42 offset:5184
v_mfma_f32_16x16x32_bf16 a[220:223],a[100:103],v[116:119],a[220:223]
ds_read_b64_tr_b16 a[148:149],v42 offset:10304
ds_read_b64_tr_b16 a[150:151],v42 offset:15424
v_mfma_f32_16x16x32_bf16 a[220:223],a[104:107],v[120:123],a[220:223]
ds_read_b64_tr_b16 a[152:153],v42 offset:20544
ds_read_b64_tr_b16 a[154:155],v42 offset:25664
v_mfma_f32_16x16x32_bf16 a[220:223],a[108:111],v[124:127],a[220:223]
ds_read_b64_tr_b16 a[156:157],v42 offset:30784
ds_read_b64_tr_b16 a[158:159],v42 offset:35904
v_mfma_f32_16x16x32_bf16 a[220:223],a[112:115],v[228:231],a[220:223]
s_waitcnt lgkmcnt(14)
ds_read_b64_tr_b16 a[160:161],v44 offset:64
ds_read_b64_tr_b16 a[162:163],v44 offset:5184
v_mfma_f32_16x16x32_bf16 a[220:223],a[116:119],v[236:239],a[220:223]
ds_read_b64_tr_b16 a[164:165],v44 offset:10304
ds_read_b64_tr_b16 a[166:167],v44 offset:15424
v_mfma_f32_16x16x32_bf16 a[220:223],a[120:123],v[244:247],a[220:223]
ds_read_b64_tr_b16 a[168:169],v44 offset:20544
ds_read_b64_tr_b16 a[170:171],v44 offset:25664
v_mfma_f32_16x16x32_bf16 a[220:223],a[124:127],v[252:255],a[220:223]
ds_read_b64_tr_b16 a[172:173],v44 offset:30784
ds_read_b64_tr_b16 a[174:175],v44 offset:35904
v_mfma_f32_16x16x32_bf16 a[236:239],a[128:131],v[152:155],a[236:239]
s_waitcnt lgkmcnt(14)
ds_read_b64_tr_b16 a[176:177],v2 offset:96
ds_read_b64_tr_b16 a[178:179],v2 offset:5216
v_mfma_f32_16x16x32_bf16 a[236:239],a[132:135],v[184:187],a[236:239]
v_bfe_u32 v24,v6,4,2
v_min_u32 v24,2,v24
v_mfma_f32_16x16x32_bf16 a[236:239],a[136:139],v[216:219],a[236:239]
ds_read_b64_tr_b16 a[180:181],v2 offset:10336
ds_read_b64_tr_b16 a[182:183],v2 offset:15456
v_mfma_f32_16x16x32_bf16 a[236:239],a[140:143],v[248:251],a[236:239]
v_add_u32 v24,s70,v24
v_add_u32 v24,1,v24
v_mfma_f32_16x16x32_bf16 a[236:239],a[144:147],v[112:115],a[236:239]
s_waitcnt lgkmcnt(10)
ds_read_b64_tr_b16 a[184:185],v2 offset:20576
ds_read_b64_tr_b16 a[186:187],v2 offset:25696
v_mfma_f32_16x16x32_bf16 a[236:239],a[148:151],v[116:119],a[236:239]
v_and_b32 v24,3,v24
v_lshlrev_b32 v24,8,v24
v_mfma_f32_16x16x32_bf16 a[236:239],a[152:155],v[120:123],a[236:239]
ds_read_b64_tr_b16 a[188:189],v2 offset:30816
ds_read_b64_tr_b16 a[190:191],v2 offset:35936
v_mfma_f32_16x16x32_bf16 a[236:239],a[156:159],v[124:127],a[236:239]
ds_read_b64_tr_b16 a[96:97],v43 offset:96
ds_read_b64_tr_b16 a[98:99],v43 offset:5216
v_mfma_f32_16x16x32_bf16 a[236:239],a[160:163],v[228:231],a[236:239]
s_waitcnt lgkmcnt(8)
ds_read_b64_tr_b16 a[100:101],v43 offset:10336
ds_read_b64_tr_b16 a[102:103],v43 offset:15456
v_mfma_f32_16x16x32_bf16 a[236:239],a[164:167],v[236:239],a[236:239]
ds_read_b64_tr_b16 a[104:105],v43 offset:20576
ds_read_b64_tr_b16 a[106:107],v43 offset:25696
v_mfma_f32_16x16x32_bf16 a[236:239],a[168:171],v[244:247],a[236:239]
ds_read_b64_tr_b16 a[108:109],v43 offset:30816
ds_read_b64_tr_b16 a[110:111],v43 offset:35936
v_mfma_f32_16x16x32_bf16 a[236:239],a[172:175],v[252:255],a[236:239]
ds_read_b64_tr_b16 a[112:113],v45 offset:96
ds_read_b64_tr_b16 a[114:115],v45 offset:5216
v_mfma_f32_16x16x32_bf16 a[252:255],a[176:179],v[152:155],a[252:255]
s_waitcnt lgkmcnt(12)
ds_read_b64_tr_b16 a[116:117],v45 offset:10336
ds_read_b64_tr_b16 a[118:119],v45 offset:15456
v_mfma_f32_16x16x32_bf16 a[252:255],a[180:183],v[184:187],a[252:255]
ds_read_b64_tr_b16 a[120:121],v45 offset:20576
ds_read_b64_tr_b16 a[122:123],v45 offset:25696
v_mfma_f32_16x16x32_bf16 a[252:255],a[184:187],v[216:219],a[252:255]
s_waitcnt lgkmcnt(14)
ds_read_b64_tr_b16 a[124:125],v45 offset:30816
ds_read_b64_tr_b16 a[126:127],v45 offset:35936
v_mfma_f32_16x16x32_bf16 a[252:255],a[188:191],v[248:251],a[252:255]
s_waitcnt lgkmcnt(14)
ds_read_b64_tr_b16 a[128:129],v1 offset:128
ds_read_b64_tr_b16 a[130:131],v1 offset:5248
v_mfma_f32_16x16x32_bf16 a[252:255],a[96:99],v[112:115],a[252:255]
s_waitcnt lgkmcnt(14)
buffer_load_dwordx2 v[26:27],v24,s[64:67],s94 offen offset:2048 sc1
ds_read_b64_tr_b16 a[132:133],v1 offset:10368
ds_read_b64_tr_b16 a[134:135],v1 offset:15488
v_mfma_f32_16x16x32_bf16 a[252:255],a[100:103],v[116:119],a[252:255]
s_waitcnt lgkmcnt(14)
ds_read_b64_tr_b16 a[136:137],v1 offset:20608
)A"
R"A(ds_read_b64_tr_b16 a[138:139],v1 offset:25728
v_mfma_f32_16x16x32_bf16 a[252:255],a[104:107],v[120:123],a[252:255]
s_waitcnt lgkmcnt(14)
ds_read_b64_tr_b16 a[140:141],v1 offset:30848
ds_read_b64_tr_b16 a[142:143],v1 offset:35968
v_mfma_f32_16x16x32_bf16 a[252:255],a[108:111],v[124:127],a[252:255]
s_waitcnt lgkmcnt(14)
ds_read_b64_tr_b16 a[144:145],v42 offset:128
ds_read_b64_tr_b16 a[146:147],v42 offset:5248
v_mfma_f32_16x16x32_bf16 a[252:255],a[112:115],v[228:231],a[252:255]
s_waitcnt lgkmcnt(14)
ds_read_b64_tr_b16 a[148:149],v42 offset:10368
ds_read_b64_tr_b16 a[150:151],v42 offset:15488
v_mfma_f32_16x16x32_bf16 a[252:255],a[116:119],v[236:239],a[252:255]
s_lshl_b32 s95,s42,2
ds_read_b64_tr_b16 a[152:153],v42 offset:20608
ds_read_b64_tr_b16 a[154:155],v42 offset:25728
s_waitcnt lgkmcnt(15)
v_mfma_f32_16x16x32_bf16 a[252:255],a[120:123],v[244:247],a[252:255]
s_add_u32 s95,s95,s40
s_lshl_b32 s95,s95,4
ds_read_b64_tr_b16 a[156:157],v42 offset:30848
ds_read_b64_tr_b16 a[158:159],v42 offset:35968
v_mov_b32 v18,s68
v_mov_b32 v19,s69
s_waitcnt lgkmcnt(15)
v_mfma_f32_16x16x32_bf16 a[252:255],a[124:127],v[252:255],a[252:255]
s_waitcnt vmcnt(1)
s_mov_b64 exec,1
buffer_store_dwordx2 v[18:19],v6,s[64:67],s95 offen offset:3072 sc1
s_mov_b64 exec,-1
ds_read_b64_tr_b16 a[160:161],v44 offset:128
ds_read_b64_tr_b16 a[162:163],v44 offset:5248
s_waitcnt lgkmcnt(15)
v_mfma_f32_16x16x32_bf16 a[12:15],a[128:131],v[152:155],a[12:15]
s_waitcnt lgkmcnt(14)
ds_read_b64_tr_b16 a[164:165],v44 offset:10368
ds_read_b64_tr_b16 a[166:167],v44 offset:15488
v_mfma_f32_16x16x32_bf16 a[12:15],a[132:135],v[184:187],a[12:15]
s_waitcnt lgkmcnt(14)
ds_read_b64_tr_b16 a[168:169],v44 offset:20608
ds_read_b64_tr_b16 a[170:171],v44 offset:25728
v_mfma_f32_16x16x32_bf16 a[12:15],a[136:139],v[216:219],a[12:15]
s_waitcnt lgkmcnt(14)
ds_read_b64_tr_b16 a[172:173],v44 offset:30848
ds_read_b64_tr_b16 a[174:175],v44 offset:35968
v_mfma_f32_16x16x32_bf16 a[12:15],a[140:143],v[248:251],a[12:15]
s_waitcnt lgkmcnt(14)
ds_read_b64_tr_b16 a[176:177],v2 offset:160
ds_read_b64_tr_b16 a[178:179],v2 offset:5280
v_mfma_f32_16x16x32_bf16 a[12:15],a[144:147],v[112:115],a[12:15]
s_waitcnt lgkmcnt(14)
ds_read_b64_tr_b16 a[180:181],v2 offset:10400
ds_read_b64_tr_b16 a[182:183],v2 offset:15520
v_mfma_f32_16x16x32_bf16 a[12:15],a[148:151],v[116:119],a[12:15]
s_waitcnt lgkmcnt(14)
ds_read_b64_tr_b16 a[184:185],v2 offset:20640
ds_read_b64_tr_b16 a[186:187],v2 offset:25760
v_mfma_f32_16x16x32_bf16 a[12:15],a[152:155],v[120:123],a[12:15]
s_waitcnt lgkmcnt(14)
ds_read_b64_tr_b16 a[188:189],v2 offset:30880
ds_read_b64_tr_b16 a[190:191],v2 offset:36000
v_mfma_f32_16x16x32_bf16 a[12:15],a[156:159],v[124:127],a[12:15]
s_waitcnt lgkmcnt(14)
ds_read_b64_tr_b16 a[96:97],v43 offset:160
ds_read_b64_tr_b16 a[98:99],v43 offset:5280
v_mfma_f32_16x16x32_bf16 a[12:15],a[160:163],v[228:231],a[12:15]
s_waitcnt lgkmcnt(14)
ds_read_b64_tr_b16 a[100:101],v43 offset:10400
ds_read_b64_tr_b16 a[102:103],v43 offset:15520
v_mfma_f32_16x16x32_bf16 a[12:15],a[164:167],v[236:239],a[12:15]
s_waitcnt lgkmcnt(14)
ds_read_b64_tr_b16 a[104:105],v43 offset:20640
ds_read_b64_tr_b16 a[106:107],v43 offset:25760
v_mfma_f32_16x16x32_bf16 a[12:15],a[168:171],v[244:247],a[12:15]
s_waitcnt lgkmcnt(14)
ds_read_b64_tr_b16 a[108:109],v43 offset:30880
ds_read_b64_tr_b16 a[110:111],v43 offset:36000
v_mfma_f32_16x16x32_bf16 a[12:15],a[172:175],v[252:255],a[12:15]
s_waitcnt lgkmcnt(14)
ds_read_b64_tr_b16 a[112:113],v45 offset:160
ds_read_b64_tr_b16 a[114:115],v45 offset:5280
v_mfma_f32_16x16x32_bf16 a[28:31],a[176:179],v[152:155],a[28:31]
s_waitcnt lgkmcnt(14)
ds_read_b64_tr_b16 a[116:117],v45 offset:10400
ds_read_b64_tr_b16 a[118:119],v45 offset:15520
v_mfma_f32_16x16x32_bf16 a[28:31],a[180:183],v[184:187],a[28:31]
s_waitcnt lgkmcnt(14)
ds_read_b64_tr_b16 a[120:121],v45 offset:20640
ds_read_b64_tr_b16 a[122:123],v45 offset:25760
v_mfma_f32_16x16x32_bf16 a[28:31],a[184:187],v[216:219],a[28:31]
s_waitcnt lgkmcnt(14)
ds_read_b64_tr_b16 a[124:125],v45 offset:30880
ds_read_b64_tr_b16 a[126:127],v45 offset:36000
v_mfma_f32_16x16x32_bf16 a[28:31],a[188:191],v[248:251],a[28:31]
s_waitcnt lgkmcnt(14)
ds_read_b64_tr_b16 a[128:129],v1 offset:192
ds_read_b64_tr_b16 a[130:131],v1 offset:5312
v_mfma_f32_16x16x32_bf16 a[28:31],a[96:99],v[112:115],a[28:31]
s_waitcnt lgkmcnt(14)
ds_read_b64_tr_b16 a[132:133],v1 offset:10432
ds_read_b64_tr_b16 a[134:135],v1 offset:15552
v_mfma_f32_16x16x32_bf16 a[28:31],a[100:103],v[116:119],a[28:31]
s_waitcnt lgkmcnt(14)
ds_read_b64_tr_b16 a[136:137],v1 offset:20672
ds_read_b64_tr_b16 a[138:139],v1 offset:25792
v_mfma_f32_16x16x32_bf16 a[28:31],a[104:107],v[120:123],a[28:31]
s_waitcnt lgkmcnt(14)
ds_read_b64_tr_b16 a[140:141],v1 offset:30912
ds_read_b64_tr_b16 a[142:143],v1 offset:36032
v_mfma_f32_16x16x32_bf16 a[28:31],a[108:111],v[124:127],a[28:31]
s_waitcnt lgkmcnt(14)
ds_read_b64_tr_b16 a[144:145],v42 offset:192
ds_read_b64_tr_b16 a[146:147],v42 offset:5312
v_mfma_f32_16x16x32_bf16 a[28:31],a[112:115],v[228:231],a[28:31]
s_waitcnt lgkmcnt(14)
ds_read_b64_tr_b16 a[148:149],v42 offset:10432
ds_read_b64_tr_b16 a[150:151],v42 offset:15552
v_mfma_f32_16x16x32_bf16 a[28:31],a[116:119],v[236:239],a[28:31]
s_waitcnt lgkmcnt(14)
ds_read_b64_tr_b16 a[152:153],v42 offset:20672
ds_read_b64_tr_b16 a[154:155],v42 offset:25792
v_mfma_f32_16x16x32_bf16 a[28:31],a[120:123],v[244:247],a[28:31]
s_waitcnt lgkmcnt(14)
ds_read_b64_tr_b16 a[156:157],v42 offset:30912
ds_read_b64_tr_b16 a[158:159],v42 offset:36032
v_mfma_f32_16x16x32_bf16 a[28:31],a[124:127],v[252:255],a[28:31]
s_waitcnt lgkmcnt(14)
ds_read_b64_tr_b16 a[160:161],v44 offset:192
ds_read_b64_tr_b16 a[162:163],v44 offset:5312
v_mfma_f32_16x16x32_bf16 a[44:47],a[128:131],v[152:155],a[44:47]
ds_read_b64_tr_b16 a[164:165],v44 offset:10432
ds_read_b64_tr_b16 a[166:167],v44 offset:15552
s_waitcnt vmcnt(1)
v_cmp_ne_u32 s[90:91],s68,v26
v_cmp_ne_u32 s[92:93],s69,v27
s_nop 0
s_or_b64 s[90:91],s[90:91],s[92:93]
s_cbranch_scc1 L_fr9_%=
L_fo9_%=:
s_waitcnt lgkmcnt(15)
v_mfma_f32_16x16x32_bf16 a[44:47],a[132:135],v[184:187],a[44:47]
s_waitcnt lgkmcnt(14)
buffer_load_dwordx4 v[64:67],v6,s[32:35],s76 offen sc1
ds_read_b64_tr_b16 a[168:169],v44 offset:20672
ds_read_b64_tr_b16 a[170:171],v44 offset:25792
v_mfma_f32_16x16x32_bf16 a[44:47],a[136:139],v[216:219],a[44:47]
s_lshl_b32 s94,s71,2
ds_read_b64_tr_b16 a[172:173],v44 offset:30912
ds_read_b64_tr_b16 a[174:175],v44 offset:36032
s_waitcnt lgkmcnt(15)
v_mfma_f32_16x16x32_bf16 a[44:47],a[140:143],v[248:251],a[44:47]
s_add_u32 s94,s94,s40
ds_read_b64_tr_b16 a[176:177],v2 offset:224
ds_read_b64_tr_b16 a[178:179],v2 offset:5344
s_waitcnt lgkmcnt(15)
v_mfma_f32_16x16x32_bf16 a[44:47],a[144:147],v[112:115],a[44:47]
s_lshl_b32 s94,s94,4
ds_read_b64_tr_b16 a[180:181],v2 offset:10464
ds_read_b64_tr_b16 a[182:183],v2 offset:15584
s_waitcnt lgkmcnt(15)
v_mfma_f32_16x16x32_bf16 a[44:47],a[148:151],v[116:119],a[44:47]
s_waitcnt lgkmcnt(14)
buffer_load_dwordx4 v[80:83],v6,s[32:35],s77 offen sc1
ds_read_b64_tr_b16 a[184:185],v2 offset:20704
ds_read_b64_tr_b16 a[186:187],v2 offset:25824
v_mfma_f32_16x16x32_bf16 a[44:47],a[152:155],v[120:123],a[44:47]
s_waitcnt lgkmcnt(14)
ds_read_b64_tr_b16 a[188:189],v2 offset:30944
ds_read_b64_tr_b16 a[190:191],v2 offset:36064
v_mfma_f32_16x16x32_bf16 a[44:47],a[156:159],v[124:127],a[44:47]
s_waitcnt lgkmcnt(14)
ds_read_b64_tr_b16 a[96:97],v43 offset:224
ds_read_b64_tr_b16 a[98:99],v43 offset:5344
v_mfma_f32_16x16x32_bf16 a[44:47],a[160:163],v[228:231],a[44:47]
s_waitcnt lgkmcnt(14)
ds_read_b64_tr_b16 a[100:101],v43 offset:10464
ds_read_b64_tr_b16 a[102:103],v43 offset:15584
v_mfma_f32_16x16x32_bf16 a[44:47],a[164:167],v[236:239],a[44:47]
s_waitcnt lgkmcnt(14)
buffer_load_dwordx4 v[96:99],v6,s[32:35],s78 offen sc1
ds_read_b64_tr_b16 a[104:105],v43 offset:20704
ds_read_b64_tr_b16 a[106:107],v43 offset:25824
v_mfma_f32_16x16x32_bf16 a[44:47],a[168:171],v[244:247],a[44:47]
s_waitcnt lgkmcnt(14)
ds_read_b64_tr_b16 a[108:109],v43 offset:30944
ds_read_b64_tr_b16 a[110:111],v43 offset:36064
v_mfma_f32_16x16x32_bf16 a[44:47],a[172:175],v[252:255],a[44:47]
s_waitcnt lgkmcnt(14)
ds_read_b64_tr_b16 a[112:113],v45 offset:224
ds_read_b64_tr_b16 a[114:115],v45 offset:5344
v_mfma_f32_16x16x32_bf16 a[60:63],a[176:179],v[152:155],a[60:63]
s_waitcnt lgkmcnt(14)
ds_read_b64_tr_b16 a[116:117],v45 offset:10464
ds_read_b64_tr_b16 a[118:119],v45 offset:15584
v_mfma_f32_16x16x32_bf16 a[60:63],a[180:183],v[184:187],a[60:63]
s_waitcnt lgkmcnt(14)
buffer_load_dwordx4 v[68:71],v6,s[32:35],s76 offen offset:1024 sc1
ds_read_b64_tr_b16 a[120:121],v45 offset:20704
ds_read_b64_tr_b16 a[122:123],v45 offset:25824
v_mfma_f32_16x16x32_bf16 a[60:63],a[184:187],v[216:219],a[60:63]
s_waitcnt lgkmcnt(14)
ds_read_b64_tr_b16 a[124:125],v45 offset:30944
ds_read_b64_tr_b16 a[126:127],v45 offset:36064
v_mfma_f32_16x16x32_bf16 a[60:63],a[188:191],v[248:251],a[60:63]
s_waitcnt lgkmcnt(14)
ds_read_b64_tr_b16 a[128:129],v1 offset:256
ds_read_b64_tr_b16 a[130:131],v1 offset:5376
v_mfma_f32_16x16x32_bf16 a[60:63],a[96:99],v[112:115],a[60:63]
s_waitcnt lgkmcnt(14)
ds_read_b64_tr_b16 a[132:133],v1 offset:10496
ds_read_b64_tr_b16 a[134:135],v1 offset:15616
v_mfma_f32_16x16x32_bf16 a[60:63],a[100:103],v[116:119],a[60:63]
s_waitcnt lgkmcnt(14)
buffer_load_dwordx4 v[84:87],v6,s[32:35],s77 offen offset:1024 sc1
ds_read_b64_tr_b16 a[136:137],v1 offset:20736
ds_read_b64_tr_b16 a[138:139],v1 offset:25856
v_mfma_f32_16x16x32_bf16 a[60:63],a[104:107],v[120:123],a[60:63]
s_waitcnt lgkmcnt(14)
ds_read_b64_tr_b16 a[140:141],v1 offset:30976
ds_read_b64_tr_b16 a[142:143],v1 offset:36096
v_mfma_f32_16x16x32_bf16 a[60:63],a[108:111],v[124:127],a[60:63]
s_waitcnt lgkmcnt(14)
ds_read_b64_tr_b16 a[144:145],v42 offset:256
ds_read_b64_tr_b16 a[146:147],v42 offset:5376
v_mfma_f32_16x16x32_bf16 a[60:63],a[112:115],v[228:231],a[60:63]
s_waitcnt lgkmcnt(14)
ds_read_b64_tr_b16 a[148:149],v42 offset:10496
ds_read_b64_tr_b16 a[150:151],v42 offset:15616
v_mfma_f32_16x16x32_bf16 a[60:63],a[116:119],v[236:239],a[60:63]
s_waitcnt lgkmcnt(14)
buffer_load_dwordx4 v[100:103],v6,s[32:35],s78 offen offset:1024 sc1
ds_read_b64_tr_b16 a[152:153],v42 offset:20736
ds_read_b64_tr_b16 a[154:155],v42 offset:25856
v_mfma_f32_16x16x32_bf16 a[60:63],a[120:123],v[244:247],a[60:63]
s_waitcnt lgkmcnt(14)
ds_read_b64_tr_b16 a[156:157],v42 offset:30976
ds_read_b64_tr_b16 a[158:159],v42 offset:36096
v_mfma_f32_16x16x32_bf16 a[60:63],a[124:127],v[252:255],a[60:63]
s_waitcnt lgkmcnt(14)
ds_read_b64_tr_b16 a[160:161],v44 offset:256
ds_read_b64_tr_b16 a[162:163],v44 offset:5376
v_mfma_f32_16x16x32_bf16 a[76:79],a[128:131],v[152:155],a[76:79]
s_waitcnt lgkmcnt(14)
ds_read_b64_tr_b16 a[164:165],v44 offset:10496
ds_read_b64_tr_b16 a[166:167],v44 offset:15616
v_mfma_f32_16x16x32_bf16 a[76:79],a[132:135],v[184:187],a[76:79]
s_waitcnt lgkmcnt(14)
buffer_load_dwordx4 v[72:75],v6,s[32:35],s76 offen offset:2048 sc1
ds_read_b64_tr_b16 a[168:169],v44 offset:20736
ds_read_b64_tr_b16 a[170:171],v44 offset:25856
v_bfe_u32 v24,v6,4,2
v_min_u32 v24,2,v24
v_mfma_f32_16x16x32_bf16 a[76:79],a[136:139],v[216:219],a[76:79]
s_waitcnt lgkmcnt(14)
ds_read_b64_tr_b16 a[172:173],v44 offset:30976
ds_read_b64_tr_b16 a[174:175],v44 offset:36096
v_add_u32 v24,s70,v24
v_add_u32 v24,1,v24
v_mfma_f32_16x16x32_bf16 a[76:79],a[140:143],v[248:251],a[76:79]
s_waitcnt lgkmcnt(14)
ds_read_b64_tr_b16 a[176:177],v2 offset:288
ds_read_b64_tr_b16 a[178:179],v2 offset:5408
v_and_b32 v24,3,v24
v_lshlrev_b32 v24,8,v24
v_mfma_f32_16x16x32_bf16 a[76:79],a[144:147],v[112:115],a[76:79]
s_waitcnt lgkmcnt(14)
ds_read_b64_tr_b16 a[180:181],v2 offset:10528
ds_read_b64_tr_b16 a[182:183],v2 offset:15648
v_mfma_f32_16x16x32_bf16 a[76:79],a[148:151],v[116:119],a[76:79]
s_waitcnt lgkmcnt(14)
buffer_load_dwordx4 v[88:91],v6,s[32:35],s77 offen offset:2048 sc1
ds_read_b64_tr_b16 a[184:185],v2 offset:20768
ds_read_b64_tr_b16 a[186:187],v2 offset:25888
v_mfma_f32_16x16x32_bf16 a[76:79],a[152:155],v[120:123],a[76:79]
s_waitcnt lgkmcnt(14)
ds_read_b64_tr_b16 a[188:189],v2 offset:31008
)A"
R"A(ds_read_b64_tr_b16 a[190:191],v2 offset:36128
v_mfma_f32_16x16x32_bf16 a[76:79],a[156:159],v[124:127],a[76:79]
s_waitcnt lgkmcnt(14)
ds_read_b64_tr_b16 a[96:97],v43 offset:288
ds_read_b64_tr_b16 a[98:99],v43 offset:5408
v_mfma_f32_16x16x32_bf16 a[76:79],a[160:163],v[228:231],a[76:79]
s_waitcnt lgkmcnt(14)
ds_read_b64_tr_b16 a[100:101],v43 offset:10528
ds_read_b64_tr_b16 a[102:103],v43 offset:15648
v_mfma_f32_16x16x32_bf16 a[76:79],a[164:167],v[236:239],a[76:79]
s_waitcnt lgkmcnt(14)
buffer_load_dwordx4 v[104:107],v6,s[32:35],s78 offen offset:2048 sc1
ds_read_b64_tr_b16 a[104:105],v43 offset:20768
ds_read_b64_tr_b16 a[106:107],v43 offset:25888
v_mfma_f32_16x16x32_bf16 a[76:79],a[168:171],v[244:247],a[76:79]
s_waitcnt lgkmcnt(14)
ds_read_b64_tr_b16 a[108:109],v43 offset:31008
ds_read_b64_tr_b16 a[110:111],v43 offset:36128
v_mfma_f32_16x16x32_bf16 a[76:79],a[172:175],v[252:255],a[76:79]
s_waitcnt lgkmcnt(14)
ds_read_b64_tr_b16 a[112:113],v45 offset:288
ds_read_b64_tr_b16 a[114:115],v45 offset:5408
v_mfma_f32_16x16x32_bf16 a[92:95],a[176:179],v[152:155],a[92:95]
s_waitcnt lgkmcnt(14)
ds_read_b64_tr_b16 a[116:117],v45 offset:10528
ds_read_b64_tr_b16 a[118:119],v45 offset:15648
v_mfma_f32_16x16x32_bf16 a[92:95],a[180:183],v[184:187],a[92:95]
s_waitcnt lgkmcnt(14)
buffer_load_dwordx4 v[76:79],v6,s[32:35],s76 offen offset:3072 sc1
ds_read_b64_tr_b16 a[120:121],v45 offset:20768
ds_read_b64_tr_b16 a[122:123],v45 offset:25888
v_mfma_f32_16x16x32_bf16 a[92:95],a[184:187],v[216:219],a[92:95]
s_waitcnt lgkmcnt(14)
ds_read_b64_tr_b16 a[124:125],v45 offset:31008
ds_read_b64_tr_b16 a[126:127],v45 offset:36128
v_mfma_f32_16x16x32_bf16 a[92:95],a[188:191],v[248:251],a[92:95]
s_waitcnt lgkmcnt(14)
v_mfma_f32_16x16x32_bf16 a[92:95],a[96:99],v[112:115],a[92:95]
s_waitcnt lgkmcnt(12)
v_mfma_f32_16x16x32_bf16 a[92:95],a[100:103],v[116:119],a[92:95]
s_waitcnt lgkmcnt(10)
buffer_load_dwordx4 v[92:95],v6,s[32:35],s77 offen offset:3072 sc1
v_mfma_f32_16x16x32_bf16 a[92:95],a[104:107],v[120:123],a[92:95]
s_waitcnt lgkmcnt(8)
v_mfma_f32_16x16x32_bf16 a[92:95],a[108:111],v[124:127],a[92:95]
s_waitcnt lgkmcnt(6)
v_mfma_f32_16x16x32_bf16 a[92:95],a[112:115],v[228:231],a[92:95]
s_waitcnt lgkmcnt(4)
v_mfma_f32_16x16x32_bf16 a[92:95],a[116:119],v[236:239],a[92:95]
s_waitcnt lgkmcnt(2)
buffer_load_dwordx4 v[108:111],v6,s[32:35],s78 offen offset:3072 sc1
v_mfma_f32_16x16x32_bf16 a[92:95],a[120:123],v[244:247],a[92:95]
s_waitcnt lgkmcnt(0)
v_mfma_f32_16x16x32_bf16 a[92:95],a[124:127],v[252:255],a[92:95]
buffer_load_dwordx2 v[26:27],v24,s[64:67],s94 offen offset:3072 sc1
s_waitcnt vmcnt(0)
v_cmp_ne_u32 s[90:91],s68,v26
v_cmp_ne_u32 s[92:93],s69,v27
s_nop 0
s_or_b64 s[90:91],s[90:91],s[92:93]
s_cbranch_scc1 L_fr10_%=
L_fo10_%=:
buffer_load_dwordx4 v[132:135],v25,s[32:35],s76 offen sc1
buffer_load_dwordx4 v[164:167],v25,s[32:35],s77 offen sc1
buffer_load_dwordx4 v[196:199],v25,s[32:35],s78 offen sc1
v_mfma_f32_16x16x32_bf16 a[204:207],v[34:37],v[64:67],a[204:207]
v_mfma_f32_16x16x32_bf16 a[204:207],v[34:37],v[80:83],a[204:207]
v_mfma_f32_16x16x32_bf16 a[204:207],v[34:37],v[96:99],a[204:207]
v_mfma_f32_16x16x32_bf16 a[220:223],v[32:35],v[64:67],a[220:223]
v_mfma_f32_16x16x32_bf16 a[220:223],v[32:35],v[80:83],a[220:223]
v_mfma_f32_16x16x32_bf16 a[220:223],v[32:35],v[96:99],a[220:223]
v_mfma_f32_16x16x32_bf16 a[236:239],v[34:37],v[68:71],a[236:239]
v_mfma_f32_16x16x32_bf16 a[236:239],v[34:37],v[84:87],a[236:239]
v_mfma_f32_16x16x32_bf16 a[236:239],v[34:37],v[100:103],a[236:239]
s_nop 1
v_accvgpr_read_b32 v128,a204
v_accvgpr_read_b32 v129,a205
v_mfma_f32_16x16x32_bf16 a[252:255],v[32:35],v[68:71],a[252:255]
v_cvt_pk_bf16_f32 v128,v128,v129
v_accvgpr_read_b32 v130,a206
v_mfma_f32_16x16x32_bf16 a[252:255],v[32:35],v[84:87],a[252:255]
v_accvgpr_read_b32 v131,a207
v_cvt_pk_bf16_f32 v129,v130,v131
v_mfma_f32_16x16x32_bf16 a[252:255],v[32:35],v[100:103],a[252:255]
buffer_store_dwordx2 v[128:129],v40,s[20:23],s43 offen
v_mfma_f32_16x16x32_bf16 a[12:15],v[34:37],v[72:75],a[12:15]
v_accvgpr_read_b32 v130,a220
v_accvgpr_read_b32 v131,a221
v_mfma_f32_16x16x32_bf16 a[12:15],v[34:37],v[88:91],a[12:15]
v_accvgpr_read_b32 v12,a222
v_accvgpr_read_b32 v13,a223
v_mfma_f32_16x16x32_bf16 a[12:15],v[34:37],v[104:107],a[12:15]
v_cvt_pk_bf16_f32 v130,v130,v131
v_cvt_pk_bf16_f32 v131,v12,v13
v_mfma_f32_16x16x32_bf16 a[28:31],v[32:35],v[72:75],a[28:31]
buffer_store_dwordx2 v[130:131],v40,s[20:23],s43 offen offset:32
v_mfma_f32_16x16x32_bf16 a[28:31],v[32:35],v[88:91],a[28:31]
v_accvgpr_read_b32 v136,a236
v_accvgpr_read_b32 v137,a237
v_mfma_f32_16x16x32_bf16 a[28:31],v[32:35],v[104:107],a[28:31]
v_cvt_pk_bf16_f32 v136,v136,v137
v_accvgpr_read_b32 v138,a238
v_mfma_f32_16x16x32_bf16 a[44:47],v[34:37],v[76:79],a[44:47]
v_accvgpr_read_b32 v139,a239
v_cvt_pk_bf16_f32 v137,v138,v139
v_mfma_f32_16x16x32_bf16 a[44:47],v[34:37],v[92:95],a[44:47]
buffer_store_dwordx2 v[136:137],v40,s[20:23],s43 offen offset:64
v_mfma_f32_16x16x32_bf16 a[44:47],v[34:37],v[108:111],a[44:47]
v_accvgpr_read_b32 v144,a12
v_accvgpr_read_b32 v145,a13
v_cvt_pk_bf16_f32 v144,v144,v145
v_accvgpr_read_b32 v146,a14
v_accvgpr_read_b32 v147,a15
v_mfma_f32_16x16x32_bf16 a[60:63],v[32:35],v[76:79],a[60:63]
v_accvgpr_read_b32 v138,a252
v_accvgpr_read_b32 v139,a253
v_cvt_pk_bf16_f32 v145,v146,v147
v_mfma_f32_16x16x32_bf16 a[60:63],v[32:35],v[92:95],a[60:63]
v_accvgpr_read_b32 v14,a254
v_accvgpr_read_b32 v15,a255
v_cvt_pk_bf16_f32 v138,v138,v139
v_cvt_pk_bf16_f32 v139,v14,v15
v_mfma_f32_16x16x32_bf16 a[60:63],v[32:35],v[108:111],a[60:63]
s_waitcnt vmcnt(5)
buffer_store_dwordx2 v[138:139],v40,s[20:23],s43 offen offset:96
buffer_store_dwordx2 v[144:145],v40,s[20:23],s43 offen offset:128
v_accvgpr_read_b32 v146,a28
v_accvgpr_read_b32 v147,a29
v_accvgpr_read_b32 v16,a30
v_accvgpr_read_b32 v17,a31
v_cvt_pk_bf16_f32 v146,v146,v147
v_cvt_pk_bf16_f32 v147,v16,v17
s_nop 3
buffer_store_dwordx2 v[146:147],v40,s[20:23],s43 offen offset:160
v_accvgpr_read_b32 v160,a44
v_accvgpr_read_b32 v161,a45
v_cvt_pk_bf16_f32 v160,v160,v161
v_accvgpr_read_b32 v162,a46
v_accvgpr_read_b32 v163,a47
v_cvt_pk_bf16_f32 v161,v162,v163
buffer_store_dwordx2 v[160:161],v40,s[20:23],s43 offen offset:192
v_accvgpr_read_b32 v162,a60
v_accvgpr_read_b32 v163,a61
v_accvgpr_read_b32 v12,a62
v_accvgpr_read_b32 v13,a63
v_cvt_pk_bf16_f32 v162,v162,v163
v_cvt_pk_bf16_f32 v163,v12,v13
buffer_store_dwordx2 v[162:163],v40,s[20:23],s43 offen offset:224
v_mfma_f32_16x16x32_bf16 a[76:79],v[34:37],v[132:135],a[76:79]
s_waitcnt vmcnt(9)
v_mfma_f32_16x16x32_bf16 a[76:79],v[34:37],v[164:167],a[76:79]
s_waitcnt vmcnt(8)
v_mfma_f32_16x16x32_bf16 a[76:79],v[34:37],v[196:199],a[76:79]
v_mfma_f32_16x16x32_bf16 a[92:95],v[32:35],v[132:135],a[92:95]
v_mfma_f32_16x16x32_bf16 a[92:95],v[32:35],v[164:167],a[92:95]
v_mfma_f32_16x16x32_bf16 a[92:95],v[32:35],v[196:199],a[92:95]
s_nop 3
s_nop 0
v_accvgpr_read_b32 v168,a76
v_accvgpr_read_b32 v169,a77
v_cvt_pk_bf16_f32 v168,v168,v169
v_accvgpr_read_b32 v170,a78
v_accvgpr_read_b32 v171,a79
v_cvt_pk_bf16_f32 v169,v170,v171
buffer_store_dwordx2 v[168:169],v40,s[20:23],s43 offen offset:256
v_accvgpr_read_b32 v170,a92
v_accvgpr_read_b32 v171,a93
v_accvgpr_read_b32 v14,a94
v_accvgpr_read_b32 v15,a95
v_cvt_pk_bf16_f32 v170,v170,v171
v_cvt_pk_bf16_f32 v171,v14,v15
buffer_store_dwordx2 v[170:171],v40,s[20:23],s43 offen offset:288
s_or_b32 s62,s42,s40
s_cmp_lg_u32 s62,0
s_cbranch_scc1 L_noep_%=
v_mov_b32 v12,s68
s_mov_b64 exec,1
buffer_store_dword v12,v25,s[64:67],0 offen sc1
s_mov_b64 exec,-1
L_noep_%=:
s_waitcnt vmcnt(0) lgkmcnt(0)
s_branch L_end_%=
L_ps1_%=:
s_min_i32 s79,s79,100
s_add_u32 s78,s78,s79
L_pl1_%=:
s_sleep 1
s_sub_u32 s79,s79,4
s_cmp_gt_i32 s79,1
s_cbranch_scc1 L_pl1_%=
s_branch L_pr1_%=
L_ps2_%=:
s_min_i32 s79,s79,100
s_add_u32 s78,s78,s79
L_pl2_%=:
s_sleep 1
s_sub_u32 s79,s79,4
s_cmp_gt_i32 s79,1
s_cbranch_scc1 L_pl2_%=
s_branch L_pr2_%=
L_ps3_%=:
s_min_i32 s79,s79,100
s_add_u32 s78,s78,s79
L_pl3_%=:
s_sleep 1
s_sub_u32 s79,s79,4
s_cmp_gt_i32 s79,1
s_cbranch_scc1 L_pl3_%=
s_branch L_pr3_%=
L_ps4_%=:
s_min_i32 s79,s79,100
s_add_u32 s78,s78,s79
L_pl4_%=:
s_sleep 1
s_sub_u32 s79,s79,4
s_cmp_gt_i32 s79,1
s_cbranch_scc1 L_pl4_%=
s_branch L_pr4_%=
L_ps5_%=:
s_min_i32 s79,s79,100
s_add_u32 s78,s78,s79
L_pl5_%=:
s_sleep 1
s_sub_u32 s79,s79,4
s_cmp_gt_i32 s79,1
s_cbranch_scc1 L_pl5_%=
s_branch L_pr5_%=
L_ps6_%=:
s_min_i32 s79,s79,100
s_add_u32 s78,s78,s79
L_pl6_%=:
s_sleep 1
s_sub_u32 s79,s79,4
s_cmp_gt_i32 s79,1
s_cbranch_scc1 L_pl6_%=
s_branch L_pr6_%=
L_fr7_%=:
s_mov_b32 s88,262144
L_fl7_%=:
buffer_load_dwordx2 v[26:27],v24,s[64:67],s94 offen sc1
s_waitcnt vmcnt(0)
s_sub_u32 s88,s88,1
s_cmp_eq_u32 s88,0
s_cbranch_scc1 L_fo7_%=
v_cmp_ne_u32 s[90:91],s68,v26
v_cmp_ne_u32 s[92:93],s69,v27
s_nop 0
s_or_b64 s[90:91],s[90:91],s[92:93]
s_cbranch_scc1 L_fl7_%=
s_branch L_fo7_%=
L_fr8_%=:
s_mov_b32 s88,262144
L_fl8_%=:
buffer_load_dwordx2 v[26:27],v24,s[64:67],s94 offen offset:1024 sc1
s_waitcnt vmcnt(0)
s_sub_u32 s88,s88,1
s_cmp_eq_u32 s88,0
s_cbranch_scc1 L_fo8_%=
v_cmp_ne_u32 s[90:91],s68,v26
v_cmp_ne_u32 s[92:93],s69,v27
s_nop 0
s_or_b64 s[90:91],s[90:91],s[92:93]
s_cbranch_scc1 L_fl8_%=
s_branch L_fo8_%=
L_fr9_%=:
s_mov_b32 s88,262144
L_fl9_%=:
buffer_load_dwordx2 v[26:27],v24,s[64:67],s94 offen offset:2048 sc1
s_waitcnt vmcnt(0)
s_sub_u32 s88,s88,1
s_cmp_eq_u32 s88,0
s_cbranch_scc1 L_fo9_%=
v_cmp_ne_u32 s[90:91],s68,v26
v_cmp_ne_u32 s[92:93],s69,v27
s_nop 0
s_or_b64 s[90:91],s[90:91],s[92:93]
s_cbranch_scc1 L_fl9_%=
s_branch L_fo9_%=
L_fr10_%=:
s_mov_b32 s88,262144
L_fl10_%=:
buffer_load_dwordx2 v[26:27],v24,s[64:67],s94 offen offset:3072 sc1
s_waitcnt vmcnt(0)
s_sub_u32 s88,s88,1
s_cmp_eq_u32 s88,0
s_cbranch_scc1 L_fo10_%=
v_cmp_ne_u32 s[90:91],s68,v26
v_cmp_ne_u32 s[92:93],s69,v27
s_nop 0
s_or_b64 s[90:91],s[90:91],s[92:93]
s_cbranch_scc1 L_fl10_%=
s_branch L_fo10_%=
L_fast_end_%=:
L_slow0_%=:
s_mov_b32 s98,0
s_branch L_slowc_%=
L_slow1_%=:
s_mov_b32 s98,1
L_slowc_%=:
s_nop 7
s_nop 7
s_waitcnt vmcnt(0) lgkmcnt(0)
s_lshl_b32 s96,%1,2
s_add_u32 s96,s96,s40
s_lshl_b32 s96,s96,15
s_add_u32 s96,s96,167772160
s_mov_b64 s[56:57],s[12:13]
s_add_u32 s56,s56,s96
s_addc_u32 s57,s57,0
s_and_b32 s57,s57,0xffff
s_mov_b32 s58,32768
s_mov_b32 s59,0x20000
s_mov_b32 s96,0
buffer_store_dwordx4 v[64:67],v6,s[56:59],s96 offen
buffer_store_dwordx4 v[68:71],v6,s[56:59],s96 offen offset:1024
buffer_store_dwordx4 v[72:75],v6,s[56:59],s96 offen offset:2048
buffer_store_dwordx4 v[76:79],v6,s[56:59],s96 offen offset:3072
s_mov_b32 s96,4096
buffer_store_dwordx4 v[80:83],v6,s[56:59],s96 offen
buffer_store_dwordx4 v[84:87],v6,s[56:59],s96 offen offset:1024
buffer_store_dwordx4 v[88:91],v6,s[56:59],s96 offen offset:2048
buffer_store_dwordx4 v[92:95],v6,s[56:59],s96 offen offset:3072
s_mov_b32 s96,8192
buffer_store_dwordx4 v[96:99],v6,s[56:59],s96 offen
buffer_store_dwordx4 v[100:103],v6,s[56:59],s96 offen offset:1024
buffer_store_dwordx4 v[104:107],v6,s[56:59],s96 offen offset:2048
buffer_store_dwordx4 v[108:111],v6,s[56:59],s96 offen offset:3072
s_mov_b32 s96,12288
buffer_store_dwordx4 v[112:115],v6,s[56:59],s96 offen
buffer_store_dwordx4 v[116:119],v6,s[56:59],s96 offen offset:1024
buffer_store_dwordx4 v[120:123],v6,s[56:59],s96 offen offset:2048
buffer_store_dwordx4 v[124:127],v6,s[56:59],s96 offen offset:3072
s_mov_b32 s96,16384
buffer_store_dwordx4 v[128:131],v6,s[56:59],s96 offen
buffer_store_dwordx4 v[132:135],v6,s[56:59],s96 offen offset:1024
buffer_store_dwordx4 v[136:139],v6,s[56:59],s96 offen offset:2048
buffer_store_dwordx4 v[140:143],v6,s[56:59],s96 offen offset:3072
s_mov_b32 s96,20480
buffer_store_dwordx4 v[144:147],v6,s[56:59],s96 offen
buffer_store_dwordx4 v[148:151],v6,s[56:59],s96 offen offset:1024
buffer_store_dwordx4 v[152:155],v6,s[56:59],s96 offen offset:2048
buffer_store_dwordx4 v[156:159],v6,s[56:59],s96 offen offset:3072
s_mov_b32 s96,24576
buffer_store_dwordx4 v[160:163],v6,s[56:59],s96 offen
buffer_store_dwordx4 v[164:167],v6,s[56:59],s96 offen offset:1024
buffer_store_dwordx4 v[168:171],v6,s[56:59],s96 offen offset:2048
)A"
R"A(buffer_store_dwordx4 v[172:175],v6,s[56:59],s96 offen offset:3072
s_mov_b32 s96,28672
buffer_store_dwordx4 v[176:179],v6,s[56:59],s96 offen
buffer_store_dwordx4 v[180:183],v6,s[56:59],s96 offen offset:1024
buffer_store_dwordx4 v[184:187],v6,s[56:59],s96 offen offset:2048
buffer_store_dwordx4 v[188:191],v6,s[56:59],s96 offen offset:3072
s_waitcnt vmcnt(0)
v_writelane_b32 v173,s88,0
v_writelane_b32 v173,s89,1
v_writelane_b32 v173,s90,2
v_writelane_b32 v173,s91,3
v_writelane_b32 v173,s92,4
v_writelane_b32 v173,s93,5
v_writelane_b32 v173,s94,6
v_writelane_b32 v173,s95,7
s_mov_b32 s88,s98
s_mul_i32 s90,s41,307200
s_mov_b64 s[92:93],s[4:5]
s_add_u32 s92,s92,s90
s_addc_u32 s93,s93,0
s_and_b32 s93,s93,0xffff
s_mov_b32 s94,307200
s_mov_b32 s95,0x20000
s_mov_b64 s[96:97],s[10:11]
s_and_b32 s97,s97,0xffff
s_mov_b32 s98,5242880
s_mov_b32 s99,0x20000
v_bfe_u32 v171,v6,4,4
v_lshrrev_b32 v172,8,v6
v_lshlrev_b32 v164,8,v171
v_lshl_add_u32 v164,v172,4,v164
v_lshlrev_b32 v165,10,v172
v_lshl_add_u32 v165,v171,1,v165
v_add_u32 v166,4096,v165
v_lshlrev_b32 v168,16,v172
v_lshrrev_b32 v170,2,v171
v_lshl_add_u32 v168,v170,4,v168
v_and_b32 v170,3,v171
v_lshl_add_u32 v168,v170,1,v168
s_lshl_b32 s90,s42,1
s_add_u32 s90,s90,s88
s_lshl_b32 s90,s90,8
v_add_u32 v168,s90,v168
s_mov_b32 s89,0
L_sjb_%=:
s_add_u32 s90,s89,s70
s_add_u32 s90,s90,1
s_and_b32 s90,s90,3
s_lshl_b32 s91,s40,2
s_add_u32 s90,s90,s91
s_lshl_b32 s90,s90,4
v_bfe_u32 v171,v6,4,4
v_lshrrev_b32 v172,8,v6
v_add_u32 v167,s90,v171
v_mul_u32_u24 v167,1280,v167
v_lshl_add_u32 v167,v172,4,v167
s_lshl_b32 s90,s88,4
s_add_u32 s90,s90,s89
s_lshl_b32 s90,s90,12
v_add_u32 v169,s90,v6
v_mov_b32 v64,0
v_mov_b32 v65,0
v_mov_b32 v66,0
v_mov_b32 v67,0
v_mov_b32 v68,0
v_mov_b32 v69,0
v_mov_b32 v70,0
v_mov_b32 v71,0
v_mov_b32 v72,0
v_mov_b32 v73,0
v_mov_b32 v74,0
v_mov_b32 v75,0
v_mov_b32 v76,0
v_mov_b32 v77,0
v_mov_b32 v78,0
v_mov_b32 v79,0
v_mov_b32 v80,0
v_mov_b32 v81,0
v_mov_b32 v82,0
v_mov_b32 v83,0
v_mov_b32 v84,0
v_mov_b32 v85,0
v_mov_b32 v86,0
v_mov_b32 v87,0
v_mov_b32 v88,0
v_mov_b32 v89,0
v_mov_b32 v90,0
v_mov_b32 v91,0
v_mov_b32 v92,0
v_mov_b32 v93,0
v_mov_b32 v94,0
v_mov_b32 v95,0
s_mov_b32 s90,0
L_sq_%=:
s_lshl_b32 s91,s90,6
buffer_load_dwordx4 v[160:163],v167,s[92:95],s91 offen
s_lshl_b32 s91,s90,18
buffer_load_ushort v112,v168,s[96:99],s91 offen
buffer_load_ushort v120,v168,s[96:99],s91 offen offset:8
buffer_load_ushort v128,v168,s[96:99],s91 offen offset:64
buffer_load_ushort v136,v168,s[96:99],s91 offen offset:72
s_add_u32 s91,s91,8192
buffer_load_ushort v113,v168,s[96:99],s91 offen
buffer_load_ushort v121,v168,s[96:99],s91 offen offset:8
buffer_load_ushort v129,v168,s[96:99],s91 offen offset:64
buffer_load_ushort v137,v168,s[96:99],s91 offen offset:72
s_add_u32 s91,s91,8192
buffer_load_ushort v114,v168,s[96:99],s91 offen
buffer_load_ushort v122,v168,s[96:99],s91 offen offset:8
buffer_load_ushort v130,v168,s[96:99],s91 offen offset:64
buffer_load_ushort v138,v168,s[96:99],s91 offen offset:72
s_add_u32 s91,s91,8192
buffer_load_ushort v115,v168,s[96:99],s91 offen
buffer_load_ushort v123,v168,s[96:99],s91 offen offset:8
buffer_load_ushort v131,v168,s[96:99],s91 offen offset:64
buffer_load_ushort v139,v168,s[96:99],s91 offen offset:72
s_add_u32 s91,s91,8192
buffer_load_ushort v116,v168,s[96:99],s91 offen
buffer_load_ushort v124,v168,s[96:99],s91 offen offset:8
buffer_load_ushort v132,v168,s[96:99],s91 offen offset:64
buffer_load_ushort v140,v168,s[96:99],s91 offen offset:72
s_add_u32 s91,s91,8192
buffer_load_ushort v117,v168,s[96:99],s91 offen
buffer_load_ushort v125,v168,s[96:99],s91 offen offset:8
buffer_load_ushort v133,v168,s[96:99],s91 offen offset:64
buffer_load_ushort v141,v168,s[96:99],s91 offen offset:72
s_add_u32 s91,s91,8192
buffer_load_ushort v118,v168,s[96:99],s91 offen
buffer_load_ushort v126,v168,s[96:99],s91 offen offset:8
buffer_load_ushort v134,v168,s[96:99],s91 offen offset:64
buffer_load_ushort v142,v168,s[96:99],s91 offen offset:72
s_add_u32 s91,s91,8192
buffer_load_ushort v119,v168,s[96:99],s91 offen
buffer_load_ushort v127,v168,s[96:99],s91 offen offset:8
buffer_load_ushort v135,v168,s[96:99],s91 offen offset:64
buffer_load_ushort v143,v168,s[96:99],s91 offen offset:72
s_add_u32 s91,s91,8192
s_waitcnt vmcnt(0)
v_lshl_or_b32 v112,v113,16,v112
v_lshl_or_b32 v113,v115,16,v114
v_lshl_or_b32 v114,v117,16,v116
v_lshl_or_b32 v115,v119,16,v118
v_lshl_or_b32 v120,v121,16,v120
v_lshl_or_b32 v121,v123,16,v122
v_lshl_or_b32 v122,v125,16,v124
v_lshl_or_b32 v123,v127,16,v126
v_lshl_or_b32 v128,v129,16,v128
v_lshl_or_b32 v129,v131,16,v130
v_lshl_or_b32 v130,v133,16,v132
v_lshl_or_b32 v131,v135,16,v134
v_lshl_or_b32 v136,v137,16,v136
v_lshl_or_b32 v137,v139,16,v138
v_lshl_or_b32 v138,v141,16,v140
v_lshl_or_b32 v139,v143,16,v142
s_nop 1
v_mfma_f32_16x16x32_bf16 v[64:67],v[112:115],v[160:163],v[64:67]
v_mfma_f32_16x16x32_bf16 v[68:71],v[120:123],v[160:163],v[68:71]
v_mfma_f32_16x16x32_bf16 v[72:75],v[128:131],v[160:163],v[72:75]
v_mfma_f32_16x16x32_bf16 v[76:79],v[136:139],v[160:163],v[76:79]
s_nop 7
s_lshl_b32 s91,s90,18
buffer_load_ushort v112,v168,s[96:99],s91 offen offset:128
buffer_load_ushort v120,v168,s[96:99],s91 offen offset:136
buffer_load_ushort v128,v168,s[96:99],s91 offen offset:192
buffer_load_ushort v136,v168,s[96:99],s91 offen offset:200
s_add_u32 s91,s91,8192
buffer_load_ushort v113,v168,s[96:99],s91 offen offset:128
buffer_load_ushort v121,v168,s[96:99],s91 offen offset:136
buffer_load_ushort v129,v168,s[96:99],s91 offen offset:192
buffer_load_ushort v137,v168,s[96:99],s91 offen offset:200
s_add_u32 s91,s91,8192
buffer_load_ushort v114,v168,s[96:99],s91 offen offset:128
buffer_load_ushort v122,v168,s[96:99],s91 offen offset:136
buffer_load_ushort v130,v168,s[96:99],s91 offen offset:192
buffer_load_ushort v138,v168,s[96:99],s91 offen offset:200
s_add_u32 s91,s91,8192
buffer_load_ushort v115,v168,s[96:99],s91 offen offset:128
buffer_load_ushort v123,v168,s[96:99],s91 offen offset:136
buffer_load_ushort v131,v168,s[96:99],s91 offen offset:192
buffer_load_ushort v139,v168,s[96:99],s91 offen offset:200
s_add_u32 s91,s91,8192
buffer_load_ushort v116,v168,s[96:99],s91 offen offset:128
buffer_load_ushort v124,v168,s[96:99],s91 offen offset:136
buffer_load_ushort v132,v168,s[96:99],s91 offen offset:192
buffer_load_ushort v140,v168,s[96:99],s91 offen offset:200
s_add_u32 s91,s91,8192
buffer_load_ushort v117,v168,s[96:99],s91 offen offset:128
buffer_load_ushort v125,v168,s[96:99],s91 offen offset:136
buffer_load_ushort v133,v168,s[96:99],s91 offen offset:192
buffer_load_ushort v141,v168,s[96:99],s91 offen offset:200
s_add_u32 s91,s91,8192
buffer_load_ushort v118,v168,s[96:99],s91 offen offset:128
buffer_load_ushort v126,v168,s[96:99],s91 offen offset:136
buffer_load_ushort v134,v168,s[96:99],s91 offen offset:192
buffer_load_ushort v142,v168,s[96:99],s91 offen offset:200
s_add_u32 s91,s91,8192
buffer_load_ushort v119,v168,s[96:99],s91 offen offset:128
buffer_load_ushort v127,v168,s[96:99],s91 offen offset:136
buffer_load_ushort v135,v168,s[96:99],s91 offen offset:192
buffer_load_ushort v143,v168,s[96:99],s91 offen offset:200
s_add_u32 s91,s91,8192
s_waitcnt vmcnt(0)
v_lshl_or_b32 v112,v113,16,v112
v_lshl_or_b32 v113,v115,16,v114
v_lshl_or_b32 v114,v117,16,v116
v_lshl_or_b32 v115,v119,16,v118
v_lshl_or_b32 v120,v121,16,v120
v_lshl_or_b32 v121,v123,16,v122
v_lshl_or_b32 v122,v125,16,v124
v_lshl_or_b32 v123,v127,16,v126
v_lshl_or_b32 v128,v129,16,v128
v_lshl_or_b32 v129,v131,16,v130
v_lshl_or_b32 v130,v133,16,v132
v_lshl_or_b32 v131,v135,16,v134
v_lshl_or_b32 v136,v137,16,v136
v_lshl_or_b32 v137,v139,16,v138
v_lshl_or_b32 v138,v141,16,v140
v_lshl_or_b32 v139,v143,16,v142
s_nop 1
v_mfma_f32_16x16x32_bf16 v[80:83],v[112:115],v[160:163],v[80:83]
v_mfma_f32_16x16x32_bf16 v[84:87],v[120:123],v[160:163],v[84:87]
v_mfma_f32_16x16x32_bf16 v[88:91],v[128:131],v[160:163],v[88:91]
v_mfma_f32_16x16x32_bf16 v[92:95],v[136:139],v[160:163],v[92:95]
s_nop 7
s_add_u32 s90,s90,1
s_cmp_lg_u32 s90,20
s_cbranch_scc1 L_sq_%=
s_nop 7
v_mul_f32 v64,s61,v64
v_mul_f32 v65,s61,v65
v_mul_f32 v66,s61,v66
v_mul_f32 v67,s61,v67
v_mul_f32 v68,s61,v68
v_mul_f32 v69,s61,v69
v_mul_f32 v70,s61,v70
v_mul_f32 v71,s61,v71
v_mul_f32 v72,s61,v72
v_mul_f32 v73,s61,v73
v_mul_f32 v74,s61,v74
v_mul_f32 v75,s61,v75
v_mul_f32 v76,s61,v76
v_mul_f32 v77,s61,v77
v_mul_f32 v78,s61,v78
v_mul_f32 v79,s61,v79
v_mul_f32 v80,s61,v80
v_mul_f32 v81,s61,v81
v_mul_f32 v82,s61,v82
v_mul_f32 v83,s61,v83
v_mul_f32 v84,s61,v84
v_mul_f32 v85,s61,v85
v_mul_f32 v86,s61,v86
v_mul_f32 v87,s61,v87
v_mul_f32 v88,s61,v88
v_mul_f32 v89,s61,v89
v_mul_f32 v90,s61,v90
v_mul_f32 v91,s61,v91
v_mul_f32 v92,s61,v92
v_mul_f32 v93,s61,v93
v_mul_f32 v94,s61,v94
v_mul_f32 v95,s61,v95
v_cvt_pk_bf16_f32 v96,v64,v65
v_cvt_pk_bf16_f32 v97,v66,v67
v_cvt_pk_bf16_f32 v98,v68,v69
v_cvt_pk_bf16_f32 v99,v70,v71
v_cvt_pk_bf16_f32 v100,v72,v73
v_cvt_pk_bf16_f32 v101,v74,v75
v_cvt_pk_bf16_f32 v102,v76,v77
v_cvt_pk_bf16_f32 v103,v78,v79
v_cvt_pk_bf16_f32 v104,v80,v81
v_cvt_pk_bf16_f32 v105,v82,v83
v_cvt_pk_bf16_f32 v106,v84,v85
v_cvt_pk_bf16_f32 v107,v86,v87
v_cvt_pk_bf16_f32 v108,v88,v89
v_cvt_pk_bf16_f32 v109,v90,v91
v_cvt_pk_bf16_f32 v110,v92,v93
v_cvt_pk_bf16_f32 v111,v94,v95
v_mov_b32 v170,0xff800000
s_lshl_b32 s91,s88,18
s_mov_b32 s90,64
L_s1_%=:
buffer_load_dwordx4 v[112:115],v164,s[20:23],s91 offen
buffer_load_dwordx4 v[116:119],v164,s[20:23],s91 offen offset:64
buffer_load_dwordx4 v[120:123],v164,s[20:23],s91 offen offset:128
buffer_load_dwordx4 v[124:127],v164,s[20:23],s91 offen offset:192
s_waitcnt vmcnt(0)
v_mfma_f32_16x16x32_bf16 v[144:147],v[112:115],v[96:99],0
v_mfma_f32_16x16x32_bf16 v[144:147],v[116:119],v[100:103],v[144:147]
v_mfma_f32_16x16x32_bf16 v[144:147],v[120:123],v[104:107],v[144:147]
v_mfma_f32_16x16x32_bf16 v[144:147],v[124:127],v[108:111],v[144:147]
s_nop 7
v_max3_f32 v170,v170,v144,v145
v_max3_f32 v170,v170,v146,v147
s_add_u32 s91,s91,4096
s_sub_u32 s90,s90,1
s_cmp_lg_u32 s90,0
s_cbranch_scc1 L_s1_%=
v_mov_b32 v171,v170
s_nop 1
v_permlane32_swap_b32 v170,v171
v_max_f32 v170,v170,v171
v_mov_b32 v171,v170
s_nop 1
v_permlane16_swap_b32 v170,v171
v_max_f32 v170,v170,v171
v_sub_f32 v152,0,v170
v_mov_b32 v156,0
v_sub_f32 v153,0,v170
v_mov_b32 v157,0
v_sub_f32 v154,0,v170
v_mov_b32 v158,0
v_sub_f32 v155,0,v170
v_mov_b32 v159,0
v_mov_b32 v64,0
v_mov_b32 v65,0
v_mov_b32 v66,0
v_mov_b32 v67,0
v_mov_b32 v68,0
v_mov_b32 v69,0
v_mov_b32 v70,0
v_mov_b32 v71,0
v_mov_b32 v72,0
v_mov_b32 v73,0
v_mov_b32 v74,0
v_mov_b32 v75,0
v_mov_b32 v76,0
v_mov_b32 v77,0
v_mov_b32 v78,0
v_mov_b32 v79,0
v_mov_b32 v80,0
v_mov_b32 v81,0
v_mov_b32 v82,0
v_mov_b32 v83,0
v_mov_b32 v84,0
v_mov_b32 v85,0
v_mov_b32 v86,0
v_mov_b32 v87,0
v_mov_b32 v88,0
v_mov_b32 v89,0
v_mov_b32 v90,0
v_mov_b32 v91,0
v_mov_b32 v92,0
v_mov_b32 v93,0
v_mov_b32 v94,0
v_mov_b32 v95,0
s_lshl_b32 s91,s88,18
s_mov_b32 s90,32
v_add_u32 v172,4096,v164
L_s2_%=:
buffer_load_dwordx4 v[112:115],v164,s[20:23],s91 offen
buffer_load_dwordx4 v[116:119],v164,s[20:23],s91 offen offset:64
buffer_load_dwordx4 v[120:123],v164,s[20:23],s91 offen offset:128
buffer_load_dwordx4 v[124:127],v164,s[20:23],s91 offen offset:192
buffer_load_dwordx4 v[128:131],v172,s[20:23],s91 offen
buffer_load_dwordx4 v[132:135],v172,s[20:23],s91 offen offset:64
buffer_load_dwordx4 v[136:139],v172,s[20:23],s91 offen offset:128
buffer_load_dwordx4 v[140:143],v172,s[20:23],s91 offen offset:192
s_waitcnt vmcnt(0)
v_mfma_f32_16x16x32_bf16 v[144:147],v[112:115],v[96:99],v[152:155]
v_mfma_f32_16x16x32_bf16 v[144:147],v[116:119],v[100:103],v[144:147]
v_mfma_f32_16x16x32_bf16 v[144:147],v[120:123],v[104:107],v[144:147]
v_mfma_f32_16x16x32_bf16 v[144:147],v[124:127],v[108:111],v[144:147]
v_mfma_f32_16x16x32_bf16 v[148:151],v[128:131],v[96:99],v[152:155]
v_mfma_f32_16x16x32_bf16 v[148:151],v[132:135],v[100:103],v[148:151]
v_mfma_f32_16x16x32_bf16 v[148:151],v[136:139],v[104:107],v[148:151]
v_mfma_f32_16x16x32_bf16 v[148:151],v[140:143],v[108:111],v[148:151]
s_nop 7
v_exp_f32 v144,v144
v_exp_f32 v145,v145
v_exp_f32 v146,v146
)A"
R"A(v_exp_f32 v147,v147
v_exp_f32 v148,v148
v_exp_f32 v149,v149
v_exp_f32 v150,v150
v_exp_f32 v151,v151
s_nop 0
v_cvt_pk_bf16_f32 v144,v144,v145
v_cvt_pk_bf16_f32 v145,v146,v147
v_cvt_pk_bf16_f32 v146,v148,v149
v_cvt_pk_bf16_f32 v147,v150,v151
buffer_load_ushort v112,v165,s[24:27],s91 offen
buffer_load_ushort v113,v165,s[24:27],s91 offen offset:256
buffer_load_ushort v114,v165,s[24:27],s91 offen offset:512
buffer_load_ushort v115,v165,s[24:27],s91 offen offset:768
buffer_load_ushort v116,v166,s[24:27],s91 offen
buffer_load_ushort v117,v166,s[24:27],s91 offen offset:256
buffer_load_ushort v118,v166,s[24:27],s91 offen offset:512
buffer_load_ushort v119,v166,s[24:27],s91 offen offset:768
buffer_load_ushort v120,v165,s[24:27],s91 offen offset:32
buffer_load_ushort v121,v165,s[24:27],s91 offen offset:288
buffer_load_ushort v122,v165,s[24:27],s91 offen offset:544
buffer_load_ushort v123,v165,s[24:27],s91 offen offset:800
buffer_load_ushort v124,v166,s[24:27],s91 offen offset:32
buffer_load_ushort v125,v166,s[24:27],s91 offen offset:288
buffer_load_ushort v126,v166,s[24:27],s91 offen offset:544
buffer_load_ushort v127,v166,s[24:27],s91 offen offset:800
buffer_load_ushort v128,v165,s[24:27],s91 offen offset:64
buffer_load_ushort v129,v165,s[24:27],s91 offen offset:320
buffer_load_ushort v130,v165,s[24:27],s91 offen offset:576
buffer_load_ushort v131,v165,s[24:27],s91 offen offset:832
buffer_load_ushort v132,v166,s[24:27],s91 offen offset:64
buffer_load_ushort v133,v166,s[24:27],s91 offen offset:320
buffer_load_ushort v134,v166,s[24:27],s91 offen offset:576
buffer_load_ushort v135,v166,s[24:27],s91 offen offset:832
buffer_load_ushort v136,v165,s[24:27],s91 offen offset:96
buffer_load_ushort v137,v165,s[24:27],s91 offen offset:352
buffer_load_ushort v138,v165,s[24:27],s91 offen offset:608
buffer_load_ushort v139,v165,s[24:27],s91 offen offset:864
buffer_load_ushort v140,v166,s[24:27],s91 offen offset:96
buffer_load_ushort v141,v166,s[24:27],s91 offen offset:352
buffer_load_ushort v142,v166,s[24:27],s91 offen offset:608
buffer_load_ushort v143,v166,s[24:27],s91 offen offset:864
s_waitcnt vmcnt(0)
v_lshl_or_b32 v112,v113,16,v112
v_lshl_or_b32 v113,v115,16,v114
v_lshl_or_b32 v114,v117,16,v116
v_lshl_or_b32 v115,v119,16,v118
v_lshl_or_b32 v120,v121,16,v120
v_lshl_or_b32 v121,v123,16,v122
v_lshl_or_b32 v122,v125,16,v124
v_lshl_or_b32 v123,v127,16,v126
v_lshl_or_b32 v128,v129,16,v128
v_lshl_or_b32 v129,v131,16,v130
v_lshl_or_b32 v130,v133,16,v132
v_lshl_or_b32 v131,v135,16,v134
v_lshl_or_b32 v136,v137,16,v136
v_lshl_or_b32 v137,v139,16,v138
v_lshl_or_b32 v138,v141,16,v140
v_lshl_or_b32 v139,v143,16,v142
s_nop 1
v_mfma_f32_16x16x32_bf16 v[64:67],v[112:115],v[144:147],v[64:67]
v_mfma_f32_16x16x32_bf16 v[68:71],v[120:123],v[144:147],v[68:71]
v_mfma_f32_16x16x32_bf16 v[72:75],v[128:131],v[144:147],v[72:75]
v_mfma_f32_16x16x32_bf16 v[76:79],v[136:139],v[144:147],v[76:79]
s_nop 7
buffer_load_ushort v112,v165,s[24:27],s91 offen offset:128
buffer_load_ushort v113,v165,s[24:27],s91 offen offset:384
buffer_load_ushort v114,v165,s[24:27],s91 offen offset:640
buffer_load_ushort v115,v165,s[24:27],s91 offen offset:896
buffer_load_ushort v116,v166,s[24:27],s91 offen offset:128
buffer_load_ushort v117,v166,s[24:27],s91 offen offset:384
buffer_load_ushort v118,v166,s[24:27],s91 offen offset:640
buffer_load_ushort v119,v166,s[24:27],s91 offen offset:896
buffer_load_ushort v120,v165,s[24:27],s91 offen offset:160
buffer_load_ushort v121,v165,s[24:27],s91 offen offset:416
buffer_load_ushort v122,v165,s[24:27],s91 offen offset:672
buffer_load_ushort v123,v165,s[24:27],s91 offen offset:928
buffer_load_ushort v124,v166,s[24:27],s91 offen offset:160
buffer_load_ushort v125,v166,s[24:27],s91 offen offset:416
buffer_load_ushort v126,v166,s[24:27],s91 offen offset:672
buffer_load_ushort v127,v166,s[24:27],s91 offen offset:928
buffer_load_ushort v128,v165,s[24:27],s91 offen offset:192
buffer_load_ushort v129,v165,s[24:27],s91 offen offset:448
buffer_load_ushort v130,v165,s[24:27],s91 offen offset:704
buffer_load_ushort v131,v165,s[24:27],s91 offen offset:960
buffer_load_ushort v132,v166,s[24:27],s91 offen offset:192
buffer_load_ushort v133,v166,s[24:27],s91 offen offset:448
buffer_load_ushort v134,v166,s[24:27],s91 offen offset:704
buffer_load_ushort v135,v166,s[24:27],s91 offen offset:960
buffer_load_ushort v136,v165,s[24:27],s91 offen offset:224
buffer_load_ushort v137,v165,s[24:27],s91 offen offset:480
buffer_load_ushort v138,v165,s[24:27],s91 offen offset:736
buffer_load_ushort v139,v165,s[24:27],s91 offen offset:992
buffer_load_ushort v140,v166,s[24:27],s91 offen offset:224
buffer_load_ushort v141,v166,s[24:27],s91 offen offset:480
buffer_load_ushort v142,v166,s[24:27],s91 offen offset:736
buffer_load_ushort v143,v166,s[24:27],s91 offen offset:992
s_waitcnt vmcnt(0)
v_lshl_or_b32 v112,v113,16,v112
v_lshl_or_b32 v113,v115,16,v114
v_lshl_or_b32 v114,v117,16,v116
v_lshl_or_b32 v115,v119,16,v118
v_lshl_or_b32 v120,v121,16,v120
v_lshl_or_b32 v121,v123,16,v122
v_lshl_or_b32 v122,v125,16,v124
v_lshl_or_b32 v123,v127,16,v126
v_lshl_or_b32 v128,v129,16,v128
v_lshl_or_b32 v129,v131,16,v130
v_lshl_or_b32 v130,v133,16,v132
v_lshl_or_b32 v131,v135,16,v134
v_lshl_or_b32 v136,v137,16,v136
v_lshl_or_b32 v137,v139,16,v138
v_lshl_or_b32 v138,v141,16,v140
v_lshl_or_b32 v139,v143,16,v142
s_nop 1
v_mfma_f32_16x16x32_bf16 v[80:83],v[112:115],v[144:147],v[80:83]
v_mfma_f32_16x16x32_bf16 v[84:87],v[120:123],v[144:147],v[84:87]
v_mfma_f32_16x16x32_bf16 v[88:91],v[128:131],v[144:147],v[88:91]
v_mfma_f32_16x16x32_bf16 v[92:95],v[136:139],v[144:147],v[92:95]
s_nop 7
v_mfma_f32_16x16x32_bf16 v[156:159],v[28:31],v[144:147],v[156:159]
s_nop 7
s_add_u32 s91,s91,8192
s_sub_u32 s90,s90,1
s_cmp_lg_u32 s90,0
s_cbranch_scc1 L_s2_%=
v_rcp_f32 v170,v156
s_nop 0
v_mul_f32 v64,v170,v64
v_mul_f32 v65,v170,v65
v_mul_f32 v66,v170,v66
v_mul_f32 v67,v170,v67
v_mul_f32 v68,v170,v68
v_mul_f32 v69,v170,v69
v_mul_f32 v70,v170,v70
v_mul_f32 v71,v170,v71
v_mul_f32 v72,v170,v72
v_mul_f32 v73,v170,v73
v_mul_f32 v74,v170,v74
v_mul_f32 v75,v170,v75
v_mul_f32 v76,v170,v76
v_mul_f32 v77,v170,v77
v_mul_f32 v78,v170,v78
v_mul_f32 v79,v170,v79
v_mul_f32 v80,v170,v80
v_mul_f32 v81,v170,v81
v_mul_f32 v82,v170,v82
v_mul_f32 v83,v170,v83
v_mul_f32 v84,v170,v84
v_mul_f32 v85,v170,v85
v_mul_f32 v86,v170,v86
v_mul_f32 v87,v170,v87
v_mul_f32 v88,v170,v88
v_mul_f32 v89,v170,v89
v_mul_f32 v90,v170,v90
v_mul_f32 v91,v170,v91
v_mul_f32 v92,v170,v92
v_mul_f32 v93,v170,v93
v_mul_f32 v94,v170,v94
v_mul_f32 v95,v170,v95
v_cvt_pk_bf16_f32 v96,v64,v65
v_cvt_pk_bf16_f32 v97,v66,v67
v_cvt_pk_bf16_f32 v98,v68,v69
v_cvt_pk_bf16_f32 v99,v70,v71
v_cvt_pk_bf16_f32 v100,v72,v73
v_cvt_pk_bf16_f32 v101,v74,v75
v_cvt_pk_bf16_f32 v102,v76,v77
v_cvt_pk_bf16_f32 v103,v78,v79
v_cvt_pk_bf16_f32 v104,v80,v81
v_cvt_pk_bf16_f32 v105,v82,v83
v_cvt_pk_bf16_f32 v106,v84,v85
v_cvt_pk_bf16_f32 v107,v86,v87
v_cvt_pk_bf16_f32 v108,v88,v89
v_cvt_pk_bf16_f32 v109,v90,v91
v_cvt_pk_bf16_f32 v110,v92,v93
v_cvt_pk_bf16_f32 v111,v94,v95
buffer_store_dwordx4 v[96:99],v169,s[36:39],s55 offen sc1
buffer_store_dwordx4 v[100:103],v169,s[36:39],s55 offen offset:1024 sc1
buffer_store_dwordx4 v[104:107],v169,s[36:39],s55 offen offset:2048 sc1
buffer_store_dwordx4 v[108:111],v169,s[36:39],s55 offen offset:3072 sc1
s_waitcnt vmcnt(0)
s_add_u32 s89,s89,1
s_cmp_lg_u32 s89,4
s_cbranch_scc1 L_sjb_%=
s_mov_b32 s98,s88
v_readlane_b32 s88,v173,0
v_readlane_b32 s89,v173,1
v_readlane_b32 s90,v173,2
v_readlane_b32 s91,v173,3
v_readlane_b32 s92,v173,4
v_readlane_b32 s93,v173,5
v_readlane_b32 s94,v173,6
v_readlane_b32 s95,v173,7
s_nop 4
s_mov_b32 s96,0
buffer_load_dwordx4 v[64:67],v6,s[56:59],s96 offen sc1
buffer_load_dwordx4 v[68:71],v6,s[56:59],s96 offen offset:1024 sc1
buffer_load_dwordx4 v[72:75],v6,s[56:59],s96 offen offset:2048 sc1
buffer_load_dwordx4 v[76:79],v6,s[56:59],s96 offen offset:3072 sc1
s_mov_b32 s96,4096
buffer_load_dwordx4 v[80:83],v6,s[56:59],s96 offen sc1
buffer_load_dwordx4 v[84:87],v6,s[56:59],s96 offen offset:1024 sc1
buffer_load_dwordx4 v[88:91],v6,s[56:59],s96 offen offset:2048 sc1
buffer_load_dwordx4 v[92:95],v6,s[56:59],s96 offen offset:3072 sc1
s_mov_b32 s96,8192
buffer_load_dwordx4 v[96:99],v6,s[56:59],s96 offen sc1
buffer_load_dwordx4 v[100:103],v6,s[56:59],s96 offen offset:1024 sc1
buffer_load_dwordx4 v[104:107],v6,s[56:59],s96 offen offset:2048 sc1
buffer_load_dwordx4 v[108:111],v6,s[56:59],s96 offen offset:3072 sc1
s_mov_b32 s96,12288
buffer_load_dwordx4 v[112:115],v6,s[56:59],s96 offen sc1
buffer_load_dwordx4 v[116:119],v6,s[56:59],s96 offen offset:1024 sc1
buffer_load_dwordx4 v[120:123],v6,s[56:59],s96 offen offset:2048 sc1
buffer_load_dwordx4 v[124:127],v6,s[56:59],s96 offen offset:3072 sc1
s_mov_b32 s96,16384
buffer_load_dwordx4 v[128:131],v6,s[56:59],s96 offen sc1
buffer_load_dwordx4 v[132:135],v6,s[56:59],s96 offen offset:1024 sc1
buffer_load_dwordx4 v[136:139],v6,s[56:59],s96 offen offset:2048 sc1
buffer_load_dwordx4 v[140:143],v6,s[56:59],s96 offen offset:3072 sc1
s_mov_b32 s96,20480
buffer_load_dwordx4 v[144:147],v6,s[56:59],s96 offen sc1
buffer_load_dwordx4 v[148:151],v6,s[56:59],s96 offen offset:1024 sc1
buffer_load_dwordx4 v[152:155],v6,s[56:59],s96 offen offset:2048 sc1
buffer_load_dwordx4 v[156:159],v6,s[56:59],s96 offen offset:3072 sc1
s_mov_b32 s96,24576
buffer_load_dwordx4 v[160:163],v6,s[56:59],s96 offen sc1
buffer_load_dwordx4 v[164:167],v6,s[56:59],s96 offen offset:1024 sc1
buffer_load_dwordx4 v[168:171],v6,s[56:59],s96 offen offset:2048 sc1
buffer_load_dwordx4 v[172:175],v6,s[56:59],s96 offen offset:3072 sc1
s_mov_b32 s96,28672
buffer_load_dwordx4 v[176:179],v6,s[56:59],s96 offen sc1
buffer_load_dwordx4 v[180:183],v6,s[56:59],s96 offen offset:1024 sc1
buffer_load_dwordx4 v[184:187],v6,s[56:59],s96 offen offset:2048 sc1
buffer_load_dwordx4 v[188:191],v6,s[56:59],s96 offen offset:3072 sc1
s_waitcnt vmcnt(0)
s_cmp_lg_u32 s98,0
s_cbranch_scc1 L_sr1_%=
buffer_load_dwordx4 v[64:67],v6,s[36:39],s84 offen sc1
buffer_load_dwordx4 v[68:71],v6,s[36:39],s84 offen offset:1024 sc1
buffer_load_dwordx4 v[72:75],v6,s[36:39],s84 offen offset:2048 sc1
buffer_load_dwordx4 v[76:79],v6,s[36:39],s84 offen offset:3072 sc1
buffer_load_dwordx4 v[80:83],v6,s[36:39],s85 offen sc1
buffer_load_dwordx4 v[84:87],v6,s[36:39],s85 offen offset:1024 sc1
buffer_load_dwordx4 v[88:91],v6,s[36:39],s85 offen offset:2048 sc1
buffer_load_dwordx4 v[92:95],v6,s[36:39],s85 offen offset:3072 sc1
buffer_load_dwordx4 v[96:99],v6,s[36:39],s86 offen sc1
buffer_load_dwordx4 v[100:103],v6,s[36:39],s86 offen offset:1024 sc1
buffer_load_dwordx4 v[104:107],v6,s[36:39],s86 offen offset:2048 sc1
buffer_load_dwordx4 v[108:111],v6,s[36:39],s86 offen offset:3072 sc1
buffer_load_dwordx4 v[112:115],v6,s[36:39],s87 offen sc1
buffer_load_dwordx4 v[116:119],v6,s[36:39],s87 offen offset:1024 sc1
buffer_load_dwordx4 v[120:123],v6,s[36:39],s87 offen offset:2048 sc1
buffer_load_dwordx4 v[124:127],v6,s[36:39],s87 offen offset:3072 sc1
s_waitcnt vmcnt(0)
s_nop 7
s_branch L_sret0_%=
L_sr1_%=:
buffer_load_dwordx4 v[132:135],v6,s[36:39],s84 offen sc1
buffer_load_dwordx4 v[140:143],v6,s[36:39],s84 offen offset:1024 sc1
buffer_load_dwordx4 v[148:151],v6,s[36:39],s84 offen offset:2048 sc1
buffer_load_dwordx4 v[156:159],v6,s[36:39],s84 offen offset:3072 sc1
buffer_load_dwordx4 v[164:167],v6,s[36:39],s85 offen sc1
buffer_load_dwordx4 v[172:175],v6,s[36:39],s85 offen offset:1024 sc1
buffer_load_dwordx4 v[180:183],v6,s[36:39],s85 offen offset:2048 sc1
buffer_load_dwordx4 v[188:191],v6,s[36:39],s85 offen offset:3072 sc1
buffer_load_dwordx4 v[196:199],v6,s[36:39],s86 offen sc1
buffer_load_dwordx4 v[204:207],v6,s[36:39],s86 offen offset:1024 sc1
buffer_load_dwordx4 v[212:215],v6,s[36:39],s86 offen offset:2048 sc1
buffer_load_dwordx4 v[220:223],v6,s[36:39],s86 offen offset:3072 sc1
buffer_load_dwordx4 v[228:231],v6,s[36:39],s87 offen sc1
buffer_load_dwordx4 v[236:239],v6,s[36:39],s87 offen offset:1024 sc1
buffer_load_dwordx4 v[244:247],v6,s[36:39],s87 offen offset:2048 sc1
buffer_load_dwordx4 v[252:255],v6,s[36:39],s87 offen offset:3072 sc1
)A"
R"A(s_waitcnt vmcnt(0)
s_nop 7
s_branch L_sret1_%=
L_end_%=:
)A"
    :: "s"(karg), "s"(blk), "s"(wid)
    : "v0", "v1", "v2", "v3", "v4", "v5", "v6", "v7", "v8", "v9", "v10", "v11", "v12", "v13", "v14", "v15", "v16", "v17", "v18", "v19", "v20", "v21", "v22", "v23", "v24", "v25", "v26", "v27", "v28", "v29", "v30", "v31", "v32", "v33", "v34", "v35", "v36", "v37", "v38", "v39", "v40", "v41", "v42", "v43", "v44", "v45", "v46", "v47", "v48", "v49", "v50", "v51", "v52", "v53", "v54", "v55", "v56", "v57", "v58", "v59", "v60", "v61", "v62", "v63", "v64", "v65", "v66", "v67", "v68", "v69", "v70", "v71", "v72", "v73", "v74", "v75", "v76", "v77", "v78", "v79", "v80", "v81", "v82", "v83", "v84", "v85", "v86", "v87", "v88", "v89", "v90", "v91", "v92", "v93", "v94", "v95", "v96", "v97", "v98", "v99", "v100", "v101", "v102", "v103", "v104", "v105", "v106", "v107", "v108", "v109", "v110", "v111", "v112", "v113", "v114", "v115", "v116", "v117", "v118", "v119", "v120", "v121", "v122", "v123", "v124", "v125", "v126", "v127", "v128", "v129", "v130", "v131", "v132", "v133", "v134", "v135", "v136", "v137", "v138", "v139", "v140", "v141", "v142", "v143", "v144", "v145", "v146", "v147", "v148", "v149", "v150", "v151", "v152", "v153", "v154", "v155", "v156", "v157", "v158", "v159", "v160", "v161", "v162", "v163", "v164", "v165", "v166", "v167", "v168", "v169", "v170", "v171", "v172", "v173", "v174", "v175", "v176", "v177", "v178", "v179", "v180", "v181", "v182", "v183", "v184", "v185", "v186", "v187", "v188", "v189", "v190", "v191", "v192", "v193", "v194", "v195", "v196", "v197", "v198", "v199", "v200", "v201", "v202", "v203", "v204", "v205", "v206", "v207", "v208", "v209", "v210", "v211", "v212", "v213", "v214", "v215", "v216", "v217", "v218", "v219", "v220", "v221", "v222", "v223", "v224", "v225", "v226", "v227", "v228", "v229", "v230", "v231", "v232", "v233", "v234", "v235", "v236", "v237", "v238", "v239", "v240", "v241", "v242", "v243", "v244", "v245", "v246", "v247", "v248", "v249", "v250", "v251", "v252", "v253", "v254", "v255", "a0", "a1", "a2", "a3", "a4", "a5", "a6", "a7", "a8", "a9", "a10", "a11", "a12", "a13", "a14", "a15", "a16", "a17", "a18", "a19", "a20", "a21", "a22", "a23", "a24", "a25", "a26", "a27", "a28", "a29", "a30", "a31", "a32", "a33", "a34", "a35", "a36", "a37", "a38", "a39", "a40", "a41", "a42", "a43", "a44", "a45", "a46", "a47", "a48", "a49", "a50", "a51", "a52", "a53", "a54", "a55", "a56", "a57", "a58", "a59", "a60", "a61", "a62", "a63", "a64", "a65", "a66", "a67", "a68", "a69", "a70", "a71", "a72", "a73", "a74", "a75", "a76", "a77", "a78", "a79", "a80", "a81", "a82", "a83", "a84", "a85", "a86", "a87", "a88", "a89", "a90", "a91", "a92", "a93", "a94", "a95", "a96", "a97", "a98", "a99", "a100", "a101", "a102", "a103", "a104", "a105", "a106", "a107", "a108", "a109", "a110", "a111", "a112", "a113", "a114", "a115", "a116", "a117", "a118", "a119", "a120", "a121", "a122", "a123", "a124", "a125", "a126", "a127", "a128", "a129", "a130", "a131", "a132", "a133", "a134", "a135", "a136", "a137", "a138", "a139", "a140", "a141", "a142", "a143", "a144", "a145", "a146", "a147", "a148", "a149", "a150", "a151", "a152", "a153", "a154", "a155", "a156", "a157", "a158", "a159", "a160", "a161", "a162", "a163", "a164", "a165", "a166", "a167", "a168", "a169", "a170", "a171", "a172", "a173", "a174", "a175", "a176", "a177", "a178", "a179", "a180", "a181", "a182", "a183", "a184", "a185", "a186", "a187", "a188", "a189", "a190", "a191", "a192", "a193", "a194", "a195", "a196", "a197", "a198", "a199", "a200", "a201", "a202", "a203", "a204", "a205", "a206", "a207", "a208", "a209", "a210", "a211", "a212", "a213", "a214", "a215", "a216", "a217", "a218", "a219", "a220", "a221", "a222", "a223", "a224", "a225", "a226", "a227", "a228", "a229", "a230", "a231", "a232", "a233", "a234", "a235", "a236", "a237", "a238", "a239", "a240", "a241", "a242", "a243", "a244", "a245", "a246", "a247", "a248", "a249", "a250", "a251", "a252", "a253", "a254", "a255", "s4", "s5", "s6", "s7", "s8", "s9", "s10", "s11", "s12", "s13", "s14", "s15", "s16", "s17", "s18", "s19", "s20", "s21", "s22", "s23", "s24", "s25", "s26", "s27", "s28", "s29", "s30", "s31", "s32", "s33", "s34", "s35", "s36", "s37", "s38", "s39", "s40", "s41", "s42", "s43", "s44", "s45", "s46", "s47", "s48", "s49", "s50", "s51", "s52", "s53", "s54", "s55", "s56", "s57", "s58", "s59", "s60", "s61", "s62", "s63", "s64", "s65", "s66", "s67", "s68", "s69", "s70", "s71", "s72", "s73", "s74", "s75", "s76", "s77", "s78", "s79", "s80", "s81", "s82", "s83", "s84", "s85", "s86", "s87", "s88", "s89", "s90", "s91", "s92", "s93", "s94", "s95", "s96", "s97", "s98", "s99", "s100", "s101", "vcc", "scc", "memory");
#endif
}

extern "C" void kernel_launch(void* const* d_in, const int* in_sizes, int n_in,
                              void* d_out, int out_size, void* d_ws, size_t ws_size,
                              hipStream_t stream) {
  (void)hipFuncSetAttribute((const void*)qattn_kernel, hipFuncAttributeMaxDynamicSharedMemorySize, LDS_ATT);
  qattn_kernel<<<256, 256, LDS_ATT, stream>>>(d_in[0], d_in[1], d_in[2], d_in[3], d_ws, d_in[4], d_out);
}
```

```cpp
#include <hip/hip_runtime.h>
#include <hip/hip_bf16.h>

constexpr int LDS_ATT = 163840;

__global__ __launch_bounds__(256, 1) void qattn_kernel(const void* __restrict__ x, const void* __restrict__ kc, const void* __restrict__ vc,
                                                       const void* __restrict__ wq, void* __restrict__ ws, const void* __restrict__ wo,
                                                       void* __restrict__ out) {
#if defined(__HIP_DEVICE_COMPILE__)
  const auto karg = __builtin_amdgcn_kernarg_segment_ptr();
  const unsigned blk = blockIdx.x;
  const unsigned wid = __builtin_amdgcn_readfirstlane(threadIdx.x >> 6);
  asm volatile(
R"A(s_memtime s[72:73]
s_memrealtime s[74:75]
s_load_dwordx8 s[4:11],%0,0x0
s_load_dwordx4 s[12:15],%0,0x20
s_load_dwordx2 s[16:17],%0,0x30
s_mov_b32 s40,%2
s_mov_b32 s43,%1
s_lshr_b32 s45,s43,3
v_mbcnt_lo_u32_b32 v12,-1,0
v_mbcnt_hi_u32_b32 v12,-1,v12
s_getpc_b64 s[88:89]
L_pc_%=:
s_and_b32 s88,s88,0xffffff80
s_and_b32 s89,s89,0xffff
s_mov_b32 s90,L_fast_end_%=-L_pc_%=
s_mov_b32 s91,0x20000
s_lshl_b32 s44,s45,2
s_add_u32 s44,s44,s40
s_mul_i32 s44,s44,768
v_lshlrev_b32 v67,7,v12
s_mov_b64 exec,63
buffer_load_dword v19,v67,s[88:91],s44 offen
s_mov_b64 exec,-1
s_and_b32 s70,s43,3
s_and_b32 s71,s45,3
s_lshl_b32 s42,s70,2
s_add_u32 s42,s42,s71
s_and_b32 s41,s43,4
s_lshl_b32 s41,s41,1
s_lshr_b32 s46,s45,2
s_add_u32 s41,s41,s46
s_lshl_b32 s56,s42,9
s_mov_b32 s57,0
s_lshl_b32 s59,s40,13
s_add_u32 s58,s59,65536
v_and_b32 v64,3,v12
v_bfe_u32 v65,v12,2,2
v_lshrrev_b32 v66,4,v12
v_lshrrev_b32 v75,3,v12
s_lshl_b32 s44,s40,6
v_add_u32 v75,s44,v75
v_mul_u32_u24 v75,1280,v75
v_and_b32 v76,7,v12
s_add_u32 s84,s70,1
s_and_b32 s84,s84,3
s_mul_i32 s84,s84,20480
s_add_u32 s84,s84,4096
s_add_u32 s85,s70,2
s_and_b32 s85,s85,3
s_mul_i32 s85,s85,20480
s_add_u32 s85,s85,2048
s_add_u32 s86,s70,3
s_and_b32 s86,s86,3
s_mul_i32 s86,s86,20480
s_add_u32 s86,s86,4096
s_add_u32 s87,s70,4
s_and_b32 s87,s87,3
s_mul_i32 s87,s87,20480
s_add_u32 s87,s87,2048
v_or_b32 v77,0,v66
v_xor_b32 v77,v77,v76
v_lshl_add_u32 v77,v77,4,v75
v_add_u32 v48,s84,v77
v_add_u32 v50,s85,v77
v_add_u32 v52,s86,v77
v_add_u32 v54,s87,v77
v_or_b32 v77,4,v66
v_xor_b32 v77,v77,v76
v_lshl_add_u32 v77,v77,4,v75
v_add_u32 v77,9216,v77
v_add_u32 v49,s84,v77
v_add_u32 v51,s85,v77
v_add_u32 v53,s86,v77
v_add_u32 v55,s87,v77
s_lshl_b32 s44,s40,17
v_lshlrev_b32 v75,13,v65
v_lshl_add_u32 v75,v66,6,v75
v_lshl_add_u32 v75,v64,4,v75
v_add_u32 v75,s44,v75
v_add_u32 v56,0,v75
v_add_u32 v57,256,v75
v_add_u32 v58,32768,v75
v_add_u32 v59,33024,v75
v_add_u32 v60,65536,v75
v_add_u32 v61,65792,v75
v_add_u32 v62,98304,v75
v_add_u32 v63,98560,v75
s_mul_i32 s44,s41,307200
s_waitcnt lgkmcnt(0)
s_mov_b64 s[28:29],s[4:5]
s_add_u32 s28,s28,s44
s_addc_u32 s29,s29,0
s_sub_u32 s28,s28,4096
s_subb_u32 s29,s29,0
s_and_b32 s29,s29,0xffff
s_mov_b32 s30,311296
s_mov_b32 s31,0x20000
s_mov_b64 s[32:33],s[10:11]
s_and_b32 s33,s33,0xffff
s_mov_b32 s34,5242880
s_mov_b32 s35,0x20000
s_add_u32 m0,s58,1024
s_nop 0
buffer_load_dwordx4 v57,s[32:35],s56 offen lds
s_add_u32 m0,s58,3072
s_nop 0
buffer_load_dwordx4 v59,s[32:35],s56 offen lds
s_add_u32 m0,s58,5120
s_nop 0
buffer_load_dwordx4 v61,s[32:35],s56 offen lds
s_add_u32 m0,s58,7168
s_nop 0
buffer_load_dwordx4 v63,s[32:35],s56 offen lds
s_add_u32 m0,s59,0
s_nop 0
buffer_load_dwordx4 v48,s[28:31],s57 offen lds
buffer_load_dwordx4 v49,s[28:31],s57 offen offset:1024 lds
buffer_load_dwordx4 v50,s[28:31],s57 offen offset:2048 lds
buffer_load_dwordx4 v51,s[28:31],s57 offen offset:3072 lds
s_add_u32 m0,s59,4096
s_nop 0
buffer_load_dwordx4 v52,s[28:31],s57 offen lds
buffer_load_dwordx4 v53,s[28:31],s57 offen offset:1024 lds
buffer_load_dwordx4 v54,s[28:31],s57 offen offset:2048 lds
buffer_load_dwordx4 v55,s[28:31],s57 offen offset:3072 lds
s_add_u32 s57,s57,128
s_xor_b32 s59,s59,131072
s_add_u32 m0,s58,0
s_nop 0
buffer_load_dwordx4 v56,s[32:35],s56 offen lds
s_add_u32 m0,s58,2048
s_nop 0
buffer_load_dwordx4 v58,s[32:35],s56 offen lds
s_add_u32 m0,s58,4096
s_nop 0
buffer_load_dwordx4 v60,s[32:35],s56 offen lds
s_add_u32 m0,s58,6144
s_nop 0
buffer_load_dwordx4 v62,s[32:35],s56 offen lds
s_add_u32 s56,s56,524288
s_add_u32 s58,s58,32768
s_cmp_ge_u32 s58,131072
s_cselect_b32 s62,98304,0
s_sub_u32 s58,s58,s62
s_add_u32 m0,s58,0
s_nop 0
buffer_load_dwordx4 v56,s[32:35],s56 offen lds
s_add_u32 m0,s58,1024
s_nop 0
buffer_load_dwordx4 v57,s[32:35],s56 offen lds
s_add_u32 m0,s58,2048
s_nop 0
buffer_load_dwordx4 v58,s[32:35],s56 offen lds
s_add_u32 m0,s58,3072
s_nop 0
buffer_load_dwordx4 v59,s[32:35],s56 offen lds
s_add_u32 m0,s58,4096
s_nop 0
buffer_load_dwordx4 v60,s[32:35],s56 offen lds
s_add_u32 m0,s58,5120
s_nop 0
buffer_load_dwordx4 v61,s[32:35],s56 offen lds
s_add_u32 m0,s58,6144
s_nop 0
buffer_load_dwordx4 v62,s[32:35],s56 offen lds
s_add_u32 m0,s58,7168
s_nop 0
buffer_load_dwordx4 v63,s[32:35],s56 offen lds
s_add_u32 s56,s56,524288
s_add_u32 s58,s58,32768
s_cmp_ge_u32 s58,131072
s_cselect_b32 s62,98304,0
s_sub_u32 s58,s58,s62
s_mov_b32 s60,65536
v_lshlrev_b32 v6,4,v12
v_lshlrev_b32 v72,12,v66
v_lshl_add_u32 v72,v65,6,v72
v_lshl_add_u32 v23,v64,4,v72
v_add_u32 v22,65536,v23
v_and_b32 v74,15,v12
v_lshrrev_b32 v76,1,v74
v_lshlrev_b32 v75,7,v74
s_lshl_b32 s44,s40,13
v_add_u32 v75,s44,v75
v_or_b32 v77,0,v66
v_xor_b32 v77,v77,v76
v_lshl_add_u32 v20,v77,4,v75
v_or_b32 v77,4,v66
v_xor_b32 v77,v77,v76
v_lshl_add_u32 v21,v77,4,v75
s_waitcnt vmcnt(12)
s_nop 0
s_memtime s[64:65]
s_memrealtime s[66:67]
s_bfe_u32 s68,%1,327683
s_waitcnt lgkmcnt(0)
s_sub_u32 s64,s64,s72
s_sub_u32 s66,s66,s74
s_mul_i32 s64,s64,10
s_mul_i32 s66,s66,215
s_cmp_lt_u32 s64,s66
s_cbranch_scc1 L_se1_%=
L_sg1_%=:
s_cmp_eq_u32 s68,0
s_cbranch_scc1 L_se1_%=
s_sleep 4
s_sub_u32 s68,s68,1
s_branch L_sg1_%=
L_se1_%=:
s_barrier
ds_read_b128 a[192:195],v20
ds_read_b64_tr_b16 a[128:129],v22 offset:1024
ds_read_b64_tr_b16 a[130:131],v22 offset:3072
ds_read_b128 a[196:199],v21
ds_read_b64_tr_b16 a[132:133],v22 offset:17408
ds_read_b64_tr_b16 a[134:135],v22 offset:19456
ds_read_b128 a[204:207],v21 offset:2048
ds_read_b128 a[200:203],v20 offset:2048
ds_read_b128 a[208:211],v20 offset:4096
ds_read_b128 a[212:215],v21 offset:4096
ds_read_b128 a[220:223],v21 offset:6144
ds_read_b128 a[216:219],v20 offset:6144
ds_read_b64_tr_b16 a[136:137],v22 offset:1032
ds_read_b64_tr_b16 a[138:139],v22 offset:3080
s_waitcnt lgkmcnt(11)
v_mfma_f32_16x16x32_bf16 v[128:131],a[128:131],a[192:195],0
s_add_u32 m0,s59,0
ds_read_b64_tr_b16 a[140:141],v22 offset:17416
ds_read_b64_tr_b16 a[142:143],v22 offset:19464
s_waitcnt lgkmcnt(10)
v_mfma_f32_16x16x32_bf16 v[128:131],a[132:135],a[196:199],v[128:131]
s_waitcnt lgkmcnt(9)
v_mfma_f32_16x16x32_bf16 v[136:139],a[132:135],a[204:207],0
s_waitcnt lgkmcnt(8)
v_mfma_f32_16x16x32_bf16 v[136:139],a[128:131],a[200:203],v[136:139]
s_waitcnt lgkmcnt(7)
v_mfma_f32_16x16x32_bf16 v[144:147],a[128:131],a[208:211],0
s_waitcnt lgkmcnt(6)
buffer_load_dwordx4 v48,s[28:31],s57 offen lds
v_mfma_f32_16x16x32_bf16 v[144:147],a[132:135],a[212:215],v[144:147]
s_waitcnt lgkmcnt(5)
v_mfma_f32_16x16x32_bf16 v[152:155],a[132:135],a[220:223],0
s_waitcnt lgkmcnt(4)
v_mfma_f32_16x16x32_bf16 v[152:155],a[128:131],a[216:219],v[152:155]
s_waitcnt lgkmcnt(2)
ds_read_b64_tr_b16 a[144:145],v22 offset:1280
ds_read_b64_tr_b16 a[146:147],v22 offset:3328
v_mfma_f32_16x16x32_bf16 v[132:135],a[136:139],a[192:195],0
s_waitcnt lgkmcnt(2)
ds_read_b64_tr_b16 a[148:149],v22 offset:17664
ds_read_b64_tr_b16 a[150:151],v22 offset:19712
buffer_load_dwordx4 v49,s[28:31],s57 offen offset:1024 lds
v_mfma_f32_16x16x32_bf16 v[132:135],a[140:143],a[196:199],v[132:135]
v_mfma_f32_16x16x32_bf16 v[140:143],a[140:143],a[204:207],0
v_mfma_f32_16x16x32_bf16 v[140:143],a[136:139],a[200:203],v[140:143]
v_mfma_f32_16x16x32_bf16 v[148:151],a[136:139],a[208:211],0
buffer_load_dwordx4 v50,s[28:31],s57 offen offset:2048 lds
v_mfma_f32_16x16x32_bf16 v[148:151],a[140:143],a[212:215],v[148:151]
v_mfma_f32_16x16x32_bf16 v[156:159],a[140:143],a[220:223],0
v_mfma_f32_16x16x32_bf16 v[156:159],a[136:139],a[216:219],v[156:159]
s_waitcnt lgkmcnt(2)
ds_read_b64_tr_b16 a[152:153],v22 offset:1288
ds_read_b64_tr_b16 a[154:155],v22 offset:3336
v_mfma_f32_16x16x32_bf16 v[160:163],a[144:147],a[192:195],0
s_waitcnt lgkmcnt(2)
buffer_load_dwordx4 v51,s[28:31],s57 offen offset:3072 lds
ds_read_b64_tr_b16 a[156:157],v22 offset:17672
ds_read_b64_tr_b16 a[158:159],v22 offset:19720
v_mfma_f32_16x16x32_bf16 v[160:163],a[148:151],a[196:199],v[160:163]
s_add_u32 m0,s59,4096
v_mfma_f32_16x16x32_bf16 v[168:171],a[148:151],a[204:207],0
v_mfma_f32_16x16x32_bf16 v[168:171],a[144:147],a[200:203],v[168:171]
v_mfma_f32_16x16x32_bf16 v[176:179],a[144:147],a[208:211],0
buffer_load_dwordx4 v52,s[28:31],s57 offen lds
v_mfma_f32_16x16x32_bf16 v[176:179],a[148:151],a[212:215],v[176:179]
v_mfma_f32_16x16x32_bf16 v[184:187],a[148:151],a[220:223],0
v_mfma_f32_16x16x32_bf16 v[184:187],a[144:147],a[216:219],v[184:187]
s_waitcnt lgkmcnt(2)
v_mfma_f32_16x16x32_bf16 v[164:167],a[152:155],a[192:195],0
s_waitcnt lgkmcnt(0)
buffer_load_dwordx4 v53,s[28:31],s57 offen offset:1024 lds
ds_read_b64_tr_b16 a[160:161],v22 offset:1536
ds_read_b64_tr_b16 a[162:163],v22 offset:3584
v_mfma_f32_16x16x32_bf16 v[164:167],a[156:159],a[196:199],v[164:167]
ds_read_b64_tr_b16 a[164:165],v22 offset:17920
ds_read_b64_tr_b16 a[166:167],v22 offset:19968
v_mfma_f32_16x16x32_bf16 v[172:175],a[156:159],a[204:207],0
v_mfma_f32_16x16x32_bf16 v[172:175],a[152:155],a[200:203],v[172:175]
v_mfma_f32_16x16x32_bf16 v[180:183],a[152:155],a[208:211],0
buffer_load_dwordx4 v54,s[28:31],s57 offen offset:2048 lds
v_mfma_f32_16x16x32_bf16 v[180:183],a[156:159],a[212:215],v[180:183]
v_mfma_f32_16x16x32_bf16 v[188:191],a[156:159],a[220:223],0
s_waitcnt vmcnt(15)
v_mfma_f32_16x16x32_bf16 v[188:191],a[152:155],a[216:219],v[188:191]
s_barrier
buffer_load_dwordx4 v55,s[28:31],s57 offen offset:3072 lds
ds_read_b64_tr_b16 a[168:169],v22 offset:1544
ds_read_b64_tr_b16 a[170:171],v22 offset:3592
s_waitcnt lgkmcnt(4)
v_mfma_f32_16x16x32_bf16 v[192:195],a[160:163],a[192:195],0
s_add_u32 m0,s58,0
ds_read_b64_tr_b16 a[172:173],v22 offset:17928
ds_read_b64_tr_b16 a[174:175],v22 offset:19976
s_waitcnt lgkmcnt(4)
v_mfma_f32_16x16x32_bf16 v[192:195],a[164:167],a[196:199],v[192:195]
v_mfma_f32_16x16x32_bf16 v[200:203],a[164:167],a[204:207],0
s_lshl_b32 s44,s41,13
s_add_u32 s44,s44,104857600
s_mov_b64 s[64:65],s[12:13]
v_mfma_f32_16x16x32_bf16 v[200:203],a[160:163],a[200:203],v[200:203]
s_mov_b32 s66,8192
buffer_load_dwordx4 v56,s[32:35],s56 offen lds
v_mfma_f32_16x16x32_bf16 v[208:211],a[160:163],a[208:211],0
s_add_u32 m0,s58,1024
s_add_u32 s64,s64,s44
s_addc_u32 s65,s65,0
v_mfma_f32_16x16x32_bf16 v[208:211],a[164:167],a[212:215],v[208:211]
s_and_b32 s65,s65,0xffff
s_mov_b32 s67,0x20000
v_mov_b32 v14,4096
v_mfma_f32_16x16x32_bf16 v[216:219],a[164:167],a[220:223],0
s_mov_b64 s[72:73],s[14:15]
s_and_b32 s73,s73,0xffff
ds_read_b64_tr_b16 a[176:177],v22 offset:1792
v_mfma_f32_16x16x32_bf16 v[216:219],a[160:163],a[216:219],v[216:219]
s_mov_b32 s74,5242880
s_mov_b32 s75,0x20000
ds_read_b64_tr_b16 a[178:179],v22 offset:3840
s_waitcnt lgkmcnt(4)
v_mfma_f32_16x16x32_bf16 v[196:199],a[168:171],a[192:195],0
s_waitcnt lgkmcnt(2)
buffer_load_dwordx4 v57,s[32:35],s56 offen lds
ds_read_b64_tr_b16 a[180:181],v22 offset:18176
ds_read_b64_tr_b16 a[182:183],v22 offset:20224
v_mfma_f32_16x16x32_bf16 v[196:199],a[172:175],a[196:199],v[196:199]
s_add_u32 m0,s58,2048
v_lshlrev_b32 v15,3,v6
v_add_u32 v16,128,v15
v_mfma_f32_16x16x32_bf16 v[204:207],a[172:175],a[204:207],0
s_lshl_b32 s83,%1,2
ds_read_b64_tr_b16 a[184:185],v22 offset:1800
v_lshrrev_b32 v16,10,v16
v_mfma_f32_16x16x32_bf16 v[204:207],a[168:171],a[200:203],v[204:207]
s_add_u32 s83,s83,s40
ds_read_b64_tr_b16 a[186:187],v22 offset:3848
v_lshl_add_u32 v15,v16,24,v15
v_mfma_f32_16x16x32_bf16 v[212:215],a[168:171],a[208:211],0
s_mul_i32 s83,s83,5120
buffer_load_dwordx4 v58,s[32:35],s56 offen lds
v_mfma_f32_16x16x32_bf16 v[212:215],a[172:175],a[212:215],v[212:215]
s_add_u32 m0,s58,3072
s_lshl_b32 s43,s41,5
ds_read_b64_tr_b16 a[188:189],v22 offset:18184
v_mfma_f32_16x16x32_bf16 v[220:223],a[172:175],a[220:223],0
s_lshl_b32 s44,s42,1
s_add_u32 s43,s43,s44
ds_read_b64_tr_b16 a[190:191],v22 offset:20232
v_mfma_f32_16x16x32_bf16 v[220:223],a[168:171],a[216:219],v[220:223]
s_lshl_b32 s44,s43,18
s_mov_b64 s[20:21],s[6:7]
ds_read_b64_tr_b16 a[128:129],v22
s_waitcnt lgkmcnt(7)
v_mfma_f32_16x16x32_bf16 v[224:227],a[176:179],a[192:195],0
s_mov_b32 s22,524288
buffer_load_dwordx4 v59,s[32:35],s56 offen lds
s_waitcnt lgkmcnt(5)
v_mfma_f32_16x16x32_bf16 v[224:227],a[180:183],a[196:199],v[224:227]
s_add_u32 m0,s58,4096
s_mov_b32 s23,0x20000
)A"
R"A(ds_read_b64_tr_b16 a[130:131],v22 offset:2048
v_mfma_f32_16x16x32_bf16 v[232:235],a[180:183],a[204:207],0
s_add_u32 s20,s20,s44
s_addc_u32 s21,s21,0
ds_read_b64_tr_b16 a[132:133],v22 offset:16384
v_mfma_f32_16x16x32_bf16 v[232:235],a[176:179],a[200:203],v[232:235]
s_and_b32 s21,s21,0xffff
s_mov_b64 s[24:25],s[8:9]
ds_read_b64_tr_b16 a[134:135],v22 offset:18432
v_mfma_f32_16x16x32_bf16 v[240:243],a[176:179],a[208:211],0
s_mov_b32 s26,524288
buffer_load_dwordx4 v60,s[32:35],s56 offen lds
v_mfma_f32_16x16x32_bf16 v[240:243],a[180:183],a[212:215],v[240:243]
s_add_u32 s24,s24,s44
s_addc_u32 s25,s25,0
ds_read_b64_tr_b16 a[136:137],v22 offset:8
v_mfma_f32_16x16x32_bf16 v[248:251],a[180:183],a[220:223],0
s_add_u32 m0,s58,5120
s_and_b32 s25,s25,0xffff
ds_read_b64_tr_b16 a[138:139],v22 offset:2056
v_mfma_f32_16x16x32_bf16 v[248:251],a[176:179],a[216:219],v[248:251]
s_mov_b32 s27,0x20000
s_lshl_b32 s44,s43,16
ds_read_b64_tr_b16 a[140:141],v22 offset:16392
s_waitcnt lgkmcnt(6)
v_mfma_f32_16x16x32_bf16 v[228:231],a[184:187],a[192:195],0
s_xor_b32 s59,s59,131072
s_mov_b64 s[36:37],s[12:13]
ds_read_b64_tr_b16 a[142:143],v22 offset:18440
v_mfma_f32_16x16x32_bf16 v[228:231],a[188:191],a[196:199],v[228:231]
s_add_u32 s57,s57,128
buffer_load_dwordx4 v61,s[32:35],s56 offen lds
v_mfma_f32_16x16x32_bf16 v[236:239],a[188:191],a[204:207],0
s_add_u32 s36,s36,s44
s_addc_u32 s37,s37,0
ds_read_b64_tr_b16 a[144:145],v22 offset:256
v_mfma_f32_16x16x32_bf16 v[236:239],a[184:187],a[200:203],v[236:239]
s_add_u32 m0,s58,6144
s_and_b32 s37,s37,0xffff
ds_read_b64_tr_b16 a[146:147],v22 offset:2304
v_mfma_f32_16x16x32_bf16 v[244:247],a[184:187],a[208:211],0
s_mov_b32 s38,131072
s_mov_b32 s39,0x20000
ds_read_b64_tr_b16 a[148:149],v22 offset:16640
v_mfma_f32_16x16x32_bf16 v[244:247],a[188:191],a[212:215],v[244:247]
s_lshl_b32 s51,s40,12
s_add_u32 s52,s51,16384
ds_read_b64_tr_b16 a[150:151],v22 offset:18688
v_mfma_f32_16x16x32_bf16 v[252:255],a[188:191],a[220:223],0
s_mul_i32 s47,s41,5
buffer_load_dwordx4 v62,s[32:35],s56 offen lds
v_mfma_f32_16x16x32_bf16 v[252:255],a[184:187],a[216:219],v[252:255]
s_add_u32 s47,s47,s42
s_and_b32 s47,s47,15
ds_read_b64_tr_b16 a[152:153],v22 offset:264
s_waitcnt lgkmcnt(6)
v_mfma_f32_16x16x32_bf16 a[0:3],a[128:131],a[192:195],0
s_add_u32 m0,s58,7168
s_lshl_b32 s47,s47,14
ds_read_b64_tr_b16 a[154:155],v22 offset:2312
v_mfma_f32_16x16x32_bf16 a[0:3],a[132:135],a[196:199],a[0:3]
s_mov_b32 s54,0
s_mov_b32 s50,98304
ds_read_b64_tr_b16 a[156:157],v22 offset:16648
v_mfma_f32_16x16x32_bf16 a[32:35],a[132:135],a[204:207],0
s_mov_b32 s49,0
s_lshl_b32 s55,s40,14
ds_read_b64_tr_b16 a[158:159],v22 offset:18696
v_mfma_f32_16x16x32_bf16 a[32:35],a[128:131],a[200:203],a[32:35]
s_mov_b32 s61,0x3e0293ee
buffer_load_dwordx4 v63,s[32:35],s56 offen lds
v_mfma_f32_16x16x32_bf16 a[64:67],a[128:131],a[208:211],0
s_mov_b32 s63,0x5d800000
s_lshl_b32 s44,s40,2
ds_read_b64_tr_b16 a[160:161],v22 offset:512
v_mfma_f32_16x16x32_bf16 a[64:67],a[132:135],a[212:215],a[64:67]
s_add_u32 m0,s59,0
s_add_u32 s44,s44,s70
ds_read_b64_tr_b16 a[162:163],v22 offset:2560
v_mfma_f32_16x16x32_bf16 a[96:99],a[132:135],a[220:223],0
s_mov_b32 s46,0xff800000
ds_read_b64_tr_b16 a[164:165],v22 offset:16896
v_lshlrev_b32 v70,8,v65
v_mfma_f32_16x16x32_bf16 a[96:99],a[128:131],a[216:219],a[96:99]
s_cmp_eq_u32 s44,14
s_cselect_b32 s18,0x7149f2ca,s46
ds_read_b64_tr_b16 a[166:167],v22 offset:18944
v_mfma_f32_16x16x32_bf16 a[4:7],a[136:139],a[192:195],0
s_add_u32 s60,s60,32768
buffer_load_dwordx4 v48,s[28:31],s57 offen lds
s_waitcnt lgkmcnt(6)
v_mfma_f32_16x16x32_bf16 a[4:7],a[140:143],a[196:199],a[4:7]
s_cmp_eq_u32 s44,13
s_cselect_b32 s19,0x7149f2ca,s46
ds_read_b64_tr_b16 a[168:169],v22 offset:520
v_mfma_f32_16x16x32_bf16 a[36:39],a[140:143],a[204:207],0
s_cmp_eq_u32 s44,12
s_cselect_b32 s100,0x7149f2ca,s46
ds_read_b64_tr_b16 a[170:171],v22 offset:2568
v_mfma_f32_16x16x32_bf16 a[36:39],a[136:139],a[200:203],a[36:39]
s_cmp_eq_u32 s44,15
s_cselect_b32 s101,0x7149f2ca,s46
ds_read_b64_tr_b16 a[172:173],v22 offset:16904
v_mfma_f32_16x16x32_bf16 a[68:71],a[136:139],a[208:211],0
s_add_u32 s58,s58,32768
ds_read_b64_tr_b16 a[174:175],v22 offset:18952
v_lshl_add_u32 v70,v66,6,v70
v_mfma_f32_16x16x32_bf16 a[68:71],a[140:143],a[212:215],a[68:71]
s_mul_i32 s81,s70,1310720
buffer_load_dwordx4 v49,s[28:31],s57 offen offset:1024 lds
v_mfma_f32_16x16x32_bf16 a[100:103],a[140:143],a[220:223],0
s_mul_i32 s44,s71,320
ds_read_b64_tr_b16 a[176:177],v22 offset:768
v_add_u32 v70,s51,v70
v_mfma_f32_16x16x32_bf16 a[100:103],a[136:139],a[216:219],a[100:103]
s_add_u32 s81,s81,s44
ds_read_b64_tr_b16 a[178:179],v22 offset:2816
v_lshl_add_u32 v4,v64,4,v70
v_mfma_f32_16x16x32_bf16 a[8:11],a[144:147],a[192:195],0
s_add_u32 s56,s56,524288
ds_read_b64_tr_b16 a[180:181],v22 offset:17152
v_xor_b32 v71,2,v64
v_mfma_f32_16x16x32_bf16 a[8:11],a[148:151],a[196:199],a[8:11]
s_mul_i32 s82,s40,10240
ds_read_b64_tr_b16 a[182:183],v22 offset:19200
v_lshl_add_u32 v5,v71,4,v70
v_mfma_f32_16x16x32_bf16 a[40:43],a[148:151],a[204:207],0
s_mul_i32 s44,s40,40960
buffer_load_dwordx4 v50,s[28:31],s57 offen offset:2048 lds
v_mfma_f32_16x16x32_bf16 a[40:43],a[144:147],a[200:203],a[40:43]
s_lshr_b32 s92,%1,3
ds_read_b64_tr_b16 a[184:185],v22 offset:776
v_lshlrev_b32 v72,10,v65
v_mfma_f32_16x16x32_bf16 a[72:75],a[144:147],a[208:211],0
s_lshl_b32 s92,s92,2
ds_read_b64_tr_b16 a[186:187],v22 offset:2824
v_lshl_add_u32 v72,v64,6,v72
v_mfma_f32_16x16x32_bf16 a[72:75],a[148:151],a[212:215],a[72:75]
s_add_u32 s92,s92,s40
ds_read_b64_tr_b16 a[188:189],v22 offset:17160
v_and_b32 v73,2,v65
v_mfma_f32_16x16x32_bf16 a[104:107],a[148:151],a[220:223],0
s_lshl_b32 s92,s92,7
ds_read_b64_tr_b16 a[190:191],v22 offset:19208
v_xor_b32 v73,v73,v66
v_mfma_f32_16x16x32_bf16 a[104:107],a[144:147],a[216:219],a[104:107]
s_add_u32 s93,s92,L_tailseg_%=-L_pc_%=
buffer_load_dwordx4 v51,s[28:31],s57 offen offset:3072 lds
v_mfma_f32_16x16x32_bf16 a[12:15],a[152:155],a[192:195],0
s_waitcnt lgkmcnt(5)
v_lshl_add_u32 v72,v73,4,v72
v_add_u32 v1,98304,v72
v_mfma_f32_16x16x32_bf16 a[12:15],a[156:159],a[196:199],a[12:15]
s_add_u32 m0,s59,4096
v_lshlrev_b32 v72,10,v66
v_lshl_add_u32 v72,v65,6,v72
v_mfma_f32_16x16x32_bf16 a[44:47],a[156:159],a[204:207],0
s_xor_b32 s59,s59,131072
v_lshl_add_u32 v72,v64,3,v72
v_add_u32 v72,114688,v72
v_mfma_f32_16x16x32_bf16 a[44:47],a[152:155],a[200:203],a[44:47]
s_cmp_eq_u32 s60,131072
s_cselect_b32 s60,32768,s60
v_and_b32 v73,1,v66
v_mfma_f32_16x16x32_bf16 a[76:79],a[152:155],a[208:211],0
buffer_load_dwordx4 v52,s[28:31],s57 offen lds
v_mfma_f32_16x16x32_bf16 a[76:79],a[156:159],a[212:215],a[76:79]
v_add_u32 v22,s60,v23
v_xor_b32 v20,131072,v20
v_mfma_f32_16x16x32_bf16 a[108:111],a[156:159],a[220:223],0
s_add_u32 s60,s60,32768
v_xor_b32 v21,131072,v21
v_lshl_add_u32 v2,v73,5,v72
v_mfma_f32_16x16x32_bf16 a[108:111],a[152:155],a[216:219],a[108:111]
s_waitcnt vmcnt(13) lgkmcnt(0)
v_xor_b32 v73,1,v73
v_lshl_add_u32 v3,v73,5,v72
v_mfma_f32_16x16x32_bf16 a[16:19],a[160:163],a[192:195],0
s_barrier
buffer_load_dwordx4 v53,s[28:31],s57 offen offset:1024 lds
v_mfma_f32_16x16x32_bf16 a[16:19],a[164:167],a[196:199],a[16:19]
ds_read_b128 a[224:227],v20
v_mov_b32 v28,0x3f803f80
v_mfma_f32_16x16x32_bf16 a[48:51],a[164:167],a[204:207],0
ds_read_b64_tr_b16 a[128:129],v22 offset:1024
v_mov_b32 v29,0x3f803f80
v_mfma_f32_16x16x32_bf16 a[48:51],a[160:163],a[200:203],a[48:51]
ds_read_b64_tr_b16 a[130:131],v22 offset:3072
v_mov_b32 v30,0x3f803f80
v_mfma_f32_16x16x32_bf16 a[80:83],a[160:163],a[208:211],0
ds_read_b128 a[228:231],v21
v_mov_b32 v31,0x3f803f80
v_mfma_f32_16x16x32_bf16 a[80:83],a[164:167],a[212:215],a[80:83]
buffer_load_dwordx4 v54,s[28:31],s57 offen offset:2048 lds
v_mfma_f32_16x16x32_bf16 a[112:115],a[164:167],a[220:223],0
s_cmp_ge_u32 s58,131072
s_cselect_b32 s62,98304,0
ds_read_b64_tr_b16 a[132:133],v22 offset:17408
v_mfma_f32_16x16x32_bf16 a[112:115],a[160:163],a[216:219],a[112:115]
s_sub_u32 s58,s58,s62
ds_read_b64_tr_b16 a[134:135],v22 offset:19456
v_add_u32 v70,0,v12
v_mfma_f32_16x16x32_bf16 a[20:23],a[168:171],a[192:195],0
ds_read_b128 a[232:235],v20 offset:2048
v_mul_u32_u24 v71,3277,v70
v_mfma_f32_16x16x32_bf16 a[20:23],a[172:175],a[196:199],a[20:23]
ds_read_b128 a[236:239],v21 offset:2048
v_lshrrev_b32 v71,16,v71
v_mfma_f32_16x16x32_bf16 a[52:55],a[172:175],a[204:207],0
buffer_load_dwordx4 v55,s[28:31],s57 offen offset:3072 lds
v_mfma_f32_16x16x32_bf16 a[52:55],a[168:171],a[200:203],a[52:55]
s_add_u32 s57,s57,128
ds_read_b128 a[240:243],v20 offset:4096
v_mul_u32_u24 v72,20,v71
v_mfma_f32_16x16x32_bf16 a[84:87],a[168:171],a[208:211],0
ds_read_b128 a[244:247],v21 offset:4096
v_sub_u32 v72,v70,v72
v_mfma_f32_16x16x32_bf16 a[84:87],a[172:175],a[212:215],a[84:87]
ds_read_b128 a[248:251],v20 offset:6144
v_bfe_u32 v73,v71,2,1
v_mfma_f32_16x16x32_bf16 a[116:119],a[172:175],a[220:223],0
buffer_load_dword v13,v14,s[64:67],0 offen sc1
v_mfma_f32_16x16x32_bf16 a[116:119],a[168:171],a[216:219],a[116:119]
ds_read_b128 a[252:255],v21 offset:6144
v_lshlrev_b32 v73,1,v73
v_mfma_f32_16x16x32_bf16 a[24:27],a[176:179],a[192:195],0
ds_read_b64_tr_b16 a[136:137],v22 offset:1032
v_xor_b32 v72,v72,v73
v_mfma_f32_16x16x32_bf16 a[24:27],a[180:183],a[196:199],a[24:27]
ds_read_b64_tr_b16 a[138:139],v22 offset:3080
v_mul_u32_u24 v71,1280,v71
v_mfma_f32_16x16x32_bf16 a[56:59],a[180:183],a[204:207],0
ds_read_b64_tr_b16 a[140:141],v22 offset:17416
v_lshl_add_u32 v71,v72,4,v71
v_mfma_f32_16x16x32_bf16 a[56:59],a[176:179],a[200:203],a[56:59]
ds_read_b64_tr_b16 a[142:143],v22 offset:19464
v_add_u32 v71,s44,v71
v_mfma_f32_16x16x32_bf16 a[88:91],a[176:179],a[208:211],0
ds_read_b64_tr_b16 a[144:145],v22 offset:1280
v_subrev_u32 v7,0,v71
v_mfma_f32_16x16x32_bf16 a[88:91],a[180:183],a[212:215],a[88:91]
ds_read_b64_tr_b16 a[146:147],v22 offset:3328
v_add_u32 v70,64,v12
v_mfma_f32_16x16x32_bf16 a[120:123],a[180:183],a[220:223],0
ds_read_b64_tr_b16 a[148:149],v22 offset:17664
v_mul_u32_u24 v71,3277,v70
v_mfma_f32_16x16x32_bf16 a[120:123],a[176:179],a[216:219],a[120:123]
ds_read_b64_tr_b16 a[150:151],v22 offset:19712
v_lshrrev_b32 v71,16,v71
v_mfma_f32_16x16x32_bf16 a[28:31],a[184:187],a[192:195],0
ds_read_b64_tr_b16 a[152:153],v22 offset:1288
v_mul_u32_u24 v72,20,v71
v_mfma_f32_16x16x32_bf16 a[28:31],a[188:191],a[196:199],a[28:31]
ds_read_b64_tr_b16 a[154:155],v22 offset:3336
ds_read_b64_tr_b16 a[156:157],v22 offset:17672
ds_read_b64_tr_b16 a[158:159],v22 offset:19720
v_mfma_f32_16x16x32_bf16 a[60:63],a[188:191],a[204:207],0
v_sub_u32 v72,v70,v72
v_bfe_u32 v73,v71,2,1
v_mfma_f32_16x16x32_bf16 a[60:63],a[184:187],a[200:203],a[60:63]
ds_read_b64_tr_b16 a[160:161],v22 offset:1536
v_lshlrev_b32 v73,1,v73
v_mfma_f32_16x16x32_bf16 a[92:95],a[184:187],a[208:211],0
ds_read_b64_tr_b16 a[162:163],v22 offset:3584
v_xor_b32 v72,v72,v73
v_mfma_f32_16x16x32_bf16 a[92:95],a[188:191],a[212:215],a[92:95]
ds_read_b64_tr_b16 a[164:165],v22 offset:17920
v_mul_u32_u24 v71,1280,v71
v_mfma_f32_16x16x32_bf16 a[124:127],a[188:191],a[220:223],0
s_add_u32 m0,s58,0
ds_read_b64_tr_b16 a[166:167],v22 offset:19968
v_lshl_add_u32 v71,v72,4,v71
v_mfma_f32_16x16x32_bf16 a[124:127],a[184:187],a[216:219],a[124:127]
s_waitcnt lgkmcnt(8)
ds_read_b64_tr_b16 a[168:169],v22 offset:1544
v_add_u32 v71,s44,v71
v_mfma_f32_16x16x32_bf16 v[128:131],a[128:131],a[224:227],v[128:131]
ds_read_b64_tr_b16 a[170:171],v22 offset:3592
v_subrev_u32 v8,1024,v71
v_mfma_f32_16x16x32_bf16 v[128:131],a[132:135],a[228:231],v[128:131]
buffer_load_dwordx4 v56,s[32:35],s56 offen lds
v_mfma_f32_16x16x32_bf16 v[136:139],a[132:135],a[236:239],v[136:139]
ds_read_b64_tr_b16 a[172:173],v22 offset:17928
v_add_u32 v70,128,v12
v_mfma_f32_16x16x32_bf16 v[136:139],a[128:131],a[232:235],v[136:139]
s_add_u32 m0,s58,1024
ds_read_b64_tr_b16 a[174:175],v22 offset:19976
v_mul_u32_u24 v71,3277,v70
v_mfma_f32_16x16x32_bf16 v[144:147],a[128:131],a[240:243],v[144:147]
v_lshrrev_b32 v71,16,v71
v_mul_u32_u24 v72,20,v71
v_mfma_f32_16x16x32_bf16 v[144:147],a[132:135],a[244:247],v[144:147]
ds_read_b64_tr_b16 a[176:177],v22 offset:1792
)A"
R"A(v_sub_u32 v72,v70,v72
v_mfma_f32_16x16x32_bf16 v[152:155],a[132:135],a[252:255],v[152:155]
buffer_load_dwordx4 v57,s[32:35],s56 offen lds
v_mfma_f32_16x16x32_bf16 v[152:155],a[128:131],a[248:251],v[152:155]
ds_read_b64_tr_b16 a[178:179],v22 offset:3840
v_bfe_u32 v73,v71,2,1
v_mfma_f32_16x16x32_bf16 v[132:135],a[136:139],a[224:227],v[132:135]
ds_read_b64_tr_b16 a[180:181],v22 offset:18176
v_lshlrev_b32 v73,1,v73
v_mfma_f32_16x16x32_bf16 v[132:135],a[140:143],a[228:231],v[132:135]
s_add_u32 m0,s58,2048
ds_read_b64_tr_b16 a[182:183],v22 offset:20224
v_xor_b32 v72,v72,v73
v_mfma_f32_16x16x32_bf16 v[140:143],a[140:143],a[236:239],v[140:143]
v_mul_u32_u24 v71,1280,v71
v_lshl_add_u32 v71,v72,4,v71
v_mfma_f32_16x16x32_bf16 v[140:143],a[136:139],a[232:235],v[140:143]
ds_read_b64_tr_b16 a[184:185],v22 offset:1800
v_add_u32 v71,s44,v71
v_mfma_f32_16x16x32_bf16 v[148:151],a[136:139],a[240:243],v[148:151]
buffer_load_dwordx4 v58,s[32:35],s56 offen lds
v_mfma_f32_16x16x32_bf16 v[148:151],a[140:143],a[244:247],v[148:151]
ds_read_b64_tr_b16 a[186:187],v22 offset:3848
v_subrev_u32 v9,2048,v71
v_mfma_f32_16x16x32_bf16 v[156:159],a[140:143],a[252:255],v[156:159]
ds_read_b64_tr_b16 a[188:189],v22 offset:18184
v_add_u32 v70,192,v12
v_mfma_f32_16x16x32_bf16 v[156:159],a[136:139],a[248:251],v[156:159]
s_add_u32 m0,s58,3072
ds_read_b64_tr_b16 a[190:191],v22 offset:20232
v_mul_u32_u24 v71,3277,v70
v_mfma_f32_16x16x32_bf16 v[160:163],a[144:147],a[224:227],v[160:163]
v_lshrrev_b32 v71,16,v71
v_mul_u32_u24 v72,20,v71
v_mfma_f32_16x16x32_bf16 v[160:163],a[148:151],a[228:231],v[160:163]
v_sub_u32 v72,v70,v72
v_bfe_u32 v73,v71,2,1
v_mfma_f32_16x16x32_bf16 v[168:171],a[148:151],a[236:239],v[168:171]
buffer_load_dwordx4 v59,s[32:35],s56 offen lds
v_mfma_f32_16x16x32_bf16 v[168:171],a[144:147],a[232:235],v[168:171]
ds_read_b64_tr_b16 a[128:129],v22
v_lshlrev_b32 v73,1,v73
v_mfma_f32_16x16x32_bf16 v[176:179],a[144:147],a[240:243],v[176:179]
s_add_u32 m0,s58,4096
ds_read_b64_tr_b16 a[130:131],v22 offset:2048
v_xor_b32 v72,v72,v73
v_mfma_f32_16x16x32_bf16 v[176:179],a[148:151],a[244:247],v[176:179]
ds_read_b64_tr_b16 a[132:133],v22 offset:16384
v_mul_u32_u24 v71,1280,v71
v_mfma_f32_16x16x32_bf16 v[184:187],a[148:151],a[252:255],v[184:187]
ds_read_b64_tr_b16 a[134:135],v22 offset:18432
v_lshl_add_u32 v71,v72,4,v71
v_mfma_f32_16x16x32_bf16 v[184:187],a[144:147],a[248:251],v[184:187]
s_waitcnt lgkmcnt(4)
buffer_load_dwordx4 v60,s[32:35],s56 offen lds
v_mfma_f32_16x16x32_bf16 v[164:167],a[152:155],a[224:227],v[164:167]
v_add_u32 v71,s44,v71
v_subrev_u32 v10,3072,v71
v_mfma_f32_16x16x32_bf16 v[164:167],a[156:159],a[228:231],v[164:167]
ds_read_b64_tr_b16 a[136:137],v22 offset:8
v_add_u32 v70,256,v12
v_mfma_f32_16x16x32_bf16 v[172:175],a[156:159],a[236:239],v[172:175]
s_add_u32 m0,s58,5120
ds_read_b64_tr_b16 a[138:139],v22 offset:2056
v_mul_u32_u24 v71,3277,v70
v_mfma_f32_16x16x32_bf16 v[172:175],a[152:155],a[232:235],v[172:175]
ds_read_b64_tr_b16 a[140:141],v22 offset:16392
v_lshrrev_b32 v71,16,v71
v_mfma_f32_16x16x32_bf16 v[180:183],a[152:155],a[240:243],v[180:183]
ds_read_b64_tr_b16 a[142:143],v22 offset:18440
v_mul_u32_u24 v72,20,v71
v_mfma_f32_16x16x32_bf16 v[180:183],a[156:159],a[244:247],v[180:183]
buffer_load_dwordx4 v61,s[32:35],s56 offen lds
v_mfma_f32_16x16x32_bf16 v[188:191],a[156:159],a[252:255],v[188:191]
v_sub_u32 v72,v70,v72
v_bfe_u32 v73,v71,2,1
v_mfma_f32_16x16x32_bf16 v[188:191],a[152:155],a[248:251],v[188:191]
ds_read_b64_tr_b16 a[144:145],v22 offset:256
v_lshlrev_b32 v73,1,v73
v_mfma_f32_16x16x32_bf16 v[192:195],a[160:163],a[224:227],v[192:195]
s_add_u32 m0,s58,6144
ds_read_b64_tr_b16 a[146:147],v22 offset:2304
v_xor_b32 v72,v72,v73
v_mfma_f32_16x16x32_bf16 v[192:195],a[164:167],a[228:231],v[192:195]
ds_read_b64_tr_b16 a[148:149],v22 offset:16640
v_mul_u32_u24 v71,1280,v71
v_mfma_f32_16x16x32_bf16 v[200:203],a[164:167],a[236:239],v[200:203]
ds_read_b64_tr_b16 a[150:151],v22 offset:18688
v_lshl_add_u32 v71,v72,4,v71
v_mfma_f32_16x16x32_bf16 v[200:203],a[160:163],a[232:235],v[200:203]
buffer_load_dwordx4 v62,s[32:35],s56 offen lds
v_mfma_f32_16x16x32_bf16 v[208:211],a[160:163],a[240:243],v[208:211]
v_add_u32 v71,s44,v71
v_subrev_u32 v11,0,v71
v_mfma_f32_16x16x32_bf16 v[208:211],a[164:167],a[244:247],v[208:211]
s_add_u32 m0,s58,7168
ds_read_b64_tr_b16 a[152:153],v22 offset:264
v_lshlrev_b32 v24,10,v6
v_mfma_f32_16x16x32_bf16 v[216:219],a[164:167],a[252:255],v[216:219]
s_cmp_eq_u32 s60,131072
s_cselect_b32 s60,32768,s60
ds_read_b64_tr_b16 a[154:155],v22 offset:2312
v_mfma_f32_16x16x32_bf16 v[216:219],a[160:163],a[248:251],v[216:219]
s_add_u32 s58,s58,32768
ds_read_b64_tr_b16 a[156:157],v22 offset:16648
v_xor_b32 v20,131072,v20
v_mfma_f32_16x16x32_bf16 v[196:199],a[168:171],a[224:227],v[196:199]
buffer_load_dwordx4 v63,s[32:35],s56 offen lds
v_mfma_f32_16x16x32_bf16 v[196:199],a[172:175],a[228:231],v[196:199]
s_add_u32 s56,s56,524288
ds_read_b64_tr_b16 a[158:159],v22 offset:18696
v_xor_b32 v21,131072,v21
v_mfma_f32_16x16x32_bf16 v[204:207],a[172:175],a[236:239],v[204:207]
s_cmp_ge_u32 s58,131072
s_cselect_b32 s62,98304,0
ds_read_b64_tr_b16 a[160:161],v22 offset:512
v_mfma_f32_16x16x32_bf16 v[204:207],a[168:171],a[232:235],v[204:207]
s_add_u32 m0,s59,0
s_sub_u32 s58,s58,s62
ds_read_b64_tr_b16 a[162:163],v22 offset:2560
v_mfma_f32_16x16x32_bf16 v[212:215],a[168:171],a[240:243],v[212:215]
ds_read_b64_tr_b16 a[164:165],v22 offset:16896
v_mfma_f32_16x16x32_bf16 v[212:215],a[172:175],a[244:247],v[212:215]
ds_read_b64_tr_b16 a[166:167],v22 offset:18944
v_mfma_f32_16x16x32_bf16 v[220:223],a[172:175],a[252:255],v[220:223]
buffer_load_dwordx4 v48,s[28:31],s57 offen lds
v_mfma_f32_16x16x32_bf16 v[220:223],a[168:171],a[248:251],v[220:223]
ds_read_b64_tr_b16 a[168:169],v22 offset:520
v_mfma_f32_16x16x32_bf16 v[224:227],a[176:179],a[224:227],v[224:227]
ds_read_b64_tr_b16 a[170:171],v22 offset:2568
v_mfma_f32_16x16x32_bf16 v[224:227],a[180:183],a[228:231],v[224:227]
ds_read_b64_tr_b16 a[172:173],v22 offset:16904
v_mfma_f32_16x16x32_bf16 v[232:235],a[180:183],a[236:239],v[232:235]
ds_read_b64_tr_b16 a[174:175],v22 offset:18952
v_mfma_f32_16x16x32_bf16 v[232:235],a[176:179],a[232:235],v[232:235]
v_mfma_f32_16x16x32_bf16 v[240:243],a[176:179],a[240:243],v[240:243]
buffer_load_dwordx4 v49,s[28:31],s57 offen offset:1024 lds
v_mfma_f32_16x16x32_bf16 v[240:243],a[180:183],a[244:247],v[240:243]
v_mfma_f32_16x16x32_bf16 v[248:251],a[180:183],a[252:255],v[248:251]
ds_read_b64_tr_b16 a[180:181],v22 offset:17152
v_mfma_f32_16x16x32_bf16 v[248:251],a[176:179],a[248:251],v[248:251]
ds_read_b64_tr_b16 a[176:177],v22 offset:768
v_mfma_f32_16x16x32_bf16 v[228:231],a[184:187],a[224:227],v[228:231]
ds_read_b64_tr_b16 a[178:179],v22 offset:2816
v_mfma_f32_16x16x32_bf16 v[228:231],a[188:191],a[228:231],v[228:231]
buffer_load_dwordx4 v50,s[28:31],s57 offen offset:2048 lds
v_mfma_f32_16x16x32_bf16 v[236:239],a[188:191],a[236:239],v[236:239]
ds_read_b64_tr_b16 a[182:183],v22 offset:19200
v_mfma_f32_16x16x32_bf16 v[236:239],a[184:187],a[232:235],v[236:239]
v_mfma_f32_16x16x32_bf16 v[244:247],a[184:187],a[240:243],v[244:247]
v_mfma_f32_16x16x32_bf16 v[244:247],a[188:191],a[244:247],v[244:247]
v_mfma_f32_16x16x32_bf16 v[252:255],a[188:191],a[252:255],v[252:255]
ds_read_b64_tr_b16 a[188:189],v22 offset:17160
v_mfma_f32_16x16x32_bf16 v[252:255],a[184:187],a[248:251],v[252:255]
s_waitcnt lgkmcnt(3)
buffer_load_dwordx4 v51,s[28:31],s57 offen offset:3072 lds
v_mfma_f32_16x16x32_bf16 a[0:3],a[128:131],a[224:227],a[0:3]
ds_read_b64_tr_b16 a[184:185],v22 offset:776
v_mfma_f32_16x16x32_bf16 a[0:3],a[132:135],a[228:231],a[0:3]
ds_read_b64_tr_b16 a[186:187],v22 offset:2824
v_mfma_f32_16x16x32_bf16 a[32:35],a[132:135],a[236:239],a[32:35]
s_add_u32 m0,s59,4096
ds_read_b64_tr_b16 a[190:191],v22 offset:19208
v_add_u32 v22,s60,v23
v_mfma_f32_16x16x32_bf16 a[32:35],a[128:131],a[232:235],a[32:35]
s_xor_b32 s59,s59,131072
v_mfma_f32_16x16x32_bf16 a[64:67],a[128:131],a[240:243],a[64:67]
v_mfma_f32_16x16x32_bf16 a[64:67],a[132:135],a[244:247],a[64:67]
buffer_load_dwordx4 v52,s[28:31],s57 offen lds
v_mfma_f32_16x16x32_bf16 a[96:99],a[132:135],a[252:255],a[96:99]
v_mfma_f32_16x16x32_bf16 a[96:99],a[128:131],a[248:251],a[96:99]
v_mfma_f32_16x16x32_bf16 a[4:7],a[136:139],a[224:227],a[4:7]
v_mfma_f32_16x16x32_bf16 a[4:7],a[140:143],a[228:231],a[4:7]
v_mfma_f32_16x16x32_bf16 a[36:39],a[140:143],a[236:239],a[36:39]
buffer_load_dwordx4 v53,s[28:31],s57 offen offset:1024 lds
v_mfma_f32_16x16x32_bf16 a[36:39],a[136:139],a[232:235],a[36:39]
v_mfma_f32_16x16x32_bf16 a[68:71],a[136:139],a[240:243],a[68:71]
v_mfma_f32_16x16x32_bf16 a[68:71],a[140:143],a[244:247],a[68:71]
v_mfma_f32_16x16x32_bf16 a[100:103],a[140:143],a[252:255],a[100:103]
v_mfma_f32_16x16x32_bf16 a[100:103],a[136:139],a[248:251],a[100:103]
v_mfma_f32_16x16x32_bf16 a[8:11],a[144:147],a[224:227],a[8:11]
buffer_load_dwordx4 v54,s[28:31],s57 offen offset:2048 lds
v_mfma_f32_16x16x32_bf16 a[8:11],a[148:151],a[228:231],a[8:11]
v_mfma_f32_16x16x32_bf16 a[40:43],a[148:151],a[236:239],a[40:43]
v_mfma_f32_16x16x32_bf16 a[40:43],a[144:147],a[232:235],a[40:43]
v_mfma_f32_16x16x32_bf16 a[72:75],a[144:147],a[240:243],a[72:75]
v_mfma_f32_16x16x32_bf16 a[72:75],a[148:151],a[244:247],a[72:75]
v_mfma_f32_16x16x32_bf16 a[104:107],a[148:151],a[252:255],a[104:107]
buffer_load_dwordx4 v55,s[28:31],s57 offen offset:3072 lds
v_mfma_f32_16x16x32_bf16 a[104:107],a[144:147],a[248:251],a[104:107]
s_add_u32 s57,s57,128
v_mfma_f32_16x16x32_bf16 a[12:15],a[152:155],a[224:227],a[12:15]
v_mfma_f32_16x16x32_bf16 a[12:15],a[156:159],a[228:231],a[12:15]
v_mfma_f32_16x16x32_bf16 a[44:47],a[156:159],a[236:239],a[44:47]
v_mfma_f32_16x16x32_bf16 a[44:47],a[152:155],a[232:235],a[44:47]
v_mfma_f32_16x16x32_bf16 a[76:79],a[152:155],a[240:243],a[76:79]
v_mfma_f32_16x16x32_bf16 a[76:79],a[156:159],a[244:247],a[76:79]
v_mfma_f32_16x16x32_bf16 a[108:111],a[156:159],a[252:255],a[108:111]
v_mfma_f32_16x16x32_bf16 a[108:111],a[152:155],a[248:251],a[108:111]
s_waitcnt vmcnt(17) lgkmcnt(0)
v_mfma_f32_16x16x32_bf16 a[16:19],a[160:163],a[224:227],a[16:19]
s_barrier
ds_read_b128 a[192:195],v20
v_mfma_f32_16x16x32_bf16 a[16:19],a[164:167],a[228:231],a[16:19]
ds_read_b128 a[196:199],v21
v_mfma_f32_16x16x32_bf16 a[48:51],a[164:167],a[236:239],a[48:51]
ds_read_b128 a[200:203],v20 offset:2048
v_mfma_f32_16x16x32_bf16 a[48:51],a[160:163],a[232:235],a[48:51]
ds_read_b128 a[204:207],v21 offset:2048
v_mfma_f32_16x16x32_bf16 a[80:83],a[160:163],a[240:243],a[80:83]
ds_read_b128 a[208:211],v20 offset:4096
v_mfma_f32_16x16x32_bf16 a[80:83],a[164:167],a[244:247],a[80:83]
ds_read_b128 a[212:215],v21 offset:4096
v_mfma_f32_16x16x32_bf16 a[112:115],a[164:167],a[252:255],a[112:115]
ds_read_b128 a[216:219],v20 offset:6144
v_mfma_f32_16x16x32_bf16 a[112:115],a[160:163],a[248:251],a[112:115]
ds_read_b128 a[220:223],v21 offset:6144
v_mfma_f32_16x16x32_bf16 a[20:23],a[168:171],a[224:227],a[20:23]
ds_read_b64_tr_b16 a[128:129],v22 offset:1024
v_mfma_f32_16x16x32_bf16 a[20:23],a[172:175],a[228:231],a[20:23]
ds_read_b64_tr_b16 a[130:131],v22 offset:3072
v_mfma_f32_16x16x32_bf16 a[52:55],a[172:175],a[236:239],a[52:55]
ds_read_b64_tr_b16 a[132:133],v22 offset:17408
v_mfma_f32_16x16x32_bf16 a[52:55],a[168:171],a[232:235],a[52:55]
ds_read_b64_tr_b16 a[134:135],v22 offset:19456
v_mfma_f32_16x16x32_bf16 a[84:87],a[168:171],a[240:243],a[84:87]
ds_read_b64_tr_b16 a[136:137],v22 offset:1032
v_mfma_f32_16x16x32_bf16 a[84:87],a[172:175],a[244:247],a[84:87]
ds_read_b64_tr_b16 a[138:139],v22 offset:3080
v_mfma_f32_16x16x32_bf16 a[116:119],a[172:175],a[252:255],a[116:119]
ds_read_b64_tr_b16 a[140:141],v22 offset:17416
v_mfma_f32_16x16x32_bf16 a[116:119],a[168:171],a[248:251],a[116:119]
ds_read_b64_tr_b16 a[142:143],v22 offset:19464
v_mfma_f32_16x16x32_bf16 a[24:27],a[176:179],a[224:227],a[24:27]
ds_read_b64_tr_b16 a[144:145],v22 offset:1280
v_mfma_f32_16x16x32_bf16 a[24:27],a[180:183],a[228:231],a[24:27]
ds_read_b64_tr_b16 a[146:147],v22 offset:3328
v_mfma_f32_16x16x32_bf16 a[56:59],a[180:183],a[236:239],a[56:59]
)A"
R"A(ds_read_b64_tr_b16 a[148:149],v22 offset:17664
v_mfma_f32_16x16x32_bf16 a[56:59],a[176:179],a[232:235],a[56:59]
ds_read_b64_tr_b16 a[150:151],v22 offset:19712
v_mfma_f32_16x16x32_bf16 a[88:91],a[176:179],a[240:243],a[88:91]
ds_read_b64_tr_b16 a[152:153],v22 offset:1288
v_mfma_f32_16x16x32_bf16 a[88:91],a[180:183],a[244:247],a[88:91]
ds_read_b64_tr_b16 a[154:155],v22 offset:3336
v_mfma_f32_16x16x32_bf16 a[120:123],a[180:183],a[252:255],a[120:123]
ds_read_b64_tr_b16 a[156:157],v22 offset:17672
v_mfma_f32_16x16x32_bf16 a[120:123],a[176:179],a[248:251],a[120:123]
ds_read_b64_tr_b16 a[158:159],v22 offset:19720
v_mfma_f32_16x16x32_bf16 a[28:31],a[184:187],a[224:227],a[28:31]
v_mfma_f32_16x16x32_bf16 a[28:31],a[188:191],a[228:231],a[28:31]
v_mfma_f32_16x16x32_bf16 a[60:63],a[188:191],a[236:239],a[60:63]
v_mfma_f32_16x16x32_bf16 a[60:63],a[184:187],a[232:235],a[60:63]
v_mfma_f32_16x16x32_bf16 a[92:95],a[184:187],a[240:243],a[92:95]
v_mfma_f32_16x16x32_bf16 a[92:95],a[188:191],a[244:247],a[92:95]
v_mfma_f32_16x16x32_bf16 a[124:127],a[188:191],a[252:255],a[124:127]
v_mfma_f32_16x16x32_bf16 a[124:127],a[184:187],a[248:251],a[124:127]
s_mov_b32 s53,3
L_qp_%=:
s_waitcnt lgkmcnt(1)
v_mfma_f32_16x16x32_bf16 v[128:131],a[128:131],a[192:195],v[128:131]
s_add_u32 m0,s58,0
s_add_u32 s60,s60,32768
ds_read_b64_tr_b16 a[160:161],v22 offset:1536
v_mfma_f32_16x16x32_bf16 v[128:131],a[132:135],a[196:199],v[128:131]
s_mov_b32 s90,L_att0_%=-L_pc_%=+128
buffer_load_dwordx4 v56,s[32:35],s56 offen lds
v_mfma_f32_16x16x32_bf16 v[136:139],a[132:135],a[204:207],v[136:139]
s_cmp_eq_u32 s60,131072
s_cselect_b32 s60,32768,s60
ds_read_b64_tr_b16 a[162:163],v22 offset:3584
v_mfma_f32_16x16x32_bf16 v[136:139],a[128:131],a[200:203],v[136:139]
s_add_u32 m0,s58,1024
ds_read_b64_tr_b16 a[164:165],v22 offset:17920
v_xor_b32 v20,131072,v20
v_mfma_f32_16x16x32_bf16 v[144:147],a[128:131],a[208:211],v[144:147]
s_sub_u32 s53,s53,1
ds_read_b64_tr_b16 a[166:167],v22 offset:19968
v_xor_b32 v21,131072,v21
v_mfma_f32_16x16x32_bf16 v[144:147],a[132:135],a[212:215],v[144:147]
ds_read_b64_tr_b16 a[168:169],v22 offset:1544
v_mfma_f32_16x16x32_bf16 v[152:155],a[132:135],a[220:223],v[152:155]
buffer_load_dwordx4 v57,s[32:35],s56 offen lds
v_mfma_f32_16x16x32_bf16 v[152:155],a[128:131],a[216:219],v[152:155]
ds_read_b64_tr_b16 a[170:171],v22 offset:3592
v_mfma_f32_16x16x32_bf16 v[132:135],a[136:139],a[192:195],v[132:135]
ds_read_b64_tr_b16 a[172:173],v22 offset:17928
v_mfma_f32_16x16x32_bf16 v[132:135],a[140:143],a[196:199],v[132:135]
s_add_u32 m0,s58,2048
ds_read_b64_tr_b16 a[174:175],v22 offset:19976
v_mfma_f32_16x16x32_bf16 v[140:143],a[140:143],a[204:207],v[140:143]
ds_read_b64_tr_b16 a[176:177],v22 offset:1792
v_mfma_f32_16x16x32_bf16 v[140:143],a[136:139],a[200:203],v[140:143]
ds_read_b64_tr_b16 a[178:179],v22 offset:3840
v_mfma_f32_16x16x32_bf16 v[148:151],a[136:139],a[208:211],v[148:151]
buffer_load_dwordx4 v58,s[32:35],s56 offen lds
v_mfma_f32_16x16x32_bf16 v[148:151],a[140:143],a[212:215],v[148:151]
ds_read_b64_tr_b16 a[180:181],v22 offset:18176
v_mfma_f32_16x16x32_bf16 v[156:159],a[140:143],a[220:223],v[156:159]
s_add_u32 m0,s58,3072
ds_read_b64_tr_b16 a[182:183],v22 offset:20224
v_mfma_f32_16x16x32_bf16 v[156:159],a[136:139],a[216:219],v[156:159]
ds_read_b64_tr_b16 a[184:185],v22 offset:1800
v_mfma_f32_16x16x32_bf16 v[160:163],a[144:147],a[192:195],v[160:163]
ds_read_b64_tr_b16 a[186:187],v22 offset:3848
v_mfma_f32_16x16x32_bf16 v[160:163],a[148:151],a[196:199],v[160:163]
buffer_load_dwordx4 v59,s[32:35],s56 offen lds
v_mfma_f32_16x16x32_bf16 v[168:171],a[148:151],a[204:207],v[168:171]
ds_read_b64_tr_b16 a[188:189],v22 offset:18184
v_mfma_f32_16x16x32_bf16 v[168:171],a[144:147],a[200:203],v[168:171]
ds_read_b64_tr_b16 a[190:191],v22 offset:20232
v_mfma_f32_16x16x32_bf16 v[176:179],a[144:147],a[208:211],v[176:179]
s_add_u32 m0,s58,4096
ds_read_b64_tr_b16 a[128:129],v22
v_mfma_f32_16x16x32_bf16 v[176:179],a[148:151],a[212:215],v[176:179]
ds_read_b64_tr_b16 a[130:131],v22 offset:2048
v_mfma_f32_16x16x32_bf16 v[184:187],a[148:151],a[220:223],v[184:187]
ds_read_b64_tr_b16 a[132:133],v22 offset:16384
v_mfma_f32_16x16x32_bf16 v[184:187],a[144:147],a[216:219],v[184:187]
buffer_load_dwordx4 v60,s[32:35],s56 offen lds
v_mfma_f32_16x16x32_bf16 v[164:167],a[152:155],a[192:195],v[164:167]
s_waitcnt lgkmcnt(5)
ds_read_b64_tr_b16 a[134:135],v22 offset:18432
v_mfma_f32_16x16x32_bf16 v[164:167],a[156:159],a[196:199],v[164:167]
s_add_u32 m0,s58,5120
ds_read_b64_tr_b16 a[136:137],v22 offset:8
v_mfma_f32_16x16x32_bf16 v[172:175],a[156:159],a[204:207],v[172:175]
ds_read_b64_tr_b16 a[138:139],v22 offset:2056
v_mfma_f32_16x16x32_bf16 v[172:175],a[152:155],a[200:203],v[172:175]
ds_read_b64_tr_b16 a[140:141],v22 offset:16392
v_mfma_f32_16x16x32_bf16 v[180:183],a[152:155],a[208:211],v[180:183]
buffer_load_dwordx4 v61,s[32:35],s56 offen lds
v_mfma_f32_16x16x32_bf16 v[180:183],a[156:159],a[212:215],v[180:183]
ds_read_b64_tr_b16 a[142:143],v22 offset:18440
v_mfma_f32_16x16x32_bf16 v[188:191],a[156:159],a[220:223],v[188:191]
ds_read_b64_tr_b16 a[144:145],v22 offset:256
v_mfma_f32_16x16x32_bf16 v[188:191],a[152:155],a[216:219],v[188:191]
s_add_u32 m0,s58,6144
ds_read_b64_tr_b16 a[146:147],v22 offset:2304
v_mfma_f32_16x16x32_bf16 v[192:195],a[160:163],a[192:195],v[192:195]
ds_read_b64_tr_b16 a[148:149],v22 offset:16640
v_mfma_f32_16x16x32_bf16 v[192:195],a[164:167],a[196:199],v[192:195]
ds_read_b64_tr_b16 a[150:151],v22 offset:18688
v_mfma_f32_16x16x32_bf16 v[200:203],a[164:167],a[204:207],v[200:203]
buffer_load_dwordx4 v62,s[32:35],s56 offen lds
v_mfma_f32_16x16x32_bf16 v[200:203],a[160:163],a[200:203],v[200:203]
ds_read_b64_tr_b16 a[152:153],v22 offset:264
v_mfma_f32_16x16x32_bf16 v[208:211],a[160:163],a[208:211],v[208:211]
s_add_u32 m0,s58,7168
s_add_u32 s58,s58,32768
ds_read_b64_tr_b16 a[154:155],v22 offset:2312
v_mfma_f32_16x16x32_bf16 v[208:211],a[164:167],a[212:215],v[208:211]
s_cmp_ge_u32 s58,131072
s_cselect_b32 s62,98304,0
ds_read_b64_tr_b16 a[156:157],v22 offset:16648
v_mfma_f32_16x16x32_bf16 v[216:219],a[164:167],a[220:223],v[216:219]
s_sub_u32 s58,s58,s62
ds_read_b64_tr_b16 a[158:159],v22 offset:18696
v_mfma_f32_16x16x32_bf16 v[216:219],a[160:163],a[216:219],v[216:219]
buffer_load_dwordx4 v63,s[32:35],s56 offen lds
v_mfma_f32_16x16x32_bf16 v[196:199],a[168:171],a[192:195],v[196:199]
s_add_u32 s56,s56,524288
ds_read_b64_tr_b16 a[160:161],v22 offset:512
v_mfma_f32_16x16x32_bf16 v[196:199],a[172:175],a[196:199],v[196:199]
ds_read_b64_tr_b16 a[162:163],v22 offset:2560
v_mfma_f32_16x16x32_bf16 v[204:207],a[172:175],a[204:207],v[204:207]
s_add_u32 m0,s59,0
ds_read_b64_tr_b16 a[164:165],v22 offset:16896
v_mfma_f32_16x16x32_bf16 v[204:207],a[168:171],a[200:203],v[204:207]
ds_read_b64_tr_b16 a[166:167],v22 offset:18944
v_mfma_f32_16x16x32_bf16 v[212:215],a[168:171],a[208:211],v[212:215]
v_mfma_f32_16x16x32_bf16 v[212:215],a[172:175],a[212:215],v[212:215]
buffer_load_dwordx4 v48,s[28:31],s57 offen lds
v_mfma_f32_16x16x32_bf16 v[220:223],a[172:175],a[220:223],v[220:223]
ds_read_b64_tr_b16 a[172:173],v22 offset:16904
v_mfma_f32_16x16x32_bf16 v[220:223],a[168:171],a[216:219],v[220:223]
ds_read_b64_tr_b16 a[168:169],v22 offset:520
v_mfma_f32_16x16x32_bf16 v[224:227],a[176:179],a[192:195],v[224:227]
ds_read_b64_tr_b16 a[170:171],v22 offset:2568
v_mfma_f32_16x16x32_bf16 v[224:227],a[180:183],a[196:199],v[224:227]
ds_read_b64_tr_b16 a[174:175],v22 offset:18952
v_mfma_f32_16x16x32_bf16 v[232:235],a[180:183],a[204:207],v[232:235]
buffer_load_dwordx4 v49,s[28:31],s57 offen offset:1024 lds
v_mfma_f32_16x16x32_bf16 v[232:235],a[176:179],a[200:203],v[232:235]
v_mfma_f32_16x16x32_bf16 v[240:243],a[176:179],a[208:211],v[240:243]
v_mfma_f32_16x16x32_bf16 v[240:243],a[180:183],a[212:215],v[240:243]
v_mfma_f32_16x16x32_bf16 v[248:251],a[180:183],a[220:223],v[248:251]
ds_read_b64_tr_b16 a[180:181],v22 offset:17152
v_mfma_f32_16x16x32_bf16 v[248:251],a[176:179],a[216:219],v[248:251]
ds_read_b64_tr_b16 a[176:177],v22 offset:768
v_mfma_f32_16x16x32_bf16 v[228:231],a[184:187],a[192:195],v[228:231]
s_waitcnt lgkmcnt(3)
buffer_load_dwordx4 v50,s[28:31],s57 offen offset:2048 lds
v_mfma_f32_16x16x32_bf16 v[228:231],a[188:191],a[196:199],v[228:231]
ds_read_b64_tr_b16 a[178:179],v22 offset:2816
v_mfma_f32_16x16x32_bf16 v[236:239],a[188:191],a[204:207],v[236:239]
ds_read_b64_tr_b16 a[182:183],v22 offset:19200
v_mfma_f32_16x16x32_bf16 v[236:239],a[184:187],a[200:203],v[236:239]
v_mfma_f32_16x16x32_bf16 v[244:247],a[184:187],a[208:211],v[244:247]
v_mfma_f32_16x16x32_bf16 v[244:247],a[188:191],a[212:215],v[244:247]
buffer_load_dwordx4 v51,s[28:31],s57 offen offset:3072 lds
v_mfma_f32_16x16x32_bf16 v[252:255],a[188:191],a[220:223],v[252:255]
ds_read_b64_tr_b16 a[188:189],v22 offset:17160
v_mfma_f32_16x16x32_bf16 v[252:255],a[184:187],a[216:219],v[252:255]
ds_read_b64_tr_b16 a[184:185],v22 offset:776
v_mfma_f32_16x16x32_bf16 a[0:3],a[128:131],a[192:195],a[0:3]
s_add_u32 m0,s59,4096
s_xor_b32 s59,s59,131072
ds_read_b64_tr_b16 a[186:187],v22 offset:2824
v_mfma_f32_16x16x32_bf16 a[0:3],a[132:135],a[196:199],a[0:3]
ds_read_b64_tr_b16 a[190:191],v22 offset:19208
v_add_u32 v22,s60,v23
v_mfma_f32_16x16x32_bf16 a[32:35],a[132:135],a[204:207],a[32:35]
s_add_u32 s60,s60,32768
s_cmp_eq_u32 s60,131072
s_cselect_b32 s60,32768,s60
v_mfma_f32_16x16x32_bf16 a[32:35],a[128:131],a[200:203],a[32:35]
buffer_load_dwordx4 v52,s[28:31],s57 offen lds
v_mfma_f32_16x16x32_bf16 a[64:67],a[128:131],a[208:211],a[64:67]
v_mfma_f32_16x16x32_bf16 a[64:67],a[132:135],a[212:215],a[64:67]
v_mfma_f32_16x16x32_bf16 a[96:99],a[132:135],a[220:223],a[96:99]
v_mfma_f32_16x16x32_bf16 a[96:99],a[128:131],a[216:219],a[96:99]
v_mfma_f32_16x16x32_bf16 a[4:7],a[136:139],a[192:195],a[4:7]
buffer_load_dwordx4 v53,s[28:31],s57 offen offset:1024 lds
v_mfma_f32_16x16x32_bf16 a[4:7],a[140:143],a[196:199],a[4:7]
v_mfma_f32_16x16x32_bf16 a[36:39],a[140:143],a[204:207],a[36:39]
v_mfma_f32_16x16x32_bf16 a[36:39],a[136:139],a[200:203],a[36:39]
v_mfma_f32_16x16x32_bf16 a[68:71],a[136:139],a[208:211],a[68:71]
v_mfma_f32_16x16x32_bf16 a[68:71],a[140:143],a[212:215],a[68:71]
v_mfma_f32_16x16x32_bf16 a[100:103],a[140:143],a[220:223],a[100:103]
buffer_load_dwordx4 v54,s[28:31],s57 offen offset:2048 lds
v_mfma_f32_16x16x32_bf16 a[100:103],a[136:139],a[216:219],a[100:103]
v_mfma_f32_16x16x32_bf16 a[8:11],a[144:147],a[192:195],a[8:11]
v_mfma_f32_16x16x32_bf16 a[8:11],a[148:151],a[196:199],a[8:11]
v_mfma_f32_16x16x32_bf16 a[40:43],a[148:151],a[204:207],a[40:43]
v_mfma_f32_16x16x32_bf16 a[40:43],a[144:147],a[200:203],a[40:43]
buffer_load_dwordx4 v55,s[28:31],s57 offen offset:3072 lds
v_mfma_f32_16x16x32_bf16 a[72:75],a[144:147],a[208:211],a[72:75]
s_add_u32 s57,s57,128
v_mfma_f32_16x16x32_bf16 a[72:75],a[148:151],a[212:215],a[72:75]
v_mfma_f32_16x16x32_bf16 a[104:107],a[148:151],a[220:223],a[104:107]
v_mfma_f32_16x16x32_bf16 a[104:107],a[144:147],a[216:219],a[104:107]
v_mfma_f32_16x16x32_bf16 a[12:15],a[152:155],a[192:195],a[12:15]
v_mfma_f32_16x16x32_bf16 a[12:15],a[156:159],a[196:199],a[12:15]
v_mfma_f32_16x16x32_bf16 a[44:47],a[156:159],a[204:207],a[44:47]
buffer_load_dword v16,v15,s[72:75],s83 offen
v_mfma_f32_16x16x32_bf16 a[44:47],a[152:155],a[200:203],a[44:47]
s_add_u32 s83,s83,896
v_mfma_f32_16x16x32_bf16 a[76:79],a[152:155],a[208:211],a[76:79]
v_mfma_f32_16x16x32_bf16 a[76:79],a[156:159],a[212:215],a[76:79]
v_mfma_f32_16x16x32_bf16 a[108:111],a[156:159],a[220:223],a[108:111]
v_mfma_f32_16x16x32_bf16 a[108:111],a[152:155],a[216:219],a[108:111]
s_waitcnt vmcnt(17) lgkmcnt(0)
v_mfma_f32_16x16x32_bf16 a[16:19],a[160:163],a[192:195],a[16:19]
s_barrier
ds_read_b128 a[224:227],v20
v_mfma_f32_16x16x32_bf16 a[16:19],a[164:167],a[196:199],a[16:19]
ds_read_b64_tr_b16 a[128:129],v22 offset:1024
v_mfma_f32_16x16x32_bf16 a[48:51],a[164:167],a[204:207],a[48:51]
ds_read_b64_tr_b16 a[130:131],v22 offset:3072
v_mfma_f32_16x16x32_bf16 a[48:51],a[160:163],a[200:203],a[48:51]
ds_read_b128 a[228:231],v21
v_mfma_f32_16x16x32_bf16 a[80:83],a[160:163],a[208:211],a[80:83]
)A"
R"A(ds_read_b64_tr_b16 a[132:133],v22 offset:17408
v_mfma_f32_16x16x32_bf16 a[80:83],a[164:167],a[212:215],a[80:83]
ds_read_b64_tr_b16 a[134:135],v22 offset:19456
v_mfma_f32_16x16x32_bf16 a[112:115],a[164:167],a[220:223],a[112:115]
ds_read_b128 a[232:235],v20 offset:2048
v_mfma_f32_16x16x32_bf16 a[112:115],a[160:163],a[216:219],a[112:115]
ds_read_b128 a[236:239],v21 offset:2048
v_mfma_f32_16x16x32_bf16 a[20:23],a[168:171],a[192:195],a[20:23]
ds_read_b128 a[240:243],v20 offset:4096
v_mfma_f32_16x16x32_bf16 a[20:23],a[172:175],a[196:199],a[20:23]
ds_read_b128 a[244:247],v21 offset:4096
v_mfma_f32_16x16x32_bf16 a[52:55],a[172:175],a[204:207],a[52:55]
ds_read_b128 a[248:251],v20 offset:6144
v_xor_b32 v20,131072,v20
v_mfma_f32_16x16x32_bf16 a[52:55],a[168:171],a[200:203],a[52:55]
ds_read_b128 a[252:255],v21 offset:6144
v_xor_b32 v21,131072,v21
v_mfma_f32_16x16x32_bf16 a[84:87],a[168:171],a[208:211],a[84:87]
ds_read_b64_tr_b16 a[136:137],v22 offset:1032
v_mfma_f32_16x16x32_bf16 a[84:87],a[172:175],a[212:215],a[84:87]
ds_read_b64_tr_b16 a[138:139],v22 offset:3080
v_mfma_f32_16x16x32_bf16 a[116:119],a[172:175],a[220:223],a[116:119]
ds_read_b64_tr_b16 a[140:141],v22 offset:17416
v_mfma_f32_16x16x32_bf16 a[116:119],a[168:171],a[216:219],a[116:119]
ds_read_b64_tr_b16 a[142:143],v22 offset:19464
v_mfma_f32_16x16x32_bf16 a[24:27],a[176:179],a[192:195],a[24:27]
ds_read_b64_tr_b16 a[144:145],v22 offset:1280
v_mfma_f32_16x16x32_bf16 a[24:27],a[180:183],a[196:199],a[24:27]
ds_read_b64_tr_b16 a[146:147],v22 offset:3328
v_mfma_f32_16x16x32_bf16 a[56:59],a[180:183],a[204:207],a[56:59]
ds_read_b64_tr_b16 a[148:149],v22 offset:17664
v_mfma_f32_16x16x32_bf16 a[56:59],a[176:179],a[200:203],a[56:59]
ds_read_b64_tr_b16 a[150:151],v22 offset:19712
v_mfma_f32_16x16x32_bf16 a[88:91],a[176:179],a[208:211],a[88:91]
ds_read_b64_tr_b16 a[152:153],v22 offset:1288
v_mfma_f32_16x16x32_bf16 a[88:91],a[180:183],a[212:215],a[88:91]
ds_read_b64_tr_b16 a[154:155],v22 offset:3336
v_mfma_f32_16x16x32_bf16 a[120:123],a[180:183],a[220:223],a[120:123]
ds_read_b64_tr_b16 a[156:157],v22 offset:17672
v_mfma_f32_16x16x32_bf16 a[120:123],a[176:179],a[216:219],a[120:123]
ds_read_b64_tr_b16 a[158:159],v22 offset:19720
v_mfma_f32_16x16x32_bf16 a[28:31],a[184:187],a[192:195],a[28:31]
ds_read_b64_tr_b16 a[160:161],v22 offset:1536
v_mfma_f32_16x16x32_bf16 a[28:31],a[188:191],a[196:199],a[28:31]
ds_read_b64_tr_b16 a[162:163],v22 offset:3584
v_mfma_f32_16x16x32_bf16 a[60:63],a[188:191],a[204:207],a[60:63]
ds_read_b64_tr_b16 a[164:165],v22 offset:17920
v_mfma_f32_16x16x32_bf16 a[60:63],a[184:187],a[200:203],a[60:63]
ds_read_b64_tr_b16 a[166:167],v22 offset:19968
v_mfma_f32_16x16x32_bf16 a[92:95],a[184:187],a[208:211],a[92:95]
ds_read_b64_tr_b16 a[168:169],v22 offset:1544
v_mfma_f32_16x16x32_bf16 a[92:95],a[188:191],a[212:215],a[92:95]
ds_read_b64_tr_b16 a[170:171],v22 offset:3592
v_mfma_f32_16x16x32_bf16 a[124:127],a[188:191],a[220:223],a[124:127]
s_add_u32 m0,s58,0
ds_read_b64_tr_b16 a[172:173],v22 offset:17928
v_mfma_f32_16x16x32_bf16 a[124:127],a[184:187],a[216:219],a[124:127]
s_waitcnt lgkmcnt(7)
ds_read_b64_tr_b16 a[174:175],v22 offset:19976
v_mfma_f32_16x16x32_bf16 v[128:131],a[128:131],a[224:227],v[128:131]
ds_read_b64_tr_b16 a[176:177],v22 offset:1792
v_mfma_f32_16x16x32_bf16 v[128:131],a[132:135],a[228:231],v[128:131]
buffer_load_dwordx4 v56,s[32:35],s56 offen lds
v_mfma_f32_16x16x32_bf16 v[136:139],a[132:135],a[236:239],v[136:139]
ds_read_b64_tr_b16 a[178:179],v22 offset:3840
v_mfma_f32_16x16x32_bf16 v[136:139],a[128:131],a[232:235],v[136:139]
s_add_u32 m0,s58,1024
ds_read_b64_tr_b16 a[180:181],v22 offset:18176
v_mfma_f32_16x16x32_bf16 v[144:147],a[128:131],a[240:243],v[144:147]
ds_read_b64_tr_b16 a[182:183],v22 offset:20224
v_mfma_f32_16x16x32_bf16 v[144:147],a[132:135],a[244:247],v[144:147]
ds_read_b64_tr_b16 a[184:185],v22 offset:1800
v_mfma_f32_16x16x32_bf16 v[152:155],a[132:135],a[252:255],v[152:155]
buffer_load_dwordx4 v57,s[32:35],s56 offen lds
v_mfma_f32_16x16x32_bf16 v[152:155],a[128:131],a[248:251],v[152:155]
ds_read_b64_tr_b16 a[186:187],v22 offset:3848
v_mfma_f32_16x16x32_bf16 v[132:135],a[136:139],a[224:227],v[132:135]
ds_read_b64_tr_b16 a[188:189],v22 offset:18184
v_mfma_f32_16x16x32_bf16 v[132:135],a[140:143],a[228:231],v[132:135]
s_add_u32 m0,s58,2048
ds_read_b64_tr_b16 a[190:191],v22 offset:20232
v_mfma_f32_16x16x32_bf16 v[140:143],a[140:143],a[236:239],v[140:143]
ds_read_b64_tr_b16 a[128:129],v22
v_mfma_f32_16x16x32_bf16 v[140:143],a[136:139],a[232:235],v[140:143]
ds_read_b64_tr_b16 a[130:131],v22 offset:2048
v_mfma_f32_16x16x32_bf16 v[148:151],a[136:139],a[240:243],v[148:151]
buffer_load_dwordx4 v58,s[32:35],s56 offen lds
v_mfma_f32_16x16x32_bf16 v[148:151],a[140:143],a[244:247],v[148:151]
ds_read_b64_tr_b16 a[132:133],v22 offset:16384
v_mfma_f32_16x16x32_bf16 v[156:159],a[140:143],a[252:255],v[156:159]
s_add_u32 m0,s58,3072
ds_read_b64_tr_b16 a[134:135],v22 offset:18432
v_mfma_f32_16x16x32_bf16 v[156:159],a[136:139],a[248:251],v[156:159]
ds_read_b64_tr_b16 a[136:137],v22 offset:8
v_mfma_f32_16x16x32_bf16 v[160:163],a[144:147],a[224:227],v[160:163]
ds_read_b64_tr_b16 a[138:139],v22 offset:2056
v_mfma_f32_16x16x32_bf16 v[160:163],a[148:151],a[228:231],v[160:163]
buffer_load_dwordx4 v59,s[32:35],s56 offen lds
v_mfma_f32_16x16x32_bf16 v[168:171],a[148:151],a[236:239],v[168:171]
ds_read_b64_tr_b16 a[140:141],v22 offset:16392
v_mfma_f32_16x16x32_bf16 v[168:171],a[144:147],a[232:235],v[168:171]
ds_read_b64_tr_b16 a[142:143],v22 offset:18440
v_mfma_f32_16x16x32_bf16 v[176:179],a[144:147],a[240:243],v[176:179]
s_add_u32 m0,s58,4096
v_mfma_f32_16x16x32_bf16 v[176:179],a[148:151],a[244:247],v[176:179]
v_mfma_f32_16x16x32_bf16 v[184:187],a[148:151],a[252:255],v[184:187]
ds_read_b64_tr_b16 a[148:149],v22 offset:16640
v_mfma_f32_16x16x32_bf16 v[184:187],a[144:147],a[248:251],v[184:187]
buffer_load_dwordx4 v60,s[32:35],s56 offen lds
v_mfma_f32_16x16x32_bf16 v[164:167],a[152:155],a[224:227],v[164:167]
ds_read_b64_tr_b16 a[144:145],v22 offset:256
v_mfma_f32_16x16x32_bf16 v[164:167],a[156:159],a[228:231],v[164:167]
s_add_u32 m0,s58,5120
ds_read_b64_tr_b16 a[146:147],v22 offset:2304
v_mfma_f32_16x16x32_bf16 v[172:175],a[156:159],a[236:239],v[172:175]
ds_read_b64_tr_b16 a[150:151],v22 offset:18688
v_mfma_f32_16x16x32_bf16 v[172:175],a[152:155],a[232:235],v[172:175]
v_mfma_f32_16x16x32_bf16 v[180:183],a[152:155],a[240:243],v[180:183]
buffer_load_dwordx4 v61,s[32:35],s56 offen lds
v_mfma_f32_16x16x32_bf16 v[180:183],a[156:159],a[244:247],v[180:183]
v_mfma_f32_16x16x32_bf16 v[188:191],a[156:159],a[252:255],v[188:191]
ds_read_b64_tr_b16 a[156:157],v22 offset:16648
v_mfma_f32_16x16x32_bf16 v[188:191],a[152:155],a[248:251],v[188:191]
s_add_u32 m0,s58,6144
ds_read_b64_tr_b16 a[152:153],v22 offset:264
s_waitcnt lgkmcnt(5)
v_mfma_f32_16x16x32_bf16 v[192:195],a[160:163],a[224:227],v[192:195]
ds_read_b64_tr_b16 a[154:155],v22 offset:2312
v_mfma_f32_16x16x32_bf16 v[192:195],a[164:167],a[228:231],v[192:195]
ds_read_b64_tr_b16 a[158:159],v22 offset:18696
v_mfma_f32_16x16x32_bf16 v[200:203],a[164:167],a[236:239],v[200:203]
buffer_load_dwordx4 v62,s[32:35],s56 offen lds
v_mfma_f32_16x16x32_bf16 v[200:203],a[160:163],a[232:235],v[200:203]
v_mfma_f32_16x16x32_bf16 v[208:211],a[160:163],a[240:243],v[208:211]
s_add_u32 m0,s58,7168
s_add_u32 s58,s58,32768
v_mfma_f32_16x16x32_bf16 v[208:211],a[164:167],a[244:247],v[208:211]
s_cmp_ge_u32 s58,131072
s_cselect_b32 s62,98304,0
s_sub_u32 s58,s58,s62
v_mfma_f32_16x16x32_bf16 v[216:219],a[164:167],a[252:255],v[216:219]
ds_read_b64_tr_b16 a[164:165],v22 offset:16896
v_mfma_f32_16x16x32_bf16 v[216:219],a[160:163],a[248:251],v[216:219]
buffer_load_dwordx4 v63,s[32:35],s56 offen lds
v_mfma_f32_16x16x32_bf16 v[196:199],a[168:171],a[224:227],v[196:199]
s_add_u32 s56,s56,524288
ds_read_b64_tr_b16 a[160:161],v22 offset:512
v_mfma_f32_16x16x32_bf16 v[196:199],a[172:175],a[228:231],v[196:199]
ds_read_b64_tr_b16 a[162:163],v22 offset:2560
v_mfma_f32_16x16x32_bf16 v[204:207],a[172:175],a[236:239],v[204:207]
s_add_u32 m0,s59,0
ds_read_b64_tr_b16 a[166:167],v22 offset:18944
v_mfma_f32_16x16x32_bf16 v[204:207],a[168:171],a[232:235],v[204:207]
v_mfma_f32_16x16x32_bf16 v[212:215],a[168:171],a[240:243],v[212:215]
v_mfma_f32_16x16x32_bf16 v[212:215],a[172:175],a[244:247],v[212:215]
buffer_load_dwordx4 v48,s[28:31],s57 offen lds
v_mfma_f32_16x16x32_bf16 v[220:223],a[172:175],a[252:255],v[220:223]
ds_read_b64_tr_b16 a[172:173],v22 offset:16904
v_mfma_f32_16x16x32_bf16 v[220:223],a[168:171],a[248:251],v[220:223]
ds_read_b64_tr_b16 a[168:169],v22 offset:520
v_mfma_f32_16x16x32_bf16 v[224:227],a[176:179],a[224:227],v[224:227]
ds_read_b64_tr_b16 a[170:171],v22 offset:2568
v_mfma_f32_16x16x32_bf16 v[224:227],a[180:183],a[228:231],v[224:227]
ds_read_b64_tr_b16 a[174:175],v22 offset:18952
v_mfma_f32_16x16x32_bf16 v[232:235],a[180:183],a[236:239],v[232:235]
buffer_load_dwordx4 v49,s[28:31],s57 offen offset:1024 lds
v_mfma_f32_16x16x32_bf16 v[232:235],a[176:179],a[232:235],v[232:235]
v_mfma_f32_16x16x32_bf16 v[240:243],a[176:179],a[240:243],v[240:243]
v_mfma_f32_16x16x32_bf16 v[240:243],a[180:183],a[244:247],v[240:243]
v_mfma_f32_16x16x32_bf16 v[248:251],a[180:183],a[252:255],v[248:251]
ds_read_b64_tr_b16 a[180:181],v22 offset:17152
v_mfma_f32_16x16x32_bf16 v[248:251],a[176:179],a[248:251],v[248:251]
ds_read_b64_tr_b16 a[176:177],v22 offset:768
v_mfma_f32_16x16x32_bf16 v[228:231],a[184:187],a[224:227],v[228:231]
buffer_load_dwordx4 v50,s[28:31],s57 offen offset:2048 lds
v_mfma_f32_16x16x32_bf16 v[228:231],a[188:191],a[228:231],v[228:231]
ds_read_b64_tr_b16 a[178:179],v22 offset:2816
v_mfma_f32_16x16x32_bf16 v[236:239],a[188:191],a[236:239],v[236:239]
ds_read_b64_tr_b16 a[182:183],v22 offset:19200
v_mfma_f32_16x16x32_bf16 v[236:239],a[184:187],a[232:235],v[236:239]
v_mfma_f32_16x16x32_bf16 v[244:247],a[184:187],a[240:243],v[244:247]
v_mfma_f32_16x16x32_bf16 v[244:247],a[188:191],a[244:247],v[244:247]
buffer_load_dwordx4 v51,s[28:31],s57 offen offset:3072 lds
v_mfma_f32_16x16x32_bf16 v[252:255],a[188:191],a[252:255],v[252:255]
ds_read_b64_tr_b16 a[188:189],v22 offset:17160
v_mfma_f32_16x16x32_bf16 v[252:255],a[184:187],a[248:251],v[252:255]
ds_read_b64_tr_b16 a[184:185],v22 offset:776
v_mfma_f32_16x16x32_bf16 a[0:3],a[128:131],a[224:227],a[0:3]
s_add_u32 m0,s59,4096
s_xor_b32 s59,s59,131072
ds_read_b64_tr_b16 a[186:187],v22 offset:2824
v_mfma_f32_16x16x32_bf16 a[0:3],a[132:135],a[228:231],a[0:3]
ds_read_b64_tr_b16 a[190:191],v22 offset:19208
v_add_u32 v22,s60,v23
v_mfma_f32_16x16x32_bf16 a[32:35],a[132:135],a[236:239],a[32:35]
v_mfma_f32_16x16x32_bf16 a[32:35],a[128:131],a[232:235],a[32:35]
buffer_load_dwordx4 v52,s[28:31],s57 offen lds
v_mfma_f32_16x16x32_bf16 a[64:67],a[128:131],a[240:243],a[64:67]
v_mfma_f32_16x16x32_bf16 a[64:67],a[132:135],a[244:247],a[64:67]
v_mfma_f32_16x16x32_bf16 a[96:99],a[132:135],a[252:255],a[96:99]
v_mfma_f32_16x16x32_bf16 a[96:99],a[128:131],a[248:251],a[96:99]
v_mfma_f32_16x16x32_bf16 a[4:7],a[136:139],a[224:227],a[4:7]
buffer_load_dwordx4 v53,s[28:31],s57 offen offset:1024 lds
v_mfma_f32_16x16x32_bf16 a[4:7],a[140:143],a[228:231],a[4:7]
v_mfma_f32_16x16x32_bf16 a[36:39],a[140:143],a[236:239],a[36:39]
v_mfma_f32_16x16x32_bf16 a[36:39],a[136:139],a[232:235],a[36:39]
v_mfma_f32_16x16x32_bf16 a[68:71],a[136:139],a[240:243],a[68:71]
v_mfma_f32_16x16x32_bf16 a[68:71],a[140:143],a[244:247],a[68:71]
v_mfma_f32_16x16x32_bf16 a[100:103],a[140:143],a[252:255],a[100:103]
buffer_load_dwordx4 v54,s[28:31],s57 offen offset:2048 lds
v_mfma_f32_16x16x32_bf16 a[100:103],a[136:139],a[248:251],a[100:103]
s_waitcnt lgkmcnt(0)
v_mfma_f32_16x16x32_bf16 a[8:11],a[144:147],a[224:227],a[8:11]
v_mfma_f32_16x16x32_bf16 a[8:11],a[148:151],a[228:231],a[8:11]
v_mfma_f32_16x16x32_bf16 a[40:43],a[148:151],a[236:239],a[40:43]
v_mfma_f32_16x16x32_bf16 a[40:43],a[144:147],a[232:235],a[40:43]
buffer_load_dwordx4 v55,s[28:31],s57 offen offset:3072 lds
v_mfma_f32_16x16x32_bf16 a[72:75],a[144:147],a[240:243],a[72:75]
s_add_u32 s57,s57,128
v_mfma_f32_16x16x32_bf16 a[72:75],a[148:151],a[244:247],a[72:75]
)A"
R"A(v_mfma_f32_16x16x32_bf16 a[104:107],a[148:151],a[252:255],a[104:107]
v_mfma_f32_16x16x32_bf16 a[104:107],a[144:147],a[248:251],a[104:107]
v_mfma_f32_16x16x32_bf16 a[12:15],a[152:155],a[224:227],a[12:15]
v_mfma_f32_16x16x32_bf16 a[12:15],a[156:159],a[228:231],a[12:15]
v_mfma_f32_16x16x32_bf16 a[44:47],a[156:159],a[236:239],a[44:47]
buffer_load_dword v16,v15,s[72:75],s83 offen
v_mfma_f32_16x16x32_bf16 a[44:47],a[152:155],a[232:235],a[44:47]
s_add_u32 s83,s83,896
v_mfma_f32_16x16x32_bf16 a[76:79],a[152:155],a[240:243],a[76:79]
v_mfma_f32_16x16x32_bf16 a[76:79],a[156:159],a[244:247],a[76:79]
v_mfma_f32_16x16x32_bf16 a[108:111],a[156:159],a[252:255],a[108:111]
v_mfma_f32_16x16x32_bf16 a[108:111],a[152:155],a[248:251],a[108:111]
s_waitcnt vmcnt(17) lgkmcnt(0)
v_mfma_f32_16x16x32_bf16 a[16:19],a[160:163],a[224:227],a[16:19]
s_barrier
ds_read_b128 a[192:195],v20
v_mfma_f32_16x16x32_bf16 a[16:19],a[164:167],a[228:231],a[16:19]
ds_read_b128 a[196:199],v21
v_mfma_f32_16x16x32_bf16 a[48:51],a[164:167],a[236:239],a[48:51]
ds_read_b128 a[200:203],v20 offset:2048
v_mfma_f32_16x16x32_bf16 a[48:51],a[160:163],a[232:235],a[48:51]
ds_read_b128 a[204:207],v21 offset:2048
v_mfma_f32_16x16x32_bf16 a[80:83],a[160:163],a[240:243],a[80:83]
ds_read_b128 a[208:211],v20 offset:4096
v_mfma_f32_16x16x32_bf16 a[80:83],a[164:167],a[244:247],a[80:83]
ds_read_b128 a[212:215],v21 offset:4096
v_mfma_f32_16x16x32_bf16 a[112:115],a[164:167],a[252:255],a[112:115]
buffer_load_dword v26,v24,s[88:91],s93 offen
v_mfma_f32_16x16x32_bf16 a[112:115],a[160:163],a[248:251],a[112:115]
ds_read_b128 a[216:219],v20 offset:6144
v_mfma_f32_16x16x32_bf16 a[20:23],a[168:171],a[224:227],a[20:23]
ds_read_b128 a[220:223],v21 offset:6144
v_mfma_f32_16x16x32_bf16 a[20:23],a[172:175],a[228:231],a[20:23]
ds_read_b64_tr_b16 a[128:129],v22 offset:1024
v_mfma_f32_16x16x32_bf16 a[52:55],a[172:175],a[236:239],a[52:55]
ds_read_b64_tr_b16 a[130:131],v22 offset:3072
v_mfma_f32_16x16x32_bf16 a[52:55],a[168:171],a[232:235],a[52:55]
ds_read_b64_tr_b16 a[132:133],v22 offset:17408
v_mfma_f32_16x16x32_bf16 a[84:87],a[168:171],a[240:243],a[84:87]
ds_read_b64_tr_b16 a[134:135],v22 offset:19456
v_mfma_f32_16x16x32_bf16 a[84:87],a[172:175],a[244:247],a[84:87]
ds_read_b64_tr_b16 a[136:137],v22 offset:1032
v_mfma_f32_16x16x32_bf16 a[116:119],a[172:175],a[252:255],a[116:119]
ds_read_b64_tr_b16 a[138:139],v22 offset:3080
v_mfma_f32_16x16x32_bf16 a[116:119],a[168:171],a[248:251],a[116:119]
ds_read_b64_tr_b16 a[140:141],v22 offset:17416
v_mfma_f32_16x16x32_bf16 a[24:27],a[176:179],a[224:227],a[24:27]
ds_read_b64_tr_b16 a[142:143],v22 offset:19464
v_mfma_f32_16x16x32_bf16 a[24:27],a[180:183],a[228:231],a[24:27]
ds_read_b64_tr_b16 a[144:145],v22 offset:1280
v_mfma_f32_16x16x32_bf16 a[56:59],a[180:183],a[236:239],a[56:59]
ds_read_b64_tr_b16 a[146:147],v22 offset:3328
v_mfma_f32_16x16x32_bf16 a[56:59],a[176:179],a[232:235],a[56:59]
ds_read_b64_tr_b16 a[148:149],v22 offset:17664
v_mfma_f32_16x16x32_bf16 a[88:91],a[176:179],a[240:243],a[88:91]
ds_read_b64_tr_b16 a[150:151],v22 offset:19712
v_mfma_f32_16x16x32_bf16 a[88:91],a[180:183],a[244:247],a[88:91]
ds_read_b64_tr_b16 a[152:153],v22 offset:1288
v_mfma_f32_16x16x32_bf16 a[120:123],a[180:183],a[252:255],a[120:123]
ds_read_b64_tr_b16 a[154:155],v22 offset:3336
v_mfma_f32_16x16x32_bf16 a[120:123],a[176:179],a[248:251],a[120:123]
ds_read_b64_tr_b16 a[156:157],v22 offset:17672
v_mfma_f32_16x16x32_bf16 a[28:31],a[184:187],a[224:227],a[28:31]
ds_read_b64_tr_b16 a[158:159],v22 offset:19720
v_mfma_f32_16x16x32_bf16 a[28:31],a[188:191],a[228:231],a[28:31]
v_mfma_f32_16x16x32_bf16 a[60:63],a[188:191],a[236:239],a[60:63]
v_mfma_f32_16x16x32_bf16 a[60:63],a[184:187],a[232:235],a[60:63]
v_mfma_f32_16x16x32_bf16 a[92:95],a[184:187],a[240:243],a[92:95]
v_mfma_f32_16x16x32_bf16 a[92:95],a[188:191],a[244:247],a[92:95]
v_mfma_f32_16x16x32_bf16 a[124:127],a[188:191],a[252:255],a[124:127]
s_cmp_lg_u32 s53,0
v_mfma_f32_16x16x32_bf16 a[124:127],a[184:187],a[248:251],a[124:127]
s_cbranch_scc1 L_qp_%=
L_tailseg_%=:
s_waitcnt lgkmcnt(1)
v_mfma_f32_16x16x32_bf16 v[128:131],a[128:131],a[192:195],v[128:131]
s_add_u32 m0,98304,s51
ds_read_b64_tr_b16 a[160:161],v22 offset:1536
v_xor_b32 v20,131072,v20
v_mfma_f32_16x16x32_bf16 v[128:131],a[132:135],a[196:199],v[128:131]
s_add_u32 s54,s54,16384
buffer_load_dwordx4 v4,s[20:23],s47 offen nt lds
v_mfma_f32_16x16x32_bf16 v[136:139],a[132:135],a[204:207],v[136:139]
s_and_b32 s46,s54,0xfffc0000
ds_read_b64_tr_b16 a[162:163],v22 offset:3584
v_xor_b32 v21,131072,v21
v_mfma_f32_16x16x32_bf16 v[136:139],a[128:131],a[200:203],v[136:139]
s_add_u32 s54,s54,16384
ds_read_b64_tr_b16 a[164:165],v22 offset:17920
v_and_b32 v18,48,v6
v_mfma_f32_16x16x32_bf16 v[144:147],a[128:131],a[208:211],v[144:147]
s_add_u32 s49,s49,32768
ds_read_b64_tr_b16 a[166:167],v22 offset:19968
v_lshrrev_b32 v19,4,v6
v_mfma_f32_16x16x32_bf16 v[144:147],a[132:135],a[212:215],v[144:147]
s_lshr_b32 s79,%1,3
ds_read_b64_tr_b16 a[168:169],v22 offset:1544
v_lshlrev_b32 v18,2,v18
v_mfma_f32_16x16x32_bf16 v[152:155],a[132:135],a[220:223],v[152:155]
s_mul_i32 s79,s79,7
ds_read_b64_tr_b16 a[170:171],v22 offset:3592
v_and_b32 v19,48,v19
v_mfma_f32_16x16x32_bf16 v[152:155],a[128:131],a[216:219],v[152:155]
s_mov_b32 s60,138
ds_read_b64_tr_b16 a[172:173],v22 offset:17928
v_add_u32 v18,v18,v19
v_mfma_f32_16x16x32_bf16 v[132:135],a[136:139],a[192:195],v[132:135]
s_lshr_b32 s79,s79,5
ds_read_b64_tr_b16 a[174:175],v22 offset:19976
v_add_u32 v18,98304,v18
v_mfma_f32_16x16x32_bf16 v[132:135],a[140:143],a[196:199],v[132:135]
s_cmp_eq_u32 s49,163840
s_cselect_b32 s49,0,s49
ds_read_b64_tr_b16 a[176:177],v22 offset:1792
v_mfma_f32_16x16x32_bf16 v[140:143],a[140:143],a[204:207],v[140:143]
s_cmp_eq_u32 s50,131072
s_cselect_b32 s62,163840,0
ds_read_b64_tr_b16 a[178:179],v22 offset:3840
v_mfma_f32_16x16x32_bf16 v[140:143],a[136:139],a[200:203],v[140:143]
s_sub_u32 s48,32768,s62
s_sub_u32 s60,s60,s79
ds_read_b64_tr_b16 a[180:181],v22 offset:18176
v_mfma_f32_16x16x32_bf16 v[148:151],a[136:139],a[208:211],v[148:151]
s_add_u32 s50,s50,s48
s_add_u32 s93,s92,L_att0_%=-L_pc_%=
ds_read_b64_tr_b16 a[182:183],v22 offset:20224
v_mfma_f32_16x16x32_bf16 v[148:151],a[140:143],a[212:215],v[148:151]
s_cmp_eq_u32 s50,131072
s_cselect_b32 s62,163840,0
ds_read_b64_tr_b16 a[184:185],v22 offset:1800
v_mfma_f32_16x16x32_bf16 v[156:159],a[140:143],a[220:223],v[156:159]
ds_read_b64_tr_b16 a[186:187],v22 offset:3848
v_mfma_f32_16x16x32_bf16 v[156:159],a[136:139],a[216:219],v[156:159]
ds_read_b64_tr_b16 a[188:189],v22 offset:18184
v_mfma_f32_16x16x32_bf16 v[160:163],a[144:147],a[192:195],v[160:163]
ds_read_b64_tr_b16 a[190:191],v22 offset:20232
v_mfma_f32_16x16x32_bf16 v[160:163],a[148:151],a[196:199],v[160:163]
buffer_load_dwordx4 v4,s[20:23],s47 offen offset:1024 nt lds
v_mfma_f32_16x16x32_bf16 v[168:171],a[148:151],a[204:207],v[168:171]
s_waitcnt vmcnt(4)
ds_read_b128 a[224:227],v20
v_mfma_f32_16x16x32_bf16 v[168:171],a[144:147],a[200:203],v[168:171]
ds_read_b64_tr_b16 a[128:129],v22
v_mfma_f32_16x16x32_bf16 v[176:179],a[144:147],a[208:211],v[176:179]
ds_read_b64_tr_b16 a[130:131],v22 offset:2048
v_mfma_f32_16x16x32_bf16 v[176:179],a[148:151],a[212:215],v[176:179]
ds_read_b64_tr_b16 a[132:133],v22 offset:16384
v_mfma_f32_16x16x32_bf16 v[184:187],a[148:151],a[220:223],v[184:187]
ds_read_b64_tr_b16 a[134:135],v22 offset:18432
v_mfma_f32_16x16x32_bf16 v[184:187],a[144:147],a[216:219],v[184:187]
ds_read_b64_tr_b16 a[144:145],v22 offset:8
v_mfma_f32_16x16x32_bf16 v[164:167],a[152:155],a[192:195],v[164:167]
s_waitcnt lgkmcnt(6)
ds_read_b64_tr_b16 a[146:147],v22 offset:2056
v_mfma_f32_16x16x32_bf16 v[164:167],a[156:159],a[196:199],v[164:167]
ds_read_b64_tr_b16 a[148:149],v22 offset:16392
v_mfma_f32_16x16x32_bf16 v[172:175],a[156:159],a[204:207],v[172:175]
ds_read_b64_tr_b16 a[150:151],v22 offset:18440
v_mfma_f32_16x16x32_bf16 v[172:175],a[152:155],a[200:203],v[172:175]
v_readfirstlane_b32 s68,v13
s_nop 1
s_add_u32 s68,s68,1
v_mfma_f32_16x16x32_bf16 v[180:183],a[152:155],a[208:211],v[180:183]
s_xor_b32 s69,s68,0x9e3779b9
ds_read_b128 a[228:231],v21
v_mfma_f32_16x16x32_bf16 v[180:183],a[156:159],a[212:215],v[180:183]
v_mfma_f32_16x16x32_bf16 v[188:191],a[156:159],a[220:223],v[188:191]
ds_read_b128 a[232:235],v20 offset:2048
v_mfma_f32_16x16x32_bf16 v[188:191],a[152:155],a[216:219],v[188:191]
v_mfma_f32_16x16x32_bf16 v[192:195],a[160:163],a[192:195],v[192:195]
ds_read_b128 a[236:239],v21 offset:2048
ds_read_b128 a[240:243],v20 offset:4096
ds_read_b128 a[244:247],v21 offset:4096
ds_read_b128 a[248:251],v20 offset:6144
ds_read_b128 a[252:255],v21 offset:6144
v_mfma_f32_16x16x32_bf16 v[192:195],a[164:167],a[196:199],v[192:195]
v_mfma_f32_16x16x32_bf16 v[200:203],a[164:167],a[204:207],v[200:203]
v_mfma_f32_16x16x32_bf16 v[200:203],a[160:163],a[200:203],v[200:203]
v_mfma_f32_16x16x32_bf16 v[208:211],a[160:163],a[208:211],v[208:211]
buffer_load_dwordx4 v5,s[20:23],s47 offen offset:2048 nt lds
v_mfma_f32_16x16x32_bf16 v[208:211],a[164:167],a[212:215],v[208:211]
v_mfma_f32_16x16x32_bf16 v[216:219],a[164:167],a[220:223],v[216:219]
s_waitcnt vmcnt(4) lgkmcnt(0)
v_mfma_f32_16x16x32_bf16 v[216:219],a[160:163],a[216:219],v[216:219]
s_barrier
ds_read_b64_tr_b16 a[136:137],v22 offset:32768
v_mfma_f32_16x16x32_bf16 v[196:199],a[168:171],a[192:195],v[196:199]
ds_read_b64_tr_b16 a[138:139],v22 offset:34816
v_mfma_f32_16x16x32_bf16 v[196:199],a[172:175],a[196:199],v[196:199]
ds_read_b64_tr_b16 a[140:141],v22 offset:49152
v_mfma_f32_16x16x32_bf16 v[204:207],a[172:175],a[204:207],v[204:207]
ds_read_b64_tr_b16 a[142:143],v22 offset:51200
v_mfma_f32_16x16x32_bf16 v[204:207],a[168:171],a[200:203],v[204:207]
ds_read_b64_tr_b16 a[152:153],v22 offset:32776
v_mfma_f32_16x16x32_bf16 v[212:215],a[168:171],a[208:211],v[212:215]
ds_read_b64_tr_b16 a[154:155],v22 offset:34824
v_mfma_f32_16x16x32_bf16 v[212:215],a[172:175],a[212:215],v[212:215]
ds_read_b64_tr_b16 a[156:157],v22 offset:49160
v_mfma_f32_16x16x32_bf16 v[220:223],a[172:175],a[220:223],v[220:223]
ds_read_b64_tr_b16 a[158:159],v22 offset:51208
v_mfma_f32_16x16x32_bf16 v[220:223],a[168:171],a[216:219],v[220:223]
ds_read_b64_tr_b16 a[160:161],v22 offset:256
v_mfma_f32_16x16x32_bf16 v[224:227],a[176:179],a[192:195],v[224:227]
buffer_load_dwordx4 v5,s[20:23],s47 offen offset:3072 nt lds
v_mfma_f32_16x16x32_bf16 v[224:227],a[180:183],a[196:199],v[224:227]
ds_read_b64_tr_b16 a[162:163],v22 offset:2304
v_mfma_f32_16x16x32_bf16 v[232:235],a[180:183],a[204:207],v[232:235]
ds_read_b64_tr_b16 a[164:165],v22 offset:16640
v_mfma_f32_16x16x32_bf16 v[232:235],a[176:179],a[200:203],v[232:235]
ds_read_b64_tr_b16 a[166:167],v22 offset:18688
v_mfma_f32_16x16x32_bf16 v[240:243],a[176:179],a[208:211],v[240:243]
ds_read_b64_tr_b16 a[168:169],v22 offset:33024
v_mfma_f32_16x16x32_bf16 v[240:243],a[180:183],a[212:215],v[240:243]
ds_read_b64_tr_b16 a[170:171],v22 offset:35072
v_mfma_f32_16x16x32_bf16 v[248:251],a[180:183],a[220:223],v[248:251]
ds_read_b64_tr_b16 a[172:173],v22 offset:49408
v_mfma_f32_16x16x32_bf16 v[248:251],a[176:179],a[216:219],v[248:251]
ds_read_b64_tr_b16 a[174:175],v22 offset:51456
v_mfma_f32_16x16x32_bf16 v[228:231],a[184:187],a[192:195],v[228:231]
ds_read_b64_tr_b16 a[176:177],v22 offset:264
v_mfma_f32_16x16x32_bf16 v[228:231],a[188:191],a[196:199],v[228:231]
ds_read_b64_tr_b16 a[178:179],v22 offset:2312
v_mfma_f32_16x16x32_bf16 v[236:239],a[188:191],a[204:207],v[236:239]
ds_read_b64_tr_b16 a[180:181],v22 offset:16648
v_mfma_f32_16x16x32_bf16 v[236:239],a[184:187],a[200:203],v[236:239]
ds_read_b64_tr_b16 a[182:183],v22 offset:18696
v_mfma_f32_16x16x32_bf16 v[244:247],a[184:187],a[208:211],v[244:247]
v_mfma_f32_16x16x32_bf16 v[244:247],a[188:191],a[212:215],v[244:247]
s_add_u32 m0,98304,s52
v_mfma_f32_16x16x32_bf16 v[252:255],a[188:191],a[220:223],v[252:255]
ds_read_b64_tr_b16 a[188:189],v22 offset:49416
v_mfma_f32_16x16x32_bf16 v[252:255],a[184:187],a[216:219],v[252:255]
ds_read_b64_tr_b16 a[184:185],v22 offset:33032
v_mfma_f32_16x16x32_bf16 a[0:3],a[128:131],a[192:195],a[0:3]
)A"
R"A(buffer_load_dwordx4 v4,s[24:27],s47 offen nt lds
v_mfma_f32_16x16x32_bf16 a[0:3],a[132:135],a[196:199],a[0:3]
s_waitcnt lgkmcnt(4)
ds_read_b64_tr_b16 a[186:187],v22 offset:35080
v_mfma_f32_16x16x32_bf16 a[0:3],a[136:139],a[224:227],a[0:3]
ds_read_b64_tr_b16 a[190:191],v22 offset:51464
v_mfma_f32_16x16x32_bf16 a[0:3],a[140:143],a[228:231],a[0:3]
v_mfma_f32_16x16x32_bf16 a[32:35],a[140:143],a[236:239],a[32:35]
v_mfma_f32_16x16x32_bf16 a[32:35],a[136:139],a[232:235],a[32:35]
v_mfma_f32_16x16x32_bf16 a[32:35],a[132:135],a[204:207],a[32:35]
v_mfma_f32_16x16x32_bf16 a[32:35],a[128:131],a[200:203],a[32:35]
v_mfma_f32_16x16x32_bf16 a[64:67],a[128:131],a[208:211],a[64:67]
v_mfma_f32_16x16x32_bf16 a[64:67],a[132:135],a[212:215],a[64:67]
s_nop 1
v_accvgpr_read_b32 v64,a0
v_accvgpr_read_b32 v65,a1
v_mfma_f32_16x16x32_bf16 a[64:67],a[136:139],a[240:243],a[64:67]
v_mul_f32 v64,s61,v64
v_mul_f32 v65,s61,v65
v_mfma_f32_16x16x32_bf16 a[64:67],a[140:143],a[244:247],a[64:67]
v_accvgpr_read_b32 v66,a2
v_accvgpr_read_b32 v67,a3
v_mfma_f32_16x16x32_bf16 a[96:99],a[140:143],a[252:255],a[96:99]
ds_read_b64_tr_b16 a[140:141],v22 offset:49664
v_cvt_pk_bf16_f32 v64,v64,v65
v_mfma_f32_16x16x32_bf16 a[96:99],a[136:139],a[248:251],a[96:99]
ds_read_b64_tr_b16 a[136:137],v22 offset:33280
v_accvgpr_read_b32 v80,a32
v_mfma_f32_16x16x32_bf16 a[96:99],a[132:135],a[220:223],a[96:99]
ds_read_b64_tr_b16 a[132:133],v22 offset:16896
v_accvgpr_read_b32 v81,a33
v_mfma_f32_16x16x32_bf16 a[96:99],a[128:131],a[216:219],a[96:99]
ds_read_b64_tr_b16 a[128:129],v22 offset:512
v_mul_f32 v80,s61,v80
v_mfma_f32_16x16x32_bf16 a[4:7],a[144:147],a[192:195],a[4:7]
buffer_load_dwordx4 v5,s[24:27],s47 offen offset:1024 nt lds
v_mfma_f32_16x16x32_bf16 a[4:7],a[148:151],a[196:199],a[4:7]
ds_read_b64_tr_b16 a[130:131],v22 offset:2560
v_mul_f32 v81,s61,v81
v_mfma_f32_16x16x32_bf16 a[4:7],a[152:155],a[224:227],a[4:7]
ds_read_b64_tr_b16 a[134:135],v22 offset:18944
v_accvgpr_read_b32 v82,a34
v_mfma_f32_16x16x32_bf16 a[4:7],a[156:159],a[228:231],a[4:7]
ds_read_b64_tr_b16 a[138:139],v22 offset:35328
v_accvgpr_read_b32 v83,a35
v_mfma_f32_16x16x32_bf16 a[36:39],a[156:159],a[236:239],a[36:39]
ds_read_b64_tr_b16 a[142:143],v22 offset:51712
v_mul_f32 v66,s61,v66
v_mfma_f32_16x16x32_bf16 a[36:39],a[152:155],a[232:235],a[36:39]
v_mul_f32 v67,s61,v67
v_cvt_pk_bf16_f32 v80,v80,v81
v_mfma_f32_16x16x32_bf16 a[36:39],a[148:151],a[204:207],a[36:39]
v_mul_f32 v82,s61,v82
v_mul_f32 v83,s61,v83
v_mfma_f32_16x16x32_bf16 a[36:39],a[144:147],a[200:203],a[36:39]
v_cvt_pk_bf16_f32 v65,v66,v67
v_cvt_pk_bf16_f32 v81,v82,v83
v_mfma_f32_16x16x32_bf16 a[68:71],a[144:147],a[208:211],a[68:71]
v_accvgpr_read_b32 v96,a64
v_accvgpr_read_b32 v97,a65
v_mfma_f32_16x16x32_bf16 a[68:71],a[148:151],a[212:215],a[68:71]
v_accvgpr_read_b32 v112,a96
v_accvgpr_read_b32 v113,a97
v_mfma_f32_16x16x32_bf16 a[68:71],a[152:155],a[240:243],a[68:71]
v_accvgpr_read_b32 v66,a4
v_accvgpr_read_b32 v67,a5
v_mfma_f32_16x16x32_bf16 a[68:71],a[156:159],a[244:247],a[68:71]
v_mul_f32 v96,s61,v96
v_mul_f32 v97,s61,v97
v_mfma_f32_16x16x32_bf16 a[100:103],a[156:159],a[252:255],a[100:103]
ds_read_b64_tr_b16 a[156:157],v22 offset:49672
v_accvgpr_read_b32 v98,a66
v_mfma_f32_16x16x32_bf16 a[100:103],a[152:155],a[248:251],a[100:103]
ds_read_b64_tr_b16 a[152:153],v22 offset:33288
v_accvgpr_read_b32 v82,a36
v_mfma_f32_16x16x32_bf16 a[100:103],a[148:151],a[220:223],a[100:103]
ds_read_b64_tr_b16 a[148:149],v22 offset:16904
v_accvgpr_read_b32 v83,a37
v_mfma_f32_16x16x32_bf16 a[100:103],a[144:147],a[216:219],a[100:103]
ds_read_b64_tr_b16 a[144:145],v22 offset:520
v_accvgpr_read_b32 v99,a67
v_mfma_f32_16x16x32_bf16 a[8:11],a[160:163],a[192:195],a[8:11]
buffer_load_dwordx4 v4,s[24:27],s47 offen offset:2048 nt lds
v_mfma_f32_16x16x32_bf16 a[8:11],a[164:167],a[196:199],a[8:11]
ds_read_b64_tr_b16 a[146:147],v22 offset:2568
v_mul_f32 v112,s61,v112
v_mfma_f32_16x16x32_bf16 a[8:11],a[168:171],a[224:227],a[8:11]
ds_read_b64_tr_b16 a[150:151],v22 offset:18952
v_mul_f32 v113,s61,v113
v_mfma_f32_16x16x32_bf16 a[8:11],a[172:175],a[228:231],a[8:11]
ds_read_b64_tr_b16 a[154:155],v22 offset:35336
v_accvgpr_read_b32 v114,a98
v_mfma_f32_16x16x32_bf16 a[40:43],a[172:175],a[236:239],a[40:43]
ds_read_b64_tr_b16 a[158:159],v22 offset:51720
v_accvgpr_read_b32 v115,a99
v_mfma_f32_16x16x32_bf16 a[40:43],a[168:171],a[232:235],a[40:43]
v_accvgpr_read_b32 v12,a6
v_accvgpr_read_b32 v13,a7
v_mfma_f32_16x16x32_bf16 a[40:43],a[164:167],a[204:207],a[40:43]
v_mul_f32 v66,s61,v66
v_mul_f32 v67,s61,v67
v_mfma_f32_16x16x32_bf16 a[40:43],a[160:163],a[200:203],a[40:43]
v_accvgpr_read_b32 v14,a38
v_accvgpr_read_b32 v15,a39
v_mfma_f32_16x16x32_bf16 a[72:75],a[160:163],a[208:211],a[72:75]
v_mul_f32 v82,s61,v82
v_mul_f32 v83,s61,v83
v_mfma_f32_16x16x32_bf16 a[72:75],a[164:167],a[212:215],a[72:75]
v_accvgpr_read_b32 v68,a8
v_accvgpr_read_b32 v69,a9
v_mfma_f32_16x16x32_bf16 a[72:75],a[168:171],a[240:243],a[72:75]
v_mul_f32 v68,s61,v68
v_mul_f32 v69,s61,v69
v_mfma_f32_16x16x32_bf16 a[72:75],a[172:175],a[244:247],a[72:75]
v_accvgpr_read_b32 v70,a10
v_accvgpr_read_b32 v71,a11
v_mfma_f32_16x16x32_bf16 a[104:107],a[172:175],a[252:255],a[104:107]
ds_read_b64_tr_b16 a[172:173],v22 offset:49920
v_cvt_pk_bf16_f32 v96,v96,v97
v_mfma_f32_16x16x32_bf16 a[104:107],a[168:171],a[248:251],a[104:107]
ds_read_b64_tr_b16 a[168:169],v22 offset:33536
v_mul_f32 v98,s61,v98
v_mfma_f32_16x16x32_bf16 a[104:107],a[164:167],a[220:223],a[104:107]
ds_read_b64_tr_b16 a[164:165],v22 offset:17152
v_mul_f32 v99,s61,v99
v_mfma_f32_16x16x32_bf16 a[104:107],a[160:163],a[216:219],a[104:107]
ds_read_b64_tr_b16 a[160:161],v22 offset:768
v_cvt_pk_bf16_f32 v112,v112,v113
v_mfma_f32_16x16x32_bf16 a[12:15],a[176:179],a[192:195],a[12:15]
s_waitcnt lgkmcnt(4)
buffer_load_dwordx4 v5,s[24:27],s47 offen offset:3072 nt lds
v_mfma_f32_16x16x32_bf16 a[12:15],a[180:183],a[196:199],a[12:15]
s_add_u32 s47,s47,16384
s_and_b32 s47,s47,0x3ffff
s_or_b32 s47,s47,s46
v_mfma_f32_16x16x32_bf16 a[12:15],a[184:187],a[224:227],a[12:15]
s_and_b32 s46,s54,0xfffc0000
ds_read_b64_tr_b16 a[162:163],v22 offset:2816
v_mul_f32 v114,s61,v114
v_mfma_f32_16x16x32_bf16 a[12:15],a[188:191],a[228:231],a[12:15]
s_add_u32 s54,s54,16384
ds_read_b64_tr_b16 a[166:167],v22 offset:19200
v_mul_f32 v115,s61,v115
v_mfma_f32_16x16x32_bf16 a[44:47],a[188:191],a[236:239],a[44:47]
ds_read_b64_tr_b16 a[170:171],v22 offset:35584
v_cvt_pk_bf16_f32 v68,v68,v69
v_mfma_f32_16x16x32_bf16 a[44:47],a[184:187],a[232:235],a[44:47]
ds_read_b64_tr_b16 a[174:175],v22 offset:51968
v_mul_f32 v70,s61,v70
v_mfma_f32_16x16x32_bf16 a[44:47],a[180:183],a[204:207],a[44:47]
v_mul_f32 v71,s61,v71
v_mul_f32 v12,s61,v12
v_mfma_f32_16x16x32_bf16 a[44:47],a[176:179],a[200:203],a[44:47]
v_mul_f32 v13,s61,v13
v_cvt_pk_bf16_f32 v66,v66,v67
v_mfma_f32_16x16x32_bf16 a[76:79],a[176:179],a[208:211],a[76:79]
v_mul_f32 v14,s61,v14
v_mul_f32 v15,s61,v15
v_mfma_f32_16x16x32_bf16 a[76:79],a[180:183],a[212:215],a[76:79]
v_cvt_pk_bf16_f32 v82,v82,v83
v_cvt_pk_bf16_f32 v97,v98,v99
v_mfma_f32_16x16x32_bf16 a[76:79],a[184:187],a[240:243],a[76:79]
v_cvt_pk_bf16_f32 v113,v114,v115
v_cvt_pk_bf16_f32 v69,v70,v71
v_mfma_f32_16x16x32_bf16 a[76:79],a[188:191],a[244:247],a[76:79]
v_accvgpr_read_b32 v84,a40
v_accvgpr_read_b32 v85,a41
v_mfma_f32_16x16x32_bf16 a[108:111],a[188:191],a[252:255],a[108:111]
ds_read_b64_tr_b16 a[188:189],v22 offset:49928
v_accvgpr_read_b32 v100,a72
v_mfma_f32_16x16x32_bf16 a[108:111],a[184:187],a[248:251],a[108:111]
ds_read_b64_tr_b16 a[184:185],v22 offset:33544
v_accvgpr_read_b32 v101,a73
v_mfma_f32_16x16x32_bf16 a[108:111],a[180:183],a[220:223],a[108:111]
ds_read_b64_tr_b16 a[180:181],v22 offset:17160
v_accvgpr_read_b32 v116,a104
v_mfma_f32_16x16x32_bf16 a[108:111],a[176:179],a[216:219],a[108:111]
ds_read_b64_tr_b16 a[176:177],v22 offset:776
v_accvgpr_read_b32 v117,a105
v_mfma_f32_16x16x32_bf16 a[16:19],a[128:131],a[192:195],a[16:19]
ds_read_b64_tr_b16 a[178:179],v22 offset:2824
v_cvt_pk_bf16_f32 v67,v12,v13
v_mfma_f32_16x16x32_bf16 a[16:19],a[132:135],a[196:199],a[16:19]
ds_read_b64_tr_b16 a[182:183],v22 offset:19208
v_cvt_pk_bf16_f32 v83,v14,v15
v_mfma_f32_16x16x32_bf16 a[16:19],a[136:139],a[224:227],a[16:19]
ds_read_b64_tr_b16 a[186:187],v22 offset:35592
v_accvgpr_read_b32 v98,a68
v_mfma_f32_16x16x32_bf16 a[16:19],a[140:143],a[228:231],a[16:19]
s_add_u32 m0,131072,s51
ds_read_b64_tr_b16 a[190:191],v22 offset:51976
v_accvgpr_read_b32 v99,a69
v_mfma_f32_16x16x32_bf16 a[48:51],a[140:143],a[236:239],a[48:51]
v_accvgpr_read_b32 v114,a100
v_accvgpr_read_b32 v115,a101
v_mfma_f32_16x16x32_bf16 a[48:51],a[136:139],a[232:235],a[48:51]
v_accvgpr_read_b32 v70,a12
v_accvgpr_read_b32 v71,a13
v_mfma_f32_16x16x32_bf16 a[48:51],a[132:135],a[204:207],a[48:51]
buffer_load_dwordx4 v4,s[20:23],s47 offen nt lds
v_mfma_f32_16x16x32_bf16 a[48:51],a[128:131],a[200:203],a[48:51]
v_mul_f32 v84,s61,v84
v_mul_f32 v85,s61,v85
v_mfma_f32_16x16x32_bf16 a[80:83],a[128:131],a[208:211],a[80:83]
v_accvgpr_read_b32 v86,a42
v_accvgpr_read_b32 v87,a43
v_mfma_f32_16x16x32_bf16 a[80:83],a[132:135],a[212:215],a[80:83]
v_mul_f32 v100,s61,v100
v_mul_f32 v101,s61,v101
v_mfma_f32_16x16x32_bf16 a[80:83],a[136:139],a[240:243],a[80:83]
v_accvgpr_read_b32 v102,a74
v_accvgpr_read_b32 v103,a75
v_mfma_f32_16x16x32_bf16 a[80:83],a[140:143],a[244:247],a[80:83]
v_mul_f32 v116,s61,v116
v_mul_f32 v117,s61,v117
v_mfma_f32_16x16x32_bf16 a[112:115],a[140:143],a[252:255],a[112:115]
ds_read_b64_tr_b16 a[140:141],v22 offset:50184
v_accvgpr_read_b32 v118,a106
v_mfma_f32_16x16x32_bf16 a[112:115],a[136:139],a[248:251],a[112:115]
ds_read_b64_tr_b16 a[136:137],v22 offset:33800
v_accvgpr_read_b32 v119,a107
v_mfma_f32_16x16x32_bf16 a[112:115],a[132:135],a[220:223],a[112:115]
ds_read_b64_tr_b16 a[132:133],v22 offset:50176
v_accvgpr_read_b32 v16,a70
v_mfma_f32_16x16x32_bf16 a[112:115],a[128:131],a[216:219],a[112:115]
ds_read_b64_tr_b16 a[128:129],v22 offset:33792
v_accvgpr_read_b32 v17,a71
v_mfma_f32_16x16x32_bf16 a[20:23],a[144:147],a[192:195],a[20:23]
ds_read_b64_tr_b16 a[130:131],v22 offset:35840
v_mul_f32 v98,s61,v98
v_mfma_f32_16x16x32_bf16 a[20:23],a[148:151],a[196:199],a[20:23]
ds_read_b64_tr_b16 a[134:135],v22 offset:52224
v_mul_f32 v99,s61,v99
v_mfma_f32_16x16x32_bf16 a[20:23],a[152:155],a[224:227],a[20:23]
ds_read_b64_tr_b16 a[138:139],v22 offset:35848
v_accvgpr_read_b32 v12,a102
v_mfma_f32_16x16x32_bf16 a[20:23],a[156:159],a[228:231],a[20:23]
ds_read_b64_tr_b16 a[142:143],v22 offset:52232
v_accvgpr_read_b32 v13,a103
v_mfma_f32_16x16x32_bf16 a[52:55],a[156:159],a[236:239],a[52:55]
v_mul_f32 v114,s61,v114
v_mul_f32 v115,s61,v115
v_mfma_f32_16x16x32_bf16 a[52:55],a[152:155],a[232:235],a[52:55]
v_accvgpr_read_b32 v14,a14
v_accvgpr_read_b32 v15,a15
v_mfma_f32_16x16x32_bf16 a[52:55],a[148:151],a[204:207],a[52:55]
buffer_load_dwordx4 v4,s[20:23],s47 offen offset:1024 nt lds
v_mfma_f32_16x16x32_bf16 a[52:55],a[144:147],a[200:203],a[52:55]
v_mul_f32 v70,s61,v70
v_mul_f32 v71,s61,v71
v_mfma_f32_16x16x32_bf16 a[84:87],a[144:147],a[208:211],a[84:87]
v_cvt_pk_bf16_f32 v84,v84,v85
v_mul_f32 v86,s61,v86
v_mfma_f32_16x16x32_bf16 a[84:87],a[148:151],a[212:215],a[84:87]
v_mul_f32 v87,s61,v87
v_cvt_pk_bf16_f32 v100,v100,v101
v_mfma_f32_16x16x32_bf16 a[84:87],a[152:155],a[240:243],a[84:87]
v_mul_f32 v102,s61,v102
v_mul_f32 v103,s61,v103
v_mfma_f32_16x16x32_bf16 a[84:87],a[156:159],a[244:247],a[84:87]
v_cvt_pk_bf16_f32 v116,v116,v117
v_mul_f32 v118,s61,v118
v_mfma_f32_16x16x32_bf16 a[116:119],a[156:159],a[252:255],a[116:119]
ds_read_b64_tr_b16 a[156:157],v22 offset:50440
v_mul_f32 v119,s61,v119
v_mfma_f32_16x16x32_bf16 a[116:119],a[152:155],a[248:251],a[116:119]
ds_read_b64_tr_b16 a[152:153],v22 offset:34056
v_mul_f32 v16,s61,v16
v_mfma_f32_16x16x32_bf16 a[116:119],a[148:151],a[220:223],a[116:119]
ds_read_b64_tr_b16 a[148:149],v22 offset:50432
v_mul_f32 v17,s61,v17
v_mfma_f32_16x16x32_bf16 a[116:119],a[144:147],a[216:219],a[116:119]
s_waitcnt lgkmcnt(3)
ds_read_b64_tr_b16 a[144:145],v22 offset:34048
v_cvt_pk_bf16_f32 v98,v98,v99
v_mfma_f32_16x16x32_bf16 a[24:27],a[160:163],a[192:195],a[24:27]
)A"
R"A(ds_read_b64_tr_b16 a[146:147],v22 offset:36096
v_mul_f32 v12,s61,v12
v_mfma_f32_16x16x32_bf16 a[24:27],a[164:167],a[196:199],a[24:27]
ds_read_b64_tr_b16 a[150:151],v22 offset:52480
v_mul_f32 v13,s61,v13
v_mfma_f32_16x16x32_bf16 a[24:27],a[168:171],a[224:227],a[24:27]
ds_read_b64_tr_b16 a[154:155],v22 offset:36104
v_cvt_pk_bf16_f32 v114,v114,v115
v_mfma_f32_16x16x32_bf16 a[24:27],a[172:175],a[228:231],a[24:27]
ds_read_b64_tr_b16 a[158:159],v22 offset:52488
v_mul_f32 v14,s61,v14
v_mfma_f32_16x16x32_bf16 a[56:59],a[172:175],a[236:239],a[56:59]
v_mul_f32 v15,s61,v15
v_cvt_pk_bf16_f32 v70,v70,v71
v_mfma_f32_16x16x32_bf16 a[56:59],a[168:171],a[232:235],a[56:59]
v_cvt_pk_bf16_f32 v85,v86,v87
v_cvt_pk_bf16_f32 v101,v102,v103
v_mfma_f32_16x16x32_bf16 a[56:59],a[164:167],a[204:207],a[56:59]
buffer_load_dwordx4 v5,s[20:23],s47 offen offset:2048 nt lds
v_mfma_f32_16x16x32_bf16 a[56:59],a[160:163],a[200:203],a[56:59]
v_cvt_pk_bf16_f32 v117,v118,v119
v_accvgpr_read_b32 v72,a16
v_mfma_f32_16x16x32_bf16 a[88:91],a[160:163],a[208:211],a[88:91]
v_accvgpr_read_b32 v73,a17
v_accvgpr_read_b32 v88,a48
v_mfma_f32_16x16x32_bf16 a[88:91],a[164:167],a[212:215],a[88:91]
v_accvgpr_read_b32 v89,a49
v_accvgpr_read_b32 v104,a80
v_mfma_f32_16x16x32_bf16 a[88:91],a[168:171],a[240:243],a[88:91]
v_accvgpr_read_b32 v105,a81
v_cvt_pk_bf16_f32 v99,v16,v17
v_mfma_f32_16x16x32_bf16 a[88:91],a[172:175],a[244:247],a[88:91]
v_cvt_pk_bf16_f32 v115,v12,v13
v_cvt_pk_bf16_f32 v71,v14,v15
v_mfma_f32_16x16x32_bf16 a[120:123],a[172:175],a[252:255],a[120:123]
ds_read_b64_tr_b16 a[172:173],v22 offset:50696
v_accvgpr_read_b32 v86,a44
v_mfma_f32_16x16x32_bf16 a[120:123],a[168:171],a[248:251],a[120:123]
ds_read_b64_tr_b16 a[168:169],v22 offset:34312
v_accvgpr_read_b32 v87,a45
v_mfma_f32_16x16x32_bf16 a[120:123],a[164:167],a[220:223],a[120:123]
ds_read_b64_tr_b16 a[164:165],v22 offset:50688
v_accvgpr_read_b32 v102,a76
v_mfma_f32_16x16x32_bf16 a[120:123],a[160:163],a[216:219],a[120:123]
ds_read_b64_tr_b16 a[160:161],v22 offset:34304
v_accvgpr_read_b32 v103,a77
v_mfma_f32_16x16x32_bf16 a[28:31],a[176:179],a[192:195],a[28:31]
ds_read_b64_tr_b16 a[162:163],v22 offset:36352
v_accvgpr_read_b32 v118,a108
v_mfma_f32_16x16x32_bf16 a[28:31],a[180:183],a[196:199],a[28:31]
ds_read_b64_tr_b16 a[166:167],v22 offset:52736
v_accvgpr_read_b32 v119,a109
v_mfma_f32_16x16x32_bf16 a[28:31],a[184:187],a[224:227],a[28:31]
ds_read_b64_tr_b16 a[170:171],v22 offset:36360
v_mul_f32 v72,s61,v72
v_mfma_f32_16x16x32_bf16 a[28:31],a[188:191],a[228:231],a[28:31]
ds_read_b64_tr_b16 a[174:175],v22 offset:52744
v_mul_f32 v73,s61,v73
v_mfma_f32_16x16x32_bf16 a[60:63],a[188:191],a[236:239],a[60:63]
v_accvgpr_read_b32 v74,a18
v_accvgpr_read_b32 v75,a19
v_mfma_f32_16x16x32_bf16 a[60:63],a[184:187],a[232:235],a[60:63]
v_mul_f32 v88,s61,v88
v_mul_f32 v89,s61,v89
v_mfma_f32_16x16x32_bf16 a[60:63],a[180:183],a[204:207],a[60:63]
buffer_load_dwordx4 v5,s[20:23],s47 offen offset:3072 nt lds
v_mfma_f32_16x16x32_bf16 a[60:63],a[176:179],a[200:203],a[60:63]
v_accvgpr_read_b32 v90,a50
v_accvgpr_read_b32 v91,a51
v_mfma_f32_16x16x32_bf16 a[92:95],a[176:179],a[208:211],a[92:95]
v_mul_f32 v104,s61,v104
v_mul_f32 v105,s61,v105
v_mfma_f32_16x16x32_bf16 a[92:95],a[180:183],a[212:215],a[92:95]
v_accvgpr_read_b32 v106,a82
v_accvgpr_read_b32 v107,a83
v_mfma_f32_16x16x32_bf16 a[92:95],a[184:187],a[240:243],a[92:95]
v_accvgpr_read_b32 v16,a46
v_accvgpr_read_b32 v17,a47
v_mfma_f32_16x16x32_bf16 a[92:95],a[188:191],a[244:247],a[92:95]
v_mul_f32 v86,s61,v86
v_mul_f32 v87,s61,v87
v_mfma_f32_16x16x32_bf16 a[124:127],a[188:191],a[252:255],a[124:127]
ds_read_b64_tr_b16 a[188:189],v22 offset:50952
v_accvgpr_read_b32 v12,a78
v_mfma_f32_16x16x32_bf16 a[124:127],a[184:187],a[248:251],a[124:127]
ds_read_b64_tr_b16 a[184:185],v22 offset:34568
v_accvgpr_read_b32 v13,a79
v_mfma_f32_16x16x32_bf16 a[124:127],a[180:183],a[220:223],a[124:127]
ds_read_b64_tr_b16 a[180:181],v22 offset:50944
v_mul_f32 v102,s61,v102
v_mfma_f32_16x16x32_bf16 a[124:127],a[176:179],a[216:219],a[124:127]
ds_read_b64_tr_b16 a[176:177],v22 offset:34560
v_mul_f32 v103,s61,v103
v_mfma_f32_16x16x32_bf16 v[128:131],a[128:131],a[224:227],v[128:131]
ds_read_b64_tr_b16 a[178:179],v22 offset:36608
v_accvgpr_read_b32 v14,a110
v_mfma_f32_16x16x32_bf16 v[128:131],a[132:135],a[228:231],v[128:131]
ds_read_b64_tr_b16 a[182:183],v22 offset:52992
v_accvgpr_read_b32 v15,a111
v_mfma_f32_16x16x32_bf16 v[136:139],a[132:135],a[236:239],v[136:139]
ds_read_b64_tr_b16 a[186:187],v22 offset:36616
v_mul_f32 v118,s61,v118
v_mfma_f32_16x16x32_bf16 v[136:139],a[128:131],a[232:235],v[136:139]
s_add_u32 m0,131072,s52
ds_read_b64_tr_b16 a[190:191],v22 offset:53000
v_mul_f32 v119,s61,v119
v_mfma_f32_16x16x32_bf16 v[144:147],a[128:131],a[240:243],v[144:147]
v_cvt_pk_bf16_f32 v72,v72,v73
v_mul_f32 v74,s61,v74
v_mfma_f32_16x16x32_bf16 v[144:147],a[132:135],a[244:247],v[144:147]
v_mul_f32 v75,s61,v75
v_cvt_pk_bf16_f32 v88,v88,v89
v_mfma_f32_16x16x32_bf16 v[152:155],a[132:135],a[252:255],v[152:155]
buffer_load_dwordx4 v4,s[24:27],s47 offen nt lds
v_mfma_f32_16x16x32_bf16 v[152:155],a[128:131],a[248:251],v[152:155]
v_mul_f32 v90,s61,v90
v_mul_f32 v91,s61,v91
v_mfma_f32_16x16x32_bf16 v[132:135],a[136:139],a[224:227],v[132:135]
v_cvt_pk_bf16_f32 v104,v104,v105
v_mul_f32 v106,s61,v106
v_mfma_f32_16x16x32_bf16 v[132:135],a[140:143],a[228:231],v[132:135]
v_mul_f32 v107,s61,v107
v_mul_f32 v16,s61,v16
v_mfma_f32_16x16x32_bf16 v[140:143],a[140:143],a[236:239],v[140:143]
v_mul_f32 v17,s61,v17
v_cvt_pk_bf16_f32 v86,v86,v87
v_mfma_f32_16x16x32_bf16 v[140:143],a[136:139],a[232:235],v[140:143]
v_mul_f32 v12,s61,v12
v_mul_f32 v13,s61,v13
v_mfma_f32_16x16x32_bf16 v[148:151],a[136:139],a[240:243],v[148:151]
v_cvt_pk_bf16_f32 v102,v102,v103
v_mul_f32 v14,s61,v14
v_mfma_f32_16x16x32_bf16 v[148:151],a[140:143],a[244:247],v[148:151]
v_mul_f32 v15,s61,v15
v_cvt_pk_bf16_f32 v118,v118,v119
v_mfma_f32_16x16x32_bf16 v[156:159],a[140:143],a[252:255],v[156:159]
s_waitcnt vmcnt(5) lgkmcnt(0)
v_cvt_pk_bf16_f32 v73,v74,v75
v_cvt_pk_bf16_f32 v89,v90,v91
v_mfma_f32_16x16x32_bf16 v[156:159],a[136:139],a[248:251],v[156:159]
s_barrier
ds_read_b128 v[48:51],v18
v_cvt_pk_bf16_f32 v105,v106,v107
v_mfma_f32_16x16x32_bf16 v[160:163],a[144:147],a[224:227],v[160:163]
v_cvt_pk_bf16_f32 v87,v16,v17
v_cvt_pk_bf16_f32 v103,v12,v13
v_mfma_f32_16x16x32_bf16 v[160:163],a[148:151],a[228:231],v[160:163]
ds_read_b128 v[52:55],v18 offset:256
v_cvt_pk_bf16_f32 v119,v14,v15
v_mfma_f32_16x16x32_bf16 v[168:171],a[148:151],a[236:239],v[168:171]
v_accvgpr_read_b32 v74,a20
v_accvgpr_read_b32 v75,a21
v_mfma_f32_16x16x32_bf16 v[168:171],a[144:147],a[232:235],v[168:171]
ds_read_b128 v[56:59],v18 offset:512
v_accvgpr_read_b32 v90,a52
v_mfma_f32_16x16x32_bf16 v[176:179],a[144:147],a[240:243],v[176:179]
v_accvgpr_read_b32 v91,a53
v_accvgpr_read_b32 v106,a84
v_mfma_f32_16x16x32_bf16 v[176:179],a[148:151],a[244:247],v[176:179]
buffer_load_dwordx4 v5,s[24:27],s47 offen offset:1024 nt lds
v_mfma_f32_16x16x32_bf16 v[184:187],a[148:151],a[252:255],v[184:187]
ds_read_b128 v[60:63],v18 offset:768
v_accvgpr_read_b32 v107,a85
v_mfma_f32_16x16x32_bf16 v[184:187],a[144:147],a[248:251],v[184:187]
v_accvgpr_read_b32 v16,a22
v_accvgpr_read_b32 v17,a23
v_mfma_f32_16x16x32_bf16 v[164:167],a[152:155],a[224:227],v[164:167]
v_mul_f32 v74,s61,v74
v_mul_f32 v75,s61,v75
v_mfma_f32_16x16x32_bf16 v[164:167],a[156:159],a[228:231],v[164:167]
v_accvgpr_read_b32 v12,a54
v_accvgpr_read_b32 v13,a55
v_mfma_f32_16x16x32_bf16 v[172:175],a[156:159],a[236:239],v[172:175]
v_mul_f32 v90,s61,v90
v_mul_f32 v91,s61,v91
v_mfma_f32_16x16x32_bf16 v[172:175],a[152:155],a[232:235],v[172:175]
v_accvgpr_read_b32 v14,a86
v_accvgpr_read_b32 v15,a87
v_mfma_f32_16x16x32_bf16 v[180:183],a[152:155],a[240:243],v[180:183]
v_mul_f32 v106,s61,v106
v_mul_f32 v107,s61,v107
v_mfma_f32_16x16x32_bf16 v[180:183],a[156:159],a[244:247],v[180:183]
v_mul_f32 v16,s61,v16
v_mul_f32 v17,s61,v17
v_mfma_f32_16x16x32_bf16 v[188:191],a[156:159],a[252:255],v[188:191]
v_cvt_pk_bf16_f32 v74,v74,v75
v_mul_f32 v12,s61,v12
v_mfma_f32_16x16x32_bf16 v[188:191],a[152:155],a[248:251],v[188:191]
v_mul_f32 v13,s61,v13
v_cvt_pk_bf16_f32 v90,v90,v91
v_mfma_f32_16x16x32_bf16 v[192:195],a[160:163],a[224:227],v[192:195]
v_mul_f32 v14,s61,v14
v_mul_f32 v15,s61,v15
v_mfma_f32_16x16x32_bf16 v[192:195],a[164:167],a[228:231],v[192:195]
v_cvt_pk_bf16_f32 v106,v106,v107
v_cvt_pk_bf16_f32 v75,v16,v17
v_mfma_f32_16x16x32_bf16 v[200:203],a[164:167],a[236:239],v[200:203]
v_cvt_pk_bf16_f32 v91,v12,v13
v_cvt_pk_bf16_f32 v107,v14,v15
v_mfma_f32_16x16x32_bf16 v[200:203],a[160:163],a[232:235],v[200:203]
v_accvgpr_read_b32 v76,a24
v_accvgpr_read_b32 v77,a25
v_accvgpr_read_b32 v78,a26
v_accvgpr_read_b32 v79,a27
v_accvgpr_read_b32 v12,a28
v_accvgpr_read_b32 v13,a29
v_accvgpr_read_b32 v14,a30
v_accvgpr_read_b32 v15,a31
v_pk_mul_f32 v[76:77],s[60:61],v[76:77] op_sel:[1,0] op_sel_hi:[1,1]
v_pk_mul_f32 v[78:79],s[60:61],v[78:79] op_sel:[1,0] op_sel_hi:[1,1]
v_pk_mul_f32 v[12:13],s[60:61],v[12:13] op_sel:[1,0] op_sel_hi:[1,1]
v_pk_mul_f32 v[14:15],s[60:61],v[14:15] op_sel:[1,0] op_sel_hi:[1,1]
v_cvt_pk_bf16_f32 v76,v76,v77
v_cvt_pk_bf16_f32 v77,v78,v79
v_cvt_pk_bf16_f32 v78,v12,v13
v_cvt_pk_bf16_f32 v79,v14,v15
v_mfma_f32_16x16x32_bf16 v[208:211],a[160:163],a[240:243],v[208:211]
v_accvgpr_read_b32 v92,a56
v_accvgpr_read_b32 v93,a57
v_accvgpr_read_b32 v94,a58
v_accvgpr_read_b32 v95,a59
v_accvgpr_read_b32 v12,a60
v_accvgpr_read_b32 v13,a61
v_accvgpr_read_b32 v14,a62
v_accvgpr_read_b32 v15,a63
v_pk_mul_f32 v[92:93],s[60:61],v[92:93] op_sel:[1,0] op_sel_hi:[1,1]
v_pk_mul_f32 v[94:95],s[60:61],v[94:95] op_sel:[1,0] op_sel_hi:[1,1]
v_pk_mul_f32 v[12:13],s[60:61],v[12:13] op_sel:[1,0] op_sel_hi:[1,1]
v_pk_mul_f32 v[14:15],s[60:61],v[14:15] op_sel:[1,0] op_sel_hi:[1,1]
v_cvt_pk_bf16_f32 v92,v92,v93
v_cvt_pk_bf16_f32 v93,v94,v95
v_cvt_pk_bf16_f32 v94,v12,v13
v_cvt_pk_bf16_f32 v95,v14,v15
v_mfma_f32_16x16x32_bf16 v[208:211],a[164:167],a[244:247],v[208:211]
buffer_load_dwordx4 v4,s[24:27],s47 offen offset:2048 nt lds
v_mfma_f32_16x16x32_bf16 v[216:219],a[164:167],a[252:255],v[216:219]
ds_read_b128 a[128:131],v1
v_accvgpr_read_b32 v120,a112
v_mfma_f32_16x16x32_bf16 v[216:219],a[160:163],a[248:251],v[216:219]
v_accvgpr_read_b32 v121,a113
v_mul_f32 v120,s61,v120
v_mfma_f32_16x16x32_bf16 v[196:199],a[168:171],a[224:227],v[196:199]
v_mul_f32 v121,s61,v121
v_accvgpr_read_b32 v122,a114
v_mfma_f32_16x16x32_bf16 v[196:199],a[172:175],a[228:231],v[196:199]
ds_read_b128 a[132:135],v1 offset:256
v_accvgpr_read_b32 v123,a115
v_mfma_f32_16x16x32_bf16 v[204:207],a[172:175],a[236:239],v[204:207]
v_accvgpr_read_b32 v108,a88
v_accvgpr_read_b32 v109,a89
v_accvgpr_read_b32 v110,a90
v_accvgpr_read_b32 v111,a91
v_accvgpr_read_b32 v12,a92
v_accvgpr_read_b32 v13,a93
v_accvgpr_read_b32 v14,a94
v_accvgpr_read_b32 v15,a95
v_pk_mul_f32 v[108:109],s[60:61],v[108:109] op_sel:[1,0] op_sel_hi:[1,1]
v_pk_mul_f32 v[110:111],s[60:61],v[110:111] op_sel:[1,0] op_sel_hi:[1,1]
v_pk_mul_f32 v[12:13],s[60:61],v[12:13] op_sel:[1,0] op_sel_hi:[1,1]
v_pk_mul_f32 v[14:15],s[60:61],v[14:15] op_sel:[1,0] op_sel_hi:[1,1]
v_cvt_pk_bf16_f32 v108,v108,v109
v_cvt_pk_bf16_f32 v109,v110,v111
v_cvt_pk_bf16_f32 v110,v12,v13
v_cvt_pk_bf16_f32 v111,v14,v15
v_mfma_f32_16x16x32_bf16 v[204:207],a[168:171],a[232:235],v[204:207]
v_cvt_pk_bf16_f32 v120,v120,v121
v_mul_f32 v122,s61,v122
v_mfma_f32_16x16x32_bf16 v[212:215],a[168:171],a[240:243],v[212:215]
v_mul_f32 v123,s61,v123
v_cvt_pk_bf16_f32 v121,v122,v123
v_mfma_f32_16x16x32_bf16 v[212:215],a[172:175],a[244:247],v[212:215]
ds_read_b128 a[136:139],v1 offset:512
v_accvgpr_read_b32 v122,a116
v_mfma_f32_16x16x32_bf16 v[220:223],a[172:175],a[252:255],v[220:223]
v_accvgpr_read_b32 v123,a117
v_accvgpr_read_b32 v16,a118
v_mfma_f32_16x16x32_bf16 v[220:223],a[168:171],a[248:251],v[220:223]
v_accvgpr_read_b32 v17,a119
v_mul_f32 v122,s61,v122
v_mfma_f32_16x16x32_bf16 v[224:227],a[176:179],a[224:227],v[224:227]
ds_read_b128 a[140:143],v1 offset:768
v_mul_f32 v123,s61,v123
v_mfma_f32_16x16x32_bf16 v[224:227],a[180:183],a[228:231],v[224:227]
)A"
R"A(v_mul_f32 v16,s61,v16
v_mul_f32 v17,s61,v17
v_mfma_f32_16x16x32_bf16 v[232:235],a[180:183],a[236:239],v[232:235]
v_cvt_pk_bf16_f32 v122,v122,v123
v_cvt_pk_bf16_f32 v123,v16,v17
v_mfma_f32_16x16x32_bf16 v[232:235],a[176:179],a[232:235],v[232:235]
ds_read_b128 a[156:159],v1 offset:4864
v_mfma_f32_16x16x32_bf16 v[240:243],a[176:179],a[240:243],v[240:243]
v_accvgpr_read_b32 v124,a120
v_accvgpr_read_b32 v125,a121
v_accvgpr_read_b32 v126,a122
v_accvgpr_read_b32 v127,a123
v_accvgpr_read_b32 v12,a124
v_accvgpr_read_b32 v13,a125
v_accvgpr_read_b32 v14,a126
v_accvgpr_read_b32 v15,a127
v_pk_mul_f32 v[124:125],s[60:61],v[124:125] op_sel:[1,0] op_sel_hi:[1,1]
v_pk_mul_f32 v[126:127],s[60:61],v[126:127] op_sel:[1,0] op_sel_hi:[1,1]
v_pk_mul_f32 v[12:13],s[60:61],v[12:13] op_sel:[1,0] op_sel_hi:[1,1]
v_pk_mul_f32 v[14:15],s[60:61],v[14:15] op_sel:[1,0] op_sel_hi:[1,1]
v_cvt_pk_bf16_f32 v124,v124,v125
v_cvt_pk_bf16_f32 v125,v126,v127
v_cvt_pk_bf16_f32 v126,v12,v13
v_cvt_pk_bf16_f32 v127,v14,v15
v_mfma_f32_16x16x32_bf16 v[240:243],a[180:183],a[244:247],v[240:243]
buffer_load_dwordx4 v5,s[24:27],s47 offen offset:3072 nt lds
v_mfma_f32_16x16x32_bf16 v[248:251],a[180:183],a[252:255],v[248:251]
s_add_u32 s47,s47,16384
s_and_b32 s47,s47,0x3ffff
ds_read_b128 a[152:155],v1 offset:4608
v_mfma_f32_16x16x32_bf16 v[248:251],a[176:179],a[248:251],v[248:251]
s_or_b32 s47,s47,s46
v_mul_f32 v128,s61,v128
v_mul_f32 v129,s61,v129
v_mul_f32 v130,s61,v130
v_mul_f32 v131,s61,v131
v_cvt_pk_bf16_f32 v128,v128,v129
v_cvt_pk_bf16_f32 v129,v130,v131
v_pk_mul_f32 v[132:133],s[60:61],v[132:133] op_sel:[1,0] op_sel_hi:[1,1]
v_pk_mul_f32 v[134:135],s[60:61],v[134:135] op_sel:[1,0] op_sel_hi:[1,1]
v_pk_mul_f32 v[136:137],s[60:61],v[136:137] op_sel:[1,0] op_sel_hi:[1,1]
v_pk_mul_f32 v[138:139],s[60:61],v[138:139] op_sel:[1,0] op_sel_hi:[1,1]
v_pk_mul_f32 v[140:141],s[60:61],v[140:141] op_sel:[1,0] op_sel_hi:[1,1]
v_pk_mul_f32 v[142:143],s[60:61],v[142:143] op_sel:[1,0] op_sel_hi:[1,1]
v_pk_mul_f32 v[144:145],s[60:61],v[144:145] op_sel:[1,0] op_sel_hi:[1,1]
v_pk_mul_f32 v[146:147],s[60:61],v[146:147] op_sel:[1,0] op_sel_hi:[1,1]
v_pk_mul_f32 v[148:149],s[60:61],v[148:149] op_sel:[1,0] op_sel_hi:[1,1]
v_pk_mul_f32 v[150:151],s[60:61],v[150:151] op_sel:[1,0] op_sel_hi:[1,1]
v_pk_mul_f32 v[152:153],s[60:61],v[152:153] op_sel:[1,0] op_sel_hi:[1,1]
v_pk_mul_f32 v[154:155],s[60:61],v[154:155] op_sel:[1,0] op_sel_hi:[1,1]
v_pk_mul_f32 v[156:157],s[60:61],v[156:157] op_sel:[1,0] op_sel_hi:[1,1]
v_pk_mul_f32 v[158:159],s[60:61],v[158:159] op_sel:[1,0] op_sel_hi:[1,1]
v_mfma_f32_16x16x32_bf16 v[228:231],a[184:187],a[224:227],v[228:231]
s_and_b32 s46,s54,0xfffc0000
ds_read_b128 a[148:151],v1 offset:4352
v_cvt_pk_bf16_f32 v130,v132,v133
v_mfma_f32_16x16x32_bf16 v[228:231],a[188:191],a[228:231],v[228:231]
s_add_u32 s54,s54,16384
v_cvt_pk_bf16_f32 v131,v134,v135
v_cvt_pk_bf16_f32 v136,v136,v137
v_mfma_f32_16x16x32_bf16 v[236:239],a[188:191],a[236:239],v[236:239]
ds_read_b128 a[144:147],v1 offset:4096
v_cvt_pk_bf16_f32 v137,v138,v139
v_mfma_f32_16x16x32_bf16 v[236:239],a[184:187],a[232:235],v[236:239]
v_cvt_pk_bf16_f32 v138,v140,v141
v_cvt_pk_bf16_f32 v139,v142,v143
v_mfma_f32_16x16x32_bf16 v[244:247],a[184:187],a[240:243],v[244:247]
ds_read_b128 a[160:163],v1 offset:8192
v_cvt_pk_bf16_f32 v144,v144,v145
v_mfma_f32_16x16x32_bf16 v[244:247],a[188:191],a[244:247],v[244:247]
v_cvt_pk_bf16_f32 v145,v146,v147
v_cvt_pk_bf16_f32 v146,v148,v149
v_mfma_f32_16x16x32_bf16 v[252:255],a[188:191],a[252:255],v[252:255]
ds_read_b128 a[164:167],v1 offset:8448
v_cvt_pk_bf16_f32 v147,v150,v151
v_mfma_f32_16x16x32_bf16 v[252:255],a[184:187],a[248:251],v[252:255]
s_waitcnt lgkmcnt(4)
v_cvt_pk_bf16_f32 v152,v152,v153
v_cvt_pk_bf16_f32 v153,v154,v155
v_mfma_f32_16x16x32_bf16 v[32:35],v[48:51],v[64:67],0
ds_read_b128 a[168:171],v1 offset:8704
v_cvt_pk_bf16_f32 v154,v156,v157
v_mfma_f32_16x16x32_bf16 v[32:35],v[52:55],v[68:71],v[32:35]
ds_read_b64_tr_b16 a[192:193],v2
v_cvt_pk_bf16_f32 v155,v158,v159
v_mfma_f32_16x16x32_bf16 v[32:35],v[56:59],v[72:75],v[32:35]
v_mul_f32 v160,s61,v160
v_mul_f32 v161,s61,v161
v_mul_f32 v162,s61,v162
v_mul_f32 v163,s61,v163
v_cvt_pk_bf16_f32 v160,v160,v161
v_cvt_pk_bf16_f32 v161,v162,v163
v_pk_mul_f32 v[164:165],s[60:61],v[164:165] op_sel:[1,0] op_sel_hi:[1,1]
v_pk_mul_f32 v[166:167],s[60:61],v[166:167] op_sel:[1,0] op_sel_hi:[1,1]
v_pk_mul_f32 v[168:169],s[60:61],v[168:169] op_sel:[1,0] op_sel_hi:[1,1]
v_pk_mul_f32 v[170:171],s[60:61],v[170:171] op_sel:[1,0] op_sel_hi:[1,1]
v_pk_mul_f32 v[172:173],s[60:61],v[172:173] op_sel:[1,0] op_sel_hi:[1,1]
v_pk_mul_f32 v[174:175],s[60:61],v[174:175] op_sel:[1,0] op_sel_hi:[1,1]
v_pk_mul_f32 v[176:177],s[60:61],v[176:177] op_sel:[1,0] op_sel_hi:[1,1]
v_pk_mul_f32 v[178:179],s[60:61],v[178:179] op_sel:[1,0] op_sel_hi:[1,1]
v_pk_mul_f32 v[180:181],s[60:61],v[180:181] op_sel:[1,0] op_sel_hi:[1,1]
v_pk_mul_f32 v[182:183],s[60:61],v[182:183] op_sel:[1,0] op_sel_hi:[1,1]
v_pk_mul_f32 v[184:185],s[60:61],v[184:185] op_sel:[1,0] op_sel_hi:[1,1]
v_pk_mul_f32 v[186:187],s[60:61],v[186:187] op_sel:[1,0] op_sel_hi:[1,1]
v_pk_mul_f32 v[188:189],s[60:61],v[188:189] op_sel:[1,0] op_sel_hi:[1,1]
v_pk_mul_f32 v[190:191],s[60:61],v[190:191] op_sel:[1,0] op_sel_hi:[1,1]
v_mfma_f32_16x16x32_bf16 v[32:35],v[60:63],v[76:79],v[32:35]
ds_read_b128 a[172:175],v1 offset:8960
v_cvt_pk_bf16_f32 v162,v164,v165
v_mfma_f32_16x16x32_bf16 v[36:39],v[48:51],v[80:83],0
v_cvt_pk_bf16_f32 v163,v166,v167
v_cvt_pk_bf16_f32 v168,v168,v169
v_mfma_f32_16x16x32_bf16 v[36:39],v[52:55],v[84:87],v[36:39]
ds_read_b128 a[188:191],v1 offset:13056
v_cvt_pk_bf16_f32 v169,v170,v171
v_mfma_f32_16x16x32_bf16 v[36:39],v[56:59],v[88:91],v[36:39]
v_cvt_pk_bf16_f32 v170,v172,v173
v_cvt_pk_bf16_f32 v171,v174,v175
v_mfma_f32_16x16x32_bf16 v[36:39],v[60:63],v[92:95],v[36:39]
ds_read_b128 a[184:187],v1 offset:12800
v_cvt_pk_bf16_f32 v176,v176,v177
v_mfma_f32_16x16x32_bf16 v[40:43],v[48:51],v[96:99],0
v_cvt_pk_bf16_f32 v177,v178,v179
v_cvt_pk_bf16_f32 v178,v180,v181
v_mfma_f32_16x16x32_bf16 v[40:43],v[52:55],v[100:103],v[40:43]
s_add_u32 m0,0,s51
v_max3_f32 v32,v32,v33,v34
v_max3_f32 v32,v32,v35,s18
v_mfma_f32_16x16x32_bf16 v[40:43],v[56:59],v[104:107],v[40:43]
v_sub_f32 v35,0,v32
v_sub_f32 v34,0,v32
v_mfma_f32_16x16x32_bf16 v[40:43],v[60:63],v[108:111],v[40:43]
v_sub_f32 v33,0,v32
v_sub_f32 v32,0,v32
v_mfma_f32_16x16x32_bf16 v[44:47],v[48:51],v[112:115],0
buffer_load_dwordx4 v4,s[20:23],s47 offen nt lds
v_mfma_f32_16x16x32_bf16 v[44:47],v[52:55],v[116:119],v[44:47]
ds_read_b128 a[180:183],v1 offset:12544
v_max3_f32 v36,v36,v37,v38
v_mfma_f32_16x16x32_bf16 v[44:47],v[56:59],v[120:123],v[44:47]
v_max3_f32 v36,v36,v39,s19
v_sub_f32 v39,0,v36
v_mfma_f32_16x16x32_bf16 v[44:47],v[60:63],v[124:127],v[44:47]
ds_read_b128 a[176:179],v1 offset:12288
v_sub_f32 v38,0,v36
v_mfma_f32_16x16x32_bf16 v[132:135],a[128:131],v[64:67],v[32:35]
v_sub_f32 v37,0,v36
v_sub_f32 v36,0,v36
v_mfma_f32_16x16x32_bf16 v[132:135],a[132:135],v[68:71],v[132:135]
v_cvt_pk_bf16_f32 v179,v182,v183
v_cvt_pk_bf16_f32 v184,v184,v185
v_mfma_f32_16x16x32_bf16 v[132:135],a[136:139],v[72:75],v[132:135]
v_cvt_pk_bf16_f32 v185,v186,v187
v_cvt_pk_bf16_f32 v186,v188,v189
v_mfma_f32_16x16x32_bf16 v[132:135],a[140:143],v[76:79],v[132:135]
v_cvt_pk_bf16_f32 v187,v190,v191
v_max3_f32 v40,v40,v41,v42
v_mfma_f32_16x16x32_bf16 v[140:143],a[156:159],v[76:79],v[32:35]
v_max3_f32 v40,v40,v43,s100
v_sub_f32 v43,0,v40
v_mfma_f32_16x16x32_bf16 v[140:143],a[152:155],v[72:75],v[140:143]
s_waitcnt lgkmcnt(1)
buffer_load_dwordx4 v4,s[20:23],s47 offen offset:1024 nt lds
v_mfma_f32_16x16x32_bf16 v[140:143],a[148:151],v[68:71],v[140:143]
v_sub_f32 v42,0,v40
v_sub_f32 v41,0,v40
v_mfma_f32_16x16x32_bf16 v[140:143],a[144:147],v[64:67],v[140:143]
ds_read_b64_tr_b16 a[194:195],v2 offset:4096
v_sub_f32 v40,0,v40
v_mfma_f32_16x16x32_bf16 v[148:151],a[160:163],v[64:67],v[32:35]
v_mul_f32 v192,s61,v192
v_mul_f32 v193,s61,v193
v_mul_f32 v194,s61,v194
v_mul_f32 v195,s61,v195
v_cvt_pk_bf16_f32 v192,v192,v193
v_cvt_pk_bf16_f32 v193,v194,v195
v_pk_mul_f32 v[196:197],s[60:61],v[196:197] op_sel:[1,0] op_sel_hi:[1,1]
v_pk_mul_f32 v[198:199],s[60:61],v[198:199] op_sel:[1,0] op_sel_hi:[1,1]
v_pk_mul_f32 v[200:201],s[60:61],v[200:201] op_sel:[1,0] op_sel_hi:[1,1]
v_pk_mul_f32 v[202:203],s[60:61],v[202:203] op_sel:[1,0] op_sel_hi:[1,1]
v_pk_mul_f32 v[204:205],s[60:61],v[204:205] op_sel:[1,0] op_sel_hi:[1,1]
v_pk_mul_f32 v[206:207],s[60:61],v[206:207] op_sel:[1,0] op_sel_hi:[1,1]
v_pk_mul_f32 v[208:209],s[60:61],v[208:209] op_sel:[1,0] op_sel_hi:[1,1]
v_pk_mul_f32 v[210:211],s[60:61],v[210:211] op_sel:[1,0] op_sel_hi:[1,1]
v_pk_mul_f32 v[212:213],s[60:61],v[212:213] op_sel:[1,0] op_sel_hi:[1,1]
v_pk_mul_f32 v[214:215],s[60:61],v[214:215] op_sel:[1,0] op_sel_hi:[1,1]
v_pk_mul_f32 v[216:217],s[60:61],v[216:217] op_sel:[1,0] op_sel_hi:[1,1]
v_pk_mul_f32 v[218:219],s[60:61],v[218:219] op_sel:[1,0] op_sel_hi:[1,1]
v_pk_mul_f32 v[220:221],s[60:61],v[220:221] op_sel:[1,0] op_sel_hi:[1,1]
v_pk_mul_f32 v[222:223],s[60:61],v[222:223] op_sel:[1,0] op_sel_hi:[1,1]
v_mfma_f32_16x16x32_bf16 v[148:151],a[164:167],v[68:71],v[148:151]
v_cvt_pk_bf16_f32 v194,v196,v197
v_cvt_pk_bf16_f32 v195,v198,v199
v_mfma_f32_16x16x32_bf16 v[148:151],a[168:171],v[72:75],v[148:151]
v_cvt_pk_bf16_f32 v200,v200,v201
v_cvt_pk_bf16_f32 v201,v202,v203
v_mfma_f32_16x16x32_bf16 v[148:151],a[172:175],v[76:79],v[148:151]
v_cvt_pk_bf16_f32 v202,v204,v205
v_cvt_pk_bf16_f32 v203,v206,v207
v_mfma_f32_16x16x32_bf16 v[156:159],a[188:191],v[76:79],v[32:35]
v_cvt_pk_bf16_f32 v208,v208,v209
v_cvt_pk_bf16_f32 v209,v210,v211
v_mfma_f32_16x16x32_bf16 v[156:159],a[184:187],v[72:75],v[156:159]
v_cvt_pk_bf16_f32 v210,v212,v213
v_cvt_pk_bf16_f32 v211,v214,v215
v_mfma_f32_16x16x32_bf16 v[156:159],a[180:183],v[68:71],v[156:159]
s_waitcnt lgkmcnt(1)
buffer_load_dwordx4 v5,s[20:23],s47 offen offset:2048 nt lds
v_mfma_f32_16x16x32_bf16 v[156:159],a[176:179],v[64:67],v[156:159]
ds_read_b64_tr_b16 a[196:197],v2 offset:8192
ds_read_b64_tr_b16 a[198:199],v2 offset:12288
v_mfma_f32_16x16x32_bf16 v[164:167],a[128:131],v[80:83],v[36:39]
ds_read_b64_tr_b16 a[204:205],v3 offset:8192
v_cvt_pk_bf16_f32 v216,v216,v217
v_mfma_f32_16x16x32_bf16 v[164:167],a[132:135],v[84:87],v[164:167]
ds_read_b64_tr_b16 a[206:207],v3 offset:12288
v_cvt_pk_bf16_f32 v217,v218,v219
v_mfma_f32_16x16x32_bf16 v[164:167],a[136:139],v[88:91],v[164:167]
ds_read_b64_tr_b16 a[200:201],v3
v_cvt_pk_bf16_f32 v218,v220,v221
v_mfma_f32_16x16x32_bf16 v[164:167],a[140:143],v[92:95],v[164:167]
ds_read_b64_tr_b16 a[202:203],v3 offset:4096
v_cvt_pk_bf16_f32 v219,v222,v223
v_mfma_f32_16x16x32_bf16 v[172:175],a[156:159],v[92:95],v[36:39]
ds_read_b64_tr_b16 a[208:209],v2 offset:256
ds_read_b64_tr_b16 a[210:211],v2 offset:4352
v_mfma_f32_16x16x32_bf16 v[172:175],a[152:155],v[88:91],v[172:175]
v_exp_f32 v132,v132
v_mfma_f32_16x16x32_bf16 v[172:175],a[148:151],v[84:87],v[172:175]
v_exp_f32 v133,v133
v_mfma_f32_16x16x32_bf16 v[172:175],a[144:147],v[80:83],v[172:175]
buffer_load_dwordx4 v5,s[20:23],s47 offen offset:3072 nt lds
v_mfma_f32_16x16x32_bf16 v[180:183],a[160:163],v[80:83],v[36:39]
v_exp_f32 v134,v134
v_mfma_f32_16x16x32_bf16 v[180:183],a[164:167],v[84:87],v[180:183]
v_exp_f32 v135,v135
v_mfma_f32_16x16x32_bf16 v[180:183],a[168:171],v[88:91],v[180:183]
ds_read_b64_tr_b16 a[212:213],v2 offset:8448
ds_read_b64_tr_b16 a[214:215],v2 offset:12544
v_mfma_f32_16x16x32_bf16 v[180:183],a[172:175],v[92:95],v[180:183]
v_cvt_pk_bf16_f32 v132,v132,v133
v_cvt_pk_bf16_f32 v133,v134,v135
v_mfma_f32_16x16x32_bf16 v[188:191],a[188:191],v[92:95],v[36:39]
v_exp_f32 v140,v140
v_mfma_f32_16x16x32_bf16 v[188:191],a[184:187],v[88:91],v[188:191]
s_add_u32 m0,0,s52
v_exp_f32 v141,v141
v_mfma_f32_16x16x32_bf16 v[188:191],a[180:183],v[84:87],v[188:191]
v_exp_f32 v142,v142
v_mfma_f32_16x16x32_bf16 v[188:191],a[176:179],v[80:83],v[188:191]
v_exp_f32 v143,v143
v_mfma_f32_16x16x32_bf16 v[196:199],a[128:131],v[96:99],v[40:43]
buffer_load_dwordx4 v4,s[24:27],s47 offen nt lds
v_mfma_f32_16x16x32_bf16 v[196:199],a[132:135],v[100:103],v[196:199]
)A"
R"A(v_exp_f32 v148,v148
v_mfma_f32_16x16x32_bf16 v[196:199],a[136:139],v[104:107],v[196:199]
v_exp_f32 v149,v149
v_mfma_f32_16x16x32_bf16 v[196:199],a[140:143],v[108:111],v[196:199]
v_exp_f32 v150,v150
v_mfma_f32_16x16x32_bf16 v[204:207],a[156:159],v[108:111],v[40:43]
v_exp_f32 v151,v151
v_mfma_f32_16x16x32_bf16 v[204:207],a[152:155],v[104:107],v[204:207]
ds_read_b64_tr_b16 a[220:221],v3 offset:8448
ds_read_b64_tr_b16 a[222:223],v3 offset:12544
v_mfma_f32_16x16x32_bf16 v[204:207],a[148:151],v[100:103],v[204:207]
v_cvt_pk_bf16_f32 v134,v140,v141
v_cvt_pk_bf16_f32 v135,v142,v143
v_mfma_f32_16x16x32_bf16 v[204:207],a[144:147],v[96:99],v[204:207]
v_cvt_pk_bf16_f32 v148,v148,v149
v_cvt_pk_bf16_f32 v149,v150,v151
v_mfma_f32_16x16x32_bf16 v[212:215],a[160:163],v[96:99],v[40:43]
v_exp_f32 v156,v156
v_mfma_f32_16x16x32_bf16 v[212:215],a[164:167],v[100:103],v[212:215]
buffer_load_dwordx4 v5,s[24:27],s47 offen offset:1024 nt lds
v_mfma_f32_16x16x32_bf16 v[212:215],a[168:171],v[104:107],v[212:215]
v_exp_f32 v157,v157
v_mfma_f32_16x16x32_bf16 v[212:215],a[172:175],v[108:111],v[212:215]
v_exp_f32 v158,v158
v_mfma_f32_16x16x32_bf16 v[220:223],a[188:191],v[108:111],v[40:43]
v_exp_f32 v159,v159
v_mfma_f32_16x16x32_bf16 v[220:223],a[184:187],v[104:107],v[220:223]
ds_read_b64_tr_b16 a[216:217],v3 offset:256
ds_read_b64_tr_b16 a[218:219],v3 offset:4352
v_mfma_f32_16x16x32_bf16 v[220:223],a[180:183],v[100:103],v[220:223]
v_cvt_pk_bf16_f32 v150,v156,v157
v_cvt_pk_bf16_f32 v151,v158,v159
v_mfma_f32_16x16x32_bf16 v[220:223],a[176:179],v[96:99],v[220:223]
s_waitcnt lgkmcnt(2)
ds_read_b64_tr_b16 a[224:225],v2 offset:512
ds_read_b64_tr_b16 a[226:227],v2 offset:4608
v_mfma_f32_16x16x32_bf16 a[0:3],a[192:195],v[132:135],0
ds_read_b64_tr_b16 a[228:229],v2 offset:8704
ds_read_b64_tr_b16 a[230:231],v2 offset:12800
v_mfma_f32_16x16x32_bf16 a[0:3],a[196:199],v[148:151],a[0:3]
ds_read_b64_tr_b16 a[236:237],v3 offset:8704
ds_read_b64_tr_b16 a[238:239],v3 offset:12800
v_mfma_f32_16x16x32_bf16 a[4:7],a[204:207],v[148:151],0
ds_read_b64_tr_b16 a[232:233],v3 offset:512
ds_read_b64_tr_b16 a[234:235],v3 offset:4608
v_mfma_f32_16x16x32_bf16 a[4:7],a[200:203],v[132:135],a[4:7]
ds_read_b64_tr_b16 a[240:241],v2 offset:768
ds_read_b64_tr_b16 a[242:243],v2 offset:4864
v_mfma_f32_16x16x32_bf16 a[8:11],a[208:211],v[132:135],0
ds_read_b64_tr_b16 a[244:245],v2 offset:8960
ds_read_b64_tr_b16 a[246:247],v2 offset:13056
v_mfma_f32_16x16x32_bf16 a[8:11],a[212:215],v[148:151],a[8:11]
s_memrealtime s[76:77]
buffer_load_dwordx4 v4,s[24:27],s47 offen offset:2048 nt lds
ds_read_b64_tr_b16 a[252:253],v3 offset:8960
ds_read_b64_tr_b16 a[254:255],v3 offset:13056
v_mfma_f32_16x16x32_bf16 a[12:15],a[220:223],v[148:151],0
s_waitcnt lgkmcnt(14)
ds_read_b64_tr_b16 a[248:249],v3 offset:768
ds_read_b64_tr_b16 a[250:251],v3 offset:4864
v_mfma_f32_16x16x32_bf16 a[12:15],a[216:219],v[132:135],a[12:15]
s_waitcnt lgkmcnt(14)
v_mfma_f32_16x16x32_bf16 a[16:19],a[224:227],v[132:135],0
s_waitcnt lgkmcnt(12)
v_mfma_f32_16x16x32_bf16 a[16:19],a[228:231],v[148:151],a[16:19]
s_waitcnt lgkmcnt(10)
v_mfma_f32_16x16x32_bf16 a[20:23],a[236:239],v[148:151],0
s_waitcnt lgkmcnt(8)
v_mfma_f32_16x16x32_bf16 a[20:23],a[232:235],v[132:135],a[20:23]
s_waitcnt lgkmcnt(0)
v_max3_f32 v44,v44,v45,v46
v_max3_f32 v44,v44,v47,s101
v_mfma_f32_16x16x32_bf16 a[24:27],a[240:243],v[132:135],0
s_add_u32 s78,s76,s60
buffer_load_dwordx4 v5,s[24:27],s47 offen offset:3072 nt lds
v_mfma_f32_16x16x32_bf16 a[24:27],a[244:247],v[148:151],a[24:27]
s_add_u32 s47,s47,16384
v_sub_f32 v47,0,v44
v_sub_f32 v46,0,v44
v_mfma_f32_16x16x32_bf16 a[28:31],a[252:255],v[148:151],0
s_and_b32 s47,s47,0x3ffff
v_sub_f32 v45,0,v44
v_sub_f32 v44,0,v44
v_mfma_f32_16x16x32_bf16 a[28:31],a[248:251],v[132:135],a[28:31]
v_mul_f32 v224,s61,v224
v_mul_f32 v225,s61,v225
v_mul_f32 v226,s61,v226
v_mul_f32 v227,s61,v227
v_cvt_pk_bf16_f32 v224,v224,v225
v_cvt_pk_bf16_f32 v225,v226,v227
v_pk_mul_f32 v[228:229],s[60:61],v[228:229] op_sel:[1,0] op_sel_hi:[1,1]
v_pk_mul_f32 v[230:231],s[60:61],v[230:231] op_sel:[1,0] op_sel_hi:[1,1]
v_pk_mul_f32 v[232:233],s[60:61],v[232:233] op_sel:[1,0] op_sel_hi:[1,1]
v_pk_mul_f32 v[234:235],s[60:61],v[234:235] op_sel:[1,0] op_sel_hi:[1,1]
v_pk_mul_f32 v[236:237],s[60:61],v[236:237] op_sel:[1,0] op_sel_hi:[1,1]
v_pk_mul_f32 v[238:239],s[60:61],v[238:239] op_sel:[1,0] op_sel_hi:[1,1]
v_pk_mul_f32 v[240:241],s[60:61],v[240:241] op_sel:[1,0] op_sel_hi:[1,1]
v_pk_mul_f32 v[242:243],s[60:61],v[242:243] op_sel:[1,0] op_sel_hi:[1,1]
v_pk_mul_f32 v[244:245],s[60:61],v[244:245] op_sel:[1,0] op_sel_hi:[1,1]
v_pk_mul_f32 v[246:247],s[60:61],v[246:247] op_sel:[1,0] op_sel_hi:[1,1]
v_pk_mul_f32 v[248:249],s[60:61],v[248:249] op_sel:[1,0] op_sel_hi:[1,1]
v_pk_mul_f32 v[250:251],s[60:61],v[250:251] op_sel:[1,0] op_sel_hi:[1,1]
v_pk_mul_f32 v[252:253],s[60:61],v[252:253] op_sel:[1,0] op_sel_hi:[1,1]
v_pk_mul_f32 v[254:255],s[60:61],v[254:255] op_sel:[1,0] op_sel_hi:[1,1]
v_mfma_f32_16x16x32_bf16 v[48:51],v[28:31],v[132:135],0
s_or_b32 s47,s47,s46
v_cvt_pk_bf16_f32 v226,v228,v229
v_cvt_pk_bf16_f32 v227,v230,v231
v_mfma_f32_16x16x32_bf16 v[48:51],v[28:31],v[148:151],v[48:51]
s_and_b32 s46,s54,0xfffc0000
v_cvt_pk_bf16_f32 v232,v232,v233
v_cvt_pk_bf16_f32 v233,v234,v235
v_mfma_f32_16x16x32_bf16 v[228:231],a[128:131],v[112:115],v[44:47]
s_waitcnt vmcnt(8)
v_cvt_pk_bf16_f32 v234,v236,v237
v_cvt_pk_bf16_f32 v235,v238,v239
v_mfma_f32_16x16x32_bf16 v[228:231],a[132:135],v[116:119],v[228:231]
s_barrier
s_add_u32 m0,s49,s51
v_cvt_pk_bf16_f32 v240,v240,v241
v_mfma_f32_16x16x32_bf16 v[228:231],a[136:139],v[120:123],v[228:231]
v_cvt_pk_bf16_f32 v241,v242,v243
v_cvt_pk_bf16_f32 v242,v244,v245
v_mfma_f32_16x16x32_bf16 v[228:231],a[140:143],v[124:127],v[228:231]
v_cvt_pk_bf16_f32 v243,v246,v247
v_cvt_pk_bf16_f32 v248,v248,v249
v_mfma_f32_16x16x32_bf16 v[236:239],a[156:159],v[124:127],v[44:47]
buffer_load_dwordx4 v4,s[20:23],s47 offen nt lds
v_mfma_f32_16x16x32_bf16 v[236:239],a[152:155],v[120:123],v[236:239]
v_cvt_pk_bf16_f32 v249,v250,v251
v_cvt_pk_bf16_f32 v250,v252,v253
v_mfma_f32_16x16x32_bf16 v[236:239],a[148:151],v[116:119],v[236:239]
v_cvt_pk_bf16_f32 v251,v254,v255
v_add_u32 v1,s48,v1
v_mfma_f32_16x16x32_bf16 v[236:239],a[144:147],v[112:115],v[236:239]
v_exp_f32 v164,v164
v_mfma_f32_16x16x32_bf16 v[244:247],a[160:163],v[112:115],v[44:47]
v_exp_f32 v165,v165
v_mfma_f32_16x16x32_bf16 v[244:247],a[164:167],v[116:119],v[244:247]
v_exp_f32 v166,v166
v_mfma_f32_16x16x32_bf16 v[244:247],a[168:171],v[120:123],v[244:247]
v_exp_f32 v167,v167
v_mfma_f32_16x16x32_bf16 v[244:247],a[172:175],v[124:127],v[244:247]
v_cvt_pk_bf16_f32 v164,v164,v165
v_cvt_pk_bf16_f32 v165,v166,v167
v_mfma_f32_16x16x32_bf16 v[252:255],a[188:191],v[124:127],v[44:47]
v_exp_f32 v172,v172
v_exp_f32 v173,v173
v_exp_f32 v174,v174
v_exp_f32 v175,v175
v_exp_f32 v180,v180
v_exp_f32 v181,v181
v_mfma_f32_16x16x32_bf16 v[252:255],a[184:187],v[120:123],v[252:255]
v_exp_f32 v182,v182
v_exp_f32 v183,v183
v_cvt_pk_bf16_f32 v166,v172,v173
v_cvt_pk_bf16_f32 v167,v174,v175
v_cvt_pk_bf16_f32 v180,v180,v181
v_exp_f32 v188,v188
v_exp_f32 v189,v189
v_exp_f32 v190,v190
v_exp_f32 v191,v191
v_mfma_f32_16x16x32_bf16 v[252:255],a[180:183],v[116:119],v[252:255]
v_cvt_pk_bf16_f32 v181,v182,v183
v_cvt_pk_bf16_f32 v182,v188,v189
v_cvt_pk_bf16_f32 v183,v190,v191
v_mfma_f32_16x16x32_bf16 v[252:255],a[176:179],v[112:115],v[252:255]
ds_read_b128 a[128:131],v1
v_add_u32 v2,s48,v2
v_mfma_f32_16x16x32_bf16 a[32:35],a[192:195],v[164:167],0
v_exp_f32 v196,v196
v_mfma_f32_16x16x32_bf16 a[32:35],a[196:199],v[180:183],a[32:35]
ds_read_b128 a[132:135],v1 offset:256
v_add_u32 v3,s48,v3
v_mfma_f32_16x16x32_bf16 a[36:39],a[204:207],v[180:183],0
s_sub_u32 s48,32768,s62
buffer_load_dwordx4 v4,s[20:23],s47 offen offset:1024 nt lds
v_mfma_f32_16x16x32_bf16 a[36:39],a[200:203],v[164:167],a[36:39]
s_add_u32 s50,s50,s48
ds_read_b128 a[136:139],v1 offset:512
v_mfma_f32_16x16x32_bf16 a[40:43],a[208:211],v[164:167],0
v_exp_f32 v197,v197
v_mfma_f32_16x16x32_bf16 a[40:43],a[212:215],v[180:183],a[40:43]
ds_read_b128 a[140:143],v1 offset:768
v_cvt_pk_bf16_f32 v196,v196,v197
v_mfma_f32_16x16x32_bf16 a[44:47],a[220:223],v[180:183],0
v_exp_f32 v198,v198
v_mfma_f32_16x16x32_bf16 a[44:47],a[216:219],v[164:167],a[44:47]
ds_read_b128 a[156:159],v1 offset:4864
v_mfma_f32_16x16x32_bf16 a[48:51],a[224:227],v[164:167],0
v_exp_f32 v199,v199
v_mfma_f32_16x16x32_bf16 a[48:51],a[228:231],v[180:183],a[48:51]
ds_read_b128 a[152:155],v1 offset:4608
v_cvt_pk_bf16_f32 v197,v198,v199
v_mfma_f32_16x16x32_bf16 a[52:55],a[236:239],v[180:183],0
v_exp_f32 v204,v204
v_mfma_f32_16x16x32_bf16 a[52:55],a[232:235],v[164:167],a[52:55]
ds_read_b128 a[148:151],v1 offset:4352
v_mfma_f32_16x16x32_bf16 a[56:59],a[240:243],v[164:167],0
v_exp_f32 v205,v205
v_mfma_f32_16x16x32_bf16 a[56:59],a[244:247],v[180:183],a[56:59]
ds_read_b128 a[144:147],v1 offset:4096
v_cvt_pk_bf16_f32 v198,v204,v205
v_mfma_f32_16x16x32_bf16 a[60:63],a[252:255],v[180:183],0
v_exp_f32 v206,v206
v_mfma_f32_16x16x32_bf16 a[60:63],a[248:251],v[164:167],a[60:63]
ds_read_b128 a[160:163],v1 offset:8192
v_mfma_f32_16x16x32_bf16 v[52:55],v[28:31],v[164:167],0
buffer_load_dwordx4 v5,s[20:23],s47 offen offset:2048 nt lds
v_mfma_f32_16x16x32_bf16 v[52:55],v[28:31],v[180:183],v[52:55]
s_waitcnt lgkmcnt(3)
ds_read_b128 a[164:167],v1 offset:8448
v_mfma_f32_16x16x32_bf16 v[132:135],a[128:131],v[64:67],v[32:35]
v_exp_f32 v207,v207
v_mfma_f32_16x16x32_bf16 v[132:135],a[132:135],v[68:71],v[132:135]
ds_read_b128 a[168:171],v1 offset:8704
v_cvt_pk_bf16_f32 v199,v206,v207
v_mfma_f32_16x16x32_bf16 v[132:135],a[136:139],v[72:75],v[132:135]
ds_read_b128 a[172:175],v1 offset:8960
v_mfma_f32_16x16x32_bf16 v[132:135],a[140:143],v[76:79],v[132:135]
ds_read_b128 a[188:191],v1 offset:13056
v_mfma_f32_16x16x32_bf16 v[140:143],a[156:159],v[76:79],v[32:35]
ds_read_b128 a[184:187],v1 offset:12800
v_mfma_f32_16x16x32_bf16 v[140:143],a[152:155],v[72:75],v[140:143]
s_waitcnt lgkmcnt(5)
ds_read_b128 a[180:183],v1 offset:12544
v_mfma_f32_16x16x32_bf16 v[140:143],a[148:151],v[68:71],v[140:143]
ds_read_b128 a[176:179],v1 offset:12288
v_mfma_f32_16x16x32_bf16 v[140:143],a[144:147],v[64:67],v[140:143]
v_mfma_f32_16x16x32_bf16 v[148:151],a[160:163],v[64:67],v[32:35]
s_waitcnt lgkmcnt(6)
v_mfma_f32_16x16x32_bf16 v[148:151],a[164:167],v[68:71],v[148:151]
s_waitcnt lgkmcnt(5)
v_exp_f32 v212,v212
v_mfma_f32_16x16x32_bf16 v[148:151],a[168:171],v[72:75],v[148:151]
s_waitcnt lgkmcnt(4)
v_exp_f32 v213,v213
v_mfma_f32_16x16x32_bf16 v[148:151],a[172:175],v[76:79],v[148:151]
s_waitcnt lgkmcnt(3)
v_exp_f32 v214,v214
v_mfma_f32_16x16x32_bf16 v[156:159],a[188:191],v[76:79],v[32:35]
s_waitcnt lgkmcnt(2)
v_exp_f32 v215,v215
v_mfma_f32_16x16x32_bf16 v[156:159],a[184:187],v[72:75],v[156:159]
s_waitcnt lgkmcnt(1)
v_cvt_pk_bf16_f32 v212,v212,v213
v_exp_f32 v220,v220
v_exp_f32 v221,v221
v_exp_f32 v222,v222
v_exp_f32 v223,v223
v_mfma_f32_16x16x32_bf16 v[156:159],a[180:183],v[68:71],v[156:159]
s_waitcnt lgkmcnt(0)
v_cvt_pk_bf16_f32 v213,v214,v215
v_cvt_pk_bf16_f32 v214,v220,v221
v_cvt_pk_bf16_f32 v215,v222,v223
v_mfma_f32_16x16x32_bf16 v[156:159],a[176:179],v[64:67],v[156:159]
buffer_load_dwordx4 v5,s[20:23],s47 offen offset:3072 nt lds
v_mfma_f32_16x16x32_bf16 a[64:67],a[192:195],v[196:199],0
v_exp_f32 v228,v228
v_mfma_f32_16x16x32_bf16 a[64:67],a[196:199],v[212:215],a[64:67]
v_exp_f32 v229,v229
v_mfma_f32_16x16x32_bf16 a[68:71],a[204:207],v[212:215],0
v_exp_f32 v230,v230
v_mfma_f32_16x16x32_bf16 a[68:71],a[200:203],v[196:199],a[68:71]
v_exp_f32 v231,v231
v_mfma_f32_16x16x32_bf16 a[72:75],a[208:211],v[196:199],0
v_cvt_pk_bf16_f32 v228,v228,v229
v_cvt_pk_bf16_f32 v229,v230,v231
v_mfma_f32_16x16x32_bf16 a[72:75],a[212:215],v[212:215],a[72:75]
v_exp_f32 v236,v236
v_mfma_f32_16x16x32_bf16 a[76:79],a[220:223],v[212:215],0
v_exp_f32 v237,v237
v_mfma_f32_16x16x32_bf16 a[76:79],a[216:219],v[196:199],a[76:79]
s_add_u32 m0,s49,s52
v_exp_f32 v238,v238
v_mfma_f32_16x16x32_bf16 a[80:83],a[224:227],v[196:199],0
s_add_u32 s49,s49,32768
v_exp_f32 v239,v239
)A"
R"A(v_mfma_f32_16x16x32_bf16 a[80:83],a[228:231],v[212:215],a[80:83]
v_exp_f32 v244,v244
v_mfma_f32_16x16x32_bf16 a[84:87],a[236:239],v[212:215],0
buffer_load_dwordx4 v4,s[24:27],s47 offen nt lds
v_mfma_f32_16x16x32_bf16 a[84:87],a[232:235],v[196:199],a[84:87]
v_exp_f32 v245,v245
v_mfma_f32_16x16x32_bf16 a[88:91],a[240:243],v[196:199],0
v_exp_f32 v246,v246
v_mfma_f32_16x16x32_bf16 a[88:91],a[244:247],v[212:215],a[88:91]
v_exp_f32 v247,v247
v_mfma_f32_16x16x32_bf16 a[92:95],a[252:255],v[212:215],0
v_cvt_pk_bf16_f32 v230,v236,v237
v_cvt_pk_bf16_f32 v231,v238,v239
v_mfma_f32_16x16x32_bf16 a[92:95],a[248:251],v[196:199],a[92:95]
v_cvt_pk_bf16_f32 v244,v244,v245
v_cvt_pk_bf16_f32 v245,v246,v247
v_mfma_f32_16x16x32_bf16 v[56:59],v[28:31],v[196:199],0
v_exp_f32 v252,v252
v_mfma_f32_16x16x32_bf16 v[56:59],v[28:31],v[212:215],v[56:59]
v_exp_f32 v253,v253
v_mfma_f32_16x16x32_bf16 v[164:167],a[128:131],v[80:83],v[36:39]
v_exp_f32 v254,v254
v_mfma_f32_16x16x32_bf16 v[164:167],a[132:135],v[84:87],v[164:167]
v_exp_f32 v255,v255
v_mfma_f32_16x16x32_bf16 v[164:167],a[136:139],v[88:91],v[164:167]
v_cvt_pk_bf16_f32 v246,v252,v253
v_cvt_pk_bf16_f32 v247,v254,v255
v_mfma_f32_16x16x32_bf16 v[164:167],a[140:143],v[92:95],v[164:167]
v_exp_f32 v132,v132
v_mfma_f32_16x16x32_bf16 v[172:175],a[156:159],v[92:95],v[36:39]
v_exp_f32 v133,v133
v_mfma_f32_16x16x32_bf16 v[172:175],a[152:155],v[88:91],v[172:175]
v_exp_f32 v134,v134
v_mfma_f32_16x16x32_bf16 v[172:175],a[148:151],v[84:87],v[172:175]
buffer_load_dwordx4 v5,s[24:27],s47 offen offset:1024 nt lds
v_mfma_f32_16x16x32_bf16 v[172:175],a[144:147],v[80:83],v[172:175]
v_exp_f32 v135,v135
v_mfma_f32_16x16x32_bf16 v[180:183],a[160:163],v[80:83],v[36:39]
v_cvt_pk_bf16_f32 v132,v132,v133
v_cvt_pk_bf16_f32 v133,v134,v135
v_mfma_f32_16x16x32_bf16 v[180:183],a[164:167],v[84:87],v[180:183]
v_exp_f32 v140,v140
v_mfma_f32_16x16x32_bf16 v[180:183],a[168:171],v[88:91],v[180:183]
v_exp_f32 v141,v141
v_mfma_f32_16x16x32_bf16 v[180:183],a[172:175],v[92:95],v[180:183]
v_exp_f32 v142,v142
v_mfma_f32_16x16x32_bf16 v[188:191],a[188:191],v[92:95],v[36:39]
v_exp_f32 v143,v143
v_mfma_f32_16x16x32_bf16 v[188:191],a[184:187],v[88:91],v[188:191]
v_exp_f32 v148,v148
v_mfma_f32_16x16x32_bf16 v[188:191],a[180:183],v[84:87],v[188:191]
v_exp_f32 v149,v149
v_mfma_f32_16x16x32_bf16 v[188:191],a[176:179],v[80:83],v[188:191]
v_exp_f32 v150,v150
v_mfma_f32_16x16x32_bf16 a[96:99],a[192:195],v[228:231],0
ds_read_b64_tr_b16 a[192:193],v2
ds_read_b64_tr_b16 a[194:195],v2 offset:4096
v_mfma_f32_16x16x32_bf16 a[96:99],a[196:199],v[244:247],a[96:99]
ds_read_b64_tr_b16 a[196:197],v2 offset:8192
v_cvt_pk_bf16_f32 v134,v140,v141
v_mfma_f32_16x16x32_bf16 a[100:103],a[204:207],v[244:247],0
ds_read_b64_tr_b16 a[198:199],v2 offset:12288
v_cvt_pk_bf16_f32 v135,v142,v143
v_mfma_f32_16x16x32_bf16 a[100:103],a[200:203],v[228:231],a[100:103]
ds_read_b64_tr_b16 a[204:205],v3 offset:8192
v_cvt_pk_bf16_f32 v148,v148,v149
v_mfma_f32_16x16x32_bf16 a[104:107],a[208:211],v[228:231],0
buffer_load_dwordx4 v4,s[24:27],s47 offen offset:2048 nt lds
v_mfma_f32_16x16x32_bf16 a[104:107],a[212:215],v[244:247],a[104:107]
ds_read_b64_tr_b16 a[206:207],v3 offset:12288
ds_read_b64_tr_b16 a[200:201],v3
v_mfma_f32_16x16x32_bf16 a[108:111],a[220:223],v[244:247],0
ds_read_b64_tr_b16 a[202:203],v3 offset:4096
v_add_u32 v1,s48,v1
v_mfma_f32_16x16x32_bf16 a[108:111],a[216:219],v[228:231],a[108:111]
s_cmp_eq_u32 s49,163840
s_cselect_b32 s49,0,s49
ds_read_b64_tr_b16 a[208:209],v2 offset:256
v_mfma_f32_16x16x32_bf16 a[112:115],a[224:227],v[228:231],0
ds_read_b64_tr_b16 a[210:211],v2 offset:4352
v_mfma_f32_16x16x32_bf16 a[112:115],a[228:231],v[244:247],a[112:115]
ds_read_b64_tr_b16 a[212:213],v2 offset:8448
ds_read_b64_tr_b16 a[214:215],v2 offset:12544
v_mfma_f32_16x16x32_bf16 a[116:119],a[236:239],v[244:247],0
v_exp_f32 v151,v151
v_mfma_f32_16x16x32_bf16 a[116:119],a[232:235],v[228:231],a[116:119]
v_exp_f32 v156,v156
v_mfma_f32_16x16x32_bf16 a[120:123],a[240:243],v[228:231],0
v_exp_f32 v157,v157
v_mfma_f32_16x16x32_bf16 a[120:123],a[244:247],v[244:247],a[120:123]
v_exp_f32 v158,v158
v_mfma_f32_16x16x32_bf16 a[124:127],a[252:255],v[244:247],0
v_exp_f32 v159,v159
v_mfma_f32_16x16x32_bf16 a[124:127],a[248:251],v[228:231],a[124:127]
ds_read_b64_tr_b16 a[220:221],v3 offset:8448
ds_read_b64_tr_b16 a[222:223],v3 offset:12544
v_mfma_f32_16x16x32_bf16 v[60:63],v[28:31],v[228:231],0
v_cvt_pk_bf16_f32 v149,v150,v151
v_cvt_pk_bf16_f32 v150,v156,v157
v_mfma_f32_16x16x32_bf16 v[60:63],v[28:31],v[244:247],v[60:63]
ds_read_b64_tr_b16 a[216:217],v3 offset:256
v_cvt_pk_bf16_f32 v151,v158,v159
v_mfma_f32_16x16x32_bf16 v[196:199],a[128:131],v[96:99],v[40:43]
buffer_load_dwordx4 v5,s[24:27],s47 offen offset:3072 nt lds
v_mfma_f32_16x16x32_bf16 v[196:199],a[132:135],v[100:103],v[196:199]
s_add_u32 s47,s47,16384
ds_read_b64_tr_b16 a[218:219],v3 offset:4352
ds_read_b64_tr_b16 a[224:225],v2 offset:512
v_mfma_f32_16x16x32_bf16 v[196:199],a[136:139],v[104:107],v[196:199]
s_and_b32 s47,s47,0x3ffff
s_or_b32 s47,s47,s46
ds_read_b64_tr_b16 a[226:227],v2 offset:4608
v_mfma_f32_16x16x32_bf16 v[196:199],a[140:143],v[108:111],v[196:199]
ds_read_b64_tr_b16 a[228:229],v2 offset:8704
v_mfma_f32_16x16x32_bf16 v[204:207],a[156:159],v[108:111],v[40:43]
ds_read_b64_tr_b16 a[230:231],v2 offset:12800
v_mfma_f32_16x16x32_bf16 v[204:207],a[152:155],v[104:107],v[204:207]
ds_read_b64_tr_b16 a[236:237],v3 offset:8704
ds_read_b64_tr_b16 a[238:239],v3 offset:12800
v_mfma_f32_16x16x32_bf16 v[204:207],a[148:151],v[100:103],v[204:207]
ds_read_b64_tr_b16 a[232:233],v3 offset:512
v_mfma_f32_16x16x32_bf16 v[204:207],a[144:147],v[96:99],v[204:207]
ds_read_b64_tr_b16 a[234:235],v3 offset:4608
v_mfma_f32_16x16x32_bf16 v[212:215],a[160:163],v[96:99],v[40:43]
ds_read_b64_tr_b16 a[240:241],v2 offset:768
v_mfma_f32_16x16x32_bf16 v[212:215],a[164:167],v[100:103],v[212:215]
ds_read_b64_tr_b16 a[242:243],v2 offset:4864
ds_read_b64_tr_b16 a[244:245],v2 offset:8960
v_mfma_f32_16x16x32_bf16 v[212:215],a[168:171],v[104:107],v[212:215]
ds_read_b64_tr_b16 a[246:247],v2 offset:13056
v_mfma_f32_16x16x32_bf16 v[212:215],a[172:175],v[108:111],v[212:215]
ds_read_b64_tr_b16 a[252:253],v3 offset:8960
v_mfma_f32_16x16x32_bf16 v[220:223],a[188:191],v[108:111],v[40:43]
ds_read_b64_tr_b16 a[254:255],v3 offset:13056
v_mfma_f32_16x16x32_bf16 v[220:223],a[184:187],v[104:107],v[220:223]
ds_read_b64_tr_b16 a[248:249],v3 offset:768
ds_read_b64_tr_b16 a[250:251],v3 offset:4864
v_mfma_f32_16x16x32_bf16 v[220:223],a[180:183],v[100:103],v[220:223]
v_exp_f32 v164,v164
v_mfma_f32_16x16x32_bf16 v[220:223],a[176:179],v[96:99],v[220:223]
s_waitcnt lgkmcnt(8)
v_exp_f32 v165,v165
v_mfma_f32_16x16x32_bf16 a[0:3],a[192:195],v[132:135],a[0:3]
v_exp_f32 v180,v180
v_mfma_f32_16x16x32_bf16 a[0:3],a[196:199],v[148:151],a[0:3]
v_exp_f32 v181,v181
v_mfma_f32_16x16x32_bf16 a[4:7],a[204:207],v[148:151],a[4:7]
v_exp_f32 v166,v166
v_mfma_f32_16x16x32_bf16 a[4:7],a[200:203],v[132:135],a[4:7]
v_exp_f32 v167,v167
v_mfma_f32_16x16x32_bf16 a[8:11],a[208:211],v[132:135],a[8:11]
s_memrealtime s[76:77]
v_exp_f32 v182,v182
v_mfma_f32_16x16x32_bf16 a[8:11],a[212:215],v[148:151],a[8:11]
v_exp_f32 v183,v183
v_mfma_f32_16x16x32_bf16 a[12:15],a[220:223],v[148:151],a[12:15]
v_cvt_pk_bf16_f32 v164,v164,v165
v_cvt_pk_bf16_f32 v180,v180,v181
v_mfma_f32_16x16x32_bf16 a[12:15],a[216:219],v[132:135],a[12:15]
v_exp_f32 v172,v172
v_mfma_f32_16x16x32_bf16 a[16:19],a[224:227],v[132:135],a[16:19]
v_exp_f32 v173,v173
v_mfma_f32_16x16x32_bf16 a[16:19],a[228:231],v[148:151],a[16:19]
v_exp_f32 v174,v174
v_mfma_f32_16x16x32_bf16 a[20:23],a[236:239],v[148:151],a[20:23]
v_exp_f32 v175,v175
v_mfma_f32_16x16x32_bf16 a[20:23],a[232:235],v[132:135],a[20:23]
s_waitcnt lgkmcnt(0)
v_exp_f32 v188,v188
v_mfma_f32_16x16x32_bf16 a[24:27],a[240:243],v[132:135],a[24:27]
s_sub_u32 s79,s78,s76
v_exp_f32 v189,v189
v_mfma_f32_16x16x32_bf16 a[24:27],a[244:247],v[148:151],a[24:27]
s_add_u32 s78,s76,s60
v_exp_f32 v190,v190
v_mfma_f32_16x16x32_bf16 a[28:31],a[252:255],v[148:151],a[28:31]
s_cmp_gt_i32 s79,0
s_cbranch_scc1 L_ps2_%=
L_pr2_%=:
v_cvt_pk_bf16_f32 v165,v166,v167
v_mfma_f32_16x16x32_bf16 a[28:31],a[248:251],v[132:135],a[28:31]
v_exp_f32 v191,v191
v_mfma_f32_16x16x32_bf16 v[48:51],v[28:31],v[132:135],v[48:51]
v_cvt_pk_bf16_f32 v181,v182,v183
v_cvt_pk_bf16_f32 v166,v172,v173
v_cvt_pk_bf16_f32 v167,v174,v175
v_cvt_pk_bf16_f32 v182,v188,v189
v_cvt_pk_bf16_f32 v183,v190,v191
v_mfma_f32_16x16x32_bf16 v[48:51],v[28:31],v[148:151],v[48:51]
s_mov_b32 s53,13
L_att0_%=:
v_mfma_f32_16x16x32_bf16 v[228:231],a[128:131],v[112:115],v[44:47]
s_waitcnt vmcnt(8) lgkmcnt(0)
v_exp_f32 v196,v196
v_mfma_f32_16x16x32_bf16 v[228:231],a[132:135],v[116:119],v[228:231]
s_barrier
s_add_u32 m0,s49,s51
ds_read_b128 a[128:131],v1
v_mfma_f32_16x16x32_bf16 v[228:231],a[136:139],v[120:123],v[228:231]
s_mov_b32 s90,L_endseg_%=-L_pc_%=+128
v_exp_f32 v197,v197
v_mfma_f32_16x16x32_bf16 v[228:231],a[140:143],v[124:127],v[228:231]
s_add_u32 s54,s54,16384
ds_read_b128 a[132:135],v1 offset:256
v_cvt_pk_bf16_f32 v196,v196,v197
v_mfma_f32_16x16x32_bf16 v[236:239],a[156:159],v[124:127],v[44:47]
s_sub_u32 s53,s53,1
buffer_load_dwordx4 v4,s[20:23],s47 offen nt lds
v_mfma_f32_16x16x32_bf16 v[236:239],a[152:155],v[120:123],v[236:239]
s_and_b32 s46,s54,0xfffc0000
ds_read_b128 a[136:139],v1 offset:512
v_add_u32 v2,s48,v2
v_mfma_f32_16x16x32_bf16 v[236:239],a[148:151],v[116:119],v[236:239]
v_exp_f32 v198,v198
v_mfma_f32_16x16x32_bf16 v[236:239],a[144:147],v[112:115],v[236:239]
ds_read_b128 a[140:143],v1 offset:768
v_add_u32 v3,s48,v3
v_mfma_f32_16x16x32_bf16 v[244:247],a[160:163],v[112:115],v[44:47]
v_exp_f32 v199,v199
v_mfma_f32_16x16x32_bf16 v[244:247],a[164:167],v[116:119],v[244:247]
ds_read_b128 a[156:159],v1 offset:4864
v_cvt_pk_bf16_f32 v197,v198,v199
v_mfma_f32_16x16x32_bf16 v[244:247],a[168:171],v[120:123],v[244:247]
v_exp_f32 v204,v204
v_mfma_f32_16x16x32_bf16 v[244:247],a[172:175],v[124:127],v[244:247]
s_cmp_eq_u32 s50,131072
s_cselect_b32 s62,163840,0
ds_read_b128 a[152:155],v1 offset:4608
v_mfma_f32_16x16x32_bf16 v[252:255],a[188:191],v[124:127],v[44:47]
s_sub_u32 s48,32768,s62
v_exp_f32 v205,v205
v_mfma_f32_16x16x32_bf16 v[252:255],a[184:187],v[120:123],v[252:255]
s_add_u32 s50,s50,s48
ds_read_b128 a[148:151],v1 offset:4352
v_cvt_pk_bf16_f32 v198,v204,v205
v_mfma_f32_16x16x32_bf16 v[252:255],a[180:183],v[116:119],v[252:255]
v_exp_f32 v206,v206
v_mfma_f32_16x16x32_bf16 v[252:255],a[176:179],v[112:115],v[252:255]
ds_read_b128 a[144:147],v1 offset:4096
v_mfma_f32_16x16x32_bf16 a[32:35],a[192:195],v[164:167],a[32:35]
v_exp_f32 v207,v207
v_mfma_f32_16x16x32_bf16 a[32:35],a[196:199],v[180:183],a[32:35]
ds_read_b128 a[160:163],v1 offset:8192
v_cvt_pk_bf16_f32 v199,v206,v207
v_mfma_f32_16x16x32_bf16 a[36:39],a[204:207],v[180:183],a[36:39]
buffer_load_dwordx4 v4,s[20:23],s47 offen offset:1024 nt lds
v_mfma_f32_16x16x32_bf16 a[36:39],a[200:203],v[164:167],a[36:39]
ds_read_b128 a[164:167],v1 offset:8448
v_mfma_f32_16x16x32_bf16 a[40:43],a[208:211],v[164:167],a[40:43]
v_exp_f32 v212,v212
v_mfma_f32_16x16x32_bf16 a[40:43],a[212:215],v[180:183],a[40:43]
ds_read_b128 a[168:171],v1 offset:8704
v_mfma_f32_16x16x32_bf16 a[44:47],a[220:223],v[180:183],a[44:47]
v_exp_f32 v213,v213
v_mfma_f32_16x16x32_bf16 a[44:47],a[216:219],v[164:167],a[44:47]
ds_read_b128 a[172:175],v1 offset:8960
v_cvt_pk_bf16_f32 v212,v212,v213
v_mfma_f32_16x16x32_bf16 a[48:51],a[224:227],v[164:167],a[48:51]
v_exp_f32 v214,v214
v_mfma_f32_16x16x32_bf16 a[48:51],a[228:231],v[180:183],a[48:51]
ds_read_b128 a[188:191],v1 offset:13056
v_mfma_f32_16x16x32_bf16 a[52:55],a[236:239],v[180:183],a[52:55]
v_exp_f32 v215,v215
v_mfma_f32_16x16x32_bf16 a[52:55],a[232:235],v[164:167],a[52:55]
ds_read_b128 a[184:187],v1 offset:12800
v_cvt_pk_bf16_f32 v213,v214,v215
v_mfma_f32_16x16x32_bf16 a[56:59],a[240:243],v[164:167],a[56:59]
v_exp_f32 v220,v220
v_mfma_f32_16x16x32_bf16 a[56:59],a[244:247],v[180:183],a[56:59]
ds_read_b128 a[180:183],v1 offset:12544
v_mfma_f32_16x16x32_bf16 a[60:63],a[252:255],v[180:183],a[60:63]
)A"
R"A(v_exp_f32 v221,v221
v_mfma_f32_16x16x32_bf16 a[60:63],a[248:251],v[164:167],a[60:63]
ds_read_b128 a[176:179],v1 offset:12288
v_cvt_pk_bf16_f32 v214,v220,v221
v_mfma_f32_16x16x32_bf16 v[52:55],v[28:31],v[164:167],v[52:55]
buffer_load_dwordx4 v5,s[20:23],s47 offen offset:2048 nt lds
v_mfma_f32_16x16x32_bf16 v[52:55],v[28:31],v[180:183],v[52:55]
s_waitcnt lgkmcnt(3)
v_exp_f32 v222,v222
v_mfma_f32_16x16x32_bf16 v[132:135],a[128:131],v[64:67],v[32:35]
v_exp_f32 v223,v223
v_mfma_f32_16x16x32_bf16 v[132:135],a[132:135],v[68:71],v[132:135]
v_cvt_pk_bf16_f32 v215,v222,v223
v_add_u32 v1,s48,v1
v_mfma_f32_16x16x32_bf16 v[132:135],a[136:139],v[72:75],v[132:135]
v_exp_f32 v228,v228
v_mfma_f32_16x16x32_bf16 v[132:135],a[140:143],v[76:79],v[132:135]
v_exp_f32 v229,v229
v_mfma_f32_16x16x32_bf16 v[140:143],a[156:159],v[76:79],v[32:35]
v_exp_f32 v230,v230
v_mfma_f32_16x16x32_bf16 v[140:143],a[152:155],v[72:75],v[140:143]
v_exp_f32 v231,v231
v_mfma_f32_16x16x32_bf16 v[140:143],a[148:151],v[68:71],v[140:143]
v_cvt_pk_bf16_f32 v228,v228,v229
v_cvt_pk_bf16_f32 v229,v230,v231
v_mfma_f32_16x16x32_bf16 v[140:143],a[144:147],v[64:67],v[140:143]
v_exp_f32 v236,v236
v_mfma_f32_16x16x32_bf16 v[148:151],a[160:163],v[64:67],v[32:35]
v_exp_f32 v237,v237
v_mfma_f32_16x16x32_bf16 v[148:151],a[164:167],v[68:71],v[148:151]
v_exp_f32 v238,v238
v_mfma_f32_16x16x32_bf16 v[148:151],a[168:171],v[72:75],v[148:151]
v_exp_f32 v239,v239
v_mfma_f32_16x16x32_bf16 v[148:151],a[172:175],v[76:79],v[148:151]
v_exp_f32 v244,v244
v_mfma_f32_16x16x32_bf16 v[156:159],a[188:191],v[76:79],v[32:35]
s_waitcnt lgkmcnt(0)
buffer_load_dwordx4 v5,s[20:23],s47 offen offset:3072 nt lds
v_mfma_f32_16x16x32_bf16 v[156:159],a[184:187],v[72:75],v[156:159]
v_exp_f32 v245,v245
v_mfma_f32_16x16x32_bf16 v[156:159],a[180:183],v[68:71],v[156:159]
v_exp_f32 v246,v246
v_mfma_f32_16x16x32_bf16 v[156:159],a[176:179],v[64:67],v[156:159]
v_exp_f32 v247,v247
v_mfma_f32_16x16x32_bf16 a[64:67],a[192:195],v[196:199],a[64:67]
v_cvt_pk_bf16_f32 v230,v236,v237
v_cvt_pk_bf16_f32 v231,v238,v239
v_mfma_f32_16x16x32_bf16 a[64:67],a[196:199],v[212:215],a[64:67]
v_cvt_pk_bf16_f32 v244,v244,v245
v_cvt_pk_bf16_f32 v245,v246,v247
v_mfma_f32_16x16x32_bf16 a[68:71],a[204:207],v[212:215],a[68:71]
v_exp_f32 v252,v252
v_mfma_f32_16x16x32_bf16 a[68:71],a[200:203],v[196:199],a[68:71]
v_exp_f32 v253,v253
v_mfma_f32_16x16x32_bf16 a[72:75],a[208:211],v[196:199],a[72:75]
v_exp_f32 v254,v254
v_mfma_f32_16x16x32_bf16 a[72:75],a[212:215],v[212:215],a[72:75]
v_exp_f32 v255,v255
v_mfma_f32_16x16x32_bf16 a[76:79],a[220:223],v[212:215],a[76:79]
v_cvt_pk_bf16_f32 v246,v252,v253
v_cvt_pk_bf16_f32 v247,v254,v255
v_mfma_f32_16x16x32_bf16 a[76:79],a[216:219],v[196:199],a[76:79]
s_add_u32 m0,s49,s52
v_exp_f32 v132,v132
v_mfma_f32_16x16x32_bf16 a[80:83],a[224:227],v[196:199],a[80:83]
s_add_u32 s49,s49,32768
v_exp_f32 v133,v133
v_mfma_f32_16x16x32_bf16 a[80:83],a[228:231],v[212:215],a[80:83]
v_exp_f32 v134,v134
v_mfma_f32_16x16x32_bf16 a[84:87],a[236:239],v[212:215],a[84:87]
buffer_load_dwordx4 v4,s[24:27],s47 offen nt lds
v_mfma_f32_16x16x32_bf16 a[84:87],a[232:235],v[196:199],a[84:87]
v_exp_f32 v135,v135
v_mfma_f32_16x16x32_bf16 a[88:91],a[240:243],v[196:199],a[88:91]
v_cvt_pk_bf16_f32 v132,v132,v133
v_cvt_pk_bf16_f32 v133,v134,v135
v_mfma_f32_16x16x32_bf16 a[88:91],a[244:247],v[212:215],a[88:91]
v_exp_f32 v140,v140
v_mfma_f32_16x16x32_bf16 a[92:95],a[252:255],v[212:215],a[92:95]
v_exp_f32 v141,v141
v_mfma_f32_16x16x32_bf16 a[92:95],a[248:251],v[196:199],a[92:95]
v_exp_f32 v142,v142
v_mfma_f32_16x16x32_bf16 v[56:59],v[28:31],v[196:199],v[56:59]
v_exp_f32 v143,v143
v_mfma_f32_16x16x32_bf16 v[56:59],v[28:31],v[212:215],v[56:59]
v_exp_f32 v148,v148
v_mfma_f32_16x16x32_bf16 v[164:167],a[128:131],v[80:83],v[36:39]
v_exp_f32 v149,v149
v_mfma_f32_16x16x32_bf16 v[164:167],a[132:135],v[84:87],v[164:167]
v_exp_f32 v150,v150
v_mfma_f32_16x16x32_bf16 v[164:167],a[136:139],v[88:91],v[164:167]
v_exp_f32 v151,v151
v_mfma_f32_16x16x32_bf16 v[164:167],a[140:143],v[92:95],v[164:167]
v_cvt_pk_bf16_f32 v134,v140,v141
v_cvt_pk_bf16_f32 v135,v142,v143
v_mfma_f32_16x16x32_bf16 v[172:175],a[156:159],v[92:95],v[36:39]
v_cvt_pk_bf16_f32 v148,v148,v149
v_cvt_pk_bf16_f32 v149,v150,v151
v_mfma_f32_16x16x32_bf16 v[172:175],a[152:155],v[88:91],v[172:175]
v_exp_f32 v156,v156
v_mfma_f32_16x16x32_bf16 v[172:175],a[148:151],v[84:87],v[172:175]
buffer_load_dwordx4 v5,s[24:27],s47 offen offset:1024 nt lds
v_mfma_f32_16x16x32_bf16 v[172:175],a[144:147],v[80:83],v[172:175]
v_exp_f32 v157,v157
v_mfma_f32_16x16x32_bf16 v[180:183],a[160:163],v[80:83],v[36:39]
v_exp_f32 v158,v158
v_mfma_f32_16x16x32_bf16 v[180:183],a[164:167],v[84:87],v[180:183]
v_exp_f32 v159,v159
v_mfma_f32_16x16x32_bf16 v[180:183],a[168:171],v[88:91],v[180:183]
v_cvt_pk_bf16_f32 v150,v156,v157
v_cvt_pk_bf16_f32 v151,v158,v159
v_mfma_f32_16x16x32_bf16 v[180:183],a[172:175],v[92:95],v[180:183]
v_exp_f32 v164,v164
v_mfma_f32_16x16x32_bf16 v[188:191],a[188:191],v[92:95],v[36:39]
v_exp_f32 v165,v165
v_mfma_f32_16x16x32_bf16 v[188:191],a[184:187],v[88:91],v[188:191]
v_exp_f32 v166,v166
v_mfma_f32_16x16x32_bf16 v[188:191],a[180:183],v[84:87],v[188:191]
v_exp_f32 v167,v167
v_mfma_f32_16x16x32_bf16 v[188:191],a[176:179],v[80:83],v[188:191]
v_cvt_pk_bf16_f32 v164,v164,v165
v_cvt_pk_bf16_f32 v165,v166,v167
v_mfma_f32_16x16x32_bf16 a[96:99],a[192:195],v[228:231],a[96:99]
s_cmp_eq_u32 s49,163840
s_cselect_b32 s49,0,s49
ds_read_b64_tr_b16 a[192:193],v2
v_mfma_f32_16x16x32_bf16 a[96:99],a[196:199],v[244:247],a[96:99]
ds_read_b64_tr_b16 a[194:195],v2 offset:4096
v_mfma_f32_16x16x32_bf16 a[100:103],a[204:207],v[244:247],a[100:103]
ds_read_b64_tr_b16 a[196:197],v2 offset:8192
v_mfma_f32_16x16x32_bf16 a[100:103],a[200:203],v[228:231],a[100:103]
ds_read_b64_tr_b16 a[198:199],v2 offset:12288
v_mfma_f32_16x16x32_bf16 a[104:107],a[208:211],v[228:231],a[104:107]
buffer_load_dwordx4 v4,s[24:27],s47 offen offset:2048 nt lds
v_mfma_f32_16x16x32_bf16 a[104:107],a[212:215],v[244:247],a[104:107]
ds_read_b64_tr_b16 a[204:205],v3 offset:8192
v_mfma_f32_16x16x32_bf16 a[108:111],a[220:223],v[244:247],a[108:111]
ds_read_b64_tr_b16 a[206:207],v3 offset:12288
v_mfma_f32_16x16x32_bf16 a[108:111],a[216:219],v[228:231],a[108:111]
ds_read_b64_tr_b16 a[200:201],v3
v_mfma_f32_16x16x32_bf16 a[112:115],a[224:227],v[228:231],a[112:115]
ds_read_b64_tr_b16 a[202:203],v3 offset:4096
v_mfma_f32_16x16x32_bf16 a[112:115],a[228:231],v[244:247],a[112:115]
ds_read_b64_tr_b16 a[208:209],v2 offset:256
v_mfma_f32_16x16x32_bf16 a[116:119],a[236:239],v[244:247],a[116:119]
ds_read_b64_tr_b16 a[210:211],v2 offset:4352
v_mfma_f32_16x16x32_bf16 a[116:119],a[232:235],v[228:231],a[116:119]
ds_read_b64_tr_b16 a[212:213],v2 offset:8448
v_mfma_f32_16x16x32_bf16 a[120:123],a[240:243],v[228:231],a[120:123]
ds_read_b64_tr_b16 a[214:215],v2 offset:12544
v_mfma_f32_16x16x32_bf16 a[120:123],a[244:247],v[244:247],a[120:123]
ds_read_b64_tr_b16 a[220:221],v3 offset:8448
v_mfma_f32_16x16x32_bf16 a[124:127],a[252:255],v[244:247],a[124:127]
ds_read_b64_tr_b16 a[222:223],v3 offset:12544
v_mfma_f32_16x16x32_bf16 a[124:127],a[248:251],v[228:231],a[124:127]
ds_read_b64_tr_b16 a[216:217],v3 offset:256
v_mfma_f32_16x16x32_bf16 v[60:63],v[28:31],v[228:231],v[60:63]
ds_read_b64_tr_b16 a[218:219],v3 offset:4352
v_mfma_f32_16x16x32_bf16 v[60:63],v[28:31],v[244:247],v[60:63]
ds_read_b64_tr_b16 a[224:225],v2 offset:512
v_mfma_f32_16x16x32_bf16 v[196:199],a[128:131],v[96:99],v[40:43]
buffer_load_dwordx4 v5,s[24:27],s47 offen offset:3072 nt lds
v_mfma_f32_16x16x32_bf16 v[196:199],a[132:135],v[100:103],v[196:199]
s_add_u32 s47,s47,16384
s_and_b32 s47,s47,0x3ffff
ds_read_b64_tr_b16 a[226:227],v2 offset:4608
v_mfma_f32_16x16x32_bf16 v[196:199],a[136:139],v[104:107],v[196:199]
s_or_b32 s47,s47,s46
ds_read_b64_tr_b16 a[228:229],v2 offset:8704
v_mfma_f32_16x16x32_bf16 v[196:199],a[140:143],v[108:111],v[196:199]
ds_read_b64_tr_b16 a[230:231],v2 offset:12800
v_mfma_f32_16x16x32_bf16 v[204:207],a[156:159],v[108:111],v[40:43]
ds_read_b64_tr_b16 a[236:237],v3 offset:8704
v_mfma_f32_16x16x32_bf16 v[204:207],a[152:155],v[104:107],v[204:207]
ds_read_b64_tr_b16 a[238:239],v3 offset:12800
v_mfma_f32_16x16x32_bf16 v[204:207],a[148:151],v[100:103],v[204:207]
ds_read_b64_tr_b16 a[232:233],v3 offset:512
v_mfma_f32_16x16x32_bf16 v[204:207],a[144:147],v[96:99],v[204:207]
ds_read_b64_tr_b16 a[234:235],v3 offset:4608
v_mfma_f32_16x16x32_bf16 v[212:215],a[160:163],v[96:99],v[40:43]
ds_read_b64_tr_b16 a[240:241],v2 offset:768
v_mfma_f32_16x16x32_bf16 v[212:215],a[164:167],v[100:103],v[212:215]
ds_read_b64_tr_b16 a[242:243],v2 offset:4864
v_mfma_f32_16x16x32_bf16 v[212:215],a[168:171],v[104:107],v[212:215]
ds_read_b64_tr_b16 a[244:245],v2 offset:8960
v_mfma_f32_16x16x32_bf16 v[212:215],a[172:175],v[108:111],v[212:215]
ds_read_b64_tr_b16 a[246:247],v2 offset:13056
v_mfma_f32_16x16x32_bf16 v[220:223],a[188:191],v[108:111],v[40:43]
ds_read_b64_tr_b16 a[252:253],v3 offset:8960
v_mfma_f32_16x16x32_bf16 v[220:223],a[184:187],v[104:107],v[220:223]
ds_read_b64_tr_b16 a[254:255],v3 offset:13056
v_mfma_f32_16x16x32_bf16 v[220:223],a[180:183],v[100:103],v[220:223]
ds_read_b64_tr_b16 a[248:249],v3 offset:768
v_mfma_f32_16x16x32_bf16 v[220:223],a[176:179],v[96:99],v[220:223]
s_waitcnt lgkmcnt(7)
ds_read_b64_tr_b16 a[250:251],v3 offset:4864
v_mfma_f32_16x16x32_bf16 a[0:3],a[192:195],v[132:135],a[0:3]
buffer_load_dword v26,v24,s[88:91],s93 offen
v_mfma_f32_16x16x32_bf16 a[0:3],a[196:199],v[148:151],a[0:3]
v_exp_f32 v180,v180
v_mfma_f32_16x16x32_bf16 a[4:7],a[204:207],v[148:151],a[4:7]
v_exp_f32 v181,v181
v_mfma_f32_16x16x32_bf16 a[4:7],a[200:203],v[132:135],a[4:7]
v_exp_f32 v182,v182
v_mfma_f32_16x16x32_bf16 a[8:11],a[208:211],v[132:135],a[8:11]
s_memrealtime s[76:77]
v_exp_f32 v183,v183
v_mfma_f32_16x16x32_bf16 a[8:11],a[212:215],v[148:151],a[8:11]
v_exp_f32 v172,v172
v_mfma_f32_16x16x32_bf16 a[12:15],a[220:223],v[148:151],a[12:15]
v_exp_f32 v173,v173
v_mfma_f32_16x16x32_bf16 a[12:15],a[216:219],v[132:135],a[12:15]
v_exp_f32 v174,v174
v_mfma_f32_16x16x32_bf16 a[16:19],a[224:227],v[132:135],a[16:19]
v_exp_f32 v175,v175
v_mfma_f32_16x16x32_bf16 a[16:19],a[228:231],v[148:151],a[16:19]
v_cvt_pk_bf16_f32 v180,v180,v181
v_cvt_pk_bf16_f32 v181,v182,v183
v_mfma_f32_16x16x32_bf16 a[20:23],a[236:239],v[148:151],a[20:23]
v_exp_f32 v188,v188
v_mfma_f32_16x16x32_bf16 a[20:23],a[232:235],v[132:135],a[20:23]
s_waitcnt lgkmcnt(0)
v_exp_f32 v189,v189
v_mfma_f32_16x16x32_bf16 a[24:27],a[240:243],v[132:135],a[24:27]
s_sub_u32 s79,s78,s76
v_exp_f32 v190,v190
v_mfma_f32_16x16x32_bf16 a[24:27],a[244:247],v[148:151],a[24:27]
s_add_u32 s78,s76,s60
v_exp_f32 v191,v191
v_mfma_f32_16x16x32_bf16 a[28:31],a[252:255],v[148:151],a[28:31]
s_cmp_gt_i32 s79,0
s_cbranch_scc1 L_ps3_%=
L_pr3_%=:
v_cvt_pk_bf16_f32 v166,v172,v173
v_mfma_f32_16x16x32_bf16 a[28:31],a[248:251],v[132:135],a[28:31]
v_cvt_pk_bf16_f32 v167,v174,v175
v_cvt_pk_bf16_f32 v182,v188,v189
v_mfma_f32_16x16x32_bf16 v[48:51],v[28:31],v[132:135],v[48:51]
v_cvt_pk_bf16_f32 v183,v190,v191
s_cmp_lg_u32 s53,0
v_mfma_f32_16x16x32_bf16 v[48:51],v[28:31],v[148:151],v[48:51]
s_cbranch_scc1 L_att0_%=
v_mfma_f32_16x16x32_bf16 v[228:231],a[128:131],v[112:115],v[44:47]
s_waitcnt vmcnt(10)
s_add_u32 s54,s54,16384
v_exp_f32 v196,v196
v_mfma_f32_16x16x32_bf16 v[228:231],a[132:135],v[116:119],v[228:231]
s_barrier
s_add_u32 m0,s49,s51
ds_read_b128 a[128:131],v1
v_mfma_f32_16x16x32_bf16 v[228:231],a[136:139],v[120:123],v[228:231]
s_and_b32 s46,s54,0xfffc0000
ds_read_b128 a[132:135],v1 offset:256
v_and_b32 v18,48,v6
v_mfma_f32_16x16x32_bf16 v[228:231],a[140:143],v[124:127],v[228:231]
s_add_u32 s54,s54,16384
ds_read_b128 a[136:139],v1 offset:512
v_lshrrev_b32 v19,4,v6
v_mfma_f32_16x16x32_bf16 v[236:239],a[156:159],v[124:127],v[44:47]
s_add_u32 s84,s55,0
buffer_load_dwordx4 v4,s[20:23],s47 offen nt lds
v_mfma_f32_16x16x32_bf16 v[236:239],a[152:155],v[120:123],v[236:239]
s_add_u32 s85,s55,4096
ds_read_b128 a[140:143],v1 offset:768
v_lshlrev_b32 v18,2,v18
v_mfma_f32_16x16x32_bf16 v[236:239],a[148:151],v[116:119],v[236:239]
)A"
R"A(s_add_u32 s86,s55,8192
ds_read_b128 a[156:159],v1 offset:4864
v_and_b32 v19,48,v19
v_mfma_f32_16x16x32_bf16 v[236:239],a[144:147],v[112:115],v[236:239]
s_add_u32 s87,s55,12288
ds_read_b128 a[152:155],v1 offset:4608
v_add_u32 v18,v18,v19
v_mfma_f32_16x16x32_bf16 v[244:247],a[160:163],v[112:115],v[44:47]
s_max_i32 s84,s84,s18
ds_read_b128 a[148:151],v1 offset:4352
v_add_u32 v18,131072,v18
v_mfma_f32_16x16x32_bf16 v[244:247],a[164:167],v[116:119],v[244:247]
s_max_i32 s85,s85,s19
ds_read_b128 a[144:147],v1 offset:4096
v_add_u32 v2,s48,v2
v_mfma_f32_16x16x32_bf16 v[244:247],a[168:171],v[120:123],v[244:247]
s_max_i32 s86,s86,s100
ds_read_b128 a[160:163],v1 offset:8192
v_add_u32 v3,s48,v3
v_mfma_f32_16x16x32_bf16 v[244:247],a[172:175],v[124:127],v[244:247]
s_cmp_eq_u32 s50,131072
s_cselect_b32 s62,163840,0
ds_read_b128 a[164:167],v1 offset:8448
v_mfma_f32_16x16x32_bf16 v[252:255],a[188:191],v[124:127],v[44:47]
s_sub_u32 s48,32768,s62
s_add_u32 s50,s50,s48
ds_read_b128 a[168:171],v1 offset:8704
v_mfma_f32_16x16x32_bf16 v[252:255],a[184:187],v[120:123],v[252:255]
s_cmp_eq_u32 s50,131072
s_cselect_b32 s62,163840,0
ds_read_b128 a[172:175],v1 offset:8960
v_mfma_f32_16x16x32_bf16 v[252:255],a[180:183],v[116:119],v[252:255]
s_max_i32 s87,s87,s101
s_add_u32 s93,s92,L_endseg_%=-L_pc_%=
ds_read_b128 a[188:191],v1 offset:13056
v_mfma_f32_16x16x32_bf16 v[252:255],a[176:179],v[112:115],v[252:255]
s_mov_b32 s80,0
s_mov_b32 s83,s55
ds_read_b128 a[184:187],v1 offset:12800
v_mfma_f32_16x16x32_bf16 a[32:35],a[192:195],v[164:167],a[32:35]
ds_read_b128 a[180:183],v1 offset:12544
v_mfma_f32_16x16x32_bf16 a[32:35],a[196:199],v[180:183],a[32:35]
ds_read_b128 a[176:179],v1 offset:12288
v_add_u32 v1,s48,v1
v_mfma_f32_16x16x32_bf16 a[36:39],a[204:207],v[180:183],a[36:39]
buffer_load_dwordx4 v4,s[20:23],s47 offen offset:1024 nt lds
v_mfma_f32_16x16x32_bf16 a[36:39],a[200:203],v[164:167],a[36:39]
v_exp_f32 v197,v197
v_mfma_f32_16x16x32_bf16 a[40:43],a[208:211],v[164:167],a[40:43]
v_exp_f32 v198,v198
v_mfma_f32_16x16x32_bf16 a[40:43],a[212:215],v[180:183],a[40:43]
v_exp_f32 v199,v199
v_mfma_f32_16x16x32_bf16 a[44:47],a[220:223],v[180:183],a[44:47]
v_cvt_pk_bf16_f32 v196,v196,v197
v_cvt_pk_bf16_f32 v197,v198,v199
v_mfma_f32_16x16x32_bf16 a[44:47],a[216:219],v[164:167],a[44:47]
v_exp_f32 v204,v204
v_mfma_f32_16x16x32_bf16 a[48:51],a[224:227],v[164:167],a[48:51]
v_exp_f32 v205,v205
v_mfma_f32_16x16x32_bf16 a[48:51],a[228:231],v[180:183],a[48:51]
v_exp_f32 v206,v206
v_mfma_f32_16x16x32_bf16 a[52:55],a[236:239],v[180:183],a[52:55]
v_exp_f32 v207,v207
v_mfma_f32_16x16x32_bf16 a[52:55],a[232:235],v[164:167],a[52:55]
v_exp_f32 v212,v212
v_mfma_f32_16x16x32_bf16 a[56:59],a[240:243],v[164:167],a[56:59]
v_exp_f32 v213,v213
v_mfma_f32_16x16x32_bf16 a[56:59],a[244:247],v[180:183],a[56:59]
v_exp_f32 v214,v214
v_mfma_f32_16x16x32_bf16 a[60:63],a[252:255],v[180:183],a[60:63]
v_exp_f32 v215,v215
v_mfma_f32_16x16x32_bf16 a[60:63],a[248:251],v[164:167],a[60:63]
v_cvt_pk_bf16_f32 v198,v204,v205
v_cvt_pk_bf16_f32 v199,v206,v207
v_mfma_f32_16x16x32_bf16 v[52:55],v[28:31],v[164:167],v[52:55]
buffer_load_dwordx4 v5,s[20:23],s47 offen offset:2048 nt lds
v_mfma_f32_16x16x32_bf16 v[52:55],v[28:31],v[180:183],v[52:55]
s_waitcnt lgkmcnt(0)
v_cvt_pk_bf16_f32 v212,v212,v213
v_cvt_pk_bf16_f32 v213,v214,v215
v_mfma_f32_16x16x32_bf16 v[132:135],a[128:131],v[64:67],v[32:35]
v_exp_f32 v220,v220
v_mfma_f32_16x16x32_bf16 v[132:135],a[132:135],v[68:71],v[132:135]
v_exp_f32 v221,v221
v_mfma_f32_16x16x32_bf16 v[132:135],a[136:139],v[72:75],v[132:135]
v_exp_f32 v222,v222
v_mfma_f32_16x16x32_bf16 v[132:135],a[140:143],v[76:79],v[132:135]
v_exp_f32 v223,v223
v_mfma_f32_16x16x32_bf16 v[140:143],a[156:159],v[76:79],v[32:35]
v_cvt_pk_bf16_f32 v214,v220,v221
v_cvt_pk_bf16_f32 v215,v222,v223
v_mfma_f32_16x16x32_bf16 v[140:143],a[152:155],v[72:75],v[140:143]
v_exp_f32 v228,v228
v_mfma_f32_16x16x32_bf16 v[140:143],a[148:151],v[68:71],v[140:143]
v_exp_f32 v229,v229
v_mfma_f32_16x16x32_bf16 v[140:143],a[144:147],v[64:67],v[140:143]
v_exp_f32 v230,v230
v_mfma_f32_16x16x32_bf16 v[148:151],a[160:163],v[64:67],v[32:35]
v_exp_f32 v231,v231
v_mfma_f32_16x16x32_bf16 v[148:151],a[164:167],v[68:71],v[148:151]
v_cvt_pk_bf16_f32 v228,v228,v229
v_cvt_pk_bf16_f32 v229,v230,v231
v_mfma_f32_16x16x32_bf16 v[148:151],a[168:171],v[72:75],v[148:151]
v_exp_f32 v236,v236
v_mfma_f32_16x16x32_bf16 v[148:151],a[172:175],v[76:79],v[148:151]
v_exp_f32 v237,v237
v_mfma_f32_16x16x32_bf16 v[156:159],a[188:191],v[76:79],v[32:35]
buffer_load_dwordx4 v5,s[20:23],s47 offen offset:3072 nt lds
v_mfma_f32_16x16x32_bf16 v[156:159],a[184:187],v[72:75],v[156:159]
v_exp_f32 v238,v238
v_mfma_f32_16x16x32_bf16 v[156:159],a[180:183],v[68:71],v[156:159]
v_exp_f32 v239,v239
v_mfma_f32_16x16x32_bf16 v[156:159],a[176:179],v[64:67],v[156:159]
v_exp_f32 v244,v244
v_mfma_f32_16x16x32_bf16 a[64:67],a[192:195],v[196:199],a[64:67]
v_exp_f32 v245,v245
v_mfma_f32_16x16x32_bf16 a[64:67],a[196:199],v[212:215],a[64:67]
v_exp_f32 v246,v246
v_mfma_f32_16x16x32_bf16 a[68:71],a[204:207],v[212:215],a[68:71]
v_exp_f32 v247,v247
v_mfma_f32_16x16x32_bf16 a[68:71],a[200:203],v[196:199],a[68:71]
v_cvt_pk_bf16_f32 v230,v236,v237
v_cvt_pk_bf16_f32 v231,v238,v239
v_mfma_f32_16x16x32_bf16 a[72:75],a[208:211],v[196:199],a[72:75]
v_cvt_pk_bf16_f32 v244,v244,v245
v_cvt_pk_bf16_f32 v245,v246,v247
v_mfma_f32_16x16x32_bf16 a[72:75],a[212:215],v[212:215],a[72:75]
v_exp_f32 v252,v252
v_mfma_f32_16x16x32_bf16 a[76:79],a[220:223],v[212:215],a[76:79]
v_exp_f32 v253,v253
v_mfma_f32_16x16x32_bf16 a[76:79],a[216:219],v[196:199],a[76:79]
s_add_u32 m0,s49,s52
v_exp_f32 v254,v254
v_mfma_f32_16x16x32_bf16 a[80:83],a[224:227],v[196:199],a[80:83]
s_add_u32 s49,s49,32768
v_exp_f32 v255,v255
v_mfma_f32_16x16x32_bf16 a[80:83],a[228:231],v[212:215],a[80:83]
v_cvt_pk_bf16_f32 v246,v252,v253
v_cvt_pk_bf16_f32 v247,v254,v255
v_mfma_f32_16x16x32_bf16 a[84:87],a[236:239],v[212:215],a[84:87]
buffer_load_dwordx4 v4,s[24:27],s47 offen nt lds
v_mfma_f32_16x16x32_bf16 a[84:87],a[232:235],v[196:199],a[84:87]
v_exp_f32 v132,v132
v_mfma_f32_16x16x32_bf16 a[88:91],a[240:243],v[196:199],a[88:91]
v_exp_f32 v133,v133
v_mfma_f32_16x16x32_bf16 a[88:91],a[244:247],v[212:215],a[88:91]
v_exp_f32 v134,v134
v_mfma_f32_16x16x32_bf16 a[92:95],a[252:255],v[212:215],a[92:95]
v_exp_f32 v135,v135
v_mfma_f32_16x16x32_bf16 a[92:95],a[248:251],v[196:199],a[92:95]
v_cvt_pk_bf16_f32 v132,v132,v133
v_cvt_pk_bf16_f32 v133,v134,v135
v_mfma_f32_16x16x32_bf16 v[56:59],v[28:31],v[196:199],v[56:59]
v_exp_f32 v140,v140
v_mfma_f32_16x16x32_bf16 v[56:59],v[28:31],v[212:215],v[56:59]
v_exp_f32 v141,v141
v_mfma_f32_16x16x32_bf16 v[164:167],a[128:131],v[80:83],v[36:39]
v_exp_f32 v142,v142
v_mfma_f32_16x16x32_bf16 v[164:167],a[132:135],v[84:87],v[164:167]
v_exp_f32 v143,v143
v_mfma_f32_16x16x32_bf16 v[164:167],a[136:139],v[88:91],v[164:167]
v_exp_f32 v148,v148
v_mfma_f32_16x16x32_bf16 v[164:167],a[140:143],v[92:95],v[164:167]
v_exp_f32 v149,v149
v_mfma_f32_16x16x32_bf16 v[172:175],a[156:159],v[92:95],v[36:39]
v_exp_f32 v150,v150
v_mfma_f32_16x16x32_bf16 v[172:175],a[152:155],v[88:91],v[172:175]
v_exp_f32 v151,v151
v_mfma_f32_16x16x32_bf16 v[172:175],a[148:151],v[84:87],v[172:175]
buffer_load_dwordx4 v5,s[24:27],s47 offen offset:1024 nt lds
v_mfma_f32_16x16x32_bf16 v[172:175],a[144:147],v[80:83],v[172:175]
v_cvt_pk_bf16_f32 v134,v140,v141
v_cvt_pk_bf16_f32 v135,v142,v143
v_mfma_f32_16x16x32_bf16 v[180:183],a[160:163],v[80:83],v[36:39]
v_cvt_pk_bf16_f32 v148,v148,v149
v_cvt_pk_bf16_f32 v149,v150,v151
v_mfma_f32_16x16x32_bf16 v[180:183],a[164:167],v[84:87],v[180:183]
v_exp_f32 v156,v156
v_mfma_f32_16x16x32_bf16 v[180:183],a[168:171],v[88:91],v[180:183]
v_exp_f32 v157,v157
v_mfma_f32_16x16x32_bf16 v[180:183],a[172:175],v[92:95],v[180:183]
v_exp_f32 v158,v158
v_mfma_f32_16x16x32_bf16 v[188:191],a[188:191],v[92:95],v[36:39]
v_exp_f32 v159,v159
v_mfma_f32_16x16x32_bf16 v[188:191],a[184:187],v[88:91],v[188:191]
v_cvt_pk_bf16_f32 v150,v156,v157
v_cvt_pk_bf16_f32 v151,v158,v159
v_mfma_f32_16x16x32_bf16 v[188:191],a[180:183],v[84:87],v[188:191]
s_waitcnt vmcnt(14)
s_cmp_eq_u32 s49,163840
s_cselect_b32 s49,0,s49
v_mfma_f32_16x16x32_bf16 v[188:191],a[176:179],v[80:83],v[188:191]
s_barrier
ds_read_b128 v[80:83],v18
v_mfma_f32_16x16x32_bf16 a[96:99],a[192:195],v[228:231],a[96:99]
ds_read_b128 v[84:87],v18 offset:256
v_mfma_f32_16x16x32_bf16 a[96:99],a[196:199],v[244:247],a[96:99]
ds_read_b128 v[88:91],v18 offset:512
v_mfma_f32_16x16x32_bf16 a[100:103],a[204:207],v[244:247],a[100:103]
ds_read_b128 v[92:95],v18 offset:768
v_mfma_f32_16x16x32_bf16 a[100:103],a[200:203],v[228:231],a[100:103]
buffer_load_dwordx4 v4,s[24:27],s47 offen offset:2048 nt lds
v_mfma_f32_16x16x32_bf16 a[104:107],a[208:211],v[228:231],a[104:107]
ds_read_b64_tr_b16 a[192:193],v2
ds_read_b64_tr_b16 a[194:195],v2 offset:4096
v_mfma_f32_16x16x32_bf16 a[104:107],a[212:215],v[244:247],a[104:107]
ds_read_b64_tr_b16 a[196:197],v2 offset:8192
ds_read_b64_tr_b16 a[198:199],v2 offset:12288
v_mfma_f32_16x16x32_bf16 a[108:111],a[220:223],v[244:247],a[108:111]
ds_read_b64_tr_b16 a[204:205],v3 offset:8192
ds_read_b64_tr_b16 a[206:207],v3 offset:12288
v_mfma_f32_16x16x32_bf16 a[108:111],a[216:219],v[228:231],a[108:111]
ds_read_b64_tr_b16 a[200:201],v3
ds_read_b64_tr_b16 a[202:203],v3 offset:4096
v_mfma_f32_16x16x32_bf16 a[112:115],a[224:227],v[228:231],a[112:115]
ds_read_b64_tr_b16 a[208:209],v2 offset:256
ds_read_b64_tr_b16 a[210:211],v2 offset:4352
v_mfma_f32_16x16x32_bf16 a[112:115],a[228:231],v[244:247],a[112:115]
ds_read_b64_tr_b16 a[212:213],v2 offset:8448
ds_read_b64_tr_b16 a[214:215],v2 offset:12544
v_mfma_f32_16x16x32_bf16 a[116:119],a[236:239],v[244:247],a[116:119]
ds_read_b64_tr_b16 a[220:221],v3 offset:8448
ds_read_b64_tr_b16 a[222:223],v3 offset:12544
v_mfma_f32_16x16x32_bf16 a[116:119],a[232:235],v[228:231],a[116:119]
ds_read_b64_tr_b16 a[216:217],v3 offset:256
ds_read_b64_tr_b16 a[218:219],v3 offset:4352
v_mfma_f32_16x16x32_bf16 a[120:123],a[240:243],v[228:231],a[120:123]
ds_read_b64_tr_b16 a[224:225],v2 offset:512
ds_read_b64_tr_b16 a[226:227],v2 offset:4608
v_mfma_f32_16x16x32_bf16 a[120:123],a[244:247],v[244:247],a[120:123]
ds_read_b64_tr_b16 a[228:229],v2 offset:8704
ds_read_b64_tr_b16 a[230:231],v2 offset:12800
v_mfma_f32_16x16x32_bf16 a[124:127],a[252:255],v[244:247],a[124:127]
ds_read_b64_tr_b16 a[236:237],v3 offset:8704
ds_read_b64_tr_b16 a[238:239],v3 offset:12800
v_mfma_f32_16x16x32_bf16 a[124:127],a[248:251],v[228:231],a[124:127]
ds_read_b64_tr_b16 a[232:233],v3 offset:512
ds_read_b64_tr_b16 a[234:235],v3 offset:4608
v_mfma_f32_16x16x32_bf16 v[60:63],v[28:31],v[228:231],v[60:63]
ds_read_b64_tr_b16 a[240:241],v2 offset:768
ds_read_b64_tr_b16 a[242:243],v2 offset:4864
v_mfma_f32_16x16x32_bf16 v[60:63],v[28:31],v[244:247],v[60:63]
buffer_load_dwordx4 v5,s[24:27],s47 offen offset:3072 nt lds
v_mfma_f32_16x16x32_bf16 v[196:199],a[128:131],v[96:99],v[40:43]
s_add_u32 s47,s47,16384
ds_read_b64_tr_b16 a[244:245],v2 offset:8960
ds_read_b64_tr_b16 a[246:247],v2 offset:13056
v_mfma_f32_16x16x32_bf16 v[196:199],a[132:135],v[100:103],v[196:199]
s_and_b32 s47,s47,0x3ffff
ds_read_b64_tr_b16 a[252:253],v3 offset:8960
ds_read_b64_tr_b16 a[254:255],v3 offset:13056
v_mfma_f32_16x16x32_bf16 v[196:199],a[136:139],v[104:107],v[196:199]
s_or_b32 s47,s47,s46
ds_read_b64_tr_b16 a[248:249],v3 offset:768
ds_read_b64_tr_b16 a[250:251],v3 offset:4864
v_mfma_f32_16x16x32_bf16 v[196:199],a[140:143],v[108:111],v[196:199]
s_and_b32 s46,s54,0xfffc0000
v_exp_f32 v164,v164
v_mfma_f32_16x16x32_bf16 v[204:207],a[156:159],v[108:111],v[40:43]
s_add_u32 s54,s54,16384
v_exp_f32 v165,v165
v_mfma_f32_16x16x32_bf16 v[204:207],a[152:155],v[104:107],v[204:207]
v_exp_f32 v166,v166
v_mfma_f32_16x16x32_bf16 v[204:207],a[148:151],v[100:103],v[204:207]
v_exp_f32 v167,v167
v_mfma_f32_16x16x32_bf16 v[204:207],a[144:147],v[96:99],v[204:207]
v_cvt_pk_bf16_f32 v164,v164,v165
v_cvt_pk_bf16_f32 v165,v166,v167
v_mfma_f32_16x16x32_bf16 v[212:215],a[160:163],v[96:99],v[40:43]
v_exp_f32 v172,v172
)A"
R"A(v_mfma_f32_16x16x32_bf16 v[212:215],a[164:167],v[100:103],v[212:215]
v_exp_f32 v173,v173
v_mfma_f32_16x16x32_bf16 v[212:215],a[168:171],v[104:107],v[212:215]
v_exp_f32 v174,v174
v_mfma_f32_16x16x32_bf16 v[212:215],a[172:175],v[108:111],v[212:215]
v_exp_f32 v175,v175
v_mfma_f32_16x16x32_bf16 v[220:223],a[188:191],v[108:111],v[40:43]
v_exp_f32 v180,v180
v_mfma_f32_16x16x32_bf16 v[220:223],a[184:187],v[104:107],v[220:223]
v_exp_f32 v181,v181
v_mfma_f32_16x16x32_bf16 v[220:223],a[180:183],v[100:103],v[220:223]
v_exp_f32 v182,v182
v_mfma_f32_16x16x32_bf16 v[220:223],a[176:179],v[96:99],v[220:223]
s_waitcnt lgkmcnt(0)
v_exp_f32 v183,v183
v_mfma_f32_16x16x32_bf16 v[32:35],v[80:83],v[128:131],0
v_cvt_pk_bf16_f32 v166,v172,v173
v_cvt_pk_bf16_f32 v167,v174,v175
v_mfma_f32_16x16x32_bf16 v[32:35],v[84:87],v[160:163],v[32:35]
v_cvt_pk_bf16_f32 v180,v180,v181
v_cvt_pk_bf16_f32 v181,v182,v183
v_mfma_f32_16x16x32_bf16 v[32:35],v[88:91],v[192:195],v[32:35]
v_exp_f32 v188,v188
v_mfma_f32_16x16x32_bf16 v[32:35],v[92:95],v[224:227],v[32:35]
v_exp_f32 v189,v189
v_mfma_f32_16x16x32_bf16 v[36:39],v[80:83],v[136:139],0
v_exp_f32 v190,v190
v_mfma_f32_16x16x32_bf16 v[36:39],v[84:87],v[168:171],v[36:39]
v_exp_f32 v191,v191
v_mfma_f32_16x16x32_bf16 v[36:39],v[88:91],v[200:203],v[36:39]
v_cvt_pk_bf16_f32 v182,v188,v189
v_cvt_pk_bf16_f32 v183,v190,v191
v_mfma_f32_16x16x32_bf16 v[36:39],v[92:95],v[232:235],v[36:39]
v_exp_f32 v196,v196
v_mfma_f32_16x16x32_bf16 v[40:43],v[80:83],v[144:147],0
v_exp_f32 v197,v197
v_mfma_f32_16x16x32_bf16 v[40:43],v[84:87],v[176:179],v[40:43]
v_max3_f32 v32,v32,v33,v34
v_max3_f32 v32,v32,v35,s18
v_mfma_f32_16x16x32_bf16 v[40:43],v[88:91],v[208:211],v[40:43]
v_sub_f32 v35,0,v32
v_sub_f32 v34,0,v32
v_mfma_f32_16x16x32_bf16 v[40:43],v[92:95],v[240:243],v[40:43]
v_sub_f32 v33,0,v32
v_sub_f32 v32,0,v32
v_mfma_f32_16x16x32_bf16 a[0:3],a[192:195],v[132:135],a[0:3]
v_exp_f32 v198,v198
v_mfma_f32_16x16x32_bf16 a[0:3],a[196:199],v[148:151],a[0:3]
v_exp_f32 v199,v199
v_mfma_f32_16x16x32_bf16 a[4:7],a[204:207],v[148:151],a[4:7]
v_cvt_pk_bf16_f32 v196,v196,v197
v_cvt_pk_bf16_f32 v197,v198,v199
v_mfma_f32_16x16x32_bf16 a[4:7],a[200:203],v[132:135],a[4:7]
v_exp_f32 v204,v204
v_mfma_f32_16x16x32_bf16 a[8:11],a[208:211],v[132:135],a[8:11]
s_memrealtime s[76:77]
v_exp_f32 v205,v205
v_mfma_f32_16x16x32_bf16 a[8:11],a[212:215],v[148:151],a[8:11]
v_exp_f32 v206,v206
v_mfma_f32_16x16x32_bf16 a[12:15],a[220:223],v[148:151],a[12:15]
v_exp_f32 v207,v207
v_mfma_f32_16x16x32_bf16 a[12:15],a[216:219],v[132:135],a[12:15]
v_exp_f32 v212,v212
v_mfma_f32_16x16x32_bf16 a[16:19],a[224:227],v[132:135],a[16:19]
v_exp_f32 v213,v213
v_mfma_f32_16x16x32_bf16 a[16:19],a[228:231],v[148:151],a[16:19]
v_exp_f32 v214,v214
v_mfma_f32_16x16x32_bf16 a[20:23],a[236:239],v[148:151],a[20:23]
v_exp_f32 v215,v215
v_mfma_f32_16x16x32_bf16 a[20:23],a[232:235],v[132:135],a[20:23]
s_waitcnt lgkmcnt(0)
v_cvt_pk_bf16_f32 v198,v204,v205
v_cvt_pk_bf16_f32 v199,v206,v207
v_mfma_f32_16x16x32_bf16 a[24:27],a[240:243],v[132:135],a[24:27]
s_sub_u32 s79,s78,s76
v_cvt_pk_bf16_f32 v212,v212,v213
v_cvt_pk_bf16_f32 v213,v214,v215
v_mfma_f32_16x16x32_bf16 a[24:27],a[244:247],v[148:151],a[24:27]
s_add_u32 s78,s76,s60
v_exp_f32 v220,v220
v_mfma_f32_16x16x32_bf16 a[28:31],a[252:255],v[148:151],a[28:31]
s_cmp_gt_i32 s79,0
s_cbranch_scc1 L_ps4_%=
L_pr4_%=:
v_max3_f32 v36,v36,v37,v38
v_mfma_f32_16x16x32_bf16 a[28:31],a[248:251],v[132:135],a[28:31]
v_exp_f32 v221,v221
v_mfma_f32_16x16x32_bf16 v[48:51],v[28:31],v[132:135],v[48:51]
v_exp_f32 v222,v222
v_mfma_f32_16x16x32_bf16 v[48:51],v[28:31],v[148:151],v[48:51]
v_exp_f32 v223,v223
v_mfma_f32_16x16x32_bf16 v[228:231],a[128:131],v[112:115],v[44:47]
s_waitcnt vmcnt(9)
v_cvt_pk_bf16_f32 v214,v220,v221
v_cvt_pk_bf16_f32 v215,v222,v223
v_mfma_f32_16x16x32_bf16 v[228:231],a[132:135],v[116:119],v[228:231]
s_barrier
s_add_u32 m0,s49,s51
ds_read_b128 a[128:131],v1
v_mfma_f32_16x16x32_bf16 v[228:231],a[136:139],v[120:123],v[228:231]
ds_read_b128 a[132:135],v1 offset:256
v_max3_f32 v36,v36,v39,s19
v_mfma_f32_16x16x32_bf16 v[228:231],a[140:143],v[124:127],v[228:231]
ds_read_b128 a[136:139],v1 offset:512
v_sub_f32 v39,0,v36
v_mfma_f32_16x16x32_bf16 v[236:239],a[156:159],v[124:127],v[44:47]
buffer_load_dwordx4 v4,s[20:23],s47 offen nt lds
v_mfma_f32_16x16x32_bf16 v[236:239],a[152:155],v[120:123],v[236:239]
ds_read_b128 a[140:143],v1 offset:768
v_sub_f32 v38,0,v36
v_mfma_f32_16x16x32_bf16 v[236:239],a[148:151],v[116:119],v[236:239]
ds_read_b128 a[156:159],v1 offset:4864
v_sub_f32 v37,0,v36
v_mfma_f32_16x16x32_bf16 v[236:239],a[144:147],v[112:115],v[236:239]
ds_read_b128 a[152:155],v1 offset:4608
v_sub_f32 v36,0,v36
v_mfma_f32_16x16x32_bf16 v[244:247],a[160:163],v[112:115],v[44:47]
ds_read_b128 a[148:151],v1 offset:4352
v_max3_f32 v40,v40,v41,v42
v_mfma_f32_16x16x32_bf16 v[244:247],a[164:167],v[116:119],v[244:247]
ds_read_b128 a[144:147],v1 offset:4096
v_max3_f32 v40,v40,v43,s100
v_mfma_f32_16x16x32_bf16 v[244:247],a[168:171],v[120:123],v[244:247]
ds_read_b128 a[160:163],v1 offset:8192
v_sub_f32 v43,0,v40
v_mfma_f32_16x16x32_bf16 v[244:247],a[172:175],v[124:127],v[244:247]
ds_read_b128 a[164:167],v1 offset:8448
v_sub_f32 v42,0,v40
v_mfma_f32_16x16x32_bf16 v[252:255],a[188:191],v[124:127],v[44:47]
ds_read_b128 a[168:171],v1 offset:8704
v_sub_f32 v41,0,v40
v_mfma_f32_16x16x32_bf16 v[252:255],a[184:187],v[120:123],v[252:255]
ds_read_b128 a[172:175],v1 offset:8960
v_sub_f32 v40,0,v40
v_mfma_f32_16x16x32_bf16 v[252:255],a[180:183],v[116:119],v[252:255]
ds_read_b128 a[188:191],v1 offset:13056
v_add_u32 v2,s48,v2
v_mfma_f32_16x16x32_bf16 v[252:255],a[176:179],v[112:115],v[252:255]
buffer_load_dwordx4 v4,s[20:23],s47 offen offset:1024 nt lds
v_mfma_f32_16x16x32_bf16 v[44:47],v[80:83],v[152:155],0
ds_read_b128 a[184:187],v1 offset:12800
v_add_u32 v3,s48,v3
v_mfma_f32_16x16x32_bf16 v[44:47],v[84:87],v[184:187],v[44:47]
s_sub_u32 s48,32768,s62
ds_read_b128 a[180:183],v1 offset:12544
v_max_f32 v20,0x00800000,v48
v_mfma_f32_16x16x32_bf16 v[44:47],v[88:91],v[216:219],v[44:47]
s_add_u32 s50,s50,s48
ds_read_b128 a[176:179],v1 offset:12288
v_accvgpr_read_b32 v64,a0
v_mfma_f32_16x16x32_bf16 v[44:47],v[92:95],v[248:251],v[44:47]
v_exp_f32 v228,v228
v_mfma_f32_16x16x32_bf16 a[32:35],a[192:195],v[164:167],a[32:35]
v_exp_f32 v229,v229
v_mfma_f32_16x16x32_bf16 a[32:35],a[196:199],v[180:183],a[32:35]
v_exp_f32 v230,v230
v_mfma_f32_16x16x32_bf16 a[36:39],a[204:207],v[180:183],a[36:39]
v_exp_f32 v231,v231
v_mfma_f32_16x16x32_bf16 a[36:39],a[200:203],v[164:167],a[36:39]
v_cvt_pk_bf16_f32 v228,v228,v229
v_cvt_pk_bf16_f32 v229,v230,v231
v_mfma_f32_16x16x32_bf16 a[40:43],a[208:211],v[164:167],a[40:43]
v_exp_f32 v236,v236
v_mfma_f32_16x16x32_bf16 a[40:43],a[212:215],v[180:183],a[40:43]
v_exp_f32 v237,v237
v_mfma_f32_16x16x32_bf16 a[44:47],a[220:223],v[180:183],a[44:47]
buffer_load_dwordx4 v5,s[20:23],s47 offen offset:2048 nt lds
v_mfma_f32_16x16x32_bf16 a[44:47],a[216:219],v[164:167],a[44:47]
v_exp_f32 v238,v238
v_mfma_f32_16x16x32_bf16 a[48:51],a[224:227],v[164:167],a[48:51]
v_exp_f32 v239,v239
v_mfma_f32_16x16x32_bf16 a[48:51],a[228:231],v[180:183],a[48:51]
v_exp_f32 v244,v244
v_mfma_f32_16x16x32_bf16 a[52:55],a[236:239],v[180:183],a[52:55]
v_exp_f32 v245,v245
v_mfma_f32_16x16x32_bf16 a[52:55],a[232:235],v[164:167],a[52:55]
v_exp_f32 v246,v246
v_mfma_f32_16x16x32_bf16 a[56:59],a[240:243],v[164:167],a[56:59]
v_exp_f32 v247,v247
v_mfma_f32_16x16x32_bf16 a[56:59],a[244:247],v[180:183],a[56:59]
v_cvt_pk_bf16_f32 v230,v236,v237
v_cvt_pk_bf16_f32 v231,v238,v239
v_mfma_f32_16x16x32_bf16 a[60:63],a[252:255],v[180:183],a[60:63]
v_cvt_pk_bf16_f32 v244,v244,v245
v_cvt_pk_bf16_f32 v245,v246,v247
v_mfma_f32_16x16x32_bf16 a[60:63],a[248:251],v[164:167],a[60:63]
v_exp_f32 v252,v252
v_mfma_f32_16x16x32_bf16 v[52:55],v[28:31],v[164:167],v[52:55]
v_exp_f32 v253,v253
v_mfma_f32_16x16x32_bf16 v[52:55],v[28:31],v[180:183],v[52:55]
s_waitcnt lgkmcnt(0)
buffer_load_dwordx4 v5,s[20:23],s47 offen offset:3072 nt lds
v_mfma_f32_16x16x32_bf16 v[132:135],a[128:131],v[128:131],v[32:35]
v_exp_f32 v254,v254
v_mfma_f32_16x16x32_bf16 v[132:135],a[132:135],v[160:163],v[132:135]
v_exp_f32 v255,v255
v_mfma_f32_16x16x32_bf16 v[132:135],a[136:139],v[192:195],v[132:135]
v_cvt_pk_bf16_f32 v246,v252,v253
v_cvt_pk_bf16_f32 v247,v254,v255
v_mfma_f32_16x16x32_bf16 v[132:135],a[140:143],v[224:227],v[132:135]
v_rcp_f32 v20,v20
v_mfma_f32_16x16x32_bf16 v[140:143],a[156:159],v[224:227],v[32:35]
v_accvgpr_read_b32 v65,a1
v_mul_f32 v64,v20,v64
v_mfma_f32_16x16x32_bf16 v[140:143],a[152:155],v[192:195],v[140:143]
v_mul_f32 v65,v20,v65
v_accvgpr_read_b32 v66,a2
v_mfma_f32_16x16x32_bf16 v[140:143],a[148:151],v[160:163],v[140:143]
v_accvgpr_read_b32 v67,a3
v_cvt_pk_bf16_f32 v64,v64,v65
v_mfma_f32_16x16x32_bf16 v[140:143],a[144:147],v[128:131],v[140:143]
s_add_u32 m0,s49,s52
s_add_u32 s49,s49,32768
v_mul_f32 v66,v20,v66
v_mfma_f32_16x16x32_bf16 v[148:151],a[160:163],v[128:131],v[32:35]
s_cmp_eq_u32 s49,163840
s_cselect_b32 s49,0,s49
v_mul_f32 v67,v20,v67
v_mfma_f32_16x16x32_bf16 v[148:151],a[164:167],v[160:163],v[148:151]
v_exp_f32 v132,v132
v_mfma_f32_16x16x32_bf16 v[148:151],a[168:171],v[192:195],v[148:151]
buffer_load_dwordx4 v4,s[24:27],s47 offen nt lds
v_mfma_f32_16x16x32_bf16 v[148:151],a[172:175],v[224:227],v[148:151]
v_exp_f32 v133,v133
v_mfma_f32_16x16x32_bf16 v[156:159],a[188:191],v[224:227],v[32:35]
v_exp_f32 v134,v134
v_mfma_f32_16x16x32_bf16 v[156:159],a[184:187],v[192:195],v[156:159]
v_exp_f32 v135,v135
v_mfma_f32_16x16x32_bf16 v[156:159],a[180:183],v[160:163],v[156:159]
v_cvt_pk_bf16_f32 v132,v132,v133
v_cvt_pk_bf16_f32 v133,v134,v135
v_mfma_f32_16x16x32_bf16 v[156:159],a[176:179],v[128:131],v[156:159]
v_exp_f32 v140,v140
v_mfma_f32_16x16x32_bf16 a[64:67],a[192:195],v[196:199],a[64:67]
v_exp_f32 v141,v141
v_mfma_f32_16x16x32_bf16 a[64:67],a[196:199],v[212:215],a[64:67]
v_exp_f32 v142,v142
v_mfma_f32_16x16x32_bf16 a[68:71],a[204:207],v[212:215],a[68:71]
v_exp_f32 v143,v143
v_mfma_f32_16x16x32_bf16 a[68:71],a[200:203],v[196:199],a[68:71]
v_exp_f32 v148,v148
v_mfma_f32_16x16x32_bf16 a[72:75],a[208:211],v[196:199],a[72:75]
v_exp_f32 v149,v149
v_mfma_f32_16x16x32_bf16 a[72:75],a[212:215],v[212:215],a[72:75]
buffer_load_dwordx4 v5,s[24:27],s47 offen offset:1024 nt lds
v_mfma_f32_16x16x32_bf16 a[76:79],a[220:223],v[212:215],a[76:79]
v_exp_f32 v150,v150
v_mfma_f32_16x16x32_bf16 a[76:79],a[216:219],v[196:199],a[76:79]
v_exp_f32 v151,v151
v_mfma_f32_16x16x32_bf16 a[80:83],a[224:227],v[196:199],a[80:83]
v_cvt_pk_bf16_f32 v134,v140,v141
v_cvt_pk_bf16_f32 v135,v142,v143
v_mfma_f32_16x16x32_bf16 a[80:83],a[228:231],v[212:215],a[80:83]
v_cvt_pk_bf16_f32 v148,v148,v149
v_cvt_pk_bf16_f32 v149,v150,v151
v_mfma_f32_16x16x32_bf16 a[84:87],a[236:239],v[212:215],a[84:87]
v_exp_f32 v156,v156
v_mfma_f32_16x16x32_bf16 a[84:87],a[232:235],v[196:199],a[84:87]
v_exp_f32 v157,v157
v_mfma_f32_16x16x32_bf16 a[88:91],a[240:243],v[196:199],a[88:91]
v_exp_f32 v158,v158
v_mfma_f32_16x16x32_bf16 a[88:91],a[244:247],v[212:215],a[88:91]
v_exp_f32 v159,v159
v_mfma_f32_16x16x32_bf16 a[92:95],a[252:255],v[212:215],a[92:95]
v_cvt_pk_bf16_f32 v150,v156,v157
v_cvt_pk_bf16_f32 v151,v158,v159
v_mfma_f32_16x16x32_bf16 a[92:95],a[248:251],v[196:199],a[92:95]
v_cvt_pk_bf16_f32 v65,v66,v67
v_accvgpr_read_b32 v66,a4
v_mfma_f32_16x16x32_bf16 v[56:59],v[28:31],v[196:199],v[56:59]
buffer_load_dwordx4 v4,s[24:27],s47 offen offset:2048 nt lds
v_mfma_f32_16x16x32_bf16 v[56:59],v[28:31],v[212:215],v[56:59]
v_accvgpr_read_b32 v67,a5
v_accvgpr_read_b32 v12,a6
v_mfma_f32_16x16x32_bf16 v[164:167],a[128:131],v[136:139],v[36:39]
v_accvgpr_read_b32 v13,a7
v_mul_f32 v66,v20,v66
v_mfma_f32_16x16x32_bf16 v[164:167],a[132:135],v[168:171],v[164:167]
v_mul_f32 v67,v20,v67
v_mul_f32 v12,v20,v12
v_mfma_f32_16x16x32_bf16 v[164:167],a[136:139],v[200:203],v[164:167]
v_mul_f32 v13,v20,v13
v_cvt_pk_bf16_f32 v66,v66,v67
v_mfma_f32_16x16x32_bf16 v[164:167],a[140:143],v[232:235],v[164:167]
v_cvt_pk_bf16_f32 v67,v12,v13
v_max3_f32 v44,v44,v45,v46
v_mfma_f32_16x16x32_bf16 v[172:175],a[156:159],v[232:235],v[36:39]
)A"
R"A(v_accvgpr_read_b32 v68,a8
v_accvgpr_read_b32 v69,a9
v_accvgpr_read_b32 v70,a10
v_accvgpr_read_b32 v71,a11
v_accvgpr_read_b32 v12,a12
v_accvgpr_read_b32 v13,a13
v_accvgpr_read_b32 v14,a14
v_accvgpr_read_b32 v15,a15
v_pk_mul_f32 v[68:69],v[20:21],v[68:69] op_sel:[0,0] op_sel_hi:[0,1]
v_pk_mul_f32 v[70:71],v[20:21],v[70:71] op_sel:[0,0] op_sel_hi:[0,1]
v_pk_mul_f32 v[12:13],v[20:21],v[12:13] op_sel:[0,0] op_sel_hi:[0,1]
v_pk_mul_f32 v[14:15],v[20:21],v[14:15] op_sel:[0,0] op_sel_hi:[0,1]
v_cvt_pk_bf16_f32 v68,v68,v69
v_cvt_pk_bf16_f32 v69,v70,v71
v_cvt_pk_bf16_f32 v70,v12,v13
v_cvt_pk_bf16_f32 v71,v14,v15
v_mfma_f32_16x16x32_bf16 v[172:175],a[152:155],v[200:203],v[172:175]
v_accvgpr_read_b32 v72,a16
v_accvgpr_read_b32 v73,a17
v_accvgpr_read_b32 v74,a18
v_accvgpr_read_b32 v75,a19
v_accvgpr_read_b32 v12,a20
v_accvgpr_read_b32 v13,a21
v_accvgpr_read_b32 v14,a22
v_accvgpr_read_b32 v15,a23
v_pk_mul_f32 v[72:73],v[20:21],v[72:73] op_sel:[0,0] op_sel_hi:[0,1]
v_pk_mul_f32 v[74:75],v[20:21],v[74:75] op_sel:[0,0] op_sel_hi:[0,1]
v_pk_mul_f32 v[12:13],v[20:21],v[12:13] op_sel:[0,0] op_sel_hi:[0,1]
v_pk_mul_f32 v[14:15],v[20:21],v[14:15] op_sel:[0,0] op_sel_hi:[0,1]
v_cvt_pk_bf16_f32 v72,v72,v73
v_cvt_pk_bf16_f32 v73,v74,v75
v_cvt_pk_bf16_f32 v74,v12,v13
v_cvt_pk_bf16_f32 v75,v14,v15
v_mfma_f32_16x16x32_bf16 v[172:175],a[148:151],v[168:171],v[172:175]
v_accvgpr_read_b32 v76,a24
v_accvgpr_read_b32 v77,a25
v_accvgpr_read_b32 v78,a26
v_accvgpr_read_b32 v79,a27
v_accvgpr_read_b32 v12,a28
v_accvgpr_read_b32 v13,a29
v_accvgpr_read_b32 v14,a30
v_accvgpr_read_b32 v15,a31
v_pk_mul_f32 v[76:77],v[20:21],v[76:77] op_sel:[0,0] op_sel_hi:[0,1]
v_pk_mul_f32 v[78:79],v[20:21],v[78:79] op_sel:[0,0] op_sel_hi:[0,1]
v_pk_mul_f32 v[12:13],v[20:21],v[12:13] op_sel:[0,0] op_sel_hi:[0,1]
v_pk_mul_f32 v[14:15],v[20:21],v[14:15] op_sel:[0,0] op_sel_hi:[0,1]
v_cvt_pk_bf16_f32 v76,v76,v77
v_cvt_pk_bf16_f32 v77,v78,v79
v_cvt_pk_bf16_f32 v78,v12,v13
v_cvt_pk_bf16_f32 v79,v14,v15
v_mfma_f32_16x16x32_bf16 v[172:175],a[144:147],v[136:139],v[172:175]
v_max3_f32 v44,v44,v47,s101
v_sub_f32 v47,0,v44
v_mfma_f32_16x16x32_bf16 v[180:183],a[160:163],v[136:139],v[36:39]
v_sub_f32 v46,0,v44
v_sub_f32 v45,0,v44
v_mfma_f32_16x16x32_bf16 v[180:183],a[164:167],v[168:171],v[180:183]
buffer_load_dwordx4 v5,s[24:27],s47 offen offset:3072 nt lds
v_mfma_f32_16x16x32_bf16 v[180:183],a[168:171],v[200:203],v[180:183]
s_add_u32 s47,s47,16384
s_and_b32 s47,s47,0x3ffff
s_or_b32 s47,s47,s46
v_mfma_f32_16x16x32_bf16 v[180:183],a[172:175],v[232:235],v[180:183]
s_and_b32 s46,s54,0xfffc0000
v_cmp_nlt_f32 s[56:57],v48,s63
v_sub_f32 v44,0,v44
v_mfma_f32_16x16x32_bf16 v[188:191],a[188:191],v[232:235],v[36:39]
s_add_u32 s54,s54,16384
v_exp_f32 v164,v164
v_mfma_f32_16x16x32_bf16 v[188:191],a[184:187],v[200:203],v[188:191]
v_exp_f32 v165,v165
v_mfma_f32_16x16x32_bf16 v[188:191],a[180:183],v[168:171],v[188:191]
v_exp_f32 v166,v166
v_mfma_f32_16x16x32_bf16 v[188:191],a[176:179],v[136:139],v[188:191]
v_exp_f32 v167,v167
v_mfma_f32_16x16x32_bf16 a[96:99],a[192:195],v[228:231],a[96:99]
ds_read_b64_tr_b16 a[192:193],v2
ds_read_b64_tr_b16 a[194:195],v2 offset:4096
v_mfma_f32_16x16x32_bf16 a[96:99],a[196:199],v[244:247],a[96:99]
s_add_u32 m0,s49,s51
ds_read_b64_tr_b16 a[196:197],v2 offset:8192
ds_read_b64_tr_b16 a[198:199],v2 offset:12288
v_mfma_f32_16x16x32_bf16 a[100:103],a[204:207],v[244:247],a[100:103]
ds_read_b64_tr_b16 a[204:205],v3 offset:8192
ds_read_b64_tr_b16 a[206:207],v3 offset:12288
v_mfma_f32_16x16x32_bf16 a[100:103],a[200:203],v[228:231],a[100:103]
ds_read_b64_tr_b16 a[200:201],v3
ds_read_b64_tr_b16 a[202:203],v3 offset:4096
v_mfma_f32_16x16x32_bf16 a[104:107],a[208:211],v[228:231],a[104:107]
buffer_load_dwordx4 v4,s[20:23],s47 offen nt lds
v_mfma_f32_16x16x32_bf16 a[104:107],a[212:215],v[244:247],a[104:107]
ds_read_b64_tr_b16 a[208:209],v2 offset:256
ds_read_b64_tr_b16 a[210:211],v2 offset:4352
v_mfma_f32_16x16x32_bf16 a[108:111],a[220:223],v[244:247],a[108:111]
ds_read_b64_tr_b16 a[212:213],v2 offset:8448
ds_read_b64_tr_b16 a[214:215],v2 offset:12544
v_mfma_f32_16x16x32_bf16 a[108:111],a[216:219],v[228:231],a[108:111]
ds_read_b64_tr_b16 a[220:221],v3 offset:8448
ds_read_b64_tr_b16 a[222:223],v3 offset:12544
v_mfma_f32_16x16x32_bf16 a[112:115],a[224:227],v[228:231],a[112:115]
ds_read_b64_tr_b16 a[216:217],v3 offset:256
ds_read_b64_tr_b16 a[218:219],v3 offset:4352
v_mfma_f32_16x16x32_bf16 a[112:115],a[228:231],v[244:247],a[112:115]
ds_read_b64_tr_b16 a[224:225],v2 offset:512
ds_read_b64_tr_b16 a[226:227],v2 offset:4608
v_mfma_f32_16x16x32_bf16 a[116:119],a[236:239],v[244:247],a[116:119]
ds_read_b64_tr_b16 a[228:229],v2 offset:8704
ds_read_b64_tr_b16 a[230:231],v2 offset:12800
v_mfma_f32_16x16x32_bf16 a[116:119],a[232:235],v[228:231],a[116:119]
ds_read_b64_tr_b16 a[236:237],v3 offset:8704
ds_read_b64_tr_b16 a[238:239],v3 offset:12800
v_mfma_f32_16x16x32_bf16 a[120:123],a[240:243],v[228:231],a[120:123]
ds_read_b64_tr_b16 a[232:233],v3 offset:512
ds_read_b64_tr_b16 a[234:235],v3 offset:4608
v_mfma_f32_16x16x32_bf16 a[120:123],a[244:247],v[244:247],a[120:123]
ds_read_b64_tr_b16 a[240:241],v2 offset:768
ds_read_b64_tr_b16 a[242:243],v2 offset:4864
v_mfma_f32_16x16x32_bf16 a[124:127],a[252:255],v[244:247],a[124:127]
ds_read_b64_tr_b16 a[244:245],v2 offset:8960
ds_read_b64_tr_b16 a[246:247],v2 offset:13056
v_mfma_f32_16x16x32_bf16 a[124:127],a[248:251],v[228:231],a[124:127]
buffer_load_dwordx4 v4,s[20:23],s47 offen offset:1024 nt lds
v_mfma_f32_16x16x32_bf16 v[60:63],v[28:31],v[228:231],v[60:63]
ds_read_b64_tr_b16 a[252:253],v3 offset:8960
ds_read_b64_tr_b16 a[254:255],v3 offset:13056
v_mfma_f32_16x16x32_bf16 v[60:63],v[28:31],v[244:247],v[60:63]
ds_read_b64_tr_b16 a[248:249],v3 offset:768
ds_read_b64_tr_b16 a[250:251],v3 offset:4864
v_mfma_f32_16x16x32_bf16 v[196:199],a[128:131],v[144:147],v[40:43]
v_cvt_pk_bf16_f32 v164,v164,v165
v_cvt_pk_bf16_f32 v165,v166,v167
v_mfma_f32_16x16x32_bf16 v[196:199],a[132:135],v[176:179],v[196:199]
v_exp_f32 v172,v172
v_mfma_f32_16x16x32_bf16 v[196:199],a[136:139],v[208:211],v[196:199]
v_exp_f32 v173,v173
v_mfma_f32_16x16x32_bf16 v[196:199],a[140:143],v[240:243],v[196:199]
v_exp_f32 v174,v174
v_mfma_f32_16x16x32_bf16 v[204:207],a[156:159],v[240:243],v[40:43]
v_exp_f32 v175,v175
v_mfma_f32_16x16x32_bf16 v[204:207],a[152:155],v[208:211],v[204:207]
v_exp_f32 v180,v180
v_mfma_f32_16x16x32_bf16 v[204:207],a[148:151],v[176:179],v[204:207]
v_exp_f32 v181,v181
v_mfma_f32_16x16x32_bf16 v[204:207],a[144:147],v[144:147],v[204:207]
v_exp_f32 v182,v182
v_mfma_f32_16x16x32_bf16 v[212:215],a[160:163],v[144:147],v[40:43]
buffer_load_dwordx4 v5,s[20:23],s47 offen offset:2048 nt lds
v_mfma_f32_16x16x32_bf16 v[212:215],a[164:167],v[176:179],v[212:215]
v_exp_f32 v183,v183
v_mfma_f32_16x16x32_bf16 v[212:215],a[168:171],v[208:211],v[212:215]
v_max_f32 v21,0x00800000,v52
v_cvt_pk_bf16_f32 v166,v172,v173
v_mfma_f32_16x16x32_bf16 v[212:215],a[172:175],v[240:243],v[212:215]
v_cvt_pk_bf16_f32 v167,v174,v175
v_cvt_pk_bf16_f32 v180,v180,v181
v_mfma_f32_16x16x32_bf16 v[220:223],a[188:191],v[240:243],v[40:43]
v_exp_f32 v188,v188
v_mfma_f32_16x16x32_bf16 v[220:223],a[184:187],v[208:211],v[220:223]
v_exp_f32 v189,v189
v_mfma_f32_16x16x32_bf16 v[220:223],a[180:183],v[176:179],v[220:223]
v_exp_f32 v190,v190
v_mfma_f32_16x16x32_bf16 v[220:223],a[176:179],v[144:147],v[220:223]
s_waitcnt lgkmcnt(0)
v_exp_f32 v191,v191
v_mfma_f32_16x16x32_bf16 a[0:3],a[192:195],v[132:135],0
v_cvt_pk_bf16_f32 v181,v182,v183
v_cvt_pk_bf16_f32 v182,v188,v189
v_mfma_f32_16x16x32_bf16 a[0:3],a[196:199],v[148:151],a[0:3]
v_rcp_f32 v21,v21
v_mfma_f32_16x16x32_bf16 a[4:7],a[204:207],v[148:151],0
v_cvt_pk_bf16_f32 v183,v190,v191
v_accvgpr_read_b32 v80,a32
v_mfma_f32_16x16x32_bf16 a[4:7],a[200:203],v[132:135],a[4:7]
buffer_load_dwordx4 v5,s[20:23],s47 offen offset:3072 nt lds
v_mfma_f32_16x16x32_bf16 a[8:11],a[208:211],v[132:135],0
s_memrealtime s[76:77]
v_accvgpr_read_b32 v81,a33
v_mul_f32 v80,v21,v80
v_mfma_f32_16x16x32_bf16 a[8:11],a[212:215],v[148:151],a[8:11]
v_mul_f32 v81,v21,v81
v_accvgpr_read_b32 v82,a34
v_mfma_f32_16x16x32_bf16 a[12:15],a[220:223],v[148:151],0
v_accvgpr_read_b32 v83,a35
v_cvt_pk_bf16_f32 v80,v80,v81
v_mfma_f32_16x16x32_bf16 a[12:15],a[216:219],v[132:135],a[12:15]
v_mul_f32 v82,v21,v82
v_mul_f32 v83,v21,v83
v_mfma_f32_16x16x32_bf16 a[16:19],a[224:227],v[132:135],0
v_cvt_pk_bf16_f32 v81,v82,v83
v_accvgpr_read_b32 v82,a36
v_mfma_f32_16x16x32_bf16 a[16:19],a[228:231],v[148:151],a[16:19]
v_accvgpr_read_b32 v83,a37
v_accvgpr_read_b32 v14,a38
v_mfma_f32_16x16x32_bf16 a[20:23],a[236:239],v[148:151],0
v_accvgpr_read_b32 v15,a39
v_mul_f32 v82,v21,v82
v_mfma_f32_16x16x32_bf16 a[20:23],a[232:235],v[132:135],a[20:23]
s_waitcnt lgkmcnt(0)
s_add_u32 m0,s49,s52
v_mul_f32 v83,v21,v83
v_mfma_f32_16x16x32_bf16 a[24:27],a[240:243],v[132:135],0
s_sub_u32 s79,s78,s76
v_mul_f32 v14,v21,v14
v_mul_f32 v15,v21,v15
v_mfma_f32_16x16x32_bf16 a[24:27],a[244:247],v[148:151],a[24:27]
s_add_u32 s78,s76,s60
v_cvt_pk_bf16_f32 v82,v82,v83
v_cvt_pk_bf16_f32 v83,v14,v15
v_mfma_f32_16x16x32_bf16 a[28:31],a[252:255],v[148:151],0
s_cmp_gt_i32 s79,0
s_cbranch_scc1 L_ps5_%=
L_pr5_%=:
v_add_u32 v1,s48,v1
v_mfma_f32_16x16x32_bf16 a[28:31],a[248:251],v[132:135],a[28:31]
s_add_u32 s49,s49,32768
buffer_load_dwordx4 v4,s[24:27],s47 offen nt lds
v_mfma_f32_16x16x32_bf16 v[48:51],v[28:31],v[132:135],0
v_accvgpr_read_b32 v84,a40
v_accvgpr_read_b32 v85,a41
v_accvgpr_read_b32 v86,a42
v_accvgpr_read_b32 v87,a43
v_accvgpr_read_b32 v12,a44
v_accvgpr_read_b32 v13,a45
v_accvgpr_read_b32 v14,a46
v_accvgpr_read_b32 v15,a47
v_pk_mul_f32 v[84:85],v[20:21],v[84:85] op_sel:[1,0] op_sel_hi:[1,1]
v_pk_mul_f32 v[86:87],v[20:21],v[86:87] op_sel:[1,0] op_sel_hi:[1,1]
v_pk_mul_f32 v[12:13],v[20:21],v[12:13] op_sel:[1,0] op_sel_hi:[1,1]
v_pk_mul_f32 v[14:15],v[20:21],v[14:15] op_sel:[1,0] op_sel_hi:[1,1]
v_cvt_pk_bf16_f32 v84,v84,v85
v_cvt_pk_bf16_f32 v85,v86,v87
v_cvt_pk_bf16_f32 v86,v12,v13
v_cvt_pk_bf16_f32 v87,v14,v15
v_mfma_f32_16x16x32_bf16 v[48:51],v[28:31],v[148:151],v[48:51]
v_accvgpr_read_b32 v88,a48
v_accvgpr_read_b32 v89,a49
v_accvgpr_read_b32 v90,a50
v_accvgpr_read_b32 v91,a51
v_accvgpr_read_b32 v12,a52
v_accvgpr_read_b32 v13,a53
v_accvgpr_read_b32 v14,a54
v_accvgpr_read_b32 v15,a55
v_pk_mul_f32 v[88:89],v[20:21],v[88:89] op_sel:[1,0] op_sel_hi:[1,1]
v_pk_mul_f32 v[90:91],v[20:21],v[90:91] op_sel:[1,0] op_sel_hi:[1,1]
v_pk_mul_f32 v[12:13],v[20:21],v[12:13] op_sel:[1,0] op_sel_hi:[1,1]
v_pk_mul_f32 v[14:15],v[20:21],v[14:15] op_sel:[1,0] op_sel_hi:[1,1]
v_cvt_pk_bf16_f32 v88,v88,v89
v_cvt_pk_bf16_f32 v89,v90,v91
v_cvt_pk_bf16_f32 v90,v12,v13
v_cvt_pk_bf16_f32 v91,v14,v15
v_mfma_f32_16x16x32_bf16 v[228:231],a[128:131],v[152:155],v[44:47]
s_waitcnt vmcnt(13)
v_accvgpr_read_b32 v92,a56
v_accvgpr_read_b32 v93,a57
v_accvgpr_read_b32 v94,a58
v_accvgpr_read_b32 v95,a59
v_accvgpr_read_b32 v12,a60
v_accvgpr_read_b32 v13,a61
v_accvgpr_read_b32 v14,a62
v_accvgpr_read_b32 v15,a63
v_pk_mul_f32 v[92:93],v[20:21],v[92:93] op_sel:[1,0] op_sel_hi:[1,1]
v_pk_mul_f32 v[94:95],v[20:21],v[94:95] op_sel:[1,0] op_sel_hi:[1,1]
v_pk_mul_f32 v[12:13],v[20:21],v[12:13] op_sel:[1,0] op_sel_hi:[1,1]
v_pk_mul_f32 v[14:15],v[20:21],v[14:15] op_sel:[1,0] op_sel_hi:[1,1]
v_cvt_pk_bf16_f32 v92,v92,v93
v_cvt_pk_bf16_f32 v93,v94,v95
v_cvt_pk_bf16_f32 v94,v12,v13
v_cvt_pk_bf16_f32 v95,v14,v15
v_mfma_f32_16x16x32_bf16 v[228:231],a[132:135],v[184:187],v[228:231]
s_barrier
ds_read_b128 a[128:131],v1
v_max_f32 v22,0x00800000,v56
v_mfma_f32_16x16x32_bf16 v[228:231],a[136:139],v[216:219],v[228:231]
s_cmp_eq_u32 s49,163840
s_cselect_b32 s49,0,s49
ds_read_b128 a[132:135],v1 offset:256
v_mfma_f32_16x16x32_bf16 v[228:231],a[140:143],v[248:251],v[228:231]
ds_read_b128 a[136:139],v1 offset:512
v_accvgpr_read_b32 v96,a64
v_mfma_f32_16x16x32_bf16 v[236:239],a[156:159],v[248:251],v[44:47]
ds_read_b128 a[140:143],v1 offset:768
v_accvgpr_read_b32 v97,a65
v_mfma_f32_16x16x32_bf16 v[236:239],a[152:155],v[216:219],v[236:239]
ds_read_b128 a[156:159],v1 offset:4864
v_accvgpr_read_b32 v98,a66
)A"
R"A(v_mfma_f32_16x16x32_bf16 v[236:239],a[148:151],v[184:187],v[236:239]
buffer_load_dwordx4 v5,s[24:27],s47 offen offset:1024 nt lds
v_mfma_f32_16x16x32_bf16 v[236:239],a[144:147],v[152:155],v[236:239]
ds_read_b128 a[152:155],v1 offset:4608
v_accvgpr_read_b32 v99,a67
v_mfma_f32_16x16x32_bf16 v[244:247],a[160:163],v[152:155],v[44:47]
v_cmp_nlt_f32 s[58:59],v52,s63
s_nop 1
s_or_b64 s[56:57],s[56:57],s[58:59]
v_mfma_f32_16x16x32_bf16 v[244:247],a[164:167],v[184:187],v[244:247]
ds_read_b128 a[148:151],v1 offset:4352
v_accvgpr_read_b32 v16,a70
v_mfma_f32_16x16x32_bf16 v[244:247],a[168:171],v[216:219],v[244:247]
ds_read_b128 a[144:147],v1 offset:4096
v_accvgpr_read_b32 v17,a71
v_mfma_f32_16x16x32_bf16 v[244:247],a[172:175],v[248:251],v[244:247]
ds_read_b128 a[160:163],v1 offset:8192
v_max_f32 v23,0x00800000,v60
v_mfma_f32_16x16x32_bf16 v[252:255],a[188:191],v[248:251],v[44:47]
ds_read_b128 a[164:167],v1 offset:8448
v_accvgpr_read_b32 v112,a96
v_mfma_f32_16x16x32_bf16 v[252:255],a[184:187],v[216:219],v[252:255]
ds_read_b128 a[168:171],v1 offset:8704
v_accvgpr_read_b32 v113,a97
v_mfma_f32_16x16x32_bf16 v[252:255],a[180:183],v[184:187],v[252:255]
ds_read_b128 a[172:175],v1 offset:8960
v_accvgpr_read_b32 v114,a98
v_mfma_f32_16x16x32_bf16 v[252:255],a[176:179],v[152:155],v[252:255]
ds_read_b128 a[188:191],v1 offset:13056
v_accvgpr_read_b32 v115,a99
v_mfma_f32_16x16x32_bf16 a[32:35],a[192:195],v[164:167],0
ds_read_b128 a[184:187],v1 offset:12800
v_add_u32 v2,s48,v2
v_mfma_f32_16x16x32_bf16 a[32:35],a[196:199],v[180:183],a[32:35]
buffer_load_dwordx4 v4,s[24:27],s47 offen offset:2048 nt lds
v_mfma_f32_16x16x32_bf16 a[36:39],a[204:207],v[180:183],0
ds_read_b128 a[180:183],v1 offset:12544
v_add_u32 v3,s48,v3
v_mfma_f32_16x16x32_bf16 a[36:39],a[200:203],v[164:167],a[36:39]
s_cmp_eq_u32 s50,131072
s_cselect_b32 s62,163840,0
ds_read_b128 a[176:179],v1 offset:12288
v_mfma_f32_16x16x32_bf16 a[40:43],a[208:211],v[164:167],0
s_sub_u32 s48,32768,s62
v_exp_f32 v196,v196
v_mfma_f32_16x16x32_bf16 a[40:43],a[212:215],v[180:183],a[40:43]
s_add_u32 s50,s50,s48
v_exp_f32 v197,v197
v_mfma_f32_16x16x32_bf16 a[44:47],a[220:223],v[180:183],0
v_exp_f32 v198,v198
v_mfma_f32_16x16x32_bf16 a[44:47],a[216:219],v[164:167],a[44:47]
v_exp_f32 v199,v199
v_mfma_f32_16x16x32_bf16 a[48:51],a[224:227],v[164:167],0
v_cvt_pk_bf16_f32 v196,v196,v197
v_cvt_pk_bf16_f32 v197,v198,v199
v_mfma_f32_16x16x32_bf16 a[48:51],a[228:231],v[180:183],a[48:51]
v_exp_f32 v204,v204
v_mfma_f32_16x16x32_bf16 a[52:55],a[236:239],v[180:183],0
v_exp_f32 v205,v205
v_mfma_f32_16x16x32_bf16 a[52:55],a[232:235],v[164:167],a[52:55]
v_exp_f32 v206,v206
v_mfma_f32_16x16x32_bf16 a[56:59],a[240:243],v[164:167],0
buffer_load_dwordx4 v5,s[24:27],s47 offen offset:3072 nt lds
v_mfma_f32_16x16x32_bf16 a[56:59],a[244:247],v[180:183],a[56:59]
s_add_u32 s47,s47,16384
s_and_b32 s47,s47,0x3ffff
s_or_b32 s47,s47,s46
v_mfma_f32_16x16x32_bf16 a[60:63],a[252:255],v[180:183],0
s_and_b32 s46,s54,0xfffc0000
v_exp_f32 v207,v207
v_mfma_f32_16x16x32_bf16 a[60:63],a[248:251],v[164:167],a[60:63]
v_exp_f32 v212,v212
v_mfma_f32_16x16x32_bf16 v[52:55],v[28:31],v[164:167],0
v_exp_f32 v213,v213
v_mfma_f32_16x16x32_bf16 v[52:55],v[28:31],v[180:183],v[52:55]
s_waitcnt lgkmcnt(0)
v_exp_f32 v214,v214
v_mfma_f32_16x16x32_bf16 v[132:135],a[128:131],v[128:131],v[32:35]
v_exp_f32 v215,v215
v_mfma_f32_16x16x32_bf16 v[132:135],a[132:135],v[160:163],v[132:135]
v_cvt_pk_bf16_f32 v198,v204,v205
v_cvt_pk_bf16_f32 v199,v206,v207
v_mfma_f32_16x16x32_bf16 v[132:135],a[136:139],v[192:195],v[132:135]
v_cvt_pk_bf16_f32 v212,v212,v213
v_cvt_pk_bf16_f32 v213,v214,v215
v_mfma_f32_16x16x32_bf16 v[132:135],a[140:143],v[224:227],v[132:135]
s_add_u32 m0,s49,s51
v_exp_f32 v220,v220
v_mfma_f32_16x16x32_bf16 v[140:143],a[156:159],v[224:227],v[32:35]
v_exp_f32 v221,v221
v_mfma_f32_16x16x32_bf16 v[140:143],a[152:155],v[192:195],v[140:143]
v_exp_f32 v222,v222
v_mfma_f32_16x16x32_bf16 v[140:143],a[148:151],v[160:163],v[140:143]
buffer_load_dwordx4 v4,s[20:23],s47 offen nt lds
v_mfma_f32_16x16x32_bf16 v[140:143],a[144:147],v[128:131],v[140:143]
v_exp_f32 v223,v223
v_mfma_f32_16x16x32_bf16 v[148:151],a[160:163],v[128:131],v[32:35]
v_cvt_pk_bf16_f32 v214,v220,v221
v_cvt_pk_bf16_f32 v215,v222,v223
v_mfma_f32_16x16x32_bf16 v[148:151],a[164:167],v[160:163],v[148:151]
v_rcp_f32 v22,v22
v_mfma_f32_16x16x32_bf16 v[148:151],a[168:171],v[192:195],v[148:151]
v_mul_f32 v96,v22,v96
v_mul_f32 v97,v22,v97
v_mfma_f32_16x16x32_bf16 v[148:151],a[172:175],v[224:227],v[148:151]
v_cvt_pk_bf16_f32 v96,v96,v97
v_mul_f32 v98,v22,v98
v_mfma_f32_16x16x32_bf16 v[156:159],a[188:191],v[224:227],v[32:35]
v_mul_f32 v99,v22,v99
v_cvt_pk_bf16_f32 v97,v98,v99
v_mfma_f32_16x16x32_bf16 v[156:159],a[184:187],v[192:195],v[156:159]
v_accvgpr_read_b32 v98,a68
v_accvgpr_read_b32 v99,a69
v_mfma_f32_16x16x32_bf16 v[156:159],a[180:183],v[160:163],v[156:159]
v_accvgpr_read_b32 v100,a72
v_accvgpr_read_b32 v101,a73
v_accvgpr_read_b32 v102,a74
v_accvgpr_read_b32 v103,a75
v_accvgpr_read_b32 v12,a76
v_accvgpr_read_b32 v13,a77
v_accvgpr_read_b32 v14,a78
v_accvgpr_read_b32 v15,a79
v_pk_mul_f32 v[100:101],v[22:23],v[100:101] op_sel:[0,0] op_sel_hi:[0,1]
v_pk_mul_f32 v[102:103],v[22:23],v[102:103] op_sel:[0,0] op_sel_hi:[0,1]
v_pk_mul_f32 v[12:13],v[22:23],v[12:13] op_sel:[0,0] op_sel_hi:[0,1]
v_pk_mul_f32 v[14:15],v[22:23],v[14:15] op_sel:[0,0] op_sel_hi:[0,1]
v_cvt_pk_bf16_f32 v100,v100,v101
v_cvt_pk_bf16_f32 v101,v102,v103
v_cvt_pk_bf16_f32 v102,v12,v13
v_cvt_pk_bf16_f32 v103,v14,v15
v_mfma_f32_16x16x32_bf16 v[156:159],a[176:179],v[128:131],v[156:159]
v_accvgpr_read_b32 v104,a80
v_accvgpr_read_b32 v105,a81
v_accvgpr_read_b32 v106,a82
v_accvgpr_read_b32 v107,a83
v_accvgpr_read_b32 v12,a84
v_accvgpr_read_b32 v13,a85
v_accvgpr_read_b32 v14,a86
v_accvgpr_read_b32 v15,a87
v_pk_mul_f32 v[104:105],v[22:23],v[104:105] op_sel:[0,0] op_sel_hi:[0,1]
v_pk_mul_f32 v[106:107],v[22:23],v[106:107] op_sel:[0,0] op_sel_hi:[0,1]
v_pk_mul_f32 v[12:13],v[22:23],v[12:13] op_sel:[0,0] op_sel_hi:[0,1]
v_pk_mul_f32 v[14:15],v[22:23],v[14:15] op_sel:[0,0] op_sel_hi:[0,1]
v_cvt_pk_bf16_f32 v104,v104,v105
v_cvt_pk_bf16_f32 v105,v106,v107
v_cvt_pk_bf16_f32 v106,v12,v13
v_cvt_pk_bf16_f32 v107,v14,v15
v_mfma_f32_16x16x32_bf16 a[64:67],a[192:195],v[196:199],0
v_accvgpr_read_b32 v108,a88
v_accvgpr_read_b32 v109,a89
v_accvgpr_read_b32 v110,a90
v_accvgpr_read_b32 v111,a91
v_accvgpr_read_b32 v12,a92
v_accvgpr_read_b32 v13,a93
v_accvgpr_read_b32 v14,a94
v_accvgpr_read_b32 v15,a95
v_pk_mul_f32 v[108:109],v[22:23],v[108:109] op_sel:[0,0] op_sel_hi:[0,1]
v_pk_mul_f32 v[110:111],v[22:23],v[110:111] op_sel:[0,0] op_sel_hi:[0,1]
v_pk_mul_f32 v[12:13],v[22:23],v[12:13] op_sel:[0,0] op_sel_hi:[0,1]
v_pk_mul_f32 v[14:15],v[22:23],v[14:15] op_sel:[0,0] op_sel_hi:[0,1]
v_cvt_pk_bf16_f32 v108,v108,v109
v_cvt_pk_bf16_f32 v109,v110,v111
v_cvt_pk_bf16_f32 v110,v12,v13
v_cvt_pk_bf16_f32 v111,v14,v15
v_mfma_f32_16x16x32_bf16 a[64:67],a[196:199],v[212:215],a[64:67]
buffer_load_dwordx4 v4,s[20:23],s47 offen offset:1024 nt lds
v_mfma_f32_16x16x32_bf16 a[68:71],a[204:207],v[212:215],0
v_cmp_nlt_f32 s[58:59],v56,s63
s_nop 1
s_or_b64 s[56:57],s[56:57],s[58:59]
v_mfma_f32_16x16x32_bf16 a[68:71],a[200:203],v[196:199],a[68:71]
v_exp_f32 v228,v228
v_mfma_f32_16x16x32_bf16 a[72:75],a[208:211],v[196:199],0
v_exp_f32 v229,v229
v_mfma_f32_16x16x32_bf16 a[72:75],a[212:215],v[212:215],a[72:75]
v_exp_f32 v230,v230
v_mfma_f32_16x16x32_bf16 a[76:79],a[220:223],v[212:215],0
v_exp_f32 v231,v231
v_mfma_f32_16x16x32_bf16 a[76:79],a[216:219],v[196:199],a[76:79]
v_cvt_pk_bf16_f32 v228,v228,v229
v_cvt_pk_bf16_f32 v229,v230,v231
v_mfma_f32_16x16x32_bf16 a[80:83],a[224:227],v[196:199],0
v_exp_f32 v236,v236
v_mfma_f32_16x16x32_bf16 a[80:83],a[228:231],v[212:215],a[80:83]
v_exp_f32 v237,v237
v_mfma_f32_16x16x32_bf16 a[84:87],a[236:239],v[212:215],0
v_exp_f32 v238,v238
v_mfma_f32_16x16x32_bf16 a[84:87],a[232:235],v[196:199],a[84:87]
v_exp_f32 v239,v239
v_mfma_f32_16x16x32_bf16 a[88:91],a[240:243],v[196:199],0
buffer_load_dwordx4 v5,s[20:23],s47 offen offset:2048 nt lds
v_mfma_f32_16x16x32_bf16 a[88:91],a[244:247],v[212:215],a[88:91]
v_exp_f32 v244,v244
v_mfma_f32_16x16x32_bf16 a[92:95],a[252:255],v[212:215],0
v_exp_f32 v245,v245
v_mfma_f32_16x16x32_bf16 a[92:95],a[248:251],v[196:199],a[92:95]
v_exp_f32 v246,v246
v_mfma_f32_16x16x32_bf16 v[56:59],v[28:31],v[196:199],0
v_exp_f32 v247,v247
v_mfma_f32_16x16x32_bf16 v[56:59],v[28:31],v[212:215],v[56:59]
v_cvt_pk_bf16_f32 v230,v236,v237
v_cvt_pk_bf16_f32 v231,v238,v239
v_mfma_f32_16x16x32_bf16 v[164:167],a[128:131],v[136:139],v[36:39]
v_cvt_pk_bf16_f32 v244,v244,v245
v_cvt_pk_bf16_f32 v245,v246,v247
v_mfma_f32_16x16x32_bf16 v[164:167],a[132:135],v[168:171],v[164:167]
v_exp_f32 v252,v252
v_mfma_f32_16x16x32_bf16 v[164:167],a[136:139],v[200:203],v[164:167]
v_exp_f32 v253,v253
v_mfma_f32_16x16x32_bf16 v[164:167],a[140:143],v[232:235],v[164:167]
v_exp_f32 v254,v254
v_mfma_f32_16x16x32_bf16 v[172:175],a[156:159],v[232:235],v[36:39]
v_exp_f32 v255,v255
v_mfma_f32_16x16x32_bf16 v[172:175],a[152:155],v[200:203],v[172:175]
buffer_load_dwordx4 v5,s[20:23],s47 offen offset:3072 nt lds
v_mfma_f32_16x16x32_bf16 v[172:175],a[148:151],v[168:171],v[172:175]
v_rcp_f32 v23,v23
v_mfma_f32_16x16x32_bf16 v[172:175],a[144:147],v[136:139],v[172:175]
v_cvt_pk_bf16_f32 v246,v252,v253
v_cvt_pk_bf16_f32 v247,v254,v255
v_mfma_f32_16x16x32_bf16 v[180:183],a[160:163],v[136:139],v[36:39]
v_mul_f32 v112,v23,v112
v_mul_f32 v113,v23,v113
v_mfma_f32_16x16x32_bf16 v[180:183],a[164:167],v[168:171],v[180:183]
v_cvt_pk_bf16_f32 v112,v112,v113
v_mul_f32 v114,v23,v114
v_mfma_f32_16x16x32_bf16 v[180:183],a[168:171],v[200:203],v[180:183]
v_mul_f32 v115,v23,v115
v_cvt_pk_bf16_f32 v113,v114,v115
v_mfma_f32_16x16x32_bf16 v[180:183],a[172:175],v[232:235],v[180:183]
v_accvgpr_read_b32 v114,a100
v_accvgpr_read_b32 v115,a101
v_mfma_f32_16x16x32_bf16 v[188:191],a[188:191],v[232:235],v[36:39]
v_accvgpr_read_b32 v12,a102
v_accvgpr_read_b32 v13,a103
v_mfma_f32_16x16x32_bf16 v[188:191],a[184:187],v[200:203],v[188:191]
s_add_u32 m0,s49,s52
v_mul_f32 v114,v23,v114
v_mul_f32 v115,v23,v115
v_mfma_f32_16x16x32_bf16 v[188:191],a[180:183],v[168:171],v[188:191]
s_add_u32 s49,s49,32768
v_mul_f32 v12,v23,v12
v_mul_f32 v13,v23,v13
v_mfma_f32_16x16x32_bf16 v[188:191],a[176:179],v[136:139],v[188:191]
v_cvt_pk_bf16_f32 v114,v114,v115
v_cvt_pk_bf16_f32 v115,v12,v13
v_mfma_f32_16x16x32_bf16 a[96:99],a[192:195],v[228:231],0
v_accvgpr_read_b32 v116,a104
v_accvgpr_read_b32 v117,a105
v_accvgpr_read_b32 v118,a106
v_accvgpr_read_b32 v119,a107
v_accvgpr_read_b32 v12,a108
v_accvgpr_read_b32 v13,a109
v_accvgpr_read_b32 v14,a110
v_accvgpr_read_b32 v15,a111
v_pk_mul_f32 v[116:117],v[22:23],v[116:117] op_sel:[1,0] op_sel_hi:[1,1]
v_pk_mul_f32 v[118:119],v[22:23],v[118:119] op_sel:[1,0] op_sel_hi:[1,1]
v_pk_mul_f32 v[12:13],v[22:23],v[12:13] op_sel:[1,0] op_sel_hi:[1,1]
v_pk_mul_f32 v[14:15],v[22:23],v[14:15] op_sel:[1,0] op_sel_hi:[1,1]
v_cvt_pk_bf16_f32 v116,v116,v117
v_cvt_pk_bf16_f32 v117,v118,v119
v_cvt_pk_bf16_f32 v118,v12,v13
v_cvt_pk_bf16_f32 v119,v14,v15
v_mfma_f32_16x16x32_bf16 a[96:99],a[196:199],v[244:247],a[96:99]
buffer_load_dwordx4 v4,s[24:27],s47 offen nt lds
v_mfma_f32_16x16x32_bf16 a[100:103],a[204:207],v[244:247],0
v_accvgpr_read_b32 v120,a112
v_accvgpr_read_b32 v121,a113
v_accvgpr_read_b32 v122,a114
v_accvgpr_read_b32 v123,a115
v_accvgpr_read_b32 v12,a116
v_accvgpr_read_b32 v13,a117
v_accvgpr_read_b32 v14,a118
v_accvgpr_read_b32 v15,a119
v_pk_mul_f32 v[120:121],v[22:23],v[120:121] op_sel:[1,0] op_sel_hi:[1,1]
v_pk_mul_f32 v[122:123],v[22:23],v[122:123] op_sel:[1,0] op_sel_hi:[1,1]
v_pk_mul_f32 v[12:13],v[22:23],v[12:13] op_sel:[1,0] op_sel_hi:[1,1]
v_pk_mul_f32 v[14:15],v[22:23],v[14:15] op_sel:[1,0] op_sel_hi:[1,1]
v_cvt_pk_bf16_f32 v120,v120,v121
v_cvt_pk_bf16_f32 v121,v122,v123
v_cvt_pk_bf16_f32 v122,v12,v13
v_cvt_pk_bf16_f32 v123,v14,v15
v_mfma_f32_16x16x32_bf16 a[100:103],a[200:203],v[228:231],a[100:103]
v_accvgpr_read_b32 v124,a120
v_accvgpr_read_b32 v125,a121
)A"
R"A(v_accvgpr_read_b32 v126,a122
v_accvgpr_read_b32 v127,a123
v_accvgpr_read_b32 v12,a124
v_accvgpr_read_b32 v13,a125
v_accvgpr_read_b32 v14,a126
v_accvgpr_read_b32 v15,a127
v_pk_mul_f32 v[124:125],v[22:23],v[124:125] op_sel:[1,0] op_sel_hi:[1,1]
v_pk_mul_f32 v[126:127],v[22:23],v[126:127] op_sel:[1,0] op_sel_hi:[1,1]
v_pk_mul_f32 v[12:13],v[22:23],v[12:13] op_sel:[1,0] op_sel_hi:[1,1]
v_pk_mul_f32 v[14:15],v[22:23],v[14:15] op_sel:[1,0] op_sel_hi:[1,1]
v_cvt_pk_bf16_f32 v124,v124,v125
v_cvt_pk_bf16_f32 v125,v126,v127
v_cvt_pk_bf16_f32 v126,v12,v13
v_cvt_pk_bf16_f32 v127,v14,v15
v_mfma_f32_16x16x32_bf16 a[104:107],a[208:211],v[228:231],0
v_cmp_nlt_f32 s[58:59],v60,s63
s_nop 1
s_or_b64 s[56:57],s[56:57],s[58:59]
v_mfma_f32_16x16x32_bf16 a[104:107],a[212:215],v[244:247],a[104:107]
ds_read_b64_tr_b16 a[192:193],v2
ds_read_b64_tr_b16 a[194:195],v2 offset:4096
v_mfma_f32_16x16x32_bf16 a[108:111],a[220:223],v[244:247],0
ds_read_b64_tr_b16 a[196:197],v2 offset:8192
ds_read_b64_tr_b16 a[198:199],v2 offset:12288
v_mfma_f32_16x16x32_bf16 a[108:111],a[216:219],v[228:231],a[108:111]
ds_read_b64_tr_b16 a[204:205],v3 offset:8192
ds_read_b64_tr_b16 a[206:207],v3 offset:12288
v_mfma_f32_16x16x32_bf16 a[112:115],a[224:227],v[228:231],0
ds_read_b64_tr_b16 a[200:201],v3
ds_read_b64_tr_b16 a[202:203],v3 offset:4096
v_mfma_f32_16x16x32_bf16 a[112:115],a[228:231],v[244:247],a[112:115]
v_exp_f32 v132,v132
v_mfma_f32_16x16x32_bf16 a[116:119],a[236:239],v[244:247],0
v_exp_f32 v133,v133
v_mfma_f32_16x16x32_bf16 a[116:119],a[232:235],v[228:231],a[116:119]
buffer_load_dwordx4 v5,s[24:27],s47 offen offset:1024 nt lds
v_mfma_f32_16x16x32_bf16 a[120:123],a[240:243],v[228:231],0
ds_read_b64_tr_b16 a[208:209],v2 offset:256
ds_read_b64_tr_b16 a[210:211],v2 offset:4352
v_mfma_f32_16x16x32_bf16 a[120:123],a[244:247],v[244:247],a[120:123]
v_exp_f32 v134,v134
v_mfma_f32_16x16x32_bf16 a[124:127],a[252:255],v[244:247],0
v_exp_f32 v135,v135
v_mfma_f32_16x16x32_bf16 a[124:127],a[248:251],v[228:231],a[124:127]
ds_read_b64_tr_b16 a[212:213],v2 offset:8448
v_cvt_pk_bf16_f32 v132,v132,v133
v_mfma_f32_16x16x32_bf16 v[60:63],v[28:31],v[228:231],0
v_exp_f32 v140,v140
v_mfma_f32_16x16x32_bf16 v[60:63],v[28:31],v[244:247],v[60:63]
v_exp_f32 v141,v141
v_mfma_f32_16x16x32_bf16 v[196:199],a[128:131],v[144:147],v[40:43]
v_exp_f32 v142,v142
v_mfma_f32_16x16x32_bf16 v[196:199],a[132:135],v[176:179],v[196:199]
v_exp_f32 v143,v143
v_mfma_f32_16x16x32_bf16 v[196:199],a[136:139],v[208:211],v[196:199]
v_exp_f32 v148,v148
v_mfma_f32_16x16x32_bf16 v[196:199],a[140:143],v[240:243],v[196:199]
v_exp_f32 v149,v149
v_mfma_f32_16x16x32_bf16 v[204:207],a[156:159],v[240:243],v[40:43]
buffer_load_dwordx4 v4,s[24:27],s47 offen offset:2048 nt lds
v_mfma_f32_16x16x32_bf16 v[204:207],a[152:155],v[208:211],v[204:207]
ds_read_b64_tr_b16 a[214:215],v2 offset:12544
v_cvt_pk_bf16_f32 v133,v134,v135
v_mfma_f32_16x16x32_bf16 v[204:207],a[148:151],v[176:179],v[204:207]
v_exp_f32 v150,v150
v_mfma_f32_16x16x32_bf16 v[204:207],a[144:147],v[144:147],v[204:207]
v_exp_f32 v151,v151
v_mfma_f32_16x16x32_bf16 v[212:215],a[160:163],v[144:147],v[40:43]
v_cvt_pk_bf16_f32 v134,v140,v141
v_cvt_pk_bf16_f32 v135,v142,v143
v_mfma_f32_16x16x32_bf16 v[212:215],a[164:167],v[176:179],v[212:215]
ds_read_b64_tr_b16 a[220:221],v3 offset:8448
v_cvt_pk_bf16_f32 v148,v148,v149
v_mfma_f32_16x16x32_bf16 v[212:215],a[168:171],v[208:211],v[212:215]
v_exp_f32 v156,v156
v_mfma_f32_16x16x32_bf16 v[212:215],a[172:175],v[240:243],v[212:215]
v_exp_f32 v157,v157
v_mfma_f32_16x16x32_bf16 v[220:223],a[188:191],v[240:243],v[40:43]
v_exp_f32 v158,v158
v_mfma_f32_16x16x32_bf16 v[220:223],a[184:187],v[208:211],v[220:223]
ds_read_b64_tr_b16 a[222:223],v3 offset:12544
v_exp_f32 v159,v159
v_mfma_f32_16x16x32_bf16 v[220:223],a[180:183],v[176:179],v[220:223]
ds_read_b64_tr_b16 a[216:217],v3 offset:256
ds_read_b64_tr_b16 a[218:219],v3 offset:4352
v_cvt_pk_bf16_f32 v149,v150,v151
v_cvt_pk_bf16_f32 v150,v156,v157
v_cvt_pk_bf16_f32 v151,v158,v159
v_mfma_f32_16x16x32_bf16 v[220:223],a[176:179],v[144:147],v[220:223]
s_waitcnt lgkmcnt(4)
ds_read_b64_tr_b16 a[224:225],v2 offset:512
ds_read_b64_tr_b16 a[226:227],v2 offset:4608
v_mfma_f32_16x16x32_bf16 a[0:3],a[192:195],v[132:135],a[0:3]
ds_read_b64_tr_b16 a[228:229],v2 offset:8704
ds_read_b64_tr_b16 a[230:231],v2 offset:12800
v_mfma_f32_16x16x32_bf16 a[0:3],a[196:199],v[148:151],a[0:3]
ds_read_b64_tr_b16 a[236:237],v3 offset:8704
ds_read_b64_tr_b16 a[238:239],v3 offset:12800
v_mfma_f32_16x16x32_bf16 a[4:7],a[204:207],v[148:151],a[4:7]
buffer_load_dwordx4 v5,s[24:27],s47 offen offset:3072 nt lds
ds_read_b64_tr_b16 a[232:233],v3 offset:512
ds_read_b64_tr_b16 a[234:235],v3 offset:4608
v_mfma_f32_16x16x32_bf16 a[4:7],a[200:203],v[132:135],a[4:7]
s_add_u32 s47,s47,16384
ds_read_b64_tr_b16 a[240:241],v2 offset:768
ds_read_b64_tr_b16 a[242:243],v2 offset:4864
v_mfma_f32_16x16x32_bf16 a[8:11],a[208:211],v[132:135],a[8:11]
s_memrealtime s[76:77]
ds_read_b64_tr_b16 a[244:245],v2 offset:8960
ds_read_b64_tr_b16 a[246:247],v2 offset:13056
v_mfma_f32_16x16x32_bf16 a[8:11],a[212:215],v[148:151],a[8:11]
s_and_b32 s47,s47,0x3ffff
ds_read_b64_tr_b16 a[252:253],v3 offset:8960
ds_read_b64_tr_b16 a[254:255],v3 offset:13056
s_waitcnt lgkmcnt(15)
v_mfma_f32_16x16x32_bf16 a[12:15],a[220:223],v[148:151],a[12:15]
s_or_b32 s47,s47,s46
ds_read_b64_tr_b16 a[248:249],v3 offset:768
ds_read_b64_tr_b16 a[250:251],v3 offset:4864
s_waitcnt lgkmcnt(15)
v_mfma_f32_16x16x32_bf16 a[12:15],a[216:219],v[132:135],a[12:15]
s_waitcnt lgkmcnt(14)
v_mul_f32 v98,v22,v98
v_mul_f32 v99,v22,v99
v_mfma_f32_16x16x32_bf16 a[16:19],a[224:227],v[132:135],a[16:19]
s_waitcnt lgkmcnt(12)
v_exp_f32 v164,v164
v_mfma_f32_16x16x32_bf16 a[16:19],a[228:231],v[148:151],a[16:19]
s_waitcnt lgkmcnt(10)
v_exp_f32 v165,v165
v_mfma_f32_16x16x32_bf16 a[20:23],a[236:239],v[148:151],a[20:23]
s_waitcnt lgkmcnt(8)
v_exp_f32 v180,v180
v_mfma_f32_16x16x32_bf16 a[20:23],a[232:235],v[132:135],a[20:23]
s_waitcnt lgkmcnt(0)
v_exp_f32 v181,v181
v_mfma_f32_16x16x32_bf16 a[24:27],a[240:243],v[132:135],a[24:27]
s_sub_u32 s79,s78,s76
v_mul_f32 v16,v22,v16
v_mul_f32 v17,v22,v17
v_mfma_f32_16x16x32_bf16 a[24:27],a[244:247],v[148:151],a[24:27]
s_add_u32 s78,s76,s60
v_cvt_pk_bf16_f32 v98,v98,v99
v_cvt_pk_bf16_f32 v99,v16,v17
v_mfma_f32_16x16x32_bf16 a[28:31],a[252:255],v[148:151],a[28:31]
s_cmp_gt_i32 s79,0
s_cbranch_scc1 L_ps6_%=
L_pr6_%=:
v_cvt_pk_bf16_f32 v164,v164,v165
v_mfma_f32_16x16x32_bf16 a[28:31],a[248:251],v[132:135],a[28:31]
v_exp_f32 v166,v166
v_exp_f32 v167,v167
v_exp_f32 v182,v182
v_exp_f32 v183,v183
v_exp_f32 v172,v172
v_exp_f32 v173,v173
v_exp_f32 v174,v174
v_exp_f32 v175,v175
v_cvt_pk_bf16_f32 v180,v180,v181
v_exp_f32 v188,v188
v_exp_f32 v189,v189
v_exp_f32 v190,v190
v_exp_f32 v191,v191
v_mfma_f32_16x16x32_bf16 v[48:51],v[28:31],v[132:135],v[48:51]
s_cmp_lg_u64 s[56:57],0
s_cbranch_scc1 L_slow0_%=
L_sret0_%=:
s_cmp_eq_u32 s49,163840
s_cselect_b32 s49,0,s49
v_cvt_pk_bf16_f32 v165,v166,v167
v_cvt_pk_bf16_f32 v181,v182,v183
v_add_u32 v1,s48,v1
v_cvt_pk_bf16_f32 v166,v172,v173
v_cvt_pk_bf16_f32 v167,v174,v175
v_cvt_pk_bf16_f32 v182,v188,v189
v_cvt_pk_bf16_f32 v183,v190,v191
v_mfma_f32_16x16x32_bf16 v[48:51],v[28:31],v[148:151],v[48:51]
s_mov_b32 s53,11
L_att1_%=:
v_mfma_f32_16x16x32_bf16 v[228:231],a[128:131],v[152:155],v[44:47]
s_waitcnt vmcnt(16) lgkmcnt(0)
v_exp_f32 v196,v196
v_mfma_f32_16x16x32_bf16 v[228:231],a[132:135],v[184:187],v[228:231]
s_barrier
s_add_u32 m0,s49,s51
ds_read_b128 a[128:131],v1
v_mfma_f32_16x16x32_bf16 v[228:231],a[136:139],v[216:219],v[228:231]
s_mov_b32 s90,L_fast_end_%=-L_pc_%=+128
v_exp_f32 v197,v197
v_mfma_f32_16x16x32_bf16 v[228:231],a[140:143],v[248:251],v[228:231]
s_add_u32 s54,s54,16384
ds_read_b128 a[132:135],v1 offset:256
v_cvt_pk_bf16_f32 v196,v196,v197
v_mfma_f32_16x16x32_bf16 v[236:239],a[156:159],v[248:251],v[44:47]
s_sub_u32 s53,s53,1
buffer_load_dwordx4 v4,s[20:23],s47 offen nt lds
v_mfma_f32_16x16x32_bf16 v[236:239],a[152:155],v[216:219],v[236:239]
s_and_b32 s46,s54,0xfffc0000
ds_read_b128 a[136:139],v1 offset:512
v_add_u32 v2,s48,v2
v_mfma_f32_16x16x32_bf16 v[236:239],a[148:151],v[184:187],v[236:239]
v_exp_f32 v198,v198
v_mfma_f32_16x16x32_bf16 v[236:239],a[144:147],v[152:155],v[236:239]
ds_read_b128 a[140:143],v1 offset:768
v_add_u32 v3,s48,v3
v_mfma_f32_16x16x32_bf16 v[244:247],a[160:163],v[152:155],v[44:47]
v_exp_f32 v199,v199
v_mfma_f32_16x16x32_bf16 v[244:247],a[164:167],v[184:187],v[244:247]
ds_read_b128 a[156:159],v1 offset:4864
v_cvt_pk_bf16_f32 v197,v198,v199
v_mfma_f32_16x16x32_bf16 v[244:247],a[168:171],v[216:219],v[244:247]
v_exp_f32 v204,v204
v_mfma_f32_16x16x32_bf16 v[244:247],a[172:175],v[248:251],v[244:247]
s_cmp_eq_u32 s50,131072
s_cselect_b32 s62,163840,0
ds_read_b128 a[152:155],v1 offset:4608
v_mfma_f32_16x16x32_bf16 v[252:255],a[188:191],v[248:251],v[44:47]
s_sub_u32 s48,32768,s62
v_exp_f32 v205,v205
v_mfma_f32_16x16x32_bf16 v[252:255],a[184:187],v[216:219],v[252:255]
s_add_u32 s50,s50,s48
ds_read_b128 a[148:151],v1 offset:4352
v_cvt_pk_bf16_f32 v198,v204,v205
v_mfma_f32_16x16x32_bf16 v[252:255],a[180:183],v[184:187],v[252:255]
v_exp_f32 v206,v206
v_mfma_f32_16x16x32_bf16 v[252:255],a[176:179],v[152:155],v[252:255]
ds_read_b128 a[144:147],v1 offset:4096
v_mfma_f32_16x16x32_bf16 a[32:35],a[192:195],v[164:167],a[32:35]
v_exp_f32 v207,v207
v_mfma_f32_16x16x32_bf16 a[32:35],a[196:199],v[180:183],a[32:35]
ds_read_b128 a[160:163],v1 offset:8192
v_cvt_pk_bf16_f32 v199,v206,v207
v_mfma_f32_16x16x32_bf16 a[36:39],a[204:207],v[180:183],a[36:39]
buffer_load_dwordx4 v4,s[20:23],s47 offen offset:1024 nt lds
v_mfma_f32_16x16x32_bf16 a[36:39],a[200:203],v[164:167],a[36:39]
ds_read_b128 a[164:167],v1 offset:8448
v_mfma_f32_16x16x32_bf16 a[40:43],a[208:211],v[164:167],a[40:43]
v_exp_f32 v212,v212
v_mfma_f32_16x16x32_bf16 a[40:43],a[212:215],v[180:183],a[40:43]
ds_read_b128 a[168:171],v1 offset:8704
v_mfma_f32_16x16x32_bf16 a[44:47],a[220:223],v[180:183],a[44:47]
v_exp_f32 v213,v213
v_mfma_f32_16x16x32_bf16 a[44:47],a[216:219],v[164:167],a[44:47]
ds_read_b128 a[172:175],v1 offset:8960
v_cvt_pk_bf16_f32 v212,v212,v213
v_mfma_f32_16x16x32_bf16 a[48:51],a[224:227],v[164:167],a[48:51]
v_exp_f32 v214,v214
v_mfma_f32_16x16x32_bf16 a[48:51],a[228:231],v[180:183],a[48:51]
ds_read_b128 a[188:191],v1 offset:13056
v_mfma_f32_16x16x32_bf16 a[52:55],a[236:239],v[180:183],a[52:55]
v_exp_f32 v215,v215
v_mfma_f32_16x16x32_bf16 a[52:55],a[232:235],v[164:167],a[52:55]
ds_read_b128 a[184:187],v1 offset:12800
v_cvt_pk_bf16_f32 v213,v214,v215
v_mfma_f32_16x16x32_bf16 a[56:59],a[240:243],v[164:167],a[56:59]
v_exp_f32 v220,v220
v_mfma_f32_16x16x32_bf16 a[56:59],a[244:247],v[180:183],a[56:59]
ds_read_b128 a[180:183],v1 offset:12544
v_mfma_f32_16x16x32_bf16 a[60:63],a[252:255],v[180:183],a[60:63]
v_exp_f32 v221,v221
v_mfma_f32_16x16x32_bf16 a[60:63],a[248:251],v[164:167],a[60:63]
ds_read_b128 a[176:179],v1 offset:12288
v_cvt_pk_bf16_f32 v214,v220,v221
v_mfma_f32_16x16x32_bf16 v[52:55],v[28:31],v[164:167],v[52:55]
buffer_load_dwordx4 v5,s[20:23],s47 offen offset:2048 nt lds
v_mfma_f32_16x16x32_bf16 v[52:55],v[28:31],v[180:183],v[52:55]
s_waitcnt lgkmcnt(3)
v_exp_f32 v222,v222
v_mfma_f32_16x16x32_bf16 v[132:135],a[128:131],v[128:131],v[32:35]
v_exp_f32 v223,v223
v_mfma_f32_16x16x32_bf16 v[132:135],a[132:135],v[160:163],v[132:135]
v_cvt_pk_bf16_f32 v215,v222,v223
v_add_u32 v1,s48,v1
v_mfma_f32_16x16x32_bf16 v[132:135],a[136:139],v[192:195],v[132:135]
v_exp_f32 v228,v228
v_mfma_f32_16x16x32_bf16 v[132:135],a[140:143],v[224:227],v[132:135]
v_exp_f32 v229,v229
v_mfma_f32_16x16x32_bf16 v[140:143],a[156:159],v[224:227],v[32:35]
v_exp_f32 v230,v230
v_mfma_f32_16x16x32_bf16 v[140:143],a[152:155],v[192:195],v[140:143]
v_exp_f32 v231,v231
v_mfma_f32_16x16x32_bf16 v[140:143],a[148:151],v[160:163],v[140:143]
v_cvt_pk_bf16_f32 v228,v228,v229
v_cvt_pk_bf16_f32 v229,v230,v231
v_mfma_f32_16x16x32_bf16 v[140:143],a[144:147],v[128:131],v[140:143]
v_exp_f32 v236,v236
v_mfma_f32_16x16x32_bf16 v[148:151],a[160:163],v[128:131],v[32:35]
v_exp_f32 v237,v237
)A"
R"A(v_mfma_f32_16x16x32_bf16 v[148:151],a[164:167],v[160:163],v[148:151]
v_exp_f32 v238,v238
v_mfma_f32_16x16x32_bf16 v[148:151],a[168:171],v[192:195],v[148:151]
v_exp_f32 v239,v239
v_mfma_f32_16x16x32_bf16 v[148:151],a[172:175],v[224:227],v[148:151]
v_exp_f32 v244,v244
v_mfma_f32_16x16x32_bf16 v[156:159],a[188:191],v[224:227],v[32:35]
s_waitcnt lgkmcnt(0)
buffer_load_dwordx4 v5,s[20:23],s47 offen offset:3072 nt lds
v_mfma_f32_16x16x32_bf16 v[156:159],a[184:187],v[192:195],v[156:159]
v_exp_f32 v245,v245
v_mfma_f32_16x16x32_bf16 v[156:159],a[180:183],v[160:163],v[156:159]
v_exp_f32 v246,v246
v_mfma_f32_16x16x32_bf16 v[156:159],a[176:179],v[128:131],v[156:159]
v_exp_f32 v247,v247
v_mfma_f32_16x16x32_bf16 a[64:67],a[192:195],v[196:199],a[64:67]
v_cvt_pk_bf16_f32 v230,v236,v237
v_cvt_pk_bf16_f32 v231,v238,v239
v_mfma_f32_16x16x32_bf16 a[64:67],a[196:199],v[212:215],a[64:67]
v_cvt_pk_bf16_f32 v244,v244,v245
v_cvt_pk_bf16_f32 v245,v246,v247
v_mfma_f32_16x16x32_bf16 a[68:71],a[204:207],v[212:215],a[68:71]
v_exp_f32 v252,v252
v_mfma_f32_16x16x32_bf16 a[68:71],a[200:203],v[196:199],a[68:71]
v_exp_f32 v253,v253
v_mfma_f32_16x16x32_bf16 a[72:75],a[208:211],v[196:199],a[72:75]
v_exp_f32 v254,v254
v_mfma_f32_16x16x32_bf16 a[72:75],a[212:215],v[212:215],a[72:75]
v_exp_f32 v255,v255
v_mfma_f32_16x16x32_bf16 a[76:79],a[220:223],v[212:215],a[76:79]
v_cvt_pk_bf16_f32 v246,v252,v253
v_cvt_pk_bf16_f32 v247,v254,v255
v_mfma_f32_16x16x32_bf16 a[76:79],a[216:219],v[196:199],a[76:79]
s_add_u32 m0,s49,s52
v_exp_f32 v132,v132
v_mfma_f32_16x16x32_bf16 a[80:83],a[224:227],v[196:199],a[80:83]
s_add_u32 s49,s49,32768
v_exp_f32 v133,v133
v_mfma_f32_16x16x32_bf16 a[80:83],a[228:231],v[212:215],a[80:83]
v_exp_f32 v134,v134
v_mfma_f32_16x16x32_bf16 a[84:87],a[236:239],v[212:215],a[84:87]
buffer_load_dwordx4 v4,s[24:27],s47 offen nt lds
v_mfma_f32_16x16x32_bf16 a[84:87],a[232:235],v[196:199],a[84:87]
v_exp_f32 v135,v135
v_mfma_f32_16x16x32_bf16 a[88:91],a[240:243],v[196:199],a[88:91]
v_cvt_pk_bf16_f32 v132,v132,v133
v_cvt_pk_bf16_f32 v133,v134,v135
v_mfma_f32_16x16x32_bf16 a[88:91],a[244:247],v[212:215],a[88:91]
v_exp_f32 v140,v140
v_mfma_f32_16x16x32_bf16 a[92:95],a[252:255],v[212:215],a[92:95]
v_exp_f32 v141,v141
v_mfma_f32_16x16x32_bf16 a[92:95],a[248:251],v[196:199],a[92:95]
v_exp_f32 v142,v142
v_mfma_f32_16x16x32_bf16 v[56:59],v[28:31],v[196:199],v[56:59]
v_exp_f32 v143,v143
v_mfma_f32_16x16x32_bf16 v[56:59],v[28:31],v[212:215],v[56:59]
v_exp_f32 v148,v148
v_mfma_f32_16x16x32_bf16 v[164:167],a[128:131],v[136:139],v[36:39]
v_exp_f32 v149,v149
v_mfma_f32_16x16x32_bf16 v[164:167],a[132:135],v[168:171],v[164:167]
v_exp_f32 v150,v150
v_mfma_f32_16x16x32_bf16 v[164:167],a[136:139],v[200:203],v[164:167]
v_exp_f32 v151,v151
v_mfma_f32_16x16x32_bf16 v[164:167],a[140:143],v[232:235],v[164:167]
v_cvt_pk_bf16_f32 v134,v140,v141
v_cvt_pk_bf16_f32 v135,v142,v143
v_mfma_f32_16x16x32_bf16 v[172:175],a[156:159],v[232:235],v[36:39]
v_cvt_pk_bf16_f32 v148,v148,v149
v_cvt_pk_bf16_f32 v149,v150,v151
v_mfma_f32_16x16x32_bf16 v[172:175],a[152:155],v[200:203],v[172:175]
v_exp_f32 v156,v156
v_mfma_f32_16x16x32_bf16 v[172:175],a[148:151],v[168:171],v[172:175]
buffer_load_dwordx4 v5,s[24:27],s47 offen offset:1024 nt lds
v_mfma_f32_16x16x32_bf16 v[172:175],a[144:147],v[136:139],v[172:175]
v_exp_f32 v157,v157
v_mfma_f32_16x16x32_bf16 v[180:183],a[160:163],v[136:139],v[36:39]
v_exp_f32 v158,v158
v_mfma_f32_16x16x32_bf16 v[180:183],a[164:167],v[168:171],v[180:183]
v_exp_f32 v159,v159
v_mfma_f32_16x16x32_bf16 v[180:183],a[168:171],v[200:203],v[180:183]
v_cvt_pk_bf16_f32 v150,v156,v157
v_cvt_pk_bf16_f32 v151,v158,v159
v_mfma_f32_16x16x32_bf16 v[180:183],a[172:175],v[232:235],v[180:183]
v_exp_f32 v164,v164
v_mfma_f32_16x16x32_bf16 v[188:191],a[188:191],v[232:235],v[36:39]
v_exp_f32 v165,v165
v_mfma_f32_16x16x32_bf16 v[188:191],a[184:187],v[200:203],v[188:191]
v_exp_f32 v166,v166
v_mfma_f32_16x16x32_bf16 v[188:191],a[180:183],v[168:171],v[188:191]
v_exp_f32 v167,v167
v_mfma_f32_16x16x32_bf16 v[188:191],a[176:179],v[136:139],v[188:191]
v_cvt_pk_bf16_f32 v164,v164,v165
v_cvt_pk_bf16_f32 v165,v166,v167
v_mfma_f32_16x16x32_bf16 a[96:99],a[192:195],v[228:231],a[96:99]
s_cmp_eq_u32 s49,163840
s_cselect_b32 s49,0,s49
ds_read_b64_tr_b16 a[192:193],v2
v_mfma_f32_16x16x32_bf16 a[96:99],a[196:199],v[244:247],a[96:99]
ds_read_b64_tr_b16 a[194:195],v2 offset:4096
v_mfma_f32_16x16x32_bf16 a[100:103],a[204:207],v[244:247],a[100:103]
ds_read_b64_tr_b16 a[196:197],v2 offset:8192
v_mfma_f32_16x16x32_bf16 a[100:103],a[200:203],v[228:231],a[100:103]
ds_read_b64_tr_b16 a[198:199],v2 offset:12288
v_mfma_f32_16x16x32_bf16 a[104:107],a[208:211],v[228:231],a[104:107]
buffer_load_dwordx4 v4,s[24:27],s47 offen offset:2048 nt lds
v_mfma_f32_16x16x32_bf16 a[104:107],a[212:215],v[244:247],a[104:107]
ds_read_b64_tr_b16 a[204:205],v3 offset:8192
v_mfma_f32_16x16x32_bf16 a[108:111],a[220:223],v[244:247],a[108:111]
ds_read_b64_tr_b16 a[206:207],v3 offset:12288
v_mfma_f32_16x16x32_bf16 a[108:111],a[216:219],v[228:231],a[108:111]
ds_read_b64_tr_b16 a[200:201],v3
v_mfma_f32_16x16x32_bf16 a[112:115],a[224:227],v[228:231],a[112:115]
ds_read_b64_tr_b16 a[202:203],v3 offset:4096
v_mfma_f32_16x16x32_bf16 a[112:115],a[228:231],v[244:247],a[112:115]
ds_read_b64_tr_b16 a[208:209],v2 offset:256
v_mfma_f32_16x16x32_bf16 a[116:119],a[236:239],v[244:247],a[116:119]
ds_read_b64_tr_b16 a[210:211],v2 offset:4352
v_mfma_f32_16x16x32_bf16 a[116:119],a[232:235],v[228:231],a[116:119]
ds_read_b64_tr_b16 a[212:213],v2 offset:8448
v_mfma_f32_16x16x32_bf16 a[120:123],a[240:243],v[228:231],a[120:123]
ds_read_b64_tr_b16 a[214:215],v2 offset:12544
v_mfma_f32_16x16x32_bf16 a[120:123],a[244:247],v[244:247],a[120:123]
ds_read_b64_tr_b16 a[220:221],v3 offset:8448
v_mfma_f32_16x16x32_bf16 a[124:127],a[252:255],v[244:247],a[124:127]
ds_read_b64_tr_b16 a[222:223],v3 offset:12544
v_mfma_f32_16x16x32_bf16 a[124:127],a[248:251],v[228:231],a[124:127]
ds_read_b64_tr_b16 a[216:217],v3 offset:256
v_mfma_f32_16x16x32_bf16 v[60:63],v[28:31],v[228:231],v[60:63]
ds_read_b64_tr_b16 a[218:219],v3 offset:4352
v_mfma_f32_16x16x32_bf16 v[60:63],v[28:31],v[244:247],v[60:63]
ds_read_b64_tr_b16 a[224:225],v2 offset:512
v_mfma_f32_16x16x32_bf16 v[196:199],a[128:131],v[144:147],v[40:43]
buffer_load_dwordx4 v5,s[24:27],s47 offen offset:3072 nt lds
v_mfma_f32_16x16x32_bf16 v[196:199],a[132:135],v[176:179],v[196:199]
s_add_u32 s47,s47,16384
s_and_b32 s47,s47,0x3ffff
ds_read_b64_tr_b16 a[226:227],v2 offset:4608
v_mfma_f32_16x16x32_bf16 v[196:199],a[136:139],v[208:211],v[196:199]
s_or_b32 s47,s47,s46
ds_read_b64_tr_b16 a[228:229],v2 offset:8704
v_mfma_f32_16x16x32_bf16 v[196:199],a[140:143],v[240:243],v[196:199]
ds_read_b64_tr_b16 a[230:231],v2 offset:12800
v_mfma_f32_16x16x32_bf16 v[204:207],a[156:159],v[240:243],v[40:43]
ds_read_b64_tr_b16 a[236:237],v3 offset:8704
v_mfma_f32_16x16x32_bf16 v[204:207],a[152:155],v[208:211],v[204:207]
ds_read_b64_tr_b16 a[238:239],v3 offset:12800
v_mfma_f32_16x16x32_bf16 v[204:207],a[148:151],v[176:179],v[204:207]
ds_read_b64_tr_b16 a[232:233],v3 offset:512
v_mfma_f32_16x16x32_bf16 v[204:207],a[144:147],v[144:147],v[204:207]
ds_read_b64_tr_b16 a[234:235],v3 offset:4608
v_mfma_f32_16x16x32_bf16 v[212:215],a[160:163],v[144:147],v[40:43]
ds_read_b64_tr_b16 a[240:241],v2 offset:768
v_mfma_f32_16x16x32_bf16 v[212:215],a[164:167],v[176:179],v[212:215]
ds_read_b64_tr_b16 a[242:243],v2 offset:4864
v_mfma_f32_16x16x32_bf16 v[212:215],a[168:171],v[208:211],v[212:215]
ds_read_b64_tr_b16 a[244:245],v2 offset:8960
v_mfma_f32_16x16x32_bf16 v[212:215],a[172:175],v[240:243],v[212:215]
ds_read_b64_tr_b16 a[246:247],v2 offset:13056
v_mfma_f32_16x16x32_bf16 v[220:223],a[188:191],v[240:243],v[40:43]
ds_read_b64_tr_b16 a[252:253],v3 offset:8960
v_mfma_f32_16x16x32_bf16 v[220:223],a[184:187],v[208:211],v[220:223]
ds_read_b64_tr_b16 a[254:255],v3 offset:13056
v_mfma_f32_16x16x32_bf16 v[220:223],a[180:183],v[176:179],v[220:223]
ds_read_b64_tr_b16 a[248:249],v3 offset:768
v_mfma_f32_16x16x32_bf16 v[220:223],a[176:179],v[144:147],v[220:223]
s_waitcnt lgkmcnt(7)
ds_read_b64_tr_b16 a[250:251],v3 offset:4864
v_mfma_f32_16x16x32_bf16 a[0:3],a[192:195],v[132:135],a[0:3]
buffer_load_dword v26,v24,s[88:91],s93 offen
v_mfma_f32_16x16x32_bf16 a[0:3],a[196:199],v[148:151],a[0:3]
v_exp_f32 v180,v180
v_mfma_f32_16x16x32_bf16 a[4:7],a[204:207],v[148:151],a[4:7]
v_exp_f32 v181,v181
v_mfma_f32_16x16x32_bf16 a[4:7],a[200:203],v[132:135],a[4:7]
v_exp_f32 v182,v182
v_mfma_f32_16x16x32_bf16 a[8:11],a[208:211],v[132:135],a[8:11]
s_memrealtime s[76:77]
v_exp_f32 v183,v183
v_mfma_f32_16x16x32_bf16 a[8:11],a[212:215],v[148:151],a[8:11]
v_exp_f32 v172,v172
v_mfma_f32_16x16x32_bf16 a[12:15],a[220:223],v[148:151],a[12:15]
v_exp_f32 v173,v173
v_mfma_f32_16x16x32_bf16 a[12:15],a[216:219],v[132:135],a[12:15]
v_exp_f32 v174,v174
v_mfma_f32_16x16x32_bf16 a[16:19],a[224:227],v[132:135],a[16:19]
v_exp_f32 v175,v175
v_mfma_f32_16x16x32_bf16 a[16:19],a[228:231],v[148:151],a[16:19]
v_cvt_pk_bf16_f32 v180,v180,v181
v_cvt_pk_bf16_f32 v181,v182,v183
v_mfma_f32_16x16x32_bf16 a[20:23],a[236:239],v[148:151],a[20:23]
v_exp_f32 v188,v188
v_mfma_f32_16x16x32_bf16 a[20:23],a[232:235],v[132:135],a[20:23]
s_waitcnt lgkmcnt(0)
v_exp_f32 v189,v189
v_mfma_f32_16x16x32_bf16 a[24:27],a[240:243],v[132:135],a[24:27]
s_sub_u32 s79,s78,s76
v_exp_f32 v190,v190
v_mfma_f32_16x16x32_bf16 a[24:27],a[244:247],v[148:151],a[24:27]
s_add_u32 s78,s76,s60
v_exp_f32 v191,v191
v_mfma_f32_16x16x32_bf16 a[28:31],a[252:255],v[148:151],a[28:31]
s_cmp_gt_i32 s79,0
s_cbranch_scc1 L_ps7_%=
L_pr7_%=:
v_cvt_pk_bf16_f32 v166,v172,v173
v_mfma_f32_16x16x32_bf16 a[28:31],a[248:251],v[132:135],a[28:31]
v_cvt_pk_bf16_f32 v167,v174,v175
v_cvt_pk_bf16_f32 v182,v188,v189
v_mfma_f32_16x16x32_bf16 v[48:51],v[28:31],v[132:135],v[48:51]
v_cvt_pk_bf16_f32 v183,v190,v191
s_cmp_lg_u32 s53,0
v_mfma_f32_16x16x32_bf16 v[48:51],v[28:31],v[148:151],v[48:51]
s_cbranch_scc1 L_att1_%=
L_endseg_%=:
v_mfma_f32_16x16x32_bf16 v[228:231],a[128:131],v[152:155],v[44:47]
s_waitcnt vmcnt(19)
s_add_u32 s80,s71,0
s_and_b32 s80,s80,3
s_lshl_b32 s80,s80,1
v_mfma_f32_16x16x32_bf16 v[228:231],a[132:135],v[184:187],v[228:231]
s_barrier
s_mul_i32 s80,s80,163840
s_add_u32 s80,s80,s81
v_mfma_f32_16x16x32_bf16 v[228:231],a[136:139],v[216:219],v[228:231]
s_add_u32 s83,s80,20480
ds_read_b128 a[128:131],v1
v_add_u32 v2,s48,v2
v_mfma_f32_16x16x32_bf16 v[228:231],a[140:143],v[248:251],v[228:231]
s_lshl_b32 s43,s41,2
v_exp_f32 v196,v196
v_mfma_f32_16x16x32_bf16 v[236:239],a[156:159],v[248:251],v[44:47]
s_add_u32 s43,s43,s70
ds_read_b128 a[132:135],v1 offset:256
v_add_u32 v3,s48,v3
v_mfma_f32_16x16x32_bf16 v[236:239],a[152:155],v[216:219],v[236:239]
s_lshl_b32 s43,s43,19
v_exp_f32 v197,v197
v_mfma_f32_16x16x32_bf16 v[236:239],a[148:151],v[184:187],v[236:239]
s_add_u32 s43,s43,s55
ds_read_b128 a[136:139],v1 offset:512
v_cvt_pk_bf16_f32 v196,v196,v197
v_mfma_f32_16x16x32_bf16 v[236:239],a[144:147],v[152:155],v[236:239]
s_mov_b64 s[28:29],s[12:13]
v_exp_f32 v198,v198
v_mfma_f32_16x16x32_bf16 v[244:247],a[160:163],v[152:155],v[44:47]
s_add_u32 m0,s82,0
ds_read_b128 a[140:143],v1 offset:768
v_lshrrev_b32 v4,6,v6
v_mfma_f32_16x16x32_bf16 v[244:247],a[164:167],v[184:187],v[244:247]
s_mov_b32 s30,475136
v_exp_f32 v199,v199
v_mfma_f32_16x16x32_bf16 v[244:247],a[168:171],v[216:219],v[244:247]
s_mov_b32 s31,0x20000
ds_read_b128 a[156:159],v1 offset:4864
v_cvt_pk_bf16_f32 v197,v198,v199
v_mfma_f32_16x16x32_bf16 v[244:247],a[172:175],v[248:251],v[244:247]
s_add_u32 s49,s49,32768
buffer_load_dwordx4 v7,s[72:75],s80 offen lds
v_mfma_f32_16x16x32_bf16 v[252:255],a[188:191],v[248:251],v[44:47]
s_cmp_eq_u32 s50,131072
s_cselect_b32 s62,163840,0
ds_read_b128 a[152:155],v1 offset:4608
v_mfma_f32_16x16x32_bf16 v[252:255],a[184:187],v[216:219],v[252:255]
s_sub_u32 s48,32768,s62
v_exp_f32 v204,v204
v_mfma_f32_16x16x32_bf16 v[252:255],a[180:183],v[184:187],v[252:255]
s_add_u32 s50,s50,s48
ds_read_b128 a[148:151],v1 offset:4352
v_mul_u32_u24 v4,320,v4
v_mfma_f32_16x16x32_bf16 v[252:255],a[176:179],v[152:155],v[252:255]
)A"
R"A(v_exp_f32 v205,v205
v_mfma_f32_16x16x32_bf16 a[32:35],a[192:195],v[164:167],a[32:35]
ds_read_b128 a[144:147],v1 offset:4096
v_cvt_pk_bf16_f32 v198,v204,v205
v_mfma_f32_16x16x32_bf16 a[32:35],a[196:199],v[180:183],a[32:35]
v_exp_f32 v206,v206
v_mfma_f32_16x16x32_bf16 a[36:39],a[204:207],v[180:183],a[36:39]
s_cmp_eq_u32 s50,131072
s_cselect_b32 s62,163840,0
ds_read_b128 a[160:163],v1 offset:8192
v_mfma_f32_16x16x32_bf16 a[36:39],a[200:203],v[164:167],a[36:39]
v_exp_f32 v207,v207
v_mfma_f32_16x16x32_bf16 a[40:43],a[208:211],v[164:167],a[40:43]
ds_read_b128 a[164:167],v1 offset:8448
v_cvt_pk_bf16_f32 v199,v206,v207
v_mfma_f32_16x16x32_bf16 a[40:43],a[212:215],v[180:183],a[40:43]
v_exp_f32 v212,v212
v_mfma_f32_16x16x32_bf16 a[44:47],a[220:223],v[180:183],a[44:47]
ds_read_b128 a[168:171],v1 offset:8704
v_bfe_u32 v5,v6,4,2
v_mfma_f32_16x16x32_bf16 a[44:47],a[216:219],v[164:167],a[44:47]
v_exp_f32 v213,v213
v_mfma_f32_16x16x32_bf16 a[48:51],a[224:227],v[164:167],a[48:51]
buffer_load_dwordx4 v8,s[72:75],s80 offen offset:1024 lds
v_mfma_f32_16x16x32_bf16 a[48:51],a[228:231],v[180:183],a[48:51]
ds_read_b128 a[172:175],v1 offset:8960
v_cvt_pk_bf16_f32 v212,v212,v213
v_mfma_f32_16x16x32_bf16 a[52:55],a[236:239],v[180:183],a[52:55]
v_exp_f32 v214,v214
v_mfma_f32_16x16x32_bf16 a[52:55],a[232:235],v[164:167],a[52:55]
ds_read_b128 a[188:191],v1 offset:13056
v_lshl_add_u32 v4,v5,3,v4
v_mfma_f32_16x16x32_bf16 a[56:59],a[240:243],v[164:167],a[56:59]
v_exp_f32 v215,v215
v_mfma_f32_16x16x32_bf16 a[56:59],a[244:247],v[180:183],a[56:59]
ds_read_b128 a[184:187],v1 offset:12800
v_cvt_pk_bf16_f32 v213,v214,v215
v_mfma_f32_16x16x32_bf16 a[60:63],a[252:255],v[180:183],a[60:63]
v_exp_f32 v220,v220
v_mfma_f32_16x16x32_bf16 a[60:63],a[248:251],v[164:167],a[60:63]
ds_read_b128 a[180:183],v1 offset:12544
v_bfe_u32 v5,v6,8,1
v_mfma_f32_16x16x32_bf16 v[52:55],v[28:31],v[164:167],v[52:55]
v_exp_f32 v221,v221
v_mfma_f32_16x16x32_bf16 v[52:55],v[28:31],v[180:183],v[52:55]
s_waitcnt lgkmcnt(3)
ds_read_b128 a[176:179],v1 offset:12288
v_cvt_pk_bf16_f32 v214,v220,v221
v_mfma_f32_16x16x32_bf16 v[132:135],a[128:131],v[128:131],v[32:35]
v_exp_f32 v222,v222
v_mfma_f32_16x16x32_bf16 v[132:135],a[132:135],v[160:163],v[132:135]
v_exp_f32 v223,v223
v_mfma_f32_16x16x32_bf16 v[132:135],a[136:139],v[192:195],v[132:135]
v_cvt_pk_bf16_f32 v215,v222,v223
v_add_u32 v1,s48,v1
v_mfma_f32_16x16x32_bf16 v[132:135],a[140:143],v[224:227],v[132:135]
buffer_load_dwordx4 v9,s[72:75],s80 offen offset:2048 lds
v_mfma_f32_16x16x32_bf16 v[140:143],a[156:159],v[224:227],v[32:35]
v_exp_f32 v228,v228
v_mfma_f32_16x16x32_bf16 v[140:143],a[152:155],v[192:195],v[140:143]
v_exp_f32 v229,v229
v_mfma_f32_16x16x32_bf16 v[140:143],a[148:151],v[160:163],v[140:143]
v_exp_f32 v230,v230
v_mfma_f32_16x16x32_bf16 v[140:143],a[144:147],v[128:131],v[140:143]
v_exp_f32 v231,v231
v_mfma_f32_16x16x32_bf16 v[148:151],a[160:163],v[128:131],v[32:35]
buffer_store_dwordx4 v[64:67],v6,s[36:39],s84 offen sc1
v_mfma_f32_16x16x32_bf16 v[148:151],a[164:167],v[160:163],v[148:151]
v_cvt_pk_bf16_f32 v228,v228,v229
v_cvt_pk_bf16_f32 v229,v230,v231
v_mfma_f32_16x16x32_bf16 v[148:151],a[168:171],v[192:195],v[148:151]
v_exp_f32 v236,v236
v_mfma_f32_16x16x32_bf16 v[148:151],a[172:175],v[224:227],v[148:151]
s_waitcnt lgkmcnt(0)
v_exp_f32 v237,v237
v_mfma_f32_16x16x32_bf16 v[156:159],a[188:191],v[224:227],v[32:35]
v_exp_f32 v238,v238
v_mfma_f32_16x16x32_bf16 v[156:159],a[184:187],v[192:195],v[156:159]
v_exp_f32 v239,v239
v_mfma_f32_16x16x32_bf16 v[156:159],a[180:183],v[160:163],v[156:159]
v_exp_f32 v244,v244
v_mfma_f32_16x16x32_bf16 v[156:159],a[176:179],v[128:131],v[156:159]
v_exp_f32 v245,v245
v_mfma_f32_16x16x32_bf16 a[64:67],a[192:195],v[196:199],a[64:67]
buffer_load_dwordx4 v10,s[72:75],s80 offen offset:3072 lds
v_mfma_f32_16x16x32_bf16 a[64:67],a[196:199],v[212:215],a[64:67]
v_exp_f32 v246,v246
v_mfma_f32_16x16x32_bf16 a[68:71],a[204:207],v[212:215],a[68:71]
v_exp_f32 v247,v247
v_mfma_f32_16x16x32_bf16 a[68:71],a[200:203],v[196:199],a[68:71]
v_cvt_pk_bf16_f32 v230,v236,v237
v_cvt_pk_bf16_f32 v231,v238,v239
v_mfma_f32_16x16x32_bf16 a[72:75],a[208:211],v[196:199],a[72:75]
v_cvt_pk_bf16_f32 v244,v244,v245
v_cvt_pk_bf16_f32 v245,v246,v247
v_mfma_f32_16x16x32_bf16 a[72:75],a[212:215],v[212:215],a[72:75]
v_exp_f32 v252,v252
v_mfma_f32_16x16x32_bf16 a[76:79],a[220:223],v[212:215],a[76:79]
v_exp_f32 v253,v253
v_mfma_f32_16x16x32_bf16 a[76:79],a[216:219],v[196:199],a[76:79]
v_exp_f32 v254,v254
v_mfma_f32_16x16x32_bf16 a[80:83],a[224:227],v[196:199],a[80:83]
v_exp_f32 v255,v255
v_mfma_f32_16x16x32_bf16 a[80:83],a[228:231],v[212:215],a[80:83]
v_cvt_pk_bf16_f32 v246,v252,v253
v_cvt_pk_bf16_f32 v247,v254,v255
v_mfma_f32_16x16x32_bf16 a[84:87],a[236:239],v[212:215],a[84:87]
s_add_u32 m0,s82,4096
v_exp_f32 v132,v132
v_mfma_f32_16x16x32_bf16 a[84:87],a[232:235],v[196:199],a[84:87]
v_exp_f32 v133,v133
v_mfma_f32_16x16x32_bf16 a[88:91],a[240:243],v[196:199],a[88:91]
v_exp_f32 v134,v134
v_mfma_f32_16x16x32_bf16 a[88:91],a[244:247],v[212:215],a[88:91]
buffer_load_dwordx4 v11,s[72:75],s80 offen lds
v_mfma_f32_16x16x32_bf16 a[92:95],a[252:255],v[212:215],a[92:95]
v_exp_f32 v135,v135
v_mfma_f32_16x16x32_bf16 a[92:95],a[248:251],v[196:199],a[92:95]
v_cvt_pk_bf16_f32 v132,v132,v133
v_cvt_pk_bf16_f32 v133,v134,v135
v_mfma_f32_16x16x32_bf16 v[56:59],v[28:31],v[196:199],v[56:59]
v_exp_f32 v140,v140
v_mfma_f32_16x16x32_bf16 v[56:59],v[28:31],v[212:215],v[56:59]
buffer_store_dwordx4 v[68:71],v6,s[36:39],s84 offen offset:1024 sc1
v_mfma_f32_16x16x32_bf16 v[164:167],a[128:131],v[136:139],v[36:39]
v_exp_f32 v141,v141
v_mfma_f32_16x16x32_bf16 v[164:167],a[132:135],v[168:171],v[164:167]
v_exp_f32 v142,v142
v_mfma_f32_16x16x32_bf16 v[164:167],a[136:139],v[200:203],v[164:167]
v_exp_f32 v143,v143
v_mfma_f32_16x16x32_bf16 v[164:167],a[140:143],v[232:235],v[164:167]
v_exp_f32 v148,v148
v_mfma_f32_16x16x32_bf16 v[172:175],a[156:159],v[232:235],v[36:39]
v_exp_f32 v149,v149
v_mfma_f32_16x16x32_bf16 v[172:175],a[152:155],v[200:203],v[172:175]
s_add_u32 m0,s82,5120
v_exp_f32 v150,v150
v_mfma_f32_16x16x32_bf16 v[172:175],a[148:151],v[168:171],v[172:175]
v_exp_f32 v151,v151
v_mfma_f32_16x16x32_bf16 v[172:175],a[144:147],v[136:139],v[172:175]
v_cvt_pk_bf16_f32 v134,v140,v141
v_cvt_pk_bf16_f32 v135,v142,v143
v_mfma_f32_16x16x32_bf16 v[180:183],a[160:163],v[136:139],v[36:39]
buffer_load_dwordx4 v7,s[72:75],s83 offen lds
v_mfma_f32_16x16x32_bf16 v[180:183],a[164:167],v[168:171],v[180:183]
v_cvt_pk_bf16_f32 v148,v148,v149
v_cvt_pk_bf16_f32 v149,v150,v151
v_mfma_f32_16x16x32_bf16 v[180:183],a[168:171],v[200:203],v[180:183]
v_exp_f32 v156,v156
v_mfma_f32_16x16x32_bf16 v[180:183],a[172:175],v[232:235],v[180:183]
v_exp_f32 v157,v157
v_mfma_f32_16x16x32_bf16 v[188:191],a[188:191],v[232:235],v[36:39]
v_exp_f32 v158,v158
v_mfma_f32_16x16x32_bf16 v[188:191],a[184:187],v[200:203],v[188:191]
v_exp_f32 v159,v159
v_mfma_f32_16x16x32_bf16 v[188:191],a[180:183],v[168:171],v[188:191]
v_cvt_pk_bf16_f32 v150,v156,v157
v_cvt_pk_bf16_f32 v151,v158,v159
v_mfma_f32_16x16x32_bf16 v[188:191],a[176:179],v[136:139],v[188:191]
v_exp_f32 v164,v164
v_mfma_f32_16x16x32_bf16 a[96:99],a[192:195],v[228:231],a[96:99]
ds_read_b64_tr_b16 a[192:193],v2
ds_read_b64_tr_b16 a[194:195],v2 offset:4096
v_mfma_f32_16x16x32_bf16 a[96:99],a[196:199],v[244:247],a[96:99]
ds_read_b64_tr_b16 a[196:197],v2 offset:8192
v_lshlrev_b32 v5,5,v5
v_mfma_f32_16x16x32_bf16 a[100:103],a[204:207],v[244:247],a[100:103]
buffer_store_dwordx4 v[72:75],v6,s[36:39],s84 offen offset:2048 sc1
v_mfma_f32_16x16x32_bf16 a[100:103],a[200:203],v[228:231],a[100:103]
ds_read_b64_tr_b16 a[198:199],v2 offset:12288
ds_read_b64_tr_b16 a[204:205],v3 offset:8192
v_mfma_f32_16x16x32_bf16 a[104:107],a[208:211],v[228:231],a[104:107]
s_add_u32 s28,s28,s43
s_addc_u32 s29,s29,0
ds_read_b64_tr_b16 a[206:207],v3 offset:12288
v_mfma_f32_16x16x32_bf16 a[104:107],a[212:215],v[244:247],a[104:107]
s_and_b32 s29,s29,0xffff
ds_read_b64_tr_b16 a[200:201],v3
v_mfma_f32_16x16x32_bf16 a[108:111],a[220:223],v[244:247],a[108:111]
buffer_load_dwordx4 v8,s[72:75],s83 offen offset:1024 lds
v_mfma_f32_16x16x32_bf16 a[108:111],a[216:219],v[228:231],a[108:111]
ds_read_b64_tr_b16 a[202:203],v3 offset:4096
ds_read_b64_tr_b16 a[208:209],v2 offset:256
v_mfma_f32_16x16x32_bf16 a[112:115],a[224:227],v[228:231],a[112:115]
s_cmp_eq_u32 s49,163840
s_cselect_b32 s49,0,s49
ds_read_b64_tr_b16 a[210:211],v2 offset:4352
v_mfma_f32_16x16x32_bf16 a[112:115],a[228:231],v[244:247],a[112:115]
s_add_u32 s49,s49,32768
ds_read_b64_tr_b16 a[212:213],v2 offset:8448
v_mfma_f32_16x16x32_bf16 a[116:119],a[236:239],v[244:247],a[116:119]
s_cmp_eq_u32 s49,163840
s_cselect_b32 s49,0,s49
ds_read_b64_tr_b16 a[214:215],v2 offset:12544
v_mfma_f32_16x16x32_bf16 a[116:119],a[232:235],v[228:231],a[116:119]
s_add_u32 s49,s49,32768
ds_read_b64_tr_b16 a[220:221],v3 offset:8448
ds_read_b64_tr_b16 a[222:223],v3 offset:12544
v_mfma_f32_16x16x32_bf16 a[120:123],a[240:243],v[228:231],a[120:123]
s_cmp_eq_u32 s49,163840
s_cselect_b32 s49,0,s49
ds_read_b64_tr_b16 a[216:217],v3 offset:256
v_mfma_f32_16x16x32_bf16 a[120:123],a[244:247],v[244:247],a[120:123]
ds_read_b64_tr_b16 a[218:219],v3 offset:4352
v_mfma_f32_16x16x32_bf16 a[124:127],a[252:255],v[244:247],a[124:127]
ds_read_b64_tr_b16 a[224:225],v2 offset:512
v_mfma_f32_16x16x32_bf16 a[124:127],a[248:251],v[228:231],a[124:127]
ds_read_b64_tr_b16 a[226:227],v2 offset:4608
ds_read_b64_tr_b16 a[228:229],v2 offset:8704
v_mfma_f32_16x16x32_bf16 v[60:63],v[28:31],v[228:231],v[60:63]
ds_read_b64_tr_b16 a[230:231],v2 offset:12800
v_mfma_f32_16x16x32_bf16 v[60:63],v[28:31],v[244:247],v[60:63]
ds_read_b64_tr_b16 a[236:237],v3 offset:8704
v_mfma_f32_16x16x32_bf16 v[196:199],a[128:131],v[144:147],v[40:43]
buffer_load_dwordx4 v9,s[72:75],s83 offen offset:2048 lds
v_mfma_f32_16x16x32_bf16 v[196:199],a[132:135],v[176:179],v[196:199]
ds_read_b64_tr_b16 a[238:239],v3 offset:12800
ds_read_b64_tr_b16 a[232:233],v3 offset:512
v_mfma_f32_16x16x32_bf16 v[196:199],a[136:139],v[208:211],v[196:199]
ds_read_b64_tr_b16 a[234:235],v3 offset:4608
v_mfma_f32_16x16x32_bf16 v[196:199],a[140:143],v[240:243],v[196:199]
ds_read_b64_tr_b16 a[240:241],v2 offset:768
v_mfma_f32_16x16x32_bf16 v[204:207],a[156:159],v[240:243],v[40:43]
ds_read_b64_tr_b16 a[242:243],v2 offset:4864
v_mfma_f32_16x16x32_bf16 v[204:207],a[152:155],v[208:211],v[204:207]
ds_read_b64_tr_b16 a[244:245],v2 offset:8960
ds_read_b64_tr_b16 a[246:247],v2 offset:13056
v_mfma_f32_16x16x32_bf16 v[204:207],a[148:151],v[176:179],v[204:207]
buffer_store_dwordx4 v[76:79],v6,s[36:39],s84 offen offset:3072 sc1
v_mfma_f32_16x16x32_bf16 v[204:207],a[144:147],v[144:147],v[204:207]
s_add_u32 s84,s55,65536
ds_read_b64_tr_b16 a[252:253],v3 offset:8960
ds_read_b64_tr_b16 a[254:255],v3 offset:13056
v_mfma_f32_16x16x32_bf16 v[212:215],a[160:163],v[144:147],v[40:43]
s_max_i32 s84,s84,s18
ds_read_b64_tr_b16 a[248:249],v3 offset:768
v_add_u32 v2,s48,v2
v_mfma_f32_16x16x32_bf16 v[212:215],a[164:167],v[176:179],v[212:215]
ds_read_b64_tr_b16 a[250:251],v3 offset:4864
v_add_u32 v3,s48,v3
v_mfma_f32_16x16x32_bf16 v[212:215],a[168:171],v[208:211],v[212:215]
s_sub_u32 s48,32768,s62
v_exp_f32 v165,v165
v_mfma_f32_16x16x32_bf16 v[212:215],a[172:175],v[240:243],v[212:215]
s_add_u32 s50,s50,s48
v_exp_f32 v166,v166
v_mfma_f32_16x16x32_bf16 v[220:223],a[188:191],v[240:243],v[40:43]
v_exp_f32 v167,v167
v_mfma_f32_16x16x32_bf16 v[220:223],a[184:187],v[208:211],v[220:223]
buffer_load_dwordx4 v10,s[72:75],s83 offen offset:3072 lds
v_mfma_f32_16x16x32_bf16 v[220:223],a[180:183],v[176:179],v[220:223]
v_cvt_pk_bf16_f32 v164,v164,v165
v_cvt_pk_bf16_f32 v165,v166,v167
v_mfma_f32_16x16x32_bf16 v[220:223],a[176:179],v[144:147],v[220:223]
s_waitcnt lgkmcnt(2)
v_exp_f32 v172,v172
v_mfma_f32_16x16x32_bf16 a[0:3],a[192:195],v[132:135],a[0:3]
v_exp_f32 v173,v173
v_mfma_f32_16x16x32_bf16 a[0:3],a[196:199],v[148:151],a[0:3]
v_exp_f32 v174,v174
v_mfma_f32_16x16x32_bf16 a[4:7],a[204:207],v[148:151],a[4:7]
v_exp_f32 v175,v175
v_mfma_f32_16x16x32_bf16 a[4:7],a[200:203],v[132:135],a[4:7]
)A"
R"A(v_exp_f32 v180,v180
v_mfma_f32_16x16x32_bf16 a[8:11],a[208:211],v[132:135],a[8:11]
v_exp_f32 v181,v181
v_mfma_f32_16x16x32_bf16 a[8:11],a[212:215],v[148:151],a[8:11]
v_exp_f32 v182,v182
v_mfma_f32_16x16x32_bf16 a[12:15],a[220:223],v[148:151],a[12:15]
s_add_u32 s80,s71,0
s_and_b32 s80,s80,3
s_lshl_b32 s80,s80,1
v_mfma_f32_16x16x32_bf16 a[12:15],a[216:219],v[132:135],a[12:15]
s_add_u32 m0,s82,9216
s_or_b32 s80,s80,1
s_mul_i32 s80,s80,163840
v_mfma_f32_16x16x32_bf16 a[16:19],a[224:227],v[132:135],a[16:19]
s_add_u32 s80,s80,s81
v_exp_f32 v183,v183
v_mfma_f32_16x16x32_bf16 a[16:19],a[228:231],v[148:151],a[16:19]
v_cvt_pk_bf16_f32 v166,v172,v173
v_cvt_pk_bf16_f32 v167,v174,v175
v_mfma_f32_16x16x32_bf16 a[20:23],a[236:239],v[148:151],a[20:23]
buffer_load_dwordx4 v11,s[72:75],s83 offen lds
v_mfma_f32_16x16x32_bf16 a[20:23],a[232:235],v[132:135],a[20:23]
s_add_u32 s83,s80,20480
v_cvt_pk_bf16_f32 v180,v180,v181
v_cvt_pk_bf16_f32 v181,v182,v183
v_mfma_f32_16x16x32_bf16 a[24:27],a[240:243],v[132:135],a[24:27]
v_exp_f32 v188,v188
v_mfma_f32_16x16x32_bf16 a[24:27],a[244:247],v[148:151],a[24:27]
v_exp_f32 v189,v189
v_mfma_f32_16x16x32_bf16 a[28:31],a[252:255],v[148:151],a[28:31]
s_waitcnt lgkmcnt(0)
buffer_store_dwordx4 v[80:83],v6,s[36:39],s85 offen sc1
v_mfma_f32_16x16x32_bf16 a[28:31],a[248:251],v[132:135],a[28:31]
v_exp_f32 v190,v190
v_mfma_f32_16x16x32_bf16 v[48:51],v[28:31],v[132:135],v[48:51]
v_exp_f32 v191,v191
v_mfma_f32_16x16x32_bf16 v[48:51],v[28:31],v[148:151],v[48:51]
v_cvt_pk_bf16_f32 v182,v188,v189
v_cvt_pk_bf16_f32 v183,v190,v191
v_mfma_f32_16x16x32_bf16 v[228:231],a[128:131],v[152:155],v[44:47]
s_waitcnt vmcnt(25)
v_exp_f32 v196,v196
v_mfma_f32_16x16x32_bf16 v[228:231],a[132:135],v[184:187],v[228:231]
s_barrier
ds_read_b128 a[128:131],v1
v_mfma_f32_16x16x32_bf16 v[228:231],a[136:139],v[216:219],v[228:231]
v_exp_f32 v197,v197
v_mfma_f32_16x16x32_bf16 v[228:231],a[140:143],v[248:251],v[228:231]
ds_read_b128 a[132:135],v1 offset:256
v_cvt_pk_bf16_f32 v196,v196,v197
v_mfma_f32_16x16x32_bf16 v[236:239],a[156:159],v[248:251],v[44:47]
v_exp_f32 v198,v198
v_mfma_f32_16x16x32_bf16 v[236:239],a[152:155],v[216:219],v[236:239]
s_cmp_eq_u32 s50,131072
s_cselect_b32 s62,163840,0
ds_read_b128 a[136:139],v1 offset:512
v_mfma_f32_16x16x32_bf16 v[236:239],a[148:151],v[184:187],v[236:239]
v_exp_f32 v199,v199
v_mfma_f32_16x16x32_bf16 v[236:239],a[144:147],v[152:155],v[236:239]
ds_read_b128 a[140:143],v1 offset:768
v_cvt_pk_bf16_f32 v197,v198,v199
v_mfma_f32_16x16x32_bf16 v[244:247],a[160:163],v[152:155],v[44:47]
s_add_u32 m0,s82,40960
v_exp_f32 v204,v204
v_mfma_f32_16x16x32_bf16 v[244:247],a[164:167],v[184:187],v[244:247]
ds_read_b128 a[156:159],v1 offset:4864
v_mfma_f32_16x16x32_bf16 v[244:247],a[168:171],v[216:219],v[244:247]
v_exp_f32 v205,v205
v_mfma_f32_16x16x32_bf16 v[244:247],a[172:175],v[248:251],v[244:247]
buffer_load_dwordx4 v7,s[72:75],s80 offen lds
v_mfma_f32_16x16x32_bf16 v[252:255],a[188:191],v[248:251],v[44:47]
ds_read_b128 a[152:155],v1 offset:4608
v_cvt_pk_bf16_f32 v198,v204,v205
v_mfma_f32_16x16x32_bf16 v[252:255],a[184:187],v[216:219],v[252:255]
v_exp_f32 v206,v206
v_mfma_f32_16x16x32_bf16 v[252:255],a[180:183],v[184:187],v[252:255]
ds_read_b128 a[148:151],v1 offset:4352
v_mfma_f32_16x16x32_bf16 v[252:255],a[176:179],v[152:155],v[252:255]
buffer_store_dwordx4 v[84:87],v6,s[36:39],s85 offen offset:1024 sc1
v_mfma_f32_16x16x32_bf16 a[32:35],a[192:195],v[164:167],a[32:35]
ds_read_b128 a[144:147],v1 offset:4096
v_mfma_f32_16x16x32_bf16 a[32:35],a[196:199],v[180:183],a[32:35]
v_exp_f32 v207,v207
v_mfma_f32_16x16x32_bf16 a[36:39],a[204:207],v[180:183],a[36:39]
ds_read_b128 a[160:163],v1 offset:8192
v_cvt_pk_bf16_f32 v199,v206,v207
v_mfma_f32_16x16x32_bf16 a[36:39],a[200:203],v[164:167],a[36:39]
v_exp_f32 v212,v212
v_mfma_f32_16x16x32_bf16 a[40:43],a[208:211],v[164:167],a[40:43]
ds_read_b128 a[164:167],v1 offset:8448
v_mfma_f32_16x16x32_bf16 a[40:43],a[212:215],v[180:183],a[40:43]
v_exp_f32 v213,v213
v_mfma_f32_16x16x32_bf16 a[44:47],a[220:223],v[180:183],a[44:47]
ds_read_b128 a[168:171],v1 offset:8704
v_cvt_pk_bf16_f32 v212,v212,v213
v_mfma_f32_16x16x32_bf16 a[44:47],a[216:219],v[164:167],a[44:47]
v_exp_f32 v214,v214
v_mfma_f32_16x16x32_bf16 a[48:51],a[224:227],v[164:167],a[48:51]
buffer_load_dwordx4 v8,s[72:75],s80 offen offset:1024 lds
v_mfma_f32_16x16x32_bf16 a[48:51],a[228:231],v[180:183],a[48:51]
ds_read_b128 a[172:175],v1 offset:8960
v_mfma_f32_16x16x32_bf16 a[52:55],a[236:239],v[180:183],a[52:55]
v_exp_f32 v215,v215
v_mfma_f32_16x16x32_bf16 a[52:55],a[232:235],v[164:167],a[52:55]
ds_read_b128 a[188:191],v1 offset:13056
v_cvt_pk_bf16_f32 v213,v214,v215
v_mfma_f32_16x16x32_bf16 a[56:59],a[240:243],v[164:167],a[56:59]
v_exp_f32 v220,v220
v_mfma_f32_16x16x32_bf16 a[56:59],a[244:247],v[180:183],a[56:59]
ds_read_b128 a[184:187],v1 offset:12800
v_mfma_f32_16x16x32_bf16 a[60:63],a[252:255],v[180:183],a[60:63]
v_exp_f32 v221,v221
v_mfma_f32_16x16x32_bf16 a[60:63],a[248:251],v[164:167],a[60:63]
ds_read_b128 a[180:183],v1 offset:12544
v_cvt_pk_bf16_f32 v214,v220,v221
v_mfma_f32_16x16x32_bf16 v[52:55],v[28:31],v[164:167],v[52:55]
v_exp_f32 v222,v222
v_mfma_f32_16x16x32_bf16 v[52:55],v[28:31],v[180:183],v[52:55]
s_waitcnt lgkmcnt(3)
ds_read_b128 a[176:179],v1 offset:12288
v_add_u32 v1,s48,v1
v_mfma_f32_16x16x32_bf16 v[132:135],a[128:131],v[128:131],v[32:35]
v_exp_f32 v223,v223
v_mfma_f32_16x16x32_bf16 v[132:135],a[132:135],v[160:163],v[132:135]
v_cvt_pk_bf16_f32 v215,v222,v223
v_mfma_f32_16x16x32_bf16 v[132:135],a[136:139],v[192:195],v[132:135]
v_exp_f32 v228,v228
v_mfma_f32_16x16x32_bf16 v[132:135],a[140:143],v[224:227],v[132:135]
buffer_store_dwordx4 v[88:91],v6,s[36:39],s85 offen offset:2048 sc1
v_mfma_f32_16x16x32_bf16 v[140:143],a[156:159],v[224:227],v[32:35]
v_exp_f32 v229,v229
v_mfma_f32_16x16x32_bf16 v[140:143],a[152:155],v[192:195],v[140:143]
v_exp_f32 v230,v230
v_mfma_f32_16x16x32_bf16 v[140:143],a[148:151],v[160:163],v[140:143]
v_exp_f32 v231,v231
v_mfma_f32_16x16x32_bf16 v[140:143],a[144:147],v[128:131],v[140:143]
buffer_load_dwordx4 v9,s[72:75],s80 offen offset:2048 lds
v_mfma_f32_16x16x32_bf16 v[148:151],a[160:163],v[128:131],v[32:35]
v_cvt_pk_bf16_f32 v228,v228,v229
v_cvt_pk_bf16_f32 v229,v230,v231
v_mfma_f32_16x16x32_bf16 v[148:151],a[164:167],v[160:163],v[148:151]
v_exp_f32 v236,v236
v_mfma_f32_16x16x32_bf16 v[148:151],a[168:171],v[192:195],v[148:151]
v_exp_f32 v237,v237
v_mfma_f32_16x16x32_bf16 v[148:151],a[172:175],v[224:227],v[148:151]
s_waitcnt lgkmcnt(0)
v_exp_f32 v238,v238
v_mfma_f32_16x16x32_bf16 v[156:159],a[188:191],v[224:227],v[32:35]
v_exp_f32 v239,v239
v_mfma_f32_16x16x32_bf16 v[156:159],a[184:187],v[192:195],v[156:159]
v_exp_f32 v244,v244
v_mfma_f32_16x16x32_bf16 v[156:159],a[180:183],v[160:163],v[156:159]
v_exp_f32 v245,v245
v_mfma_f32_16x16x32_bf16 v[156:159],a[176:179],v[128:131],v[156:159]
v_exp_f32 v246,v246
v_mfma_f32_16x16x32_bf16 a[64:67],a[192:195],v[196:199],a[64:67]
buffer_load_dwordx4 v10,s[72:75],s80 offen offset:3072 lds
v_mfma_f32_16x16x32_bf16 a[64:67],a[196:199],v[212:215],a[64:67]
v_exp_f32 v247,v247
v_mfma_f32_16x16x32_bf16 a[68:71],a[204:207],v[212:215],a[68:71]
v_cvt_pk_bf16_f32 v230,v236,v237
v_cvt_pk_bf16_f32 v231,v238,v239
v_mfma_f32_16x16x32_bf16 a[68:71],a[200:203],v[196:199],a[68:71]
v_cvt_pk_bf16_f32 v244,v244,v245
v_cvt_pk_bf16_f32 v245,v246,v247
v_mfma_f32_16x16x32_bf16 a[72:75],a[208:211],v[196:199],a[72:75]
v_exp_f32 v252,v252
v_mfma_f32_16x16x32_bf16 a[72:75],a[212:215],v[212:215],a[72:75]
v_exp_f32 v253,v253
v_mfma_f32_16x16x32_bf16 a[76:79],a[220:223],v[212:215],a[76:79]
v_exp_f32 v254,v254
v_mfma_f32_16x16x32_bf16 a[76:79],a[216:219],v[196:199],a[76:79]
v_exp_f32 v255,v255
v_mfma_f32_16x16x32_bf16 a[80:83],a[224:227],v[196:199],a[80:83]
v_cvt_pk_bf16_f32 v246,v252,v253
v_cvt_pk_bf16_f32 v247,v254,v255
v_mfma_f32_16x16x32_bf16 a[80:83],a[228:231],v[212:215],a[80:83]
buffer_store_dwordx4 v[92:95],v6,s[36:39],s85 offen offset:3072 sc1
v_mfma_f32_16x16x32_bf16 a[84:87],a[236:239],v[212:215],a[84:87]
s_add_u32 m0,s82,45056
v_exp_f32 v132,v132
v_mfma_f32_16x16x32_bf16 a[84:87],a[232:235],v[196:199],a[84:87]
s_add_u32 s85,s55,69632
v_exp_f32 v133,v133
v_mfma_f32_16x16x32_bf16 a[88:91],a[240:243],v[196:199],a[88:91]
s_max_i32 s85,s85,s19
v_exp_f32 v134,v134
v_mfma_f32_16x16x32_bf16 a[88:91],a[244:247],v[212:215],a[88:91]
buffer_load_dwordx4 v11,s[72:75],s80 offen lds
v_mfma_f32_16x16x32_bf16 a[92:95],a[252:255],v[212:215],a[92:95]
v_exp_f32 v135,v135
v_mfma_f32_16x16x32_bf16 a[92:95],a[248:251],v[196:199],a[92:95]
v_cvt_pk_bf16_f32 v132,v132,v133
v_cvt_pk_bf16_f32 v133,v134,v135
v_mfma_f32_16x16x32_bf16 v[56:59],v[28:31],v[196:199],v[56:59]
v_exp_f32 v140,v140
v_mfma_f32_16x16x32_bf16 v[56:59],v[28:31],v[212:215],v[56:59]
v_exp_f32 v141,v141
v_mfma_f32_16x16x32_bf16 v[164:167],a[128:131],v[136:139],v[36:39]
v_exp_f32 v142,v142
v_mfma_f32_16x16x32_bf16 v[164:167],a[132:135],v[168:171],v[164:167]
v_exp_f32 v143,v143
v_mfma_f32_16x16x32_bf16 v[164:167],a[136:139],v[200:203],v[164:167]
v_exp_f32 v148,v148
v_mfma_f32_16x16x32_bf16 v[164:167],a[140:143],v[232:235],v[164:167]
v_exp_f32 v149,v149
v_mfma_f32_16x16x32_bf16 v[172:175],a[156:159],v[232:235],v[36:39]
v_exp_f32 v150,v150
v_mfma_f32_16x16x32_bf16 v[172:175],a[152:155],v[200:203],v[172:175]
s_add_u32 m0,s82,46080
v_exp_f32 v151,v151
v_mfma_f32_16x16x32_bf16 v[172:175],a[148:151],v[168:171],v[172:175]
v_cvt_pk_bf16_f32 v134,v140,v141
v_cvt_pk_bf16_f32 v135,v142,v143
v_mfma_f32_16x16x32_bf16 v[172:175],a[144:147],v[136:139],v[172:175]
v_cvt_pk_bf16_f32 v148,v148,v149
v_cvt_pk_bf16_f32 v149,v150,v151
v_mfma_f32_16x16x32_bf16 v[180:183],a[160:163],v[136:139],v[36:39]
buffer_load_dwordx4 v7,s[72:75],s83 offen lds
v_mfma_f32_16x16x32_bf16 v[180:183],a[164:167],v[168:171],v[180:183]
v_exp_f32 v156,v156
v_mfma_f32_16x16x32_bf16 v[180:183],a[168:171],v[200:203],v[180:183]
v_exp_f32 v157,v157
v_mfma_f32_16x16x32_bf16 v[180:183],a[172:175],v[232:235],v[180:183]
v_exp_f32 v158,v158
v_mfma_f32_16x16x32_bf16 v[188:191],a[188:191],v[232:235],v[36:39]
v_exp_f32 v159,v159
v_mfma_f32_16x16x32_bf16 v[188:191],a[184:187],v[200:203],v[188:191]
buffer_store_dwordx4 v[96:99],v6,s[36:39],s86 offen sc1
v_mfma_f32_16x16x32_bf16 v[188:191],a[180:183],v[168:171],v[188:191]
v_cvt_pk_bf16_f32 v150,v156,v157
v_cvt_pk_bf16_f32 v151,v158,v159
v_mfma_f32_16x16x32_bf16 v[188:191],a[176:179],v[136:139],v[188:191]
v_exp_f32 v164,v164
v_mfma_f32_16x16x32_bf16 a[96:99],a[192:195],v[228:231],a[96:99]
ds_read_b64_tr_b16 a[192:193],v2
ds_read_b64_tr_b16 a[194:195],v2 offset:4096
v_mfma_f32_16x16x32_bf16 a[96:99],a[196:199],v[244:247],a[96:99]
ds_read_b64_tr_b16 a[196:197],v2 offset:8192
v_mfma_f32_16x16x32_bf16 a[100:103],a[204:207],v[244:247],a[100:103]
ds_read_b64_tr_b16 a[198:199],v2 offset:12288
v_mfma_f32_16x16x32_bf16 a[100:103],a[200:203],v[228:231],a[100:103]
ds_read_b64_tr_b16 a[204:205],v3 offset:8192
v_mfma_f32_16x16x32_bf16 a[104:107],a[208:211],v[228:231],a[104:107]
ds_read_b64_tr_b16 a[206:207],v3 offset:12288
ds_read_b64_tr_b16 a[200:201],v3
v_mfma_f32_16x16x32_bf16 a[104:107],a[212:215],v[244:247],a[104:107]
buffer_load_dwordx4 v8,s[72:75],s83 offen offset:1024 lds
v_mfma_f32_16x16x32_bf16 a[108:111],a[220:223],v[244:247],a[108:111]
ds_read_b64_tr_b16 a[202:203],v3 offset:4096
ds_read_b64_tr_b16 a[208:209],v2 offset:256
v_mfma_f32_16x16x32_bf16 a[108:111],a[216:219],v[228:231],a[108:111]
ds_read_b64_tr_b16 a[210:211],v2 offset:4352
v_mfma_f32_16x16x32_bf16 a[112:115],a[224:227],v[228:231],a[112:115]
ds_read_b64_tr_b16 a[212:213],v2 offset:8448
v_mfma_f32_16x16x32_bf16 a[112:115],a[228:231],v[244:247],a[112:115]
ds_read_b64_tr_b16 a[214:215],v2 offset:12544
v_mfma_f32_16x16x32_bf16 a[116:119],a[236:239],v[244:247],a[116:119]
ds_read_b64_tr_b16 a[220:221],v3 offset:8448
ds_read_b64_tr_b16 a[222:223],v3 offset:12544
v_mfma_f32_16x16x32_bf16 a[116:119],a[232:235],v[228:231],a[116:119]
ds_read_b64_tr_b16 a[216:217],v3 offset:256
v_mfma_f32_16x16x32_bf16 a[120:123],a[240:243],v[228:231],a[120:123]
ds_read_b64_tr_b16 a[218:219],v3 offset:4352
)A"
R"A(v_mfma_f32_16x16x32_bf16 a[120:123],a[244:247],v[244:247],a[120:123]
ds_read_b64_tr_b16 a[224:225],v2 offset:512
v_mfma_f32_16x16x32_bf16 a[124:127],a[252:255],v[244:247],a[124:127]
ds_read_b64_tr_b16 a[226:227],v2 offset:4608
ds_read_b64_tr_b16 a[228:229],v2 offset:8704
v_mfma_f32_16x16x32_bf16 a[124:127],a[248:251],v[228:231],a[124:127]
ds_read_b64_tr_b16 a[230:231],v2 offset:12800
v_mfma_f32_16x16x32_bf16 v[60:63],v[28:31],v[228:231],v[60:63]
ds_read_b64_tr_b16 a[236:237],v3 offset:8704
v_mfma_f32_16x16x32_bf16 v[60:63],v[28:31],v[244:247],v[60:63]
ds_read_b64_tr_b16 a[238:239],v3 offset:12800
v_mfma_f32_16x16x32_bf16 v[196:199],a[128:131],v[144:147],v[40:43]
buffer_load_dwordx4 v9,s[72:75],s83 offen offset:2048 lds
v_mfma_f32_16x16x32_bf16 v[196:199],a[132:135],v[176:179],v[196:199]
ds_read_b64_tr_b16 a[232:233],v3 offset:512
ds_read_b64_tr_b16 a[234:235],v3 offset:4608
v_mfma_f32_16x16x32_bf16 v[196:199],a[136:139],v[208:211],v[196:199]
ds_read_b64_tr_b16 a[240:241],v2 offset:768
v_mfma_f32_16x16x32_bf16 v[196:199],a[140:143],v[240:243],v[196:199]
ds_read_b64_tr_b16 a[242:243],v2 offset:4864
v_mfma_f32_16x16x32_bf16 v[204:207],a[156:159],v[240:243],v[40:43]
buffer_store_dwordx4 v[100:103],v6,s[36:39],s86 offen offset:1024 sc1
v_mfma_f32_16x16x32_bf16 v[204:207],a[152:155],v[208:211],v[204:207]
ds_read_b64_tr_b16 a[244:245],v2 offset:8960
ds_read_b64_tr_b16 a[246:247],v2 offset:13056
v_mfma_f32_16x16x32_bf16 v[204:207],a[148:151],v[176:179],v[204:207]
ds_read_b64_tr_b16 a[252:253],v3 offset:8960
v_add_u32 v2,s48,v2
v_mfma_f32_16x16x32_bf16 v[204:207],a[144:147],v[144:147],v[204:207]
ds_read_b64_tr_b16 a[254:255],v3 offset:13056
v_mfma_f32_16x16x32_bf16 v[212:215],a[160:163],v[144:147],v[40:43]
ds_read_b64_tr_b16 a[248:249],v3 offset:768
v_mfma_f32_16x16x32_bf16 v[212:215],a[164:167],v[176:179],v[212:215]
ds_read_b64_tr_b16 a[250:251],v3 offset:4864
v_add_u32 v3,s48,v3
v_mfma_f32_16x16x32_bf16 v[212:215],a[168:171],v[208:211],v[212:215]
s_sub_u32 s48,32768,s62
v_exp_f32 v165,v165
v_mfma_f32_16x16x32_bf16 v[212:215],a[172:175],v[240:243],v[212:215]
s_add_u32 s50,s50,s48
v_exp_f32 v166,v166
v_mfma_f32_16x16x32_bf16 v[220:223],a[188:191],v[240:243],v[40:43]
v_exp_f32 v167,v167
v_mfma_f32_16x16x32_bf16 v[220:223],a[184:187],v[208:211],v[220:223]
buffer_load_dwordx4 v10,s[72:75],s83 offen offset:3072 lds
v_mfma_f32_16x16x32_bf16 v[220:223],a[180:183],v[176:179],v[220:223]
v_cvt_pk_bf16_f32 v164,v164,v165
v_cvt_pk_bf16_f32 v165,v166,v167
v_mfma_f32_16x16x32_bf16 v[220:223],a[176:179],v[144:147],v[220:223]
s_waitcnt lgkmcnt(2)
v_exp_f32 v172,v172
v_mfma_f32_16x16x32_bf16 a[0:3],a[192:195],v[132:135],a[0:3]
v_exp_f32 v173,v173
v_mfma_f32_16x16x32_bf16 a[0:3],a[196:199],v[148:151],a[0:3]
v_exp_f32 v174,v174
v_mfma_f32_16x16x32_bf16 a[4:7],a[204:207],v[148:151],a[4:7]
v_exp_f32 v175,v175
v_mfma_f32_16x16x32_bf16 a[4:7],a[200:203],v[132:135],a[4:7]
v_exp_f32 v180,v180
v_mfma_f32_16x16x32_bf16 a[8:11],a[208:211],v[132:135],a[8:11]
v_exp_f32 v181,v181
v_mfma_f32_16x16x32_bf16 a[8:11],a[212:215],v[148:151],a[8:11]
v_exp_f32 v182,v182
v_mfma_f32_16x16x32_bf16 a[12:15],a[220:223],v[148:151],a[12:15]
s_cmp_ge_u32 0,s71
s_cselect_b32 s80,2,0
s_mul_i32 s80,s80,163840
v_mfma_f32_16x16x32_bf16 a[12:15],a[216:219],v[132:135],a[12:15]
s_add_u32 m0,s82,50176
buffer_store_dwordx4 v[104:107],v6,s[36:39],s86 offen offset:2048 sc1
v_mfma_f32_16x16x32_bf16 a[16:19],a[224:227],v[132:135],a[16:19]
s_add_u32 s80,s80,s81
v_exp_f32 v183,v183
v_mfma_f32_16x16x32_bf16 a[16:19],a[228:231],v[148:151],a[16:19]
v_cvt_pk_bf16_f32 v166,v172,v173
v_cvt_pk_bf16_f32 v167,v174,v175
v_mfma_f32_16x16x32_bf16 a[20:23],a[236:239],v[148:151],a[20:23]
v_cvt_pk_bf16_f32 v180,v180,v181
v_cvt_pk_bf16_f32 v181,v182,v183
v_mfma_f32_16x16x32_bf16 a[20:23],a[232:235],v[132:135],a[20:23]
buffer_load_dwordx4 v11,s[72:75],s83 offen lds
v_mfma_f32_16x16x32_bf16 a[24:27],a[240:243],v[132:135],a[24:27]
s_add_u32 s83,s80,20480
v_exp_f32 v188,v188
v_mfma_f32_16x16x32_bf16 a[24:27],a[244:247],v[148:151],a[24:27]
v_exp_f32 v189,v189
v_mfma_f32_16x16x32_bf16 a[28:31],a[252:255],v[148:151],a[28:31]
s_waitcnt lgkmcnt(0)
v_exp_f32 v190,v190
v_mfma_f32_16x16x32_bf16 a[28:31],a[248:251],v[132:135],a[28:31]
v_exp_f32 v191,v191
v_mfma_f32_16x16x32_bf16 v[48:51],v[28:31],v[132:135],v[48:51]
v_cvt_pk_bf16_f32 v182,v188,v189
v_cvt_pk_bf16_f32 v183,v190,v191
v_mfma_f32_16x16x32_bf16 v[48:51],v[28:31],v[148:151],v[48:51]
v_exp_f32 v196,v196
v_mfma_f32_16x16x32_bf16 v[228:231],a[128:131],v[152:155],v[44:47]
s_waitcnt vmcnt(32)
v_exp_f32 v197,v197
v_mfma_f32_16x16x32_bf16 v[228:231],a[132:135],v[184:187],v[228:231]
s_barrier
ds_read_b128 a[128:131],v1
v_cvt_pk_bf16_f32 v196,v196,v197
v_mfma_f32_16x16x32_bf16 v[228:231],a[136:139],v[216:219],v[228:231]
v_exp_f32 v198,v198
v_mfma_f32_16x16x32_bf16 v[228:231],a[140:143],v[248:251],v[228:231]
ds_read_b128 a[132:135],v1 offset:256
v_mfma_f32_16x16x32_bf16 v[236:239],a[156:159],v[248:251],v[44:47]
v_exp_f32 v199,v199
v_mfma_f32_16x16x32_bf16 v[236:239],a[152:155],v[216:219],v[236:239]
ds_read_b128 a[136:139],v1 offset:512
v_cvt_pk_bf16_f32 v197,v198,v199
v_mfma_f32_16x16x32_bf16 v[236:239],a[148:151],v[184:187],v[236:239]
v_exp_f32 v204,v204
v_mfma_f32_16x16x32_bf16 v[236:239],a[144:147],v[152:155],v[236:239]
ds_read_b128 a[140:143],v1 offset:768
v_mfma_f32_16x16x32_bf16 v[244:247],a[160:163],v[152:155],v[44:47]
s_add_u32 m0,s82,81920
v_exp_f32 v205,v205
v_mfma_f32_16x16x32_bf16 v[244:247],a[164:167],v[184:187],v[244:247]
ds_read_b128 a[156:159],v1 offset:4864
v_cvt_pk_bf16_f32 v198,v204,v205
v_mfma_f32_16x16x32_bf16 v[244:247],a[168:171],v[216:219],v[244:247]
buffer_store_dwordx4 v[108:111],v6,s[36:39],s86 offen offset:3072 sc1
v_mfma_f32_16x16x32_bf16 v[244:247],a[172:175],v[248:251],v[244:247]
s_add_u32 s86,s55,73728
s_max_i32 s86,s86,s100
ds_read_b128 a[152:155],v1 offset:4608
v_mfma_f32_16x16x32_bf16 v[252:255],a[188:191],v[248:251],v[44:47]
v_exp_f32 v206,v206
v_mfma_f32_16x16x32_bf16 v[252:255],a[184:187],v[216:219],v[252:255]
ds_read_b128 a[148:151],v1 offset:4352
v_mfma_f32_16x16x32_bf16 v[252:255],a[180:183],v[184:187],v[252:255]
buffer_load_dwordx4 v7,s[72:75],s80 offen lds
v_mfma_f32_16x16x32_bf16 v[252:255],a[176:179],v[152:155],v[252:255]
ds_read_b128 a[144:147],v1 offset:4096
v_mfma_f32_16x16x32_bf16 a[32:35],a[192:195],v[164:167],a[32:35]
v_exp_f32 v207,v207
v_mfma_f32_16x16x32_bf16 a[32:35],a[196:199],v[180:183],a[32:35]
ds_read_b128 a[160:163],v1 offset:8192
v_cvt_pk_bf16_f32 v199,v206,v207
v_mfma_f32_16x16x32_bf16 a[36:39],a[204:207],v[180:183],a[36:39]
v_exp_f32 v212,v212
v_mfma_f32_16x16x32_bf16 a[36:39],a[200:203],v[164:167],a[36:39]
ds_read_b128 a[164:167],v1 offset:8448
v_mfma_f32_16x16x32_bf16 a[40:43],a[208:211],v[164:167],a[40:43]
v_exp_f32 v213,v213
v_mfma_f32_16x16x32_bf16 a[40:43],a[212:215],v[180:183],a[40:43]
ds_read_b128 a[168:171],v1 offset:8704
v_cvt_pk_bf16_f32 v212,v212,v213
v_mfma_f32_16x16x32_bf16 a[44:47],a[220:223],v[180:183],a[44:47]
v_exp_f32 v214,v214
v_mfma_f32_16x16x32_bf16 a[44:47],a[216:219],v[164:167],a[44:47]
ds_read_b128 a[172:175],v1 offset:8960
v_mfma_f32_16x16x32_bf16 a[48:51],a[224:227],v[164:167],a[48:51]
buffer_load_dwordx4 v8,s[72:75],s80 offen offset:1024 lds
v_mfma_f32_16x16x32_bf16 a[48:51],a[228:231],v[180:183],a[48:51]
ds_read_b128 a[188:191],v1 offset:13056
v_mfma_f32_16x16x32_bf16 a[52:55],a[236:239],v[180:183],a[52:55]
v_exp_f32 v215,v215
v_mfma_f32_16x16x32_bf16 a[52:55],a[232:235],v[164:167],a[52:55]
ds_read_b128 a[184:187],v1 offset:12800
v_cvt_pk_bf16_f32 v213,v214,v215
v_mfma_f32_16x16x32_bf16 a[56:59],a[240:243],v[164:167],a[56:59]
v_exp_f32 v220,v220
v_mfma_f32_16x16x32_bf16 a[56:59],a[244:247],v[180:183],a[56:59]
ds_read_b128 a[180:183],v1 offset:12544
v_mfma_f32_16x16x32_bf16 a[60:63],a[252:255],v[180:183],a[60:63]
v_exp_f32 v221,v221
v_mfma_f32_16x16x32_bf16 a[60:63],a[248:251],v[164:167],a[60:63]
ds_read_b128 a[176:179],v1 offset:12288
v_cvt_pk_bf16_f32 v214,v220,v221
v_mfma_f32_16x16x32_bf16 v[52:55],v[28:31],v[164:167],v[52:55]
buffer_store_dwordx4 v[112:115],v6,s[36:39],s87 offen sc1
v_mfma_f32_16x16x32_bf16 v[52:55],v[28:31],v[180:183],v[52:55]
s_waitcnt lgkmcnt(3)
v_exp_f32 v222,v222
v_mfma_f32_16x16x32_bf16 v[132:135],a[128:131],v[128:131],v[32:35]
v_exp_f32 v223,v223
v_mfma_f32_16x16x32_bf16 v[132:135],a[132:135],v[160:163],v[132:135]
v_cvt_pk_bf16_f32 v215,v222,v223
v_add_u32 v1,s48,v1
v_mfma_f32_16x16x32_bf16 v[132:135],a[136:139],v[192:195],v[132:135]
v_exp_f32 v228,v228
v_mfma_f32_16x16x32_bf16 v[132:135],a[140:143],v[224:227],v[132:135]
buffer_load_dwordx4 v9,s[72:75],s80 offen offset:2048 lds
v_mfma_f32_16x16x32_bf16 v[140:143],a[156:159],v[224:227],v[32:35]
v_exp_f32 v229,v229
v_mfma_f32_16x16x32_bf16 v[140:143],a[152:155],v[192:195],v[140:143]
v_exp_f32 v230,v230
v_mfma_f32_16x16x32_bf16 v[140:143],a[148:151],v[160:163],v[140:143]
v_exp_f32 v231,v231
v_mfma_f32_16x16x32_bf16 v[140:143],a[144:147],v[128:131],v[140:143]
v_cvt_pk_bf16_f32 v228,v228,v229
v_cvt_pk_bf16_f32 v229,v230,v231
v_mfma_f32_16x16x32_bf16 v[148:151],a[160:163],v[128:131],v[32:35]
v_exp_f32 v236,v236
v_mfma_f32_16x16x32_bf16 v[148:151],a[164:167],v[160:163],v[148:151]
v_exp_f32 v237,v237
v_mfma_f32_16x16x32_bf16 v[148:151],a[168:171],v[192:195],v[148:151]
v_exp_f32 v238,v238
v_mfma_f32_16x16x32_bf16 v[148:151],a[172:175],v[224:227],v[148:151]
v_exp_f32 v239,v239
v_mfma_f32_16x16x32_bf16 v[156:159],a[188:191],v[224:227],v[32:35]
s_waitcnt lgkmcnt(0)
v_exp_f32 v244,v244
v_mfma_f32_16x16x32_bf16 v[156:159],a[184:187],v[192:195],v[156:159]
v_exp_f32 v245,v245
v_mfma_f32_16x16x32_bf16 v[156:159],a[180:183],v[160:163],v[156:159]
v_exp_f32 v246,v246
v_mfma_f32_16x16x32_bf16 v[156:159],a[176:179],v[128:131],v[156:159]
v_exp_f32 v247,v247
v_mfma_f32_16x16x32_bf16 a[64:67],a[192:195],v[196:199],a[64:67]
buffer_load_dwordx4 v10,s[72:75],s80 offen offset:3072 lds
v_mfma_f32_16x16x32_bf16 a[64:67],a[196:199],v[212:215],a[64:67]
v_cvt_pk_bf16_f32 v230,v236,v237
v_cvt_pk_bf16_f32 v231,v238,v239
v_mfma_f32_16x16x32_bf16 a[68:71],a[204:207],v[212:215],a[68:71]
v_cvt_pk_bf16_f32 v244,v244,v245
v_cvt_pk_bf16_f32 v245,v246,v247
v_mfma_f32_16x16x32_bf16 a[68:71],a[200:203],v[196:199],a[68:71]
v_exp_f32 v252,v252
v_mfma_f32_16x16x32_bf16 a[72:75],a[208:211],v[196:199],a[72:75]
buffer_store_dwordx4 v[116:119],v6,s[36:39],s87 offen offset:1024 sc1
v_mfma_f32_16x16x32_bf16 a[72:75],a[212:215],v[212:215],a[72:75]
v_exp_f32 v253,v253
v_mfma_f32_16x16x32_bf16 a[76:79],a[220:223],v[212:215],a[76:79]
v_exp_f32 v254,v254
v_mfma_f32_16x16x32_bf16 a[76:79],a[216:219],v[196:199],a[76:79]
v_exp_f32 v255,v255
v_mfma_f32_16x16x32_bf16 a[80:83],a[224:227],v[196:199],a[80:83]
v_cvt_pk_bf16_f32 v246,v252,v253
v_cvt_pk_bf16_f32 v247,v254,v255
v_mfma_f32_16x16x32_bf16 a[80:83],a[228:231],v[212:215],a[80:83]
v_exp_f32 v132,v132
v_mfma_f32_16x16x32_bf16 a[84:87],a[236:239],v[212:215],a[84:87]
s_add_u32 m0,s82,86016
v_exp_f32 v133,v133
v_mfma_f32_16x16x32_bf16 a[84:87],a[232:235],v[196:199],a[84:87]
v_exp_f32 v134,v134
v_mfma_f32_16x16x32_bf16 a[88:91],a[240:243],v[196:199],a[88:91]
v_exp_f32 v135,v135
v_mfma_f32_16x16x32_bf16 a[88:91],a[244:247],v[212:215],a[88:91]
buffer_load_dwordx4 v11,s[72:75],s80 offen lds
v_mfma_f32_16x16x32_bf16 a[92:95],a[252:255],v[212:215],a[92:95]
v_cvt_pk_bf16_f32 v132,v132,v133
v_cvt_pk_bf16_f32 v133,v134,v135
v_mfma_f32_16x16x32_bf16 a[92:95],a[248:251],v[196:199],a[92:95]
v_exp_f32 v140,v140
v_mfma_f32_16x16x32_bf16 v[56:59],v[28:31],v[196:199],v[56:59]
v_exp_f32 v141,v141
v_mfma_f32_16x16x32_bf16 v[56:59],v[28:31],v[212:215],v[56:59]
v_exp_f32 v142,v142
v_mfma_f32_16x16x32_bf16 v[164:167],a[128:131],v[136:139],v[36:39]
v_exp_f32 v143,v143
v_mfma_f32_16x16x32_bf16 v[164:167],a[132:135],v[168:171],v[164:167]
v_exp_f32 v148,v148
v_mfma_f32_16x16x32_bf16 v[164:167],a[136:139],v[200:203],v[164:167]
v_exp_f32 v149,v149
v_mfma_f32_16x16x32_bf16 v[164:167],a[140:143],v[232:235],v[164:167]
v_exp_f32 v150,v150
v_mfma_f32_16x16x32_bf16 v[172:175],a[156:159],v[232:235],v[36:39]
)A"
R"A(v_exp_f32 v151,v151
v_mfma_f32_16x16x32_bf16 v[172:175],a[152:155],v[200:203],v[172:175]
s_add_u32 m0,s82,87040
v_cvt_pk_bf16_f32 v134,v140,v141
v_cvt_pk_bf16_f32 v135,v142,v143
v_mfma_f32_16x16x32_bf16 v[172:175],a[148:151],v[168:171],v[172:175]
v_cvt_pk_bf16_f32 v148,v148,v149
v_cvt_pk_bf16_f32 v149,v150,v151
v_mfma_f32_16x16x32_bf16 v[172:175],a[144:147],v[136:139],v[172:175]
v_exp_f32 v156,v156
v_mfma_f32_16x16x32_bf16 v[180:183],a[160:163],v[136:139],v[36:39]
buffer_store_dwordx4 v[120:123],v6,s[36:39],s87 offen offset:2048 sc1
v_mfma_f32_16x16x32_bf16 v[180:183],a[164:167],v[168:171],v[180:183]
v_exp_f32 v157,v157
v_mfma_f32_16x16x32_bf16 v[180:183],a[168:171],v[200:203],v[180:183]
v_exp_f32 v158,v158
v_mfma_f32_16x16x32_bf16 v[180:183],a[172:175],v[232:235],v[180:183]
v_exp_f32 v159,v159
v_mfma_f32_16x16x32_bf16 v[188:191],a[188:191],v[232:235],v[36:39]
buffer_load_dwordx4 v7,s[72:75],s83 offen lds
v_mfma_f32_16x16x32_bf16 v[188:191],a[184:187],v[200:203],v[188:191]
v_cvt_pk_bf16_f32 v150,v156,v157
v_cvt_pk_bf16_f32 v151,v158,v159
v_mfma_f32_16x16x32_bf16 v[188:191],a[180:183],v[168:171],v[188:191]
v_exp_f32 v164,v164
v_mfma_f32_16x16x32_bf16 v[188:191],a[176:179],v[136:139],v[188:191]
v_exp_f32 v165,v165
v_mfma_f32_16x16x32_bf16 a[96:99],a[192:195],v[228:231],a[96:99]
ds_read_b64_tr_b16 a[192:193],v2
ds_read_b64_tr_b16 a[194:195],v2 offset:4096
v_mfma_f32_16x16x32_bf16 a[96:99],a[196:199],v[244:247],a[96:99]
ds_read_b64_tr_b16 a[196:197],v2 offset:8192
v_cvt_pk_bf16_f32 v164,v164,v165
v_mfma_f32_16x16x32_bf16 a[100:103],a[204:207],v[244:247],a[100:103]
ds_read_b64_tr_b16 a[198:199],v2 offset:12288
v_add_u32 v1,v4,v5
v_mfma_f32_16x16x32_bf16 a[100:103],a[200:203],v[228:231],a[100:103]
ds_read_b64_tr_b16 a[204:205],v3 offset:8192
v_mfma_f32_16x16x32_bf16 a[104:107],a[208:211],v[228:231],a[104:107]
ds_read_b64_tr_b16 a[206:207],v3 offset:12288
ds_read_b64_tr_b16 a[200:201],v3
v_mfma_f32_16x16x32_bf16 a[104:107],a[212:215],v[244:247],a[104:107]
buffer_load_dwordx4 v8,s[72:75],s83 offen offset:1024 lds
v_mfma_f32_16x16x32_bf16 a[108:111],a[220:223],v[244:247],a[108:111]
ds_read_b64_tr_b16 a[202:203],v3 offset:4096
ds_read_b64_tr_b16 a[208:209],v2 offset:256
v_mfma_f32_16x16x32_bf16 a[108:111],a[216:219],v[228:231],a[108:111]
ds_read_b64_tr_b16 a[210:211],v2 offset:4352
v_mfma_f32_16x16x32_bf16 a[112:115],a[224:227],v[228:231],a[112:115]
ds_read_b64_tr_b16 a[212:213],v2 offset:8448
v_mfma_f32_16x16x32_bf16 a[112:115],a[228:231],v[244:247],a[112:115]
ds_read_b64_tr_b16 a[214:215],v2 offset:12544
v_mfma_f32_16x16x32_bf16 a[116:119],a[236:239],v[244:247],a[116:119]
ds_read_b64_tr_b16 a[220:221],v3 offset:8448
ds_read_b64_tr_b16 a[222:223],v3 offset:12544
v_mfma_f32_16x16x32_bf16 a[116:119],a[232:235],v[228:231],a[116:119]
ds_read_b64_tr_b16 a[216:217],v3 offset:256
v_mfma_f32_16x16x32_bf16 a[120:123],a[240:243],v[228:231],a[120:123]
ds_read_b64_tr_b16 a[218:219],v3 offset:4352
v_mfma_f32_16x16x32_bf16 a[120:123],a[244:247],v[244:247],a[120:123]
ds_read_b64_tr_b16 a[224:225],v2 offset:512
v_mfma_f32_16x16x32_bf16 a[124:127],a[252:255],v[244:247],a[124:127]
buffer_store_dwordx4 v[124:127],v6,s[36:39],s87 offen offset:3072 sc1
v_mfma_f32_16x16x32_bf16 a[124:127],a[248:251],v[228:231],a[124:127]
s_add_u32 s87,s55,77824
ds_read_b64_tr_b16 a[226:227],v2 offset:4608
ds_read_b64_tr_b16 a[228:229],v2 offset:8704
v_mfma_f32_16x16x32_bf16 v[60:63],v[28:31],v[228:231],v[60:63]
s_max_i32 s87,s87,s101
ds_read_b64_tr_b16 a[230:231],v2 offset:12800
v_mfma_f32_16x16x32_bf16 v[60:63],v[28:31],v[244:247],v[60:63]
ds_read_b64_tr_b16 a[236:237],v3 offset:8704
v_mfma_f32_16x16x32_bf16 v[196:199],a[128:131],v[144:147],v[40:43]
buffer_load_dwordx4 v9,s[72:75],s83 offen offset:2048 lds
v_mfma_f32_16x16x32_bf16 v[196:199],a[132:135],v[176:179],v[196:199]
ds_read_b64_tr_b16 a[238:239],v3 offset:12800
ds_read_b64_tr_b16 a[232:233],v3 offset:512
v_mfma_f32_16x16x32_bf16 v[196:199],a[136:139],v[208:211],v[196:199]
ds_read_b64_tr_b16 a[234:235],v3 offset:4608
v_mfma_f32_16x16x32_bf16 v[196:199],a[140:143],v[240:243],v[196:199]
ds_read_b64_tr_b16 a[240:241],v2 offset:768
v_mfma_f32_16x16x32_bf16 v[204:207],a[156:159],v[240:243],v[40:43]
ds_read_b64_tr_b16 a[242:243],v2 offset:4864
v_mfma_f32_16x16x32_bf16 v[204:207],a[152:155],v[208:211],v[204:207]
ds_read_b64_tr_b16 a[244:245],v2 offset:8960
ds_read_b64_tr_b16 a[246:247],v2 offset:13056
v_mfma_f32_16x16x32_bf16 v[204:207],a[148:151],v[176:179],v[204:207]
ds_read_b64_tr_b16 a[252:253],v3 offset:8960
v_sub_u32 v2,v4,v5
v_mfma_f32_16x16x32_bf16 v[204:207],a[144:147],v[144:147],v[204:207]
ds_read_b64_tr_b16 a[254:255],v3 offset:13056
v_mfma_f32_16x16x32_bf16 v[212:215],a[160:163],v[144:147],v[40:43]
ds_read_b64_tr_b16 a[248:249],v3 offset:768
v_mfma_f32_16x16x32_bf16 v[212:215],a[164:167],v[176:179],v[212:215]
ds_read_b64_tr_b16 a[250:251],v3 offset:4864
v_mfma_f32_16x16x32_bf16 v[212:215],a[168:171],v[208:211],v[212:215]
v_exp_f32 v166,v166
v_mfma_f32_16x16x32_bf16 v[212:215],a[172:175],v[240:243],v[212:215]
v_exp_f32 v167,v167
v_mfma_f32_16x16x32_bf16 v[220:223],a[188:191],v[240:243],v[40:43]
v_exp_f32 v172,v172
v_mfma_f32_16x16x32_bf16 v[220:223],a[184:187],v[208:211],v[220:223]
buffer_load_dwordx4 v10,s[72:75],s83 offen offset:3072 lds
v_mfma_f32_16x16x32_bf16 v[220:223],a[180:183],v[176:179],v[220:223]
v_exp_f32 v173,v173
v_mfma_f32_16x16x32_bf16 v[220:223],a[176:179],v[144:147],v[220:223]
s_waitcnt lgkmcnt(2)
v_exp_f32 v174,v174
v_mfma_f32_16x16x32_bf16 a[0:3],a[192:195],v[132:135],a[0:3]
v_exp_f32 v175,v175
v_mfma_f32_16x16x32_bf16 a[0:3],a[196:199],v[148:151],a[0:3]
v_exp_f32 v180,v180
v_mfma_f32_16x16x32_bf16 a[4:7],a[204:207],v[148:151],a[4:7]
v_exp_f32 v181,v181
v_mfma_f32_16x16x32_bf16 a[4:7],a[200:203],v[132:135],a[4:7]
v_cvt_pk_bf16_f32 v165,v166,v167
v_cvt_pk_bf16_f32 v166,v172,v173
v_mfma_f32_16x16x32_bf16 a[8:11],a[208:211],v[132:135],a[8:11]
v_exp_f32 v182,v182
v_mfma_f32_16x16x32_bf16 a[8:11],a[212:215],v[148:151],a[8:11]
v_exp_f32 v183,v183
v_mfma_f32_16x16x32_bf16 a[12:15],a[220:223],v[148:151],a[12:15]
v_cvt_pk_bf16_f32 v167,v174,v175
v_cvt_pk_bf16_f32 v180,v180,v181
v_mfma_f32_16x16x32_bf16 a[12:15],a[216:219],v[132:135],a[12:15]
s_add_u32 m0,s82,91136
v_exp_f32 v188,v188
v_mfma_f32_16x16x32_bf16 a[16:19],a[224:227],v[132:135],a[16:19]
v_exp_f32 v189,v189
v_mfma_f32_16x16x32_bf16 a[16:19],a[228:231],v[148:151],a[16:19]
v_exp_f32 v190,v190
v_mfma_f32_16x16x32_bf16 a[20:23],a[236:239],v[148:151],a[20:23]
buffer_load_dwordx4 v11,s[72:75],s83 offen lds
v_mfma_f32_16x16x32_bf16 a[20:23],a[232:235],v[132:135],a[20:23]
v_exp_f32 v191,v191
v_mfma_f32_16x16x32_bf16 a[24:27],a[240:243],v[132:135],a[24:27]
v_cvt_pk_bf16_f32 v181,v182,v183
v_cvt_pk_bf16_f32 v182,v188,v189
v_mfma_f32_16x16x32_bf16 a[24:27],a[244:247],v[148:151],a[24:27]
v_cvt_pk_bf16_f32 v183,v190,v191
v_mfma_f32_16x16x32_bf16 a[28:31],a[252:255],v[148:151],a[28:31]
s_waitcnt lgkmcnt(0)
v_exp_f32 v196,v196
v_mfma_f32_16x16x32_bf16 a[28:31],a[248:251],v[132:135],a[28:31]
s_cmp_ge_u32 1,s71
s_cselect_b32 s80,4,2
s_mul_i32 s80,s80,163840
v_mfma_f32_16x16x32_bf16 v[48:51],v[28:31],v[132:135],v[48:51]
s_waitcnt vmcnt(15)
s_add_u32 s80,s80,s81
v_exp_f32 v197,v197
v_mfma_f32_16x16x32_bf16 v[48:51],v[28:31],v[148:151],v[48:51]
s_barrier
v_exp_f32 v198,v198
v_mfma_f32_16x16x32_bf16 v[228:231],a[128:131],v[152:155],v[44:47]
s_add_u32 s83,s80,20480
v_exp_f32 v199,v199
v_mfma_f32_16x16x32_bf16 v[228:231],a[132:135],v[184:187],v[228:231]
v_cvt_pk_bf16_f32 v196,v196,v197
v_cvt_pk_bf16_f32 v197,v198,v199
v_mfma_f32_16x16x32_bf16 v[228:231],a[136:139],v[216:219],v[228:231]
v_exp_f32 v204,v204
v_mfma_f32_16x16x32_bf16 v[228:231],a[140:143],v[248:251],v[228:231]
v_exp_f32 v205,v205
v_mfma_f32_16x16x32_bf16 v[236:239],a[156:159],v[248:251],v[44:47]
v_exp_f32 v206,v206
v_mfma_f32_16x16x32_bf16 v[236:239],a[152:155],v[216:219],v[236:239]
v_exp_f32 v207,v207
v_mfma_f32_16x16x32_bf16 v[236:239],a[148:151],v[184:187],v[236:239]
v_exp_f32 v212,v212
v_mfma_f32_16x16x32_bf16 v[236:239],a[144:147],v[152:155],v[236:239]
v_exp_f32 v213,v213
v_mfma_f32_16x16x32_bf16 v[244:247],a[160:163],v[152:155],v[44:47]
v_exp_f32 v214,v214
v_mfma_f32_16x16x32_bf16 v[244:247],a[164:167],v[184:187],v[244:247]
v_exp_f32 v215,v215
v_mfma_f32_16x16x32_bf16 v[244:247],a[168:171],v[216:219],v[244:247]
v_cvt_pk_bf16_f32 v198,v204,v205
v_cvt_pk_bf16_f32 v199,v206,v207
v_mfma_f32_16x16x32_bf16 v[244:247],a[172:175],v[248:251],v[244:247]
s_lshl_b32 s95,s42,2
s_add_u32 s95,s95,s40
s_lshl_b32 s95,s95,4
v_mfma_f32_16x16x32_bf16 v[252:255],a[188:191],v[248:251],v[44:47]
v_mov_b32 v18,s68
v_mov_b32 v19,s69
v_mfma_f32_16x16x32_bf16 v[252:255],a[184:187],v[216:219],v[252:255]
v_cvt_pk_bf16_f32 v212,v212,v213
v_cvt_pk_bf16_f32 v213,v214,v215
v_mfma_f32_16x16x32_bf16 v[252:255],a[180:183],v[184:187],v[252:255]
v_exp_f32 v220,v220
v_mfma_f32_16x16x32_bf16 v[252:255],a[176:179],v[152:155],v[252:255]
v_exp_f32 v221,v221
v_mfma_f32_16x16x32_bf16 a[32:35],a[192:195],v[164:167],a[32:35]
v_exp_f32 v222,v222
v_mfma_f32_16x16x32_bf16 a[32:35],a[196:199],v[180:183],a[32:35]
s_add_u32 m0,s82,122880
v_exp_f32 v223,v223
v_mfma_f32_16x16x32_bf16 a[36:39],a[204:207],v[180:183],a[36:39]
v_cvt_pk_bf16_f32 v214,v220,v221
v_cvt_pk_bf16_f32 v215,v222,v223
v_mfma_f32_16x16x32_bf16 a[36:39],a[200:203],v[164:167],a[36:39]
s_waitcnt vmcnt(3)
s_mov_b64 exec,1
buffer_store_dwordx2 v[18:19],v6,s[64:67],s95 offen sc1
s_mov_b64 exec,-1
v_mfma_f32_16x16x32_bf16 a[40:43],a[208:211],v[164:167],a[40:43]
ds_read_b64_tr_b16 a[128:129],v1
v_max_f32 v20,0x00800000,v48
v_mfma_f32_16x16x32_bf16 a[40:43],a[212:215],v[180:183],a[40:43]
v_exp_f32 v228,v228
v_mfma_f32_16x16x32_bf16 a[44:47],a[220:223],v[180:183],a[44:47]
v_exp_f32 v229,v229
v_mfma_f32_16x16x32_bf16 a[44:47],a[216:219],v[164:167],a[44:47]
buffer_load_dwordx4 v7,s[72:75],s80 offen lds
v_mfma_f32_16x16x32_bf16 a[48:51],a[224:227],v[164:167],a[48:51]
v_exp_f32 v230,v230
v_mfma_f32_16x16x32_bf16 a[48:51],a[228:231],v[180:183],a[48:51]
v_exp_f32 v231,v231
v_mfma_f32_16x16x32_bf16 a[52:55],a[236:239],v[180:183],a[52:55]
v_rcp_f32 v20,v20
v_mfma_f32_16x16x32_bf16 a[52:55],a[232:235],v[164:167],a[52:55]
v_cvt_pk_bf16_f32 v228,v228,v229
v_cvt_pk_bf16_f32 v229,v230,v231
v_mfma_f32_16x16x32_bf16 a[56:59],a[240:243],v[164:167],a[56:59]
v_exp_f32 v236,v236
v_mfma_f32_16x16x32_bf16 a[56:59],a[244:247],v[180:183],a[56:59]
v_exp_f32 v237,v237
v_mfma_f32_16x16x32_bf16 a[60:63],a[252:255],v[180:183],a[60:63]
v_exp_f32 v238,v238
v_mfma_f32_16x16x32_bf16 a[60:63],a[248:251],v[164:167],a[60:63]
v_exp_f32 v239,v239
v_mfma_f32_16x16x32_bf16 v[52:55],v[28:31],v[164:167],v[52:55]
v_exp_f32 v244,v244
v_mfma_f32_16x16x32_bf16 v[52:55],v[28:31],v[180:183],v[52:55]
v_exp_f32 v245,v245
v_mfma_f32_16x16x32_bf16 a[64:67],a[192:195],v[196:199],a[64:67]
v_exp_f32 v246,v246
v_mfma_f32_16x16x32_bf16 a[64:67],a[196:199],v[212:215],a[64:67]
v_exp_f32 v247,v247
v_mfma_f32_16x16x32_bf16 a[68:71],a[204:207],v[212:215],a[68:71]
buffer_load_dwordx4 v8,s[72:75],s80 offen offset:1024 lds
v_mfma_f32_16x16x32_bf16 a[68:71],a[200:203],v[196:199],a[68:71]
v_accvgpr_read_b32 v132,a0
v_accvgpr_read_b32 v133,a1
v_accvgpr_read_b32 v134,a2
v_accvgpr_read_b32 v135,a3
v_accvgpr_read_b32 v12,a4
v_accvgpr_read_b32 v13,a5
v_accvgpr_read_b32 v14,a6
v_accvgpr_read_b32 v15,a7
v_pk_mul_f32 v[132:133],v[20:21],v[132:133] op_sel:[0,0] op_sel_hi:[0,1]
v_pk_mul_f32 v[134:135],v[20:21],v[134:135] op_sel:[0,0] op_sel_hi:[0,1]
v_pk_mul_f32 v[12:13],v[20:21],v[12:13] op_sel:[0,0] op_sel_hi:[0,1]
v_pk_mul_f32 v[14:15],v[20:21],v[14:15] op_sel:[0,0] op_sel_hi:[0,1]
v_cvt_pk_bf16_f32 v132,v132,v133
v_cvt_pk_bf16_f32 v133,v134,v135
v_cvt_pk_bf16_f32 v134,v12,v13
v_cvt_pk_bf16_f32 v135,v14,v15
v_mfma_f32_16x16x32_bf16 a[72:75],a[208:211],v[196:199],a[72:75]
v_cvt_pk_bf16_f32 v230,v236,v237
v_cvt_pk_bf16_f32 v231,v238,v239
v_mfma_f32_16x16x32_bf16 a[72:75],a[212:215],v[212:215],a[72:75]
v_cvt_pk_bf16_f32 v244,v244,v245
v_cvt_pk_bf16_f32 v245,v246,v247
v_mfma_f32_16x16x32_bf16 a[76:79],a[220:223],v[212:215],a[76:79]
)A"
R"A(v_exp_f32 v252,v252
v_mfma_f32_16x16x32_bf16 a[76:79],a[216:219],v[196:199],a[76:79]
v_exp_f32 v253,v253
v_mfma_f32_16x16x32_bf16 a[80:83],a[224:227],v[196:199],a[80:83]
v_exp_f32 v254,v254
v_mfma_f32_16x16x32_bf16 a[80:83],a[228:231],v[212:215],a[80:83]
v_exp_f32 v255,v255
v_mfma_f32_16x16x32_bf16 a[84:87],a[236:239],v[212:215],a[84:87]
v_cvt_pk_bf16_f32 v246,v252,v253
v_cvt_pk_bf16_f32 v247,v254,v255
v_mfma_f32_16x16x32_bf16 a[84:87],a[232:235],v[196:199],a[84:87]
v_accvgpr_read_b32 v140,a8
v_accvgpr_read_b32 v141,a9
v_accvgpr_read_b32 v142,a10
v_accvgpr_read_b32 v143,a11
v_accvgpr_read_b32 v12,a12
v_accvgpr_read_b32 v13,a13
v_accvgpr_read_b32 v14,a14
v_accvgpr_read_b32 v15,a15
v_pk_mul_f32 v[140:141],v[20:21],v[140:141] op_sel:[0,0] op_sel_hi:[0,1]
v_pk_mul_f32 v[142:143],v[20:21],v[142:143] op_sel:[0,0] op_sel_hi:[0,1]
v_pk_mul_f32 v[12:13],v[20:21],v[12:13] op_sel:[0,0] op_sel_hi:[0,1]
v_pk_mul_f32 v[14:15],v[20:21],v[14:15] op_sel:[0,0] op_sel_hi:[0,1]
v_cvt_pk_bf16_f32 v140,v140,v141
v_cvt_pk_bf16_f32 v141,v142,v143
v_cvt_pk_bf16_f32 v142,v12,v13
v_cvt_pk_bf16_f32 v143,v14,v15
v_mfma_f32_16x16x32_bf16 a[88:91],a[240:243],v[196:199],a[88:91]
ds_read_b64_tr_b16 a[130:131],v1 offset:5120
ds_read_b64_tr_b16 a[132:133],v1 offset:10240
v_mfma_f32_16x16x32_bf16 a[88:91],a[244:247],v[212:215],a[88:91]
ds_read_b64_tr_b16 a[134:135],v1 offset:15360
v_accvgpr_read_b32 v148,a16
v_mfma_f32_16x16x32_bf16 a[92:95],a[252:255],v[212:215],a[92:95]
ds_read_b64_tr_b16 a[136:137],v1 offset:20480
v_accvgpr_read_b32 v149,a17
v_mfma_f32_16x16x32_bf16 a[92:95],a[248:251],v[196:199],a[92:95]
buffer_store_dwordx4 v[132:135],v6,s[36:39],s84 offen sc1
v_mfma_f32_16x16x32_bf16 v[56:59],v[28:31],v[196:199],v[56:59]
ds_read_b64_tr_b16 a[138:139],v1 offset:25600
ds_read_b64_tr_b16 a[140:141],v1 offset:30720
v_mfma_f32_16x16x32_bf16 v[56:59],v[28:31],v[212:215],v[56:59]
ds_read_b64_tr_b16 a[142:143],v1 offset:35840
v_mul_f32 v148,v20,v148
v_mfma_f32_16x16x32_bf16 a[96:99],a[192:195],v[228:231],a[96:99]
v_mul_f32 v149,v20,v149
v_accvgpr_read_b32 v150,a18
v_mfma_f32_16x16x32_bf16 a[96:99],a[196:199],v[244:247],a[96:99]
buffer_load_dwordx4 v9,s[72:75],s80 offen offset:2048 lds
v_mfma_f32_16x16x32_bf16 a[100:103],a[204:207],v[244:247],a[100:103]
v_accvgpr_read_b32 v151,a19
v_cvt_pk_bf16_f32 v148,v148,v149
v_mfma_f32_16x16x32_bf16 a[100:103],a[200:203],v[228:231],a[100:103]
v_mul_f32 v150,v20,v150
v_mul_f32 v151,v20,v151
v_mfma_f32_16x16x32_bf16 a[104:107],a[208:211],v[228:231],a[104:107]
v_cvt_pk_bf16_f32 v149,v150,v151
v_accvgpr_read_b32 v150,a20
v_mfma_f32_16x16x32_bf16 a[104:107],a[212:215],v[244:247],a[104:107]
v_accvgpr_read_b32 v151,a21
v_accvgpr_read_b32 v14,a22
v_mfma_f32_16x16x32_bf16 a[108:111],a[220:223],v[244:247],a[108:111]
v_accvgpr_read_b32 v15,a23
v_mul_f32 v150,v20,v150
v_mfma_f32_16x16x32_bf16 a[108:111],a[216:219],v[228:231],a[108:111]
v_mul_f32 v151,v20,v151
v_mul_f32 v14,v20,v14
v_mfma_f32_16x16x32_bf16 a[112:115],a[224:227],v[228:231],a[112:115]
v_mul_f32 v15,v20,v15
v_cvt_pk_bf16_f32 v150,v150,v151
v_mfma_f32_16x16x32_bf16 a[112:115],a[228:231],v[244:247],a[112:115]
ds_read_b64_tr_b16 a[144:145],v2 offset:32
v_cvt_pk_bf16_f32 v151,v14,v15
v_mfma_f32_16x16x32_bf16 a[116:119],a[236:239],v[244:247],a[116:119]
ds_read_b64_tr_b16 a[146:147],v2 offset:5152
ds_read_b64_tr_b16 a[148:149],v2 offset:10272
v_mfma_f32_16x16x32_bf16 a[116:119],a[232:235],v[228:231],a[116:119]
ds_read_b64_tr_b16 a[150:151],v2 offset:15392
v_accvgpr_read_b32 v156,a24
v_mfma_f32_16x16x32_bf16 a[120:123],a[240:243],v[228:231],a[120:123]
ds_read_b64_tr_b16 a[152:153],v2 offset:20512
v_accvgpr_read_b32 v157,a25
v_mfma_f32_16x16x32_bf16 a[120:123],a[244:247],v[244:247],a[120:123]
ds_read_b64_tr_b16 a[154:155],v2 offset:25632
v_mul_f32 v156,v20,v156
v_mfma_f32_16x16x32_bf16 a[124:127],a[252:255],v[244:247],a[124:127]
ds_read_b64_tr_b16 a[156:157],v2 offset:30752
ds_read_b64_tr_b16 a[158:159],v2 offset:35872
v_mfma_f32_16x16x32_bf16 a[124:127],a[248:251],v[228:231],a[124:127]
v_mul_f32 v157,v20,v157
v_accvgpr_read_b32 v158,a26
v_mfma_f32_16x16x32_bf16 v[60:63],v[28:31],v[228:231],v[60:63]
buffer_load_dwordx4 v10,s[72:75],s80 offen offset:3072 lds
v_mfma_f32_16x16x32_bf16 v[60:63],v[28:31],v[244:247],v[60:63]
s_waitcnt lgkmcnt(7)
v_accvgpr_read_b32 v159,a27
v_cvt_pk_bf16_f32 v156,v156,v157
v_mfma_f32_16x16x32_bf16 a[192:195],a[128:131],v[64:67],0
v_mul_f32 v158,v20,v158
v_mul_f32 v159,v20,v159
v_mfma_f32_16x16x32_bf16 a[192:195],a[132:135],v[68:71],a[192:195]
v_cvt_pk_bf16_f32 v157,v158,v159
v_accvgpr_read_b32 v158,a28
v_mfma_f32_16x16x32_bf16 a[192:195],a[136:139],v[72:75],a[192:195]
buffer_store_dwordx4 v[140:143],v6,s[36:39],s84 offen offset:1024 sc1
v_mfma_f32_16x16x32_bf16 a[192:195],a[140:143],v[76:79],a[192:195]
v_accvgpr_read_b32 v159,a29
v_accvgpr_read_b32 v16,a30
v_mfma_f32_16x16x32_bf16 a[196:199],a[140:143],v[92:95],0
v_accvgpr_read_b32 v17,a31
v_mul_f32 v158,v20,v158
v_mfma_f32_16x16x32_bf16 a[196:199],a[136:139],v[88:91],a[196:199]
v_mul_f32 v159,v20,v159
v_mul_f32 v16,v20,v16
v_mfma_f32_16x16x32_bf16 a[196:199],a[132:135],v[84:87],a[196:199]
v_mul_f32 v17,v20,v17
v_cvt_pk_bf16_f32 v158,v158,v159
v_mfma_f32_16x16x32_bf16 a[196:199],a[128:131],v[80:83],a[196:199]
ds_read_b64_tr_b16 a[160:161],v1 offset:64
v_cvt_pk_bf16_f32 v159,v16,v17
v_mfma_f32_16x16x32_bf16 a[200:203],a[128:131],v[96:99],0
ds_read_b64_tr_b16 a[162:163],v1 offset:5184
ds_read_b64_tr_b16 a[164:165],v1 offset:10304
v_mfma_f32_16x16x32_bf16 a[200:203],a[132:135],v[100:103],a[200:203]
ds_read_b64_tr_b16 a[166:167],v1 offset:15424
v_max_f32 v21,0x00800000,v52
v_mfma_f32_16x16x32_bf16 a[200:203],a[136:139],v[104:107],a[200:203]
ds_read_b64_tr_b16 a[168:169],v1 offset:20544
v_accvgpr_read_b32 v180,a48
v_mfma_f32_16x16x32_bf16 a[200:203],a[140:143],v[108:111],a[200:203]
s_add_u32 m0,s82,126976
ds_read_b64_tr_b16 a[170:171],v1 offset:25664
v_accvgpr_read_b32 v181,a49
v_mfma_f32_16x16x32_bf16 a[204:207],a[140:143],v[124:127],0
ds_read_b64_tr_b16 a[172:173],v1 offset:30784
ds_read_b64_tr_b16 a[174:175],v1 offset:35904
v_mfma_f32_16x16x32_bf16 a[204:207],a[136:139],v[120:123],a[204:207]
v_rcp_f32 v21,v21
v_mfma_f32_16x16x32_bf16 a[204:207],a[132:135],v[116:119],a[204:207]
buffer_load_dwordx4 v11,s[72:75],s80 offen lds
v_mfma_f32_16x16x32_bf16 a[204:207],a[128:131],v[112:115],a[204:207]
s_waitcnt lgkmcnt(7)
v_accvgpr_read_b32 v164,a32
v_accvgpr_read_b32 v165,a33
v_accvgpr_read_b32 v166,a34
v_accvgpr_read_b32 v167,a35
v_accvgpr_read_b32 v12,a36
v_accvgpr_read_b32 v13,a37
v_accvgpr_read_b32 v14,a38
v_accvgpr_read_b32 v15,a39
v_pk_mul_f32 v[164:165],v[20:21],v[164:165] op_sel:[1,0] op_sel_hi:[1,1]
v_pk_mul_f32 v[166:167],v[20:21],v[166:167] op_sel:[1,0] op_sel_hi:[1,1]
v_pk_mul_f32 v[12:13],v[20:21],v[12:13] op_sel:[1,0] op_sel_hi:[1,1]
v_pk_mul_f32 v[14:15],v[20:21],v[14:15] op_sel:[1,0] op_sel_hi:[1,1]
v_cvt_pk_bf16_f32 v164,v164,v165
v_cvt_pk_bf16_f32 v165,v166,v167
v_cvt_pk_bf16_f32 v166,v12,v13
v_cvt_pk_bf16_f32 v167,v14,v15
v_mfma_f32_16x16x32_bf16 a[208:211],a[144:147],v[64:67],0
ds_read_b64_tr_b16 a[176:177],v2 offset:96
ds_read_b64_tr_b16 a[178:179],v2 offset:5216
v_mfma_f32_16x16x32_bf16 a[208:211],a[148:151],v[68:71],a[208:211]
v_accvgpr_read_b32 v172,a40
v_accvgpr_read_b32 v173,a41
v_accvgpr_read_b32 v174,a42
v_accvgpr_read_b32 v175,a43
v_accvgpr_read_b32 v12,a44
v_accvgpr_read_b32 v13,a45
v_accvgpr_read_b32 v14,a46
v_accvgpr_read_b32 v15,a47
v_pk_mul_f32 v[172:173],v[20:21],v[172:173] op_sel:[1,0] op_sel_hi:[1,1]
v_pk_mul_f32 v[174:175],v[20:21],v[174:175] op_sel:[1,0] op_sel_hi:[1,1]
v_pk_mul_f32 v[12:13],v[20:21],v[12:13] op_sel:[1,0] op_sel_hi:[1,1]
v_pk_mul_f32 v[14:15],v[20:21],v[14:15] op_sel:[1,0] op_sel_hi:[1,1]
v_cvt_pk_bf16_f32 v172,v172,v173
v_cvt_pk_bf16_f32 v173,v174,v175
v_cvt_pk_bf16_f32 v174,v12,v13
v_cvt_pk_bf16_f32 v175,v14,v15
v_mfma_f32_16x16x32_bf16 a[208:211],a[152:155],v[72:75],a[208:211]
ds_read_b64_tr_b16 a[180:181],v2 offset:10336
ds_read_b64_tr_b16 a[182:183],v2 offset:15456
v_mfma_f32_16x16x32_bf16 a[208:211],a[156:159],v[76:79],a[208:211]
buffer_store_dwordx4 v[148:151],v6,s[36:39],s84 offen offset:2048 sc1
v_mfma_f32_16x16x32_bf16 a[212:215],a[156:159],v[92:95],0
ds_read_b64_tr_b16 a[184:185],v2 offset:20576
ds_read_b64_tr_b16 a[186:187],v2 offset:25696
v_mfma_f32_16x16x32_bf16 a[212:215],a[152:155],v[88:91],a[212:215]
ds_read_b64_tr_b16 a[188:189],v2 offset:30816
v_mul_f32 v180,v21,v180
v_mfma_f32_16x16x32_bf16 a[212:215],a[148:151],v[84:87],a[212:215]
ds_read_b64_tr_b16 a[190:191],v2 offset:35936
v_mul_f32 v181,v21,v181
v_mfma_f32_16x16x32_bf16 a[212:215],a[144:147],v[80:83],a[212:215]
v_accvgpr_read_b32 v182,a50
v_accvgpr_read_b32 v183,a51
v_mfma_f32_16x16x32_bf16 a[216:219],a[144:147],v[96:99],0
v_cvt_pk_bf16_f32 v180,v180,v181
v_mul_f32 v182,v21,v182
v_mfma_f32_16x16x32_bf16 a[216:219],a[148:151],v[100:103],a[216:219]
v_mul_f32 v183,v21,v183
v_cvt_pk_bf16_f32 v181,v182,v183
v_mfma_f32_16x16x32_bf16 a[216:219],a[152:155],v[104:107],a[216:219]
v_accvgpr_read_b32 v182,a52
v_accvgpr_read_b32 v183,a53
v_mfma_f32_16x16x32_bf16 a[216:219],a[156:159],v[108:111],a[216:219]
s_add_u32 m0,s82,128000
v_accvgpr_read_b32 v12,a54
v_accvgpr_read_b32 v13,a55
v_mfma_f32_16x16x32_bf16 a[220:223],a[156:159],v[124:127],0
v_mul_f32 v182,v21,v182
v_mul_f32 v183,v21,v183
v_mfma_f32_16x16x32_bf16 a[220:223],a[152:155],v[120:123],a[220:223]
v_mul_f32 v12,v21,v12
v_mul_f32 v13,v21,v13
v_mfma_f32_16x16x32_bf16 a[220:223],a[148:151],v[116:119],a[220:223]
buffer_load_dwordx4 v7,s[72:75],s83 offen lds
v_mfma_f32_16x16x32_bf16 a[220:223],a[144:147],v[112:115],a[220:223]
s_waitcnt lgkmcnt(0)
v_cvt_pk_bf16_f32 v182,v182,v183
v_cvt_pk_bf16_f32 v183,v12,v13
v_mfma_f32_16x16x32_bf16 a[224:227],a[160:163],v[64:67],0
ds_read_b64_tr_b16 a[128:129],v1 offset:128
ds_read_b64_tr_b16 a[130:131],v1 offset:5248
v_mfma_f32_16x16x32_bf16 a[224:227],a[164:167],v[68:71],a[224:227]
ds_read_b64_tr_b16 a[132:133],v1 offset:10368
v_accvgpr_read_b32 v188,a56
v_mfma_f32_16x16x32_bf16 a[224:227],a[168:171],v[72:75],a[224:227]
ds_read_b64_tr_b16 a[134:135],v1 offset:15488
v_accvgpr_read_b32 v189,a57
v_mfma_f32_16x16x32_bf16 a[224:227],a[172:175],v[76:79],a[224:227]
ds_read_b64_tr_b16 a[136:137],v1 offset:20608
v_mul_f32 v188,v21,v188
v_mfma_f32_16x16x32_bf16 a[228:231],a[172:175],v[92:95],0
ds_read_b64_tr_b16 a[138:139],v1 offset:25728
ds_read_b64_tr_b16 a[140:141],v1 offset:30848
v_mfma_f32_16x16x32_bf16 a[228:231],a[168:171],v[88:91],a[228:231]
ds_read_b64_tr_b16 a[142:143],v1 offset:35968
v_mul_f32 v189,v21,v189
v_mfma_f32_16x16x32_bf16 a[228:231],a[164:167],v[84:87],a[228:231]
v_accvgpr_read_b32 v190,a58
v_accvgpr_read_b32 v191,a59
v_mfma_f32_16x16x32_bf16 a[228:231],a[160:163],v[80:83],a[228:231]
buffer_store_dwordx4 v[156:159],v6,s[36:39],s84 offen offset:3072 sc1
v_mfma_f32_16x16x32_bf16 a[232:235],a[160:163],v[96:99],0
v_cvt_pk_bf16_f32 v188,v188,v189
v_mul_f32 v190,v21,v190
v_mfma_f32_16x16x32_bf16 a[232:235],a[164:167],v[100:103],a[232:235]
v_mul_f32 v191,v21,v191
v_cvt_pk_bf16_f32 v189,v190,v191
v_mfma_f32_16x16x32_bf16 a[232:235],a[168:171],v[104:107],a[232:235]
v_accvgpr_read_b32 v190,a60
v_accvgpr_read_b32 v191,a61
v_mfma_f32_16x16x32_bf16 a[232:235],a[172:175],v[108:111],a[232:235]
v_accvgpr_read_b32 v14,a62
v_accvgpr_read_b32 v15,a63
v_mfma_f32_16x16x32_bf16 a[236:239],a[172:175],v[124:127],0
v_mul_f32 v190,v21,v190
v_mul_f32 v191,v21,v191
v_mfma_f32_16x16x32_bf16 a[236:239],a[168:171],v[120:123],a[236:239]
v_mul_f32 v14,v21,v14
v_mul_f32 v15,v21,v15
v_mfma_f32_16x16x32_bf16 a[236:239],a[164:167],v[116:119],a[236:239]
buffer_load_dwordx4 v8,s[72:75],s83 offen offset:1024 lds
v_mfma_f32_16x16x32_bf16 a[236:239],a[160:163],v[112:115],a[236:239]
v_cvt_pk_bf16_f32 v190,v190,v191
v_cvt_pk_bf16_f32 v191,v14,v15
v_mfma_f32_16x16x32_bf16 a[240:243],a[176:179],v[64:67],0
ds_read_b64_tr_b16 a[144:145],v2 offset:160
v_max_f32 v22,0x00800000,v56
v_mfma_f32_16x16x32_bf16 a[240:243],a[180:183],v[68:71],a[240:243]
ds_read_b64_tr_b16 a[146:147],v2 offset:5280
ds_read_b64_tr_b16 a[148:149],v2 offset:10400
)A"
R"A(v_mfma_f32_16x16x32_bf16 a[240:243],a[184:187],v[72:75],a[240:243]
v_rcp_f32 v22,v22
v_mfma_f32_16x16x32_bf16 a[240:243],a[188:191],v[76:79],a[240:243]
ds_read_b64_tr_b16 a[150:151],v2 offset:15520
v_accvgpr_read_b32 v196,a64
v_mfma_f32_16x16x32_bf16 a[244:247],a[188:191],v[92:95],0
v_accvgpr_read_b32 v197,a65
v_mul_f32 v196,v22,v196
v_mfma_f32_16x16x32_bf16 a[244:247],a[184:187],v[88:91],a[244:247]
v_mul_f32 v197,v22,v197
v_accvgpr_read_b32 v198,a66
v_mfma_f32_16x16x32_bf16 a[244:247],a[180:183],v[84:87],a[244:247]
ds_read_b64_tr_b16 a[152:153],v2 offset:20640
v_accvgpr_read_b32 v199,a67
v_mfma_f32_16x16x32_bf16 a[244:247],a[176:179],v[80:83],a[244:247]
ds_read_b64_tr_b16 a[154:155],v2 offset:25760
v_cvt_pk_bf16_f32 v196,v196,v197
v_mfma_f32_16x16x32_bf16 a[248:251],a[176:179],v[96:99],0
v_mul_f32 v198,v22,v198
v_mul_f32 v199,v22,v199
v_mfma_f32_16x16x32_bf16 a[248:251],a[180:183],v[100:103],a[248:251]
ds_read_b64_tr_b16 a[156:157],v2 offset:30880
v_cvt_pk_bf16_f32 v197,v198,v199
v_mfma_f32_16x16x32_bf16 a[248:251],a[184:187],v[104:107],a[248:251]
ds_read_b64_tr_b16 a[158:159],v2 offset:36000
v_accvgpr_read_b32 v198,a68
v_mfma_f32_16x16x32_bf16 a[248:251],a[188:191],v[108:111],a[248:251]
v_accvgpr_read_b32 v199,a69
v_accvgpr_read_b32 v16,a70
v_mfma_f32_16x16x32_bf16 a[252:255],a[188:191],v[124:127],0
buffer_store_dwordx4 v[164:167],v6,s[36:39],s85 offen sc1
v_mfma_f32_16x16x32_bf16 a[252:255],a[184:187],v[120:123],a[252:255]
v_accvgpr_read_b32 v17,a71
v_mul_f32 v198,v22,v198
v_mfma_f32_16x16x32_bf16 a[252:255],a[180:183],v[116:119],a[252:255]
v_mul_f32 v199,v22,v199
v_mul_f32 v16,v22,v16
v_mfma_f32_16x16x32_bf16 a[252:255],a[176:179],v[112:115],a[252:255]
s_waitcnt lgkmcnt(2)
v_mul_f32 v17,v22,v17
v_cvt_pk_bf16_f32 v198,v198,v199
v_mfma_f32_16x16x32_bf16 a[0:3],a[128:131],v[64:67],0
buffer_load_dwordx4 v9,s[72:75],s83 offen offset:2048 lds
v_mfma_f32_16x16x32_bf16 a[0:3],a[132:135],v[68:71],a[0:3]
v_cvt_pk_bf16_f32 v199,v16,v17
v_accvgpr_read_b32 v204,a72
v_mfma_f32_16x16x32_bf16 a[0:3],a[136:139],v[72:75],a[0:3]
v_accvgpr_read_b32 v205,a73
v_mul_f32 v204,v22,v204
v_mfma_f32_16x16x32_bf16 a[0:3],a[140:143],v[76:79],a[0:3]
v_mul_f32 v205,v22,v205
v_accvgpr_read_b32 v206,a74
v_mfma_f32_16x16x32_bf16 a[4:7],a[140:143],v[92:95],0
v_accvgpr_read_b32 v207,a75
v_cvt_pk_bf16_f32 v204,v204,v205
v_mfma_f32_16x16x32_bf16 a[4:7],a[136:139],v[88:91],a[4:7]
v_mul_f32 v206,v22,v206
v_mul_f32 v207,v22,v207
v_mfma_f32_16x16x32_bf16 a[4:7],a[132:135],v[84:87],a[4:7]
s_lshl_b32 s94,s70,4
v_cvt_pk_bf16_f32 v205,v206,v207
v_accvgpr_read_b32 v206,a76
v_mfma_f32_16x16x32_bf16 a[4:7],a[128:131],v[80:83],a[4:7]
s_add_u32 s94,s94,s40
v_accvgpr_read_b32 v207,a77
v_accvgpr_read_b32 v12,a78
v_mfma_f32_16x16x32_bf16 a[8:11],a[128:131],v[96:99],0
s_lshl_b32 s94,s94,4
v_accvgpr_read_b32 v13,a79
v_mul_f32 v206,v22,v206
v_mfma_f32_16x16x32_bf16 a[8:11],a[132:135],v[100:103],a[8:11]
ds_read_b64_tr_b16 a[160:161],v1 offset:192
v_mul_f32 v207,v22,v207
v_mfma_f32_16x16x32_bf16 a[8:11],a[136:139],v[104:107],a[8:11]
ds_read_b64_tr_b16 a[162:163],v1 offset:5312
v_mul_f32 v12,v22,v12
v_mfma_f32_16x16x32_bf16 a[8:11],a[140:143],v[108:111],a[8:11]
v_mul_f32 v13,v22,v13
v_cvt_pk_bf16_f32 v206,v206,v207
v_mfma_f32_16x16x32_bf16 a[12:15],a[140:143],v[124:127],0
ds_read_b64_tr_b16 a[164:165],v1 offset:10432
v_cvt_pk_bf16_f32 v207,v12,v13
v_mfma_f32_16x16x32_bf16 a[12:15],a[136:139],v[120:123],a[12:15]
ds_read_b64_tr_b16 a[166:167],v1 offset:15552
ds_read_b64_tr_b16 a[168:169],v1 offset:20672
v_mfma_f32_16x16x32_bf16 a[12:15],a[132:135],v[116:119],a[12:15]
buffer_load_dwordx4 v10,s[72:75],s83 offen offset:3072 lds
v_mfma_f32_16x16x32_bf16 a[12:15],a[128:131],v[112:115],a[12:15]
ds_read_b64_tr_b16 a[170:171],v1 offset:25792
ds_read_b64_tr_b16 a[172:173],v1 offset:30912
v_mfma_f32_16x16x32_bf16 a[16:19],a[144:147],v[64:67],0
ds_read_b64_tr_b16 a[174:175],v1 offset:36032
v_accvgpr_read_b32 v212,a80
v_mfma_f32_16x16x32_bf16 a[16:19],a[148:151],v[68:71],a[16:19]
v_accvgpr_read_b32 v213,a81
v_mul_f32 v212,v22,v212
v_mfma_f32_16x16x32_bf16 a[16:19],a[152:155],v[72:75],a[16:19]
s_waitcnt lgkmcnt(6)
buffer_store_dwordx4 v[172:175],v6,s[36:39],s85 offen offset:1024 sc1
v_mfma_f32_16x16x32_bf16 a[16:19],a[156:159],v[76:79],a[16:19]
v_mul_f32 v213,v22,v213
v_accvgpr_read_b32 v214,a82
v_mfma_f32_16x16x32_bf16 a[20:23],a[156:159],v[92:95],0
v_accvgpr_read_b32 v215,a83
v_cvt_pk_bf16_f32 v212,v212,v213
v_mfma_f32_16x16x32_bf16 a[20:23],a[152:155],v[88:91],a[20:23]
v_mul_f32 v214,v22,v214
v_mul_f32 v215,v22,v215
v_mfma_f32_16x16x32_bf16 a[20:23],a[148:151],v[84:87],a[20:23]
v_cvt_pk_bf16_f32 v213,v214,v215
v_accvgpr_read_b32 v214,a84
v_mfma_f32_16x16x32_bf16 a[20:23],a[144:147],v[80:83],a[20:23]
v_accvgpr_read_b32 v215,a85
v_accvgpr_read_b32 v14,a86
v_mfma_f32_16x16x32_bf16 a[24:27],a[144:147],v[96:99],0
v_accvgpr_read_b32 v15,a87
v_mul_f32 v214,v22,v214
v_mfma_f32_16x16x32_bf16 a[24:27],a[148:151],v[100:103],a[24:27]
v_mul_f32 v215,v22,v215
v_mul_f32 v14,v22,v14
v_mfma_f32_16x16x32_bf16 a[24:27],a[152:155],v[104:107],a[24:27]
v_mul_f32 v15,v22,v15
v_cvt_pk_bf16_f32 v214,v214,v215
v_mfma_f32_16x16x32_bf16 a[24:27],a[156:159],v[108:111],a[24:27]
s_add_u32 m0,s82,132096
ds_read_b64_tr_b16 a[176:177],v2 offset:224
v_cvt_pk_bf16_f32 v215,v14,v15
v_mfma_f32_16x16x32_bf16 a[28:31],a[156:159],v[124:127],0
ds_read_b64_tr_b16 a[178:179],v2 offset:5344
ds_read_b64_tr_b16 a[180:181],v2 offset:10464
v_mfma_f32_16x16x32_bf16 a[28:31],a[152:155],v[120:123],a[28:31]
ds_read_b64_tr_b16 a[182:183],v2 offset:15584
v_accvgpr_read_b32 v220,a88
v_mfma_f32_16x16x32_bf16 a[28:31],a[148:151],v[116:119],a[28:31]
buffer_load_dwordx4 v11,s[72:75],s83 offen lds
v_mfma_f32_16x16x32_bf16 a[28:31],a[144:147],v[112:115],a[28:31]
ds_read_b64_tr_b16 a[184:185],v2 offset:20704
ds_read_b64_tr_b16 a[186:187],v2 offset:25824
v_mfma_f32_16x16x32_bf16 a[32:35],a[160:163],v[64:67],0
s_waitcnt lgkmcnt(6)
ds_read_b64_tr_b16 a[188:189],v2 offset:30944
v_accvgpr_read_b32 v221,a89
v_mfma_f32_16x16x32_bf16 a[32:35],a[164:167],v[68:71],a[32:35]
ds_read_b64_tr_b16 a[190:191],v2 offset:36064
v_bfe_u32 v24,v6,4,2
v_mfma_f32_16x16x32_bf16 a[32:35],a[168:171],v[72:75],a[32:35]
v_mul_f32 v220,v22,v220
v_mul_f32 v221,v22,v221
v_mfma_f32_16x16x32_bf16 a[32:35],a[172:175],v[76:79],a[32:35]
v_accvgpr_read_b32 v222,a90
v_accvgpr_read_b32 v223,a91
v_mfma_f32_16x16x32_bf16 a[36:39],a[172:175],v[92:95],0
buffer_store_dwordx4 v[180:183],v6,s[36:39],s85 offen offset:2048 sc1
v_mfma_f32_16x16x32_bf16 a[36:39],a[168:171],v[88:91],a[36:39]
v_min_u32 v24,2,v24
v_cvt_pk_bf16_f32 v220,v220,v221
v_mfma_f32_16x16x32_bf16 a[36:39],a[164:167],v[84:87],a[36:39]
v_mul_f32 v222,v22,v222
v_mul_f32 v223,v22,v223
v_mfma_f32_16x16x32_bf16 a[36:39],a[160:163],v[80:83],a[36:39]
v_add_u32 v24,s71,v24
v_cvt_pk_bf16_f32 v221,v222,v223
v_mfma_f32_16x16x32_bf16 a[40:43],a[160:163],v[96:99],0
v_add_u32 v24,1,v24
v_accvgpr_read_b32 v222,a92
v_mfma_f32_16x16x32_bf16 a[40:43],a[164:167],v[100:103],a[40:43]
v_accvgpr_read_b32 v223,a93
v_and_b32 v24,3,v24
v_mfma_f32_16x16x32_bf16 a[40:43],a[168:171],v[104:107],a[40:43]
v_accvgpr_read_b32 v16,a94
v_accvgpr_read_b32 v17,a95
v_mfma_f32_16x16x32_bf16 a[40:43],a[172:175],v[108:111],a[40:43]
v_mul_f32 v222,v22,v222
v_mul_f32 v223,v22,v223
v_mfma_f32_16x16x32_bf16 a[44:47],a[172:175],v[124:127],0
v_lshlrev_b32 v24,6,v24
v_mul_f32 v16,v22,v16
v_mfma_f32_16x16x32_bf16 a[44:47],a[168:171],v[120:123],a[44:47]
v_mul_f32 v17,v22,v17
v_cvt_pk_bf16_f32 v222,v222,v223
v_mfma_f32_16x16x32_bf16 a[44:47],a[164:167],v[116:119],a[44:47]
v_cvt_pk_bf16_f32 v223,v16,v17
v_max_f32 v23,0x00800000,v60
v_mfma_f32_16x16x32_bf16 a[44:47],a[160:163],v[112:115],a[44:47]
s_waitcnt lgkmcnt(0)
buffer_load_dwordx2 v[26:27],v24,s[64:67],s94 offen sc1
v_mfma_f32_16x16x32_bf16 a[48:51],a[176:179],v[64:67],0
ds_read_b64_tr_b16 a[128:129],v1 offset:256
ds_read_b64_tr_b16 a[130:131],v1 offset:5376
v_mfma_f32_16x16x32_bf16 a[48:51],a[180:183],v[68:71],a[48:51]
v_rcp_f32 v23,v23
v_mfma_f32_16x16x32_bf16 a[48:51],a[184:187],v[72:75],a[48:51]
ds_read_b64_tr_b16 a[132:133],v1 offset:10496
ds_read_b64_tr_b16 a[134:135],v1 offset:15616
v_mfma_f32_16x16x32_bf16 a[48:51],a[188:191],v[76:79],a[48:51]
v_accvgpr_read_b32 v228,a96
v_accvgpr_read_b32 v229,a97
v_mfma_f32_16x16x32_bf16 a[52:55],a[188:191],v[92:95],0
v_mul_f32 v228,v23,v228
v_mul_f32 v229,v23,v229
v_mfma_f32_16x16x32_bf16 a[52:55],a[184:187],v[88:91],a[52:55]
v_accvgpr_read_b32 v230,a98
v_accvgpr_read_b32 v231,a99
v_mfma_f32_16x16x32_bf16 a[52:55],a[180:183],v[84:87],a[52:55]
ds_read_b64_tr_b16 a[136:137],v1 offset:20736
ds_read_b64_tr_b16 a[138:139],v1 offset:25856
v_mfma_f32_16x16x32_bf16 a[52:55],a[176:179],v[80:83],a[52:55]
v_cvt_pk_bf16_f32 v228,v228,v229
v_mul_f32 v230,v23,v230
v_mfma_f32_16x16x32_bf16 a[56:59],a[176:179],v[96:99],0
buffer_store_dwordx4 v[188:191],v6,s[36:39],s85 offen offset:3072 sc1
v_mfma_f32_16x16x32_bf16 a[56:59],a[180:183],v[100:103],a[56:59]
v_mul_f32 v231,v23,v231
v_cvt_pk_bf16_f32 v229,v230,v231
v_mfma_f32_16x16x32_bf16 a[56:59],a[184:187],v[104:107],a[56:59]
ds_read_b64_tr_b16 a[140:141],v1 offset:30976
ds_read_b64_tr_b16 a[142:143],v1 offset:36096
v_mfma_f32_16x16x32_bf16 a[56:59],a[188:191],v[108:111],a[56:59]
v_accvgpr_read_b32 v230,a100
v_accvgpr_read_b32 v231,a101
v_mfma_f32_16x16x32_bf16 a[60:63],a[188:191],v[124:127],0
v_accvgpr_read_b32 v12,a102
v_accvgpr_read_b32 v13,a103
v_mfma_f32_16x16x32_bf16 a[60:63],a[184:187],v[120:123],a[60:63]
v_mul_f32 v230,v23,v230
v_mul_f32 v231,v23,v231
v_mfma_f32_16x16x32_bf16 a[60:63],a[180:183],v[116:119],a[60:63]
v_mul_f32 v12,v23,v12
v_mul_f32 v13,v23,v13
v_mfma_f32_16x16x32_bf16 a[60:63],a[176:179],v[112:115],a[60:63]
s_waitcnt lgkmcnt(2)
v_cvt_pk_bf16_f32 v230,v230,v231
v_cvt_pk_bf16_f32 v231,v12,v13
v_mfma_f32_16x16x32_bf16 a[64:67],a[128:131],v[64:67],0
v_accvgpr_read_b32 v236,a104
v_accvgpr_read_b32 v237,a105
v_mfma_f32_16x16x32_bf16 a[64:67],a[132:135],v[68:71],a[64:67]
v_mul_f32 v236,v23,v236
v_mul_f32 v237,v23,v237
v_mfma_f32_16x16x32_bf16 a[64:67],a[136:139],v[72:75],a[64:67]
s_waitcnt lgkmcnt(0)
v_accvgpr_read_b32 v238,a106
v_accvgpr_read_b32 v239,a107
v_mfma_f32_16x16x32_bf16 a[64:67],a[140:143],v[76:79],a[64:67]
v_cvt_pk_bf16_f32 v236,v236,v237
v_mul_f32 v238,v23,v238
v_mfma_f32_16x16x32_bf16 a[68:71],a[140:143],v[92:95],0
v_mul_f32 v239,v23,v239
v_cvt_pk_bf16_f32 v237,v238,v239
v_mfma_f32_16x16x32_bf16 a[68:71],a[136:139],v[88:91],a[68:71]
v_accvgpr_read_b32 v238,a108
v_accvgpr_read_b32 v239,a109
v_mfma_f32_16x16x32_bf16 a[68:71],a[132:135],v[84:87],a[68:71]
v_accvgpr_read_b32 v14,a110
v_accvgpr_read_b32 v15,a111
v_mfma_f32_16x16x32_bf16 a[68:71],a[128:131],v[80:83],a[68:71]
ds_read_b64_tr_b16 a[144:145],v2 offset:288
ds_read_b64_tr_b16 a[146:147],v2 offset:5408
v_mfma_f32_16x16x32_bf16 a[72:75],a[128:131],v[96:99],0
ds_read_b64_tr_b16 a[148:149],v2 offset:10528
ds_read_b64_tr_b16 a[150:151],v2 offset:15648
v_mfma_f32_16x16x32_bf16 a[72:75],a[132:135],v[100:103],a[72:75]
ds_read_b64_tr_b16 a[152:153],v2 offset:20768
ds_read_b64_tr_b16 a[154:155],v2 offset:25888
v_mfma_f32_16x16x32_bf16 a[72:75],a[136:139],v[104:107],a[72:75]
ds_read_b64_tr_b16 a[156:157],v2 offset:31008
ds_read_b64_tr_b16 a[158:159],v2 offset:36128
v_mfma_f32_16x16x32_bf16 a[72:75],a[140:143],v[108:111],a[72:75]
v_mfma_f32_16x16x32_bf16 a[76:79],a[140:143],v[124:127],0
v_mul_f32 v238,v23,v238
v_mul_f32 v239,v23,v239
v_mfma_f32_16x16x32_bf16 a[76:79],a[136:139],v[120:123],a[76:79]
v_mul_f32 v14,v23,v14
v_mul_f32 v15,v23,v15
v_mfma_f32_16x16x32_bf16 a[76:79],a[132:135],v[116:119],a[76:79]
v_cvt_pk_bf16_f32 v238,v238,v239
v_cvt_pk_bf16_f32 v239,v14,v15
v_mfma_f32_16x16x32_bf16 a[76:79],a[128:131],v[112:115],a[76:79]
s_waitcnt lgkmcnt(6)
v_accvgpr_read_b32 v244,a112
v_accvgpr_read_b32 v245,a113
v_mfma_f32_16x16x32_bf16 a[80:83],a[144:147],v[64:67],0
s_waitcnt lgkmcnt(4)
v_mul_f32 v244,v23,v244
v_mul_f32 v245,v23,v245
v_mfma_f32_16x16x32_bf16 a[80:83],a[148:151],v[68:71],a[80:83]
s_waitcnt lgkmcnt(2)
v_accvgpr_read_b32 v246,a114
)A"
R"A(v_accvgpr_read_b32 v247,a115
v_mfma_f32_16x16x32_bf16 a[80:83],a[152:155],v[72:75],a[80:83]
s_waitcnt lgkmcnt(0)
v_cvt_pk_bf16_f32 v244,v244,v245
v_mul_f32 v246,v23,v246
v_mfma_f32_16x16x32_bf16 a[80:83],a[156:159],v[76:79],a[80:83]
v_mul_f32 v247,v23,v247
v_cvt_pk_bf16_f32 v245,v246,v247
v_mfma_f32_16x16x32_bf16 a[84:87],a[156:159],v[92:95],0
v_accvgpr_read_b32 v246,a116
v_accvgpr_read_b32 v247,a117
v_mfma_f32_16x16x32_bf16 a[84:87],a[152:155],v[88:91],a[84:87]
v_accvgpr_read_b32 v16,a118
v_accvgpr_read_b32 v17,a119
v_mfma_f32_16x16x32_bf16 a[84:87],a[148:151],v[84:87],a[84:87]
v_add_u32 v1,40960,v1
v_mul_f32 v246,v23,v246
v_mfma_f32_16x16x32_bf16 a[84:87],a[144:147],v[80:83],a[84:87]
ds_read_b64_tr_b16 a[160:161],v1
ds_read_b64_tr_b16 a[162:163],v1 offset:5120
v_mfma_f32_16x16x32_bf16 a[88:91],a[144:147],v[96:99],0
ds_read_b64_tr_b16 a[164:165],v1 offset:10240
ds_read_b64_tr_b16 a[166:167],v1 offset:15360
v_mfma_f32_16x16x32_bf16 a[88:91],a[148:151],v[100:103],a[88:91]
ds_read_b64_tr_b16 a[168:169],v1 offset:20480
ds_read_b64_tr_b16 a[170:171],v1 offset:25600
v_mfma_f32_16x16x32_bf16 a[88:91],a[152:155],v[104:107],a[88:91]
ds_read_b64_tr_b16 a[172:173],v1 offset:30720
ds_read_b64_tr_b16 a[174:175],v1 offset:35840
v_mfma_f32_16x16x32_bf16 a[88:91],a[156:159],v[108:111],a[88:91]
v_accvgpr_read_b32 v252,a120
v_accvgpr_read_b32 v253,a121
v_cmp_nlt_f32 s[56:57],v48,s63
v_mul_f32 v252,v23,v252
v_mul_f32 v253,v23,v253
v_accvgpr_read_b32 v254,a122
v_accvgpr_read_b32 v255,a123
v_mfma_f32_16x16x32_bf16 a[92:95],a[156:159],v[124:127],0
v_cvt_pk_bf16_f32 v252,v252,v253
v_mul_f32 v254,v23,v254
v_mul_f32 v255,v23,v255
v_cmp_nlt_f32 s[58:59],v52,s63
s_nop 1
s_or_b64 s[56:57],s[56:57],s[58:59]
v_cvt_pk_bf16_f32 v253,v254,v255
v_mfma_f32_16x16x32_bf16 a[92:95],a[152:155],v[120:123],a[92:95]
v_mul_f32 v247,v23,v247
v_accvgpr_read_b32 v254,a124
v_accvgpr_read_b32 v255,a125
v_cmp_nlt_f32 s[58:59],v56,s63
s_nop 1
s_or_b64 s[56:57],s[56:57],s[58:59]
v_accvgpr_read_b32 v12,a126
v_accvgpr_read_b32 v13,a127
v_mul_f32 v254,v23,v254
v_mul_f32 v255,v23,v255
v_mfma_f32_16x16x32_bf16 a[92:95],a[148:151],v[116:119],a[92:95]
v_mul_f32 v16,v23,v16
v_mul_f32 v17,v23,v17
v_cvt_pk_bf16_f32 v246,v246,v247
v_cvt_pk_bf16_f32 v247,v16,v17
v_mul_f32 v12,v23,v12
v_mul_f32 v13,v23,v13
v_cvt_pk_bf16_f32 v254,v254,v255
v_cmp_nlt_f32 s[58:59],v60,s63
s_nop 1
s_or_b64 s[56:57],s[56:57],s[58:59]
v_cvt_pk_bf16_f32 v255,v12,v13
s_waitcnt vmcnt(1)
v_cmp_ne_u32 s[90:91],s68,v26
v_cmp_ne_u32 s[92:93],s69,v27
s_nop 0
s_or_b64 s[90:91],s[90:91],s[92:93]
s_cbranch_scc1 L_fr8_%=
L_fo8_%=:
v_mfma_f32_16x16x32_bf16 a[92:95],a[144:147],v[112:115],a[92:95]
s_cmp_lg_u64 s[56:57],0
s_cbranch_scc1 L_slow1_%=
L_sret1_%=:
v_add_u32 v2,40960,v2
s_waitcnt lgkmcnt(6)
v_mfma_f32_16x16x32_bf16 a[192:195],a[160:163],v[132:135],a[192:195]
s_waitcnt lgkmcnt(4)
buffer_store_dwordx4 v[196:199],v6,s[36:39],s86 offen sc1
v_mfma_f32_16x16x32_bf16 a[192:195],a[164:167],v[140:143],a[192:195]
s_cmp_ge_u32 0,s71
s_cselect_b32 s76,2,0
s_lshl_b32 s76,s76,16
s_waitcnt lgkmcnt(2)
v_mfma_f32_16x16x32_bf16 a[192:195],a[168:171],v[148:151],a[192:195]
s_add_u32 s77,s76,4096
s_add_u32 s78,s76,8192
s_add_u32 s79,s76,12288
s_max_i32 s76,s76,s18
s_waitcnt lgkmcnt(0)
v_mfma_f32_16x16x32_bf16 a[192:195],a[172:175],v[156:159],a[192:195]
s_max_i32 s77,s77,s19
ds_read_b64_tr_b16 a[176:177],v2 offset:32
ds_read_b64_tr_b16 a[178:179],v2 offset:5152
v_mfma_f32_16x16x32_bf16 a[196:199],a[172:175],v[188:191],a[196:199]
s_max_i32 s78,s78,s100
buffer_load_dwordx4 v[128:131],v6,s[28:31],s76 offen sc1
v_mfma_f32_16x16x32_bf16 a[196:199],a[168:171],v[180:183],a[196:199]
s_max_i32 s79,s79,s101
ds_read_b64_tr_b16 a[180:181],v2 offset:10272
ds_read_b64_tr_b16 a[182:183],v2 offset:15392
v_mfma_f32_16x16x32_bf16 a[196:199],a[164:167],v[172:175],a[196:199]
s_cmp_ge_u32 2,s71
s_cselect_b32 s80,6,4
ds_read_b64_tr_b16 a[184:185],v2 offset:20512
v_mfma_f32_16x16x32_bf16 a[196:199],a[160:163],v[164:167],a[196:199]
s_mul_i32 s80,s80,163840
s_add_u32 s80,s80,s81
ds_read_b64_tr_b16 a[186:187],v2 offset:25632
v_mfma_f32_16x16x32_bf16 a[200:203],a[160:163],v[196:199],a[200:203]
s_add_u32 s83,s80,20480
buffer_store_dwordx4 v[204:207],v6,s[36:39],s86 offen offset:1024 sc1
v_mfma_f32_16x16x32_bf16 a[200:203],a[164:167],v[204:207],a[200:203]
ds_read_b64_tr_b16 a[188:189],v2 offset:30752
ds_read_b64_tr_b16 a[190:191],v2 offset:35872
v_mfma_f32_16x16x32_bf16 a[200:203],a[168:171],v[212:215],a[200:203]
ds_read_b64_tr_b16 a[128:129],v1 offset:64
v_mfma_f32_16x16x32_bf16 a[200:203],a[172:175],v[220:223],a[200:203]
ds_read_b64_tr_b16 a[130:131],v1 offset:5184
v_mfma_f32_16x16x32_bf16 a[204:207],a[172:175],v[252:255],a[204:207]
buffer_load_dwordx4 v[160:163],v6,s[28:31],s76 offen offset:1024 sc1
v_mfma_f32_16x16x32_bf16 a[204:207],a[168:171],v[244:247],a[204:207]
ds_read_b64_tr_b16 a[132:133],v1 offset:10304
ds_read_b64_tr_b16 a[134:135],v1 offset:15424
v_mfma_f32_16x16x32_bf16 a[204:207],a[164:167],v[236:239],a[204:207]
ds_read_b64_tr_b16 a[136:137],v1 offset:20544
v_mfma_f32_16x16x32_bf16 a[204:207],a[160:163],v[228:231],a[204:207]
s_barrier
buffer_load_dwordx4 v[192:195],v6,s[28:31],s76 offen offset:2048 sc1
s_waitcnt lgkmcnt(7)
v_mfma_f32_16x16x32_bf16 a[208:211],a[176:179],v[132:135],a[208:211]
ds_read_b64_tr_b16 a[138:139],v1 offset:25664
ds_read_b64_tr_b16 a[140:141],v1 offset:30784
v_mfma_f32_16x16x32_bf16 a[208:211],a[180:183],v[140:143],a[208:211]
ds_read_b64_tr_b16 a[142:143],v1 offset:35904
v_mfma_f32_16x16x32_bf16 a[208:211],a[184:187],v[148:151],a[208:211]
s_add_u32 m0,s82,0
ds_read_b64_tr_b16 a[144:145],v2 offset:96
s_waitcnt lgkmcnt(8)
v_mfma_f32_16x16x32_bf16 a[208:211],a[188:191],v[156:159],a[208:211]
buffer_store_dwordx4 v[212:215],v6,s[36:39],s86 offen offset:2048 sc1
v_mfma_f32_16x16x32_bf16 a[212:215],a[188:191],v[188:191],a[212:215]
ds_read_b64_tr_b16 a[146:147],v2 offset:5216
ds_read_b64_tr_b16 a[148:149],v2 offset:10336
v_mfma_f32_16x16x32_bf16 a[212:215],a[184:187],v[180:183],a[212:215]
ds_read_b64_tr_b16 a[150:151],v2 offset:15456
v_mfma_f32_16x16x32_bf16 a[212:215],a[180:183],v[172:175],a[212:215]
ds_read_b64_tr_b16 a[152:153],v2 offset:20576
v_mfma_f32_16x16x32_bf16 a[212:215],a[176:179],v[164:167],a[212:215]
buffer_load_dwordx4 v[224:227],v6,s[28:31],s76 offen offset:3072 sc1
v_mfma_f32_16x16x32_bf16 a[216:219],a[176:179],v[196:199],a[216:219]
ds_read_b64_tr_b16 a[154:155],v2 offset:25696
ds_read_b64_tr_b16 a[156:157],v2 offset:30816
v_mfma_f32_16x16x32_bf16 a[216:219],a[180:183],v[204:207],a[216:219]
ds_read_b64_tr_b16 a[158:159],v2 offset:35936
v_mfma_f32_16x16x32_bf16 a[216:219],a[184:187],v[212:215],a[216:219]
ds_read_b64_tr_b16 a[160:161],v1 offset:128
v_mfma_f32_16x16x32_bf16 a[216:219],a[188:191],v[220:223],a[216:219]
buffer_load_dwordx4 v7,s[72:75],s80 offen lds
v_mfma_f32_16x16x32_bf16 a[220:223],a[188:191],v[252:255],a[220:223]
ds_read_b64_tr_b16 a[162:163],v1 offset:5248
ds_read_b64_tr_b16 a[164:165],v1 offset:10368
v_mfma_f32_16x16x32_bf16 a[220:223],a[184:187],v[244:247],a[220:223]
ds_read_b64_tr_b16 a[166:167],v1 offset:15488
v_mfma_f32_16x16x32_bf16 a[220:223],a[180:183],v[236:239],a[220:223]
ds_read_b64_tr_b16 a[168:169],v1 offset:20608
v_mfma_f32_16x16x32_bf16 a[220:223],a[176:179],v[228:231],a[220:223]
s_waitcnt lgkmcnt(8)
buffer_load_dwordx4 v[136:139],v6,s[28:31],s77 offen sc1
v_mfma_f32_16x16x32_bf16 a[224:227],a[128:131],v[132:135],a[224:227]
ds_read_b64_tr_b16 a[170:171],v1 offset:25728
ds_read_b64_tr_b16 a[172:173],v1 offset:30848
v_mfma_f32_16x16x32_bf16 a[224:227],a[132:135],v[140:143],a[224:227]
ds_read_b64_tr_b16 a[174:175],v1 offset:35968
v_mfma_f32_16x16x32_bf16 a[224:227],a[136:139],v[148:151],a[224:227]
ds_read_b64_tr_b16 a[176:177],v2 offset:160
v_mfma_f32_16x16x32_bf16 a[224:227],a[140:143],v[156:159],a[224:227]
buffer_load_dwordx4 v[168:171],v6,s[28:31],s77 offen offset:1024 sc1
v_mfma_f32_16x16x32_bf16 a[228:231],a[140:143],v[188:191],a[228:231]
ds_read_b64_tr_b16 a[178:179],v2 offset:5280
ds_read_b64_tr_b16 a[180:181],v2 offset:10400
v_mfma_f32_16x16x32_bf16 a[228:231],a[136:139],v[180:183],a[228:231]
ds_read_b64_tr_b16 a[182:183],v2 offset:15520
v_mfma_f32_16x16x32_bf16 a[228:231],a[132:135],v[172:175],a[228:231]
ds_read_b64_tr_b16 a[184:185],v2 offset:20640
v_mfma_f32_16x16x32_bf16 a[228:231],a[128:131],v[164:167],a[228:231]
buffer_load_dwordx4 v8,s[72:75],s80 offen offset:1024 lds
v_mfma_f32_16x16x32_bf16 a[232:235],a[128:131],v[196:199],a[232:235]
ds_read_b64_tr_b16 a[186:187],v2 offset:25760
ds_read_b64_tr_b16 a[188:189],v2 offset:30880
v_mfma_f32_16x16x32_bf16 a[232:235],a[132:135],v[204:207],a[232:235]
ds_read_b64_tr_b16 a[190:191],v2 offset:36000
v_mfma_f32_16x16x32_bf16 a[232:235],a[136:139],v[212:215],a[232:235]
v_mfma_f32_16x16x32_bf16 a[232:235],a[140:143],v[220:223],a[232:235]
buffer_store_dwordx4 v[220:223],v6,s[36:39],s86 offen offset:3072 sc1
v_mfma_f32_16x16x32_bf16 a[236:239],a[140:143],v[252:255],a[236:239]
ds_read_b64_tr_b16 a[140:141],v1 offset:30912
ds_read_b64_tr_b16 a[142:143],v1 offset:36032
v_mfma_f32_16x16x32_bf16 a[236:239],a[136:139],v[244:247],a[236:239]
ds_read_b64_tr_b16 a[136:137],v1 offset:20672
v_mfma_f32_16x16x32_bf16 a[236:239],a[132:135],v[236:239],a[236:239]
ds_read_b64_tr_b16 a[132:133],v1 offset:10432
v_mfma_f32_16x16x32_bf16 a[236:239],a[128:131],v[228:231],a[236:239]
buffer_load_dwordx4 v[200:203],v6,s[28:31],s77 offen offset:2048 sc1
v_mfma_f32_16x16x32_bf16 a[240:243],a[144:147],v[132:135],a[240:243]
ds_read_b64_tr_b16 a[128:129],v1 offset:192
ds_read_b64_tr_b16 a[130:131],v1 offset:5312
v_mfma_f32_16x16x32_bf16 a[240:243],a[148:151],v[140:143],a[240:243]
s_waitcnt lgkmcnt(6)
ds_read_b64_tr_b16 a[134:135],v1 offset:15552
v_mfma_f32_16x16x32_bf16 a[240:243],a[152:155],v[148:151],a[240:243]
ds_read_b64_tr_b16 a[138:139],v1 offset:25792
v_mfma_f32_16x16x32_bf16 a[240:243],a[156:159],v[156:159],a[240:243]
buffer_load_dwordx4 v[232:235],v6,s[28:31],s77 offen offset:3072 sc1
v_mfma_f32_16x16x32_bf16 a[244:247],a[156:159],v[188:191],a[244:247]
v_mfma_f32_16x16x32_bf16 a[244:247],a[152:155],v[180:183],a[244:247]
v_mfma_f32_16x16x32_bf16 a[244:247],a[148:151],v[172:175],a[244:247]
v_mfma_f32_16x16x32_bf16 a[244:247],a[144:147],v[164:167],a[244:247]
buffer_load_dwordx4 v9,s[72:75],s80 offen offset:2048 lds
v_mfma_f32_16x16x32_bf16 a[248:251],a[144:147],v[196:199],a[248:251]
v_mfma_f32_16x16x32_bf16 a[248:251],a[148:151],v[204:207],a[248:251]
v_mfma_f32_16x16x32_bf16 a[248:251],a[152:155],v[212:215],a[248:251]
v_mfma_f32_16x16x32_bf16 a[248:251],a[156:159],v[220:223],a[248:251]
buffer_load_dwordx4 v[144:147],v6,s[28:31],s78 offen sc1
v_mfma_f32_16x16x32_bf16 a[252:255],a[156:159],v[252:255],a[252:255]
ds_read_b64_tr_b16 a[156:157],v2 offset:30944
ds_read_b64_tr_b16 a[158:159],v2 offset:36064
v_mfma_f32_16x16x32_bf16 a[252:255],a[152:155],v[244:247],a[252:255]
ds_read_b64_tr_b16 a[152:153],v2 offset:20704
v_mfma_f32_16x16x32_bf16 a[252:255],a[148:151],v[236:239],a[252:255]
ds_read_b64_tr_b16 a[148:149],v2 offset:10464
v_mfma_f32_16x16x32_bf16 a[252:255],a[144:147],v[228:231],a[252:255]
buffer_store_dwordx4 v[228:231],v6,s[36:39],s87 offen sc1
v_mfma_f32_16x16x32_bf16 a[0:3],a[160:163],v[132:135],a[0:3]
ds_read_b64_tr_b16 a[144:145],v2 offset:224
ds_read_b64_tr_b16 a[146:147],v2 offset:5344
v_mfma_f32_16x16x32_bf16 a[0:3],a[164:167],v[140:143],a[0:3]
ds_read_b64_tr_b16 a[150:151],v2 offset:15584
v_mfma_f32_16x16x32_bf16 a[0:3],a[168:171],v[148:151],a[0:3]
ds_read_b64_tr_b16 a[154:155],v2 offset:25824
v_mfma_f32_16x16x32_bf16 a[0:3],a[172:175],v[156:159],a[0:3]
buffer_load_dwordx4 v[176:179],v6,s[28:31],s78 offen offset:1024 sc1
v_mfma_f32_16x16x32_bf16 a[4:7],a[172:175],v[188:191],a[4:7]
v_mfma_f32_16x16x32_bf16 a[4:7],a[168:171],v[180:183],a[4:7]
v_mfma_f32_16x16x32_bf16 a[4:7],a[164:167],v[172:175],a[4:7]
v_mfma_f32_16x16x32_bf16 a[4:7],a[160:163],v[164:167],a[4:7]
buffer_load_dwordx4 v10,s[72:75],s80 offen offset:3072 lds
v_mfma_f32_16x16x32_bf16 a[8:11],a[160:163],v[196:199],a[8:11]
v_mfma_f32_16x16x32_bf16 a[8:11],a[164:167],v[204:207],a[8:11]
)A"
R"A(v_mfma_f32_16x16x32_bf16 a[8:11],a[168:171],v[212:215],a[8:11]
v_mfma_f32_16x16x32_bf16 a[8:11],a[172:175],v[220:223],a[8:11]
buffer_load_dwordx4 v[208:211],v6,s[28:31],s78 offen offset:2048 sc1
v_mfma_f32_16x16x32_bf16 a[12:15],a[172:175],v[252:255],a[12:15]
ds_read_b64_tr_b16 a[172:173],v1 offset:30976
ds_read_b64_tr_b16 a[174:175],v1 offset:36096
v_mfma_f32_16x16x32_bf16 a[12:15],a[168:171],v[244:247],a[12:15]
ds_read_b64_tr_b16 a[168:169],v1 offset:20736
v_mfma_f32_16x16x32_bf16 a[12:15],a[164:167],v[236:239],a[12:15]
s_add_u32 m0,s82,4096
ds_read_b64_tr_b16 a[164:165],v1 offset:10496
v_mfma_f32_16x16x32_bf16 a[12:15],a[160:163],v[228:231],a[12:15]
buffer_load_dwordx4 v[240:243],v6,s[28:31],s78 offen offset:3072 sc1
v_mfma_f32_16x16x32_bf16 a[16:19],a[176:179],v[132:135],a[16:19]
ds_read_b64_tr_b16 a[160:161],v1 offset:256
ds_read_b64_tr_b16 a[162:163],v1 offset:5376
v_mfma_f32_16x16x32_bf16 a[16:19],a[180:183],v[140:143],a[16:19]
ds_read_b64_tr_b16 a[166:167],v1 offset:15616
v_mfma_f32_16x16x32_bf16 a[16:19],a[184:187],v[148:151],a[16:19]
ds_read_b64_tr_b16 a[170:171],v1 offset:25856
v_add_u32 v1,40960,v1
v_mfma_f32_16x16x32_bf16 a[16:19],a[188:191],v[156:159],a[16:19]
buffer_store_dwordx4 v[236:239],v6,s[36:39],s87 offen offset:1024 sc1
v_mfma_f32_16x16x32_bf16 a[20:23],a[188:191],v[188:191],a[20:23]
ds_read_b64_tr_b16 a[96:97],v1 offset:128
ds_read_b64_tr_b16 a[98:99],v1 offset:5248
v_mfma_f32_16x16x32_bf16 a[20:23],a[184:187],v[180:183],a[20:23]
ds_read_b64_tr_b16 a[100:101],v1 offset:10368
v_mfma_f32_16x16x32_bf16 a[20:23],a[180:183],v[172:175],a[20:23]
ds_read_b64_tr_b16 a[102:103],v1 offset:15488
v_mfma_f32_16x16x32_bf16 a[20:23],a[176:179],v[164:167],a[20:23]
buffer_load_dwordx4 v11,s[72:75],s80 offen lds
v_mfma_f32_16x16x32_bf16 a[24:27],a[176:179],v[196:199],a[24:27]
ds_read_b64_tr_b16 a[104:105],v1 offset:20608
ds_read_b64_tr_b16 a[106:107],v1 offset:25728
v_mfma_f32_16x16x32_bf16 a[24:27],a[180:183],v[204:207],a[24:27]
ds_read_b64_tr_b16 a[108:109],v1 offset:30848
v_mfma_f32_16x16x32_bf16 a[24:27],a[184:187],v[212:215],a[24:27]
ds_read_b64_tr_b16 a[110:111],v1 offset:35968
v_mfma_f32_16x16x32_bf16 a[24:27],a[188:191],v[220:223],a[24:27]
buffer_load_dwordx4 v[152:155],v6,s[28:31],s79 offen sc1
v_mfma_f32_16x16x32_bf16 a[28:31],a[188:191],v[252:255],a[28:31]
ds_read_b64_tr_b16 a[188:189],v2 offset:31008
ds_read_b64_tr_b16 a[190:191],v2 offset:36128
v_mfma_f32_16x16x32_bf16 a[28:31],a[184:187],v[244:247],a[28:31]
s_add_u32 m0,s82,5120
ds_read_b64_tr_b16 a[184:185],v2 offset:20768
v_mfma_f32_16x16x32_bf16 a[28:31],a[180:183],v[236:239],a[28:31]
ds_read_b64_tr_b16 a[180:181],v2 offset:10528
v_mfma_f32_16x16x32_bf16 a[28:31],a[176:179],v[228:231],a[28:31]
s_waitcnt lgkmcnt(8)
buffer_load_dwordx4 v[184:187],v6,s[28:31],s79 offen offset:1024 sc1
v_mfma_f32_16x16x32_bf16 a[32:35],a[128:131],v[132:135],a[32:35]
ds_read_b64_tr_b16 a[176:177],v2 offset:288
ds_read_b64_tr_b16 a[178:179],v2 offset:5408
v_mfma_f32_16x16x32_bf16 a[32:35],a[132:135],v[140:143],a[32:35]
ds_read_b64_tr_b16 a[182:183],v2 offset:15648
v_mfma_f32_16x16x32_bf16 a[32:35],a[136:139],v[148:151],a[32:35]
ds_read_b64_tr_b16 a[186:187],v2 offset:25888
v_add_u32 v2,40960,v2
v_mfma_f32_16x16x32_bf16 a[32:35],a[140:143],v[156:159],a[32:35]
buffer_load_dwordx4 v7,s[72:75],s83 offen lds
v_mfma_f32_16x16x32_bf16 a[36:39],a[140:143],v[188:191],a[36:39]
ds_read_b64_tr_b16 a[112:113],v2 offset:160
ds_read_b64_tr_b16 a[114:115],v2 offset:5280
v_mfma_f32_16x16x32_bf16 a[36:39],a[136:139],v[180:183],a[36:39]
ds_read_b64_tr_b16 a[116:117],v2 offset:10400
v_mfma_f32_16x16x32_bf16 a[36:39],a[132:135],v[172:175],a[36:39]
ds_read_b64_tr_b16 a[118:119],v2 offset:15520
v_mfma_f32_16x16x32_bf16 a[36:39],a[128:131],v[164:167],a[36:39]
buffer_load_dwordx4 v[216:219],v6,s[28:31],s79 offen offset:2048 sc1
v_mfma_f32_16x16x32_bf16 a[40:43],a[128:131],v[196:199],a[40:43]
ds_read_b64_tr_b16 a[120:121],v2 offset:20640
ds_read_b64_tr_b16 a[122:123],v2 offset:25760
v_mfma_f32_16x16x32_bf16 a[40:43],a[132:135],v[204:207],a[40:43]
ds_read_b64_tr_b16 a[124:125],v2 offset:30880
v_mfma_f32_16x16x32_bf16 a[40:43],a[136:139],v[212:215],a[40:43]
ds_read_b64_tr_b16 a[126:127],v2 offset:36000
v_mfma_f32_16x16x32_bf16 a[40:43],a[140:143],v[220:223],a[40:43]
buffer_load_dwordx4 v[248:251],v6,s[28:31],s79 offen offset:3072 sc1
v_mfma_f32_16x16x32_bf16 a[44:47],a[140:143],v[252:255],a[44:47]
ds_read_b64_tr_b16 a[140:141],v1 offset:30720
ds_read_b64_tr_b16 a[142:143],v1 offset:35840
v_mfma_f32_16x16x32_bf16 a[44:47],a[136:139],v[244:247],a[44:47]
ds_read_b64_tr_b16 a[136:137],v1 offset:20480
v_mfma_f32_16x16x32_bf16 a[44:47],a[132:135],v[236:239],a[44:47]
ds_read_b64_tr_b16 a[132:133],v1 offset:10240
v_mfma_f32_16x16x32_bf16 a[44:47],a[128:131],v[228:231],a[44:47]
buffer_store_dwordx4 v[244:247],v6,s[36:39],s87 offen offset:2048 sc1
v_mfma_f32_16x16x32_bf16 a[48:51],a[144:147],v[132:135],a[48:51]
ds_read_b64_tr_b16 a[128:129],v1
ds_read_b64_tr_b16 a[130:131],v1 offset:5120
v_mfma_f32_16x16x32_bf16 a[48:51],a[148:151],v[140:143],a[48:51]
ds_read_b64_tr_b16 a[134:135],v1 offset:15360
v_mfma_f32_16x16x32_bf16 a[48:51],a[152:155],v[148:151],a[48:51]
ds_read_b64_tr_b16 a[138:139],v1 offset:25600
v_mfma_f32_16x16x32_bf16 a[48:51],a[156:159],v[156:159],a[48:51]
buffer_load_dwordx4 v8,s[72:75],s83 offen offset:1024 lds
v_mfma_f32_16x16x32_bf16 a[52:55],a[156:159],v[188:191],a[52:55]
s_cmp_ge_u32 1,s71
s_cselect_b32 s76,4,2
s_lshl_b32 s76,s76,16
v_mfma_f32_16x16x32_bf16 a[52:55],a[152:155],v[180:183],a[52:55]
s_add_u32 s77,s76,4096
s_add_u32 s78,s76,8192
s_add_u32 s79,s76,12288
v_mfma_f32_16x16x32_bf16 a[52:55],a[148:151],v[172:175],a[52:55]
s_max_i32 s76,s76,s18
s_max_i32 s77,s77,s19
s_max_i32 s78,s78,s100
v_mfma_f32_16x16x32_bf16 a[52:55],a[144:147],v[164:167],a[52:55]
s_max_i32 s79,s79,s101
buffer_store_dwordx4 v[252:255],v6,s[36:39],s87 offen offset:3072 sc1
v_mfma_f32_16x16x32_bf16 a[56:59],a[144:147],v[196:199],a[56:59]
v_mfma_f32_16x16x32_bf16 a[56:59],a[148:151],v[204:207],a[56:59]
v_mfma_f32_16x16x32_bf16 a[56:59],a[152:155],v[212:215],a[56:59]
v_mfma_f32_16x16x32_bf16 a[56:59],a[156:159],v[220:223],a[56:59]
v_mfma_f32_16x16x32_bf16 a[60:63],a[156:159],v[252:255],a[60:63]
ds_read_b64_tr_b16 a[156:157],v2 offset:30752
ds_read_b64_tr_b16 a[158:159],v2 offset:35872
v_mfma_f32_16x16x32_bf16 a[60:63],a[152:155],v[244:247],a[60:63]
ds_read_b64_tr_b16 a[152:153],v2 offset:20512
v_mfma_f32_16x16x32_bf16 a[60:63],a[148:151],v[236:239],a[60:63]
buffer_load_dwordx4 v9,s[72:75],s83 offen offset:2048 lds
v_mfma_f32_16x16x32_bf16 a[60:63],a[144:147],v[228:231],a[60:63]
ds_read_b64_tr_b16 a[144:145],v2 offset:32
ds_read_b64_tr_b16 a[146:147],v2 offset:5152
v_mfma_f32_16x16x32_bf16 a[64:67],a[160:163],v[132:135],a[64:67]
ds_read_b64_tr_b16 a[148:149],v2 offset:10272
v_mfma_f32_16x16x32_bf16 a[64:67],a[164:167],v[140:143],a[64:67]
ds_read_b64_tr_b16 a[150:151],v2 offset:15392
v_mfma_f32_16x16x32_bf16 a[64:67],a[168:171],v[148:151],a[64:67]
ds_read_b64_tr_b16 a[154:155],v2 offset:25632
v_mfma_f32_16x16x32_bf16 a[64:67],a[172:175],v[156:159],a[64:67]
v_mfma_f32_16x16x32_bf16 a[68:71],a[172:175],v[188:191],a[68:71]
v_mfma_f32_16x16x32_bf16 a[68:71],a[168:171],v[180:183],a[68:71]
v_mfma_f32_16x16x32_bf16 a[68:71],a[164:167],v[172:175],a[68:71]
v_mfma_f32_16x16x32_bf16 a[68:71],a[160:163],v[164:167],a[68:71]
v_mfma_f32_16x16x32_bf16 a[72:75],a[160:163],v[196:199],a[72:75]
v_mfma_f32_16x16x32_bf16 a[72:75],a[164:167],v[204:207],a[72:75]
v_mfma_f32_16x16x32_bf16 a[72:75],a[168:171],v[212:215],a[72:75]
v_mfma_f32_16x16x32_bf16 a[72:75],a[172:175],v[220:223],a[72:75]
buffer_load_dwordx4 v[64:67],v6,s[28:31],s76 offen sc1
v_mfma_f32_16x16x32_bf16 a[76:79],a[172:175],v[252:255],a[76:79]
ds_read_b64_tr_b16 a[172:173],v1 offset:30784
ds_read_b64_tr_b16 a[174:175],v1 offset:35904
v_mfma_f32_16x16x32_bf16 a[76:79],a[168:171],v[244:247],a[76:79]
ds_read_b64_tr_b16 a[168:169],v1 offset:20544
v_mfma_f32_16x16x32_bf16 a[76:79],a[164:167],v[236:239],a[76:79]
ds_read_b64_tr_b16 a[164:165],v1 offset:10304
v_mfma_f32_16x16x32_bf16 a[76:79],a[160:163],v[228:231],a[76:79]
s_waitcnt lgkmcnt(4)
buffer_load_dwordx4 v10,s[72:75],s83 offen offset:3072 lds
v_mfma_f32_16x16x32_bf16 a[80:83],a[176:179],v[132:135],a[80:83]
ds_read_b64_tr_b16 a[160:161],v1 offset:64
ds_read_b64_tr_b16 a[162:163],v1 offset:5184
v_mfma_f32_16x16x32_bf16 a[80:83],a[180:183],v[140:143],a[80:83]
ds_read_b64_tr_b16 a[166:167],v1 offset:15424
v_mfma_f32_16x16x32_bf16 a[80:83],a[184:187],v[148:151],a[80:83]
ds_read_b64_tr_b16 a[170:171],v1 offset:25664
v_mfma_f32_16x16x32_bf16 a[80:83],a[188:191],v[156:159],a[80:83]
buffer_load_dwordx4 v[68:71],v6,s[28:31],s76 offen offset:1024 sc1
v_mfma_f32_16x16x32_bf16 a[84:87],a[188:191],v[188:191],a[84:87]
v_mfma_f32_16x16x32_bf16 a[84:87],a[184:187],v[180:183],a[84:87]
v_mfma_f32_16x16x32_bf16 a[84:87],a[180:183],v[172:175],a[84:87]
v_mfma_f32_16x16x32_bf16 a[84:87],a[176:179],v[164:167],a[84:87]
v_mfma_f32_16x16x32_bf16 a[88:91],a[176:179],v[196:199],a[88:91]
v_mfma_f32_16x16x32_bf16 a[88:91],a[180:183],v[204:207],a[88:91]
s_add_u32 m0,s82,9216
v_mfma_f32_16x16x32_bf16 a[88:91],a[184:187],v[212:215],a[88:91]
v_mfma_f32_16x16x32_bf16 a[88:91],a[188:191],v[220:223],a[88:91]
buffer_load_dwordx4 v[72:75],v6,s[28:31],s76 offen offset:2048 sc1
v_mfma_f32_16x16x32_bf16 a[92:95],a[188:191],v[252:255],a[92:95]
ds_read_b64_tr_b16 a[188:189],v2 offset:30816
ds_read_b64_tr_b16 a[190:191],v2 offset:35936
v_mfma_f32_16x16x32_bf16 a[92:95],a[184:187],v[244:247],a[92:95]
ds_read_b64_tr_b16 a[184:185],v2 offset:20576
v_mfma_f32_16x16x32_bf16 a[92:95],a[180:183],v[236:239],a[92:95]
s_cmp_ge_u32 0,s71
s_cselect_b32 s80,3,1
s_mul_i32 s80,s80,163840
v_mfma_f32_16x16x32_bf16 a[92:95],a[176:179],v[228:231],a[92:95]
s_add_u32 s80,s80,s81
buffer_load_dwordx4 v11,s[72:75],s83 offen lds
s_waitcnt vmcnt(14)
v_mfma_f32_16x16x32_bf16 a[192:195],a[128:131],v[128:131],a[192:195]
s_add_u32 s83,s80,20480
ds_read_b64_tr_b16 a[176:177],v2 offset:96
ds_read_b64_tr_b16 a[178:179],v2 offset:5216
v_mfma_f32_16x16x32_bf16 a[192:195],a[132:135],v[160:163],a[192:195]
ds_read_b64_tr_b16 a[180:181],v2 offset:10336
v_mfma_f32_16x16x32_bf16 a[192:195],a[136:139],v[192:195],a[192:195]
ds_read_b64_tr_b16 a[182:183],v2 offset:15456
v_mfma_f32_16x16x32_bf16 a[192:195],a[140:143],v[224:227],a[192:195]
buffer_load_dwordx4 v[76:79],v6,s[28:31],s76 offen offset:3072 sc1
v_mfma_f32_16x16x32_bf16 a[196:199],a[140:143],v[232:235],a[196:199]
ds_read_b64_tr_b16 a[186:187],v2 offset:25696
v_mfma_f32_16x16x32_bf16 a[196:199],a[136:139],v[200:203],a[196:199]
v_mfma_f32_16x16x32_bf16 a[196:199],a[132:135],v[168:171],a[196:199]
v_mfma_f32_16x16x32_bf16 a[196:199],a[128:131],v[136:139],a[196:199]
v_mfma_f32_16x16x32_bf16 a[200:203],a[128:131],v[144:147],a[200:203]
v_mfma_f32_16x16x32_bf16 a[200:203],a[132:135],v[176:179],a[200:203]
s_lshl_b32 s95,s42,2
s_add_u32 s95,s95,s40
s_lshl_b32 s95,s95,4
v_mfma_f32_16x16x32_bf16 a[200:203],a[136:139],v[208:211],a[200:203]
v_mov_b32 v18,s68
v_mov_b32 v19,s69
v_mfma_f32_16x16x32_bf16 a[200:203],a[140:143],v[240:243],a[200:203]
s_waitcnt vmcnt(10)
buffer_load_dwordx4 v[80:83],v6,s[28:31],s77 offen sc1
v_mfma_f32_16x16x32_bf16 a[204:207],a[140:143],v[248:251],a[204:207]
ds_read_b64_tr_b16 a[140:141],v1 offset:30912
ds_read_b64_tr_b16 a[142:143],v1 offset:36032
v_mfma_f32_16x16x32_bf16 a[204:207],a[136:139],v[216:219],a[204:207]
ds_read_b64_tr_b16 a[136:137],v1 offset:20672
v_mfma_f32_16x16x32_bf16 a[204:207],a[132:135],v[184:187],a[204:207]
ds_read_b64_tr_b16 a[132:133],v1 offset:10432
v_mfma_f32_16x16x32_bf16 a[204:207],a[128:131],v[152:155],a[204:207]
s_barrier
ds_read_b64_tr_b16 a[128:129],v1 offset:192
ds_read_b64_tr_b16 a[130:131],v1 offset:5312
v_mfma_f32_16x16x32_bf16 a[208:211],a[144:147],v[128:131],a[208:211]
s_waitcnt vmcnt(8)
s_mov_b64 exec,1
buffer_store_dwordx2 v[18:19],v6,s[64:67],s95 offen offset:1024 sc1
s_mov_b64 exec,-1
v_mfma_f32_16x16x32_bf16 a[208:211],a[148:151],v[160:163],a[208:211]
ds_read_b64_tr_b16 a[134:135],v1 offset:15552
)A"
R"A(ds_read_b64_tr_b16 a[138:139],v1 offset:25792
v_mfma_f32_16x16x32_bf16 a[208:211],a[152:155],v[192:195],a[208:211]
s_add_u32 m0,s82,40960
v_mfma_f32_16x16x32_bf16 a[208:211],a[156:159],v[224:227],a[208:211]
v_mfma_f32_16x16x32_bf16 a[212:215],a[156:159],v[232:235],a[212:215]
buffer_load_dwordx4 v[84:87],v6,s[28:31],s77 offen offset:1024 sc1
v_mfma_f32_16x16x32_bf16 a[212:215],a[152:155],v[200:203],a[212:215]
v_mfma_f32_16x16x32_bf16 a[212:215],a[148:151],v[168:171],a[212:215]
v_mfma_f32_16x16x32_bf16 a[212:215],a[144:147],v[136:139],a[212:215]
v_mfma_f32_16x16x32_bf16 a[216:219],a[144:147],v[144:147],a[216:219]
buffer_load_dwordx4 v7,s[72:75],s80 offen lds
v_mfma_f32_16x16x32_bf16 a[216:219],a[148:151],v[176:179],a[216:219]
v_mfma_f32_16x16x32_bf16 a[216:219],a[152:155],v[208:211],a[216:219]
v_mfma_f32_16x16x32_bf16 a[216:219],a[156:159],v[240:243],a[216:219]
v_mfma_f32_16x16x32_bf16 a[220:223],a[156:159],v[248:251],a[220:223]
buffer_load_dwordx4 v[88:91],v6,s[28:31],s77 offen offset:2048 sc1
v_mfma_f32_16x16x32_bf16 a[220:223],a[152:155],v[216:219],a[220:223]
ds_read_b64_tr_b16 a[152:153],v2 offset:20704
ds_read_b64_tr_b16 a[154:155],v2 offset:25824
v_mfma_f32_16x16x32_bf16 a[220:223],a[148:151],v[184:187],a[220:223]
ds_read_b64_tr_b16 a[148:149],v2 offset:10464
v_mfma_f32_16x16x32_bf16 a[220:223],a[144:147],v[152:155],a[220:223]
s_waitcnt lgkmcnt(3)
ds_read_b64_tr_b16 a[144:145],v2 offset:224
v_mfma_f32_16x16x32_bf16 a[224:227],a[160:163],v[128:131],a[224:227]
ds_read_b64_tr_b16 a[146:147],v2 offset:5344
v_mfma_f32_16x16x32_bf16 a[224:227],a[164:167],v[160:163],a[224:227]
ds_read_b64_tr_b16 a[150:151],v2 offset:15584
ds_read_b64_tr_b16 a[156:157],v2 offset:30944
v_mfma_f32_16x16x32_bf16 a[224:227],a[168:171],v[192:195],a[224:227]
buffer_load_dwordx4 v[92:95],v6,s[28:31],s77 offen offset:3072 sc1
v_mfma_f32_16x16x32_bf16 a[224:227],a[172:175],v[224:227],a[224:227]
ds_read_b64_tr_b16 a[158:159],v2 offset:36064
v_mfma_f32_16x16x32_bf16 a[228:231],a[172:175],v[232:235],a[228:231]
v_mfma_f32_16x16x32_bf16 a[228:231],a[168:171],v[200:203],a[228:231]
v_mfma_f32_16x16x32_bf16 a[228:231],a[164:167],v[168:171],a[228:231]
buffer_load_dwordx4 v8,s[72:75],s80 offen offset:1024 lds
v_mfma_f32_16x16x32_bf16 a[228:231],a[160:163],v[136:139],a[228:231]
v_mfma_f32_16x16x32_bf16 a[232:235],a[160:163],v[144:147],a[232:235]
v_mfma_f32_16x16x32_bf16 a[232:235],a[164:167],v[176:179],a[232:235]
v_mfma_f32_16x16x32_bf16 a[232:235],a[168:171],v[208:211],a[232:235]
buffer_load_dwordx4 v[96:99],v6,s[28:31],s78 offen sc1
v_mfma_f32_16x16x32_bf16 a[232:235],a[172:175],v[240:243],a[232:235]
v_mfma_f32_16x16x32_bf16 a[236:239],a[172:175],v[248:251],a[236:239]
ds_read_b64_tr_b16 a[172:173],v1 offset:30976
ds_read_b64_tr_b16 a[174:175],v1 offset:36096
v_mfma_f32_16x16x32_bf16 a[236:239],a[168:171],v[216:219],a[236:239]
ds_read_b64_tr_b16 a[168:169],v1 offset:20736
v_mfma_f32_16x16x32_bf16 a[236:239],a[164:167],v[184:187],a[236:239]
ds_read_b64_tr_b16 a[164:165],v1 offset:10496
v_mfma_f32_16x16x32_bf16 a[236:239],a[160:163],v[152:155],a[236:239]
ds_read_b64_tr_b16 a[160:161],v1 offset:256
v_mfma_f32_16x16x32_bf16 a[240:243],a[176:179],v[128:131],a[240:243]
ds_read_b64_tr_b16 a[162:163],v1 offset:5376
ds_read_b64_tr_b16 a[166:167],v1 offset:15616
v_mfma_f32_16x16x32_bf16 a[240:243],a[180:183],v[160:163],a[240:243]
ds_read_b64_tr_b16 a[170:171],v1 offset:25856
v_add_u32 v1,40960,v1
v_mfma_f32_16x16x32_bf16 a[240:243],a[184:187],v[192:195],a[240:243]
buffer_load_dwordx4 v[100:103],v6,s[28:31],s78 offen offset:1024 sc1
v_mfma_f32_16x16x32_bf16 a[240:243],a[188:191],v[224:227],a[240:243]
v_mfma_f32_16x16x32_bf16 a[244:247],a[188:191],v[232:235],a[244:247]
v_mfma_f32_16x16x32_bf16 a[244:247],a[184:187],v[200:203],a[244:247]
v_mfma_f32_16x16x32_bf16 a[244:247],a[180:183],v[168:171],a[244:247]
buffer_load_dwordx4 v9,s[72:75],s80 offen offset:2048 lds
v_mfma_f32_16x16x32_bf16 a[244:247],a[176:179],v[136:139],a[244:247]
v_mfma_f32_16x16x32_bf16 a[248:251],a[176:179],v[144:147],a[248:251]
v_mfma_f32_16x16x32_bf16 a[248:251],a[180:183],v[176:179],a[248:251]
v_mfma_f32_16x16x32_bf16 a[248:251],a[184:187],v[208:211],a[248:251]
buffer_load_dwordx4 v[104:107],v6,s[28:31],s78 offen offset:2048 sc1
v_mfma_f32_16x16x32_bf16 a[248:251],a[188:191],v[240:243],a[248:251]
v_mfma_f32_16x16x32_bf16 a[252:255],a[188:191],v[248:251],a[252:255]
ds_read_b64_tr_b16 a[188:189],v2 offset:31008
ds_read_b64_tr_b16 a[190:191],v2 offset:36128
v_mfma_f32_16x16x32_bf16 a[252:255],a[184:187],v[216:219],a[252:255]
ds_read_b64_tr_b16 a[184:185],v2 offset:20768
v_mfma_f32_16x16x32_bf16 a[252:255],a[180:183],v[184:187],a[252:255]
ds_read_b64_tr_b16 a[180:181],v2 offset:10528
v_mfma_f32_16x16x32_bf16 a[252:255],a[176:179],v[152:155],a[252:255]
ds_read_b64_tr_b16 a[176:177],v2 offset:288
v_mfma_f32_16x16x32_bf16 a[0:3],a[96:99],v[128:131],a[0:3]
ds_read_b64_tr_b16 a[178:179],v2 offset:5408
ds_read_b64_tr_b16 a[182:183],v2 offset:15648
v_mfma_f32_16x16x32_bf16 a[0:3],a[100:103],v[160:163],a[0:3]
ds_read_b64_tr_b16 a[186:187],v2 offset:25888
v_add_u32 v2,40960,v2
v_mfma_f32_16x16x32_bf16 a[0:3],a[104:107],v[192:195],a[0:3]
buffer_load_dwordx4 v[108:111],v6,s[28:31],s78 offen offset:3072 sc1
v_mfma_f32_16x16x32_bf16 a[0:3],a[108:111],v[224:227],a[0:3]
v_mfma_f32_16x16x32_bf16 a[4:7],a[108:111],v[232:235],a[4:7]
v_mfma_f32_16x16x32_bf16 a[4:7],a[104:107],v[200:203],a[4:7]
v_mfma_f32_16x16x32_bf16 a[4:7],a[100:103],v[168:171],a[4:7]
buffer_load_dwordx4 v10,s[72:75],s80 offen offset:3072 lds
v_mfma_f32_16x16x32_bf16 a[4:7],a[96:99],v[136:139],a[4:7]
v_mfma_f32_16x16x32_bf16 a[8:11],a[96:99],v[144:147],a[8:11]
v_mfma_f32_16x16x32_bf16 a[8:11],a[100:103],v[176:179],a[8:11]
v_mfma_f32_16x16x32_bf16 a[8:11],a[104:107],v[208:211],a[8:11]
buffer_load_dwordx4 v[112:115],v6,s[28:31],s79 offen sc1
v_mfma_f32_16x16x32_bf16 a[8:11],a[108:111],v[240:243],a[8:11]
v_mfma_f32_16x16x32_bf16 a[12:15],a[108:111],v[248:251],a[12:15]
ds_read_b64_tr_b16 a[108:109],v1 offset:30720
ds_read_b64_tr_b16 a[110:111],v1 offset:35840
v_mfma_f32_16x16x32_bf16 a[12:15],a[104:107],v[216:219],a[12:15]
ds_read_b64_tr_b16 a[104:105],v1 offset:20480
v_mfma_f32_16x16x32_bf16 a[12:15],a[100:103],v[184:187],a[12:15]
s_add_u32 m0,s82,45056
ds_read_b64_tr_b16 a[100:101],v1 offset:10240
v_mfma_f32_16x16x32_bf16 a[12:15],a[96:99],v[152:155],a[12:15]
ds_read_b64_tr_b16 a[96:97],v1
v_mfma_f32_16x16x32_bf16 a[16:19],a[112:115],v[128:131],a[16:19]
ds_read_b64_tr_b16 a[98:99],v1 offset:5120
ds_read_b64_tr_b16 a[102:103],v1 offset:15360
v_mfma_f32_16x16x32_bf16 a[16:19],a[116:119],v[160:163],a[16:19]
buffer_load_dwordx4 v11,s[72:75],s80 offen lds
v_mfma_f32_16x16x32_bf16 a[16:19],a[120:123],v[192:195],a[16:19]
ds_read_b64_tr_b16 a[106:107],v1 offset:25600
v_mfma_f32_16x16x32_bf16 a[16:19],a[124:127],v[224:227],a[16:19]
v_mfma_f32_16x16x32_bf16 a[20:23],a[124:127],v[232:235],a[20:23]
v_mfma_f32_16x16x32_bf16 a[20:23],a[120:123],v[200:203],a[20:23]
buffer_load_dwordx4 v[116:119],v6,s[28:31],s79 offen offset:1024 sc1
v_mfma_f32_16x16x32_bf16 a[20:23],a[116:119],v[168:171],a[20:23]
v_mfma_f32_16x16x32_bf16 a[20:23],a[112:115],v[136:139],a[20:23]
v_mfma_f32_16x16x32_bf16 a[24:27],a[112:115],v[144:147],a[24:27]
v_mfma_f32_16x16x32_bf16 a[24:27],a[116:119],v[176:179],a[24:27]
v_mfma_f32_16x16x32_bf16 a[24:27],a[120:123],v[208:211],a[24:27]
buffer_load_dwordx4 v[120:123],v6,s[28:31],s79 offen offset:2048 sc1
v_mfma_f32_16x16x32_bf16 a[24:27],a[124:127],v[240:243],a[24:27]
v_mfma_f32_16x16x32_bf16 a[28:31],a[124:127],v[248:251],a[28:31]
ds_read_b64_tr_b16 a[124:125],v2 offset:30752
ds_read_b64_tr_b16 a[126:127],v2 offset:35872
v_mfma_f32_16x16x32_bf16 a[28:31],a[120:123],v[216:219],a[28:31]
s_add_u32 m0,s82,46080
ds_read_b64_tr_b16 a[120:121],v2 offset:20512
v_mfma_f32_16x16x32_bf16 a[28:31],a[116:119],v[184:187],a[28:31]
ds_read_b64_tr_b16 a[116:117],v2 offset:10272
v_mfma_f32_16x16x32_bf16 a[28:31],a[112:115],v[152:155],a[28:31]
s_cmp_ge_u32 2,s71
s_cselect_b32 s76,6,4
s_lshl_b32 s76,s76,16
v_mfma_f32_16x16x32_bf16 a[32:35],a[128:131],v[128:131],a[32:35]
s_add_u32 s77,s76,4096
buffer_load_dwordx4 v7,s[72:75],s83 offen lds
v_mfma_f32_16x16x32_bf16 a[32:35],a[132:135],v[160:163],a[32:35]
s_add_u32 s78,s76,8192
s_max_i32 s77,s77,s19
ds_read_b64_tr_b16 a[112:113],v2 offset:32
v_mfma_f32_16x16x32_bf16 a[32:35],a[136:139],v[192:195],a[32:35]
s_max_i32 s78,s78,s100
ds_read_b64_tr_b16 a[114:115],v2 offset:5152
ds_read_b64_tr_b16 a[118:119],v2 offset:15392
v_mfma_f32_16x16x32_bf16 a[32:35],a[140:143],v[224:227],a[32:35]
ds_read_b64_tr_b16 a[122:123],v2 offset:25632
v_mfma_f32_16x16x32_bf16 a[36:39],a[140:143],v[232:235],a[36:39]
buffer_load_dwordx4 v[124:127],v6,s[28:31],s79 offen offset:3072 sc1
v_mfma_f32_16x16x32_bf16 a[36:39],a[136:139],v[200:203],a[36:39]
s_add_u32 s79,s76,12288
s_max_i32 s76,s76,s18
s_max_i32 s79,s79,s101
v_mfma_f32_16x16x32_bf16 a[36:39],a[132:135],v[168:171],a[36:39]
v_mfma_f32_16x16x32_bf16 a[36:39],a[128:131],v[136:139],a[36:39]
v_mfma_f32_16x16x32_bf16 a[40:43],a[128:131],v[144:147],a[40:43]
v_mfma_f32_16x16x32_bf16 a[40:43],a[132:135],v[176:179],a[40:43]
v_mfma_f32_16x16x32_bf16 a[40:43],a[136:139],v[208:211],a[40:43]
v_mfma_f32_16x16x32_bf16 a[40:43],a[140:143],v[240:243],a[40:43]
v_mfma_f32_16x16x32_bf16 a[44:47],a[140:143],v[248:251],a[44:47]
ds_read_b64_tr_b16 a[140:141],v1 offset:30784
ds_read_b64_tr_b16 a[142:143],v1 offset:35904
v_mfma_f32_16x16x32_bf16 a[44:47],a[136:139],v[216:219],a[44:47]
ds_read_b64_tr_b16 a[136:137],v1 offset:20544
v_mfma_f32_16x16x32_bf16 a[44:47],a[132:135],v[184:187],a[44:47]
ds_read_b64_tr_b16 a[132:133],v1 offset:10304
v_mfma_f32_16x16x32_bf16 a[44:47],a[128:131],v[152:155],a[44:47]
s_waitcnt lgkmcnt(4)
buffer_load_dwordx4 v8,s[72:75],s83 offen offset:1024 lds
v_mfma_f32_16x16x32_bf16 a[48:51],a[144:147],v[128:131],a[48:51]
ds_read_b64_tr_b16 a[128:129],v1 offset:64
ds_read_b64_tr_b16 a[130:131],v1 offset:5184
v_mfma_f32_16x16x32_bf16 a[48:51],a[148:151],v[160:163],a[48:51]
ds_read_b64_tr_b16 a[134:135],v1 offset:15424
v_mfma_f32_16x16x32_bf16 a[48:51],a[152:155],v[192:195],a[48:51]
ds_read_b64_tr_b16 a[138:139],v1 offset:25664
v_mfma_f32_16x16x32_bf16 a[48:51],a[156:159],v[224:227],a[48:51]
s_lshl_b32 s94,s70,4
v_bfe_u32 v24,v6,4,2
v_min_u32 v24,2,v24
v_mfma_f32_16x16x32_bf16 a[52:55],a[156:159],v[232:235],a[52:55]
s_add_u32 s94,s94,s40
v_add_u32 v24,s71,v24
v_add_u32 v24,1,v24
v_mfma_f32_16x16x32_bf16 a[52:55],a[152:155],v[200:203],a[52:55]
s_lshl_b32 s94,s94,4
v_and_b32 v24,3,v24
v_lshlrev_b32 v24,6,v24
v_mfma_f32_16x16x32_bf16 a[52:55],a[148:151],v[168:171],a[52:55]
v_mfma_f32_16x16x32_bf16 a[52:55],a[144:147],v[136:139],a[52:55]
buffer_load_dwordx4 v[132:135],v6,s[28:31],s76 offen sc1
v_mfma_f32_16x16x32_bf16 a[56:59],a[144:147],v[144:147],a[56:59]
v_mfma_f32_16x16x32_bf16 a[56:59],a[148:151],v[176:179],a[56:59]
v_mfma_f32_16x16x32_bf16 a[56:59],a[152:155],v[208:211],a[56:59]
v_mfma_f32_16x16x32_bf16 a[56:59],a[156:159],v[240:243],a[56:59]
v_mfma_f32_16x16x32_bf16 a[60:63],a[156:159],v[248:251],a[60:63]
ds_read_b64_tr_b16 a[156:157],v2 offset:30816
ds_read_b64_tr_b16 a[158:159],v2 offset:35936
v_mfma_f32_16x16x32_bf16 a[60:63],a[152:155],v[216:219],a[60:63]
buffer_load_dwordx4 v[140:143],v6,s[28:31],s76 offen offset:1024 sc1
v_mfma_f32_16x16x32_bf16 a[60:63],a[148:151],v[184:187],a[60:63]
ds_read_b64_tr_b16 a[148:149],v2 offset:10336
ds_read_b64_tr_b16 a[150:151],v2 offset:15456
v_mfma_f32_16x16x32_bf16 a[60:63],a[144:147],v[152:155],a[60:63]
ds_read_b64_tr_b16 a[144:145],v2 offset:96
v_mfma_f32_16x16x32_bf16 a[64:67],a[160:163],v[128:131],a[64:67]
ds_read_b64_tr_b16 a[146:147],v2 offset:5216
v_mfma_f32_16x16x32_bf16 a[64:67],a[164:167],v[160:163],a[64:67]
buffer_load_dwordx4 v9,s[72:75],s83 offen offset:2048 lds
v_mfma_f32_16x16x32_bf16 a[64:67],a[168:171],v[192:195],a[64:67]
ds_read_b64_tr_b16 a[152:153],v2 offset:20576
ds_read_b64_tr_b16 a[154:155],v2 offset:25696
v_mfma_f32_16x16x32_bf16 a[64:67],a[172:175],v[224:227],a[64:67]
v_mfma_f32_16x16x32_bf16 a[68:71],a[172:175],v[232:235],a[68:71]
)A"
R"A(v_mfma_f32_16x16x32_bf16 a[68:71],a[168:171],v[200:203],a[68:71]
buffer_load_dwordx4 v[148:151],v6,s[28:31],s76 offen offset:2048 sc1
v_mfma_f32_16x16x32_bf16 a[68:71],a[164:167],v[168:171],a[68:71]
v_mfma_f32_16x16x32_bf16 a[68:71],a[160:163],v[136:139],a[68:71]
v_mfma_f32_16x16x32_bf16 a[72:75],a[160:163],v[144:147],a[72:75]
v_mfma_f32_16x16x32_bf16 a[72:75],a[164:167],v[176:179],a[72:75]
buffer_load_dwordx4 v[156:159],v6,s[28:31],s76 offen offset:3072 sc1
v_mfma_f32_16x16x32_bf16 a[72:75],a[168:171],v[208:211],a[72:75]
v_mfma_f32_16x16x32_bf16 a[72:75],a[172:175],v[240:243],a[72:75]
v_mfma_f32_16x16x32_bf16 a[76:79],a[172:175],v[248:251],a[76:79]
ds_read_b64_tr_b16 a[172:173],v1 offset:30848
ds_read_b64_tr_b16 a[174:175],v1 offset:35968
v_mfma_f32_16x16x32_bf16 a[76:79],a[168:171],v[216:219],a[76:79]
buffer_load_dwordx4 v10,s[72:75],s83 offen offset:3072 lds
v_mfma_f32_16x16x32_bf16 a[76:79],a[164:167],v[184:187],a[76:79]
ds_read_b64_tr_b16 a[164:165],v1 offset:10368
ds_read_b64_tr_b16 a[166:167],v1 offset:15488
v_mfma_f32_16x16x32_bf16 a[76:79],a[160:163],v[152:155],a[76:79]
ds_read_b64_tr_b16 a[160:161],v1 offset:128
v_mfma_f32_16x16x32_bf16 a[80:83],a[176:179],v[128:131],a[80:83]
ds_read_b64_tr_b16 a[162:163],v1 offset:5248
v_mfma_f32_16x16x32_bf16 a[80:83],a[180:183],v[160:163],a[80:83]
buffer_load_dwordx4 v[164:167],v6,s[28:31],s77 offen sc1
v_mfma_f32_16x16x32_bf16 a[80:83],a[184:187],v[192:195],a[80:83]
ds_read_b64_tr_b16 a[168:169],v1 offset:20608
ds_read_b64_tr_b16 a[170:171],v1 offset:25728
v_mfma_f32_16x16x32_bf16 a[80:83],a[188:191],v[224:227],a[80:83]
v_mfma_f32_16x16x32_bf16 a[84:87],a[188:191],v[232:235],a[84:87]
v_mfma_f32_16x16x32_bf16 a[84:87],a[184:187],v[200:203],a[84:87]
buffer_load_dwordx4 v[172:175],v6,s[28:31],s77 offen offset:1024 sc1
v_mfma_f32_16x16x32_bf16 a[84:87],a[180:183],v[168:171],a[84:87]
v_mfma_f32_16x16x32_bf16 a[84:87],a[176:179],v[136:139],a[84:87]
v_mfma_f32_16x16x32_bf16 a[88:91],a[176:179],v[144:147],a[88:91]
v_mfma_f32_16x16x32_bf16 a[88:91],a[180:183],v[176:179],a[88:91]
s_add_u32 m0,s82,50176
v_mfma_f32_16x16x32_bf16 a[88:91],a[184:187],v[208:211],a[88:91]
v_mfma_f32_16x16x32_bf16 a[88:91],a[188:191],v[240:243],a[88:91]
buffer_load_dwordx2 v[26:27],v24,s[64:67],s94 offen offset:1024 sc1
v_mfma_f32_16x16x32_bf16 a[92:95],a[188:191],v[248:251],a[92:95]
ds_read_b64_tr_b16 a[188:189],v2 offset:30880
ds_read_b64_tr_b16 a[190:191],v2 offset:36000
v_mfma_f32_16x16x32_bf16 a[92:95],a[184:187],v[216:219],a[92:95]
ds_read_b64_tr_b16 a[184:185],v2 offset:20640
v_mfma_f32_16x16x32_bf16 a[92:95],a[180:183],v[184:187],a[92:95]
ds_read_b64_tr_b16 a[180:181],v2 offset:10400
v_mfma_f32_16x16x32_bf16 a[92:95],a[176:179],v[152:155],a[92:95]
s_waitcnt vmcnt(13)
buffer_load_dwordx4 v[180:183],v6,s[28:31],s77 offen offset:2048 sc1
v_mfma_f32_16x16x32_bf16 a[192:195],a[96:99],v[64:67],a[192:195]
s_lshl_b32 s43,s41,2
ds_read_b64_tr_b16 a[176:177],v2 offset:160
ds_read_b64_tr_b16 a[178:179],v2 offset:5280
v_mfma_f32_16x16x32_bf16 a[192:195],a[100:103],v[68:71],a[192:195]
s_add_u32 s84,s70,1
s_add_u32 s85,s70,2
ds_read_b64_tr_b16 a[182:183],v2 offset:15520
v_mfma_f32_16x16x32_bf16 a[192:195],a[104:107],v[72:75],a[192:195]
s_add_u32 s86,s70,3
s_add_u32 s43,s43,s71
ds_read_b64_tr_b16 a[186:187],v2 offset:25760
v_mfma_f32_16x16x32_bf16 a[192:195],a[108:111],v[76:79],a[192:195]
s_and_b32 s84,s84,3
buffer_load_dwordx4 v11,s[72:75],s83 offen lds
v_mfma_f32_16x16x32_bf16 a[196:199],a[108:111],v[92:95],a[196:199]
s_and_b32 s85,s85,3
s_and_b32 s86,s86,3
s_mul_i32 s43,s43,245760
v_mfma_f32_16x16x32_bf16 a[196:199],a[104:107],v[88:91],a[196:199]
s_mul_i32 s84,s84,12
s_mul_i32 s85,s85,12
s_mul_i32 s86,s86,12
v_mfma_f32_16x16x32_bf16 a[196:199],a[100:103],v[84:87],a[196:199]
s_add_u32 s43,s43,134217728
s_mov_b64 s[32:33],s[12:13]
s_add_u32 s84,s84,0
v_mfma_f32_16x16x32_bf16 a[196:199],a[96:99],v[80:83],a[196:199]
s_add_u32 s85,s85,4
buffer_load_dwordx4 v[188:191],v6,s[28:31],s77 offen offset:3072 sc1
v_mfma_f32_16x16x32_bf16 a[200:203],a[96:99],v[96:99],a[200:203]
s_add_u32 s86,s86,8
s_add_u32 s84,s84,s40
s_add_u32 s85,s85,s40
v_mfma_f32_16x16x32_bf16 a[200:203],a[100:103],v[100:103],a[200:203]
s_add_u32 s86,s86,s40
s_add_u32 s32,s32,s43
s_addc_u32 s33,s33,0
v_mfma_f32_16x16x32_bf16 a[200:203],a[104:107],v[104:107],a[200:203]
s_mul_i32 s84,s84,5120
s_mul_i32 s85,s85,5120
s_mul_i32 s86,s86,5120
v_mfma_f32_16x16x32_bf16 a[200:203],a[108:111],v[108:111],a[200:203]
s_and_b32 s33,s33,0xffff
buffer_load_dwordx4 v[196:199],v6,s[28:31],s78 offen sc1
s_waitcnt vmcnt(14)
v_mfma_f32_16x16x32_bf16 a[204:207],a[108:111],v[124:127],a[204:207]
s_mov_b32 s34,245760
ds_read_b64_tr_b16 a[108:109],v1 offset:30912
ds_read_b64_tr_b16 a[110:111],v1 offset:36032
v_mfma_f32_16x16x32_bf16 a[204:207],a[104:107],v[120:123],a[204:207]
s_mov_b32 s35,0x20000
s_max_i32 s84,s84,s18
ds_read_b64_tr_b16 a[104:105],v1 offset:20672
v_mfma_f32_16x16x32_bf16 a[204:207],a[100:103],v[116:119],a[204:207]
s_max_i32 s85,s85,s19
s_max_i32 s86,s86,s100
ds_read_b64_tr_b16 a[100:101],v1 offset:10432
v_mfma_f32_16x16x32_bf16 a[204:207],a[96:99],v[112:115],a[204:207]
s_barrier
buffer_load_dwordx4 v[204:207],v6,s[28:31],s78 offen offset:1024 sc1
v_mfma_f32_16x16x32_bf16 a[208:211],a[112:115],v[64:67],a[208:211]
s_mul_i32 s44,s41,307200
ds_read_b64_tr_b16 a[96:97],v1 offset:192
ds_read_b64_tr_b16 a[98:99],v1 offset:5312
v_mfma_f32_16x16x32_bf16 a[208:211],a[116:119],v[68:71],a[208:211]
s_mov_b64 s[20:21],s[16:17]
ds_read_b64_tr_b16 a[102:103],v1 offset:15552
v_bfe_u32 v18,v6,6,2
v_mfma_f32_16x16x32_bf16 a[208:211],a[120:123],v[72:75],a[208:211]
s_lshl_b32 s43,s40,2
ds_read_b64_tr_b16 a[106:107],v1 offset:25792
v_lshrrev_b32 v19,8,v6
v_mfma_f32_16x16x32_bf16 a[208:211],a[124:127],v[76:79],a[208:211]
s_add_u32 s43,s43,s70
buffer_load_dwordx4 v[212:215],v6,s[28:31],s78 offen offset:2048 sc1
v_mfma_f32_16x16x32_bf16 a[212:215],a[124:127],v[92:95],a[212:215]
s_mul_i32 s43,s43,20480
v_xor_b32 v18,v18,v19
v_lshlrev_b32 v18,2,v18
v_mfma_f32_16x16x32_bf16 a[212:215],a[120:123],v[88:91],a[212:215]
s_mov_b32 s22,307200
v_bfe_u32 v19,v6,4,2
v_or_b32 v18,v18,v19
v_mfma_f32_16x16x32_bf16 a[212:215],a[116:119],v[84:87],a[212:215]
s_mov_b32 s23,0x20000
v_and_b32 v19,16,v6
v_mov_b32 v34,0x3f80
v_mfma_f32_16x16x32_bf16 a[212:215],a[112:115],v[80:83],a[212:215]
s_cmp_ge_u32 0,s71
s_cselect_b32 s76,3,1
s_lshl_b32 s76,s76,16
v_mfma_f32_16x16x32_bf16 a[216:219],a[112:115],v[96:99],a[216:219]
s_add_u32 s77,s76,4096
buffer_load_dwordx4 v[220:223],v6,s[28:31],s78 offen offset:3072 sc1
v_mfma_f32_16x16x32_bf16 a[216:219],a[116:119],v[100:103],a[216:219]
s_add_u32 s78,s76,8192
s_max_i32 s77,s77,s19
s_max_i32 s78,s78,s100
v_mfma_f32_16x16x32_bf16 a[216:219],a[120:123],v[104:107],a[216:219]
v_lshlrev_b32 v19,v19,v34
v_lshrrev_b32 v18,1,v18
v_mfma_f32_16x16x32_bf16 a[216:219],a[124:127],v[108:111],a[216:219]
v_cmp_eq_u32 vcc,0,v18
s_nop 1
v_cndmask_b32 v34,0,v19,vcc
v_mfma_f32_16x16x32_bf16 a[220:223],a[124:127],v[124:127],a[220:223]
ds_read_b64_tr_b16 a[124:125],v2 offset:30944
ds_read_b64_tr_b16 a[126:127],v2 offset:36064
v_mfma_f32_16x16x32_bf16 a[220:223],a[120:123],v[120:123],a[220:223]
ds_read_b64_tr_b16 a[120:121],v2 offset:20704
v_add_u32 v25,4096,v6
v_mfma_f32_16x16x32_bf16 a[220:223],a[116:119],v[116:119],a[220:223]
buffer_load_dwordx4 v[228:231],v6,s[28:31],s79 offen sc1
v_mfma_f32_16x16x32_bf16 a[220:223],a[112:115],v[112:115],a[220:223]
s_waitcnt lgkmcnt(3)
ds_read_b64_tr_b16 a[112:113],v2 offset:224
ds_read_b64_tr_b16 a[114:115],v2 offset:5344
v_mfma_f32_16x16x32_bf16 a[224:227],a[128:131],v[64:67],a[224:227]
ds_read_b64_tr_b16 a[116:117],v2 offset:10464
v_mov_b32 v36,0
v_mfma_f32_16x16x32_bf16 a[224:227],a[132:135],v[68:71],a[224:227]
ds_read_b64_tr_b16 a[118:119],v2 offset:15584
v_mov_b32 v37,0
v_mfma_f32_16x16x32_bf16 a[224:227],a[136:139],v[72:75],a[224:227]
ds_read_b64_tr_b16 a[122:123],v2 offset:25824
v_mov_b32 v32,0
v_mfma_f32_16x16x32_bf16 a[224:227],a[140:143],v[76:79],a[224:227]
v_cmp_eq_u32 vcc,1,v18
s_nop 1
v_cndmask_b32 v35,0,v19,vcc
v_mfma_f32_16x16x32_bf16 a[228:231],a[140:143],v[92:95],a[228:231]
buffer_load_dwordx4 v[236:239],v6,s[28:31],s79 offen offset:1024 sc1
v_mfma_f32_16x16x32_bf16 a[228:231],a[136:139],v[88:91],a[228:231]
s_add_u32 s20,s20,s44
s_addc_u32 s21,s21,0
v_mov_b32 v33,0
v_mfma_f32_16x16x32_bf16 a[228:231],a[132:135],v[84:87],a[228:231]
s_mul_i32 s44,s71,320
v_bfe_u32 v40,v6,4,4
v_mul_u32_u24 v40,1280,v40
v_mfma_f32_16x16x32_bf16 a[228:231],a[128:131],v[80:83],a[228:231]
s_waitcnt vmcnt(9)
v_cmp_ne_u32 s[90:91],s68,v26
v_cmp_ne_u32 s[92:93],s69,v27
s_nop 0
s_or_b64 s[90:91],s[90:91],s[92:93]
s_cbranch_scc1 L_fr9_%=
L_fo9_%=:
v_mfma_f32_16x16x32_bf16 a[232:235],a[128:131],v[96:99],a[232:235]
s_and_b32 s21,s21,0xffff
v_lshrrev_b32 v41,8,v6
v_lshl_add_u32 v40,v41,3,v40
v_mfma_f32_16x16x32_bf16 a[232:235],a[132:135],v[100:103],a[232:235]
s_add_u32 s43,s43,s44
v_mfma_f32_16x16x32_bf16 a[232:235],a[136:139],v[104:107],a[232:235]
buffer_load_dwordx4 v[244:247],v6,s[28:31],s79 offen offset:2048 sc1
v_mfma_f32_16x16x32_bf16 a[232:235],a[140:143],v[108:111],a[232:235]
v_mfma_f32_16x16x32_bf16 a[236:239],a[140:143],v[124:127],a[236:239]
ds_read_b64_tr_b16 a[140:141],v1 offset:30976
ds_read_b64_tr_b16 a[142:143],v1 offset:36096
v_mfma_f32_16x16x32_bf16 a[236:239],a[136:139],v[120:123],a[236:239]
ds_read_b64_tr_b16 a[136:137],v1 offset:20736
v_mfma_f32_16x16x32_bf16 a[236:239],a[132:135],v[116:119],a[236:239]
ds_read_b64_tr_b16 a[132:133],v1 offset:10496
v_mfma_f32_16x16x32_bf16 a[236:239],a[128:131],v[112:115],a[236:239]
ds_read_b64_tr_b16 a[128:129],v1 offset:256
v_mfma_f32_16x16x32_bf16 a[240:243],a[144:147],v[64:67],a[240:243]
buffer_load_dwordx4 v[252:255],v6,s[28:31],s79 offen offset:3072 sc1
v_mfma_f32_16x16x32_bf16 a[240:243],a[148:151],v[68:71],a[240:243]
s_add_u32 s79,s76,12288
s_max_i32 s76,s76,s18
ds_read_b64_tr_b16 a[130:131],v1 offset:5376
v_mfma_f32_16x16x32_bf16 a[240:243],a[152:155],v[72:75],a[240:243]
s_max_i32 s79,s79,s101
ds_read_b64_tr_b16 a[134:135],v1 offset:15616
ds_read_b64_tr_b16 a[138:139],v1 offset:25856
v_mfma_f32_16x16x32_bf16 a[240:243],a[156:159],v[76:79],a[240:243]
v_subrev_u32 v1,122880,v1
v_mfma_f32_16x16x32_bf16 a[244:247],a[156:159],v[92:95],a[244:247]
buffer_load_dwordx4 v[128:131],v6,s[28:31],s76 offen sc1
v_mfma_f32_16x16x32_bf16 a[244:247],a[152:155],v[88:91],a[244:247]
v_mfma_f32_16x16x32_bf16 a[244:247],a[148:151],v[84:87],a[244:247]
v_mfma_f32_16x16x32_bf16 a[244:247],a[144:147],v[80:83],a[244:247]
v_mfma_f32_16x16x32_bf16 a[248:251],a[144:147],v[96:99],a[248:251]
buffer_load_dwordx4 v[160:163],v6,s[28:31],s76 offen offset:1024 sc1
v_mfma_f32_16x16x32_bf16 a[248:251],a[148:151],v[100:103],a[248:251]
v_mfma_f32_16x16x32_bf16 a[248:251],a[152:155],v[104:107],a[248:251]
v_mfma_f32_16x16x32_bf16 a[248:251],a[156:159],v[108:111],a[248:251]
v_mfma_f32_16x16x32_bf16 a[252:255],a[156:159],v[124:127],a[252:255]
buffer_load_dwordx4 v[192:195],v6,s[28:31],s76 offen offset:2048 sc1
v_mfma_f32_16x16x32_bf16 a[252:255],a[152:155],v[120:123],a[252:255]
ds_read_b64_tr_b16 a[152:153],v2 offset:20768
ds_read_b64_tr_b16 a[154:155],v2 offset:25888
v_mfma_f32_16x16x32_bf16 a[252:255],a[148:151],v[116:119],a[252:255]
ds_read_b64_tr_b16 a[148:149],v2 offset:10528
v_mfma_f32_16x16x32_bf16 a[252:255],a[144:147],v[112:115],a[252:255]
ds_read_b64_tr_b16 a[144:145],v2 offset:288
v_mfma_f32_16x16x32_bf16 a[0:3],a[160:163],v[64:67],a[0:3]
ds_read_b64_tr_b16 a[146:147],v2 offset:5408
v_mfma_f32_16x16x32_bf16 a[0:3],a[164:167],v[68:71],a[0:3]
buffer_load_dwordx4 v[224:227],v6,s[28:31],s76 offen offset:3072 sc1
v_mfma_f32_16x16x32_bf16 a[0:3],a[168:171],v[72:75],a[0:3]
ds_read_b64_tr_b16 a[150:151],v2 offset:15648
ds_read_b64_tr_b16 a[156:157],v2 offset:31008
v_mfma_f32_16x16x32_bf16 a[0:3],a[172:175],v[76:79],a[0:3]
ds_read_b64_tr_b16 a[158:159],v2 offset:36128
v_subrev_u32 v2,122880,v2
v_mfma_f32_16x16x32_bf16 a[4:7],a[172:175],v[92:95],a[4:7]
v_mfma_f32_16x16x32_bf16 a[4:7],a[168:171],v[88:91],a[4:7]
)A"
R"A(v_mfma_f32_16x16x32_bf16 a[4:7],a[164:167],v[84:87],a[4:7]
v_mfma_f32_16x16x32_bf16 a[4:7],a[160:163],v[80:83],a[4:7]
buffer_load_dwordx4 v[136:139],v6,s[28:31],s77 offen sc1
v_mfma_f32_16x16x32_bf16 a[8:11],a[160:163],v[96:99],a[8:11]
v_mfma_f32_16x16x32_bf16 a[8:11],a[164:167],v[100:103],a[8:11]
v_mfma_f32_16x16x32_bf16 a[8:11],a[168:171],v[104:107],a[8:11]
v_mfma_f32_16x16x32_bf16 a[8:11],a[172:175],v[108:111],a[8:11]
v_mfma_f32_16x16x32_bf16 a[12:15],a[172:175],v[124:127],a[12:15]
ds_read_b64_tr_b16 a[172:173],v1 offset:30720
ds_read_b64_tr_b16 a[174:175],v1 offset:35840
v_mfma_f32_16x16x32_bf16 a[12:15],a[168:171],v[120:123],a[12:15]
buffer_load_dwordx4 v[168:171],v6,s[28:31],s77 offen offset:1024 sc1
v_mfma_f32_16x16x32_bf16 a[12:15],a[164:167],v[116:119],a[12:15]
ds_read_b64_tr_b16 a[164:165],v1 offset:10240
ds_read_b64_tr_b16 a[166:167],v1 offset:15360
v_mfma_f32_16x16x32_bf16 a[12:15],a[160:163],v[112:115],a[12:15]
ds_read_b64_tr_b16 a[160:161],v1
v_mfma_f32_16x16x32_bf16 a[16:19],a[176:179],v[64:67],a[16:19]
ds_read_b64_tr_b16 a[162:163],v1 offset:5120
v_mfma_f32_16x16x32_bf16 a[16:19],a[180:183],v[68:71],a[16:19]
ds_read_b64_tr_b16 a[168:169],v1 offset:20480
v_mfma_f32_16x16x32_bf16 a[16:19],a[184:187],v[72:75],a[16:19]
ds_read_b64_tr_b16 a[170:171],v1 offset:25600
v_mfma_f32_16x16x32_bf16 a[16:19],a[188:191],v[76:79],a[16:19]
buffer_load_dwordx4 v[200:203],v6,s[28:31],s77 offen offset:2048 sc1
v_mfma_f32_16x16x32_bf16 a[20:23],a[188:191],v[92:95],a[20:23]
v_mfma_f32_16x16x32_bf16 a[20:23],a[184:187],v[88:91],a[20:23]
v_mfma_f32_16x16x32_bf16 a[20:23],a[180:183],v[84:87],a[20:23]
v_mfma_f32_16x16x32_bf16 a[20:23],a[176:179],v[80:83],a[20:23]
v_mfma_f32_16x16x32_bf16 a[24:27],a[176:179],v[96:99],a[24:27]
v_mfma_f32_16x16x32_bf16 a[24:27],a[180:183],v[100:103],a[24:27]
buffer_load_dwordx4 v[232:235],v6,s[28:31],s77 offen offset:3072 sc1
v_mfma_f32_16x16x32_bf16 a[24:27],a[184:187],v[104:107],a[24:27]
v_mfma_f32_16x16x32_bf16 a[24:27],a[188:191],v[108:111],a[24:27]
v_mfma_f32_16x16x32_bf16 a[28:31],a[188:191],v[124:127],a[28:31]
ds_read_b64_tr_b16 a[188:189],v2 offset:30752
ds_read_b64_tr_b16 a[190:191],v2 offset:35872
v_mfma_f32_16x16x32_bf16 a[28:31],a[184:187],v[120:123],a[28:31]
ds_read_b64_tr_b16 a[184:185],v2 offset:20512
v_mfma_f32_16x16x32_bf16 a[28:31],a[180:183],v[116:119],a[28:31]
ds_read_b64_tr_b16 a[180:181],v2 offset:10272
v_mfma_f32_16x16x32_bf16 a[28:31],a[176:179],v[112:115],a[28:31]
buffer_load_dwordx4 v[144:147],v6,s[28:31],s78 offen sc1
v_mfma_f32_16x16x32_bf16 a[32:35],a[96:99],v[64:67],a[32:35]
ds_read_b64_tr_b16 a[176:177],v2 offset:32
ds_read_b64_tr_b16 a[178:179],v2 offset:5152
v_mfma_f32_16x16x32_bf16 a[32:35],a[100:103],v[68:71],a[32:35]
ds_read_b64_tr_b16 a[182:183],v2 offset:15392
v_mfma_f32_16x16x32_bf16 a[32:35],a[104:107],v[72:75],a[32:35]
ds_read_b64_tr_b16 a[186:187],v2 offset:25632
v_mfma_f32_16x16x32_bf16 a[32:35],a[108:111],v[76:79],a[32:35]
v_mfma_f32_16x16x32_bf16 a[36:39],a[108:111],v[92:95],a[36:39]
v_mfma_f32_16x16x32_bf16 a[36:39],a[104:107],v[88:91],a[36:39]
buffer_load_dwordx4 v[176:179],v6,s[28:31],s78 offen offset:1024 sc1
v_mfma_f32_16x16x32_bf16 a[36:39],a[100:103],v[84:87],a[36:39]
v_mfma_f32_16x16x32_bf16 a[36:39],a[96:99],v[80:83],a[36:39]
v_mfma_f32_16x16x32_bf16 a[40:43],a[96:99],v[96:99],a[40:43]
v_mfma_f32_16x16x32_bf16 a[40:43],a[100:103],v[100:103],a[40:43]
v_mfma_f32_16x16x32_bf16 a[40:43],a[104:107],v[104:107],a[40:43]
v_mfma_f32_16x16x32_bf16 a[40:43],a[108:111],v[108:111],a[40:43]
buffer_load_dwordx4 v[208:211],v6,s[28:31],s78 offen offset:2048 sc1
v_mfma_f32_16x16x32_bf16 a[44:47],a[108:111],v[124:127],a[44:47]
ds_read_b64_tr_b16 a[108:109],v1 offset:30784
ds_read_b64_tr_b16 a[110:111],v1 offset:35904
v_mfma_f32_16x16x32_bf16 a[44:47],a[104:107],v[120:123],a[44:47]
ds_read_b64_tr_b16 a[104:105],v1 offset:20544
v_mfma_f32_16x16x32_bf16 a[44:47],a[100:103],v[116:119],a[44:47]
ds_read_b64_tr_b16 a[100:101],v1 offset:10304
v_mfma_f32_16x16x32_bf16 a[44:47],a[96:99],v[112:115],a[44:47]
s_waitcnt lgkmcnt(4)
ds_read_b64_tr_b16 a[96:97],v1 offset:64
v_mfma_f32_16x16x32_bf16 a[48:51],a[112:115],v[64:67],a[48:51]
ds_read_b64_tr_b16 a[98:99],v1 offset:5184
ds_read_b64_tr_b16 a[102:103],v1 offset:15424
v_mfma_f32_16x16x32_bf16 a[48:51],a[116:119],v[68:71],a[48:51]
buffer_load_dwordx4 v[240:243],v6,s[28:31],s78 offen offset:3072 sc1
v_mfma_f32_16x16x32_bf16 a[48:51],a[120:123],v[72:75],a[48:51]
ds_read_b64_tr_b16 a[106:107],v1 offset:25664
v_mfma_f32_16x16x32_bf16 a[48:51],a[124:127],v[76:79],a[48:51]
v_mfma_f32_16x16x32_bf16 a[52:55],a[124:127],v[92:95],a[52:55]
v_mfma_f32_16x16x32_bf16 a[52:55],a[120:123],v[88:91],a[52:55]
v_mfma_f32_16x16x32_bf16 a[52:55],a[116:119],v[84:87],a[52:55]
v_mfma_f32_16x16x32_bf16 a[52:55],a[112:115],v[80:83],a[52:55]
buffer_load_dwordx4 v[152:155],v6,s[28:31],s79 offen sc1
v_mfma_f32_16x16x32_bf16 a[56:59],a[112:115],v[96:99],a[56:59]
v_mfma_f32_16x16x32_bf16 a[56:59],a[116:119],v[100:103],a[56:59]
s_cmp_ge_u32 1,s71
s_cselect_b32 s80,5,3
s_mul_i32 s80,s80,163840
v_mfma_f32_16x16x32_bf16 a[56:59],a[120:123],v[104:107],a[56:59]
s_add_u32 s80,s80,s81
s_add_u32 s83,s80,20480
v_mfma_f32_16x16x32_bf16 a[56:59],a[124:127],v[108:111],a[56:59]
v_mfma_f32_16x16x32_bf16 a[60:63],a[124:127],v[124:127],a[60:63]
ds_read_b64_tr_b16 a[124:125],v2 offset:30816
ds_read_b64_tr_b16 a[126:127],v2 offset:35936
v_mfma_f32_16x16x32_bf16 a[60:63],a[120:123],v[120:123],a[60:63]
buffer_load_dwordx4 v[184:187],v6,s[28:31],s79 offen offset:1024 sc1
v_mfma_f32_16x16x32_bf16 a[60:63],a[116:119],v[116:119],a[60:63]
ds_read_b64_tr_b16 a[116:117],v2 offset:10336
ds_read_b64_tr_b16 a[118:119],v2 offset:15456
v_mfma_f32_16x16x32_bf16 a[60:63],a[112:115],v[112:115],a[60:63]
ds_read_b64_tr_b16 a[112:113],v2 offset:96
v_mfma_f32_16x16x32_bf16 a[64:67],a[128:131],v[64:67],a[64:67]
ds_read_b64_tr_b16 a[114:115],v2 offset:5216
v_mfma_f32_16x16x32_bf16 a[64:67],a[132:135],v[68:71],a[64:67]
ds_read_b64_tr_b16 a[120:121],v2 offset:20576
v_mfma_f32_16x16x32_bf16 a[64:67],a[136:139],v[72:75],a[64:67]
ds_read_b64_tr_b16 a[122:123],v2 offset:25696
v_mfma_f32_16x16x32_bf16 a[64:67],a[140:143],v[76:79],a[64:67]
buffer_load_dwordx4 v[216:219],v6,s[28:31],s79 offen offset:2048 sc1
v_mfma_f32_16x16x32_bf16 a[68:71],a[140:143],v[92:95],a[68:71]
v_mfma_f32_16x16x32_bf16 a[68:71],a[136:139],v[88:91],a[68:71]
v_mfma_f32_16x16x32_bf16 a[68:71],a[132:135],v[84:87],a[68:71]
v_mfma_f32_16x16x32_bf16 a[68:71],a[128:131],v[80:83],a[68:71]
v_mfma_f32_16x16x32_bf16 a[72:75],a[128:131],v[96:99],a[72:75]
v_mfma_f32_16x16x32_bf16 a[72:75],a[132:135],v[100:103],a[72:75]
buffer_load_dwordx4 v[248:251],v6,s[28:31],s79 offen offset:3072 sc1
v_mfma_f32_16x16x32_bf16 a[72:75],a[136:139],v[104:107],a[72:75]
v_mfma_f32_16x16x32_bf16 a[72:75],a[140:143],v[108:111],a[72:75]
v_mfma_f32_16x16x32_bf16 a[76:79],a[140:143],v[124:127],a[76:79]
ds_read_b64_tr_b16 a[140:141],v1 offset:30848
ds_read_b64_tr_b16 a[142:143],v1 offset:35968
v_mfma_f32_16x16x32_bf16 a[76:79],a[136:139],v[120:123],a[76:79]
ds_read_b64_tr_b16 a[136:137],v1 offset:20608
v_mfma_f32_16x16x32_bf16 a[76:79],a[132:135],v[116:119],a[76:79]
s_add_u32 m0,s82,81920
ds_read_b64_tr_b16 a[132:133],v1 offset:10368
v_mfma_f32_16x16x32_bf16 a[76:79],a[128:131],v[112:115],a[76:79]
ds_read_b64_tr_b16 a[128:129],v1 offset:128
v_mfma_f32_16x16x32_bf16 a[80:83],a[144:147],v[64:67],a[80:83]
ds_read_b64_tr_b16 a[130:131],v1 offset:5248
ds_read_b64_tr_b16 a[134:135],v1 offset:15488
v_mfma_f32_16x16x32_bf16 a[80:83],a[148:151],v[68:71],a[80:83]
buffer_load_dwordx4 v7,s[72:75],s80 offen lds
v_mfma_f32_16x16x32_bf16 a[80:83],a[152:155],v[72:75],a[80:83]
ds_read_b64_tr_b16 a[138:139],v1 offset:25728
v_mfma_f32_16x16x32_bf16 a[80:83],a[156:159],v[76:79],a[80:83]
v_mfma_f32_16x16x32_bf16 a[84:87],a[156:159],v[92:95],a[84:87]
v_mfma_f32_16x16x32_bf16 a[84:87],a[152:155],v[88:91],a[84:87]
v_mfma_f32_16x16x32_bf16 a[84:87],a[148:151],v[84:87],a[84:87]
buffer_load_dwordx4 v8,s[72:75],s80 offen offset:1024 lds
v_mfma_f32_16x16x32_bf16 a[84:87],a[144:147],v[80:83],a[84:87]
v_mfma_f32_16x16x32_bf16 a[88:91],a[144:147],v[96:99],a[88:91]
v_mfma_f32_16x16x32_bf16 a[88:91],a[148:151],v[100:103],a[88:91]
v_mfma_f32_16x16x32_bf16 a[88:91],a[152:155],v[104:107],a[88:91]
v_mfma_f32_16x16x32_bf16 a[88:91],a[156:159],v[108:111],a[88:91]
buffer_load_dwordx4 v9,s[72:75],s80 offen offset:2048 lds
v_mfma_f32_16x16x32_bf16 a[92:95],a[156:159],v[124:127],a[92:95]
ds_read_b64_tr_b16 a[156:157],v2 offset:30880
ds_read_b64_tr_b16 a[158:159],v2 offset:36000
v_mfma_f32_16x16x32_bf16 a[92:95],a[152:155],v[120:123],a[92:95]
ds_read_b64_tr_b16 a[152:153],v2 offset:20640
v_mfma_f32_16x16x32_bf16 a[92:95],a[148:151],v[116:119],a[92:95]
ds_read_b64_tr_b16 a[148:149],v2 offset:10400
v_mfma_f32_16x16x32_bf16 a[92:95],a[144:147],v[112:115],a[92:95]
ds_read_b64_tr_b16 a[144:145],v2 offset:160
v_mfma_f32_16x16x32_bf16 a[192:195],a[160:163],v[132:135],a[192:195]
buffer_load_dwordx4 v10,s[72:75],s80 offen offset:3072 lds
v_mfma_f32_16x16x32_bf16 a[192:195],a[164:167],v[140:143],a[192:195]
ds_read_b64_tr_b16 a[146:147],v2 offset:5280
ds_read_b64_tr_b16 a[150:151],v2 offset:15520
v_mfma_f32_16x16x32_bf16 a[192:195],a[168:171],v[148:151],a[192:195]
s_add_u32 m0,s82,86016
ds_read_b64_tr_b16 a[154:155],v2 offset:25760
v_mfma_f32_16x16x32_bf16 a[192:195],a[172:175],v[156:159],a[192:195]
s_waitcnt vmcnt(12)
v_mfma_f32_16x16x32_bf16 a[196:199],a[172:175],v[188:191],a[196:199]
v_mfma_f32_16x16x32_bf16 a[196:199],a[168:171],v[180:183],a[196:199]
buffer_load_dwordx4 v11,s[72:75],s80 offen lds
v_mfma_f32_16x16x32_bf16 a[196:199],a[164:167],v[172:175],a[196:199]
v_mfma_f32_16x16x32_bf16 a[196:199],a[160:163],v[164:167],a[196:199]
s_add_u32 m0,s82,87040
v_mfma_f32_16x16x32_bf16 a[200:203],a[160:163],v[196:199],a[200:203]
v_mfma_f32_16x16x32_bf16 a[200:203],a[164:167],v[204:207],a[200:203]
v_mfma_f32_16x16x32_bf16 a[200:203],a[168:171],v[212:215],a[200:203]
buffer_load_dwordx4 v7,s[72:75],s83 offen lds
v_mfma_f32_16x16x32_bf16 a[200:203],a[172:175],v[220:223],a[200:203]
v_mfma_f32_16x16x32_bf16 a[204:207],a[172:175],v[252:255],a[204:207]
ds_read_b64_tr_b16 a[172:173],v1 offset:30912
ds_read_b64_tr_b16 a[174:175],v1 offset:36032
v_mfma_f32_16x16x32_bf16 a[204:207],a[168:171],v[244:247],a[204:207]
ds_read_b64_tr_b16 a[168:169],v1 offset:20672
v_mfma_f32_16x16x32_bf16 a[204:207],a[164:167],v[236:239],a[204:207]
ds_read_b64_tr_b16 a[164:165],v1 offset:10432
v_mfma_f32_16x16x32_bf16 a[204:207],a[160:163],v[228:231],a[204:207]
s_barrier
buffer_load_dwordx4 v8,s[72:75],s83 offen offset:1024 lds
v_mfma_f32_16x16x32_bf16 a[208:211],a[176:179],v[132:135],a[208:211]
s_cmp_ge_u32 1,s71
s_cselect_b32 s76,5,3
s_lshl_b32 s76,s76,16
v_mfma_f32_16x16x32_bf16 a[208:211],a[180:183],v[140:143],a[208:211]
s_add_u32 s77,s76,4096
s_add_u32 s78,s76,8192
s_add_u32 s79,s76,12288
v_mfma_f32_16x16x32_bf16 a[208:211],a[184:187],v[148:151],a[208:211]
s_max_i32 s76,s76,s18
s_max_i32 s77,s77,s19
s_max_i32 s78,s78,s100
v_mfma_f32_16x16x32_bf16 a[208:211],a[188:191],v[156:159],a[208:211]
s_max_i32 s79,s79,s101
buffer_load_dwordx4 v9,s[72:75],s83 offen offset:2048 lds
v_mfma_f32_16x16x32_bf16 a[212:215],a[188:191],v[188:191],a[212:215]
ds_read_b64_tr_b16 a[160:161],v1 offset:192
ds_read_b64_tr_b16 a[162:163],v1 offset:5312
v_mfma_f32_16x16x32_bf16 a[212:215],a[184:187],v[180:183],a[212:215]
ds_read_b64_tr_b16 a[166:167],v1 offset:15552
v_mfma_f32_16x16x32_bf16 a[212:215],a[180:183],v[172:175],a[212:215]
ds_read_b64_tr_b16 a[170:171],v1 offset:25792
v_mfma_f32_16x16x32_bf16 a[212:215],a[176:179],v[164:167],a[212:215]
v_mfma_f32_16x16x32_bf16 a[216:219],a[176:179],v[196:199],a[216:219]
buffer_load_dwordx4 v10,s[72:75],s83 offen offset:3072 lds
v_mfma_f32_16x16x32_bf16 a[216:219],a[180:183],v[204:207],a[216:219]
v_mfma_f32_16x16x32_bf16 a[216:219],a[184:187],v[212:215],a[216:219]
s_add_u32 m0,s82,91136
v_mfma_f32_16x16x32_bf16 a[216:219],a[188:191],v[220:223],a[216:219]
v_mfma_f32_16x16x32_bf16 a[220:223],a[188:191],v[252:255],a[220:223]
)A"
R"A(ds_read_b64_tr_b16 a[188:189],v2 offset:30944
ds_read_b64_tr_b16 a[190:191],v2 offset:36064
v_mfma_f32_16x16x32_bf16 a[220:223],a[184:187],v[244:247],a[220:223]
buffer_load_dwordx4 v11,s[72:75],s83 offen lds
v_mfma_f32_16x16x32_bf16 a[220:223],a[180:183],v[236:239],a[220:223]
ds_read_b64_tr_b16 a[180:181],v2 offset:10464
ds_read_b64_tr_b16 a[182:183],v2 offset:15584
v_mfma_f32_16x16x32_bf16 a[220:223],a[176:179],v[228:231],a[220:223]
s_waitcnt lgkmcnt(4)
ds_read_b64_tr_b16 a[176:177],v2 offset:224
v_mfma_f32_16x16x32_bf16 a[224:227],a[96:99],v[132:135],a[224:227]
ds_read_b64_tr_b16 a[178:179],v2 offset:5344
v_mfma_f32_16x16x32_bf16 a[224:227],a[100:103],v[140:143],a[224:227]
ds_read_b64_tr_b16 a[184:185],v2 offset:20704
v_mfma_f32_16x16x32_bf16 a[224:227],a[104:107],v[148:151],a[224:227]
ds_read_b64_tr_b16 a[186:187],v2 offset:25824
v_mfma_f32_16x16x32_bf16 a[224:227],a[108:111],v[156:159],a[224:227]
v_mfma_f32_16x16x32_bf16 a[228:231],a[108:111],v[188:191],a[228:231]
v_mfma_f32_16x16x32_bf16 a[228:231],a[104:107],v[180:183],a[228:231]
v_mfma_f32_16x16x32_bf16 a[228:231],a[100:103],v[172:175],a[228:231]
v_mfma_f32_16x16x32_bf16 a[228:231],a[96:99],v[164:167],a[228:231]
buffer_load_dwordx4 v[64:67],v6,s[28:31],s76 offen sc1
v_mfma_f32_16x16x32_bf16 a[232:235],a[96:99],v[196:199],a[232:235]
v_mfma_f32_16x16x32_bf16 a[232:235],a[100:103],v[204:207],a[232:235]
v_mfma_f32_16x16x32_bf16 a[232:235],a[104:107],v[212:215],a[232:235]
v_mfma_f32_16x16x32_bf16 a[232:235],a[108:111],v[220:223],a[232:235]
v_mfma_f32_16x16x32_bf16 a[236:239],a[108:111],v[252:255],a[236:239]
buffer_load_dwordx4 v[68:71],v6,s[28:31],s76 offen offset:1024 sc1
v_mfma_f32_16x16x32_bf16 a[236:239],a[104:107],v[244:247],a[236:239]
ds_read_b64_tr_b16 a[104:105],v1 offset:20736
ds_read_b64_tr_b16 a[106:107],v1 offset:25856
v_mfma_f32_16x16x32_bf16 a[236:239],a[100:103],v[236:239],a[236:239]
ds_read_b64_tr_b16 a[100:101],v1 offset:10496
v_mfma_f32_16x16x32_bf16 a[236:239],a[96:99],v[228:231],a[236:239]
ds_read_b64_tr_b16 a[96:97],v1 offset:256
v_mfma_f32_16x16x32_bf16 a[240:243],a[112:115],v[132:135],a[240:243]
ds_read_b64_tr_b16 a[98:99],v1 offset:5376
v_mfma_f32_16x16x32_bf16 a[240:243],a[116:119],v[140:143],a[240:243]
buffer_load_dwordx4 v[72:75],v6,s[28:31],s76 offen offset:2048 sc1
v_mfma_f32_16x16x32_bf16 a[240:243],a[120:123],v[148:151],a[240:243]
ds_read_b64_tr_b16 a[102:103],v1 offset:15616
ds_read_b64_tr_b16 a[108:109],v1 offset:30976
v_mfma_f32_16x16x32_bf16 a[240:243],a[124:127],v[156:159],a[240:243]
ds_read_b64_tr_b16 a[110:111],v1 offset:36096
v_add_u32 v1,40960,v1
v_mfma_f32_16x16x32_bf16 a[244:247],a[124:127],v[188:191],a[244:247]
v_add_u32 v42,40960,v1
v_add_u32 v44,81920,v1
v_mfma_f32_16x16x32_bf16 a[244:247],a[120:123],v[180:183],a[244:247]
v_mfma_f32_16x16x32_bf16 a[244:247],a[116:119],v[172:175],a[244:247]
buffer_load_dwordx4 v[76:79],v6,s[28:31],s76 offen offset:3072 sc1
v_mfma_f32_16x16x32_bf16 a[244:247],a[112:115],v[164:167],a[244:247]
v_mfma_f32_16x16x32_bf16 a[248:251],a[112:115],v[196:199],a[248:251]
v_mfma_f32_16x16x32_bf16 a[248:251],a[116:119],v[204:207],a[248:251]
v_mfma_f32_16x16x32_bf16 a[248:251],a[120:123],v[212:215],a[248:251]
v_mfma_f32_16x16x32_bf16 a[248:251],a[124:127],v[220:223],a[248:251]
buffer_load_dwordx4 v[80:83],v6,s[28:31],s77 offen sc1
v_mfma_f32_16x16x32_bf16 a[252:255],a[124:127],v[252:255],a[252:255]
ds_read_b64_tr_b16 a[124:125],v2 offset:31008
ds_read_b64_tr_b16 a[126:127],v2 offset:36128
v_mfma_f32_16x16x32_bf16 a[252:255],a[120:123],v[244:247],a[252:255]
ds_read_b64_tr_b16 a[120:121],v2 offset:20768
v_mfma_f32_16x16x32_bf16 a[252:255],a[116:119],v[236:239],a[252:255]
ds_read_b64_tr_b16 a[116:117],v2 offset:10528
v_mfma_f32_16x16x32_bf16 a[252:255],a[112:115],v[228:231],a[252:255]
ds_read_b64_tr_b16 a[112:113],v2 offset:288
v_mfma_f32_16x16x32_bf16 a[0:3],a[128:131],v[132:135],a[0:3]
buffer_load_dwordx4 v[84:87],v6,s[28:31],s77 offen offset:1024 sc1
v_mfma_f32_16x16x32_bf16 a[0:3],a[132:135],v[140:143],a[0:3]
ds_read_b64_tr_b16 a[114:115],v2 offset:5408
ds_read_b64_tr_b16 a[118:119],v2 offset:15648
v_mfma_f32_16x16x32_bf16 a[0:3],a[136:139],v[148:151],a[0:3]
ds_read_b64_tr_b16 a[122:123],v2 offset:25888
v_add_u32 v2,40960,v2
v_mfma_f32_16x16x32_bf16 a[0:3],a[140:143],v[156:159],a[0:3]
v_add_u32 v43,40960,v2
v_add_u32 v45,81920,v2
v_mfma_f32_16x16x32_bf16 a[4:7],a[140:143],v[188:191],a[4:7]
v_mfma_f32_16x16x32_bf16 a[4:7],a[136:139],v[180:183],a[4:7]
buffer_load_dwordx4 v[88:91],v6,s[28:31],s77 offen offset:2048 sc1
v_mfma_f32_16x16x32_bf16 a[4:7],a[132:135],v[172:175],a[4:7]
v_mfma_f32_16x16x32_bf16 a[4:7],a[128:131],v[164:167],a[4:7]
v_mfma_f32_16x16x32_bf16 a[8:11],a[128:131],v[196:199],a[8:11]
v_mfma_f32_16x16x32_bf16 a[8:11],a[132:135],v[204:207],a[8:11]
v_mfma_f32_16x16x32_bf16 a[8:11],a[136:139],v[212:215],a[8:11]
buffer_load_dwordx4 v[92:95],v6,s[28:31],s77 offen offset:3072 sc1
v_mfma_f32_16x16x32_bf16 a[8:11],a[140:143],v[220:223],a[8:11]
v_mfma_f32_16x16x32_bf16 a[12:15],a[140:143],v[252:255],a[12:15]
ds_read_b64_tr_b16 a[140:141],v1 offset:30720
ds_read_b64_tr_b16 a[142:143],v1 offset:35840
v_mfma_f32_16x16x32_bf16 a[12:15],a[136:139],v[244:247],a[12:15]
ds_read_b64_tr_b16 a[136:137],v1 offset:20480
v_mfma_f32_16x16x32_bf16 a[12:15],a[132:135],v[236:239],a[12:15]
ds_read_b64_tr_b16 a[132:133],v1 offset:10240
v_mfma_f32_16x16x32_bf16 a[12:15],a[128:131],v[228:231],a[12:15]
buffer_load_dwordx4 v[96:99],v6,s[28:31],s78 offen sc1
v_mfma_f32_16x16x32_bf16 a[16:19],a[144:147],v[132:135],a[16:19]
ds_read_b64_tr_b16 a[128:129],v1
ds_read_b64_tr_b16 a[130:131],v1 offset:5120
v_mfma_f32_16x16x32_bf16 a[16:19],a[148:151],v[140:143],a[16:19]
ds_read_b64_tr_b16 a[134:135],v1 offset:15360
v_mfma_f32_16x16x32_bf16 a[16:19],a[152:155],v[148:151],a[16:19]
ds_read_b64_tr_b16 a[138:139],v1 offset:25600
v_mfma_f32_16x16x32_bf16 a[16:19],a[156:159],v[156:159],a[16:19]
v_mfma_f32_16x16x32_bf16 a[20:23],a[156:159],v[188:191],a[20:23]
buffer_load_dwordx4 v[100:103],v6,s[28:31],s78 offen offset:1024 sc1
v_mfma_f32_16x16x32_bf16 a[20:23],a[152:155],v[180:183],a[20:23]
v_mfma_f32_16x16x32_bf16 a[20:23],a[148:151],v[172:175],a[20:23]
v_mfma_f32_16x16x32_bf16 a[20:23],a[144:147],v[164:167],a[20:23]
v_mfma_f32_16x16x32_bf16 a[24:27],a[144:147],v[196:199],a[24:27]
v_mfma_f32_16x16x32_bf16 a[24:27],a[148:151],v[204:207],a[24:27]
buffer_load_dwordx4 v[104:107],v6,s[28:31],s78 offen offset:2048 sc1
v_mfma_f32_16x16x32_bf16 a[24:27],a[152:155],v[212:215],a[24:27]
v_mfma_f32_16x16x32_bf16 a[24:27],a[156:159],v[220:223],a[24:27]
v_mfma_f32_16x16x32_bf16 a[28:31],a[156:159],v[252:255],a[28:31]
ds_read_b64_tr_b16 a[156:157],v2 offset:30752
ds_read_b64_tr_b16 a[158:159],v2 offset:35872
v_mfma_f32_16x16x32_bf16 a[28:31],a[152:155],v[244:247],a[28:31]
ds_read_b64_tr_b16 a[152:153],v2 offset:20512
v_mfma_f32_16x16x32_bf16 a[28:31],a[148:151],v[236:239],a[28:31]
buffer_load_dwordx4 v[108:111],v6,s[28:31],s78 offen offset:3072 sc1
v_mfma_f32_16x16x32_bf16 a[28:31],a[144:147],v[228:231],a[28:31]
s_cmp_ge_u32 2,s71
s_cselect_b32 s80,7,5
s_mul_i32 s80,s80,163840
v_mfma_f32_16x16x32_bf16 a[32:35],a[160:163],v[132:135],a[32:35]
s_add_u32 s80,s80,s81
s_add_u32 s83,s80,20480
ds_read_b64_tr_b16 a[144:145],v2 offset:32
v_mfma_f32_16x16x32_bf16 a[32:35],a[164:167],v[140:143],a[32:35]
ds_read_b64_tr_b16 a[146:147],v2 offset:5152
ds_read_b64_tr_b16 a[148:149],v2 offset:10272
v_mfma_f32_16x16x32_bf16 a[32:35],a[168:171],v[148:151],a[32:35]
ds_read_b64_tr_b16 a[150:151],v2 offset:15392
v_mfma_f32_16x16x32_bf16 a[32:35],a[172:175],v[156:159],a[32:35]
buffer_load_dwordx4 v[112:115],v6,s[28:31],s79 offen sc1
v_mfma_f32_16x16x32_bf16 a[36:39],a[172:175],v[188:191],a[36:39]
ds_read_b64_tr_b16 a[154:155],v2 offset:25632
v_mfma_f32_16x16x32_bf16 a[36:39],a[168:171],v[180:183],a[36:39]
v_mfma_f32_16x16x32_bf16 a[36:39],a[164:167],v[172:175],a[36:39]
v_mfma_f32_16x16x32_bf16 a[36:39],a[160:163],v[164:167],a[36:39]
v_mfma_f32_16x16x32_bf16 a[40:43],a[160:163],v[196:199],a[40:43]
buffer_load_dwordx4 v[116:119],v6,s[28:31],s79 offen offset:1024 sc1
v_mfma_f32_16x16x32_bf16 a[40:43],a[164:167],v[204:207],a[40:43]
v_mfma_f32_16x16x32_bf16 a[40:43],a[168:171],v[212:215],a[40:43]
v_mfma_f32_16x16x32_bf16 a[40:43],a[172:175],v[220:223],a[40:43]
v_mfma_f32_16x16x32_bf16 a[44:47],a[172:175],v[252:255],a[44:47]
ds_read_b64_tr_b16 a[172:173],v1 offset:30784
ds_read_b64_tr_b16 a[174:175],v1 offset:35904
v_mfma_f32_16x16x32_bf16 a[44:47],a[168:171],v[244:247],a[44:47]
buffer_load_dwordx4 v[120:123],v6,s[28:31],s79 offen offset:2048 sc1
v_mfma_f32_16x16x32_bf16 a[44:47],a[164:167],v[236:239],a[44:47]
ds_read_b64_tr_b16 a[164:165],v1 offset:10304
ds_read_b64_tr_b16 a[166:167],v1 offset:15424
v_mfma_f32_16x16x32_bf16 a[44:47],a[160:163],v[228:231],a[44:47]
s_waitcnt lgkmcnt(4)
ds_read_b64_tr_b16 a[160:161],v1 offset:64
v_mfma_f32_16x16x32_bf16 a[48:51],a[176:179],v[132:135],a[48:51]
ds_read_b64_tr_b16 a[162:163],v1 offset:5184
v_mfma_f32_16x16x32_bf16 a[48:51],a[180:183],v[140:143],a[48:51]
ds_read_b64_tr_b16 a[168:169],v1 offset:20544
v_mfma_f32_16x16x32_bf16 a[48:51],a[184:187],v[148:151],a[48:51]
buffer_load_dwordx4 v[124:127],v6,s[28:31],s79 offen offset:3072 sc1
v_mfma_f32_16x16x32_bf16 a[48:51],a[188:191],v[156:159],a[48:51]
ds_read_b64_tr_b16 a[170:171],v1 offset:25664
v_mfma_f32_16x16x32_bf16 a[52:55],a[188:191],v[188:191],a[52:55]
s_add_u32 m0,s82,122880
v_mfma_f32_16x16x32_bf16 a[52:55],a[184:187],v[180:183],a[52:55]
v_mfma_f32_16x16x32_bf16 a[52:55],a[180:183],v[172:175],a[52:55]
v_mfma_f32_16x16x32_bf16 a[52:55],a[176:179],v[164:167],a[52:55]
buffer_load_dwordx4 v7,s[72:75],s80 offen lds
v_mfma_f32_16x16x32_bf16 a[56:59],a[176:179],v[196:199],a[56:59]
v_mfma_f32_16x16x32_bf16 a[56:59],a[180:183],v[204:207],a[56:59]
v_mfma_f32_16x16x32_bf16 a[56:59],a[184:187],v[212:215],a[56:59]
v_mfma_f32_16x16x32_bf16 a[56:59],a[188:191],v[220:223],a[56:59]
v_mfma_f32_16x16x32_bf16 a[60:63],a[188:191],v[252:255],a[60:63]
buffer_load_dwordx4 v8,s[72:75],s80 offen offset:1024 lds
v_mfma_f32_16x16x32_bf16 a[60:63],a[184:187],v[244:247],a[60:63]
ds_read_b64_tr_b16 a[184:185],v2 offset:20576
ds_read_b64_tr_b16 a[186:187],v2 offset:25696
v_mfma_f32_16x16x32_bf16 a[60:63],a[180:183],v[236:239],a[60:63]
ds_read_b64_tr_b16 a[180:181],v2 offset:10336
v_mfma_f32_16x16x32_bf16 a[60:63],a[176:179],v[228:231],a[60:63]
ds_read_b64_tr_b16 a[176:177],v2 offset:96
v_mfma_f32_16x16x32_bf16 a[64:67],a[96:99],v[132:135],a[64:67]
ds_read_b64_tr_b16 a[178:179],v2 offset:5216
v_mfma_f32_16x16x32_bf16 a[64:67],a[100:103],v[140:143],a[64:67]
buffer_load_dwordx4 v9,s[72:75],s80 offen offset:2048 lds
v_mfma_f32_16x16x32_bf16 a[64:67],a[104:107],v[148:151],a[64:67]
ds_read_b64_tr_b16 a[182:183],v2 offset:15456
ds_read_b64_tr_b16 a[188:189],v2 offset:30816
v_mfma_f32_16x16x32_bf16 a[64:67],a[108:111],v[156:159],a[64:67]
ds_read_b64_tr_b16 a[190:191],v2 offset:35936
v_mfma_f32_16x16x32_bf16 a[68:71],a[108:111],v[188:191],a[68:71]
v_mfma_f32_16x16x32_bf16 a[68:71],a[104:107],v[180:183],a[68:71]
v_mfma_f32_16x16x32_bf16 a[68:71],a[100:103],v[172:175],a[68:71]
buffer_load_dwordx4 v10,s[72:75],s80 offen offset:3072 lds
v_mfma_f32_16x16x32_bf16 a[68:71],a[96:99],v[164:167],a[68:71]
v_mfma_f32_16x16x32_bf16 a[72:75],a[96:99],v[196:199],a[72:75]
s_add_u32 m0,s82,126976
v_mfma_f32_16x16x32_bf16 a[72:75],a[100:103],v[204:207],a[72:75]
v_mfma_f32_16x16x32_bf16 a[72:75],a[104:107],v[212:215],a[72:75]
s_cmp_ge_u32 2,s71
s_cselect_b32 s76,7,5
s_lshl_b32 s76,s76,16
v_mfma_f32_16x16x32_bf16 a[72:75],a[108:111],v[220:223],a[72:75]
s_add_u32 s77,s76,4096
buffer_load_dwordx4 v11,s[72:75],s80 offen lds
v_mfma_f32_16x16x32_bf16 a[76:79],a[108:111],v[252:255],a[76:79]
s_add_u32 s78,s76,8192
s_add_u32 s79,s76,12288
s_max_i32 s76,s76,s18
v_mfma_f32_16x16x32_bf16 a[76:79],a[104:107],v[244:247],a[76:79]
s_add_u32 m0,s82,128000
s_max_i32 s77,s77,s19
ds_read_b64_tr_b16 a[104:105],v1 offset:20608
v_mfma_f32_16x16x32_bf16 a[76:79],a[100:103],v[236:239],a[76:79]
s_max_i32 s78,s78,s100
ds_read_b64_tr_b16 a[100:101],v1 offset:10368
)A"
R"A(ds_read_b64_tr_b16 a[102:103],v1 offset:15488
v_mfma_f32_16x16x32_bf16 a[76:79],a[96:99],v[228:231],a[76:79]
s_max_i32 s79,s79,s101
ds_read_b64_tr_b16 a[96:97],v1 offset:128
v_mfma_f32_16x16x32_bf16 a[80:83],a[112:115],v[132:135],a[80:83]
buffer_load_dwordx4 v7,s[72:75],s83 offen lds
v_mfma_f32_16x16x32_bf16 a[80:83],a[116:119],v[140:143],a[80:83]
ds_read_b64_tr_b16 a[98:99],v1 offset:5248
ds_read_b64_tr_b16 a[106:107],v1 offset:25728
v_mfma_f32_16x16x32_bf16 a[80:83],a[120:123],v[148:151],a[80:83]
ds_read_b64_tr_b16 a[108:109],v1 offset:30848
v_mfma_f32_16x16x32_bf16 a[80:83],a[124:127],v[156:159],a[80:83]
ds_read_b64_tr_b16 a[110:111],v1 offset:35968
v_mfma_f32_16x16x32_bf16 a[84:87],a[124:127],v[188:191],a[84:87]
v_mfma_f32_16x16x32_bf16 a[84:87],a[120:123],v[180:183],a[84:87]
buffer_load_dwordx4 v8,s[72:75],s83 offen offset:1024 lds
v_mfma_f32_16x16x32_bf16 a[84:87],a[116:119],v[172:175],a[84:87]
v_mfma_f32_16x16x32_bf16 a[84:87],a[112:115],v[164:167],a[84:87]
v_mfma_f32_16x16x32_bf16 a[88:91],a[112:115],v[196:199],a[88:91]
v_mfma_f32_16x16x32_bf16 a[88:91],a[116:119],v[204:207],a[88:91]
v_mfma_f32_16x16x32_bf16 a[88:91],a[120:123],v[212:215],a[88:91]
buffer_load_dwordx4 v9,s[72:75],s83 offen offset:2048 lds
v_mfma_f32_16x16x32_bf16 a[88:91],a[124:127],v[220:223],a[88:91]
v_mfma_f32_16x16x32_bf16 a[92:95],a[124:127],v[252:255],a[92:95]
ds_read_b64_tr_b16 a[124:125],v2 offset:30880
ds_read_b64_tr_b16 a[126:127],v2 offset:36000
v_mfma_f32_16x16x32_bf16 a[92:95],a[120:123],v[244:247],a[92:95]
ds_read_b64_tr_b16 a[120:121],v2 offset:20640
v_mfma_f32_16x16x32_bf16 a[92:95],a[116:119],v[236:239],a[92:95]
ds_read_b64_tr_b16 a[116:117],v2 offset:10400
v_mfma_f32_16x16x32_bf16 a[92:95],a[112:115],v[228:231],a[92:95]
buffer_load_dwordx4 v10,s[72:75],s83 offen offset:3072 lds
v_mfma_f32_16x16x32_bf16 a[192:195],a[128:131],v[128:131],a[192:195]
ds_read_b64_tr_b16 a[112:113],v2 offset:160
ds_read_b64_tr_b16 a[114:115],v2 offset:5280
v_mfma_f32_16x16x32_bf16 a[192:195],a[132:135],v[160:163],a[192:195]
s_add_u32 m0,s82,132096
ds_read_b64_tr_b16 a[118:119],v2 offset:15520
v_mfma_f32_16x16x32_bf16 a[192:195],a[136:139],v[192:195],a[192:195]
ds_read_b64_tr_b16 a[122:123],v2 offset:25760
v_mfma_f32_16x16x32_bf16 a[192:195],a[140:143],v[224:227],a[192:195]
buffer_load_dwordx4 v[132:135],v6,s[28:31],s76 offen sc1
v_mfma_f32_16x16x32_bf16 a[196:199],a[140:143],v[232:235],a[196:199]
v_mfma_f32_16x16x32_bf16 a[196:199],a[136:139],v[200:203],a[196:199]
v_mfma_f32_16x16x32_bf16 a[196:199],a[132:135],v[168:171],a[196:199]
v_mfma_f32_16x16x32_bf16 a[196:199],a[128:131],v[136:139],a[196:199]
s_waitcnt vmcnt(14)
buffer_load_dwordx4 v11,s[72:75],s83 offen lds
v_mfma_f32_16x16x32_bf16 a[200:203],a[128:131],v[144:147],a[200:203]
ds_read_b64_tr_b16 a[128:129],v1 offset:192
ds_read_b64_tr_b16 a[130:131],v1 offset:5312
v_mfma_f32_16x16x32_bf16 a[200:203],a[132:135],v[176:179],a[200:203]
ds_read_b64_tr_b16 a[132:133],v1 offset:10432
v_mfma_f32_16x16x32_bf16 a[200:203],a[136:139],v[208:211],a[200:203]
ds_read_b64_tr_b16 a[134:135],v1 offset:15552
v_mfma_f32_16x16x32_bf16 a[200:203],a[140:143],v[240:243],a[200:203]
buffer_load_dwordx4 v[140:143],v6,s[28:31],s76 offen offset:1024 sc1
v_mfma_f32_16x16x32_bf16 a[208:211],a[144:147],v[128:131],a[208:211]
ds_read_b64_tr_b16 a[136:137],v1 offset:20672
ds_read_b64_tr_b16 a[138:139],v1 offset:25792
v_mfma_f32_16x16x32_bf16 a[208:211],a[148:151],v[160:163],a[208:211]
ds_read_b64_tr_b16 a[140:141],v1 offset:30912
v_mfma_f32_16x16x32_bf16 a[208:211],a[152:155],v[192:195],a[208:211]
ds_read_b64_tr_b16 a[142:143],v1 offset:36032
v_mfma_f32_16x16x32_bf16 a[208:211],a[156:159],v[224:227],a[208:211]
buffer_load_dwordx4 v[148:151],v6,s[28:31],s76 offen offset:2048 sc1
v_mfma_f32_16x16x32_bf16 a[212:215],a[156:159],v[232:235],a[212:215]
v_mfma_f32_16x16x32_bf16 a[212:215],a[152:155],v[200:203],a[212:215]
v_mfma_f32_16x16x32_bf16 a[212:215],a[148:151],v[168:171],a[212:215]
v_mfma_f32_16x16x32_bf16 a[212:215],a[144:147],v[136:139],a[212:215]
buffer_load_dwordx4 v[156:159],v6,s[28:31],s76 offen offset:3072 sc1
v_mfma_f32_16x16x32_bf16 a[216:219],a[144:147],v[144:147],a[216:219]
s_mul_i32 s76,s70,12
ds_read_b64_tr_b16 a[144:145],v2 offset:224
ds_read_b64_tr_b16 a[146:147],v2 offset:5344
v_mfma_f32_16x16x32_bf16 a[216:219],a[148:151],v[176:179],a[216:219]
s_add_u32 s76,s76,0
s_add_u32 s76,s76,s40
ds_read_b64_tr_b16 a[148:149],v2 offset:10464
v_mfma_f32_16x16x32_bf16 a[216:219],a[152:155],v[208:211],a[216:219]
s_mul_i32 s76,s76,5120
s_max_i32 s76,s76,s101
ds_read_b64_tr_b16 a[150:151],v2 offset:15584
v_mfma_f32_16x16x32_bf16 a[216:219],a[156:159],v[240:243],a[216:219]
s_waitcnt lgkmcnt(4)
buffer_load_dwordx4 v[164:167],v6,s[28:31],s77 offen sc1
v_mfma_f32_16x16x32_bf16 a[224:227],a[160:163],v[128:131],a[224:227]
ds_read_b64_tr_b16 a[152:153],v2 offset:20704
ds_read_b64_tr_b16 a[154:155],v2 offset:25824
v_mfma_f32_16x16x32_bf16 a[224:227],a[164:167],v[160:163],a[224:227]
ds_read_b64_tr_b16 a[156:157],v2 offset:30944
v_mfma_f32_16x16x32_bf16 a[224:227],a[168:171],v[192:195],a[224:227]
ds_read_b64_tr_b16 a[158:159],v2 offset:36064
v_mfma_f32_16x16x32_bf16 a[224:227],a[172:175],v[224:227],a[224:227]
buffer_load_dwordx4 v[172:175],v6,s[28:31],s77 offen offset:1024 sc1
v_mfma_f32_16x16x32_bf16 a[228:231],a[172:175],v[232:235],a[228:231]
v_mfma_f32_16x16x32_bf16 a[228:231],a[168:171],v[200:203],a[228:231]
v_mfma_f32_16x16x32_bf16 a[228:231],a[164:167],v[168:171],a[228:231]
v_mfma_f32_16x16x32_bf16 a[228:231],a[160:163],v[136:139],a[228:231]
v_mfma_f32_16x16x32_bf16 a[232:235],a[160:163],v[144:147],a[232:235]
buffer_load_dwordx4 v[180:183],v6,s[28:31],s77 offen offset:2048 sc1
v_mfma_f32_16x16x32_bf16 a[232:235],a[164:167],v[176:179],a[232:235]
ds_read_b64_tr_b16 a[160:161],v1 offset:256
ds_read_b64_tr_b16 a[162:163],v1 offset:5376
v_mfma_f32_16x16x32_bf16 a[232:235],a[168:171],v[208:211],a[232:235]
ds_read_b64_tr_b16 a[164:165],v1 offset:10496
v_mfma_f32_16x16x32_bf16 a[232:235],a[172:175],v[240:243],a[232:235]
ds_read_b64_tr_b16 a[166:167],v1 offset:15616
v_mfma_f32_16x16x32_bf16 a[240:243],a[176:179],v[128:131],a[240:243]
ds_read_b64_tr_b16 a[168:169],v1 offset:20736
v_mfma_f32_16x16x32_bf16 a[240:243],a[180:183],v[160:163],a[240:243]
buffer_load_dwordx4 v[188:191],v6,s[28:31],s77 offen offset:3072 sc1
v_mfma_f32_16x16x32_bf16 a[240:243],a[184:187],v[192:195],a[240:243]
s_mul_i32 s77,s70,12
ds_read_b64_tr_b16 a[170:171],v1 offset:25856
ds_read_b64_tr_b16 a[172:173],v1 offset:30976
v_mfma_f32_16x16x32_bf16 a[240:243],a[188:191],v[224:227],a[240:243]
s_add_u32 s77,s77,4
s_add_u32 s77,s77,s40
ds_read_b64_tr_b16 a[174:175],v1 offset:36096
v_mfma_f32_16x16x32_bf16 a[244:247],a[188:191],v[232:235],a[244:247]
s_mul_i32 s77,s77,5120
s_max_i32 s77,s77,s101
v_mfma_f32_16x16x32_bf16 a[244:247],a[184:187],v[200:203],a[244:247]
v_mfma_f32_16x16x32_bf16 a[244:247],a[180:183],v[168:171],a[244:247]
buffer_load_dwordx4 v[196:199],v6,s[28:31],s78 offen sc1
v_mfma_f32_16x16x32_bf16 a[244:247],a[176:179],v[136:139],a[244:247]
v_mfma_f32_16x16x32_bf16 a[248:251],a[176:179],v[144:147],a[248:251]
ds_read_b64_tr_b16 a[176:177],v2 offset:288
ds_read_b64_tr_b16 a[178:179],v2 offset:5408
v_mfma_f32_16x16x32_bf16 a[248:251],a[180:183],v[176:179],a[248:251]
ds_read_b64_tr_b16 a[180:181],v2 offset:10528
v_mfma_f32_16x16x32_bf16 a[248:251],a[184:187],v[208:211],a[248:251]
ds_read_b64_tr_b16 a[182:183],v2 offset:15648
v_mfma_f32_16x16x32_bf16 a[248:251],a[188:191],v[240:243],a[248:251]
buffer_load_dwordx4 v[204:207],v6,s[28:31],s78 offen offset:1024 sc1
v_mfma_f32_16x16x32_bf16 a[0:3],a[96:99],v[128:131],a[0:3]
ds_read_b64_tr_b16 a[184:185],v2 offset:20768
ds_read_b64_tr_b16 a[186:187],v2 offset:25888
v_mfma_f32_16x16x32_bf16 a[0:3],a[100:103],v[160:163],a[0:3]
ds_read_b64_tr_b16 a[188:189],v2 offset:31008
v_mfma_f32_16x16x32_bf16 a[0:3],a[104:107],v[192:195],a[0:3]
ds_read_b64_tr_b16 a[190:191],v2 offset:36128
v_mfma_f32_16x16x32_bf16 a[0:3],a[108:111],v[224:227],a[0:3]
v_mfma_f32_16x16x32_bf16 a[4:7],a[108:111],v[232:235],a[4:7]
buffer_load_dwordx4 v[212:215],v6,s[28:31],s78 offen offset:2048 sc1
v_mfma_f32_16x16x32_bf16 a[4:7],a[104:107],v[200:203],a[4:7]
v_mfma_f32_16x16x32_bf16 a[4:7],a[100:103],v[168:171],a[4:7]
v_mfma_f32_16x16x32_bf16 a[4:7],a[96:99],v[136:139],a[4:7]
v_mfma_f32_16x16x32_bf16 a[8:11],a[96:99],v[144:147],a[8:11]
v_mfma_f32_16x16x32_bf16 a[8:11],a[100:103],v[176:179],a[8:11]
buffer_load_dwordx4 v[220:223],v6,s[28:31],s78 offen offset:3072 sc1
v_mfma_f32_16x16x32_bf16 a[8:11],a[104:107],v[208:211],a[8:11]
s_mul_i32 s78,s70,12
s_add_u32 s78,s78,8
s_add_u32 s78,s78,s40
v_mfma_f32_16x16x32_bf16 a[8:11],a[108:111],v[240:243],a[8:11]
s_mul_i32 s78,s78,5120
s_max_i32 s78,s78,s101
v_mfma_f32_16x16x32_bf16 a[16:19],a[112:115],v[128:131],a[16:19]
v_mfma_f32_16x16x32_bf16 a[16:19],a[116:119],v[160:163],a[16:19]
v_mfma_f32_16x16x32_bf16 a[16:19],a[120:123],v[192:195],a[16:19]
buffer_load_dwordx4 v[228:231],v6,s[28:31],s79 offen sc1
v_mfma_f32_16x16x32_bf16 a[16:19],a[124:127],v[224:227],a[16:19]
v_mfma_f32_16x16x32_bf16 a[20:23],a[124:127],v[232:235],a[20:23]
v_mfma_f32_16x16x32_bf16 a[20:23],a[120:123],v[200:203],a[20:23]
v_mfma_f32_16x16x32_bf16 a[20:23],a[116:119],v[168:171],a[20:23]
v_mfma_f32_16x16x32_bf16 a[20:23],a[112:115],v[136:139],a[20:23]
buffer_load_dwordx4 v[236:239],v6,s[28:31],s79 offen offset:1024 sc1
v_mfma_f32_16x16x32_bf16 a[24:27],a[112:115],v[144:147],a[24:27]
v_mfma_f32_16x16x32_bf16 a[24:27],a[116:119],v[176:179],a[24:27]
v_mfma_f32_16x16x32_bf16 a[24:27],a[120:123],v[208:211],a[24:27]
v_mfma_f32_16x16x32_bf16 a[24:27],a[124:127],v[240:243],a[24:27]
v_mfma_f32_16x16x32_bf16 a[32:35],a[128:131],v[128:131],a[32:35]
buffer_load_dwordx4 v[244:247],v6,s[28:31],s79 offen offset:2048 sc1
v_mfma_f32_16x16x32_bf16 a[32:35],a[132:135],v[160:163],a[32:35]
v_mfma_f32_16x16x32_bf16 a[32:35],a[136:139],v[192:195],a[32:35]
v_mfma_f32_16x16x32_bf16 a[32:35],a[140:143],v[224:227],a[32:35]
v_mfma_f32_16x16x32_bf16 a[36:39],a[140:143],v[232:235],a[36:39]
v_mfma_f32_16x16x32_bf16 a[36:39],a[136:139],v[200:203],a[36:39]
buffer_load_dwordx4 v[252:255],v6,s[28:31],s79 offen offset:3072 sc1
v_mfma_f32_16x16x32_bf16 a[36:39],a[132:135],v[168:171],a[36:39]
v_mfma_f32_16x16x32_bf16 a[36:39],a[128:131],v[136:139],a[36:39]
v_mfma_f32_16x16x32_bf16 a[40:43],a[128:131],v[144:147],a[40:43]
v_mfma_f32_16x16x32_bf16 a[40:43],a[132:135],v[176:179],a[40:43]
v_mfma_f32_16x16x32_bf16 a[40:43],a[136:139],v[208:211],a[40:43]
s_waitcnt vmcnt(15)
v_mfma_f32_16x16x32_bf16 a[40:43],a[140:143],v[240:243],a[40:43]
s_barrier
ds_read_b64_tr_b16 a[96:97],v42
ds_read_b64_tr_b16 a[98:99],v42 offset:5120
s_waitcnt lgkmcnt(2)
v_mfma_f32_16x16x32_bf16 a[48:51],a[144:147],v[128:131],a[48:51]
ds_read_b64_tr_b16 a[100:101],v42 offset:10240
v_mfma_f32_16x16x32_bf16 a[48:51],a[148:151],v[160:163],a[48:51]
ds_read_b64_tr_b16 a[102:103],v42 offset:15360
v_mfma_f32_16x16x32_bf16 a[48:51],a[152:155],v[192:195],a[48:51]
ds_read_b64_tr_b16 a[104:105],v42 offset:20480
v_mfma_f32_16x16x32_bf16 a[48:51],a[156:159],v[224:227],a[48:51]
ds_read_b64_tr_b16 a[106:107],v42 offset:25600
ds_read_b64_tr_b16 a[108:109],v42 offset:30720
v_mfma_f32_16x16x32_bf16 a[52:55],a[156:159],v[232:235],a[52:55]
ds_read_b64_tr_b16 a[110:111],v42 offset:35840
v_mfma_f32_16x16x32_bf16 a[52:55],a[152:155],v[200:203],a[52:55]
ds_read_b64_tr_b16 a[112:113],v44
v_mfma_f32_16x16x32_bf16 a[52:55],a[148:151],v[168:171],a[52:55]
ds_read_b64_tr_b16 a[114:115],v44 offset:5120
v_mfma_f32_16x16x32_bf16 a[52:55],a[144:147],v[136:139],a[52:55]
ds_read_b64_tr_b16 a[116:117],v44 offset:10240
ds_read_b64_tr_b16 a[118:119],v44 offset:15360
v_mfma_f32_16x16x32_bf16 a[56:59],a[144:147],v[144:147],a[56:59]
ds_read_b64_tr_b16 a[120:121],v44 offset:20480
v_mfma_f32_16x16x32_bf16 a[56:59],a[148:151],v[176:179],a[56:59]
ds_read_b64_tr_b16 a[122:123],v44 offset:25600
v_mfma_f32_16x16x32_bf16 a[56:59],a[152:155],v[208:211],a[56:59]
ds_read_b64_tr_b16 a[124:125],v44 offset:30720
v_mfma_f32_16x16x32_bf16 a[56:59],a[156:159],v[240:243],a[56:59]
)A"
R"A(ds_read_b64_tr_b16 a[126:127],v44 offset:35840
ds_read_b64_tr_b16 a[128:129],v43 offset:32
v_mfma_f32_16x16x32_bf16 a[64:67],a[160:163],v[128:131],a[64:67]
ds_read_b64_tr_b16 a[130:131],v43 offset:5152
v_mfma_f32_16x16x32_bf16 a[64:67],a[164:167],v[160:163],a[64:67]
ds_read_b64_tr_b16 a[132:133],v43 offset:10272
v_mfma_f32_16x16x32_bf16 a[64:67],a[168:171],v[192:195],a[64:67]
ds_read_b64_tr_b16 a[134:135],v43 offset:15392
v_mfma_f32_16x16x32_bf16 a[64:67],a[172:175],v[224:227],a[64:67]
ds_read_b64_tr_b16 a[136:137],v43 offset:20512
ds_read_b64_tr_b16 a[138:139],v43 offset:25632
v_mfma_f32_16x16x32_bf16 a[68:71],a[172:175],v[232:235],a[68:71]
ds_read_b64_tr_b16 a[140:141],v43 offset:30752
v_mfma_f32_16x16x32_bf16 a[68:71],a[168:171],v[200:203],a[68:71]
ds_read_b64_tr_b16 a[142:143],v43 offset:35872
v_mfma_f32_16x16x32_bf16 a[68:71],a[164:167],v[168:171],a[68:71]
ds_read_b64_tr_b16 a[144:145],v45 offset:32
v_mfma_f32_16x16x32_bf16 a[68:71],a[160:163],v[136:139],a[68:71]
ds_read_b64_tr_b16 a[146:147],v45 offset:5152
ds_read_b64_tr_b16 a[148:149],v45 offset:10272
v_mfma_f32_16x16x32_bf16 a[72:75],a[160:163],v[144:147],a[72:75]
ds_read_b64_tr_b16 a[150:151],v45 offset:15392
v_mfma_f32_16x16x32_bf16 a[72:75],a[164:167],v[176:179],a[72:75]
ds_read_b64_tr_b16 a[152:153],v45 offset:20512
v_mfma_f32_16x16x32_bf16 a[72:75],a[168:171],v[208:211],a[72:75]
ds_read_b64_tr_b16 a[154:155],v45 offset:25632
v_mfma_f32_16x16x32_bf16 a[72:75],a[172:175],v[240:243],a[72:75]
ds_read_b64_tr_b16 a[156:157],v45 offset:30752
ds_read_b64_tr_b16 a[158:159],v45 offset:35872
v_mfma_f32_16x16x32_bf16 a[80:83],a[176:179],v[128:131],a[80:83]
ds_read_b64_tr_b16 a[160:161],v42 offset:64
v_mfma_f32_16x16x32_bf16 a[80:83],a[180:183],v[160:163],a[80:83]
ds_read_b64_tr_b16 a[162:163],v42 offset:5184
v_mfma_f32_16x16x32_bf16 a[80:83],a[184:187],v[192:195],a[80:83]
ds_read_b64_tr_b16 a[164:165],v42 offset:10304
v_mfma_f32_16x16x32_bf16 a[80:83],a[188:191],v[224:227],a[80:83]
ds_read_b64_tr_b16 a[166:167],v42 offset:15424
ds_read_b64_tr_b16 a[168:169],v42 offset:20544
v_mfma_f32_16x16x32_bf16 a[84:87],a[188:191],v[232:235],a[84:87]
ds_read_b64_tr_b16 a[170:171],v42 offset:25664
v_mfma_f32_16x16x32_bf16 a[84:87],a[184:187],v[200:203],a[84:87]
ds_read_b64_tr_b16 a[172:173],v42 offset:30784
v_mfma_f32_16x16x32_bf16 a[84:87],a[180:183],v[168:171],a[84:87]
ds_read_b64_tr_b16 a[174:175],v42 offset:35904
v_mfma_f32_16x16x32_bf16 a[84:87],a[176:179],v[136:139],a[84:87]
v_mfma_f32_16x16x32_bf16 a[88:91],a[176:179],v[144:147],a[88:91]
ds_read_b64_tr_b16 a[176:177],v44 offset:64
ds_read_b64_tr_b16 a[178:179],v44 offset:5184
v_mfma_f32_16x16x32_bf16 a[88:91],a[180:183],v[176:179],a[88:91]
ds_read_b64_tr_b16 a[180:181],v44 offset:10304
v_mfma_f32_16x16x32_bf16 a[88:91],a[184:187],v[208:211],a[88:91]
ds_read_b64_tr_b16 a[182:183],v44 offset:15424
v_mfma_f32_16x16x32_bf16 a[88:91],a[188:191],v[240:243],a[88:91]
s_waitcnt lgkmcnt(7)
ds_read_b64_tr_b16 a[184:185],v44 offset:20544
v_mfma_f32_16x16x32_bf16 a[192:195],a[96:99],v[64:67],a[192:195]
ds_read_b64_tr_b16 a[186:187],v44 offset:25664
ds_read_b64_tr_b16 a[188:189],v44 offset:30784
v_mfma_f32_16x16x32_bf16 a[192:195],a[100:103],v[68:71],a[192:195]
ds_read_b64_tr_b16 a[190:191],v44 offset:35904
v_mfma_f32_16x16x32_bf16 a[192:195],a[104:107],v[72:75],a[192:195]
v_mfma_f32_16x16x32_bf16 a[192:195],a[108:111],v[76:79],a[192:195]
v_mfma_f32_16x16x32_bf16 a[196:199],a[108:111],v[92:95],a[196:199]
v_mfma_f32_16x16x32_bf16 a[196:199],a[104:107],v[88:91],a[196:199]
v_mfma_f32_16x16x32_bf16 a[196:199],a[100:103],v[84:87],a[196:199]
v_mfma_f32_16x16x32_bf16 a[196:199],a[96:99],v[80:83],a[196:199]
v_mfma_f32_16x16x32_bf16 a[200:203],a[96:99],v[96:99],a[200:203]
ds_read_b64_tr_b16 a[96:97],v43 offset:96
ds_read_b64_tr_b16 a[98:99],v43 offset:5216
v_mfma_f32_16x16x32_bf16 a[200:203],a[100:103],v[100:103],a[200:203]
ds_read_b64_tr_b16 a[100:101],v43 offset:10336
v_mfma_f32_16x16x32_bf16 a[200:203],a[104:107],v[104:107],a[200:203]
ds_read_b64_tr_b16 a[102:103],v43 offset:15456
v_mfma_f32_16x16x32_bf16 a[200:203],a[108:111],v[108:111],a[200:203]
ds_read_b64_tr_b16 a[104:105],v43 offset:20576
v_mfma_f32_16x16x32_bf16 a[192:195],a[112:115],v[132:135],a[192:195]
s_waitcnt vmcnt(3)
ds_read_b64_tr_b16 a[106:107],v43 offset:25696
ds_read_b64_tr_b16 a[108:109],v43 offset:30816
v_mfma_f32_16x16x32_bf16 a[192:195],a[116:119],v[140:143],a[192:195]
ds_read_b64_tr_b16 a[110:111],v43 offset:35936
v_mfma_f32_16x16x32_bf16 a[192:195],a[120:123],v[148:151],a[192:195]
v_mfma_f32_16x16x32_bf16 a[192:195],a[124:127],v[156:159],a[192:195]
v_mfma_f32_16x16x32_bf16 a[196:199],a[124:127],v[188:191],a[196:199]
v_mfma_f32_16x16x32_bf16 a[196:199],a[120:123],v[180:183],a[196:199]
v_mfma_f32_16x16x32_bf16 a[196:199],a[116:119],v[172:175],a[196:199]
v_mfma_f32_16x16x32_bf16 a[196:199],a[112:115],v[164:167],a[196:199]
v_mfma_f32_16x16x32_bf16 a[200:203],a[112:115],v[196:199],a[200:203]
ds_read_b64_tr_b16 a[112:113],v45 offset:96
ds_read_b64_tr_b16 a[114:115],v45 offset:5216
v_mfma_f32_16x16x32_bf16 a[200:203],a[116:119],v[204:207],a[200:203]
v_accvgpr_read_b32 v128,a192
v_accvgpr_read_b32 v129,a193
v_mfma_f32_16x16x32_bf16 a[200:203],a[120:123],v[212:215],a[200:203]
v_cvt_pk_bf16_f32 v128,v128,v129
v_accvgpr_read_b32 v130,a194
v_mfma_f32_16x16x32_bf16 a[200:203],a[124:127],v[220:223],a[200:203]
v_accvgpr_read_b32 v131,a195
v_cvt_pk_bf16_f32 v129,v130,v131
v_mfma_f32_16x16x32_bf16 a[208:211],a[128:131],v[64:67],a[208:211]
ds_read_b64_tr_b16 a[116:117],v45 offset:10336
ds_read_b64_tr_b16 a[118:119],v45 offset:15456
v_mfma_f32_16x16x32_bf16 a[208:211],a[132:135],v[68:71],a[208:211]
v_accvgpr_read_b32 v136,a196
v_accvgpr_read_b32 v137,a197
v_mfma_f32_16x16x32_bf16 a[208:211],a[136:139],v[72:75],a[208:211]
v_cvt_pk_bf16_f32 v136,v136,v137
v_accvgpr_read_b32 v138,a198
v_mfma_f32_16x16x32_bf16 a[208:211],a[140:143],v[76:79],a[208:211]
v_accvgpr_read_b32 v139,a199
v_cvt_pk_bf16_f32 v137,v138,v139
v_mfma_f32_16x16x32_bf16 a[212:215],a[140:143],v[92:95],a[212:215]
ds_read_b64_tr_b16 a[120:121],v45 offset:20576
ds_read_b64_tr_b16 a[122:123],v45 offset:25696
v_mfma_f32_16x16x32_bf16 a[212:215],a[136:139],v[88:91],a[212:215]
ds_read_b64_tr_b16 a[124:125],v45 offset:30816
v_accvgpr_read_b32 v144,a200
v_mfma_f32_16x16x32_bf16 a[212:215],a[132:135],v[84:87],a[212:215]
ds_read_b64_tr_b16 a[126:127],v45 offset:35936
v_accvgpr_read_b32 v145,a201
v_mfma_f32_16x16x32_bf16 a[212:215],a[128:131],v[80:83],a[212:215]
v_cvt_pk_bf16_f32 v144,v144,v145
v_accvgpr_read_b32 v146,a202
v_mfma_f32_16x16x32_bf16 a[216:219],a[128:131],v[96:99],a[216:219]
v_accvgpr_read_b32 v147,a203
v_cvt_pk_bf16_f32 v145,v146,v147
v_mfma_f32_16x16x32_bf16 a[216:219],a[132:135],v[100:103],a[216:219]
ds_read_b64_tr_b16 a[128:129],v42 offset:128
ds_read_b64_tr_b16 a[130:131],v42 offset:5248
v_mfma_f32_16x16x32_bf16 a[216:219],a[136:139],v[104:107],a[216:219]
ds_read_b64_tr_b16 a[132:133],v42 offset:10368
v_mfma_f32_16x16x32_bf16 a[216:219],a[140:143],v[108:111],a[216:219]
ds_read_b64_tr_b16 a[134:135],v42 offset:15488
v_mfma_f32_16x16x32_bf16 a[208:211],a[144:147],v[132:135],a[208:211]
ds_read_b64_tr_b16 a[136:137],v42 offset:20608
v_mfma_f32_16x16x32_bf16 a[208:211],a[148:151],v[140:143],a[208:211]
ds_read_b64_tr_b16 a[138:139],v42 offset:25728
ds_read_b64_tr_b16 a[140:141],v42 offset:30848
v_mfma_f32_16x16x32_bf16 a[208:211],a[152:155],v[148:151],a[208:211]
ds_read_b64_tr_b16 a[142:143],v42 offset:35968
v_mfma_f32_16x16x32_bf16 a[208:211],a[156:159],v[156:159],a[208:211]
v_mfma_f32_16x16x32_bf16 a[212:215],a[156:159],v[188:191],a[212:215]
v_mfma_f32_16x16x32_bf16 a[212:215],a[152:155],v[180:183],a[212:215]
v_mfma_f32_16x16x32_bf16 a[212:215],a[148:151],v[172:175],a[212:215]
v_mfma_f32_16x16x32_bf16 a[212:215],a[144:147],v[164:167],a[212:215]
v_mfma_f32_16x16x32_bf16 a[216:219],a[144:147],v[196:199],a[216:219]
ds_read_b64_tr_b16 a[144:145],v44 offset:128
ds_read_b64_tr_b16 a[146:147],v44 offset:5248
v_mfma_f32_16x16x32_bf16 a[216:219],a[148:151],v[204:207],a[216:219]
v_accvgpr_read_b32 v130,a208
v_accvgpr_read_b32 v131,a209
v_mfma_f32_16x16x32_bf16 a[216:219],a[152:155],v[212:215],a[216:219]
v_accvgpr_read_b32 v14,a210
v_accvgpr_read_b32 v15,a211
v_mfma_f32_16x16x32_bf16 a[216:219],a[156:159],v[220:223],a[216:219]
v_cvt_pk_bf16_f32 v130,v130,v131
v_cvt_pk_bf16_f32 v131,v14,v15
v_mfma_f32_16x16x32_bf16 a[224:227],a[160:163],v[64:67],a[224:227]
buffer_store_dwordx4 v[128:131],v6,s[32:35],s84 offen sc1
v_mfma_f32_16x16x32_bf16 a[224:227],a[164:167],v[68:71],a[224:227]
s_waitcnt lgkmcnt(2)
v_accvgpr_read_b32 v138,a212
v_accvgpr_read_b32 v139,a213
v_mfma_f32_16x16x32_bf16 a[224:227],a[168:171],v[72:75],a[224:227]
v_accvgpr_read_b32 v16,a214
v_accvgpr_read_b32 v17,a215
v_mfma_f32_16x16x32_bf16 a[224:227],a[172:175],v[76:79],a[224:227]
v_cvt_pk_bf16_f32 v138,v138,v139
v_cvt_pk_bf16_f32 v139,v16,v17
v_mfma_f32_16x16x32_bf16 a[228:231],a[172:175],v[92:95],a[228:231]
buffer_store_dwordx4 v[136:139],v6,s[32:35],s85 offen sc1
v_mfma_f32_16x16x32_bf16 a[228:231],a[168:171],v[88:91],a[228:231]
v_accvgpr_read_b32 v146,a216
v_accvgpr_read_b32 v147,a217
v_mfma_f32_16x16x32_bf16 a[228:231],a[164:167],v[84:87],a[228:231]
v_accvgpr_read_b32 v12,a218
v_accvgpr_read_b32 v13,a219
v_mfma_f32_16x16x32_bf16 a[228:231],a[160:163],v[80:83],a[228:231]
v_cvt_pk_bf16_f32 v146,v146,v147
v_cvt_pk_bf16_f32 v147,v12,v13
v_mfma_f32_16x16x32_bf16 a[232:235],a[160:163],v[96:99],a[232:235]
buffer_store_dwordx4 v[144:147],v6,s[32:35],s86 offen sc1
v_mfma_f32_16x16x32_bf16 a[232:235],a[164:167],v[100:103],a[232:235]
ds_read_b64_tr_b16 a[148:149],v44 offset:10368
ds_read_b64_tr_b16 a[150:151],v44 offset:15488
v_mfma_f32_16x16x32_bf16 a[232:235],a[168:171],v[104:107],a[232:235]
ds_read_b64_tr_b16 a[152:153],v44 offset:20608
v_mfma_f32_16x16x32_bf16 a[232:235],a[172:175],v[108:111],a[232:235]
ds_read_b64_tr_b16 a[154:155],v44 offset:25728
v_mfma_f32_16x16x32_bf16 a[224:227],a[176:179],v[132:135],a[224:227]
ds_read_b64_tr_b16 a[156:157],v44 offset:30848
v_mfma_f32_16x16x32_bf16 a[224:227],a[180:183],v[140:143],a[224:227]
ds_read_b64_tr_b16 a[158:159],v44 offset:35968
ds_read_b64_tr_b16 a[160:161],v43 offset:160
v_mfma_f32_16x16x32_bf16 a[224:227],a[184:187],v[148:151],a[224:227]
ds_read_b64_tr_b16 a[162:163],v43 offset:5280
v_mfma_f32_16x16x32_bf16 a[224:227],a[188:191],v[156:159],a[224:227]
ds_read_b64_tr_b16 a[164:165],v43 offset:10400
v_mfma_f32_16x16x32_bf16 a[228:231],a[188:191],v[188:191],a[228:231]
ds_read_b64_tr_b16 a[166:167],v43 offset:15520
v_mfma_f32_16x16x32_bf16 a[228:231],a[184:187],v[180:183],a[228:231]
ds_read_b64_tr_b16 a[168:169],v43 offset:20640
ds_read_b64_tr_b16 a[170:171],v43 offset:25760
v_mfma_f32_16x16x32_bf16 a[228:231],a[180:183],v[172:175],a[228:231]
ds_read_b64_tr_b16 a[172:173],v43 offset:30880
v_mfma_f32_16x16x32_bf16 a[228:231],a[176:179],v[164:167],a[228:231]
ds_read_b64_tr_b16 a[174:175],v43 offset:36000
v_mfma_f32_16x16x32_bf16 a[232:235],a[176:179],v[196:199],a[232:235]
ds_read_b64_tr_b16 a[176:177],v45 offset:160
v_mfma_f32_16x16x32_bf16 a[232:235],a[180:183],v[204:207],a[232:235]
v_accvgpr_read_b32 v160,a224
v_accvgpr_read_b32 v161,a225
v_mfma_f32_16x16x32_bf16 a[232:235],a[184:187],v[212:215],a[232:235]
v_cvt_pk_bf16_f32 v160,v160,v161
v_accvgpr_read_b32 v162,a226
v_mfma_f32_16x16x32_bf16 a[232:235],a[188:191],v[220:223],a[232:235]
v_accvgpr_read_b32 v163,a227
v_cvt_pk_bf16_f32 v161,v162,v163
v_mfma_f32_16x16x32_bf16 a[240:243],a[96:99],v[64:67],a[240:243]
ds_read_b64_tr_b16 a[178:179],v45 offset:5280
ds_read_b64_tr_b16 a[180:181],v45 offset:10400
v_mfma_f32_16x16x32_bf16 a[240:243],a[100:103],v[68:71],a[240:243]
v_accvgpr_read_b32 v168,a228
v_accvgpr_read_b32 v169,a229
v_mfma_f32_16x16x32_bf16 a[240:243],a[104:107],v[72:75],a[240:243]
v_cvt_pk_bf16_f32 v168,v168,v169
v_accvgpr_read_b32 v170,a230
v_mfma_f32_16x16x32_bf16 a[240:243],a[108:111],v[76:79],a[240:243]
v_accvgpr_read_b32 v171,a231
v_cvt_pk_bf16_f32 v169,v170,v171
v_mfma_f32_16x16x32_bf16 a[244:247],a[108:111],v[92:95],a[244:247]
)A"
R"A(ds_read_b64_tr_b16 a[182:183],v45 offset:15520
ds_read_b64_tr_b16 a[184:185],v45 offset:20640
v_mfma_f32_16x16x32_bf16 a[244:247],a[104:107],v[88:91],a[244:247]
ds_read_b64_tr_b16 a[186:187],v45 offset:25760
v_accvgpr_read_b32 v176,a232
v_mfma_f32_16x16x32_bf16 a[244:247],a[100:103],v[84:87],a[244:247]
ds_read_b64_tr_b16 a[188:189],v45 offset:30880
v_accvgpr_read_b32 v177,a233
v_mfma_f32_16x16x32_bf16 a[244:247],a[96:99],v[80:83],a[244:247]
ds_read_b64_tr_b16 a[190:191],v45 offset:36000
v_cvt_pk_bf16_f32 v176,v176,v177
v_mfma_f32_16x16x32_bf16 a[248:251],a[96:99],v[96:99],a[248:251]
v_accvgpr_read_b32 v178,a234
v_accvgpr_read_b32 v179,a235
v_mfma_f32_16x16x32_bf16 a[248:251],a[100:103],v[100:103],a[248:251]
ds_read_b64_tr_b16 a[96:97],v42 offset:192
v_cvt_pk_bf16_f32 v177,v178,v179
v_mfma_f32_16x16x32_bf16 a[248:251],a[104:107],v[104:107],a[248:251]
ds_read_b64_tr_b16 a[98:99],v42 offset:5312
ds_read_b64_tr_b16 a[100:101],v42 offset:10432
v_mfma_f32_16x16x32_bf16 a[248:251],a[108:111],v[108:111],a[248:251]
ds_read_b64_tr_b16 a[102:103],v42 offset:15552
v_mfma_f32_16x16x32_bf16 a[240:243],a[112:115],v[132:135],a[240:243]
ds_read_b64_tr_b16 a[104:105],v42 offset:20672
v_mfma_f32_16x16x32_bf16 a[240:243],a[116:119],v[140:143],a[240:243]
ds_read_b64_tr_b16 a[106:107],v42 offset:25792
v_mfma_f32_16x16x32_bf16 a[240:243],a[120:123],v[148:151],a[240:243]
ds_read_b64_tr_b16 a[108:109],v42 offset:30912
ds_read_b64_tr_b16 a[110:111],v42 offset:36032
v_mfma_f32_16x16x32_bf16 a[240:243],a[124:127],v[156:159],a[240:243]
v_mfma_f32_16x16x32_bf16 a[244:247],a[124:127],v[188:191],a[244:247]
v_mfma_f32_16x16x32_bf16 a[244:247],a[120:123],v[180:183],a[244:247]
v_mfma_f32_16x16x32_bf16 a[244:247],a[116:119],v[172:175],a[244:247]
v_mfma_f32_16x16x32_bf16 a[244:247],a[112:115],v[164:167],a[244:247]
v_mfma_f32_16x16x32_bf16 a[248:251],a[112:115],v[196:199],a[248:251]
ds_read_b64_tr_b16 a[112:113],v44 offset:192
ds_read_b64_tr_b16 a[114:115],v44 offset:5312
v_mfma_f32_16x16x32_bf16 a[248:251],a[116:119],v[204:207],a[248:251]
v_accvgpr_read_b32 v162,a240
v_accvgpr_read_b32 v163,a241
v_mfma_f32_16x16x32_bf16 a[248:251],a[120:123],v[212:215],a[248:251]
v_accvgpr_read_b32 v14,a242
v_accvgpr_read_b32 v15,a243
v_mfma_f32_16x16x32_bf16 a[248:251],a[124:127],v[220:223],a[248:251]
v_cvt_pk_bf16_f32 v162,v162,v163
v_cvt_pk_bf16_f32 v163,v14,v15
v_mfma_f32_16x16x32_bf16 a[0:3],a[128:131],v[64:67],a[0:3]
buffer_store_dwordx4 v[160:163],v6,s[32:35],s84 offen offset:1024 sc1
v_mfma_f32_16x16x32_bf16 a[0:3],a[132:135],v[68:71],a[0:3]
v_accvgpr_read_b32 v170,a244
v_accvgpr_read_b32 v171,a245
v_mfma_f32_16x16x32_bf16 a[0:3],a[136:139],v[72:75],a[0:3]
v_accvgpr_read_b32 v16,a246
v_accvgpr_read_b32 v17,a247
v_mfma_f32_16x16x32_bf16 a[0:3],a[140:143],v[76:79],a[0:3]
v_cvt_pk_bf16_f32 v170,v170,v171
v_cvt_pk_bf16_f32 v171,v16,v17
v_mfma_f32_16x16x32_bf16 a[4:7],a[140:143],v[92:95],a[4:7]
buffer_store_dwordx4 v[168:171],v6,s[32:35],s85 offen offset:1024 sc1
v_mfma_f32_16x16x32_bf16 a[4:7],a[136:139],v[88:91],a[4:7]
v_accvgpr_read_b32 v178,a248
v_accvgpr_read_b32 v179,a249
v_mfma_f32_16x16x32_bf16 a[4:7],a[132:135],v[84:87],a[4:7]
v_accvgpr_read_b32 v12,a250
v_accvgpr_read_b32 v13,a251
v_mfma_f32_16x16x32_bf16 a[4:7],a[128:131],v[80:83],a[4:7]
v_cvt_pk_bf16_f32 v178,v178,v179
v_cvt_pk_bf16_f32 v179,v12,v13
v_mfma_f32_16x16x32_bf16 a[8:11],a[128:131],v[96:99],a[8:11]
buffer_store_dwordx4 v[176:179],v6,s[32:35],s86 offen offset:1024 sc1
v_mfma_f32_16x16x32_bf16 a[8:11],a[132:135],v[100:103],a[8:11]
ds_read_b64_tr_b16 a[116:117],v44 offset:10432
ds_read_b64_tr_b16 a[118:119],v44 offset:15552
v_mfma_f32_16x16x32_bf16 a[8:11],a[136:139],v[104:107],a[8:11]
ds_read_b64_tr_b16 a[120:121],v44 offset:20672
v_mfma_f32_16x16x32_bf16 a[8:11],a[140:143],v[108:111],a[8:11]
s_waitcnt lgkmcnt(3)
ds_read_b64_tr_b16 a[122:123],v44 offset:25792
v_mfma_f32_16x16x32_bf16 a[0:3],a[144:147],v[132:135],a[0:3]
ds_read_b64_tr_b16 a[124:125],v44 offset:30912
v_mfma_f32_16x16x32_bf16 a[0:3],a[148:151],v[140:143],a[0:3]
ds_read_b64_tr_b16 a[126:127],v44 offset:36032
ds_read_b64_tr_b16 a[128:129],v43 offset:224
v_mfma_f32_16x16x32_bf16 a[0:3],a[152:155],v[148:151],a[0:3]
ds_read_b64_tr_b16 a[130:131],v43 offset:5344
v_mfma_f32_16x16x32_bf16 a[0:3],a[156:159],v[156:159],a[0:3]
ds_read_b64_tr_b16 a[132:133],v43 offset:10464
v_mfma_f32_16x16x32_bf16 a[4:7],a[156:159],v[188:191],a[4:7]
ds_read_b64_tr_b16 a[134:135],v43 offset:15584
v_mfma_f32_16x16x32_bf16 a[4:7],a[152:155],v[180:183],a[4:7]
ds_read_b64_tr_b16 a[136:137],v43 offset:20704
ds_read_b64_tr_b16 a[138:139],v43 offset:25824
v_mfma_f32_16x16x32_bf16 a[4:7],a[148:151],v[172:175],a[4:7]
ds_read_b64_tr_b16 a[140:141],v43 offset:30944
v_mfma_f32_16x16x32_bf16 a[4:7],a[144:147],v[164:167],a[4:7]
ds_read_b64_tr_b16 a[142:143],v43 offset:36064
v_mfma_f32_16x16x32_bf16 a[8:11],a[144:147],v[196:199],a[8:11]
ds_read_b64_tr_b16 a[144:145],v45 offset:224
v_mfma_f32_16x16x32_bf16 a[8:11],a[148:151],v[204:207],a[8:11]
v_accvgpr_read_b32 v192,a0
v_accvgpr_read_b32 v193,a1
v_mfma_f32_16x16x32_bf16 a[8:11],a[152:155],v[212:215],a[8:11]
v_cvt_pk_bf16_f32 v192,v192,v193
v_accvgpr_read_b32 v194,a2
v_mfma_f32_16x16x32_bf16 a[8:11],a[156:159],v[220:223],a[8:11]
v_accvgpr_read_b32 v195,a3
v_cvt_pk_bf16_f32 v193,v194,v195
v_mfma_f32_16x16x32_bf16 a[16:19],a[160:163],v[64:67],a[16:19]
ds_read_b64_tr_b16 a[146:147],v45 offset:5344
ds_read_b64_tr_b16 a[148:149],v45 offset:10464
v_mfma_f32_16x16x32_bf16 a[16:19],a[164:167],v[68:71],a[16:19]
v_accvgpr_read_b32 v200,a4
v_accvgpr_read_b32 v201,a5
v_mfma_f32_16x16x32_bf16 a[16:19],a[168:171],v[72:75],a[16:19]
v_cvt_pk_bf16_f32 v200,v200,v201
v_accvgpr_read_b32 v202,a6
v_mfma_f32_16x16x32_bf16 a[16:19],a[172:175],v[76:79],a[16:19]
v_accvgpr_read_b32 v203,a7
v_cvt_pk_bf16_f32 v201,v202,v203
v_mfma_f32_16x16x32_bf16 a[20:23],a[172:175],v[92:95],a[20:23]
ds_read_b64_tr_b16 a[150:151],v45 offset:15584
ds_read_b64_tr_b16 a[152:153],v45 offset:20704
v_mfma_f32_16x16x32_bf16 a[20:23],a[168:171],v[88:91],a[20:23]
ds_read_b64_tr_b16 a[154:155],v45 offset:25824
v_accvgpr_read_b32 v208,a8
v_mfma_f32_16x16x32_bf16 a[20:23],a[164:167],v[84:87],a[20:23]
ds_read_b64_tr_b16 a[156:157],v45 offset:30944
v_accvgpr_read_b32 v209,a9
v_mfma_f32_16x16x32_bf16 a[20:23],a[160:163],v[80:83],a[20:23]
ds_read_b64_tr_b16 a[158:159],v45 offset:36064
v_cvt_pk_bf16_f32 v208,v208,v209
v_mfma_f32_16x16x32_bf16 a[24:27],a[160:163],v[96:99],a[24:27]
v_accvgpr_read_b32 v210,a10
v_accvgpr_read_b32 v211,a11
v_mfma_f32_16x16x32_bf16 a[24:27],a[164:167],v[100:103],a[24:27]
ds_read_b64_tr_b16 a[160:161],v42 offset:256
v_cvt_pk_bf16_f32 v209,v210,v211
v_mfma_f32_16x16x32_bf16 a[24:27],a[168:171],v[104:107],a[24:27]
ds_read_b64_tr_b16 a[162:163],v42 offset:5376
ds_read_b64_tr_b16 a[164:165],v42 offset:10496
v_mfma_f32_16x16x32_bf16 a[24:27],a[172:175],v[108:111],a[24:27]
ds_read_b64_tr_b16 a[166:167],v42 offset:15616
v_mfma_f32_16x16x32_bf16 a[16:19],a[176:179],v[132:135],a[16:19]
ds_read_b64_tr_b16 a[168:169],v42 offset:20736
v_mfma_f32_16x16x32_bf16 a[16:19],a[180:183],v[140:143],a[16:19]
ds_read_b64_tr_b16 a[170:171],v42 offset:25856
v_mfma_f32_16x16x32_bf16 a[16:19],a[184:187],v[148:151],a[16:19]
ds_read_b64_tr_b16 a[172:173],v42 offset:30976
ds_read_b64_tr_b16 a[174:175],v42 offset:36096
v_mfma_f32_16x16x32_bf16 a[16:19],a[188:191],v[156:159],a[16:19]
v_mfma_f32_16x16x32_bf16 a[20:23],a[188:191],v[188:191],a[20:23]
v_mfma_f32_16x16x32_bf16 a[20:23],a[184:187],v[180:183],a[20:23]
v_mfma_f32_16x16x32_bf16 a[20:23],a[180:183],v[172:175],a[20:23]
v_mfma_f32_16x16x32_bf16 a[20:23],a[176:179],v[164:167],a[20:23]
v_mfma_f32_16x16x32_bf16 a[24:27],a[176:179],v[196:199],a[24:27]
ds_read_b64_tr_b16 a[176:177],v44 offset:256
ds_read_b64_tr_b16 a[178:179],v44 offset:5376
v_mfma_f32_16x16x32_bf16 a[24:27],a[180:183],v[204:207],a[24:27]
v_accvgpr_read_b32 v194,a16
v_accvgpr_read_b32 v195,a17
v_mfma_f32_16x16x32_bf16 a[24:27],a[184:187],v[212:215],a[24:27]
v_accvgpr_read_b32 v14,a18
v_accvgpr_read_b32 v15,a19
v_mfma_f32_16x16x32_bf16 a[24:27],a[188:191],v[220:223],a[24:27]
v_cvt_pk_bf16_f32 v194,v194,v195
v_cvt_pk_bf16_f32 v195,v14,v15
v_mfma_f32_16x16x32_bf16 a[32:35],a[96:99],v[64:67],a[32:35]
buffer_store_dwordx4 v[192:195],v6,s[32:35],s84 offen offset:2048 sc1
v_mfma_f32_16x16x32_bf16 a[32:35],a[100:103],v[68:71],a[32:35]
v_accvgpr_read_b32 v202,a20
v_accvgpr_read_b32 v203,a21
v_mfma_f32_16x16x32_bf16 a[32:35],a[104:107],v[72:75],a[32:35]
v_accvgpr_read_b32 v16,a22
v_accvgpr_read_b32 v17,a23
v_mfma_f32_16x16x32_bf16 a[32:35],a[108:111],v[76:79],a[32:35]
v_cvt_pk_bf16_f32 v202,v202,v203
v_cvt_pk_bf16_f32 v203,v16,v17
v_mfma_f32_16x16x32_bf16 a[36:39],a[108:111],v[92:95],a[36:39]
buffer_store_dwordx4 v[200:203],v6,s[32:35],s85 offen offset:2048 sc1
v_mfma_f32_16x16x32_bf16 a[36:39],a[104:107],v[88:91],a[36:39]
v_accvgpr_read_b32 v210,a24
v_accvgpr_read_b32 v211,a25
v_mfma_f32_16x16x32_bf16 a[36:39],a[100:103],v[84:87],a[36:39]
v_accvgpr_read_b32 v12,a26
v_accvgpr_read_b32 v13,a27
v_mfma_f32_16x16x32_bf16 a[36:39],a[96:99],v[80:83],a[36:39]
v_cvt_pk_bf16_f32 v210,v210,v211
v_cvt_pk_bf16_f32 v211,v12,v13
v_mfma_f32_16x16x32_bf16 a[40:43],a[96:99],v[96:99],a[40:43]
buffer_store_dwordx4 v[208:211],v6,s[32:35],s86 offen offset:2048 sc1
v_mfma_f32_16x16x32_bf16 a[40:43],a[100:103],v[100:103],a[40:43]
ds_read_b64_tr_b16 a[180:181],v44 offset:10496
ds_read_b64_tr_b16 a[182:183],v44 offset:15616
v_mfma_f32_16x16x32_bf16 a[40:43],a[104:107],v[104:107],a[40:43]
ds_read_b64_tr_b16 a[184:185],v44 offset:20736
v_mfma_f32_16x16x32_bf16 a[40:43],a[108:111],v[108:111],a[40:43]
ds_read_b64_tr_b16 a[186:187],v44 offset:25856
v_mfma_f32_16x16x32_bf16 a[32:35],a[112:115],v[132:135],a[32:35]
s_waitcnt lgkmcnt(4)
ds_read_b64_tr_b16 a[188:189],v44 offset:30976
v_mfma_f32_16x16x32_bf16 a[32:35],a[116:119],v[140:143],a[32:35]
ds_read_b64_tr_b16 a[190:191],v44 offset:36096
ds_read_b64_tr_b16 a[96:97],v43 offset:288
v_mfma_f32_16x16x32_bf16 a[32:35],a[120:123],v[148:151],a[32:35]
ds_read_b64_tr_b16 a[98:99],v43 offset:5408
v_mfma_f32_16x16x32_bf16 a[32:35],a[124:127],v[156:159],a[32:35]
ds_read_b64_tr_b16 a[100:101],v43 offset:10528
v_mfma_f32_16x16x32_bf16 a[36:39],a[124:127],v[188:191],a[36:39]
ds_read_b64_tr_b16 a[102:103],v43 offset:15648
v_mfma_f32_16x16x32_bf16 a[36:39],a[120:123],v[180:183],a[36:39]
ds_read_b64_tr_b16 a[104:105],v43 offset:20768
ds_read_b64_tr_b16 a[106:107],v43 offset:25888
v_mfma_f32_16x16x32_bf16 a[36:39],a[116:119],v[172:175],a[36:39]
ds_read_b64_tr_b16 a[108:109],v43 offset:31008
v_mfma_f32_16x16x32_bf16 a[36:39],a[112:115],v[164:167],a[36:39]
ds_read_b64_tr_b16 a[110:111],v43 offset:36128
v_mfma_f32_16x16x32_bf16 a[40:43],a[112:115],v[196:199],a[40:43]
ds_read_b64_tr_b16 a[112:113],v45 offset:288
v_mfma_f32_16x16x32_bf16 a[40:43],a[116:119],v[204:207],a[40:43]
v_accvgpr_read_b32 v224,a32
v_accvgpr_read_b32 v225,a33
v_mfma_f32_16x16x32_bf16 a[40:43],a[120:123],v[212:215],a[40:43]
v_cvt_pk_bf16_f32 v224,v224,v225
v_accvgpr_read_b32 v226,a34
v_mfma_f32_16x16x32_bf16 a[40:43],a[124:127],v[220:223],a[40:43]
v_accvgpr_read_b32 v227,a35
v_cvt_pk_bf16_f32 v225,v226,v227
v_mfma_f32_16x16x32_bf16 a[48:51],a[128:131],v[64:67],a[48:51]
ds_read_b64_tr_b16 a[114:115],v45 offset:5408
ds_read_b64_tr_b16 a[116:117],v45 offset:10528
v_mfma_f32_16x16x32_bf16 a[48:51],a[132:135],v[68:71],a[48:51]
v_accvgpr_read_b32 v232,a36
v_accvgpr_read_b32 v233,a37
v_mfma_f32_16x16x32_bf16 a[48:51],a[136:139],v[72:75],a[48:51]
v_cvt_pk_bf16_f32 v232,v232,v233
v_accvgpr_read_b32 v234,a38
v_mfma_f32_16x16x32_bf16 a[48:51],a[140:143],v[76:79],a[48:51]
v_accvgpr_read_b32 v235,a39
v_cvt_pk_bf16_f32 v233,v234,v235
v_mfma_f32_16x16x32_bf16 a[52:55],a[140:143],v[92:95],a[52:55]
ds_read_b64_tr_b16 a[118:119],v45 offset:15648
ds_read_b64_tr_b16 a[120:121],v45 offset:20768
v_mfma_f32_16x16x32_bf16 a[52:55],a[136:139],v[88:91],a[52:55]
v_accvgpr_read_b32 v240,a40
v_accvgpr_read_b32 v241,a41
v_mfma_f32_16x16x32_bf16 a[52:55],a[132:135],v[84:87],a[52:55]
v_cvt_pk_bf16_f32 v240,v240,v241
)A"
R"A(v_accvgpr_read_b32 v242,a42
v_mfma_f32_16x16x32_bf16 a[52:55],a[128:131],v[80:83],a[52:55]
v_accvgpr_read_b32 v243,a43
v_cvt_pk_bf16_f32 v241,v242,v243
v_mfma_f32_16x16x32_bf16 a[56:59],a[128:131],v[96:99],a[56:59]
ds_read_b64_tr_b16 a[122:123],v45 offset:25888
ds_read_b64_tr_b16 a[124:125],v45 offset:31008
v_mfma_f32_16x16x32_bf16 a[56:59],a[132:135],v[100:103],a[56:59]
ds_read_b64_tr_b16 a[126:127],v45 offset:36128
v_mfma_f32_16x16x32_bf16 a[56:59],a[136:139],v[104:107],a[56:59]
ds_read_b64_tr_b16 a[128:129],v1
v_mfma_f32_16x16x32_bf16 a[56:59],a[140:143],v[108:111],a[56:59]
ds_read_b64_tr_b16 a[130:131],v1 offset:5120
v_mfma_f32_16x16x32_bf16 a[48:51],a[144:147],v[132:135],a[48:51]
ds_read_b64_tr_b16 a[132:133],v1 offset:10240
ds_read_b64_tr_b16 a[134:135],v1 offset:15360
v_mfma_f32_16x16x32_bf16 a[48:51],a[148:151],v[140:143],a[48:51]
ds_read_b64_tr_b16 a[136:137],v1 offset:20480
v_mfma_f32_16x16x32_bf16 a[48:51],a[152:155],v[148:151],a[48:51]
ds_read_b64_tr_b16 a[138:139],v1 offset:25600
v_mfma_f32_16x16x32_bf16 a[48:51],a[156:159],v[156:159],a[48:51]
ds_read_b64_tr_b16 a[140:141],v1 offset:30720
v_mfma_f32_16x16x32_bf16 a[52:55],a[156:159],v[188:191],a[52:55]
ds_read_b64_tr_b16 a[142:143],v1 offset:35840
v_mfma_f32_16x16x32_bf16 a[52:55],a[152:155],v[180:183],a[52:55]
v_mfma_f32_16x16x32_bf16 a[52:55],a[148:151],v[172:175],a[52:55]
v_mfma_f32_16x16x32_bf16 a[52:55],a[144:147],v[164:167],a[52:55]
v_mfma_f32_16x16x32_bf16 a[56:59],a[144:147],v[196:199],a[56:59]
ds_read_b64_tr_b16 a[144:145],v42
ds_read_b64_tr_b16 a[146:147],v42 offset:5120
v_mfma_f32_16x16x32_bf16 a[56:59],a[148:151],v[204:207],a[56:59]
v_accvgpr_read_b32 v226,a48
v_accvgpr_read_b32 v227,a49
v_mfma_f32_16x16x32_bf16 a[56:59],a[152:155],v[212:215],a[56:59]
v_accvgpr_read_b32 v14,a50
v_accvgpr_read_b32 v15,a51
v_mfma_f32_16x16x32_bf16 a[56:59],a[156:159],v[220:223],a[56:59]
v_cvt_pk_bf16_f32 v226,v226,v227
v_cvt_pk_bf16_f32 v227,v14,v15
v_mfma_f32_16x16x32_bf16 a[64:67],a[160:163],v[64:67],a[64:67]
buffer_store_dwordx4 v[224:227],v6,s[32:35],s84 offen offset:3072 sc1
v_mfma_f32_16x16x32_bf16 a[64:67],a[164:167],v[68:71],a[64:67]
v_accvgpr_read_b32 v234,a52
v_accvgpr_read_b32 v235,a53
v_mfma_f32_16x16x32_bf16 a[64:67],a[168:171],v[72:75],a[64:67]
v_accvgpr_read_b32 v16,a54
v_accvgpr_read_b32 v17,a55
v_mfma_f32_16x16x32_bf16 a[64:67],a[172:175],v[76:79],a[64:67]
v_cvt_pk_bf16_f32 v234,v234,v235
v_cvt_pk_bf16_f32 v235,v16,v17
v_mfma_f32_16x16x32_bf16 a[68:71],a[172:175],v[92:95],a[68:71]
buffer_store_dwordx4 v[232:235],v6,s[32:35],s85 offen offset:3072 sc1
v_mfma_f32_16x16x32_bf16 a[68:71],a[168:171],v[88:91],a[68:71]
v_accvgpr_read_b32 v242,a56
v_accvgpr_read_b32 v243,a57
v_mfma_f32_16x16x32_bf16 a[68:71],a[164:167],v[84:87],a[68:71]
v_accvgpr_read_b32 v12,a58
v_accvgpr_read_b32 v13,a59
v_mfma_f32_16x16x32_bf16 a[68:71],a[160:163],v[80:83],a[68:71]
v_cvt_pk_bf16_f32 v242,v242,v243
v_cvt_pk_bf16_f32 v243,v12,v13
v_mfma_f32_16x16x32_bf16 a[72:75],a[160:163],v[96:99],a[72:75]
buffer_store_dwordx4 v[240:243],v6,s[32:35],s86 offen offset:3072 sc1
v_mfma_f32_16x16x32_bf16 a[72:75],a[164:167],v[100:103],a[72:75]
ds_read_b64_tr_b16 a[148:149],v42 offset:10240
ds_read_b64_tr_b16 a[150:151],v42 offset:15360
v_mfma_f32_16x16x32_bf16 a[72:75],a[168:171],v[104:107],a[72:75]
ds_read_b64_tr_b16 a[152:153],v42 offset:20480
v_mfma_f32_16x16x32_bf16 a[72:75],a[172:175],v[108:111],a[72:75]
ds_read_b64_tr_b16 a[154:155],v42 offset:25600
v_mfma_f32_16x16x32_bf16 a[64:67],a[176:179],v[132:135],a[64:67]
s_waitcnt lgkmcnt(4)
ds_read_b64_tr_b16 a[156:157],v42 offset:30720
v_mfma_f32_16x16x32_bf16 a[64:67],a[180:183],v[140:143],a[64:67]
ds_read_b64_tr_b16 a[158:159],v42 offset:35840
ds_read_b64_tr_b16 a[160:161],v44
v_mfma_f32_16x16x32_bf16 a[64:67],a[184:187],v[148:151],a[64:67]
ds_read_b64_tr_b16 a[162:163],v44 offset:5120
v_mfma_f32_16x16x32_bf16 a[64:67],a[188:191],v[156:159],a[64:67]
ds_read_b64_tr_b16 a[164:165],v44 offset:10240
v_mfma_f32_16x16x32_bf16 a[68:71],a[188:191],v[188:191],a[68:71]
ds_read_b64_tr_b16 a[166:167],v44 offset:15360
v_mfma_f32_16x16x32_bf16 a[68:71],a[184:187],v[180:183],a[68:71]
ds_read_b64_tr_b16 a[168:169],v44 offset:20480
ds_read_b64_tr_b16 a[170:171],v44 offset:25600
v_mfma_f32_16x16x32_bf16 a[68:71],a[180:183],v[172:175],a[68:71]
ds_read_b64_tr_b16 a[172:173],v44 offset:30720
v_mfma_f32_16x16x32_bf16 a[68:71],a[176:179],v[164:167],a[68:71]
ds_read_b64_tr_b16 a[174:175],v44 offset:35840
v_mfma_f32_16x16x32_bf16 a[72:75],a[176:179],v[196:199],a[72:75]
ds_read_b64_tr_b16 a[176:177],v2 offset:32
v_mfma_f32_16x16x32_bf16 a[72:75],a[180:183],v[204:207],a[72:75]
v_accvgpr_read_b32 v128,a64
v_accvgpr_read_b32 v129,a65
v_mfma_f32_16x16x32_bf16 a[72:75],a[184:187],v[212:215],a[72:75]
v_cvt_pk_bf16_f32 v128,v128,v129
v_accvgpr_read_b32 v130,a66
v_mfma_f32_16x16x32_bf16 a[72:75],a[188:191],v[220:223],a[72:75]
v_accvgpr_read_b32 v131,a67
v_cvt_pk_bf16_f32 v129,v130,v131
v_mfma_f32_16x16x32_bf16 a[80:83],a[96:99],v[64:67],a[80:83]
ds_read_b64_tr_b16 a[178:179],v2 offset:5152
ds_read_b64_tr_b16 a[180:181],v2 offset:10272
v_mfma_f32_16x16x32_bf16 a[80:83],a[100:103],v[68:71],a[80:83]
v_accvgpr_read_b32 v136,a68
v_accvgpr_read_b32 v137,a69
v_mfma_f32_16x16x32_bf16 a[80:83],a[104:107],v[72:75],a[80:83]
v_cvt_pk_bf16_f32 v136,v136,v137
v_accvgpr_read_b32 v138,a70
v_mfma_f32_16x16x32_bf16 a[80:83],a[108:111],v[76:79],a[80:83]
ds_read_b64_tr_b16 a[182:183],v2 offset:15392
v_accvgpr_read_b32 v139,a71
v_mfma_f32_16x16x32_bf16 a[84:87],a[108:111],v[92:95],a[84:87]
ds_read_b64_tr_b16 a[184:185],v2 offset:20512
v_cvt_pk_bf16_f32 v137,v138,v139
v_mfma_f32_16x16x32_bf16 a[84:87],a[104:107],v[88:91],a[84:87]
v_accvgpr_read_b32 v144,a72
v_accvgpr_read_b32 v145,a73
v_mfma_f32_16x16x32_bf16 a[84:87],a[100:103],v[84:87],a[84:87]
v_cvt_pk_bf16_f32 v144,v144,v145
v_accvgpr_read_b32 v146,a74
v_mfma_f32_16x16x32_bf16 a[84:87],a[96:99],v[80:83],a[84:87]
v_accvgpr_read_b32 v147,a75
v_cvt_pk_bf16_f32 v145,v146,v147
v_mfma_f32_16x16x32_bf16 a[88:91],a[96:99],v[96:99],a[88:91]
ds_read_b64_tr_b16 a[186:187],v2 offset:25632
ds_read_b64_tr_b16 a[188:189],v2 offset:30752
v_mfma_f32_16x16x32_bf16 a[88:91],a[100:103],v[100:103],a[88:91]
ds_read_b64_tr_b16 a[190:191],v2 offset:35872
v_mfma_f32_16x16x32_bf16 a[88:91],a[104:107],v[104:107],a[88:91]
ds_read_b64_tr_b16 a[96:97],v43 offset:32
v_mfma_f32_16x16x32_bf16 a[88:91],a[108:111],v[108:111],a[88:91]
ds_read_b64_tr_b16 a[98:99],v43 offset:5152
v_mfma_f32_16x16x32_bf16 a[80:83],a[112:115],v[132:135],a[80:83]
ds_read_b64_tr_b16 a[100:101],v43 offset:10272
ds_read_b64_tr_b16 a[102:103],v43 offset:15392
v_mfma_f32_16x16x32_bf16 a[80:83],a[116:119],v[140:143],a[80:83]
ds_read_b64_tr_b16 a[104:105],v43 offset:20512
v_mfma_f32_16x16x32_bf16 a[80:83],a[120:123],v[148:151],a[80:83]
ds_read_b64_tr_b16 a[106:107],v43 offset:25632
v_mfma_f32_16x16x32_bf16 a[80:83],a[124:127],v[156:159],a[80:83]
ds_read_b64_tr_b16 a[108:109],v43 offset:30752
v_mfma_f32_16x16x32_bf16 a[84:87],a[124:127],v[188:191],a[84:87]
ds_read_b64_tr_b16 a[110:111],v43 offset:35872
v_mfma_f32_16x16x32_bf16 a[84:87],a[120:123],v[180:183],a[84:87]
v_mfma_f32_16x16x32_bf16 a[84:87],a[116:119],v[172:175],a[84:87]
v_mfma_f32_16x16x32_bf16 a[84:87],a[112:115],v[164:167],a[84:87]
v_mfma_f32_16x16x32_bf16 a[88:91],a[112:115],v[196:199],a[88:91]
ds_read_b64_tr_b16 a[112:113],v45 offset:32
ds_read_b64_tr_b16 a[114:115],v45 offset:5152
v_mfma_f32_16x16x32_bf16 a[88:91],a[116:119],v[204:207],a[88:91]
v_accvgpr_read_b32 v130,a80
v_accvgpr_read_b32 v131,a81
v_mfma_f32_16x16x32_bf16 a[88:91],a[120:123],v[212:215],a[88:91]
v_accvgpr_read_b32 v14,a82
v_accvgpr_read_b32 v15,a83
v_mfma_f32_16x16x32_bf16 a[88:91],a[124:127],v[220:223],a[88:91]
v_cvt_pk_bf16_f32 v130,v130,v131
v_cvt_pk_bf16_f32 v131,v14,v15
v_mfma_f32_16x16x32_bf16 a[204:207],a[128:131],v[152:155],a[204:207]
s_lshl_b32 s94,s71,2
buffer_store_dwordx4 v[128:131],v25,s[32:35],s84 offen sc1
v_mfma_f32_16x16x32_bf16 a[204:207],a[132:135],v[184:187],a[204:207]
s_add_u32 s94,s94,s40
v_accvgpr_read_b32 v138,a84
v_accvgpr_read_b32 v139,a85
v_mfma_f32_16x16x32_bf16 a[204:207],a[136:139],v[216:219],a[204:207]
s_lshl_b32 s95,s42,2
s_add_u32 s95,s95,s40
s_lshl_b32 s95,s95,4
v_mfma_f32_16x16x32_bf16 a[204:207],a[140:143],v[248:251],a[204:207]
s_lshl_b32 s94,s94,4
v_mov_b32 v18,s68
v_mov_b32 v19,s69
v_mfma_f32_16x16x32_bf16 a[204:207],a[144:147],v[112:115],a[204:207]
s_waitcnt vmcnt(1)
s_mov_b64 exec,1
buffer_store_dwordx2 v[18:19],v6,s[64:67],s95 offen offset:2048 sc1
s_mov_b64 exec,-1
s_waitcnt lgkmcnt(0)
v_mfma_f32_16x16x32_bf16 a[204:207],a[148:151],v[116:119],a[204:207]
v_accvgpr_read_b32 v146,a88
v_accvgpr_read_b32 v147,a89
v_mfma_f32_16x16x32_bf16 a[204:207],a[152:155],v[120:123],a[204:207]
v_accvgpr_read_b32 v16,a86
v_accvgpr_read_b32 v17,a87
v_cvt_pk_bf16_f32 v138,v138,v139
v_accvgpr_read_b32 v12,a90
v_accvgpr_read_b32 v13,a91
v_cvt_pk_bf16_f32 v146,v146,v147
v_cvt_pk_bf16_f32 v139,v16,v17
v_cvt_pk_bf16_f32 v147,v12,v13
v_mfma_f32_16x16x32_bf16 a[204:207],a[156:159],v[124:127],a[204:207]
ds_read_b64_tr_b16 a[116:117],v45 offset:10272
ds_read_b64_tr_b16 a[118:119],v45 offset:15392
v_mfma_f32_16x16x32_bf16 a[204:207],a[160:163],v[228:231],a[204:207]
buffer_store_dwordx4 v[136:139],v25,s[32:35],s85 offen sc1
v_mfma_f32_16x16x32_bf16 a[204:207],a[164:167],v[236:239],a[204:207]
ds_read_b64_tr_b16 a[120:121],v45 offset:20512
ds_read_b64_tr_b16 a[122:123],v45 offset:25632
v_mfma_f32_16x16x32_bf16 a[204:207],a[168:171],v[244:247],a[204:207]
ds_read_b64_tr_b16 a[124:125],v45 offset:30752
ds_read_b64_tr_b16 a[126:127],v45 offset:35872
v_mfma_f32_16x16x32_bf16 a[204:207],a[172:175],v[252:255],a[204:207]
ds_read_b64_tr_b16 a[128:129],v1 offset:64
ds_read_b64_tr_b16 a[130:131],v1 offset:5184
v_mfma_f32_16x16x32_bf16 a[220:223],a[176:179],v[152:155],a[220:223]
buffer_store_dwordx4 v[144:147],v25,s[32:35],s86 offen sc1
v_mfma_f32_16x16x32_bf16 a[220:223],a[180:183],v[184:187],a[220:223]
ds_read_b64_tr_b16 a[132:133],v1 offset:10304
ds_read_b64_tr_b16 a[134:135],v1 offset:15424
v_mfma_f32_16x16x32_bf16 a[220:223],a[184:187],v[216:219],a[220:223]
ds_read_b64_tr_b16 a[136:137],v1 offset:20544
ds_read_b64_tr_b16 a[138:139],v1 offset:25664
v_mfma_f32_16x16x32_bf16 a[220:223],a[188:191],v[248:251],a[220:223]
ds_read_b64_tr_b16 a[140:141],v1 offset:30784
ds_read_b64_tr_b16 a[142:143],v1 offset:35904
v_mfma_f32_16x16x32_bf16 a[220:223],a[96:99],v[112:115],a[220:223]
ds_read_b64_tr_b16 a[144:145],v42 offset:64
ds_read_b64_tr_b16 a[146:147],v42 offset:5184
v_mfma_f32_16x16x32_bf16 a[220:223],a[100:103],v[116:119],a[220:223]
ds_read_b64_tr_b16 a[148:149],v42 offset:10304
ds_read_b64_tr_b16 a[150:151],v42 offset:15424
v_mfma_f32_16x16x32_bf16 a[220:223],a[104:107],v[120:123],a[220:223]
ds_read_b64_tr_b16 a[152:153],v42 offset:20544
ds_read_b64_tr_b16 a[154:155],v42 offset:25664
v_mfma_f32_16x16x32_bf16 a[220:223],a[108:111],v[124:127],a[220:223]
ds_read_b64_tr_b16 a[156:157],v42 offset:30784
ds_read_b64_tr_b16 a[158:159],v42 offset:35904
v_mfma_f32_16x16x32_bf16 a[220:223],a[112:115],v[228:231],a[220:223]
s_waitcnt lgkmcnt(14)
ds_read_b64_tr_b16 a[160:161],v44 offset:64
ds_read_b64_tr_b16 a[162:163],v44 offset:5184
v_mfma_f32_16x16x32_bf16 a[220:223],a[116:119],v[236:239],a[220:223]
ds_read_b64_tr_b16 a[164:165],v44 offset:10304
ds_read_b64_tr_b16 a[166:167],v44 offset:15424
v_mfma_f32_16x16x32_bf16 a[220:223],a[120:123],v[244:247],a[220:223]
ds_read_b64_tr_b16 a[168:169],v44 offset:20544
ds_read_b64_tr_b16 a[170:171],v44 offset:25664
v_mfma_f32_16x16x32_bf16 a[220:223],a[124:127],v[252:255],a[220:223]
ds_read_b64_tr_b16 a[172:173],v44 offset:30784
ds_read_b64_tr_b16 a[174:175],v44 offset:35904
v_mfma_f32_16x16x32_bf16 a[236:239],a[128:131],v[152:155],a[236:239]
s_waitcnt lgkmcnt(14)
ds_read_b64_tr_b16 a[176:177],v2 offset:96
ds_read_b64_tr_b16 a[178:179],v2 offset:5216
v_mfma_f32_16x16x32_bf16 a[236:239],a[132:135],v[184:187],a[236:239]
v_bfe_u32 v24,v6,4,2
v_min_u32 v24,2,v24
v_mfma_f32_16x16x32_bf16 a[236:239],a[136:139],v[216:219],a[236:239]
)A"
R"A(ds_read_b64_tr_b16 a[180:181],v2 offset:10336
ds_read_b64_tr_b16 a[182:183],v2 offset:15456
v_mfma_f32_16x16x32_bf16 a[236:239],a[140:143],v[248:251],a[236:239]
v_add_u32 v24,s70,v24
v_add_u32 v24,1,v24
v_mfma_f32_16x16x32_bf16 a[236:239],a[144:147],v[112:115],a[236:239]
s_waitcnt lgkmcnt(10)
ds_read_b64_tr_b16 a[184:185],v2 offset:20576
ds_read_b64_tr_b16 a[186:187],v2 offset:25696
v_mfma_f32_16x16x32_bf16 a[236:239],a[148:151],v[116:119],a[236:239]
v_and_b32 v24,3,v24
v_lshlrev_b32 v24,8,v24
v_mfma_f32_16x16x32_bf16 a[236:239],a[152:155],v[120:123],a[236:239]
ds_read_b64_tr_b16 a[188:189],v2 offset:30816
ds_read_b64_tr_b16 a[190:191],v2 offset:35936
v_mfma_f32_16x16x32_bf16 a[236:239],a[156:159],v[124:127],a[236:239]
ds_read_b64_tr_b16 a[96:97],v43 offset:96
ds_read_b64_tr_b16 a[98:99],v43 offset:5216
v_mfma_f32_16x16x32_bf16 a[236:239],a[160:163],v[228:231],a[236:239]
s_waitcnt lgkmcnt(8)
ds_read_b64_tr_b16 a[100:101],v43 offset:10336
ds_read_b64_tr_b16 a[102:103],v43 offset:15456
v_mfma_f32_16x16x32_bf16 a[236:239],a[164:167],v[236:239],a[236:239]
ds_read_b64_tr_b16 a[104:105],v43 offset:20576
ds_read_b64_tr_b16 a[106:107],v43 offset:25696
v_mfma_f32_16x16x32_bf16 a[236:239],a[168:171],v[244:247],a[236:239]
ds_read_b64_tr_b16 a[108:109],v43 offset:30816
ds_read_b64_tr_b16 a[110:111],v43 offset:35936
v_mfma_f32_16x16x32_bf16 a[236:239],a[172:175],v[252:255],a[236:239]
ds_read_b64_tr_b16 a[112:113],v45 offset:96
ds_read_b64_tr_b16 a[114:115],v45 offset:5216
v_mfma_f32_16x16x32_bf16 a[252:255],a[176:179],v[152:155],a[252:255]
s_waitcnt lgkmcnt(12)
ds_read_b64_tr_b16 a[116:117],v45 offset:10336
ds_read_b64_tr_b16 a[118:119],v45 offset:15456
v_mfma_f32_16x16x32_bf16 a[252:255],a[180:183],v[184:187],a[252:255]
ds_read_b64_tr_b16 a[120:121],v45 offset:20576
ds_read_b64_tr_b16 a[122:123],v45 offset:25696
v_mfma_f32_16x16x32_bf16 a[252:255],a[184:187],v[216:219],a[252:255]
s_waitcnt lgkmcnt(14)
ds_read_b64_tr_b16 a[124:125],v45 offset:30816
ds_read_b64_tr_b16 a[126:127],v45 offset:35936
v_mfma_f32_16x16x32_bf16 a[252:255],a[188:191],v[248:251],a[252:255]
s_waitcnt lgkmcnt(14)
ds_read_b64_tr_b16 a[128:129],v1 offset:128
ds_read_b64_tr_b16 a[130:131],v1 offset:5248
v_mfma_f32_16x16x32_bf16 a[252:255],a[96:99],v[112:115],a[252:255]
s_waitcnt lgkmcnt(14)
buffer_load_dwordx2 v[26:27],v24,s[64:67],s94 offen offset:2048 sc1
ds_read_b64_tr_b16 a[132:133],v1 offset:10368
ds_read_b64_tr_b16 a[134:135],v1 offset:15488
v_mfma_f32_16x16x32_bf16 a[252:255],a[100:103],v[116:119],a[252:255]
s_waitcnt lgkmcnt(14)
ds_read_b64_tr_b16 a[136:137],v1 offset:20608
ds_read_b64_tr_b16 a[138:139],v1 offset:25728
v_mfma_f32_16x16x32_bf16 a[252:255],a[104:107],v[120:123],a[252:255]
s_waitcnt lgkmcnt(14)
ds_read_b64_tr_b16 a[140:141],v1 offset:30848
ds_read_b64_tr_b16 a[142:143],v1 offset:35968
v_mfma_f32_16x16x32_bf16 a[252:255],a[108:111],v[124:127],a[252:255]
s_waitcnt lgkmcnt(14)
ds_read_b64_tr_b16 a[144:145],v42 offset:128
ds_read_b64_tr_b16 a[146:147],v42 offset:5248
v_mfma_f32_16x16x32_bf16 a[252:255],a[112:115],v[228:231],a[252:255]
s_waitcnt lgkmcnt(14)
ds_read_b64_tr_b16 a[148:149],v42 offset:10368
ds_read_b64_tr_b16 a[150:151],v42 offset:15488
v_mfma_f32_16x16x32_bf16 a[252:255],a[116:119],v[236:239],a[252:255]
s_lshl_b32 s95,s42,2
ds_read_b64_tr_b16 a[152:153],v42 offset:20608
ds_read_b64_tr_b16 a[154:155],v42 offset:25728
s_waitcnt lgkmcnt(15)
v_mfma_f32_16x16x32_bf16 a[252:255],a[120:123],v[244:247],a[252:255]
s_add_u32 s95,s95,s40
s_lshl_b32 s95,s95,4
ds_read_b64_tr_b16 a[156:157],v42 offset:30848
ds_read_b64_tr_b16 a[158:159],v42 offset:35968
v_mov_b32 v18,s68
v_mov_b32 v19,s69
s_waitcnt lgkmcnt(15)
v_mfma_f32_16x16x32_bf16 a[252:255],a[124:127],v[252:255],a[252:255]
s_waitcnt vmcnt(1)
s_mov_b64 exec,1
buffer_store_dwordx2 v[18:19],v6,s[64:67],s95 offen offset:3072 sc1
s_mov_b64 exec,-1
ds_read_b64_tr_b16 a[160:161],v44 offset:128
ds_read_b64_tr_b16 a[162:163],v44 offset:5248
s_waitcnt lgkmcnt(15)
v_mfma_f32_16x16x32_bf16 a[12:15],a[128:131],v[152:155],a[12:15]
s_waitcnt lgkmcnt(14)
ds_read_b64_tr_b16 a[164:165],v44 offset:10368
ds_read_b64_tr_b16 a[166:167],v44 offset:15488
v_mfma_f32_16x16x32_bf16 a[12:15],a[132:135],v[184:187],a[12:15]
s_waitcnt lgkmcnt(14)
ds_read_b64_tr_b16 a[168:169],v44 offset:20608
ds_read_b64_tr_b16 a[170:171],v44 offset:25728
v_mfma_f32_16x16x32_bf16 a[12:15],a[136:139],v[216:219],a[12:15]
s_waitcnt lgkmcnt(14)
ds_read_b64_tr_b16 a[172:173],v44 offset:30848
ds_read_b64_tr_b16 a[174:175],v44 offset:35968
v_mfma_f32_16x16x32_bf16 a[12:15],a[140:143],v[248:251],a[12:15]
s_waitcnt lgkmcnt(14)
ds_read_b64_tr_b16 a[176:177],v2 offset:160
ds_read_b64_tr_b16 a[178:179],v2 offset:5280
v_mfma_f32_16x16x32_bf16 a[12:15],a[144:147],v[112:115],a[12:15]
s_waitcnt lgkmcnt(14)
ds_read_b64_tr_b16 a[180:181],v2 offset:10400
ds_read_b64_tr_b16 a[182:183],v2 offset:15520
v_mfma_f32_16x16x32_bf16 a[12:15],a[148:151],v[116:119],a[12:15]
s_waitcnt lgkmcnt(14)
ds_read_b64_tr_b16 a[184:185],v2 offset:20640
ds_read_b64_tr_b16 a[186:187],v2 offset:25760
v_mfma_f32_16x16x32_bf16 a[12:15],a[152:155],v[120:123],a[12:15]
s_waitcnt lgkmcnt(14)
ds_read_b64_tr_b16 a[188:189],v2 offset:30880
ds_read_b64_tr_b16 a[190:191],v2 offset:36000
v_mfma_f32_16x16x32_bf16 a[12:15],a[156:159],v[124:127],a[12:15]
s_waitcnt lgkmcnt(14)
ds_read_b64_tr_b16 a[96:97],v43 offset:160
ds_read_b64_tr_b16 a[98:99],v43 offset:5280
v_mfma_f32_16x16x32_bf16 a[12:15],a[160:163],v[228:231],a[12:15]
s_waitcnt lgkmcnt(14)
ds_read_b64_tr_b16 a[100:101],v43 offset:10400
ds_read_b64_tr_b16 a[102:103],v43 offset:15520
v_mfma_f32_16x16x32_bf16 a[12:15],a[164:167],v[236:239],a[12:15]
s_waitcnt lgkmcnt(14)
ds_read_b64_tr_b16 a[104:105],v43 offset:20640
ds_read_b64_tr_b16 a[106:107],v43 offset:25760
v_mfma_f32_16x16x32_bf16 a[12:15],a[168:171],v[244:247],a[12:15]
s_waitcnt lgkmcnt(14)
ds_read_b64_tr_b16 a[108:109],v43 offset:30880
ds_read_b64_tr_b16 a[110:111],v43 offset:36000
v_mfma_f32_16x16x32_bf16 a[12:15],a[172:175],v[252:255],a[12:15]
s_waitcnt lgkmcnt(14)
ds_read_b64_tr_b16 a[112:113],v45 offset:160
ds_read_b64_tr_b16 a[114:115],v45 offset:5280
v_mfma_f32_16x16x32_bf16 a[28:31],a[176:179],v[152:155],a[28:31]
s_waitcnt lgkmcnt(14)
ds_read_b64_tr_b16 a[116:117],v45 offset:10400
ds_read_b64_tr_b16 a[118:119],v45 offset:15520
v_mfma_f32_16x16x32_bf16 a[28:31],a[180:183],v[184:187],a[28:31]
s_waitcnt lgkmcnt(14)
ds_read_b64_tr_b16 a[120:121],v45 offset:20640
ds_read_b64_tr_b16 a[122:123],v45 offset:25760
v_mfma_f32_16x16x32_bf16 a[28:31],a[184:187],v[216:219],a[28:31]
s_waitcnt lgkmcnt(14)
ds_read_b64_tr_b16 a[124:125],v45 offset:30880
ds_read_b64_tr_b16 a[126:127],v45 offset:36000
v_mfma_f32_16x16x32_bf16 a[28:31],a[188:191],v[248:251],a[28:31]
s_waitcnt lgkmcnt(14)
ds_read_b64_tr_b16 a[128:129],v1 offset:192
ds_read_b64_tr_b16 a[130:131],v1 offset:5312
v_mfma_f32_16x16x32_bf16 a[28:31],a[96:99],v[112:115],a[28:31]
s_waitcnt lgkmcnt(14)
ds_read_b64_tr_b16 a[132:133],v1 offset:10432
ds_read_b64_tr_b16 a[134:135],v1 offset:15552
v_mfma_f32_16x16x32_bf16 a[28:31],a[100:103],v[116:119],a[28:31]
s_waitcnt lgkmcnt(14)
ds_read_b64_tr_b16 a[136:137],v1 offset:20672
ds_read_b64_tr_b16 a[138:139],v1 offset:25792
v_mfma_f32_16x16x32_bf16 a[28:31],a[104:107],v[120:123],a[28:31]
s_waitcnt lgkmcnt(14)
ds_read_b64_tr_b16 a[140:141],v1 offset:30912
ds_read_b64_tr_b16 a[142:143],v1 offset:36032
v_mfma_f32_16x16x32_bf16 a[28:31],a[108:111],v[124:127],a[28:31]
s_waitcnt lgkmcnt(14)
ds_read_b64_tr_b16 a[144:145],v42 offset:192
ds_read_b64_tr_b16 a[146:147],v42 offset:5312
v_mfma_f32_16x16x32_bf16 a[28:31],a[112:115],v[228:231],a[28:31]
s_waitcnt lgkmcnt(14)
ds_read_b64_tr_b16 a[148:149],v42 offset:10432
ds_read_b64_tr_b16 a[150:151],v42 offset:15552
v_mfma_f32_16x16x32_bf16 a[28:31],a[116:119],v[236:239],a[28:31]
s_waitcnt lgkmcnt(14)
ds_read_b64_tr_b16 a[152:153],v42 offset:20672
ds_read_b64_tr_b16 a[154:155],v42 offset:25792
v_mfma_f32_16x16x32_bf16 a[28:31],a[120:123],v[244:247],a[28:31]
s_waitcnt lgkmcnt(14)
ds_read_b64_tr_b16 a[156:157],v42 offset:30912
ds_read_b64_tr_b16 a[158:159],v42 offset:36032
v_mfma_f32_16x16x32_bf16 a[28:31],a[124:127],v[252:255],a[28:31]
s_waitcnt lgkmcnt(14)
ds_read_b64_tr_b16 a[160:161],v44 offset:192
ds_read_b64_tr_b16 a[162:163],v44 offset:5312
v_mfma_f32_16x16x32_bf16 a[44:47],a[128:131],v[152:155],a[44:47]
ds_read_b64_tr_b16 a[164:165],v44 offset:10432
ds_read_b64_tr_b16 a[166:167],v44 offset:15552
s_waitcnt vmcnt(1)
v_cmp_ne_u32 s[90:91],s68,v26
v_cmp_ne_u32 s[92:93],s69,v27
s_nop 0
s_or_b64 s[90:91],s[90:91],s[92:93]
s_cbranch_scc1 L_fr10_%=
L_fo10_%=:
s_waitcnt lgkmcnt(15)
v_mfma_f32_16x16x32_bf16 a[44:47],a[132:135],v[184:187],a[44:47]
s_waitcnt lgkmcnt(14)
buffer_load_dwordx4 v[64:67],v6,s[32:35],s76 offen sc1
ds_read_b64_tr_b16 a[168:169],v44 offset:20672
ds_read_b64_tr_b16 a[170:171],v44 offset:25792
v_mfma_f32_16x16x32_bf16 a[44:47],a[136:139],v[216:219],a[44:47]
s_lshl_b32 s94,s71,2
ds_read_b64_tr_b16 a[172:173],v44 offset:30912
ds_read_b64_tr_b16 a[174:175],v44 offset:36032
s_waitcnt lgkmcnt(15)
v_mfma_f32_16x16x32_bf16 a[44:47],a[140:143],v[248:251],a[44:47]
s_add_u32 s94,s94,s40
ds_read_b64_tr_b16 a[176:177],v2 offset:224
ds_read_b64_tr_b16 a[178:179],v2 offset:5344
s_waitcnt lgkmcnt(15)
v_mfma_f32_16x16x32_bf16 a[44:47],a[144:147],v[112:115],a[44:47]
s_lshl_b32 s94,s94,4
ds_read_b64_tr_b16 a[180:181],v2 offset:10464
ds_read_b64_tr_b16 a[182:183],v2 offset:15584
s_waitcnt lgkmcnt(15)
v_mfma_f32_16x16x32_bf16 a[44:47],a[148:151],v[116:119],a[44:47]
s_waitcnt lgkmcnt(14)
buffer_load_dwordx4 v[80:83],v6,s[32:35],s77 offen sc1
ds_read_b64_tr_b16 a[184:185],v2 offset:20704
ds_read_b64_tr_b16 a[186:187],v2 offset:25824
v_mfma_f32_16x16x32_bf16 a[44:47],a[152:155],v[120:123],a[44:47]
s_waitcnt lgkmcnt(14)
ds_read_b64_tr_b16 a[188:189],v2 offset:30944
ds_read_b64_tr_b16 a[190:191],v2 offset:36064
v_mfma_f32_16x16x32_bf16 a[44:47],a[156:159],v[124:127],a[44:47]
s_waitcnt lgkmcnt(14)
ds_read_b64_tr_b16 a[96:97],v43 offset:224
ds_read_b64_tr_b16 a[98:99],v43 offset:5344
v_mfma_f32_16x16x32_bf16 a[44:47],a[160:163],v[228:231],a[44:47]
s_waitcnt lgkmcnt(14)
ds_read_b64_tr_b16 a[100:101],v43 offset:10464
ds_read_b64_tr_b16 a[102:103],v43 offset:15584
v_mfma_f32_16x16x32_bf16 a[44:47],a[164:167],v[236:239],a[44:47]
s_waitcnt lgkmcnt(14)
buffer_load_dwordx4 v[96:99],v6,s[32:35],s78 offen sc1
ds_read_b64_tr_b16 a[104:105],v43 offset:20704
ds_read_b64_tr_b16 a[106:107],v43 offset:25824
v_mfma_f32_16x16x32_bf16 a[44:47],a[168:171],v[244:247],a[44:47]
s_waitcnt lgkmcnt(14)
ds_read_b64_tr_b16 a[108:109],v43 offset:30944
ds_read_b64_tr_b16 a[110:111],v43 offset:36064
v_mfma_f32_16x16x32_bf16 a[44:47],a[172:175],v[252:255],a[44:47]
s_waitcnt lgkmcnt(14)
ds_read_b64_tr_b16 a[112:113],v45 offset:224
ds_read_b64_tr_b16 a[114:115],v45 offset:5344
v_mfma_f32_16x16x32_bf16 a[60:63],a[176:179],v[152:155],a[60:63]
s_waitcnt lgkmcnt(14)
ds_read_b64_tr_b16 a[116:117],v45 offset:10464
ds_read_b64_tr_b16 a[118:119],v45 offset:15584
v_mfma_f32_16x16x32_bf16 a[60:63],a[180:183],v[184:187],a[60:63]
s_waitcnt lgkmcnt(14)
buffer_load_dwordx4 v[68:71],v6,s[32:35],s76 offen offset:1024 sc1
ds_read_b64_tr_b16 a[120:121],v45 offset:20704
ds_read_b64_tr_b16 a[122:123],v45 offset:25824
v_mfma_f32_16x16x32_bf16 a[60:63],a[184:187],v[216:219],a[60:63]
s_waitcnt lgkmcnt(14)
ds_read_b64_tr_b16 a[124:125],v45 offset:30944
ds_read_b64_tr_b16 a[126:127],v45 offset:36064
v_mfma_f32_16x16x32_bf16 a[60:63],a[188:191],v[248:251],a[60:63]
s_waitcnt lgkmcnt(14)
ds_read_b64_tr_b16 a[128:129],v1 offset:256
ds_read_b64_tr_b16 a[130:131],v1 offset:5376
v_mfma_f32_16x16x32_bf16 a[60:63],a[96:99],v[112:115],a[60:63]
s_waitcnt lgkmcnt(14)
ds_read_b64_tr_b16 a[132:133],v1 offset:10496
ds_read_b64_tr_b16 a[134:135],v1 offset:15616
v_mfma_f32_16x16x32_bf16 a[60:63],a[100:103],v[116:119],a[60:63]
s_waitcnt lgkmcnt(14)
buffer_load_dwordx4 v[84:87],v6,s[32:35],s77 offen offset:1024 sc1
ds_read_b64_tr_b16 a[136:137],v1 offset:20736
ds_read_b64_tr_b16 a[138:139],v1 offset:25856
)A"
R"A(v_mfma_f32_16x16x32_bf16 a[60:63],a[104:107],v[120:123],a[60:63]
s_waitcnt lgkmcnt(14)
ds_read_b64_tr_b16 a[140:141],v1 offset:30976
ds_read_b64_tr_b16 a[142:143],v1 offset:36096
v_mfma_f32_16x16x32_bf16 a[60:63],a[108:111],v[124:127],a[60:63]
s_waitcnt lgkmcnt(14)
ds_read_b64_tr_b16 a[144:145],v42 offset:256
ds_read_b64_tr_b16 a[146:147],v42 offset:5376
v_mfma_f32_16x16x32_bf16 a[60:63],a[112:115],v[228:231],a[60:63]
s_waitcnt lgkmcnt(14)
ds_read_b64_tr_b16 a[148:149],v42 offset:10496
ds_read_b64_tr_b16 a[150:151],v42 offset:15616
v_mfma_f32_16x16x32_bf16 a[60:63],a[116:119],v[236:239],a[60:63]
s_waitcnt lgkmcnt(14)
buffer_load_dwordx4 v[100:103],v6,s[32:35],s78 offen offset:1024 sc1
ds_read_b64_tr_b16 a[152:153],v42 offset:20736
ds_read_b64_tr_b16 a[154:155],v42 offset:25856
v_mfma_f32_16x16x32_bf16 a[60:63],a[120:123],v[244:247],a[60:63]
s_waitcnt lgkmcnt(14)
ds_read_b64_tr_b16 a[156:157],v42 offset:30976
ds_read_b64_tr_b16 a[158:159],v42 offset:36096
v_mfma_f32_16x16x32_bf16 a[60:63],a[124:127],v[252:255],a[60:63]
s_waitcnt lgkmcnt(14)
ds_read_b64_tr_b16 a[160:161],v44 offset:256
ds_read_b64_tr_b16 a[162:163],v44 offset:5376
v_mfma_f32_16x16x32_bf16 a[76:79],a[128:131],v[152:155],a[76:79]
s_waitcnt lgkmcnt(14)
ds_read_b64_tr_b16 a[164:165],v44 offset:10496
ds_read_b64_tr_b16 a[166:167],v44 offset:15616
v_mfma_f32_16x16x32_bf16 a[76:79],a[132:135],v[184:187],a[76:79]
s_waitcnt lgkmcnt(14)
buffer_load_dwordx4 v[72:75],v6,s[32:35],s76 offen offset:2048 sc1
ds_read_b64_tr_b16 a[168:169],v44 offset:20736
ds_read_b64_tr_b16 a[170:171],v44 offset:25856
v_bfe_u32 v24,v6,4,2
v_min_u32 v24,2,v24
v_mfma_f32_16x16x32_bf16 a[76:79],a[136:139],v[216:219],a[76:79]
s_waitcnt lgkmcnt(14)
ds_read_b64_tr_b16 a[172:173],v44 offset:30976
ds_read_b64_tr_b16 a[174:175],v44 offset:36096
v_add_u32 v24,s70,v24
v_add_u32 v24,1,v24
v_mfma_f32_16x16x32_bf16 a[76:79],a[140:143],v[248:251],a[76:79]
s_waitcnt lgkmcnt(14)
ds_read_b64_tr_b16 a[176:177],v2 offset:288
ds_read_b64_tr_b16 a[178:179],v2 offset:5408
v_and_b32 v24,3,v24
v_lshlrev_b32 v24,8,v24
v_mfma_f32_16x16x32_bf16 a[76:79],a[144:147],v[112:115],a[76:79]
s_waitcnt lgkmcnt(14)
ds_read_b64_tr_b16 a[180:181],v2 offset:10528
ds_read_b64_tr_b16 a[182:183],v2 offset:15648
v_mfma_f32_16x16x32_bf16 a[76:79],a[148:151],v[116:119],a[76:79]
s_waitcnt lgkmcnt(14)
buffer_load_dwordx4 v[88:91],v6,s[32:35],s77 offen offset:2048 sc1
ds_read_b64_tr_b16 a[184:185],v2 offset:20768
ds_read_b64_tr_b16 a[186:187],v2 offset:25888
v_mfma_f32_16x16x32_bf16 a[76:79],a[152:155],v[120:123],a[76:79]
s_waitcnt lgkmcnt(14)
ds_read_b64_tr_b16 a[188:189],v2 offset:31008
ds_read_b64_tr_b16 a[190:191],v2 offset:36128
v_mfma_f32_16x16x32_bf16 a[76:79],a[156:159],v[124:127],a[76:79]
s_waitcnt lgkmcnt(14)
ds_read_b64_tr_b16 a[96:97],v43 offset:288
ds_read_b64_tr_b16 a[98:99],v43 offset:5408
v_mfma_f32_16x16x32_bf16 a[76:79],a[160:163],v[228:231],a[76:79]
s_waitcnt lgkmcnt(14)
ds_read_b64_tr_b16 a[100:101],v43 offset:10528
ds_read_b64_tr_b16 a[102:103],v43 offset:15648
v_mfma_f32_16x16x32_bf16 a[76:79],a[164:167],v[236:239],a[76:79]
s_waitcnt lgkmcnt(14)
buffer_load_dwordx4 v[104:107],v6,s[32:35],s78 offen offset:2048 sc1
ds_read_b64_tr_b16 a[104:105],v43 offset:20768
ds_read_b64_tr_b16 a[106:107],v43 offset:25888
v_mfma_f32_16x16x32_bf16 a[76:79],a[168:171],v[244:247],a[76:79]
s_waitcnt lgkmcnt(14)
ds_read_b64_tr_b16 a[108:109],v43 offset:31008
ds_read_b64_tr_b16 a[110:111],v43 offset:36128
v_mfma_f32_16x16x32_bf16 a[76:79],a[172:175],v[252:255],a[76:79]
s_waitcnt lgkmcnt(14)
ds_read_b64_tr_b16 a[112:113],v45 offset:288
ds_read_b64_tr_b16 a[114:115],v45 offset:5408
v_mfma_f32_16x16x32_bf16 a[92:95],a[176:179],v[152:155],a[92:95]
s_waitcnt lgkmcnt(14)
ds_read_b64_tr_b16 a[116:117],v45 offset:10528
ds_read_b64_tr_b16 a[118:119],v45 offset:15648
v_mfma_f32_16x16x32_bf16 a[92:95],a[180:183],v[184:187],a[92:95]
s_waitcnt lgkmcnt(14)
buffer_load_dwordx4 v[76:79],v6,s[32:35],s76 offen offset:3072 sc1
ds_read_b64_tr_b16 a[120:121],v45 offset:20768
ds_read_b64_tr_b16 a[122:123],v45 offset:25888
v_mfma_f32_16x16x32_bf16 a[92:95],a[184:187],v[216:219],a[92:95]
s_waitcnt lgkmcnt(14)
ds_read_b64_tr_b16 a[124:125],v45 offset:31008
ds_read_b64_tr_b16 a[126:127],v45 offset:36128
v_mfma_f32_16x16x32_bf16 a[92:95],a[188:191],v[248:251],a[92:95]
s_waitcnt lgkmcnt(14)
v_mfma_f32_16x16x32_bf16 a[92:95],a[96:99],v[112:115],a[92:95]
s_waitcnt lgkmcnt(12)
v_mfma_f32_16x16x32_bf16 a[92:95],a[100:103],v[116:119],a[92:95]
s_waitcnt lgkmcnt(10)
buffer_load_dwordx4 v[92:95],v6,s[32:35],s77 offen offset:3072 sc1
v_mfma_f32_16x16x32_bf16 a[92:95],a[104:107],v[120:123],a[92:95]
s_waitcnt lgkmcnt(8)
v_mfma_f32_16x16x32_bf16 a[92:95],a[108:111],v[124:127],a[92:95]
s_waitcnt lgkmcnt(6)
v_mfma_f32_16x16x32_bf16 a[92:95],a[112:115],v[228:231],a[92:95]
s_waitcnt lgkmcnt(4)
v_mfma_f32_16x16x32_bf16 a[92:95],a[116:119],v[236:239],a[92:95]
s_waitcnt lgkmcnt(2)
buffer_load_dwordx4 v[108:111],v6,s[32:35],s78 offen offset:3072 sc1
v_mfma_f32_16x16x32_bf16 a[92:95],a[120:123],v[244:247],a[92:95]
s_waitcnt lgkmcnt(0)
v_mfma_f32_16x16x32_bf16 a[92:95],a[124:127],v[252:255],a[92:95]
buffer_load_dwordx2 v[26:27],v24,s[64:67],s94 offen offset:3072 sc1
s_waitcnt vmcnt(0)
v_cmp_ne_u32 s[90:91],s68,v26
v_cmp_ne_u32 s[92:93],s69,v27
s_nop 0
s_or_b64 s[90:91],s[90:91],s[92:93]
s_cbranch_scc1 L_fr11_%=
L_fo11_%=:
buffer_load_dwordx4 v[132:135],v25,s[32:35],s76 offen sc1
buffer_load_dwordx4 v[164:167],v25,s[32:35],s77 offen sc1
buffer_load_dwordx4 v[196:199],v25,s[32:35],s78 offen sc1
v_mfma_f32_16x16x32_bf16 a[204:207],v[34:37],v[64:67],a[204:207]
v_mfma_f32_16x16x32_bf16 a[204:207],v[34:37],v[80:83],a[204:207]
v_mfma_f32_16x16x32_bf16 a[204:207],v[34:37],v[96:99],a[204:207]
v_mfma_f32_16x16x32_bf16 a[220:223],v[32:35],v[64:67],a[220:223]
v_mfma_f32_16x16x32_bf16 a[220:223],v[32:35],v[80:83],a[220:223]
v_mfma_f32_16x16x32_bf16 a[220:223],v[32:35],v[96:99],a[220:223]
v_mfma_f32_16x16x32_bf16 a[236:239],v[34:37],v[68:71],a[236:239]
v_mfma_f32_16x16x32_bf16 a[236:239],v[34:37],v[84:87],a[236:239]
v_mfma_f32_16x16x32_bf16 a[236:239],v[34:37],v[100:103],a[236:239]
s_nop 1
v_accvgpr_read_b32 v128,a204
v_accvgpr_read_b32 v129,a205
v_mfma_f32_16x16x32_bf16 a[252:255],v[32:35],v[68:71],a[252:255]
v_cvt_pk_bf16_f32 v128,v128,v129
v_accvgpr_read_b32 v130,a206
v_mfma_f32_16x16x32_bf16 a[252:255],v[32:35],v[84:87],a[252:255]
v_accvgpr_read_b32 v131,a207
v_cvt_pk_bf16_f32 v129,v130,v131
v_mfma_f32_16x16x32_bf16 a[252:255],v[32:35],v[100:103],a[252:255]
buffer_store_dwordx2 v[128:129],v40,s[20:23],s43 offen
v_mfma_f32_16x16x32_bf16 a[12:15],v[34:37],v[72:75],a[12:15]
v_accvgpr_read_b32 v130,a220
v_accvgpr_read_b32 v131,a221
v_mfma_f32_16x16x32_bf16 a[12:15],v[34:37],v[88:91],a[12:15]
v_accvgpr_read_b32 v14,a222
v_accvgpr_read_b32 v15,a223
v_mfma_f32_16x16x32_bf16 a[12:15],v[34:37],v[104:107],a[12:15]
v_cvt_pk_bf16_f32 v130,v130,v131
v_cvt_pk_bf16_f32 v131,v14,v15
v_mfma_f32_16x16x32_bf16 a[28:31],v[32:35],v[72:75],a[28:31]
buffer_store_dwordx2 v[130:131],v40,s[20:23],s43 offen offset:32
v_mfma_f32_16x16x32_bf16 a[28:31],v[32:35],v[88:91],a[28:31]
v_accvgpr_read_b32 v136,a236
v_accvgpr_read_b32 v137,a237
v_mfma_f32_16x16x32_bf16 a[28:31],v[32:35],v[104:107],a[28:31]
v_cvt_pk_bf16_f32 v136,v136,v137
v_accvgpr_read_b32 v138,a238
v_mfma_f32_16x16x32_bf16 a[44:47],v[34:37],v[76:79],a[44:47]
v_accvgpr_read_b32 v139,a239
v_cvt_pk_bf16_f32 v137,v138,v139
v_mfma_f32_16x16x32_bf16 a[44:47],v[34:37],v[92:95],a[44:47]
buffer_store_dwordx2 v[136:137],v40,s[20:23],s43 offen offset:64
v_mfma_f32_16x16x32_bf16 a[44:47],v[34:37],v[108:111],a[44:47]
v_accvgpr_read_b32 v144,a12
v_accvgpr_read_b32 v145,a13
v_cvt_pk_bf16_f32 v144,v144,v145
v_accvgpr_read_b32 v146,a14
v_accvgpr_read_b32 v147,a15
v_mfma_f32_16x16x32_bf16 a[60:63],v[32:35],v[76:79],a[60:63]
v_accvgpr_read_b32 v138,a252
v_accvgpr_read_b32 v139,a253
v_cvt_pk_bf16_f32 v145,v146,v147
v_mfma_f32_16x16x32_bf16 a[60:63],v[32:35],v[92:95],a[60:63]
v_accvgpr_read_b32 v16,a254
v_accvgpr_read_b32 v17,a255
v_cvt_pk_bf16_f32 v138,v138,v139
v_cvt_pk_bf16_f32 v139,v16,v17
v_mfma_f32_16x16x32_bf16 a[60:63],v[32:35],v[108:111],a[60:63]
s_waitcnt vmcnt(5)
buffer_store_dwordx2 v[138:139],v40,s[20:23],s43 offen offset:96
buffer_store_dwordx2 v[144:145],v40,s[20:23],s43 offen offset:128
v_accvgpr_read_b32 v146,a28
v_accvgpr_read_b32 v147,a29
v_accvgpr_read_b32 v12,a30
v_accvgpr_read_b32 v13,a31
v_cvt_pk_bf16_f32 v146,v146,v147
v_cvt_pk_bf16_f32 v147,v12,v13
s_nop 3
buffer_store_dwordx2 v[146:147],v40,s[20:23],s43 offen offset:160
v_accvgpr_read_b32 v160,a44
v_accvgpr_read_b32 v161,a45
v_cvt_pk_bf16_f32 v160,v160,v161
v_accvgpr_read_b32 v162,a46
v_accvgpr_read_b32 v163,a47
v_cvt_pk_bf16_f32 v161,v162,v163
buffer_store_dwordx2 v[160:161],v40,s[20:23],s43 offen offset:192
v_accvgpr_read_b32 v162,a60
v_accvgpr_read_b32 v163,a61
v_accvgpr_read_b32 v14,a62
v_accvgpr_read_b32 v15,a63
v_cvt_pk_bf16_f32 v162,v162,v163
v_cvt_pk_bf16_f32 v163,v14,v15
buffer_store_dwordx2 v[162:163],v40,s[20:23],s43 offen offset:224
v_mfma_f32_16x16x32_bf16 a[76:79],v[34:37],v[132:135],a[76:79]
s_waitcnt vmcnt(9)
v_mfma_f32_16x16x32_bf16 a[76:79],v[34:37],v[164:167],a[76:79]
s_waitcnt vmcnt(8)
v_mfma_f32_16x16x32_bf16 a[76:79],v[34:37],v[196:199],a[76:79]
v_mfma_f32_16x16x32_bf16 a[92:95],v[32:35],v[132:135],a[92:95]
v_mfma_f32_16x16x32_bf16 a[92:95],v[32:35],v[164:167],a[92:95]
v_mfma_f32_16x16x32_bf16 a[92:95],v[32:35],v[196:199],a[92:95]
s_nop 3
s_nop 0
v_accvgpr_read_b32 v168,a76
v_accvgpr_read_b32 v169,a77
v_cvt_pk_bf16_f32 v168,v168,v169
v_accvgpr_read_b32 v170,a78
v_accvgpr_read_b32 v171,a79
v_cvt_pk_bf16_f32 v169,v170,v171
buffer_store_dwordx2 v[168:169],v40,s[20:23],s43 offen offset:256
v_accvgpr_read_b32 v170,a92
v_accvgpr_read_b32 v171,a93
v_accvgpr_read_b32 v16,a94
v_accvgpr_read_b32 v17,a95
v_cvt_pk_bf16_f32 v170,v170,v171
v_cvt_pk_bf16_f32 v171,v16,v17
buffer_store_dwordx2 v[170:171],v40,s[20:23],s43 offen offset:288
s_or_b32 s62,s42,s40
s_cmp_lg_u32 s62,0
s_cbranch_scc1 L_noep_%=
v_mov_b32 v12,s68
s_mov_b64 exec,1
buffer_store_dword v12,v25,s[64:67],0 offen sc1
s_mov_b64 exec,-1
L_noep_%=:
s_waitcnt vmcnt(0) lgkmcnt(0)
s_branch L_end_%=
L_ps2_%=:
s_min_i32 s79,s79,100
s_add_u32 s78,s78,s79
L_pl2_%=:
s_sleep 1
s_sub_u32 s79,s79,4
s_cmp_gt_i32 s79,1
s_cbranch_scc1 L_pl2_%=
s_branch L_pr2_%=
L_ps3_%=:
s_min_i32 s79,s79,100
s_add_u32 s78,s78,s79
L_pl3_%=:
s_sleep 1
s_sub_u32 s79,s79,4
s_cmp_gt_i32 s79,1
s_cbranch_scc1 L_pl3_%=
s_branch L_pr3_%=
L_ps4_%=:
s_min_i32 s79,s79,100
s_add_u32 s78,s78,s79
L_pl4_%=:
s_sleep 1
s_sub_u32 s79,s79,4
s_cmp_gt_i32 s79,1
s_cbranch_scc1 L_pl4_%=
s_branch L_pr4_%=
L_ps5_%=:
s_min_i32 s79,s79,100
s_add_u32 s78,s78,s79
L_pl5_%=:
s_sleep 1
s_sub_u32 s79,s79,4
s_cmp_gt_i32 s79,1
s_cbranch_scc1 L_pl5_%=
s_branch L_pr5_%=
L_ps6_%=:
s_min_i32 s79,s79,100
s_add_u32 s78,s78,s79
L_pl6_%=:
s_sleep 1
s_sub_u32 s79,s79,4
s_cmp_gt_i32 s79,1
s_cbranch_scc1 L_pl6_%=
s_branch L_pr6_%=
L_ps7_%=:
s_min_i32 s79,s79,100
s_add_u32 s78,s78,s79
L_pl7_%=:
s_sleep 1
s_sub_u32 s79,s79,4
s_cmp_gt_i32 s79,1
s_cbranch_scc1 L_pl7_%=
s_branch L_pr7_%=
L_fr8_%=:
s_mov_b32 s88,262144
L_fl8_%=:
buffer_load_dwordx2 v[26:27],v24,s[64:67],s94 offen sc1
s_waitcnt vmcnt(0)
s_sub_u32 s88,s88,1
s_cmp_eq_u32 s88,0
s_cbranch_scc1 L_fo8_%=
v_cmp_ne_u32 s[90:91],s68,v26
v_cmp_ne_u32 s[92:93],s69,v27
s_nop 0
s_or_b64 s[90:91],s[90:91],s[92:93]
s_cbranch_scc1 L_fl8_%=
s_branch L_fo8_%=
L_fr9_%=:
s_mov_b32 s88,262144
L_fl9_%=:
buffer_load_dwordx2 v[26:27],v24,s[64:67],s94 offen offset:1024 sc1
s_waitcnt vmcnt(0)
s_sub_u32 s88,s88,1
s_cmp_eq_u32 s88,0
s_cbranch_scc1 L_fo9_%=
v_cmp_ne_u32 s[90:91],s68,v26
v_cmp_ne_u32 s[92:93],s69,v27
s_nop 0
s_or_b64 s[90:91],s[90:91],s[92:93]
s_cbranch_scc1 L_fl9_%=
s_branch L_fo9_%=
L_fr10_%=:
s_mov_b32 s88,262144
L_fl10_%=:
buffer_load_dwordx2 v[26:27],v24,s[64:67],s94 offen offset:2048 sc1
s_waitcnt vmcnt(0)
s_sub_u32 s88,s88,1
s_cmp_eq_u32 s88,0
s_cbranch_scc1 L_fo10_%=
v_cmp_ne_u32 s[90:91],s68,v26
v_cmp_ne_u32 s[92:93],s69,v27
)A"
R"A(s_nop 0
s_or_b64 s[90:91],s[90:91],s[92:93]
s_cbranch_scc1 L_fl10_%=
s_branch L_fo10_%=
L_fr11_%=:
s_mov_b32 s88,262144
L_fl11_%=:
buffer_load_dwordx2 v[26:27],v24,s[64:67],s94 offen offset:3072 sc1
s_waitcnt vmcnt(0)
s_sub_u32 s88,s88,1
s_cmp_eq_u32 s88,0
s_cbranch_scc1 L_fo11_%=
v_cmp_ne_u32 s[90:91],s68,v26
v_cmp_ne_u32 s[92:93],s69,v27
s_nop 0
s_or_b64 s[90:91],s[90:91],s[92:93]
s_cbranch_scc1 L_fl11_%=
s_branch L_fo11_%=
L_fast_end_%=:
L_slow0_%=:
s_mov_b32 s98,0
s_branch L_slowc_%=
L_slow1_%=:
s_mov_b32 s98,1
L_slowc_%=:
s_nop 7
s_nop 7
s_waitcnt vmcnt(0) lgkmcnt(0)
s_lshl_b32 s96,%1,2
s_add_u32 s96,s96,s40
s_lshl_b32 s96,s96,15
s_add_u32 s96,s96,167772160
s_mov_b64 s[56:57],s[12:13]
s_add_u32 s56,s56,s96
s_addc_u32 s57,s57,0
s_and_b32 s57,s57,0xffff
s_mov_b32 s58,32768
s_mov_b32 s59,0x20000
s_mov_b32 s96,0
buffer_store_dwordx4 v[64:67],v6,s[56:59],s96 offen
buffer_store_dwordx4 v[68:71],v6,s[56:59],s96 offen offset:1024
buffer_store_dwordx4 v[72:75],v6,s[56:59],s96 offen offset:2048
buffer_store_dwordx4 v[76:79],v6,s[56:59],s96 offen offset:3072
s_mov_b32 s96,4096
buffer_store_dwordx4 v[80:83],v6,s[56:59],s96 offen
buffer_store_dwordx4 v[84:87],v6,s[56:59],s96 offen offset:1024
buffer_store_dwordx4 v[88:91],v6,s[56:59],s96 offen offset:2048
buffer_store_dwordx4 v[92:95],v6,s[56:59],s96 offen offset:3072
s_mov_b32 s96,8192
buffer_store_dwordx4 v[96:99],v6,s[56:59],s96 offen
buffer_store_dwordx4 v[100:103],v6,s[56:59],s96 offen offset:1024
buffer_store_dwordx4 v[104:107],v6,s[56:59],s96 offen offset:2048
buffer_store_dwordx4 v[108:111],v6,s[56:59],s96 offen offset:3072
s_mov_b32 s96,12288
buffer_store_dwordx4 v[112:115],v6,s[56:59],s96 offen
buffer_store_dwordx4 v[116:119],v6,s[56:59],s96 offen offset:1024
buffer_store_dwordx4 v[120:123],v6,s[56:59],s96 offen offset:2048
buffer_store_dwordx4 v[124:127],v6,s[56:59],s96 offen offset:3072
s_mov_b32 s96,16384
buffer_store_dwordx4 v[128:131],v6,s[56:59],s96 offen
buffer_store_dwordx4 v[132:135],v6,s[56:59],s96 offen offset:1024
buffer_store_dwordx4 v[136:139],v6,s[56:59],s96 offen offset:2048
buffer_store_dwordx4 v[140:143],v6,s[56:59],s96 offen offset:3072
s_mov_b32 s96,20480
buffer_store_dwordx4 v[144:147],v6,s[56:59],s96 offen
buffer_store_dwordx4 v[148:151],v6,s[56:59],s96 offen offset:1024
buffer_store_dwordx4 v[152:155],v6,s[56:59],s96 offen offset:2048
buffer_store_dwordx4 v[156:159],v6,s[56:59],s96 offen offset:3072
s_mov_b32 s96,24576
buffer_store_dwordx4 v[160:163],v6,s[56:59],s96 offen
buffer_store_dwordx4 v[164:167],v6,s[56:59],s96 offen offset:1024
buffer_store_dwordx4 v[168:171],v6,s[56:59],s96 offen offset:2048
buffer_store_dwordx4 v[172:175],v6,s[56:59],s96 offen offset:3072
s_mov_b32 s96,28672
buffer_store_dwordx4 v[176:179],v6,s[56:59],s96 offen
buffer_store_dwordx4 v[180:183],v6,s[56:59],s96 offen offset:1024
buffer_store_dwordx4 v[184:187],v6,s[56:59],s96 offen offset:2048
buffer_store_dwordx4 v[188:191],v6,s[56:59],s96 offen offset:3072
s_waitcnt vmcnt(0)
v_writelane_b32 v173,s88,0
v_writelane_b32 v173,s89,1
v_writelane_b32 v173,s90,2
v_writelane_b32 v173,s91,3
v_writelane_b32 v173,s92,4
v_writelane_b32 v173,s93,5
v_writelane_b32 v173,s94,6
v_writelane_b32 v173,s95,7
s_mov_b32 s88,s98
s_mul_i32 s90,s41,307200
s_mov_b64 s[92:93],s[4:5]
s_add_u32 s92,s92,s90
s_addc_u32 s93,s93,0
s_and_b32 s93,s93,0xffff
s_mov_b32 s94,307200
s_mov_b32 s95,0x20000
s_mov_b64 s[96:97],s[10:11]
s_and_b32 s97,s97,0xffff
s_mov_b32 s98,5242880
s_mov_b32 s99,0x20000
v_bfe_u32 v171,v6,4,4
v_lshrrev_b32 v172,8,v6
v_lshlrev_b32 v164,8,v171
v_lshl_add_u32 v164,v172,4,v164
v_lshlrev_b32 v165,10,v172
v_lshl_add_u32 v165,v171,1,v165
v_add_u32 v166,4096,v165
v_lshlrev_b32 v168,16,v172
v_lshrrev_b32 v170,2,v171
v_lshl_add_u32 v168,v170,4,v168
v_and_b32 v170,3,v171
v_lshl_add_u32 v168,v170,1,v168
s_lshl_b32 s90,s42,1
s_add_u32 s90,s90,s88
s_lshl_b32 s90,s90,8
v_add_u32 v168,s90,v168
s_mov_b32 s89,0
L_sjb_%=:
s_add_u32 s90,s89,s70
s_add_u32 s90,s90,1
s_and_b32 s90,s90,3
s_lshl_b32 s91,s40,2
s_add_u32 s90,s90,s91
s_lshl_b32 s90,s90,4
v_bfe_u32 v171,v6,4,4
v_lshrrev_b32 v172,8,v6
v_add_u32 v167,s90,v171
v_mul_u32_u24 v167,1280,v167
v_lshl_add_u32 v167,v172,4,v167
s_lshl_b32 s90,s88,4
s_add_u32 s90,s90,s89
s_lshl_b32 s90,s90,12
v_add_u32 v169,s90,v6
v_mov_b32 v64,0
v_mov_b32 v65,0
v_mov_b32 v66,0
v_mov_b32 v67,0
v_mov_b32 v68,0
v_mov_b32 v69,0
v_mov_b32 v70,0
v_mov_b32 v71,0
v_mov_b32 v72,0
v_mov_b32 v73,0
v_mov_b32 v74,0
v_mov_b32 v75,0
v_mov_b32 v76,0
v_mov_b32 v77,0
v_mov_b32 v78,0
v_mov_b32 v79,0
v_mov_b32 v80,0
v_mov_b32 v81,0
v_mov_b32 v82,0
v_mov_b32 v83,0
v_mov_b32 v84,0
v_mov_b32 v85,0
v_mov_b32 v86,0
v_mov_b32 v87,0
v_mov_b32 v88,0
v_mov_b32 v89,0
v_mov_b32 v90,0
v_mov_b32 v91,0
v_mov_b32 v92,0
v_mov_b32 v93,0
v_mov_b32 v94,0
v_mov_b32 v95,0
s_mov_b32 s90,0
L_sq_%=:
s_lshl_b32 s91,s90,6
buffer_load_dwordx4 v[160:163],v167,s[92:95],s91 offen
s_lshl_b32 s91,s90,18
buffer_load_ushort v112,v168,s[96:99],s91 offen
buffer_load_ushort v120,v168,s[96:99],s91 offen offset:8
buffer_load_ushort v128,v168,s[96:99],s91 offen offset:64
buffer_load_ushort v136,v168,s[96:99],s91 offen offset:72
s_add_u32 s91,s91,8192
buffer_load_ushort v113,v168,s[96:99],s91 offen
buffer_load_ushort v121,v168,s[96:99],s91 offen offset:8
buffer_load_ushort v129,v168,s[96:99],s91 offen offset:64
buffer_load_ushort v137,v168,s[96:99],s91 offen offset:72
s_add_u32 s91,s91,8192
buffer_load_ushort v114,v168,s[96:99],s91 offen
buffer_load_ushort v122,v168,s[96:99],s91 offen offset:8
buffer_load_ushort v130,v168,s[96:99],s91 offen offset:64
buffer_load_ushort v138,v168,s[96:99],s91 offen offset:72
s_add_u32 s91,s91,8192
buffer_load_ushort v115,v168,s[96:99],s91 offen
buffer_load_ushort v123,v168,s[96:99],s91 offen offset:8
buffer_load_ushort v131,v168,s[96:99],s91 offen offset:64
buffer_load_ushort v139,v168,s[96:99],s91 offen offset:72
s_add_u32 s91,s91,8192
buffer_load_ushort v116,v168,s[96:99],s91 offen
buffer_load_ushort v124,v168,s[96:99],s91 offen offset:8
buffer_load_ushort v132,v168,s[96:99],s91 offen offset:64
buffer_load_ushort v140,v168,s[96:99],s91 offen offset:72
s_add_u32 s91,s91,8192
buffer_load_ushort v117,v168,s[96:99],s91 offen
buffer_load_ushort v125,v168,s[96:99],s91 offen offset:8
buffer_load_ushort v133,v168,s[96:99],s91 offen offset:64
buffer_load_ushort v141,v168,s[96:99],s91 offen offset:72
s_add_u32 s91,s91,8192
buffer_load_ushort v118,v168,s[96:99],s91 offen
buffer_load_ushort v126,v168,s[96:99],s91 offen offset:8
buffer_load_ushort v134,v168,s[96:99],s91 offen offset:64
buffer_load_ushort v142,v168,s[96:99],s91 offen offset:72
s_add_u32 s91,s91,8192
buffer_load_ushort v119,v168,s[96:99],s91 offen
buffer_load_ushort v127,v168,s[96:99],s91 offen offset:8
buffer_load_ushort v135,v168,s[96:99],s91 offen offset:64
buffer_load_ushort v143,v168,s[96:99],s91 offen offset:72
s_add_u32 s91,s91,8192
s_waitcnt vmcnt(0)
v_lshl_or_b32 v112,v113,16,v112
v_lshl_or_b32 v113,v115,16,v114
v_lshl_or_b32 v114,v117,16,v116
v_lshl_or_b32 v115,v119,16,v118
v_lshl_or_b32 v120,v121,16,v120
v_lshl_or_b32 v121,v123,16,v122
v_lshl_or_b32 v122,v125,16,v124
v_lshl_or_b32 v123,v127,16,v126
v_lshl_or_b32 v128,v129,16,v128
v_lshl_or_b32 v129,v131,16,v130
v_lshl_or_b32 v130,v133,16,v132
v_lshl_or_b32 v131,v135,16,v134
v_lshl_or_b32 v136,v137,16,v136
v_lshl_or_b32 v137,v139,16,v138
v_lshl_or_b32 v138,v141,16,v140
v_lshl_or_b32 v139,v143,16,v142
s_nop 1
v_mfma_f32_16x16x32_bf16 v[64:67],v[112:115],v[160:163],v[64:67]
v_mfma_f32_16x16x32_bf16 v[68:71],v[120:123],v[160:163],v[68:71]
v_mfma_f32_16x16x32_bf16 v[72:75],v[128:131],v[160:163],v[72:75]
v_mfma_f32_16x16x32_bf16 v[76:79],v[136:139],v[160:163],v[76:79]
s_nop 7
s_lshl_b32 s91,s90,18
buffer_load_ushort v112,v168,s[96:99],s91 offen offset:128
buffer_load_ushort v120,v168,s[96:99],s91 offen offset:136
buffer_load_ushort v128,v168,s[96:99],s91 offen offset:192
buffer_load_ushort v136,v168,s[96:99],s91 offen offset:200
s_add_u32 s91,s91,8192
buffer_load_ushort v113,v168,s[96:99],s91 offen offset:128
buffer_load_ushort v121,v168,s[96:99],s91 offen offset:136
buffer_load_ushort v129,v168,s[96:99],s91 offen offset:192
buffer_load_ushort v137,v168,s[96:99],s91 offen offset:200
s_add_u32 s91,s91,8192
buffer_load_ushort v114,v168,s[96:99],s91 offen offset:128
buffer_load_ushort v122,v168,s[96:99],s91 offen offset:136
buffer_load_ushort v130,v168,s[96:99],s91 offen offset:192
buffer_load_ushort v138,v168,s[96:99],s91 offen offset:200
s_add_u32 s91,s91,8192
buffer_load_ushort v115,v168,s[96:99],s91 offen offset:128
buffer_load_ushort v123,v168,s[96:99],s91 offen offset:136
buffer_load_ushort v131,v168,s[96:99],s91 offen offset:192
buffer_load_ushort v139,v168,s[96:99],s91 offen offset:200
s_add_u32 s91,s91,8192
buffer_load_ushort v116,v168,s[96:99],s91 offen offset:128
buffer_load_ushort v124,v168,s[96:99],s91 offen offset:136
buffer_load_ushort v132,v168,s[96:99],s91 offen offset:192
buffer_load_ushort v140,v168,s[96:99],s91 offen offset:200
s_add_u32 s91,s91,8192
buffer_load_ushort v117,v168,s[96:99],s91 offen offset:128
buffer_load_ushort v125,v168,s[96:99],s91 offen offset:136
buffer_load_ushort v133,v168,s[96:99],s91 offen offset:192
buffer_load_ushort v141,v168,s[96:99],s91 offen offset:200
s_add_u32 s91,s91,8192
buffer_load_ushort v118,v168,s[96:99],s91 offen offset:128
buffer_load_ushort v126,v168,s[96:99],s91 offen offset:136
buffer_load_ushort v134,v168,s[96:99],s91 offen offset:192
buffer_load_ushort v142,v168,s[96:99],s91 offen offset:200
s_add_u32 s91,s91,8192
buffer_load_ushort v119,v168,s[96:99],s91 offen offset:128
buffer_load_ushort v127,v168,s[96:99],s91 offen offset:136
buffer_load_ushort v135,v168,s[96:99],s91 offen offset:192
buffer_load_ushort v143,v168,s[96:99],s91 offen offset:200
s_add_u32 s91,s91,8192
s_waitcnt vmcnt(0)
v_lshl_or_b32 v112,v113,16,v112
v_lshl_or_b32 v113,v115,16,v114
v_lshl_or_b32 v114,v117,16,v116
v_lshl_or_b32 v115,v119,16,v118
v_lshl_or_b32 v120,v121,16,v120
v_lshl_or_b32 v121,v123,16,v122
v_lshl_or_b32 v122,v125,16,v124
v_lshl_or_b32 v123,v127,16,v126
v_lshl_or_b32 v128,v129,16,v128
v_lshl_or_b32 v129,v131,16,v130
v_lshl_or_b32 v130,v133,16,v132
v_lshl_or_b32 v131,v135,16,v134
v_lshl_or_b32 v136,v137,16,v136
v_lshl_or_b32 v137,v139,16,v138
v_lshl_or_b32 v138,v141,16,v140
v_lshl_or_b32 v139,v143,16,v142
s_nop 1
v_mfma_f32_16x16x32_bf16 v[80:83],v[112:115],v[160:163],v[80:83]
v_mfma_f32_16x16x32_bf16 v[84:87],v[120:123],v[160:163],v[84:87]
v_mfma_f32_16x16x32_bf16 v[88:91],v[128:131],v[160:163],v[88:91]
v_mfma_f32_16x16x32_bf16 v[92:95],v[136:139],v[160:163],v[92:95]
s_nop 7
s_add_u32 s90,s90,1
s_cmp_lg_u32 s90,20
s_cbranch_scc1 L_sq_%=
s_nop 7
v_mul_f32 v64,s61,v64
v_mul_f32 v65,s61,v65
v_mul_f32 v66,s61,v66
v_mul_f32 v67,s61,v67
v_mul_f32 v68,s61,v68
v_mul_f32 v69,s61,v69
v_mul_f32 v70,s61,v70
v_mul_f32 v71,s61,v71
v_mul_f32 v72,s61,v72
v_mul_f32 v73,s61,v73
v_mul_f32 v74,s61,v74
v_mul_f32 v75,s61,v75
v_mul_f32 v76,s61,v76
v_mul_f32 v77,s61,v77
v_mul_f32 v78,s61,v78
v_mul_f32 v79,s61,v79
v_mul_f32 v80,s61,v80
v_mul_f32 v81,s61,v81
v_mul_f32 v82,s61,v82
v_mul_f32 v83,s61,v83
v_mul_f32 v84,s61,v84
v_mul_f32 v85,s61,v85
v_mul_f32 v86,s61,v86
v_mul_f32 v87,s61,v87
v_mul_f32 v88,s61,v88
v_mul_f32 v89,s61,v89
v_mul_f32 v90,s61,v90
v_mul_f32 v91,s61,v91
v_mul_f32 v92,s61,v92
v_mul_f32 v93,s61,v93
v_mul_f32 v94,s61,v94
v_mul_f32 v95,s61,v95
v_cvt_pk_bf16_f32 v96,v64,v65
v_cvt_pk_bf16_f32 v97,v66,v67
v_cvt_pk_bf16_f32 v98,v68,v69
v_cvt_pk_bf16_f32 v99,v70,v71
v_cvt_pk_bf16_f32 v100,v72,v73
v_cvt_pk_bf16_f32 v101,v74,v75
v_cvt_pk_bf16_f32 v102,v76,v77
v_cvt_pk_bf16_f32 v103,v78,v79
v_cvt_pk_bf16_f32 v104,v80,v81
v_cvt_pk_bf16_f32 v105,v82,v83
v_cvt_pk_bf16_f32 v106,v84,v85
v_cvt_pk_bf16_f32 v107,v86,v87
v_cvt_pk_bf16_f32 v108,v88,v89
v_cvt_pk_bf16_f32 v109,v90,v91
v_cvt_pk_bf16_f32 v110,v92,v93
v_cvt_pk_bf16_f32 v111,v94,v95
v_mov_b32 v170,0xff800000
s_lshl_b32 s91,s88,18
s_mov_b32 s90,64
L_s1_%=:
buffer_load_dwordx4 v[112:115],v164,s[20:23],s91 offen
buffer_load_dwordx4 v[116:119],v164,s[20:23],s91 offen offset:64
)A"
R"A(buffer_load_dwordx4 v[120:123],v164,s[20:23],s91 offen offset:128
buffer_load_dwordx4 v[124:127],v164,s[20:23],s91 offen offset:192
s_waitcnt vmcnt(0)
v_mfma_f32_16x16x32_bf16 v[144:147],v[112:115],v[96:99],0
v_mfma_f32_16x16x32_bf16 v[144:147],v[116:119],v[100:103],v[144:147]
v_mfma_f32_16x16x32_bf16 v[144:147],v[120:123],v[104:107],v[144:147]
v_mfma_f32_16x16x32_bf16 v[144:147],v[124:127],v[108:111],v[144:147]
s_nop 7
v_max3_f32 v170,v170,v144,v145
v_max3_f32 v170,v170,v146,v147
s_add_u32 s91,s91,4096
s_sub_u32 s90,s90,1
s_cmp_lg_u32 s90,0
s_cbranch_scc1 L_s1_%=
v_mov_b32 v171,v170
s_nop 1
v_permlane32_swap_b32 v170,v171
v_max_f32 v170,v170,v171
v_mov_b32 v171,v170
s_nop 1
v_permlane16_swap_b32 v170,v171
v_max_f32 v170,v170,v171
v_sub_f32 v152,0,v170
v_mov_b32 v156,0
v_sub_f32 v153,0,v170
v_mov_b32 v157,0
v_sub_f32 v154,0,v170
v_mov_b32 v158,0
v_sub_f32 v155,0,v170
v_mov_b32 v159,0
v_mov_b32 v64,0
v_mov_b32 v65,0
v_mov_b32 v66,0
v_mov_b32 v67,0
v_mov_b32 v68,0
v_mov_b32 v69,0
v_mov_b32 v70,0
v_mov_b32 v71,0
v_mov_b32 v72,0
v_mov_b32 v73,0
v_mov_b32 v74,0
v_mov_b32 v75,0
v_mov_b32 v76,0
v_mov_b32 v77,0
v_mov_b32 v78,0
v_mov_b32 v79,0
v_mov_b32 v80,0
v_mov_b32 v81,0
v_mov_b32 v82,0
v_mov_b32 v83,0
v_mov_b32 v84,0
v_mov_b32 v85,0
v_mov_b32 v86,0
v_mov_b32 v87,0
v_mov_b32 v88,0
v_mov_b32 v89,0
v_mov_b32 v90,0
v_mov_b32 v91,0
v_mov_b32 v92,0
v_mov_b32 v93,0
v_mov_b32 v94,0
v_mov_b32 v95,0
s_lshl_b32 s91,s88,18
s_mov_b32 s90,32
v_add_u32 v172,4096,v164
L_s2_%=:
buffer_load_dwordx4 v[112:115],v164,s[20:23],s91 offen
buffer_load_dwordx4 v[116:119],v164,s[20:23],s91 offen offset:64
buffer_load_dwordx4 v[120:123],v164,s[20:23],s91 offen offset:128
buffer_load_dwordx4 v[124:127],v164,s[20:23],s91 offen offset:192
buffer_load_dwordx4 v[128:131],v172,s[20:23],s91 offen
buffer_load_dwordx4 v[132:135],v172,s[20:23],s91 offen offset:64
buffer_load_dwordx4 v[136:139],v172,s[20:23],s91 offen offset:128
buffer_load_dwordx4 v[140:143],v172,s[20:23],s91 offen offset:192
s_waitcnt vmcnt(0)
v_mfma_f32_16x16x32_bf16 v[144:147],v[112:115],v[96:99],v[152:155]
v_mfma_f32_16x16x32_bf16 v[144:147],v[116:119],v[100:103],v[144:147]
v_mfma_f32_16x16x32_bf16 v[144:147],v[120:123],v[104:107],v[144:147]
v_mfma_f32_16x16x32_bf16 v[144:147],v[124:127],v[108:111],v[144:147]
v_mfma_f32_16x16x32_bf16 v[148:151],v[128:131],v[96:99],v[152:155]
v_mfma_f32_16x16x32_bf16 v[148:151],v[132:135],v[100:103],v[148:151]
v_mfma_f32_16x16x32_bf16 v[148:151],v[136:139],v[104:107],v[148:151]
v_mfma_f32_16x16x32_bf16 v[148:151],v[140:143],v[108:111],v[148:151]
s_nop 7
v_exp_f32 v144,v144
v_exp_f32 v145,v145
v_exp_f32 v146,v146
v_exp_f32 v147,v147
v_exp_f32 v148,v148
v_exp_f32 v149,v149
v_exp_f32 v150,v150
v_exp_f32 v151,v151
s_nop 0
v_cvt_pk_bf16_f32 v144,v144,v145
v_cvt_pk_bf16_f32 v145,v146,v147
v_cvt_pk_bf16_f32 v146,v148,v149
v_cvt_pk_bf16_f32 v147,v150,v151
buffer_load_ushort v112,v165,s[24:27],s91 offen
buffer_load_ushort v113,v165,s[24:27],s91 offen offset:256
buffer_load_ushort v114,v165,s[24:27],s91 offen offset:512
buffer_load_ushort v115,v165,s[24:27],s91 offen offset:768
buffer_load_ushort v116,v166,s[24:27],s91 offen
buffer_load_ushort v117,v166,s[24:27],s91 offen offset:256
buffer_load_ushort v118,v166,s[24:27],s91 offen offset:512
buffer_load_ushort v119,v166,s[24:27],s91 offen offset:768
buffer_load_ushort v120,v165,s[24:27],s91 offen offset:32
buffer_load_ushort v121,v165,s[24:27],s91 offen offset:288
buffer_load_ushort v122,v165,s[24:27],s91 offen offset:544
buffer_load_ushort v123,v165,s[24:27],s91 offen offset:800
buffer_load_ushort v124,v166,s[24:27],s91 offen offset:32
buffer_load_ushort v125,v166,s[24:27],s91 offen offset:288
buffer_load_ushort v126,v166,s[24:27],s91 offen offset:544
buffer_load_ushort v127,v166,s[24:27],s91 offen offset:800
buffer_load_ushort v128,v165,s[24:27],s91 offen offset:64
buffer_load_ushort v129,v165,s[24:27],s91 offen offset:320
buffer_load_ushort v130,v165,s[24:27],s91 offen offset:576
buffer_load_ushort v131,v165,s[24:27],s91 offen offset:832
buffer_load_ushort v132,v166,s[24:27],s91 offen offset:64
buffer_load_ushort v133,v166,s[24:27],s91 offen offset:320
buffer_load_ushort v134,v166,s[24:27],s91 offen offset:576
buffer_load_ushort v135,v166,s[24:27],s91 offen offset:832
buffer_load_ushort v136,v165,s[24:27],s91 offen offset:96
buffer_load_ushort v137,v165,s[24:27],s91 offen offset:352
buffer_load_ushort v138,v165,s[24:27],s91 offen offset:608
buffer_load_ushort v139,v165,s[24:27],s91 offen offset:864
buffer_load_ushort v140,v166,s[24:27],s91 offen offset:96
buffer_load_ushort v141,v166,s[24:27],s91 offen offset:352
buffer_load_ushort v142,v166,s[24:27],s91 offen offset:608
buffer_load_ushort v143,v166,s[24:27],s91 offen offset:864
s_waitcnt vmcnt(0)
v_lshl_or_b32 v112,v113,16,v112
v_lshl_or_b32 v113,v115,16,v114
v_lshl_or_b32 v114,v117,16,v116
v_lshl_or_b32 v115,v119,16,v118
v_lshl_or_b32 v120,v121,16,v120
v_lshl_or_b32 v121,v123,16,v122
v_lshl_or_b32 v122,v125,16,v124
v_lshl_or_b32 v123,v127,16,v126
v_lshl_or_b32 v128,v129,16,v128
v_lshl_or_b32 v129,v131,16,v130
v_lshl_or_b32 v130,v133,16,v132
v_lshl_or_b32 v131,v135,16,v134
v_lshl_or_b32 v136,v137,16,v136
v_lshl_or_b32 v137,v139,16,v138
v_lshl_or_b32 v138,v141,16,v140
v_lshl_or_b32 v139,v143,16,v142
s_nop 1
v_mfma_f32_16x16x32_bf16 v[64:67],v[112:115],v[144:147],v[64:67]
v_mfma_f32_16x16x32_bf16 v[68:71],v[120:123],v[144:147],v[68:71]
v_mfma_f32_16x16x32_bf16 v[72:75],v[128:131],v[144:147],v[72:75]
v_mfma_f32_16x16x32_bf16 v[76:79],v[136:139],v[144:147],v[76:79]
s_nop 7
buffer_load_ushort v112,v165,s[24:27],s91 offen offset:128
buffer_load_ushort v113,v165,s[24:27],s91 offen offset:384
buffer_load_ushort v114,v165,s[24:27],s91 offen offset:640
buffer_load_ushort v115,v165,s[24:27],s91 offen offset:896
buffer_load_ushort v116,v166,s[24:27],s91 offen offset:128
buffer_load_ushort v117,v166,s[24:27],s91 offen offset:384
buffer_load_ushort v118,v166,s[24:27],s91 offen offset:640
buffer_load_ushort v119,v166,s[24:27],s91 offen offset:896
buffer_load_ushort v120,v165,s[24:27],s91 offen offset:160
buffer_load_ushort v121,v165,s[24:27],s91 offen offset:416
buffer_load_ushort v122,v165,s[24:27],s91 offen offset:672
buffer_load_ushort v123,v165,s[24:27],s91 offen offset:928
buffer_load_ushort v124,v166,s[24:27],s91 offen offset:160
buffer_load_ushort v125,v166,s[24:27],s91 offen offset:416
buffer_load_ushort v126,v166,s[24:27],s91 offen offset:672
buffer_load_ushort v127,v166,s[24:27],s91 offen offset:928
buffer_load_ushort v128,v165,s[24:27],s91 offen offset:192
buffer_load_ushort v129,v165,s[24:27],s91 offen offset:448
buffer_load_ushort v130,v165,s[24:27],s91 offen offset:704
buffer_load_ushort v131,v165,s[24:27],s91 offen offset:960
buffer_load_ushort v132,v166,s[24:27],s91 offen offset:192
buffer_load_ushort v133,v166,s[24:27],s91 offen offset:448
buffer_load_ushort v134,v166,s[24:27],s91 offen offset:704
buffer_load_ushort v135,v166,s[24:27],s91 offen offset:960
buffer_load_ushort v136,v165,s[24:27],s91 offen offset:224
buffer_load_ushort v137,v165,s[24:27],s91 offen offset:480
buffer_load_ushort v138,v165,s[24:27],s91 offen offset:736
buffer_load_ushort v139,v165,s[24:27],s91 offen offset:992
buffer_load_ushort v140,v166,s[24:27],s91 offen offset:224
buffer_load_ushort v141,v166,s[24:27],s91 offen offset:480
buffer_load_ushort v142,v166,s[24:27],s91 offen offset:736
buffer_load_ushort v143,v166,s[24:27],s91 offen offset:992
s_waitcnt vmcnt(0)
v_lshl_or_b32 v112,v113,16,v112
v_lshl_or_b32 v113,v115,16,v114
v_lshl_or_b32 v114,v117,16,v116
v_lshl_or_b32 v115,v119,16,v118
v_lshl_or_b32 v120,v121,16,v120
v_lshl_or_b32 v121,v123,16,v122
v_lshl_or_b32 v122,v125,16,v124
v_lshl_or_b32 v123,v127,16,v126
v_lshl_or_b32 v128,v129,16,v128
v_lshl_or_b32 v129,v131,16,v130
v_lshl_or_b32 v130,v133,16,v132
v_lshl_or_b32 v131,v135,16,v134
v_lshl_or_b32 v136,v137,16,v136
v_lshl_or_b32 v137,v139,16,v138
v_lshl_or_b32 v138,v141,16,v140
v_lshl_or_b32 v139,v143,16,v142
s_nop 1
v_mfma_f32_16x16x32_bf16 v[80:83],v[112:115],v[144:147],v[80:83]
v_mfma_f32_16x16x32_bf16 v[84:87],v[120:123],v[144:147],v[84:87]
v_mfma_f32_16x16x32_bf16 v[88:91],v[128:131],v[144:147],v[88:91]
v_mfma_f32_16x16x32_bf16 v[92:95],v[136:139],v[144:147],v[92:95]
s_nop 7
v_mfma_f32_16x16x32_bf16 v[156:159],v[28:31],v[144:147],v[156:159]
s_nop 7
s_add_u32 s91,s91,8192
s_sub_u32 s90,s90,1
s_cmp_lg_u32 s90,0
s_cbranch_scc1 L_s2_%=
v_rcp_f32 v170,v156
s_nop 0
v_mul_f32 v64,v170,v64
v_mul_f32 v65,v170,v65
v_mul_f32 v66,v170,v66
v_mul_f32 v67,v170,v67
v_mul_f32 v68,v170,v68
v_mul_f32 v69,v170,v69
v_mul_f32 v70,v170,v70
v_mul_f32 v71,v170,v71
v_mul_f32 v72,v170,v72
v_mul_f32 v73,v170,v73
v_mul_f32 v74,v170,v74
v_mul_f32 v75,v170,v75
v_mul_f32 v76,v170,v76
v_mul_f32 v77,v170,v77
v_mul_f32 v78,v170,v78
v_mul_f32 v79,v170,v79
v_mul_f32 v80,v170,v80
v_mul_f32 v81,v170,v81
v_mul_f32 v82,v170,v82
v_mul_f32 v83,v170,v83
v_mul_f32 v84,v170,v84
v_mul_f32 v85,v170,v85
v_mul_f32 v86,v170,v86
v_mul_f32 v87,v170,v87
v_mul_f32 v88,v170,v88
v_mul_f32 v89,v170,v89
v_mul_f32 v90,v170,v90
v_mul_f32 v91,v170,v91
v_mul_f32 v92,v170,v92
v_mul_f32 v93,v170,v93
v_mul_f32 v94,v170,v94
v_mul_f32 v95,v170,v95
v_cvt_pk_bf16_f32 v96,v64,v65
v_cvt_pk_bf16_f32 v97,v66,v67
v_cvt_pk_bf16_f32 v98,v68,v69
v_cvt_pk_bf16_f32 v99,v70,v71
v_cvt_pk_bf16_f32 v100,v72,v73
v_cvt_pk_bf16_f32 v101,v74,v75
v_cvt_pk_bf16_f32 v102,v76,v77
v_cvt_pk_bf16_f32 v103,v78,v79
v_cvt_pk_bf16_f32 v104,v80,v81
v_cvt_pk_bf16_f32 v105,v82,v83
v_cvt_pk_bf16_f32 v106,v84,v85
v_cvt_pk_bf16_f32 v107,v86,v87
v_cvt_pk_bf16_f32 v108,v88,v89
v_cvt_pk_bf16_f32 v109,v90,v91
v_cvt_pk_bf16_f32 v110,v92,v93
v_cvt_pk_bf16_f32 v111,v94,v95
buffer_store_dwordx4 v[96:99],v169,s[36:39],s55 offen sc1
buffer_store_dwordx4 v[100:103],v169,s[36:39],s55 offen offset:1024 sc1
buffer_store_dwordx4 v[104:107],v169,s[36:39],s55 offen offset:2048 sc1
buffer_store_dwordx4 v[108:111],v169,s[36:39],s55 offen offset:3072 sc1
s_waitcnt vmcnt(0)
s_add_u32 s89,s89,1
s_cmp_lg_u32 s89,4
s_cbranch_scc1 L_sjb_%=
s_mov_b32 s98,s88
v_readlane_b32 s88,v173,0
v_readlane_b32 s89,v173,1
v_readlane_b32 s90,v173,2
v_readlane_b32 s91,v173,3
v_readlane_b32 s92,v173,4
v_readlane_b32 s93,v173,5
v_readlane_b32 s94,v173,6
v_readlane_b32 s95,v173,7
s_nop 4
s_mov_b32 s96,0
buffer_load_dwordx4 v[64:67],v6,s[56:59],s96 offen sc1
buffer_load_dwordx4 v[68:71],v6,s[56:59],s96 offen offset:1024 sc1
buffer_load_dwordx4 v[72:75],v6,s[56:59],s96 offen offset:2048 sc1
buffer_load_dwordx4 v[76:79],v6,s[56:59],s96 offen offset:3072 sc1
s_mov_b32 s96,4096
buffer_load_dwordx4 v[80:83],v6,s[56:59],s96 offen sc1
buffer_load_dwordx4 v[84:87],v6,s[56:59],s96 offen offset:1024 sc1
buffer_load_dwordx4 v[88:91],v6,s[56:59],s96 offen offset:2048 sc1
buffer_load_dwordx4 v[92:95],v6,s[56:59],s96 offen offset:3072 sc1
s_mov_b32 s96,8192
buffer_load_dwordx4 v[96:99],v6,s[56:59],s96 offen sc1
buffer_load_dwordx4 v[100:103],v6,s[56:59],s96 offen offset:1024 sc1
buffer_load_dwordx4 v[104:107],v6,s[56:59],s96 offen offset:2048 sc1
buffer_load_dwordx4 v[108:111],v6,s[56:59],s96 offen offset:3072 sc1
s_mov_b32 s96,12288
buffer_load_dwordx4 v[112:115],v6,s[56:59],s96 offen sc1
buffer_load_dwordx4 v[116:119],v6,s[56:59],s96 offen offset:1024 sc1
buffer_load_dwordx4 v[120:123],v6,s[56:59],s96 offen offset:2048 sc1
buffer_load_dwordx4 v[124:127],v6,s[56:59],s96 offen offset:3072 sc1
s_mov_b32 s96,16384
buffer_load_dwordx4 v[128:131],v6,s[56:59],s96 offen sc1
buffer_load_dwordx4 v[132:135],v6,s[56:59],s96 offen offset:1024 sc1
buffer_load_dwordx4 v[136:139],v6,s[56:59],s96 offen offset:2048 sc1
buffer_load_dwordx4 v[140:143],v6,s[56:59],s96 offen offset:3072 sc1
s_mov_b32 s96,20480
buffer_load_dwordx4 v[144:147],v6,s[56:59],s96 offen sc1
buffer_load_dwordx4 v[148:151],v6,s[56:59],s96 offen offset:1024 sc1
buffer_load_dwordx4 v[152:155],v6,s[56:59],s96 offen offset:2048 sc1
buffer_load_dwordx4 v[156:159],v6,s[56:59],s96 offen offset:3072 sc1
s_mov_b32 s96,24576
buffer_load_dwordx4 v[160:163],v6,s[56:59],s96 offen sc1
buffer_load_dwordx4 v[164:167],v6,s[56:59],s96 offen offset:1024 sc1
)A"
R"A(buffer_load_dwordx4 v[168:171],v6,s[56:59],s96 offen offset:2048 sc1
buffer_load_dwordx4 v[172:175],v6,s[56:59],s96 offen offset:3072 sc1
s_mov_b32 s96,28672
buffer_load_dwordx4 v[176:179],v6,s[56:59],s96 offen sc1
buffer_load_dwordx4 v[180:183],v6,s[56:59],s96 offen offset:1024 sc1
buffer_load_dwordx4 v[184:187],v6,s[56:59],s96 offen offset:2048 sc1
buffer_load_dwordx4 v[188:191],v6,s[56:59],s96 offen offset:3072 sc1
s_waitcnt vmcnt(0)
s_cmp_lg_u32 s98,0
s_cbranch_scc1 L_sr1_%=
buffer_load_dwordx4 v[64:67],v6,s[36:39],s84 offen sc1
buffer_load_dwordx4 v[68:71],v6,s[36:39],s84 offen offset:1024 sc1
buffer_load_dwordx4 v[72:75],v6,s[36:39],s84 offen offset:2048 sc1
buffer_load_dwordx4 v[76:79],v6,s[36:39],s84 offen offset:3072 sc1
buffer_load_dwordx4 v[80:83],v6,s[36:39],s85 offen sc1
buffer_load_dwordx4 v[84:87],v6,s[36:39],s85 offen offset:1024 sc1
buffer_load_dwordx4 v[88:91],v6,s[36:39],s85 offen offset:2048 sc1
buffer_load_dwordx4 v[92:95],v6,s[36:39],s85 offen offset:3072 sc1
buffer_load_dwordx4 v[96:99],v6,s[36:39],s86 offen sc1
buffer_load_dwordx4 v[100:103],v6,s[36:39],s86 offen offset:1024 sc1
buffer_load_dwordx4 v[104:107],v6,s[36:39],s86 offen offset:2048 sc1
buffer_load_dwordx4 v[108:111],v6,s[36:39],s86 offen offset:3072 sc1
buffer_load_dwordx4 v[112:115],v6,s[36:39],s87 offen sc1
buffer_load_dwordx4 v[116:119],v6,s[36:39],s87 offen offset:1024 sc1
buffer_load_dwordx4 v[120:123],v6,s[36:39],s87 offen offset:2048 sc1
buffer_load_dwordx4 v[124:127],v6,s[36:39],s87 offen offset:3072 sc1
s_waitcnt vmcnt(0)
s_nop 7
s_branch L_sret0_%=
L_sr1_%=:
buffer_load_dwordx4 v[132:135],v6,s[36:39],s84 offen sc1
buffer_load_dwordx4 v[140:143],v6,s[36:39],s84 offen offset:1024 sc1
buffer_load_dwordx4 v[148:151],v6,s[36:39],s84 offen offset:2048 sc1
buffer_load_dwordx4 v[156:159],v6,s[36:39],s84 offen offset:3072 sc1
buffer_load_dwordx4 v[164:167],v6,s[36:39],s85 offen sc1
buffer_load_dwordx4 v[172:175],v6,s[36:39],s85 offen offset:1024 sc1
buffer_load_dwordx4 v[180:183],v6,s[36:39],s85 offen offset:2048 sc1
buffer_load_dwordx4 v[188:191],v6,s[36:39],s85 offen offset:3072 sc1
buffer_load_dwordx4 v[196:199],v6,s[36:39],s86 offen sc1
buffer_load_dwordx4 v[204:207],v6,s[36:39],s86 offen offset:1024 sc1
buffer_load_dwordx4 v[212:215],v6,s[36:39],s86 offen offset:2048 sc1
buffer_load_dwordx4 v[220:223],v6,s[36:39],s86 offen offset:3072 sc1
buffer_load_dwordx4 v[228:231],v6,s[36:39],s87 offen sc1
buffer_load_dwordx4 v[236:239],v6,s[36:39],s87 offen offset:1024 sc1
buffer_load_dwordx4 v[244:247],v6,s[36:39],s87 offen offset:2048 sc1
buffer_load_dwordx4 v[252:255],v6,s[36:39],s87 offen offset:3072 sc1
s_waitcnt vmcnt(0)
s_nop 7
s_branch L_sret1_%=
L_end_%=:
)A"
    :: "s"(karg), "s"(blk), "s"(wid)
    : "v0", "v1", "v2", "v3", "v4", "v5", "v6", "v7", "v8", "v9", "v10", "v11", "v12", "v13", "v14", "v15", "v16", "v17", "v18", "v19", "v20", "v21", "v22", "v23", "v24", "v25", "v26", "v27", "v28", "v29", "v30", "v31", "v32", "v33", "v34", "v35", "v36", "v37", "v38", "v39", "v40", "v41", "v42", "v43", "v44", "v45", "v46", "v47", "v48", "v49", "v50", "v51", "v52", "v53", "v54", "v55", "v56", "v57", "v58", "v59", "v60", "v61", "v62", "v63", "v64", "v65", "v66", "v67", "v68", "v69", "v70", "v71", "v72", "v73", "v74", "v75", "v76", "v77", "v78", "v79", "v80", "v81", "v82", "v83", "v84", "v85", "v86", "v87", "v88", "v89", "v90", "v91", "v92", "v93", "v94", "v95", "v96", "v97", "v98", "v99", "v100", "v101", "v102", "v103", "v104", "v105", "v106", "v107", "v108", "v109", "v110", "v111", "v112", "v113", "v114", "v115", "v116", "v117", "v118", "v119", "v120", "v121", "v122", "v123", "v124", "v125", "v126", "v127", "v128", "v129", "v130", "v131", "v132", "v133", "v134", "v135", "v136", "v137", "v138", "v139", "v140", "v141", "v142", "v143", "v144", "v145", "v146", "v147", "v148", "v149", "v150", "v151", "v152", "v153", "v154", "v155", "v156", "v157", "v158", "v159", "v160", "v161", "v162", "v163", "v164", "v165", "v166", "v167", "v168", "v169", "v170", "v171", "v172", "v173", "v174", "v175", "v176", "v177", "v178", "v179", "v180", "v181", "v182", "v183", "v184", "v185", "v186", "v187", "v188", "v189", "v190", "v191", "v192", "v193", "v194", "v195", "v196", "v197", "v198", "v199", "v200", "v201", "v202", "v203", "v204", "v205", "v206", "v207", "v208", "v209", "v210", "v211", "v212", "v213", "v214", "v215", "v216", "v217", "v218", "v219", "v220", "v221", "v222", "v223", "v224", "v225", "v226", "v227", "v228", "v229", "v230", "v231", "v232", "v233", "v234", "v235", "v236", "v237", "v238", "v239", "v240", "v241", "v242", "v243", "v244", "v245", "v246", "v247", "v248", "v249", "v250", "v251", "v252", "v253", "v254", "v255", "a0", "a1", "a2", "a3", "a4", "a5", "a6", "a7", "a8", "a9", "a10", "a11", "a12", "a13", "a14", "a15", "a16", "a17", "a18", "a19", "a20", "a21", "a22", "a23", "a24", "a25", "a26", "a27", "a28", "a29", "a30", "a31", "a32", "a33", "a34", "a35", "a36", "a37", "a38", "a39", "a40", "a41", "a42", "a43", "a44", "a45", "a46", "a47", "a48", "a49", "a50", "a51", "a52", "a53", "a54", "a55", "a56", "a57", "a58", "a59", "a60", "a61", "a62", "a63", "a64", "a65", "a66", "a67", "a68", "a69", "a70", "a71", "a72", "a73", "a74", "a75", "a76", "a77", "a78", "a79", "a80", "a81", "a82", "a83", "a84", "a85", "a86", "a87", "a88", "a89", "a90", "a91", "a92", "a93", "a94", "a95", "a96", "a97", "a98", "a99", "a100", "a101", "a102", "a103", "a104", "a105", "a106", "a107", "a108", "a109", "a110", "a111", "a112", "a113", "a114", "a115", "a116", "a117", "a118", "a119", "a120", "a121", "a122", "a123", "a124", "a125", "a126", "a127", "a128", "a129", "a130", "a131", "a132", "a133", "a134", "a135", "a136", "a137", "a138", "a139", "a140", "a141", "a142", "a143", "a144", "a145", "a146", "a147", "a148", "a149", "a150", "a151", "a152", "a153", "a154", "a155", "a156", "a157", "a158", "a159", "a160", "a161", "a162", "a163", "a164", "a165", "a166", "a167", "a168", "a169", "a170", "a171", "a172", "a173", "a174", "a175", "a176", "a177", "a178", "a179", "a180", "a181", "a182", "a183", "a184", "a185", "a186", "a187", "a188", "a189", "a190", "a191", "a192", "a193", "a194", "a195", "a196", "a197", "a198", "a199", "a200", "a201", "a202", "a203", "a204", "a205", "a206", "a207", "a208", "a209", "a210", "a211", "a212", "a213", "a214", "a215", "a216", "a217", "a218", "a219", "a220", "a221", "a222", "a223", "a224", "a225", "a226", "a227", "a228", "a229", "a230", "a231", "a232", "a233", "a234", "a235", "a236", "a237", "a238", "a239", "a240", "a241", "a242", "a243", "a244", "a245", "a246", "a247", "a248", "a249", "a250", "a251", "a252", "a253", "a254", "a255", "s4", "s5", "s6", "s7", "s8", "s9", "s10", "s11", "s12", "s13", "s14", "s15", "s16", "s17", "s18", "s19", "s20", "s21", "s22", "s23", "s24", "s25", "s26", "s27", "s28", "s29", "s30", "s31", "s32", "s33", "s34", "s35", "s36", "s37", "s38", "s39", "s40", "s41", "s42", "s43", "s44", "s45", "s46", "s47", "s48", "s49", "s50", "s51", "s52", "s53", "s54", "s55", "s56", "s57", "s58", "s59", "s60", "s61", "s62", "s63", "s64", "s65", "s66", "s67", "s68", "s69", "s70", "s71", "s72", "s73", "s74", "s75", "s76", "s77", "s78", "s79", "s80", "s81", "s82", "s83", "s84", "s85", "s86", "s87", "s88", "s89", "s90", "s91", "s92", "s93", "s94", "s95", "s96", "s97", "s98", "s99", "s100", "s101", "vcc", "scc", "memory");
#endif
}

extern "C" void kernel_launch(void* const* d_in, const int* in_sizes, int n_in,
                              void* d_out, int out_size, void* d_ws, size_t ws_size,
                              hipStream_t stream) {
  (void)hipFuncSetAttribute((const void*)qattn_kernel, hipFuncAttributeMaxDynamicSharedMemorySize, LDS_ATT);
  qattn_kernel<<<256, 256, LDS_ATT, stream>>>(d_in[0], d_in[1], d_in[2], d_in[3], d_ws, d_in[4], d_out);
}
```

```cpp
#include <hip/hip_runtime.h>
#include <hip/hip_bf16.h>

constexpr int LDS_ATT = 163840;

__global__ __launch_bounds__(256, 1) void qattn_kernel(const void* __restrict__ x, const void* __restrict__ kc, const void* __restrict__ vc,
                                                       const void* __restrict__ wq, void* __restrict__ ws, const void* __restrict__ wo,
                                                       void* __restrict__ out) {
#if defined(__HIP_DEVICE_COMPILE__)
  const auto karg = __builtin_amdgcn_kernarg_segment_ptr();
  const unsigned blk = blockIdx.x;
  const unsigned wid = __builtin_amdgcn_readfirstlane(threadIdx.x >> 6);
  asm volatile(
R"A(s_memtime s[72:73]
s_memrealtime s[74:75]
s_load_dwordx8 s[4:11],%0,0x0
s_load_dwordx4 s[12:15],%0,0x20
s_load_dwordx2 s[16:17],%0,0x30
s_mov_b32 s40,%2
s_mov_b32 s43,%1
s_lshr_b32 s45,s43,3
v_mbcnt_lo_u32_b32 v12,-1,0
v_mbcnt_hi_u32_b32 v12,-1,v12
s_getpc_b64 s[88:89]
L_pc_%=:
s_and_b32 s88,s88,0xffffff80
s_and_b32 s89,s89,0xffff
s_mov_b32 s90,L_fast_end_%=-L_pc_%=
s_mov_b32 s91,0x20000
s_lshl_b32 s44,s45,2
s_add_u32 s44,s44,s40
s_mul_i32 s44,s44,768
v_lshlrev_b32 v67,7,v12
s_mov_b64 exec,63
buffer_load_dword v19,v67,s[88:91],s44 offen
s_mov_b64 exec,-1
s_and_b32 s70,s43,3
s_and_b32 s71,s45,3
s_lshl_b32 s42,s70,2
s_add_u32 s42,s42,s71
s_and_b32 s41,s43,4
s_lshl_b32 s41,s41,1
s_lshr_b32 s46,s45,2
s_add_u32 s41,s41,s46
s_lshl_b32 s56,s42,9
s_mov_b32 s57,0
s_lshl_b32 s59,s40,13
s_add_u32 s58,s59,65536
v_and_b32 v64,3,v12
v_bfe_u32 v65,v12,2,2
v_lshrrev_b32 v66,4,v12
v_lshrrev_b32 v75,3,v12
s_lshl_b32 s44,s40,6
v_add_u32 v75,s44,v75
v_mul_u32_u24 v75,1280,v75
v_and_b32 v76,7,v12
s_add_u32 s84,s70,1
s_and_b32 s84,s84,3
s_mul_i32 s84,s84,20480
s_add_u32 s84,s84,4096
s_add_u32 s85,s70,2
s_and_b32 s85,s85,3
s_mul_i32 s85,s85,20480
s_add_u32 s85,s85,2048
s_add_u32 s86,s70,3
s_and_b32 s86,s86,3
s_mul_i32 s86,s86,20480
s_add_u32 s86,s86,4096
s_add_u32 s87,s70,4
s_and_b32 s87,s87,3
s_mul_i32 s87,s87,20480
s_add_u32 s87,s87,2048
v_or_b32 v77,0,v66
v_xor_b32 v77,v77,v76
v_lshl_add_u32 v77,v77,4,v75
v_add_u32 v48,s84,v77
v_add_u32 v50,s85,v77
v_add_u32 v52,s86,v77
v_add_u32 v54,s87,v77
v_or_b32 v77,4,v66
v_xor_b32 v77,v77,v76
v_lshl_add_u32 v77,v77,4,v75
v_add_u32 v77,9216,v77
v_add_u32 v49,s84,v77
v_add_u32 v51,s85,v77
v_add_u32 v53,s86,v77
v_add_u32 v55,s87,v77
s_lshl_b32 s44,s40,17
v_lshlrev_b32 v75,13,v65
v_lshl_add_u32 v75,v66,6,v75
v_lshl_add_u32 v75,v64,4,v75
v_add_u32 v75,s44,v75
v_add_u32 v56,0,v75
v_add_u32 v57,256,v75
v_add_u32 v58,32768,v75
v_add_u32 v59,33024,v75
v_add_u32 v60,65536,v75
v_add_u32 v61,65792,v75
v_add_u32 v62,98304,v75
v_add_u32 v63,98560,v75
s_mul_i32 s44,s41,307200
s_waitcnt lgkmcnt(0)
s_mov_b64 s[28:29],s[4:5]
s_add_u32 s28,s28,s44
s_addc_u32 s29,s29,0
s_sub_u32 s28,s28,4096
s_subb_u32 s29,s29,0
s_and_b32 s29,s29,0xffff
s_mov_b32 s30,311296
s_mov_b32 s31,0x20000
s_mov_b64 s[32:33],s[10:11]
s_and_b32 s33,s33,0xffff
s_mov_b32 s34,5242880
s_mov_b32 s35,0x20000
s_add_u32 m0,s58,1024
s_nop 0
buffer_load_dwordx4 v57,s[32:35],s56 offen lds
s_add_u32 m0,s58,3072
s_nop 0
buffer_load_dwordx4 v59,s[32:35],s56 offen lds
s_add_u32 m0,s58,5120
s_nop 0
buffer_load_dwordx4 v61,s[32:35],s56 offen lds
s_add_u32 m0,s58,7168
s_nop 0
buffer_load_dwordx4 v63,s[32:35],s56 offen lds
s_add_u32 m0,s59,0
s_nop 0
buffer_load_dwordx4 v48,s[28:31],s57 offen lds
buffer_load_dwordx4 v49,s[28:31],s57 offen offset:1024 lds
buffer_load_dwordx4 v50,s[28:31],s57 offen offset:2048 lds
buffer_load_dwordx4 v51,s[28:31],s57 offen offset:3072 lds
s_add_u32 m0,s59,4096
s_nop 0
buffer_load_dwordx4 v52,s[28:31],s57 offen lds
buffer_load_dwordx4 v53,s[28:31],s57 offen offset:1024 lds
buffer_load_dwordx4 v54,s[28:31],s57 offen offset:2048 lds
buffer_load_dwordx4 v55,s[28:31],s57 offen offset:3072 lds
s_add_u32 s57,s57,128
s_xor_b32 s59,s59,131072
s_add_u32 m0,s58,0
s_nop 0
buffer_load_dwordx4 v56,s[32:35],s56 offen lds
s_add_u32 m0,s58,2048
s_nop 0
buffer_load_dwordx4 v58,s[32:35],s56 offen lds
s_add_u32 m0,s58,4096
s_nop 0
buffer_load_dwordx4 v60,s[32:35],s56 offen lds
s_add_u32 m0,s58,6144
s_nop 0
buffer_load_dwordx4 v62,s[32:35],s56 offen lds
s_add_u32 s56,s56,524288
s_add_u32 s58,s58,32768
s_cmp_ge_u32 s58,131072
s_cselect_b32 s62,98304,0
s_sub_u32 s58,s58,s62
s_add_u32 m0,s58,0
s_nop 0
buffer_load_dwordx4 v56,s[32:35],s56 offen lds
s_add_u32 m0,s58,1024
s_nop 0
buffer_load_dwordx4 v57,s[32:35],s56 offen lds
s_add_u32 m0,s58,2048
s_nop 0
buffer_load_dwordx4 v58,s[32:35],s56 offen lds
s_add_u32 m0,s58,3072
s_nop 0
buffer_load_dwordx4 v59,s[32:35],s56 offen lds
s_add_u32 m0,s58,4096
s_nop 0
buffer_load_dwordx4 v60,s[32:35],s56 offen lds
s_add_u32 m0,s58,5120
s_nop 0
buffer_load_dwordx4 v61,s[32:35],s56 offen lds
s_add_u32 m0,s58,6144
s_nop 0
buffer_load_dwordx4 v62,s[32:35],s56 offen lds
s_add_u32 m0,s58,7168
s_nop 0
buffer_load_dwordx4 v63,s[32:35],s56 offen lds
s_add_u32 s56,s56,524288
s_add_u32 s58,s58,32768
s_cmp_ge_u32 s58,131072
s_cselect_b32 s62,98304,0
s_sub_u32 s58,s58,s62
s_mov_b32 s60,65536
v_lshlrev_b32 v6,4,v12
v_lshlrev_b32 v72,12,v66
v_lshl_add_u32 v72,v65,6,v72
v_lshl_add_u32 v23,v64,4,v72
v_add_u32 v22,65536,v23
v_and_b32 v74,15,v12
v_lshrrev_b32 v76,1,v74
v_lshlrev_b32 v75,7,v74
s_lshl_b32 s44,s40,13
v_add_u32 v75,s44,v75
v_or_b32 v77,0,v66
v_xor_b32 v77,v77,v76
v_lshl_add_u32 v20,v77,4,v75
v_or_b32 v77,4,v66
v_xor_b32 v77,v77,v76
v_lshl_add_u32 v21,v77,4,v75
s_waitcnt vmcnt(12)
s_nop 0
s_memtime s[64:65]
s_memrealtime s[66:67]
s_bfe_u32 s68,%1,327683
s_waitcnt lgkmcnt(0)
s_sub_u32 s64,s64,s72
s_sub_u32 s66,s66,s74
s_mul_i32 s64,s64,10
s_mul_i32 s66,s66,215
s_cmp_lt_u32 s64,s66
s_cbranch_scc1 L_se1_%=
L_sg1_%=:
s_cmp_eq_u32 s68,0
s_cbranch_scc1 L_se1_%=
s_sleep 4
s_sub_u32 s68,s68,1
s_branch L_sg1_%=
L_se1_%=:
s_barrier
ds_read_b128 a[192:195],v20
ds_read_b64_tr_b16 a[128:129],v22 offset:1024
ds_read_b64_tr_b16 a[130:131],v22 offset:3072
ds_read_b128 a[196:199],v21
ds_read_b64_tr_b16 a[132:133],v22 offset:17408
ds_read_b64_tr_b16 a[134:135],v22 offset:19456
ds_read_b128 a[204:207],v21 offset:2048
ds_read_b128 a[200:203],v20 offset:2048
ds_read_b128 a[208:211],v20 offset:4096
ds_read_b128 a[212:215],v21 offset:4096
ds_read_b128 a[220:223],v21 offset:6144
ds_read_b128 a[216:219],v20 offset:6144
ds_read_b64_tr_b16 a[136:137],v22 offset:1032
ds_read_b64_tr_b16 a[138:139],v22 offset:3080
s_waitcnt lgkmcnt(11)
v_mfma_f32_16x16x32_bf16 v[128:131],a[128:131],a[192:195],0
s_add_u32 m0,s59,0
ds_read_b64_tr_b16 a[140:141],v22 offset:17416
ds_read_b64_tr_b16 a[142:143],v22 offset:19464
s_waitcnt lgkmcnt(10)
v_mfma_f32_16x16x32_bf16 v[128:131],a[132:135],a[196:199],v[128:131]
s_waitcnt lgkmcnt(9)
v_mfma_f32_16x16x32_bf16 v[136:139],a[132:135],a[204:207],0
s_waitcnt lgkmcnt(8)
v_mfma_f32_16x16x32_bf16 v[136:139],a[128:131],a[200:203],v[136:139]
s_waitcnt lgkmcnt(7)
v_mfma_f32_16x16x32_bf16 v[144:147],a[128:131],a[208:211],0
s_waitcnt lgkmcnt(6)
buffer_load_dwordx4 v48,s[28:31],s57 offen lds
v_mfma_f32_16x16x32_bf16 v[144:147],a[132:135],a[212:215],v[144:147]
s_waitcnt lgkmcnt(5)
v_mfma_f32_16x16x32_bf16 v[152:155],a[132:135],a[220:223],0
s_waitcnt lgkmcnt(4)
v_mfma_f32_16x16x32_bf16 v[152:155],a[128:131],a[216:219],v[152:155]
s_waitcnt lgkmcnt(2)
ds_read_b64_tr_b16 a[144:145],v22 offset:1280
ds_read_b64_tr_b16 a[146:147],v22 offset:3328
v_mfma_f32_16x16x32_bf16 v[132:135],a[136:139],a[192:195],0
s_waitcnt lgkmcnt(2)
ds_read_b64_tr_b16 a[148:149],v22 offset:17664
ds_read_b64_tr_b16 a[150:151],v22 offset:19712
buffer_load_dwordx4 v49,s[28:31],s57 offen offset:1024 lds
v_mfma_f32_16x16x32_bf16 v[132:135],a[140:143],a[196:199],v[132:135]
v_mfma_f32_16x16x32_bf16 v[140:143],a[140:143],a[204:207],0
v_mfma_f32_16x16x32_bf16 v[140:143],a[136:139],a[200:203],v[140:143]
v_mfma_f32_16x16x32_bf16 v[148:151],a[136:139],a[208:211],0
buffer_load_dwordx4 v50,s[28:31],s57 offen offset:2048 lds
v_mfma_f32_16x16x32_bf16 v[148:151],a[140:143],a[212:215],v[148:151]
v_mfma_f32_16x16x32_bf16 v[156:159],a[140:143],a[220:223],0
v_mfma_f32_16x16x32_bf16 v[156:159],a[136:139],a[216:219],v[156:159]
s_waitcnt lgkmcnt(2)
ds_read_b64_tr_b16 a[152:153],v22 offset:1288
ds_read_b64_tr_b16 a[154:155],v22 offset:3336
v_mfma_f32_16x16x32_bf16 v[160:163],a[144:147],a[192:195],0
s_waitcnt lgkmcnt(2)
buffer_load_dwordx4 v51,s[28:31],s57 offen offset:3072 lds
ds_read_b64_tr_b16 a[156:157],v22 offset:17672
ds_read_b64_tr_b16 a[158:159],v22 offset:19720
v_mfma_f32_16x16x32_bf16 v[160:163],a[148:151],a[196:199],v[160:163]
s_add_u32 m0,s59,4096
v_mfma_f32_16x16x32_bf16 v[168:171],a[148:151],a[204:207],0
v_mfma_f32_16x16x32_bf16 v[168:171],a[144:147],a[200:203],v[168:171]
v_mfma_f32_16x16x32_bf16 v[176:179],a[144:147],a[208:211],0
buffer_load_dwordx4 v52,s[28:31],s57 offen lds
v_mfma_f32_16x16x32_bf16 v[176:179],a[148:151],a[212:215],v[176:179]
v_mfma_f32_16x16x32_bf16 v[184:187],a[148:151],a[220:223],0
v_mfma_f32_16x16x32_bf16 v[184:187],a[144:147],a[216:219],v[184:187]
s_waitcnt lgkmcnt(2)
v_mfma_f32_16x16x32_bf16 v[164:167],a[152:155],a[192:195],0
s_waitcnt lgkmcnt(0)
buffer_load_dwordx4 v53,s[28:31],s57 offen offset:1024 lds
ds_read_b64_tr_b16 a[160:161],v22 offset:1536
ds_read_b64_tr_b16 a[162:163],v22 offset:3584
v_mfma_f32_16x16x32_bf16 v[164:167],a[156:159],a[196:199],v[164:167]
ds_read_b64_tr_b16 a[164:165],v22 offset:17920
ds_read_b64_tr_b16 a[166:167],v22 offset:19968
v_mfma_f32_16x16x32_bf16 v[172:175],a[156:159],a[204:207],0
v_mfma_f32_16x16x32_bf16 v[172:175],a[152:155],a[200:203],v[172:175]
v_mfma_f32_16x16x32_bf16 v[180:183],a[152:155],a[208:211],0
buffer_load_dwordx4 v54,s[28:31],s57 offen offset:2048 lds
v_mfma_f32_16x16x32_bf16 v[180:183],a[156:159],a[212:215],v[180:183]
v_mfma_f32_16x16x32_bf16 v[188:191],a[156:159],a[220:223],0
s_waitcnt vmcnt(15)
v_mfma_f32_16x16x32_bf16 v[188:191],a[152:155],a[216:219],v[188:191]
s_barrier
buffer_load_dwordx4 v55,s[28:31],s57 offen offset:3072 lds
ds_read_b64_tr_b16 a[168:169],v22 offset:1544
ds_read_b64_tr_b16 a[170:171],v22 offset:3592
s_waitcnt lgkmcnt(4)
v_mfma_f32_16x16x32_bf16 v[192:195],a[160:163],a[192:195],0
s_add_u32 m0,s58,0
ds_read_b64_tr_b16 a[172:173],v22 offset:17928
ds_read_b64_tr_b16 a[174:175],v22 offset:19976
s_waitcnt lgkmcnt(4)
v_mfma_f32_16x16x32_bf16 v[192:195],a[164:167],a[196:199],v[192:195]
v_mfma_f32_16x16x32_bf16 v[200:203],a[164:167],a[204:207],0
s_lshl_b32 s44,s41,13
s_add_u32 s44,s44,104857600
s_mov_b64 s[64:65],s[12:13]
v_mfma_f32_16x16x32_bf16 v[200:203],a[160:163],a[200:203],v[200:203]
s_mov_b32 s66,8192
buffer_load_dwordx4 v56,s[32:35],s56 offen lds
v_mfma_f32_16x16x32_bf16 v[208:211],a[160:163],a[208:211],0
s_add_u32 m0,s58,1024
s_add_u32 s64,s64,s44
s_addc_u32 s65,s65,0
v_mfma_f32_16x16x32_bf16 v[208:211],a[164:167],a[212:215],v[208:211]
s_and_b32 s65,s65,0xffff
s_mov_b32 s67,0x20000
v_mov_b32 v14,4096
v_mfma_f32_16x16x32_bf16 v[216:219],a[164:167],a[220:223],0
s_mov_b64 s[72:73],s[14:15]
s_and_b32 s73,s73,0xffff
ds_read_b64_tr_b16 a[176:177],v22 offset:1792
v_mfma_f32_16x16x32_bf16 v[216:219],a[160:163],a[216:219],v[216:219]
s_mov_b32 s74,5242880
s_mov_b32 s75,0x20000
ds_read_b64_tr_b16 a[178:179],v22 offset:3840
s_waitcnt lgkmcnt(4)
v_mfma_f32_16x16x32_bf16 v[196:199],a[168:171],a[192:195],0
s_waitcnt lgkmcnt(2)
buffer_load_dwordx4 v57,s[32:35],s56 offen lds
ds_read_b64_tr_b16 a[180:181],v22 offset:18176
ds_read_b64_tr_b16 a[182:183],v22 offset:20224
v_mfma_f32_16x16x32_bf16 v[196:199],a[172:175],a[196:199],v[196:199]
s_add_u32 m0,s58,2048
v_lshlrev_b32 v15,3,v6
v_add_u32 v16,128,v15
v_mfma_f32_16x16x32_bf16 v[204:207],a[172:175],a[204:207],0
s_lshl_b32 s83,%1,2
ds_read_b64_tr_b16 a[184:185],v22 offset:1800
v_lshrrev_b32 v16,10,v16
v_mfma_f32_16x16x32_bf16 v[204:207],a[168:171],a[200:203],v[204:207]
s_add_u32 s83,s83,s40
ds_read_b64_tr_b16 a[186:187],v22 offset:3848
v_lshl_add_u32 v15,v16,24,v15
v_mfma_f32_16x16x32_bf16 v[212:215],a[168:171],a[208:211],0
s_mul_i32 s83,s83,5120
buffer_load_dwordx4 v58,s[32:35],s56 offen lds
v_mfma_f32_16x16x32_bf16 v[212:215],a[172:175],a[212:215],v[212:215]
s_add_u32 m0,s58,3072
s_lshl_b32 s43,s41,5
ds_read_b64_tr_b16 a[188:189],v22 offset:18184
v_mfma_f32_16x16x32_bf16 v[220:223],a[172:175],a[220:223],0
s_lshl_b32 s44,s42,1
s_add_u32 s43,s43,s44
ds_read_b64_tr_b16 a[190:191],v22 offset:20232
v_mfma_f32_16x16x32_bf16 v[220:223],a[168:171],a[216:219],v[220:223]
s_lshl_b32 s44,s43,18
s_mov_b64 s[20:21],s[6:7]
ds_read_b64_tr_b16 a[128:129],v22
s_waitcnt lgkmcnt(7)
v_mfma_f32_16x16x32_bf16 v[224:227],a[176:179],a[192:195],0
s_mov_b32 s22,524288
buffer_load_dwordx4 v59,s[32:35],s56 offen lds
s_waitcnt lgkmcnt(5)
v_mfma_f32_16x16x32_bf16 v[224:227],a[180:183],a[196:199],v[224:227]
s_add_u32 m0,s58,4096
s_mov_b32 s23,0x20000
)A"
R"A(ds_read_b64_tr_b16 a[130:131],v22 offset:2048
v_mfma_f32_16x16x32_bf16 v[232:235],a[180:183],a[204:207],0
s_add_u32 s20,s20,s44
s_addc_u32 s21,s21,0
ds_read_b64_tr_b16 a[132:133],v22 offset:16384
v_mfma_f32_16x16x32_bf16 v[232:235],a[176:179],a[200:203],v[232:235]
s_and_b32 s21,s21,0xffff
s_mov_b64 s[24:25],s[8:9]
ds_read_b64_tr_b16 a[134:135],v22 offset:18432
v_mfma_f32_16x16x32_bf16 v[240:243],a[176:179],a[208:211],0
s_mov_b32 s26,524288
buffer_load_dwordx4 v60,s[32:35],s56 offen lds
v_mfma_f32_16x16x32_bf16 v[240:243],a[180:183],a[212:215],v[240:243]
s_add_u32 s24,s24,s44
s_addc_u32 s25,s25,0
ds_read_b64_tr_b16 a[136:137],v22 offset:8
v_mfma_f32_16x16x32_bf16 v[248:251],a[180:183],a[220:223],0
s_add_u32 m0,s58,5120
s_and_b32 s25,s25,0xffff
ds_read_b64_tr_b16 a[138:139],v22 offset:2056
v_mfma_f32_16x16x32_bf16 v[248:251],a[176:179],a[216:219],v[248:251]
s_mov_b32 s27,0x20000
s_lshl_b32 s44,s43,16
ds_read_b64_tr_b16 a[140:141],v22 offset:16392
s_waitcnt lgkmcnt(6)
v_mfma_f32_16x16x32_bf16 v[228:231],a[184:187],a[192:195],0
s_xor_b32 s59,s59,131072
s_mov_b64 s[36:37],s[12:13]
ds_read_b64_tr_b16 a[142:143],v22 offset:18440
v_mfma_f32_16x16x32_bf16 v[228:231],a[188:191],a[196:199],v[228:231]
s_add_u32 s57,s57,128
buffer_load_dwordx4 v61,s[32:35],s56 offen lds
v_mfma_f32_16x16x32_bf16 v[236:239],a[188:191],a[204:207],0
s_add_u32 s36,s36,s44
s_addc_u32 s37,s37,0
ds_read_b64_tr_b16 a[144:145],v22 offset:256
v_mfma_f32_16x16x32_bf16 v[236:239],a[184:187],a[200:203],v[236:239]
s_add_u32 m0,s58,6144
s_and_b32 s37,s37,0xffff
ds_read_b64_tr_b16 a[146:147],v22 offset:2304
v_mfma_f32_16x16x32_bf16 v[244:247],a[184:187],a[208:211],0
s_mov_b32 s38,131072
s_mov_b32 s39,0x20000
ds_read_b64_tr_b16 a[148:149],v22 offset:16640
v_mfma_f32_16x16x32_bf16 v[244:247],a[188:191],a[212:215],v[244:247]
s_lshl_b32 s51,s40,12
s_add_u32 s52,s51,16384
ds_read_b64_tr_b16 a[150:151],v22 offset:18688
v_mfma_f32_16x16x32_bf16 v[252:255],a[188:191],a[220:223],0
s_mul_i32 s47,s41,5
buffer_load_dwordx4 v62,s[32:35],s56 offen lds
v_mfma_f32_16x16x32_bf16 v[252:255],a[184:187],a[216:219],v[252:255]
s_add_u32 s47,s47,s42
s_and_b32 s47,s47,15
ds_read_b64_tr_b16 a[152:153],v22 offset:264
s_waitcnt lgkmcnt(6)
v_mfma_f32_16x16x32_bf16 a[0:3],a[128:131],a[192:195],0
s_add_u32 m0,s58,7168
s_lshl_b32 s47,s47,14
ds_read_b64_tr_b16 a[154:155],v22 offset:2312
v_mfma_f32_16x16x32_bf16 a[0:3],a[132:135],a[196:199],a[0:3]
s_mov_b32 s54,0
s_mov_b32 s50,98304
ds_read_b64_tr_b16 a[156:157],v22 offset:16648
v_mfma_f32_16x16x32_bf16 a[32:35],a[132:135],a[204:207],0
s_mov_b32 s49,0
s_lshl_b32 s55,s40,14
ds_read_b64_tr_b16 a[158:159],v22 offset:18696
v_mfma_f32_16x16x32_bf16 a[32:35],a[128:131],a[200:203],a[32:35]
s_mov_b32 s61,0x3e0293ee
buffer_load_dwordx4 v63,s[32:35],s56 offen lds
v_mfma_f32_16x16x32_bf16 a[64:67],a[128:131],a[208:211],0
s_mov_b32 s63,0x5d800000
s_lshl_b32 s44,s40,2
ds_read_b64_tr_b16 a[160:161],v22 offset:512
v_mfma_f32_16x16x32_bf16 a[64:67],a[132:135],a[212:215],a[64:67]
s_add_u32 m0,s59,0
s_add_u32 s44,s44,s70
ds_read_b64_tr_b16 a[162:163],v22 offset:2560
v_mfma_f32_16x16x32_bf16 a[96:99],a[132:135],a[220:223],0
s_mov_b32 s46,0xff800000
ds_read_b64_tr_b16 a[164:165],v22 offset:16896
v_lshlrev_b32 v70,8,v65
v_mfma_f32_16x16x32_bf16 a[96:99],a[128:131],a[216:219],a[96:99]
s_cmp_eq_u32 s44,14
s_cselect_b32 s18,0x7149f2ca,s46
ds_read_b64_tr_b16 a[166:167],v22 offset:18944
v_mfma_f32_16x16x32_bf16 a[4:7],a[136:139],a[192:195],0
s_add_u32 s60,s60,32768
buffer_load_dwordx4 v48,s[28:31],s57 offen lds
s_waitcnt lgkmcnt(6)
v_mfma_f32_16x16x32_bf16 a[4:7],a[140:143],a[196:199],a[4:7]
s_cmp_eq_u32 s44,13
s_cselect_b32 s19,0x7149f2ca,s46
ds_read_b64_tr_b16 a[168:169],v22 offset:520
v_mfma_f32_16x16x32_bf16 a[36:39],a[140:143],a[204:207],0
s_cmp_eq_u32 s44,12
s_cselect_b32 s100,0x7149f2ca,s46
ds_read_b64_tr_b16 a[170:171],v22 offset:2568
v_mfma_f32_16x16x32_bf16 a[36:39],a[136:139],a[200:203],a[36:39]
s_cmp_eq_u32 s44,15
s_cselect_b32 s101,0x7149f2ca,s46
ds_read_b64_tr_b16 a[172:173],v22 offset:16904
v_mfma_f32_16x16x32_bf16 a[68:71],a[136:139],a[208:211],0
s_add_u32 s58,s58,32768
ds_read_b64_tr_b16 a[174:175],v22 offset:18952
v_lshl_add_u32 v70,v66,6,v70
v_mfma_f32_16x16x32_bf16 a[68:71],a[140:143],a[212:215],a[68:71]
s_mul_i32 s81,s70,1310720
buffer_load_dwordx4 v49,s[28:31],s57 offen offset:1024 lds
v_mfma_f32_16x16x32_bf16 a[100:103],a[140:143],a[220:223],0
s_mul_i32 s44,s71,320
ds_read_b64_tr_b16 a[176:177],v22 offset:768
v_add_u32 v70,s51,v70
v_mfma_f32_16x16x32_bf16 a[100:103],a[136:139],a[216:219],a[100:103]
s_add_u32 s56,s56,524288
ds_read_b64_tr_b16 a[178:179],v22 offset:2816
v_lshl_add_u32 v4,v64,4,v70
v_mfma_f32_16x16x32_bf16 a[8:11],a[144:147],a[192:195],0
s_add_u32 s81,s81,s44
ds_read_b64_tr_b16 a[180:181],v22 offset:17152
v_xor_b32 v71,2,v64
v_mfma_f32_16x16x32_bf16 a[8:11],a[148:151],a[196:199],a[8:11]
s_mul_i32 s82,s40,10240
ds_read_b64_tr_b16 a[182:183],v22 offset:19200
v_lshl_add_u32 v5,v71,4,v70
v_mfma_f32_16x16x32_bf16 a[40:43],a[148:151],a[204:207],0
s_mul_i32 s44,s40,40960
buffer_load_dwordx4 v50,s[28:31],s57 offen offset:2048 lds
v_mfma_f32_16x16x32_bf16 a[40:43],a[144:147],a[200:203],a[40:43]
s_lshr_b32 s92,%1,3
ds_read_b64_tr_b16 a[184:185],v22 offset:776
v_lshlrev_b32 v72,10,v65
v_mfma_f32_16x16x32_bf16 a[72:75],a[144:147],a[208:211],0
s_lshl_b32 s92,s92,2
ds_read_b64_tr_b16 a[186:187],v22 offset:2824
v_lshl_add_u32 v72,v64,6,v72
v_mfma_f32_16x16x32_bf16 a[72:75],a[148:151],a[212:215],a[72:75]
s_add_u32 s92,s92,s40
ds_read_b64_tr_b16 a[188:189],v22 offset:17160
v_and_b32 v73,2,v65
v_mfma_f32_16x16x32_bf16 a[104:107],a[148:151],a[220:223],0
s_lshl_b32 s92,s92,7
ds_read_b64_tr_b16 a[190:191],v22 offset:19208
v_xor_b32 v73,v73,v66
v_mfma_f32_16x16x32_bf16 a[104:107],a[144:147],a[216:219],a[104:107]
s_add_u32 s93,s92,L_tailseg_%=-L_pc_%=
buffer_load_dwordx4 v51,s[28:31],s57 offen offset:3072 lds
v_mfma_f32_16x16x32_bf16 a[12:15],a[152:155],a[192:195],0
s_waitcnt lgkmcnt(5)
v_lshl_add_u32 v72,v73,4,v72
v_add_u32 v1,98304,v72
v_mfma_f32_16x16x32_bf16 a[12:15],a[156:159],a[196:199],a[12:15]
s_add_u32 m0,s59,4096
v_lshlrev_b32 v72,10,v66
v_lshl_add_u32 v72,v65,6,v72
v_mfma_f32_16x16x32_bf16 a[44:47],a[156:159],a[204:207],0
s_xor_b32 s59,s59,131072
v_lshl_add_u32 v72,v64,3,v72
v_add_u32 v72,114688,v72
v_mfma_f32_16x16x32_bf16 a[44:47],a[152:155],a[200:203],a[44:47]
s_cmp_eq_u32 s60,131072
s_cselect_b32 s60,32768,s60
v_and_b32 v73,1,v66
v_mfma_f32_16x16x32_bf16 a[76:79],a[152:155],a[208:211],0
buffer_load_dwordx4 v52,s[28:31],s57 offen lds
v_mfma_f32_16x16x32_bf16 a[76:79],a[156:159],a[212:215],a[76:79]
v_add_u32 v22,s60,v23
v_xor_b32 v20,131072,v20
v_mfma_f32_16x16x32_bf16 a[108:111],a[156:159],a[220:223],0
s_add_u32 s60,s60,32768
v_xor_b32 v21,131072,v21
v_lshl_add_u32 v2,v73,5,v72
v_mfma_f32_16x16x32_bf16 a[108:111],a[152:155],a[216:219],a[108:111]
s_waitcnt vmcnt(13) lgkmcnt(0)
v_xor_b32 v73,1,v73
v_lshl_add_u32 v3,v73,5,v72
v_mfma_f32_16x16x32_bf16 a[16:19],a[160:163],a[192:195],0
s_barrier
buffer_load_dwordx4 v53,s[28:31],s57 offen offset:1024 lds
v_mfma_f32_16x16x32_bf16 a[16:19],a[164:167],a[196:199],a[16:19]
ds_read_b128 a[224:227],v20
v_mov_b32 v73,0x3f803f80
v_mfma_f32_16x16x32_bf16 a[48:51],a[164:167],a[204:207],0
s_cmp_ge_u32 s58,131072
s_cselect_b32 s62,98304,0
ds_read_b64_tr_b16 a[128:129],v22 offset:1024
v_mfma_f32_16x16x32_bf16 a[48:51],a[160:163],a[200:203],a[48:51]
s_sub_u32 s58,s58,s62
ds_read_b64_tr_b16 a[130:131],v22 offset:3072
v_add_u32 v70,0,v12
v_mfma_f32_16x16x32_bf16 a[80:83],a[160:163],a[208:211],0
ds_read_b128 a[228:231],v21
v_mul_u32_u24 v71,3277,v70
v_mfma_f32_16x16x32_bf16 a[80:83],a[164:167],a[212:215],a[80:83]
buffer_load_dwordx4 v54,s[28:31],s57 offen offset:2048 lds
v_mfma_f32_16x16x32_bf16 a[112:115],a[164:167],a[220:223],0
ds_read_b64_tr_b16 a[132:133],v22 offset:17408
v_lshrrev_b32 v71,16,v71
v_mfma_f32_16x16x32_bf16 a[112:115],a[160:163],a[216:219],a[112:115]
ds_read_b64_tr_b16 a[134:135],v22 offset:19456
v_mul_u32_u24 v72,20,v71
v_mfma_f32_16x16x32_bf16 a[20:23],a[168:171],a[192:195],0
ds_read_b128 a[232:235],v20 offset:2048
v_sub_u32 v72,v70,v72
v_mfma_f32_16x16x32_bf16 a[20:23],a[172:175],a[196:199],a[20:23]
ds_read_b128 a[236:239],v21 offset:2048
v_add_u32 v70,64,v12
v_mfma_f32_16x16x32_bf16 a[52:55],a[172:175],a[204:207],0
buffer_load_dwordx4 v55,s[28:31],s57 offen offset:3072 lds
v_mfma_f32_16x16x32_bf16 a[52:55],a[168:171],a[200:203],a[52:55]
s_add_u32 s57,s57,128
ds_read_b128 a[240:243],v20 offset:4096
v_lshlrev_b32 v24,10,v6
v_mfma_f32_16x16x32_bf16 a[84:87],a[168:171],a[208:211],0
s_cmp_eq_u32 s60,131072
s_cselect_b32 s60,32768,s60
ds_read_b128 a[244:247],v21 offset:4096
v_mfma_f32_16x16x32_bf16 a[84:87],a[172:175],a[212:215],a[84:87]
ds_read_b128 a[248:251],v20 offset:6144
v_xor_b32 v20,131072,v20
v_mfma_f32_16x16x32_bf16 a[116:119],a[172:175],a[220:223],0
buffer_load_dword v13,v14,s[64:67],0 offen sc1
v_mfma_f32_16x16x32_bf16 a[116:119],a[168:171],a[216:219],a[116:119]
ds_read_b128 a[252:255],v21 offset:6144
v_xor_b32 v21,131072,v21
v_mfma_f32_16x16x32_bf16 a[24:27],a[176:179],a[192:195],0
ds_read_b64_tr_b16 a[136:137],v22 offset:1032
v_mfma_f32_16x16x32_bf16 a[24:27],a[180:183],a[196:199],a[24:27]
ds_read_b64_tr_b16 a[138:139],v22 offset:3080
v_mfma_f32_16x16x32_bf16 a[56:59],a[180:183],a[204:207],0
ds_read_b64_tr_b16 a[140:141],v22 offset:17416
v_mfma_f32_16x16x32_bf16 a[56:59],a[176:179],a[200:203],a[56:59]
ds_read_b64_tr_b16 a[142:143],v22 offset:19464
v_mfma_f32_16x16x32_bf16 a[88:91],a[176:179],a[208:211],0
ds_read_b64_tr_b16 a[144:145],v22 offset:1280
v_mfma_f32_16x16x32_bf16 a[88:91],a[180:183],a[212:215],a[88:91]
ds_read_b64_tr_b16 a[146:147],v22 offset:3328
v_mfma_f32_16x16x32_bf16 a[120:123],a[180:183],a[220:223],0
ds_read_b64_tr_b16 a[148:149],v22 offset:17664
v_mfma_f32_16x16x32_bf16 a[120:123],a[176:179],a[216:219],a[120:123]
ds_read_b64_tr_b16 a[150:151],v22 offset:19712
v_mfma_f32_16x16x32_bf16 a[28:31],a[184:187],a[192:195],0
ds_read_b64_tr_b16 a[152:153],v22 offset:1288
v_mfma_f32_16x16x32_bf16 a[28:31],a[188:191],a[196:199],a[28:31]
ds_read_b64_tr_b16 a[154:155],v22 offset:3336
ds_read_b64_tr_b16 a[156:157],v22 offset:17672
ds_read_b64_tr_b16 a[158:159],v22 offset:19720
v_mfma_f32_16x16x32_bf16 a[60:63],a[188:191],a[204:207],0
v_cmp_eq_u32 vcc,0,v64
s_nop 1
v_cndmask_b32 v28,0,v73,vcc
v_mfma_f32_16x16x32_bf16 a[60:63],a[184:187],a[200:203],a[60:63]
v_mov_b32 v29,v28
v_mov_b32 v30,v28
v_mfma_f32_16x16x32_bf16 a[92:95],a[184:187],a[208:211],0
v_mov_b32 v31,v28
v_bfe_u32 v73,v71,2,1
v_mfma_f32_16x16x32_bf16 a[92:95],a[188:191],a[212:215],a[92:95]
v_lshlrev_b32 v73,1,v73
v_xor_b32 v72,v72,v73
v_mfma_f32_16x16x32_bf16 a[124:127],a[188:191],a[220:223],0
s_add_u32 m0,s58,0
v_mul_u32_u24 v71,1280,v71
v_lshl_add_u32 v71,v72,4,v71
v_mfma_f32_16x16x32_bf16 a[124:127],a[184:187],a[216:219],a[124:127]
s_waitcnt lgkmcnt(4)
v_add_u32 v71,s44,v71
v_subrev_u32 v7,0,v71
v_mfma_f32_16x16x32_bf16 v[128:131],a[128:131],a[224:227],v[128:131]
ds_read_b64_tr_b16 a[160:161],v22 offset:1536
v_mul_u32_u24 v71,3277,v70
v_mfma_f32_16x16x32_bf16 v[128:131],a[132:135],a[228:231],v[128:131]
buffer_load_dwordx4 v56,s[32:35],s56 offen lds
v_mfma_f32_16x16x32_bf16 v[136:139],a[132:135],a[236:239],v[136:139]
ds_read_b64_tr_b16 a[162:163],v22 offset:3584
v_lshrrev_b32 v71,16,v71
v_mfma_f32_16x16x32_bf16 v[136:139],a[128:131],a[232:235],v[136:139]
s_add_u32 m0,s58,1024
ds_read_b64_tr_b16 a[164:165],v22 offset:17920
v_mul_u32_u24 v72,20,v71
v_mfma_f32_16x16x32_bf16 v[144:147],a[128:131],a[240:243],v[144:147]
ds_read_b64_tr_b16 a[166:167],v22 offset:19968
v_sub_u32 v72,v70,v72
v_mfma_f32_16x16x32_bf16 v[144:147],a[132:135],a[244:247],v[144:147]
v_bfe_u32 v73,v71,2,1
v_lshlrev_b32 v73,1,v73
v_mfma_f32_16x16x32_bf16 v[152:155],a[132:135],a[252:255],v[152:155]
buffer_load_dwordx4 v57,s[32:35],s56 offen lds
v_mfma_f32_16x16x32_bf16 v[152:155],a[128:131],a[248:251],v[152:155]
v_xor_b32 v72,v72,v73
v_mul_u32_u24 v71,1280,v71
v_mfma_f32_16x16x32_bf16 v[132:135],a[136:139],a[224:227],v[132:135]
)A"
R"A(ds_read_b64_tr_b16 a[168:169],v22 offset:1544
v_lshl_add_u32 v71,v72,4,v71
v_mfma_f32_16x16x32_bf16 v[132:135],a[140:143],a[228:231],v[132:135]
s_add_u32 m0,s58,2048
ds_read_b64_tr_b16 a[170:171],v22 offset:3592
v_add_u32 v71,s44,v71
v_mfma_f32_16x16x32_bf16 v[140:143],a[140:143],a[236:239],v[140:143]
ds_read_b64_tr_b16 a[172:173],v22 offset:17928
v_subrev_u32 v8,1024,v71
v_mfma_f32_16x16x32_bf16 v[140:143],a[136:139],a[232:235],v[140:143]
ds_read_b64_tr_b16 a[174:175],v22 offset:19976
v_add_u32 v70,128,v12
v_mfma_f32_16x16x32_bf16 v[148:151],a[136:139],a[240:243],v[148:151]
buffer_load_dwordx4 v58,s[32:35],s56 offen lds
v_mfma_f32_16x16x32_bf16 v[148:151],a[140:143],a[244:247],v[148:151]
v_mul_u32_u24 v71,3277,v70
v_lshrrev_b32 v71,16,v71
v_mfma_f32_16x16x32_bf16 v[156:159],a[140:143],a[252:255],v[156:159]
ds_read_b64_tr_b16 a[176:177],v22 offset:1792
v_mul_u32_u24 v72,20,v71
v_mfma_f32_16x16x32_bf16 v[156:159],a[136:139],a[248:251],v[156:159]
s_add_u32 m0,s58,3072
ds_read_b64_tr_b16 a[178:179],v22 offset:3840
v_sub_u32 v72,v70,v72
v_mfma_f32_16x16x32_bf16 v[160:163],a[144:147],a[224:227],v[160:163]
ds_read_b64_tr_b16 a[180:181],v22 offset:18176
v_bfe_u32 v73,v71,2,1
v_mfma_f32_16x16x32_bf16 v[160:163],a[148:151],a[228:231],v[160:163]
ds_read_b64_tr_b16 a[182:183],v22 offset:20224
v_lshlrev_b32 v73,1,v73
v_mfma_f32_16x16x32_bf16 v[168:171],a[148:151],a[236:239],v[168:171]
buffer_load_dwordx4 v59,s[32:35],s56 offen lds
v_mfma_f32_16x16x32_bf16 v[168:171],a[144:147],a[232:235],v[168:171]
v_xor_b32 v72,v72,v73
v_mul_u32_u24 v71,1280,v71
v_mfma_f32_16x16x32_bf16 v[176:179],a[144:147],a[240:243],v[176:179]
s_add_u32 m0,s58,4096
ds_read_b64_tr_b16 a[184:185],v22 offset:1800
v_lshl_add_u32 v71,v72,4,v71
v_mfma_f32_16x16x32_bf16 v[176:179],a[148:151],a[244:247],v[176:179]
ds_read_b64_tr_b16 a[186:187],v22 offset:3848
v_add_u32 v71,s44,v71
v_mfma_f32_16x16x32_bf16 v[184:187],a[148:151],a[252:255],v[184:187]
ds_read_b64_tr_b16 a[188:189],v22 offset:18184
v_subrev_u32 v9,2048,v71
v_mfma_f32_16x16x32_bf16 v[184:187],a[144:147],a[248:251],v[184:187]
s_waitcnt lgkmcnt(5)
buffer_load_dwordx4 v60,s[32:35],s56 offen lds
v_mfma_f32_16x16x32_bf16 v[164:167],a[152:155],a[224:227],v[164:167]
ds_read_b64_tr_b16 a[190:191],v22 offset:20232
v_add_u32 v70,192,v12
v_mfma_f32_16x16x32_bf16 v[164:167],a[156:159],a[228:231],v[164:167]
v_mul_u32_u24 v71,3277,v70
v_lshrrev_b32 v71,16,v71
v_mfma_f32_16x16x32_bf16 v[172:175],a[156:159],a[236:239],v[172:175]
s_add_u32 m0,s58,5120
v_mul_u32_u24 v72,20,v71
v_sub_u32 v72,v70,v72
v_mfma_f32_16x16x32_bf16 v[172:175],a[152:155],a[232:235],v[172:175]
ds_read_b64_tr_b16 a[128:129],v22
v_bfe_u32 v73,v71,2,1
v_mfma_f32_16x16x32_bf16 v[180:183],a[152:155],a[240:243],v[180:183]
ds_read_b64_tr_b16 a[130:131],v22 offset:2048
v_lshlrev_b32 v73,1,v73
v_mfma_f32_16x16x32_bf16 v[180:183],a[156:159],a[244:247],v[180:183]
buffer_load_dwordx4 v61,s[32:35],s56 offen lds
v_mfma_f32_16x16x32_bf16 v[188:191],a[156:159],a[252:255],v[188:191]
ds_read_b64_tr_b16 a[132:133],v22 offset:16384
v_xor_b32 v72,v72,v73
v_mfma_f32_16x16x32_bf16 v[188:191],a[152:155],a[248:251],v[188:191]
ds_read_b64_tr_b16 a[134:135],v22 offset:18432
v_mul_u32_u24 v71,1280,v71
v_mfma_f32_16x16x32_bf16 v[192:195],a[160:163],a[224:227],v[192:195]
s_add_u32 m0,s58,6144
v_lshl_add_u32 v71,v72,4,v71
v_add_u32 v71,s44,v71
v_mfma_f32_16x16x32_bf16 v[192:195],a[164:167],a[228:231],v[192:195]
ds_read_b64_tr_b16 a[136:137],v22 offset:8
v_subrev_u32 v10,3072,v71
v_mfma_f32_16x16x32_bf16 v[200:203],a[164:167],a[236:239],v[200:203]
ds_read_b64_tr_b16 a[138:139],v22 offset:2056
v_add_u32 v70,256,v12
v_mfma_f32_16x16x32_bf16 v[200:203],a[160:163],a[232:235],v[200:203]
buffer_load_dwordx4 v62,s[32:35],s56 offen lds
v_mfma_f32_16x16x32_bf16 v[208:211],a[160:163],a[240:243],v[208:211]
ds_read_b64_tr_b16 a[140:141],v22 offset:16392
v_mul_u32_u24 v71,3277,v70
v_mfma_f32_16x16x32_bf16 v[208:211],a[164:167],a[244:247],v[208:211]
s_add_u32 m0,s58,7168
ds_read_b64_tr_b16 a[142:143],v22 offset:18440
v_lshrrev_b32 v71,16,v71
v_mfma_f32_16x16x32_bf16 v[216:219],a[164:167],a[252:255],v[216:219]
s_add_u32 s58,s58,32768
v_mul_u32_u24 v72,20,v71
v_sub_u32 v72,v70,v72
v_mfma_f32_16x16x32_bf16 v[216:219],a[160:163],a[248:251],v[216:219]
ds_read_b64_tr_b16 a[144:145],v22 offset:256
v_bfe_u32 v73,v71,2,1
v_mfma_f32_16x16x32_bf16 v[196:199],a[168:171],a[224:227],v[196:199]
buffer_load_dwordx4 v63,s[32:35],s56 offen lds
v_mfma_f32_16x16x32_bf16 v[196:199],a[172:175],a[228:231],v[196:199]
s_add_u32 s56,s56,524288
ds_read_b64_tr_b16 a[146:147],v22 offset:2304
v_lshlrev_b32 v73,1,v73
v_mfma_f32_16x16x32_bf16 v[204:207],a[172:175],a[236:239],v[204:207]
ds_read_b64_tr_b16 a[148:149],v22 offset:16640
v_xor_b32 v72,v72,v73
v_mfma_f32_16x16x32_bf16 v[204:207],a[168:171],a[232:235],v[204:207]
s_add_u32 m0,s59,0
ds_read_b64_tr_b16 a[150:151],v22 offset:18688
v_mul_u32_u24 v71,1280,v71
v_mfma_f32_16x16x32_bf16 v[212:215],a[168:171],a[240:243],v[212:215]
v_lshl_add_u32 v71,v72,4,v71
v_add_u32 v71,s44,v71
v_mfma_f32_16x16x32_bf16 v[212:215],a[172:175],a[244:247],v[212:215]
ds_read_b64_tr_b16 a[152:153],v22 offset:264
v_subrev_u32 v11,0,v71
v_mfma_f32_16x16x32_bf16 v[220:223],a[172:175],a[252:255],v[220:223]
buffer_load_dwordx4 v48,s[28:31],s57 offen lds
v_mfma_f32_16x16x32_bf16 v[220:223],a[168:171],a[248:251],v[220:223]
s_cmp_ge_u32 s58,131072
s_cselect_b32 s62,98304,0
ds_read_b64_tr_b16 a[154:155],v22 offset:2312
v_mfma_f32_16x16x32_bf16 v[224:227],a[176:179],a[224:227],v[224:227]
s_sub_u32 s58,s58,s62
ds_read_b64_tr_b16 a[156:157],v22 offset:16648
s_waitcnt lgkmcnt(6)
v_mfma_f32_16x16x32_bf16 v[224:227],a[180:183],a[228:231],v[224:227]
ds_read_b64_tr_b16 a[158:159],v22 offset:18696
v_mfma_f32_16x16x32_bf16 v[232:235],a[180:183],a[236:239],v[232:235]
ds_read_b64_tr_b16 a[160:161],v22 offset:512
v_mfma_f32_16x16x32_bf16 v[232:235],a[176:179],a[232:235],v[232:235]
ds_read_b64_tr_b16 a[162:163],v22 offset:2560
v_mfma_f32_16x16x32_bf16 v[240:243],a[176:179],a[240:243],v[240:243]
buffer_load_dwordx4 v49,s[28:31],s57 offen offset:1024 lds
v_mfma_f32_16x16x32_bf16 v[240:243],a[180:183],a[244:247],v[240:243]
ds_read_b64_tr_b16 a[164:165],v22 offset:16896
v_mfma_f32_16x16x32_bf16 v[248:251],a[180:183],a[252:255],v[248:251]
ds_read_b64_tr_b16 a[166:167],v22 offset:18944
v_mfma_f32_16x16x32_bf16 v[248:251],a[176:179],a[248:251],v[248:251]
ds_read_b64_tr_b16 a[168:169],v22 offset:520
v_mfma_f32_16x16x32_bf16 v[228:231],a[184:187],a[224:227],v[228:231]
ds_read_b64_tr_b16 a[170:171],v22 offset:2568
v_mfma_f32_16x16x32_bf16 v[228:231],a[188:191],a[228:231],v[228:231]
buffer_load_dwordx4 v50,s[28:31],s57 offen offset:2048 lds
v_mfma_f32_16x16x32_bf16 v[236:239],a[188:191],a[236:239],v[236:239]
ds_read_b64_tr_b16 a[172:173],v22 offset:16904
v_mfma_f32_16x16x32_bf16 v[236:239],a[184:187],a[232:235],v[236:239]
ds_read_b64_tr_b16 a[174:175],v22 offset:18952
v_mfma_f32_16x16x32_bf16 v[244:247],a[184:187],a[240:243],v[244:247]
ds_read_b64_tr_b16 a[176:177],v22 offset:768
v_mfma_f32_16x16x32_bf16 v[244:247],a[188:191],a[244:247],v[244:247]
ds_read_b64_tr_b16 a[178:179],v22 offset:2816
v_mfma_f32_16x16x32_bf16 v[252:255],a[188:191],a[252:255],v[252:255]
ds_read_b64_tr_b16 a[180:181],v22 offset:17152
v_mfma_f32_16x16x32_bf16 v[252:255],a[184:187],a[248:251],v[252:255]
buffer_load_dwordx4 v51,s[28:31],s57 offen offset:3072 lds
v_mfma_f32_16x16x32_bf16 a[0:3],a[128:131],a[224:227],a[0:3]
ds_read_b64_tr_b16 a[182:183],v22 offset:19200
v_mfma_f32_16x16x32_bf16 a[0:3],a[132:135],a[228:231],a[0:3]
ds_read_b64_tr_b16 a[184:185],v22 offset:776
v_mfma_f32_16x16x32_bf16 a[32:35],a[132:135],a[236:239],a[32:35]
s_add_u32 m0,s59,4096
s_xor_b32 s59,s59,131072
ds_read_b64_tr_b16 a[186:187],v22 offset:2824
v_mfma_f32_16x16x32_bf16 a[32:35],a[128:131],a[232:235],a[32:35]
ds_read_b64_tr_b16 a[188:189],v22 offset:17160
v_mfma_f32_16x16x32_bf16 a[64:67],a[128:131],a[240:243],a[64:67]
ds_read_b64_tr_b16 a[190:191],v22 offset:19208
v_add_u32 v22,s60,v23
v_mfma_f32_16x16x32_bf16 a[64:67],a[132:135],a[244:247],a[64:67]
buffer_load_dwordx4 v52,s[28:31],s57 offen lds
v_mfma_f32_16x16x32_bf16 a[96:99],a[132:135],a[252:255],a[96:99]
v_mfma_f32_16x16x32_bf16 a[96:99],a[128:131],a[248:251],a[96:99]
v_mfma_f32_16x16x32_bf16 a[4:7],a[136:139],a[224:227],a[4:7]
v_mfma_f32_16x16x32_bf16 a[4:7],a[140:143],a[228:231],a[4:7]
v_mfma_f32_16x16x32_bf16 a[36:39],a[140:143],a[236:239],a[36:39]
buffer_load_dwordx4 v53,s[28:31],s57 offen offset:1024 lds
v_mfma_f32_16x16x32_bf16 a[36:39],a[136:139],a[232:235],a[36:39]
v_mfma_f32_16x16x32_bf16 a[68:71],a[136:139],a[240:243],a[68:71]
v_mfma_f32_16x16x32_bf16 a[68:71],a[140:143],a[244:247],a[68:71]
v_mfma_f32_16x16x32_bf16 a[100:103],a[140:143],a[252:255],a[100:103]
v_mfma_f32_16x16x32_bf16 a[100:103],a[136:139],a[248:251],a[100:103]
s_waitcnt lgkmcnt(0)
v_mfma_f32_16x16x32_bf16 a[8:11],a[144:147],a[224:227],a[8:11]
buffer_load_dwordx4 v54,s[28:31],s57 offen offset:2048 lds
v_mfma_f32_16x16x32_bf16 a[8:11],a[148:151],a[228:231],a[8:11]
v_mfma_f32_16x16x32_bf16 a[40:43],a[148:151],a[236:239],a[40:43]
v_mfma_f32_16x16x32_bf16 a[40:43],a[144:147],a[232:235],a[40:43]
v_mfma_f32_16x16x32_bf16 a[72:75],a[144:147],a[240:243],a[72:75]
v_mfma_f32_16x16x32_bf16 a[72:75],a[148:151],a[244:247],a[72:75]
v_mfma_f32_16x16x32_bf16 a[104:107],a[148:151],a[252:255],a[104:107]
buffer_load_dwordx4 v55,s[28:31],s57 offen offset:3072 lds
v_mfma_f32_16x16x32_bf16 a[104:107],a[144:147],a[248:251],a[104:107]
s_add_u32 s57,s57,128
v_mfma_f32_16x16x32_bf16 a[12:15],a[152:155],a[224:227],a[12:15]
v_mfma_f32_16x16x32_bf16 a[12:15],a[156:159],a[228:231],a[12:15]
v_mfma_f32_16x16x32_bf16 a[44:47],a[156:159],a[236:239],a[44:47]
v_mfma_f32_16x16x32_bf16 a[44:47],a[152:155],a[232:235],a[44:47]
v_mfma_f32_16x16x32_bf16 a[76:79],a[152:155],a[240:243],a[76:79]
v_mfma_f32_16x16x32_bf16 a[76:79],a[156:159],a[244:247],a[76:79]
v_mfma_f32_16x16x32_bf16 a[108:111],a[156:159],a[252:255],a[108:111]
v_mfma_f32_16x16x32_bf16 a[108:111],a[152:155],a[248:251],a[108:111]
s_waitcnt vmcnt(17)
v_mfma_f32_16x16x32_bf16 a[16:19],a[160:163],a[224:227],a[16:19]
s_barrier
ds_read_b128 a[192:195],v20
v_mfma_f32_16x16x32_bf16 a[16:19],a[164:167],a[228:231],a[16:19]
ds_read_b128 a[196:199],v21
v_mfma_f32_16x16x32_bf16 a[48:51],a[164:167],a[236:239],a[48:51]
ds_read_b128 a[200:203],v20 offset:2048
v_mfma_f32_16x16x32_bf16 a[48:51],a[160:163],a[232:235],a[48:51]
ds_read_b128 a[204:207],v21 offset:2048
v_mfma_f32_16x16x32_bf16 a[80:83],a[160:163],a[240:243],a[80:83]
ds_read_b128 a[208:211],v20 offset:4096
v_mfma_f32_16x16x32_bf16 a[80:83],a[164:167],a[244:247],a[80:83]
ds_read_b128 a[212:215],v21 offset:4096
v_mfma_f32_16x16x32_bf16 a[112:115],a[164:167],a[252:255],a[112:115]
ds_read_b128 a[216:219],v20 offset:6144
v_mfma_f32_16x16x32_bf16 a[112:115],a[160:163],a[248:251],a[112:115]
ds_read_b128 a[220:223],v21 offset:6144
v_mfma_f32_16x16x32_bf16 a[20:23],a[168:171],a[224:227],a[20:23]
ds_read_b64_tr_b16 a[128:129],v22 offset:1024
v_mfma_f32_16x16x32_bf16 a[20:23],a[172:175],a[228:231],a[20:23]
ds_read_b64_tr_b16 a[130:131],v22 offset:3072
v_mfma_f32_16x16x32_bf16 a[52:55],a[172:175],a[236:239],a[52:55]
ds_read_b64_tr_b16 a[132:133],v22 offset:17408
v_mfma_f32_16x16x32_bf16 a[52:55],a[168:171],a[232:235],a[52:55]
ds_read_b64_tr_b16 a[134:135],v22 offset:19456
v_mfma_f32_16x16x32_bf16 a[84:87],a[168:171],a[240:243],a[84:87]
ds_read_b64_tr_b16 a[136:137],v22 offset:1032
v_mfma_f32_16x16x32_bf16 a[84:87],a[172:175],a[244:247],a[84:87]
ds_read_b64_tr_b16 a[138:139],v22 offset:3080
v_mfma_f32_16x16x32_bf16 a[116:119],a[172:175],a[252:255],a[116:119]
ds_read_b64_tr_b16 a[140:141],v22 offset:17416
v_mfma_f32_16x16x32_bf16 a[116:119],a[168:171],a[248:251],a[116:119]
ds_read_b64_tr_b16 a[142:143],v22 offset:19464
v_mfma_f32_16x16x32_bf16 a[24:27],a[176:179],a[224:227],a[24:27]
ds_read_b64_tr_b16 a[144:145],v22 offset:1280
v_mfma_f32_16x16x32_bf16 a[24:27],a[180:183],a[228:231],a[24:27]
ds_read_b64_tr_b16 a[146:147],v22 offset:3328
v_mfma_f32_16x16x32_bf16 a[56:59],a[180:183],a[236:239],a[56:59]
)A"
R"A(ds_read_b64_tr_b16 a[148:149],v22 offset:17664
v_mfma_f32_16x16x32_bf16 a[56:59],a[176:179],a[232:235],a[56:59]
ds_read_b64_tr_b16 a[150:151],v22 offset:19712
v_mfma_f32_16x16x32_bf16 a[88:91],a[176:179],a[240:243],a[88:91]
ds_read_b64_tr_b16 a[152:153],v22 offset:1288
v_mfma_f32_16x16x32_bf16 a[88:91],a[180:183],a[244:247],a[88:91]
ds_read_b64_tr_b16 a[154:155],v22 offset:3336
v_mfma_f32_16x16x32_bf16 a[120:123],a[180:183],a[252:255],a[120:123]
ds_read_b64_tr_b16 a[156:157],v22 offset:17672
v_mfma_f32_16x16x32_bf16 a[120:123],a[176:179],a[248:251],a[120:123]
ds_read_b64_tr_b16 a[158:159],v22 offset:19720
v_mfma_f32_16x16x32_bf16 a[28:31],a[184:187],a[224:227],a[28:31]
v_mfma_f32_16x16x32_bf16 a[28:31],a[188:191],a[228:231],a[28:31]
v_mfma_f32_16x16x32_bf16 a[60:63],a[188:191],a[236:239],a[60:63]
v_mfma_f32_16x16x32_bf16 a[60:63],a[184:187],a[232:235],a[60:63]
v_mfma_f32_16x16x32_bf16 a[92:95],a[184:187],a[240:243],a[92:95]
v_mfma_f32_16x16x32_bf16 a[92:95],a[188:191],a[244:247],a[92:95]
v_mfma_f32_16x16x32_bf16 a[124:127],a[188:191],a[252:255],a[124:127]
v_mfma_f32_16x16x32_bf16 a[124:127],a[184:187],a[248:251],a[124:127]
s_mov_b32 s53,3
L_qp_%=:
s_waitcnt lgkmcnt(1)
v_mfma_f32_16x16x32_bf16 v[128:131],a[128:131],a[192:195],v[128:131]
s_add_u32 m0,s58,0
s_add_u32 s60,s60,32768
ds_read_b64_tr_b16 a[160:161],v22 offset:1536
v_mfma_f32_16x16x32_bf16 v[128:131],a[132:135],a[196:199],v[128:131]
s_mov_b32 s90,L_att0x_%=-L_pc_%=+128
buffer_load_dwordx4 v56,s[32:35],s56 offen lds
v_mfma_f32_16x16x32_bf16 v[136:139],a[132:135],a[204:207],v[136:139]
s_cmp_eq_u32 s60,131072
s_cselect_b32 s60,32768,s60
ds_read_b64_tr_b16 a[162:163],v22 offset:3584
v_mfma_f32_16x16x32_bf16 v[136:139],a[128:131],a[200:203],v[136:139]
s_add_u32 m0,s58,1024
ds_read_b64_tr_b16 a[164:165],v22 offset:17920
v_xor_b32 v20,131072,v20
v_mfma_f32_16x16x32_bf16 v[144:147],a[128:131],a[208:211],v[144:147]
s_sub_u32 s53,s53,1
ds_read_b64_tr_b16 a[166:167],v22 offset:19968
v_xor_b32 v21,131072,v21
v_mfma_f32_16x16x32_bf16 v[144:147],a[132:135],a[212:215],v[144:147]
ds_read_b64_tr_b16 a[168:169],v22 offset:1544
v_mfma_f32_16x16x32_bf16 v[152:155],a[132:135],a[220:223],v[152:155]
buffer_load_dwordx4 v57,s[32:35],s56 offen lds
v_mfma_f32_16x16x32_bf16 v[152:155],a[128:131],a[216:219],v[152:155]
ds_read_b64_tr_b16 a[170:171],v22 offset:3592
v_mfma_f32_16x16x32_bf16 v[132:135],a[136:139],a[192:195],v[132:135]
ds_read_b64_tr_b16 a[172:173],v22 offset:17928
v_mfma_f32_16x16x32_bf16 v[132:135],a[140:143],a[196:199],v[132:135]
s_add_u32 m0,s58,2048
ds_read_b64_tr_b16 a[174:175],v22 offset:19976
v_mfma_f32_16x16x32_bf16 v[140:143],a[140:143],a[204:207],v[140:143]
ds_read_b64_tr_b16 a[176:177],v22 offset:1792
v_mfma_f32_16x16x32_bf16 v[140:143],a[136:139],a[200:203],v[140:143]
ds_read_b64_tr_b16 a[178:179],v22 offset:3840
v_mfma_f32_16x16x32_bf16 v[148:151],a[136:139],a[208:211],v[148:151]
buffer_load_dwordx4 v58,s[32:35],s56 offen lds
v_mfma_f32_16x16x32_bf16 v[148:151],a[140:143],a[212:215],v[148:151]
ds_read_b64_tr_b16 a[180:181],v22 offset:18176
v_mfma_f32_16x16x32_bf16 v[156:159],a[140:143],a[220:223],v[156:159]
s_add_u32 m0,s58,3072
ds_read_b64_tr_b16 a[182:183],v22 offset:20224
v_mfma_f32_16x16x32_bf16 v[156:159],a[136:139],a[216:219],v[156:159]
ds_read_b64_tr_b16 a[184:185],v22 offset:1800
v_mfma_f32_16x16x32_bf16 v[160:163],a[144:147],a[192:195],v[160:163]
ds_read_b64_tr_b16 a[186:187],v22 offset:3848
v_mfma_f32_16x16x32_bf16 v[160:163],a[148:151],a[196:199],v[160:163]
buffer_load_dwordx4 v59,s[32:35],s56 offen lds
v_mfma_f32_16x16x32_bf16 v[168:171],a[148:151],a[204:207],v[168:171]
ds_read_b64_tr_b16 a[188:189],v22 offset:18184
v_mfma_f32_16x16x32_bf16 v[168:171],a[144:147],a[200:203],v[168:171]
ds_read_b64_tr_b16 a[190:191],v22 offset:20232
v_mfma_f32_16x16x32_bf16 v[176:179],a[144:147],a[208:211],v[176:179]
s_add_u32 m0,s58,4096
ds_read_b64_tr_b16 a[128:129],v22
v_mfma_f32_16x16x32_bf16 v[176:179],a[148:151],a[212:215],v[176:179]
ds_read_b64_tr_b16 a[130:131],v22 offset:2048
v_mfma_f32_16x16x32_bf16 v[184:187],a[148:151],a[220:223],v[184:187]
ds_read_b64_tr_b16 a[132:133],v22 offset:16384
v_mfma_f32_16x16x32_bf16 v[184:187],a[144:147],a[216:219],v[184:187]
buffer_load_dwordx4 v60,s[32:35],s56 offen lds
v_mfma_f32_16x16x32_bf16 v[164:167],a[152:155],a[192:195],v[164:167]
s_waitcnt lgkmcnt(5)
ds_read_b64_tr_b16 a[134:135],v22 offset:18432
v_mfma_f32_16x16x32_bf16 v[164:167],a[156:159],a[196:199],v[164:167]
s_add_u32 m0,s58,5120
ds_read_b64_tr_b16 a[136:137],v22 offset:8
v_mfma_f32_16x16x32_bf16 v[172:175],a[156:159],a[204:207],v[172:175]
ds_read_b64_tr_b16 a[138:139],v22 offset:2056
v_mfma_f32_16x16x32_bf16 v[172:175],a[152:155],a[200:203],v[172:175]
ds_read_b64_tr_b16 a[140:141],v22 offset:16392
v_mfma_f32_16x16x32_bf16 v[180:183],a[152:155],a[208:211],v[180:183]
buffer_load_dwordx4 v61,s[32:35],s56 offen lds
v_mfma_f32_16x16x32_bf16 v[180:183],a[156:159],a[212:215],v[180:183]
ds_read_b64_tr_b16 a[142:143],v22 offset:18440
v_mfma_f32_16x16x32_bf16 v[188:191],a[156:159],a[220:223],v[188:191]
ds_read_b64_tr_b16 a[144:145],v22 offset:256
v_mfma_f32_16x16x32_bf16 v[188:191],a[152:155],a[216:219],v[188:191]
s_add_u32 m0,s58,6144
ds_read_b64_tr_b16 a[146:147],v22 offset:2304
v_mfma_f32_16x16x32_bf16 v[192:195],a[160:163],a[192:195],v[192:195]
ds_read_b64_tr_b16 a[148:149],v22 offset:16640
v_mfma_f32_16x16x32_bf16 v[192:195],a[164:167],a[196:199],v[192:195]
ds_read_b64_tr_b16 a[150:151],v22 offset:18688
v_mfma_f32_16x16x32_bf16 v[200:203],a[164:167],a[204:207],v[200:203]
buffer_load_dwordx4 v62,s[32:35],s56 offen lds
v_mfma_f32_16x16x32_bf16 v[200:203],a[160:163],a[200:203],v[200:203]
ds_read_b64_tr_b16 a[152:153],v22 offset:264
v_mfma_f32_16x16x32_bf16 v[208:211],a[160:163],a[208:211],v[208:211]
s_add_u32 m0,s58,7168
s_add_u32 s58,s58,32768
ds_read_b64_tr_b16 a[154:155],v22 offset:2312
v_mfma_f32_16x16x32_bf16 v[208:211],a[164:167],a[212:215],v[208:211]
s_cmp_ge_u32 s58,131072
s_cselect_b32 s62,98304,0
ds_read_b64_tr_b16 a[156:157],v22 offset:16648
v_mfma_f32_16x16x32_bf16 v[216:219],a[164:167],a[220:223],v[216:219]
s_sub_u32 s58,s58,s62
ds_read_b64_tr_b16 a[158:159],v22 offset:18696
v_mfma_f32_16x16x32_bf16 v[216:219],a[160:163],a[216:219],v[216:219]
buffer_load_dwordx4 v63,s[32:35],s56 offen lds
v_mfma_f32_16x16x32_bf16 v[196:199],a[168:171],a[192:195],v[196:199]
s_add_u32 s56,s56,524288
ds_read_b64_tr_b16 a[160:161],v22 offset:512
v_mfma_f32_16x16x32_bf16 v[196:199],a[172:175],a[196:199],v[196:199]
ds_read_b64_tr_b16 a[162:163],v22 offset:2560
v_mfma_f32_16x16x32_bf16 v[204:207],a[172:175],a[204:207],v[204:207]
s_add_u32 m0,s59,0
ds_read_b64_tr_b16 a[164:165],v22 offset:16896
v_mfma_f32_16x16x32_bf16 v[204:207],a[168:171],a[200:203],v[204:207]
ds_read_b64_tr_b16 a[166:167],v22 offset:18944
v_mfma_f32_16x16x32_bf16 v[212:215],a[168:171],a[208:211],v[212:215]
v_mfma_f32_16x16x32_bf16 v[212:215],a[172:175],a[212:215],v[212:215]
buffer_load_dwordx4 v48,s[28:31],s57 offen lds
v_mfma_f32_16x16x32_bf16 v[220:223],a[172:175],a[220:223],v[220:223]
ds_read_b64_tr_b16 a[172:173],v22 offset:16904
v_mfma_f32_16x16x32_bf16 v[220:223],a[168:171],a[216:219],v[220:223]
ds_read_b64_tr_b16 a[168:169],v22 offset:520
v_mfma_f32_16x16x32_bf16 v[224:227],a[176:179],a[192:195],v[224:227]
ds_read_b64_tr_b16 a[170:171],v22 offset:2568
v_mfma_f32_16x16x32_bf16 v[224:227],a[180:183],a[196:199],v[224:227]
ds_read_b64_tr_b16 a[174:175],v22 offset:18952
v_mfma_f32_16x16x32_bf16 v[232:235],a[180:183],a[204:207],v[232:235]
buffer_load_dwordx4 v49,s[28:31],s57 offen offset:1024 lds
v_mfma_f32_16x16x32_bf16 v[232:235],a[176:179],a[200:203],v[232:235]
v_mfma_f32_16x16x32_bf16 v[240:243],a[176:179],a[208:211],v[240:243]
v_mfma_f32_16x16x32_bf16 v[240:243],a[180:183],a[212:215],v[240:243]
v_mfma_f32_16x16x32_bf16 v[248:251],a[180:183],a[220:223],v[248:251]
ds_read_b64_tr_b16 a[180:181],v22 offset:17152
v_mfma_f32_16x16x32_bf16 v[248:251],a[176:179],a[216:219],v[248:251]
ds_read_b64_tr_b16 a[176:177],v22 offset:768
v_mfma_f32_16x16x32_bf16 v[228:231],a[184:187],a[192:195],v[228:231]
s_waitcnt lgkmcnt(3)
buffer_load_dwordx4 v50,s[28:31],s57 offen offset:2048 lds
v_mfma_f32_16x16x32_bf16 v[228:231],a[188:191],a[196:199],v[228:231]
ds_read_b64_tr_b16 a[178:179],v22 offset:2816
v_mfma_f32_16x16x32_bf16 v[236:239],a[188:191],a[204:207],v[236:239]
ds_read_b64_tr_b16 a[182:183],v22 offset:19200
v_mfma_f32_16x16x32_bf16 v[236:239],a[184:187],a[200:203],v[236:239]
v_mfma_f32_16x16x32_bf16 v[244:247],a[184:187],a[208:211],v[244:247]
v_mfma_f32_16x16x32_bf16 v[244:247],a[188:191],a[212:215],v[244:247]
buffer_load_dwordx4 v51,s[28:31],s57 offen offset:3072 lds
v_mfma_f32_16x16x32_bf16 v[252:255],a[188:191],a[220:223],v[252:255]
ds_read_b64_tr_b16 a[188:189],v22 offset:17160
v_mfma_f32_16x16x32_bf16 v[252:255],a[184:187],a[216:219],v[252:255]
ds_read_b64_tr_b16 a[184:185],v22 offset:776
v_mfma_f32_16x16x32_bf16 a[0:3],a[128:131],a[192:195],a[0:3]
s_add_u32 m0,s59,4096
s_xor_b32 s59,s59,131072
ds_read_b64_tr_b16 a[186:187],v22 offset:2824
v_mfma_f32_16x16x32_bf16 a[0:3],a[132:135],a[196:199],a[0:3]
ds_read_b64_tr_b16 a[190:191],v22 offset:19208
v_add_u32 v22,s60,v23
v_mfma_f32_16x16x32_bf16 a[32:35],a[132:135],a[204:207],a[32:35]
s_add_u32 s60,s60,32768
s_cmp_eq_u32 s60,131072
s_cselect_b32 s60,32768,s60
v_mfma_f32_16x16x32_bf16 a[32:35],a[128:131],a[200:203],a[32:35]
buffer_load_dwordx4 v52,s[28:31],s57 offen lds
v_mfma_f32_16x16x32_bf16 a[64:67],a[128:131],a[208:211],a[64:67]
v_mfma_f32_16x16x32_bf16 a[64:67],a[132:135],a[212:215],a[64:67]
v_mfma_f32_16x16x32_bf16 a[96:99],a[132:135],a[220:223],a[96:99]
v_mfma_f32_16x16x32_bf16 a[96:99],a[128:131],a[216:219],a[96:99]
v_mfma_f32_16x16x32_bf16 a[4:7],a[136:139],a[192:195],a[4:7]
buffer_load_dwordx4 v53,s[28:31],s57 offen offset:1024 lds
v_mfma_f32_16x16x32_bf16 a[4:7],a[140:143],a[196:199],a[4:7]
v_mfma_f32_16x16x32_bf16 a[36:39],a[140:143],a[204:207],a[36:39]
v_mfma_f32_16x16x32_bf16 a[36:39],a[136:139],a[200:203],a[36:39]
v_mfma_f32_16x16x32_bf16 a[68:71],a[136:139],a[208:211],a[68:71]
v_mfma_f32_16x16x32_bf16 a[68:71],a[140:143],a[212:215],a[68:71]
v_mfma_f32_16x16x32_bf16 a[100:103],a[140:143],a[220:223],a[100:103]
buffer_load_dwordx4 v54,s[28:31],s57 offen offset:2048 lds
v_mfma_f32_16x16x32_bf16 a[100:103],a[136:139],a[216:219],a[100:103]
v_mfma_f32_16x16x32_bf16 a[8:11],a[144:147],a[192:195],a[8:11]
v_mfma_f32_16x16x32_bf16 a[8:11],a[148:151],a[196:199],a[8:11]
v_mfma_f32_16x16x32_bf16 a[40:43],a[148:151],a[204:207],a[40:43]
v_mfma_f32_16x16x32_bf16 a[40:43],a[144:147],a[200:203],a[40:43]
buffer_load_dwordx4 v55,s[28:31],s57 offen offset:3072 lds
v_mfma_f32_16x16x32_bf16 a[72:75],a[144:147],a[208:211],a[72:75]
s_add_u32 s57,s57,128
v_mfma_f32_16x16x32_bf16 a[72:75],a[148:151],a[212:215],a[72:75]
v_mfma_f32_16x16x32_bf16 a[104:107],a[148:151],a[220:223],a[104:107]
v_mfma_f32_16x16x32_bf16 a[104:107],a[144:147],a[216:219],a[104:107]
v_mfma_f32_16x16x32_bf16 a[12:15],a[152:155],a[192:195],a[12:15]
v_mfma_f32_16x16x32_bf16 a[12:15],a[156:159],a[196:199],a[12:15]
v_mfma_f32_16x16x32_bf16 a[44:47],a[156:159],a[204:207],a[44:47]
buffer_load_dword v16,v15,s[72:75],s83 offen
v_mfma_f32_16x16x32_bf16 a[44:47],a[152:155],a[200:203],a[44:47]
s_add_u32 s83,s83,896
v_mfma_f32_16x16x32_bf16 a[76:79],a[152:155],a[208:211],a[76:79]
v_mfma_f32_16x16x32_bf16 a[76:79],a[156:159],a[212:215],a[76:79]
v_mfma_f32_16x16x32_bf16 a[108:111],a[156:159],a[220:223],a[108:111]
v_mfma_f32_16x16x32_bf16 a[108:111],a[152:155],a[216:219],a[108:111]
s_waitcnt vmcnt(17) lgkmcnt(0)
v_mfma_f32_16x16x32_bf16 a[16:19],a[160:163],a[192:195],a[16:19]
s_barrier
ds_read_b128 a[224:227],v20
v_mfma_f32_16x16x32_bf16 a[16:19],a[164:167],a[196:199],a[16:19]
ds_read_b64_tr_b16 a[128:129],v22 offset:1024
v_mfma_f32_16x16x32_bf16 a[48:51],a[164:167],a[204:207],a[48:51]
ds_read_b64_tr_b16 a[130:131],v22 offset:3072
v_mfma_f32_16x16x32_bf16 a[48:51],a[160:163],a[200:203],a[48:51]
ds_read_b128 a[228:231],v21
v_mfma_f32_16x16x32_bf16 a[80:83],a[160:163],a[208:211],a[80:83]
)A"
R"A(ds_read_b64_tr_b16 a[132:133],v22 offset:17408
v_mfma_f32_16x16x32_bf16 a[80:83],a[164:167],a[212:215],a[80:83]
ds_read_b64_tr_b16 a[134:135],v22 offset:19456
v_mfma_f32_16x16x32_bf16 a[112:115],a[164:167],a[220:223],a[112:115]
ds_read_b128 a[232:235],v20 offset:2048
v_mfma_f32_16x16x32_bf16 a[112:115],a[160:163],a[216:219],a[112:115]
ds_read_b128 a[236:239],v21 offset:2048
v_mfma_f32_16x16x32_bf16 a[20:23],a[168:171],a[192:195],a[20:23]
ds_read_b128 a[240:243],v20 offset:4096
v_mfma_f32_16x16x32_bf16 a[20:23],a[172:175],a[196:199],a[20:23]
ds_read_b128 a[244:247],v21 offset:4096
v_mfma_f32_16x16x32_bf16 a[52:55],a[172:175],a[204:207],a[52:55]
ds_read_b128 a[248:251],v20 offset:6144
v_xor_b32 v20,131072,v20
v_mfma_f32_16x16x32_bf16 a[52:55],a[168:171],a[200:203],a[52:55]
ds_read_b128 a[252:255],v21 offset:6144
v_xor_b32 v21,131072,v21
v_mfma_f32_16x16x32_bf16 a[84:87],a[168:171],a[208:211],a[84:87]
ds_read_b64_tr_b16 a[136:137],v22 offset:1032
v_mfma_f32_16x16x32_bf16 a[84:87],a[172:175],a[212:215],a[84:87]
ds_read_b64_tr_b16 a[138:139],v22 offset:3080
v_mfma_f32_16x16x32_bf16 a[116:119],a[172:175],a[220:223],a[116:119]
ds_read_b64_tr_b16 a[140:141],v22 offset:17416
v_mfma_f32_16x16x32_bf16 a[116:119],a[168:171],a[216:219],a[116:119]
ds_read_b64_tr_b16 a[142:143],v22 offset:19464
v_mfma_f32_16x16x32_bf16 a[24:27],a[176:179],a[192:195],a[24:27]
ds_read_b64_tr_b16 a[144:145],v22 offset:1280
v_mfma_f32_16x16x32_bf16 a[24:27],a[180:183],a[196:199],a[24:27]
ds_read_b64_tr_b16 a[146:147],v22 offset:3328
v_mfma_f32_16x16x32_bf16 a[56:59],a[180:183],a[204:207],a[56:59]
ds_read_b64_tr_b16 a[148:149],v22 offset:17664
v_mfma_f32_16x16x32_bf16 a[56:59],a[176:179],a[200:203],a[56:59]
ds_read_b64_tr_b16 a[150:151],v22 offset:19712
v_mfma_f32_16x16x32_bf16 a[88:91],a[176:179],a[208:211],a[88:91]
ds_read_b64_tr_b16 a[152:153],v22 offset:1288
v_mfma_f32_16x16x32_bf16 a[88:91],a[180:183],a[212:215],a[88:91]
ds_read_b64_tr_b16 a[154:155],v22 offset:3336
v_mfma_f32_16x16x32_bf16 a[120:123],a[180:183],a[220:223],a[120:123]
ds_read_b64_tr_b16 a[156:157],v22 offset:17672
v_mfma_f32_16x16x32_bf16 a[120:123],a[176:179],a[216:219],a[120:123]
ds_read_b64_tr_b16 a[158:159],v22 offset:19720
v_mfma_f32_16x16x32_bf16 a[28:31],a[184:187],a[192:195],a[28:31]
ds_read_b64_tr_b16 a[160:161],v22 offset:1536
v_mfma_f32_16x16x32_bf16 a[28:31],a[188:191],a[196:199],a[28:31]
ds_read_b64_tr_b16 a[162:163],v22 offset:3584
v_mfma_f32_16x16x32_bf16 a[60:63],a[188:191],a[204:207],a[60:63]
ds_read_b64_tr_b16 a[164:165],v22 offset:17920
v_mfma_f32_16x16x32_bf16 a[60:63],a[184:187],a[200:203],a[60:63]
ds_read_b64_tr_b16 a[166:167],v22 offset:19968
v_mfma_f32_16x16x32_bf16 a[92:95],a[184:187],a[208:211],a[92:95]
ds_read_b64_tr_b16 a[168:169],v22 offset:1544
v_mfma_f32_16x16x32_bf16 a[92:95],a[188:191],a[212:215],a[92:95]
ds_read_b64_tr_b16 a[170:171],v22 offset:3592
v_mfma_f32_16x16x32_bf16 a[124:127],a[188:191],a[220:223],a[124:127]
s_add_u32 m0,s58,0
ds_read_b64_tr_b16 a[172:173],v22 offset:17928
v_mfma_f32_16x16x32_bf16 a[124:127],a[184:187],a[216:219],a[124:127]
s_waitcnt lgkmcnt(7)
ds_read_b64_tr_b16 a[174:175],v22 offset:19976
v_mfma_f32_16x16x32_bf16 v[128:131],a[128:131],a[224:227],v[128:131]
ds_read_b64_tr_b16 a[176:177],v22 offset:1792
v_mfma_f32_16x16x32_bf16 v[128:131],a[132:135],a[228:231],v[128:131]
buffer_load_dwordx4 v56,s[32:35],s56 offen lds
v_mfma_f32_16x16x32_bf16 v[136:139],a[132:135],a[236:239],v[136:139]
ds_read_b64_tr_b16 a[178:179],v22 offset:3840
v_mfma_f32_16x16x32_bf16 v[136:139],a[128:131],a[232:235],v[136:139]
s_add_u32 m0,s58,1024
ds_read_b64_tr_b16 a[180:181],v22 offset:18176
v_mfma_f32_16x16x32_bf16 v[144:147],a[128:131],a[240:243],v[144:147]
ds_read_b64_tr_b16 a[182:183],v22 offset:20224
v_mfma_f32_16x16x32_bf16 v[144:147],a[132:135],a[244:247],v[144:147]
ds_read_b64_tr_b16 a[184:185],v22 offset:1800
v_mfma_f32_16x16x32_bf16 v[152:155],a[132:135],a[252:255],v[152:155]
buffer_load_dwordx4 v57,s[32:35],s56 offen lds
v_mfma_f32_16x16x32_bf16 v[152:155],a[128:131],a[248:251],v[152:155]
ds_read_b64_tr_b16 a[186:187],v22 offset:3848
v_mfma_f32_16x16x32_bf16 v[132:135],a[136:139],a[224:227],v[132:135]
ds_read_b64_tr_b16 a[188:189],v22 offset:18184
v_mfma_f32_16x16x32_bf16 v[132:135],a[140:143],a[228:231],v[132:135]
s_add_u32 m0,s58,2048
ds_read_b64_tr_b16 a[190:191],v22 offset:20232
v_mfma_f32_16x16x32_bf16 v[140:143],a[140:143],a[236:239],v[140:143]
ds_read_b64_tr_b16 a[128:129],v22
v_mfma_f32_16x16x32_bf16 v[140:143],a[136:139],a[232:235],v[140:143]
ds_read_b64_tr_b16 a[130:131],v22 offset:2048
v_mfma_f32_16x16x32_bf16 v[148:151],a[136:139],a[240:243],v[148:151]
buffer_load_dwordx4 v58,s[32:35],s56 offen lds
v_mfma_f32_16x16x32_bf16 v[148:151],a[140:143],a[244:247],v[148:151]
ds_read_b64_tr_b16 a[132:133],v22 offset:16384
v_mfma_f32_16x16x32_bf16 v[156:159],a[140:143],a[252:255],v[156:159]
s_add_u32 m0,s58,3072
ds_read_b64_tr_b16 a[134:135],v22 offset:18432
v_mfma_f32_16x16x32_bf16 v[156:159],a[136:139],a[248:251],v[156:159]
ds_read_b64_tr_b16 a[136:137],v22 offset:8
v_mfma_f32_16x16x32_bf16 v[160:163],a[144:147],a[224:227],v[160:163]
ds_read_b64_tr_b16 a[138:139],v22 offset:2056
v_mfma_f32_16x16x32_bf16 v[160:163],a[148:151],a[228:231],v[160:163]
buffer_load_dwordx4 v59,s[32:35],s56 offen lds
v_mfma_f32_16x16x32_bf16 v[168:171],a[148:151],a[236:239],v[168:171]
ds_read_b64_tr_b16 a[140:141],v22 offset:16392
v_mfma_f32_16x16x32_bf16 v[168:171],a[144:147],a[232:235],v[168:171]
ds_read_b64_tr_b16 a[142:143],v22 offset:18440
v_mfma_f32_16x16x32_bf16 v[176:179],a[144:147],a[240:243],v[176:179]
s_add_u32 m0,s58,4096
v_mfma_f32_16x16x32_bf16 v[176:179],a[148:151],a[244:247],v[176:179]
v_mfma_f32_16x16x32_bf16 v[184:187],a[148:151],a[252:255],v[184:187]
ds_read_b64_tr_b16 a[148:149],v22 offset:16640
v_mfma_f32_16x16x32_bf16 v[184:187],a[144:147],a[248:251],v[184:187]
buffer_load_dwordx4 v60,s[32:35],s56 offen lds
v_mfma_f32_16x16x32_bf16 v[164:167],a[152:155],a[224:227],v[164:167]
ds_read_b64_tr_b16 a[144:145],v22 offset:256
v_mfma_f32_16x16x32_bf16 v[164:167],a[156:159],a[228:231],v[164:167]
s_add_u32 m0,s58,5120
ds_read_b64_tr_b16 a[146:147],v22 offset:2304
v_mfma_f32_16x16x32_bf16 v[172:175],a[156:159],a[236:239],v[172:175]
ds_read_b64_tr_b16 a[150:151],v22 offset:18688
v_mfma_f32_16x16x32_bf16 v[172:175],a[152:155],a[232:235],v[172:175]
v_mfma_f32_16x16x32_bf16 v[180:183],a[152:155],a[240:243],v[180:183]
buffer_load_dwordx4 v61,s[32:35],s56 offen lds
v_mfma_f32_16x16x32_bf16 v[180:183],a[156:159],a[244:247],v[180:183]
v_mfma_f32_16x16x32_bf16 v[188:191],a[156:159],a[252:255],v[188:191]
ds_read_b64_tr_b16 a[156:157],v22 offset:16648
v_mfma_f32_16x16x32_bf16 v[188:191],a[152:155],a[248:251],v[188:191]
s_add_u32 m0,s58,6144
ds_read_b64_tr_b16 a[152:153],v22 offset:264
s_waitcnt lgkmcnt(5)
v_mfma_f32_16x16x32_bf16 v[192:195],a[160:163],a[224:227],v[192:195]
ds_read_b64_tr_b16 a[154:155],v22 offset:2312
v_mfma_f32_16x16x32_bf16 v[192:195],a[164:167],a[228:231],v[192:195]
ds_read_b64_tr_b16 a[158:159],v22 offset:18696
v_mfma_f32_16x16x32_bf16 v[200:203],a[164:167],a[236:239],v[200:203]
buffer_load_dwordx4 v62,s[32:35],s56 offen lds
v_mfma_f32_16x16x32_bf16 v[200:203],a[160:163],a[232:235],v[200:203]
v_mfma_f32_16x16x32_bf16 v[208:211],a[160:163],a[240:243],v[208:211]
s_add_u32 m0,s58,7168
s_add_u32 s58,s58,32768
v_mfma_f32_16x16x32_bf16 v[208:211],a[164:167],a[244:247],v[208:211]
s_cmp_ge_u32 s58,131072
s_cselect_b32 s62,98304,0
s_sub_u32 s58,s58,s62
v_mfma_f32_16x16x32_bf16 v[216:219],a[164:167],a[252:255],v[216:219]
ds_read_b64_tr_b16 a[164:165],v22 offset:16896
v_mfma_f32_16x16x32_bf16 v[216:219],a[160:163],a[248:251],v[216:219]
buffer_load_dwordx4 v63,s[32:35],s56 offen lds
v_mfma_f32_16x16x32_bf16 v[196:199],a[168:171],a[224:227],v[196:199]
s_add_u32 s56,s56,524288
ds_read_b64_tr_b16 a[160:161],v22 offset:512
v_mfma_f32_16x16x32_bf16 v[196:199],a[172:175],a[228:231],v[196:199]
ds_read_b64_tr_b16 a[162:163],v22 offset:2560
v_mfma_f32_16x16x32_bf16 v[204:207],a[172:175],a[236:239],v[204:207]
s_add_u32 m0,s59,0
ds_read_b64_tr_b16 a[166:167],v22 offset:18944
v_mfma_f32_16x16x32_bf16 v[204:207],a[168:171],a[232:235],v[204:207]
v_mfma_f32_16x16x32_bf16 v[212:215],a[168:171],a[240:243],v[212:215]
v_mfma_f32_16x16x32_bf16 v[212:215],a[172:175],a[244:247],v[212:215]
buffer_load_dwordx4 v48,s[28:31],s57 offen lds
v_mfma_f32_16x16x32_bf16 v[220:223],a[172:175],a[252:255],v[220:223]
ds_read_b64_tr_b16 a[172:173],v22 offset:16904
v_mfma_f32_16x16x32_bf16 v[220:223],a[168:171],a[248:251],v[220:223]
ds_read_b64_tr_b16 a[168:169],v22 offset:520
v_mfma_f32_16x16x32_bf16 v[224:227],a[176:179],a[224:227],v[224:227]
ds_read_b64_tr_b16 a[170:171],v22 offset:2568
v_mfma_f32_16x16x32_bf16 v[224:227],a[180:183],a[228:231],v[224:227]
ds_read_b64_tr_b16 a[174:175],v22 offset:18952
v_mfma_f32_16x16x32_bf16 v[232:235],a[180:183],a[236:239],v[232:235]
buffer_load_dwordx4 v49,s[28:31],s57 offen offset:1024 lds
v_mfma_f32_16x16x32_bf16 v[232:235],a[176:179],a[232:235],v[232:235]
v_mfma_f32_16x16x32_bf16 v[240:243],a[176:179],a[240:243],v[240:243]
v_mfma_f32_16x16x32_bf16 v[240:243],a[180:183],a[244:247],v[240:243]
v_mfma_f32_16x16x32_bf16 v[248:251],a[180:183],a[252:255],v[248:251]
ds_read_b64_tr_b16 a[180:181],v22 offset:17152
v_mfma_f32_16x16x32_bf16 v[248:251],a[176:179],a[248:251],v[248:251]
ds_read_b64_tr_b16 a[176:177],v22 offset:768
v_mfma_f32_16x16x32_bf16 v[228:231],a[184:187],a[224:227],v[228:231]
buffer_load_dwordx4 v50,s[28:31],s57 offen offset:2048 lds
v_mfma_f32_16x16x32_bf16 v[228:231],a[188:191],a[228:231],v[228:231]
ds_read_b64_tr_b16 a[178:179],v22 offset:2816
v_mfma_f32_16x16x32_bf16 v[236:239],a[188:191],a[236:239],v[236:239]
ds_read_b64_tr_b16 a[182:183],v22 offset:19200
v_mfma_f32_16x16x32_bf16 v[236:239],a[184:187],a[232:235],v[236:239]
v_mfma_f32_16x16x32_bf16 v[244:247],a[184:187],a[240:243],v[244:247]
v_mfma_f32_16x16x32_bf16 v[244:247],a[188:191],a[244:247],v[244:247]
buffer_load_dwordx4 v51,s[28:31],s57 offen offset:3072 lds
v_mfma_f32_16x16x32_bf16 v[252:255],a[188:191],a[252:255],v[252:255]
ds_read_b64_tr_b16 a[188:189],v22 offset:17160
v_mfma_f32_16x16x32_bf16 v[252:255],a[184:187],a[248:251],v[252:255]
ds_read_b64_tr_b16 a[184:185],v22 offset:776
v_mfma_f32_16x16x32_bf16 a[0:3],a[128:131],a[224:227],a[0:3]
s_add_u32 m0,s59,4096
s_xor_b32 s59,s59,131072
ds_read_b64_tr_b16 a[186:187],v22 offset:2824
v_mfma_f32_16x16x32_bf16 a[0:3],a[132:135],a[228:231],a[0:3]
ds_read_b64_tr_b16 a[190:191],v22 offset:19208
v_add_u32 v22,s60,v23
v_mfma_f32_16x16x32_bf16 a[32:35],a[132:135],a[236:239],a[32:35]
v_mfma_f32_16x16x32_bf16 a[32:35],a[128:131],a[232:235],a[32:35]
buffer_load_dwordx4 v52,s[28:31],s57 offen lds
v_mfma_f32_16x16x32_bf16 a[64:67],a[128:131],a[240:243],a[64:67]
v_mfma_f32_16x16x32_bf16 a[64:67],a[132:135],a[244:247],a[64:67]
v_mfma_f32_16x16x32_bf16 a[96:99],a[132:135],a[252:255],a[96:99]
v_mfma_f32_16x16x32_bf16 a[96:99],a[128:131],a[248:251],a[96:99]
v_mfma_f32_16x16x32_bf16 a[4:7],a[136:139],a[224:227],a[4:7]
buffer_load_dwordx4 v53,s[28:31],s57 offen offset:1024 lds
v_mfma_f32_16x16x32_bf16 a[4:7],a[140:143],a[228:231],a[4:7]
v_mfma_f32_16x16x32_bf16 a[36:39],a[140:143],a[236:239],a[36:39]
v_mfma_f32_16x16x32_bf16 a[36:39],a[136:139],a[232:235],a[36:39]
v_mfma_f32_16x16x32_bf16 a[68:71],a[136:139],a[240:243],a[68:71]
v_mfma_f32_16x16x32_bf16 a[68:71],a[140:143],a[244:247],a[68:71]
v_mfma_f32_16x16x32_bf16 a[100:103],a[140:143],a[252:255],a[100:103]
buffer_load_dwordx4 v54,s[28:31],s57 offen offset:2048 lds
v_mfma_f32_16x16x32_bf16 a[100:103],a[136:139],a[248:251],a[100:103]
s_waitcnt lgkmcnt(0)
v_mfma_f32_16x16x32_bf16 a[8:11],a[144:147],a[224:227],a[8:11]
v_mfma_f32_16x16x32_bf16 a[8:11],a[148:151],a[228:231],a[8:11]
v_mfma_f32_16x16x32_bf16 a[40:43],a[148:151],a[236:239],a[40:43]
v_mfma_f32_16x16x32_bf16 a[40:43],a[144:147],a[232:235],a[40:43]
buffer_load_dwordx4 v55,s[28:31],s57 offen offset:3072 lds
v_mfma_f32_16x16x32_bf16 a[72:75],a[144:147],a[240:243],a[72:75]
s_add_u32 s57,s57,128
v_mfma_f32_16x16x32_bf16 a[72:75],a[148:151],a[244:247],a[72:75]
)A"
R"A(v_mfma_f32_16x16x32_bf16 a[104:107],a[148:151],a[252:255],a[104:107]
v_mfma_f32_16x16x32_bf16 a[104:107],a[144:147],a[248:251],a[104:107]
v_mfma_f32_16x16x32_bf16 a[12:15],a[152:155],a[224:227],a[12:15]
v_mfma_f32_16x16x32_bf16 a[12:15],a[156:159],a[228:231],a[12:15]
v_mfma_f32_16x16x32_bf16 a[44:47],a[156:159],a[236:239],a[44:47]
buffer_load_dword v16,v15,s[72:75],s83 offen
v_mfma_f32_16x16x32_bf16 a[44:47],a[152:155],a[232:235],a[44:47]
s_add_u32 s83,s83,896
v_mfma_f32_16x16x32_bf16 a[76:79],a[152:155],a[240:243],a[76:79]
v_mfma_f32_16x16x32_bf16 a[76:79],a[156:159],a[244:247],a[76:79]
v_mfma_f32_16x16x32_bf16 a[108:111],a[156:159],a[252:255],a[108:111]
v_mfma_f32_16x16x32_bf16 a[108:111],a[152:155],a[248:251],a[108:111]
s_waitcnt vmcnt(17) lgkmcnt(0)
v_mfma_f32_16x16x32_bf16 a[16:19],a[160:163],a[224:227],a[16:19]
s_barrier
ds_read_b128 a[192:195],v20
v_mfma_f32_16x16x32_bf16 a[16:19],a[164:167],a[228:231],a[16:19]
ds_read_b128 a[196:199],v21
v_mfma_f32_16x16x32_bf16 a[48:51],a[164:167],a[236:239],a[48:51]
ds_read_b128 a[200:203],v20 offset:2048
v_mfma_f32_16x16x32_bf16 a[48:51],a[160:163],a[232:235],a[48:51]
ds_read_b128 a[204:207],v21 offset:2048
v_mfma_f32_16x16x32_bf16 a[80:83],a[160:163],a[240:243],a[80:83]
ds_read_b128 a[208:211],v20 offset:4096
v_mfma_f32_16x16x32_bf16 a[80:83],a[164:167],a[244:247],a[80:83]
ds_read_b128 a[212:215],v21 offset:4096
v_mfma_f32_16x16x32_bf16 a[112:115],a[164:167],a[252:255],a[112:115]
buffer_load_dword v26,v24,s[88:91],s93 offen
v_mfma_f32_16x16x32_bf16 a[112:115],a[160:163],a[248:251],a[112:115]
ds_read_b128 a[216:219],v20 offset:6144
v_mfma_f32_16x16x32_bf16 a[20:23],a[168:171],a[224:227],a[20:23]
ds_read_b128 a[220:223],v21 offset:6144
v_mfma_f32_16x16x32_bf16 a[20:23],a[172:175],a[228:231],a[20:23]
ds_read_b64_tr_b16 a[128:129],v22 offset:1024
v_mfma_f32_16x16x32_bf16 a[52:55],a[172:175],a[236:239],a[52:55]
ds_read_b64_tr_b16 a[130:131],v22 offset:3072
v_mfma_f32_16x16x32_bf16 a[52:55],a[168:171],a[232:235],a[52:55]
ds_read_b64_tr_b16 a[132:133],v22 offset:17408
v_mfma_f32_16x16x32_bf16 a[84:87],a[168:171],a[240:243],a[84:87]
ds_read_b64_tr_b16 a[134:135],v22 offset:19456
v_mfma_f32_16x16x32_bf16 a[84:87],a[172:175],a[244:247],a[84:87]
ds_read_b64_tr_b16 a[136:137],v22 offset:1032
v_mfma_f32_16x16x32_bf16 a[116:119],a[172:175],a[252:255],a[116:119]
ds_read_b64_tr_b16 a[138:139],v22 offset:3080
v_mfma_f32_16x16x32_bf16 a[116:119],a[168:171],a[248:251],a[116:119]
ds_read_b64_tr_b16 a[140:141],v22 offset:17416
v_mfma_f32_16x16x32_bf16 a[24:27],a[176:179],a[224:227],a[24:27]
ds_read_b64_tr_b16 a[142:143],v22 offset:19464
v_mfma_f32_16x16x32_bf16 a[24:27],a[180:183],a[228:231],a[24:27]
ds_read_b64_tr_b16 a[144:145],v22 offset:1280
v_mfma_f32_16x16x32_bf16 a[56:59],a[180:183],a[236:239],a[56:59]
ds_read_b64_tr_b16 a[146:147],v22 offset:3328
v_mfma_f32_16x16x32_bf16 a[56:59],a[176:179],a[232:235],a[56:59]
ds_read_b64_tr_b16 a[148:149],v22 offset:17664
v_mfma_f32_16x16x32_bf16 a[88:91],a[176:179],a[240:243],a[88:91]
ds_read_b64_tr_b16 a[150:151],v22 offset:19712
v_mfma_f32_16x16x32_bf16 a[88:91],a[180:183],a[244:247],a[88:91]
ds_read_b64_tr_b16 a[152:153],v22 offset:1288
v_mfma_f32_16x16x32_bf16 a[120:123],a[180:183],a[252:255],a[120:123]
ds_read_b64_tr_b16 a[154:155],v22 offset:3336
v_mfma_f32_16x16x32_bf16 a[120:123],a[176:179],a[248:251],a[120:123]
ds_read_b64_tr_b16 a[156:157],v22 offset:17672
v_mfma_f32_16x16x32_bf16 a[28:31],a[184:187],a[224:227],a[28:31]
ds_read_b64_tr_b16 a[158:159],v22 offset:19720
v_mfma_f32_16x16x32_bf16 a[28:31],a[188:191],a[228:231],a[28:31]
v_mfma_f32_16x16x32_bf16 a[60:63],a[188:191],a[236:239],a[60:63]
v_mfma_f32_16x16x32_bf16 a[60:63],a[184:187],a[232:235],a[60:63]
v_mfma_f32_16x16x32_bf16 a[92:95],a[184:187],a[240:243],a[92:95]
v_mfma_f32_16x16x32_bf16 a[92:95],a[188:191],a[244:247],a[92:95]
v_mfma_f32_16x16x32_bf16 a[124:127],a[188:191],a[252:255],a[124:127]
s_cmp_lg_u32 s53,0
v_mfma_f32_16x16x32_bf16 a[124:127],a[184:187],a[248:251],a[124:127]
s_cbranch_scc1 L_qp_%=
L_qpx_%=:
L_tailseg_%=:
s_waitcnt lgkmcnt(1)
v_mfma_f32_16x16x32_bf16 v[128:131],a[128:131],a[192:195],v[128:131]
s_add_u32 m0,98304,s51
ds_read_b64_tr_b16 a[160:161],v22 offset:1536
v_xor_b32 v20,131072,v20
v_mfma_f32_16x16x32_bf16 v[128:131],a[132:135],a[196:199],v[128:131]
s_add_u32 s54,s54,16384
buffer_load_dwordx4 v4,s[20:23],s47 offen nt lds
v_mfma_f32_16x16x32_bf16 v[136:139],a[132:135],a[204:207],v[136:139]
s_and_b32 s46,s54,0xfffc0000
ds_read_b64_tr_b16 a[162:163],v22 offset:3584
v_xor_b32 v21,131072,v21
v_mfma_f32_16x16x32_bf16 v[136:139],a[128:131],a[200:203],v[136:139]
s_add_u32 s54,s54,16384
ds_read_b64_tr_b16 a[164:165],v22 offset:17920
v_and_b32 v18,48,v6
v_mfma_f32_16x16x32_bf16 v[144:147],a[128:131],a[208:211],v[144:147]
s_add_u32 s49,s49,32768
ds_read_b64_tr_b16 a[166:167],v22 offset:19968
v_lshrrev_b32 v19,4,v6
v_mfma_f32_16x16x32_bf16 v[144:147],a[132:135],a[212:215],v[144:147]
s_lshr_b32 s79,%1,3
ds_read_b64_tr_b16 a[168:169],v22 offset:1544
v_lshlrev_b32 v18,2,v18
v_mfma_f32_16x16x32_bf16 v[152:155],a[132:135],a[220:223],v[152:155]
s_mul_i32 s79,s79,7
ds_read_b64_tr_b16 a[170:171],v22 offset:3592
v_and_b32 v19,48,v19
v_mfma_f32_16x16x32_bf16 v[152:155],a[128:131],a[216:219],v[152:155]
s_mov_b32 s60,138
ds_read_b64_tr_b16 a[172:173],v22 offset:17928
v_add_u32 v18,v18,v19
v_mfma_f32_16x16x32_bf16 v[132:135],a[136:139],a[192:195],v[132:135]
s_lshr_b32 s79,s79,5
ds_read_b64_tr_b16 a[174:175],v22 offset:19976
v_add_u32 v18,98304,v18
v_mfma_f32_16x16x32_bf16 v[132:135],a[140:143],a[196:199],v[132:135]
s_cmp_eq_u32 s49,163840
s_cselect_b32 s49,0,s49
ds_read_b64_tr_b16 a[176:177],v22 offset:1792
v_mfma_f32_16x16x32_bf16 v[140:143],a[140:143],a[204:207],v[140:143]
s_cmp_eq_u32 s50,131072
s_cselect_b32 s62,163840,0
ds_read_b64_tr_b16 a[178:179],v22 offset:3840
v_mfma_f32_16x16x32_bf16 v[140:143],a[136:139],a[200:203],v[140:143]
s_sub_u32 s48,32768,s62
s_sub_u32 s60,s60,s79
ds_read_b64_tr_b16 a[180:181],v22 offset:18176
v_mfma_f32_16x16x32_bf16 v[148:151],a[136:139],a[208:211],v[148:151]
s_add_u32 s50,s50,s48
s_add_u32 s93,s92,L_att0x_%=-L_pc_%=
ds_read_b64_tr_b16 a[182:183],v22 offset:20224
v_mfma_f32_16x16x32_bf16 v[148:151],a[140:143],a[212:215],v[148:151]
s_cmp_eq_u32 s50,131072
s_cselect_b32 s62,163840,0
ds_read_b64_tr_b16 a[184:185],v22 offset:1800
v_mfma_f32_16x16x32_bf16 v[156:159],a[140:143],a[220:223],v[156:159]
ds_read_b64_tr_b16 a[186:187],v22 offset:3848
v_mfma_f32_16x16x32_bf16 v[156:159],a[136:139],a[216:219],v[156:159]
ds_read_b64_tr_b16 a[188:189],v22 offset:18184
v_mfma_f32_16x16x32_bf16 v[160:163],a[144:147],a[192:195],v[160:163]
ds_read_b64_tr_b16 a[190:191],v22 offset:20232
v_mfma_f32_16x16x32_bf16 v[160:163],a[148:151],a[196:199],v[160:163]
buffer_load_dwordx4 v4,s[20:23],s47 offen offset:1024 nt lds
v_mfma_f32_16x16x32_bf16 v[168:171],a[148:151],a[204:207],v[168:171]
s_waitcnt vmcnt(4)
ds_read_b128 a[224:227],v20
v_mfma_f32_16x16x32_bf16 v[168:171],a[144:147],a[200:203],v[168:171]
ds_read_b64_tr_b16 a[128:129],v22
v_mfma_f32_16x16x32_bf16 v[176:179],a[144:147],a[208:211],v[176:179]
ds_read_b64_tr_b16 a[130:131],v22 offset:2048
v_mfma_f32_16x16x32_bf16 v[176:179],a[148:151],a[212:215],v[176:179]
ds_read_b64_tr_b16 a[132:133],v22 offset:16384
v_mfma_f32_16x16x32_bf16 v[184:187],a[148:151],a[220:223],v[184:187]
ds_read_b64_tr_b16 a[134:135],v22 offset:18432
v_mfma_f32_16x16x32_bf16 v[184:187],a[144:147],a[216:219],v[184:187]
ds_read_b64_tr_b16 a[144:145],v22 offset:8
v_mfma_f32_16x16x32_bf16 v[164:167],a[152:155],a[192:195],v[164:167]
s_waitcnt lgkmcnt(6)
ds_read_b64_tr_b16 a[146:147],v22 offset:2056
v_mfma_f32_16x16x32_bf16 v[164:167],a[156:159],a[196:199],v[164:167]
ds_read_b64_tr_b16 a[148:149],v22 offset:16392
v_mfma_f32_16x16x32_bf16 v[172:175],a[156:159],a[204:207],v[172:175]
ds_read_b64_tr_b16 a[150:151],v22 offset:18440
v_mfma_f32_16x16x32_bf16 v[172:175],a[152:155],a[200:203],v[172:175]
v_readfirstlane_b32 s68,v13
s_nop 1
s_add_u32 s68,s68,1
v_mfma_f32_16x16x32_bf16 v[180:183],a[152:155],a[208:211],v[180:183]
s_xor_b32 s69,s68,0x9e3779b9
ds_read_b128 a[228:231],v21
v_mfma_f32_16x16x32_bf16 v[180:183],a[156:159],a[212:215],v[180:183]
v_mfma_f32_16x16x32_bf16 v[188:191],a[156:159],a[220:223],v[188:191]
ds_read_b128 a[232:235],v20 offset:2048
v_mfma_f32_16x16x32_bf16 v[188:191],a[152:155],a[216:219],v[188:191]
v_mfma_f32_16x16x32_bf16 v[192:195],a[160:163],a[192:195],v[192:195]
ds_read_b128 a[236:239],v21 offset:2048
ds_read_b128 a[240:243],v20 offset:4096
ds_read_b128 a[244:247],v21 offset:4096
ds_read_b128 a[248:251],v20 offset:6144
ds_read_b128 a[252:255],v21 offset:6144
v_mfma_f32_16x16x32_bf16 v[192:195],a[164:167],a[196:199],v[192:195]
v_mfma_f32_16x16x32_bf16 v[200:203],a[164:167],a[204:207],v[200:203]
v_mfma_f32_16x16x32_bf16 v[200:203],a[160:163],a[200:203],v[200:203]
v_mfma_f32_16x16x32_bf16 v[208:211],a[160:163],a[208:211],v[208:211]
buffer_load_dwordx4 v5,s[20:23],s47 offen offset:2048 nt lds
v_mfma_f32_16x16x32_bf16 v[208:211],a[164:167],a[212:215],v[208:211]
v_mfma_f32_16x16x32_bf16 v[216:219],a[164:167],a[220:223],v[216:219]
s_waitcnt vmcnt(4) lgkmcnt(0)
v_mfma_f32_16x16x32_bf16 v[216:219],a[160:163],a[216:219],v[216:219]
s_barrier
ds_read_b64_tr_b16 a[136:137],v22 offset:32768
v_mfma_f32_16x16x32_bf16 v[196:199],a[168:171],a[192:195],v[196:199]
ds_read_b64_tr_b16 a[138:139],v22 offset:34816
v_mfma_f32_16x16x32_bf16 v[196:199],a[172:175],a[196:199],v[196:199]
ds_read_b64_tr_b16 a[140:141],v22 offset:49152
v_mfma_f32_16x16x32_bf16 v[204:207],a[172:175],a[204:207],v[204:207]
ds_read_b64_tr_b16 a[142:143],v22 offset:51200
v_mfma_f32_16x16x32_bf16 v[204:207],a[168:171],a[200:203],v[204:207]
ds_read_b64_tr_b16 a[152:153],v22 offset:32776
v_mfma_f32_16x16x32_bf16 v[212:215],a[168:171],a[208:211],v[212:215]
ds_read_b64_tr_b16 a[154:155],v22 offset:34824
v_mfma_f32_16x16x32_bf16 v[212:215],a[172:175],a[212:215],v[212:215]
ds_read_b64_tr_b16 a[156:157],v22 offset:49160
v_mfma_f32_16x16x32_bf16 v[220:223],a[172:175],a[220:223],v[220:223]
ds_read_b64_tr_b16 a[158:159],v22 offset:51208
v_mfma_f32_16x16x32_bf16 v[220:223],a[168:171],a[216:219],v[220:223]
ds_read_b64_tr_b16 a[160:161],v22 offset:256
v_mfma_f32_16x16x32_bf16 v[224:227],a[176:179],a[192:195],v[224:227]
buffer_load_dwordx4 v5,s[20:23],s47 offen offset:3072 nt lds
v_mfma_f32_16x16x32_bf16 v[224:227],a[180:183],a[196:199],v[224:227]
ds_read_b64_tr_b16 a[162:163],v22 offset:2304
v_mfma_f32_16x16x32_bf16 v[232:235],a[180:183],a[204:207],v[232:235]
ds_read_b64_tr_b16 a[164:165],v22 offset:16640
v_mfma_f32_16x16x32_bf16 v[232:235],a[176:179],a[200:203],v[232:235]
ds_read_b64_tr_b16 a[166:167],v22 offset:18688
v_mfma_f32_16x16x32_bf16 v[240:243],a[176:179],a[208:211],v[240:243]
ds_read_b64_tr_b16 a[168:169],v22 offset:33024
v_mfma_f32_16x16x32_bf16 v[240:243],a[180:183],a[212:215],v[240:243]
ds_read_b64_tr_b16 a[170:171],v22 offset:35072
v_mfma_f32_16x16x32_bf16 v[248:251],a[180:183],a[220:223],v[248:251]
ds_read_b64_tr_b16 a[172:173],v22 offset:49408
v_mfma_f32_16x16x32_bf16 v[248:251],a[176:179],a[216:219],v[248:251]
ds_read_b64_tr_b16 a[174:175],v22 offset:51456
v_mfma_f32_16x16x32_bf16 v[228:231],a[184:187],a[192:195],v[228:231]
ds_read_b64_tr_b16 a[176:177],v22 offset:264
v_mfma_f32_16x16x32_bf16 v[228:231],a[188:191],a[196:199],v[228:231]
ds_read_b64_tr_b16 a[178:179],v22 offset:2312
v_mfma_f32_16x16x32_bf16 v[236:239],a[188:191],a[204:207],v[236:239]
ds_read_b64_tr_b16 a[180:181],v22 offset:16648
v_mfma_f32_16x16x32_bf16 v[236:239],a[184:187],a[200:203],v[236:239]
ds_read_b64_tr_b16 a[182:183],v22 offset:18696
v_mfma_f32_16x16x32_bf16 v[244:247],a[184:187],a[208:211],v[244:247]
v_mfma_f32_16x16x32_bf16 v[244:247],a[188:191],a[212:215],v[244:247]
s_add_u32 m0,98304,s52
v_mfma_f32_16x16x32_bf16 v[252:255],a[188:191],a[220:223],v[252:255]
ds_read_b64_tr_b16 a[188:189],v22 offset:49416
v_mfma_f32_16x16x32_bf16 v[252:255],a[184:187],a[216:219],v[252:255]
ds_read_b64_tr_b16 a[184:185],v22 offset:33032
v_mfma_f32_16x16x32_bf16 a[0:3],a[128:131],a[192:195],a[0:3]
)A"
R"A(buffer_load_dwordx4 v4,s[24:27],s47 offen nt lds
v_mfma_f32_16x16x32_bf16 a[0:3],a[132:135],a[196:199],a[0:3]
s_waitcnt lgkmcnt(4)
ds_read_b64_tr_b16 a[186:187],v22 offset:35080
v_mfma_f32_16x16x32_bf16 a[0:3],a[136:139],a[224:227],a[0:3]
ds_read_b64_tr_b16 a[190:191],v22 offset:51464
v_mfma_f32_16x16x32_bf16 a[0:3],a[140:143],a[228:231],a[0:3]
v_mfma_f32_16x16x32_bf16 a[32:35],a[140:143],a[236:239],a[32:35]
v_mfma_f32_16x16x32_bf16 a[32:35],a[136:139],a[232:235],a[32:35]
v_mfma_f32_16x16x32_bf16 a[32:35],a[132:135],a[204:207],a[32:35]
v_mfma_f32_16x16x32_bf16 a[32:35],a[128:131],a[200:203],a[32:35]
v_mfma_f32_16x16x32_bf16 a[64:67],a[128:131],a[208:211],a[64:67]
v_mfma_f32_16x16x32_bf16 a[64:67],a[132:135],a[212:215],a[64:67]
s_nop 1
v_accvgpr_read_b32 v64,a0
v_accvgpr_read_b32 v65,a1
v_mfma_f32_16x16x32_bf16 a[64:67],a[136:139],a[240:243],a[64:67]
v_mul_f32 v64,s61,v64
v_mul_f32 v65,s61,v65
v_mfma_f32_16x16x32_bf16 a[64:67],a[140:143],a[244:247],a[64:67]
v_accvgpr_read_b32 v66,a2
v_accvgpr_read_b32 v67,a3
v_mfma_f32_16x16x32_bf16 a[96:99],a[140:143],a[252:255],a[96:99]
ds_read_b64_tr_b16 a[140:141],v22 offset:49664
v_cvt_pk_bf16_f32 v64,v64,v65
v_mfma_f32_16x16x32_bf16 a[96:99],a[136:139],a[248:251],a[96:99]
ds_read_b64_tr_b16 a[136:137],v22 offset:33280
v_accvgpr_read_b32 v80,a32
v_mfma_f32_16x16x32_bf16 a[96:99],a[132:135],a[220:223],a[96:99]
ds_read_b64_tr_b16 a[132:133],v22 offset:16896
v_accvgpr_read_b32 v81,a33
v_mfma_f32_16x16x32_bf16 a[96:99],a[128:131],a[216:219],a[96:99]
ds_read_b64_tr_b16 a[128:129],v22 offset:512
v_mul_f32 v80,s61,v80
v_mfma_f32_16x16x32_bf16 a[4:7],a[144:147],a[192:195],a[4:7]
buffer_load_dwordx4 v5,s[24:27],s47 offen offset:1024 nt lds
v_mfma_f32_16x16x32_bf16 a[4:7],a[148:151],a[196:199],a[4:7]
ds_read_b64_tr_b16 a[130:131],v22 offset:2560
v_mul_f32 v81,s61,v81
v_mfma_f32_16x16x32_bf16 a[4:7],a[152:155],a[224:227],a[4:7]
ds_read_b64_tr_b16 a[134:135],v22 offset:18944
v_accvgpr_read_b32 v82,a34
v_mfma_f32_16x16x32_bf16 a[4:7],a[156:159],a[228:231],a[4:7]
ds_read_b64_tr_b16 a[138:139],v22 offset:35328
v_accvgpr_read_b32 v83,a35
v_mfma_f32_16x16x32_bf16 a[36:39],a[156:159],a[236:239],a[36:39]
ds_read_b64_tr_b16 a[142:143],v22 offset:51712
v_mul_f32 v66,s61,v66
v_mfma_f32_16x16x32_bf16 a[36:39],a[152:155],a[232:235],a[36:39]
v_mul_f32 v67,s61,v67
v_cvt_pk_bf16_f32 v80,v80,v81
v_mfma_f32_16x16x32_bf16 a[36:39],a[148:151],a[204:207],a[36:39]
v_mul_f32 v82,s61,v82
v_mul_f32 v83,s61,v83
v_mfma_f32_16x16x32_bf16 a[36:39],a[144:147],a[200:203],a[36:39]
v_cvt_pk_bf16_f32 v65,v66,v67
v_cvt_pk_bf16_f32 v81,v82,v83
v_mfma_f32_16x16x32_bf16 a[68:71],a[144:147],a[208:211],a[68:71]
v_accvgpr_read_b32 v96,a64
v_accvgpr_read_b32 v97,a65
v_mfma_f32_16x16x32_bf16 a[68:71],a[148:151],a[212:215],a[68:71]
v_accvgpr_read_b32 v112,a96
v_accvgpr_read_b32 v113,a97
v_mfma_f32_16x16x32_bf16 a[68:71],a[152:155],a[240:243],a[68:71]
v_accvgpr_read_b32 v66,a4
v_accvgpr_read_b32 v67,a5
v_mfma_f32_16x16x32_bf16 a[68:71],a[156:159],a[244:247],a[68:71]
v_mul_f32 v96,s61,v96
v_mul_f32 v97,s61,v97
v_mfma_f32_16x16x32_bf16 a[100:103],a[156:159],a[252:255],a[100:103]
ds_read_b64_tr_b16 a[156:157],v22 offset:49672
v_accvgpr_read_b32 v98,a66
v_mfma_f32_16x16x32_bf16 a[100:103],a[152:155],a[248:251],a[100:103]
ds_read_b64_tr_b16 a[152:153],v22 offset:33288
v_accvgpr_read_b32 v82,a36
v_mfma_f32_16x16x32_bf16 a[100:103],a[148:151],a[220:223],a[100:103]
ds_read_b64_tr_b16 a[148:149],v22 offset:16904
v_accvgpr_read_b32 v83,a37
v_mfma_f32_16x16x32_bf16 a[100:103],a[144:147],a[216:219],a[100:103]
ds_read_b64_tr_b16 a[144:145],v22 offset:520
v_accvgpr_read_b32 v99,a67
v_mfma_f32_16x16x32_bf16 a[8:11],a[160:163],a[192:195],a[8:11]
buffer_load_dwordx4 v4,s[24:27],s47 offen offset:2048 nt lds
v_mfma_f32_16x16x32_bf16 a[8:11],a[164:167],a[196:199],a[8:11]
ds_read_b64_tr_b16 a[146:147],v22 offset:2568
v_mul_f32 v112,s61,v112
v_mfma_f32_16x16x32_bf16 a[8:11],a[168:171],a[224:227],a[8:11]
ds_read_b64_tr_b16 a[150:151],v22 offset:18952
v_mul_f32 v113,s61,v113
v_mfma_f32_16x16x32_bf16 a[8:11],a[172:175],a[228:231],a[8:11]
ds_read_b64_tr_b16 a[154:155],v22 offset:35336
v_accvgpr_read_b32 v114,a98
v_mfma_f32_16x16x32_bf16 a[40:43],a[172:175],a[236:239],a[40:43]
ds_read_b64_tr_b16 a[158:159],v22 offset:51720
v_accvgpr_read_b32 v115,a99
v_mfma_f32_16x16x32_bf16 a[40:43],a[168:171],a[232:235],a[40:43]
v_accvgpr_read_b32 v12,a6
v_accvgpr_read_b32 v13,a7
v_mfma_f32_16x16x32_bf16 a[40:43],a[164:167],a[204:207],a[40:43]
v_mul_f32 v66,s61,v66
v_mul_f32 v67,s61,v67
v_mfma_f32_16x16x32_bf16 a[40:43],a[160:163],a[200:203],a[40:43]
v_accvgpr_read_b32 v14,a38
v_accvgpr_read_b32 v15,a39
v_mfma_f32_16x16x32_bf16 a[72:75],a[160:163],a[208:211],a[72:75]
v_mul_f32 v82,s61,v82
v_mul_f32 v83,s61,v83
v_mfma_f32_16x16x32_bf16 a[72:75],a[164:167],a[212:215],a[72:75]
v_accvgpr_read_b32 v68,a8
v_accvgpr_read_b32 v69,a9
v_mfma_f32_16x16x32_bf16 a[72:75],a[168:171],a[240:243],a[72:75]
v_mul_f32 v68,s61,v68
v_mul_f32 v69,s61,v69
v_mfma_f32_16x16x32_bf16 a[72:75],a[172:175],a[244:247],a[72:75]
v_accvgpr_read_b32 v70,a10
v_accvgpr_read_b32 v71,a11
v_mfma_f32_16x16x32_bf16 a[104:107],a[172:175],a[252:255],a[104:107]
ds_read_b64_tr_b16 a[172:173],v22 offset:49920
v_cvt_pk_bf16_f32 v96,v96,v97
v_mfma_f32_16x16x32_bf16 a[104:107],a[168:171],a[248:251],a[104:107]
ds_read_b64_tr_b16 a[168:169],v22 offset:33536
v_mul_f32 v98,s61,v98
v_mfma_f32_16x16x32_bf16 a[104:107],a[164:167],a[220:223],a[104:107]
ds_read_b64_tr_b16 a[164:165],v22 offset:17152
v_mul_f32 v99,s61,v99
v_mfma_f32_16x16x32_bf16 a[104:107],a[160:163],a[216:219],a[104:107]
ds_read_b64_tr_b16 a[160:161],v22 offset:768
v_cvt_pk_bf16_f32 v112,v112,v113
v_mfma_f32_16x16x32_bf16 a[12:15],a[176:179],a[192:195],a[12:15]
s_waitcnt lgkmcnt(4)
buffer_load_dwordx4 v5,s[24:27],s47 offen offset:3072 nt lds
v_mfma_f32_16x16x32_bf16 a[12:15],a[180:183],a[196:199],a[12:15]
s_add_u32 s47,s47,16384
s_and_b32 s47,s47,0x3ffff
s_or_b32 s47,s47,s46
v_mfma_f32_16x16x32_bf16 a[12:15],a[184:187],a[224:227],a[12:15]
s_and_b32 s46,s54,0xfffc0000
ds_read_b64_tr_b16 a[162:163],v22 offset:2816
v_mul_f32 v114,s61,v114
v_mfma_f32_16x16x32_bf16 a[12:15],a[188:191],a[228:231],a[12:15]
s_add_u32 s54,s54,16384
ds_read_b64_tr_b16 a[166:167],v22 offset:19200
v_mul_f32 v115,s61,v115
v_mfma_f32_16x16x32_bf16 a[44:47],a[188:191],a[236:239],a[44:47]
ds_read_b64_tr_b16 a[170:171],v22 offset:35584
v_cvt_pk_bf16_f32 v68,v68,v69
v_mfma_f32_16x16x32_bf16 a[44:47],a[184:187],a[232:235],a[44:47]
ds_read_b64_tr_b16 a[174:175],v22 offset:51968
v_mul_f32 v70,s61,v70
v_mfma_f32_16x16x32_bf16 a[44:47],a[180:183],a[204:207],a[44:47]
v_mul_f32 v71,s61,v71
v_mul_f32 v12,s61,v12
v_mfma_f32_16x16x32_bf16 a[44:47],a[176:179],a[200:203],a[44:47]
v_mul_f32 v13,s61,v13
v_cvt_pk_bf16_f32 v66,v66,v67
v_mfma_f32_16x16x32_bf16 a[76:79],a[176:179],a[208:211],a[76:79]
v_mul_f32 v14,s61,v14
v_mul_f32 v15,s61,v15
v_mfma_f32_16x16x32_bf16 a[76:79],a[180:183],a[212:215],a[76:79]
v_cvt_pk_bf16_f32 v82,v82,v83
v_cvt_pk_bf16_f32 v97,v98,v99
v_mfma_f32_16x16x32_bf16 a[76:79],a[184:187],a[240:243],a[76:79]
v_cvt_pk_bf16_f32 v113,v114,v115
v_cvt_pk_bf16_f32 v69,v70,v71
v_mfma_f32_16x16x32_bf16 a[76:79],a[188:191],a[244:247],a[76:79]
v_accvgpr_read_b32 v84,a40
v_accvgpr_read_b32 v85,a41
v_mfma_f32_16x16x32_bf16 a[108:111],a[188:191],a[252:255],a[108:111]
ds_read_b64_tr_b16 a[188:189],v22 offset:49928
v_accvgpr_read_b32 v100,a72
v_mfma_f32_16x16x32_bf16 a[108:111],a[184:187],a[248:251],a[108:111]
ds_read_b64_tr_b16 a[184:185],v22 offset:33544
v_accvgpr_read_b32 v101,a73
v_mfma_f32_16x16x32_bf16 a[108:111],a[180:183],a[220:223],a[108:111]
ds_read_b64_tr_b16 a[180:181],v22 offset:17160
v_accvgpr_read_b32 v116,a104
v_mfma_f32_16x16x32_bf16 a[108:111],a[176:179],a[216:219],a[108:111]
ds_read_b64_tr_b16 a[176:177],v22 offset:776
v_accvgpr_read_b32 v117,a105
v_mfma_f32_16x16x32_bf16 a[16:19],a[128:131],a[192:195],a[16:19]
ds_read_b64_tr_b16 a[178:179],v22 offset:2824
v_cvt_pk_bf16_f32 v67,v12,v13
v_mfma_f32_16x16x32_bf16 a[16:19],a[132:135],a[196:199],a[16:19]
ds_read_b64_tr_b16 a[182:183],v22 offset:19208
v_cvt_pk_bf16_f32 v83,v14,v15
v_mfma_f32_16x16x32_bf16 a[16:19],a[136:139],a[224:227],a[16:19]
ds_read_b64_tr_b16 a[186:187],v22 offset:35592
v_accvgpr_read_b32 v98,a68
v_mfma_f32_16x16x32_bf16 a[16:19],a[140:143],a[228:231],a[16:19]
s_add_u32 m0,131072,s51
ds_read_b64_tr_b16 a[190:191],v22 offset:51976
v_accvgpr_read_b32 v99,a69
v_mfma_f32_16x16x32_bf16 a[48:51],a[140:143],a[236:239],a[48:51]
v_accvgpr_read_b32 v114,a100
v_accvgpr_read_b32 v115,a101
v_mfma_f32_16x16x32_bf16 a[48:51],a[136:139],a[232:235],a[48:51]
v_accvgpr_read_b32 v70,a12
v_accvgpr_read_b32 v71,a13
v_mfma_f32_16x16x32_bf16 a[48:51],a[132:135],a[204:207],a[48:51]
buffer_load_dwordx4 v4,s[20:23],s47 offen nt lds
v_mfma_f32_16x16x32_bf16 a[48:51],a[128:131],a[200:203],a[48:51]
v_mul_f32 v84,s61,v84
v_mul_f32 v85,s61,v85
v_mfma_f32_16x16x32_bf16 a[80:83],a[128:131],a[208:211],a[80:83]
v_accvgpr_read_b32 v86,a42
v_accvgpr_read_b32 v87,a43
v_mfma_f32_16x16x32_bf16 a[80:83],a[132:135],a[212:215],a[80:83]
v_mul_f32 v100,s61,v100
v_mul_f32 v101,s61,v101
v_mfma_f32_16x16x32_bf16 a[80:83],a[136:139],a[240:243],a[80:83]
v_accvgpr_read_b32 v102,a74
v_accvgpr_read_b32 v103,a75
v_mfma_f32_16x16x32_bf16 a[80:83],a[140:143],a[244:247],a[80:83]
v_mul_f32 v116,s61,v116
v_mul_f32 v117,s61,v117
v_mfma_f32_16x16x32_bf16 a[112:115],a[140:143],a[252:255],a[112:115]
ds_read_b64_tr_b16 a[140:141],v22 offset:50184
v_accvgpr_read_b32 v118,a106
v_mfma_f32_16x16x32_bf16 a[112:115],a[136:139],a[248:251],a[112:115]
ds_read_b64_tr_b16 a[136:137],v22 offset:33800
v_accvgpr_read_b32 v119,a107
v_mfma_f32_16x16x32_bf16 a[112:115],a[132:135],a[220:223],a[112:115]
ds_read_b64_tr_b16 a[132:133],v22 offset:50176
v_accvgpr_read_b32 v16,a70
v_mfma_f32_16x16x32_bf16 a[112:115],a[128:131],a[216:219],a[112:115]
ds_read_b64_tr_b16 a[128:129],v22 offset:33792
v_accvgpr_read_b32 v17,a71
v_mfma_f32_16x16x32_bf16 a[20:23],a[144:147],a[192:195],a[20:23]
ds_read_b64_tr_b16 a[130:131],v22 offset:35840
v_mul_f32 v98,s61,v98
v_mfma_f32_16x16x32_bf16 a[20:23],a[148:151],a[196:199],a[20:23]
ds_read_b64_tr_b16 a[134:135],v22 offset:52224
v_mul_f32 v99,s61,v99
v_mfma_f32_16x16x32_bf16 a[20:23],a[152:155],a[224:227],a[20:23]
ds_read_b64_tr_b16 a[138:139],v22 offset:35848
v_accvgpr_read_b32 v12,a102
v_mfma_f32_16x16x32_bf16 a[20:23],a[156:159],a[228:231],a[20:23]
ds_read_b64_tr_b16 a[142:143],v22 offset:52232
v_accvgpr_read_b32 v13,a103
v_mfma_f32_16x16x32_bf16 a[52:55],a[156:159],a[236:239],a[52:55]
v_mul_f32 v114,s61,v114
v_mul_f32 v115,s61,v115
v_mfma_f32_16x16x32_bf16 a[52:55],a[152:155],a[232:235],a[52:55]
v_accvgpr_read_b32 v14,a14
v_accvgpr_read_b32 v15,a15
v_mfma_f32_16x16x32_bf16 a[52:55],a[148:151],a[204:207],a[52:55]
buffer_load_dwordx4 v4,s[20:23],s47 offen offset:1024 nt lds
v_mfma_f32_16x16x32_bf16 a[52:55],a[144:147],a[200:203],a[52:55]
v_mul_f32 v70,s61,v70
v_mul_f32 v71,s61,v71
v_mfma_f32_16x16x32_bf16 a[84:87],a[144:147],a[208:211],a[84:87]
v_cvt_pk_bf16_f32 v84,v84,v85
v_mul_f32 v86,s61,v86
v_mfma_f32_16x16x32_bf16 a[84:87],a[148:151],a[212:215],a[84:87]
v_mul_f32 v87,s61,v87
v_cvt_pk_bf16_f32 v100,v100,v101
v_mfma_f32_16x16x32_bf16 a[84:87],a[152:155],a[240:243],a[84:87]
v_mul_f32 v102,s61,v102
v_mul_f32 v103,s61,v103
v_mfma_f32_16x16x32_bf16 a[84:87],a[156:159],a[244:247],a[84:87]
v_cvt_pk_bf16_f32 v116,v116,v117
v_mul_f32 v118,s61,v118
v_mfma_f32_16x16x32_bf16 a[116:119],a[156:159],a[252:255],a[116:119]
ds_read_b64_tr_b16 a[156:157],v22 offset:50440
v_mul_f32 v119,s61,v119
v_mfma_f32_16x16x32_bf16 a[116:119],a[152:155],a[248:251],a[116:119]
ds_read_b64_tr_b16 a[152:153],v22 offset:34056
v_mul_f32 v16,s61,v16
v_mfma_f32_16x16x32_bf16 a[116:119],a[148:151],a[220:223],a[116:119]
ds_read_b64_tr_b16 a[148:149],v22 offset:50432
v_mul_f32 v17,s61,v17
v_mfma_f32_16x16x32_bf16 a[116:119],a[144:147],a[216:219],a[116:119]
s_waitcnt lgkmcnt(3)
ds_read_b64_tr_b16 a[144:145],v22 offset:34048
v_cvt_pk_bf16_f32 v98,v98,v99
v_mfma_f32_16x16x32_bf16 a[24:27],a[160:163],a[192:195],a[24:27]
)A"
R"A(ds_read_b64_tr_b16 a[146:147],v22 offset:36096
v_mul_f32 v12,s61,v12
v_mfma_f32_16x16x32_bf16 a[24:27],a[164:167],a[196:199],a[24:27]
ds_read_b64_tr_b16 a[150:151],v22 offset:52480
v_mul_f32 v13,s61,v13
v_mfma_f32_16x16x32_bf16 a[24:27],a[168:171],a[224:227],a[24:27]
ds_read_b64_tr_b16 a[154:155],v22 offset:36104
v_cvt_pk_bf16_f32 v114,v114,v115
v_mfma_f32_16x16x32_bf16 a[24:27],a[172:175],a[228:231],a[24:27]
ds_read_b64_tr_b16 a[158:159],v22 offset:52488
v_mul_f32 v14,s61,v14
v_mfma_f32_16x16x32_bf16 a[56:59],a[172:175],a[236:239],a[56:59]
v_mul_f32 v15,s61,v15
v_cvt_pk_bf16_f32 v70,v70,v71
v_mfma_f32_16x16x32_bf16 a[56:59],a[168:171],a[232:235],a[56:59]
v_cvt_pk_bf16_f32 v85,v86,v87
v_cvt_pk_bf16_f32 v101,v102,v103
v_mfma_f32_16x16x32_bf16 a[56:59],a[164:167],a[204:207],a[56:59]
buffer_load_dwordx4 v5,s[20:23],s47 offen offset:2048 nt lds
v_mfma_f32_16x16x32_bf16 a[56:59],a[160:163],a[200:203],a[56:59]
v_cvt_pk_bf16_f32 v117,v118,v119
v_accvgpr_read_b32 v72,a16
v_mfma_f32_16x16x32_bf16 a[88:91],a[160:163],a[208:211],a[88:91]
v_accvgpr_read_b32 v73,a17
v_accvgpr_read_b32 v88,a48
v_mfma_f32_16x16x32_bf16 a[88:91],a[164:167],a[212:215],a[88:91]
v_accvgpr_read_b32 v89,a49
v_accvgpr_read_b32 v104,a80
v_mfma_f32_16x16x32_bf16 a[88:91],a[168:171],a[240:243],a[88:91]
v_accvgpr_read_b32 v105,a81
v_cvt_pk_bf16_f32 v99,v16,v17
v_mfma_f32_16x16x32_bf16 a[88:91],a[172:175],a[244:247],a[88:91]
v_cvt_pk_bf16_f32 v115,v12,v13
v_cvt_pk_bf16_f32 v71,v14,v15
v_mfma_f32_16x16x32_bf16 a[120:123],a[172:175],a[252:255],a[120:123]
ds_read_b64_tr_b16 a[172:173],v22 offset:50696
v_accvgpr_read_b32 v86,a44
v_mfma_f32_16x16x32_bf16 a[120:123],a[168:171],a[248:251],a[120:123]
ds_read_b64_tr_b16 a[168:169],v22 offset:34312
v_accvgpr_read_b32 v87,a45
v_mfma_f32_16x16x32_bf16 a[120:123],a[164:167],a[220:223],a[120:123]
ds_read_b64_tr_b16 a[164:165],v22 offset:50688
v_accvgpr_read_b32 v102,a76
v_mfma_f32_16x16x32_bf16 a[120:123],a[160:163],a[216:219],a[120:123]
ds_read_b64_tr_b16 a[160:161],v22 offset:34304
v_accvgpr_read_b32 v103,a77
v_mfma_f32_16x16x32_bf16 a[28:31],a[176:179],a[192:195],a[28:31]
ds_read_b64_tr_b16 a[162:163],v22 offset:36352
v_accvgpr_read_b32 v118,a108
v_mfma_f32_16x16x32_bf16 a[28:31],a[180:183],a[196:199],a[28:31]
ds_read_b64_tr_b16 a[166:167],v22 offset:52736
v_accvgpr_read_b32 v119,a109
v_mfma_f32_16x16x32_bf16 a[28:31],a[184:187],a[224:227],a[28:31]
ds_read_b64_tr_b16 a[170:171],v22 offset:36360
v_mul_f32 v72,s61,v72
v_mfma_f32_16x16x32_bf16 a[28:31],a[188:191],a[228:231],a[28:31]
ds_read_b64_tr_b16 a[174:175],v22 offset:52744
v_mul_f32 v73,s61,v73
v_mfma_f32_16x16x32_bf16 a[60:63],a[188:191],a[236:239],a[60:63]
v_accvgpr_read_b32 v74,a18
v_accvgpr_read_b32 v75,a19
v_mfma_f32_16x16x32_bf16 a[60:63],a[184:187],a[232:235],a[60:63]
v_mul_f32 v88,s61,v88
v_mul_f32 v89,s61,v89
v_mfma_f32_16x16x32_bf16 a[60:63],a[180:183],a[204:207],a[60:63]
buffer_load_dwordx4 v5,s[20:23],s47 offen offset:3072 nt lds
v_mfma_f32_16x16x32_bf16 a[60:63],a[176:179],a[200:203],a[60:63]
v_accvgpr_read_b32 v90,a50
v_accvgpr_read_b32 v91,a51
v_mfma_f32_16x16x32_bf16 a[92:95],a[176:179],a[208:211],a[92:95]
v_mul_f32 v104,s61,v104
v_mul_f32 v105,s61,v105
v_mfma_f32_16x16x32_bf16 a[92:95],a[180:183],a[212:215],a[92:95]
v_accvgpr_read_b32 v106,a82
v_accvgpr_read_b32 v107,a83
v_mfma_f32_16x16x32_bf16 a[92:95],a[184:187],a[240:243],a[92:95]
v_accvgpr_read_b32 v16,a46
v_accvgpr_read_b32 v17,a47
v_mfma_f32_16x16x32_bf16 a[92:95],a[188:191],a[244:247],a[92:95]
v_mul_f32 v86,s61,v86
v_mul_f32 v87,s61,v87
v_mfma_f32_16x16x32_bf16 a[124:127],a[188:191],a[252:255],a[124:127]
ds_read_b64_tr_b16 a[188:189],v22 offset:50952
v_accvgpr_read_b32 v12,a78
v_mfma_f32_16x16x32_bf16 a[124:127],a[184:187],a[248:251],a[124:127]
ds_read_b64_tr_b16 a[184:185],v22 offset:34568
v_accvgpr_read_b32 v13,a79
v_mfma_f32_16x16x32_bf16 a[124:127],a[180:183],a[220:223],a[124:127]
ds_read_b64_tr_b16 a[180:181],v22 offset:50944
v_mul_f32 v102,s61,v102
v_mfma_f32_16x16x32_bf16 a[124:127],a[176:179],a[216:219],a[124:127]
ds_read_b64_tr_b16 a[176:177],v22 offset:34560
v_mul_f32 v103,s61,v103
v_mfma_f32_16x16x32_bf16 v[128:131],a[128:131],a[224:227],v[128:131]
ds_read_b64_tr_b16 a[178:179],v22 offset:36608
v_accvgpr_read_b32 v14,a110
v_mfma_f32_16x16x32_bf16 v[128:131],a[132:135],a[228:231],v[128:131]
ds_read_b64_tr_b16 a[182:183],v22 offset:52992
v_accvgpr_read_b32 v15,a111
v_mfma_f32_16x16x32_bf16 v[136:139],a[132:135],a[236:239],v[136:139]
ds_read_b64_tr_b16 a[186:187],v22 offset:36616
v_mul_f32 v118,s61,v118
v_mfma_f32_16x16x32_bf16 v[136:139],a[128:131],a[232:235],v[136:139]
s_add_u32 m0,131072,s52
ds_read_b64_tr_b16 a[190:191],v22 offset:53000
v_mul_f32 v119,s61,v119
v_mfma_f32_16x16x32_bf16 v[144:147],a[128:131],a[240:243],v[144:147]
v_cvt_pk_bf16_f32 v72,v72,v73
v_mul_f32 v74,s61,v74
v_mfma_f32_16x16x32_bf16 v[144:147],a[132:135],a[244:247],v[144:147]
v_mul_f32 v75,s61,v75
v_cvt_pk_bf16_f32 v88,v88,v89
v_mfma_f32_16x16x32_bf16 v[152:155],a[132:135],a[252:255],v[152:155]
buffer_load_dwordx4 v4,s[24:27],s47 offen nt lds
v_mfma_f32_16x16x32_bf16 v[152:155],a[128:131],a[248:251],v[152:155]
v_mul_f32 v90,s61,v90
v_mul_f32 v91,s61,v91
v_mfma_f32_16x16x32_bf16 v[132:135],a[136:139],a[224:227],v[132:135]
v_cvt_pk_bf16_f32 v104,v104,v105
v_mul_f32 v106,s61,v106
v_mfma_f32_16x16x32_bf16 v[132:135],a[140:143],a[228:231],v[132:135]
v_mul_f32 v107,s61,v107
v_mul_f32 v16,s61,v16
v_mfma_f32_16x16x32_bf16 v[140:143],a[140:143],a[236:239],v[140:143]
v_mul_f32 v17,s61,v17
v_cvt_pk_bf16_f32 v86,v86,v87
v_mfma_f32_16x16x32_bf16 v[140:143],a[136:139],a[232:235],v[140:143]
v_mul_f32 v12,s61,v12
v_mul_f32 v13,s61,v13
v_mfma_f32_16x16x32_bf16 v[148:151],a[136:139],a[240:243],v[148:151]
v_cvt_pk_bf16_f32 v102,v102,v103
v_mul_f32 v14,s61,v14
v_mfma_f32_16x16x32_bf16 v[148:151],a[140:143],a[244:247],v[148:151]
v_mul_f32 v15,s61,v15
v_cvt_pk_bf16_f32 v118,v118,v119
v_mfma_f32_16x16x32_bf16 v[156:159],a[140:143],a[252:255],v[156:159]
s_waitcnt vmcnt(5) lgkmcnt(0)
v_cvt_pk_bf16_f32 v73,v74,v75
v_cvt_pk_bf16_f32 v89,v90,v91
v_mfma_f32_16x16x32_bf16 v[156:159],a[136:139],a[248:251],v[156:159]
s_barrier
ds_read_b128 v[48:51],v18
v_cvt_pk_bf16_f32 v105,v106,v107
v_mfma_f32_16x16x32_bf16 v[160:163],a[144:147],a[224:227],v[160:163]
v_cvt_pk_bf16_f32 v87,v16,v17
v_cvt_pk_bf16_f32 v103,v12,v13
v_mfma_f32_16x16x32_bf16 v[160:163],a[148:151],a[228:231],v[160:163]
ds_read_b128 v[52:55],v18 offset:256
v_cvt_pk_bf16_f32 v119,v14,v15
v_mfma_f32_16x16x32_bf16 v[168:171],a[148:151],a[236:239],v[168:171]
v_accvgpr_read_b32 v74,a20
v_accvgpr_read_b32 v75,a21
v_mfma_f32_16x16x32_bf16 v[168:171],a[144:147],a[232:235],v[168:171]
ds_read_b128 v[56:59],v18 offset:512
v_accvgpr_read_b32 v90,a52
v_mfma_f32_16x16x32_bf16 v[176:179],a[144:147],a[240:243],v[176:179]
v_accvgpr_read_b32 v91,a53
v_accvgpr_read_b32 v106,a84
v_mfma_f32_16x16x32_bf16 v[176:179],a[148:151],a[244:247],v[176:179]
buffer_load_dwordx4 v5,s[24:27],s47 offen offset:1024 nt lds
v_mfma_f32_16x16x32_bf16 v[184:187],a[148:151],a[252:255],v[184:187]
ds_read_b128 v[60:63],v18 offset:768
v_accvgpr_read_b32 v107,a85
v_mfma_f32_16x16x32_bf16 v[184:187],a[144:147],a[248:251],v[184:187]
v_accvgpr_read_b32 v16,a22
v_accvgpr_read_b32 v17,a23
v_mfma_f32_16x16x32_bf16 v[164:167],a[152:155],a[224:227],v[164:167]
v_mul_f32 v74,s61,v74
v_mul_f32 v75,s61,v75
v_mfma_f32_16x16x32_bf16 v[164:167],a[156:159],a[228:231],v[164:167]
v_accvgpr_read_b32 v12,a54
v_accvgpr_read_b32 v13,a55
v_mfma_f32_16x16x32_bf16 v[172:175],a[156:159],a[236:239],v[172:175]
v_mul_f32 v90,s61,v90
v_mul_f32 v91,s61,v91
v_mfma_f32_16x16x32_bf16 v[172:175],a[152:155],a[232:235],v[172:175]
v_accvgpr_read_b32 v14,a86
v_accvgpr_read_b32 v15,a87
v_mfma_f32_16x16x32_bf16 v[180:183],a[152:155],a[240:243],v[180:183]
v_mul_f32 v106,s61,v106
v_mul_f32 v107,s61,v107
v_mfma_f32_16x16x32_bf16 v[180:183],a[156:159],a[244:247],v[180:183]
v_mul_f32 v16,s61,v16
v_mul_f32 v17,s61,v17
v_mfma_f32_16x16x32_bf16 v[188:191],a[156:159],a[252:255],v[188:191]
v_cvt_pk_bf16_f32 v74,v74,v75
v_mul_f32 v12,s61,v12
v_mfma_f32_16x16x32_bf16 v[188:191],a[152:155],a[248:251],v[188:191]
v_mul_f32 v13,s61,v13
v_cvt_pk_bf16_f32 v90,v90,v91
v_mfma_f32_16x16x32_bf16 v[192:195],a[160:163],a[224:227],v[192:195]
v_mul_f32 v14,s61,v14
v_mul_f32 v15,s61,v15
v_mfma_f32_16x16x32_bf16 v[192:195],a[164:167],a[228:231],v[192:195]
v_cvt_pk_bf16_f32 v106,v106,v107
v_cvt_pk_bf16_f32 v75,v16,v17
v_mfma_f32_16x16x32_bf16 v[200:203],a[164:167],a[236:239],v[200:203]
v_cvt_pk_bf16_f32 v91,v12,v13
v_cvt_pk_bf16_f32 v107,v14,v15
v_mfma_f32_16x16x32_bf16 v[200:203],a[160:163],a[232:235],v[200:203]
v_accvgpr_read_b32 v76,a24
v_accvgpr_read_b32 v77,a25
v_accvgpr_read_b32 v78,a26
v_accvgpr_read_b32 v79,a27
v_accvgpr_read_b32 v12,a28
v_accvgpr_read_b32 v13,a29
v_accvgpr_read_b32 v14,a30
v_accvgpr_read_b32 v15,a31
v_pk_mul_f32 v[76:77],s[60:61],v[76:77] op_sel:[1,0] op_sel_hi:[1,1]
v_pk_mul_f32 v[78:79],s[60:61],v[78:79] op_sel:[1,0] op_sel_hi:[1,1]
v_pk_mul_f32 v[12:13],s[60:61],v[12:13] op_sel:[1,0] op_sel_hi:[1,1]
v_pk_mul_f32 v[14:15],s[60:61],v[14:15] op_sel:[1,0] op_sel_hi:[1,1]
v_cvt_pk_bf16_f32 v76,v76,v77
v_cvt_pk_bf16_f32 v77,v78,v79
v_cvt_pk_bf16_f32 v78,v12,v13
v_cvt_pk_bf16_f32 v79,v14,v15
v_mfma_f32_16x16x32_bf16 v[208:211],a[160:163],a[240:243],v[208:211]
v_accvgpr_read_b32 v92,a56
v_accvgpr_read_b32 v93,a57
v_accvgpr_read_b32 v94,a58
v_accvgpr_read_b32 v95,a59
v_accvgpr_read_b32 v12,a60
v_accvgpr_read_b32 v13,a61
v_accvgpr_read_b32 v14,a62
v_accvgpr_read_b32 v15,a63
v_pk_mul_f32 v[92:93],s[60:61],v[92:93] op_sel:[1,0] op_sel_hi:[1,1]
v_pk_mul_f32 v[94:95],s[60:61],v[94:95] op_sel:[1,0] op_sel_hi:[1,1]
v_pk_mul_f32 v[12:13],s[60:61],v[12:13] op_sel:[1,0] op_sel_hi:[1,1]
v_pk_mul_f32 v[14:15],s[60:61],v[14:15] op_sel:[1,0] op_sel_hi:[1,1]
v_cvt_pk_bf16_f32 v92,v92,v93
v_cvt_pk_bf16_f32 v93,v94,v95
v_cvt_pk_bf16_f32 v94,v12,v13
v_cvt_pk_bf16_f32 v95,v14,v15
v_mfma_f32_16x16x32_bf16 v[208:211],a[164:167],a[244:247],v[208:211]
buffer_load_dwordx4 v4,s[24:27],s47 offen offset:2048 nt lds
v_mfma_f32_16x16x32_bf16 v[216:219],a[164:167],a[252:255],v[216:219]
ds_read_b128 a[128:131],v1
v_accvgpr_read_b32 v120,a112
v_mfma_f32_16x16x32_bf16 v[216:219],a[160:163],a[248:251],v[216:219]
v_accvgpr_read_b32 v121,a113
v_mul_f32 v120,s61,v120
v_mfma_f32_16x16x32_bf16 v[196:199],a[168:171],a[224:227],v[196:199]
v_mul_f32 v121,s61,v121
v_accvgpr_read_b32 v122,a114
v_mfma_f32_16x16x32_bf16 v[196:199],a[172:175],a[228:231],v[196:199]
ds_read_b128 a[132:135],v1 offset:256
v_accvgpr_read_b32 v123,a115
v_mfma_f32_16x16x32_bf16 v[204:207],a[172:175],a[236:239],v[204:207]
v_accvgpr_read_b32 v108,a88
v_accvgpr_read_b32 v109,a89
v_accvgpr_read_b32 v110,a90
v_accvgpr_read_b32 v111,a91
v_accvgpr_read_b32 v12,a92
v_accvgpr_read_b32 v13,a93
v_accvgpr_read_b32 v14,a94
v_accvgpr_read_b32 v15,a95
v_pk_mul_f32 v[108:109],s[60:61],v[108:109] op_sel:[1,0] op_sel_hi:[1,1]
v_pk_mul_f32 v[110:111],s[60:61],v[110:111] op_sel:[1,0] op_sel_hi:[1,1]
v_pk_mul_f32 v[12:13],s[60:61],v[12:13] op_sel:[1,0] op_sel_hi:[1,1]
v_pk_mul_f32 v[14:15],s[60:61],v[14:15] op_sel:[1,0] op_sel_hi:[1,1]
v_cvt_pk_bf16_f32 v108,v108,v109
v_cvt_pk_bf16_f32 v109,v110,v111
v_cvt_pk_bf16_f32 v110,v12,v13
v_cvt_pk_bf16_f32 v111,v14,v15
v_mfma_f32_16x16x32_bf16 v[204:207],a[168:171],a[232:235],v[204:207]
v_cvt_pk_bf16_f32 v120,v120,v121
v_mul_f32 v122,s61,v122
v_mfma_f32_16x16x32_bf16 v[212:215],a[168:171],a[240:243],v[212:215]
v_mul_f32 v123,s61,v123
v_cvt_pk_bf16_f32 v121,v122,v123
v_mfma_f32_16x16x32_bf16 v[212:215],a[172:175],a[244:247],v[212:215]
ds_read_b128 a[136:139],v1 offset:512
v_accvgpr_read_b32 v122,a116
v_mfma_f32_16x16x32_bf16 v[220:223],a[172:175],a[252:255],v[220:223]
v_accvgpr_read_b32 v123,a117
v_accvgpr_read_b32 v16,a118
v_mfma_f32_16x16x32_bf16 v[220:223],a[168:171],a[248:251],v[220:223]
v_accvgpr_read_b32 v17,a119
v_mul_f32 v122,s61,v122
v_mfma_f32_16x16x32_bf16 v[224:227],a[176:179],a[224:227],v[224:227]
ds_read_b128 a[140:143],v1 offset:768
v_mul_f32 v123,s61,v123
v_mfma_f32_16x16x32_bf16 v[224:227],a[180:183],a[228:231],v[224:227]
)A"
R"A(v_mul_f32 v16,s61,v16
v_mul_f32 v17,s61,v17
v_mfma_f32_16x16x32_bf16 v[232:235],a[180:183],a[236:239],v[232:235]
v_cvt_pk_bf16_f32 v122,v122,v123
v_cvt_pk_bf16_f32 v123,v16,v17
v_mfma_f32_16x16x32_bf16 v[232:235],a[176:179],a[232:235],v[232:235]
ds_read_b128 a[156:159],v1 offset:4864
v_mfma_f32_16x16x32_bf16 v[240:243],a[176:179],a[240:243],v[240:243]
v_accvgpr_read_b32 v124,a120
v_accvgpr_read_b32 v125,a121
v_accvgpr_read_b32 v126,a122
v_accvgpr_read_b32 v127,a123
v_accvgpr_read_b32 v12,a124
v_accvgpr_read_b32 v13,a125
v_accvgpr_read_b32 v14,a126
v_accvgpr_read_b32 v15,a127
v_pk_mul_f32 v[124:125],s[60:61],v[124:125] op_sel:[1,0] op_sel_hi:[1,1]
v_pk_mul_f32 v[126:127],s[60:61],v[126:127] op_sel:[1,0] op_sel_hi:[1,1]
v_pk_mul_f32 v[12:13],s[60:61],v[12:13] op_sel:[1,0] op_sel_hi:[1,1]
v_pk_mul_f32 v[14:15],s[60:61],v[14:15] op_sel:[1,0] op_sel_hi:[1,1]
v_cvt_pk_bf16_f32 v124,v124,v125
v_cvt_pk_bf16_f32 v125,v126,v127
v_cvt_pk_bf16_f32 v126,v12,v13
v_cvt_pk_bf16_f32 v127,v14,v15
v_mfma_f32_16x16x32_bf16 v[240:243],a[180:183],a[244:247],v[240:243]
buffer_load_dwordx4 v5,s[24:27],s47 offen offset:3072 nt lds
v_mfma_f32_16x16x32_bf16 v[248:251],a[180:183],a[252:255],v[248:251]
s_add_u32 s47,s47,16384
s_and_b32 s47,s47,0x3ffff
ds_read_b128 a[152:155],v1 offset:4608
v_mfma_f32_16x16x32_bf16 v[248:251],a[176:179],a[248:251],v[248:251]
s_or_b32 s47,s47,s46
v_mul_f32 v128,s61,v128
v_mul_f32 v129,s61,v129
v_mul_f32 v130,s61,v130
v_mul_f32 v131,s61,v131
v_cvt_pk_bf16_f32 v128,v128,v129
v_cvt_pk_bf16_f32 v129,v130,v131
v_pk_mul_f32 v[132:133],s[60:61],v[132:133] op_sel:[1,0] op_sel_hi:[1,1]
v_pk_mul_f32 v[134:135],s[60:61],v[134:135] op_sel:[1,0] op_sel_hi:[1,1]
v_pk_mul_f32 v[136:137],s[60:61],v[136:137] op_sel:[1,0] op_sel_hi:[1,1]
v_pk_mul_f32 v[138:139],s[60:61],v[138:139] op_sel:[1,0] op_sel_hi:[1,1]
v_pk_mul_f32 v[140:141],s[60:61],v[140:141] op_sel:[1,0] op_sel_hi:[1,1]
v_pk_mul_f32 v[142:143],s[60:61],v[142:143] op_sel:[1,0] op_sel_hi:[1,1]
v_pk_mul_f32 v[144:145],s[60:61],v[144:145] op_sel:[1,0] op_sel_hi:[1,1]
v_pk_mul_f32 v[146:147],s[60:61],v[146:147] op_sel:[1,0] op_sel_hi:[1,1]
v_pk_mul_f32 v[148:149],s[60:61],v[148:149] op_sel:[1,0] op_sel_hi:[1,1]
v_pk_mul_f32 v[150:151],s[60:61],v[150:151] op_sel:[1,0] op_sel_hi:[1,1]
v_pk_mul_f32 v[152:153],s[60:61],v[152:153] op_sel:[1,0] op_sel_hi:[1,1]
v_pk_mul_f32 v[154:155],s[60:61],v[154:155] op_sel:[1,0] op_sel_hi:[1,1]
v_pk_mul_f32 v[156:157],s[60:61],v[156:157] op_sel:[1,0] op_sel_hi:[1,1]
v_pk_mul_f32 v[158:159],s[60:61],v[158:159] op_sel:[1,0] op_sel_hi:[1,1]
v_mfma_f32_16x16x32_bf16 v[228:231],a[184:187],a[224:227],v[228:231]
s_and_b32 s46,s54,0xfffc0000
ds_read_b128 a[148:151],v1 offset:4352
v_cvt_pk_bf16_f32 v130,v132,v133
v_mfma_f32_16x16x32_bf16 v[228:231],a[188:191],a[228:231],v[228:231]
s_add_u32 s54,s54,16384
v_cvt_pk_bf16_f32 v131,v134,v135
v_cvt_pk_bf16_f32 v136,v136,v137
v_mfma_f32_16x16x32_bf16 v[236:239],a[188:191],a[236:239],v[236:239]
ds_read_b128 a[144:147],v1 offset:4096
v_cvt_pk_bf16_f32 v137,v138,v139
v_mfma_f32_16x16x32_bf16 v[236:239],a[184:187],a[232:235],v[236:239]
v_cvt_pk_bf16_f32 v138,v140,v141
v_cvt_pk_bf16_f32 v139,v142,v143
v_mfma_f32_16x16x32_bf16 v[244:247],a[184:187],a[240:243],v[244:247]
ds_read_b128 a[160:163],v1 offset:8192
v_cvt_pk_bf16_f32 v144,v144,v145
v_mfma_f32_16x16x32_bf16 v[244:247],a[188:191],a[244:247],v[244:247]
v_cvt_pk_bf16_f32 v145,v146,v147
v_cvt_pk_bf16_f32 v146,v148,v149
v_mfma_f32_16x16x32_bf16 v[252:255],a[188:191],a[252:255],v[252:255]
ds_read_b128 a[164:167],v1 offset:8448
v_cvt_pk_bf16_f32 v147,v150,v151
v_mfma_f32_16x16x32_bf16 v[252:255],a[184:187],a[248:251],v[252:255]
s_waitcnt lgkmcnt(4)
v_cvt_pk_bf16_f32 v152,v152,v153
v_cvt_pk_bf16_f32 v153,v154,v155
v_mfma_f32_16x16x32_bf16 v[32:35],v[48:51],v[64:67],0
ds_read_b128 a[168:171],v1 offset:8704
v_cvt_pk_bf16_f32 v154,v156,v157
v_mfma_f32_16x16x32_bf16 v[32:35],v[52:55],v[68:71],v[32:35]
ds_read_b64_tr_b16 a[192:193],v2
v_cvt_pk_bf16_f32 v155,v158,v159
v_mfma_f32_16x16x32_bf16 v[32:35],v[56:59],v[72:75],v[32:35]
v_mul_f32 v160,s61,v160
v_mul_f32 v161,s61,v161
v_mul_f32 v162,s61,v162
v_mul_f32 v163,s61,v163
v_cvt_pk_bf16_f32 v160,v160,v161
v_cvt_pk_bf16_f32 v161,v162,v163
v_pk_mul_f32 v[164:165],s[60:61],v[164:165] op_sel:[1,0] op_sel_hi:[1,1]
v_pk_mul_f32 v[166:167],s[60:61],v[166:167] op_sel:[1,0] op_sel_hi:[1,1]
v_pk_mul_f32 v[168:169],s[60:61],v[168:169] op_sel:[1,0] op_sel_hi:[1,1]
v_pk_mul_f32 v[170:171],s[60:61],v[170:171] op_sel:[1,0] op_sel_hi:[1,1]
v_pk_mul_f32 v[172:173],s[60:61],v[172:173] op_sel:[1,0] op_sel_hi:[1,1]
v_pk_mul_f32 v[174:175],s[60:61],v[174:175] op_sel:[1,0] op_sel_hi:[1,1]
v_pk_mul_f32 v[176:177],s[60:61],v[176:177] op_sel:[1,0] op_sel_hi:[1,1]
v_pk_mul_f32 v[178:179],s[60:61],v[178:179] op_sel:[1,0] op_sel_hi:[1,1]
v_pk_mul_f32 v[180:181],s[60:61],v[180:181] op_sel:[1,0] op_sel_hi:[1,1]
v_pk_mul_f32 v[182:183],s[60:61],v[182:183] op_sel:[1,0] op_sel_hi:[1,1]
v_pk_mul_f32 v[184:185],s[60:61],v[184:185] op_sel:[1,0] op_sel_hi:[1,1]
v_pk_mul_f32 v[186:187],s[60:61],v[186:187] op_sel:[1,0] op_sel_hi:[1,1]
v_pk_mul_f32 v[188:189],s[60:61],v[188:189] op_sel:[1,0] op_sel_hi:[1,1]
v_pk_mul_f32 v[190:191],s[60:61],v[190:191] op_sel:[1,0] op_sel_hi:[1,1]
v_mfma_f32_16x16x32_bf16 v[32:35],v[60:63],v[76:79],v[32:35]
ds_read_b128 a[172:175],v1 offset:8960
v_cvt_pk_bf16_f32 v162,v164,v165
v_mfma_f32_16x16x32_bf16 v[36:39],v[48:51],v[80:83],0
v_cvt_pk_bf16_f32 v163,v166,v167
v_cvt_pk_bf16_f32 v168,v168,v169
v_mfma_f32_16x16x32_bf16 v[36:39],v[52:55],v[84:87],v[36:39]
ds_read_b128 a[188:191],v1 offset:13056
v_cvt_pk_bf16_f32 v169,v170,v171
v_mfma_f32_16x16x32_bf16 v[36:39],v[56:59],v[88:91],v[36:39]
v_cvt_pk_bf16_f32 v170,v172,v173
v_cvt_pk_bf16_f32 v171,v174,v175
v_mfma_f32_16x16x32_bf16 v[36:39],v[60:63],v[92:95],v[36:39]
ds_read_b128 a[184:187],v1 offset:12800
v_cvt_pk_bf16_f32 v176,v176,v177
v_mfma_f32_16x16x32_bf16 v[40:43],v[48:51],v[96:99],0
v_cvt_pk_bf16_f32 v177,v178,v179
v_cvt_pk_bf16_f32 v178,v180,v181
v_mfma_f32_16x16x32_bf16 v[40:43],v[52:55],v[100:103],v[40:43]
s_add_u32 m0,0,s51
v_max3_f32 v32,v32,v33,v34
v_max3_f32 v32,v32,v35,s18
v_mfma_f32_16x16x32_bf16 v[40:43],v[56:59],v[104:107],v[40:43]
v_sub_f32 v35,0,v32
v_sub_f32 v34,0,v32
v_mfma_f32_16x16x32_bf16 v[40:43],v[60:63],v[108:111],v[40:43]
v_sub_f32 v33,0,v32
v_sub_f32 v32,0,v32
v_mfma_f32_16x16x32_bf16 v[44:47],v[48:51],v[112:115],0
buffer_load_dwordx4 v4,s[20:23],s47 offen nt lds
v_mfma_f32_16x16x32_bf16 v[44:47],v[52:55],v[116:119],v[44:47]
ds_read_b128 a[180:183],v1 offset:12544
v_max3_f32 v36,v36,v37,v38
v_mfma_f32_16x16x32_bf16 v[44:47],v[56:59],v[120:123],v[44:47]
v_max3_f32 v36,v36,v39,s19
v_sub_f32 v39,0,v36
v_mfma_f32_16x16x32_bf16 v[44:47],v[60:63],v[124:127],v[44:47]
ds_read_b128 a[176:179],v1 offset:12288
v_sub_f32 v38,0,v36
v_mfma_f32_16x16x32_bf16 v[132:135],a[128:131],v[64:67],v[32:35]
v_sub_f32 v37,0,v36
v_sub_f32 v36,0,v36
v_mfma_f32_16x16x32_bf16 v[132:135],a[132:135],v[68:71],v[132:135]
v_cvt_pk_bf16_f32 v179,v182,v183
v_cvt_pk_bf16_f32 v184,v184,v185
v_mfma_f32_16x16x32_bf16 v[132:135],a[136:139],v[72:75],v[132:135]
v_cvt_pk_bf16_f32 v185,v186,v187
v_cvt_pk_bf16_f32 v186,v188,v189
v_mfma_f32_16x16x32_bf16 v[132:135],a[140:143],v[76:79],v[132:135]
v_cvt_pk_bf16_f32 v187,v190,v191
v_max3_f32 v40,v40,v41,v42
v_mfma_f32_16x16x32_bf16 v[140:143],a[156:159],v[76:79],v[32:35]
v_max3_f32 v40,v40,v43,s100
v_sub_f32 v43,0,v40
v_mfma_f32_16x16x32_bf16 v[140:143],a[152:155],v[72:75],v[140:143]
s_waitcnt lgkmcnt(1)
buffer_load_dwordx4 v4,s[20:23],s47 offen offset:1024 nt lds
v_mfma_f32_16x16x32_bf16 v[140:143],a[148:151],v[68:71],v[140:143]
v_sub_f32 v42,0,v40
v_sub_f32 v41,0,v40
v_mfma_f32_16x16x32_bf16 v[140:143],a[144:147],v[64:67],v[140:143]
ds_read_b64_tr_b16 a[194:195],v2 offset:4096
v_sub_f32 v40,0,v40
v_mfma_f32_16x16x32_bf16 v[148:151],a[160:163],v[64:67],v[32:35]
v_mul_f32 v192,s61,v192
v_mul_f32 v193,s61,v193
v_mul_f32 v194,s61,v194
v_mul_f32 v195,s61,v195
v_cvt_pk_bf16_f32 v192,v192,v193
v_cvt_pk_bf16_f32 v193,v194,v195
v_pk_mul_f32 v[196:197],s[60:61],v[196:197] op_sel:[1,0] op_sel_hi:[1,1]
v_pk_mul_f32 v[198:199],s[60:61],v[198:199] op_sel:[1,0] op_sel_hi:[1,1]
v_pk_mul_f32 v[200:201],s[60:61],v[200:201] op_sel:[1,0] op_sel_hi:[1,1]
v_pk_mul_f32 v[202:203],s[60:61],v[202:203] op_sel:[1,0] op_sel_hi:[1,1]
v_pk_mul_f32 v[204:205],s[60:61],v[204:205] op_sel:[1,0] op_sel_hi:[1,1]
v_pk_mul_f32 v[206:207],s[60:61],v[206:207] op_sel:[1,0] op_sel_hi:[1,1]
v_pk_mul_f32 v[208:209],s[60:61],v[208:209] op_sel:[1,0] op_sel_hi:[1,1]
v_pk_mul_f32 v[210:211],s[60:61],v[210:211] op_sel:[1,0] op_sel_hi:[1,1]
v_pk_mul_f32 v[212:213],s[60:61],v[212:213] op_sel:[1,0] op_sel_hi:[1,1]
v_pk_mul_f32 v[214:215],s[60:61],v[214:215] op_sel:[1,0] op_sel_hi:[1,1]
v_pk_mul_f32 v[216:217],s[60:61],v[216:217] op_sel:[1,0] op_sel_hi:[1,1]
v_pk_mul_f32 v[218:219],s[60:61],v[218:219] op_sel:[1,0] op_sel_hi:[1,1]
v_pk_mul_f32 v[220:221],s[60:61],v[220:221] op_sel:[1,0] op_sel_hi:[1,1]
v_pk_mul_f32 v[222:223],s[60:61],v[222:223] op_sel:[1,0] op_sel_hi:[1,1]
v_mfma_f32_16x16x32_bf16 v[148:151],a[164:167],v[68:71],v[148:151]
v_cvt_pk_bf16_f32 v194,v196,v197
v_cvt_pk_bf16_f32 v195,v198,v199
v_mfma_f32_16x16x32_bf16 v[148:151],a[168:171],v[72:75],v[148:151]
v_cvt_pk_bf16_f32 v200,v200,v201
v_cvt_pk_bf16_f32 v201,v202,v203
v_mfma_f32_16x16x32_bf16 v[148:151],a[172:175],v[76:79],v[148:151]
v_cvt_pk_bf16_f32 v202,v204,v205
v_cvt_pk_bf16_f32 v203,v206,v207
v_mfma_f32_16x16x32_bf16 v[156:159],a[188:191],v[76:79],v[32:35]
v_cvt_pk_bf16_f32 v208,v208,v209
v_cvt_pk_bf16_f32 v209,v210,v211
v_mfma_f32_16x16x32_bf16 v[156:159],a[184:187],v[72:75],v[156:159]
v_cvt_pk_bf16_f32 v210,v212,v213
v_cvt_pk_bf16_f32 v211,v214,v215
v_mfma_f32_16x16x32_bf16 v[156:159],a[180:183],v[68:71],v[156:159]
s_waitcnt lgkmcnt(1)
buffer_load_dwordx4 v5,s[20:23],s47 offen offset:2048 nt lds
v_mfma_f32_16x16x32_bf16 v[156:159],a[176:179],v[64:67],v[156:159]
ds_read_b64_tr_b16 a[196:197],v2 offset:8192
ds_read_b64_tr_b16 a[198:199],v2 offset:12288
v_mfma_f32_16x16x32_bf16 v[164:167],a[128:131],v[80:83],v[36:39]
ds_read_b64_tr_b16 a[204:205],v3 offset:8192
v_cvt_pk_bf16_f32 v216,v216,v217
v_mfma_f32_16x16x32_bf16 v[164:167],a[132:135],v[84:87],v[164:167]
ds_read_b64_tr_b16 a[206:207],v3 offset:12288
v_cvt_pk_bf16_f32 v217,v218,v219
v_mfma_f32_16x16x32_bf16 v[164:167],a[136:139],v[88:91],v[164:167]
ds_read_b64_tr_b16 a[200:201],v3
v_cvt_pk_bf16_f32 v218,v220,v221
v_mfma_f32_16x16x32_bf16 v[164:167],a[140:143],v[92:95],v[164:167]
ds_read_b64_tr_b16 a[202:203],v3 offset:4096
v_cvt_pk_bf16_f32 v219,v222,v223
v_mfma_f32_16x16x32_bf16 v[172:175],a[156:159],v[92:95],v[36:39]
ds_read_b64_tr_b16 a[208:209],v2 offset:256
ds_read_b64_tr_b16 a[210:211],v2 offset:4352
v_mfma_f32_16x16x32_bf16 v[172:175],a[152:155],v[88:91],v[172:175]
v_exp_f32 v132,v132
v_mfma_f32_16x16x32_bf16 v[172:175],a[148:151],v[84:87],v[172:175]
v_exp_f32 v133,v133
v_mfma_f32_16x16x32_bf16 v[172:175],a[144:147],v[80:83],v[172:175]
buffer_load_dwordx4 v5,s[20:23],s47 offen offset:3072 nt lds
v_mfma_f32_16x16x32_bf16 v[180:183],a[160:163],v[80:83],v[36:39]
v_exp_f32 v134,v134
v_mfma_f32_16x16x32_bf16 v[180:183],a[164:167],v[84:87],v[180:183]
v_exp_f32 v135,v135
v_mfma_f32_16x16x32_bf16 v[180:183],a[168:171],v[88:91],v[180:183]
ds_read_b64_tr_b16 a[212:213],v2 offset:8448
ds_read_b64_tr_b16 a[214:215],v2 offset:12544
v_mfma_f32_16x16x32_bf16 v[180:183],a[172:175],v[92:95],v[180:183]
v_cvt_pk_bf16_f32 v132,v132,v133
v_cvt_pk_bf16_f32 v133,v134,v135
v_mfma_f32_16x16x32_bf16 v[188:191],a[188:191],v[92:95],v[36:39]
v_exp_f32 v140,v140
v_mfma_f32_16x16x32_bf16 v[188:191],a[184:187],v[88:91],v[188:191]
s_add_u32 m0,0,s52
v_exp_f32 v141,v141
v_mfma_f32_16x16x32_bf16 v[188:191],a[180:183],v[84:87],v[188:191]
v_exp_f32 v142,v142
v_mfma_f32_16x16x32_bf16 v[188:191],a[176:179],v[80:83],v[188:191]
v_exp_f32 v143,v143
v_mfma_f32_16x16x32_bf16 v[196:199],a[128:131],v[96:99],v[40:43]
buffer_load_dwordx4 v4,s[24:27],s47 offen nt lds
v_mfma_f32_16x16x32_bf16 v[196:199],a[132:135],v[100:103],v[196:199]
)A"
R"A(v_exp_f32 v148,v148
v_mfma_f32_16x16x32_bf16 v[196:199],a[136:139],v[104:107],v[196:199]
v_exp_f32 v149,v149
v_mfma_f32_16x16x32_bf16 v[196:199],a[140:143],v[108:111],v[196:199]
v_exp_f32 v150,v150
v_mfma_f32_16x16x32_bf16 v[204:207],a[156:159],v[108:111],v[40:43]
v_exp_f32 v151,v151
v_mfma_f32_16x16x32_bf16 v[204:207],a[152:155],v[104:107],v[204:207]
ds_read_b64_tr_b16 a[220:221],v3 offset:8448
ds_read_b64_tr_b16 a[222:223],v3 offset:12544
v_mfma_f32_16x16x32_bf16 v[204:207],a[148:151],v[100:103],v[204:207]
v_cvt_pk_bf16_f32 v134,v140,v141
v_cvt_pk_bf16_f32 v135,v142,v143
v_mfma_f32_16x16x32_bf16 v[204:207],a[144:147],v[96:99],v[204:207]
v_cvt_pk_bf16_f32 v148,v148,v149
v_cvt_pk_bf16_f32 v149,v150,v151
v_mfma_f32_16x16x32_bf16 v[212:215],a[160:163],v[96:99],v[40:43]
v_exp_f32 v156,v156
v_mfma_f32_16x16x32_bf16 v[212:215],a[164:167],v[100:103],v[212:215]
buffer_load_dwordx4 v5,s[24:27],s47 offen offset:1024 nt lds
v_mfma_f32_16x16x32_bf16 v[212:215],a[168:171],v[104:107],v[212:215]
v_exp_f32 v157,v157
v_mfma_f32_16x16x32_bf16 v[212:215],a[172:175],v[108:111],v[212:215]
v_exp_f32 v158,v158
v_mfma_f32_16x16x32_bf16 v[220:223],a[188:191],v[108:111],v[40:43]
v_exp_f32 v159,v159
v_mfma_f32_16x16x32_bf16 v[220:223],a[184:187],v[104:107],v[220:223]
ds_read_b64_tr_b16 a[216:217],v3 offset:256
ds_read_b64_tr_b16 a[218:219],v3 offset:4352
v_mfma_f32_16x16x32_bf16 v[220:223],a[180:183],v[100:103],v[220:223]
v_cvt_pk_bf16_f32 v150,v156,v157
v_cvt_pk_bf16_f32 v151,v158,v159
v_mfma_f32_16x16x32_bf16 v[220:223],a[176:179],v[96:99],v[220:223]
s_waitcnt lgkmcnt(2)
ds_read_b64_tr_b16 a[224:225],v2 offset:512
ds_read_b64_tr_b16 a[226:227],v2 offset:4608
v_mfma_f32_16x16x32_bf16 a[0:3],a[192:195],v[132:135],0
ds_read_b64_tr_b16 a[228:229],v2 offset:8704
ds_read_b64_tr_b16 a[230:231],v2 offset:12800
v_mfma_f32_16x16x32_bf16 a[0:3],a[196:199],v[148:151],a[0:3]
ds_read_b64_tr_b16 a[236:237],v3 offset:8704
ds_read_b64_tr_b16 a[238:239],v3 offset:12800
v_mfma_f32_16x16x32_bf16 a[4:7],a[204:207],v[148:151],0
ds_read_b64_tr_b16 a[232:233],v3 offset:512
ds_read_b64_tr_b16 a[234:235],v3 offset:4608
v_mfma_f32_16x16x32_bf16 a[4:7],a[200:203],v[132:135],a[4:7]
ds_read_b64_tr_b16 a[240:241],v2 offset:768
ds_read_b64_tr_b16 a[242:243],v2 offset:4864
v_mfma_f32_16x16x32_bf16 a[8:11],a[208:211],v[132:135],0
ds_read_b64_tr_b16 a[244:245],v2 offset:8960
ds_read_b64_tr_b16 a[246:247],v2 offset:13056
v_mfma_f32_16x16x32_bf16 a[8:11],a[212:215],v[148:151],a[8:11]
s_memrealtime s[76:77]
buffer_load_dwordx4 v4,s[24:27],s47 offen offset:2048 nt lds
ds_read_b64_tr_b16 a[252:253],v3 offset:8960
ds_read_b64_tr_b16 a[254:255],v3 offset:13056
v_mfma_f32_16x16x32_bf16 a[12:15],a[220:223],v[148:151],0
s_waitcnt lgkmcnt(14)
ds_read_b64_tr_b16 a[248:249],v3 offset:768
ds_read_b64_tr_b16 a[250:251],v3 offset:4864
v_mfma_f32_16x16x32_bf16 a[12:15],a[216:219],v[132:135],a[12:15]
s_waitcnt lgkmcnt(14)
v_mfma_f32_16x16x32_bf16 a[16:19],a[224:227],v[132:135],0
s_waitcnt lgkmcnt(12)
v_mfma_f32_16x16x32_bf16 a[16:19],a[228:231],v[148:151],a[16:19]
s_waitcnt lgkmcnt(10)
v_mfma_f32_16x16x32_bf16 a[20:23],a[236:239],v[148:151],0
s_waitcnt lgkmcnt(8)
v_mfma_f32_16x16x32_bf16 a[20:23],a[232:235],v[132:135],a[20:23]
s_waitcnt lgkmcnt(0)
v_max3_f32 v44,v44,v45,v46
v_max3_f32 v44,v44,v47,s101
v_mfma_f32_16x16x32_bf16 a[24:27],a[240:243],v[132:135],0
s_add_u32 s78,s76,s60
buffer_load_dwordx4 v5,s[24:27],s47 offen offset:3072 nt lds
v_mfma_f32_16x16x32_bf16 a[24:27],a[244:247],v[148:151],a[24:27]
s_add_u32 s47,s47,16384
v_sub_f32 v47,0,v44
v_sub_f32 v46,0,v44
v_mfma_f32_16x16x32_bf16 a[28:31],a[252:255],v[148:151],0
s_and_b32 s47,s47,0x3ffff
v_sub_f32 v45,0,v44
v_sub_f32 v44,0,v44
v_mfma_f32_16x16x32_bf16 a[28:31],a[248:251],v[132:135],a[28:31]
v_mul_f32 v224,s61,v224
v_mul_f32 v225,s61,v225
v_mul_f32 v226,s61,v226
v_mul_f32 v227,s61,v227
v_cvt_pk_bf16_f32 v224,v224,v225
v_cvt_pk_bf16_f32 v225,v226,v227
v_pk_mul_f32 v[228:229],s[60:61],v[228:229] op_sel:[1,0] op_sel_hi:[1,1]
v_pk_mul_f32 v[230:231],s[60:61],v[230:231] op_sel:[1,0] op_sel_hi:[1,1]
v_pk_mul_f32 v[232:233],s[60:61],v[232:233] op_sel:[1,0] op_sel_hi:[1,1]
v_pk_mul_f32 v[234:235],s[60:61],v[234:235] op_sel:[1,0] op_sel_hi:[1,1]
v_pk_mul_f32 v[236:237],s[60:61],v[236:237] op_sel:[1,0] op_sel_hi:[1,1]
v_pk_mul_f32 v[238:239],s[60:61],v[238:239] op_sel:[1,0] op_sel_hi:[1,1]
v_pk_mul_f32 v[240:241],s[60:61],v[240:241] op_sel:[1,0] op_sel_hi:[1,1]
v_pk_mul_f32 v[242:243],s[60:61],v[242:243] op_sel:[1,0] op_sel_hi:[1,1]
v_pk_mul_f32 v[244:245],s[60:61],v[244:245] op_sel:[1,0] op_sel_hi:[1,1]
v_pk_mul_f32 v[246:247],s[60:61],v[246:247] op_sel:[1,0] op_sel_hi:[1,1]
v_pk_mul_f32 v[248:249],s[60:61],v[248:249] op_sel:[1,0] op_sel_hi:[1,1]
v_pk_mul_f32 v[250:251],s[60:61],v[250:251] op_sel:[1,0] op_sel_hi:[1,1]
v_pk_mul_f32 v[252:253],s[60:61],v[252:253] op_sel:[1,0] op_sel_hi:[1,1]
v_pk_mul_f32 v[254:255],s[60:61],v[254:255] op_sel:[1,0] op_sel_hi:[1,1]
v_mfma_f32_16x16x32_bf16 v[48:51],v[28:31],v[132:135],0
s_or_b32 s47,s47,s46
v_cvt_pk_bf16_f32 v226,v228,v229
v_cvt_pk_bf16_f32 v227,v230,v231
v_mfma_f32_16x16x32_bf16 v[48:51],v[28:31],v[148:151],v[48:51]
s_and_b32 s46,s54,0xfffc0000
v_cvt_pk_bf16_f32 v232,v232,v233
v_cvt_pk_bf16_f32 v233,v234,v235
v_mfma_f32_16x16x32_bf16 v[228:231],a[128:131],v[112:115],v[44:47]
s_waitcnt vmcnt(8)
v_cvt_pk_bf16_f32 v234,v236,v237
v_cvt_pk_bf16_f32 v235,v238,v239
v_mfma_f32_16x16x32_bf16 v[228:231],a[132:135],v[116:119],v[228:231]
s_barrier
s_add_u32 m0,s49,s51
v_cvt_pk_bf16_f32 v240,v240,v241
v_mfma_f32_16x16x32_bf16 v[228:231],a[136:139],v[120:123],v[228:231]
v_cvt_pk_bf16_f32 v241,v242,v243
v_cvt_pk_bf16_f32 v242,v244,v245
v_mfma_f32_16x16x32_bf16 v[228:231],a[140:143],v[124:127],v[228:231]
v_cvt_pk_bf16_f32 v243,v246,v247
v_cvt_pk_bf16_f32 v248,v248,v249
v_mfma_f32_16x16x32_bf16 v[236:239],a[156:159],v[124:127],v[44:47]
buffer_load_dwordx4 v4,s[20:23],s47 offen nt lds
v_mfma_f32_16x16x32_bf16 v[236:239],a[152:155],v[120:123],v[236:239]
v_cvt_pk_bf16_f32 v249,v250,v251
v_cvt_pk_bf16_f32 v250,v252,v253
v_mfma_f32_16x16x32_bf16 v[236:239],a[148:151],v[116:119],v[236:239]
v_cvt_pk_bf16_f32 v251,v254,v255
v_add_u32 v1,s48,v1
v_mfma_f32_16x16x32_bf16 v[236:239],a[144:147],v[112:115],v[236:239]
v_exp_f32 v164,v164
v_mfma_f32_16x16x32_bf16 v[244:247],a[160:163],v[112:115],v[44:47]
v_exp_f32 v165,v165
v_mfma_f32_16x16x32_bf16 v[244:247],a[164:167],v[116:119],v[244:247]
v_exp_f32 v166,v166
v_mfma_f32_16x16x32_bf16 v[244:247],a[168:171],v[120:123],v[244:247]
v_exp_f32 v167,v167
v_mfma_f32_16x16x32_bf16 v[244:247],a[172:175],v[124:127],v[244:247]
v_cvt_pk_bf16_f32 v164,v164,v165
v_cvt_pk_bf16_f32 v165,v166,v167
v_mfma_f32_16x16x32_bf16 v[252:255],a[188:191],v[124:127],v[44:47]
v_exp_f32 v172,v172
v_exp_f32 v173,v173
v_exp_f32 v174,v174
v_exp_f32 v175,v175
v_exp_f32 v180,v180
v_exp_f32 v181,v181
v_mfma_f32_16x16x32_bf16 v[252:255],a[184:187],v[120:123],v[252:255]
v_exp_f32 v182,v182
v_exp_f32 v183,v183
v_cvt_pk_bf16_f32 v166,v172,v173
v_cvt_pk_bf16_f32 v167,v174,v175
v_cvt_pk_bf16_f32 v180,v180,v181
v_exp_f32 v188,v188
v_exp_f32 v189,v189
v_exp_f32 v190,v190
v_exp_f32 v191,v191
v_mfma_f32_16x16x32_bf16 v[252:255],a[180:183],v[116:119],v[252:255]
v_cvt_pk_bf16_f32 v181,v182,v183
v_cvt_pk_bf16_f32 v182,v188,v189
v_cvt_pk_bf16_f32 v183,v190,v191
v_mfma_f32_16x16x32_bf16 v[252:255],a[176:179],v[112:115],v[252:255]
ds_read_b128 a[128:131],v1
v_add_u32 v2,s48,v2
v_mfma_f32_16x16x32_bf16 a[32:35],a[192:195],v[164:167],0
v_exp_f32 v196,v196
v_mfma_f32_16x16x32_bf16 a[32:35],a[196:199],v[180:183],a[32:35]
ds_read_b128 a[132:135],v1 offset:256
v_add_u32 v3,s48,v3
v_mfma_f32_16x16x32_bf16 a[36:39],a[204:207],v[180:183],0
s_sub_u32 s48,32768,s62
buffer_load_dwordx4 v4,s[20:23],s47 offen offset:1024 nt lds
v_mfma_f32_16x16x32_bf16 a[36:39],a[200:203],v[164:167],a[36:39]
s_add_u32 s50,s50,s48
ds_read_b128 a[136:139],v1 offset:512
v_mfma_f32_16x16x32_bf16 a[40:43],a[208:211],v[164:167],0
v_exp_f32 v197,v197
v_mfma_f32_16x16x32_bf16 a[40:43],a[212:215],v[180:183],a[40:43]
ds_read_b128 a[140:143],v1 offset:768
v_cvt_pk_bf16_f32 v196,v196,v197
v_mfma_f32_16x16x32_bf16 a[44:47],a[220:223],v[180:183],0
v_exp_f32 v198,v198
v_mfma_f32_16x16x32_bf16 a[44:47],a[216:219],v[164:167],a[44:47]
ds_read_b128 a[156:159],v1 offset:4864
v_mfma_f32_16x16x32_bf16 a[48:51],a[224:227],v[164:167],0
v_exp_f32 v199,v199
v_mfma_f32_16x16x32_bf16 a[48:51],a[228:231],v[180:183],a[48:51]
ds_read_b128 a[152:155],v1 offset:4608
v_cvt_pk_bf16_f32 v197,v198,v199
v_mfma_f32_16x16x32_bf16 a[52:55],a[236:239],v[180:183],0
v_exp_f32 v204,v204
v_mfma_f32_16x16x32_bf16 a[52:55],a[232:235],v[164:167],a[52:55]
ds_read_b128 a[148:151],v1 offset:4352
v_mfma_f32_16x16x32_bf16 a[56:59],a[240:243],v[164:167],0
v_exp_f32 v205,v205
v_mfma_f32_16x16x32_bf16 a[56:59],a[244:247],v[180:183],a[56:59]
ds_read_b128 a[144:147],v1 offset:4096
v_cvt_pk_bf16_f32 v198,v204,v205
v_mfma_f32_16x16x32_bf16 a[60:63],a[252:255],v[180:183],0
v_exp_f32 v206,v206
v_mfma_f32_16x16x32_bf16 a[60:63],a[248:251],v[164:167],a[60:63]
ds_read_b128 a[160:163],v1 offset:8192
v_mfma_f32_16x16x32_bf16 v[52:55],v[28:31],v[164:167],0
buffer_load_dwordx4 v5,s[20:23],s47 offen offset:2048 nt lds
v_mfma_f32_16x16x32_bf16 v[52:55],v[28:31],v[180:183],v[52:55]
s_waitcnt lgkmcnt(3)
ds_read_b128 a[164:167],v1 offset:8448
v_mfma_f32_16x16x32_bf16 v[132:135],a[128:131],v[64:67],v[32:35]
v_exp_f32 v207,v207
v_mfma_f32_16x16x32_bf16 v[132:135],a[132:135],v[68:71],v[132:135]
ds_read_b128 a[168:171],v1 offset:8704
v_cvt_pk_bf16_f32 v199,v206,v207
v_mfma_f32_16x16x32_bf16 v[132:135],a[136:139],v[72:75],v[132:135]
ds_read_b128 a[172:175],v1 offset:8960
v_mfma_f32_16x16x32_bf16 v[132:135],a[140:143],v[76:79],v[132:135]
ds_read_b128 a[188:191],v1 offset:13056
v_mfma_f32_16x16x32_bf16 v[140:143],a[156:159],v[76:79],v[32:35]
ds_read_b128 a[184:187],v1 offset:12800
v_mfma_f32_16x16x32_bf16 v[140:143],a[152:155],v[72:75],v[140:143]
s_waitcnt lgkmcnt(5)
ds_read_b128 a[180:183],v1 offset:12544
v_mfma_f32_16x16x32_bf16 v[140:143],a[148:151],v[68:71],v[140:143]
ds_read_b128 a[176:179],v1 offset:12288
v_mfma_f32_16x16x32_bf16 v[140:143],a[144:147],v[64:67],v[140:143]
v_mfma_f32_16x16x32_bf16 v[148:151],a[160:163],v[64:67],v[32:35]
s_waitcnt lgkmcnt(6)
v_mfma_f32_16x16x32_bf16 v[148:151],a[164:167],v[68:71],v[148:151]
s_waitcnt lgkmcnt(5)
v_exp_f32 v212,v212
v_mfma_f32_16x16x32_bf16 v[148:151],a[168:171],v[72:75],v[148:151]
s_waitcnt lgkmcnt(4)
v_exp_f32 v213,v213
v_mfma_f32_16x16x32_bf16 v[148:151],a[172:175],v[76:79],v[148:151]
s_waitcnt lgkmcnt(3)
v_exp_f32 v214,v214
v_mfma_f32_16x16x32_bf16 v[156:159],a[188:191],v[76:79],v[32:35]
s_waitcnt lgkmcnt(2)
v_exp_f32 v215,v215
v_mfma_f32_16x16x32_bf16 v[156:159],a[184:187],v[72:75],v[156:159]
s_waitcnt lgkmcnt(1)
v_cvt_pk_bf16_f32 v212,v212,v213
v_exp_f32 v220,v220
v_exp_f32 v221,v221
v_exp_f32 v222,v222
v_exp_f32 v223,v223
v_mfma_f32_16x16x32_bf16 v[156:159],a[180:183],v[68:71],v[156:159]
s_waitcnt lgkmcnt(0)
v_cvt_pk_bf16_f32 v213,v214,v215
v_cvt_pk_bf16_f32 v214,v220,v221
v_cvt_pk_bf16_f32 v215,v222,v223
v_mfma_f32_16x16x32_bf16 v[156:159],a[176:179],v[64:67],v[156:159]
buffer_load_dwordx4 v5,s[20:23],s47 offen offset:3072 nt lds
v_mfma_f32_16x16x32_bf16 a[64:67],a[192:195],v[196:199],0
v_exp_f32 v228,v228
v_mfma_f32_16x16x32_bf16 a[64:67],a[196:199],v[212:215],a[64:67]
v_exp_f32 v229,v229
v_mfma_f32_16x16x32_bf16 a[68:71],a[204:207],v[212:215],0
v_exp_f32 v230,v230
v_mfma_f32_16x16x32_bf16 a[68:71],a[200:203],v[196:199],a[68:71]
v_exp_f32 v231,v231
v_mfma_f32_16x16x32_bf16 a[72:75],a[208:211],v[196:199],0
v_cvt_pk_bf16_f32 v228,v228,v229
v_cvt_pk_bf16_f32 v229,v230,v231
v_mfma_f32_16x16x32_bf16 a[72:75],a[212:215],v[212:215],a[72:75]
v_exp_f32 v236,v236
v_mfma_f32_16x16x32_bf16 a[76:79],a[220:223],v[212:215],0
v_exp_f32 v237,v237
v_mfma_f32_16x16x32_bf16 a[76:79],a[216:219],v[196:199],a[76:79]
s_add_u32 m0,s49,s52
v_exp_f32 v238,v238
v_mfma_f32_16x16x32_bf16 a[80:83],a[224:227],v[196:199],0
s_add_u32 s49,s49,32768
v_exp_f32 v239,v239
)A"
R"A(v_mfma_f32_16x16x32_bf16 a[80:83],a[228:231],v[212:215],a[80:83]
v_exp_f32 v244,v244
v_mfma_f32_16x16x32_bf16 a[84:87],a[236:239],v[212:215],0
buffer_load_dwordx4 v4,s[24:27],s47 offen nt lds
v_mfma_f32_16x16x32_bf16 a[84:87],a[232:235],v[196:199],a[84:87]
v_exp_f32 v245,v245
v_mfma_f32_16x16x32_bf16 a[88:91],a[240:243],v[196:199],0
v_exp_f32 v246,v246
v_mfma_f32_16x16x32_bf16 a[88:91],a[244:247],v[212:215],a[88:91]
v_exp_f32 v247,v247
v_mfma_f32_16x16x32_bf16 a[92:95],a[252:255],v[212:215],0
v_cvt_pk_bf16_f32 v230,v236,v237
v_cvt_pk_bf16_f32 v231,v238,v239
v_mfma_f32_16x16x32_bf16 a[92:95],a[248:251],v[196:199],a[92:95]
v_cvt_pk_bf16_f32 v244,v244,v245
v_cvt_pk_bf16_f32 v245,v246,v247
v_mfma_f32_16x16x32_bf16 v[56:59],v[28:31],v[196:199],0
v_exp_f32 v252,v252
v_mfma_f32_16x16x32_bf16 v[56:59],v[28:31],v[212:215],v[56:59]
v_exp_f32 v253,v253
v_mfma_f32_16x16x32_bf16 v[164:167],a[128:131],v[80:83],v[36:39]
v_exp_f32 v254,v254
v_mfma_f32_16x16x32_bf16 v[164:167],a[132:135],v[84:87],v[164:167]
v_exp_f32 v255,v255
v_mfma_f32_16x16x32_bf16 v[164:167],a[136:139],v[88:91],v[164:167]
v_cvt_pk_bf16_f32 v246,v252,v253
v_cvt_pk_bf16_f32 v247,v254,v255
v_mfma_f32_16x16x32_bf16 v[164:167],a[140:143],v[92:95],v[164:167]
v_exp_f32 v132,v132
v_mfma_f32_16x16x32_bf16 v[172:175],a[156:159],v[92:95],v[36:39]
v_exp_f32 v133,v133
v_mfma_f32_16x16x32_bf16 v[172:175],a[152:155],v[88:91],v[172:175]
v_exp_f32 v134,v134
v_mfma_f32_16x16x32_bf16 v[172:175],a[148:151],v[84:87],v[172:175]
buffer_load_dwordx4 v5,s[24:27],s47 offen offset:1024 nt lds
v_mfma_f32_16x16x32_bf16 v[172:175],a[144:147],v[80:83],v[172:175]
v_exp_f32 v135,v135
v_mfma_f32_16x16x32_bf16 v[180:183],a[160:163],v[80:83],v[36:39]
v_cvt_pk_bf16_f32 v132,v132,v133
v_cvt_pk_bf16_f32 v133,v134,v135
v_mfma_f32_16x16x32_bf16 v[180:183],a[164:167],v[84:87],v[180:183]
v_exp_f32 v140,v140
v_mfma_f32_16x16x32_bf16 v[180:183],a[168:171],v[88:91],v[180:183]
v_exp_f32 v141,v141
v_mfma_f32_16x16x32_bf16 v[180:183],a[172:175],v[92:95],v[180:183]
v_exp_f32 v142,v142
v_mfma_f32_16x16x32_bf16 v[188:191],a[188:191],v[92:95],v[36:39]
v_exp_f32 v143,v143
v_mfma_f32_16x16x32_bf16 v[188:191],a[184:187],v[88:91],v[188:191]
v_exp_f32 v148,v148
v_mfma_f32_16x16x32_bf16 v[188:191],a[180:183],v[84:87],v[188:191]
v_exp_f32 v149,v149
v_mfma_f32_16x16x32_bf16 v[188:191],a[176:179],v[80:83],v[188:191]
v_exp_f32 v150,v150
v_mfma_f32_16x16x32_bf16 a[96:99],a[192:195],v[228:231],0
ds_read_b64_tr_b16 a[192:193],v2
ds_read_b64_tr_b16 a[194:195],v2 offset:4096
v_mfma_f32_16x16x32_bf16 a[96:99],a[196:199],v[244:247],a[96:99]
ds_read_b64_tr_b16 a[196:197],v2 offset:8192
v_cvt_pk_bf16_f32 v134,v140,v141
v_mfma_f32_16x16x32_bf16 a[100:103],a[204:207],v[244:247],0
ds_read_b64_tr_b16 a[198:199],v2 offset:12288
v_cvt_pk_bf16_f32 v135,v142,v143
v_mfma_f32_16x16x32_bf16 a[100:103],a[200:203],v[228:231],a[100:103]
ds_read_b64_tr_b16 a[204:205],v3 offset:8192
v_cvt_pk_bf16_f32 v148,v148,v149
v_mfma_f32_16x16x32_bf16 a[104:107],a[208:211],v[228:231],0
buffer_load_dwordx4 v4,s[24:27],s47 offen offset:2048 nt lds
v_mfma_f32_16x16x32_bf16 a[104:107],a[212:215],v[244:247],a[104:107]
ds_read_b64_tr_b16 a[206:207],v3 offset:12288
ds_read_b64_tr_b16 a[200:201],v3
v_mfma_f32_16x16x32_bf16 a[108:111],a[220:223],v[244:247],0
ds_read_b64_tr_b16 a[202:203],v3 offset:4096
v_add_u32 v1,s48,v1
v_mfma_f32_16x16x32_bf16 a[108:111],a[216:219],v[228:231],a[108:111]
s_cmp_eq_u32 s49,163840
s_cselect_b32 s49,0,s49
ds_read_b64_tr_b16 a[208:209],v2 offset:256
v_mfma_f32_16x16x32_bf16 a[112:115],a[224:227],v[228:231],0
ds_read_b64_tr_b16 a[210:211],v2 offset:4352
v_mfma_f32_16x16x32_bf16 a[112:115],a[228:231],v[244:247],a[112:115]
ds_read_b64_tr_b16 a[212:213],v2 offset:8448
ds_read_b64_tr_b16 a[214:215],v2 offset:12544
v_mfma_f32_16x16x32_bf16 a[116:119],a[236:239],v[244:247],0
v_exp_f32 v151,v151
v_mfma_f32_16x16x32_bf16 a[116:119],a[232:235],v[228:231],a[116:119]
v_exp_f32 v156,v156
v_mfma_f32_16x16x32_bf16 a[120:123],a[240:243],v[228:231],0
v_exp_f32 v157,v157
v_mfma_f32_16x16x32_bf16 a[120:123],a[244:247],v[244:247],a[120:123]
v_exp_f32 v158,v158
v_mfma_f32_16x16x32_bf16 a[124:127],a[252:255],v[244:247],0
v_exp_f32 v159,v159
v_mfma_f32_16x16x32_bf16 a[124:127],a[248:251],v[228:231],a[124:127]
ds_read_b64_tr_b16 a[220:221],v3 offset:8448
ds_read_b64_tr_b16 a[222:223],v3 offset:12544
v_mfma_f32_16x16x32_bf16 v[60:63],v[28:31],v[228:231],0
v_cvt_pk_bf16_f32 v149,v150,v151
v_cvt_pk_bf16_f32 v150,v156,v157
v_mfma_f32_16x16x32_bf16 v[60:63],v[28:31],v[244:247],v[60:63]
ds_read_b64_tr_b16 a[216:217],v3 offset:256
v_cvt_pk_bf16_f32 v151,v158,v159
v_mfma_f32_16x16x32_bf16 v[196:199],a[128:131],v[96:99],v[40:43]
buffer_load_dwordx4 v5,s[24:27],s47 offen offset:3072 nt lds
v_mfma_f32_16x16x32_bf16 v[196:199],a[132:135],v[100:103],v[196:199]
s_add_u32 s47,s47,16384
ds_read_b64_tr_b16 a[218:219],v3 offset:4352
ds_read_b64_tr_b16 a[224:225],v2 offset:512
v_mfma_f32_16x16x32_bf16 v[196:199],a[136:139],v[104:107],v[196:199]
s_and_b32 s47,s47,0x3ffff
s_or_b32 s47,s47,s46
ds_read_b64_tr_b16 a[226:227],v2 offset:4608
v_mfma_f32_16x16x32_bf16 v[196:199],a[140:143],v[108:111],v[196:199]
ds_read_b64_tr_b16 a[228:229],v2 offset:8704
v_mfma_f32_16x16x32_bf16 v[204:207],a[156:159],v[108:111],v[40:43]
ds_read_b64_tr_b16 a[230:231],v2 offset:12800
v_mfma_f32_16x16x32_bf16 v[204:207],a[152:155],v[104:107],v[204:207]
ds_read_b64_tr_b16 a[236:237],v3 offset:8704
ds_read_b64_tr_b16 a[238:239],v3 offset:12800
v_mfma_f32_16x16x32_bf16 v[204:207],a[148:151],v[100:103],v[204:207]
ds_read_b64_tr_b16 a[232:233],v3 offset:512
v_mfma_f32_16x16x32_bf16 v[204:207],a[144:147],v[96:99],v[204:207]
ds_read_b64_tr_b16 a[234:235],v3 offset:4608
v_mfma_f32_16x16x32_bf16 v[212:215],a[160:163],v[96:99],v[40:43]
ds_read_b64_tr_b16 a[240:241],v2 offset:768
v_mfma_f32_16x16x32_bf16 v[212:215],a[164:167],v[100:103],v[212:215]
ds_read_b64_tr_b16 a[242:243],v2 offset:4864
ds_read_b64_tr_b16 a[244:245],v2 offset:8960
v_mfma_f32_16x16x32_bf16 v[212:215],a[168:171],v[104:107],v[212:215]
ds_read_b64_tr_b16 a[246:247],v2 offset:13056
v_mfma_f32_16x16x32_bf16 v[212:215],a[172:175],v[108:111],v[212:215]
ds_read_b64_tr_b16 a[252:253],v3 offset:8960
v_mfma_f32_16x16x32_bf16 v[220:223],a[188:191],v[108:111],v[40:43]
ds_read_b64_tr_b16 a[254:255],v3 offset:13056
v_mfma_f32_16x16x32_bf16 v[220:223],a[184:187],v[104:107],v[220:223]
ds_read_b64_tr_b16 a[248:249],v3 offset:768
ds_read_b64_tr_b16 a[250:251],v3 offset:4864
v_mfma_f32_16x16x32_bf16 v[220:223],a[180:183],v[100:103],v[220:223]
v_exp_f32 v164,v164
v_mfma_f32_16x16x32_bf16 v[220:223],a[176:179],v[96:99],v[220:223]
s_waitcnt lgkmcnt(8)
v_exp_f32 v165,v165
v_mfma_f32_16x16x32_bf16 a[0:3],a[192:195],v[132:135],a[0:3]
v_exp_f32 v180,v180
v_mfma_f32_16x16x32_bf16 a[0:3],a[196:199],v[148:151],a[0:3]
v_exp_f32 v181,v181
v_mfma_f32_16x16x32_bf16 a[4:7],a[204:207],v[148:151],a[4:7]
v_exp_f32 v166,v166
v_mfma_f32_16x16x32_bf16 a[4:7],a[200:203],v[132:135],a[4:7]
v_exp_f32 v167,v167
v_mfma_f32_16x16x32_bf16 a[8:11],a[208:211],v[132:135],a[8:11]
s_memrealtime s[76:77]
v_exp_f32 v182,v182
v_mfma_f32_16x16x32_bf16 a[8:11],a[212:215],v[148:151],a[8:11]
v_exp_f32 v183,v183
v_mfma_f32_16x16x32_bf16 a[12:15],a[220:223],v[148:151],a[12:15]
v_cvt_pk_bf16_f32 v164,v164,v165
v_cvt_pk_bf16_f32 v180,v180,v181
v_mfma_f32_16x16x32_bf16 a[12:15],a[216:219],v[132:135],a[12:15]
v_exp_f32 v172,v172
v_mfma_f32_16x16x32_bf16 a[16:19],a[224:227],v[132:135],a[16:19]
v_exp_f32 v173,v173
v_mfma_f32_16x16x32_bf16 a[16:19],a[228:231],v[148:151],a[16:19]
v_exp_f32 v174,v174
v_mfma_f32_16x16x32_bf16 a[20:23],a[236:239],v[148:151],a[20:23]
v_exp_f32 v175,v175
v_mfma_f32_16x16x32_bf16 a[20:23],a[232:235],v[132:135],a[20:23]
s_waitcnt lgkmcnt(0)
v_exp_f32 v188,v188
v_mfma_f32_16x16x32_bf16 a[24:27],a[240:243],v[132:135],a[24:27]
s_sub_u32 s79,s78,s76
v_exp_f32 v189,v189
v_mfma_f32_16x16x32_bf16 a[24:27],a[244:247],v[148:151],a[24:27]
s_add_u32 s78,s76,s60
v_exp_f32 v190,v190
v_mfma_f32_16x16x32_bf16 a[28:31],a[252:255],v[148:151],a[28:31]
s_cmp_gt_i32 s79,0
s_cbranch_scc1 L_ps2_%=
L_pr2_%=:
v_cvt_pk_bf16_f32 v165,v166,v167
v_mfma_f32_16x16x32_bf16 a[28:31],a[248:251],v[132:135],a[28:31]
v_exp_f32 v191,v191
v_mfma_f32_16x16x32_bf16 v[48:51],v[28:31],v[132:135],v[48:51]
v_cvt_pk_bf16_f32 v181,v182,v183
v_cvt_pk_bf16_f32 v166,v172,v173
v_cvt_pk_bf16_f32 v167,v174,v175
v_cvt_pk_bf16_f32 v182,v188,v189
v_cvt_pk_bf16_f32 v183,v190,v191
v_mfma_f32_16x16x32_bf16 v[48:51],v[28:31],v[148:151],v[48:51]
s_mov_b32 s53,13
L_att0_%=:
v_mfma_f32_16x16x32_bf16 v[228:231],a[128:131],v[112:115],v[44:47]
s_waitcnt vmcnt(8) lgkmcnt(0)
v_exp_f32 v196,v196
v_mfma_f32_16x16x32_bf16 v[228:231],a[132:135],v[116:119],v[228:231]
s_barrier
s_add_u32 m0,s49,s51
ds_read_b128 a[128:131],v1
v_mfma_f32_16x16x32_bf16 v[228:231],a[136:139],v[120:123],v[228:231]
s_mov_b32 s90,L_endseg_%=-L_pc_%=+128
v_exp_f32 v197,v197
v_mfma_f32_16x16x32_bf16 v[228:231],a[140:143],v[124:127],v[228:231]
s_add_u32 s54,s54,16384
ds_read_b128 a[132:135],v1 offset:256
v_cvt_pk_bf16_f32 v196,v196,v197
v_mfma_f32_16x16x32_bf16 v[236:239],a[156:159],v[124:127],v[44:47]
s_sub_u32 s53,s53,1
buffer_load_dwordx4 v4,s[20:23],s47 offen nt lds
v_mfma_f32_16x16x32_bf16 v[236:239],a[152:155],v[120:123],v[236:239]
s_and_b32 s46,s54,0xfffc0000
ds_read_b128 a[136:139],v1 offset:512
v_add_u32 v2,s48,v2
v_mfma_f32_16x16x32_bf16 v[236:239],a[148:151],v[116:119],v[236:239]
v_exp_f32 v198,v198
v_mfma_f32_16x16x32_bf16 v[236:239],a[144:147],v[112:115],v[236:239]
ds_read_b128 a[140:143],v1 offset:768
v_add_u32 v3,s48,v3
v_mfma_f32_16x16x32_bf16 v[244:247],a[160:163],v[112:115],v[44:47]
v_exp_f32 v199,v199
v_mfma_f32_16x16x32_bf16 v[244:247],a[164:167],v[116:119],v[244:247]
ds_read_b128 a[156:159],v1 offset:4864
v_cvt_pk_bf16_f32 v197,v198,v199
v_mfma_f32_16x16x32_bf16 v[244:247],a[168:171],v[120:123],v[244:247]
v_exp_f32 v204,v204
v_mfma_f32_16x16x32_bf16 v[244:247],a[172:175],v[124:127],v[244:247]
s_cmp_eq_u32 s50,131072
s_cselect_b32 s62,163840,0
ds_read_b128 a[152:155],v1 offset:4608
v_mfma_f32_16x16x32_bf16 v[252:255],a[188:191],v[124:127],v[44:47]
s_sub_u32 s48,32768,s62
v_exp_f32 v205,v205
v_mfma_f32_16x16x32_bf16 v[252:255],a[184:187],v[120:123],v[252:255]
s_add_u32 s50,s50,s48
ds_read_b128 a[148:151],v1 offset:4352
v_cvt_pk_bf16_f32 v198,v204,v205
v_mfma_f32_16x16x32_bf16 v[252:255],a[180:183],v[116:119],v[252:255]
v_exp_f32 v206,v206
v_mfma_f32_16x16x32_bf16 v[252:255],a[176:179],v[112:115],v[252:255]
ds_read_b128 a[144:147],v1 offset:4096
v_mfma_f32_16x16x32_bf16 a[32:35],a[192:195],v[164:167],a[32:35]
v_exp_f32 v207,v207
v_mfma_f32_16x16x32_bf16 a[32:35],a[196:199],v[180:183],a[32:35]
ds_read_b128 a[160:163],v1 offset:8192
v_cvt_pk_bf16_f32 v199,v206,v207
v_mfma_f32_16x16x32_bf16 a[36:39],a[204:207],v[180:183],a[36:39]
buffer_load_dwordx4 v4,s[20:23],s47 offen offset:1024 nt lds
v_mfma_f32_16x16x32_bf16 a[36:39],a[200:203],v[164:167],a[36:39]
ds_read_b128 a[164:167],v1 offset:8448
v_mfma_f32_16x16x32_bf16 a[40:43],a[208:211],v[164:167],a[40:43]
v_exp_f32 v212,v212
v_mfma_f32_16x16x32_bf16 a[40:43],a[212:215],v[180:183],a[40:43]
ds_read_b128 a[168:171],v1 offset:8704
v_mfma_f32_16x16x32_bf16 a[44:47],a[220:223],v[180:183],a[44:47]
v_exp_f32 v213,v213
v_mfma_f32_16x16x32_bf16 a[44:47],a[216:219],v[164:167],a[44:47]
ds_read_b128 a[172:175],v1 offset:8960
v_cvt_pk_bf16_f32 v212,v212,v213
v_mfma_f32_16x16x32_bf16 a[48:51],a[224:227],v[164:167],a[48:51]
v_exp_f32 v214,v214
v_mfma_f32_16x16x32_bf16 a[48:51],a[228:231],v[180:183],a[48:51]
ds_read_b128 a[188:191],v1 offset:13056
v_mfma_f32_16x16x32_bf16 a[52:55],a[236:239],v[180:183],a[52:55]
v_exp_f32 v215,v215
v_mfma_f32_16x16x32_bf16 a[52:55],a[232:235],v[164:167],a[52:55]
ds_read_b128 a[184:187],v1 offset:12800
v_cvt_pk_bf16_f32 v213,v214,v215
v_mfma_f32_16x16x32_bf16 a[56:59],a[240:243],v[164:167],a[56:59]
v_exp_f32 v220,v220
v_mfma_f32_16x16x32_bf16 a[56:59],a[244:247],v[180:183],a[56:59]
ds_read_b128 a[180:183],v1 offset:12544
v_mfma_f32_16x16x32_bf16 a[60:63],a[252:255],v[180:183],a[60:63]
)A"
R"A(v_exp_f32 v221,v221
v_mfma_f32_16x16x32_bf16 a[60:63],a[248:251],v[164:167],a[60:63]
ds_read_b128 a[176:179],v1 offset:12288
v_cvt_pk_bf16_f32 v214,v220,v221
v_mfma_f32_16x16x32_bf16 v[52:55],v[28:31],v[164:167],v[52:55]
buffer_load_dwordx4 v5,s[20:23],s47 offen offset:2048 nt lds
v_mfma_f32_16x16x32_bf16 v[52:55],v[28:31],v[180:183],v[52:55]
s_waitcnt lgkmcnt(3)
v_exp_f32 v222,v222
v_mfma_f32_16x16x32_bf16 v[132:135],a[128:131],v[64:67],v[32:35]
v_exp_f32 v223,v223
v_mfma_f32_16x16x32_bf16 v[132:135],a[132:135],v[68:71],v[132:135]
v_cvt_pk_bf16_f32 v215,v222,v223
v_add_u32 v1,s48,v1
v_mfma_f32_16x16x32_bf16 v[132:135],a[136:139],v[72:75],v[132:135]
v_exp_f32 v228,v228
v_mfma_f32_16x16x32_bf16 v[132:135],a[140:143],v[76:79],v[132:135]
v_exp_f32 v229,v229
v_mfma_f32_16x16x32_bf16 v[140:143],a[156:159],v[76:79],v[32:35]
v_exp_f32 v230,v230
v_mfma_f32_16x16x32_bf16 v[140:143],a[152:155],v[72:75],v[140:143]
v_exp_f32 v231,v231
v_mfma_f32_16x16x32_bf16 v[140:143],a[148:151],v[68:71],v[140:143]
v_cvt_pk_bf16_f32 v228,v228,v229
v_cvt_pk_bf16_f32 v229,v230,v231
v_mfma_f32_16x16x32_bf16 v[140:143],a[144:147],v[64:67],v[140:143]
v_exp_f32 v236,v236
v_mfma_f32_16x16x32_bf16 v[148:151],a[160:163],v[64:67],v[32:35]
v_exp_f32 v237,v237
v_mfma_f32_16x16x32_bf16 v[148:151],a[164:167],v[68:71],v[148:151]
v_exp_f32 v238,v238
v_mfma_f32_16x16x32_bf16 v[148:151],a[168:171],v[72:75],v[148:151]
v_exp_f32 v239,v239
v_mfma_f32_16x16x32_bf16 v[148:151],a[172:175],v[76:79],v[148:151]
v_exp_f32 v244,v244
v_mfma_f32_16x16x32_bf16 v[156:159],a[188:191],v[76:79],v[32:35]
s_waitcnt lgkmcnt(0)
buffer_load_dwordx4 v5,s[20:23],s47 offen offset:3072 nt lds
v_mfma_f32_16x16x32_bf16 v[156:159],a[184:187],v[72:75],v[156:159]
v_exp_f32 v245,v245
v_mfma_f32_16x16x32_bf16 v[156:159],a[180:183],v[68:71],v[156:159]
v_exp_f32 v246,v246
v_mfma_f32_16x16x32_bf16 v[156:159],a[176:179],v[64:67],v[156:159]
v_exp_f32 v247,v247
v_mfma_f32_16x16x32_bf16 a[64:67],a[192:195],v[196:199],a[64:67]
v_cvt_pk_bf16_f32 v230,v236,v237
v_cvt_pk_bf16_f32 v231,v238,v239
v_mfma_f32_16x16x32_bf16 a[64:67],a[196:199],v[212:215],a[64:67]
v_cvt_pk_bf16_f32 v244,v244,v245
v_cvt_pk_bf16_f32 v245,v246,v247
v_mfma_f32_16x16x32_bf16 a[68:71],a[204:207],v[212:215],a[68:71]
v_exp_f32 v252,v252
v_mfma_f32_16x16x32_bf16 a[68:71],a[200:203],v[196:199],a[68:71]
v_exp_f32 v253,v253
v_mfma_f32_16x16x32_bf16 a[72:75],a[208:211],v[196:199],a[72:75]
v_exp_f32 v254,v254
v_mfma_f32_16x16x32_bf16 a[72:75],a[212:215],v[212:215],a[72:75]
v_exp_f32 v255,v255
v_mfma_f32_16x16x32_bf16 a[76:79],a[220:223],v[212:215],a[76:79]
v_cvt_pk_bf16_f32 v246,v252,v253
v_cvt_pk_bf16_f32 v247,v254,v255
v_mfma_f32_16x16x32_bf16 a[76:79],a[216:219],v[196:199],a[76:79]
s_add_u32 m0,s49,s52
v_exp_f32 v132,v132
v_mfma_f32_16x16x32_bf16 a[80:83],a[224:227],v[196:199],a[80:83]
s_add_u32 s49,s49,32768
v_exp_f32 v133,v133
v_mfma_f32_16x16x32_bf16 a[80:83],a[228:231],v[212:215],a[80:83]
v_exp_f32 v134,v134
v_mfma_f32_16x16x32_bf16 a[84:87],a[236:239],v[212:215],a[84:87]
buffer_load_dwordx4 v4,s[24:27],s47 offen nt lds
v_mfma_f32_16x16x32_bf16 a[84:87],a[232:235],v[196:199],a[84:87]
v_exp_f32 v135,v135
v_mfma_f32_16x16x32_bf16 a[88:91],a[240:243],v[196:199],a[88:91]
v_cvt_pk_bf16_f32 v132,v132,v133
v_cvt_pk_bf16_f32 v133,v134,v135
v_mfma_f32_16x16x32_bf16 a[88:91],a[244:247],v[212:215],a[88:91]
v_exp_f32 v140,v140
v_mfma_f32_16x16x32_bf16 a[92:95],a[252:255],v[212:215],a[92:95]
v_exp_f32 v141,v141
v_mfma_f32_16x16x32_bf16 a[92:95],a[248:251],v[196:199],a[92:95]
v_exp_f32 v142,v142
v_mfma_f32_16x16x32_bf16 v[56:59],v[28:31],v[196:199],v[56:59]
v_exp_f32 v143,v143
v_mfma_f32_16x16x32_bf16 v[56:59],v[28:31],v[212:215],v[56:59]
v_exp_f32 v148,v148
v_mfma_f32_16x16x32_bf16 v[164:167],a[128:131],v[80:83],v[36:39]
v_exp_f32 v149,v149
v_mfma_f32_16x16x32_bf16 v[164:167],a[132:135],v[84:87],v[164:167]
v_exp_f32 v150,v150
v_mfma_f32_16x16x32_bf16 v[164:167],a[136:139],v[88:91],v[164:167]
v_exp_f32 v151,v151
v_mfma_f32_16x16x32_bf16 v[164:167],a[140:143],v[92:95],v[164:167]
v_cvt_pk_bf16_f32 v134,v140,v141
v_cvt_pk_bf16_f32 v135,v142,v143
v_mfma_f32_16x16x32_bf16 v[172:175],a[156:159],v[92:95],v[36:39]
v_cvt_pk_bf16_f32 v148,v148,v149
v_cvt_pk_bf16_f32 v149,v150,v151
v_mfma_f32_16x16x32_bf16 v[172:175],a[152:155],v[88:91],v[172:175]
v_exp_f32 v156,v156
v_mfma_f32_16x16x32_bf16 v[172:175],a[148:151],v[84:87],v[172:175]
buffer_load_dwordx4 v5,s[24:27],s47 offen offset:1024 nt lds
v_mfma_f32_16x16x32_bf16 v[172:175],a[144:147],v[80:83],v[172:175]
v_exp_f32 v157,v157
v_mfma_f32_16x16x32_bf16 v[180:183],a[160:163],v[80:83],v[36:39]
v_exp_f32 v158,v158
v_mfma_f32_16x16x32_bf16 v[180:183],a[164:167],v[84:87],v[180:183]
v_exp_f32 v159,v159
v_mfma_f32_16x16x32_bf16 v[180:183],a[168:171],v[88:91],v[180:183]
v_cvt_pk_bf16_f32 v150,v156,v157
v_cvt_pk_bf16_f32 v151,v158,v159
v_mfma_f32_16x16x32_bf16 v[180:183],a[172:175],v[92:95],v[180:183]
v_exp_f32 v164,v164
v_mfma_f32_16x16x32_bf16 v[188:191],a[188:191],v[92:95],v[36:39]
v_exp_f32 v165,v165
v_mfma_f32_16x16x32_bf16 v[188:191],a[184:187],v[88:91],v[188:191]
v_exp_f32 v166,v166
v_mfma_f32_16x16x32_bf16 v[188:191],a[180:183],v[84:87],v[188:191]
v_exp_f32 v167,v167
v_mfma_f32_16x16x32_bf16 v[188:191],a[176:179],v[80:83],v[188:191]
v_cvt_pk_bf16_f32 v164,v164,v165
v_cvt_pk_bf16_f32 v165,v166,v167
v_mfma_f32_16x16x32_bf16 a[96:99],a[192:195],v[228:231],a[96:99]
s_cmp_eq_u32 s49,163840
s_cselect_b32 s49,0,s49
ds_read_b64_tr_b16 a[192:193],v2
v_mfma_f32_16x16x32_bf16 a[96:99],a[196:199],v[244:247],a[96:99]
ds_read_b64_tr_b16 a[194:195],v2 offset:4096
v_mfma_f32_16x16x32_bf16 a[100:103],a[204:207],v[244:247],a[100:103]
ds_read_b64_tr_b16 a[196:197],v2 offset:8192
v_mfma_f32_16x16x32_bf16 a[100:103],a[200:203],v[228:231],a[100:103]
ds_read_b64_tr_b16 a[198:199],v2 offset:12288
v_mfma_f32_16x16x32_bf16 a[104:107],a[208:211],v[228:231],a[104:107]
buffer_load_dwordx4 v4,s[24:27],s47 offen offset:2048 nt lds
v_mfma_f32_16x16x32_bf16 a[104:107],a[212:215],v[244:247],a[104:107]
ds_read_b64_tr_b16 a[204:205],v3 offset:8192
v_mfma_f32_16x16x32_bf16 a[108:111],a[220:223],v[244:247],a[108:111]
ds_read_b64_tr_b16 a[206:207],v3 offset:12288
v_mfma_f32_16x16x32_bf16 a[108:111],a[216:219],v[228:231],a[108:111]
ds_read_b64_tr_b16 a[200:201],v3
v_mfma_f32_16x16x32_bf16 a[112:115],a[224:227],v[228:231],a[112:115]
ds_read_b64_tr_b16 a[202:203],v3 offset:4096
v_mfma_f32_16x16x32_bf16 a[112:115],a[228:231],v[244:247],a[112:115]
ds_read_b64_tr_b16 a[208:209],v2 offset:256
v_mfma_f32_16x16x32_bf16 a[116:119],a[236:239],v[244:247],a[116:119]
ds_read_b64_tr_b16 a[210:211],v2 offset:4352
v_mfma_f32_16x16x32_bf16 a[116:119],a[232:235],v[228:231],a[116:119]
ds_read_b64_tr_b16 a[212:213],v2 offset:8448
v_mfma_f32_16x16x32_bf16 a[120:123],a[240:243],v[228:231],a[120:123]
ds_read_b64_tr_b16 a[214:215],v2 offset:12544
v_mfma_f32_16x16x32_bf16 a[120:123],a[244:247],v[244:247],a[120:123]
ds_read_b64_tr_b16 a[220:221],v3 offset:8448
v_mfma_f32_16x16x32_bf16 a[124:127],a[252:255],v[244:247],a[124:127]
ds_read_b64_tr_b16 a[222:223],v3 offset:12544
v_mfma_f32_16x16x32_bf16 a[124:127],a[248:251],v[228:231],a[124:127]
ds_read_b64_tr_b16 a[216:217],v3 offset:256
v_mfma_f32_16x16x32_bf16 v[60:63],v[28:31],v[228:231],v[60:63]
ds_read_b64_tr_b16 a[218:219],v3 offset:4352
v_mfma_f32_16x16x32_bf16 v[60:63],v[28:31],v[244:247],v[60:63]
ds_read_b64_tr_b16 a[224:225],v2 offset:512
v_mfma_f32_16x16x32_bf16 v[196:199],a[128:131],v[96:99],v[40:43]
buffer_load_dwordx4 v5,s[24:27],s47 offen offset:3072 nt lds
v_mfma_f32_16x16x32_bf16 v[196:199],a[132:135],v[100:103],v[196:199]
s_add_u32 s47,s47,16384
s_and_b32 s47,s47,0x3ffff
ds_read_b64_tr_b16 a[226:227],v2 offset:4608
v_mfma_f32_16x16x32_bf16 v[196:199],a[136:139],v[104:107],v[196:199]
s_or_b32 s47,s47,s46
ds_read_b64_tr_b16 a[228:229],v2 offset:8704
v_mfma_f32_16x16x32_bf16 v[196:199],a[140:143],v[108:111],v[196:199]
ds_read_b64_tr_b16 a[230:231],v2 offset:12800
v_mfma_f32_16x16x32_bf16 v[204:207],a[156:159],v[108:111],v[40:43]
ds_read_b64_tr_b16 a[236:237],v3 offset:8704
v_mfma_f32_16x16x32_bf16 v[204:207],a[152:155],v[104:107],v[204:207]
ds_read_b64_tr_b16 a[238:239],v3 offset:12800
v_mfma_f32_16x16x32_bf16 v[204:207],a[148:151],v[100:103],v[204:207]
ds_read_b64_tr_b16 a[232:233],v3 offset:512
v_mfma_f32_16x16x32_bf16 v[204:207],a[144:147],v[96:99],v[204:207]
ds_read_b64_tr_b16 a[234:235],v3 offset:4608
v_mfma_f32_16x16x32_bf16 v[212:215],a[160:163],v[96:99],v[40:43]
ds_read_b64_tr_b16 a[240:241],v2 offset:768
v_mfma_f32_16x16x32_bf16 v[212:215],a[164:167],v[100:103],v[212:215]
ds_read_b64_tr_b16 a[242:243],v2 offset:4864
v_mfma_f32_16x16x32_bf16 v[212:215],a[168:171],v[104:107],v[212:215]
ds_read_b64_tr_b16 a[244:245],v2 offset:8960
v_mfma_f32_16x16x32_bf16 v[212:215],a[172:175],v[108:111],v[212:215]
ds_read_b64_tr_b16 a[246:247],v2 offset:13056
v_mfma_f32_16x16x32_bf16 v[220:223],a[188:191],v[108:111],v[40:43]
ds_read_b64_tr_b16 a[252:253],v3 offset:8960
v_mfma_f32_16x16x32_bf16 v[220:223],a[184:187],v[104:107],v[220:223]
ds_read_b64_tr_b16 a[254:255],v3 offset:13056
v_mfma_f32_16x16x32_bf16 v[220:223],a[180:183],v[100:103],v[220:223]
ds_read_b64_tr_b16 a[248:249],v3 offset:768
v_mfma_f32_16x16x32_bf16 v[220:223],a[176:179],v[96:99],v[220:223]
s_waitcnt lgkmcnt(7)
ds_read_b64_tr_b16 a[250:251],v3 offset:4864
v_mfma_f32_16x16x32_bf16 a[0:3],a[192:195],v[132:135],a[0:3]
buffer_load_dword v26,v24,s[88:91],s93 offen
v_mfma_f32_16x16x32_bf16 a[0:3],a[196:199],v[148:151],a[0:3]
v_exp_f32 v180,v180
v_mfma_f32_16x16x32_bf16 a[4:7],a[204:207],v[148:151],a[4:7]
v_exp_f32 v181,v181
v_mfma_f32_16x16x32_bf16 a[4:7],a[200:203],v[132:135],a[4:7]
v_exp_f32 v182,v182
v_mfma_f32_16x16x32_bf16 a[8:11],a[208:211],v[132:135],a[8:11]
s_memrealtime s[76:77]
v_exp_f32 v183,v183
v_mfma_f32_16x16x32_bf16 a[8:11],a[212:215],v[148:151],a[8:11]
v_exp_f32 v172,v172
v_mfma_f32_16x16x32_bf16 a[12:15],a[220:223],v[148:151],a[12:15]
v_exp_f32 v173,v173
v_mfma_f32_16x16x32_bf16 a[12:15],a[216:219],v[132:135],a[12:15]
v_exp_f32 v174,v174
v_mfma_f32_16x16x32_bf16 a[16:19],a[224:227],v[132:135],a[16:19]
v_exp_f32 v175,v175
v_mfma_f32_16x16x32_bf16 a[16:19],a[228:231],v[148:151],a[16:19]
v_cvt_pk_bf16_f32 v180,v180,v181
v_cvt_pk_bf16_f32 v181,v182,v183
v_mfma_f32_16x16x32_bf16 a[20:23],a[236:239],v[148:151],a[20:23]
v_exp_f32 v188,v188
v_mfma_f32_16x16x32_bf16 a[20:23],a[232:235],v[132:135],a[20:23]
s_waitcnt lgkmcnt(0)
v_exp_f32 v189,v189
v_mfma_f32_16x16x32_bf16 a[24:27],a[240:243],v[132:135],a[24:27]
s_sub_u32 s79,s78,s76
v_exp_f32 v190,v190
v_mfma_f32_16x16x32_bf16 a[24:27],a[244:247],v[148:151],a[24:27]
s_add_u32 s78,s76,s60
v_exp_f32 v191,v191
v_mfma_f32_16x16x32_bf16 a[28:31],a[252:255],v[148:151],a[28:31]
s_cmp_gt_i32 s79,0
s_cbranch_scc1 L_ps3_%=
L_pr3_%=:
v_cvt_pk_bf16_f32 v166,v172,v173
v_mfma_f32_16x16x32_bf16 a[28:31],a[248:251],v[132:135],a[28:31]
v_cvt_pk_bf16_f32 v167,v174,v175
v_cvt_pk_bf16_f32 v182,v188,v189
v_mfma_f32_16x16x32_bf16 v[48:51],v[28:31],v[132:135],v[48:51]
v_cvt_pk_bf16_f32 v183,v190,v191
s_cmp_lg_u32 s53,0
v_mfma_f32_16x16x32_bf16 v[48:51],v[28:31],v[148:151],v[48:51]
s_cbranch_scc1 L_att0_%=
L_att0x_%=:
v_mfma_f32_16x16x32_bf16 v[228:231],a[128:131],v[112:115],v[44:47]
s_waitcnt vmcnt(10)
s_add_u32 s54,s54,16384
v_exp_f32 v196,v196
v_mfma_f32_16x16x32_bf16 v[228:231],a[132:135],v[116:119],v[228:231]
s_barrier
s_add_u32 m0,s49,s51
ds_read_b128 a[128:131],v1
v_mfma_f32_16x16x32_bf16 v[228:231],a[136:139],v[120:123],v[228:231]
s_and_b32 s46,s54,0xfffc0000
ds_read_b128 a[132:135],v1 offset:256
v_and_b32 v18,48,v6
v_mfma_f32_16x16x32_bf16 v[228:231],a[140:143],v[124:127],v[228:231]
s_add_u32 s54,s54,16384
ds_read_b128 a[136:139],v1 offset:512
v_lshrrev_b32 v19,4,v6
v_mfma_f32_16x16x32_bf16 v[236:239],a[156:159],v[124:127],v[44:47]
s_add_u32 s84,s55,0
buffer_load_dwordx4 v4,s[20:23],s47 offen nt lds
v_mfma_f32_16x16x32_bf16 v[236:239],a[152:155],v[120:123],v[236:239]
s_add_u32 s85,s55,4096
ds_read_b128 a[140:143],v1 offset:768
v_lshlrev_b32 v18,2,v18
v_mfma_f32_16x16x32_bf16 v[236:239],a[148:151],v[116:119],v[236:239]
)A"
R"A(s_add_u32 s86,s55,8192
ds_read_b128 a[156:159],v1 offset:4864
v_and_b32 v19,48,v19
v_mfma_f32_16x16x32_bf16 v[236:239],a[144:147],v[112:115],v[236:239]
s_add_u32 s87,s55,12288
ds_read_b128 a[152:155],v1 offset:4608
v_add_u32 v18,v18,v19
v_mfma_f32_16x16x32_bf16 v[244:247],a[160:163],v[112:115],v[44:47]
s_max_i32 s84,s84,s18
ds_read_b128 a[148:151],v1 offset:4352
v_add_u32 v18,131072,v18
v_mfma_f32_16x16x32_bf16 v[244:247],a[164:167],v[116:119],v[244:247]
s_max_i32 s85,s85,s19
ds_read_b128 a[144:147],v1 offset:4096
v_add_u32 v2,s48,v2
v_mfma_f32_16x16x32_bf16 v[244:247],a[168:171],v[120:123],v[244:247]
s_max_i32 s86,s86,s100
ds_read_b128 a[160:163],v1 offset:8192
v_add_u32 v3,s48,v3
v_mfma_f32_16x16x32_bf16 v[244:247],a[172:175],v[124:127],v[244:247]
s_cmp_eq_u32 s50,131072
s_cselect_b32 s62,163840,0
ds_read_b128 a[164:167],v1 offset:8448
v_mfma_f32_16x16x32_bf16 v[252:255],a[188:191],v[124:127],v[44:47]
s_sub_u32 s48,32768,s62
s_add_u32 s50,s50,s48
ds_read_b128 a[168:171],v1 offset:8704
v_mfma_f32_16x16x32_bf16 v[252:255],a[184:187],v[120:123],v[252:255]
s_cmp_eq_u32 s50,131072
s_cselect_b32 s62,163840,0
ds_read_b128 a[172:175],v1 offset:8960
v_mfma_f32_16x16x32_bf16 v[252:255],a[180:183],v[116:119],v[252:255]
s_max_i32 s87,s87,s101
s_add_u32 s93,s92,L_endseg_%=-L_pc_%=
ds_read_b128 a[188:191],v1 offset:13056
v_mfma_f32_16x16x32_bf16 v[252:255],a[176:179],v[112:115],v[252:255]
s_mov_b32 s80,0
s_mov_b32 s83,s55
ds_read_b128 a[184:187],v1 offset:12800
v_mfma_f32_16x16x32_bf16 a[32:35],a[192:195],v[164:167],a[32:35]
ds_read_b128 a[180:183],v1 offset:12544
v_mfma_f32_16x16x32_bf16 a[32:35],a[196:199],v[180:183],a[32:35]
ds_read_b128 a[176:179],v1 offset:12288
v_add_u32 v1,s48,v1
v_mfma_f32_16x16x32_bf16 a[36:39],a[204:207],v[180:183],a[36:39]
buffer_load_dwordx4 v4,s[20:23],s47 offen offset:1024 nt lds
v_mfma_f32_16x16x32_bf16 a[36:39],a[200:203],v[164:167],a[36:39]
v_exp_f32 v197,v197
v_mfma_f32_16x16x32_bf16 a[40:43],a[208:211],v[164:167],a[40:43]
v_exp_f32 v198,v198
v_mfma_f32_16x16x32_bf16 a[40:43],a[212:215],v[180:183],a[40:43]
v_exp_f32 v199,v199
v_mfma_f32_16x16x32_bf16 a[44:47],a[220:223],v[180:183],a[44:47]
v_cvt_pk_bf16_f32 v196,v196,v197
v_cvt_pk_bf16_f32 v197,v198,v199
v_mfma_f32_16x16x32_bf16 a[44:47],a[216:219],v[164:167],a[44:47]
v_exp_f32 v204,v204
v_mfma_f32_16x16x32_bf16 a[48:51],a[224:227],v[164:167],a[48:51]
v_exp_f32 v205,v205
v_mfma_f32_16x16x32_bf16 a[48:51],a[228:231],v[180:183],a[48:51]
v_exp_f32 v206,v206
v_mfma_f32_16x16x32_bf16 a[52:55],a[236:239],v[180:183],a[52:55]
v_exp_f32 v207,v207
v_mfma_f32_16x16x32_bf16 a[52:55],a[232:235],v[164:167],a[52:55]
v_exp_f32 v212,v212
v_mfma_f32_16x16x32_bf16 a[56:59],a[240:243],v[164:167],a[56:59]
v_exp_f32 v213,v213
v_mfma_f32_16x16x32_bf16 a[56:59],a[244:247],v[180:183],a[56:59]
v_exp_f32 v214,v214
v_mfma_f32_16x16x32_bf16 a[60:63],a[252:255],v[180:183],a[60:63]
v_exp_f32 v215,v215
v_mfma_f32_16x16x32_bf16 a[60:63],a[248:251],v[164:167],a[60:63]
v_cvt_pk_bf16_f32 v198,v204,v205
v_cvt_pk_bf16_f32 v199,v206,v207
v_mfma_f32_16x16x32_bf16 v[52:55],v[28:31],v[164:167],v[52:55]
buffer_load_dwordx4 v5,s[20:23],s47 offen offset:2048 nt lds
v_mfma_f32_16x16x32_bf16 v[52:55],v[28:31],v[180:183],v[52:55]
s_waitcnt lgkmcnt(0)
v_cvt_pk_bf16_f32 v212,v212,v213
v_cvt_pk_bf16_f32 v213,v214,v215
v_mfma_f32_16x16x32_bf16 v[132:135],a[128:131],v[64:67],v[32:35]
v_exp_f32 v220,v220
v_mfma_f32_16x16x32_bf16 v[132:135],a[132:135],v[68:71],v[132:135]
v_exp_f32 v221,v221
v_mfma_f32_16x16x32_bf16 v[132:135],a[136:139],v[72:75],v[132:135]
v_exp_f32 v222,v222
v_mfma_f32_16x16x32_bf16 v[132:135],a[140:143],v[76:79],v[132:135]
v_exp_f32 v223,v223
v_mfma_f32_16x16x32_bf16 v[140:143],a[156:159],v[76:79],v[32:35]
v_cvt_pk_bf16_f32 v214,v220,v221
v_cvt_pk_bf16_f32 v215,v222,v223
v_mfma_f32_16x16x32_bf16 v[140:143],a[152:155],v[72:75],v[140:143]
v_exp_f32 v228,v228
v_mfma_f32_16x16x32_bf16 v[140:143],a[148:151],v[68:71],v[140:143]
v_exp_f32 v229,v229
v_mfma_f32_16x16x32_bf16 v[140:143],a[144:147],v[64:67],v[140:143]
v_exp_f32 v230,v230
v_mfma_f32_16x16x32_bf16 v[148:151],a[160:163],v[64:67],v[32:35]
v_exp_f32 v231,v231
v_mfma_f32_16x16x32_bf16 v[148:151],a[164:167],v[68:71],v[148:151]
v_cvt_pk_bf16_f32 v228,v228,v229
v_cvt_pk_bf16_f32 v229,v230,v231
v_mfma_f32_16x16x32_bf16 v[148:151],a[168:171],v[72:75],v[148:151]
v_exp_f32 v236,v236
v_mfma_f32_16x16x32_bf16 v[148:151],a[172:175],v[76:79],v[148:151]
v_exp_f32 v237,v237
v_mfma_f32_16x16x32_bf16 v[156:159],a[188:191],v[76:79],v[32:35]
buffer_load_dwordx4 v5,s[20:23],s47 offen offset:3072 nt lds
v_mfma_f32_16x16x32_bf16 v[156:159],a[184:187],v[72:75],v[156:159]
v_exp_f32 v238,v238
v_mfma_f32_16x16x32_bf16 v[156:159],a[180:183],v[68:71],v[156:159]
v_exp_f32 v239,v239
v_mfma_f32_16x16x32_bf16 v[156:159],a[176:179],v[64:67],v[156:159]
v_exp_f32 v244,v244
v_mfma_f32_16x16x32_bf16 a[64:67],a[192:195],v[196:199],a[64:67]
v_exp_f32 v245,v245
v_mfma_f32_16x16x32_bf16 a[64:67],a[196:199],v[212:215],a[64:67]
v_exp_f32 v246,v246
v_mfma_f32_16x16x32_bf16 a[68:71],a[204:207],v[212:215],a[68:71]
v_exp_f32 v247,v247
v_mfma_f32_16x16x32_bf16 a[68:71],a[200:203],v[196:199],a[68:71]
v_cvt_pk_bf16_f32 v230,v236,v237
v_cvt_pk_bf16_f32 v231,v238,v239
v_mfma_f32_16x16x32_bf16 a[72:75],a[208:211],v[196:199],a[72:75]
v_cvt_pk_bf16_f32 v244,v244,v245
v_cvt_pk_bf16_f32 v245,v246,v247
v_mfma_f32_16x16x32_bf16 a[72:75],a[212:215],v[212:215],a[72:75]
v_exp_f32 v252,v252
v_mfma_f32_16x16x32_bf16 a[76:79],a[220:223],v[212:215],a[76:79]
v_exp_f32 v253,v253
v_mfma_f32_16x16x32_bf16 a[76:79],a[216:219],v[196:199],a[76:79]
s_add_u32 m0,s49,s52
v_exp_f32 v254,v254
v_mfma_f32_16x16x32_bf16 a[80:83],a[224:227],v[196:199],a[80:83]
s_add_u32 s49,s49,32768
v_exp_f32 v255,v255
v_mfma_f32_16x16x32_bf16 a[80:83],a[228:231],v[212:215],a[80:83]
v_cvt_pk_bf16_f32 v246,v252,v253
v_cvt_pk_bf16_f32 v247,v254,v255
v_mfma_f32_16x16x32_bf16 a[84:87],a[236:239],v[212:215],a[84:87]
buffer_load_dwordx4 v4,s[24:27],s47 offen nt lds
v_mfma_f32_16x16x32_bf16 a[84:87],a[232:235],v[196:199],a[84:87]
v_exp_f32 v132,v132
v_mfma_f32_16x16x32_bf16 a[88:91],a[240:243],v[196:199],a[88:91]
v_exp_f32 v133,v133
v_mfma_f32_16x16x32_bf16 a[88:91],a[244:247],v[212:215],a[88:91]
v_exp_f32 v134,v134
v_mfma_f32_16x16x32_bf16 a[92:95],a[252:255],v[212:215],a[92:95]
v_exp_f32 v135,v135
v_mfma_f32_16x16x32_bf16 a[92:95],a[248:251],v[196:199],a[92:95]
v_cvt_pk_bf16_f32 v132,v132,v133
v_cvt_pk_bf16_f32 v133,v134,v135
v_mfma_f32_16x16x32_bf16 v[56:59],v[28:31],v[196:199],v[56:59]
v_exp_f32 v140,v140
v_mfma_f32_16x16x32_bf16 v[56:59],v[28:31],v[212:215],v[56:59]
v_exp_f32 v141,v141
v_mfma_f32_16x16x32_bf16 v[164:167],a[128:131],v[80:83],v[36:39]
v_exp_f32 v142,v142
v_mfma_f32_16x16x32_bf16 v[164:167],a[132:135],v[84:87],v[164:167]
v_exp_f32 v143,v143
v_mfma_f32_16x16x32_bf16 v[164:167],a[136:139],v[88:91],v[164:167]
v_exp_f32 v148,v148
v_mfma_f32_16x16x32_bf16 v[164:167],a[140:143],v[92:95],v[164:167]
v_exp_f32 v149,v149
v_mfma_f32_16x16x32_bf16 v[172:175],a[156:159],v[92:95],v[36:39]
v_exp_f32 v150,v150
v_mfma_f32_16x16x32_bf16 v[172:175],a[152:155],v[88:91],v[172:175]
v_exp_f32 v151,v151
v_mfma_f32_16x16x32_bf16 v[172:175],a[148:151],v[84:87],v[172:175]
buffer_load_dwordx4 v5,s[24:27],s47 offen offset:1024 nt lds
v_mfma_f32_16x16x32_bf16 v[172:175],a[144:147],v[80:83],v[172:175]
v_cvt_pk_bf16_f32 v134,v140,v141
v_cvt_pk_bf16_f32 v135,v142,v143
v_mfma_f32_16x16x32_bf16 v[180:183],a[160:163],v[80:83],v[36:39]
v_cvt_pk_bf16_f32 v148,v148,v149
v_cvt_pk_bf16_f32 v149,v150,v151
v_mfma_f32_16x16x32_bf16 v[180:183],a[164:167],v[84:87],v[180:183]
v_exp_f32 v156,v156
v_mfma_f32_16x16x32_bf16 v[180:183],a[168:171],v[88:91],v[180:183]
v_exp_f32 v157,v157
v_mfma_f32_16x16x32_bf16 v[180:183],a[172:175],v[92:95],v[180:183]
v_exp_f32 v158,v158
v_mfma_f32_16x16x32_bf16 v[188:191],a[188:191],v[92:95],v[36:39]
v_exp_f32 v159,v159
v_mfma_f32_16x16x32_bf16 v[188:191],a[184:187],v[88:91],v[188:191]
v_cvt_pk_bf16_f32 v150,v156,v157
v_cvt_pk_bf16_f32 v151,v158,v159
v_mfma_f32_16x16x32_bf16 v[188:191],a[180:183],v[84:87],v[188:191]
s_waitcnt vmcnt(14)
s_cmp_eq_u32 s49,163840
s_cselect_b32 s49,0,s49
v_mfma_f32_16x16x32_bf16 v[188:191],a[176:179],v[80:83],v[188:191]
s_barrier
ds_read_b128 v[80:83],v18
v_mfma_f32_16x16x32_bf16 a[96:99],a[192:195],v[228:231],a[96:99]
ds_read_b128 v[84:87],v18 offset:256
v_mfma_f32_16x16x32_bf16 a[96:99],a[196:199],v[244:247],a[96:99]
ds_read_b128 v[88:91],v18 offset:512
v_mfma_f32_16x16x32_bf16 a[100:103],a[204:207],v[244:247],a[100:103]
ds_read_b128 v[92:95],v18 offset:768
v_mfma_f32_16x16x32_bf16 a[100:103],a[200:203],v[228:231],a[100:103]
buffer_load_dwordx4 v4,s[24:27],s47 offen offset:2048 nt lds
v_mfma_f32_16x16x32_bf16 a[104:107],a[208:211],v[228:231],a[104:107]
ds_read_b64_tr_b16 a[192:193],v2
ds_read_b64_tr_b16 a[194:195],v2 offset:4096
v_mfma_f32_16x16x32_bf16 a[104:107],a[212:215],v[244:247],a[104:107]
ds_read_b64_tr_b16 a[196:197],v2 offset:8192
ds_read_b64_tr_b16 a[198:199],v2 offset:12288
v_mfma_f32_16x16x32_bf16 a[108:111],a[220:223],v[244:247],a[108:111]
ds_read_b64_tr_b16 a[204:205],v3 offset:8192
ds_read_b64_tr_b16 a[206:207],v3 offset:12288
v_mfma_f32_16x16x32_bf16 a[108:111],a[216:219],v[228:231],a[108:111]
ds_read_b64_tr_b16 a[200:201],v3
ds_read_b64_tr_b16 a[202:203],v3 offset:4096
v_mfma_f32_16x16x32_bf16 a[112:115],a[224:227],v[228:231],a[112:115]
ds_read_b64_tr_b16 a[208:209],v2 offset:256
ds_read_b64_tr_b16 a[210:211],v2 offset:4352
v_mfma_f32_16x16x32_bf16 a[112:115],a[228:231],v[244:247],a[112:115]
ds_read_b64_tr_b16 a[212:213],v2 offset:8448
ds_read_b64_tr_b16 a[214:215],v2 offset:12544
v_mfma_f32_16x16x32_bf16 a[116:119],a[236:239],v[244:247],a[116:119]
ds_read_b64_tr_b16 a[220:221],v3 offset:8448
ds_read_b64_tr_b16 a[222:223],v3 offset:12544
v_mfma_f32_16x16x32_bf16 a[116:119],a[232:235],v[228:231],a[116:119]
ds_read_b64_tr_b16 a[216:217],v3 offset:256
ds_read_b64_tr_b16 a[218:219],v3 offset:4352
v_mfma_f32_16x16x32_bf16 a[120:123],a[240:243],v[228:231],a[120:123]
ds_read_b64_tr_b16 a[224:225],v2 offset:512
ds_read_b64_tr_b16 a[226:227],v2 offset:4608
v_mfma_f32_16x16x32_bf16 a[120:123],a[244:247],v[244:247],a[120:123]
ds_read_b64_tr_b16 a[228:229],v2 offset:8704
ds_read_b64_tr_b16 a[230:231],v2 offset:12800
v_mfma_f32_16x16x32_bf16 a[124:127],a[252:255],v[244:247],a[124:127]
ds_read_b64_tr_b16 a[236:237],v3 offset:8704
ds_read_b64_tr_b16 a[238:239],v3 offset:12800
v_mfma_f32_16x16x32_bf16 a[124:127],a[248:251],v[228:231],a[124:127]
ds_read_b64_tr_b16 a[232:233],v3 offset:512
ds_read_b64_tr_b16 a[234:235],v3 offset:4608
v_mfma_f32_16x16x32_bf16 v[60:63],v[28:31],v[228:231],v[60:63]
ds_read_b64_tr_b16 a[240:241],v2 offset:768
ds_read_b64_tr_b16 a[242:243],v2 offset:4864
v_mfma_f32_16x16x32_bf16 v[60:63],v[28:31],v[244:247],v[60:63]
buffer_load_dwordx4 v5,s[24:27],s47 offen offset:3072 nt lds
v_mfma_f32_16x16x32_bf16 v[196:199],a[128:131],v[96:99],v[40:43]
s_add_u32 s47,s47,16384
ds_read_b64_tr_b16 a[244:245],v2 offset:8960
ds_read_b64_tr_b16 a[246:247],v2 offset:13056
v_mfma_f32_16x16x32_bf16 v[196:199],a[132:135],v[100:103],v[196:199]
s_and_b32 s47,s47,0x3ffff
ds_read_b64_tr_b16 a[252:253],v3 offset:8960
ds_read_b64_tr_b16 a[254:255],v3 offset:13056
v_mfma_f32_16x16x32_bf16 v[196:199],a[136:139],v[104:107],v[196:199]
s_or_b32 s47,s47,s46
ds_read_b64_tr_b16 a[248:249],v3 offset:768
ds_read_b64_tr_b16 a[250:251],v3 offset:4864
v_mfma_f32_16x16x32_bf16 v[196:199],a[140:143],v[108:111],v[196:199]
s_and_b32 s46,s54,0xfffc0000
v_exp_f32 v164,v164
v_mfma_f32_16x16x32_bf16 v[204:207],a[156:159],v[108:111],v[40:43]
s_add_u32 s54,s54,16384
v_exp_f32 v165,v165
v_mfma_f32_16x16x32_bf16 v[204:207],a[152:155],v[104:107],v[204:207]
v_exp_f32 v166,v166
v_mfma_f32_16x16x32_bf16 v[204:207],a[148:151],v[100:103],v[204:207]
v_exp_f32 v167,v167
v_mfma_f32_16x16x32_bf16 v[204:207],a[144:147],v[96:99],v[204:207]
v_cvt_pk_bf16_f32 v164,v164,v165
v_cvt_pk_bf16_f32 v165,v166,v167
v_mfma_f32_16x16x32_bf16 v[212:215],a[160:163],v[96:99],v[40:43]
v_exp_f32 v172,v172
)A"
R"A(v_mfma_f32_16x16x32_bf16 v[212:215],a[164:167],v[100:103],v[212:215]
v_exp_f32 v173,v173
v_mfma_f32_16x16x32_bf16 v[212:215],a[168:171],v[104:107],v[212:215]
v_exp_f32 v174,v174
v_mfma_f32_16x16x32_bf16 v[212:215],a[172:175],v[108:111],v[212:215]
v_exp_f32 v175,v175
v_mfma_f32_16x16x32_bf16 v[220:223],a[188:191],v[108:111],v[40:43]
v_exp_f32 v180,v180
v_mfma_f32_16x16x32_bf16 v[220:223],a[184:187],v[104:107],v[220:223]
v_exp_f32 v181,v181
v_mfma_f32_16x16x32_bf16 v[220:223],a[180:183],v[100:103],v[220:223]
v_exp_f32 v182,v182
v_mfma_f32_16x16x32_bf16 v[220:223],a[176:179],v[96:99],v[220:223]
s_waitcnt lgkmcnt(0)
v_exp_f32 v183,v183
v_mfma_f32_16x16x32_bf16 v[32:35],v[80:83],v[128:131],0
v_cvt_pk_bf16_f32 v166,v172,v173
v_cvt_pk_bf16_f32 v167,v174,v175
v_mfma_f32_16x16x32_bf16 v[32:35],v[84:87],v[160:163],v[32:35]
v_cvt_pk_bf16_f32 v180,v180,v181
v_cvt_pk_bf16_f32 v181,v182,v183
v_mfma_f32_16x16x32_bf16 v[32:35],v[88:91],v[192:195],v[32:35]
v_exp_f32 v188,v188
v_mfma_f32_16x16x32_bf16 v[32:35],v[92:95],v[224:227],v[32:35]
v_exp_f32 v189,v189
v_mfma_f32_16x16x32_bf16 v[36:39],v[80:83],v[136:139],0
v_exp_f32 v190,v190
v_mfma_f32_16x16x32_bf16 v[36:39],v[84:87],v[168:171],v[36:39]
v_exp_f32 v191,v191
v_mfma_f32_16x16x32_bf16 v[36:39],v[88:91],v[200:203],v[36:39]
v_cvt_pk_bf16_f32 v182,v188,v189
v_cvt_pk_bf16_f32 v183,v190,v191
v_mfma_f32_16x16x32_bf16 v[36:39],v[92:95],v[232:235],v[36:39]
v_exp_f32 v196,v196
v_mfma_f32_16x16x32_bf16 v[40:43],v[80:83],v[144:147],0
v_exp_f32 v197,v197
v_mfma_f32_16x16x32_bf16 v[40:43],v[84:87],v[176:179],v[40:43]
v_max3_f32 v32,v32,v33,v34
v_max3_f32 v32,v32,v35,s18
v_mfma_f32_16x16x32_bf16 v[40:43],v[88:91],v[208:211],v[40:43]
v_sub_f32 v35,0,v32
v_sub_f32 v34,0,v32
v_mfma_f32_16x16x32_bf16 v[40:43],v[92:95],v[240:243],v[40:43]
v_sub_f32 v33,0,v32
v_sub_f32 v32,0,v32
v_mfma_f32_16x16x32_bf16 a[0:3],a[192:195],v[132:135],a[0:3]
v_exp_f32 v198,v198
v_mfma_f32_16x16x32_bf16 a[0:3],a[196:199],v[148:151],a[0:3]
v_exp_f32 v199,v199
v_mfma_f32_16x16x32_bf16 a[4:7],a[204:207],v[148:151],a[4:7]
v_cvt_pk_bf16_f32 v196,v196,v197
v_cvt_pk_bf16_f32 v197,v198,v199
v_mfma_f32_16x16x32_bf16 a[4:7],a[200:203],v[132:135],a[4:7]
v_exp_f32 v204,v204
v_mfma_f32_16x16x32_bf16 a[8:11],a[208:211],v[132:135],a[8:11]
s_memrealtime s[76:77]
v_exp_f32 v205,v205
v_mfma_f32_16x16x32_bf16 a[8:11],a[212:215],v[148:151],a[8:11]
v_exp_f32 v206,v206
v_mfma_f32_16x16x32_bf16 a[12:15],a[220:223],v[148:151],a[12:15]
v_exp_f32 v207,v207
v_mfma_f32_16x16x32_bf16 a[12:15],a[216:219],v[132:135],a[12:15]
v_exp_f32 v212,v212
v_mfma_f32_16x16x32_bf16 a[16:19],a[224:227],v[132:135],a[16:19]
v_exp_f32 v213,v213
v_mfma_f32_16x16x32_bf16 a[16:19],a[228:231],v[148:151],a[16:19]
v_exp_f32 v214,v214
v_mfma_f32_16x16x32_bf16 a[20:23],a[236:239],v[148:151],a[20:23]
v_exp_f32 v215,v215
v_mfma_f32_16x16x32_bf16 a[20:23],a[232:235],v[132:135],a[20:23]
s_waitcnt lgkmcnt(0)
v_cvt_pk_bf16_f32 v198,v204,v205
v_cvt_pk_bf16_f32 v199,v206,v207
v_mfma_f32_16x16x32_bf16 a[24:27],a[240:243],v[132:135],a[24:27]
s_sub_u32 s79,s78,s76
v_cvt_pk_bf16_f32 v212,v212,v213
v_cvt_pk_bf16_f32 v213,v214,v215
v_mfma_f32_16x16x32_bf16 a[24:27],a[244:247],v[148:151],a[24:27]
s_add_u32 s78,s76,s60
v_exp_f32 v220,v220
v_mfma_f32_16x16x32_bf16 a[28:31],a[252:255],v[148:151],a[28:31]
s_cmp_gt_i32 s79,0
s_cbranch_scc1 L_ps4_%=
L_pr4_%=:
v_max3_f32 v36,v36,v37,v38
v_mfma_f32_16x16x32_bf16 a[28:31],a[248:251],v[132:135],a[28:31]
v_exp_f32 v221,v221
v_mfma_f32_16x16x32_bf16 v[48:51],v[28:31],v[132:135],v[48:51]
v_exp_f32 v222,v222
v_mfma_f32_16x16x32_bf16 v[48:51],v[28:31],v[148:151],v[48:51]
v_exp_f32 v223,v223
v_mfma_f32_16x16x32_bf16 v[228:231],a[128:131],v[112:115],v[44:47]
s_waitcnt vmcnt(9)
v_cvt_pk_bf16_f32 v214,v220,v221
v_cvt_pk_bf16_f32 v215,v222,v223
v_mfma_f32_16x16x32_bf16 v[228:231],a[132:135],v[116:119],v[228:231]
s_barrier
s_add_u32 m0,s49,s51
ds_read_b128 a[128:131],v1
v_mfma_f32_16x16x32_bf16 v[228:231],a[136:139],v[120:123],v[228:231]
ds_read_b128 a[132:135],v1 offset:256
v_max3_f32 v36,v36,v39,s19
v_mfma_f32_16x16x32_bf16 v[228:231],a[140:143],v[124:127],v[228:231]
ds_read_b128 a[136:139],v1 offset:512
v_sub_f32 v39,0,v36
v_mfma_f32_16x16x32_bf16 v[236:239],a[156:159],v[124:127],v[44:47]
buffer_load_dwordx4 v4,s[20:23],s47 offen nt lds
v_mfma_f32_16x16x32_bf16 v[236:239],a[152:155],v[120:123],v[236:239]
ds_read_b128 a[140:143],v1 offset:768
v_sub_f32 v38,0,v36
v_mfma_f32_16x16x32_bf16 v[236:239],a[148:151],v[116:119],v[236:239]
ds_read_b128 a[156:159],v1 offset:4864
v_sub_f32 v37,0,v36
v_mfma_f32_16x16x32_bf16 v[236:239],a[144:147],v[112:115],v[236:239]
ds_read_b128 a[152:155],v1 offset:4608
v_sub_f32 v36,0,v36
v_mfma_f32_16x16x32_bf16 v[244:247],a[160:163],v[112:115],v[44:47]
ds_read_b128 a[148:151],v1 offset:4352
v_max3_f32 v40,v40,v41,v42
v_mfma_f32_16x16x32_bf16 v[244:247],a[164:167],v[116:119],v[244:247]
ds_read_b128 a[144:147],v1 offset:4096
v_max3_f32 v40,v40,v43,s100
v_mfma_f32_16x16x32_bf16 v[244:247],a[168:171],v[120:123],v[244:247]
ds_read_b128 a[160:163],v1 offset:8192
v_sub_f32 v43,0,v40
v_mfma_f32_16x16x32_bf16 v[244:247],a[172:175],v[124:127],v[244:247]
ds_read_b128 a[164:167],v1 offset:8448
v_sub_f32 v42,0,v40
v_mfma_f32_16x16x32_bf16 v[252:255],a[188:191],v[124:127],v[44:47]
ds_read_b128 a[168:171],v1 offset:8704
v_sub_f32 v41,0,v40
v_mfma_f32_16x16x32_bf16 v[252:255],a[184:187],v[120:123],v[252:255]
ds_read_b128 a[172:175],v1 offset:8960
v_sub_f32 v40,0,v40
v_mfma_f32_16x16x32_bf16 v[252:255],a[180:183],v[116:119],v[252:255]
ds_read_b128 a[188:191],v1 offset:13056
v_add_u32 v2,s48,v2
v_mfma_f32_16x16x32_bf16 v[252:255],a[176:179],v[112:115],v[252:255]
ds_read_b128 a[184:187],v1 offset:12800
v_add_u32 v3,s48,v3
v_mfma_f32_16x16x32_bf16 v[44:47],v[80:83],v[152:155],0
s_sub_u32 s48,32768,s62
ds_read_b128 a[180:183],v1 offset:12544
v_max_f32 v20,0x00800000,v48
v_mfma_f32_16x16x32_bf16 v[44:47],v[84:87],v[184:187],v[44:47]
s_add_u32 s50,s50,s48
ds_read_b128 a[176:179],v1 offset:12288
v_accvgpr_read_b32 v64,a0
v_mfma_f32_16x16x32_bf16 v[44:47],v[88:91],v[216:219],v[44:47]
buffer_load_dwordx4 v4,s[20:23],s47 offen offset:1024 nt lds
v_mfma_f32_16x16x32_bf16 v[44:47],v[92:95],v[248:251],v[44:47]
v_exp_f32 v228,v228
v_mfma_f32_16x16x32_bf16 a[32:35],a[192:195],v[164:167],a[32:35]
v_exp_f32 v229,v229
v_mfma_f32_16x16x32_bf16 a[32:35],a[196:199],v[180:183],a[32:35]
v_exp_f32 v230,v230
v_mfma_f32_16x16x32_bf16 a[36:39],a[204:207],v[180:183],a[36:39]
v_exp_f32 v231,v231
v_mfma_f32_16x16x32_bf16 a[36:39],a[200:203],v[164:167],a[36:39]
v_cvt_pk_bf16_f32 v228,v228,v229
v_cvt_pk_bf16_f32 v229,v230,v231
v_mfma_f32_16x16x32_bf16 a[40:43],a[208:211],v[164:167],a[40:43]
v_exp_f32 v236,v236
v_mfma_f32_16x16x32_bf16 a[40:43],a[212:215],v[180:183],a[40:43]
v_exp_f32 v237,v237
v_mfma_f32_16x16x32_bf16 a[44:47],a[220:223],v[180:183],a[44:47]
v_exp_f32 v238,v238
v_mfma_f32_16x16x32_bf16 a[44:47],a[216:219],v[164:167],a[44:47]
v_exp_f32 v239,v239
v_mfma_f32_16x16x32_bf16 a[48:51],a[224:227],v[164:167],a[48:51]
v_exp_f32 v244,v244
v_mfma_f32_16x16x32_bf16 a[48:51],a[228:231],v[180:183],a[48:51]
v_exp_f32 v245,v245
v_mfma_f32_16x16x32_bf16 a[52:55],a[236:239],v[180:183],a[52:55]
v_exp_f32 v246,v246
v_mfma_f32_16x16x32_bf16 a[52:55],a[232:235],v[164:167],a[52:55]
v_exp_f32 v247,v247
v_mfma_f32_16x16x32_bf16 a[56:59],a[240:243],v[164:167],a[56:59]
buffer_load_dwordx4 v5,s[20:23],s47 offen offset:2048 nt lds
v_mfma_f32_16x16x32_bf16 a[56:59],a[244:247],v[180:183],a[56:59]
v_cvt_pk_bf16_f32 v230,v236,v237
v_cvt_pk_bf16_f32 v231,v238,v239
v_mfma_f32_16x16x32_bf16 a[60:63],a[252:255],v[180:183],a[60:63]
v_cvt_pk_bf16_f32 v244,v244,v245
v_cvt_pk_bf16_f32 v245,v246,v247
v_mfma_f32_16x16x32_bf16 a[60:63],a[248:251],v[164:167],a[60:63]
v_exp_f32 v252,v252
v_mfma_f32_16x16x32_bf16 v[52:55],v[28:31],v[164:167],v[52:55]
v_exp_f32 v253,v253
v_mfma_f32_16x16x32_bf16 v[52:55],v[28:31],v[180:183],v[52:55]
s_waitcnt lgkmcnt(0)
v_exp_f32 v254,v254
v_mfma_f32_16x16x32_bf16 v[132:135],a[128:131],v[128:131],v[32:35]
v_exp_f32 v255,v255
v_mfma_f32_16x16x32_bf16 v[132:135],a[132:135],v[160:163],v[132:135]
v_cvt_pk_bf16_f32 v246,v252,v253
v_cvt_pk_bf16_f32 v247,v254,v255
v_mfma_f32_16x16x32_bf16 v[132:135],a[136:139],v[192:195],v[132:135]
v_rcp_f32 v20,v20
v_mfma_f32_16x16x32_bf16 v[132:135],a[140:143],v[224:227],v[132:135]
v_accvgpr_read_b32 v65,a1
v_mul_f32 v64,v20,v64
v_mfma_f32_16x16x32_bf16 v[140:143],a[156:159],v[224:227],v[32:35]
v_mul_f32 v65,v20,v65
v_accvgpr_read_b32 v66,a2
v_mfma_f32_16x16x32_bf16 v[140:143],a[152:155],v[192:195],v[140:143]
v_accvgpr_read_b32 v67,a3
v_cvt_pk_bf16_f32 v64,v64,v65
v_mfma_f32_16x16x32_bf16 v[140:143],a[148:151],v[160:163],v[140:143]
v_mul_f32 v66,v20,v66
v_mul_f32 v67,v20,v67
v_mfma_f32_16x16x32_bf16 v[140:143],a[144:147],v[128:131],v[140:143]
v_cvt_pk_bf16_f32 v65,v66,v67
v_accvgpr_read_b32 v66,a4
v_mfma_f32_16x16x32_bf16 v[148:151],a[160:163],v[128:131],v[32:35]
buffer_load_dwordx4 v5,s[20:23],s47 offen offset:3072 nt lds
v_mfma_f32_16x16x32_bf16 v[148:151],a[164:167],v[160:163],v[148:151]
v_exp_f32 v132,v132
v_mfma_f32_16x16x32_bf16 v[148:151],a[168:171],v[192:195],v[148:151]
v_exp_f32 v133,v133
v_mfma_f32_16x16x32_bf16 v[148:151],a[172:175],v[224:227],v[148:151]
v_exp_f32 v134,v134
v_mfma_f32_16x16x32_bf16 v[156:159],a[188:191],v[224:227],v[32:35]
v_exp_f32 v135,v135
v_mfma_f32_16x16x32_bf16 v[156:159],a[184:187],v[192:195],v[156:159]
v_cvt_pk_bf16_f32 v132,v132,v133
v_cvt_pk_bf16_f32 v133,v134,v135
v_mfma_f32_16x16x32_bf16 v[156:159],a[180:183],v[160:163],v[156:159]
v_exp_f32 v140,v140
v_mfma_f32_16x16x32_bf16 v[156:159],a[176:179],v[128:131],v[156:159]
v_exp_f32 v141,v141
v_mfma_f32_16x16x32_bf16 a[64:67],a[192:195],v[196:199],a[64:67]
v_exp_f32 v142,v142
v_mfma_f32_16x16x32_bf16 a[64:67],a[196:199],v[212:215],a[64:67]
v_exp_f32 v143,v143
v_mfma_f32_16x16x32_bf16 a[68:71],a[204:207],v[212:215],a[68:71]
v_exp_f32 v148,v148
v_mfma_f32_16x16x32_bf16 a[68:71],a[200:203],v[196:199],a[68:71]
s_add_u32 m0,s49,s52
v_exp_f32 v149,v149
v_mfma_f32_16x16x32_bf16 a[72:75],a[208:211],v[196:199],a[72:75]
s_add_u32 s49,s49,32768
v_exp_f32 v150,v150
v_mfma_f32_16x16x32_bf16 a[72:75],a[212:215],v[212:215],a[72:75]
v_exp_f32 v151,v151
v_mfma_f32_16x16x32_bf16 a[76:79],a[220:223],v[212:215],a[76:79]
buffer_load_dwordx4 v4,s[24:27],s47 offen nt lds
v_mfma_f32_16x16x32_bf16 a[76:79],a[216:219],v[196:199],a[76:79]
v_cvt_pk_bf16_f32 v134,v140,v141
v_cvt_pk_bf16_f32 v135,v142,v143
v_mfma_f32_16x16x32_bf16 a[80:83],a[224:227],v[196:199],a[80:83]
v_cvt_pk_bf16_f32 v148,v148,v149
v_cvt_pk_bf16_f32 v149,v150,v151
v_mfma_f32_16x16x32_bf16 a[80:83],a[228:231],v[212:215],a[80:83]
v_exp_f32 v156,v156
v_mfma_f32_16x16x32_bf16 a[84:87],a[236:239],v[212:215],a[84:87]
v_exp_f32 v157,v157
v_mfma_f32_16x16x32_bf16 a[84:87],a[232:235],v[196:199],a[84:87]
v_exp_f32 v158,v158
v_mfma_f32_16x16x32_bf16 a[88:91],a[240:243],v[196:199],a[88:91]
v_exp_f32 v159,v159
v_mfma_f32_16x16x32_bf16 a[88:91],a[244:247],v[212:215],a[88:91]
v_cvt_pk_bf16_f32 v150,v156,v157
v_cvt_pk_bf16_f32 v151,v158,v159
v_mfma_f32_16x16x32_bf16 a[92:95],a[252:255],v[212:215],a[92:95]
v_accvgpr_read_b32 v67,a5
v_accvgpr_read_b32 v12,a6
v_mfma_f32_16x16x32_bf16 a[92:95],a[248:251],v[196:199],a[92:95]
v_accvgpr_read_b32 v13,a7
v_mul_f32 v66,v20,v66
v_mfma_f32_16x16x32_bf16 v[56:59],v[28:31],v[196:199],v[56:59]
v_mul_f32 v67,v20,v67
v_mul_f32 v12,v20,v12
v_mfma_f32_16x16x32_bf16 v[56:59],v[28:31],v[212:215],v[56:59]
v_mul_f32 v13,v20,v13
v_cvt_pk_bf16_f32 v66,v66,v67
v_mfma_f32_16x16x32_bf16 v[164:167],a[128:131],v[136:139],v[36:39]
v_cvt_pk_bf16_f32 v67,v12,v13
v_max3_f32 v44,v44,v45,v46
v_mfma_f32_16x16x32_bf16 v[164:167],a[132:135],v[168:171],v[164:167]
v_accvgpr_read_b32 v68,a8
v_accvgpr_read_b32 v69,a9
v_accvgpr_read_b32 v70,a10
v_accvgpr_read_b32 v71,a11
v_accvgpr_read_b32 v12,a12
v_accvgpr_read_b32 v13,a13
v_accvgpr_read_b32 v14,a14
v_accvgpr_read_b32 v15,a15
v_pk_mul_f32 v[68:69],v[20:21],v[68:69] op_sel:[0,0] op_sel_hi:[0,1]
v_pk_mul_f32 v[70:71],v[20:21],v[70:71] op_sel:[0,0] op_sel_hi:[0,1]
)A"
R"A(v_pk_mul_f32 v[12:13],v[20:21],v[12:13] op_sel:[0,0] op_sel_hi:[0,1]
v_pk_mul_f32 v[14:15],v[20:21],v[14:15] op_sel:[0,0] op_sel_hi:[0,1]
v_cvt_pk_bf16_f32 v68,v68,v69
v_cvt_pk_bf16_f32 v69,v70,v71
v_cvt_pk_bf16_f32 v70,v12,v13
v_cvt_pk_bf16_f32 v71,v14,v15
v_mfma_f32_16x16x32_bf16 v[164:167],a[136:139],v[200:203],v[164:167]
buffer_load_dwordx4 v5,s[24:27],s47 offen offset:1024 nt lds
v_mfma_f32_16x16x32_bf16 v[164:167],a[140:143],v[232:235],v[164:167]
v_accvgpr_read_b32 v72,a16
v_accvgpr_read_b32 v73,a17
v_accvgpr_read_b32 v74,a18
v_accvgpr_read_b32 v75,a19
v_accvgpr_read_b32 v12,a20
v_accvgpr_read_b32 v13,a21
v_accvgpr_read_b32 v14,a22
v_accvgpr_read_b32 v15,a23
v_pk_mul_f32 v[72:73],v[20:21],v[72:73] op_sel:[0,0] op_sel_hi:[0,1]
v_pk_mul_f32 v[74:75],v[20:21],v[74:75] op_sel:[0,0] op_sel_hi:[0,1]
v_pk_mul_f32 v[12:13],v[20:21],v[12:13] op_sel:[0,0] op_sel_hi:[0,1]
v_pk_mul_f32 v[14:15],v[20:21],v[14:15] op_sel:[0,0] op_sel_hi:[0,1]
v_cvt_pk_bf16_f32 v72,v72,v73
v_cvt_pk_bf16_f32 v73,v74,v75
v_cvt_pk_bf16_f32 v74,v12,v13
v_cvt_pk_bf16_f32 v75,v14,v15
v_mfma_f32_16x16x32_bf16 v[172:175],a[156:159],v[232:235],v[36:39]
v_accvgpr_read_b32 v76,a24
v_accvgpr_read_b32 v77,a25
v_accvgpr_read_b32 v78,a26
v_accvgpr_read_b32 v79,a27
v_accvgpr_read_b32 v12,a28
v_accvgpr_read_b32 v13,a29
v_accvgpr_read_b32 v14,a30
v_accvgpr_read_b32 v15,a31
v_pk_mul_f32 v[76:77],v[20:21],v[76:77] op_sel:[0,0] op_sel_hi:[0,1]
v_pk_mul_f32 v[78:79],v[20:21],v[78:79] op_sel:[0,0] op_sel_hi:[0,1]
v_pk_mul_f32 v[12:13],v[20:21],v[12:13] op_sel:[0,0] op_sel_hi:[0,1]
v_pk_mul_f32 v[14:15],v[20:21],v[14:15] op_sel:[0,0] op_sel_hi:[0,1]
v_cvt_pk_bf16_f32 v76,v76,v77
v_cvt_pk_bf16_f32 v77,v78,v79
v_cvt_pk_bf16_f32 v78,v12,v13
v_cvt_pk_bf16_f32 v79,v14,v15
v_mfma_f32_16x16x32_bf16 v[172:175],a[152:155],v[200:203],v[172:175]
v_max3_f32 v44,v44,v47,s101
v_sub_f32 v47,0,v44
v_mfma_f32_16x16x32_bf16 v[172:175],a[148:151],v[168:171],v[172:175]
v_sub_f32 v46,0,v44
v_sub_f32 v45,0,v44
v_mfma_f32_16x16x32_bf16 v[172:175],a[144:147],v[136:139],v[172:175]
v_cmp_nlt_f32 s[56:57],v48,s63
v_sub_f32 v44,0,v44
v_mfma_f32_16x16x32_bf16 v[180:183],a[160:163],v[136:139],v[36:39]
s_cmp_eq_u32 s49,163840
s_cselect_b32 s49,0,s49
v_max_f32 v21,0x00800000,v52
v_mfma_f32_16x16x32_bf16 v[180:183],a[164:167],v[168:171],v[180:183]
v_exp_f32 v164,v164
v_mfma_f32_16x16x32_bf16 v[180:183],a[168:171],v[200:203],v[180:183]
v_exp_f32 v165,v165
v_mfma_f32_16x16x32_bf16 v[180:183],a[172:175],v[232:235],v[180:183]
v_exp_f32 v166,v166
v_mfma_f32_16x16x32_bf16 v[188:191],a[188:191],v[232:235],v[36:39]
v_exp_f32 v167,v167
v_mfma_f32_16x16x32_bf16 v[188:191],a[184:187],v[200:203],v[188:191]
v_cvt_pk_bf16_f32 v164,v164,v165
v_cvt_pk_bf16_f32 v165,v166,v167
v_mfma_f32_16x16x32_bf16 v[188:191],a[180:183],v[168:171],v[188:191]
v_exp_f32 v172,v172
v_mfma_f32_16x16x32_bf16 v[188:191],a[176:179],v[136:139],v[188:191]
v_exp_f32 v173,v173
v_mfma_f32_16x16x32_bf16 a[96:99],a[192:195],v[228:231],a[96:99]
buffer_load_dwordx4 v4,s[24:27],s47 offen offset:2048 nt lds
v_mfma_f32_16x16x32_bf16 a[96:99],a[196:199],v[244:247],a[96:99]
ds_read_b64_tr_b16 a[192:193],v2
ds_read_b64_tr_b16 a[194:195],v2 offset:4096
v_mfma_f32_16x16x32_bf16 a[100:103],a[204:207],v[244:247],a[100:103]
ds_read_b64_tr_b16 a[196:197],v2 offset:8192
ds_read_b64_tr_b16 a[198:199],v2 offset:12288
v_mfma_f32_16x16x32_bf16 a[100:103],a[200:203],v[228:231],a[100:103]
ds_read_b64_tr_b16 a[204:205],v3 offset:8192
ds_read_b64_tr_b16 a[206:207],v3 offset:12288
v_mfma_f32_16x16x32_bf16 a[104:107],a[208:211],v[228:231],a[104:107]
ds_read_b64_tr_b16 a[200:201],v3
ds_read_b64_tr_b16 a[202:203],v3 offset:4096
v_mfma_f32_16x16x32_bf16 a[104:107],a[212:215],v[244:247],a[104:107]
ds_read_b64_tr_b16 a[208:209],v2 offset:256
ds_read_b64_tr_b16 a[210:211],v2 offset:4352
v_mfma_f32_16x16x32_bf16 a[108:111],a[220:223],v[244:247],a[108:111]
ds_read_b64_tr_b16 a[212:213],v2 offset:8448
ds_read_b64_tr_b16 a[214:215],v2 offset:12544
v_mfma_f32_16x16x32_bf16 a[108:111],a[216:219],v[228:231],a[108:111]
ds_read_b64_tr_b16 a[220:221],v3 offset:8448
ds_read_b64_tr_b16 a[222:223],v3 offset:12544
v_mfma_f32_16x16x32_bf16 a[112:115],a[224:227],v[228:231],a[112:115]
ds_read_b64_tr_b16 a[216:217],v3 offset:256
ds_read_b64_tr_b16 a[218:219],v3 offset:4352
v_mfma_f32_16x16x32_bf16 a[112:115],a[228:231],v[244:247],a[112:115]
ds_read_b64_tr_b16 a[224:225],v2 offset:512
ds_read_b64_tr_b16 a[226:227],v2 offset:4608
v_mfma_f32_16x16x32_bf16 a[116:119],a[236:239],v[244:247],a[116:119]
ds_read_b64_tr_b16 a[228:229],v2 offset:8704
ds_read_b64_tr_b16 a[230:231],v2 offset:12800
v_mfma_f32_16x16x32_bf16 a[116:119],a[232:235],v[228:231],a[116:119]
ds_read_b64_tr_b16 a[236:237],v3 offset:8704
ds_read_b64_tr_b16 a[238:239],v3 offset:12800
v_mfma_f32_16x16x32_bf16 a[120:123],a[240:243],v[228:231],a[120:123]
ds_read_b64_tr_b16 a[232:233],v3 offset:512
ds_read_b64_tr_b16 a[234:235],v3 offset:4608
v_mfma_f32_16x16x32_bf16 a[120:123],a[244:247],v[244:247],a[120:123]
ds_read_b64_tr_b16 a[240:241],v2 offset:768
ds_read_b64_tr_b16 a[242:243],v2 offset:4864
v_mfma_f32_16x16x32_bf16 a[124:127],a[252:255],v[244:247],a[124:127]
buffer_load_dwordx4 v5,s[24:27],s47 offen offset:3072 nt lds
v_mfma_f32_16x16x32_bf16 a[124:127],a[248:251],v[228:231],a[124:127]
s_add_u32 s47,s47,16384
ds_read_b64_tr_b16 a[244:245],v2 offset:8960
ds_read_b64_tr_b16 a[246:247],v2 offset:13056
v_mfma_f32_16x16x32_bf16 v[60:63],v[28:31],v[228:231],v[60:63]
s_and_b32 s47,s47,0x3ffff
ds_read_b64_tr_b16 a[252:253],v3 offset:8960
ds_read_b64_tr_b16 a[254:255],v3 offset:13056
v_mfma_f32_16x16x32_bf16 v[60:63],v[28:31],v[244:247],v[60:63]
s_or_b32 s47,s47,s46
ds_read_b64_tr_b16 a[248:249],v3 offset:768
ds_read_b64_tr_b16 a[250:251],v3 offset:4864
v_mfma_f32_16x16x32_bf16 v[196:199],a[128:131],v[144:147],v[40:43]
s_and_b32 s46,s54,0xfffc0000
v_exp_f32 v174,v174
v_mfma_f32_16x16x32_bf16 v[196:199],a[132:135],v[176:179],v[196:199]
v_exp_f32 v175,v175
v_mfma_f32_16x16x32_bf16 v[196:199],a[136:139],v[208:211],v[196:199]
v_exp_f32 v180,v180
v_mfma_f32_16x16x32_bf16 v[196:199],a[140:143],v[240:243],v[196:199]
v_exp_f32 v181,v181
v_mfma_f32_16x16x32_bf16 v[204:207],a[156:159],v[240:243],v[40:43]
v_exp_f32 v182,v182
v_mfma_f32_16x16x32_bf16 v[204:207],a[152:155],v[208:211],v[204:207]
v_exp_f32 v183,v183
v_mfma_f32_16x16x32_bf16 v[204:207],a[148:151],v[176:179],v[204:207]
v_cvt_pk_bf16_f32 v166,v172,v173
v_cvt_pk_bf16_f32 v167,v174,v175
v_mfma_f32_16x16x32_bf16 v[204:207],a[144:147],v[144:147],v[204:207]
v_cvt_pk_bf16_f32 v180,v180,v181
v_cvt_pk_bf16_f32 v181,v182,v183
v_mfma_f32_16x16x32_bf16 v[212:215],a[160:163],v[144:147],v[40:43]
v_exp_f32 v188,v188
v_mfma_f32_16x16x32_bf16 v[212:215],a[164:167],v[176:179],v[212:215]
v_exp_f32 v189,v189
v_mfma_f32_16x16x32_bf16 v[212:215],a[168:171],v[208:211],v[212:215]
v_exp_f32 v190,v190
v_mfma_f32_16x16x32_bf16 v[212:215],a[172:175],v[240:243],v[212:215]
v_exp_f32 v191,v191
v_mfma_f32_16x16x32_bf16 v[220:223],a[188:191],v[240:243],v[40:43]
v_rcp_f32 v21,v21
v_mfma_f32_16x16x32_bf16 v[220:223],a[184:187],v[208:211],v[220:223]
v_cvt_pk_bf16_f32 v182,v188,v189
v_cvt_pk_bf16_f32 v183,v190,v191
v_mfma_f32_16x16x32_bf16 v[220:223],a[180:183],v[176:179],v[220:223]
v_accvgpr_read_b32 v80,a32
v_accvgpr_read_b32 v81,a33
v_mfma_f32_16x16x32_bf16 v[220:223],a[176:179],v[144:147],v[220:223]
s_waitcnt lgkmcnt(0)
v_mul_f32 v80,v21,v80
v_mul_f32 v81,v21,v81
v_mfma_f32_16x16x32_bf16 a[0:3],a[192:195],v[132:135],0
v_accvgpr_read_b32 v82,a34
v_accvgpr_read_b32 v83,a35
v_mfma_f32_16x16x32_bf16 a[0:3],a[196:199],v[148:151],a[0:3]
v_cvt_pk_bf16_f32 v80,v80,v81
v_mul_f32 v82,v21,v82
v_mfma_f32_16x16x32_bf16 a[4:7],a[204:207],v[148:151],0
v_mul_f32 v83,v21,v83
v_cvt_pk_bf16_f32 v81,v82,v83
v_mfma_f32_16x16x32_bf16 a[4:7],a[200:203],v[132:135],a[4:7]
v_accvgpr_read_b32 v82,a36
v_accvgpr_read_b32 v83,a37
v_mfma_f32_16x16x32_bf16 a[8:11],a[208:211],v[132:135],0
s_memrealtime s[76:77]
v_accvgpr_read_b32 v14,a38
v_accvgpr_read_b32 v15,a39
v_mfma_f32_16x16x32_bf16 a[8:11],a[212:215],v[148:151],a[8:11]
v_mul_f32 v82,v21,v82
v_mul_f32 v83,v21,v83
v_mfma_f32_16x16x32_bf16 a[12:15],a[220:223],v[148:151],0
v_mul_f32 v14,v21,v14
v_mul_f32 v15,v21,v15
v_mfma_f32_16x16x32_bf16 a[12:15],a[216:219],v[132:135],a[12:15]
v_cvt_pk_bf16_f32 v82,v82,v83
v_cvt_pk_bf16_f32 v83,v14,v15
v_mfma_f32_16x16x32_bf16 a[16:19],a[224:227],v[132:135],0
v_accvgpr_read_b32 v84,a40
v_accvgpr_read_b32 v85,a41
v_accvgpr_read_b32 v86,a42
v_accvgpr_read_b32 v87,a43
v_accvgpr_read_b32 v12,a44
v_accvgpr_read_b32 v13,a45
v_accvgpr_read_b32 v14,a46
v_accvgpr_read_b32 v15,a47
v_pk_mul_f32 v[84:85],v[20:21],v[84:85] op_sel:[1,0] op_sel_hi:[1,1]
v_pk_mul_f32 v[86:87],v[20:21],v[86:87] op_sel:[1,0] op_sel_hi:[1,1]
v_pk_mul_f32 v[12:13],v[20:21],v[12:13] op_sel:[1,0] op_sel_hi:[1,1]
v_pk_mul_f32 v[14:15],v[20:21],v[14:15] op_sel:[1,0] op_sel_hi:[1,1]
v_cvt_pk_bf16_f32 v84,v84,v85
v_cvt_pk_bf16_f32 v85,v86,v87
v_cvt_pk_bf16_f32 v86,v12,v13
v_cvt_pk_bf16_f32 v87,v14,v15
v_mfma_f32_16x16x32_bf16 a[16:19],a[228:231],v[148:151],a[16:19]
v_accvgpr_read_b32 v88,a48
v_accvgpr_read_b32 v89,a49
v_accvgpr_read_b32 v90,a50
v_accvgpr_read_b32 v91,a51
v_accvgpr_read_b32 v12,a52
v_accvgpr_read_b32 v13,a53
v_accvgpr_read_b32 v14,a54
v_accvgpr_read_b32 v15,a55
v_pk_mul_f32 v[88:89],v[20:21],v[88:89] op_sel:[1,0] op_sel_hi:[1,1]
v_pk_mul_f32 v[90:91],v[20:21],v[90:91] op_sel:[1,0] op_sel_hi:[1,1]
v_pk_mul_f32 v[12:13],v[20:21],v[12:13] op_sel:[1,0] op_sel_hi:[1,1]
v_pk_mul_f32 v[14:15],v[20:21],v[14:15] op_sel:[1,0] op_sel_hi:[1,1]
v_cvt_pk_bf16_f32 v88,v88,v89
v_cvt_pk_bf16_f32 v89,v90,v91
v_cvt_pk_bf16_f32 v90,v12,v13
v_cvt_pk_bf16_f32 v91,v14,v15
v_mfma_f32_16x16x32_bf16 a[20:23],a[236:239],v[148:151],0
v_add_u32 v1,s48,v1
v_max_f32 v22,0x00800000,v56
v_mfma_f32_16x16x32_bf16 a[20:23],a[232:235],v[132:135],a[20:23]
s_waitcnt lgkmcnt(0)
v_accvgpr_read_b32 v92,a56
v_accvgpr_read_b32 v93,a57
v_accvgpr_read_b32 v94,a58
v_accvgpr_read_b32 v95,a59
v_accvgpr_read_b32 v12,a60
v_accvgpr_read_b32 v13,a61
v_accvgpr_read_b32 v14,a62
v_accvgpr_read_b32 v15,a63
v_pk_mul_f32 v[92:93],v[20:21],v[92:93] op_sel:[1,0] op_sel_hi:[1,1]
v_pk_mul_f32 v[94:95],v[20:21],v[94:95] op_sel:[1,0] op_sel_hi:[1,1]
v_pk_mul_f32 v[12:13],v[20:21],v[12:13] op_sel:[1,0] op_sel_hi:[1,1]
v_pk_mul_f32 v[14:15],v[20:21],v[14:15] op_sel:[1,0] op_sel_hi:[1,1]
v_cvt_pk_bf16_f32 v92,v92,v93
v_cvt_pk_bf16_f32 v93,v94,v95
v_cvt_pk_bf16_f32 v94,v12,v13
v_cvt_pk_bf16_f32 v95,v14,v15
v_mfma_f32_16x16x32_bf16 a[24:27],a[240:243],v[132:135],0
s_sub_u32 s79,s78,s76
v_cmp_nlt_f32 s[58:59],v52,s63
s_nop 1
s_or_b64 s[56:57],s[56:57],s[58:59]
v_mfma_f32_16x16x32_bf16 a[24:27],a[244:247],v[148:151],a[24:27]
s_add_u32 s78,s76,s60
v_exp_f32 v196,v196
v_mfma_f32_16x16x32_bf16 a[28:31],a[252:255],v[148:151],0
s_cmp_gt_i32 s79,0
s_cbranch_scc1 L_ps5_%=
L_pr5_%=:
v_accvgpr_read_b32 v96,a64
v_mfma_f32_16x16x32_bf16 a[28:31],a[248:251],v[132:135],a[28:31]
v_exp_f32 v197,v197
v_mfma_f32_16x16x32_bf16 v[48:51],v[28:31],v[132:135],0
v_exp_f32 v198,v198
v_mfma_f32_16x16x32_bf16 v[48:51],v[28:31],v[148:151],v[48:51]
v_exp_f32 v199,v199
v_mfma_f32_16x16x32_bf16 v[228:231],a[128:131],v[152:155],v[44:47]
s_waitcnt vmcnt(8)
v_cvt_pk_bf16_f32 v196,v196,v197
v_cvt_pk_bf16_f32 v197,v198,v199
v_mfma_f32_16x16x32_bf16 v[228:231],a[132:135],v[184:187],v[228:231]
s_barrier
s_add_u32 m0,s49,s51
ds_read_b128 a[128:131],v1
v_mfma_f32_16x16x32_bf16 v[228:231],a[136:139],v[216:219],v[228:231]
ds_read_b128 a[132:135],v1 offset:256
v_accvgpr_read_b32 v97,a65
v_mfma_f32_16x16x32_bf16 v[228:231],a[140:143],v[248:251],v[228:231]
ds_read_b128 a[136:139],v1 offset:512
v_accvgpr_read_b32 v98,a66
v_mfma_f32_16x16x32_bf16 v[236:239],a[156:159],v[248:251],v[44:47]
buffer_load_dwordx4 v4,s[20:23],s47 offen nt lds
v_mfma_f32_16x16x32_bf16 v[236:239],a[152:155],v[216:219],v[236:239]
ds_read_b128 a[140:143],v1 offset:768
v_accvgpr_read_b32 v99,a67
v_mfma_f32_16x16x32_bf16 v[236:239],a[148:151],v[184:187],v[236:239]
ds_read_b128 a[156:159],v1 offset:4864
)A"
R"A(v_accvgpr_read_b32 v16,a70
v_mfma_f32_16x16x32_bf16 v[236:239],a[144:147],v[152:155],v[236:239]
ds_read_b128 a[152:155],v1 offset:4608
v_accvgpr_read_b32 v17,a71
v_mfma_f32_16x16x32_bf16 v[244:247],a[160:163],v[152:155],v[44:47]
ds_read_b128 a[148:151],v1 offset:4352
v_max_f32 v23,0x00800000,v60
v_mfma_f32_16x16x32_bf16 v[244:247],a[164:167],v[184:187],v[244:247]
ds_read_b128 a[144:147],v1 offset:4096
v_accvgpr_read_b32 v112,a96
v_mfma_f32_16x16x32_bf16 v[244:247],a[168:171],v[216:219],v[244:247]
ds_read_b128 a[160:163],v1 offset:8192
v_accvgpr_read_b32 v113,a97
v_mfma_f32_16x16x32_bf16 v[244:247],a[172:175],v[248:251],v[244:247]
ds_read_b128 a[164:167],v1 offset:8448
v_accvgpr_read_b32 v114,a98
v_mfma_f32_16x16x32_bf16 v[252:255],a[188:191],v[248:251],v[44:47]
ds_read_b128 a[168:171],v1 offset:8704
v_accvgpr_read_b32 v115,a99
v_mfma_f32_16x16x32_bf16 v[252:255],a[184:187],v[216:219],v[252:255]
ds_read_b128 a[172:175],v1 offset:8960
v_add_u32 v2,s48,v2
v_mfma_f32_16x16x32_bf16 v[252:255],a[180:183],v[184:187],v[252:255]
ds_read_b128 a[188:191],v1 offset:13056
v_add_u32 v3,s48,v3
v_mfma_f32_16x16x32_bf16 v[252:255],a[176:179],v[152:155],v[252:255]
s_cmp_eq_u32 s50,131072
s_cselect_b32 s62,163840,0
ds_read_b128 a[184:187],v1 offset:12800
v_mfma_f32_16x16x32_bf16 a[32:35],a[192:195],v[164:167],0
s_sub_u32 s48,32768,s62
s_add_u32 s50,s50,s48
ds_read_b128 a[180:183],v1 offset:12544
v_mfma_f32_16x16x32_bf16 a[32:35],a[196:199],v[180:183],a[32:35]
ds_read_b128 a[176:179],v1 offset:12288
v_add_u32 v1,s48,v1
v_mfma_f32_16x16x32_bf16 a[36:39],a[204:207],v[180:183],0
buffer_load_dwordx4 v4,s[20:23],s47 offen offset:1024 nt lds
v_mfma_f32_16x16x32_bf16 a[36:39],a[200:203],v[164:167],a[36:39]
v_exp_f32 v204,v204
v_mfma_f32_16x16x32_bf16 a[40:43],a[208:211],v[164:167],0
v_exp_f32 v205,v205
v_mfma_f32_16x16x32_bf16 a[40:43],a[212:215],v[180:183],a[40:43]
v_exp_f32 v206,v206
v_mfma_f32_16x16x32_bf16 a[44:47],a[220:223],v[180:183],0
v_exp_f32 v207,v207
v_mfma_f32_16x16x32_bf16 a[44:47],a[216:219],v[164:167],a[44:47]
v_exp_f32 v212,v212
v_mfma_f32_16x16x32_bf16 a[48:51],a[224:227],v[164:167],0
v_exp_f32 v213,v213
v_mfma_f32_16x16x32_bf16 a[48:51],a[228:231],v[180:183],a[48:51]
v_exp_f32 v214,v214
v_mfma_f32_16x16x32_bf16 a[52:55],a[236:239],v[180:183],0
v_exp_f32 v215,v215
v_mfma_f32_16x16x32_bf16 a[52:55],a[232:235],v[164:167],a[52:55]
v_cvt_pk_bf16_f32 v198,v204,v205
v_cvt_pk_bf16_f32 v199,v206,v207
v_mfma_f32_16x16x32_bf16 a[56:59],a[240:243],v[164:167],0
v_cvt_pk_bf16_f32 v212,v212,v213
v_cvt_pk_bf16_f32 v213,v214,v215
v_mfma_f32_16x16x32_bf16 a[56:59],a[244:247],v[180:183],a[56:59]
v_exp_f32 v220,v220
v_mfma_f32_16x16x32_bf16 a[60:63],a[252:255],v[180:183],0
v_exp_f32 v221,v221
v_mfma_f32_16x16x32_bf16 a[60:63],a[248:251],v[164:167],a[60:63]
v_exp_f32 v222,v222
v_mfma_f32_16x16x32_bf16 v[52:55],v[28:31],v[164:167],0
buffer_load_dwordx4 v5,s[20:23],s47 offen offset:2048 nt lds
v_mfma_f32_16x16x32_bf16 v[52:55],v[28:31],v[180:183],v[52:55]
s_waitcnt lgkmcnt(0)
v_exp_f32 v223,v223
v_mfma_f32_16x16x32_bf16 v[132:135],a[128:131],v[128:131],v[32:35]
v_cvt_pk_bf16_f32 v214,v220,v221
v_cvt_pk_bf16_f32 v215,v222,v223
v_mfma_f32_16x16x32_bf16 v[132:135],a[132:135],v[160:163],v[132:135]
v_rcp_f32 v22,v22
v_mfma_f32_16x16x32_bf16 v[132:135],a[136:139],v[192:195],v[132:135]
v_mul_f32 v96,v22,v96
v_mul_f32 v97,v22,v97
v_mfma_f32_16x16x32_bf16 v[132:135],a[140:143],v[224:227],v[132:135]
v_cvt_pk_bf16_f32 v96,v96,v97
v_mul_f32 v98,v22,v98
v_mfma_f32_16x16x32_bf16 v[140:143],a[156:159],v[224:227],v[32:35]
v_mul_f32 v99,v22,v99
v_cvt_pk_bf16_f32 v97,v98,v99
v_mfma_f32_16x16x32_bf16 v[140:143],a[152:155],v[192:195],v[140:143]
v_accvgpr_read_b32 v98,a68
v_accvgpr_read_b32 v99,a69
v_mfma_f32_16x16x32_bf16 v[140:143],a[148:151],v[160:163],v[140:143]
v_accvgpr_read_b32 v100,a72
v_accvgpr_read_b32 v101,a73
v_accvgpr_read_b32 v102,a74
v_accvgpr_read_b32 v103,a75
v_accvgpr_read_b32 v12,a76
v_accvgpr_read_b32 v13,a77
v_accvgpr_read_b32 v14,a78
v_accvgpr_read_b32 v15,a79
v_pk_mul_f32 v[100:101],v[22:23],v[100:101] op_sel:[0,0] op_sel_hi:[0,1]
v_pk_mul_f32 v[102:103],v[22:23],v[102:103] op_sel:[0,0] op_sel_hi:[0,1]
v_pk_mul_f32 v[12:13],v[22:23],v[12:13] op_sel:[0,0] op_sel_hi:[0,1]
v_pk_mul_f32 v[14:15],v[22:23],v[14:15] op_sel:[0,0] op_sel_hi:[0,1]
v_cvt_pk_bf16_f32 v100,v100,v101
v_cvt_pk_bf16_f32 v101,v102,v103
v_cvt_pk_bf16_f32 v102,v12,v13
v_cvt_pk_bf16_f32 v103,v14,v15
v_mfma_f32_16x16x32_bf16 v[140:143],a[144:147],v[128:131],v[140:143]
v_accvgpr_read_b32 v104,a80
v_accvgpr_read_b32 v105,a81
v_accvgpr_read_b32 v106,a82
v_accvgpr_read_b32 v107,a83
v_accvgpr_read_b32 v12,a84
v_accvgpr_read_b32 v13,a85
v_accvgpr_read_b32 v14,a86
v_accvgpr_read_b32 v15,a87
v_pk_mul_f32 v[104:105],v[22:23],v[104:105] op_sel:[0,0] op_sel_hi:[0,1]
v_pk_mul_f32 v[106:107],v[22:23],v[106:107] op_sel:[0,0] op_sel_hi:[0,1]
v_pk_mul_f32 v[12:13],v[22:23],v[12:13] op_sel:[0,0] op_sel_hi:[0,1]
v_pk_mul_f32 v[14:15],v[22:23],v[14:15] op_sel:[0,0] op_sel_hi:[0,1]
v_cvt_pk_bf16_f32 v104,v104,v105
v_cvt_pk_bf16_f32 v105,v106,v107
v_cvt_pk_bf16_f32 v106,v12,v13
v_cvt_pk_bf16_f32 v107,v14,v15
v_mfma_f32_16x16x32_bf16 v[148:151],a[160:163],v[128:131],v[32:35]
v_accvgpr_read_b32 v108,a88
v_accvgpr_read_b32 v109,a89
v_accvgpr_read_b32 v110,a90
v_accvgpr_read_b32 v111,a91
v_accvgpr_read_b32 v12,a92
v_accvgpr_read_b32 v13,a93
v_accvgpr_read_b32 v14,a94
v_accvgpr_read_b32 v15,a95
v_pk_mul_f32 v[108:109],v[22:23],v[108:109] op_sel:[0,0] op_sel_hi:[0,1]
v_pk_mul_f32 v[110:111],v[22:23],v[110:111] op_sel:[0,0] op_sel_hi:[0,1]
v_pk_mul_f32 v[12:13],v[22:23],v[12:13] op_sel:[0,0] op_sel_hi:[0,1]
v_pk_mul_f32 v[14:15],v[22:23],v[14:15] op_sel:[0,0] op_sel_hi:[0,1]
v_cvt_pk_bf16_f32 v108,v108,v109
v_cvt_pk_bf16_f32 v109,v110,v111
v_cvt_pk_bf16_f32 v110,v12,v13
v_cvt_pk_bf16_f32 v111,v14,v15
v_mfma_f32_16x16x32_bf16 v[148:151],a[164:167],v[160:163],v[148:151]
v_cmp_nlt_f32 s[58:59],v56,s63
s_nop 1
s_or_b64 s[56:57],s[56:57],s[58:59]
v_mfma_f32_16x16x32_bf16 v[148:151],a[168:171],v[192:195],v[148:151]
v_exp_f32 v228,v228
v_mfma_f32_16x16x32_bf16 v[148:151],a[172:175],v[224:227],v[148:151]
v_exp_f32 v229,v229
v_mfma_f32_16x16x32_bf16 v[156:159],a[188:191],v[224:227],v[32:35]
buffer_load_dwordx4 v5,s[20:23],s47 offen offset:3072 nt lds
v_mfma_f32_16x16x32_bf16 v[156:159],a[184:187],v[192:195],v[156:159]
v_exp_f32 v230,v230
v_mfma_f32_16x16x32_bf16 v[156:159],a[180:183],v[160:163],v[156:159]
v_exp_f32 v231,v231
v_mfma_f32_16x16x32_bf16 v[156:159],a[176:179],v[128:131],v[156:159]
v_cvt_pk_bf16_f32 v228,v228,v229
v_cvt_pk_bf16_f32 v229,v230,v231
v_mfma_f32_16x16x32_bf16 a[64:67],a[192:195],v[196:199],0
v_exp_f32 v236,v236
v_mfma_f32_16x16x32_bf16 a[64:67],a[196:199],v[212:215],a[64:67]
v_exp_f32 v237,v237
v_mfma_f32_16x16x32_bf16 a[68:71],a[204:207],v[212:215],0
v_exp_f32 v238,v238
v_mfma_f32_16x16x32_bf16 a[68:71],a[200:203],v[196:199],a[68:71]
v_exp_f32 v239,v239
v_mfma_f32_16x16x32_bf16 a[72:75],a[208:211],v[196:199],0
v_exp_f32 v244,v244
v_mfma_f32_16x16x32_bf16 a[72:75],a[212:215],v[212:215],a[72:75]
v_exp_f32 v245,v245
v_mfma_f32_16x16x32_bf16 a[76:79],a[220:223],v[212:215],0
v_exp_f32 v246,v246
v_mfma_f32_16x16x32_bf16 a[76:79],a[216:219],v[196:199],a[76:79]
s_add_u32 m0,s49,s52
v_exp_f32 v247,v247
v_mfma_f32_16x16x32_bf16 a[80:83],a[224:227],v[196:199],0
s_add_u32 s49,s49,32768
v_cvt_pk_bf16_f32 v230,v236,v237
v_cvt_pk_bf16_f32 v231,v238,v239
v_mfma_f32_16x16x32_bf16 a[80:83],a[228:231],v[212:215],a[80:83]
v_cvt_pk_bf16_f32 v244,v244,v245
v_cvt_pk_bf16_f32 v245,v246,v247
v_mfma_f32_16x16x32_bf16 a[84:87],a[236:239],v[212:215],0
buffer_load_dwordx4 v4,s[24:27],s47 offen nt lds
v_mfma_f32_16x16x32_bf16 a[84:87],a[232:235],v[196:199],a[84:87]
v_exp_f32 v252,v252
v_mfma_f32_16x16x32_bf16 a[88:91],a[240:243],v[196:199],0
v_exp_f32 v253,v253
v_mfma_f32_16x16x32_bf16 a[88:91],a[244:247],v[212:215],a[88:91]
v_exp_f32 v254,v254
v_mfma_f32_16x16x32_bf16 a[92:95],a[252:255],v[212:215],0
v_exp_f32 v255,v255
v_mfma_f32_16x16x32_bf16 a[92:95],a[248:251],v[196:199],a[92:95]
v_rcp_f32 v23,v23
v_mfma_f32_16x16x32_bf16 v[56:59],v[28:31],v[196:199],0
v_cvt_pk_bf16_f32 v246,v252,v253
v_cvt_pk_bf16_f32 v247,v254,v255
v_mfma_f32_16x16x32_bf16 v[56:59],v[28:31],v[212:215],v[56:59]
v_mul_f32 v112,v23,v112
v_mul_f32 v113,v23,v113
v_mfma_f32_16x16x32_bf16 v[164:167],a[128:131],v[136:139],v[36:39]
v_cvt_pk_bf16_f32 v112,v112,v113
v_mul_f32 v114,v23,v114
v_mfma_f32_16x16x32_bf16 v[164:167],a[132:135],v[168:171],v[164:167]
v_mul_f32 v115,v23,v115
v_cvt_pk_bf16_f32 v113,v114,v115
v_mfma_f32_16x16x32_bf16 v[164:167],a[136:139],v[200:203],v[164:167]
v_accvgpr_read_b32 v114,a100
v_accvgpr_read_b32 v115,a101
v_mfma_f32_16x16x32_bf16 v[164:167],a[140:143],v[232:235],v[164:167]
v_accvgpr_read_b32 v12,a102
v_accvgpr_read_b32 v13,a103
v_mfma_f32_16x16x32_bf16 v[172:175],a[156:159],v[232:235],v[36:39]
v_mul_f32 v114,v23,v114
v_mul_f32 v115,v23,v115
v_mfma_f32_16x16x32_bf16 v[172:175],a[152:155],v[200:203],v[172:175]
v_mul_f32 v12,v23,v12
v_mul_f32 v13,v23,v13
v_mfma_f32_16x16x32_bf16 v[172:175],a[148:151],v[168:171],v[172:175]
buffer_load_dwordx4 v5,s[24:27],s47 offen offset:1024 nt lds
v_mfma_f32_16x16x32_bf16 v[172:175],a[144:147],v[136:139],v[172:175]
v_cvt_pk_bf16_f32 v114,v114,v115
v_cvt_pk_bf16_f32 v115,v12,v13
v_mfma_f32_16x16x32_bf16 v[180:183],a[160:163],v[136:139],v[36:39]
v_accvgpr_read_b32 v116,a104
v_accvgpr_read_b32 v117,a105
v_accvgpr_read_b32 v118,a106
v_accvgpr_read_b32 v119,a107
v_accvgpr_read_b32 v12,a108
v_accvgpr_read_b32 v13,a109
v_accvgpr_read_b32 v14,a110
v_accvgpr_read_b32 v15,a111
v_pk_mul_f32 v[116:117],v[22:23],v[116:117] op_sel:[1,0] op_sel_hi:[1,1]
v_pk_mul_f32 v[118:119],v[22:23],v[118:119] op_sel:[1,0] op_sel_hi:[1,1]
v_pk_mul_f32 v[12:13],v[22:23],v[12:13] op_sel:[1,0] op_sel_hi:[1,1]
v_pk_mul_f32 v[14:15],v[22:23],v[14:15] op_sel:[1,0] op_sel_hi:[1,1]
v_cvt_pk_bf16_f32 v116,v116,v117
v_cvt_pk_bf16_f32 v117,v118,v119
v_cvt_pk_bf16_f32 v118,v12,v13
v_cvt_pk_bf16_f32 v119,v14,v15
v_mfma_f32_16x16x32_bf16 v[180:183],a[164:167],v[168:171],v[180:183]
v_accvgpr_read_b32 v120,a112
v_accvgpr_read_b32 v121,a113
v_accvgpr_read_b32 v122,a114
v_accvgpr_read_b32 v123,a115
v_accvgpr_read_b32 v12,a116
v_accvgpr_read_b32 v13,a117
v_accvgpr_read_b32 v14,a118
v_accvgpr_read_b32 v15,a119
v_pk_mul_f32 v[120:121],v[22:23],v[120:121] op_sel:[1,0] op_sel_hi:[1,1]
v_pk_mul_f32 v[122:123],v[22:23],v[122:123] op_sel:[1,0] op_sel_hi:[1,1]
v_pk_mul_f32 v[12:13],v[22:23],v[12:13] op_sel:[1,0] op_sel_hi:[1,1]
v_pk_mul_f32 v[14:15],v[22:23],v[14:15] op_sel:[1,0] op_sel_hi:[1,1]
v_cvt_pk_bf16_f32 v120,v120,v121
v_cvt_pk_bf16_f32 v121,v122,v123
v_cvt_pk_bf16_f32 v122,v12,v13
v_cvt_pk_bf16_f32 v123,v14,v15
v_mfma_f32_16x16x32_bf16 v[180:183],a[168:171],v[200:203],v[180:183]
v_accvgpr_read_b32 v124,a120
v_accvgpr_read_b32 v125,a121
v_accvgpr_read_b32 v126,a122
v_accvgpr_read_b32 v127,a123
v_accvgpr_read_b32 v12,a124
v_accvgpr_read_b32 v13,a125
v_accvgpr_read_b32 v14,a126
v_accvgpr_read_b32 v15,a127
v_pk_mul_f32 v[124:125],v[22:23],v[124:125] op_sel:[1,0] op_sel_hi:[1,1]
v_pk_mul_f32 v[126:127],v[22:23],v[126:127] op_sel:[1,0] op_sel_hi:[1,1]
v_pk_mul_f32 v[12:13],v[22:23],v[12:13] op_sel:[1,0] op_sel_hi:[1,1]
v_pk_mul_f32 v[14:15],v[22:23],v[14:15] op_sel:[1,0] op_sel_hi:[1,1]
v_cvt_pk_bf16_f32 v124,v124,v125
v_cvt_pk_bf16_f32 v125,v126,v127
v_cvt_pk_bf16_f32 v126,v12,v13
v_cvt_pk_bf16_f32 v127,v14,v15
v_mfma_f32_16x16x32_bf16 v[180:183],a[172:175],v[232:235],v[180:183]
v_cmp_nlt_f32 s[58:59],v60,s63
s_nop 1
s_or_b64 s[56:57],s[56:57],s[58:59]
v_mfma_f32_16x16x32_bf16 v[188:191],a[188:191],v[232:235],v[36:39]
v_exp_f32 v132,v132
v_mfma_f32_16x16x32_bf16 v[188:191],a[184:187],v[200:203],v[188:191]
v_exp_f32 v133,v133
v_mfma_f32_16x16x32_bf16 v[188:191],a[180:183],v[168:171],v[188:191]
v_exp_f32 v134,v134
v_mfma_f32_16x16x32_bf16 v[188:191],a[176:179],v[136:139],v[188:191]
v_exp_f32 v135,v135
v_mfma_f32_16x16x32_bf16 a[96:99],a[192:195],v[228:231],0
ds_read_b64_tr_b16 a[192:193],v2
ds_read_b64_tr_b16 a[194:195],v2 offset:4096
)A"
R"A(v_mfma_f32_16x16x32_bf16 a[96:99],a[196:199],v[244:247],a[96:99]
ds_read_b64_tr_b16 a[196:197],v2 offset:8192
ds_read_b64_tr_b16 a[198:199],v2 offset:12288
v_mfma_f32_16x16x32_bf16 a[100:103],a[204:207],v[244:247],0
ds_read_b64_tr_b16 a[204:205],v3 offset:8192
ds_read_b64_tr_b16 a[206:207],v3 offset:12288
v_mfma_f32_16x16x32_bf16 a[100:103],a[200:203],v[228:231],a[100:103]
ds_read_b64_tr_b16 a[200:201],v3
ds_read_b64_tr_b16 a[202:203],v3 offset:4096
v_mfma_f32_16x16x32_bf16 a[104:107],a[208:211],v[228:231],0
buffer_load_dwordx4 v4,s[24:27],s47 offen offset:2048 nt lds
v_mfma_f32_16x16x32_bf16 a[104:107],a[212:215],v[244:247],a[104:107]
ds_read_b64_tr_b16 a[208:209],v2 offset:256
ds_read_b64_tr_b16 a[210:211],v2 offset:4352
v_mfma_f32_16x16x32_bf16 a[108:111],a[220:223],v[244:247],0
ds_read_b64_tr_b16 a[212:213],v2 offset:8448
v_cvt_pk_bf16_f32 v132,v132,v133
v_mfma_f32_16x16x32_bf16 a[108:111],a[216:219],v[228:231],a[108:111]
v_exp_f32 v140,v140
v_mfma_f32_16x16x32_bf16 a[112:115],a[224:227],v[228:231],0
v_exp_f32 v141,v141
v_mfma_f32_16x16x32_bf16 a[112:115],a[228:231],v[244:247],a[112:115]
v_exp_f32 v142,v142
v_mfma_f32_16x16x32_bf16 a[116:119],a[236:239],v[244:247],0
v_exp_f32 v143,v143
v_mfma_f32_16x16x32_bf16 a[116:119],a[232:235],v[228:231],a[116:119]
v_exp_f32 v148,v148
v_mfma_f32_16x16x32_bf16 a[120:123],a[240:243],v[228:231],0
v_exp_f32 v149,v149
v_mfma_f32_16x16x32_bf16 a[120:123],a[244:247],v[244:247],a[120:123]
ds_read_b64_tr_b16 a[214:215],v2 offset:12544
v_cvt_pk_bf16_f32 v133,v134,v135
v_mfma_f32_16x16x32_bf16 a[124:127],a[252:255],v[244:247],0
v_exp_f32 v150,v150
v_mfma_f32_16x16x32_bf16 a[124:127],a[248:251],v[228:231],a[124:127]
v_exp_f32 v151,v151
v_mfma_f32_16x16x32_bf16 v[60:63],v[28:31],v[228:231],0
v_cvt_pk_bf16_f32 v134,v140,v141
v_cvt_pk_bf16_f32 v135,v142,v143
v_mfma_f32_16x16x32_bf16 v[60:63],v[28:31],v[244:247],v[60:63]
ds_read_b64_tr_b16 a[220:221],v3 offset:8448
v_cvt_pk_bf16_f32 v148,v148,v149
v_mfma_f32_16x16x32_bf16 v[196:199],a[128:131],v[144:147],v[40:43]
buffer_load_dwordx4 v5,s[24:27],s47 offen offset:3072 nt lds
v_mfma_f32_16x16x32_bf16 v[196:199],a[132:135],v[176:179],v[196:199]
s_add_u32 s47,s47,16384
v_exp_f32 v156,v156
v_mfma_f32_16x16x32_bf16 v[196:199],a[136:139],v[208:211],v[196:199]
s_and_b32 s47,s47,0x3ffff
v_exp_f32 v157,v157
v_mfma_f32_16x16x32_bf16 v[196:199],a[140:143],v[240:243],v[196:199]
s_or_b32 s47,s47,s46
v_exp_f32 v158,v158
v_mfma_f32_16x16x32_bf16 v[204:207],a[156:159],v[240:243],v[40:43]
v_exp_f32 v159,v159
v_mfma_f32_16x16x32_bf16 v[204:207],a[152:155],v[208:211],v[204:207]
ds_read_b64_tr_b16 a[222:223],v3 offset:12544
v_cvt_pk_bf16_f32 v149,v150,v151
v_mfma_f32_16x16x32_bf16 v[204:207],a[148:151],v[176:179],v[204:207]
v_cvt_pk_bf16_f32 v150,v156,v157
v_cvt_pk_bf16_f32 v151,v158,v159
v_mfma_f32_16x16x32_bf16 v[204:207],a[144:147],v[144:147],v[204:207]
ds_read_b64_tr_b16 a[216:217],v3 offset:256
ds_read_b64_tr_b16 a[218:219],v3 offset:4352
v_mfma_f32_16x16x32_bf16 v[212:215],a[160:163],v[144:147],v[40:43]
ds_read_b64_tr_b16 a[224:225],v2 offset:512
ds_read_b64_tr_b16 a[226:227],v2 offset:4608
v_mfma_f32_16x16x32_bf16 v[212:215],a[164:167],v[176:179],v[212:215]
ds_read_b64_tr_b16 a[228:229],v2 offset:8704
ds_read_b64_tr_b16 a[230:231],v2 offset:12800
v_mfma_f32_16x16x32_bf16 v[212:215],a[168:171],v[208:211],v[212:215]
ds_read_b64_tr_b16 a[236:237],v3 offset:8704
ds_read_b64_tr_b16 a[238:239],v3 offset:12800
v_mfma_f32_16x16x32_bf16 v[212:215],a[172:175],v[240:243],v[212:215]
ds_read_b64_tr_b16 a[232:233],v3 offset:512
ds_read_b64_tr_b16 a[234:235],v3 offset:4608
v_mfma_f32_16x16x32_bf16 v[220:223],a[188:191],v[240:243],v[40:43]
ds_read_b64_tr_b16 a[240:241],v2 offset:768
ds_read_b64_tr_b16 a[242:243],v2 offset:4864
v_mfma_f32_16x16x32_bf16 v[220:223],a[184:187],v[208:211],v[220:223]
ds_read_b64_tr_b16 a[244:245],v2 offset:8960
ds_read_b64_tr_b16 a[246:247],v2 offset:13056
v_mfma_f32_16x16x32_bf16 v[220:223],a[180:183],v[176:179],v[220:223]
ds_read_b64_tr_b16 a[252:253],v3 offset:8960
ds_read_b64_tr_b16 a[254:255],v3 offset:13056
v_mfma_f32_16x16x32_bf16 v[220:223],a[176:179],v[144:147],v[220:223]
s_waitcnt lgkmcnt(14)
ds_read_b64_tr_b16 a[248:249],v3 offset:768
ds_read_b64_tr_b16 a[250:251],v3 offset:4864
v_mfma_f32_16x16x32_bf16 a[0:3],a[192:195],v[132:135],a[0:3]
v_mul_f32 v98,v22,v98
v_mul_f32 v99,v22,v99
v_mfma_f32_16x16x32_bf16 a[0:3],a[196:199],v[148:151],a[0:3]
v_exp_f32 v164,v164
v_mfma_f32_16x16x32_bf16 a[4:7],a[204:207],v[148:151],a[4:7]
v_exp_f32 v165,v165
v_mfma_f32_16x16x32_bf16 a[4:7],a[200:203],v[132:135],a[4:7]
v_exp_f32 v180,v180
v_mfma_f32_16x16x32_bf16 a[8:11],a[208:211],v[132:135],a[8:11]
s_memrealtime s[76:77]
v_exp_f32 v181,v181
v_mfma_f32_16x16x32_bf16 a[8:11],a[212:215],v[148:151],a[8:11]
v_mul_f32 v16,v22,v16
v_mul_f32 v17,v22,v17
v_mfma_f32_16x16x32_bf16 a[12:15],a[220:223],v[148:151],a[12:15]
v_cvt_pk_bf16_f32 v98,v98,v99
v_cvt_pk_bf16_f32 v99,v16,v17
v_mfma_f32_16x16x32_bf16 a[12:15],a[216:219],v[132:135],a[12:15]
s_waitcnt lgkmcnt(0)
v_exp_f32 v166,v166
v_mfma_f32_16x16x32_bf16 a[16:19],a[224:227],v[132:135],a[16:19]
v_exp_f32 v167,v167
v_mfma_f32_16x16x32_bf16 a[16:19],a[228:231],v[148:151],a[16:19]
v_exp_f32 v182,v182
v_mfma_f32_16x16x32_bf16 a[20:23],a[236:239],v[148:151],a[20:23]
v_exp_f32 v183,v183
v_mfma_f32_16x16x32_bf16 a[20:23],a[232:235],v[132:135],a[20:23]
s_waitcnt lgkmcnt(0)
v_cvt_pk_bf16_f32 v164,v164,v165
v_cvt_pk_bf16_f32 v180,v180,v181
v_mfma_f32_16x16x32_bf16 a[24:27],a[240:243],v[132:135],a[24:27]
s_sub_u32 s79,s78,s76
v_exp_f32 v172,v172
v_mfma_f32_16x16x32_bf16 a[24:27],a[244:247],v[148:151],a[24:27]
s_add_u32 s78,s76,s60
v_exp_f32 v173,v173
v_mfma_f32_16x16x32_bf16 a[28:31],a[252:255],v[148:151],a[28:31]
s_cmp_gt_i32 s79,0
s_cbranch_scc1 L_ps6_%=
L_pr6_%=:
v_cvt_pk_bf16_f32 v165,v166,v167
v_mfma_f32_16x16x32_bf16 a[28:31],a[248:251],v[132:135],a[28:31]
v_exp_f32 v174,v174
v_exp_f32 v175,v175
v_exp_f32 v188,v188
v_exp_f32 v189,v189
v_exp_f32 v190,v190
v_exp_f32 v191,v191
v_mfma_f32_16x16x32_bf16 v[48:51],v[28:31],v[132:135],v[48:51]
s_cmp_lg_u64 s[56:57],0
s_cbranch_scc1 L_slow0_%=
L_sret0_%=:
s_cmp_eq_u32 s49,163840
s_cselect_b32 s49,0,s49
v_cvt_pk_bf16_f32 v181,v182,v183
v_cvt_pk_bf16_f32 v166,v172,v173
v_cvt_pk_bf16_f32 v167,v174,v175
v_cvt_pk_bf16_f32 v182,v188,v189
v_cvt_pk_bf16_f32 v183,v190,v191
v_mfma_f32_16x16x32_bf16 v[48:51],v[28:31],v[148:151],v[48:51]
s_mov_b32 s53,6
L_att1_%=:
v_mfma_f32_16x16x32_bf16 v[228:231],a[128:131],v[152:155],v[44:47]
s_waitcnt vmcnt(8) lgkmcnt(0)
v_exp_f32 v196,v196
v_mfma_f32_16x16x32_bf16 v[228:231],a[132:135],v[184:187],v[228:231]
s_barrier
s_add_u32 m0,s49,s51
ds_read_b128 a[128:131],v1
v_mfma_f32_16x16x32_bf16 v[228:231],a[136:139],v[216:219],v[228:231]
s_mov_b32 s90,L_out_end_%=-L_pc_%=+128
v_exp_f32 v197,v197
v_mfma_f32_16x16x32_bf16 v[228:231],a[140:143],v[248:251],v[228:231]
s_add_u32 s54,s54,16384
ds_read_b128 a[132:135],v1 offset:256
v_cvt_pk_bf16_f32 v196,v196,v197
v_mfma_f32_16x16x32_bf16 v[236:239],a[156:159],v[248:251],v[44:47]
s_sub_u32 s53,s53,1
buffer_load_dwordx4 v4,s[20:23],s47 offen nt lds
v_mfma_f32_16x16x32_bf16 v[236:239],a[152:155],v[216:219],v[236:239]
s_and_b32 s46,s54,0xfffc0000
ds_read_b128 a[136:139],v1 offset:512
v_add_u32 v2,s48,v2
v_mfma_f32_16x16x32_bf16 v[236:239],a[148:151],v[184:187],v[236:239]
v_exp_f32 v198,v198
v_mfma_f32_16x16x32_bf16 v[236:239],a[144:147],v[152:155],v[236:239]
ds_read_b128 a[140:143],v1 offset:768
v_add_u32 v3,s48,v3
v_mfma_f32_16x16x32_bf16 v[244:247],a[160:163],v[152:155],v[44:47]
v_exp_f32 v199,v199
v_mfma_f32_16x16x32_bf16 v[244:247],a[164:167],v[184:187],v[244:247]
ds_read_b128 a[156:159],v1 offset:4864
v_cvt_pk_bf16_f32 v197,v198,v199
v_mfma_f32_16x16x32_bf16 v[244:247],a[168:171],v[216:219],v[244:247]
v_exp_f32 v204,v204
v_mfma_f32_16x16x32_bf16 v[244:247],a[172:175],v[248:251],v[244:247]
s_cmp_eq_u32 s50,131072
s_cselect_b32 s62,163840,0
ds_read_b128 a[152:155],v1 offset:4608
v_mfma_f32_16x16x32_bf16 v[252:255],a[188:191],v[248:251],v[44:47]
s_sub_u32 s48,32768,s62
v_exp_f32 v205,v205
v_mfma_f32_16x16x32_bf16 v[252:255],a[184:187],v[216:219],v[252:255]
s_add_u32 s50,s50,s48
ds_read_b128 a[148:151],v1 offset:4352
v_cvt_pk_bf16_f32 v198,v204,v205
v_mfma_f32_16x16x32_bf16 v[252:255],a[180:183],v[184:187],v[252:255]
v_exp_f32 v206,v206
v_mfma_f32_16x16x32_bf16 v[252:255],a[176:179],v[152:155],v[252:255]
ds_read_b128 a[144:147],v1 offset:4096
v_mfma_f32_16x16x32_bf16 a[32:35],a[192:195],v[164:167],a[32:35]
v_exp_f32 v207,v207
v_mfma_f32_16x16x32_bf16 a[32:35],a[196:199],v[180:183],a[32:35]
ds_read_b128 a[160:163],v1 offset:8192
v_cvt_pk_bf16_f32 v199,v206,v207
v_mfma_f32_16x16x32_bf16 a[36:39],a[204:207],v[180:183],a[36:39]
buffer_load_dwordx4 v4,s[20:23],s47 offen offset:1024 nt lds
v_mfma_f32_16x16x32_bf16 a[36:39],a[200:203],v[164:167],a[36:39]
ds_read_b128 a[164:167],v1 offset:8448
v_mfma_f32_16x16x32_bf16 a[40:43],a[208:211],v[164:167],a[40:43]
v_exp_f32 v212,v212
v_mfma_f32_16x16x32_bf16 a[40:43],a[212:215],v[180:183],a[40:43]
ds_read_b128 a[168:171],v1 offset:8704
v_mfma_f32_16x16x32_bf16 a[44:47],a[220:223],v[180:183],a[44:47]
v_exp_f32 v213,v213
v_mfma_f32_16x16x32_bf16 a[44:47],a[216:219],v[164:167],a[44:47]
ds_read_b128 a[172:175],v1 offset:8960
v_cvt_pk_bf16_f32 v212,v212,v213
v_mfma_f32_16x16x32_bf16 a[48:51],a[224:227],v[164:167],a[48:51]
v_exp_f32 v214,v214
v_mfma_f32_16x16x32_bf16 a[48:51],a[228:231],v[180:183],a[48:51]
ds_read_b128 a[188:191],v1 offset:13056
v_mfma_f32_16x16x32_bf16 a[52:55],a[236:239],v[180:183],a[52:55]
v_exp_f32 v215,v215
v_mfma_f32_16x16x32_bf16 a[52:55],a[232:235],v[164:167],a[52:55]
ds_read_b128 a[184:187],v1 offset:12800
v_cvt_pk_bf16_f32 v213,v214,v215
v_mfma_f32_16x16x32_bf16 a[56:59],a[240:243],v[164:167],a[56:59]
v_exp_f32 v220,v220
v_mfma_f32_16x16x32_bf16 a[56:59],a[244:247],v[180:183],a[56:59]
ds_read_b128 a[180:183],v1 offset:12544
v_mfma_f32_16x16x32_bf16 a[60:63],a[252:255],v[180:183],a[60:63]
v_exp_f32 v221,v221
v_mfma_f32_16x16x32_bf16 a[60:63],a[248:251],v[164:167],a[60:63]
ds_read_b128 a[176:179],v1 offset:12288
v_cvt_pk_bf16_f32 v214,v220,v221
v_mfma_f32_16x16x32_bf16 v[52:55],v[28:31],v[164:167],v[52:55]
buffer_load_dwordx4 v5,s[20:23],s47 offen offset:2048 nt lds
v_mfma_f32_16x16x32_bf16 v[52:55],v[28:31],v[180:183],v[52:55]
s_waitcnt lgkmcnt(3)
v_exp_f32 v222,v222
v_mfma_f32_16x16x32_bf16 v[132:135],a[128:131],v[128:131],v[32:35]
v_exp_f32 v223,v223
v_mfma_f32_16x16x32_bf16 v[132:135],a[132:135],v[160:163],v[132:135]
v_cvt_pk_bf16_f32 v215,v222,v223
v_add_u32 v1,s48,v1
v_mfma_f32_16x16x32_bf16 v[132:135],a[136:139],v[192:195],v[132:135]
v_exp_f32 v228,v228
v_mfma_f32_16x16x32_bf16 v[132:135],a[140:143],v[224:227],v[132:135]
v_exp_f32 v229,v229
v_mfma_f32_16x16x32_bf16 v[140:143],a[156:159],v[224:227],v[32:35]
v_exp_f32 v230,v230
v_mfma_f32_16x16x32_bf16 v[140:143],a[152:155],v[192:195],v[140:143]
v_exp_f32 v231,v231
v_mfma_f32_16x16x32_bf16 v[140:143],a[148:151],v[160:163],v[140:143]
v_cvt_pk_bf16_f32 v228,v228,v229
v_cvt_pk_bf16_f32 v229,v230,v231
v_mfma_f32_16x16x32_bf16 v[140:143],a[144:147],v[128:131],v[140:143]
v_exp_f32 v236,v236
v_mfma_f32_16x16x32_bf16 v[148:151],a[160:163],v[128:131],v[32:35]
v_exp_f32 v237,v237
v_mfma_f32_16x16x32_bf16 v[148:151],a[164:167],v[160:163],v[148:151]
v_exp_f32 v238,v238
v_mfma_f32_16x16x32_bf16 v[148:151],a[168:171],v[192:195],v[148:151]
v_exp_f32 v239,v239
v_mfma_f32_16x16x32_bf16 v[148:151],a[172:175],v[224:227],v[148:151]
v_exp_f32 v244,v244
v_mfma_f32_16x16x32_bf16 v[156:159],a[188:191],v[224:227],v[32:35]
s_waitcnt lgkmcnt(0)
buffer_load_dwordx4 v5,s[20:23],s47 offen offset:3072 nt lds
v_mfma_f32_16x16x32_bf16 v[156:159],a[184:187],v[192:195],v[156:159]
v_exp_f32 v245,v245
v_mfma_f32_16x16x32_bf16 v[156:159],a[180:183],v[160:163],v[156:159]
v_exp_f32 v246,v246
v_mfma_f32_16x16x32_bf16 v[156:159],a[176:179],v[128:131],v[156:159]
v_exp_f32 v247,v247
v_mfma_f32_16x16x32_bf16 a[64:67],a[192:195],v[196:199],a[64:67]
v_cvt_pk_bf16_f32 v230,v236,v237
v_cvt_pk_bf16_f32 v231,v238,v239
)A"
R"A(v_mfma_f32_16x16x32_bf16 a[64:67],a[196:199],v[212:215],a[64:67]
v_cvt_pk_bf16_f32 v244,v244,v245
v_cvt_pk_bf16_f32 v245,v246,v247
v_mfma_f32_16x16x32_bf16 a[68:71],a[204:207],v[212:215],a[68:71]
v_exp_f32 v252,v252
v_mfma_f32_16x16x32_bf16 a[68:71],a[200:203],v[196:199],a[68:71]
v_exp_f32 v253,v253
v_mfma_f32_16x16x32_bf16 a[72:75],a[208:211],v[196:199],a[72:75]
v_exp_f32 v254,v254
v_mfma_f32_16x16x32_bf16 a[72:75],a[212:215],v[212:215],a[72:75]
v_exp_f32 v255,v255
v_mfma_f32_16x16x32_bf16 a[76:79],a[220:223],v[212:215],a[76:79]
v_cvt_pk_bf16_f32 v246,v252,v253
v_cvt_pk_bf16_f32 v247,v254,v255
v_mfma_f32_16x16x32_bf16 a[76:79],a[216:219],v[196:199],a[76:79]
s_add_u32 m0,s49,s52
v_exp_f32 v132,v132
v_mfma_f32_16x16x32_bf16 a[80:83],a[224:227],v[196:199],a[80:83]
s_add_u32 s49,s49,32768
v_exp_f32 v133,v133
v_mfma_f32_16x16x32_bf16 a[80:83],a[228:231],v[212:215],a[80:83]
v_exp_f32 v134,v134
v_mfma_f32_16x16x32_bf16 a[84:87],a[236:239],v[212:215],a[84:87]
buffer_load_dwordx4 v4,s[24:27],s47 offen nt lds
v_mfma_f32_16x16x32_bf16 a[84:87],a[232:235],v[196:199],a[84:87]
v_exp_f32 v135,v135
v_mfma_f32_16x16x32_bf16 a[88:91],a[240:243],v[196:199],a[88:91]
v_cvt_pk_bf16_f32 v132,v132,v133
v_cvt_pk_bf16_f32 v133,v134,v135
v_mfma_f32_16x16x32_bf16 a[88:91],a[244:247],v[212:215],a[88:91]
v_exp_f32 v140,v140
v_mfma_f32_16x16x32_bf16 a[92:95],a[252:255],v[212:215],a[92:95]
v_exp_f32 v141,v141
v_mfma_f32_16x16x32_bf16 a[92:95],a[248:251],v[196:199],a[92:95]
v_exp_f32 v142,v142
v_mfma_f32_16x16x32_bf16 v[56:59],v[28:31],v[196:199],v[56:59]
v_exp_f32 v143,v143
v_mfma_f32_16x16x32_bf16 v[56:59],v[28:31],v[212:215],v[56:59]
v_exp_f32 v148,v148
v_mfma_f32_16x16x32_bf16 v[164:167],a[128:131],v[136:139],v[36:39]
v_exp_f32 v149,v149
v_mfma_f32_16x16x32_bf16 v[164:167],a[132:135],v[168:171],v[164:167]
v_exp_f32 v150,v150
v_mfma_f32_16x16x32_bf16 v[164:167],a[136:139],v[200:203],v[164:167]
v_exp_f32 v151,v151
v_mfma_f32_16x16x32_bf16 v[164:167],a[140:143],v[232:235],v[164:167]
v_cvt_pk_bf16_f32 v134,v140,v141
v_cvt_pk_bf16_f32 v135,v142,v143
v_mfma_f32_16x16x32_bf16 v[172:175],a[156:159],v[232:235],v[36:39]
v_cvt_pk_bf16_f32 v148,v148,v149
v_cvt_pk_bf16_f32 v149,v150,v151
v_mfma_f32_16x16x32_bf16 v[172:175],a[152:155],v[200:203],v[172:175]
v_exp_f32 v156,v156
v_mfma_f32_16x16x32_bf16 v[172:175],a[148:151],v[168:171],v[172:175]
buffer_load_dwordx4 v5,s[24:27],s47 offen offset:1024 nt lds
v_mfma_f32_16x16x32_bf16 v[172:175],a[144:147],v[136:139],v[172:175]
v_exp_f32 v157,v157
v_mfma_f32_16x16x32_bf16 v[180:183],a[160:163],v[136:139],v[36:39]
v_exp_f32 v158,v158
v_mfma_f32_16x16x32_bf16 v[180:183],a[164:167],v[168:171],v[180:183]
v_exp_f32 v159,v159
v_mfma_f32_16x16x32_bf16 v[180:183],a[168:171],v[200:203],v[180:183]
v_cvt_pk_bf16_f32 v150,v156,v157
v_cvt_pk_bf16_f32 v151,v158,v159
v_mfma_f32_16x16x32_bf16 v[180:183],a[172:175],v[232:235],v[180:183]
v_exp_f32 v164,v164
v_mfma_f32_16x16x32_bf16 v[188:191],a[188:191],v[232:235],v[36:39]
v_exp_f32 v165,v165
v_mfma_f32_16x16x32_bf16 v[188:191],a[184:187],v[200:203],v[188:191]
v_exp_f32 v166,v166
v_mfma_f32_16x16x32_bf16 v[188:191],a[180:183],v[168:171],v[188:191]
v_exp_f32 v167,v167
v_mfma_f32_16x16x32_bf16 v[188:191],a[176:179],v[136:139],v[188:191]
v_cvt_pk_bf16_f32 v164,v164,v165
v_cvt_pk_bf16_f32 v165,v166,v167
v_mfma_f32_16x16x32_bf16 a[96:99],a[192:195],v[228:231],a[96:99]
s_cmp_eq_u32 s49,163840
s_cselect_b32 s49,0,s49
ds_read_b64_tr_b16 a[192:193],v2
v_mfma_f32_16x16x32_bf16 a[96:99],a[196:199],v[244:247],a[96:99]
ds_read_b64_tr_b16 a[194:195],v2 offset:4096
v_mfma_f32_16x16x32_bf16 a[100:103],a[204:207],v[244:247],a[100:103]
ds_read_b64_tr_b16 a[196:197],v2 offset:8192
v_mfma_f32_16x16x32_bf16 a[100:103],a[200:203],v[228:231],a[100:103]
ds_read_b64_tr_b16 a[198:199],v2 offset:12288
v_mfma_f32_16x16x32_bf16 a[104:107],a[208:211],v[228:231],a[104:107]
buffer_load_dwordx4 v4,s[24:27],s47 offen offset:2048 nt lds
v_mfma_f32_16x16x32_bf16 a[104:107],a[212:215],v[244:247],a[104:107]
ds_read_b64_tr_b16 a[204:205],v3 offset:8192
v_mfma_f32_16x16x32_bf16 a[108:111],a[220:223],v[244:247],a[108:111]
ds_read_b64_tr_b16 a[206:207],v3 offset:12288
v_mfma_f32_16x16x32_bf16 a[108:111],a[216:219],v[228:231],a[108:111]
ds_read_b64_tr_b16 a[200:201],v3
v_mfma_f32_16x16x32_bf16 a[112:115],a[224:227],v[228:231],a[112:115]
ds_read_b64_tr_b16 a[202:203],v3 offset:4096
v_mfma_f32_16x16x32_bf16 a[112:115],a[228:231],v[244:247],a[112:115]
ds_read_b64_tr_b16 a[208:209],v2 offset:256
v_mfma_f32_16x16x32_bf16 a[116:119],a[236:239],v[244:247],a[116:119]
ds_read_b64_tr_b16 a[210:211],v2 offset:4352
v_mfma_f32_16x16x32_bf16 a[116:119],a[232:235],v[228:231],a[116:119]
ds_read_b64_tr_b16 a[212:213],v2 offset:8448
v_mfma_f32_16x16x32_bf16 a[120:123],a[240:243],v[228:231],a[120:123]
ds_read_b64_tr_b16 a[214:215],v2 offset:12544
v_mfma_f32_16x16x32_bf16 a[120:123],a[244:247],v[244:247],a[120:123]
ds_read_b64_tr_b16 a[220:221],v3 offset:8448
v_mfma_f32_16x16x32_bf16 a[124:127],a[252:255],v[244:247],a[124:127]
ds_read_b64_tr_b16 a[222:223],v3 offset:12544
v_mfma_f32_16x16x32_bf16 a[124:127],a[248:251],v[228:231],a[124:127]
ds_read_b64_tr_b16 a[216:217],v3 offset:256
v_mfma_f32_16x16x32_bf16 v[60:63],v[28:31],v[228:231],v[60:63]
ds_read_b64_tr_b16 a[218:219],v3 offset:4352
v_mfma_f32_16x16x32_bf16 v[60:63],v[28:31],v[244:247],v[60:63]
ds_read_b64_tr_b16 a[224:225],v2 offset:512
v_mfma_f32_16x16x32_bf16 v[196:199],a[128:131],v[144:147],v[40:43]
buffer_load_dwordx4 v5,s[24:27],s47 offen offset:3072 nt lds
v_mfma_f32_16x16x32_bf16 v[196:199],a[132:135],v[176:179],v[196:199]
s_add_u32 s47,s47,16384
s_and_b32 s47,s47,0x3ffff
ds_read_b64_tr_b16 a[226:227],v2 offset:4608
v_mfma_f32_16x16x32_bf16 v[196:199],a[136:139],v[208:211],v[196:199]
s_or_b32 s47,s47,s46
ds_read_b64_tr_b16 a[228:229],v2 offset:8704
v_mfma_f32_16x16x32_bf16 v[196:199],a[140:143],v[240:243],v[196:199]
ds_read_b64_tr_b16 a[230:231],v2 offset:12800
v_mfma_f32_16x16x32_bf16 v[204:207],a[156:159],v[240:243],v[40:43]
ds_read_b64_tr_b16 a[236:237],v3 offset:8704
v_mfma_f32_16x16x32_bf16 v[204:207],a[152:155],v[208:211],v[204:207]
ds_read_b64_tr_b16 a[238:239],v3 offset:12800
v_mfma_f32_16x16x32_bf16 v[204:207],a[148:151],v[176:179],v[204:207]
ds_read_b64_tr_b16 a[232:233],v3 offset:512
v_mfma_f32_16x16x32_bf16 v[204:207],a[144:147],v[144:147],v[204:207]
ds_read_b64_tr_b16 a[234:235],v3 offset:4608
v_mfma_f32_16x16x32_bf16 v[212:215],a[160:163],v[144:147],v[40:43]
ds_read_b64_tr_b16 a[240:241],v2 offset:768
v_mfma_f32_16x16x32_bf16 v[212:215],a[164:167],v[176:179],v[212:215]
ds_read_b64_tr_b16 a[242:243],v2 offset:4864
v_mfma_f32_16x16x32_bf16 v[212:215],a[168:171],v[208:211],v[212:215]
ds_read_b64_tr_b16 a[244:245],v2 offset:8960
v_mfma_f32_16x16x32_bf16 v[212:215],a[172:175],v[240:243],v[212:215]
ds_read_b64_tr_b16 a[246:247],v2 offset:13056
v_mfma_f32_16x16x32_bf16 v[220:223],a[188:191],v[240:243],v[40:43]
ds_read_b64_tr_b16 a[252:253],v3 offset:8960
v_mfma_f32_16x16x32_bf16 v[220:223],a[184:187],v[208:211],v[220:223]
ds_read_b64_tr_b16 a[254:255],v3 offset:13056
v_mfma_f32_16x16x32_bf16 v[220:223],a[180:183],v[176:179],v[220:223]
ds_read_b64_tr_b16 a[248:249],v3 offset:768
v_mfma_f32_16x16x32_bf16 v[220:223],a[176:179],v[144:147],v[220:223]
s_waitcnt lgkmcnt(7)
ds_read_b64_tr_b16 a[250:251],v3 offset:4864
v_mfma_f32_16x16x32_bf16 a[0:3],a[192:195],v[132:135],a[0:3]
buffer_load_dword v26,v24,s[88:91],s93 offen
v_mfma_f32_16x16x32_bf16 a[0:3],a[196:199],v[148:151],a[0:3]
v_exp_f32 v180,v180
v_mfma_f32_16x16x32_bf16 a[4:7],a[204:207],v[148:151],a[4:7]
v_exp_f32 v181,v181
v_mfma_f32_16x16x32_bf16 a[4:7],a[200:203],v[132:135],a[4:7]
v_exp_f32 v182,v182
v_mfma_f32_16x16x32_bf16 a[8:11],a[208:211],v[132:135],a[8:11]
s_memrealtime s[76:77]
v_exp_f32 v183,v183
v_mfma_f32_16x16x32_bf16 a[8:11],a[212:215],v[148:151],a[8:11]
v_exp_f32 v172,v172
v_mfma_f32_16x16x32_bf16 a[12:15],a[220:223],v[148:151],a[12:15]
v_exp_f32 v173,v173
v_mfma_f32_16x16x32_bf16 a[12:15],a[216:219],v[132:135],a[12:15]
v_exp_f32 v174,v174
v_mfma_f32_16x16x32_bf16 a[16:19],a[224:227],v[132:135],a[16:19]
v_exp_f32 v175,v175
v_mfma_f32_16x16x32_bf16 a[16:19],a[228:231],v[148:151],a[16:19]
v_cvt_pk_bf16_f32 v180,v180,v181
v_cvt_pk_bf16_f32 v181,v182,v183
v_mfma_f32_16x16x32_bf16 a[20:23],a[236:239],v[148:151],a[20:23]
v_exp_f32 v188,v188
v_mfma_f32_16x16x32_bf16 a[20:23],a[232:235],v[132:135],a[20:23]
s_waitcnt lgkmcnt(0)
v_exp_f32 v189,v189
v_mfma_f32_16x16x32_bf16 a[24:27],a[240:243],v[132:135],a[24:27]
s_sub_u32 s79,s78,s76
v_exp_f32 v190,v190
v_mfma_f32_16x16x32_bf16 a[24:27],a[244:247],v[148:151],a[24:27]
s_add_u32 s78,s76,s60
v_exp_f32 v191,v191
v_mfma_f32_16x16x32_bf16 a[28:31],a[252:255],v[148:151],a[28:31]
s_cmp_gt_i32 s79,0
s_cbranch_scc1 L_ps7_%=
L_pr7_%=:
v_cvt_pk_bf16_f32 v166,v172,v173
v_mfma_f32_16x16x32_bf16 a[28:31],a[248:251],v[132:135],a[28:31]
v_cvt_pk_bf16_f32 v167,v174,v175
v_cvt_pk_bf16_f32 v182,v188,v189
v_mfma_f32_16x16x32_bf16 v[48:51],v[28:31],v[132:135],v[48:51]
v_cvt_pk_bf16_f32 v183,v190,v191
s_cmp_lg_u32 s53,0
v_mfma_f32_16x16x32_bf16 v[48:51],v[28:31],v[148:151],v[48:51]
s_cbranch_scc1 L_att1_%=
L_att1x_%=:
L_endseg_%=:
v_mfma_f32_16x16x32_bf16 v[228:231],a[128:131],v[152:155],v[44:47]
s_waitcnt vmcnt(10)
s_add_u32 s54,s54,16384
v_exp_f32 v196,v196
v_mfma_f32_16x16x32_bf16 v[228:231],a[132:135],v[184:187],v[228:231]
s_barrier
s_add_u32 m0,s49,s51
ds_read_b128 a[128:131],v1
v_mfma_f32_16x16x32_bf16 v[228:231],a[136:139],v[216:219],v[228:231]
s_and_b32 s46,s54,0xfffc0000
v_exp_f32 v197,v197
v_mfma_f32_16x16x32_bf16 v[228:231],a[140:143],v[248:251],v[228:231]
s_add_u32 s54,s54,16384
ds_read_b128 a[132:135],v1 offset:256
v_cvt_pk_bf16_f32 v196,v196,v197
v_mfma_f32_16x16x32_bf16 v[236:239],a[156:159],v[248:251],v[44:47]
s_lshl_b32 s43,s41,2
buffer_load_dwordx4 v4,s[20:23],s47 offen nt lds
v_mfma_f32_16x16x32_bf16 v[236:239],a[152:155],v[216:219],v[236:239]
s_add_u32 s43,s43,s70
ds_read_b128 a[136:139],v1 offset:512
v_add_u32 v2,s48,v2
v_mfma_f32_16x16x32_bf16 v[236:239],a[148:151],v[184:187],v[236:239]
s_lshl_b32 s43,s43,19
v_exp_f32 v198,v198
v_mfma_f32_16x16x32_bf16 v[236:239],a[144:147],v[152:155],v[236:239]
s_add_u32 s43,s43,s55
ds_read_b128 a[140:143],v1 offset:768
v_add_u32 v3,s48,v3
v_mfma_f32_16x16x32_bf16 v[244:247],a[160:163],v[152:155],v[44:47]
s_mov_b64 s[28:29],s[12:13]
v_exp_f32 v199,v199
v_mfma_f32_16x16x32_bf16 v[244:247],a[164:167],v[184:187],v[244:247]
s_mov_b32 s30,475136
ds_read_b128 a[156:159],v1 offset:4864
v_cvt_pk_bf16_f32 v197,v198,v199
v_mfma_f32_16x16x32_bf16 v[244:247],a[168:171],v[216:219],v[244:247]
s_mov_b32 s31,0x20000
v_exp_f32 v204,v204
v_mfma_f32_16x16x32_bf16 v[244:247],a[172:175],v[248:251],v[244:247]
s_cmp_eq_u32 s50,131072
s_cselect_b32 s62,163840,0
ds_read_b128 a[152:155],v1 offset:4608
v_mfma_f32_16x16x32_bf16 v[252:255],a[188:191],v[248:251],v[44:47]
s_sub_u32 s48,32768,s62
v_exp_f32 v205,v205
v_mfma_f32_16x16x32_bf16 v[252:255],a[184:187],v[216:219],v[252:255]
s_add_u32 s50,s50,s48
ds_read_b128 a[148:151],v1 offset:4352
v_cvt_pk_bf16_f32 v198,v204,v205
v_mfma_f32_16x16x32_bf16 v[252:255],a[180:183],v[184:187],v[252:255]
v_exp_f32 v206,v206
v_mfma_f32_16x16x32_bf16 v[252:255],a[176:179],v[152:155],v[252:255]
s_cmp_eq_u32 s50,131072
s_cselect_b32 s62,163840,0
ds_read_b128 a[144:147],v1 offset:4096
v_mfma_f32_16x16x32_bf16 a[32:35],a[192:195],v[164:167],a[32:35]
v_exp_f32 v207,v207
v_mfma_f32_16x16x32_bf16 a[32:35],a[196:199],v[180:183],a[32:35]
ds_read_b128 a[160:163],v1 offset:8192
v_cvt_pk_bf16_f32 v199,v206,v207
v_mfma_f32_16x16x32_bf16 a[36:39],a[204:207],v[180:183],a[36:39]
buffer_load_dwordx4 v4,s[20:23],s47 offen offset:1024 nt lds
v_mfma_f32_16x16x32_bf16 a[36:39],a[200:203],v[164:167],a[36:39]
s_add_u32 s28,s28,s43
s_addc_u32 s29,s29,0
ds_read_b128 a[164:167],v1 offset:8448
v_mfma_f32_16x16x32_bf16 a[40:43],a[208:211],v[164:167],a[40:43]
s_and_b32 s29,s29,0xffff
v_exp_f32 v212,v212
v_mfma_f32_16x16x32_bf16 a[40:43],a[212:215],v[180:183],a[40:43]
ds_read_b128 a[168:171],v1 offset:8704
v_mfma_f32_16x16x32_bf16 a[44:47],a[220:223],v[180:183],a[44:47]
)A"
R"A(v_exp_f32 v213,v213
v_mfma_f32_16x16x32_bf16 a[44:47],a[216:219],v[164:167],a[44:47]
ds_read_b128 a[172:175],v1 offset:8960
v_cvt_pk_bf16_f32 v212,v212,v213
v_mfma_f32_16x16x32_bf16 a[48:51],a[224:227],v[164:167],a[48:51]
v_exp_f32 v214,v214
v_mfma_f32_16x16x32_bf16 a[48:51],a[228:231],v[180:183],a[48:51]
ds_read_b128 a[188:191],v1 offset:13056
v_mfma_f32_16x16x32_bf16 a[52:55],a[236:239],v[180:183],a[52:55]
v_exp_f32 v215,v215
v_mfma_f32_16x16x32_bf16 a[52:55],a[232:235],v[164:167],a[52:55]
ds_read_b128 a[184:187],v1 offset:12800
v_cvt_pk_bf16_f32 v213,v214,v215
v_mfma_f32_16x16x32_bf16 a[56:59],a[240:243],v[164:167],a[56:59]
v_exp_f32 v220,v220
v_mfma_f32_16x16x32_bf16 a[56:59],a[244:247],v[180:183],a[56:59]
ds_read_b128 a[180:183],v1 offset:12544
v_mfma_f32_16x16x32_bf16 a[60:63],a[252:255],v[180:183],a[60:63]
v_exp_f32 v221,v221
v_mfma_f32_16x16x32_bf16 a[60:63],a[248:251],v[164:167],a[60:63]
ds_read_b128 a[176:179],v1 offset:12288
v_cvt_pk_bf16_f32 v214,v220,v221
v_mfma_f32_16x16x32_bf16 v[52:55],v[28:31],v[164:167],v[52:55]
buffer_load_dwordx4 v5,s[20:23],s47 offen offset:2048 nt lds
v_mfma_f32_16x16x32_bf16 v[52:55],v[28:31],v[180:183],v[52:55]
s_waitcnt lgkmcnt(3)
v_exp_f32 v222,v222
v_mfma_f32_16x16x32_bf16 v[132:135],a[128:131],v[128:131],v[32:35]
v_exp_f32 v223,v223
v_mfma_f32_16x16x32_bf16 v[132:135],a[132:135],v[160:163],v[132:135]
v_cvt_pk_bf16_f32 v215,v222,v223
v_add_u32 v1,s48,v1
v_mfma_f32_16x16x32_bf16 v[132:135],a[136:139],v[192:195],v[132:135]
v_exp_f32 v228,v228
v_mfma_f32_16x16x32_bf16 v[132:135],a[140:143],v[224:227],v[132:135]
v_exp_f32 v229,v229
v_mfma_f32_16x16x32_bf16 v[140:143],a[156:159],v[224:227],v[32:35]
v_exp_f32 v230,v230
v_mfma_f32_16x16x32_bf16 v[140:143],a[152:155],v[192:195],v[140:143]
v_exp_f32 v231,v231
v_mfma_f32_16x16x32_bf16 v[140:143],a[148:151],v[160:163],v[140:143]
v_cvt_pk_bf16_f32 v228,v228,v229
v_cvt_pk_bf16_f32 v229,v230,v231
v_mfma_f32_16x16x32_bf16 v[140:143],a[144:147],v[128:131],v[140:143]
v_exp_f32 v236,v236
v_mfma_f32_16x16x32_bf16 v[148:151],a[160:163],v[128:131],v[32:35]
v_exp_f32 v237,v237
v_mfma_f32_16x16x32_bf16 v[148:151],a[164:167],v[160:163],v[148:151]
v_exp_f32 v238,v238
v_mfma_f32_16x16x32_bf16 v[148:151],a[168:171],v[192:195],v[148:151]
v_exp_f32 v239,v239
v_mfma_f32_16x16x32_bf16 v[148:151],a[172:175],v[224:227],v[148:151]
v_exp_f32 v244,v244
v_mfma_f32_16x16x32_bf16 v[156:159],a[188:191],v[224:227],v[32:35]
s_waitcnt lgkmcnt(0)
buffer_load_dwordx4 v5,s[20:23],s47 offen offset:3072 nt lds
v_mfma_f32_16x16x32_bf16 v[156:159],a[184:187],v[192:195],v[156:159]
v_exp_f32 v245,v245
v_mfma_f32_16x16x32_bf16 v[156:159],a[180:183],v[160:163],v[156:159]
v_exp_f32 v246,v246
v_mfma_f32_16x16x32_bf16 v[156:159],a[176:179],v[128:131],v[156:159]
v_exp_f32 v247,v247
v_mfma_f32_16x16x32_bf16 a[64:67],a[192:195],v[196:199],a[64:67]
v_cvt_pk_bf16_f32 v230,v236,v237
v_cvt_pk_bf16_f32 v231,v238,v239
v_mfma_f32_16x16x32_bf16 a[64:67],a[196:199],v[212:215],a[64:67]
v_cvt_pk_bf16_f32 v244,v244,v245
v_cvt_pk_bf16_f32 v245,v246,v247
v_mfma_f32_16x16x32_bf16 a[68:71],a[204:207],v[212:215],a[68:71]
v_exp_f32 v252,v252
v_mfma_f32_16x16x32_bf16 a[68:71],a[200:203],v[196:199],a[68:71]
v_exp_f32 v253,v253
v_mfma_f32_16x16x32_bf16 a[72:75],a[208:211],v[196:199],a[72:75]
v_exp_f32 v254,v254
v_mfma_f32_16x16x32_bf16 a[72:75],a[212:215],v[212:215],a[72:75]
v_exp_f32 v255,v255
v_mfma_f32_16x16x32_bf16 a[76:79],a[220:223],v[212:215],a[76:79]
v_cvt_pk_bf16_f32 v246,v252,v253
v_cvt_pk_bf16_f32 v247,v254,v255
v_mfma_f32_16x16x32_bf16 a[76:79],a[216:219],v[196:199],a[76:79]
v_exp_f32 v132,v132
v_mfma_f32_16x16x32_bf16 a[80:83],a[224:227],v[196:199],a[80:83]
s_add_u32 m0,s49,s52
v_exp_f32 v133,v133
v_mfma_f32_16x16x32_bf16 a[80:83],a[228:231],v[212:215],a[80:83]
s_add_u32 s49,s49,32768
v_exp_f32 v134,v134
v_mfma_f32_16x16x32_bf16 a[84:87],a[236:239],v[212:215],a[84:87]
v_exp_f32 v135,v135
v_mfma_f32_16x16x32_bf16 a[84:87],a[232:235],v[196:199],a[84:87]
buffer_load_dwordx4 v4,s[24:27],s47 offen nt lds
v_mfma_f32_16x16x32_bf16 a[88:91],a[240:243],v[196:199],a[88:91]
v_cvt_pk_bf16_f32 v132,v132,v133
v_cvt_pk_bf16_f32 v133,v134,v135
v_mfma_f32_16x16x32_bf16 a[88:91],a[244:247],v[212:215],a[88:91]
v_exp_f32 v140,v140
v_mfma_f32_16x16x32_bf16 a[92:95],a[252:255],v[212:215],a[92:95]
v_exp_f32 v141,v141
v_mfma_f32_16x16x32_bf16 a[92:95],a[248:251],v[196:199],a[92:95]
v_exp_f32 v142,v142
v_mfma_f32_16x16x32_bf16 v[56:59],v[28:31],v[196:199],v[56:59]
v_exp_f32 v143,v143
v_mfma_f32_16x16x32_bf16 v[56:59],v[28:31],v[212:215],v[56:59]
v_exp_f32 v148,v148
v_mfma_f32_16x16x32_bf16 v[164:167],a[128:131],v[136:139],v[36:39]
v_exp_f32 v149,v149
v_mfma_f32_16x16x32_bf16 v[164:167],a[132:135],v[168:171],v[164:167]
v_exp_f32 v150,v150
v_mfma_f32_16x16x32_bf16 v[164:167],a[136:139],v[200:203],v[164:167]
v_exp_f32 v151,v151
v_mfma_f32_16x16x32_bf16 v[164:167],a[140:143],v[232:235],v[164:167]
v_cvt_pk_bf16_f32 v134,v140,v141
v_cvt_pk_bf16_f32 v135,v142,v143
v_mfma_f32_16x16x32_bf16 v[172:175],a[156:159],v[232:235],v[36:39]
v_cvt_pk_bf16_f32 v148,v148,v149
v_cvt_pk_bf16_f32 v149,v150,v151
v_mfma_f32_16x16x32_bf16 v[172:175],a[152:155],v[200:203],v[172:175]
v_exp_f32 v156,v156
v_mfma_f32_16x16x32_bf16 v[172:175],a[148:151],v[168:171],v[172:175]
v_exp_f32 v157,v157
v_mfma_f32_16x16x32_bf16 v[172:175],a[144:147],v[136:139],v[172:175]
buffer_load_dwordx4 v5,s[24:27],s47 offen offset:1024 nt lds
v_mfma_f32_16x16x32_bf16 v[180:183],a[160:163],v[136:139],v[36:39]
v_exp_f32 v158,v158
v_mfma_f32_16x16x32_bf16 v[180:183],a[164:167],v[168:171],v[180:183]
v_exp_f32 v159,v159
v_mfma_f32_16x16x32_bf16 v[180:183],a[168:171],v[200:203],v[180:183]
v_cvt_pk_bf16_f32 v150,v156,v157
v_cvt_pk_bf16_f32 v151,v158,v159
v_mfma_f32_16x16x32_bf16 v[180:183],a[172:175],v[232:235],v[180:183]
s_cmp_eq_u32 s49,163840
s_cselect_b32 s49,0,s49
v_mfma_f32_16x16x32_bf16 v[188:191],a[188:191],v[232:235],v[36:39]
v_exp_f32 v164,v164
v_mfma_f32_16x16x32_bf16 v[188:191],a[184:187],v[200:203],v[188:191]
v_exp_f32 v165,v165
v_mfma_f32_16x16x32_bf16 v[188:191],a[180:183],v[168:171],v[188:191]
v_exp_f32 v166,v166
v_mfma_f32_16x16x32_bf16 v[188:191],a[176:179],v[136:139],v[188:191]
v_exp_f32 v167,v167
v_mfma_f32_16x16x32_bf16 a[96:99],a[192:195],v[228:231],a[96:99]
ds_read_b64_tr_b16 a[192:193],v2
ds_read_b64_tr_b16 a[194:195],v2 offset:4096
v_mfma_f32_16x16x32_bf16 a[96:99],a[196:199],v[244:247],a[96:99]
ds_read_b64_tr_b16 a[196:197],v2 offset:8192
v_cvt_pk_bf16_f32 v164,v164,v165
v_mfma_f32_16x16x32_bf16 a[100:103],a[204:207],v[244:247],a[100:103]
ds_read_b64_tr_b16 a[198:199],v2 offset:12288
v_cvt_pk_bf16_f32 v165,v166,v167
v_mfma_f32_16x16x32_bf16 a[100:103],a[200:203],v[228:231],a[100:103]
ds_read_b64_tr_b16 a[204:205],v3 offset:8192
v_mfma_f32_16x16x32_bf16 a[104:107],a[208:211],v[228:231],a[104:107]
ds_read_b64_tr_b16 a[206:207],v3 offset:12288
ds_read_b64_tr_b16 a[200:201],v3
v_mfma_f32_16x16x32_bf16 a[104:107],a[212:215],v[244:247],a[104:107]
buffer_load_dwordx4 v4,s[24:27],s47 offen offset:2048 nt lds
v_mfma_f32_16x16x32_bf16 a[108:111],a[220:223],v[244:247],a[108:111]
ds_read_b64_tr_b16 a[202:203],v3 offset:4096
ds_read_b64_tr_b16 a[208:209],v2 offset:256
v_mfma_f32_16x16x32_bf16 a[108:111],a[216:219],v[228:231],a[108:111]
ds_read_b64_tr_b16 a[210:211],v2 offset:4352
v_mfma_f32_16x16x32_bf16 a[112:115],a[224:227],v[228:231],a[112:115]
ds_read_b64_tr_b16 a[212:213],v2 offset:8448
v_mfma_f32_16x16x32_bf16 a[112:115],a[228:231],v[244:247],a[112:115]
ds_read_b64_tr_b16 a[214:215],v2 offset:12544
v_mfma_f32_16x16x32_bf16 a[116:119],a[236:239],v[244:247],a[116:119]
ds_read_b64_tr_b16 a[220:221],v3 offset:8448
ds_read_b64_tr_b16 a[222:223],v3 offset:12544
v_mfma_f32_16x16x32_bf16 a[116:119],a[232:235],v[228:231],a[116:119]
ds_read_b64_tr_b16 a[216:217],v3 offset:256
v_mfma_f32_16x16x32_bf16 a[120:123],a[240:243],v[228:231],a[120:123]
ds_read_b64_tr_b16 a[218:219],v3 offset:4352
v_mfma_f32_16x16x32_bf16 a[120:123],a[244:247],v[244:247],a[120:123]
ds_read_b64_tr_b16 a[224:225],v2 offset:512
v_mfma_f32_16x16x32_bf16 a[124:127],a[252:255],v[244:247],a[124:127]
ds_read_b64_tr_b16 a[226:227],v2 offset:4608
ds_read_b64_tr_b16 a[228:229],v2 offset:8704
v_mfma_f32_16x16x32_bf16 a[124:127],a[248:251],v[228:231],a[124:127]
ds_read_b64_tr_b16 a[230:231],v2 offset:12800
v_mfma_f32_16x16x32_bf16 v[60:63],v[28:31],v[228:231],v[60:63]
ds_read_b64_tr_b16 a[236:237],v3 offset:8704
v_mfma_f32_16x16x32_bf16 v[60:63],v[28:31],v[244:247],v[60:63]
ds_read_b64_tr_b16 a[238:239],v3 offset:12800
v_mfma_f32_16x16x32_bf16 v[196:199],a[128:131],v[144:147],v[40:43]
ds_read_b64_tr_b16 a[232:233],v3 offset:512
ds_read_b64_tr_b16 a[234:235],v3 offset:4608
v_mfma_f32_16x16x32_bf16 v[196:199],a[132:135],v[176:179],v[196:199]
buffer_load_dwordx4 v5,s[24:27],s47 offen offset:3072 nt lds
v_mfma_f32_16x16x32_bf16 v[196:199],a[136:139],v[208:211],v[196:199]
s_add_u32 s47,s47,16384
s_and_b32 s47,s47,0x3ffff
s_or_b32 s47,s47,s46
v_mfma_f32_16x16x32_bf16 v[196:199],a[140:143],v[240:243],v[196:199]
s_and_b32 s46,s54,0xfffc0000
ds_read_b64_tr_b16 a[240:241],v2 offset:768
ds_read_b64_tr_b16 a[242:243],v2 offset:4864
v_mfma_f32_16x16x32_bf16 v[204:207],a[156:159],v[240:243],v[40:43]
s_add_u32 s54,s54,16384
ds_read_b64_tr_b16 a[244:245],v2 offset:8960
v_mfma_f32_16x16x32_bf16 v[204:207],a[152:155],v[208:211],v[204:207]
ds_read_b64_tr_b16 a[246:247],v2 offset:13056
v_add_u32 v2,s48,v2
v_mfma_f32_16x16x32_bf16 v[204:207],a[148:151],v[176:179],v[204:207]
ds_read_b64_tr_b16 a[252:253],v3 offset:8960
v_mfma_f32_16x16x32_bf16 v[204:207],a[144:147],v[144:147],v[204:207]
ds_read_b64_tr_b16 a[254:255],v3 offset:13056
ds_read_b64_tr_b16 a[248:249],v3 offset:768
v_mfma_f32_16x16x32_bf16 v[212:215],a[160:163],v[144:147],v[40:43]
ds_read_b64_tr_b16 a[250:251],v3 offset:4864
v_add_u32 v3,s48,v3
v_mfma_f32_16x16x32_bf16 v[212:215],a[164:167],v[176:179],v[212:215]
s_sub_u32 s48,32768,s62
v_exp_f32 v172,v172
v_mfma_f32_16x16x32_bf16 v[212:215],a[168:171],v[208:211],v[212:215]
s_add_u32 s50,s50,s48
v_exp_f32 v173,v173
v_mfma_f32_16x16x32_bf16 v[212:215],a[172:175],v[240:243],v[212:215]
v_exp_f32 v174,v174
v_mfma_f32_16x16x32_bf16 v[220:223],a[188:191],v[240:243],v[40:43]
v_exp_f32 v175,v175
v_mfma_f32_16x16x32_bf16 v[220:223],a[184:187],v[208:211],v[220:223]
s_add_u32 m0,s49,s51
v_exp_f32 v180,v180
v_mfma_f32_16x16x32_bf16 v[220:223],a[180:183],v[176:179],v[220:223]
v_exp_f32 v181,v181
v_mfma_f32_16x16x32_bf16 v[220:223],a[176:179],v[144:147],v[220:223]
s_waitcnt lgkmcnt(1)
v_exp_f32 v182,v182
v_mfma_f32_16x16x32_bf16 a[0:3],a[192:195],v[132:135],a[0:3]
buffer_load_dwordx4 v4,s[20:23],s47 offen nt lds
v_mfma_f32_16x16x32_bf16 a[0:3],a[196:199],v[148:151],a[0:3]
v_exp_f32 v183,v183
v_mfma_f32_16x16x32_bf16 a[4:7],a[204:207],v[148:151],a[4:7]
v_cvt_pk_bf16_f32 v166,v172,v173
v_cvt_pk_bf16_f32 v167,v174,v175
v_mfma_f32_16x16x32_bf16 a[4:7],a[200:203],v[132:135],a[4:7]
v_cvt_pk_bf16_f32 v180,v180,v181
v_cvt_pk_bf16_f32 v181,v182,v183
v_mfma_f32_16x16x32_bf16 a[8:11],a[208:211],v[132:135],a[8:11]
s_memrealtime s[76:77]
v_exp_f32 v188,v188
v_mfma_f32_16x16x32_bf16 a[8:11],a[212:215],v[148:151],a[8:11]
v_exp_f32 v189,v189
v_mfma_f32_16x16x32_bf16 a[12:15],a[220:223],v[148:151],a[12:15]
v_exp_f32 v190,v190
v_mfma_f32_16x16x32_bf16 a[12:15],a[216:219],v[132:135],a[12:15]
v_exp_f32 v191,v191
v_mfma_f32_16x16x32_bf16 a[16:19],a[224:227],v[132:135],a[16:19]
v_cvt_pk_bf16_f32 v182,v188,v189
v_cvt_pk_bf16_f32 v183,v190,v191
v_mfma_f32_16x16x32_bf16 a[16:19],a[228:231],v[148:151],a[16:19]
v_exp_f32 v196,v196
v_mfma_f32_16x16x32_bf16 a[20:23],a[236:239],v[148:151],a[20:23]
v_exp_f32 v197,v197
v_mfma_f32_16x16x32_bf16 a[20:23],a[232:235],v[132:135],a[20:23]
s_waitcnt lgkmcnt(0)
v_exp_f32 v198,v198
v_mfma_f32_16x16x32_bf16 a[24:27],a[240:243],v[132:135],a[24:27]
s_sub_u32 s79,s78,s76
v_exp_f32 v199,v199
v_mfma_f32_16x16x32_bf16 a[24:27],a[244:247],v[148:151],a[24:27]
s_add_u32 s78,s76,s60
v_cvt_pk_bf16_f32 v196,v196,v197
v_cvt_pk_bf16_f32 v197,v198,v199
v_mfma_f32_16x16x32_bf16 a[28:31],a[252:255],v[148:151],a[28:31]
)A"
R"A(s_cmp_gt_i32 s79,0
s_cbranch_scc1 L_ps8_%=
L_pr8_%=:
v_mfma_f32_16x16x32_bf16 a[28:31],a[248:251],v[132:135],a[28:31]
buffer_load_dwordx4 v4,s[20:23],s47 offen offset:1024 nt lds
v_mfma_f32_16x16x32_bf16 v[48:51],v[28:31],v[132:135],v[48:51]
v_exp_f32 v204,v204
v_mfma_f32_16x16x32_bf16 v[48:51],v[28:31],v[148:151],v[48:51]
v_exp_f32 v205,v205
v_mfma_f32_16x16x32_bf16 v[228:231],a[128:131],v[152:155],v[44:47]
s_waitcnt vmcnt(11)
v_exp_f32 v206,v206
v_mfma_f32_16x16x32_bf16 v[228:231],a[132:135],v[184:187],v[228:231]
s_barrier
ds_read_b128 a[128:131],v1
v_cvt_pk_bf16_f32 v198,v204,v205
v_mfma_f32_16x16x32_bf16 v[228:231],a[136:139],v[216:219],v[228:231]
v_exp_f32 v207,v207
v_mfma_f32_16x16x32_bf16 v[228:231],a[140:143],v[248:251],v[228:231]
ds_read_b128 a[132:135],v1 offset:256
v_cvt_pk_bf16_f32 v199,v206,v207
v_mfma_f32_16x16x32_bf16 v[236:239],a[156:159],v[248:251],v[44:47]
v_exp_f32 v212,v212
v_mfma_f32_16x16x32_bf16 v[236:239],a[152:155],v[216:219],v[236:239]
s_cmp_eq_u32 s50,131072
s_cselect_b32 s62,163840,0
ds_read_b128 a[136:139],v1 offset:512
v_mfma_f32_16x16x32_bf16 v[236:239],a[148:151],v[184:187],v[236:239]
v_exp_f32 v213,v213
v_mfma_f32_16x16x32_bf16 v[236:239],a[144:147],v[152:155],v[236:239]
ds_read_b128 a[140:143],v1 offset:768
v_cvt_pk_bf16_f32 v212,v212,v213
v_mfma_f32_16x16x32_bf16 v[244:247],a[160:163],v[152:155],v[44:47]
v_exp_f32 v214,v214
v_mfma_f32_16x16x32_bf16 v[244:247],a[164:167],v[184:187],v[244:247]
buffer_load_dwordx4 v5,s[20:23],s47 offen offset:2048 nt lds
v_mfma_f32_16x16x32_bf16 v[244:247],a[168:171],v[216:219],v[244:247]
ds_read_b128 a[156:159],v1 offset:4864
v_mfma_f32_16x16x32_bf16 v[244:247],a[172:175],v[248:251],v[244:247]
v_exp_f32 v215,v215
v_mfma_f32_16x16x32_bf16 v[252:255],a[188:191],v[248:251],v[44:47]
ds_read_b128 a[152:155],v1 offset:4608
v_cvt_pk_bf16_f32 v213,v214,v215
v_mfma_f32_16x16x32_bf16 v[252:255],a[184:187],v[216:219],v[252:255]
v_exp_f32 v220,v220
v_mfma_f32_16x16x32_bf16 v[252:255],a[180:183],v[184:187],v[252:255]
ds_read_b128 a[148:151],v1 offset:4352
v_mfma_f32_16x16x32_bf16 v[252:255],a[176:179],v[152:155],v[252:255]
v_exp_f32 v221,v221
v_mfma_f32_16x16x32_bf16 a[32:35],a[192:195],v[164:167],a[32:35]
ds_read_b128 a[144:147],v1 offset:4096
v_cvt_pk_bf16_f32 v214,v220,v221
v_mfma_f32_16x16x32_bf16 a[32:35],a[196:199],v[180:183],a[32:35]
v_exp_f32 v222,v222
v_mfma_f32_16x16x32_bf16 a[36:39],a[204:207],v[180:183],a[36:39]
ds_read_b128 a[160:163],v1 offset:8192
v_mfma_f32_16x16x32_bf16 a[36:39],a[200:203],v[164:167],a[36:39]
v_exp_f32 v223,v223
v_mfma_f32_16x16x32_bf16 a[40:43],a[208:211],v[164:167],a[40:43]
ds_read_b128 a[164:167],v1 offset:8448
v_cvt_pk_bf16_f32 v215,v222,v223
v_mfma_f32_16x16x32_bf16 a[40:43],a[212:215],v[180:183],a[40:43]
v_exp_f32 v228,v228
v_mfma_f32_16x16x32_bf16 a[44:47],a[220:223],v[180:183],a[44:47]
ds_read_b128 a[168:171],v1 offset:8704
v_mfma_f32_16x16x32_bf16 a[44:47],a[216:219],v[164:167],a[44:47]
buffer_load_dwordx4 v5,s[20:23],s47 offen offset:3072 nt lds
v_mfma_f32_16x16x32_bf16 a[48:51],a[224:227],v[164:167],a[48:51]
ds_read_b128 a[172:175],v1 offset:8960
v_mfma_f32_16x16x32_bf16 a[48:51],a[228:231],v[180:183],a[48:51]
v_exp_f32 v229,v229
v_mfma_f32_16x16x32_bf16 a[52:55],a[236:239],v[180:183],a[52:55]
ds_read_b128 a[188:191],v1 offset:13056
v_cvt_pk_bf16_f32 v228,v228,v229
v_mfma_f32_16x16x32_bf16 a[52:55],a[232:235],v[164:167],a[52:55]
v_exp_f32 v230,v230
v_mfma_f32_16x16x32_bf16 a[56:59],a[240:243],v[164:167],a[56:59]
ds_read_b128 a[184:187],v1 offset:12800
v_mfma_f32_16x16x32_bf16 a[56:59],a[244:247],v[180:183],a[56:59]
v_exp_f32 v231,v231
v_mfma_f32_16x16x32_bf16 a[60:63],a[252:255],v[180:183],a[60:63]
ds_read_b128 a[180:183],v1 offset:12544
v_cvt_pk_bf16_f32 v229,v230,v231
v_mfma_f32_16x16x32_bf16 a[60:63],a[248:251],v[164:167],a[60:63]
v_exp_f32 v236,v236
v_mfma_f32_16x16x32_bf16 v[52:55],v[28:31],v[164:167],v[52:55]
ds_read_b128 a[176:179],v1 offset:12288
v_add_u32 v1,s48,v1
v_mfma_f32_16x16x32_bf16 v[52:55],v[28:31],v[180:183],v[52:55]
s_waitcnt lgkmcnt(4)
v_exp_f32 v237,v237
v_mfma_f32_16x16x32_bf16 v[132:135],a[128:131],v[128:131],v[32:35]
v_exp_f32 v238,v238
v_mfma_f32_16x16x32_bf16 v[132:135],a[132:135],v[160:163],v[132:135]
s_add_u32 m0,s49,s52
v_exp_f32 v239,v239
v_mfma_f32_16x16x32_bf16 v[132:135],a[136:139],v[192:195],v[132:135]
s_add_u32 s49,s49,32768
v_exp_f32 v244,v244
v_mfma_f32_16x16x32_bf16 v[132:135],a[140:143],v[224:227],v[132:135]
v_exp_f32 v245,v245
v_mfma_f32_16x16x32_bf16 v[140:143],a[156:159],v[224:227],v[32:35]
buffer_load_dwordx4 v4,s[24:27],s47 offen nt lds
v_mfma_f32_16x16x32_bf16 v[140:143],a[152:155],v[192:195],v[140:143]
v_exp_f32 v246,v246
v_mfma_f32_16x16x32_bf16 v[140:143],a[148:151],v[160:163],v[140:143]
v_exp_f32 v247,v247
v_mfma_f32_16x16x32_bf16 v[140:143],a[144:147],v[128:131],v[140:143]
v_cvt_pk_bf16_f32 v230,v236,v237
v_cvt_pk_bf16_f32 v231,v238,v239
v_mfma_f32_16x16x32_bf16 v[148:151],a[160:163],v[128:131],v[32:35]
v_cvt_pk_bf16_f32 v244,v244,v245
v_cvt_pk_bf16_f32 v245,v246,v247
v_mfma_f32_16x16x32_bf16 v[148:151],a[164:167],v[160:163],v[148:151]
v_exp_f32 v252,v252
v_mfma_f32_16x16x32_bf16 v[148:151],a[168:171],v[192:195],v[148:151]
v_exp_f32 v253,v253
v_mfma_f32_16x16x32_bf16 v[148:151],a[172:175],v[224:227],v[148:151]
s_waitcnt lgkmcnt(0)
v_exp_f32 v254,v254
v_mfma_f32_16x16x32_bf16 v[156:159],a[188:191],v[224:227],v[32:35]
v_exp_f32 v255,v255
v_mfma_f32_16x16x32_bf16 v[156:159],a[184:187],v[192:195],v[156:159]
v_cvt_pk_bf16_f32 v246,v252,v253
v_cvt_pk_bf16_f32 v247,v254,v255
v_mfma_f32_16x16x32_bf16 v[156:159],a[180:183],v[160:163],v[156:159]
s_cmp_eq_u32 s49,163840
s_cselect_b32 s49,0,s49
v_mfma_f32_16x16x32_bf16 v[156:159],a[176:179],v[128:131],v[156:159]
v_exp_f32 v132,v132
v_mfma_f32_16x16x32_bf16 a[64:67],a[192:195],v[196:199],a[64:67]
v_exp_f32 v133,v133
v_mfma_f32_16x16x32_bf16 a[64:67],a[196:199],v[212:215],a[64:67]
v_exp_f32 v134,v134
v_mfma_f32_16x16x32_bf16 a[68:71],a[204:207],v[212:215],a[68:71]
buffer_load_dwordx4 v5,s[24:27],s47 offen offset:1024 nt lds
v_mfma_f32_16x16x32_bf16 a[68:71],a[200:203],v[196:199],a[68:71]
v_exp_f32 v135,v135
v_mfma_f32_16x16x32_bf16 a[72:75],a[208:211],v[196:199],a[72:75]
v_cvt_pk_bf16_f32 v132,v132,v133
v_cvt_pk_bf16_f32 v133,v134,v135
v_mfma_f32_16x16x32_bf16 a[72:75],a[212:215],v[212:215],a[72:75]
v_exp_f32 v140,v140
v_mfma_f32_16x16x32_bf16 a[76:79],a[220:223],v[212:215],a[76:79]
v_exp_f32 v141,v141
v_mfma_f32_16x16x32_bf16 a[76:79],a[216:219],v[196:199],a[76:79]
v_exp_f32 v142,v142
v_mfma_f32_16x16x32_bf16 a[80:83],a[224:227],v[196:199],a[80:83]
v_exp_f32 v143,v143
v_mfma_f32_16x16x32_bf16 a[80:83],a[228:231],v[212:215],a[80:83]
v_exp_f32 v148,v148
v_mfma_f32_16x16x32_bf16 a[84:87],a[236:239],v[212:215],a[84:87]
v_exp_f32 v149,v149
v_mfma_f32_16x16x32_bf16 a[84:87],a[232:235],v[196:199],a[84:87]
v_exp_f32 v150,v150
v_mfma_f32_16x16x32_bf16 a[88:91],a[240:243],v[196:199],a[88:91]
v_exp_f32 v151,v151
v_mfma_f32_16x16x32_bf16 a[88:91],a[244:247],v[212:215],a[88:91]
v_cvt_pk_bf16_f32 v134,v140,v141
v_cvt_pk_bf16_f32 v135,v142,v143
v_mfma_f32_16x16x32_bf16 a[92:95],a[252:255],v[212:215],a[92:95]
v_cvt_pk_bf16_f32 v148,v148,v149
v_cvt_pk_bf16_f32 v149,v150,v151
v_mfma_f32_16x16x32_bf16 a[92:95],a[248:251],v[196:199],a[92:95]
v_exp_f32 v156,v156
v_mfma_f32_16x16x32_bf16 v[56:59],v[28:31],v[196:199],v[56:59]
buffer_load_dwordx4 v4,s[24:27],s47 offen offset:2048 nt lds
v_mfma_f32_16x16x32_bf16 v[56:59],v[28:31],v[212:215],v[56:59]
v_exp_f32 v157,v157
v_mfma_f32_16x16x32_bf16 v[164:167],a[128:131],v[136:139],v[36:39]
v_exp_f32 v158,v158
v_mfma_f32_16x16x32_bf16 v[164:167],a[132:135],v[168:171],v[164:167]
v_exp_f32 v159,v159
v_mfma_f32_16x16x32_bf16 v[164:167],a[136:139],v[200:203],v[164:167]
v_cvt_pk_bf16_f32 v150,v156,v157
v_cvt_pk_bf16_f32 v151,v158,v159
v_mfma_f32_16x16x32_bf16 v[164:167],a[140:143],v[232:235],v[164:167]
v_mfma_f32_16x16x32_bf16 v[172:175],a[156:159],v[232:235],v[36:39]
v_mfma_f32_16x16x32_bf16 v[172:175],a[152:155],v[200:203],v[172:175]
v_mfma_f32_16x16x32_bf16 v[172:175],a[148:151],v[168:171],v[172:175]
v_mfma_f32_16x16x32_bf16 v[172:175],a[144:147],v[136:139],v[172:175]
v_mfma_f32_16x16x32_bf16 v[180:183],a[160:163],v[136:139],v[36:39]
v_mfma_f32_16x16x32_bf16 v[180:183],a[164:167],v[168:171],v[180:183]
s_nop 1
v_exp_f32 v164,v164
v_mfma_f32_16x16x32_bf16 v[180:183],a[168:171],v[200:203],v[180:183]
v_exp_f32 v165,v165
v_mfma_f32_16x16x32_bf16 v[180:183],a[172:175],v[232:235],v[180:183]
v_exp_f32 v166,v166
v_mfma_f32_16x16x32_bf16 v[188:191],a[188:191],v[232:235],v[36:39]
v_exp_f32 v167,v167
v_mfma_f32_16x16x32_bf16 v[188:191],a[184:187],v[200:203],v[188:191]
buffer_load_dwordx4 v5,s[24:27],s47 offen offset:3072 nt lds
v_mfma_f32_16x16x32_bf16 v[188:191],a[180:183],v[168:171],v[188:191]
s_add_u32 s47,s47,16384
s_and_b32 s47,s47,0x3ffff
s_or_b32 s47,s47,s46
v_mfma_f32_16x16x32_bf16 v[188:191],a[176:179],v[136:139],v[188:191]
s_and_b32 s46,s54,0xfffc0000
v_cvt_pk_bf16_f32 v164,v164,v165
v_cvt_pk_bf16_f32 v165,v166,v167
v_mfma_f32_16x16x32_bf16 a[96:99],a[192:195],v[228:231],a[96:99]
s_add_u32 s54,s54,16384
ds_read_b64_tr_b16 a[192:193],v2
ds_read_b64_tr_b16 a[194:195],v2 offset:4096
v_mfma_f32_16x16x32_bf16 a[96:99],a[196:199],v[244:247],a[96:99]
ds_read_b64_tr_b16 a[196:197],v2 offset:8192
v_mfma_f32_16x16x32_bf16 a[100:103],a[204:207],v[244:247],a[100:103]
ds_read_b64_tr_b16 a[198:199],v2 offset:12288
v_mfma_f32_16x16x32_bf16 a[100:103],a[200:203],v[228:231],a[100:103]
ds_read_b64_tr_b16 a[204:205],v3 offset:8192
v_mfma_f32_16x16x32_bf16 a[104:107],a[208:211],v[228:231],a[104:107]
ds_read_b64_tr_b16 a[206:207],v3 offset:12288
ds_read_b64_tr_b16 a[200:201],v3
v_mfma_f32_16x16x32_bf16 a[104:107],a[212:215],v[244:247],a[104:107]
ds_read_b64_tr_b16 a[202:203],v3 offset:4096
v_mfma_f32_16x16x32_bf16 a[108:111],a[220:223],v[244:247],a[108:111]
ds_read_b64_tr_b16 a[208:209],v2 offset:256
v_mfma_f32_16x16x32_bf16 a[108:111],a[216:219],v[228:231],a[108:111]
ds_read_b64_tr_b16 a[210:211],v2 offset:4352
v_mfma_f32_16x16x32_bf16 a[112:115],a[224:227],v[228:231],a[112:115]
s_add_u32 m0,s49,s51
ds_read_b64_tr_b16 a[212:213],v2 offset:8448
ds_read_b64_tr_b16 a[214:215],v2 offset:12544
v_mfma_f32_16x16x32_bf16 a[112:115],a[228:231],v[244:247],a[112:115]
ds_read_b64_tr_b16 a[220:221],v3 offset:8448
v_mfma_f32_16x16x32_bf16 a[116:119],a[236:239],v[244:247],a[116:119]
ds_read_b64_tr_b16 a[222:223],v3 offset:12544
v_mfma_f32_16x16x32_bf16 a[116:119],a[232:235],v[228:231],a[116:119]
buffer_load_dwordx4 v4,s[20:23],s47 offen nt lds
v_mfma_f32_16x16x32_bf16 a[120:123],a[240:243],v[228:231],a[120:123]
ds_read_b64_tr_b16 a[216:217],v3 offset:256
ds_read_b64_tr_b16 a[218:219],v3 offset:4352
v_mfma_f32_16x16x32_bf16 a[120:123],a[244:247],v[244:247],a[120:123]
ds_read_b64_tr_b16 a[224:225],v2 offset:512
v_mfma_f32_16x16x32_bf16 a[124:127],a[252:255],v[244:247],a[124:127]
ds_read_b64_tr_b16 a[226:227],v2 offset:4608
v_mfma_f32_16x16x32_bf16 a[124:127],a[248:251],v[228:231],a[124:127]
ds_read_b64_tr_b16 a[228:229],v2 offset:8704
v_mfma_f32_16x16x32_bf16 v[60:63],v[28:31],v[228:231],v[60:63]
ds_read_b64_tr_b16 a[230:231],v2 offset:12800
ds_read_b64_tr_b16 a[236:237],v3 offset:8704
v_mfma_f32_16x16x32_bf16 v[60:63],v[28:31],v[244:247],v[60:63]
ds_read_b64_tr_b16 a[238:239],v3 offset:12800
v_mfma_f32_16x16x32_bf16 v[196:199],a[128:131],v[144:147],v[40:43]
ds_read_b64_tr_b16 a[232:233],v3 offset:512
v_mfma_f32_16x16x32_bf16 v[196:199],a[132:135],v[176:179],v[196:199]
ds_read_b64_tr_b16 a[234:235],v3 offset:4608
v_mfma_f32_16x16x32_bf16 v[196:199],a[136:139],v[208:211],v[196:199]
ds_read_b64_tr_b16 a[240:241],v2 offset:768
ds_read_b64_tr_b16 a[242:243],v2 offset:4864
v_mfma_f32_16x16x32_bf16 v[196:199],a[140:143],v[240:243],v[196:199]
ds_read_b64_tr_b16 a[244:245],v2 offset:8960
v_mfma_f32_16x16x32_bf16 v[204:207],a[156:159],v[240:243],v[40:43]
ds_read_b64_tr_b16 a[246:247],v2 offset:13056
v_add_u32 v2,s48,v2
v_mfma_f32_16x16x32_bf16 v[204:207],a[152:155],v[208:211],v[204:207]
ds_read_b64_tr_b16 a[252:253],v3 offset:8960
)A"
R"A(v_mfma_f32_16x16x32_bf16 v[204:207],a[148:151],v[176:179],v[204:207]
ds_read_b64_tr_b16 a[254:255],v3 offset:13056
ds_read_b64_tr_b16 a[248:249],v3 offset:768
v_mfma_f32_16x16x32_bf16 v[204:207],a[144:147],v[144:147],v[204:207]
buffer_load_dwordx4 v4,s[20:23],s47 offen offset:1024 nt lds
v_mfma_f32_16x16x32_bf16 v[212:215],a[160:163],v[144:147],v[40:43]
ds_read_b64_tr_b16 a[250:251],v3 offset:4864
v_add_u32 v3,s48,v3
v_mfma_f32_16x16x32_bf16 v[212:215],a[164:167],v[176:179],v[212:215]
s_sub_u32 s48,32768,s62
v_exp_f32 v172,v172
v_mfma_f32_16x16x32_bf16 v[212:215],a[168:171],v[208:211],v[212:215]
s_add_u32 s50,s50,s48
v_exp_f32 v173,v173
v_mfma_f32_16x16x32_bf16 v[212:215],a[172:175],v[240:243],v[212:215]
v_exp_f32 v174,v174
v_mfma_f32_16x16x32_bf16 v[220:223],a[188:191],v[240:243],v[40:43]
v_exp_f32 v175,v175
v_mfma_f32_16x16x32_bf16 v[220:223],a[184:187],v[208:211],v[220:223]
v_exp_f32 v180,v180
v_mfma_f32_16x16x32_bf16 v[220:223],a[180:183],v[176:179],v[220:223]
v_exp_f32 v181,v181
v_mfma_f32_16x16x32_bf16 v[220:223],a[176:179],v[144:147],v[220:223]
s_waitcnt lgkmcnt(1)
v_exp_f32 v182,v182
v_mfma_f32_16x16x32_bf16 a[0:3],a[192:195],v[132:135],a[0:3]
v_exp_f32 v183,v183
v_mfma_f32_16x16x32_bf16 a[0:3],a[196:199],v[148:151],a[0:3]
v_cvt_pk_bf16_f32 v166,v172,v173
v_cvt_pk_bf16_f32 v167,v174,v175
v_mfma_f32_16x16x32_bf16 a[4:7],a[204:207],v[148:151],a[4:7]
v_cvt_pk_bf16_f32 v180,v180,v181
v_cvt_pk_bf16_f32 v181,v182,v183
v_mfma_f32_16x16x32_bf16 a[4:7],a[200:203],v[132:135],a[4:7]
v_exp_f32 v188,v188
v_mfma_f32_16x16x32_bf16 a[8:11],a[208:211],v[132:135],a[8:11]
s_memrealtime s[76:77]
v_exp_f32 v189,v189
v_mfma_f32_16x16x32_bf16 a[8:11],a[212:215],v[148:151],a[8:11]
buffer_load_dwordx4 v5,s[20:23],s47 offen offset:2048 nt lds
v_mfma_f32_16x16x32_bf16 a[12:15],a[220:223],v[148:151],a[12:15]
v_exp_f32 v190,v190
v_mfma_f32_16x16x32_bf16 a[12:15],a[216:219],v[132:135],a[12:15]
v_exp_f32 v191,v191
v_mfma_f32_16x16x32_bf16 a[16:19],a[224:227],v[132:135],a[16:19]
v_cvt_pk_bf16_f32 v182,v188,v189
v_cvt_pk_bf16_f32 v183,v190,v191
v_mfma_f32_16x16x32_bf16 a[16:19],a[228:231],v[148:151],a[16:19]
v_exp_f32 v196,v196
v_mfma_f32_16x16x32_bf16 a[20:23],a[236:239],v[148:151],a[20:23]
v_exp_f32 v197,v197
v_mfma_f32_16x16x32_bf16 a[20:23],a[232:235],v[132:135],a[20:23]
s_waitcnt lgkmcnt(0)
v_exp_f32 v198,v198
v_mfma_f32_16x16x32_bf16 a[24:27],a[240:243],v[132:135],a[24:27]
s_sub_u32 s79,s78,s76
v_exp_f32 v199,v199
v_mfma_f32_16x16x32_bf16 a[24:27],a[244:247],v[148:151],a[24:27]
s_add_u32 s78,s76,s60
v_cvt_pk_bf16_f32 v196,v196,v197
v_cvt_pk_bf16_f32 v197,v198,v199
v_mfma_f32_16x16x32_bf16 a[28:31],a[252:255],v[148:151],a[28:31]
s_cmp_gt_i32 s79,0
s_cbranch_scc1 L_ps9_%=
L_pr9_%=:
v_mfma_f32_16x16x32_bf16 a[28:31],a[248:251],v[132:135],a[28:31]
v_exp_f32 v204,v204
v_mfma_f32_16x16x32_bf16 v[48:51],v[28:31],v[132:135],v[48:51]
v_exp_f32 v205,v205
v_mfma_f32_16x16x32_bf16 v[48:51],v[28:31],v[148:151],v[48:51]
v_exp_f32 v206,v206
v_mfma_f32_16x16x32_bf16 v[228:231],a[128:131],v[152:155],v[44:47]
s_waitcnt vmcnt(11)
v_exp_f32 v207,v207
v_mfma_f32_16x16x32_bf16 v[228:231],a[132:135],v[184:187],v[228:231]
s_barrier
buffer_load_dwordx4 v5,s[20:23],s47 offen offset:3072 nt lds
v_mfma_f32_16x16x32_bf16 v[228:231],a[136:139],v[216:219],v[228:231]
ds_read_b128 a[128:131],v1
v_cvt_pk_bf16_f32 v198,v204,v205
v_mfma_f32_16x16x32_bf16 v[228:231],a[140:143],v[248:251],v[228:231]
v_exp_f32 v212,v212
v_mfma_f32_16x16x32_bf16 v[236:239],a[156:159],v[248:251],v[44:47]
ds_read_b128 a[132:135],v1 offset:256
v_cvt_pk_bf16_f32 v199,v206,v207
v_mfma_f32_16x16x32_bf16 v[236:239],a[152:155],v[216:219],v[236:239]
v_exp_f32 v213,v213
v_mfma_f32_16x16x32_bf16 v[236:239],a[148:151],v[184:187],v[236:239]
ds_read_b128 a[136:139],v1 offset:512
v_cvt_pk_bf16_f32 v212,v212,v213
v_mfma_f32_16x16x32_bf16 v[236:239],a[144:147],v[152:155],v[236:239]
v_exp_f32 v214,v214
v_mfma_f32_16x16x32_bf16 v[244:247],a[160:163],v[152:155],v[44:47]
s_cmp_eq_u32 s50,131072
s_cselect_b32 s62,163840,0
ds_read_b128 a[140:143],v1 offset:768
v_mfma_f32_16x16x32_bf16 v[244:247],a[164:167],v[184:187],v[244:247]
v_exp_f32 v215,v215
v_mfma_f32_16x16x32_bf16 v[244:247],a[168:171],v[216:219],v[244:247]
ds_read_b128 a[156:159],v1 offset:4864
v_cvt_pk_bf16_f32 v213,v214,v215
v_mfma_f32_16x16x32_bf16 v[244:247],a[172:175],v[248:251],v[244:247]
v_exp_f32 v220,v220
v_mfma_f32_16x16x32_bf16 v[252:255],a[188:191],v[248:251],v[44:47]
s_add_u32 m0,s49,s52
s_add_u32 s49,s49,32768
ds_read_b128 a[152:155],v1 offset:4608
v_mfma_f32_16x16x32_bf16 v[252:255],a[184:187],v[216:219],v[252:255]
v_exp_f32 v221,v221
v_mfma_f32_16x16x32_bf16 v[252:255],a[180:183],v[184:187],v[252:255]
ds_read_b128 a[148:151],v1 offset:4352
v_cvt_pk_bf16_f32 v214,v220,v221
v_mfma_f32_16x16x32_bf16 v[252:255],a[176:179],v[152:155],v[252:255]
buffer_load_dwordx4 v4,s[24:27],s47 offen nt lds
v_mfma_f32_16x16x32_bf16 a[32:35],a[192:195],v[164:167],a[32:35]
s_cmp_eq_u32 s49,163840
s_cselect_b32 s49,0,s49
ds_read_b128 a[144:147],v1 offset:4096
v_mfma_f32_16x16x32_bf16 a[32:35],a[196:199],v[180:183],a[32:35]
v_exp_f32 v222,v222
v_mfma_f32_16x16x32_bf16 a[36:39],a[204:207],v[180:183],a[36:39]
ds_read_b128 a[160:163],v1 offset:8192
v_mfma_f32_16x16x32_bf16 a[36:39],a[200:203],v[164:167],a[36:39]
v_exp_f32 v223,v223
v_mfma_f32_16x16x32_bf16 a[40:43],a[208:211],v[164:167],a[40:43]
ds_read_b128 a[164:167],v1 offset:8448
v_cvt_pk_bf16_f32 v215,v222,v223
v_mfma_f32_16x16x32_bf16 a[40:43],a[212:215],v[180:183],a[40:43]
v_exp_f32 v228,v228
v_mfma_f32_16x16x32_bf16 a[44:47],a[220:223],v[180:183],a[44:47]
ds_read_b128 a[168:171],v1 offset:8704
v_mfma_f32_16x16x32_bf16 a[44:47],a[216:219],v[164:167],a[44:47]
v_exp_f32 v229,v229
v_mfma_f32_16x16x32_bf16 a[48:51],a[224:227],v[164:167],a[48:51]
ds_read_b128 a[172:175],v1 offset:8960
v_cvt_pk_bf16_f32 v228,v228,v229
v_mfma_f32_16x16x32_bf16 a[48:51],a[228:231],v[180:183],a[48:51]
v_exp_f32 v230,v230
v_mfma_f32_16x16x32_bf16 a[52:55],a[236:239],v[180:183],a[52:55]
ds_read_b128 a[188:191],v1 offset:13056
v_mfma_f32_16x16x32_bf16 a[52:55],a[232:235],v[164:167],a[52:55]
v_exp_f32 v231,v231
v_mfma_f32_16x16x32_bf16 a[56:59],a[240:243],v[164:167],a[56:59]
ds_read_b128 a[184:187],v1 offset:12800
v_cvt_pk_bf16_f32 v229,v230,v231
v_mfma_f32_16x16x32_bf16 a[56:59],a[244:247],v[180:183],a[56:59]
buffer_load_dwordx4 v5,s[24:27],s47 offen offset:1024 nt lds
v_mfma_f32_16x16x32_bf16 a[60:63],a[252:255],v[180:183],a[60:63]
ds_read_b128 a[180:183],v1 offset:12544
v_mfma_f32_16x16x32_bf16 a[60:63],a[248:251],v[164:167],a[60:63]
v_exp_f32 v236,v236
v_mfma_f32_16x16x32_bf16 v[52:55],v[28:31],v[164:167],v[52:55]
ds_read_b128 a[176:179],v1 offset:12288
v_add_u32 v1,s48,v1
v_mfma_f32_16x16x32_bf16 v[52:55],v[28:31],v[180:183],v[52:55]
s_waitcnt lgkmcnt(4)
v_exp_f32 v237,v237
v_mfma_f32_16x16x32_bf16 v[132:135],a[128:131],v[128:131],v[32:35]
v_exp_f32 v238,v238
v_mfma_f32_16x16x32_bf16 v[132:135],a[132:135],v[160:163],v[132:135]
v_exp_f32 v239,v239
v_mfma_f32_16x16x32_bf16 v[132:135],a[136:139],v[192:195],v[132:135]
v_exp_f32 v244,v244
v_mfma_f32_16x16x32_bf16 v[132:135],a[140:143],v[224:227],v[132:135]
v_exp_f32 v245,v245
v_mfma_f32_16x16x32_bf16 v[140:143],a[156:159],v[224:227],v[32:35]
v_exp_f32 v246,v246
v_mfma_f32_16x16x32_bf16 v[140:143],a[152:155],v[192:195],v[140:143]
v_exp_f32 v247,v247
v_mfma_f32_16x16x32_bf16 v[140:143],a[148:151],v[160:163],v[140:143]
v_cvt_pk_bf16_f32 v230,v236,v237
v_cvt_pk_bf16_f32 v231,v238,v239
v_mfma_f32_16x16x32_bf16 v[140:143],a[144:147],v[128:131],v[140:143]
v_cvt_pk_bf16_f32 v244,v244,v245
v_cvt_pk_bf16_f32 v245,v246,v247
v_mfma_f32_16x16x32_bf16 v[148:151],a[160:163],v[128:131],v[32:35]
v_exp_f32 v252,v252
v_mfma_f32_16x16x32_bf16 v[148:151],a[164:167],v[160:163],v[148:151]
buffer_load_dwordx4 v4,s[24:27],s47 offen offset:2048 nt lds
v_mfma_f32_16x16x32_bf16 v[148:151],a[168:171],v[192:195],v[148:151]
v_exp_f32 v253,v253
v_mfma_f32_16x16x32_bf16 v[148:151],a[172:175],v[224:227],v[148:151]
s_waitcnt lgkmcnt(0)
v_exp_f32 v254,v254
v_mfma_f32_16x16x32_bf16 v[156:159],a[188:191],v[224:227],v[32:35]
v_exp_f32 v255,v255
v_mfma_f32_16x16x32_bf16 v[156:159],a[184:187],v[192:195],v[156:159]
v_cvt_pk_bf16_f32 v246,v252,v253
v_cvt_pk_bf16_f32 v247,v254,v255
v_mfma_f32_16x16x32_bf16 v[156:159],a[180:183],v[160:163],v[156:159]
v_exp_f32 v132,v132
v_mfma_f32_16x16x32_bf16 v[156:159],a[176:179],v[128:131],v[156:159]
v_exp_f32 v133,v133
v_mfma_f32_16x16x32_bf16 a[64:67],a[192:195],v[196:199],a[64:67]
v_exp_f32 v134,v134
v_mfma_f32_16x16x32_bf16 a[64:67],a[196:199],v[212:215],a[64:67]
v_exp_f32 v135,v135
v_mfma_f32_16x16x32_bf16 a[68:71],a[204:207],v[212:215],a[68:71]
v_cvt_pk_bf16_f32 v132,v132,v133
v_cvt_pk_bf16_f32 v133,v134,v135
v_mfma_f32_16x16x32_bf16 a[68:71],a[200:203],v[196:199],a[68:71]
v_exp_f32 v140,v140
v_mfma_f32_16x16x32_bf16 a[72:75],a[208:211],v[196:199],a[72:75]
v_exp_f32 v141,v141
v_mfma_f32_16x16x32_bf16 a[72:75],a[212:215],v[212:215],a[72:75]
v_exp_f32 v142,v142
v_mfma_f32_16x16x32_bf16 a[76:79],a[220:223],v[212:215],a[76:79]
v_exp_f32 v143,v143
v_mfma_f32_16x16x32_bf16 a[76:79],a[216:219],v[196:199],a[76:79]
buffer_load_dwordx4 v5,s[24:27],s47 offen offset:3072 nt lds
v_mfma_f32_16x16x32_bf16 a[80:83],a[224:227],v[196:199],a[80:83]
s_add_u32 s47,s47,16384
s_and_b32 s47,s47,0x3ffff
s_or_b32 s47,s47,s46
v_mfma_f32_16x16x32_bf16 a[80:83],a[228:231],v[212:215],a[80:83]
s_and_b32 s46,s54,0xfffc0000
v_exp_f32 v148,v148
v_mfma_f32_16x16x32_bf16 a[84:87],a[236:239],v[212:215],a[84:87]
s_add_u32 s54,s54,16384
v_exp_f32 v149,v149
v_mfma_f32_16x16x32_bf16 a[84:87],a[232:235],v[196:199],a[84:87]
v_exp_f32 v150,v150
v_mfma_f32_16x16x32_bf16 a[88:91],a[240:243],v[196:199],a[88:91]
v_exp_f32 v151,v151
v_mfma_f32_16x16x32_bf16 a[88:91],a[244:247],v[212:215],a[88:91]
v_cvt_pk_bf16_f32 v134,v140,v141
v_cvt_pk_bf16_f32 v135,v142,v143
v_mfma_f32_16x16x32_bf16 a[92:95],a[252:255],v[212:215],a[92:95]
v_cvt_pk_bf16_f32 v148,v148,v149
v_cvt_pk_bf16_f32 v149,v150,v151
v_mfma_f32_16x16x32_bf16 a[92:95],a[248:251],v[196:199],a[92:95]
v_exp_f32 v156,v156
v_mfma_f32_16x16x32_bf16 v[56:59],v[28:31],v[196:199],v[56:59]
v_exp_f32 v157,v157
v_mfma_f32_16x16x32_bf16 v[56:59],v[28:31],v[212:215],v[56:59]
v_exp_f32 v158,v158
v_mfma_f32_16x16x32_bf16 v[164:167],a[128:131],v[136:139],v[36:39]
v_exp_f32 v159,v159
v_mfma_f32_16x16x32_bf16 v[164:167],a[132:135],v[168:171],v[164:167]
s_add_u32 m0,s49,s51
v_cvt_pk_bf16_f32 v150,v156,v157
v_cvt_pk_bf16_f32 v151,v158,v159
v_mfma_f32_16x16x32_bf16 v[164:167],a[136:139],v[200:203],v[164:167]
v_mfma_f32_16x16x32_bf16 v[164:167],a[140:143],v[232:235],v[164:167]
v_mfma_f32_16x16x32_bf16 v[172:175],a[156:159],v[232:235],v[36:39]
buffer_load_dwordx4 v4,s[20:23],s47 offen nt lds
v_mfma_f32_16x16x32_bf16 v[172:175],a[152:155],v[200:203],v[172:175]
v_mfma_f32_16x16x32_bf16 v[172:175],a[148:151],v[168:171],v[172:175]
v_mfma_f32_16x16x32_bf16 v[172:175],a[144:147],v[136:139],v[172:175]
v_mfma_f32_16x16x32_bf16 v[180:183],a[160:163],v[136:139],v[36:39]
v_mfma_f32_16x16x32_bf16 v[180:183],a[164:167],v[168:171],v[180:183]
s_nop 0
v_exp_f32 v164,v164
v_mfma_f32_16x16x32_bf16 v[180:183],a[168:171],v[200:203],v[180:183]
v_exp_f32 v165,v165
v_mfma_f32_16x16x32_bf16 v[180:183],a[172:175],v[232:235],v[180:183]
v_exp_f32 v166,v166
v_mfma_f32_16x16x32_bf16 v[188:191],a[188:191],v[232:235],v[36:39]
v_exp_f32 v167,v167
v_mfma_f32_16x16x32_bf16 v[188:191],a[184:187],v[200:203],v[188:191]
v_cvt_pk_bf16_f32 v164,v164,v165
v_cvt_pk_bf16_f32 v165,v166,v167
v_mfma_f32_16x16x32_bf16 v[188:191],a[180:183],v[168:171],v[188:191]
v_exp_f32 v172,v172
v_mfma_f32_16x16x32_bf16 v[188:191],a[176:179],v[136:139],v[188:191]
v_exp_f32 v173,v173
v_mfma_f32_16x16x32_bf16 a[96:99],a[192:195],v[228:231],a[96:99]
ds_read_b64_tr_b16 a[192:193],v2
ds_read_b64_tr_b16 a[194:195],v2 offset:4096
v_mfma_f32_16x16x32_bf16 a[96:99],a[196:199],v[244:247],a[96:99]
ds_read_b64_tr_b16 a[196:197],v2 offset:8192
v_cvt_pk_bf16_f32 v166,v172,v173
v_mfma_f32_16x16x32_bf16 a[100:103],a[204:207],v[244:247],a[100:103]
)A"
R"A(buffer_load_dwordx4 v4,s[20:23],s47 offen offset:1024 nt lds
v_mfma_f32_16x16x32_bf16 a[100:103],a[200:203],v[228:231],a[100:103]
ds_read_b64_tr_b16 a[198:199],v2 offset:12288
ds_read_b64_tr_b16 a[204:205],v3 offset:8192
v_mfma_f32_16x16x32_bf16 a[104:107],a[208:211],v[228:231],a[104:107]
ds_read_b64_tr_b16 a[206:207],v3 offset:12288
v_mfma_f32_16x16x32_bf16 a[104:107],a[212:215],v[244:247],a[104:107]
ds_read_b64_tr_b16 a[200:201],v3
v_mfma_f32_16x16x32_bf16 a[108:111],a[220:223],v[244:247],a[108:111]
ds_read_b64_tr_b16 a[202:203],v3 offset:4096
v_mfma_f32_16x16x32_bf16 a[108:111],a[216:219],v[228:231],a[108:111]
ds_read_b64_tr_b16 a[208:209],v2 offset:256
ds_read_b64_tr_b16 a[210:211],v2 offset:4352
v_mfma_f32_16x16x32_bf16 a[112:115],a[224:227],v[228:231],a[112:115]
ds_read_b64_tr_b16 a[212:213],v2 offset:8448
v_mfma_f32_16x16x32_bf16 a[112:115],a[228:231],v[244:247],a[112:115]
ds_read_b64_tr_b16 a[214:215],v2 offset:12544
v_mfma_f32_16x16x32_bf16 a[116:119],a[236:239],v[244:247],a[116:119]
ds_read_b64_tr_b16 a[220:221],v3 offset:8448
v_mfma_f32_16x16x32_bf16 a[116:119],a[232:235],v[228:231],a[116:119]
ds_read_b64_tr_b16 a[222:223],v3 offset:12544
ds_read_b64_tr_b16 a[216:217],v3 offset:256
v_mfma_f32_16x16x32_bf16 a[120:123],a[240:243],v[228:231],a[120:123]
ds_read_b64_tr_b16 a[218:219],v3 offset:4352
v_mfma_f32_16x16x32_bf16 a[120:123],a[244:247],v[244:247],a[120:123]
ds_read_b64_tr_b16 a[224:225],v2 offset:512
v_mfma_f32_16x16x32_bf16 a[124:127],a[252:255],v[244:247],a[124:127]
ds_read_b64_tr_b16 a[226:227],v2 offset:4608
v_mfma_f32_16x16x32_bf16 a[124:127],a[248:251],v[228:231],a[124:127]
ds_read_b64_tr_b16 a[228:229],v2 offset:8704
ds_read_b64_tr_b16 a[230:231],v2 offset:12800
v_mfma_f32_16x16x32_bf16 v[60:63],v[28:31],v[228:231],v[60:63]
buffer_load_dwordx4 v5,s[20:23],s47 offen offset:2048 nt lds
v_mfma_f32_16x16x32_bf16 v[60:63],v[28:31],v[244:247],v[60:63]
ds_read_b64_tr_b16 a[236:237],v3 offset:8704
ds_read_b64_tr_b16 a[238:239],v3 offset:12800
v_mfma_f32_16x16x32_bf16 v[196:199],a[128:131],v[144:147],v[40:43]
ds_read_b64_tr_b16 a[232:233],v3 offset:512
v_mfma_f32_16x16x32_bf16 v[196:199],a[132:135],v[176:179],v[196:199]
ds_read_b64_tr_b16 a[234:235],v3 offset:4608
v_mfma_f32_16x16x32_bf16 v[196:199],a[136:139],v[208:211],v[196:199]
ds_read_b64_tr_b16 a[240:241],v2 offset:768
v_mfma_f32_16x16x32_bf16 v[196:199],a[140:143],v[240:243],v[196:199]
ds_read_b64_tr_b16 a[242:243],v2 offset:4864
ds_read_b64_tr_b16 a[244:245],v2 offset:8960
v_mfma_f32_16x16x32_bf16 v[204:207],a[156:159],v[240:243],v[40:43]
ds_read_b64_tr_b16 a[246:247],v2 offset:13056
v_add_u32 v2,s48,v2
v_mfma_f32_16x16x32_bf16 v[204:207],a[152:155],v[208:211],v[204:207]
ds_read_b64_tr_b16 a[252:253],v3 offset:8960
v_mfma_f32_16x16x32_bf16 v[204:207],a[148:151],v[176:179],v[204:207]
ds_read_b64_tr_b16 a[254:255],v3 offset:13056
v_mfma_f32_16x16x32_bf16 v[204:207],a[144:147],v[144:147],v[204:207]
ds_read_b64_tr_b16 a[248:249],v3 offset:768
ds_read_b64_tr_b16 a[250:251],v3 offset:4864
v_mfma_f32_16x16x32_bf16 v[212:215],a[160:163],v[144:147],v[40:43]
v_exp_f32 v174,v174
v_mfma_f32_16x16x32_bf16 v[212:215],a[164:167],v[176:179],v[212:215]
v_exp_f32 v175,v175
v_mfma_f32_16x16x32_bf16 v[212:215],a[168:171],v[208:211],v[212:215]
v_exp_f32 v180,v180
v_mfma_f32_16x16x32_bf16 v[212:215],a[172:175],v[240:243],v[212:215]
v_exp_f32 v181,v181
v_mfma_f32_16x16x32_bf16 v[220:223],a[188:191],v[240:243],v[40:43]
buffer_load_dwordx4 v5,s[20:23],s47 offen offset:3072 nt lds
v_mfma_f32_16x16x32_bf16 v[220:223],a[184:187],v[208:211],v[220:223]
v_exp_f32 v182,v182
v_mfma_f32_16x16x32_bf16 v[220:223],a[180:183],v[176:179],v[220:223]
v_exp_f32 v183,v183
v_mfma_f32_16x16x32_bf16 v[220:223],a[176:179],v[144:147],v[220:223]
s_waitcnt lgkmcnt(0)
v_cvt_pk_bf16_f32 v167,v174,v175
v_cvt_pk_bf16_f32 v180,v180,v181
v_mfma_f32_16x16x32_bf16 a[0:3],a[192:195],v[132:135],a[0:3]
v_exp_f32 v188,v188
v_mfma_f32_16x16x32_bf16 a[0:3],a[196:199],v[148:151],a[0:3]
v_exp_f32 v189,v189
v_mfma_f32_16x16x32_bf16 a[4:7],a[204:207],v[148:151],a[4:7]
v_exp_f32 v190,v190
v_mfma_f32_16x16x32_bf16 a[4:7],a[200:203],v[132:135],a[4:7]
v_exp_f32 v191,v191
v_mfma_f32_16x16x32_bf16 a[8:11],a[208:211],v[132:135],a[8:11]
s_memrealtime s[76:77]
v_cvt_pk_bf16_f32 v181,v182,v183
v_cvt_pk_bf16_f32 v182,v188,v189
v_mfma_f32_16x16x32_bf16 a[8:11],a[212:215],v[148:151],a[8:11]
v_cvt_pk_bf16_f32 v183,v190,v191
v_add_u32 v3,s48,v3
v_mfma_f32_16x16x32_bf16 a[12:15],a[220:223],v[148:151],a[12:15]
s_sub_u32 s48,32768,s62
v_exp_f32 v196,v196
v_mfma_f32_16x16x32_bf16 a[12:15],a[216:219],v[132:135],a[12:15]
s_add_u32 s50,s50,s48
v_exp_f32 v197,v197
v_mfma_f32_16x16x32_bf16 a[16:19],a[224:227],v[132:135],a[16:19]
s_add_u32 m0,s49,s52
v_exp_f32 v198,v198
v_mfma_f32_16x16x32_bf16 a[16:19],a[228:231],v[148:151],a[16:19]
s_add_u32 s49,s49,32768
v_exp_f32 v199,v199
v_mfma_f32_16x16x32_bf16 a[20:23],a[236:239],v[148:151],a[20:23]
v_cvt_pk_bf16_f32 v196,v196,v197
v_cvt_pk_bf16_f32 v197,v198,v199
v_mfma_f32_16x16x32_bf16 a[20:23],a[232:235],v[132:135],a[20:23]
s_waitcnt lgkmcnt(0)
buffer_load_dwordx4 v4,s[24:27],s47 offen nt lds
v_mfma_f32_16x16x32_bf16 a[24:27],a[240:243],v[132:135],a[24:27]
s_sub_u32 s79,s78,s76
v_exp_f32 v204,v204
v_mfma_f32_16x16x32_bf16 a[24:27],a[244:247],v[148:151],a[24:27]
s_add_u32 s78,s76,s60
v_exp_f32 v205,v205
v_mfma_f32_16x16x32_bf16 a[28:31],a[252:255],v[148:151],a[28:31]
s_cmp_gt_i32 s79,0
s_cbranch_scc1 L_ps10_%=
L_pr10_%=:
v_cvt_pk_bf16_f32 v198,v204,v205
v_mfma_f32_16x16x32_bf16 a[28:31],a[248:251],v[132:135],a[28:31]
v_exp_f32 v206,v206
v_mfma_f32_16x16x32_bf16 v[48:51],v[28:31],v[132:135],v[48:51]
v_exp_f32 v207,v207
v_mfma_f32_16x16x32_bf16 v[48:51],v[28:31],v[148:151],v[48:51]
v_exp_f32 v212,v212
v_mfma_f32_16x16x32_bf16 v[228:231],a[128:131],v[152:155],v[44:47]
s_waitcnt vmcnt(13)
v_exp_f32 v213,v213
v_mfma_f32_16x16x32_bf16 v[228:231],a[132:135],v[184:187],v[228:231]
s_barrier
ds_read_b128 a[128:131],v1
v_cvt_pk_bf16_f32 v199,v206,v207
v_mfma_f32_16x16x32_bf16 v[228:231],a[136:139],v[216:219],v[228:231]
v_exp_f32 v214,v214
v_mfma_f32_16x16x32_bf16 v[228:231],a[140:143],v[248:251],v[228:231]
ds_read_b128 a[132:135],v1 offset:256
v_cvt_pk_bf16_f32 v212,v212,v213
v_mfma_f32_16x16x32_bf16 v[236:239],a[156:159],v[248:251],v[44:47]
v_exp_f32 v215,v215
v_mfma_f32_16x16x32_bf16 v[236:239],a[152:155],v[216:219],v[236:239]
s_cmp_eq_u32 s49,163840
s_cselect_b32 s49,0,s49
ds_read_b128 a[136:139],v1 offset:512
v_mfma_f32_16x16x32_bf16 v[236:239],a[148:151],v[184:187],v[236:239]
buffer_load_dwordx4 v5,s[24:27],s47 offen offset:1024 nt lds
v_mfma_f32_16x16x32_bf16 v[236:239],a[144:147],v[152:155],v[236:239]
ds_read_b128 a[140:143],v1 offset:768
v_cvt_pk_bf16_f32 v213,v214,v215
v_mfma_f32_16x16x32_bf16 v[244:247],a[160:163],v[152:155],v[44:47]
v_exp_f32 v220,v220
v_mfma_f32_16x16x32_bf16 v[244:247],a[164:167],v[184:187],v[244:247]
s_cmp_eq_u32 s50,131072
s_cselect_b32 s62,163840,0
ds_read_b128 a[156:159],v1 offset:4864
v_mfma_f32_16x16x32_bf16 v[244:247],a[168:171],v[216:219],v[244:247]
v_exp_f32 v221,v221
v_mfma_f32_16x16x32_bf16 v[244:247],a[172:175],v[248:251],v[244:247]
ds_read_b128 a[152:155],v1 offset:4608
v_cvt_pk_bf16_f32 v214,v220,v221
v_mfma_f32_16x16x32_bf16 v[252:255],a[188:191],v[248:251],v[44:47]
v_exp_f32 v222,v222
v_mfma_f32_16x16x32_bf16 v[252:255],a[184:187],v[216:219],v[252:255]
ds_read_b128 a[148:151],v1 offset:4352
v_mfma_f32_16x16x32_bf16 v[252:255],a[180:183],v[184:187],v[252:255]
v_exp_f32 v223,v223
v_mfma_f32_16x16x32_bf16 v[252:255],a[176:179],v[152:155],v[252:255]
ds_read_b128 a[144:147],v1 offset:4096
v_cvt_pk_bf16_f32 v215,v222,v223
v_mfma_f32_16x16x32_bf16 a[32:35],a[192:195],v[164:167],a[32:35]
v_exp_f32 v228,v228
v_mfma_f32_16x16x32_bf16 a[32:35],a[196:199],v[180:183],a[32:35]
ds_read_b128 a[160:163],v1 offset:8192
v_mfma_f32_16x16x32_bf16 a[36:39],a[204:207],v[180:183],a[36:39]
v_exp_f32 v229,v229
v_mfma_f32_16x16x32_bf16 a[36:39],a[200:203],v[164:167],a[36:39]
ds_read_b128 a[164:167],v1 offset:8448
v_cvt_pk_bf16_f32 v228,v228,v229
v_mfma_f32_16x16x32_bf16 a[40:43],a[208:211],v[164:167],a[40:43]
buffer_load_dwordx4 v4,s[24:27],s47 offen offset:2048 nt lds
v_mfma_f32_16x16x32_bf16 a[40:43],a[212:215],v[180:183],a[40:43]
ds_read_b128 a[168:171],v1 offset:8704
v_mfma_f32_16x16x32_bf16 a[44:47],a[220:223],v[180:183],a[44:47]
v_exp_f32 v230,v230
v_mfma_f32_16x16x32_bf16 a[44:47],a[216:219],v[164:167],a[44:47]
ds_read_b128 a[172:175],v1 offset:8960
v_mfma_f32_16x16x32_bf16 a[48:51],a[224:227],v[164:167],a[48:51]
v_exp_f32 v231,v231
v_mfma_f32_16x16x32_bf16 a[48:51],a[228:231],v[180:183],a[48:51]
ds_read_b128 a[188:191],v1 offset:13056
v_cvt_pk_bf16_f32 v229,v230,v231
v_mfma_f32_16x16x32_bf16 a[52:55],a[236:239],v[180:183],a[52:55]
v_exp_f32 v236,v236
v_mfma_f32_16x16x32_bf16 a[52:55],a[232:235],v[164:167],a[52:55]
ds_read_b128 a[184:187],v1 offset:12800
v_mfma_f32_16x16x32_bf16 a[56:59],a[240:243],v[164:167],a[56:59]
v_exp_f32 v237,v237
v_mfma_f32_16x16x32_bf16 a[56:59],a[244:247],v[180:183],a[56:59]
ds_read_b128 a[180:183],v1 offset:12544
v_cvt_pk_bf16_f32 v230,v236,v237
v_mfma_f32_16x16x32_bf16 a[60:63],a[252:255],v[180:183],a[60:63]
v_exp_f32 v238,v238
v_mfma_f32_16x16x32_bf16 a[60:63],a[248:251],v[164:167],a[60:63]
ds_read_b128 a[176:179],v1 offset:12288
v_add_u32 v1,s48,v1
v_mfma_f32_16x16x32_bf16 v[52:55],v[28:31],v[164:167],v[52:55]
v_exp_f32 v239,v239
v_mfma_f32_16x16x32_bf16 v[52:55],v[28:31],v[180:183],v[52:55]
s_waitcnt lgkmcnt(3)
v_exp_f32 v244,v244
v_mfma_f32_16x16x32_bf16 v[132:135],a[128:131],v[128:131],v[32:35]
buffer_load_dwordx4 v5,s[24:27],s47 offen offset:3072 nt lds
v_mfma_f32_16x16x32_bf16 v[132:135],a[132:135],v[160:163],v[132:135]
s_add_u32 s47,s47,16384
s_and_b32 s47,s47,0x3ffff
s_or_b32 s47,s47,s46
v_mfma_f32_16x16x32_bf16 v[132:135],a[136:139],v[192:195],v[132:135]
s_and_b32 s46,s54,0xfffc0000
v_exp_f32 v245,v245
v_mfma_f32_16x16x32_bf16 v[132:135],a[140:143],v[224:227],v[132:135]
s_add_u32 s54,s54,16384
v_exp_f32 v246,v246
v_mfma_f32_16x16x32_bf16 v[140:143],a[156:159],v[224:227],v[32:35]
v_exp_f32 v247,v247
v_mfma_f32_16x16x32_bf16 v[140:143],a[152:155],v[192:195],v[140:143]
v_cvt_pk_bf16_f32 v231,v238,v239
v_cvt_pk_bf16_f32 v244,v244,v245
v_mfma_f32_16x16x32_bf16 v[140:143],a[148:151],v[160:163],v[140:143]
v_exp_f32 v252,v252
v_mfma_f32_16x16x32_bf16 v[140:143],a[144:147],v[128:131],v[140:143]
v_exp_f32 v253,v253
v_mfma_f32_16x16x32_bf16 v[148:151],a[160:163],v[128:131],v[32:35]
v_exp_f32 v254,v254
v_mfma_f32_16x16x32_bf16 v[148:151],a[164:167],v[160:163],v[148:151]
v_exp_f32 v255,v255
v_mfma_f32_16x16x32_bf16 v[148:151],a[168:171],v[192:195],v[148:151]
v_cvt_pk_bf16_f32 v245,v246,v247
v_cvt_pk_bf16_f32 v246,v252,v253
v_mfma_f32_16x16x32_bf16 v[148:151],a[172:175],v[224:227],v[148:151]
v_cvt_pk_bf16_f32 v247,v254,v255
v_mfma_f32_16x16x32_bf16 v[156:159],a[188:191],v[224:227],v[32:35]
s_add_u32 m0,s49,s51
v_exp_f32 v132,v132
s_waitcnt lgkmcnt(0)
v_mfma_f32_16x16x32_bf16 v[156:159],a[184:187],v[192:195],v[156:159]
v_exp_f32 v133,v133
v_mfma_f32_16x16x32_bf16 v[156:159],a[180:183],v[160:163],v[156:159]
v_exp_f32 v134,v134
v_mfma_f32_16x16x32_bf16 v[156:159],a[176:179],v[128:131],v[156:159]
buffer_load_dwordx4 v4,s[20:23],s47 offen nt lds
v_mfma_f32_16x16x32_bf16 a[64:67],a[192:195],v[196:199],a[64:67]
v_exp_f32 v135,v135
v_mfma_f32_16x16x32_bf16 a[64:67],a[196:199],v[212:215],a[64:67]
v_cvt_pk_bf16_f32 v132,v132,v133
v_cvt_pk_bf16_f32 v133,v134,v135
v_mfma_f32_16x16x32_bf16 a[68:71],a[204:207],v[212:215],a[68:71]
v_exp_f32 v140,v140
v_mfma_f32_16x16x32_bf16 a[68:71],a[200:203],v[196:199],a[68:71]
v_exp_f32 v141,v141
v_mfma_f32_16x16x32_bf16 a[72:75],a[208:211],v[196:199],a[72:75]
v_exp_f32 v142,v142
v_mfma_f32_16x16x32_bf16 a[72:75],a[212:215],v[212:215],a[72:75]
v_exp_f32 v143,v143
v_mfma_f32_16x16x32_bf16 a[76:79],a[220:223],v[212:215],a[76:79]
v_exp_f32 v148,v148
v_mfma_f32_16x16x32_bf16 a[76:79],a[216:219],v[196:199],a[76:79]
v_exp_f32 v149,v149
v_mfma_f32_16x16x32_bf16 a[80:83],a[224:227],v[196:199],a[80:83]
)A"
R"A(v_exp_f32 v150,v150
v_mfma_f32_16x16x32_bf16 a[80:83],a[228:231],v[212:215],a[80:83]
v_exp_f32 v151,v151
v_mfma_f32_16x16x32_bf16 a[84:87],a[236:239],v[212:215],a[84:87]
v_cvt_pk_bf16_f32 v134,v140,v141
v_cvt_pk_bf16_f32 v135,v142,v143
v_mfma_f32_16x16x32_bf16 a[84:87],a[232:235],v[196:199],a[84:87]
v_cvt_pk_bf16_f32 v148,v148,v149
v_cvt_pk_bf16_f32 v149,v150,v151
v_mfma_f32_16x16x32_bf16 a[88:91],a[240:243],v[196:199],a[88:91]
v_exp_f32 v156,v156
v_mfma_f32_16x16x32_bf16 a[88:91],a[244:247],v[212:215],a[88:91]
buffer_load_dwordx4 v4,s[20:23],s47 offen offset:1024 nt lds
v_mfma_f32_16x16x32_bf16 a[92:95],a[252:255],v[212:215],a[92:95]
v_exp_f32 v157,v157
v_mfma_f32_16x16x32_bf16 a[92:95],a[248:251],v[196:199],a[92:95]
v_exp_f32 v158,v158
v_mfma_f32_16x16x32_bf16 v[56:59],v[28:31],v[196:199],v[56:59]
v_exp_f32 v159,v159
v_mfma_f32_16x16x32_bf16 v[56:59],v[28:31],v[212:215],v[56:59]
v_cvt_pk_bf16_f32 v150,v156,v157
v_cvt_pk_bf16_f32 v151,v158,v159
v_mfma_f32_16x16x32_bf16 v[164:167],a[128:131],v[136:139],v[36:39]
v_mfma_f32_16x16x32_bf16 v[164:167],a[132:135],v[168:171],v[164:167]
v_mfma_f32_16x16x32_bf16 v[164:167],a[136:139],v[200:203],v[164:167]
v_mfma_f32_16x16x32_bf16 v[164:167],a[140:143],v[232:235],v[164:167]
v_mfma_f32_16x16x32_bf16 v[172:175],a[156:159],v[232:235],v[36:39]
v_mfma_f32_16x16x32_bf16 v[172:175],a[152:155],v[200:203],v[172:175]
v_mfma_f32_16x16x32_bf16 v[172:175],a[148:151],v[168:171],v[172:175]
v_mfma_f32_16x16x32_bf16 v[172:175],a[144:147],v[136:139],v[172:175]
v_mfma_f32_16x16x32_bf16 v[180:183],a[160:163],v[136:139],v[36:39]
v_mfma_f32_16x16x32_bf16 v[180:183],a[164:167],v[168:171],v[180:183]
buffer_load_dwordx4 v5,s[20:23],s47 offen offset:2048 nt lds
v_mfma_f32_16x16x32_bf16 v[180:183],a[168:171],v[200:203],v[180:183]
v_exp_f32 v164,v164
v_mfma_f32_16x16x32_bf16 v[180:183],a[172:175],v[232:235],v[180:183]
v_exp_f32 v165,v165
v_mfma_f32_16x16x32_bf16 v[188:191],a[188:191],v[232:235],v[36:39]
v_exp_f32 v166,v166
v_mfma_f32_16x16x32_bf16 v[188:191],a[184:187],v[200:203],v[188:191]
v_exp_f32 v167,v167
v_mfma_f32_16x16x32_bf16 v[188:191],a[180:183],v[168:171],v[188:191]
v_cvt_pk_bf16_f32 v164,v164,v165
v_cvt_pk_bf16_f32 v165,v166,v167
v_mfma_f32_16x16x32_bf16 v[188:191],a[176:179],v[136:139],v[188:191]
v_exp_f32 v172,v172
v_mfma_f32_16x16x32_bf16 a[96:99],a[192:195],v[228:231],a[96:99]
ds_read_b64_tr_b16 a[192:193],v2
ds_read_b64_tr_b16 a[194:195],v2 offset:4096
v_mfma_f32_16x16x32_bf16 a[96:99],a[196:199],v[244:247],a[96:99]
ds_read_b64_tr_b16 a[196:197],v2 offset:8192
v_mfma_f32_16x16x32_bf16 a[100:103],a[204:207],v[244:247],a[100:103]
ds_read_b64_tr_b16 a[198:199],v2 offset:12288
v_mfma_f32_16x16x32_bf16 a[100:103],a[200:203],v[228:231],a[100:103]
ds_read_b64_tr_b16 a[204:205],v3 offset:8192
v_mfma_f32_16x16x32_bf16 a[104:107],a[208:211],v[228:231],a[104:107]
ds_read_b64_tr_b16 a[206:207],v3 offset:12288
ds_read_b64_tr_b16 a[200:201],v3
v_mfma_f32_16x16x32_bf16 a[104:107],a[212:215],v[244:247],a[104:107]
ds_read_b64_tr_b16 a[202:203],v3 offset:4096
v_mfma_f32_16x16x32_bf16 a[108:111],a[220:223],v[244:247],a[108:111]
ds_read_b64_tr_b16 a[208:209],v2 offset:256
v_mfma_f32_16x16x32_bf16 a[108:111],a[216:219],v[228:231],a[108:111]
buffer_load_dwordx4 v5,s[20:23],s47 offen offset:3072 nt lds
v_mfma_f32_16x16x32_bf16 a[112:115],a[224:227],v[228:231],a[112:115]
ds_read_b64_tr_b16 a[210:211],v2 offset:4352
ds_read_b64_tr_b16 a[212:213],v2 offset:8448
v_mfma_f32_16x16x32_bf16 a[112:115],a[228:231],v[244:247],a[112:115]
ds_read_b64_tr_b16 a[214:215],v2 offset:12544
v_mfma_f32_16x16x32_bf16 a[116:119],a[236:239],v[244:247],a[116:119]
ds_read_b64_tr_b16 a[220:221],v3 offset:8448
v_mfma_f32_16x16x32_bf16 a[116:119],a[232:235],v[228:231],a[116:119]
ds_read_b64_tr_b16 a[222:223],v3 offset:12544
v_mfma_f32_16x16x32_bf16 a[120:123],a[240:243],v[228:231],a[120:123]
ds_read_b64_tr_b16 a[216:217],v3 offset:256
ds_read_b64_tr_b16 a[218:219],v3 offset:4352
v_mfma_f32_16x16x32_bf16 a[120:123],a[244:247],v[244:247],a[120:123]
ds_read_b64_tr_b16 a[224:225],v2 offset:512
v_mfma_f32_16x16x32_bf16 a[124:127],a[252:255],v[244:247],a[124:127]
ds_read_b64_tr_b16 a[226:227],v2 offset:4608
v_mfma_f32_16x16x32_bf16 a[124:127],a[248:251],v[228:231],a[124:127]
ds_read_b64_tr_b16 a[228:229],v2 offset:8704
v_mfma_f32_16x16x32_bf16 v[60:63],v[28:31],v[228:231],v[60:63]
ds_read_b64_tr_b16 a[230:231],v2 offset:12800
ds_read_b64_tr_b16 a[236:237],v3 offset:8704
v_mfma_f32_16x16x32_bf16 v[60:63],v[28:31],v[244:247],v[60:63]
ds_read_b64_tr_b16 a[238:239],v3 offset:12800
v_mfma_f32_16x16x32_bf16 v[196:199],a[128:131],v[144:147],v[40:43]
ds_read_b64_tr_b16 a[232:233],v3 offset:512
v_mfma_f32_16x16x32_bf16 v[196:199],a[132:135],v[176:179],v[196:199]
s_add_u32 m0,s49,s52
s_add_u32 s49,s49,32768
ds_read_b64_tr_b16 a[234:235],v3 offset:4608
v_mfma_f32_16x16x32_bf16 v[196:199],a[136:139],v[208:211],v[196:199]
ds_read_b64_tr_b16 a[240:241],v2 offset:768
ds_read_b64_tr_b16 a[242:243],v2 offset:4864
v_mfma_f32_16x16x32_bf16 v[196:199],a[140:143],v[240:243],v[196:199]
s_cmp_eq_u32 s49,163840
s_cselect_b32 s49,0,s49
ds_read_b64_tr_b16 a[244:245],v2 offset:8960
v_mfma_f32_16x16x32_bf16 v[204:207],a[156:159],v[240:243],v[40:43]
buffer_load_dwordx4 v4,s[24:27],s47 offen nt lds
v_mfma_f32_16x16x32_bf16 v[204:207],a[152:155],v[208:211],v[204:207]
ds_read_b64_tr_b16 a[246:247],v2 offset:13056
ds_read_b64_tr_b16 a[252:253],v3 offset:8960
v_mfma_f32_16x16x32_bf16 v[204:207],a[148:151],v[176:179],v[204:207]
ds_read_b64_tr_b16 a[254:255],v3 offset:13056
v_add_u32 v2,s48,v2
v_mfma_f32_16x16x32_bf16 v[204:207],a[144:147],v[144:147],v[204:207]
ds_read_b64_tr_b16 a[248:249],v3 offset:768
v_mfma_f32_16x16x32_bf16 v[212:215],a[160:163],v[144:147],v[40:43]
ds_read_b64_tr_b16 a[250:251],v3 offset:4864
v_add_u32 v3,s48,v3
v_mfma_f32_16x16x32_bf16 v[212:215],a[164:167],v[176:179],v[212:215]
s_sub_u32 s48,32768,s62
v_exp_f32 v173,v173
v_mfma_f32_16x16x32_bf16 v[212:215],a[168:171],v[208:211],v[212:215]
s_add_u32 s50,s50,s48
v_exp_f32 v174,v174
v_mfma_f32_16x16x32_bf16 v[212:215],a[172:175],v[240:243],v[212:215]
v_exp_f32 v175,v175
v_mfma_f32_16x16x32_bf16 v[220:223],a[188:191],v[240:243],v[40:43]
v_exp_f32 v180,v180
v_mfma_f32_16x16x32_bf16 v[220:223],a[184:187],v[208:211],v[220:223]
v_exp_f32 v181,v181
v_mfma_f32_16x16x32_bf16 v[220:223],a[180:183],v[176:179],v[220:223]
v_exp_f32 v182,v182
v_mfma_f32_16x16x32_bf16 v[220:223],a[176:179],v[144:147],v[220:223]
s_waitcnt lgkmcnt(1)
v_exp_f32 v183,v183
v_mfma_f32_16x16x32_bf16 a[0:3],a[192:195],v[132:135],a[0:3]
v_cvt_pk_bf16_f32 v166,v172,v173
v_cvt_pk_bf16_f32 v167,v174,v175
v_mfma_f32_16x16x32_bf16 a[0:3],a[196:199],v[148:151],a[0:3]
v_cvt_pk_bf16_f32 v180,v180,v181
v_cvt_pk_bf16_f32 v181,v182,v183
v_mfma_f32_16x16x32_bf16 a[4:7],a[204:207],v[148:151],a[4:7]
buffer_load_dwordx4 v5,s[24:27],s47 offen offset:1024 nt lds
v_mfma_f32_16x16x32_bf16 a[4:7],a[200:203],v[132:135],a[4:7]
v_exp_f32 v188,v188
v_mfma_f32_16x16x32_bf16 a[8:11],a[208:211],v[132:135],a[8:11]
s_memrealtime s[76:77]
v_exp_f32 v189,v189
v_mfma_f32_16x16x32_bf16 a[8:11],a[212:215],v[148:151],a[8:11]
v_exp_f32 v190,v190
v_mfma_f32_16x16x32_bf16 a[12:15],a[220:223],v[148:151],a[12:15]
v_exp_f32 v191,v191
v_mfma_f32_16x16x32_bf16 a[12:15],a[216:219],v[132:135],a[12:15]
v_cvt_pk_bf16_f32 v182,v188,v189
v_cvt_pk_bf16_f32 v183,v190,v191
v_mfma_f32_16x16x32_bf16 a[16:19],a[224:227],v[132:135],a[16:19]
v_exp_f32 v196,v196
v_mfma_f32_16x16x32_bf16 a[16:19],a[228:231],v[148:151],a[16:19]
v_exp_f32 v197,v197
v_mfma_f32_16x16x32_bf16 a[20:23],a[236:239],v[148:151],a[20:23]
v_exp_f32 v198,v198
v_mfma_f32_16x16x32_bf16 a[20:23],a[232:235],v[132:135],a[20:23]
s_waitcnt lgkmcnt(0)
v_exp_f32 v199,v199
v_mfma_f32_16x16x32_bf16 a[24:27],a[240:243],v[132:135],a[24:27]
s_sub_u32 s79,s78,s76
v_cvt_pk_bf16_f32 v196,v196,v197
v_cvt_pk_bf16_f32 v197,v198,v199
v_mfma_f32_16x16x32_bf16 a[24:27],a[244:247],v[148:151],a[24:27]
s_add_u32 s78,s76,s60
v_exp_f32 v204,v204
v_mfma_f32_16x16x32_bf16 a[28:31],a[252:255],v[148:151],a[28:31]
s_cmp_gt_i32 s79,0
s_cbranch_scc1 L_ps11_%=
L_pr11_%=:
v_mfma_f32_16x16x32_bf16 a[28:31],a[248:251],v[132:135],a[28:31]
v_exp_f32 v205,v205
v_mfma_f32_16x16x32_bf16 v[48:51],v[28:31],v[132:135],v[48:51]
buffer_load_dwordx4 v4,s[24:27],s47 offen offset:2048 nt lds
v_mfma_f32_16x16x32_bf16 v[48:51],v[28:31],v[148:151],v[48:51]
v_exp_f32 v206,v206
v_mfma_f32_16x16x32_bf16 v[228:231],a[128:131],v[152:155],v[44:47]
s_waitcnt vmcnt(15)
v_exp_f32 v207,v207
v_mfma_f32_16x16x32_bf16 v[228:231],a[132:135],v[184:187],v[228:231]
s_barrier
ds_read_b128 a[128:131],v1
v_cvt_pk_bf16_f32 v198,v204,v205
v_mfma_f32_16x16x32_bf16 v[228:231],a[136:139],v[216:219],v[228:231]
v_exp_f32 v212,v212
v_mfma_f32_16x16x32_bf16 v[228:231],a[140:143],v[248:251],v[228:231]
ds_read_b128 a[132:135],v1 offset:256
v_cvt_pk_bf16_f32 v199,v206,v207
v_mfma_f32_16x16x32_bf16 v[236:239],a[156:159],v[248:251],v[44:47]
v_exp_f32 v213,v213
v_mfma_f32_16x16x32_bf16 v[236:239],a[152:155],v[216:219],v[236:239]
ds_read_b128 a[136:139],v1 offset:512
v_cvt_pk_bf16_f32 v212,v212,v213
v_mfma_f32_16x16x32_bf16 v[236:239],a[148:151],v[184:187],v[236:239]
v_exp_f32 v214,v214
v_mfma_f32_16x16x32_bf16 v[236:239],a[144:147],v[152:155],v[236:239]
s_cmp_eq_u32 s50,131072
s_cselect_b32 s62,163840,0
ds_read_b128 a[140:143],v1 offset:768
v_mfma_f32_16x16x32_bf16 v[244:247],a[160:163],v[152:155],v[44:47]
v_exp_f32 v215,v215
v_mfma_f32_16x16x32_bf16 v[244:247],a[164:167],v[184:187],v[244:247]
ds_read_b128 a[156:159],v1 offset:4864
v_cvt_pk_bf16_f32 v213,v214,v215
v_mfma_f32_16x16x32_bf16 v[244:247],a[168:171],v[216:219],v[244:247]
v_exp_f32 v220,v220
v_mfma_f32_16x16x32_bf16 v[244:247],a[172:175],v[248:251],v[244:247]
buffer_load_dwordx4 v5,s[24:27],s47 offen offset:3072 nt lds
v_mfma_f32_16x16x32_bf16 v[252:255],a[188:191],v[248:251],v[44:47]
s_add_u32 s47,s47,16384
s_and_b32 s47,s47,0x3ffff
s_or_b32 s47,s47,s46
v_mfma_f32_16x16x32_bf16 v[252:255],a[184:187],v[216:219],v[252:255]
s_and_b32 s46,s54,0xfffc0000
ds_read_b128 a[152:155],v1 offset:4608
v_mfma_f32_16x16x32_bf16 v[252:255],a[180:183],v[184:187],v[252:255]
v_exp_f32 v221,v221
v_mfma_f32_16x16x32_bf16 v[252:255],a[176:179],v[152:155],v[252:255]
ds_read_b128 a[148:151],v1 offset:4352
v_cvt_pk_bf16_f32 v214,v220,v221
v_mfma_f32_16x16x32_bf16 a[32:35],a[192:195],v[164:167],a[32:35]
v_exp_f32 v222,v222
v_mfma_f32_16x16x32_bf16 a[32:35],a[196:199],v[180:183],a[32:35]
ds_read_b128 a[144:147],v1 offset:4096
v_mfma_f32_16x16x32_bf16 a[36:39],a[204:207],v[180:183],a[36:39]
v_exp_f32 v223,v223
v_mfma_f32_16x16x32_bf16 a[36:39],a[200:203],v[164:167],a[36:39]
ds_read_b128 a[160:163],v1 offset:8192
v_cvt_pk_bf16_f32 v215,v222,v223
v_mfma_f32_16x16x32_bf16 a[40:43],a[208:211],v[164:167],a[40:43]
v_exp_f32 v228,v228
v_mfma_f32_16x16x32_bf16 a[40:43],a[212:215],v[180:183],a[40:43]
ds_read_b128 a[164:167],v1 offset:8448
v_mfma_f32_16x16x32_bf16 a[44:47],a[220:223],v[180:183],a[44:47]
v_exp_f32 v229,v229
v_mfma_f32_16x16x32_bf16 a[44:47],a[216:219],v[164:167],a[44:47]
s_add_u32 m0,s49,s51
ds_read_b128 a[168:171],v1 offset:8704
v_cvt_pk_bf16_f32 v228,v228,v229
v_mfma_f32_16x16x32_bf16 a[48:51],a[224:227],v[164:167],a[48:51]
v_exp_f32 v230,v230
v_mfma_f32_16x16x32_bf16 a[48:51],a[228:231],v[180:183],a[48:51]
ds_read_b128 a[172:175],v1 offset:8960
v_mfma_f32_16x16x32_bf16 a[52:55],a[236:239],v[180:183],a[52:55]
buffer_load_dwordx4 v4,s[20:23],s47 offen nt lds
v_mfma_f32_16x16x32_bf16 a[52:55],a[232:235],v[164:167],a[52:55]
ds_read_b128 a[188:191],v1 offset:13056
v_mfma_f32_16x16x32_bf16 a[56:59],a[240:243],v[164:167],a[56:59]
v_exp_f32 v231,v231
v_mfma_f32_16x16x32_bf16 a[56:59],a[244:247],v[180:183],a[56:59]
ds_read_b128 a[184:187],v1 offset:12800
v_cvt_pk_bf16_f32 v229,v230,v231
v_mfma_f32_16x16x32_bf16 a[60:63],a[252:255],v[180:183],a[60:63]
v_exp_f32 v236,v236
v_mfma_f32_16x16x32_bf16 a[60:63],a[248:251],v[164:167],a[60:63]
ds_read_b128 a[180:183],v1 offset:12544
v_mfma_f32_16x16x32_bf16 v[52:55],v[28:31],v[164:167],v[52:55]
v_exp_f32 v237,v237
)A"
R"A(v_mfma_f32_16x16x32_bf16 v[52:55],v[28:31],v[180:183],v[52:55]
s_waitcnt lgkmcnt(3)
ds_read_b128 a[176:179],v1 offset:12288
v_cvt_pk_bf16_f32 v230,v236,v237
v_mfma_f32_16x16x32_bf16 v[132:135],a[128:131],v[128:131],v[32:35]
v_exp_f32 v238,v238
v_mfma_f32_16x16x32_bf16 v[132:135],a[132:135],v[160:163],v[132:135]
v_exp_f32 v239,v239
v_mfma_f32_16x16x32_bf16 v[132:135],a[136:139],v[192:195],v[132:135]
v_exp_f32 v244,v244
v_mfma_f32_16x16x32_bf16 v[132:135],a[140:143],v[224:227],v[132:135]
v_exp_f32 v245,v245
v_mfma_f32_16x16x32_bf16 v[140:143],a[156:159],v[224:227],v[32:35]
v_exp_f32 v246,v246
v_mfma_f32_16x16x32_bf16 v[140:143],a[152:155],v[192:195],v[140:143]
v_exp_f32 v247,v247
v_mfma_f32_16x16x32_bf16 v[140:143],a[148:151],v[160:163],v[140:143]
buffer_load_dwordx4 v4,s[20:23],s47 offen offset:1024 nt lds
v_mfma_f32_16x16x32_bf16 v[140:143],a[144:147],v[128:131],v[140:143]
v_cvt_pk_bf16_f32 v231,v238,v239
v_cvt_pk_bf16_f32 v244,v244,v245
v_mfma_f32_16x16x32_bf16 v[148:151],a[160:163],v[128:131],v[32:35]
v_exp_f32 v252,v252
v_mfma_f32_16x16x32_bf16 v[148:151],a[164:167],v[160:163],v[148:151]
v_exp_f32 v253,v253
v_mfma_f32_16x16x32_bf16 v[148:151],a[168:171],v[192:195],v[148:151]
v_exp_f32 v254,v254
v_mfma_f32_16x16x32_bf16 v[148:151],a[172:175],v[224:227],v[148:151]
s_waitcnt lgkmcnt(0)
v_exp_f32 v255,v255
v_mfma_f32_16x16x32_bf16 v[156:159],a[188:191],v[224:227],v[32:35]
v_cvt_pk_bf16_f32 v245,v246,v247
v_cvt_pk_bf16_f32 v246,v252,v253
v_mfma_f32_16x16x32_bf16 v[156:159],a[184:187],v[192:195],v[156:159]
v_cvt_pk_bf16_f32 v247,v254,v255
v_add_u32 v1,s48,v1
v_mfma_f32_16x16x32_bf16 v[156:159],a[180:183],v[160:163],v[156:159]
v_exp_f32 v132,v132
v_mfma_f32_16x16x32_bf16 v[156:159],a[176:179],v[128:131],v[156:159]
v_exp_f32 v133,v133
v_mfma_f32_16x16x32_bf16 a[64:67],a[192:195],v[196:199],a[64:67]
v_exp_f32 v134,v134
v_mfma_f32_16x16x32_bf16 a[64:67],a[196:199],v[212:215],a[64:67]
v_exp_f32 v135,v135
v_mfma_f32_16x16x32_bf16 a[68:71],a[204:207],v[212:215],a[68:71]
v_cvt_pk_bf16_f32 v132,v132,v133
v_cvt_pk_bf16_f32 v133,v134,v135
v_mfma_f32_16x16x32_bf16 a[68:71],a[200:203],v[196:199],a[68:71]
v_exp_f32 v140,v140
v_mfma_f32_16x16x32_bf16 a[72:75],a[208:211],v[196:199],a[72:75]
buffer_load_dwordx4 v5,s[20:23],s47 offen offset:2048 nt lds
v_mfma_f32_16x16x32_bf16 a[72:75],a[212:215],v[212:215],a[72:75]
v_exp_f32 v141,v141
v_mfma_f32_16x16x32_bf16 a[76:79],a[220:223],v[212:215],a[76:79]
v_exp_f32 v142,v142
v_mfma_f32_16x16x32_bf16 a[76:79],a[216:219],v[196:199],a[76:79]
v_exp_f32 v143,v143
v_mfma_f32_16x16x32_bf16 a[80:83],a[224:227],v[196:199],a[80:83]
v_exp_f32 v148,v148
v_mfma_f32_16x16x32_bf16 a[80:83],a[228:231],v[212:215],a[80:83]
v_exp_f32 v149,v149
v_mfma_f32_16x16x32_bf16 a[84:87],a[236:239],v[212:215],a[84:87]
v_exp_f32 v150,v150
v_mfma_f32_16x16x32_bf16 a[84:87],a[232:235],v[196:199],a[84:87]
v_exp_f32 v151,v151
v_mfma_f32_16x16x32_bf16 a[88:91],a[240:243],v[196:199],a[88:91]
v_cvt_pk_bf16_f32 v134,v140,v141
v_cvt_pk_bf16_f32 v135,v142,v143
v_mfma_f32_16x16x32_bf16 a[88:91],a[244:247],v[212:215],a[88:91]
v_cvt_pk_bf16_f32 v148,v148,v149
v_cvt_pk_bf16_f32 v149,v150,v151
v_mfma_f32_16x16x32_bf16 a[92:95],a[252:255],v[212:215],a[92:95]
v_exp_f32 v156,v156
v_mfma_f32_16x16x32_bf16 a[92:95],a[248:251],v[196:199],a[92:95]
v_exp_f32 v157,v157
v_mfma_f32_16x16x32_bf16 v[56:59],v[28:31],v[196:199],v[56:59]
v_exp_f32 v158,v158
v_mfma_f32_16x16x32_bf16 v[56:59],v[28:31],v[212:215],v[56:59]
v_exp_f32 v159,v159
v_mfma_f32_16x16x32_bf16 v[164:167],a[128:131],v[136:139],v[36:39]
buffer_load_dwordx4 v5,s[20:23],s47 offen offset:3072 nt lds
v_mfma_f32_16x16x32_bf16 v[164:167],a[132:135],v[168:171],v[164:167]
v_cvt_pk_bf16_f32 v150,v156,v157
v_cvt_pk_bf16_f32 v151,v158,v159
v_mfma_f32_16x16x32_bf16 v[164:167],a[136:139],v[200:203],v[164:167]
v_mfma_f32_16x16x32_bf16 v[164:167],a[140:143],v[232:235],v[164:167]
v_mfma_f32_16x16x32_bf16 v[172:175],a[156:159],v[232:235],v[36:39]
v_mfma_f32_16x16x32_bf16 v[172:175],a[152:155],v[200:203],v[172:175]
v_mfma_f32_16x16x32_bf16 v[172:175],a[148:151],v[168:171],v[172:175]
v_mfma_f32_16x16x32_bf16 v[172:175],a[144:147],v[136:139],v[172:175]
v_mfma_f32_16x16x32_bf16 v[180:183],a[160:163],v[136:139],v[36:39]
v_mfma_f32_16x16x32_bf16 v[180:183],a[164:167],v[168:171],v[180:183]
s_nop 1
v_exp_f32 v164,v164
v_mfma_f32_16x16x32_bf16 v[180:183],a[168:171],v[200:203],v[180:183]
v_exp_f32 v165,v165
v_mfma_f32_16x16x32_bf16 v[180:183],a[172:175],v[232:235],v[180:183]
v_exp_f32 v166,v166
v_mfma_f32_16x16x32_bf16 v[188:191],a[188:191],v[232:235],v[36:39]
s_add_u32 m0,s49,s52
v_exp_f32 v167,v167
v_mfma_f32_16x16x32_bf16 v[188:191],a[184:187],v[200:203],v[188:191]
s_add_u32 s49,s49,32768
v_cvt_pk_bf16_f32 v164,v164,v165
v_cvt_pk_bf16_f32 v165,v166,v167
v_mfma_f32_16x16x32_bf16 v[188:191],a[180:183],v[168:171],v[188:191]
v_exp_f32 v172,v172
v_mfma_f32_16x16x32_bf16 v[188:191],a[176:179],v[136:139],v[188:191]
buffer_load_dwordx4 v4,s[24:27],s47 offen nt lds
v_mfma_f32_16x16x32_bf16 a[96:99],a[192:195],v[228:231],a[96:99]
ds_read_b64_tr_b16 a[192:193],v2
ds_read_b64_tr_b16 a[194:195],v2 offset:4096
v_mfma_f32_16x16x32_bf16 a[96:99],a[196:199],v[244:247],a[96:99]
s_cmp_eq_u32 s49,163840
s_cselect_b32 s49,0,s49
ds_read_b64_tr_b16 a[196:197],v2 offset:8192
v_mfma_f32_16x16x32_bf16 a[100:103],a[204:207],v[244:247],a[100:103]
s_add_u32 s49,s49,32768
ds_read_b64_tr_b16 a[198:199],v2 offset:12288
v_mfma_f32_16x16x32_bf16 a[100:103],a[200:203],v[228:231],a[100:103]
s_cmp_eq_u32 s49,163840
s_cselect_b32 s49,0,s49
ds_read_b64_tr_b16 a[204:205],v3 offset:8192
v_mfma_f32_16x16x32_bf16 a[104:107],a[208:211],v[228:231],a[104:107]
s_add_u32 s49,s49,32768
ds_read_b64_tr_b16 a[206:207],v3 offset:12288
ds_read_b64_tr_b16 a[200:201],v3
v_mfma_f32_16x16x32_bf16 a[104:107],a[212:215],v[244:247],a[104:107]
s_cmp_eq_u32 s49,163840
s_cselect_b32 s49,0,s49
ds_read_b64_tr_b16 a[202:203],v3 offset:4096
v_mfma_f32_16x16x32_bf16 a[108:111],a[220:223],v[244:247],a[108:111]
s_add_u32 s49,s49,32768
ds_read_b64_tr_b16 a[208:209],v2 offset:256
v_mfma_f32_16x16x32_bf16 a[108:111],a[216:219],v[228:231],a[108:111]
s_cmp_eq_u32 s49,163840
s_cselect_b32 s49,0,s49
ds_read_b64_tr_b16 a[210:211],v2 offset:4352
v_mfma_f32_16x16x32_bf16 a[112:115],a[224:227],v[228:231],a[112:115]
ds_read_b64_tr_b16 a[212:213],v2 offset:8448
ds_read_b64_tr_b16 a[214:215],v2 offset:12544
v_mfma_f32_16x16x32_bf16 a[112:115],a[228:231],v[244:247],a[112:115]
ds_read_b64_tr_b16 a[220:221],v3 offset:8448
v_mfma_f32_16x16x32_bf16 a[116:119],a[236:239],v[244:247],a[116:119]
ds_read_b64_tr_b16 a[222:223],v3 offset:12544
v_mfma_f32_16x16x32_bf16 a[116:119],a[232:235],v[228:231],a[116:119]
ds_read_b64_tr_b16 a[216:217],v3 offset:256
v_mfma_f32_16x16x32_bf16 a[120:123],a[240:243],v[228:231],a[120:123]
ds_read_b64_tr_b16 a[218:219],v3 offset:4352
ds_read_b64_tr_b16 a[224:225],v2 offset:512
v_mfma_f32_16x16x32_bf16 a[120:123],a[244:247],v[244:247],a[120:123]
buffer_load_dwordx4 v5,s[24:27],s47 offen offset:1024 nt lds
v_mfma_f32_16x16x32_bf16 a[124:127],a[252:255],v[244:247],a[124:127]
ds_read_b64_tr_b16 a[226:227],v2 offset:4608
ds_read_b64_tr_b16 a[228:229],v2 offset:8704
v_mfma_f32_16x16x32_bf16 a[124:127],a[248:251],v[228:231],a[124:127]
ds_read_b64_tr_b16 a[230:231],v2 offset:12800
v_mfma_f32_16x16x32_bf16 v[60:63],v[28:31],v[228:231],v[60:63]
ds_read_b64_tr_b16 a[236:237],v3 offset:8704
v_mfma_f32_16x16x32_bf16 v[60:63],v[28:31],v[244:247],v[60:63]
ds_read_b64_tr_b16 a[238:239],v3 offset:12800
v_mfma_f32_16x16x32_bf16 v[196:199],a[128:131],v[144:147],v[40:43]
ds_read_b64_tr_b16 a[232:233],v3 offset:512
ds_read_b64_tr_b16 a[234:235],v3 offset:4608
v_mfma_f32_16x16x32_bf16 v[196:199],a[132:135],v[176:179],v[196:199]
ds_read_b64_tr_b16 a[240:241],v2 offset:768
v_mfma_f32_16x16x32_bf16 v[196:199],a[136:139],v[208:211],v[196:199]
ds_read_b64_tr_b16 a[242:243],v2 offset:4864
v_mfma_f32_16x16x32_bf16 v[196:199],a[140:143],v[240:243],v[196:199]
ds_read_b64_tr_b16 a[244:245],v2 offset:8960
v_mfma_f32_16x16x32_bf16 v[204:207],a[156:159],v[240:243],v[40:43]
ds_read_b64_tr_b16 a[246:247],v2 offset:13056
ds_read_b64_tr_b16 a[252:253],v3 offset:8960
v_mfma_f32_16x16x32_bf16 v[204:207],a[152:155],v[208:211],v[204:207]
ds_read_b64_tr_b16 a[254:255],v3 offset:13056
v_add_u32 v2,s48,v2
v_mfma_f32_16x16x32_bf16 v[204:207],a[148:151],v[176:179],v[204:207]
ds_read_b64_tr_b16 a[248:249],v3 offset:768
v_mfma_f32_16x16x32_bf16 v[204:207],a[144:147],v[144:147],v[204:207]
ds_read_b64_tr_b16 a[250:251],v3 offset:4864
v_add_u32 v3,s48,v3
v_mfma_f32_16x16x32_bf16 v[212:215],a[160:163],v[144:147],v[40:43]
s_sub_u32 s48,32768,s62
v_exp_f32 v173,v173
v_mfma_f32_16x16x32_bf16 v[212:215],a[164:167],v[176:179],v[212:215]
s_add_u32 s50,s50,s48
buffer_load_dwordx4 v4,s[24:27],s47 offen offset:2048 nt lds
v_mfma_f32_16x16x32_bf16 v[212:215],a[168:171],v[208:211],v[212:215]
v_exp_f32 v174,v174
v_mfma_f32_16x16x32_bf16 v[212:215],a[172:175],v[240:243],v[212:215]
v_exp_f32 v175,v175
v_mfma_f32_16x16x32_bf16 v[220:223],a[188:191],v[240:243],v[40:43]
v_exp_f32 v180,v180
v_mfma_f32_16x16x32_bf16 v[220:223],a[184:187],v[208:211],v[220:223]
v_exp_f32 v181,v181
v_mfma_f32_16x16x32_bf16 v[220:223],a[180:183],v[176:179],v[220:223]
v_exp_f32 v182,v182
v_mfma_f32_16x16x32_bf16 v[220:223],a[176:179],v[144:147],v[220:223]
s_waitcnt lgkmcnt(0)
v_exp_f32 v183,v183
v_mfma_f32_16x16x32_bf16 a[0:3],a[192:195],v[132:135],a[0:3]
v_cvt_pk_bf16_f32 v166,v172,v173
v_cvt_pk_bf16_f32 v167,v174,v175
v_mfma_f32_16x16x32_bf16 a[0:3],a[196:199],v[148:151],a[0:3]
v_cvt_pk_bf16_f32 v180,v180,v181
v_cvt_pk_bf16_f32 v181,v182,v183
v_mfma_f32_16x16x32_bf16 a[4:7],a[204:207],v[148:151],a[4:7]
v_exp_f32 v188,v188
v_mfma_f32_16x16x32_bf16 a[4:7],a[200:203],v[132:135],a[4:7]
v_exp_f32 v189,v189
v_mfma_f32_16x16x32_bf16 a[8:11],a[208:211],v[132:135],a[8:11]
s_memrealtime s[76:77]
v_exp_f32 v190,v190
v_mfma_f32_16x16x32_bf16 a[8:11],a[212:215],v[148:151],a[8:11]
v_exp_f32 v191,v191
v_mfma_f32_16x16x32_bf16 a[12:15],a[220:223],v[148:151],a[12:15]
s_add_u32 s80,s71,0
s_and_b32 s80,s80,3
s_lshl_b32 s80,s80,1
v_mfma_f32_16x16x32_bf16 a[12:15],a[216:219],v[132:135],a[12:15]
s_mul_i32 s80,s80,163840
buffer_load_dwordx4 v5,s[24:27],s47 offen offset:3072 nt lds
v_mfma_f32_16x16x32_bf16 a[16:19],a[224:227],v[132:135],a[16:19]
s_add_u32 s80,s80,s81
v_cvt_pk_bf16_f32 v182,v188,v189
v_cvt_pk_bf16_f32 v183,v190,v191
v_mfma_f32_16x16x32_bf16 a[16:19],a[228:231],v[148:151],a[16:19]
s_add_u32 s83,s80,20480
v_exp_f32 v196,v196
v_mfma_f32_16x16x32_bf16 a[20:23],a[236:239],v[148:151],a[20:23]
s_add_u32 s47,s47,16384
v_exp_f32 v197,v197
v_mfma_f32_16x16x32_bf16 a[20:23],a[232:235],v[132:135],a[20:23]
s_waitcnt lgkmcnt(0)
v_exp_f32 v198,v198
v_mfma_f32_16x16x32_bf16 a[24:27],a[240:243],v[132:135],a[24:27]
s_sub_u32 s79,s78,s76
v_exp_f32 v199,v199
v_mfma_f32_16x16x32_bf16 a[24:27],a[244:247],v[148:151],a[24:27]
s_add_u32 s78,s76,s60
v_cvt_pk_bf16_f32 v196,v196,v197
v_cvt_pk_bf16_f32 v197,v198,v199
v_mfma_f32_16x16x32_bf16 a[28:31],a[252:255],v[148:151],a[28:31]
s_cmp_gt_i32 s79,0
s_cbranch_scc1 L_ps12_%=
L_pr12_%=:
v_lshrrev_b32 v4,6,v6
v_mfma_f32_16x16x32_bf16 a[28:31],a[248:251],v[132:135],a[28:31]
s_and_b32 s47,s47,0x3ffff
v_exp_f32 v204,v204
v_mfma_f32_16x16x32_bf16 v[48:51],v[28:31],v[132:135],v[48:51]
s_or_b32 s47,s47,s46
v_exp_f32 v205,v205
v_mfma_f32_16x16x32_bf16 v[48:51],v[28:31],v[148:151],v[48:51]
v_exp_f32 v206,v206
v_mfma_f32_16x16x32_bf16 v[228:231],a[128:131],v[152:155],v[44:47]
s_waitcnt vmcnt(16)
v_exp_f32 v207,v207
v_mfma_f32_16x16x32_bf16 v[228:231],a[132:135],v[184:187],v[228:231]
s_barrier
ds_read_b128 a[128:131],v1
v_cvt_pk_bf16_f32 v198,v204,v205
v_mfma_f32_16x16x32_bf16 v[228:231],a[136:139],v[216:219],v[228:231]
v_exp_f32 v212,v212
v_mfma_f32_16x16x32_bf16 v[228:231],a[140:143],v[248:251],v[228:231]
ds_read_b128 a[132:135],v1 offset:256
v_cvt_pk_bf16_f32 v199,v206,v207
v_mfma_f32_16x16x32_bf16 v[236:239],a[156:159],v[248:251],v[44:47]
v_exp_f32 v213,v213
v_mfma_f32_16x16x32_bf16 v[236:239],a[152:155],v[216:219],v[236:239]
)A"
R"A(ds_read_b128 a[136:139],v1 offset:512
v_cvt_pk_bf16_f32 v212,v212,v213
v_mfma_f32_16x16x32_bf16 v[236:239],a[148:151],v[184:187],v[236:239]
v_exp_f32 v214,v214
v_mfma_f32_16x16x32_bf16 v[236:239],a[144:147],v[152:155],v[236:239]
s_cmp_eq_u32 s50,131072
s_cselect_b32 s62,163840,0
ds_read_b128 a[140:143],v1 offset:768
v_mfma_f32_16x16x32_bf16 v[244:247],a[160:163],v[152:155],v[44:47]
s_add_u32 m0,s82,0
v_exp_f32 v215,v215
v_mfma_f32_16x16x32_bf16 v[244:247],a[164:167],v[184:187],v[244:247]
ds_read_b128 a[156:159],v1 offset:4864
v_cvt_pk_bf16_f32 v213,v214,v215
v_mfma_f32_16x16x32_bf16 v[244:247],a[168:171],v[216:219],v[244:247]
v_exp_f32 v220,v220
v_mfma_f32_16x16x32_bf16 v[244:247],a[172:175],v[248:251],v[244:247]
buffer_load_dwordx4 v7,s[72:75],s80 offen lds
v_mfma_f32_16x16x32_bf16 v[252:255],a[188:191],v[248:251],v[44:47]
ds_read_b128 a[152:155],v1 offset:4608
v_mul_u32_u24 v4,320,v4
v_mfma_f32_16x16x32_bf16 v[252:255],a[184:187],v[216:219],v[252:255]
v_exp_f32 v221,v221
v_mfma_f32_16x16x32_bf16 v[252:255],a[180:183],v[184:187],v[252:255]
ds_read_b128 a[148:151],v1 offset:4352
v_cvt_pk_bf16_f32 v214,v220,v221
v_mfma_f32_16x16x32_bf16 v[252:255],a[176:179],v[152:155],v[252:255]
v_exp_f32 v222,v222
v_mfma_f32_16x16x32_bf16 a[32:35],a[192:195],v[164:167],a[32:35]
ds_read_b128 a[144:147],v1 offset:4096
v_bfe_u32 v5,v6,4,2
v_mfma_f32_16x16x32_bf16 a[32:35],a[196:199],v[180:183],a[32:35]
v_exp_f32 v223,v223
v_mfma_f32_16x16x32_bf16 a[36:39],a[204:207],v[180:183],a[36:39]
ds_read_b128 a[160:163],v1 offset:8192
v_cvt_pk_bf16_f32 v215,v222,v223
v_mfma_f32_16x16x32_bf16 a[36:39],a[200:203],v[164:167],a[36:39]
v_exp_f32 v228,v228
v_mfma_f32_16x16x32_bf16 a[40:43],a[208:211],v[164:167],a[40:43]
ds_read_b128 a[164:167],v1 offset:8448
v_lshl_add_u32 v4,v5,3,v4
v_mfma_f32_16x16x32_bf16 a[40:43],a[212:215],v[180:183],a[40:43]
v_exp_f32 v229,v229
v_mfma_f32_16x16x32_bf16 a[44:47],a[220:223],v[180:183],a[44:47]
ds_read_b128 a[168:171],v1 offset:8704
v_cvt_pk_bf16_f32 v228,v228,v229
v_mfma_f32_16x16x32_bf16 a[44:47],a[216:219],v[164:167],a[44:47]
v_exp_f32 v230,v230
v_mfma_f32_16x16x32_bf16 a[48:51],a[224:227],v[164:167],a[48:51]
buffer_load_dwordx4 v8,s[72:75],s80 offen offset:1024 lds
v_mfma_f32_16x16x32_bf16 a[48:51],a[228:231],v[180:183],a[48:51]
ds_read_b128 a[172:175],v1 offset:8960
v_bfe_u32 v5,v6,8,1
v_mfma_f32_16x16x32_bf16 a[52:55],a[236:239],v[180:183],a[52:55]
v_exp_f32 v231,v231
v_mfma_f32_16x16x32_bf16 a[52:55],a[232:235],v[164:167],a[52:55]
ds_read_b128 a[188:191],v1 offset:13056
v_cvt_pk_bf16_f32 v229,v230,v231
v_mfma_f32_16x16x32_bf16 a[56:59],a[240:243],v[164:167],a[56:59]
v_exp_f32 v236,v236
v_mfma_f32_16x16x32_bf16 a[56:59],a[244:247],v[180:183],a[56:59]
ds_read_b128 a[184:187],v1 offset:12800
v_lshlrev_b32 v5,5,v5
v_mfma_f32_16x16x32_bf16 a[60:63],a[252:255],v[180:183],a[60:63]
v_exp_f32 v237,v237
v_mfma_f32_16x16x32_bf16 a[60:63],a[248:251],v[164:167],a[60:63]
ds_read_b128 a[180:183],v1 offset:12544
v_cvt_pk_bf16_f32 v230,v236,v237
v_mfma_f32_16x16x32_bf16 v[52:55],v[28:31],v[164:167],v[52:55]
v_exp_f32 v238,v238
v_mfma_f32_16x16x32_bf16 v[52:55],v[28:31],v[180:183],v[52:55]
s_waitcnt lgkmcnt(3)
ds_read_b128 a[176:179],v1 offset:12288
v_add_u32 v1,s48,v1
v_mfma_f32_16x16x32_bf16 v[132:135],a[128:131],v[128:131],v[32:35]
v_exp_f32 v239,v239
v_mfma_f32_16x16x32_bf16 v[132:135],a[132:135],v[160:163],v[132:135]
v_exp_f32 v244,v244
v_mfma_f32_16x16x32_bf16 v[132:135],a[136:139],v[192:195],v[132:135]
v_exp_f32 v245,v245
v_mfma_f32_16x16x32_bf16 v[132:135],a[140:143],v[224:227],v[132:135]
buffer_load_dwordx4 v9,s[72:75],s80 offen offset:2048 lds
v_mfma_f32_16x16x32_bf16 v[140:143],a[156:159],v[224:227],v[32:35]
v_exp_f32 v246,v246
v_mfma_f32_16x16x32_bf16 v[140:143],a[152:155],v[192:195],v[140:143]
v_exp_f32 v247,v247
v_mfma_f32_16x16x32_bf16 v[140:143],a[148:151],v[160:163],v[140:143]
v_cvt_pk_bf16_f32 v231,v238,v239
v_cvt_pk_bf16_f32 v244,v244,v245
v_mfma_f32_16x16x32_bf16 v[140:143],a[144:147],v[128:131],v[140:143]
v_exp_f32 v252,v252
v_mfma_f32_16x16x32_bf16 v[148:151],a[160:163],v[128:131],v[32:35]
buffer_store_dwordx4 v[64:67],v6,s[36:39],s84 offen sc1
v_mfma_f32_16x16x32_bf16 v[148:151],a[164:167],v[160:163],v[148:151]
v_exp_f32 v253,v253
v_mfma_f32_16x16x32_bf16 v[148:151],a[168:171],v[192:195],v[148:151]
v_exp_f32 v254,v254
v_mfma_f32_16x16x32_bf16 v[148:151],a[172:175],v[224:227],v[148:151]
s_waitcnt lgkmcnt(0)
v_exp_f32 v255,v255
v_mfma_f32_16x16x32_bf16 v[156:159],a[188:191],v[224:227],v[32:35]
v_cvt_pk_bf16_f32 v245,v246,v247
v_cvt_pk_bf16_f32 v246,v252,v253
v_mfma_f32_16x16x32_bf16 v[156:159],a[184:187],v[192:195],v[156:159]
v_cvt_pk_bf16_f32 v247,v254,v255
v_mfma_f32_16x16x32_bf16 v[156:159],a[180:183],v[160:163],v[156:159]
v_exp_f32 v132,v132
v_mfma_f32_16x16x32_bf16 v[156:159],a[176:179],v[128:131],v[156:159]
v_exp_f32 v133,v133
v_mfma_f32_16x16x32_bf16 a[64:67],a[192:195],v[196:199],a[64:67]
buffer_load_dwordx4 v10,s[72:75],s80 offen offset:3072 lds
v_mfma_f32_16x16x32_bf16 a[64:67],a[196:199],v[212:215],a[64:67]
v_exp_f32 v134,v134
v_mfma_f32_16x16x32_bf16 a[68:71],a[204:207],v[212:215],a[68:71]
v_exp_f32 v135,v135
v_mfma_f32_16x16x32_bf16 a[68:71],a[200:203],v[196:199],a[68:71]
v_cvt_pk_bf16_f32 v132,v132,v133
v_cvt_pk_bf16_f32 v133,v134,v135
v_mfma_f32_16x16x32_bf16 a[72:75],a[208:211],v[196:199],a[72:75]
v_exp_f32 v140,v140
v_mfma_f32_16x16x32_bf16 a[72:75],a[212:215],v[212:215],a[72:75]
v_exp_f32 v141,v141
v_mfma_f32_16x16x32_bf16 a[76:79],a[220:223],v[212:215],a[76:79]
v_exp_f32 v142,v142
v_mfma_f32_16x16x32_bf16 a[76:79],a[216:219],v[196:199],a[76:79]
v_exp_f32 v143,v143
v_mfma_f32_16x16x32_bf16 a[80:83],a[224:227],v[196:199],a[80:83]
v_exp_f32 v148,v148
v_mfma_f32_16x16x32_bf16 a[80:83],a[228:231],v[212:215],a[80:83]
v_exp_f32 v149,v149
v_mfma_f32_16x16x32_bf16 a[84:87],a[236:239],v[212:215],a[84:87]
s_add_u32 m0,s82,4096
v_exp_f32 v150,v150
v_mfma_f32_16x16x32_bf16 a[84:87],a[232:235],v[196:199],a[84:87]
v_exp_f32 v151,v151
v_mfma_f32_16x16x32_bf16 a[88:91],a[240:243],v[196:199],a[88:91]
v_cvt_pk_bf16_f32 v134,v140,v141
v_cvt_pk_bf16_f32 v135,v142,v143
v_mfma_f32_16x16x32_bf16 a[88:91],a[244:247],v[212:215],a[88:91]
buffer_load_dwordx4 v11,s[72:75],s80 offen lds
v_mfma_f32_16x16x32_bf16 a[92:95],a[252:255],v[212:215],a[92:95]
v_cvt_pk_bf16_f32 v148,v148,v149
v_cvt_pk_bf16_f32 v149,v150,v151
v_mfma_f32_16x16x32_bf16 a[92:95],a[248:251],v[196:199],a[92:95]
v_exp_f32 v156,v156
v_mfma_f32_16x16x32_bf16 v[56:59],v[28:31],v[196:199],v[56:59]
v_exp_f32 v157,v157
v_mfma_f32_16x16x32_bf16 v[56:59],v[28:31],v[212:215],v[56:59]
buffer_store_dwordx4 v[68:71],v6,s[36:39],s84 offen offset:1024 sc1
v_mfma_f32_16x16x32_bf16 v[164:167],a[128:131],v[136:139],v[36:39]
v_exp_f32 v158,v158
v_mfma_f32_16x16x32_bf16 v[164:167],a[132:135],v[168:171],v[164:167]
v_exp_f32 v159,v159
v_mfma_f32_16x16x32_bf16 v[164:167],a[136:139],v[200:203],v[164:167]
v_cvt_pk_bf16_f32 v150,v156,v157
v_cvt_pk_bf16_f32 v151,v158,v159
v_mfma_f32_16x16x32_bf16 v[164:167],a[140:143],v[232:235],v[164:167]
v_mfma_f32_16x16x32_bf16 v[172:175],a[156:159],v[232:235],v[36:39]
v_mfma_f32_16x16x32_bf16 v[172:175],a[152:155],v[200:203],v[172:175]
s_add_u32 m0,s82,5120
v_mfma_f32_16x16x32_bf16 v[172:175],a[148:151],v[168:171],v[172:175]
v_mfma_f32_16x16x32_bf16 v[172:175],a[144:147],v[136:139],v[172:175]
v_mfma_f32_16x16x32_bf16 v[180:183],a[160:163],v[136:139],v[36:39]
buffer_load_dwordx4 v7,s[72:75],s83 offen lds
v_mfma_f32_16x16x32_bf16 v[180:183],a[164:167],v[168:171],v[180:183]
v_exp_f32 v164,v164
v_mfma_f32_16x16x32_bf16 v[180:183],a[168:171],v[200:203],v[180:183]
v_exp_f32 v165,v165
v_mfma_f32_16x16x32_bf16 v[180:183],a[172:175],v[232:235],v[180:183]
v_exp_f32 v166,v166
v_mfma_f32_16x16x32_bf16 v[188:191],a[188:191],v[232:235],v[36:39]
v_exp_f32 v167,v167
v_mfma_f32_16x16x32_bf16 v[188:191],a[184:187],v[200:203],v[188:191]
v_cvt_pk_bf16_f32 v164,v164,v165
v_cvt_pk_bf16_f32 v165,v166,v167
v_mfma_f32_16x16x32_bf16 v[188:191],a[180:183],v[168:171],v[188:191]
v_exp_f32 v172,v172
v_mfma_f32_16x16x32_bf16 v[188:191],a[176:179],v[136:139],v[188:191]
v_exp_f32 v173,v173
v_mfma_f32_16x16x32_bf16 a[96:99],a[192:195],v[228:231],a[96:99]
ds_read_b64_tr_b16 a[192:193],v2
ds_read_b64_tr_b16 a[194:195],v2 offset:4096
v_mfma_f32_16x16x32_bf16 a[96:99],a[196:199],v[244:247],a[96:99]
ds_read_b64_tr_b16 a[196:197],v2 offset:8192
v_cvt_pk_bf16_f32 v166,v172,v173
v_mfma_f32_16x16x32_bf16 a[100:103],a[204:207],v[244:247],a[100:103]
buffer_store_dwordx4 v[72:75],v6,s[36:39],s84 offen offset:2048 sc1
v_mfma_f32_16x16x32_bf16 a[100:103],a[200:203],v[228:231],a[100:103]
ds_read_b64_tr_b16 a[198:199],v2 offset:12288
ds_read_b64_tr_b16 a[204:205],v3 offset:8192
v_mfma_f32_16x16x32_bf16 a[104:107],a[208:211],v[228:231],a[104:107]
ds_read_b64_tr_b16 a[206:207],v3 offset:12288
v_mfma_f32_16x16x32_bf16 a[104:107],a[212:215],v[244:247],a[104:107]
ds_read_b64_tr_b16 a[200:201],v3
v_mfma_f32_16x16x32_bf16 a[108:111],a[220:223],v[244:247],a[108:111]
buffer_load_dwordx4 v8,s[72:75],s83 offen offset:1024 lds
v_mfma_f32_16x16x32_bf16 a[108:111],a[216:219],v[228:231],a[108:111]
ds_read_b64_tr_b16 a[202:203],v3 offset:4096
ds_read_b64_tr_b16 a[208:209],v2 offset:256
v_mfma_f32_16x16x32_bf16 a[112:115],a[224:227],v[228:231],a[112:115]
ds_read_b64_tr_b16 a[210:211],v2 offset:4352
v_mfma_f32_16x16x32_bf16 a[112:115],a[228:231],v[244:247],a[112:115]
ds_read_b64_tr_b16 a[212:213],v2 offset:8448
v_mfma_f32_16x16x32_bf16 a[116:119],a[236:239],v[244:247],a[116:119]
ds_read_b64_tr_b16 a[214:215],v2 offset:12544
v_mfma_f32_16x16x32_bf16 a[116:119],a[232:235],v[228:231],a[116:119]
ds_read_b64_tr_b16 a[220:221],v3 offset:8448
ds_read_b64_tr_b16 a[222:223],v3 offset:12544
v_mfma_f32_16x16x32_bf16 a[120:123],a[240:243],v[228:231],a[120:123]
ds_read_b64_tr_b16 a[216:217],v3 offset:256
v_mfma_f32_16x16x32_bf16 a[120:123],a[244:247],v[244:247],a[120:123]
ds_read_b64_tr_b16 a[218:219],v3 offset:4352
v_mfma_f32_16x16x32_bf16 a[124:127],a[252:255],v[244:247],a[124:127]
ds_read_b64_tr_b16 a[224:225],v2 offset:512
v_mfma_f32_16x16x32_bf16 a[124:127],a[248:251],v[228:231],a[124:127]
ds_read_b64_tr_b16 a[226:227],v2 offset:4608
ds_read_b64_tr_b16 a[228:229],v2 offset:8704
v_mfma_f32_16x16x32_bf16 v[60:63],v[28:31],v[228:231],v[60:63]
ds_read_b64_tr_b16 a[230:231],v2 offset:12800
v_mfma_f32_16x16x32_bf16 v[60:63],v[28:31],v[244:247],v[60:63]
ds_read_b64_tr_b16 a[236:237],v3 offset:8704
v_mfma_f32_16x16x32_bf16 v[196:199],a[128:131],v[144:147],v[40:43]
buffer_load_dwordx4 v9,s[72:75],s83 offen offset:2048 lds
v_mfma_f32_16x16x32_bf16 v[196:199],a[132:135],v[176:179],v[196:199]
ds_read_b64_tr_b16 a[238:239],v3 offset:12800
ds_read_b64_tr_b16 a[232:233],v3 offset:512
v_mfma_f32_16x16x32_bf16 v[196:199],a[136:139],v[208:211],v[196:199]
ds_read_b64_tr_b16 a[234:235],v3 offset:4608
v_mfma_f32_16x16x32_bf16 v[196:199],a[140:143],v[240:243],v[196:199]
ds_read_b64_tr_b16 a[240:241],v2 offset:768
v_mfma_f32_16x16x32_bf16 v[204:207],a[156:159],v[240:243],v[40:43]
ds_read_b64_tr_b16 a[242:243],v2 offset:4864
v_mfma_f32_16x16x32_bf16 v[204:207],a[152:155],v[208:211],v[204:207]
ds_read_b64_tr_b16 a[244:245],v2 offset:8960
ds_read_b64_tr_b16 a[246:247],v2 offset:13056
v_mfma_f32_16x16x32_bf16 v[204:207],a[148:151],v[176:179],v[204:207]
buffer_store_dwordx4 v[76:79],v6,s[36:39],s84 offen offset:3072 sc1
v_mfma_f32_16x16x32_bf16 v[204:207],a[144:147],v[144:147],v[204:207]
s_add_u32 s84,s55,65536
ds_read_b64_tr_b16 a[252:253],v3 offset:8960
ds_read_b64_tr_b16 a[254:255],v3 offset:13056
v_mfma_f32_16x16x32_bf16 v[212:215],a[160:163],v[144:147],v[40:43]
s_max_i32 s84,s84,s18
ds_read_b64_tr_b16 a[248:249],v3 offset:768
v_add_u32 v2,s48,v2
v_mfma_f32_16x16x32_bf16 v[212:215],a[164:167],v[176:179],v[212:215]
ds_read_b64_tr_b16 a[250:251],v3 offset:4864
v_add_u32 v3,s48,v3
v_mfma_f32_16x16x32_bf16 v[212:215],a[168:171],v[208:211],v[212:215]
s_sub_u32 s48,32768,s62
v_exp_f32 v174,v174
v_mfma_f32_16x16x32_bf16 v[212:215],a[172:175],v[240:243],v[212:215]
s_add_u32 s50,s50,s48
v_exp_f32 v175,v175
)A"
R"A(v_mfma_f32_16x16x32_bf16 v[220:223],a[188:191],v[240:243],v[40:43]
v_exp_f32 v180,v180
v_mfma_f32_16x16x32_bf16 v[220:223],a[184:187],v[208:211],v[220:223]
buffer_load_dwordx4 v10,s[72:75],s83 offen offset:3072 lds
v_mfma_f32_16x16x32_bf16 v[220:223],a[180:183],v[176:179],v[220:223]
v_exp_f32 v181,v181
v_mfma_f32_16x16x32_bf16 v[220:223],a[176:179],v[144:147],v[220:223]
s_waitcnt lgkmcnt(2)
v_exp_f32 v182,v182
v_mfma_f32_16x16x32_bf16 a[0:3],a[192:195],v[132:135],a[0:3]
v_exp_f32 v183,v183
v_mfma_f32_16x16x32_bf16 a[0:3],a[196:199],v[148:151],a[0:3]
v_cvt_pk_bf16_f32 v167,v174,v175
v_cvt_pk_bf16_f32 v180,v180,v181
v_mfma_f32_16x16x32_bf16 a[4:7],a[204:207],v[148:151],a[4:7]
v_exp_f32 v188,v188
v_mfma_f32_16x16x32_bf16 a[4:7],a[200:203],v[132:135],a[4:7]
v_exp_f32 v189,v189
v_mfma_f32_16x16x32_bf16 a[8:11],a[208:211],v[132:135],a[8:11]
v_exp_f32 v190,v190
v_mfma_f32_16x16x32_bf16 a[8:11],a[212:215],v[148:151],a[8:11]
v_exp_f32 v191,v191
v_mfma_f32_16x16x32_bf16 a[12:15],a[220:223],v[148:151],a[12:15]
s_add_u32 s80,s71,0
s_and_b32 s80,s80,3
s_lshl_b32 s80,s80,1
v_mfma_f32_16x16x32_bf16 a[12:15],a[216:219],v[132:135],a[12:15]
s_add_u32 m0,s82,9216
s_or_b32 s80,s80,1
s_mul_i32 s80,s80,163840
v_mfma_f32_16x16x32_bf16 a[16:19],a[224:227],v[132:135],a[16:19]
s_add_u32 s80,s80,s81
v_cvt_pk_bf16_f32 v181,v182,v183
v_cvt_pk_bf16_f32 v182,v188,v189
v_mfma_f32_16x16x32_bf16 a[16:19],a[228:231],v[148:151],a[16:19]
s_cmp_eq_u32 s50,131072
s_cselect_b32 s62,163840,0
v_cvt_pk_bf16_f32 v183,v190,v191
v_mfma_f32_16x16x32_bf16 a[20:23],a[236:239],v[148:151],a[20:23]
buffer_load_dwordx4 v11,s[72:75],s83 offen lds
v_mfma_f32_16x16x32_bf16 a[20:23],a[232:235],v[132:135],a[20:23]
s_add_u32 s83,s80,20480
v_exp_f32 v196,v196
v_mfma_f32_16x16x32_bf16 a[24:27],a[240:243],v[132:135],a[24:27]
v_exp_f32 v197,v197
v_mfma_f32_16x16x32_bf16 a[24:27],a[244:247],v[148:151],a[24:27]
v_exp_f32 v198,v198
v_mfma_f32_16x16x32_bf16 a[28:31],a[252:255],v[148:151],a[28:31]
s_waitcnt lgkmcnt(0)
buffer_store_dwordx4 v[80:83],v6,s[36:39],s85 offen sc1
v_mfma_f32_16x16x32_bf16 a[28:31],a[248:251],v[132:135],a[28:31]
v_exp_f32 v199,v199
v_mfma_f32_16x16x32_bf16 v[48:51],v[28:31],v[132:135],v[48:51]
v_cvt_pk_bf16_f32 v196,v196,v197
v_cvt_pk_bf16_f32 v197,v198,v199
v_mfma_f32_16x16x32_bf16 v[48:51],v[28:31],v[148:151],v[48:51]
v_exp_f32 v204,v204
v_mfma_f32_16x16x32_bf16 v[228:231],a[128:131],v[152:155],v[44:47]
s_waitcnt vmcnt(23)
v_exp_f32 v205,v205
v_mfma_f32_16x16x32_bf16 v[228:231],a[132:135],v[184:187],v[228:231]
s_barrier
ds_read_b128 a[128:131],v1
v_cvt_pk_bf16_f32 v198,v204,v205
v_mfma_f32_16x16x32_bf16 v[228:231],a[136:139],v[216:219],v[228:231]
v_exp_f32 v206,v206
v_mfma_f32_16x16x32_bf16 v[228:231],a[140:143],v[248:251],v[228:231]
ds_read_b128 a[132:135],v1 offset:256
v_mfma_f32_16x16x32_bf16 v[236:239],a[156:159],v[248:251],v[44:47]
v_exp_f32 v207,v207
v_mfma_f32_16x16x32_bf16 v[236:239],a[152:155],v[216:219],v[236:239]
ds_read_b128 a[136:139],v1 offset:512
v_cvt_pk_bf16_f32 v199,v206,v207
v_mfma_f32_16x16x32_bf16 v[236:239],a[148:151],v[184:187],v[236:239]
v_exp_f32 v212,v212
v_mfma_f32_16x16x32_bf16 v[236:239],a[144:147],v[152:155],v[236:239]
ds_read_b128 a[140:143],v1 offset:768
v_mfma_f32_16x16x32_bf16 v[244:247],a[160:163],v[152:155],v[44:47]
s_add_u32 m0,s82,40960
v_exp_f32 v213,v213
v_mfma_f32_16x16x32_bf16 v[244:247],a[164:167],v[184:187],v[244:247]
ds_read_b128 a[156:159],v1 offset:4864
v_cvt_pk_bf16_f32 v212,v212,v213
v_mfma_f32_16x16x32_bf16 v[244:247],a[168:171],v[216:219],v[244:247]
v_exp_f32 v214,v214
v_mfma_f32_16x16x32_bf16 v[244:247],a[172:175],v[248:251],v[244:247]
buffer_load_dwordx4 v7,s[72:75],s80 offen lds
v_mfma_f32_16x16x32_bf16 v[252:255],a[188:191],v[248:251],v[44:47]
ds_read_b128 a[152:155],v1 offset:4608
v_mfma_f32_16x16x32_bf16 v[252:255],a[184:187],v[216:219],v[252:255]
v_exp_f32 v215,v215
v_mfma_f32_16x16x32_bf16 v[252:255],a[180:183],v[184:187],v[252:255]
ds_read_b128 a[148:151],v1 offset:4352
v_cvt_pk_bf16_f32 v213,v214,v215
v_mfma_f32_16x16x32_bf16 v[252:255],a[176:179],v[152:155],v[252:255]
buffer_store_dwordx4 v[84:87],v6,s[36:39],s85 offen offset:1024 sc1
v_mfma_f32_16x16x32_bf16 a[32:35],a[192:195],v[164:167],a[32:35]
ds_read_b128 a[144:147],v1 offset:4096
v_mfma_f32_16x16x32_bf16 a[32:35],a[196:199],v[180:183],a[32:35]
v_exp_f32 v220,v220
v_mfma_f32_16x16x32_bf16 a[36:39],a[204:207],v[180:183],a[36:39]
ds_read_b128 a[160:163],v1 offset:8192
v_mfma_f32_16x16x32_bf16 a[36:39],a[200:203],v[164:167],a[36:39]
v_exp_f32 v221,v221
v_mfma_f32_16x16x32_bf16 a[40:43],a[208:211],v[164:167],a[40:43]
ds_read_b128 a[164:167],v1 offset:8448
v_cvt_pk_bf16_f32 v214,v220,v221
v_mfma_f32_16x16x32_bf16 a[40:43],a[212:215],v[180:183],a[40:43]
v_exp_f32 v222,v222
v_mfma_f32_16x16x32_bf16 a[44:47],a[220:223],v[180:183],a[44:47]
ds_read_b128 a[168:171],v1 offset:8704
v_mfma_f32_16x16x32_bf16 a[44:47],a[216:219],v[164:167],a[44:47]
v_exp_f32 v223,v223
v_mfma_f32_16x16x32_bf16 a[48:51],a[224:227],v[164:167],a[48:51]
buffer_load_dwordx4 v8,s[72:75],s80 offen offset:1024 lds
v_mfma_f32_16x16x32_bf16 a[48:51],a[228:231],v[180:183],a[48:51]
ds_read_b128 a[172:175],v1 offset:8960
v_cvt_pk_bf16_f32 v215,v222,v223
v_mfma_f32_16x16x32_bf16 a[52:55],a[236:239],v[180:183],a[52:55]
v_exp_f32 v228,v228
v_mfma_f32_16x16x32_bf16 a[52:55],a[232:235],v[164:167],a[52:55]
ds_read_b128 a[188:191],v1 offset:13056
v_mfma_f32_16x16x32_bf16 a[56:59],a[240:243],v[164:167],a[56:59]
v_exp_f32 v229,v229
v_mfma_f32_16x16x32_bf16 a[56:59],a[244:247],v[180:183],a[56:59]
ds_read_b128 a[184:187],v1 offset:12800
v_cvt_pk_bf16_f32 v228,v228,v229
v_mfma_f32_16x16x32_bf16 a[60:63],a[252:255],v[180:183],a[60:63]
v_exp_f32 v230,v230
v_mfma_f32_16x16x32_bf16 a[60:63],a[248:251],v[164:167],a[60:63]
ds_read_b128 a[180:183],v1 offset:12544
v_mfma_f32_16x16x32_bf16 v[52:55],v[28:31],v[164:167],v[52:55]
v_exp_f32 v231,v231
v_mfma_f32_16x16x32_bf16 v[52:55],v[28:31],v[180:183],v[52:55]
s_waitcnt lgkmcnt(3)
ds_read_b128 a[176:179],v1 offset:12288
v_cvt_pk_bf16_f32 v229,v230,v231
v_mfma_f32_16x16x32_bf16 v[132:135],a[128:131],v[128:131],v[32:35]
v_exp_f32 v236,v236
v_mfma_f32_16x16x32_bf16 v[132:135],a[132:135],v[160:163],v[132:135]
v_exp_f32 v237,v237
v_mfma_f32_16x16x32_bf16 v[132:135],a[136:139],v[192:195],v[132:135]
v_exp_f32 v238,v238
v_mfma_f32_16x16x32_bf16 v[132:135],a[140:143],v[224:227],v[132:135]
buffer_store_dwordx4 v[88:91],v6,s[36:39],s85 offen offset:2048 sc1
v_mfma_f32_16x16x32_bf16 v[140:143],a[156:159],v[224:227],v[32:35]
v_exp_f32 v239,v239
v_mfma_f32_16x16x32_bf16 v[140:143],a[152:155],v[192:195],v[140:143]
v_exp_f32 v244,v244
v_mfma_f32_16x16x32_bf16 v[140:143],a[148:151],v[160:163],v[140:143]
v_exp_f32 v245,v245
v_mfma_f32_16x16x32_bf16 v[140:143],a[144:147],v[128:131],v[140:143]
buffer_load_dwordx4 v9,s[72:75],s80 offen offset:2048 lds
v_mfma_f32_16x16x32_bf16 v[148:151],a[160:163],v[128:131],v[32:35]
v_exp_f32 v246,v246
v_mfma_f32_16x16x32_bf16 v[148:151],a[164:167],v[160:163],v[148:151]
v_exp_f32 v247,v247
v_mfma_f32_16x16x32_bf16 v[148:151],a[168:171],v[192:195],v[148:151]
v_cvt_pk_bf16_f32 v230,v236,v237
v_cvt_pk_bf16_f32 v231,v238,v239
v_mfma_f32_16x16x32_bf16 v[148:151],a[172:175],v[224:227],v[148:151]
s_waitcnt lgkmcnt(0)
v_cvt_pk_bf16_f32 v244,v244,v245
v_cvt_pk_bf16_f32 v245,v246,v247
v_mfma_f32_16x16x32_bf16 v[156:159],a[188:191],v[224:227],v[32:35]
v_exp_f32 v252,v252
v_mfma_f32_16x16x32_bf16 v[156:159],a[184:187],v[192:195],v[156:159]
v_exp_f32 v253,v253
v_mfma_f32_16x16x32_bf16 v[156:159],a[180:183],v[160:163],v[156:159]
v_exp_f32 v254,v254
v_mfma_f32_16x16x32_bf16 v[156:159],a[176:179],v[128:131],v[156:159]
v_exp_f32 v255,v255
v_mfma_f32_16x16x32_bf16 a[64:67],a[192:195],v[196:199],a[64:67]
buffer_load_dwordx4 v10,s[72:75],s80 offen offset:3072 lds
v_mfma_f32_16x16x32_bf16 a[64:67],a[196:199],v[212:215],a[64:67]
v_cvt_pk_bf16_f32 v246,v252,v253
v_cvt_pk_bf16_f32 v247,v254,v255
v_mfma_f32_16x16x32_bf16 a[68:71],a[204:207],v[212:215],a[68:71]
v_exp_f32 v132,v132
v_mfma_f32_16x16x32_bf16 a[68:71],a[200:203],v[196:199],a[68:71]
v_exp_f32 v133,v133
v_mfma_f32_16x16x32_bf16 a[72:75],a[208:211],v[196:199],a[72:75]
v_exp_f32 v134,v134
v_mfma_f32_16x16x32_bf16 a[72:75],a[212:215],v[212:215],a[72:75]
v_exp_f32 v135,v135
v_mfma_f32_16x16x32_bf16 a[76:79],a[220:223],v[212:215],a[76:79]
v_cvt_pk_bf16_f32 v132,v132,v133
v_cvt_pk_bf16_f32 v133,v134,v135
v_mfma_f32_16x16x32_bf16 a[76:79],a[216:219],v[196:199],a[76:79]
v_exp_f32 v140,v140
v_mfma_f32_16x16x32_bf16 a[80:83],a[224:227],v[196:199],a[80:83]
v_exp_f32 v141,v141
v_mfma_f32_16x16x32_bf16 a[80:83],a[228:231],v[212:215],a[80:83]
buffer_store_dwordx4 v[92:95],v6,s[36:39],s85 offen offset:3072 sc1
v_mfma_f32_16x16x32_bf16 a[84:87],a[236:239],v[212:215],a[84:87]
s_add_u32 m0,s82,45056
v_exp_f32 v142,v142
v_mfma_f32_16x16x32_bf16 a[84:87],a[232:235],v[196:199],a[84:87]
s_add_u32 s85,s55,69632
v_exp_f32 v143,v143
v_mfma_f32_16x16x32_bf16 a[88:91],a[240:243],v[196:199],a[88:91]
s_max_i32 s85,s85,s19
v_exp_f32 v148,v148
v_mfma_f32_16x16x32_bf16 a[88:91],a[244:247],v[212:215],a[88:91]
buffer_load_dwordx4 v11,s[72:75],s80 offen lds
v_mfma_f32_16x16x32_bf16 a[92:95],a[252:255],v[212:215],a[92:95]
v_exp_f32 v149,v149
v_mfma_f32_16x16x32_bf16 a[92:95],a[248:251],v[196:199],a[92:95]
v_exp_f32 v150,v150
v_mfma_f32_16x16x32_bf16 v[56:59],v[28:31],v[196:199],v[56:59]
v_exp_f32 v151,v151
v_mfma_f32_16x16x32_bf16 v[56:59],v[28:31],v[212:215],v[56:59]
v_cvt_pk_bf16_f32 v134,v140,v141
v_cvt_pk_bf16_f32 v135,v142,v143
v_mfma_f32_16x16x32_bf16 v[164:167],a[128:131],v[136:139],v[36:39]
v_cvt_pk_bf16_f32 v148,v148,v149
v_cvt_pk_bf16_f32 v149,v150,v151
v_mfma_f32_16x16x32_bf16 v[164:167],a[132:135],v[168:171],v[164:167]
v_exp_f32 v156,v156
v_mfma_f32_16x16x32_bf16 v[164:167],a[136:139],v[200:203],v[164:167]
v_exp_f32 v157,v157
v_mfma_f32_16x16x32_bf16 v[164:167],a[140:143],v[232:235],v[164:167]
v_exp_f32 v158,v158
v_mfma_f32_16x16x32_bf16 v[172:175],a[156:159],v[232:235],v[36:39]
v_exp_f32 v159,v159
v_mfma_f32_16x16x32_bf16 v[172:175],a[152:155],v[200:203],v[172:175]
s_add_u32 m0,s82,46080
v_cvt_pk_bf16_f32 v150,v156,v157
v_cvt_pk_bf16_f32 v151,v158,v159
v_mfma_f32_16x16x32_bf16 v[172:175],a[148:151],v[168:171],v[172:175]
v_add_u32 v1,s48,v1
v_mfma_f32_16x16x32_bf16 v[172:175],a[144:147],v[136:139],v[172:175]
v_mfma_f32_16x16x32_bf16 v[180:183],a[160:163],v[136:139],v[36:39]
buffer_load_dwordx4 v7,s[72:75],s83 offen lds
v_mfma_f32_16x16x32_bf16 v[180:183],a[164:167],v[168:171],v[180:183]
v_exp_f32 v164,v164
v_mfma_f32_16x16x32_bf16 v[180:183],a[168:171],v[200:203],v[180:183]
v_exp_f32 v165,v165
v_mfma_f32_16x16x32_bf16 v[180:183],a[172:175],v[232:235],v[180:183]
v_exp_f32 v166,v166
v_mfma_f32_16x16x32_bf16 v[188:191],a[188:191],v[232:235],v[36:39]
v_exp_f32 v167,v167
v_mfma_f32_16x16x32_bf16 v[188:191],a[184:187],v[200:203],v[188:191]
buffer_store_dwordx4 v[96:99],v6,s[36:39],s86 offen sc1
v_mfma_f32_16x16x32_bf16 v[188:191],a[180:183],v[168:171],v[188:191]
v_cvt_pk_bf16_f32 v164,v164,v165
v_cvt_pk_bf16_f32 v165,v166,v167
v_mfma_f32_16x16x32_bf16 v[188:191],a[176:179],v[136:139],v[188:191]
v_exp_f32 v172,v172
v_mfma_f32_16x16x32_bf16 a[96:99],a[192:195],v[228:231],a[96:99]
ds_read_b64_tr_b16 a[192:193],v2
ds_read_b64_tr_b16 a[194:195],v2 offset:4096
v_mfma_f32_16x16x32_bf16 a[96:99],a[196:199],v[244:247],a[96:99]
ds_read_b64_tr_b16 a[196:197],v2 offset:8192
v_mfma_f32_16x16x32_bf16 a[100:103],a[204:207],v[244:247],a[100:103]
ds_read_b64_tr_b16 a[198:199],v2 offset:12288
v_mfma_f32_16x16x32_bf16 a[100:103],a[200:203],v[228:231],a[100:103]
ds_read_b64_tr_b16 a[204:205],v3 offset:8192
v_mfma_f32_16x16x32_bf16 a[104:107],a[208:211],v[228:231],a[104:107]
ds_read_b64_tr_b16 a[206:207],v3 offset:12288
ds_read_b64_tr_b16 a[200:201],v3
v_mfma_f32_16x16x32_bf16 a[104:107],a[212:215],v[244:247],a[104:107]
buffer_load_dwordx4 v8,s[72:75],s83 offen offset:1024 lds
v_mfma_f32_16x16x32_bf16 a[108:111],a[220:223],v[244:247],a[108:111]
ds_read_b64_tr_b16 a[202:203],v3 offset:4096
ds_read_b64_tr_b16 a[208:209],v2 offset:256
)A"
R"A(v_mfma_f32_16x16x32_bf16 a[108:111],a[216:219],v[228:231],a[108:111]
ds_read_b64_tr_b16 a[210:211],v2 offset:4352
v_mfma_f32_16x16x32_bf16 a[112:115],a[224:227],v[228:231],a[112:115]
ds_read_b64_tr_b16 a[212:213],v2 offset:8448
v_mfma_f32_16x16x32_bf16 a[112:115],a[228:231],v[244:247],a[112:115]
ds_read_b64_tr_b16 a[214:215],v2 offset:12544
v_mfma_f32_16x16x32_bf16 a[116:119],a[236:239],v[244:247],a[116:119]
ds_read_b64_tr_b16 a[220:221],v3 offset:8448
ds_read_b64_tr_b16 a[222:223],v3 offset:12544
v_mfma_f32_16x16x32_bf16 a[116:119],a[232:235],v[228:231],a[116:119]
ds_read_b64_tr_b16 a[216:217],v3 offset:256
v_mfma_f32_16x16x32_bf16 a[120:123],a[240:243],v[228:231],a[120:123]
ds_read_b64_tr_b16 a[218:219],v3 offset:4352
v_mfma_f32_16x16x32_bf16 a[120:123],a[244:247],v[244:247],a[120:123]
ds_read_b64_tr_b16 a[224:225],v2 offset:512
v_mfma_f32_16x16x32_bf16 a[124:127],a[252:255],v[244:247],a[124:127]
ds_read_b64_tr_b16 a[226:227],v2 offset:4608
ds_read_b64_tr_b16 a[228:229],v2 offset:8704
v_mfma_f32_16x16x32_bf16 a[124:127],a[248:251],v[228:231],a[124:127]
ds_read_b64_tr_b16 a[230:231],v2 offset:12800
v_mfma_f32_16x16x32_bf16 v[60:63],v[28:31],v[228:231],v[60:63]
ds_read_b64_tr_b16 a[236:237],v3 offset:8704
v_mfma_f32_16x16x32_bf16 v[60:63],v[28:31],v[244:247],v[60:63]
ds_read_b64_tr_b16 a[238:239],v3 offset:12800
v_mfma_f32_16x16x32_bf16 v[196:199],a[128:131],v[144:147],v[40:43]
buffer_load_dwordx4 v9,s[72:75],s83 offen offset:2048 lds
v_mfma_f32_16x16x32_bf16 v[196:199],a[132:135],v[176:179],v[196:199]
ds_read_b64_tr_b16 a[232:233],v3 offset:512
ds_read_b64_tr_b16 a[234:235],v3 offset:4608
v_mfma_f32_16x16x32_bf16 v[196:199],a[136:139],v[208:211],v[196:199]
ds_read_b64_tr_b16 a[240:241],v2 offset:768
v_mfma_f32_16x16x32_bf16 v[196:199],a[140:143],v[240:243],v[196:199]
ds_read_b64_tr_b16 a[242:243],v2 offset:4864
v_mfma_f32_16x16x32_bf16 v[204:207],a[156:159],v[240:243],v[40:43]
buffer_store_dwordx4 v[100:103],v6,s[36:39],s86 offen offset:1024 sc1
v_mfma_f32_16x16x32_bf16 v[204:207],a[152:155],v[208:211],v[204:207]
ds_read_b64_tr_b16 a[244:245],v2 offset:8960
ds_read_b64_tr_b16 a[246:247],v2 offset:13056
v_mfma_f32_16x16x32_bf16 v[204:207],a[148:151],v[176:179],v[204:207]
ds_read_b64_tr_b16 a[252:253],v3 offset:8960
v_add_u32 v2,s48,v2
v_mfma_f32_16x16x32_bf16 v[204:207],a[144:147],v[144:147],v[204:207]
ds_read_b64_tr_b16 a[254:255],v3 offset:13056
v_mfma_f32_16x16x32_bf16 v[212:215],a[160:163],v[144:147],v[40:43]
ds_read_b64_tr_b16 a[248:249],v3 offset:768
v_mfma_f32_16x16x32_bf16 v[212:215],a[164:167],v[176:179],v[212:215]
ds_read_b64_tr_b16 a[250:251],v3 offset:4864
v_add_u32 v3,s48,v3
v_mfma_f32_16x16x32_bf16 v[212:215],a[168:171],v[208:211],v[212:215]
s_sub_u32 s48,32768,s62
v_exp_f32 v173,v173
v_mfma_f32_16x16x32_bf16 v[212:215],a[172:175],v[240:243],v[212:215]
s_add_u32 s50,s50,s48
v_exp_f32 v174,v174
v_mfma_f32_16x16x32_bf16 v[220:223],a[188:191],v[240:243],v[40:43]
v_exp_f32 v175,v175
v_mfma_f32_16x16x32_bf16 v[220:223],a[184:187],v[208:211],v[220:223]
buffer_load_dwordx4 v10,s[72:75],s83 offen offset:3072 lds
v_mfma_f32_16x16x32_bf16 v[220:223],a[180:183],v[176:179],v[220:223]
v_exp_f32 v180,v180
v_mfma_f32_16x16x32_bf16 v[220:223],a[176:179],v[144:147],v[220:223]
s_waitcnt lgkmcnt(2)
v_exp_f32 v181,v181
v_mfma_f32_16x16x32_bf16 a[0:3],a[192:195],v[132:135],a[0:3]
v_exp_f32 v182,v182
v_mfma_f32_16x16x32_bf16 a[0:3],a[196:199],v[148:151],a[0:3]
v_exp_f32 v183,v183
v_mfma_f32_16x16x32_bf16 a[4:7],a[204:207],v[148:151],a[4:7]
v_cvt_pk_bf16_f32 v166,v172,v173
v_cvt_pk_bf16_f32 v167,v174,v175
v_mfma_f32_16x16x32_bf16 a[4:7],a[200:203],v[132:135],a[4:7]
v_cvt_pk_bf16_f32 v180,v180,v181
v_cvt_pk_bf16_f32 v181,v182,v183
v_mfma_f32_16x16x32_bf16 a[8:11],a[208:211],v[132:135],a[8:11]
v_exp_f32 v188,v188
v_mfma_f32_16x16x32_bf16 a[8:11],a[212:215],v[148:151],a[8:11]
v_exp_f32 v189,v189
v_mfma_f32_16x16x32_bf16 a[12:15],a[220:223],v[148:151],a[12:15]
s_cmp_ge_u32 0,s71
s_cselect_b32 s80,2,0
s_mul_i32 s80,s80,163840
v_mfma_f32_16x16x32_bf16 a[12:15],a[216:219],v[132:135],a[12:15]
s_add_u32 m0,s82,50176
buffer_store_dwordx4 v[104:107],v6,s[36:39],s86 offen offset:2048 sc1
v_mfma_f32_16x16x32_bf16 a[16:19],a[224:227],v[132:135],a[16:19]
s_add_u32 s80,s80,s81
v_exp_f32 v190,v190
v_mfma_f32_16x16x32_bf16 a[16:19],a[228:231],v[148:151],a[16:19]
v_exp_f32 v191,v191
v_mfma_f32_16x16x32_bf16 a[20:23],a[236:239],v[148:151],a[20:23]
v_cvt_pk_bf16_f32 v182,v188,v189
v_cvt_pk_bf16_f32 v183,v190,v191
v_mfma_f32_16x16x32_bf16 a[20:23],a[232:235],v[132:135],a[20:23]
buffer_load_dwordx4 v11,s[72:75],s83 offen lds
v_mfma_f32_16x16x32_bf16 a[24:27],a[240:243],v[132:135],a[24:27]
s_add_u32 s83,s80,20480
v_exp_f32 v196,v196
v_mfma_f32_16x16x32_bf16 a[24:27],a[244:247],v[148:151],a[24:27]
v_exp_f32 v197,v197
v_mfma_f32_16x16x32_bf16 a[28:31],a[252:255],v[148:151],a[28:31]
s_waitcnt lgkmcnt(0)
v_exp_f32 v198,v198
v_mfma_f32_16x16x32_bf16 a[28:31],a[248:251],v[132:135],a[28:31]
v_exp_f32 v199,v199
v_mfma_f32_16x16x32_bf16 v[48:51],v[28:31],v[132:135],v[48:51]
v_cvt_pk_bf16_f32 v196,v196,v197
v_cvt_pk_bf16_f32 v197,v198,v199
v_mfma_f32_16x16x32_bf16 v[48:51],v[28:31],v[148:151],v[48:51]
v_exp_f32 v204,v204
v_mfma_f32_16x16x32_bf16 v[228:231],a[128:131],v[152:155],v[44:47]
s_waitcnt vmcnt(31)
v_exp_f32 v205,v205
v_mfma_f32_16x16x32_bf16 v[228:231],a[132:135],v[184:187],v[228:231]
s_barrier
ds_read_b128 a[128:131],v1
v_cvt_pk_bf16_f32 v198,v204,v205
v_mfma_f32_16x16x32_bf16 v[228:231],a[136:139],v[216:219],v[228:231]
v_exp_f32 v206,v206
v_mfma_f32_16x16x32_bf16 v[228:231],a[140:143],v[248:251],v[228:231]
ds_read_b128 a[132:135],v1 offset:256
v_mfma_f32_16x16x32_bf16 v[236:239],a[156:159],v[248:251],v[44:47]
v_exp_f32 v207,v207
v_mfma_f32_16x16x32_bf16 v[236:239],a[152:155],v[216:219],v[236:239]
ds_read_b128 a[136:139],v1 offset:512
v_cvt_pk_bf16_f32 v199,v206,v207
v_mfma_f32_16x16x32_bf16 v[236:239],a[148:151],v[184:187],v[236:239]
v_exp_f32 v212,v212
v_mfma_f32_16x16x32_bf16 v[236:239],a[144:147],v[152:155],v[236:239]
ds_read_b128 a[140:143],v1 offset:768
v_mfma_f32_16x16x32_bf16 v[244:247],a[160:163],v[152:155],v[44:47]
s_add_u32 m0,s82,81920
v_exp_f32 v213,v213
v_mfma_f32_16x16x32_bf16 v[244:247],a[164:167],v[184:187],v[244:247]
ds_read_b128 a[156:159],v1 offset:4864
v_cvt_pk_bf16_f32 v212,v212,v213
v_mfma_f32_16x16x32_bf16 v[244:247],a[168:171],v[216:219],v[244:247]
buffer_store_dwordx4 v[108:111],v6,s[36:39],s86 offen offset:3072 sc1
v_mfma_f32_16x16x32_bf16 v[244:247],a[172:175],v[248:251],v[244:247]
s_add_u32 s86,s55,73728
s_max_i32 s86,s86,s100
ds_read_b128 a[152:155],v1 offset:4608
v_mfma_f32_16x16x32_bf16 v[252:255],a[188:191],v[248:251],v[44:47]
v_exp_f32 v214,v214
v_mfma_f32_16x16x32_bf16 v[252:255],a[184:187],v[216:219],v[252:255]
ds_read_b128 a[148:151],v1 offset:4352
v_mfma_f32_16x16x32_bf16 v[252:255],a[180:183],v[184:187],v[252:255]
buffer_load_dwordx4 v7,s[72:75],s80 offen lds
v_mfma_f32_16x16x32_bf16 v[252:255],a[176:179],v[152:155],v[252:255]
ds_read_b128 a[144:147],v1 offset:4096
v_mfma_f32_16x16x32_bf16 a[32:35],a[192:195],v[164:167],a[32:35]
v_exp_f32 v215,v215
v_mfma_f32_16x16x32_bf16 a[32:35],a[196:199],v[180:183],a[32:35]
ds_read_b128 a[160:163],v1 offset:8192
v_cvt_pk_bf16_f32 v213,v214,v215
v_mfma_f32_16x16x32_bf16 a[36:39],a[204:207],v[180:183],a[36:39]
v_exp_f32 v220,v220
v_mfma_f32_16x16x32_bf16 a[36:39],a[200:203],v[164:167],a[36:39]
ds_read_b128 a[164:167],v1 offset:8448
v_mfma_f32_16x16x32_bf16 a[40:43],a[208:211],v[164:167],a[40:43]
v_exp_f32 v221,v221
v_mfma_f32_16x16x32_bf16 a[40:43],a[212:215],v[180:183],a[40:43]
ds_read_b128 a[168:171],v1 offset:8704
v_cvt_pk_bf16_f32 v214,v220,v221
v_mfma_f32_16x16x32_bf16 a[44:47],a[220:223],v[180:183],a[44:47]
v_exp_f32 v222,v222
v_mfma_f32_16x16x32_bf16 a[44:47],a[216:219],v[164:167],a[44:47]
ds_read_b128 a[172:175],v1 offset:8960
v_mfma_f32_16x16x32_bf16 a[48:51],a[224:227],v[164:167],a[48:51]
buffer_load_dwordx4 v8,s[72:75],s80 offen offset:1024 lds
v_mfma_f32_16x16x32_bf16 a[48:51],a[228:231],v[180:183],a[48:51]
ds_read_b128 a[188:191],v1 offset:13056
v_mfma_f32_16x16x32_bf16 a[52:55],a[236:239],v[180:183],a[52:55]
v_exp_f32 v223,v223
v_mfma_f32_16x16x32_bf16 a[52:55],a[232:235],v[164:167],a[52:55]
ds_read_b128 a[184:187],v1 offset:12800
v_cvt_pk_bf16_f32 v215,v222,v223
v_mfma_f32_16x16x32_bf16 a[56:59],a[240:243],v[164:167],a[56:59]
v_exp_f32 v228,v228
v_mfma_f32_16x16x32_bf16 a[56:59],a[244:247],v[180:183],a[56:59]
ds_read_b128 a[180:183],v1 offset:12544
v_mfma_f32_16x16x32_bf16 a[60:63],a[252:255],v[180:183],a[60:63]
v_exp_f32 v229,v229
v_mfma_f32_16x16x32_bf16 a[60:63],a[248:251],v[164:167],a[60:63]
ds_read_b128 a[176:179],v1 offset:12288
v_cvt_pk_bf16_f32 v228,v228,v229
v_mfma_f32_16x16x32_bf16 v[52:55],v[28:31],v[164:167],v[52:55]
buffer_store_dwordx4 v[112:115],v6,s[36:39],s87 offen sc1
v_mfma_f32_16x16x32_bf16 v[52:55],v[28:31],v[180:183],v[52:55]
s_waitcnt lgkmcnt(3)
v_exp_f32 v230,v230
v_mfma_f32_16x16x32_bf16 v[132:135],a[128:131],v[128:131],v[32:35]
v_exp_f32 v231,v231
v_mfma_f32_16x16x32_bf16 v[132:135],a[132:135],v[160:163],v[132:135]
v_exp_f32 v236,v236
v_mfma_f32_16x16x32_bf16 v[132:135],a[136:139],v[192:195],v[132:135]
v_exp_f32 v237,v237
v_mfma_f32_16x16x32_bf16 v[132:135],a[140:143],v[224:227],v[132:135]
buffer_load_dwordx4 v9,s[72:75],s80 offen offset:2048 lds
v_mfma_f32_16x16x32_bf16 v[140:143],a[156:159],v[224:227],v[32:35]
v_exp_f32 v238,v238
v_mfma_f32_16x16x32_bf16 v[140:143],a[152:155],v[192:195],v[140:143]
v_exp_f32 v239,v239
v_mfma_f32_16x16x32_bf16 v[140:143],a[148:151],v[160:163],v[140:143]
v_exp_f32 v244,v244
v_mfma_f32_16x16x32_bf16 v[140:143],a[144:147],v[128:131],v[140:143]
v_exp_f32 v245,v245
v_mfma_f32_16x16x32_bf16 v[148:151],a[160:163],v[128:131],v[32:35]
v_cvt_pk_bf16_f32 v229,v230,v231
v_cvt_pk_bf16_f32 v230,v236,v237
v_mfma_f32_16x16x32_bf16 v[148:151],a[164:167],v[160:163],v[148:151]
v_exp_f32 v246,v246
v_mfma_f32_16x16x32_bf16 v[148:151],a[168:171],v[192:195],v[148:151]
v_exp_f32 v247,v247
v_mfma_f32_16x16x32_bf16 v[148:151],a[172:175],v[224:227],v[148:151]
v_cvt_pk_bf16_f32 v231,v238,v239
v_cvt_pk_bf16_f32 v244,v244,v245
v_mfma_f32_16x16x32_bf16 v[156:159],a[188:191],v[224:227],v[32:35]
s_waitcnt lgkmcnt(0)
v_exp_f32 v252,v252
v_mfma_f32_16x16x32_bf16 v[156:159],a[184:187],v[192:195],v[156:159]
v_exp_f32 v253,v253
v_mfma_f32_16x16x32_bf16 v[156:159],a[180:183],v[160:163],v[156:159]
v_exp_f32 v254,v254
v_mfma_f32_16x16x32_bf16 v[156:159],a[176:179],v[128:131],v[156:159]
v_exp_f32 v255,v255
v_mfma_f32_16x16x32_bf16 a[64:67],a[192:195],v[196:199],a[64:67]
buffer_load_dwordx4 v10,s[72:75],s80 offen offset:3072 lds
v_mfma_f32_16x16x32_bf16 a[64:67],a[196:199],v[212:215],a[64:67]
v_cvt_pk_bf16_f32 v245,v246,v247
v_cvt_pk_bf16_f32 v246,v252,v253
v_mfma_f32_16x16x32_bf16 a[68:71],a[204:207],v[212:215],a[68:71]
v_cvt_pk_bf16_f32 v247,v254,v255
v_add_u32 v1,s48,v1
v_mfma_f32_16x16x32_bf16 a[68:71],a[200:203],v[196:199],a[68:71]
v_exp_f32 v132,v132
v_mfma_f32_16x16x32_bf16 a[72:75],a[208:211],v[196:199],a[72:75]
buffer_store_dwordx4 v[116:119],v6,s[36:39],s87 offen offset:1024 sc1
v_mfma_f32_16x16x32_bf16 a[72:75],a[212:215],v[212:215],a[72:75]
v_exp_f32 v133,v133
v_mfma_f32_16x16x32_bf16 a[76:79],a[220:223],v[212:215],a[76:79]
v_exp_f32 v134,v134
v_mfma_f32_16x16x32_bf16 a[76:79],a[216:219],v[196:199],a[76:79]
v_exp_f32 v135,v135
v_mfma_f32_16x16x32_bf16 a[80:83],a[224:227],v[196:199],a[80:83]
v_cvt_pk_bf16_f32 v132,v132,v133
v_cvt_pk_bf16_f32 v133,v134,v135
v_mfma_f32_16x16x32_bf16 a[80:83],a[228:231],v[212:215],a[80:83]
v_exp_f32 v140,v140
v_mfma_f32_16x16x32_bf16 a[84:87],a[236:239],v[212:215],a[84:87]
s_add_u32 m0,s82,86016
v_exp_f32 v141,v141
v_mfma_f32_16x16x32_bf16 a[84:87],a[232:235],v[196:199],a[84:87]
v_exp_f32 v142,v142
v_mfma_f32_16x16x32_bf16 a[88:91],a[240:243],v[196:199],a[88:91]
v_exp_f32 v143,v143
v_mfma_f32_16x16x32_bf16 a[88:91],a[244:247],v[212:215],a[88:91]
buffer_load_dwordx4 v11,s[72:75],s80 offen lds
v_mfma_f32_16x16x32_bf16 a[92:95],a[252:255],v[212:215],a[92:95]
)A"
R"A(v_exp_f32 v148,v148
v_mfma_f32_16x16x32_bf16 a[92:95],a[248:251],v[196:199],a[92:95]
v_exp_f32 v149,v149
v_mfma_f32_16x16x32_bf16 v[56:59],v[28:31],v[196:199],v[56:59]
v_exp_f32 v150,v150
v_mfma_f32_16x16x32_bf16 v[56:59],v[28:31],v[212:215],v[56:59]
v_exp_f32 v151,v151
v_mfma_f32_16x16x32_bf16 v[164:167],a[128:131],v[136:139],v[36:39]
v_cvt_pk_bf16_f32 v134,v140,v141
v_cvt_pk_bf16_f32 v135,v142,v143
v_mfma_f32_16x16x32_bf16 v[164:167],a[132:135],v[168:171],v[164:167]
v_cvt_pk_bf16_f32 v148,v148,v149
v_cvt_pk_bf16_f32 v149,v150,v151
v_mfma_f32_16x16x32_bf16 v[164:167],a[136:139],v[200:203],v[164:167]
v_exp_f32 v156,v156
v_mfma_f32_16x16x32_bf16 v[164:167],a[140:143],v[232:235],v[164:167]
v_exp_f32 v157,v157
v_mfma_f32_16x16x32_bf16 v[172:175],a[156:159],v[232:235],v[36:39]
v_exp_f32 v158,v158
v_mfma_f32_16x16x32_bf16 v[172:175],a[152:155],v[200:203],v[172:175]
s_add_u32 m0,s82,87040
v_exp_f32 v159,v159
v_mfma_f32_16x16x32_bf16 v[172:175],a[148:151],v[168:171],v[172:175]
v_cvt_pk_bf16_f32 v150,v156,v157
v_cvt_pk_bf16_f32 v151,v158,v159
v_mfma_f32_16x16x32_bf16 v[172:175],a[144:147],v[136:139],v[172:175]
v_add_u32 v1,v4,v5
v_mfma_f32_16x16x32_bf16 v[180:183],a[160:163],v[136:139],v[36:39]
buffer_store_dwordx4 v[120:123],v6,s[36:39],s87 offen offset:2048 sc1
v_mfma_f32_16x16x32_bf16 v[180:183],a[164:167],v[168:171],v[180:183]
v_exp_f32 v164,v164
v_mfma_f32_16x16x32_bf16 v[180:183],a[168:171],v[200:203],v[180:183]
v_exp_f32 v165,v165
v_mfma_f32_16x16x32_bf16 v[180:183],a[172:175],v[232:235],v[180:183]
v_exp_f32 v166,v166
v_mfma_f32_16x16x32_bf16 v[188:191],a[188:191],v[232:235],v[36:39]
buffer_load_dwordx4 v7,s[72:75],s83 offen lds
v_mfma_f32_16x16x32_bf16 v[188:191],a[184:187],v[200:203],v[188:191]
v_exp_f32 v167,v167
v_mfma_f32_16x16x32_bf16 v[188:191],a[180:183],v[168:171],v[188:191]
v_cvt_pk_bf16_f32 v164,v164,v165
v_cvt_pk_bf16_f32 v165,v166,v167
v_mfma_f32_16x16x32_bf16 v[188:191],a[176:179],v[136:139],v[188:191]
v_exp_f32 v172,v172
v_mfma_f32_16x16x32_bf16 a[96:99],a[192:195],v[228:231],a[96:99]
ds_read_b64_tr_b16 a[192:193],v2
ds_read_b64_tr_b16 a[194:195],v2 offset:4096
v_mfma_f32_16x16x32_bf16 a[96:99],a[196:199],v[244:247],a[96:99]
ds_read_b64_tr_b16 a[196:197],v2 offset:8192
v_mfma_f32_16x16x32_bf16 a[100:103],a[204:207],v[244:247],a[100:103]
ds_read_b64_tr_b16 a[198:199],v2 offset:12288
v_mfma_f32_16x16x32_bf16 a[100:103],a[200:203],v[228:231],a[100:103]
ds_read_b64_tr_b16 a[204:205],v3 offset:8192
v_mfma_f32_16x16x32_bf16 a[104:107],a[208:211],v[228:231],a[104:107]
ds_read_b64_tr_b16 a[206:207],v3 offset:12288
ds_read_b64_tr_b16 a[200:201],v3
v_mfma_f32_16x16x32_bf16 a[104:107],a[212:215],v[244:247],a[104:107]
buffer_load_dwordx4 v8,s[72:75],s83 offen offset:1024 lds
v_mfma_f32_16x16x32_bf16 a[108:111],a[220:223],v[244:247],a[108:111]
ds_read_b64_tr_b16 a[202:203],v3 offset:4096
ds_read_b64_tr_b16 a[208:209],v2 offset:256
v_mfma_f32_16x16x32_bf16 a[108:111],a[216:219],v[228:231],a[108:111]
ds_read_b64_tr_b16 a[210:211],v2 offset:4352
v_mfma_f32_16x16x32_bf16 a[112:115],a[224:227],v[228:231],a[112:115]
ds_read_b64_tr_b16 a[212:213],v2 offset:8448
v_mfma_f32_16x16x32_bf16 a[112:115],a[228:231],v[244:247],a[112:115]
ds_read_b64_tr_b16 a[214:215],v2 offset:12544
v_mfma_f32_16x16x32_bf16 a[116:119],a[236:239],v[244:247],a[116:119]
ds_read_b64_tr_b16 a[220:221],v3 offset:8448
ds_read_b64_tr_b16 a[222:223],v3 offset:12544
v_mfma_f32_16x16x32_bf16 a[116:119],a[232:235],v[228:231],a[116:119]
ds_read_b64_tr_b16 a[216:217],v3 offset:256
v_mfma_f32_16x16x32_bf16 a[120:123],a[240:243],v[228:231],a[120:123]
ds_read_b64_tr_b16 a[218:219],v3 offset:4352
v_mfma_f32_16x16x32_bf16 a[120:123],a[244:247],v[244:247],a[120:123]
ds_read_b64_tr_b16 a[224:225],v2 offset:512
v_mfma_f32_16x16x32_bf16 a[124:127],a[252:255],v[244:247],a[124:127]
buffer_store_dwordx4 v[124:127],v6,s[36:39],s87 offen offset:3072 sc1
v_mfma_f32_16x16x32_bf16 a[124:127],a[248:251],v[228:231],a[124:127]
s_add_u32 s87,s55,77824
ds_read_b64_tr_b16 a[226:227],v2 offset:4608
ds_read_b64_tr_b16 a[228:229],v2 offset:8704
v_mfma_f32_16x16x32_bf16 v[60:63],v[28:31],v[228:231],v[60:63]
s_max_i32 s87,s87,s101
ds_read_b64_tr_b16 a[230:231],v2 offset:12800
v_mfma_f32_16x16x32_bf16 v[60:63],v[28:31],v[244:247],v[60:63]
ds_read_b64_tr_b16 a[236:237],v3 offset:8704
v_mfma_f32_16x16x32_bf16 v[196:199],a[128:131],v[144:147],v[40:43]
buffer_load_dwordx4 v9,s[72:75],s83 offen offset:2048 lds
v_mfma_f32_16x16x32_bf16 v[196:199],a[132:135],v[176:179],v[196:199]
ds_read_b64_tr_b16 a[238:239],v3 offset:12800
ds_read_b64_tr_b16 a[232:233],v3 offset:512
v_mfma_f32_16x16x32_bf16 v[196:199],a[136:139],v[208:211],v[196:199]
ds_read_b64_tr_b16 a[234:235],v3 offset:4608
v_mfma_f32_16x16x32_bf16 v[196:199],a[140:143],v[240:243],v[196:199]
ds_read_b64_tr_b16 a[240:241],v2 offset:768
v_mfma_f32_16x16x32_bf16 v[204:207],a[156:159],v[240:243],v[40:43]
ds_read_b64_tr_b16 a[242:243],v2 offset:4864
v_mfma_f32_16x16x32_bf16 v[204:207],a[152:155],v[208:211],v[204:207]
ds_read_b64_tr_b16 a[244:245],v2 offset:8960
ds_read_b64_tr_b16 a[246:247],v2 offset:13056
v_mfma_f32_16x16x32_bf16 v[204:207],a[148:151],v[176:179],v[204:207]
ds_read_b64_tr_b16 a[252:253],v3 offset:8960
v_sub_u32 v2,v4,v5
v_mfma_f32_16x16x32_bf16 v[204:207],a[144:147],v[144:147],v[204:207]
ds_read_b64_tr_b16 a[254:255],v3 offset:13056
v_mfma_f32_16x16x32_bf16 v[212:215],a[160:163],v[144:147],v[40:43]
ds_read_b64_tr_b16 a[248:249],v3 offset:768
v_mfma_f32_16x16x32_bf16 v[212:215],a[164:167],v[176:179],v[212:215]
ds_read_b64_tr_b16 a[250:251],v3 offset:4864
v_mfma_f32_16x16x32_bf16 v[212:215],a[168:171],v[208:211],v[212:215]
v_exp_f32 v173,v173
v_mfma_f32_16x16x32_bf16 v[212:215],a[172:175],v[240:243],v[212:215]
v_exp_f32 v174,v174
v_mfma_f32_16x16x32_bf16 v[220:223],a[188:191],v[240:243],v[40:43]
v_exp_f32 v175,v175
v_mfma_f32_16x16x32_bf16 v[220:223],a[184:187],v[208:211],v[220:223]
buffer_load_dwordx4 v10,s[72:75],s83 offen offset:3072 lds
v_mfma_f32_16x16x32_bf16 v[220:223],a[180:183],v[176:179],v[220:223]
v_exp_f32 v180,v180
v_mfma_f32_16x16x32_bf16 v[220:223],a[176:179],v[144:147],v[220:223]
s_waitcnt lgkmcnt(2)
v_exp_f32 v181,v181
v_mfma_f32_16x16x32_bf16 a[0:3],a[192:195],v[132:135],a[0:3]
v_exp_f32 v182,v182
v_mfma_f32_16x16x32_bf16 a[0:3],a[196:199],v[148:151],a[0:3]
v_exp_f32 v183,v183
v_mfma_f32_16x16x32_bf16 a[4:7],a[204:207],v[148:151],a[4:7]
v_cvt_pk_bf16_f32 v166,v172,v173
v_cvt_pk_bf16_f32 v167,v174,v175
v_mfma_f32_16x16x32_bf16 a[4:7],a[200:203],v[132:135],a[4:7]
v_cvt_pk_bf16_f32 v180,v180,v181
v_cvt_pk_bf16_f32 v181,v182,v183
v_mfma_f32_16x16x32_bf16 a[8:11],a[208:211],v[132:135],a[8:11]
v_exp_f32 v188,v188
v_mfma_f32_16x16x32_bf16 a[8:11],a[212:215],v[148:151],a[8:11]
v_exp_f32 v189,v189
v_mfma_f32_16x16x32_bf16 a[12:15],a[220:223],v[148:151],a[12:15]
v_exp_f32 v190,v190
v_mfma_f32_16x16x32_bf16 a[12:15],a[216:219],v[132:135],a[12:15]
s_add_u32 m0,s82,91136
v_exp_f32 v191,v191
v_mfma_f32_16x16x32_bf16 a[16:19],a[224:227],v[132:135],a[16:19]
v_cvt_pk_bf16_f32 v182,v188,v189
v_cvt_pk_bf16_f32 v183,v190,v191
v_mfma_f32_16x16x32_bf16 a[16:19],a[228:231],v[148:151],a[16:19]
v_exp_f32 v196,v196
v_mfma_f32_16x16x32_bf16 a[20:23],a[236:239],v[148:151],a[20:23]
buffer_load_dwordx4 v11,s[72:75],s83 offen lds
v_mfma_f32_16x16x32_bf16 a[20:23],a[232:235],v[132:135],a[20:23]
v_exp_f32 v197,v197
v_mfma_f32_16x16x32_bf16 a[24:27],a[240:243],v[132:135],a[24:27]
v_exp_f32 v198,v198
v_mfma_f32_16x16x32_bf16 a[24:27],a[244:247],v[148:151],a[24:27]
v_exp_f32 v199,v199
v_mfma_f32_16x16x32_bf16 a[28:31],a[252:255],v[148:151],a[28:31]
s_waitcnt lgkmcnt(0)
v_cvt_pk_bf16_f32 v196,v196,v197
v_cvt_pk_bf16_f32 v197,v198,v199
v_mfma_f32_16x16x32_bf16 a[28:31],a[248:251],v[132:135],a[28:31]
s_cmp_ge_u32 1,s71
s_cselect_b32 s80,4,2
s_mul_i32 s80,s80,163840
v_mfma_f32_16x16x32_bf16 v[48:51],v[28:31],v[132:135],v[48:51]
s_waitcnt vmcnt(15)
s_add_u32 s80,s80,s81
v_exp_f32 v204,v204
v_mfma_f32_16x16x32_bf16 v[48:51],v[28:31],v[148:151],v[48:51]
s_barrier
v_exp_f32 v205,v205
v_mfma_f32_16x16x32_bf16 v[228:231],a[128:131],v[152:155],v[44:47]
s_add_u32 s83,s80,20480
v_exp_f32 v206,v206
v_mfma_f32_16x16x32_bf16 v[228:231],a[132:135],v[184:187],v[228:231]
v_exp_f32 v207,v207
v_mfma_f32_16x16x32_bf16 v[228:231],a[136:139],v[216:219],v[228:231]
v_exp_f32 v212,v212
v_mfma_f32_16x16x32_bf16 v[228:231],a[140:143],v[248:251],v[228:231]
v_exp_f32 v213,v213
v_mfma_f32_16x16x32_bf16 v[236:239],a[156:159],v[248:251],v[44:47]
v_exp_f32 v214,v214
v_mfma_f32_16x16x32_bf16 v[236:239],a[152:155],v[216:219],v[236:239]
v_exp_f32 v215,v215
v_mfma_f32_16x16x32_bf16 v[236:239],a[148:151],v[184:187],v[236:239]
v_cvt_pk_bf16_f32 v198,v204,v205
v_cvt_pk_bf16_f32 v199,v206,v207
v_mfma_f32_16x16x32_bf16 v[236:239],a[144:147],v[152:155],v[236:239]
v_cvt_pk_bf16_f32 v212,v212,v213
v_cvt_pk_bf16_f32 v213,v214,v215
v_mfma_f32_16x16x32_bf16 v[244:247],a[160:163],v[152:155],v[44:47]
v_exp_f32 v220,v220
v_mfma_f32_16x16x32_bf16 v[244:247],a[164:167],v[184:187],v[244:247]
v_exp_f32 v221,v221
v_mfma_f32_16x16x32_bf16 v[244:247],a[168:171],v[216:219],v[244:247]
v_exp_f32 v222,v222
v_mfma_f32_16x16x32_bf16 v[244:247],a[172:175],v[248:251],v[244:247]
s_lshl_b32 s95,s42,2
s_add_u32 s95,s95,s40
s_lshl_b32 s95,s95,4
v_mfma_f32_16x16x32_bf16 v[252:255],a[188:191],v[248:251],v[44:47]
v_mov_b32 v18,s68
v_mov_b32 v19,s69
v_mfma_f32_16x16x32_bf16 v[252:255],a[184:187],v[216:219],v[252:255]
v_exp_f32 v223,v223
v_mfma_f32_16x16x32_bf16 v[252:255],a[180:183],v[184:187],v[252:255]
v_cvt_pk_bf16_f32 v214,v220,v221
v_cvt_pk_bf16_f32 v215,v222,v223
v_mfma_f32_16x16x32_bf16 v[252:255],a[176:179],v[152:155],v[252:255]
ds_read_b64_tr_b16 a[128:129],v1
v_max_f32 v20,0x00800000,v48
v_mfma_f32_16x16x32_bf16 a[32:35],a[192:195],v[164:167],a[32:35]
v_exp_f32 v228,v228
v_mfma_f32_16x16x32_bf16 a[32:35],a[196:199],v[180:183],a[32:35]
s_add_u32 m0,s82,122880
v_exp_f32 v229,v229
v_mfma_f32_16x16x32_bf16 a[36:39],a[204:207],v[180:183],a[36:39]
v_exp_f32 v230,v230
v_mfma_f32_16x16x32_bf16 a[36:39],a[200:203],v[164:167],a[36:39]
s_waitcnt vmcnt(3)
s_mov_b64 exec,1
buffer_store_dwordx2 v[18:19],v6,s[64:67],s95 offen sc1
s_mov_b64 exec,-1
v_mfma_f32_16x16x32_bf16 a[40:43],a[208:211],v[164:167],a[40:43]
v_exp_f32 v231,v231
v_mfma_f32_16x16x32_bf16 a[40:43],a[212:215],v[180:183],a[40:43]
v_rcp_f32 v20,v20
v_mfma_f32_16x16x32_bf16 a[44:47],a[220:223],v[180:183],a[44:47]
v_cvt_pk_bf16_f32 v228,v228,v229
v_cvt_pk_bf16_f32 v229,v230,v231
v_mfma_f32_16x16x32_bf16 a[44:47],a[216:219],v[164:167],a[44:47]
buffer_load_dwordx4 v7,s[72:75],s80 offen lds
v_mfma_f32_16x16x32_bf16 a[48:51],a[224:227],v[164:167],a[48:51]
v_exp_f32 v236,v236
v_mfma_f32_16x16x32_bf16 a[48:51],a[228:231],v[180:183],a[48:51]
v_exp_f32 v237,v237
v_mfma_f32_16x16x32_bf16 a[52:55],a[236:239],v[180:183],a[52:55]
v_exp_f32 v238,v238
v_mfma_f32_16x16x32_bf16 a[52:55],a[232:235],v[164:167],a[52:55]
v_exp_f32 v239,v239
v_mfma_f32_16x16x32_bf16 a[56:59],a[240:243],v[164:167],a[56:59]
v_exp_f32 v244,v244
v_mfma_f32_16x16x32_bf16 a[56:59],a[244:247],v[180:183],a[56:59]
v_exp_f32 v245,v245
v_mfma_f32_16x16x32_bf16 a[60:63],a[252:255],v[180:183],a[60:63]
v_exp_f32 v246,v246
v_mfma_f32_16x16x32_bf16 a[60:63],a[248:251],v[164:167],a[60:63]
v_exp_f32 v247,v247
v_mfma_f32_16x16x32_bf16 v[52:55],v[28:31],v[164:167],v[52:55]
v_accvgpr_read_b32 v132,a0
v_accvgpr_read_b32 v133,a1
v_accvgpr_read_b32 v134,a2
v_accvgpr_read_b32 v135,a3
v_accvgpr_read_b32 v12,a4
v_accvgpr_read_b32 v13,a5
v_accvgpr_read_b32 v14,a6
v_accvgpr_read_b32 v15,a7
v_pk_mul_f32 v[132:133],v[20:21],v[132:133] op_sel:[0,0] op_sel_hi:[0,1]
v_pk_mul_f32 v[134:135],v[20:21],v[134:135] op_sel:[0,0] op_sel_hi:[0,1]
v_pk_mul_f32 v[12:13],v[20:21],v[12:13] op_sel:[0,0] op_sel_hi:[0,1]
v_pk_mul_f32 v[14:15],v[20:21],v[14:15] op_sel:[0,0] op_sel_hi:[0,1]
v_cvt_pk_bf16_f32 v132,v132,v133
v_cvt_pk_bf16_f32 v133,v134,v135
v_cvt_pk_bf16_f32 v134,v12,v13
v_cvt_pk_bf16_f32 v135,v14,v15
v_mfma_f32_16x16x32_bf16 v[52:55],v[28:31],v[180:183],v[52:55]
v_cvt_pk_bf16_f32 v230,v236,v237
)A"
R"A(v_cvt_pk_bf16_f32 v231,v238,v239
v_mfma_f32_16x16x32_bf16 a[64:67],a[192:195],v[196:199],a[64:67]
v_cvt_pk_bf16_f32 v244,v244,v245
v_cvt_pk_bf16_f32 v245,v246,v247
v_mfma_f32_16x16x32_bf16 a[64:67],a[196:199],v[212:215],a[64:67]
v_exp_f32 v252,v252
v_mfma_f32_16x16x32_bf16 a[68:71],a[204:207],v[212:215],a[68:71]
buffer_load_dwordx4 v8,s[72:75],s80 offen offset:1024 lds
v_mfma_f32_16x16x32_bf16 a[68:71],a[200:203],v[196:199],a[68:71]
v_exp_f32 v253,v253
v_mfma_f32_16x16x32_bf16 a[72:75],a[208:211],v[196:199],a[72:75]
v_exp_f32 v254,v254
v_mfma_f32_16x16x32_bf16 a[72:75],a[212:215],v[212:215],a[72:75]
v_exp_f32 v255,v255
v_mfma_f32_16x16x32_bf16 a[76:79],a[220:223],v[212:215],a[76:79]
v_cvt_pk_bf16_f32 v246,v252,v253
v_cvt_pk_bf16_f32 v247,v254,v255
v_mfma_f32_16x16x32_bf16 a[76:79],a[216:219],v[196:199],a[76:79]
v_accvgpr_read_b32 v140,a8
v_accvgpr_read_b32 v141,a9
v_accvgpr_read_b32 v142,a10
v_accvgpr_read_b32 v143,a11
v_accvgpr_read_b32 v12,a12
v_accvgpr_read_b32 v13,a13
v_accvgpr_read_b32 v14,a14
v_accvgpr_read_b32 v15,a15
v_pk_mul_f32 v[140:141],v[20:21],v[140:141] op_sel:[0,0] op_sel_hi:[0,1]
v_pk_mul_f32 v[142:143],v[20:21],v[142:143] op_sel:[0,0] op_sel_hi:[0,1]
v_pk_mul_f32 v[12:13],v[20:21],v[12:13] op_sel:[0,0] op_sel_hi:[0,1]
v_pk_mul_f32 v[14:15],v[20:21],v[14:15] op_sel:[0,0] op_sel_hi:[0,1]
v_cvt_pk_bf16_f32 v140,v140,v141
v_cvt_pk_bf16_f32 v141,v142,v143
v_cvt_pk_bf16_f32 v142,v12,v13
v_cvt_pk_bf16_f32 v143,v14,v15
v_mfma_f32_16x16x32_bf16 a[80:83],a[224:227],v[196:199],a[80:83]
ds_read_b64_tr_b16 a[130:131],v1 offset:5120
ds_read_b64_tr_b16 a[132:133],v1 offset:10240
v_mfma_f32_16x16x32_bf16 a[80:83],a[228:231],v[212:215],a[80:83]
ds_read_b64_tr_b16 a[134:135],v1 offset:15360
v_accvgpr_read_b32 v148,a16
v_mfma_f32_16x16x32_bf16 a[84:87],a[236:239],v[212:215],a[84:87]
ds_read_b64_tr_b16 a[136:137],v1 offset:20480
v_accvgpr_read_b32 v149,a17
v_mfma_f32_16x16x32_bf16 a[84:87],a[232:235],v[196:199],a[84:87]
ds_read_b64_tr_b16 a[138:139],v1 offset:25600
v_mul_f32 v148,v20,v148
v_mfma_f32_16x16x32_bf16 a[88:91],a[240:243],v[196:199],a[88:91]
ds_read_b64_tr_b16 a[140:141],v1 offset:30720
ds_read_b64_tr_b16 a[142:143],v1 offset:35840
v_mfma_f32_16x16x32_bf16 a[88:91],a[244:247],v[212:215],a[88:91]
v_mul_f32 v149,v20,v149
v_accvgpr_read_b32 v150,a18
v_mfma_f32_16x16x32_bf16 a[92:95],a[252:255],v[212:215],a[92:95]
v_accvgpr_read_b32 v151,a19
v_cvt_pk_bf16_f32 v148,v148,v149
v_mfma_f32_16x16x32_bf16 a[92:95],a[248:251],v[196:199],a[92:95]
buffer_store_dwordx4 v[132:135],v6,s[36:39],s84 offen sc1
v_mfma_f32_16x16x32_bf16 v[56:59],v[28:31],v[196:199],v[56:59]
v_mul_f32 v150,v20,v150
v_mul_f32 v151,v20,v151
v_mfma_f32_16x16x32_bf16 v[56:59],v[28:31],v[212:215],v[56:59]
v_cvt_pk_bf16_f32 v149,v150,v151
v_accvgpr_read_b32 v150,a20
v_mfma_f32_16x16x32_bf16 a[96:99],a[192:195],v[228:231],a[96:99]
v_accvgpr_read_b32 v151,a21
v_accvgpr_read_b32 v14,a22
v_mfma_f32_16x16x32_bf16 a[96:99],a[196:199],v[244:247],a[96:99]
buffer_load_dwordx4 v9,s[72:75],s80 offen offset:2048 lds
v_mfma_f32_16x16x32_bf16 a[100:103],a[204:207],v[244:247],a[100:103]
v_accvgpr_read_b32 v15,a23
v_mul_f32 v150,v20,v150
v_mfma_f32_16x16x32_bf16 a[100:103],a[200:203],v[228:231],a[100:103]
v_mul_f32 v151,v20,v151
v_mul_f32 v14,v20,v14
v_mfma_f32_16x16x32_bf16 a[104:107],a[208:211],v[228:231],a[104:107]
v_mul_f32 v15,v20,v15
v_cvt_pk_bf16_f32 v150,v150,v151
v_mfma_f32_16x16x32_bf16 a[104:107],a[212:215],v[244:247],a[104:107]
ds_read_b64_tr_b16 a[144:145],v2 offset:32
v_cvt_pk_bf16_f32 v151,v14,v15
v_mfma_f32_16x16x32_bf16 a[108:111],a[220:223],v[244:247],a[108:111]
ds_read_b64_tr_b16 a[146:147],v2 offset:5152
ds_read_b64_tr_b16 a[148:149],v2 offset:10272
v_mfma_f32_16x16x32_bf16 a[108:111],a[216:219],v[228:231],a[108:111]
ds_read_b64_tr_b16 a[150:151],v2 offset:15392
v_accvgpr_read_b32 v156,a24
v_mfma_f32_16x16x32_bf16 a[112:115],a[224:227],v[228:231],a[112:115]
ds_read_b64_tr_b16 a[152:153],v2 offset:20512
v_accvgpr_read_b32 v157,a25
v_mfma_f32_16x16x32_bf16 a[112:115],a[228:231],v[244:247],a[112:115]
ds_read_b64_tr_b16 a[154:155],v2 offset:25632
v_mul_f32 v156,v20,v156
v_mfma_f32_16x16x32_bf16 a[116:119],a[236:239],v[244:247],a[116:119]
ds_read_b64_tr_b16 a[156:157],v2 offset:30752
ds_read_b64_tr_b16 a[158:159],v2 offset:35872
v_mfma_f32_16x16x32_bf16 a[116:119],a[232:235],v[228:231],a[116:119]
v_mul_f32 v157,v20,v157
v_accvgpr_read_b32 v158,a26
v_mfma_f32_16x16x32_bf16 a[120:123],a[240:243],v[228:231],a[120:123]
v_accvgpr_read_b32 v159,a27
v_cvt_pk_bf16_f32 v156,v156,v157
v_mfma_f32_16x16x32_bf16 a[120:123],a[244:247],v[244:247],a[120:123]
v_mul_f32 v158,v20,v158
v_mul_f32 v159,v20,v159
v_mfma_f32_16x16x32_bf16 a[124:127],a[252:255],v[244:247],a[124:127]
v_cvt_pk_bf16_f32 v157,v158,v159
v_accvgpr_read_b32 v158,a28
v_mfma_f32_16x16x32_bf16 a[124:127],a[248:251],v[228:231],a[124:127]
v_accvgpr_read_b32 v159,a29
v_accvgpr_read_b32 v16,a30
v_mfma_f32_16x16x32_bf16 v[60:63],v[28:31],v[228:231],v[60:63]
buffer_load_dwordx4 v10,s[72:75],s80 offen offset:3072 lds
v_mfma_f32_16x16x32_bf16 v[60:63],v[28:31],v[244:247],v[60:63]
s_waitcnt lgkmcnt(2)
v_accvgpr_read_b32 v17,a31
v_mul_f32 v158,v20,v158
v_mfma_f32_16x16x32_bf16 a[192:195],a[128:131],v[64:67],0
v_mul_f32 v159,v20,v159
v_mul_f32 v16,v20,v16
v_mfma_f32_16x16x32_bf16 a[192:195],a[132:135],v[68:71],a[192:195]
v_mul_f32 v17,v20,v17
v_cvt_pk_bf16_f32 v158,v158,v159
v_mfma_f32_16x16x32_bf16 a[192:195],a[136:139],v[72:75],a[192:195]
buffer_store_dwordx4 v[140:143],v6,s[36:39],s84 offen offset:1024 sc1
v_mfma_f32_16x16x32_bf16 a[192:195],a[140:143],v[76:79],a[192:195]
ds_read_b64_tr_b16 a[160:161],v1 offset:64
v_cvt_pk_bf16_f32 v159,v16,v17
v_mfma_f32_16x16x32_bf16 a[196:199],a[140:143],v[92:95],0
ds_read_b64_tr_b16 a[162:163],v1 offset:5184
ds_read_b64_tr_b16 a[164:165],v1 offset:10304
v_mfma_f32_16x16x32_bf16 a[196:199],a[136:139],v[88:91],a[196:199]
ds_read_b64_tr_b16 a[166:167],v1 offset:15424
v_max_f32 v21,0x00800000,v52
v_mfma_f32_16x16x32_bf16 a[196:199],a[132:135],v[84:87],a[196:199]
ds_read_b64_tr_b16 a[168:169],v1 offset:20544
v_accvgpr_read_b32 v180,a48
v_mfma_f32_16x16x32_bf16 a[196:199],a[128:131],v[80:83],a[196:199]
ds_read_b64_tr_b16 a[170:171],v1 offset:25664
v_accvgpr_read_b32 v181,a49
v_mfma_f32_16x16x32_bf16 a[200:203],a[128:131],v[96:99],0
ds_read_b64_tr_b16 a[172:173],v1 offset:30784
ds_read_b64_tr_b16 a[174:175],v1 offset:35904
v_mfma_f32_16x16x32_bf16 a[200:203],a[132:135],v[100:103],a[200:203]
v_rcp_f32 v21,v21
v_mfma_f32_16x16x32_bf16 a[200:203],a[136:139],v[104:107],a[200:203]
v_accvgpr_read_b32 v164,a32
v_accvgpr_read_b32 v165,a33
v_accvgpr_read_b32 v166,a34
v_accvgpr_read_b32 v167,a35
v_accvgpr_read_b32 v12,a36
v_accvgpr_read_b32 v13,a37
v_accvgpr_read_b32 v14,a38
v_accvgpr_read_b32 v15,a39
v_pk_mul_f32 v[164:165],v[20:21],v[164:165] op_sel:[1,0] op_sel_hi:[1,1]
v_pk_mul_f32 v[166:167],v[20:21],v[166:167] op_sel:[1,0] op_sel_hi:[1,1]
v_pk_mul_f32 v[12:13],v[20:21],v[12:13] op_sel:[1,0] op_sel_hi:[1,1]
v_pk_mul_f32 v[14:15],v[20:21],v[14:15] op_sel:[1,0] op_sel_hi:[1,1]
v_cvt_pk_bf16_f32 v164,v164,v165
v_cvt_pk_bf16_f32 v165,v166,v167
v_cvt_pk_bf16_f32 v166,v12,v13
v_cvt_pk_bf16_f32 v167,v14,v15
v_mfma_f32_16x16x32_bf16 a[200:203],a[140:143],v[108:111],a[200:203]
s_add_u32 m0,s82,126976
ds_read_b64_tr_b16 a[176:177],v2 offset:96
ds_read_b64_tr_b16 a[178:179],v2 offset:5216
v_mfma_f32_16x16x32_bf16 a[204:207],a[140:143],v[124:127],0
v_accvgpr_read_b32 v172,a40
v_accvgpr_read_b32 v173,a41
v_accvgpr_read_b32 v174,a42
v_accvgpr_read_b32 v175,a43
v_accvgpr_read_b32 v12,a44
v_accvgpr_read_b32 v13,a45
v_accvgpr_read_b32 v14,a46
v_accvgpr_read_b32 v15,a47
v_pk_mul_f32 v[172:173],v[20:21],v[172:173] op_sel:[1,0] op_sel_hi:[1,1]
v_pk_mul_f32 v[174:175],v[20:21],v[174:175] op_sel:[1,0] op_sel_hi:[1,1]
v_pk_mul_f32 v[12:13],v[20:21],v[12:13] op_sel:[1,0] op_sel_hi:[1,1]
v_pk_mul_f32 v[14:15],v[20:21],v[14:15] op_sel:[1,0] op_sel_hi:[1,1]
v_cvt_pk_bf16_f32 v172,v172,v173
v_cvt_pk_bf16_f32 v173,v174,v175
v_cvt_pk_bf16_f32 v174,v12,v13
v_cvt_pk_bf16_f32 v175,v14,v15
v_mfma_f32_16x16x32_bf16 a[204:207],a[136:139],v[120:123],a[204:207]
ds_read_b64_tr_b16 a[180:181],v2 offset:10336
ds_read_b64_tr_b16 a[182:183],v2 offset:15456
v_mfma_f32_16x16x32_bf16 a[204:207],a[132:135],v[116:119],a[204:207]
buffer_load_dwordx4 v11,s[72:75],s80 offen lds
v_mfma_f32_16x16x32_bf16 a[204:207],a[128:131],v[112:115],a[204:207]
ds_read_b64_tr_b16 a[184:185],v2 offset:20576
ds_read_b64_tr_b16 a[186:187],v2 offset:25696
v_mfma_f32_16x16x32_bf16 a[208:211],a[144:147],v[64:67],0
ds_read_b64_tr_b16 a[188:189],v2 offset:30816
v_mul_f32 v180,v21,v180
v_mfma_f32_16x16x32_bf16 a[208:211],a[148:151],v[68:71],a[208:211]
ds_read_b64_tr_b16 a[190:191],v2 offset:35936
v_mul_f32 v181,v21,v181
v_mfma_f32_16x16x32_bf16 a[208:211],a[152:155],v[72:75],a[208:211]
s_waitcnt lgkmcnt(8)
v_accvgpr_read_b32 v182,a50
v_accvgpr_read_b32 v183,a51
v_mfma_f32_16x16x32_bf16 a[208:211],a[156:159],v[76:79],a[208:211]
buffer_store_dwordx4 v[148:151],v6,s[36:39],s84 offen offset:2048 sc1
v_mfma_f32_16x16x32_bf16 a[212:215],a[156:159],v[92:95],0
v_cvt_pk_bf16_f32 v180,v180,v181
v_mul_f32 v182,v21,v182
v_mfma_f32_16x16x32_bf16 a[212:215],a[152:155],v[88:91],a[212:215]
v_mul_f32 v183,v21,v183
v_cvt_pk_bf16_f32 v181,v182,v183
v_mfma_f32_16x16x32_bf16 a[212:215],a[148:151],v[84:87],a[212:215]
v_accvgpr_read_b32 v182,a52
v_accvgpr_read_b32 v183,a53
v_mfma_f32_16x16x32_bf16 a[212:215],a[144:147],v[80:83],a[212:215]
v_accvgpr_read_b32 v12,a54
v_accvgpr_read_b32 v13,a55
v_mfma_f32_16x16x32_bf16 a[216:219],a[144:147],v[96:99],0
v_mul_f32 v182,v21,v182
v_mul_f32 v183,v21,v183
v_mfma_f32_16x16x32_bf16 a[216:219],a[148:151],v[100:103],a[216:219]
v_mul_f32 v12,v21,v12
v_mul_f32 v13,v21,v13
v_mfma_f32_16x16x32_bf16 a[216:219],a[152:155],v[104:107],a[216:219]
v_cvt_pk_bf16_f32 v182,v182,v183
v_cvt_pk_bf16_f32 v183,v12,v13
v_mfma_f32_16x16x32_bf16 a[216:219],a[156:159],v[108:111],a[216:219]
s_add_u32 m0,s82,128000
ds_read_b64_tr_b16 a[128:129],v1 offset:128
ds_read_b64_tr_b16 a[130:131],v1 offset:5248
v_mfma_f32_16x16x32_bf16 a[220:223],a[156:159],v[124:127],0
ds_read_b64_tr_b16 a[132:133],v1 offset:10368
v_accvgpr_read_b32 v188,a56
v_mfma_f32_16x16x32_bf16 a[220:223],a[152:155],v[120:123],a[220:223]
ds_read_b64_tr_b16 a[134:135],v1 offset:15488
v_accvgpr_read_b32 v189,a57
v_mfma_f32_16x16x32_bf16 a[220:223],a[148:151],v[116:119],a[220:223]
buffer_load_dwordx4 v7,s[72:75],s83 offen lds
v_mfma_f32_16x16x32_bf16 a[220:223],a[144:147],v[112:115],a[220:223]
ds_read_b64_tr_b16 a[136:137],v1 offset:20608
ds_read_b64_tr_b16 a[138:139],v1 offset:25728
v_mfma_f32_16x16x32_bf16 a[224:227],a[160:163],v[64:67],0
ds_read_b64_tr_b16 a[140:141],v1 offset:30848
v_mul_f32 v188,v21,v188
v_mfma_f32_16x16x32_bf16 a[224:227],a[164:167],v[68:71],a[224:227]
ds_read_b64_tr_b16 a[142:143],v1 offset:35968
v_mul_f32 v189,v21,v189
v_mfma_f32_16x16x32_bf16 a[224:227],a[168:171],v[72:75],a[224:227]
v_accvgpr_read_b32 v190,a58
v_accvgpr_read_b32 v191,a59
v_mfma_f32_16x16x32_bf16 a[224:227],a[172:175],v[76:79],a[224:227]
v_cvt_pk_bf16_f32 v188,v188,v189
v_mul_f32 v190,v21,v190
v_mfma_f32_16x16x32_bf16 a[228:231],a[172:175],v[92:95],0
v_mul_f32 v191,v21,v191
v_cvt_pk_bf16_f32 v189,v190,v191
v_mfma_f32_16x16x32_bf16 a[228:231],a[168:171],v[88:91],a[228:231]
v_accvgpr_read_b32 v190,a60
v_accvgpr_read_b32 v191,a61
v_mfma_f32_16x16x32_bf16 a[228:231],a[164:167],v[84:87],a[228:231]
v_accvgpr_read_b32 v14,a62
v_accvgpr_read_b32 v15,a63
v_mfma_f32_16x16x32_bf16 a[228:231],a[160:163],v[80:83],a[228:231]
buffer_store_dwordx4 v[156:159],v6,s[36:39],s84 offen offset:3072 sc1
v_mfma_f32_16x16x32_bf16 a[232:235],a[160:163],v[96:99],0
v_mul_f32 v190,v21,v190
v_mul_f32 v191,v21,v191
v_mfma_f32_16x16x32_bf16 a[232:235],a[164:167],v[100:103],a[232:235]
v_mul_f32 v14,v21,v14
v_mul_f32 v15,v21,v15
v_mfma_f32_16x16x32_bf16 a[232:235],a[168:171],v[104:107],a[232:235]
v_cvt_pk_bf16_f32 v190,v190,v191
v_cvt_pk_bf16_f32 v191,v14,v15
v_mfma_f32_16x16x32_bf16 a[232:235],a[172:175],v[108:111],a[232:235]
)A"
R"A(ds_read_b64_tr_b16 a[144:145],v2 offset:160
v_max_f32 v22,0x00800000,v56
v_mfma_f32_16x16x32_bf16 a[236:239],a[172:175],v[124:127],0
ds_read_b64_tr_b16 a[146:147],v2 offset:5280
ds_read_b64_tr_b16 a[148:149],v2 offset:10400
v_mfma_f32_16x16x32_bf16 a[236:239],a[168:171],v[120:123],a[236:239]
v_rcp_f32 v22,v22
v_mfma_f32_16x16x32_bf16 a[236:239],a[164:167],v[116:119],a[236:239]
buffer_load_dwordx4 v8,s[72:75],s83 offen offset:1024 lds
v_mfma_f32_16x16x32_bf16 a[236:239],a[160:163],v[112:115],a[236:239]
s_waitcnt lgkmcnt(3)
ds_read_b64_tr_b16 a[150:151],v2 offset:15520
v_accvgpr_read_b32 v196,a64
v_mfma_f32_16x16x32_bf16 a[240:243],a[176:179],v[64:67],0
v_accvgpr_read_b32 v197,a65
v_mul_f32 v196,v22,v196
v_mfma_f32_16x16x32_bf16 a[240:243],a[180:183],v[68:71],a[240:243]
v_mul_f32 v197,v22,v197
v_accvgpr_read_b32 v198,a66
v_mfma_f32_16x16x32_bf16 a[240:243],a[184:187],v[72:75],a[240:243]
ds_read_b64_tr_b16 a[152:153],v2 offset:20640
v_accvgpr_read_b32 v199,a67
v_mfma_f32_16x16x32_bf16 a[240:243],a[188:191],v[76:79],a[240:243]
ds_read_b64_tr_b16 a[154:155],v2 offset:25760
v_cvt_pk_bf16_f32 v196,v196,v197
v_mfma_f32_16x16x32_bf16 a[244:247],a[188:191],v[92:95],0
v_mul_f32 v198,v22,v198
v_mul_f32 v199,v22,v199
v_mfma_f32_16x16x32_bf16 a[244:247],a[184:187],v[88:91],a[244:247]
ds_read_b64_tr_b16 a[156:157],v2 offset:30880
v_cvt_pk_bf16_f32 v197,v198,v199
v_mfma_f32_16x16x32_bf16 a[244:247],a[180:183],v[84:87],a[244:247]
ds_read_b64_tr_b16 a[158:159],v2 offset:36000
v_accvgpr_read_b32 v198,a68
v_mfma_f32_16x16x32_bf16 a[244:247],a[176:179],v[80:83],a[244:247]
v_accvgpr_read_b32 v199,a69
v_accvgpr_read_b32 v16,a70
v_mfma_f32_16x16x32_bf16 a[248:251],a[176:179],v[96:99],0
v_accvgpr_read_b32 v17,a71
v_mul_f32 v198,v22,v198
v_mfma_f32_16x16x32_bf16 a[248:251],a[180:183],v[100:103],a[248:251]
v_mul_f32 v199,v22,v199
v_mul_f32 v16,v22,v16
v_mfma_f32_16x16x32_bf16 a[248:251],a[184:187],v[104:107],a[248:251]
v_mul_f32 v17,v22,v17
v_cvt_pk_bf16_f32 v198,v198,v199
v_mfma_f32_16x16x32_bf16 a[248:251],a[188:191],v[108:111],a[248:251]
v_cvt_pk_bf16_f32 v199,v16,v17
v_accvgpr_read_b32 v204,a72
v_mfma_f32_16x16x32_bf16 a[252:255],a[188:191],v[124:127],0
buffer_store_dwordx4 v[164:167],v6,s[36:39],s85 offen sc1
v_mfma_f32_16x16x32_bf16 a[252:255],a[184:187],v[120:123],a[252:255]
v_accvgpr_read_b32 v205,a73
v_mul_f32 v204,v22,v204
v_mfma_f32_16x16x32_bf16 a[252:255],a[180:183],v[116:119],a[252:255]
v_mul_f32 v205,v22,v205
v_accvgpr_read_b32 v206,a74
v_mfma_f32_16x16x32_bf16 a[252:255],a[176:179],v[112:115],a[252:255]
v_accvgpr_read_b32 v207,a75
v_cvt_pk_bf16_f32 v204,v204,v205
v_mfma_f32_16x16x32_bf16 a[0:3],a[128:131],v[64:67],0
buffer_load_dwordx4 v9,s[72:75],s83 offen offset:2048 lds
v_mfma_f32_16x16x32_bf16 a[0:3],a[132:135],v[68:71],a[0:3]
v_mul_f32 v206,v22,v206
v_mul_f32 v207,v22,v207
v_mfma_f32_16x16x32_bf16 a[0:3],a[136:139],v[72:75],a[0:3]
v_cvt_pk_bf16_f32 v205,v206,v207
v_accvgpr_read_b32 v206,a76
v_mfma_f32_16x16x32_bf16 a[0:3],a[140:143],v[76:79],a[0:3]
v_accvgpr_read_b32 v207,a77
v_accvgpr_read_b32 v12,a78
v_mfma_f32_16x16x32_bf16 a[4:7],a[140:143],v[92:95],0
v_accvgpr_read_b32 v13,a79
v_mul_f32 v206,v22,v206
v_mfma_f32_16x16x32_bf16 a[4:7],a[136:139],v[88:91],a[4:7]
ds_read_b64_tr_b16 a[160:161],v1 offset:192
v_mul_f32 v207,v22,v207
v_mfma_f32_16x16x32_bf16 a[4:7],a[132:135],v[84:87],a[4:7]
s_lshl_b32 s94,s70,4
ds_read_b64_tr_b16 a[162:163],v1 offset:5312
v_mul_f32 v12,v22,v12
v_mfma_f32_16x16x32_bf16 a[4:7],a[128:131],v[80:83],a[4:7]
s_add_u32 s94,s94,s40
v_mul_f32 v13,v22,v13
v_cvt_pk_bf16_f32 v206,v206,v207
v_mfma_f32_16x16x32_bf16 a[8:11],a[128:131],v[96:99],0
s_lshl_b32 s94,s94,4
ds_read_b64_tr_b16 a[164:165],v1 offset:10432
v_cvt_pk_bf16_f32 v207,v12,v13
v_mfma_f32_16x16x32_bf16 a[8:11],a[132:135],v[100:103],a[8:11]
ds_read_b64_tr_b16 a[166:167],v1 offset:15552
ds_read_b64_tr_b16 a[168:169],v1 offset:20672
v_mfma_f32_16x16x32_bf16 a[8:11],a[136:139],v[104:107],a[8:11]
ds_read_b64_tr_b16 a[170:171],v1 offset:25792
v_accvgpr_read_b32 v212,a80
v_mfma_f32_16x16x32_bf16 a[8:11],a[140:143],v[108:111],a[8:11]
ds_read_b64_tr_b16 a[172:173],v1 offset:30912
v_accvgpr_read_b32 v213,a81
v_mfma_f32_16x16x32_bf16 a[12:15],a[140:143],v[124:127],0
ds_read_b64_tr_b16 a[174:175],v1 offset:36032
v_mul_f32 v212,v22,v212
v_mfma_f32_16x16x32_bf16 a[12:15],a[136:139],v[120:123],a[12:15]
v_mul_f32 v213,v22,v213
v_accvgpr_read_b32 v214,a82
v_mfma_f32_16x16x32_bf16 a[12:15],a[132:135],v[116:119],a[12:15]
buffer_load_dwordx4 v10,s[72:75],s83 offen offset:3072 lds
v_mfma_f32_16x16x32_bf16 a[12:15],a[128:131],v[112:115],a[12:15]
s_waitcnt lgkmcnt(6)
v_accvgpr_read_b32 v215,a83
v_cvt_pk_bf16_f32 v212,v212,v213
v_mfma_f32_16x16x32_bf16 a[16:19],a[144:147],v[64:67],0
v_mul_f32 v214,v22,v214
v_mul_f32 v215,v22,v215
v_mfma_f32_16x16x32_bf16 a[16:19],a[148:151],v[68:71],a[16:19]
v_cvt_pk_bf16_f32 v213,v214,v215
v_accvgpr_read_b32 v214,a84
v_mfma_f32_16x16x32_bf16 a[16:19],a[152:155],v[72:75],a[16:19]
buffer_store_dwordx4 v[172:175],v6,s[36:39],s85 offen offset:1024 sc1
v_mfma_f32_16x16x32_bf16 a[16:19],a[156:159],v[76:79],a[16:19]
v_accvgpr_read_b32 v215,a85
v_accvgpr_read_b32 v14,a86
v_mfma_f32_16x16x32_bf16 a[20:23],a[156:159],v[92:95],0
v_accvgpr_read_b32 v15,a87
v_mul_f32 v214,v22,v214
v_mfma_f32_16x16x32_bf16 a[20:23],a[152:155],v[88:91],a[20:23]
v_mul_f32 v215,v22,v215
v_mul_f32 v14,v22,v14
v_mfma_f32_16x16x32_bf16 a[20:23],a[148:151],v[84:87],a[20:23]
v_mul_f32 v15,v22,v15
v_cvt_pk_bf16_f32 v214,v214,v215
v_mfma_f32_16x16x32_bf16 a[20:23],a[144:147],v[80:83],a[20:23]
ds_read_b64_tr_b16 a[176:177],v2 offset:224
v_cvt_pk_bf16_f32 v215,v14,v15
v_mfma_f32_16x16x32_bf16 a[24:27],a[144:147],v[96:99],0
ds_read_b64_tr_b16 a[178:179],v2 offset:5344
ds_read_b64_tr_b16 a[180:181],v2 offset:10464
v_mfma_f32_16x16x32_bf16 a[24:27],a[148:151],v[100:103],a[24:27]
ds_read_b64_tr_b16 a[182:183],v2 offset:15584
v_accvgpr_read_b32 v220,a88
v_mfma_f32_16x16x32_bf16 a[24:27],a[152:155],v[104:107],a[24:27]
ds_read_b64_tr_b16 a[184:185],v2 offset:20704
v_accvgpr_read_b32 v221,a89
v_mfma_f32_16x16x32_bf16 a[24:27],a[156:159],v[108:111],a[24:27]
s_add_u32 m0,s82,132096
ds_read_b64_tr_b16 a[186:187],v2 offset:25824
v_bfe_u32 v24,v6,4,2
v_mfma_f32_16x16x32_bf16 a[28:31],a[156:159],v[124:127],0
ds_read_b64_tr_b16 a[188:189],v2 offset:30944
ds_read_b64_tr_b16 a[190:191],v2 offset:36064
v_mfma_f32_16x16x32_bf16 a[28:31],a[152:155],v[120:123],a[28:31]
v_mul_f32 v220,v22,v220
v_mul_f32 v221,v22,v221
v_mfma_f32_16x16x32_bf16 a[28:31],a[148:151],v[116:119],a[28:31]
buffer_load_dwordx4 v11,s[72:75],s83 offen lds
v_mfma_f32_16x16x32_bf16 a[28:31],a[144:147],v[112:115],a[28:31]
v_accvgpr_read_b32 v222,a90
v_accvgpr_read_b32 v223,a91
v_mfma_f32_16x16x32_bf16 a[32:35],a[160:163],v[64:67],0
s_waitcnt lgkmcnt(5)
v_min_u32 v24,2,v24
v_cvt_pk_bf16_f32 v220,v220,v221
v_mfma_f32_16x16x32_bf16 a[32:35],a[164:167],v[68:71],a[32:35]
v_mul_f32 v222,v22,v222
v_mul_f32 v223,v22,v223
v_mfma_f32_16x16x32_bf16 a[32:35],a[168:171],v[72:75],a[32:35]
v_add_u32 v24,s71,v24
v_cvt_pk_bf16_f32 v221,v222,v223
v_mfma_f32_16x16x32_bf16 a[32:35],a[172:175],v[76:79],a[32:35]
v_add_u32 v24,1,v24
v_accvgpr_read_b32 v222,a92
v_mfma_f32_16x16x32_bf16 a[36:39],a[172:175],v[92:95],0
buffer_store_dwordx4 v[180:183],v6,s[36:39],s85 offen offset:2048 sc1
v_mfma_f32_16x16x32_bf16 a[36:39],a[168:171],v[88:91],a[36:39]
v_accvgpr_read_b32 v223,a93
v_and_b32 v24,3,v24
v_mfma_f32_16x16x32_bf16 a[36:39],a[164:167],v[84:87],a[36:39]
v_accvgpr_read_b32 v16,a94
v_accvgpr_read_b32 v17,a95
v_mfma_f32_16x16x32_bf16 a[36:39],a[160:163],v[80:83],a[36:39]
v_mul_f32 v222,v22,v222
v_mul_f32 v223,v22,v223
v_mfma_f32_16x16x32_bf16 a[40:43],a[160:163],v[96:99],0
v_lshlrev_b32 v24,6,v24
v_mul_f32 v16,v22,v16
v_mfma_f32_16x16x32_bf16 a[40:43],a[164:167],v[100:103],a[40:43]
v_mul_f32 v17,v22,v17
v_cvt_pk_bf16_f32 v222,v222,v223
v_mfma_f32_16x16x32_bf16 a[40:43],a[168:171],v[104:107],a[40:43]
v_cvt_pk_bf16_f32 v223,v16,v17
v_max_f32 v23,0x00800000,v60
v_mfma_f32_16x16x32_bf16 a[40:43],a[172:175],v[108:111],a[40:43]
ds_read_b64_tr_b16 a[128:129],v1 offset:256
ds_read_b64_tr_b16 a[130:131],v1 offset:5376
v_mfma_f32_16x16x32_bf16 a[44:47],a[172:175],v[124:127],0
v_rcp_f32 v23,v23
v_mfma_f32_16x16x32_bf16 a[44:47],a[168:171],v[120:123],a[44:47]
ds_read_b64_tr_b16 a[132:133],v1 offset:10496
ds_read_b64_tr_b16 a[134:135],v1 offset:15616
v_mfma_f32_16x16x32_bf16 a[44:47],a[164:167],v[116:119],a[44:47]
buffer_load_dwordx2 v[26:27],v24,s[64:67],s94 offen sc1
v_mfma_f32_16x16x32_bf16 a[44:47],a[160:163],v[112:115],a[44:47]
v_accvgpr_read_b32 v228,a96
v_accvgpr_read_b32 v229,a97
v_mfma_f32_16x16x32_bf16 a[48:51],a[176:179],v[64:67],0
s_waitcnt lgkmcnt(4)
v_mul_f32 v228,v23,v228
v_mul_f32 v229,v23,v229
v_mfma_f32_16x16x32_bf16 a[48:51],a[180:183],v[68:71],a[48:51]
v_accvgpr_read_b32 v230,a98
v_accvgpr_read_b32 v231,a99
v_mfma_f32_16x16x32_bf16 a[48:51],a[184:187],v[72:75],a[48:51]
ds_read_b64_tr_b16 a[136:137],v1 offset:20736
ds_read_b64_tr_b16 a[138:139],v1 offset:25856
v_mfma_f32_16x16x32_bf16 a[48:51],a[188:191],v[76:79],a[48:51]
v_cvt_pk_bf16_f32 v228,v228,v229
v_mul_f32 v230,v23,v230
v_mfma_f32_16x16x32_bf16 a[52:55],a[188:191],v[92:95],0
v_mul_f32 v231,v23,v231
v_cvt_pk_bf16_f32 v229,v230,v231
v_mfma_f32_16x16x32_bf16 a[52:55],a[184:187],v[88:91],a[52:55]
ds_read_b64_tr_b16 a[140:141],v1 offset:30976
ds_read_b64_tr_b16 a[142:143],v1 offset:36096
v_mfma_f32_16x16x32_bf16 a[52:55],a[180:183],v[84:87],a[52:55]
v_accvgpr_read_b32 v230,a100
v_accvgpr_read_b32 v231,a101
v_mfma_f32_16x16x32_bf16 a[52:55],a[176:179],v[80:83],a[52:55]
v_accvgpr_read_b32 v12,a102
v_accvgpr_read_b32 v13,a103
v_mfma_f32_16x16x32_bf16 a[56:59],a[176:179],v[96:99],0
buffer_store_dwordx4 v[188:191],v6,s[36:39],s85 offen offset:3072 sc1
v_mfma_f32_16x16x32_bf16 a[56:59],a[180:183],v[100:103],a[56:59]
v_mul_f32 v230,v23,v230
v_mul_f32 v231,v23,v231
v_mfma_f32_16x16x32_bf16 a[56:59],a[184:187],v[104:107],a[56:59]
v_mul_f32 v12,v23,v12
v_mul_f32 v13,v23,v13
v_mfma_f32_16x16x32_bf16 a[56:59],a[188:191],v[108:111],a[56:59]
v_cvt_pk_bf16_f32 v230,v230,v231
v_cvt_pk_bf16_f32 v231,v12,v13
v_mfma_f32_16x16x32_bf16 a[60:63],a[188:191],v[124:127],0
v_accvgpr_read_b32 v236,a104
v_accvgpr_read_b32 v237,a105
v_mfma_f32_16x16x32_bf16 a[60:63],a[184:187],v[120:123],a[60:63]
v_mul_f32 v236,v23,v236
v_mul_f32 v237,v23,v237
v_mfma_f32_16x16x32_bf16 a[60:63],a[180:183],v[116:119],a[60:63]
v_accvgpr_read_b32 v238,a106
v_accvgpr_read_b32 v239,a107
v_mfma_f32_16x16x32_bf16 a[60:63],a[176:179],v[112:115],a[60:63]
s_waitcnt lgkmcnt(0)
v_cvt_pk_bf16_f32 v236,v236,v237
v_mul_f32 v238,v23,v238
v_mfma_f32_16x16x32_bf16 a[64:67],a[128:131],v[64:67],0
v_mul_f32 v239,v23,v239
v_cvt_pk_bf16_f32 v237,v238,v239
v_mfma_f32_16x16x32_bf16 a[64:67],a[132:135],v[68:71],a[64:67]
v_accvgpr_read_b32 v238,a108
v_accvgpr_read_b32 v239,a109
v_mfma_f32_16x16x32_bf16 a[64:67],a[136:139],v[72:75],a[64:67]
v_accvgpr_read_b32 v14,a110
v_accvgpr_read_b32 v15,a111
v_mfma_f32_16x16x32_bf16 a[64:67],a[140:143],v[76:79],a[64:67]
v_mul_f32 v238,v23,v238
v_mul_f32 v239,v23,v239
v_mfma_f32_16x16x32_bf16 a[68:71],a[140:143],v[92:95],0
ds_read_b64_tr_b16 a[144:145],v2 offset:288
ds_read_b64_tr_b16 a[146:147],v2 offset:5408
v_mfma_f32_16x16x32_bf16 a[68:71],a[136:139],v[88:91],a[68:71]
v_mul_f32 v14,v23,v14
v_mul_f32 v15,v23,v15
v_mfma_f32_16x16x32_bf16 a[68:71],a[132:135],v[84:87],a[68:71]
v_cvt_pk_bf16_f32 v238,v238,v239
v_cvt_pk_bf16_f32 v239,v14,v15
v_mfma_f32_16x16x32_bf16 a[68:71],a[128:131],v[80:83],a[68:71]
ds_read_b64_tr_b16 a[148:149],v2 offset:10528
ds_read_b64_tr_b16 a[150:151],v2 offset:15648
v_mfma_f32_16x16x32_bf16 a[72:75],a[128:131],v[96:99],0
ds_read_b64_tr_b16 a[152:153],v2 offset:20768
v_accvgpr_read_b32 v244,a112
v_mfma_f32_16x16x32_bf16 a[72:75],a[132:135],v[100:103],a[72:75]
ds_read_b64_tr_b16 a[154:155],v2 offset:25888
v_accvgpr_read_b32 v245,a113
v_mfma_f32_16x16x32_bf16 a[72:75],a[136:139],v[104:107],a[72:75]
ds_read_b64_tr_b16 a[156:157],v2 offset:31008
ds_read_b64_tr_b16 a[158:159],v2 offset:36128
)A"
R"A(v_mfma_f32_16x16x32_bf16 a[72:75],a[140:143],v[108:111],a[72:75]
v_mul_f32 v244,v23,v244
v_mul_f32 v245,v23,v245
v_mfma_f32_16x16x32_bf16 a[76:79],a[140:143],v[124:127],0
v_accvgpr_read_b32 v246,a114
v_accvgpr_read_b32 v247,a115
v_mfma_f32_16x16x32_bf16 a[76:79],a[136:139],v[120:123],a[76:79]
v_cvt_pk_bf16_f32 v244,v244,v245
v_mul_f32 v246,v23,v246
v_mfma_f32_16x16x32_bf16 a[76:79],a[132:135],v[116:119],a[76:79]
v_mul_f32 v247,v23,v247
v_cvt_pk_bf16_f32 v245,v246,v247
v_mfma_f32_16x16x32_bf16 a[76:79],a[128:131],v[112:115],a[76:79]
s_waitcnt lgkmcnt(4)
v_accvgpr_read_b32 v246,a116
v_accvgpr_read_b32 v247,a117
v_mfma_f32_16x16x32_bf16 a[80:83],a[144:147],v[64:67],0
v_accvgpr_read_b32 v16,a118
v_accvgpr_read_b32 v17,a119
v_mfma_f32_16x16x32_bf16 a[80:83],a[148:151],v[68:71],a[80:83]
s_waitcnt lgkmcnt(2)
v_mul_f32 v246,v23,v246
v_mul_f32 v247,v23,v247
v_mfma_f32_16x16x32_bf16 a[80:83],a[152:155],v[72:75],a[80:83]
s_waitcnt lgkmcnt(0)
v_mul_f32 v16,v23,v16
v_mul_f32 v17,v23,v17
v_mfma_f32_16x16x32_bf16 a[80:83],a[156:159],v[76:79],a[80:83]
v_cvt_pk_bf16_f32 v246,v246,v247
v_cvt_pk_bf16_f32 v247,v16,v17
v_mfma_f32_16x16x32_bf16 a[84:87],a[156:159],v[92:95],0
v_add_u32 v1,40960,v1
v_accvgpr_read_b32 v252,a120
v_mfma_f32_16x16x32_bf16 a[84:87],a[152:155],v[88:91],a[84:87]
ds_read_b64_tr_b16 a[160:161],v1
ds_read_b64_tr_b16 a[162:163],v1 offset:5120
v_mfma_f32_16x16x32_bf16 a[84:87],a[148:151],v[84:87],a[84:87]
ds_read_b64_tr_b16 a[164:165],v1 offset:10240
v_accvgpr_read_b32 v253,a121
v_mfma_f32_16x16x32_bf16 a[84:87],a[144:147],v[80:83],a[84:87]
ds_read_b64_tr_b16 a[166:167],v1 offset:15360
v_cmp_nlt_f32 s[56:57],v48,s63
v_mfma_f32_16x16x32_bf16 a[88:91],a[144:147],v[96:99],0
ds_read_b64_tr_b16 a[168:169],v1 offset:20480
v_mul_f32 v252,v23,v252
v_mfma_f32_16x16x32_bf16 a[88:91],a[148:151],v[100:103],a[88:91]
ds_read_b64_tr_b16 a[170:171],v1 offset:25600
ds_read_b64_tr_b16 a[172:173],v1 offset:30720
v_mfma_f32_16x16x32_bf16 a[88:91],a[152:155],v[104:107],a[88:91]
ds_read_b64_tr_b16 a[174:175],v1 offset:35840
v_mul_f32 v253,v23,v253
v_mfma_f32_16x16x32_bf16 a[88:91],a[156:159],v[108:111],a[88:91]
v_accvgpr_read_b32 v254,a122
v_accvgpr_read_b32 v255,a123
v_mfma_f32_16x16x32_bf16 a[92:95],a[156:159],v[124:127],0
v_cvt_pk_bf16_f32 v252,v252,v253
v_mul_f32 v254,v23,v254
v_mul_f32 v255,v23,v255
v_cmp_nlt_f32 s[58:59],v52,s63
s_nop 1
s_or_b64 s[56:57],s[56:57],s[58:59]
v_cvt_pk_bf16_f32 v253,v254,v255
v_mfma_f32_16x16x32_bf16 a[92:95],a[152:155],v[120:123],a[92:95]
v_accvgpr_read_b32 v254,a124
v_accvgpr_read_b32 v255,a125
v_cmp_nlt_f32 s[58:59],v56,s63
s_nop 1
s_or_b64 s[56:57],s[56:57],s[58:59]
v_accvgpr_read_b32 v12,a126
v_accvgpr_read_b32 v13,a127
v_mul_f32 v254,v23,v254
v_mul_f32 v255,v23,v255
v_mfma_f32_16x16x32_bf16 a[92:95],a[148:151],v[116:119],a[92:95]
v_mul_f32 v12,v23,v12
v_mul_f32 v13,v23,v13
v_cvt_pk_bf16_f32 v254,v254,v255
v_cmp_nlt_f32 s[58:59],v60,s63
s_nop 1
s_or_b64 s[56:57],s[56:57],s[58:59]
v_cvt_pk_bf16_f32 v255,v12,v13
s_waitcnt vmcnt(1)
v_cmp_ne_u32 s[90:91],s68,v26
v_cmp_ne_u32 s[92:93],s69,v27
s_nop 0
s_or_b64 s[90:91],s[90:91],s[92:93]
s_cbranch_scc1 L_fr13_%=
L_fo13_%=:
v_mfma_f32_16x16x32_bf16 a[92:95],a[144:147],v[112:115],a[92:95]
s_cmp_lg_u64 s[56:57],0
s_cbranch_scc1 L_slow1_%=
L_sret1_%=:
v_add_u32 v2,40960,v2
s_waitcnt lgkmcnt(4)
v_mfma_f32_16x16x32_bf16 a[192:195],a[160:163],v[132:135],a[192:195]
buffer_store_dwordx4 v[196:199],v6,s[36:39],s86 offen sc1
v_mfma_f32_16x16x32_bf16 a[192:195],a[164:167],v[140:143],a[192:195]
s_cmp_ge_u32 0,s71
s_cselect_b32 s76,2,0
s_lshl_b32 s76,s76,16
s_waitcnt lgkmcnt(1)
v_mfma_f32_16x16x32_bf16 a[192:195],a[168:171],v[148:151],a[192:195]
s_add_u32 s77,s76,4096
s_add_u32 s78,s76,8192
s_add_u32 s79,s76,12288
s_max_i32 s76,s76,s18
s_waitcnt lgkmcnt(0)
v_mfma_f32_16x16x32_bf16 a[192:195],a[172:175],v[156:159],a[192:195]
s_max_i32 s77,s77,s19
ds_read_b64_tr_b16 a[176:177],v2 offset:32
ds_read_b64_tr_b16 a[178:179],v2 offset:5152
v_mfma_f32_16x16x32_bf16 a[196:199],a[172:175],v[188:191],a[196:199]
s_max_i32 s78,s78,s100
buffer_load_dwordx4 v[128:131],v6,s[28:31],s76 offen sc1
v_mfma_f32_16x16x32_bf16 a[196:199],a[168:171],v[180:183],a[196:199]
s_max_i32 s79,s79,s101
ds_read_b64_tr_b16 a[180:181],v2 offset:10272
ds_read_b64_tr_b16 a[182:183],v2 offset:15392
v_mfma_f32_16x16x32_bf16 a[196:199],a[164:167],v[172:175],a[196:199]
s_cmp_ge_u32 2,s71
s_cselect_b32 s80,6,4
ds_read_b64_tr_b16 a[184:185],v2 offset:20512
v_mfma_f32_16x16x32_bf16 a[196:199],a[160:163],v[164:167],a[196:199]
s_mul_i32 s80,s80,163840
s_add_u32 s80,s80,s81
ds_read_b64_tr_b16 a[186:187],v2 offset:25632
v_mfma_f32_16x16x32_bf16 a[200:203],a[160:163],v[196:199],a[200:203]
s_add_u32 s83,s80,20480
buffer_store_dwordx4 v[204:207],v6,s[36:39],s86 offen offset:1024 sc1
v_mfma_f32_16x16x32_bf16 a[200:203],a[164:167],v[204:207],a[200:203]
ds_read_b64_tr_b16 a[188:189],v2 offset:30752
ds_read_b64_tr_b16 a[190:191],v2 offset:35872
v_mfma_f32_16x16x32_bf16 a[200:203],a[168:171],v[212:215],a[200:203]
ds_read_b64_tr_b16 a[128:129],v1 offset:64
v_mfma_f32_16x16x32_bf16 a[200:203],a[172:175],v[220:223],a[200:203]
ds_read_b64_tr_b16 a[130:131],v1 offset:5184
v_mfma_f32_16x16x32_bf16 a[204:207],a[172:175],v[252:255],a[204:207]
buffer_load_dwordx4 v[160:163],v6,s[28:31],s76 offen offset:1024 sc1
v_mfma_f32_16x16x32_bf16 a[204:207],a[168:171],v[244:247],a[204:207]
ds_read_b64_tr_b16 a[132:133],v1 offset:10304
ds_read_b64_tr_b16 a[134:135],v1 offset:15424
v_mfma_f32_16x16x32_bf16 a[204:207],a[164:167],v[236:239],a[204:207]
ds_read_b64_tr_b16 a[136:137],v1 offset:20544
v_mfma_f32_16x16x32_bf16 a[204:207],a[160:163],v[228:231],a[204:207]
s_barrier
buffer_load_dwordx4 v[192:195],v6,s[28:31],s76 offen offset:2048 sc1
s_waitcnt lgkmcnt(7)
v_mfma_f32_16x16x32_bf16 a[208:211],a[176:179],v[132:135],a[208:211]
ds_read_b64_tr_b16 a[138:139],v1 offset:25664
ds_read_b64_tr_b16 a[140:141],v1 offset:30784
v_mfma_f32_16x16x32_bf16 a[208:211],a[180:183],v[140:143],a[208:211]
ds_read_b64_tr_b16 a[142:143],v1 offset:35904
v_mfma_f32_16x16x32_bf16 a[208:211],a[184:187],v[148:151],a[208:211]
s_add_u32 m0,s82,0
ds_read_b64_tr_b16 a[144:145],v2 offset:96
s_waitcnt lgkmcnt(8)
v_mfma_f32_16x16x32_bf16 a[208:211],a[188:191],v[156:159],a[208:211]
buffer_store_dwordx4 v[212:215],v6,s[36:39],s86 offen offset:2048 sc1
v_mfma_f32_16x16x32_bf16 a[212:215],a[188:191],v[188:191],a[212:215]
ds_read_b64_tr_b16 a[146:147],v2 offset:5216
ds_read_b64_tr_b16 a[148:149],v2 offset:10336
v_mfma_f32_16x16x32_bf16 a[212:215],a[184:187],v[180:183],a[212:215]
ds_read_b64_tr_b16 a[150:151],v2 offset:15456
v_mfma_f32_16x16x32_bf16 a[212:215],a[180:183],v[172:175],a[212:215]
ds_read_b64_tr_b16 a[152:153],v2 offset:20576
v_mfma_f32_16x16x32_bf16 a[212:215],a[176:179],v[164:167],a[212:215]
buffer_load_dwordx4 v[224:227],v6,s[28:31],s76 offen offset:3072 sc1
v_mfma_f32_16x16x32_bf16 a[216:219],a[176:179],v[196:199],a[216:219]
ds_read_b64_tr_b16 a[154:155],v2 offset:25696
ds_read_b64_tr_b16 a[156:157],v2 offset:30816
v_mfma_f32_16x16x32_bf16 a[216:219],a[180:183],v[204:207],a[216:219]
ds_read_b64_tr_b16 a[158:159],v2 offset:35936
v_mfma_f32_16x16x32_bf16 a[216:219],a[184:187],v[212:215],a[216:219]
ds_read_b64_tr_b16 a[160:161],v1 offset:128
v_mfma_f32_16x16x32_bf16 a[216:219],a[188:191],v[220:223],a[216:219]
buffer_load_dwordx4 v7,s[72:75],s80 offen lds
v_mfma_f32_16x16x32_bf16 a[220:223],a[188:191],v[252:255],a[220:223]
ds_read_b64_tr_b16 a[162:163],v1 offset:5248
ds_read_b64_tr_b16 a[164:165],v1 offset:10368
v_mfma_f32_16x16x32_bf16 a[220:223],a[184:187],v[244:247],a[220:223]
ds_read_b64_tr_b16 a[166:167],v1 offset:15488
v_mfma_f32_16x16x32_bf16 a[220:223],a[180:183],v[236:239],a[220:223]
ds_read_b64_tr_b16 a[168:169],v1 offset:20608
v_mfma_f32_16x16x32_bf16 a[220:223],a[176:179],v[228:231],a[220:223]
s_waitcnt lgkmcnt(8)
buffer_load_dwordx4 v[136:139],v6,s[28:31],s77 offen sc1
v_mfma_f32_16x16x32_bf16 a[224:227],a[128:131],v[132:135],a[224:227]
ds_read_b64_tr_b16 a[170:171],v1 offset:25728
ds_read_b64_tr_b16 a[172:173],v1 offset:30848
v_mfma_f32_16x16x32_bf16 a[224:227],a[132:135],v[140:143],a[224:227]
ds_read_b64_tr_b16 a[174:175],v1 offset:35968
v_mfma_f32_16x16x32_bf16 a[224:227],a[136:139],v[148:151],a[224:227]
ds_read_b64_tr_b16 a[176:177],v2 offset:160
v_mfma_f32_16x16x32_bf16 a[224:227],a[140:143],v[156:159],a[224:227]
buffer_load_dwordx4 v[168:171],v6,s[28:31],s77 offen offset:1024 sc1
v_mfma_f32_16x16x32_bf16 a[228:231],a[140:143],v[188:191],a[228:231]
ds_read_b64_tr_b16 a[178:179],v2 offset:5280
ds_read_b64_tr_b16 a[180:181],v2 offset:10400
v_mfma_f32_16x16x32_bf16 a[228:231],a[136:139],v[180:183],a[228:231]
ds_read_b64_tr_b16 a[182:183],v2 offset:15520
v_mfma_f32_16x16x32_bf16 a[228:231],a[132:135],v[172:175],a[228:231]
ds_read_b64_tr_b16 a[184:185],v2 offset:20640
v_mfma_f32_16x16x32_bf16 a[228:231],a[128:131],v[164:167],a[228:231]
buffer_load_dwordx4 v8,s[72:75],s80 offen offset:1024 lds
v_mfma_f32_16x16x32_bf16 a[232:235],a[128:131],v[196:199],a[232:235]
ds_read_b64_tr_b16 a[186:187],v2 offset:25760
ds_read_b64_tr_b16 a[188:189],v2 offset:30880
v_mfma_f32_16x16x32_bf16 a[232:235],a[132:135],v[204:207],a[232:235]
ds_read_b64_tr_b16 a[190:191],v2 offset:36000
v_mfma_f32_16x16x32_bf16 a[232:235],a[136:139],v[212:215],a[232:235]
v_mfma_f32_16x16x32_bf16 a[232:235],a[140:143],v[220:223],a[232:235]
buffer_store_dwordx4 v[220:223],v6,s[36:39],s86 offen offset:3072 sc1
v_mfma_f32_16x16x32_bf16 a[236:239],a[140:143],v[252:255],a[236:239]
ds_read_b64_tr_b16 a[140:141],v1 offset:30912
ds_read_b64_tr_b16 a[142:143],v1 offset:36032
v_mfma_f32_16x16x32_bf16 a[236:239],a[136:139],v[244:247],a[236:239]
ds_read_b64_tr_b16 a[136:137],v1 offset:20672
v_mfma_f32_16x16x32_bf16 a[236:239],a[132:135],v[236:239],a[236:239]
ds_read_b64_tr_b16 a[132:133],v1 offset:10432
v_mfma_f32_16x16x32_bf16 a[236:239],a[128:131],v[228:231],a[236:239]
buffer_load_dwordx4 v[200:203],v6,s[28:31],s77 offen offset:2048 sc1
v_mfma_f32_16x16x32_bf16 a[240:243],a[144:147],v[132:135],a[240:243]
ds_read_b64_tr_b16 a[128:129],v1 offset:192
ds_read_b64_tr_b16 a[130:131],v1 offset:5312
v_mfma_f32_16x16x32_bf16 a[240:243],a[148:151],v[140:143],a[240:243]
s_waitcnt lgkmcnt(6)
ds_read_b64_tr_b16 a[134:135],v1 offset:15552
v_mfma_f32_16x16x32_bf16 a[240:243],a[152:155],v[148:151],a[240:243]
ds_read_b64_tr_b16 a[138:139],v1 offset:25792
v_mfma_f32_16x16x32_bf16 a[240:243],a[156:159],v[156:159],a[240:243]
buffer_load_dwordx4 v[232:235],v6,s[28:31],s77 offen offset:3072 sc1
v_mfma_f32_16x16x32_bf16 a[244:247],a[156:159],v[188:191],a[244:247]
v_mfma_f32_16x16x32_bf16 a[244:247],a[152:155],v[180:183],a[244:247]
v_mfma_f32_16x16x32_bf16 a[244:247],a[148:151],v[172:175],a[244:247]
v_mfma_f32_16x16x32_bf16 a[244:247],a[144:147],v[164:167],a[244:247]
buffer_load_dwordx4 v9,s[72:75],s80 offen offset:2048 lds
v_mfma_f32_16x16x32_bf16 a[248:251],a[144:147],v[196:199],a[248:251]
v_mfma_f32_16x16x32_bf16 a[248:251],a[148:151],v[204:207],a[248:251]
v_mfma_f32_16x16x32_bf16 a[248:251],a[152:155],v[212:215],a[248:251]
v_mfma_f32_16x16x32_bf16 a[248:251],a[156:159],v[220:223],a[248:251]
buffer_load_dwordx4 v[144:147],v6,s[28:31],s78 offen sc1
v_mfma_f32_16x16x32_bf16 a[252:255],a[156:159],v[252:255],a[252:255]
ds_read_b64_tr_b16 a[156:157],v2 offset:30944
ds_read_b64_tr_b16 a[158:159],v2 offset:36064
v_mfma_f32_16x16x32_bf16 a[252:255],a[152:155],v[244:247],a[252:255]
ds_read_b64_tr_b16 a[152:153],v2 offset:20704
v_mfma_f32_16x16x32_bf16 a[252:255],a[148:151],v[236:239],a[252:255]
ds_read_b64_tr_b16 a[148:149],v2 offset:10464
v_mfma_f32_16x16x32_bf16 a[252:255],a[144:147],v[228:231],a[252:255]
buffer_store_dwordx4 v[228:231],v6,s[36:39],s87 offen sc1
v_mfma_f32_16x16x32_bf16 a[0:3],a[160:163],v[132:135],a[0:3]
ds_read_b64_tr_b16 a[144:145],v2 offset:224
ds_read_b64_tr_b16 a[146:147],v2 offset:5344
v_mfma_f32_16x16x32_bf16 a[0:3],a[164:167],v[140:143],a[0:3]
ds_read_b64_tr_b16 a[150:151],v2 offset:15584
v_mfma_f32_16x16x32_bf16 a[0:3],a[168:171],v[148:151],a[0:3]
ds_read_b64_tr_b16 a[154:155],v2 offset:25824
)A"
R"A(v_mfma_f32_16x16x32_bf16 a[0:3],a[172:175],v[156:159],a[0:3]
buffer_load_dwordx4 v[176:179],v6,s[28:31],s78 offen offset:1024 sc1
v_mfma_f32_16x16x32_bf16 a[4:7],a[172:175],v[188:191],a[4:7]
v_mfma_f32_16x16x32_bf16 a[4:7],a[168:171],v[180:183],a[4:7]
v_mfma_f32_16x16x32_bf16 a[4:7],a[164:167],v[172:175],a[4:7]
v_mfma_f32_16x16x32_bf16 a[4:7],a[160:163],v[164:167],a[4:7]
buffer_load_dwordx4 v10,s[72:75],s80 offen offset:3072 lds
v_mfma_f32_16x16x32_bf16 a[8:11],a[160:163],v[196:199],a[8:11]
v_mfma_f32_16x16x32_bf16 a[8:11],a[164:167],v[204:207],a[8:11]
v_mfma_f32_16x16x32_bf16 a[8:11],a[168:171],v[212:215],a[8:11]
v_mfma_f32_16x16x32_bf16 a[8:11],a[172:175],v[220:223],a[8:11]
buffer_load_dwordx4 v[208:211],v6,s[28:31],s78 offen offset:2048 sc1
v_mfma_f32_16x16x32_bf16 a[12:15],a[172:175],v[252:255],a[12:15]
ds_read_b64_tr_b16 a[172:173],v1 offset:30976
ds_read_b64_tr_b16 a[174:175],v1 offset:36096
v_mfma_f32_16x16x32_bf16 a[12:15],a[168:171],v[244:247],a[12:15]
ds_read_b64_tr_b16 a[168:169],v1 offset:20736
v_mfma_f32_16x16x32_bf16 a[12:15],a[164:167],v[236:239],a[12:15]
s_add_u32 m0,s82,4096
ds_read_b64_tr_b16 a[164:165],v1 offset:10496
v_mfma_f32_16x16x32_bf16 a[12:15],a[160:163],v[228:231],a[12:15]
buffer_load_dwordx4 v[240:243],v6,s[28:31],s78 offen offset:3072 sc1
v_mfma_f32_16x16x32_bf16 a[16:19],a[176:179],v[132:135],a[16:19]
ds_read_b64_tr_b16 a[160:161],v1 offset:256
ds_read_b64_tr_b16 a[162:163],v1 offset:5376
v_mfma_f32_16x16x32_bf16 a[16:19],a[180:183],v[140:143],a[16:19]
ds_read_b64_tr_b16 a[166:167],v1 offset:15616
v_mfma_f32_16x16x32_bf16 a[16:19],a[184:187],v[148:151],a[16:19]
ds_read_b64_tr_b16 a[170:171],v1 offset:25856
v_add_u32 v1,40960,v1
v_mfma_f32_16x16x32_bf16 a[16:19],a[188:191],v[156:159],a[16:19]
buffer_store_dwordx4 v[236:239],v6,s[36:39],s87 offen offset:1024 sc1
v_mfma_f32_16x16x32_bf16 a[20:23],a[188:191],v[188:191],a[20:23]
ds_read_b64_tr_b16 a[96:97],v1 offset:128
ds_read_b64_tr_b16 a[98:99],v1 offset:5248
v_mfma_f32_16x16x32_bf16 a[20:23],a[184:187],v[180:183],a[20:23]
ds_read_b64_tr_b16 a[100:101],v1 offset:10368
v_mfma_f32_16x16x32_bf16 a[20:23],a[180:183],v[172:175],a[20:23]
ds_read_b64_tr_b16 a[102:103],v1 offset:15488
v_mfma_f32_16x16x32_bf16 a[20:23],a[176:179],v[164:167],a[20:23]
buffer_load_dwordx4 v11,s[72:75],s80 offen lds
v_mfma_f32_16x16x32_bf16 a[24:27],a[176:179],v[196:199],a[24:27]
ds_read_b64_tr_b16 a[104:105],v1 offset:20608
ds_read_b64_tr_b16 a[106:107],v1 offset:25728
v_mfma_f32_16x16x32_bf16 a[24:27],a[180:183],v[204:207],a[24:27]
ds_read_b64_tr_b16 a[108:109],v1 offset:30848
v_mfma_f32_16x16x32_bf16 a[24:27],a[184:187],v[212:215],a[24:27]
ds_read_b64_tr_b16 a[110:111],v1 offset:35968
v_mfma_f32_16x16x32_bf16 a[24:27],a[188:191],v[220:223],a[24:27]
buffer_load_dwordx4 v[152:155],v6,s[28:31],s79 offen sc1
v_mfma_f32_16x16x32_bf16 a[28:31],a[188:191],v[252:255],a[28:31]
ds_read_b64_tr_b16 a[188:189],v2 offset:31008
ds_read_b64_tr_b16 a[190:191],v2 offset:36128
v_mfma_f32_16x16x32_bf16 a[28:31],a[184:187],v[244:247],a[28:31]
s_add_u32 m0,s82,5120
ds_read_b64_tr_b16 a[184:185],v2 offset:20768
v_mfma_f32_16x16x32_bf16 a[28:31],a[180:183],v[236:239],a[28:31]
ds_read_b64_tr_b16 a[180:181],v2 offset:10528
v_mfma_f32_16x16x32_bf16 a[28:31],a[176:179],v[228:231],a[28:31]
s_waitcnt lgkmcnt(8)
buffer_load_dwordx4 v[184:187],v6,s[28:31],s79 offen offset:1024 sc1
v_mfma_f32_16x16x32_bf16 a[32:35],a[128:131],v[132:135],a[32:35]
ds_read_b64_tr_b16 a[176:177],v2 offset:288
ds_read_b64_tr_b16 a[178:179],v2 offset:5408
v_mfma_f32_16x16x32_bf16 a[32:35],a[132:135],v[140:143],a[32:35]
ds_read_b64_tr_b16 a[182:183],v2 offset:15648
v_mfma_f32_16x16x32_bf16 a[32:35],a[136:139],v[148:151],a[32:35]
ds_read_b64_tr_b16 a[186:187],v2 offset:25888
v_add_u32 v2,40960,v2
v_mfma_f32_16x16x32_bf16 a[32:35],a[140:143],v[156:159],a[32:35]
buffer_load_dwordx4 v7,s[72:75],s83 offen lds
v_mfma_f32_16x16x32_bf16 a[36:39],a[140:143],v[188:191],a[36:39]
ds_read_b64_tr_b16 a[112:113],v2 offset:160
ds_read_b64_tr_b16 a[114:115],v2 offset:5280
v_mfma_f32_16x16x32_bf16 a[36:39],a[136:139],v[180:183],a[36:39]
ds_read_b64_tr_b16 a[116:117],v2 offset:10400
v_mfma_f32_16x16x32_bf16 a[36:39],a[132:135],v[172:175],a[36:39]
ds_read_b64_tr_b16 a[118:119],v2 offset:15520
v_mfma_f32_16x16x32_bf16 a[36:39],a[128:131],v[164:167],a[36:39]
buffer_load_dwordx4 v[216:219],v6,s[28:31],s79 offen offset:2048 sc1
v_mfma_f32_16x16x32_bf16 a[40:43],a[128:131],v[196:199],a[40:43]
ds_read_b64_tr_b16 a[120:121],v2 offset:20640
ds_read_b64_tr_b16 a[122:123],v2 offset:25760
v_mfma_f32_16x16x32_bf16 a[40:43],a[132:135],v[204:207],a[40:43]
ds_read_b64_tr_b16 a[124:125],v2 offset:30880
v_mfma_f32_16x16x32_bf16 a[40:43],a[136:139],v[212:215],a[40:43]
ds_read_b64_tr_b16 a[126:127],v2 offset:36000
v_mfma_f32_16x16x32_bf16 a[40:43],a[140:143],v[220:223],a[40:43]
buffer_load_dwordx4 v[248:251],v6,s[28:31],s79 offen offset:3072 sc1
v_mfma_f32_16x16x32_bf16 a[44:47],a[140:143],v[252:255],a[44:47]
ds_read_b64_tr_b16 a[140:141],v1 offset:30720
ds_read_b64_tr_b16 a[142:143],v1 offset:35840
v_mfma_f32_16x16x32_bf16 a[44:47],a[136:139],v[244:247],a[44:47]
ds_read_b64_tr_b16 a[136:137],v1 offset:20480
v_mfma_f32_16x16x32_bf16 a[44:47],a[132:135],v[236:239],a[44:47]
ds_read_b64_tr_b16 a[132:133],v1 offset:10240
v_mfma_f32_16x16x32_bf16 a[44:47],a[128:131],v[228:231],a[44:47]
buffer_store_dwordx4 v[244:247],v6,s[36:39],s87 offen offset:2048 sc1
v_mfma_f32_16x16x32_bf16 a[48:51],a[144:147],v[132:135],a[48:51]
ds_read_b64_tr_b16 a[128:129],v1
ds_read_b64_tr_b16 a[130:131],v1 offset:5120
v_mfma_f32_16x16x32_bf16 a[48:51],a[148:151],v[140:143],a[48:51]
ds_read_b64_tr_b16 a[134:135],v1 offset:15360
v_mfma_f32_16x16x32_bf16 a[48:51],a[152:155],v[148:151],a[48:51]
ds_read_b64_tr_b16 a[138:139],v1 offset:25600
v_mfma_f32_16x16x32_bf16 a[48:51],a[156:159],v[156:159],a[48:51]
buffer_load_dwordx4 v8,s[72:75],s83 offen offset:1024 lds
v_mfma_f32_16x16x32_bf16 a[52:55],a[156:159],v[188:191],a[52:55]
s_cmp_ge_u32 1,s71
s_cselect_b32 s76,4,2
s_lshl_b32 s76,s76,16
v_mfma_f32_16x16x32_bf16 a[52:55],a[152:155],v[180:183],a[52:55]
s_add_u32 s77,s76,4096
s_add_u32 s78,s76,8192
s_add_u32 s79,s76,12288
v_mfma_f32_16x16x32_bf16 a[52:55],a[148:151],v[172:175],a[52:55]
s_max_i32 s76,s76,s18
s_max_i32 s77,s77,s19
s_max_i32 s78,s78,s100
v_mfma_f32_16x16x32_bf16 a[52:55],a[144:147],v[164:167],a[52:55]
s_max_i32 s79,s79,s101
buffer_store_dwordx4 v[252:255],v6,s[36:39],s87 offen offset:3072 sc1
v_mfma_f32_16x16x32_bf16 a[56:59],a[144:147],v[196:199],a[56:59]
v_mfma_f32_16x16x32_bf16 a[56:59],a[148:151],v[204:207],a[56:59]
v_mfma_f32_16x16x32_bf16 a[56:59],a[152:155],v[212:215],a[56:59]
v_mfma_f32_16x16x32_bf16 a[56:59],a[156:159],v[220:223],a[56:59]
v_mfma_f32_16x16x32_bf16 a[60:63],a[156:159],v[252:255],a[60:63]
ds_read_b64_tr_b16 a[156:157],v2 offset:30752
ds_read_b64_tr_b16 a[158:159],v2 offset:35872
v_mfma_f32_16x16x32_bf16 a[60:63],a[152:155],v[244:247],a[60:63]
ds_read_b64_tr_b16 a[152:153],v2 offset:20512
v_mfma_f32_16x16x32_bf16 a[60:63],a[148:151],v[236:239],a[60:63]
buffer_load_dwordx4 v9,s[72:75],s83 offen offset:2048 lds
v_mfma_f32_16x16x32_bf16 a[60:63],a[144:147],v[228:231],a[60:63]
ds_read_b64_tr_b16 a[144:145],v2 offset:32
ds_read_b64_tr_b16 a[146:147],v2 offset:5152
v_mfma_f32_16x16x32_bf16 a[64:67],a[160:163],v[132:135],a[64:67]
ds_read_b64_tr_b16 a[148:149],v2 offset:10272
v_mfma_f32_16x16x32_bf16 a[64:67],a[164:167],v[140:143],a[64:67]
ds_read_b64_tr_b16 a[150:151],v2 offset:15392
v_mfma_f32_16x16x32_bf16 a[64:67],a[168:171],v[148:151],a[64:67]
ds_read_b64_tr_b16 a[154:155],v2 offset:25632
v_mfma_f32_16x16x32_bf16 a[64:67],a[172:175],v[156:159],a[64:67]
v_mfma_f32_16x16x32_bf16 a[68:71],a[172:175],v[188:191],a[68:71]
v_mfma_f32_16x16x32_bf16 a[68:71],a[168:171],v[180:183],a[68:71]
v_mfma_f32_16x16x32_bf16 a[68:71],a[164:167],v[172:175],a[68:71]
v_mfma_f32_16x16x32_bf16 a[68:71],a[160:163],v[164:167],a[68:71]
v_mfma_f32_16x16x32_bf16 a[72:75],a[160:163],v[196:199],a[72:75]
v_mfma_f32_16x16x32_bf16 a[72:75],a[164:167],v[204:207],a[72:75]
v_mfma_f32_16x16x32_bf16 a[72:75],a[168:171],v[212:215],a[72:75]
v_mfma_f32_16x16x32_bf16 a[72:75],a[172:175],v[220:223],a[72:75]
buffer_load_dwordx4 v[64:67],v6,s[28:31],s76 offen sc1
v_mfma_f32_16x16x32_bf16 a[76:79],a[172:175],v[252:255],a[76:79]
ds_read_b64_tr_b16 a[172:173],v1 offset:30784
ds_read_b64_tr_b16 a[174:175],v1 offset:35904
v_mfma_f32_16x16x32_bf16 a[76:79],a[168:171],v[244:247],a[76:79]
ds_read_b64_tr_b16 a[168:169],v1 offset:20544
v_mfma_f32_16x16x32_bf16 a[76:79],a[164:167],v[236:239],a[76:79]
ds_read_b64_tr_b16 a[164:165],v1 offset:10304
v_mfma_f32_16x16x32_bf16 a[76:79],a[160:163],v[228:231],a[76:79]
s_waitcnt lgkmcnt(4)
buffer_load_dwordx4 v10,s[72:75],s83 offen offset:3072 lds
v_mfma_f32_16x16x32_bf16 a[80:83],a[176:179],v[132:135],a[80:83]
ds_read_b64_tr_b16 a[160:161],v1 offset:64
ds_read_b64_tr_b16 a[162:163],v1 offset:5184
v_mfma_f32_16x16x32_bf16 a[80:83],a[180:183],v[140:143],a[80:83]
ds_read_b64_tr_b16 a[166:167],v1 offset:15424
v_mfma_f32_16x16x32_bf16 a[80:83],a[184:187],v[148:151],a[80:83]
ds_read_b64_tr_b16 a[170:171],v1 offset:25664
v_mfma_f32_16x16x32_bf16 a[80:83],a[188:191],v[156:159],a[80:83]
buffer_load_dwordx4 v[68:71],v6,s[28:31],s76 offen offset:1024 sc1
v_mfma_f32_16x16x32_bf16 a[84:87],a[188:191],v[188:191],a[84:87]
v_mfma_f32_16x16x32_bf16 a[84:87],a[184:187],v[180:183],a[84:87]
v_mfma_f32_16x16x32_bf16 a[84:87],a[180:183],v[172:175],a[84:87]
v_mfma_f32_16x16x32_bf16 a[84:87],a[176:179],v[164:167],a[84:87]
v_mfma_f32_16x16x32_bf16 a[88:91],a[176:179],v[196:199],a[88:91]
v_mfma_f32_16x16x32_bf16 a[88:91],a[180:183],v[204:207],a[88:91]
s_add_u32 m0,s82,9216
v_mfma_f32_16x16x32_bf16 a[88:91],a[184:187],v[212:215],a[88:91]
v_mfma_f32_16x16x32_bf16 a[88:91],a[188:191],v[220:223],a[88:91]
buffer_load_dwordx4 v[72:75],v6,s[28:31],s76 offen offset:2048 sc1
v_mfma_f32_16x16x32_bf16 a[92:95],a[188:191],v[252:255],a[92:95]
ds_read_b64_tr_b16 a[188:189],v2 offset:30816
ds_read_b64_tr_b16 a[190:191],v2 offset:35936
v_mfma_f32_16x16x32_bf16 a[92:95],a[184:187],v[244:247],a[92:95]
ds_read_b64_tr_b16 a[184:185],v2 offset:20576
v_mfma_f32_16x16x32_bf16 a[92:95],a[180:183],v[236:239],a[92:95]
s_cmp_ge_u32 0,s71
s_cselect_b32 s80,3,1
s_mul_i32 s80,s80,163840
v_mfma_f32_16x16x32_bf16 a[92:95],a[176:179],v[228:231],a[92:95]
s_add_u32 s80,s80,s81
buffer_load_dwordx4 v11,s[72:75],s83 offen lds
s_waitcnt vmcnt(14)
v_mfma_f32_16x16x32_bf16 a[192:195],a[128:131],v[128:131],a[192:195]
s_add_u32 s83,s80,20480
ds_read_b64_tr_b16 a[176:177],v2 offset:96
ds_read_b64_tr_b16 a[178:179],v2 offset:5216
v_mfma_f32_16x16x32_bf16 a[192:195],a[132:135],v[160:163],a[192:195]
ds_read_b64_tr_b16 a[180:181],v2 offset:10336
v_mfma_f32_16x16x32_bf16 a[192:195],a[136:139],v[192:195],a[192:195]
ds_read_b64_tr_b16 a[182:183],v2 offset:15456
v_mfma_f32_16x16x32_bf16 a[192:195],a[140:143],v[224:227],a[192:195]
buffer_load_dwordx4 v[76:79],v6,s[28:31],s76 offen offset:3072 sc1
v_mfma_f32_16x16x32_bf16 a[196:199],a[140:143],v[232:235],a[196:199]
ds_read_b64_tr_b16 a[186:187],v2 offset:25696
v_mfma_f32_16x16x32_bf16 a[196:199],a[136:139],v[200:203],a[196:199]
v_mfma_f32_16x16x32_bf16 a[196:199],a[132:135],v[168:171],a[196:199]
v_mfma_f32_16x16x32_bf16 a[196:199],a[128:131],v[136:139],a[196:199]
v_mfma_f32_16x16x32_bf16 a[200:203],a[128:131],v[144:147],a[200:203]
v_mfma_f32_16x16x32_bf16 a[200:203],a[132:135],v[176:179],a[200:203]
s_lshl_b32 s95,s42,2
s_add_u32 s95,s95,s40
s_lshl_b32 s95,s95,4
v_mfma_f32_16x16x32_bf16 a[200:203],a[136:139],v[208:211],a[200:203]
v_mov_b32 v18,s68
v_mov_b32 v19,s69
v_mfma_f32_16x16x32_bf16 a[200:203],a[140:143],v[240:243],a[200:203]
s_waitcnt vmcnt(10)
buffer_load_dwordx4 v[80:83],v6,s[28:31],s77 offen sc1
v_mfma_f32_16x16x32_bf16 a[204:207],a[140:143],v[248:251],a[204:207]
ds_read_b64_tr_b16 a[140:141],v1 offset:30912
ds_read_b64_tr_b16 a[142:143],v1 offset:36032
v_mfma_f32_16x16x32_bf16 a[204:207],a[136:139],v[216:219],a[204:207]
ds_read_b64_tr_b16 a[136:137],v1 offset:20672
)A"
R"A(v_mfma_f32_16x16x32_bf16 a[204:207],a[132:135],v[184:187],a[204:207]
ds_read_b64_tr_b16 a[132:133],v1 offset:10432
v_mfma_f32_16x16x32_bf16 a[204:207],a[128:131],v[152:155],a[204:207]
s_barrier
ds_read_b64_tr_b16 a[128:129],v1 offset:192
ds_read_b64_tr_b16 a[130:131],v1 offset:5312
v_mfma_f32_16x16x32_bf16 a[208:211],a[144:147],v[128:131],a[208:211]
s_waitcnt vmcnt(8)
s_mov_b64 exec,1
buffer_store_dwordx2 v[18:19],v6,s[64:67],s95 offen offset:1024 sc1
s_mov_b64 exec,-1
v_mfma_f32_16x16x32_bf16 a[208:211],a[148:151],v[160:163],a[208:211]
ds_read_b64_tr_b16 a[134:135],v1 offset:15552
ds_read_b64_tr_b16 a[138:139],v1 offset:25792
v_mfma_f32_16x16x32_bf16 a[208:211],a[152:155],v[192:195],a[208:211]
s_add_u32 m0,s82,40960
v_mfma_f32_16x16x32_bf16 a[208:211],a[156:159],v[224:227],a[208:211]
v_mfma_f32_16x16x32_bf16 a[212:215],a[156:159],v[232:235],a[212:215]
buffer_load_dwordx4 v[84:87],v6,s[28:31],s77 offen offset:1024 sc1
v_mfma_f32_16x16x32_bf16 a[212:215],a[152:155],v[200:203],a[212:215]
v_mfma_f32_16x16x32_bf16 a[212:215],a[148:151],v[168:171],a[212:215]
v_mfma_f32_16x16x32_bf16 a[212:215],a[144:147],v[136:139],a[212:215]
v_mfma_f32_16x16x32_bf16 a[216:219],a[144:147],v[144:147],a[216:219]
buffer_load_dwordx4 v7,s[72:75],s80 offen lds
v_mfma_f32_16x16x32_bf16 a[216:219],a[148:151],v[176:179],a[216:219]
v_mfma_f32_16x16x32_bf16 a[216:219],a[152:155],v[208:211],a[216:219]
v_mfma_f32_16x16x32_bf16 a[216:219],a[156:159],v[240:243],a[216:219]
v_mfma_f32_16x16x32_bf16 a[220:223],a[156:159],v[248:251],a[220:223]
buffer_load_dwordx4 v[88:91],v6,s[28:31],s77 offen offset:2048 sc1
v_mfma_f32_16x16x32_bf16 a[220:223],a[152:155],v[216:219],a[220:223]
ds_read_b64_tr_b16 a[152:153],v2 offset:20704
ds_read_b64_tr_b16 a[154:155],v2 offset:25824
v_mfma_f32_16x16x32_bf16 a[220:223],a[148:151],v[184:187],a[220:223]
ds_read_b64_tr_b16 a[148:149],v2 offset:10464
v_mfma_f32_16x16x32_bf16 a[220:223],a[144:147],v[152:155],a[220:223]
s_waitcnt lgkmcnt(3)
ds_read_b64_tr_b16 a[144:145],v2 offset:224
v_mfma_f32_16x16x32_bf16 a[224:227],a[160:163],v[128:131],a[224:227]
ds_read_b64_tr_b16 a[146:147],v2 offset:5344
v_mfma_f32_16x16x32_bf16 a[224:227],a[164:167],v[160:163],a[224:227]
ds_read_b64_tr_b16 a[150:151],v2 offset:15584
ds_read_b64_tr_b16 a[156:157],v2 offset:30944
v_mfma_f32_16x16x32_bf16 a[224:227],a[168:171],v[192:195],a[224:227]
buffer_load_dwordx4 v[92:95],v6,s[28:31],s77 offen offset:3072 sc1
v_mfma_f32_16x16x32_bf16 a[224:227],a[172:175],v[224:227],a[224:227]
ds_read_b64_tr_b16 a[158:159],v2 offset:36064
v_mfma_f32_16x16x32_bf16 a[228:231],a[172:175],v[232:235],a[228:231]
v_mfma_f32_16x16x32_bf16 a[228:231],a[168:171],v[200:203],a[228:231]
v_mfma_f32_16x16x32_bf16 a[228:231],a[164:167],v[168:171],a[228:231]
buffer_load_dwordx4 v8,s[72:75],s80 offen offset:1024 lds
v_mfma_f32_16x16x32_bf16 a[228:231],a[160:163],v[136:139],a[228:231]
v_mfma_f32_16x16x32_bf16 a[232:235],a[160:163],v[144:147],a[232:235]
v_mfma_f32_16x16x32_bf16 a[232:235],a[164:167],v[176:179],a[232:235]
v_mfma_f32_16x16x32_bf16 a[232:235],a[168:171],v[208:211],a[232:235]
buffer_load_dwordx4 v[96:99],v6,s[28:31],s78 offen sc1
v_mfma_f32_16x16x32_bf16 a[232:235],a[172:175],v[240:243],a[232:235]
v_mfma_f32_16x16x32_bf16 a[236:239],a[172:175],v[248:251],a[236:239]
ds_read_b64_tr_b16 a[172:173],v1 offset:30976
ds_read_b64_tr_b16 a[174:175],v1 offset:36096
v_mfma_f32_16x16x32_bf16 a[236:239],a[168:171],v[216:219],a[236:239]
ds_read_b64_tr_b16 a[168:169],v1 offset:20736
v_mfma_f32_16x16x32_bf16 a[236:239],a[164:167],v[184:187],a[236:239]
ds_read_b64_tr_b16 a[164:165],v1 offset:10496
v_mfma_f32_16x16x32_bf16 a[236:239],a[160:163],v[152:155],a[236:239]
ds_read_b64_tr_b16 a[160:161],v1 offset:256
v_mfma_f32_16x16x32_bf16 a[240:243],a[176:179],v[128:131],a[240:243]
ds_read_b64_tr_b16 a[162:163],v1 offset:5376
ds_read_b64_tr_b16 a[166:167],v1 offset:15616
v_mfma_f32_16x16x32_bf16 a[240:243],a[180:183],v[160:163],a[240:243]
ds_read_b64_tr_b16 a[170:171],v1 offset:25856
v_add_u32 v1,40960,v1
v_mfma_f32_16x16x32_bf16 a[240:243],a[184:187],v[192:195],a[240:243]
buffer_load_dwordx4 v[100:103],v6,s[28:31],s78 offen offset:1024 sc1
v_mfma_f32_16x16x32_bf16 a[240:243],a[188:191],v[224:227],a[240:243]
v_mfma_f32_16x16x32_bf16 a[244:247],a[188:191],v[232:235],a[244:247]
v_mfma_f32_16x16x32_bf16 a[244:247],a[184:187],v[200:203],a[244:247]
v_mfma_f32_16x16x32_bf16 a[244:247],a[180:183],v[168:171],a[244:247]
buffer_load_dwordx4 v9,s[72:75],s80 offen offset:2048 lds
v_mfma_f32_16x16x32_bf16 a[244:247],a[176:179],v[136:139],a[244:247]
v_mfma_f32_16x16x32_bf16 a[248:251],a[176:179],v[144:147],a[248:251]
v_mfma_f32_16x16x32_bf16 a[248:251],a[180:183],v[176:179],a[248:251]
v_mfma_f32_16x16x32_bf16 a[248:251],a[184:187],v[208:211],a[248:251]
buffer_load_dwordx4 v[104:107],v6,s[28:31],s78 offen offset:2048 sc1
v_mfma_f32_16x16x32_bf16 a[248:251],a[188:191],v[240:243],a[248:251]
v_mfma_f32_16x16x32_bf16 a[252:255],a[188:191],v[248:251],a[252:255]
ds_read_b64_tr_b16 a[188:189],v2 offset:31008
ds_read_b64_tr_b16 a[190:191],v2 offset:36128
v_mfma_f32_16x16x32_bf16 a[252:255],a[184:187],v[216:219],a[252:255]
ds_read_b64_tr_b16 a[184:185],v2 offset:20768
v_mfma_f32_16x16x32_bf16 a[252:255],a[180:183],v[184:187],a[252:255]
ds_read_b64_tr_b16 a[180:181],v2 offset:10528
v_mfma_f32_16x16x32_bf16 a[252:255],a[176:179],v[152:155],a[252:255]
ds_read_b64_tr_b16 a[176:177],v2 offset:288
v_mfma_f32_16x16x32_bf16 a[0:3],a[96:99],v[128:131],a[0:3]
ds_read_b64_tr_b16 a[178:179],v2 offset:5408
ds_read_b64_tr_b16 a[182:183],v2 offset:15648
v_mfma_f32_16x16x32_bf16 a[0:3],a[100:103],v[160:163],a[0:3]
ds_read_b64_tr_b16 a[186:187],v2 offset:25888
v_add_u32 v2,40960,v2
v_mfma_f32_16x16x32_bf16 a[0:3],a[104:107],v[192:195],a[0:3]
buffer_load_dwordx4 v[108:111],v6,s[28:31],s78 offen offset:3072 sc1
v_mfma_f32_16x16x32_bf16 a[0:3],a[108:111],v[224:227],a[0:3]
v_mfma_f32_16x16x32_bf16 a[4:7],a[108:111],v[232:235],a[4:7]
v_mfma_f32_16x16x32_bf16 a[4:7],a[104:107],v[200:203],a[4:7]
v_mfma_f32_16x16x32_bf16 a[4:7],a[100:103],v[168:171],a[4:7]
buffer_load_dwordx4 v10,s[72:75],s80 offen offset:3072 lds
v_mfma_f32_16x16x32_bf16 a[4:7],a[96:99],v[136:139],a[4:7]
v_mfma_f32_16x16x32_bf16 a[8:11],a[96:99],v[144:147],a[8:11]
v_mfma_f32_16x16x32_bf16 a[8:11],a[100:103],v[176:179],a[8:11]
v_mfma_f32_16x16x32_bf16 a[8:11],a[104:107],v[208:211],a[8:11]
buffer_load_dwordx4 v[112:115],v6,s[28:31],s79 offen sc1
v_mfma_f32_16x16x32_bf16 a[8:11],a[108:111],v[240:243],a[8:11]
v_mfma_f32_16x16x32_bf16 a[12:15],a[108:111],v[248:251],a[12:15]
ds_read_b64_tr_b16 a[108:109],v1 offset:30720
ds_read_b64_tr_b16 a[110:111],v1 offset:35840
v_mfma_f32_16x16x32_bf16 a[12:15],a[104:107],v[216:219],a[12:15]
ds_read_b64_tr_b16 a[104:105],v1 offset:20480
v_mfma_f32_16x16x32_bf16 a[12:15],a[100:103],v[184:187],a[12:15]
s_add_u32 m0,s82,45056
ds_read_b64_tr_b16 a[100:101],v1 offset:10240
v_mfma_f32_16x16x32_bf16 a[12:15],a[96:99],v[152:155],a[12:15]
ds_read_b64_tr_b16 a[96:97],v1
v_mfma_f32_16x16x32_bf16 a[16:19],a[112:115],v[128:131],a[16:19]
ds_read_b64_tr_b16 a[98:99],v1 offset:5120
ds_read_b64_tr_b16 a[102:103],v1 offset:15360
v_mfma_f32_16x16x32_bf16 a[16:19],a[116:119],v[160:163],a[16:19]
buffer_load_dwordx4 v11,s[72:75],s80 offen lds
v_mfma_f32_16x16x32_bf16 a[16:19],a[120:123],v[192:195],a[16:19]
ds_read_b64_tr_b16 a[106:107],v1 offset:25600
v_mfma_f32_16x16x32_bf16 a[16:19],a[124:127],v[224:227],a[16:19]
v_mfma_f32_16x16x32_bf16 a[20:23],a[124:127],v[232:235],a[20:23]
v_mfma_f32_16x16x32_bf16 a[20:23],a[120:123],v[200:203],a[20:23]
buffer_load_dwordx4 v[116:119],v6,s[28:31],s79 offen offset:1024 sc1
v_mfma_f32_16x16x32_bf16 a[20:23],a[116:119],v[168:171],a[20:23]
v_mfma_f32_16x16x32_bf16 a[20:23],a[112:115],v[136:139],a[20:23]
v_mfma_f32_16x16x32_bf16 a[24:27],a[112:115],v[144:147],a[24:27]
v_mfma_f32_16x16x32_bf16 a[24:27],a[116:119],v[176:179],a[24:27]
v_mfma_f32_16x16x32_bf16 a[24:27],a[120:123],v[208:211],a[24:27]
buffer_load_dwordx4 v[120:123],v6,s[28:31],s79 offen offset:2048 sc1
v_mfma_f32_16x16x32_bf16 a[24:27],a[124:127],v[240:243],a[24:27]
v_mfma_f32_16x16x32_bf16 a[28:31],a[124:127],v[248:251],a[28:31]
ds_read_b64_tr_b16 a[124:125],v2 offset:30752
ds_read_b64_tr_b16 a[126:127],v2 offset:35872
v_mfma_f32_16x16x32_bf16 a[28:31],a[120:123],v[216:219],a[28:31]
s_add_u32 m0,s82,46080
ds_read_b64_tr_b16 a[120:121],v2 offset:20512
v_mfma_f32_16x16x32_bf16 a[28:31],a[116:119],v[184:187],a[28:31]
ds_read_b64_tr_b16 a[116:117],v2 offset:10272
v_mfma_f32_16x16x32_bf16 a[28:31],a[112:115],v[152:155],a[28:31]
s_cmp_ge_u32 2,s71
s_cselect_b32 s76,6,4
s_lshl_b32 s76,s76,16
v_mfma_f32_16x16x32_bf16 a[32:35],a[128:131],v[128:131],a[32:35]
s_add_u32 s77,s76,4096
buffer_load_dwordx4 v7,s[72:75],s83 offen lds
v_mfma_f32_16x16x32_bf16 a[32:35],a[132:135],v[160:163],a[32:35]
s_add_u32 s78,s76,8192
s_max_i32 s77,s77,s19
ds_read_b64_tr_b16 a[112:113],v2 offset:32
v_mfma_f32_16x16x32_bf16 a[32:35],a[136:139],v[192:195],a[32:35]
s_max_i32 s78,s78,s100
ds_read_b64_tr_b16 a[114:115],v2 offset:5152
ds_read_b64_tr_b16 a[118:119],v2 offset:15392
v_mfma_f32_16x16x32_bf16 a[32:35],a[140:143],v[224:227],a[32:35]
ds_read_b64_tr_b16 a[122:123],v2 offset:25632
v_mfma_f32_16x16x32_bf16 a[36:39],a[140:143],v[232:235],a[36:39]
buffer_load_dwordx4 v[124:127],v6,s[28:31],s79 offen offset:3072 sc1
v_mfma_f32_16x16x32_bf16 a[36:39],a[136:139],v[200:203],a[36:39]
s_add_u32 s79,s76,12288
s_max_i32 s76,s76,s18
s_max_i32 s79,s79,s101
v_mfma_f32_16x16x32_bf16 a[36:39],a[132:135],v[168:171],a[36:39]
v_mfma_f32_16x16x32_bf16 a[36:39],a[128:131],v[136:139],a[36:39]
v_mfma_f32_16x16x32_bf16 a[40:43],a[128:131],v[144:147],a[40:43]
v_mfma_f32_16x16x32_bf16 a[40:43],a[132:135],v[176:179],a[40:43]
v_mfma_f32_16x16x32_bf16 a[40:43],a[136:139],v[208:211],a[40:43]
v_mfma_f32_16x16x32_bf16 a[40:43],a[140:143],v[240:243],a[40:43]
v_mfma_f32_16x16x32_bf16 a[44:47],a[140:143],v[248:251],a[44:47]
ds_read_b64_tr_b16 a[140:141],v1 offset:30784
ds_read_b64_tr_b16 a[142:143],v1 offset:35904
v_mfma_f32_16x16x32_bf16 a[44:47],a[136:139],v[216:219],a[44:47]
ds_read_b64_tr_b16 a[136:137],v1 offset:20544
v_mfma_f32_16x16x32_bf16 a[44:47],a[132:135],v[184:187],a[44:47]
ds_read_b64_tr_b16 a[132:133],v1 offset:10304
v_mfma_f32_16x16x32_bf16 a[44:47],a[128:131],v[152:155],a[44:47]
s_waitcnt lgkmcnt(4)
buffer_load_dwordx4 v8,s[72:75],s83 offen offset:1024 lds
v_mfma_f32_16x16x32_bf16 a[48:51],a[144:147],v[128:131],a[48:51]
ds_read_b64_tr_b16 a[128:129],v1 offset:64
ds_read_b64_tr_b16 a[130:131],v1 offset:5184
v_mfma_f32_16x16x32_bf16 a[48:51],a[148:151],v[160:163],a[48:51]
ds_read_b64_tr_b16 a[134:135],v1 offset:15424
v_mfma_f32_16x16x32_bf16 a[48:51],a[152:155],v[192:195],a[48:51]
ds_read_b64_tr_b16 a[138:139],v1 offset:25664
v_mfma_f32_16x16x32_bf16 a[48:51],a[156:159],v[224:227],a[48:51]
s_lshl_b32 s94,s70,4
v_bfe_u32 v24,v6,4,2
v_min_u32 v24,2,v24
v_mfma_f32_16x16x32_bf16 a[52:55],a[156:159],v[232:235],a[52:55]
s_add_u32 s94,s94,s40
v_add_u32 v24,s71,v24
v_add_u32 v24,1,v24
v_mfma_f32_16x16x32_bf16 a[52:55],a[152:155],v[200:203],a[52:55]
s_lshl_b32 s94,s94,4
v_and_b32 v24,3,v24
v_lshlrev_b32 v24,6,v24
v_mfma_f32_16x16x32_bf16 a[52:55],a[148:151],v[168:171],a[52:55]
v_mfma_f32_16x16x32_bf16 a[52:55],a[144:147],v[136:139],a[52:55]
buffer_load_dwordx4 v[132:135],v6,s[28:31],s76 offen sc1
v_mfma_f32_16x16x32_bf16 a[56:59],a[144:147],v[144:147],a[56:59]
v_mfma_f32_16x16x32_bf16 a[56:59],a[148:151],v[176:179],a[56:59]
v_mfma_f32_16x16x32_bf16 a[56:59],a[152:155],v[208:211],a[56:59]
v_mfma_f32_16x16x32_bf16 a[56:59],a[156:159],v[240:243],a[56:59]
v_mfma_f32_16x16x32_bf16 a[60:63],a[156:159],v[248:251],a[60:63]
ds_read_b64_tr_b16 a[156:157],v2 offset:30816
ds_read_b64_tr_b16 a[158:159],v2 offset:35936
v_mfma_f32_16x16x32_bf16 a[60:63],a[152:155],v[216:219],a[60:63]
buffer_load_dwordx4 v[140:143],v6,s[28:31],s76 offen offset:1024 sc1
v_mfma_f32_16x16x32_bf16 a[60:63],a[148:151],v[184:187],a[60:63]
ds_read_b64_tr_b16 a[148:149],v2 offset:10336
ds_read_b64_tr_b16 a[150:151],v2 offset:15456
)A"
R"A(v_mfma_f32_16x16x32_bf16 a[60:63],a[144:147],v[152:155],a[60:63]
ds_read_b64_tr_b16 a[144:145],v2 offset:96
v_mfma_f32_16x16x32_bf16 a[64:67],a[160:163],v[128:131],a[64:67]
ds_read_b64_tr_b16 a[146:147],v2 offset:5216
v_mfma_f32_16x16x32_bf16 a[64:67],a[164:167],v[160:163],a[64:67]
buffer_load_dwordx4 v9,s[72:75],s83 offen offset:2048 lds
v_mfma_f32_16x16x32_bf16 a[64:67],a[168:171],v[192:195],a[64:67]
ds_read_b64_tr_b16 a[152:153],v2 offset:20576
ds_read_b64_tr_b16 a[154:155],v2 offset:25696
v_mfma_f32_16x16x32_bf16 a[64:67],a[172:175],v[224:227],a[64:67]
v_mfma_f32_16x16x32_bf16 a[68:71],a[172:175],v[232:235],a[68:71]
v_mfma_f32_16x16x32_bf16 a[68:71],a[168:171],v[200:203],a[68:71]
buffer_load_dwordx4 v[148:151],v6,s[28:31],s76 offen offset:2048 sc1
v_mfma_f32_16x16x32_bf16 a[68:71],a[164:167],v[168:171],a[68:71]
v_mfma_f32_16x16x32_bf16 a[68:71],a[160:163],v[136:139],a[68:71]
v_mfma_f32_16x16x32_bf16 a[72:75],a[160:163],v[144:147],a[72:75]
v_mfma_f32_16x16x32_bf16 a[72:75],a[164:167],v[176:179],a[72:75]
buffer_load_dwordx4 v[156:159],v6,s[28:31],s76 offen offset:3072 sc1
v_mfma_f32_16x16x32_bf16 a[72:75],a[168:171],v[208:211],a[72:75]
v_mfma_f32_16x16x32_bf16 a[72:75],a[172:175],v[240:243],a[72:75]
v_mfma_f32_16x16x32_bf16 a[76:79],a[172:175],v[248:251],a[76:79]
ds_read_b64_tr_b16 a[172:173],v1 offset:30848
ds_read_b64_tr_b16 a[174:175],v1 offset:35968
v_mfma_f32_16x16x32_bf16 a[76:79],a[168:171],v[216:219],a[76:79]
buffer_load_dwordx4 v10,s[72:75],s83 offen offset:3072 lds
v_mfma_f32_16x16x32_bf16 a[76:79],a[164:167],v[184:187],a[76:79]
ds_read_b64_tr_b16 a[164:165],v1 offset:10368
ds_read_b64_tr_b16 a[166:167],v1 offset:15488
v_mfma_f32_16x16x32_bf16 a[76:79],a[160:163],v[152:155],a[76:79]
ds_read_b64_tr_b16 a[160:161],v1 offset:128
v_mfma_f32_16x16x32_bf16 a[80:83],a[176:179],v[128:131],a[80:83]
ds_read_b64_tr_b16 a[162:163],v1 offset:5248
v_mfma_f32_16x16x32_bf16 a[80:83],a[180:183],v[160:163],a[80:83]
buffer_load_dwordx4 v[164:167],v6,s[28:31],s77 offen sc1
v_mfma_f32_16x16x32_bf16 a[80:83],a[184:187],v[192:195],a[80:83]
ds_read_b64_tr_b16 a[168:169],v1 offset:20608
ds_read_b64_tr_b16 a[170:171],v1 offset:25728
v_mfma_f32_16x16x32_bf16 a[80:83],a[188:191],v[224:227],a[80:83]
v_mfma_f32_16x16x32_bf16 a[84:87],a[188:191],v[232:235],a[84:87]
v_mfma_f32_16x16x32_bf16 a[84:87],a[184:187],v[200:203],a[84:87]
buffer_load_dwordx4 v[172:175],v6,s[28:31],s77 offen offset:1024 sc1
v_mfma_f32_16x16x32_bf16 a[84:87],a[180:183],v[168:171],a[84:87]
v_mfma_f32_16x16x32_bf16 a[84:87],a[176:179],v[136:139],a[84:87]
v_mfma_f32_16x16x32_bf16 a[88:91],a[176:179],v[144:147],a[88:91]
v_mfma_f32_16x16x32_bf16 a[88:91],a[180:183],v[176:179],a[88:91]
s_add_u32 m0,s82,50176
v_mfma_f32_16x16x32_bf16 a[88:91],a[184:187],v[208:211],a[88:91]
v_mfma_f32_16x16x32_bf16 a[88:91],a[188:191],v[240:243],a[88:91]
buffer_load_dwordx2 v[26:27],v24,s[64:67],s94 offen offset:1024 sc1
v_mfma_f32_16x16x32_bf16 a[92:95],a[188:191],v[248:251],a[92:95]
ds_read_b64_tr_b16 a[188:189],v2 offset:30880
ds_read_b64_tr_b16 a[190:191],v2 offset:36000
v_mfma_f32_16x16x32_bf16 a[92:95],a[184:187],v[216:219],a[92:95]
ds_read_b64_tr_b16 a[184:185],v2 offset:20640
v_mfma_f32_16x16x32_bf16 a[92:95],a[180:183],v[184:187],a[92:95]
ds_read_b64_tr_b16 a[180:181],v2 offset:10400
v_mfma_f32_16x16x32_bf16 a[92:95],a[176:179],v[152:155],a[92:95]
s_waitcnt vmcnt(13)
buffer_load_dwordx4 v[180:183],v6,s[28:31],s77 offen offset:2048 sc1
v_mfma_f32_16x16x32_bf16 a[192:195],a[96:99],v[64:67],a[192:195]
s_lshl_b32 s43,s41,2
ds_read_b64_tr_b16 a[176:177],v2 offset:160
ds_read_b64_tr_b16 a[178:179],v2 offset:5280
v_mfma_f32_16x16x32_bf16 a[192:195],a[100:103],v[68:71],a[192:195]
s_add_u32 s84,s70,1
s_add_u32 s85,s70,2
ds_read_b64_tr_b16 a[182:183],v2 offset:15520
v_mfma_f32_16x16x32_bf16 a[192:195],a[104:107],v[72:75],a[192:195]
s_add_u32 s86,s70,3
s_add_u32 s43,s43,s71
ds_read_b64_tr_b16 a[186:187],v2 offset:25760
v_mfma_f32_16x16x32_bf16 a[192:195],a[108:111],v[76:79],a[192:195]
s_and_b32 s84,s84,3
buffer_load_dwordx4 v11,s[72:75],s83 offen lds
v_mfma_f32_16x16x32_bf16 a[196:199],a[108:111],v[92:95],a[196:199]
s_and_b32 s85,s85,3
s_and_b32 s86,s86,3
s_mul_i32 s43,s43,245760
v_mfma_f32_16x16x32_bf16 a[196:199],a[104:107],v[88:91],a[196:199]
s_mul_i32 s84,s84,12
s_mul_i32 s85,s85,12
s_mul_i32 s86,s86,12
v_mfma_f32_16x16x32_bf16 a[196:199],a[100:103],v[84:87],a[196:199]
s_add_u32 s43,s43,134217728
s_mov_b64 s[32:33],s[12:13]
s_add_u32 s84,s84,0
v_mfma_f32_16x16x32_bf16 a[196:199],a[96:99],v[80:83],a[196:199]
s_add_u32 s85,s85,4
buffer_load_dwordx4 v[188:191],v6,s[28:31],s77 offen offset:3072 sc1
v_mfma_f32_16x16x32_bf16 a[200:203],a[96:99],v[96:99],a[200:203]
s_add_u32 s86,s86,8
s_add_u32 s84,s84,s40
s_add_u32 s85,s85,s40
v_mfma_f32_16x16x32_bf16 a[200:203],a[100:103],v[100:103],a[200:203]
s_add_u32 s86,s86,s40
s_add_u32 s32,s32,s43
s_addc_u32 s33,s33,0
v_mfma_f32_16x16x32_bf16 a[200:203],a[104:107],v[104:107],a[200:203]
s_mul_i32 s84,s84,5120
s_mul_i32 s85,s85,5120
s_mul_i32 s86,s86,5120
v_mfma_f32_16x16x32_bf16 a[200:203],a[108:111],v[108:111],a[200:203]
s_and_b32 s33,s33,0xffff
buffer_load_dwordx4 v[196:199],v6,s[28:31],s78 offen sc1
s_waitcnt vmcnt(14)
v_mfma_f32_16x16x32_bf16 a[204:207],a[108:111],v[124:127],a[204:207]
s_mov_b32 s34,245760
ds_read_b64_tr_b16 a[108:109],v1 offset:30912
ds_read_b64_tr_b16 a[110:111],v1 offset:36032
v_mfma_f32_16x16x32_bf16 a[204:207],a[104:107],v[120:123],a[204:207]
s_mov_b32 s35,0x20000
s_max_i32 s84,s84,s18
ds_read_b64_tr_b16 a[104:105],v1 offset:20672
v_mfma_f32_16x16x32_bf16 a[204:207],a[100:103],v[116:119],a[204:207]
s_max_i32 s85,s85,s19
s_max_i32 s86,s86,s100
ds_read_b64_tr_b16 a[100:101],v1 offset:10432
v_mfma_f32_16x16x32_bf16 a[204:207],a[96:99],v[112:115],a[204:207]
s_barrier
buffer_load_dwordx4 v[204:207],v6,s[28:31],s78 offen offset:1024 sc1
v_mfma_f32_16x16x32_bf16 a[208:211],a[112:115],v[64:67],a[208:211]
s_mul_i32 s44,s41,307200
ds_read_b64_tr_b16 a[96:97],v1 offset:192
ds_read_b64_tr_b16 a[98:99],v1 offset:5312
v_mfma_f32_16x16x32_bf16 a[208:211],a[116:119],v[68:71],a[208:211]
s_mov_b64 s[20:21],s[16:17]
ds_read_b64_tr_b16 a[102:103],v1 offset:15552
v_bfe_u32 v18,v6,6,2
v_mfma_f32_16x16x32_bf16 a[208:211],a[120:123],v[72:75],a[208:211]
s_lshl_b32 s43,s40,2
ds_read_b64_tr_b16 a[106:107],v1 offset:25792
v_lshrrev_b32 v19,8,v6
v_mfma_f32_16x16x32_bf16 a[208:211],a[124:127],v[76:79],a[208:211]
s_add_u32 s43,s43,s70
buffer_load_dwordx4 v[212:215],v6,s[28:31],s78 offen offset:2048 sc1
v_mfma_f32_16x16x32_bf16 a[212:215],a[124:127],v[92:95],a[212:215]
s_mul_i32 s43,s43,20480
v_xor_b32 v18,v18,v19
v_lshlrev_b32 v18,2,v18
v_mfma_f32_16x16x32_bf16 a[212:215],a[120:123],v[88:91],a[212:215]
s_mov_b32 s22,307200
v_bfe_u32 v19,v6,4,2
v_or_b32 v18,v18,v19
v_mfma_f32_16x16x32_bf16 a[212:215],a[116:119],v[84:87],a[212:215]
s_mov_b32 s23,0x20000
v_and_b32 v19,16,v6
v_mov_b32 v34,0x3f80
v_mfma_f32_16x16x32_bf16 a[212:215],a[112:115],v[80:83],a[212:215]
s_cmp_ge_u32 0,s71
s_cselect_b32 s76,3,1
s_lshl_b32 s76,s76,16
v_mfma_f32_16x16x32_bf16 a[216:219],a[112:115],v[96:99],a[216:219]
s_add_u32 s77,s76,4096
buffer_load_dwordx4 v[220:223],v6,s[28:31],s78 offen offset:3072 sc1
v_mfma_f32_16x16x32_bf16 a[216:219],a[116:119],v[100:103],a[216:219]
s_add_u32 s78,s76,8192
s_max_i32 s77,s77,s19
s_max_i32 s78,s78,s100
v_mfma_f32_16x16x32_bf16 a[216:219],a[120:123],v[104:107],a[216:219]
v_lshlrev_b32 v19,v19,v34
v_lshrrev_b32 v18,1,v18
v_mfma_f32_16x16x32_bf16 a[216:219],a[124:127],v[108:111],a[216:219]
v_cmp_eq_u32 vcc,0,v18
s_nop 1
v_cndmask_b32 v34,0,v19,vcc
v_mfma_f32_16x16x32_bf16 a[220:223],a[124:127],v[124:127],a[220:223]
ds_read_b64_tr_b16 a[124:125],v2 offset:30944
ds_read_b64_tr_b16 a[126:127],v2 offset:36064
v_mfma_f32_16x16x32_bf16 a[220:223],a[120:123],v[120:123],a[220:223]
ds_read_b64_tr_b16 a[120:121],v2 offset:20704
v_add_u32 v25,4096,v6
v_mfma_f32_16x16x32_bf16 a[220:223],a[116:119],v[116:119],a[220:223]
buffer_load_dwordx4 v[228:231],v6,s[28:31],s79 offen sc1
v_mfma_f32_16x16x32_bf16 a[220:223],a[112:115],v[112:115],a[220:223]
s_waitcnt lgkmcnt(3)
ds_read_b64_tr_b16 a[112:113],v2 offset:224
ds_read_b64_tr_b16 a[114:115],v2 offset:5344
v_mfma_f32_16x16x32_bf16 a[224:227],a[128:131],v[64:67],a[224:227]
ds_read_b64_tr_b16 a[116:117],v2 offset:10464
v_mov_b32 v36,0
v_mfma_f32_16x16x32_bf16 a[224:227],a[132:135],v[68:71],a[224:227]
ds_read_b64_tr_b16 a[118:119],v2 offset:15584
v_mov_b32 v37,0
v_mfma_f32_16x16x32_bf16 a[224:227],a[136:139],v[72:75],a[224:227]
ds_read_b64_tr_b16 a[122:123],v2 offset:25824
v_mov_b32 v32,0
v_mfma_f32_16x16x32_bf16 a[224:227],a[140:143],v[76:79],a[224:227]
v_cmp_eq_u32 vcc,1,v18
s_nop 1
v_cndmask_b32 v35,0,v19,vcc
v_mfma_f32_16x16x32_bf16 a[228:231],a[140:143],v[92:95],a[228:231]
buffer_load_dwordx4 v[236:239],v6,s[28:31],s79 offen offset:1024 sc1
v_mfma_f32_16x16x32_bf16 a[228:231],a[136:139],v[88:91],a[228:231]
s_add_u32 s20,s20,s44
s_addc_u32 s21,s21,0
v_mov_b32 v33,0
v_mfma_f32_16x16x32_bf16 a[228:231],a[132:135],v[84:87],a[228:231]
s_mul_i32 s44,s71,320
v_bfe_u32 v40,v6,4,4
v_mul_u32_u24 v40,1280,v40
v_mfma_f32_16x16x32_bf16 a[228:231],a[128:131],v[80:83],a[228:231]
s_waitcnt vmcnt(9)
v_cmp_ne_u32 s[90:91],s68,v26
v_cmp_ne_u32 s[92:93],s69,v27
s_nop 0
s_or_b64 s[90:91],s[90:91],s[92:93]
s_cbranch_scc1 L_fr14_%=
L_fo14_%=:
v_mfma_f32_16x16x32_bf16 a[232:235],a[128:131],v[96:99],a[232:235]
s_and_b32 s21,s21,0xffff
v_lshrrev_b32 v41,8,v6
v_lshl_add_u32 v40,v41,3,v40
v_mfma_f32_16x16x32_bf16 a[232:235],a[132:135],v[100:103],a[232:235]
s_add_u32 s43,s43,s44
v_mfma_f32_16x16x32_bf16 a[232:235],a[136:139],v[104:107],a[232:235]
buffer_load_dwordx4 v[244:247],v6,s[28:31],s79 offen offset:2048 sc1
v_mfma_f32_16x16x32_bf16 a[232:235],a[140:143],v[108:111],a[232:235]
v_mfma_f32_16x16x32_bf16 a[236:239],a[140:143],v[124:127],a[236:239]
ds_read_b64_tr_b16 a[140:141],v1 offset:30976
ds_read_b64_tr_b16 a[142:143],v1 offset:36096
v_mfma_f32_16x16x32_bf16 a[236:239],a[136:139],v[120:123],a[236:239]
ds_read_b64_tr_b16 a[136:137],v1 offset:20736
v_mfma_f32_16x16x32_bf16 a[236:239],a[132:135],v[116:119],a[236:239]
ds_read_b64_tr_b16 a[132:133],v1 offset:10496
v_mfma_f32_16x16x32_bf16 a[236:239],a[128:131],v[112:115],a[236:239]
ds_read_b64_tr_b16 a[128:129],v1 offset:256
v_mfma_f32_16x16x32_bf16 a[240:243],a[144:147],v[64:67],a[240:243]
buffer_load_dwordx4 v[252:255],v6,s[28:31],s79 offen offset:3072 sc1
v_mfma_f32_16x16x32_bf16 a[240:243],a[148:151],v[68:71],a[240:243]
s_add_u32 s79,s76,12288
s_max_i32 s76,s76,s18
ds_read_b64_tr_b16 a[130:131],v1 offset:5376
v_mfma_f32_16x16x32_bf16 a[240:243],a[152:155],v[72:75],a[240:243]
s_max_i32 s79,s79,s101
ds_read_b64_tr_b16 a[134:135],v1 offset:15616
ds_read_b64_tr_b16 a[138:139],v1 offset:25856
v_mfma_f32_16x16x32_bf16 a[240:243],a[156:159],v[76:79],a[240:243]
v_subrev_u32 v1,122880,v1
v_mfma_f32_16x16x32_bf16 a[244:247],a[156:159],v[92:95],a[244:247]
buffer_load_dwordx4 v[128:131],v6,s[28:31],s76 offen sc1
v_mfma_f32_16x16x32_bf16 a[244:247],a[152:155],v[88:91],a[244:247]
v_mfma_f32_16x16x32_bf16 a[244:247],a[148:151],v[84:87],a[244:247]
v_mfma_f32_16x16x32_bf16 a[244:247],a[144:147],v[80:83],a[244:247]
v_mfma_f32_16x16x32_bf16 a[248:251],a[144:147],v[96:99],a[248:251]
buffer_load_dwordx4 v[160:163],v6,s[28:31],s76 offen offset:1024 sc1
v_mfma_f32_16x16x32_bf16 a[248:251],a[148:151],v[100:103],a[248:251]
v_mfma_f32_16x16x32_bf16 a[248:251],a[152:155],v[104:107],a[248:251]
v_mfma_f32_16x16x32_bf16 a[248:251],a[156:159],v[108:111],a[248:251]
v_mfma_f32_16x16x32_bf16 a[252:255],a[156:159],v[124:127],a[252:255]
buffer_load_dwordx4 v[192:195],v6,s[28:31],s76 offen offset:2048 sc1
v_mfma_f32_16x16x32_bf16 a[252:255],a[152:155],v[120:123],a[252:255]
ds_read_b64_tr_b16 a[152:153],v2 offset:20768
ds_read_b64_tr_b16 a[154:155],v2 offset:25888
v_mfma_f32_16x16x32_bf16 a[252:255],a[148:151],v[116:119],a[252:255]
ds_read_b64_tr_b16 a[148:149],v2 offset:10528
v_mfma_f32_16x16x32_bf16 a[252:255],a[144:147],v[112:115],a[252:255]
ds_read_b64_tr_b16 a[144:145],v2 offset:288
)A"
R"A(v_mfma_f32_16x16x32_bf16 a[0:3],a[160:163],v[64:67],a[0:3]
ds_read_b64_tr_b16 a[146:147],v2 offset:5408
v_mfma_f32_16x16x32_bf16 a[0:3],a[164:167],v[68:71],a[0:3]
buffer_load_dwordx4 v[224:227],v6,s[28:31],s76 offen offset:3072 sc1
v_mfma_f32_16x16x32_bf16 a[0:3],a[168:171],v[72:75],a[0:3]
ds_read_b64_tr_b16 a[150:151],v2 offset:15648
ds_read_b64_tr_b16 a[156:157],v2 offset:31008
v_mfma_f32_16x16x32_bf16 a[0:3],a[172:175],v[76:79],a[0:3]
ds_read_b64_tr_b16 a[158:159],v2 offset:36128
v_subrev_u32 v2,122880,v2
v_mfma_f32_16x16x32_bf16 a[4:7],a[172:175],v[92:95],a[4:7]
v_mfma_f32_16x16x32_bf16 a[4:7],a[168:171],v[88:91],a[4:7]
v_mfma_f32_16x16x32_bf16 a[4:7],a[164:167],v[84:87],a[4:7]
v_mfma_f32_16x16x32_bf16 a[4:7],a[160:163],v[80:83],a[4:7]
buffer_load_dwordx4 v[136:139],v6,s[28:31],s77 offen sc1
v_mfma_f32_16x16x32_bf16 a[8:11],a[160:163],v[96:99],a[8:11]
v_mfma_f32_16x16x32_bf16 a[8:11],a[164:167],v[100:103],a[8:11]
v_mfma_f32_16x16x32_bf16 a[8:11],a[168:171],v[104:107],a[8:11]
v_mfma_f32_16x16x32_bf16 a[8:11],a[172:175],v[108:111],a[8:11]
v_mfma_f32_16x16x32_bf16 a[12:15],a[172:175],v[124:127],a[12:15]
ds_read_b64_tr_b16 a[172:173],v1 offset:30720
ds_read_b64_tr_b16 a[174:175],v1 offset:35840
v_mfma_f32_16x16x32_bf16 a[12:15],a[168:171],v[120:123],a[12:15]
buffer_load_dwordx4 v[168:171],v6,s[28:31],s77 offen offset:1024 sc1
v_mfma_f32_16x16x32_bf16 a[12:15],a[164:167],v[116:119],a[12:15]
ds_read_b64_tr_b16 a[164:165],v1 offset:10240
ds_read_b64_tr_b16 a[166:167],v1 offset:15360
v_mfma_f32_16x16x32_bf16 a[12:15],a[160:163],v[112:115],a[12:15]
ds_read_b64_tr_b16 a[160:161],v1
v_mfma_f32_16x16x32_bf16 a[16:19],a[176:179],v[64:67],a[16:19]
ds_read_b64_tr_b16 a[162:163],v1 offset:5120
v_mfma_f32_16x16x32_bf16 a[16:19],a[180:183],v[68:71],a[16:19]
ds_read_b64_tr_b16 a[168:169],v1 offset:20480
v_mfma_f32_16x16x32_bf16 a[16:19],a[184:187],v[72:75],a[16:19]
ds_read_b64_tr_b16 a[170:171],v1 offset:25600
v_mfma_f32_16x16x32_bf16 a[16:19],a[188:191],v[76:79],a[16:19]
buffer_load_dwordx4 v[200:203],v6,s[28:31],s77 offen offset:2048 sc1
v_mfma_f32_16x16x32_bf16 a[20:23],a[188:191],v[92:95],a[20:23]
v_mfma_f32_16x16x32_bf16 a[20:23],a[184:187],v[88:91],a[20:23]
v_mfma_f32_16x16x32_bf16 a[20:23],a[180:183],v[84:87],a[20:23]
v_mfma_f32_16x16x32_bf16 a[20:23],a[176:179],v[80:83],a[20:23]
v_mfma_f32_16x16x32_bf16 a[24:27],a[176:179],v[96:99],a[24:27]
v_mfma_f32_16x16x32_bf16 a[24:27],a[180:183],v[100:103],a[24:27]
buffer_load_dwordx4 v[232:235],v6,s[28:31],s77 offen offset:3072 sc1
v_mfma_f32_16x16x32_bf16 a[24:27],a[184:187],v[104:107],a[24:27]
v_mfma_f32_16x16x32_bf16 a[24:27],a[188:191],v[108:111],a[24:27]
v_mfma_f32_16x16x32_bf16 a[28:31],a[188:191],v[124:127],a[28:31]
ds_read_b64_tr_b16 a[188:189],v2 offset:30752
ds_read_b64_tr_b16 a[190:191],v2 offset:35872
v_mfma_f32_16x16x32_bf16 a[28:31],a[184:187],v[120:123],a[28:31]
ds_read_b64_tr_b16 a[184:185],v2 offset:20512
v_mfma_f32_16x16x32_bf16 a[28:31],a[180:183],v[116:119],a[28:31]
ds_read_b64_tr_b16 a[180:181],v2 offset:10272
v_mfma_f32_16x16x32_bf16 a[28:31],a[176:179],v[112:115],a[28:31]
buffer_load_dwordx4 v[144:147],v6,s[28:31],s78 offen sc1
v_mfma_f32_16x16x32_bf16 a[32:35],a[96:99],v[64:67],a[32:35]
ds_read_b64_tr_b16 a[176:177],v2 offset:32
ds_read_b64_tr_b16 a[178:179],v2 offset:5152
v_mfma_f32_16x16x32_bf16 a[32:35],a[100:103],v[68:71],a[32:35]
ds_read_b64_tr_b16 a[182:183],v2 offset:15392
v_mfma_f32_16x16x32_bf16 a[32:35],a[104:107],v[72:75],a[32:35]
ds_read_b64_tr_b16 a[186:187],v2 offset:25632
v_mfma_f32_16x16x32_bf16 a[32:35],a[108:111],v[76:79],a[32:35]
v_mfma_f32_16x16x32_bf16 a[36:39],a[108:111],v[92:95],a[36:39]
v_mfma_f32_16x16x32_bf16 a[36:39],a[104:107],v[88:91],a[36:39]
buffer_load_dwordx4 v[176:179],v6,s[28:31],s78 offen offset:1024 sc1
v_mfma_f32_16x16x32_bf16 a[36:39],a[100:103],v[84:87],a[36:39]
v_mfma_f32_16x16x32_bf16 a[36:39],a[96:99],v[80:83],a[36:39]
v_mfma_f32_16x16x32_bf16 a[40:43],a[96:99],v[96:99],a[40:43]
v_mfma_f32_16x16x32_bf16 a[40:43],a[100:103],v[100:103],a[40:43]
v_mfma_f32_16x16x32_bf16 a[40:43],a[104:107],v[104:107],a[40:43]
v_mfma_f32_16x16x32_bf16 a[40:43],a[108:111],v[108:111],a[40:43]
buffer_load_dwordx4 v[208:211],v6,s[28:31],s78 offen offset:2048 sc1
v_mfma_f32_16x16x32_bf16 a[44:47],a[108:111],v[124:127],a[44:47]
ds_read_b64_tr_b16 a[108:109],v1 offset:30784
ds_read_b64_tr_b16 a[110:111],v1 offset:35904
v_mfma_f32_16x16x32_bf16 a[44:47],a[104:107],v[120:123],a[44:47]
ds_read_b64_tr_b16 a[104:105],v1 offset:20544
v_mfma_f32_16x16x32_bf16 a[44:47],a[100:103],v[116:119],a[44:47]
ds_read_b64_tr_b16 a[100:101],v1 offset:10304
v_mfma_f32_16x16x32_bf16 a[44:47],a[96:99],v[112:115],a[44:47]
s_waitcnt lgkmcnt(4)
ds_read_b64_tr_b16 a[96:97],v1 offset:64
v_mfma_f32_16x16x32_bf16 a[48:51],a[112:115],v[64:67],a[48:51]
ds_read_b64_tr_b16 a[98:99],v1 offset:5184
ds_read_b64_tr_b16 a[102:103],v1 offset:15424
v_mfma_f32_16x16x32_bf16 a[48:51],a[116:119],v[68:71],a[48:51]
buffer_load_dwordx4 v[240:243],v6,s[28:31],s78 offen offset:3072 sc1
v_mfma_f32_16x16x32_bf16 a[48:51],a[120:123],v[72:75],a[48:51]
ds_read_b64_tr_b16 a[106:107],v1 offset:25664
v_mfma_f32_16x16x32_bf16 a[48:51],a[124:127],v[76:79],a[48:51]
v_mfma_f32_16x16x32_bf16 a[52:55],a[124:127],v[92:95],a[52:55]
v_mfma_f32_16x16x32_bf16 a[52:55],a[120:123],v[88:91],a[52:55]
v_mfma_f32_16x16x32_bf16 a[52:55],a[116:119],v[84:87],a[52:55]
v_mfma_f32_16x16x32_bf16 a[52:55],a[112:115],v[80:83],a[52:55]
buffer_load_dwordx4 v[152:155],v6,s[28:31],s79 offen sc1
v_mfma_f32_16x16x32_bf16 a[56:59],a[112:115],v[96:99],a[56:59]
v_mfma_f32_16x16x32_bf16 a[56:59],a[116:119],v[100:103],a[56:59]
s_cmp_ge_u32 1,s71
s_cselect_b32 s80,5,3
s_mul_i32 s80,s80,163840
v_mfma_f32_16x16x32_bf16 a[56:59],a[120:123],v[104:107],a[56:59]
s_add_u32 s80,s80,s81
s_add_u32 s83,s80,20480
v_mfma_f32_16x16x32_bf16 a[56:59],a[124:127],v[108:111],a[56:59]
v_mfma_f32_16x16x32_bf16 a[60:63],a[124:127],v[124:127],a[60:63]
ds_read_b64_tr_b16 a[124:125],v2 offset:30816
ds_read_b64_tr_b16 a[126:127],v2 offset:35936
v_mfma_f32_16x16x32_bf16 a[60:63],a[120:123],v[120:123],a[60:63]
buffer_load_dwordx4 v[184:187],v6,s[28:31],s79 offen offset:1024 sc1
v_mfma_f32_16x16x32_bf16 a[60:63],a[116:119],v[116:119],a[60:63]
ds_read_b64_tr_b16 a[116:117],v2 offset:10336
ds_read_b64_tr_b16 a[118:119],v2 offset:15456
v_mfma_f32_16x16x32_bf16 a[60:63],a[112:115],v[112:115],a[60:63]
ds_read_b64_tr_b16 a[112:113],v2 offset:96
v_mfma_f32_16x16x32_bf16 a[64:67],a[128:131],v[64:67],a[64:67]
ds_read_b64_tr_b16 a[114:115],v2 offset:5216
v_mfma_f32_16x16x32_bf16 a[64:67],a[132:135],v[68:71],a[64:67]
ds_read_b64_tr_b16 a[120:121],v2 offset:20576
v_mfma_f32_16x16x32_bf16 a[64:67],a[136:139],v[72:75],a[64:67]
ds_read_b64_tr_b16 a[122:123],v2 offset:25696
v_mfma_f32_16x16x32_bf16 a[64:67],a[140:143],v[76:79],a[64:67]
buffer_load_dwordx4 v[216:219],v6,s[28:31],s79 offen offset:2048 sc1
v_mfma_f32_16x16x32_bf16 a[68:71],a[140:143],v[92:95],a[68:71]
v_mfma_f32_16x16x32_bf16 a[68:71],a[136:139],v[88:91],a[68:71]
v_mfma_f32_16x16x32_bf16 a[68:71],a[132:135],v[84:87],a[68:71]
v_mfma_f32_16x16x32_bf16 a[68:71],a[128:131],v[80:83],a[68:71]
v_mfma_f32_16x16x32_bf16 a[72:75],a[128:131],v[96:99],a[72:75]
v_mfma_f32_16x16x32_bf16 a[72:75],a[132:135],v[100:103],a[72:75]
buffer_load_dwordx4 v[248:251],v6,s[28:31],s79 offen offset:3072 sc1
v_mfma_f32_16x16x32_bf16 a[72:75],a[136:139],v[104:107],a[72:75]
v_mfma_f32_16x16x32_bf16 a[72:75],a[140:143],v[108:111],a[72:75]
v_mfma_f32_16x16x32_bf16 a[76:79],a[140:143],v[124:127],a[76:79]
ds_read_b64_tr_b16 a[140:141],v1 offset:30848
ds_read_b64_tr_b16 a[142:143],v1 offset:35968
v_mfma_f32_16x16x32_bf16 a[76:79],a[136:139],v[120:123],a[76:79]
ds_read_b64_tr_b16 a[136:137],v1 offset:20608
v_mfma_f32_16x16x32_bf16 a[76:79],a[132:135],v[116:119],a[76:79]
s_add_u32 m0,s82,81920
ds_read_b64_tr_b16 a[132:133],v1 offset:10368
v_mfma_f32_16x16x32_bf16 a[76:79],a[128:131],v[112:115],a[76:79]
ds_read_b64_tr_b16 a[128:129],v1 offset:128
v_mfma_f32_16x16x32_bf16 a[80:83],a[144:147],v[64:67],a[80:83]
ds_read_b64_tr_b16 a[130:131],v1 offset:5248
ds_read_b64_tr_b16 a[134:135],v1 offset:15488
v_mfma_f32_16x16x32_bf16 a[80:83],a[148:151],v[68:71],a[80:83]
buffer_load_dwordx4 v7,s[72:75],s80 offen lds
v_mfma_f32_16x16x32_bf16 a[80:83],a[152:155],v[72:75],a[80:83]
ds_read_b64_tr_b16 a[138:139],v1 offset:25728
v_mfma_f32_16x16x32_bf16 a[80:83],a[156:159],v[76:79],a[80:83]
v_mfma_f32_16x16x32_bf16 a[84:87],a[156:159],v[92:95],a[84:87]
v_mfma_f32_16x16x32_bf16 a[84:87],a[152:155],v[88:91],a[84:87]
v_mfma_f32_16x16x32_bf16 a[84:87],a[148:151],v[84:87],a[84:87]
buffer_load_dwordx4 v8,s[72:75],s80 offen offset:1024 lds
v_mfma_f32_16x16x32_bf16 a[84:87],a[144:147],v[80:83],a[84:87]
v_mfma_f32_16x16x32_bf16 a[88:91],a[144:147],v[96:99],a[88:91]
v_mfma_f32_16x16x32_bf16 a[88:91],a[148:151],v[100:103],a[88:91]
v_mfma_f32_16x16x32_bf16 a[88:91],a[152:155],v[104:107],a[88:91]
v_mfma_f32_16x16x32_bf16 a[88:91],a[156:159],v[108:111],a[88:91]
buffer_load_dwordx4 v9,s[72:75],s80 offen offset:2048 lds
v_mfma_f32_16x16x32_bf16 a[92:95],a[156:159],v[124:127],a[92:95]
ds_read_b64_tr_b16 a[156:157],v2 offset:30880
ds_read_b64_tr_b16 a[158:159],v2 offset:36000
v_mfma_f32_16x16x32_bf16 a[92:95],a[152:155],v[120:123],a[92:95]
ds_read_b64_tr_b16 a[152:153],v2 offset:20640
v_mfma_f32_16x16x32_bf16 a[92:95],a[148:151],v[116:119],a[92:95]
ds_read_b64_tr_b16 a[148:149],v2 offset:10400
v_mfma_f32_16x16x32_bf16 a[92:95],a[144:147],v[112:115],a[92:95]
ds_read_b64_tr_b16 a[144:145],v2 offset:160
v_mfma_f32_16x16x32_bf16 a[192:195],a[160:163],v[132:135],a[192:195]
buffer_load_dwordx4 v10,s[72:75],s80 offen offset:3072 lds
v_mfma_f32_16x16x32_bf16 a[192:195],a[164:167],v[140:143],a[192:195]
ds_read_b64_tr_b16 a[146:147],v2 offset:5280
ds_read_b64_tr_b16 a[150:151],v2 offset:15520
v_mfma_f32_16x16x32_bf16 a[192:195],a[168:171],v[148:151],a[192:195]
s_add_u32 m0,s82,86016
ds_read_b64_tr_b16 a[154:155],v2 offset:25760
v_mfma_f32_16x16x32_bf16 a[192:195],a[172:175],v[156:159],a[192:195]
s_waitcnt vmcnt(12)
v_mfma_f32_16x16x32_bf16 a[196:199],a[172:175],v[188:191],a[196:199]
v_mfma_f32_16x16x32_bf16 a[196:199],a[168:171],v[180:183],a[196:199]
buffer_load_dwordx4 v11,s[72:75],s80 offen lds
v_mfma_f32_16x16x32_bf16 a[196:199],a[164:167],v[172:175],a[196:199]
v_mfma_f32_16x16x32_bf16 a[196:199],a[160:163],v[164:167],a[196:199]
s_add_u32 m0,s82,87040
v_mfma_f32_16x16x32_bf16 a[200:203],a[160:163],v[196:199],a[200:203]
v_mfma_f32_16x16x32_bf16 a[200:203],a[164:167],v[204:207],a[200:203]
v_mfma_f32_16x16x32_bf16 a[200:203],a[168:171],v[212:215],a[200:203]
buffer_load_dwordx4 v7,s[72:75],s83 offen lds
v_mfma_f32_16x16x32_bf16 a[200:203],a[172:175],v[220:223],a[200:203]
v_mfma_f32_16x16x32_bf16 a[204:207],a[172:175],v[252:255],a[204:207]
ds_read_b64_tr_b16 a[172:173],v1 offset:30912
ds_read_b64_tr_b16 a[174:175],v1 offset:36032
v_mfma_f32_16x16x32_bf16 a[204:207],a[168:171],v[244:247],a[204:207]
ds_read_b64_tr_b16 a[168:169],v1 offset:20672
v_mfma_f32_16x16x32_bf16 a[204:207],a[164:167],v[236:239],a[204:207]
ds_read_b64_tr_b16 a[164:165],v1 offset:10432
v_mfma_f32_16x16x32_bf16 a[204:207],a[160:163],v[228:231],a[204:207]
s_barrier
buffer_load_dwordx4 v8,s[72:75],s83 offen offset:1024 lds
v_mfma_f32_16x16x32_bf16 a[208:211],a[176:179],v[132:135],a[208:211]
s_cmp_ge_u32 1,s71
s_cselect_b32 s76,5,3
s_lshl_b32 s76,s76,16
v_mfma_f32_16x16x32_bf16 a[208:211],a[180:183],v[140:143],a[208:211]
s_add_u32 s77,s76,4096
s_add_u32 s78,s76,8192
s_add_u32 s79,s76,12288
v_mfma_f32_16x16x32_bf16 a[208:211],a[184:187],v[148:151],a[208:211]
s_max_i32 s76,s76,s18
s_max_i32 s77,s77,s19
s_max_i32 s78,s78,s100
v_mfma_f32_16x16x32_bf16 a[208:211],a[188:191],v[156:159],a[208:211]
s_max_i32 s79,s79,s101
buffer_load_dwordx4 v9,s[72:75],s83 offen offset:2048 lds
v_mfma_f32_16x16x32_bf16 a[212:215],a[188:191],v[188:191],a[212:215]
ds_read_b64_tr_b16 a[160:161],v1 offset:192
ds_read_b64_tr_b16 a[162:163],v1 offset:5312
v_mfma_f32_16x16x32_bf16 a[212:215],a[184:187],v[180:183],a[212:215]
)A"
R"A(ds_read_b64_tr_b16 a[166:167],v1 offset:15552
v_mfma_f32_16x16x32_bf16 a[212:215],a[180:183],v[172:175],a[212:215]
ds_read_b64_tr_b16 a[170:171],v1 offset:25792
v_mfma_f32_16x16x32_bf16 a[212:215],a[176:179],v[164:167],a[212:215]
v_mfma_f32_16x16x32_bf16 a[216:219],a[176:179],v[196:199],a[216:219]
buffer_load_dwordx4 v10,s[72:75],s83 offen offset:3072 lds
v_mfma_f32_16x16x32_bf16 a[216:219],a[180:183],v[204:207],a[216:219]
v_mfma_f32_16x16x32_bf16 a[216:219],a[184:187],v[212:215],a[216:219]
s_add_u32 m0,s82,91136
v_mfma_f32_16x16x32_bf16 a[216:219],a[188:191],v[220:223],a[216:219]
v_mfma_f32_16x16x32_bf16 a[220:223],a[188:191],v[252:255],a[220:223]
ds_read_b64_tr_b16 a[188:189],v2 offset:30944
ds_read_b64_tr_b16 a[190:191],v2 offset:36064
v_mfma_f32_16x16x32_bf16 a[220:223],a[184:187],v[244:247],a[220:223]
buffer_load_dwordx4 v11,s[72:75],s83 offen lds
v_mfma_f32_16x16x32_bf16 a[220:223],a[180:183],v[236:239],a[220:223]
ds_read_b64_tr_b16 a[180:181],v2 offset:10464
ds_read_b64_tr_b16 a[182:183],v2 offset:15584
v_mfma_f32_16x16x32_bf16 a[220:223],a[176:179],v[228:231],a[220:223]
s_waitcnt lgkmcnt(4)
ds_read_b64_tr_b16 a[176:177],v2 offset:224
v_mfma_f32_16x16x32_bf16 a[224:227],a[96:99],v[132:135],a[224:227]
ds_read_b64_tr_b16 a[178:179],v2 offset:5344
v_mfma_f32_16x16x32_bf16 a[224:227],a[100:103],v[140:143],a[224:227]
ds_read_b64_tr_b16 a[184:185],v2 offset:20704
v_mfma_f32_16x16x32_bf16 a[224:227],a[104:107],v[148:151],a[224:227]
ds_read_b64_tr_b16 a[186:187],v2 offset:25824
v_mfma_f32_16x16x32_bf16 a[224:227],a[108:111],v[156:159],a[224:227]
v_mfma_f32_16x16x32_bf16 a[228:231],a[108:111],v[188:191],a[228:231]
v_mfma_f32_16x16x32_bf16 a[228:231],a[104:107],v[180:183],a[228:231]
v_mfma_f32_16x16x32_bf16 a[228:231],a[100:103],v[172:175],a[228:231]
v_mfma_f32_16x16x32_bf16 a[228:231],a[96:99],v[164:167],a[228:231]
buffer_load_dwordx4 v[64:67],v6,s[28:31],s76 offen sc1
v_mfma_f32_16x16x32_bf16 a[232:235],a[96:99],v[196:199],a[232:235]
v_mfma_f32_16x16x32_bf16 a[232:235],a[100:103],v[204:207],a[232:235]
v_mfma_f32_16x16x32_bf16 a[232:235],a[104:107],v[212:215],a[232:235]
v_mfma_f32_16x16x32_bf16 a[232:235],a[108:111],v[220:223],a[232:235]
v_mfma_f32_16x16x32_bf16 a[236:239],a[108:111],v[252:255],a[236:239]
buffer_load_dwordx4 v[68:71],v6,s[28:31],s76 offen offset:1024 sc1
v_mfma_f32_16x16x32_bf16 a[236:239],a[104:107],v[244:247],a[236:239]
ds_read_b64_tr_b16 a[104:105],v1 offset:20736
ds_read_b64_tr_b16 a[106:107],v1 offset:25856
v_mfma_f32_16x16x32_bf16 a[236:239],a[100:103],v[236:239],a[236:239]
ds_read_b64_tr_b16 a[100:101],v1 offset:10496
v_mfma_f32_16x16x32_bf16 a[236:239],a[96:99],v[228:231],a[236:239]
ds_read_b64_tr_b16 a[96:97],v1 offset:256
v_mfma_f32_16x16x32_bf16 a[240:243],a[112:115],v[132:135],a[240:243]
ds_read_b64_tr_b16 a[98:99],v1 offset:5376
v_mfma_f32_16x16x32_bf16 a[240:243],a[116:119],v[140:143],a[240:243]
buffer_load_dwordx4 v[72:75],v6,s[28:31],s76 offen offset:2048 sc1
v_mfma_f32_16x16x32_bf16 a[240:243],a[120:123],v[148:151],a[240:243]
ds_read_b64_tr_b16 a[102:103],v1 offset:15616
ds_read_b64_tr_b16 a[108:109],v1 offset:30976
v_mfma_f32_16x16x32_bf16 a[240:243],a[124:127],v[156:159],a[240:243]
ds_read_b64_tr_b16 a[110:111],v1 offset:36096
v_add_u32 v1,40960,v1
v_mfma_f32_16x16x32_bf16 a[244:247],a[124:127],v[188:191],a[244:247]
v_add_u32 v42,40960,v1
v_add_u32 v44,81920,v1
v_mfma_f32_16x16x32_bf16 a[244:247],a[120:123],v[180:183],a[244:247]
v_mfma_f32_16x16x32_bf16 a[244:247],a[116:119],v[172:175],a[244:247]
buffer_load_dwordx4 v[76:79],v6,s[28:31],s76 offen offset:3072 sc1
v_mfma_f32_16x16x32_bf16 a[244:247],a[112:115],v[164:167],a[244:247]
v_mfma_f32_16x16x32_bf16 a[248:251],a[112:115],v[196:199],a[248:251]
v_mfma_f32_16x16x32_bf16 a[248:251],a[116:119],v[204:207],a[248:251]
v_mfma_f32_16x16x32_bf16 a[248:251],a[120:123],v[212:215],a[248:251]
v_mfma_f32_16x16x32_bf16 a[248:251],a[124:127],v[220:223],a[248:251]
buffer_load_dwordx4 v[80:83],v6,s[28:31],s77 offen sc1
v_mfma_f32_16x16x32_bf16 a[252:255],a[124:127],v[252:255],a[252:255]
ds_read_b64_tr_b16 a[124:125],v2 offset:31008
ds_read_b64_tr_b16 a[126:127],v2 offset:36128
v_mfma_f32_16x16x32_bf16 a[252:255],a[120:123],v[244:247],a[252:255]
ds_read_b64_tr_b16 a[120:121],v2 offset:20768
v_mfma_f32_16x16x32_bf16 a[252:255],a[116:119],v[236:239],a[252:255]
ds_read_b64_tr_b16 a[116:117],v2 offset:10528
v_mfma_f32_16x16x32_bf16 a[252:255],a[112:115],v[228:231],a[252:255]
ds_read_b64_tr_b16 a[112:113],v2 offset:288
v_mfma_f32_16x16x32_bf16 a[0:3],a[128:131],v[132:135],a[0:3]
buffer_load_dwordx4 v[84:87],v6,s[28:31],s77 offen offset:1024 sc1
v_mfma_f32_16x16x32_bf16 a[0:3],a[132:135],v[140:143],a[0:3]
ds_read_b64_tr_b16 a[114:115],v2 offset:5408
ds_read_b64_tr_b16 a[118:119],v2 offset:15648
v_mfma_f32_16x16x32_bf16 a[0:3],a[136:139],v[148:151],a[0:3]
ds_read_b64_tr_b16 a[122:123],v2 offset:25888
v_add_u32 v2,40960,v2
v_mfma_f32_16x16x32_bf16 a[0:3],a[140:143],v[156:159],a[0:3]
v_add_u32 v43,40960,v2
v_add_u32 v45,81920,v2
v_mfma_f32_16x16x32_bf16 a[4:7],a[140:143],v[188:191],a[4:7]
v_mfma_f32_16x16x32_bf16 a[4:7],a[136:139],v[180:183],a[4:7]
buffer_load_dwordx4 v[88:91],v6,s[28:31],s77 offen offset:2048 sc1
v_mfma_f32_16x16x32_bf16 a[4:7],a[132:135],v[172:175],a[4:7]
v_mfma_f32_16x16x32_bf16 a[4:7],a[128:131],v[164:167],a[4:7]
v_mfma_f32_16x16x32_bf16 a[8:11],a[128:131],v[196:199],a[8:11]
v_mfma_f32_16x16x32_bf16 a[8:11],a[132:135],v[204:207],a[8:11]
v_mfma_f32_16x16x32_bf16 a[8:11],a[136:139],v[212:215],a[8:11]
buffer_load_dwordx4 v[92:95],v6,s[28:31],s77 offen offset:3072 sc1
v_mfma_f32_16x16x32_bf16 a[8:11],a[140:143],v[220:223],a[8:11]
v_mfma_f32_16x16x32_bf16 a[12:15],a[140:143],v[252:255],a[12:15]
ds_read_b64_tr_b16 a[140:141],v1 offset:30720
ds_read_b64_tr_b16 a[142:143],v1 offset:35840
v_mfma_f32_16x16x32_bf16 a[12:15],a[136:139],v[244:247],a[12:15]
ds_read_b64_tr_b16 a[136:137],v1 offset:20480
v_mfma_f32_16x16x32_bf16 a[12:15],a[132:135],v[236:239],a[12:15]
ds_read_b64_tr_b16 a[132:133],v1 offset:10240
v_mfma_f32_16x16x32_bf16 a[12:15],a[128:131],v[228:231],a[12:15]
buffer_load_dwordx4 v[96:99],v6,s[28:31],s78 offen sc1
v_mfma_f32_16x16x32_bf16 a[16:19],a[144:147],v[132:135],a[16:19]
ds_read_b64_tr_b16 a[128:129],v1
ds_read_b64_tr_b16 a[130:131],v1 offset:5120
v_mfma_f32_16x16x32_bf16 a[16:19],a[148:151],v[140:143],a[16:19]
ds_read_b64_tr_b16 a[134:135],v1 offset:15360
v_mfma_f32_16x16x32_bf16 a[16:19],a[152:155],v[148:151],a[16:19]
ds_read_b64_tr_b16 a[138:139],v1 offset:25600
v_mfma_f32_16x16x32_bf16 a[16:19],a[156:159],v[156:159],a[16:19]
v_mfma_f32_16x16x32_bf16 a[20:23],a[156:159],v[188:191],a[20:23]
buffer_load_dwordx4 v[100:103],v6,s[28:31],s78 offen offset:1024 sc1
v_mfma_f32_16x16x32_bf16 a[20:23],a[152:155],v[180:183],a[20:23]
v_mfma_f32_16x16x32_bf16 a[20:23],a[148:151],v[172:175],a[20:23]
v_mfma_f32_16x16x32_bf16 a[20:23],a[144:147],v[164:167],a[20:23]
v_mfma_f32_16x16x32_bf16 a[24:27],a[144:147],v[196:199],a[24:27]
v_mfma_f32_16x16x32_bf16 a[24:27],a[148:151],v[204:207],a[24:27]
buffer_load_dwordx4 v[104:107],v6,s[28:31],s78 offen offset:2048 sc1
v_mfma_f32_16x16x32_bf16 a[24:27],a[152:155],v[212:215],a[24:27]
v_mfma_f32_16x16x32_bf16 a[24:27],a[156:159],v[220:223],a[24:27]
v_mfma_f32_16x16x32_bf16 a[28:31],a[156:159],v[252:255],a[28:31]
ds_read_b64_tr_b16 a[156:157],v2 offset:30752
ds_read_b64_tr_b16 a[158:159],v2 offset:35872
v_mfma_f32_16x16x32_bf16 a[28:31],a[152:155],v[244:247],a[28:31]
ds_read_b64_tr_b16 a[152:153],v2 offset:20512
v_mfma_f32_16x16x32_bf16 a[28:31],a[148:151],v[236:239],a[28:31]
buffer_load_dwordx4 v[108:111],v6,s[28:31],s78 offen offset:3072 sc1
v_mfma_f32_16x16x32_bf16 a[28:31],a[144:147],v[228:231],a[28:31]
s_cmp_ge_u32 2,s71
s_cselect_b32 s80,7,5
s_mul_i32 s80,s80,163840
v_mfma_f32_16x16x32_bf16 a[32:35],a[160:163],v[132:135],a[32:35]
s_add_u32 s80,s80,s81
s_add_u32 s83,s80,20480
ds_read_b64_tr_b16 a[144:145],v2 offset:32
v_mfma_f32_16x16x32_bf16 a[32:35],a[164:167],v[140:143],a[32:35]
ds_read_b64_tr_b16 a[146:147],v2 offset:5152
ds_read_b64_tr_b16 a[148:149],v2 offset:10272
v_mfma_f32_16x16x32_bf16 a[32:35],a[168:171],v[148:151],a[32:35]
ds_read_b64_tr_b16 a[150:151],v2 offset:15392
v_mfma_f32_16x16x32_bf16 a[32:35],a[172:175],v[156:159],a[32:35]
buffer_load_dwordx4 v[112:115],v6,s[28:31],s79 offen sc1
v_mfma_f32_16x16x32_bf16 a[36:39],a[172:175],v[188:191],a[36:39]
ds_read_b64_tr_b16 a[154:155],v2 offset:25632
v_mfma_f32_16x16x32_bf16 a[36:39],a[168:171],v[180:183],a[36:39]
v_mfma_f32_16x16x32_bf16 a[36:39],a[164:167],v[172:175],a[36:39]
v_mfma_f32_16x16x32_bf16 a[36:39],a[160:163],v[164:167],a[36:39]
v_mfma_f32_16x16x32_bf16 a[40:43],a[160:163],v[196:199],a[40:43]
buffer_load_dwordx4 v[116:119],v6,s[28:31],s79 offen offset:1024 sc1
v_mfma_f32_16x16x32_bf16 a[40:43],a[164:167],v[204:207],a[40:43]
v_mfma_f32_16x16x32_bf16 a[40:43],a[168:171],v[212:215],a[40:43]
v_mfma_f32_16x16x32_bf16 a[40:43],a[172:175],v[220:223],a[40:43]
v_mfma_f32_16x16x32_bf16 a[44:47],a[172:175],v[252:255],a[44:47]
ds_read_b64_tr_b16 a[172:173],v1 offset:30784
ds_read_b64_tr_b16 a[174:175],v1 offset:35904
v_mfma_f32_16x16x32_bf16 a[44:47],a[168:171],v[244:247],a[44:47]
buffer_load_dwordx4 v[120:123],v6,s[28:31],s79 offen offset:2048 sc1
v_mfma_f32_16x16x32_bf16 a[44:47],a[164:167],v[236:239],a[44:47]
ds_read_b64_tr_b16 a[164:165],v1 offset:10304
ds_read_b64_tr_b16 a[166:167],v1 offset:15424
v_mfma_f32_16x16x32_bf16 a[44:47],a[160:163],v[228:231],a[44:47]
s_waitcnt lgkmcnt(4)
ds_read_b64_tr_b16 a[160:161],v1 offset:64
v_mfma_f32_16x16x32_bf16 a[48:51],a[176:179],v[132:135],a[48:51]
ds_read_b64_tr_b16 a[162:163],v1 offset:5184
v_mfma_f32_16x16x32_bf16 a[48:51],a[180:183],v[140:143],a[48:51]
ds_read_b64_tr_b16 a[168:169],v1 offset:20544
v_mfma_f32_16x16x32_bf16 a[48:51],a[184:187],v[148:151],a[48:51]
buffer_load_dwordx4 v[124:127],v6,s[28:31],s79 offen offset:3072 sc1
v_mfma_f32_16x16x32_bf16 a[48:51],a[188:191],v[156:159],a[48:51]
ds_read_b64_tr_b16 a[170:171],v1 offset:25664
v_mfma_f32_16x16x32_bf16 a[52:55],a[188:191],v[188:191],a[52:55]
s_add_u32 m0,s82,122880
v_mfma_f32_16x16x32_bf16 a[52:55],a[184:187],v[180:183],a[52:55]
v_mfma_f32_16x16x32_bf16 a[52:55],a[180:183],v[172:175],a[52:55]
v_mfma_f32_16x16x32_bf16 a[52:55],a[176:179],v[164:167],a[52:55]
buffer_load_dwordx4 v7,s[72:75],s80 offen lds
v_mfma_f32_16x16x32_bf16 a[56:59],a[176:179],v[196:199],a[56:59]
v_mfma_f32_16x16x32_bf16 a[56:59],a[180:183],v[204:207],a[56:59]
v_mfma_f32_16x16x32_bf16 a[56:59],a[184:187],v[212:215],a[56:59]
v_mfma_f32_16x16x32_bf16 a[56:59],a[188:191],v[220:223],a[56:59]
v_mfma_f32_16x16x32_bf16 a[60:63],a[188:191],v[252:255],a[60:63]
buffer_load_dwordx4 v8,s[72:75],s80 offen offset:1024 lds
v_mfma_f32_16x16x32_bf16 a[60:63],a[184:187],v[244:247],a[60:63]
ds_read_b64_tr_b16 a[184:185],v2 offset:20576
ds_read_b64_tr_b16 a[186:187],v2 offset:25696
v_mfma_f32_16x16x32_bf16 a[60:63],a[180:183],v[236:239],a[60:63]
ds_read_b64_tr_b16 a[180:181],v2 offset:10336
v_mfma_f32_16x16x32_bf16 a[60:63],a[176:179],v[228:231],a[60:63]
ds_read_b64_tr_b16 a[176:177],v2 offset:96
v_mfma_f32_16x16x32_bf16 a[64:67],a[96:99],v[132:135],a[64:67]
ds_read_b64_tr_b16 a[178:179],v2 offset:5216
v_mfma_f32_16x16x32_bf16 a[64:67],a[100:103],v[140:143],a[64:67]
buffer_load_dwordx4 v9,s[72:75],s80 offen offset:2048 lds
v_mfma_f32_16x16x32_bf16 a[64:67],a[104:107],v[148:151],a[64:67]
ds_read_b64_tr_b16 a[182:183],v2 offset:15456
ds_read_b64_tr_b16 a[188:189],v2 offset:30816
v_mfma_f32_16x16x32_bf16 a[64:67],a[108:111],v[156:159],a[64:67]
ds_read_b64_tr_b16 a[190:191],v2 offset:35936
v_mfma_f32_16x16x32_bf16 a[68:71],a[108:111],v[188:191],a[68:71]
v_mfma_f32_16x16x32_bf16 a[68:71],a[104:107],v[180:183],a[68:71]
v_mfma_f32_16x16x32_bf16 a[68:71],a[100:103],v[172:175],a[68:71]
buffer_load_dwordx4 v10,s[72:75],s80 offen offset:3072 lds
v_mfma_f32_16x16x32_bf16 a[68:71],a[96:99],v[164:167],a[68:71]
v_mfma_f32_16x16x32_bf16 a[72:75],a[96:99],v[196:199],a[72:75]
s_add_u32 m0,s82,126976
v_mfma_f32_16x16x32_bf16 a[72:75],a[100:103],v[204:207],a[72:75]
)A"
R"A(v_mfma_f32_16x16x32_bf16 a[72:75],a[104:107],v[212:215],a[72:75]
s_cmp_ge_u32 2,s71
s_cselect_b32 s76,7,5
s_lshl_b32 s76,s76,16
v_mfma_f32_16x16x32_bf16 a[72:75],a[108:111],v[220:223],a[72:75]
s_add_u32 s77,s76,4096
buffer_load_dwordx4 v11,s[72:75],s80 offen lds
v_mfma_f32_16x16x32_bf16 a[76:79],a[108:111],v[252:255],a[76:79]
s_add_u32 s78,s76,8192
s_add_u32 s79,s76,12288
s_max_i32 s76,s76,s18
v_mfma_f32_16x16x32_bf16 a[76:79],a[104:107],v[244:247],a[76:79]
s_add_u32 m0,s82,128000
s_max_i32 s77,s77,s19
ds_read_b64_tr_b16 a[104:105],v1 offset:20608
v_mfma_f32_16x16x32_bf16 a[76:79],a[100:103],v[236:239],a[76:79]
s_max_i32 s78,s78,s100
ds_read_b64_tr_b16 a[100:101],v1 offset:10368
ds_read_b64_tr_b16 a[102:103],v1 offset:15488
v_mfma_f32_16x16x32_bf16 a[76:79],a[96:99],v[228:231],a[76:79]
s_max_i32 s79,s79,s101
ds_read_b64_tr_b16 a[96:97],v1 offset:128
v_mfma_f32_16x16x32_bf16 a[80:83],a[112:115],v[132:135],a[80:83]
buffer_load_dwordx4 v7,s[72:75],s83 offen lds
v_mfma_f32_16x16x32_bf16 a[80:83],a[116:119],v[140:143],a[80:83]
ds_read_b64_tr_b16 a[98:99],v1 offset:5248
ds_read_b64_tr_b16 a[106:107],v1 offset:25728
v_mfma_f32_16x16x32_bf16 a[80:83],a[120:123],v[148:151],a[80:83]
ds_read_b64_tr_b16 a[108:109],v1 offset:30848
v_mfma_f32_16x16x32_bf16 a[80:83],a[124:127],v[156:159],a[80:83]
ds_read_b64_tr_b16 a[110:111],v1 offset:35968
v_mfma_f32_16x16x32_bf16 a[84:87],a[124:127],v[188:191],a[84:87]
v_mfma_f32_16x16x32_bf16 a[84:87],a[120:123],v[180:183],a[84:87]
buffer_load_dwordx4 v8,s[72:75],s83 offen offset:1024 lds
v_mfma_f32_16x16x32_bf16 a[84:87],a[116:119],v[172:175],a[84:87]
v_mfma_f32_16x16x32_bf16 a[84:87],a[112:115],v[164:167],a[84:87]
v_mfma_f32_16x16x32_bf16 a[88:91],a[112:115],v[196:199],a[88:91]
v_mfma_f32_16x16x32_bf16 a[88:91],a[116:119],v[204:207],a[88:91]
v_mfma_f32_16x16x32_bf16 a[88:91],a[120:123],v[212:215],a[88:91]
buffer_load_dwordx4 v9,s[72:75],s83 offen offset:2048 lds
v_mfma_f32_16x16x32_bf16 a[88:91],a[124:127],v[220:223],a[88:91]
v_mfma_f32_16x16x32_bf16 a[92:95],a[124:127],v[252:255],a[92:95]
ds_read_b64_tr_b16 a[124:125],v2 offset:30880
ds_read_b64_tr_b16 a[126:127],v2 offset:36000
v_mfma_f32_16x16x32_bf16 a[92:95],a[120:123],v[244:247],a[92:95]
ds_read_b64_tr_b16 a[120:121],v2 offset:20640
v_mfma_f32_16x16x32_bf16 a[92:95],a[116:119],v[236:239],a[92:95]
ds_read_b64_tr_b16 a[116:117],v2 offset:10400
v_mfma_f32_16x16x32_bf16 a[92:95],a[112:115],v[228:231],a[92:95]
buffer_load_dwordx4 v10,s[72:75],s83 offen offset:3072 lds
v_mfma_f32_16x16x32_bf16 a[192:195],a[128:131],v[128:131],a[192:195]
ds_read_b64_tr_b16 a[112:113],v2 offset:160
ds_read_b64_tr_b16 a[114:115],v2 offset:5280
v_mfma_f32_16x16x32_bf16 a[192:195],a[132:135],v[160:163],a[192:195]
s_add_u32 m0,s82,132096
ds_read_b64_tr_b16 a[118:119],v2 offset:15520
v_mfma_f32_16x16x32_bf16 a[192:195],a[136:139],v[192:195],a[192:195]
ds_read_b64_tr_b16 a[122:123],v2 offset:25760
v_mfma_f32_16x16x32_bf16 a[192:195],a[140:143],v[224:227],a[192:195]
buffer_load_dwordx4 v[132:135],v6,s[28:31],s76 offen sc1
v_mfma_f32_16x16x32_bf16 a[196:199],a[140:143],v[232:235],a[196:199]
v_mfma_f32_16x16x32_bf16 a[196:199],a[136:139],v[200:203],a[196:199]
v_mfma_f32_16x16x32_bf16 a[196:199],a[132:135],v[168:171],a[196:199]
v_mfma_f32_16x16x32_bf16 a[196:199],a[128:131],v[136:139],a[196:199]
s_waitcnt vmcnt(14)
buffer_load_dwordx4 v11,s[72:75],s83 offen lds
v_mfma_f32_16x16x32_bf16 a[200:203],a[128:131],v[144:147],a[200:203]
ds_read_b64_tr_b16 a[128:129],v1 offset:192
ds_read_b64_tr_b16 a[130:131],v1 offset:5312
v_mfma_f32_16x16x32_bf16 a[200:203],a[132:135],v[176:179],a[200:203]
ds_read_b64_tr_b16 a[132:133],v1 offset:10432
v_mfma_f32_16x16x32_bf16 a[200:203],a[136:139],v[208:211],a[200:203]
ds_read_b64_tr_b16 a[134:135],v1 offset:15552
v_mfma_f32_16x16x32_bf16 a[200:203],a[140:143],v[240:243],a[200:203]
buffer_load_dwordx4 v[140:143],v6,s[28:31],s76 offen offset:1024 sc1
v_mfma_f32_16x16x32_bf16 a[208:211],a[144:147],v[128:131],a[208:211]
ds_read_b64_tr_b16 a[136:137],v1 offset:20672
ds_read_b64_tr_b16 a[138:139],v1 offset:25792
v_mfma_f32_16x16x32_bf16 a[208:211],a[148:151],v[160:163],a[208:211]
ds_read_b64_tr_b16 a[140:141],v1 offset:30912
v_mfma_f32_16x16x32_bf16 a[208:211],a[152:155],v[192:195],a[208:211]
ds_read_b64_tr_b16 a[142:143],v1 offset:36032
v_mfma_f32_16x16x32_bf16 a[208:211],a[156:159],v[224:227],a[208:211]
buffer_load_dwordx4 v[148:151],v6,s[28:31],s76 offen offset:2048 sc1
v_mfma_f32_16x16x32_bf16 a[212:215],a[156:159],v[232:235],a[212:215]
v_mfma_f32_16x16x32_bf16 a[212:215],a[152:155],v[200:203],a[212:215]
v_mfma_f32_16x16x32_bf16 a[212:215],a[148:151],v[168:171],a[212:215]
v_mfma_f32_16x16x32_bf16 a[212:215],a[144:147],v[136:139],a[212:215]
buffer_load_dwordx4 v[156:159],v6,s[28:31],s76 offen offset:3072 sc1
v_mfma_f32_16x16x32_bf16 a[216:219],a[144:147],v[144:147],a[216:219]
s_mul_i32 s76,s70,12
ds_read_b64_tr_b16 a[144:145],v2 offset:224
ds_read_b64_tr_b16 a[146:147],v2 offset:5344
v_mfma_f32_16x16x32_bf16 a[216:219],a[148:151],v[176:179],a[216:219]
s_add_u32 s76,s76,0
s_add_u32 s76,s76,s40
ds_read_b64_tr_b16 a[148:149],v2 offset:10464
v_mfma_f32_16x16x32_bf16 a[216:219],a[152:155],v[208:211],a[216:219]
s_mul_i32 s76,s76,5120
s_max_i32 s76,s76,s101
ds_read_b64_tr_b16 a[150:151],v2 offset:15584
v_mfma_f32_16x16x32_bf16 a[216:219],a[156:159],v[240:243],a[216:219]
s_waitcnt lgkmcnt(4)
buffer_load_dwordx4 v[164:167],v6,s[28:31],s77 offen sc1
v_mfma_f32_16x16x32_bf16 a[224:227],a[160:163],v[128:131],a[224:227]
ds_read_b64_tr_b16 a[152:153],v2 offset:20704
ds_read_b64_tr_b16 a[154:155],v2 offset:25824
v_mfma_f32_16x16x32_bf16 a[224:227],a[164:167],v[160:163],a[224:227]
ds_read_b64_tr_b16 a[156:157],v2 offset:30944
v_mfma_f32_16x16x32_bf16 a[224:227],a[168:171],v[192:195],a[224:227]
ds_read_b64_tr_b16 a[158:159],v2 offset:36064
v_mfma_f32_16x16x32_bf16 a[224:227],a[172:175],v[224:227],a[224:227]
buffer_load_dwordx4 v[172:175],v6,s[28:31],s77 offen offset:1024 sc1
v_mfma_f32_16x16x32_bf16 a[228:231],a[172:175],v[232:235],a[228:231]
v_mfma_f32_16x16x32_bf16 a[228:231],a[168:171],v[200:203],a[228:231]
v_mfma_f32_16x16x32_bf16 a[228:231],a[164:167],v[168:171],a[228:231]
v_mfma_f32_16x16x32_bf16 a[228:231],a[160:163],v[136:139],a[228:231]
v_mfma_f32_16x16x32_bf16 a[232:235],a[160:163],v[144:147],a[232:235]
buffer_load_dwordx4 v[180:183],v6,s[28:31],s77 offen offset:2048 sc1
v_mfma_f32_16x16x32_bf16 a[232:235],a[164:167],v[176:179],a[232:235]
ds_read_b64_tr_b16 a[160:161],v1 offset:256
ds_read_b64_tr_b16 a[162:163],v1 offset:5376
v_mfma_f32_16x16x32_bf16 a[232:235],a[168:171],v[208:211],a[232:235]
ds_read_b64_tr_b16 a[164:165],v1 offset:10496
v_mfma_f32_16x16x32_bf16 a[232:235],a[172:175],v[240:243],a[232:235]
ds_read_b64_tr_b16 a[166:167],v1 offset:15616
v_mfma_f32_16x16x32_bf16 a[240:243],a[176:179],v[128:131],a[240:243]
ds_read_b64_tr_b16 a[168:169],v1 offset:20736
v_mfma_f32_16x16x32_bf16 a[240:243],a[180:183],v[160:163],a[240:243]
buffer_load_dwordx4 v[188:191],v6,s[28:31],s77 offen offset:3072 sc1
v_mfma_f32_16x16x32_bf16 a[240:243],a[184:187],v[192:195],a[240:243]
s_mul_i32 s77,s70,12
ds_read_b64_tr_b16 a[170:171],v1 offset:25856
ds_read_b64_tr_b16 a[172:173],v1 offset:30976
v_mfma_f32_16x16x32_bf16 a[240:243],a[188:191],v[224:227],a[240:243]
s_add_u32 s77,s77,4
s_add_u32 s77,s77,s40
ds_read_b64_tr_b16 a[174:175],v1 offset:36096
v_mfma_f32_16x16x32_bf16 a[244:247],a[188:191],v[232:235],a[244:247]
s_mul_i32 s77,s77,5120
s_max_i32 s77,s77,s101
v_mfma_f32_16x16x32_bf16 a[244:247],a[184:187],v[200:203],a[244:247]
v_mfma_f32_16x16x32_bf16 a[244:247],a[180:183],v[168:171],a[244:247]
buffer_load_dwordx4 v[196:199],v6,s[28:31],s78 offen sc1
v_mfma_f32_16x16x32_bf16 a[244:247],a[176:179],v[136:139],a[244:247]
v_mfma_f32_16x16x32_bf16 a[248:251],a[176:179],v[144:147],a[248:251]
ds_read_b64_tr_b16 a[176:177],v2 offset:288
ds_read_b64_tr_b16 a[178:179],v2 offset:5408
v_mfma_f32_16x16x32_bf16 a[248:251],a[180:183],v[176:179],a[248:251]
ds_read_b64_tr_b16 a[180:181],v2 offset:10528
v_mfma_f32_16x16x32_bf16 a[248:251],a[184:187],v[208:211],a[248:251]
ds_read_b64_tr_b16 a[182:183],v2 offset:15648
v_mfma_f32_16x16x32_bf16 a[248:251],a[188:191],v[240:243],a[248:251]
buffer_load_dwordx4 v[204:207],v6,s[28:31],s78 offen offset:1024 sc1
v_mfma_f32_16x16x32_bf16 a[0:3],a[96:99],v[128:131],a[0:3]
ds_read_b64_tr_b16 a[184:185],v2 offset:20768
ds_read_b64_tr_b16 a[186:187],v2 offset:25888
v_mfma_f32_16x16x32_bf16 a[0:3],a[100:103],v[160:163],a[0:3]
ds_read_b64_tr_b16 a[188:189],v2 offset:31008
v_mfma_f32_16x16x32_bf16 a[0:3],a[104:107],v[192:195],a[0:3]
ds_read_b64_tr_b16 a[190:191],v2 offset:36128
v_mfma_f32_16x16x32_bf16 a[0:3],a[108:111],v[224:227],a[0:3]
v_mfma_f32_16x16x32_bf16 a[4:7],a[108:111],v[232:235],a[4:7]
buffer_load_dwordx4 v[212:215],v6,s[28:31],s78 offen offset:2048 sc1
v_mfma_f32_16x16x32_bf16 a[4:7],a[104:107],v[200:203],a[4:7]
v_mfma_f32_16x16x32_bf16 a[4:7],a[100:103],v[168:171],a[4:7]
v_mfma_f32_16x16x32_bf16 a[4:7],a[96:99],v[136:139],a[4:7]
v_mfma_f32_16x16x32_bf16 a[8:11],a[96:99],v[144:147],a[8:11]
v_mfma_f32_16x16x32_bf16 a[8:11],a[100:103],v[176:179],a[8:11]
buffer_load_dwordx4 v[220:223],v6,s[28:31],s78 offen offset:3072 sc1
v_mfma_f32_16x16x32_bf16 a[8:11],a[104:107],v[208:211],a[8:11]
s_mul_i32 s78,s70,12
s_add_u32 s78,s78,8
s_add_u32 s78,s78,s40
v_mfma_f32_16x16x32_bf16 a[8:11],a[108:111],v[240:243],a[8:11]
s_mul_i32 s78,s78,5120
s_max_i32 s78,s78,s101
v_mfma_f32_16x16x32_bf16 a[16:19],a[112:115],v[128:131],a[16:19]
v_mfma_f32_16x16x32_bf16 a[16:19],a[116:119],v[160:163],a[16:19]
v_mfma_f32_16x16x32_bf16 a[16:19],a[120:123],v[192:195],a[16:19]
buffer_load_dwordx4 v[228:231],v6,s[28:31],s79 offen sc1
v_mfma_f32_16x16x32_bf16 a[16:19],a[124:127],v[224:227],a[16:19]
v_mfma_f32_16x16x32_bf16 a[20:23],a[124:127],v[232:235],a[20:23]
v_mfma_f32_16x16x32_bf16 a[20:23],a[120:123],v[200:203],a[20:23]
v_mfma_f32_16x16x32_bf16 a[20:23],a[116:119],v[168:171],a[20:23]
v_mfma_f32_16x16x32_bf16 a[20:23],a[112:115],v[136:139],a[20:23]
buffer_load_dwordx4 v[236:239],v6,s[28:31],s79 offen offset:1024 sc1
v_mfma_f32_16x16x32_bf16 a[24:27],a[112:115],v[144:147],a[24:27]
v_mfma_f32_16x16x32_bf16 a[24:27],a[116:119],v[176:179],a[24:27]
v_mfma_f32_16x16x32_bf16 a[24:27],a[120:123],v[208:211],a[24:27]
v_mfma_f32_16x16x32_bf16 a[24:27],a[124:127],v[240:243],a[24:27]
v_mfma_f32_16x16x32_bf16 a[32:35],a[128:131],v[128:131],a[32:35]
buffer_load_dwordx4 v[244:247],v6,s[28:31],s79 offen offset:2048 sc1
v_mfma_f32_16x16x32_bf16 a[32:35],a[132:135],v[160:163],a[32:35]
v_mfma_f32_16x16x32_bf16 a[32:35],a[136:139],v[192:195],a[32:35]
v_mfma_f32_16x16x32_bf16 a[32:35],a[140:143],v[224:227],a[32:35]
v_mfma_f32_16x16x32_bf16 a[36:39],a[140:143],v[232:235],a[36:39]
v_mfma_f32_16x16x32_bf16 a[36:39],a[136:139],v[200:203],a[36:39]
buffer_load_dwordx4 v[252:255],v6,s[28:31],s79 offen offset:3072 sc1
v_mfma_f32_16x16x32_bf16 a[36:39],a[132:135],v[168:171],a[36:39]
v_mfma_f32_16x16x32_bf16 a[36:39],a[128:131],v[136:139],a[36:39]
v_mfma_f32_16x16x32_bf16 a[40:43],a[128:131],v[144:147],a[40:43]
v_mfma_f32_16x16x32_bf16 a[40:43],a[132:135],v[176:179],a[40:43]
v_mfma_f32_16x16x32_bf16 a[40:43],a[136:139],v[208:211],a[40:43]
s_waitcnt vmcnt(15)
v_mfma_f32_16x16x32_bf16 a[40:43],a[140:143],v[240:243],a[40:43]
s_barrier
ds_read_b64_tr_b16 a[96:97],v42
ds_read_b64_tr_b16 a[98:99],v42 offset:5120
s_waitcnt lgkmcnt(2)
v_mfma_f32_16x16x32_bf16 a[48:51],a[144:147],v[128:131],a[48:51]
ds_read_b64_tr_b16 a[100:101],v42 offset:10240
v_mfma_f32_16x16x32_bf16 a[48:51],a[148:151],v[160:163],a[48:51]
ds_read_b64_tr_b16 a[102:103],v42 offset:15360
v_mfma_f32_16x16x32_bf16 a[48:51],a[152:155],v[192:195],a[48:51]
ds_read_b64_tr_b16 a[104:105],v42 offset:20480
v_mfma_f32_16x16x32_bf16 a[48:51],a[156:159],v[224:227],a[48:51]
ds_read_b64_tr_b16 a[106:107],v42 offset:25600
ds_read_b64_tr_b16 a[108:109],v42 offset:30720
v_mfma_f32_16x16x32_bf16 a[52:55],a[156:159],v[232:235],a[52:55]
ds_read_b64_tr_b16 a[110:111],v42 offset:35840
v_mfma_f32_16x16x32_bf16 a[52:55],a[152:155],v[200:203],a[52:55]
)A"
R"A(ds_read_b64_tr_b16 a[112:113],v44
v_mfma_f32_16x16x32_bf16 a[52:55],a[148:151],v[168:171],a[52:55]
ds_read_b64_tr_b16 a[114:115],v44 offset:5120
v_mfma_f32_16x16x32_bf16 a[52:55],a[144:147],v[136:139],a[52:55]
ds_read_b64_tr_b16 a[116:117],v44 offset:10240
ds_read_b64_tr_b16 a[118:119],v44 offset:15360
v_mfma_f32_16x16x32_bf16 a[56:59],a[144:147],v[144:147],a[56:59]
ds_read_b64_tr_b16 a[120:121],v44 offset:20480
v_mfma_f32_16x16x32_bf16 a[56:59],a[148:151],v[176:179],a[56:59]
ds_read_b64_tr_b16 a[122:123],v44 offset:25600
v_mfma_f32_16x16x32_bf16 a[56:59],a[152:155],v[208:211],a[56:59]
ds_read_b64_tr_b16 a[124:125],v44 offset:30720
v_mfma_f32_16x16x32_bf16 a[56:59],a[156:159],v[240:243],a[56:59]
ds_read_b64_tr_b16 a[126:127],v44 offset:35840
ds_read_b64_tr_b16 a[128:129],v43 offset:32
v_mfma_f32_16x16x32_bf16 a[64:67],a[160:163],v[128:131],a[64:67]
ds_read_b64_tr_b16 a[130:131],v43 offset:5152
v_mfma_f32_16x16x32_bf16 a[64:67],a[164:167],v[160:163],a[64:67]
ds_read_b64_tr_b16 a[132:133],v43 offset:10272
v_mfma_f32_16x16x32_bf16 a[64:67],a[168:171],v[192:195],a[64:67]
ds_read_b64_tr_b16 a[134:135],v43 offset:15392
v_mfma_f32_16x16x32_bf16 a[64:67],a[172:175],v[224:227],a[64:67]
ds_read_b64_tr_b16 a[136:137],v43 offset:20512
ds_read_b64_tr_b16 a[138:139],v43 offset:25632
v_mfma_f32_16x16x32_bf16 a[68:71],a[172:175],v[232:235],a[68:71]
ds_read_b64_tr_b16 a[140:141],v43 offset:30752
v_mfma_f32_16x16x32_bf16 a[68:71],a[168:171],v[200:203],a[68:71]
ds_read_b64_tr_b16 a[142:143],v43 offset:35872
v_mfma_f32_16x16x32_bf16 a[68:71],a[164:167],v[168:171],a[68:71]
ds_read_b64_tr_b16 a[144:145],v45 offset:32
v_mfma_f32_16x16x32_bf16 a[68:71],a[160:163],v[136:139],a[68:71]
ds_read_b64_tr_b16 a[146:147],v45 offset:5152
ds_read_b64_tr_b16 a[148:149],v45 offset:10272
v_mfma_f32_16x16x32_bf16 a[72:75],a[160:163],v[144:147],a[72:75]
ds_read_b64_tr_b16 a[150:151],v45 offset:15392
v_mfma_f32_16x16x32_bf16 a[72:75],a[164:167],v[176:179],a[72:75]
ds_read_b64_tr_b16 a[152:153],v45 offset:20512
v_mfma_f32_16x16x32_bf16 a[72:75],a[168:171],v[208:211],a[72:75]
ds_read_b64_tr_b16 a[154:155],v45 offset:25632
v_mfma_f32_16x16x32_bf16 a[72:75],a[172:175],v[240:243],a[72:75]
ds_read_b64_tr_b16 a[156:157],v45 offset:30752
ds_read_b64_tr_b16 a[158:159],v45 offset:35872
v_mfma_f32_16x16x32_bf16 a[80:83],a[176:179],v[128:131],a[80:83]
ds_read_b64_tr_b16 a[160:161],v42 offset:64
v_mfma_f32_16x16x32_bf16 a[80:83],a[180:183],v[160:163],a[80:83]
ds_read_b64_tr_b16 a[162:163],v42 offset:5184
v_mfma_f32_16x16x32_bf16 a[80:83],a[184:187],v[192:195],a[80:83]
ds_read_b64_tr_b16 a[164:165],v42 offset:10304
v_mfma_f32_16x16x32_bf16 a[80:83],a[188:191],v[224:227],a[80:83]
ds_read_b64_tr_b16 a[166:167],v42 offset:15424
ds_read_b64_tr_b16 a[168:169],v42 offset:20544
v_mfma_f32_16x16x32_bf16 a[84:87],a[188:191],v[232:235],a[84:87]
ds_read_b64_tr_b16 a[170:171],v42 offset:25664
v_mfma_f32_16x16x32_bf16 a[84:87],a[184:187],v[200:203],a[84:87]
ds_read_b64_tr_b16 a[172:173],v42 offset:30784
v_mfma_f32_16x16x32_bf16 a[84:87],a[180:183],v[168:171],a[84:87]
ds_read_b64_tr_b16 a[174:175],v42 offset:35904
v_mfma_f32_16x16x32_bf16 a[84:87],a[176:179],v[136:139],a[84:87]
v_mfma_f32_16x16x32_bf16 a[88:91],a[176:179],v[144:147],a[88:91]
ds_read_b64_tr_b16 a[176:177],v44 offset:64
ds_read_b64_tr_b16 a[178:179],v44 offset:5184
v_mfma_f32_16x16x32_bf16 a[88:91],a[180:183],v[176:179],a[88:91]
ds_read_b64_tr_b16 a[180:181],v44 offset:10304
v_mfma_f32_16x16x32_bf16 a[88:91],a[184:187],v[208:211],a[88:91]
ds_read_b64_tr_b16 a[182:183],v44 offset:15424
v_mfma_f32_16x16x32_bf16 a[88:91],a[188:191],v[240:243],a[88:91]
s_waitcnt lgkmcnt(7)
ds_read_b64_tr_b16 a[184:185],v44 offset:20544
v_mfma_f32_16x16x32_bf16 a[192:195],a[96:99],v[64:67],a[192:195]
ds_read_b64_tr_b16 a[186:187],v44 offset:25664
ds_read_b64_tr_b16 a[188:189],v44 offset:30784
v_mfma_f32_16x16x32_bf16 a[192:195],a[100:103],v[68:71],a[192:195]
ds_read_b64_tr_b16 a[190:191],v44 offset:35904
v_mfma_f32_16x16x32_bf16 a[192:195],a[104:107],v[72:75],a[192:195]
v_mfma_f32_16x16x32_bf16 a[192:195],a[108:111],v[76:79],a[192:195]
v_mfma_f32_16x16x32_bf16 a[196:199],a[108:111],v[92:95],a[196:199]
v_mfma_f32_16x16x32_bf16 a[196:199],a[104:107],v[88:91],a[196:199]
v_mfma_f32_16x16x32_bf16 a[196:199],a[100:103],v[84:87],a[196:199]
v_mfma_f32_16x16x32_bf16 a[196:199],a[96:99],v[80:83],a[196:199]
v_mfma_f32_16x16x32_bf16 a[200:203],a[96:99],v[96:99],a[200:203]
ds_read_b64_tr_b16 a[96:97],v43 offset:96
ds_read_b64_tr_b16 a[98:99],v43 offset:5216
v_mfma_f32_16x16x32_bf16 a[200:203],a[100:103],v[100:103],a[200:203]
ds_read_b64_tr_b16 a[100:101],v43 offset:10336
v_mfma_f32_16x16x32_bf16 a[200:203],a[104:107],v[104:107],a[200:203]
ds_read_b64_tr_b16 a[102:103],v43 offset:15456
v_mfma_f32_16x16x32_bf16 a[200:203],a[108:111],v[108:111],a[200:203]
ds_read_b64_tr_b16 a[104:105],v43 offset:20576
v_mfma_f32_16x16x32_bf16 a[192:195],a[112:115],v[132:135],a[192:195]
s_waitcnt vmcnt(3)
ds_read_b64_tr_b16 a[106:107],v43 offset:25696
ds_read_b64_tr_b16 a[108:109],v43 offset:30816
v_mfma_f32_16x16x32_bf16 a[192:195],a[116:119],v[140:143],a[192:195]
ds_read_b64_tr_b16 a[110:111],v43 offset:35936
v_mfma_f32_16x16x32_bf16 a[192:195],a[120:123],v[148:151],a[192:195]
v_mfma_f32_16x16x32_bf16 a[192:195],a[124:127],v[156:159],a[192:195]
v_mfma_f32_16x16x32_bf16 a[196:199],a[124:127],v[188:191],a[196:199]
v_mfma_f32_16x16x32_bf16 a[196:199],a[120:123],v[180:183],a[196:199]
v_mfma_f32_16x16x32_bf16 a[196:199],a[116:119],v[172:175],a[196:199]
v_mfma_f32_16x16x32_bf16 a[196:199],a[112:115],v[164:167],a[196:199]
v_mfma_f32_16x16x32_bf16 a[200:203],a[112:115],v[196:199],a[200:203]
ds_read_b64_tr_b16 a[112:113],v45 offset:96
ds_read_b64_tr_b16 a[114:115],v45 offset:5216
v_mfma_f32_16x16x32_bf16 a[200:203],a[116:119],v[204:207],a[200:203]
v_accvgpr_read_b32 v128,a192
v_accvgpr_read_b32 v129,a193
v_mfma_f32_16x16x32_bf16 a[200:203],a[120:123],v[212:215],a[200:203]
v_cvt_pk_bf16_f32 v128,v128,v129
v_accvgpr_read_b32 v130,a194
v_mfma_f32_16x16x32_bf16 a[200:203],a[124:127],v[220:223],a[200:203]
v_accvgpr_read_b32 v131,a195
v_cvt_pk_bf16_f32 v129,v130,v131
v_mfma_f32_16x16x32_bf16 a[208:211],a[128:131],v[64:67],a[208:211]
ds_read_b64_tr_b16 a[116:117],v45 offset:10336
ds_read_b64_tr_b16 a[118:119],v45 offset:15456
v_mfma_f32_16x16x32_bf16 a[208:211],a[132:135],v[68:71],a[208:211]
v_accvgpr_read_b32 v136,a196
v_accvgpr_read_b32 v137,a197
v_mfma_f32_16x16x32_bf16 a[208:211],a[136:139],v[72:75],a[208:211]
v_cvt_pk_bf16_f32 v136,v136,v137
v_accvgpr_read_b32 v138,a198
v_mfma_f32_16x16x32_bf16 a[208:211],a[140:143],v[76:79],a[208:211]
v_accvgpr_read_b32 v139,a199
v_cvt_pk_bf16_f32 v137,v138,v139
v_mfma_f32_16x16x32_bf16 a[212:215],a[140:143],v[92:95],a[212:215]
ds_read_b64_tr_b16 a[120:121],v45 offset:20576
ds_read_b64_tr_b16 a[122:123],v45 offset:25696
v_mfma_f32_16x16x32_bf16 a[212:215],a[136:139],v[88:91],a[212:215]
ds_read_b64_tr_b16 a[124:125],v45 offset:30816
v_accvgpr_read_b32 v144,a200
v_mfma_f32_16x16x32_bf16 a[212:215],a[132:135],v[84:87],a[212:215]
ds_read_b64_tr_b16 a[126:127],v45 offset:35936
v_accvgpr_read_b32 v145,a201
v_mfma_f32_16x16x32_bf16 a[212:215],a[128:131],v[80:83],a[212:215]
v_cvt_pk_bf16_f32 v144,v144,v145
v_accvgpr_read_b32 v146,a202
v_mfma_f32_16x16x32_bf16 a[216:219],a[128:131],v[96:99],a[216:219]
v_accvgpr_read_b32 v147,a203
v_cvt_pk_bf16_f32 v145,v146,v147
v_mfma_f32_16x16x32_bf16 a[216:219],a[132:135],v[100:103],a[216:219]
ds_read_b64_tr_b16 a[128:129],v42 offset:128
ds_read_b64_tr_b16 a[130:131],v42 offset:5248
v_mfma_f32_16x16x32_bf16 a[216:219],a[136:139],v[104:107],a[216:219]
ds_read_b64_tr_b16 a[132:133],v42 offset:10368
v_mfma_f32_16x16x32_bf16 a[216:219],a[140:143],v[108:111],a[216:219]
ds_read_b64_tr_b16 a[134:135],v42 offset:15488
v_mfma_f32_16x16x32_bf16 a[208:211],a[144:147],v[132:135],a[208:211]
ds_read_b64_tr_b16 a[136:137],v42 offset:20608
v_mfma_f32_16x16x32_bf16 a[208:211],a[148:151],v[140:143],a[208:211]
ds_read_b64_tr_b16 a[138:139],v42 offset:25728
ds_read_b64_tr_b16 a[140:141],v42 offset:30848
v_mfma_f32_16x16x32_bf16 a[208:211],a[152:155],v[148:151],a[208:211]
ds_read_b64_tr_b16 a[142:143],v42 offset:35968
v_mfma_f32_16x16x32_bf16 a[208:211],a[156:159],v[156:159],a[208:211]
v_mfma_f32_16x16x32_bf16 a[212:215],a[156:159],v[188:191],a[212:215]
v_mfma_f32_16x16x32_bf16 a[212:215],a[152:155],v[180:183],a[212:215]
v_mfma_f32_16x16x32_bf16 a[212:215],a[148:151],v[172:175],a[212:215]
v_mfma_f32_16x16x32_bf16 a[212:215],a[144:147],v[164:167],a[212:215]
v_mfma_f32_16x16x32_bf16 a[216:219],a[144:147],v[196:199],a[216:219]
ds_read_b64_tr_b16 a[144:145],v44 offset:128
ds_read_b64_tr_b16 a[146:147],v44 offset:5248
v_mfma_f32_16x16x32_bf16 a[216:219],a[148:151],v[204:207],a[216:219]
v_accvgpr_read_b32 v130,a208
v_accvgpr_read_b32 v131,a209
v_mfma_f32_16x16x32_bf16 a[216:219],a[152:155],v[212:215],a[216:219]
v_accvgpr_read_b32 v14,a210
v_accvgpr_read_b32 v15,a211
v_mfma_f32_16x16x32_bf16 a[216:219],a[156:159],v[220:223],a[216:219]
v_cvt_pk_bf16_f32 v130,v130,v131
v_cvt_pk_bf16_f32 v131,v14,v15
v_mfma_f32_16x16x32_bf16 a[224:227],a[160:163],v[64:67],a[224:227]
buffer_store_dwordx4 v[128:131],v6,s[32:35],s84 offen sc1
v_mfma_f32_16x16x32_bf16 a[224:227],a[164:167],v[68:71],a[224:227]
s_waitcnt lgkmcnt(2)
v_accvgpr_read_b32 v138,a212
v_accvgpr_read_b32 v139,a213
v_mfma_f32_16x16x32_bf16 a[224:227],a[168:171],v[72:75],a[224:227]
v_accvgpr_read_b32 v16,a214
v_accvgpr_read_b32 v17,a215
v_mfma_f32_16x16x32_bf16 a[224:227],a[172:175],v[76:79],a[224:227]
v_cvt_pk_bf16_f32 v138,v138,v139
v_cvt_pk_bf16_f32 v139,v16,v17
v_mfma_f32_16x16x32_bf16 a[228:231],a[172:175],v[92:95],a[228:231]
buffer_store_dwordx4 v[136:139],v6,s[32:35],s85 offen sc1
v_mfma_f32_16x16x32_bf16 a[228:231],a[168:171],v[88:91],a[228:231]
v_accvgpr_read_b32 v146,a216
v_accvgpr_read_b32 v147,a217
v_mfma_f32_16x16x32_bf16 a[228:231],a[164:167],v[84:87],a[228:231]
v_accvgpr_read_b32 v12,a218
v_accvgpr_read_b32 v13,a219
v_mfma_f32_16x16x32_bf16 a[228:231],a[160:163],v[80:83],a[228:231]
v_cvt_pk_bf16_f32 v146,v146,v147
v_cvt_pk_bf16_f32 v147,v12,v13
v_mfma_f32_16x16x32_bf16 a[232:235],a[160:163],v[96:99],a[232:235]
buffer_store_dwordx4 v[144:147],v6,s[32:35],s86 offen sc1
v_mfma_f32_16x16x32_bf16 a[232:235],a[164:167],v[100:103],a[232:235]
ds_read_b64_tr_b16 a[148:149],v44 offset:10368
ds_read_b64_tr_b16 a[150:151],v44 offset:15488
v_mfma_f32_16x16x32_bf16 a[232:235],a[168:171],v[104:107],a[232:235]
ds_read_b64_tr_b16 a[152:153],v44 offset:20608
v_mfma_f32_16x16x32_bf16 a[232:235],a[172:175],v[108:111],a[232:235]
ds_read_b64_tr_b16 a[154:155],v44 offset:25728
v_mfma_f32_16x16x32_bf16 a[224:227],a[176:179],v[132:135],a[224:227]
ds_read_b64_tr_b16 a[156:157],v44 offset:30848
v_mfma_f32_16x16x32_bf16 a[224:227],a[180:183],v[140:143],a[224:227]
ds_read_b64_tr_b16 a[158:159],v44 offset:35968
ds_read_b64_tr_b16 a[160:161],v43 offset:160
v_mfma_f32_16x16x32_bf16 a[224:227],a[184:187],v[148:151],a[224:227]
ds_read_b64_tr_b16 a[162:163],v43 offset:5280
v_mfma_f32_16x16x32_bf16 a[224:227],a[188:191],v[156:159],a[224:227]
ds_read_b64_tr_b16 a[164:165],v43 offset:10400
v_mfma_f32_16x16x32_bf16 a[228:231],a[188:191],v[188:191],a[228:231]
ds_read_b64_tr_b16 a[166:167],v43 offset:15520
v_mfma_f32_16x16x32_bf16 a[228:231],a[184:187],v[180:183],a[228:231]
ds_read_b64_tr_b16 a[168:169],v43 offset:20640
ds_read_b64_tr_b16 a[170:171],v43 offset:25760
v_mfma_f32_16x16x32_bf16 a[228:231],a[180:183],v[172:175],a[228:231]
ds_read_b64_tr_b16 a[172:173],v43 offset:30880
v_mfma_f32_16x16x32_bf16 a[228:231],a[176:179],v[164:167],a[228:231]
ds_read_b64_tr_b16 a[174:175],v43 offset:36000
v_mfma_f32_16x16x32_bf16 a[232:235],a[176:179],v[196:199],a[232:235]
ds_read_b64_tr_b16 a[176:177],v45 offset:160
v_mfma_f32_16x16x32_bf16 a[232:235],a[180:183],v[204:207],a[232:235]
v_accvgpr_read_b32 v160,a224
v_accvgpr_read_b32 v161,a225
v_mfma_f32_16x16x32_bf16 a[232:235],a[184:187],v[212:215],a[232:235]
v_cvt_pk_bf16_f32 v160,v160,v161
v_accvgpr_read_b32 v162,a226
v_mfma_f32_16x16x32_bf16 a[232:235],a[188:191],v[220:223],a[232:235]
)A"
R"A(v_accvgpr_read_b32 v163,a227
v_cvt_pk_bf16_f32 v161,v162,v163
v_mfma_f32_16x16x32_bf16 a[240:243],a[96:99],v[64:67],a[240:243]
ds_read_b64_tr_b16 a[178:179],v45 offset:5280
ds_read_b64_tr_b16 a[180:181],v45 offset:10400
v_mfma_f32_16x16x32_bf16 a[240:243],a[100:103],v[68:71],a[240:243]
v_accvgpr_read_b32 v168,a228
v_accvgpr_read_b32 v169,a229
v_mfma_f32_16x16x32_bf16 a[240:243],a[104:107],v[72:75],a[240:243]
v_cvt_pk_bf16_f32 v168,v168,v169
v_accvgpr_read_b32 v170,a230
v_mfma_f32_16x16x32_bf16 a[240:243],a[108:111],v[76:79],a[240:243]
v_accvgpr_read_b32 v171,a231
v_cvt_pk_bf16_f32 v169,v170,v171
v_mfma_f32_16x16x32_bf16 a[244:247],a[108:111],v[92:95],a[244:247]
ds_read_b64_tr_b16 a[182:183],v45 offset:15520
ds_read_b64_tr_b16 a[184:185],v45 offset:20640
v_mfma_f32_16x16x32_bf16 a[244:247],a[104:107],v[88:91],a[244:247]
ds_read_b64_tr_b16 a[186:187],v45 offset:25760
v_accvgpr_read_b32 v176,a232
v_mfma_f32_16x16x32_bf16 a[244:247],a[100:103],v[84:87],a[244:247]
ds_read_b64_tr_b16 a[188:189],v45 offset:30880
v_accvgpr_read_b32 v177,a233
v_mfma_f32_16x16x32_bf16 a[244:247],a[96:99],v[80:83],a[244:247]
ds_read_b64_tr_b16 a[190:191],v45 offset:36000
v_cvt_pk_bf16_f32 v176,v176,v177
v_mfma_f32_16x16x32_bf16 a[248:251],a[96:99],v[96:99],a[248:251]
v_accvgpr_read_b32 v178,a234
v_accvgpr_read_b32 v179,a235
v_mfma_f32_16x16x32_bf16 a[248:251],a[100:103],v[100:103],a[248:251]
ds_read_b64_tr_b16 a[96:97],v42 offset:192
v_cvt_pk_bf16_f32 v177,v178,v179
v_mfma_f32_16x16x32_bf16 a[248:251],a[104:107],v[104:107],a[248:251]
ds_read_b64_tr_b16 a[98:99],v42 offset:5312
ds_read_b64_tr_b16 a[100:101],v42 offset:10432
v_mfma_f32_16x16x32_bf16 a[248:251],a[108:111],v[108:111],a[248:251]
ds_read_b64_tr_b16 a[102:103],v42 offset:15552
v_mfma_f32_16x16x32_bf16 a[240:243],a[112:115],v[132:135],a[240:243]
ds_read_b64_tr_b16 a[104:105],v42 offset:20672
v_mfma_f32_16x16x32_bf16 a[240:243],a[116:119],v[140:143],a[240:243]
ds_read_b64_tr_b16 a[106:107],v42 offset:25792
v_mfma_f32_16x16x32_bf16 a[240:243],a[120:123],v[148:151],a[240:243]
ds_read_b64_tr_b16 a[108:109],v42 offset:30912
ds_read_b64_tr_b16 a[110:111],v42 offset:36032
v_mfma_f32_16x16x32_bf16 a[240:243],a[124:127],v[156:159],a[240:243]
v_mfma_f32_16x16x32_bf16 a[244:247],a[124:127],v[188:191],a[244:247]
v_mfma_f32_16x16x32_bf16 a[244:247],a[120:123],v[180:183],a[244:247]
v_mfma_f32_16x16x32_bf16 a[244:247],a[116:119],v[172:175],a[244:247]
v_mfma_f32_16x16x32_bf16 a[244:247],a[112:115],v[164:167],a[244:247]
v_mfma_f32_16x16x32_bf16 a[248:251],a[112:115],v[196:199],a[248:251]
ds_read_b64_tr_b16 a[112:113],v44 offset:192
ds_read_b64_tr_b16 a[114:115],v44 offset:5312
v_mfma_f32_16x16x32_bf16 a[248:251],a[116:119],v[204:207],a[248:251]
v_accvgpr_read_b32 v162,a240
v_accvgpr_read_b32 v163,a241
v_mfma_f32_16x16x32_bf16 a[248:251],a[120:123],v[212:215],a[248:251]
v_accvgpr_read_b32 v14,a242
v_accvgpr_read_b32 v15,a243
v_mfma_f32_16x16x32_bf16 a[248:251],a[124:127],v[220:223],a[248:251]
v_cvt_pk_bf16_f32 v162,v162,v163
v_cvt_pk_bf16_f32 v163,v14,v15
v_mfma_f32_16x16x32_bf16 a[0:3],a[128:131],v[64:67],a[0:3]
buffer_store_dwordx4 v[160:163],v6,s[32:35],s84 offen offset:1024 sc1
v_mfma_f32_16x16x32_bf16 a[0:3],a[132:135],v[68:71],a[0:3]
v_accvgpr_read_b32 v170,a244
v_accvgpr_read_b32 v171,a245
v_mfma_f32_16x16x32_bf16 a[0:3],a[136:139],v[72:75],a[0:3]
v_accvgpr_read_b32 v16,a246
v_accvgpr_read_b32 v17,a247
v_mfma_f32_16x16x32_bf16 a[0:3],a[140:143],v[76:79],a[0:3]
v_cvt_pk_bf16_f32 v170,v170,v171
v_cvt_pk_bf16_f32 v171,v16,v17
v_mfma_f32_16x16x32_bf16 a[4:7],a[140:143],v[92:95],a[4:7]
buffer_store_dwordx4 v[168:171],v6,s[32:35],s85 offen offset:1024 sc1
v_mfma_f32_16x16x32_bf16 a[4:7],a[136:139],v[88:91],a[4:7]
v_accvgpr_read_b32 v178,a248
v_accvgpr_read_b32 v179,a249
v_mfma_f32_16x16x32_bf16 a[4:7],a[132:135],v[84:87],a[4:7]
v_accvgpr_read_b32 v12,a250
v_accvgpr_read_b32 v13,a251
v_mfma_f32_16x16x32_bf16 a[4:7],a[128:131],v[80:83],a[4:7]
v_cvt_pk_bf16_f32 v178,v178,v179
v_cvt_pk_bf16_f32 v179,v12,v13
v_mfma_f32_16x16x32_bf16 a[8:11],a[128:131],v[96:99],a[8:11]
buffer_store_dwordx4 v[176:179],v6,s[32:35],s86 offen offset:1024 sc1
v_mfma_f32_16x16x32_bf16 a[8:11],a[132:135],v[100:103],a[8:11]
ds_read_b64_tr_b16 a[116:117],v44 offset:10432
ds_read_b64_tr_b16 a[118:119],v44 offset:15552
v_mfma_f32_16x16x32_bf16 a[8:11],a[136:139],v[104:107],a[8:11]
ds_read_b64_tr_b16 a[120:121],v44 offset:20672
v_mfma_f32_16x16x32_bf16 a[8:11],a[140:143],v[108:111],a[8:11]
s_waitcnt lgkmcnt(3)
ds_read_b64_tr_b16 a[122:123],v44 offset:25792
v_mfma_f32_16x16x32_bf16 a[0:3],a[144:147],v[132:135],a[0:3]
ds_read_b64_tr_b16 a[124:125],v44 offset:30912
v_mfma_f32_16x16x32_bf16 a[0:3],a[148:151],v[140:143],a[0:3]
ds_read_b64_tr_b16 a[126:127],v44 offset:36032
ds_read_b64_tr_b16 a[128:129],v43 offset:224
v_mfma_f32_16x16x32_bf16 a[0:3],a[152:155],v[148:151],a[0:3]
ds_read_b64_tr_b16 a[130:131],v43 offset:5344
v_mfma_f32_16x16x32_bf16 a[0:3],a[156:159],v[156:159],a[0:3]
ds_read_b64_tr_b16 a[132:133],v43 offset:10464
v_mfma_f32_16x16x32_bf16 a[4:7],a[156:159],v[188:191],a[4:7]
ds_read_b64_tr_b16 a[134:135],v43 offset:15584
v_mfma_f32_16x16x32_bf16 a[4:7],a[152:155],v[180:183],a[4:7]
ds_read_b64_tr_b16 a[136:137],v43 offset:20704
ds_read_b64_tr_b16 a[138:139],v43 offset:25824
v_mfma_f32_16x16x32_bf16 a[4:7],a[148:151],v[172:175],a[4:7]
ds_read_b64_tr_b16 a[140:141],v43 offset:30944
v_mfma_f32_16x16x32_bf16 a[4:7],a[144:147],v[164:167],a[4:7]
ds_read_b64_tr_b16 a[142:143],v43 offset:36064
v_mfma_f32_16x16x32_bf16 a[8:11],a[144:147],v[196:199],a[8:11]
ds_read_b64_tr_b16 a[144:145],v45 offset:224
v_mfma_f32_16x16x32_bf16 a[8:11],a[148:151],v[204:207],a[8:11]
v_accvgpr_read_b32 v192,a0
v_accvgpr_read_b32 v193,a1
v_mfma_f32_16x16x32_bf16 a[8:11],a[152:155],v[212:215],a[8:11]
v_cvt_pk_bf16_f32 v192,v192,v193
v_accvgpr_read_b32 v194,a2
v_mfma_f32_16x16x32_bf16 a[8:11],a[156:159],v[220:223],a[8:11]
v_accvgpr_read_b32 v195,a3
v_cvt_pk_bf16_f32 v193,v194,v195
v_mfma_f32_16x16x32_bf16 a[16:19],a[160:163],v[64:67],a[16:19]
ds_read_b64_tr_b16 a[146:147],v45 offset:5344
ds_read_b64_tr_b16 a[148:149],v45 offset:10464
v_mfma_f32_16x16x32_bf16 a[16:19],a[164:167],v[68:71],a[16:19]
v_accvgpr_read_b32 v200,a4
v_accvgpr_read_b32 v201,a5
v_mfma_f32_16x16x32_bf16 a[16:19],a[168:171],v[72:75],a[16:19]
v_cvt_pk_bf16_f32 v200,v200,v201
v_accvgpr_read_b32 v202,a6
v_mfma_f32_16x16x32_bf16 a[16:19],a[172:175],v[76:79],a[16:19]
v_accvgpr_read_b32 v203,a7
v_cvt_pk_bf16_f32 v201,v202,v203
v_mfma_f32_16x16x32_bf16 a[20:23],a[172:175],v[92:95],a[20:23]
ds_read_b64_tr_b16 a[150:151],v45 offset:15584
ds_read_b64_tr_b16 a[152:153],v45 offset:20704
v_mfma_f32_16x16x32_bf16 a[20:23],a[168:171],v[88:91],a[20:23]
ds_read_b64_tr_b16 a[154:155],v45 offset:25824
v_accvgpr_read_b32 v208,a8
v_mfma_f32_16x16x32_bf16 a[20:23],a[164:167],v[84:87],a[20:23]
ds_read_b64_tr_b16 a[156:157],v45 offset:30944
v_accvgpr_read_b32 v209,a9
v_mfma_f32_16x16x32_bf16 a[20:23],a[160:163],v[80:83],a[20:23]
ds_read_b64_tr_b16 a[158:159],v45 offset:36064
v_cvt_pk_bf16_f32 v208,v208,v209
v_mfma_f32_16x16x32_bf16 a[24:27],a[160:163],v[96:99],a[24:27]
v_accvgpr_read_b32 v210,a10
v_accvgpr_read_b32 v211,a11
v_mfma_f32_16x16x32_bf16 a[24:27],a[164:167],v[100:103],a[24:27]
ds_read_b64_tr_b16 a[160:161],v42 offset:256
v_cvt_pk_bf16_f32 v209,v210,v211
v_mfma_f32_16x16x32_bf16 a[24:27],a[168:171],v[104:107],a[24:27]
ds_read_b64_tr_b16 a[162:163],v42 offset:5376
ds_read_b64_tr_b16 a[164:165],v42 offset:10496
v_mfma_f32_16x16x32_bf16 a[24:27],a[172:175],v[108:111],a[24:27]
ds_read_b64_tr_b16 a[166:167],v42 offset:15616
v_mfma_f32_16x16x32_bf16 a[16:19],a[176:179],v[132:135],a[16:19]
ds_read_b64_tr_b16 a[168:169],v42 offset:20736
v_mfma_f32_16x16x32_bf16 a[16:19],a[180:183],v[140:143],a[16:19]
ds_read_b64_tr_b16 a[170:171],v42 offset:25856
v_mfma_f32_16x16x32_bf16 a[16:19],a[184:187],v[148:151],a[16:19]
ds_read_b64_tr_b16 a[172:173],v42 offset:30976
ds_read_b64_tr_b16 a[174:175],v42 offset:36096
v_mfma_f32_16x16x32_bf16 a[16:19],a[188:191],v[156:159],a[16:19]
v_mfma_f32_16x16x32_bf16 a[20:23],a[188:191],v[188:191],a[20:23]
v_mfma_f32_16x16x32_bf16 a[20:23],a[184:187],v[180:183],a[20:23]
v_mfma_f32_16x16x32_bf16 a[20:23],a[180:183],v[172:175],a[20:23]
v_mfma_f32_16x16x32_bf16 a[20:23],a[176:179],v[164:167],a[20:23]
v_mfma_f32_16x16x32_bf16 a[24:27],a[176:179],v[196:199],a[24:27]
ds_read_b64_tr_b16 a[176:177],v44 offset:256
ds_read_b64_tr_b16 a[178:179],v44 offset:5376
v_mfma_f32_16x16x32_bf16 a[24:27],a[180:183],v[204:207],a[24:27]
v_accvgpr_read_b32 v194,a16
v_accvgpr_read_b32 v195,a17
v_mfma_f32_16x16x32_bf16 a[24:27],a[184:187],v[212:215],a[24:27]
v_accvgpr_read_b32 v14,a18
v_accvgpr_read_b32 v15,a19
v_mfma_f32_16x16x32_bf16 a[24:27],a[188:191],v[220:223],a[24:27]
v_cvt_pk_bf16_f32 v194,v194,v195
v_cvt_pk_bf16_f32 v195,v14,v15
v_mfma_f32_16x16x32_bf16 a[32:35],a[96:99],v[64:67],a[32:35]
buffer_store_dwordx4 v[192:195],v6,s[32:35],s84 offen offset:2048 sc1
v_mfma_f32_16x16x32_bf16 a[32:35],a[100:103],v[68:71],a[32:35]
v_accvgpr_read_b32 v202,a20
v_accvgpr_read_b32 v203,a21
v_mfma_f32_16x16x32_bf16 a[32:35],a[104:107],v[72:75],a[32:35]
v_accvgpr_read_b32 v16,a22
v_accvgpr_read_b32 v17,a23
v_mfma_f32_16x16x32_bf16 a[32:35],a[108:111],v[76:79],a[32:35]
v_cvt_pk_bf16_f32 v202,v202,v203
v_cvt_pk_bf16_f32 v203,v16,v17
v_mfma_f32_16x16x32_bf16 a[36:39],a[108:111],v[92:95],a[36:39]
buffer_store_dwordx4 v[200:203],v6,s[32:35],s85 offen offset:2048 sc1
v_mfma_f32_16x16x32_bf16 a[36:39],a[104:107],v[88:91],a[36:39]
v_accvgpr_read_b32 v210,a24
v_accvgpr_read_b32 v211,a25
v_mfma_f32_16x16x32_bf16 a[36:39],a[100:103],v[84:87],a[36:39]
v_accvgpr_read_b32 v12,a26
v_accvgpr_read_b32 v13,a27
v_mfma_f32_16x16x32_bf16 a[36:39],a[96:99],v[80:83],a[36:39]
v_cvt_pk_bf16_f32 v210,v210,v211
v_cvt_pk_bf16_f32 v211,v12,v13
v_mfma_f32_16x16x32_bf16 a[40:43],a[96:99],v[96:99],a[40:43]
buffer_store_dwordx4 v[208:211],v6,s[32:35],s86 offen offset:2048 sc1
v_mfma_f32_16x16x32_bf16 a[40:43],a[100:103],v[100:103],a[40:43]
ds_read_b64_tr_b16 a[180:181],v44 offset:10496
ds_read_b64_tr_b16 a[182:183],v44 offset:15616
v_mfma_f32_16x16x32_bf16 a[40:43],a[104:107],v[104:107],a[40:43]
ds_read_b64_tr_b16 a[184:185],v44 offset:20736
v_mfma_f32_16x16x32_bf16 a[40:43],a[108:111],v[108:111],a[40:43]
ds_read_b64_tr_b16 a[186:187],v44 offset:25856
v_mfma_f32_16x16x32_bf16 a[32:35],a[112:115],v[132:135],a[32:35]
s_waitcnt lgkmcnt(4)
ds_read_b64_tr_b16 a[188:189],v44 offset:30976
v_mfma_f32_16x16x32_bf16 a[32:35],a[116:119],v[140:143],a[32:35]
ds_read_b64_tr_b16 a[190:191],v44 offset:36096
ds_read_b64_tr_b16 a[96:97],v43 offset:288
v_mfma_f32_16x16x32_bf16 a[32:35],a[120:123],v[148:151],a[32:35]
ds_read_b64_tr_b16 a[98:99],v43 offset:5408
v_mfma_f32_16x16x32_bf16 a[32:35],a[124:127],v[156:159],a[32:35]
ds_read_b64_tr_b16 a[100:101],v43 offset:10528
v_mfma_f32_16x16x32_bf16 a[36:39],a[124:127],v[188:191],a[36:39]
ds_read_b64_tr_b16 a[102:103],v43 offset:15648
v_mfma_f32_16x16x32_bf16 a[36:39],a[120:123],v[180:183],a[36:39]
ds_read_b64_tr_b16 a[104:105],v43 offset:20768
ds_read_b64_tr_b16 a[106:107],v43 offset:25888
v_mfma_f32_16x16x32_bf16 a[36:39],a[116:119],v[172:175],a[36:39]
ds_read_b64_tr_b16 a[108:109],v43 offset:31008
v_mfma_f32_16x16x32_bf16 a[36:39],a[112:115],v[164:167],a[36:39]
ds_read_b64_tr_b16 a[110:111],v43 offset:36128
v_mfma_f32_16x16x32_bf16 a[40:43],a[112:115],v[196:199],a[40:43]
ds_read_b64_tr_b16 a[112:113],v45 offset:288
v_mfma_f32_16x16x32_bf16 a[40:43],a[116:119],v[204:207],a[40:43]
v_accvgpr_read_b32 v224,a32
v_accvgpr_read_b32 v225,a33
v_mfma_f32_16x16x32_bf16 a[40:43],a[120:123],v[212:215],a[40:43]
v_cvt_pk_bf16_f32 v224,v224,v225
v_accvgpr_read_b32 v226,a34
v_mfma_f32_16x16x32_bf16 a[40:43],a[124:127],v[220:223],a[40:43]
v_accvgpr_read_b32 v227,a35
v_cvt_pk_bf16_f32 v225,v226,v227
v_mfma_f32_16x16x32_bf16 a[48:51],a[128:131],v[64:67],a[48:51]
ds_read_b64_tr_b16 a[114:115],v45 offset:5408
ds_read_b64_tr_b16 a[116:117],v45 offset:10528
v_mfma_f32_16x16x32_bf16 a[48:51],a[132:135],v[68:71],a[48:51]
)A"
R"A(v_accvgpr_read_b32 v232,a36
v_accvgpr_read_b32 v233,a37
v_mfma_f32_16x16x32_bf16 a[48:51],a[136:139],v[72:75],a[48:51]
v_cvt_pk_bf16_f32 v232,v232,v233
v_accvgpr_read_b32 v234,a38
v_mfma_f32_16x16x32_bf16 a[48:51],a[140:143],v[76:79],a[48:51]
v_accvgpr_read_b32 v235,a39
v_cvt_pk_bf16_f32 v233,v234,v235
v_mfma_f32_16x16x32_bf16 a[52:55],a[140:143],v[92:95],a[52:55]
ds_read_b64_tr_b16 a[118:119],v45 offset:15648
ds_read_b64_tr_b16 a[120:121],v45 offset:20768
v_mfma_f32_16x16x32_bf16 a[52:55],a[136:139],v[88:91],a[52:55]
v_accvgpr_read_b32 v240,a40
v_accvgpr_read_b32 v241,a41
v_mfma_f32_16x16x32_bf16 a[52:55],a[132:135],v[84:87],a[52:55]
v_cvt_pk_bf16_f32 v240,v240,v241
v_accvgpr_read_b32 v242,a42
v_mfma_f32_16x16x32_bf16 a[52:55],a[128:131],v[80:83],a[52:55]
v_accvgpr_read_b32 v243,a43
v_cvt_pk_bf16_f32 v241,v242,v243
v_mfma_f32_16x16x32_bf16 a[56:59],a[128:131],v[96:99],a[56:59]
ds_read_b64_tr_b16 a[122:123],v45 offset:25888
ds_read_b64_tr_b16 a[124:125],v45 offset:31008
v_mfma_f32_16x16x32_bf16 a[56:59],a[132:135],v[100:103],a[56:59]
ds_read_b64_tr_b16 a[126:127],v45 offset:36128
v_mfma_f32_16x16x32_bf16 a[56:59],a[136:139],v[104:107],a[56:59]
ds_read_b64_tr_b16 a[128:129],v1
v_mfma_f32_16x16x32_bf16 a[56:59],a[140:143],v[108:111],a[56:59]
ds_read_b64_tr_b16 a[130:131],v1 offset:5120
v_mfma_f32_16x16x32_bf16 a[48:51],a[144:147],v[132:135],a[48:51]
ds_read_b64_tr_b16 a[132:133],v1 offset:10240
ds_read_b64_tr_b16 a[134:135],v1 offset:15360
v_mfma_f32_16x16x32_bf16 a[48:51],a[148:151],v[140:143],a[48:51]
ds_read_b64_tr_b16 a[136:137],v1 offset:20480
v_mfma_f32_16x16x32_bf16 a[48:51],a[152:155],v[148:151],a[48:51]
ds_read_b64_tr_b16 a[138:139],v1 offset:25600
v_mfma_f32_16x16x32_bf16 a[48:51],a[156:159],v[156:159],a[48:51]
ds_read_b64_tr_b16 a[140:141],v1 offset:30720
v_mfma_f32_16x16x32_bf16 a[52:55],a[156:159],v[188:191],a[52:55]
ds_read_b64_tr_b16 a[142:143],v1 offset:35840
v_mfma_f32_16x16x32_bf16 a[52:55],a[152:155],v[180:183],a[52:55]
v_mfma_f32_16x16x32_bf16 a[52:55],a[148:151],v[172:175],a[52:55]
v_mfma_f32_16x16x32_bf16 a[52:55],a[144:147],v[164:167],a[52:55]
v_mfma_f32_16x16x32_bf16 a[56:59],a[144:147],v[196:199],a[56:59]
ds_read_b64_tr_b16 a[144:145],v42
ds_read_b64_tr_b16 a[146:147],v42 offset:5120
v_mfma_f32_16x16x32_bf16 a[56:59],a[148:151],v[204:207],a[56:59]
v_accvgpr_read_b32 v226,a48
v_accvgpr_read_b32 v227,a49
v_mfma_f32_16x16x32_bf16 a[56:59],a[152:155],v[212:215],a[56:59]
v_accvgpr_read_b32 v14,a50
v_accvgpr_read_b32 v15,a51
v_mfma_f32_16x16x32_bf16 a[56:59],a[156:159],v[220:223],a[56:59]
v_cvt_pk_bf16_f32 v226,v226,v227
v_cvt_pk_bf16_f32 v227,v14,v15
v_mfma_f32_16x16x32_bf16 a[64:67],a[160:163],v[64:67],a[64:67]
buffer_store_dwordx4 v[224:227],v6,s[32:35],s84 offen offset:3072 sc1
v_mfma_f32_16x16x32_bf16 a[64:67],a[164:167],v[68:71],a[64:67]
v_accvgpr_read_b32 v234,a52
v_accvgpr_read_b32 v235,a53
v_mfma_f32_16x16x32_bf16 a[64:67],a[168:171],v[72:75],a[64:67]
v_accvgpr_read_b32 v16,a54
v_accvgpr_read_b32 v17,a55
v_mfma_f32_16x16x32_bf16 a[64:67],a[172:175],v[76:79],a[64:67]
v_cvt_pk_bf16_f32 v234,v234,v235
v_cvt_pk_bf16_f32 v235,v16,v17
v_mfma_f32_16x16x32_bf16 a[68:71],a[172:175],v[92:95],a[68:71]
buffer_store_dwordx4 v[232:235],v6,s[32:35],s85 offen offset:3072 sc1
v_mfma_f32_16x16x32_bf16 a[68:71],a[168:171],v[88:91],a[68:71]
v_accvgpr_read_b32 v242,a56
v_accvgpr_read_b32 v243,a57
v_mfma_f32_16x16x32_bf16 a[68:71],a[164:167],v[84:87],a[68:71]
v_accvgpr_read_b32 v12,a58
v_accvgpr_read_b32 v13,a59
v_mfma_f32_16x16x32_bf16 a[68:71],a[160:163],v[80:83],a[68:71]
v_cvt_pk_bf16_f32 v242,v242,v243
v_cvt_pk_bf16_f32 v243,v12,v13
v_mfma_f32_16x16x32_bf16 a[72:75],a[160:163],v[96:99],a[72:75]
buffer_store_dwordx4 v[240:243],v6,s[32:35],s86 offen offset:3072 sc1
v_mfma_f32_16x16x32_bf16 a[72:75],a[164:167],v[100:103],a[72:75]
ds_read_b64_tr_b16 a[148:149],v42 offset:10240
ds_read_b64_tr_b16 a[150:151],v42 offset:15360
v_mfma_f32_16x16x32_bf16 a[72:75],a[168:171],v[104:107],a[72:75]
ds_read_b64_tr_b16 a[152:153],v42 offset:20480
v_mfma_f32_16x16x32_bf16 a[72:75],a[172:175],v[108:111],a[72:75]
ds_read_b64_tr_b16 a[154:155],v42 offset:25600
v_mfma_f32_16x16x32_bf16 a[64:67],a[176:179],v[132:135],a[64:67]
s_waitcnt lgkmcnt(4)
ds_read_b64_tr_b16 a[156:157],v42 offset:30720
v_mfma_f32_16x16x32_bf16 a[64:67],a[180:183],v[140:143],a[64:67]
ds_read_b64_tr_b16 a[158:159],v42 offset:35840
ds_read_b64_tr_b16 a[160:161],v44
v_mfma_f32_16x16x32_bf16 a[64:67],a[184:187],v[148:151],a[64:67]
ds_read_b64_tr_b16 a[162:163],v44 offset:5120
v_mfma_f32_16x16x32_bf16 a[64:67],a[188:191],v[156:159],a[64:67]
ds_read_b64_tr_b16 a[164:165],v44 offset:10240
v_mfma_f32_16x16x32_bf16 a[68:71],a[188:191],v[188:191],a[68:71]
ds_read_b64_tr_b16 a[166:167],v44 offset:15360
v_mfma_f32_16x16x32_bf16 a[68:71],a[184:187],v[180:183],a[68:71]
ds_read_b64_tr_b16 a[168:169],v44 offset:20480
ds_read_b64_tr_b16 a[170:171],v44 offset:25600
v_mfma_f32_16x16x32_bf16 a[68:71],a[180:183],v[172:175],a[68:71]
ds_read_b64_tr_b16 a[172:173],v44 offset:30720
v_mfma_f32_16x16x32_bf16 a[68:71],a[176:179],v[164:167],a[68:71]
ds_read_b64_tr_b16 a[174:175],v44 offset:35840
v_mfma_f32_16x16x32_bf16 a[72:75],a[176:179],v[196:199],a[72:75]
ds_read_b64_tr_b16 a[176:177],v2 offset:32
v_mfma_f32_16x16x32_bf16 a[72:75],a[180:183],v[204:207],a[72:75]
v_accvgpr_read_b32 v128,a64
v_accvgpr_read_b32 v129,a65
v_mfma_f32_16x16x32_bf16 a[72:75],a[184:187],v[212:215],a[72:75]
v_cvt_pk_bf16_f32 v128,v128,v129
v_accvgpr_read_b32 v130,a66
v_mfma_f32_16x16x32_bf16 a[72:75],a[188:191],v[220:223],a[72:75]
v_accvgpr_read_b32 v131,a67
v_cvt_pk_bf16_f32 v129,v130,v131
v_mfma_f32_16x16x32_bf16 a[80:83],a[96:99],v[64:67],a[80:83]
ds_read_b64_tr_b16 a[178:179],v2 offset:5152
ds_read_b64_tr_b16 a[180:181],v2 offset:10272
v_mfma_f32_16x16x32_bf16 a[80:83],a[100:103],v[68:71],a[80:83]
v_accvgpr_read_b32 v136,a68
v_accvgpr_read_b32 v137,a69
v_mfma_f32_16x16x32_bf16 a[80:83],a[104:107],v[72:75],a[80:83]
v_cvt_pk_bf16_f32 v136,v136,v137
v_accvgpr_read_b32 v138,a70
v_mfma_f32_16x16x32_bf16 a[80:83],a[108:111],v[76:79],a[80:83]
ds_read_b64_tr_b16 a[182:183],v2 offset:15392
v_accvgpr_read_b32 v139,a71
v_mfma_f32_16x16x32_bf16 a[84:87],a[108:111],v[92:95],a[84:87]
ds_read_b64_tr_b16 a[184:185],v2 offset:20512
v_cvt_pk_bf16_f32 v137,v138,v139
v_mfma_f32_16x16x32_bf16 a[84:87],a[104:107],v[88:91],a[84:87]
v_accvgpr_read_b32 v144,a72
v_accvgpr_read_b32 v145,a73
v_mfma_f32_16x16x32_bf16 a[84:87],a[100:103],v[84:87],a[84:87]
v_cvt_pk_bf16_f32 v144,v144,v145
v_accvgpr_read_b32 v146,a74
v_mfma_f32_16x16x32_bf16 a[84:87],a[96:99],v[80:83],a[84:87]
v_accvgpr_read_b32 v147,a75
v_cvt_pk_bf16_f32 v145,v146,v147
v_mfma_f32_16x16x32_bf16 a[88:91],a[96:99],v[96:99],a[88:91]
ds_read_b64_tr_b16 a[186:187],v2 offset:25632
ds_read_b64_tr_b16 a[188:189],v2 offset:30752
v_mfma_f32_16x16x32_bf16 a[88:91],a[100:103],v[100:103],a[88:91]
ds_read_b64_tr_b16 a[190:191],v2 offset:35872
v_mfma_f32_16x16x32_bf16 a[88:91],a[104:107],v[104:107],a[88:91]
ds_read_b64_tr_b16 a[96:97],v43 offset:32
v_mfma_f32_16x16x32_bf16 a[88:91],a[108:111],v[108:111],a[88:91]
ds_read_b64_tr_b16 a[98:99],v43 offset:5152
v_mfma_f32_16x16x32_bf16 a[80:83],a[112:115],v[132:135],a[80:83]
ds_read_b64_tr_b16 a[100:101],v43 offset:10272
ds_read_b64_tr_b16 a[102:103],v43 offset:15392
v_mfma_f32_16x16x32_bf16 a[80:83],a[116:119],v[140:143],a[80:83]
ds_read_b64_tr_b16 a[104:105],v43 offset:20512
v_mfma_f32_16x16x32_bf16 a[80:83],a[120:123],v[148:151],a[80:83]
ds_read_b64_tr_b16 a[106:107],v43 offset:25632
v_mfma_f32_16x16x32_bf16 a[80:83],a[124:127],v[156:159],a[80:83]
ds_read_b64_tr_b16 a[108:109],v43 offset:30752
v_mfma_f32_16x16x32_bf16 a[84:87],a[124:127],v[188:191],a[84:87]
ds_read_b64_tr_b16 a[110:111],v43 offset:35872
v_mfma_f32_16x16x32_bf16 a[84:87],a[120:123],v[180:183],a[84:87]
v_mfma_f32_16x16x32_bf16 a[84:87],a[116:119],v[172:175],a[84:87]
v_mfma_f32_16x16x32_bf16 a[84:87],a[112:115],v[164:167],a[84:87]
v_mfma_f32_16x16x32_bf16 a[88:91],a[112:115],v[196:199],a[88:91]
ds_read_b64_tr_b16 a[112:113],v45 offset:32
ds_read_b64_tr_b16 a[114:115],v45 offset:5152
v_mfma_f32_16x16x32_bf16 a[88:91],a[116:119],v[204:207],a[88:91]
v_accvgpr_read_b32 v130,a80
v_accvgpr_read_b32 v131,a81
v_mfma_f32_16x16x32_bf16 a[88:91],a[120:123],v[212:215],a[88:91]
v_accvgpr_read_b32 v14,a82
v_accvgpr_read_b32 v15,a83
v_mfma_f32_16x16x32_bf16 a[88:91],a[124:127],v[220:223],a[88:91]
v_cvt_pk_bf16_f32 v130,v130,v131
v_cvt_pk_bf16_f32 v131,v14,v15
v_mfma_f32_16x16x32_bf16 a[204:207],a[128:131],v[152:155],a[204:207]
s_lshl_b32 s94,s71,2
buffer_store_dwordx4 v[128:131],v25,s[32:35],s84 offen sc1
v_mfma_f32_16x16x32_bf16 a[204:207],a[132:135],v[184:187],a[204:207]
s_add_u32 s94,s94,s40
v_accvgpr_read_b32 v138,a84
v_accvgpr_read_b32 v139,a85
v_mfma_f32_16x16x32_bf16 a[204:207],a[136:139],v[216:219],a[204:207]
s_lshl_b32 s95,s42,2
s_add_u32 s95,s95,s40
s_lshl_b32 s95,s95,4
v_mfma_f32_16x16x32_bf16 a[204:207],a[140:143],v[248:251],a[204:207]
s_lshl_b32 s94,s94,4
v_mov_b32 v18,s68
v_mov_b32 v19,s69
v_mfma_f32_16x16x32_bf16 a[204:207],a[144:147],v[112:115],a[204:207]
s_waitcnt vmcnt(1)
s_mov_b64 exec,1
buffer_store_dwordx2 v[18:19],v6,s[64:67],s95 offen offset:2048 sc1
s_mov_b64 exec,-1
s_waitcnt lgkmcnt(0)
v_mfma_f32_16x16x32_bf16 a[204:207],a[148:151],v[116:119],a[204:207]
v_accvgpr_read_b32 v146,a88
v_accvgpr_read_b32 v147,a89
v_mfma_f32_16x16x32_bf16 a[204:207],a[152:155],v[120:123],a[204:207]
v_accvgpr_read_b32 v16,a86
v_accvgpr_read_b32 v17,a87
v_cvt_pk_bf16_f32 v138,v138,v139
v_accvgpr_read_b32 v12,a90
v_accvgpr_read_b32 v13,a91
v_cvt_pk_bf16_f32 v146,v146,v147
v_cvt_pk_bf16_f32 v139,v16,v17
v_cvt_pk_bf16_f32 v147,v12,v13
v_mfma_f32_16x16x32_bf16 a[204:207],a[156:159],v[124:127],a[204:207]
ds_read_b64_tr_b16 a[116:117],v45 offset:10272
ds_read_b64_tr_b16 a[118:119],v45 offset:15392
v_mfma_f32_16x16x32_bf16 a[204:207],a[160:163],v[228:231],a[204:207]
buffer_store_dwordx4 v[136:139],v25,s[32:35],s85 offen sc1
v_mfma_f32_16x16x32_bf16 a[204:207],a[164:167],v[236:239],a[204:207]
ds_read_b64_tr_b16 a[120:121],v45 offset:20512
ds_read_b64_tr_b16 a[122:123],v45 offset:25632
v_mfma_f32_16x16x32_bf16 a[204:207],a[168:171],v[244:247],a[204:207]
ds_read_b64_tr_b16 a[124:125],v45 offset:30752
ds_read_b64_tr_b16 a[126:127],v45 offset:35872
v_mfma_f32_16x16x32_bf16 a[204:207],a[172:175],v[252:255],a[204:207]
ds_read_b64_tr_b16 a[128:129],v1 offset:64
ds_read_b64_tr_b16 a[130:131],v1 offset:5184
v_mfma_f32_16x16x32_bf16 a[220:223],a[176:179],v[152:155],a[220:223]
buffer_store_dwordx4 v[144:147],v25,s[32:35],s86 offen sc1
v_mfma_f32_16x16x32_bf16 a[220:223],a[180:183],v[184:187],a[220:223]
ds_read_b64_tr_b16 a[132:133],v1 offset:10304
ds_read_b64_tr_b16 a[134:135],v1 offset:15424
v_mfma_f32_16x16x32_bf16 a[220:223],a[184:187],v[216:219],a[220:223]
ds_read_b64_tr_b16 a[136:137],v1 offset:20544
ds_read_b64_tr_b16 a[138:139],v1 offset:25664
v_mfma_f32_16x16x32_bf16 a[220:223],a[188:191],v[248:251],a[220:223]
ds_read_b64_tr_b16 a[140:141],v1 offset:30784
ds_read_b64_tr_b16 a[142:143],v1 offset:35904
v_mfma_f32_16x16x32_bf16 a[220:223],a[96:99],v[112:115],a[220:223]
ds_read_b64_tr_b16 a[144:145],v42 offset:64
ds_read_b64_tr_b16 a[146:147],v42 offset:5184
v_mfma_f32_16x16x32_bf16 a[220:223],a[100:103],v[116:119],a[220:223]
ds_read_b64_tr_b16 a[148:149],v42 offset:10304
ds_read_b64_tr_b16 a[150:151],v42 offset:15424
v_mfma_f32_16x16x32_bf16 a[220:223],a[104:107],v[120:123],a[220:223]
ds_read_b64_tr_b16 a[152:153],v42 offset:20544
ds_read_b64_tr_b16 a[154:155],v42 offset:25664
v_mfma_f32_16x16x32_bf16 a[220:223],a[108:111],v[124:127],a[220:223]
ds_read_b64_tr_b16 a[156:157],v42 offset:30784
ds_read_b64_tr_b16 a[158:159],v42 offset:35904
v_mfma_f32_16x16x32_bf16 a[220:223],a[112:115],v[228:231],a[220:223]
s_waitcnt lgkmcnt(14)
ds_read_b64_tr_b16 a[160:161],v44 offset:64
ds_read_b64_tr_b16 a[162:163],v44 offset:5184
v_mfma_f32_16x16x32_bf16 a[220:223],a[116:119],v[236:239],a[220:223]
ds_read_b64_tr_b16 a[164:165],v44 offset:10304
ds_read_b64_tr_b16 a[166:167],v44 offset:15424
)A"
R"A(v_mfma_f32_16x16x32_bf16 a[220:223],a[120:123],v[244:247],a[220:223]
ds_read_b64_tr_b16 a[168:169],v44 offset:20544
ds_read_b64_tr_b16 a[170:171],v44 offset:25664
v_mfma_f32_16x16x32_bf16 a[220:223],a[124:127],v[252:255],a[220:223]
ds_read_b64_tr_b16 a[172:173],v44 offset:30784
ds_read_b64_tr_b16 a[174:175],v44 offset:35904
v_mfma_f32_16x16x32_bf16 a[236:239],a[128:131],v[152:155],a[236:239]
s_waitcnt lgkmcnt(14)
ds_read_b64_tr_b16 a[176:177],v2 offset:96
ds_read_b64_tr_b16 a[178:179],v2 offset:5216
v_mfma_f32_16x16x32_bf16 a[236:239],a[132:135],v[184:187],a[236:239]
v_bfe_u32 v24,v6,4,2
v_min_u32 v24,2,v24
v_mfma_f32_16x16x32_bf16 a[236:239],a[136:139],v[216:219],a[236:239]
ds_read_b64_tr_b16 a[180:181],v2 offset:10336
ds_read_b64_tr_b16 a[182:183],v2 offset:15456
v_mfma_f32_16x16x32_bf16 a[236:239],a[140:143],v[248:251],a[236:239]
v_add_u32 v24,s70,v24
v_add_u32 v24,1,v24
v_mfma_f32_16x16x32_bf16 a[236:239],a[144:147],v[112:115],a[236:239]
s_waitcnt lgkmcnt(10)
ds_read_b64_tr_b16 a[184:185],v2 offset:20576
ds_read_b64_tr_b16 a[186:187],v2 offset:25696
v_mfma_f32_16x16x32_bf16 a[236:239],a[148:151],v[116:119],a[236:239]
v_and_b32 v24,3,v24
v_lshlrev_b32 v24,8,v24
v_mfma_f32_16x16x32_bf16 a[236:239],a[152:155],v[120:123],a[236:239]
ds_read_b64_tr_b16 a[188:189],v2 offset:30816
ds_read_b64_tr_b16 a[190:191],v2 offset:35936
v_mfma_f32_16x16x32_bf16 a[236:239],a[156:159],v[124:127],a[236:239]
ds_read_b64_tr_b16 a[96:97],v43 offset:96
ds_read_b64_tr_b16 a[98:99],v43 offset:5216
v_mfma_f32_16x16x32_bf16 a[236:239],a[160:163],v[228:231],a[236:239]
s_waitcnt lgkmcnt(8)
ds_read_b64_tr_b16 a[100:101],v43 offset:10336
ds_read_b64_tr_b16 a[102:103],v43 offset:15456
v_mfma_f32_16x16x32_bf16 a[236:239],a[164:167],v[236:239],a[236:239]
ds_read_b64_tr_b16 a[104:105],v43 offset:20576
ds_read_b64_tr_b16 a[106:107],v43 offset:25696
v_mfma_f32_16x16x32_bf16 a[236:239],a[168:171],v[244:247],a[236:239]
ds_read_b64_tr_b16 a[108:109],v43 offset:30816
ds_read_b64_tr_b16 a[110:111],v43 offset:35936
v_mfma_f32_16x16x32_bf16 a[236:239],a[172:175],v[252:255],a[236:239]
ds_read_b64_tr_b16 a[112:113],v45 offset:96
ds_read_b64_tr_b16 a[114:115],v45 offset:5216
v_mfma_f32_16x16x32_bf16 a[252:255],a[176:179],v[152:155],a[252:255]
s_waitcnt lgkmcnt(12)
ds_read_b64_tr_b16 a[116:117],v45 offset:10336
ds_read_b64_tr_b16 a[118:119],v45 offset:15456
v_mfma_f32_16x16x32_bf16 a[252:255],a[180:183],v[184:187],a[252:255]
ds_read_b64_tr_b16 a[120:121],v45 offset:20576
ds_read_b64_tr_b16 a[122:123],v45 offset:25696
v_mfma_f32_16x16x32_bf16 a[252:255],a[184:187],v[216:219],a[252:255]
s_waitcnt lgkmcnt(14)
ds_read_b64_tr_b16 a[124:125],v45 offset:30816
ds_read_b64_tr_b16 a[126:127],v45 offset:35936
v_mfma_f32_16x16x32_bf16 a[252:255],a[188:191],v[248:251],a[252:255]
s_waitcnt lgkmcnt(14)
ds_read_b64_tr_b16 a[128:129],v1 offset:128
ds_read_b64_tr_b16 a[130:131],v1 offset:5248
v_mfma_f32_16x16x32_bf16 a[252:255],a[96:99],v[112:115],a[252:255]
s_waitcnt lgkmcnt(14)
buffer_load_dwordx2 v[26:27],v24,s[64:67],s94 offen offset:2048 sc1
ds_read_b64_tr_b16 a[132:133],v1 offset:10368
ds_read_b64_tr_b16 a[134:135],v1 offset:15488
v_mfma_f32_16x16x32_bf16 a[252:255],a[100:103],v[116:119],a[252:255]
s_waitcnt lgkmcnt(14)
ds_read_b64_tr_b16 a[136:137],v1 offset:20608
ds_read_b64_tr_b16 a[138:139],v1 offset:25728
v_mfma_f32_16x16x32_bf16 a[252:255],a[104:107],v[120:123],a[252:255]
s_waitcnt lgkmcnt(14)
ds_read_b64_tr_b16 a[140:141],v1 offset:30848
ds_read_b64_tr_b16 a[142:143],v1 offset:35968
v_mfma_f32_16x16x32_bf16 a[252:255],a[108:111],v[124:127],a[252:255]
s_waitcnt lgkmcnt(14)
ds_read_b64_tr_b16 a[144:145],v42 offset:128
ds_read_b64_tr_b16 a[146:147],v42 offset:5248
v_mfma_f32_16x16x32_bf16 a[252:255],a[112:115],v[228:231],a[252:255]
s_waitcnt lgkmcnt(14)
ds_read_b64_tr_b16 a[148:149],v42 offset:10368
ds_read_b64_tr_b16 a[150:151],v42 offset:15488
v_mfma_f32_16x16x32_bf16 a[252:255],a[116:119],v[236:239],a[252:255]
s_lshl_b32 s95,s42,2
ds_read_b64_tr_b16 a[152:153],v42 offset:20608
ds_read_b64_tr_b16 a[154:155],v42 offset:25728
s_waitcnt lgkmcnt(15)
v_mfma_f32_16x16x32_bf16 a[252:255],a[120:123],v[244:247],a[252:255]
s_add_u32 s95,s95,s40
s_lshl_b32 s95,s95,4
ds_read_b64_tr_b16 a[156:157],v42 offset:30848
ds_read_b64_tr_b16 a[158:159],v42 offset:35968
v_mov_b32 v18,s68
v_mov_b32 v19,s69
s_waitcnt lgkmcnt(15)
v_mfma_f32_16x16x32_bf16 a[252:255],a[124:127],v[252:255],a[252:255]
s_waitcnt vmcnt(1)
s_mov_b64 exec,1
buffer_store_dwordx2 v[18:19],v6,s[64:67],s95 offen offset:3072 sc1
s_mov_b64 exec,-1
ds_read_b64_tr_b16 a[160:161],v44 offset:128
ds_read_b64_tr_b16 a[162:163],v44 offset:5248
s_waitcnt lgkmcnt(15)
v_mfma_f32_16x16x32_bf16 a[12:15],a[128:131],v[152:155],a[12:15]
s_waitcnt lgkmcnt(14)
ds_read_b64_tr_b16 a[164:165],v44 offset:10368
ds_read_b64_tr_b16 a[166:167],v44 offset:15488
v_mfma_f32_16x16x32_bf16 a[12:15],a[132:135],v[184:187],a[12:15]
s_waitcnt lgkmcnt(14)
ds_read_b64_tr_b16 a[168:169],v44 offset:20608
ds_read_b64_tr_b16 a[170:171],v44 offset:25728
v_mfma_f32_16x16x32_bf16 a[12:15],a[136:139],v[216:219],a[12:15]
s_waitcnt lgkmcnt(14)
ds_read_b64_tr_b16 a[172:173],v44 offset:30848
ds_read_b64_tr_b16 a[174:175],v44 offset:35968
v_mfma_f32_16x16x32_bf16 a[12:15],a[140:143],v[248:251],a[12:15]
s_waitcnt lgkmcnt(14)
ds_read_b64_tr_b16 a[176:177],v2 offset:160
ds_read_b64_tr_b16 a[178:179],v2 offset:5280
v_mfma_f32_16x16x32_bf16 a[12:15],a[144:147],v[112:115],a[12:15]
s_waitcnt lgkmcnt(14)
ds_read_b64_tr_b16 a[180:181],v2 offset:10400
ds_read_b64_tr_b16 a[182:183],v2 offset:15520
v_mfma_f32_16x16x32_bf16 a[12:15],a[148:151],v[116:119],a[12:15]
s_waitcnt lgkmcnt(14)
ds_read_b64_tr_b16 a[184:185],v2 offset:20640
ds_read_b64_tr_b16 a[186:187],v2 offset:25760
v_mfma_f32_16x16x32_bf16 a[12:15],a[152:155],v[120:123],a[12:15]
s_waitcnt lgkmcnt(14)
ds_read_b64_tr_b16 a[188:189],v2 offset:30880
ds_read_b64_tr_b16 a[190:191],v2 offset:36000
v_mfma_f32_16x16x32_bf16 a[12:15],a[156:159],v[124:127],a[12:15]
s_waitcnt lgkmcnt(14)
ds_read_b64_tr_b16 a[96:97],v43 offset:160
ds_read_b64_tr_b16 a[98:99],v43 offset:5280
v_mfma_f32_16x16x32_bf16 a[12:15],a[160:163],v[228:231],a[12:15]
s_waitcnt lgkmcnt(14)
ds_read_b64_tr_b16 a[100:101],v43 offset:10400
ds_read_b64_tr_b16 a[102:103],v43 offset:15520
v_mfma_f32_16x16x32_bf16 a[12:15],a[164:167],v[236:239],a[12:15]
s_waitcnt lgkmcnt(14)
ds_read_b64_tr_b16 a[104:105],v43 offset:20640
ds_read_b64_tr_b16 a[106:107],v43 offset:25760
v_mfma_f32_16x16x32_bf16 a[12:15],a[168:171],v[244:247],a[12:15]
s_waitcnt lgkmcnt(14)
ds_read_b64_tr_b16 a[108:109],v43 offset:30880
ds_read_b64_tr_b16 a[110:111],v43 offset:36000
v_mfma_f32_16x16x32_bf16 a[12:15],a[172:175],v[252:255],a[12:15]
s_waitcnt lgkmcnt(14)
ds_read_b64_tr_b16 a[112:113],v45 offset:160
ds_read_b64_tr_b16 a[114:115],v45 offset:5280
v_mfma_f32_16x16x32_bf16 a[28:31],a[176:179],v[152:155],a[28:31]
s_waitcnt lgkmcnt(14)
ds_read_b64_tr_b16 a[116:117],v45 offset:10400
ds_read_b64_tr_b16 a[118:119],v45 offset:15520
v_mfma_f32_16x16x32_bf16 a[28:31],a[180:183],v[184:187],a[28:31]
s_waitcnt lgkmcnt(14)
ds_read_b64_tr_b16 a[120:121],v45 offset:20640
ds_read_b64_tr_b16 a[122:123],v45 offset:25760
v_mfma_f32_16x16x32_bf16 a[28:31],a[184:187],v[216:219],a[28:31]
s_waitcnt lgkmcnt(14)
ds_read_b64_tr_b16 a[124:125],v45 offset:30880
ds_read_b64_tr_b16 a[126:127],v45 offset:36000
v_mfma_f32_16x16x32_bf16 a[28:31],a[188:191],v[248:251],a[28:31]
s_waitcnt lgkmcnt(14)
ds_read_b64_tr_b16 a[128:129],v1 offset:192
ds_read_b64_tr_b16 a[130:131],v1 offset:5312
v_mfma_f32_16x16x32_bf16 a[28:31],a[96:99],v[112:115],a[28:31]
s_waitcnt lgkmcnt(14)
ds_read_b64_tr_b16 a[132:133],v1 offset:10432
ds_read_b64_tr_b16 a[134:135],v1 offset:15552
v_mfma_f32_16x16x32_bf16 a[28:31],a[100:103],v[116:119],a[28:31]
s_waitcnt lgkmcnt(14)
ds_read_b64_tr_b16 a[136:137],v1 offset:20672
ds_read_b64_tr_b16 a[138:139],v1 offset:25792
v_mfma_f32_16x16x32_bf16 a[28:31],a[104:107],v[120:123],a[28:31]
s_waitcnt lgkmcnt(14)
ds_read_b64_tr_b16 a[140:141],v1 offset:30912
ds_read_b64_tr_b16 a[142:143],v1 offset:36032
v_mfma_f32_16x16x32_bf16 a[28:31],a[108:111],v[124:127],a[28:31]
s_waitcnt lgkmcnt(14)
ds_read_b64_tr_b16 a[144:145],v42 offset:192
ds_read_b64_tr_b16 a[146:147],v42 offset:5312
v_mfma_f32_16x16x32_bf16 a[28:31],a[112:115],v[228:231],a[28:31]
s_waitcnt lgkmcnt(14)
ds_read_b64_tr_b16 a[148:149],v42 offset:10432
ds_read_b64_tr_b16 a[150:151],v42 offset:15552
v_mfma_f32_16x16x32_bf16 a[28:31],a[116:119],v[236:239],a[28:31]
s_waitcnt lgkmcnt(14)
ds_read_b64_tr_b16 a[152:153],v42 offset:20672
ds_read_b64_tr_b16 a[154:155],v42 offset:25792
v_mfma_f32_16x16x32_bf16 a[28:31],a[120:123],v[244:247],a[28:31]
s_waitcnt lgkmcnt(14)
ds_read_b64_tr_b16 a[156:157],v42 offset:30912
ds_read_b64_tr_b16 a[158:159],v42 offset:36032
v_mfma_f32_16x16x32_bf16 a[28:31],a[124:127],v[252:255],a[28:31]
s_waitcnt lgkmcnt(14)
ds_read_b64_tr_b16 a[160:161],v44 offset:192
ds_read_b64_tr_b16 a[162:163],v44 offset:5312
v_mfma_f32_16x16x32_bf16 a[44:47],a[128:131],v[152:155],a[44:47]
ds_read_b64_tr_b16 a[164:165],v44 offset:10432
ds_read_b64_tr_b16 a[166:167],v44 offset:15552
s_waitcnt vmcnt(1)
v_cmp_ne_u32 s[90:91],s68,v26
v_cmp_ne_u32 s[92:93],s69,v27
s_nop 0
s_or_b64 s[90:91],s[90:91],s[92:93]
s_cbranch_scc1 L_fr15_%=
L_fo15_%=:
s_waitcnt lgkmcnt(15)
v_mfma_f32_16x16x32_bf16 a[44:47],a[132:135],v[184:187],a[44:47]
s_waitcnt lgkmcnt(14)
buffer_load_dwordx4 v[64:67],v6,s[32:35],s76 offen sc1
ds_read_b64_tr_b16 a[168:169],v44 offset:20672
ds_read_b64_tr_b16 a[170:171],v44 offset:25792
v_mfma_f32_16x16x32_bf16 a[44:47],a[136:139],v[216:219],a[44:47]
s_lshl_b32 s94,s71,2
ds_read_b64_tr_b16 a[172:173],v44 offset:30912
ds_read_b64_tr_b16 a[174:175],v44 offset:36032
s_waitcnt lgkmcnt(15)
v_mfma_f32_16x16x32_bf16 a[44:47],a[140:143],v[248:251],a[44:47]
s_add_u32 s94,s94,s40
ds_read_b64_tr_b16 a[176:177],v2 offset:224
ds_read_b64_tr_b16 a[178:179],v2 offset:5344
s_waitcnt lgkmcnt(15)
v_mfma_f32_16x16x32_bf16 a[44:47],a[144:147],v[112:115],a[44:47]
s_lshl_b32 s94,s94,4
ds_read_b64_tr_b16 a[180:181],v2 offset:10464
ds_read_b64_tr_b16 a[182:183],v2 offset:15584
s_waitcnt lgkmcnt(15)
v_mfma_f32_16x16x32_bf16 a[44:47],a[148:151],v[116:119],a[44:47]
s_waitcnt lgkmcnt(14)
buffer_load_dwordx4 v[80:83],v6,s[32:35],s77 offen sc1
ds_read_b64_tr_b16 a[184:185],v2 offset:20704
ds_read_b64_tr_b16 a[186:187],v2 offset:25824
v_mfma_f32_16x16x32_bf16 a[44:47],a[152:155],v[120:123],a[44:47]
s_waitcnt lgkmcnt(14)
ds_read_b64_tr_b16 a[188:189],v2 offset:30944
ds_read_b64_tr_b16 a[190:191],v2 offset:36064
v_mfma_f32_16x16x32_bf16 a[44:47],a[156:159],v[124:127],a[44:47]
s_waitcnt lgkmcnt(14)
ds_read_b64_tr_b16 a[96:97],v43 offset:224
ds_read_b64_tr_b16 a[98:99],v43 offset:5344
v_mfma_f32_16x16x32_bf16 a[44:47],a[160:163],v[228:231],a[44:47]
s_waitcnt lgkmcnt(14)
ds_read_b64_tr_b16 a[100:101],v43 offset:10464
ds_read_b64_tr_b16 a[102:103],v43 offset:15584
v_mfma_f32_16x16x32_bf16 a[44:47],a[164:167],v[236:239],a[44:47]
s_waitcnt lgkmcnt(14)
buffer_load_dwordx4 v[96:99],v6,s[32:35],s78 offen sc1
ds_read_b64_tr_b16 a[104:105],v43 offset:20704
ds_read_b64_tr_b16 a[106:107],v43 offset:25824
v_mfma_f32_16x16x32_bf16 a[44:47],a[168:171],v[244:247],a[44:47]
s_waitcnt lgkmcnt(14)
ds_read_b64_tr_b16 a[108:109],v43 offset:30944
ds_read_b64_tr_b16 a[110:111],v43 offset:36064
v_mfma_f32_16x16x32_bf16 a[44:47],a[172:175],v[252:255],a[44:47]
s_waitcnt lgkmcnt(14)
ds_read_b64_tr_b16 a[112:113],v45 offset:224
ds_read_b64_tr_b16 a[114:115],v45 offset:5344
v_mfma_f32_16x16x32_bf16 a[60:63],a[176:179],v[152:155],a[60:63]
s_waitcnt lgkmcnt(14)
ds_read_b64_tr_b16 a[116:117],v45 offset:10464
ds_read_b64_tr_b16 a[118:119],v45 offset:15584
v_mfma_f32_16x16x32_bf16 a[60:63],a[180:183],v[184:187],a[60:63]
s_waitcnt lgkmcnt(14)
buffer_load_dwordx4 v[68:71],v6,s[32:35],s76 offen offset:1024 sc1
ds_read_b64_tr_b16 a[120:121],v45 offset:20704
ds_read_b64_tr_b16 a[122:123],v45 offset:25824
v_mfma_f32_16x16x32_bf16 a[60:63],a[184:187],v[216:219],a[60:63]
s_waitcnt lgkmcnt(14)
ds_read_b64_tr_b16 a[124:125],v45 offset:30944
)A"
R"A(ds_read_b64_tr_b16 a[126:127],v45 offset:36064
v_mfma_f32_16x16x32_bf16 a[60:63],a[188:191],v[248:251],a[60:63]
s_waitcnt lgkmcnt(14)
ds_read_b64_tr_b16 a[128:129],v1 offset:256
ds_read_b64_tr_b16 a[130:131],v1 offset:5376
v_mfma_f32_16x16x32_bf16 a[60:63],a[96:99],v[112:115],a[60:63]
s_waitcnt lgkmcnt(14)
ds_read_b64_tr_b16 a[132:133],v1 offset:10496
ds_read_b64_tr_b16 a[134:135],v1 offset:15616
v_mfma_f32_16x16x32_bf16 a[60:63],a[100:103],v[116:119],a[60:63]
s_waitcnt lgkmcnt(14)
buffer_load_dwordx4 v[84:87],v6,s[32:35],s77 offen offset:1024 sc1
ds_read_b64_tr_b16 a[136:137],v1 offset:20736
ds_read_b64_tr_b16 a[138:139],v1 offset:25856
v_mfma_f32_16x16x32_bf16 a[60:63],a[104:107],v[120:123],a[60:63]
s_waitcnt lgkmcnt(14)
ds_read_b64_tr_b16 a[140:141],v1 offset:30976
ds_read_b64_tr_b16 a[142:143],v1 offset:36096
v_mfma_f32_16x16x32_bf16 a[60:63],a[108:111],v[124:127],a[60:63]
s_waitcnt lgkmcnt(14)
ds_read_b64_tr_b16 a[144:145],v42 offset:256
ds_read_b64_tr_b16 a[146:147],v42 offset:5376
v_mfma_f32_16x16x32_bf16 a[60:63],a[112:115],v[228:231],a[60:63]
s_waitcnt lgkmcnt(14)
ds_read_b64_tr_b16 a[148:149],v42 offset:10496
ds_read_b64_tr_b16 a[150:151],v42 offset:15616
v_mfma_f32_16x16x32_bf16 a[60:63],a[116:119],v[236:239],a[60:63]
s_waitcnt lgkmcnt(14)
buffer_load_dwordx4 v[100:103],v6,s[32:35],s78 offen offset:1024 sc1
ds_read_b64_tr_b16 a[152:153],v42 offset:20736
ds_read_b64_tr_b16 a[154:155],v42 offset:25856
v_mfma_f32_16x16x32_bf16 a[60:63],a[120:123],v[244:247],a[60:63]
s_waitcnt lgkmcnt(14)
ds_read_b64_tr_b16 a[156:157],v42 offset:30976
ds_read_b64_tr_b16 a[158:159],v42 offset:36096
v_mfma_f32_16x16x32_bf16 a[60:63],a[124:127],v[252:255],a[60:63]
s_waitcnt lgkmcnt(14)
ds_read_b64_tr_b16 a[160:161],v44 offset:256
ds_read_b64_tr_b16 a[162:163],v44 offset:5376
v_mfma_f32_16x16x32_bf16 a[76:79],a[128:131],v[152:155],a[76:79]
s_waitcnt lgkmcnt(14)
ds_read_b64_tr_b16 a[164:165],v44 offset:10496
ds_read_b64_tr_b16 a[166:167],v44 offset:15616
v_mfma_f32_16x16x32_bf16 a[76:79],a[132:135],v[184:187],a[76:79]
s_waitcnt lgkmcnt(14)
buffer_load_dwordx4 v[72:75],v6,s[32:35],s76 offen offset:2048 sc1
ds_read_b64_tr_b16 a[168:169],v44 offset:20736
ds_read_b64_tr_b16 a[170:171],v44 offset:25856
v_bfe_u32 v24,v6,4,2
v_min_u32 v24,2,v24
v_mfma_f32_16x16x32_bf16 a[76:79],a[136:139],v[216:219],a[76:79]
s_waitcnt lgkmcnt(14)
ds_read_b64_tr_b16 a[172:173],v44 offset:30976
ds_read_b64_tr_b16 a[174:175],v44 offset:36096
v_add_u32 v24,s70,v24
v_add_u32 v24,1,v24
v_mfma_f32_16x16x32_bf16 a[76:79],a[140:143],v[248:251],a[76:79]
s_waitcnt lgkmcnt(14)
ds_read_b64_tr_b16 a[176:177],v2 offset:288
ds_read_b64_tr_b16 a[178:179],v2 offset:5408
v_and_b32 v24,3,v24
v_lshlrev_b32 v24,8,v24
v_mfma_f32_16x16x32_bf16 a[76:79],a[144:147],v[112:115],a[76:79]
s_waitcnt lgkmcnt(14)
ds_read_b64_tr_b16 a[180:181],v2 offset:10528
ds_read_b64_tr_b16 a[182:183],v2 offset:15648
v_mfma_f32_16x16x32_bf16 a[76:79],a[148:151],v[116:119],a[76:79]
s_waitcnt lgkmcnt(14)
buffer_load_dwordx4 v[88:91],v6,s[32:35],s77 offen offset:2048 sc1
ds_read_b64_tr_b16 a[184:185],v2 offset:20768
ds_read_b64_tr_b16 a[186:187],v2 offset:25888
v_mfma_f32_16x16x32_bf16 a[76:79],a[152:155],v[120:123],a[76:79]
s_waitcnt lgkmcnt(14)
ds_read_b64_tr_b16 a[188:189],v2 offset:31008
ds_read_b64_tr_b16 a[190:191],v2 offset:36128
v_mfma_f32_16x16x32_bf16 a[76:79],a[156:159],v[124:127],a[76:79]
s_waitcnt lgkmcnt(14)
ds_read_b64_tr_b16 a[96:97],v43 offset:288
ds_read_b64_tr_b16 a[98:99],v43 offset:5408
v_mfma_f32_16x16x32_bf16 a[76:79],a[160:163],v[228:231],a[76:79]
s_waitcnt lgkmcnt(14)
ds_read_b64_tr_b16 a[100:101],v43 offset:10528
ds_read_b64_tr_b16 a[102:103],v43 offset:15648
v_mfma_f32_16x16x32_bf16 a[76:79],a[164:167],v[236:239],a[76:79]
s_waitcnt lgkmcnt(14)
buffer_load_dwordx4 v[104:107],v6,s[32:35],s78 offen offset:2048 sc1
ds_read_b64_tr_b16 a[104:105],v43 offset:20768
ds_read_b64_tr_b16 a[106:107],v43 offset:25888
v_mfma_f32_16x16x32_bf16 a[76:79],a[168:171],v[244:247],a[76:79]
s_waitcnt lgkmcnt(14)
ds_read_b64_tr_b16 a[108:109],v43 offset:31008
ds_read_b64_tr_b16 a[110:111],v43 offset:36128
v_mfma_f32_16x16x32_bf16 a[76:79],a[172:175],v[252:255],a[76:79]
s_waitcnt lgkmcnt(14)
ds_read_b64_tr_b16 a[112:113],v45 offset:288
ds_read_b64_tr_b16 a[114:115],v45 offset:5408
v_mfma_f32_16x16x32_bf16 a[92:95],a[176:179],v[152:155],a[92:95]
s_waitcnt lgkmcnt(14)
ds_read_b64_tr_b16 a[116:117],v45 offset:10528
ds_read_b64_tr_b16 a[118:119],v45 offset:15648
v_mfma_f32_16x16x32_bf16 a[92:95],a[180:183],v[184:187],a[92:95]
s_waitcnt lgkmcnt(14)
buffer_load_dwordx4 v[76:79],v6,s[32:35],s76 offen offset:3072 sc1
ds_read_b64_tr_b16 a[120:121],v45 offset:20768
ds_read_b64_tr_b16 a[122:123],v45 offset:25888
v_mfma_f32_16x16x32_bf16 a[92:95],a[184:187],v[216:219],a[92:95]
s_waitcnt lgkmcnt(14)
ds_read_b64_tr_b16 a[124:125],v45 offset:31008
ds_read_b64_tr_b16 a[126:127],v45 offset:36128
v_mfma_f32_16x16x32_bf16 a[92:95],a[188:191],v[248:251],a[92:95]
s_waitcnt lgkmcnt(14)
v_mfma_f32_16x16x32_bf16 a[92:95],a[96:99],v[112:115],a[92:95]
s_waitcnt lgkmcnt(12)
v_mfma_f32_16x16x32_bf16 a[92:95],a[100:103],v[116:119],a[92:95]
s_waitcnt lgkmcnt(10)
buffer_load_dwordx4 v[92:95],v6,s[32:35],s77 offen offset:3072 sc1
v_mfma_f32_16x16x32_bf16 a[92:95],a[104:107],v[120:123],a[92:95]
s_waitcnt lgkmcnt(8)
v_mfma_f32_16x16x32_bf16 a[92:95],a[108:111],v[124:127],a[92:95]
s_waitcnt lgkmcnt(6)
v_mfma_f32_16x16x32_bf16 a[92:95],a[112:115],v[228:231],a[92:95]
s_waitcnt lgkmcnt(4)
v_mfma_f32_16x16x32_bf16 a[92:95],a[116:119],v[236:239],a[92:95]
s_waitcnt lgkmcnt(2)
buffer_load_dwordx4 v[108:111],v6,s[32:35],s78 offen offset:3072 sc1
v_mfma_f32_16x16x32_bf16 a[92:95],a[120:123],v[244:247],a[92:95]
s_waitcnt lgkmcnt(0)
v_mfma_f32_16x16x32_bf16 a[92:95],a[124:127],v[252:255],a[92:95]
buffer_load_dwordx2 v[26:27],v24,s[64:67],s94 offen offset:3072 sc1
s_waitcnt vmcnt(0)
v_cmp_ne_u32 s[90:91],s68,v26
v_cmp_ne_u32 s[92:93],s69,v27
s_nop 0
s_or_b64 s[90:91],s[90:91],s[92:93]
s_cbranch_scc1 L_fr16_%=
L_fo16_%=:
buffer_load_dwordx4 v[132:135],v25,s[32:35],s76 offen sc1
buffer_load_dwordx4 v[164:167],v25,s[32:35],s77 offen sc1
buffer_load_dwordx4 v[196:199],v25,s[32:35],s78 offen sc1
v_mfma_f32_16x16x32_bf16 a[204:207],v[34:37],v[64:67],a[204:207]
v_mfma_f32_16x16x32_bf16 a[204:207],v[34:37],v[80:83],a[204:207]
v_mfma_f32_16x16x32_bf16 a[204:207],v[34:37],v[96:99],a[204:207]
v_mfma_f32_16x16x32_bf16 a[220:223],v[32:35],v[64:67],a[220:223]
v_mfma_f32_16x16x32_bf16 a[220:223],v[32:35],v[80:83],a[220:223]
v_mfma_f32_16x16x32_bf16 a[220:223],v[32:35],v[96:99],a[220:223]
v_mfma_f32_16x16x32_bf16 a[236:239],v[34:37],v[68:71],a[236:239]
v_mfma_f32_16x16x32_bf16 a[236:239],v[34:37],v[84:87],a[236:239]
v_mfma_f32_16x16x32_bf16 a[236:239],v[34:37],v[100:103],a[236:239]
s_nop 1
v_accvgpr_read_b32 v128,a204
v_accvgpr_read_b32 v129,a205
v_mfma_f32_16x16x32_bf16 a[252:255],v[32:35],v[68:71],a[252:255]
v_cvt_pk_bf16_f32 v128,v128,v129
v_accvgpr_read_b32 v130,a206
v_mfma_f32_16x16x32_bf16 a[252:255],v[32:35],v[84:87],a[252:255]
v_accvgpr_read_b32 v131,a207
v_cvt_pk_bf16_f32 v129,v130,v131
v_mfma_f32_16x16x32_bf16 a[252:255],v[32:35],v[100:103],a[252:255]
buffer_store_dwordx2 v[128:129],v40,s[20:23],s43 offen
v_mfma_f32_16x16x32_bf16 a[12:15],v[34:37],v[72:75],a[12:15]
v_accvgpr_read_b32 v130,a220
v_accvgpr_read_b32 v131,a221
v_mfma_f32_16x16x32_bf16 a[12:15],v[34:37],v[88:91],a[12:15]
v_accvgpr_read_b32 v14,a222
v_accvgpr_read_b32 v15,a223
v_mfma_f32_16x16x32_bf16 a[12:15],v[34:37],v[104:107],a[12:15]
v_cvt_pk_bf16_f32 v130,v130,v131
v_cvt_pk_bf16_f32 v131,v14,v15
v_mfma_f32_16x16x32_bf16 a[28:31],v[32:35],v[72:75],a[28:31]
buffer_store_dwordx2 v[130:131],v40,s[20:23],s43 offen offset:32
v_mfma_f32_16x16x32_bf16 a[28:31],v[32:35],v[88:91],a[28:31]
v_accvgpr_read_b32 v136,a236
v_accvgpr_read_b32 v137,a237
v_mfma_f32_16x16x32_bf16 a[28:31],v[32:35],v[104:107],a[28:31]
v_cvt_pk_bf16_f32 v136,v136,v137
v_accvgpr_read_b32 v138,a238
v_mfma_f32_16x16x32_bf16 a[44:47],v[34:37],v[76:79],a[44:47]
v_accvgpr_read_b32 v139,a239
v_cvt_pk_bf16_f32 v137,v138,v139
v_mfma_f32_16x16x32_bf16 a[44:47],v[34:37],v[92:95],a[44:47]
buffer_store_dwordx2 v[136:137],v40,s[20:23],s43 offen offset:64
v_mfma_f32_16x16x32_bf16 a[44:47],v[34:37],v[108:111],a[44:47]
v_accvgpr_read_b32 v144,a12
v_accvgpr_read_b32 v145,a13
v_cvt_pk_bf16_f32 v144,v144,v145
v_accvgpr_read_b32 v146,a14
v_accvgpr_read_b32 v147,a15
v_mfma_f32_16x16x32_bf16 a[60:63],v[32:35],v[76:79],a[60:63]
v_accvgpr_read_b32 v138,a252
v_accvgpr_read_b32 v139,a253
v_cvt_pk_bf16_f32 v145,v146,v147
v_mfma_f32_16x16x32_bf16 a[60:63],v[32:35],v[92:95],a[60:63]
v_accvgpr_read_b32 v16,a254
v_accvgpr_read_b32 v17,a255
v_cvt_pk_bf16_f32 v138,v138,v139
v_cvt_pk_bf16_f32 v139,v16,v17
v_mfma_f32_16x16x32_bf16 a[60:63],v[32:35],v[108:111],a[60:63]
s_waitcnt vmcnt(5)
buffer_store_dwordx2 v[138:139],v40,s[20:23],s43 offen offset:96
buffer_store_dwordx2 v[144:145],v40,s[20:23],s43 offen offset:128
v_accvgpr_read_b32 v146,a28
v_accvgpr_read_b32 v147,a29
v_accvgpr_read_b32 v12,a30
v_accvgpr_read_b32 v13,a31
v_cvt_pk_bf16_f32 v146,v146,v147
v_cvt_pk_bf16_f32 v147,v12,v13
s_nop 3
buffer_store_dwordx2 v[146:147],v40,s[20:23],s43 offen offset:160
v_accvgpr_read_b32 v160,a44
v_accvgpr_read_b32 v161,a45
v_cvt_pk_bf16_f32 v160,v160,v161
v_accvgpr_read_b32 v162,a46
v_accvgpr_read_b32 v163,a47
v_cvt_pk_bf16_f32 v161,v162,v163
buffer_store_dwordx2 v[160:161],v40,s[20:23],s43 offen offset:192
v_accvgpr_read_b32 v162,a60
v_accvgpr_read_b32 v163,a61
v_accvgpr_read_b32 v14,a62
v_accvgpr_read_b32 v15,a63
v_cvt_pk_bf16_f32 v162,v162,v163
v_cvt_pk_bf16_f32 v163,v14,v15
buffer_store_dwordx2 v[162:163],v40,s[20:23],s43 offen offset:224
v_mfma_f32_16x16x32_bf16 a[76:79],v[34:37],v[132:135],a[76:79]
s_waitcnt vmcnt(9)
v_mfma_f32_16x16x32_bf16 a[76:79],v[34:37],v[164:167],a[76:79]
s_waitcnt vmcnt(8)
v_mfma_f32_16x16x32_bf16 a[76:79],v[34:37],v[196:199],a[76:79]
v_mfma_f32_16x16x32_bf16 a[92:95],v[32:35],v[132:135],a[92:95]
v_mfma_f32_16x16x32_bf16 a[92:95],v[32:35],v[164:167],a[92:95]
v_mfma_f32_16x16x32_bf16 a[92:95],v[32:35],v[196:199],a[92:95]
s_nop 3
s_nop 0
v_accvgpr_read_b32 v168,a76
v_accvgpr_read_b32 v169,a77
v_cvt_pk_bf16_f32 v168,v168,v169
v_accvgpr_read_b32 v170,a78
v_accvgpr_read_b32 v171,a79
v_cvt_pk_bf16_f32 v169,v170,v171
buffer_store_dwordx2 v[168:169],v40,s[20:23],s43 offen offset:256
v_accvgpr_read_b32 v170,a92
v_accvgpr_read_b32 v171,a93
v_accvgpr_read_b32 v16,a94
v_accvgpr_read_b32 v17,a95
v_cvt_pk_bf16_f32 v170,v170,v171
v_cvt_pk_bf16_f32 v171,v16,v17
buffer_store_dwordx2 v[170:171],v40,s[20:23],s43 offen offset:288
s_or_b32 s62,s42,s40
s_cmp_lg_u32 s62,0
s_cbranch_scc1 L_noep_%=
v_mov_b32 v12,s68
s_mov_b64 exec,1
buffer_store_dword v12,v25,s[64:67],0 offen sc1
s_mov_b64 exec,-1
L_noep_%=:
s_waitcnt vmcnt(0) lgkmcnt(0)
s_branch L_end_%=
L_ps2_%=:
s_min_i32 s79,s79,100
s_add_u32 s78,s78,s79
L_pl2_%=:
s_sleep 1
s_sub_u32 s79,s79,4
s_cmp_gt_i32 s79,1
s_cbranch_scc1 L_pl2_%=
s_branch L_pr2_%=
L_ps3_%=:
s_min_i32 s79,s79,100
s_add_u32 s78,s78,s79
L_pl3_%=:
s_sleep 1
s_sub_u32 s79,s79,4
s_cmp_gt_i32 s79,1
s_cbranch_scc1 L_pl3_%=
s_branch L_pr3_%=
L_ps4_%=:
s_min_i32 s79,s79,100
s_add_u32 s78,s78,s79
L_pl4_%=:
s_sleep 1
s_sub_u32 s79,s79,4
s_cmp_gt_i32 s79,1
s_cbranch_scc1 L_pl4_%=
s_branch L_pr4_%=
L_ps5_%=:
s_min_i32 s79,s79,100
s_add_u32 s78,s78,s79
L_pl5_%=:
s_sleep 1
s_sub_u32 s79,s79,4
s_cmp_gt_i32 s79,1
s_cbranch_scc1 L_pl5_%=
s_branch L_pr5_%=
L_ps6_%=:
s_min_i32 s79,s79,100
s_add_u32 s78,s78,s79
L_pl6_%=:
s_sleep 1
s_sub_u32 s79,s79,4
s_cmp_gt_i32 s79,1
s_cbranch_scc1 L_pl6_%=
s_branch L_pr6_%=
L_ps7_%=:
s_min_i32 s79,s79,100
s_add_u32 s78,s78,s79
L_pl7_%=:
s_sleep 1
s_sub_u32 s79,s79,4
s_cmp_gt_i32 s79,1
s_cbranch_scc1 L_pl7_%=
s_branch L_pr7_%=
L_ps8_%=:
s_min_i32 s79,s79,100
s_add_u32 s78,s78,s79
L_pl8_%=:
s_sleep 1
s_sub_u32 s79,s79,4
s_cmp_gt_i32 s79,1
s_cbranch_scc1 L_pl8_%=
s_branch L_pr8_%=
L_ps9_%=:
s_min_i32 s79,s79,100
s_add_u32 s78,s78,s79
L_pl9_%=:
s_sleep 1
s_sub_u32 s79,s79,4
s_cmp_gt_i32 s79,1
)A"
R"A(s_cbranch_scc1 L_pl9_%=
s_branch L_pr9_%=
L_ps10_%=:
s_min_i32 s79,s79,100
s_add_u32 s78,s78,s79
L_pl10_%=:
s_sleep 1
s_sub_u32 s79,s79,4
s_cmp_gt_i32 s79,1
s_cbranch_scc1 L_pl10_%=
s_branch L_pr10_%=
L_ps11_%=:
s_min_i32 s79,s79,100
s_add_u32 s78,s78,s79
L_pl11_%=:
s_sleep 1
s_sub_u32 s79,s79,4
s_cmp_gt_i32 s79,1
s_cbranch_scc1 L_pl11_%=
s_branch L_pr11_%=
L_ps12_%=:
s_min_i32 s79,s79,100
s_add_u32 s78,s78,s79
L_pl12_%=:
s_sleep 1
s_sub_u32 s79,s79,4
s_cmp_gt_i32 s79,1
s_cbranch_scc1 L_pl12_%=
s_branch L_pr12_%=
L_fr13_%=:
s_mov_b32 s88,262144
L_fl13_%=:
buffer_load_dwordx2 v[26:27],v24,s[64:67],s94 offen sc1
s_waitcnt vmcnt(0)
s_sub_u32 s88,s88,1
s_cmp_eq_u32 s88,0
s_cbranch_scc1 L_fo13_%=
v_cmp_ne_u32 s[90:91],s68,v26
v_cmp_ne_u32 s[92:93],s69,v27
s_nop 0
s_or_b64 s[90:91],s[90:91],s[92:93]
s_cbranch_scc1 L_fl13_%=
s_branch L_fo13_%=
L_fr14_%=:
s_mov_b32 s88,262144
L_fl14_%=:
buffer_load_dwordx2 v[26:27],v24,s[64:67],s94 offen offset:1024 sc1
s_waitcnt vmcnt(0)
s_sub_u32 s88,s88,1
s_cmp_eq_u32 s88,0
s_cbranch_scc1 L_fo14_%=
v_cmp_ne_u32 s[90:91],s68,v26
v_cmp_ne_u32 s[92:93],s69,v27
s_nop 0
s_or_b64 s[90:91],s[90:91],s[92:93]
s_cbranch_scc1 L_fl14_%=
s_branch L_fo14_%=
L_fr15_%=:
s_mov_b32 s88,262144
L_fl15_%=:
buffer_load_dwordx2 v[26:27],v24,s[64:67],s94 offen offset:2048 sc1
s_waitcnt vmcnt(0)
s_sub_u32 s88,s88,1
s_cmp_eq_u32 s88,0
s_cbranch_scc1 L_fo15_%=
v_cmp_ne_u32 s[90:91],s68,v26
v_cmp_ne_u32 s[92:93],s69,v27
s_nop 0
s_or_b64 s[90:91],s[90:91],s[92:93]
s_cbranch_scc1 L_fl15_%=
s_branch L_fo15_%=
L_fr16_%=:
s_mov_b32 s88,262144
L_fl16_%=:
buffer_load_dwordx2 v[26:27],v24,s[64:67],s94 offen offset:3072 sc1
s_waitcnt vmcnt(0)
s_sub_u32 s88,s88,1
s_cmp_eq_u32 s88,0
s_cbranch_scc1 L_fo16_%=
v_cmp_ne_u32 s[90:91],s68,v26
v_cmp_ne_u32 s[92:93],s69,v27
s_nop 0
s_or_b64 s[90:91],s[90:91],s[92:93]
s_cbranch_scc1 L_fl16_%=
s_branch L_fo16_%=
L_out_end_%=:
L_fast_end_%=:
L_slow0_%=:
s_mov_b32 s98,0
s_branch L_slowc_%=
L_slow1_%=:
s_mov_b32 s98,1
L_slowc_%=:
s_nop 7
s_nop 7
s_waitcnt vmcnt(0) lgkmcnt(0)
s_lshl_b32 s96,%1,2
s_add_u32 s96,s96,s40
s_lshl_b32 s96,s96,15
s_add_u32 s96,s96,167772160
s_mov_b64 s[56:57],s[12:13]
s_add_u32 s56,s56,s96
s_addc_u32 s57,s57,0
s_and_b32 s57,s57,0xffff
s_mov_b32 s58,32768
s_mov_b32 s59,0x20000
s_mov_b32 s96,0
buffer_store_dwordx4 v[64:67],v6,s[56:59],s96 offen
buffer_store_dwordx4 v[68:71],v6,s[56:59],s96 offen offset:1024
buffer_store_dwordx4 v[72:75],v6,s[56:59],s96 offen offset:2048
buffer_store_dwordx4 v[76:79],v6,s[56:59],s96 offen offset:3072
s_mov_b32 s96,4096
buffer_store_dwordx4 v[80:83],v6,s[56:59],s96 offen
buffer_store_dwordx4 v[84:87],v6,s[56:59],s96 offen offset:1024
buffer_store_dwordx4 v[88:91],v6,s[56:59],s96 offen offset:2048
buffer_store_dwordx4 v[92:95],v6,s[56:59],s96 offen offset:3072
s_mov_b32 s96,8192
buffer_store_dwordx4 v[96:99],v6,s[56:59],s96 offen
buffer_store_dwordx4 v[100:103],v6,s[56:59],s96 offen offset:1024
buffer_store_dwordx4 v[104:107],v6,s[56:59],s96 offen offset:2048
buffer_store_dwordx4 v[108:111],v6,s[56:59],s96 offen offset:3072
s_mov_b32 s96,12288
buffer_store_dwordx4 v[112:115],v6,s[56:59],s96 offen
buffer_store_dwordx4 v[116:119],v6,s[56:59],s96 offen offset:1024
buffer_store_dwordx4 v[120:123],v6,s[56:59],s96 offen offset:2048
buffer_store_dwordx4 v[124:127],v6,s[56:59],s96 offen offset:3072
s_mov_b32 s96,16384
buffer_store_dwordx4 v[128:131],v6,s[56:59],s96 offen
buffer_store_dwordx4 v[132:135],v6,s[56:59],s96 offen offset:1024
buffer_store_dwordx4 v[136:139],v6,s[56:59],s96 offen offset:2048
buffer_store_dwordx4 v[140:143],v6,s[56:59],s96 offen offset:3072
s_mov_b32 s96,20480
buffer_store_dwordx4 v[144:147],v6,s[56:59],s96 offen
buffer_store_dwordx4 v[148:151],v6,s[56:59],s96 offen offset:1024
buffer_store_dwordx4 v[152:155],v6,s[56:59],s96 offen offset:2048
buffer_store_dwordx4 v[156:159],v6,s[56:59],s96 offen offset:3072
s_mov_b32 s96,24576
buffer_store_dwordx4 v[160:163],v6,s[56:59],s96 offen
buffer_store_dwordx4 v[164:167],v6,s[56:59],s96 offen offset:1024
buffer_store_dwordx4 v[168:171],v6,s[56:59],s96 offen offset:2048
buffer_store_dwordx4 v[172:175],v6,s[56:59],s96 offen offset:3072
s_mov_b32 s96,28672
buffer_store_dwordx4 v[176:179],v6,s[56:59],s96 offen
buffer_store_dwordx4 v[180:183],v6,s[56:59],s96 offen offset:1024
buffer_store_dwordx4 v[184:187],v6,s[56:59],s96 offen offset:2048
buffer_store_dwordx4 v[188:191],v6,s[56:59],s96 offen offset:3072
s_waitcnt vmcnt(0)
v_writelane_b32 v173,s88,0
v_writelane_b32 v173,s89,1
v_writelane_b32 v173,s90,2
v_writelane_b32 v173,s91,3
v_writelane_b32 v173,s92,4
v_writelane_b32 v173,s93,5
v_writelane_b32 v173,s94,6
v_writelane_b32 v173,s95,7
s_mov_b32 s88,s98
s_mul_i32 s90,s41,307200
s_mov_b64 s[92:93],s[4:5]
s_add_u32 s92,s92,s90
s_addc_u32 s93,s93,0
s_and_b32 s93,s93,0xffff
s_mov_b32 s94,307200
s_mov_b32 s95,0x20000
s_mov_b64 s[96:97],s[10:11]
s_and_b32 s97,s97,0xffff
s_mov_b32 s98,5242880
s_mov_b32 s99,0x20000
v_bfe_u32 v171,v6,4,4
v_lshrrev_b32 v172,8,v6
v_lshlrev_b32 v164,8,v171
v_lshl_add_u32 v164,v172,4,v164
v_lshlrev_b32 v165,10,v172
v_lshl_add_u32 v165,v171,1,v165
v_add_u32 v166,4096,v165
v_lshlrev_b32 v168,16,v172
v_lshrrev_b32 v170,2,v171
v_lshl_add_u32 v168,v170,4,v168
v_and_b32 v170,3,v171
v_lshl_add_u32 v168,v170,1,v168
s_lshl_b32 s90,s42,1
s_add_u32 s90,s90,s88
s_lshl_b32 s90,s90,8
v_add_u32 v168,s90,v168
s_mov_b32 s89,0
L_sjb_%=:
s_add_u32 s90,s89,s70
s_add_u32 s90,s90,1
s_and_b32 s90,s90,3
s_lshl_b32 s91,s40,2
s_add_u32 s90,s90,s91
s_lshl_b32 s90,s90,4
v_bfe_u32 v171,v6,4,4
v_lshrrev_b32 v172,8,v6
v_add_u32 v167,s90,v171
v_mul_u32_u24 v167,1280,v167
v_lshl_add_u32 v167,v172,4,v167
s_lshl_b32 s90,s88,4
s_add_u32 s90,s90,s89
s_lshl_b32 s90,s90,12
v_add_u32 v169,s90,v6
v_mov_b32 v64,0
v_mov_b32 v65,0
v_mov_b32 v66,0
v_mov_b32 v67,0
v_mov_b32 v68,0
v_mov_b32 v69,0
v_mov_b32 v70,0
v_mov_b32 v71,0
v_mov_b32 v72,0
v_mov_b32 v73,0
v_mov_b32 v74,0
v_mov_b32 v75,0
v_mov_b32 v76,0
v_mov_b32 v77,0
v_mov_b32 v78,0
v_mov_b32 v79,0
v_mov_b32 v80,0
v_mov_b32 v81,0
v_mov_b32 v82,0
v_mov_b32 v83,0
v_mov_b32 v84,0
v_mov_b32 v85,0
v_mov_b32 v86,0
v_mov_b32 v87,0
v_mov_b32 v88,0
v_mov_b32 v89,0
v_mov_b32 v90,0
v_mov_b32 v91,0
v_mov_b32 v92,0
v_mov_b32 v93,0
v_mov_b32 v94,0
v_mov_b32 v95,0
s_mov_b32 s90,0
L_sq_%=:
s_lshl_b32 s91,s90,6
buffer_load_dwordx4 v[160:163],v167,s[92:95],s91 offen
s_lshl_b32 s91,s90,18
buffer_load_ushort v112,v168,s[96:99],s91 offen
buffer_load_ushort v120,v168,s[96:99],s91 offen offset:8
buffer_load_ushort v128,v168,s[96:99],s91 offen offset:64
buffer_load_ushort v136,v168,s[96:99],s91 offen offset:72
s_add_u32 s91,s91,8192
buffer_load_ushort v113,v168,s[96:99],s91 offen
buffer_load_ushort v121,v168,s[96:99],s91 offen offset:8
buffer_load_ushort v129,v168,s[96:99],s91 offen offset:64
buffer_load_ushort v137,v168,s[96:99],s91 offen offset:72
s_add_u32 s91,s91,8192
buffer_load_ushort v114,v168,s[96:99],s91 offen
buffer_load_ushort v122,v168,s[96:99],s91 offen offset:8
buffer_load_ushort v130,v168,s[96:99],s91 offen offset:64
buffer_load_ushort v138,v168,s[96:99],s91 offen offset:72
s_add_u32 s91,s91,8192
buffer_load_ushort v115,v168,s[96:99],s91 offen
buffer_load_ushort v123,v168,s[96:99],s91 offen offset:8
buffer_load_ushort v131,v168,s[96:99],s91 offen offset:64
buffer_load_ushort v139,v168,s[96:99],s91 offen offset:72
s_add_u32 s91,s91,8192
buffer_load_ushort v116,v168,s[96:99],s91 offen
buffer_load_ushort v124,v168,s[96:99],s91 offen offset:8
buffer_load_ushort v132,v168,s[96:99],s91 offen offset:64
buffer_load_ushort v140,v168,s[96:99],s91 offen offset:72
s_add_u32 s91,s91,8192
buffer_load_ushort v117,v168,s[96:99],s91 offen
buffer_load_ushort v125,v168,s[96:99],s91 offen offset:8
buffer_load_ushort v133,v168,s[96:99],s91 offen offset:64
buffer_load_ushort v141,v168,s[96:99],s91 offen offset:72
s_add_u32 s91,s91,8192
buffer_load_ushort v118,v168,s[96:99],s91 offen
buffer_load_ushort v126,v168,s[96:99],s91 offen offset:8
buffer_load_ushort v134,v168,s[96:99],s91 offen offset:64
buffer_load_ushort v142,v168,s[96:99],s91 offen offset:72
s_add_u32 s91,s91,8192
buffer_load_ushort v119,v168,s[96:99],s91 offen
buffer_load_ushort v127,v168,s[96:99],s91 offen offset:8
buffer_load_ushort v135,v168,s[96:99],s91 offen offset:64
buffer_load_ushort v143,v168,s[96:99],s91 offen offset:72
s_add_u32 s91,s91,8192
s_waitcnt vmcnt(0)
v_lshl_or_b32 v112,v113,16,v112
v_lshl_or_b32 v113,v115,16,v114
v_lshl_or_b32 v114,v117,16,v116
v_lshl_or_b32 v115,v119,16,v118
v_lshl_or_b32 v120,v121,16,v120
v_lshl_or_b32 v121,v123,16,v122
v_lshl_or_b32 v122,v125,16,v124
v_lshl_or_b32 v123,v127,16,v126
v_lshl_or_b32 v128,v129,16,v128
v_lshl_or_b32 v129,v131,16,v130
v_lshl_or_b32 v130,v133,16,v132
v_lshl_or_b32 v131,v135,16,v134
v_lshl_or_b32 v136,v137,16,v136
v_lshl_or_b32 v137,v139,16,v138
v_lshl_or_b32 v138,v141,16,v140
v_lshl_or_b32 v139,v143,16,v142
s_nop 1
v_mfma_f32_16x16x32_bf16 v[64:67],v[112:115],v[160:163],v[64:67]
v_mfma_f32_16x16x32_bf16 v[68:71],v[120:123],v[160:163],v[68:71]
v_mfma_f32_16x16x32_bf16 v[72:75],v[128:131],v[160:163],v[72:75]
v_mfma_f32_16x16x32_bf16 v[76:79],v[136:139],v[160:163],v[76:79]
s_nop 7
s_lshl_b32 s91,s90,18
buffer_load_ushort v112,v168,s[96:99],s91 offen offset:128
buffer_load_ushort v120,v168,s[96:99],s91 offen offset:136
buffer_load_ushort v128,v168,s[96:99],s91 offen offset:192
buffer_load_ushort v136,v168,s[96:99],s91 offen offset:200
s_add_u32 s91,s91,8192
buffer_load_ushort v113,v168,s[96:99],s91 offen offset:128
buffer_load_ushort v121,v168,s[96:99],s91 offen offset:136
buffer_load_ushort v129,v168,s[96:99],s91 offen offset:192
buffer_load_ushort v137,v168,s[96:99],s91 offen offset:200
s_add_u32 s91,s91,8192
buffer_load_ushort v114,v168,s[96:99],s91 offen offset:128
buffer_load_ushort v122,v168,s[96:99],s91 offen offset:136
buffer_load_ushort v130,v168,s[96:99],s91 offen offset:192
buffer_load_ushort v138,v168,s[96:99],s91 offen offset:200
s_add_u32 s91,s91,8192
buffer_load_ushort v115,v168,s[96:99],s91 offen offset:128
buffer_load_ushort v123,v168,s[96:99],s91 offen offset:136
buffer_load_ushort v131,v168,s[96:99],s91 offen offset:192
buffer_load_ushort v139,v168,s[96:99],s91 offen offset:200
s_add_u32 s91,s91,8192
buffer_load_ushort v116,v168,s[96:99],s91 offen offset:128
buffer_load_ushort v124,v168,s[96:99],s91 offen offset:136
buffer_load_ushort v132,v168,s[96:99],s91 offen offset:192
buffer_load_ushort v140,v168,s[96:99],s91 offen offset:200
s_add_u32 s91,s91,8192
buffer_load_ushort v117,v168,s[96:99],s91 offen offset:128
buffer_load_ushort v125,v168,s[96:99],s91 offen offset:136
buffer_load_ushort v133,v168,s[96:99],s91 offen offset:192
buffer_load_ushort v141,v168,s[96:99],s91 offen offset:200
s_add_u32 s91,s91,8192
buffer_load_ushort v118,v168,s[96:99],s91 offen offset:128
buffer_load_ushort v126,v168,s[96:99],s91 offen offset:136
buffer_load_ushort v134,v168,s[96:99],s91 offen offset:192
buffer_load_ushort v142,v168,s[96:99],s91 offen offset:200
s_add_u32 s91,s91,8192
buffer_load_ushort v119,v168,s[96:99],s91 offen offset:128
buffer_load_ushort v127,v168,s[96:99],s91 offen offset:136
buffer_load_ushort v135,v168,s[96:99],s91 offen offset:192
buffer_load_ushort v143,v168,s[96:99],s91 offen offset:200
s_add_u32 s91,s91,8192
s_waitcnt vmcnt(0)
v_lshl_or_b32 v112,v113,16,v112
v_lshl_or_b32 v113,v115,16,v114
v_lshl_or_b32 v114,v117,16,v116
v_lshl_or_b32 v115,v119,16,v118
v_lshl_or_b32 v120,v121,16,v120
v_lshl_or_b32 v121,v123,16,v122
v_lshl_or_b32 v122,v125,16,v124
v_lshl_or_b32 v123,v127,16,v126
v_lshl_or_b32 v128,v129,16,v128
v_lshl_or_b32 v129,v131,16,v130
v_lshl_or_b32 v130,v133,16,v132
v_lshl_or_b32 v131,v135,16,v134
v_lshl_or_b32 v136,v137,16,v136
v_lshl_or_b32 v137,v139,16,v138
v_lshl_or_b32 v138,v141,16,v140
v_lshl_or_b32 v139,v143,16,v142
s_nop 1
v_mfma_f32_16x16x32_bf16 v[80:83],v[112:115],v[160:163],v[80:83]
v_mfma_f32_16x16x32_bf16 v[84:87],v[120:123],v[160:163],v[84:87]
v_mfma_f32_16x16x32_bf16 v[88:91],v[128:131],v[160:163],v[88:91]
v_mfma_f32_16x16x32_bf16 v[92:95],v[136:139],v[160:163],v[92:95]
)A"
R"A(s_nop 7
s_add_u32 s90,s90,1
s_cmp_lg_u32 s90,20
s_cbranch_scc1 L_sq_%=
s_nop 7
v_mul_f32 v64,s61,v64
v_mul_f32 v65,s61,v65
v_mul_f32 v66,s61,v66
v_mul_f32 v67,s61,v67
v_mul_f32 v68,s61,v68
v_mul_f32 v69,s61,v69
v_mul_f32 v70,s61,v70
v_mul_f32 v71,s61,v71
v_mul_f32 v72,s61,v72
v_mul_f32 v73,s61,v73
v_mul_f32 v74,s61,v74
v_mul_f32 v75,s61,v75
v_mul_f32 v76,s61,v76
v_mul_f32 v77,s61,v77
v_mul_f32 v78,s61,v78
v_mul_f32 v79,s61,v79
v_mul_f32 v80,s61,v80
v_mul_f32 v81,s61,v81
v_mul_f32 v82,s61,v82
v_mul_f32 v83,s61,v83
v_mul_f32 v84,s61,v84
v_mul_f32 v85,s61,v85
v_mul_f32 v86,s61,v86
v_mul_f32 v87,s61,v87
v_mul_f32 v88,s61,v88
v_mul_f32 v89,s61,v89
v_mul_f32 v90,s61,v90
v_mul_f32 v91,s61,v91
v_mul_f32 v92,s61,v92
v_mul_f32 v93,s61,v93
v_mul_f32 v94,s61,v94
v_mul_f32 v95,s61,v95
v_cvt_pk_bf16_f32 v96,v64,v65
v_cvt_pk_bf16_f32 v97,v66,v67
v_cvt_pk_bf16_f32 v98,v68,v69
v_cvt_pk_bf16_f32 v99,v70,v71
v_cvt_pk_bf16_f32 v100,v72,v73
v_cvt_pk_bf16_f32 v101,v74,v75
v_cvt_pk_bf16_f32 v102,v76,v77
v_cvt_pk_bf16_f32 v103,v78,v79
v_cvt_pk_bf16_f32 v104,v80,v81
v_cvt_pk_bf16_f32 v105,v82,v83
v_cvt_pk_bf16_f32 v106,v84,v85
v_cvt_pk_bf16_f32 v107,v86,v87
v_cvt_pk_bf16_f32 v108,v88,v89
v_cvt_pk_bf16_f32 v109,v90,v91
v_cvt_pk_bf16_f32 v110,v92,v93
v_cvt_pk_bf16_f32 v111,v94,v95
v_mov_b32 v170,0xff800000
s_lshl_b32 s91,s88,18
s_mov_b32 s90,64
L_s1_%=:
buffer_load_dwordx4 v[112:115],v164,s[20:23],s91 offen
buffer_load_dwordx4 v[116:119],v164,s[20:23],s91 offen offset:64
buffer_load_dwordx4 v[120:123],v164,s[20:23],s91 offen offset:128
buffer_load_dwordx4 v[124:127],v164,s[20:23],s91 offen offset:192
s_waitcnt vmcnt(0)
v_mfma_f32_16x16x32_bf16 v[144:147],v[112:115],v[96:99],0
v_mfma_f32_16x16x32_bf16 v[144:147],v[116:119],v[100:103],v[144:147]
v_mfma_f32_16x16x32_bf16 v[144:147],v[120:123],v[104:107],v[144:147]
v_mfma_f32_16x16x32_bf16 v[144:147],v[124:127],v[108:111],v[144:147]
s_nop 7
v_max3_f32 v170,v170,v144,v145
v_max3_f32 v170,v170,v146,v147
s_add_u32 s91,s91,4096
s_sub_u32 s90,s90,1
s_cmp_lg_u32 s90,0
s_cbranch_scc1 L_s1_%=
v_mov_b32 v171,v170
s_nop 1
v_permlane32_swap_b32 v170,v171
v_max_f32 v170,v170,v171
v_mov_b32 v171,v170
s_nop 1
v_permlane16_swap_b32 v170,v171
v_max_f32 v170,v170,v171
v_sub_f32 v152,0,v170
v_mov_b32 v156,0
v_sub_f32 v153,0,v170
v_mov_b32 v157,0
v_sub_f32 v154,0,v170
v_mov_b32 v158,0
v_sub_f32 v155,0,v170
v_mov_b32 v159,0
v_mov_b32 v64,0
v_mov_b32 v65,0
v_mov_b32 v66,0
v_mov_b32 v67,0
v_mov_b32 v68,0
v_mov_b32 v69,0
v_mov_b32 v70,0
v_mov_b32 v71,0
v_mov_b32 v72,0
v_mov_b32 v73,0
v_mov_b32 v74,0
v_mov_b32 v75,0
v_mov_b32 v76,0
v_mov_b32 v77,0
v_mov_b32 v78,0
v_mov_b32 v79,0
v_mov_b32 v80,0
v_mov_b32 v81,0
v_mov_b32 v82,0
v_mov_b32 v83,0
v_mov_b32 v84,0
v_mov_b32 v85,0
v_mov_b32 v86,0
v_mov_b32 v87,0
v_mov_b32 v88,0
v_mov_b32 v89,0
v_mov_b32 v90,0
v_mov_b32 v91,0
v_mov_b32 v92,0
v_mov_b32 v93,0
v_mov_b32 v94,0
v_mov_b32 v95,0
s_lshl_b32 s91,s88,18
s_mov_b32 s90,32
v_add_u32 v172,4096,v164
L_s2_%=:
buffer_load_dwordx4 v[112:115],v164,s[20:23],s91 offen
buffer_load_dwordx4 v[116:119],v164,s[20:23],s91 offen offset:64
buffer_load_dwordx4 v[120:123],v164,s[20:23],s91 offen offset:128
buffer_load_dwordx4 v[124:127],v164,s[20:23],s91 offen offset:192
buffer_load_dwordx4 v[128:131],v172,s[20:23],s91 offen
buffer_load_dwordx4 v[132:135],v172,s[20:23],s91 offen offset:64
buffer_load_dwordx4 v[136:139],v172,s[20:23],s91 offen offset:128
buffer_load_dwordx4 v[140:143],v172,s[20:23],s91 offen offset:192
s_waitcnt vmcnt(0)
v_mfma_f32_16x16x32_bf16 v[144:147],v[112:115],v[96:99],v[152:155]
v_mfma_f32_16x16x32_bf16 v[144:147],v[116:119],v[100:103],v[144:147]
v_mfma_f32_16x16x32_bf16 v[144:147],v[120:123],v[104:107],v[144:147]
v_mfma_f32_16x16x32_bf16 v[144:147],v[124:127],v[108:111],v[144:147]
v_mfma_f32_16x16x32_bf16 v[148:151],v[128:131],v[96:99],v[152:155]
v_mfma_f32_16x16x32_bf16 v[148:151],v[132:135],v[100:103],v[148:151]
v_mfma_f32_16x16x32_bf16 v[148:151],v[136:139],v[104:107],v[148:151]
v_mfma_f32_16x16x32_bf16 v[148:151],v[140:143],v[108:111],v[148:151]
s_nop 7
v_exp_f32 v144,v144
v_exp_f32 v145,v145
v_exp_f32 v146,v146
v_exp_f32 v147,v147
v_exp_f32 v148,v148
v_exp_f32 v149,v149
v_exp_f32 v150,v150
v_exp_f32 v151,v151
s_nop 0
v_cvt_pk_bf16_f32 v144,v144,v145
v_cvt_pk_bf16_f32 v145,v146,v147
v_cvt_pk_bf16_f32 v146,v148,v149
v_cvt_pk_bf16_f32 v147,v150,v151
buffer_load_ushort v112,v165,s[24:27],s91 offen
buffer_load_ushort v113,v165,s[24:27],s91 offen offset:256
buffer_load_ushort v114,v165,s[24:27],s91 offen offset:512
buffer_load_ushort v115,v165,s[24:27],s91 offen offset:768
buffer_load_ushort v116,v166,s[24:27],s91 offen
buffer_load_ushort v117,v166,s[24:27],s91 offen offset:256
buffer_load_ushort v118,v166,s[24:27],s91 offen offset:512
buffer_load_ushort v119,v166,s[24:27],s91 offen offset:768
buffer_load_ushort v120,v165,s[24:27],s91 offen offset:32
buffer_load_ushort v121,v165,s[24:27],s91 offen offset:288
buffer_load_ushort v122,v165,s[24:27],s91 offen offset:544
buffer_load_ushort v123,v165,s[24:27],s91 offen offset:800
buffer_load_ushort v124,v166,s[24:27],s91 offen offset:32
buffer_load_ushort v125,v166,s[24:27],s91 offen offset:288
buffer_load_ushort v126,v166,s[24:27],s91 offen offset:544
buffer_load_ushort v127,v166,s[24:27],s91 offen offset:800
buffer_load_ushort v128,v165,s[24:27],s91 offen offset:64
buffer_load_ushort v129,v165,s[24:27],s91 offen offset:320
buffer_load_ushort v130,v165,s[24:27],s91 offen offset:576
buffer_load_ushort v131,v165,s[24:27],s91 offen offset:832
buffer_load_ushort v132,v166,s[24:27],s91 offen offset:64
buffer_load_ushort v133,v166,s[24:27],s91 offen offset:320
buffer_load_ushort v134,v166,s[24:27],s91 offen offset:576
buffer_load_ushort v135,v166,s[24:27],s91 offen offset:832
buffer_load_ushort v136,v165,s[24:27],s91 offen offset:96
buffer_load_ushort v137,v165,s[24:27],s91 offen offset:352
buffer_load_ushort v138,v165,s[24:27],s91 offen offset:608
buffer_load_ushort v139,v165,s[24:27],s91 offen offset:864
buffer_load_ushort v140,v166,s[24:27],s91 offen offset:96
buffer_load_ushort v141,v166,s[24:27],s91 offen offset:352
buffer_load_ushort v142,v166,s[24:27],s91 offen offset:608
buffer_load_ushort v143,v166,s[24:27],s91 offen offset:864
s_waitcnt vmcnt(0)
v_lshl_or_b32 v112,v113,16,v112
v_lshl_or_b32 v113,v115,16,v114
v_lshl_or_b32 v114,v117,16,v116
v_lshl_or_b32 v115,v119,16,v118
v_lshl_or_b32 v120,v121,16,v120
v_lshl_or_b32 v121,v123,16,v122
v_lshl_or_b32 v122,v125,16,v124
v_lshl_or_b32 v123,v127,16,v126
v_lshl_or_b32 v128,v129,16,v128
v_lshl_or_b32 v129,v131,16,v130
v_lshl_or_b32 v130,v133,16,v132
v_lshl_or_b32 v131,v135,16,v134
v_lshl_or_b32 v136,v137,16,v136
v_lshl_or_b32 v137,v139,16,v138
v_lshl_or_b32 v138,v141,16,v140
v_lshl_or_b32 v139,v143,16,v142
s_nop 1
v_mfma_f32_16x16x32_bf16 v[64:67],v[112:115],v[144:147],v[64:67]
v_mfma_f32_16x16x32_bf16 v[68:71],v[120:123],v[144:147],v[68:71]
v_mfma_f32_16x16x32_bf16 v[72:75],v[128:131],v[144:147],v[72:75]
v_mfma_f32_16x16x32_bf16 v[76:79],v[136:139],v[144:147],v[76:79]
s_nop 7
buffer_load_ushort v112,v165,s[24:27],s91 offen offset:128
buffer_load_ushort v113,v165,s[24:27],s91 offen offset:384
buffer_load_ushort v114,v165,s[24:27],s91 offen offset:640
buffer_load_ushort v115,v165,s[24:27],s91 offen offset:896
buffer_load_ushort v116,v166,s[24:27],s91 offen offset:128
buffer_load_ushort v117,v166,s[24:27],s91 offen offset:384
buffer_load_ushort v118,v166,s[24:27],s91 offen offset:640
buffer_load_ushort v119,v166,s[24:27],s91 offen offset:896
buffer_load_ushort v120,v165,s[24:27],s91 offen offset:160
buffer_load_ushort v121,v165,s[24:27],s91 offen offset:416
buffer_load_ushort v122,v165,s[24:27],s91 offen offset:672
buffer_load_ushort v123,v165,s[24:27],s91 offen offset:928
buffer_load_ushort v124,v166,s[24:27],s91 offen offset:160
buffer_load_ushort v125,v166,s[24:27],s91 offen offset:416
buffer_load_ushort v126,v166,s[24:27],s91 offen offset:672
buffer_load_ushort v127,v166,s[24:27],s91 offen offset:928
buffer_load_ushort v128,v165,s[24:27],s91 offen offset:192
buffer_load_ushort v129,v165,s[24:27],s91 offen offset:448
buffer_load_ushort v130,v165,s[24:27],s91 offen offset:704
buffer_load_ushort v131,v165,s[24:27],s91 offen offset:960
buffer_load_ushort v132,v166,s[24:27],s91 offen offset:192
buffer_load_ushort v133,v166,s[24:27],s91 offen offset:448
buffer_load_ushort v134,v166,s[24:27],s91 offen offset:704
buffer_load_ushort v135,v166,s[24:27],s91 offen offset:960
buffer_load_ushort v136,v165,s[24:27],s91 offen offset:224
buffer_load_ushort v137,v165,s[24:27],s91 offen offset:480
buffer_load_ushort v138,v165,s[24:27],s91 offen offset:736
buffer_load_ushort v139,v165,s[24:27],s91 offen offset:992
buffer_load_ushort v140,v166,s[24:27],s91 offen offset:224
buffer_load_ushort v141,v166,s[24:27],s91 offen offset:480
buffer_load_ushort v142,v166,s[24:27],s91 offen offset:736
buffer_load_ushort v143,v166,s[24:27],s91 offen offset:992
s_waitcnt vmcnt(0)
v_lshl_or_b32 v112,v113,16,v112
v_lshl_or_b32 v113,v115,16,v114
v_lshl_or_b32 v114,v117,16,v116
v_lshl_or_b32 v115,v119,16,v118
v_lshl_or_b32 v120,v121,16,v120
v_lshl_or_b32 v121,v123,16,v122
v_lshl_or_b32 v122,v125,16,v124
v_lshl_or_b32 v123,v127,16,v126
v_lshl_or_b32 v128,v129,16,v128
v_lshl_or_b32 v129,v131,16,v130
v_lshl_or_b32 v130,v133,16,v132
v_lshl_or_b32 v131,v135,16,v134
v_lshl_or_b32 v136,v137,16,v136
v_lshl_or_b32 v137,v139,16,v138
v_lshl_or_b32 v138,v141,16,v140
v_lshl_or_b32 v139,v143,16,v142
s_nop 1
v_mfma_f32_16x16x32_bf16 v[80:83],v[112:115],v[144:147],v[80:83]
v_mfma_f32_16x16x32_bf16 v[84:87],v[120:123],v[144:147],v[84:87]
v_mfma_f32_16x16x32_bf16 v[88:91],v[128:131],v[144:147],v[88:91]
v_mfma_f32_16x16x32_bf16 v[92:95],v[136:139],v[144:147],v[92:95]
s_nop 7
v_mfma_f32_16x16x32_bf16 v[156:159],v[28:31],v[144:147],v[156:159]
s_nop 7
s_add_u32 s91,s91,8192
s_sub_u32 s90,s90,1
s_cmp_lg_u32 s90,0
s_cbranch_scc1 L_s2_%=
v_rcp_f32 v170,v156
s_nop 0
v_mul_f32 v64,v170,v64
v_mul_f32 v65,v170,v65
v_mul_f32 v66,v170,v66
v_mul_f32 v67,v170,v67
v_mul_f32 v68,v170,v68
v_mul_f32 v69,v170,v69
v_mul_f32 v70,v170,v70
v_mul_f32 v71,v170,v71
v_mul_f32 v72,v170,v72
v_mul_f32 v73,v170,v73
v_mul_f32 v74,v170,v74
v_mul_f32 v75,v170,v75
v_mul_f32 v76,v170,v76
v_mul_f32 v77,v170,v77
v_mul_f32 v78,v170,v78
v_mul_f32 v79,v170,v79
v_mul_f32 v80,v170,v80
v_mul_f32 v81,v170,v81
v_mul_f32 v82,v170,v82
v_mul_f32 v83,v170,v83
v_mul_f32 v84,v170,v84
v_mul_f32 v85,v170,v85
v_mul_f32 v86,v170,v86
v_mul_f32 v87,v170,v87
v_mul_f32 v88,v170,v88
v_mul_f32 v89,v170,v89
v_mul_f32 v90,v170,v90
v_mul_f32 v91,v170,v91
v_mul_f32 v92,v170,v92
v_mul_f32 v93,v170,v93
v_mul_f32 v94,v170,v94
v_mul_f32 v95,v170,v95
v_cvt_pk_bf16_f32 v96,v64,v65
v_cvt_pk_bf16_f32 v97,v66,v67
v_cvt_pk_bf16_f32 v98,v68,v69
v_cvt_pk_bf16_f32 v99,v70,v71
v_cvt_pk_bf16_f32 v100,v72,v73
v_cvt_pk_bf16_f32 v101,v74,v75
v_cvt_pk_bf16_f32 v102,v76,v77
v_cvt_pk_bf16_f32 v103,v78,v79
v_cvt_pk_bf16_f32 v104,v80,v81
v_cvt_pk_bf16_f32 v105,v82,v83
v_cvt_pk_bf16_f32 v106,v84,v85
v_cvt_pk_bf16_f32 v107,v86,v87
v_cvt_pk_bf16_f32 v108,v88,v89
v_cvt_pk_bf16_f32 v109,v90,v91
v_cvt_pk_bf16_f32 v110,v92,v93
v_cvt_pk_bf16_f32 v111,v94,v95
buffer_store_dwordx4 v[96:99],v169,s[36:39],s55 offen sc1
buffer_store_dwordx4 v[100:103],v169,s[36:39],s55 offen offset:1024 sc1
buffer_store_dwordx4 v[104:107],v169,s[36:39],s55 offen offset:2048 sc1
buffer_store_dwordx4 v[108:111],v169,s[36:39],s55 offen offset:3072 sc1
s_waitcnt vmcnt(0)
s_add_u32 s89,s89,1
s_cmp_lg_u32 s89,4
s_cbranch_scc1 L_sjb_%=
s_mov_b32 s98,s88
v_readlane_b32 s88,v173,0
v_readlane_b32 s89,v173,1
v_readlane_b32 s90,v173,2
v_readlane_b32 s91,v173,3
v_readlane_b32 s92,v173,4
v_readlane_b32 s93,v173,5
v_readlane_b32 s94,v173,6
v_readlane_b32 s95,v173,7
s_nop 4
s_mov_b32 s96,0
buffer_load_dwordx4 v[64:67],v6,s[56:59],s96 offen sc1
buffer_load_dwordx4 v[68:71],v6,s[56:59],s96 offen offset:1024 sc1
buffer_load_dwordx4 v[72:75],v6,s[56:59],s96 offen offset:2048 sc1
buffer_load_dwordx4 v[76:79],v6,s[56:59],s96 offen offset:3072 sc1
s_mov_b32 s96,4096
buffer_load_dwordx4 v[80:83],v6,s[56:59],s96 offen sc1
)A"
R"A(buffer_load_dwordx4 v[84:87],v6,s[56:59],s96 offen offset:1024 sc1
buffer_load_dwordx4 v[88:91],v6,s[56:59],s96 offen offset:2048 sc1
buffer_load_dwordx4 v[92:95],v6,s[56:59],s96 offen offset:3072 sc1
s_mov_b32 s96,8192
buffer_load_dwordx4 v[96:99],v6,s[56:59],s96 offen sc1
buffer_load_dwordx4 v[100:103],v6,s[56:59],s96 offen offset:1024 sc1
buffer_load_dwordx4 v[104:107],v6,s[56:59],s96 offen offset:2048 sc1
buffer_load_dwordx4 v[108:111],v6,s[56:59],s96 offen offset:3072 sc1
s_mov_b32 s96,12288
buffer_load_dwordx4 v[112:115],v6,s[56:59],s96 offen sc1
buffer_load_dwordx4 v[116:119],v6,s[56:59],s96 offen offset:1024 sc1
buffer_load_dwordx4 v[120:123],v6,s[56:59],s96 offen offset:2048 sc1
buffer_load_dwordx4 v[124:127],v6,s[56:59],s96 offen offset:3072 sc1
s_mov_b32 s96,16384
buffer_load_dwordx4 v[128:131],v6,s[56:59],s96 offen sc1
buffer_load_dwordx4 v[132:135],v6,s[56:59],s96 offen offset:1024 sc1
buffer_load_dwordx4 v[136:139],v6,s[56:59],s96 offen offset:2048 sc1
buffer_load_dwordx4 v[140:143],v6,s[56:59],s96 offen offset:3072 sc1
s_mov_b32 s96,20480
buffer_load_dwordx4 v[144:147],v6,s[56:59],s96 offen sc1
buffer_load_dwordx4 v[148:151],v6,s[56:59],s96 offen offset:1024 sc1
buffer_load_dwordx4 v[152:155],v6,s[56:59],s96 offen offset:2048 sc1
buffer_load_dwordx4 v[156:159],v6,s[56:59],s96 offen offset:3072 sc1
s_mov_b32 s96,24576
buffer_load_dwordx4 v[160:163],v6,s[56:59],s96 offen sc1
buffer_load_dwordx4 v[164:167],v6,s[56:59],s96 offen offset:1024 sc1
buffer_load_dwordx4 v[168:171],v6,s[56:59],s96 offen offset:2048 sc1
buffer_load_dwordx4 v[172:175],v6,s[56:59],s96 offen offset:3072 sc1
s_mov_b32 s96,28672
buffer_load_dwordx4 v[176:179],v6,s[56:59],s96 offen sc1
buffer_load_dwordx4 v[180:183],v6,s[56:59],s96 offen offset:1024 sc1
buffer_load_dwordx4 v[184:187],v6,s[56:59],s96 offen offset:2048 sc1
buffer_load_dwordx4 v[188:191],v6,s[56:59],s96 offen offset:3072 sc1
s_waitcnt vmcnt(0)
s_cmp_lg_u32 s98,0
s_cbranch_scc1 L_sr1_%=
buffer_load_dwordx4 v[64:67],v6,s[36:39],s84 offen sc1
buffer_load_dwordx4 v[68:71],v6,s[36:39],s84 offen offset:1024 sc1
buffer_load_dwordx4 v[72:75],v6,s[36:39],s84 offen offset:2048 sc1
buffer_load_dwordx4 v[76:79],v6,s[36:39],s84 offen offset:3072 sc1
buffer_load_dwordx4 v[80:83],v6,s[36:39],s85 offen sc1
buffer_load_dwordx4 v[84:87],v6,s[36:39],s85 offen offset:1024 sc1
buffer_load_dwordx4 v[88:91],v6,s[36:39],s85 offen offset:2048 sc1
buffer_load_dwordx4 v[92:95],v6,s[36:39],s85 offen offset:3072 sc1
buffer_load_dwordx4 v[96:99],v6,s[36:39],s86 offen sc1
buffer_load_dwordx4 v[100:103],v6,s[36:39],s86 offen offset:1024 sc1
buffer_load_dwordx4 v[104:107],v6,s[36:39],s86 offen offset:2048 sc1
buffer_load_dwordx4 v[108:111],v6,s[36:39],s86 offen offset:3072 sc1
buffer_load_dwordx4 v[112:115],v6,s[36:39],s87 offen sc1
buffer_load_dwordx4 v[116:119],v6,s[36:39],s87 offen offset:1024 sc1
buffer_load_dwordx4 v[120:123],v6,s[36:39],s87 offen offset:2048 sc1
buffer_load_dwordx4 v[124:127],v6,s[36:39],s87 offen offset:3072 sc1
s_waitcnt vmcnt(0)
s_nop 7
s_branch L_sret0_%=
L_sr1_%=:
buffer_load_dwordx4 v[132:135],v6,s[36:39],s84 offen sc1
buffer_load_dwordx4 v[140:143],v6,s[36:39],s84 offen offset:1024 sc1
buffer_load_dwordx4 v[148:151],v6,s[36:39],s84 offen offset:2048 sc1
buffer_load_dwordx4 v[156:159],v6,s[36:39],s84 offen offset:3072 sc1
buffer_load_dwordx4 v[164:167],v6,s[36:39],s85 offen sc1
buffer_load_dwordx4 v[172:175],v6,s[36:39],s85 offen offset:1024 sc1
buffer_load_dwordx4 v[180:183],v6,s[36:39],s85 offen offset:2048 sc1
buffer_load_dwordx4 v[188:191],v6,s[36:39],s85 offen offset:3072 sc1
buffer_load_dwordx4 v[196:199],v6,s[36:39],s86 offen sc1
buffer_load_dwordx4 v[204:207],v6,s[36:39],s86 offen offset:1024 sc1
buffer_load_dwordx4 v[212:215],v6,s[36:39],s86 offen offset:2048 sc1
buffer_load_dwordx4 v[220:223],v6,s[36:39],s86 offen offset:3072 sc1
buffer_load_dwordx4 v[228:231],v6,s[36:39],s87 offen sc1
buffer_load_dwordx4 v[236:239],v6,s[36:39],s87 offen offset:1024 sc1
buffer_load_dwordx4 v[244:247],v6,s[36:39],s87 offen offset:2048 sc1
buffer_load_dwordx4 v[252:255],v6,s[36:39],s87 offen offset:3072 sc1
s_waitcnt vmcnt(0)
s_nop 7
s_branch L_sret1_%=
L_end_%=:
)A"
    :: "s"(karg), "s"(blk), "s"(wid)
    : "v0", "v1", "v2", "v3", "v4", "v5", "v6", "v7", "v8", "v9", "v10", "v11", "v12", "v13", "v14", "v15", "v16", "v17", "v18", "v19", "v20", "v21", "v22", "v23", "v24", "v25", "v26", "v27", "v28", "v29", "v30", "v31", "v32", "v33", "v34", "v35", "v36", "v37", "v38", "v39", "v40", "v41", "v42", "v43", "v44", "v45", "v46", "v47", "v48", "v49", "v50", "v51", "v52", "v53", "v54", "v55", "v56", "v57", "v58", "v59", "v60", "v61", "v62", "v63", "v64", "v65", "v66", "v67", "v68", "v69", "v70", "v71", "v72", "v73", "v74", "v75", "v76", "v77", "v78", "v79", "v80", "v81", "v82", "v83", "v84", "v85", "v86", "v87", "v88", "v89", "v90", "v91", "v92", "v93", "v94", "v95", "v96", "v97", "v98", "v99", "v100", "v101", "v102", "v103", "v104", "v105", "v106", "v107", "v108", "v109", "v110", "v111", "v112", "v113", "v114", "v115", "v116", "v117", "v118", "v119", "v120", "v121", "v122", "v123", "v124", "v125", "v126", "v127", "v128", "v129", "v130", "v131", "v132", "v133", "v134", "v135", "v136", "v137", "v138", "v139", "v140", "v141", "v142", "v143", "v144", "v145", "v146", "v147", "v148", "v149", "v150", "v151", "v152", "v153", "v154", "v155", "v156", "v157", "v158", "v159", "v160", "v161", "v162", "v163", "v164", "v165", "v166", "v167", "v168", "v169", "v170", "v171", "v172", "v173", "v174", "v175", "v176", "v177", "v178", "v179", "v180", "v181", "v182", "v183", "v184", "v185", "v186", "v187", "v188", "v189", "v190", "v191", "v192", "v193", "v194", "v195", "v196", "v197", "v198", "v199", "v200", "v201", "v202", "v203", "v204", "v205", "v206", "v207", "v208", "v209", "v210", "v211", "v212", "v213", "v214", "v215", "v216", "v217", "v218", "v219", "v220", "v221", "v222", "v223", "v224", "v225", "v226", "v227", "v228", "v229", "v230", "v231", "v232", "v233", "v234", "v235", "v236", "v237", "v238", "v239", "v240", "v241", "v242", "v243", "v244", "v245", "v246", "v247", "v248", "v249", "v250", "v251", "v252", "v253", "v254", "v255", "a0", "a1", "a2", "a3", "a4", "a5", "a6", "a7", "a8", "a9", "a10", "a11", "a12", "a13", "a14", "a15", "a16", "a17", "a18", "a19", "a20", "a21", "a22", "a23", "a24", "a25", "a26", "a27", "a28", "a29", "a30", "a31", "a32", "a33", "a34", "a35", "a36", "a37", "a38", "a39", "a40", "a41", "a42", "a43", "a44", "a45", "a46", "a47", "a48", "a49", "a50", "a51", "a52", "a53", "a54", "a55", "a56", "a57", "a58", "a59", "a60", "a61", "a62", "a63", "a64", "a65", "a66", "a67", "a68", "a69", "a70", "a71", "a72", "a73", "a74", "a75", "a76", "a77", "a78", "a79", "a80", "a81", "a82", "a83", "a84", "a85", "a86", "a87", "a88", "a89", "a90", "a91", "a92", "a93", "a94", "a95", "a96", "a97", "a98", "a99", "a100", "a101", "a102", "a103", "a104", "a105", "a106", "a107", "a108", "a109", "a110", "a111", "a112", "a113", "a114", "a115", "a116", "a117", "a118", "a119", "a120", "a121", "a122", "a123", "a124", "a125", "a126", "a127", "a128", "a129", "a130", "a131", "a132", "a133", "a134", "a135", "a136", "a137", "a138", "a139", "a140", "a141", "a142", "a143", "a144", "a145", "a146", "a147", "a148", "a149", "a150", "a151", "a152", "a153", "a154", "a155", "a156", "a157", "a158", "a159", "a160", "a161", "a162", "a163", "a164", "a165", "a166", "a167", "a168", "a169", "a170", "a171", "a172", "a173", "a174", "a175", "a176", "a177", "a178", "a179", "a180", "a181", "a182", "a183", "a184", "a185", "a186", "a187", "a188", "a189", "a190", "a191", "a192", "a193", "a194", "a195", "a196", "a197", "a198", "a199", "a200", "a201", "a202", "a203", "a204", "a205", "a206", "a207", "a208", "a209", "a210", "a211", "a212", "a213", "a214", "a215", "a216", "a217", "a218", "a219", "a220", "a221", "a222", "a223", "a224", "a225", "a226", "a227", "a228", "a229", "a230", "a231", "a232", "a233", "a234", "a235", "a236", "a237", "a238", "a239", "a240", "a241", "a242", "a243", "a244", "a245", "a246", "a247", "a248", "a249", "a250", "a251", "a252", "a253", "a254", "a255", "s4", "s5", "s6", "s7", "s8", "s9", "s10", "s11", "s12", "s13", "s14", "s15", "s16", "s17", "s18", "s19", "s20", "s21", "s22", "s23", "s24", "s25", "s26", "s27", "s28", "s29", "s30", "s31", "s32", "s33", "s34", "s35", "s36", "s37", "s38", "s39", "s40", "s41", "s42", "s43", "s44", "s45", "s46", "s47", "s48", "s49", "s50", "s51", "s52", "s53", "s54", "s55", "s56", "s57", "s58", "s59", "s60", "s61", "s62", "s63", "s64", "s65", "s66", "s67", "s68", "s69", "s70", "s71", "s72", "s73", "s74", "s75", "s76", "s77", "s78", "s79", "s80", "s81", "s82", "s83", "s84", "s85", "s86", "s87", "s88", "s89", "s90", "s91", "s92", "s93", "s94", "s95", "s96", "s97", "s98", "s99", "s100", "s101", "vcc", "scc", "memory");
#endif
}

extern "C" void kernel_launch(void* const* d_in, const int* in_sizes, int n_in,
                              void* d_out, int out_size, void* d_ws, size_t ws_size,
                              hipStream_t stream) {
  (void)hipFuncSetAttribute((const void*)qattn_kernel, hipFuncAttributeMaxDynamicSharedMemorySize, LDS_ATT);
  qattn_kernel<<<256, 256, LDS_ATT, stream>>>(d_in[0], d_in[1], d_in[2], d_in[3], d_ws, d_in[4], d_out);
}
```

```cpp
#include <hip/hip_runtime.h>
#include <hip/hip_bf16.h>

constexpr int LDS_ATT = 163840;

__global__ __launch_bounds__(256, 1) void qattn_kernel(const void* __restrict__ x, const void* __restrict__ kc, const void* __restrict__ vc,
                                                       const void* __restrict__ wq, void* __restrict__ ws, const void* __restrict__ wo,
                                                       void* __restrict__ out) {
#if defined(__HIP_DEVICE_COMPILE__)
  const auto karg = __builtin_amdgcn_kernarg_segment_ptr();
  const unsigned blk = blockIdx.x;
  const unsigned wid = __builtin_amdgcn_readfirstlane(threadIdx.x >> 6);
  asm volatile(
R"A(s_memtime s[72:73]
s_memrealtime s[74:75]
s_load_dwordx8 s[4:11],%0,0x0
s_load_dwordx4 s[12:15],%0,0x20
s_load_dwordx2 s[16:17],%0,0x30
s_mov_b32 s40,%2
s_mov_b32 s43,%1
s_lshr_b32 s45,s43,3
v_mbcnt_lo_u32_b32 v12,-1,0
v_mbcnt_hi_u32_b32 v12,-1,v12
s_getpc_b64 s[88:89]
L_pc_%=:
s_and_b32 s88,s88,0xffffff80
s_and_b32 s89,s89,0xffff
s_mov_b32 s90,L_fast_end_%=-L_pc_%=
s_mov_b32 s91,0x20000
s_lshl_b32 s44,s45,2
s_add_u32 s44,s44,s40
s_mul_i32 s44,s44,768
v_lshlrev_b32 v67,7,v12
s_mov_b64 exec,63
buffer_load_dword v19,v67,s[88:91],s44 offen
s_mov_b64 exec,-1
s_and_b32 s70,s43,3
s_and_b32 s71,s45,3
s_lshl_b32 s42,s70,2
s_add_u32 s42,s42,s71
s_and_b32 s41,s43,4
s_lshl_b32 s41,s41,1
s_lshr_b32 s46,s45,2
s_add_u32 s41,s41,s46
s_lshl_b32 s56,s42,9
s_mov_b32 s57,0
s_lshl_b32 s59,s40,13
s_add_u32 s58,s59,65536
v_and_b32 v64,3,v12
v_bfe_u32 v65,v12,2,2
v_lshrrev_b32 v66,4,v12
v_lshrrev_b32 v75,3,v12
s_lshl_b32 s44,s40,6
v_add_u32 v75,s44,v75
v_mul_u32_u24 v75,1280,v75
v_and_b32 v76,7,v12
s_add_u32 s84,s70,1
s_and_b32 s84,s84,3
s_mul_i32 s84,s84,20480
s_add_u32 s84,s84,4096
s_add_u32 s85,s70,2
s_and_b32 s85,s85,3
s_mul_i32 s85,s85,20480
s_add_u32 s85,s85,2048
s_add_u32 s86,s70,3
s_and_b32 s86,s86,3
s_mul_i32 s86,s86,20480
s_add_u32 s86,s86,4096
s_add_u32 s87,s70,4
s_and_b32 s87,s87,3
s_mul_i32 s87,s87,20480
s_add_u32 s87,s87,2048
v_or_b32 v77,0,v66
v_xor_b32 v77,v77,v76
v_lshl_add_u32 v77,v77,4,v75
v_add_u32 v48,s84,v77
v_add_u32 v50,s85,v77
v_add_u32 v52,s86,v77
v_add_u32 v54,s87,v77
v_or_b32 v77,4,v66
v_xor_b32 v77,v77,v76
v_lshl_add_u32 v77,v77,4,v75
v_add_u32 v77,9216,v77
v_add_u32 v49,s84,v77
v_add_u32 v51,s85,v77
v_add_u32 v53,s86,v77
v_add_u32 v55,s87,v77
s_lshl_b32 s44,s40,17
v_lshlrev_b32 v75,13,v65
v_lshl_add_u32 v75,v66,6,v75
v_lshl_add_u32 v75,v64,4,v75
v_add_u32 v75,s44,v75
v_add_u32 v56,0,v75
v_add_u32 v57,256,v75
v_add_u32 v58,32768,v75
v_add_u32 v59,33024,v75
v_add_u32 v60,65536,v75
v_add_u32 v61,65792,v75
v_add_u32 v62,98304,v75
v_add_u32 v63,98560,v75
s_mul_i32 s44,s41,307200
s_waitcnt lgkmcnt(0)
s_mov_b64 s[28:29],s[4:5]
s_add_u32 s28,s28,s44
s_addc_u32 s29,s29,0
s_sub_u32 s28,s28,4096
s_subb_u32 s29,s29,0
s_and_b32 s29,s29,0xffff
s_mov_b32 s30,311296
s_mov_b32 s31,0x20000
s_mov_b64 s[32:33],s[10:11]
s_and_b32 s33,s33,0xffff
s_mov_b32 s34,5242880
s_mov_b32 s35,0x20000
s_add_u32 m0,s58,1024
s_nop 0
buffer_load_dwordx4 v57,s[32:35],s56 offen lds
s_add_u32 m0,s58,3072
s_nop 0
buffer_load_dwordx4 v59,s[32:35],s56 offen lds
s_add_u32 m0,s58,5120
s_nop 0
buffer_load_dwordx4 v61,s[32:35],s56 offen lds
s_add_u32 m0,s58,7168
s_nop 0
buffer_load_dwordx4 v63,s[32:35],s56 offen lds
s_add_u32 m0,s59,0
s_nop 0
buffer_load_dwordx4 v48,s[28:31],s57 offen lds
buffer_load_dwordx4 v49,s[28:31],s57 offen offset:1024 lds
buffer_load_dwordx4 v50,s[28:31],s57 offen offset:2048 lds
buffer_load_dwordx4 v51,s[28:31],s57 offen offset:3072 lds
s_add_u32 m0,s59,4096
s_nop 0
buffer_load_dwordx4 v52,s[28:31],s57 offen lds
buffer_load_dwordx4 v53,s[28:31],s57 offen offset:1024 lds
buffer_load_dwordx4 v54,s[28:31],s57 offen offset:2048 lds
buffer_load_dwordx4 v55,s[28:31],s57 offen offset:3072 lds
s_add_u32 s57,s57,128
s_xor_b32 s59,s59,131072
s_add_u32 m0,s58,0
s_nop 0
buffer_load_dwordx4 v56,s[32:35],s56 offen lds
s_add_u32 m0,s58,2048
s_nop 0
buffer_load_dwordx4 v58,s[32:35],s56 offen lds
s_add_u32 m0,s58,4096
s_nop 0
buffer_load_dwordx4 v60,s[32:35],s56 offen lds
s_add_u32 m0,s58,6144
s_nop 0
buffer_load_dwordx4 v62,s[32:35],s56 offen lds
s_add_u32 s56,s56,524288
s_add_u32 s58,s58,32768
s_cmp_ge_u32 s58,131072
s_cselect_b32 s62,98304,0
s_sub_u32 s58,s58,s62
s_add_u32 m0,s58,0
s_nop 0
buffer_load_dwordx4 v56,s[32:35],s56 offen lds
s_add_u32 m0,s58,1024
s_nop 0
buffer_load_dwordx4 v57,s[32:35],s56 offen lds
s_add_u32 m0,s58,2048
s_nop 0
buffer_load_dwordx4 v58,s[32:35],s56 offen lds
s_add_u32 m0,s58,3072
s_nop 0
buffer_load_dwordx4 v59,s[32:35],s56 offen lds
s_add_u32 m0,s58,4096
s_nop 0
buffer_load_dwordx4 v60,s[32:35],s56 offen lds
s_add_u32 m0,s58,5120
s_nop 0
buffer_load_dwordx4 v61,s[32:35],s56 offen lds
s_add_u32 m0,s58,6144
s_nop 0
buffer_load_dwordx4 v62,s[32:35],s56 offen lds
s_add_u32 m0,s58,7168
s_nop 0
buffer_load_dwordx4 v63,s[32:35],s56 offen lds
s_add_u32 s56,s56,524288
s_add_u32 s58,s58,32768
s_cmp_ge_u32 s58,131072
s_cselect_b32 s62,98304,0
s_sub_u32 s58,s58,s62
s_mov_b32 s60,65536
v_lshlrev_b32 v6,4,v12
v_lshlrev_b32 v72,12,v66
v_lshl_add_u32 v72,v65,6,v72
v_lshl_add_u32 v23,v64,4,v72
v_add_u32 v22,65536,v23
v_and_b32 v74,15,v12
v_lshrrev_b32 v76,1,v74
v_lshlrev_b32 v75,7,v74
s_lshl_b32 s44,s40,13
v_add_u32 v75,s44,v75
v_or_b32 v77,0,v66
v_xor_b32 v77,v77,v76
v_lshl_add_u32 v20,v77,4,v75
v_or_b32 v77,4,v66
v_xor_b32 v77,v77,v76
v_lshl_add_u32 v21,v77,4,v75
s_waitcnt vmcnt(12)
s_nop 0
s_memtime s[64:65]
s_memrealtime s[66:67]
s_bfe_u32 s68,%1,327683
s_waitcnt lgkmcnt(0)
s_sub_u32 s64,s64,s72
s_sub_u32 s66,s66,s74
s_mul_i32 s64,s64,10
s_mul_i32 s66,s66,215
s_cmp_lt_u32 s64,s66
s_cbranch_scc1 L_se1_%=
L_sg1_%=:
s_cmp_eq_u32 s68,0
s_cbranch_scc1 L_se1_%=
s_sleep 4
s_sub_u32 s68,s68,1
s_branch L_sg1_%=
L_se1_%=:
s_barrier
ds_read_b128 a[192:195],v20
ds_read_b64_tr_b16 a[128:129],v22 offset:1024
ds_read_b64_tr_b16 a[130:131],v22 offset:3072
ds_read_b128 a[196:199],v21
ds_read_b64_tr_b16 a[132:133],v22 offset:17408
ds_read_b64_tr_b16 a[134:135],v22 offset:19456
ds_read_b128 a[204:207],v21 offset:2048
ds_read_b128 a[200:203],v20 offset:2048
ds_read_b128 a[208:211],v20 offset:4096
ds_read_b128 a[212:215],v21 offset:4096
ds_read_b128 a[220:223],v21 offset:6144
ds_read_b128 a[216:219],v20 offset:6144
ds_read_b64_tr_b16 a[136:137],v22 offset:1032
ds_read_b64_tr_b16 a[138:139],v22 offset:3080
s_waitcnt lgkmcnt(11)
v_mfma_f32_16x16x32_bf16 v[128:131],a[128:131],a[192:195],0
s_add_u32 m0,s59,0
ds_read_b64_tr_b16 a[140:141],v22 offset:17416
ds_read_b64_tr_b16 a[142:143],v22 offset:19464
s_waitcnt lgkmcnt(10)
v_mfma_f32_16x16x32_bf16 v[128:131],a[132:135],a[196:199],v[128:131]
s_waitcnt lgkmcnt(9)
v_mfma_f32_16x16x32_bf16 v[136:139],a[132:135],a[204:207],0
s_waitcnt lgkmcnt(8)
v_mfma_f32_16x16x32_bf16 v[136:139],a[128:131],a[200:203],v[136:139]
s_waitcnt lgkmcnt(7)
v_mfma_f32_16x16x32_bf16 v[144:147],a[128:131],a[208:211],0
s_waitcnt lgkmcnt(6)
buffer_load_dwordx4 v48,s[28:31],s57 offen lds
v_mfma_f32_16x16x32_bf16 v[144:147],a[132:135],a[212:215],v[144:147]
s_waitcnt lgkmcnt(5)
v_mfma_f32_16x16x32_bf16 v[152:155],a[132:135],a[220:223],0
s_waitcnt lgkmcnt(4)
v_mfma_f32_16x16x32_bf16 v[152:155],a[128:131],a[216:219],v[152:155]
s_waitcnt lgkmcnt(2)
ds_read_b64_tr_b16 a[144:145],v22 offset:1280
ds_read_b64_tr_b16 a[146:147],v22 offset:3328
v_mfma_f32_16x16x32_bf16 v[132:135],a[136:139],a[192:195],0
s_waitcnt lgkmcnt(2)
ds_read_b64_tr_b16 a[148:149],v22 offset:17664
ds_read_b64_tr_b16 a[150:151],v22 offset:19712
buffer_load_dwordx4 v49,s[28:31],s57 offen offset:1024 lds
v_mfma_f32_16x16x32_bf16 v[132:135],a[140:143],a[196:199],v[132:135]
v_mfma_f32_16x16x32_bf16 v[140:143],a[140:143],a[204:207],0
v_mfma_f32_16x16x32_bf16 v[140:143],a[136:139],a[200:203],v[140:143]
v_mfma_f32_16x16x32_bf16 v[148:151],a[136:139],a[208:211],0
buffer_load_dwordx4 v50,s[28:31],s57 offen offset:2048 lds
v_mfma_f32_16x16x32_bf16 v[148:151],a[140:143],a[212:215],v[148:151]
v_mfma_f32_16x16x32_bf16 v[156:159],a[140:143],a[220:223],0
v_mfma_f32_16x16x32_bf16 v[156:159],a[136:139],a[216:219],v[156:159]
s_waitcnt lgkmcnt(2)
ds_read_b64_tr_b16 a[152:153],v22 offset:1288
ds_read_b64_tr_b16 a[154:155],v22 offset:3336
v_mfma_f32_16x16x32_bf16 v[160:163],a[144:147],a[192:195],0
s_waitcnt lgkmcnt(2)
buffer_load_dwordx4 v51,s[28:31],s57 offen offset:3072 lds
ds_read_b64_tr_b16 a[156:157],v22 offset:17672
ds_read_b64_tr_b16 a[158:159],v22 offset:19720
v_mfma_f32_16x16x32_bf16 v[160:163],a[148:151],a[196:199],v[160:163]
s_add_u32 m0,s59,4096
v_mfma_f32_16x16x32_bf16 v[168:171],a[148:151],a[204:207],0
v_mfma_f32_16x16x32_bf16 v[168:171],a[144:147],a[200:203],v[168:171]
v_mfma_f32_16x16x32_bf16 v[176:179],a[144:147],a[208:211],0
buffer_load_dwordx4 v52,s[28:31],s57 offen lds
v_mfma_f32_16x16x32_bf16 v[176:179],a[148:151],a[212:215],v[176:179]
v_mfma_f32_16x16x32_bf16 v[184:187],a[148:151],a[220:223],0
v_mfma_f32_16x16x32_bf16 v[184:187],a[144:147],a[216:219],v[184:187]
s_waitcnt lgkmcnt(2)
v_mfma_f32_16x16x32_bf16 v[164:167],a[152:155],a[192:195],0
s_waitcnt lgkmcnt(0)
buffer_load_dwordx4 v53,s[28:31],s57 offen offset:1024 lds
ds_read_b64_tr_b16 a[160:161],v22 offset:1536
ds_read_b64_tr_b16 a[162:163],v22 offset:3584
v_mfma_f32_16x16x32_bf16 v[164:167],a[156:159],a[196:199],v[164:167]
ds_read_b64_tr_b16 a[164:165],v22 offset:17920
ds_read_b64_tr_b16 a[166:167],v22 offset:19968
v_mfma_f32_16x16x32_bf16 v[172:175],a[156:159],a[204:207],0
v_mfma_f32_16x16x32_bf16 v[172:175],a[152:155],a[200:203],v[172:175]
v_mfma_f32_16x16x32_bf16 v[180:183],a[152:155],a[208:211],0
buffer_load_dwordx4 v54,s[28:31],s57 offen offset:2048 lds
v_mfma_f32_16x16x32_bf16 v[180:183],a[156:159],a[212:215],v[180:183]
v_mfma_f32_16x16x32_bf16 v[188:191],a[156:159],a[220:223],0
s_waitcnt vmcnt(15)
v_mfma_f32_16x16x32_bf16 v[188:191],a[152:155],a[216:219],v[188:191]
s_barrier
buffer_load_dwordx4 v55,s[28:31],s57 offen offset:3072 lds
ds_read_b64_tr_b16 a[168:169],v22 offset:1544
ds_read_b64_tr_b16 a[170:171],v22 offset:3592
s_waitcnt lgkmcnt(4)
v_mfma_f32_16x16x32_bf16 v[192:195],a[160:163],a[192:195],0
s_add_u32 m0,s58,0
ds_read_b64_tr_b16 a[172:173],v22 offset:17928
ds_read_b64_tr_b16 a[174:175],v22 offset:19976
s_waitcnt lgkmcnt(4)
v_mfma_f32_16x16x32_bf16 v[192:195],a[164:167],a[196:199],v[192:195]
v_mfma_f32_16x16x32_bf16 v[200:203],a[164:167],a[204:207],0
s_lshl_b32 s44,s41,13
s_add_u32 s44,s44,104857600
s_mov_b64 s[64:65],s[12:13]
v_mfma_f32_16x16x32_bf16 v[200:203],a[160:163],a[200:203],v[200:203]
s_mov_b32 s66,8192
buffer_load_dwordx4 v56,s[32:35],s56 offen lds
v_mfma_f32_16x16x32_bf16 v[208:211],a[160:163],a[208:211],0
s_add_u32 m0,s58,1024
s_add_u32 s64,s64,s44
s_addc_u32 s65,s65,0
v_mfma_f32_16x16x32_bf16 v[208:211],a[164:167],a[212:215],v[208:211]
s_and_b32 s65,s65,0xffff
s_mov_b32 s67,0x20000
v_mov_b32 v14,4096
v_mfma_f32_16x16x32_bf16 v[216:219],a[164:167],a[220:223],0
s_mov_b64 s[72:73],s[14:15]
s_and_b32 s73,s73,0xffff
ds_read_b64_tr_b16 a[176:177],v22 offset:1792
v_mfma_f32_16x16x32_bf16 v[216:219],a[160:163],a[216:219],v[216:219]
s_mov_b32 s74,5242880
s_mov_b32 s75,0x20000
ds_read_b64_tr_b16 a[178:179],v22 offset:3840
s_waitcnt lgkmcnt(4)
v_mfma_f32_16x16x32_bf16 v[196:199],a[168:171],a[192:195],0
s_waitcnt lgkmcnt(2)
buffer_load_dwordx4 v57,s[32:35],s56 offen lds
ds_read_b64_tr_b16 a[180:181],v22 offset:18176
ds_read_b64_tr_b16 a[182:183],v22 offset:20224
v_mfma_f32_16x16x32_bf16 v[196:199],a[172:175],a[196:199],v[196:199]
s_add_u32 m0,s58,2048
v_lshlrev_b32 v15,3,v6
v_add_u32 v16,128,v15
v_mfma_f32_16x16x32_bf16 v[204:207],a[172:175],a[204:207],0
s_lshl_b32 s83,%1,2
ds_read_b64_tr_b16 a[184:185],v22 offset:1800
v_lshrrev_b32 v16,10,v16
v_mfma_f32_16x16x32_bf16 v[204:207],a[168:171],a[200:203],v[204:207]
s_add_u32 s83,s83,s40
ds_read_b64_tr_b16 a[186:187],v22 offset:3848
v_lshl_add_u32 v15,v16,24,v15
v_mfma_f32_16x16x32_bf16 v[212:215],a[168:171],a[208:211],0
s_mul_i32 s83,s83,5120
buffer_load_dwordx4 v58,s[32:35],s56 offen lds
v_mfma_f32_16x16x32_bf16 v[212:215],a[172:175],a[212:215],v[212:215]
s_add_u32 m0,s58,3072
s_lshl_b32 s43,s41,5
ds_read_b64_tr_b16 a[188:189],v22 offset:18184
v_mfma_f32_16x16x32_bf16 v[220:223],a[172:175],a[220:223],0
s_lshl_b32 s44,s42,1
s_add_u32 s43,s43,s44
ds_read_b64_tr_b16 a[190:191],v22 offset:20232
v_mfma_f32_16x16x32_bf16 v[220:223],a[168:171],a[216:219],v[220:223]
s_lshl_b32 s44,s43,18
s_mov_b64 s[20:21],s[6:7]
ds_read_b64_tr_b16 a[128:129],v22
s_waitcnt lgkmcnt(7)
v_mfma_f32_16x16x32_bf16 v[224:227],a[176:179],a[192:195],0
s_mov_b32 s22,524288
buffer_load_dwordx4 v59,s[32:35],s56 offen lds
s_waitcnt lgkmcnt(5)
v_mfma_f32_16x16x32_bf16 v[224:227],a[180:183],a[196:199],v[224:227]
s_add_u32 m0,s58,4096
s_mov_b32 s23,0x20000
)A"
R"A(ds_read_b64_tr_b16 a[130:131],v22 offset:2048
v_mfma_f32_16x16x32_bf16 v[232:235],a[180:183],a[204:207],0
s_add_u32 s20,s20,s44
s_addc_u32 s21,s21,0
ds_read_b64_tr_b16 a[132:133],v22 offset:16384
v_mfma_f32_16x16x32_bf16 v[232:235],a[176:179],a[200:203],v[232:235]
s_and_b32 s21,s21,0xffff
s_mov_b64 s[24:25],s[8:9]
ds_read_b64_tr_b16 a[134:135],v22 offset:18432
v_mfma_f32_16x16x32_bf16 v[240:243],a[176:179],a[208:211],0
s_mov_b32 s26,524288
buffer_load_dwordx4 v60,s[32:35],s56 offen lds
v_mfma_f32_16x16x32_bf16 v[240:243],a[180:183],a[212:215],v[240:243]
s_add_u32 s24,s24,s44
s_addc_u32 s25,s25,0
ds_read_b64_tr_b16 a[136:137],v22 offset:8
v_mfma_f32_16x16x32_bf16 v[248:251],a[180:183],a[220:223],0
s_add_u32 m0,s58,5120
s_and_b32 s25,s25,0xffff
ds_read_b64_tr_b16 a[138:139],v22 offset:2056
v_mfma_f32_16x16x32_bf16 v[248:251],a[176:179],a[216:219],v[248:251]
s_mov_b32 s27,0x20000
s_lshl_b32 s44,s43,16
ds_read_b64_tr_b16 a[140:141],v22 offset:16392
s_waitcnt lgkmcnt(6)
v_mfma_f32_16x16x32_bf16 v[228:231],a[184:187],a[192:195],0
s_xor_b32 s59,s59,131072
s_mov_b64 s[36:37],s[12:13]
ds_read_b64_tr_b16 a[142:143],v22 offset:18440
v_mfma_f32_16x16x32_bf16 v[228:231],a[188:191],a[196:199],v[228:231]
s_add_u32 s57,s57,128
buffer_load_dwordx4 v61,s[32:35],s56 offen lds
v_mfma_f32_16x16x32_bf16 v[236:239],a[188:191],a[204:207],0
s_add_u32 s36,s36,s44
s_addc_u32 s37,s37,0
ds_read_b64_tr_b16 a[144:145],v22 offset:256
v_mfma_f32_16x16x32_bf16 v[236:239],a[184:187],a[200:203],v[236:239]
s_add_u32 m0,s58,6144
s_and_b32 s37,s37,0xffff
ds_read_b64_tr_b16 a[146:147],v22 offset:2304
v_mfma_f32_16x16x32_bf16 v[244:247],a[184:187],a[208:211],0
s_mov_b32 s38,131072
s_mov_b32 s39,0x20000
ds_read_b64_tr_b16 a[148:149],v22 offset:16640
v_mfma_f32_16x16x32_bf16 v[244:247],a[188:191],a[212:215],v[244:247]
s_lshl_b32 s51,s40,12
s_add_u32 s52,s51,16384
ds_read_b64_tr_b16 a[150:151],v22 offset:18688
v_mfma_f32_16x16x32_bf16 v[252:255],a[188:191],a[220:223],0
s_mul_i32 s47,s41,5
buffer_load_dwordx4 v62,s[32:35],s56 offen lds
v_mfma_f32_16x16x32_bf16 v[252:255],a[184:187],a[216:219],v[252:255]
s_add_u32 s47,s47,s42
s_and_b32 s47,s47,15
ds_read_b64_tr_b16 a[152:153],v22 offset:264
s_waitcnt lgkmcnt(6)
v_mfma_f32_16x16x32_bf16 a[0:3],a[128:131],a[192:195],0
s_add_u32 m0,s58,7168
s_lshl_b32 s47,s47,14
ds_read_b64_tr_b16 a[154:155],v22 offset:2312
v_mfma_f32_16x16x32_bf16 a[0:3],a[132:135],a[196:199],a[0:3]
s_mov_b32 s54,0
s_lshl_b32 s55,s40,14
ds_read_b64_tr_b16 a[156:157],v22 offset:16648
v_mfma_f32_16x16x32_bf16 a[32:35],a[132:135],a[204:207],0
s_mov_b32 s61,0x3e0293ee
s_mov_b32 s63,0x5d800000
ds_read_b64_tr_b16 a[158:159],v22 offset:18696
v_mfma_f32_16x16x32_bf16 a[32:35],a[128:131],a[200:203],a[32:35]
s_lshl_b32 s44,s40,2
buffer_load_dwordx4 v63,s[32:35],s56 offen lds
v_mfma_f32_16x16x32_bf16 a[64:67],a[128:131],a[208:211],0
s_add_u32 s44,s44,s70
s_mov_b32 s46,0xff800000
ds_read_b64_tr_b16 a[160:161],v22 offset:512
v_mfma_f32_16x16x32_bf16 a[64:67],a[132:135],a[212:215],a[64:67]
s_add_u32 m0,s59,0
ds_read_b64_tr_b16 a[162:163],v22 offset:2560
v_lshlrev_b32 v70,8,v65
v_mfma_f32_16x16x32_bf16 a[96:99],a[132:135],a[220:223],0
s_cmp_eq_u32 s44,14
s_cselect_b32 s18,0x7149f2ca,s46
ds_read_b64_tr_b16 a[164:165],v22 offset:16896
v_mfma_f32_16x16x32_bf16 a[96:99],a[128:131],a[216:219],a[96:99]
s_cmp_eq_u32 s44,13
s_cselect_b32 s19,0x7149f2ca,s46
ds_read_b64_tr_b16 a[166:167],v22 offset:18944
v_mfma_f32_16x16x32_bf16 a[4:7],a[136:139],a[192:195],0
s_add_u32 s60,s60,32768
buffer_load_dwordx4 v48,s[28:31],s57 offen lds
s_waitcnt lgkmcnt(6)
v_mfma_f32_16x16x32_bf16 a[4:7],a[140:143],a[196:199],a[4:7]
s_cmp_eq_u32 s44,12
s_cselect_b32 s100,0x7149f2ca,s46
ds_read_b64_tr_b16 a[168:169],v22 offset:520
v_mfma_f32_16x16x32_bf16 a[36:39],a[140:143],a[204:207],0
s_cmp_eq_u32 s44,15
s_cselect_b32 s101,0x7149f2ca,s46
ds_read_b64_tr_b16 a[170:171],v22 offset:2568
v_mfma_f32_16x16x32_bf16 a[36:39],a[136:139],a[200:203],a[36:39]
s_add_u32 s58,s58,32768
ds_read_b64_tr_b16 a[172:173],v22 offset:16904
v_lshl_add_u32 v70,v66,6,v70
v_mfma_f32_16x16x32_bf16 a[68:71],a[136:139],a[208:211],0
s_mul_i32 s81,s70,1310720
ds_read_b64_tr_b16 a[174:175],v22 offset:18952
v_add_u32 v70,s51,v70
v_mfma_f32_16x16x32_bf16 a[68:71],a[140:143],a[212:215],a[68:71]
s_mul_i32 s44,s71,320
buffer_load_dwordx4 v49,s[28:31],s57 offen offset:1024 lds
v_mfma_f32_16x16x32_bf16 a[100:103],a[140:143],a[220:223],0
s_add_u32 s81,s81,s44
ds_read_b64_tr_b16 a[176:177],v22 offset:768
v_lshl_add_u32 v4,v64,4,v70
v_mfma_f32_16x16x32_bf16 a[100:103],a[136:139],a[216:219],a[100:103]
s_mul_i32 s82,s40,10240
ds_read_b64_tr_b16 a[178:179],v22 offset:2816
v_xor_b32 v71,2,v64
v_mfma_f32_16x16x32_bf16 a[8:11],a[144:147],a[192:195],0
s_add_u32 s56,s56,524288
ds_read_b64_tr_b16 a[180:181],v22 offset:17152
v_lshl_add_u32 v5,v71,4,v70
v_mfma_f32_16x16x32_bf16 a[8:11],a[148:151],a[196:199],a[8:11]
s_mul_i32 s44,s40,40960
ds_read_b64_tr_b16 a[182:183],v22 offset:19200
v_lshlrev_b32 v72,10,v65
v_mfma_f32_16x16x32_bf16 a[40:43],a[148:151],a[204:207],0
s_lshr_b32 s92,%1,3
buffer_load_dwordx4 v50,s[28:31],s57 offen offset:2048 lds
v_mfma_f32_16x16x32_bf16 a[40:43],a[144:147],a[200:203],a[40:43]
s_lshl_b32 s92,s92,2
ds_read_b64_tr_b16 a[184:185],v22 offset:776
v_lshl_add_u32 v72,v64,6,v72
v_mfma_f32_16x16x32_bf16 a[72:75],a[144:147],a[208:211],0
s_add_u32 s92,s92,s40
ds_read_b64_tr_b16 a[186:187],v22 offset:2824
v_and_b32 v73,2,v65
v_mfma_f32_16x16x32_bf16 a[72:75],a[148:151],a[212:215],a[72:75]
s_lshl_b32 s92,s92,7
ds_read_b64_tr_b16 a[188:189],v22 offset:17160
v_xor_b32 v73,v73,v66
v_mfma_f32_16x16x32_bf16 a[104:107],a[148:151],a[220:223],0
s_add_u32 s93,s92,L_tailseg_%=-L_pc_%=
ds_read_b64_tr_b16 a[190:191],v22 offset:19208
v_lshl_add_u32 v72,v73,4,v72
v_mfma_f32_16x16x32_bf16 a[104:107],a[144:147],a[216:219],a[104:107]
buffer_load_dwordx4 v51,s[28:31],s57 offen offset:3072 lds
v_mfma_f32_16x16x32_bf16 a[12:15],a[152:155],a[192:195],0
s_waitcnt lgkmcnt(5)
v_add_u32 v1,98304,v72
v_lshlrev_b32 v72,10,v66
v_mfma_f32_16x16x32_bf16 a[12:15],a[156:159],a[196:199],a[12:15]
s_add_u32 m0,s59,4096
v_lshl_add_u32 v72,v65,6,v72
v_lshl_add_u32 v72,v64,3,v72
v_mfma_f32_16x16x32_bf16 a[44:47],a[156:159],a[204:207],0
s_xor_b32 s59,s59,131072
v_add_u32 v72,114688,v72
v_and_b32 v73,1,v66
v_mfma_f32_16x16x32_bf16 a[44:47],a[152:155],a[200:203],a[44:47]
s_cmp_eq_u32 s60,131072
s_cselect_b32 s60,32768,s60
v_lshl_add_u32 v2,v73,5,v72
v_mfma_f32_16x16x32_bf16 a[76:79],a[152:155],a[208:211],0
buffer_load_dwordx4 v52,s[28:31],s57 offen lds
v_mfma_f32_16x16x32_bf16 a[76:79],a[156:159],a[212:215],a[76:79]
v_xor_b32 v73,1,v73
v_add_u32 v22,s60,v23
v_mfma_f32_16x16x32_bf16 a[108:111],a[156:159],a[220:223],0
s_add_u32 s60,s60,32768
v_xor_b32 v20,131072,v20
v_xor_b32 v21,131072,v21
v_mfma_f32_16x16x32_bf16 a[108:111],a[152:155],a[216:219],a[108:111]
s_waitcnt vmcnt(13) lgkmcnt(0)
v_lshl_add_u32 v3,v73,5,v72
v_mov_b32 v73,0x3f803f80
v_mfma_f32_16x16x32_bf16 a[16:19],a[160:163],a[192:195],0
s_barrier
buffer_load_dwordx4 v53,s[28:31],s57 offen offset:1024 lds
v_mfma_f32_16x16x32_bf16 a[16:19],a[164:167],a[196:199],a[16:19]
s_cmp_ge_u32 s58,131072
s_cselect_b32 s62,98304,0
ds_read_b128 a[224:227],v20
v_mfma_f32_16x16x32_bf16 a[48:51],a[164:167],a[204:207],0
s_sub_u32 s58,s58,s62
ds_read_b64_tr_b16 a[128:129],v22 offset:1024
v_add_u32 v70,0,v12
v_mfma_f32_16x16x32_bf16 a[48:51],a[160:163],a[200:203],a[48:51]
ds_read_b64_tr_b16 a[130:131],v22 offset:3072
v_mul_u32_u24 v71,3277,v70
v_mfma_f32_16x16x32_bf16 a[80:83],a[160:163],a[208:211],0
ds_read_b128 a[228:231],v21
v_lshrrev_b32 v71,16,v71
v_mfma_f32_16x16x32_bf16 a[80:83],a[164:167],a[212:215],a[80:83]
buffer_load_dwordx4 v54,s[28:31],s57 offen offset:2048 lds
v_mfma_f32_16x16x32_bf16 a[112:115],a[164:167],a[220:223],0
ds_read_b64_tr_b16 a[132:133],v22 offset:17408
v_mul_u32_u24 v72,20,v71
v_mfma_f32_16x16x32_bf16 a[112:115],a[160:163],a[216:219],a[112:115]
ds_read_b64_tr_b16 a[134:135],v22 offset:19456
v_sub_u32 v72,v70,v72
v_mfma_f32_16x16x32_bf16 a[20:23],a[168:171],a[192:195],0
ds_read_b128 a[232:235],v20 offset:2048
v_add_u32 v70,64,v12
v_mfma_f32_16x16x32_bf16 a[20:23],a[172:175],a[196:199],a[20:23]
ds_read_b128 a[236:239],v21 offset:2048
v_lshlrev_b32 v24,10,v6
v_mfma_f32_16x16x32_bf16 a[52:55],a[172:175],a[204:207],0
buffer_load_dwordx4 v55,s[28:31],s57 offen offset:3072 lds
v_mfma_f32_16x16x32_bf16 a[52:55],a[168:171],a[200:203],a[52:55]
s_add_u32 s57,s57,128
ds_read_b128 a[240:243],v20 offset:4096
v_mfma_f32_16x16x32_bf16 a[84:87],a[168:171],a[208:211],0
s_cmp_eq_u32 s60,131072
s_cselect_b32 s60,32768,s60
ds_read_b128 a[244:247],v21 offset:4096
v_mfma_f32_16x16x32_bf16 a[84:87],a[172:175],a[212:215],a[84:87]
ds_read_b128 a[248:251],v20 offset:6144
v_xor_b32 v20,131072,v20
v_mfma_f32_16x16x32_bf16 a[116:119],a[172:175],a[220:223],0
buffer_load_dword v13,v14,s[64:67],0 offen sc1
v_mfma_f32_16x16x32_bf16 a[116:119],a[168:171],a[216:219],a[116:119]
ds_read_b128 a[252:255],v21 offset:6144
v_xor_b32 v21,131072,v21
v_mfma_f32_16x16x32_bf16 a[24:27],a[176:179],a[192:195],0
ds_read_b64_tr_b16 a[136:137],v22 offset:1032
v_mfma_f32_16x16x32_bf16 a[24:27],a[180:183],a[196:199],a[24:27]
ds_read_b64_tr_b16 a[138:139],v22 offset:3080
v_mfma_f32_16x16x32_bf16 a[56:59],a[180:183],a[204:207],0
ds_read_b64_tr_b16 a[140:141],v22 offset:17416
v_mfma_f32_16x16x32_bf16 a[56:59],a[176:179],a[200:203],a[56:59]
ds_read_b64_tr_b16 a[142:143],v22 offset:19464
v_mfma_f32_16x16x32_bf16 a[88:91],a[176:179],a[208:211],0
ds_read_b64_tr_b16 a[144:145],v22 offset:1280
v_mfma_f32_16x16x32_bf16 a[88:91],a[180:183],a[212:215],a[88:91]
ds_read_b64_tr_b16 a[146:147],v22 offset:3328
v_mfma_f32_16x16x32_bf16 a[120:123],a[180:183],a[220:223],0
ds_read_b64_tr_b16 a[148:149],v22 offset:17664
v_mfma_f32_16x16x32_bf16 a[120:123],a[176:179],a[216:219],a[120:123]
ds_read_b64_tr_b16 a[150:151],v22 offset:19712
v_mfma_f32_16x16x32_bf16 a[28:31],a[184:187],a[192:195],0
ds_read_b64_tr_b16 a[152:153],v22 offset:1288
v_mfma_f32_16x16x32_bf16 a[28:31],a[188:191],a[196:199],a[28:31]
ds_read_b64_tr_b16 a[154:155],v22 offset:3336
ds_read_b64_tr_b16 a[156:157],v22 offset:17672
ds_read_b64_tr_b16 a[158:159],v22 offset:19720
v_mfma_f32_16x16x32_bf16 a[60:63],a[188:191],a[204:207],0
v_cmp_eq_u32 vcc,0,v64
s_nop 1
v_cndmask_b32 v28,0,v73,vcc
v_mfma_f32_16x16x32_bf16 a[60:63],a[184:187],a[200:203],a[60:63]
v_mov_b32 v29,v28
v_mov_b32 v30,v28
v_mfma_f32_16x16x32_bf16 a[92:95],a[184:187],a[208:211],0
v_mov_b32 v31,v28
v_bfe_u32 v73,v71,2,1
v_mfma_f32_16x16x32_bf16 a[92:95],a[188:191],a[212:215],a[92:95]
v_lshlrev_b32 v73,1,v73
v_xor_b32 v72,v72,v73
v_mfma_f32_16x16x32_bf16 a[124:127],a[188:191],a[220:223],0
s_add_u32 m0,s58,0
v_mul_u32_u24 v71,1280,v71
v_lshl_add_u32 v71,v72,4,v71
v_mfma_f32_16x16x32_bf16 a[124:127],a[184:187],a[216:219],a[124:127]
s_waitcnt lgkmcnt(4)
v_add_u32 v71,s44,v71
v_subrev_u32 v7,0,v71
v_mfma_f32_16x16x32_bf16 v[128:131],a[128:131],a[224:227],v[128:131]
v_mul_u32_u24 v71,3277,v70
v_lshrrev_b32 v71,16,v71
v_mfma_f32_16x16x32_bf16 v[128:131],a[132:135],a[228:231],v[128:131]
buffer_load_dwordx4 v56,s[32:35],s56 offen lds
v_mfma_f32_16x16x32_bf16 v[136:139],a[132:135],a[236:239],v[136:139]
ds_read_b64_tr_b16 a[160:161],v22 offset:1536
v_mul_u32_u24 v72,20,v71
v_mfma_f32_16x16x32_bf16 v[136:139],a[128:131],a[232:235],v[136:139]
s_add_u32 m0,s58,1024
ds_read_b64_tr_b16 a[162:163],v22 offset:3584
v_sub_u32 v72,v70,v72
v_mfma_f32_16x16x32_bf16 v[144:147],a[128:131],a[240:243],v[144:147]
ds_read_b64_tr_b16 a[164:165],v22 offset:17920
v_bfe_u32 v73,v71,2,1
v_mfma_f32_16x16x32_bf16 v[144:147],a[132:135],a[244:247],v[144:147]
ds_read_b64_tr_b16 a[166:167],v22 offset:19968
v_lshlrev_b32 v73,1,v73
v_mfma_f32_16x16x32_bf16 v[152:155],a[132:135],a[252:255],v[152:155]
buffer_load_dwordx4 v57,s[32:35],s56 offen lds
v_mfma_f32_16x16x32_bf16 v[152:155],a[128:131],a[248:251],v[152:155]
v_xor_b32 v72,v72,v73
v_mul_u32_u24 v71,1280,v71
v_mfma_f32_16x16x32_bf16 v[132:135],a[136:139],a[224:227],v[132:135]
v_lshl_add_u32 v71,v72,4,v71
)A"
R"A(v_add_u32 v71,s44,v71
v_mfma_f32_16x16x32_bf16 v[132:135],a[140:143],a[228:231],v[132:135]
s_add_u32 m0,s58,2048
ds_read_b64_tr_b16 a[168:169],v22 offset:1544
v_subrev_u32 v8,1024,v71
v_mfma_f32_16x16x32_bf16 v[140:143],a[140:143],a[236:239],v[140:143]
ds_read_b64_tr_b16 a[170:171],v22 offset:3592
v_add_u32 v70,128,v12
v_mfma_f32_16x16x32_bf16 v[140:143],a[136:139],a[232:235],v[140:143]
ds_read_b64_tr_b16 a[172:173],v22 offset:17928
v_mul_u32_u24 v71,3277,v70
v_mfma_f32_16x16x32_bf16 v[148:151],a[136:139],a[240:243],v[148:151]
buffer_load_dwordx4 v58,s[32:35],s56 offen lds
v_mfma_f32_16x16x32_bf16 v[148:151],a[140:143],a[244:247],v[148:151]
ds_read_b64_tr_b16 a[174:175],v22 offset:19976
v_lshrrev_b32 v71,16,v71
v_mfma_f32_16x16x32_bf16 v[156:159],a[140:143],a[252:255],v[156:159]
v_mul_u32_u24 v72,20,v71
v_sub_u32 v72,v70,v72
v_mfma_f32_16x16x32_bf16 v[156:159],a[136:139],a[248:251],v[156:159]
s_add_u32 m0,s58,3072
ds_read_b64_tr_b16 a[176:177],v22 offset:1792
v_bfe_u32 v73,v71,2,1
v_mfma_f32_16x16x32_bf16 v[160:163],a[144:147],a[224:227],v[160:163]
ds_read_b64_tr_b16 a[178:179],v22 offset:3840
v_lshlrev_b32 v73,1,v73
v_mfma_f32_16x16x32_bf16 v[160:163],a[148:151],a[228:231],v[160:163]
ds_read_b64_tr_b16 a[180:181],v22 offset:18176
v_xor_b32 v72,v72,v73
v_mfma_f32_16x16x32_bf16 v[168:171],a[148:151],a[236:239],v[168:171]
buffer_load_dwordx4 v59,s[32:35],s56 offen lds
v_mfma_f32_16x16x32_bf16 v[168:171],a[144:147],a[232:235],v[168:171]
ds_read_b64_tr_b16 a[182:183],v22 offset:20224
v_mul_u32_u24 v71,1280,v71
v_mfma_f32_16x16x32_bf16 v[176:179],a[144:147],a[240:243],v[176:179]
s_add_u32 m0,s58,4096
v_lshl_add_u32 v71,v72,4,v71
v_add_u32 v71,s44,v71
v_mfma_f32_16x16x32_bf16 v[176:179],a[148:151],a[244:247],v[176:179]
ds_read_b64_tr_b16 a[184:185],v22 offset:1800
v_subrev_u32 v9,2048,v71
v_mfma_f32_16x16x32_bf16 v[184:187],a[148:151],a[252:255],v[184:187]
ds_read_b64_tr_b16 a[186:187],v22 offset:3848
v_add_u32 v70,192,v12
v_mfma_f32_16x16x32_bf16 v[184:187],a[144:147],a[248:251],v[184:187]
s_waitcnt lgkmcnt(5)
buffer_load_dwordx4 v60,s[32:35],s56 offen lds
v_mfma_f32_16x16x32_bf16 v[164:167],a[152:155],a[224:227],v[164:167]
ds_read_b64_tr_b16 a[188:189],v22 offset:18184
v_mul_u32_u24 v71,3277,v70
v_mfma_f32_16x16x32_bf16 v[164:167],a[156:159],a[228:231],v[164:167]
ds_read_b64_tr_b16 a[190:191],v22 offset:20232
v_lshrrev_b32 v71,16,v71
v_mfma_f32_16x16x32_bf16 v[172:175],a[156:159],a[236:239],v[172:175]
s_add_u32 m0,s58,5120
v_mul_u32_u24 v72,20,v71
v_sub_u32 v72,v70,v72
v_mfma_f32_16x16x32_bf16 v[172:175],a[152:155],a[232:235],v[172:175]
v_bfe_u32 v73,v71,2,1
v_lshlrev_b32 v73,1,v73
v_mfma_f32_16x16x32_bf16 v[180:183],a[152:155],a[240:243],v[180:183]
ds_read_b64_tr_b16 a[128:129],v22
v_xor_b32 v72,v72,v73
v_mfma_f32_16x16x32_bf16 v[180:183],a[156:159],a[244:247],v[180:183]
buffer_load_dwordx4 v61,s[32:35],s56 offen lds
v_mfma_f32_16x16x32_bf16 v[188:191],a[156:159],a[252:255],v[188:191]
ds_read_b64_tr_b16 a[130:131],v22 offset:2048
v_mul_u32_u24 v71,1280,v71
v_mfma_f32_16x16x32_bf16 v[188:191],a[152:155],a[248:251],v[188:191]
ds_read_b64_tr_b16 a[132:133],v22 offset:16384
v_lshl_add_u32 v71,v72,4,v71
v_mfma_f32_16x16x32_bf16 v[192:195],a[160:163],a[224:227],v[192:195]
s_add_u32 m0,s58,6144
ds_read_b64_tr_b16 a[134:135],v22 offset:18432
v_add_u32 v71,s44,v71
v_mfma_f32_16x16x32_bf16 v[192:195],a[164:167],a[228:231],v[192:195]
v_subrev_u32 v10,3072,v71
v_add_u32 v70,256,v12
v_mfma_f32_16x16x32_bf16 v[200:203],a[164:167],a[236:239],v[200:203]
ds_read_b64_tr_b16 a[136:137],v22 offset:8
v_mul_u32_u24 v71,3277,v70
v_mfma_f32_16x16x32_bf16 v[200:203],a[160:163],a[232:235],v[200:203]
buffer_load_dwordx4 v62,s[32:35],s56 offen lds
v_mfma_f32_16x16x32_bf16 v[208:211],a[160:163],a[240:243],v[208:211]
ds_read_b64_tr_b16 a[138:139],v22 offset:2056
v_lshrrev_b32 v71,16,v71
v_mfma_f32_16x16x32_bf16 v[208:211],a[164:167],a[244:247],v[208:211]
s_add_u32 m0,s58,7168
ds_read_b64_tr_b16 a[140:141],v22 offset:16392
v_mul_u32_u24 v72,20,v71
v_mfma_f32_16x16x32_bf16 v[216:219],a[164:167],a[252:255],v[216:219]
s_add_u32 s58,s58,32768
ds_read_b64_tr_b16 a[142:143],v22 offset:18440
v_sub_u32 v72,v70,v72
v_mfma_f32_16x16x32_bf16 v[216:219],a[160:163],a[248:251],v[216:219]
v_bfe_u32 v73,v71,2,1
v_lshlrev_b32 v73,1,v73
v_mfma_f32_16x16x32_bf16 v[196:199],a[168:171],a[224:227],v[196:199]
buffer_load_dwordx4 v63,s[32:35],s56 offen lds
v_mfma_f32_16x16x32_bf16 v[196:199],a[172:175],a[228:231],v[196:199]
s_add_u32 s56,s56,524288
ds_read_b64_tr_b16 a[144:145],v22 offset:256
v_xor_b32 v72,v72,v73
v_mfma_f32_16x16x32_bf16 v[204:207],a[172:175],a[236:239],v[204:207]
ds_read_b64_tr_b16 a[146:147],v22 offset:2304
v_mul_u32_u24 v71,1280,v71
v_mfma_f32_16x16x32_bf16 v[204:207],a[168:171],a[232:235],v[204:207]
s_add_u32 m0,s59,0
ds_read_b64_tr_b16 a[148:149],v22 offset:16640
v_lshl_add_u32 v71,v72,4,v71
v_mfma_f32_16x16x32_bf16 v[212:215],a[168:171],a[240:243],v[212:215]
ds_read_b64_tr_b16 a[150:151],v22 offset:18688
v_add_u32 v71,s44,v71
v_mfma_f32_16x16x32_bf16 v[212:215],a[172:175],a[244:247],v[212:215]
ds_read_b64_tr_b16 a[152:153],v22 offset:264
v_subrev_u32 v11,0,v71
v_mfma_f32_16x16x32_bf16 v[220:223],a[172:175],a[252:255],v[220:223]
buffer_load_dwordx4 v48,s[28:31],s57 offen lds
v_mfma_f32_16x16x32_bf16 v[220:223],a[168:171],a[248:251],v[220:223]
s_cmp_ge_u32 s58,131072
s_cselect_b32 s62,98304,0
ds_read_b64_tr_b16 a[154:155],v22 offset:2312
s_waitcnt lgkmcnt(6)
v_mfma_f32_16x16x32_bf16 v[224:227],a[176:179],a[224:227],v[224:227]
s_sub_u32 s58,s58,s62
ds_read_b64_tr_b16 a[156:157],v22 offset:16648
v_mfma_f32_16x16x32_bf16 v[224:227],a[180:183],a[228:231],v[224:227]
ds_read_b64_tr_b16 a[158:159],v22 offset:18696
v_mfma_f32_16x16x32_bf16 v[232:235],a[180:183],a[236:239],v[232:235]
ds_read_b64_tr_b16 a[160:161],v22 offset:512
v_mfma_f32_16x16x32_bf16 v[232:235],a[176:179],a[232:235],v[232:235]
ds_read_b64_tr_b16 a[162:163],v22 offset:2560
v_mfma_f32_16x16x32_bf16 v[240:243],a[176:179],a[240:243],v[240:243]
buffer_load_dwordx4 v49,s[28:31],s57 offen offset:1024 lds
v_mfma_f32_16x16x32_bf16 v[240:243],a[180:183],a[244:247],v[240:243]
ds_read_b64_tr_b16 a[164:165],v22 offset:16896
v_mfma_f32_16x16x32_bf16 v[248:251],a[180:183],a[252:255],v[248:251]
ds_read_b64_tr_b16 a[166:167],v22 offset:18944
v_mfma_f32_16x16x32_bf16 v[248:251],a[176:179],a[248:251],v[248:251]
ds_read_b64_tr_b16 a[168:169],v22 offset:520
v_mfma_f32_16x16x32_bf16 v[228:231],a[184:187],a[224:227],v[228:231]
ds_read_b64_tr_b16 a[170:171],v22 offset:2568
v_mfma_f32_16x16x32_bf16 v[228:231],a[188:191],a[228:231],v[228:231]
buffer_load_dwordx4 v50,s[28:31],s57 offen offset:2048 lds
v_mfma_f32_16x16x32_bf16 v[236:239],a[188:191],a[236:239],v[236:239]
ds_read_b64_tr_b16 a[172:173],v22 offset:16904
v_mfma_f32_16x16x32_bf16 v[236:239],a[184:187],a[232:235],v[236:239]
ds_read_b64_tr_b16 a[174:175],v22 offset:18952
v_mfma_f32_16x16x32_bf16 v[244:247],a[184:187],a[240:243],v[244:247]
ds_read_b64_tr_b16 a[176:177],v22 offset:768
v_mfma_f32_16x16x32_bf16 v[244:247],a[188:191],a[244:247],v[244:247]
ds_read_b64_tr_b16 a[178:179],v22 offset:2816
v_mfma_f32_16x16x32_bf16 v[252:255],a[188:191],a[252:255],v[252:255]
ds_read_b64_tr_b16 a[180:181],v22 offset:17152
v_mfma_f32_16x16x32_bf16 v[252:255],a[184:187],a[248:251],v[252:255]
buffer_load_dwordx4 v51,s[28:31],s57 offen offset:3072 lds
v_mfma_f32_16x16x32_bf16 a[0:3],a[128:131],a[224:227],a[0:3]
ds_read_b64_tr_b16 a[182:183],v22 offset:19200
v_mfma_f32_16x16x32_bf16 a[0:3],a[132:135],a[228:231],a[0:3]
ds_read_b64_tr_b16 a[184:185],v22 offset:776
v_mfma_f32_16x16x32_bf16 a[32:35],a[132:135],a[236:239],a[32:35]
s_add_u32 m0,s59,4096
s_xor_b32 s59,s59,131072
ds_read_b64_tr_b16 a[186:187],v22 offset:2824
v_mfma_f32_16x16x32_bf16 a[32:35],a[128:131],a[232:235],a[32:35]
ds_read_b64_tr_b16 a[188:189],v22 offset:17160
v_mfma_f32_16x16x32_bf16 a[64:67],a[128:131],a[240:243],a[64:67]
ds_read_b64_tr_b16 a[190:191],v22 offset:19208
v_add_u32 v22,s60,v23
v_mfma_f32_16x16x32_bf16 a[64:67],a[132:135],a[244:247],a[64:67]
buffer_load_dwordx4 v52,s[28:31],s57 offen lds
v_mfma_f32_16x16x32_bf16 a[96:99],a[132:135],a[252:255],a[96:99]
v_mfma_f32_16x16x32_bf16 a[96:99],a[128:131],a[248:251],a[96:99]
v_mfma_f32_16x16x32_bf16 a[4:7],a[136:139],a[224:227],a[4:7]
v_mfma_f32_16x16x32_bf16 a[4:7],a[140:143],a[228:231],a[4:7]
v_mfma_f32_16x16x32_bf16 a[36:39],a[140:143],a[236:239],a[36:39]
buffer_load_dwordx4 v53,s[28:31],s57 offen offset:1024 lds
v_mfma_f32_16x16x32_bf16 a[36:39],a[136:139],a[232:235],a[36:39]
v_mfma_f32_16x16x32_bf16 a[68:71],a[136:139],a[240:243],a[68:71]
v_mfma_f32_16x16x32_bf16 a[68:71],a[140:143],a[244:247],a[68:71]
v_mfma_f32_16x16x32_bf16 a[100:103],a[140:143],a[252:255],a[100:103]
v_mfma_f32_16x16x32_bf16 a[100:103],a[136:139],a[248:251],a[100:103]
s_waitcnt lgkmcnt(0)
v_mfma_f32_16x16x32_bf16 a[8:11],a[144:147],a[224:227],a[8:11]
buffer_load_dwordx4 v54,s[28:31],s57 offen offset:2048 lds
v_mfma_f32_16x16x32_bf16 a[8:11],a[148:151],a[228:231],a[8:11]
v_mfma_f32_16x16x32_bf16 a[40:43],a[148:151],a[236:239],a[40:43]
v_mfma_f32_16x16x32_bf16 a[40:43],a[144:147],a[232:235],a[40:43]
v_mfma_f32_16x16x32_bf16 a[72:75],a[144:147],a[240:243],a[72:75]
v_mfma_f32_16x16x32_bf16 a[72:75],a[148:151],a[244:247],a[72:75]
v_mfma_f32_16x16x32_bf16 a[104:107],a[148:151],a[252:255],a[104:107]
buffer_load_dwordx4 v55,s[28:31],s57 offen offset:3072 lds
v_mfma_f32_16x16x32_bf16 a[104:107],a[144:147],a[248:251],a[104:107]
s_add_u32 s57,s57,128
v_mfma_f32_16x16x32_bf16 a[12:15],a[152:155],a[224:227],a[12:15]
v_mfma_f32_16x16x32_bf16 a[12:15],a[156:159],a[228:231],a[12:15]
v_mfma_f32_16x16x32_bf16 a[44:47],a[156:159],a[236:239],a[44:47]
v_mfma_f32_16x16x32_bf16 a[44:47],a[152:155],a[232:235],a[44:47]
v_mfma_f32_16x16x32_bf16 a[76:79],a[152:155],a[240:243],a[76:79]
v_mfma_f32_16x16x32_bf16 a[76:79],a[156:159],a[244:247],a[76:79]
v_mfma_f32_16x16x32_bf16 a[108:111],a[156:159],a[252:255],a[108:111]
v_mfma_f32_16x16x32_bf16 a[108:111],a[152:155],a[248:251],a[108:111]
s_waitcnt vmcnt(17)
v_mfma_f32_16x16x32_bf16 a[16:19],a[160:163],a[224:227],a[16:19]
s_barrier
ds_read_b128 a[192:195],v20
v_mfma_f32_16x16x32_bf16 a[16:19],a[164:167],a[228:231],a[16:19]
ds_read_b128 a[196:199],v21
v_mfma_f32_16x16x32_bf16 a[48:51],a[164:167],a[236:239],a[48:51]
ds_read_b128 a[200:203],v20 offset:2048
v_mfma_f32_16x16x32_bf16 a[48:51],a[160:163],a[232:235],a[48:51]
ds_read_b128 a[204:207],v21 offset:2048
v_mfma_f32_16x16x32_bf16 a[80:83],a[160:163],a[240:243],a[80:83]
ds_read_b128 a[208:211],v20 offset:4096
v_mfma_f32_16x16x32_bf16 a[80:83],a[164:167],a[244:247],a[80:83]
ds_read_b128 a[212:215],v21 offset:4096
v_mfma_f32_16x16x32_bf16 a[112:115],a[164:167],a[252:255],a[112:115]
ds_read_b128 a[216:219],v20 offset:6144
v_mfma_f32_16x16x32_bf16 a[112:115],a[160:163],a[248:251],a[112:115]
ds_read_b128 a[220:223],v21 offset:6144
v_mfma_f32_16x16x32_bf16 a[20:23],a[168:171],a[224:227],a[20:23]
ds_read_b64_tr_b16 a[128:129],v22 offset:1024
v_mfma_f32_16x16x32_bf16 a[20:23],a[172:175],a[228:231],a[20:23]
ds_read_b64_tr_b16 a[130:131],v22 offset:3072
v_mfma_f32_16x16x32_bf16 a[52:55],a[172:175],a[236:239],a[52:55]
ds_read_b64_tr_b16 a[132:133],v22 offset:17408
v_mfma_f32_16x16x32_bf16 a[52:55],a[168:171],a[232:235],a[52:55]
ds_read_b64_tr_b16 a[134:135],v22 offset:19456
v_mfma_f32_16x16x32_bf16 a[84:87],a[168:171],a[240:243],a[84:87]
ds_read_b64_tr_b16 a[136:137],v22 offset:1032
v_mfma_f32_16x16x32_bf16 a[84:87],a[172:175],a[244:247],a[84:87]
ds_read_b64_tr_b16 a[138:139],v22 offset:3080
v_mfma_f32_16x16x32_bf16 a[116:119],a[172:175],a[252:255],a[116:119]
ds_read_b64_tr_b16 a[140:141],v22 offset:17416
v_mfma_f32_16x16x32_bf16 a[116:119],a[168:171],a[248:251],a[116:119]
ds_read_b64_tr_b16 a[142:143],v22 offset:19464
v_mfma_f32_16x16x32_bf16 a[24:27],a[176:179],a[224:227],a[24:27]
ds_read_b64_tr_b16 a[144:145],v22 offset:1280
v_mfma_f32_16x16x32_bf16 a[24:27],a[180:183],a[228:231],a[24:27]
ds_read_b64_tr_b16 a[146:147],v22 offset:3328
v_mfma_f32_16x16x32_bf16 a[56:59],a[180:183],a[236:239],a[56:59]
)A"
R"A(ds_read_b64_tr_b16 a[148:149],v22 offset:17664
v_mfma_f32_16x16x32_bf16 a[56:59],a[176:179],a[232:235],a[56:59]
ds_read_b64_tr_b16 a[150:151],v22 offset:19712
v_mfma_f32_16x16x32_bf16 a[88:91],a[176:179],a[240:243],a[88:91]
ds_read_b64_tr_b16 a[152:153],v22 offset:1288
v_mfma_f32_16x16x32_bf16 a[88:91],a[180:183],a[244:247],a[88:91]
ds_read_b64_tr_b16 a[154:155],v22 offset:3336
v_mfma_f32_16x16x32_bf16 a[120:123],a[180:183],a[252:255],a[120:123]
ds_read_b64_tr_b16 a[156:157],v22 offset:17672
v_mfma_f32_16x16x32_bf16 a[120:123],a[176:179],a[248:251],a[120:123]
ds_read_b64_tr_b16 a[158:159],v22 offset:19720
v_mfma_f32_16x16x32_bf16 a[28:31],a[184:187],a[224:227],a[28:31]
v_mfma_f32_16x16x32_bf16 a[28:31],a[188:191],a[228:231],a[28:31]
v_mfma_f32_16x16x32_bf16 a[60:63],a[188:191],a[236:239],a[60:63]
v_mfma_f32_16x16x32_bf16 a[60:63],a[184:187],a[232:235],a[60:63]
v_mfma_f32_16x16x32_bf16 a[92:95],a[184:187],a[240:243],a[92:95]
v_mfma_f32_16x16x32_bf16 a[92:95],a[188:191],a[244:247],a[92:95]
v_mfma_f32_16x16x32_bf16 a[124:127],a[188:191],a[252:255],a[124:127]
v_mfma_f32_16x16x32_bf16 a[124:127],a[184:187],a[248:251],a[124:127]
s_mov_b32 s53,3
.p2align 5
L_qp_%=:
s_waitcnt lgkmcnt(1)
v_mfma_f32_16x16x32_bf16 v[128:131],a[128:131],a[192:195],v[128:131]
s_add_u32 m0,s58,0
s_add_u32 s60,s60,32768
ds_read_b64_tr_b16 a[160:161],v22 offset:1536
v_mfma_f32_16x16x32_bf16 v[128:131],a[132:135],a[196:199],v[128:131]
s_mov_b32 s90,L_att0x_%=-L_pc_%=+128
buffer_load_dwordx4 v56,s[32:35],s56 offen lds
v_mfma_f32_16x16x32_bf16 v[136:139],a[132:135],a[204:207],v[136:139]
s_cmp_eq_u32 s60,131072
s_cselect_b32 s60,32768,s60
ds_read_b64_tr_b16 a[162:163],v22 offset:3584
v_mfma_f32_16x16x32_bf16 v[136:139],a[128:131],a[200:203],v[136:139]
s_add_u32 m0,s58,1024
ds_read_b64_tr_b16 a[164:165],v22 offset:17920
v_xor_b32 v20,131072,v20
v_mfma_f32_16x16x32_bf16 v[144:147],a[128:131],a[208:211],v[144:147]
s_sub_u32 s53,s53,1
ds_read_b64_tr_b16 a[166:167],v22 offset:19968
v_xor_b32 v21,131072,v21
v_mfma_f32_16x16x32_bf16 v[144:147],a[132:135],a[212:215],v[144:147]
ds_read_b64_tr_b16 a[168:169],v22 offset:1544
v_mfma_f32_16x16x32_bf16 v[152:155],a[132:135],a[220:223],v[152:155]
buffer_load_dwordx4 v57,s[32:35],s56 offen lds
v_mfma_f32_16x16x32_bf16 v[152:155],a[128:131],a[216:219],v[152:155]
ds_read_b64_tr_b16 a[170:171],v22 offset:3592
v_mfma_f32_16x16x32_bf16 v[132:135],a[136:139],a[192:195],v[132:135]
ds_read_b64_tr_b16 a[172:173],v22 offset:17928
v_mfma_f32_16x16x32_bf16 v[132:135],a[140:143],a[196:199],v[132:135]
s_add_u32 m0,s58,2048
ds_read_b64_tr_b16 a[174:175],v22 offset:19976
v_mfma_f32_16x16x32_bf16 v[140:143],a[140:143],a[204:207],v[140:143]
ds_read_b64_tr_b16 a[176:177],v22 offset:1792
v_mfma_f32_16x16x32_bf16 v[140:143],a[136:139],a[200:203],v[140:143]
ds_read_b64_tr_b16 a[178:179],v22 offset:3840
v_mfma_f32_16x16x32_bf16 v[148:151],a[136:139],a[208:211],v[148:151]
buffer_load_dwordx4 v58,s[32:35],s56 offen lds
v_mfma_f32_16x16x32_bf16 v[148:151],a[140:143],a[212:215],v[148:151]
ds_read_b64_tr_b16 a[180:181],v22 offset:18176
v_mfma_f32_16x16x32_bf16 v[156:159],a[140:143],a[220:223],v[156:159]
s_add_u32 m0,s58,3072
ds_read_b64_tr_b16 a[182:183],v22 offset:20224
v_mfma_f32_16x16x32_bf16 v[156:159],a[136:139],a[216:219],v[156:159]
ds_read_b64_tr_b16 a[184:185],v22 offset:1800
v_mfma_f32_16x16x32_bf16 v[160:163],a[144:147],a[192:195],v[160:163]
ds_read_b64_tr_b16 a[186:187],v22 offset:3848
v_mfma_f32_16x16x32_bf16 v[160:163],a[148:151],a[196:199],v[160:163]
buffer_load_dwordx4 v59,s[32:35],s56 offen lds
v_mfma_f32_16x16x32_bf16 v[168:171],a[148:151],a[204:207],v[168:171]
ds_read_b64_tr_b16 a[188:189],v22 offset:18184
v_mfma_f32_16x16x32_bf16 v[168:171],a[144:147],a[200:203],v[168:171]
ds_read_b64_tr_b16 a[190:191],v22 offset:20232
v_mfma_f32_16x16x32_bf16 v[176:179],a[144:147],a[208:211],v[176:179]
s_add_u32 m0,s58,4096
ds_read_b64_tr_b16 a[128:129],v22
v_mfma_f32_16x16x32_bf16 v[176:179],a[148:151],a[212:215],v[176:179]
ds_read_b64_tr_b16 a[130:131],v22 offset:2048
v_mfma_f32_16x16x32_bf16 v[184:187],a[148:151],a[220:223],v[184:187]
ds_read_b64_tr_b16 a[132:133],v22 offset:16384
v_mfma_f32_16x16x32_bf16 v[184:187],a[144:147],a[216:219],v[184:187]
buffer_load_dwordx4 v60,s[32:35],s56 offen lds
v_mfma_f32_16x16x32_bf16 v[164:167],a[152:155],a[192:195],v[164:167]
s_waitcnt lgkmcnt(5)
ds_read_b64_tr_b16 a[134:135],v22 offset:18432
v_mfma_f32_16x16x32_bf16 v[164:167],a[156:159],a[196:199],v[164:167]
s_add_u32 m0,s58,5120
ds_read_b64_tr_b16 a[136:137],v22 offset:8
v_mfma_f32_16x16x32_bf16 v[172:175],a[156:159],a[204:207],v[172:175]
ds_read_b64_tr_b16 a[138:139],v22 offset:2056
v_mfma_f32_16x16x32_bf16 v[172:175],a[152:155],a[200:203],v[172:175]
ds_read_b64_tr_b16 a[140:141],v22 offset:16392
v_mfma_f32_16x16x32_bf16 v[180:183],a[152:155],a[208:211],v[180:183]
buffer_load_dwordx4 v61,s[32:35],s56 offen lds
v_mfma_f32_16x16x32_bf16 v[180:183],a[156:159],a[212:215],v[180:183]
ds_read_b64_tr_b16 a[142:143],v22 offset:18440
v_mfma_f32_16x16x32_bf16 v[188:191],a[156:159],a[220:223],v[188:191]
ds_read_b64_tr_b16 a[144:145],v22 offset:256
v_mfma_f32_16x16x32_bf16 v[188:191],a[152:155],a[216:219],v[188:191]
s_add_u32 m0,s58,6144
ds_read_b64_tr_b16 a[146:147],v22 offset:2304
v_mfma_f32_16x16x32_bf16 v[192:195],a[160:163],a[192:195],v[192:195]
ds_read_b64_tr_b16 a[148:149],v22 offset:16640
v_mfma_f32_16x16x32_bf16 v[192:195],a[164:167],a[196:199],v[192:195]
ds_read_b64_tr_b16 a[150:151],v22 offset:18688
v_mfma_f32_16x16x32_bf16 v[200:203],a[164:167],a[204:207],v[200:203]
buffer_load_dwordx4 v62,s[32:35],s56 offen lds
v_mfma_f32_16x16x32_bf16 v[200:203],a[160:163],a[200:203],v[200:203]
ds_read_b64_tr_b16 a[152:153],v22 offset:264
v_mfma_f32_16x16x32_bf16 v[208:211],a[160:163],a[208:211],v[208:211]
s_add_u32 m0,s58,7168
s_add_u32 s58,s58,32768
ds_read_b64_tr_b16 a[154:155],v22 offset:2312
v_mfma_f32_16x16x32_bf16 v[208:211],a[164:167],a[212:215],v[208:211]
s_cmp_ge_u32 s58,131072
s_cselect_b32 s62,98304,0
ds_read_b64_tr_b16 a[156:157],v22 offset:16648
v_mfma_f32_16x16x32_bf16 v[216:219],a[164:167],a[220:223],v[216:219]
s_sub_u32 s58,s58,s62
ds_read_b64_tr_b16 a[158:159],v22 offset:18696
v_mfma_f32_16x16x32_bf16 v[216:219],a[160:163],a[216:219],v[216:219]
buffer_load_dwordx4 v63,s[32:35],s56 offen lds
v_mfma_f32_16x16x32_bf16 v[196:199],a[168:171],a[192:195],v[196:199]
s_add_u32 s56,s56,524288
ds_read_b64_tr_b16 a[160:161],v22 offset:512
v_mfma_f32_16x16x32_bf16 v[196:199],a[172:175],a[196:199],v[196:199]
ds_read_b64_tr_b16 a[162:163],v22 offset:2560
v_mfma_f32_16x16x32_bf16 v[204:207],a[172:175],a[204:207],v[204:207]
s_add_u32 m0,s59,0
ds_read_b64_tr_b16 a[164:165],v22 offset:16896
v_mfma_f32_16x16x32_bf16 v[204:207],a[168:171],a[200:203],v[204:207]
ds_read_b64_tr_b16 a[166:167],v22 offset:18944
v_mfma_f32_16x16x32_bf16 v[212:215],a[168:171],a[208:211],v[212:215]
v_mfma_f32_16x16x32_bf16 v[212:215],a[172:175],a[212:215],v[212:215]
buffer_load_dwordx4 v48,s[28:31],s57 offen lds
v_mfma_f32_16x16x32_bf16 v[220:223],a[172:175],a[220:223],v[220:223]
ds_read_b64_tr_b16 a[172:173],v22 offset:16904
v_mfma_f32_16x16x32_bf16 v[220:223],a[168:171],a[216:219],v[220:223]
ds_read_b64_tr_b16 a[168:169],v22 offset:520
v_mfma_f32_16x16x32_bf16 v[224:227],a[176:179],a[192:195],v[224:227]
ds_read_b64_tr_b16 a[170:171],v22 offset:2568
v_mfma_f32_16x16x32_bf16 v[224:227],a[180:183],a[196:199],v[224:227]
ds_read_b64_tr_b16 a[174:175],v22 offset:18952
v_mfma_f32_16x16x32_bf16 v[232:235],a[180:183],a[204:207],v[232:235]
buffer_load_dwordx4 v49,s[28:31],s57 offen offset:1024 lds
v_mfma_f32_16x16x32_bf16 v[232:235],a[176:179],a[200:203],v[232:235]
v_mfma_f32_16x16x32_bf16 v[240:243],a[176:179],a[208:211],v[240:243]
v_mfma_f32_16x16x32_bf16 v[240:243],a[180:183],a[212:215],v[240:243]
v_mfma_f32_16x16x32_bf16 v[248:251],a[180:183],a[220:223],v[248:251]
ds_read_b64_tr_b16 a[180:181],v22 offset:17152
v_mfma_f32_16x16x32_bf16 v[248:251],a[176:179],a[216:219],v[248:251]
ds_read_b64_tr_b16 a[176:177],v22 offset:768
v_mfma_f32_16x16x32_bf16 v[228:231],a[184:187],a[192:195],v[228:231]
s_waitcnt lgkmcnt(3)
buffer_load_dwordx4 v50,s[28:31],s57 offen offset:2048 lds
v_mfma_f32_16x16x32_bf16 v[228:231],a[188:191],a[196:199],v[228:231]
ds_read_b64_tr_b16 a[178:179],v22 offset:2816
v_mfma_f32_16x16x32_bf16 v[236:239],a[188:191],a[204:207],v[236:239]
ds_read_b64_tr_b16 a[182:183],v22 offset:19200
v_mfma_f32_16x16x32_bf16 v[236:239],a[184:187],a[200:203],v[236:239]
v_mfma_f32_16x16x32_bf16 v[244:247],a[184:187],a[208:211],v[244:247]
v_mfma_f32_16x16x32_bf16 v[244:247],a[188:191],a[212:215],v[244:247]
buffer_load_dwordx4 v51,s[28:31],s57 offen offset:3072 lds
v_mfma_f32_16x16x32_bf16 v[252:255],a[188:191],a[220:223],v[252:255]
ds_read_b64_tr_b16 a[188:189],v22 offset:17160
v_mfma_f32_16x16x32_bf16 v[252:255],a[184:187],a[216:219],v[252:255]
ds_read_b64_tr_b16 a[184:185],v22 offset:776
v_mfma_f32_16x16x32_bf16 a[0:3],a[128:131],a[192:195],a[0:3]
s_add_u32 m0,s59,4096
s_xor_b32 s59,s59,131072
ds_read_b64_tr_b16 a[186:187],v22 offset:2824
v_mfma_f32_16x16x32_bf16 a[0:3],a[132:135],a[196:199],a[0:3]
ds_read_b64_tr_b16 a[190:191],v22 offset:19208
v_add_u32 v22,s60,v23
v_mfma_f32_16x16x32_bf16 a[32:35],a[132:135],a[204:207],a[32:35]
s_add_u32 s60,s60,32768
s_cmp_eq_u32 s60,131072
s_cselect_b32 s60,32768,s60
v_mfma_f32_16x16x32_bf16 a[32:35],a[128:131],a[200:203],a[32:35]
buffer_load_dwordx4 v52,s[28:31],s57 offen lds
v_mfma_f32_16x16x32_bf16 a[64:67],a[128:131],a[208:211],a[64:67]
v_mfma_f32_16x16x32_bf16 a[64:67],a[132:135],a[212:215],a[64:67]
v_mfma_f32_16x16x32_bf16 a[96:99],a[132:135],a[220:223],a[96:99]
v_mfma_f32_16x16x32_bf16 a[96:99],a[128:131],a[216:219],a[96:99]
v_mfma_f32_16x16x32_bf16 a[4:7],a[136:139],a[192:195],a[4:7]
buffer_load_dwordx4 v53,s[28:31],s57 offen offset:1024 lds
v_mfma_f32_16x16x32_bf16 a[4:7],a[140:143],a[196:199],a[4:7]
v_mfma_f32_16x16x32_bf16 a[36:39],a[140:143],a[204:207],a[36:39]
v_mfma_f32_16x16x32_bf16 a[36:39],a[136:139],a[200:203],a[36:39]
v_mfma_f32_16x16x32_bf16 a[68:71],a[136:139],a[208:211],a[68:71]
v_mfma_f32_16x16x32_bf16 a[68:71],a[140:143],a[212:215],a[68:71]
v_mfma_f32_16x16x32_bf16 a[100:103],a[140:143],a[220:223],a[100:103]
buffer_load_dwordx4 v54,s[28:31],s57 offen offset:2048 lds
v_mfma_f32_16x16x32_bf16 a[100:103],a[136:139],a[216:219],a[100:103]
v_mfma_f32_16x16x32_bf16 a[8:11],a[144:147],a[192:195],a[8:11]
v_mfma_f32_16x16x32_bf16 a[8:11],a[148:151],a[196:199],a[8:11]
v_mfma_f32_16x16x32_bf16 a[40:43],a[148:151],a[204:207],a[40:43]
v_mfma_f32_16x16x32_bf16 a[40:43],a[144:147],a[200:203],a[40:43]
buffer_load_dwordx4 v55,s[28:31],s57 offen offset:3072 lds
v_mfma_f32_16x16x32_bf16 a[72:75],a[144:147],a[208:211],a[72:75]
s_add_u32 s57,s57,128
v_mfma_f32_16x16x32_bf16 a[72:75],a[148:151],a[212:215],a[72:75]
v_mfma_f32_16x16x32_bf16 a[104:107],a[148:151],a[220:223],a[104:107]
v_mfma_f32_16x16x32_bf16 a[104:107],a[144:147],a[216:219],a[104:107]
v_mfma_f32_16x16x32_bf16 a[12:15],a[152:155],a[192:195],a[12:15]
v_mfma_f32_16x16x32_bf16 a[12:15],a[156:159],a[196:199],a[12:15]
v_mfma_f32_16x16x32_bf16 a[44:47],a[156:159],a[204:207],a[44:47]
buffer_load_dword v16,v15,s[72:75],s83 offen
v_mfma_f32_16x16x32_bf16 a[44:47],a[152:155],a[200:203],a[44:47]
s_add_u32 s83,s83,896
v_mfma_f32_16x16x32_bf16 a[76:79],a[152:155],a[208:211],a[76:79]
v_mfma_f32_16x16x32_bf16 a[76:79],a[156:159],a[212:215],a[76:79]
v_mfma_f32_16x16x32_bf16 a[108:111],a[156:159],a[220:223],a[108:111]
v_mfma_f32_16x16x32_bf16 a[108:111],a[152:155],a[216:219],a[108:111]
s_waitcnt vmcnt(17) lgkmcnt(0)
v_mfma_f32_16x16x32_bf16 a[16:19],a[160:163],a[192:195],a[16:19]
s_barrier
ds_read_b128 a[224:227],v20
v_mfma_f32_16x16x32_bf16 a[16:19],a[164:167],a[196:199],a[16:19]
ds_read_b64_tr_b16 a[128:129],v22 offset:1024
v_mfma_f32_16x16x32_bf16 a[48:51],a[164:167],a[204:207],a[48:51]
ds_read_b64_tr_b16 a[130:131],v22 offset:3072
v_mfma_f32_16x16x32_bf16 a[48:51],a[160:163],a[200:203],a[48:51]
ds_read_b128 a[228:231],v21
v_mfma_f32_16x16x32_bf16 a[80:83],a[160:163],a[208:211],a[80:83]
)A"
R"A(ds_read_b64_tr_b16 a[132:133],v22 offset:17408
v_mfma_f32_16x16x32_bf16 a[80:83],a[164:167],a[212:215],a[80:83]
ds_read_b64_tr_b16 a[134:135],v22 offset:19456
v_mfma_f32_16x16x32_bf16 a[112:115],a[164:167],a[220:223],a[112:115]
ds_read_b128 a[232:235],v20 offset:2048
v_mfma_f32_16x16x32_bf16 a[112:115],a[160:163],a[216:219],a[112:115]
ds_read_b128 a[236:239],v21 offset:2048
v_mfma_f32_16x16x32_bf16 a[20:23],a[168:171],a[192:195],a[20:23]
ds_read_b128 a[240:243],v20 offset:4096
v_mfma_f32_16x16x32_bf16 a[20:23],a[172:175],a[196:199],a[20:23]
ds_read_b128 a[244:247],v21 offset:4096
v_mfma_f32_16x16x32_bf16 a[52:55],a[172:175],a[204:207],a[52:55]
ds_read_b128 a[248:251],v20 offset:6144
v_xor_b32 v20,131072,v20
v_mfma_f32_16x16x32_bf16 a[52:55],a[168:171],a[200:203],a[52:55]
ds_read_b128 a[252:255],v21 offset:6144
v_xor_b32 v21,131072,v21
v_mfma_f32_16x16x32_bf16 a[84:87],a[168:171],a[208:211],a[84:87]
ds_read_b64_tr_b16 a[136:137],v22 offset:1032
v_mfma_f32_16x16x32_bf16 a[84:87],a[172:175],a[212:215],a[84:87]
ds_read_b64_tr_b16 a[138:139],v22 offset:3080
v_mfma_f32_16x16x32_bf16 a[116:119],a[172:175],a[220:223],a[116:119]
ds_read_b64_tr_b16 a[140:141],v22 offset:17416
v_mfma_f32_16x16x32_bf16 a[116:119],a[168:171],a[216:219],a[116:119]
ds_read_b64_tr_b16 a[142:143],v22 offset:19464
v_mfma_f32_16x16x32_bf16 a[24:27],a[176:179],a[192:195],a[24:27]
ds_read_b64_tr_b16 a[144:145],v22 offset:1280
v_mfma_f32_16x16x32_bf16 a[24:27],a[180:183],a[196:199],a[24:27]
ds_read_b64_tr_b16 a[146:147],v22 offset:3328
v_mfma_f32_16x16x32_bf16 a[56:59],a[180:183],a[204:207],a[56:59]
ds_read_b64_tr_b16 a[148:149],v22 offset:17664
v_mfma_f32_16x16x32_bf16 a[56:59],a[176:179],a[200:203],a[56:59]
ds_read_b64_tr_b16 a[150:151],v22 offset:19712
v_mfma_f32_16x16x32_bf16 a[88:91],a[176:179],a[208:211],a[88:91]
ds_read_b64_tr_b16 a[152:153],v22 offset:1288
v_mfma_f32_16x16x32_bf16 a[88:91],a[180:183],a[212:215],a[88:91]
ds_read_b64_tr_b16 a[154:155],v22 offset:3336
v_mfma_f32_16x16x32_bf16 a[120:123],a[180:183],a[220:223],a[120:123]
ds_read_b64_tr_b16 a[156:157],v22 offset:17672
v_mfma_f32_16x16x32_bf16 a[120:123],a[176:179],a[216:219],a[120:123]
ds_read_b64_tr_b16 a[158:159],v22 offset:19720
v_mfma_f32_16x16x32_bf16 a[28:31],a[184:187],a[192:195],a[28:31]
ds_read_b64_tr_b16 a[160:161],v22 offset:1536
v_mfma_f32_16x16x32_bf16 a[28:31],a[188:191],a[196:199],a[28:31]
ds_read_b64_tr_b16 a[162:163],v22 offset:3584
v_mfma_f32_16x16x32_bf16 a[60:63],a[188:191],a[204:207],a[60:63]
ds_read_b64_tr_b16 a[164:165],v22 offset:17920
v_mfma_f32_16x16x32_bf16 a[60:63],a[184:187],a[200:203],a[60:63]
ds_read_b64_tr_b16 a[166:167],v22 offset:19968
v_mfma_f32_16x16x32_bf16 a[92:95],a[184:187],a[208:211],a[92:95]
ds_read_b64_tr_b16 a[168:169],v22 offset:1544
v_mfma_f32_16x16x32_bf16 a[92:95],a[188:191],a[212:215],a[92:95]
ds_read_b64_tr_b16 a[170:171],v22 offset:3592
v_mfma_f32_16x16x32_bf16 a[124:127],a[188:191],a[220:223],a[124:127]
s_add_u32 m0,s58,0
ds_read_b64_tr_b16 a[172:173],v22 offset:17928
v_mfma_f32_16x16x32_bf16 a[124:127],a[184:187],a[216:219],a[124:127]
s_waitcnt lgkmcnt(7)
ds_read_b64_tr_b16 a[174:175],v22 offset:19976
v_mfma_f32_16x16x32_bf16 v[128:131],a[128:131],a[224:227],v[128:131]
ds_read_b64_tr_b16 a[176:177],v22 offset:1792
v_mfma_f32_16x16x32_bf16 v[128:131],a[132:135],a[228:231],v[128:131]
buffer_load_dwordx4 v56,s[32:35],s56 offen lds
v_mfma_f32_16x16x32_bf16 v[136:139],a[132:135],a[236:239],v[136:139]
ds_read_b64_tr_b16 a[178:179],v22 offset:3840
v_mfma_f32_16x16x32_bf16 v[136:139],a[128:131],a[232:235],v[136:139]
s_add_u32 m0,s58,1024
ds_read_b64_tr_b16 a[180:181],v22 offset:18176
v_mfma_f32_16x16x32_bf16 v[144:147],a[128:131],a[240:243],v[144:147]
ds_read_b64_tr_b16 a[182:183],v22 offset:20224
v_mfma_f32_16x16x32_bf16 v[144:147],a[132:135],a[244:247],v[144:147]
ds_read_b64_tr_b16 a[184:185],v22 offset:1800
v_mfma_f32_16x16x32_bf16 v[152:155],a[132:135],a[252:255],v[152:155]
buffer_load_dwordx4 v57,s[32:35],s56 offen lds
v_mfma_f32_16x16x32_bf16 v[152:155],a[128:131],a[248:251],v[152:155]
ds_read_b64_tr_b16 a[186:187],v22 offset:3848
v_mfma_f32_16x16x32_bf16 v[132:135],a[136:139],a[224:227],v[132:135]
ds_read_b64_tr_b16 a[188:189],v22 offset:18184
v_mfma_f32_16x16x32_bf16 v[132:135],a[140:143],a[228:231],v[132:135]
s_add_u32 m0,s58,2048
ds_read_b64_tr_b16 a[190:191],v22 offset:20232
v_mfma_f32_16x16x32_bf16 v[140:143],a[140:143],a[236:239],v[140:143]
ds_read_b64_tr_b16 a[128:129],v22
v_mfma_f32_16x16x32_bf16 v[140:143],a[136:139],a[232:235],v[140:143]
ds_read_b64_tr_b16 a[130:131],v22 offset:2048
v_mfma_f32_16x16x32_bf16 v[148:151],a[136:139],a[240:243],v[148:151]
buffer_load_dwordx4 v58,s[32:35],s56 offen lds
v_mfma_f32_16x16x32_bf16 v[148:151],a[140:143],a[244:247],v[148:151]
ds_read_b64_tr_b16 a[132:133],v22 offset:16384
v_mfma_f32_16x16x32_bf16 v[156:159],a[140:143],a[252:255],v[156:159]
s_add_u32 m0,s58,3072
ds_read_b64_tr_b16 a[134:135],v22 offset:18432
v_mfma_f32_16x16x32_bf16 v[156:159],a[136:139],a[248:251],v[156:159]
ds_read_b64_tr_b16 a[136:137],v22 offset:8
v_mfma_f32_16x16x32_bf16 v[160:163],a[144:147],a[224:227],v[160:163]
ds_read_b64_tr_b16 a[138:139],v22 offset:2056
v_mfma_f32_16x16x32_bf16 v[160:163],a[148:151],a[228:231],v[160:163]
buffer_load_dwordx4 v59,s[32:35],s56 offen lds
v_mfma_f32_16x16x32_bf16 v[168:171],a[148:151],a[236:239],v[168:171]
ds_read_b64_tr_b16 a[140:141],v22 offset:16392
v_mfma_f32_16x16x32_bf16 v[168:171],a[144:147],a[232:235],v[168:171]
ds_read_b64_tr_b16 a[142:143],v22 offset:18440
v_mfma_f32_16x16x32_bf16 v[176:179],a[144:147],a[240:243],v[176:179]
s_add_u32 m0,s58,4096
v_mfma_f32_16x16x32_bf16 v[176:179],a[148:151],a[244:247],v[176:179]
v_mfma_f32_16x16x32_bf16 v[184:187],a[148:151],a[252:255],v[184:187]
ds_read_b64_tr_b16 a[148:149],v22 offset:16640
v_mfma_f32_16x16x32_bf16 v[184:187],a[144:147],a[248:251],v[184:187]
buffer_load_dwordx4 v60,s[32:35],s56 offen lds
v_mfma_f32_16x16x32_bf16 v[164:167],a[152:155],a[224:227],v[164:167]
ds_read_b64_tr_b16 a[144:145],v22 offset:256
v_mfma_f32_16x16x32_bf16 v[164:167],a[156:159],a[228:231],v[164:167]
s_add_u32 m0,s58,5120
ds_read_b64_tr_b16 a[146:147],v22 offset:2304
v_mfma_f32_16x16x32_bf16 v[172:175],a[156:159],a[236:239],v[172:175]
ds_read_b64_tr_b16 a[150:151],v22 offset:18688
v_mfma_f32_16x16x32_bf16 v[172:175],a[152:155],a[232:235],v[172:175]
v_mfma_f32_16x16x32_bf16 v[180:183],a[152:155],a[240:243],v[180:183]
buffer_load_dwordx4 v61,s[32:35],s56 offen lds
v_mfma_f32_16x16x32_bf16 v[180:183],a[156:159],a[244:247],v[180:183]
v_mfma_f32_16x16x32_bf16 v[188:191],a[156:159],a[252:255],v[188:191]
ds_read_b64_tr_b16 a[156:157],v22 offset:16648
v_mfma_f32_16x16x32_bf16 v[188:191],a[152:155],a[248:251],v[188:191]
s_add_u32 m0,s58,6144
ds_read_b64_tr_b16 a[152:153],v22 offset:264
s_waitcnt lgkmcnt(5)
v_mfma_f32_16x16x32_bf16 v[192:195],a[160:163],a[224:227],v[192:195]
ds_read_b64_tr_b16 a[154:155],v22 offset:2312
v_mfma_f32_16x16x32_bf16 v[192:195],a[164:167],a[228:231],v[192:195]
ds_read_b64_tr_b16 a[158:159],v22 offset:18696
v_mfma_f32_16x16x32_bf16 v[200:203],a[164:167],a[236:239],v[200:203]
buffer_load_dwordx4 v62,s[32:35],s56 offen lds
v_mfma_f32_16x16x32_bf16 v[200:203],a[160:163],a[232:235],v[200:203]
v_mfma_f32_16x16x32_bf16 v[208:211],a[160:163],a[240:243],v[208:211]
s_add_u32 m0,s58,7168
s_add_u32 s58,s58,32768
v_mfma_f32_16x16x32_bf16 v[208:211],a[164:167],a[244:247],v[208:211]
s_cmp_ge_u32 s58,131072
s_cselect_b32 s62,98304,0
s_sub_u32 s58,s58,s62
v_mfma_f32_16x16x32_bf16 v[216:219],a[164:167],a[252:255],v[216:219]
ds_read_b64_tr_b16 a[164:165],v22 offset:16896
v_mfma_f32_16x16x32_bf16 v[216:219],a[160:163],a[248:251],v[216:219]
buffer_load_dwordx4 v63,s[32:35],s56 offen lds
v_mfma_f32_16x16x32_bf16 v[196:199],a[168:171],a[224:227],v[196:199]
s_add_u32 s56,s56,524288
ds_read_b64_tr_b16 a[160:161],v22 offset:512
v_mfma_f32_16x16x32_bf16 v[196:199],a[172:175],a[228:231],v[196:199]
ds_read_b64_tr_b16 a[162:163],v22 offset:2560
v_mfma_f32_16x16x32_bf16 v[204:207],a[172:175],a[236:239],v[204:207]
s_add_u32 m0,s59,0
ds_read_b64_tr_b16 a[166:167],v22 offset:18944
v_mfma_f32_16x16x32_bf16 v[204:207],a[168:171],a[232:235],v[204:207]
v_mfma_f32_16x16x32_bf16 v[212:215],a[168:171],a[240:243],v[212:215]
v_mfma_f32_16x16x32_bf16 v[212:215],a[172:175],a[244:247],v[212:215]
buffer_load_dwordx4 v48,s[28:31],s57 offen lds
v_mfma_f32_16x16x32_bf16 v[220:223],a[172:175],a[252:255],v[220:223]
ds_read_b64_tr_b16 a[172:173],v22 offset:16904
v_mfma_f32_16x16x32_bf16 v[220:223],a[168:171],a[248:251],v[220:223]
ds_read_b64_tr_b16 a[168:169],v22 offset:520
v_mfma_f32_16x16x32_bf16 v[224:227],a[176:179],a[224:227],v[224:227]
ds_read_b64_tr_b16 a[170:171],v22 offset:2568
v_mfma_f32_16x16x32_bf16 v[224:227],a[180:183],a[228:231],v[224:227]
ds_read_b64_tr_b16 a[174:175],v22 offset:18952
v_mfma_f32_16x16x32_bf16 v[232:235],a[180:183],a[236:239],v[232:235]
buffer_load_dwordx4 v49,s[28:31],s57 offen offset:1024 lds
v_mfma_f32_16x16x32_bf16 v[232:235],a[176:179],a[232:235],v[232:235]
v_mfma_f32_16x16x32_bf16 v[240:243],a[176:179],a[240:243],v[240:243]
v_mfma_f32_16x16x32_bf16 v[240:243],a[180:183],a[244:247],v[240:243]
v_mfma_f32_16x16x32_bf16 v[248:251],a[180:183],a[252:255],v[248:251]
ds_read_b64_tr_b16 a[180:181],v22 offset:17152
v_mfma_f32_16x16x32_bf16 v[248:251],a[176:179],a[248:251],v[248:251]
ds_read_b64_tr_b16 a[176:177],v22 offset:768
v_mfma_f32_16x16x32_bf16 v[228:231],a[184:187],a[224:227],v[228:231]
buffer_load_dwordx4 v50,s[28:31],s57 offen offset:2048 lds
v_mfma_f32_16x16x32_bf16 v[228:231],a[188:191],a[228:231],v[228:231]
ds_read_b64_tr_b16 a[178:179],v22 offset:2816
v_mfma_f32_16x16x32_bf16 v[236:239],a[188:191],a[236:239],v[236:239]
ds_read_b64_tr_b16 a[182:183],v22 offset:19200
v_mfma_f32_16x16x32_bf16 v[236:239],a[184:187],a[232:235],v[236:239]
v_mfma_f32_16x16x32_bf16 v[244:247],a[184:187],a[240:243],v[244:247]
v_mfma_f32_16x16x32_bf16 v[244:247],a[188:191],a[244:247],v[244:247]
buffer_load_dwordx4 v51,s[28:31],s57 offen offset:3072 lds
v_mfma_f32_16x16x32_bf16 v[252:255],a[188:191],a[252:255],v[252:255]
ds_read_b64_tr_b16 a[188:189],v22 offset:17160
v_mfma_f32_16x16x32_bf16 v[252:255],a[184:187],a[248:251],v[252:255]
ds_read_b64_tr_b16 a[184:185],v22 offset:776
v_mfma_f32_16x16x32_bf16 a[0:3],a[128:131],a[224:227],a[0:3]
s_add_u32 m0,s59,4096
s_xor_b32 s59,s59,131072
ds_read_b64_tr_b16 a[186:187],v22 offset:2824
v_mfma_f32_16x16x32_bf16 a[0:3],a[132:135],a[228:231],a[0:3]
ds_read_b64_tr_b16 a[190:191],v22 offset:19208
v_add_u32 v22,s60,v23
v_mfma_f32_16x16x32_bf16 a[32:35],a[132:135],a[236:239],a[32:35]
v_mfma_f32_16x16x32_bf16 a[32:35],a[128:131],a[232:235],a[32:35]
buffer_load_dwordx4 v52,s[28:31],s57 offen lds
v_mfma_f32_16x16x32_bf16 a[64:67],a[128:131],a[240:243],a[64:67]
v_mfma_f32_16x16x32_bf16 a[64:67],a[132:135],a[244:247],a[64:67]
v_mfma_f32_16x16x32_bf16 a[96:99],a[132:135],a[252:255],a[96:99]
v_mfma_f32_16x16x32_bf16 a[96:99],a[128:131],a[248:251],a[96:99]
v_mfma_f32_16x16x32_bf16 a[4:7],a[136:139],a[224:227],a[4:7]
buffer_load_dwordx4 v53,s[28:31],s57 offen offset:1024 lds
v_mfma_f32_16x16x32_bf16 a[4:7],a[140:143],a[228:231],a[4:7]
v_mfma_f32_16x16x32_bf16 a[36:39],a[140:143],a[236:239],a[36:39]
v_mfma_f32_16x16x32_bf16 a[36:39],a[136:139],a[232:235],a[36:39]
v_mfma_f32_16x16x32_bf16 a[68:71],a[136:139],a[240:243],a[68:71]
v_mfma_f32_16x16x32_bf16 a[68:71],a[140:143],a[244:247],a[68:71]
v_mfma_f32_16x16x32_bf16 a[100:103],a[140:143],a[252:255],a[100:103]
buffer_load_dwordx4 v54,s[28:31],s57 offen offset:2048 lds
v_mfma_f32_16x16x32_bf16 a[100:103],a[136:139],a[248:251],a[100:103]
s_waitcnt lgkmcnt(0)
v_mfma_f32_16x16x32_bf16 a[8:11],a[144:147],a[224:227],a[8:11]
v_mfma_f32_16x16x32_bf16 a[8:11],a[148:151],a[228:231],a[8:11]
v_mfma_f32_16x16x32_bf16 a[40:43],a[148:151],a[236:239],a[40:43]
v_mfma_f32_16x16x32_bf16 a[40:43],a[144:147],a[232:235],a[40:43]
buffer_load_dwordx4 v55,s[28:31],s57 offen offset:3072 lds
v_mfma_f32_16x16x32_bf16 a[72:75],a[144:147],a[240:243],a[72:75]
s_add_u32 s57,s57,128
v_mfma_f32_16x16x32_bf16 a[72:75],a[148:151],a[244:247],a[72:75]
)A"
R"A(v_mfma_f32_16x16x32_bf16 a[104:107],a[148:151],a[252:255],a[104:107]
v_mfma_f32_16x16x32_bf16 a[104:107],a[144:147],a[248:251],a[104:107]
v_mfma_f32_16x16x32_bf16 a[12:15],a[152:155],a[224:227],a[12:15]
v_mfma_f32_16x16x32_bf16 a[12:15],a[156:159],a[228:231],a[12:15]
v_mfma_f32_16x16x32_bf16 a[44:47],a[156:159],a[236:239],a[44:47]
buffer_load_dword v16,v15,s[72:75],s83 offen
v_mfma_f32_16x16x32_bf16 a[44:47],a[152:155],a[232:235],a[44:47]
s_add_u32 s83,s83,896
v_mfma_f32_16x16x32_bf16 a[76:79],a[152:155],a[240:243],a[76:79]
v_mfma_f32_16x16x32_bf16 a[76:79],a[156:159],a[244:247],a[76:79]
v_mfma_f32_16x16x32_bf16 a[108:111],a[156:159],a[252:255],a[108:111]
v_mfma_f32_16x16x32_bf16 a[108:111],a[152:155],a[248:251],a[108:111]
s_waitcnt vmcnt(17) lgkmcnt(0)
v_mfma_f32_16x16x32_bf16 a[16:19],a[160:163],a[224:227],a[16:19]
s_barrier
ds_read_b128 a[192:195],v20
v_mfma_f32_16x16x32_bf16 a[16:19],a[164:167],a[228:231],a[16:19]
ds_read_b128 a[196:199],v21
v_mfma_f32_16x16x32_bf16 a[48:51],a[164:167],a[236:239],a[48:51]
ds_read_b128 a[200:203],v20 offset:2048
v_mfma_f32_16x16x32_bf16 a[48:51],a[160:163],a[232:235],a[48:51]
ds_read_b128 a[204:207],v21 offset:2048
v_mfma_f32_16x16x32_bf16 a[80:83],a[160:163],a[240:243],a[80:83]
ds_read_b128 a[208:211],v20 offset:4096
v_mfma_f32_16x16x32_bf16 a[80:83],a[164:167],a[244:247],a[80:83]
ds_read_b128 a[212:215],v21 offset:4096
v_mfma_f32_16x16x32_bf16 a[112:115],a[164:167],a[252:255],a[112:115]
buffer_load_dword v26,v24,s[88:91],s93 offen
v_mfma_f32_16x16x32_bf16 a[112:115],a[160:163],a[248:251],a[112:115]
ds_read_b128 a[216:219],v20 offset:6144
v_mfma_f32_16x16x32_bf16 a[20:23],a[168:171],a[224:227],a[20:23]
ds_read_b128 a[220:223],v21 offset:6144
v_mfma_f32_16x16x32_bf16 a[20:23],a[172:175],a[228:231],a[20:23]
ds_read_b64_tr_b16 a[128:129],v22 offset:1024
v_mfma_f32_16x16x32_bf16 a[52:55],a[172:175],a[236:239],a[52:55]
ds_read_b64_tr_b16 a[130:131],v22 offset:3072
v_mfma_f32_16x16x32_bf16 a[52:55],a[168:171],a[232:235],a[52:55]
ds_read_b64_tr_b16 a[132:133],v22 offset:17408
v_mfma_f32_16x16x32_bf16 a[84:87],a[168:171],a[240:243],a[84:87]
ds_read_b64_tr_b16 a[134:135],v22 offset:19456
v_mfma_f32_16x16x32_bf16 a[84:87],a[172:175],a[244:247],a[84:87]
ds_read_b64_tr_b16 a[136:137],v22 offset:1032
v_mfma_f32_16x16x32_bf16 a[116:119],a[172:175],a[252:255],a[116:119]
ds_read_b64_tr_b16 a[138:139],v22 offset:3080
v_mfma_f32_16x16x32_bf16 a[116:119],a[168:171],a[248:251],a[116:119]
ds_read_b64_tr_b16 a[140:141],v22 offset:17416
v_mfma_f32_16x16x32_bf16 a[24:27],a[176:179],a[224:227],a[24:27]
ds_read_b64_tr_b16 a[142:143],v22 offset:19464
v_mfma_f32_16x16x32_bf16 a[24:27],a[180:183],a[228:231],a[24:27]
ds_read_b64_tr_b16 a[144:145],v22 offset:1280
v_mfma_f32_16x16x32_bf16 a[56:59],a[180:183],a[236:239],a[56:59]
ds_read_b64_tr_b16 a[146:147],v22 offset:3328
v_mfma_f32_16x16x32_bf16 a[56:59],a[176:179],a[232:235],a[56:59]
ds_read_b64_tr_b16 a[148:149],v22 offset:17664
v_mfma_f32_16x16x32_bf16 a[88:91],a[176:179],a[240:243],a[88:91]
ds_read_b64_tr_b16 a[150:151],v22 offset:19712
v_mfma_f32_16x16x32_bf16 a[88:91],a[180:183],a[244:247],a[88:91]
ds_read_b64_tr_b16 a[152:153],v22 offset:1288
v_mfma_f32_16x16x32_bf16 a[120:123],a[180:183],a[252:255],a[120:123]
ds_read_b64_tr_b16 a[154:155],v22 offset:3336
v_mfma_f32_16x16x32_bf16 a[120:123],a[176:179],a[248:251],a[120:123]
ds_read_b64_tr_b16 a[156:157],v22 offset:17672
v_mfma_f32_16x16x32_bf16 a[28:31],a[184:187],a[224:227],a[28:31]
ds_read_b64_tr_b16 a[158:159],v22 offset:19720
v_mfma_f32_16x16x32_bf16 a[28:31],a[188:191],a[228:231],a[28:31]
v_mfma_f32_16x16x32_bf16 a[60:63],a[188:191],a[236:239],a[60:63]
v_mfma_f32_16x16x32_bf16 a[60:63],a[184:187],a[232:235],a[60:63]
v_mfma_f32_16x16x32_bf16 a[92:95],a[184:187],a[240:243],a[92:95]
v_mfma_f32_16x16x32_bf16 a[92:95],a[188:191],a[244:247],a[92:95]
v_mfma_f32_16x16x32_bf16 a[124:127],a[188:191],a[252:255],a[124:127]
s_cmp_lg_u32 s53,0
v_mfma_f32_16x16x32_bf16 a[124:127],a[184:187],a[248:251],a[124:127]
s_cbranch_scc1 L_qp_%=
L_qpx_%=:
L_tailseg_%=:
s_waitcnt lgkmcnt(1)
v_mfma_f32_16x16x32_bf16 v[128:131],a[128:131],a[192:195],v[128:131]
s_add_u32 m0,98304,s51
ds_read_b64_tr_b16 a[160:161],v22 offset:1536
v_xor_b32 v20,131072,v20
v_mfma_f32_16x16x32_bf16 v[128:131],a[132:135],a[196:199],v[128:131]
s_add_u32 s54,s54,16384
buffer_load_dwordx4 v4,s[20:23],s47 offen nt lds
v_mfma_f32_16x16x32_bf16 v[136:139],a[132:135],a[204:207],v[136:139]
s_and_b32 s46,s54,0xfffc0000
ds_read_b64_tr_b16 a[162:163],v22 offset:3584
v_xor_b32 v21,131072,v21
v_mfma_f32_16x16x32_bf16 v[136:139],a[128:131],a[200:203],v[136:139]
s_add_u32 s54,s54,16384
ds_read_b64_tr_b16 a[164:165],v22 offset:17920
v_and_b32 v18,48,v6
v_mfma_f32_16x16x32_bf16 v[144:147],a[128:131],a[208:211],v[144:147]
s_mov_b32 s48,-98304
ds_read_b64_tr_b16 a[166:167],v22 offset:19968
v_lshrrev_b32 v19,4,v6
v_mfma_f32_16x16x32_bf16 v[144:147],a[132:135],a[212:215],v[144:147]
s_lshr_b32 s79,%1,3
ds_read_b64_tr_b16 a[168:169],v22 offset:1544
v_lshlrev_b32 v18,2,v18
v_mfma_f32_16x16x32_bf16 v[152:155],a[132:135],a[220:223],v[152:155]
s_mul_i32 s79,s79,7
ds_read_b64_tr_b16 a[170:171],v22 offset:3592
v_and_b32 v19,48,v19
v_mfma_f32_16x16x32_bf16 v[152:155],a[128:131],a[216:219],v[152:155]
s_mov_b32 s60,138
ds_read_b64_tr_b16 a[172:173],v22 offset:17928
v_add_u32 v18,v18,v19
v_mfma_f32_16x16x32_bf16 v[132:135],a[136:139],a[192:195],v[132:135]
s_lshr_b32 s79,s79,5
ds_read_b64_tr_b16 a[174:175],v22 offset:19976
v_add_u32 v18,98304,v18
v_mfma_f32_16x16x32_bf16 v[132:135],a[140:143],a[196:199],v[132:135]
s_sub_u32 s60,s60,s79
s_mul_i32 s83,s41,5
ds_read_b64_tr_b16 a[176:177],v22 offset:1792
v_mfma_f32_16x16x32_bf16 v[140:143],a[140:143],a[204:207],v[140:143]
s_lshl_b32 s44,s40,4
s_add_u32 s83,s83,s42
ds_read_b64_tr_b16 a[178:179],v22 offset:3840
v_mfma_f32_16x16x32_bf16 v[140:143],a[136:139],a[200:203],v[140:143]
s_and_b32 s44,s44,32
ds_read_b64_tr_b16 a[180:181],v22 offset:18176
v_subrev_u32 v25,s51,v4
v_mfma_f32_16x16x32_bf16 v[148:151],a[136:139],a[208:211],v[148:151]
s_add_u32 s83,s83,15
s_lshl_b32 s80,s40,10
ds_read_b64_tr_b16 a[182:183],v22 offset:20224
v_mfma_f32_16x16x32_bf16 v[148:151],a[140:143],a[212:215],v[148:151]
s_and_b32 s83,s83,15
ds_read_b64_tr_b16 a[184:185],v22 offset:1800
v_xor_b32 v25,s44,v25
v_mfma_f32_16x16x32_bf16 v[156:159],a[140:143],a[220:223],v[156:159]
s_add_u32 s83,s83,16
ds_read_b64_tr_b16 a[186:187],v22 offset:3848
v_add_u32 v25,s80,v25
v_mfma_f32_16x16x32_bf16 v[156:159],a[136:139],a[216:219],v[156:159]
s_add_u32 s93,s92,L_att0x_%=-L_pc_%=
s_mov_b32 s50,32768
ds_read_b64_tr_b16 a[188:189],v22 offset:18184
v_mfma_f32_16x16x32_bf16 v[160:163],a[144:147],a[192:195],v[160:163]
s_mov_b32 s49,98304
s_mov_b32 s45,0x1249
ds_read_b64_tr_b16 a[190:191],v22 offset:20232
v_mfma_f32_16x16x32_bf16 v[160:163],a[148:151],a[196:199],v[160:163]
s_add_u32 s80,s80,131072
buffer_load_dwordx4 v4,s[20:23],s47 offen offset:1024 nt lds
v_mfma_f32_16x16x32_bf16 v[168:171],a[148:151],a[204:207],v[168:171]
s_lshl_b32 s83,s83,14
s_waitcnt vmcnt(4)
ds_read_b128 a[224:227],v20
v_mfma_f32_16x16x32_bf16 v[168:171],a[144:147],a[200:203],v[168:171]
ds_read_b64_tr_b16 a[128:129],v22
v_mfma_f32_16x16x32_bf16 v[176:179],a[144:147],a[208:211],v[176:179]
ds_read_b64_tr_b16 a[130:131],v22 offset:2048
v_mfma_f32_16x16x32_bf16 v[176:179],a[148:151],a[212:215],v[176:179]
ds_read_b64_tr_b16 a[132:133],v22 offset:16384
v_mfma_f32_16x16x32_bf16 v[184:187],a[148:151],a[220:223],v[184:187]
ds_read_b64_tr_b16 a[134:135],v22 offset:18432
v_mfma_f32_16x16x32_bf16 v[184:187],a[144:147],a[216:219],v[184:187]
ds_read_b64_tr_b16 a[144:145],v22 offset:8
v_mfma_f32_16x16x32_bf16 v[164:167],a[152:155],a[192:195],v[164:167]
s_waitcnt lgkmcnt(6)
ds_read_b64_tr_b16 a[146:147],v22 offset:2056
v_mfma_f32_16x16x32_bf16 v[164:167],a[156:159],a[196:199],v[164:167]
ds_read_b64_tr_b16 a[148:149],v22 offset:16392
v_mfma_f32_16x16x32_bf16 v[172:175],a[156:159],a[204:207],v[172:175]
ds_read_b64_tr_b16 a[150:151],v22 offset:18440
v_mfma_f32_16x16x32_bf16 v[172:175],a[152:155],a[200:203],v[172:175]
v_readfirstlane_b32 s68,v13
s_nop 1
s_add_u32 s68,s68,1
v_mfma_f32_16x16x32_bf16 v[180:183],a[152:155],a[208:211],v[180:183]
s_xor_b32 s69,s68,0x9e3779b9
ds_read_b128 a[228:231],v21
v_mfma_f32_16x16x32_bf16 v[180:183],a[156:159],a[212:215],v[180:183]
v_mfma_f32_16x16x32_bf16 v[188:191],a[156:159],a[220:223],v[188:191]
ds_read_b128 a[232:235],v20 offset:2048
v_mfma_f32_16x16x32_bf16 v[188:191],a[152:155],a[216:219],v[188:191]
v_mfma_f32_16x16x32_bf16 v[192:195],a[160:163],a[192:195],v[192:195]
ds_read_b128 a[236:239],v21 offset:2048
ds_read_b128 a[240:243],v20 offset:4096
ds_read_b128 a[244:247],v21 offset:4096
ds_read_b128 a[248:251],v20 offset:6144
ds_read_b128 a[252:255],v21 offset:6144
v_mfma_f32_16x16x32_bf16 v[192:195],a[164:167],a[196:199],v[192:195]
v_mfma_f32_16x16x32_bf16 v[200:203],a[164:167],a[204:207],v[200:203]
v_mfma_f32_16x16x32_bf16 v[200:203],a[160:163],a[200:203],v[200:203]
v_mfma_f32_16x16x32_bf16 v[208:211],a[160:163],a[208:211],v[208:211]
buffer_load_dwordx4 v5,s[20:23],s47 offen offset:2048 nt lds
v_mfma_f32_16x16x32_bf16 v[208:211],a[164:167],a[212:215],v[208:211]
v_mfma_f32_16x16x32_bf16 v[216:219],a[164:167],a[220:223],v[216:219]
s_waitcnt vmcnt(4) lgkmcnt(0)
v_mfma_f32_16x16x32_bf16 v[216:219],a[160:163],a[216:219],v[216:219]
s_barrier
ds_read_b64_tr_b16 a[136:137],v22 offset:32768
v_mfma_f32_16x16x32_bf16 v[196:199],a[168:171],a[192:195],v[196:199]
ds_read_b64_tr_b16 a[138:139],v22 offset:34816
v_mfma_f32_16x16x32_bf16 v[196:199],a[172:175],a[196:199],v[196:199]
ds_read_b64_tr_b16 a[140:141],v22 offset:49152
v_mfma_f32_16x16x32_bf16 v[204:207],a[172:175],a[204:207],v[204:207]
ds_read_b64_tr_b16 a[142:143],v22 offset:51200
v_mfma_f32_16x16x32_bf16 v[204:207],a[168:171],a[200:203],v[204:207]
ds_read_b64_tr_b16 a[152:153],v22 offset:32776
v_mfma_f32_16x16x32_bf16 v[212:215],a[168:171],a[208:211],v[212:215]
ds_read_b64_tr_b16 a[154:155],v22 offset:34824
v_mfma_f32_16x16x32_bf16 v[212:215],a[172:175],a[212:215],v[212:215]
ds_read_b64_tr_b16 a[156:157],v22 offset:49160
v_mfma_f32_16x16x32_bf16 v[220:223],a[172:175],a[220:223],v[220:223]
ds_read_b64_tr_b16 a[158:159],v22 offset:51208
v_mfma_f32_16x16x32_bf16 v[220:223],a[168:171],a[216:219],v[220:223]
ds_read_b64_tr_b16 a[160:161],v22 offset:256
v_mfma_f32_16x16x32_bf16 v[224:227],a[176:179],a[192:195],v[224:227]
buffer_load_dwordx4 v5,s[20:23],s47 offen offset:3072 nt lds
v_mfma_f32_16x16x32_bf16 v[224:227],a[180:183],a[196:199],v[224:227]
ds_read_b64_tr_b16 a[162:163],v22 offset:2304
v_mfma_f32_16x16x32_bf16 v[232:235],a[180:183],a[204:207],v[232:235]
ds_read_b64_tr_b16 a[164:165],v22 offset:16640
v_mfma_f32_16x16x32_bf16 v[232:235],a[176:179],a[200:203],v[232:235]
ds_read_b64_tr_b16 a[166:167],v22 offset:18688
v_mfma_f32_16x16x32_bf16 v[240:243],a[176:179],a[208:211],v[240:243]
ds_read_b64_tr_b16 a[168:169],v22 offset:33024
v_mfma_f32_16x16x32_bf16 v[240:243],a[180:183],a[212:215],v[240:243]
ds_read_b64_tr_b16 a[170:171],v22 offset:35072
v_mfma_f32_16x16x32_bf16 v[248:251],a[180:183],a[220:223],v[248:251]
ds_read_b64_tr_b16 a[172:173],v22 offset:49408
v_mfma_f32_16x16x32_bf16 v[248:251],a[176:179],a[216:219],v[248:251]
ds_read_b64_tr_b16 a[174:175],v22 offset:51456
v_mfma_f32_16x16x32_bf16 v[228:231],a[184:187],a[192:195],v[228:231]
ds_read_b64_tr_b16 a[176:177],v22 offset:264
v_mfma_f32_16x16x32_bf16 v[228:231],a[188:191],a[196:199],v[228:231]
ds_read_b64_tr_b16 a[178:179],v22 offset:2312
v_mfma_f32_16x16x32_bf16 v[236:239],a[188:191],a[204:207],v[236:239]
ds_read_b64_tr_b16 a[180:181],v22 offset:16648
v_mfma_f32_16x16x32_bf16 v[236:239],a[184:187],a[200:203],v[236:239]
ds_read_b64_tr_b16 a[182:183],v22 offset:18696
v_mfma_f32_16x16x32_bf16 v[244:247],a[184:187],a[208:211],v[244:247]
v_mfma_f32_16x16x32_bf16 v[244:247],a[188:191],a[212:215],v[244:247]
s_add_u32 m0,98304,s52
v_mfma_f32_16x16x32_bf16 v[252:255],a[188:191],a[220:223],v[252:255]
ds_read_b64_tr_b16 a[188:189],v22 offset:49416
v_mfma_f32_16x16x32_bf16 v[252:255],a[184:187],a[216:219],v[252:255]
)A"
R"A(ds_read_b64_tr_b16 a[184:185],v22 offset:33032
v_mfma_f32_16x16x32_bf16 a[0:3],a[128:131],a[192:195],a[0:3]
buffer_load_dwordx4 v4,s[24:27],s47 offen nt lds
v_mfma_f32_16x16x32_bf16 a[0:3],a[132:135],a[196:199],a[0:3]
s_waitcnt lgkmcnt(4)
ds_read_b64_tr_b16 a[186:187],v22 offset:35080
v_mfma_f32_16x16x32_bf16 a[0:3],a[136:139],a[224:227],a[0:3]
ds_read_b64_tr_b16 a[190:191],v22 offset:51464
v_mfma_f32_16x16x32_bf16 a[0:3],a[140:143],a[228:231],a[0:3]
v_mfma_f32_16x16x32_bf16 a[32:35],a[140:143],a[236:239],a[32:35]
v_mfma_f32_16x16x32_bf16 a[32:35],a[136:139],a[232:235],a[32:35]
v_mfma_f32_16x16x32_bf16 a[32:35],a[132:135],a[204:207],a[32:35]
v_mfma_f32_16x16x32_bf16 a[32:35],a[128:131],a[200:203],a[32:35]
v_mfma_f32_16x16x32_bf16 a[64:67],a[128:131],a[208:211],a[64:67]
v_mfma_f32_16x16x32_bf16 a[64:67],a[132:135],a[212:215],a[64:67]
s_nop 1
v_accvgpr_read_b32 v64,a0
v_accvgpr_read_b32 v65,a1
v_mfma_f32_16x16x32_bf16 a[64:67],a[136:139],a[240:243],a[64:67]
v_mul_f32 v64,s61,v64
v_mul_f32 v65,s61,v65
v_mfma_f32_16x16x32_bf16 a[64:67],a[140:143],a[244:247],a[64:67]
v_accvgpr_read_b32 v66,a2
v_accvgpr_read_b32 v67,a3
v_mfma_f32_16x16x32_bf16 a[96:99],a[140:143],a[252:255],a[96:99]
ds_read_b64_tr_b16 a[140:141],v22 offset:49664
v_cvt_pk_bf16_f32 v64,v64,v65
v_mfma_f32_16x16x32_bf16 a[96:99],a[136:139],a[248:251],a[96:99]
ds_read_b64_tr_b16 a[136:137],v22 offset:33280
v_accvgpr_read_b32 v80,a32
v_mfma_f32_16x16x32_bf16 a[96:99],a[132:135],a[220:223],a[96:99]
ds_read_b64_tr_b16 a[132:133],v22 offset:16896
v_accvgpr_read_b32 v81,a33
v_mfma_f32_16x16x32_bf16 a[96:99],a[128:131],a[216:219],a[96:99]
ds_read_b64_tr_b16 a[128:129],v22 offset:512
v_mul_f32 v80,s61,v80
v_mfma_f32_16x16x32_bf16 a[4:7],a[144:147],a[192:195],a[4:7]
buffer_load_dwordx4 v5,s[24:27],s47 offen offset:1024 nt lds
v_mfma_f32_16x16x32_bf16 a[4:7],a[148:151],a[196:199],a[4:7]
ds_read_b64_tr_b16 a[130:131],v22 offset:2560
v_mul_f32 v81,s61,v81
v_mfma_f32_16x16x32_bf16 a[4:7],a[152:155],a[224:227],a[4:7]
ds_read_b64_tr_b16 a[134:135],v22 offset:18944
v_accvgpr_read_b32 v82,a34
v_mfma_f32_16x16x32_bf16 a[4:7],a[156:159],a[228:231],a[4:7]
ds_read_b64_tr_b16 a[138:139],v22 offset:35328
v_accvgpr_read_b32 v83,a35
v_mfma_f32_16x16x32_bf16 a[36:39],a[156:159],a[236:239],a[36:39]
ds_read_b64_tr_b16 a[142:143],v22 offset:51712
v_mul_f32 v66,s61,v66
v_mfma_f32_16x16x32_bf16 a[36:39],a[152:155],a[232:235],a[36:39]
v_mul_f32 v67,s61,v67
v_cvt_pk_bf16_f32 v80,v80,v81
v_mfma_f32_16x16x32_bf16 a[36:39],a[148:151],a[204:207],a[36:39]
v_mul_f32 v82,s61,v82
v_mul_f32 v83,s61,v83
v_mfma_f32_16x16x32_bf16 a[36:39],a[144:147],a[200:203],a[36:39]
v_cvt_pk_bf16_f32 v65,v66,v67
v_cvt_pk_bf16_f32 v81,v82,v83
v_mfma_f32_16x16x32_bf16 a[68:71],a[144:147],a[208:211],a[68:71]
v_accvgpr_read_b32 v96,a64
v_accvgpr_read_b32 v97,a65
v_mfma_f32_16x16x32_bf16 a[68:71],a[148:151],a[212:215],a[68:71]
v_accvgpr_read_b32 v112,a96
v_accvgpr_read_b32 v113,a97
v_mfma_f32_16x16x32_bf16 a[68:71],a[152:155],a[240:243],a[68:71]
v_accvgpr_read_b32 v66,a4
v_accvgpr_read_b32 v67,a5
v_mfma_f32_16x16x32_bf16 a[68:71],a[156:159],a[244:247],a[68:71]
v_mul_f32 v96,s61,v96
v_mul_f32 v97,s61,v97
v_mfma_f32_16x16x32_bf16 a[100:103],a[156:159],a[252:255],a[100:103]
ds_read_b64_tr_b16 a[156:157],v22 offset:49672
v_accvgpr_read_b32 v98,a66
v_mfma_f32_16x16x32_bf16 a[100:103],a[152:155],a[248:251],a[100:103]
ds_read_b64_tr_b16 a[152:153],v22 offset:33288
v_accvgpr_read_b32 v82,a36
v_mfma_f32_16x16x32_bf16 a[100:103],a[148:151],a[220:223],a[100:103]
ds_read_b64_tr_b16 a[148:149],v22 offset:16904
v_accvgpr_read_b32 v83,a37
v_mfma_f32_16x16x32_bf16 a[100:103],a[144:147],a[216:219],a[100:103]
ds_read_b64_tr_b16 a[144:145],v22 offset:520
v_accvgpr_read_b32 v99,a67
v_mfma_f32_16x16x32_bf16 a[8:11],a[160:163],a[192:195],a[8:11]
buffer_load_dwordx4 v4,s[24:27],s47 offen offset:2048 nt lds
v_mfma_f32_16x16x32_bf16 a[8:11],a[164:167],a[196:199],a[8:11]
ds_read_b64_tr_b16 a[146:147],v22 offset:2568
v_mul_f32 v112,s61,v112
v_mfma_f32_16x16x32_bf16 a[8:11],a[168:171],a[224:227],a[8:11]
ds_read_b64_tr_b16 a[150:151],v22 offset:18952
v_mul_f32 v113,s61,v113
v_mfma_f32_16x16x32_bf16 a[8:11],a[172:175],a[228:231],a[8:11]
ds_read_b64_tr_b16 a[154:155],v22 offset:35336
v_accvgpr_read_b32 v114,a98
v_mfma_f32_16x16x32_bf16 a[40:43],a[172:175],a[236:239],a[40:43]
ds_read_b64_tr_b16 a[158:159],v22 offset:51720
v_accvgpr_read_b32 v115,a99
v_mfma_f32_16x16x32_bf16 a[40:43],a[168:171],a[232:235],a[40:43]
v_accvgpr_read_b32 v12,a6
v_accvgpr_read_b32 v13,a7
v_mfma_f32_16x16x32_bf16 a[40:43],a[164:167],a[204:207],a[40:43]
v_mul_f32 v66,s61,v66
v_mul_f32 v67,s61,v67
v_mfma_f32_16x16x32_bf16 a[40:43],a[160:163],a[200:203],a[40:43]
v_accvgpr_read_b32 v14,a38
v_accvgpr_read_b32 v15,a39
v_mfma_f32_16x16x32_bf16 a[72:75],a[160:163],a[208:211],a[72:75]
v_mul_f32 v82,s61,v82
v_mul_f32 v83,s61,v83
v_mfma_f32_16x16x32_bf16 a[72:75],a[164:167],a[212:215],a[72:75]
v_accvgpr_read_b32 v68,a8
v_accvgpr_read_b32 v69,a9
v_mfma_f32_16x16x32_bf16 a[72:75],a[168:171],a[240:243],a[72:75]
v_mul_f32 v68,s61,v68
v_mul_f32 v69,s61,v69
v_mfma_f32_16x16x32_bf16 a[72:75],a[172:175],a[244:247],a[72:75]
v_accvgpr_read_b32 v70,a10
v_accvgpr_read_b32 v71,a11
v_mfma_f32_16x16x32_bf16 a[104:107],a[172:175],a[252:255],a[104:107]
ds_read_b64_tr_b16 a[172:173],v22 offset:49920
v_cvt_pk_bf16_f32 v96,v96,v97
v_mfma_f32_16x16x32_bf16 a[104:107],a[168:171],a[248:251],a[104:107]
ds_read_b64_tr_b16 a[168:169],v22 offset:33536
v_mul_f32 v98,s61,v98
v_mfma_f32_16x16x32_bf16 a[104:107],a[164:167],a[220:223],a[104:107]
ds_read_b64_tr_b16 a[164:165],v22 offset:17152
v_mul_f32 v99,s61,v99
v_mfma_f32_16x16x32_bf16 a[104:107],a[160:163],a[216:219],a[104:107]
ds_read_b64_tr_b16 a[160:161],v22 offset:768
v_cvt_pk_bf16_f32 v112,v112,v113
v_mfma_f32_16x16x32_bf16 a[12:15],a[176:179],a[192:195],a[12:15]
s_waitcnt lgkmcnt(4)
buffer_load_dwordx4 v5,s[24:27],s47 offen offset:3072 nt lds
v_mfma_f32_16x16x32_bf16 a[12:15],a[180:183],a[196:199],a[12:15]
s_add_u32 s47,s47,16384
s_and_b32 s47,s47,0x3ffff
s_or_b32 s47,s47,s46
v_mfma_f32_16x16x32_bf16 a[12:15],a[184:187],a[224:227],a[12:15]
s_and_b32 s46,s54,0xfffc0000
ds_read_b64_tr_b16 a[162:163],v22 offset:2816
v_mul_f32 v114,s61,v114
v_mfma_f32_16x16x32_bf16 a[12:15],a[188:191],a[228:231],a[12:15]
s_add_u32 s54,s54,16384
ds_read_b64_tr_b16 a[166:167],v22 offset:19200
v_mul_f32 v115,s61,v115
v_mfma_f32_16x16x32_bf16 a[44:47],a[188:191],a[236:239],a[44:47]
ds_read_b64_tr_b16 a[170:171],v22 offset:35584
v_cvt_pk_bf16_f32 v68,v68,v69
v_mfma_f32_16x16x32_bf16 a[44:47],a[184:187],a[232:235],a[44:47]
ds_read_b64_tr_b16 a[174:175],v22 offset:51968
v_mul_f32 v70,s61,v70
v_mfma_f32_16x16x32_bf16 a[44:47],a[180:183],a[204:207],a[44:47]
v_mul_f32 v71,s61,v71
v_mul_f32 v12,s61,v12
v_mfma_f32_16x16x32_bf16 a[44:47],a[176:179],a[200:203],a[44:47]
v_mul_f32 v13,s61,v13
v_cvt_pk_bf16_f32 v66,v66,v67
v_mfma_f32_16x16x32_bf16 a[76:79],a[176:179],a[208:211],a[76:79]
v_mul_f32 v14,s61,v14
v_mul_f32 v15,s61,v15
v_mfma_f32_16x16x32_bf16 a[76:79],a[180:183],a[212:215],a[76:79]
v_cvt_pk_bf16_f32 v82,v82,v83
v_cvt_pk_bf16_f32 v97,v98,v99
v_mfma_f32_16x16x32_bf16 a[76:79],a[184:187],a[240:243],a[76:79]
v_cvt_pk_bf16_f32 v113,v114,v115
v_cvt_pk_bf16_f32 v69,v70,v71
v_mfma_f32_16x16x32_bf16 a[76:79],a[188:191],a[244:247],a[76:79]
v_accvgpr_read_b32 v84,a40
v_accvgpr_read_b32 v85,a41
v_mfma_f32_16x16x32_bf16 a[108:111],a[188:191],a[252:255],a[108:111]
ds_read_b64_tr_b16 a[188:189],v22 offset:49928
v_accvgpr_read_b32 v100,a72
v_mfma_f32_16x16x32_bf16 a[108:111],a[184:187],a[248:251],a[108:111]
ds_read_b64_tr_b16 a[184:185],v22 offset:33544
v_accvgpr_read_b32 v101,a73
v_mfma_f32_16x16x32_bf16 a[108:111],a[180:183],a[220:223],a[108:111]
ds_read_b64_tr_b16 a[180:181],v22 offset:17160
v_accvgpr_read_b32 v116,a104
v_mfma_f32_16x16x32_bf16 a[108:111],a[176:179],a[216:219],a[108:111]
ds_read_b64_tr_b16 a[176:177],v22 offset:776
v_accvgpr_read_b32 v117,a105
v_mfma_f32_16x16x32_bf16 a[16:19],a[128:131],a[192:195],a[16:19]
ds_read_b64_tr_b16 a[178:179],v22 offset:2824
v_cvt_pk_bf16_f32 v67,v12,v13
v_mfma_f32_16x16x32_bf16 a[16:19],a[132:135],a[196:199],a[16:19]
ds_read_b64_tr_b16 a[182:183],v22 offset:19208
v_cvt_pk_bf16_f32 v83,v14,v15
v_mfma_f32_16x16x32_bf16 a[16:19],a[136:139],a[224:227],a[16:19]
ds_read_b64_tr_b16 a[186:187],v22 offset:35592
v_accvgpr_read_b32 v98,a68
v_mfma_f32_16x16x32_bf16 a[16:19],a[140:143],a[228:231],a[16:19]
s_add_u32 m0,0,s51
ds_read_b64_tr_b16 a[190:191],v22 offset:51976
v_accvgpr_read_b32 v99,a69
v_mfma_f32_16x16x32_bf16 a[48:51],a[140:143],a[236:239],a[48:51]
v_accvgpr_read_b32 v114,a100
v_accvgpr_read_b32 v115,a101
v_mfma_f32_16x16x32_bf16 a[48:51],a[136:139],a[232:235],a[48:51]
v_accvgpr_read_b32 v70,a12
v_accvgpr_read_b32 v71,a13
v_mfma_f32_16x16x32_bf16 a[48:51],a[132:135],a[204:207],a[48:51]
buffer_load_dwordx4 v4,s[20:23],s47 offen nt lds
v_mfma_f32_16x16x32_bf16 a[48:51],a[128:131],a[200:203],a[48:51]
v_mul_f32 v84,s61,v84
v_mul_f32 v85,s61,v85
v_mfma_f32_16x16x32_bf16 a[80:83],a[128:131],a[208:211],a[80:83]
v_accvgpr_read_b32 v86,a42
v_accvgpr_read_b32 v87,a43
v_mfma_f32_16x16x32_bf16 a[80:83],a[132:135],a[212:215],a[80:83]
v_mul_f32 v100,s61,v100
v_mul_f32 v101,s61,v101
v_mfma_f32_16x16x32_bf16 a[80:83],a[136:139],a[240:243],a[80:83]
v_accvgpr_read_b32 v102,a74
v_accvgpr_read_b32 v103,a75
v_mfma_f32_16x16x32_bf16 a[80:83],a[140:143],a[244:247],a[80:83]
v_mul_f32 v116,s61,v116
v_mul_f32 v117,s61,v117
v_mfma_f32_16x16x32_bf16 a[112:115],a[140:143],a[252:255],a[112:115]
ds_read_b64_tr_b16 a[140:141],v22 offset:50184
v_accvgpr_read_b32 v118,a106
v_mfma_f32_16x16x32_bf16 a[112:115],a[136:139],a[248:251],a[112:115]
ds_read_b64_tr_b16 a[136:137],v22 offset:33800
v_accvgpr_read_b32 v119,a107
v_mfma_f32_16x16x32_bf16 a[112:115],a[132:135],a[220:223],a[112:115]
ds_read_b64_tr_b16 a[132:133],v22 offset:50176
v_accvgpr_read_b32 v16,a70
v_mfma_f32_16x16x32_bf16 a[112:115],a[128:131],a[216:219],a[112:115]
ds_read_b64_tr_b16 a[128:129],v22 offset:33792
v_accvgpr_read_b32 v17,a71
v_mfma_f32_16x16x32_bf16 a[20:23],a[144:147],a[192:195],a[20:23]
ds_read_b64_tr_b16 a[130:131],v22 offset:35840
v_mul_f32 v98,s61,v98
v_mfma_f32_16x16x32_bf16 a[20:23],a[148:151],a[196:199],a[20:23]
ds_read_b64_tr_b16 a[134:135],v22 offset:52224
v_mul_f32 v99,s61,v99
v_mfma_f32_16x16x32_bf16 a[20:23],a[152:155],a[224:227],a[20:23]
ds_read_b64_tr_b16 a[138:139],v22 offset:35848
v_accvgpr_read_b32 v12,a102
v_mfma_f32_16x16x32_bf16 a[20:23],a[156:159],a[228:231],a[20:23]
ds_read_b64_tr_b16 a[142:143],v22 offset:52232
v_accvgpr_read_b32 v13,a103
v_mfma_f32_16x16x32_bf16 a[52:55],a[156:159],a[236:239],a[52:55]
v_mul_f32 v114,s61,v114
v_mul_f32 v115,s61,v115
v_mfma_f32_16x16x32_bf16 a[52:55],a[152:155],a[232:235],a[52:55]
v_accvgpr_read_b32 v14,a14
v_accvgpr_read_b32 v15,a15
v_mfma_f32_16x16x32_bf16 a[52:55],a[148:151],a[204:207],a[52:55]
buffer_load_dwordx4 v4,s[20:23],s47 offen offset:1024 nt lds
v_mfma_f32_16x16x32_bf16 a[52:55],a[144:147],a[200:203],a[52:55]
v_mul_f32 v70,s61,v70
v_mul_f32 v71,s61,v71
v_mfma_f32_16x16x32_bf16 a[84:87],a[144:147],a[208:211],a[84:87]
v_cvt_pk_bf16_f32 v84,v84,v85
v_mul_f32 v86,s61,v86
v_mfma_f32_16x16x32_bf16 a[84:87],a[148:151],a[212:215],a[84:87]
v_mul_f32 v87,s61,v87
v_cvt_pk_bf16_f32 v100,v100,v101
v_mfma_f32_16x16x32_bf16 a[84:87],a[152:155],a[240:243],a[84:87]
v_mul_f32 v102,s61,v102
v_mul_f32 v103,s61,v103
v_mfma_f32_16x16x32_bf16 a[84:87],a[156:159],a[244:247],a[84:87]
v_cvt_pk_bf16_f32 v116,v116,v117
v_mul_f32 v118,s61,v118
v_mfma_f32_16x16x32_bf16 a[116:119],a[156:159],a[252:255],a[116:119]
ds_read_b64_tr_b16 a[156:157],v22 offset:50440
v_mul_f32 v119,s61,v119
v_mfma_f32_16x16x32_bf16 a[116:119],a[152:155],a[248:251],a[116:119]
ds_read_b64_tr_b16 a[152:153],v22 offset:34056
v_mul_f32 v16,s61,v16
v_mfma_f32_16x16x32_bf16 a[116:119],a[148:151],a[220:223],a[116:119]
ds_read_b64_tr_b16 a[148:149],v22 offset:50432
v_mul_f32 v17,s61,v17
v_mfma_f32_16x16x32_bf16 a[116:119],a[144:147],a[216:219],a[116:119]
s_waitcnt lgkmcnt(3)
)A"
R"A(ds_read_b64_tr_b16 a[144:145],v22 offset:34048
v_cvt_pk_bf16_f32 v98,v98,v99
v_mfma_f32_16x16x32_bf16 a[24:27],a[160:163],a[192:195],a[24:27]
ds_read_b64_tr_b16 a[146:147],v22 offset:36096
v_mul_f32 v12,s61,v12
v_mfma_f32_16x16x32_bf16 a[24:27],a[164:167],a[196:199],a[24:27]
ds_read_b64_tr_b16 a[150:151],v22 offset:52480
v_mul_f32 v13,s61,v13
v_mfma_f32_16x16x32_bf16 a[24:27],a[168:171],a[224:227],a[24:27]
ds_read_b64_tr_b16 a[154:155],v22 offset:36104
v_cvt_pk_bf16_f32 v114,v114,v115
v_mfma_f32_16x16x32_bf16 a[24:27],a[172:175],a[228:231],a[24:27]
ds_read_b64_tr_b16 a[158:159],v22 offset:52488
v_mul_f32 v14,s61,v14
v_mfma_f32_16x16x32_bf16 a[56:59],a[172:175],a[236:239],a[56:59]
v_mul_f32 v15,s61,v15
v_cvt_pk_bf16_f32 v70,v70,v71
v_mfma_f32_16x16x32_bf16 a[56:59],a[168:171],a[232:235],a[56:59]
v_cvt_pk_bf16_f32 v85,v86,v87
v_cvt_pk_bf16_f32 v101,v102,v103
v_mfma_f32_16x16x32_bf16 a[56:59],a[164:167],a[204:207],a[56:59]
buffer_load_dwordx4 v5,s[20:23],s47 offen offset:2048 nt lds
v_mfma_f32_16x16x32_bf16 a[56:59],a[160:163],a[200:203],a[56:59]
v_cvt_pk_bf16_f32 v117,v118,v119
v_accvgpr_read_b32 v72,a16
v_mfma_f32_16x16x32_bf16 a[88:91],a[160:163],a[208:211],a[88:91]
v_accvgpr_read_b32 v73,a17
v_accvgpr_read_b32 v88,a48
v_mfma_f32_16x16x32_bf16 a[88:91],a[164:167],a[212:215],a[88:91]
v_accvgpr_read_b32 v89,a49
v_accvgpr_read_b32 v104,a80
v_mfma_f32_16x16x32_bf16 a[88:91],a[168:171],a[240:243],a[88:91]
v_accvgpr_read_b32 v105,a81
v_cvt_pk_bf16_f32 v99,v16,v17
v_mfma_f32_16x16x32_bf16 a[88:91],a[172:175],a[244:247],a[88:91]
v_cvt_pk_bf16_f32 v115,v12,v13
v_cvt_pk_bf16_f32 v71,v14,v15
v_mfma_f32_16x16x32_bf16 a[120:123],a[172:175],a[252:255],a[120:123]
ds_read_b64_tr_b16 a[172:173],v22 offset:50696
v_accvgpr_read_b32 v86,a44
v_mfma_f32_16x16x32_bf16 a[120:123],a[168:171],a[248:251],a[120:123]
ds_read_b64_tr_b16 a[168:169],v22 offset:34312
v_accvgpr_read_b32 v87,a45
v_mfma_f32_16x16x32_bf16 a[120:123],a[164:167],a[220:223],a[120:123]
ds_read_b64_tr_b16 a[164:165],v22 offset:50688
v_accvgpr_read_b32 v102,a76
v_mfma_f32_16x16x32_bf16 a[120:123],a[160:163],a[216:219],a[120:123]
ds_read_b64_tr_b16 a[160:161],v22 offset:34304
v_accvgpr_read_b32 v103,a77
v_mfma_f32_16x16x32_bf16 a[28:31],a[176:179],a[192:195],a[28:31]
ds_read_b64_tr_b16 a[162:163],v22 offset:36352
v_accvgpr_read_b32 v118,a108
v_mfma_f32_16x16x32_bf16 a[28:31],a[180:183],a[196:199],a[28:31]
ds_read_b64_tr_b16 a[166:167],v22 offset:52736
v_accvgpr_read_b32 v119,a109
v_mfma_f32_16x16x32_bf16 a[28:31],a[184:187],a[224:227],a[28:31]
ds_read_b64_tr_b16 a[170:171],v22 offset:36360
v_mul_f32 v72,s61,v72
v_mfma_f32_16x16x32_bf16 a[28:31],a[188:191],a[228:231],a[28:31]
ds_read_b64_tr_b16 a[174:175],v22 offset:52744
v_mul_f32 v73,s61,v73
v_mfma_f32_16x16x32_bf16 a[60:63],a[188:191],a[236:239],a[60:63]
v_accvgpr_read_b32 v74,a18
v_accvgpr_read_b32 v75,a19
v_mfma_f32_16x16x32_bf16 a[60:63],a[184:187],a[232:235],a[60:63]
v_mul_f32 v88,s61,v88
v_mul_f32 v89,s61,v89
v_mfma_f32_16x16x32_bf16 a[60:63],a[180:183],a[204:207],a[60:63]
buffer_load_dwordx4 v5,s[20:23],s47 offen offset:3072 nt lds
v_mfma_f32_16x16x32_bf16 a[60:63],a[176:179],a[200:203],a[60:63]
v_accvgpr_read_b32 v90,a50
v_accvgpr_read_b32 v91,a51
v_mfma_f32_16x16x32_bf16 a[92:95],a[176:179],a[208:211],a[92:95]
v_mul_f32 v104,s61,v104
v_mul_f32 v105,s61,v105
v_mfma_f32_16x16x32_bf16 a[92:95],a[180:183],a[212:215],a[92:95]
v_accvgpr_read_b32 v106,a82
v_accvgpr_read_b32 v107,a83
v_mfma_f32_16x16x32_bf16 a[92:95],a[184:187],a[240:243],a[92:95]
v_accvgpr_read_b32 v16,a46
v_accvgpr_read_b32 v17,a47
v_mfma_f32_16x16x32_bf16 a[92:95],a[188:191],a[244:247],a[92:95]
v_mul_f32 v86,s61,v86
v_mul_f32 v87,s61,v87
v_mfma_f32_16x16x32_bf16 a[124:127],a[188:191],a[252:255],a[124:127]
ds_read_b64_tr_b16 a[188:189],v22 offset:50952
v_accvgpr_read_b32 v12,a78
v_mfma_f32_16x16x32_bf16 a[124:127],a[184:187],a[248:251],a[124:127]
ds_read_b64_tr_b16 a[184:185],v22 offset:34568
v_accvgpr_read_b32 v13,a79
v_mfma_f32_16x16x32_bf16 a[124:127],a[180:183],a[220:223],a[124:127]
ds_read_b64_tr_b16 a[180:181],v22 offset:50944
v_mul_f32 v102,s61,v102
v_mfma_f32_16x16x32_bf16 a[124:127],a[176:179],a[216:219],a[124:127]
ds_read_b64_tr_b16 a[176:177],v22 offset:34560
v_mul_f32 v103,s61,v103
v_mfma_f32_16x16x32_bf16 v[128:131],a[128:131],a[224:227],v[128:131]
ds_read_b64_tr_b16 a[178:179],v22 offset:36608
v_accvgpr_read_b32 v14,a110
v_mfma_f32_16x16x32_bf16 v[128:131],a[132:135],a[228:231],v[128:131]
ds_read_b64_tr_b16 a[182:183],v22 offset:52992
v_accvgpr_read_b32 v15,a111
v_mfma_f32_16x16x32_bf16 v[136:139],a[132:135],a[236:239],v[136:139]
ds_read_b64_tr_b16 a[186:187],v22 offset:36616
v_mul_f32 v118,s61,v118
v_mfma_f32_16x16x32_bf16 v[136:139],a[128:131],a[232:235],v[136:139]
s_add_u32 m0,0,s52
ds_read_b64_tr_b16 a[190:191],v22 offset:53000
v_mul_f32 v119,s61,v119
v_mfma_f32_16x16x32_bf16 v[144:147],a[128:131],a[240:243],v[144:147]
v_cvt_pk_bf16_f32 v72,v72,v73
v_mul_f32 v74,s61,v74
v_mfma_f32_16x16x32_bf16 v[144:147],a[132:135],a[244:247],v[144:147]
v_mul_f32 v75,s61,v75
v_cvt_pk_bf16_f32 v88,v88,v89
v_mfma_f32_16x16x32_bf16 v[152:155],a[132:135],a[252:255],v[152:155]
buffer_load_dwordx4 v4,s[24:27],s47 offen nt lds
v_mfma_f32_16x16x32_bf16 v[152:155],a[128:131],a[248:251],v[152:155]
v_mul_f32 v90,s61,v90
v_mul_f32 v91,s61,v91
v_mfma_f32_16x16x32_bf16 v[132:135],a[136:139],a[224:227],v[132:135]
v_cvt_pk_bf16_f32 v104,v104,v105
v_mul_f32 v106,s61,v106
v_mfma_f32_16x16x32_bf16 v[132:135],a[140:143],a[228:231],v[132:135]
v_mul_f32 v107,s61,v107
v_mul_f32 v16,s61,v16
v_mfma_f32_16x16x32_bf16 v[140:143],a[140:143],a[236:239],v[140:143]
v_mul_f32 v17,s61,v17
v_cvt_pk_bf16_f32 v86,v86,v87
v_mfma_f32_16x16x32_bf16 v[140:143],a[136:139],a[232:235],v[140:143]
v_mul_f32 v12,s61,v12
v_mul_f32 v13,s61,v13
v_mfma_f32_16x16x32_bf16 v[148:151],a[136:139],a[240:243],v[148:151]
v_cvt_pk_bf16_f32 v102,v102,v103
v_mul_f32 v14,s61,v14
v_mfma_f32_16x16x32_bf16 v[148:151],a[140:143],a[244:247],v[148:151]
v_mul_f32 v15,s61,v15
v_cvt_pk_bf16_f32 v118,v118,v119
v_mfma_f32_16x16x32_bf16 v[156:159],a[140:143],a[252:255],v[156:159]
s_waitcnt vmcnt(5) lgkmcnt(0)
v_cvt_pk_bf16_f32 v73,v74,v75
v_cvt_pk_bf16_f32 v89,v90,v91
v_mfma_f32_16x16x32_bf16 v[156:159],a[136:139],a[248:251],v[156:159]
s_barrier
ds_read_b128 v[48:51],v18
v_cvt_pk_bf16_f32 v105,v106,v107
v_mfma_f32_16x16x32_bf16 v[160:163],a[144:147],a[224:227],v[160:163]
v_cvt_pk_bf16_f32 v87,v16,v17
v_cvt_pk_bf16_f32 v103,v12,v13
v_mfma_f32_16x16x32_bf16 v[160:163],a[148:151],a[228:231],v[160:163]
ds_read_b128 v[52:55],v18 offset:256
v_cvt_pk_bf16_f32 v119,v14,v15
v_mfma_f32_16x16x32_bf16 v[168:171],a[148:151],a[236:239],v[168:171]
v_accvgpr_read_b32 v74,a20
v_accvgpr_read_b32 v75,a21
v_mfma_f32_16x16x32_bf16 v[168:171],a[144:147],a[232:235],v[168:171]
ds_read_b128 v[56:59],v18 offset:512
v_accvgpr_read_b32 v90,a52
v_mfma_f32_16x16x32_bf16 v[176:179],a[144:147],a[240:243],v[176:179]
v_accvgpr_read_b32 v91,a53
v_accvgpr_read_b32 v106,a84
v_mfma_f32_16x16x32_bf16 v[176:179],a[148:151],a[244:247],v[176:179]
buffer_load_dwordx4 v5,s[24:27],s47 offen offset:1024 nt lds
v_mfma_f32_16x16x32_bf16 v[184:187],a[148:151],a[252:255],v[184:187]
ds_read_b128 v[60:63],v18 offset:768
v_accvgpr_read_b32 v107,a85
v_mfma_f32_16x16x32_bf16 v[184:187],a[144:147],a[248:251],v[184:187]
v_accvgpr_read_b32 v16,a22
v_accvgpr_read_b32 v17,a23
v_mfma_f32_16x16x32_bf16 v[164:167],a[152:155],a[224:227],v[164:167]
v_mul_f32 v74,s61,v74
v_mul_f32 v75,s61,v75
v_mfma_f32_16x16x32_bf16 v[164:167],a[156:159],a[228:231],v[164:167]
v_accvgpr_read_b32 v12,a54
v_accvgpr_read_b32 v13,a55
v_mfma_f32_16x16x32_bf16 v[172:175],a[156:159],a[236:239],v[172:175]
v_mul_f32 v90,s61,v90
v_mul_f32 v91,s61,v91
v_mfma_f32_16x16x32_bf16 v[172:175],a[152:155],a[232:235],v[172:175]
v_accvgpr_read_b32 v14,a86
v_accvgpr_read_b32 v15,a87
v_mfma_f32_16x16x32_bf16 v[180:183],a[152:155],a[240:243],v[180:183]
v_mul_f32 v106,s61,v106
v_mul_f32 v107,s61,v107
v_mfma_f32_16x16x32_bf16 v[180:183],a[156:159],a[244:247],v[180:183]
v_mul_f32 v16,s61,v16
v_mul_f32 v17,s61,v17
v_mfma_f32_16x16x32_bf16 v[188:191],a[156:159],a[252:255],v[188:191]
v_cvt_pk_bf16_f32 v74,v74,v75
v_mul_f32 v12,s61,v12
v_mfma_f32_16x16x32_bf16 v[188:191],a[152:155],a[248:251],v[188:191]
v_mul_f32 v13,s61,v13
v_cvt_pk_bf16_f32 v90,v90,v91
v_mfma_f32_16x16x32_bf16 v[192:195],a[160:163],a[224:227],v[192:195]
v_mul_f32 v14,s61,v14
v_mul_f32 v15,s61,v15
v_mfma_f32_16x16x32_bf16 v[192:195],a[164:167],a[228:231],v[192:195]
v_cvt_pk_bf16_f32 v106,v106,v107
v_cvt_pk_bf16_f32 v75,v16,v17
v_mfma_f32_16x16x32_bf16 v[200:203],a[164:167],a[236:239],v[200:203]
v_cvt_pk_bf16_f32 v91,v12,v13
v_cvt_pk_bf16_f32 v107,v14,v15
v_mfma_f32_16x16x32_bf16 v[200:203],a[160:163],a[232:235],v[200:203]
v_accvgpr_read_b32 v76,a24
v_accvgpr_read_b32 v77,a25
v_accvgpr_read_b32 v78,a26
v_accvgpr_read_b32 v79,a27
v_accvgpr_read_b32 v12,a28
v_accvgpr_read_b32 v13,a29
v_accvgpr_read_b32 v14,a30
v_accvgpr_read_b32 v15,a31
v_pk_mul_f32 v[76:77],s[60:61],v[76:77] op_sel:[1,0] op_sel_hi:[1,1]
v_pk_mul_f32 v[78:79],s[60:61],v[78:79] op_sel:[1,0] op_sel_hi:[1,1]
v_pk_mul_f32 v[12:13],s[60:61],v[12:13] op_sel:[1,0] op_sel_hi:[1,1]
v_pk_mul_f32 v[14:15],s[60:61],v[14:15] op_sel:[1,0] op_sel_hi:[1,1]
v_cvt_pk_bf16_f32 v76,v76,v77
v_cvt_pk_bf16_f32 v77,v78,v79
v_cvt_pk_bf16_f32 v78,v12,v13
v_cvt_pk_bf16_f32 v79,v14,v15
v_mfma_f32_16x16x32_bf16 v[208:211],a[160:163],a[240:243],v[208:211]
v_accvgpr_read_b32 v92,a56
v_accvgpr_read_b32 v93,a57
v_accvgpr_read_b32 v94,a58
v_accvgpr_read_b32 v95,a59
v_accvgpr_read_b32 v12,a60
v_accvgpr_read_b32 v13,a61
v_accvgpr_read_b32 v14,a62
v_accvgpr_read_b32 v15,a63
v_pk_mul_f32 v[92:93],s[60:61],v[92:93] op_sel:[1,0] op_sel_hi:[1,1]
v_pk_mul_f32 v[94:95],s[60:61],v[94:95] op_sel:[1,0] op_sel_hi:[1,1]
v_pk_mul_f32 v[12:13],s[60:61],v[12:13] op_sel:[1,0] op_sel_hi:[1,1]
v_pk_mul_f32 v[14:15],s[60:61],v[14:15] op_sel:[1,0] op_sel_hi:[1,1]
v_cvt_pk_bf16_f32 v92,v92,v93
v_cvt_pk_bf16_f32 v93,v94,v95
v_cvt_pk_bf16_f32 v94,v12,v13
v_cvt_pk_bf16_f32 v95,v14,v15
v_mfma_f32_16x16x32_bf16 v[208:211],a[164:167],a[244:247],v[208:211]
buffer_load_dwordx4 v4,s[24:27],s47 offen offset:2048 nt lds
v_mfma_f32_16x16x32_bf16 v[216:219],a[164:167],a[252:255],v[216:219]
ds_read_b128 a[128:131],v1
v_accvgpr_read_b32 v120,a112
v_mfma_f32_16x16x32_bf16 v[216:219],a[160:163],a[248:251],v[216:219]
v_accvgpr_read_b32 v121,a113
v_mul_f32 v120,s61,v120
v_mfma_f32_16x16x32_bf16 v[196:199],a[168:171],a[224:227],v[196:199]
v_mul_f32 v121,s61,v121
v_accvgpr_read_b32 v122,a114
v_mfma_f32_16x16x32_bf16 v[196:199],a[172:175],a[228:231],v[196:199]
ds_read_b128 a[132:135],v1 offset:256
v_accvgpr_read_b32 v123,a115
v_mfma_f32_16x16x32_bf16 v[204:207],a[172:175],a[236:239],v[204:207]
v_accvgpr_read_b32 v108,a88
v_accvgpr_read_b32 v109,a89
v_accvgpr_read_b32 v110,a90
v_accvgpr_read_b32 v111,a91
v_accvgpr_read_b32 v12,a92
v_accvgpr_read_b32 v13,a93
v_accvgpr_read_b32 v14,a94
v_accvgpr_read_b32 v15,a95
v_pk_mul_f32 v[108:109],s[60:61],v[108:109] op_sel:[1,0] op_sel_hi:[1,1]
v_pk_mul_f32 v[110:111],s[60:61],v[110:111] op_sel:[1,0] op_sel_hi:[1,1]
v_pk_mul_f32 v[12:13],s[60:61],v[12:13] op_sel:[1,0] op_sel_hi:[1,1]
v_pk_mul_f32 v[14:15],s[60:61],v[14:15] op_sel:[1,0] op_sel_hi:[1,1]
v_cvt_pk_bf16_f32 v108,v108,v109
v_cvt_pk_bf16_f32 v109,v110,v111
v_cvt_pk_bf16_f32 v110,v12,v13
v_cvt_pk_bf16_f32 v111,v14,v15
v_mfma_f32_16x16x32_bf16 v[204:207],a[168:171],a[232:235],v[204:207]
v_cvt_pk_bf16_f32 v120,v120,v121
v_mul_f32 v122,s61,v122
v_mfma_f32_16x16x32_bf16 v[212:215],a[168:171],a[240:243],v[212:215]
v_mul_f32 v123,s61,v123
v_cvt_pk_bf16_f32 v121,v122,v123
v_mfma_f32_16x16x32_bf16 v[212:215],a[172:175],a[244:247],v[212:215]
ds_read_b128 a[136:139],v1 offset:512
v_accvgpr_read_b32 v122,a116
v_mfma_f32_16x16x32_bf16 v[220:223],a[172:175],a[252:255],v[220:223]
v_accvgpr_read_b32 v123,a117
v_accvgpr_read_b32 v16,a118
v_mfma_f32_16x16x32_bf16 v[220:223],a[168:171],a[248:251],v[220:223]
v_accvgpr_read_b32 v17,a119
v_mul_f32 v122,s61,v122
v_mfma_f32_16x16x32_bf16 v[224:227],a[176:179],a[224:227],v[224:227]
)A"
R"A(ds_read_b128 a[140:143],v1 offset:768
v_mul_f32 v123,s61,v123
v_mfma_f32_16x16x32_bf16 v[224:227],a[180:183],a[228:231],v[224:227]
v_mul_f32 v16,s61,v16
v_mul_f32 v17,s61,v17
v_mfma_f32_16x16x32_bf16 v[232:235],a[180:183],a[236:239],v[232:235]
v_cvt_pk_bf16_f32 v122,v122,v123
v_cvt_pk_bf16_f32 v123,v16,v17
v_mfma_f32_16x16x32_bf16 v[232:235],a[176:179],a[232:235],v[232:235]
ds_read_b128 a[156:159],v1 offset:4864
v_mfma_f32_16x16x32_bf16 v[240:243],a[176:179],a[240:243],v[240:243]
v_accvgpr_read_b32 v124,a120
v_accvgpr_read_b32 v125,a121
v_accvgpr_read_b32 v126,a122
v_accvgpr_read_b32 v127,a123
v_accvgpr_read_b32 v12,a124
v_accvgpr_read_b32 v13,a125
v_accvgpr_read_b32 v14,a126
v_accvgpr_read_b32 v15,a127
v_pk_mul_f32 v[124:125],s[60:61],v[124:125] op_sel:[1,0] op_sel_hi:[1,1]
v_pk_mul_f32 v[126:127],s[60:61],v[126:127] op_sel:[1,0] op_sel_hi:[1,1]
v_pk_mul_f32 v[12:13],s[60:61],v[12:13] op_sel:[1,0] op_sel_hi:[1,1]
v_pk_mul_f32 v[14:15],s[60:61],v[14:15] op_sel:[1,0] op_sel_hi:[1,1]
v_cvt_pk_bf16_f32 v124,v124,v125
v_cvt_pk_bf16_f32 v125,v126,v127
v_cvt_pk_bf16_f32 v126,v12,v13
v_cvt_pk_bf16_f32 v127,v14,v15
v_mfma_f32_16x16x32_bf16 v[240:243],a[180:183],a[244:247],v[240:243]
buffer_load_dwordx4 v5,s[24:27],s47 offen offset:3072 nt lds
v_mfma_f32_16x16x32_bf16 v[248:251],a[180:183],a[252:255],v[248:251]
s_add_u32 s47,s47,16384
s_and_b32 s47,s47,0x3ffff
ds_read_b128 a[152:155],v1 offset:4608
v_mfma_f32_16x16x32_bf16 v[248:251],a[176:179],a[248:251],v[248:251]
s_or_b32 s47,s47,s46
v_mul_f32 v128,s61,v128
v_mul_f32 v129,s61,v129
v_mul_f32 v130,s61,v130
v_mul_f32 v131,s61,v131
v_cvt_pk_bf16_f32 v128,v128,v129
v_cvt_pk_bf16_f32 v129,v130,v131
v_pk_mul_f32 v[132:133],s[60:61],v[132:133] op_sel:[1,0] op_sel_hi:[1,1]
v_pk_mul_f32 v[134:135],s[60:61],v[134:135] op_sel:[1,0] op_sel_hi:[1,1]
v_pk_mul_f32 v[136:137],s[60:61],v[136:137] op_sel:[1,0] op_sel_hi:[1,1]
v_pk_mul_f32 v[138:139],s[60:61],v[138:139] op_sel:[1,0] op_sel_hi:[1,1]
v_pk_mul_f32 v[140:141],s[60:61],v[140:141] op_sel:[1,0] op_sel_hi:[1,1]
v_pk_mul_f32 v[142:143],s[60:61],v[142:143] op_sel:[1,0] op_sel_hi:[1,1]
v_pk_mul_f32 v[144:145],s[60:61],v[144:145] op_sel:[1,0] op_sel_hi:[1,1]
v_pk_mul_f32 v[146:147],s[60:61],v[146:147] op_sel:[1,0] op_sel_hi:[1,1]
v_pk_mul_f32 v[148:149],s[60:61],v[148:149] op_sel:[1,0] op_sel_hi:[1,1]
v_pk_mul_f32 v[150:151],s[60:61],v[150:151] op_sel:[1,0] op_sel_hi:[1,1]
v_pk_mul_f32 v[152:153],s[60:61],v[152:153] op_sel:[1,0] op_sel_hi:[1,1]
v_pk_mul_f32 v[154:155],s[60:61],v[154:155] op_sel:[1,0] op_sel_hi:[1,1]
v_pk_mul_f32 v[156:157],s[60:61],v[156:157] op_sel:[1,0] op_sel_hi:[1,1]
v_pk_mul_f32 v[158:159],s[60:61],v[158:159] op_sel:[1,0] op_sel_hi:[1,1]
v_mfma_f32_16x16x32_bf16 v[228:231],a[184:187],a[224:227],v[228:231]
s_and_b32 s46,s54,0xfffc0000
ds_read_b128 a[148:151],v1 offset:4352
v_cvt_pk_bf16_f32 v130,v132,v133
v_mfma_f32_16x16x32_bf16 v[228:231],a[188:191],a[228:231],v[228:231]
s_add_u32 s54,s54,16384
v_cvt_pk_bf16_f32 v131,v134,v135
v_cvt_pk_bf16_f32 v136,v136,v137
v_mfma_f32_16x16x32_bf16 v[236:239],a[188:191],a[236:239],v[236:239]
ds_read_b128 a[144:147],v1 offset:4096
v_cvt_pk_bf16_f32 v137,v138,v139
v_mfma_f32_16x16x32_bf16 v[236:239],a[184:187],a[232:235],v[236:239]
v_cvt_pk_bf16_f32 v138,v140,v141
v_cvt_pk_bf16_f32 v139,v142,v143
v_mfma_f32_16x16x32_bf16 v[244:247],a[184:187],a[240:243],v[244:247]
ds_read_b128 a[160:163],v1 offset:8192
v_cvt_pk_bf16_f32 v144,v144,v145
v_mfma_f32_16x16x32_bf16 v[244:247],a[188:191],a[244:247],v[244:247]
v_cvt_pk_bf16_f32 v145,v146,v147
v_cvt_pk_bf16_f32 v146,v148,v149
v_mfma_f32_16x16x32_bf16 v[252:255],a[188:191],a[252:255],v[252:255]
ds_read_b128 a[164:167],v1 offset:8448
v_cvt_pk_bf16_f32 v147,v150,v151
v_mfma_f32_16x16x32_bf16 v[252:255],a[184:187],a[248:251],v[252:255]
s_waitcnt lgkmcnt(4)
v_cvt_pk_bf16_f32 v152,v152,v153
v_cvt_pk_bf16_f32 v153,v154,v155
v_mfma_f32_16x16x32_bf16 v[32:35],v[48:51],v[64:67],0
ds_read_b128 a[168:171],v1 offset:8704
v_cvt_pk_bf16_f32 v154,v156,v157
v_mfma_f32_16x16x32_bf16 v[32:35],v[52:55],v[68:71],v[32:35]
ds_read_b64_tr_b16 a[192:193],v2
v_cvt_pk_bf16_f32 v155,v158,v159
v_mfma_f32_16x16x32_bf16 v[32:35],v[56:59],v[72:75],v[32:35]
v_mul_f32 v160,s61,v160
v_mul_f32 v161,s61,v161
v_mul_f32 v162,s61,v162
v_mul_f32 v163,s61,v163
v_cvt_pk_bf16_f32 v160,v160,v161
v_cvt_pk_bf16_f32 v161,v162,v163
v_pk_mul_f32 v[164:165],s[60:61],v[164:165] op_sel:[1,0] op_sel_hi:[1,1]
v_pk_mul_f32 v[166:167],s[60:61],v[166:167] op_sel:[1,0] op_sel_hi:[1,1]
v_pk_mul_f32 v[168:169],s[60:61],v[168:169] op_sel:[1,0] op_sel_hi:[1,1]
v_pk_mul_f32 v[170:171],s[60:61],v[170:171] op_sel:[1,0] op_sel_hi:[1,1]
v_pk_mul_f32 v[172:173],s[60:61],v[172:173] op_sel:[1,0] op_sel_hi:[1,1]
v_pk_mul_f32 v[174:175],s[60:61],v[174:175] op_sel:[1,0] op_sel_hi:[1,1]
v_pk_mul_f32 v[176:177],s[60:61],v[176:177] op_sel:[1,0] op_sel_hi:[1,1]
v_pk_mul_f32 v[178:179],s[60:61],v[178:179] op_sel:[1,0] op_sel_hi:[1,1]
v_pk_mul_f32 v[180:181],s[60:61],v[180:181] op_sel:[1,0] op_sel_hi:[1,1]
v_pk_mul_f32 v[182:183],s[60:61],v[182:183] op_sel:[1,0] op_sel_hi:[1,1]
v_pk_mul_f32 v[184:185],s[60:61],v[184:185] op_sel:[1,0] op_sel_hi:[1,1]
v_pk_mul_f32 v[186:187],s[60:61],v[186:187] op_sel:[1,0] op_sel_hi:[1,1]
v_pk_mul_f32 v[188:189],s[60:61],v[188:189] op_sel:[1,0] op_sel_hi:[1,1]
v_pk_mul_f32 v[190:191],s[60:61],v[190:191] op_sel:[1,0] op_sel_hi:[1,1]
v_mfma_f32_16x16x32_bf16 v[32:35],v[60:63],v[76:79],v[32:35]
ds_read_b128 a[172:175],v1 offset:8960
v_cvt_pk_bf16_f32 v162,v164,v165
v_mfma_f32_16x16x32_bf16 v[36:39],v[48:51],v[80:83],0
v_cvt_pk_bf16_f32 v163,v166,v167
v_cvt_pk_bf16_f32 v168,v168,v169
v_mfma_f32_16x16x32_bf16 v[36:39],v[52:55],v[84:87],v[36:39]
ds_read_b128 a[188:191],v1 offset:13056
v_cvt_pk_bf16_f32 v169,v170,v171
v_mfma_f32_16x16x32_bf16 v[36:39],v[56:59],v[88:91],v[36:39]
v_cvt_pk_bf16_f32 v170,v172,v173
v_cvt_pk_bf16_f32 v171,v174,v175
v_mfma_f32_16x16x32_bf16 v[36:39],v[60:63],v[92:95],v[36:39]
ds_read_b128 a[184:187],v1 offset:12800
v_cvt_pk_bf16_f32 v176,v176,v177
v_mfma_f32_16x16x32_bf16 v[40:43],v[48:51],v[96:99],0
v_cvt_pk_bf16_f32 v177,v178,v179
v_cvt_pk_bf16_f32 v178,v180,v181
v_mfma_f32_16x16x32_bf16 v[40:43],v[52:55],v[100:103],v[40:43]
s_add_u32 m0,32768,s51
v_max3_f32 v32,v32,v33,v34
v_max3_f32 v32,v32,v35,s18
v_mfma_f32_16x16x32_bf16 v[40:43],v[56:59],v[104:107],v[40:43]
v_sub_f32 v35,0,v32
v_sub_f32 v34,0,v32
v_mfma_f32_16x16x32_bf16 v[40:43],v[60:63],v[108:111],v[40:43]
v_sub_f32 v33,0,v32
v_sub_f32 v32,0,v32
v_mfma_f32_16x16x32_bf16 v[44:47],v[48:51],v[112:115],0
buffer_load_dwordx4 v4,s[20:23],s47 offen nt lds
v_mfma_f32_16x16x32_bf16 v[44:47],v[52:55],v[116:119],v[44:47]
ds_read_b128 a[180:183],v1 offset:12544
v_max3_f32 v36,v36,v37,v38
v_mfma_f32_16x16x32_bf16 v[44:47],v[56:59],v[120:123],v[44:47]
v_max3_f32 v36,v36,v39,s19
v_sub_f32 v39,0,v36
v_mfma_f32_16x16x32_bf16 v[44:47],v[60:63],v[124:127],v[44:47]
ds_read_b128 a[176:179],v1 offset:12288
v_sub_f32 v38,0,v36
v_mfma_f32_16x16x32_bf16 v[132:135],a[128:131],v[64:67],v[32:35]
v_sub_f32 v37,0,v36
v_sub_f32 v36,0,v36
v_mfma_f32_16x16x32_bf16 v[132:135],a[132:135],v[68:71],v[132:135]
v_cvt_pk_bf16_f32 v179,v182,v183
v_cvt_pk_bf16_f32 v184,v184,v185
v_mfma_f32_16x16x32_bf16 v[132:135],a[136:139],v[72:75],v[132:135]
v_cvt_pk_bf16_f32 v185,v186,v187
v_cvt_pk_bf16_f32 v186,v188,v189
v_mfma_f32_16x16x32_bf16 v[132:135],a[140:143],v[76:79],v[132:135]
v_cvt_pk_bf16_f32 v187,v190,v191
v_max3_f32 v40,v40,v41,v42
v_mfma_f32_16x16x32_bf16 v[140:143],a[156:159],v[76:79],v[32:35]
v_max3_f32 v40,v40,v43,s100
v_sub_f32 v43,0,v40
v_mfma_f32_16x16x32_bf16 v[140:143],a[152:155],v[72:75],v[140:143]
s_waitcnt lgkmcnt(1)
buffer_load_dwordx4 v4,s[20:23],s47 offen offset:1024 nt lds
v_mfma_f32_16x16x32_bf16 v[140:143],a[148:151],v[68:71],v[140:143]
v_sub_f32 v42,0,v40
v_sub_f32 v41,0,v40
v_mfma_f32_16x16x32_bf16 v[140:143],a[144:147],v[64:67],v[140:143]
ds_read_b64_tr_b16 a[194:195],v2 offset:4096
v_sub_f32 v40,0,v40
v_mfma_f32_16x16x32_bf16 v[148:151],a[160:163],v[64:67],v[32:35]
v_mul_f32 v192,s61,v192
v_mul_f32 v193,s61,v193
v_mul_f32 v194,s61,v194
v_mul_f32 v195,s61,v195
v_cvt_pk_bf16_f32 v192,v192,v193
v_cvt_pk_bf16_f32 v193,v194,v195
v_pk_mul_f32 v[196:197],s[60:61],v[196:197] op_sel:[1,0] op_sel_hi:[1,1]
v_pk_mul_f32 v[198:199],s[60:61],v[198:199] op_sel:[1,0] op_sel_hi:[1,1]
v_pk_mul_f32 v[200:201],s[60:61],v[200:201] op_sel:[1,0] op_sel_hi:[1,1]
v_pk_mul_f32 v[202:203],s[60:61],v[202:203] op_sel:[1,0] op_sel_hi:[1,1]
v_pk_mul_f32 v[204:205],s[60:61],v[204:205] op_sel:[1,0] op_sel_hi:[1,1]
v_pk_mul_f32 v[206:207],s[60:61],v[206:207] op_sel:[1,0] op_sel_hi:[1,1]
v_pk_mul_f32 v[208:209],s[60:61],v[208:209] op_sel:[1,0] op_sel_hi:[1,1]
v_pk_mul_f32 v[210:211],s[60:61],v[210:211] op_sel:[1,0] op_sel_hi:[1,1]
v_pk_mul_f32 v[212:213],s[60:61],v[212:213] op_sel:[1,0] op_sel_hi:[1,1]
v_pk_mul_f32 v[214:215],s[60:61],v[214:215] op_sel:[1,0] op_sel_hi:[1,1]
v_pk_mul_f32 v[216:217],s[60:61],v[216:217] op_sel:[1,0] op_sel_hi:[1,1]
v_pk_mul_f32 v[218:219],s[60:61],v[218:219] op_sel:[1,0] op_sel_hi:[1,1]
v_pk_mul_f32 v[220:221],s[60:61],v[220:221] op_sel:[1,0] op_sel_hi:[1,1]
v_pk_mul_f32 v[222:223],s[60:61],v[222:223] op_sel:[1,0] op_sel_hi:[1,1]
v_mfma_f32_16x16x32_bf16 v[148:151],a[164:167],v[68:71],v[148:151]
v_cvt_pk_bf16_f32 v194,v196,v197
v_cvt_pk_bf16_f32 v195,v198,v199
v_mfma_f32_16x16x32_bf16 v[148:151],a[168:171],v[72:75],v[148:151]
v_cvt_pk_bf16_f32 v200,v200,v201
v_cvt_pk_bf16_f32 v201,v202,v203
v_mfma_f32_16x16x32_bf16 v[148:151],a[172:175],v[76:79],v[148:151]
v_cvt_pk_bf16_f32 v202,v204,v205
v_cvt_pk_bf16_f32 v203,v206,v207
v_mfma_f32_16x16x32_bf16 v[156:159],a[188:191],v[76:79],v[32:35]
v_cvt_pk_bf16_f32 v208,v208,v209
v_cvt_pk_bf16_f32 v209,v210,v211
v_mfma_f32_16x16x32_bf16 v[156:159],a[184:187],v[72:75],v[156:159]
v_cvt_pk_bf16_f32 v210,v212,v213
v_cvt_pk_bf16_f32 v211,v214,v215
v_mfma_f32_16x16x32_bf16 v[156:159],a[180:183],v[68:71],v[156:159]
s_waitcnt lgkmcnt(1)
buffer_load_dwordx4 v5,s[20:23],s47 offen offset:2048 nt lds
v_mfma_f32_16x16x32_bf16 v[156:159],a[176:179],v[64:67],v[156:159]
ds_read_b64_tr_b16 a[196:197],v2 offset:8192
ds_read_b64_tr_b16 a[198:199],v2 offset:12288
v_mfma_f32_16x16x32_bf16 v[164:167],a[128:131],v[80:83],v[36:39]
ds_read_b64_tr_b16 a[204:205],v3 offset:8192
v_cvt_pk_bf16_f32 v216,v216,v217
v_mfma_f32_16x16x32_bf16 v[164:167],a[132:135],v[84:87],v[164:167]
ds_read_b64_tr_b16 a[206:207],v3 offset:12288
v_cvt_pk_bf16_f32 v217,v218,v219
v_mfma_f32_16x16x32_bf16 v[164:167],a[136:139],v[88:91],v[164:167]
ds_read_b64_tr_b16 a[200:201],v3
v_cvt_pk_bf16_f32 v218,v220,v221
v_mfma_f32_16x16x32_bf16 v[164:167],a[140:143],v[92:95],v[164:167]
ds_read_b64_tr_b16 a[202:203],v3 offset:4096
v_cvt_pk_bf16_f32 v219,v222,v223
v_mfma_f32_16x16x32_bf16 v[172:175],a[156:159],v[92:95],v[36:39]
ds_read_b64_tr_b16 a[208:209],v2 offset:256
ds_read_b64_tr_b16 a[210:211],v2 offset:4352
v_mfma_f32_16x16x32_bf16 v[172:175],a[152:155],v[88:91],v[172:175]
v_exp_f32 v132,v132
v_mfma_f32_16x16x32_bf16 v[172:175],a[148:151],v[84:87],v[172:175]
v_exp_f32 v133,v133
v_mfma_f32_16x16x32_bf16 v[172:175],a[144:147],v[80:83],v[172:175]
buffer_load_dwordx4 v5,s[20:23],s47 offen offset:3072 nt lds
v_mfma_f32_16x16x32_bf16 v[180:183],a[160:163],v[80:83],v[36:39]
v_exp_f32 v134,v134
v_mfma_f32_16x16x32_bf16 v[180:183],a[164:167],v[84:87],v[180:183]
v_exp_f32 v135,v135
v_mfma_f32_16x16x32_bf16 v[180:183],a[168:171],v[88:91],v[180:183]
ds_read_b64_tr_b16 a[212:213],v2 offset:8448
ds_read_b64_tr_b16 a[214:215],v2 offset:12544
v_mfma_f32_16x16x32_bf16 v[180:183],a[172:175],v[92:95],v[180:183]
v_cvt_pk_bf16_f32 v132,v132,v133
v_cvt_pk_bf16_f32 v133,v134,v135
v_mfma_f32_16x16x32_bf16 v[188:191],a[188:191],v[92:95],v[36:39]
v_exp_f32 v140,v140
v_mfma_f32_16x16x32_bf16 v[188:191],a[184:187],v[88:91],v[188:191]
s_add_u32 m0,32768,s52
v_exp_f32 v141,v141
v_mfma_f32_16x16x32_bf16 v[188:191],a[180:183],v[84:87],v[188:191]
v_exp_f32 v142,v142
v_mfma_f32_16x16x32_bf16 v[188:191],a[176:179],v[80:83],v[188:191]
v_exp_f32 v143,v143
)A"
R"A(v_mfma_f32_16x16x32_bf16 v[196:199],a[128:131],v[96:99],v[40:43]
buffer_load_dwordx4 v4,s[24:27],s47 offen nt lds
v_mfma_f32_16x16x32_bf16 v[196:199],a[132:135],v[100:103],v[196:199]
v_exp_f32 v148,v148
v_mfma_f32_16x16x32_bf16 v[196:199],a[136:139],v[104:107],v[196:199]
v_exp_f32 v149,v149
v_mfma_f32_16x16x32_bf16 v[196:199],a[140:143],v[108:111],v[196:199]
v_exp_f32 v150,v150
v_mfma_f32_16x16x32_bf16 v[204:207],a[156:159],v[108:111],v[40:43]
v_exp_f32 v151,v151
v_mfma_f32_16x16x32_bf16 v[204:207],a[152:155],v[104:107],v[204:207]
ds_read_b64_tr_b16 a[220:221],v3 offset:8448
ds_read_b64_tr_b16 a[222:223],v3 offset:12544
v_mfma_f32_16x16x32_bf16 v[204:207],a[148:151],v[100:103],v[204:207]
v_cvt_pk_bf16_f32 v134,v140,v141
v_cvt_pk_bf16_f32 v135,v142,v143
v_mfma_f32_16x16x32_bf16 v[204:207],a[144:147],v[96:99],v[204:207]
v_cvt_pk_bf16_f32 v148,v148,v149
v_cvt_pk_bf16_f32 v149,v150,v151
v_mfma_f32_16x16x32_bf16 v[212:215],a[160:163],v[96:99],v[40:43]
v_exp_f32 v156,v156
v_mfma_f32_16x16x32_bf16 v[212:215],a[164:167],v[100:103],v[212:215]
buffer_load_dwordx4 v5,s[24:27],s47 offen offset:1024 nt lds
v_mfma_f32_16x16x32_bf16 v[212:215],a[168:171],v[104:107],v[212:215]
v_exp_f32 v157,v157
v_mfma_f32_16x16x32_bf16 v[212:215],a[172:175],v[108:111],v[212:215]
v_exp_f32 v158,v158
v_mfma_f32_16x16x32_bf16 v[220:223],a[188:191],v[108:111],v[40:43]
v_exp_f32 v159,v159
v_mfma_f32_16x16x32_bf16 v[220:223],a[184:187],v[104:107],v[220:223]
ds_read_b64_tr_b16 a[216:217],v3 offset:256
ds_read_b64_tr_b16 a[218:219],v3 offset:4352
v_mfma_f32_16x16x32_bf16 v[220:223],a[180:183],v[100:103],v[220:223]
v_cvt_pk_bf16_f32 v150,v156,v157
v_cvt_pk_bf16_f32 v151,v158,v159
v_mfma_f32_16x16x32_bf16 v[220:223],a[176:179],v[96:99],v[220:223]
s_waitcnt lgkmcnt(2)
ds_read_b64_tr_b16 a[224:225],v2 offset:512
ds_read_b64_tr_b16 a[226:227],v2 offset:4608
v_mfma_f32_16x16x32_bf16 a[0:3],a[192:195],v[132:135],0
ds_read_b64_tr_b16 a[228:229],v2 offset:8704
ds_read_b64_tr_b16 a[230:231],v2 offset:12800
v_mfma_f32_16x16x32_bf16 a[0:3],a[196:199],v[148:151],a[0:3]
ds_read_b64_tr_b16 a[236:237],v3 offset:8704
ds_read_b64_tr_b16 a[238:239],v3 offset:12800
v_mfma_f32_16x16x32_bf16 a[4:7],a[204:207],v[148:151],0
ds_read_b64_tr_b16 a[232:233],v3 offset:512
ds_read_b64_tr_b16 a[234:235],v3 offset:4608
v_mfma_f32_16x16x32_bf16 a[4:7],a[200:203],v[132:135],a[4:7]
ds_read_b64_tr_b16 a[240:241],v2 offset:768
ds_read_b64_tr_b16 a[242:243],v2 offset:4864
v_mfma_f32_16x16x32_bf16 a[8:11],a[208:211],v[132:135],0
ds_read_b64_tr_b16 a[244:245],v2 offset:8960
ds_read_b64_tr_b16 a[246:247],v2 offset:13056
v_mfma_f32_16x16x32_bf16 a[8:11],a[212:215],v[148:151],a[8:11]
s_memrealtime s[76:77]
buffer_load_dwordx4 v4,s[24:27],s47 offen offset:2048 nt lds
ds_read_b64_tr_b16 a[252:253],v3 offset:8960
ds_read_b64_tr_b16 a[254:255],v3 offset:13056
v_mfma_f32_16x16x32_bf16 a[12:15],a[220:223],v[148:151],0
s_waitcnt lgkmcnt(14)
ds_read_b64_tr_b16 a[248:249],v3 offset:768
ds_read_b64_tr_b16 a[250:251],v3 offset:4864
v_mfma_f32_16x16x32_bf16 a[12:15],a[216:219],v[132:135],a[12:15]
s_waitcnt lgkmcnt(14)
v_mfma_f32_16x16x32_bf16 a[16:19],a[224:227],v[132:135],0
s_waitcnt lgkmcnt(12)
v_mfma_f32_16x16x32_bf16 a[16:19],a[228:231],v[148:151],a[16:19]
s_waitcnt lgkmcnt(10)
v_mfma_f32_16x16x32_bf16 a[20:23],a[236:239],v[148:151],0
s_waitcnt lgkmcnt(8)
v_mfma_f32_16x16x32_bf16 a[20:23],a[232:235],v[132:135],a[20:23]
s_waitcnt lgkmcnt(0)
v_max3_f32 v44,v44,v45,v46
v_max3_f32 v44,v44,v47,s101
v_mfma_f32_16x16x32_bf16 a[24:27],a[240:243],v[132:135],0
s_add_u32 s78,s76,s60
buffer_load_dwordx4 v5,s[24:27],s47 offen offset:3072 nt lds
v_mfma_f32_16x16x32_bf16 a[24:27],a[244:247],v[148:151],a[24:27]
s_add_u32 s47,s47,16384
v_sub_f32 v47,0,v44
v_sub_f32 v46,0,v44
v_mfma_f32_16x16x32_bf16 a[28:31],a[252:255],v[148:151],0
s_and_b32 s47,s47,0x3ffff
v_sub_f32 v45,0,v44
v_sub_f32 v44,0,v44
v_mfma_f32_16x16x32_bf16 a[28:31],a[248:251],v[132:135],a[28:31]
v_mul_f32 v224,s61,v224
v_mul_f32 v225,s61,v225
v_mul_f32 v226,s61,v226
v_mul_f32 v227,s61,v227
v_cvt_pk_bf16_f32 v224,v224,v225
v_cvt_pk_bf16_f32 v225,v226,v227
v_pk_mul_f32 v[228:229],s[60:61],v[228:229] op_sel:[1,0] op_sel_hi:[1,1]
v_pk_mul_f32 v[230:231],s[60:61],v[230:231] op_sel:[1,0] op_sel_hi:[1,1]
v_pk_mul_f32 v[232:233],s[60:61],v[232:233] op_sel:[1,0] op_sel_hi:[1,1]
v_pk_mul_f32 v[234:235],s[60:61],v[234:235] op_sel:[1,0] op_sel_hi:[1,1]
v_pk_mul_f32 v[236:237],s[60:61],v[236:237] op_sel:[1,0] op_sel_hi:[1,1]
v_pk_mul_f32 v[238:239],s[60:61],v[238:239] op_sel:[1,0] op_sel_hi:[1,1]
v_pk_mul_f32 v[240:241],s[60:61],v[240:241] op_sel:[1,0] op_sel_hi:[1,1]
v_pk_mul_f32 v[242:243],s[60:61],v[242:243] op_sel:[1,0] op_sel_hi:[1,1]
v_pk_mul_f32 v[244:245],s[60:61],v[244:245] op_sel:[1,0] op_sel_hi:[1,1]
v_pk_mul_f32 v[246:247],s[60:61],v[246:247] op_sel:[1,0] op_sel_hi:[1,1]
v_pk_mul_f32 v[248:249],s[60:61],v[248:249] op_sel:[1,0] op_sel_hi:[1,1]
v_pk_mul_f32 v[250:251],s[60:61],v[250:251] op_sel:[1,0] op_sel_hi:[1,1]
v_pk_mul_f32 v[252:253],s[60:61],v[252:253] op_sel:[1,0] op_sel_hi:[1,1]
v_pk_mul_f32 v[254:255],s[60:61],v[254:255] op_sel:[1,0] op_sel_hi:[1,1]
v_mfma_f32_16x16x32_bf16 v[48:51],v[28:31],v[132:135],0
s_or_b32 s47,s47,s46
v_cvt_pk_bf16_f32 v226,v228,v229
v_cvt_pk_bf16_f32 v227,v230,v231
v_mfma_f32_16x16x32_bf16 v[48:51],v[28:31],v[148:151],v[48:51]
s_and_b32 s46,s54,0xfffc0000
v_cvt_pk_bf16_f32 v232,v232,v233
v_cvt_pk_bf16_f32 v233,v234,v235
v_mfma_f32_16x16x32_bf16 v[228:231],a[128:131],v[112:115],v[44:47]
s_waitcnt vmcnt(8)
v_cvt_pk_bf16_f32 v234,v236,v237
v_cvt_pk_bf16_f32 v235,v238,v239
v_mfma_f32_16x16x32_bf16 v[228:231],a[132:135],v[116:119],v[228:231]
s_barrier
s_add_u32 m0,65536,s51
v_cvt_pk_bf16_f32 v240,v240,v241
v_mfma_f32_16x16x32_bf16 v[228:231],a[136:139],v[120:123],v[228:231]
v_cvt_pk_bf16_f32 v241,v242,v243
v_cvt_pk_bf16_f32 v242,v244,v245
v_mfma_f32_16x16x32_bf16 v[228:231],a[140:143],v[124:127],v[228:231]
v_cvt_pk_bf16_f32 v243,v246,v247
v_cvt_pk_bf16_f32 v248,v248,v249
v_mfma_f32_16x16x32_bf16 v[236:239],a[156:159],v[124:127],v[44:47]
buffer_load_dwordx4 v4,s[20:23],s47 offen nt lds
v_mfma_f32_16x16x32_bf16 v[236:239],a[152:155],v[120:123],v[236:239]
v_cvt_pk_bf16_f32 v249,v250,v251
v_cvt_pk_bf16_f32 v250,v252,v253
v_mfma_f32_16x16x32_bf16 v[236:239],a[148:151],v[116:119],v[236:239]
v_cvt_pk_bf16_f32 v251,v254,v255
v_add_u32 v1,s48,v1
v_mfma_f32_16x16x32_bf16 v[236:239],a[144:147],v[112:115],v[236:239]
v_exp_f32 v164,v164
v_mfma_f32_16x16x32_bf16 v[244:247],a[160:163],v[112:115],v[44:47]
v_exp_f32 v165,v165
v_mfma_f32_16x16x32_bf16 v[244:247],a[164:167],v[116:119],v[244:247]
v_exp_f32 v166,v166
v_mfma_f32_16x16x32_bf16 v[244:247],a[168:171],v[120:123],v[244:247]
v_exp_f32 v167,v167
v_mfma_f32_16x16x32_bf16 v[244:247],a[172:175],v[124:127],v[244:247]
v_cvt_pk_bf16_f32 v164,v164,v165
v_cvt_pk_bf16_f32 v165,v166,v167
v_mfma_f32_16x16x32_bf16 v[252:255],a[188:191],v[124:127],v[44:47]
v_exp_f32 v172,v172
v_exp_f32 v173,v173
v_exp_f32 v174,v174
v_exp_f32 v175,v175
v_exp_f32 v180,v180
v_exp_f32 v181,v181
v_mfma_f32_16x16x32_bf16 v[252:255],a[184:187],v[120:123],v[252:255]
v_exp_f32 v182,v182
v_exp_f32 v183,v183
v_cvt_pk_bf16_f32 v166,v172,v173
v_cvt_pk_bf16_f32 v167,v174,v175
v_cvt_pk_bf16_f32 v180,v180,v181
v_exp_f32 v188,v188
v_exp_f32 v189,v189
v_exp_f32 v190,v190
v_exp_f32 v191,v191
v_mfma_f32_16x16x32_bf16 v[252:255],a[180:183],v[116:119],v[252:255]
v_cvt_pk_bf16_f32 v181,v182,v183
v_cvt_pk_bf16_f32 v182,v188,v189
v_cvt_pk_bf16_f32 v183,v190,v191
v_mfma_f32_16x16x32_bf16 v[252:255],a[176:179],v[112:115],v[252:255]
ds_read_b128 a[128:131],v1
v_add_u32 v2,s48,v2
v_mfma_f32_16x16x32_bf16 a[32:35],a[192:195],v[164:167],0
v_exp_f32 v196,v196
v_mfma_f32_16x16x32_bf16 a[32:35],a[196:199],v[180:183],a[32:35]
ds_read_b128 a[132:135],v1 offset:256
v_add_u32 v3,s48,v3
v_mfma_f32_16x16x32_bf16 a[36:39],a[204:207],v[180:183],0
s_mov_b32 s48,32768
buffer_load_dwordx4 v4,s[20:23],s47 offen offset:1024 nt lds
v_mfma_f32_16x16x32_bf16 a[36:39],a[200:203],v[164:167],a[36:39]
ds_read_b128 a[136:139],v1 offset:512
v_mfma_f32_16x16x32_bf16 a[40:43],a[208:211],v[164:167],0
v_exp_f32 v197,v197
v_mfma_f32_16x16x32_bf16 a[40:43],a[212:215],v[180:183],a[40:43]
ds_read_b128 a[140:143],v1 offset:768
v_cvt_pk_bf16_f32 v196,v196,v197
v_mfma_f32_16x16x32_bf16 a[44:47],a[220:223],v[180:183],0
v_exp_f32 v198,v198
v_mfma_f32_16x16x32_bf16 a[44:47],a[216:219],v[164:167],a[44:47]
ds_read_b128 a[156:159],v1 offset:4864
v_mfma_f32_16x16x32_bf16 a[48:51],a[224:227],v[164:167],0
v_exp_f32 v199,v199
v_mfma_f32_16x16x32_bf16 a[48:51],a[228:231],v[180:183],a[48:51]
ds_read_b128 a[152:155],v1 offset:4608
v_cvt_pk_bf16_f32 v197,v198,v199
v_mfma_f32_16x16x32_bf16 a[52:55],a[236:239],v[180:183],0
v_exp_f32 v204,v204
v_mfma_f32_16x16x32_bf16 a[52:55],a[232:235],v[164:167],a[52:55]
ds_read_b128 a[148:151],v1 offset:4352
v_mfma_f32_16x16x32_bf16 a[56:59],a[240:243],v[164:167],0
v_exp_f32 v205,v205
v_mfma_f32_16x16x32_bf16 a[56:59],a[244:247],v[180:183],a[56:59]
ds_read_b128 a[144:147],v1 offset:4096
v_cvt_pk_bf16_f32 v198,v204,v205
v_mfma_f32_16x16x32_bf16 a[60:63],a[252:255],v[180:183],0
v_exp_f32 v206,v206
v_mfma_f32_16x16x32_bf16 a[60:63],a[248:251],v[164:167],a[60:63]
ds_read_b128 a[160:163],v1 offset:8192
v_mfma_f32_16x16x32_bf16 v[52:55],v[28:31],v[164:167],0
buffer_load_dwordx4 v5,s[20:23],s47 offen offset:2048 nt lds
v_mfma_f32_16x16x32_bf16 v[52:55],v[28:31],v[180:183],v[52:55]
s_waitcnt lgkmcnt(3)
ds_read_b128 a[164:167],v1 offset:8448
v_mfma_f32_16x16x32_bf16 v[132:135],a[128:131],v[64:67],v[32:35]
v_exp_f32 v207,v207
v_mfma_f32_16x16x32_bf16 v[132:135],a[132:135],v[68:71],v[132:135]
ds_read_b128 a[168:171],v1 offset:8704
v_cvt_pk_bf16_f32 v199,v206,v207
v_mfma_f32_16x16x32_bf16 v[132:135],a[136:139],v[72:75],v[132:135]
ds_read_b128 a[172:175],v1 offset:8960
v_mfma_f32_16x16x32_bf16 v[132:135],a[140:143],v[76:79],v[132:135]
ds_read_b128 a[188:191],v1 offset:13056
v_mfma_f32_16x16x32_bf16 v[140:143],a[156:159],v[76:79],v[32:35]
ds_read_b128 a[184:187],v1 offset:12800
v_mfma_f32_16x16x32_bf16 v[140:143],a[152:155],v[72:75],v[140:143]
s_waitcnt lgkmcnt(5)
ds_read_b128 a[180:183],v1 offset:12544
v_mfma_f32_16x16x32_bf16 v[140:143],a[148:151],v[68:71],v[140:143]
ds_read_b128 a[176:179],v1 offset:12288
v_mfma_f32_16x16x32_bf16 v[140:143],a[144:147],v[64:67],v[140:143]
v_mfma_f32_16x16x32_bf16 v[148:151],a[160:163],v[64:67],v[32:35]
s_waitcnt lgkmcnt(6)
v_mfma_f32_16x16x32_bf16 v[148:151],a[164:167],v[68:71],v[148:151]
s_waitcnt lgkmcnt(5)
v_exp_f32 v212,v212
v_mfma_f32_16x16x32_bf16 v[148:151],a[168:171],v[72:75],v[148:151]
s_waitcnt lgkmcnt(4)
v_exp_f32 v213,v213
v_mfma_f32_16x16x32_bf16 v[148:151],a[172:175],v[76:79],v[148:151]
s_waitcnt lgkmcnt(3)
v_exp_f32 v214,v214
v_mfma_f32_16x16x32_bf16 v[156:159],a[188:191],v[76:79],v[32:35]
s_waitcnt lgkmcnt(2)
v_exp_f32 v215,v215
v_mfma_f32_16x16x32_bf16 v[156:159],a[184:187],v[72:75],v[156:159]
s_waitcnt lgkmcnt(1)
v_cvt_pk_bf16_f32 v212,v212,v213
v_exp_f32 v220,v220
v_exp_f32 v221,v221
v_exp_f32 v222,v222
v_exp_f32 v223,v223
v_mfma_f32_16x16x32_bf16 v[156:159],a[180:183],v[68:71],v[156:159]
s_waitcnt lgkmcnt(0)
v_cvt_pk_bf16_f32 v213,v214,v215
v_cvt_pk_bf16_f32 v214,v220,v221
v_cvt_pk_bf16_f32 v215,v222,v223
v_mfma_f32_16x16x32_bf16 v[156:159],a[176:179],v[64:67],v[156:159]
buffer_load_dwordx4 v5,s[20:23],s47 offen offset:3072 nt lds
v_mfma_f32_16x16x32_bf16 a[64:67],a[192:195],v[196:199],0
v_exp_f32 v228,v228
v_mfma_f32_16x16x32_bf16 a[64:67],a[196:199],v[212:215],a[64:67]
v_exp_f32 v229,v229
v_mfma_f32_16x16x32_bf16 a[68:71],a[204:207],v[212:215],0
v_exp_f32 v230,v230
v_mfma_f32_16x16x32_bf16 a[68:71],a[200:203],v[196:199],a[68:71]
v_exp_f32 v231,v231
v_mfma_f32_16x16x32_bf16 a[72:75],a[208:211],v[196:199],0
v_cvt_pk_bf16_f32 v228,v228,v229
v_cvt_pk_bf16_f32 v229,v230,v231
v_mfma_f32_16x16x32_bf16 a[72:75],a[212:215],v[212:215],a[72:75]
v_exp_f32 v236,v236
v_mfma_f32_16x16x32_bf16 a[76:79],a[220:223],v[212:215],0
v_exp_f32 v237,v237
v_mfma_f32_16x16x32_bf16 a[76:79],a[216:219],v[196:199],a[76:79]
)A"
R"A(s_add_u32 m0,65536,s52
v_exp_f32 v238,v238
v_mfma_f32_16x16x32_bf16 a[80:83],a[224:227],v[196:199],0
v_exp_f32 v239,v239
v_mfma_f32_16x16x32_bf16 a[80:83],a[228:231],v[212:215],a[80:83]
v_exp_f32 v244,v244
v_mfma_f32_16x16x32_bf16 a[84:87],a[236:239],v[212:215],0
buffer_load_dwordx4 v4,s[24:27],s47 offen nt lds
v_mfma_f32_16x16x32_bf16 a[84:87],a[232:235],v[196:199],a[84:87]
v_exp_f32 v245,v245
v_mfma_f32_16x16x32_bf16 a[88:91],a[240:243],v[196:199],0
v_exp_f32 v246,v246
v_mfma_f32_16x16x32_bf16 a[88:91],a[244:247],v[212:215],a[88:91]
v_exp_f32 v247,v247
v_mfma_f32_16x16x32_bf16 a[92:95],a[252:255],v[212:215],0
v_cvt_pk_bf16_f32 v230,v236,v237
v_cvt_pk_bf16_f32 v231,v238,v239
v_mfma_f32_16x16x32_bf16 a[92:95],a[248:251],v[196:199],a[92:95]
v_cvt_pk_bf16_f32 v244,v244,v245
v_cvt_pk_bf16_f32 v245,v246,v247
v_mfma_f32_16x16x32_bf16 v[56:59],v[28:31],v[196:199],0
v_exp_f32 v252,v252
v_mfma_f32_16x16x32_bf16 v[56:59],v[28:31],v[212:215],v[56:59]
v_exp_f32 v253,v253
v_mfma_f32_16x16x32_bf16 v[164:167],a[128:131],v[80:83],v[36:39]
v_exp_f32 v254,v254
v_mfma_f32_16x16x32_bf16 v[164:167],a[132:135],v[84:87],v[164:167]
v_exp_f32 v255,v255
v_mfma_f32_16x16x32_bf16 v[164:167],a[136:139],v[88:91],v[164:167]
v_cvt_pk_bf16_f32 v246,v252,v253
v_cvt_pk_bf16_f32 v247,v254,v255
v_mfma_f32_16x16x32_bf16 v[164:167],a[140:143],v[92:95],v[164:167]
v_exp_f32 v132,v132
v_mfma_f32_16x16x32_bf16 v[172:175],a[156:159],v[92:95],v[36:39]
v_exp_f32 v133,v133
v_mfma_f32_16x16x32_bf16 v[172:175],a[152:155],v[88:91],v[172:175]
v_exp_f32 v134,v134
v_mfma_f32_16x16x32_bf16 v[172:175],a[148:151],v[84:87],v[172:175]
buffer_load_dwordx4 v5,s[24:27],s47 offen offset:1024 nt lds
v_mfma_f32_16x16x32_bf16 v[172:175],a[144:147],v[80:83],v[172:175]
v_exp_f32 v135,v135
v_mfma_f32_16x16x32_bf16 v[180:183],a[160:163],v[80:83],v[36:39]
v_cvt_pk_bf16_f32 v132,v132,v133
v_cvt_pk_bf16_f32 v133,v134,v135
v_mfma_f32_16x16x32_bf16 v[180:183],a[164:167],v[84:87],v[180:183]
v_exp_f32 v140,v140
v_mfma_f32_16x16x32_bf16 v[180:183],a[168:171],v[88:91],v[180:183]
v_exp_f32 v141,v141
v_mfma_f32_16x16x32_bf16 v[180:183],a[172:175],v[92:95],v[180:183]
v_exp_f32 v142,v142
v_mfma_f32_16x16x32_bf16 v[188:191],a[188:191],v[92:95],v[36:39]
v_exp_f32 v143,v143
v_mfma_f32_16x16x32_bf16 v[188:191],a[184:187],v[88:91],v[188:191]
v_exp_f32 v148,v148
v_mfma_f32_16x16x32_bf16 v[188:191],a[180:183],v[84:87],v[188:191]
v_exp_f32 v149,v149
v_mfma_f32_16x16x32_bf16 v[188:191],a[176:179],v[80:83],v[188:191]
v_exp_f32 v150,v150
v_mfma_f32_16x16x32_bf16 a[96:99],a[192:195],v[228:231],0
ds_read_b64_tr_b16 a[192:193],v2
ds_read_b64_tr_b16 a[194:195],v2 offset:4096
v_mfma_f32_16x16x32_bf16 a[96:99],a[196:199],v[244:247],a[96:99]
ds_read_b64_tr_b16 a[196:197],v2 offset:8192
v_cvt_pk_bf16_f32 v134,v140,v141
v_mfma_f32_16x16x32_bf16 a[100:103],a[204:207],v[244:247],0
ds_read_b64_tr_b16 a[198:199],v2 offset:12288
v_cvt_pk_bf16_f32 v135,v142,v143
v_mfma_f32_16x16x32_bf16 a[100:103],a[200:203],v[228:231],a[100:103]
ds_read_b64_tr_b16 a[204:205],v3 offset:8192
v_cvt_pk_bf16_f32 v148,v148,v149
v_mfma_f32_16x16x32_bf16 a[104:107],a[208:211],v[228:231],0
buffer_load_dwordx4 v4,s[24:27],s47 offen offset:2048 nt lds
v_mfma_f32_16x16x32_bf16 a[104:107],a[212:215],v[244:247],a[104:107]
ds_read_b64_tr_b16 a[206:207],v3 offset:12288
ds_read_b64_tr_b16 a[200:201],v3
v_mfma_f32_16x16x32_bf16 a[108:111],a[220:223],v[244:247],0
ds_read_b64_tr_b16 a[202:203],v3 offset:4096
v_add_u32 v1,s48,v1
v_mfma_f32_16x16x32_bf16 a[108:111],a[216:219],v[228:231],a[108:111]
ds_read_b64_tr_b16 a[208:209],v2 offset:256
v_mfma_f32_16x16x32_bf16 a[112:115],a[224:227],v[228:231],0
ds_read_b64_tr_b16 a[210:211],v2 offset:4352
v_mfma_f32_16x16x32_bf16 a[112:115],a[228:231],v[244:247],a[112:115]
ds_read_b64_tr_b16 a[212:213],v2 offset:8448
ds_read_b64_tr_b16 a[214:215],v2 offset:12544
v_mfma_f32_16x16x32_bf16 a[116:119],a[236:239],v[244:247],0
v_exp_f32 v151,v151
v_mfma_f32_16x16x32_bf16 a[116:119],a[232:235],v[228:231],a[116:119]
v_exp_f32 v156,v156
v_mfma_f32_16x16x32_bf16 a[120:123],a[240:243],v[228:231],0
v_exp_f32 v157,v157
v_mfma_f32_16x16x32_bf16 a[120:123],a[244:247],v[244:247],a[120:123]
v_exp_f32 v158,v158
v_mfma_f32_16x16x32_bf16 a[124:127],a[252:255],v[244:247],0
v_exp_f32 v159,v159
v_mfma_f32_16x16x32_bf16 a[124:127],a[248:251],v[228:231],a[124:127]
ds_read_b64_tr_b16 a[220:221],v3 offset:8448
ds_read_b64_tr_b16 a[222:223],v3 offset:12544
v_mfma_f32_16x16x32_bf16 v[60:63],v[28:31],v[228:231],0
v_cvt_pk_bf16_f32 v149,v150,v151
v_cvt_pk_bf16_f32 v150,v156,v157
v_mfma_f32_16x16x32_bf16 v[60:63],v[28:31],v[244:247],v[60:63]
ds_read_b64_tr_b16 a[216:217],v3 offset:256
v_cvt_pk_bf16_f32 v151,v158,v159
v_mfma_f32_16x16x32_bf16 v[196:199],a[128:131],v[96:99],v[40:43]
buffer_load_dwordx4 v5,s[24:27],s47 offen offset:3072 nt lds
v_mfma_f32_16x16x32_bf16 v[196:199],a[132:135],v[100:103],v[196:199]
s_add_u32 s47,s47,16384
ds_read_b64_tr_b16 a[218:219],v3 offset:4352
ds_read_b64_tr_b16 a[224:225],v2 offset:512
v_mfma_f32_16x16x32_bf16 v[196:199],a[136:139],v[104:107],v[196:199]
s_and_b32 s47,s47,0x3ffff
s_or_b32 s47,s47,s46
ds_read_b64_tr_b16 a[226:227],v2 offset:4608
v_mfma_f32_16x16x32_bf16 v[196:199],a[140:143],v[108:111],v[196:199]
ds_read_b64_tr_b16 a[228:229],v2 offset:8704
v_mfma_f32_16x16x32_bf16 v[204:207],a[156:159],v[108:111],v[40:43]
ds_read_b64_tr_b16 a[230:231],v2 offset:12800
v_mfma_f32_16x16x32_bf16 v[204:207],a[152:155],v[104:107],v[204:207]
ds_read_b64_tr_b16 a[236:237],v3 offset:8704
ds_read_b64_tr_b16 a[238:239],v3 offset:12800
v_mfma_f32_16x16x32_bf16 v[204:207],a[148:151],v[100:103],v[204:207]
ds_read_b64_tr_b16 a[232:233],v3 offset:512
v_mfma_f32_16x16x32_bf16 v[204:207],a[144:147],v[96:99],v[204:207]
ds_read_b64_tr_b16 a[234:235],v3 offset:4608
v_mfma_f32_16x16x32_bf16 v[212:215],a[160:163],v[96:99],v[40:43]
ds_read_b64_tr_b16 a[240:241],v2 offset:768
v_mfma_f32_16x16x32_bf16 v[212:215],a[164:167],v[100:103],v[212:215]
ds_read_b64_tr_b16 a[242:243],v2 offset:4864
ds_read_b64_tr_b16 a[244:245],v2 offset:8960
v_mfma_f32_16x16x32_bf16 v[212:215],a[168:171],v[104:107],v[212:215]
ds_read_b64_tr_b16 a[246:247],v2 offset:13056
v_mfma_f32_16x16x32_bf16 v[212:215],a[172:175],v[108:111],v[212:215]
ds_read_b64_tr_b16 a[252:253],v3 offset:8960
v_mfma_f32_16x16x32_bf16 v[220:223],a[188:191],v[108:111],v[40:43]
ds_read_b64_tr_b16 a[254:255],v3 offset:13056
v_mfma_f32_16x16x32_bf16 v[220:223],a[184:187],v[104:107],v[220:223]
ds_read_b64_tr_b16 a[248:249],v3 offset:768
ds_read_b64_tr_b16 a[250:251],v3 offset:4864
v_mfma_f32_16x16x32_bf16 v[220:223],a[180:183],v[100:103],v[220:223]
v_exp_f32 v164,v164
v_mfma_f32_16x16x32_bf16 v[220:223],a[176:179],v[96:99],v[220:223]
s_waitcnt lgkmcnt(8)
v_exp_f32 v165,v165
v_mfma_f32_16x16x32_bf16 a[0:3],a[192:195],v[132:135],a[0:3]
v_exp_f32 v180,v180
v_mfma_f32_16x16x32_bf16 a[0:3],a[196:199],v[148:151],a[0:3]
v_exp_f32 v181,v181
v_mfma_f32_16x16x32_bf16 a[4:7],a[204:207],v[148:151],a[4:7]
v_exp_f32 v166,v166
v_mfma_f32_16x16x32_bf16 a[4:7],a[200:203],v[132:135],a[4:7]
v_exp_f32 v167,v167
v_mfma_f32_16x16x32_bf16 a[8:11],a[208:211],v[132:135],a[8:11]
s_memrealtime s[76:77]
v_exp_f32 v182,v182
v_mfma_f32_16x16x32_bf16 a[8:11],a[212:215],v[148:151],a[8:11]
v_exp_f32 v183,v183
v_mfma_f32_16x16x32_bf16 a[12:15],a[220:223],v[148:151],a[12:15]
v_cvt_pk_bf16_f32 v164,v164,v165
v_cvt_pk_bf16_f32 v180,v180,v181
v_mfma_f32_16x16x32_bf16 a[12:15],a[216:219],v[132:135],a[12:15]
v_exp_f32 v172,v172
v_mfma_f32_16x16x32_bf16 a[16:19],a[224:227],v[132:135],a[16:19]
v_exp_f32 v173,v173
v_mfma_f32_16x16x32_bf16 a[16:19],a[228:231],v[148:151],a[16:19]
v_exp_f32 v174,v174
v_mfma_f32_16x16x32_bf16 a[20:23],a[236:239],v[148:151],a[20:23]
v_exp_f32 v175,v175
v_mfma_f32_16x16x32_bf16 a[20:23],a[232:235],v[132:135],a[20:23]
s_waitcnt lgkmcnt(0)
v_exp_f32 v188,v188
v_mfma_f32_16x16x32_bf16 a[24:27],a[240:243],v[132:135],a[24:27]
s_sub_u32 s79,s78,s76
v_exp_f32 v189,v189
v_mfma_f32_16x16x32_bf16 a[24:27],a[244:247],v[148:151],a[24:27]
s_add_u32 s78,s76,s60
v_exp_f32 v190,v190
v_mfma_f32_16x16x32_bf16 a[28:31],a[252:255],v[148:151],a[28:31]
s_cmp_gt_i32 s79,0
s_cbranch_scc1 L_ps2_%=
L_pr2_%=:
v_cvt_pk_bf16_f32 v165,v166,v167
v_mfma_f32_16x16x32_bf16 a[28:31],a[248:251],v[132:135],a[28:31]
v_exp_f32 v191,v191
v_mfma_f32_16x16x32_bf16 v[48:51],v[28:31],v[132:135],v[48:51]
v_cvt_pk_bf16_f32 v181,v182,v183
v_cvt_pk_bf16_f32 v166,v172,v173
v_cvt_pk_bf16_f32 v167,v174,v175
v_cvt_pk_bf16_f32 v182,v188,v189
v_cvt_pk_bf16_f32 v183,v190,v191
v_mfma_f32_16x16x32_bf16 v[48:51],v[28:31],v[148:151],v[48:51]
s_mov_b32 s53,13
.p2align 5
L_att0_%=:
v_mfma_f32_16x16x32_bf16 v[228:231],a[128:131],v[112:115],v[44:47]
s_waitcnt vmcnt(8) lgkmcnt(0)
v_exp_f32 v196,v196
v_mfma_f32_16x16x32_bf16 v[228:231],a[132:135],v[116:119],v[228:231]
s_barrier
s_add_u32 m0,s49,s51
ds_read_b128 a[128:131],v1
v_mfma_f32_16x16x32_bf16 v[228:231],a[136:139],v[120:123],v[228:231]
s_mov_b32 s90,L_endseg_%=-L_pc_%=+128
v_exp_f32 v197,v197
v_mfma_f32_16x16x32_bf16 v[228:231],a[140:143],v[124:127],v[228:231]
s_lshr_b32 s45,s45,1
ds_read_b128 a[132:135],v1 offset:256
v_cvt_pk_bf16_f32 v196,v196,v197
v_mfma_f32_16x16x32_bf16 v[236:239],a[156:159],v[124:127],v[44:47]
s_add_u32 s62,s50,32768
buffer_load_dwordx4 v4,s[20:23],s47 offen nt lds
v_mfma_f32_16x16x32_bf16 v[236:239],a[152:155],v[120:123],v[236:239]
s_and_b32 s62,s62,131071
ds_read_b128 a[136:139],v1 offset:512
v_add_u32 v2,s48,v2
v_mfma_f32_16x16x32_bf16 v[236:239],a[148:151],v[116:119],v[236:239]
s_add_u32 s54,s54,16384
v_exp_f32 v198,v198
v_mfma_f32_16x16x32_bf16 v[236:239],a[144:147],v[112:115],v[236:239]
s_sub_u32 s53,s53,1
ds_read_b128 a[140:143],v1 offset:768
v_add_u32 v3,s48,v3
v_mfma_f32_16x16x32_bf16 v[244:247],a[160:163],v[112:115],v[44:47]
s_sub_u32 s48,s62,s50
v_exp_f32 v199,v199
v_mfma_f32_16x16x32_bf16 v[244:247],a[164:167],v[116:119],v[244:247]
s_and_b32 s46,s54,0xfffc0000
ds_read_b128 a[156:159],v1 offset:4864
v_cvt_pk_bf16_f32 v197,v198,v199
v_mfma_f32_16x16x32_bf16 v[244:247],a[168:171],v[120:123],v[244:247]
s_mov_b32 s50,s62
v_exp_f32 v204,v204
v_mfma_f32_16x16x32_bf16 v[244:247],a[172:175],v[124:127],v[244:247]
ds_read_b128 a[152:155],v1 offset:4608
v_mfma_f32_16x16x32_bf16 v[252:255],a[188:191],v[124:127],v[44:47]
v_exp_f32 v205,v205
v_mfma_f32_16x16x32_bf16 v[252:255],a[184:187],v[120:123],v[252:255]
ds_read_b128 a[148:151],v1 offset:4352
v_cvt_pk_bf16_f32 v198,v204,v205
v_mfma_f32_16x16x32_bf16 v[252:255],a[180:183],v[116:119],v[252:255]
v_exp_f32 v206,v206
v_mfma_f32_16x16x32_bf16 v[252:255],a[176:179],v[112:115],v[252:255]
ds_read_b128 a[144:147],v1 offset:4096
v_mfma_f32_16x16x32_bf16 a[32:35],a[192:195],v[164:167],a[32:35]
v_exp_f32 v207,v207
v_mfma_f32_16x16x32_bf16 a[32:35],a[196:199],v[180:183],a[32:35]
ds_read_b128 a[160:163],v1 offset:8192
v_cvt_pk_bf16_f32 v199,v206,v207
v_mfma_f32_16x16x32_bf16 a[36:39],a[204:207],v[180:183],a[36:39]
buffer_load_dwordx4 v4,s[20:23],s47 offen offset:1024 nt lds
v_mfma_f32_16x16x32_bf16 a[36:39],a[200:203],v[164:167],a[36:39]
ds_read_b128 a[164:167],v1 offset:8448
v_mfma_f32_16x16x32_bf16 a[40:43],a[208:211],v[164:167],a[40:43]
v_exp_f32 v212,v212
v_mfma_f32_16x16x32_bf16 a[40:43],a[212:215],v[180:183],a[40:43]
ds_read_b128 a[168:171],v1 offset:8704
v_mfma_f32_16x16x32_bf16 a[44:47],a[220:223],v[180:183],a[44:47]
v_exp_f32 v213,v213
v_mfma_f32_16x16x32_bf16 a[44:47],a[216:219],v[164:167],a[44:47]
ds_read_b128 a[172:175],v1 offset:8960
v_cvt_pk_bf16_f32 v212,v212,v213
v_mfma_f32_16x16x32_bf16 a[48:51],a[224:227],v[164:167],a[48:51]
v_exp_f32 v214,v214
v_mfma_f32_16x16x32_bf16 a[48:51],a[228:231],v[180:183],a[48:51]
ds_read_b128 a[188:191],v1 offset:13056
v_mfma_f32_16x16x32_bf16 a[52:55],a[236:239],v[180:183],a[52:55]
v_exp_f32 v215,v215
v_mfma_f32_16x16x32_bf16 a[52:55],a[232:235],v[164:167],a[52:55]
ds_read_b128 a[184:187],v1 offset:12800
v_cvt_pk_bf16_f32 v213,v214,v215
v_mfma_f32_16x16x32_bf16 a[56:59],a[240:243],v[164:167],a[56:59]
v_exp_f32 v220,v220
v_mfma_f32_16x16x32_bf16 a[56:59],a[244:247],v[180:183],a[56:59]
)A"
R"A(ds_read_b128 a[180:183],v1 offset:12544
v_mfma_f32_16x16x32_bf16 a[60:63],a[252:255],v[180:183],a[60:63]
v_exp_f32 v221,v221
v_mfma_f32_16x16x32_bf16 a[60:63],a[248:251],v[164:167],a[60:63]
ds_read_b128 a[176:179],v1 offset:12288
v_cvt_pk_bf16_f32 v214,v220,v221
v_mfma_f32_16x16x32_bf16 v[52:55],v[28:31],v[164:167],v[52:55]
buffer_load_dwordx4 v5,s[20:23],s47 offen offset:2048 nt lds
v_mfma_f32_16x16x32_bf16 v[52:55],v[28:31],v[180:183],v[52:55]
s_waitcnt lgkmcnt(3)
v_exp_f32 v222,v222
v_mfma_f32_16x16x32_bf16 v[132:135],a[128:131],v[64:67],v[32:35]
v_exp_f32 v223,v223
v_mfma_f32_16x16x32_bf16 v[132:135],a[132:135],v[68:71],v[132:135]
v_cvt_pk_bf16_f32 v215,v222,v223
v_add_u32 v1,s48,v1
v_mfma_f32_16x16x32_bf16 v[132:135],a[136:139],v[72:75],v[132:135]
v_exp_f32 v228,v228
v_mfma_f32_16x16x32_bf16 v[132:135],a[140:143],v[76:79],v[132:135]
v_exp_f32 v229,v229
v_mfma_f32_16x16x32_bf16 v[140:143],a[156:159],v[76:79],v[32:35]
v_exp_f32 v230,v230
v_mfma_f32_16x16x32_bf16 v[140:143],a[152:155],v[72:75],v[140:143]
v_exp_f32 v231,v231
v_mfma_f32_16x16x32_bf16 v[140:143],a[148:151],v[68:71],v[140:143]
v_cvt_pk_bf16_f32 v228,v228,v229
v_cvt_pk_bf16_f32 v229,v230,v231
v_mfma_f32_16x16x32_bf16 v[140:143],a[144:147],v[64:67],v[140:143]
v_exp_f32 v236,v236
v_mfma_f32_16x16x32_bf16 v[148:151],a[160:163],v[64:67],v[32:35]
v_exp_f32 v237,v237
v_mfma_f32_16x16x32_bf16 v[148:151],a[164:167],v[68:71],v[148:151]
v_exp_f32 v238,v238
v_mfma_f32_16x16x32_bf16 v[148:151],a[168:171],v[72:75],v[148:151]
v_exp_f32 v239,v239
v_mfma_f32_16x16x32_bf16 v[148:151],a[172:175],v[76:79],v[148:151]
v_exp_f32 v244,v244
v_mfma_f32_16x16x32_bf16 v[156:159],a[188:191],v[76:79],v[32:35]
s_waitcnt lgkmcnt(0)
buffer_load_dwordx4 v5,s[20:23],s47 offen offset:3072 nt lds
v_mfma_f32_16x16x32_bf16 v[156:159],a[184:187],v[72:75],v[156:159]
v_exp_f32 v245,v245
v_mfma_f32_16x16x32_bf16 v[156:159],a[180:183],v[68:71],v[156:159]
v_exp_f32 v246,v246
v_mfma_f32_16x16x32_bf16 v[156:159],a[176:179],v[64:67],v[156:159]
v_exp_f32 v247,v247
v_mfma_f32_16x16x32_bf16 a[64:67],a[192:195],v[196:199],a[64:67]
v_cvt_pk_bf16_f32 v230,v236,v237
v_cvt_pk_bf16_f32 v231,v238,v239
v_mfma_f32_16x16x32_bf16 a[64:67],a[196:199],v[212:215],a[64:67]
v_cvt_pk_bf16_f32 v244,v244,v245
v_cvt_pk_bf16_f32 v245,v246,v247
v_mfma_f32_16x16x32_bf16 a[68:71],a[204:207],v[212:215],a[68:71]
v_exp_f32 v252,v252
v_mfma_f32_16x16x32_bf16 a[68:71],a[200:203],v[196:199],a[68:71]
v_exp_f32 v253,v253
v_mfma_f32_16x16x32_bf16 a[72:75],a[208:211],v[196:199],a[72:75]
v_exp_f32 v254,v254
v_mfma_f32_16x16x32_bf16 a[72:75],a[212:215],v[212:215],a[72:75]
v_exp_f32 v255,v255
v_mfma_f32_16x16x32_bf16 a[76:79],a[220:223],v[212:215],a[76:79]
v_cvt_pk_bf16_f32 v246,v252,v253
v_cvt_pk_bf16_f32 v247,v254,v255
v_mfma_f32_16x16x32_bf16 a[76:79],a[216:219],v[196:199],a[76:79]
s_add_u32 m0,s49,s52
v_exp_f32 v132,v132
v_mfma_f32_16x16x32_bf16 a[80:83],a[224:227],v[196:199],a[80:83]
s_xor_b32 s49,s50,65536
v_exp_f32 v133,v133
v_mfma_f32_16x16x32_bf16 a[80:83],a[228:231],v[212:215],a[80:83]
v_exp_f32 v134,v134
v_mfma_f32_16x16x32_bf16 a[84:87],a[236:239],v[212:215],a[84:87]
buffer_load_dwordx4 v4,s[24:27],s47 offen nt lds
v_mfma_f32_16x16x32_bf16 a[84:87],a[232:235],v[196:199],a[84:87]
v_exp_f32 v135,v135
v_mfma_f32_16x16x32_bf16 a[88:91],a[240:243],v[196:199],a[88:91]
v_cvt_pk_bf16_f32 v132,v132,v133
v_cvt_pk_bf16_f32 v133,v134,v135
v_mfma_f32_16x16x32_bf16 a[88:91],a[244:247],v[212:215],a[88:91]
v_exp_f32 v140,v140
v_mfma_f32_16x16x32_bf16 a[92:95],a[252:255],v[212:215],a[92:95]
v_exp_f32 v141,v141
v_mfma_f32_16x16x32_bf16 a[92:95],a[248:251],v[196:199],a[92:95]
v_exp_f32 v142,v142
v_mfma_f32_16x16x32_bf16 v[56:59],v[28:31],v[196:199],v[56:59]
v_exp_f32 v143,v143
v_mfma_f32_16x16x32_bf16 v[56:59],v[28:31],v[212:215],v[56:59]
v_exp_f32 v148,v148
v_mfma_f32_16x16x32_bf16 v[164:167],a[128:131],v[80:83],v[36:39]
v_exp_f32 v149,v149
v_mfma_f32_16x16x32_bf16 v[164:167],a[132:135],v[84:87],v[164:167]
v_exp_f32 v150,v150
v_mfma_f32_16x16x32_bf16 v[164:167],a[136:139],v[88:91],v[164:167]
v_exp_f32 v151,v151
v_mfma_f32_16x16x32_bf16 v[164:167],a[140:143],v[92:95],v[164:167]
v_cvt_pk_bf16_f32 v134,v140,v141
v_cvt_pk_bf16_f32 v135,v142,v143
v_mfma_f32_16x16x32_bf16 v[172:175],a[156:159],v[92:95],v[36:39]
v_cvt_pk_bf16_f32 v148,v148,v149
v_cvt_pk_bf16_f32 v149,v150,v151
v_mfma_f32_16x16x32_bf16 v[172:175],a[152:155],v[88:91],v[172:175]
v_exp_f32 v156,v156
v_mfma_f32_16x16x32_bf16 v[172:175],a[148:151],v[84:87],v[172:175]
buffer_load_dwordx4 v5,s[24:27],s47 offen offset:1024 nt lds
v_mfma_f32_16x16x32_bf16 v[172:175],a[144:147],v[80:83],v[172:175]
v_exp_f32 v157,v157
v_mfma_f32_16x16x32_bf16 v[180:183],a[160:163],v[80:83],v[36:39]
v_exp_f32 v158,v158
v_mfma_f32_16x16x32_bf16 v[180:183],a[164:167],v[84:87],v[180:183]
v_exp_f32 v159,v159
v_mfma_f32_16x16x32_bf16 v[180:183],a[168:171],v[88:91],v[180:183]
v_cvt_pk_bf16_f32 v150,v156,v157
v_cvt_pk_bf16_f32 v151,v158,v159
v_mfma_f32_16x16x32_bf16 v[180:183],a[172:175],v[92:95],v[180:183]
v_exp_f32 v164,v164
v_mfma_f32_16x16x32_bf16 v[188:191],a[188:191],v[92:95],v[36:39]
v_exp_f32 v165,v165
v_mfma_f32_16x16x32_bf16 v[188:191],a[184:187],v[88:91],v[188:191]
v_exp_f32 v166,v166
v_mfma_f32_16x16x32_bf16 v[188:191],a[180:183],v[84:87],v[188:191]
v_exp_f32 v167,v167
v_mfma_f32_16x16x32_bf16 v[188:191],a[176:179],v[80:83],v[188:191]
v_cvt_pk_bf16_f32 v164,v164,v165
v_cvt_pk_bf16_f32 v165,v166,v167
v_mfma_f32_16x16x32_bf16 a[96:99],a[192:195],v[228:231],a[96:99]
ds_read_b64_tr_b16 a[192:193],v2
v_mfma_f32_16x16x32_bf16 a[96:99],a[196:199],v[244:247],a[96:99]
ds_read_b64_tr_b16 a[194:195],v2 offset:4096
v_mfma_f32_16x16x32_bf16 a[100:103],a[204:207],v[244:247],a[100:103]
ds_read_b64_tr_b16 a[196:197],v2 offset:8192
v_mfma_f32_16x16x32_bf16 a[100:103],a[200:203],v[228:231],a[100:103]
ds_read_b64_tr_b16 a[198:199],v2 offset:12288
v_mfma_f32_16x16x32_bf16 a[104:107],a[208:211],v[228:231],a[104:107]
buffer_load_dwordx4 v4,s[24:27],s47 offen offset:2048 nt lds
v_mfma_f32_16x16x32_bf16 a[104:107],a[212:215],v[244:247],a[104:107]
ds_read_b64_tr_b16 a[204:205],v3 offset:8192
v_mfma_f32_16x16x32_bf16 a[108:111],a[220:223],v[244:247],a[108:111]
ds_read_b64_tr_b16 a[206:207],v3 offset:12288
v_mfma_f32_16x16x32_bf16 a[108:111],a[216:219],v[228:231],a[108:111]
ds_read_b64_tr_b16 a[200:201],v3
v_mfma_f32_16x16x32_bf16 a[112:115],a[224:227],v[228:231],a[112:115]
ds_read_b64_tr_b16 a[202:203],v3 offset:4096
v_mfma_f32_16x16x32_bf16 a[112:115],a[228:231],v[244:247],a[112:115]
ds_read_b64_tr_b16 a[208:209],v2 offset:256
v_mfma_f32_16x16x32_bf16 a[116:119],a[236:239],v[244:247],a[116:119]
ds_read_b64_tr_b16 a[210:211],v2 offset:4352
v_mfma_f32_16x16x32_bf16 a[116:119],a[232:235],v[228:231],a[116:119]
ds_read_b64_tr_b16 a[212:213],v2 offset:8448
v_mfma_f32_16x16x32_bf16 a[120:123],a[240:243],v[228:231],a[120:123]
ds_read_b64_tr_b16 a[214:215],v2 offset:12544
v_mfma_f32_16x16x32_bf16 a[120:123],a[244:247],v[244:247],a[120:123]
ds_read_b64_tr_b16 a[220:221],v3 offset:8448
v_mfma_f32_16x16x32_bf16 a[124:127],a[252:255],v[244:247],a[124:127]
ds_read_b64_tr_b16 a[222:223],v3 offset:12544
v_mfma_f32_16x16x32_bf16 a[124:127],a[248:251],v[228:231],a[124:127]
ds_read_b64_tr_b16 a[216:217],v3 offset:256
v_mfma_f32_16x16x32_bf16 v[60:63],v[28:31],v[228:231],v[60:63]
ds_read_b64_tr_b16 a[218:219],v3 offset:4352
v_mfma_f32_16x16x32_bf16 v[60:63],v[28:31],v[244:247],v[60:63]
ds_read_b64_tr_b16 a[224:225],v2 offset:512
v_mfma_f32_16x16x32_bf16 v[196:199],a[128:131],v[96:99],v[40:43]
buffer_load_dwordx4 v5,s[24:27],s47 offen offset:3072 nt lds
v_mfma_f32_16x16x32_bf16 v[196:199],a[132:135],v[100:103],v[196:199]
s_add_u32 s47,s47,16384
s_and_b32 s47,s47,0x3ffff
ds_read_b64_tr_b16 a[226:227],v2 offset:4608
v_mfma_f32_16x16x32_bf16 v[196:199],a[136:139],v[104:107],v[196:199]
s_or_b32 s47,s47,s46
ds_read_b64_tr_b16 a[228:229],v2 offset:8704
v_mfma_f32_16x16x32_bf16 v[196:199],a[140:143],v[108:111],v[196:199]
ds_read_b64_tr_b16 a[230:231],v2 offset:12800
v_mfma_f32_16x16x32_bf16 v[204:207],a[156:159],v[108:111],v[40:43]
ds_read_b64_tr_b16 a[236:237],v3 offset:8704
v_mfma_f32_16x16x32_bf16 v[204:207],a[152:155],v[104:107],v[204:207]
ds_read_b64_tr_b16 a[238:239],v3 offset:12800
v_mfma_f32_16x16x32_bf16 v[204:207],a[148:151],v[100:103],v[204:207]
ds_read_b64_tr_b16 a[232:233],v3 offset:512
v_mfma_f32_16x16x32_bf16 v[204:207],a[144:147],v[96:99],v[204:207]
ds_read_b64_tr_b16 a[234:235],v3 offset:4608
v_mfma_f32_16x16x32_bf16 v[212:215],a[160:163],v[96:99],v[40:43]
ds_read_b64_tr_b16 a[240:241],v2 offset:768
v_mfma_f32_16x16x32_bf16 v[212:215],a[164:167],v[100:103],v[212:215]
ds_read_b64_tr_b16 a[242:243],v2 offset:4864
v_mfma_f32_16x16x32_bf16 v[212:215],a[168:171],v[104:107],v[212:215]
ds_read_b64_tr_b16 a[244:245],v2 offset:8960
v_mfma_f32_16x16x32_bf16 v[212:215],a[172:175],v[108:111],v[212:215]
ds_read_b64_tr_b16 a[246:247],v2 offset:13056
v_mfma_f32_16x16x32_bf16 v[220:223],a[188:191],v[108:111],v[40:43]
ds_read_b64_tr_b16 a[252:253],v3 offset:8960
v_mfma_f32_16x16x32_bf16 v[220:223],a[184:187],v[104:107],v[220:223]
ds_read_b64_tr_b16 a[254:255],v3 offset:13056
v_mfma_f32_16x16x32_bf16 v[220:223],a[180:183],v[100:103],v[220:223]
ds_read_b64_tr_b16 a[248:249],v3 offset:768
v_mfma_f32_16x16x32_bf16 v[220:223],a[176:179],v[96:99],v[220:223]
s_waitcnt lgkmcnt(7)
ds_read_b64_tr_b16 a[250:251],v3 offset:4864
v_mfma_f32_16x16x32_bf16 a[0:3],a[192:195],v[132:135],a[0:3]
buffer_load_dword v26,v24,s[88:91],s93 offen
v_mfma_f32_16x16x32_bf16 a[0:3],a[196:199],v[148:151],a[0:3]
v_exp_f32 v180,v180
v_mfma_f32_16x16x32_bf16 a[4:7],a[204:207],v[148:151],a[4:7]
v_exp_f32 v181,v181
v_mfma_f32_16x16x32_bf16 a[4:7],a[200:203],v[132:135],a[4:7]
v_exp_f32 v182,v182
v_mfma_f32_16x16x32_bf16 a[8:11],a[208:211],v[132:135],a[8:11]
s_memrealtime s[76:77]
v_exp_f32 v183,v183
v_mfma_f32_16x16x32_bf16 a[8:11],a[212:215],v[148:151],a[8:11]
v_exp_f32 v172,v172
v_mfma_f32_16x16x32_bf16 a[12:15],a[220:223],v[148:151],a[12:15]
v_exp_f32 v173,v173
v_mfma_f32_16x16x32_bf16 a[12:15],a[216:219],v[132:135],a[12:15]
v_exp_f32 v174,v174
v_mfma_f32_16x16x32_bf16 a[16:19],a[224:227],v[132:135],a[16:19]
s_bitcmp1_b32 s45,0
s_cbranch_scc0 L_t310_%=
s_mov_b32 m0,s80
s_add_u32 s80,s80,4096
buffer_load_dwordx4 v25,s[20:23],s83 offen nt lds
s_add_u32 s83,s83,4096
L_t310_%=:
v_mfma_f32_16x16x32_bf16 a[16:19],a[228:231],v[148:151],a[16:19]
v_exp_f32 v175,v175
v_mfma_f32_16x16x32_bf16 a[20:23],a[236:239],v[148:151],a[20:23]
v_cvt_pk_bf16_f32 v180,v180,v181
v_cvt_pk_bf16_f32 v181,v182,v183
v_mfma_f32_16x16x32_bf16 a[20:23],a[232:235],v[132:135],a[20:23]
s_waitcnt lgkmcnt(0)
v_exp_f32 v188,v188
v_mfma_f32_16x16x32_bf16 a[24:27],a[240:243],v[132:135],a[24:27]
s_sub_u32 s79,s78,s76
v_exp_f32 v189,v189
v_mfma_f32_16x16x32_bf16 a[24:27],a[244:247],v[148:151],a[24:27]
s_add_u32 s78,s76,s60
v_exp_f32 v190,v190
v_mfma_f32_16x16x32_bf16 a[28:31],a[252:255],v[148:151],a[28:31]
s_cmp_gt_i32 s79,0
s_cbranch_scc1 L_ps3_%=
L_pr3_%=:
v_cvt_pk_bf16_f32 v166,v172,v173
v_mfma_f32_16x16x32_bf16 a[28:31],a[248:251],v[132:135],a[28:31]
v_exp_f32 v191,v191
v_mfma_f32_16x16x32_bf16 v[48:51],v[28:31],v[132:135],v[48:51]
v_cvt_pk_bf16_f32 v167,v174,v175
v_cvt_pk_bf16_f32 v182,v188,v189
v_cvt_pk_bf16_f32 v183,v190,v191
s_cmp_lg_u32 s53,0
v_mfma_f32_16x16x32_bf16 v[48:51],v[28:31],v[148:151],v[48:51]
s_cbranch_scc1 L_att0_%=
L_att0x_%=:
v_mfma_f32_16x16x32_bf16 v[228:231],a[128:131],v[112:115],v[44:47]
s_waitcnt vmcnt(10)
s_add_u32 s54,s54,16384
v_exp_f32 v196,v196
v_mfma_f32_16x16x32_bf16 v[228:231],a[132:135],v[116:119],v[228:231]
s_barrier
s_add_u32 m0,0,s51
ds_read_b128 a[128:131],v1
v_mfma_f32_16x16x32_bf16 v[228:231],a[136:139],v[120:123],v[228:231]
s_and_b32 s46,s54,0xfffc0000
ds_read_b128 a[132:135],v1 offset:256
v_and_b32 v18,48,v6
v_mfma_f32_16x16x32_bf16 v[228:231],a[140:143],v[124:127],v[228:231]
s_add_u32 s54,s54,16384
ds_read_b128 a[136:139],v1 offset:512
v_lshrrev_b32 v19,4,v6
v_mfma_f32_16x16x32_bf16 v[236:239],a[156:159],v[124:127],v[44:47]
s_mul_i32 s83,s41,5
buffer_load_dwordx4 v4,s[20:23],s47 offen nt lds
)A"
R"A(v_mfma_f32_16x16x32_bf16 v[236:239],a[152:155],v[120:123],v[236:239]
s_lshl_b32 s44,s40,5
ds_read_b128 a[140:143],v1 offset:768
v_lshlrev_b32 v18,2,v18
v_mfma_f32_16x16x32_bf16 v[236:239],a[148:151],v[116:119],v[236:239]
s_add_u32 s83,s83,s42
ds_read_b128 a[156:159],v1 offset:4864
v_and_b32 v19,48,v19
v_mfma_f32_16x16x32_bf16 v[236:239],a[144:147],v[112:115],v[236:239]
s_add_u32 s84,s55,0
ds_read_b128 a[152:155],v1 offset:4608
v_add_u32 v18,v18,v19
v_mfma_f32_16x16x32_bf16 v[244:247],a[160:163],v[112:115],v[44:47]
s_add_u32 s85,s55,4096
ds_read_b128 a[148:151],v1 offset:4352
v_add_u32 v18,98304,v18
v_mfma_f32_16x16x32_bf16 v[244:247],a[164:167],v[116:119],v[244:247]
s_add_u32 s86,s55,8192
ds_read_b128 a[144:147],v1 offset:4096
v_add_u32 v2,s48,v2
v_mfma_f32_16x16x32_bf16 v[244:247],a[168:171],v[120:123],v[244:247]
s_add_u32 s87,s55,12288
ds_read_b128 a[160:163],v1 offset:8192
v_add_u32 v3,s48,v3
v_mfma_f32_16x16x32_bf16 v[244:247],a[172:175],v[124:127],v[244:247]
s_mov_b32 s48,32768
s_and_b32 s44,s44,32
ds_read_b128 a[164:167],v1 offset:8448
v_mfma_f32_16x16x32_bf16 v[252:255],a[188:191],v[124:127],v[44:47]
s_add_u32 s83,s83,15
ds_read_b128 a[168:171],v1 offset:8704
v_subrev_u32 v25,s51,v4
v_mfma_f32_16x16x32_bf16 v[252:255],a[184:187],v[120:123],v[252:255]
s_max_i32 s84,s84,s18
s_max_i32 s85,s85,s19
ds_read_b128 a[172:175],v1 offset:8960
v_mfma_f32_16x16x32_bf16 v[252:255],a[180:183],v[116:119],v[252:255]
s_max_i32 s86,s86,s100
s_max_i32 s87,s87,s101
ds_read_b128 a[188:191],v1 offset:13056
v_mfma_f32_16x16x32_bf16 v[252:255],a[176:179],v[112:115],v[252:255]
s_lshl_b32 s80,s40,10
ds_read_b128 a[184:187],v1 offset:12800
v_xor_b32 v25,s44,v25
v_mfma_f32_16x16x32_bf16 a[32:35],a[192:195],v[164:167],a[32:35]
s_and_b32 s83,s83,15
ds_read_b128 a[180:183],v1 offset:12544
v_add_u32 v25,s80,v25
v_mfma_f32_16x16x32_bf16 a[32:35],a[196:199],v[180:183],a[32:35]
s_add_u32 s83,s83,16
ds_read_b128 a[176:179],v1 offset:12288
v_add_u32 v1,s48,v1
v_mfma_f32_16x16x32_bf16 a[36:39],a[204:207],v[180:183],a[36:39]
s_mov_b32 s50,32768
buffer_load_dwordx4 v4,s[20:23],s47 offen offset:1024 nt lds
v_mfma_f32_16x16x32_bf16 a[36:39],a[200:203],v[164:167],a[36:39]
s_mov_b32 s49,98304
v_exp_f32 v197,v197
v_mfma_f32_16x16x32_bf16 a[40:43],a[208:211],v[164:167],a[40:43]
s_mov_b32 s45,0x2a8
v_exp_f32 v198,v198
v_mfma_f32_16x16x32_bf16 a[40:43],a[212:215],v[180:183],a[40:43]
s_add_u32 s80,s80,147456
v_exp_f32 v199,v199
v_mfma_f32_16x16x32_bf16 a[44:47],a[220:223],v[180:183],a[44:47]
s_lshl_b32 s83,s83,14
v_cvt_pk_bf16_f32 v196,v196,v197
v_cvt_pk_bf16_f32 v197,v198,v199
v_mfma_f32_16x16x32_bf16 a[44:47],a[216:219],v[164:167],a[44:47]
s_add_u32 s93,s92,L_endseg_%=-L_pc_%=
v_exp_f32 v204,v204
v_mfma_f32_16x16x32_bf16 a[48:51],a[224:227],v[164:167],a[48:51]
v_exp_f32 v205,v205
v_mfma_f32_16x16x32_bf16 a[48:51],a[228:231],v[180:183],a[48:51]
v_exp_f32 v206,v206
v_mfma_f32_16x16x32_bf16 a[52:55],a[236:239],v[180:183],a[52:55]
v_exp_f32 v207,v207
v_mfma_f32_16x16x32_bf16 a[52:55],a[232:235],v[164:167],a[52:55]
v_exp_f32 v212,v212
v_mfma_f32_16x16x32_bf16 a[56:59],a[240:243],v[164:167],a[56:59]
v_exp_f32 v213,v213
v_mfma_f32_16x16x32_bf16 a[56:59],a[244:247],v[180:183],a[56:59]
v_exp_f32 v214,v214
v_mfma_f32_16x16x32_bf16 a[60:63],a[252:255],v[180:183],a[60:63]
v_exp_f32 v215,v215
v_mfma_f32_16x16x32_bf16 a[60:63],a[248:251],v[164:167],a[60:63]
v_cvt_pk_bf16_f32 v198,v204,v205
v_cvt_pk_bf16_f32 v199,v206,v207
v_mfma_f32_16x16x32_bf16 v[52:55],v[28:31],v[164:167],v[52:55]
buffer_load_dwordx4 v5,s[20:23],s47 offen offset:2048 nt lds
v_mfma_f32_16x16x32_bf16 v[52:55],v[28:31],v[180:183],v[52:55]
s_waitcnt lgkmcnt(0)
v_cvt_pk_bf16_f32 v212,v212,v213
v_cvt_pk_bf16_f32 v213,v214,v215
v_mfma_f32_16x16x32_bf16 v[132:135],a[128:131],v[64:67],v[32:35]
v_exp_f32 v220,v220
v_mfma_f32_16x16x32_bf16 v[132:135],a[132:135],v[68:71],v[132:135]
v_exp_f32 v221,v221
v_mfma_f32_16x16x32_bf16 v[132:135],a[136:139],v[72:75],v[132:135]
v_exp_f32 v222,v222
v_mfma_f32_16x16x32_bf16 v[132:135],a[140:143],v[76:79],v[132:135]
v_exp_f32 v223,v223
v_mfma_f32_16x16x32_bf16 v[140:143],a[156:159],v[76:79],v[32:35]
v_cvt_pk_bf16_f32 v214,v220,v221
v_cvt_pk_bf16_f32 v215,v222,v223
v_mfma_f32_16x16x32_bf16 v[140:143],a[152:155],v[72:75],v[140:143]
v_exp_f32 v228,v228
v_mfma_f32_16x16x32_bf16 v[140:143],a[148:151],v[68:71],v[140:143]
v_exp_f32 v229,v229
v_mfma_f32_16x16x32_bf16 v[140:143],a[144:147],v[64:67],v[140:143]
v_exp_f32 v230,v230
v_mfma_f32_16x16x32_bf16 v[148:151],a[160:163],v[64:67],v[32:35]
v_exp_f32 v231,v231
v_mfma_f32_16x16x32_bf16 v[148:151],a[164:167],v[68:71],v[148:151]
v_cvt_pk_bf16_f32 v228,v228,v229
v_cvt_pk_bf16_f32 v229,v230,v231
v_mfma_f32_16x16x32_bf16 v[148:151],a[168:171],v[72:75],v[148:151]
v_exp_f32 v236,v236
v_mfma_f32_16x16x32_bf16 v[148:151],a[172:175],v[76:79],v[148:151]
v_exp_f32 v237,v237
v_mfma_f32_16x16x32_bf16 v[156:159],a[188:191],v[76:79],v[32:35]
buffer_load_dwordx4 v5,s[20:23],s47 offen offset:3072 nt lds
v_mfma_f32_16x16x32_bf16 v[156:159],a[184:187],v[72:75],v[156:159]
v_exp_f32 v238,v238
v_mfma_f32_16x16x32_bf16 v[156:159],a[180:183],v[68:71],v[156:159]
v_exp_f32 v239,v239
v_mfma_f32_16x16x32_bf16 v[156:159],a[176:179],v[64:67],v[156:159]
v_exp_f32 v244,v244
v_mfma_f32_16x16x32_bf16 a[64:67],a[192:195],v[196:199],a[64:67]
v_exp_f32 v245,v245
v_mfma_f32_16x16x32_bf16 a[64:67],a[196:199],v[212:215],a[64:67]
v_exp_f32 v246,v246
v_mfma_f32_16x16x32_bf16 a[68:71],a[204:207],v[212:215],a[68:71]
v_exp_f32 v247,v247
v_mfma_f32_16x16x32_bf16 a[68:71],a[200:203],v[196:199],a[68:71]
v_cvt_pk_bf16_f32 v230,v236,v237
v_cvt_pk_bf16_f32 v231,v238,v239
v_mfma_f32_16x16x32_bf16 a[72:75],a[208:211],v[196:199],a[72:75]
v_cvt_pk_bf16_f32 v244,v244,v245
v_cvt_pk_bf16_f32 v245,v246,v247
v_mfma_f32_16x16x32_bf16 a[72:75],a[212:215],v[212:215],a[72:75]
v_exp_f32 v252,v252
v_mfma_f32_16x16x32_bf16 a[76:79],a[220:223],v[212:215],a[76:79]
v_exp_f32 v253,v253
v_mfma_f32_16x16x32_bf16 a[76:79],a[216:219],v[196:199],a[76:79]
s_add_u32 m0,0,s52
v_exp_f32 v254,v254
v_mfma_f32_16x16x32_bf16 a[80:83],a[224:227],v[196:199],a[80:83]
v_exp_f32 v255,v255
v_mfma_f32_16x16x32_bf16 a[80:83],a[228:231],v[212:215],a[80:83]
v_cvt_pk_bf16_f32 v246,v252,v253
v_cvt_pk_bf16_f32 v247,v254,v255
v_mfma_f32_16x16x32_bf16 a[84:87],a[236:239],v[212:215],a[84:87]
buffer_load_dwordx4 v4,s[24:27],s47 offen nt lds
v_mfma_f32_16x16x32_bf16 a[84:87],a[232:235],v[196:199],a[84:87]
v_exp_f32 v132,v132
v_mfma_f32_16x16x32_bf16 a[88:91],a[240:243],v[196:199],a[88:91]
v_exp_f32 v133,v133
v_mfma_f32_16x16x32_bf16 a[88:91],a[244:247],v[212:215],a[88:91]
v_exp_f32 v134,v134
v_mfma_f32_16x16x32_bf16 a[92:95],a[252:255],v[212:215],a[92:95]
v_exp_f32 v135,v135
v_mfma_f32_16x16x32_bf16 a[92:95],a[248:251],v[196:199],a[92:95]
v_cvt_pk_bf16_f32 v132,v132,v133
v_cvt_pk_bf16_f32 v133,v134,v135
v_mfma_f32_16x16x32_bf16 v[56:59],v[28:31],v[196:199],v[56:59]
v_exp_f32 v140,v140
v_mfma_f32_16x16x32_bf16 v[56:59],v[28:31],v[212:215],v[56:59]
v_exp_f32 v141,v141
v_mfma_f32_16x16x32_bf16 v[164:167],a[128:131],v[80:83],v[36:39]
v_exp_f32 v142,v142
v_mfma_f32_16x16x32_bf16 v[164:167],a[132:135],v[84:87],v[164:167]
v_exp_f32 v143,v143
v_mfma_f32_16x16x32_bf16 v[164:167],a[136:139],v[88:91],v[164:167]
v_exp_f32 v148,v148
v_mfma_f32_16x16x32_bf16 v[164:167],a[140:143],v[92:95],v[164:167]
v_exp_f32 v149,v149
v_mfma_f32_16x16x32_bf16 v[172:175],a[156:159],v[92:95],v[36:39]
v_exp_f32 v150,v150
v_mfma_f32_16x16x32_bf16 v[172:175],a[152:155],v[88:91],v[172:175]
v_exp_f32 v151,v151
v_mfma_f32_16x16x32_bf16 v[172:175],a[148:151],v[84:87],v[172:175]
buffer_load_dwordx4 v5,s[24:27],s47 offen offset:1024 nt lds
v_mfma_f32_16x16x32_bf16 v[172:175],a[144:147],v[80:83],v[172:175]
v_cvt_pk_bf16_f32 v134,v140,v141
v_cvt_pk_bf16_f32 v135,v142,v143
v_mfma_f32_16x16x32_bf16 v[180:183],a[160:163],v[80:83],v[36:39]
v_cvt_pk_bf16_f32 v148,v148,v149
v_cvt_pk_bf16_f32 v149,v150,v151
v_mfma_f32_16x16x32_bf16 v[180:183],a[164:167],v[84:87],v[180:183]
v_exp_f32 v156,v156
v_mfma_f32_16x16x32_bf16 v[180:183],a[168:171],v[88:91],v[180:183]
v_exp_f32 v157,v157
v_mfma_f32_16x16x32_bf16 v[180:183],a[172:175],v[92:95],v[180:183]
v_exp_f32 v158,v158
v_mfma_f32_16x16x32_bf16 v[188:191],a[188:191],v[92:95],v[36:39]
v_exp_f32 v159,v159
v_mfma_f32_16x16x32_bf16 v[188:191],a[184:187],v[88:91],v[188:191]
v_cvt_pk_bf16_f32 v150,v156,v157
v_cvt_pk_bf16_f32 v151,v158,v159
v_mfma_f32_16x16x32_bf16 v[188:191],a[180:183],v[84:87],v[188:191]
s_waitcnt vmcnt(14)
v_exp_f32 v164,v164
v_mfma_f32_16x16x32_bf16 v[188:191],a[176:179],v[80:83],v[188:191]
s_barrier
ds_read_b128 v[80:83],v18
v_mfma_f32_16x16x32_bf16 a[96:99],a[192:195],v[228:231],a[96:99]
ds_read_b128 v[84:87],v18 offset:256
v_mfma_f32_16x16x32_bf16 a[96:99],a[196:199],v[244:247],a[96:99]
ds_read_b128 v[88:91],v18 offset:512
v_mfma_f32_16x16x32_bf16 a[100:103],a[204:207],v[244:247],a[100:103]
ds_read_b128 v[92:95],v18 offset:768
v_mfma_f32_16x16x32_bf16 a[100:103],a[200:203],v[228:231],a[100:103]
buffer_load_dwordx4 v4,s[24:27],s47 offen offset:2048 nt lds
v_mfma_f32_16x16x32_bf16 a[104:107],a[208:211],v[228:231],a[104:107]
ds_read_b64_tr_b16 a[192:193],v2
ds_read_b64_tr_b16 a[194:195],v2 offset:4096
v_mfma_f32_16x16x32_bf16 a[104:107],a[212:215],v[244:247],a[104:107]
ds_read_b64_tr_b16 a[196:197],v2 offset:8192
ds_read_b64_tr_b16 a[198:199],v2 offset:12288
v_mfma_f32_16x16x32_bf16 a[108:111],a[220:223],v[244:247],a[108:111]
ds_read_b64_tr_b16 a[204:205],v3 offset:8192
ds_read_b64_tr_b16 a[206:207],v3 offset:12288
v_mfma_f32_16x16x32_bf16 a[108:111],a[216:219],v[228:231],a[108:111]
ds_read_b64_tr_b16 a[200:201],v3
ds_read_b64_tr_b16 a[202:203],v3 offset:4096
v_mfma_f32_16x16x32_bf16 a[112:115],a[224:227],v[228:231],a[112:115]
ds_read_b64_tr_b16 a[208:209],v2 offset:256
ds_read_b64_tr_b16 a[210:211],v2 offset:4352
v_mfma_f32_16x16x32_bf16 a[112:115],a[228:231],v[244:247],a[112:115]
ds_read_b64_tr_b16 a[212:213],v2 offset:8448
ds_read_b64_tr_b16 a[214:215],v2 offset:12544
v_mfma_f32_16x16x32_bf16 a[116:119],a[236:239],v[244:247],a[116:119]
ds_read_b64_tr_b16 a[220:221],v3 offset:8448
ds_read_b64_tr_b16 a[222:223],v3 offset:12544
v_mfma_f32_16x16x32_bf16 a[116:119],a[232:235],v[228:231],a[116:119]
ds_read_b64_tr_b16 a[216:217],v3 offset:256
ds_read_b64_tr_b16 a[218:219],v3 offset:4352
v_mfma_f32_16x16x32_bf16 a[120:123],a[240:243],v[228:231],a[120:123]
ds_read_b64_tr_b16 a[224:225],v2 offset:512
ds_read_b64_tr_b16 a[226:227],v2 offset:4608
v_mfma_f32_16x16x32_bf16 a[120:123],a[244:247],v[244:247],a[120:123]
ds_read_b64_tr_b16 a[228:229],v2 offset:8704
ds_read_b64_tr_b16 a[230:231],v2 offset:12800
v_mfma_f32_16x16x32_bf16 a[124:127],a[252:255],v[244:247],a[124:127]
ds_read_b64_tr_b16 a[236:237],v3 offset:8704
ds_read_b64_tr_b16 a[238:239],v3 offset:12800
v_mfma_f32_16x16x32_bf16 a[124:127],a[248:251],v[228:231],a[124:127]
ds_read_b64_tr_b16 a[232:233],v3 offset:512
ds_read_b64_tr_b16 a[234:235],v3 offset:4608
v_mfma_f32_16x16x32_bf16 v[60:63],v[28:31],v[228:231],v[60:63]
ds_read_b64_tr_b16 a[240:241],v2 offset:768
ds_read_b64_tr_b16 a[242:243],v2 offset:4864
v_mfma_f32_16x16x32_bf16 v[60:63],v[28:31],v[244:247],v[60:63]
buffer_load_dwordx4 v5,s[24:27],s47 offen offset:3072 nt lds
v_mfma_f32_16x16x32_bf16 v[196:199],a[128:131],v[96:99],v[40:43]
s_add_u32 s47,s47,16384
ds_read_b64_tr_b16 a[244:245],v2 offset:8960
ds_read_b64_tr_b16 a[246:247],v2 offset:13056
v_mfma_f32_16x16x32_bf16 v[196:199],a[132:135],v[100:103],v[196:199]
s_and_b32 s47,s47,0x3ffff
ds_read_b64_tr_b16 a[252:253],v3 offset:8960
ds_read_b64_tr_b16 a[254:255],v3 offset:13056
v_mfma_f32_16x16x32_bf16 v[196:199],a[136:139],v[104:107],v[196:199]
s_or_b32 s47,s47,s46
ds_read_b64_tr_b16 a[248:249],v3 offset:768
ds_read_b64_tr_b16 a[250:251],v3 offset:4864
v_mfma_f32_16x16x32_bf16 v[196:199],a[140:143],v[108:111],v[196:199]
s_and_b32 s46,s54,0xfffc0000
v_exp_f32 v165,v165
v_mfma_f32_16x16x32_bf16 v[204:207],a[156:159],v[108:111],v[40:43]
s_add_u32 s54,s54,16384
v_exp_f32 v166,v166
v_mfma_f32_16x16x32_bf16 v[204:207],a[152:155],v[104:107],v[204:207]
)A"
R"A(v_exp_f32 v167,v167
v_mfma_f32_16x16x32_bf16 v[204:207],a[148:151],v[100:103],v[204:207]
v_cvt_pk_bf16_f32 v164,v164,v165
v_cvt_pk_bf16_f32 v165,v166,v167
v_mfma_f32_16x16x32_bf16 v[204:207],a[144:147],v[96:99],v[204:207]
v_exp_f32 v172,v172
v_mfma_f32_16x16x32_bf16 v[212:215],a[160:163],v[96:99],v[40:43]
v_exp_f32 v173,v173
v_mfma_f32_16x16x32_bf16 v[212:215],a[164:167],v[100:103],v[212:215]
v_exp_f32 v174,v174
v_mfma_f32_16x16x32_bf16 v[212:215],a[168:171],v[104:107],v[212:215]
v_exp_f32 v175,v175
v_mfma_f32_16x16x32_bf16 v[212:215],a[172:175],v[108:111],v[212:215]
v_exp_f32 v180,v180
v_mfma_f32_16x16x32_bf16 v[220:223],a[188:191],v[108:111],v[40:43]
v_exp_f32 v181,v181
v_mfma_f32_16x16x32_bf16 v[220:223],a[184:187],v[104:107],v[220:223]
v_exp_f32 v182,v182
v_mfma_f32_16x16x32_bf16 v[220:223],a[180:183],v[100:103],v[220:223]
v_exp_f32 v183,v183
v_mfma_f32_16x16x32_bf16 v[220:223],a[176:179],v[96:99],v[220:223]
s_waitcnt lgkmcnt(0)
v_cvt_pk_bf16_f32 v166,v172,v173
v_cvt_pk_bf16_f32 v167,v174,v175
v_mfma_f32_16x16x32_bf16 v[32:35],v[80:83],v[128:131],0
v_cvt_pk_bf16_f32 v180,v180,v181
v_cvt_pk_bf16_f32 v181,v182,v183
v_mfma_f32_16x16x32_bf16 v[32:35],v[84:87],v[160:163],v[32:35]
v_exp_f32 v188,v188
v_mfma_f32_16x16x32_bf16 v[32:35],v[88:91],v[192:195],v[32:35]
v_exp_f32 v189,v189
v_mfma_f32_16x16x32_bf16 v[32:35],v[92:95],v[224:227],v[32:35]
v_exp_f32 v190,v190
v_mfma_f32_16x16x32_bf16 v[36:39],v[80:83],v[136:139],0
v_exp_f32 v191,v191
v_mfma_f32_16x16x32_bf16 v[36:39],v[84:87],v[168:171],v[36:39]
v_cvt_pk_bf16_f32 v182,v188,v189
v_cvt_pk_bf16_f32 v183,v190,v191
v_mfma_f32_16x16x32_bf16 v[36:39],v[88:91],v[200:203],v[36:39]
v_exp_f32 v196,v196
v_mfma_f32_16x16x32_bf16 v[36:39],v[92:95],v[232:235],v[36:39]
v_exp_f32 v197,v197
v_mfma_f32_16x16x32_bf16 v[40:43],v[80:83],v[144:147],0
v_exp_f32 v198,v198
v_mfma_f32_16x16x32_bf16 v[40:43],v[84:87],v[176:179],v[40:43]
v_max3_f32 v32,v32,v33,v34
v_max3_f32 v32,v32,v35,s18
v_mfma_f32_16x16x32_bf16 v[40:43],v[88:91],v[208:211],v[40:43]
v_sub_f32 v35,0,v32
v_sub_f32 v34,0,v32
v_mfma_f32_16x16x32_bf16 v[40:43],v[92:95],v[240:243],v[40:43]
v_sub_f32 v33,0,v32
v_sub_f32 v32,0,v32
v_mfma_f32_16x16x32_bf16 a[0:3],a[192:195],v[132:135],a[0:3]
v_exp_f32 v199,v199
v_mfma_f32_16x16x32_bf16 a[0:3],a[196:199],v[148:151],a[0:3]
v_cvt_pk_bf16_f32 v196,v196,v197
v_cvt_pk_bf16_f32 v197,v198,v199
v_mfma_f32_16x16x32_bf16 a[4:7],a[204:207],v[148:151],a[4:7]
v_exp_f32 v204,v204
v_mfma_f32_16x16x32_bf16 a[4:7],a[200:203],v[132:135],a[4:7]
v_exp_f32 v205,v205
v_mfma_f32_16x16x32_bf16 a[8:11],a[208:211],v[132:135],a[8:11]
s_memrealtime s[76:77]
v_exp_f32 v206,v206
v_mfma_f32_16x16x32_bf16 a[8:11],a[212:215],v[148:151],a[8:11]
v_exp_f32 v207,v207
v_mfma_f32_16x16x32_bf16 a[12:15],a[220:223],v[148:151],a[12:15]
v_exp_f32 v212,v212
v_mfma_f32_16x16x32_bf16 a[12:15],a[216:219],v[132:135],a[12:15]
v_exp_f32 v213,v213
v_mfma_f32_16x16x32_bf16 a[16:19],a[224:227],v[132:135],a[16:19]
v_exp_f32 v214,v214
v_mfma_f32_16x16x32_bf16 a[16:19],a[228:231],v[148:151],a[16:19]
v_exp_f32 v215,v215
v_mfma_f32_16x16x32_bf16 a[20:23],a[236:239],v[148:151],a[20:23]
v_cvt_pk_bf16_f32 v198,v204,v205
v_cvt_pk_bf16_f32 v199,v206,v207
v_mfma_f32_16x16x32_bf16 a[20:23],a[232:235],v[132:135],a[20:23]
s_waitcnt lgkmcnt(0)
v_cvt_pk_bf16_f32 v212,v212,v213
v_cvt_pk_bf16_f32 v213,v214,v215
v_mfma_f32_16x16x32_bf16 a[24:27],a[240:243],v[132:135],a[24:27]
s_sub_u32 s79,s78,s76
v_exp_f32 v220,v220
v_mfma_f32_16x16x32_bf16 a[24:27],a[244:247],v[148:151],a[24:27]
s_add_u32 s78,s76,s60
v_exp_f32 v221,v221
v_mfma_f32_16x16x32_bf16 a[28:31],a[252:255],v[148:151],a[28:31]
s_cmp_gt_i32 s79,0
s_cbranch_scc1 L_ps4_%=
L_pr4_%=:
v_cvt_pk_bf16_f32 v214,v220,v221
v_mfma_f32_16x16x32_bf16 a[28:31],a[248:251],v[132:135],a[28:31]
v_exp_f32 v222,v222
v_mfma_f32_16x16x32_bf16 v[48:51],v[28:31],v[132:135],v[48:51]
v_exp_f32 v223,v223
v_mfma_f32_16x16x32_bf16 v[48:51],v[28:31],v[148:151],v[48:51]
v_cvt_pk_bf16_f32 v215,v222,v223
v_max3_f32 v36,v36,v37,v38
v_mfma_f32_16x16x32_bf16 v[228:231],a[128:131],v[112:115],v[44:47]
s_waitcnt vmcnt(9)
v_max3_f32 v36,v36,v39,s19
v_sub_f32 v39,0,v36
v_mfma_f32_16x16x32_bf16 v[228:231],a[132:135],v[116:119],v[228:231]
s_barrier
s_add_u32 m0,32768,s51
ds_read_b128 a[128:131],v1
v_mfma_f32_16x16x32_bf16 v[228:231],a[136:139],v[120:123],v[228:231]
ds_read_b128 a[132:135],v1 offset:256
v_sub_f32 v38,0,v36
v_mfma_f32_16x16x32_bf16 v[228:231],a[140:143],v[124:127],v[228:231]
ds_read_b128 a[136:139],v1 offset:512
v_sub_f32 v37,0,v36
v_mfma_f32_16x16x32_bf16 v[236:239],a[156:159],v[124:127],v[44:47]
buffer_load_dwordx4 v4,s[20:23],s47 offen nt lds
v_mfma_f32_16x16x32_bf16 v[236:239],a[152:155],v[120:123],v[236:239]
ds_read_b128 a[140:143],v1 offset:768
v_sub_f32 v36,0,v36
v_mfma_f32_16x16x32_bf16 v[236:239],a[148:151],v[116:119],v[236:239]
ds_read_b128 a[156:159],v1 offset:4864
v_max3_f32 v40,v40,v41,v42
v_mfma_f32_16x16x32_bf16 v[236:239],a[144:147],v[112:115],v[236:239]
ds_read_b128 a[152:155],v1 offset:4608
v_max3_f32 v40,v40,v43,s100
v_mfma_f32_16x16x32_bf16 v[244:247],a[160:163],v[112:115],v[44:47]
ds_read_b128 a[148:151],v1 offset:4352
v_sub_f32 v43,0,v40
v_mfma_f32_16x16x32_bf16 v[244:247],a[164:167],v[116:119],v[244:247]
ds_read_b128 a[144:147],v1 offset:4096
v_sub_f32 v42,0,v40
v_mfma_f32_16x16x32_bf16 v[244:247],a[168:171],v[120:123],v[244:247]
ds_read_b128 a[160:163],v1 offset:8192
v_sub_f32 v41,0,v40
v_mfma_f32_16x16x32_bf16 v[244:247],a[172:175],v[124:127],v[244:247]
ds_read_b128 a[164:167],v1 offset:8448
v_sub_f32 v40,0,v40
v_mfma_f32_16x16x32_bf16 v[252:255],a[188:191],v[124:127],v[44:47]
ds_read_b128 a[168:171],v1 offset:8704
v_add_u32 v2,s48,v2
v_mfma_f32_16x16x32_bf16 v[252:255],a[184:187],v[120:123],v[252:255]
ds_read_b128 a[172:175],v1 offset:8960
v_add_u32 v3,s48,v3
v_mfma_f32_16x16x32_bf16 v[252:255],a[180:183],v[116:119],v[252:255]
s_mov_b32 s48,-98304
ds_read_b128 a[188:191],v1 offset:13056
v_max_f32 v20,0x00800000,v48
v_mfma_f32_16x16x32_bf16 v[252:255],a[176:179],v[112:115],v[252:255]
ds_read_b128 a[184:187],v1 offset:12800
v_accvgpr_read_b32 v64,a0
v_mfma_f32_16x16x32_bf16 v[44:47],v[80:83],v[152:155],0
ds_read_b128 a[180:183],v1 offset:12544
v_accvgpr_read_b32 v65,a1
v_mfma_f32_16x16x32_bf16 v[44:47],v[84:87],v[184:187],v[44:47]
ds_read_b128 a[176:179],v1 offset:12288
v_accvgpr_read_b32 v66,a2
v_mfma_f32_16x16x32_bf16 v[44:47],v[88:91],v[216:219],v[44:47]
buffer_load_dwordx4 v4,s[20:23],s47 offen offset:1024 nt lds
v_mfma_f32_16x16x32_bf16 v[44:47],v[92:95],v[248:251],v[44:47]
v_exp_f32 v228,v228
v_mfma_f32_16x16x32_bf16 a[32:35],a[192:195],v[164:167],a[32:35]
v_exp_f32 v229,v229
v_mfma_f32_16x16x32_bf16 a[32:35],a[196:199],v[180:183],a[32:35]
v_exp_f32 v230,v230
v_mfma_f32_16x16x32_bf16 a[36:39],a[204:207],v[180:183],a[36:39]
v_exp_f32 v231,v231
v_mfma_f32_16x16x32_bf16 a[36:39],a[200:203],v[164:167],a[36:39]
v_cvt_pk_bf16_f32 v228,v228,v229
v_cvt_pk_bf16_f32 v229,v230,v231
v_mfma_f32_16x16x32_bf16 a[40:43],a[208:211],v[164:167],a[40:43]
v_exp_f32 v236,v236
v_mfma_f32_16x16x32_bf16 a[40:43],a[212:215],v[180:183],a[40:43]
v_exp_f32 v237,v237
v_mfma_f32_16x16x32_bf16 a[44:47],a[220:223],v[180:183],a[44:47]
v_exp_f32 v238,v238
v_mfma_f32_16x16x32_bf16 a[44:47],a[216:219],v[164:167],a[44:47]
v_exp_f32 v239,v239
v_mfma_f32_16x16x32_bf16 a[48:51],a[224:227],v[164:167],a[48:51]
v_exp_f32 v244,v244
v_mfma_f32_16x16x32_bf16 a[48:51],a[228:231],v[180:183],a[48:51]
v_exp_f32 v245,v245
v_mfma_f32_16x16x32_bf16 a[52:55],a[236:239],v[180:183],a[52:55]
v_exp_f32 v246,v246
v_mfma_f32_16x16x32_bf16 a[52:55],a[232:235],v[164:167],a[52:55]
v_exp_f32 v247,v247
v_mfma_f32_16x16x32_bf16 a[56:59],a[240:243],v[164:167],a[56:59]
buffer_load_dwordx4 v5,s[20:23],s47 offen offset:2048 nt lds
v_mfma_f32_16x16x32_bf16 a[56:59],a[244:247],v[180:183],a[56:59]
v_cvt_pk_bf16_f32 v230,v236,v237
v_cvt_pk_bf16_f32 v231,v238,v239
v_mfma_f32_16x16x32_bf16 a[60:63],a[252:255],v[180:183],a[60:63]
v_cvt_pk_bf16_f32 v244,v244,v245
v_cvt_pk_bf16_f32 v245,v246,v247
v_mfma_f32_16x16x32_bf16 a[60:63],a[248:251],v[164:167],a[60:63]
v_exp_f32 v252,v252
v_mfma_f32_16x16x32_bf16 v[52:55],v[28:31],v[164:167],v[52:55]
v_exp_f32 v253,v253
v_mfma_f32_16x16x32_bf16 v[52:55],v[28:31],v[180:183],v[52:55]
s_waitcnt lgkmcnt(0)
v_exp_f32 v254,v254
v_mfma_f32_16x16x32_bf16 v[132:135],a[128:131],v[128:131],v[32:35]
v_exp_f32 v255,v255
v_mfma_f32_16x16x32_bf16 v[132:135],a[132:135],v[160:163],v[132:135]
v_cvt_pk_bf16_f32 v246,v252,v253
v_cvt_pk_bf16_f32 v247,v254,v255
v_mfma_f32_16x16x32_bf16 v[132:135],a[136:139],v[192:195],v[132:135]
v_rcp_f32 v20,v20
v_mfma_f32_16x16x32_bf16 v[132:135],a[140:143],v[224:227],v[132:135]
v_mul_f32 v64,v20,v64
v_mul_f32 v65,v20,v65
v_mfma_f32_16x16x32_bf16 v[140:143],a[156:159],v[224:227],v[32:35]
v_accvgpr_read_b32 v67,a3
v_cvt_pk_bf16_f32 v64,v64,v65
v_mfma_f32_16x16x32_bf16 v[140:143],a[152:155],v[192:195],v[140:143]
v_mul_f32 v66,v20,v66
v_mul_f32 v67,v20,v67
v_mfma_f32_16x16x32_bf16 v[140:143],a[148:151],v[160:163],v[140:143]
v_cvt_pk_bf16_f32 v65,v66,v67
v_accvgpr_read_b32 v66,a4
v_mfma_f32_16x16x32_bf16 v[140:143],a[144:147],v[128:131],v[140:143]
v_accvgpr_read_b32 v67,a5
v_accvgpr_read_b32 v12,a6
v_mfma_f32_16x16x32_bf16 v[148:151],a[160:163],v[128:131],v[32:35]
buffer_load_dwordx4 v5,s[20:23],s47 offen offset:3072 nt lds
v_mfma_f32_16x16x32_bf16 v[148:151],a[164:167],v[160:163],v[148:151]
v_exp_f32 v132,v132
v_mfma_f32_16x16x32_bf16 v[148:151],a[168:171],v[192:195],v[148:151]
v_exp_f32 v133,v133
v_mfma_f32_16x16x32_bf16 v[148:151],a[172:175],v[224:227],v[148:151]
v_exp_f32 v134,v134
v_mfma_f32_16x16x32_bf16 v[156:159],a[188:191],v[224:227],v[32:35]
v_exp_f32 v135,v135
v_mfma_f32_16x16x32_bf16 v[156:159],a[184:187],v[192:195],v[156:159]
v_cvt_pk_bf16_f32 v132,v132,v133
v_cvt_pk_bf16_f32 v133,v134,v135
v_mfma_f32_16x16x32_bf16 v[156:159],a[180:183],v[160:163],v[156:159]
v_exp_f32 v140,v140
v_mfma_f32_16x16x32_bf16 v[156:159],a[176:179],v[128:131],v[156:159]
v_exp_f32 v141,v141
v_mfma_f32_16x16x32_bf16 a[64:67],a[192:195],v[196:199],a[64:67]
v_exp_f32 v142,v142
v_mfma_f32_16x16x32_bf16 a[64:67],a[196:199],v[212:215],a[64:67]
v_exp_f32 v143,v143
v_mfma_f32_16x16x32_bf16 a[68:71],a[204:207],v[212:215],a[68:71]
v_exp_f32 v148,v148
v_mfma_f32_16x16x32_bf16 a[68:71],a[200:203],v[196:199],a[68:71]
s_add_u32 m0,32768,s52
v_exp_f32 v149,v149
v_mfma_f32_16x16x32_bf16 a[72:75],a[208:211],v[196:199],a[72:75]
v_exp_f32 v150,v150
v_mfma_f32_16x16x32_bf16 a[72:75],a[212:215],v[212:215],a[72:75]
v_exp_f32 v151,v151
v_mfma_f32_16x16x32_bf16 a[76:79],a[220:223],v[212:215],a[76:79]
buffer_load_dwordx4 v4,s[24:27],s47 offen nt lds
v_mfma_f32_16x16x32_bf16 a[76:79],a[216:219],v[196:199],a[76:79]
v_cvt_pk_bf16_f32 v134,v140,v141
v_cvt_pk_bf16_f32 v135,v142,v143
v_mfma_f32_16x16x32_bf16 a[80:83],a[224:227],v[196:199],a[80:83]
v_cvt_pk_bf16_f32 v148,v148,v149
v_cvt_pk_bf16_f32 v149,v150,v151
v_mfma_f32_16x16x32_bf16 a[80:83],a[228:231],v[212:215],a[80:83]
v_exp_f32 v156,v156
v_mfma_f32_16x16x32_bf16 a[84:87],a[236:239],v[212:215],a[84:87]
v_exp_f32 v157,v157
v_mfma_f32_16x16x32_bf16 a[84:87],a[232:235],v[196:199],a[84:87]
v_exp_f32 v158,v158
v_mfma_f32_16x16x32_bf16 a[88:91],a[240:243],v[196:199],a[88:91]
v_exp_f32 v159,v159
v_mfma_f32_16x16x32_bf16 a[88:91],a[244:247],v[212:215],a[88:91]
v_cvt_pk_bf16_f32 v150,v156,v157
v_cvt_pk_bf16_f32 v151,v158,v159
v_mfma_f32_16x16x32_bf16 a[92:95],a[252:255],v[212:215],a[92:95]
v_accvgpr_read_b32 v13,a7
v_mul_f32 v66,v20,v66
v_mfma_f32_16x16x32_bf16 a[92:95],a[248:251],v[196:199],a[92:95]
v_mul_f32 v67,v20,v67
v_mul_f32 v12,v20,v12
v_mfma_f32_16x16x32_bf16 v[56:59],v[28:31],v[196:199],v[56:59]
v_mul_f32 v13,v20,v13
v_cvt_pk_bf16_f32 v66,v66,v67
v_mfma_f32_16x16x32_bf16 v[56:59],v[28:31],v[212:215],v[56:59]
v_cvt_pk_bf16_f32 v67,v12,v13
v_max3_f32 v44,v44,v45,v46
v_mfma_f32_16x16x32_bf16 v[164:167],a[128:131],v[136:139],v[36:39]
v_accvgpr_read_b32 v68,a8
v_accvgpr_read_b32 v69,a9
v_accvgpr_read_b32 v70,a10
v_accvgpr_read_b32 v71,a11
v_accvgpr_read_b32 v12,a12
v_accvgpr_read_b32 v13,a13
)A"
R"A(v_accvgpr_read_b32 v14,a14
v_accvgpr_read_b32 v15,a15
v_pk_mul_f32 v[68:69],v[20:21],v[68:69] op_sel:[0,0] op_sel_hi:[0,1]
v_pk_mul_f32 v[70:71],v[20:21],v[70:71] op_sel:[0,0] op_sel_hi:[0,1]
v_pk_mul_f32 v[12:13],v[20:21],v[12:13] op_sel:[0,0] op_sel_hi:[0,1]
v_pk_mul_f32 v[14:15],v[20:21],v[14:15] op_sel:[0,0] op_sel_hi:[0,1]
v_cvt_pk_bf16_f32 v68,v68,v69
v_cvt_pk_bf16_f32 v69,v70,v71
v_cvt_pk_bf16_f32 v70,v12,v13
v_cvt_pk_bf16_f32 v71,v14,v15
v_mfma_f32_16x16x32_bf16 v[164:167],a[132:135],v[168:171],v[164:167]
v_accvgpr_read_b32 v72,a16
v_accvgpr_read_b32 v73,a17
v_accvgpr_read_b32 v74,a18
v_accvgpr_read_b32 v75,a19
v_accvgpr_read_b32 v12,a20
v_accvgpr_read_b32 v13,a21
v_accvgpr_read_b32 v14,a22
v_accvgpr_read_b32 v15,a23
v_pk_mul_f32 v[72:73],v[20:21],v[72:73] op_sel:[0,0] op_sel_hi:[0,1]
v_pk_mul_f32 v[74:75],v[20:21],v[74:75] op_sel:[0,0] op_sel_hi:[0,1]
v_pk_mul_f32 v[12:13],v[20:21],v[12:13] op_sel:[0,0] op_sel_hi:[0,1]
v_pk_mul_f32 v[14:15],v[20:21],v[14:15] op_sel:[0,0] op_sel_hi:[0,1]
v_cvt_pk_bf16_f32 v72,v72,v73
v_cvt_pk_bf16_f32 v73,v74,v75
v_cvt_pk_bf16_f32 v74,v12,v13
v_cvt_pk_bf16_f32 v75,v14,v15
v_mfma_f32_16x16x32_bf16 v[164:167],a[136:139],v[200:203],v[164:167]
buffer_load_dwordx4 v5,s[24:27],s47 offen offset:1024 nt lds
v_mfma_f32_16x16x32_bf16 v[164:167],a[140:143],v[232:235],v[164:167]
v_accvgpr_read_b32 v76,a24
v_accvgpr_read_b32 v77,a25
v_accvgpr_read_b32 v78,a26
v_accvgpr_read_b32 v79,a27
v_accvgpr_read_b32 v12,a28
v_accvgpr_read_b32 v13,a29
v_accvgpr_read_b32 v14,a30
v_accvgpr_read_b32 v15,a31
v_pk_mul_f32 v[76:77],v[20:21],v[76:77] op_sel:[0,0] op_sel_hi:[0,1]
v_pk_mul_f32 v[78:79],v[20:21],v[78:79] op_sel:[0,0] op_sel_hi:[0,1]
v_pk_mul_f32 v[12:13],v[20:21],v[12:13] op_sel:[0,0] op_sel_hi:[0,1]
v_pk_mul_f32 v[14:15],v[20:21],v[14:15] op_sel:[0,0] op_sel_hi:[0,1]
v_cvt_pk_bf16_f32 v76,v76,v77
v_cvt_pk_bf16_f32 v77,v78,v79
v_cvt_pk_bf16_f32 v78,v12,v13
v_cvt_pk_bf16_f32 v79,v14,v15
v_mfma_f32_16x16x32_bf16 v[172:175],a[156:159],v[232:235],v[36:39]
v_max3_f32 v44,v44,v47,s101
v_sub_f32 v47,0,v44
v_mfma_f32_16x16x32_bf16 v[172:175],a[152:155],v[200:203],v[172:175]
v_sub_f32 v46,0,v44
v_sub_f32 v45,0,v44
v_mfma_f32_16x16x32_bf16 v[172:175],a[148:151],v[168:171],v[172:175]
v_cmp_nlt_f32 s[56:57],v48,s63
v_sub_f32 v44,0,v44
v_mfma_f32_16x16x32_bf16 v[172:175],a[144:147],v[136:139],v[172:175]
v_max_f32 v21,0x00800000,v52
v_accvgpr_read_b32 v80,a32
v_mfma_f32_16x16x32_bf16 v[180:183],a[160:163],v[136:139],v[36:39]
v_rcp_f32 v21,v21
v_mfma_f32_16x16x32_bf16 v[180:183],a[164:167],v[168:171],v[180:183]
v_exp_f32 v164,v164
v_mfma_f32_16x16x32_bf16 v[180:183],a[168:171],v[200:203],v[180:183]
v_exp_f32 v165,v165
v_mfma_f32_16x16x32_bf16 v[180:183],a[172:175],v[232:235],v[180:183]
v_exp_f32 v166,v166
v_mfma_f32_16x16x32_bf16 v[188:191],a[188:191],v[232:235],v[36:39]
v_exp_f32 v167,v167
v_mfma_f32_16x16x32_bf16 v[188:191],a[184:187],v[200:203],v[188:191]
v_cvt_pk_bf16_f32 v164,v164,v165
v_cvt_pk_bf16_f32 v165,v166,v167
v_mfma_f32_16x16x32_bf16 v[188:191],a[180:183],v[168:171],v[188:191]
v_exp_f32 v172,v172
v_mfma_f32_16x16x32_bf16 v[188:191],a[176:179],v[136:139],v[188:191]
v_exp_f32 v173,v173
v_mfma_f32_16x16x32_bf16 a[96:99],a[192:195],v[228:231],a[96:99]
buffer_load_dwordx4 v4,s[24:27],s47 offen offset:2048 nt lds
v_mfma_f32_16x16x32_bf16 a[96:99],a[196:199],v[244:247],a[96:99]
ds_read_b64_tr_b16 a[192:193],v2
ds_read_b64_tr_b16 a[194:195],v2 offset:4096
v_mfma_f32_16x16x32_bf16 a[100:103],a[204:207],v[244:247],a[100:103]
ds_read_b64_tr_b16 a[196:197],v2 offset:8192
ds_read_b64_tr_b16 a[198:199],v2 offset:12288
v_mfma_f32_16x16x32_bf16 a[100:103],a[200:203],v[228:231],a[100:103]
ds_read_b64_tr_b16 a[204:205],v3 offset:8192
ds_read_b64_tr_b16 a[206:207],v3 offset:12288
v_mfma_f32_16x16x32_bf16 a[104:107],a[208:211],v[228:231],a[104:107]
ds_read_b64_tr_b16 a[200:201],v3
ds_read_b64_tr_b16 a[202:203],v3 offset:4096
v_mfma_f32_16x16x32_bf16 a[104:107],a[212:215],v[244:247],a[104:107]
ds_read_b64_tr_b16 a[208:209],v2 offset:256
ds_read_b64_tr_b16 a[210:211],v2 offset:4352
v_mfma_f32_16x16x32_bf16 a[108:111],a[220:223],v[244:247],a[108:111]
ds_read_b64_tr_b16 a[212:213],v2 offset:8448
ds_read_b64_tr_b16 a[214:215],v2 offset:12544
v_mfma_f32_16x16x32_bf16 a[108:111],a[216:219],v[228:231],a[108:111]
ds_read_b64_tr_b16 a[220:221],v3 offset:8448
ds_read_b64_tr_b16 a[222:223],v3 offset:12544
v_mfma_f32_16x16x32_bf16 a[112:115],a[224:227],v[228:231],a[112:115]
ds_read_b64_tr_b16 a[216:217],v3 offset:256
ds_read_b64_tr_b16 a[218:219],v3 offset:4352
v_mfma_f32_16x16x32_bf16 a[112:115],a[228:231],v[244:247],a[112:115]
ds_read_b64_tr_b16 a[224:225],v2 offset:512
ds_read_b64_tr_b16 a[226:227],v2 offset:4608
v_mfma_f32_16x16x32_bf16 a[116:119],a[236:239],v[244:247],a[116:119]
ds_read_b64_tr_b16 a[228:229],v2 offset:8704
ds_read_b64_tr_b16 a[230:231],v2 offset:12800
v_mfma_f32_16x16x32_bf16 a[116:119],a[232:235],v[228:231],a[116:119]
ds_read_b64_tr_b16 a[236:237],v3 offset:8704
ds_read_b64_tr_b16 a[238:239],v3 offset:12800
v_mfma_f32_16x16x32_bf16 a[120:123],a[240:243],v[228:231],a[120:123]
ds_read_b64_tr_b16 a[232:233],v3 offset:512
ds_read_b64_tr_b16 a[234:235],v3 offset:4608
v_mfma_f32_16x16x32_bf16 a[120:123],a[244:247],v[244:247],a[120:123]
ds_read_b64_tr_b16 a[240:241],v2 offset:768
ds_read_b64_tr_b16 a[242:243],v2 offset:4864
v_mfma_f32_16x16x32_bf16 a[124:127],a[252:255],v[244:247],a[124:127]
buffer_load_dwordx4 v5,s[24:27],s47 offen offset:3072 nt lds
v_mfma_f32_16x16x32_bf16 a[124:127],a[248:251],v[228:231],a[124:127]
s_add_u32 s47,s47,16384
ds_read_b64_tr_b16 a[244:245],v2 offset:8960
ds_read_b64_tr_b16 a[246:247],v2 offset:13056
v_mfma_f32_16x16x32_bf16 v[60:63],v[28:31],v[228:231],v[60:63]
s_and_b32 s47,s47,0x3ffff
ds_read_b64_tr_b16 a[252:253],v3 offset:8960
ds_read_b64_tr_b16 a[254:255],v3 offset:13056
v_mfma_f32_16x16x32_bf16 v[60:63],v[28:31],v[244:247],v[60:63]
s_or_b32 s47,s47,s46
ds_read_b64_tr_b16 a[248:249],v3 offset:768
ds_read_b64_tr_b16 a[250:251],v3 offset:4864
v_mfma_f32_16x16x32_bf16 v[196:199],a[128:131],v[144:147],v[40:43]
s_and_b32 s46,s54,0xfffc0000
v_exp_f32 v174,v174
v_mfma_f32_16x16x32_bf16 v[196:199],a[132:135],v[176:179],v[196:199]
v_exp_f32 v175,v175
v_mfma_f32_16x16x32_bf16 v[196:199],a[136:139],v[208:211],v[196:199]
v_exp_f32 v180,v180
v_mfma_f32_16x16x32_bf16 v[196:199],a[140:143],v[240:243],v[196:199]
v_exp_f32 v181,v181
v_mfma_f32_16x16x32_bf16 v[204:207],a[156:159],v[240:243],v[40:43]
v_exp_f32 v182,v182
v_mfma_f32_16x16x32_bf16 v[204:207],a[152:155],v[208:211],v[204:207]
v_exp_f32 v183,v183
v_mfma_f32_16x16x32_bf16 v[204:207],a[148:151],v[176:179],v[204:207]
v_cvt_pk_bf16_f32 v166,v172,v173
v_cvt_pk_bf16_f32 v167,v174,v175
v_mfma_f32_16x16x32_bf16 v[204:207],a[144:147],v[144:147],v[204:207]
v_cvt_pk_bf16_f32 v180,v180,v181
v_cvt_pk_bf16_f32 v181,v182,v183
v_mfma_f32_16x16x32_bf16 v[212:215],a[160:163],v[144:147],v[40:43]
v_exp_f32 v188,v188
v_mfma_f32_16x16x32_bf16 v[212:215],a[164:167],v[176:179],v[212:215]
v_exp_f32 v189,v189
v_mfma_f32_16x16x32_bf16 v[212:215],a[168:171],v[208:211],v[212:215]
v_exp_f32 v190,v190
v_mfma_f32_16x16x32_bf16 v[212:215],a[172:175],v[240:243],v[212:215]
v_exp_f32 v191,v191
v_mfma_f32_16x16x32_bf16 v[220:223],a[188:191],v[240:243],v[40:43]
v_cvt_pk_bf16_f32 v182,v188,v189
v_cvt_pk_bf16_f32 v183,v190,v191
v_mfma_f32_16x16x32_bf16 v[220:223],a[184:187],v[208:211],v[220:223]
v_accvgpr_read_b32 v81,a33
v_mul_f32 v80,v21,v80
v_mfma_f32_16x16x32_bf16 v[220:223],a[180:183],v[176:179],v[220:223]
v_mul_f32 v81,v21,v81
v_accvgpr_read_b32 v82,a34
v_mfma_f32_16x16x32_bf16 v[220:223],a[176:179],v[144:147],v[220:223]
s_waitcnt lgkmcnt(0)
v_accvgpr_read_b32 v83,a35
v_cvt_pk_bf16_f32 v80,v80,v81
v_mfma_f32_16x16x32_bf16 a[0:3],a[192:195],v[132:135],0
v_mul_f32 v82,v21,v82
v_mul_f32 v83,v21,v83
v_mfma_f32_16x16x32_bf16 a[0:3],a[196:199],v[148:151],a[0:3]
v_cvt_pk_bf16_f32 v81,v82,v83
v_accvgpr_read_b32 v82,a36
v_mfma_f32_16x16x32_bf16 a[4:7],a[204:207],v[148:151],0
v_accvgpr_read_b32 v83,a37
v_accvgpr_read_b32 v14,a38
v_mfma_f32_16x16x32_bf16 a[4:7],a[200:203],v[132:135],a[4:7]
v_accvgpr_read_b32 v15,a39
v_mul_f32 v82,v21,v82
v_mfma_f32_16x16x32_bf16 a[8:11],a[208:211],v[132:135],0
s_memrealtime s[76:77]
v_mul_f32 v83,v21,v83
v_mul_f32 v14,v21,v14
v_mfma_f32_16x16x32_bf16 a[8:11],a[212:215],v[148:151],a[8:11]
v_mul_f32 v15,v21,v15
v_cvt_pk_bf16_f32 v82,v82,v83
v_mfma_f32_16x16x32_bf16 a[12:15],a[220:223],v[148:151],0
v_cvt_pk_bf16_f32 v83,v14,v15
v_add_u32 v1,s48,v1
v_mfma_f32_16x16x32_bf16 a[12:15],a[216:219],v[132:135],a[12:15]
v_accvgpr_read_b32 v84,a40
v_accvgpr_read_b32 v85,a41
v_accvgpr_read_b32 v86,a42
v_accvgpr_read_b32 v87,a43
v_accvgpr_read_b32 v12,a44
v_accvgpr_read_b32 v13,a45
v_accvgpr_read_b32 v14,a46
v_accvgpr_read_b32 v15,a47
v_pk_mul_f32 v[84:85],v[20:21],v[84:85] op_sel:[1,0] op_sel_hi:[1,1]
v_pk_mul_f32 v[86:87],v[20:21],v[86:87] op_sel:[1,0] op_sel_hi:[1,1]
v_pk_mul_f32 v[12:13],v[20:21],v[12:13] op_sel:[1,0] op_sel_hi:[1,1]
v_pk_mul_f32 v[14:15],v[20:21],v[14:15] op_sel:[1,0] op_sel_hi:[1,1]
v_cvt_pk_bf16_f32 v84,v84,v85
v_cvt_pk_bf16_f32 v85,v86,v87
v_cvt_pk_bf16_f32 v86,v12,v13
v_cvt_pk_bf16_f32 v87,v14,v15
v_mfma_f32_16x16x32_bf16 a[16:19],a[224:227],v[132:135],0
v_accvgpr_read_b32 v88,a48
v_accvgpr_read_b32 v89,a49
v_accvgpr_read_b32 v90,a50
v_accvgpr_read_b32 v91,a51
v_accvgpr_read_b32 v12,a52
v_accvgpr_read_b32 v13,a53
v_accvgpr_read_b32 v14,a54
v_accvgpr_read_b32 v15,a55
v_pk_mul_f32 v[88:89],v[20:21],v[88:89] op_sel:[1,0] op_sel_hi:[1,1]
v_pk_mul_f32 v[90:91],v[20:21],v[90:91] op_sel:[1,0] op_sel_hi:[1,1]
v_pk_mul_f32 v[12:13],v[20:21],v[12:13] op_sel:[1,0] op_sel_hi:[1,1]
v_pk_mul_f32 v[14:15],v[20:21],v[14:15] op_sel:[1,0] op_sel_hi:[1,1]
v_cvt_pk_bf16_f32 v88,v88,v89
v_cvt_pk_bf16_f32 v89,v90,v91
v_cvt_pk_bf16_f32 v90,v12,v13
v_cvt_pk_bf16_f32 v91,v14,v15
v_mfma_f32_16x16x32_bf16 a[16:19],a[228:231],v[148:151],a[16:19]
v_accvgpr_read_b32 v92,a56
v_accvgpr_read_b32 v93,a57
v_accvgpr_read_b32 v94,a58
v_accvgpr_read_b32 v95,a59
v_accvgpr_read_b32 v12,a60
v_accvgpr_read_b32 v13,a61
v_accvgpr_read_b32 v14,a62
v_accvgpr_read_b32 v15,a63
v_pk_mul_f32 v[92:93],v[20:21],v[92:93] op_sel:[1,0] op_sel_hi:[1,1]
v_pk_mul_f32 v[94:95],v[20:21],v[94:95] op_sel:[1,0] op_sel_hi:[1,1]
v_pk_mul_f32 v[12:13],v[20:21],v[12:13] op_sel:[1,0] op_sel_hi:[1,1]
v_pk_mul_f32 v[14:15],v[20:21],v[14:15] op_sel:[1,0] op_sel_hi:[1,1]
v_cvt_pk_bf16_f32 v92,v92,v93
v_cvt_pk_bf16_f32 v93,v94,v95
v_cvt_pk_bf16_f32 v94,v12,v13
v_cvt_pk_bf16_f32 v95,v14,v15
v_mfma_f32_16x16x32_bf16 a[20:23],a[236:239],v[148:151],0
v_cmp_nlt_f32 s[58:59],v52,s63
s_nop 1
s_or_b64 s[56:57],s[56:57],s[58:59]
v_mfma_f32_16x16x32_bf16 a[20:23],a[232:235],v[132:135],a[20:23]
s_waitcnt lgkmcnt(0)
v_exp_f32 v196,v196
v_mfma_f32_16x16x32_bf16 a[24:27],a[240:243],v[132:135],0
s_sub_u32 s79,s78,s76
v_exp_f32 v197,v197
v_mfma_f32_16x16x32_bf16 a[24:27],a[244:247],v[148:151],a[24:27]
s_add_u32 s78,s76,s60
v_exp_f32 v198,v198
v_mfma_f32_16x16x32_bf16 a[28:31],a[252:255],v[148:151],0
s_cmp_gt_i32 s79,0
s_cbranch_scc1 L_ps5_%=
L_pr5_%=:
v_cvt_pk_bf16_f32 v196,v196,v197
v_mfma_f32_16x16x32_bf16 a[28:31],a[248:251],v[132:135],a[28:31]
v_exp_f32 v199,v199
v_mfma_f32_16x16x32_bf16 v[48:51],v[28:31],v[132:135],0
v_exp_f32 v204,v204
v_mfma_f32_16x16x32_bf16 v[48:51],v[28:31],v[148:151],v[48:51]
v_exp_f32 v205,v205
v_mfma_f32_16x16x32_bf16 v[228:231],a[128:131],v[152:155],v[44:47]
s_waitcnt vmcnt(8)
v_exp_f32 v206,v206
v_mfma_f32_16x16x32_bf16 v[228:231],a[132:135],v[184:187],v[228:231]
s_barrier
s_add_u32 m0,65536,s51
ds_read_b128 a[128:131],v1
v_mfma_f32_16x16x32_bf16 v[228:231],a[136:139],v[216:219],v[228:231]
ds_read_b128 a[132:135],v1 offset:256
v_cvt_pk_bf16_f32 v197,v198,v199
v_mfma_f32_16x16x32_bf16 v[228:231],a[140:143],v[248:251],v[228:231]
ds_read_b128 a[136:139],v1 offset:512
v_cvt_pk_bf16_f32 v198,v204,v205
v_mfma_f32_16x16x32_bf16 v[236:239],a[156:159],v[248:251],v[44:47]
buffer_load_dwordx4 v4,s[20:23],s47 offen nt lds
v_mfma_f32_16x16x32_bf16 v[236:239],a[152:155],v[216:219],v[236:239]
)A"
R"A(ds_read_b128 a[140:143],v1 offset:768
v_max_f32 v22,0x00800000,v56
v_mfma_f32_16x16x32_bf16 v[236:239],a[148:151],v[184:187],v[236:239]
ds_read_b128 a[156:159],v1 offset:4864
v_accvgpr_read_b32 v96,a64
v_mfma_f32_16x16x32_bf16 v[236:239],a[144:147],v[152:155],v[236:239]
ds_read_b128 a[152:155],v1 offset:4608
v_accvgpr_read_b32 v97,a65
v_mfma_f32_16x16x32_bf16 v[244:247],a[160:163],v[152:155],v[44:47]
ds_read_b128 a[148:151],v1 offset:4352
v_accvgpr_read_b32 v98,a66
v_mfma_f32_16x16x32_bf16 v[244:247],a[164:167],v[184:187],v[244:247]
ds_read_b128 a[144:147],v1 offset:4096
v_accvgpr_read_b32 v99,a67
v_mfma_f32_16x16x32_bf16 v[244:247],a[168:171],v[216:219],v[244:247]
ds_read_b128 a[160:163],v1 offset:8192
v_accvgpr_read_b32 v16,a70
v_mfma_f32_16x16x32_bf16 v[244:247],a[172:175],v[248:251],v[244:247]
ds_read_b128 a[164:167],v1 offset:8448
v_accvgpr_read_b32 v17,a71
v_mfma_f32_16x16x32_bf16 v[252:255],a[188:191],v[248:251],v[44:47]
ds_read_b128 a[168:171],v1 offset:8704
v_max_f32 v23,0x00800000,v60
v_mfma_f32_16x16x32_bf16 v[252:255],a[184:187],v[216:219],v[252:255]
ds_read_b128 a[172:175],v1 offset:8960
v_accvgpr_read_b32 v112,a96
v_mfma_f32_16x16x32_bf16 v[252:255],a[180:183],v[184:187],v[252:255]
ds_read_b128 a[188:191],v1 offset:13056
v_accvgpr_read_b32 v113,a97
v_mfma_f32_16x16x32_bf16 v[252:255],a[176:179],v[152:155],v[252:255]
ds_read_b128 a[184:187],v1 offset:12800
v_accvgpr_read_b32 v114,a98
v_mfma_f32_16x16x32_bf16 a[32:35],a[192:195],v[164:167],0
ds_read_b128 a[180:183],v1 offset:12544
v_accvgpr_read_b32 v115,a99
v_mfma_f32_16x16x32_bf16 a[32:35],a[196:199],v[180:183],a[32:35]
ds_read_b128 a[176:179],v1 offset:12288
v_add_u32 v2,s48,v2
v_mfma_f32_16x16x32_bf16 a[36:39],a[204:207],v[180:183],0
buffer_load_dwordx4 v4,s[20:23],s47 offen offset:1024 nt lds
v_mfma_f32_16x16x32_bf16 a[36:39],a[200:203],v[164:167],a[36:39]
v_exp_f32 v207,v207
v_mfma_f32_16x16x32_bf16 a[40:43],a[208:211],v[164:167],0
v_exp_f32 v212,v212
v_mfma_f32_16x16x32_bf16 a[40:43],a[212:215],v[180:183],a[40:43]
v_exp_f32 v213,v213
v_mfma_f32_16x16x32_bf16 a[44:47],a[220:223],v[180:183],0
v_exp_f32 v214,v214
v_mfma_f32_16x16x32_bf16 a[44:47],a[216:219],v[164:167],a[44:47]
v_exp_f32 v215,v215
v_mfma_f32_16x16x32_bf16 a[48:51],a[224:227],v[164:167],0
v_cvt_pk_bf16_f32 v199,v206,v207
v_cvt_pk_bf16_f32 v212,v212,v213
v_mfma_f32_16x16x32_bf16 a[48:51],a[228:231],v[180:183],a[48:51]
v_exp_f32 v220,v220
v_mfma_f32_16x16x32_bf16 a[52:55],a[236:239],v[180:183],0
v_exp_f32 v221,v221
v_mfma_f32_16x16x32_bf16 a[52:55],a[232:235],v[164:167],a[52:55]
v_exp_f32 v222,v222
v_mfma_f32_16x16x32_bf16 a[56:59],a[240:243],v[164:167],0
v_exp_f32 v223,v223
v_mfma_f32_16x16x32_bf16 a[56:59],a[244:247],v[180:183],a[56:59]
v_cvt_pk_bf16_f32 v213,v214,v215
v_cvt_pk_bf16_f32 v214,v220,v221
v_mfma_f32_16x16x32_bf16 a[60:63],a[252:255],v[180:183],0
v_cvt_pk_bf16_f32 v215,v222,v223
v_add_u32 v3,s48,v3
v_mfma_f32_16x16x32_bf16 a[60:63],a[248:251],v[164:167],a[60:63]
s_mov_b32 s48,32768
v_rcp_f32 v22,v22
v_mfma_f32_16x16x32_bf16 v[52:55],v[28:31],v[164:167],0
buffer_load_dwordx4 v5,s[20:23],s47 offen offset:2048 nt lds
v_mfma_f32_16x16x32_bf16 v[52:55],v[28:31],v[180:183],v[52:55]
s_waitcnt lgkmcnt(0)
v_mul_f32 v96,v22,v96
v_mul_f32 v97,v22,v97
v_mfma_f32_16x16x32_bf16 v[132:135],a[128:131],v[128:131],v[32:35]
v_cvt_pk_bf16_f32 v96,v96,v97
v_mul_f32 v98,v22,v98
v_mfma_f32_16x16x32_bf16 v[132:135],a[132:135],v[160:163],v[132:135]
v_mul_f32 v99,v22,v99
v_cvt_pk_bf16_f32 v97,v98,v99
v_mfma_f32_16x16x32_bf16 v[132:135],a[136:139],v[192:195],v[132:135]
v_accvgpr_read_b32 v98,a68
v_accvgpr_read_b32 v99,a69
v_mfma_f32_16x16x32_bf16 v[132:135],a[140:143],v[224:227],v[132:135]
v_accvgpr_read_b32 v100,a72
v_accvgpr_read_b32 v101,a73
v_accvgpr_read_b32 v102,a74
v_accvgpr_read_b32 v103,a75
v_accvgpr_read_b32 v12,a76
v_accvgpr_read_b32 v13,a77
v_accvgpr_read_b32 v14,a78
v_accvgpr_read_b32 v15,a79
v_pk_mul_f32 v[100:101],v[22:23],v[100:101] op_sel:[0,0] op_sel_hi:[0,1]
v_pk_mul_f32 v[102:103],v[22:23],v[102:103] op_sel:[0,0] op_sel_hi:[0,1]
v_pk_mul_f32 v[12:13],v[22:23],v[12:13] op_sel:[0,0] op_sel_hi:[0,1]
v_pk_mul_f32 v[14:15],v[22:23],v[14:15] op_sel:[0,0] op_sel_hi:[0,1]
v_cvt_pk_bf16_f32 v100,v100,v101
v_cvt_pk_bf16_f32 v101,v102,v103
v_cvt_pk_bf16_f32 v102,v12,v13
v_cvt_pk_bf16_f32 v103,v14,v15
v_mfma_f32_16x16x32_bf16 v[140:143],a[156:159],v[224:227],v[32:35]
v_accvgpr_read_b32 v104,a80
v_accvgpr_read_b32 v105,a81
v_accvgpr_read_b32 v106,a82
v_accvgpr_read_b32 v107,a83
v_accvgpr_read_b32 v12,a84
v_accvgpr_read_b32 v13,a85
v_accvgpr_read_b32 v14,a86
v_accvgpr_read_b32 v15,a87
v_pk_mul_f32 v[104:105],v[22:23],v[104:105] op_sel:[0,0] op_sel_hi:[0,1]
v_pk_mul_f32 v[106:107],v[22:23],v[106:107] op_sel:[0,0] op_sel_hi:[0,1]
v_pk_mul_f32 v[12:13],v[22:23],v[12:13] op_sel:[0,0] op_sel_hi:[0,1]
v_pk_mul_f32 v[14:15],v[22:23],v[14:15] op_sel:[0,0] op_sel_hi:[0,1]
v_cvt_pk_bf16_f32 v104,v104,v105
v_cvt_pk_bf16_f32 v105,v106,v107
v_cvt_pk_bf16_f32 v106,v12,v13
v_cvt_pk_bf16_f32 v107,v14,v15
v_mfma_f32_16x16x32_bf16 v[140:143],a[152:155],v[192:195],v[140:143]
v_accvgpr_read_b32 v108,a88
v_accvgpr_read_b32 v109,a89
v_accvgpr_read_b32 v110,a90
v_accvgpr_read_b32 v111,a91
v_accvgpr_read_b32 v12,a92
v_accvgpr_read_b32 v13,a93
v_accvgpr_read_b32 v14,a94
v_accvgpr_read_b32 v15,a95
v_pk_mul_f32 v[108:109],v[22:23],v[108:109] op_sel:[0,0] op_sel_hi:[0,1]
v_pk_mul_f32 v[110:111],v[22:23],v[110:111] op_sel:[0,0] op_sel_hi:[0,1]
v_pk_mul_f32 v[12:13],v[22:23],v[12:13] op_sel:[0,0] op_sel_hi:[0,1]
v_pk_mul_f32 v[14:15],v[22:23],v[14:15] op_sel:[0,0] op_sel_hi:[0,1]
v_cvt_pk_bf16_f32 v108,v108,v109
v_cvt_pk_bf16_f32 v109,v110,v111
v_cvt_pk_bf16_f32 v110,v12,v13
v_cvt_pk_bf16_f32 v111,v14,v15
v_mfma_f32_16x16x32_bf16 v[140:143],a[148:151],v[160:163],v[140:143]
v_cmp_nlt_f32 s[58:59],v56,s63
s_nop 1
s_or_b64 s[56:57],s[56:57],s[58:59]
v_mfma_f32_16x16x32_bf16 v[140:143],a[144:147],v[128:131],v[140:143]
v_exp_f32 v228,v228
v_mfma_f32_16x16x32_bf16 v[148:151],a[160:163],v[128:131],v[32:35]
v_exp_f32 v229,v229
v_mfma_f32_16x16x32_bf16 v[148:151],a[164:167],v[160:163],v[148:151]
v_exp_f32 v230,v230
v_mfma_f32_16x16x32_bf16 v[148:151],a[168:171],v[192:195],v[148:151]
v_exp_f32 v231,v231
v_mfma_f32_16x16x32_bf16 v[148:151],a[172:175],v[224:227],v[148:151]
v_cvt_pk_bf16_f32 v228,v228,v229
v_cvt_pk_bf16_f32 v229,v230,v231
v_mfma_f32_16x16x32_bf16 v[156:159],a[188:191],v[224:227],v[32:35]
buffer_load_dwordx4 v5,s[20:23],s47 offen offset:3072 nt lds
v_mfma_f32_16x16x32_bf16 v[156:159],a[184:187],v[192:195],v[156:159]
v_exp_f32 v236,v236
v_mfma_f32_16x16x32_bf16 v[156:159],a[180:183],v[160:163],v[156:159]
v_exp_f32 v237,v237
v_mfma_f32_16x16x32_bf16 v[156:159],a[176:179],v[128:131],v[156:159]
v_exp_f32 v238,v238
v_mfma_f32_16x16x32_bf16 a[64:67],a[192:195],v[196:199],0
v_exp_f32 v239,v239
v_mfma_f32_16x16x32_bf16 a[64:67],a[196:199],v[212:215],a[64:67]
v_exp_f32 v244,v244
v_mfma_f32_16x16x32_bf16 a[68:71],a[204:207],v[212:215],0
v_exp_f32 v245,v245
v_mfma_f32_16x16x32_bf16 a[68:71],a[200:203],v[196:199],a[68:71]
v_exp_f32 v246,v246
v_mfma_f32_16x16x32_bf16 a[72:75],a[208:211],v[196:199],0
v_exp_f32 v247,v247
v_mfma_f32_16x16x32_bf16 a[72:75],a[212:215],v[212:215],a[72:75]
v_cvt_pk_bf16_f32 v230,v236,v237
v_cvt_pk_bf16_f32 v231,v238,v239
v_mfma_f32_16x16x32_bf16 a[76:79],a[220:223],v[212:215],0
v_cvt_pk_bf16_f32 v244,v244,v245
v_cvt_pk_bf16_f32 v245,v246,v247
v_mfma_f32_16x16x32_bf16 a[76:79],a[216:219],v[196:199],a[76:79]
s_add_u32 m0,65536,s52
v_exp_f32 v252,v252
v_mfma_f32_16x16x32_bf16 a[80:83],a[224:227],v[196:199],0
v_exp_f32 v253,v253
v_mfma_f32_16x16x32_bf16 a[80:83],a[228:231],v[212:215],a[80:83]
v_exp_f32 v254,v254
v_mfma_f32_16x16x32_bf16 a[84:87],a[236:239],v[212:215],0
buffer_load_dwordx4 v4,s[24:27],s47 offen nt lds
v_mfma_f32_16x16x32_bf16 a[84:87],a[232:235],v[196:199],a[84:87]
v_exp_f32 v255,v255
v_mfma_f32_16x16x32_bf16 a[88:91],a[240:243],v[196:199],0
v_rcp_f32 v23,v23
v_mfma_f32_16x16x32_bf16 a[88:91],a[244:247],v[212:215],a[88:91]
v_cvt_pk_bf16_f32 v246,v252,v253
v_cvt_pk_bf16_f32 v247,v254,v255
v_mfma_f32_16x16x32_bf16 a[92:95],a[252:255],v[212:215],0
v_mul_f32 v112,v23,v112
v_mul_f32 v113,v23,v113
v_mfma_f32_16x16x32_bf16 a[92:95],a[248:251],v[196:199],a[92:95]
v_cvt_pk_bf16_f32 v112,v112,v113
v_mul_f32 v114,v23,v114
v_mfma_f32_16x16x32_bf16 v[56:59],v[28:31],v[196:199],0
v_mul_f32 v115,v23,v115
v_cvt_pk_bf16_f32 v113,v114,v115
v_mfma_f32_16x16x32_bf16 v[56:59],v[28:31],v[212:215],v[56:59]
v_accvgpr_read_b32 v114,a100
v_accvgpr_read_b32 v115,a101
v_mfma_f32_16x16x32_bf16 v[164:167],a[128:131],v[136:139],v[36:39]
v_accvgpr_read_b32 v12,a102
v_accvgpr_read_b32 v13,a103
v_mfma_f32_16x16x32_bf16 v[164:167],a[132:135],v[168:171],v[164:167]
v_mul_f32 v114,v23,v114
v_mul_f32 v115,v23,v115
v_mfma_f32_16x16x32_bf16 v[164:167],a[136:139],v[200:203],v[164:167]
v_mul_f32 v12,v23,v12
v_mul_f32 v13,v23,v13
v_mfma_f32_16x16x32_bf16 v[164:167],a[140:143],v[232:235],v[164:167]
v_cvt_pk_bf16_f32 v114,v114,v115
v_cvt_pk_bf16_f32 v115,v12,v13
v_mfma_f32_16x16x32_bf16 v[172:175],a[156:159],v[232:235],v[36:39]
v_accvgpr_read_b32 v116,a104
v_accvgpr_read_b32 v117,a105
v_accvgpr_read_b32 v118,a106
v_accvgpr_read_b32 v119,a107
v_accvgpr_read_b32 v12,a108
v_accvgpr_read_b32 v13,a109
v_accvgpr_read_b32 v14,a110
v_accvgpr_read_b32 v15,a111
v_pk_mul_f32 v[116:117],v[22:23],v[116:117] op_sel:[1,0] op_sel_hi:[1,1]
v_pk_mul_f32 v[118:119],v[22:23],v[118:119] op_sel:[1,0] op_sel_hi:[1,1]
v_pk_mul_f32 v[12:13],v[22:23],v[12:13] op_sel:[1,0] op_sel_hi:[1,1]
v_pk_mul_f32 v[14:15],v[22:23],v[14:15] op_sel:[1,0] op_sel_hi:[1,1]
v_cvt_pk_bf16_f32 v116,v116,v117
v_cvt_pk_bf16_f32 v117,v118,v119
v_cvt_pk_bf16_f32 v118,v12,v13
v_cvt_pk_bf16_f32 v119,v14,v15
v_mfma_f32_16x16x32_bf16 v[172:175],a[152:155],v[200:203],v[172:175]
v_accvgpr_read_b32 v120,a112
v_accvgpr_read_b32 v121,a113
v_accvgpr_read_b32 v122,a114
v_accvgpr_read_b32 v123,a115
v_accvgpr_read_b32 v12,a116
v_accvgpr_read_b32 v13,a117
v_accvgpr_read_b32 v14,a118
v_accvgpr_read_b32 v15,a119
v_pk_mul_f32 v[120:121],v[22:23],v[120:121] op_sel:[1,0] op_sel_hi:[1,1]
v_pk_mul_f32 v[122:123],v[22:23],v[122:123] op_sel:[1,0] op_sel_hi:[1,1]
v_pk_mul_f32 v[12:13],v[22:23],v[12:13] op_sel:[1,0] op_sel_hi:[1,1]
v_pk_mul_f32 v[14:15],v[22:23],v[14:15] op_sel:[1,0] op_sel_hi:[1,1]
v_cvt_pk_bf16_f32 v120,v120,v121
v_cvt_pk_bf16_f32 v121,v122,v123
v_cvt_pk_bf16_f32 v122,v12,v13
v_cvt_pk_bf16_f32 v123,v14,v15
v_mfma_f32_16x16x32_bf16 v[172:175],a[148:151],v[168:171],v[172:175]
buffer_load_dwordx4 v5,s[24:27],s47 offen offset:1024 nt lds
v_mfma_f32_16x16x32_bf16 v[172:175],a[144:147],v[136:139],v[172:175]
v_accvgpr_read_b32 v124,a120
v_accvgpr_read_b32 v125,a121
v_accvgpr_read_b32 v126,a122
v_accvgpr_read_b32 v127,a123
v_accvgpr_read_b32 v12,a124
v_accvgpr_read_b32 v13,a125
v_accvgpr_read_b32 v14,a126
v_accvgpr_read_b32 v15,a127
v_pk_mul_f32 v[124:125],v[22:23],v[124:125] op_sel:[1,0] op_sel_hi:[1,1]
v_pk_mul_f32 v[126:127],v[22:23],v[126:127] op_sel:[1,0] op_sel_hi:[1,1]
v_pk_mul_f32 v[12:13],v[22:23],v[12:13] op_sel:[1,0] op_sel_hi:[1,1]
v_pk_mul_f32 v[14:15],v[22:23],v[14:15] op_sel:[1,0] op_sel_hi:[1,1]
v_cvt_pk_bf16_f32 v124,v124,v125
v_cvt_pk_bf16_f32 v125,v126,v127
v_cvt_pk_bf16_f32 v126,v12,v13
v_cvt_pk_bf16_f32 v127,v14,v15
v_mfma_f32_16x16x32_bf16 v[180:183],a[160:163],v[136:139],v[36:39]
v_cmp_nlt_f32 s[58:59],v60,s63
s_nop 1
s_or_b64 s[56:57],s[56:57],s[58:59]
v_mfma_f32_16x16x32_bf16 v[180:183],a[164:167],v[168:171],v[180:183]
v_exp_f32 v132,v132
v_mfma_f32_16x16x32_bf16 v[180:183],a[168:171],v[200:203],v[180:183]
v_exp_f32 v133,v133
v_mfma_f32_16x16x32_bf16 v[180:183],a[172:175],v[232:235],v[180:183]
v_exp_f32 v134,v134
v_mfma_f32_16x16x32_bf16 v[188:191],a[188:191],v[232:235],v[36:39]
v_exp_f32 v135,v135
v_mfma_f32_16x16x32_bf16 v[188:191],a[184:187],v[200:203],v[188:191]
v_cvt_pk_bf16_f32 v132,v132,v133
v_cvt_pk_bf16_f32 v133,v134,v135
v_mfma_f32_16x16x32_bf16 v[188:191],a[180:183],v[168:171],v[188:191]
v_exp_f32 v140,v140
v_mfma_f32_16x16x32_bf16 v[188:191],a[176:179],v[136:139],v[188:191]
)A"
R"A(v_exp_f32 v141,v141
v_mfma_f32_16x16x32_bf16 a[96:99],a[192:195],v[228:231],0
ds_read_b64_tr_b16 a[192:193],v2
ds_read_b64_tr_b16 a[194:195],v2 offset:4096
v_mfma_f32_16x16x32_bf16 a[96:99],a[196:199],v[244:247],a[96:99]
ds_read_b64_tr_b16 a[196:197],v2 offset:8192
ds_read_b64_tr_b16 a[198:199],v2 offset:12288
v_mfma_f32_16x16x32_bf16 a[100:103],a[204:207],v[244:247],0
ds_read_b64_tr_b16 a[204:205],v3 offset:8192
ds_read_b64_tr_b16 a[206:207],v3 offset:12288
v_mfma_f32_16x16x32_bf16 a[100:103],a[200:203],v[228:231],a[100:103]
ds_read_b64_tr_b16 a[200:201],v3
ds_read_b64_tr_b16 a[202:203],v3 offset:4096
v_mfma_f32_16x16x32_bf16 a[104:107],a[208:211],v[228:231],0
buffer_load_dwordx4 v4,s[24:27],s47 offen offset:2048 nt lds
v_mfma_f32_16x16x32_bf16 a[104:107],a[212:215],v[244:247],a[104:107]
ds_read_b64_tr_b16 a[208:209],v2 offset:256
ds_read_b64_tr_b16 a[210:211],v2 offset:4352
v_mfma_f32_16x16x32_bf16 a[108:111],a[220:223],v[244:247],0
v_exp_f32 v142,v142
v_mfma_f32_16x16x32_bf16 a[108:111],a[216:219],v[228:231],a[108:111]
v_exp_f32 v143,v143
v_mfma_f32_16x16x32_bf16 a[112:115],a[224:227],v[228:231],0
v_exp_f32 v148,v148
v_mfma_f32_16x16x32_bf16 a[112:115],a[228:231],v[244:247],a[112:115]
v_exp_f32 v149,v149
v_mfma_f32_16x16x32_bf16 a[116:119],a[236:239],v[244:247],0
ds_read_b64_tr_b16 a[212:213],v2 offset:8448
ds_read_b64_tr_b16 a[214:215],v2 offset:12544
v_mfma_f32_16x16x32_bf16 a[116:119],a[232:235],v[228:231],a[116:119]
v_exp_f32 v150,v150
v_mfma_f32_16x16x32_bf16 a[120:123],a[240:243],v[228:231],0
v_exp_f32 v151,v151
v_mfma_f32_16x16x32_bf16 a[120:123],a[244:247],v[244:247],a[120:123]
v_cvt_pk_bf16_f32 v134,v140,v141
v_cvt_pk_bf16_f32 v135,v142,v143
v_mfma_f32_16x16x32_bf16 a[124:127],a[252:255],v[244:247],0
ds_read_b64_tr_b16 a[220:221],v3 offset:8448
v_cvt_pk_bf16_f32 v148,v148,v149
v_mfma_f32_16x16x32_bf16 a[124:127],a[248:251],v[228:231],a[124:127]
v_exp_f32 v156,v156
v_mfma_f32_16x16x32_bf16 v[60:63],v[28:31],v[228:231],0
v_exp_f32 v157,v157
v_mfma_f32_16x16x32_bf16 v[60:63],v[28:31],v[244:247],v[60:63]
v_exp_f32 v158,v158
v_mfma_f32_16x16x32_bf16 v[196:199],a[128:131],v[144:147],v[40:43]
buffer_load_dwordx4 v5,s[24:27],s47 offen offset:3072 nt lds
v_mfma_f32_16x16x32_bf16 v[196:199],a[132:135],v[176:179],v[196:199]
s_add_u32 s47,s47,16384
v_exp_f32 v159,v159
v_mfma_f32_16x16x32_bf16 v[196:199],a[136:139],v[208:211],v[196:199]
s_and_b32 s47,s47,0x3ffff
ds_read_b64_tr_b16 a[222:223],v3 offset:12544
v_cvt_pk_bf16_f32 v149,v150,v151
v_mfma_f32_16x16x32_bf16 v[196:199],a[140:143],v[240:243],v[196:199]
s_or_b32 s47,s47,s46
v_cvt_pk_bf16_f32 v150,v156,v157
v_cvt_pk_bf16_f32 v151,v158,v159
v_mfma_f32_16x16x32_bf16 v[204:207],a[156:159],v[240:243],v[40:43]
ds_read_b64_tr_b16 a[216:217],v3 offset:256
ds_read_b64_tr_b16 a[218:219],v3 offset:4352
v_mfma_f32_16x16x32_bf16 v[204:207],a[152:155],v[208:211],v[204:207]
ds_read_b64_tr_b16 a[224:225],v2 offset:512
ds_read_b64_tr_b16 a[226:227],v2 offset:4608
v_mfma_f32_16x16x32_bf16 v[204:207],a[148:151],v[176:179],v[204:207]
ds_read_b64_tr_b16 a[228:229],v2 offset:8704
ds_read_b64_tr_b16 a[230:231],v2 offset:12800
v_mfma_f32_16x16x32_bf16 v[204:207],a[144:147],v[144:147],v[204:207]
ds_read_b64_tr_b16 a[236:237],v3 offset:8704
ds_read_b64_tr_b16 a[238:239],v3 offset:12800
v_mfma_f32_16x16x32_bf16 v[212:215],a[160:163],v[144:147],v[40:43]
ds_read_b64_tr_b16 a[232:233],v3 offset:512
ds_read_b64_tr_b16 a[234:235],v3 offset:4608
v_mfma_f32_16x16x32_bf16 v[212:215],a[164:167],v[176:179],v[212:215]
ds_read_b64_tr_b16 a[240:241],v2 offset:768
ds_read_b64_tr_b16 a[242:243],v2 offset:4864
v_mfma_f32_16x16x32_bf16 v[212:215],a[168:171],v[208:211],v[212:215]
ds_read_b64_tr_b16 a[244:245],v2 offset:8960
ds_read_b64_tr_b16 a[246:247],v2 offset:13056
v_mfma_f32_16x16x32_bf16 v[212:215],a[172:175],v[240:243],v[212:215]
ds_read_b64_tr_b16 a[252:253],v3 offset:8960
ds_read_b64_tr_b16 a[254:255],v3 offset:13056
v_mfma_f32_16x16x32_bf16 v[220:223],a[188:191],v[240:243],v[40:43]
ds_read_b64_tr_b16 a[248:249],v3 offset:768
ds_read_b64_tr_b16 a[250:251],v3 offset:4864
v_mfma_f32_16x16x32_bf16 v[220:223],a[184:187],v[208:211],v[220:223]
v_mul_f32 v98,v22,v98
v_mul_f32 v99,v22,v99
v_mfma_f32_16x16x32_bf16 v[220:223],a[180:183],v[176:179],v[220:223]
v_exp_f32 v164,v164
v_mfma_f32_16x16x32_bf16 v[220:223],a[176:179],v[144:147],v[220:223]
s_waitcnt lgkmcnt(10)
v_exp_f32 v165,v165
v_mfma_f32_16x16x32_bf16 a[0:3],a[192:195],v[132:135],a[0:3]
v_exp_f32 v180,v180
v_mfma_f32_16x16x32_bf16 a[0:3],a[196:199],v[148:151],a[0:3]
v_exp_f32 v181,v181
v_mfma_f32_16x16x32_bf16 a[4:7],a[204:207],v[148:151],a[4:7]
v_mul_f32 v16,v22,v16
v_mul_f32 v17,v22,v17
v_mfma_f32_16x16x32_bf16 a[4:7],a[200:203],v[132:135],a[4:7]
v_cvt_pk_bf16_f32 v98,v98,v99
v_cvt_pk_bf16_f32 v99,v16,v17
v_mfma_f32_16x16x32_bf16 a[8:11],a[208:211],v[132:135],a[8:11]
s_memrealtime s[76:77]
v_exp_f32 v166,v166
v_mfma_f32_16x16x32_bf16 a[8:11],a[212:215],v[148:151],a[8:11]
v_exp_f32 v167,v167
v_mfma_f32_16x16x32_bf16 a[12:15],a[220:223],v[148:151],a[12:15]
v_exp_f32 v182,v182
v_mfma_f32_16x16x32_bf16 a[12:15],a[216:219],v[132:135],a[12:15]
v_exp_f32 v183,v183
v_mfma_f32_16x16x32_bf16 a[16:19],a[224:227],v[132:135],a[16:19]
v_cvt_pk_bf16_f32 v164,v164,v165
v_cvt_pk_bf16_f32 v180,v180,v181
v_mfma_f32_16x16x32_bf16 a[16:19],a[228:231],v[148:151],a[16:19]
v_exp_f32 v172,v172
v_mfma_f32_16x16x32_bf16 a[20:23],a[236:239],v[148:151],a[20:23]
s_waitcnt lgkmcnt(0)
v_exp_f32 v173,v173
v_mfma_f32_16x16x32_bf16 a[20:23],a[232:235],v[132:135],a[20:23]
s_waitcnt lgkmcnt(0)
v_exp_f32 v174,v174
v_mfma_f32_16x16x32_bf16 a[24:27],a[240:243],v[132:135],a[24:27]
s_sub_u32 s79,s78,s76
v_exp_f32 v175,v175
v_mfma_f32_16x16x32_bf16 a[24:27],a[244:247],v[148:151],a[24:27]
s_add_u32 s78,s76,s60
v_exp_f32 v188,v188
v_mfma_f32_16x16x32_bf16 a[28:31],a[252:255],v[148:151],a[28:31]
s_cmp_gt_i32 s79,0
s_cbranch_scc1 L_ps6_%=
L_pr6_%=:
v_cvt_pk_bf16_f32 v165,v166,v167
v_mfma_f32_16x16x32_bf16 a[28:31],a[248:251],v[132:135],a[28:31]
v_exp_f32 v189,v189
v_exp_f32 v190,v190
v_exp_f32 v191,v191
v_mfma_f32_16x16x32_bf16 v[48:51],v[28:31],v[132:135],v[48:51]
s_cmp_lg_u64 s[56:57],0
s_cbranch_scc1 L_slow0_%=
L_sret0_%=:
v_cvt_pk_bf16_f32 v181,v182,v183
v_add_u32 v1,s48,v1
v_cvt_pk_bf16_f32 v166,v172,v173
v_cvt_pk_bf16_f32 v167,v174,v175
v_cvt_pk_bf16_f32 v182,v188,v189
v_cvt_pk_bf16_f32 v183,v190,v191
v_mfma_f32_16x16x32_bf16 v[48:51],v[28:31],v[148:151],v[48:51]
s_mov_b32 s53,11
.p2align 5
L_att1_%=:
v_mfma_f32_16x16x32_bf16 v[228:231],a[128:131],v[152:155],v[44:47]
s_waitcnt vmcnt(8) lgkmcnt(0)
v_exp_f32 v196,v196
v_mfma_f32_16x16x32_bf16 v[228:231],a[132:135],v[184:187],v[228:231]
s_barrier
s_add_u32 m0,s49,s51
ds_read_b128 a[128:131],v1
v_mfma_f32_16x16x32_bf16 v[228:231],a[136:139],v[216:219],v[228:231]
s_mov_b32 s90,L_out_end_%=-L_pc_%=+128
v_exp_f32 v197,v197
v_mfma_f32_16x16x32_bf16 v[228:231],a[140:143],v[248:251],v[228:231]
s_lshr_b32 s45,s45,1
ds_read_b128 a[132:135],v1 offset:256
v_cvt_pk_bf16_f32 v196,v196,v197
v_mfma_f32_16x16x32_bf16 v[236:239],a[156:159],v[248:251],v[44:47]
s_add_u32 s62,s50,32768
buffer_load_dwordx4 v4,s[20:23],s47 offen nt lds
v_mfma_f32_16x16x32_bf16 v[236:239],a[152:155],v[216:219],v[236:239]
s_and_b32 s62,s62,131071
ds_read_b128 a[136:139],v1 offset:512
v_add_u32 v2,s48,v2
v_mfma_f32_16x16x32_bf16 v[236:239],a[148:151],v[184:187],v[236:239]
s_add_u32 s54,s54,16384
v_exp_f32 v198,v198
v_mfma_f32_16x16x32_bf16 v[236:239],a[144:147],v[152:155],v[236:239]
s_sub_u32 s53,s53,1
ds_read_b128 a[140:143],v1 offset:768
v_add_u32 v3,s48,v3
v_mfma_f32_16x16x32_bf16 v[244:247],a[160:163],v[152:155],v[44:47]
s_sub_u32 s48,s62,s50
v_exp_f32 v199,v199
v_mfma_f32_16x16x32_bf16 v[244:247],a[164:167],v[184:187],v[244:247]
s_and_b32 s46,s54,0xfffc0000
ds_read_b128 a[156:159],v1 offset:4864
v_cvt_pk_bf16_f32 v197,v198,v199
v_mfma_f32_16x16x32_bf16 v[244:247],a[168:171],v[216:219],v[244:247]
s_mov_b32 s50,s62
v_exp_f32 v204,v204
v_mfma_f32_16x16x32_bf16 v[244:247],a[172:175],v[248:251],v[244:247]
ds_read_b128 a[152:155],v1 offset:4608
v_mfma_f32_16x16x32_bf16 v[252:255],a[188:191],v[248:251],v[44:47]
v_exp_f32 v205,v205
v_mfma_f32_16x16x32_bf16 v[252:255],a[184:187],v[216:219],v[252:255]
ds_read_b128 a[148:151],v1 offset:4352
v_cvt_pk_bf16_f32 v198,v204,v205
v_mfma_f32_16x16x32_bf16 v[252:255],a[180:183],v[184:187],v[252:255]
v_exp_f32 v206,v206
v_mfma_f32_16x16x32_bf16 v[252:255],a[176:179],v[152:155],v[252:255]
ds_read_b128 a[144:147],v1 offset:4096
v_mfma_f32_16x16x32_bf16 a[32:35],a[192:195],v[164:167],a[32:35]
v_exp_f32 v207,v207
v_mfma_f32_16x16x32_bf16 a[32:35],a[196:199],v[180:183],a[32:35]
ds_read_b128 a[160:163],v1 offset:8192
v_cvt_pk_bf16_f32 v199,v206,v207
v_mfma_f32_16x16x32_bf16 a[36:39],a[204:207],v[180:183],a[36:39]
buffer_load_dwordx4 v4,s[20:23],s47 offen offset:1024 nt lds
v_mfma_f32_16x16x32_bf16 a[36:39],a[200:203],v[164:167],a[36:39]
ds_read_b128 a[164:167],v1 offset:8448
v_mfma_f32_16x16x32_bf16 a[40:43],a[208:211],v[164:167],a[40:43]
v_exp_f32 v212,v212
v_mfma_f32_16x16x32_bf16 a[40:43],a[212:215],v[180:183],a[40:43]
ds_read_b128 a[168:171],v1 offset:8704
v_mfma_f32_16x16x32_bf16 a[44:47],a[220:223],v[180:183],a[44:47]
v_exp_f32 v213,v213
v_mfma_f32_16x16x32_bf16 a[44:47],a[216:219],v[164:167],a[44:47]
ds_read_b128 a[172:175],v1 offset:8960
v_cvt_pk_bf16_f32 v212,v212,v213
v_mfma_f32_16x16x32_bf16 a[48:51],a[224:227],v[164:167],a[48:51]
v_exp_f32 v214,v214
v_mfma_f32_16x16x32_bf16 a[48:51],a[228:231],v[180:183],a[48:51]
ds_read_b128 a[188:191],v1 offset:13056
v_mfma_f32_16x16x32_bf16 a[52:55],a[236:239],v[180:183],a[52:55]
v_exp_f32 v215,v215
v_mfma_f32_16x16x32_bf16 a[52:55],a[232:235],v[164:167],a[52:55]
ds_read_b128 a[184:187],v1 offset:12800
v_cvt_pk_bf16_f32 v213,v214,v215
v_mfma_f32_16x16x32_bf16 a[56:59],a[240:243],v[164:167],a[56:59]
v_exp_f32 v220,v220
v_mfma_f32_16x16x32_bf16 a[56:59],a[244:247],v[180:183],a[56:59]
ds_read_b128 a[180:183],v1 offset:12544
v_mfma_f32_16x16x32_bf16 a[60:63],a[252:255],v[180:183],a[60:63]
v_exp_f32 v221,v221
v_mfma_f32_16x16x32_bf16 a[60:63],a[248:251],v[164:167],a[60:63]
ds_read_b128 a[176:179],v1 offset:12288
v_cvt_pk_bf16_f32 v214,v220,v221
v_mfma_f32_16x16x32_bf16 v[52:55],v[28:31],v[164:167],v[52:55]
buffer_load_dwordx4 v5,s[20:23],s47 offen offset:2048 nt lds
v_mfma_f32_16x16x32_bf16 v[52:55],v[28:31],v[180:183],v[52:55]
s_waitcnt lgkmcnt(3)
v_exp_f32 v222,v222
v_mfma_f32_16x16x32_bf16 v[132:135],a[128:131],v[128:131],v[32:35]
v_exp_f32 v223,v223
v_mfma_f32_16x16x32_bf16 v[132:135],a[132:135],v[160:163],v[132:135]
v_cvt_pk_bf16_f32 v215,v222,v223
v_add_u32 v1,s48,v1
v_mfma_f32_16x16x32_bf16 v[132:135],a[136:139],v[192:195],v[132:135]
v_exp_f32 v228,v228
v_mfma_f32_16x16x32_bf16 v[132:135],a[140:143],v[224:227],v[132:135]
v_exp_f32 v229,v229
v_mfma_f32_16x16x32_bf16 v[140:143],a[156:159],v[224:227],v[32:35]
v_exp_f32 v230,v230
v_mfma_f32_16x16x32_bf16 v[140:143],a[152:155],v[192:195],v[140:143]
v_exp_f32 v231,v231
v_mfma_f32_16x16x32_bf16 v[140:143],a[148:151],v[160:163],v[140:143]
v_cvt_pk_bf16_f32 v228,v228,v229
v_cvt_pk_bf16_f32 v229,v230,v231
v_mfma_f32_16x16x32_bf16 v[140:143],a[144:147],v[128:131],v[140:143]
v_exp_f32 v236,v236
v_mfma_f32_16x16x32_bf16 v[148:151],a[160:163],v[128:131],v[32:35]
v_exp_f32 v237,v237
v_mfma_f32_16x16x32_bf16 v[148:151],a[164:167],v[160:163],v[148:151]
v_exp_f32 v238,v238
v_mfma_f32_16x16x32_bf16 v[148:151],a[168:171],v[192:195],v[148:151]
v_exp_f32 v239,v239
v_mfma_f32_16x16x32_bf16 v[148:151],a[172:175],v[224:227],v[148:151]
v_exp_f32 v244,v244
v_mfma_f32_16x16x32_bf16 v[156:159],a[188:191],v[224:227],v[32:35]
s_waitcnt lgkmcnt(0)
buffer_load_dwordx4 v5,s[20:23],s47 offen offset:3072 nt lds
v_mfma_f32_16x16x32_bf16 v[156:159],a[184:187],v[192:195],v[156:159]
v_exp_f32 v245,v245
v_mfma_f32_16x16x32_bf16 v[156:159],a[180:183],v[160:163],v[156:159]
v_exp_f32 v246,v246
v_mfma_f32_16x16x32_bf16 v[156:159],a[176:179],v[128:131],v[156:159]
v_exp_f32 v247,v247
v_mfma_f32_16x16x32_bf16 a[64:67],a[192:195],v[196:199],a[64:67]
v_cvt_pk_bf16_f32 v230,v236,v237
)A"
R"A(v_cvt_pk_bf16_f32 v231,v238,v239
v_mfma_f32_16x16x32_bf16 a[64:67],a[196:199],v[212:215],a[64:67]
v_cvt_pk_bf16_f32 v244,v244,v245
v_cvt_pk_bf16_f32 v245,v246,v247
v_mfma_f32_16x16x32_bf16 a[68:71],a[204:207],v[212:215],a[68:71]
v_exp_f32 v252,v252
v_mfma_f32_16x16x32_bf16 a[68:71],a[200:203],v[196:199],a[68:71]
v_exp_f32 v253,v253
v_mfma_f32_16x16x32_bf16 a[72:75],a[208:211],v[196:199],a[72:75]
v_exp_f32 v254,v254
v_mfma_f32_16x16x32_bf16 a[72:75],a[212:215],v[212:215],a[72:75]
v_exp_f32 v255,v255
v_mfma_f32_16x16x32_bf16 a[76:79],a[220:223],v[212:215],a[76:79]
v_cvt_pk_bf16_f32 v246,v252,v253
v_cvt_pk_bf16_f32 v247,v254,v255
v_mfma_f32_16x16x32_bf16 a[76:79],a[216:219],v[196:199],a[76:79]
s_add_u32 m0,s49,s52
v_exp_f32 v132,v132
v_mfma_f32_16x16x32_bf16 a[80:83],a[224:227],v[196:199],a[80:83]
s_xor_b32 s49,s50,65536
v_exp_f32 v133,v133
v_mfma_f32_16x16x32_bf16 a[80:83],a[228:231],v[212:215],a[80:83]
v_exp_f32 v134,v134
v_mfma_f32_16x16x32_bf16 a[84:87],a[236:239],v[212:215],a[84:87]
buffer_load_dwordx4 v4,s[24:27],s47 offen nt lds
v_mfma_f32_16x16x32_bf16 a[84:87],a[232:235],v[196:199],a[84:87]
v_exp_f32 v135,v135
v_mfma_f32_16x16x32_bf16 a[88:91],a[240:243],v[196:199],a[88:91]
v_cvt_pk_bf16_f32 v132,v132,v133
v_cvt_pk_bf16_f32 v133,v134,v135
v_mfma_f32_16x16x32_bf16 a[88:91],a[244:247],v[212:215],a[88:91]
v_exp_f32 v140,v140
v_mfma_f32_16x16x32_bf16 a[92:95],a[252:255],v[212:215],a[92:95]
v_exp_f32 v141,v141
v_mfma_f32_16x16x32_bf16 a[92:95],a[248:251],v[196:199],a[92:95]
v_exp_f32 v142,v142
v_mfma_f32_16x16x32_bf16 v[56:59],v[28:31],v[196:199],v[56:59]
v_exp_f32 v143,v143
v_mfma_f32_16x16x32_bf16 v[56:59],v[28:31],v[212:215],v[56:59]
v_exp_f32 v148,v148
v_mfma_f32_16x16x32_bf16 v[164:167],a[128:131],v[136:139],v[36:39]
v_exp_f32 v149,v149
v_mfma_f32_16x16x32_bf16 v[164:167],a[132:135],v[168:171],v[164:167]
v_exp_f32 v150,v150
v_mfma_f32_16x16x32_bf16 v[164:167],a[136:139],v[200:203],v[164:167]
v_exp_f32 v151,v151
v_mfma_f32_16x16x32_bf16 v[164:167],a[140:143],v[232:235],v[164:167]
v_cvt_pk_bf16_f32 v134,v140,v141
v_cvt_pk_bf16_f32 v135,v142,v143
v_mfma_f32_16x16x32_bf16 v[172:175],a[156:159],v[232:235],v[36:39]
v_cvt_pk_bf16_f32 v148,v148,v149
v_cvt_pk_bf16_f32 v149,v150,v151
v_mfma_f32_16x16x32_bf16 v[172:175],a[152:155],v[200:203],v[172:175]
v_exp_f32 v156,v156
v_mfma_f32_16x16x32_bf16 v[172:175],a[148:151],v[168:171],v[172:175]
buffer_load_dwordx4 v5,s[24:27],s47 offen offset:1024 nt lds
v_mfma_f32_16x16x32_bf16 v[172:175],a[144:147],v[136:139],v[172:175]
v_exp_f32 v157,v157
v_mfma_f32_16x16x32_bf16 v[180:183],a[160:163],v[136:139],v[36:39]
v_exp_f32 v158,v158
v_mfma_f32_16x16x32_bf16 v[180:183],a[164:167],v[168:171],v[180:183]
v_exp_f32 v159,v159
v_mfma_f32_16x16x32_bf16 v[180:183],a[168:171],v[200:203],v[180:183]
v_cvt_pk_bf16_f32 v150,v156,v157
v_cvt_pk_bf16_f32 v151,v158,v159
v_mfma_f32_16x16x32_bf16 v[180:183],a[172:175],v[232:235],v[180:183]
v_exp_f32 v164,v164
v_mfma_f32_16x16x32_bf16 v[188:191],a[188:191],v[232:235],v[36:39]
v_exp_f32 v165,v165
v_mfma_f32_16x16x32_bf16 v[188:191],a[184:187],v[200:203],v[188:191]
v_exp_f32 v166,v166
v_mfma_f32_16x16x32_bf16 v[188:191],a[180:183],v[168:171],v[188:191]
v_exp_f32 v167,v167
v_mfma_f32_16x16x32_bf16 v[188:191],a[176:179],v[136:139],v[188:191]
v_cvt_pk_bf16_f32 v164,v164,v165
v_cvt_pk_bf16_f32 v165,v166,v167
v_mfma_f32_16x16x32_bf16 a[96:99],a[192:195],v[228:231],a[96:99]
ds_read_b64_tr_b16 a[192:193],v2
v_mfma_f32_16x16x32_bf16 a[96:99],a[196:199],v[244:247],a[96:99]
ds_read_b64_tr_b16 a[194:195],v2 offset:4096
v_mfma_f32_16x16x32_bf16 a[100:103],a[204:207],v[244:247],a[100:103]
ds_read_b64_tr_b16 a[196:197],v2 offset:8192
v_mfma_f32_16x16x32_bf16 a[100:103],a[200:203],v[228:231],a[100:103]
ds_read_b64_tr_b16 a[198:199],v2 offset:12288
v_mfma_f32_16x16x32_bf16 a[104:107],a[208:211],v[228:231],a[104:107]
buffer_load_dwordx4 v4,s[24:27],s47 offen offset:2048 nt lds
v_mfma_f32_16x16x32_bf16 a[104:107],a[212:215],v[244:247],a[104:107]
ds_read_b64_tr_b16 a[204:205],v3 offset:8192
v_mfma_f32_16x16x32_bf16 a[108:111],a[220:223],v[244:247],a[108:111]
ds_read_b64_tr_b16 a[206:207],v3 offset:12288
v_mfma_f32_16x16x32_bf16 a[108:111],a[216:219],v[228:231],a[108:111]
ds_read_b64_tr_b16 a[200:201],v3
v_mfma_f32_16x16x32_bf16 a[112:115],a[224:227],v[228:231],a[112:115]
ds_read_b64_tr_b16 a[202:203],v3 offset:4096
v_mfma_f32_16x16x32_bf16 a[112:115],a[228:231],v[244:247],a[112:115]
ds_read_b64_tr_b16 a[208:209],v2 offset:256
v_mfma_f32_16x16x32_bf16 a[116:119],a[236:239],v[244:247],a[116:119]
ds_read_b64_tr_b16 a[210:211],v2 offset:4352
v_mfma_f32_16x16x32_bf16 a[116:119],a[232:235],v[228:231],a[116:119]
ds_read_b64_tr_b16 a[212:213],v2 offset:8448
v_mfma_f32_16x16x32_bf16 a[120:123],a[240:243],v[228:231],a[120:123]
ds_read_b64_tr_b16 a[214:215],v2 offset:12544
v_mfma_f32_16x16x32_bf16 a[120:123],a[244:247],v[244:247],a[120:123]
ds_read_b64_tr_b16 a[220:221],v3 offset:8448
v_mfma_f32_16x16x32_bf16 a[124:127],a[252:255],v[244:247],a[124:127]
ds_read_b64_tr_b16 a[222:223],v3 offset:12544
v_mfma_f32_16x16x32_bf16 a[124:127],a[248:251],v[228:231],a[124:127]
ds_read_b64_tr_b16 a[216:217],v3 offset:256
v_mfma_f32_16x16x32_bf16 v[60:63],v[28:31],v[228:231],v[60:63]
ds_read_b64_tr_b16 a[218:219],v3 offset:4352
v_mfma_f32_16x16x32_bf16 v[60:63],v[28:31],v[244:247],v[60:63]
ds_read_b64_tr_b16 a[224:225],v2 offset:512
v_mfma_f32_16x16x32_bf16 v[196:199],a[128:131],v[144:147],v[40:43]
buffer_load_dwordx4 v5,s[24:27],s47 offen offset:3072 nt lds
v_mfma_f32_16x16x32_bf16 v[196:199],a[132:135],v[176:179],v[196:199]
s_add_u32 s47,s47,16384
s_and_b32 s47,s47,0x3ffff
ds_read_b64_tr_b16 a[226:227],v2 offset:4608
v_mfma_f32_16x16x32_bf16 v[196:199],a[136:139],v[208:211],v[196:199]
s_or_b32 s47,s47,s46
ds_read_b64_tr_b16 a[228:229],v2 offset:8704
v_mfma_f32_16x16x32_bf16 v[196:199],a[140:143],v[240:243],v[196:199]
ds_read_b64_tr_b16 a[230:231],v2 offset:12800
v_mfma_f32_16x16x32_bf16 v[204:207],a[156:159],v[240:243],v[40:43]
ds_read_b64_tr_b16 a[236:237],v3 offset:8704
v_mfma_f32_16x16x32_bf16 v[204:207],a[152:155],v[208:211],v[204:207]
ds_read_b64_tr_b16 a[238:239],v3 offset:12800
v_mfma_f32_16x16x32_bf16 v[204:207],a[148:151],v[176:179],v[204:207]
ds_read_b64_tr_b16 a[232:233],v3 offset:512
v_mfma_f32_16x16x32_bf16 v[204:207],a[144:147],v[144:147],v[204:207]
ds_read_b64_tr_b16 a[234:235],v3 offset:4608
v_mfma_f32_16x16x32_bf16 v[212:215],a[160:163],v[144:147],v[40:43]
ds_read_b64_tr_b16 a[240:241],v2 offset:768
v_mfma_f32_16x16x32_bf16 v[212:215],a[164:167],v[176:179],v[212:215]
ds_read_b64_tr_b16 a[242:243],v2 offset:4864
v_mfma_f32_16x16x32_bf16 v[212:215],a[168:171],v[208:211],v[212:215]
ds_read_b64_tr_b16 a[244:245],v2 offset:8960
v_mfma_f32_16x16x32_bf16 v[212:215],a[172:175],v[240:243],v[212:215]
ds_read_b64_tr_b16 a[246:247],v2 offset:13056
v_mfma_f32_16x16x32_bf16 v[220:223],a[188:191],v[240:243],v[40:43]
ds_read_b64_tr_b16 a[252:253],v3 offset:8960
v_mfma_f32_16x16x32_bf16 v[220:223],a[184:187],v[208:211],v[220:223]
ds_read_b64_tr_b16 a[254:255],v3 offset:13056
v_mfma_f32_16x16x32_bf16 v[220:223],a[180:183],v[176:179],v[220:223]
ds_read_b64_tr_b16 a[248:249],v3 offset:768
v_mfma_f32_16x16x32_bf16 v[220:223],a[176:179],v[144:147],v[220:223]
s_waitcnt lgkmcnt(7)
ds_read_b64_tr_b16 a[250:251],v3 offset:4864
v_mfma_f32_16x16x32_bf16 a[0:3],a[192:195],v[132:135],a[0:3]
buffer_load_dword v26,v24,s[88:91],s93 offen
v_mfma_f32_16x16x32_bf16 a[0:3],a[196:199],v[148:151],a[0:3]
v_exp_f32 v180,v180
v_mfma_f32_16x16x32_bf16 a[4:7],a[204:207],v[148:151],a[4:7]
v_exp_f32 v181,v181
v_mfma_f32_16x16x32_bf16 a[4:7],a[200:203],v[132:135],a[4:7]
v_exp_f32 v182,v182
v_mfma_f32_16x16x32_bf16 a[8:11],a[208:211],v[132:135],a[8:11]
s_memrealtime s[76:77]
v_exp_f32 v183,v183
v_mfma_f32_16x16x32_bf16 a[8:11],a[212:215],v[148:151],a[8:11]
v_exp_f32 v172,v172
v_mfma_f32_16x16x32_bf16 a[12:15],a[220:223],v[148:151],a[12:15]
v_exp_f32 v173,v173
v_mfma_f32_16x16x32_bf16 a[12:15],a[216:219],v[132:135],a[12:15]
v_exp_f32 v174,v174
v_mfma_f32_16x16x32_bf16 a[16:19],a[224:227],v[132:135],a[16:19]
s_bitcmp1_b32 s45,0
s_cbranch_scc0 L_t311_%=
s_mov_b32 m0,s80
s_add_u32 s80,s80,4096
buffer_load_dwordx4 v25,s[24:27],s83 offen nt lds
s_add_u32 s83,s83,4096
L_t311_%=:
v_mfma_f32_16x16x32_bf16 a[16:19],a[228:231],v[148:151],a[16:19]
v_exp_f32 v175,v175
v_mfma_f32_16x16x32_bf16 a[20:23],a[236:239],v[148:151],a[20:23]
v_cvt_pk_bf16_f32 v180,v180,v181
v_cvt_pk_bf16_f32 v181,v182,v183
v_mfma_f32_16x16x32_bf16 a[20:23],a[232:235],v[132:135],a[20:23]
s_waitcnt lgkmcnt(0)
v_exp_f32 v188,v188
v_mfma_f32_16x16x32_bf16 a[24:27],a[240:243],v[132:135],a[24:27]
s_sub_u32 s79,s78,s76
v_exp_f32 v189,v189
v_mfma_f32_16x16x32_bf16 a[24:27],a[244:247],v[148:151],a[24:27]
s_add_u32 s78,s76,s60
v_exp_f32 v190,v190
v_mfma_f32_16x16x32_bf16 a[28:31],a[252:255],v[148:151],a[28:31]
s_cmp_gt_i32 s79,0
s_cbranch_scc1 L_ps7_%=
L_pr7_%=:
v_cvt_pk_bf16_f32 v166,v172,v173
v_mfma_f32_16x16x32_bf16 a[28:31],a[248:251],v[132:135],a[28:31]
v_exp_f32 v191,v191
v_mfma_f32_16x16x32_bf16 v[48:51],v[28:31],v[132:135],v[48:51]
v_cvt_pk_bf16_f32 v167,v174,v175
v_cvt_pk_bf16_f32 v182,v188,v189
v_cvt_pk_bf16_f32 v183,v190,v191
s_cmp_lg_u32 s53,0
v_mfma_f32_16x16x32_bf16 v[48:51],v[28:31],v[148:151],v[48:51]
s_cbranch_scc1 L_att1_%=
L_att1x_%=:
L_endseg_%=:
v_mfma_f32_16x16x32_bf16 v[228:231],a[128:131],v[152:155],v[44:47]
s_waitcnt vmcnt(10)
s_lshl_b32 s43,s41,2
v_exp_f32 v196,v196
v_mfma_f32_16x16x32_bf16 v[228:231],a[132:135],v[184:187],v[228:231]
s_barrier
ds_read_b128 a[128:131],v1
v_add_u32 v2,s48,v2
v_mfma_f32_16x16x32_bf16 v[228:231],a[136:139],v[216:219],v[228:231]
s_add_u32 s43,s43,s70
v_exp_f32 v197,v197
v_mfma_f32_16x16x32_bf16 v[228:231],a[140:143],v[248:251],v[228:231]
s_lshl_b32 s43,s43,19
ds_read_b128 a[132:135],v1 offset:256
v_cvt_pk_bf16_f32 v196,v196,v197
v_mfma_f32_16x16x32_bf16 v[236:239],a[156:159],v[248:251],v[44:47]
s_add_u32 s43,s43,s55
v_exp_f32 v198,v198
v_mfma_f32_16x16x32_bf16 v[236:239],a[152:155],v[216:219],v[236:239]
s_mov_b64 s[28:29],s[12:13]
ds_read_b128 a[136:139],v1 offset:512
v_add_u32 v3,s48,v3
v_mfma_f32_16x16x32_bf16 v[236:239],a[148:151],v[184:187],v[236:239]
s_mov_b32 s48,32768
v_exp_f32 v199,v199
v_mfma_f32_16x16x32_bf16 v[236:239],a[144:147],v[152:155],v[236:239]
s_mov_b32 s30,475136
ds_read_b128 a[140:143],v1 offset:768
v_cvt_pk_bf16_f32 v197,v198,v199
v_mfma_f32_16x16x32_bf16 v[244:247],a[160:163],v[152:155],v[44:47]
s_mov_b32 s31,0x20000
v_exp_f32 v204,v204
v_mfma_f32_16x16x32_bf16 v[244:247],a[164:167],v[184:187],v[244:247]
ds_read_b128 a[156:159],v1 offset:4864
v_lshrrev_b32 v4,6,v6
v_mfma_f32_16x16x32_bf16 v[244:247],a[168:171],v[216:219],v[244:247]
v_exp_f32 v205,v205
v_mfma_f32_16x16x32_bf16 v[244:247],a[172:175],v[248:251],v[244:247]
ds_read_b128 a[152:155],v1 offset:4608
v_cvt_pk_bf16_f32 v198,v204,v205
v_mfma_f32_16x16x32_bf16 v[252:255],a[188:191],v[248:251],v[44:47]
v_exp_f32 v206,v206
v_mfma_f32_16x16x32_bf16 v[252:255],a[184:187],v[216:219],v[252:255]
ds_read_b128 a[148:151],v1 offset:4352
v_mul_u32_u24 v4,320,v4
v_mfma_f32_16x16x32_bf16 v[252:255],a[180:183],v[184:187],v[252:255]
v_exp_f32 v207,v207
v_mfma_f32_16x16x32_bf16 v[252:255],a[176:179],v[152:155],v[252:255]
ds_read_b128 a[144:147],v1 offset:4096
v_cvt_pk_bf16_f32 v199,v206,v207
v_mfma_f32_16x16x32_bf16 a[32:35],a[192:195],v[164:167],a[32:35]
v_exp_f32 v212,v212
v_mfma_f32_16x16x32_bf16 a[32:35],a[196:199],v[180:183],a[32:35]
ds_read_b128 a[160:163],v1 offset:8192
v_bfe_u32 v5,v6,4,2
v_mfma_f32_16x16x32_bf16 a[36:39],a[204:207],v[180:183],a[36:39]
v_exp_f32 v213,v213
v_mfma_f32_16x16x32_bf16 a[36:39],a[200:203],v[164:167],a[36:39]
ds_read_b128 a[164:167],v1 offset:8448
v_cvt_pk_bf16_f32 v212,v212,v213
v_mfma_f32_16x16x32_bf16 a[40:43],a[208:211],v[164:167],a[40:43]
v_exp_f32 v214,v214
v_mfma_f32_16x16x32_bf16 a[40:43],a[212:215],v[180:183],a[40:43]
ds_read_b128 a[168:171],v1 offset:8704
v_lshl_add_u32 v4,v5,3,v4
v_mfma_f32_16x16x32_bf16 a[44:47],a[220:223],v[180:183],a[44:47]
v_exp_f32 v215,v215
v_mfma_f32_16x16x32_bf16 a[44:47],a[216:219],v[164:167],a[44:47]
)A"
R"A(ds_read_b128 a[172:175],v1 offset:8960
v_cvt_pk_bf16_f32 v213,v214,v215
v_mfma_f32_16x16x32_bf16 a[48:51],a[224:227],v[164:167],a[48:51]
v_exp_f32 v220,v220
v_mfma_f32_16x16x32_bf16 a[48:51],a[228:231],v[180:183],a[48:51]
ds_read_b128 a[188:191],v1 offset:13056
v_bfe_u32 v5,v6,8,1
v_mfma_f32_16x16x32_bf16 a[52:55],a[236:239],v[180:183],a[52:55]
v_exp_f32 v221,v221
v_mfma_f32_16x16x32_bf16 a[52:55],a[232:235],v[164:167],a[52:55]
ds_read_b128 a[184:187],v1 offset:12800
v_cvt_pk_bf16_f32 v214,v220,v221
v_mfma_f32_16x16x32_bf16 a[56:59],a[240:243],v[164:167],a[56:59]
v_exp_f32 v222,v222
v_mfma_f32_16x16x32_bf16 a[56:59],a[244:247],v[180:183],a[56:59]
ds_read_b128 a[180:183],v1 offset:12544
v_lshlrev_b32 v5,5,v5
v_mfma_f32_16x16x32_bf16 a[60:63],a[252:255],v[180:183],a[60:63]
v_exp_f32 v223,v223
v_mfma_f32_16x16x32_bf16 a[60:63],a[248:251],v[164:167],a[60:63]
ds_read_b128 a[176:179],v1 offset:12288
v_cvt_pk_bf16_f32 v215,v222,v223
v_mfma_f32_16x16x32_bf16 v[52:55],v[28:31],v[164:167],v[52:55]
v_exp_f32 v228,v228
v_mfma_f32_16x16x32_bf16 v[52:55],v[28:31],v[180:183],v[52:55]
s_waitcnt lgkmcnt(3)
v_exp_f32 v229,v229
v_mfma_f32_16x16x32_bf16 v[132:135],a[128:131],v[128:131],v[32:35]
v_exp_f32 v230,v230
v_mfma_f32_16x16x32_bf16 v[132:135],a[132:135],v[160:163],v[132:135]
v_exp_f32 v231,v231
v_mfma_f32_16x16x32_bf16 v[132:135],a[136:139],v[192:195],v[132:135]
v_cvt_pk_bf16_f32 v228,v228,v229
v_cvt_pk_bf16_f32 v229,v230,v231
v_mfma_f32_16x16x32_bf16 v[132:135],a[140:143],v[224:227],v[132:135]
v_exp_f32 v236,v236
v_mfma_f32_16x16x32_bf16 v[140:143],a[156:159],v[224:227],v[32:35]
s_add_u32 s80,s71,0
s_and_b32 s80,s80,3
s_lshl_b32 s80,s80,1
v_mfma_f32_16x16x32_bf16 v[140:143],a[152:155],v[192:195],v[140:143]
s_mul_i32 s80,s80,163840
s_add_u32 s80,s80,s81
v_add_u32 v1,s48,v1
v_mfma_f32_16x16x32_bf16 v[140:143],a[148:151],v[160:163],v[140:143]
s_add_u32 s83,s80,20480
v_exp_f32 v237,v237
v_mfma_f32_16x16x32_bf16 v[140:143],a[144:147],v[128:131],v[140:143]
v_exp_f32 v238,v238
v_mfma_f32_16x16x32_bf16 v[148:151],a[160:163],v[128:131],v[32:35]
buffer_store_dwordx4 v[64:67],v6,s[36:39],s84 offen sc1
v_mfma_f32_16x16x32_bf16 v[148:151],a[164:167],v[160:163],v[148:151]
v_exp_f32 v239,v239
v_mfma_f32_16x16x32_bf16 v[148:151],a[168:171],v[192:195],v[148:151]
v_exp_f32 v244,v244
v_mfma_f32_16x16x32_bf16 v[148:151],a[172:175],v[224:227],v[148:151]
v_exp_f32 v245,v245
v_mfma_f32_16x16x32_bf16 v[156:159],a[188:191],v[224:227],v[32:35]
s_waitcnt lgkmcnt(0)
v_exp_f32 v246,v246
v_mfma_f32_16x16x32_bf16 v[156:159],a[184:187],v[192:195],v[156:159]
v_exp_f32 v247,v247
v_mfma_f32_16x16x32_bf16 v[156:159],a[180:183],v[160:163],v[156:159]
v_cvt_pk_bf16_f32 v230,v236,v237
v_cvt_pk_bf16_f32 v231,v238,v239
v_mfma_f32_16x16x32_bf16 v[156:159],a[176:179],v[128:131],v[156:159]
v_cvt_pk_bf16_f32 v244,v244,v245
v_cvt_pk_bf16_f32 v245,v246,v247
v_mfma_f32_16x16x32_bf16 a[64:67],a[192:195],v[196:199],a[64:67]
v_exp_f32 v252,v252
v_mfma_f32_16x16x32_bf16 a[64:67],a[196:199],v[212:215],a[64:67]
v_exp_f32 v253,v253
v_mfma_f32_16x16x32_bf16 a[68:71],a[204:207],v[212:215],a[68:71]
v_exp_f32 v254,v254
v_mfma_f32_16x16x32_bf16 a[68:71],a[200:203],v[196:199],a[68:71]
v_exp_f32 v255,v255
v_mfma_f32_16x16x32_bf16 a[72:75],a[208:211],v[196:199],a[72:75]
v_cvt_pk_bf16_f32 v246,v252,v253
v_cvt_pk_bf16_f32 v247,v254,v255
v_mfma_f32_16x16x32_bf16 a[72:75],a[212:215],v[212:215],a[72:75]
v_exp_f32 v132,v132
v_mfma_f32_16x16x32_bf16 a[76:79],a[220:223],v[212:215],a[76:79]
v_exp_f32 v133,v133
v_mfma_f32_16x16x32_bf16 a[76:79],a[216:219],v[196:199],a[76:79]
v_exp_f32 v134,v134
v_mfma_f32_16x16x32_bf16 a[80:83],a[224:227],v[196:199],a[80:83]
v_exp_f32 v135,v135
v_mfma_f32_16x16x32_bf16 a[80:83],a[228:231],v[212:215],a[80:83]
s_add_u32 m0,s82,81920
v_cvt_pk_bf16_f32 v132,v132,v133
v_cvt_pk_bf16_f32 v133,v134,v135
v_mfma_f32_16x16x32_bf16 a[84:87],a[236:239],v[212:215],a[84:87]
v_exp_f32 v140,v140
v_mfma_f32_16x16x32_bf16 a[84:87],a[232:235],v[196:199],a[84:87]
v_exp_f32 v141,v141
v_mfma_f32_16x16x32_bf16 a[88:91],a[240:243],v[196:199],a[88:91]
buffer_load_dwordx4 v7,s[72:75],s80 offen lds
v_mfma_f32_16x16x32_bf16 a[88:91],a[244:247],v[212:215],a[88:91]
v_exp_f32 v142,v142
v_mfma_f32_16x16x32_bf16 a[92:95],a[252:255],v[212:215],a[92:95]
v_exp_f32 v143,v143
v_mfma_f32_16x16x32_bf16 a[92:95],a[248:251],v[196:199],a[92:95]
v_exp_f32 v148,v148
v_mfma_f32_16x16x32_bf16 v[56:59],v[28:31],v[196:199],v[56:59]
buffer_store_dwordx4 v[68:71],v6,s[36:39],s84 offen offset:1024 sc1
v_mfma_f32_16x16x32_bf16 v[56:59],v[28:31],v[212:215],v[56:59]
v_exp_f32 v149,v149
v_mfma_f32_16x16x32_bf16 v[164:167],a[128:131],v[136:139],v[36:39]
v_exp_f32 v150,v150
v_mfma_f32_16x16x32_bf16 v[164:167],a[132:135],v[168:171],v[164:167]
v_exp_f32 v151,v151
v_mfma_f32_16x16x32_bf16 v[164:167],a[136:139],v[200:203],v[164:167]
v_cvt_pk_bf16_f32 v134,v140,v141
v_cvt_pk_bf16_f32 v135,v142,v143
v_mfma_f32_16x16x32_bf16 v[164:167],a[140:143],v[232:235],v[164:167]
v_cvt_pk_bf16_f32 v148,v148,v149
v_cvt_pk_bf16_f32 v149,v150,v151
v_mfma_f32_16x16x32_bf16 v[172:175],a[156:159],v[232:235],v[36:39]
v_exp_f32 v156,v156
v_mfma_f32_16x16x32_bf16 v[172:175],a[152:155],v[200:203],v[172:175]
v_exp_f32 v157,v157
v_mfma_f32_16x16x32_bf16 v[172:175],a[148:151],v[168:171],v[172:175]
v_exp_f32 v158,v158
v_mfma_f32_16x16x32_bf16 v[172:175],a[144:147],v[136:139],v[172:175]
v_exp_f32 v159,v159
v_mfma_f32_16x16x32_bf16 v[180:183],a[160:163],v[136:139],v[36:39]
v_cvt_pk_bf16_f32 v150,v156,v157
v_cvt_pk_bf16_f32 v151,v158,v159
v_mfma_f32_16x16x32_bf16 v[180:183],a[164:167],v[168:171],v[180:183]
v_exp_f32 v164,v164
v_mfma_f32_16x16x32_bf16 v[180:183],a[168:171],v[200:203],v[180:183]
v_exp_f32 v165,v165
v_mfma_f32_16x16x32_bf16 v[180:183],a[172:175],v[232:235],v[180:183]
v_exp_f32 v166,v166
v_mfma_f32_16x16x32_bf16 v[188:191],a[188:191],v[232:235],v[36:39]
v_exp_f32 v167,v167
v_mfma_f32_16x16x32_bf16 v[188:191],a[184:187],v[200:203],v[188:191]
v_cvt_pk_bf16_f32 v164,v164,v165
v_cvt_pk_bf16_f32 v165,v166,v167
v_mfma_f32_16x16x32_bf16 v[188:191],a[180:183],v[168:171],v[188:191]
buffer_load_dwordx4 v8,s[72:75],s80 offen offset:1024 lds
v_mfma_f32_16x16x32_bf16 v[188:191],a[176:179],v[136:139],v[188:191]
v_exp_f32 v172,v172
v_mfma_f32_16x16x32_bf16 a[96:99],a[192:195],v[228:231],a[96:99]
ds_read_b64_tr_b16 a[192:193],v2
ds_read_b64_tr_b16 a[194:195],v2 offset:4096
v_mfma_f32_16x16x32_bf16 a[96:99],a[196:199],v[244:247],a[96:99]
s_add_u32 s28,s28,s43
s_addc_u32 s29,s29,0
ds_read_b64_tr_b16 a[196:197],v2 offset:8192
v_mfma_f32_16x16x32_bf16 a[100:103],a[204:207],v[244:247],a[100:103]
s_and_b32 s29,s29,0xffff
buffer_store_dwordx4 v[72:75],v6,s[36:39],s84 offen offset:2048 sc1
v_mfma_f32_16x16x32_bf16 a[100:103],a[200:203],v[228:231],a[100:103]
ds_read_b64_tr_b16 a[198:199],v2 offset:12288
ds_read_b64_tr_b16 a[204:205],v3 offset:8192
v_mfma_f32_16x16x32_bf16 a[104:107],a[208:211],v[228:231],a[104:107]
ds_read_b64_tr_b16 a[206:207],v3 offset:12288
v_mfma_f32_16x16x32_bf16 a[104:107],a[212:215],v[244:247],a[104:107]
ds_read_b64_tr_b16 a[200:201],v3
v_mfma_f32_16x16x32_bf16 a[108:111],a[220:223],v[244:247],a[108:111]
ds_read_b64_tr_b16 a[202:203],v3 offset:4096
v_mfma_f32_16x16x32_bf16 a[108:111],a[216:219],v[228:231],a[108:111]
ds_read_b64_tr_b16 a[208:209],v2 offset:256
ds_read_b64_tr_b16 a[210:211],v2 offset:4352
v_mfma_f32_16x16x32_bf16 a[112:115],a[224:227],v[228:231],a[112:115]
ds_read_b64_tr_b16 a[212:213],v2 offset:8448
v_mfma_f32_16x16x32_bf16 a[112:115],a[228:231],v[244:247],a[112:115]
ds_read_b64_tr_b16 a[214:215],v2 offset:12544
v_mfma_f32_16x16x32_bf16 a[116:119],a[236:239],v[244:247],a[116:119]
ds_read_b64_tr_b16 a[220:221],v3 offset:8448
v_mfma_f32_16x16x32_bf16 a[116:119],a[232:235],v[228:231],a[116:119]
ds_read_b64_tr_b16 a[222:223],v3 offset:12544
ds_read_b64_tr_b16 a[216:217],v3 offset:256
v_mfma_f32_16x16x32_bf16 a[120:123],a[240:243],v[228:231],a[120:123]
ds_read_b64_tr_b16 a[218:219],v3 offset:4352
v_mfma_f32_16x16x32_bf16 a[120:123],a[244:247],v[244:247],a[120:123]
ds_read_b64_tr_b16 a[224:225],v2 offset:512
v_mfma_f32_16x16x32_bf16 a[124:127],a[252:255],v[244:247],a[124:127]
ds_read_b64_tr_b16 a[226:227],v2 offset:4608
v_mfma_f32_16x16x32_bf16 a[124:127],a[248:251],v[228:231],a[124:127]
ds_read_b64_tr_b16 a[228:229],v2 offset:8704
ds_read_b64_tr_b16 a[230:231],v2 offset:12800
v_mfma_f32_16x16x32_bf16 v[60:63],v[28:31],v[228:231],v[60:63]
ds_read_b64_tr_b16 a[236:237],v3 offset:8704
v_mfma_f32_16x16x32_bf16 v[60:63],v[28:31],v[244:247],v[60:63]
ds_read_b64_tr_b16 a[238:239],v3 offset:12800
v_mfma_f32_16x16x32_bf16 v[196:199],a[128:131],v[144:147],v[40:43]
buffer_load_dwordx4 v9,s[72:75],s80 offen offset:2048 lds
v_mfma_f32_16x16x32_bf16 v[196:199],a[132:135],v[176:179],v[196:199]
ds_read_b64_tr_b16 a[232:233],v3 offset:512
ds_read_b64_tr_b16 a[234:235],v3 offset:4608
v_mfma_f32_16x16x32_bf16 v[196:199],a[136:139],v[208:211],v[196:199]
ds_read_b64_tr_b16 a[240:241],v2 offset:768
v_mfma_f32_16x16x32_bf16 v[196:199],a[140:143],v[240:243],v[196:199]
ds_read_b64_tr_b16 a[242:243],v2 offset:4864
v_mfma_f32_16x16x32_bf16 v[204:207],a[156:159],v[240:243],v[40:43]
ds_read_b64_tr_b16 a[244:245],v2 offset:8960
v_mfma_f32_16x16x32_bf16 v[204:207],a[152:155],v[208:211],v[204:207]
ds_read_b64_tr_b16 a[246:247],v2 offset:13056
ds_read_b64_tr_b16 a[252:253],v3 offset:8960
v_mfma_f32_16x16x32_bf16 v[204:207],a[148:151],v[176:179],v[204:207]
buffer_store_dwordx4 v[76:79],v6,s[36:39],s84 offen offset:3072 sc1
v_mfma_f32_16x16x32_bf16 v[204:207],a[144:147],v[144:147],v[204:207]
s_add_u32 s84,s55,65536
ds_read_b64_tr_b16 a[254:255],v3 offset:13056
ds_read_b64_tr_b16 a[248:249],v3 offset:768
v_mfma_f32_16x16x32_bf16 v[212:215],a[160:163],v[144:147],v[40:43]
s_max_i32 s84,s84,s18
ds_read_b64_tr_b16 a[250:251],v3 offset:4864
v_add_u32 v2,s48,v2
v_mfma_f32_16x16x32_bf16 v[212:215],a[164:167],v[176:179],v[212:215]
v_exp_f32 v173,v173
v_mfma_f32_16x16x32_bf16 v[212:215],a[168:171],v[208:211],v[212:215]
v_exp_f32 v174,v174
v_mfma_f32_16x16x32_bf16 v[212:215],a[172:175],v[240:243],v[212:215]
v_exp_f32 v175,v175
v_mfma_f32_16x16x32_bf16 v[220:223],a[188:191],v[240:243],v[40:43]
v_exp_f32 v180,v180
v_mfma_f32_16x16x32_bf16 v[220:223],a[184:187],v[208:211],v[220:223]
v_exp_f32 v181,v181
v_mfma_f32_16x16x32_bf16 v[220:223],a[180:183],v[176:179],v[220:223]
v_exp_f32 v182,v182
v_mfma_f32_16x16x32_bf16 v[220:223],a[176:179],v[144:147],v[220:223]
s_waitcnt lgkmcnt(1)
v_exp_f32 v183,v183
v_mfma_f32_16x16x32_bf16 a[0:3],a[192:195],v[132:135],a[0:3]
v_cvt_pk_bf16_f32 v166,v172,v173
v_cvt_pk_bf16_f32 v167,v174,v175
v_mfma_f32_16x16x32_bf16 a[0:3],a[196:199],v[148:151],a[0:3]
v_cvt_pk_bf16_f32 v180,v180,v181
v_cvt_pk_bf16_f32 v181,v182,v183
v_mfma_f32_16x16x32_bf16 a[4:7],a[204:207],v[148:151],a[4:7]
v_exp_f32 v188,v188
v_mfma_f32_16x16x32_bf16 a[4:7],a[200:203],v[132:135],a[4:7]
v_exp_f32 v189,v189
v_mfma_f32_16x16x32_bf16 a[8:11],a[208:211],v[132:135],a[8:11]
buffer_load_dwordx4 v10,s[72:75],s80 offen offset:3072 lds
v_mfma_f32_16x16x32_bf16 a[8:11],a[212:215],v[148:151],a[8:11]
v_exp_f32 v190,v190
v_mfma_f32_16x16x32_bf16 a[12:15],a[220:223],v[148:151],a[12:15]
v_exp_f32 v191,v191
v_mfma_f32_16x16x32_bf16 a[12:15],a[216:219],v[132:135],a[12:15]
v_cvt_pk_bf16_f32 v182,v188,v189
v_cvt_pk_bf16_f32 v183,v190,v191
v_mfma_f32_16x16x32_bf16 a[16:19],a[224:227],v[132:135],a[16:19]
v_exp_f32 v196,v196
v_mfma_f32_16x16x32_bf16 a[16:19],a[228:231],v[148:151],a[16:19]
v_exp_f32 v197,v197
v_mfma_f32_16x16x32_bf16 a[20:23],a[236:239],v[148:151],a[20:23]
v_exp_f32 v198,v198
v_mfma_f32_16x16x32_bf16 a[20:23],a[232:235],v[132:135],a[20:23]
v_exp_f32 v199,v199
v_mfma_f32_16x16x32_bf16 a[24:27],a[240:243],v[132:135],a[24:27]
buffer_store_dwordx4 v[80:83],v6,s[36:39],s85 offen sc1
v_mfma_f32_16x16x32_bf16 a[24:27],a[244:247],v[148:151],a[24:27]
v_cvt_pk_bf16_f32 v196,v196,v197
v_cvt_pk_bf16_f32 v197,v198,v199
v_mfma_f32_16x16x32_bf16 a[28:31],a[252:255],v[148:151],a[28:31]
s_waitcnt lgkmcnt(0)
v_exp_f32 v204,v204
v_mfma_f32_16x16x32_bf16 a[28:31],a[248:251],v[132:135],a[28:31]
)A"
R"A(v_exp_f32 v205,v205
v_mfma_f32_16x16x32_bf16 v[48:51],v[28:31],v[132:135],v[48:51]
v_exp_f32 v206,v206
v_mfma_f32_16x16x32_bf16 v[48:51],v[28:31],v[148:151],v[48:51]
v_exp_f32 v207,v207
v_mfma_f32_16x16x32_bf16 v[228:231],a[128:131],v[152:155],v[44:47]
s_waitcnt vmcnt(10)
v_exp_f32 v212,v212
v_mfma_f32_16x16x32_bf16 v[228:231],a[132:135],v[184:187],v[228:231]
s_barrier
ds_read_b128 a[128:131],v1
v_cvt_pk_bf16_f32 v198,v204,v205
v_mfma_f32_16x16x32_bf16 v[228:231],a[136:139],v[216:219],v[228:231]
s_add_u32 m0,s82,86016
v_exp_f32 v213,v213
v_mfma_f32_16x16x32_bf16 v[228:231],a[140:143],v[248:251],v[228:231]
ds_read_b128 a[132:135],v1 offset:256
v_cvt_pk_bf16_f32 v199,v206,v207
v_mfma_f32_16x16x32_bf16 v[236:239],a[156:159],v[248:251],v[44:47]
v_exp_f32 v214,v214
v_mfma_f32_16x16x32_bf16 v[236:239],a[152:155],v[216:219],v[236:239]
buffer_load_dwordx4 v11,s[72:75],s80 offen lds
v_mfma_f32_16x16x32_bf16 v[236:239],a[148:151],v[184:187],v[236:239]
ds_read_b128 a[136:139],v1 offset:512
v_cvt_pk_bf16_f32 v212,v212,v213
v_mfma_f32_16x16x32_bf16 v[236:239],a[144:147],v[152:155],v[236:239]
v_exp_f32 v215,v215
v_mfma_f32_16x16x32_bf16 v[244:247],a[160:163],v[152:155],v[44:47]
ds_read_b128 a[140:143],v1 offset:768
v_cvt_pk_bf16_f32 v213,v214,v215
v_mfma_f32_16x16x32_bf16 v[244:247],a[164:167],v[184:187],v[244:247]
v_exp_f32 v220,v220
v_mfma_f32_16x16x32_bf16 v[244:247],a[168:171],v[216:219],v[244:247]
ds_read_b128 a[156:159],v1 offset:4864
v_add_u32 v3,s48,v3
v_mfma_f32_16x16x32_bf16 v[244:247],a[172:175],v[248:251],v[244:247]
s_mov_b32 s48,98304
v_exp_f32 v221,v221
v_mfma_f32_16x16x32_bf16 v[252:255],a[188:191],v[248:251],v[44:47]
ds_read_b128 a[152:155],v1 offset:4608
v_cvt_pk_bf16_f32 v214,v220,v221
v_mfma_f32_16x16x32_bf16 v[252:255],a[184:187],v[216:219],v[252:255]
v_exp_f32 v222,v222
v_mfma_f32_16x16x32_bf16 v[252:255],a[180:183],v[184:187],v[252:255]
ds_read_b128 a[148:151],v1 offset:4352
v_mfma_f32_16x16x32_bf16 v[252:255],a[176:179],v[152:155],v[252:255]
buffer_store_dwordx4 v[84:87],v6,s[36:39],s85 offen offset:1024 sc1
v_mfma_f32_16x16x32_bf16 a[32:35],a[192:195],v[164:167],a[32:35]
ds_read_b128 a[144:147],v1 offset:4096
v_mfma_f32_16x16x32_bf16 a[32:35],a[196:199],v[180:183],a[32:35]
v_exp_f32 v223,v223
v_mfma_f32_16x16x32_bf16 a[36:39],a[204:207],v[180:183],a[36:39]
ds_read_b128 a[160:163],v1 offset:8192
v_cvt_pk_bf16_f32 v215,v222,v223
v_mfma_f32_16x16x32_bf16 a[36:39],a[200:203],v[164:167],a[36:39]
v_exp_f32 v228,v228
v_mfma_f32_16x16x32_bf16 a[40:43],a[208:211],v[164:167],a[40:43]
ds_read_b128 a[164:167],v1 offset:8448
v_mfma_f32_16x16x32_bf16 a[40:43],a[212:215],v[180:183],a[40:43]
v_exp_f32 v229,v229
v_mfma_f32_16x16x32_bf16 a[44:47],a[220:223],v[180:183],a[44:47]
s_add_u32 m0,s82,87040
ds_read_b128 a[168:171],v1 offset:8704
v_cvt_pk_bf16_f32 v228,v228,v229
v_mfma_f32_16x16x32_bf16 a[44:47],a[216:219],v[164:167],a[44:47]
v_exp_f32 v230,v230
v_mfma_f32_16x16x32_bf16 a[48:51],a[224:227],v[164:167],a[48:51]
ds_read_b128 a[172:175],v1 offset:8960
v_mfma_f32_16x16x32_bf16 a[48:51],a[228:231],v[180:183],a[48:51]
buffer_load_dwordx4 v7,s[72:75],s83 offen lds
v_mfma_f32_16x16x32_bf16 a[52:55],a[236:239],v[180:183],a[52:55]
ds_read_b128 a[188:191],v1 offset:13056
v_mfma_f32_16x16x32_bf16 a[52:55],a[232:235],v[164:167],a[52:55]
v_exp_f32 v231,v231
v_mfma_f32_16x16x32_bf16 a[56:59],a[240:243],v[164:167],a[56:59]
ds_read_b128 a[184:187],v1 offset:12800
v_cvt_pk_bf16_f32 v229,v230,v231
v_mfma_f32_16x16x32_bf16 a[56:59],a[244:247],v[180:183],a[56:59]
v_exp_f32 v236,v236
v_mfma_f32_16x16x32_bf16 a[60:63],a[252:255],v[180:183],a[60:63]
ds_read_b128 a[180:183],v1 offset:12544
v_mfma_f32_16x16x32_bf16 a[60:63],a[248:251],v[164:167],a[60:63]
v_exp_f32 v237,v237
v_mfma_f32_16x16x32_bf16 v[52:55],v[28:31],v[164:167],v[52:55]
ds_read_b128 a[176:179],v1 offset:12288
v_cvt_pk_bf16_f32 v230,v236,v237
v_mfma_f32_16x16x32_bf16 v[52:55],v[28:31],v[180:183],v[52:55]
s_waitcnt lgkmcnt(4)
v_exp_f32 v238,v238
v_mfma_f32_16x16x32_bf16 v[132:135],a[128:131],v[128:131],v[32:35]
v_exp_f32 v239,v239
v_mfma_f32_16x16x32_bf16 v[132:135],a[132:135],v[160:163],v[132:135]
v_exp_f32 v244,v244
v_mfma_f32_16x16x32_bf16 v[132:135],a[136:139],v[192:195],v[132:135]
v_exp_f32 v245,v245
v_mfma_f32_16x16x32_bf16 v[132:135],a[140:143],v[224:227],v[132:135]
buffer_store_dwordx4 v[88:91],v6,s[36:39],s85 offen offset:2048 sc1
v_mfma_f32_16x16x32_bf16 v[140:143],a[156:159],v[224:227],v[32:35]
v_exp_f32 v246,v246
v_mfma_f32_16x16x32_bf16 v[140:143],a[152:155],v[192:195],v[140:143]
v_exp_f32 v247,v247
v_mfma_f32_16x16x32_bf16 v[140:143],a[148:151],v[160:163],v[140:143]
v_cvt_pk_bf16_f32 v231,v238,v239
v_cvt_pk_bf16_f32 v244,v244,v245
v_mfma_f32_16x16x32_bf16 v[140:143],a[144:147],v[128:131],v[140:143]
v_exp_f32 v252,v252
v_mfma_f32_16x16x32_bf16 v[148:151],a[160:163],v[128:131],v[32:35]
v_exp_f32 v253,v253
v_mfma_f32_16x16x32_bf16 v[148:151],a[164:167],v[160:163],v[148:151]
v_exp_f32 v254,v254
v_mfma_f32_16x16x32_bf16 v[148:151],a[168:171],v[192:195],v[148:151]
v_exp_f32 v255,v255
v_mfma_f32_16x16x32_bf16 v[148:151],a[172:175],v[224:227],v[148:151]
s_waitcnt lgkmcnt(0)
buffer_load_dwordx4 v8,s[72:75],s83 offen offset:1024 lds
v_mfma_f32_16x16x32_bf16 v[156:159],a[188:191],v[224:227],v[32:35]
v_cvt_pk_bf16_f32 v245,v246,v247
v_cvt_pk_bf16_f32 v246,v252,v253
v_mfma_f32_16x16x32_bf16 v[156:159],a[184:187],v[192:195],v[156:159]
v_cvt_pk_bf16_f32 v247,v254,v255
v_add_u32 v1,s48,v1
v_mfma_f32_16x16x32_bf16 v[156:159],a[180:183],v[160:163],v[156:159]
v_exp_f32 v132,v132
v_mfma_f32_16x16x32_bf16 v[156:159],a[176:179],v[128:131],v[156:159]
v_exp_f32 v133,v133
v_mfma_f32_16x16x32_bf16 a[64:67],a[192:195],v[196:199],a[64:67]
v_exp_f32 v134,v134
v_mfma_f32_16x16x32_bf16 a[64:67],a[196:199],v[212:215],a[64:67]
v_exp_f32 v135,v135
v_mfma_f32_16x16x32_bf16 a[68:71],a[204:207],v[212:215],a[68:71]
v_cvt_pk_bf16_f32 v132,v132,v133
v_cvt_pk_bf16_f32 v133,v134,v135
v_mfma_f32_16x16x32_bf16 a[68:71],a[200:203],v[196:199],a[68:71]
v_exp_f32 v140,v140
v_mfma_f32_16x16x32_bf16 a[72:75],a[208:211],v[196:199],a[72:75]
v_exp_f32 v141,v141
v_mfma_f32_16x16x32_bf16 a[72:75],a[212:215],v[212:215],a[72:75]
v_exp_f32 v142,v142
v_mfma_f32_16x16x32_bf16 a[76:79],a[220:223],v[212:215],a[76:79]
v_exp_f32 v143,v143
v_mfma_f32_16x16x32_bf16 a[76:79],a[216:219],v[196:199],a[76:79]
v_exp_f32 v148,v148
v_mfma_f32_16x16x32_bf16 a[80:83],a[224:227],v[196:199],a[80:83]
v_exp_f32 v149,v149
v_mfma_f32_16x16x32_bf16 a[80:83],a[228:231],v[212:215],a[80:83]
buffer_store_dwordx4 v[92:95],v6,s[36:39],s85 offen offset:3072 sc1
v_mfma_f32_16x16x32_bf16 a[84:87],a[236:239],v[212:215],a[84:87]
s_add_u32 s85,s55,69632
v_exp_f32 v150,v150
v_mfma_f32_16x16x32_bf16 a[84:87],a[232:235],v[196:199],a[84:87]
s_max_i32 s85,s85,s19
v_exp_f32 v151,v151
v_mfma_f32_16x16x32_bf16 a[88:91],a[240:243],v[196:199],a[88:91]
v_cvt_pk_bf16_f32 v134,v140,v141
v_cvt_pk_bf16_f32 v135,v142,v143
v_mfma_f32_16x16x32_bf16 a[88:91],a[244:247],v[212:215],a[88:91]
v_cvt_pk_bf16_f32 v148,v148,v149
v_cvt_pk_bf16_f32 v149,v150,v151
v_mfma_f32_16x16x32_bf16 a[92:95],a[252:255],v[212:215],a[92:95]
v_exp_f32 v156,v156
v_mfma_f32_16x16x32_bf16 a[92:95],a[248:251],v[196:199],a[92:95]
buffer_load_dwordx4 v9,s[72:75],s83 offen offset:2048 lds
v_mfma_f32_16x16x32_bf16 v[56:59],v[28:31],v[196:199],v[56:59]
v_exp_f32 v157,v157
v_mfma_f32_16x16x32_bf16 v[56:59],v[28:31],v[212:215],v[56:59]
v_exp_f32 v158,v158
v_mfma_f32_16x16x32_bf16 v[164:167],a[128:131],v[136:139],v[36:39]
v_exp_f32 v159,v159
v_mfma_f32_16x16x32_bf16 v[164:167],a[132:135],v[168:171],v[164:167]
v_cvt_pk_bf16_f32 v150,v156,v157
v_cvt_pk_bf16_f32 v151,v158,v159
v_mfma_f32_16x16x32_bf16 v[164:167],a[136:139],v[200:203],v[164:167]
v_mfma_f32_16x16x32_bf16 v[164:167],a[140:143],v[232:235],v[164:167]
v_mfma_f32_16x16x32_bf16 v[172:175],a[156:159],v[232:235],v[36:39]
v_mfma_f32_16x16x32_bf16 v[172:175],a[152:155],v[200:203],v[172:175]
v_mfma_f32_16x16x32_bf16 v[172:175],a[148:151],v[168:171],v[172:175]
v_mfma_f32_16x16x32_bf16 v[172:175],a[144:147],v[136:139],v[172:175]
v_mfma_f32_16x16x32_bf16 v[180:183],a[160:163],v[136:139],v[36:39]
v_mfma_f32_16x16x32_bf16 v[180:183],a[164:167],v[168:171],v[180:183]
s_nop 1
v_exp_f32 v164,v164
v_mfma_f32_16x16x32_bf16 v[180:183],a[168:171],v[200:203],v[180:183]
v_exp_f32 v165,v165
v_mfma_f32_16x16x32_bf16 v[180:183],a[172:175],v[232:235],v[180:183]
v_exp_f32 v166,v166
v_mfma_f32_16x16x32_bf16 v[188:191],a[188:191],v[232:235],v[36:39]
v_exp_f32 v167,v167
v_mfma_f32_16x16x32_bf16 v[188:191],a[184:187],v[200:203],v[188:191]
buffer_store_dwordx4 v[96:99],v6,s[36:39],s86 offen sc1
v_mfma_f32_16x16x32_bf16 v[188:191],a[180:183],v[168:171],v[188:191]
v_cvt_pk_bf16_f32 v164,v164,v165
v_cvt_pk_bf16_f32 v165,v166,v167
v_mfma_f32_16x16x32_bf16 v[188:191],a[176:179],v[136:139],v[188:191]
v_exp_f32 v172,v172
v_mfma_f32_16x16x32_bf16 a[96:99],a[192:195],v[228:231],a[96:99]
ds_read_b64_tr_b16 a[192:193],v2
ds_read_b64_tr_b16 a[194:195],v2 offset:4096
v_mfma_f32_16x16x32_bf16 a[96:99],a[196:199],v[244:247],a[96:99]
buffer_load_dwordx4 v10,s[72:75],s83 offen offset:3072 lds
v_mfma_f32_16x16x32_bf16 a[100:103],a[204:207],v[244:247],a[100:103]
ds_read_b64_tr_b16 a[196:197],v2 offset:8192
ds_read_b64_tr_b16 a[198:199],v2 offset:12288
v_mfma_f32_16x16x32_bf16 a[100:103],a[200:203],v[228:231],a[100:103]
ds_read_b64_tr_b16 a[204:205],v3 offset:8192
v_mfma_f32_16x16x32_bf16 a[104:107],a[208:211],v[228:231],a[104:107]
ds_read_b64_tr_b16 a[206:207],v3 offset:12288
v_mfma_f32_16x16x32_bf16 a[104:107],a[212:215],v[244:247],a[104:107]
ds_read_b64_tr_b16 a[200:201],v3
v_mfma_f32_16x16x32_bf16 a[108:111],a[220:223],v[244:247],a[108:111]
ds_read_b64_tr_b16 a[202:203],v3 offset:4096
ds_read_b64_tr_b16 a[208:209],v2 offset:256
v_mfma_f32_16x16x32_bf16 a[108:111],a[216:219],v[228:231],a[108:111]
ds_read_b64_tr_b16 a[210:211],v2 offset:4352
v_mfma_f32_16x16x32_bf16 a[112:115],a[224:227],v[228:231],a[112:115]
ds_read_b64_tr_b16 a[212:213],v2 offset:8448
v_mfma_f32_16x16x32_bf16 a[112:115],a[228:231],v[244:247],a[112:115]
ds_read_b64_tr_b16 a[214:215],v2 offset:12544
v_mfma_f32_16x16x32_bf16 a[116:119],a[236:239],v[244:247],a[116:119]
ds_read_b64_tr_b16 a[220:221],v3 offset:8448
ds_read_b64_tr_b16 a[222:223],v3 offset:12544
v_mfma_f32_16x16x32_bf16 a[116:119],a[232:235],v[228:231],a[116:119]
ds_read_b64_tr_b16 a[216:217],v3 offset:256
v_mfma_f32_16x16x32_bf16 a[120:123],a[240:243],v[228:231],a[120:123]
ds_read_b64_tr_b16 a[218:219],v3 offset:4352
v_mfma_f32_16x16x32_bf16 a[120:123],a[244:247],v[244:247],a[120:123]
ds_read_b64_tr_b16 a[224:225],v2 offset:512
v_mfma_f32_16x16x32_bf16 a[124:127],a[252:255],v[244:247],a[124:127]
ds_read_b64_tr_b16 a[226:227],v2 offset:4608
ds_read_b64_tr_b16 a[228:229],v2 offset:8704
v_mfma_f32_16x16x32_bf16 a[124:127],a[248:251],v[228:231],a[124:127]
ds_read_b64_tr_b16 a[230:231],v2 offset:12800
v_mfma_f32_16x16x32_bf16 v[60:63],v[28:31],v[228:231],v[60:63]
ds_read_b64_tr_b16 a[236:237],v3 offset:8704
v_mfma_f32_16x16x32_bf16 v[60:63],v[28:31],v[244:247],v[60:63]
ds_read_b64_tr_b16 a[238:239],v3 offset:12800
v_mfma_f32_16x16x32_bf16 v[196:199],a[128:131],v[144:147],v[40:43]
s_add_u32 m0,s82,91136
ds_read_b64_tr_b16 a[232:233],v3 offset:512
ds_read_b64_tr_b16 a[234:235],v3 offset:4608
v_mfma_f32_16x16x32_bf16 v[196:199],a[132:135],v[176:179],v[196:199]
buffer_store_dwordx4 v[100:103],v6,s[36:39],s86 offen offset:1024 sc1
v_mfma_f32_16x16x32_bf16 v[196:199],a[136:139],v[208:211],v[196:199]
ds_read_b64_tr_b16 a[240:241],v2 offset:768
ds_read_b64_tr_b16 a[242:243],v2 offset:4864
v_mfma_f32_16x16x32_bf16 v[196:199],a[140:143],v[240:243],v[196:199]
ds_read_b64_tr_b16 a[244:245],v2 offset:8960
v_mfma_f32_16x16x32_bf16 v[204:207],a[156:159],v[240:243],v[40:43]
ds_read_b64_tr_b16 a[246:247],v2 offset:13056
v_add_u32 v2,s48,v2
v_mfma_f32_16x16x32_bf16 v[204:207],a[152:155],v[208:211],v[204:207]
buffer_load_dwordx4 v11,s[72:75],s83 offen lds
v_mfma_f32_16x16x32_bf16 v[204:207],a[148:151],v[176:179],v[204:207]
ds_read_b64_tr_b16 a[252:253],v3 offset:8960
)A"
R"A(ds_read_b64_tr_b16 a[254:255],v3 offset:13056
v_mfma_f32_16x16x32_bf16 v[204:207],a[144:147],v[144:147],v[204:207]
ds_read_b64_tr_b16 a[248:249],v3 offset:768
v_mfma_f32_16x16x32_bf16 v[212:215],a[160:163],v[144:147],v[40:43]
ds_read_b64_tr_b16 a[250:251],v3 offset:4864
v_add_u32 v3,s48,v3
v_mfma_f32_16x16x32_bf16 v[212:215],a[164:167],v[176:179],v[212:215]
v_exp_f32 v173,v173
v_mfma_f32_16x16x32_bf16 v[212:215],a[168:171],v[208:211],v[212:215]
v_exp_f32 v174,v174
v_mfma_f32_16x16x32_bf16 v[212:215],a[172:175],v[240:243],v[212:215]
v_exp_f32 v175,v175
v_mfma_f32_16x16x32_bf16 v[220:223],a[188:191],v[240:243],v[40:43]
v_exp_f32 v180,v180
v_mfma_f32_16x16x32_bf16 v[220:223],a[184:187],v[208:211],v[220:223]
v_exp_f32 v181,v181
v_mfma_f32_16x16x32_bf16 v[220:223],a[180:183],v[176:179],v[220:223]
v_exp_f32 v182,v182
v_mfma_f32_16x16x32_bf16 v[220:223],a[176:179],v[144:147],v[220:223]
s_waitcnt lgkmcnt(1)
v_exp_f32 v183,v183
v_mfma_f32_16x16x32_bf16 a[0:3],a[192:195],v[132:135],a[0:3]
v_cvt_pk_bf16_f32 v166,v172,v173
v_cvt_pk_bf16_f32 v167,v174,v175
v_mfma_f32_16x16x32_bf16 a[0:3],a[196:199],v[148:151],a[0:3]
v_cvt_pk_bf16_f32 v180,v180,v181
v_cvt_pk_bf16_f32 v181,v182,v183
v_mfma_f32_16x16x32_bf16 a[4:7],a[204:207],v[148:151],a[4:7]
s_add_u32 s80,s71,0
s_and_b32 s80,s80,3
s_lshl_b32 s80,s80,1
v_mfma_f32_16x16x32_bf16 a[4:7],a[200:203],v[132:135],a[4:7]
s_or_b32 s80,s80,1
s_mul_i32 s80,s80,163840
s_add_u32 s80,s80,s81
v_mfma_f32_16x16x32_bf16 a[8:11],a[208:211],v[132:135],a[8:11]
s_add_u32 s83,s80,20480
v_exp_f32 v188,v188
v_mfma_f32_16x16x32_bf16 a[8:11],a[212:215],v[148:151],a[8:11]
v_exp_f32 v189,v189
v_mfma_f32_16x16x32_bf16 a[12:15],a[220:223],v[148:151],a[12:15]
v_exp_f32 v190,v190
v_mfma_f32_16x16x32_bf16 a[12:15],a[216:219],v[132:135],a[12:15]
buffer_store_dwordx4 v[104:107],v6,s[36:39],s86 offen offset:2048 sc1
v_mfma_f32_16x16x32_bf16 a[16:19],a[224:227],v[132:135],a[16:19]
v_exp_f32 v191,v191
v_mfma_f32_16x16x32_bf16 a[16:19],a[228:231],v[148:151],a[16:19]
v_cvt_pk_bf16_f32 v182,v188,v189
v_cvt_pk_bf16_f32 v183,v190,v191
v_mfma_f32_16x16x32_bf16 a[20:23],a[236:239],v[148:151],a[20:23]
v_exp_f32 v196,v196
v_mfma_f32_16x16x32_bf16 a[20:23],a[232:235],v[132:135],a[20:23]
v_exp_f32 v197,v197
v_mfma_f32_16x16x32_bf16 a[24:27],a[240:243],v[132:135],a[24:27]
v_exp_f32 v198,v198
v_mfma_f32_16x16x32_bf16 a[24:27],a[244:247],v[148:151],a[24:27]
v_exp_f32 v199,v199
v_mfma_f32_16x16x32_bf16 a[28:31],a[252:255],v[148:151],a[28:31]
s_waitcnt lgkmcnt(0)
v_cvt_pk_bf16_f32 v196,v196,v197
v_cvt_pk_bf16_f32 v197,v198,v199
v_mfma_f32_16x16x32_bf16 a[28:31],a[248:251],v[132:135],a[28:31]
v_exp_f32 v204,v204
v_mfma_f32_16x16x32_bf16 v[48:51],v[28:31],v[132:135],v[48:51]
v_exp_f32 v205,v205
v_mfma_f32_16x16x32_bf16 v[48:51],v[28:31],v[148:151],v[48:51]
v_exp_f32 v206,v206
v_mfma_f32_16x16x32_bf16 v[228:231],a[128:131],v[152:155],v[44:47]
v_exp_f32 v207,v207
v_mfma_f32_16x16x32_bf16 v[228:231],a[132:135],v[184:187],v[228:231]
s_barrier
ds_read_b128 a[128:131],v1
v_cvt_pk_bf16_f32 v198,v204,v205
v_mfma_f32_16x16x32_bf16 v[228:231],a[136:139],v[216:219],v[228:231]
v_exp_f32 v212,v212
v_mfma_f32_16x16x32_bf16 v[228:231],a[140:143],v[248:251],v[228:231]
ds_read_b128 a[132:135],v1 offset:256
v_cvt_pk_bf16_f32 v199,v206,v207
v_mfma_f32_16x16x32_bf16 v[236:239],a[156:159],v[248:251],v[44:47]
s_add_u32 m0,s82,0
v_exp_f32 v213,v213
v_mfma_f32_16x16x32_bf16 v[236:239],a[152:155],v[216:219],v[236:239]
ds_read_b128 a[136:139],v1 offset:512
v_cvt_pk_bf16_f32 v212,v212,v213
v_mfma_f32_16x16x32_bf16 v[236:239],a[148:151],v[184:187],v[236:239]
v_exp_f32 v214,v214
v_mfma_f32_16x16x32_bf16 v[236:239],a[144:147],v[152:155],v[236:239]
buffer_load_dwordx4 v7,s[72:75],s80 offen lds
v_mfma_f32_16x16x32_bf16 v[244:247],a[160:163],v[152:155],v[44:47]
ds_read_b128 a[140:143],v1 offset:768
v_mfma_f32_16x16x32_bf16 v[244:247],a[164:167],v[184:187],v[244:247]
v_exp_f32 v215,v215
v_mfma_f32_16x16x32_bf16 v[244:247],a[168:171],v[216:219],v[244:247]
ds_read_b128 a[156:159],v1 offset:4864
v_cvt_pk_bf16_f32 v213,v214,v215
v_mfma_f32_16x16x32_bf16 v[244:247],a[172:175],v[248:251],v[244:247]
buffer_store_dwordx4 v[108:111],v6,s[36:39],s86 offen offset:3072 sc1
v_mfma_f32_16x16x32_bf16 v[252:255],a[188:191],v[248:251],v[44:47]
s_add_u32 s86,s55,73728
s_max_i32 s86,s86,s100
ds_read_b128 a[152:155],v1 offset:4608
v_mfma_f32_16x16x32_bf16 v[252:255],a[184:187],v[216:219],v[252:255]
v_exp_f32 v220,v220
v_mfma_f32_16x16x32_bf16 v[252:255],a[180:183],v[184:187],v[252:255]
ds_read_b128 a[148:151],v1 offset:4352
v_mfma_f32_16x16x32_bf16 v[252:255],a[176:179],v[152:155],v[252:255]
v_exp_f32 v221,v221
v_mfma_f32_16x16x32_bf16 a[32:35],a[192:195],v[164:167],a[32:35]
ds_read_b128 a[144:147],v1 offset:4096
v_cvt_pk_bf16_f32 v214,v220,v221
v_mfma_f32_16x16x32_bf16 a[32:35],a[196:199],v[180:183],a[32:35]
v_exp_f32 v222,v222
v_mfma_f32_16x16x32_bf16 a[36:39],a[204:207],v[180:183],a[36:39]
buffer_load_dwordx4 v8,s[72:75],s80 offen offset:1024 lds
v_mfma_f32_16x16x32_bf16 a[36:39],a[200:203],v[164:167],a[36:39]
ds_read_b128 a[160:163],v1 offset:8192
v_mfma_f32_16x16x32_bf16 a[40:43],a[208:211],v[164:167],a[40:43]
v_exp_f32 v223,v223
v_mfma_f32_16x16x32_bf16 a[40:43],a[212:215],v[180:183],a[40:43]
ds_read_b128 a[164:167],v1 offset:8448
v_cvt_pk_bf16_f32 v215,v222,v223
v_mfma_f32_16x16x32_bf16 a[44:47],a[220:223],v[180:183],a[44:47]
v_exp_f32 v228,v228
v_mfma_f32_16x16x32_bf16 a[44:47],a[216:219],v[164:167],a[44:47]
ds_read_b128 a[168:171],v1 offset:8704
v_mfma_f32_16x16x32_bf16 a[48:51],a[224:227],v[164:167],a[48:51]
v_exp_f32 v229,v229
v_mfma_f32_16x16x32_bf16 a[48:51],a[228:231],v[180:183],a[48:51]
ds_read_b128 a[172:175],v1 offset:8960
v_cvt_pk_bf16_f32 v228,v228,v229
v_mfma_f32_16x16x32_bf16 a[52:55],a[236:239],v[180:183],a[52:55]
v_exp_f32 v230,v230
v_mfma_f32_16x16x32_bf16 a[52:55],a[232:235],v[164:167],a[52:55]
ds_read_b128 a[188:191],v1 offset:13056
v_mfma_f32_16x16x32_bf16 a[56:59],a[240:243],v[164:167],a[56:59]
v_exp_f32 v231,v231
v_mfma_f32_16x16x32_bf16 a[56:59],a[244:247],v[180:183],a[56:59]
buffer_load_dwordx4 v9,s[72:75],s80 offen offset:2048 lds
v_mfma_f32_16x16x32_bf16 a[60:63],a[252:255],v[180:183],a[60:63]
ds_read_b128 a[184:187],v1 offset:12800
v_cvt_pk_bf16_f32 v229,v230,v231
v_mfma_f32_16x16x32_bf16 a[60:63],a[248:251],v[164:167],a[60:63]
v_exp_f32 v236,v236
v_mfma_f32_16x16x32_bf16 v[52:55],v[28:31],v[164:167],v[52:55]
ds_read_b128 a[180:183],v1 offset:12544
v_mfma_f32_16x16x32_bf16 v[52:55],v[28:31],v[180:183],v[52:55]
s_waitcnt lgkmcnt(3)
buffer_store_dwordx4 v[112:115],v6,s[36:39],s87 offen sc1
v_mfma_f32_16x16x32_bf16 v[132:135],a[128:131],v[128:131],v[32:35]
ds_read_b128 a[176:179],v1 offset:12288
v_add_u32 v1,v4,v5
v_mfma_f32_16x16x32_bf16 v[132:135],a[132:135],v[160:163],v[132:135]
v_exp_f32 v237,v237
v_mfma_f32_16x16x32_bf16 v[132:135],a[136:139],v[192:195],v[132:135]
v_exp_f32 v238,v238
v_mfma_f32_16x16x32_bf16 v[132:135],a[140:143],v[224:227],v[132:135]
v_exp_f32 v239,v239
v_mfma_f32_16x16x32_bf16 v[140:143],a[156:159],v[224:227],v[32:35]
v_exp_f32 v244,v244
v_mfma_f32_16x16x32_bf16 v[140:143],a[152:155],v[192:195],v[140:143]
v_exp_f32 v245,v245
v_mfma_f32_16x16x32_bf16 v[140:143],a[148:151],v[160:163],v[140:143]
buffer_load_dwordx4 v10,s[72:75],s80 offen offset:3072 lds
v_mfma_f32_16x16x32_bf16 v[140:143],a[144:147],v[128:131],v[140:143]
v_exp_f32 v246,v246
v_mfma_f32_16x16x32_bf16 v[148:151],a[160:163],v[128:131],v[32:35]
v_exp_f32 v247,v247
v_mfma_f32_16x16x32_bf16 v[148:151],a[164:167],v[160:163],v[148:151]
v_cvt_pk_bf16_f32 v230,v236,v237
v_cvt_pk_bf16_f32 v231,v238,v239
v_mfma_f32_16x16x32_bf16 v[148:151],a[168:171],v[192:195],v[148:151]
v_cvt_pk_bf16_f32 v244,v244,v245
v_cvt_pk_bf16_f32 v245,v246,v247
v_mfma_f32_16x16x32_bf16 v[148:151],a[172:175],v[224:227],v[148:151]
s_waitcnt lgkmcnt(0)
v_exp_f32 v252,v252
v_mfma_f32_16x16x32_bf16 v[156:159],a[188:191],v[224:227],v[32:35]
v_exp_f32 v253,v253
v_mfma_f32_16x16x32_bf16 v[156:159],a[184:187],v[192:195],v[156:159]
v_exp_f32 v254,v254
v_mfma_f32_16x16x32_bf16 v[156:159],a[180:183],v[160:163],v[156:159]
s_add_u32 m0,s82,4096
v_exp_f32 v255,v255
v_mfma_f32_16x16x32_bf16 v[156:159],a[176:179],v[128:131],v[156:159]
v_cvt_pk_bf16_f32 v246,v252,v253
v_cvt_pk_bf16_f32 v247,v254,v255
v_mfma_f32_16x16x32_bf16 a[64:67],a[192:195],v[196:199],a[64:67]
v_exp_f32 v132,v132
v_mfma_f32_16x16x32_bf16 a[64:67],a[196:199],v[212:215],a[64:67]
buffer_load_dwordx4 v11,s[72:75],s80 offen lds
v_mfma_f32_16x16x32_bf16 a[68:71],a[204:207],v[212:215],a[68:71]
v_exp_f32 v133,v133
v_mfma_f32_16x16x32_bf16 a[68:71],a[200:203],v[196:199],a[68:71]
v_exp_f32 v134,v134
v_mfma_f32_16x16x32_bf16 a[72:75],a[208:211],v[196:199],a[72:75]
v_exp_f32 v135,v135
v_mfma_f32_16x16x32_bf16 a[72:75],a[212:215],v[212:215],a[72:75]
buffer_store_dwordx4 v[116:119],v6,s[36:39],s87 offen offset:1024 sc1
v_mfma_f32_16x16x32_bf16 a[76:79],a[220:223],v[212:215],a[76:79]
v_cvt_pk_bf16_f32 v132,v132,v133
v_cvt_pk_bf16_f32 v133,v134,v135
v_mfma_f32_16x16x32_bf16 a[76:79],a[216:219],v[196:199],a[76:79]
v_exp_f32 v140,v140
v_mfma_f32_16x16x32_bf16 a[80:83],a[224:227],v[196:199],a[80:83]
v_exp_f32 v141,v141
v_mfma_f32_16x16x32_bf16 a[80:83],a[228:231],v[212:215],a[80:83]
s_add_u32 m0,s82,5120
v_exp_f32 v142,v142
v_mfma_f32_16x16x32_bf16 a[84:87],a[236:239],v[212:215],a[84:87]
v_exp_f32 v143,v143
v_mfma_f32_16x16x32_bf16 a[84:87],a[232:235],v[196:199],a[84:87]
v_exp_f32 v148,v148
v_mfma_f32_16x16x32_bf16 a[88:91],a[240:243],v[196:199],a[88:91]
buffer_load_dwordx4 v7,s[72:75],s83 offen lds
v_mfma_f32_16x16x32_bf16 a[88:91],a[244:247],v[212:215],a[88:91]
v_exp_f32 v149,v149
v_mfma_f32_16x16x32_bf16 a[92:95],a[252:255],v[212:215],a[92:95]
v_exp_f32 v150,v150
v_mfma_f32_16x16x32_bf16 a[92:95],a[248:251],v[196:199],a[92:95]
v_exp_f32 v151,v151
v_mfma_f32_16x16x32_bf16 v[56:59],v[28:31],v[196:199],v[56:59]
v_cvt_pk_bf16_f32 v134,v140,v141
v_cvt_pk_bf16_f32 v135,v142,v143
v_mfma_f32_16x16x32_bf16 v[56:59],v[28:31],v[212:215],v[56:59]
v_cvt_pk_bf16_f32 v148,v148,v149
v_cvt_pk_bf16_f32 v149,v150,v151
v_mfma_f32_16x16x32_bf16 v[164:167],a[128:131],v[136:139],v[36:39]
v_exp_f32 v156,v156
v_mfma_f32_16x16x32_bf16 v[164:167],a[132:135],v[168:171],v[164:167]
v_exp_f32 v157,v157
v_mfma_f32_16x16x32_bf16 v[164:167],a[136:139],v[200:203],v[164:167]
v_exp_f32 v158,v158
v_mfma_f32_16x16x32_bf16 v[164:167],a[140:143],v[232:235],v[164:167]
v_exp_f32 v159,v159
v_mfma_f32_16x16x32_bf16 v[172:175],a[156:159],v[232:235],v[36:39]
v_cvt_pk_bf16_f32 v150,v156,v157
v_cvt_pk_bf16_f32 v151,v158,v159
v_mfma_f32_16x16x32_bf16 v[172:175],a[152:155],v[200:203],v[172:175]
buffer_load_dwordx4 v8,s[72:75],s83 offen offset:1024 lds
v_mfma_f32_16x16x32_bf16 v[172:175],a[148:151],v[168:171],v[172:175]
v_add_u32 v1,81920,v1
v_mfma_f32_16x16x32_bf16 v[172:175],a[144:147],v[136:139],v[172:175]
v_mfma_f32_16x16x32_bf16 v[180:183],a[160:163],v[136:139],v[36:39]
v_mfma_f32_16x16x32_bf16 v[180:183],a[164:167],v[168:171],v[180:183]
buffer_store_dwordx4 v[120:123],v6,s[36:39],s87 offen offset:2048 sc1
v_mfma_f32_16x16x32_bf16 v[180:183],a[168:171],v[200:203],v[180:183]
v_exp_f32 v164,v164
v_mfma_f32_16x16x32_bf16 v[180:183],a[172:175],v[232:235],v[180:183]
v_exp_f32 v165,v165
v_mfma_f32_16x16x32_bf16 v[188:191],a[188:191],v[232:235],v[36:39]
v_exp_f32 v166,v166
v_mfma_f32_16x16x32_bf16 v[188:191],a[184:187],v[200:203],v[188:191]
v_exp_f32 v167,v167
v_mfma_f32_16x16x32_bf16 v[188:191],a[180:183],v[168:171],v[188:191]
v_cvt_pk_bf16_f32 v164,v164,v165
v_cvt_pk_bf16_f32 v165,v166,v167
v_mfma_f32_16x16x32_bf16 v[188:191],a[176:179],v[136:139],v[188:191]
v_exp_f32 v172,v172
v_mfma_f32_16x16x32_bf16 a[96:99],a[192:195],v[228:231],a[96:99]
buffer_load_dwordx4 v9,s[72:75],s83 offen offset:2048 lds
v_mfma_f32_16x16x32_bf16 a[96:99],a[196:199],v[244:247],a[96:99]
ds_read_b64_tr_b16 a[192:193],v2
ds_read_b64_tr_b16 a[194:195],v2 offset:4096
v_mfma_f32_16x16x32_bf16 a[100:103],a[204:207],v[244:247],a[100:103]
ds_read_b64_tr_b16 a[196:197],v2 offset:8192
v_mfma_f32_16x16x32_bf16 a[100:103],a[200:203],v[228:231],a[100:103]
)A"
R"A(ds_read_b64_tr_b16 a[198:199],v2 offset:12288
v_mfma_f32_16x16x32_bf16 a[104:107],a[208:211],v[228:231],a[104:107]
ds_read_b64_tr_b16 a[204:205],v3 offset:8192
v_mfma_f32_16x16x32_bf16 a[104:107],a[212:215],v[244:247],a[104:107]
ds_read_b64_tr_b16 a[206:207],v3 offset:12288
ds_read_b64_tr_b16 a[200:201],v3
v_mfma_f32_16x16x32_bf16 a[108:111],a[220:223],v[244:247],a[108:111]
ds_read_b64_tr_b16 a[202:203],v3 offset:4096
v_mfma_f32_16x16x32_bf16 a[108:111],a[216:219],v[228:231],a[108:111]
ds_read_b64_tr_b16 a[208:209],v2 offset:256
v_mfma_f32_16x16x32_bf16 a[112:115],a[224:227],v[228:231],a[112:115]
ds_read_b64_tr_b16 a[210:211],v2 offset:4352
v_mfma_f32_16x16x32_bf16 a[112:115],a[228:231],v[244:247],a[112:115]
ds_read_b64_tr_b16 a[212:213],v2 offset:8448
ds_read_b64_tr_b16 a[214:215],v2 offset:12544
v_mfma_f32_16x16x32_bf16 a[116:119],a[236:239],v[244:247],a[116:119]
s_cmp_ge_u32 0,s71
s_cselect_b32 s80,2,0
ds_read_b64_tr_b16 a[220:221],v3 offset:8448
v_mfma_f32_16x16x32_bf16 a[116:119],a[232:235],v[228:231],a[116:119]
s_mul_i32 s80,s80,163840
buffer_load_dwordx4 v10,s[72:75],s83 offen offset:3072 lds
v_mfma_f32_16x16x32_bf16 a[120:123],a[240:243],v[228:231],a[120:123]
s_add_u32 s80,s80,s81
ds_read_b64_tr_b16 a[222:223],v3 offset:12544
ds_read_b64_tr_b16 a[216:217],v3 offset:256
v_mfma_f32_16x16x32_bf16 a[120:123],a[244:247],v[244:247],a[120:123]
ds_read_b64_tr_b16 a[218:219],v3 offset:4352
v_mfma_f32_16x16x32_bf16 a[124:127],a[252:255],v[244:247],a[124:127]
ds_read_b64_tr_b16 a[224:225],v2 offset:512
v_mfma_f32_16x16x32_bf16 a[124:127],a[248:251],v[228:231],a[124:127]
buffer_store_dwordx4 v[124:127],v6,s[36:39],s87 offen offset:3072 sc1
v_mfma_f32_16x16x32_bf16 v[60:63],v[28:31],v[228:231],v[60:63]
s_add_u32 s87,s55,77824
ds_read_b64_tr_b16 a[226:227],v2 offset:4608
ds_read_b64_tr_b16 a[228:229],v2 offset:8704
v_mfma_f32_16x16x32_bf16 v[60:63],v[28:31],v[244:247],v[60:63]
s_max_i32 s87,s87,s101
ds_read_b64_tr_b16 a[230:231],v2 offset:12800
v_mfma_f32_16x16x32_bf16 v[196:199],a[128:131],v[144:147],v[40:43]
ds_read_b64_tr_b16 a[236:237],v3 offset:8704
v_mfma_f32_16x16x32_bf16 v[196:199],a[132:135],v[176:179],v[196:199]
s_add_u32 m0,s82,9216
ds_read_b64_tr_b16 a[238:239],v3 offset:12800
v_mfma_f32_16x16x32_bf16 v[196:199],a[136:139],v[208:211],v[196:199]
ds_read_b64_tr_b16 a[232:233],v3 offset:512
ds_read_b64_tr_b16 a[234:235],v3 offset:4608
v_mfma_f32_16x16x32_bf16 v[196:199],a[140:143],v[240:243],v[196:199]
ds_read_b64_tr_b16 a[240:241],v2 offset:768
v_mfma_f32_16x16x32_bf16 v[204:207],a[156:159],v[240:243],v[40:43]
buffer_load_dwordx4 v11,s[72:75],s83 offen lds
v_mfma_f32_16x16x32_bf16 v[204:207],a[152:155],v[208:211],v[204:207]
s_add_u32 s83,s80,20480
ds_read_b64_tr_b16 a[242:243],v2 offset:4864
ds_read_b64_tr_b16 a[244:245],v2 offset:8960
v_mfma_f32_16x16x32_bf16 v[204:207],a[148:151],v[176:179],v[204:207]
ds_read_b64_tr_b16 a[246:247],v2 offset:13056
v_sub_u32 v2,v4,v5
v_mfma_f32_16x16x32_bf16 v[204:207],a[144:147],v[144:147],v[204:207]
ds_read_b64_tr_b16 a[252:253],v3 offset:8960
v_add_u32 v2,81920,v2
v_mfma_f32_16x16x32_bf16 v[212:215],a[160:163],v[144:147],v[40:43]
ds_read_b64_tr_b16 a[254:255],v3 offset:13056
v_mfma_f32_16x16x32_bf16 v[212:215],a[164:167],v[176:179],v[212:215]
ds_read_b64_tr_b16 a[248:249],v3 offset:768
ds_read_b64_tr_b16 a[250:251],v3 offset:4864
v_mfma_f32_16x16x32_bf16 v[212:215],a[168:171],v[208:211],v[212:215]
v_exp_f32 v173,v173
v_mfma_f32_16x16x32_bf16 v[212:215],a[172:175],v[240:243],v[212:215]
v_exp_f32 v174,v174
v_mfma_f32_16x16x32_bf16 v[220:223],a[188:191],v[240:243],v[40:43]
s_add_u32 m0,s82,40960
v_exp_f32 v175,v175
v_mfma_f32_16x16x32_bf16 v[220:223],a[184:187],v[208:211],v[220:223]
v_exp_f32 v180,v180
v_mfma_f32_16x16x32_bf16 v[220:223],a[180:183],v[176:179],v[220:223]
v_exp_f32 v181,v181
v_mfma_f32_16x16x32_bf16 v[220:223],a[176:179],v[144:147],v[220:223]
s_waitcnt lgkmcnt(3)
buffer_load_dwordx4 v7,s[72:75],s80 offen lds
v_mfma_f32_16x16x32_bf16 a[0:3],a[192:195],v[132:135],a[0:3]
v_exp_f32 v182,v182
v_mfma_f32_16x16x32_bf16 a[0:3],a[196:199],v[148:151],a[0:3]
v_exp_f32 v183,v183
v_mfma_f32_16x16x32_bf16 a[4:7],a[204:207],v[148:151],a[4:7]
v_cvt_pk_bf16_f32 v166,v172,v173
v_cvt_pk_bf16_f32 v167,v174,v175
v_mfma_f32_16x16x32_bf16 a[4:7],a[200:203],v[132:135],a[4:7]
v_cvt_pk_bf16_f32 v180,v180,v181
v_cvt_pk_bf16_f32 v181,v182,v183
v_mfma_f32_16x16x32_bf16 a[8:11],a[208:211],v[132:135],a[8:11]
v_exp_f32 v188,v188
v_mfma_f32_16x16x32_bf16 a[8:11],a[212:215],v[148:151],a[8:11]
v_exp_f32 v189,v189
v_mfma_f32_16x16x32_bf16 a[12:15],a[220:223],v[148:151],a[12:15]
v_exp_f32 v190,v190
v_mfma_f32_16x16x32_bf16 a[12:15],a[216:219],v[132:135],a[12:15]
v_exp_f32 v191,v191
v_mfma_f32_16x16x32_bf16 a[16:19],a[224:227],v[132:135],a[16:19]
v_cvt_pk_bf16_f32 v182,v188,v189
v_cvt_pk_bf16_f32 v183,v190,v191
v_mfma_f32_16x16x32_bf16 a[16:19],a[228:231],v[148:151],a[16:19]
v_exp_f32 v196,v196
v_mfma_f32_16x16x32_bf16 a[20:23],a[236:239],v[148:151],a[20:23]
buffer_load_dwordx4 v8,s[72:75],s80 offen offset:1024 lds
v_mfma_f32_16x16x32_bf16 a[20:23],a[232:235],v[132:135],a[20:23]
v_exp_f32 v197,v197
v_mfma_f32_16x16x32_bf16 a[24:27],a[240:243],v[132:135],a[24:27]
v_exp_f32 v198,v198
v_mfma_f32_16x16x32_bf16 a[24:27],a[244:247],v[148:151],a[24:27]
s_waitcnt lgkmcnt(0)
v_exp_f32 v199,v199
v_mfma_f32_16x16x32_bf16 a[28:31],a[252:255],v[148:151],a[28:31]
v_cvt_pk_bf16_f32 v196,v196,v197
v_cvt_pk_bf16_f32 v197,v198,v199
v_mfma_f32_16x16x32_bf16 a[28:31],a[248:251],v[132:135],a[28:31]
v_exp_f32 v204,v204
v_mfma_f32_16x16x32_bf16 v[48:51],v[28:31],v[132:135],v[48:51]
s_waitcnt vmcnt(18)
v_exp_f32 v205,v205
v_mfma_f32_16x16x32_bf16 v[48:51],v[28:31],v[148:151],v[48:51]
s_barrier
v_exp_f32 v206,v206
v_mfma_f32_16x16x32_bf16 v[228:231],a[128:131],v[152:155],v[44:47]
v_exp_f32 v207,v207
v_mfma_f32_16x16x32_bf16 v[228:231],a[132:135],v[184:187],v[228:231]
v_exp_f32 v212,v212
v_mfma_f32_16x16x32_bf16 v[228:231],a[136:139],v[216:219],v[228:231]
buffer_load_dwordx4 v9,s[72:75],s80 offen offset:2048 lds
v_mfma_f32_16x16x32_bf16 v[228:231],a[140:143],v[248:251],v[228:231]
v_exp_f32 v213,v213
v_mfma_f32_16x16x32_bf16 v[236:239],a[156:159],v[248:251],v[44:47]
v_exp_f32 v214,v214
v_mfma_f32_16x16x32_bf16 v[236:239],a[152:155],v[216:219],v[236:239]
v_exp_f32 v215,v215
v_mfma_f32_16x16x32_bf16 v[236:239],a[148:151],v[184:187],v[236:239]
v_cvt_pk_bf16_f32 v198,v204,v205
v_cvt_pk_bf16_f32 v199,v206,v207
v_mfma_f32_16x16x32_bf16 v[236:239],a[144:147],v[152:155],v[236:239]
v_cvt_pk_bf16_f32 v212,v212,v213
v_cvt_pk_bf16_f32 v213,v214,v215
v_mfma_f32_16x16x32_bf16 v[244:247],a[160:163],v[152:155],v[44:47]
v_exp_f32 v220,v220
v_mfma_f32_16x16x32_bf16 v[244:247],a[164:167],v[184:187],v[244:247]
v_exp_f32 v221,v221
v_mfma_f32_16x16x32_bf16 v[244:247],a[168:171],v[216:219],v[244:247]
v_exp_f32 v222,v222
v_mfma_f32_16x16x32_bf16 v[244:247],a[172:175],v[248:251],v[244:247]
s_lshl_b32 s95,s42,2
s_add_u32 s95,s95,s40
s_lshl_b32 s95,s95,4
v_mfma_f32_16x16x32_bf16 v[252:255],a[188:191],v[248:251],v[44:47]
v_mov_b32 v18,s68
v_mov_b32 v19,s69
v_mfma_f32_16x16x32_bf16 v[252:255],a[184:187],v[216:219],v[252:255]
buffer_load_dwordx4 v10,s[72:75],s80 offen offset:3072 lds
v_mfma_f32_16x16x32_bf16 v[252:255],a[180:183],v[184:187],v[252:255]
v_exp_f32 v223,v223
v_mfma_f32_16x16x32_bf16 v[252:255],a[176:179],v[152:155],v[252:255]
v_cvt_pk_bf16_f32 v214,v220,v221
v_cvt_pk_bf16_f32 v215,v222,v223
v_mfma_f32_16x16x32_bf16 a[32:35],a[192:195],v[164:167],a[32:35]
ds_read_b64_tr_b16 a[128:129],v1
v_max_f32 v20,0x00800000,v48
v_mfma_f32_16x16x32_bf16 a[32:35],a[196:199],v[180:183],a[32:35]
v_exp_f32 v228,v228
v_mfma_f32_16x16x32_bf16 a[36:39],a[204:207],v[180:183],a[36:39]
v_exp_f32 v229,v229
v_mfma_f32_16x16x32_bf16 a[36:39],a[200:203],v[164:167],a[36:39]
s_waitcnt vmcnt(5)
s_mov_b64 exec,1
buffer_store_dwordx2 v[18:19],v6,s[64:67],s95 offen sc1
s_mov_b64 exec,-1
v_mfma_f32_16x16x32_bf16 a[40:43],a[208:211],v[164:167],a[40:43]
v_exp_f32 v230,v230
v_mfma_f32_16x16x32_bf16 a[40:43],a[212:215],v[180:183],a[40:43]
s_add_u32 m0,s82,45056
v_exp_f32 v231,v231
v_mfma_f32_16x16x32_bf16 a[44:47],a[220:223],v[180:183],a[44:47]
v_rcp_f32 v20,v20
v_mfma_f32_16x16x32_bf16 a[44:47],a[216:219],v[164:167],a[44:47]
v_cvt_pk_bf16_f32 v228,v228,v229
v_cvt_pk_bf16_f32 v229,v230,v231
v_mfma_f32_16x16x32_bf16 a[48:51],a[224:227],v[164:167],a[48:51]
buffer_load_dwordx4 v11,s[72:75],s80 offen lds
v_mfma_f32_16x16x32_bf16 a[48:51],a[228:231],v[180:183],a[48:51]
v_exp_f32 v236,v236
v_mfma_f32_16x16x32_bf16 a[52:55],a[236:239],v[180:183],a[52:55]
v_exp_f32 v237,v237
v_mfma_f32_16x16x32_bf16 a[52:55],a[232:235],v[164:167],a[52:55]
v_exp_f32 v238,v238
v_mfma_f32_16x16x32_bf16 a[56:59],a[240:243],v[164:167],a[56:59]
v_exp_f32 v239,v239
v_mfma_f32_16x16x32_bf16 a[56:59],a[244:247],v[180:183],a[56:59]
v_exp_f32 v244,v244
v_mfma_f32_16x16x32_bf16 a[60:63],a[252:255],v[180:183],a[60:63]
v_exp_f32 v245,v245
v_mfma_f32_16x16x32_bf16 a[60:63],a[248:251],v[164:167],a[60:63]
v_exp_f32 v246,v246
v_mfma_f32_16x16x32_bf16 v[52:55],v[28:31],v[164:167],v[52:55]
s_add_u32 m0,s82,46080
v_exp_f32 v247,v247
v_mfma_f32_16x16x32_bf16 v[52:55],v[28:31],v[180:183],v[52:55]
v_accvgpr_read_b32 v132,a0
v_accvgpr_read_b32 v133,a1
v_accvgpr_read_b32 v134,a2
v_accvgpr_read_b32 v135,a3
v_accvgpr_read_b32 v12,a4
v_accvgpr_read_b32 v13,a5
v_accvgpr_read_b32 v14,a6
v_accvgpr_read_b32 v15,a7
v_pk_mul_f32 v[132:133],v[20:21],v[132:133] op_sel:[0,0] op_sel_hi:[0,1]
v_pk_mul_f32 v[134:135],v[20:21],v[134:135] op_sel:[0,0] op_sel_hi:[0,1]
v_pk_mul_f32 v[12:13],v[20:21],v[12:13] op_sel:[0,0] op_sel_hi:[0,1]
v_pk_mul_f32 v[14:15],v[20:21],v[14:15] op_sel:[0,0] op_sel_hi:[0,1]
v_cvt_pk_bf16_f32 v132,v132,v133
v_cvt_pk_bf16_f32 v133,v134,v135
v_cvt_pk_bf16_f32 v134,v12,v13
v_cvt_pk_bf16_f32 v135,v14,v15
v_mfma_f32_16x16x32_bf16 a[64:67],a[192:195],v[196:199],a[64:67]
v_cvt_pk_bf16_f32 v230,v236,v237
v_cvt_pk_bf16_f32 v231,v238,v239
v_mfma_f32_16x16x32_bf16 a[64:67],a[196:199],v[212:215],a[64:67]
buffer_load_dwordx4 v7,s[72:75],s83 offen lds
v_mfma_f32_16x16x32_bf16 a[68:71],a[204:207],v[212:215],a[68:71]
v_cvt_pk_bf16_f32 v244,v244,v245
v_cvt_pk_bf16_f32 v245,v246,v247
v_mfma_f32_16x16x32_bf16 a[68:71],a[200:203],v[196:199],a[68:71]
v_exp_f32 v252,v252
v_mfma_f32_16x16x32_bf16 a[72:75],a[208:211],v[196:199],a[72:75]
v_exp_f32 v253,v253
v_mfma_f32_16x16x32_bf16 a[72:75],a[212:215],v[212:215],a[72:75]
v_exp_f32 v254,v254
v_mfma_f32_16x16x32_bf16 a[76:79],a[220:223],v[212:215],a[76:79]
v_exp_f32 v255,v255
v_mfma_f32_16x16x32_bf16 a[76:79],a[216:219],v[196:199],a[76:79]
v_cvt_pk_bf16_f32 v246,v252,v253
v_cvt_pk_bf16_f32 v247,v254,v255
v_mfma_f32_16x16x32_bf16 a[80:83],a[224:227],v[196:199],a[80:83]
v_accvgpr_read_b32 v140,a8
v_accvgpr_read_b32 v141,a9
v_accvgpr_read_b32 v142,a10
v_accvgpr_read_b32 v143,a11
v_accvgpr_read_b32 v12,a12
v_accvgpr_read_b32 v13,a13
v_accvgpr_read_b32 v14,a14
v_accvgpr_read_b32 v15,a15
v_pk_mul_f32 v[140:141],v[20:21],v[140:141] op_sel:[0,0] op_sel_hi:[0,1]
v_pk_mul_f32 v[142:143],v[20:21],v[142:143] op_sel:[0,0] op_sel_hi:[0,1]
v_pk_mul_f32 v[12:13],v[20:21],v[12:13] op_sel:[0,0] op_sel_hi:[0,1]
v_pk_mul_f32 v[14:15],v[20:21],v[14:15] op_sel:[0,0] op_sel_hi:[0,1]
v_cvt_pk_bf16_f32 v140,v140,v141
v_cvt_pk_bf16_f32 v141,v142,v143
v_cvt_pk_bf16_f32 v142,v12,v13
v_cvt_pk_bf16_f32 v143,v14,v15
v_mfma_f32_16x16x32_bf16 a[80:83],a[228:231],v[212:215],a[80:83]
ds_read_b64_tr_b16 a[130:131],v1 offset:5120
ds_read_b64_tr_b16 a[132:133],v1 offset:10240
v_mfma_f32_16x16x32_bf16 a[84:87],a[236:239],v[212:215],a[84:87]
ds_read_b64_tr_b16 a[134:135],v1 offset:15360
v_accvgpr_read_b32 v148,a16
v_mfma_f32_16x16x32_bf16 a[84:87],a[232:235],v[196:199],a[84:87]
ds_read_b64_tr_b16 a[136:137],v1 offset:20480
v_accvgpr_read_b32 v149,a17
v_mfma_f32_16x16x32_bf16 a[88:91],a[240:243],v[196:199],a[88:91]
buffer_load_dwordx4 v8,s[72:75],s83 offen offset:1024 lds
v_mfma_f32_16x16x32_bf16 a[88:91],a[244:247],v[212:215],a[88:91]
ds_read_b64_tr_b16 a[138:139],v1 offset:25600
ds_read_b64_tr_b16 a[140:141],v1 offset:30720
)A"
R"A(v_mfma_f32_16x16x32_bf16 a[92:95],a[252:255],v[212:215],a[92:95]
ds_read_b64_tr_b16 a[142:143],v1 offset:35840
v_mul_f32 v148,v20,v148
v_mfma_f32_16x16x32_bf16 a[92:95],a[248:251],v[196:199],a[92:95]
v_mul_f32 v149,v20,v149
v_accvgpr_read_b32 v150,a18
v_mfma_f32_16x16x32_bf16 v[56:59],v[28:31],v[196:199],v[56:59]
buffer_store_dwordx4 v[132:135],v6,s[36:39],s84 offen sc1
v_mfma_f32_16x16x32_bf16 v[56:59],v[28:31],v[212:215],v[56:59]
v_accvgpr_read_b32 v151,a19
v_cvt_pk_bf16_f32 v148,v148,v149
v_mfma_f32_16x16x32_bf16 a[96:99],a[192:195],v[228:231],a[96:99]
v_mul_f32 v150,v20,v150
v_mul_f32 v151,v20,v151
v_mfma_f32_16x16x32_bf16 a[96:99],a[196:199],v[244:247],a[96:99]
v_cvt_pk_bf16_f32 v149,v150,v151
v_accvgpr_read_b32 v150,a20
v_mfma_f32_16x16x32_bf16 a[100:103],a[204:207],v[244:247],a[100:103]
v_accvgpr_read_b32 v151,a21
v_accvgpr_read_b32 v14,a22
v_mfma_f32_16x16x32_bf16 a[100:103],a[200:203],v[228:231],a[100:103]
v_accvgpr_read_b32 v15,a23
v_mul_f32 v150,v20,v150
v_mfma_f32_16x16x32_bf16 a[104:107],a[208:211],v[228:231],a[104:107]
v_mul_f32 v151,v20,v151
v_mul_f32 v14,v20,v14
v_mfma_f32_16x16x32_bf16 a[104:107],a[212:215],v[244:247],a[104:107]
buffer_load_dwordx4 v9,s[72:75],s83 offen offset:2048 lds
v_mfma_f32_16x16x32_bf16 a[108:111],a[220:223],v[244:247],a[108:111]
v_mul_f32 v15,v20,v15
v_cvt_pk_bf16_f32 v150,v150,v151
v_mfma_f32_16x16x32_bf16 a[108:111],a[216:219],v[228:231],a[108:111]
ds_read_b64_tr_b16 a[144:145],v2 offset:32
v_cvt_pk_bf16_f32 v151,v14,v15
v_mfma_f32_16x16x32_bf16 a[112:115],a[224:227],v[228:231],a[112:115]
ds_read_b64_tr_b16 a[146:147],v2 offset:5152
ds_read_b64_tr_b16 a[148:149],v2 offset:10272
v_mfma_f32_16x16x32_bf16 a[112:115],a[228:231],v[244:247],a[112:115]
ds_read_b64_tr_b16 a[150:151],v2 offset:15392
v_accvgpr_read_b32 v156,a24
v_mfma_f32_16x16x32_bf16 a[116:119],a[236:239],v[244:247],a[116:119]
ds_read_b64_tr_b16 a[152:153],v2 offset:20512
v_accvgpr_read_b32 v157,a25
v_mfma_f32_16x16x32_bf16 a[116:119],a[232:235],v[228:231],a[116:119]
ds_read_b64_tr_b16 a[154:155],v2 offset:25632
v_mul_f32 v156,v20,v156
v_mfma_f32_16x16x32_bf16 a[120:123],a[240:243],v[228:231],a[120:123]
ds_read_b64_tr_b16 a[156:157],v2 offset:30752
ds_read_b64_tr_b16 a[158:159],v2 offset:35872
v_mfma_f32_16x16x32_bf16 a[120:123],a[244:247],v[244:247],a[120:123]
v_mul_f32 v157,v20,v157
v_accvgpr_read_b32 v158,a26
v_mfma_f32_16x16x32_bf16 a[124:127],a[252:255],v[244:247],a[124:127]
v_accvgpr_read_b32 v159,a27
v_cvt_pk_bf16_f32 v156,v156,v157
v_mfma_f32_16x16x32_bf16 a[124:127],a[248:251],v[228:231],a[124:127]
v_mul_f32 v158,v20,v158
v_mul_f32 v159,v20,v159
v_mfma_f32_16x16x32_bf16 v[60:63],v[28:31],v[228:231],v[60:63]
buffer_load_dwordx4 v10,s[72:75],s83 offen offset:3072 lds
v_mfma_f32_16x16x32_bf16 v[60:63],v[28:31],v[244:247],v[60:63]
s_waitcnt lgkmcnt(4)
v_cvt_pk_bf16_f32 v157,v158,v159
v_accvgpr_read_b32 v158,a28
v_mfma_f32_16x16x32_bf16 a[192:195],a[128:131],v[64:67],0
v_accvgpr_read_b32 v159,a29
v_accvgpr_read_b32 v16,a30
v_mfma_f32_16x16x32_bf16 a[192:195],a[132:135],v[68:71],a[192:195]
v_accvgpr_read_b32 v17,a31
v_mul_f32 v158,v20,v158
v_mfma_f32_16x16x32_bf16 a[192:195],a[136:139],v[72:75],a[192:195]
buffer_store_dwordx4 v[140:143],v6,s[36:39],s84 offen offset:1024 sc1
v_mfma_f32_16x16x32_bf16 a[192:195],a[140:143],v[76:79],a[192:195]
v_mul_f32 v159,v20,v159
v_mul_f32 v16,v20,v16
v_mfma_f32_16x16x32_bf16 a[196:199],a[140:143],v[92:95],0
v_mul_f32 v17,v20,v17
v_cvt_pk_bf16_f32 v158,v158,v159
v_mfma_f32_16x16x32_bf16 a[196:199],a[136:139],v[88:91],a[196:199]
ds_read_b64_tr_b16 a[160:161],v1 offset:64
v_cvt_pk_bf16_f32 v159,v16,v17
v_mfma_f32_16x16x32_bf16 a[196:199],a[132:135],v[84:87],a[196:199]
s_add_u32 m0,s82,50176
ds_read_b64_tr_b16 a[162:163],v1 offset:5184
ds_read_b64_tr_b16 a[164:165],v1 offset:10304
v_mfma_f32_16x16x32_bf16 a[196:199],a[128:131],v[80:83],a[196:199]
ds_read_b64_tr_b16 a[166:167],v1 offset:15424
v_max_f32 v21,0x00800000,v52
v_mfma_f32_16x16x32_bf16 a[200:203],a[128:131],v[96:99],0
s_cmp_ge_u32 1,s71
s_cselect_b32 s80,4,2
ds_read_b64_tr_b16 a[168:169],v1 offset:20544
v_mfma_f32_16x16x32_bf16 a[200:203],a[132:135],v[100:103],a[200:203]
s_mul_i32 s80,s80,163840
buffer_load_dwordx4 v11,s[72:75],s83 offen lds
v_mfma_f32_16x16x32_bf16 a[200:203],a[136:139],v[104:107],a[200:203]
s_add_u32 s80,s80,s81
ds_read_b64_tr_b16 a[170:171],v1 offset:25664
ds_read_b64_tr_b16 a[172:173],v1 offset:30784
v_mfma_f32_16x16x32_bf16 a[200:203],a[140:143],v[108:111],a[200:203]
s_add_u32 s83,s80,20480
ds_read_b64_tr_b16 a[174:175],v1 offset:35904
v_accvgpr_read_b32 v180,a48
v_mfma_f32_16x16x32_bf16 a[204:207],a[140:143],v[124:127],0
v_rcp_f32 v21,v21
v_mfma_f32_16x16x32_bf16 a[204:207],a[136:139],v[120:123],a[204:207]
v_accvgpr_read_b32 v164,a32
v_accvgpr_read_b32 v165,a33
v_accvgpr_read_b32 v166,a34
v_accvgpr_read_b32 v167,a35
v_accvgpr_read_b32 v12,a36
v_accvgpr_read_b32 v13,a37
v_accvgpr_read_b32 v14,a38
v_accvgpr_read_b32 v15,a39
v_pk_mul_f32 v[164:165],v[20:21],v[164:165] op_sel:[1,0] op_sel_hi:[1,1]
v_pk_mul_f32 v[166:167],v[20:21],v[166:167] op_sel:[1,0] op_sel_hi:[1,1]
v_pk_mul_f32 v[12:13],v[20:21],v[12:13] op_sel:[1,0] op_sel_hi:[1,1]
v_pk_mul_f32 v[14:15],v[20:21],v[14:15] op_sel:[1,0] op_sel_hi:[1,1]
v_cvt_pk_bf16_f32 v164,v164,v165
v_cvt_pk_bf16_f32 v165,v166,v167
v_cvt_pk_bf16_f32 v166,v12,v13
v_cvt_pk_bf16_f32 v167,v14,v15
v_mfma_f32_16x16x32_bf16 a[204:207],a[132:135],v[116:119],a[204:207]
ds_read_b64_tr_b16 a[176:177],v2 offset:96
ds_read_b64_tr_b16 a[178:179],v2 offset:5216
v_mfma_f32_16x16x32_bf16 a[204:207],a[128:131],v[112:115],a[204:207]
v_accvgpr_read_b32 v172,a40
v_accvgpr_read_b32 v173,a41
v_accvgpr_read_b32 v174,a42
v_accvgpr_read_b32 v175,a43
v_accvgpr_read_b32 v12,a44
v_accvgpr_read_b32 v13,a45
v_accvgpr_read_b32 v14,a46
v_accvgpr_read_b32 v15,a47
v_pk_mul_f32 v[172:173],v[20:21],v[172:173] op_sel:[1,0] op_sel_hi:[1,1]
v_pk_mul_f32 v[174:175],v[20:21],v[174:175] op_sel:[1,0] op_sel_hi:[1,1]
v_pk_mul_f32 v[12:13],v[20:21],v[12:13] op_sel:[1,0] op_sel_hi:[1,1]
v_pk_mul_f32 v[14:15],v[20:21],v[14:15] op_sel:[1,0] op_sel_hi:[1,1]
v_cvt_pk_bf16_f32 v172,v172,v173
v_cvt_pk_bf16_f32 v173,v174,v175
v_cvt_pk_bf16_f32 v174,v12,v13
v_cvt_pk_bf16_f32 v175,v14,v15
v_mfma_f32_16x16x32_bf16 a[208:211],a[144:147],v[64:67],0
ds_read_b64_tr_b16 a[180:181],v2 offset:10336
ds_read_b64_tr_b16 a[182:183],v2 offset:15456
v_mfma_f32_16x16x32_bf16 a[208:211],a[148:151],v[68:71],a[208:211]
s_waitcnt lgkmcnt(7)
ds_read_b64_tr_b16 a[184:185],v2 offset:20576
v_accvgpr_read_b32 v181,a49
v_mfma_f32_16x16x32_bf16 a[208:211],a[152:155],v[72:75],a[208:211]
ds_read_b64_tr_b16 a[186:187],v2 offset:25696
v_mul_f32 v180,v21,v180
v_mfma_f32_16x16x32_bf16 a[208:211],a[156:159],v[76:79],a[208:211]
buffer_store_dwordx4 v[148:151],v6,s[36:39],s84 offen offset:2048 sc1
v_mfma_f32_16x16x32_bf16 a[212:215],a[156:159],v[92:95],0
ds_read_b64_tr_b16 a[188:189],v2 offset:30816
ds_read_b64_tr_b16 a[190:191],v2 offset:35936
v_mfma_f32_16x16x32_bf16 a[212:215],a[152:155],v[88:91],a[212:215]
v_mul_f32 v181,v21,v181
v_accvgpr_read_b32 v182,a50
v_mfma_f32_16x16x32_bf16 a[212:215],a[148:151],v[84:87],a[212:215]
v_accvgpr_read_b32 v183,a51
v_cvt_pk_bf16_f32 v180,v180,v181
v_mfma_f32_16x16x32_bf16 a[212:215],a[144:147],v[80:83],a[212:215]
v_mul_f32 v182,v21,v182
v_mul_f32 v183,v21,v183
v_mfma_f32_16x16x32_bf16 a[216:219],a[144:147],v[96:99],0
v_cvt_pk_bf16_f32 v181,v182,v183
v_accvgpr_read_b32 v182,a52
v_mfma_f32_16x16x32_bf16 a[216:219],a[148:151],v[100:103],a[216:219]
v_accvgpr_read_b32 v183,a53
v_accvgpr_read_b32 v12,a54
v_mfma_f32_16x16x32_bf16 a[216:219],a[152:155],v[104:107],a[216:219]
v_accvgpr_read_b32 v13,a55
v_mul_f32 v182,v21,v182
v_mfma_f32_16x16x32_bf16 a[216:219],a[156:159],v[108:111],a[216:219]
s_add_u32 m0,s82,122880
v_mul_f32 v183,v21,v183
v_mul_f32 v12,v21,v12
v_mfma_f32_16x16x32_bf16 a[220:223],a[156:159],v[124:127],0
v_mul_f32 v13,v21,v13
v_cvt_pk_bf16_f32 v182,v182,v183
v_mfma_f32_16x16x32_bf16 a[220:223],a[152:155],v[120:123],a[220:223]
ds_read_b64_tr_b16 a[128:129],v1 offset:128
v_cvt_pk_bf16_f32 v183,v12,v13
v_mfma_f32_16x16x32_bf16 a[220:223],a[148:151],v[116:119],a[220:223]
buffer_load_dwordx4 v7,s[72:75],s80 offen lds
v_mfma_f32_16x16x32_bf16 a[220:223],a[144:147],v[112:115],a[220:223]
ds_read_b64_tr_b16 a[130:131],v1 offset:5248
ds_read_b64_tr_b16 a[132:133],v1 offset:10368
v_mfma_f32_16x16x32_bf16 a[224:227],a[160:163],v[64:67],0
ds_read_b64_tr_b16 a[134:135],v1 offset:15488
v_accvgpr_read_b32 v188,a56
v_mfma_f32_16x16x32_bf16 a[224:227],a[164:167],v[68:71],a[224:227]
s_waitcnt lgkmcnt(4)
ds_read_b64_tr_b16 a[136:137],v1 offset:20608
v_accvgpr_read_b32 v189,a57
v_mfma_f32_16x16x32_bf16 a[224:227],a[168:171],v[72:75],a[224:227]
ds_read_b64_tr_b16 a[138:139],v1 offset:25728
v_mul_f32 v188,v21,v188
v_mfma_f32_16x16x32_bf16 a[224:227],a[172:175],v[76:79],a[224:227]
ds_read_b64_tr_b16 a[140:141],v1 offset:30848
ds_read_b64_tr_b16 a[142:143],v1 offset:35968
v_mfma_f32_16x16x32_bf16 a[228:231],a[172:175],v[92:95],0
v_mul_f32 v189,v21,v189
v_accvgpr_read_b32 v190,a58
v_mfma_f32_16x16x32_bf16 a[228:231],a[168:171],v[88:91],a[228:231]
v_accvgpr_read_b32 v191,a59
v_cvt_pk_bf16_f32 v188,v188,v189
v_mfma_f32_16x16x32_bf16 a[228:231],a[164:167],v[84:87],a[228:231]
v_mul_f32 v190,v21,v190
v_mul_f32 v191,v21,v191
v_mfma_f32_16x16x32_bf16 a[228:231],a[160:163],v[80:83],a[228:231]
buffer_store_dwordx4 v[156:159],v6,s[36:39],s84 offen offset:3072 sc1
v_mfma_f32_16x16x32_bf16 a[232:235],a[160:163],v[96:99],0
v_cvt_pk_bf16_f32 v189,v190,v191
v_accvgpr_read_b32 v190,a60
v_mfma_f32_16x16x32_bf16 a[232:235],a[164:167],v[100:103],a[232:235]
v_accvgpr_read_b32 v191,a61
v_accvgpr_read_b32 v14,a62
v_mfma_f32_16x16x32_bf16 a[232:235],a[168:171],v[104:107],a[232:235]
v_accvgpr_read_b32 v15,a63
v_mul_f32 v190,v21,v190
v_mfma_f32_16x16x32_bf16 a[232:235],a[172:175],v[108:111],a[232:235]
buffer_load_dwordx4 v8,s[72:75],s80 offen offset:1024 lds
v_mfma_f32_16x16x32_bf16 a[236:239],a[172:175],v[124:127],0
v_mul_f32 v191,v21,v191
v_mul_f32 v14,v21,v14
v_mfma_f32_16x16x32_bf16 a[236:239],a[168:171],v[120:123],a[236:239]
v_mul_f32 v15,v21,v15
v_cvt_pk_bf16_f32 v190,v190,v191
v_mfma_f32_16x16x32_bf16 a[236:239],a[164:167],v[116:119],a[236:239]
v_cvt_pk_bf16_f32 v191,v14,v15
v_max_f32 v22,0x00800000,v56
v_mfma_f32_16x16x32_bf16 a[236:239],a[160:163],v[112:115],a[236:239]
v_rcp_f32 v22,v22
v_mfma_f32_16x16x32_bf16 a[240:243],a[176:179],v[64:67],0
ds_read_b64_tr_b16 a[144:145],v2 offset:160
ds_read_b64_tr_b16 a[146:147],v2 offset:5280
v_mfma_f32_16x16x32_bf16 a[240:243],a[180:183],v[68:71],a[240:243]
v_accvgpr_read_b32 v196,a64
v_accvgpr_read_b32 v197,a65
v_mfma_f32_16x16x32_bf16 a[240:243],a[184:187],v[72:75],a[240:243]
v_mul_f32 v196,v22,v196
v_mul_f32 v197,v22,v197
v_mfma_f32_16x16x32_bf16 a[240:243],a[188:191],v[76:79],a[240:243]
v_accvgpr_read_b32 v198,a66
v_accvgpr_read_b32 v199,a67
v_mfma_f32_16x16x32_bf16 a[244:247],a[188:191],v[92:95],0
ds_read_b64_tr_b16 a[148:149],v2 offset:10400
ds_read_b64_tr_b16 a[150:151],v2 offset:15520
v_mfma_f32_16x16x32_bf16 a[244:247],a[184:187],v[88:91],a[244:247]
v_cvt_pk_bf16_f32 v196,v196,v197
v_mul_f32 v198,v22,v198
v_mfma_f32_16x16x32_bf16 a[244:247],a[180:183],v[84:87],a[244:247]
buffer_load_dwordx4 v9,s[72:75],s80 offen offset:2048 lds
v_mfma_f32_16x16x32_bf16 a[244:247],a[176:179],v[80:83],a[244:247]
v_mul_f32 v199,v22,v199
v_cvt_pk_bf16_f32 v197,v198,v199
v_mfma_f32_16x16x32_bf16 a[248:251],a[176:179],v[96:99],0
ds_read_b64_tr_b16 a[152:153],v2 offset:20640
ds_read_b64_tr_b16 a[154:155],v2 offset:25760
v_mfma_f32_16x16x32_bf16 a[248:251],a[180:183],v[100:103],a[248:251]
v_accvgpr_read_b32 v198,a68
v_accvgpr_read_b32 v199,a69
v_mfma_f32_16x16x32_bf16 a[248:251],a[184:187],v[104:107],a[248:251]
v_accvgpr_read_b32 v16,a70
v_accvgpr_read_b32 v17,a71
v_mfma_f32_16x16x32_bf16 a[248:251],a[188:191],v[108:111],a[248:251]
v_mul_f32 v198,v22,v198
v_mul_f32 v199,v22,v199
v_mfma_f32_16x16x32_bf16 a[252:255],a[188:191],v[124:127],0
buffer_store_dwordx4 v[164:167],v6,s[36:39],s85 offen sc1
)A"
R"A(v_mfma_f32_16x16x32_bf16 a[252:255],a[184:187],v[120:123],a[252:255]
ds_read_b64_tr_b16 a[156:157],v2 offset:30880
ds_read_b64_tr_b16 a[158:159],v2 offset:36000
v_mfma_f32_16x16x32_bf16 a[252:255],a[180:183],v[116:119],a[252:255]
s_waitcnt vmcnt(19)
v_mul_f32 v16,v22,v16
v_mul_f32 v17,v22,v17
v_mfma_f32_16x16x32_bf16 a[252:255],a[176:179],v[112:115],a[252:255]
s_barrier
v_cvt_pk_bf16_f32 v198,v198,v199
v_cvt_pk_bf16_f32 v199,v16,v17
s_waitcnt lgkmcnt(4)
v_mfma_f32_16x16x32_bf16 a[0:3],a[128:131],v[64:67],0
v_accvgpr_read_b32 v204,a72
v_accvgpr_read_b32 v205,a73
v_mfma_f32_16x16x32_bf16 a[0:3],a[132:135],v[68:71],a[0:3]
buffer_load_dwordx4 v10,s[72:75],s80 offen offset:3072 lds
v_mfma_f32_16x16x32_bf16 a[0:3],a[136:139],v[72:75],a[0:3]
v_mul_f32 v204,v22,v204
v_mul_f32 v205,v22,v205
v_mfma_f32_16x16x32_bf16 a[0:3],a[140:143],v[76:79],a[0:3]
v_accvgpr_read_b32 v206,a74
v_accvgpr_read_b32 v207,a75
v_mfma_f32_16x16x32_bf16 a[4:7],a[140:143],v[92:95],0
v_cvt_pk_bf16_f32 v204,v204,v205
v_mul_f32 v206,v22,v206
v_mfma_f32_16x16x32_bf16 a[4:7],a[136:139],v[88:91],a[4:7]
s_lshl_b32 s94,s70,4
v_mul_f32 v207,v22,v207
v_cvt_pk_bf16_f32 v205,v206,v207
v_mfma_f32_16x16x32_bf16 a[4:7],a[132:135],v[84:87],a[4:7]
s_add_u32 s94,s94,s40
v_accvgpr_read_b32 v206,a76
v_accvgpr_read_b32 v207,a77
v_mfma_f32_16x16x32_bf16 a[4:7],a[128:131],v[80:83],a[4:7]
s_lshl_b32 s94,s94,4
v_accvgpr_read_b32 v12,a78
v_accvgpr_read_b32 v13,a79
v_mfma_f32_16x16x32_bf16 a[8:11],a[128:131],v[96:99],0
v_mul_f32 v206,v22,v206
v_mul_f32 v207,v22,v207
v_mfma_f32_16x16x32_bf16 a[8:11],a[132:135],v[100:103],a[8:11]
v_mul_f32 v12,v22,v12
v_mul_f32 v13,v22,v13
v_mfma_f32_16x16x32_bf16 a[8:11],a[136:139],v[104:107],a[8:11]
s_add_u32 m0,s82,126976
v_cvt_pk_bf16_f32 v206,v206,v207
v_cvt_pk_bf16_f32 v207,v12,v13
v_mfma_f32_16x16x32_bf16 a[8:11],a[140:143],v[108:111],a[8:11]
ds_read_b64_tr_b16 a[160:161],v1 offset:192
ds_read_b64_tr_b16 a[162:163],v1 offset:5312
v_mfma_f32_16x16x32_bf16 a[12:15],a[140:143],v[124:127],0
ds_read_b64_tr_b16 a[164:165],v1 offset:10432
v_accvgpr_read_b32 v212,a80
v_mfma_f32_16x16x32_bf16 a[12:15],a[136:139],v[120:123],a[12:15]
buffer_load_dwordx4 v11,s[72:75],s80 offen lds
v_mfma_f32_16x16x32_bf16 a[12:15],a[132:135],v[116:119],a[12:15]
ds_read_b64_tr_b16 a[166:167],v1 offset:15552
ds_read_b64_tr_b16 a[168:169],v1 offset:20672
v_mfma_f32_16x16x32_bf16 a[12:15],a[128:131],v[112:115],a[12:15]
ds_read_b64_tr_b16 a[170:171],v1 offset:25792
v_accvgpr_read_b32 v213,a81
v_mfma_f32_16x16x32_bf16 a[16:19],a[144:147],v[64:67],0
ds_read_b64_tr_b16 a[172:173],v1 offset:30912
v_mul_f32 v212,v22,v212
v_mfma_f32_16x16x32_bf16 a[16:19],a[148:151],v[68:71],a[16:19]
s_waitcnt lgkmcnt(7)
buffer_store_dwordx4 v[172:175],v6,s[36:39],s85 offen offset:1024 sc1
v_mfma_f32_16x16x32_bf16 a[16:19],a[152:155],v[72:75],a[16:19]
ds_read_b64_tr_b16 a[174:175],v1 offset:36032
v_mul_f32 v213,v22,v213
v_mfma_f32_16x16x32_bf16 a[16:19],a[156:159],v[76:79],a[16:19]
v_accvgpr_read_b32 v214,a82
v_accvgpr_read_b32 v215,a83
v_mfma_f32_16x16x32_bf16 a[20:23],a[156:159],v[92:95],0
v_cvt_pk_bf16_f32 v212,v212,v213
v_mul_f32 v214,v22,v214
v_mfma_f32_16x16x32_bf16 a[20:23],a[152:155],v[88:91],a[20:23]
v_mul_f32 v215,v22,v215
v_cvt_pk_bf16_f32 v213,v214,v215
v_mfma_f32_16x16x32_bf16 a[20:23],a[148:151],v[84:87],a[20:23]
s_add_u32 m0,s82,128000
v_accvgpr_read_b32 v214,a84
v_accvgpr_read_b32 v215,a85
v_mfma_f32_16x16x32_bf16 a[20:23],a[144:147],v[80:83],a[20:23]
v_accvgpr_read_b32 v14,a86
v_accvgpr_read_b32 v15,a87
v_mfma_f32_16x16x32_bf16 a[24:27],a[144:147],v[96:99],0
v_mul_f32 v214,v22,v214
v_mul_f32 v215,v22,v215
v_mfma_f32_16x16x32_bf16 a[24:27],a[148:151],v[100:103],a[24:27]
buffer_load_dwordx4 v7,s[72:75],s83 offen lds
v_mfma_f32_16x16x32_bf16 a[24:27],a[152:155],v[104:107],a[24:27]
v_mul_f32 v14,v22,v14
v_mul_f32 v15,v22,v15
v_mfma_f32_16x16x32_bf16 a[24:27],a[156:159],v[108:111],a[24:27]
v_cvt_pk_bf16_f32 v214,v214,v215
v_cvt_pk_bf16_f32 v215,v14,v15
v_mfma_f32_16x16x32_bf16 a[28:31],a[156:159],v[124:127],0
ds_read_b64_tr_b16 a[176:177],v2 offset:224
ds_read_b64_tr_b16 a[178:179],v2 offset:5344
v_mfma_f32_16x16x32_bf16 a[28:31],a[152:155],v[120:123],a[28:31]
ds_read_b64_tr_b16 a[180:181],v2 offset:10464
v_accvgpr_read_b32 v220,a88
v_mfma_f32_16x16x32_bf16 a[28:31],a[148:151],v[116:119],a[28:31]
ds_read_b64_tr_b16 a[182:183],v2 offset:15584
v_accvgpr_read_b32 v221,a89
v_mfma_f32_16x16x32_bf16 a[28:31],a[144:147],v[112:115],a[28:31]
s_waitcnt lgkmcnt(4)
ds_read_b64_tr_b16 a[184:185],v2 offset:20704
v_bfe_u32 v24,v6,4,2
v_mfma_f32_16x16x32_bf16 a[32:35],a[160:163],v[64:67],0
ds_read_b64_tr_b16 a[186:187],v2 offset:25824
ds_read_b64_tr_b16 a[188:189],v2 offset:30944
v_mfma_f32_16x16x32_bf16 a[32:35],a[164:167],v[68:71],a[32:35]
ds_read_b64_tr_b16 a[190:191],v2 offset:36064
v_mul_f32 v220,v22,v220
v_mfma_f32_16x16x32_bf16 a[32:35],a[168:171],v[72:75],a[32:35]
v_mul_f32 v221,v22,v221
v_accvgpr_read_b32 v222,a90
v_mfma_f32_16x16x32_bf16 a[32:35],a[172:175],v[76:79],a[32:35]
buffer_store_dwordx4 v[180:183],v6,s[36:39],s85 offen offset:2048 sc1
v_mfma_f32_16x16x32_bf16 a[36:39],a[172:175],v[92:95],0
v_accvgpr_read_b32 v223,a91
v_min_u32 v24,2,v24
v_mfma_f32_16x16x32_bf16 a[36:39],a[168:171],v[88:91],a[36:39]
v_cvt_pk_bf16_f32 v220,v220,v221
v_mul_f32 v222,v22,v222
v_mfma_f32_16x16x32_bf16 a[36:39],a[164:167],v[84:87],a[36:39]
v_mul_f32 v223,v22,v223
v_add_u32 v24,s71,v24
v_mfma_f32_16x16x32_bf16 a[36:39],a[160:163],v[80:83],a[36:39]
buffer_load_dwordx4 v8,s[72:75],s83 offen offset:1024 lds
v_mfma_f32_16x16x32_bf16 a[40:43],a[160:163],v[96:99],0
v_cvt_pk_bf16_f32 v221,v222,v223
v_add_u32 v24,1,v24
v_mfma_f32_16x16x32_bf16 a[40:43],a[164:167],v[100:103],a[40:43]
v_accvgpr_read_b32 v222,a92
v_accvgpr_read_b32 v223,a93
v_mfma_f32_16x16x32_bf16 a[40:43],a[168:171],v[104:107],a[40:43]
v_and_b32 v24,3,v24
v_accvgpr_read_b32 v16,a94
v_mfma_f32_16x16x32_bf16 a[40:43],a[172:175],v[108:111],a[40:43]
v_accvgpr_read_b32 v17,a95
v_mul_f32 v222,v22,v222
v_mfma_f32_16x16x32_bf16 a[44:47],a[172:175],v[124:127],0
v_mul_f32 v223,v22,v223
v_lshlrev_b32 v24,6,v24
v_mfma_f32_16x16x32_bf16 a[44:47],a[168:171],v[120:123],a[44:47]
v_mul_f32 v16,v22,v16
v_mul_f32 v17,v22,v17
v_mfma_f32_16x16x32_bf16 a[44:47],a[164:167],v[116:119],a[44:47]
v_cvt_pk_bf16_f32 v222,v222,v223
v_cvt_pk_bf16_f32 v223,v16,v17
v_mfma_f32_16x16x32_bf16 a[44:47],a[160:163],v[112:115],a[44:47]
s_waitcnt lgkmcnt(0)
buffer_load_dwordx2 v[26:27],v24,s[64:67],s94 offen sc1
v_mfma_f32_16x16x32_bf16 a[48:51],a[176:179],v[64:67],0
ds_read_b64_tr_b16 a[128:129],v1 offset:256
v_max_f32 v23,0x00800000,v60
v_mfma_f32_16x16x32_bf16 a[48:51],a[180:183],v[68:71],a[48:51]
v_rcp_f32 v23,v23
v_mfma_f32_16x16x32_bf16 a[48:51],a[184:187],v[72:75],a[48:51]
ds_read_b64_tr_b16 a[130:131],v1 offset:5376
v_accvgpr_read_b32 v228,a96
v_mfma_f32_16x16x32_bf16 a[48:51],a[188:191],v[76:79],a[48:51]
buffer_load_dwordx4 v9,s[72:75],s83 offen offset:2048 lds
v_mfma_f32_16x16x32_bf16 a[52:55],a[188:191],v[92:95],0
v_accvgpr_read_b32 v229,a97
v_mul_f32 v228,v23,v228
v_mfma_f32_16x16x32_bf16 a[52:55],a[184:187],v[88:91],a[52:55]
v_mul_f32 v229,v23,v229
v_accvgpr_read_b32 v230,a98
v_mfma_f32_16x16x32_bf16 a[52:55],a[180:183],v[84:87],a[52:55]
ds_read_b64_tr_b16 a[132:133],v1 offset:10496
v_accvgpr_read_b32 v231,a99
v_mfma_f32_16x16x32_bf16 a[52:55],a[176:179],v[80:83],a[52:55]
buffer_store_dwordx4 v[188:191],v6,s[36:39],s85 offen offset:3072 sc1
v_mfma_f32_16x16x32_bf16 a[56:59],a[176:179],v[96:99],0
ds_read_b64_tr_b16 a[134:135],v1 offset:15616
v_cvt_pk_bf16_f32 v228,v228,v229
v_mfma_f32_16x16x32_bf16 a[56:59],a[180:183],v[100:103],a[56:59]
ds_read_b64_tr_b16 a[136:137],v1 offset:20736
ds_read_b64_tr_b16 a[138:139],v1 offset:25856
v_mfma_f32_16x16x32_bf16 a[56:59],a[184:187],v[104:107],a[56:59]
ds_read_b64_tr_b16 a[140:141],v1 offset:30976
ds_read_b64_tr_b16 a[142:143],v1 offset:36096
v_mfma_f32_16x16x32_bf16 a[56:59],a[188:191],v[108:111],a[56:59]
v_mul_f32 v230,v23,v230
v_mul_f32 v231,v23,v231
v_mfma_f32_16x16x32_bf16 a[60:63],a[188:191],v[124:127],0
v_cvt_pk_bf16_f32 v229,v230,v231
v_accvgpr_read_b32 v230,a100
v_mfma_f32_16x16x32_bf16 a[60:63],a[184:187],v[120:123],a[60:63]
v_accvgpr_read_b32 v231,a101
v_accvgpr_read_b32 v12,a102
v_mfma_f32_16x16x32_bf16 a[60:63],a[180:183],v[116:119],a[60:63]
v_accvgpr_read_b32 v13,a103
v_mul_f32 v230,v23,v230
v_mfma_f32_16x16x32_bf16 a[60:63],a[176:179],v[112:115],a[60:63]
s_waitcnt lgkmcnt(5)
v_mul_f32 v231,v23,v231
v_mul_f32 v12,v23,v12
v_mfma_f32_16x16x32_bf16 a[64:67],a[128:131],v[64:67],0
s_waitcnt lgkmcnt(4)
buffer_load_dwordx4 v10,s[72:75],s83 offen offset:3072 lds
v_mfma_f32_16x16x32_bf16 a[64:67],a[132:135],v[68:71],a[64:67]
s_waitcnt lgkmcnt(2)
v_mul_f32 v13,v23,v13
v_cvt_pk_bf16_f32 v230,v230,v231
v_mfma_f32_16x16x32_bf16 a[64:67],a[136:139],v[72:75],a[64:67]
s_waitcnt lgkmcnt(0)
v_cvt_pk_bf16_f32 v231,v12,v13
v_accvgpr_read_b32 v236,a104
v_mfma_f32_16x16x32_bf16 a[64:67],a[140:143],v[76:79],a[64:67]
v_accvgpr_read_b32 v237,a105
v_mul_f32 v236,v23,v236
v_mfma_f32_16x16x32_bf16 a[68:71],a[140:143],v[92:95],0
v_mul_f32 v237,v23,v237
v_accvgpr_read_b32 v238,a106
v_mfma_f32_16x16x32_bf16 a[68:71],a[136:139],v[88:91],a[68:71]
v_accvgpr_read_b32 v239,a107
v_cvt_pk_bf16_f32 v236,v236,v237
v_mfma_f32_16x16x32_bf16 a[68:71],a[132:135],v[84:87],a[68:71]
s_add_u32 m0,s82,132096
v_mul_f32 v238,v23,v238
v_mul_f32 v239,v23,v239
v_mfma_f32_16x16x32_bf16 a[68:71],a[128:131],v[80:83],a[68:71]
ds_read_b64_tr_b16 a[144:145],v2 offset:288
ds_read_b64_tr_b16 a[146:147],v2 offset:5408
v_mfma_f32_16x16x32_bf16 a[72:75],a[128:131],v[96:99],0
ds_read_b64_tr_b16 a[148:149],v2 offset:10528
ds_read_b64_tr_b16 a[150:151],v2 offset:15648
v_mfma_f32_16x16x32_bf16 a[72:75],a[132:135],v[100:103],a[72:75]
ds_read_b64_tr_b16 a[152:153],v2 offset:20768
ds_read_b64_tr_b16 a[154:155],v2 offset:25888
v_mfma_f32_16x16x32_bf16 a[72:75],a[136:139],v[104:107],a[72:75]
ds_read_b64_tr_b16 a[156:157],v2 offset:31008
ds_read_b64_tr_b16 a[158:159],v2 offset:36128
v_mfma_f32_16x16x32_bf16 a[72:75],a[140:143],v[108:111],a[72:75]
v_mfma_f32_16x16x32_bf16 a[76:79],a[140:143],v[124:127],0
buffer_load_dwordx4 v11,s[72:75],s83 offen lds
v_mfma_f32_16x16x32_bf16 a[76:79],a[136:139],v[120:123],a[76:79]
v_cvt_pk_bf16_f32 v237,v238,v239
v_accvgpr_read_b32 v238,a108
v_mfma_f32_16x16x32_bf16 a[76:79],a[132:135],v[116:119],a[76:79]
v_accvgpr_read_b32 v239,a109
v_accvgpr_read_b32 v14,a110
v_mfma_f32_16x16x32_bf16 a[76:79],a[128:131],v[112:115],a[76:79]
s_waitcnt lgkmcnt(6)
v_accvgpr_read_b32 v15,a111
v_mul_f32 v238,v23,v238
v_mfma_f32_16x16x32_bf16 a[80:83],a[144:147],v[64:67],0
s_waitcnt lgkmcnt(4)
v_mul_f32 v239,v23,v239
v_mul_f32 v14,v23,v14
v_mfma_f32_16x16x32_bf16 a[80:83],a[148:151],v[68:71],a[80:83]
s_waitcnt lgkmcnt(2)
v_mul_f32 v15,v23,v15
v_cvt_pk_bf16_f32 v238,v238,v239
v_mfma_f32_16x16x32_bf16 a[80:83],a[152:155],v[72:75],a[80:83]
s_waitcnt lgkmcnt(0)
v_cvt_pk_bf16_f32 v239,v14,v15
v_accvgpr_read_b32 v244,a112
v_mfma_f32_16x16x32_bf16 a[80:83],a[156:159],v[76:79],a[80:83]
v_accvgpr_read_b32 v245,a113
v_mul_f32 v244,v23,v244
v_mfma_f32_16x16x32_bf16 a[84:87],a[156:159],v[92:95],0
v_mul_f32 v245,v23,v245
v_accvgpr_read_b32 v246,a114
v_mfma_f32_16x16x32_bf16 a[84:87],a[152:155],v[88:91],a[84:87]
v_accvgpr_read_b32 v247,a115
v_cvt_pk_bf16_f32 v244,v244,v245
v_mfma_f32_16x16x32_bf16 a[84:87],a[148:151],v[84:87],a[84:87]
v_subrev_u32 v1,81920,v1
v_mul_f32 v246,v23,v246
v_mfma_f32_16x16x32_bf16 a[84:87],a[144:147],v[80:83],a[84:87]
ds_read_b64_tr_b16 a[160:161],v1
ds_read_b64_tr_b16 a[162:163],v1 offset:5120
v_mfma_f32_16x16x32_bf16 a[88:91],a[144:147],v[96:99],0
ds_read_b64_tr_b16 a[164:165],v1 offset:10240
ds_read_b64_tr_b16 a[166:167],v1 offset:15360
v_mfma_f32_16x16x32_bf16 a[88:91],a[148:151],v[100:103],a[88:91]
ds_read_b64_tr_b16 a[168:169],v1 offset:20480
ds_read_b64_tr_b16 a[170:171],v1 offset:25600
v_mfma_f32_16x16x32_bf16 a[88:91],a[152:155],v[104:107],a[88:91]
ds_read_b64_tr_b16 a[172:173],v1 offset:30720
ds_read_b64_tr_b16 a[174:175],v1 offset:35840
)A"
R"A(v_mfma_f32_16x16x32_bf16 a[88:91],a[156:159],v[108:111],a[88:91]
v_accvgpr_read_b32 v252,a120
v_accvgpr_read_b32 v253,a121
v_cmp_nlt_f32 s[56:57],v48,s63
v_mul_f32 v252,v23,v252
v_mul_f32 v253,v23,v253
v_accvgpr_read_b32 v254,a122
v_accvgpr_read_b32 v255,a123
v_mfma_f32_16x16x32_bf16 a[92:95],a[156:159],v[124:127],0
v_mul_f32 v247,v23,v247
v_cvt_pk_bf16_f32 v245,v246,v247
v_cvt_pk_bf16_f32 v252,v252,v253
v_mul_f32 v254,v23,v254
v_mul_f32 v255,v23,v255
v_cmp_nlt_f32 s[58:59],v52,s63
s_nop 1
s_or_b64 s[56:57],s[56:57],s[58:59]
v_cvt_pk_bf16_f32 v253,v254,v255
v_mfma_f32_16x16x32_bf16 a[92:95],a[152:155],v[120:123],a[92:95]
v_accvgpr_read_b32 v246,a116
v_accvgpr_read_b32 v247,a117
v_accvgpr_read_b32 v16,a118
v_accvgpr_read_b32 v17,a119
v_mul_f32 v246,v23,v246
v_mul_f32 v247,v23,v247
v_accvgpr_read_b32 v254,a124
v_accvgpr_read_b32 v255,a125
v_cmp_nlt_f32 s[58:59],v56,s63
s_nop 1
s_or_b64 s[56:57],s[56:57],s[58:59]
v_accvgpr_read_b32 v12,a126
v_accvgpr_read_b32 v13,a127
v_mul_f32 v254,v23,v254
v_mul_f32 v255,v23,v255
s_waitcnt vmcnt(4)
v_cmp_ne_u32 s[90:91],s68,v26
v_cmp_ne_u32 s[92:93],s69,v27
s_nop 0
s_or_b64 s[90:91],s[90:91],s[92:93]
s_cbranch_scc1 L_fr8_%=
L_fo8_%=:
v_mfma_f32_16x16x32_bf16 a[92:95],a[148:151],v[116:119],a[92:95]
v_mul_f32 v16,v23,v16
v_mul_f32 v17,v23,v17
v_cvt_pk_bf16_f32 v246,v246,v247
v_cvt_pk_bf16_f32 v247,v16,v17
v_mul_f32 v12,v23,v12
v_mul_f32 v13,v23,v13
v_cvt_pk_bf16_f32 v254,v254,v255
v_cmp_nlt_f32 s[58:59],v60,s63
s_nop 1
s_or_b64 s[56:57],s[56:57],s[58:59]
v_cvt_pk_bf16_f32 v255,v12,v13
v_mfma_f32_16x16x32_bf16 a[92:95],a[144:147],v[112:115],a[92:95]
s_cmp_lg_u64 s[56:57],0
s_cbranch_scc1 L_slow1_%=
L_sret1_%=:
v_subrev_u32 v2,81920,v2
s_waitcnt lgkmcnt(6)
v_mfma_f32_16x16x32_bf16 a[192:195],a[160:163],v[132:135],a[192:195]
s_cmp_ge_u32 0,s71
s_cselect_b32 s76,2,0
s_lshl_b32 s76,s76,16
s_waitcnt lgkmcnt(4)
v_mfma_f32_16x16x32_bf16 a[192:195],a[164:167],v[140:143],a[192:195]
s_add_u32 s77,s76,4096
s_add_u32 s78,s76,8192
s_add_u32 s79,s76,12288
s_max_i32 s76,s76,s18
s_waitcnt lgkmcnt(2)
v_mfma_f32_16x16x32_bf16 a[192:195],a[168:171],v[148:151],a[192:195]
s_max_i32 s77,s77,s19
buffer_load_dwordx4 v[128:131],v6,s[28:31],s76 offen sc1
s_waitcnt lgkmcnt(0)
v_mfma_f32_16x16x32_bf16 a[192:195],a[172:175],v[156:159],a[192:195]
s_max_i32 s78,s78,s100
ds_read_b64_tr_b16 a[176:177],v2 offset:32
ds_read_b64_tr_b16 a[178:179],v2 offset:5152
v_mfma_f32_16x16x32_bf16 a[196:199],a[172:175],v[188:191],a[196:199]
s_cmp_ge_u32 2,s71
s_cselect_b32 s80,6,4
ds_read_b64_tr_b16 a[180:181],v2 offset:10272
v_mfma_f32_16x16x32_bf16 a[196:199],a[168:171],v[180:183],a[196:199]
s_mul_i32 s80,s80,163840
s_add_u32 s80,s80,s81
ds_read_b64_tr_b16 a[182:183],v2 offset:15392
v_mfma_f32_16x16x32_bf16 a[196:199],a[164:167],v[172:175],a[196:199]
s_max_i32 s79,s79,s101
buffer_store_dwordx4 v[196:199],v6,s[36:39],s86 offen sc1
v_mfma_f32_16x16x32_bf16 a[196:199],a[160:163],v[164:167],a[196:199]
s_add_u32 s83,s80,20480
ds_read_b64_tr_b16 a[184:185],v2 offset:20512
ds_read_b64_tr_b16 a[186:187],v2 offset:25632
v_mfma_f32_16x16x32_bf16 a[200:203],a[160:163],v[196:199],a[200:203]
ds_read_b64_tr_b16 a[188:189],v2 offset:30752
v_mfma_f32_16x16x32_bf16 a[200:203],a[164:167],v[204:207],a[200:203]
ds_read_b64_tr_b16 a[190:191],v2 offset:35872
v_mfma_f32_16x16x32_bf16 a[200:203],a[168:171],v[212:215],a[200:203]
buffer_load_dwordx4 v[160:163],v6,s[28:31],s76 offen offset:1024 sc1
v_mfma_f32_16x16x32_bf16 a[200:203],a[172:175],v[220:223],a[200:203]
ds_read_b64_tr_b16 a[128:129],v1 offset:64
ds_read_b64_tr_b16 a[130:131],v1 offset:5184
v_mfma_f32_16x16x32_bf16 a[204:207],a[172:175],v[252:255],a[204:207]
ds_read_b64_tr_b16 a[132:133],v1 offset:10304
v_mfma_f32_16x16x32_bf16 a[204:207],a[168:171],v[244:247],a[204:207]
ds_read_b64_tr_b16 a[134:135],v1 offset:15424
v_mfma_f32_16x16x32_bf16 a[204:207],a[164:167],v[236:239],a[204:207]
buffer_store_dwordx4 v[204:207],v6,s[36:39],s86 offen offset:1024 sc1
v_mfma_f32_16x16x32_bf16 a[204:207],a[160:163],v[228:231],a[204:207]
s_barrier
ds_read_b64_tr_b16 a[136:137],v1 offset:20544
ds_read_b64_tr_b16 a[138:139],v1 offset:25664
s_waitcnt lgkmcnt(8)
v_mfma_f32_16x16x32_bf16 a[208:211],a[176:179],v[132:135],a[208:211]
ds_read_b64_tr_b16 a[140:141],v1 offset:30784
v_mfma_f32_16x16x32_bf16 a[208:211],a[180:183],v[140:143],a[208:211]
buffer_load_dwordx4 v[192:195],v6,s[28:31],s76 offen offset:2048 sc1
v_mfma_f32_16x16x32_bf16 a[208:211],a[184:187],v[148:151],a[208:211]
s_add_u32 m0,s82,81920
ds_read_b64_tr_b16 a[142:143],v1 offset:35904
ds_read_b64_tr_b16 a[144:145],v2 offset:96
s_waitcnt lgkmcnt(9)
v_mfma_f32_16x16x32_bf16 a[208:211],a[188:191],v[156:159],a[208:211]
ds_read_b64_tr_b16 a[146:147],v2 offset:5216
v_mfma_f32_16x16x32_bf16 a[212:215],a[188:191],v[188:191],a[212:215]
ds_read_b64_tr_b16 a[148:149],v2 offset:10336
v_mfma_f32_16x16x32_bf16 a[212:215],a[184:187],v[180:183],a[212:215]
buffer_store_dwordx4 v[212:215],v6,s[36:39],s86 offen offset:2048 sc1
v_mfma_f32_16x16x32_bf16 a[212:215],a[180:183],v[172:175],a[212:215]
ds_read_b64_tr_b16 a[150:151],v2 offset:15456
ds_read_b64_tr_b16 a[152:153],v2 offset:20576
v_mfma_f32_16x16x32_bf16 a[212:215],a[176:179],v[164:167],a[212:215]
ds_read_b64_tr_b16 a[154:155],v2 offset:25696
v_mfma_f32_16x16x32_bf16 a[216:219],a[176:179],v[196:199],a[216:219]
ds_read_b64_tr_b16 a[156:157],v2 offset:30816
v_mfma_f32_16x16x32_bf16 a[216:219],a[180:183],v[204:207],a[216:219]
buffer_load_dwordx4 v[224:227],v6,s[28:31],s76 offen offset:3072 sc1
v_mfma_f32_16x16x32_bf16 a[216:219],a[184:187],v[212:215],a[216:219]
ds_read_b64_tr_b16 a[158:159],v2 offset:35936
ds_read_b64_tr_b16 a[160:161],v1 offset:128
v_mfma_f32_16x16x32_bf16 a[216:219],a[188:191],v[220:223],a[216:219]
ds_read_b64_tr_b16 a[162:163],v1 offset:5248
v_mfma_f32_16x16x32_bf16 a[220:223],a[188:191],v[252:255],a[220:223]
ds_read_b64_tr_b16 a[164:165],v1 offset:10368
v_mfma_f32_16x16x32_bf16 a[220:223],a[184:187],v[244:247],a[220:223]
buffer_load_dwordx4 v7,s[72:75],s80 offen lds
v_mfma_f32_16x16x32_bf16 a[220:223],a[180:183],v[236:239],a[220:223]
ds_read_b64_tr_b16 a[166:167],v1 offset:15488
ds_read_b64_tr_b16 a[168:169],v1 offset:20608
v_mfma_f32_16x16x32_bf16 a[220:223],a[176:179],v[228:231],a[220:223]
s_waitcnt lgkmcnt(7)
ds_read_b64_tr_b16 a[170:171],v1 offset:25728
v_mfma_f32_16x16x32_bf16 a[224:227],a[128:131],v[132:135],a[224:227]
ds_read_b64_tr_b16 a[172:173],v1 offset:30848
v_mfma_f32_16x16x32_bf16 a[224:227],a[132:135],v[140:143],a[224:227]
buffer_load_dwordx4 v[136:139],v6,s[28:31],s77 offen sc1
v_mfma_f32_16x16x32_bf16 a[224:227],a[136:139],v[148:151],a[224:227]
ds_read_b64_tr_b16 a[174:175],v1 offset:35968
ds_read_b64_tr_b16 a[176:177],v2 offset:160
v_mfma_f32_16x16x32_bf16 a[224:227],a[140:143],v[156:159],a[224:227]
ds_read_b64_tr_b16 a[178:179],v2 offset:5280
v_mfma_f32_16x16x32_bf16 a[228:231],a[140:143],v[188:191],a[228:231]
ds_read_b64_tr_b16 a[180:181],v2 offset:10400
v_mfma_f32_16x16x32_bf16 a[228:231],a[136:139],v[180:183],a[228:231]
buffer_load_dwordx4 v[168:171],v6,s[28:31],s77 offen offset:1024 sc1
v_mfma_f32_16x16x32_bf16 a[228:231],a[132:135],v[172:175],a[228:231]
ds_read_b64_tr_b16 a[182:183],v2 offset:15520
ds_read_b64_tr_b16 a[184:185],v2 offset:20640
v_mfma_f32_16x16x32_bf16 a[228:231],a[128:131],v[164:167],a[228:231]
ds_read_b64_tr_b16 a[186:187],v2 offset:25760
v_mfma_f32_16x16x32_bf16 a[232:235],a[128:131],v[196:199],a[232:235]
ds_read_b64_tr_b16 a[188:189],v2 offset:30880
v_mfma_f32_16x16x32_bf16 a[232:235],a[132:135],v[204:207],a[232:235]
buffer_load_dwordx4 v8,s[72:75],s80 offen offset:1024 lds
v_mfma_f32_16x16x32_bf16 a[232:235],a[136:139],v[212:215],a[232:235]
ds_read_b64_tr_b16 a[190:191],v2 offset:36000
v_mfma_f32_16x16x32_bf16 a[232:235],a[140:143],v[220:223],a[232:235]
v_mfma_f32_16x16x32_bf16 a[236:239],a[140:143],v[252:255],a[236:239]
ds_read_b64_tr_b16 a[140:141],v1 offset:30912
ds_read_b64_tr_b16 a[142:143],v1 offset:36032
v_mfma_f32_16x16x32_bf16 a[236:239],a[136:139],v[244:247],a[236:239]
buffer_load_dwordx4 v[200:203],v6,s[28:31],s77 offen offset:2048 sc1
v_mfma_f32_16x16x32_bf16 a[236:239],a[132:135],v[236:239],a[236:239]
ds_read_b64_tr_b16 a[132:133],v1 offset:10432
ds_read_b64_tr_b16 a[134:135],v1 offset:15552
v_mfma_f32_16x16x32_bf16 a[236:239],a[128:131],v[228:231],a[236:239]
ds_read_b64_tr_b16 a[128:129],v1 offset:192
v_mfma_f32_16x16x32_bf16 a[240:243],a[144:147],v[132:135],a[240:243]
ds_read_b64_tr_b16 a[130:131],v1 offset:5312
v_mfma_f32_16x16x32_bf16 a[240:243],a[148:151],v[140:143],a[240:243]
buffer_store_dwordx4 v[220:223],v6,s[36:39],s86 offen offset:3072 sc1
v_mfma_f32_16x16x32_bf16 a[240:243],a[152:155],v[148:151],a[240:243]
s_waitcnt lgkmcnt(6)
ds_read_b64_tr_b16 a[136:137],v1 offset:20672
ds_read_b64_tr_b16 a[138:139],v1 offset:25792
v_mfma_f32_16x16x32_bf16 a[240:243],a[156:159],v[156:159],a[240:243]
v_mfma_f32_16x16x32_bf16 a[244:247],a[156:159],v[188:191],a[244:247]
v_mfma_f32_16x16x32_bf16 a[244:247],a[152:155],v[180:183],a[244:247]
buffer_load_dwordx4 v[232:235],v6,s[28:31],s77 offen offset:3072 sc1
v_mfma_f32_16x16x32_bf16 a[244:247],a[148:151],v[172:175],a[244:247]
v_mfma_f32_16x16x32_bf16 a[244:247],a[144:147],v[164:167],a[244:247]
v_mfma_f32_16x16x32_bf16 a[248:251],a[144:147],v[196:199],a[248:251]
v_mfma_f32_16x16x32_bf16 a[248:251],a[148:151],v[204:207],a[248:251]
buffer_load_dwordx4 v9,s[72:75],s80 offen offset:2048 lds
v_mfma_f32_16x16x32_bf16 a[248:251],a[152:155],v[212:215],a[248:251]
v_mfma_f32_16x16x32_bf16 a[248:251],a[156:159],v[220:223],a[248:251]
v_mfma_f32_16x16x32_bf16 a[252:255],a[156:159],v[252:255],a[252:255]
ds_read_b64_tr_b16 a[156:157],v2 offset:30944
ds_read_b64_tr_b16 a[158:159],v2 offset:36064
v_mfma_f32_16x16x32_bf16 a[252:255],a[152:155],v[244:247],a[252:255]
buffer_load_dwordx4 v[144:147],v6,s[28:31],s78 offen sc1
v_mfma_f32_16x16x32_bf16 a[252:255],a[148:151],v[236:239],a[252:255]
ds_read_b64_tr_b16 a[148:149],v2 offset:10464
ds_read_b64_tr_b16 a[150:151],v2 offset:15584
v_mfma_f32_16x16x32_bf16 a[252:255],a[144:147],v[228:231],a[252:255]
ds_read_b64_tr_b16 a[144:145],v2 offset:224
v_mfma_f32_16x16x32_bf16 a[0:3],a[160:163],v[132:135],a[0:3]
ds_read_b64_tr_b16 a[146:147],v2 offset:5344
v_mfma_f32_16x16x32_bf16 a[0:3],a[164:167],v[140:143],a[0:3]
buffer_load_dwordx4 v[176:179],v6,s[28:31],s78 offen offset:1024 sc1
v_mfma_f32_16x16x32_bf16 a[0:3],a[168:171],v[148:151],a[0:3]
ds_read_b64_tr_b16 a[152:153],v2 offset:20704
ds_read_b64_tr_b16 a[154:155],v2 offset:25824
v_mfma_f32_16x16x32_bf16 a[0:3],a[172:175],v[156:159],a[0:3]
v_mfma_f32_16x16x32_bf16 a[4:7],a[172:175],v[188:191],a[4:7]
v_mfma_f32_16x16x32_bf16 a[4:7],a[168:171],v[180:183],a[4:7]
buffer_load_dwordx4 v10,s[72:75],s80 offen offset:3072 lds
v_mfma_f32_16x16x32_bf16 a[4:7],a[164:167],v[172:175],a[4:7]
v_mfma_f32_16x16x32_bf16 a[4:7],a[160:163],v[164:167],a[4:7]
v_mfma_f32_16x16x32_bf16 a[8:11],a[160:163],v[196:199],a[8:11]
v_mfma_f32_16x16x32_bf16 a[8:11],a[164:167],v[204:207],a[8:11]
buffer_load_dwordx4 v[208:211],v6,s[28:31],s78 offen offset:2048 sc1
v_mfma_f32_16x16x32_bf16 a[8:11],a[168:171],v[212:215],a[8:11]
v_mfma_f32_16x16x32_bf16 a[8:11],a[172:175],v[220:223],a[8:11]
v_mfma_f32_16x16x32_bf16 a[12:15],a[172:175],v[252:255],a[12:15]
ds_read_b64_tr_b16 a[172:173],v1 offset:30976
ds_read_b64_tr_b16 a[174:175],v1 offset:36096
v_mfma_f32_16x16x32_bf16 a[12:15],a[168:171],v[244:247],a[12:15]
buffer_store_dwordx4 v[228:231],v6,s[36:39],s87 offen sc1
v_mfma_f32_16x16x32_bf16 a[12:15],a[164:167],v[236:239],a[12:15]
s_add_u32 m0,s82,86016
ds_read_b64_tr_b16 a[164:165],v1 offset:10496
ds_read_b64_tr_b16 a[166:167],v1 offset:15616
v_mfma_f32_16x16x32_bf16 a[12:15],a[160:163],v[228:231],a[12:15]
ds_read_b64_tr_b16 a[160:161],v1 offset:256
v_mfma_f32_16x16x32_bf16 a[16:19],a[176:179],v[132:135],a[16:19]
ds_read_b64_tr_b16 a[162:163],v1 offset:5376
v_mfma_f32_16x16x32_bf16 a[16:19],a[180:183],v[140:143],a[16:19]
buffer_load_dwordx4 v[240:243],v6,s[28:31],s78 offen offset:3072 sc1
v_mfma_f32_16x16x32_bf16 a[16:19],a[184:187],v[148:151],a[16:19]
)A"
R"A(ds_read_b64_tr_b16 a[168:169],v1 offset:20736
ds_read_b64_tr_b16 a[170:171],v1 offset:25856
v_mfma_f32_16x16x32_bf16 a[16:19],a[188:191],v[156:159],a[16:19]
v_add_u32 v1,40960,v1
v_mfma_f32_16x16x32_bf16 a[20:23],a[188:191],v[188:191],a[20:23]
ds_read_b64_tr_b16 a[96:97],v1 offset:128
ds_read_b64_tr_b16 a[98:99],v1 offset:5248
v_mfma_f32_16x16x32_bf16 a[20:23],a[184:187],v[180:183],a[20:23]
buffer_load_dwordx4 v11,s[72:75],s80 offen lds
v_mfma_f32_16x16x32_bf16 a[20:23],a[180:183],v[172:175],a[20:23]
ds_read_b64_tr_b16 a[100:101],v1 offset:10368
ds_read_b64_tr_b16 a[102:103],v1 offset:15488
v_mfma_f32_16x16x32_bf16 a[20:23],a[176:179],v[164:167],a[20:23]
ds_read_b64_tr_b16 a[104:105],v1 offset:20608
v_mfma_f32_16x16x32_bf16 a[24:27],a[176:179],v[196:199],a[24:27]
ds_read_b64_tr_b16 a[106:107],v1 offset:25728
v_mfma_f32_16x16x32_bf16 a[24:27],a[180:183],v[204:207],a[24:27]
buffer_load_dwordx4 v[152:155],v6,s[28:31],s79 offen sc1
v_mfma_f32_16x16x32_bf16 a[24:27],a[184:187],v[212:215],a[24:27]
ds_read_b64_tr_b16 a[108:109],v1 offset:30848
ds_read_b64_tr_b16 a[110:111],v1 offset:35968
v_mfma_f32_16x16x32_bf16 a[24:27],a[188:191],v[220:223],a[24:27]
v_mfma_f32_16x16x32_bf16 a[28:31],a[188:191],v[252:255],a[28:31]
ds_read_b64_tr_b16 a[188:189],v2 offset:31008
ds_read_b64_tr_b16 a[190:191],v2 offset:36128
v_mfma_f32_16x16x32_bf16 a[28:31],a[184:187],v[244:247],a[28:31]
s_add_u32 m0,s82,87040
buffer_load_dwordx4 v[184:187],v6,s[28:31],s79 offen offset:1024 sc1
v_mfma_f32_16x16x32_bf16 a[28:31],a[180:183],v[236:239],a[28:31]
ds_read_b64_tr_b16 a[180:181],v2 offset:10528
ds_read_b64_tr_b16 a[182:183],v2 offset:15648
v_mfma_f32_16x16x32_bf16 a[28:31],a[176:179],v[228:231],a[28:31]
s_waitcnt lgkmcnt(7)
ds_read_b64_tr_b16 a[176:177],v2 offset:288
v_mfma_f32_16x16x32_bf16 a[32:35],a[128:131],v[132:135],a[32:35]
ds_read_b64_tr_b16 a[178:179],v2 offset:5408
v_mfma_f32_16x16x32_bf16 a[32:35],a[132:135],v[140:143],a[32:35]
buffer_store_dwordx4 v[236:239],v6,s[36:39],s87 offen offset:1024 sc1
v_mfma_f32_16x16x32_bf16 a[32:35],a[136:139],v[148:151],a[32:35]
ds_read_b64_tr_b16 a[184:185],v2 offset:20768
ds_read_b64_tr_b16 a[186:187],v2 offset:25888
v_mfma_f32_16x16x32_bf16 a[32:35],a[140:143],v[156:159],a[32:35]
v_add_u32 v2,40960,v2
v_mfma_f32_16x16x32_bf16 a[36:39],a[140:143],v[188:191],a[36:39]
ds_read_b64_tr_b16 a[112:113],v2 offset:160
ds_read_b64_tr_b16 a[114:115],v2 offset:5280
v_mfma_f32_16x16x32_bf16 a[36:39],a[136:139],v[180:183],a[36:39]
buffer_load_dwordx4 v[216:219],v6,s[28:31],s79 offen offset:2048 sc1
v_mfma_f32_16x16x32_bf16 a[36:39],a[132:135],v[172:175],a[36:39]
ds_read_b64_tr_b16 a[116:117],v2 offset:10400
ds_read_b64_tr_b16 a[118:119],v2 offset:15520
v_mfma_f32_16x16x32_bf16 a[36:39],a[128:131],v[164:167],a[36:39]
ds_read_b64_tr_b16 a[120:121],v2 offset:20640
v_mfma_f32_16x16x32_bf16 a[40:43],a[128:131],v[196:199],a[40:43]
ds_read_b64_tr_b16 a[122:123],v2 offset:25760
v_mfma_f32_16x16x32_bf16 a[40:43],a[132:135],v[204:207],a[40:43]
buffer_load_dwordx4 v7,s[72:75],s83 offen lds
v_mfma_f32_16x16x32_bf16 a[40:43],a[136:139],v[212:215],a[40:43]
ds_read_b64_tr_b16 a[124:125],v2 offset:30880
ds_read_b64_tr_b16 a[126:127],v2 offset:36000
v_mfma_f32_16x16x32_bf16 a[40:43],a[140:143],v[220:223],a[40:43]
v_mfma_f32_16x16x32_bf16 a[44:47],a[140:143],v[252:255],a[44:47]
ds_read_b64_tr_b16 a[140:141],v1 offset:30720
ds_read_b64_tr_b16 a[142:143],v1 offset:35840
v_mfma_f32_16x16x32_bf16 a[44:47],a[136:139],v[244:247],a[44:47]
buffer_load_dwordx4 v[248:251],v6,s[28:31],s79 offen offset:3072 sc1
v_mfma_f32_16x16x32_bf16 a[44:47],a[132:135],v[236:239],a[44:47]
ds_read_b64_tr_b16 a[132:133],v1 offset:10240
ds_read_b64_tr_b16 a[134:135],v1 offset:15360
v_mfma_f32_16x16x32_bf16 a[44:47],a[128:131],v[228:231],a[44:47]
ds_read_b64_tr_b16 a[128:129],v1
v_mfma_f32_16x16x32_bf16 a[48:51],a[144:147],v[132:135],a[48:51]
ds_read_b64_tr_b16 a[130:131],v1 offset:5120
v_mfma_f32_16x16x32_bf16 a[48:51],a[148:151],v[140:143],a[48:51]
buffer_store_dwordx4 v[244:247],v6,s[36:39],s87 offen offset:2048 sc1
v_mfma_f32_16x16x32_bf16 a[48:51],a[152:155],v[148:151],a[48:51]
ds_read_b64_tr_b16 a[136:137],v1 offset:20480
ds_read_b64_tr_b16 a[138:139],v1 offset:25600
v_mfma_f32_16x16x32_bf16 a[48:51],a[156:159],v[156:159],a[48:51]
s_cmp_ge_u32 1,s71
s_cselect_b32 s76,4,2
s_lshl_b32 s76,s76,16
v_mfma_f32_16x16x32_bf16 a[52:55],a[156:159],v[188:191],a[52:55]
s_add_u32 s77,s76,4096
s_add_u32 s78,s76,8192
s_add_u32 s79,s76,12288
v_mfma_f32_16x16x32_bf16 a[52:55],a[152:155],v[180:183],a[52:55]
s_max_i32 s76,s76,s18
buffer_load_dwordx4 v8,s[72:75],s83 offen offset:1024 lds
v_mfma_f32_16x16x32_bf16 a[52:55],a[148:151],v[172:175],a[52:55]
s_max_i32 s77,s77,s19
s_max_i32 s78,s78,s100
s_max_i32 s79,s79,s101
v_mfma_f32_16x16x32_bf16 a[52:55],a[144:147],v[164:167],a[52:55]
v_mfma_f32_16x16x32_bf16 a[56:59],a[144:147],v[196:199],a[56:59]
v_mfma_f32_16x16x32_bf16 a[56:59],a[148:151],v[204:207],a[56:59]
buffer_store_dwordx4 v[252:255],v6,s[36:39],s87 offen offset:3072 sc1
v_mfma_f32_16x16x32_bf16 a[56:59],a[152:155],v[212:215],a[56:59]
v_mfma_f32_16x16x32_bf16 a[56:59],a[156:159],v[220:223],a[56:59]
v_mfma_f32_16x16x32_bf16 a[60:63],a[156:159],v[252:255],a[60:63]
ds_read_b64_tr_b16 a[156:157],v2 offset:30752
ds_read_b64_tr_b16 a[158:159],v2 offset:35872
v_mfma_f32_16x16x32_bf16 a[60:63],a[152:155],v[244:247],a[60:63]
ds_read_b64_tr_b16 a[152:153],v2 offset:20512
v_mfma_f32_16x16x32_bf16 a[60:63],a[148:151],v[236:239],a[60:63]
buffer_load_dwordx4 v9,s[72:75],s83 offen offset:2048 lds
v_mfma_f32_16x16x32_bf16 a[60:63],a[144:147],v[228:231],a[60:63]
ds_read_b64_tr_b16 a[144:145],v2 offset:32
ds_read_b64_tr_b16 a[146:147],v2 offset:5152
v_mfma_f32_16x16x32_bf16 a[64:67],a[160:163],v[132:135],a[64:67]
ds_read_b64_tr_b16 a[148:149],v2 offset:10272
v_mfma_f32_16x16x32_bf16 a[64:67],a[164:167],v[140:143],a[64:67]
ds_read_b64_tr_b16 a[150:151],v2 offset:15392
v_mfma_f32_16x16x32_bf16 a[64:67],a[168:171],v[148:151],a[64:67]
ds_read_b64_tr_b16 a[154:155],v2 offset:25632
v_mfma_f32_16x16x32_bf16 a[64:67],a[172:175],v[156:159],a[64:67]
v_mfma_f32_16x16x32_bf16 a[68:71],a[172:175],v[188:191],a[68:71]
v_mfma_f32_16x16x32_bf16 a[68:71],a[168:171],v[180:183],a[68:71]
v_mfma_f32_16x16x32_bf16 a[68:71],a[164:167],v[172:175],a[68:71]
v_mfma_f32_16x16x32_bf16 a[68:71],a[160:163],v[164:167],a[68:71]
v_mfma_f32_16x16x32_bf16 a[72:75],a[160:163],v[196:199],a[72:75]
v_mfma_f32_16x16x32_bf16 a[72:75],a[164:167],v[204:207],a[72:75]
v_mfma_f32_16x16x32_bf16 a[72:75],a[168:171],v[212:215],a[72:75]
v_mfma_f32_16x16x32_bf16 a[72:75],a[172:175],v[220:223],a[72:75]
buffer_load_dwordx4 v[64:67],v6,s[28:31],s76 offen sc1
v_mfma_f32_16x16x32_bf16 a[76:79],a[172:175],v[252:255],a[76:79]
ds_read_b64_tr_b16 a[172:173],v1 offset:30784
ds_read_b64_tr_b16 a[174:175],v1 offset:35904
v_mfma_f32_16x16x32_bf16 a[76:79],a[168:171],v[244:247],a[76:79]
ds_read_b64_tr_b16 a[168:169],v1 offset:20544
v_mfma_f32_16x16x32_bf16 a[76:79],a[164:167],v[236:239],a[76:79]
ds_read_b64_tr_b16 a[164:165],v1 offset:10304
v_mfma_f32_16x16x32_bf16 a[76:79],a[160:163],v[228:231],a[76:79]
s_waitcnt lgkmcnt(4)
buffer_load_dwordx4 v10,s[72:75],s83 offen offset:3072 lds
v_mfma_f32_16x16x32_bf16 a[80:83],a[176:179],v[132:135],a[80:83]
ds_read_b64_tr_b16 a[160:161],v1 offset:64
ds_read_b64_tr_b16 a[162:163],v1 offset:5184
v_mfma_f32_16x16x32_bf16 a[80:83],a[180:183],v[140:143],a[80:83]
ds_read_b64_tr_b16 a[166:167],v1 offset:15424
v_mfma_f32_16x16x32_bf16 a[80:83],a[184:187],v[148:151],a[80:83]
ds_read_b64_tr_b16 a[170:171],v1 offset:25664
v_mfma_f32_16x16x32_bf16 a[80:83],a[188:191],v[156:159],a[80:83]
buffer_load_dwordx4 v[68:71],v6,s[28:31],s76 offen offset:1024 sc1
v_mfma_f32_16x16x32_bf16 a[84:87],a[188:191],v[188:191],a[84:87]
v_mfma_f32_16x16x32_bf16 a[84:87],a[184:187],v[180:183],a[84:87]
v_mfma_f32_16x16x32_bf16 a[84:87],a[180:183],v[172:175],a[84:87]
v_mfma_f32_16x16x32_bf16 a[84:87],a[176:179],v[164:167],a[84:87]
v_mfma_f32_16x16x32_bf16 a[88:91],a[176:179],v[196:199],a[88:91]
v_mfma_f32_16x16x32_bf16 a[88:91],a[180:183],v[204:207],a[88:91]
s_add_u32 m0,s82,91136
v_mfma_f32_16x16x32_bf16 a[88:91],a[184:187],v[212:215],a[88:91]
v_mfma_f32_16x16x32_bf16 a[88:91],a[188:191],v[220:223],a[88:91]
buffer_load_dwordx4 v[72:75],v6,s[28:31],s76 offen offset:2048 sc1
v_mfma_f32_16x16x32_bf16 a[92:95],a[188:191],v[252:255],a[92:95]
ds_read_b64_tr_b16 a[188:189],v2 offset:30816
ds_read_b64_tr_b16 a[190:191],v2 offset:35936
v_mfma_f32_16x16x32_bf16 a[92:95],a[184:187],v[244:247],a[92:95]
ds_read_b64_tr_b16 a[184:185],v2 offset:20576
v_mfma_f32_16x16x32_bf16 a[92:95],a[180:183],v[236:239],a[92:95]
s_cmp_ge_u32 0,s71
s_cselect_b32 s80,3,1
s_mul_i32 s80,s80,163840
v_mfma_f32_16x16x32_bf16 a[92:95],a[176:179],v[228:231],a[92:95]
s_add_u32 s80,s80,s81
buffer_load_dwordx4 v11,s[72:75],s83 offen lds
s_waitcnt vmcnt(14)
v_mfma_f32_16x16x32_bf16 a[192:195],a[128:131],v[128:131],a[192:195]
s_add_u32 s83,s80,20480
ds_read_b64_tr_b16 a[176:177],v2 offset:96
ds_read_b64_tr_b16 a[178:179],v2 offset:5216
v_mfma_f32_16x16x32_bf16 a[192:195],a[132:135],v[160:163],a[192:195]
ds_read_b64_tr_b16 a[180:181],v2 offset:10336
v_mfma_f32_16x16x32_bf16 a[192:195],a[136:139],v[192:195],a[192:195]
ds_read_b64_tr_b16 a[182:183],v2 offset:15456
v_mfma_f32_16x16x32_bf16 a[192:195],a[140:143],v[224:227],a[192:195]
buffer_load_dwordx4 v[76:79],v6,s[28:31],s76 offen offset:3072 sc1
v_mfma_f32_16x16x32_bf16 a[196:199],a[140:143],v[232:235],a[196:199]
ds_read_b64_tr_b16 a[186:187],v2 offset:25696
v_mfma_f32_16x16x32_bf16 a[196:199],a[136:139],v[200:203],a[196:199]
v_mfma_f32_16x16x32_bf16 a[196:199],a[132:135],v[168:171],a[196:199]
v_mfma_f32_16x16x32_bf16 a[196:199],a[128:131],v[136:139],a[196:199]
v_mfma_f32_16x16x32_bf16 a[200:203],a[128:131],v[144:147],a[200:203]
s_lshl_b32 s95,s42,2
s_add_u32 s95,s95,s40
s_lshl_b32 s95,s95,4
v_mfma_f32_16x16x32_bf16 a[200:203],a[132:135],v[176:179],a[200:203]
v_mov_b32 v18,s68
v_mov_b32 v19,s69
v_mfma_f32_16x16x32_bf16 a[200:203],a[136:139],v[208:211],a[200:203]
v_mfma_f32_16x16x32_bf16 a[200:203],a[140:143],v[240:243],a[200:203]
s_waitcnt vmcnt(10)
buffer_load_dwordx4 v[80:83],v6,s[28:31],s77 offen sc1
v_mfma_f32_16x16x32_bf16 a[204:207],a[140:143],v[248:251],a[204:207]
ds_read_b64_tr_b16 a[140:141],v1 offset:30912
ds_read_b64_tr_b16 a[142:143],v1 offset:36032
v_mfma_f32_16x16x32_bf16 a[204:207],a[136:139],v[216:219],a[204:207]
ds_read_b64_tr_b16 a[136:137],v1 offset:20672
v_mfma_f32_16x16x32_bf16 a[204:207],a[132:135],v[184:187],a[204:207]
ds_read_b64_tr_b16 a[132:133],v1 offset:10432
v_mfma_f32_16x16x32_bf16 a[204:207],a[128:131],v[152:155],a[204:207]
s_barrier
s_waitcnt vmcnt(8)
s_mov_b64 exec,1
buffer_store_dwordx2 v[18:19],v6,s[64:67],s95 offen offset:1024 sc1
s_mov_b64 exec,-1
v_mfma_f32_16x16x32_bf16 a[208:211],a[144:147],v[128:131],a[208:211]
ds_read_b64_tr_b16 a[128:129],v1 offset:192
ds_read_b64_tr_b16 a[130:131],v1 offset:5312
v_mfma_f32_16x16x32_bf16 a[208:211],a[148:151],v[160:163],a[208:211]
ds_read_b64_tr_b16 a[134:135],v1 offset:15552
v_mfma_f32_16x16x32_bf16 a[208:211],a[152:155],v[192:195],a[208:211]
s_add_u32 m0,s82,0
ds_read_b64_tr_b16 a[138:139],v1 offset:25792
v_mfma_f32_16x16x32_bf16 a[208:211],a[156:159],v[224:227],a[208:211]
buffer_load_dwordx4 v[84:87],v6,s[28:31],s77 offen offset:1024 sc1
v_mfma_f32_16x16x32_bf16 a[212:215],a[156:159],v[232:235],a[212:215]
v_mfma_f32_16x16x32_bf16 a[212:215],a[152:155],v[200:203],a[212:215]
v_mfma_f32_16x16x32_bf16 a[212:215],a[148:151],v[168:171],a[212:215]
v_mfma_f32_16x16x32_bf16 a[212:215],a[144:147],v[136:139],a[212:215]
buffer_load_dwordx4 v7,s[72:75],s80 offen lds
v_mfma_f32_16x16x32_bf16 a[216:219],a[144:147],v[144:147],a[216:219]
v_mfma_f32_16x16x32_bf16 a[216:219],a[148:151],v[176:179],a[216:219]
v_mfma_f32_16x16x32_bf16 a[216:219],a[152:155],v[208:211],a[216:219]
v_mfma_f32_16x16x32_bf16 a[216:219],a[156:159],v[240:243],a[216:219]
buffer_load_dwordx4 v[88:91],v6,s[28:31],s77 offen offset:2048 sc1
v_mfma_f32_16x16x32_bf16 a[220:223],a[156:159],v[248:251],a[220:223]
)A"
R"A(ds_read_b64_tr_b16 a[156:157],v2 offset:30944
ds_read_b64_tr_b16 a[158:159],v2 offset:36064
v_mfma_f32_16x16x32_bf16 a[220:223],a[152:155],v[216:219],a[220:223]
ds_read_b64_tr_b16 a[152:153],v2 offset:20704
v_mfma_f32_16x16x32_bf16 a[220:223],a[148:151],v[184:187],a[220:223]
ds_read_b64_tr_b16 a[148:149],v2 offset:10464
v_mfma_f32_16x16x32_bf16 a[220:223],a[144:147],v[152:155],a[220:223]
s_waitcnt lgkmcnt(4)
ds_read_b64_tr_b16 a[144:145],v2 offset:224
v_mfma_f32_16x16x32_bf16 a[224:227],a[160:163],v[128:131],a[224:227]
ds_read_b64_tr_b16 a[146:147],v2 offset:5344
ds_read_b64_tr_b16 a[150:151],v2 offset:15584
v_mfma_f32_16x16x32_bf16 a[224:227],a[164:167],v[160:163],a[224:227]
ds_read_b64_tr_b16 a[154:155],v2 offset:25824
v_mfma_f32_16x16x32_bf16 a[224:227],a[168:171],v[192:195],a[224:227]
buffer_load_dwordx4 v[92:95],v6,s[28:31],s77 offen offset:3072 sc1
v_mfma_f32_16x16x32_bf16 a[224:227],a[172:175],v[224:227],a[224:227]
v_mfma_f32_16x16x32_bf16 a[228:231],a[172:175],v[232:235],a[228:231]
v_mfma_f32_16x16x32_bf16 a[228:231],a[168:171],v[200:203],a[228:231]
v_mfma_f32_16x16x32_bf16 a[228:231],a[164:167],v[168:171],a[228:231]
buffer_load_dwordx4 v8,s[72:75],s80 offen offset:1024 lds
v_mfma_f32_16x16x32_bf16 a[228:231],a[160:163],v[136:139],a[228:231]
v_mfma_f32_16x16x32_bf16 a[232:235],a[160:163],v[144:147],a[232:235]
v_mfma_f32_16x16x32_bf16 a[232:235],a[164:167],v[176:179],a[232:235]
v_mfma_f32_16x16x32_bf16 a[232:235],a[168:171],v[208:211],a[232:235]
buffer_load_dwordx4 v[96:99],v6,s[28:31],s78 offen sc1
v_mfma_f32_16x16x32_bf16 a[232:235],a[172:175],v[240:243],a[232:235]
v_mfma_f32_16x16x32_bf16 a[236:239],a[172:175],v[248:251],a[236:239]
ds_read_b64_tr_b16 a[172:173],v1 offset:30976
ds_read_b64_tr_b16 a[174:175],v1 offset:36096
v_mfma_f32_16x16x32_bf16 a[236:239],a[168:171],v[216:219],a[236:239]
ds_read_b64_tr_b16 a[168:169],v1 offset:20736
v_mfma_f32_16x16x32_bf16 a[236:239],a[164:167],v[184:187],a[236:239]
ds_read_b64_tr_b16 a[164:165],v1 offset:10496
v_mfma_f32_16x16x32_bf16 a[236:239],a[160:163],v[152:155],a[236:239]
ds_read_b64_tr_b16 a[160:161],v1 offset:256
v_mfma_f32_16x16x32_bf16 a[240:243],a[176:179],v[128:131],a[240:243]
ds_read_b64_tr_b16 a[162:163],v1 offset:5376
ds_read_b64_tr_b16 a[166:167],v1 offset:15616
v_mfma_f32_16x16x32_bf16 a[240:243],a[180:183],v[160:163],a[240:243]
ds_read_b64_tr_b16 a[170:171],v1 offset:25856
v_add_u32 v1,81920,v1
v_mfma_f32_16x16x32_bf16 a[240:243],a[184:187],v[192:195],a[240:243]
buffer_load_dwordx4 v[100:103],v6,s[28:31],s78 offen offset:1024 sc1
v_mfma_f32_16x16x32_bf16 a[240:243],a[188:191],v[224:227],a[240:243]
v_mfma_f32_16x16x32_bf16 a[244:247],a[188:191],v[232:235],a[244:247]
v_mfma_f32_16x16x32_bf16 a[244:247],a[184:187],v[200:203],a[244:247]
v_mfma_f32_16x16x32_bf16 a[244:247],a[180:183],v[168:171],a[244:247]
buffer_load_dwordx4 v9,s[72:75],s80 offen offset:2048 lds
v_mfma_f32_16x16x32_bf16 a[244:247],a[176:179],v[136:139],a[244:247]
v_mfma_f32_16x16x32_bf16 a[248:251],a[176:179],v[144:147],a[248:251]
v_mfma_f32_16x16x32_bf16 a[248:251],a[180:183],v[176:179],a[248:251]
v_mfma_f32_16x16x32_bf16 a[248:251],a[184:187],v[208:211],a[248:251]
buffer_load_dwordx4 v[104:107],v6,s[28:31],s78 offen offset:2048 sc1
v_mfma_f32_16x16x32_bf16 a[248:251],a[188:191],v[240:243],a[248:251]
v_mfma_f32_16x16x32_bf16 a[252:255],a[188:191],v[248:251],a[252:255]
ds_read_b64_tr_b16 a[188:189],v2 offset:31008
ds_read_b64_tr_b16 a[190:191],v2 offset:36128
v_mfma_f32_16x16x32_bf16 a[252:255],a[184:187],v[216:219],a[252:255]
ds_read_b64_tr_b16 a[184:185],v2 offset:20768
v_mfma_f32_16x16x32_bf16 a[252:255],a[180:183],v[184:187],a[252:255]
ds_read_b64_tr_b16 a[180:181],v2 offset:10528
v_mfma_f32_16x16x32_bf16 a[252:255],a[176:179],v[152:155],a[252:255]
ds_read_b64_tr_b16 a[176:177],v2 offset:288
v_mfma_f32_16x16x32_bf16 a[0:3],a[96:99],v[128:131],a[0:3]
ds_read_b64_tr_b16 a[178:179],v2 offset:5408
ds_read_b64_tr_b16 a[182:183],v2 offset:15648
v_mfma_f32_16x16x32_bf16 a[0:3],a[100:103],v[160:163],a[0:3]
ds_read_b64_tr_b16 a[186:187],v2 offset:25888
v_add_u32 v2,81920,v2
v_mfma_f32_16x16x32_bf16 a[0:3],a[104:107],v[192:195],a[0:3]
buffer_load_dwordx4 v[108:111],v6,s[28:31],s78 offen offset:3072 sc1
v_mfma_f32_16x16x32_bf16 a[0:3],a[108:111],v[224:227],a[0:3]
v_mfma_f32_16x16x32_bf16 a[4:7],a[108:111],v[232:235],a[4:7]
v_mfma_f32_16x16x32_bf16 a[4:7],a[104:107],v[200:203],a[4:7]
v_mfma_f32_16x16x32_bf16 a[4:7],a[100:103],v[168:171],a[4:7]
buffer_load_dwordx4 v10,s[72:75],s80 offen offset:3072 lds
v_mfma_f32_16x16x32_bf16 a[4:7],a[96:99],v[136:139],a[4:7]
v_mfma_f32_16x16x32_bf16 a[8:11],a[96:99],v[144:147],a[8:11]
v_mfma_f32_16x16x32_bf16 a[8:11],a[100:103],v[176:179],a[8:11]
v_mfma_f32_16x16x32_bf16 a[8:11],a[104:107],v[208:211],a[8:11]
buffer_load_dwordx4 v[112:115],v6,s[28:31],s79 offen sc1
v_mfma_f32_16x16x32_bf16 a[8:11],a[108:111],v[240:243],a[8:11]
v_mfma_f32_16x16x32_bf16 a[12:15],a[108:111],v[248:251],a[12:15]
ds_read_b64_tr_b16 a[108:109],v1 offset:30720
ds_read_b64_tr_b16 a[110:111],v1 offset:35840
v_mfma_f32_16x16x32_bf16 a[12:15],a[104:107],v[216:219],a[12:15]
ds_read_b64_tr_b16 a[104:105],v1 offset:20480
v_mfma_f32_16x16x32_bf16 a[12:15],a[100:103],v[184:187],a[12:15]
s_add_u32 m0,s82,4096
ds_read_b64_tr_b16 a[100:101],v1 offset:10240
v_mfma_f32_16x16x32_bf16 a[12:15],a[96:99],v[152:155],a[12:15]
ds_read_b64_tr_b16 a[96:97],v1
v_mfma_f32_16x16x32_bf16 a[16:19],a[112:115],v[128:131],a[16:19]
ds_read_b64_tr_b16 a[98:99],v1 offset:5120
ds_read_b64_tr_b16 a[102:103],v1 offset:15360
v_mfma_f32_16x16x32_bf16 a[16:19],a[116:119],v[160:163],a[16:19]
buffer_load_dwordx4 v11,s[72:75],s80 offen lds
v_mfma_f32_16x16x32_bf16 a[16:19],a[120:123],v[192:195],a[16:19]
ds_read_b64_tr_b16 a[106:107],v1 offset:25600
v_mfma_f32_16x16x32_bf16 a[16:19],a[124:127],v[224:227],a[16:19]
v_mfma_f32_16x16x32_bf16 a[20:23],a[124:127],v[232:235],a[20:23]
v_mfma_f32_16x16x32_bf16 a[20:23],a[120:123],v[200:203],a[20:23]
buffer_load_dwordx4 v[116:119],v6,s[28:31],s79 offen offset:1024 sc1
v_mfma_f32_16x16x32_bf16 a[20:23],a[116:119],v[168:171],a[20:23]
v_mfma_f32_16x16x32_bf16 a[20:23],a[112:115],v[136:139],a[20:23]
v_mfma_f32_16x16x32_bf16 a[24:27],a[112:115],v[144:147],a[24:27]
v_mfma_f32_16x16x32_bf16 a[24:27],a[116:119],v[176:179],a[24:27]
v_mfma_f32_16x16x32_bf16 a[24:27],a[120:123],v[208:211],a[24:27]
buffer_load_dwordx4 v[120:123],v6,s[28:31],s79 offen offset:2048 sc1
v_mfma_f32_16x16x32_bf16 a[24:27],a[124:127],v[240:243],a[24:27]
v_mfma_f32_16x16x32_bf16 a[28:31],a[124:127],v[248:251],a[28:31]
ds_read_b64_tr_b16 a[124:125],v2 offset:30752
ds_read_b64_tr_b16 a[126:127],v2 offset:35872
v_mfma_f32_16x16x32_bf16 a[28:31],a[120:123],v[216:219],a[28:31]
s_add_u32 m0,s82,5120
ds_read_b64_tr_b16 a[120:121],v2 offset:20512
v_mfma_f32_16x16x32_bf16 a[28:31],a[116:119],v[184:187],a[28:31]
ds_read_b64_tr_b16 a[116:117],v2 offset:10272
v_mfma_f32_16x16x32_bf16 a[28:31],a[112:115],v[152:155],a[28:31]
s_cmp_ge_u32 2,s71
s_cselect_b32 s76,6,4
s_lshl_b32 s76,s76,16
v_mfma_f32_16x16x32_bf16 a[32:35],a[128:131],v[128:131],a[32:35]
s_add_u32 s77,s76,4096
buffer_load_dwordx4 v7,s[72:75],s83 offen lds
v_mfma_f32_16x16x32_bf16 a[32:35],a[132:135],v[160:163],a[32:35]
s_add_u32 s78,s76,8192
s_max_i32 s77,s77,s19
ds_read_b64_tr_b16 a[112:113],v2 offset:32
v_mfma_f32_16x16x32_bf16 a[32:35],a[136:139],v[192:195],a[32:35]
s_max_i32 s78,s78,s100
ds_read_b64_tr_b16 a[114:115],v2 offset:5152
ds_read_b64_tr_b16 a[118:119],v2 offset:15392
v_mfma_f32_16x16x32_bf16 a[32:35],a[140:143],v[224:227],a[32:35]
ds_read_b64_tr_b16 a[122:123],v2 offset:25632
v_mfma_f32_16x16x32_bf16 a[36:39],a[140:143],v[232:235],a[36:39]
buffer_load_dwordx4 v[124:127],v6,s[28:31],s79 offen offset:3072 sc1
v_mfma_f32_16x16x32_bf16 a[36:39],a[136:139],v[200:203],a[36:39]
s_add_u32 s79,s76,12288
s_max_i32 s76,s76,s18
s_max_i32 s79,s79,s101
v_mfma_f32_16x16x32_bf16 a[36:39],a[132:135],v[168:171],a[36:39]
v_mfma_f32_16x16x32_bf16 a[36:39],a[128:131],v[136:139],a[36:39]
v_mfma_f32_16x16x32_bf16 a[40:43],a[128:131],v[144:147],a[40:43]
v_mfma_f32_16x16x32_bf16 a[40:43],a[132:135],v[176:179],a[40:43]
v_mfma_f32_16x16x32_bf16 a[40:43],a[136:139],v[208:211],a[40:43]
v_mfma_f32_16x16x32_bf16 a[40:43],a[140:143],v[240:243],a[40:43]
v_mfma_f32_16x16x32_bf16 a[44:47],a[140:143],v[248:251],a[44:47]
ds_read_b64_tr_b16 a[140:141],v1 offset:30784
ds_read_b64_tr_b16 a[142:143],v1 offset:35904
v_mfma_f32_16x16x32_bf16 a[44:47],a[136:139],v[216:219],a[44:47]
ds_read_b64_tr_b16 a[136:137],v1 offset:20544
v_mfma_f32_16x16x32_bf16 a[44:47],a[132:135],v[184:187],a[44:47]
ds_read_b64_tr_b16 a[132:133],v1 offset:10304
v_mfma_f32_16x16x32_bf16 a[44:47],a[128:131],v[152:155],a[44:47]
s_waitcnt lgkmcnt(4)
buffer_load_dwordx4 v8,s[72:75],s83 offen offset:1024 lds
v_mfma_f32_16x16x32_bf16 a[48:51],a[144:147],v[128:131],a[48:51]
ds_read_b64_tr_b16 a[128:129],v1 offset:64
ds_read_b64_tr_b16 a[130:131],v1 offset:5184
v_mfma_f32_16x16x32_bf16 a[48:51],a[148:151],v[160:163],a[48:51]
ds_read_b64_tr_b16 a[134:135],v1 offset:15424
v_mfma_f32_16x16x32_bf16 a[48:51],a[152:155],v[192:195],a[48:51]
ds_read_b64_tr_b16 a[138:139],v1 offset:25664
v_mfma_f32_16x16x32_bf16 a[48:51],a[156:159],v[224:227],a[48:51]
s_lshl_b32 s94,s70,4
v_bfe_u32 v24,v6,4,2
v_min_u32 v24,2,v24
v_mfma_f32_16x16x32_bf16 a[52:55],a[156:159],v[232:235],a[52:55]
s_add_u32 s94,s94,s40
v_add_u32 v24,s71,v24
v_add_u32 v24,1,v24
v_mfma_f32_16x16x32_bf16 a[52:55],a[152:155],v[200:203],a[52:55]
s_lshl_b32 s94,s94,4
v_and_b32 v24,3,v24
v_lshlrev_b32 v24,6,v24
v_mfma_f32_16x16x32_bf16 a[52:55],a[148:151],v[168:171],a[52:55]
v_mfma_f32_16x16x32_bf16 a[52:55],a[144:147],v[136:139],a[52:55]
buffer_load_dwordx4 v[132:135],v6,s[28:31],s76 offen sc1
v_mfma_f32_16x16x32_bf16 a[56:59],a[144:147],v[144:147],a[56:59]
v_mfma_f32_16x16x32_bf16 a[56:59],a[148:151],v[176:179],a[56:59]
v_mfma_f32_16x16x32_bf16 a[56:59],a[152:155],v[208:211],a[56:59]
v_mfma_f32_16x16x32_bf16 a[56:59],a[156:159],v[240:243],a[56:59]
v_mfma_f32_16x16x32_bf16 a[60:63],a[156:159],v[248:251],a[60:63]
ds_read_b64_tr_b16 a[156:157],v2 offset:30816
ds_read_b64_tr_b16 a[158:159],v2 offset:35936
v_mfma_f32_16x16x32_bf16 a[60:63],a[152:155],v[216:219],a[60:63]
buffer_load_dwordx4 v[140:143],v6,s[28:31],s76 offen offset:1024 sc1
v_mfma_f32_16x16x32_bf16 a[60:63],a[148:151],v[184:187],a[60:63]
ds_read_b64_tr_b16 a[148:149],v2 offset:10336
ds_read_b64_tr_b16 a[150:151],v2 offset:15456
v_mfma_f32_16x16x32_bf16 a[60:63],a[144:147],v[152:155],a[60:63]
ds_read_b64_tr_b16 a[144:145],v2 offset:96
v_mfma_f32_16x16x32_bf16 a[64:67],a[160:163],v[128:131],a[64:67]
ds_read_b64_tr_b16 a[146:147],v2 offset:5216
v_mfma_f32_16x16x32_bf16 a[64:67],a[164:167],v[160:163],a[64:67]
buffer_load_dwordx4 v9,s[72:75],s83 offen offset:2048 lds
v_mfma_f32_16x16x32_bf16 a[64:67],a[168:171],v[192:195],a[64:67]
ds_read_b64_tr_b16 a[152:153],v2 offset:20576
ds_read_b64_tr_b16 a[154:155],v2 offset:25696
v_mfma_f32_16x16x32_bf16 a[64:67],a[172:175],v[224:227],a[64:67]
v_mfma_f32_16x16x32_bf16 a[68:71],a[172:175],v[232:235],a[68:71]
v_mfma_f32_16x16x32_bf16 a[68:71],a[168:171],v[200:203],a[68:71]
buffer_load_dwordx4 v[148:151],v6,s[28:31],s76 offen offset:2048 sc1
v_mfma_f32_16x16x32_bf16 a[68:71],a[164:167],v[168:171],a[68:71]
v_mfma_f32_16x16x32_bf16 a[68:71],a[160:163],v[136:139],a[68:71]
v_mfma_f32_16x16x32_bf16 a[72:75],a[160:163],v[144:147],a[72:75]
v_mfma_f32_16x16x32_bf16 a[72:75],a[164:167],v[176:179],a[72:75]
buffer_load_dwordx4 v[156:159],v6,s[28:31],s76 offen offset:3072 sc1
v_mfma_f32_16x16x32_bf16 a[72:75],a[168:171],v[208:211],a[72:75]
v_mfma_f32_16x16x32_bf16 a[72:75],a[172:175],v[240:243],a[72:75]
v_mfma_f32_16x16x32_bf16 a[76:79],a[172:175],v[248:251],a[76:79]
ds_read_b64_tr_b16 a[172:173],v1 offset:30848
ds_read_b64_tr_b16 a[174:175],v1 offset:35968
v_mfma_f32_16x16x32_bf16 a[76:79],a[168:171],v[216:219],a[76:79]
buffer_load_dwordx4 v10,s[72:75],s83 offen offset:3072 lds
v_mfma_f32_16x16x32_bf16 a[76:79],a[164:167],v[184:187],a[76:79]
ds_read_b64_tr_b16 a[164:165],v1 offset:10368
)A"
R"A(ds_read_b64_tr_b16 a[166:167],v1 offset:15488
v_mfma_f32_16x16x32_bf16 a[76:79],a[160:163],v[152:155],a[76:79]
ds_read_b64_tr_b16 a[160:161],v1 offset:128
v_mfma_f32_16x16x32_bf16 a[80:83],a[176:179],v[128:131],a[80:83]
ds_read_b64_tr_b16 a[162:163],v1 offset:5248
v_mfma_f32_16x16x32_bf16 a[80:83],a[180:183],v[160:163],a[80:83]
buffer_load_dwordx4 v[164:167],v6,s[28:31],s77 offen sc1
v_mfma_f32_16x16x32_bf16 a[80:83],a[184:187],v[192:195],a[80:83]
ds_read_b64_tr_b16 a[168:169],v1 offset:20608
ds_read_b64_tr_b16 a[170:171],v1 offset:25728
v_mfma_f32_16x16x32_bf16 a[80:83],a[188:191],v[224:227],a[80:83]
v_mfma_f32_16x16x32_bf16 a[84:87],a[188:191],v[232:235],a[84:87]
v_mfma_f32_16x16x32_bf16 a[84:87],a[184:187],v[200:203],a[84:87]
buffer_load_dwordx4 v[172:175],v6,s[28:31],s77 offen offset:1024 sc1
v_mfma_f32_16x16x32_bf16 a[84:87],a[180:183],v[168:171],a[84:87]
v_mfma_f32_16x16x32_bf16 a[84:87],a[176:179],v[136:139],a[84:87]
v_mfma_f32_16x16x32_bf16 a[88:91],a[176:179],v[144:147],a[88:91]
v_mfma_f32_16x16x32_bf16 a[88:91],a[180:183],v[176:179],a[88:91]
s_add_u32 m0,s82,9216
v_mfma_f32_16x16x32_bf16 a[88:91],a[184:187],v[208:211],a[88:91]
v_mfma_f32_16x16x32_bf16 a[88:91],a[188:191],v[240:243],a[88:91]
buffer_load_dwordx2 v[26:27],v24,s[64:67],s94 offen offset:1024 sc1
v_mfma_f32_16x16x32_bf16 a[92:95],a[188:191],v[248:251],a[92:95]
ds_read_b64_tr_b16 a[188:189],v2 offset:30880
ds_read_b64_tr_b16 a[190:191],v2 offset:36000
v_mfma_f32_16x16x32_bf16 a[92:95],a[184:187],v[216:219],a[92:95]
ds_read_b64_tr_b16 a[184:185],v2 offset:20640
v_mfma_f32_16x16x32_bf16 a[92:95],a[180:183],v[184:187],a[92:95]
ds_read_b64_tr_b16 a[180:181],v2 offset:10400
v_mfma_f32_16x16x32_bf16 a[92:95],a[176:179],v[152:155],a[92:95]
s_waitcnt vmcnt(13)
buffer_load_dwordx4 v[180:183],v6,s[28:31],s77 offen offset:2048 sc1
v_mfma_f32_16x16x32_bf16 a[192:195],a[96:99],v[64:67],a[192:195]
s_lshl_b32 s43,s41,2
ds_read_b64_tr_b16 a[176:177],v2 offset:160
ds_read_b64_tr_b16 a[178:179],v2 offset:5280
v_mfma_f32_16x16x32_bf16 a[192:195],a[100:103],v[68:71],a[192:195]
s_add_u32 s84,s70,1
s_add_u32 s85,s70,2
ds_read_b64_tr_b16 a[182:183],v2 offset:15520
v_mfma_f32_16x16x32_bf16 a[192:195],a[104:107],v[72:75],a[192:195]
s_add_u32 s86,s70,3
s_add_u32 s43,s43,s71
ds_read_b64_tr_b16 a[186:187],v2 offset:25760
v_mfma_f32_16x16x32_bf16 a[192:195],a[108:111],v[76:79],a[192:195]
s_and_b32 s84,s84,3
buffer_load_dwordx4 v11,s[72:75],s83 offen lds
v_mfma_f32_16x16x32_bf16 a[196:199],a[108:111],v[92:95],a[196:199]
s_and_b32 s85,s85,3
s_and_b32 s86,s86,3
s_mul_i32 s43,s43,245760
v_mfma_f32_16x16x32_bf16 a[196:199],a[104:107],v[88:91],a[196:199]
s_mul_i32 s84,s84,12
s_mul_i32 s85,s85,12
s_mul_i32 s86,s86,12
v_mfma_f32_16x16x32_bf16 a[196:199],a[100:103],v[84:87],a[196:199]
s_add_u32 s43,s43,134217728
s_mov_b64 s[32:33],s[12:13]
s_add_u32 s84,s84,0
v_mfma_f32_16x16x32_bf16 a[196:199],a[96:99],v[80:83],a[196:199]
s_add_u32 s85,s85,4
buffer_load_dwordx4 v[188:191],v6,s[28:31],s77 offen offset:3072 sc1
v_mfma_f32_16x16x32_bf16 a[200:203],a[96:99],v[96:99],a[200:203]
s_add_u32 s86,s86,8
s_add_u32 s84,s84,s40
s_add_u32 s85,s85,s40
v_mfma_f32_16x16x32_bf16 a[200:203],a[100:103],v[100:103],a[200:203]
s_add_u32 s86,s86,s40
s_add_u32 s32,s32,s43
s_addc_u32 s33,s33,0
v_mfma_f32_16x16x32_bf16 a[200:203],a[104:107],v[104:107],a[200:203]
s_mul_i32 s84,s84,5120
s_mul_i32 s85,s85,5120
s_mul_i32 s86,s86,5120
v_mfma_f32_16x16x32_bf16 a[200:203],a[108:111],v[108:111],a[200:203]
s_and_b32 s33,s33,0xffff
buffer_load_dwordx4 v[196:199],v6,s[28:31],s78 offen sc1
s_waitcnt vmcnt(14)
v_mfma_f32_16x16x32_bf16 a[204:207],a[108:111],v[124:127],a[204:207]
s_mov_b32 s34,245760
ds_read_b64_tr_b16 a[108:109],v1 offset:30912
ds_read_b64_tr_b16 a[110:111],v1 offset:36032
v_mfma_f32_16x16x32_bf16 a[204:207],a[104:107],v[120:123],a[204:207]
s_mov_b32 s35,0x20000
s_max_i32 s84,s84,s18
ds_read_b64_tr_b16 a[104:105],v1 offset:20672
v_mfma_f32_16x16x32_bf16 a[204:207],a[100:103],v[116:119],a[204:207]
s_max_i32 s85,s85,s19
s_max_i32 s86,s86,s100
ds_read_b64_tr_b16 a[100:101],v1 offset:10432
v_mfma_f32_16x16x32_bf16 a[204:207],a[96:99],v[112:115],a[204:207]
s_barrier
buffer_load_dwordx4 v[204:207],v6,s[28:31],s78 offen offset:1024 sc1
v_mfma_f32_16x16x32_bf16 a[208:211],a[112:115],v[64:67],a[208:211]
s_mul_i32 s44,s41,307200
ds_read_b64_tr_b16 a[96:97],v1 offset:192
ds_read_b64_tr_b16 a[98:99],v1 offset:5312
v_mfma_f32_16x16x32_bf16 a[208:211],a[116:119],v[68:71],a[208:211]
s_mov_b64 s[20:21],s[16:17]
ds_read_b64_tr_b16 a[102:103],v1 offset:15552
v_bfe_u32 v18,v6,6,2
v_mfma_f32_16x16x32_bf16 a[208:211],a[120:123],v[72:75],a[208:211]
s_lshl_b32 s43,s40,2
ds_read_b64_tr_b16 a[106:107],v1 offset:25792
v_lshrrev_b32 v19,8,v6
v_mfma_f32_16x16x32_bf16 a[208:211],a[124:127],v[76:79],a[208:211]
s_add_u32 s43,s43,s70
buffer_load_dwordx4 v[212:215],v6,s[28:31],s78 offen offset:2048 sc1
v_mfma_f32_16x16x32_bf16 a[212:215],a[124:127],v[92:95],a[212:215]
s_mul_i32 s43,s43,20480
v_xor_b32 v18,v18,v19
v_lshlrev_b32 v18,2,v18
v_mfma_f32_16x16x32_bf16 a[212:215],a[120:123],v[88:91],a[212:215]
s_mov_b32 s22,307200
v_bfe_u32 v19,v6,4,2
v_or_b32 v18,v18,v19
v_mfma_f32_16x16x32_bf16 a[212:215],a[116:119],v[84:87],a[212:215]
s_mov_b32 s23,0x20000
v_and_b32 v19,16,v6
v_mov_b32 v34,0x3f80
v_mfma_f32_16x16x32_bf16 a[212:215],a[112:115],v[80:83],a[212:215]
s_cmp_ge_u32 0,s71
s_cselect_b32 s76,3,1
s_lshl_b32 s76,s76,16
v_mfma_f32_16x16x32_bf16 a[216:219],a[112:115],v[96:99],a[216:219]
s_add_u32 s77,s76,4096
buffer_load_dwordx4 v[220:223],v6,s[28:31],s78 offen offset:3072 sc1
v_mfma_f32_16x16x32_bf16 a[216:219],a[116:119],v[100:103],a[216:219]
s_add_u32 s78,s76,8192
s_max_i32 s77,s77,s19
s_max_i32 s78,s78,s100
v_mfma_f32_16x16x32_bf16 a[216:219],a[120:123],v[104:107],a[216:219]
v_lshlrev_b32 v19,v19,v34
v_lshrrev_b32 v18,1,v18
v_mfma_f32_16x16x32_bf16 a[216:219],a[124:127],v[108:111],a[216:219]
v_cmp_eq_u32 vcc,0,v18
s_nop 1
v_cndmask_b32 v34,0,v19,vcc
v_mfma_f32_16x16x32_bf16 a[220:223],a[124:127],v[124:127],a[220:223]
ds_read_b64_tr_b16 a[124:125],v2 offset:30944
ds_read_b64_tr_b16 a[126:127],v2 offset:36064
v_mfma_f32_16x16x32_bf16 a[220:223],a[120:123],v[120:123],a[220:223]
ds_read_b64_tr_b16 a[120:121],v2 offset:20704
v_add_u32 v25,4096,v6
v_mfma_f32_16x16x32_bf16 a[220:223],a[116:119],v[116:119],a[220:223]
buffer_load_dwordx4 v[228:231],v6,s[28:31],s79 offen sc1
v_mfma_f32_16x16x32_bf16 a[220:223],a[112:115],v[112:115],a[220:223]
s_waitcnt lgkmcnt(3)
ds_read_b64_tr_b16 a[112:113],v2 offset:224
ds_read_b64_tr_b16 a[114:115],v2 offset:5344
v_mfma_f32_16x16x32_bf16 a[224:227],a[128:131],v[64:67],a[224:227]
ds_read_b64_tr_b16 a[116:117],v2 offset:10464
v_mov_b32 v36,0
v_mfma_f32_16x16x32_bf16 a[224:227],a[132:135],v[68:71],a[224:227]
ds_read_b64_tr_b16 a[118:119],v2 offset:15584
v_mov_b32 v37,0
v_mfma_f32_16x16x32_bf16 a[224:227],a[136:139],v[72:75],a[224:227]
ds_read_b64_tr_b16 a[122:123],v2 offset:25824
v_mov_b32 v32,0
v_mfma_f32_16x16x32_bf16 a[224:227],a[140:143],v[76:79],a[224:227]
v_cmp_eq_u32 vcc,1,v18
s_nop 1
v_cndmask_b32 v35,0,v19,vcc
v_mfma_f32_16x16x32_bf16 a[228:231],a[140:143],v[92:95],a[228:231]
buffer_load_dwordx4 v[236:239],v6,s[28:31],s79 offen offset:1024 sc1
v_mfma_f32_16x16x32_bf16 a[228:231],a[136:139],v[88:91],a[228:231]
s_add_u32 s20,s20,s44
s_addc_u32 s21,s21,0
v_mov_b32 v33,0
v_mfma_f32_16x16x32_bf16 a[228:231],a[132:135],v[84:87],a[228:231]
s_mul_i32 s44,s71,320
v_bfe_u32 v40,v6,4,4
v_mul_u32_u24 v40,1280,v40
v_mfma_f32_16x16x32_bf16 a[228:231],a[128:131],v[80:83],a[228:231]
s_waitcnt vmcnt(9)
v_cmp_ne_u32 s[90:91],s68,v26
v_cmp_ne_u32 s[92:93],s69,v27
s_nop 0
s_or_b64 s[90:91],s[90:91],s[92:93]
s_cbranch_scc1 L_fr9_%=
L_fo9_%=:
v_mfma_f32_16x16x32_bf16 a[232:235],a[128:131],v[96:99],a[232:235]
s_and_b32 s21,s21,0xffff
v_lshrrev_b32 v41,8,v6
v_lshl_add_u32 v40,v41,3,v40
v_mfma_f32_16x16x32_bf16 a[232:235],a[132:135],v[100:103],a[232:235]
s_add_u32 s43,s43,s44
v_mfma_f32_16x16x32_bf16 a[232:235],a[136:139],v[104:107],a[232:235]
buffer_load_dwordx4 v[244:247],v6,s[28:31],s79 offen offset:2048 sc1
v_mfma_f32_16x16x32_bf16 a[232:235],a[140:143],v[108:111],a[232:235]
v_mfma_f32_16x16x32_bf16 a[236:239],a[140:143],v[124:127],a[236:239]
ds_read_b64_tr_b16 a[140:141],v1 offset:30976
ds_read_b64_tr_b16 a[142:143],v1 offset:36096
v_mfma_f32_16x16x32_bf16 a[236:239],a[136:139],v[120:123],a[236:239]
ds_read_b64_tr_b16 a[136:137],v1 offset:20736
v_mfma_f32_16x16x32_bf16 a[236:239],a[132:135],v[116:119],a[236:239]
ds_read_b64_tr_b16 a[132:133],v1 offset:10496
v_mfma_f32_16x16x32_bf16 a[236:239],a[128:131],v[112:115],a[236:239]
ds_read_b64_tr_b16 a[128:129],v1 offset:256
v_mfma_f32_16x16x32_bf16 a[240:243],a[144:147],v[64:67],a[240:243]
buffer_load_dwordx4 v[252:255],v6,s[28:31],s79 offen offset:3072 sc1
v_mfma_f32_16x16x32_bf16 a[240:243],a[148:151],v[68:71],a[240:243]
s_add_u32 s79,s76,12288
s_max_i32 s76,s76,s18
ds_read_b64_tr_b16 a[130:131],v1 offset:5376
v_mfma_f32_16x16x32_bf16 a[240:243],a[152:155],v[72:75],a[240:243]
s_max_i32 s79,s79,s101
ds_read_b64_tr_b16 a[134:135],v1 offset:15616
ds_read_b64_tr_b16 a[138:139],v1 offset:25856
v_mfma_f32_16x16x32_bf16 a[240:243],a[156:159],v[76:79],a[240:243]
v_subrev_u32 v1,40960,v1
v_mfma_f32_16x16x32_bf16 a[244:247],a[156:159],v[92:95],a[244:247]
buffer_load_dwordx4 v[128:131],v6,s[28:31],s76 offen sc1
v_mfma_f32_16x16x32_bf16 a[244:247],a[152:155],v[88:91],a[244:247]
v_mfma_f32_16x16x32_bf16 a[244:247],a[148:151],v[84:87],a[244:247]
v_mfma_f32_16x16x32_bf16 a[244:247],a[144:147],v[80:83],a[244:247]
v_mfma_f32_16x16x32_bf16 a[248:251],a[144:147],v[96:99],a[248:251]
buffer_load_dwordx4 v[160:163],v6,s[28:31],s76 offen offset:1024 sc1
v_mfma_f32_16x16x32_bf16 a[248:251],a[148:151],v[100:103],a[248:251]
v_mfma_f32_16x16x32_bf16 a[248:251],a[152:155],v[104:107],a[248:251]
v_mfma_f32_16x16x32_bf16 a[248:251],a[156:159],v[108:111],a[248:251]
v_mfma_f32_16x16x32_bf16 a[252:255],a[156:159],v[124:127],a[252:255]
buffer_load_dwordx4 v[192:195],v6,s[28:31],s76 offen offset:2048 sc1
v_mfma_f32_16x16x32_bf16 a[252:255],a[152:155],v[120:123],a[252:255]
ds_read_b64_tr_b16 a[152:153],v2 offset:20768
ds_read_b64_tr_b16 a[154:155],v2 offset:25888
v_mfma_f32_16x16x32_bf16 a[252:255],a[148:151],v[116:119],a[252:255]
ds_read_b64_tr_b16 a[148:149],v2 offset:10528
v_mfma_f32_16x16x32_bf16 a[252:255],a[144:147],v[112:115],a[252:255]
ds_read_b64_tr_b16 a[144:145],v2 offset:288
v_mfma_f32_16x16x32_bf16 a[0:3],a[160:163],v[64:67],a[0:3]
ds_read_b64_tr_b16 a[146:147],v2 offset:5408
v_mfma_f32_16x16x32_bf16 a[0:3],a[164:167],v[68:71],a[0:3]
buffer_load_dwordx4 v[224:227],v6,s[28:31],s76 offen offset:3072 sc1
v_mfma_f32_16x16x32_bf16 a[0:3],a[168:171],v[72:75],a[0:3]
ds_read_b64_tr_b16 a[150:151],v2 offset:15648
ds_read_b64_tr_b16 a[156:157],v2 offset:31008
v_mfma_f32_16x16x32_bf16 a[0:3],a[172:175],v[76:79],a[0:3]
ds_read_b64_tr_b16 a[158:159],v2 offset:36128
v_subrev_u32 v2,40960,v2
v_mfma_f32_16x16x32_bf16 a[4:7],a[172:175],v[92:95],a[4:7]
v_mfma_f32_16x16x32_bf16 a[4:7],a[168:171],v[88:91],a[4:7]
v_mfma_f32_16x16x32_bf16 a[4:7],a[164:167],v[84:87],a[4:7]
v_mfma_f32_16x16x32_bf16 a[4:7],a[160:163],v[80:83],a[4:7]
buffer_load_dwordx4 v[136:139],v6,s[28:31],s77 offen sc1
v_mfma_f32_16x16x32_bf16 a[8:11],a[160:163],v[96:99],a[8:11]
v_mfma_f32_16x16x32_bf16 a[8:11],a[164:167],v[100:103],a[8:11]
v_mfma_f32_16x16x32_bf16 a[8:11],a[168:171],v[104:107],a[8:11]
v_mfma_f32_16x16x32_bf16 a[8:11],a[172:175],v[108:111],a[8:11]
v_mfma_f32_16x16x32_bf16 a[12:15],a[172:175],v[124:127],a[12:15]
ds_read_b64_tr_b16 a[172:173],v1 offset:30720
ds_read_b64_tr_b16 a[174:175],v1 offset:35840
v_mfma_f32_16x16x32_bf16 a[12:15],a[168:171],v[120:123],a[12:15]
buffer_load_dwordx4 v[168:171],v6,s[28:31],s77 offen offset:1024 sc1
v_mfma_f32_16x16x32_bf16 a[12:15],a[164:167],v[116:119],a[12:15]
ds_read_b64_tr_b16 a[164:165],v1 offset:10240
ds_read_b64_tr_b16 a[166:167],v1 offset:15360
v_mfma_f32_16x16x32_bf16 a[12:15],a[160:163],v[112:115],a[12:15]
ds_read_b64_tr_b16 a[160:161],v1
v_mfma_f32_16x16x32_bf16 a[16:19],a[176:179],v[64:67],a[16:19]
)A"
R"A(ds_read_b64_tr_b16 a[162:163],v1 offset:5120
v_mfma_f32_16x16x32_bf16 a[16:19],a[180:183],v[68:71],a[16:19]
ds_read_b64_tr_b16 a[168:169],v1 offset:20480
v_mfma_f32_16x16x32_bf16 a[16:19],a[184:187],v[72:75],a[16:19]
ds_read_b64_tr_b16 a[170:171],v1 offset:25600
v_mfma_f32_16x16x32_bf16 a[16:19],a[188:191],v[76:79],a[16:19]
buffer_load_dwordx4 v[200:203],v6,s[28:31],s77 offen offset:2048 sc1
v_mfma_f32_16x16x32_bf16 a[20:23],a[188:191],v[92:95],a[20:23]
v_mfma_f32_16x16x32_bf16 a[20:23],a[184:187],v[88:91],a[20:23]
v_mfma_f32_16x16x32_bf16 a[20:23],a[180:183],v[84:87],a[20:23]
v_mfma_f32_16x16x32_bf16 a[20:23],a[176:179],v[80:83],a[20:23]
v_mfma_f32_16x16x32_bf16 a[24:27],a[176:179],v[96:99],a[24:27]
v_mfma_f32_16x16x32_bf16 a[24:27],a[180:183],v[100:103],a[24:27]
buffer_load_dwordx4 v[232:235],v6,s[28:31],s77 offen offset:3072 sc1
v_mfma_f32_16x16x32_bf16 a[24:27],a[184:187],v[104:107],a[24:27]
v_mfma_f32_16x16x32_bf16 a[24:27],a[188:191],v[108:111],a[24:27]
v_mfma_f32_16x16x32_bf16 a[28:31],a[188:191],v[124:127],a[28:31]
ds_read_b64_tr_b16 a[188:189],v2 offset:30752
ds_read_b64_tr_b16 a[190:191],v2 offset:35872
v_mfma_f32_16x16x32_bf16 a[28:31],a[184:187],v[120:123],a[28:31]
ds_read_b64_tr_b16 a[184:185],v2 offset:20512
v_mfma_f32_16x16x32_bf16 a[28:31],a[180:183],v[116:119],a[28:31]
ds_read_b64_tr_b16 a[180:181],v2 offset:10272
v_mfma_f32_16x16x32_bf16 a[28:31],a[176:179],v[112:115],a[28:31]
buffer_load_dwordx4 v[144:147],v6,s[28:31],s78 offen sc1
v_mfma_f32_16x16x32_bf16 a[32:35],a[96:99],v[64:67],a[32:35]
ds_read_b64_tr_b16 a[176:177],v2 offset:32
ds_read_b64_tr_b16 a[178:179],v2 offset:5152
v_mfma_f32_16x16x32_bf16 a[32:35],a[100:103],v[68:71],a[32:35]
ds_read_b64_tr_b16 a[182:183],v2 offset:15392
v_mfma_f32_16x16x32_bf16 a[32:35],a[104:107],v[72:75],a[32:35]
ds_read_b64_tr_b16 a[186:187],v2 offset:25632
v_mfma_f32_16x16x32_bf16 a[32:35],a[108:111],v[76:79],a[32:35]
v_mfma_f32_16x16x32_bf16 a[36:39],a[108:111],v[92:95],a[36:39]
v_mfma_f32_16x16x32_bf16 a[36:39],a[104:107],v[88:91],a[36:39]
buffer_load_dwordx4 v[176:179],v6,s[28:31],s78 offen offset:1024 sc1
v_mfma_f32_16x16x32_bf16 a[36:39],a[100:103],v[84:87],a[36:39]
v_mfma_f32_16x16x32_bf16 a[36:39],a[96:99],v[80:83],a[36:39]
v_mfma_f32_16x16x32_bf16 a[40:43],a[96:99],v[96:99],a[40:43]
v_mfma_f32_16x16x32_bf16 a[40:43],a[100:103],v[100:103],a[40:43]
v_mfma_f32_16x16x32_bf16 a[40:43],a[104:107],v[104:107],a[40:43]
v_mfma_f32_16x16x32_bf16 a[40:43],a[108:111],v[108:111],a[40:43]
buffer_load_dwordx4 v[208:211],v6,s[28:31],s78 offen offset:2048 sc1
v_mfma_f32_16x16x32_bf16 a[44:47],a[108:111],v[124:127],a[44:47]
ds_read_b64_tr_b16 a[108:109],v1 offset:30784
ds_read_b64_tr_b16 a[110:111],v1 offset:35904
v_mfma_f32_16x16x32_bf16 a[44:47],a[104:107],v[120:123],a[44:47]
ds_read_b64_tr_b16 a[104:105],v1 offset:20544
v_mfma_f32_16x16x32_bf16 a[44:47],a[100:103],v[116:119],a[44:47]
ds_read_b64_tr_b16 a[100:101],v1 offset:10304
v_mfma_f32_16x16x32_bf16 a[44:47],a[96:99],v[112:115],a[44:47]
s_waitcnt lgkmcnt(4)
ds_read_b64_tr_b16 a[96:97],v1 offset:64
v_mfma_f32_16x16x32_bf16 a[48:51],a[112:115],v[64:67],a[48:51]
ds_read_b64_tr_b16 a[98:99],v1 offset:5184
ds_read_b64_tr_b16 a[102:103],v1 offset:15424
v_mfma_f32_16x16x32_bf16 a[48:51],a[116:119],v[68:71],a[48:51]
buffer_load_dwordx4 v[240:243],v6,s[28:31],s78 offen offset:3072 sc1
v_mfma_f32_16x16x32_bf16 a[48:51],a[120:123],v[72:75],a[48:51]
ds_read_b64_tr_b16 a[106:107],v1 offset:25664
v_mfma_f32_16x16x32_bf16 a[48:51],a[124:127],v[76:79],a[48:51]
v_mfma_f32_16x16x32_bf16 a[52:55],a[124:127],v[92:95],a[52:55]
v_mfma_f32_16x16x32_bf16 a[52:55],a[120:123],v[88:91],a[52:55]
v_mfma_f32_16x16x32_bf16 a[52:55],a[116:119],v[84:87],a[52:55]
v_mfma_f32_16x16x32_bf16 a[52:55],a[112:115],v[80:83],a[52:55]
buffer_load_dwordx4 v[152:155],v6,s[28:31],s79 offen sc1
v_mfma_f32_16x16x32_bf16 a[56:59],a[112:115],v[96:99],a[56:59]
v_mfma_f32_16x16x32_bf16 a[56:59],a[116:119],v[100:103],a[56:59]
s_cmp_ge_u32 1,s71
s_cselect_b32 s80,5,3
s_mul_i32 s80,s80,163840
v_mfma_f32_16x16x32_bf16 a[56:59],a[120:123],v[104:107],a[56:59]
s_add_u32 s80,s80,s81
s_add_u32 s83,s80,20480
v_mfma_f32_16x16x32_bf16 a[56:59],a[124:127],v[108:111],a[56:59]
v_mfma_f32_16x16x32_bf16 a[60:63],a[124:127],v[124:127],a[60:63]
ds_read_b64_tr_b16 a[124:125],v2 offset:30816
ds_read_b64_tr_b16 a[126:127],v2 offset:35936
v_mfma_f32_16x16x32_bf16 a[60:63],a[120:123],v[120:123],a[60:63]
buffer_load_dwordx4 v[184:187],v6,s[28:31],s79 offen offset:1024 sc1
v_mfma_f32_16x16x32_bf16 a[60:63],a[116:119],v[116:119],a[60:63]
ds_read_b64_tr_b16 a[116:117],v2 offset:10336
ds_read_b64_tr_b16 a[118:119],v2 offset:15456
v_mfma_f32_16x16x32_bf16 a[60:63],a[112:115],v[112:115],a[60:63]
ds_read_b64_tr_b16 a[112:113],v2 offset:96
v_mfma_f32_16x16x32_bf16 a[64:67],a[128:131],v[64:67],a[64:67]
ds_read_b64_tr_b16 a[114:115],v2 offset:5216
v_mfma_f32_16x16x32_bf16 a[64:67],a[132:135],v[68:71],a[64:67]
ds_read_b64_tr_b16 a[120:121],v2 offset:20576
v_mfma_f32_16x16x32_bf16 a[64:67],a[136:139],v[72:75],a[64:67]
ds_read_b64_tr_b16 a[122:123],v2 offset:25696
v_mfma_f32_16x16x32_bf16 a[64:67],a[140:143],v[76:79],a[64:67]
buffer_load_dwordx4 v[216:219],v6,s[28:31],s79 offen offset:2048 sc1
v_mfma_f32_16x16x32_bf16 a[68:71],a[140:143],v[92:95],a[68:71]
v_mfma_f32_16x16x32_bf16 a[68:71],a[136:139],v[88:91],a[68:71]
v_mfma_f32_16x16x32_bf16 a[68:71],a[132:135],v[84:87],a[68:71]
v_mfma_f32_16x16x32_bf16 a[68:71],a[128:131],v[80:83],a[68:71]
v_mfma_f32_16x16x32_bf16 a[72:75],a[128:131],v[96:99],a[72:75]
v_mfma_f32_16x16x32_bf16 a[72:75],a[132:135],v[100:103],a[72:75]
buffer_load_dwordx4 v[248:251],v6,s[28:31],s79 offen offset:3072 sc1
v_mfma_f32_16x16x32_bf16 a[72:75],a[136:139],v[104:107],a[72:75]
v_mfma_f32_16x16x32_bf16 a[72:75],a[140:143],v[108:111],a[72:75]
v_mfma_f32_16x16x32_bf16 a[76:79],a[140:143],v[124:127],a[76:79]
ds_read_b64_tr_b16 a[140:141],v1 offset:30848
ds_read_b64_tr_b16 a[142:143],v1 offset:35968
v_mfma_f32_16x16x32_bf16 a[76:79],a[136:139],v[120:123],a[76:79]
ds_read_b64_tr_b16 a[136:137],v1 offset:20608
v_mfma_f32_16x16x32_bf16 a[76:79],a[132:135],v[116:119],a[76:79]
s_add_u32 m0,s82,40960
ds_read_b64_tr_b16 a[132:133],v1 offset:10368
v_mfma_f32_16x16x32_bf16 a[76:79],a[128:131],v[112:115],a[76:79]
ds_read_b64_tr_b16 a[128:129],v1 offset:128
v_mfma_f32_16x16x32_bf16 a[80:83],a[144:147],v[64:67],a[80:83]
ds_read_b64_tr_b16 a[130:131],v1 offset:5248
ds_read_b64_tr_b16 a[134:135],v1 offset:15488
v_mfma_f32_16x16x32_bf16 a[80:83],a[148:151],v[68:71],a[80:83]
buffer_load_dwordx4 v7,s[72:75],s80 offen lds
v_mfma_f32_16x16x32_bf16 a[80:83],a[152:155],v[72:75],a[80:83]
ds_read_b64_tr_b16 a[138:139],v1 offset:25728
v_mfma_f32_16x16x32_bf16 a[80:83],a[156:159],v[76:79],a[80:83]
v_mfma_f32_16x16x32_bf16 a[84:87],a[156:159],v[92:95],a[84:87]
v_mfma_f32_16x16x32_bf16 a[84:87],a[152:155],v[88:91],a[84:87]
v_mfma_f32_16x16x32_bf16 a[84:87],a[148:151],v[84:87],a[84:87]
buffer_load_dwordx4 v8,s[72:75],s80 offen offset:1024 lds
v_mfma_f32_16x16x32_bf16 a[84:87],a[144:147],v[80:83],a[84:87]
v_mfma_f32_16x16x32_bf16 a[88:91],a[144:147],v[96:99],a[88:91]
v_mfma_f32_16x16x32_bf16 a[88:91],a[148:151],v[100:103],a[88:91]
v_mfma_f32_16x16x32_bf16 a[88:91],a[152:155],v[104:107],a[88:91]
v_mfma_f32_16x16x32_bf16 a[88:91],a[156:159],v[108:111],a[88:91]
buffer_load_dwordx4 v9,s[72:75],s80 offen offset:2048 lds
v_mfma_f32_16x16x32_bf16 a[92:95],a[156:159],v[124:127],a[92:95]
ds_read_b64_tr_b16 a[156:157],v2 offset:30880
ds_read_b64_tr_b16 a[158:159],v2 offset:36000
v_mfma_f32_16x16x32_bf16 a[92:95],a[152:155],v[120:123],a[92:95]
ds_read_b64_tr_b16 a[152:153],v2 offset:20640
v_mfma_f32_16x16x32_bf16 a[92:95],a[148:151],v[116:119],a[92:95]
ds_read_b64_tr_b16 a[148:149],v2 offset:10400
v_mfma_f32_16x16x32_bf16 a[92:95],a[144:147],v[112:115],a[92:95]
ds_read_b64_tr_b16 a[144:145],v2 offset:160
v_mfma_f32_16x16x32_bf16 a[192:195],a[160:163],v[132:135],a[192:195]
buffer_load_dwordx4 v10,s[72:75],s80 offen offset:3072 lds
v_mfma_f32_16x16x32_bf16 a[192:195],a[164:167],v[140:143],a[192:195]
ds_read_b64_tr_b16 a[146:147],v2 offset:5280
ds_read_b64_tr_b16 a[150:151],v2 offset:15520
v_mfma_f32_16x16x32_bf16 a[192:195],a[168:171],v[148:151],a[192:195]
s_add_u32 m0,s82,45056
ds_read_b64_tr_b16 a[154:155],v2 offset:25760
v_mfma_f32_16x16x32_bf16 a[192:195],a[172:175],v[156:159],a[192:195]
s_waitcnt vmcnt(12)
v_mfma_f32_16x16x32_bf16 a[196:199],a[172:175],v[188:191],a[196:199]
v_mfma_f32_16x16x32_bf16 a[196:199],a[168:171],v[180:183],a[196:199]
buffer_load_dwordx4 v11,s[72:75],s80 offen lds
v_mfma_f32_16x16x32_bf16 a[196:199],a[164:167],v[172:175],a[196:199]
v_mfma_f32_16x16x32_bf16 a[196:199],a[160:163],v[164:167],a[196:199]
s_add_u32 m0,s82,46080
v_mfma_f32_16x16x32_bf16 a[200:203],a[160:163],v[196:199],a[200:203]
v_mfma_f32_16x16x32_bf16 a[200:203],a[164:167],v[204:207],a[200:203]
v_mfma_f32_16x16x32_bf16 a[200:203],a[168:171],v[212:215],a[200:203]
buffer_load_dwordx4 v7,s[72:75],s83 offen lds
v_mfma_f32_16x16x32_bf16 a[200:203],a[172:175],v[220:223],a[200:203]
v_mfma_f32_16x16x32_bf16 a[204:207],a[172:175],v[252:255],a[204:207]
ds_read_b64_tr_b16 a[172:173],v1 offset:30912
ds_read_b64_tr_b16 a[174:175],v1 offset:36032
v_mfma_f32_16x16x32_bf16 a[204:207],a[168:171],v[244:247],a[204:207]
ds_read_b64_tr_b16 a[168:169],v1 offset:20672
v_mfma_f32_16x16x32_bf16 a[204:207],a[164:167],v[236:239],a[204:207]
ds_read_b64_tr_b16 a[164:165],v1 offset:10432
v_mfma_f32_16x16x32_bf16 a[204:207],a[160:163],v[228:231],a[204:207]
s_barrier
buffer_load_dwordx4 v8,s[72:75],s83 offen offset:1024 lds
v_mfma_f32_16x16x32_bf16 a[208:211],a[176:179],v[132:135],a[208:211]
s_cmp_ge_u32 1,s71
s_cselect_b32 s76,5,3
s_lshl_b32 s76,s76,16
v_mfma_f32_16x16x32_bf16 a[208:211],a[180:183],v[140:143],a[208:211]
s_add_u32 s77,s76,4096
s_add_u32 s78,s76,8192
s_add_u32 s79,s76,12288
v_mfma_f32_16x16x32_bf16 a[208:211],a[184:187],v[148:151],a[208:211]
s_max_i32 s76,s76,s18
s_max_i32 s77,s77,s19
s_max_i32 s78,s78,s100
v_mfma_f32_16x16x32_bf16 a[208:211],a[188:191],v[156:159],a[208:211]
s_max_i32 s79,s79,s101
buffer_load_dwordx4 v9,s[72:75],s83 offen offset:2048 lds
v_mfma_f32_16x16x32_bf16 a[212:215],a[188:191],v[188:191],a[212:215]
ds_read_b64_tr_b16 a[160:161],v1 offset:192
ds_read_b64_tr_b16 a[162:163],v1 offset:5312
v_mfma_f32_16x16x32_bf16 a[212:215],a[184:187],v[180:183],a[212:215]
ds_read_b64_tr_b16 a[166:167],v1 offset:15552
v_mfma_f32_16x16x32_bf16 a[212:215],a[180:183],v[172:175],a[212:215]
ds_read_b64_tr_b16 a[170:171],v1 offset:25792
v_mfma_f32_16x16x32_bf16 a[212:215],a[176:179],v[164:167],a[212:215]
v_mfma_f32_16x16x32_bf16 a[216:219],a[176:179],v[196:199],a[216:219]
buffer_load_dwordx4 v10,s[72:75],s83 offen offset:3072 lds
v_mfma_f32_16x16x32_bf16 a[216:219],a[180:183],v[204:207],a[216:219]
v_mfma_f32_16x16x32_bf16 a[216:219],a[184:187],v[212:215],a[216:219]
s_add_u32 m0,s82,50176
v_mfma_f32_16x16x32_bf16 a[216:219],a[188:191],v[220:223],a[216:219]
v_mfma_f32_16x16x32_bf16 a[220:223],a[188:191],v[252:255],a[220:223]
ds_read_b64_tr_b16 a[188:189],v2 offset:30944
ds_read_b64_tr_b16 a[190:191],v2 offset:36064
v_mfma_f32_16x16x32_bf16 a[220:223],a[184:187],v[244:247],a[220:223]
buffer_load_dwordx4 v11,s[72:75],s83 offen lds
v_mfma_f32_16x16x32_bf16 a[220:223],a[180:183],v[236:239],a[220:223]
ds_read_b64_tr_b16 a[180:181],v2 offset:10464
ds_read_b64_tr_b16 a[182:183],v2 offset:15584
v_mfma_f32_16x16x32_bf16 a[220:223],a[176:179],v[228:231],a[220:223]
s_waitcnt lgkmcnt(4)
ds_read_b64_tr_b16 a[176:177],v2 offset:224
v_mfma_f32_16x16x32_bf16 a[224:227],a[96:99],v[132:135],a[224:227]
ds_read_b64_tr_b16 a[178:179],v2 offset:5344
v_mfma_f32_16x16x32_bf16 a[224:227],a[100:103],v[140:143],a[224:227]
ds_read_b64_tr_b16 a[184:185],v2 offset:20704
v_mfma_f32_16x16x32_bf16 a[224:227],a[104:107],v[148:151],a[224:227]
ds_read_b64_tr_b16 a[186:187],v2 offset:25824
v_mfma_f32_16x16x32_bf16 a[224:227],a[108:111],v[156:159],a[224:227]
v_mfma_f32_16x16x32_bf16 a[228:231],a[108:111],v[188:191],a[228:231]
v_mfma_f32_16x16x32_bf16 a[228:231],a[104:107],v[180:183],a[228:231]
)A"
R"A(v_mfma_f32_16x16x32_bf16 a[228:231],a[100:103],v[172:175],a[228:231]
v_mfma_f32_16x16x32_bf16 a[228:231],a[96:99],v[164:167],a[228:231]
buffer_load_dwordx4 v[64:67],v6,s[28:31],s76 offen sc1
v_mfma_f32_16x16x32_bf16 a[232:235],a[96:99],v[196:199],a[232:235]
v_mfma_f32_16x16x32_bf16 a[232:235],a[100:103],v[204:207],a[232:235]
v_mfma_f32_16x16x32_bf16 a[232:235],a[104:107],v[212:215],a[232:235]
v_mfma_f32_16x16x32_bf16 a[232:235],a[108:111],v[220:223],a[232:235]
v_mfma_f32_16x16x32_bf16 a[236:239],a[108:111],v[252:255],a[236:239]
buffer_load_dwordx4 v[68:71],v6,s[28:31],s76 offen offset:1024 sc1
v_mfma_f32_16x16x32_bf16 a[236:239],a[104:107],v[244:247],a[236:239]
ds_read_b64_tr_b16 a[104:105],v1 offset:20736
ds_read_b64_tr_b16 a[106:107],v1 offset:25856
v_mfma_f32_16x16x32_bf16 a[236:239],a[100:103],v[236:239],a[236:239]
ds_read_b64_tr_b16 a[100:101],v1 offset:10496
v_mfma_f32_16x16x32_bf16 a[236:239],a[96:99],v[228:231],a[236:239]
ds_read_b64_tr_b16 a[96:97],v1 offset:256
v_mfma_f32_16x16x32_bf16 a[240:243],a[112:115],v[132:135],a[240:243]
ds_read_b64_tr_b16 a[98:99],v1 offset:5376
v_mfma_f32_16x16x32_bf16 a[240:243],a[116:119],v[140:143],a[240:243]
buffer_load_dwordx4 v[72:75],v6,s[28:31],s76 offen offset:2048 sc1
v_mfma_f32_16x16x32_bf16 a[240:243],a[120:123],v[148:151],a[240:243]
ds_read_b64_tr_b16 a[102:103],v1 offset:15616
ds_read_b64_tr_b16 a[108:109],v1 offset:30976
v_mfma_f32_16x16x32_bf16 a[240:243],a[124:127],v[156:159],a[240:243]
ds_read_b64_tr_b16 a[110:111],v1 offset:36096
v_subrev_u32 v1,81920,v1
v_mfma_f32_16x16x32_bf16 a[244:247],a[124:127],v[188:191],a[244:247]
v_add_u32 v42,40960,v1
v_add_u32 v44,122880,v1
v_mfma_f32_16x16x32_bf16 a[244:247],a[120:123],v[180:183],a[244:247]
v_mfma_f32_16x16x32_bf16 a[244:247],a[116:119],v[172:175],a[244:247]
buffer_load_dwordx4 v[76:79],v6,s[28:31],s76 offen offset:3072 sc1
v_mfma_f32_16x16x32_bf16 a[244:247],a[112:115],v[164:167],a[244:247]
v_mfma_f32_16x16x32_bf16 a[248:251],a[112:115],v[196:199],a[248:251]
v_mfma_f32_16x16x32_bf16 a[248:251],a[116:119],v[204:207],a[248:251]
v_mfma_f32_16x16x32_bf16 a[248:251],a[120:123],v[212:215],a[248:251]
v_mfma_f32_16x16x32_bf16 a[248:251],a[124:127],v[220:223],a[248:251]
buffer_load_dwordx4 v[80:83],v6,s[28:31],s77 offen sc1
v_mfma_f32_16x16x32_bf16 a[252:255],a[124:127],v[252:255],a[252:255]
ds_read_b64_tr_b16 a[124:125],v2 offset:31008
ds_read_b64_tr_b16 a[126:127],v2 offset:36128
v_mfma_f32_16x16x32_bf16 a[252:255],a[120:123],v[244:247],a[252:255]
ds_read_b64_tr_b16 a[120:121],v2 offset:20768
v_mfma_f32_16x16x32_bf16 a[252:255],a[116:119],v[236:239],a[252:255]
ds_read_b64_tr_b16 a[116:117],v2 offset:10528
v_mfma_f32_16x16x32_bf16 a[252:255],a[112:115],v[228:231],a[252:255]
ds_read_b64_tr_b16 a[112:113],v2 offset:288
v_mfma_f32_16x16x32_bf16 a[0:3],a[128:131],v[132:135],a[0:3]
buffer_load_dwordx4 v[84:87],v6,s[28:31],s77 offen offset:1024 sc1
v_mfma_f32_16x16x32_bf16 a[0:3],a[132:135],v[140:143],a[0:3]
ds_read_b64_tr_b16 a[114:115],v2 offset:5408
ds_read_b64_tr_b16 a[118:119],v2 offset:15648
v_mfma_f32_16x16x32_bf16 a[0:3],a[136:139],v[148:151],a[0:3]
ds_read_b64_tr_b16 a[122:123],v2 offset:25888
v_subrev_u32 v2,81920,v2
v_mfma_f32_16x16x32_bf16 a[0:3],a[140:143],v[156:159],a[0:3]
v_add_u32 v43,40960,v2
v_add_u32 v45,122880,v2
v_mfma_f32_16x16x32_bf16 a[4:7],a[140:143],v[188:191],a[4:7]
v_mfma_f32_16x16x32_bf16 a[4:7],a[136:139],v[180:183],a[4:7]
buffer_load_dwordx4 v[88:91],v6,s[28:31],s77 offen offset:2048 sc1
v_mfma_f32_16x16x32_bf16 a[4:7],a[132:135],v[172:175],a[4:7]
v_mfma_f32_16x16x32_bf16 a[4:7],a[128:131],v[164:167],a[4:7]
v_mfma_f32_16x16x32_bf16 a[8:11],a[128:131],v[196:199],a[8:11]
v_mfma_f32_16x16x32_bf16 a[8:11],a[132:135],v[204:207],a[8:11]
v_mfma_f32_16x16x32_bf16 a[8:11],a[136:139],v[212:215],a[8:11]
buffer_load_dwordx4 v[92:95],v6,s[28:31],s77 offen offset:3072 sc1
v_mfma_f32_16x16x32_bf16 a[8:11],a[140:143],v[220:223],a[8:11]
v_mfma_f32_16x16x32_bf16 a[12:15],a[140:143],v[252:255],a[12:15]
ds_read_b64_tr_b16 a[140:141],v1 offset:30720
ds_read_b64_tr_b16 a[142:143],v1 offset:35840
v_mfma_f32_16x16x32_bf16 a[12:15],a[136:139],v[244:247],a[12:15]
ds_read_b64_tr_b16 a[136:137],v1 offset:20480
v_mfma_f32_16x16x32_bf16 a[12:15],a[132:135],v[236:239],a[12:15]
ds_read_b64_tr_b16 a[132:133],v1 offset:10240
v_mfma_f32_16x16x32_bf16 a[12:15],a[128:131],v[228:231],a[12:15]
buffer_load_dwordx4 v[96:99],v6,s[28:31],s78 offen sc1
v_mfma_f32_16x16x32_bf16 a[16:19],a[144:147],v[132:135],a[16:19]
ds_read_b64_tr_b16 a[128:129],v1
ds_read_b64_tr_b16 a[130:131],v1 offset:5120
v_mfma_f32_16x16x32_bf16 a[16:19],a[148:151],v[140:143],a[16:19]
ds_read_b64_tr_b16 a[134:135],v1 offset:15360
v_mfma_f32_16x16x32_bf16 a[16:19],a[152:155],v[148:151],a[16:19]
ds_read_b64_tr_b16 a[138:139],v1 offset:25600
v_mfma_f32_16x16x32_bf16 a[16:19],a[156:159],v[156:159],a[16:19]
v_mfma_f32_16x16x32_bf16 a[20:23],a[156:159],v[188:191],a[20:23]
buffer_load_dwordx4 v[100:103],v6,s[28:31],s78 offen offset:1024 sc1
v_mfma_f32_16x16x32_bf16 a[20:23],a[152:155],v[180:183],a[20:23]
v_mfma_f32_16x16x32_bf16 a[20:23],a[148:151],v[172:175],a[20:23]
v_mfma_f32_16x16x32_bf16 a[20:23],a[144:147],v[164:167],a[20:23]
v_mfma_f32_16x16x32_bf16 a[24:27],a[144:147],v[196:199],a[24:27]
v_mfma_f32_16x16x32_bf16 a[24:27],a[148:151],v[204:207],a[24:27]
buffer_load_dwordx4 v[104:107],v6,s[28:31],s78 offen offset:2048 sc1
v_mfma_f32_16x16x32_bf16 a[24:27],a[152:155],v[212:215],a[24:27]
v_mfma_f32_16x16x32_bf16 a[24:27],a[156:159],v[220:223],a[24:27]
v_mfma_f32_16x16x32_bf16 a[28:31],a[156:159],v[252:255],a[28:31]
ds_read_b64_tr_b16 a[156:157],v2 offset:30752
ds_read_b64_tr_b16 a[158:159],v2 offset:35872
v_mfma_f32_16x16x32_bf16 a[28:31],a[152:155],v[244:247],a[28:31]
ds_read_b64_tr_b16 a[152:153],v2 offset:20512
v_mfma_f32_16x16x32_bf16 a[28:31],a[148:151],v[236:239],a[28:31]
buffer_load_dwordx4 v[108:111],v6,s[28:31],s78 offen offset:3072 sc1
v_mfma_f32_16x16x32_bf16 a[28:31],a[144:147],v[228:231],a[28:31]
s_cmp_ge_u32 2,s71
s_cselect_b32 s80,7,5
s_mul_i32 s80,s80,163840
v_mfma_f32_16x16x32_bf16 a[32:35],a[160:163],v[132:135],a[32:35]
s_add_u32 s80,s80,s81
s_add_u32 s83,s80,20480
ds_read_b64_tr_b16 a[144:145],v2 offset:32
v_mfma_f32_16x16x32_bf16 a[32:35],a[164:167],v[140:143],a[32:35]
ds_read_b64_tr_b16 a[146:147],v2 offset:5152
ds_read_b64_tr_b16 a[148:149],v2 offset:10272
v_mfma_f32_16x16x32_bf16 a[32:35],a[168:171],v[148:151],a[32:35]
ds_read_b64_tr_b16 a[150:151],v2 offset:15392
v_mfma_f32_16x16x32_bf16 a[32:35],a[172:175],v[156:159],a[32:35]
buffer_load_dwordx4 v[112:115],v6,s[28:31],s79 offen sc1
v_mfma_f32_16x16x32_bf16 a[36:39],a[172:175],v[188:191],a[36:39]
ds_read_b64_tr_b16 a[154:155],v2 offset:25632
v_mfma_f32_16x16x32_bf16 a[36:39],a[168:171],v[180:183],a[36:39]
v_mfma_f32_16x16x32_bf16 a[36:39],a[164:167],v[172:175],a[36:39]
v_mfma_f32_16x16x32_bf16 a[36:39],a[160:163],v[164:167],a[36:39]
v_mfma_f32_16x16x32_bf16 a[40:43],a[160:163],v[196:199],a[40:43]
buffer_load_dwordx4 v[116:119],v6,s[28:31],s79 offen offset:1024 sc1
v_mfma_f32_16x16x32_bf16 a[40:43],a[164:167],v[204:207],a[40:43]
v_mfma_f32_16x16x32_bf16 a[40:43],a[168:171],v[212:215],a[40:43]
v_mfma_f32_16x16x32_bf16 a[40:43],a[172:175],v[220:223],a[40:43]
v_mfma_f32_16x16x32_bf16 a[44:47],a[172:175],v[252:255],a[44:47]
ds_read_b64_tr_b16 a[172:173],v1 offset:30784
ds_read_b64_tr_b16 a[174:175],v1 offset:35904
v_mfma_f32_16x16x32_bf16 a[44:47],a[168:171],v[244:247],a[44:47]
buffer_load_dwordx4 v[120:123],v6,s[28:31],s79 offen offset:2048 sc1
v_mfma_f32_16x16x32_bf16 a[44:47],a[164:167],v[236:239],a[44:47]
ds_read_b64_tr_b16 a[164:165],v1 offset:10304
ds_read_b64_tr_b16 a[166:167],v1 offset:15424
v_mfma_f32_16x16x32_bf16 a[44:47],a[160:163],v[228:231],a[44:47]
s_waitcnt lgkmcnt(4)
ds_read_b64_tr_b16 a[160:161],v1 offset:64
v_mfma_f32_16x16x32_bf16 a[48:51],a[176:179],v[132:135],a[48:51]
ds_read_b64_tr_b16 a[162:163],v1 offset:5184
v_mfma_f32_16x16x32_bf16 a[48:51],a[180:183],v[140:143],a[48:51]
ds_read_b64_tr_b16 a[168:169],v1 offset:20544
v_mfma_f32_16x16x32_bf16 a[48:51],a[184:187],v[148:151],a[48:51]
buffer_load_dwordx4 v[124:127],v6,s[28:31],s79 offen offset:3072 sc1
v_mfma_f32_16x16x32_bf16 a[48:51],a[188:191],v[156:159],a[48:51]
ds_read_b64_tr_b16 a[170:171],v1 offset:25664
v_mfma_f32_16x16x32_bf16 a[52:55],a[188:191],v[188:191],a[52:55]
s_add_u32 m0,s82,122880
v_mfma_f32_16x16x32_bf16 a[52:55],a[184:187],v[180:183],a[52:55]
v_mfma_f32_16x16x32_bf16 a[52:55],a[180:183],v[172:175],a[52:55]
v_mfma_f32_16x16x32_bf16 a[52:55],a[176:179],v[164:167],a[52:55]
buffer_load_dwordx4 v7,s[72:75],s80 offen lds
v_mfma_f32_16x16x32_bf16 a[56:59],a[176:179],v[196:199],a[56:59]
v_mfma_f32_16x16x32_bf16 a[56:59],a[180:183],v[204:207],a[56:59]
v_mfma_f32_16x16x32_bf16 a[56:59],a[184:187],v[212:215],a[56:59]
v_mfma_f32_16x16x32_bf16 a[56:59],a[188:191],v[220:223],a[56:59]
v_mfma_f32_16x16x32_bf16 a[60:63],a[188:191],v[252:255],a[60:63]
buffer_load_dwordx4 v8,s[72:75],s80 offen offset:1024 lds
v_mfma_f32_16x16x32_bf16 a[60:63],a[184:187],v[244:247],a[60:63]
ds_read_b64_tr_b16 a[184:185],v2 offset:20576
ds_read_b64_tr_b16 a[186:187],v2 offset:25696
v_mfma_f32_16x16x32_bf16 a[60:63],a[180:183],v[236:239],a[60:63]
ds_read_b64_tr_b16 a[180:181],v2 offset:10336
v_mfma_f32_16x16x32_bf16 a[60:63],a[176:179],v[228:231],a[60:63]
ds_read_b64_tr_b16 a[176:177],v2 offset:96
v_mfma_f32_16x16x32_bf16 a[64:67],a[96:99],v[132:135],a[64:67]
ds_read_b64_tr_b16 a[178:179],v2 offset:5216
v_mfma_f32_16x16x32_bf16 a[64:67],a[100:103],v[140:143],a[64:67]
buffer_load_dwordx4 v9,s[72:75],s80 offen offset:2048 lds
v_mfma_f32_16x16x32_bf16 a[64:67],a[104:107],v[148:151],a[64:67]
ds_read_b64_tr_b16 a[182:183],v2 offset:15456
ds_read_b64_tr_b16 a[188:189],v2 offset:30816
v_mfma_f32_16x16x32_bf16 a[64:67],a[108:111],v[156:159],a[64:67]
ds_read_b64_tr_b16 a[190:191],v2 offset:35936
v_mfma_f32_16x16x32_bf16 a[68:71],a[108:111],v[188:191],a[68:71]
v_mfma_f32_16x16x32_bf16 a[68:71],a[104:107],v[180:183],a[68:71]
v_mfma_f32_16x16x32_bf16 a[68:71],a[100:103],v[172:175],a[68:71]
buffer_load_dwordx4 v10,s[72:75],s80 offen offset:3072 lds
v_mfma_f32_16x16x32_bf16 a[68:71],a[96:99],v[164:167],a[68:71]
v_mfma_f32_16x16x32_bf16 a[72:75],a[96:99],v[196:199],a[72:75]
s_add_u32 m0,s82,126976
v_mfma_f32_16x16x32_bf16 a[72:75],a[100:103],v[204:207],a[72:75]
v_mfma_f32_16x16x32_bf16 a[72:75],a[104:107],v[212:215],a[72:75]
s_cmp_ge_u32 2,s71
s_cselect_b32 s76,7,5
s_lshl_b32 s76,s76,16
v_mfma_f32_16x16x32_bf16 a[72:75],a[108:111],v[220:223],a[72:75]
s_add_u32 s77,s76,4096
buffer_load_dwordx4 v11,s[72:75],s80 offen lds
v_mfma_f32_16x16x32_bf16 a[76:79],a[108:111],v[252:255],a[76:79]
s_add_u32 s78,s76,8192
s_add_u32 s79,s76,12288
s_max_i32 s76,s76,s18
v_mfma_f32_16x16x32_bf16 a[76:79],a[104:107],v[244:247],a[76:79]
s_add_u32 m0,s82,128000
s_max_i32 s77,s77,s19
ds_read_b64_tr_b16 a[104:105],v1 offset:20608
v_mfma_f32_16x16x32_bf16 a[76:79],a[100:103],v[236:239],a[76:79]
s_max_i32 s78,s78,s100
ds_read_b64_tr_b16 a[100:101],v1 offset:10368
ds_read_b64_tr_b16 a[102:103],v1 offset:15488
v_mfma_f32_16x16x32_bf16 a[76:79],a[96:99],v[228:231],a[76:79]
s_max_i32 s79,s79,s101
ds_read_b64_tr_b16 a[96:97],v1 offset:128
v_mfma_f32_16x16x32_bf16 a[80:83],a[112:115],v[132:135],a[80:83]
buffer_load_dwordx4 v7,s[72:75],s83 offen lds
v_mfma_f32_16x16x32_bf16 a[80:83],a[116:119],v[140:143],a[80:83]
ds_read_b64_tr_b16 a[98:99],v1 offset:5248
ds_read_b64_tr_b16 a[106:107],v1 offset:25728
v_mfma_f32_16x16x32_bf16 a[80:83],a[120:123],v[148:151],a[80:83]
ds_read_b64_tr_b16 a[108:109],v1 offset:30848
v_mfma_f32_16x16x32_bf16 a[80:83],a[124:127],v[156:159],a[80:83]
ds_read_b64_tr_b16 a[110:111],v1 offset:35968
v_mfma_f32_16x16x32_bf16 a[84:87],a[124:127],v[188:191],a[84:87]
v_mfma_f32_16x16x32_bf16 a[84:87],a[120:123],v[180:183],a[84:87]
buffer_load_dwordx4 v8,s[72:75],s83 offen offset:1024 lds
v_mfma_f32_16x16x32_bf16 a[84:87],a[116:119],v[172:175],a[84:87]
v_mfma_f32_16x16x32_bf16 a[84:87],a[112:115],v[164:167],a[84:87]
v_mfma_f32_16x16x32_bf16 a[88:91],a[112:115],v[196:199],a[88:91]
)A"
R"A(v_mfma_f32_16x16x32_bf16 a[88:91],a[116:119],v[204:207],a[88:91]
v_mfma_f32_16x16x32_bf16 a[88:91],a[120:123],v[212:215],a[88:91]
buffer_load_dwordx4 v9,s[72:75],s83 offen offset:2048 lds
v_mfma_f32_16x16x32_bf16 a[88:91],a[124:127],v[220:223],a[88:91]
v_mfma_f32_16x16x32_bf16 a[92:95],a[124:127],v[252:255],a[92:95]
ds_read_b64_tr_b16 a[124:125],v2 offset:30880
ds_read_b64_tr_b16 a[126:127],v2 offset:36000
v_mfma_f32_16x16x32_bf16 a[92:95],a[120:123],v[244:247],a[92:95]
ds_read_b64_tr_b16 a[120:121],v2 offset:20640
v_mfma_f32_16x16x32_bf16 a[92:95],a[116:119],v[236:239],a[92:95]
ds_read_b64_tr_b16 a[116:117],v2 offset:10400
v_mfma_f32_16x16x32_bf16 a[92:95],a[112:115],v[228:231],a[92:95]
buffer_load_dwordx4 v10,s[72:75],s83 offen offset:3072 lds
v_mfma_f32_16x16x32_bf16 a[192:195],a[128:131],v[128:131],a[192:195]
ds_read_b64_tr_b16 a[112:113],v2 offset:160
ds_read_b64_tr_b16 a[114:115],v2 offset:5280
v_mfma_f32_16x16x32_bf16 a[192:195],a[132:135],v[160:163],a[192:195]
s_add_u32 m0,s82,132096
ds_read_b64_tr_b16 a[118:119],v2 offset:15520
v_mfma_f32_16x16x32_bf16 a[192:195],a[136:139],v[192:195],a[192:195]
ds_read_b64_tr_b16 a[122:123],v2 offset:25760
v_mfma_f32_16x16x32_bf16 a[192:195],a[140:143],v[224:227],a[192:195]
buffer_load_dwordx4 v[132:135],v6,s[28:31],s76 offen sc1
v_mfma_f32_16x16x32_bf16 a[196:199],a[140:143],v[232:235],a[196:199]
v_mfma_f32_16x16x32_bf16 a[196:199],a[136:139],v[200:203],a[196:199]
v_mfma_f32_16x16x32_bf16 a[196:199],a[132:135],v[168:171],a[196:199]
v_mfma_f32_16x16x32_bf16 a[196:199],a[128:131],v[136:139],a[196:199]
s_waitcnt vmcnt(14)
buffer_load_dwordx4 v11,s[72:75],s83 offen lds
v_mfma_f32_16x16x32_bf16 a[200:203],a[128:131],v[144:147],a[200:203]
ds_read_b64_tr_b16 a[128:129],v1 offset:192
ds_read_b64_tr_b16 a[130:131],v1 offset:5312
v_mfma_f32_16x16x32_bf16 a[200:203],a[132:135],v[176:179],a[200:203]
ds_read_b64_tr_b16 a[132:133],v1 offset:10432
v_mfma_f32_16x16x32_bf16 a[200:203],a[136:139],v[208:211],a[200:203]
ds_read_b64_tr_b16 a[134:135],v1 offset:15552
v_mfma_f32_16x16x32_bf16 a[200:203],a[140:143],v[240:243],a[200:203]
buffer_load_dwordx4 v[140:143],v6,s[28:31],s76 offen offset:1024 sc1
v_mfma_f32_16x16x32_bf16 a[208:211],a[144:147],v[128:131],a[208:211]
ds_read_b64_tr_b16 a[136:137],v1 offset:20672
ds_read_b64_tr_b16 a[138:139],v1 offset:25792
v_mfma_f32_16x16x32_bf16 a[208:211],a[148:151],v[160:163],a[208:211]
ds_read_b64_tr_b16 a[140:141],v1 offset:30912
v_mfma_f32_16x16x32_bf16 a[208:211],a[152:155],v[192:195],a[208:211]
ds_read_b64_tr_b16 a[142:143],v1 offset:36032
v_mfma_f32_16x16x32_bf16 a[208:211],a[156:159],v[224:227],a[208:211]
buffer_load_dwordx4 v[148:151],v6,s[28:31],s76 offen offset:2048 sc1
v_mfma_f32_16x16x32_bf16 a[212:215],a[156:159],v[232:235],a[212:215]
v_mfma_f32_16x16x32_bf16 a[212:215],a[152:155],v[200:203],a[212:215]
v_mfma_f32_16x16x32_bf16 a[212:215],a[148:151],v[168:171],a[212:215]
v_mfma_f32_16x16x32_bf16 a[212:215],a[144:147],v[136:139],a[212:215]
buffer_load_dwordx4 v[156:159],v6,s[28:31],s76 offen offset:3072 sc1
v_mfma_f32_16x16x32_bf16 a[216:219],a[144:147],v[144:147],a[216:219]
s_mul_i32 s76,s70,12
ds_read_b64_tr_b16 a[144:145],v2 offset:224
ds_read_b64_tr_b16 a[146:147],v2 offset:5344
v_mfma_f32_16x16x32_bf16 a[216:219],a[148:151],v[176:179],a[216:219]
s_add_u32 s76,s76,0
s_add_u32 s76,s76,s40
ds_read_b64_tr_b16 a[148:149],v2 offset:10464
v_mfma_f32_16x16x32_bf16 a[216:219],a[152:155],v[208:211],a[216:219]
s_mul_i32 s76,s76,5120
s_max_i32 s76,s76,s101
ds_read_b64_tr_b16 a[150:151],v2 offset:15584
v_mfma_f32_16x16x32_bf16 a[216:219],a[156:159],v[240:243],a[216:219]
s_waitcnt lgkmcnt(4)
buffer_load_dwordx4 v[164:167],v6,s[28:31],s77 offen sc1
v_mfma_f32_16x16x32_bf16 a[224:227],a[160:163],v[128:131],a[224:227]
ds_read_b64_tr_b16 a[152:153],v2 offset:20704
ds_read_b64_tr_b16 a[154:155],v2 offset:25824
v_mfma_f32_16x16x32_bf16 a[224:227],a[164:167],v[160:163],a[224:227]
ds_read_b64_tr_b16 a[156:157],v2 offset:30944
v_mfma_f32_16x16x32_bf16 a[224:227],a[168:171],v[192:195],a[224:227]
ds_read_b64_tr_b16 a[158:159],v2 offset:36064
v_mfma_f32_16x16x32_bf16 a[224:227],a[172:175],v[224:227],a[224:227]
buffer_load_dwordx4 v[172:175],v6,s[28:31],s77 offen offset:1024 sc1
v_mfma_f32_16x16x32_bf16 a[228:231],a[172:175],v[232:235],a[228:231]
v_mfma_f32_16x16x32_bf16 a[228:231],a[168:171],v[200:203],a[228:231]
v_mfma_f32_16x16x32_bf16 a[228:231],a[164:167],v[168:171],a[228:231]
v_mfma_f32_16x16x32_bf16 a[228:231],a[160:163],v[136:139],a[228:231]
v_mfma_f32_16x16x32_bf16 a[232:235],a[160:163],v[144:147],a[232:235]
buffer_load_dwordx4 v[180:183],v6,s[28:31],s77 offen offset:2048 sc1
v_mfma_f32_16x16x32_bf16 a[232:235],a[164:167],v[176:179],a[232:235]
ds_read_b64_tr_b16 a[160:161],v1 offset:256
ds_read_b64_tr_b16 a[162:163],v1 offset:5376
v_mfma_f32_16x16x32_bf16 a[232:235],a[168:171],v[208:211],a[232:235]
ds_read_b64_tr_b16 a[164:165],v1 offset:10496
v_mfma_f32_16x16x32_bf16 a[232:235],a[172:175],v[240:243],a[232:235]
ds_read_b64_tr_b16 a[166:167],v1 offset:15616
v_mfma_f32_16x16x32_bf16 a[240:243],a[176:179],v[128:131],a[240:243]
ds_read_b64_tr_b16 a[168:169],v1 offset:20736
v_mfma_f32_16x16x32_bf16 a[240:243],a[180:183],v[160:163],a[240:243]
buffer_load_dwordx4 v[188:191],v6,s[28:31],s77 offen offset:3072 sc1
v_mfma_f32_16x16x32_bf16 a[240:243],a[184:187],v[192:195],a[240:243]
s_mul_i32 s77,s70,12
ds_read_b64_tr_b16 a[170:171],v1 offset:25856
ds_read_b64_tr_b16 a[172:173],v1 offset:30976
v_mfma_f32_16x16x32_bf16 a[240:243],a[188:191],v[224:227],a[240:243]
s_add_u32 s77,s77,4
s_add_u32 s77,s77,s40
ds_read_b64_tr_b16 a[174:175],v1 offset:36096
v_mfma_f32_16x16x32_bf16 a[244:247],a[188:191],v[232:235],a[244:247]
s_mul_i32 s77,s77,5120
s_max_i32 s77,s77,s101
v_mfma_f32_16x16x32_bf16 a[244:247],a[184:187],v[200:203],a[244:247]
v_mfma_f32_16x16x32_bf16 a[244:247],a[180:183],v[168:171],a[244:247]
buffer_load_dwordx4 v[196:199],v6,s[28:31],s78 offen sc1
v_mfma_f32_16x16x32_bf16 a[244:247],a[176:179],v[136:139],a[244:247]
v_mfma_f32_16x16x32_bf16 a[248:251],a[176:179],v[144:147],a[248:251]
ds_read_b64_tr_b16 a[176:177],v2 offset:288
ds_read_b64_tr_b16 a[178:179],v2 offset:5408
v_mfma_f32_16x16x32_bf16 a[248:251],a[180:183],v[176:179],a[248:251]
ds_read_b64_tr_b16 a[180:181],v2 offset:10528
v_mfma_f32_16x16x32_bf16 a[248:251],a[184:187],v[208:211],a[248:251]
ds_read_b64_tr_b16 a[182:183],v2 offset:15648
v_mfma_f32_16x16x32_bf16 a[248:251],a[188:191],v[240:243],a[248:251]
buffer_load_dwordx4 v[204:207],v6,s[28:31],s78 offen offset:1024 sc1
v_mfma_f32_16x16x32_bf16 a[0:3],a[96:99],v[128:131],a[0:3]
ds_read_b64_tr_b16 a[184:185],v2 offset:20768
ds_read_b64_tr_b16 a[186:187],v2 offset:25888
v_mfma_f32_16x16x32_bf16 a[0:3],a[100:103],v[160:163],a[0:3]
ds_read_b64_tr_b16 a[188:189],v2 offset:31008
v_mfma_f32_16x16x32_bf16 a[0:3],a[104:107],v[192:195],a[0:3]
ds_read_b64_tr_b16 a[190:191],v2 offset:36128
v_mfma_f32_16x16x32_bf16 a[0:3],a[108:111],v[224:227],a[0:3]
v_mfma_f32_16x16x32_bf16 a[4:7],a[108:111],v[232:235],a[4:7]
buffer_load_dwordx4 v[212:215],v6,s[28:31],s78 offen offset:2048 sc1
v_mfma_f32_16x16x32_bf16 a[4:7],a[104:107],v[200:203],a[4:7]
v_mfma_f32_16x16x32_bf16 a[4:7],a[100:103],v[168:171],a[4:7]
v_mfma_f32_16x16x32_bf16 a[4:7],a[96:99],v[136:139],a[4:7]
v_mfma_f32_16x16x32_bf16 a[8:11],a[96:99],v[144:147],a[8:11]
v_mfma_f32_16x16x32_bf16 a[8:11],a[100:103],v[176:179],a[8:11]
buffer_load_dwordx4 v[220:223],v6,s[28:31],s78 offen offset:3072 sc1
v_mfma_f32_16x16x32_bf16 a[8:11],a[104:107],v[208:211],a[8:11]
s_mul_i32 s78,s70,12
s_add_u32 s78,s78,8
s_add_u32 s78,s78,s40
v_mfma_f32_16x16x32_bf16 a[8:11],a[108:111],v[240:243],a[8:11]
s_mul_i32 s78,s78,5120
s_max_i32 s78,s78,s101
v_mfma_f32_16x16x32_bf16 a[16:19],a[112:115],v[128:131],a[16:19]
v_mfma_f32_16x16x32_bf16 a[16:19],a[116:119],v[160:163],a[16:19]
v_mfma_f32_16x16x32_bf16 a[16:19],a[120:123],v[192:195],a[16:19]
buffer_load_dwordx4 v[228:231],v6,s[28:31],s79 offen sc1
v_mfma_f32_16x16x32_bf16 a[16:19],a[124:127],v[224:227],a[16:19]
v_mfma_f32_16x16x32_bf16 a[20:23],a[124:127],v[232:235],a[20:23]
v_mfma_f32_16x16x32_bf16 a[20:23],a[120:123],v[200:203],a[20:23]
v_mfma_f32_16x16x32_bf16 a[20:23],a[116:119],v[168:171],a[20:23]
v_mfma_f32_16x16x32_bf16 a[20:23],a[112:115],v[136:139],a[20:23]
buffer_load_dwordx4 v[236:239],v6,s[28:31],s79 offen offset:1024 sc1
v_mfma_f32_16x16x32_bf16 a[24:27],a[112:115],v[144:147],a[24:27]
v_mfma_f32_16x16x32_bf16 a[24:27],a[116:119],v[176:179],a[24:27]
v_mfma_f32_16x16x32_bf16 a[24:27],a[120:123],v[208:211],a[24:27]
v_mfma_f32_16x16x32_bf16 a[24:27],a[124:127],v[240:243],a[24:27]
v_mfma_f32_16x16x32_bf16 a[32:35],a[128:131],v[128:131],a[32:35]
buffer_load_dwordx4 v[244:247],v6,s[28:31],s79 offen offset:2048 sc1
v_mfma_f32_16x16x32_bf16 a[32:35],a[132:135],v[160:163],a[32:35]
v_mfma_f32_16x16x32_bf16 a[32:35],a[136:139],v[192:195],a[32:35]
v_mfma_f32_16x16x32_bf16 a[32:35],a[140:143],v[224:227],a[32:35]
v_mfma_f32_16x16x32_bf16 a[36:39],a[140:143],v[232:235],a[36:39]
v_mfma_f32_16x16x32_bf16 a[36:39],a[136:139],v[200:203],a[36:39]
buffer_load_dwordx4 v[252:255],v6,s[28:31],s79 offen offset:3072 sc1
v_mfma_f32_16x16x32_bf16 a[36:39],a[132:135],v[168:171],a[36:39]
v_mfma_f32_16x16x32_bf16 a[36:39],a[128:131],v[136:139],a[36:39]
v_mfma_f32_16x16x32_bf16 a[40:43],a[128:131],v[144:147],a[40:43]
v_mfma_f32_16x16x32_bf16 a[40:43],a[132:135],v[176:179],a[40:43]
v_mfma_f32_16x16x32_bf16 a[40:43],a[136:139],v[208:211],a[40:43]
s_waitcnt vmcnt(15)
v_mfma_f32_16x16x32_bf16 a[40:43],a[140:143],v[240:243],a[40:43]
s_barrier
ds_read_b64_tr_b16 a[96:97],v42
ds_read_b64_tr_b16 a[98:99],v42 offset:5120
s_waitcnt lgkmcnt(2)
v_mfma_f32_16x16x32_bf16 a[48:51],a[144:147],v[128:131],a[48:51]
ds_read_b64_tr_b16 a[100:101],v42 offset:10240
v_mfma_f32_16x16x32_bf16 a[48:51],a[148:151],v[160:163],a[48:51]
ds_read_b64_tr_b16 a[102:103],v42 offset:15360
v_mfma_f32_16x16x32_bf16 a[48:51],a[152:155],v[192:195],a[48:51]
ds_read_b64_tr_b16 a[104:105],v42 offset:20480
v_mfma_f32_16x16x32_bf16 a[48:51],a[156:159],v[224:227],a[48:51]
ds_read_b64_tr_b16 a[106:107],v42 offset:25600
ds_read_b64_tr_b16 a[108:109],v42 offset:30720
v_mfma_f32_16x16x32_bf16 a[52:55],a[156:159],v[232:235],a[52:55]
ds_read_b64_tr_b16 a[110:111],v42 offset:35840
v_mfma_f32_16x16x32_bf16 a[52:55],a[152:155],v[200:203],a[52:55]
ds_read_b64_tr_b16 a[112:113],v44
v_mfma_f32_16x16x32_bf16 a[52:55],a[148:151],v[168:171],a[52:55]
ds_read_b64_tr_b16 a[114:115],v44 offset:5120
v_mfma_f32_16x16x32_bf16 a[52:55],a[144:147],v[136:139],a[52:55]
ds_read_b64_tr_b16 a[116:117],v44 offset:10240
ds_read_b64_tr_b16 a[118:119],v44 offset:15360
v_mfma_f32_16x16x32_bf16 a[56:59],a[144:147],v[144:147],a[56:59]
ds_read_b64_tr_b16 a[120:121],v44 offset:20480
v_mfma_f32_16x16x32_bf16 a[56:59],a[148:151],v[176:179],a[56:59]
ds_read_b64_tr_b16 a[122:123],v44 offset:25600
v_mfma_f32_16x16x32_bf16 a[56:59],a[152:155],v[208:211],a[56:59]
ds_read_b64_tr_b16 a[124:125],v44 offset:30720
v_mfma_f32_16x16x32_bf16 a[56:59],a[156:159],v[240:243],a[56:59]
ds_read_b64_tr_b16 a[126:127],v44 offset:35840
ds_read_b64_tr_b16 a[128:129],v43 offset:32
v_mfma_f32_16x16x32_bf16 a[64:67],a[160:163],v[128:131],a[64:67]
ds_read_b64_tr_b16 a[130:131],v43 offset:5152
v_mfma_f32_16x16x32_bf16 a[64:67],a[164:167],v[160:163],a[64:67]
ds_read_b64_tr_b16 a[132:133],v43 offset:10272
v_mfma_f32_16x16x32_bf16 a[64:67],a[168:171],v[192:195],a[64:67]
ds_read_b64_tr_b16 a[134:135],v43 offset:15392
v_mfma_f32_16x16x32_bf16 a[64:67],a[172:175],v[224:227],a[64:67]
ds_read_b64_tr_b16 a[136:137],v43 offset:20512
ds_read_b64_tr_b16 a[138:139],v43 offset:25632
v_mfma_f32_16x16x32_bf16 a[68:71],a[172:175],v[232:235],a[68:71]
ds_read_b64_tr_b16 a[140:141],v43 offset:30752
v_mfma_f32_16x16x32_bf16 a[68:71],a[168:171],v[200:203],a[68:71]
ds_read_b64_tr_b16 a[142:143],v43 offset:35872
v_mfma_f32_16x16x32_bf16 a[68:71],a[164:167],v[168:171],a[68:71]
ds_read_b64_tr_b16 a[144:145],v45 offset:32
v_mfma_f32_16x16x32_bf16 a[68:71],a[160:163],v[136:139],a[68:71]
ds_read_b64_tr_b16 a[146:147],v45 offset:5152
)A"
R"A(ds_read_b64_tr_b16 a[148:149],v45 offset:10272
v_mfma_f32_16x16x32_bf16 a[72:75],a[160:163],v[144:147],a[72:75]
ds_read_b64_tr_b16 a[150:151],v45 offset:15392
v_mfma_f32_16x16x32_bf16 a[72:75],a[164:167],v[176:179],a[72:75]
ds_read_b64_tr_b16 a[152:153],v45 offset:20512
v_mfma_f32_16x16x32_bf16 a[72:75],a[168:171],v[208:211],a[72:75]
ds_read_b64_tr_b16 a[154:155],v45 offset:25632
v_mfma_f32_16x16x32_bf16 a[72:75],a[172:175],v[240:243],a[72:75]
ds_read_b64_tr_b16 a[156:157],v45 offset:30752
ds_read_b64_tr_b16 a[158:159],v45 offset:35872
v_mfma_f32_16x16x32_bf16 a[80:83],a[176:179],v[128:131],a[80:83]
ds_read_b64_tr_b16 a[160:161],v42 offset:64
v_mfma_f32_16x16x32_bf16 a[80:83],a[180:183],v[160:163],a[80:83]
ds_read_b64_tr_b16 a[162:163],v42 offset:5184
v_mfma_f32_16x16x32_bf16 a[80:83],a[184:187],v[192:195],a[80:83]
ds_read_b64_tr_b16 a[164:165],v42 offset:10304
v_mfma_f32_16x16x32_bf16 a[80:83],a[188:191],v[224:227],a[80:83]
ds_read_b64_tr_b16 a[166:167],v42 offset:15424
ds_read_b64_tr_b16 a[168:169],v42 offset:20544
v_mfma_f32_16x16x32_bf16 a[84:87],a[188:191],v[232:235],a[84:87]
ds_read_b64_tr_b16 a[170:171],v42 offset:25664
v_mfma_f32_16x16x32_bf16 a[84:87],a[184:187],v[200:203],a[84:87]
ds_read_b64_tr_b16 a[172:173],v42 offset:30784
v_mfma_f32_16x16x32_bf16 a[84:87],a[180:183],v[168:171],a[84:87]
ds_read_b64_tr_b16 a[174:175],v42 offset:35904
v_mfma_f32_16x16x32_bf16 a[84:87],a[176:179],v[136:139],a[84:87]
v_mfma_f32_16x16x32_bf16 a[88:91],a[176:179],v[144:147],a[88:91]
ds_read_b64_tr_b16 a[176:177],v44 offset:64
ds_read_b64_tr_b16 a[178:179],v44 offset:5184
v_mfma_f32_16x16x32_bf16 a[88:91],a[180:183],v[176:179],a[88:91]
ds_read_b64_tr_b16 a[180:181],v44 offset:10304
v_mfma_f32_16x16x32_bf16 a[88:91],a[184:187],v[208:211],a[88:91]
ds_read_b64_tr_b16 a[182:183],v44 offset:15424
v_mfma_f32_16x16x32_bf16 a[88:91],a[188:191],v[240:243],a[88:91]
s_waitcnt lgkmcnt(7)
ds_read_b64_tr_b16 a[184:185],v44 offset:20544
v_mfma_f32_16x16x32_bf16 a[192:195],a[96:99],v[64:67],a[192:195]
ds_read_b64_tr_b16 a[186:187],v44 offset:25664
ds_read_b64_tr_b16 a[188:189],v44 offset:30784
v_mfma_f32_16x16x32_bf16 a[192:195],a[100:103],v[68:71],a[192:195]
ds_read_b64_tr_b16 a[190:191],v44 offset:35904
v_mfma_f32_16x16x32_bf16 a[192:195],a[104:107],v[72:75],a[192:195]
v_mfma_f32_16x16x32_bf16 a[192:195],a[108:111],v[76:79],a[192:195]
v_mfma_f32_16x16x32_bf16 a[196:199],a[108:111],v[92:95],a[196:199]
v_mfma_f32_16x16x32_bf16 a[196:199],a[104:107],v[88:91],a[196:199]
v_mfma_f32_16x16x32_bf16 a[196:199],a[100:103],v[84:87],a[196:199]
v_mfma_f32_16x16x32_bf16 a[196:199],a[96:99],v[80:83],a[196:199]
v_mfma_f32_16x16x32_bf16 a[200:203],a[96:99],v[96:99],a[200:203]
ds_read_b64_tr_b16 a[96:97],v43 offset:96
ds_read_b64_tr_b16 a[98:99],v43 offset:5216
v_mfma_f32_16x16x32_bf16 a[200:203],a[100:103],v[100:103],a[200:203]
ds_read_b64_tr_b16 a[100:101],v43 offset:10336
v_mfma_f32_16x16x32_bf16 a[200:203],a[104:107],v[104:107],a[200:203]
ds_read_b64_tr_b16 a[102:103],v43 offset:15456
v_mfma_f32_16x16x32_bf16 a[200:203],a[108:111],v[108:111],a[200:203]
ds_read_b64_tr_b16 a[104:105],v43 offset:20576
v_mfma_f32_16x16x32_bf16 a[192:195],a[112:115],v[132:135],a[192:195]
s_waitcnt vmcnt(3)
ds_read_b64_tr_b16 a[106:107],v43 offset:25696
ds_read_b64_tr_b16 a[108:109],v43 offset:30816
v_mfma_f32_16x16x32_bf16 a[192:195],a[116:119],v[140:143],a[192:195]
ds_read_b64_tr_b16 a[110:111],v43 offset:35936
v_mfma_f32_16x16x32_bf16 a[192:195],a[120:123],v[148:151],a[192:195]
v_mfma_f32_16x16x32_bf16 a[192:195],a[124:127],v[156:159],a[192:195]
v_mfma_f32_16x16x32_bf16 a[196:199],a[124:127],v[188:191],a[196:199]
v_mfma_f32_16x16x32_bf16 a[196:199],a[120:123],v[180:183],a[196:199]
v_mfma_f32_16x16x32_bf16 a[196:199],a[116:119],v[172:175],a[196:199]
v_mfma_f32_16x16x32_bf16 a[196:199],a[112:115],v[164:167],a[196:199]
v_mfma_f32_16x16x32_bf16 a[200:203],a[112:115],v[196:199],a[200:203]
ds_read_b64_tr_b16 a[112:113],v45 offset:96
ds_read_b64_tr_b16 a[114:115],v45 offset:5216
v_mfma_f32_16x16x32_bf16 a[200:203],a[116:119],v[204:207],a[200:203]
v_accvgpr_read_b32 v128,a192
v_accvgpr_read_b32 v129,a193
v_mfma_f32_16x16x32_bf16 a[200:203],a[120:123],v[212:215],a[200:203]
v_cvt_pk_bf16_f32 v128,v128,v129
v_accvgpr_read_b32 v130,a194
v_mfma_f32_16x16x32_bf16 a[200:203],a[124:127],v[220:223],a[200:203]
v_accvgpr_read_b32 v131,a195
v_cvt_pk_bf16_f32 v129,v130,v131
v_mfma_f32_16x16x32_bf16 a[208:211],a[128:131],v[64:67],a[208:211]
ds_read_b64_tr_b16 a[116:117],v45 offset:10336
ds_read_b64_tr_b16 a[118:119],v45 offset:15456
v_mfma_f32_16x16x32_bf16 a[208:211],a[132:135],v[68:71],a[208:211]
v_accvgpr_read_b32 v136,a196
v_accvgpr_read_b32 v137,a197
v_mfma_f32_16x16x32_bf16 a[208:211],a[136:139],v[72:75],a[208:211]
v_cvt_pk_bf16_f32 v136,v136,v137
v_accvgpr_read_b32 v138,a198
v_mfma_f32_16x16x32_bf16 a[208:211],a[140:143],v[76:79],a[208:211]
v_accvgpr_read_b32 v139,a199
v_cvt_pk_bf16_f32 v137,v138,v139
v_mfma_f32_16x16x32_bf16 a[212:215],a[140:143],v[92:95],a[212:215]
ds_read_b64_tr_b16 a[120:121],v45 offset:20576
ds_read_b64_tr_b16 a[122:123],v45 offset:25696
v_mfma_f32_16x16x32_bf16 a[212:215],a[136:139],v[88:91],a[212:215]
ds_read_b64_tr_b16 a[124:125],v45 offset:30816
v_accvgpr_read_b32 v144,a200
v_mfma_f32_16x16x32_bf16 a[212:215],a[132:135],v[84:87],a[212:215]
ds_read_b64_tr_b16 a[126:127],v45 offset:35936
v_accvgpr_read_b32 v145,a201
v_mfma_f32_16x16x32_bf16 a[212:215],a[128:131],v[80:83],a[212:215]
v_cvt_pk_bf16_f32 v144,v144,v145
v_accvgpr_read_b32 v146,a202
v_mfma_f32_16x16x32_bf16 a[216:219],a[128:131],v[96:99],a[216:219]
v_accvgpr_read_b32 v147,a203
v_cvt_pk_bf16_f32 v145,v146,v147
v_mfma_f32_16x16x32_bf16 a[216:219],a[132:135],v[100:103],a[216:219]
ds_read_b64_tr_b16 a[128:129],v42 offset:128
ds_read_b64_tr_b16 a[130:131],v42 offset:5248
v_mfma_f32_16x16x32_bf16 a[216:219],a[136:139],v[104:107],a[216:219]
ds_read_b64_tr_b16 a[132:133],v42 offset:10368
v_mfma_f32_16x16x32_bf16 a[216:219],a[140:143],v[108:111],a[216:219]
ds_read_b64_tr_b16 a[134:135],v42 offset:15488
v_mfma_f32_16x16x32_bf16 a[208:211],a[144:147],v[132:135],a[208:211]
ds_read_b64_tr_b16 a[136:137],v42 offset:20608
v_mfma_f32_16x16x32_bf16 a[208:211],a[148:151],v[140:143],a[208:211]
ds_read_b64_tr_b16 a[138:139],v42 offset:25728
ds_read_b64_tr_b16 a[140:141],v42 offset:30848
v_mfma_f32_16x16x32_bf16 a[208:211],a[152:155],v[148:151],a[208:211]
ds_read_b64_tr_b16 a[142:143],v42 offset:35968
v_mfma_f32_16x16x32_bf16 a[208:211],a[156:159],v[156:159],a[208:211]
v_mfma_f32_16x16x32_bf16 a[212:215],a[156:159],v[188:191],a[212:215]
v_mfma_f32_16x16x32_bf16 a[212:215],a[152:155],v[180:183],a[212:215]
v_mfma_f32_16x16x32_bf16 a[212:215],a[148:151],v[172:175],a[212:215]
v_mfma_f32_16x16x32_bf16 a[212:215],a[144:147],v[164:167],a[212:215]
v_mfma_f32_16x16x32_bf16 a[216:219],a[144:147],v[196:199],a[216:219]
ds_read_b64_tr_b16 a[144:145],v44 offset:128
ds_read_b64_tr_b16 a[146:147],v44 offset:5248
v_mfma_f32_16x16x32_bf16 a[216:219],a[148:151],v[204:207],a[216:219]
v_accvgpr_read_b32 v130,a208
v_accvgpr_read_b32 v131,a209
v_mfma_f32_16x16x32_bf16 a[216:219],a[152:155],v[212:215],a[216:219]
v_accvgpr_read_b32 v14,a210
v_accvgpr_read_b32 v15,a211
v_mfma_f32_16x16x32_bf16 a[216:219],a[156:159],v[220:223],a[216:219]
v_cvt_pk_bf16_f32 v130,v130,v131
v_cvt_pk_bf16_f32 v131,v14,v15
v_mfma_f32_16x16x32_bf16 a[224:227],a[160:163],v[64:67],a[224:227]
buffer_store_dwordx4 v[128:131],v6,s[32:35],s84 offen sc1
v_mfma_f32_16x16x32_bf16 a[224:227],a[164:167],v[68:71],a[224:227]
s_waitcnt lgkmcnt(2)
v_accvgpr_read_b32 v138,a212
v_accvgpr_read_b32 v139,a213
v_mfma_f32_16x16x32_bf16 a[224:227],a[168:171],v[72:75],a[224:227]
v_accvgpr_read_b32 v16,a214
v_accvgpr_read_b32 v17,a215
v_mfma_f32_16x16x32_bf16 a[224:227],a[172:175],v[76:79],a[224:227]
v_cvt_pk_bf16_f32 v138,v138,v139
v_cvt_pk_bf16_f32 v139,v16,v17
v_mfma_f32_16x16x32_bf16 a[228:231],a[172:175],v[92:95],a[228:231]
buffer_store_dwordx4 v[136:139],v6,s[32:35],s85 offen sc1
v_mfma_f32_16x16x32_bf16 a[228:231],a[168:171],v[88:91],a[228:231]
v_accvgpr_read_b32 v146,a216
v_accvgpr_read_b32 v147,a217
v_mfma_f32_16x16x32_bf16 a[228:231],a[164:167],v[84:87],a[228:231]
v_accvgpr_read_b32 v12,a218
v_accvgpr_read_b32 v13,a219
v_mfma_f32_16x16x32_bf16 a[228:231],a[160:163],v[80:83],a[228:231]
v_cvt_pk_bf16_f32 v146,v146,v147
v_cvt_pk_bf16_f32 v147,v12,v13
v_mfma_f32_16x16x32_bf16 a[232:235],a[160:163],v[96:99],a[232:235]
buffer_store_dwordx4 v[144:147],v6,s[32:35],s86 offen sc1
v_mfma_f32_16x16x32_bf16 a[232:235],a[164:167],v[100:103],a[232:235]
ds_read_b64_tr_b16 a[148:149],v44 offset:10368
ds_read_b64_tr_b16 a[150:151],v44 offset:15488
v_mfma_f32_16x16x32_bf16 a[232:235],a[168:171],v[104:107],a[232:235]
ds_read_b64_tr_b16 a[152:153],v44 offset:20608
v_mfma_f32_16x16x32_bf16 a[232:235],a[172:175],v[108:111],a[232:235]
ds_read_b64_tr_b16 a[154:155],v44 offset:25728
v_mfma_f32_16x16x32_bf16 a[224:227],a[176:179],v[132:135],a[224:227]
ds_read_b64_tr_b16 a[156:157],v44 offset:30848
v_mfma_f32_16x16x32_bf16 a[224:227],a[180:183],v[140:143],a[224:227]
ds_read_b64_tr_b16 a[158:159],v44 offset:35968
ds_read_b64_tr_b16 a[160:161],v43 offset:160
v_mfma_f32_16x16x32_bf16 a[224:227],a[184:187],v[148:151],a[224:227]
ds_read_b64_tr_b16 a[162:163],v43 offset:5280
v_mfma_f32_16x16x32_bf16 a[224:227],a[188:191],v[156:159],a[224:227]
ds_read_b64_tr_b16 a[164:165],v43 offset:10400
v_mfma_f32_16x16x32_bf16 a[228:231],a[188:191],v[188:191],a[228:231]
ds_read_b64_tr_b16 a[166:167],v43 offset:15520
v_mfma_f32_16x16x32_bf16 a[228:231],a[184:187],v[180:183],a[228:231]
ds_read_b64_tr_b16 a[168:169],v43 offset:20640
ds_read_b64_tr_b16 a[170:171],v43 offset:25760
v_mfma_f32_16x16x32_bf16 a[228:231],a[180:183],v[172:175],a[228:231]
ds_read_b64_tr_b16 a[172:173],v43 offset:30880
v_mfma_f32_16x16x32_bf16 a[228:231],a[176:179],v[164:167],a[228:231]
ds_read_b64_tr_b16 a[174:175],v43 offset:36000
v_mfma_f32_16x16x32_bf16 a[232:235],a[176:179],v[196:199],a[232:235]
ds_read_b64_tr_b16 a[176:177],v45 offset:160
v_mfma_f32_16x16x32_bf16 a[232:235],a[180:183],v[204:207],a[232:235]
v_accvgpr_read_b32 v160,a224
v_accvgpr_read_b32 v161,a225
v_mfma_f32_16x16x32_bf16 a[232:235],a[184:187],v[212:215],a[232:235]
v_cvt_pk_bf16_f32 v160,v160,v161
v_accvgpr_read_b32 v162,a226
v_mfma_f32_16x16x32_bf16 a[232:235],a[188:191],v[220:223],a[232:235]
v_accvgpr_read_b32 v163,a227
v_cvt_pk_bf16_f32 v161,v162,v163
v_mfma_f32_16x16x32_bf16 a[240:243],a[96:99],v[64:67],a[240:243]
ds_read_b64_tr_b16 a[178:179],v45 offset:5280
ds_read_b64_tr_b16 a[180:181],v45 offset:10400
v_mfma_f32_16x16x32_bf16 a[240:243],a[100:103],v[68:71],a[240:243]
v_accvgpr_read_b32 v168,a228
v_accvgpr_read_b32 v169,a229
v_mfma_f32_16x16x32_bf16 a[240:243],a[104:107],v[72:75],a[240:243]
v_cvt_pk_bf16_f32 v168,v168,v169
v_accvgpr_read_b32 v170,a230
v_mfma_f32_16x16x32_bf16 a[240:243],a[108:111],v[76:79],a[240:243]
v_accvgpr_read_b32 v171,a231
v_cvt_pk_bf16_f32 v169,v170,v171
v_mfma_f32_16x16x32_bf16 a[244:247],a[108:111],v[92:95],a[244:247]
ds_read_b64_tr_b16 a[182:183],v45 offset:15520
ds_read_b64_tr_b16 a[184:185],v45 offset:20640
v_mfma_f32_16x16x32_bf16 a[244:247],a[104:107],v[88:91],a[244:247]
ds_read_b64_tr_b16 a[186:187],v45 offset:25760
v_accvgpr_read_b32 v176,a232
v_mfma_f32_16x16x32_bf16 a[244:247],a[100:103],v[84:87],a[244:247]
ds_read_b64_tr_b16 a[188:189],v45 offset:30880
v_accvgpr_read_b32 v177,a233
v_mfma_f32_16x16x32_bf16 a[244:247],a[96:99],v[80:83],a[244:247]
ds_read_b64_tr_b16 a[190:191],v45 offset:36000
v_cvt_pk_bf16_f32 v176,v176,v177
v_mfma_f32_16x16x32_bf16 a[248:251],a[96:99],v[96:99],a[248:251]
v_accvgpr_read_b32 v178,a234
v_accvgpr_read_b32 v179,a235
v_mfma_f32_16x16x32_bf16 a[248:251],a[100:103],v[100:103],a[248:251]
ds_read_b64_tr_b16 a[96:97],v42 offset:192
v_cvt_pk_bf16_f32 v177,v178,v179
v_mfma_f32_16x16x32_bf16 a[248:251],a[104:107],v[104:107],a[248:251]
ds_read_b64_tr_b16 a[98:99],v42 offset:5312
ds_read_b64_tr_b16 a[100:101],v42 offset:10432
v_mfma_f32_16x16x32_bf16 a[248:251],a[108:111],v[108:111],a[248:251]
)A"
R"A(ds_read_b64_tr_b16 a[102:103],v42 offset:15552
v_mfma_f32_16x16x32_bf16 a[240:243],a[112:115],v[132:135],a[240:243]
ds_read_b64_tr_b16 a[104:105],v42 offset:20672
v_mfma_f32_16x16x32_bf16 a[240:243],a[116:119],v[140:143],a[240:243]
ds_read_b64_tr_b16 a[106:107],v42 offset:25792
v_mfma_f32_16x16x32_bf16 a[240:243],a[120:123],v[148:151],a[240:243]
ds_read_b64_tr_b16 a[108:109],v42 offset:30912
ds_read_b64_tr_b16 a[110:111],v42 offset:36032
v_mfma_f32_16x16x32_bf16 a[240:243],a[124:127],v[156:159],a[240:243]
v_mfma_f32_16x16x32_bf16 a[244:247],a[124:127],v[188:191],a[244:247]
v_mfma_f32_16x16x32_bf16 a[244:247],a[120:123],v[180:183],a[244:247]
v_mfma_f32_16x16x32_bf16 a[244:247],a[116:119],v[172:175],a[244:247]
v_mfma_f32_16x16x32_bf16 a[244:247],a[112:115],v[164:167],a[244:247]
v_mfma_f32_16x16x32_bf16 a[248:251],a[112:115],v[196:199],a[248:251]
ds_read_b64_tr_b16 a[112:113],v44 offset:192
ds_read_b64_tr_b16 a[114:115],v44 offset:5312
v_mfma_f32_16x16x32_bf16 a[248:251],a[116:119],v[204:207],a[248:251]
v_accvgpr_read_b32 v162,a240
v_accvgpr_read_b32 v163,a241
v_mfma_f32_16x16x32_bf16 a[248:251],a[120:123],v[212:215],a[248:251]
v_accvgpr_read_b32 v14,a242
v_accvgpr_read_b32 v15,a243
v_mfma_f32_16x16x32_bf16 a[248:251],a[124:127],v[220:223],a[248:251]
v_cvt_pk_bf16_f32 v162,v162,v163
v_cvt_pk_bf16_f32 v163,v14,v15
v_mfma_f32_16x16x32_bf16 a[0:3],a[128:131],v[64:67],a[0:3]
buffer_store_dwordx4 v[160:163],v6,s[32:35],s84 offen offset:1024 sc1
v_mfma_f32_16x16x32_bf16 a[0:3],a[132:135],v[68:71],a[0:3]
v_accvgpr_read_b32 v170,a244
v_accvgpr_read_b32 v171,a245
v_mfma_f32_16x16x32_bf16 a[0:3],a[136:139],v[72:75],a[0:3]
v_accvgpr_read_b32 v16,a246
v_accvgpr_read_b32 v17,a247
v_mfma_f32_16x16x32_bf16 a[0:3],a[140:143],v[76:79],a[0:3]
v_cvt_pk_bf16_f32 v170,v170,v171
v_cvt_pk_bf16_f32 v171,v16,v17
v_mfma_f32_16x16x32_bf16 a[4:7],a[140:143],v[92:95],a[4:7]
buffer_store_dwordx4 v[168:171],v6,s[32:35],s85 offen offset:1024 sc1
v_mfma_f32_16x16x32_bf16 a[4:7],a[136:139],v[88:91],a[4:7]
v_accvgpr_read_b32 v178,a248
v_accvgpr_read_b32 v179,a249
v_mfma_f32_16x16x32_bf16 a[4:7],a[132:135],v[84:87],a[4:7]
v_accvgpr_read_b32 v12,a250
v_accvgpr_read_b32 v13,a251
v_mfma_f32_16x16x32_bf16 a[4:7],a[128:131],v[80:83],a[4:7]
v_cvt_pk_bf16_f32 v178,v178,v179
v_cvt_pk_bf16_f32 v179,v12,v13
v_mfma_f32_16x16x32_bf16 a[8:11],a[128:131],v[96:99],a[8:11]
buffer_store_dwordx4 v[176:179],v6,s[32:35],s86 offen offset:1024 sc1
v_mfma_f32_16x16x32_bf16 a[8:11],a[132:135],v[100:103],a[8:11]
ds_read_b64_tr_b16 a[116:117],v44 offset:10432
ds_read_b64_tr_b16 a[118:119],v44 offset:15552
v_mfma_f32_16x16x32_bf16 a[8:11],a[136:139],v[104:107],a[8:11]
ds_read_b64_tr_b16 a[120:121],v44 offset:20672
v_mfma_f32_16x16x32_bf16 a[8:11],a[140:143],v[108:111],a[8:11]
s_waitcnt lgkmcnt(3)
ds_read_b64_tr_b16 a[122:123],v44 offset:25792
v_mfma_f32_16x16x32_bf16 a[0:3],a[144:147],v[132:135],a[0:3]
ds_read_b64_tr_b16 a[124:125],v44 offset:30912
v_mfma_f32_16x16x32_bf16 a[0:3],a[148:151],v[140:143],a[0:3]
ds_read_b64_tr_b16 a[126:127],v44 offset:36032
ds_read_b64_tr_b16 a[128:129],v43 offset:224
v_mfma_f32_16x16x32_bf16 a[0:3],a[152:155],v[148:151],a[0:3]
ds_read_b64_tr_b16 a[130:131],v43 offset:5344
v_mfma_f32_16x16x32_bf16 a[0:3],a[156:159],v[156:159],a[0:3]
ds_read_b64_tr_b16 a[132:133],v43 offset:10464
v_mfma_f32_16x16x32_bf16 a[4:7],a[156:159],v[188:191],a[4:7]
ds_read_b64_tr_b16 a[134:135],v43 offset:15584
v_mfma_f32_16x16x32_bf16 a[4:7],a[152:155],v[180:183],a[4:7]
ds_read_b64_tr_b16 a[136:137],v43 offset:20704
ds_read_b64_tr_b16 a[138:139],v43 offset:25824
v_mfma_f32_16x16x32_bf16 a[4:7],a[148:151],v[172:175],a[4:7]
ds_read_b64_tr_b16 a[140:141],v43 offset:30944
v_mfma_f32_16x16x32_bf16 a[4:7],a[144:147],v[164:167],a[4:7]
ds_read_b64_tr_b16 a[142:143],v43 offset:36064
v_mfma_f32_16x16x32_bf16 a[8:11],a[144:147],v[196:199],a[8:11]
ds_read_b64_tr_b16 a[144:145],v45 offset:224
v_mfma_f32_16x16x32_bf16 a[8:11],a[148:151],v[204:207],a[8:11]
v_accvgpr_read_b32 v192,a0
v_accvgpr_read_b32 v193,a1
v_mfma_f32_16x16x32_bf16 a[8:11],a[152:155],v[212:215],a[8:11]
v_cvt_pk_bf16_f32 v192,v192,v193
v_accvgpr_read_b32 v194,a2
v_mfma_f32_16x16x32_bf16 a[8:11],a[156:159],v[220:223],a[8:11]
v_accvgpr_read_b32 v195,a3
v_cvt_pk_bf16_f32 v193,v194,v195
v_mfma_f32_16x16x32_bf16 a[16:19],a[160:163],v[64:67],a[16:19]
ds_read_b64_tr_b16 a[146:147],v45 offset:5344
ds_read_b64_tr_b16 a[148:149],v45 offset:10464
v_mfma_f32_16x16x32_bf16 a[16:19],a[164:167],v[68:71],a[16:19]
v_accvgpr_read_b32 v200,a4
v_accvgpr_read_b32 v201,a5
v_mfma_f32_16x16x32_bf16 a[16:19],a[168:171],v[72:75],a[16:19]
v_cvt_pk_bf16_f32 v200,v200,v201
v_accvgpr_read_b32 v202,a6
v_mfma_f32_16x16x32_bf16 a[16:19],a[172:175],v[76:79],a[16:19]
v_accvgpr_read_b32 v203,a7
v_cvt_pk_bf16_f32 v201,v202,v203
v_mfma_f32_16x16x32_bf16 a[20:23],a[172:175],v[92:95],a[20:23]
ds_read_b64_tr_b16 a[150:151],v45 offset:15584
ds_read_b64_tr_b16 a[152:153],v45 offset:20704
v_mfma_f32_16x16x32_bf16 a[20:23],a[168:171],v[88:91],a[20:23]
ds_read_b64_tr_b16 a[154:155],v45 offset:25824
v_accvgpr_read_b32 v208,a8
v_mfma_f32_16x16x32_bf16 a[20:23],a[164:167],v[84:87],a[20:23]
ds_read_b64_tr_b16 a[156:157],v45 offset:30944
v_accvgpr_read_b32 v209,a9
v_mfma_f32_16x16x32_bf16 a[20:23],a[160:163],v[80:83],a[20:23]
ds_read_b64_tr_b16 a[158:159],v45 offset:36064
v_cvt_pk_bf16_f32 v208,v208,v209
v_mfma_f32_16x16x32_bf16 a[24:27],a[160:163],v[96:99],a[24:27]
v_accvgpr_read_b32 v210,a10
v_accvgpr_read_b32 v211,a11
v_mfma_f32_16x16x32_bf16 a[24:27],a[164:167],v[100:103],a[24:27]
ds_read_b64_tr_b16 a[160:161],v42 offset:256
v_cvt_pk_bf16_f32 v209,v210,v211
v_mfma_f32_16x16x32_bf16 a[24:27],a[168:171],v[104:107],a[24:27]
ds_read_b64_tr_b16 a[162:163],v42 offset:5376
ds_read_b64_tr_b16 a[164:165],v42 offset:10496
v_mfma_f32_16x16x32_bf16 a[24:27],a[172:175],v[108:111],a[24:27]
ds_read_b64_tr_b16 a[166:167],v42 offset:15616
v_mfma_f32_16x16x32_bf16 a[16:19],a[176:179],v[132:135],a[16:19]
ds_read_b64_tr_b16 a[168:169],v42 offset:20736
v_mfma_f32_16x16x32_bf16 a[16:19],a[180:183],v[140:143],a[16:19]
ds_read_b64_tr_b16 a[170:171],v42 offset:25856
v_mfma_f32_16x16x32_bf16 a[16:19],a[184:187],v[148:151],a[16:19]
ds_read_b64_tr_b16 a[172:173],v42 offset:30976
ds_read_b64_tr_b16 a[174:175],v42 offset:36096
v_mfma_f32_16x16x32_bf16 a[16:19],a[188:191],v[156:159],a[16:19]
v_mfma_f32_16x16x32_bf16 a[20:23],a[188:191],v[188:191],a[20:23]
v_mfma_f32_16x16x32_bf16 a[20:23],a[184:187],v[180:183],a[20:23]
v_mfma_f32_16x16x32_bf16 a[20:23],a[180:183],v[172:175],a[20:23]
v_mfma_f32_16x16x32_bf16 a[20:23],a[176:179],v[164:167],a[20:23]
v_mfma_f32_16x16x32_bf16 a[24:27],a[176:179],v[196:199],a[24:27]
ds_read_b64_tr_b16 a[176:177],v44 offset:256
ds_read_b64_tr_b16 a[178:179],v44 offset:5376
v_mfma_f32_16x16x32_bf16 a[24:27],a[180:183],v[204:207],a[24:27]
v_accvgpr_read_b32 v194,a16
v_accvgpr_read_b32 v195,a17
v_mfma_f32_16x16x32_bf16 a[24:27],a[184:187],v[212:215],a[24:27]
v_accvgpr_read_b32 v14,a18
v_accvgpr_read_b32 v15,a19
v_mfma_f32_16x16x32_bf16 a[24:27],a[188:191],v[220:223],a[24:27]
v_cvt_pk_bf16_f32 v194,v194,v195
v_cvt_pk_bf16_f32 v195,v14,v15
v_mfma_f32_16x16x32_bf16 a[32:35],a[96:99],v[64:67],a[32:35]
buffer_store_dwordx4 v[192:195],v6,s[32:35],s84 offen offset:2048 sc1
v_mfma_f32_16x16x32_bf16 a[32:35],a[100:103],v[68:71],a[32:35]
v_accvgpr_read_b32 v202,a20
v_accvgpr_read_b32 v203,a21
v_mfma_f32_16x16x32_bf16 a[32:35],a[104:107],v[72:75],a[32:35]
v_accvgpr_read_b32 v16,a22
v_accvgpr_read_b32 v17,a23
v_mfma_f32_16x16x32_bf16 a[32:35],a[108:111],v[76:79],a[32:35]
v_cvt_pk_bf16_f32 v202,v202,v203
v_cvt_pk_bf16_f32 v203,v16,v17
v_mfma_f32_16x16x32_bf16 a[36:39],a[108:111],v[92:95],a[36:39]
buffer_store_dwordx4 v[200:203],v6,s[32:35],s85 offen offset:2048 sc1
v_mfma_f32_16x16x32_bf16 a[36:39],a[104:107],v[88:91],a[36:39]
v_accvgpr_read_b32 v210,a24
v_accvgpr_read_b32 v211,a25
v_mfma_f32_16x16x32_bf16 a[36:39],a[100:103],v[84:87],a[36:39]
v_accvgpr_read_b32 v12,a26
v_accvgpr_read_b32 v13,a27
v_mfma_f32_16x16x32_bf16 a[36:39],a[96:99],v[80:83],a[36:39]
v_cvt_pk_bf16_f32 v210,v210,v211
v_cvt_pk_bf16_f32 v211,v12,v13
v_mfma_f32_16x16x32_bf16 a[40:43],a[96:99],v[96:99],a[40:43]
buffer_store_dwordx4 v[208:211],v6,s[32:35],s86 offen offset:2048 sc1
v_mfma_f32_16x16x32_bf16 a[40:43],a[100:103],v[100:103],a[40:43]
ds_read_b64_tr_b16 a[180:181],v44 offset:10496
ds_read_b64_tr_b16 a[182:183],v44 offset:15616
v_mfma_f32_16x16x32_bf16 a[40:43],a[104:107],v[104:107],a[40:43]
ds_read_b64_tr_b16 a[184:185],v44 offset:20736
v_mfma_f32_16x16x32_bf16 a[40:43],a[108:111],v[108:111],a[40:43]
ds_read_b64_tr_b16 a[186:187],v44 offset:25856
v_mfma_f32_16x16x32_bf16 a[32:35],a[112:115],v[132:135],a[32:35]
s_waitcnt lgkmcnt(4)
ds_read_b64_tr_b16 a[188:189],v44 offset:30976
v_mfma_f32_16x16x32_bf16 a[32:35],a[116:119],v[140:143],a[32:35]
ds_read_b64_tr_b16 a[190:191],v44 offset:36096
ds_read_b64_tr_b16 a[96:97],v43 offset:288
v_mfma_f32_16x16x32_bf16 a[32:35],a[120:123],v[148:151],a[32:35]
ds_read_b64_tr_b16 a[98:99],v43 offset:5408
v_mfma_f32_16x16x32_bf16 a[32:35],a[124:127],v[156:159],a[32:35]
ds_read_b64_tr_b16 a[100:101],v43 offset:10528
v_mfma_f32_16x16x32_bf16 a[36:39],a[124:127],v[188:191],a[36:39]
ds_read_b64_tr_b16 a[102:103],v43 offset:15648
v_mfma_f32_16x16x32_bf16 a[36:39],a[120:123],v[180:183],a[36:39]
ds_read_b64_tr_b16 a[104:105],v43 offset:20768
ds_read_b64_tr_b16 a[106:107],v43 offset:25888
v_mfma_f32_16x16x32_bf16 a[36:39],a[116:119],v[172:175],a[36:39]
ds_read_b64_tr_b16 a[108:109],v43 offset:31008
v_mfma_f32_16x16x32_bf16 a[36:39],a[112:115],v[164:167],a[36:39]
ds_read_b64_tr_b16 a[110:111],v43 offset:36128
v_mfma_f32_16x16x32_bf16 a[40:43],a[112:115],v[196:199],a[40:43]
ds_read_b64_tr_b16 a[112:113],v45 offset:288
v_mfma_f32_16x16x32_bf16 a[40:43],a[116:119],v[204:207],a[40:43]
v_accvgpr_read_b32 v224,a32
v_accvgpr_read_b32 v225,a33
v_mfma_f32_16x16x32_bf16 a[40:43],a[120:123],v[212:215],a[40:43]
v_cvt_pk_bf16_f32 v224,v224,v225
v_accvgpr_read_b32 v226,a34
v_mfma_f32_16x16x32_bf16 a[40:43],a[124:127],v[220:223],a[40:43]
v_accvgpr_read_b32 v227,a35
v_cvt_pk_bf16_f32 v225,v226,v227
v_mfma_f32_16x16x32_bf16 a[48:51],a[128:131],v[64:67],a[48:51]
ds_read_b64_tr_b16 a[114:115],v45 offset:5408
ds_read_b64_tr_b16 a[116:117],v45 offset:10528
v_mfma_f32_16x16x32_bf16 a[48:51],a[132:135],v[68:71],a[48:51]
v_accvgpr_read_b32 v232,a36
v_accvgpr_read_b32 v233,a37
v_mfma_f32_16x16x32_bf16 a[48:51],a[136:139],v[72:75],a[48:51]
v_cvt_pk_bf16_f32 v232,v232,v233
v_accvgpr_read_b32 v234,a38
v_mfma_f32_16x16x32_bf16 a[48:51],a[140:143],v[76:79],a[48:51]
v_accvgpr_read_b32 v235,a39
v_cvt_pk_bf16_f32 v233,v234,v235
v_mfma_f32_16x16x32_bf16 a[52:55],a[140:143],v[92:95],a[52:55]
ds_read_b64_tr_b16 a[118:119],v45 offset:15648
ds_read_b64_tr_b16 a[120:121],v45 offset:20768
v_mfma_f32_16x16x32_bf16 a[52:55],a[136:139],v[88:91],a[52:55]
v_accvgpr_read_b32 v240,a40
v_accvgpr_read_b32 v241,a41
v_mfma_f32_16x16x32_bf16 a[52:55],a[132:135],v[84:87],a[52:55]
v_cvt_pk_bf16_f32 v240,v240,v241
v_accvgpr_read_b32 v242,a42
v_mfma_f32_16x16x32_bf16 a[52:55],a[128:131],v[80:83],a[52:55]
v_accvgpr_read_b32 v243,a43
v_cvt_pk_bf16_f32 v241,v242,v243
v_mfma_f32_16x16x32_bf16 a[56:59],a[128:131],v[96:99],a[56:59]
ds_read_b64_tr_b16 a[122:123],v45 offset:25888
ds_read_b64_tr_b16 a[124:125],v45 offset:31008
v_mfma_f32_16x16x32_bf16 a[56:59],a[132:135],v[100:103],a[56:59]
ds_read_b64_tr_b16 a[126:127],v45 offset:36128
v_mfma_f32_16x16x32_bf16 a[56:59],a[136:139],v[104:107],a[56:59]
ds_read_b64_tr_b16 a[128:129],v1
v_mfma_f32_16x16x32_bf16 a[56:59],a[140:143],v[108:111],a[56:59]
ds_read_b64_tr_b16 a[130:131],v1 offset:5120
v_mfma_f32_16x16x32_bf16 a[48:51],a[144:147],v[132:135],a[48:51]
ds_read_b64_tr_b16 a[132:133],v1 offset:10240
ds_read_b64_tr_b16 a[134:135],v1 offset:15360
v_mfma_f32_16x16x32_bf16 a[48:51],a[148:151],v[140:143],a[48:51]
ds_read_b64_tr_b16 a[136:137],v1 offset:20480
v_mfma_f32_16x16x32_bf16 a[48:51],a[152:155],v[148:151],a[48:51]
ds_read_b64_tr_b16 a[138:139],v1 offset:25600
)A"
R"A(v_mfma_f32_16x16x32_bf16 a[48:51],a[156:159],v[156:159],a[48:51]
ds_read_b64_tr_b16 a[140:141],v1 offset:30720
v_mfma_f32_16x16x32_bf16 a[52:55],a[156:159],v[188:191],a[52:55]
ds_read_b64_tr_b16 a[142:143],v1 offset:35840
v_mfma_f32_16x16x32_bf16 a[52:55],a[152:155],v[180:183],a[52:55]
v_mfma_f32_16x16x32_bf16 a[52:55],a[148:151],v[172:175],a[52:55]
v_mfma_f32_16x16x32_bf16 a[52:55],a[144:147],v[164:167],a[52:55]
v_mfma_f32_16x16x32_bf16 a[56:59],a[144:147],v[196:199],a[56:59]
ds_read_b64_tr_b16 a[144:145],v42
ds_read_b64_tr_b16 a[146:147],v42 offset:5120
v_mfma_f32_16x16x32_bf16 a[56:59],a[148:151],v[204:207],a[56:59]
v_accvgpr_read_b32 v226,a48
v_accvgpr_read_b32 v227,a49
v_mfma_f32_16x16x32_bf16 a[56:59],a[152:155],v[212:215],a[56:59]
v_accvgpr_read_b32 v14,a50
v_accvgpr_read_b32 v15,a51
v_mfma_f32_16x16x32_bf16 a[56:59],a[156:159],v[220:223],a[56:59]
v_cvt_pk_bf16_f32 v226,v226,v227
v_cvt_pk_bf16_f32 v227,v14,v15
v_mfma_f32_16x16x32_bf16 a[64:67],a[160:163],v[64:67],a[64:67]
buffer_store_dwordx4 v[224:227],v6,s[32:35],s84 offen offset:3072 sc1
v_mfma_f32_16x16x32_bf16 a[64:67],a[164:167],v[68:71],a[64:67]
v_accvgpr_read_b32 v234,a52
v_accvgpr_read_b32 v235,a53
v_mfma_f32_16x16x32_bf16 a[64:67],a[168:171],v[72:75],a[64:67]
v_accvgpr_read_b32 v16,a54
v_accvgpr_read_b32 v17,a55
v_mfma_f32_16x16x32_bf16 a[64:67],a[172:175],v[76:79],a[64:67]
v_cvt_pk_bf16_f32 v234,v234,v235
v_cvt_pk_bf16_f32 v235,v16,v17
v_mfma_f32_16x16x32_bf16 a[68:71],a[172:175],v[92:95],a[68:71]
buffer_store_dwordx4 v[232:235],v6,s[32:35],s85 offen offset:3072 sc1
v_mfma_f32_16x16x32_bf16 a[68:71],a[168:171],v[88:91],a[68:71]
v_accvgpr_read_b32 v242,a56
v_accvgpr_read_b32 v243,a57
v_mfma_f32_16x16x32_bf16 a[68:71],a[164:167],v[84:87],a[68:71]
v_accvgpr_read_b32 v12,a58
v_accvgpr_read_b32 v13,a59
v_mfma_f32_16x16x32_bf16 a[68:71],a[160:163],v[80:83],a[68:71]
v_cvt_pk_bf16_f32 v242,v242,v243
v_cvt_pk_bf16_f32 v243,v12,v13
v_mfma_f32_16x16x32_bf16 a[72:75],a[160:163],v[96:99],a[72:75]
buffer_store_dwordx4 v[240:243],v6,s[32:35],s86 offen offset:3072 sc1
v_mfma_f32_16x16x32_bf16 a[72:75],a[164:167],v[100:103],a[72:75]
ds_read_b64_tr_b16 a[148:149],v42 offset:10240
ds_read_b64_tr_b16 a[150:151],v42 offset:15360
v_mfma_f32_16x16x32_bf16 a[72:75],a[168:171],v[104:107],a[72:75]
ds_read_b64_tr_b16 a[152:153],v42 offset:20480
v_mfma_f32_16x16x32_bf16 a[72:75],a[172:175],v[108:111],a[72:75]
ds_read_b64_tr_b16 a[154:155],v42 offset:25600
v_mfma_f32_16x16x32_bf16 a[64:67],a[176:179],v[132:135],a[64:67]
s_waitcnt lgkmcnt(4)
ds_read_b64_tr_b16 a[156:157],v42 offset:30720
v_mfma_f32_16x16x32_bf16 a[64:67],a[180:183],v[140:143],a[64:67]
ds_read_b64_tr_b16 a[158:159],v42 offset:35840
ds_read_b64_tr_b16 a[160:161],v44
v_mfma_f32_16x16x32_bf16 a[64:67],a[184:187],v[148:151],a[64:67]
ds_read_b64_tr_b16 a[162:163],v44 offset:5120
v_mfma_f32_16x16x32_bf16 a[64:67],a[188:191],v[156:159],a[64:67]
ds_read_b64_tr_b16 a[164:165],v44 offset:10240
v_mfma_f32_16x16x32_bf16 a[68:71],a[188:191],v[188:191],a[68:71]
ds_read_b64_tr_b16 a[166:167],v44 offset:15360
v_mfma_f32_16x16x32_bf16 a[68:71],a[184:187],v[180:183],a[68:71]
ds_read_b64_tr_b16 a[168:169],v44 offset:20480
ds_read_b64_tr_b16 a[170:171],v44 offset:25600
v_mfma_f32_16x16x32_bf16 a[68:71],a[180:183],v[172:175],a[68:71]
ds_read_b64_tr_b16 a[172:173],v44 offset:30720
v_mfma_f32_16x16x32_bf16 a[68:71],a[176:179],v[164:167],a[68:71]
ds_read_b64_tr_b16 a[174:175],v44 offset:35840
v_mfma_f32_16x16x32_bf16 a[72:75],a[176:179],v[196:199],a[72:75]
ds_read_b64_tr_b16 a[176:177],v2 offset:32
v_mfma_f32_16x16x32_bf16 a[72:75],a[180:183],v[204:207],a[72:75]
v_accvgpr_read_b32 v128,a64
v_accvgpr_read_b32 v129,a65
v_mfma_f32_16x16x32_bf16 a[72:75],a[184:187],v[212:215],a[72:75]
v_cvt_pk_bf16_f32 v128,v128,v129
v_accvgpr_read_b32 v130,a66
v_mfma_f32_16x16x32_bf16 a[72:75],a[188:191],v[220:223],a[72:75]
v_accvgpr_read_b32 v131,a67
v_cvt_pk_bf16_f32 v129,v130,v131
v_mfma_f32_16x16x32_bf16 a[80:83],a[96:99],v[64:67],a[80:83]
ds_read_b64_tr_b16 a[178:179],v2 offset:5152
ds_read_b64_tr_b16 a[180:181],v2 offset:10272
v_mfma_f32_16x16x32_bf16 a[80:83],a[100:103],v[68:71],a[80:83]
v_accvgpr_read_b32 v136,a68
v_accvgpr_read_b32 v137,a69
v_mfma_f32_16x16x32_bf16 a[80:83],a[104:107],v[72:75],a[80:83]
v_cvt_pk_bf16_f32 v136,v136,v137
v_accvgpr_read_b32 v138,a70
v_mfma_f32_16x16x32_bf16 a[80:83],a[108:111],v[76:79],a[80:83]
ds_read_b64_tr_b16 a[182:183],v2 offset:15392
v_accvgpr_read_b32 v139,a71
v_mfma_f32_16x16x32_bf16 a[84:87],a[108:111],v[92:95],a[84:87]
ds_read_b64_tr_b16 a[184:185],v2 offset:20512
v_cvt_pk_bf16_f32 v137,v138,v139
v_mfma_f32_16x16x32_bf16 a[84:87],a[104:107],v[88:91],a[84:87]
v_accvgpr_read_b32 v144,a72
v_accvgpr_read_b32 v145,a73
v_mfma_f32_16x16x32_bf16 a[84:87],a[100:103],v[84:87],a[84:87]
v_cvt_pk_bf16_f32 v144,v144,v145
v_accvgpr_read_b32 v146,a74
v_mfma_f32_16x16x32_bf16 a[84:87],a[96:99],v[80:83],a[84:87]
v_accvgpr_read_b32 v147,a75
v_cvt_pk_bf16_f32 v145,v146,v147
v_mfma_f32_16x16x32_bf16 a[88:91],a[96:99],v[96:99],a[88:91]
ds_read_b64_tr_b16 a[186:187],v2 offset:25632
ds_read_b64_tr_b16 a[188:189],v2 offset:30752
v_mfma_f32_16x16x32_bf16 a[88:91],a[100:103],v[100:103],a[88:91]
ds_read_b64_tr_b16 a[190:191],v2 offset:35872
v_mfma_f32_16x16x32_bf16 a[88:91],a[104:107],v[104:107],a[88:91]
ds_read_b64_tr_b16 a[96:97],v43 offset:32
v_mfma_f32_16x16x32_bf16 a[88:91],a[108:111],v[108:111],a[88:91]
ds_read_b64_tr_b16 a[98:99],v43 offset:5152
v_mfma_f32_16x16x32_bf16 a[80:83],a[112:115],v[132:135],a[80:83]
ds_read_b64_tr_b16 a[100:101],v43 offset:10272
ds_read_b64_tr_b16 a[102:103],v43 offset:15392
v_mfma_f32_16x16x32_bf16 a[80:83],a[116:119],v[140:143],a[80:83]
ds_read_b64_tr_b16 a[104:105],v43 offset:20512
v_mfma_f32_16x16x32_bf16 a[80:83],a[120:123],v[148:151],a[80:83]
ds_read_b64_tr_b16 a[106:107],v43 offset:25632
v_mfma_f32_16x16x32_bf16 a[80:83],a[124:127],v[156:159],a[80:83]
ds_read_b64_tr_b16 a[108:109],v43 offset:30752
v_mfma_f32_16x16x32_bf16 a[84:87],a[124:127],v[188:191],a[84:87]
ds_read_b64_tr_b16 a[110:111],v43 offset:35872
v_mfma_f32_16x16x32_bf16 a[84:87],a[120:123],v[180:183],a[84:87]
v_mfma_f32_16x16x32_bf16 a[84:87],a[116:119],v[172:175],a[84:87]
v_mfma_f32_16x16x32_bf16 a[84:87],a[112:115],v[164:167],a[84:87]
v_mfma_f32_16x16x32_bf16 a[88:91],a[112:115],v[196:199],a[88:91]
ds_read_b64_tr_b16 a[112:113],v45 offset:32
ds_read_b64_tr_b16 a[114:115],v45 offset:5152
v_mfma_f32_16x16x32_bf16 a[88:91],a[116:119],v[204:207],a[88:91]
v_accvgpr_read_b32 v130,a80
v_accvgpr_read_b32 v131,a81
v_mfma_f32_16x16x32_bf16 a[88:91],a[120:123],v[212:215],a[88:91]
v_accvgpr_read_b32 v14,a82
v_accvgpr_read_b32 v15,a83
v_mfma_f32_16x16x32_bf16 a[88:91],a[124:127],v[220:223],a[88:91]
v_cvt_pk_bf16_f32 v130,v130,v131
v_cvt_pk_bf16_f32 v131,v14,v15
v_mfma_f32_16x16x32_bf16 a[204:207],a[128:131],v[152:155],a[204:207]
s_lshl_b32 s94,s71,2
buffer_store_dwordx4 v[128:131],v25,s[32:35],s84 offen sc1
v_mfma_f32_16x16x32_bf16 a[204:207],a[132:135],v[184:187],a[204:207]
s_add_u32 s94,s94,s40
v_accvgpr_read_b32 v138,a84
v_accvgpr_read_b32 v139,a85
v_mfma_f32_16x16x32_bf16 a[204:207],a[136:139],v[216:219],a[204:207]
s_lshl_b32 s95,s42,2
s_add_u32 s95,s95,s40
s_lshl_b32 s95,s95,4
v_mfma_f32_16x16x32_bf16 a[204:207],a[140:143],v[248:251],a[204:207]
s_lshl_b32 s94,s94,4
v_mov_b32 v18,s68
v_mov_b32 v19,s69
v_mfma_f32_16x16x32_bf16 a[204:207],a[144:147],v[112:115],a[204:207]
s_waitcnt vmcnt(1)
s_mov_b64 exec,1
buffer_store_dwordx2 v[18:19],v6,s[64:67],s95 offen offset:2048 sc1
s_mov_b64 exec,-1
s_waitcnt lgkmcnt(0)
v_mfma_f32_16x16x32_bf16 a[204:207],a[148:151],v[116:119],a[204:207]
v_accvgpr_read_b32 v146,a88
v_accvgpr_read_b32 v147,a89
v_mfma_f32_16x16x32_bf16 a[204:207],a[152:155],v[120:123],a[204:207]
v_accvgpr_read_b32 v16,a86
v_accvgpr_read_b32 v17,a87
v_cvt_pk_bf16_f32 v138,v138,v139
v_accvgpr_read_b32 v12,a90
v_accvgpr_read_b32 v13,a91
v_cvt_pk_bf16_f32 v146,v146,v147
v_cvt_pk_bf16_f32 v139,v16,v17
v_cvt_pk_bf16_f32 v147,v12,v13
v_mfma_f32_16x16x32_bf16 a[204:207],a[156:159],v[124:127],a[204:207]
ds_read_b64_tr_b16 a[116:117],v45 offset:10272
ds_read_b64_tr_b16 a[118:119],v45 offset:15392
v_mfma_f32_16x16x32_bf16 a[204:207],a[160:163],v[228:231],a[204:207]
buffer_store_dwordx4 v[136:139],v25,s[32:35],s85 offen sc1
v_mfma_f32_16x16x32_bf16 a[204:207],a[164:167],v[236:239],a[204:207]
ds_read_b64_tr_b16 a[120:121],v45 offset:20512
ds_read_b64_tr_b16 a[122:123],v45 offset:25632
v_mfma_f32_16x16x32_bf16 a[204:207],a[168:171],v[244:247],a[204:207]
ds_read_b64_tr_b16 a[124:125],v45 offset:30752
ds_read_b64_tr_b16 a[126:127],v45 offset:35872
v_mfma_f32_16x16x32_bf16 a[204:207],a[172:175],v[252:255],a[204:207]
ds_read_b64_tr_b16 a[128:129],v1 offset:64
ds_read_b64_tr_b16 a[130:131],v1 offset:5184
v_mfma_f32_16x16x32_bf16 a[220:223],a[176:179],v[152:155],a[220:223]
buffer_store_dwordx4 v[144:147],v25,s[32:35],s86 offen sc1
v_mfma_f32_16x16x32_bf16 a[220:223],a[180:183],v[184:187],a[220:223]
ds_read_b64_tr_b16 a[132:133],v1 offset:10304
ds_read_b64_tr_b16 a[134:135],v1 offset:15424
v_mfma_f32_16x16x32_bf16 a[220:223],a[184:187],v[216:219],a[220:223]
ds_read_b64_tr_b16 a[136:137],v1 offset:20544
ds_read_b64_tr_b16 a[138:139],v1 offset:25664
v_mfma_f32_16x16x32_bf16 a[220:223],a[188:191],v[248:251],a[220:223]
ds_read_b64_tr_b16 a[140:141],v1 offset:30784
ds_read_b64_tr_b16 a[142:143],v1 offset:35904
v_mfma_f32_16x16x32_bf16 a[220:223],a[96:99],v[112:115],a[220:223]
ds_read_b64_tr_b16 a[144:145],v42 offset:64
ds_read_b64_tr_b16 a[146:147],v42 offset:5184
v_mfma_f32_16x16x32_bf16 a[220:223],a[100:103],v[116:119],a[220:223]
ds_read_b64_tr_b16 a[148:149],v42 offset:10304
ds_read_b64_tr_b16 a[150:151],v42 offset:15424
v_mfma_f32_16x16x32_bf16 a[220:223],a[104:107],v[120:123],a[220:223]
ds_read_b64_tr_b16 a[152:153],v42 offset:20544
ds_read_b64_tr_b16 a[154:155],v42 offset:25664
v_mfma_f32_16x16x32_bf16 a[220:223],a[108:111],v[124:127],a[220:223]
ds_read_b64_tr_b16 a[156:157],v42 offset:30784
ds_read_b64_tr_b16 a[158:159],v42 offset:35904
v_mfma_f32_16x16x32_bf16 a[220:223],a[112:115],v[228:231],a[220:223]
s_waitcnt lgkmcnt(14)
ds_read_b64_tr_b16 a[160:161],v44 offset:64
ds_read_b64_tr_b16 a[162:163],v44 offset:5184
v_mfma_f32_16x16x32_bf16 a[220:223],a[116:119],v[236:239],a[220:223]
ds_read_b64_tr_b16 a[164:165],v44 offset:10304
ds_read_b64_tr_b16 a[166:167],v44 offset:15424
v_mfma_f32_16x16x32_bf16 a[220:223],a[120:123],v[244:247],a[220:223]
ds_read_b64_tr_b16 a[168:169],v44 offset:20544
ds_read_b64_tr_b16 a[170:171],v44 offset:25664
v_mfma_f32_16x16x32_bf16 a[220:223],a[124:127],v[252:255],a[220:223]
ds_read_b64_tr_b16 a[172:173],v44 offset:30784
ds_read_b64_tr_b16 a[174:175],v44 offset:35904
v_mfma_f32_16x16x32_bf16 a[236:239],a[128:131],v[152:155],a[236:239]
s_waitcnt lgkmcnt(14)
ds_read_b64_tr_b16 a[176:177],v2 offset:96
ds_read_b64_tr_b16 a[178:179],v2 offset:5216
v_mfma_f32_16x16x32_bf16 a[236:239],a[132:135],v[184:187],a[236:239]
v_bfe_u32 v24,v6,4,2
v_min_u32 v24,2,v24
v_mfma_f32_16x16x32_bf16 a[236:239],a[136:139],v[216:219],a[236:239]
ds_read_b64_tr_b16 a[180:181],v2 offset:10336
ds_read_b64_tr_b16 a[182:183],v2 offset:15456
v_mfma_f32_16x16x32_bf16 a[236:239],a[140:143],v[248:251],a[236:239]
v_add_u32 v24,s70,v24
v_add_u32 v24,1,v24
v_mfma_f32_16x16x32_bf16 a[236:239],a[144:147],v[112:115],a[236:239]
s_waitcnt lgkmcnt(10)
ds_read_b64_tr_b16 a[184:185],v2 offset:20576
ds_read_b64_tr_b16 a[186:187],v2 offset:25696
v_mfma_f32_16x16x32_bf16 a[236:239],a[148:151],v[116:119],a[236:239]
v_and_b32 v24,3,v24
v_lshlrev_b32 v24,8,v24
v_mfma_f32_16x16x32_bf16 a[236:239],a[152:155],v[120:123],a[236:239]
ds_read_b64_tr_b16 a[188:189],v2 offset:30816
ds_read_b64_tr_b16 a[190:191],v2 offset:35936
v_mfma_f32_16x16x32_bf16 a[236:239],a[156:159],v[124:127],a[236:239]
ds_read_b64_tr_b16 a[96:97],v43 offset:96
ds_read_b64_tr_b16 a[98:99],v43 offset:5216
v_mfma_f32_16x16x32_bf16 a[236:239],a[160:163],v[228:231],a[236:239]
s_waitcnt lgkmcnt(8)
ds_read_b64_tr_b16 a[100:101],v43 offset:10336
ds_read_b64_tr_b16 a[102:103],v43 offset:15456
)A"
R"A(v_mfma_f32_16x16x32_bf16 a[236:239],a[164:167],v[236:239],a[236:239]
ds_read_b64_tr_b16 a[104:105],v43 offset:20576
ds_read_b64_tr_b16 a[106:107],v43 offset:25696
v_mfma_f32_16x16x32_bf16 a[236:239],a[168:171],v[244:247],a[236:239]
ds_read_b64_tr_b16 a[108:109],v43 offset:30816
ds_read_b64_tr_b16 a[110:111],v43 offset:35936
v_mfma_f32_16x16x32_bf16 a[236:239],a[172:175],v[252:255],a[236:239]
ds_read_b64_tr_b16 a[112:113],v45 offset:96
ds_read_b64_tr_b16 a[114:115],v45 offset:5216
v_mfma_f32_16x16x32_bf16 a[252:255],a[176:179],v[152:155],a[252:255]
s_waitcnt lgkmcnt(12)
ds_read_b64_tr_b16 a[116:117],v45 offset:10336
ds_read_b64_tr_b16 a[118:119],v45 offset:15456
v_mfma_f32_16x16x32_bf16 a[252:255],a[180:183],v[184:187],a[252:255]
ds_read_b64_tr_b16 a[120:121],v45 offset:20576
ds_read_b64_tr_b16 a[122:123],v45 offset:25696
v_mfma_f32_16x16x32_bf16 a[252:255],a[184:187],v[216:219],a[252:255]
s_waitcnt lgkmcnt(14)
ds_read_b64_tr_b16 a[124:125],v45 offset:30816
ds_read_b64_tr_b16 a[126:127],v45 offset:35936
v_mfma_f32_16x16x32_bf16 a[252:255],a[188:191],v[248:251],a[252:255]
s_waitcnt lgkmcnt(14)
ds_read_b64_tr_b16 a[128:129],v1 offset:128
ds_read_b64_tr_b16 a[130:131],v1 offset:5248
v_mfma_f32_16x16x32_bf16 a[252:255],a[96:99],v[112:115],a[252:255]
s_waitcnt lgkmcnt(14)
buffer_load_dwordx2 v[26:27],v24,s[64:67],s94 offen offset:2048 sc1
ds_read_b64_tr_b16 a[132:133],v1 offset:10368
ds_read_b64_tr_b16 a[134:135],v1 offset:15488
v_mfma_f32_16x16x32_bf16 a[252:255],a[100:103],v[116:119],a[252:255]
s_waitcnt lgkmcnt(14)
ds_read_b64_tr_b16 a[136:137],v1 offset:20608
ds_read_b64_tr_b16 a[138:139],v1 offset:25728
v_mfma_f32_16x16x32_bf16 a[252:255],a[104:107],v[120:123],a[252:255]
s_waitcnt lgkmcnt(14)
ds_read_b64_tr_b16 a[140:141],v1 offset:30848
ds_read_b64_tr_b16 a[142:143],v1 offset:35968
v_mfma_f32_16x16x32_bf16 a[252:255],a[108:111],v[124:127],a[252:255]
s_waitcnt lgkmcnt(14)
ds_read_b64_tr_b16 a[144:145],v42 offset:128
ds_read_b64_tr_b16 a[146:147],v42 offset:5248
v_mfma_f32_16x16x32_bf16 a[252:255],a[112:115],v[228:231],a[252:255]
s_waitcnt lgkmcnt(14)
ds_read_b64_tr_b16 a[148:149],v42 offset:10368
ds_read_b64_tr_b16 a[150:151],v42 offset:15488
v_mfma_f32_16x16x32_bf16 a[252:255],a[116:119],v[236:239],a[252:255]
s_lshl_b32 s95,s42,2
ds_read_b64_tr_b16 a[152:153],v42 offset:20608
ds_read_b64_tr_b16 a[154:155],v42 offset:25728
s_waitcnt lgkmcnt(15)
v_mfma_f32_16x16x32_bf16 a[252:255],a[120:123],v[244:247],a[252:255]
s_add_u32 s95,s95,s40
s_lshl_b32 s95,s95,4
ds_read_b64_tr_b16 a[156:157],v42 offset:30848
ds_read_b64_tr_b16 a[158:159],v42 offset:35968
v_mov_b32 v18,s68
v_mov_b32 v19,s69
s_waitcnt lgkmcnt(15)
v_mfma_f32_16x16x32_bf16 a[252:255],a[124:127],v[252:255],a[252:255]
s_waitcnt vmcnt(1)
s_mov_b64 exec,1
buffer_store_dwordx2 v[18:19],v6,s[64:67],s95 offen offset:3072 sc1
s_mov_b64 exec,-1
ds_read_b64_tr_b16 a[160:161],v44 offset:128
ds_read_b64_tr_b16 a[162:163],v44 offset:5248
s_waitcnt lgkmcnt(15)
v_mfma_f32_16x16x32_bf16 a[12:15],a[128:131],v[152:155],a[12:15]
s_waitcnt lgkmcnt(14)
ds_read_b64_tr_b16 a[164:165],v44 offset:10368
ds_read_b64_tr_b16 a[166:167],v44 offset:15488
v_mfma_f32_16x16x32_bf16 a[12:15],a[132:135],v[184:187],a[12:15]
s_waitcnt lgkmcnt(14)
ds_read_b64_tr_b16 a[168:169],v44 offset:20608
ds_read_b64_tr_b16 a[170:171],v44 offset:25728
v_mfma_f32_16x16x32_bf16 a[12:15],a[136:139],v[216:219],a[12:15]
s_waitcnt lgkmcnt(14)
ds_read_b64_tr_b16 a[172:173],v44 offset:30848
ds_read_b64_tr_b16 a[174:175],v44 offset:35968
v_mfma_f32_16x16x32_bf16 a[12:15],a[140:143],v[248:251],a[12:15]
s_waitcnt lgkmcnt(14)
ds_read_b64_tr_b16 a[176:177],v2 offset:160
ds_read_b64_tr_b16 a[178:179],v2 offset:5280
v_mfma_f32_16x16x32_bf16 a[12:15],a[144:147],v[112:115],a[12:15]
s_waitcnt lgkmcnt(14)
ds_read_b64_tr_b16 a[180:181],v2 offset:10400
ds_read_b64_tr_b16 a[182:183],v2 offset:15520
v_mfma_f32_16x16x32_bf16 a[12:15],a[148:151],v[116:119],a[12:15]
s_waitcnt lgkmcnt(14)
ds_read_b64_tr_b16 a[184:185],v2 offset:20640
ds_read_b64_tr_b16 a[186:187],v2 offset:25760
v_mfma_f32_16x16x32_bf16 a[12:15],a[152:155],v[120:123],a[12:15]
s_waitcnt lgkmcnt(14)
ds_read_b64_tr_b16 a[188:189],v2 offset:30880
ds_read_b64_tr_b16 a[190:191],v2 offset:36000
v_mfma_f32_16x16x32_bf16 a[12:15],a[156:159],v[124:127],a[12:15]
s_waitcnt lgkmcnt(14)
ds_read_b64_tr_b16 a[96:97],v43 offset:160
ds_read_b64_tr_b16 a[98:99],v43 offset:5280
v_mfma_f32_16x16x32_bf16 a[12:15],a[160:163],v[228:231],a[12:15]
s_waitcnt lgkmcnt(14)
ds_read_b64_tr_b16 a[100:101],v43 offset:10400
ds_read_b64_tr_b16 a[102:103],v43 offset:15520
v_mfma_f32_16x16x32_bf16 a[12:15],a[164:167],v[236:239],a[12:15]
s_waitcnt lgkmcnt(14)
ds_read_b64_tr_b16 a[104:105],v43 offset:20640
ds_read_b64_tr_b16 a[106:107],v43 offset:25760
v_mfma_f32_16x16x32_bf16 a[12:15],a[168:171],v[244:247],a[12:15]
s_waitcnt lgkmcnt(14)
ds_read_b64_tr_b16 a[108:109],v43 offset:30880
ds_read_b64_tr_b16 a[110:111],v43 offset:36000
v_mfma_f32_16x16x32_bf16 a[12:15],a[172:175],v[252:255],a[12:15]
s_waitcnt lgkmcnt(14)
ds_read_b64_tr_b16 a[112:113],v45 offset:160
ds_read_b64_tr_b16 a[114:115],v45 offset:5280
v_mfma_f32_16x16x32_bf16 a[28:31],a[176:179],v[152:155],a[28:31]
s_waitcnt lgkmcnt(14)
ds_read_b64_tr_b16 a[116:117],v45 offset:10400
ds_read_b64_tr_b16 a[118:119],v45 offset:15520
v_mfma_f32_16x16x32_bf16 a[28:31],a[180:183],v[184:187],a[28:31]
s_waitcnt lgkmcnt(14)
ds_read_b64_tr_b16 a[120:121],v45 offset:20640
ds_read_b64_tr_b16 a[122:123],v45 offset:25760
v_mfma_f32_16x16x32_bf16 a[28:31],a[184:187],v[216:219],a[28:31]
s_waitcnt lgkmcnt(14)
ds_read_b64_tr_b16 a[124:125],v45 offset:30880
ds_read_b64_tr_b16 a[126:127],v45 offset:36000
v_mfma_f32_16x16x32_bf16 a[28:31],a[188:191],v[248:251],a[28:31]
s_waitcnt lgkmcnt(14)
ds_read_b64_tr_b16 a[128:129],v1 offset:192
ds_read_b64_tr_b16 a[130:131],v1 offset:5312
v_mfma_f32_16x16x32_bf16 a[28:31],a[96:99],v[112:115],a[28:31]
s_waitcnt lgkmcnt(14)
ds_read_b64_tr_b16 a[132:133],v1 offset:10432
ds_read_b64_tr_b16 a[134:135],v1 offset:15552
v_mfma_f32_16x16x32_bf16 a[28:31],a[100:103],v[116:119],a[28:31]
s_waitcnt lgkmcnt(14)
ds_read_b64_tr_b16 a[136:137],v1 offset:20672
ds_read_b64_tr_b16 a[138:139],v1 offset:25792
v_mfma_f32_16x16x32_bf16 a[28:31],a[104:107],v[120:123],a[28:31]
s_waitcnt lgkmcnt(14)
ds_read_b64_tr_b16 a[140:141],v1 offset:30912
ds_read_b64_tr_b16 a[142:143],v1 offset:36032
v_mfma_f32_16x16x32_bf16 a[28:31],a[108:111],v[124:127],a[28:31]
s_waitcnt lgkmcnt(14)
ds_read_b64_tr_b16 a[144:145],v42 offset:192
ds_read_b64_tr_b16 a[146:147],v42 offset:5312
v_mfma_f32_16x16x32_bf16 a[28:31],a[112:115],v[228:231],a[28:31]
s_waitcnt lgkmcnt(14)
ds_read_b64_tr_b16 a[148:149],v42 offset:10432
ds_read_b64_tr_b16 a[150:151],v42 offset:15552
v_mfma_f32_16x16x32_bf16 a[28:31],a[116:119],v[236:239],a[28:31]
s_waitcnt lgkmcnt(14)
ds_read_b64_tr_b16 a[152:153],v42 offset:20672
ds_read_b64_tr_b16 a[154:155],v42 offset:25792
v_mfma_f32_16x16x32_bf16 a[28:31],a[120:123],v[244:247],a[28:31]
s_waitcnt lgkmcnt(14)
ds_read_b64_tr_b16 a[156:157],v42 offset:30912
ds_read_b64_tr_b16 a[158:159],v42 offset:36032
v_mfma_f32_16x16x32_bf16 a[28:31],a[124:127],v[252:255],a[28:31]
s_waitcnt lgkmcnt(14)
ds_read_b64_tr_b16 a[160:161],v44 offset:192
ds_read_b64_tr_b16 a[162:163],v44 offset:5312
v_mfma_f32_16x16x32_bf16 a[44:47],a[128:131],v[152:155],a[44:47]
ds_read_b64_tr_b16 a[164:165],v44 offset:10432
ds_read_b64_tr_b16 a[166:167],v44 offset:15552
s_waitcnt vmcnt(1)
v_cmp_ne_u32 s[90:91],s68,v26
v_cmp_ne_u32 s[92:93],s69,v27
s_nop 0
s_or_b64 s[90:91],s[90:91],s[92:93]
s_cbranch_scc1 L_fr10_%=
L_fo10_%=:
s_waitcnt lgkmcnt(15)
v_mfma_f32_16x16x32_bf16 a[44:47],a[132:135],v[184:187],a[44:47]
s_waitcnt lgkmcnt(14)
buffer_load_dwordx4 v[64:67],v6,s[32:35],s76 offen sc1
ds_read_b64_tr_b16 a[168:169],v44 offset:20672
ds_read_b64_tr_b16 a[170:171],v44 offset:25792
v_mfma_f32_16x16x32_bf16 a[44:47],a[136:139],v[216:219],a[44:47]
s_lshl_b32 s94,s71,2
ds_read_b64_tr_b16 a[172:173],v44 offset:30912
ds_read_b64_tr_b16 a[174:175],v44 offset:36032
s_waitcnt lgkmcnt(15)
v_mfma_f32_16x16x32_bf16 a[44:47],a[140:143],v[248:251],a[44:47]
s_add_u32 s94,s94,s40
ds_read_b64_tr_b16 a[176:177],v2 offset:224
ds_read_b64_tr_b16 a[178:179],v2 offset:5344
s_waitcnt lgkmcnt(15)
v_mfma_f32_16x16x32_bf16 a[44:47],a[144:147],v[112:115],a[44:47]
s_lshl_b32 s94,s94,4
ds_read_b64_tr_b16 a[180:181],v2 offset:10464
ds_read_b64_tr_b16 a[182:183],v2 offset:15584
s_waitcnt lgkmcnt(15)
v_mfma_f32_16x16x32_bf16 a[44:47],a[148:151],v[116:119],a[44:47]
s_waitcnt lgkmcnt(14)
buffer_load_dwordx4 v[80:83],v6,s[32:35],s77 offen sc1
ds_read_b64_tr_b16 a[184:185],v2 offset:20704
ds_read_b64_tr_b16 a[186:187],v2 offset:25824
v_mfma_f32_16x16x32_bf16 a[44:47],a[152:155],v[120:123],a[44:47]
s_waitcnt lgkmcnt(14)
ds_read_b64_tr_b16 a[188:189],v2 offset:30944
ds_read_b64_tr_b16 a[190:191],v2 offset:36064
v_mfma_f32_16x16x32_bf16 a[44:47],a[156:159],v[124:127],a[44:47]
s_waitcnt lgkmcnt(14)
ds_read_b64_tr_b16 a[96:97],v43 offset:224
ds_read_b64_tr_b16 a[98:99],v43 offset:5344
v_mfma_f32_16x16x32_bf16 a[44:47],a[160:163],v[228:231],a[44:47]
s_waitcnt lgkmcnt(14)
ds_read_b64_tr_b16 a[100:101],v43 offset:10464
ds_read_b64_tr_b16 a[102:103],v43 offset:15584
v_mfma_f32_16x16x32_bf16 a[44:47],a[164:167],v[236:239],a[44:47]
s_waitcnt lgkmcnt(14)
buffer_load_dwordx4 v[96:99],v6,s[32:35],s78 offen sc1
ds_read_b64_tr_b16 a[104:105],v43 offset:20704
ds_read_b64_tr_b16 a[106:107],v43 offset:25824
v_mfma_f32_16x16x32_bf16 a[44:47],a[168:171],v[244:247],a[44:47]
s_waitcnt lgkmcnt(14)
ds_read_b64_tr_b16 a[108:109],v43 offset:30944
ds_read_b64_tr_b16 a[110:111],v43 offset:36064
v_mfma_f32_16x16x32_bf16 a[44:47],a[172:175],v[252:255],a[44:47]
s_waitcnt lgkmcnt(14)
ds_read_b64_tr_b16 a[112:113],v45 offset:224
ds_read_b64_tr_b16 a[114:115],v45 offset:5344
v_mfma_f32_16x16x32_bf16 a[60:63],a[176:179],v[152:155],a[60:63]
s_waitcnt lgkmcnt(14)
ds_read_b64_tr_b16 a[116:117],v45 offset:10464
ds_read_b64_tr_b16 a[118:119],v45 offset:15584
v_mfma_f32_16x16x32_bf16 a[60:63],a[180:183],v[184:187],a[60:63]
s_waitcnt lgkmcnt(14)
buffer_load_dwordx4 v[68:71],v6,s[32:35],s76 offen offset:1024 sc1
ds_read_b64_tr_b16 a[120:121],v45 offset:20704
ds_read_b64_tr_b16 a[122:123],v45 offset:25824
v_mfma_f32_16x16x32_bf16 a[60:63],a[184:187],v[216:219],a[60:63]
s_waitcnt lgkmcnt(14)
ds_read_b64_tr_b16 a[124:125],v45 offset:30944
ds_read_b64_tr_b16 a[126:127],v45 offset:36064
v_mfma_f32_16x16x32_bf16 a[60:63],a[188:191],v[248:251],a[60:63]
s_waitcnt lgkmcnt(14)
ds_read_b64_tr_b16 a[128:129],v1 offset:256
ds_read_b64_tr_b16 a[130:131],v1 offset:5376
v_mfma_f32_16x16x32_bf16 a[60:63],a[96:99],v[112:115],a[60:63]
s_waitcnt lgkmcnt(14)
ds_read_b64_tr_b16 a[132:133],v1 offset:10496
ds_read_b64_tr_b16 a[134:135],v1 offset:15616
v_mfma_f32_16x16x32_bf16 a[60:63],a[100:103],v[116:119],a[60:63]
s_waitcnt lgkmcnt(14)
buffer_load_dwordx4 v[84:87],v6,s[32:35],s77 offen offset:1024 sc1
ds_read_b64_tr_b16 a[136:137],v1 offset:20736
ds_read_b64_tr_b16 a[138:139],v1 offset:25856
v_mfma_f32_16x16x32_bf16 a[60:63],a[104:107],v[120:123],a[60:63]
s_waitcnt lgkmcnt(14)
ds_read_b64_tr_b16 a[140:141],v1 offset:30976
ds_read_b64_tr_b16 a[142:143],v1 offset:36096
v_mfma_f32_16x16x32_bf16 a[60:63],a[108:111],v[124:127],a[60:63]
s_waitcnt lgkmcnt(14)
ds_read_b64_tr_b16 a[144:145],v42 offset:256
ds_read_b64_tr_b16 a[146:147],v42 offset:5376
v_mfma_f32_16x16x32_bf16 a[60:63],a[112:115],v[228:231],a[60:63]
s_waitcnt lgkmcnt(14)
ds_read_b64_tr_b16 a[148:149],v42 offset:10496
ds_read_b64_tr_b16 a[150:151],v42 offset:15616
v_mfma_f32_16x16x32_bf16 a[60:63],a[116:119],v[236:239],a[60:63]
s_waitcnt lgkmcnt(14)
buffer_load_dwordx4 v[100:103],v6,s[32:35],s78 offen offset:1024 sc1
ds_read_b64_tr_b16 a[152:153],v42 offset:20736
ds_read_b64_tr_b16 a[154:155],v42 offset:25856
v_mfma_f32_16x16x32_bf16 a[60:63],a[120:123],v[244:247],a[60:63]
s_waitcnt lgkmcnt(14)
ds_read_b64_tr_b16 a[156:157],v42 offset:30976
ds_read_b64_tr_b16 a[158:159],v42 offset:36096
v_mfma_f32_16x16x32_bf16 a[60:63],a[124:127],v[252:255],a[60:63]
)A"
R"A(s_waitcnt lgkmcnt(14)
ds_read_b64_tr_b16 a[160:161],v44 offset:256
ds_read_b64_tr_b16 a[162:163],v44 offset:5376
v_mfma_f32_16x16x32_bf16 a[76:79],a[128:131],v[152:155],a[76:79]
s_waitcnt lgkmcnt(14)
ds_read_b64_tr_b16 a[164:165],v44 offset:10496
ds_read_b64_tr_b16 a[166:167],v44 offset:15616
v_mfma_f32_16x16x32_bf16 a[76:79],a[132:135],v[184:187],a[76:79]
s_waitcnt lgkmcnt(14)
buffer_load_dwordx4 v[72:75],v6,s[32:35],s76 offen offset:2048 sc1
ds_read_b64_tr_b16 a[168:169],v44 offset:20736
ds_read_b64_tr_b16 a[170:171],v44 offset:25856
v_bfe_u32 v24,v6,4,2
v_min_u32 v24,2,v24
v_mfma_f32_16x16x32_bf16 a[76:79],a[136:139],v[216:219],a[76:79]
s_waitcnt lgkmcnt(14)
ds_read_b64_tr_b16 a[172:173],v44 offset:30976
ds_read_b64_tr_b16 a[174:175],v44 offset:36096
v_add_u32 v24,s70,v24
v_add_u32 v24,1,v24
v_mfma_f32_16x16x32_bf16 a[76:79],a[140:143],v[248:251],a[76:79]
s_waitcnt lgkmcnt(14)
ds_read_b64_tr_b16 a[176:177],v2 offset:288
ds_read_b64_tr_b16 a[178:179],v2 offset:5408
v_and_b32 v24,3,v24
v_lshlrev_b32 v24,8,v24
v_mfma_f32_16x16x32_bf16 a[76:79],a[144:147],v[112:115],a[76:79]
s_waitcnt lgkmcnt(14)
ds_read_b64_tr_b16 a[180:181],v2 offset:10528
ds_read_b64_tr_b16 a[182:183],v2 offset:15648
v_mfma_f32_16x16x32_bf16 a[76:79],a[148:151],v[116:119],a[76:79]
s_waitcnt lgkmcnt(14)
buffer_load_dwordx4 v[88:91],v6,s[32:35],s77 offen offset:2048 sc1
ds_read_b64_tr_b16 a[184:185],v2 offset:20768
ds_read_b64_tr_b16 a[186:187],v2 offset:25888
v_mfma_f32_16x16x32_bf16 a[76:79],a[152:155],v[120:123],a[76:79]
s_waitcnt lgkmcnt(14)
ds_read_b64_tr_b16 a[188:189],v2 offset:31008
ds_read_b64_tr_b16 a[190:191],v2 offset:36128
v_mfma_f32_16x16x32_bf16 a[76:79],a[156:159],v[124:127],a[76:79]
s_waitcnt lgkmcnt(14)
ds_read_b64_tr_b16 a[96:97],v43 offset:288
ds_read_b64_tr_b16 a[98:99],v43 offset:5408
v_mfma_f32_16x16x32_bf16 a[76:79],a[160:163],v[228:231],a[76:79]
s_waitcnt lgkmcnt(14)
ds_read_b64_tr_b16 a[100:101],v43 offset:10528
ds_read_b64_tr_b16 a[102:103],v43 offset:15648
v_mfma_f32_16x16x32_bf16 a[76:79],a[164:167],v[236:239],a[76:79]
s_waitcnt lgkmcnt(14)
buffer_load_dwordx4 v[104:107],v6,s[32:35],s78 offen offset:2048 sc1
ds_read_b64_tr_b16 a[104:105],v43 offset:20768
ds_read_b64_tr_b16 a[106:107],v43 offset:25888
v_mfma_f32_16x16x32_bf16 a[76:79],a[168:171],v[244:247],a[76:79]
s_waitcnt lgkmcnt(14)
ds_read_b64_tr_b16 a[108:109],v43 offset:31008
ds_read_b64_tr_b16 a[110:111],v43 offset:36128
v_mfma_f32_16x16x32_bf16 a[76:79],a[172:175],v[252:255],a[76:79]
s_waitcnt lgkmcnt(14)
ds_read_b64_tr_b16 a[112:113],v45 offset:288
ds_read_b64_tr_b16 a[114:115],v45 offset:5408
v_mfma_f32_16x16x32_bf16 a[92:95],a[176:179],v[152:155],a[92:95]
s_waitcnt lgkmcnt(14)
ds_read_b64_tr_b16 a[116:117],v45 offset:10528
ds_read_b64_tr_b16 a[118:119],v45 offset:15648
v_mfma_f32_16x16x32_bf16 a[92:95],a[180:183],v[184:187],a[92:95]
s_waitcnt lgkmcnt(14)
buffer_load_dwordx4 v[76:79],v6,s[32:35],s76 offen offset:3072 sc1
ds_read_b64_tr_b16 a[120:121],v45 offset:20768
ds_read_b64_tr_b16 a[122:123],v45 offset:25888
v_mfma_f32_16x16x32_bf16 a[92:95],a[184:187],v[216:219],a[92:95]
s_waitcnt lgkmcnt(14)
ds_read_b64_tr_b16 a[124:125],v45 offset:31008
ds_read_b64_tr_b16 a[126:127],v45 offset:36128
v_mfma_f32_16x16x32_bf16 a[92:95],a[188:191],v[248:251],a[92:95]
s_waitcnt lgkmcnt(14)
v_mfma_f32_16x16x32_bf16 a[92:95],a[96:99],v[112:115],a[92:95]
s_waitcnt lgkmcnt(12)
v_mfma_f32_16x16x32_bf16 a[92:95],a[100:103],v[116:119],a[92:95]
s_waitcnt lgkmcnt(10)
buffer_load_dwordx4 v[92:95],v6,s[32:35],s77 offen offset:3072 sc1
v_mfma_f32_16x16x32_bf16 a[92:95],a[104:107],v[120:123],a[92:95]
s_waitcnt lgkmcnt(8)
v_mfma_f32_16x16x32_bf16 a[92:95],a[108:111],v[124:127],a[92:95]
s_waitcnt lgkmcnt(6)
v_mfma_f32_16x16x32_bf16 a[92:95],a[112:115],v[228:231],a[92:95]
s_waitcnt lgkmcnt(4)
v_mfma_f32_16x16x32_bf16 a[92:95],a[116:119],v[236:239],a[92:95]
s_waitcnt lgkmcnt(2)
buffer_load_dwordx4 v[108:111],v6,s[32:35],s78 offen offset:3072 sc1
v_mfma_f32_16x16x32_bf16 a[92:95],a[120:123],v[244:247],a[92:95]
s_waitcnt lgkmcnt(0)
v_mfma_f32_16x16x32_bf16 a[92:95],a[124:127],v[252:255],a[92:95]
buffer_load_dwordx2 v[26:27],v24,s[64:67],s94 offen offset:3072 sc1
s_waitcnt vmcnt(0)
v_cmp_ne_u32 s[90:91],s68,v26
v_cmp_ne_u32 s[92:93],s69,v27
s_nop 0
s_or_b64 s[90:91],s[90:91],s[92:93]
s_cbranch_scc1 L_fr11_%=
L_fo11_%=:
buffer_load_dwordx4 v[132:135],v25,s[32:35],s76 offen sc1
buffer_load_dwordx4 v[164:167],v25,s[32:35],s77 offen sc1
buffer_load_dwordx4 v[196:199],v25,s[32:35],s78 offen sc1
v_mfma_f32_16x16x32_bf16 a[204:207],v[34:37],v[64:67],a[204:207]
v_mfma_f32_16x16x32_bf16 a[204:207],v[34:37],v[80:83],a[204:207]
v_mfma_f32_16x16x32_bf16 a[204:207],v[34:37],v[96:99],a[204:207]
v_mfma_f32_16x16x32_bf16 a[220:223],v[32:35],v[64:67],a[220:223]
v_mfma_f32_16x16x32_bf16 a[220:223],v[32:35],v[80:83],a[220:223]
v_mfma_f32_16x16x32_bf16 a[220:223],v[32:35],v[96:99],a[220:223]
v_mfma_f32_16x16x32_bf16 a[236:239],v[34:37],v[68:71],a[236:239]
v_mfma_f32_16x16x32_bf16 a[236:239],v[34:37],v[84:87],a[236:239]
v_mfma_f32_16x16x32_bf16 a[236:239],v[34:37],v[100:103],a[236:239]
s_nop 1
v_accvgpr_read_b32 v128,a204
v_accvgpr_read_b32 v129,a205
v_mfma_f32_16x16x32_bf16 a[252:255],v[32:35],v[68:71],a[252:255]
v_cvt_pk_bf16_f32 v128,v128,v129
v_accvgpr_read_b32 v130,a206
v_mfma_f32_16x16x32_bf16 a[252:255],v[32:35],v[84:87],a[252:255]
v_accvgpr_read_b32 v131,a207
v_cvt_pk_bf16_f32 v129,v130,v131
v_mfma_f32_16x16x32_bf16 a[252:255],v[32:35],v[100:103],a[252:255]
buffer_store_dwordx2 v[128:129],v40,s[20:23],s43 offen
v_mfma_f32_16x16x32_bf16 a[12:15],v[34:37],v[72:75],a[12:15]
v_accvgpr_read_b32 v130,a220
v_accvgpr_read_b32 v131,a221
v_mfma_f32_16x16x32_bf16 a[12:15],v[34:37],v[88:91],a[12:15]
v_accvgpr_read_b32 v14,a222
v_accvgpr_read_b32 v15,a223
v_mfma_f32_16x16x32_bf16 a[12:15],v[34:37],v[104:107],a[12:15]
v_cvt_pk_bf16_f32 v130,v130,v131
v_cvt_pk_bf16_f32 v131,v14,v15
v_mfma_f32_16x16x32_bf16 a[28:31],v[32:35],v[72:75],a[28:31]
buffer_store_dwordx2 v[130:131],v40,s[20:23],s43 offen offset:32
v_mfma_f32_16x16x32_bf16 a[28:31],v[32:35],v[88:91],a[28:31]
v_accvgpr_read_b32 v136,a236
v_accvgpr_read_b32 v137,a237
v_mfma_f32_16x16x32_bf16 a[28:31],v[32:35],v[104:107],a[28:31]
v_cvt_pk_bf16_f32 v136,v136,v137
v_accvgpr_read_b32 v138,a238
v_mfma_f32_16x16x32_bf16 a[44:47],v[34:37],v[76:79],a[44:47]
v_accvgpr_read_b32 v139,a239
v_cvt_pk_bf16_f32 v137,v138,v139
v_mfma_f32_16x16x32_bf16 a[44:47],v[34:37],v[92:95],a[44:47]
buffer_store_dwordx2 v[136:137],v40,s[20:23],s43 offen offset:64
v_mfma_f32_16x16x32_bf16 a[44:47],v[34:37],v[108:111],a[44:47]
v_accvgpr_read_b32 v144,a12
v_accvgpr_read_b32 v145,a13
v_cvt_pk_bf16_f32 v144,v144,v145
v_accvgpr_read_b32 v146,a14
v_accvgpr_read_b32 v147,a15
v_mfma_f32_16x16x32_bf16 a[60:63],v[32:35],v[76:79],a[60:63]
v_accvgpr_read_b32 v138,a252
v_accvgpr_read_b32 v139,a253
v_cvt_pk_bf16_f32 v145,v146,v147
v_mfma_f32_16x16x32_bf16 a[60:63],v[32:35],v[92:95],a[60:63]
v_accvgpr_read_b32 v16,a254
v_accvgpr_read_b32 v17,a255
v_cvt_pk_bf16_f32 v138,v138,v139
v_cvt_pk_bf16_f32 v139,v16,v17
v_mfma_f32_16x16x32_bf16 a[60:63],v[32:35],v[108:111],a[60:63]
s_waitcnt vmcnt(5)
buffer_store_dwordx2 v[138:139],v40,s[20:23],s43 offen offset:96
buffer_store_dwordx2 v[144:145],v40,s[20:23],s43 offen offset:128
v_accvgpr_read_b32 v146,a28
v_accvgpr_read_b32 v147,a29
v_accvgpr_read_b32 v12,a30
v_accvgpr_read_b32 v13,a31
v_cvt_pk_bf16_f32 v146,v146,v147
v_cvt_pk_bf16_f32 v147,v12,v13
s_nop 3
buffer_store_dwordx2 v[146:147],v40,s[20:23],s43 offen offset:160
v_accvgpr_read_b32 v160,a44
v_accvgpr_read_b32 v161,a45
v_cvt_pk_bf16_f32 v160,v160,v161
v_accvgpr_read_b32 v162,a46
v_accvgpr_read_b32 v163,a47
v_cvt_pk_bf16_f32 v161,v162,v163
buffer_store_dwordx2 v[160:161],v40,s[20:23],s43 offen offset:192
v_accvgpr_read_b32 v162,a60
v_accvgpr_read_b32 v163,a61
v_accvgpr_read_b32 v14,a62
v_accvgpr_read_b32 v15,a63
v_cvt_pk_bf16_f32 v162,v162,v163
v_cvt_pk_bf16_f32 v163,v14,v15
buffer_store_dwordx2 v[162:163],v40,s[20:23],s43 offen offset:224
v_mfma_f32_16x16x32_bf16 a[76:79],v[34:37],v[132:135],a[76:79]
s_waitcnt vmcnt(9)
v_mfma_f32_16x16x32_bf16 a[76:79],v[34:37],v[164:167],a[76:79]
s_waitcnt vmcnt(8)
v_mfma_f32_16x16x32_bf16 a[76:79],v[34:37],v[196:199],a[76:79]
v_mfma_f32_16x16x32_bf16 a[92:95],v[32:35],v[132:135],a[92:95]
v_mfma_f32_16x16x32_bf16 a[92:95],v[32:35],v[164:167],a[92:95]
v_mfma_f32_16x16x32_bf16 a[92:95],v[32:35],v[196:199],a[92:95]
s_nop 3
s_nop 0
v_accvgpr_read_b32 v168,a76
v_accvgpr_read_b32 v169,a77
v_cvt_pk_bf16_f32 v168,v168,v169
v_accvgpr_read_b32 v170,a78
v_accvgpr_read_b32 v171,a79
v_cvt_pk_bf16_f32 v169,v170,v171
buffer_store_dwordx2 v[168:169],v40,s[20:23],s43 offen offset:256
v_accvgpr_read_b32 v170,a92
v_accvgpr_read_b32 v171,a93
v_accvgpr_read_b32 v16,a94
v_accvgpr_read_b32 v17,a95
v_cvt_pk_bf16_f32 v170,v170,v171
v_cvt_pk_bf16_f32 v171,v16,v17
buffer_store_dwordx2 v[170:171],v40,s[20:23],s43 offen offset:288
s_or_b32 s62,s42,s40
s_cmp_lg_u32 s62,0
s_cbranch_scc1 L_noep_%=
v_mov_b32 v12,s68
s_mov_b64 exec,1
buffer_store_dword v12,v25,s[64:67],0 offen sc1
s_mov_b64 exec,-1
L_noep_%=:
s_waitcnt vmcnt(0) lgkmcnt(0)
s_branch L_end_%=
L_ps2_%=:
s_min_i32 s79,s79,100
s_add_u32 s78,s78,s79
L_pl2_%=:
s_sleep 1
s_sub_u32 s79,s79,4
s_cmp_gt_i32 s79,1
s_cbranch_scc1 L_pl2_%=
s_branch L_pr2_%=
L_ps3_%=:
s_min_i32 s79,s79,100
s_add_u32 s78,s78,s79
L_pl3_%=:
s_sleep 1
s_sub_u32 s79,s79,4
s_cmp_gt_i32 s79,1
s_cbranch_scc1 L_pl3_%=
s_branch L_pr3_%=
L_ps4_%=:
s_min_i32 s79,s79,100
s_add_u32 s78,s78,s79
L_pl4_%=:
s_sleep 1
s_sub_u32 s79,s79,4
s_cmp_gt_i32 s79,1
s_cbranch_scc1 L_pl4_%=
s_branch L_pr4_%=
L_ps5_%=:
s_min_i32 s79,s79,100
s_add_u32 s78,s78,s79
L_pl5_%=:
s_sleep 1
s_sub_u32 s79,s79,4
s_cmp_gt_i32 s79,1
s_cbranch_scc1 L_pl5_%=
s_branch L_pr5_%=
L_ps6_%=:
s_min_i32 s79,s79,100
s_add_u32 s78,s78,s79
L_pl6_%=:
s_sleep 1
s_sub_u32 s79,s79,4
s_cmp_gt_i32 s79,1
s_cbranch_scc1 L_pl6_%=
s_branch L_pr6_%=
L_ps7_%=:
s_min_i32 s79,s79,100
s_add_u32 s78,s78,s79
L_pl7_%=:
s_sleep 1
s_sub_u32 s79,s79,4
s_cmp_gt_i32 s79,1
s_cbranch_scc1 L_pl7_%=
s_branch L_pr7_%=
L_fr8_%=:
s_mov_b32 s88,262144
L_fl8_%=:
buffer_load_dwordx2 v[26:27],v24,s[64:67],s94 offen sc1
s_waitcnt vmcnt(0)
s_sub_u32 s88,s88,1
s_cmp_eq_u32 s88,0
s_cbranch_scc1 L_fo8_%=
v_cmp_ne_u32 s[90:91],s68,v26
v_cmp_ne_u32 s[92:93],s69,v27
s_nop 0
s_or_b64 s[90:91],s[90:91],s[92:93]
s_cbranch_scc1 L_fl8_%=
s_branch L_fo8_%=
L_fr9_%=:
s_mov_b32 s88,262144
L_fl9_%=:
buffer_load_dwordx2 v[26:27],v24,s[64:67],s94 offen offset:1024 sc1
s_waitcnt vmcnt(0)
s_sub_u32 s88,s88,1
s_cmp_eq_u32 s88,0
s_cbranch_scc1 L_fo9_%=
v_cmp_ne_u32 s[90:91],s68,v26
v_cmp_ne_u32 s[92:93],s69,v27
s_nop 0
s_or_b64 s[90:91],s[90:91],s[92:93]
s_cbranch_scc1 L_fl9_%=
s_branch L_fo9_%=
L_fr10_%=:
s_mov_b32 s88,262144
L_fl10_%=:
buffer_load_dwordx2 v[26:27],v24,s[64:67],s94 offen offset:2048 sc1
s_waitcnt vmcnt(0)
s_sub_u32 s88,s88,1
s_cmp_eq_u32 s88,0
s_cbranch_scc1 L_fo10_%=
v_cmp_ne_u32 s[90:91],s68,v26
v_cmp_ne_u32 s[92:93],s69,v27
s_nop 0
s_or_b64 s[90:91],s[90:91],s[92:93]
s_cbranch_scc1 L_fl10_%=
s_branch L_fo10_%=
L_fr11_%=:
s_mov_b32 s88,262144
L_fl11_%=:
buffer_load_dwordx2 v[26:27],v24,s[64:67],s94 offen offset:3072 sc1
s_waitcnt vmcnt(0)
s_sub_u32 s88,s88,1
s_cmp_eq_u32 s88,0
s_cbranch_scc1 L_fo11_%=
v_cmp_ne_u32 s[90:91],s68,v26
v_cmp_ne_u32 s[92:93],s69,v27
s_nop 0
s_or_b64 s[90:91],s[90:91],s[92:93]
s_cbranch_scc1 L_fl11_%=
s_branch L_fo11_%=
L_out_end_%=:
L_fast_end_%=:
L_slow0_%=:
s_mov_b32 s98,0
s_branch L_slowc_%=
L_slow1_%=:
s_mov_b32 s98,1
L_slowc_%=:
s_nop 7
s_nop 7
s_waitcnt vmcnt(0) lgkmcnt(0)
s_lshl_b32 s96,%1,2
s_add_u32 s96,s96,s40
s_lshl_b32 s96,s96,15
s_add_u32 s96,s96,167772160
s_mov_b64 s[56:57],s[12:13]
s_add_u32 s56,s56,s96
s_addc_u32 s57,s57,0
s_and_b32 s57,s57,0xffff
s_mov_b32 s58,32768
s_mov_b32 s59,0x20000
s_mov_b32 s96,0
buffer_store_dwordx4 v[64:67],v6,s[56:59],s96 offen
buffer_store_dwordx4 v[68:71],v6,s[56:59],s96 offen offset:1024
buffer_store_dwordx4 v[72:75],v6,s[56:59],s96 offen offset:2048
buffer_store_dwordx4 v[76:79],v6,s[56:59],s96 offen offset:3072
)A"
R"A(s_mov_b32 s96,4096
buffer_store_dwordx4 v[80:83],v6,s[56:59],s96 offen
buffer_store_dwordx4 v[84:87],v6,s[56:59],s96 offen offset:1024
buffer_store_dwordx4 v[88:91],v6,s[56:59],s96 offen offset:2048
buffer_store_dwordx4 v[92:95],v6,s[56:59],s96 offen offset:3072
s_mov_b32 s96,8192
buffer_store_dwordx4 v[96:99],v6,s[56:59],s96 offen
buffer_store_dwordx4 v[100:103],v6,s[56:59],s96 offen offset:1024
buffer_store_dwordx4 v[104:107],v6,s[56:59],s96 offen offset:2048
buffer_store_dwordx4 v[108:111],v6,s[56:59],s96 offen offset:3072
s_mov_b32 s96,12288
buffer_store_dwordx4 v[112:115],v6,s[56:59],s96 offen
buffer_store_dwordx4 v[116:119],v6,s[56:59],s96 offen offset:1024
buffer_store_dwordx4 v[120:123],v6,s[56:59],s96 offen offset:2048
buffer_store_dwordx4 v[124:127],v6,s[56:59],s96 offen offset:3072
s_mov_b32 s96,16384
buffer_store_dwordx4 v[128:131],v6,s[56:59],s96 offen
buffer_store_dwordx4 v[132:135],v6,s[56:59],s96 offen offset:1024
buffer_store_dwordx4 v[136:139],v6,s[56:59],s96 offen offset:2048
buffer_store_dwordx4 v[140:143],v6,s[56:59],s96 offen offset:3072
s_mov_b32 s96,20480
buffer_store_dwordx4 v[144:147],v6,s[56:59],s96 offen
buffer_store_dwordx4 v[148:151],v6,s[56:59],s96 offen offset:1024
buffer_store_dwordx4 v[152:155],v6,s[56:59],s96 offen offset:2048
buffer_store_dwordx4 v[156:159],v6,s[56:59],s96 offen offset:3072
s_mov_b32 s96,24576
buffer_store_dwordx4 v[160:163],v6,s[56:59],s96 offen
buffer_store_dwordx4 v[164:167],v6,s[56:59],s96 offen offset:1024
buffer_store_dwordx4 v[168:171],v6,s[56:59],s96 offen offset:2048
buffer_store_dwordx4 v[172:175],v6,s[56:59],s96 offen offset:3072
s_mov_b32 s96,28672
buffer_store_dwordx4 v[176:179],v6,s[56:59],s96 offen
buffer_store_dwordx4 v[180:183],v6,s[56:59],s96 offen offset:1024
buffer_store_dwordx4 v[184:187],v6,s[56:59],s96 offen offset:2048
buffer_store_dwordx4 v[188:191],v6,s[56:59],s96 offen offset:3072
s_waitcnt vmcnt(0)
v_writelane_b32 v173,s88,0
v_writelane_b32 v173,s89,1
v_writelane_b32 v173,s90,2
v_writelane_b32 v173,s91,3
v_writelane_b32 v173,s92,4
v_writelane_b32 v173,s93,5
v_writelane_b32 v173,s94,6
v_writelane_b32 v173,s95,7
s_mov_b32 s88,s98
s_mul_i32 s90,s41,307200
s_mov_b64 s[92:93],s[4:5]
s_add_u32 s92,s92,s90
s_addc_u32 s93,s93,0
s_and_b32 s93,s93,0xffff
s_mov_b32 s94,307200
s_mov_b32 s95,0x20000
s_mov_b64 s[96:97],s[10:11]
s_and_b32 s97,s97,0xffff
s_mov_b32 s98,5242880
s_mov_b32 s99,0x20000
v_bfe_u32 v171,v6,4,4
v_lshrrev_b32 v172,8,v6
v_lshlrev_b32 v164,8,v171
v_lshl_add_u32 v164,v172,4,v164
v_lshlrev_b32 v165,10,v172
v_lshl_add_u32 v165,v171,1,v165
v_add_u32 v166,4096,v165
v_lshlrev_b32 v168,16,v172
v_lshrrev_b32 v170,2,v171
v_lshl_add_u32 v168,v170,4,v168
v_and_b32 v170,3,v171
v_lshl_add_u32 v168,v170,1,v168
s_lshl_b32 s90,s42,1
s_add_u32 s90,s90,s88
s_lshl_b32 s90,s90,8
v_add_u32 v168,s90,v168
s_mov_b32 s89,0
L_sjb_%=:
s_add_u32 s90,s89,s70
s_add_u32 s90,s90,1
s_and_b32 s90,s90,3
s_lshl_b32 s91,s40,2
s_add_u32 s90,s90,s91
s_lshl_b32 s90,s90,4
v_bfe_u32 v171,v6,4,4
v_lshrrev_b32 v172,8,v6
v_add_u32 v167,s90,v171
v_mul_u32_u24 v167,1280,v167
v_lshl_add_u32 v167,v172,4,v167
s_lshl_b32 s90,s88,4
s_add_u32 s90,s90,s89
s_lshl_b32 s90,s90,12
v_add_u32 v169,s90,v6
v_mov_b32 v64,0
v_mov_b32 v65,0
v_mov_b32 v66,0
v_mov_b32 v67,0
v_mov_b32 v68,0
v_mov_b32 v69,0
v_mov_b32 v70,0
v_mov_b32 v71,0
v_mov_b32 v72,0
v_mov_b32 v73,0
v_mov_b32 v74,0
v_mov_b32 v75,0
v_mov_b32 v76,0
v_mov_b32 v77,0
v_mov_b32 v78,0
v_mov_b32 v79,0
v_mov_b32 v80,0
v_mov_b32 v81,0
v_mov_b32 v82,0
v_mov_b32 v83,0
v_mov_b32 v84,0
v_mov_b32 v85,0
v_mov_b32 v86,0
v_mov_b32 v87,0
v_mov_b32 v88,0
v_mov_b32 v89,0
v_mov_b32 v90,0
v_mov_b32 v91,0
v_mov_b32 v92,0
v_mov_b32 v93,0
v_mov_b32 v94,0
v_mov_b32 v95,0
s_mov_b32 s90,0
L_sq_%=:
s_lshl_b32 s91,s90,6
buffer_load_dwordx4 v[160:163],v167,s[92:95],s91 offen
s_lshl_b32 s91,s90,18
buffer_load_ushort v112,v168,s[96:99],s91 offen
buffer_load_ushort v120,v168,s[96:99],s91 offen offset:8
buffer_load_ushort v128,v168,s[96:99],s91 offen offset:64
buffer_load_ushort v136,v168,s[96:99],s91 offen offset:72
s_add_u32 s91,s91,8192
buffer_load_ushort v113,v168,s[96:99],s91 offen
buffer_load_ushort v121,v168,s[96:99],s91 offen offset:8
buffer_load_ushort v129,v168,s[96:99],s91 offen offset:64
buffer_load_ushort v137,v168,s[96:99],s91 offen offset:72
s_add_u32 s91,s91,8192
buffer_load_ushort v114,v168,s[96:99],s91 offen
buffer_load_ushort v122,v168,s[96:99],s91 offen offset:8
buffer_load_ushort v130,v168,s[96:99],s91 offen offset:64
buffer_load_ushort v138,v168,s[96:99],s91 offen offset:72
s_add_u32 s91,s91,8192
buffer_load_ushort v115,v168,s[96:99],s91 offen
buffer_load_ushort v123,v168,s[96:99],s91 offen offset:8
buffer_load_ushort v131,v168,s[96:99],s91 offen offset:64
buffer_load_ushort v139,v168,s[96:99],s91 offen offset:72
s_add_u32 s91,s91,8192
buffer_load_ushort v116,v168,s[96:99],s91 offen
buffer_load_ushort v124,v168,s[96:99],s91 offen offset:8
buffer_load_ushort v132,v168,s[96:99],s91 offen offset:64
buffer_load_ushort v140,v168,s[96:99],s91 offen offset:72
s_add_u32 s91,s91,8192
buffer_load_ushort v117,v168,s[96:99],s91 offen
buffer_load_ushort v125,v168,s[96:99],s91 offen offset:8
buffer_load_ushort v133,v168,s[96:99],s91 offen offset:64
buffer_load_ushort v141,v168,s[96:99],s91 offen offset:72
s_add_u32 s91,s91,8192
buffer_load_ushort v118,v168,s[96:99],s91 offen
buffer_load_ushort v126,v168,s[96:99],s91 offen offset:8
buffer_load_ushort v134,v168,s[96:99],s91 offen offset:64
buffer_load_ushort v142,v168,s[96:99],s91 offen offset:72
s_add_u32 s91,s91,8192
buffer_load_ushort v119,v168,s[96:99],s91 offen
buffer_load_ushort v127,v168,s[96:99],s91 offen offset:8
buffer_load_ushort v135,v168,s[96:99],s91 offen offset:64
buffer_load_ushort v143,v168,s[96:99],s91 offen offset:72
s_add_u32 s91,s91,8192
s_waitcnt vmcnt(0)
v_lshl_or_b32 v112,v113,16,v112
v_lshl_or_b32 v113,v115,16,v114
v_lshl_or_b32 v114,v117,16,v116
v_lshl_or_b32 v115,v119,16,v118
v_lshl_or_b32 v120,v121,16,v120
v_lshl_or_b32 v121,v123,16,v122
v_lshl_or_b32 v122,v125,16,v124
v_lshl_or_b32 v123,v127,16,v126
v_lshl_or_b32 v128,v129,16,v128
v_lshl_or_b32 v129,v131,16,v130
v_lshl_or_b32 v130,v133,16,v132
v_lshl_or_b32 v131,v135,16,v134
v_lshl_or_b32 v136,v137,16,v136
v_lshl_or_b32 v137,v139,16,v138
v_lshl_or_b32 v138,v141,16,v140
v_lshl_or_b32 v139,v143,16,v142
s_nop 1
v_mfma_f32_16x16x32_bf16 v[64:67],v[112:115],v[160:163],v[64:67]
v_mfma_f32_16x16x32_bf16 v[68:71],v[120:123],v[160:163],v[68:71]
v_mfma_f32_16x16x32_bf16 v[72:75],v[128:131],v[160:163],v[72:75]
v_mfma_f32_16x16x32_bf16 v[76:79],v[136:139],v[160:163],v[76:79]
s_nop 7
s_lshl_b32 s91,s90,18
buffer_load_ushort v112,v168,s[96:99],s91 offen offset:128
buffer_load_ushort v120,v168,s[96:99],s91 offen offset:136
buffer_load_ushort v128,v168,s[96:99],s91 offen offset:192
buffer_load_ushort v136,v168,s[96:99],s91 offen offset:200
s_add_u32 s91,s91,8192
buffer_load_ushort v113,v168,s[96:99],s91 offen offset:128
buffer_load_ushort v121,v168,s[96:99],s91 offen offset:136
buffer_load_ushort v129,v168,s[96:99],s91 offen offset:192
buffer_load_ushort v137,v168,s[96:99],s91 offen offset:200
s_add_u32 s91,s91,8192
buffer_load_ushort v114,v168,s[96:99],s91 offen offset:128
buffer_load_ushort v122,v168,s[96:99],s91 offen offset:136
buffer_load_ushort v130,v168,s[96:99],s91 offen offset:192
buffer_load_ushort v138,v168,s[96:99],s91 offen offset:200
s_add_u32 s91,s91,8192
buffer_load_ushort v115,v168,s[96:99],s91 offen offset:128
buffer_load_ushort v123,v168,s[96:99],s91 offen offset:136
buffer_load_ushort v131,v168,s[96:99],s91 offen offset:192
buffer_load_ushort v139,v168,s[96:99],s91 offen offset:200
s_add_u32 s91,s91,8192
buffer_load_ushort v116,v168,s[96:99],s91 offen offset:128
buffer_load_ushort v124,v168,s[96:99],s91 offen offset:136
buffer_load_ushort v132,v168,s[96:99],s91 offen offset:192
buffer_load_ushort v140,v168,s[96:99],s91 offen offset:200
s_add_u32 s91,s91,8192
buffer_load_ushort v117,v168,s[96:99],s91 offen offset:128
buffer_load_ushort v125,v168,s[96:99],s91 offen offset:136
buffer_load_ushort v133,v168,s[96:99],s91 offen offset:192
buffer_load_ushort v141,v168,s[96:99],s91 offen offset:200
s_add_u32 s91,s91,8192
buffer_load_ushort v118,v168,s[96:99],s91 offen offset:128
buffer_load_ushort v126,v168,s[96:99],s91 offen offset:136
buffer_load_ushort v134,v168,s[96:99],s91 offen offset:192
buffer_load_ushort v142,v168,s[96:99],s91 offen offset:200
s_add_u32 s91,s91,8192
buffer_load_ushort v119,v168,s[96:99],s91 offen offset:128
buffer_load_ushort v127,v168,s[96:99],s91 offen offset:136
buffer_load_ushort v135,v168,s[96:99],s91 offen offset:192
buffer_load_ushort v143,v168,s[96:99],s91 offen offset:200
s_add_u32 s91,s91,8192
s_waitcnt vmcnt(0)
v_lshl_or_b32 v112,v113,16,v112
v_lshl_or_b32 v113,v115,16,v114
v_lshl_or_b32 v114,v117,16,v116
v_lshl_or_b32 v115,v119,16,v118
v_lshl_or_b32 v120,v121,16,v120
v_lshl_or_b32 v121,v123,16,v122
v_lshl_or_b32 v122,v125,16,v124
v_lshl_or_b32 v123,v127,16,v126
v_lshl_or_b32 v128,v129,16,v128
v_lshl_or_b32 v129,v131,16,v130
v_lshl_or_b32 v130,v133,16,v132
v_lshl_or_b32 v131,v135,16,v134
v_lshl_or_b32 v136,v137,16,v136
v_lshl_or_b32 v137,v139,16,v138
v_lshl_or_b32 v138,v141,16,v140
v_lshl_or_b32 v139,v143,16,v142
s_nop 1
v_mfma_f32_16x16x32_bf16 v[80:83],v[112:115],v[160:163],v[80:83]
v_mfma_f32_16x16x32_bf16 v[84:87],v[120:123],v[160:163],v[84:87]
v_mfma_f32_16x16x32_bf16 v[88:91],v[128:131],v[160:163],v[88:91]
v_mfma_f32_16x16x32_bf16 v[92:95],v[136:139],v[160:163],v[92:95]
s_nop 7
s_add_u32 s90,s90,1
s_cmp_lg_u32 s90,20
s_cbranch_scc1 L_sq_%=
s_nop 7
v_mul_f32 v64,s61,v64
v_mul_f32 v65,s61,v65
v_mul_f32 v66,s61,v66
v_mul_f32 v67,s61,v67
v_mul_f32 v68,s61,v68
v_mul_f32 v69,s61,v69
v_mul_f32 v70,s61,v70
v_mul_f32 v71,s61,v71
v_mul_f32 v72,s61,v72
v_mul_f32 v73,s61,v73
v_mul_f32 v74,s61,v74
v_mul_f32 v75,s61,v75
v_mul_f32 v76,s61,v76
v_mul_f32 v77,s61,v77
v_mul_f32 v78,s61,v78
v_mul_f32 v79,s61,v79
v_mul_f32 v80,s61,v80
v_mul_f32 v81,s61,v81
v_mul_f32 v82,s61,v82
v_mul_f32 v83,s61,v83
v_mul_f32 v84,s61,v84
v_mul_f32 v85,s61,v85
v_mul_f32 v86,s61,v86
v_mul_f32 v87,s61,v87
v_mul_f32 v88,s61,v88
v_mul_f32 v89,s61,v89
v_mul_f32 v90,s61,v90
v_mul_f32 v91,s61,v91
v_mul_f32 v92,s61,v92
v_mul_f32 v93,s61,v93
v_mul_f32 v94,s61,v94
v_mul_f32 v95,s61,v95
v_cvt_pk_bf16_f32 v96,v64,v65
v_cvt_pk_bf16_f32 v97,v66,v67
v_cvt_pk_bf16_f32 v98,v68,v69
v_cvt_pk_bf16_f32 v99,v70,v71
v_cvt_pk_bf16_f32 v100,v72,v73
v_cvt_pk_bf16_f32 v101,v74,v75
v_cvt_pk_bf16_f32 v102,v76,v77
v_cvt_pk_bf16_f32 v103,v78,v79
v_cvt_pk_bf16_f32 v104,v80,v81
v_cvt_pk_bf16_f32 v105,v82,v83
v_cvt_pk_bf16_f32 v106,v84,v85
v_cvt_pk_bf16_f32 v107,v86,v87
v_cvt_pk_bf16_f32 v108,v88,v89
v_cvt_pk_bf16_f32 v109,v90,v91
v_cvt_pk_bf16_f32 v110,v92,v93
v_cvt_pk_bf16_f32 v111,v94,v95
v_mov_b32 v170,0xff800000
s_lshl_b32 s91,s88,18
s_mov_b32 s90,64
L_s1_%=:
buffer_load_dwordx4 v[112:115],v164,s[20:23],s91 offen
buffer_load_dwordx4 v[116:119],v164,s[20:23],s91 offen offset:64
buffer_load_dwordx4 v[120:123],v164,s[20:23],s91 offen offset:128
buffer_load_dwordx4 v[124:127],v164,s[20:23],s91 offen offset:192
s_waitcnt vmcnt(0)
v_mfma_f32_16x16x32_bf16 v[144:147],v[112:115],v[96:99],0
v_mfma_f32_16x16x32_bf16 v[144:147],v[116:119],v[100:103],v[144:147]
v_mfma_f32_16x16x32_bf16 v[144:147],v[120:123],v[104:107],v[144:147]
v_mfma_f32_16x16x32_bf16 v[144:147],v[124:127],v[108:111],v[144:147]
s_nop 7
v_max3_f32 v170,v170,v144,v145
v_max3_f32 v170,v170,v146,v147
s_add_u32 s91,s91,4096
s_sub_u32 s90,s90,1
s_cmp_lg_u32 s90,0
s_cbranch_scc1 L_s1_%=
v_mov_b32 v171,v170
s_nop 1
v_permlane32_swap_b32 v170,v171
v_max_f32 v170,v170,v171
v_mov_b32 v171,v170
s_nop 1
v_permlane16_swap_b32 v170,v171
v_max_f32 v170,v170,v171
v_sub_f32 v152,0,v170
v_mov_b32 v156,0
v_sub_f32 v153,0,v170
v_mov_b32 v157,0
v_sub_f32 v154,0,v170
v_mov_b32 v158,0
v_sub_f32 v155,0,v170
v_mov_b32 v159,0
v_mov_b32 v64,0
v_mov_b32 v65,0
v_mov_b32 v66,0
v_mov_b32 v67,0
v_mov_b32 v68,0
v_mov_b32 v69,0
v_mov_b32 v70,0
v_mov_b32 v71,0
v_mov_b32 v72,0
v_mov_b32 v73,0
)A"
R"A(v_mov_b32 v74,0
v_mov_b32 v75,0
v_mov_b32 v76,0
v_mov_b32 v77,0
v_mov_b32 v78,0
v_mov_b32 v79,0
v_mov_b32 v80,0
v_mov_b32 v81,0
v_mov_b32 v82,0
v_mov_b32 v83,0
v_mov_b32 v84,0
v_mov_b32 v85,0
v_mov_b32 v86,0
v_mov_b32 v87,0
v_mov_b32 v88,0
v_mov_b32 v89,0
v_mov_b32 v90,0
v_mov_b32 v91,0
v_mov_b32 v92,0
v_mov_b32 v93,0
v_mov_b32 v94,0
v_mov_b32 v95,0
s_lshl_b32 s91,s88,18
s_mov_b32 s90,32
v_add_u32 v172,4096,v164
L_s2_%=:
buffer_load_dwordx4 v[112:115],v164,s[20:23],s91 offen
buffer_load_dwordx4 v[116:119],v164,s[20:23],s91 offen offset:64
buffer_load_dwordx4 v[120:123],v164,s[20:23],s91 offen offset:128
buffer_load_dwordx4 v[124:127],v164,s[20:23],s91 offen offset:192
buffer_load_dwordx4 v[128:131],v172,s[20:23],s91 offen
buffer_load_dwordx4 v[132:135],v172,s[20:23],s91 offen offset:64
buffer_load_dwordx4 v[136:139],v172,s[20:23],s91 offen offset:128
buffer_load_dwordx4 v[140:143],v172,s[20:23],s91 offen offset:192
s_waitcnt vmcnt(0)
v_mfma_f32_16x16x32_bf16 v[144:147],v[112:115],v[96:99],v[152:155]
v_mfma_f32_16x16x32_bf16 v[144:147],v[116:119],v[100:103],v[144:147]
v_mfma_f32_16x16x32_bf16 v[144:147],v[120:123],v[104:107],v[144:147]
v_mfma_f32_16x16x32_bf16 v[144:147],v[124:127],v[108:111],v[144:147]
v_mfma_f32_16x16x32_bf16 v[148:151],v[128:131],v[96:99],v[152:155]
v_mfma_f32_16x16x32_bf16 v[148:151],v[132:135],v[100:103],v[148:151]
v_mfma_f32_16x16x32_bf16 v[148:151],v[136:139],v[104:107],v[148:151]
v_mfma_f32_16x16x32_bf16 v[148:151],v[140:143],v[108:111],v[148:151]
s_nop 7
v_exp_f32 v144,v144
v_exp_f32 v145,v145
v_exp_f32 v146,v146
v_exp_f32 v147,v147
v_exp_f32 v148,v148
v_exp_f32 v149,v149
v_exp_f32 v150,v150
v_exp_f32 v151,v151
s_nop 0
v_cvt_pk_bf16_f32 v144,v144,v145
v_cvt_pk_bf16_f32 v145,v146,v147
v_cvt_pk_bf16_f32 v146,v148,v149
v_cvt_pk_bf16_f32 v147,v150,v151
buffer_load_ushort v112,v165,s[24:27],s91 offen
buffer_load_ushort v113,v165,s[24:27],s91 offen offset:256
buffer_load_ushort v114,v165,s[24:27],s91 offen offset:512
buffer_load_ushort v115,v165,s[24:27],s91 offen offset:768
buffer_load_ushort v116,v166,s[24:27],s91 offen
buffer_load_ushort v117,v166,s[24:27],s91 offen offset:256
buffer_load_ushort v118,v166,s[24:27],s91 offen offset:512
buffer_load_ushort v119,v166,s[24:27],s91 offen offset:768
buffer_load_ushort v120,v165,s[24:27],s91 offen offset:32
buffer_load_ushort v121,v165,s[24:27],s91 offen offset:288
buffer_load_ushort v122,v165,s[24:27],s91 offen offset:544
buffer_load_ushort v123,v165,s[24:27],s91 offen offset:800
buffer_load_ushort v124,v166,s[24:27],s91 offen offset:32
buffer_load_ushort v125,v166,s[24:27],s91 offen offset:288
buffer_load_ushort v126,v166,s[24:27],s91 offen offset:544
buffer_load_ushort v127,v166,s[24:27],s91 offen offset:800
buffer_load_ushort v128,v165,s[24:27],s91 offen offset:64
buffer_load_ushort v129,v165,s[24:27],s91 offen offset:320
buffer_load_ushort v130,v165,s[24:27],s91 offen offset:576
buffer_load_ushort v131,v165,s[24:27],s91 offen offset:832
buffer_load_ushort v132,v166,s[24:27],s91 offen offset:64
buffer_load_ushort v133,v166,s[24:27],s91 offen offset:320
buffer_load_ushort v134,v166,s[24:27],s91 offen offset:576
buffer_load_ushort v135,v166,s[24:27],s91 offen offset:832
buffer_load_ushort v136,v165,s[24:27],s91 offen offset:96
buffer_load_ushort v137,v165,s[24:27],s91 offen offset:352
buffer_load_ushort v138,v165,s[24:27],s91 offen offset:608
buffer_load_ushort v139,v165,s[24:27],s91 offen offset:864
buffer_load_ushort v140,v166,s[24:27],s91 offen offset:96
buffer_load_ushort v141,v166,s[24:27],s91 offen offset:352
buffer_load_ushort v142,v166,s[24:27],s91 offen offset:608
buffer_load_ushort v143,v166,s[24:27],s91 offen offset:864
s_waitcnt vmcnt(0)
v_lshl_or_b32 v112,v113,16,v112
v_lshl_or_b32 v113,v115,16,v114
v_lshl_or_b32 v114,v117,16,v116
v_lshl_or_b32 v115,v119,16,v118
v_lshl_or_b32 v120,v121,16,v120
v_lshl_or_b32 v121,v123,16,v122
v_lshl_or_b32 v122,v125,16,v124
v_lshl_or_b32 v123,v127,16,v126
v_lshl_or_b32 v128,v129,16,v128
v_lshl_or_b32 v129,v131,16,v130
v_lshl_or_b32 v130,v133,16,v132
v_lshl_or_b32 v131,v135,16,v134
v_lshl_or_b32 v136,v137,16,v136
v_lshl_or_b32 v137,v139,16,v138
v_lshl_or_b32 v138,v141,16,v140
v_lshl_or_b32 v139,v143,16,v142
s_nop 1
v_mfma_f32_16x16x32_bf16 v[64:67],v[112:115],v[144:147],v[64:67]
v_mfma_f32_16x16x32_bf16 v[68:71],v[120:123],v[144:147],v[68:71]
v_mfma_f32_16x16x32_bf16 v[72:75],v[128:131],v[144:147],v[72:75]
v_mfma_f32_16x16x32_bf16 v[76:79],v[136:139],v[144:147],v[76:79]
s_nop 7
buffer_load_ushort v112,v165,s[24:27],s91 offen offset:128
buffer_load_ushort v113,v165,s[24:27],s91 offen offset:384
buffer_load_ushort v114,v165,s[24:27],s91 offen offset:640
buffer_load_ushort v115,v165,s[24:27],s91 offen offset:896
buffer_load_ushort v116,v166,s[24:27],s91 offen offset:128
buffer_load_ushort v117,v166,s[24:27],s91 offen offset:384
buffer_load_ushort v118,v166,s[24:27],s91 offen offset:640
buffer_load_ushort v119,v166,s[24:27],s91 offen offset:896
buffer_load_ushort v120,v165,s[24:27],s91 offen offset:160
buffer_load_ushort v121,v165,s[24:27],s91 offen offset:416
buffer_load_ushort v122,v165,s[24:27],s91 offen offset:672
buffer_load_ushort v123,v165,s[24:27],s91 offen offset:928
buffer_load_ushort v124,v166,s[24:27],s91 offen offset:160
buffer_load_ushort v125,v166,s[24:27],s91 offen offset:416
buffer_load_ushort v126,v166,s[24:27],s91 offen offset:672
buffer_load_ushort v127,v166,s[24:27],s91 offen offset:928
buffer_load_ushort v128,v165,s[24:27],s91 offen offset:192
buffer_load_ushort v129,v165,s[24:27],s91 offen offset:448
buffer_load_ushort v130,v165,s[24:27],s91 offen offset:704
buffer_load_ushort v131,v165,s[24:27],s91 offen offset:960
buffer_load_ushort v132,v166,s[24:27],s91 offen offset:192
buffer_load_ushort v133,v166,s[24:27],s91 offen offset:448
buffer_load_ushort v134,v166,s[24:27],s91 offen offset:704
buffer_load_ushort v135,v166,s[24:27],s91 offen offset:960
buffer_load_ushort v136,v165,s[24:27],s91 offen offset:224
buffer_load_ushort v137,v165,s[24:27],s91 offen offset:480
buffer_load_ushort v138,v165,s[24:27],s91 offen offset:736
buffer_load_ushort v139,v165,s[24:27],s91 offen offset:992
buffer_load_ushort v140,v166,s[24:27],s91 offen offset:224
buffer_load_ushort v141,v166,s[24:27],s91 offen offset:480
buffer_load_ushort v142,v166,s[24:27],s91 offen offset:736
buffer_load_ushort v143,v166,s[24:27],s91 offen offset:992
s_waitcnt vmcnt(0)
v_lshl_or_b32 v112,v113,16,v112
v_lshl_or_b32 v113,v115,16,v114
v_lshl_or_b32 v114,v117,16,v116
v_lshl_or_b32 v115,v119,16,v118
v_lshl_or_b32 v120,v121,16,v120
v_lshl_or_b32 v121,v123,16,v122
v_lshl_or_b32 v122,v125,16,v124
v_lshl_or_b32 v123,v127,16,v126
v_lshl_or_b32 v128,v129,16,v128
v_lshl_or_b32 v129,v131,16,v130
v_lshl_or_b32 v130,v133,16,v132
v_lshl_or_b32 v131,v135,16,v134
v_lshl_or_b32 v136,v137,16,v136
v_lshl_or_b32 v137,v139,16,v138
v_lshl_or_b32 v138,v141,16,v140
v_lshl_or_b32 v139,v143,16,v142
s_nop 1
v_mfma_f32_16x16x32_bf16 v[80:83],v[112:115],v[144:147],v[80:83]
v_mfma_f32_16x16x32_bf16 v[84:87],v[120:123],v[144:147],v[84:87]
v_mfma_f32_16x16x32_bf16 v[88:91],v[128:131],v[144:147],v[88:91]
v_mfma_f32_16x16x32_bf16 v[92:95],v[136:139],v[144:147],v[92:95]
s_nop 7
v_mfma_f32_16x16x32_bf16 v[156:159],v[28:31],v[144:147],v[156:159]
s_nop 7
s_add_u32 s91,s91,8192
s_sub_u32 s90,s90,1
s_cmp_lg_u32 s90,0
s_cbranch_scc1 L_s2_%=
v_rcp_f32 v170,v156
s_nop 0
v_mul_f32 v64,v170,v64
v_mul_f32 v65,v170,v65
v_mul_f32 v66,v170,v66
v_mul_f32 v67,v170,v67
v_mul_f32 v68,v170,v68
v_mul_f32 v69,v170,v69
v_mul_f32 v70,v170,v70
v_mul_f32 v71,v170,v71
v_mul_f32 v72,v170,v72
v_mul_f32 v73,v170,v73
v_mul_f32 v74,v170,v74
v_mul_f32 v75,v170,v75
v_mul_f32 v76,v170,v76
v_mul_f32 v77,v170,v77
v_mul_f32 v78,v170,v78
v_mul_f32 v79,v170,v79
v_mul_f32 v80,v170,v80
v_mul_f32 v81,v170,v81
v_mul_f32 v82,v170,v82
v_mul_f32 v83,v170,v83
v_mul_f32 v84,v170,v84
v_mul_f32 v85,v170,v85
v_mul_f32 v86,v170,v86
v_mul_f32 v87,v170,v87
v_mul_f32 v88,v170,v88
v_mul_f32 v89,v170,v89
v_mul_f32 v90,v170,v90
v_mul_f32 v91,v170,v91
v_mul_f32 v92,v170,v92
v_mul_f32 v93,v170,v93
v_mul_f32 v94,v170,v94
v_mul_f32 v95,v170,v95
v_cvt_pk_bf16_f32 v96,v64,v65
v_cvt_pk_bf16_f32 v97,v66,v67
v_cvt_pk_bf16_f32 v98,v68,v69
v_cvt_pk_bf16_f32 v99,v70,v71
v_cvt_pk_bf16_f32 v100,v72,v73
v_cvt_pk_bf16_f32 v101,v74,v75
v_cvt_pk_bf16_f32 v102,v76,v77
v_cvt_pk_bf16_f32 v103,v78,v79
v_cvt_pk_bf16_f32 v104,v80,v81
v_cvt_pk_bf16_f32 v105,v82,v83
v_cvt_pk_bf16_f32 v106,v84,v85
v_cvt_pk_bf16_f32 v107,v86,v87
v_cvt_pk_bf16_f32 v108,v88,v89
v_cvt_pk_bf16_f32 v109,v90,v91
v_cvt_pk_bf16_f32 v110,v92,v93
v_cvt_pk_bf16_f32 v111,v94,v95
buffer_store_dwordx4 v[96:99],v169,s[36:39],s55 offen sc1
buffer_store_dwordx4 v[100:103],v169,s[36:39],s55 offen offset:1024 sc1
buffer_store_dwordx4 v[104:107],v169,s[36:39],s55 offen offset:2048 sc1
buffer_store_dwordx4 v[108:111],v169,s[36:39],s55 offen offset:3072 sc1
s_waitcnt vmcnt(0)
s_add_u32 s89,s89,1
s_cmp_lg_u32 s89,4
s_cbranch_scc1 L_sjb_%=
s_mov_b32 s98,s88
v_readlane_b32 s88,v173,0
v_readlane_b32 s89,v173,1
v_readlane_b32 s90,v173,2
v_readlane_b32 s91,v173,3
v_readlane_b32 s92,v173,4
v_readlane_b32 s93,v173,5
v_readlane_b32 s94,v173,6
v_readlane_b32 s95,v173,7
s_nop 4
s_mov_b32 s96,0
buffer_load_dwordx4 v[64:67],v6,s[56:59],s96 offen sc1
buffer_load_dwordx4 v[68:71],v6,s[56:59],s96 offen offset:1024 sc1
buffer_load_dwordx4 v[72:75],v6,s[56:59],s96 offen offset:2048 sc1
buffer_load_dwordx4 v[76:79],v6,s[56:59],s96 offen offset:3072 sc1
s_mov_b32 s96,4096
buffer_load_dwordx4 v[80:83],v6,s[56:59],s96 offen sc1
buffer_load_dwordx4 v[84:87],v6,s[56:59],s96 offen offset:1024 sc1
buffer_load_dwordx4 v[88:91],v6,s[56:59],s96 offen offset:2048 sc1
buffer_load_dwordx4 v[92:95],v6,s[56:59],s96 offen offset:3072 sc1
s_mov_b32 s96,8192
buffer_load_dwordx4 v[96:99],v6,s[56:59],s96 offen sc1
buffer_load_dwordx4 v[100:103],v6,s[56:59],s96 offen offset:1024 sc1
buffer_load_dwordx4 v[104:107],v6,s[56:59],s96 offen offset:2048 sc1
buffer_load_dwordx4 v[108:111],v6,s[56:59],s96 offen offset:3072 sc1
s_mov_b32 s96,12288
buffer_load_dwordx4 v[112:115],v6,s[56:59],s96 offen sc1
buffer_load_dwordx4 v[116:119],v6,s[56:59],s96 offen offset:1024 sc1
buffer_load_dwordx4 v[120:123],v6,s[56:59],s96 offen offset:2048 sc1
buffer_load_dwordx4 v[124:127],v6,s[56:59],s96 offen offset:3072 sc1
s_mov_b32 s96,16384
buffer_load_dwordx4 v[128:131],v6,s[56:59],s96 offen sc1
buffer_load_dwordx4 v[132:135],v6,s[56:59],s96 offen offset:1024 sc1
buffer_load_dwordx4 v[136:139],v6,s[56:59],s96 offen offset:2048 sc1
buffer_load_dwordx4 v[140:143],v6,s[56:59],s96 offen offset:3072 sc1
s_mov_b32 s96,20480
buffer_load_dwordx4 v[144:147],v6,s[56:59],s96 offen sc1
buffer_load_dwordx4 v[148:151],v6,s[56:59],s96 offen offset:1024 sc1
buffer_load_dwordx4 v[152:155],v6,s[56:59],s96 offen offset:2048 sc1
buffer_load_dwordx4 v[156:159],v6,s[56:59],s96 offen offset:3072 sc1
s_mov_b32 s96,24576
buffer_load_dwordx4 v[160:163],v6,s[56:59],s96 offen sc1
buffer_load_dwordx4 v[164:167],v6,s[56:59],s96 offen offset:1024 sc1
buffer_load_dwordx4 v[168:171],v6,s[56:59],s96 offen offset:2048 sc1
buffer_load_dwordx4 v[172:175],v6,s[56:59],s96 offen offset:3072 sc1
s_mov_b32 s96,28672
buffer_load_dwordx4 v[176:179],v6,s[56:59],s96 offen sc1
buffer_load_dwordx4 v[180:183],v6,s[56:59],s96 offen offset:1024 sc1
buffer_load_dwordx4 v[184:187],v6,s[56:59],s96 offen offset:2048 sc1
buffer_load_dwordx4 v[188:191],v6,s[56:59],s96 offen offset:3072 sc1
s_waitcnt vmcnt(0)
s_cmp_lg_u32 s98,0
s_cbranch_scc1 L_sr1_%=
buffer_load_dwordx4 v[64:67],v6,s[36:39],s84 offen sc1
buffer_load_dwordx4 v[68:71],v6,s[36:39],s84 offen offset:1024 sc1
buffer_load_dwordx4 v[72:75],v6,s[36:39],s84 offen offset:2048 sc1
buffer_load_dwordx4 v[76:79],v6,s[36:39],s84 offen offset:3072 sc1
buffer_load_dwordx4 v[80:83],v6,s[36:39],s85 offen sc1
buffer_load_dwordx4 v[84:87],v6,s[36:39],s85 offen offset:1024 sc1
buffer_load_dwordx4 v[88:91],v6,s[36:39],s85 offen offset:2048 sc1
buffer_load_dwordx4 v[92:95],v6,s[36:39],s85 offen offset:3072 sc1
buffer_load_dwordx4 v[96:99],v6,s[36:39],s86 offen sc1
)A"
R"A(buffer_load_dwordx4 v[100:103],v6,s[36:39],s86 offen offset:1024 sc1
buffer_load_dwordx4 v[104:107],v6,s[36:39],s86 offen offset:2048 sc1
buffer_load_dwordx4 v[108:111],v6,s[36:39],s86 offen offset:3072 sc1
buffer_load_dwordx4 v[112:115],v6,s[36:39],s87 offen sc1
buffer_load_dwordx4 v[116:119],v6,s[36:39],s87 offen offset:1024 sc1
buffer_load_dwordx4 v[120:123],v6,s[36:39],s87 offen offset:2048 sc1
buffer_load_dwordx4 v[124:127],v6,s[36:39],s87 offen offset:3072 sc1
s_waitcnt vmcnt(0)
s_nop 7
s_branch L_sret0_%=
L_sr1_%=:
buffer_load_dwordx4 v[132:135],v6,s[36:39],s84 offen sc1
buffer_load_dwordx4 v[140:143],v6,s[36:39],s84 offen offset:1024 sc1
buffer_load_dwordx4 v[148:151],v6,s[36:39],s84 offen offset:2048 sc1
buffer_load_dwordx4 v[156:159],v6,s[36:39],s84 offen offset:3072 sc1
buffer_load_dwordx4 v[164:167],v6,s[36:39],s85 offen sc1
buffer_load_dwordx4 v[172:175],v6,s[36:39],s85 offen offset:1024 sc1
buffer_load_dwordx4 v[180:183],v6,s[36:39],s85 offen offset:2048 sc1
buffer_load_dwordx4 v[188:191],v6,s[36:39],s85 offen offset:3072 sc1
buffer_load_dwordx4 v[196:199],v6,s[36:39],s86 offen sc1
buffer_load_dwordx4 v[204:207],v6,s[36:39],s86 offen offset:1024 sc1
buffer_load_dwordx4 v[212:215],v6,s[36:39],s86 offen offset:2048 sc1
buffer_load_dwordx4 v[220:223],v6,s[36:39],s86 offen offset:3072 sc1
buffer_load_dwordx4 v[228:231],v6,s[36:39],s87 offen sc1
buffer_load_dwordx4 v[236:239],v6,s[36:39],s87 offen offset:1024 sc1
buffer_load_dwordx4 v[244:247],v6,s[36:39],s87 offen offset:2048 sc1
buffer_load_dwordx4 v[252:255],v6,s[36:39],s87 offen offset:3072 sc1
s_waitcnt vmcnt(0)
s_nop 7
s_branch L_sret1_%=
L_end_%=:
)A"
    :: "s"(karg), "s"(blk), "s"(wid)
    : "v0", "v1", "v2", "v3", "v4", "v5", "v6", "v7", "v8", "v9", "v10", "v11", "v12", "v13", "v14", "v15", "v16", "v17", "v18", "v19", "v20", "v21", "v22", "v23", "v24", "v25", "v26", "v27", "v28", "v29", "v30", "v31", "v32", "v33", "v34", "v35", "v36", "v37", "v38", "v39", "v40", "v41", "v42", "v43", "v44", "v45", "v46", "v47", "v48", "v49", "v50", "v51", "v52", "v53", "v54", "v55", "v56", "v57", "v58", "v59", "v60", "v61", "v62", "v63", "v64", "v65", "v66", "v67", "v68", "v69", "v70", "v71", "v72", "v73", "v74", "v75", "v76", "v77", "v78", "v79", "v80", "v81", "v82", "v83", "v84", "v85", "v86", "v87", "v88", "v89", "v90", "v91", "v92", "v93", "v94", "v95", "v96", "v97", "v98", "v99", "v100", "v101", "v102", "v103", "v104", "v105", "v106", "v107", "v108", "v109", "v110", "v111", "v112", "v113", "v114", "v115", "v116", "v117", "v118", "v119", "v120", "v121", "v122", "v123", "v124", "v125", "v126", "v127", "v128", "v129", "v130", "v131", "v132", "v133", "v134", "v135", "v136", "v137", "v138", "v139", "v140", "v141", "v142", "v143", "v144", "v145", "v146", "v147", "v148", "v149", "v150", "v151", "v152", "v153", "v154", "v155", "v156", "v157", "v158", "v159", "v160", "v161", "v162", "v163", "v164", "v165", "v166", "v167", "v168", "v169", "v170", "v171", "v172", "v173", "v174", "v175", "v176", "v177", "v178", "v179", "v180", "v181", "v182", "v183", "v184", "v185", "v186", "v187", "v188", "v189", "v190", "v191", "v192", "v193", "v194", "v195", "v196", "v197", "v198", "v199", "v200", "v201", "v202", "v203", "v204", "v205", "v206", "v207", "v208", "v209", "v210", "v211", "v212", "v213", "v214", "v215", "v216", "v217", "v218", "v219", "v220", "v221", "v222", "v223", "v224", "v225", "v226", "v227", "v228", "v229", "v230", "v231", "v232", "v233", "v234", "v235", "v236", "v237", "v238", "v239", "v240", "v241", "v242", "v243", "v244", "v245", "v246", "v247", "v248", "v249", "v250", "v251", "v252", "v253", "v254", "v255", "a0", "a1", "a2", "a3", "a4", "a5", "a6", "a7", "a8", "a9", "a10", "a11", "a12", "a13", "a14", "a15", "a16", "a17", "a18", "a19", "a20", "a21", "a22", "a23", "a24", "a25", "a26", "a27", "a28", "a29", "a30", "a31", "a32", "a33", "a34", "a35", "a36", "a37", "a38", "a39", "a40", "a41", "a42", "a43", "a44", "a45", "a46", "a47", "a48", "a49", "a50", "a51", "a52", "a53", "a54", "a55", "a56", "a57", "a58", "a59", "a60", "a61", "a62", "a63", "a64", "a65", "a66", "a67", "a68", "a69", "a70", "a71", "a72", "a73", "a74", "a75", "a76", "a77", "a78", "a79", "a80", "a81", "a82", "a83", "a84", "a85", "a86", "a87", "a88", "a89", "a90", "a91", "a92", "a93", "a94", "a95", "a96", "a97", "a98", "a99", "a100", "a101", "a102", "a103", "a104", "a105", "a106", "a107", "a108", "a109", "a110", "a111", "a112", "a113", "a114", "a115", "a116", "a117", "a118", "a119", "a120", "a121", "a122", "a123", "a124", "a125", "a126", "a127", "a128", "a129", "a130", "a131", "a132", "a133", "a134", "a135", "a136", "a137", "a138", "a139", "a140", "a141", "a142", "a143", "a144", "a145", "a146", "a147", "a148", "a149", "a150", "a151", "a152", "a153", "a154", "a155", "a156", "a157", "a158", "a159", "a160", "a161", "a162", "a163", "a164", "a165", "a166", "a167", "a168", "a169", "a170", "a171", "a172", "a173", "a174", "a175", "a176", "a177", "a178", "a179", "a180", "a181", "a182", "a183", "a184", "a185", "a186", "a187", "a188", "a189", "a190", "a191", "a192", "a193", "a194", "a195", "a196", "a197", "a198", "a199", "a200", "a201", "a202", "a203", "a204", "a205", "a206", "a207", "a208", "a209", "a210", "a211", "a212", "a213", "a214", "a215", "a216", "a217", "a218", "a219", "a220", "a221", "a222", "a223", "a224", "a225", "a226", "a227", "a228", "a229", "a230", "a231", "a232", "a233", "a234", "a235", "a236", "a237", "a238", "a239", "a240", "a241", "a242", "a243", "a244", "a245", "a246", "a247", "a248", "a249", "a250", "a251", "a252", "a253", "a254", "a255", "s4", "s5", "s6", "s7", "s8", "s9", "s10", "s11", "s12", "s13", "s14", "s15", "s16", "s17", "s18", "s19", "s20", "s21", "s22", "s23", "s24", "s25", "s26", "s27", "s28", "s29", "s30", "s31", "s32", "s33", "s34", "s35", "s36", "s37", "s38", "s39", "s40", "s41", "s42", "s43", "s44", "s45", "s46", "s47", "s48", "s49", "s50", "s51", "s52", "s53", "s54", "s55", "s56", "s57", "s58", "s59", "s60", "s61", "s62", "s63", "s64", "s65", "s66", "s67", "s68", "s69", "s70", "s71", "s72", "s73", "s74", "s75", "s76", "s77", "s78", "s79", "s80", "s81", "s82", "s83", "s84", "s85", "s86", "s87", "s88", "s89", "s90", "s91", "s92", "s93", "s94", "s95", "s96", "s97", "s98", "s99", "s100", "s101", "vcc", "scc", "memory");
#endif
}

extern "C" void kernel_launch(void* const* d_in, const int* in_sizes, int n_in,
                              void* d_out, int out_size, void* d_ws, size_t ws_size,
                              hipStream_t stream) {
  (void)hipFuncSetAttribute((const void*)qattn_kernel, hipFuncAttributeMaxDynamicSharedMemorySize, LDS_ATT);
  qattn_kernel<<<256, 256, LDS_ATT, stream>>>(d_in[0], d_in[1], d_in[2], d_in[3], d_ws, d_in[4], d_out);
}
```

```cpp
#include <hip/hip_runtime.h>
#include <hip/hip_bf16.h>

constexpr int LDS_ATT = 163840;

__global__ __launch_bounds__(256, 1) void qattn_kernel(const void* __restrict__ x, const void* __restrict__ kc, const void* __restrict__ vc,
                                                       const void* __restrict__ wq, void* __restrict__ ws, const void* __restrict__ wo,
                                                       void* __restrict__ out) {
#if defined(__HIP_DEVICE_COMPILE__)
  const auto karg = __builtin_amdgcn_kernarg_segment_ptr();
  const unsigned blk = blockIdx.x;
  const unsigned wid = __builtin_amdgcn_readfirstlane(threadIdx.x >> 6);
  asm volatile(
R"A(s_memtime s[72:73]
s_memrealtime s[74:75]
s_load_dwordx8 s[4:11],%0,0x0
s_load_dwordx4 s[12:15],%0,0x20
s_load_dwordx2 s[16:17],%0,0x30
s_mov_b32 s40,%2
s_mov_b32 s43,%1
s_lshr_b32 s45,s43,3
v_mbcnt_lo_u32_b32 v12,-1,0
v_mbcnt_hi_u32_b32 v12,-1,v12
s_getpc_b64 s[88:89]
L_pc_%=:
s_and_b32 s88,s88,0xffffff80
s_and_b32 s89,s89,0xffff
s_mov_b32 s90,L_fast_end_%=-L_pc_%=
s_mov_b32 s91,0x20000
s_lshl_b32 s44,s45,2
s_add_u32 s44,s44,s40
s_mul_i32 s44,s44,896
v_lshlrev_b32 v67,7,v12
s_mov_b64 exec,127
buffer_load_dword v19,v67,s[88:91],s44 offen
s_mov_b64 exec,-1
s_and_b32 s70,s43,3
s_and_b32 s71,s45,3
s_lshl_b32 s42,s70,2
s_add_u32 s42,s42,s71
s_and_b32 s41,s43,4
s_lshl_b32 s41,s41,1
s_lshr_b32 s46,s45,2
s_add_u32 s41,s41,s46
s_lshl_b32 s56,s42,9
s_mov_b32 s57,0
s_lshl_b32 s59,s40,13
s_add_u32 s58,s59,65536
v_and_b32 v64,3,v12
v_bfe_u32 v65,v12,2,2
v_lshrrev_b32 v66,4,v12
v_lshrrev_b32 v75,3,v12
s_lshl_b32 s44,s40,6
v_add_u32 v75,s44,v75
v_mul_u32_u24 v75,1280,v75
v_and_b32 v76,7,v12
s_add_u32 s84,s70,1
s_and_b32 s84,s84,3
s_mul_i32 s84,s84,20480
s_add_u32 s84,s84,4096
s_add_u32 s85,s70,2
s_and_b32 s85,s85,3
s_mul_i32 s85,s85,20480
s_add_u32 s85,s85,2048
s_add_u32 s86,s70,3
s_and_b32 s86,s86,3
s_mul_i32 s86,s86,20480
s_add_u32 s86,s86,4096
s_add_u32 s87,s70,4
s_and_b32 s87,s87,3
s_mul_i32 s87,s87,20480
s_add_u32 s87,s87,2048
v_or_b32 v77,0,v66
v_xor_b32 v77,v77,v76
v_lshl_add_u32 v77,v77,4,v75
v_add_u32 v48,s84,v77
v_add_u32 v50,s85,v77
v_add_u32 v52,s86,v77
v_add_u32 v54,s87,v77
v_or_b32 v77,4,v66
v_xor_b32 v77,v77,v76
v_lshl_add_u32 v77,v77,4,v75
v_add_u32 v77,9216,v77
v_add_u32 v49,s84,v77
v_add_u32 v51,s85,v77
v_add_u32 v53,s86,v77
v_add_u32 v55,s87,v77
s_lshl_b32 s44,s40,17
v_lshlrev_b32 v75,13,v65
v_lshl_add_u32 v75,v66,6,v75
v_lshl_add_u32 v75,v64,4,v75
v_add_u32 v75,s44,v75
v_add_u32 v56,0,v75
v_add_u32 v57,256,v75
v_add_u32 v58,32768,v75
v_add_u32 v59,33024,v75
v_add_u32 v60,65536,v75
v_add_u32 v61,65792,v75
v_add_u32 v62,98304,v75
v_add_u32 v63,98560,v75
s_mul_i32 s44,s41,307200
s_waitcnt lgkmcnt(0)
s_mov_b64 s[28:29],s[4:5]
s_add_u32 s28,s28,s44
s_addc_u32 s29,s29,0
s_sub_u32 s28,s28,4096
s_subb_u32 s29,s29,0
s_and_b32 s29,s29,0xffff
s_mov_b32 s30,311296
s_mov_b32 s31,0x20000
s_mov_b64 s[32:33],s[10:11]
s_and_b32 s33,s33,0xffff
s_mov_b32 s34,5242880
s_mov_b32 s35,0x20000
s_add_u32 m0,s58,1024
s_nop 0
buffer_load_dwordx4 v57,s[32:35],s56 offen lds
s_add_u32 m0,s58,3072
s_nop 0
buffer_load_dwordx4 v59,s[32:35],s56 offen lds
s_add_u32 m0,s58,5120
s_nop 0
buffer_load_dwordx4 v61,s[32:35],s56 offen lds
s_add_u32 m0,s58,7168
s_nop 0
buffer_load_dwordx4 v63,s[32:35],s56 offen lds
s_add_u32 m0,s59,0
s_nop 0
buffer_load_dwordx4 v48,s[28:31],s57 offen lds
buffer_load_dwordx4 v49,s[28:31],s57 offen offset:1024 lds
buffer_load_dwordx4 v50,s[28:31],s57 offen offset:2048 lds
buffer_load_dwordx4 v51,s[28:31],s57 offen offset:3072 lds
s_add_u32 m0,s59,4096
s_nop 0
buffer_load_dwordx4 v52,s[28:31],s57 offen lds
buffer_load_dwordx4 v53,s[28:31],s57 offen offset:1024 lds
buffer_load_dwordx4 v54,s[28:31],s57 offen offset:2048 lds
buffer_load_dwordx4 v55,s[28:31],s57 offen offset:3072 lds
s_add_u32 s57,s57,128
s_xor_b32 s59,s59,131072
s_add_u32 m0,s58,0
s_nop 0
buffer_load_dwordx4 v56,s[32:35],s56 offen lds
s_add_u32 m0,s58,2048
s_nop 0
buffer_load_dwordx4 v58,s[32:35],s56 offen lds
s_add_u32 m0,s58,4096
s_nop 0
buffer_load_dwordx4 v60,s[32:35],s56 offen lds
s_add_u32 m0,s58,6144
s_nop 0
buffer_load_dwordx4 v62,s[32:35],s56 offen lds
s_add_u32 s56,s56,524288
s_add_u32 s58,s58,32768
s_cmp_ge_u32 s58,131072
s_cselect_b32 s62,98304,0
s_sub_u32 s58,s58,s62
s_add_u32 m0,s58,0
s_nop 0
buffer_load_dwordx4 v56,s[32:35],s56 offen lds
s_add_u32 m0,s58,1024
s_nop 0
buffer_load_dwordx4 v57,s[32:35],s56 offen lds
s_add_u32 m0,s58,2048
s_nop 0
buffer_load_dwordx4 v58,s[32:35],s56 offen lds
s_add_u32 m0,s58,3072
s_nop 0
buffer_load_dwordx4 v59,s[32:35],s56 offen lds
s_add_u32 m0,s58,4096
s_nop 0
buffer_load_dwordx4 v60,s[32:35],s56 offen lds
s_add_u32 m0,s58,5120
s_nop 0
buffer_load_dwordx4 v61,s[32:35],s56 offen lds
s_add_u32 m0,s58,6144
s_nop 0
buffer_load_dwordx4 v62,s[32:35],s56 offen lds
s_add_u32 m0,s58,7168
s_nop 0
buffer_load_dwordx4 v63,s[32:35],s56 offen lds
s_add_u32 s56,s56,524288
s_add_u32 s58,s58,32768
s_cmp_ge_u32 s58,131072
s_cselect_b32 s62,98304,0
s_sub_u32 s58,s58,s62
s_mov_b32 s60,65536
v_lshlrev_b32 v6,4,v12
v_lshlrev_b32 v72,12,v66
v_lshl_add_u32 v72,v65,6,v72
v_lshl_add_u32 v23,v64,4,v72
v_add_u32 v22,65536,v23
v_and_b32 v74,15,v12
v_lshrrev_b32 v76,1,v74
v_lshlrev_b32 v75,7,v74
s_lshl_b32 s44,s40,13
v_add_u32 v75,s44,v75
v_or_b32 v77,0,v66
v_xor_b32 v77,v77,v76
v_lshl_add_u32 v20,v77,4,v75
v_or_b32 v77,4,v66
v_xor_b32 v77,v77,v76
v_lshl_add_u32 v21,v77,4,v75
s_waitcnt vmcnt(12)
s_nop 0
s_memtime s[64:65]
s_memrealtime s[66:67]
s_bfe_u32 s68,%1,327683
s_waitcnt lgkmcnt(0)
s_sub_u32 s64,s64,s72
s_sub_u32 s66,s66,s74
s_mul_i32 s64,s64,10
s_mul_i32 s66,s66,215
s_mov_b32 s78,138
s_cmp_lt_u32 s64,s66
s_cselect_b32 s78,64,s78
s_cbranch_scc1 L_se1_%=
L_sg1_%=:
s_cmp_eq_u32 s68,0
s_cbranch_scc1 L_se1_%=
s_sleep 4
s_sub_u32 s68,s68,1
s_branch L_sg1_%=
L_se1_%=:
s_barrier
ds_read_b128 a[192:195],v20
ds_read_b64_tr_b16 a[128:129],v22 offset:1024
ds_read_b64_tr_b16 a[130:131],v22 offset:3072
ds_read_b128 a[196:199],v21
ds_read_b64_tr_b16 a[132:133],v22 offset:17408
ds_read_b64_tr_b16 a[134:135],v22 offset:19456
ds_read_b128 a[204:207],v21 offset:2048
ds_read_b128 a[200:203],v20 offset:2048
ds_read_b128 a[208:211],v20 offset:4096
ds_read_b128 a[212:215],v21 offset:4096
ds_read_b128 a[220:223],v21 offset:6144
ds_read_b128 a[216:219],v20 offset:6144
ds_read_b64_tr_b16 a[136:137],v22 offset:1032
ds_read_b64_tr_b16 a[138:139],v22 offset:3080
s_waitcnt lgkmcnt(11)
v_mfma_f32_16x16x32_bf16 v[128:131],a[128:131],a[192:195],0
s_add_u32 m0,s59,0
ds_read_b64_tr_b16 a[140:141],v22 offset:17416
ds_read_b64_tr_b16 a[142:143],v22 offset:19464
s_waitcnt lgkmcnt(10)
v_mfma_f32_16x16x32_bf16 v[128:131],a[132:135],a[196:199],v[128:131]
s_waitcnt lgkmcnt(9)
v_mfma_f32_16x16x32_bf16 v[136:139],a[132:135],a[204:207],0
s_waitcnt lgkmcnt(8)
v_mfma_f32_16x16x32_bf16 v[136:139],a[128:131],a[200:203],v[136:139]
s_waitcnt lgkmcnt(7)
v_mfma_f32_16x16x32_bf16 v[144:147],a[128:131],a[208:211],0
s_waitcnt lgkmcnt(6)
buffer_load_dwordx4 v48,s[28:31],s57 offen lds
v_mfma_f32_16x16x32_bf16 v[144:147],a[132:135],a[212:215],v[144:147]
s_waitcnt lgkmcnt(5)
v_mfma_f32_16x16x32_bf16 v[152:155],a[132:135],a[220:223],0
s_waitcnt lgkmcnt(4)
v_mfma_f32_16x16x32_bf16 v[152:155],a[128:131],a[216:219],v[152:155]
s_waitcnt lgkmcnt(2)
ds_read_b64_tr_b16 a[144:145],v22 offset:1280
ds_read_b64_tr_b16 a[146:147],v22 offset:3328
v_mfma_f32_16x16x32_bf16 v[132:135],a[136:139],a[192:195],0
s_waitcnt lgkmcnt(2)
ds_read_b64_tr_b16 a[148:149],v22 offset:17664
ds_read_b64_tr_b16 a[150:151],v22 offset:19712
buffer_load_dwordx4 v49,s[28:31],s57 offen offset:1024 lds
v_mfma_f32_16x16x32_bf16 v[132:135],a[140:143],a[196:199],v[132:135]
v_mfma_f32_16x16x32_bf16 v[140:143],a[140:143],a[204:207],0
v_mfma_f32_16x16x32_bf16 v[140:143],a[136:139],a[200:203],v[140:143]
v_mfma_f32_16x16x32_bf16 v[148:151],a[136:139],a[208:211],0
buffer_load_dwordx4 v50,s[28:31],s57 offen offset:2048 lds
v_mfma_f32_16x16x32_bf16 v[148:151],a[140:143],a[212:215],v[148:151]
v_mfma_f32_16x16x32_bf16 v[156:159],a[140:143],a[220:223],0
v_mfma_f32_16x16x32_bf16 v[156:159],a[136:139],a[216:219],v[156:159]
s_waitcnt lgkmcnt(2)
ds_read_b64_tr_b16 a[152:153],v22 offset:1288
ds_read_b64_tr_b16 a[154:155],v22 offset:3336
v_mfma_f32_16x16x32_bf16 v[160:163],a[144:147],a[192:195],0
s_waitcnt lgkmcnt(2)
buffer_load_dwordx4 v51,s[28:31],s57 offen offset:3072 lds
ds_read_b64_tr_b16 a[156:157],v22 offset:17672
ds_read_b64_tr_b16 a[158:159],v22 offset:19720
v_mfma_f32_16x16x32_bf16 v[160:163],a[148:151],a[196:199],v[160:163]
s_add_u32 m0,s59,4096
v_mfma_f32_16x16x32_bf16 v[168:171],a[148:151],a[204:207],0
v_mfma_f32_16x16x32_bf16 v[168:171],a[144:147],a[200:203],v[168:171]
v_mfma_f32_16x16x32_bf16 v[176:179],a[144:147],a[208:211],0
buffer_load_dwordx4 v52,s[28:31],s57 offen lds
v_mfma_f32_16x16x32_bf16 v[176:179],a[148:151],a[212:215],v[176:179]
v_mfma_f32_16x16x32_bf16 v[184:187],a[148:151],a[220:223],0
v_mfma_f32_16x16x32_bf16 v[184:187],a[144:147],a[216:219],v[184:187]
s_waitcnt lgkmcnt(2)
v_mfma_f32_16x16x32_bf16 v[164:167],a[152:155],a[192:195],0
s_waitcnt lgkmcnt(0)
buffer_load_dwordx4 v53,s[28:31],s57 offen offset:1024 lds
ds_read_b64_tr_b16 a[160:161],v22 offset:1536
ds_read_b64_tr_b16 a[162:163],v22 offset:3584
v_mfma_f32_16x16x32_bf16 v[164:167],a[156:159],a[196:199],v[164:167]
ds_read_b64_tr_b16 a[164:165],v22 offset:17920
ds_read_b64_tr_b16 a[166:167],v22 offset:19968
v_mfma_f32_16x16x32_bf16 v[172:175],a[156:159],a[204:207],0
v_mfma_f32_16x16x32_bf16 v[172:175],a[152:155],a[200:203],v[172:175]
v_mfma_f32_16x16x32_bf16 v[180:183],a[152:155],a[208:211],0
buffer_load_dwordx4 v54,s[28:31],s57 offen offset:2048 lds
v_mfma_f32_16x16x32_bf16 v[180:183],a[156:159],a[212:215],v[180:183]
v_mfma_f32_16x16x32_bf16 v[188:191],a[156:159],a[220:223],0
s_waitcnt vmcnt(15)
v_mfma_f32_16x16x32_bf16 v[188:191],a[152:155],a[216:219],v[188:191]
s_barrier
buffer_load_dwordx4 v55,s[28:31],s57 offen offset:3072 lds
ds_read_b64_tr_b16 a[168:169],v22 offset:1544
ds_read_b64_tr_b16 a[170:171],v22 offset:3592
s_waitcnt lgkmcnt(4)
v_mfma_f32_16x16x32_bf16 v[192:195],a[160:163],a[192:195],0
s_add_u32 m0,s58,0
ds_read_b64_tr_b16 a[172:173],v22 offset:17928
ds_read_b64_tr_b16 a[174:175],v22 offset:19976
s_waitcnt lgkmcnt(4)
v_mfma_f32_16x16x32_bf16 v[192:195],a[164:167],a[196:199],v[192:195]
v_mfma_f32_16x16x32_bf16 v[200:203],a[164:167],a[204:207],0
s_lshl_b32 s44,s41,13
s_add_u32 s44,s44,104857600
s_mov_b64 s[64:65],s[12:13]
v_mfma_f32_16x16x32_bf16 v[200:203],a[160:163],a[200:203],v[200:203]
s_mov_b32 s66,8192
buffer_load_dwordx4 v56,s[32:35],s56 offen lds
v_mfma_f32_16x16x32_bf16 v[208:211],a[160:163],a[208:211],0
s_add_u32 m0,s58,1024
s_add_u32 s64,s64,s44
s_addc_u32 s65,s65,0
v_mfma_f32_16x16x32_bf16 v[208:211],a[164:167],a[212:215],v[208:211]
s_and_b32 s65,s65,0xffff
s_mov_b32 s67,0x20000
v_mov_b32 v14,4096
v_mfma_f32_16x16x32_bf16 v[216:219],a[164:167],a[220:223],0
s_mov_b64 s[72:73],s[14:15]
s_and_b32 s73,s73,0xffff
ds_read_b64_tr_b16 a[176:177],v22 offset:1792
v_mfma_f32_16x16x32_bf16 v[216:219],a[160:163],a[216:219],v[216:219]
s_mov_b32 s74,5242880
s_mov_b32 s75,0x20000
ds_read_b64_tr_b16 a[178:179],v22 offset:3840
s_waitcnt lgkmcnt(4)
v_mfma_f32_16x16x32_bf16 v[196:199],a[168:171],a[192:195],0
s_waitcnt lgkmcnt(2)
buffer_load_dwordx4 v57,s[32:35],s56 offen lds
ds_read_b64_tr_b16 a[180:181],v22 offset:18176
ds_read_b64_tr_b16 a[182:183],v22 offset:20224
v_mfma_f32_16x16x32_bf16 v[196:199],a[172:175],a[196:199],v[196:199]
s_add_u32 m0,s58,2048
v_lshlrev_b32 v15,3,v6
v_add_u32 v16,128,v15
v_mfma_f32_16x16x32_bf16 v[204:207],a[172:175],a[204:207],0
s_lshl_b32 s83,%1,2
ds_read_b64_tr_b16 a[184:185],v22 offset:1800
v_lshrrev_b32 v16,10,v16
v_mfma_f32_16x16x32_bf16 v[204:207],a[168:171],a[200:203],v[204:207]
s_add_u32 s83,s83,s40
ds_read_b64_tr_b16 a[186:187],v22 offset:3848
v_lshl_add_u32 v15,v16,24,v15
v_mfma_f32_16x16x32_bf16 v[212:215],a[168:171],a[208:211],0
s_mul_i32 s83,s83,5120
buffer_load_dwordx4 v58,s[32:35],s56 offen lds
v_mfma_f32_16x16x32_bf16 v[212:215],a[172:175],a[212:215],v[212:215]
s_add_u32 m0,s58,3072
s_lshl_b32 s43,s41,5
ds_read_b64_tr_b16 a[188:189],v22 offset:18184
v_mfma_f32_16x16x32_bf16 v[220:223],a[172:175],a[220:223],0
s_lshl_b32 s44,s42,1
s_add_u32 s43,s43,s44
ds_read_b64_tr_b16 a[190:191],v22 offset:20232
v_mfma_f32_16x16x32_bf16 v[220:223],a[168:171],a[216:219],v[220:223]
s_lshl_b32 s44,s43,18
s_mov_b64 s[20:21],s[6:7]
ds_read_b64_tr_b16 a[128:129],v22
s_waitcnt lgkmcnt(7)
v_mfma_f32_16x16x32_bf16 v[224:227],a[176:179],a[192:195],0
s_mov_b32 s22,524288
buffer_load_dwordx4 v59,s[32:35],s56 offen lds
s_waitcnt lgkmcnt(5)
v_mfma_f32_16x16x32_bf16 v[224:227],a[180:183],a[196:199],v[224:227]
)A"
R"A(s_add_u32 m0,s58,4096
s_mov_b32 s23,0x20000
ds_read_b64_tr_b16 a[130:131],v22 offset:2048
v_mfma_f32_16x16x32_bf16 v[232:235],a[180:183],a[204:207],0
s_add_u32 s20,s20,s44
s_addc_u32 s21,s21,0
ds_read_b64_tr_b16 a[132:133],v22 offset:16384
v_mfma_f32_16x16x32_bf16 v[232:235],a[176:179],a[200:203],v[232:235]
s_and_b32 s21,s21,0xffff
s_mov_b64 s[24:25],s[8:9]
ds_read_b64_tr_b16 a[134:135],v22 offset:18432
v_mfma_f32_16x16x32_bf16 v[240:243],a[176:179],a[208:211],0
s_mov_b32 s26,524288
buffer_load_dwordx4 v60,s[32:35],s56 offen lds
v_mfma_f32_16x16x32_bf16 v[240:243],a[180:183],a[212:215],v[240:243]
s_add_u32 s24,s24,s44
s_addc_u32 s25,s25,0
ds_read_b64_tr_b16 a[136:137],v22 offset:8
v_mfma_f32_16x16x32_bf16 v[248:251],a[180:183],a[220:223],0
s_add_u32 m0,s58,5120
s_and_b32 s25,s25,0xffff
ds_read_b64_tr_b16 a[138:139],v22 offset:2056
v_mfma_f32_16x16x32_bf16 v[248:251],a[176:179],a[216:219],v[248:251]
s_mov_b32 s27,0x20000
s_lshl_b32 s44,s43,16
ds_read_b64_tr_b16 a[140:141],v22 offset:16392
s_waitcnt lgkmcnt(6)
v_mfma_f32_16x16x32_bf16 v[228:231],a[184:187],a[192:195],0
s_xor_b32 s59,s59,131072
s_mov_b64 s[36:37],s[12:13]
ds_read_b64_tr_b16 a[142:143],v22 offset:18440
v_mfma_f32_16x16x32_bf16 v[228:231],a[188:191],a[196:199],v[228:231]
s_add_u32 s57,s57,128
buffer_load_dwordx4 v61,s[32:35],s56 offen lds
v_mfma_f32_16x16x32_bf16 v[236:239],a[188:191],a[204:207],0
s_add_u32 s36,s36,s44
s_addc_u32 s37,s37,0
ds_read_b64_tr_b16 a[144:145],v22 offset:256
v_mfma_f32_16x16x32_bf16 v[236:239],a[184:187],a[200:203],v[236:239]
s_add_u32 m0,s58,6144
s_and_b32 s37,s37,0xffff
ds_read_b64_tr_b16 a[146:147],v22 offset:2304
v_mfma_f32_16x16x32_bf16 v[244:247],a[184:187],a[208:211],0
s_mov_b32 s38,131072
s_mov_b32 s39,0x20000
ds_read_b64_tr_b16 a[148:149],v22 offset:16640
v_mfma_f32_16x16x32_bf16 v[244:247],a[188:191],a[212:215],v[244:247]
s_lshl_b32 s51,s40,12
s_add_u32 s52,s51,16384
ds_read_b64_tr_b16 a[150:151],v22 offset:18688
v_mfma_f32_16x16x32_bf16 v[252:255],a[188:191],a[220:223],0
s_mul_i32 s47,s41,5
buffer_load_dwordx4 v62,s[32:35],s56 offen lds
v_mfma_f32_16x16x32_bf16 v[252:255],a[184:187],a[216:219],v[252:255]
s_add_u32 s47,s47,s42
s_and_b32 s47,s47,15
ds_read_b64_tr_b16 a[152:153],v22 offset:264
s_waitcnt lgkmcnt(6)
v_mfma_f32_16x16x32_bf16 a[0:3],a[128:131],a[192:195],0
s_add_u32 m0,s58,7168
s_lshl_b32 s47,s47,14
ds_read_b64_tr_b16 a[154:155],v22 offset:2312
v_mfma_f32_16x16x32_bf16 a[0:3],a[132:135],a[196:199],a[0:3]
s_mov_b32 s54,0
s_lshl_b32 s55,s40,14
ds_read_b64_tr_b16 a[156:157],v22 offset:16648
v_mfma_f32_16x16x32_bf16 a[32:35],a[132:135],a[204:207],0
s_mov_b32 s61,0x3e0293ee
s_mov_b32 s63,0x5d800000
ds_read_b64_tr_b16 a[158:159],v22 offset:18696
v_mfma_f32_16x16x32_bf16 a[32:35],a[128:131],a[200:203],a[32:35]
s_lshl_b32 s44,s40,2
buffer_load_dwordx4 v63,s[32:35],s56 offen lds
v_mfma_f32_16x16x32_bf16 a[64:67],a[128:131],a[208:211],0
s_add_u32 s44,s44,s70
s_mov_b32 s46,0xff800000
ds_read_b64_tr_b16 a[160:161],v22 offset:512
v_mfma_f32_16x16x32_bf16 a[64:67],a[132:135],a[212:215],a[64:67]
s_add_u32 m0,s59,0
ds_read_b64_tr_b16 a[162:163],v22 offset:2560
v_lshlrev_b32 v70,8,v65
v_mfma_f32_16x16x32_bf16 a[96:99],a[132:135],a[220:223],0
s_cmp_eq_u32 s44,14
s_cselect_b32 s18,0x7149f2ca,s46
ds_read_b64_tr_b16 a[164:165],v22 offset:16896
v_mfma_f32_16x16x32_bf16 a[96:99],a[128:131],a[216:219],a[96:99]
s_cmp_eq_u32 s44,13
s_cselect_b32 s19,0x7149f2ca,s46
ds_read_b64_tr_b16 a[166:167],v22 offset:18944
v_mfma_f32_16x16x32_bf16 a[4:7],a[136:139],a[192:195],0
s_add_u32 s60,s60,32768
buffer_load_dwordx4 v48,s[28:31],s57 offen lds
s_waitcnt lgkmcnt(6)
v_mfma_f32_16x16x32_bf16 a[4:7],a[140:143],a[196:199],a[4:7]
s_cmp_eq_u32 s44,12
s_cselect_b32 s100,0x7149f2ca,s46
ds_read_b64_tr_b16 a[168:169],v22 offset:520
v_mfma_f32_16x16x32_bf16 a[36:39],a[140:143],a[204:207],0
s_cmp_eq_u32 s44,15
s_cselect_b32 s101,0x7149f2ca,s46
ds_read_b64_tr_b16 a[170:171],v22 offset:2568
v_mfma_f32_16x16x32_bf16 a[36:39],a[136:139],a[200:203],a[36:39]
s_add_u32 s58,s58,32768
ds_read_b64_tr_b16 a[172:173],v22 offset:16904
v_lshl_add_u32 v70,v66,6,v70
v_mfma_f32_16x16x32_bf16 a[68:71],a[136:139],a[208:211],0
s_mul_i32 s81,s70,1310720
ds_read_b64_tr_b16 a[174:175],v22 offset:18952
v_add_u32 v70,s51,v70
v_mfma_f32_16x16x32_bf16 a[68:71],a[140:143],a[212:215],a[68:71]
s_mul_i32 s44,s71,320
buffer_load_dwordx4 v49,s[28:31],s57 offen offset:1024 lds
v_mfma_f32_16x16x32_bf16 a[100:103],a[140:143],a[220:223],0
s_add_u32 s81,s81,s44
ds_read_b64_tr_b16 a[176:177],v22 offset:768
v_lshl_add_u32 v4,v64,4,v70
v_mfma_f32_16x16x32_bf16 a[100:103],a[136:139],a[216:219],a[100:103]
s_mul_i32 s82,s40,10240
ds_read_b64_tr_b16 a[178:179],v22 offset:2816
v_xor_b32 v71,2,v64
v_mfma_f32_16x16x32_bf16 a[8:11],a[144:147],a[192:195],0
s_add_u32 s56,s56,524288
ds_read_b64_tr_b16 a[180:181],v22 offset:17152
v_lshl_add_u32 v5,v71,4,v70
v_mfma_f32_16x16x32_bf16 a[8:11],a[148:151],a[196:199],a[8:11]
s_mul_i32 s44,s40,40960
ds_read_b64_tr_b16 a[182:183],v22 offset:19200
v_lshlrev_b32 v72,10,v65
v_mfma_f32_16x16x32_bf16 a[40:43],a[148:151],a[204:207],0
s_lshr_b32 s92,%1,3
buffer_load_dwordx4 v50,s[28:31],s57 offen offset:2048 lds
v_mfma_f32_16x16x32_bf16 a[40:43],a[144:147],a[200:203],a[40:43]
s_lshl_b32 s92,s92,2
ds_read_b64_tr_b16 a[184:185],v22 offset:776
v_lshl_add_u32 v72,v64,6,v72
v_mfma_f32_16x16x32_bf16 a[72:75],a[144:147],a[208:211],0
s_add_u32 s92,s92,s40
ds_read_b64_tr_b16 a[186:187],v22 offset:2824
v_and_b32 v73,2,v65
v_mfma_f32_16x16x32_bf16 a[72:75],a[148:151],a[212:215],a[72:75]
s_lshl_b32 s92,s92,7
ds_read_b64_tr_b16 a[188:189],v22 offset:17160
v_xor_b32 v73,v73,v66
v_mfma_f32_16x16x32_bf16 a[104:107],a[148:151],a[220:223],0
s_add_u32 s93,s92,L_tailseg_%=-L_pc_%=
ds_read_b64_tr_b16 a[190:191],v22 offset:19208
v_lshl_add_u32 v72,v73,4,v72
v_mfma_f32_16x16x32_bf16 a[104:107],a[144:147],a[216:219],a[104:107]
buffer_load_dwordx4 v51,s[28:31],s57 offen offset:3072 lds
v_mfma_f32_16x16x32_bf16 a[12:15],a[152:155],a[192:195],0
s_waitcnt lgkmcnt(5)
v_add_u32 v1,98304,v72
v_lshlrev_b32 v72,10,v66
v_mfma_f32_16x16x32_bf16 a[12:15],a[156:159],a[196:199],a[12:15]
s_add_u32 m0,s59,4096
v_lshl_add_u32 v72,v65,6,v72
v_lshl_add_u32 v72,v64,3,v72
v_mfma_f32_16x16x32_bf16 a[44:47],a[156:159],a[204:207],0
s_xor_b32 s59,s59,131072
v_add_u32 v72,114688,v72
v_and_b32 v73,1,v66
v_mfma_f32_16x16x32_bf16 a[44:47],a[152:155],a[200:203],a[44:47]
s_cmp_eq_u32 s60,131072
s_cselect_b32 s60,32768,s60
v_lshl_add_u32 v2,v73,5,v72
v_mfma_f32_16x16x32_bf16 a[76:79],a[152:155],a[208:211],0
buffer_load_dwordx4 v52,s[28:31],s57 offen lds
v_mfma_f32_16x16x32_bf16 a[76:79],a[156:159],a[212:215],a[76:79]
v_xor_b32 v73,1,v73
v_add_u32 v22,s60,v23
v_mfma_f32_16x16x32_bf16 a[108:111],a[156:159],a[220:223],0
s_add_u32 s60,s60,32768
v_xor_b32 v20,131072,v20
v_xor_b32 v21,131072,v21
v_mfma_f32_16x16x32_bf16 a[108:111],a[152:155],a[216:219],a[108:111]
s_waitcnt vmcnt(13) lgkmcnt(0)
v_lshl_add_u32 v3,v73,5,v72
v_mov_b32 v73,0x3f803f80
v_mfma_f32_16x16x32_bf16 a[16:19],a[160:163],a[192:195],0
s_barrier
buffer_load_dwordx4 v53,s[28:31],s57 offen offset:1024 lds
v_mfma_f32_16x16x32_bf16 a[16:19],a[164:167],a[196:199],a[16:19]
s_cmp_ge_u32 s58,131072
s_cselect_b32 s62,98304,0
ds_read_b128 a[224:227],v20
v_mfma_f32_16x16x32_bf16 a[48:51],a[164:167],a[204:207],0
s_sub_u32 s58,s58,s62
ds_read_b64_tr_b16 a[128:129],v22 offset:1024
v_add_u32 v70,0,v12
v_mfma_f32_16x16x32_bf16 a[48:51],a[160:163],a[200:203],a[48:51]
ds_read_b64_tr_b16 a[130:131],v22 offset:3072
v_mul_u32_u24 v71,3277,v70
v_mfma_f32_16x16x32_bf16 a[80:83],a[160:163],a[208:211],0
ds_read_b128 a[228:231],v21
v_lshrrev_b32 v71,16,v71
v_mfma_f32_16x16x32_bf16 a[80:83],a[164:167],a[212:215],a[80:83]
buffer_load_dwordx4 v54,s[28:31],s57 offen offset:2048 lds
v_mfma_f32_16x16x32_bf16 a[112:115],a[164:167],a[220:223],0
ds_read_b64_tr_b16 a[132:133],v22 offset:17408
v_mul_u32_u24 v72,20,v71
v_mfma_f32_16x16x32_bf16 a[112:115],a[160:163],a[216:219],a[112:115]
ds_read_b64_tr_b16 a[134:135],v22 offset:19456
v_sub_u32 v72,v70,v72
v_mfma_f32_16x16x32_bf16 a[20:23],a[168:171],a[192:195],0
ds_read_b128 a[232:235],v20 offset:2048
v_add_u32 v70,64,v12
v_mfma_f32_16x16x32_bf16 a[20:23],a[172:175],a[196:199],a[20:23]
ds_read_b128 a[236:239],v21 offset:2048
v_lshlrev_b32 v24,10,v6
v_mfma_f32_16x16x32_bf16 a[52:55],a[172:175],a[204:207],0
buffer_load_dwordx4 v55,s[28:31],s57 offen offset:3072 lds
v_mfma_f32_16x16x32_bf16 a[52:55],a[168:171],a[200:203],a[52:55]
s_add_u32 s57,s57,128
ds_read_b128 a[240:243],v20 offset:4096
v_mfma_f32_16x16x32_bf16 a[84:87],a[168:171],a[208:211],0
s_cmp_eq_u32 s60,131072
s_cselect_b32 s60,32768,s60
ds_read_b128 a[244:247],v21 offset:4096
v_mfma_f32_16x16x32_bf16 a[84:87],a[172:175],a[212:215],a[84:87]
ds_read_b128 a[248:251],v20 offset:6144
v_xor_b32 v20,131072,v20
v_mfma_f32_16x16x32_bf16 a[116:119],a[172:175],a[220:223],0
buffer_load_dword v13,v14,s[64:67],0 offen sc1
v_mfma_f32_16x16x32_bf16 a[116:119],a[168:171],a[216:219],a[116:119]
ds_read_b128 a[252:255],v21 offset:6144
v_xor_b32 v21,131072,v21
v_mfma_f32_16x16x32_bf16 a[24:27],a[176:179],a[192:195],0
ds_read_b64_tr_b16 a[136:137],v22 offset:1032
v_mfma_f32_16x16x32_bf16 a[24:27],a[180:183],a[196:199],a[24:27]
ds_read_b64_tr_b16 a[138:139],v22 offset:3080
v_mfma_f32_16x16x32_bf16 a[56:59],a[180:183],a[204:207],0
ds_read_b64_tr_b16 a[140:141],v22 offset:17416
v_mfma_f32_16x16x32_bf16 a[56:59],a[176:179],a[200:203],a[56:59]
ds_read_b64_tr_b16 a[142:143],v22 offset:19464
v_mfma_f32_16x16x32_bf16 a[88:91],a[176:179],a[208:211],0
ds_read_b64_tr_b16 a[144:145],v22 offset:1280
v_mfma_f32_16x16x32_bf16 a[88:91],a[180:183],a[212:215],a[88:91]
ds_read_b64_tr_b16 a[146:147],v22 offset:3328
v_mfma_f32_16x16x32_bf16 a[120:123],a[180:183],a[220:223],0
ds_read_b64_tr_b16 a[148:149],v22 offset:17664
v_mfma_f32_16x16x32_bf16 a[120:123],a[176:179],a[216:219],a[120:123]
ds_read_b64_tr_b16 a[150:151],v22 offset:19712
v_mfma_f32_16x16x32_bf16 a[28:31],a[184:187],a[192:195],0
ds_read_b64_tr_b16 a[152:153],v22 offset:1288
v_mfma_f32_16x16x32_bf16 a[28:31],a[188:191],a[196:199],a[28:31]
ds_read_b64_tr_b16 a[154:155],v22 offset:3336
ds_read_b64_tr_b16 a[156:157],v22 offset:17672
ds_read_b64_tr_b16 a[158:159],v22 offset:19720
v_mfma_f32_16x16x32_bf16 a[60:63],a[188:191],a[204:207],0
v_cmp_eq_u32 vcc,0,v64
s_nop 1
v_cndmask_b32 v28,0,v73,vcc
v_mfma_f32_16x16x32_bf16 a[60:63],a[184:187],a[200:203],a[60:63]
v_mov_b32 v29,v28
v_mov_b32 v30,v28
v_mfma_f32_16x16x32_bf16 a[92:95],a[184:187],a[208:211],0
v_mov_b32 v31,v28
v_bfe_u32 v73,v71,2,1
v_mfma_f32_16x16x32_bf16 a[92:95],a[188:191],a[212:215],a[92:95]
v_lshlrev_b32 v73,1,v73
v_xor_b32 v72,v72,v73
v_mfma_f32_16x16x32_bf16 a[124:127],a[188:191],a[220:223],0
s_add_u32 m0,s58,0
v_mul_u32_u24 v71,1280,v71
v_lshl_add_u32 v71,v72,4,v71
v_mfma_f32_16x16x32_bf16 a[124:127],a[184:187],a[216:219],a[124:127]
s_waitcnt lgkmcnt(4)
v_add_u32 v71,s44,v71
v_subrev_u32 v7,0,v71
v_mfma_f32_16x16x32_bf16 v[128:131],a[128:131],a[224:227],v[128:131]
v_mul_u32_u24 v71,3277,v70
v_lshrrev_b32 v71,16,v71
v_mfma_f32_16x16x32_bf16 v[128:131],a[132:135],a[228:231],v[128:131]
buffer_load_dwordx4 v56,s[32:35],s56 offen lds
v_mfma_f32_16x16x32_bf16 v[136:139],a[132:135],a[236:239],v[136:139]
ds_read_b64_tr_b16 a[160:161],v22 offset:1536
v_mul_u32_u24 v72,20,v71
v_mfma_f32_16x16x32_bf16 v[136:139],a[128:131],a[232:235],v[136:139]
s_add_u32 m0,s58,1024
ds_read_b64_tr_b16 a[162:163],v22 offset:3584
v_sub_u32 v72,v70,v72
v_mfma_f32_16x16x32_bf16 v[144:147],a[128:131],a[240:243],v[144:147]
ds_read_b64_tr_b16 a[164:165],v22 offset:17920
v_bfe_u32 v73,v71,2,1
v_mfma_f32_16x16x32_bf16 v[144:147],a[132:135],a[244:247],v[144:147]
ds_read_b64_tr_b16 a[166:167],v22 offset:19968
v_lshlrev_b32 v73,1,v73
v_mfma_f32_16x16x32_bf16 v[152:155],a[132:135],a[252:255],v[152:155]
buffer_load_dwordx4 v57,s[32:35],s56 offen lds
v_mfma_f32_16x16x32_bf16 v[152:155],a[128:131],a[248:251],v[152:155]
v_xor_b32 v72,v72,v73
v_mul_u32_u24 v71,1280,v71
v_mfma_f32_16x16x32_bf16 v[132:135],a[136:139],a[224:227],v[132:135]
)A"
R"A(v_lshl_add_u32 v71,v72,4,v71
v_add_u32 v71,s44,v71
v_mfma_f32_16x16x32_bf16 v[132:135],a[140:143],a[228:231],v[132:135]
s_add_u32 m0,s58,2048
ds_read_b64_tr_b16 a[168:169],v22 offset:1544
v_subrev_u32 v8,1024,v71
v_mfma_f32_16x16x32_bf16 v[140:143],a[140:143],a[236:239],v[140:143]
ds_read_b64_tr_b16 a[170:171],v22 offset:3592
v_add_u32 v70,128,v12
v_mfma_f32_16x16x32_bf16 v[140:143],a[136:139],a[232:235],v[140:143]
ds_read_b64_tr_b16 a[172:173],v22 offset:17928
v_mul_u32_u24 v71,3277,v70
v_mfma_f32_16x16x32_bf16 v[148:151],a[136:139],a[240:243],v[148:151]
buffer_load_dwordx4 v58,s[32:35],s56 offen lds
v_mfma_f32_16x16x32_bf16 v[148:151],a[140:143],a[244:247],v[148:151]
ds_read_b64_tr_b16 a[174:175],v22 offset:19976
v_lshrrev_b32 v71,16,v71
v_mfma_f32_16x16x32_bf16 v[156:159],a[140:143],a[252:255],v[156:159]
v_mul_u32_u24 v72,20,v71
v_sub_u32 v72,v70,v72
v_mfma_f32_16x16x32_bf16 v[156:159],a[136:139],a[248:251],v[156:159]
s_add_u32 m0,s58,3072
ds_read_b64_tr_b16 a[176:177],v22 offset:1792
v_bfe_u32 v73,v71,2,1
v_mfma_f32_16x16x32_bf16 v[160:163],a[144:147],a[224:227],v[160:163]
ds_read_b64_tr_b16 a[178:179],v22 offset:3840
v_lshlrev_b32 v73,1,v73
v_mfma_f32_16x16x32_bf16 v[160:163],a[148:151],a[228:231],v[160:163]
ds_read_b64_tr_b16 a[180:181],v22 offset:18176
v_xor_b32 v72,v72,v73
v_mfma_f32_16x16x32_bf16 v[168:171],a[148:151],a[236:239],v[168:171]
buffer_load_dwordx4 v59,s[32:35],s56 offen lds
v_mfma_f32_16x16x32_bf16 v[168:171],a[144:147],a[232:235],v[168:171]
ds_read_b64_tr_b16 a[182:183],v22 offset:20224
v_mul_u32_u24 v71,1280,v71
v_mfma_f32_16x16x32_bf16 v[176:179],a[144:147],a[240:243],v[176:179]
s_add_u32 m0,s58,4096
v_lshl_add_u32 v71,v72,4,v71
v_add_u32 v71,s44,v71
v_mfma_f32_16x16x32_bf16 v[176:179],a[148:151],a[244:247],v[176:179]
ds_read_b64_tr_b16 a[184:185],v22 offset:1800
v_subrev_u32 v9,2048,v71
v_mfma_f32_16x16x32_bf16 v[184:187],a[148:151],a[252:255],v[184:187]
ds_read_b64_tr_b16 a[186:187],v22 offset:3848
v_add_u32 v70,192,v12
v_mfma_f32_16x16x32_bf16 v[184:187],a[144:147],a[248:251],v[184:187]
s_waitcnt lgkmcnt(5)
buffer_load_dwordx4 v60,s[32:35],s56 offen lds
v_mfma_f32_16x16x32_bf16 v[164:167],a[152:155],a[224:227],v[164:167]
ds_read_b64_tr_b16 a[188:189],v22 offset:18184
v_mul_u32_u24 v71,3277,v70
v_mfma_f32_16x16x32_bf16 v[164:167],a[156:159],a[228:231],v[164:167]
ds_read_b64_tr_b16 a[190:191],v22 offset:20232
v_lshrrev_b32 v71,16,v71
v_mfma_f32_16x16x32_bf16 v[172:175],a[156:159],a[236:239],v[172:175]
s_add_u32 m0,s58,5120
v_mul_u32_u24 v72,20,v71
v_sub_u32 v72,v70,v72
v_mfma_f32_16x16x32_bf16 v[172:175],a[152:155],a[232:235],v[172:175]
v_bfe_u32 v73,v71,2,1
v_lshlrev_b32 v73,1,v73
v_mfma_f32_16x16x32_bf16 v[180:183],a[152:155],a[240:243],v[180:183]
ds_read_b64_tr_b16 a[128:129],v22
v_xor_b32 v72,v72,v73
v_mfma_f32_16x16x32_bf16 v[180:183],a[156:159],a[244:247],v[180:183]
buffer_load_dwordx4 v61,s[32:35],s56 offen lds
v_mfma_f32_16x16x32_bf16 v[188:191],a[156:159],a[252:255],v[188:191]
ds_read_b64_tr_b16 a[130:131],v22 offset:2048
v_mul_u32_u24 v71,1280,v71
v_mfma_f32_16x16x32_bf16 v[188:191],a[152:155],a[248:251],v[188:191]
ds_read_b64_tr_b16 a[132:133],v22 offset:16384
v_lshl_add_u32 v71,v72,4,v71
v_mfma_f32_16x16x32_bf16 v[192:195],a[160:163],a[224:227],v[192:195]
s_add_u32 m0,s58,6144
ds_read_b64_tr_b16 a[134:135],v22 offset:18432
v_add_u32 v71,s44,v71
v_mfma_f32_16x16x32_bf16 v[192:195],a[164:167],a[228:231],v[192:195]
v_subrev_u32 v10,3072,v71
v_add_u32 v70,256,v12
v_mfma_f32_16x16x32_bf16 v[200:203],a[164:167],a[236:239],v[200:203]
ds_read_b64_tr_b16 a[136:137],v22 offset:8
v_mul_u32_u24 v71,3277,v70
v_mfma_f32_16x16x32_bf16 v[200:203],a[160:163],a[232:235],v[200:203]
buffer_load_dwordx4 v62,s[32:35],s56 offen lds
v_mfma_f32_16x16x32_bf16 v[208:211],a[160:163],a[240:243],v[208:211]
ds_read_b64_tr_b16 a[138:139],v22 offset:2056
v_lshrrev_b32 v71,16,v71
v_mfma_f32_16x16x32_bf16 v[208:211],a[164:167],a[244:247],v[208:211]
s_add_u32 m0,s58,7168
ds_read_b64_tr_b16 a[140:141],v22 offset:16392
v_mul_u32_u24 v72,20,v71
v_mfma_f32_16x16x32_bf16 v[216:219],a[164:167],a[252:255],v[216:219]
s_add_u32 s58,s58,32768
ds_read_b64_tr_b16 a[142:143],v22 offset:18440
v_sub_u32 v72,v70,v72
v_mfma_f32_16x16x32_bf16 v[216:219],a[160:163],a[248:251],v[216:219]
v_bfe_u32 v73,v71,2,1
v_lshlrev_b32 v73,1,v73
v_mfma_f32_16x16x32_bf16 v[196:199],a[168:171],a[224:227],v[196:199]
buffer_load_dwordx4 v63,s[32:35],s56 offen lds
v_mfma_f32_16x16x32_bf16 v[196:199],a[172:175],a[228:231],v[196:199]
s_add_u32 s56,s56,524288
ds_read_b64_tr_b16 a[144:145],v22 offset:256
v_xor_b32 v72,v72,v73
v_mfma_f32_16x16x32_bf16 v[204:207],a[172:175],a[236:239],v[204:207]
ds_read_b64_tr_b16 a[146:147],v22 offset:2304
v_mul_u32_u24 v71,1280,v71
v_mfma_f32_16x16x32_bf16 v[204:207],a[168:171],a[232:235],v[204:207]
s_add_u32 m0,s59,0
ds_read_b64_tr_b16 a[148:149],v22 offset:16640
v_lshl_add_u32 v71,v72,4,v71
v_mfma_f32_16x16x32_bf16 v[212:215],a[168:171],a[240:243],v[212:215]
ds_read_b64_tr_b16 a[150:151],v22 offset:18688
v_add_u32 v71,s44,v71
v_mfma_f32_16x16x32_bf16 v[212:215],a[172:175],a[244:247],v[212:215]
ds_read_b64_tr_b16 a[152:153],v22 offset:264
v_subrev_u32 v11,0,v71
v_mfma_f32_16x16x32_bf16 v[220:223],a[172:175],a[252:255],v[220:223]
buffer_load_dwordx4 v48,s[28:31],s57 offen lds
v_mfma_f32_16x16x32_bf16 v[220:223],a[168:171],a[248:251],v[220:223]
s_cmp_ge_u32 s58,131072
s_cselect_b32 s62,98304,0
ds_read_b64_tr_b16 a[154:155],v22 offset:2312
s_waitcnt lgkmcnt(6)
v_mfma_f32_16x16x32_bf16 v[224:227],a[176:179],a[224:227],v[224:227]
s_sub_u32 s58,s58,s62
ds_read_b64_tr_b16 a[156:157],v22 offset:16648
v_mfma_f32_16x16x32_bf16 v[224:227],a[180:183],a[228:231],v[224:227]
ds_read_b64_tr_b16 a[158:159],v22 offset:18696
v_mfma_f32_16x16x32_bf16 v[232:235],a[180:183],a[236:239],v[232:235]
ds_read_b64_tr_b16 a[160:161],v22 offset:512
v_mfma_f32_16x16x32_bf16 v[232:235],a[176:179],a[232:235],v[232:235]
ds_read_b64_tr_b16 a[162:163],v22 offset:2560
v_mfma_f32_16x16x32_bf16 v[240:243],a[176:179],a[240:243],v[240:243]
buffer_load_dwordx4 v49,s[28:31],s57 offen offset:1024 lds
v_mfma_f32_16x16x32_bf16 v[240:243],a[180:183],a[244:247],v[240:243]
ds_read_b64_tr_b16 a[164:165],v22 offset:16896
v_mfma_f32_16x16x32_bf16 v[248:251],a[180:183],a[252:255],v[248:251]
ds_read_b64_tr_b16 a[166:167],v22 offset:18944
v_mfma_f32_16x16x32_bf16 v[248:251],a[176:179],a[248:251],v[248:251]
ds_read_b64_tr_b16 a[168:169],v22 offset:520
v_mfma_f32_16x16x32_bf16 v[228:231],a[184:187],a[224:227],v[228:231]
ds_read_b64_tr_b16 a[170:171],v22 offset:2568
v_mfma_f32_16x16x32_bf16 v[228:231],a[188:191],a[228:231],v[228:231]
buffer_load_dwordx4 v50,s[28:31],s57 offen offset:2048 lds
v_mfma_f32_16x16x32_bf16 v[236:239],a[188:191],a[236:239],v[236:239]
ds_read_b64_tr_b16 a[172:173],v22 offset:16904
v_mfma_f32_16x16x32_bf16 v[236:239],a[184:187],a[232:235],v[236:239]
ds_read_b64_tr_b16 a[174:175],v22 offset:18952
v_mfma_f32_16x16x32_bf16 v[244:247],a[184:187],a[240:243],v[244:247]
ds_read_b64_tr_b16 a[176:177],v22 offset:768
v_mfma_f32_16x16x32_bf16 v[244:247],a[188:191],a[244:247],v[244:247]
ds_read_b64_tr_b16 a[178:179],v22 offset:2816
v_mfma_f32_16x16x32_bf16 v[252:255],a[188:191],a[252:255],v[252:255]
ds_read_b64_tr_b16 a[180:181],v22 offset:17152
v_mfma_f32_16x16x32_bf16 v[252:255],a[184:187],a[248:251],v[252:255]
buffer_load_dwordx4 v51,s[28:31],s57 offen offset:3072 lds
v_mfma_f32_16x16x32_bf16 a[0:3],a[128:131],a[224:227],a[0:3]
ds_read_b64_tr_b16 a[182:183],v22 offset:19200
v_mfma_f32_16x16x32_bf16 a[0:3],a[132:135],a[228:231],a[0:3]
ds_read_b64_tr_b16 a[184:185],v22 offset:776
v_mfma_f32_16x16x32_bf16 a[32:35],a[132:135],a[236:239],a[32:35]
s_add_u32 m0,s59,4096
s_xor_b32 s59,s59,131072
ds_read_b64_tr_b16 a[186:187],v22 offset:2824
v_mfma_f32_16x16x32_bf16 a[32:35],a[128:131],a[232:235],a[32:35]
ds_read_b64_tr_b16 a[188:189],v22 offset:17160
v_mfma_f32_16x16x32_bf16 a[64:67],a[128:131],a[240:243],a[64:67]
ds_read_b64_tr_b16 a[190:191],v22 offset:19208
v_add_u32 v22,s60,v23
v_mfma_f32_16x16x32_bf16 a[64:67],a[132:135],a[244:247],a[64:67]
buffer_load_dwordx4 v52,s[28:31],s57 offen lds
v_mfma_f32_16x16x32_bf16 a[96:99],a[132:135],a[252:255],a[96:99]
v_mfma_f32_16x16x32_bf16 a[96:99],a[128:131],a[248:251],a[96:99]
v_mfma_f32_16x16x32_bf16 a[4:7],a[136:139],a[224:227],a[4:7]
v_mfma_f32_16x16x32_bf16 a[4:7],a[140:143],a[228:231],a[4:7]
v_mfma_f32_16x16x32_bf16 a[36:39],a[140:143],a[236:239],a[36:39]
buffer_load_dwordx4 v53,s[28:31],s57 offen offset:1024 lds
v_mfma_f32_16x16x32_bf16 a[36:39],a[136:139],a[232:235],a[36:39]
v_mfma_f32_16x16x32_bf16 a[68:71],a[136:139],a[240:243],a[68:71]
v_mfma_f32_16x16x32_bf16 a[68:71],a[140:143],a[244:247],a[68:71]
v_mfma_f32_16x16x32_bf16 a[100:103],a[140:143],a[252:255],a[100:103]
v_mfma_f32_16x16x32_bf16 a[100:103],a[136:139],a[248:251],a[100:103]
s_waitcnt lgkmcnt(0)
v_mfma_f32_16x16x32_bf16 a[8:11],a[144:147],a[224:227],a[8:11]
buffer_load_dwordx4 v54,s[28:31],s57 offen offset:2048 lds
v_mfma_f32_16x16x32_bf16 a[8:11],a[148:151],a[228:231],a[8:11]
v_mfma_f32_16x16x32_bf16 a[40:43],a[148:151],a[236:239],a[40:43]
v_mfma_f32_16x16x32_bf16 a[40:43],a[144:147],a[232:235],a[40:43]
v_mfma_f32_16x16x32_bf16 a[72:75],a[144:147],a[240:243],a[72:75]
v_mfma_f32_16x16x32_bf16 a[72:75],a[148:151],a[244:247],a[72:75]
v_mfma_f32_16x16x32_bf16 a[104:107],a[148:151],a[252:255],a[104:107]
buffer_load_dwordx4 v55,s[28:31],s57 offen offset:3072 lds
v_mfma_f32_16x16x32_bf16 a[104:107],a[144:147],a[248:251],a[104:107]
s_add_u32 s57,s57,128
v_mfma_f32_16x16x32_bf16 a[12:15],a[152:155],a[224:227],a[12:15]
v_mfma_f32_16x16x32_bf16 a[12:15],a[156:159],a[228:231],a[12:15]
v_mfma_f32_16x16x32_bf16 a[44:47],a[156:159],a[236:239],a[44:47]
v_mfma_f32_16x16x32_bf16 a[44:47],a[152:155],a[232:235],a[44:47]
v_mfma_f32_16x16x32_bf16 a[76:79],a[152:155],a[240:243],a[76:79]
v_mfma_f32_16x16x32_bf16 a[76:79],a[156:159],a[244:247],a[76:79]
v_mfma_f32_16x16x32_bf16 a[108:111],a[156:159],a[252:255],a[108:111]
v_mfma_f32_16x16x32_bf16 a[108:111],a[152:155],a[248:251],a[108:111]
s_waitcnt vmcnt(17)
v_mfma_f32_16x16x32_bf16 a[16:19],a[160:163],a[224:227],a[16:19]
s_barrier
ds_read_b128 a[192:195],v20
v_mfma_f32_16x16x32_bf16 a[16:19],a[164:167],a[228:231],a[16:19]
ds_read_b128 a[196:199],v21
v_mfma_f32_16x16x32_bf16 a[48:51],a[164:167],a[236:239],a[48:51]
ds_read_b128 a[200:203],v20 offset:2048
v_mfma_f32_16x16x32_bf16 a[48:51],a[160:163],a[232:235],a[48:51]
ds_read_b128 a[204:207],v21 offset:2048
v_mfma_f32_16x16x32_bf16 a[80:83],a[160:163],a[240:243],a[80:83]
ds_read_b128 a[208:211],v20 offset:4096
v_mfma_f32_16x16x32_bf16 a[80:83],a[164:167],a[244:247],a[80:83]
ds_read_b128 a[212:215],v21 offset:4096
v_mfma_f32_16x16x32_bf16 a[112:115],a[164:167],a[252:255],a[112:115]
ds_read_b128 a[216:219],v20 offset:6144
v_mfma_f32_16x16x32_bf16 a[112:115],a[160:163],a[248:251],a[112:115]
ds_read_b128 a[220:223],v21 offset:6144
v_mfma_f32_16x16x32_bf16 a[20:23],a[168:171],a[224:227],a[20:23]
ds_read_b64_tr_b16 a[128:129],v22 offset:1024
v_mfma_f32_16x16x32_bf16 a[20:23],a[172:175],a[228:231],a[20:23]
ds_read_b64_tr_b16 a[130:131],v22 offset:3072
v_mfma_f32_16x16x32_bf16 a[52:55],a[172:175],a[236:239],a[52:55]
ds_read_b64_tr_b16 a[132:133],v22 offset:17408
v_mfma_f32_16x16x32_bf16 a[52:55],a[168:171],a[232:235],a[52:55]
ds_read_b64_tr_b16 a[134:135],v22 offset:19456
v_mfma_f32_16x16x32_bf16 a[84:87],a[168:171],a[240:243],a[84:87]
ds_read_b64_tr_b16 a[136:137],v22 offset:1032
v_mfma_f32_16x16x32_bf16 a[84:87],a[172:175],a[244:247],a[84:87]
ds_read_b64_tr_b16 a[138:139],v22 offset:3080
v_mfma_f32_16x16x32_bf16 a[116:119],a[172:175],a[252:255],a[116:119]
ds_read_b64_tr_b16 a[140:141],v22 offset:17416
v_mfma_f32_16x16x32_bf16 a[116:119],a[168:171],a[248:251],a[116:119]
ds_read_b64_tr_b16 a[142:143],v22 offset:19464
v_mfma_f32_16x16x32_bf16 a[24:27],a[176:179],a[224:227],a[24:27]
ds_read_b64_tr_b16 a[144:145],v22 offset:1280
v_mfma_f32_16x16x32_bf16 a[24:27],a[180:183],a[228:231],a[24:27]
ds_read_b64_tr_b16 a[146:147],v22 offset:3328
v_mfma_f32_16x16x32_bf16 a[56:59],a[180:183],a[236:239],a[56:59]
)A"
R"A(ds_read_b64_tr_b16 a[148:149],v22 offset:17664
v_mfma_f32_16x16x32_bf16 a[56:59],a[176:179],a[232:235],a[56:59]
ds_read_b64_tr_b16 a[150:151],v22 offset:19712
v_mfma_f32_16x16x32_bf16 a[88:91],a[176:179],a[240:243],a[88:91]
ds_read_b64_tr_b16 a[152:153],v22 offset:1288
v_mfma_f32_16x16x32_bf16 a[88:91],a[180:183],a[244:247],a[88:91]
ds_read_b64_tr_b16 a[154:155],v22 offset:3336
v_mfma_f32_16x16x32_bf16 a[120:123],a[180:183],a[252:255],a[120:123]
ds_read_b64_tr_b16 a[156:157],v22 offset:17672
v_mfma_f32_16x16x32_bf16 a[120:123],a[176:179],a[248:251],a[120:123]
ds_read_b64_tr_b16 a[158:159],v22 offset:19720
v_mfma_f32_16x16x32_bf16 a[28:31],a[184:187],a[224:227],a[28:31]
v_mfma_f32_16x16x32_bf16 a[28:31],a[188:191],a[228:231],a[28:31]
v_mfma_f32_16x16x32_bf16 a[60:63],a[188:191],a[236:239],a[60:63]
v_mfma_f32_16x16x32_bf16 a[60:63],a[184:187],a[232:235],a[60:63]
v_mfma_f32_16x16x32_bf16 a[92:95],a[184:187],a[240:243],a[92:95]
v_mfma_f32_16x16x32_bf16 a[92:95],a[188:191],a[244:247],a[92:95]
v_mfma_f32_16x16x32_bf16 a[124:127],a[188:191],a[252:255],a[124:127]
v_mfma_f32_16x16x32_bf16 a[124:127],a[184:187],a[248:251],a[124:127]
s_mov_b32 s53,3
.p2align 5
L_qp_%=:
s_waitcnt lgkmcnt(1)
v_mfma_f32_16x16x32_bf16 v[128:131],a[128:131],a[192:195],v[128:131]
s_add_u32 m0,s58,0
s_add_u32 s60,s60,32768
ds_read_b64_tr_b16 a[160:161],v22 offset:1536
v_mfma_f32_16x16x32_bf16 v[128:131],a[132:135],a[196:199],v[128:131]
s_mov_b32 s90,L_att0x_%=-L_pc_%=+128
buffer_load_dwordx4 v56,s[32:35],s56 offen lds
v_mfma_f32_16x16x32_bf16 v[136:139],a[132:135],a[204:207],v[136:139]
s_cmp_eq_u32 s60,131072
s_cselect_b32 s60,32768,s60
ds_read_b64_tr_b16 a[162:163],v22 offset:3584
v_mfma_f32_16x16x32_bf16 v[136:139],a[128:131],a[200:203],v[136:139]
s_add_u32 m0,s58,1024
ds_read_b64_tr_b16 a[164:165],v22 offset:17920
v_xor_b32 v20,131072,v20
v_mfma_f32_16x16x32_bf16 v[144:147],a[128:131],a[208:211],v[144:147]
s_sub_u32 s53,s53,1
ds_read_b64_tr_b16 a[166:167],v22 offset:19968
v_xor_b32 v21,131072,v21
v_mfma_f32_16x16x32_bf16 v[144:147],a[132:135],a[212:215],v[144:147]
ds_read_b64_tr_b16 a[168:169],v22 offset:1544
v_mfma_f32_16x16x32_bf16 v[152:155],a[132:135],a[220:223],v[152:155]
buffer_load_dwordx4 v57,s[32:35],s56 offen lds
v_mfma_f32_16x16x32_bf16 v[152:155],a[128:131],a[216:219],v[152:155]
ds_read_b64_tr_b16 a[170:171],v22 offset:3592
v_mfma_f32_16x16x32_bf16 v[132:135],a[136:139],a[192:195],v[132:135]
ds_read_b64_tr_b16 a[172:173],v22 offset:17928
v_mfma_f32_16x16x32_bf16 v[132:135],a[140:143],a[196:199],v[132:135]
s_add_u32 m0,s58,2048
ds_read_b64_tr_b16 a[174:175],v22 offset:19976
v_mfma_f32_16x16x32_bf16 v[140:143],a[140:143],a[204:207],v[140:143]
ds_read_b64_tr_b16 a[176:177],v22 offset:1792
v_mfma_f32_16x16x32_bf16 v[140:143],a[136:139],a[200:203],v[140:143]
ds_read_b64_tr_b16 a[178:179],v22 offset:3840
v_mfma_f32_16x16x32_bf16 v[148:151],a[136:139],a[208:211],v[148:151]
buffer_load_dwordx4 v58,s[32:35],s56 offen lds
v_mfma_f32_16x16x32_bf16 v[148:151],a[140:143],a[212:215],v[148:151]
ds_read_b64_tr_b16 a[180:181],v22 offset:18176
v_mfma_f32_16x16x32_bf16 v[156:159],a[140:143],a[220:223],v[156:159]
s_add_u32 m0,s58,3072
ds_read_b64_tr_b16 a[182:183],v22 offset:20224
v_mfma_f32_16x16x32_bf16 v[156:159],a[136:139],a[216:219],v[156:159]
ds_read_b64_tr_b16 a[184:185],v22 offset:1800
v_mfma_f32_16x16x32_bf16 v[160:163],a[144:147],a[192:195],v[160:163]
ds_read_b64_tr_b16 a[186:187],v22 offset:3848
v_mfma_f32_16x16x32_bf16 v[160:163],a[148:151],a[196:199],v[160:163]
buffer_load_dwordx4 v59,s[32:35],s56 offen lds
v_mfma_f32_16x16x32_bf16 v[168:171],a[148:151],a[204:207],v[168:171]
ds_read_b64_tr_b16 a[188:189],v22 offset:18184
v_mfma_f32_16x16x32_bf16 v[168:171],a[144:147],a[200:203],v[168:171]
ds_read_b64_tr_b16 a[190:191],v22 offset:20232
v_mfma_f32_16x16x32_bf16 v[176:179],a[144:147],a[208:211],v[176:179]
s_add_u32 m0,s58,4096
ds_read_b64_tr_b16 a[128:129],v22
v_mfma_f32_16x16x32_bf16 v[176:179],a[148:151],a[212:215],v[176:179]
ds_read_b64_tr_b16 a[130:131],v22 offset:2048
v_mfma_f32_16x16x32_bf16 v[184:187],a[148:151],a[220:223],v[184:187]
ds_read_b64_tr_b16 a[132:133],v22 offset:16384
v_mfma_f32_16x16x32_bf16 v[184:187],a[144:147],a[216:219],v[184:187]
buffer_load_dwordx4 v60,s[32:35],s56 offen lds
v_mfma_f32_16x16x32_bf16 v[164:167],a[152:155],a[192:195],v[164:167]
s_waitcnt lgkmcnt(5)
ds_read_b64_tr_b16 a[134:135],v22 offset:18432
v_mfma_f32_16x16x32_bf16 v[164:167],a[156:159],a[196:199],v[164:167]
s_add_u32 m0,s58,5120
ds_read_b64_tr_b16 a[136:137],v22 offset:8
v_mfma_f32_16x16x32_bf16 v[172:175],a[156:159],a[204:207],v[172:175]
ds_read_b64_tr_b16 a[138:139],v22 offset:2056
v_mfma_f32_16x16x32_bf16 v[172:175],a[152:155],a[200:203],v[172:175]
ds_read_b64_tr_b16 a[140:141],v22 offset:16392
v_mfma_f32_16x16x32_bf16 v[180:183],a[152:155],a[208:211],v[180:183]
buffer_load_dwordx4 v61,s[32:35],s56 offen lds
v_mfma_f32_16x16x32_bf16 v[180:183],a[156:159],a[212:215],v[180:183]
ds_read_b64_tr_b16 a[142:143],v22 offset:18440
v_mfma_f32_16x16x32_bf16 v[188:191],a[156:159],a[220:223],v[188:191]
ds_read_b64_tr_b16 a[144:145],v22 offset:256
v_mfma_f32_16x16x32_bf16 v[188:191],a[152:155],a[216:219],v[188:191]
s_add_u32 m0,s58,6144
ds_read_b64_tr_b16 a[146:147],v22 offset:2304
v_mfma_f32_16x16x32_bf16 v[192:195],a[160:163],a[192:195],v[192:195]
ds_read_b64_tr_b16 a[148:149],v22 offset:16640
v_mfma_f32_16x16x32_bf16 v[192:195],a[164:167],a[196:199],v[192:195]
ds_read_b64_tr_b16 a[150:151],v22 offset:18688
v_mfma_f32_16x16x32_bf16 v[200:203],a[164:167],a[204:207],v[200:203]
buffer_load_dwordx4 v62,s[32:35],s56 offen lds
v_mfma_f32_16x16x32_bf16 v[200:203],a[160:163],a[200:203],v[200:203]
ds_read_b64_tr_b16 a[152:153],v22 offset:264
v_mfma_f32_16x16x32_bf16 v[208:211],a[160:163],a[208:211],v[208:211]
s_add_u32 m0,s58,7168
s_add_u32 s58,s58,32768
ds_read_b64_tr_b16 a[154:155],v22 offset:2312
v_mfma_f32_16x16x32_bf16 v[208:211],a[164:167],a[212:215],v[208:211]
s_cmp_ge_u32 s58,131072
s_cselect_b32 s62,98304,0
ds_read_b64_tr_b16 a[156:157],v22 offset:16648
v_mfma_f32_16x16x32_bf16 v[216:219],a[164:167],a[220:223],v[216:219]
s_sub_u32 s58,s58,s62
ds_read_b64_tr_b16 a[158:159],v22 offset:18696
v_mfma_f32_16x16x32_bf16 v[216:219],a[160:163],a[216:219],v[216:219]
buffer_load_dwordx4 v63,s[32:35],s56 offen lds
v_mfma_f32_16x16x32_bf16 v[196:199],a[168:171],a[192:195],v[196:199]
s_add_u32 s56,s56,524288
ds_read_b64_tr_b16 a[160:161],v22 offset:512
v_mfma_f32_16x16x32_bf16 v[196:199],a[172:175],a[196:199],v[196:199]
ds_read_b64_tr_b16 a[162:163],v22 offset:2560
v_mfma_f32_16x16x32_bf16 v[204:207],a[172:175],a[204:207],v[204:207]
s_add_u32 m0,s59,0
ds_read_b64_tr_b16 a[164:165],v22 offset:16896
v_mfma_f32_16x16x32_bf16 v[204:207],a[168:171],a[200:203],v[204:207]
ds_read_b64_tr_b16 a[166:167],v22 offset:18944
v_mfma_f32_16x16x32_bf16 v[212:215],a[168:171],a[208:211],v[212:215]
v_mfma_f32_16x16x32_bf16 v[212:215],a[172:175],a[212:215],v[212:215]
buffer_load_dwordx4 v48,s[28:31],s57 offen lds
v_mfma_f32_16x16x32_bf16 v[220:223],a[172:175],a[220:223],v[220:223]
ds_read_b64_tr_b16 a[172:173],v22 offset:16904
v_mfma_f32_16x16x32_bf16 v[220:223],a[168:171],a[216:219],v[220:223]
ds_read_b64_tr_b16 a[168:169],v22 offset:520
v_mfma_f32_16x16x32_bf16 v[224:227],a[176:179],a[192:195],v[224:227]
ds_read_b64_tr_b16 a[170:171],v22 offset:2568
v_mfma_f32_16x16x32_bf16 v[224:227],a[180:183],a[196:199],v[224:227]
ds_read_b64_tr_b16 a[174:175],v22 offset:18952
v_mfma_f32_16x16x32_bf16 v[232:235],a[180:183],a[204:207],v[232:235]
buffer_load_dwordx4 v49,s[28:31],s57 offen offset:1024 lds
v_mfma_f32_16x16x32_bf16 v[232:235],a[176:179],a[200:203],v[232:235]
v_mfma_f32_16x16x32_bf16 v[240:243],a[176:179],a[208:211],v[240:243]
v_mfma_f32_16x16x32_bf16 v[240:243],a[180:183],a[212:215],v[240:243]
v_mfma_f32_16x16x32_bf16 v[248:251],a[180:183],a[220:223],v[248:251]
ds_read_b64_tr_b16 a[180:181],v22 offset:17152
v_mfma_f32_16x16x32_bf16 v[248:251],a[176:179],a[216:219],v[248:251]
ds_read_b64_tr_b16 a[176:177],v22 offset:768
v_mfma_f32_16x16x32_bf16 v[228:231],a[184:187],a[192:195],v[228:231]
s_waitcnt lgkmcnt(3)
buffer_load_dwordx4 v50,s[28:31],s57 offen offset:2048 lds
v_mfma_f32_16x16x32_bf16 v[228:231],a[188:191],a[196:199],v[228:231]
ds_read_b64_tr_b16 a[178:179],v22 offset:2816
v_mfma_f32_16x16x32_bf16 v[236:239],a[188:191],a[204:207],v[236:239]
ds_read_b64_tr_b16 a[182:183],v22 offset:19200
v_mfma_f32_16x16x32_bf16 v[236:239],a[184:187],a[200:203],v[236:239]
v_mfma_f32_16x16x32_bf16 v[244:247],a[184:187],a[208:211],v[244:247]
v_mfma_f32_16x16x32_bf16 v[244:247],a[188:191],a[212:215],v[244:247]
buffer_load_dwordx4 v51,s[28:31],s57 offen offset:3072 lds
v_mfma_f32_16x16x32_bf16 v[252:255],a[188:191],a[220:223],v[252:255]
ds_read_b64_tr_b16 a[188:189],v22 offset:17160
v_mfma_f32_16x16x32_bf16 v[252:255],a[184:187],a[216:219],v[252:255]
ds_read_b64_tr_b16 a[184:185],v22 offset:776
v_mfma_f32_16x16x32_bf16 a[0:3],a[128:131],a[192:195],a[0:3]
s_add_u32 m0,s59,4096
s_xor_b32 s59,s59,131072
ds_read_b64_tr_b16 a[186:187],v22 offset:2824
v_mfma_f32_16x16x32_bf16 a[0:3],a[132:135],a[196:199],a[0:3]
ds_read_b64_tr_b16 a[190:191],v22 offset:19208
v_add_u32 v22,s60,v23
v_mfma_f32_16x16x32_bf16 a[32:35],a[132:135],a[204:207],a[32:35]
s_add_u32 s60,s60,32768
s_cmp_eq_u32 s60,131072
s_cselect_b32 s60,32768,s60
v_mfma_f32_16x16x32_bf16 a[32:35],a[128:131],a[200:203],a[32:35]
buffer_load_dwordx4 v52,s[28:31],s57 offen lds
v_mfma_f32_16x16x32_bf16 a[64:67],a[128:131],a[208:211],a[64:67]
v_mfma_f32_16x16x32_bf16 a[64:67],a[132:135],a[212:215],a[64:67]
v_mfma_f32_16x16x32_bf16 a[96:99],a[132:135],a[220:223],a[96:99]
v_mfma_f32_16x16x32_bf16 a[96:99],a[128:131],a[216:219],a[96:99]
v_mfma_f32_16x16x32_bf16 a[4:7],a[136:139],a[192:195],a[4:7]
buffer_load_dwordx4 v53,s[28:31],s57 offen offset:1024 lds
v_mfma_f32_16x16x32_bf16 a[4:7],a[140:143],a[196:199],a[4:7]
v_mfma_f32_16x16x32_bf16 a[36:39],a[140:143],a[204:207],a[36:39]
v_mfma_f32_16x16x32_bf16 a[36:39],a[136:139],a[200:203],a[36:39]
v_mfma_f32_16x16x32_bf16 a[68:71],a[136:139],a[208:211],a[68:71]
v_mfma_f32_16x16x32_bf16 a[68:71],a[140:143],a[212:215],a[68:71]
v_mfma_f32_16x16x32_bf16 a[100:103],a[140:143],a[220:223],a[100:103]
buffer_load_dwordx4 v54,s[28:31],s57 offen offset:2048 lds
v_mfma_f32_16x16x32_bf16 a[100:103],a[136:139],a[216:219],a[100:103]
v_mfma_f32_16x16x32_bf16 a[8:11],a[144:147],a[192:195],a[8:11]
v_mfma_f32_16x16x32_bf16 a[8:11],a[148:151],a[196:199],a[8:11]
v_mfma_f32_16x16x32_bf16 a[40:43],a[148:151],a[204:207],a[40:43]
v_mfma_f32_16x16x32_bf16 a[40:43],a[144:147],a[200:203],a[40:43]
buffer_load_dwordx4 v55,s[28:31],s57 offen offset:3072 lds
v_mfma_f32_16x16x32_bf16 a[72:75],a[144:147],a[208:211],a[72:75]
s_add_u32 s57,s57,128
v_mfma_f32_16x16x32_bf16 a[72:75],a[148:151],a[212:215],a[72:75]
v_mfma_f32_16x16x32_bf16 a[104:107],a[148:151],a[220:223],a[104:107]
v_mfma_f32_16x16x32_bf16 a[104:107],a[144:147],a[216:219],a[104:107]
v_mfma_f32_16x16x32_bf16 a[12:15],a[152:155],a[192:195],a[12:15]
v_mfma_f32_16x16x32_bf16 a[12:15],a[156:159],a[196:199],a[12:15]
v_mfma_f32_16x16x32_bf16 a[44:47],a[156:159],a[204:207],a[44:47]
buffer_load_dword v16,v15,s[72:75],s83 offen
v_mfma_f32_16x16x32_bf16 a[44:47],a[152:155],a[200:203],a[44:47]
s_add_u32 s83,s83,896
v_mfma_f32_16x16x32_bf16 a[76:79],a[152:155],a[208:211],a[76:79]
v_mfma_f32_16x16x32_bf16 a[76:79],a[156:159],a[212:215],a[76:79]
v_mfma_f32_16x16x32_bf16 a[108:111],a[156:159],a[220:223],a[108:111]
v_mfma_f32_16x16x32_bf16 a[108:111],a[152:155],a[216:219],a[108:111]
s_waitcnt vmcnt(17) lgkmcnt(0)
v_mfma_f32_16x16x32_bf16 a[16:19],a[160:163],a[192:195],a[16:19]
s_barrier
ds_read_b128 a[224:227],v20
v_mfma_f32_16x16x32_bf16 a[16:19],a[164:167],a[196:199],a[16:19]
ds_read_b64_tr_b16 a[128:129],v22 offset:1024
v_mfma_f32_16x16x32_bf16 a[48:51],a[164:167],a[204:207],a[48:51]
ds_read_b64_tr_b16 a[130:131],v22 offset:3072
v_mfma_f32_16x16x32_bf16 a[48:51],a[160:163],a[200:203],a[48:51]
ds_read_b128 a[228:231],v21
v_mfma_f32_16x16x32_bf16 a[80:83],a[160:163],a[208:211],a[80:83]
)A"
R"A(ds_read_b64_tr_b16 a[132:133],v22 offset:17408
v_mfma_f32_16x16x32_bf16 a[80:83],a[164:167],a[212:215],a[80:83]
ds_read_b64_tr_b16 a[134:135],v22 offset:19456
v_mfma_f32_16x16x32_bf16 a[112:115],a[164:167],a[220:223],a[112:115]
ds_read_b128 a[232:235],v20 offset:2048
v_mfma_f32_16x16x32_bf16 a[112:115],a[160:163],a[216:219],a[112:115]
ds_read_b128 a[236:239],v21 offset:2048
v_mfma_f32_16x16x32_bf16 a[20:23],a[168:171],a[192:195],a[20:23]
ds_read_b128 a[240:243],v20 offset:4096
v_mfma_f32_16x16x32_bf16 a[20:23],a[172:175],a[196:199],a[20:23]
ds_read_b128 a[244:247],v21 offset:4096
v_mfma_f32_16x16x32_bf16 a[52:55],a[172:175],a[204:207],a[52:55]
ds_read_b128 a[248:251],v20 offset:6144
v_xor_b32 v20,131072,v20
v_mfma_f32_16x16x32_bf16 a[52:55],a[168:171],a[200:203],a[52:55]
ds_read_b128 a[252:255],v21 offset:6144
v_xor_b32 v21,131072,v21
v_mfma_f32_16x16x32_bf16 a[84:87],a[168:171],a[208:211],a[84:87]
ds_read_b64_tr_b16 a[136:137],v22 offset:1032
v_mfma_f32_16x16x32_bf16 a[84:87],a[172:175],a[212:215],a[84:87]
ds_read_b64_tr_b16 a[138:139],v22 offset:3080
v_mfma_f32_16x16x32_bf16 a[116:119],a[172:175],a[220:223],a[116:119]
ds_read_b64_tr_b16 a[140:141],v22 offset:17416
v_mfma_f32_16x16x32_bf16 a[116:119],a[168:171],a[216:219],a[116:119]
ds_read_b64_tr_b16 a[142:143],v22 offset:19464
v_mfma_f32_16x16x32_bf16 a[24:27],a[176:179],a[192:195],a[24:27]
ds_read_b64_tr_b16 a[144:145],v22 offset:1280
v_mfma_f32_16x16x32_bf16 a[24:27],a[180:183],a[196:199],a[24:27]
ds_read_b64_tr_b16 a[146:147],v22 offset:3328
v_mfma_f32_16x16x32_bf16 a[56:59],a[180:183],a[204:207],a[56:59]
ds_read_b64_tr_b16 a[148:149],v22 offset:17664
v_mfma_f32_16x16x32_bf16 a[56:59],a[176:179],a[200:203],a[56:59]
ds_read_b64_tr_b16 a[150:151],v22 offset:19712
v_mfma_f32_16x16x32_bf16 a[88:91],a[176:179],a[208:211],a[88:91]
ds_read_b64_tr_b16 a[152:153],v22 offset:1288
v_mfma_f32_16x16x32_bf16 a[88:91],a[180:183],a[212:215],a[88:91]
ds_read_b64_tr_b16 a[154:155],v22 offset:3336
v_mfma_f32_16x16x32_bf16 a[120:123],a[180:183],a[220:223],a[120:123]
ds_read_b64_tr_b16 a[156:157],v22 offset:17672
v_mfma_f32_16x16x32_bf16 a[120:123],a[176:179],a[216:219],a[120:123]
ds_read_b64_tr_b16 a[158:159],v22 offset:19720
v_mfma_f32_16x16x32_bf16 a[28:31],a[184:187],a[192:195],a[28:31]
ds_read_b64_tr_b16 a[160:161],v22 offset:1536
v_mfma_f32_16x16x32_bf16 a[28:31],a[188:191],a[196:199],a[28:31]
ds_read_b64_tr_b16 a[162:163],v22 offset:3584
v_mfma_f32_16x16x32_bf16 a[60:63],a[188:191],a[204:207],a[60:63]
ds_read_b64_tr_b16 a[164:165],v22 offset:17920
v_mfma_f32_16x16x32_bf16 a[60:63],a[184:187],a[200:203],a[60:63]
ds_read_b64_tr_b16 a[166:167],v22 offset:19968
v_mfma_f32_16x16x32_bf16 a[92:95],a[184:187],a[208:211],a[92:95]
ds_read_b64_tr_b16 a[168:169],v22 offset:1544
v_mfma_f32_16x16x32_bf16 a[92:95],a[188:191],a[212:215],a[92:95]
ds_read_b64_tr_b16 a[170:171],v22 offset:3592
v_mfma_f32_16x16x32_bf16 a[124:127],a[188:191],a[220:223],a[124:127]
s_add_u32 m0,s58,0
ds_read_b64_tr_b16 a[172:173],v22 offset:17928
v_mfma_f32_16x16x32_bf16 a[124:127],a[184:187],a[216:219],a[124:127]
s_waitcnt lgkmcnt(7)
ds_read_b64_tr_b16 a[174:175],v22 offset:19976
v_mfma_f32_16x16x32_bf16 v[128:131],a[128:131],a[224:227],v[128:131]
ds_read_b64_tr_b16 a[176:177],v22 offset:1792
v_mfma_f32_16x16x32_bf16 v[128:131],a[132:135],a[228:231],v[128:131]
buffer_load_dwordx4 v56,s[32:35],s56 offen lds
v_mfma_f32_16x16x32_bf16 v[136:139],a[132:135],a[236:239],v[136:139]
ds_read_b64_tr_b16 a[178:179],v22 offset:3840
v_mfma_f32_16x16x32_bf16 v[136:139],a[128:131],a[232:235],v[136:139]
s_add_u32 m0,s58,1024
ds_read_b64_tr_b16 a[180:181],v22 offset:18176
v_mfma_f32_16x16x32_bf16 v[144:147],a[128:131],a[240:243],v[144:147]
ds_read_b64_tr_b16 a[182:183],v22 offset:20224
v_mfma_f32_16x16x32_bf16 v[144:147],a[132:135],a[244:247],v[144:147]
ds_read_b64_tr_b16 a[184:185],v22 offset:1800
v_mfma_f32_16x16x32_bf16 v[152:155],a[132:135],a[252:255],v[152:155]
buffer_load_dwordx4 v57,s[32:35],s56 offen lds
v_mfma_f32_16x16x32_bf16 v[152:155],a[128:131],a[248:251],v[152:155]
ds_read_b64_tr_b16 a[186:187],v22 offset:3848
v_mfma_f32_16x16x32_bf16 v[132:135],a[136:139],a[224:227],v[132:135]
ds_read_b64_tr_b16 a[188:189],v22 offset:18184
v_mfma_f32_16x16x32_bf16 v[132:135],a[140:143],a[228:231],v[132:135]
s_add_u32 m0,s58,2048
ds_read_b64_tr_b16 a[190:191],v22 offset:20232
v_mfma_f32_16x16x32_bf16 v[140:143],a[140:143],a[236:239],v[140:143]
ds_read_b64_tr_b16 a[128:129],v22
v_mfma_f32_16x16x32_bf16 v[140:143],a[136:139],a[232:235],v[140:143]
ds_read_b64_tr_b16 a[130:131],v22 offset:2048
v_mfma_f32_16x16x32_bf16 v[148:151],a[136:139],a[240:243],v[148:151]
buffer_load_dwordx4 v58,s[32:35],s56 offen lds
v_mfma_f32_16x16x32_bf16 v[148:151],a[140:143],a[244:247],v[148:151]
ds_read_b64_tr_b16 a[132:133],v22 offset:16384
v_mfma_f32_16x16x32_bf16 v[156:159],a[140:143],a[252:255],v[156:159]
s_add_u32 m0,s58,3072
ds_read_b64_tr_b16 a[134:135],v22 offset:18432
v_mfma_f32_16x16x32_bf16 v[156:159],a[136:139],a[248:251],v[156:159]
ds_read_b64_tr_b16 a[136:137],v22 offset:8
v_mfma_f32_16x16x32_bf16 v[160:163],a[144:147],a[224:227],v[160:163]
ds_read_b64_tr_b16 a[138:139],v22 offset:2056
v_mfma_f32_16x16x32_bf16 v[160:163],a[148:151],a[228:231],v[160:163]
buffer_load_dwordx4 v59,s[32:35],s56 offen lds
v_mfma_f32_16x16x32_bf16 v[168:171],a[148:151],a[236:239],v[168:171]
ds_read_b64_tr_b16 a[140:141],v22 offset:16392
v_mfma_f32_16x16x32_bf16 v[168:171],a[144:147],a[232:235],v[168:171]
ds_read_b64_tr_b16 a[142:143],v22 offset:18440
v_mfma_f32_16x16x32_bf16 v[176:179],a[144:147],a[240:243],v[176:179]
s_add_u32 m0,s58,4096
v_mfma_f32_16x16x32_bf16 v[176:179],a[148:151],a[244:247],v[176:179]
v_mfma_f32_16x16x32_bf16 v[184:187],a[148:151],a[252:255],v[184:187]
ds_read_b64_tr_b16 a[148:149],v22 offset:16640
v_mfma_f32_16x16x32_bf16 v[184:187],a[144:147],a[248:251],v[184:187]
buffer_load_dwordx4 v60,s[32:35],s56 offen lds
v_mfma_f32_16x16x32_bf16 v[164:167],a[152:155],a[224:227],v[164:167]
ds_read_b64_tr_b16 a[144:145],v22 offset:256
v_mfma_f32_16x16x32_bf16 v[164:167],a[156:159],a[228:231],v[164:167]
s_add_u32 m0,s58,5120
ds_read_b64_tr_b16 a[146:147],v22 offset:2304
v_mfma_f32_16x16x32_bf16 v[172:175],a[156:159],a[236:239],v[172:175]
ds_read_b64_tr_b16 a[150:151],v22 offset:18688
v_mfma_f32_16x16x32_bf16 v[172:175],a[152:155],a[232:235],v[172:175]
v_mfma_f32_16x16x32_bf16 v[180:183],a[152:155],a[240:243],v[180:183]
buffer_load_dwordx4 v61,s[32:35],s56 offen lds
v_mfma_f32_16x16x32_bf16 v[180:183],a[156:159],a[244:247],v[180:183]
v_mfma_f32_16x16x32_bf16 v[188:191],a[156:159],a[252:255],v[188:191]
ds_read_b64_tr_b16 a[156:157],v22 offset:16648
v_mfma_f32_16x16x32_bf16 v[188:191],a[152:155],a[248:251],v[188:191]
s_add_u32 m0,s58,6144
ds_read_b64_tr_b16 a[152:153],v22 offset:264
s_waitcnt lgkmcnt(5)
v_mfma_f32_16x16x32_bf16 v[192:195],a[160:163],a[224:227],v[192:195]
ds_read_b64_tr_b16 a[154:155],v22 offset:2312
v_mfma_f32_16x16x32_bf16 v[192:195],a[164:167],a[228:231],v[192:195]
ds_read_b64_tr_b16 a[158:159],v22 offset:18696
v_mfma_f32_16x16x32_bf16 v[200:203],a[164:167],a[236:239],v[200:203]
buffer_load_dwordx4 v62,s[32:35],s56 offen lds
v_mfma_f32_16x16x32_bf16 v[200:203],a[160:163],a[232:235],v[200:203]
v_mfma_f32_16x16x32_bf16 v[208:211],a[160:163],a[240:243],v[208:211]
s_add_u32 m0,s58,7168
s_add_u32 s58,s58,32768
v_mfma_f32_16x16x32_bf16 v[208:211],a[164:167],a[244:247],v[208:211]
s_cmp_ge_u32 s58,131072
s_cselect_b32 s62,98304,0
s_sub_u32 s58,s58,s62
v_mfma_f32_16x16x32_bf16 v[216:219],a[164:167],a[252:255],v[216:219]
ds_read_b64_tr_b16 a[164:165],v22 offset:16896
v_mfma_f32_16x16x32_bf16 v[216:219],a[160:163],a[248:251],v[216:219]
buffer_load_dwordx4 v63,s[32:35],s56 offen lds
v_mfma_f32_16x16x32_bf16 v[196:199],a[168:171],a[224:227],v[196:199]
s_add_u32 s56,s56,524288
ds_read_b64_tr_b16 a[160:161],v22 offset:512
v_mfma_f32_16x16x32_bf16 v[196:199],a[172:175],a[228:231],v[196:199]
ds_read_b64_tr_b16 a[162:163],v22 offset:2560
v_mfma_f32_16x16x32_bf16 v[204:207],a[172:175],a[236:239],v[204:207]
s_add_u32 m0,s59,0
ds_read_b64_tr_b16 a[166:167],v22 offset:18944
v_mfma_f32_16x16x32_bf16 v[204:207],a[168:171],a[232:235],v[204:207]
v_mfma_f32_16x16x32_bf16 v[212:215],a[168:171],a[240:243],v[212:215]
v_mfma_f32_16x16x32_bf16 v[212:215],a[172:175],a[244:247],v[212:215]
buffer_load_dwordx4 v48,s[28:31],s57 offen lds
v_mfma_f32_16x16x32_bf16 v[220:223],a[172:175],a[252:255],v[220:223]
ds_read_b64_tr_b16 a[172:173],v22 offset:16904
v_mfma_f32_16x16x32_bf16 v[220:223],a[168:171],a[248:251],v[220:223]
ds_read_b64_tr_b16 a[168:169],v22 offset:520
v_mfma_f32_16x16x32_bf16 v[224:227],a[176:179],a[224:227],v[224:227]
ds_read_b64_tr_b16 a[170:171],v22 offset:2568
v_mfma_f32_16x16x32_bf16 v[224:227],a[180:183],a[228:231],v[224:227]
ds_read_b64_tr_b16 a[174:175],v22 offset:18952
v_mfma_f32_16x16x32_bf16 v[232:235],a[180:183],a[236:239],v[232:235]
buffer_load_dwordx4 v49,s[28:31],s57 offen offset:1024 lds
v_mfma_f32_16x16x32_bf16 v[232:235],a[176:179],a[232:235],v[232:235]
v_mfma_f32_16x16x32_bf16 v[240:243],a[176:179],a[240:243],v[240:243]
v_mfma_f32_16x16x32_bf16 v[240:243],a[180:183],a[244:247],v[240:243]
v_mfma_f32_16x16x32_bf16 v[248:251],a[180:183],a[252:255],v[248:251]
ds_read_b64_tr_b16 a[180:181],v22 offset:17152
v_mfma_f32_16x16x32_bf16 v[248:251],a[176:179],a[248:251],v[248:251]
ds_read_b64_tr_b16 a[176:177],v22 offset:768
v_mfma_f32_16x16x32_bf16 v[228:231],a[184:187],a[224:227],v[228:231]
buffer_load_dwordx4 v50,s[28:31],s57 offen offset:2048 lds
v_mfma_f32_16x16x32_bf16 v[228:231],a[188:191],a[228:231],v[228:231]
ds_read_b64_tr_b16 a[178:179],v22 offset:2816
v_mfma_f32_16x16x32_bf16 v[236:239],a[188:191],a[236:239],v[236:239]
ds_read_b64_tr_b16 a[182:183],v22 offset:19200
v_mfma_f32_16x16x32_bf16 v[236:239],a[184:187],a[232:235],v[236:239]
v_mfma_f32_16x16x32_bf16 v[244:247],a[184:187],a[240:243],v[244:247]
v_mfma_f32_16x16x32_bf16 v[244:247],a[188:191],a[244:247],v[244:247]
buffer_load_dwordx4 v51,s[28:31],s57 offen offset:3072 lds
v_mfma_f32_16x16x32_bf16 v[252:255],a[188:191],a[252:255],v[252:255]
ds_read_b64_tr_b16 a[188:189],v22 offset:17160
v_mfma_f32_16x16x32_bf16 v[252:255],a[184:187],a[248:251],v[252:255]
ds_read_b64_tr_b16 a[184:185],v22 offset:776
v_mfma_f32_16x16x32_bf16 a[0:3],a[128:131],a[224:227],a[0:3]
s_add_u32 m0,s59,4096
s_xor_b32 s59,s59,131072
ds_read_b64_tr_b16 a[186:187],v22 offset:2824
v_mfma_f32_16x16x32_bf16 a[0:3],a[132:135],a[228:231],a[0:3]
ds_read_b64_tr_b16 a[190:191],v22 offset:19208
v_add_u32 v22,s60,v23
v_mfma_f32_16x16x32_bf16 a[32:35],a[132:135],a[236:239],a[32:35]
v_mfma_f32_16x16x32_bf16 a[32:35],a[128:131],a[232:235],a[32:35]
buffer_load_dwordx4 v52,s[28:31],s57 offen lds
v_mfma_f32_16x16x32_bf16 a[64:67],a[128:131],a[240:243],a[64:67]
v_mfma_f32_16x16x32_bf16 a[64:67],a[132:135],a[244:247],a[64:67]
v_mfma_f32_16x16x32_bf16 a[96:99],a[132:135],a[252:255],a[96:99]
v_mfma_f32_16x16x32_bf16 a[96:99],a[128:131],a[248:251],a[96:99]
v_mfma_f32_16x16x32_bf16 a[4:7],a[136:139],a[224:227],a[4:7]
buffer_load_dwordx4 v53,s[28:31],s57 offen offset:1024 lds
v_mfma_f32_16x16x32_bf16 a[4:7],a[140:143],a[228:231],a[4:7]
v_mfma_f32_16x16x32_bf16 a[36:39],a[140:143],a[236:239],a[36:39]
v_mfma_f32_16x16x32_bf16 a[36:39],a[136:139],a[232:235],a[36:39]
v_mfma_f32_16x16x32_bf16 a[68:71],a[136:139],a[240:243],a[68:71]
v_mfma_f32_16x16x32_bf16 a[68:71],a[140:143],a[244:247],a[68:71]
v_mfma_f32_16x16x32_bf16 a[100:103],a[140:143],a[252:255],a[100:103]
buffer_load_dwordx4 v54,s[28:31],s57 offen offset:2048 lds
v_mfma_f32_16x16x32_bf16 a[100:103],a[136:139],a[248:251],a[100:103]
s_waitcnt lgkmcnt(0)
v_mfma_f32_16x16x32_bf16 a[8:11],a[144:147],a[224:227],a[8:11]
v_mfma_f32_16x16x32_bf16 a[8:11],a[148:151],a[228:231],a[8:11]
v_mfma_f32_16x16x32_bf16 a[40:43],a[148:151],a[236:239],a[40:43]
v_mfma_f32_16x16x32_bf16 a[40:43],a[144:147],a[232:235],a[40:43]
buffer_load_dwordx4 v55,s[28:31],s57 offen offset:3072 lds
v_mfma_f32_16x16x32_bf16 a[72:75],a[144:147],a[240:243],a[72:75]
s_add_u32 s57,s57,128
v_mfma_f32_16x16x32_bf16 a[72:75],a[148:151],a[244:247],a[72:75]
)A"
R"A(v_mfma_f32_16x16x32_bf16 a[104:107],a[148:151],a[252:255],a[104:107]
v_mfma_f32_16x16x32_bf16 a[104:107],a[144:147],a[248:251],a[104:107]
v_mfma_f32_16x16x32_bf16 a[12:15],a[152:155],a[224:227],a[12:15]
v_mfma_f32_16x16x32_bf16 a[12:15],a[156:159],a[228:231],a[12:15]
v_mfma_f32_16x16x32_bf16 a[44:47],a[156:159],a[236:239],a[44:47]
buffer_load_dword v16,v15,s[72:75],s83 offen
v_mfma_f32_16x16x32_bf16 a[44:47],a[152:155],a[232:235],a[44:47]
s_add_u32 s83,s83,896
v_mfma_f32_16x16x32_bf16 a[76:79],a[152:155],a[240:243],a[76:79]
v_mfma_f32_16x16x32_bf16 a[76:79],a[156:159],a[244:247],a[76:79]
v_mfma_f32_16x16x32_bf16 a[108:111],a[156:159],a[252:255],a[108:111]
v_mfma_f32_16x16x32_bf16 a[108:111],a[152:155],a[248:251],a[108:111]
s_waitcnt vmcnt(17) lgkmcnt(0)
v_mfma_f32_16x16x32_bf16 a[16:19],a[160:163],a[224:227],a[16:19]
s_barrier
ds_read_b128 a[192:195],v20
v_mfma_f32_16x16x32_bf16 a[16:19],a[164:167],a[228:231],a[16:19]
ds_read_b128 a[196:199],v21
v_mfma_f32_16x16x32_bf16 a[48:51],a[164:167],a[236:239],a[48:51]
ds_read_b128 a[200:203],v20 offset:2048
v_mfma_f32_16x16x32_bf16 a[48:51],a[160:163],a[232:235],a[48:51]
ds_read_b128 a[204:207],v21 offset:2048
v_mfma_f32_16x16x32_bf16 a[80:83],a[160:163],a[240:243],a[80:83]
ds_read_b128 a[208:211],v20 offset:4096
v_mfma_f32_16x16x32_bf16 a[80:83],a[164:167],a[244:247],a[80:83]
ds_read_b128 a[212:215],v21 offset:4096
v_mfma_f32_16x16x32_bf16 a[112:115],a[164:167],a[252:255],a[112:115]
buffer_load_dword v26,v24,s[88:91],s93 offen
v_mfma_f32_16x16x32_bf16 a[112:115],a[160:163],a[248:251],a[112:115]
ds_read_b128 a[216:219],v20 offset:6144
v_mfma_f32_16x16x32_bf16 a[20:23],a[168:171],a[224:227],a[20:23]
ds_read_b128 a[220:223],v21 offset:6144
v_mfma_f32_16x16x32_bf16 a[20:23],a[172:175],a[228:231],a[20:23]
ds_read_b64_tr_b16 a[128:129],v22 offset:1024
v_mfma_f32_16x16x32_bf16 a[52:55],a[172:175],a[236:239],a[52:55]
ds_read_b64_tr_b16 a[130:131],v22 offset:3072
v_mfma_f32_16x16x32_bf16 a[52:55],a[168:171],a[232:235],a[52:55]
ds_read_b64_tr_b16 a[132:133],v22 offset:17408
v_mfma_f32_16x16x32_bf16 a[84:87],a[168:171],a[240:243],a[84:87]
ds_read_b64_tr_b16 a[134:135],v22 offset:19456
v_mfma_f32_16x16x32_bf16 a[84:87],a[172:175],a[244:247],a[84:87]
ds_read_b64_tr_b16 a[136:137],v22 offset:1032
v_mfma_f32_16x16x32_bf16 a[116:119],a[172:175],a[252:255],a[116:119]
ds_read_b64_tr_b16 a[138:139],v22 offset:3080
v_mfma_f32_16x16x32_bf16 a[116:119],a[168:171],a[248:251],a[116:119]
ds_read_b64_tr_b16 a[140:141],v22 offset:17416
v_mfma_f32_16x16x32_bf16 a[24:27],a[176:179],a[224:227],a[24:27]
ds_read_b64_tr_b16 a[142:143],v22 offset:19464
v_mfma_f32_16x16x32_bf16 a[24:27],a[180:183],a[228:231],a[24:27]
ds_read_b64_tr_b16 a[144:145],v22 offset:1280
v_mfma_f32_16x16x32_bf16 a[56:59],a[180:183],a[236:239],a[56:59]
ds_read_b64_tr_b16 a[146:147],v22 offset:3328
v_mfma_f32_16x16x32_bf16 a[56:59],a[176:179],a[232:235],a[56:59]
ds_read_b64_tr_b16 a[148:149],v22 offset:17664
v_mfma_f32_16x16x32_bf16 a[88:91],a[176:179],a[240:243],a[88:91]
ds_read_b64_tr_b16 a[150:151],v22 offset:19712
v_mfma_f32_16x16x32_bf16 a[88:91],a[180:183],a[244:247],a[88:91]
ds_read_b64_tr_b16 a[152:153],v22 offset:1288
v_mfma_f32_16x16x32_bf16 a[120:123],a[180:183],a[252:255],a[120:123]
ds_read_b64_tr_b16 a[154:155],v22 offset:3336
v_mfma_f32_16x16x32_bf16 a[120:123],a[176:179],a[248:251],a[120:123]
ds_read_b64_tr_b16 a[156:157],v22 offset:17672
v_mfma_f32_16x16x32_bf16 a[28:31],a[184:187],a[224:227],a[28:31]
ds_read_b64_tr_b16 a[158:159],v22 offset:19720
v_mfma_f32_16x16x32_bf16 a[28:31],a[188:191],a[228:231],a[28:31]
v_mfma_f32_16x16x32_bf16 a[60:63],a[188:191],a[236:239],a[60:63]
v_mfma_f32_16x16x32_bf16 a[60:63],a[184:187],a[232:235],a[60:63]
v_mfma_f32_16x16x32_bf16 a[92:95],a[184:187],a[240:243],a[92:95]
v_mfma_f32_16x16x32_bf16 a[92:95],a[188:191],a[244:247],a[92:95]
v_mfma_f32_16x16x32_bf16 a[124:127],a[188:191],a[252:255],a[124:127]
s_cmp_lg_u32 s53,0
v_mfma_f32_16x16x32_bf16 a[124:127],a[184:187],a[248:251],a[124:127]
s_cbranch_scc1 L_qp_%=
L_qpx_%=:
L_tailseg_%=:
s_waitcnt lgkmcnt(1)
v_mfma_f32_16x16x32_bf16 v[128:131],a[128:131],a[192:195],v[128:131]
s_add_u32 m0,98304,s51
ds_read_b64_tr_b16 a[160:161],v22 offset:1536
v_xor_b32 v20,131072,v20
v_mfma_f32_16x16x32_bf16 v[128:131],a[132:135],a[196:199],v[128:131]
s_add_u32 s54,s54,16384
buffer_load_dwordx4 v4,s[20:23],s47 offen nt lds
v_mfma_f32_16x16x32_bf16 v[136:139],a[132:135],a[204:207],v[136:139]
s_and_b32 s46,s54,0xfffc0000
ds_read_b64_tr_b16 a[162:163],v22 offset:3584
v_xor_b32 v21,131072,v21
v_mfma_f32_16x16x32_bf16 v[136:139],a[128:131],a[200:203],v[136:139]
s_add_u32 s54,s54,16384
ds_read_b64_tr_b16 a[164:165],v22 offset:17920
v_and_b32 v18,48,v6
v_mfma_f32_16x16x32_bf16 v[144:147],a[128:131],a[208:211],v[144:147]
s_mov_b32 s48,-98304
ds_read_b64_tr_b16 a[166:167],v22 offset:19968
v_lshrrev_b32 v19,4,v6
v_mfma_f32_16x16x32_bf16 v[144:147],a[132:135],a[212:215],v[144:147]
s_lshr_b32 s79,%1,3
ds_read_b64_tr_b16 a[168:169],v22 offset:1544
v_lshlrev_b32 v18,2,v18
v_mfma_f32_16x16x32_bf16 v[152:155],a[132:135],a[220:223],v[152:155]
s_mul_i32 s79,s79,7
ds_read_b64_tr_b16 a[170:171],v22 offset:3592
v_and_b32 v19,48,v19
v_mfma_f32_16x16x32_bf16 v[152:155],a[128:131],a[216:219],v[152:155]
s_mov_b32 s60,s78
ds_read_b64_tr_b16 a[172:173],v22 offset:17928
v_add_u32 v18,v18,v19
v_mfma_f32_16x16x32_bf16 v[132:135],a[136:139],a[192:195],v[132:135]
s_lshr_b32 s79,s79,5
ds_read_b64_tr_b16 a[174:175],v22 offset:19976
v_add_u32 v18,98304,v18
v_mfma_f32_16x16x32_bf16 v[132:135],a[140:143],a[196:199],v[132:135]
s_sub_u32 s60,s60,s79
s_mul_i32 s83,s41,5
ds_read_b64_tr_b16 a[176:177],v22 offset:1792
v_mfma_f32_16x16x32_bf16 v[140:143],a[140:143],a[204:207],v[140:143]
s_lshl_b32 s44,s40,4
s_add_u32 s83,s83,s42
ds_read_b64_tr_b16 a[178:179],v22 offset:3840
v_mfma_f32_16x16x32_bf16 v[140:143],a[136:139],a[200:203],v[140:143]
s_and_b32 s44,s44,32
ds_read_b64_tr_b16 a[180:181],v22 offset:18176
v_subrev_u32 v25,s51,v4
v_mfma_f32_16x16x32_bf16 v[148:151],a[136:139],a[208:211],v[148:151]
s_add_u32 s83,s83,15
s_lshl_b32 s80,s40,10
ds_read_b64_tr_b16 a[182:183],v22 offset:20224
v_mfma_f32_16x16x32_bf16 v[148:151],a[140:143],a[212:215],v[148:151]
s_and_b32 s83,s83,15
ds_read_b64_tr_b16 a[184:185],v22 offset:1800
v_xor_b32 v25,s44,v25
v_mfma_f32_16x16x32_bf16 v[156:159],a[140:143],a[220:223],v[156:159]
s_add_u32 s83,s83,16
ds_read_b64_tr_b16 a[186:187],v22 offset:3848
v_add_u32 v25,s80,v25
v_mfma_f32_16x16x32_bf16 v[156:159],a[136:139],a[216:219],v[156:159]
s_add_u32 s93,s92,L_att0x_%=-L_pc_%=
s_mov_b32 s50,32768
ds_read_b64_tr_b16 a[188:189],v22 offset:18184
v_mfma_f32_16x16x32_bf16 v[160:163],a[144:147],a[192:195],v[160:163]
s_mov_b32 s49,98304
s_mov_b32 s45,0x1249
ds_read_b64_tr_b16 a[190:191],v22 offset:20232
v_mfma_f32_16x16x32_bf16 v[160:163],a[148:151],a[196:199],v[160:163]
s_add_u32 s80,s80,131072
buffer_load_dwordx4 v4,s[20:23],s47 offen offset:1024 nt lds
v_mfma_f32_16x16x32_bf16 v[168:171],a[148:151],a[204:207],v[168:171]
s_lshl_b32 s83,s83,14
s_waitcnt vmcnt(4)
ds_read_b128 a[224:227],v20
v_mfma_f32_16x16x32_bf16 v[168:171],a[144:147],a[200:203],v[168:171]
ds_read_b64_tr_b16 a[128:129],v22
v_mfma_f32_16x16x32_bf16 v[176:179],a[144:147],a[208:211],v[176:179]
ds_read_b64_tr_b16 a[130:131],v22 offset:2048
v_mfma_f32_16x16x32_bf16 v[176:179],a[148:151],a[212:215],v[176:179]
ds_read_b64_tr_b16 a[132:133],v22 offset:16384
v_mfma_f32_16x16x32_bf16 v[184:187],a[148:151],a[220:223],v[184:187]
ds_read_b64_tr_b16 a[134:135],v22 offset:18432
v_mfma_f32_16x16x32_bf16 v[184:187],a[144:147],a[216:219],v[184:187]
ds_read_b64_tr_b16 a[144:145],v22 offset:8
v_mfma_f32_16x16x32_bf16 v[164:167],a[152:155],a[192:195],v[164:167]
s_waitcnt lgkmcnt(6)
ds_read_b64_tr_b16 a[146:147],v22 offset:2056
v_mfma_f32_16x16x32_bf16 v[164:167],a[156:159],a[196:199],v[164:167]
ds_read_b64_tr_b16 a[148:149],v22 offset:16392
v_mfma_f32_16x16x32_bf16 v[172:175],a[156:159],a[204:207],v[172:175]
ds_read_b64_tr_b16 a[150:151],v22 offset:18440
v_mfma_f32_16x16x32_bf16 v[172:175],a[152:155],a[200:203],v[172:175]
v_readfirstlane_b32 s68,v13
s_nop 1
s_add_u32 s68,s68,1
v_mfma_f32_16x16x32_bf16 v[180:183],a[152:155],a[208:211],v[180:183]
s_xor_b32 s69,s68,0x9e3779b9
ds_read_b128 a[228:231],v21
v_mfma_f32_16x16x32_bf16 v[180:183],a[156:159],a[212:215],v[180:183]
v_mfma_f32_16x16x32_bf16 v[188:191],a[156:159],a[220:223],v[188:191]
ds_read_b128 a[232:235],v20 offset:2048
v_mfma_f32_16x16x32_bf16 v[188:191],a[152:155],a[216:219],v[188:191]
v_mfma_f32_16x16x32_bf16 v[192:195],a[160:163],a[192:195],v[192:195]
ds_read_b128 a[236:239],v21 offset:2048
ds_read_b128 a[240:243],v20 offset:4096
ds_read_b128 a[244:247],v21 offset:4096
ds_read_b128 a[248:251],v20 offset:6144
ds_read_b128 a[252:255],v21 offset:6144
v_mfma_f32_16x16x32_bf16 v[192:195],a[164:167],a[196:199],v[192:195]
v_mfma_f32_16x16x32_bf16 v[200:203],a[164:167],a[204:207],v[200:203]
v_mfma_f32_16x16x32_bf16 v[200:203],a[160:163],a[200:203],v[200:203]
v_mfma_f32_16x16x32_bf16 v[208:211],a[160:163],a[208:211],v[208:211]
buffer_load_dwordx4 v5,s[20:23],s47 offen offset:2048 nt lds
v_mfma_f32_16x16x32_bf16 v[208:211],a[164:167],a[212:215],v[208:211]
v_mfma_f32_16x16x32_bf16 v[216:219],a[164:167],a[220:223],v[216:219]
s_waitcnt vmcnt(4) lgkmcnt(0)
v_mfma_f32_16x16x32_bf16 v[216:219],a[160:163],a[216:219],v[216:219]
s_barrier
ds_read_b64_tr_b16 a[136:137],v22 offset:32768
v_mfma_f32_16x16x32_bf16 v[196:199],a[168:171],a[192:195],v[196:199]
ds_read_b64_tr_b16 a[138:139],v22 offset:34816
v_mfma_f32_16x16x32_bf16 v[196:199],a[172:175],a[196:199],v[196:199]
ds_read_b64_tr_b16 a[140:141],v22 offset:49152
v_mfma_f32_16x16x32_bf16 v[204:207],a[172:175],a[204:207],v[204:207]
ds_read_b64_tr_b16 a[142:143],v22 offset:51200
v_mfma_f32_16x16x32_bf16 v[204:207],a[168:171],a[200:203],v[204:207]
ds_read_b64_tr_b16 a[152:153],v22 offset:32776
v_mfma_f32_16x16x32_bf16 v[212:215],a[168:171],a[208:211],v[212:215]
ds_read_b64_tr_b16 a[154:155],v22 offset:34824
v_mfma_f32_16x16x32_bf16 v[212:215],a[172:175],a[212:215],v[212:215]
ds_read_b64_tr_b16 a[156:157],v22 offset:49160
v_mfma_f32_16x16x32_bf16 v[220:223],a[172:175],a[220:223],v[220:223]
ds_read_b64_tr_b16 a[158:159],v22 offset:51208
v_mfma_f32_16x16x32_bf16 v[220:223],a[168:171],a[216:219],v[220:223]
ds_read_b64_tr_b16 a[160:161],v22 offset:256
v_mfma_f32_16x16x32_bf16 v[224:227],a[176:179],a[192:195],v[224:227]
buffer_load_dwordx4 v5,s[20:23],s47 offen offset:3072 nt lds
v_mfma_f32_16x16x32_bf16 v[224:227],a[180:183],a[196:199],v[224:227]
ds_read_b64_tr_b16 a[162:163],v22 offset:2304
v_mfma_f32_16x16x32_bf16 v[232:235],a[180:183],a[204:207],v[232:235]
ds_read_b64_tr_b16 a[164:165],v22 offset:16640
v_mfma_f32_16x16x32_bf16 v[232:235],a[176:179],a[200:203],v[232:235]
ds_read_b64_tr_b16 a[166:167],v22 offset:18688
v_mfma_f32_16x16x32_bf16 v[240:243],a[176:179],a[208:211],v[240:243]
ds_read_b64_tr_b16 a[168:169],v22 offset:33024
v_mfma_f32_16x16x32_bf16 v[240:243],a[180:183],a[212:215],v[240:243]
ds_read_b64_tr_b16 a[170:171],v22 offset:35072
v_mfma_f32_16x16x32_bf16 v[248:251],a[180:183],a[220:223],v[248:251]
ds_read_b64_tr_b16 a[172:173],v22 offset:49408
v_mfma_f32_16x16x32_bf16 v[248:251],a[176:179],a[216:219],v[248:251]
ds_read_b64_tr_b16 a[174:175],v22 offset:51456
v_mfma_f32_16x16x32_bf16 v[228:231],a[184:187],a[192:195],v[228:231]
ds_read_b64_tr_b16 a[176:177],v22 offset:264
v_mfma_f32_16x16x32_bf16 v[228:231],a[188:191],a[196:199],v[228:231]
ds_read_b64_tr_b16 a[178:179],v22 offset:2312
v_mfma_f32_16x16x32_bf16 v[236:239],a[188:191],a[204:207],v[236:239]
ds_read_b64_tr_b16 a[180:181],v22 offset:16648
v_mfma_f32_16x16x32_bf16 v[236:239],a[184:187],a[200:203],v[236:239]
ds_read_b64_tr_b16 a[182:183],v22 offset:18696
v_mfma_f32_16x16x32_bf16 v[244:247],a[184:187],a[208:211],v[244:247]
v_mfma_f32_16x16x32_bf16 v[244:247],a[188:191],a[212:215],v[244:247]
s_add_u32 m0,98304,s52
v_mfma_f32_16x16x32_bf16 v[252:255],a[188:191],a[220:223],v[252:255]
ds_read_b64_tr_b16 a[188:189],v22 offset:49416
v_mfma_f32_16x16x32_bf16 v[252:255],a[184:187],a[216:219],v[252:255]
)A"
R"A(ds_read_b64_tr_b16 a[184:185],v22 offset:33032
v_mfma_f32_16x16x32_bf16 a[0:3],a[128:131],a[192:195],a[0:3]
buffer_load_dwordx4 v4,s[24:27],s47 offen nt lds
v_mfma_f32_16x16x32_bf16 a[0:3],a[132:135],a[196:199],a[0:3]
s_waitcnt lgkmcnt(4)
ds_read_b64_tr_b16 a[186:187],v22 offset:35080
v_mfma_f32_16x16x32_bf16 a[0:3],a[136:139],a[224:227],a[0:3]
ds_read_b64_tr_b16 a[190:191],v22 offset:51464
v_mfma_f32_16x16x32_bf16 a[0:3],a[140:143],a[228:231],a[0:3]
v_mfma_f32_16x16x32_bf16 a[32:35],a[140:143],a[236:239],a[32:35]
v_mfma_f32_16x16x32_bf16 a[32:35],a[136:139],a[232:235],a[32:35]
v_mfma_f32_16x16x32_bf16 a[32:35],a[132:135],a[204:207],a[32:35]
v_mfma_f32_16x16x32_bf16 a[32:35],a[128:131],a[200:203],a[32:35]
v_mfma_f32_16x16x32_bf16 a[64:67],a[128:131],a[208:211],a[64:67]
v_mfma_f32_16x16x32_bf16 a[64:67],a[132:135],a[212:215],a[64:67]
s_nop 1
v_accvgpr_read_b32 v64,a0
v_accvgpr_read_b32 v65,a1
v_mfma_f32_16x16x32_bf16 a[64:67],a[136:139],a[240:243],a[64:67]
v_mul_f32 v64,s61,v64
v_mul_f32 v65,s61,v65
v_mfma_f32_16x16x32_bf16 a[64:67],a[140:143],a[244:247],a[64:67]
v_accvgpr_read_b32 v66,a2
v_accvgpr_read_b32 v67,a3
v_mfma_f32_16x16x32_bf16 a[96:99],a[140:143],a[252:255],a[96:99]
ds_read_b64_tr_b16 a[140:141],v22 offset:49664
v_cvt_pk_bf16_f32 v64,v64,v65
v_mfma_f32_16x16x32_bf16 a[96:99],a[136:139],a[248:251],a[96:99]
ds_read_b64_tr_b16 a[136:137],v22 offset:33280
v_accvgpr_read_b32 v80,a32
v_mfma_f32_16x16x32_bf16 a[96:99],a[132:135],a[220:223],a[96:99]
ds_read_b64_tr_b16 a[132:133],v22 offset:16896
v_accvgpr_read_b32 v81,a33
v_mfma_f32_16x16x32_bf16 a[96:99],a[128:131],a[216:219],a[96:99]
ds_read_b64_tr_b16 a[128:129],v22 offset:512
v_mul_f32 v80,s61,v80
v_mfma_f32_16x16x32_bf16 a[4:7],a[144:147],a[192:195],a[4:7]
buffer_load_dwordx4 v5,s[24:27],s47 offen offset:1024 nt lds
v_mfma_f32_16x16x32_bf16 a[4:7],a[148:151],a[196:199],a[4:7]
ds_read_b64_tr_b16 a[130:131],v22 offset:2560
v_mul_f32 v81,s61,v81
v_mfma_f32_16x16x32_bf16 a[4:7],a[152:155],a[224:227],a[4:7]
ds_read_b64_tr_b16 a[134:135],v22 offset:18944
v_accvgpr_read_b32 v82,a34
v_mfma_f32_16x16x32_bf16 a[4:7],a[156:159],a[228:231],a[4:7]
ds_read_b64_tr_b16 a[138:139],v22 offset:35328
v_accvgpr_read_b32 v83,a35
v_mfma_f32_16x16x32_bf16 a[36:39],a[156:159],a[236:239],a[36:39]
ds_read_b64_tr_b16 a[142:143],v22 offset:51712
v_mul_f32 v66,s61,v66
v_mfma_f32_16x16x32_bf16 a[36:39],a[152:155],a[232:235],a[36:39]
v_mul_f32 v67,s61,v67
v_cvt_pk_bf16_f32 v80,v80,v81
v_mfma_f32_16x16x32_bf16 a[36:39],a[148:151],a[204:207],a[36:39]
v_mul_f32 v82,s61,v82
v_mul_f32 v83,s61,v83
v_mfma_f32_16x16x32_bf16 a[36:39],a[144:147],a[200:203],a[36:39]
v_cvt_pk_bf16_f32 v65,v66,v67
v_cvt_pk_bf16_f32 v81,v82,v83
v_mfma_f32_16x16x32_bf16 a[68:71],a[144:147],a[208:211],a[68:71]
v_accvgpr_read_b32 v96,a64
v_accvgpr_read_b32 v97,a65
v_mfma_f32_16x16x32_bf16 a[68:71],a[148:151],a[212:215],a[68:71]
v_accvgpr_read_b32 v112,a96
v_accvgpr_read_b32 v113,a97
v_mfma_f32_16x16x32_bf16 a[68:71],a[152:155],a[240:243],a[68:71]
v_accvgpr_read_b32 v66,a4
v_accvgpr_read_b32 v67,a5
v_mfma_f32_16x16x32_bf16 a[68:71],a[156:159],a[244:247],a[68:71]
v_mul_f32 v96,s61,v96
v_mul_f32 v97,s61,v97
v_mfma_f32_16x16x32_bf16 a[100:103],a[156:159],a[252:255],a[100:103]
ds_read_b64_tr_b16 a[156:157],v22 offset:49672
v_accvgpr_read_b32 v98,a66
v_mfma_f32_16x16x32_bf16 a[100:103],a[152:155],a[248:251],a[100:103]
ds_read_b64_tr_b16 a[152:153],v22 offset:33288
v_accvgpr_read_b32 v82,a36
v_mfma_f32_16x16x32_bf16 a[100:103],a[148:151],a[220:223],a[100:103]
ds_read_b64_tr_b16 a[148:149],v22 offset:16904
v_accvgpr_read_b32 v83,a37
v_mfma_f32_16x16x32_bf16 a[100:103],a[144:147],a[216:219],a[100:103]
ds_read_b64_tr_b16 a[144:145],v22 offset:520
v_accvgpr_read_b32 v99,a67
v_mfma_f32_16x16x32_bf16 a[8:11],a[160:163],a[192:195],a[8:11]
buffer_load_dwordx4 v4,s[24:27],s47 offen offset:2048 nt lds
v_mfma_f32_16x16x32_bf16 a[8:11],a[164:167],a[196:199],a[8:11]
ds_read_b64_tr_b16 a[146:147],v22 offset:2568
v_mul_f32 v112,s61,v112
v_mfma_f32_16x16x32_bf16 a[8:11],a[168:171],a[224:227],a[8:11]
ds_read_b64_tr_b16 a[150:151],v22 offset:18952
v_mul_f32 v113,s61,v113
v_mfma_f32_16x16x32_bf16 a[8:11],a[172:175],a[228:231],a[8:11]
ds_read_b64_tr_b16 a[154:155],v22 offset:35336
v_accvgpr_read_b32 v114,a98
v_mfma_f32_16x16x32_bf16 a[40:43],a[172:175],a[236:239],a[40:43]
ds_read_b64_tr_b16 a[158:159],v22 offset:51720
v_accvgpr_read_b32 v115,a99
v_mfma_f32_16x16x32_bf16 a[40:43],a[168:171],a[232:235],a[40:43]
v_accvgpr_read_b32 v12,a6
v_accvgpr_read_b32 v13,a7
v_mfma_f32_16x16x32_bf16 a[40:43],a[164:167],a[204:207],a[40:43]
v_mul_f32 v66,s61,v66
v_mul_f32 v67,s61,v67
v_mfma_f32_16x16x32_bf16 a[40:43],a[160:163],a[200:203],a[40:43]
v_accvgpr_read_b32 v14,a38
v_accvgpr_read_b32 v15,a39
v_mfma_f32_16x16x32_bf16 a[72:75],a[160:163],a[208:211],a[72:75]
v_mul_f32 v82,s61,v82
v_mul_f32 v83,s61,v83
v_mfma_f32_16x16x32_bf16 a[72:75],a[164:167],a[212:215],a[72:75]
v_accvgpr_read_b32 v68,a8
v_accvgpr_read_b32 v69,a9
v_mfma_f32_16x16x32_bf16 a[72:75],a[168:171],a[240:243],a[72:75]
v_mul_f32 v68,s61,v68
v_mul_f32 v69,s61,v69
v_mfma_f32_16x16x32_bf16 a[72:75],a[172:175],a[244:247],a[72:75]
v_accvgpr_read_b32 v70,a10
v_accvgpr_read_b32 v71,a11
v_mfma_f32_16x16x32_bf16 a[104:107],a[172:175],a[252:255],a[104:107]
ds_read_b64_tr_b16 a[172:173],v22 offset:49920
v_cvt_pk_bf16_f32 v96,v96,v97
v_mfma_f32_16x16x32_bf16 a[104:107],a[168:171],a[248:251],a[104:107]
ds_read_b64_tr_b16 a[168:169],v22 offset:33536
v_mul_f32 v98,s61,v98
v_mfma_f32_16x16x32_bf16 a[104:107],a[164:167],a[220:223],a[104:107]
ds_read_b64_tr_b16 a[164:165],v22 offset:17152
v_mul_f32 v99,s61,v99
v_mfma_f32_16x16x32_bf16 a[104:107],a[160:163],a[216:219],a[104:107]
ds_read_b64_tr_b16 a[160:161],v22 offset:768
v_cvt_pk_bf16_f32 v112,v112,v113
v_mfma_f32_16x16x32_bf16 a[12:15],a[176:179],a[192:195],a[12:15]
s_waitcnt lgkmcnt(4)
buffer_load_dwordx4 v5,s[24:27],s47 offen offset:3072 nt lds
v_mfma_f32_16x16x32_bf16 a[12:15],a[180:183],a[196:199],a[12:15]
s_add_u32 s47,s47,16384
s_and_b32 s47,s47,0x3ffff
s_or_b32 s47,s47,s46
v_mfma_f32_16x16x32_bf16 a[12:15],a[184:187],a[224:227],a[12:15]
s_and_b32 s46,s54,0xfffc0000
ds_read_b64_tr_b16 a[162:163],v22 offset:2816
v_mul_f32 v114,s61,v114
v_mfma_f32_16x16x32_bf16 a[12:15],a[188:191],a[228:231],a[12:15]
s_add_u32 s54,s54,16384
ds_read_b64_tr_b16 a[166:167],v22 offset:19200
v_mul_f32 v115,s61,v115
v_mfma_f32_16x16x32_bf16 a[44:47],a[188:191],a[236:239],a[44:47]
ds_read_b64_tr_b16 a[170:171],v22 offset:35584
v_cvt_pk_bf16_f32 v68,v68,v69
v_mfma_f32_16x16x32_bf16 a[44:47],a[184:187],a[232:235],a[44:47]
ds_read_b64_tr_b16 a[174:175],v22 offset:51968
v_mul_f32 v70,s61,v70
v_mfma_f32_16x16x32_bf16 a[44:47],a[180:183],a[204:207],a[44:47]
v_mul_f32 v71,s61,v71
v_mul_f32 v12,s61,v12
v_mfma_f32_16x16x32_bf16 a[44:47],a[176:179],a[200:203],a[44:47]
v_mul_f32 v13,s61,v13
v_cvt_pk_bf16_f32 v66,v66,v67
v_mfma_f32_16x16x32_bf16 a[76:79],a[176:179],a[208:211],a[76:79]
v_mul_f32 v14,s61,v14
v_mul_f32 v15,s61,v15
v_mfma_f32_16x16x32_bf16 a[76:79],a[180:183],a[212:215],a[76:79]
v_cvt_pk_bf16_f32 v82,v82,v83
v_cvt_pk_bf16_f32 v97,v98,v99
v_mfma_f32_16x16x32_bf16 a[76:79],a[184:187],a[240:243],a[76:79]
v_cvt_pk_bf16_f32 v113,v114,v115
v_cvt_pk_bf16_f32 v69,v70,v71
v_mfma_f32_16x16x32_bf16 a[76:79],a[188:191],a[244:247],a[76:79]
v_accvgpr_read_b32 v84,a40
v_accvgpr_read_b32 v85,a41
v_mfma_f32_16x16x32_bf16 a[108:111],a[188:191],a[252:255],a[108:111]
ds_read_b64_tr_b16 a[188:189],v22 offset:49928
v_accvgpr_read_b32 v100,a72
v_mfma_f32_16x16x32_bf16 a[108:111],a[184:187],a[248:251],a[108:111]
ds_read_b64_tr_b16 a[184:185],v22 offset:33544
v_accvgpr_read_b32 v101,a73
v_mfma_f32_16x16x32_bf16 a[108:111],a[180:183],a[220:223],a[108:111]
ds_read_b64_tr_b16 a[180:181],v22 offset:17160
v_accvgpr_read_b32 v116,a104
v_mfma_f32_16x16x32_bf16 a[108:111],a[176:179],a[216:219],a[108:111]
ds_read_b64_tr_b16 a[176:177],v22 offset:776
v_accvgpr_read_b32 v117,a105
v_mfma_f32_16x16x32_bf16 a[16:19],a[128:131],a[192:195],a[16:19]
ds_read_b64_tr_b16 a[178:179],v22 offset:2824
v_cvt_pk_bf16_f32 v67,v12,v13
v_mfma_f32_16x16x32_bf16 a[16:19],a[132:135],a[196:199],a[16:19]
ds_read_b64_tr_b16 a[182:183],v22 offset:19208
v_cvt_pk_bf16_f32 v83,v14,v15
v_mfma_f32_16x16x32_bf16 a[16:19],a[136:139],a[224:227],a[16:19]
ds_read_b64_tr_b16 a[186:187],v22 offset:35592
v_accvgpr_read_b32 v98,a68
v_mfma_f32_16x16x32_bf16 a[16:19],a[140:143],a[228:231],a[16:19]
s_add_u32 m0,0,s51
ds_read_b64_tr_b16 a[190:191],v22 offset:51976
v_accvgpr_read_b32 v99,a69
v_mfma_f32_16x16x32_bf16 a[48:51],a[140:143],a[236:239],a[48:51]
v_accvgpr_read_b32 v114,a100
v_accvgpr_read_b32 v115,a101
v_mfma_f32_16x16x32_bf16 a[48:51],a[136:139],a[232:235],a[48:51]
v_accvgpr_read_b32 v70,a12
v_accvgpr_read_b32 v71,a13
v_mfma_f32_16x16x32_bf16 a[48:51],a[132:135],a[204:207],a[48:51]
buffer_load_dwordx4 v4,s[20:23],s47 offen nt lds
v_mfma_f32_16x16x32_bf16 a[48:51],a[128:131],a[200:203],a[48:51]
v_mul_f32 v84,s61,v84
v_mul_f32 v85,s61,v85
v_mfma_f32_16x16x32_bf16 a[80:83],a[128:131],a[208:211],a[80:83]
v_accvgpr_read_b32 v86,a42
v_accvgpr_read_b32 v87,a43
v_mfma_f32_16x16x32_bf16 a[80:83],a[132:135],a[212:215],a[80:83]
v_mul_f32 v100,s61,v100
v_mul_f32 v101,s61,v101
v_mfma_f32_16x16x32_bf16 a[80:83],a[136:139],a[240:243],a[80:83]
v_accvgpr_read_b32 v102,a74
v_accvgpr_read_b32 v103,a75
v_mfma_f32_16x16x32_bf16 a[80:83],a[140:143],a[244:247],a[80:83]
v_mul_f32 v116,s61,v116
v_mul_f32 v117,s61,v117
v_mfma_f32_16x16x32_bf16 a[112:115],a[140:143],a[252:255],a[112:115]
ds_read_b64_tr_b16 a[140:141],v22 offset:50184
v_accvgpr_read_b32 v118,a106
v_mfma_f32_16x16x32_bf16 a[112:115],a[136:139],a[248:251],a[112:115]
ds_read_b64_tr_b16 a[136:137],v22 offset:33800
v_accvgpr_read_b32 v119,a107
v_mfma_f32_16x16x32_bf16 a[112:115],a[132:135],a[220:223],a[112:115]
ds_read_b64_tr_b16 a[132:133],v22 offset:50176
v_accvgpr_read_b32 v16,a70
v_mfma_f32_16x16x32_bf16 a[112:115],a[128:131],a[216:219],a[112:115]
ds_read_b64_tr_b16 a[128:129],v22 offset:33792
v_accvgpr_read_b32 v17,a71
v_mfma_f32_16x16x32_bf16 a[20:23],a[144:147],a[192:195],a[20:23]
ds_read_b64_tr_b16 a[130:131],v22 offset:35840
v_mul_f32 v98,s61,v98
v_mfma_f32_16x16x32_bf16 a[20:23],a[148:151],a[196:199],a[20:23]
ds_read_b64_tr_b16 a[134:135],v22 offset:52224
v_mul_f32 v99,s61,v99
v_mfma_f32_16x16x32_bf16 a[20:23],a[152:155],a[224:227],a[20:23]
ds_read_b64_tr_b16 a[138:139],v22 offset:35848
v_accvgpr_read_b32 v12,a102
v_mfma_f32_16x16x32_bf16 a[20:23],a[156:159],a[228:231],a[20:23]
ds_read_b64_tr_b16 a[142:143],v22 offset:52232
v_accvgpr_read_b32 v13,a103
v_mfma_f32_16x16x32_bf16 a[52:55],a[156:159],a[236:239],a[52:55]
v_mul_f32 v114,s61,v114
v_mul_f32 v115,s61,v115
v_mfma_f32_16x16x32_bf16 a[52:55],a[152:155],a[232:235],a[52:55]
v_accvgpr_read_b32 v14,a14
v_accvgpr_read_b32 v15,a15
v_mfma_f32_16x16x32_bf16 a[52:55],a[148:151],a[204:207],a[52:55]
buffer_load_dwordx4 v4,s[20:23],s47 offen offset:1024 nt lds
v_mfma_f32_16x16x32_bf16 a[52:55],a[144:147],a[200:203],a[52:55]
v_mul_f32 v70,s61,v70
v_mul_f32 v71,s61,v71
v_mfma_f32_16x16x32_bf16 a[84:87],a[144:147],a[208:211],a[84:87]
v_cvt_pk_bf16_f32 v84,v84,v85
v_mul_f32 v86,s61,v86
v_mfma_f32_16x16x32_bf16 a[84:87],a[148:151],a[212:215],a[84:87]
v_mul_f32 v87,s61,v87
v_cvt_pk_bf16_f32 v100,v100,v101
v_mfma_f32_16x16x32_bf16 a[84:87],a[152:155],a[240:243],a[84:87]
v_mul_f32 v102,s61,v102
v_mul_f32 v103,s61,v103
v_mfma_f32_16x16x32_bf16 a[84:87],a[156:159],a[244:247],a[84:87]
v_cvt_pk_bf16_f32 v116,v116,v117
v_mul_f32 v118,s61,v118
v_mfma_f32_16x16x32_bf16 a[116:119],a[156:159],a[252:255],a[116:119]
ds_read_b64_tr_b16 a[156:157],v22 offset:50440
v_mul_f32 v119,s61,v119
v_mfma_f32_16x16x32_bf16 a[116:119],a[152:155],a[248:251],a[116:119]
ds_read_b64_tr_b16 a[152:153],v22 offset:34056
v_mul_f32 v16,s61,v16
v_mfma_f32_16x16x32_bf16 a[116:119],a[148:151],a[220:223],a[116:119]
ds_read_b64_tr_b16 a[148:149],v22 offset:50432
v_mul_f32 v17,s61,v17
v_mfma_f32_16x16x32_bf16 a[116:119],a[144:147],a[216:219],a[116:119]
s_waitcnt lgkmcnt(3)
)A"
R"A(ds_read_b64_tr_b16 a[144:145],v22 offset:34048
v_cvt_pk_bf16_f32 v98,v98,v99
v_mfma_f32_16x16x32_bf16 a[24:27],a[160:163],a[192:195],a[24:27]
ds_read_b64_tr_b16 a[146:147],v22 offset:36096
v_mul_f32 v12,s61,v12
v_mfma_f32_16x16x32_bf16 a[24:27],a[164:167],a[196:199],a[24:27]
ds_read_b64_tr_b16 a[150:151],v22 offset:52480
v_mul_f32 v13,s61,v13
v_mfma_f32_16x16x32_bf16 a[24:27],a[168:171],a[224:227],a[24:27]
ds_read_b64_tr_b16 a[154:155],v22 offset:36104
v_cvt_pk_bf16_f32 v114,v114,v115
v_mfma_f32_16x16x32_bf16 a[24:27],a[172:175],a[228:231],a[24:27]
ds_read_b64_tr_b16 a[158:159],v22 offset:52488
v_mul_f32 v14,s61,v14
v_mfma_f32_16x16x32_bf16 a[56:59],a[172:175],a[236:239],a[56:59]
v_mul_f32 v15,s61,v15
v_cvt_pk_bf16_f32 v70,v70,v71
v_mfma_f32_16x16x32_bf16 a[56:59],a[168:171],a[232:235],a[56:59]
v_cvt_pk_bf16_f32 v85,v86,v87
v_cvt_pk_bf16_f32 v101,v102,v103
v_mfma_f32_16x16x32_bf16 a[56:59],a[164:167],a[204:207],a[56:59]
buffer_load_dwordx4 v5,s[20:23],s47 offen offset:2048 nt lds
v_mfma_f32_16x16x32_bf16 a[56:59],a[160:163],a[200:203],a[56:59]
v_cvt_pk_bf16_f32 v117,v118,v119
v_accvgpr_read_b32 v72,a16
v_mfma_f32_16x16x32_bf16 a[88:91],a[160:163],a[208:211],a[88:91]
v_accvgpr_read_b32 v73,a17
v_accvgpr_read_b32 v88,a48
v_mfma_f32_16x16x32_bf16 a[88:91],a[164:167],a[212:215],a[88:91]
v_accvgpr_read_b32 v89,a49
v_accvgpr_read_b32 v104,a80
v_mfma_f32_16x16x32_bf16 a[88:91],a[168:171],a[240:243],a[88:91]
v_accvgpr_read_b32 v105,a81
v_cvt_pk_bf16_f32 v99,v16,v17
v_mfma_f32_16x16x32_bf16 a[88:91],a[172:175],a[244:247],a[88:91]
v_cvt_pk_bf16_f32 v115,v12,v13
v_cvt_pk_bf16_f32 v71,v14,v15
v_mfma_f32_16x16x32_bf16 a[120:123],a[172:175],a[252:255],a[120:123]
ds_read_b64_tr_b16 a[172:173],v22 offset:50696
v_accvgpr_read_b32 v86,a44
v_mfma_f32_16x16x32_bf16 a[120:123],a[168:171],a[248:251],a[120:123]
ds_read_b64_tr_b16 a[168:169],v22 offset:34312
v_accvgpr_read_b32 v87,a45
v_mfma_f32_16x16x32_bf16 a[120:123],a[164:167],a[220:223],a[120:123]
ds_read_b64_tr_b16 a[164:165],v22 offset:50688
v_accvgpr_read_b32 v102,a76
v_mfma_f32_16x16x32_bf16 a[120:123],a[160:163],a[216:219],a[120:123]
ds_read_b64_tr_b16 a[160:161],v22 offset:34304
v_accvgpr_read_b32 v103,a77
v_mfma_f32_16x16x32_bf16 a[28:31],a[176:179],a[192:195],a[28:31]
ds_read_b64_tr_b16 a[162:163],v22 offset:36352
v_accvgpr_read_b32 v118,a108
v_mfma_f32_16x16x32_bf16 a[28:31],a[180:183],a[196:199],a[28:31]
ds_read_b64_tr_b16 a[166:167],v22 offset:52736
v_accvgpr_read_b32 v119,a109
v_mfma_f32_16x16x32_bf16 a[28:31],a[184:187],a[224:227],a[28:31]
ds_read_b64_tr_b16 a[170:171],v22 offset:36360
v_mul_f32 v72,s61,v72
v_mfma_f32_16x16x32_bf16 a[28:31],a[188:191],a[228:231],a[28:31]
ds_read_b64_tr_b16 a[174:175],v22 offset:52744
v_mul_f32 v73,s61,v73
v_mfma_f32_16x16x32_bf16 a[60:63],a[188:191],a[236:239],a[60:63]
v_accvgpr_read_b32 v74,a18
v_accvgpr_read_b32 v75,a19
v_mfma_f32_16x16x32_bf16 a[60:63],a[184:187],a[232:235],a[60:63]
v_mul_f32 v88,s61,v88
v_mul_f32 v89,s61,v89
v_mfma_f32_16x16x32_bf16 a[60:63],a[180:183],a[204:207],a[60:63]
buffer_load_dwordx4 v5,s[20:23],s47 offen offset:3072 nt lds
v_mfma_f32_16x16x32_bf16 a[60:63],a[176:179],a[200:203],a[60:63]
v_accvgpr_read_b32 v90,a50
v_accvgpr_read_b32 v91,a51
v_mfma_f32_16x16x32_bf16 a[92:95],a[176:179],a[208:211],a[92:95]
v_mul_f32 v104,s61,v104
v_mul_f32 v105,s61,v105
v_mfma_f32_16x16x32_bf16 a[92:95],a[180:183],a[212:215],a[92:95]
v_accvgpr_read_b32 v106,a82
v_accvgpr_read_b32 v107,a83
v_mfma_f32_16x16x32_bf16 a[92:95],a[184:187],a[240:243],a[92:95]
v_accvgpr_read_b32 v16,a46
v_accvgpr_read_b32 v17,a47
v_mfma_f32_16x16x32_bf16 a[92:95],a[188:191],a[244:247],a[92:95]
v_mul_f32 v86,s61,v86
v_mul_f32 v87,s61,v87
v_mfma_f32_16x16x32_bf16 a[124:127],a[188:191],a[252:255],a[124:127]
ds_read_b64_tr_b16 a[188:189],v22 offset:50952
v_accvgpr_read_b32 v12,a78
v_mfma_f32_16x16x32_bf16 a[124:127],a[184:187],a[248:251],a[124:127]
ds_read_b64_tr_b16 a[184:185],v22 offset:34568
v_accvgpr_read_b32 v13,a79
v_mfma_f32_16x16x32_bf16 a[124:127],a[180:183],a[220:223],a[124:127]
ds_read_b64_tr_b16 a[180:181],v22 offset:50944
v_mul_f32 v102,s61,v102
v_mfma_f32_16x16x32_bf16 a[124:127],a[176:179],a[216:219],a[124:127]
ds_read_b64_tr_b16 a[176:177],v22 offset:34560
v_mul_f32 v103,s61,v103
v_mfma_f32_16x16x32_bf16 v[128:131],a[128:131],a[224:227],v[128:131]
ds_read_b64_tr_b16 a[178:179],v22 offset:36608
v_accvgpr_read_b32 v14,a110
v_mfma_f32_16x16x32_bf16 v[128:131],a[132:135],a[228:231],v[128:131]
ds_read_b64_tr_b16 a[182:183],v22 offset:52992
v_accvgpr_read_b32 v15,a111
v_mfma_f32_16x16x32_bf16 v[136:139],a[132:135],a[236:239],v[136:139]
ds_read_b64_tr_b16 a[186:187],v22 offset:36616
v_mul_f32 v118,s61,v118
v_mfma_f32_16x16x32_bf16 v[136:139],a[128:131],a[232:235],v[136:139]
s_add_u32 m0,0,s52
ds_read_b64_tr_b16 a[190:191],v22 offset:53000
v_mul_f32 v119,s61,v119
v_mfma_f32_16x16x32_bf16 v[144:147],a[128:131],a[240:243],v[144:147]
v_cvt_pk_bf16_f32 v72,v72,v73
v_mul_f32 v74,s61,v74
v_mfma_f32_16x16x32_bf16 v[144:147],a[132:135],a[244:247],v[144:147]
v_mul_f32 v75,s61,v75
v_cvt_pk_bf16_f32 v88,v88,v89
v_mfma_f32_16x16x32_bf16 v[152:155],a[132:135],a[252:255],v[152:155]
buffer_load_dwordx4 v4,s[24:27],s47 offen nt lds
v_mfma_f32_16x16x32_bf16 v[152:155],a[128:131],a[248:251],v[152:155]
v_mul_f32 v90,s61,v90
v_mul_f32 v91,s61,v91
v_mfma_f32_16x16x32_bf16 v[132:135],a[136:139],a[224:227],v[132:135]
v_cvt_pk_bf16_f32 v104,v104,v105
v_mul_f32 v106,s61,v106
v_mfma_f32_16x16x32_bf16 v[132:135],a[140:143],a[228:231],v[132:135]
v_mul_f32 v107,s61,v107
v_mul_f32 v16,s61,v16
v_mfma_f32_16x16x32_bf16 v[140:143],a[140:143],a[236:239],v[140:143]
v_mul_f32 v17,s61,v17
v_cvt_pk_bf16_f32 v86,v86,v87
v_mfma_f32_16x16x32_bf16 v[140:143],a[136:139],a[232:235],v[140:143]
v_mul_f32 v12,s61,v12
v_mul_f32 v13,s61,v13
v_mfma_f32_16x16x32_bf16 v[148:151],a[136:139],a[240:243],v[148:151]
v_cvt_pk_bf16_f32 v102,v102,v103
v_mul_f32 v14,s61,v14
v_mfma_f32_16x16x32_bf16 v[148:151],a[140:143],a[244:247],v[148:151]
v_mul_f32 v15,s61,v15
v_cvt_pk_bf16_f32 v118,v118,v119
v_mfma_f32_16x16x32_bf16 v[156:159],a[140:143],a[252:255],v[156:159]
s_waitcnt vmcnt(5) lgkmcnt(0)
v_cvt_pk_bf16_f32 v73,v74,v75
v_cvt_pk_bf16_f32 v89,v90,v91
v_mfma_f32_16x16x32_bf16 v[156:159],a[136:139],a[248:251],v[156:159]
s_barrier
ds_read_b128 v[48:51],v18
v_cvt_pk_bf16_f32 v105,v106,v107
v_mfma_f32_16x16x32_bf16 v[160:163],a[144:147],a[224:227],v[160:163]
v_cvt_pk_bf16_f32 v87,v16,v17
v_cvt_pk_bf16_f32 v103,v12,v13
v_mfma_f32_16x16x32_bf16 v[160:163],a[148:151],a[228:231],v[160:163]
ds_read_b128 v[52:55],v18 offset:256
v_cvt_pk_bf16_f32 v119,v14,v15
v_mfma_f32_16x16x32_bf16 v[168:171],a[148:151],a[236:239],v[168:171]
v_accvgpr_read_b32 v74,a20
v_accvgpr_read_b32 v75,a21
v_mfma_f32_16x16x32_bf16 v[168:171],a[144:147],a[232:235],v[168:171]
ds_read_b128 v[56:59],v18 offset:512
v_accvgpr_read_b32 v90,a52
v_mfma_f32_16x16x32_bf16 v[176:179],a[144:147],a[240:243],v[176:179]
v_accvgpr_read_b32 v91,a53
v_accvgpr_read_b32 v106,a84
v_mfma_f32_16x16x32_bf16 v[176:179],a[148:151],a[244:247],v[176:179]
buffer_load_dwordx4 v5,s[24:27],s47 offen offset:1024 nt lds
v_mfma_f32_16x16x32_bf16 v[184:187],a[148:151],a[252:255],v[184:187]
ds_read_b128 v[60:63],v18 offset:768
v_accvgpr_read_b32 v107,a85
v_mfma_f32_16x16x32_bf16 v[184:187],a[144:147],a[248:251],v[184:187]
v_accvgpr_read_b32 v16,a22
v_accvgpr_read_b32 v17,a23
v_mfma_f32_16x16x32_bf16 v[164:167],a[152:155],a[224:227],v[164:167]
v_mul_f32 v74,s61,v74
v_mul_f32 v75,s61,v75
v_mfma_f32_16x16x32_bf16 v[164:167],a[156:159],a[228:231],v[164:167]
v_accvgpr_read_b32 v12,a54
v_accvgpr_read_b32 v13,a55
v_mfma_f32_16x16x32_bf16 v[172:175],a[156:159],a[236:239],v[172:175]
v_mul_f32 v90,s61,v90
v_mul_f32 v91,s61,v91
v_mfma_f32_16x16x32_bf16 v[172:175],a[152:155],a[232:235],v[172:175]
v_accvgpr_read_b32 v14,a86
v_accvgpr_read_b32 v15,a87
v_mfma_f32_16x16x32_bf16 v[180:183],a[152:155],a[240:243],v[180:183]
v_mul_f32 v106,s61,v106
v_mul_f32 v107,s61,v107
v_mfma_f32_16x16x32_bf16 v[180:183],a[156:159],a[244:247],v[180:183]
v_mul_f32 v16,s61,v16
v_mul_f32 v17,s61,v17
v_mfma_f32_16x16x32_bf16 v[188:191],a[156:159],a[252:255],v[188:191]
v_cvt_pk_bf16_f32 v74,v74,v75
v_mul_f32 v12,s61,v12
v_mfma_f32_16x16x32_bf16 v[188:191],a[152:155],a[248:251],v[188:191]
v_mul_f32 v13,s61,v13
v_cvt_pk_bf16_f32 v90,v90,v91
v_mfma_f32_16x16x32_bf16 v[192:195],a[160:163],a[224:227],v[192:195]
v_mul_f32 v14,s61,v14
v_mul_f32 v15,s61,v15
v_mfma_f32_16x16x32_bf16 v[192:195],a[164:167],a[228:231],v[192:195]
v_cvt_pk_bf16_f32 v106,v106,v107
v_cvt_pk_bf16_f32 v75,v16,v17
v_mfma_f32_16x16x32_bf16 v[200:203],a[164:167],a[236:239],v[200:203]
v_cvt_pk_bf16_f32 v91,v12,v13
v_cvt_pk_bf16_f32 v107,v14,v15
v_mfma_f32_16x16x32_bf16 v[200:203],a[160:163],a[232:235],v[200:203]
v_accvgpr_read_b32 v76,a24
v_accvgpr_read_b32 v77,a25
v_accvgpr_read_b32 v78,a26
v_accvgpr_read_b32 v79,a27
v_accvgpr_read_b32 v12,a28
v_accvgpr_read_b32 v13,a29
v_accvgpr_read_b32 v14,a30
v_accvgpr_read_b32 v15,a31
v_pk_mul_f32 v[76:77],s[60:61],v[76:77] op_sel:[1,0] op_sel_hi:[1,1]
v_pk_mul_f32 v[78:79],s[60:61],v[78:79] op_sel:[1,0] op_sel_hi:[1,1]
v_pk_mul_f32 v[12:13],s[60:61],v[12:13] op_sel:[1,0] op_sel_hi:[1,1]
v_pk_mul_f32 v[14:15],s[60:61],v[14:15] op_sel:[1,0] op_sel_hi:[1,1]
v_cvt_pk_bf16_f32 v76,v76,v77
v_cvt_pk_bf16_f32 v77,v78,v79
v_cvt_pk_bf16_f32 v78,v12,v13
v_cvt_pk_bf16_f32 v79,v14,v15
v_mfma_f32_16x16x32_bf16 v[208:211],a[160:163],a[240:243],v[208:211]
v_accvgpr_read_b32 v92,a56
v_accvgpr_read_b32 v93,a57
v_accvgpr_read_b32 v94,a58
v_accvgpr_read_b32 v95,a59
v_accvgpr_read_b32 v12,a60
v_accvgpr_read_b32 v13,a61
v_accvgpr_read_b32 v14,a62
v_accvgpr_read_b32 v15,a63
v_pk_mul_f32 v[92:93],s[60:61],v[92:93] op_sel:[1,0] op_sel_hi:[1,1]
v_pk_mul_f32 v[94:95],s[60:61],v[94:95] op_sel:[1,0] op_sel_hi:[1,1]
v_pk_mul_f32 v[12:13],s[60:61],v[12:13] op_sel:[1,0] op_sel_hi:[1,1]
v_pk_mul_f32 v[14:15],s[60:61],v[14:15] op_sel:[1,0] op_sel_hi:[1,1]
v_cvt_pk_bf16_f32 v92,v92,v93
v_cvt_pk_bf16_f32 v93,v94,v95
v_cvt_pk_bf16_f32 v94,v12,v13
v_cvt_pk_bf16_f32 v95,v14,v15
v_mfma_f32_16x16x32_bf16 v[208:211],a[164:167],a[244:247],v[208:211]
buffer_load_dwordx4 v4,s[24:27],s47 offen offset:2048 nt lds
v_mfma_f32_16x16x32_bf16 v[216:219],a[164:167],a[252:255],v[216:219]
ds_read_b128 a[128:131],v1
v_accvgpr_read_b32 v120,a112
v_mfma_f32_16x16x32_bf16 v[216:219],a[160:163],a[248:251],v[216:219]
v_accvgpr_read_b32 v121,a113
v_mul_f32 v120,s61,v120
v_mfma_f32_16x16x32_bf16 v[196:199],a[168:171],a[224:227],v[196:199]
v_mul_f32 v121,s61,v121
v_accvgpr_read_b32 v122,a114
v_mfma_f32_16x16x32_bf16 v[196:199],a[172:175],a[228:231],v[196:199]
ds_read_b128 a[132:135],v1 offset:256
v_accvgpr_read_b32 v123,a115
v_mfma_f32_16x16x32_bf16 v[204:207],a[172:175],a[236:239],v[204:207]
v_accvgpr_read_b32 v108,a88
v_accvgpr_read_b32 v109,a89
v_accvgpr_read_b32 v110,a90
v_accvgpr_read_b32 v111,a91
v_accvgpr_read_b32 v12,a92
v_accvgpr_read_b32 v13,a93
v_accvgpr_read_b32 v14,a94
v_accvgpr_read_b32 v15,a95
v_pk_mul_f32 v[108:109],s[60:61],v[108:109] op_sel:[1,0] op_sel_hi:[1,1]
v_pk_mul_f32 v[110:111],s[60:61],v[110:111] op_sel:[1,0] op_sel_hi:[1,1]
v_pk_mul_f32 v[12:13],s[60:61],v[12:13] op_sel:[1,0] op_sel_hi:[1,1]
v_pk_mul_f32 v[14:15],s[60:61],v[14:15] op_sel:[1,0] op_sel_hi:[1,1]
v_cvt_pk_bf16_f32 v108,v108,v109
v_cvt_pk_bf16_f32 v109,v110,v111
v_cvt_pk_bf16_f32 v110,v12,v13
v_cvt_pk_bf16_f32 v111,v14,v15
v_mfma_f32_16x16x32_bf16 v[204:207],a[168:171],a[232:235],v[204:207]
v_cvt_pk_bf16_f32 v120,v120,v121
v_mul_f32 v122,s61,v122
v_mfma_f32_16x16x32_bf16 v[212:215],a[168:171],a[240:243],v[212:215]
v_mul_f32 v123,s61,v123
v_cvt_pk_bf16_f32 v121,v122,v123
v_mfma_f32_16x16x32_bf16 v[212:215],a[172:175],a[244:247],v[212:215]
ds_read_b128 a[136:139],v1 offset:512
v_accvgpr_read_b32 v122,a116
v_mfma_f32_16x16x32_bf16 v[220:223],a[172:175],a[252:255],v[220:223]
v_accvgpr_read_b32 v123,a117
v_accvgpr_read_b32 v16,a118
v_mfma_f32_16x16x32_bf16 v[220:223],a[168:171],a[248:251],v[220:223]
v_accvgpr_read_b32 v17,a119
v_mul_f32 v122,s61,v122
v_mfma_f32_16x16x32_bf16 v[224:227],a[176:179],a[224:227],v[224:227]
)A"
R"A(ds_read_b128 a[140:143],v1 offset:768
v_mul_f32 v123,s61,v123
v_mfma_f32_16x16x32_bf16 v[224:227],a[180:183],a[228:231],v[224:227]
v_mul_f32 v16,s61,v16
v_mul_f32 v17,s61,v17
v_mfma_f32_16x16x32_bf16 v[232:235],a[180:183],a[236:239],v[232:235]
v_cvt_pk_bf16_f32 v122,v122,v123
v_cvt_pk_bf16_f32 v123,v16,v17
v_mfma_f32_16x16x32_bf16 v[232:235],a[176:179],a[232:235],v[232:235]
ds_read_b128 a[156:159],v1 offset:4864
v_mfma_f32_16x16x32_bf16 v[240:243],a[176:179],a[240:243],v[240:243]
v_accvgpr_read_b32 v124,a120
v_accvgpr_read_b32 v125,a121
v_accvgpr_read_b32 v126,a122
v_accvgpr_read_b32 v127,a123
v_accvgpr_read_b32 v12,a124
v_accvgpr_read_b32 v13,a125
v_accvgpr_read_b32 v14,a126
v_accvgpr_read_b32 v15,a127
v_pk_mul_f32 v[124:125],s[60:61],v[124:125] op_sel:[1,0] op_sel_hi:[1,1]
v_pk_mul_f32 v[126:127],s[60:61],v[126:127] op_sel:[1,0] op_sel_hi:[1,1]
v_pk_mul_f32 v[12:13],s[60:61],v[12:13] op_sel:[1,0] op_sel_hi:[1,1]
v_pk_mul_f32 v[14:15],s[60:61],v[14:15] op_sel:[1,0] op_sel_hi:[1,1]
v_cvt_pk_bf16_f32 v124,v124,v125
v_cvt_pk_bf16_f32 v125,v126,v127
v_cvt_pk_bf16_f32 v126,v12,v13
v_cvt_pk_bf16_f32 v127,v14,v15
v_mfma_f32_16x16x32_bf16 v[240:243],a[180:183],a[244:247],v[240:243]
buffer_load_dwordx4 v5,s[24:27],s47 offen offset:3072 nt lds
v_mfma_f32_16x16x32_bf16 v[248:251],a[180:183],a[252:255],v[248:251]
s_add_u32 s47,s47,16384
s_and_b32 s47,s47,0x3ffff
ds_read_b128 a[152:155],v1 offset:4608
v_mfma_f32_16x16x32_bf16 v[248:251],a[176:179],a[248:251],v[248:251]
s_or_b32 s47,s47,s46
v_mul_f32 v128,s61,v128
v_mul_f32 v129,s61,v129
v_mul_f32 v130,s61,v130
v_mul_f32 v131,s61,v131
v_cvt_pk_bf16_f32 v128,v128,v129
v_cvt_pk_bf16_f32 v129,v130,v131
v_pk_mul_f32 v[132:133],s[60:61],v[132:133] op_sel:[1,0] op_sel_hi:[1,1]
v_pk_mul_f32 v[134:135],s[60:61],v[134:135] op_sel:[1,0] op_sel_hi:[1,1]
v_pk_mul_f32 v[136:137],s[60:61],v[136:137] op_sel:[1,0] op_sel_hi:[1,1]
v_pk_mul_f32 v[138:139],s[60:61],v[138:139] op_sel:[1,0] op_sel_hi:[1,1]
v_pk_mul_f32 v[140:141],s[60:61],v[140:141] op_sel:[1,0] op_sel_hi:[1,1]
v_pk_mul_f32 v[142:143],s[60:61],v[142:143] op_sel:[1,0] op_sel_hi:[1,1]
v_pk_mul_f32 v[144:145],s[60:61],v[144:145] op_sel:[1,0] op_sel_hi:[1,1]
v_pk_mul_f32 v[146:147],s[60:61],v[146:147] op_sel:[1,0] op_sel_hi:[1,1]
v_pk_mul_f32 v[148:149],s[60:61],v[148:149] op_sel:[1,0] op_sel_hi:[1,1]
v_pk_mul_f32 v[150:151],s[60:61],v[150:151] op_sel:[1,0] op_sel_hi:[1,1]
v_pk_mul_f32 v[152:153],s[60:61],v[152:153] op_sel:[1,0] op_sel_hi:[1,1]
v_pk_mul_f32 v[154:155],s[60:61],v[154:155] op_sel:[1,0] op_sel_hi:[1,1]
v_pk_mul_f32 v[156:157],s[60:61],v[156:157] op_sel:[1,0] op_sel_hi:[1,1]
v_pk_mul_f32 v[158:159],s[60:61],v[158:159] op_sel:[1,0] op_sel_hi:[1,1]
v_mfma_f32_16x16x32_bf16 v[228:231],a[184:187],a[224:227],v[228:231]
s_and_b32 s46,s54,0xfffc0000
ds_read_b128 a[148:151],v1 offset:4352
v_cvt_pk_bf16_f32 v130,v132,v133
v_mfma_f32_16x16x32_bf16 v[228:231],a[188:191],a[228:231],v[228:231]
s_add_u32 s54,s54,16384
v_cvt_pk_bf16_f32 v131,v134,v135
v_cvt_pk_bf16_f32 v136,v136,v137
v_mfma_f32_16x16x32_bf16 v[236:239],a[188:191],a[236:239],v[236:239]
ds_read_b128 a[144:147],v1 offset:4096
v_cvt_pk_bf16_f32 v137,v138,v139
v_mfma_f32_16x16x32_bf16 v[236:239],a[184:187],a[232:235],v[236:239]
v_cvt_pk_bf16_f32 v138,v140,v141
v_cvt_pk_bf16_f32 v139,v142,v143
v_mfma_f32_16x16x32_bf16 v[244:247],a[184:187],a[240:243],v[244:247]
ds_read_b128 a[160:163],v1 offset:8192
v_cvt_pk_bf16_f32 v144,v144,v145
v_mfma_f32_16x16x32_bf16 v[244:247],a[188:191],a[244:247],v[244:247]
v_cvt_pk_bf16_f32 v145,v146,v147
v_cvt_pk_bf16_f32 v146,v148,v149
v_mfma_f32_16x16x32_bf16 v[252:255],a[188:191],a[252:255],v[252:255]
ds_read_b128 a[164:167],v1 offset:8448
v_cvt_pk_bf16_f32 v147,v150,v151
v_mfma_f32_16x16x32_bf16 v[252:255],a[184:187],a[248:251],v[252:255]
s_waitcnt lgkmcnt(4)
v_cvt_pk_bf16_f32 v152,v152,v153
v_cvt_pk_bf16_f32 v153,v154,v155
v_mfma_f32_16x16x32_bf16 v[32:35],v[48:51],v[64:67],0
ds_read_b128 a[168:171],v1 offset:8704
v_cvt_pk_bf16_f32 v154,v156,v157
v_mfma_f32_16x16x32_bf16 v[32:35],v[52:55],v[68:71],v[32:35]
ds_read_b64_tr_b16 a[192:193],v2
v_cvt_pk_bf16_f32 v155,v158,v159
v_mfma_f32_16x16x32_bf16 v[32:35],v[56:59],v[72:75],v[32:35]
v_mul_f32 v160,s61,v160
v_mul_f32 v161,s61,v161
v_mul_f32 v162,s61,v162
v_mul_f32 v163,s61,v163
v_cvt_pk_bf16_f32 v160,v160,v161
v_cvt_pk_bf16_f32 v161,v162,v163
v_pk_mul_f32 v[164:165],s[60:61],v[164:165] op_sel:[1,0] op_sel_hi:[1,1]
v_pk_mul_f32 v[166:167],s[60:61],v[166:167] op_sel:[1,0] op_sel_hi:[1,1]
v_pk_mul_f32 v[168:169],s[60:61],v[168:169] op_sel:[1,0] op_sel_hi:[1,1]
v_pk_mul_f32 v[170:171],s[60:61],v[170:171] op_sel:[1,0] op_sel_hi:[1,1]
v_pk_mul_f32 v[172:173],s[60:61],v[172:173] op_sel:[1,0] op_sel_hi:[1,1]
v_pk_mul_f32 v[174:175],s[60:61],v[174:175] op_sel:[1,0] op_sel_hi:[1,1]
v_pk_mul_f32 v[176:177],s[60:61],v[176:177] op_sel:[1,0] op_sel_hi:[1,1]
v_pk_mul_f32 v[178:179],s[60:61],v[178:179] op_sel:[1,0] op_sel_hi:[1,1]
v_pk_mul_f32 v[180:181],s[60:61],v[180:181] op_sel:[1,0] op_sel_hi:[1,1]
v_pk_mul_f32 v[182:183],s[60:61],v[182:183] op_sel:[1,0] op_sel_hi:[1,1]
v_pk_mul_f32 v[184:185],s[60:61],v[184:185] op_sel:[1,0] op_sel_hi:[1,1]
v_pk_mul_f32 v[186:187],s[60:61],v[186:187] op_sel:[1,0] op_sel_hi:[1,1]
v_pk_mul_f32 v[188:189],s[60:61],v[188:189] op_sel:[1,0] op_sel_hi:[1,1]
v_pk_mul_f32 v[190:191],s[60:61],v[190:191] op_sel:[1,0] op_sel_hi:[1,1]
v_mfma_f32_16x16x32_bf16 v[32:35],v[60:63],v[76:79],v[32:35]
ds_read_b128 a[172:175],v1 offset:8960
v_cvt_pk_bf16_f32 v162,v164,v165
v_mfma_f32_16x16x32_bf16 v[36:39],v[48:51],v[80:83],0
v_cvt_pk_bf16_f32 v163,v166,v167
v_cvt_pk_bf16_f32 v168,v168,v169
v_mfma_f32_16x16x32_bf16 v[36:39],v[52:55],v[84:87],v[36:39]
ds_read_b128 a[188:191],v1 offset:13056
v_cvt_pk_bf16_f32 v169,v170,v171
v_mfma_f32_16x16x32_bf16 v[36:39],v[56:59],v[88:91],v[36:39]
v_cvt_pk_bf16_f32 v170,v172,v173
v_cvt_pk_bf16_f32 v171,v174,v175
v_mfma_f32_16x16x32_bf16 v[36:39],v[60:63],v[92:95],v[36:39]
ds_read_b128 a[184:187],v1 offset:12800
v_cvt_pk_bf16_f32 v176,v176,v177
v_mfma_f32_16x16x32_bf16 v[40:43],v[48:51],v[96:99],0
v_cvt_pk_bf16_f32 v177,v178,v179
v_cvt_pk_bf16_f32 v178,v180,v181
v_mfma_f32_16x16x32_bf16 v[40:43],v[52:55],v[100:103],v[40:43]
s_add_u32 m0,32768,s51
v_max3_f32 v32,v32,v33,v34
v_max3_f32 v32,v32,v35,s18
v_mfma_f32_16x16x32_bf16 v[40:43],v[56:59],v[104:107],v[40:43]
v_sub_f32 v35,0,v32
v_sub_f32 v34,0,v32
v_mfma_f32_16x16x32_bf16 v[40:43],v[60:63],v[108:111],v[40:43]
v_sub_f32 v33,0,v32
v_sub_f32 v32,0,v32
v_mfma_f32_16x16x32_bf16 v[44:47],v[48:51],v[112:115],0
buffer_load_dwordx4 v4,s[20:23],s47 offen nt lds
v_mfma_f32_16x16x32_bf16 v[44:47],v[52:55],v[116:119],v[44:47]
ds_read_b128 a[180:183],v1 offset:12544
v_max3_f32 v36,v36,v37,v38
v_mfma_f32_16x16x32_bf16 v[44:47],v[56:59],v[120:123],v[44:47]
v_max3_f32 v36,v36,v39,s19
v_sub_f32 v39,0,v36
v_mfma_f32_16x16x32_bf16 v[44:47],v[60:63],v[124:127],v[44:47]
ds_read_b128 a[176:179],v1 offset:12288
v_sub_f32 v38,0,v36
v_mfma_f32_16x16x32_bf16 v[132:135],a[128:131],v[64:67],v[32:35]
v_sub_f32 v37,0,v36
v_sub_f32 v36,0,v36
v_mfma_f32_16x16x32_bf16 v[132:135],a[132:135],v[68:71],v[132:135]
v_cvt_pk_bf16_f32 v179,v182,v183
v_cvt_pk_bf16_f32 v184,v184,v185
v_mfma_f32_16x16x32_bf16 v[132:135],a[136:139],v[72:75],v[132:135]
v_cvt_pk_bf16_f32 v185,v186,v187
v_cvt_pk_bf16_f32 v186,v188,v189
v_mfma_f32_16x16x32_bf16 v[132:135],a[140:143],v[76:79],v[132:135]
v_cvt_pk_bf16_f32 v187,v190,v191
v_max3_f32 v40,v40,v41,v42
v_mfma_f32_16x16x32_bf16 v[140:143],a[156:159],v[76:79],v[32:35]
v_max3_f32 v40,v40,v43,s100
v_sub_f32 v43,0,v40
v_mfma_f32_16x16x32_bf16 v[140:143],a[152:155],v[72:75],v[140:143]
s_waitcnt lgkmcnt(1)
buffer_load_dwordx4 v4,s[20:23],s47 offen offset:1024 nt lds
v_mfma_f32_16x16x32_bf16 v[140:143],a[148:151],v[68:71],v[140:143]
v_sub_f32 v42,0,v40
v_sub_f32 v41,0,v40
v_mfma_f32_16x16x32_bf16 v[140:143],a[144:147],v[64:67],v[140:143]
ds_read_b64_tr_b16 a[194:195],v2 offset:4096
v_sub_f32 v40,0,v40
v_mfma_f32_16x16x32_bf16 v[148:151],a[160:163],v[64:67],v[32:35]
v_mul_f32 v192,s61,v192
v_mul_f32 v193,s61,v193
v_mul_f32 v194,s61,v194
v_mul_f32 v195,s61,v195
v_cvt_pk_bf16_f32 v192,v192,v193
v_cvt_pk_bf16_f32 v193,v194,v195
v_pk_mul_f32 v[196:197],s[60:61],v[196:197] op_sel:[1,0] op_sel_hi:[1,1]
v_pk_mul_f32 v[198:199],s[60:61],v[198:199] op_sel:[1,0] op_sel_hi:[1,1]
v_pk_mul_f32 v[200:201],s[60:61],v[200:201] op_sel:[1,0] op_sel_hi:[1,1]
v_pk_mul_f32 v[202:203],s[60:61],v[202:203] op_sel:[1,0] op_sel_hi:[1,1]
v_pk_mul_f32 v[204:205],s[60:61],v[204:205] op_sel:[1,0] op_sel_hi:[1,1]
v_pk_mul_f32 v[206:207],s[60:61],v[206:207] op_sel:[1,0] op_sel_hi:[1,1]
v_pk_mul_f32 v[208:209],s[60:61],v[208:209] op_sel:[1,0] op_sel_hi:[1,1]
v_pk_mul_f32 v[210:211],s[60:61],v[210:211] op_sel:[1,0] op_sel_hi:[1,1]
v_pk_mul_f32 v[212:213],s[60:61],v[212:213] op_sel:[1,0] op_sel_hi:[1,1]
v_pk_mul_f32 v[214:215],s[60:61],v[214:215] op_sel:[1,0] op_sel_hi:[1,1]
v_pk_mul_f32 v[216:217],s[60:61],v[216:217] op_sel:[1,0] op_sel_hi:[1,1]
v_pk_mul_f32 v[218:219],s[60:61],v[218:219] op_sel:[1,0] op_sel_hi:[1,1]
v_pk_mul_f32 v[220:221],s[60:61],v[220:221] op_sel:[1,0] op_sel_hi:[1,1]
v_pk_mul_f32 v[222:223],s[60:61],v[222:223] op_sel:[1,0] op_sel_hi:[1,1]
v_mfma_f32_16x16x32_bf16 v[148:151],a[164:167],v[68:71],v[148:151]
v_cvt_pk_bf16_f32 v194,v196,v197
v_cvt_pk_bf16_f32 v195,v198,v199
v_mfma_f32_16x16x32_bf16 v[148:151],a[168:171],v[72:75],v[148:151]
v_cvt_pk_bf16_f32 v200,v200,v201
v_cvt_pk_bf16_f32 v201,v202,v203
v_mfma_f32_16x16x32_bf16 v[148:151],a[172:175],v[76:79],v[148:151]
v_cvt_pk_bf16_f32 v202,v204,v205
v_cvt_pk_bf16_f32 v203,v206,v207
v_mfma_f32_16x16x32_bf16 v[156:159],a[188:191],v[76:79],v[32:35]
v_cvt_pk_bf16_f32 v208,v208,v209
v_cvt_pk_bf16_f32 v209,v210,v211
v_mfma_f32_16x16x32_bf16 v[156:159],a[184:187],v[72:75],v[156:159]
v_cvt_pk_bf16_f32 v210,v212,v213
v_cvt_pk_bf16_f32 v211,v214,v215
v_mfma_f32_16x16x32_bf16 v[156:159],a[180:183],v[68:71],v[156:159]
s_waitcnt lgkmcnt(1)
buffer_load_dwordx4 v5,s[20:23],s47 offen offset:2048 nt lds
v_mfma_f32_16x16x32_bf16 v[156:159],a[176:179],v[64:67],v[156:159]
ds_read_b64_tr_b16 a[196:197],v2 offset:8192
ds_read_b64_tr_b16 a[198:199],v2 offset:12288
v_mfma_f32_16x16x32_bf16 v[164:167],a[128:131],v[80:83],v[36:39]
ds_read_b64_tr_b16 a[204:205],v3 offset:8192
v_cvt_pk_bf16_f32 v216,v216,v217
v_mfma_f32_16x16x32_bf16 v[164:167],a[132:135],v[84:87],v[164:167]
ds_read_b64_tr_b16 a[206:207],v3 offset:12288
v_cvt_pk_bf16_f32 v217,v218,v219
v_mfma_f32_16x16x32_bf16 v[164:167],a[136:139],v[88:91],v[164:167]
ds_read_b64_tr_b16 a[200:201],v3
v_cvt_pk_bf16_f32 v218,v220,v221
v_mfma_f32_16x16x32_bf16 v[164:167],a[140:143],v[92:95],v[164:167]
ds_read_b64_tr_b16 a[202:203],v3 offset:4096
v_cvt_pk_bf16_f32 v219,v222,v223
v_mfma_f32_16x16x32_bf16 v[172:175],a[156:159],v[92:95],v[36:39]
ds_read_b64_tr_b16 a[208:209],v2 offset:256
ds_read_b64_tr_b16 a[210:211],v2 offset:4352
v_mfma_f32_16x16x32_bf16 v[172:175],a[152:155],v[88:91],v[172:175]
v_exp_f32 v132,v132
v_mfma_f32_16x16x32_bf16 v[172:175],a[148:151],v[84:87],v[172:175]
v_exp_f32 v133,v133
v_mfma_f32_16x16x32_bf16 v[172:175],a[144:147],v[80:83],v[172:175]
buffer_load_dwordx4 v5,s[20:23],s47 offen offset:3072 nt lds
v_mfma_f32_16x16x32_bf16 v[180:183],a[160:163],v[80:83],v[36:39]
v_exp_f32 v134,v134
v_mfma_f32_16x16x32_bf16 v[180:183],a[164:167],v[84:87],v[180:183]
v_exp_f32 v135,v135
v_mfma_f32_16x16x32_bf16 v[180:183],a[168:171],v[88:91],v[180:183]
ds_read_b64_tr_b16 a[212:213],v2 offset:8448
ds_read_b64_tr_b16 a[214:215],v2 offset:12544
v_mfma_f32_16x16x32_bf16 v[180:183],a[172:175],v[92:95],v[180:183]
v_cvt_pk_bf16_f32 v132,v132,v133
v_cvt_pk_bf16_f32 v133,v134,v135
v_mfma_f32_16x16x32_bf16 v[188:191],a[188:191],v[92:95],v[36:39]
v_exp_f32 v140,v140
v_mfma_f32_16x16x32_bf16 v[188:191],a[184:187],v[88:91],v[188:191]
s_add_u32 m0,32768,s52
v_exp_f32 v141,v141
v_mfma_f32_16x16x32_bf16 v[188:191],a[180:183],v[84:87],v[188:191]
v_exp_f32 v142,v142
v_mfma_f32_16x16x32_bf16 v[188:191],a[176:179],v[80:83],v[188:191]
v_exp_f32 v143,v143
)A"
R"A(v_mfma_f32_16x16x32_bf16 v[196:199],a[128:131],v[96:99],v[40:43]
buffer_load_dwordx4 v4,s[24:27],s47 offen nt lds
v_mfma_f32_16x16x32_bf16 v[196:199],a[132:135],v[100:103],v[196:199]
v_exp_f32 v148,v148
v_mfma_f32_16x16x32_bf16 v[196:199],a[136:139],v[104:107],v[196:199]
v_exp_f32 v149,v149
v_mfma_f32_16x16x32_bf16 v[196:199],a[140:143],v[108:111],v[196:199]
v_exp_f32 v150,v150
v_mfma_f32_16x16x32_bf16 v[204:207],a[156:159],v[108:111],v[40:43]
v_exp_f32 v151,v151
v_mfma_f32_16x16x32_bf16 v[204:207],a[152:155],v[104:107],v[204:207]
ds_read_b64_tr_b16 a[220:221],v3 offset:8448
ds_read_b64_tr_b16 a[222:223],v3 offset:12544
v_mfma_f32_16x16x32_bf16 v[204:207],a[148:151],v[100:103],v[204:207]
v_cvt_pk_bf16_f32 v134,v140,v141
v_cvt_pk_bf16_f32 v135,v142,v143
v_mfma_f32_16x16x32_bf16 v[204:207],a[144:147],v[96:99],v[204:207]
v_cvt_pk_bf16_f32 v148,v148,v149
v_cvt_pk_bf16_f32 v149,v150,v151
v_mfma_f32_16x16x32_bf16 v[212:215],a[160:163],v[96:99],v[40:43]
v_exp_f32 v156,v156
v_mfma_f32_16x16x32_bf16 v[212:215],a[164:167],v[100:103],v[212:215]
buffer_load_dwordx4 v5,s[24:27],s47 offen offset:1024 nt lds
v_mfma_f32_16x16x32_bf16 v[212:215],a[168:171],v[104:107],v[212:215]
v_exp_f32 v157,v157
v_mfma_f32_16x16x32_bf16 v[212:215],a[172:175],v[108:111],v[212:215]
v_exp_f32 v158,v158
v_mfma_f32_16x16x32_bf16 v[220:223],a[188:191],v[108:111],v[40:43]
v_exp_f32 v159,v159
v_mfma_f32_16x16x32_bf16 v[220:223],a[184:187],v[104:107],v[220:223]
ds_read_b64_tr_b16 a[216:217],v3 offset:256
ds_read_b64_tr_b16 a[218:219],v3 offset:4352
v_mfma_f32_16x16x32_bf16 v[220:223],a[180:183],v[100:103],v[220:223]
v_cvt_pk_bf16_f32 v150,v156,v157
v_cvt_pk_bf16_f32 v151,v158,v159
v_mfma_f32_16x16x32_bf16 v[220:223],a[176:179],v[96:99],v[220:223]
s_waitcnt lgkmcnt(2)
ds_read_b64_tr_b16 a[224:225],v2 offset:512
ds_read_b64_tr_b16 a[226:227],v2 offset:4608
v_mfma_f32_16x16x32_bf16 a[0:3],a[192:195],v[132:135],0
ds_read_b64_tr_b16 a[228:229],v2 offset:8704
ds_read_b64_tr_b16 a[230:231],v2 offset:12800
v_mfma_f32_16x16x32_bf16 a[0:3],a[196:199],v[148:151],a[0:3]
ds_read_b64_tr_b16 a[236:237],v3 offset:8704
ds_read_b64_tr_b16 a[238:239],v3 offset:12800
v_mfma_f32_16x16x32_bf16 a[4:7],a[204:207],v[148:151],0
ds_read_b64_tr_b16 a[232:233],v3 offset:512
ds_read_b64_tr_b16 a[234:235],v3 offset:4608
v_mfma_f32_16x16x32_bf16 a[4:7],a[200:203],v[132:135],a[4:7]
ds_read_b64_tr_b16 a[240:241],v2 offset:768
ds_read_b64_tr_b16 a[242:243],v2 offset:4864
v_mfma_f32_16x16x32_bf16 a[8:11],a[208:211],v[132:135],0
ds_read_b64_tr_b16 a[244:245],v2 offset:8960
ds_read_b64_tr_b16 a[246:247],v2 offset:13056
v_mfma_f32_16x16x32_bf16 a[8:11],a[212:215],v[148:151],a[8:11]
s_memrealtime s[76:77]
buffer_load_dwordx4 v4,s[24:27],s47 offen offset:2048 nt lds
ds_read_b64_tr_b16 a[252:253],v3 offset:8960
ds_read_b64_tr_b16 a[254:255],v3 offset:13056
v_mfma_f32_16x16x32_bf16 a[12:15],a[220:223],v[148:151],0
s_waitcnt lgkmcnt(14)
ds_read_b64_tr_b16 a[248:249],v3 offset:768
ds_read_b64_tr_b16 a[250:251],v3 offset:4864
v_mfma_f32_16x16x32_bf16 a[12:15],a[216:219],v[132:135],a[12:15]
s_waitcnt lgkmcnt(14)
v_mfma_f32_16x16x32_bf16 a[16:19],a[224:227],v[132:135],0
s_waitcnt lgkmcnt(12)
v_mfma_f32_16x16x32_bf16 a[16:19],a[228:231],v[148:151],a[16:19]
s_waitcnt lgkmcnt(10)
v_mfma_f32_16x16x32_bf16 a[20:23],a[236:239],v[148:151],0
s_waitcnt lgkmcnt(8)
v_mfma_f32_16x16x32_bf16 a[20:23],a[232:235],v[132:135],a[20:23]
s_waitcnt lgkmcnt(0)
v_max3_f32 v44,v44,v45,v46
v_max3_f32 v44,v44,v47,s101
v_mfma_f32_16x16x32_bf16 a[24:27],a[240:243],v[132:135],0
s_add_u32 s78,s76,s60
buffer_load_dwordx4 v5,s[24:27],s47 offen offset:3072 nt lds
v_mfma_f32_16x16x32_bf16 a[24:27],a[244:247],v[148:151],a[24:27]
s_add_u32 s47,s47,16384
v_sub_f32 v47,0,v44
v_sub_f32 v46,0,v44
v_mfma_f32_16x16x32_bf16 a[28:31],a[252:255],v[148:151],0
s_and_b32 s47,s47,0x3ffff
v_sub_f32 v45,0,v44
v_sub_f32 v44,0,v44
v_mfma_f32_16x16x32_bf16 a[28:31],a[248:251],v[132:135],a[28:31]
v_mul_f32 v224,s61,v224
v_mul_f32 v225,s61,v225
v_mul_f32 v226,s61,v226
v_mul_f32 v227,s61,v227
v_cvt_pk_bf16_f32 v224,v224,v225
v_cvt_pk_bf16_f32 v225,v226,v227
v_pk_mul_f32 v[228:229],s[60:61],v[228:229] op_sel:[1,0] op_sel_hi:[1,1]
v_pk_mul_f32 v[230:231],s[60:61],v[230:231] op_sel:[1,0] op_sel_hi:[1,1]
v_pk_mul_f32 v[232:233],s[60:61],v[232:233] op_sel:[1,0] op_sel_hi:[1,1]
v_pk_mul_f32 v[234:235],s[60:61],v[234:235] op_sel:[1,0] op_sel_hi:[1,1]
v_pk_mul_f32 v[236:237],s[60:61],v[236:237] op_sel:[1,0] op_sel_hi:[1,1]
v_pk_mul_f32 v[238:239],s[60:61],v[238:239] op_sel:[1,0] op_sel_hi:[1,1]
v_pk_mul_f32 v[240:241],s[60:61],v[240:241] op_sel:[1,0] op_sel_hi:[1,1]
v_pk_mul_f32 v[242:243],s[60:61],v[242:243] op_sel:[1,0] op_sel_hi:[1,1]
v_pk_mul_f32 v[244:245],s[60:61],v[244:245] op_sel:[1,0] op_sel_hi:[1,1]
v_pk_mul_f32 v[246:247],s[60:61],v[246:247] op_sel:[1,0] op_sel_hi:[1,1]
v_pk_mul_f32 v[248:249],s[60:61],v[248:249] op_sel:[1,0] op_sel_hi:[1,1]
v_pk_mul_f32 v[250:251],s[60:61],v[250:251] op_sel:[1,0] op_sel_hi:[1,1]
v_pk_mul_f32 v[252:253],s[60:61],v[252:253] op_sel:[1,0] op_sel_hi:[1,1]
v_pk_mul_f32 v[254:255],s[60:61],v[254:255] op_sel:[1,0] op_sel_hi:[1,1]
v_mfma_f32_16x16x32_bf16 v[48:51],v[28:31],v[132:135],0
s_or_b32 s47,s47,s46
v_cvt_pk_bf16_f32 v226,v228,v229
v_cvt_pk_bf16_f32 v227,v230,v231
v_mfma_f32_16x16x32_bf16 v[48:51],v[28:31],v[148:151],v[48:51]
s_and_b32 s46,s54,0xfffc0000
v_cvt_pk_bf16_f32 v232,v232,v233
v_cvt_pk_bf16_f32 v233,v234,v235
v_mfma_f32_16x16x32_bf16 v[228:231],a[128:131],v[112:115],v[44:47]
s_waitcnt vmcnt(8)
v_cvt_pk_bf16_f32 v234,v236,v237
v_cvt_pk_bf16_f32 v235,v238,v239
v_mfma_f32_16x16x32_bf16 v[228:231],a[132:135],v[116:119],v[228:231]
s_barrier
s_add_u32 m0,65536,s51
v_cvt_pk_bf16_f32 v240,v240,v241
v_mfma_f32_16x16x32_bf16 v[228:231],a[136:139],v[120:123],v[228:231]
v_cvt_pk_bf16_f32 v241,v242,v243
v_cvt_pk_bf16_f32 v242,v244,v245
v_mfma_f32_16x16x32_bf16 v[228:231],a[140:143],v[124:127],v[228:231]
v_cvt_pk_bf16_f32 v243,v246,v247
v_cvt_pk_bf16_f32 v248,v248,v249
v_mfma_f32_16x16x32_bf16 v[236:239],a[156:159],v[124:127],v[44:47]
buffer_load_dwordx4 v4,s[20:23],s47 offen nt lds
v_mfma_f32_16x16x32_bf16 v[236:239],a[152:155],v[120:123],v[236:239]
v_cvt_pk_bf16_f32 v249,v250,v251
v_cvt_pk_bf16_f32 v250,v252,v253
v_mfma_f32_16x16x32_bf16 v[236:239],a[148:151],v[116:119],v[236:239]
v_cvt_pk_bf16_f32 v251,v254,v255
v_add_u32 v1,s48,v1
v_mfma_f32_16x16x32_bf16 v[236:239],a[144:147],v[112:115],v[236:239]
v_exp_f32 v164,v164
v_mfma_f32_16x16x32_bf16 v[244:247],a[160:163],v[112:115],v[44:47]
v_exp_f32 v165,v165
v_mfma_f32_16x16x32_bf16 v[244:247],a[164:167],v[116:119],v[244:247]
v_exp_f32 v166,v166
v_mfma_f32_16x16x32_bf16 v[244:247],a[168:171],v[120:123],v[244:247]
v_exp_f32 v167,v167
v_mfma_f32_16x16x32_bf16 v[244:247],a[172:175],v[124:127],v[244:247]
v_cvt_pk_bf16_f32 v164,v164,v165
v_cvt_pk_bf16_f32 v165,v166,v167
v_mfma_f32_16x16x32_bf16 v[252:255],a[188:191],v[124:127],v[44:47]
v_exp_f32 v172,v172
v_exp_f32 v173,v173
v_exp_f32 v174,v174
v_exp_f32 v175,v175
v_exp_f32 v180,v180
v_exp_f32 v181,v181
v_mfma_f32_16x16x32_bf16 v[252:255],a[184:187],v[120:123],v[252:255]
v_exp_f32 v182,v182
v_exp_f32 v183,v183
v_cvt_pk_bf16_f32 v166,v172,v173
v_cvt_pk_bf16_f32 v167,v174,v175
v_cvt_pk_bf16_f32 v180,v180,v181
v_exp_f32 v188,v188
v_exp_f32 v189,v189
v_exp_f32 v190,v190
v_exp_f32 v191,v191
v_mfma_f32_16x16x32_bf16 v[252:255],a[180:183],v[116:119],v[252:255]
v_cvt_pk_bf16_f32 v181,v182,v183
v_cvt_pk_bf16_f32 v182,v188,v189
v_cvt_pk_bf16_f32 v183,v190,v191
v_mfma_f32_16x16x32_bf16 v[252:255],a[176:179],v[112:115],v[252:255]
ds_read_b128 a[128:131],v1
v_add_u32 v2,s48,v2
v_mfma_f32_16x16x32_bf16 a[32:35],a[192:195],v[164:167],0
v_exp_f32 v196,v196
v_mfma_f32_16x16x32_bf16 a[32:35],a[196:199],v[180:183],a[32:35]
ds_read_b128 a[132:135],v1 offset:256
v_add_u32 v3,s48,v3
v_mfma_f32_16x16x32_bf16 a[36:39],a[204:207],v[180:183],0
s_mov_b32 s48,32768
buffer_load_dwordx4 v4,s[20:23],s47 offen offset:1024 nt lds
v_mfma_f32_16x16x32_bf16 a[36:39],a[200:203],v[164:167],a[36:39]
ds_read_b128 a[136:139],v1 offset:512
v_mfma_f32_16x16x32_bf16 a[40:43],a[208:211],v[164:167],0
v_exp_f32 v197,v197
v_mfma_f32_16x16x32_bf16 a[40:43],a[212:215],v[180:183],a[40:43]
ds_read_b128 a[140:143],v1 offset:768
v_cvt_pk_bf16_f32 v196,v196,v197
v_mfma_f32_16x16x32_bf16 a[44:47],a[220:223],v[180:183],0
v_exp_f32 v198,v198
v_mfma_f32_16x16x32_bf16 a[44:47],a[216:219],v[164:167],a[44:47]
ds_read_b128 a[156:159],v1 offset:4864
v_mfma_f32_16x16x32_bf16 a[48:51],a[224:227],v[164:167],0
v_exp_f32 v199,v199
v_mfma_f32_16x16x32_bf16 a[48:51],a[228:231],v[180:183],a[48:51]
ds_read_b128 a[152:155],v1 offset:4608
v_cvt_pk_bf16_f32 v197,v198,v199
v_mfma_f32_16x16x32_bf16 a[52:55],a[236:239],v[180:183],0
v_exp_f32 v204,v204
v_mfma_f32_16x16x32_bf16 a[52:55],a[232:235],v[164:167],a[52:55]
ds_read_b128 a[148:151],v1 offset:4352
v_mfma_f32_16x16x32_bf16 a[56:59],a[240:243],v[164:167],0
v_exp_f32 v205,v205
v_mfma_f32_16x16x32_bf16 a[56:59],a[244:247],v[180:183],a[56:59]
ds_read_b128 a[144:147],v1 offset:4096
v_cvt_pk_bf16_f32 v198,v204,v205
v_mfma_f32_16x16x32_bf16 a[60:63],a[252:255],v[180:183],0
v_exp_f32 v206,v206
v_mfma_f32_16x16x32_bf16 a[60:63],a[248:251],v[164:167],a[60:63]
ds_read_b128 a[160:163],v1 offset:8192
v_mfma_f32_16x16x32_bf16 v[52:55],v[28:31],v[164:167],0
buffer_load_dwordx4 v5,s[20:23],s47 offen offset:2048 nt lds
v_mfma_f32_16x16x32_bf16 v[52:55],v[28:31],v[180:183],v[52:55]
s_waitcnt lgkmcnt(3)
ds_read_b128 a[164:167],v1 offset:8448
v_mfma_f32_16x16x32_bf16 v[132:135],a[128:131],v[64:67],v[32:35]
v_exp_f32 v207,v207
v_mfma_f32_16x16x32_bf16 v[132:135],a[132:135],v[68:71],v[132:135]
ds_read_b128 a[168:171],v1 offset:8704
v_cvt_pk_bf16_f32 v199,v206,v207
v_mfma_f32_16x16x32_bf16 v[132:135],a[136:139],v[72:75],v[132:135]
ds_read_b128 a[172:175],v1 offset:8960
v_mfma_f32_16x16x32_bf16 v[132:135],a[140:143],v[76:79],v[132:135]
ds_read_b128 a[188:191],v1 offset:13056
v_mfma_f32_16x16x32_bf16 v[140:143],a[156:159],v[76:79],v[32:35]
ds_read_b128 a[184:187],v1 offset:12800
v_mfma_f32_16x16x32_bf16 v[140:143],a[152:155],v[72:75],v[140:143]
s_waitcnt lgkmcnt(5)
ds_read_b128 a[180:183],v1 offset:12544
v_mfma_f32_16x16x32_bf16 v[140:143],a[148:151],v[68:71],v[140:143]
ds_read_b128 a[176:179],v1 offset:12288
v_mfma_f32_16x16x32_bf16 v[140:143],a[144:147],v[64:67],v[140:143]
v_mfma_f32_16x16x32_bf16 v[148:151],a[160:163],v[64:67],v[32:35]
s_waitcnt lgkmcnt(6)
v_mfma_f32_16x16x32_bf16 v[148:151],a[164:167],v[68:71],v[148:151]
s_waitcnt lgkmcnt(5)
v_exp_f32 v212,v212
v_mfma_f32_16x16x32_bf16 v[148:151],a[168:171],v[72:75],v[148:151]
s_waitcnt lgkmcnt(4)
v_exp_f32 v213,v213
v_mfma_f32_16x16x32_bf16 v[148:151],a[172:175],v[76:79],v[148:151]
s_waitcnt lgkmcnt(3)
v_exp_f32 v214,v214
v_mfma_f32_16x16x32_bf16 v[156:159],a[188:191],v[76:79],v[32:35]
s_waitcnt lgkmcnt(2)
v_exp_f32 v215,v215
v_mfma_f32_16x16x32_bf16 v[156:159],a[184:187],v[72:75],v[156:159]
s_waitcnt lgkmcnt(1)
v_cvt_pk_bf16_f32 v212,v212,v213
v_exp_f32 v220,v220
v_exp_f32 v221,v221
v_exp_f32 v222,v222
v_exp_f32 v223,v223
v_mfma_f32_16x16x32_bf16 v[156:159],a[180:183],v[68:71],v[156:159]
s_waitcnt lgkmcnt(0)
v_cvt_pk_bf16_f32 v213,v214,v215
v_cvt_pk_bf16_f32 v214,v220,v221
v_cvt_pk_bf16_f32 v215,v222,v223
v_mfma_f32_16x16x32_bf16 v[156:159],a[176:179],v[64:67],v[156:159]
buffer_load_dwordx4 v5,s[20:23],s47 offen offset:3072 nt lds
v_mfma_f32_16x16x32_bf16 a[64:67],a[192:195],v[196:199],0
v_exp_f32 v228,v228
v_mfma_f32_16x16x32_bf16 a[64:67],a[196:199],v[212:215],a[64:67]
v_exp_f32 v229,v229
v_mfma_f32_16x16x32_bf16 a[68:71],a[204:207],v[212:215],0
v_exp_f32 v230,v230
v_mfma_f32_16x16x32_bf16 a[68:71],a[200:203],v[196:199],a[68:71]
v_exp_f32 v231,v231
v_mfma_f32_16x16x32_bf16 a[72:75],a[208:211],v[196:199],0
v_cvt_pk_bf16_f32 v228,v228,v229
v_cvt_pk_bf16_f32 v229,v230,v231
v_mfma_f32_16x16x32_bf16 a[72:75],a[212:215],v[212:215],a[72:75]
v_exp_f32 v236,v236
v_mfma_f32_16x16x32_bf16 a[76:79],a[220:223],v[212:215],0
v_exp_f32 v237,v237
v_mfma_f32_16x16x32_bf16 a[76:79],a[216:219],v[196:199],a[76:79]
)A"
R"A(s_add_u32 m0,65536,s52
v_exp_f32 v238,v238
v_mfma_f32_16x16x32_bf16 a[80:83],a[224:227],v[196:199],0
v_exp_f32 v239,v239
v_mfma_f32_16x16x32_bf16 a[80:83],a[228:231],v[212:215],a[80:83]
v_exp_f32 v244,v244
v_mfma_f32_16x16x32_bf16 a[84:87],a[236:239],v[212:215],0
buffer_load_dwordx4 v4,s[24:27],s47 offen nt lds
v_mfma_f32_16x16x32_bf16 a[84:87],a[232:235],v[196:199],a[84:87]
v_exp_f32 v245,v245
v_mfma_f32_16x16x32_bf16 a[88:91],a[240:243],v[196:199],0
v_exp_f32 v246,v246
v_mfma_f32_16x16x32_bf16 a[88:91],a[244:247],v[212:215],a[88:91]
v_exp_f32 v247,v247
v_mfma_f32_16x16x32_bf16 a[92:95],a[252:255],v[212:215],0
v_cvt_pk_bf16_f32 v230,v236,v237
v_cvt_pk_bf16_f32 v231,v238,v239
v_mfma_f32_16x16x32_bf16 a[92:95],a[248:251],v[196:199],a[92:95]
v_cvt_pk_bf16_f32 v244,v244,v245
v_cvt_pk_bf16_f32 v245,v246,v247
v_mfma_f32_16x16x32_bf16 v[56:59],v[28:31],v[196:199],0
v_exp_f32 v252,v252
v_mfma_f32_16x16x32_bf16 v[56:59],v[28:31],v[212:215],v[56:59]
v_exp_f32 v253,v253
v_mfma_f32_16x16x32_bf16 v[164:167],a[128:131],v[80:83],v[36:39]
v_exp_f32 v254,v254
v_mfma_f32_16x16x32_bf16 v[164:167],a[132:135],v[84:87],v[164:167]
v_exp_f32 v255,v255
v_mfma_f32_16x16x32_bf16 v[164:167],a[136:139],v[88:91],v[164:167]
v_cvt_pk_bf16_f32 v246,v252,v253
v_cvt_pk_bf16_f32 v247,v254,v255
v_mfma_f32_16x16x32_bf16 v[164:167],a[140:143],v[92:95],v[164:167]
v_exp_f32 v132,v132
v_mfma_f32_16x16x32_bf16 v[172:175],a[156:159],v[92:95],v[36:39]
v_exp_f32 v133,v133
v_mfma_f32_16x16x32_bf16 v[172:175],a[152:155],v[88:91],v[172:175]
v_exp_f32 v134,v134
v_mfma_f32_16x16x32_bf16 v[172:175],a[148:151],v[84:87],v[172:175]
buffer_load_dwordx4 v5,s[24:27],s47 offen offset:1024 nt lds
v_mfma_f32_16x16x32_bf16 v[172:175],a[144:147],v[80:83],v[172:175]
v_exp_f32 v135,v135
v_mfma_f32_16x16x32_bf16 v[180:183],a[160:163],v[80:83],v[36:39]
v_cvt_pk_bf16_f32 v132,v132,v133
v_cvt_pk_bf16_f32 v133,v134,v135
v_mfma_f32_16x16x32_bf16 v[180:183],a[164:167],v[84:87],v[180:183]
v_exp_f32 v140,v140
v_mfma_f32_16x16x32_bf16 v[180:183],a[168:171],v[88:91],v[180:183]
v_exp_f32 v141,v141
v_mfma_f32_16x16x32_bf16 v[180:183],a[172:175],v[92:95],v[180:183]
v_exp_f32 v142,v142
v_mfma_f32_16x16x32_bf16 v[188:191],a[188:191],v[92:95],v[36:39]
v_exp_f32 v143,v143
v_mfma_f32_16x16x32_bf16 v[188:191],a[184:187],v[88:91],v[188:191]
v_exp_f32 v148,v148
v_mfma_f32_16x16x32_bf16 v[188:191],a[180:183],v[84:87],v[188:191]
v_exp_f32 v149,v149
v_mfma_f32_16x16x32_bf16 v[188:191],a[176:179],v[80:83],v[188:191]
v_exp_f32 v150,v150
v_mfma_f32_16x16x32_bf16 a[96:99],a[192:195],v[228:231],0
ds_read_b64_tr_b16 a[192:193],v2
ds_read_b64_tr_b16 a[194:195],v2 offset:4096
v_mfma_f32_16x16x32_bf16 a[96:99],a[196:199],v[244:247],a[96:99]
ds_read_b64_tr_b16 a[196:197],v2 offset:8192
v_cvt_pk_bf16_f32 v134,v140,v141
v_mfma_f32_16x16x32_bf16 a[100:103],a[204:207],v[244:247],0
ds_read_b64_tr_b16 a[198:199],v2 offset:12288
v_cvt_pk_bf16_f32 v135,v142,v143
v_mfma_f32_16x16x32_bf16 a[100:103],a[200:203],v[228:231],a[100:103]
ds_read_b64_tr_b16 a[204:205],v3 offset:8192
v_cvt_pk_bf16_f32 v148,v148,v149
v_mfma_f32_16x16x32_bf16 a[104:107],a[208:211],v[228:231],0
buffer_load_dwordx4 v4,s[24:27],s47 offen offset:2048 nt lds
v_mfma_f32_16x16x32_bf16 a[104:107],a[212:215],v[244:247],a[104:107]
ds_read_b64_tr_b16 a[206:207],v3 offset:12288
ds_read_b64_tr_b16 a[200:201],v3
v_mfma_f32_16x16x32_bf16 a[108:111],a[220:223],v[244:247],0
ds_read_b64_tr_b16 a[202:203],v3 offset:4096
v_add_u32 v1,s48,v1
v_mfma_f32_16x16x32_bf16 a[108:111],a[216:219],v[228:231],a[108:111]
ds_read_b64_tr_b16 a[208:209],v2 offset:256
v_mfma_f32_16x16x32_bf16 a[112:115],a[224:227],v[228:231],0
ds_read_b64_tr_b16 a[210:211],v2 offset:4352
v_mfma_f32_16x16x32_bf16 a[112:115],a[228:231],v[244:247],a[112:115]
ds_read_b64_tr_b16 a[212:213],v2 offset:8448
ds_read_b64_tr_b16 a[214:215],v2 offset:12544
v_mfma_f32_16x16x32_bf16 a[116:119],a[236:239],v[244:247],0
v_exp_f32 v151,v151
v_mfma_f32_16x16x32_bf16 a[116:119],a[232:235],v[228:231],a[116:119]
v_exp_f32 v156,v156
v_mfma_f32_16x16x32_bf16 a[120:123],a[240:243],v[228:231],0
v_exp_f32 v157,v157
v_mfma_f32_16x16x32_bf16 a[120:123],a[244:247],v[244:247],a[120:123]
v_exp_f32 v158,v158
v_mfma_f32_16x16x32_bf16 a[124:127],a[252:255],v[244:247],0
v_exp_f32 v159,v159
v_mfma_f32_16x16x32_bf16 a[124:127],a[248:251],v[228:231],a[124:127]
ds_read_b64_tr_b16 a[220:221],v3 offset:8448
ds_read_b64_tr_b16 a[222:223],v3 offset:12544
v_mfma_f32_16x16x32_bf16 v[60:63],v[28:31],v[228:231],0
v_cvt_pk_bf16_f32 v149,v150,v151
v_cvt_pk_bf16_f32 v150,v156,v157
v_mfma_f32_16x16x32_bf16 v[60:63],v[28:31],v[244:247],v[60:63]
ds_read_b64_tr_b16 a[216:217],v3 offset:256
v_cvt_pk_bf16_f32 v151,v158,v159
v_mfma_f32_16x16x32_bf16 v[196:199],a[128:131],v[96:99],v[40:43]
buffer_load_dwordx4 v5,s[24:27],s47 offen offset:3072 nt lds
v_mfma_f32_16x16x32_bf16 v[196:199],a[132:135],v[100:103],v[196:199]
s_add_u32 s47,s47,16384
ds_read_b64_tr_b16 a[218:219],v3 offset:4352
ds_read_b64_tr_b16 a[224:225],v2 offset:512
v_mfma_f32_16x16x32_bf16 v[196:199],a[136:139],v[104:107],v[196:199]
s_and_b32 s47,s47,0x3ffff
s_or_b32 s47,s47,s46
ds_read_b64_tr_b16 a[226:227],v2 offset:4608
v_mfma_f32_16x16x32_bf16 v[196:199],a[140:143],v[108:111],v[196:199]
ds_read_b64_tr_b16 a[228:229],v2 offset:8704
v_mfma_f32_16x16x32_bf16 v[204:207],a[156:159],v[108:111],v[40:43]
ds_read_b64_tr_b16 a[230:231],v2 offset:12800
v_mfma_f32_16x16x32_bf16 v[204:207],a[152:155],v[104:107],v[204:207]
ds_read_b64_tr_b16 a[236:237],v3 offset:8704
ds_read_b64_tr_b16 a[238:239],v3 offset:12800
v_mfma_f32_16x16x32_bf16 v[204:207],a[148:151],v[100:103],v[204:207]
ds_read_b64_tr_b16 a[232:233],v3 offset:512
v_mfma_f32_16x16x32_bf16 v[204:207],a[144:147],v[96:99],v[204:207]
ds_read_b64_tr_b16 a[234:235],v3 offset:4608
v_mfma_f32_16x16x32_bf16 v[212:215],a[160:163],v[96:99],v[40:43]
ds_read_b64_tr_b16 a[240:241],v2 offset:768
v_mfma_f32_16x16x32_bf16 v[212:215],a[164:167],v[100:103],v[212:215]
ds_read_b64_tr_b16 a[242:243],v2 offset:4864
ds_read_b64_tr_b16 a[244:245],v2 offset:8960
v_mfma_f32_16x16x32_bf16 v[212:215],a[168:171],v[104:107],v[212:215]
ds_read_b64_tr_b16 a[246:247],v2 offset:13056
v_mfma_f32_16x16x32_bf16 v[212:215],a[172:175],v[108:111],v[212:215]
ds_read_b64_tr_b16 a[252:253],v3 offset:8960
v_mfma_f32_16x16x32_bf16 v[220:223],a[188:191],v[108:111],v[40:43]
ds_read_b64_tr_b16 a[254:255],v3 offset:13056
v_mfma_f32_16x16x32_bf16 v[220:223],a[184:187],v[104:107],v[220:223]
ds_read_b64_tr_b16 a[248:249],v3 offset:768
ds_read_b64_tr_b16 a[250:251],v3 offset:4864
v_mfma_f32_16x16x32_bf16 v[220:223],a[180:183],v[100:103],v[220:223]
v_exp_f32 v164,v164
v_mfma_f32_16x16x32_bf16 v[220:223],a[176:179],v[96:99],v[220:223]
s_waitcnt lgkmcnt(8)
v_exp_f32 v165,v165
v_mfma_f32_16x16x32_bf16 a[0:3],a[192:195],v[132:135],a[0:3]
v_exp_f32 v180,v180
v_mfma_f32_16x16x32_bf16 a[0:3],a[196:199],v[148:151],a[0:3]
v_exp_f32 v181,v181
v_mfma_f32_16x16x32_bf16 a[4:7],a[204:207],v[148:151],a[4:7]
v_exp_f32 v166,v166
v_mfma_f32_16x16x32_bf16 a[4:7],a[200:203],v[132:135],a[4:7]
v_exp_f32 v167,v167
v_mfma_f32_16x16x32_bf16 a[8:11],a[208:211],v[132:135],a[8:11]
s_memrealtime s[76:77]
v_exp_f32 v182,v182
v_mfma_f32_16x16x32_bf16 a[8:11],a[212:215],v[148:151],a[8:11]
v_exp_f32 v183,v183
v_mfma_f32_16x16x32_bf16 a[12:15],a[220:223],v[148:151],a[12:15]
v_cvt_pk_bf16_f32 v164,v164,v165
v_cvt_pk_bf16_f32 v180,v180,v181
v_mfma_f32_16x16x32_bf16 a[12:15],a[216:219],v[132:135],a[12:15]
v_exp_f32 v172,v172
v_mfma_f32_16x16x32_bf16 a[16:19],a[224:227],v[132:135],a[16:19]
v_exp_f32 v173,v173
v_mfma_f32_16x16x32_bf16 a[16:19],a[228:231],v[148:151],a[16:19]
v_exp_f32 v174,v174
v_mfma_f32_16x16x32_bf16 a[20:23],a[236:239],v[148:151],a[20:23]
v_exp_f32 v175,v175
v_mfma_f32_16x16x32_bf16 a[20:23],a[232:235],v[132:135],a[20:23]
s_waitcnt lgkmcnt(0)
v_exp_f32 v188,v188
v_mfma_f32_16x16x32_bf16 a[24:27],a[240:243],v[132:135],a[24:27]
s_sub_u32 s79,s78,s76
v_exp_f32 v189,v189
v_mfma_f32_16x16x32_bf16 a[24:27],a[244:247],v[148:151],a[24:27]
s_add_u32 s78,s76,s60
v_exp_f32 v190,v190
v_mfma_f32_16x16x32_bf16 a[28:31],a[252:255],v[148:151],a[28:31]
s_cmp_gt_i32 s79,0
s_cbranch_scc1 L_ps2_%=
L_pr2_%=:
v_cvt_pk_bf16_f32 v165,v166,v167
v_mfma_f32_16x16x32_bf16 a[28:31],a[248:251],v[132:135],a[28:31]
v_exp_f32 v191,v191
v_mfma_f32_16x16x32_bf16 v[48:51],v[28:31],v[132:135],v[48:51]
v_cvt_pk_bf16_f32 v181,v182,v183
v_cvt_pk_bf16_f32 v166,v172,v173
v_cvt_pk_bf16_f32 v167,v174,v175
v_cvt_pk_bf16_f32 v182,v188,v189
v_cvt_pk_bf16_f32 v183,v190,v191
v_mfma_f32_16x16x32_bf16 v[48:51],v[28:31],v[148:151],v[48:51]
s_mov_b32 s53,13
s_cmp_eq_u32 s40,3
s_cbranch_scc0 L_att0_%=
s_cmp_gt_i32 s18,0
s_cbranch_scc1 L_att0v0_%=
s_cmp_gt_i32 s19,0
s_cbranch_scc1 L_att0v1_%=
s_cmp_gt_i32 s100,0
s_cbranch_scc1 L_att0v2_%=
.p2align 5
L_att0v3_%=:
s_waitcnt vmcnt(8) lgkmcnt(0)
v_exp_f32 v196,v196
s_barrier
s_add_u32 m0,s49,s51
ds_read_b128 a[128:131],v1
s_mov_b32 s90,L_endseg_%=-L_pc_%=+128
v_exp_f32 v197,v197
s_lshr_b32 s45,s45,1
ds_read_b128 a[132:135],v1 offset:256
v_cvt_pk_bf16_f32 v196,v196,v197
s_add_u32 s62,s50,32768
buffer_load_dwordx4 v4,s[20:23],s47 offen nt lds
s_and_b32 s62,s62,131071
ds_read_b128 a[136:139],v1 offset:512
v_add_u32 v2,s48,v2
s_add_u32 s54,s54,16384
v_exp_f32 v198,v198
s_sub_u32 s53,s53,1
ds_read_b128 a[140:143],v1 offset:768
v_add_u32 v3,s48,v3
s_sub_u32 s48,s62,s50
v_exp_f32 v199,v199
s_and_b32 s46,s54,0xfffc0000
ds_read_b128 a[156:159],v1 offset:4864
v_cvt_pk_bf16_f32 v197,v198,v199
s_mov_b32 s50,s62
v_exp_f32 v204,v204
ds_read_b128 a[152:155],v1 offset:4608
v_exp_f32 v205,v205
ds_read_b128 a[148:151],v1 offset:4352
v_cvt_pk_bf16_f32 v198,v204,v205
v_exp_f32 v206,v206
ds_read_b128 a[144:147],v1 offset:4096
v_mfma_f32_16x16x32_bf16 a[32:35],a[192:195],v[164:167],a[32:35]
v_exp_f32 v207,v207
v_mfma_f32_16x16x32_bf16 a[32:35],a[196:199],v[180:183],a[32:35]
ds_read_b128 a[160:163],v1 offset:8192
v_cvt_pk_bf16_f32 v199,v206,v207
v_mfma_f32_16x16x32_bf16 a[36:39],a[204:207],v[180:183],a[36:39]
buffer_load_dwordx4 v4,s[20:23],s47 offen offset:1024 nt lds
v_mfma_f32_16x16x32_bf16 a[36:39],a[200:203],v[164:167],a[36:39]
ds_read_b128 a[164:167],v1 offset:8448
v_mfma_f32_16x16x32_bf16 a[40:43],a[208:211],v[164:167],a[40:43]
v_exp_f32 v212,v212
v_mfma_f32_16x16x32_bf16 a[40:43],a[212:215],v[180:183],a[40:43]
ds_read_b128 a[168:171],v1 offset:8704
v_mfma_f32_16x16x32_bf16 a[44:47],a[220:223],v[180:183],a[44:47]
v_exp_f32 v213,v213
v_mfma_f32_16x16x32_bf16 a[44:47],a[216:219],v[164:167],a[44:47]
ds_read_b128 a[172:175],v1 offset:8960
v_cvt_pk_bf16_f32 v212,v212,v213
v_mfma_f32_16x16x32_bf16 a[48:51],a[224:227],v[164:167],a[48:51]
v_exp_f32 v214,v214
v_mfma_f32_16x16x32_bf16 a[48:51],a[228:231],v[180:183],a[48:51]
ds_read_b128 a[188:191],v1 offset:13056
v_mfma_f32_16x16x32_bf16 a[52:55],a[236:239],v[180:183],a[52:55]
v_exp_f32 v215,v215
v_mfma_f32_16x16x32_bf16 a[52:55],a[232:235],v[164:167],a[52:55]
ds_read_b128 a[184:187],v1 offset:12800
v_cvt_pk_bf16_f32 v213,v214,v215
v_mfma_f32_16x16x32_bf16 a[56:59],a[240:243],v[164:167],a[56:59]
v_exp_f32 v220,v220
v_mfma_f32_16x16x32_bf16 a[56:59],a[244:247],v[180:183],a[56:59]
ds_read_b128 a[180:183],v1 offset:12544
v_mfma_f32_16x16x32_bf16 a[60:63],a[252:255],v[180:183],a[60:63]
v_exp_f32 v221,v221
v_mfma_f32_16x16x32_bf16 a[60:63],a[248:251],v[164:167],a[60:63]
ds_read_b128 a[176:179],v1 offset:12288
v_cvt_pk_bf16_f32 v214,v220,v221
v_mfma_f32_16x16x32_bf16 v[52:55],v[28:31],v[164:167],v[52:55]
buffer_load_dwordx4 v5,s[20:23],s47 offen offset:2048 nt lds
v_mfma_f32_16x16x32_bf16 v[52:55],v[28:31],v[180:183],v[52:55]
s_waitcnt lgkmcnt(3)
v_exp_f32 v222,v222
v_mfma_f32_16x16x32_bf16 v[132:135],a[128:131],v[64:67],v[32:35]
v_exp_f32 v223,v223
v_mfma_f32_16x16x32_bf16 v[132:135],a[132:135],v[68:71],v[132:135]
v_cvt_pk_bf16_f32 v215,v222,v223
v_add_u32 v1,s48,v1
v_mfma_f32_16x16x32_bf16 v[132:135],a[136:139],v[72:75],v[132:135]
v_mfma_f32_16x16x32_bf16 v[132:135],a[140:143],v[76:79],v[132:135]
v_mfma_f32_16x16x32_bf16 v[140:143],a[156:159],v[76:79],v[32:35]
v_mfma_f32_16x16x32_bf16 v[140:143],a[152:155],v[72:75],v[140:143]
)A"
R"A(v_mfma_f32_16x16x32_bf16 v[140:143],a[148:151],v[68:71],v[140:143]
v_mfma_f32_16x16x32_bf16 v[140:143],a[144:147],v[64:67],v[140:143]
v_mfma_f32_16x16x32_bf16 v[148:151],a[160:163],v[64:67],v[32:35]
v_mfma_f32_16x16x32_bf16 v[148:151],a[164:167],v[68:71],v[148:151]
v_mfma_f32_16x16x32_bf16 v[148:151],a[168:171],v[72:75],v[148:151]
v_mfma_f32_16x16x32_bf16 v[148:151],a[172:175],v[76:79],v[148:151]
v_mfma_f32_16x16x32_bf16 v[156:159],a[188:191],v[76:79],v[32:35]
s_waitcnt lgkmcnt(0)
buffer_load_dwordx4 v5,s[20:23],s47 offen offset:3072 nt lds
v_mfma_f32_16x16x32_bf16 v[156:159],a[184:187],v[72:75],v[156:159]
v_mfma_f32_16x16x32_bf16 v[156:159],a[180:183],v[68:71],v[156:159]
v_mfma_f32_16x16x32_bf16 v[156:159],a[176:179],v[64:67],v[156:159]
v_mfma_f32_16x16x32_bf16 a[64:67],a[192:195],v[196:199],a[64:67]
v_mfma_f32_16x16x32_bf16 a[64:67],a[196:199],v[212:215],a[64:67]
v_mfma_f32_16x16x32_bf16 a[68:71],a[204:207],v[212:215],a[68:71]
v_mfma_f32_16x16x32_bf16 a[68:71],a[200:203],v[196:199],a[68:71]
v_mfma_f32_16x16x32_bf16 a[72:75],a[208:211],v[196:199],a[72:75]
v_mfma_f32_16x16x32_bf16 a[72:75],a[212:215],v[212:215],a[72:75]
v_mfma_f32_16x16x32_bf16 a[76:79],a[220:223],v[212:215],a[76:79]
v_mfma_f32_16x16x32_bf16 a[76:79],a[216:219],v[196:199],a[76:79]
s_add_u32 m0,s49,s52
v_exp_f32 v132,v132
v_mfma_f32_16x16x32_bf16 a[80:83],a[224:227],v[196:199],a[80:83]
s_xor_b32 s49,s50,65536
v_exp_f32 v133,v133
v_mfma_f32_16x16x32_bf16 a[80:83],a[228:231],v[212:215],a[80:83]
v_exp_f32 v134,v134
v_mfma_f32_16x16x32_bf16 a[84:87],a[236:239],v[212:215],a[84:87]
buffer_load_dwordx4 v4,s[24:27],s47 offen nt lds
v_mfma_f32_16x16x32_bf16 a[84:87],a[232:235],v[196:199],a[84:87]
v_exp_f32 v135,v135
v_mfma_f32_16x16x32_bf16 a[88:91],a[240:243],v[196:199],a[88:91]
v_cvt_pk_bf16_f32 v132,v132,v133
v_cvt_pk_bf16_f32 v133,v134,v135
v_mfma_f32_16x16x32_bf16 a[88:91],a[244:247],v[212:215],a[88:91]
v_exp_f32 v140,v140
v_mfma_f32_16x16x32_bf16 a[92:95],a[252:255],v[212:215],a[92:95]
v_exp_f32 v141,v141
v_mfma_f32_16x16x32_bf16 a[92:95],a[248:251],v[196:199],a[92:95]
v_exp_f32 v142,v142
v_mfma_f32_16x16x32_bf16 v[56:59],v[28:31],v[196:199],v[56:59]
v_exp_f32 v143,v143
v_mfma_f32_16x16x32_bf16 v[56:59],v[28:31],v[212:215],v[56:59]
v_exp_f32 v148,v148
v_mfma_f32_16x16x32_bf16 v[164:167],a[128:131],v[80:83],v[36:39]
v_exp_f32 v149,v149
v_mfma_f32_16x16x32_bf16 v[164:167],a[132:135],v[84:87],v[164:167]
v_exp_f32 v150,v150
v_mfma_f32_16x16x32_bf16 v[164:167],a[136:139],v[88:91],v[164:167]
v_exp_f32 v151,v151
v_mfma_f32_16x16x32_bf16 v[164:167],a[140:143],v[92:95],v[164:167]
v_cvt_pk_bf16_f32 v134,v140,v141
v_cvt_pk_bf16_f32 v135,v142,v143
v_mfma_f32_16x16x32_bf16 v[172:175],a[156:159],v[92:95],v[36:39]
v_cvt_pk_bf16_f32 v148,v148,v149
v_cvt_pk_bf16_f32 v149,v150,v151
v_mfma_f32_16x16x32_bf16 v[172:175],a[152:155],v[88:91],v[172:175]
v_exp_f32 v156,v156
v_mfma_f32_16x16x32_bf16 v[172:175],a[148:151],v[84:87],v[172:175]
buffer_load_dwordx4 v5,s[24:27],s47 offen offset:1024 nt lds
v_mfma_f32_16x16x32_bf16 v[172:175],a[144:147],v[80:83],v[172:175]
v_exp_f32 v157,v157
v_mfma_f32_16x16x32_bf16 v[180:183],a[160:163],v[80:83],v[36:39]
v_exp_f32 v158,v158
v_mfma_f32_16x16x32_bf16 v[180:183],a[164:167],v[84:87],v[180:183]
v_exp_f32 v159,v159
v_mfma_f32_16x16x32_bf16 v[180:183],a[168:171],v[88:91],v[180:183]
v_cvt_pk_bf16_f32 v150,v156,v157
v_cvt_pk_bf16_f32 v151,v158,v159
v_mfma_f32_16x16x32_bf16 v[180:183],a[172:175],v[92:95],v[180:183]
v_exp_f32 v164,v164
v_mfma_f32_16x16x32_bf16 v[188:191],a[188:191],v[92:95],v[36:39]
v_exp_f32 v165,v165
v_mfma_f32_16x16x32_bf16 v[188:191],a[184:187],v[88:91],v[188:191]
v_exp_f32 v166,v166
v_mfma_f32_16x16x32_bf16 v[188:191],a[180:183],v[84:87],v[188:191]
v_exp_f32 v167,v167
v_mfma_f32_16x16x32_bf16 v[188:191],a[176:179],v[80:83],v[188:191]
v_cvt_pk_bf16_f32 v164,v164,v165
v_cvt_pk_bf16_f32 v165,v166,v167
ds_read_b64_tr_b16 a[192:193],v2
ds_read_b64_tr_b16 a[194:195],v2 offset:4096
ds_read_b64_tr_b16 a[196:197],v2 offset:8192
ds_read_b64_tr_b16 a[198:199],v2 offset:12288
buffer_load_dwordx4 v4,s[24:27],s47 offen offset:2048 nt lds
ds_read_b64_tr_b16 a[204:205],v3 offset:8192
ds_read_b64_tr_b16 a[206:207],v3 offset:12288
ds_read_b64_tr_b16 a[200:201],v3
ds_read_b64_tr_b16 a[202:203],v3 offset:4096
ds_read_b64_tr_b16 a[208:209],v2 offset:256
ds_read_b64_tr_b16 a[210:211],v2 offset:4352
ds_read_b64_tr_b16 a[212:213],v2 offset:8448
ds_read_b64_tr_b16 a[214:215],v2 offset:12544
ds_read_b64_tr_b16 a[220:221],v3 offset:8448
ds_read_b64_tr_b16 a[222:223],v3 offset:12544
ds_read_b64_tr_b16 a[216:217],v3 offset:256
ds_read_b64_tr_b16 a[218:219],v3 offset:4352
ds_read_b64_tr_b16 a[224:225],v2 offset:512
v_mfma_f32_16x16x32_bf16 v[196:199],a[128:131],v[96:99],v[40:43]
buffer_load_dwordx4 v5,s[24:27],s47 offen offset:3072 nt lds
v_mfma_f32_16x16x32_bf16 v[196:199],a[132:135],v[100:103],v[196:199]
s_add_u32 s47,s47,16384
s_and_b32 s47,s47,0x3ffff
ds_read_b64_tr_b16 a[226:227],v2 offset:4608
v_mfma_f32_16x16x32_bf16 v[196:199],a[136:139],v[104:107],v[196:199]
s_or_b32 s47,s47,s46
ds_read_b64_tr_b16 a[228:229],v2 offset:8704
v_mfma_f32_16x16x32_bf16 v[196:199],a[140:143],v[108:111],v[196:199]
ds_read_b64_tr_b16 a[230:231],v2 offset:12800
v_mfma_f32_16x16x32_bf16 v[204:207],a[156:159],v[108:111],v[40:43]
ds_read_b64_tr_b16 a[236:237],v3 offset:8704
v_mfma_f32_16x16x32_bf16 v[204:207],a[152:155],v[104:107],v[204:207]
ds_read_b64_tr_b16 a[238:239],v3 offset:12800
v_mfma_f32_16x16x32_bf16 v[204:207],a[148:151],v[100:103],v[204:207]
ds_read_b64_tr_b16 a[232:233],v3 offset:512
v_mfma_f32_16x16x32_bf16 v[204:207],a[144:147],v[96:99],v[204:207]
ds_read_b64_tr_b16 a[234:235],v3 offset:4608
v_mfma_f32_16x16x32_bf16 v[212:215],a[160:163],v[96:99],v[40:43]
ds_read_b64_tr_b16 a[240:241],v2 offset:768
v_mfma_f32_16x16x32_bf16 v[212:215],a[164:167],v[100:103],v[212:215]
ds_read_b64_tr_b16 a[242:243],v2 offset:4864
v_mfma_f32_16x16x32_bf16 v[212:215],a[168:171],v[104:107],v[212:215]
ds_read_b64_tr_b16 a[244:245],v2 offset:8960
v_mfma_f32_16x16x32_bf16 v[212:215],a[172:175],v[108:111],v[212:215]
ds_read_b64_tr_b16 a[246:247],v2 offset:13056
v_mfma_f32_16x16x32_bf16 v[220:223],a[188:191],v[108:111],v[40:43]
ds_read_b64_tr_b16 a[252:253],v3 offset:8960
v_mfma_f32_16x16x32_bf16 v[220:223],a[184:187],v[104:107],v[220:223]
ds_read_b64_tr_b16 a[254:255],v3 offset:13056
v_mfma_f32_16x16x32_bf16 v[220:223],a[180:183],v[100:103],v[220:223]
ds_read_b64_tr_b16 a[248:249],v3 offset:768
v_mfma_f32_16x16x32_bf16 v[220:223],a[176:179],v[96:99],v[220:223]
s_waitcnt lgkmcnt(7)
ds_read_b64_tr_b16 a[250:251],v3 offset:4864
v_mfma_f32_16x16x32_bf16 a[0:3],a[192:195],v[132:135],a[0:3]
buffer_load_dword v26,v24,s[88:91],s93 offen
v_mfma_f32_16x16x32_bf16 a[0:3],a[196:199],v[148:151],a[0:3]
v_exp_f32 v180,v180
v_mfma_f32_16x16x32_bf16 a[4:7],a[204:207],v[148:151],a[4:7]
v_exp_f32 v181,v181
v_mfma_f32_16x16x32_bf16 a[4:7],a[200:203],v[132:135],a[4:7]
v_exp_f32 v182,v182
v_mfma_f32_16x16x32_bf16 a[8:11],a[208:211],v[132:135],a[8:11]
v_exp_f32 v183,v183
v_mfma_f32_16x16x32_bf16 a[8:11],a[212:215],v[148:151],a[8:11]
v_exp_f32 v172,v172
v_mfma_f32_16x16x32_bf16 a[12:15],a[220:223],v[148:151],a[12:15]
v_exp_f32 v173,v173
v_mfma_f32_16x16x32_bf16 a[12:15],a[216:219],v[132:135],a[12:15]
v_exp_f32 v174,v174
v_mfma_f32_16x16x32_bf16 a[16:19],a[224:227],v[132:135],a[16:19]
s_bitcmp1_b32 s45,0
s_cbranch_scc0 L_t31v30_%=
s_mov_b32 m0,s80
s_add_u32 s80,s80,4096
buffer_load_dwordx4 v25,s[20:23],s83 offen nt lds
s_add_u32 s83,s83,4096
L_t31v30_%=:
v_mfma_f32_16x16x32_bf16 a[16:19],a[228:231],v[148:151],a[16:19]
v_exp_f32 v175,v175
v_mfma_f32_16x16x32_bf16 a[20:23],a[236:239],v[148:151],a[20:23]
v_cvt_pk_bf16_f32 v180,v180,v181
v_cvt_pk_bf16_f32 v181,v182,v183
v_mfma_f32_16x16x32_bf16 a[20:23],a[232:235],v[132:135],a[20:23]
s_waitcnt lgkmcnt(0)
v_exp_f32 v188,v188
v_mfma_f32_16x16x32_bf16 a[24:27],a[240:243],v[132:135],a[24:27]
v_exp_f32 v189,v189
v_mfma_f32_16x16x32_bf16 a[24:27],a[244:247],v[148:151],a[24:27]
v_exp_f32 v190,v190
v_mfma_f32_16x16x32_bf16 a[28:31],a[252:255],v[148:151],a[28:31]
v_cvt_pk_bf16_f32 v166,v172,v173
v_mfma_f32_16x16x32_bf16 a[28:31],a[248:251],v[132:135],a[28:31]
v_exp_f32 v191,v191
v_mfma_f32_16x16x32_bf16 v[48:51],v[28:31],v[132:135],v[48:51]
v_cvt_pk_bf16_f32 v167,v174,v175
v_cvt_pk_bf16_f32 v182,v188,v189
v_cvt_pk_bf16_f32 v183,v190,v191
s_cmp_lg_u32 s53,0
v_mfma_f32_16x16x32_bf16 v[48:51],v[28:31],v[148:151],v[48:51]
s_cbranch_scc1 L_att0v3_%=
s_waitcnt lgkmcnt(0)
s_nop 7
s_branch L_att0x_%=
.p2align 5
L_att0v0_%=:
v_mfma_f32_16x16x32_bf16 v[228:231],a[128:131],v[112:115],v[44:47]
s_waitcnt vmcnt(8) lgkmcnt(0)
v_exp_f32 v196,v196
v_mfma_f32_16x16x32_bf16 v[228:231],a[132:135],v[116:119],v[228:231]
s_barrier
s_add_u32 m0,s49,s51
ds_read_b128 a[128:131],v1
v_mfma_f32_16x16x32_bf16 v[228:231],a[136:139],v[120:123],v[228:231]
s_mov_b32 s90,L_endseg_%=-L_pc_%=+128
v_exp_f32 v197,v197
v_mfma_f32_16x16x32_bf16 v[228:231],a[140:143],v[124:127],v[228:231]
s_lshr_b32 s45,s45,1
ds_read_b128 a[132:135],v1 offset:256
v_cvt_pk_bf16_f32 v196,v196,v197
v_mfma_f32_16x16x32_bf16 v[236:239],a[156:159],v[124:127],v[44:47]
s_add_u32 s62,s50,32768
buffer_load_dwordx4 v4,s[20:23],s47 offen nt lds
v_mfma_f32_16x16x32_bf16 v[236:239],a[152:155],v[120:123],v[236:239]
s_and_b32 s62,s62,131071
ds_read_b128 a[136:139],v1 offset:512
v_add_u32 v2,s48,v2
v_mfma_f32_16x16x32_bf16 v[236:239],a[148:151],v[116:119],v[236:239]
s_add_u32 s54,s54,16384
v_exp_f32 v198,v198
v_mfma_f32_16x16x32_bf16 v[236:239],a[144:147],v[112:115],v[236:239]
s_sub_u32 s53,s53,1
ds_read_b128 a[140:143],v1 offset:768
v_add_u32 v3,s48,v3
v_mfma_f32_16x16x32_bf16 v[244:247],a[160:163],v[112:115],v[44:47]
s_sub_u32 s48,s62,s50
v_exp_f32 v199,v199
v_mfma_f32_16x16x32_bf16 v[244:247],a[164:167],v[116:119],v[244:247]
s_and_b32 s46,s54,0xfffc0000
ds_read_b128 a[156:159],v1 offset:4864
v_cvt_pk_bf16_f32 v197,v198,v199
v_mfma_f32_16x16x32_bf16 v[244:247],a[168:171],v[120:123],v[244:247]
s_mov_b32 s50,s62
v_exp_f32 v204,v204
v_mfma_f32_16x16x32_bf16 v[244:247],a[172:175],v[124:127],v[244:247]
ds_read_b128 a[152:155],v1 offset:4608
v_mfma_f32_16x16x32_bf16 v[252:255],a[188:191],v[124:127],v[44:47]
v_exp_f32 v205,v205
v_mfma_f32_16x16x32_bf16 v[252:255],a[184:187],v[120:123],v[252:255]
ds_read_b128 a[148:151],v1 offset:4352
v_cvt_pk_bf16_f32 v198,v204,v205
v_mfma_f32_16x16x32_bf16 v[252:255],a[180:183],v[116:119],v[252:255]
v_exp_f32 v206,v206
v_mfma_f32_16x16x32_bf16 v[252:255],a[176:179],v[112:115],v[252:255]
ds_read_b128 a[144:147],v1 offset:4096
v_mfma_f32_16x16x32_bf16 a[32:35],a[192:195],v[164:167],a[32:35]
v_exp_f32 v207,v207
v_mfma_f32_16x16x32_bf16 a[32:35],a[196:199],v[180:183],a[32:35]
ds_read_b128 a[160:163],v1 offset:8192
v_cvt_pk_bf16_f32 v199,v206,v207
v_mfma_f32_16x16x32_bf16 a[36:39],a[204:207],v[180:183],a[36:39]
buffer_load_dwordx4 v4,s[20:23],s47 offen offset:1024 nt lds
v_mfma_f32_16x16x32_bf16 a[36:39],a[200:203],v[164:167],a[36:39]
ds_read_b128 a[164:167],v1 offset:8448
v_mfma_f32_16x16x32_bf16 a[40:43],a[208:211],v[164:167],a[40:43]
v_exp_f32 v212,v212
v_mfma_f32_16x16x32_bf16 a[40:43],a[212:215],v[180:183],a[40:43]
ds_read_b128 a[168:171],v1 offset:8704
v_mfma_f32_16x16x32_bf16 a[44:47],a[220:223],v[180:183],a[44:47]
v_exp_f32 v213,v213
v_mfma_f32_16x16x32_bf16 a[44:47],a[216:219],v[164:167],a[44:47]
ds_read_b128 a[172:175],v1 offset:8960
v_cvt_pk_bf16_f32 v212,v212,v213
v_mfma_f32_16x16x32_bf16 a[48:51],a[224:227],v[164:167],a[48:51]
v_exp_f32 v214,v214
v_mfma_f32_16x16x32_bf16 a[48:51],a[228:231],v[180:183],a[48:51]
ds_read_b128 a[188:191],v1 offset:13056
v_mfma_f32_16x16x32_bf16 a[52:55],a[236:239],v[180:183],a[52:55]
v_exp_f32 v215,v215
v_mfma_f32_16x16x32_bf16 a[52:55],a[232:235],v[164:167],a[52:55]
ds_read_b128 a[184:187],v1 offset:12800
v_cvt_pk_bf16_f32 v213,v214,v215
v_mfma_f32_16x16x32_bf16 a[56:59],a[240:243],v[164:167],a[56:59]
v_exp_f32 v220,v220
v_mfma_f32_16x16x32_bf16 a[56:59],a[244:247],v[180:183],a[56:59]
ds_read_b128 a[180:183],v1 offset:12544
v_mfma_f32_16x16x32_bf16 a[60:63],a[252:255],v[180:183],a[60:63]
)A"
R"A(v_exp_f32 v221,v221
v_mfma_f32_16x16x32_bf16 a[60:63],a[248:251],v[164:167],a[60:63]
ds_read_b128 a[176:179],v1 offset:12288
v_cvt_pk_bf16_f32 v214,v220,v221
v_mfma_f32_16x16x32_bf16 v[52:55],v[28:31],v[164:167],v[52:55]
buffer_load_dwordx4 v5,s[20:23],s47 offen offset:2048 nt lds
v_mfma_f32_16x16x32_bf16 v[52:55],v[28:31],v[180:183],v[52:55]
v_exp_f32 v222,v222
v_exp_f32 v223,v223
s_nop 0
v_cvt_pk_bf16_f32 v215,v222,v223
v_add_u32 v1,s48,v1
v_exp_f32 v228,v228
v_exp_f32 v229,v229
v_exp_f32 v230,v230
v_exp_f32 v231,v231
v_cvt_pk_bf16_f32 v228,v228,v229
v_cvt_pk_bf16_f32 v229,v230,v231
v_exp_f32 v236,v236
v_exp_f32 v237,v237
v_exp_f32 v238,v238
v_exp_f32 v239,v239
v_exp_f32 v244,v244
buffer_load_dwordx4 v5,s[20:23],s47 offen offset:3072 nt lds
v_exp_f32 v245,v245
v_exp_f32 v246,v246
v_exp_f32 v247,v247
v_mfma_f32_16x16x32_bf16 a[64:67],a[192:195],v[196:199],a[64:67]
v_cvt_pk_bf16_f32 v230,v236,v237
v_cvt_pk_bf16_f32 v231,v238,v239
v_mfma_f32_16x16x32_bf16 a[64:67],a[196:199],v[212:215],a[64:67]
v_cvt_pk_bf16_f32 v244,v244,v245
v_cvt_pk_bf16_f32 v245,v246,v247
v_mfma_f32_16x16x32_bf16 a[68:71],a[204:207],v[212:215],a[68:71]
v_exp_f32 v252,v252
v_mfma_f32_16x16x32_bf16 a[68:71],a[200:203],v[196:199],a[68:71]
v_exp_f32 v253,v253
v_mfma_f32_16x16x32_bf16 a[72:75],a[208:211],v[196:199],a[72:75]
v_exp_f32 v254,v254
v_mfma_f32_16x16x32_bf16 a[72:75],a[212:215],v[212:215],a[72:75]
v_exp_f32 v255,v255
v_mfma_f32_16x16x32_bf16 a[76:79],a[220:223],v[212:215],a[76:79]
v_cvt_pk_bf16_f32 v246,v252,v253
v_cvt_pk_bf16_f32 v247,v254,v255
v_mfma_f32_16x16x32_bf16 a[76:79],a[216:219],v[196:199],a[76:79]
s_add_u32 m0,s49,s52
v_mfma_f32_16x16x32_bf16 a[80:83],a[224:227],v[196:199],a[80:83]
s_xor_b32 s49,s50,65536
v_mfma_f32_16x16x32_bf16 a[80:83],a[228:231],v[212:215],a[80:83]
v_mfma_f32_16x16x32_bf16 a[84:87],a[236:239],v[212:215],a[84:87]
buffer_load_dwordx4 v4,s[24:27],s47 offen nt lds
v_mfma_f32_16x16x32_bf16 a[84:87],a[232:235],v[196:199],a[84:87]
v_mfma_f32_16x16x32_bf16 a[88:91],a[240:243],v[196:199],a[88:91]
v_mfma_f32_16x16x32_bf16 a[88:91],a[244:247],v[212:215],a[88:91]
v_mfma_f32_16x16x32_bf16 a[92:95],a[252:255],v[212:215],a[92:95]
v_mfma_f32_16x16x32_bf16 a[92:95],a[248:251],v[196:199],a[92:95]
v_mfma_f32_16x16x32_bf16 v[56:59],v[28:31],v[196:199],v[56:59]
v_mfma_f32_16x16x32_bf16 v[56:59],v[28:31],v[212:215],v[56:59]
s_waitcnt lgkmcnt(0)
v_mfma_f32_16x16x32_bf16 v[164:167],a[128:131],v[80:83],v[36:39]
v_mfma_f32_16x16x32_bf16 v[164:167],a[132:135],v[84:87],v[164:167]
v_mfma_f32_16x16x32_bf16 v[164:167],a[136:139],v[88:91],v[164:167]
v_mfma_f32_16x16x32_bf16 v[164:167],a[140:143],v[92:95],v[164:167]
v_mfma_f32_16x16x32_bf16 v[172:175],a[156:159],v[92:95],v[36:39]
v_mfma_f32_16x16x32_bf16 v[172:175],a[152:155],v[88:91],v[172:175]
v_mfma_f32_16x16x32_bf16 v[172:175],a[148:151],v[84:87],v[172:175]
buffer_load_dwordx4 v5,s[24:27],s47 offen offset:1024 nt lds
v_mfma_f32_16x16x32_bf16 v[172:175],a[144:147],v[80:83],v[172:175]
v_mfma_f32_16x16x32_bf16 v[180:183],a[160:163],v[80:83],v[36:39]
v_mfma_f32_16x16x32_bf16 v[180:183],a[164:167],v[84:87],v[180:183]
v_mfma_f32_16x16x32_bf16 v[180:183],a[168:171],v[88:91],v[180:183]
v_mfma_f32_16x16x32_bf16 v[180:183],a[172:175],v[92:95],v[180:183]
v_exp_f32 v164,v164
v_mfma_f32_16x16x32_bf16 v[188:191],a[188:191],v[92:95],v[36:39]
v_exp_f32 v165,v165
v_mfma_f32_16x16x32_bf16 v[188:191],a[184:187],v[88:91],v[188:191]
v_exp_f32 v166,v166
v_mfma_f32_16x16x32_bf16 v[188:191],a[180:183],v[84:87],v[188:191]
v_exp_f32 v167,v167
v_mfma_f32_16x16x32_bf16 v[188:191],a[176:179],v[80:83],v[188:191]
v_cvt_pk_bf16_f32 v164,v164,v165
v_cvt_pk_bf16_f32 v165,v166,v167
v_mfma_f32_16x16x32_bf16 a[96:99],a[192:195],v[228:231],a[96:99]
ds_read_b64_tr_b16 a[192:193],v2
v_mfma_f32_16x16x32_bf16 a[96:99],a[196:199],v[244:247],a[96:99]
ds_read_b64_tr_b16 a[194:195],v2 offset:4096
v_mfma_f32_16x16x32_bf16 a[100:103],a[204:207],v[244:247],a[100:103]
ds_read_b64_tr_b16 a[196:197],v2 offset:8192
v_mfma_f32_16x16x32_bf16 a[100:103],a[200:203],v[228:231],a[100:103]
ds_read_b64_tr_b16 a[198:199],v2 offset:12288
v_mfma_f32_16x16x32_bf16 a[104:107],a[208:211],v[228:231],a[104:107]
buffer_load_dwordx4 v4,s[24:27],s47 offen offset:2048 nt lds
v_mfma_f32_16x16x32_bf16 a[104:107],a[212:215],v[244:247],a[104:107]
ds_read_b64_tr_b16 a[204:205],v3 offset:8192
v_mfma_f32_16x16x32_bf16 a[108:111],a[220:223],v[244:247],a[108:111]
ds_read_b64_tr_b16 a[206:207],v3 offset:12288
v_mfma_f32_16x16x32_bf16 a[108:111],a[216:219],v[228:231],a[108:111]
ds_read_b64_tr_b16 a[200:201],v3
v_mfma_f32_16x16x32_bf16 a[112:115],a[224:227],v[228:231],a[112:115]
ds_read_b64_tr_b16 a[202:203],v3 offset:4096
v_mfma_f32_16x16x32_bf16 a[112:115],a[228:231],v[244:247],a[112:115]
ds_read_b64_tr_b16 a[208:209],v2 offset:256
v_mfma_f32_16x16x32_bf16 a[116:119],a[236:239],v[244:247],a[116:119]
ds_read_b64_tr_b16 a[210:211],v2 offset:4352
v_mfma_f32_16x16x32_bf16 a[116:119],a[232:235],v[228:231],a[116:119]
ds_read_b64_tr_b16 a[212:213],v2 offset:8448
v_mfma_f32_16x16x32_bf16 a[120:123],a[240:243],v[228:231],a[120:123]
ds_read_b64_tr_b16 a[214:215],v2 offset:12544
v_mfma_f32_16x16x32_bf16 a[120:123],a[244:247],v[244:247],a[120:123]
ds_read_b64_tr_b16 a[220:221],v3 offset:8448
v_mfma_f32_16x16x32_bf16 a[124:127],a[252:255],v[244:247],a[124:127]
ds_read_b64_tr_b16 a[222:223],v3 offset:12544
v_mfma_f32_16x16x32_bf16 a[124:127],a[248:251],v[228:231],a[124:127]
ds_read_b64_tr_b16 a[216:217],v3 offset:256
v_mfma_f32_16x16x32_bf16 v[60:63],v[28:31],v[228:231],v[60:63]
ds_read_b64_tr_b16 a[218:219],v3 offset:4352
v_mfma_f32_16x16x32_bf16 v[60:63],v[28:31],v[244:247],v[60:63]
ds_read_b64_tr_b16 a[224:225],v2 offset:512
v_mfma_f32_16x16x32_bf16 v[196:199],a[128:131],v[96:99],v[40:43]
buffer_load_dwordx4 v5,s[24:27],s47 offen offset:3072 nt lds
v_mfma_f32_16x16x32_bf16 v[196:199],a[132:135],v[100:103],v[196:199]
s_add_u32 s47,s47,16384
s_and_b32 s47,s47,0x3ffff
ds_read_b64_tr_b16 a[226:227],v2 offset:4608
v_mfma_f32_16x16x32_bf16 v[196:199],a[136:139],v[104:107],v[196:199]
s_or_b32 s47,s47,s46
ds_read_b64_tr_b16 a[228:229],v2 offset:8704
v_mfma_f32_16x16x32_bf16 v[196:199],a[140:143],v[108:111],v[196:199]
ds_read_b64_tr_b16 a[230:231],v2 offset:12800
v_mfma_f32_16x16x32_bf16 v[204:207],a[156:159],v[108:111],v[40:43]
ds_read_b64_tr_b16 a[236:237],v3 offset:8704
v_mfma_f32_16x16x32_bf16 v[204:207],a[152:155],v[104:107],v[204:207]
ds_read_b64_tr_b16 a[238:239],v3 offset:12800
v_mfma_f32_16x16x32_bf16 v[204:207],a[148:151],v[100:103],v[204:207]
ds_read_b64_tr_b16 a[232:233],v3 offset:512
v_mfma_f32_16x16x32_bf16 v[204:207],a[144:147],v[96:99],v[204:207]
ds_read_b64_tr_b16 a[234:235],v3 offset:4608
v_mfma_f32_16x16x32_bf16 v[212:215],a[160:163],v[96:99],v[40:43]
ds_read_b64_tr_b16 a[240:241],v2 offset:768
v_mfma_f32_16x16x32_bf16 v[212:215],a[164:167],v[100:103],v[212:215]
ds_read_b64_tr_b16 a[242:243],v2 offset:4864
v_mfma_f32_16x16x32_bf16 v[212:215],a[168:171],v[104:107],v[212:215]
ds_read_b64_tr_b16 a[244:245],v2 offset:8960
v_mfma_f32_16x16x32_bf16 v[212:215],a[172:175],v[108:111],v[212:215]
ds_read_b64_tr_b16 a[246:247],v2 offset:13056
v_mfma_f32_16x16x32_bf16 v[220:223],a[188:191],v[108:111],v[40:43]
ds_read_b64_tr_b16 a[252:253],v3 offset:8960
v_mfma_f32_16x16x32_bf16 v[220:223],a[184:187],v[104:107],v[220:223]
ds_read_b64_tr_b16 a[254:255],v3 offset:13056
v_mfma_f32_16x16x32_bf16 v[220:223],a[180:183],v[100:103],v[220:223]
ds_read_b64_tr_b16 a[248:249],v3 offset:768
v_mfma_f32_16x16x32_bf16 v[220:223],a[176:179],v[96:99],v[220:223]
ds_read_b64_tr_b16 a[250:251],v3 offset:4864
buffer_load_dword v26,v24,s[88:91],s93 offen
v_exp_f32 v180,v180
v_exp_f32 v181,v181
v_exp_f32 v182,v182
v_exp_f32 v183,v183
v_exp_f32 v172,v172
v_exp_f32 v173,v173
v_exp_f32 v174,v174
s_bitcmp1_b32 s45,0
s_cbranch_scc0 L_t31v00_%=
s_mov_b32 m0,s80
s_add_u32 s80,s80,4096
buffer_load_dwordx4 v25,s[20:23],s83 offen nt lds
s_add_u32 s83,s83,4096
L_t31v00_%=:
v_exp_f32 v175,v175
v_cvt_pk_bf16_f32 v180,v180,v181
v_cvt_pk_bf16_f32 v181,v182,v183
v_exp_f32 v188,v188
v_exp_f32 v189,v189
v_exp_f32 v190,v190
v_cvt_pk_bf16_f32 v166,v172,v173
v_exp_f32 v191,v191
v_cvt_pk_bf16_f32 v167,v174,v175
v_cvt_pk_bf16_f32 v182,v188,v189
v_cvt_pk_bf16_f32 v183,v190,v191
s_cmp_lg_u32 s53,0
s_cbranch_scc1 L_att0v0_%=
s_waitcnt lgkmcnt(0)
s_nop 7
s_branch L_att0x_%=
.p2align 5
L_att0v1_%=:
v_mfma_f32_16x16x32_bf16 v[228:231],a[128:131],v[112:115],v[44:47]
s_waitcnt vmcnt(8) lgkmcnt(0)
v_exp_f32 v196,v196
v_mfma_f32_16x16x32_bf16 v[228:231],a[132:135],v[116:119],v[228:231]
s_barrier
s_add_u32 m0,s49,s51
ds_read_b128 a[128:131],v1
v_mfma_f32_16x16x32_bf16 v[228:231],a[136:139],v[120:123],v[228:231]
s_mov_b32 s90,L_endseg_%=-L_pc_%=+128
v_exp_f32 v197,v197
v_mfma_f32_16x16x32_bf16 v[228:231],a[140:143],v[124:127],v[228:231]
s_lshr_b32 s45,s45,1
ds_read_b128 a[132:135],v1 offset:256
v_cvt_pk_bf16_f32 v196,v196,v197
v_mfma_f32_16x16x32_bf16 v[236:239],a[156:159],v[124:127],v[44:47]
s_add_u32 s62,s50,32768
buffer_load_dwordx4 v4,s[20:23],s47 offen nt lds
v_mfma_f32_16x16x32_bf16 v[236:239],a[152:155],v[120:123],v[236:239]
s_and_b32 s62,s62,131071
ds_read_b128 a[136:139],v1 offset:512
v_add_u32 v2,s48,v2
v_mfma_f32_16x16x32_bf16 v[236:239],a[148:151],v[116:119],v[236:239]
s_add_u32 s54,s54,16384
v_exp_f32 v198,v198
v_mfma_f32_16x16x32_bf16 v[236:239],a[144:147],v[112:115],v[236:239]
s_sub_u32 s53,s53,1
ds_read_b128 a[140:143],v1 offset:768
v_add_u32 v3,s48,v3
v_mfma_f32_16x16x32_bf16 v[244:247],a[160:163],v[112:115],v[44:47]
s_sub_u32 s48,s62,s50
v_exp_f32 v199,v199
v_mfma_f32_16x16x32_bf16 v[244:247],a[164:167],v[116:119],v[244:247]
s_and_b32 s46,s54,0xfffc0000
ds_read_b128 a[156:159],v1 offset:4864
v_cvt_pk_bf16_f32 v197,v198,v199
v_mfma_f32_16x16x32_bf16 v[244:247],a[168:171],v[120:123],v[244:247]
s_mov_b32 s50,s62
v_exp_f32 v204,v204
v_mfma_f32_16x16x32_bf16 v[244:247],a[172:175],v[124:127],v[244:247]
ds_read_b128 a[152:155],v1 offset:4608
v_mfma_f32_16x16x32_bf16 v[252:255],a[188:191],v[124:127],v[44:47]
v_exp_f32 v205,v205
v_mfma_f32_16x16x32_bf16 v[252:255],a[184:187],v[120:123],v[252:255]
ds_read_b128 a[148:151],v1 offset:4352
v_cvt_pk_bf16_f32 v198,v204,v205
v_mfma_f32_16x16x32_bf16 v[252:255],a[180:183],v[116:119],v[252:255]
v_exp_f32 v206,v206
v_mfma_f32_16x16x32_bf16 v[252:255],a[176:179],v[112:115],v[252:255]
s_waitcnt lgkmcnt(3)
ds_read_b128 a[144:147],v1 offset:4096
v_exp_f32 v207,v207
ds_read_b128 a[160:163],v1 offset:8192
v_cvt_pk_bf16_f32 v199,v206,v207
buffer_load_dwordx4 v4,s[20:23],s47 offen offset:1024 nt lds
ds_read_b128 a[164:167],v1 offset:8448
v_exp_f32 v212,v212
ds_read_b128 a[168:171],v1 offset:8704
v_exp_f32 v213,v213
ds_read_b128 a[172:175],v1 offset:8960
v_cvt_pk_bf16_f32 v212,v212,v213
v_exp_f32 v214,v214
ds_read_b128 a[188:191],v1 offset:13056
v_exp_f32 v215,v215
ds_read_b128 a[184:187],v1 offset:12800
v_cvt_pk_bf16_f32 v213,v214,v215
v_exp_f32 v220,v220
ds_read_b128 a[180:183],v1 offset:12544
v_exp_f32 v221,v221
ds_read_b128 a[176:179],v1 offset:12288
v_cvt_pk_bf16_f32 v214,v220,v221
buffer_load_dwordx4 v5,s[20:23],s47 offen offset:2048 nt lds
v_exp_f32 v222,v222
v_mfma_f32_16x16x32_bf16 v[132:135],a[128:131],v[64:67],v[32:35]
v_exp_f32 v223,v223
v_mfma_f32_16x16x32_bf16 v[132:135],a[132:135],v[68:71],v[132:135]
v_cvt_pk_bf16_f32 v215,v222,v223
v_add_u32 v1,s48,v1
v_mfma_f32_16x16x32_bf16 v[132:135],a[136:139],v[72:75],v[132:135]
v_exp_f32 v228,v228
v_mfma_f32_16x16x32_bf16 v[132:135],a[140:143],v[76:79],v[132:135]
s_waitcnt lgkmcnt(10)
v_exp_f32 v229,v229
v_mfma_f32_16x16x32_bf16 v[140:143],a[156:159],v[76:79],v[32:35]
v_exp_f32 v230,v230
v_mfma_f32_16x16x32_bf16 v[140:143],a[152:155],v[72:75],v[140:143]
s_waitcnt lgkmcnt(9)
v_exp_f32 v231,v231
v_mfma_f32_16x16x32_bf16 v[140:143],a[148:151],v[68:71],v[140:143]
s_waitcnt lgkmcnt(8)
v_cvt_pk_bf16_f32 v228,v228,v229
v_cvt_pk_bf16_f32 v229,v230,v231
v_mfma_f32_16x16x32_bf16 v[140:143],a[144:147],v[64:67],v[140:143]
s_waitcnt lgkmcnt(0)
v_exp_f32 v236,v236
v_mfma_f32_16x16x32_bf16 v[148:151],a[160:163],v[64:67],v[32:35]
)A"
R"A(v_exp_f32 v237,v237
v_mfma_f32_16x16x32_bf16 v[148:151],a[164:167],v[68:71],v[148:151]
v_exp_f32 v238,v238
v_mfma_f32_16x16x32_bf16 v[148:151],a[168:171],v[72:75],v[148:151]
v_exp_f32 v239,v239
v_mfma_f32_16x16x32_bf16 v[148:151],a[172:175],v[76:79],v[148:151]
v_exp_f32 v244,v244
v_mfma_f32_16x16x32_bf16 v[156:159],a[188:191],v[76:79],v[32:35]
buffer_load_dwordx4 v5,s[20:23],s47 offen offset:3072 nt lds
v_mfma_f32_16x16x32_bf16 v[156:159],a[184:187],v[72:75],v[156:159]
v_exp_f32 v245,v245
v_mfma_f32_16x16x32_bf16 v[156:159],a[180:183],v[68:71],v[156:159]
v_exp_f32 v246,v246
v_mfma_f32_16x16x32_bf16 v[156:159],a[176:179],v[64:67],v[156:159]
v_exp_f32 v247,v247
v_mfma_f32_16x16x32_bf16 a[64:67],a[192:195],v[196:199],a[64:67]
v_cvt_pk_bf16_f32 v230,v236,v237
v_cvt_pk_bf16_f32 v231,v238,v239
v_mfma_f32_16x16x32_bf16 a[64:67],a[196:199],v[212:215],a[64:67]
v_cvt_pk_bf16_f32 v244,v244,v245
v_cvt_pk_bf16_f32 v245,v246,v247
v_mfma_f32_16x16x32_bf16 a[68:71],a[204:207],v[212:215],a[68:71]
v_exp_f32 v252,v252
v_mfma_f32_16x16x32_bf16 a[68:71],a[200:203],v[196:199],a[68:71]
v_exp_f32 v253,v253
v_mfma_f32_16x16x32_bf16 a[72:75],a[208:211],v[196:199],a[72:75]
v_exp_f32 v254,v254
v_mfma_f32_16x16x32_bf16 a[72:75],a[212:215],v[212:215],a[72:75]
v_exp_f32 v255,v255
v_mfma_f32_16x16x32_bf16 a[76:79],a[220:223],v[212:215],a[76:79]
v_cvt_pk_bf16_f32 v246,v252,v253
v_cvt_pk_bf16_f32 v247,v254,v255
v_mfma_f32_16x16x32_bf16 a[76:79],a[216:219],v[196:199],a[76:79]
s_add_u32 m0,s49,s52
v_exp_f32 v132,v132
v_mfma_f32_16x16x32_bf16 a[80:83],a[224:227],v[196:199],a[80:83]
s_xor_b32 s49,s50,65536
v_exp_f32 v133,v133
v_mfma_f32_16x16x32_bf16 a[80:83],a[228:231],v[212:215],a[80:83]
v_exp_f32 v134,v134
v_mfma_f32_16x16x32_bf16 a[84:87],a[236:239],v[212:215],a[84:87]
buffer_load_dwordx4 v4,s[24:27],s47 offen nt lds
v_mfma_f32_16x16x32_bf16 a[84:87],a[232:235],v[196:199],a[84:87]
v_exp_f32 v135,v135
v_mfma_f32_16x16x32_bf16 a[88:91],a[240:243],v[196:199],a[88:91]
v_cvt_pk_bf16_f32 v132,v132,v133
v_cvt_pk_bf16_f32 v133,v134,v135
v_mfma_f32_16x16x32_bf16 a[88:91],a[244:247],v[212:215],a[88:91]
v_exp_f32 v140,v140
v_mfma_f32_16x16x32_bf16 a[92:95],a[252:255],v[212:215],a[92:95]
v_exp_f32 v141,v141
v_mfma_f32_16x16x32_bf16 a[92:95],a[248:251],v[196:199],a[92:95]
v_exp_f32 v142,v142
v_mfma_f32_16x16x32_bf16 v[56:59],v[28:31],v[196:199],v[56:59]
v_exp_f32 v143,v143
v_mfma_f32_16x16x32_bf16 v[56:59],v[28:31],v[212:215],v[56:59]
v_exp_f32 v148,v148
v_exp_f32 v149,v149
v_exp_f32 v150,v150
v_exp_f32 v151,v151
v_cvt_pk_bf16_f32 v134,v140,v141
v_cvt_pk_bf16_f32 v135,v142,v143
v_cvt_pk_bf16_f32 v148,v148,v149
v_cvt_pk_bf16_f32 v149,v150,v151
v_exp_f32 v156,v156
buffer_load_dwordx4 v5,s[24:27],s47 offen offset:1024 nt lds
v_exp_f32 v157,v157
v_exp_f32 v158,v158
v_exp_f32 v159,v159
v_cvt_pk_bf16_f32 v150,v156,v157
v_cvt_pk_bf16_f32 v151,v158,v159
v_mfma_f32_16x16x32_bf16 a[96:99],a[192:195],v[228:231],a[96:99]
ds_read_b64_tr_b16 a[192:193],v2
v_mfma_f32_16x16x32_bf16 a[96:99],a[196:199],v[244:247],a[96:99]
ds_read_b64_tr_b16 a[194:195],v2 offset:4096
v_mfma_f32_16x16x32_bf16 a[100:103],a[204:207],v[244:247],a[100:103]
ds_read_b64_tr_b16 a[196:197],v2 offset:8192
v_mfma_f32_16x16x32_bf16 a[100:103],a[200:203],v[228:231],a[100:103]
ds_read_b64_tr_b16 a[198:199],v2 offset:12288
v_mfma_f32_16x16x32_bf16 a[104:107],a[208:211],v[228:231],a[104:107]
buffer_load_dwordx4 v4,s[24:27],s47 offen offset:2048 nt lds
v_mfma_f32_16x16x32_bf16 a[104:107],a[212:215],v[244:247],a[104:107]
ds_read_b64_tr_b16 a[204:205],v3 offset:8192
v_mfma_f32_16x16x32_bf16 a[108:111],a[220:223],v[244:247],a[108:111]
ds_read_b64_tr_b16 a[206:207],v3 offset:12288
v_mfma_f32_16x16x32_bf16 a[108:111],a[216:219],v[228:231],a[108:111]
ds_read_b64_tr_b16 a[200:201],v3
v_mfma_f32_16x16x32_bf16 a[112:115],a[224:227],v[228:231],a[112:115]
ds_read_b64_tr_b16 a[202:203],v3 offset:4096
v_mfma_f32_16x16x32_bf16 a[112:115],a[228:231],v[244:247],a[112:115]
ds_read_b64_tr_b16 a[208:209],v2 offset:256
v_mfma_f32_16x16x32_bf16 a[116:119],a[236:239],v[244:247],a[116:119]
ds_read_b64_tr_b16 a[210:211],v2 offset:4352
v_mfma_f32_16x16x32_bf16 a[116:119],a[232:235],v[228:231],a[116:119]
ds_read_b64_tr_b16 a[212:213],v2 offset:8448
v_mfma_f32_16x16x32_bf16 a[120:123],a[240:243],v[228:231],a[120:123]
ds_read_b64_tr_b16 a[214:215],v2 offset:12544
v_mfma_f32_16x16x32_bf16 a[120:123],a[244:247],v[244:247],a[120:123]
ds_read_b64_tr_b16 a[220:221],v3 offset:8448
v_mfma_f32_16x16x32_bf16 a[124:127],a[252:255],v[244:247],a[124:127]
ds_read_b64_tr_b16 a[222:223],v3 offset:12544
v_mfma_f32_16x16x32_bf16 a[124:127],a[248:251],v[228:231],a[124:127]
ds_read_b64_tr_b16 a[216:217],v3 offset:256
v_mfma_f32_16x16x32_bf16 v[60:63],v[28:31],v[228:231],v[60:63]
ds_read_b64_tr_b16 a[218:219],v3 offset:4352
v_mfma_f32_16x16x32_bf16 v[60:63],v[28:31],v[244:247],v[60:63]
ds_read_b64_tr_b16 a[224:225],v2 offset:512
v_mfma_f32_16x16x32_bf16 v[196:199],a[128:131],v[96:99],v[40:43]
buffer_load_dwordx4 v5,s[24:27],s47 offen offset:3072 nt lds
v_mfma_f32_16x16x32_bf16 v[196:199],a[132:135],v[100:103],v[196:199]
s_add_u32 s47,s47,16384
s_and_b32 s47,s47,0x3ffff
ds_read_b64_tr_b16 a[226:227],v2 offset:4608
v_mfma_f32_16x16x32_bf16 v[196:199],a[136:139],v[104:107],v[196:199]
s_or_b32 s47,s47,s46
ds_read_b64_tr_b16 a[228:229],v2 offset:8704
v_mfma_f32_16x16x32_bf16 v[196:199],a[140:143],v[108:111],v[196:199]
ds_read_b64_tr_b16 a[230:231],v2 offset:12800
v_mfma_f32_16x16x32_bf16 v[204:207],a[156:159],v[108:111],v[40:43]
ds_read_b64_tr_b16 a[236:237],v3 offset:8704
v_mfma_f32_16x16x32_bf16 v[204:207],a[152:155],v[104:107],v[204:207]
ds_read_b64_tr_b16 a[238:239],v3 offset:12800
v_mfma_f32_16x16x32_bf16 v[204:207],a[148:151],v[100:103],v[204:207]
ds_read_b64_tr_b16 a[232:233],v3 offset:512
v_mfma_f32_16x16x32_bf16 v[204:207],a[144:147],v[96:99],v[204:207]
ds_read_b64_tr_b16 a[234:235],v3 offset:4608
v_mfma_f32_16x16x32_bf16 v[212:215],a[160:163],v[96:99],v[40:43]
ds_read_b64_tr_b16 a[240:241],v2 offset:768
v_mfma_f32_16x16x32_bf16 v[212:215],a[164:167],v[100:103],v[212:215]
ds_read_b64_tr_b16 a[242:243],v2 offset:4864
v_mfma_f32_16x16x32_bf16 v[212:215],a[168:171],v[104:107],v[212:215]
ds_read_b64_tr_b16 a[244:245],v2 offset:8960
v_mfma_f32_16x16x32_bf16 v[212:215],a[172:175],v[108:111],v[212:215]
ds_read_b64_tr_b16 a[246:247],v2 offset:13056
v_mfma_f32_16x16x32_bf16 v[220:223],a[188:191],v[108:111],v[40:43]
ds_read_b64_tr_b16 a[252:253],v3 offset:8960
v_mfma_f32_16x16x32_bf16 v[220:223],a[184:187],v[104:107],v[220:223]
ds_read_b64_tr_b16 a[254:255],v3 offset:13056
v_mfma_f32_16x16x32_bf16 v[220:223],a[180:183],v[100:103],v[220:223]
ds_read_b64_tr_b16 a[248:249],v3 offset:768
v_mfma_f32_16x16x32_bf16 v[220:223],a[176:179],v[96:99],v[220:223]
s_waitcnt lgkmcnt(7)
ds_read_b64_tr_b16 a[250:251],v3 offset:4864
v_mfma_f32_16x16x32_bf16 a[0:3],a[192:195],v[132:135],a[0:3]
buffer_load_dword v26,v24,s[88:91],s93 offen
v_mfma_f32_16x16x32_bf16 a[0:3],a[196:199],v[148:151],a[0:3]
v_mfma_f32_16x16x32_bf16 a[4:7],a[204:207],v[148:151],a[4:7]
v_mfma_f32_16x16x32_bf16 a[4:7],a[200:203],v[132:135],a[4:7]
v_mfma_f32_16x16x32_bf16 a[8:11],a[208:211],v[132:135],a[8:11]
v_mfma_f32_16x16x32_bf16 a[8:11],a[212:215],v[148:151],a[8:11]
v_mfma_f32_16x16x32_bf16 a[12:15],a[220:223],v[148:151],a[12:15]
v_mfma_f32_16x16x32_bf16 a[12:15],a[216:219],v[132:135],a[12:15]
v_mfma_f32_16x16x32_bf16 a[16:19],a[224:227],v[132:135],a[16:19]
s_bitcmp1_b32 s45,0
s_cbranch_scc0 L_t31v10_%=
s_mov_b32 m0,s80
s_add_u32 s80,s80,4096
buffer_load_dwordx4 v25,s[20:23],s83 offen nt lds
s_add_u32 s83,s83,4096
L_t31v10_%=:
v_mfma_f32_16x16x32_bf16 a[16:19],a[228:231],v[148:151],a[16:19]
v_mfma_f32_16x16x32_bf16 a[20:23],a[236:239],v[148:151],a[20:23]
v_mfma_f32_16x16x32_bf16 a[20:23],a[232:235],v[132:135],a[20:23]
s_waitcnt lgkmcnt(0)
v_mfma_f32_16x16x32_bf16 a[24:27],a[240:243],v[132:135],a[24:27]
v_mfma_f32_16x16x32_bf16 a[24:27],a[244:247],v[148:151],a[24:27]
v_mfma_f32_16x16x32_bf16 a[28:31],a[252:255],v[148:151],a[28:31]
v_mfma_f32_16x16x32_bf16 a[28:31],a[248:251],v[132:135],a[28:31]
v_mfma_f32_16x16x32_bf16 v[48:51],v[28:31],v[132:135],v[48:51]
s_cmp_lg_u32 s53,0
v_mfma_f32_16x16x32_bf16 v[48:51],v[28:31],v[148:151],v[48:51]
s_cbranch_scc1 L_att0v1_%=
s_waitcnt lgkmcnt(0)
s_nop 7
s_branch L_att0x_%=
.p2align 5
L_att0v2_%=:
v_mfma_f32_16x16x32_bf16 v[228:231],a[128:131],v[112:115],v[44:47]
s_waitcnt vmcnt(8) lgkmcnt(0)
v_mfma_f32_16x16x32_bf16 v[228:231],a[132:135],v[116:119],v[228:231]
s_barrier
s_add_u32 m0,s49,s51
ds_read_b128 a[128:131],v1
v_mfma_f32_16x16x32_bf16 v[228:231],a[136:139],v[120:123],v[228:231]
s_mov_b32 s90,L_endseg_%=-L_pc_%=+128
v_mfma_f32_16x16x32_bf16 v[228:231],a[140:143],v[124:127],v[228:231]
s_lshr_b32 s45,s45,1
ds_read_b128 a[132:135],v1 offset:256
v_mfma_f32_16x16x32_bf16 v[236:239],a[156:159],v[124:127],v[44:47]
s_add_u32 s62,s50,32768
buffer_load_dwordx4 v4,s[20:23],s47 offen nt lds
v_mfma_f32_16x16x32_bf16 v[236:239],a[152:155],v[120:123],v[236:239]
s_and_b32 s62,s62,131071
ds_read_b128 a[136:139],v1 offset:512
v_add_u32 v2,s48,v2
v_mfma_f32_16x16x32_bf16 v[236:239],a[148:151],v[116:119],v[236:239]
s_add_u32 s54,s54,16384
v_mfma_f32_16x16x32_bf16 v[236:239],a[144:147],v[112:115],v[236:239]
s_sub_u32 s53,s53,1
ds_read_b128 a[140:143],v1 offset:768
v_add_u32 v3,s48,v3
v_mfma_f32_16x16x32_bf16 v[244:247],a[160:163],v[112:115],v[44:47]
s_sub_u32 s48,s62,s50
v_mfma_f32_16x16x32_bf16 v[244:247],a[164:167],v[116:119],v[244:247]
s_and_b32 s46,s54,0xfffc0000
ds_read_b128 a[156:159],v1 offset:4864
v_mfma_f32_16x16x32_bf16 v[244:247],a[168:171],v[120:123],v[244:247]
s_mov_b32 s50,s62
v_mfma_f32_16x16x32_bf16 v[244:247],a[172:175],v[124:127],v[244:247]
ds_read_b128 a[152:155],v1 offset:4608
v_mfma_f32_16x16x32_bf16 v[252:255],a[188:191],v[124:127],v[44:47]
v_mfma_f32_16x16x32_bf16 v[252:255],a[184:187],v[120:123],v[252:255]
ds_read_b128 a[148:151],v1 offset:4352
v_mfma_f32_16x16x32_bf16 v[252:255],a[180:183],v[116:119],v[252:255]
v_mfma_f32_16x16x32_bf16 v[252:255],a[176:179],v[112:115],v[252:255]
ds_read_b128 a[144:147],v1 offset:4096
v_mfma_f32_16x16x32_bf16 a[32:35],a[192:195],v[164:167],a[32:35]
v_mfma_f32_16x16x32_bf16 a[32:35],a[196:199],v[180:183],a[32:35]
ds_read_b128 a[160:163],v1 offset:8192
v_mfma_f32_16x16x32_bf16 a[36:39],a[204:207],v[180:183],a[36:39]
buffer_load_dwordx4 v4,s[20:23],s47 offen offset:1024 nt lds
v_mfma_f32_16x16x32_bf16 a[36:39],a[200:203],v[164:167],a[36:39]
ds_read_b128 a[164:167],v1 offset:8448
v_mfma_f32_16x16x32_bf16 a[40:43],a[208:211],v[164:167],a[40:43]
v_mfma_f32_16x16x32_bf16 a[40:43],a[212:215],v[180:183],a[40:43]
ds_read_b128 a[168:171],v1 offset:8704
v_mfma_f32_16x16x32_bf16 a[44:47],a[220:223],v[180:183],a[44:47]
v_mfma_f32_16x16x32_bf16 a[44:47],a[216:219],v[164:167],a[44:47]
ds_read_b128 a[172:175],v1 offset:8960
v_mfma_f32_16x16x32_bf16 a[48:51],a[224:227],v[164:167],a[48:51]
v_mfma_f32_16x16x32_bf16 a[48:51],a[228:231],v[180:183],a[48:51]
ds_read_b128 a[188:191],v1 offset:13056
v_mfma_f32_16x16x32_bf16 a[52:55],a[236:239],v[180:183],a[52:55]
v_mfma_f32_16x16x32_bf16 a[52:55],a[232:235],v[164:167],a[52:55]
ds_read_b128 a[184:187],v1 offset:12800
v_mfma_f32_16x16x32_bf16 a[56:59],a[240:243],v[164:167],a[56:59]
v_mfma_f32_16x16x32_bf16 a[56:59],a[244:247],v[180:183],a[56:59]
ds_read_b128 a[180:183],v1 offset:12544
v_mfma_f32_16x16x32_bf16 a[60:63],a[252:255],v[180:183],a[60:63]
v_mfma_f32_16x16x32_bf16 a[60:63],a[248:251],v[164:167],a[60:63]
ds_read_b128 a[176:179],v1 offset:12288
v_mfma_f32_16x16x32_bf16 v[52:55],v[28:31],v[164:167],v[52:55]
buffer_load_dwordx4 v5,s[20:23],s47 offen offset:2048 nt lds
v_mfma_f32_16x16x32_bf16 v[52:55],v[28:31],v[180:183],v[52:55]
s_waitcnt lgkmcnt(3)
v_mfma_f32_16x16x32_bf16 v[132:135],a[128:131],v[64:67],v[32:35]
v_mfma_f32_16x16x32_bf16 v[132:135],a[132:135],v[68:71],v[132:135]
v_add_u32 v1,s48,v1
v_mfma_f32_16x16x32_bf16 v[132:135],a[136:139],v[72:75],v[132:135]
v_exp_f32 v228,v228
v_mfma_f32_16x16x32_bf16 v[132:135],a[140:143],v[76:79],v[132:135]
v_exp_f32 v229,v229
)A"
R"A(v_mfma_f32_16x16x32_bf16 v[140:143],a[156:159],v[76:79],v[32:35]
v_exp_f32 v230,v230
v_mfma_f32_16x16x32_bf16 v[140:143],a[152:155],v[72:75],v[140:143]
v_exp_f32 v231,v231
v_mfma_f32_16x16x32_bf16 v[140:143],a[148:151],v[68:71],v[140:143]
v_cvt_pk_bf16_f32 v228,v228,v229
v_cvt_pk_bf16_f32 v229,v230,v231
v_mfma_f32_16x16x32_bf16 v[140:143],a[144:147],v[64:67],v[140:143]
v_exp_f32 v236,v236
v_mfma_f32_16x16x32_bf16 v[148:151],a[160:163],v[64:67],v[32:35]
v_exp_f32 v237,v237
v_mfma_f32_16x16x32_bf16 v[148:151],a[164:167],v[68:71],v[148:151]
v_exp_f32 v238,v238
v_mfma_f32_16x16x32_bf16 v[148:151],a[168:171],v[72:75],v[148:151]
v_exp_f32 v239,v239
v_mfma_f32_16x16x32_bf16 v[148:151],a[172:175],v[76:79],v[148:151]
v_exp_f32 v244,v244
v_mfma_f32_16x16x32_bf16 v[156:159],a[188:191],v[76:79],v[32:35]
s_waitcnt lgkmcnt(0)
buffer_load_dwordx4 v5,s[20:23],s47 offen offset:3072 nt lds
v_mfma_f32_16x16x32_bf16 v[156:159],a[184:187],v[72:75],v[156:159]
v_exp_f32 v245,v245
v_mfma_f32_16x16x32_bf16 v[156:159],a[180:183],v[68:71],v[156:159]
v_exp_f32 v246,v246
v_mfma_f32_16x16x32_bf16 v[156:159],a[176:179],v[64:67],v[156:159]
v_exp_f32 v247,v247
v_cvt_pk_bf16_f32 v230,v236,v237
v_cvt_pk_bf16_f32 v231,v238,v239
v_cvt_pk_bf16_f32 v244,v244,v245
v_cvt_pk_bf16_f32 v245,v246,v247
v_exp_f32 v252,v252
v_exp_f32 v253,v253
v_exp_f32 v254,v254
v_exp_f32 v255,v255
v_cvt_pk_bf16_f32 v246,v252,v253
v_cvt_pk_bf16_f32 v247,v254,v255
s_add_u32 m0,s49,s52
v_exp_f32 v132,v132
s_xor_b32 s49,s50,65536
v_exp_f32 v133,v133
v_exp_f32 v134,v134
buffer_load_dwordx4 v4,s[24:27],s47 offen nt lds
v_exp_f32 v135,v135
v_cvt_pk_bf16_f32 v132,v132,v133
v_cvt_pk_bf16_f32 v133,v134,v135
v_exp_f32 v140,v140
v_exp_f32 v141,v141
v_exp_f32 v142,v142
v_exp_f32 v143,v143
v_exp_f32 v148,v148
v_mfma_f32_16x16x32_bf16 v[164:167],a[128:131],v[80:83],v[36:39]
v_exp_f32 v149,v149
v_mfma_f32_16x16x32_bf16 v[164:167],a[132:135],v[84:87],v[164:167]
v_exp_f32 v150,v150
v_mfma_f32_16x16x32_bf16 v[164:167],a[136:139],v[88:91],v[164:167]
v_exp_f32 v151,v151
v_mfma_f32_16x16x32_bf16 v[164:167],a[140:143],v[92:95],v[164:167]
v_cvt_pk_bf16_f32 v134,v140,v141
v_cvt_pk_bf16_f32 v135,v142,v143
v_mfma_f32_16x16x32_bf16 v[172:175],a[156:159],v[92:95],v[36:39]
v_cvt_pk_bf16_f32 v148,v148,v149
v_cvt_pk_bf16_f32 v149,v150,v151
v_mfma_f32_16x16x32_bf16 v[172:175],a[152:155],v[88:91],v[172:175]
v_exp_f32 v156,v156
v_mfma_f32_16x16x32_bf16 v[172:175],a[148:151],v[84:87],v[172:175]
buffer_load_dwordx4 v5,s[24:27],s47 offen offset:1024 nt lds
v_mfma_f32_16x16x32_bf16 v[172:175],a[144:147],v[80:83],v[172:175]
v_exp_f32 v157,v157
v_mfma_f32_16x16x32_bf16 v[180:183],a[160:163],v[80:83],v[36:39]
v_exp_f32 v158,v158
v_mfma_f32_16x16x32_bf16 v[180:183],a[164:167],v[84:87],v[180:183]
v_exp_f32 v159,v159
v_mfma_f32_16x16x32_bf16 v[180:183],a[168:171],v[88:91],v[180:183]
v_cvt_pk_bf16_f32 v150,v156,v157
v_cvt_pk_bf16_f32 v151,v158,v159
v_mfma_f32_16x16x32_bf16 v[180:183],a[172:175],v[92:95],v[180:183]
v_exp_f32 v164,v164
v_mfma_f32_16x16x32_bf16 v[188:191],a[188:191],v[92:95],v[36:39]
v_exp_f32 v165,v165
v_mfma_f32_16x16x32_bf16 v[188:191],a[184:187],v[88:91],v[188:191]
v_exp_f32 v166,v166
v_mfma_f32_16x16x32_bf16 v[188:191],a[180:183],v[84:87],v[188:191]
v_exp_f32 v167,v167
v_mfma_f32_16x16x32_bf16 v[188:191],a[176:179],v[80:83],v[188:191]
v_cvt_pk_bf16_f32 v164,v164,v165
v_cvt_pk_bf16_f32 v165,v166,v167
v_mfma_f32_16x16x32_bf16 a[96:99],a[192:195],v[228:231],a[96:99]
ds_read_b64_tr_b16 a[192:193],v2
v_mfma_f32_16x16x32_bf16 a[96:99],a[196:199],v[244:247],a[96:99]
ds_read_b64_tr_b16 a[194:195],v2 offset:4096
v_mfma_f32_16x16x32_bf16 a[100:103],a[204:207],v[244:247],a[100:103]
ds_read_b64_tr_b16 a[196:197],v2 offset:8192
v_mfma_f32_16x16x32_bf16 a[100:103],a[200:203],v[228:231],a[100:103]
ds_read_b64_tr_b16 a[198:199],v2 offset:12288
v_mfma_f32_16x16x32_bf16 a[104:107],a[208:211],v[228:231],a[104:107]
buffer_load_dwordx4 v4,s[24:27],s47 offen offset:2048 nt lds
v_mfma_f32_16x16x32_bf16 a[104:107],a[212:215],v[244:247],a[104:107]
ds_read_b64_tr_b16 a[204:205],v3 offset:8192
v_mfma_f32_16x16x32_bf16 a[108:111],a[220:223],v[244:247],a[108:111]
ds_read_b64_tr_b16 a[206:207],v3 offset:12288
v_mfma_f32_16x16x32_bf16 a[108:111],a[216:219],v[228:231],a[108:111]
ds_read_b64_tr_b16 a[200:201],v3
v_mfma_f32_16x16x32_bf16 a[112:115],a[224:227],v[228:231],a[112:115]
ds_read_b64_tr_b16 a[202:203],v3 offset:4096
v_mfma_f32_16x16x32_bf16 a[112:115],a[228:231],v[244:247],a[112:115]
ds_read_b64_tr_b16 a[208:209],v2 offset:256
v_mfma_f32_16x16x32_bf16 a[116:119],a[236:239],v[244:247],a[116:119]
ds_read_b64_tr_b16 a[210:211],v2 offset:4352
v_mfma_f32_16x16x32_bf16 a[116:119],a[232:235],v[228:231],a[116:119]
ds_read_b64_tr_b16 a[212:213],v2 offset:8448
v_mfma_f32_16x16x32_bf16 a[120:123],a[240:243],v[228:231],a[120:123]
ds_read_b64_tr_b16 a[214:215],v2 offset:12544
v_mfma_f32_16x16x32_bf16 a[120:123],a[244:247],v[244:247],a[120:123]
ds_read_b64_tr_b16 a[220:221],v3 offset:8448
v_mfma_f32_16x16x32_bf16 a[124:127],a[252:255],v[244:247],a[124:127]
ds_read_b64_tr_b16 a[222:223],v3 offset:12544
v_mfma_f32_16x16x32_bf16 a[124:127],a[248:251],v[228:231],a[124:127]
ds_read_b64_tr_b16 a[216:217],v3 offset:256
v_mfma_f32_16x16x32_bf16 v[60:63],v[28:31],v[228:231],v[60:63]
ds_read_b64_tr_b16 a[218:219],v3 offset:4352
v_mfma_f32_16x16x32_bf16 v[60:63],v[28:31],v[244:247],v[60:63]
s_waitcnt lgkmcnt(7)
ds_read_b64_tr_b16 a[224:225],v2 offset:512
buffer_load_dwordx4 v5,s[24:27],s47 offen offset:3072 nt lds
s_add_u32 s47,s47,16384
s_and_b32 s47,s47,0x3ffff
ds_read_b64_tr_b16 a[226:227],v2 offset:4608
s_or_b32 s47,s47,s46
ds_read_b64_tr_b16 a[228:229],v2 offset:8704
ds_read_b64_tr_b16 a[230:231],v2 offset:12800
ds_read_b64_tr_b16 a[236:237],v3 offset:8704
ds_read_b64_tr_b16 a[238:239],v3 offset:12800
ds_read_b64_tr_b16 a[232:233],v3 offset:512
ds_read_b64_tr_b16 a[234:235],v3 offset:4608
ds_read_b64_tr_b16 a[240:241],v2 offset:768
ds_read_b64_tr_b16 a[242:243],v2 offset:4864
ds_read_b64_tr_b16 a[244:245],v2 offset:8960
ds_read_b64_tr_b16 a[246:247],v2 offset:13056
ds_read_b64_tr_b16 a[252:253],v3 offset:8960
ds_read_b64_tr_b16 a[254:255],v3 offset:13056
ds_read_b64_tr_b16 a[248:249],v3 offset:768
ds_read_b64_tr_b16 a[250:251],v3 offset:4864
v_mfma_f32_16x16x32_bf16 a[0:3],a[192:195],v[132:135],a[0:3]
buffer_load_dword v26,v24,s[88:91],s93 offen
v_mfma_f32_16x16x32_bf16 a[0:3],a[196:199],v[148:151],a[0:3]
v_exp_f32 v180,v180
v_mfma_f32_16x16x32_bf16 a[4:7],a[204:207],v[148:151],a[4:7]
v_exp_f32 v181,v181
v_mfma_f32_16x16x32_bf16 a[4:7],a[200:203],v[132:135],a[4:7]
s_waitcnt lgkmcnt(15)
v_exp_f32 v182,v182
v_mfma_f32_16x16x32_bf16 a[8:11],a[208:211],v[132:135],a[8:11]
v_exp_f32 v183,v183
v_mfma_f32_16x16x32_bf16 a[8:11],a[212:215],v[148:151],a[8:11]
v_exp_f32 v172,v172
v_mfma_f32_16x16x32_bf16 a[12:15],a[220:223],v[148:151],a[12:15]
v_exp_f32 v173,v173
v_mfma_f32_16x16x32_bf16 a[12:15],a[216:219],v[132:135],a[12:15]
s_waitcnt lgkmcnt(0)
v_exp_f32 v174,v174
v_mfma_f32_16x16x32_bf16 a[16:19],a[224:227],v[132:135],a[16:19]
s_bitcmp1_b32 s45,0
s_cbranch_scc0 L_t31v20_%=
s_mov_b32 m0,s80
s_add_u32 s80,s80,4096
buffer_load_dwordx4 v25,s[20:23],s83 offen nt lds
s_add_u32 s83,s83,4096
L_t31v20_%=:
v_mfma_f32_16x16x32_bf16 a[16:19],a[228:231],v[148:151],a[16:19]
v_exp_f32 v175,v175
v_mfma_f32_16x16x32_bf16 a[20:23],a[236:239],v[148:151],a[20:23]
v_cvt_pk_bf16_f32 v180,v180,v181
v_cvt_pk_bf16_f32 v181,v182,v183
v_mfma_f32_16x16x32_bf16 a[20:23],a[232:235],v[132:135],a[20:23]
v_exp_f32 v188,v188
v_mfma_f32_16x16x32_bf16 a[24:27],a[240:243],v[132:135],a[24:27]
v_exp_f32 v189,v189
v_mfma_f32_16x16x32_bf16 a[24:27],a[244:247],v[148:151],a[24:27]
v_exp_f32 v190,v190
v_mfma_f32_16x16x32_bf16 a[28:31],a[252:255],v[148:151],a[28:31]
v_cvt_pk_bf16_f32 v166,v172,v173
v_mfma_f32_16x16x32_bf16 a[28:31],a[248:251],v[132:135],a[28:31]
v_exp_f32 v191,v191
v_mfma_f32_16x16x32_bf16 v[48:51],v[28:31],v[132:135],v[48:51]
v_cvt_pk_bf16_f32 v167,v174,v175
v_cvt_pk_bf16_f32 v182,v188,v189
v_cvt_pk_bf16_f32 v183,v190,v191
s_cmp_lg_u32 s53,0
v_mfma_f32_16x16x32_bf16 v[48:51],v[28:31],v[148:151],v[48:51]
s_cbranch_scc1 L_att0v2_%=
s_waitcnt lgkmcnt(0)
s_nop 7
s_branch L_att0x_%=
.p2align 5
L_att0_%=:
v_mfma_f32_16x16x32_bf16 v[228:231],a[128:131],v[112:115],v[44:47]
s_waitcnt vmcnt(8) lgkmcnt(0)
v_exp_f32 v196,v196
v_mfma_f32_16x16x32_bf16 v[228:231],a[132:135],v[116:119],v[228:231]
s_barrier
s_add_u32 m0,s49,s51
ds_read_b128 a[128:131],v1
v_mfma_f32_16x16x32_bf16 v[228:231],a[136:139],v[120:123],v[228:231]
s_mov_b32 s90,L_endseg_%=-L_pc_%=+128
v_exp_f32 v197,v197
v_mfma_f32_16x16x32_bf16 v[228:231],a[140:143],v[124:127],v[228:231]
s_lshr_b32 s45,s45,1
ds_read_b128 a[132:135],v1 offset:256
v_cvt_pk_bf16_f32 v196,v196,v197
v_mfma_f32_16x16x32_bf16 v[236:239],a[156:159],v[124:127],v[44:47]
s_add_u32 s62,s50,32768
buffer_load_dwordx4 v4,s[20:23],s47 offen nt lds
v_mfma_f32_16x16x32_bf16 v[236:239],a[152:155],v[120:123],v[236:239]
s_and_b32 s62,s62,131071
ds_read_b128 a[136:139],v1 offset:512
v_add_u32 v2,s48,v2
v_mfma_f32_16x16x32_bf16 v[236:239],a[148:151],v[116:119],v[236:239]
s_add_u32 s54,s54,16384
v_exp_f32 v198,v198
v_mfma_f32_16x16x32_bf16 v[236:239],a[144:147],v[112:115],v[236:239]
s_sub_u32 s53,s53,1
ds_read_b128 a[140:143],v1 offset:768
v_add_u32 v3,s48,v3
v_mfma_f32_16x16x32_bf16 v[244:247],a[160:163],v[112:115],v[44:47]
s_sub_u32 s48,s62,s50
v_exp_f32 v199,v199
v_mfma_f32_16x16x32_bf16 v[244:247],a[164:167],v[116:119],v[244:247]
s_and_b32 s46,s54,0xfffc0000
ds_read_b128 a[156:159],v1 offset:4864
v_cvt_pk_bf16_f32 v197,v198,v199
v_mfma_f32_16x16x32_bf16 v[244:247],a[168:171],v[120:123],v[244:247]
s_mov_b32 s50,s62
v_exp_f32 v204,v204
v_mfma_f32_16x16x32_bf16 v[244:247],a[172:175],v[124:127],v[244:247]
ds_read_b128 a[152:155],v1 offset:4608
v_mfma_f32_16x16x32_bf16 v[252:255],a[188:191],v[124:127],v[44:47]
v_exp_f32 v205,v205
v_mfma_f32_16x16x32_bf16 v[252:255],a[184:187],v[120:123],v[252:255]
ds_read_b128 a[148:151],v1 offset:4352
v_cvt_pk_bf16_f32 v198,v204,v205
v_mfma_f32_16x16x32_bf16 v[252:255],a[180:183],v[116:119],v[252:255]
v_exp_f32 v206,v206
v_mfma_f32_16x16x32_bf16 v[252:255],a[176:179],v[112:115],v[252:255]
ds_read_b128 a[144:147],v1 offset:4096
v_mfma_f32_16x16x32_bf16 a[32:35],a[192:195],v[164:167],a[32:35]
v_exp_f32 v207,v207
v_mfma_f32_16x16x32_bf16 a[32:35],a[196:199],v[180:183],a[32:35]
ds_read_b128 a[160:163],v1 offset:8192
v_cvt_pk_bf16_f32 v199,v206,v207
v_mfma_f32_16x16x32_bf16 a[36:39],a[204:207],v[180:183],a[36:39]
buffer_load_dwordx4 v4,s[20:23],s47 offen offset:1024 nt lds
v_mfma_f32_16x16x32_bf16 a[36:39],a[200:203],v[164:167],a[36:39]
ds_read_b128 a[164:167],v1 offset:8448
v_mfma_f32_16x16x32_bf16 a[40:43],a[208:211],v[164:167],a[40:43]
v_exp_f32 v212,v212
v_mfma_f32_16x16x32_bf16 a[40:43],a[212:215],v[180:183],a[40:43]
ds_read_b128 a[168:171],v1 offset:8704
v_mfma_f32_16x16x32_bf16 a[44:47],a[220:223],v[180:183],a[44:47]
v_exp_f32 v213,v213
v_mfma_f32_16x16x32_bf16 a[44:47],a[216:219],v[164:167],a[44:47]
ds_read_b128 a[172:175],v1 offset:8960
v_cvt_pk_bf16_f32 v212,v212,v213
v_mfma_f32_16x16x32_bf16 a[48:51],a[224:227],v[164:167],a[48:51]
v_exp_f32 v214,v214
v_mfma_f32_16x16x32_bf16 a[48:51],a[228:231],v[180:183],a[48:51]
ds_read_b128 a[188:191],v1 offset:13056
v_mfma_f32_16x16x32_bf16 a[52:55],a[236:239],v[180:183],a[52:55]
v_exp_f32 v215,v215
v_mfma_f32_16x16x32_bf16 a[52:55],a[232:235],v[164:167],a[52:55]
ds_read_b128 a[184:187],v1 offset:12800
v_cvt_pk_bf16_f32 v213,v214,v215
v_mfma_f32_16x16x32_bf16 a[56:59],a[240:243],v[164:167],a[56:59]
v_exp_f32 v220,v220
v_mfma_f32_16x16x32_bf16 a[56:59],a[244:247],v[180:183],a[56:59]
ds_read_b128 a[180:183],v1 offset:12544
v_mfma_f32_16x16x32_bf16 a[60:63],a[252:255],v[180:183],a[60:63]
v_exp_f32 v221,v221
v_mfma_f32_16x16x32_bf16 a[60:63],a[248:251],v[164:167],a[60:63]
ds_read_b128 a[176:179],v1 offset:12288
v_cvt_pk_bf16_f32 v214,v220,v221
v_mfma_f32_16x16x32_bf16 v[52:55],v[28:31],v[164:167],v[52:55]
buffer_load_dwordx4 v5,s[20:23],s47 offen offset:2048 nt lds
)A"
R"A(v_mfma_f32_16x16x32_bf16 v[52:55],v[28:31],v[180:183],v[52:55]
s_waitcnt lgkmcnt(3)
v_exp_f32 v222,v222
v_mfma_f32_16x16x32_bf16 v[132:135],a[128:131],v[64:67],v[32:35]
v_exp_f32 v223,v223
v_mfma_f32_16x16x32_bf16 v[132:135],a[132:135],v[68:71],v[132:135]
v_cvt_pk_bf16_f32 v215,v222,v223
v_add_u32 v1,s48,v1
v_mfma_f32_16x16x32_bf16 v[132:135],a[136:139],v[72:75],v[132:135]
v_exp_f32 v228,v228
v_mfma_f32_16x16x32_bf16 v[132:135],a[140:143],v[76:79],v[132:135]
v_exp_f32 v229,v229
v_mfma_f32_16x16x32_bf16 v[140:143],a[156:159],v[76:79],v[32:35]
v_exp_f32 v230,v230
v_mfma_f32_16x16x32_bf16 v[140:143],a[152:155],v[72:75],v[140:143]
v_exp_f32 v231,v231
v_mfma_f32_16x16x32_bf16 v[140:143],a[148:151],v[68:71],v[140:143]
v_cvt_pk_bf16_f32 v228,v228,v229
v_cvt_pk_bf16_f32 v229,v230,v231
v_mfma_f32_16x16x32_bf16 v[140:143],a[144:147],v[64:67],v[140:143]
v_exp_f32 v236,v236
v_mfma_f32_16x16x32_bf16 v[148:151],a[160:163],v[64:67],v[32:35]
v_exp_f32 v237,v237
v_mfma_f32_16x16x32_bf16 v[148:151],a[164:167],v[68:71],v[148:151]
v_exp_f32 v238,v238
v_mfma_f32_16x16x32_bf16 v[148:151],a[168:171],v[72:75],v[148:151]
v_exp_f32 v239,v239
v_mfma_f32_16x16x32_bf16 v[148:151],a[172:175],v[76:79],v[148:151]
v_exp_f32 v244,v244
v_mfma_f32_16x16x32_bf16 v[156:159],a[188:191],v[76:79],v[32:35]
s_waitcnt lgkmcnt(0)
buffer_load_dwordx4 v5,s[20:23],s47 offen offset:3072 nt lds
v_mfma_f32_16x16x32_bf16 v[156:159],a[184:187],v[72:75],v[156:159]
v_exp_f32 v245,v245
v_mfma_f32_16x16x32_bf16 v[156:159],a[180:183],v[68:71],v[156:159]
v_exp_f32 v246,v246
v_mfma_f32_16x16x32_bf16 v[156:159],a[176:179],v[64:67],v[156:159]
v_exp_f32 v247,v247
v_mfma_f32_16x16x32_bf16 a[64:67],a[192:195],v[196:199],a[64:67]
v_cvt_pk_bf16_f32 v230,v236,v237
v_cvt_pk_bf16_f32 v231,v238,v239
v_mfma_f32_16x16x32_bf16 a[64:67],a[196:199],v[212:215],a[64:67]
v_cvt_pk_bf16_f32 v244,v244,v245
v_cvt_pk_bf16_f32 v245,v246,v247
v_mfma_f32_16x16x32_bf16 a[68:71],a[204:207],v[212:215],a[68:71]
v_exp_f32 v252,v252
v_mfma_f32_16x16x32_bf16 a[68:71],a[200:203],v[196:199],a[68:71]
v_exp_f32 v253,v253
v_mfma_f32_16x16x32_bf16 a[72:75],a[208:211],v[196:199],a[72:75]
v_exp_f32 v254,v254
v_mfma_f32_16x16x32_bf16 a[72:75],a[212:215],v[212:215],a[72:75]
v_exp_f32 v255,v255
v_mfma_f32_16x16x32_bf16 a[76:79],a[220:223],v[212:215],a[76:79]
v_cvt_pk_bf16_f32 v246,v252,v253
v_cvt_pk_bf16_f32 v247,v254,v255
v_mfma_f32_16x16x32_bf16 a[76:79],a[216:219],v[196:199],a[76:79]
s_add_u32 m0,s49,s52
v_exp_f32 v132,v132
v_mfma_f32_16x16x32_bf16 a[80:83],a[224:227],v[196:199],a[80:83]
s_xor_b32 s49,s50,65536
v_exp_f32 v133,v133
v_mfma_f32_16x16x32_bf16 a[80:83],a[228:231],v[212:215],a[80:83]
v_exp_f32 v134,v134
v_mfma_f32_16x16x32_bf16 a[84:87],a[236:239],v[212:215],a[84:87]
buffer_load_dwordx4 v4,s[24:27],s47 offen nt lds
v_mfma_f32_16x16x32_bf16 a[84:87],a[232:235],v[196:199],a[84:87]
v_exp_f32 v135,v135
v_mfma_f32_16x16x32_bf16 a[88:91],a[240:243],v[196:199],a[88:91]
v_cvt_pk_bf16_f32 v132,v132,v133
v_cvt_pk_bf16_f32 v133,v134,v135
v_mfma_f32_16x16x32_bf16 a[88:91],a[244:247],v[212:215],a[88:91]
v_exp_f32 v140,v140
v_mfma_f32_16x16x32_bf16 a[92:95],a[252:255],v[212:215],a[92:95]
v_exp_f32 v141,v141
v_mfma_f32_16x16x32_bf16 a[92:95],a[248:251],v[196:199],a[92:95]
v_exp_f32 v142,v142
v_mfma_f32_16x16x32_bf16 v[56:59],v[28:31],v[196:199],v[56:59]
v_exp_f32 v143,v143
v_mfma_f32_16x16x32_bf16 v[56:59],v[28:31],v[212:215],v[56:59]
v_exp_f32 v148,v148
v_mfma_f32_16x16x32_bf16 v[164:167],a[128:131],v[80:83],v[36:39]
v_exp_f32 v149,v149
v_mfma_f32_16x16x32_bf16 v[164:167],a[132:135],v[84:87],v[164:167]
v_exp_f32 v150,v150
v_mfma_f32_16x16x32_bf16 v[164:167],a[136:139],v[88:91],v[164:167]
v_exp_f32 v151,v151
v_mfma_f32_16x16x32_bf16 v[164:167],a[140:143],v[92:95],v[164:167]
v_cvt_pk_bf16_f32 v134,v140,v141
v_cvt_pk_bf16_f32 v135,v142,v143
v_mfma_f32_16x16x32_bf16 v[172:175],a[156:159],v[92:95],v[36:39]
v_cvt_pk_bf16_f32 v148,v148,v149
v_cvt_pk_bf16_f32 v149,v150,v151
v_mfma_f32_16x16x32_bf16 v[172:175],a[152:155],v[88:91],v[172:175]
v_exp_f32 v156,v156
v_mfma_f32_16x16x32_bf16 v[172:175],a[148:151],v[84:87],v[172:175]
buffer_load_dwordx4 v5,s[24:27],s47 offen offset:1024 nt lds
v_mfma_f32_16x16x32_bf16 v[172:175],a[144:147],v[80:83],v[172:175]
v_exp_f32 v157,v157
v_mfma_f32_16x16x32_bf16 v[180:183],a[160:163],v[80:83],v[36:39]
v_exp_f32 v158,v158
v_mfma_f32_16x16x32_bf16 v[180:183],a[164:167],v[84:87],v[180:183]
v_exp_f32 v159,v159
v_mfma_f32_16x16x32_bf16 v[180:183],a[168:171],v[88:91],v[180:183]
v_cvt_pk_bf16_f32 v150,v156,v157
v_cvt_pk_bf16_f32 v151,v158,v159
v_mfma_f32_16x16x32_bf16 v[180:183],a[172:175],v[92:95],v[180:183]
v_exp_f32 v164,v164
v_mfma_f32_16x16x32_bf16 v[188:191],a[188:191],v[92:95],v[36:39]
v_exp_f32 v165,v165
v_mfma_f32_16x16x32_bf16 v[188:191],a[184:187],v[88:91],v[188:191]
v_exp_f32 v166,v166
v_mfma_f32_16x16x32_bf16 v[188:191],a[180:183],v[84:87],v[188:191]
v_exp_f32 v167,v167
v_mfma_f32_16x16x32_bf16 v[188:191],a[176:179],v[80:83],v[188:191]
v_cvt_pk_bf16_f32 v164,v164,v165
v_cvt_pk_bf16_f32 v165,v166,v167
v_mfma_f32_16x16x32_bf16 a[96:99],a[192:195],v[228:231],a[96:99]
ds_read_b64_tr_b16 a[192:193],v2
v_mfma_f32_16x16x32_bf16 a[96:99],a[196:199],v[244:247],a[96:99]
ds_read_b64_tr_b16 a[194:195],v2 offset:4096
v_mfma_f32_16x16x32_bf16 a[100:103],a[204:207],v[244:247],a[100:103]
ds_read_b64_tr_b16 a[196:197],v2 offset:8192
v_mfma_f32_16x16x32_bf16 a[100:103],a[200:203],v[228:231],a[100:103]
ds_read_b64_tr_b16 a[198:199],v2 offset:12288
v_mfma_f32_16x16x32_bf16 a[104:107],a[208:211],v[228:231],a[104:107]
buffer_load_dwordx4 v4,s[24:27],s47 offen offset:2048 nt lds
v_mfma_f32_16x16x32_bf16 a[104:107],a[212:215],v[244:247],a[104:107]
ds_read_b64_tr_b16 a[204:205],v3 offset:8192
v_mfma_f32_16x16x32_bf16 a[108:111],a[220:223],v[244:247],a[108:111]
ds_read_b64_tr_b16 a[206:207],v3 offset:12288
v_mfma_f32_16x16x32_bf16 a[108:111],a[216:219],v[228:231],a[108:111]
ds_read_b64_tr_b16 a[200:201],v3
v_mfma_f32_16x16x32_bf16 a[112:115],a[224:227],v[228:231],a[112:115]
ds_read_b64_tr_b16 a[202:203],v3 offset:4096
v_mfma_f32_16x16x32_bf16 a[112:115],a[228:231],v[244:247],a[112:115]
ds_read_b64_tr_b16 a[208:209],v2 offset:256
v_mfma_f32_16x16x32_bf16 a[116:119],a[236:239],v[244:247],a[116:119]
ds_read_b64_tr_b16 a[210:211],v2 offset:4352
v_mfma_f32_16x16x32_bf16 a[116:119],a[232:235],v[228:231],a[116:119]
ds_read_b64_tr_b16 a[212:213],v2 offset:8448
v_mfma_f32_16x16x32_bf16 a[120:123],a[240:243],v[228:231],a[120:123]
ds_read_b64_tr_b16 a[214:215],v2 offset:12544
v_mfma_f32_16x16x32_bf16 a[120:123],a[244:247],v[244:247],a[120:123]
ds_read_b64_tr_b16 a[220:221],v3 offset:8448
v_mfma_f32_16x16x32_bf16 a[124:127],a[252:255],v[244:247],a[124:127]
ds_read_b64_tr_b16 a[222:223],v3 offset:12544
v_mfma_f32_16x16x32_bf16 a[124:127],a[248:251],v[228:231],a[124:127]
ds_read_b64_tr_b16 a[216:217],v3 offset:256
v_mfma_f32_16x16x32_bf16 v[60:63],v[28:31],v[228:231],v[60:63]
ds_read_b64_tr_b16 a[218:219],v3 offset:4352
v_mfma_f32_16x16x32_bf16 v[60:63],v[28:31],v[244:247],v[60:63]
ds_read_b64_tr_b16 a[224:225],v2 offset:512
v_mfma_f32_16x16x32_bf16 v[196:199],a[128:131],v[96:99],v[40:43]
buffer_load_dwordx4 v5,s[24:27],s47 offen offset:3072 nt lds
v_mfma_f32_16x16x32_bf16 v[196:199],a[132:135],v[100:103],v[196:199]
s_add_u32 s47,s47,16384
s_and_b32 s47,s47,0x3ffff
ds_read_b64_tr_b16 a[226:227],v2 offset:4608
v_mfma_f32_16x16x32_bf16 v[196:199],a[136:139],v[104:107],v[196:199]
s_or_b32 s47,s47,s46
ds_read_b64_tr_b16 a[228:229],v2 offset:8704
v_mfma_f32_16x16x32_bf16 v[196:199],a[140:143],v[108:111],v[196:199]
ds_read_b64_tr_b16 a[230:231],v2 offset:12800
v_mfma_f32_16x16x32_bf16 v[204:207],a[156:159],v[108:111],v[40:43]
ds_read_b64_tr_b16 a[236:237],v3 offset:8704
v_mfma_f32_16x16x32_bf16 v[204:207],a[152:155],v[104:107],v[204:207]
ds_read_b64_tr_b16 a[238:239],v3 offset:12800
v_mfma_f32_16x16x32_bf16 v[204:207],a[148:151],v[100:103],v[204:207]
ds_read_b64_tr_b16 a[232:233],v3 offset:512
v_mfma_f32_16x16x32_bf16 v[204:207],a[144:147],v[96:99],v[204:207]
ds_read_b64_tr_b16 a[234:235],v3 offset:4608
v_mfma_f32_16x16x32_bf16 v[212:215],a[160:163],v[96:99],v[40:43]
ds_read_b64_tr_b16 a[240:241],v2 offset:768
v_mfma_f32_16x16x32_bf16 v[212:215],a[164:167],v[100:103],v[212:215]
ds_read_b64_tr_b16 a[242:243],v2 offset:4864
v_mfma_f32_16x16x32_bf16 v[212:215],a[168:171],v[104:107],v[212:215]
ds_read_b64_tr_b16 a[244:245],v2 offset:8960
v_mfma_f32_16x16x32_bf16 v[212:215],a[172:175],v[108:111],v[212:215]
ds_read_b64_tr_b16 a[246:247],v2 offset:13056
v_mfma_f32_16x16x32_bf16 v[220:223],a[188:191],v[108:111],v[40:43]
ds_read_b64_tr_b16 a[252:253],v3 offset:8960
v_mfma_f32_16x16x32_bf16 v[220:223],a[184:187],v[104:107],v[220:223]
ds_read_b64_tr_b16 a[254:255],v3 offset:13056
v_mfma_f32_16x16x32_bf16 v[220:223],a[180:183],v[100:103],v[220:223]
ds_read_b64_tr_b16 a[248:249],v3 offset:768
v_mfma_f32_16x16x32_bf16 v[220:223],a[176:179],v[96:99],v[220:223]
s_waitcnt lgkmcnt(7)
ds_read_b64_tr_b16 a[250:251],v3 offset:4864
v_mfma_f32_16x16x32_bf16 a[0:3],a[192:195],v[132:135],a[0:3]
buffer_load_dword v26,v24,s[88:91],s93 offen
v_mfma_f32_16x16x32_bf16 a[0:3],a[196:199],v[148:151],a[0:3]
v_exp_f32 v180,v180
v_mfma_f32_16x16x32_bf16 a[4:7],a[204:207],v[148:151],a[4:7]
v_exp_f32 v181,v181
v_mfma_f32_16x16x32_bf16 a[4:7],a[200:203],v[132:135],a[4:7]
v_exp_f32 v182,v182
v_mfma_f32_16x16x32_bf16 a[8:11],a[208:211],v[132:135],a[8:11]
s_memrealtime s[76:77]
v_exp_f32 v183,v183
v_mfma_f32_16x16x32_bf16 a[8:11],a[212:215],v[148:151],a[8:11]
v_exp_f32 v172,v172
v_mfma_f32_16x16x32_bf16 a[12:15],a[220:223],v[148:151],a[12:15]
v_exp_f32 v173,v173
v_mfma_f32_16x16x32_bf16 a[12:15],a[216:219],v[132:135],a[12:15]
v_exp_f32 v174,v174
v_mfma_f32_16x16x32_bf16 a[16:19],a[224:227],v[132:135],a[16:19]
s_bitcmp1_b32 s45,0
s_cbranch_scc0 L_t310_%=
s_mov_b32 m0,s80
s_add_u32 s80,s80,4096
buffer_load_dwordx4 v25,s[20:23],s83 offen nt lds
s_add_u32 s83,s83,4096
L_t310_%=:
v_mfma_f32_16x16x32_bf16 a[16:19],a[228:231],v[148:151],a[16:19]
v_exp_f32 v175,v175
v_mfma_f32_16x16x32_bf16 a[20:23],a[236:239],v[148:151],a[20:23]
v_cvt_pk_bf16_f32 v180,v180,v181
v_cvt_pk_bf16_f32 v181,v182,v183
v_mfma_f32_16x16x32_bf16 a[20:23],a[232:235],v[132:135],a[20:23]
s_waitcnt lgkmcnt(0)
v_exp_f32 v188,v188
v_mfma_f32_16x16x32_bf16 a[24:27],a[240:243],v[132:135],a[24:27]
s_sub_u32 s79,s78,s76
v_exp_f32 v189,v189
v_mfma_f32_16x16x32_bf16 a[24:27],a[244:247],v[148:151],a[24:27]
s_add_u32 s78,s76,s60
v_exp_f32 v190,v190
v_mfma_f32_16x16x32_bf16 a[28:31],a[252:255],v[148:151],a[28:31]
s_cmp_gt_i32 s79,0
s_cbranch_scc1 L_ps3_%=
L_pr3_%=:
v_cvt_pk_bf16_f32 v166,v172,v173
v_mfma_f32_16x16x32_bf16 a[28:31],a[248:251],v[132:135],a[28:31]
v_exp_f32 v191,v191
v_mfma_f32_16x16x32_bf16 v[48:51],v[28:31],v[132:135],v[48:51]
v_cvt_pk_bf16_f32 v167,v174,v175
v_cvt_pk_bf16_f32 v182,v188,v189
v_cvt_pk_bf16_f32 v183,v190,v191
s_cmp_lg_u32 s53,0
v_mfma_f32_16x16x32_bf16 v[48:51],v[28:31],v[148:151],v[48:51]
s_cbranch_scc1 L_att0_%=
L_att0x_%=:
v_mfma_f32_16x16x32_bf16 v[228:231],a[128:131],v[112:115],v[44:47]
s_waitcnt vmcnt(10)
s_add_u32 s54,s54,16384
v_exp_f32 v196,v196
v_mfma_f32_16x16x32_bf16 v[228:231],a[132:135],v[116:119],v[228:231]
s_barrier
s_add_u32 m0,0,s51
ds_read_b128 a[128:131],v1
v_mfma_f32_16x16x32_bf16 v[228:231],a[136:139],v[120:123],v[228:231]
s_and_b32 s46,s54,0xfffc0000
ds_read_b128 a[132:135],v1 offset:256
v_and_b32 v18,48,v6
v_mfma_f32_16x16x32_bf16 v[228:231],a[140:143],v[124:127],v[228:231]
s_add_u32 s54,s54,16384
ds_read_b128 a[136:139],v1 offset:512
v_lshrrev_b32 v19,4,v6
v_mfma_f32_16x16x32_bf16 v[236:239],a[156:159],v[124:127],v[44:47]
s_mul_i32 s83,s41,5
buffer_load_dwordx4 v4,s[20:23],s47 offen nt lds
v_mfma_f32_16x16x32_bf16 v[236:239],a[152:155],v[120:123],v[236:239]
s_lshl_b32 s44,s40,5
ds_read_b128 a[140:143],v1 offset:768
v_lshlrev_b32 v18,2,v18
v_mfma_f32_16x16x32_bf16 v[236:239],a[148:151],v[116:119],v[236:239]
s_add_u32 s83,s83,s42
ds_read_b128 a[156:159],v1 offset:4864
v_and_b32 v19,48,v19
v_mfma_f32_16x16x32_bf16 v[236:239],a[144:147],v[112:115],v[236:239]
)A"
R"A(s_add_u32 s84,s55,0
ds_read_b128 a[152:155],v1 offset:4608
v_add_u32 v18,v18,v19
v_mfma_f32_16x16x32_bf16 v[244:247],a[160:163],v[112:115],v[44:47]
s_add_u32 s85,s55,4096
ds_read_b128 a[148:151],v1 offset:4352
v_add_u32 v18,98304,v18
v_mfma_f32_16x16x32_bf16 v[244:247],a[164:167],v[116:119],v[244:247]
s_add_u32 s86,s55,8192
ds_read_b128 a[144:147],v1 offset:4096
v_add_u32 v2,s48,v2
v_mfma_f32_16x16x32_bf16 v[244:247],a[168:171],v[120:123],v[244:247]
s_add_u32 s87,s55,12288
ds_read_b128 a[160:163],v1 offset:8192
v_add_u32 v3,s48,v3
v_mfma_f32_16x16x32_bf16 v[244:247],a[172:175],v[124:127],v[244:247]
s_mov_b32 s48,32768
s_and_b32 s44,s44,32
ds_read_b128 a[164:167],v1 offset:8448
v_mfma_f32_16x16x32_bf16 v[252:255],a[188:191],v[124:127],v[44:47]
s_add_u32 s83,s83,15
ds_read_b128 a[168:171],v1 offset:8704
v_subrev_u32 v25,s51,v4
v_mfma_f32_16x16x32_bf16 v[252:255],a[184:187],v[120:123],v[252:255]
s_max_i32 s84,s84,s18
s_max_i32 s85,s85,s19
ds_read_b128 a[172:175],v1 offset:8960
v_mfma_f32_16x16x32_bf16 v[252:255],a[180:183],v[116:119],v[252:255]
s_max_i32 s86,s86,s100
s_max_i32 s87,s87,s101
ds_read_b128 a[188:191],v1 offset:13056
v_mfma_f32_16x16x32_bf16 v[252:255],a[176:179],v[112:115],v[252:255]
s_lshl_b32 s80,s40,10
ds_read_b128 a[184:187],v1 offset:12800
v_xor_b32 v25,s44,v25
v_mfma_f32_16x16x32_bf16 a[32:35],a[192:195],v[164:167],a[32:35]
s_and_b32 s83,s83,15
ds_read_b128 a[180:183],v1 offset:12544
v_add_u32 v25,s80,v25
v_mfma_f32_16x16x32_bf16 a[32:35],a[196:199],v[180:183],a[32:35]
s_add_u32 s83,s83,16
ds_read_b128 a[176:179],v1 offset:12288
v_add_u32 v1,s48,v1
v_mfma_f32_16x16x32_bf16 a[36:39],a[204:207],v[180:183],a[36:39]
s_mov_b32 s50,32768
buffer_load_dwordx4 v4,s[20:23],s47 offen offset:1024 nt lds
v_mfma_f32_16x16x32_bf16 a[36:39],a[200:203],v[164:167],a[36:39]
s_mov_b32 s49,98304
v_exp_f32 v197,v197
v_mfma_f32_16x16x32_bf16 a[40:43],a[208:211],v[164:167],a[40:43]
s_mov_b32 s45,0x2a8
v_exp_f32 v198,v198
v_mfma_f32_16x16x32_bf16 a[40:43],a[212:215],v[180:183],a[40:43]
s_add_u32 s80,s80,147456
v_exp_f32 v199,v199
v_mfma_f32_16x16x32_bf16 a[44:47],a[220:223],v[180:183],a[44:47]
s_lshl_b32 s83,s83,14
v_cvt_pk_bf16_f32 v196,v196,v197
v_cvt_pk_bf16_f32 v197,v198,v199
v_mfma_f32_16x16x32_bf16 a[44:47],a[216:219],v[164:167],a[44:47]
s_add_u32 s93,s92,L_endseg_%=-L_pc_%=
v_exp_f32 v204,v204
v_mfma_f32_16x16x32_bf16 a[48:51],a[224:227],v[164:167],a[48:51]
v_exp_f32 v205,v205
v_mfma_f32_16x16x32_bf16 a[48:51],a[228:231],v[180:183],a[48:51]
v_exp_f32 v206,v206
v_mfma_f32_16x16x32_bf16 a[52:55],a[236:239],v[180:183],a[52:55]
v_exp_f32 v207,v207
v_mfma_f32_16x16x32_bf16 a[52:55],a[232:235],v[164:167],a[52:55]
v_exp_f32 v212,v212
v_mfma_f32_16x16x32_bf16 a[56:59],a[240:243],v[164:167],a[56:59]
v_exp_f32 v213,v213
v_mfma_f32_16x16x32_bf16 a[56:59],a[244:247],v[180:183],a[56:59]
v_exp_f32 v214,v214
v_mfma_f32_16x16x32_bf16 a[60:63],a[252:255],v[180:183],a[60:63]
v_exp_f32 v215,v215
v_mfma_f32_16x16x32_bf16 a[60:63],a[248:251],v[164:167],a[60:63]
v_cvt_pk_bf16_f32 v198,v204,v205
v_cvt_pk_bf16_f32 v199,v206,v207
v_mfma_f32_16x16x32_bf16 v[52:55],v[28:31],v[164:167],v[52:55]
buffer_load_dwordx4 v5,s[20:23],s47 offen offset:2048 nt lds
v_mfma_f32_16x16x32_bf16 v[52:55],v[28:31],v[180:183],v[52:55]
s_waitcnt lgkmcnt(0)
v_cvt_pk_bf16_f32 v212,v212,v213
v_cvt_pk_bf16_f32 v213,v214,v215
v_mfma_f32_16x16x32_bf16 v[132:135],a[128:131],v[64:67],v[32:35]
v_exp_f32 v220,v220
v_mfma_f32_16x16x32_bf16 v[132:135],a[132:135],v[68:71],v[132:135]
v_exp_f32 v221,v221
v_mfma_f32_16x16x32_bf16 v[132:135],a[136:139],v[72:75],v[132:135]
v_exp_f32 v222,v222
v_mfma_f32_16x16x32_bf16 v[132:135],a[140:143],v[76:79],v[132:135]
v_exp_f32 v223,v223
v_mfma_f32_16x16x32_bf16 v[140:143],a[156:159],v[76:79],v[32:35]
v_cvt_pk_bf16_f32 v214,v220,v221
v_cvt_pk_bf16_f32 v215,v222,v223
v_mfma_f32_16x16x32_bf16 v[140:143],a[152:155],v[72:75],v[140:143]
v_exp_f32 v228,v228
v_mfma_f32_16x16x32_bf16 v[140:143],a[148:151],v[68:71],v[140:143]
v_exp_f32 v229,v229
v_mfma_f32_16x16x32_bf16 v[140:143],a[144:147],v[64:67],v[140:143]
v_exp_f32 v230,v230
v_mfma_f32_16x16x32_bf16 v[148:151],a[160:163],v[64:67],v[32:35]
v_exp_f32 v231,v231
v_mfma_f32_16x16x32_bf16 v[148:151],a[164:167],v[68:71],v[148:151]
v_cvt_pk_bf16_f32 v228,v228,v229
v_cvt_pk_bf16_f32 v229,v230,v231
v_mfma_f32_16x16x32_bf16 v[148:151],a[168:171],v[72:75],v[148:151]
v_exp_f32 v236,v236
v_mfma_f32_16x16x32_bf16 v[148:151],a[172:175],v[76:79],v[148:151]
v_exp_f32 v237,v237
v_mfma_f32_16x16x32_bf16 v[156:159],a[188:191],v[76:79],v[32:35]
buffer_load_dwordx4 v5,s[20:23],s47 offen offset:3072 nt lds
v_mfma_f32_16x16x32_bf16 v[156:159],a[184:187],v[72:75],v[156:159]
v_exp_f32 v238,v238
v_mfma_f32_16x16x32_bf16 v[156:159],a[180:183],v[68:71],v[156:159]
v_exp_f32 v239,v239
v_mfma_f32_16x16x32_bf16 v[156:159],a[176:179],v[64:67],v[156:159]
v_exp_f32 v244,v244
v_mfma_f32_16x16x32_bf16 a[64:67],a[192:195],v[196:199],a[64:67]
v_exp_f32 v245,v245
v_mfma_f32_16x16x32_bf16 a[64:67],a[196:199],v[212:215],a[64:67]
v_exp_f32 v246,v246
v_mfma_f32_16x16x32_bf16 a[68:71],a[204:207],v[212:215],a[68:71]
v_exp_f32 v247,v247
v_mfma_f32_16x16x32_bf16 a[68:71],a[200:203],v[196:199],a[68:71]
v_cvt_pk_bf16_f32 v230,v236,v237
v_cvt_pk_bf16_f32 v231,v238,v239
v_mfma_f32_16x16x32_bf16 a[72:75],a[208:211],v[196:199],a[72:75]
v_cvt_pk_bf16_f32 v244,v244,v245
v_cvt_pk_bf16_f32 v245,v246,v247
v_mfma_f32_16x16x32_bf16 a[72:75],a[212:215],v[212:215],a[72:75]
v_exp_f32 v252,v252
v_mfma_f32_16x16x32_bf16 a[76:79],a[220:223],v[212:215],a[76:79]
v_exp_f32 v253,v253
v_mfma_f32_16x16x32_bf16 a[76:79],a[216:219],v[196:199],a[76:79]
s_add_u32 m0,0,s52
v_exp_f32 v254,v254
v_mfma_f32_16x16x32_bf16 a[80:83],a[224:227],v[196:199],a[80:83]
v_exp_f32 v255,v255
v_mfma_f32_16x16x32_bf16 a[80:83],a[228:231],v[212:215],a[80:83]
v_cvt_pk_bf16_f32 v246,v252,v253
v_cvt_pk_bf16_f32 v247,v254,v255
v_mfma_f32_16x16x32_bf16 a[84:87],a[236:239],v[212:215],a[84:87]
buffer_load_dwordx4 v4,s[24:27],s47 offen nt lds
v_mfma_f32_16x16x32_bf16 a[84:87],a[232:235],v[196:199],a[84:87]
v_exp_f32 v132,v132
v_mfma_f32_16x16x32_bf16 a[88:91],a[240:243],v[196:199],a[88:91]
v_exp_f32 v133,v133
v_mfma_f32_16x16x32_bf16 a[88:91],a[244:247],v[212:215],a[88:91]
v_exp_f32 v134,v134
v_mfma_f32_16x16x32_bf16 a[92:95],a[252:255],v[212:215],a[92:95]
v_exp_f32 v135,v135
v_mfma_f32_16x16x32_bf16 a[92:95],a[248:251],v[196:199],a[92:95]
v_cvt_pk_bf16_f32 v132,v132,v133
v_cvt_pk_bf16_f32 v133,v134,v135
v_mfma_f32_16x16x32_bf16 v[56:59],v[28:31],v[196:199],v[56:59]
v_exp_f32 v140,v140
v_mfma_f32_16x16x32_bf16 v[56:59],v[28:31],v[212:215],v[56:59]
v_exp_f32 v141,v141
v_mfma_f32_16x16x32_bf16 v[164:167],a[128:131],v[80:83],v[36:39]
v_exp_f32 v142,v142
v_mfma_f32_16x16x32_bf16 v[164:167],a[132:135],v[84:87],v[164:167]
v_exp_f32 v143,v143
v_mfma_f32_16x16x32_bf16 v[164:167],a[136:139],v[88:91],v[164:167]
v_exp_f32 v148,v148
v_mfma_f32_16x16x32_bf16 v[164:167],a[140:143],v[92:95],v[164:167]
v_exp_f32 v149,v149
v_mfma_f32_16x16x32_bf16 v[172:175],a[156:159],v[92:95],v[36:39]
v_exp_f32 v150,v150
v_mfma_f32_16x16x32_bf16 v[172:175],a[152:155],v[88:91],v[172:175]
v_exp_f32 v151,v151
v_mfma_f32_16x16x32_bf16 v[172:175],a[148:151],v[84:87],v[172:175]
buffer_load_dwordx4 v5,s[24:27],s47 offen offset:1024 nt lds
v_mfma_f32_16x16x32_bf16 v[172:175],a[144:147],v[80:83],v[172:175]
v_cvt_pk_bf16_f32 v134,v140,v141
v_cvt_pk_bf16_f32 v135,v142,v143
v_mfma_f32_16x16x32_bf16 v[180:183],a[160:163],v[80:83],v[36:39]
v_cvt_pk_bf16_f32 v148,v148,v149
v_cvt_pk_bf16_f32 v149,v150,v151
v_mfma_f32_16x16x32_bf16 v[180:183],a[164:167],v[84:87],v[180:183]
v_exp_f32 v156,v156
v_mfma_f32_16x16x32_bf16 v[180:183],a[168:171],v[88:91],v[180:183]
v_exp_f32 v157,v157
v_mfma_f32_16x16x32_bf16 v[180:183],a[172:175],v[92:95],v[180:183]
v_exp_f32 v158,v158
v_mfma_f32_16x16x32_bf16 v[188:191],a[188:191],v[92:95],v[36:39]
v_exp_f32 v159,v159
v_mfma_f32_16x16x32_bf16 v[188:191],a[184:187],v[88:91],v[188:191]
v_cvt_pk_bf16_f32 v150,v156,v157
v_cvt_pk_bf16_f32 v151,v158,v159
v_mfma_f32_16x16x32_bf16 v[188:191],a[180:183],v[84:87],v[188:191]
s_waitcnt vmcnt(14)
v_exp_f32 v164,v164
v_mfma_f32_16x16x32_bf16 v[188:191],a[176:179],v[80:83],v[188:191]
s_barrier
ds_read_b128 v[80:83],v18
v_mfma_f32_16x16x32_bf16 a[96:99],a[192:195],v[228:231],a[96:99]
ds_read_b128 v[84:87],v18 offset:256
v_mfma_f32_16x16x32_bf16 a[96:99],a[196:199],v[244:247],a[96:99]
ds_read_b128 v[88:91],v18 offset:512
v_mfma_f32_16x16x32_bf16 a[100:103],a[204:207],v[244:247],a[100:103]
ds_read_b128 v[92:95],v18 offset:768
v_mfma_f32_16x16x32_bf16 a[100:103],a[200:203],v[228:231],a[100:103]
buffer_load_dwordx4 v4,s[24:27],s47 offen offset:2048 nt lds
v_mfma_f32_16x16x32_bf16 a[104:107],a[208:211],v[228:231],a[104:107]
ds_read_b64_tr_b16 a[192:193],v2
ds_read_b64_tr_b16 a[194:195],v2 offset:4096
v_mfma_f32_16x16x32_bf16 a[104:107],a[212:215],v[244:247],a[104:107]
ds_read_b64_tr_b16 a[196:197],v2 offset:8192
ds_read_b64_tr_b16 a[198:199],v2 offset:12288
v_mfma_f32_16x16x32_bf16 a[108:111],a[220:223],v[244:247],a[108:111]
ds_read_b64_tr_b16 a[204:205],v3 offset:8192
ds_read_b64_tr_b16 a[206:207],v3 offset:12288
v_mfma_f32_16x16x32_bf16 a[108:111],a[216:219],v[228:231],a[108:111]
ds_read_b64_tr_b16 a[200:201],v3
ds_read_b64_tr_b16 a[202:203],v3 offset:4096
v_mfma_f32_16x16x32_bf16 a[112:115],a[224:227],v[228:231],a[112:115]
ds_read_b64_tr_b16 a[208:209],v2 offset:256
ds_read_b64_tr_b16 a[210:211],v2 offset:4352
v_mfma_f32_16x16x32_bf16 a[112:115],a[228:231],v[244:247],a[112:115]
ds_read_b64_tr_b16 a[212:213],v2 offset:8448
ds_read_b64_tr_b16 a[214:215],v2 offset:12544
v_mfma_f32_16x16x32_bf16 a[116:119],a[236:239],v[244:247],a[116:119]
ds_read_b64_tr_b16 a[220:221],v3 offset:8448
ds_read_b64_tr_b16 a[222:223],v3 offset:12544
v_mfma_f32_16x16x32_bf16 a[116:119],a[232:235],v[228:231],a[116:119]
ds_read_b64_tr_b16 a[216:217],v3 offset:256
ds_read_b64_tr_b16 a[218:219],v3 offset:4352
v_mfma_f32_16x16x32_bf16 a[120:123],a[240:243],v[228:231],a[120:123]
ds_read_b64_tr_b16 a[224:225],v2 offset:512
ds_read_b64_tr_b16 a[226:227],v2 offset:4608
v_mfma_f32_16x16x32_bf16 a[120:123],a[244:247],v[244:247],a[120:123]
ds_read_b64_tr_b16 a[228:229],v2 offset:8704
ds_read_b64_tr_b16 a[230:231],v2 offset:12800
v_mfma_f32_16x16x32_bf16 a[124:127],a[252:255],v[244:247],a[124:127]
ds_read_b64_tr_b16 a[236:237],v3 offset:8704
ds_read_b64_tr_b16 a[238:239],v3 offset:12800
v_mfma_f32_16x16x32_bf16 a[124:127],a[248:251],v[228:231],a[124:127]
ds_read_b64_tr_b16 a[232:233],v3 offset:512
ds_read_b64_tr_b16 a[234:235],v3 offset:4608
v_mfma_f32_16x16x32_bf16 v[60:63],v[28:31],v[228:231],v[60:63]
ds_read_b64_tr_b16 a[240:241],v2 offset:768
ds_read_b64_tr_b16 a[242:243],v2 offset:4864
v_mfma_f32_16x16x32_bf16 v[60:63],v[28:31],v[244:247],v[60:63]
buffer_load_dwordx4 v5,s[24:27],s47 offen offset:3072 nt lds
v_mfma_f32_16x16x32_bf16 v[196:199],a[128:131],v[96:99],v[40:43]
s_add_u32 s47,s47,16384
ds_read_b64_tr_b16 a[244:245],v2 offset:8960
ds_read_b64_tr_b16 a[246:247],v2 offset:13056
v_mfma_f32_16x16x32_bf16 v[196:199],a[132:135],v[100:103],v[196:199]
s_and_b32 s47,s47,0x3ffff
ds_read_b64_tr_b16 a[252:253],v3 offset:8960
ds_read_b64_tr_b16 a[254:255],v3 offset:13056
v_mfma_f32_16x16x32_bf16 v[196:199],a[136:139],v[104:107],v[196:199]
s_or_b32 s47,s47,s46
ds_read_b64_tr_b16 a[248:249],v3 offset:768
ds_read_b64_tr_b16 a[250:251],v3 offset:4864
v_mfma_f32_16x16x32_bf16 v[196:199],a[140:143],v[108:111],v[196:199]
s_and_b32 s46,s54,0xfffc0000
v_exp_f32 v165,v165
v_mfma_f32_16x16x32_bf16 v[204:207],a[156:159],v[108:111],v[40:43]
s_add_u32 s54,s54,16384
v_exp_f32 v166,v166
v_mfma_f32_16x16x32_bf16 v[204:207],a[152:155],v[104:107],v[204:207]
v_exp_f32 v167,v167
v_mfma_f32_16x16x32_bf16 v[204:207],a[148:151],v[100:103],v[204:207]
v_cvt_pk_bf16_f32 v164,v164,v165
v_cvt_pk_bf16_f32 v165,v166,v167
v_mfma_f32_16x16x32_bf16 v[204:207],a[144:147],v[96:99],v[204:207]
v_exp_f32 v172,v172
v_mfma_f32_16x16x32_bf16 v[212:215],a[160:163],v[96:99],v[40:43]
v_exp_f32 v173,v173
v_mfma_f32_16x16x32_bf16 v[212:215],a[164:167],v[100:103],v[212:215]
)A"
R"A(v_exp_f32 v174,v174
v_mfma_f32_16x16x32_bf16 v[212:215],a[168:171],v[104:107],v[212:215]
v_exp_f32 v175,v175
v_mfma_f32_16x16x32_bf16 v[212:215],a[172:175],v[108:111],v[212:215]
v_exp_f32 v180,v180
v_mfma_f32_16x16x32_bf16 v[220:223],a[188:191],v[108:111],v[40:43]
v_exp_f32 v181,v181
v_mfma_f32_16x16x32_bf16 v[220:223],a[184:187],v[104:107],v[220:223]
v_exp_f32 v182,v182
v_mfma_f32_16x16x32_bf16 v[220:223],a[180:183],v[100:103],v[220:223]
v_exp_f32 v183,v183
v_mfma_f32_16x16x32_bf16 v[220:223],a[176:179],v[96:99],v[220:223]
s_waitcnt lgkmcnt(0)
v_cvt_pk_bf16_f32 v166,v172,v173
v_cvt_pk_bf16_f32 v167,v174,v175
v_mfma_f32_16x16x32_bf16 v[32:35],v[80:83],v[128:131],0
v_cvt_pk_bf16_f32 v180,v180,v181
v_cvt_pk_bf16_f32 v181,v182,v183
v_mfma_f32_16x16x32_bf16 v[32:35],v[84:87],v[160:163],v[32:35]
v_exp_f32 v188,v188
v_mfma_f32_16x16x32_bf16 v[32:35],v[88:91],v[192:195],v[32:35]
v_exp_f32 v189,v189
v_mfma_f32_16x16x32_bf16 v[32:35],v[92:95],v[224:227],v[32:35]
v_exp_f32 v190,v190
v_mfma_f32_16x16x32_bf16 v[36:39],v[80:83],v[136:139],0
v_exp_f32 v191,v191
v_mfma_f32_16x16x32_bf16 v[36:39],v[84:87],v[168:171],v[36:39]
v_cvt_pk_bf16_f32 v182,v188,v189
v_cvt_pk_bf16_f32 v183,v190,v191
v_mfma_f32_16x16x32_bf16 v[36:39],v[88:91],v[200:203],v[36:39]
v_exp_f32 v196,v196
v_mfma_f32_16x16x32_bf16 v[36:39],v[92:95],v[232:235],v[36:39]
v_exp_f32 v197,v197
v_mfma_f32_16x16x32_bf16 v[40:43],v[80:83],v[144:147],0
v_exp_f32 v198,v198
v_mfma_f32_16x16x32_bf16 v[40:43],v[84:87],v[176:179],v[40:43]
v_max3_f32 v32,v32,v33,v34
v_max3_f32 v32,v32,v35,s18
v_mfma_f32_16x16x32_bf16 v[40:43],v[88:91],v[208:211],v[40:43]
v_sub_f32 v35,0,v32
v_sub_f32 v34,0,v32
v_mfma_f32_16x16x32_bf16 v[40:43],v[92:95],v[240:243],v[40:43]
v_sub_f32 v33,0,v32
v_sub_f32 v32,0,v32
v_mfma_f32_16x16x32_bf16 a[0:3],a[192:195],v[132:135],a[0:3]
v_exp_f32 v199,v199
v_mfma_f32_16x16x32_bf16 a[0:3],a[196:199],v[148:151],a[0:3]
v_cvt_pk_bf16_f32 v196,v196,v197
v_cvt_pk_bf16_f32 v197,v198,v199
v_mfma_f32_16x16x32_bf16 a[4:7],a[204:207],v[148:151],a[4:7]
v_exp_f32 v204,v204
v_mfma_f32_16x16x32_bf16 a[4:7],a[200:203],v[132:135],a[4:7]
v_exp_f32 v205,v205
v_mfma_f32_16x16x32_bf16 a[8:11],a[208:211],v[132:135],a[8:11]
s_memrealtime s[76:77]
v_exp_f32 v206,v206
v_mfma_f32_16x16x32_bf16 a[8:11],a[212:215],v[148:151],a[8:11]
v_exp_f32 v207,v207
v_mfma_f32_16x16x32_bf16 a[12:15],a[220:223],v[148:151],a[12:15]
v_exp_f32 v212,v212
v_mfma_f32_16x16x32_bf16 a[12:15],a[216:219],v[132:135],a[12:15]
v_exp_f32 v213,v213
v_mfma_f32_16x16x32_bf16 a[16:19],a[224:227],v[132:135],a[16:19]
v_exp_f32 v214,v214
v_mfma_f32_16x16x32_bf16 a[16:19],a[228:231],v[148:151],a[16:19]
v_exp_f32 v215,v215
v_mfma_f32_16x16x32_bf16 a[20:23],a[236:239],v[148:151],a[20:23]
v_cvt_pk_bf16_f32 v198,v204,v205
v_cvt_pk_bf16_f32 v199,v206,v207
v_mfma_f32_16x16x32_bf16 a[20:23],a[232:235],v[132:135],a[20:23]
s_waitcnt lgkmcnt(0)
v_cvt_pk_bf16_f32 v212,v212,v213
v_cvt_pk_bf16_f32 v213,v214,v215
v_mfma_f32_16x16x32_bf16 a[24:27],a[240:243],v[132:135],a[24:27]
s_sub_u32 s79,s78,s76
v_exp_f32 v220,v220
v_mfma_f32_16x16x32_bf16 a[24:27],a[244:247],v[148:151],a[24:27]
s_add_u32 s78,s76,s60
v_exp_f32 v221,v221
v_mfma_f32_16x16x32_bf16 a[28:31],a[252:255],v[148:151],a[28:31]
s_cmp_gt_i32 s79,0
s_cbranch_scc1 L_ps4_%=
L_pr4_%=:
v_cvt_pk_bf16_f32 v214,v220,v221
v_mfma_f32_16x16x32_bf16 a[28:31],a[248:251],v[132:135],a[28:31]
v_exp_f32 v222,v222
v_mfma_f32_16x16x32_bf16 v[48:51],v[28:31],v[132:135],v[48:51]
v_exp_f32 v223,v223
v_mfma_f32_16x16x32_bf16 v[48:51],v[28:31],v[148:151],v[48:51]
v_cvt_pk_bf16_f32 v215,v222,v223
v_max3_f32 v36,v36,v37,v38
v_mfma_f32_16x16x32_bf16 v[228:231],a[128:131],v[112:115],v[44:47]
s_waitcnt vmcnt(9)
v_max3_f32 v36,v36,v39,s19
v_sub_f32 v39,0,v36
v_mfma_f32_16x16x32_bf16 v[228:231],a[132:135],v[116:119],v[228:231]
s_barrier
s_add_u32 m0,32768,s51
ds_read_b128 a[128:131],v1
v_mfma_f32_16x16x32_bf16 v[228:231],a[136:139],v[120:123],v[228:231]
ds_read_b128 a[132:135],v1 offset:256
v_sub_f32 v38,0,v36
v_mfma_f32_16x16x32_bf16 v[228:231],a[140:143],v[124:127],v[228:231]
ds_read_b128 a[136:139],v1 offset:512
v_sub_f32 v37,0,v36
v_mfma_f32_16x16x32_bf16 v[236:239],a[156:159],v[124:127],v[44:47]
buffer_load_dwordx4 v4,s[20:23],s47 offen nt lds
v_mfma_f32_16x16x32_bf16 v[236:239],a[152:155],v[120:123],v[236:239]
ds_read_b128 a[140:143],v1 offset:768
v_sub_f32 v36,0,v36
v_mfma_f32_16x16x32_bf16 v[236:239],a[148:151],v[116:119],v[236:239]
ds_read_b128 a[156:159],v1 offset:4864
v_max3_f32 v40,v40,v41,v42
v_mfma_f32_16x16x32_bf16 v[236:239],a[144:147],v[112:115],v[236:239]
ds_read_b128 a[152:155],v1 offset:4608
v_max3_f32 v40,v40,v43,s100
v_mfma_f32_16x16x32_bf16 v[244:247],a[160:163],v[112:115],v[44:47]
ds_read_b128 a[148:151],v1 offset:4352
v_sub_f32 v43,0,v40
v_mfma_f32_16x16x32_bf16 v[244:247],a[164:167],v[116:119],v[244:247]
ds_read_b128 a[144:147],v1 offset:4096
v_sub_f32 v42,0,v40
v_mfma_f32_16x16x32_bf16 v[244:247],a[168:171],v[120:123],v[244:247]
ds_read_b128 a[160:163],v1 offset:8192
v_sub_f32 v41,0,v40
v_mfma_f32_16x16x32_bf16 v[244:247],a[172:175],v[124:127],v[244:247]
ds_read_b128 a[164:167],v1 offset:8448
v_sub_f32 v40,0,v40
v_mfma_f32_16x16x32_bf16 v[252:255],a[188:191],v[124:127],v[44:47]
ds_read_b128 a[168:171],v1 offset:8704
v_add_u32 v2,s48,v2
v_mfma_f32_16x16x32_bf16 v[252:255],a[184:187],v[120:123],v[252:255]
ds_read_b128 a[172:175],v1 offset:8960
v_add_u32 v3,s48,v3
v_mfma_f32_16x16x32_bf16 v[252:255],a[180:183],v[116:119],v[252:255]
s_mov_b32 s48,-98304
ds_read_b128 a[188:191],v1 offset:13056
v_max_f32 v20,0x00800000,v48
v_mfma_f32_16x16x32_bf16 v[252:255],a[176:179],v[112:115],v[252:255]
ds_read_b128 a[184:187],v1 offset:12800
v_accvgpr_read_b32 v64,a0
v_mfma_f32_16x16x32_bf16 v[44:47],v[80:83],v[152:155],0
ds_read_b128 a[180:183],v1 offset:12544
v_accvgpr_read_b32 v65,a1
v_mfma_f32_16x16x32_bf16 v[44:47],v[84:87],v[184:187],v[44:47]
ds_read_b128 a[176:179],v1 offset:12288
v_accvgpr_read_b32 v66,a2
v_mfma_f32_16x16x32_bf16 v[44:47],v[88:91],v[216:219],v[44:47]
buffer_load_dwordx4 v4,s[20:23],s47 offen offset:1024 nt lds
v_mfma_f32_16x16x32_bf16 v[44:47],v[92:95],v[248:251],v[44:47]
v_exp_f32 v228,v228
v_mfma_f32_16x16x32_bf16 a[32:35],a[192:195],v[164:167],a[32:35]
v_exp_f32 v229,v229
v_mfma_f32_16x16x32_bf16 a[32:35],a[196:199],v[180:183],a[32:35]
v_exp_f32 v230,v230
v_mfma_f32_16x16x32_bf16 a[36:39],a[204:207],v[180:183],a[36:39]
v_exp_f32 v231,v231
v_mfma_f32_16x16x32_bf16 a[36:39],a[200:203],v[164:167],a[36:39]
v_cvt_pk_bf16_f32 v228,v228,v229
v_cvt_pk_bf16_f32 v229,v230,v231
v_mfma_f32_16x16x32_bf16 a[40:43],a[208:211],v[164:167],a[40:43]
v_exp_f32 v236,v236
v_mfma_f32_16x16x32_bf16 a[40:43],a[212:215],v[180:183],a[40:43]
v_exp_f32 v237,v237
v_mfma_f32_16x16x32_bf16 a[44:47],a[220:223],v[180:183],a[44:47]
v_exp_f32 v238,v238
v_mfma_f32_16x16x32_bf16 a[44:47],a[216:219],v[164:167],a[44:47]
v_exp_f32 v239,v239
v_mfma_f32_16x16x32_bf16 a[48:51],a[224:227],v[164:167],a[48:51]
v_exp_f32 v244,v244
v_mfma_f32_16x16x32_bf16 a[48:51],a[228:231],v[180:183],a[48:51]
v_exp_f32 v245,v245
v_mfma_f32_16x16x32_bf16 a[52:55],a[236:239],v[180:183],a[52:55]
v_exp_f32 v246,v246
v_mfma_f32_16x16x32_bf16 a[52:55],a[232:235],v[164:167],a[52:55]
v_exp_f32 v247,v247
v_mfma_f32_16x16x32_bf16 a[56:59],a[240:243],v[164:167],a[56:59]
buffer_load_dwordx4 v5,s[20:23],s47 offen offset:2048 nt lds
v_mfma_f32_16x16x32_bf16 a[56:59],a[244:247],v[180:183],a[56:59]
v_cvt_pk_bf16_f32 v230,v236,v237
v_cvt_pk_bf16_f32 v231,v238,v239
v_mfma_f32_16x16x32_bf16 a[60:63],a[252:255],v[180:183],a[60:63]
v_cvt_pk_bf16_f32 v244,v244,v245
v_cvt_pk_bf16_f32 v245,v246,v247
v_mfma_f32_16x16x32_bf16 a[60:63],a[248:251],v[164:167],a[60:63]
v_exp_f32 v252,v252
v_mfma_f32_16x16x32_bf16 v[52:55],v[28:31],v[164:167],v[52:55]
v_exp_f32 v253,v253
v_mfma_f32_16x16x32_bf16 v[52:55],v[28:31],v[180:183],v[52:55]
s_waitcnt lgkmcnt(0)
v_exp_f32 v254,v254
v_mfma_f32_16x16x32_bf16 v[132:135],a[128:131],v[128:131],v[32:35]
v_exp_f32 v255,v255
v_mfma_f32_16x16x32_bf16 v[132:135],a[132:135],v[160:163],v[132:135]
v_cvt_pk_bf16_f32 v246,v252,v253
v_cvt_pk_bf16_f32 v247,v254,v255
v_mfma_f32_16x16x32_bf16 v[132:135],a[136:139],v[192:195],v[132:135]
v_rcp_f32 v20,v20
v_mfma_f32_16x16x32_bf16 v[132:135],a[140:143],v[224:227],v[132:135]
v_mul_f32 v64,v20,v64
v_mul_f32 v65,v20,v65
v_mfma_f32_16x16x32_bf16 v[140:143],a[156:159],v[224:227],v[32:35]
v_accvgpr_read_b32 v67,a3
v_cvt_pk_bf16_f32 v64,v64,v65
v_mfma_f32_16x16x32_bf16 v[140:143],a[152:155],v[192:195],v[140:143]
v_mul_f32 v66,v20,v66
v_mul_f32 v67,v20,v67
v_mfma_f32_16x16x32_bf16 v[140:143],a[148:151],v[160:163],v[140:143]
v_cvt_pk_bf16_f32 v65,v66,v67
v_accvgpr_read_b32 v66,a4
v_mfma_f32_16x16x32_bf16 v[140:143],a[144:147],v[128:131],v[140:143]
v_accvgpr_read_b32 v67,a5
v_accvgpr_read_b32 v12,a6
v_mfma_f32_16x16x32_bf16 v[148:151],a[160:163],v[128:131],v[32:35]
buffer_load_dwordx4 v5,s[20:23],s47 offen offset:3072 nt lds
v_mfma_f32_16x16x32_bf16 v[148:151],a[164:167],v[160:163],v[148:151]
v_exp_f32 v132,v132
v_mfma_f32_16x16x32_bf16 v[148:151],a[168:171],v[192:195],v[148:151]
v_exp_f32 v133,v133
v_mfma_f32_16x16x32_bf16 v[148:151],a[172:175],v[224:227],v[148:151]
v_exp_f32 v134,v134
v_mfma_f32_16x16x32_bf16 v[156:159],a[188:191],v[224:227],v[32:35]
v_exp_f32 v135,v135
v_mfma_f32_16x16x32_bf16 v[156:159],a[184:187],v[192:195],v[156:159]
v_cvt_pk_bf16_f32 v132,v132,v133
v_cvt_pk_bf16_f32 v133,v134,v135
v_mfma_f32_16x16x32_bf16 v[156:159],a[180:183],v[160:163],v[156:159]
v_exp_f32 v140,v140
v_mfma_f32_16x16x32_bf16 v[156:159],a[176:179],v[128:131],v[156:159]
v_exp_f32 v141,v141
v_mfma_f32_16x16x32_bf16 a[64:67],a[192:195],v[196:199],a[64:67]
v_exp_f32 v142,v142
v_mfma_f32_16x16x32_bf16 a[64:67],a[196:199],v[212:215],a[64:67]
v_exp_f32 v143,v143
v_mfma_f32_16x16x32_bf16 a[68:71],a[204:207],v[212:215],a[68:71]
v_exp_f32 v148,v148
v_mfma_f32_16x16x32_bf16 a[68:71],a[200:203],v[196:199],a[68:71]
s_add_u32 m0,32768,s52
v_exp_f32 v149,v149
v_mfma_f32_16x16x32_bf16 a[72:75],a[208:211],v[196:199],a[72:75]
v_exp_f32 v150,v150
v_mfma_f32_16x16x32_bf16 a[72:75],a[212:215],v[212:215],a[72:75]
v_exp_f32 v151,v151
v_mfma_f32_16x16x32_bf16 a[76:79],a[220:223],v[212:215],a[76:79]
buffer_load_dwordx4 v4,s[24:27],s47 offen nt lds
v_mfma_f32_16x16x32_bf16 a[76:79],a[216:219],v[196:199],a[76:79]
v_cvt_pk_bf16_f32 v134,v140,v141
v_cvt_pk_bf16_f32 v135,v142,v143
v_mfma_f32_16x16x32_bf16 a[80:83],a[224:227],v[196:199],a[80:83]
v_cvt_pk_bf16_f32 v148,v148,v149
v_cvt_pk_bf16_f32 v149,v150,v151
v_mfma_f32_16x16x32_bf16 a[80:83],a[228:231],v[212:215],a[80:83]
v_exp_f32 v156,v156
v_mfma_f32_16x16x32_bf16 a[84:87],a[236:239],v[212:215],a[84:87]
v_exp_f32 v157,v157
v_mfma_f32_16x16x32_bf16 a[84:87],a[232:235],v[196:199],a[84:87]
v_exp_f32 v158,v158
v_mfma_f32_16x16x32_bf16 a[88:91],a[240:243],v[196:199],a[88:91]
v_exp_f32 v159,v159
v_mfma_f32_16x16x32_bf16 a[88:91],a[244:247],v[212:215],a[88:91]
v_cvt_pk_bf16_f32 v150,v156,v157
v_cvt_pk_bf16_f32 v151,v158,v159
v_mfma_f32_16x16x32_bf16 a[92:95],a[252:255],v[212:215],a[92:95]
v_accvgpr_read_b32 v13,a7
v_mul_f32 v66,v20,v66
v_mfma_f32_16x16x32_bf16 a[92:95],a[248:251],v[196:199],a[92:95]
v_mul_f32 v67,v20,v67
v_mul_f32 v12,v20,v12
v_mfma_f32_16x16x32_bf16 v[56:59],v[28:31],v[196:199],v[56:59]
v_mul_f32 v13,v20,v13
v_cvt_pk_bf16_f32 v66,v66,v67
v_mfma_f32_16x16x32_bf16 v[56:59],v[28:31],v[212:215],v[56:59]
v_cvt_pk_bf16_f32 v67,v12,v13
v_max3_f32 v44,v44,v45,v46
v_mfma_f32_16x16x32_bf16 v[164:167],a[128:131],v[136:139],v[36:39]
v_accvgpr_read_b32 v68,a8
v_accvgpr_read_b32 v69,a9
v_accvgpr_read_b32 v70,a10
v_accvgpr_read_b32 v71,a11
v_accvgpr_read_b32 v12,a12
v_accvgpr_read_b32 v13,a13
v_accvgpr_read_b32 v14,a14
v_accvgpr_read_b32 v15,a15
v_pk_mul_f32 v[68:69],v[20:21],v[68:69] op_sel:[0,0] op_sel_hi:[0,1]
v_pk_mul_f32 v[70:71],v[20:21],v[70:71] op_sel:[0,0] op_sel_hi:[0,1]
v_pk_mul_f32 v[12:13],v[20:21],v[12:13] op_sel:[0,0] op_sel_hi:[0,1]
v_pk_mul_f32 v[14:15],v[20:21],v[14:15] op_sel:[0,0] op_sel_hi:[0,1]
v_cvt_pk_bf16_f32 v68,v68,v69
v_cvt_pk_bf16_f32 v69,v70,v71
)A"
R"A(v_cvt_pk_bf16_f32 v70,v12,v13
v_cvt_pk_bf16_f32 v71,v14,v15
v_mfma_f32_16x16x32_bf16 v[164:167],a[132:135],v[168:171],v[164:167]
v_accvgpr_read_b32 v72,a16
v_accvgpr_read_b32 v73,a17
v_accvgpr_read_b32 v74,a18
v_accvgpr_read_b32 v75,a19
v_accvgpr_read_b32 v12,a20
v_accvgpr_read_b32 v13,a21
v_accvgpr_read_b32 v14,a22
v_accvgpr_read_b32 v15,a23
v_pk_mul_f32 v[72:73],v[20:21],v[72:73] op_sel:[0,0] op_sel_hi:[0,1]
v_pk_mul_f32 v[74:75],v[20:21],v[74:75] op_sel:[0,0] op_sel_hi:[0,1]
v_pk_mul_f32 v[12:13],v[20:21],v[12:13] op_sel:[0,0] op_sel_hi:[0,1]
v_pk_mul_f32 v[14:15],v[20:21],v[14:15] op_sel:[0,0] op_sel_hi:[0,1]
v_cvt_pk_bf16_f32 v72,v72,v73
v_cvt_pk_bf16_f32 v73,v74,v75
v_cvt_pk_bf16_f32 v74,v12,v13
v_cvt_pk_bf16_f32 v75,v14,v15
v_mfma_f32_16x16x32_bf16 v[164:167],a[136:139],v[200:203],v[164:167]
buffer_load_dwordx4 v5,s[24:27],s47 offen offset:1024 nt lds
v_mfma_f32_16x16x32_bf16 v[164:167],a[140:143],v[232:235],v[164:167]
v_accvgpr_read_b32 v76,a24
v_accvgpr_read_b32 v77,a25
v_accvgpr_read_b32 v78,a26
v_accvgpr_read_b32 v79,a27
v_accvgpr_read_b32 v12,a28
v_accvgpr_read_b32 v13,a29
v_accvgpr_read_b32 v14,a30
v_accvgpr_read_b32 v15,a31
v_pk_mul_f32 v[76:77],v[20:21],v[76:77] op_sel:[0,0] op_sel_hi:[0,1]
v_pk_mul_f32 v[78:79],v[20:21],v[78:79] op_sel:[0,0] op_sel_hi:[0,1]
v_pk_mul_f32 v[12:13],v[20:21],v[12:13] op_sel:[0,0] op_sel_hi:[0,1]
v_pk_mul_f32 v[14:15],v[20:21],v[14:15] op_sel:[0,0] op_sel_hi:[0,1]
v_cvt_pk_bf16_f32 v76,v76,v77
v_cvt_pk_bf16_f32 v77,v78,v79
v_cvt_pk_bf16_f32 v78,v12,v13
v_cvt_pk_bf16_f32 v79,v14,v15
v_mfma_f32_16x16x32_bf16 v[172:175],a[156:159],v[232:235],v[36:39]
v_max3_f32 v44,v44,v47,s101
v_sub_f32 v47,0,v44
v_mfma_f32_16x16x32_bf16 v[172:175],a[152:155],v[200:203],v[172:175]
v_sub_f32 v46,0,v44
v_sub_f32 v45,0,v44
v_mfma_f32_16x16x32_bf16 v[172:175],a[148:151],v[168:171],v[172:175]
v_cmp_nlt_f32 s[56:57],v48,s63
v_sub_f32 v44,0,v44
v_mfma_f32_16x16x32_bf16 v[172:175],a[144:147],v[136:139],v[172:175]
v_max_f32 v21,0x00800000,v52
v_accvgpr_read_b32 v80,a32
v_mfma_f32_16x16x32_bf16 v[180:183],a[160:163],v[136:139],v[36:39]
v_rcp_f32 v21,v21
v_mfma_f32_16x16x32_bf16 v[180:183],a[164:167],v[168:171],v[180:183]
v_exp_f32 v164,v164
v_mfma_f32_16x16x32_bf16 v[180:183],a[168:171],v[200:203],v[180:183]
v_exp_f32 v165,v165
v_mfma_f32_16x16x32_bf16 v[180:183],a[172:175],v[232:235],v[180:183]
v_exp_f32 v166,v166
v_mfma_f32_16x16x32_bf16 v[188:191],a[188:191],v[232:235],v[36:39]
v_exp_f32 v167,v167
v_mfma_f32_16x16x32_bf16 v[188:191],a[184:187],v[200:203],v[188:191]
v_cvt_pk_bf16_f32 v164,v164,v165
v_cvt_pk_bf16_f32 v165,v166,v167
v_mfma_f32_16x16x32_bf16 v[188:191],a[180:183],v[168:171],v[188:191]
v_exp_f32 v172,v172
v_mfma_f32_16x16x32_bf16 v[188:191],a[176:179],v[136:139],v[188:191]
v_exp_f32 v173,v173
v_mfma_f32_16x16x32_bf16 a[96:99],a[192:195],v[228:231],a[96:99]
buffer_load_dwordx4 v4,s[24:27],s47 offen offset:2048 nt lds
v_mfma_f32_16x16x32_bf16 a[96:99],a[196:199],v[244:247],a[96:99]
ds_read_b64_tr_b16 a[192:193],v2
ds_read_b64_tr_b16 a[194:195],v2 offset:4096
v_mfma_f32_16x16x32_bf16 a[100:103],a[204:207],v[244:247],a[100:103]
ds_read_b64_tr_b16 a[196:197],v2 offset:8192
ds_read_b64_tr_b16 a[198:199],v2 offset:12288
v_mfma_f32_16x16x32_bf16 a[100:103],a[200:203],v[228:231],a[100:103]
ds_read_b64_tr_b16 a[204:205],v3 offset:8192
ds_read_b64_tr_b16 a[206:207],v3 offset:12288
v_mfma_f32_16x16x32_bf16 a[104:107],a[208:211],v[228:231],a[104:107]
ds_read_b64_tr_b16 a[200:201],v3
ds_read_b64_tr_b16 a[202:203],v3 offset:4096
v_mfma_f32_16x16x32_bf16 a[104:107],a[212:215],v[244:247],a[104:107]
ds_read_b64_tr_b16 a[208:209],v2 offset:256
ds_read_b64_tr_b16 a[210:211],v2 offset:4352
v_mfma_f32_16x16x32_bf16 a[108:111],a[220:223],v[244:247],a[108:111]
ds_read_b64_tr_b16 a[212:213],v2 offset:8448
ds_read_b64_tr_b16 a[214:215],v2 offset:12544
v_mfma_f32_16x16x32_bf16 a[108:111],a[216:219],v[228:231],a[108:111]
ds_read_b64_tr_b16 a[220:221],v3 offset:8448
ds_read_b64_tr_b16 a[222:223],v3 offset:12544
v_mfma_f32_16x16x32_bf16 a[112:115],a[224:227],v[228:231],a[112:115]
ds_read_b64_tr_b16 a[216:217],v3 offset:256
ds_read_b64_tr_b16 a[218:219],v3 offset:4352
v_mfma_f32_16x16x32_bf16 a[112:115],a[228:231],v[244:247],a[112:115]
ds_read_b64_tr_b16 a[224:225],v2 offset:512
ds_read_b64_tr_b16 a[226:227],v2 offset:4608
v_mfma_f32_16x16x32_bf16 a[116:119],a[236:239],v[244:247],a[116:119]
ds_read_b64_tr_b16 a[228:229],v2 offset:8704
ds_read_b64_tr_b16 a[230:231],v2 offset:12800
v_mfma_f32_16x16x32_bf16 a[116:119],a[232:235],v[228:231],a[116:119]
ds_read_b64_tr_b16 a[236:237],v3 offset:8704
ds_read_b64_tr_b16 a[238:239],v3 offset:12800
v_mfma_f32_16x16x32_bf16 a[120:123],a[240:243],v[228:231],a[120:123]
ds_read_b64_tr_b16 a[232:233],v3 offset:512
ds_read_b64_tr_b16 a[234:235],v3 offset:4608
v_mfma_f32_16x16x32_bf16 a[120:123],a[244:247],v[244:247],a[120:123]
ds_read_b64_tr_b16 a[240:241],v2 offset:768
ds_read_b64_tr_b16 a[242:243],v2 offset:4864
v_mfma_f32_16x16x32_bf16 a[124:127],a[252:255],v[244:247],a[124:127]
buffer_load_dwordx4 v5,s[24:27],s47 offen offset:3072 nt lds
v_mfma_f32_16x16x32_bf16 a[124:127],a[248:251],v[228:231],a[124:127]
s_add_u32 s47,s47,16384
ds_read_b64_tr_b16 a[244:245],v2 offset:8960
ds_read_b64_tr_b16 a[246:247],v2 offset:13056
v_mfma_f32_16x16x32_bf16 v[60:63],v[28:31],v[228:231],v[60:63]
s_and_b32 s47,s47,0x3ffff
ds_read_b64_tr_b16 a[252:253],v3 offset:8960
ds_read_b64_tr_b16 a[254:255],v3 offset:13056
v_mfma_f32_16x16x32_bf16 v[60:63],v[28:31],v[244:247],v[60:63]
s_or_b32 s47,s47,s46
ds_read_b64_tr_b16 a[248:249],v3 offset:768
ds_read_b64_tr_b16 a[250:251],v3 offset:4864
v_mfma_f32_16x16x32_bf16 v[196:199],a[128:131],v[144:147],v[40:43]
s_and_b32 s46,s54,0xfffc0000
v_exp_f32 v174,v174
v_mfma_f32_16x16x32_bf16 v[196:199],a[132:135],v[176:179],v[196:199]
v_exp_f32 v175,v175
v_mfma_f32_16x16x32_bf16 v[196:199],a[136:139],v[208:211],v[196:199]
v_exp_f32 v180,v180
v_mfma_f32_16x16x32_bf16 v[196:199],a[140:143],v[240:243],v[196:199]
v_exp_f32 v181,v181
v_mfma_f32_16x16x32_bf16 v[204:207],a[156:159],v[240:243],v[40:43]
v_exp_f32 v182,v182
v_mfma_f32_16x16x32_bf16 v[204:207],a[152:155],v[208:211],v[204:207]
v_exp_f32 v183,v183
v_mfma_f32_16x16x32_bf16 v[204:207],a[148:151],v[176:179],v[204:207]
v_cvt_pk_bf16_f32 v166,v172,v173
v_cvt_pk_bf16_f32 v167,v174,v175
v_mfma_f32_16x16x32_bf16 v[204:207],a[144:147],v[144:147],v[204:207]
v_cvt_pk_bf16_f32 v180,v180,v181
v_cvt_pk_bf16_f32 v181,v182,v183
v_mfma_f32_16x16x32_bf16 v[212:215],a[160:163],v[144:147],v[40:43]
v_exp_f32 v188,v188
v_mfma_f32_16x16x32_bf16 v[212:215],a[164:167],v[176:179],v[212:215]
v_exp_f32 v189,v189
v_mfma_f32_16x16x32_bf16 v[212:215],a[168:171],v[208:211],v[212:215]
v_exp_f32 v190,v190
v_mfma_f32_16x16x32_bf16 v[212:215],a[172:175],v[240:243],v[212:215]
v_exp_f32 v191,v191
v_mfma_f32_16x16x32_bf16 v[220:223],a[188:191],v[240:243],v[40:43]
v_cvt_pk_bf16_f32 v182,v188,v189
v_cvt_pk_bf16_f32 v183,v190,v191
v_mfma_f32_16x16x32_bf16 v[220:223],a[184:187],v[208:211],v[220:223]
v_accvgpr_read_b32 v81,a33
v_mul_f32 v80,v21,v80
v_mfma_f32_16x16x32_bf16 v[220:223],a[180:183],v[176:179],v[220:223]
v_mul_f32 v81,v21,v81
v_accvgpr_read_b32 v82,a34
v_mfma_f32_16x16x32_bf16 v[220:223],a[176:179],v[144:147],v[220:223]
s_waitcnt lgkmcnt(0)
v_accvgpr_read_b32 v83,a35
v_cvt_pk_bf16_f32 v80,v80,v81
v_mfma_f32_16x16x32_bf16 a[0:3],a[192:195],v[132:135],0
v_mul_f32 v82,v21,v82
v_mul_f32 v83,v21,v83
v_mfma_f32_16x16x32_bf16 a[0:3],a[196:199],v[148:151],a[0:3]
v_cvt_pk_bf16_f32 v81,v82,v83
v_accvgpr_read_b32 v82,a36
v_mfma_f32_16x16x32_bf16 a[4:7],a[204:207],v[148:151],0
v_accvgpr_read_b32 v83,a37
v_accvgpr_read_b32 v14,a38
v_mfma_f32_16x16x32_bf16 a[4:7],a[200:203],v[132:135],a[4:7]
v_accvgpr_read_b32 v15,a39
v_mul_f32 v82,v21,v82
v_mfma_f32_16x16x32_bf16 a[8:11],a[208:211],v[132:135],0
s_memrealtime s[76:77]
v_mul_f32 v83,v21,v83
v_mul_f32 v14,v21,v14
v_mfma_f32_16x16x32_bf16 a[8:11],a[212:215],v[148:151],a[8:11]
v_mul_f32 v15,v21,v15
v_cvt_pk_bf16_f32 v82,v82,v83
v_mfma_f32_16x16x32_bf16 a[12:15],a[220:223],v[148:151],0
v_cvt_pk_bf16_f32 v83,v14,v15
v_add_u32 v1,s48,v1
v_mfma_f32_16x16x32_bf16 a[12:15],a[216:219],v[132:135],a[12:15]
v_accvgpr_read_b32 v84,a40
v_accvgpr_read_b32 v85,a41
v_accvgpr_read_b32 v86,a42
v_accvgpr_read_b32 v87,a43
v_accvgpr_read_b32 v12,a44
v_accvgpr_read_b32 v13,a45
v_accvgpr_read_b32 v14,a46
v_accvgpr_read_b32 v15,a47
v_pk_mul_f32 v[84:85],v[20:21],v[84:85] op_sel:[1,0] op_sel_hi:[1,1]
v_pk_mul_f32 v[86:87],v[20:21],v[86:87] op_sel:[1,0] op_sel_hi:[1,1]
v_pk_mul_f32 v[12:13],v[20:21],v[12:13] op_sel:[1,0] op_sel_hi:[1,1]
v_pk_mul_f32 v[14:15],v[20:21],v[14:15] op_sel:[1,0] op_sel_hi:[1,1]
v_cvt_pk_bf16_f32 v84,v84,v85
v_cvt_pk_bf16_f32 v85,v86,v87
v_cvt_pk_bf16_f32 v86,v12,v13
v_cvt_pk_bf16_f32 v87,v14,v15
v_mfma_f32_16x16x32_bf16 a[16:19],a[224:227],v[132:135],0
v_accvgpr_read_b32 v88,a48
v_accvgpr_read_b32 v89,a49
v_accvgpr_read_b32 v90,a50
v_accvgpr_read_b32 v91,a51
v_accvgpr_read_b32 v12,a52
v_accvgpr_read_b32 v13,a53
v_accvgpr_read_b32 v14,a54
v_accvgpr_read_b32 v15,a55
v_pk_mul_f32 v[88:89],v[20:21],v[88:89] op_sel:[1,0] op_sel_hi:[1,1]
v_pk_mul_f32 v[90:91],v[20:21],v[90:91] op_sel:[1,0] op_sel_hi:[1,1]
v_pk_mul_f32 v[12:13],v[20:21],v[12:13] op_sel:[1,0] op_sel_hi:[1,1]
v_pk_mul_f32 v[14:15],v[20:21],v[14:15] op_sel:[1,0] op_sel_hi:[1,1]
v_cvt_pk_bf16_f32 v88,v88,v89
v_cvt_pk_bf16_f32 v89,v90,v91
v_cvt_pk_bf16_f32 v90,v12,v13
v_cvt_pk_bf16_f32 v91,v14,v15
v_mfma_f32_16x16x32_bf16 a[16:19],a[228:231],v[148:151],a[16:19]
v_accvgpr_read_b32 v92,a56
v_accvgpr_read_b32 v93,a57
v_accvgpr_read_b32 v94,a58
v_accvgpr_read_b32 v95,a59
v_accvgpr_read_b32 v12,a60
v_accvgpr_read_b32 v13,a61
v_accvgpr_read_b32 v14,a62
v_accvgpr_read_b32 v15,a63
v_pk_mul_f32 v[92:93],v[20:21],v[92:93] op_sel:[1,0] op_sel_hi:[1,1]
v_pk_mul_f32 v[94:95],v[20:21],v[94:95] op_sel:[1,0] op_sel_hi:[1,1]
v_pk_mul_f32 v[12:13],v[20:21],v[12:13] op_sel:[1,0] op_sel_hi:[1,1]
v_pk_mul_f32 v[14:15],v[20:21],v[14:15] op_sel:[1,0] op_sel_hi:[1,1]
v_cvt_pk_bf16_f32 v92,v92,v93
v_cvt_pk_bf16_f32 v93,v94,v95
v_cvt_pk_bf16_f32 v94,v12,v13
v_cvt_pk_bf16_f32 v95,v14,v15
v_mfma_f32_16x16x32_bf16 a[20:23],a[236:239],v[148:151],0
v_cmp_nlt_f32 s[58:59],v52,s63
s_nop 1
s_or_b64 s[56:57],s[56:57],s[58:59]
v_mfma_f32_16x16x32_bf16 a[20:23],a[232:235],v[132:135],a[20:23]
s_waitcnt lgkmcnt(0)
v_exp_f32 v196,v196
v_mfma_f32_16x16x32_bf16 a[24:27],a[240:243],v[132:135],0
s_sub_u32 s79,s78,s76
v_exp_f32 v197,v197
v_mfma_f32_16x16x32_bf16 a[24:27],a[244:247],v[148:151],a[24:27]
s_add_u32 s78,s76,s60
v_exp_f32 v198,v198
v_mfma_f32_16x16x32_bf16 a[28:31],a[252:255],v[148:151],0
s_cmp_gt_i32 s79,0
s_cbranch_scc1 L_ps5_%=
L_pr5_%=:
v_cvt_pk_bf16_f32 v196,v196,v197
v_mfma_f32_16x16x32_bf16 a[28:31],a[248:251],v[132:135],a[28:31]
v_exp_f32 v199,v199
v_mfma_f32_16x16x32_bf16 v[48:51],v[28:31],v[132:135],0
v_exp_f32 v204,v204
v_mfma_f32_16x16x32_bf16 v[48:51],v[28:31],v[148:151],v[48:51]
v_exp_f32 v205,v205
v_mfma_f32_16x16x32_bf16 v[228:231],a[128:131],v[152:155],v[44:47]
s_waitcnt vmcnt(8)
v_exp_f32 v206,v206
v_mfma_f32_16x16x32_bf16 v[228:231],a[132:135],v[184:187],v[228:231]
s_barrier
s_add_u32 m0,65536,s51
ds_read_b128 a[128:131],v1
v_mfma_f32_16x16x32_bf16 v[228:231],a[136:139],v[216:219],v[228:231]
ds_read_b128 a[132:135],v1 offset:256
v_cvt_pk_bf16_f32 v197,v198,v199
v_mfma_f32_16x16x32_bf16 v[228:231],a[140:143],v[248:251],v[228:231]
ds_read_b128 a[136:139],v1 offset:512
v_cvt_pk_bf16_f32 v198,v204,v205
v_mfma_f32_16x16x32_bf16 v[236:239],a[156:159],v[248:251],v[44:47]
buffer_load_dwordx4 v4,s[20:23],s47 offen nt lds
v_mfma_f32_16x16x32_bf16 v[236:239],a[152:155],v[216:219],v[236:239]
ds_read_b128 a[140:143],v1 offset:768
v_max_f32 v22,0x00800000,v56
v_mfma_f32_16x16x32_bf16 v[236:239],a[148:151],v[184:187],v[236:239]
ds_read_b128 a[156:159],v1 offset:4864
v_accvgpr_read_b32 v96,a64
v_mfma_f32_16x16x32_bf16 v[236:239],a[144:147],v[152:155],v[236:239]
ds_read_b128 a[152:155],v1 offset:4608
v_accvgpr_read_b32 v97,a65
v_mfma_f32_16x16x32_bf16 v[244:247],a[160:163],v[152:155],v[44:47]
)A"
R"A(ds_read_b128 a[148:151],v1 offset:4352
v_accvgpr_read_b32 v98,a66
v_mfma_f32_16x16x32_bf16 v[244:247],a[164:167],v[184:187],v[244:247]
ds_read_b128 a[144:147],v1 offset:4096
v_accvgpr_read_b32 v99,a67
v_mfma_f32_16x16x32_bf16 v[244:247],a[168:171],v[216:219],v[244:247]
ds_read_b128 a[160:163],v1 offset:8192
v_accvgpr_read_b32 v16,a70
v_mfma_f32_16x16x32_bf16 v[244:247],a[172:175],v[248:251],v[244:247]
ds_read_b128 a[164:167],v1 offset:8448
v_accvgpr_read_b32 v17,a71
v_mfma_f32_16x16x32_bf16 v[252:255],a[188:191],v[248:251],v[44:47]
ds_read_b128 a[168:171],v1 offset:8704
v_max_f32 v23,0x00800000,v60
v_mfma_f32_16x16x32_bf16 v[252:255],a[184:187],v[216:219],v[252:255]
ds_read_b128 a[172:175],v1 offset:8960
v_accvgpr_read_b32 v112,a96
v_mfma_f32_16x16x32_bf16 v[252:255],a[180:183],v[184:187],v[252:255]
ds_read_b128 a[188:191],v1 offset:13056
v_accvgpr_read_b32 v113,a97
v_mfma_f32_16x16x32_bf16 v[252:255],a[176:179],v[152:155],v[252:255]
ds_read_b128 a[184:187],v1 offset:12800
v_accvgpr_read_b32 v114,a98
v_mfma_f32_16x16x32_bf16 a[32:35],a[192:195],v[164:167],0
ds_read_b128 a[180:183],v1 offset:12544
v_accvgpr_read_b32 v115,a99
v_mfma_f32_16x16x32_bf16 a[32:35],a[196:199],v[180:183],a[32:35]
ds_read_b128 a[176:179],v1 offset:12288
v_add_u32 v2,s48,v2
v_mfma_f32_16x16x32_bf16 a[36:39],a[204:207],v[180:183],0
buffer_load_dwordx4 v4,s[20:23],s47 offen offset:1024 nt lds
v_mfma_f32_16x16x32_bf16 a[36:39],a[200:203],v[164:167],a[36:39]
v_exp_f32 v207,v207
v_mfma_f32_16x16x32_bf16 a[40:43],a[208:211],v[164:167],0
v_exp_f32 v212,v212
v_mfma_f32_16x16x32_bf16 a[40:43],a[212:215],v[180:183],a[40:43]
v_exp_f32 v213,v213
v_mfma_f32_16x16x32_bf16 a[44:47],a[220:223],v[180:183],0
v_exp_f32 v214,v214
v_mfma_f32_16x16x32_bf16 a[44:47],a[216:219],v[164:167],a[44:47]
v_exp_f32 v215,v215
v_mfma_f32_16x16x32_bf16 a[48:51],a[224:227],v[164:167],0
v_cvt_pk_bf16_f32 v199,v206,v207
v_cvt_pk_bf16_f32 v212,v212,v213
v_mfma_f32_16x16x32_bf16 a[48:51],a[228:231],v[180:183],a[48:51]
v_exp_f32 v220,v220
v_mfma_f32_16x16x32_bf16 a[52:55],a[236:239],v[180:183],0
v_exp_f32 v221,v221
v_mfma_f32_16x16x32_bf16 a[52:55],a[232:235],v[164:167],a[52:55]
v_exp_f32 v222,v222
v_mfma_f32_16x16x32_bf16 a[56:59],a[240:243],v[164:167],0
v_exp_f32 v223,v223
v_mfma_f32_16x16x32_bf16 a[56:59],a[244:247],v[180:183],a[56:59]
v_cvt_pk_bf16_f32 v213,v214,v215
v_cvt_pk_bf16_f32 v214,v220,v221
v_mfma_f32_16x16x32_bf16 a[60:63],a[252:255],v[180:183],0
v_cvt_pk_bf16_f32 v215,v222,v223
v_add_u32 v3,s48,v3
v_mfma_f32_16x16x32_bf16 a[60:63],a[248:251],v[164:167],a[60:63]
s_mov_b32 s48,32768
v_rcp_f32 v22,v22
v_mfma_f32_16x16x32_bf16 v[52:55],v[28:31],v[164:167],0
buffer_load_dwordx4 v5,s[20:23],s47 offen offset:2048 nt lds
v_mfma_f32_16x16x32_bf16 v[52:55],v[28:31],v[180:183],v[52:55]
s_waitcnt lgkmcnt(0)
v_mul_f32 v96,v22,v96
v_mul_f32 v97,v22,v97
v_mfma_f32_16x16x32_bf16 v[132:135],a[128:131],v[128:131],v[32:35]
v_cvt_pk_bf16_f32 v96,v96,v97
v_mul_f32 v98,v22,v98
v_mfma_f32_16x16x32_bf16 v[132:135],a[132:135],v[160:163],v[132:135]
v_mul_f32 v99,v22,v99
v_cvt_pk_bf16_f32 v97,v98,v99
v_mfma_f32_16x16x32_bf16 v[132:135],a[136:139],v[192:195],v[132:135]
v_accvgpr_read_b32 v98,a68
v_accvgpr_read_b32 v99,a69
v_mfma_f32_16x16x32_bf16 v[132:135],a[140:143],v[224:227],v[132:135]
v_accvgpr_read_b32 v100,a72
v_accvgpr_read_b32 v101,a73
v_accvgpr_read_b32 v102,a74
v_accvgpr_read_b32 v103,a75
v_accvgpr_read_b32 v12,a76
v_accvgpr_read_b32 v13,a77
v_accvgpr_read_b32 v14,a78
v_accvgpr_read_b32 v15,a79
v_pk_mul_f32 v[100:101],v[22:23],v[100:101] op_sel:[0,0] op_sel_hi:[0,1]
v_pk_mul_f32 v[102:103],v[22:23],v[102:103] op_sel:[0,0] op_sel_hi:[0,1]
v_pk_mul_f32 v[12:13],v[22:23],v[12:13] op_sel:[0,0] op_sel_hi:[0,1]
v_pk_mul_f32 v[14:15],v[22:23],v[14:15] op_sel:[0,0] op_sel_hi:[0,1]
v_cvt_pk_bf16_f32 v100,v100,v101
v_cvt_pk_bf16_f32 v101,v102,v103
v_cvt_pk_bf16_f32 v102,v12,v13
v_cvt_pk_bf16_f32 v103,v14,v15
v_mfma_f32_16x16x32_bf16 v[140:143],a[156:159],v[224:227],v[32:35]
v_accvgpr_read_b32 v104,a80
v_accvgpr_read_b32 v105,a81
v_accvgpr_read_b32 v106,a82
v_accvgpr_read_b32 v107,a83
v_accvgpr_read_b32 v12,a84
v_accvgpr_read_b32 v13,a85
v_accvgpr_read_b32 v14,a86
v_accvgpr_read_b32 v15,a87
v_pk_mul_f32 v[104:105],v[22:23],v[104:105] op_sel:[0,0] op_sel_hi:[0,1]
v_pk_mul_f32 v[106:107],v[22:23],v[106:107] op_sel:[0,0] op_sel_hi:[0,1]
v_pk_mul_f32 v[12:13],v[22:23],v[12:13] op_sel:[0,0] op_sel_hi:[0,1]
v_pk_mul_f32 v[14:15],v[22:23],v[14:15] op_sel:[0,0] op_sel_hi:[0,1]
v_cvt_pk_bf16_f32 v104,v104,v105
v_cvt_pk_bf16_f32 v105,v106,v107
v_cvt_pk_bf16_f32 v106,v12,v13
v_cvt_pk_bf16_f32 v107,v14,v15
v_mfma_f32_16x16x32_bf16 v[140:143],a[152:155],v[192:195],v[140:143]
v_accvgpr_read_b32 v108,a88
v_accvgpr_read_b32 v109,a89
v_accvgpr_read_b32 v110,a90
v_accvgpr_read_b32 v111,a91
v_accvgpr_read_b32 v12,a92
v_accvgpr_read_b32 v13,a93
v_accvgpr_read_b32 v14,a94
v_accvgpr_read_b32 v15,a95
v_pk_mul_f32 v[108:109],v[22:23],v[108:109] op_sel:[0,0] op_sel_hi:[0,1]
v_pk_mul_f32 v[110:111],v[22:23],v[110:111] op_sel:[0,0] op_sel_hi:[0,1]
v_pk_mul_f32 v[12:13],v[22:23],v[12:13] op_sel:[0,0] op_sel_hi:[0,1]
v_pk_mul_f32 v[14:15],v[22:23],v[14:15] op_sel:[0,0] op_sel_hi:[0,1]
v_cvt_pk_bf16_f32 v108,v108,v109
v_cvt_pk_bf16_f32 v109,v110,v111
v_cvt_pk_bf16_f32 v110,v12,v13
v_cvt_pk_bf16_f32 v111,v14,v15
v_mfma_f32_16x16x32_bf16 v[140:143],a[148:151],v[160:163],v[140:143]
v_cmp_nlt_f32 s[58:59],v56,s63
s_nop 1
s_or_b64 s[56:57],s[56:57],s[58:59]
v_mfma_f32_16x16x32_bf16 v[140:143],a[144:147],v[128:131],v[140:143]
v_exp_f32 v228,v228
v_mfma_f32_16x16x32_bf16 v[148:151],a[160:163],v[128:131],v[32:35]
v_exp_f32 v229,v229
v_mfma_f32_16x16x32_bf16 v[148:151],a[164:167],v[160:163],v[148:151]
v_exp_f32 v230,v230
v_mfma_f32_16x16x32_bf16 v[148:151],a[168:171],v[192:195],v[148:151]
v_exp_f32 v231,v231
v_mfma_f32_16x16x32_bf16 v[148:151],a[172:175],v[224:227],v[148:151]
v_cvt_pk_bf16_f32 v228,v228,v229
v_cvt_pk_bf16_f32 v229,v230,v231
v_mfma_f32_16x16x32_bf16 v[156:159],a[188:191],v[224:227],v[32:35]
buffer_load_dwordx4 v5,s[20:23],s47 offen offset:3072 nt lds
v_mfma_f32_16x16x32_bf16 v[156:159],a[184:187],v[192:195],v[156:159]
v_exp_f32 v236,v236
v_mfma_f32_16x16x32_bf16 v[156:159],a[180:183],v[160:163],v[156:159]
v_exp_f32 v237,v237
v_mfma_f32_16x16x32_bf16 v[156:159],a[176:179],v[128:131],v[156:159]
v_exp_f32 v238,v238
v_mfma_f32_16x16x32_bf16 a[64:67],a[192:195],v[196:199],0
v_exp_f32 v239,v239
v_mfma_f32_16x16x32_bf16 a[64:67],a[196:199],v[212:215],a[64:67]
v_exp_f32 v244,v244
v_mfma_f32_16x16x32_bf16 a[68:71],a[204:207],v[212:215],0
v_exp_f32 v245,v245
v_mfma_f32_16x16x32_bf16 a[68:71],a[200:203],v[196:199],a[68:71]
v_exp_f32 v246,v246
v_mfma_f32_16x16x32_bf16 a[72:75],a[208:211],v[196:199],0
v_exp_f32 v247,v247
v_mfma_f32_16x16x32_bf16 a[72:75],a[212:215],v[212:215],a[72:75]
v_cvt_pk_bf16_f32 v230,v236,v237
v_cvt_pk_bf16_f32 v231,v238,v239
v_mfma_f32_16x16x32_bf16 a[76:79],a[220:223],v[212:215],0
v_cvt_pk_bf16_f32 v244,v244,v245
v_cvt_pk_bf16_f32 v245,v246,v247
v_mfma_f32_16x16x32_bf16 a[76:79],a[216:219],v[196:199],a[76:79]
s_add_u32 m0,65536,s52
v_exp_f32 v252,v252
v_mfma_f32_16x16x32_bf16 a[80:83],a[224:227],v[196:199],0
v_exp_f32 v253,v253
v_mfma_f32_16x16x32_bf16 a[80:83],a[228:231],v[212:215],a[80:83]
v_exp_f32 v254,v254
v_mfma_f32_16x16x32_bf16 a[84:87],a[236:239],v[212:215],0
buffer_load_dwordx4 v4,s[24:27],s47 offen nt lds
v_mfma_f32_16x16x32_bf16 a[84:87],a[232:235],v[196:199],a[84:87]
v_exp_f32 v255,v255
v_mfma_f32_16x16x32_bf16 a[88:91],a[240:243],v[196:199],0
v_rcp_f32 v23,v23
v_mfma_f32_16x16x32_bf16 a[88:91],a[244:247],v[212:215],a[88:91]
v_cvt_pk_bf16_f32 v246,v252,v253
v_cvt_pk_bf16_f32 v247,v254,v255
v_mfma_f32_16x16x32_bf16 a[92:95],a[252:255],v[212:215],0
v_mul_f32 v112,v23,v112
v_mul_f32 v113,v23,v113
v_mfma_f32_16x16x32_bf16 a[92:95],a[248:251],v[196:199],a[92:95]
v_cvt_pk_bf16_f32 v112,v112,v113
v_mul_f32 v114,v23,v114
v_mfma_f32_16x16x32_bf16 v[56:59],v[28:31],v[196:199],0
v_mul_f32 v115,v23,v115
v_cvt_pk_bf16_f32 v113,v114,v115
v_mfma_f32_16x16x32_bf16 v[56:59],v[28:31],v[212:215],v[56:59]
v_accvgpr_read_b32 v114,a100
v_accvgpr_read_b32 v115,a101
v_mfma_f32_16x16x32_bf16 v[164:167],a[128:131],v[136:139],v[36:39]
v_accvgpr_read_b32 v12,a102
v_accvgpr_read_b32 v13,a103
v_mfma_f32_16x16x32_bf16 v[164:167],a[132:135],v[168:171],v[164:167]
v_mul_f32 v114,v23,v114
v_mul_f32 v115,v23,v115
v_mfma_f32_16x16x32_bf16 v[164:167],a[136:139],v[200:203],v[164:167]
v_mul_f32 v12,v23,v12
v_mul_f32 v13,v23,v13
v_mfma_f32_16x16x32_bf16 v[164:167],a[140:143],v[232:235],v[164:167]
v_cvt_pk_bf16_f32 v114,v114,v115
v_cvt_pk_bf16_f32 v115,v12,v13
v_mfma_f32_16x16x32_bf16 v[172:175],a[156:159],v[232:235],v[36:39]
v_accvgpr_read_b32 v116,a104
v_accvgpr_read_b32 v117,a105
v_accvgpr_read_b32 v118,a106
v_accvgpr_read_b32 v119,a107
v_accvgpr_read_b32 v12,a108
v_accvgpr_read_b32 v13,a109
v_accvgpr_read_b32 v14,a110
v_accvgpr_read_b32 v15,a111
v_pk_mul_f32 v[116:117],v[22:23],v[116:117] op_sel:[1,0] op_sel_hi:[1,1]
v_pk_mul_f32 v[118:119],v[22:23],v[118:119] op_sel:[1,0] op_sel_hi:[1,1]
v_pk_mul_f32 v[12:13],v[22:23],v[12:13] op_sel:[1,0] op_sel_hi:[1,1]
v_pk_mul_f32 v[14:15],v[22:23],v[14:15] op_sel:[1,0] op_sel_hi:[1,1]
v_cvt_pk_bf16_f32 v116,v116,v117
v_cvt_pk_bf16_f32 v117,v118,v119
v_cvt_pk_bf16_f32 v118,v12,v13
v_cvt_pk_bf16_f32 v119,v14,v15
v_mfma_f32_16x16x32_bf16 v[172:175],a[152:155],v[200:203],v[172:175]
v_accvgpr_read_b32 v120,a112
v_accvgpr_read_b32 v121,a113
v_accvgpr_read_b32 v122,a114
v_accvgpr_read_b32 v123,a115
v_accvgpr_read_b32 v12,a116
v_accvgpr_read_b32 v13,a117
v_accvgpr_read_b32 v14,a118
v_accvgpr_read_b32 v15,a119
v_pk_mul_f32 v[120:121],v[22:23],v[120:121] op_sel:[1,0] op_sel_hi:[1,1]
v_pk_mul_f32 v[122:123],v[22:23],v[122:123] op_sel:[1,0] op_sel_hi:[1,1]
v_pk_mul_f32 v[12:13],v[22:23],v[12:13] op_sel:[1,0] op_sel_hi:[1,1]
v_pk_mul_f32 v[14:15],v[22:23],v[14:15] op_sel:[1,0] op_sel_hi:[1,1]
v_cvt_pk_bf16_f32 v120,v120,v121
v_cvt_pk_bf16_f32 v121,v122,v123
v_cvt_pk_bf16_f32 v122,v12,v13
v_cvt_pk_bf16_f32 v123,v14,v15
v_mfma_f32_16x16x32_bf16 v[172:175],a[148:151],v[168:171],v[172:175]
buffer_load_dwordx4 v5,s[24:27],s47 offen offset:1024 nt lds
v_mfma_f32_16x16x32_bf16 v[172:175],a[144:147],v[136:139],v[172:175]
v_accvgpr_read_b32 v124,a120
v_accvgpr_read_b32 v125,a121
v_accvgpr_read_b32 v126,a122
v_accvgpr_read_b32 v127,a123
v_accvgpr_read_b32 v12,a124
v_accvgpr_read_b32 v13,a125
v_accvgpr_read_b32 v14,a126
v_accvgpr_read_b32 v15,a127
v_pk_mul_f32 v[124:125],v[22:23],v[124:125] op_sel:[1,0] op_sel_hi:[1,1]
v_pk_mul_f32 v[126:127],v[22:23],v[126:127] op_sel:[1,0] op_sel_hi:[1,1]
v_pk_mul_f32 v[12:13],v[22:23],v[12:13] op_sel:[1,0] op_sel_hi:[1,1]
v_pk_mul_f32 v[14:15],v[22:23],v[14:15] op_sel:[1,0] op_sel_hi:[1,1]
v_cvt_pk_bf16_f32 v124,v124,v125
v_cvt_pk_bf16_f32 v125,v126,v127
v_cvt_pk_bf16_f32 v126,v12,v13
v_cvt_pk_bf16_f32 v127,v14,v15
v_mfma_f32_16x16x32_bf16 v[180:183],a[160:163],v[136:139],v[36:39]
v_cmp_nlt_f32 s[58:59],v60,s63
s_nop 1
s_or_b64 s[56:57],s[56:57],s[58:59]
v_mfma_f32_16x16x32_bf16 v[180:183],a[164:167],v[168:171],v[180:183]
v_exp_f32 v132,v132
v_mfma_f32_16x16x32_bf16 v[180:183],a[168:171],v[200:203],v[180:183]
v_exp_f32 v133,v133
v_mfma_f32_16x16x32_bf16 v[180:183],a[172:175],v[232:235],v[180:183]
v_exp_f32 v134,v134
v_mfma_f32_16x16x32_bf16 v[188:191],a[188:191],v[232:235],v[36:39]
v_exp_f32 v135,v135
v_mfma_f32_16x16x32_bf16 v[188:191],a[184:187],v[200:203],v[188:191]
v_cvt_pk_bf16_f32 v132,v132,v133
v_cvt_pk_bf16_f32 v133,v134,v135
v_mfma_f32_16x16x32_bf16 v[188:191],a[180:183],v[168:171],v[188:191]
v_exp_f32 v140,v140
v_mfma_f32_16x16x32_bf16 v[188:191],a[176:179],v[136:139],v[188:191]
v_exp_f32 v141,v141
v_mfma_f32_16x16x32_bf16 a[96:99],a[192:195],v[228:231],0
ds_read_b64_tr_b16 a[192:193],v2
ds_read_b64_tr_b16 a[194:195],v2 offset:4096
v_mfma_f32_16x16x32_bf16 a[96:99],a[196:199],v[244:247],a[96:99]
ds_read_b64_tr_b16 a[196:197],v2 offset:8192
ds_read_b64_tr_b16 a[198:199],v2 offset:12288
v_mfma_f32_16x16x32_bf16 a[100:103],a[204:207],v[244:247],0
)A"
R"A(ds_read_b64_tr_b16 a[204:205],v3 offset:8192
ds_read_b64_tr_b16 a[206:207],v3 offset:12288
v_mfma_f32_16x16x32_bf16 a[100:103],a[200:203],v[228:231],a[100:103]
ds_read_b64_tr_b16 a[200:201],v3
ds_read_b64_tr_b16 a[202:203],v3 offset:4096
v_mfma_f32_16x16x32_bf16 a[104:107],a[208:211],v[228:231],0
buffer_load_dwordx4 v4,s[24:27],s47 offen offset:2048 nt lds
v_mfma_f32_16x16x32_bf16 a[104:107],a[212:215],v[244:247],a[104:107]
ds_read_b64_tr_b16 a[208:209],v2 offset:256
ds_read_b64_tr_b16 a[210:211],v2 offset:4352
v_mfma_f32_16x16x32_bf16 a[108:111],a[220:223],v[244:247],0
v_exp_f32 v142,v142
v_mfma_f32_16x16x32_bf16 a[108:111],a[216:219],v[228:231],a[108:111]
v_exp_f32 v143,v143
v_mfma_f32_16x16x32_bf16 a[112:115],a[224:227],v[228:231],0
v_exp_f32 v148,v148
v_mfma_f32_16x16x32_bf16 a[112:115],a[228:231],v[244:247],a[112:115]
v_exp_f32 v149,v149
v_mfma_f32_16x16x32_bf16 a[116:119],a[236:239],v[244:247],0
ds_read_b64_tr_b16 a[212:213],v2 offset:8448
ds_read_b64_tr_b16 a[214:215],v2 offset:12544
v_mfma_f32_16x16x32_bf16 a[116:119],a[232:235],v[228:231],a[116:119]
v_exp_f32 v150,v150
v_mfma_f32_16x16x32_bf16 a[120:123],a[240:243],v[228:231],0
v_exp_f32 v151,v151
v_mfma_f32_16x16x32_bf16 a[120:123],a[244:247],v[244:247],a[120:123]
v_cvt_pk_bf16_f32 v134,v140,v141
v_cvt_pk_bf16_f32 v135,v142,v143
v_mfma_f32_16x16x32_bf16 a[124:127],a[252:255],v[244:247],0
ds_read_b64_tr_b16 a[220:221],v3 offset:8448
v_cvt_pk_bf16_f32 v148,v148,v149
v_mfma_f32_16x16x32_bf16 a[124:127],a[248:251],v[228:231],a[124:127]
v_exp_f32 v156,v156
v_mfma_f32_16x16x32_bf16 v[60:63],v[28:31],v[228:231],0
v_exp_f32 v157,v157
v_mfma_f32_16x16x32_bf16 v[60:63],v[28:31],v[244:247],v[60:63]
v_exp_f32 v158,v158
v_mfma_f32_16x16x32_bf16 v[196:199],a[128:131],v[144:147],v[40:43]
buffer_load_dwordx4 v5,s[24:27],s47 offen offset:3072 nt lds
v_mfma_f32_16x16x32_bf16 v[196:199],a[132:135],v[176:179],v[196:199]
s_add_u32 s47,s47,16384
v_exp_f32 v159,v159
v_mfma_f32_16x16x32_bf16 v[196:199],a[136:139],v[208:211],v[196:199]
s_and_b32 s47,s47,0x3ffff
ds_read_b64_tr_b16 a[222:223],v3 offset:12544
v_cvt_pk_bf16_f32 v149,v150,v151
v_mfma_f32_16x16x32_bf16 v[196:199],a[140:143],v[240:243],v[196:199]
s_or_b32 s47,s47,s46
v_cvt_pk_bf16_f32 v150,v156,v157
v_cvt_pk_bf16_f32 v151,v158,v159
v_mfma_f32_16x16x32_bf16 v[204:207],a[156:159],v[240:243],v[40:43]
ds_read_b64_tr_b16 a[216:217],v3 offset:256
ds_read_b64_tr_b16 a[218:219],v3 offset:4352
v_mfma_f32_16x16x32_bf16 v[204:207],a[152:155],v[208:211],v[204:207]
ds_read_b64_tr_b16 a[224:225],v2 offset:512
ds_read_b64_tr_b16 a[226:227],v2 offset:4608
v_mfma_f32_16x16x32_bf16 v[204:207],a[148:151],v[176:179],v[204:207]
ds_read_b64_tr_b16 a[228:229],v2 offset:8704
ds_read_b64_tr_b16 a[230:231],v2 offset:12800
v_mfma_f32_16x16x32_bf16 v[204:207],a[144:147],v[144:147],v[204:207]
ds_read_b64_tr_b16 a[236:237],v3 offset:8704
ds_read_b64_tr_b16 a[238:239],v3 offset:12800
v_mfma_f32_16x16x32_bf16 v[212:215],a[160:163],v[144:147],v[40:43]
ds_read_b64_tr_b16 a[232:233],v3 offset:512
ds_read_b64_tr_b16 a[234:235],v3 offset:4608
v_mfma_f32_16x16x32_bf16 v[212:215],a[164:167],v[176:179],v[212:215]
ds_read_b64_tr_b16 a[240:241],v2 offset:768
ds_read_b64_tr_b16 a[242:243],v2 offset:4864
v_mfma_f32_16x16x32_bf16 v[212:215],a[168:171],v[208:211],v[212:215]
ds_read_b64_tr_b16 a[244:245],v2 offset:8960
ds_read_b64_tr_b16 a[246:247],v2 offset:13056
v_mfma_f32_16x16x32_bf16 v[212:215],a[172:175],v[240:243],v[212:215]
ds_read_b64_tr_b16 a[252:253],v3 offset:8960
ds_read_b64_tr_b16 a[254:255],v3 offset:13056
v_mfma_f32_16x16x32_bf16 v[220:223],a[188:191],v[240:243],v[40:43]
ds_read_b64_tr_b16 a[248:249],v3 offset:768
ds_read_b64_tr_b16 a[250:251],v3 offset:4864
v_mfma_f32_16x16x32_bf16 v[220:223],a[184:187],v[208:211],v[220:223]
v_mul_f32 v98,v22,v98
v_mul_f32 v99,v22,v99
v_mfma_f32_16x16x32_bf16 v[220:223],a[180:183],v[176:179],v[220:223]
v_exp_f32 v164,v164
v_mfma_f32_16x16x32_bf16 v[220:223],a[176:179],v[144:147],v[220:223]
s_waitcnt lgkmcnt(10)
v_exp_f32 v165,v165
v_mfma_f32_16x16x32_bf16 a[0:3],a[192:195],v[132:135],a[0:3]
v_exp_f32 v180,v180
v_mfma_f32_16x16x32_bf16 a[0:3],a[196:199],v[148:151],a[0:3]
v_exp_f32 v181,v181
v_mfma_f32_16x16x32_bf16 a[4:7],a[204:207],v[148:151],a[4:7]
v_mul_f32 v16,v22,v16
v_mul_f32 v17,v22,v17
v_mfma_f32_16x16x32_bf16 a[4:7],a[200:203],v[132:135],a[4:7]
v_cvt_pk_bf16_f32 v98,v98,v99
v_cvt_pk_bf16_f32 v99,v16,v17
v_mfma_f32_16x16x32_bf16 a[8:11],a[208:211],v[132:135],a[8:11]
s_memrealtime s[76:77]
v_exp_f32 v166,v166
v_mfma_f32_16x16x32_bf16 a[8:11],a[212:215],v[148:151],a[8:11]
v_exp_f32 v167,v167
v_mfma_f32_16x16x32_bf16 a[12:15],a[220:223],v[148:151],a[12:15]
v_exp_f32 v182,v182
v_mfma_f32_16x16x32_bf16 a[12:15],a[216:219],v[132:135],a[12:15]
v_exp_f32 v183,v183
v_mfma_f32_16x16x32_bf16 a[16:19],a[224:227],v[132:135],a[16:19]
v_cvt_pk_bf16_f32 v164,v164,v165
v_cvt_pk_bf16_f32 v180,v180,v181
v_mfma_f32_16x16x32_bf16 a[16:19],a[228:231],v[148:151],a[16:19]
v_exp_f32 v172,v172
v_mfma_f32_16x16x32_bf16 a[20:23],a[236:239],v[148:151],a[20:23]
s_waitcnt lgkmcnt(0)
v_exp_f32 v173,v173
v_mfma_f32_16x16x32_bf16 a[20:23],a[232:235],v[132:135],a[20:23]
s_waitcnt lgkmcnt(0)
v_exp_f32 v174,v174
v_mfma_f32_16x16x32_bf16 a[24:27],a[240:243],v[132:135],a[24:27]
s_sub_u32 s79,s78,s76
v_exp_f32 v175,v175
v_mfma_f32_16x16x32_bf16 a[24:27],a[244:247],v[148:151],a[24:27]
s_add_u32 s78,s76,s60
v_exp_f32 v188,v188
v_mfma_f32_16x16x32_bf16 a[28:31],a[252:255],v[148:151],a[28:31]
s_cmp_gt_i32 s79,0
s_cbranch_scc1 L_ps6_%=
L_pr6_%=:
v_cvt_pk_bf16_f32 v165,v166,v167
v_mfma_f32_16x16x32_bf16 a[28:31],a[248:251],v[132:135],a[28:31]
v_exp_f32 v189,v189
v_exp_f32 v190,v190
v_exp_f32 v191,v191
v_mfma_f32_16x16x32_bf16 v[48:51],v[28:31],v[132:135],v[48:51]
s_cmp_lg_u64 s[56:57],0
s_cbranch_scc1 L_slow0_%=
L_sret0_%=:
v_cvt_pk_bf16_f32 v181,v182,v183
v_add_u32 v1,s48,v1
v_cvt_pk_bf16_f32 v166,v172,v173
v_cvt_pk_bf16_f32 v167,v174,v175
v_cvt_pk_bf16_f32 v182,v188,v189
v_cvt_pk_bf16_f32 v183,v190,v191
v_mfma_f32_16x16x32_bf16 v[48:51],v[28:31],v[148:151],v[48:51]
s_mov_b32 s53,11
s_cmp_eq_u32 s40,3
s_cbranch_scc0 L_att1_%=
s_cmp_gt_i32 s18,0
s_cbranch_scc1 L_att1v0_%=
s_cmp_gt_i32 s19,0
s_cbranch_scc1 L_att1v1_%=
s_cmp_gt_i32 s100,0
s_cbranch_scc1 L_att1v2_%=
.p2align 5
L_att1v3_%=:
s_waitcnt vmcnt(8) lgkmcnt(0)
v_exp_f32 v196,v196
s_barrier
s_add_u32 m0,s49,s51
ds_read_b128 a[128:131],v1
s_mov_b32 s90,L_out_end_%=-L_pc_%=+128
v_exp_f32 v197,v197
s_lshr_b32 s45,s45,1
ds_read_b128 a[132:135],v1 offset:256
v_cvt_pk_bf16_f32 v196,v196,v197
s_add_u32 s62,s50,32768
buffer_load_dwordx4 v4,s[20:23],s47 offen nt lds
s_and_b32 s62,s62,131071
ds_read_b128 a[136:139],v1 offset:512
v_add_u32 v2,s48,v2
s_add_u32 s54,s54,16384
v_exp_f32 v198,v198
s_sub_u32 s53,s53,1
ds_read_b128 a[140:143],v1 offset:768
v_add_u32 v3,s48,v3
s_sub_u32 s48,s62,s50
v_exp_f32 v199,v199
s_and_b32 s46,s54,0xfffc0000
ds_read_b128 a[156:159],v1 offset:4864
v_cvt_pk_bf16_f32 v197,v198,v199
s_mov_b32 s50,s62
v_exp_f32 v204,v204
ds_read_b128 a[152:155],v1 offset:4608
v_exp_f32 v205,v205
ds_read_b128 a[148:151],v1 offset:4352
v_cvt_pk_bf16_f32 v198,v204,v205
v_exp_f32 v206,v206
ds_read_b128 a[144:147],v1 offset:4096
v_mfma_f32_16x16x32_bf16 a[32:35],a[192:195],v[164:167],a[32:35]
v_exp_f32 v207,v207
v_mfma_f32_16x16x32_bf16 a[32:35],a[196:199],v[180:183],a[32:35]
ds_read_b128 a[160:163],v1 offset:8192
v_cvt_pk_bf16_f32 v199,v206,v207
v_mfma_f32_16x16x32_bf16 a[36:39],a[204:207],v[180:183],a[36:39]
buffer_load_dwordx4 v4,s[20:23],s47 offen offset:1024 nt lds
v_mfma_f32_16x16x32_bf16 a[36:39],a[200:203],v[164:167],a[36:39]
ds_read_b128 a[164:167],v1 offset:8448
v_mfma_f32_16x16x32_bf16 a[40:43],a[208:211],v[164:167],a[40:43]
v_exp_f32 v212,v212
v_mfma_f32_16x16x32_bf16 a[40:43],a[212:215],v[180:183],a[40:43]
ds_read_b128 a[168:171],v1 offset:8704
v_mfma_f32_16x16x32_bf16 a[44:47],a[220:223],v[180:183],a[44:47]
v_exp_f32 v213,v213
v_mfma_f32_16x16x32_bf16 a[44:47],a[216:219],v[164:167],a[44:47]
ds_read_b128 a[172:175],v1 offset:8960
v_cvt_pk_bf16_f32 v212,v212,v213
v_mfma_f32_16x16x32_bf16 a[48:51],a[224:227],v[164:167],a[48:51]
v_exp_f32 v214,v214
v_mfma_f32_16x16x32_bf16 a[48:51],a[228:231],v[180:183],a[48:51]
ds_read_b128 a[188:191],v1 offset:13056
v_mfma_f32_16x16x32_bf16 a[52:55],a[236:239],v[180:183],a[52:55]
v_exp_f32 v215,v215
v_mfma_f32_16x16x32_bf16 a[52:55],a[232:235],v[164:167],a[52:55]
ds_read_b128 a[184:187],v1 offset:12800
v_cvt_pk_bf16_f32 v213,v214,v215
v_mfma_f32_16x16x32_bf16 a[56:59],a[240:243],v[164:167],a[56:59]
v_exp_f32 v220,v220
v_mfma_f32_16x16x32_bf16 a[56:59],a[244:247],v[180:183],a[56:59]
ds_read_b128 a[180:183],v1 offset:12544
v_mfma_f32_16x16x32_bf16 a[60:63],a[252:255],v[180:183],a[60:63]
v_exp_f32 v221,v221
v_mfma_f32_16x16x32_bf16 a[60:63],a[248:251],v[164:167],a[60:63]
ds_read_b128 a[176:179],v1 offset:12288
v_cvt_pk_bf16_f32 v214,v220,v221
v_mfma_f32_16x16x32_bf16 v[52:55],v[28:31],v[164:167],v[52:55]
buffer_load_dwordx4 v5,s[20:23],s47 offen offset:2048 nt lds
v_mfma_f32_16x16x32_bf16 v[52:55],v[28:31],v[180:183],v[52:55]
s_waitcnt lgkmcnt(3)
v_exp_f32 v222,v222
v_mfma_f32_16x16x32_bf16 v[132:135],a[128:131],v[128:131],v[32:35]
v_exp_f32 v223,v223
v_mfma_f32_16x16x32_bf16 v[132:135],a[132:135],v[160:163],v[132:135]
v_cvt_pk_bf16_f32 v215,v222,v223
v_add_u32 v1,s48,v1
v_mfma_f32_16x16x32_bf16 v[132:135],a[136:139],v[192:195],v[132:135]
v_mfma_f32_16x16x32_bf16 v[132:135],a[140:143],v[224:227],v[132:135]
v_mfma_f32_16x16x32_bf16 v[140:143],a[156:159],v[224:227],v[32:35]
v_mfma_f32_16x16x32_bf16 v[140:143],a[152:155],v[192:195],v[140:143]
v_mfma_f32_16x16x32_bf16 v[140:143],a[148:151],v[160:163],v[140:143]
v_mfma_f32_16x16x32_bf16 v[140:143],a[144:147],v[128:131],v[140:143]
v_mfma_f32_16x16x32_bf16 v[148:151],a[160:163],v[128:131],v[32:35]
v_mfma_f32_16x16x32_bf16 v[148:151],a[164:167],v[160:163],v[148:151]
v_mfma_f32_16x16x32_bf16 v[148:151],a[168:171],v[192:195],v[148:151]
v_mfma_f32_16x16x32_bf16 v[148:151],a[172:175],v[224:227],v[148:151]
v_mfma_f32_16x16x32_bf16 v[156:159],a[188:191],v[224:227],v[32:35]
s_waitcnt lgkmcnt(0)
buffer_load_dwordx4 v5,s[20:23],s47 offen offset:3072 nt lds
v_mfma_f32_16x16x32_bf16 v[156:159],a[184:187],v[192:195],v[156:159]
v_mfma_f32_16x16x32_bf16 v[156:159],a[180:183],v[160:163],v[156:159]
v_mfma_f32_16x16x32_bf16 v[156:159],a[176:179],v[128:131],v[156:159]
v_mfma_f32_16x16x32_bf16 a[64:67],a[192:195],v[196:199],a[64:67]
v_mfma_f32_16x16x32_bf16 a[64:67],a[196:199],v[212:215],a[64:67]
v_mfma_f32_16x16x32_bf16 a[68:71],a[204:207],v[212:215],a[68:71]
v_mfma_f32_16x16x32_bf16 a[68:71],a[200:203],v[196:199],a[68:71]
v_mfma_f32_16x16x32_bf16 a[72:75],a[208:211],v[196:199],a[72:75]
v_mfma_f32_16x16x32_bf16 a[72:75],a[212:215],v[212:215],a[72:75]
v_mfma_f32_16x16x32_bf16 a[76:79],a[220:223],v[212:215],a[76:79]
v_mfma_f32_16x16x32_bf16 a[76:79],a[216:219],v[196:199],a[76:79]
s_add_u32 m0,s49,s52
v_exp_f32 v132,v132
v_mfma_f32_16x16x32_bf16 a[80:83],a[224:227],v[196:199],a[80:83]
s_xor_b32 s49,s50,65536
v_exp_f32 v133,v133
v_mfma_f32_16x16x32_bf16 a[80:83],a[228:231],v[212:215],a[80:83]
v_exp_f32 v134,v134
v_mfma_f32_16x16x32_bf16 a[84:87],a[236:239],v[212:215],a[84:87]
buffer_load_dwordx4 v4,s[24:27],s47 offen nt lds
v_mfma_f32_16x16x32_bf16 a[84:87],a[232:235],v[196:199],a[84:87]
v_exp_f32 v135,v135
v_mfma_f32_16x16x32_bf16 a[88:91],a[240:243],v[196:199],a[88:91]
v_cvt_pk_bf16_f32 v132,v132,v133
v_cvt_pk_bf16_f32 v133,v134,v135
v_mfma_f32_16x16x32_bf16 a[88:91],a[244:247],v[212:215],a[88:91]
v_exp_f32 v140,v140
v_mfma_f32_16x16x32_bf16 a[92:95],a[252:255],v[212:215],a[92:95]
v_exp_f32 v141,v141
v_mfma_f32_16x16x32_bf16 a[92:95],a[248:251],v[196:199],a[92:95]
v_exp_f32 v142,v142
v_mfma_f32_16x16x32_bf16 v[56:59],v[28:31],v[196:199],v[56:59]
v_exp_f32 v143,v143
v_mfma_f32_16x16x32_bf16 v[56:59],v[28:31],v[212:215],v[56:59]
v_exp_f32 v148,v148
v_mfma_f32_16x16x32_bf16 v[164:167],a[128:131],v[136:139],v[36:39]
v_exp_f32 v149,v149
v_mfma_f32_16x16x32_bf16 v[164:167],a[132:135],v[168:171],v[164:167]
)A"
R"A(v_exp_f32 v150,v150
v_mfma_f32_16x16x32_bf16 v[164:167],a[136:139],v[200:203],v[164:167]
v_exp_f32 v151,v151
v_mfma_f32_16x16x32_bf16 v[164:167],a[140:143],v[232:235],v[164:167]
v_cvt_pk_bf16_f32 v134,v140,v141
v_cvt_pk_bf16_f32 v135,v142,v143
v_mfma_f32_16x16x32_bf16 v[172:175],a[156:159],v[232:235],v[36:39]
v_cvt_pk_bf16_f32 v148,v148,v149
v_cvt_pk_bf16_f32 v149,v150,v151
v_mfma_f32_16x16x32_bf16 v[172:175],a[152:155],v[200:203],v[172:175]
v_exp_f32 v156,v156
v_mfma_f32_16x16x32_bf16 v[172:175],a[148:151],v[168:171],v[172:175]
buffer_load_dwordx4 v5,s[24:27],s47 offen offset:1024 nt lds
v_mfma_f32_16x16x32_bf16 v[172:175],a[144:147],v[136:139],v[172:175]
v_exp_f32 v157,v157
v_mfma_f32_16x16x32_bf16 v[180:183],a[160:163],v[136:139],v[36:39]
v_exp_f32 v158,v158
v_mfma_f32_16x16x32_bf16 v[180:183],a[164:167],v[168:171],v[180:183]
v_exp_f32 v159,v159
v_mfma_f32_16x16x32_bf16 v[180:183],a[168:171],v[200:203],v[180:183]
v_cvt_pk_bf16_f32 v150,v156,v157
v_cvt_pk_bf16_f32 v151,v158,v159
v_mfma_f32_16x16x32_bf16 v[180:183],a[172:175],v[232:235],v[180:183]
v_exp_f32 v164,v164
v_mfma_f32_16x16x32_bf16 v[188:191],a[188:191],v[232:235],v[36:39]
v_exp_f32 v165,v165
v_mfma_f32_16x16x32_bf16 v[188:191],a[184:187],v[200:203],v[188:191]
v_exp_f32 v166,v166
v_mfma_f32_16x16x32_bf16 v[188:191],a[180:183],v[168:171],v[188:191]
v_exp_f32 v167,v167
v_mfma_f32_16x16x32_bf16 v[188:191],a[176:179],v[136:139],v[188:191]
v_cvt_pk_bf16_f32 v164,v164,v165
v_cvt_pk_bf16_f32 v165,v166,v167
ds_read_b64_tr_b16 a[192:193],v2
ds_read_b64_tr_b16 a[194:195],v2 offset:4096
ds_read_b64_tr_b16 a[196:197],v2 offset:8192
ds_read_b64_tr_b16 a[198:199],v2 offset:12288
buffer_load_dwordx4 v4,s[24:27],s47 offen offset:2048 nt lds
ds_read_b64_tr_b16 a[204:205],v3 offset:8192
ds_read_b64_tr_b16 a[206:207],v3 offset:12288
ds_read_b64_tr_b16 a[200:201],v3
ds_read_b64_tr_b16 a[202:203],v3 offset:4096
ds_read_b64_tr_b16 a[208:209],v2 offset:256
ds_read_b64_tr_b16 a[210:211],v2 offset:4352
ds_read_b64_tr_b16 a[212:213],v2 offset:8448
ds_read_b64_tr_b16 a[214:215],v2 offset:12544
ds_read_b64_tr_b16 a[220:221],v3 offset:8448
ds_read_b64_tr_b16 a[222:223],v3 offset:12544
ds_read_b64_tr_b16 a[216:217],v3 offset:256
ds_read_b64_tr_b16 a[218:219],v3 offset:4352
ds_read_b64_tr_b16 a[224:225],v2 offset:512
v_mfma_f32_16x16x32_bf16 v[196:199],a[128:131],v[144:147],v[40:43]
buffer_load_dwordx4 v5,s[24:27],s47 offen offset:3072 nt lds
v_mfma_f32_16x16x32_bf16 v[196:199],a[132:135],v[176:179],v[196:199]
s_add_u32 s47,s47,16384
s_and_b32 s47,s47,0x3ffff
ds_read_b64_tr_b16 a[226:227],v2 offset:4608
v_mfma_f32_16x16x32_bf16 v[196:199],a[136:139],v[208:211],v[196:199]
s_or_b32 s47,s47,s46
ds_read_b64_tr_b16 a[228:229],v2 offset:8704
v_mfma_f32_16x16x32_bf16 v[196:199],a[140:143],v[240:243],v[196:199]
ds_read_b64_tr_b16 a[230:231],v2 offset:12800
v_mfma_f32_16x16x32_bf16 v[204:207],a[156:159],v[240:243],v[40:43]
ds_read_b64_tr_b16 a[236:237],v3 offset:8704
v_mfma_f32_16x16x32_bf16 v[204:207],a[152:155],v[208:211],v[204:207]
ds_read_b64_tr_b16 a[238:239],v3 offset:12800
v_mfma_f32_16x16x32_bf16 v[204:207],a[148:151],v[176:179],v[204:207]
ds_read_b64_tr_b16 a[232:233],v3 offset:512
v_mfma_f32_16x16x32_bf16 v[204:207],a[144:147],v[144:147],v[204:207]
ds_read_b64_tr_b16 a[234:235],v3 offset:4608
v_mfma_f32_16x16x32_bf16 v[212:215],a[160:163],v[144:147],v[40:43]
ds_read_b64_tr_b16 a[240:241],v2 offset:768
v_mfma_f32_16x16x32_bf16 v[212:215],a[164:167],v[176:179],v[212:215]
ds_read_b64_tr_b16 a[242:243],v2 offset:4864
v_mfma_f32_16x16x32_bf16 v[212:215],a[168:171],v[208:211],v[212:215]
ds_read_b64_tr_b16 a[244:245],v2 offset:8960
v_mfma_f32_16x16x32_bf16 v[212:215],a[172:175],v[240:243],v[212:215]
ds_read_b64_tr_b16 a[246:247],v2 offset:13056
v_mfma_f32_16x16x32_bf16 v[220:223],a[188:191],v[240:243],v[40:43]
ds_read_b64_tr_b16 a[252:253],v3 offset:8960
v_mfma_f32_16x16x32_bf16 v[220:223],a[184:187],v[208:211],v[220:223]
ds_read_b64_tr_b16 a[254:255],v3 offset:13056
v_mfma_f32_16x16x32_bf16 v[220:223],a[180:183],v[176:179],v[220:223]
ds_read_b64_tr_b16 a[248:249],v3 offset:768
v_mfma_f32_16x16x32_bf16 v[220:223],a[176:179],v[144:147],v[220:223]
s_waitcnt lgkmcnt(7)
ds_read_b64_tr_b16 a[250:251],v3 offset:4864
v_mfma_f32_16x16x32_bf16 a[0:3],a[192:195],v[132:135],a[0:3]
buffer_load_dword v26,v24,s[88:91],s93 offen
v_mfma_f32_16x16x32_bf16 a[0:3],a[196:199],v[148:151],a[0:3]
v_exp_f32 v180,v180
v_mfma_f32_16x16x32_bf16 a[4:7],a[204:207],v[148:151],a[4:7]
v_exp_f32 v181,v181
v_mfma_f32_16x16x32_bf16 a[4:7],a[200:203],v[132:135],a[4:7]
v_exp_f32 v182,v182
v_mfma_f32_16x16x32_bf16 a[8:11],a[208:211],v[132:135],a[8:11]
v_exp_f32 v183,v183
v_mfma_f32_16x16x32_bf16 a[8:11],a[212:215],v[148:151],a[8:11]
v_exp_f32 v172,v172
v_mfma_f32_16x16x32_bf16 a[12:15],a[220:223],v[148:151],a[12:15]
v_exp_f32 v173,v173
v_mfma_f32_16x16x32_bf16 a[12:15],a[216:219],v[132:135],a[12:15]
v_exp_f32 v174,v174
v_mfma_f32_16x16x32_bf16 a[16:19],a[224:227],v[132:135],a[16:19]
s_bitcmp1_b32 s45,0
s_cbranch_scc0 L_t31v31_%=
s_mov_b32 m0,s80
s_add_u32 s80,s80,4096
buffer_load_dwordx4 v25,s[24:27],s83 offen nt lds
s_add_u32 s83,s83,4096
L_t31v31_%=:
v_mfma_f32_16x16x32_bf16 a[16:19],a[228:231],v[148:151],a[16:19]
v_exp_f32 v175,v175
v_mfma_f32_16x16x32_bf16 a[20:23],a[236:239],v[148:151],a[20:23]
v_cvt_pk_bf16_f32 v180,v180,v181
v_cvt_pk_bf16_f32 v181,v182,v183
v_mfma_f32_16x16x32_bf16 a[20:23],a[232:235],v[132:135],a[20:23]
s_waitcnt lgkmcnt(0)
v_exp_f32 v188,v188
v_mfma_f32_16x16x32_bf16 a[24:27],a[240:243],v[132:135],a[24:27]
v_exp_f32 v189,v189
v_mfma_f32_16x16x32_bf16 a[24:27],a[244:247],v[148:151],a[24:27]
v_exp_f32 v190,v190
v_mfma_f32_16x16x32_bf16 a[28:31],a[252:255],v[148:151],a[28:31]
v_cvt_pk_bf16_f32 v166,v172,v173
v_mfma_f32_16x16x32_bf16 a[28:31],a[248:251],v[132:135],a[28:31]
v_exp_f32 v191,v191
v_mfma_f32_16x16x32_bf16 v[48:51],v[28:31],v[132:135],v[48:51]
v_cvt_pk_bf16_f32 v167,v174,v175
v_cvt_pk_bf16_f32 v182,v188,v189
v_cvt_pk_bf16_f32 v183,v190,v191
s_cmp_lg_u32 s53,0
v_mfma_f32_16x16x32_bf16 v[48:51],v[28:31],v[148:151],v[48:51]
s_cbranch_scc1 L_att1v3_%=
s_waitcnt lgkmcnt(0)
s_nop 7
s_branch L_att1x_%=
.p2align 5
L_att1v0_%=:
v_mfma_f32_16x16x32_bf16 v[228:231],a[128:131],v[152:155],v[44:47]
s_waitcnt vmcnt(8) lgkmcnt(0)
v_exp_f32 v196,v196
v_mfma_f32_16x16x32_bf16 v[228:231],a[132:135],v[184:187],v[228:231]
s_barrier
s_add_u32 m0,s49,s51
ds_read_b128 a[128:131],v1
v_mfma_f32_16x16x32_bf16 v[228:231],a[136:139],v[216:219],v[228:231]
s_mov_b32 s90,L_out_end_%=-L_pc_%=+128
v_exp_f32 v197,v197
v_mfma_f32_16x16x32_bf16 v[228:231],a[140:143],v[248:251],v[228:231]
s_lshr_b32 s45,s45,1
ds_read_b128 a[132:135],v1 offset:256
v_cvt_pk_bf16_f32 v196,v196,v197
v_mfma_f32_16x16x32_bf16 v[236:239],a[156:159],v[248:251],v[44:47]
s_add_u32 s62,s50,32768
buffer_load_dwordx4 v4,s[20:23],s47 offen nt lds
v_mfma_f32_16x16x32_bf16 v[236:239],a[152:155],v[216:219],v[236:239]
s_and_b32 s62,s62,131071
ds_read_b128 a[136:139],v1 offset:512
v_add_u32 v2,s48,v2
v_mfma_f32_16x16x32_bf16 v[236:239],a[148:151],v[184:187],v[236:239]
s_add_u32 s54,s54,16384
v_exp_f32 v198,v198
v_mfma_f32_16x16x32_bf16 v[236:239],a[144:147],v[152:155],v[236:239]
s_sub_u32 s53,s53,1
ds_read_b128 a[140:143],v1 offset:768
v_add_u32 v3,s48,v3
v_mfma_f32_16x16x32_bf16 v[244:247],a[160:163],v[152:155],v[44:47]
s_sub_u32 s48,s62,s50
v_exp_f32 v199,v199
v_mfma_f32_16x16x32_bf16 v[244:247],a[164:167],v[184:187],v[244:247]
s_and_b32 s46,s54,0xfffc0000
ds_read_b128 a[156:159],v1 offset:4864
v_cvt_pk_bf16_f32 v197,v198,v199
v_mfma_f32_16x16x32_bf16 v[244:247],a[168:171],v[216:219],v[244:247]
s_mov_b32 s50,s62
v_exp_f32 v204,v204
v_mfma_f32_16x16x32_bf16 v[244:247],a[172:175],v[248:251],v[244:247]
ds_read_b128 a[152:155],v1 offset:4608
v_mfma_f32_16x16x32_bf16 v[252:255],a[188:191],v[248:251],v[44:47]
v_exp_f32 v205,v205
v_mfma_f32_16x16x32_bf16 v[252:255],a[184:187],v[216:219],v[252:255]
ds_read_b128 a[148:151],v1 offset:4352
v_cvt_pk_bf16_f32 v198,v204,v205
v_mfma_f32_16x16x32_bf16 v[252:255],a[180:183],v[184:187],v[252:255]
v_exp_f32 v206,v206
v_mfma_f32_16x16x32_bf16 v[252:255],a[176:179],v[152:155],v[252:255]
ds_read_b128 a[144:147],v1 offset:4096
v_mfma_f32_16x16x32_bf16 a[32:35],a[192:195],v[164:167],a[32:35]
v_exp_f32 v207,v207
v_mfma_f32_16x16x32_bf16 a[32:35],a[196:199],v[180:183],a[32:35]
ds_read_b128 a[160:163],v1 offset:8192
v_cvt_pk_bf16_f32 v199,v206,v207
v_mfma_f32_16x16x32_bf16 a[36:39],a[204:207],v[180:183],a[36:39]
buffer_load_dwordx4 v4,s[20:23],s47 offen offset:1024 nt lds
v_mfma_f32_16x16x32_bf16 a[36:39],a[200:203],v[164:167],a[36:39]
ds_read_b128 a[164:167],v1 offset:8448
v_mfma_f32_16x16x32_bf16 a[40:43],a[208:211],v[164:167],a[40:43]
v_exp_f32 v212,v212
v_mfma_f32_16x16x32_bf16 a[40:43],a[212:215],v[180:183],a[40:43]
ds_read_b128 a[168:171],v1 offset:8704
v_mfma_f32_16x16x32_bf16 a[44:47],a[220:223],v[180:183],a[44:47]
v_exp_f32 v213,v213
v_mfma_f32_16x16x32_bf16 a[44:47],a[216:219],v[164:167],a[44:47]
ds_read_b128 a[172:175],v1 offset:8960
v_cvt_pk_bf16_f32 v212,v212,v213
v_mfma_f32_16x16x32_bf16 a[48:51],a[224:227],v[164:167],a[48:51]
v_exp_f32 v214,v214
v_mfma_f32_16x16x32_bf16 a[48:51],a[228:231],v[180:183],a[48:51]
ds_read_b128 a[188:191],v1 offset:13056
v_mfma_f32_16x16x32_bf16 a[52:55],a[236:239],v[180:183],a[52:55]
v_exp_f32 v215,v215
v_mfma_f32_16x16x32_bf16 a[52:55],a[232:235],v[164:167],a[52:55]
ds_read_b128 a[184:187],v1 offset:12800
v_cvt_pk_bf16_f32 v213,v214,v215
v_mfma_f32_16x16x32_bf16 a[56:59],a[240:243],v[164:167],a[56:59]
v_exp_f32 v220,v220
v_mfma_f32_16x16x32_bf16 a[56:59],a[244:247],v[180:183],a[56:59]
ds_read_b128 a[180:183],v1 offset:12544
v_mfma_f32_16x16x32_bf16 a[60:63],a[252:255],v[180:183],a[60:63]
v_exp_f32 v221,v221
v_mfma_f32_16x16x32_bf16 a[60:63],a[248:251],v[164:167],a[60:63]
ds_read_b128 a[176:179],v1 offset:12288
v_cvt_pk_bf16_f32 v214,v220,v221
v_mfma_f32_16x16x32_bf16 v[52:55],v[28:31],v[164:167],v[52:55]
buffer_load_dwordx4 v5,s[20:23],s47 offen offset:2048 nt lds
v_mfma_f32_16x16x32_bf16 v[52:55],v[28:31],v[180:183],v[52:55]
v_exp_f32 v222,v222
v_exp_f32 v223,v223
s_nop 0
v_cvt_pk_bf16_f32 v215,v222,v223
v_add_u32 v1,s48,v1
v_exp_f32 v228,v228
v_exp_f32 v229,v229
v_exp_f32 v230,v230
v_exp_f32 v231,v231
v_cvt_pk_bf16_f32 v228,v228,v229
v_cvt_pk_bf16_f32 v229,v230,v231
v_exp_f32 v236,v236
v_exp_f32 v237,v237
v_exp_f32 v238,v238
v_exp_f32 v239,v239
v_exp_f32 v244,v244
buffer_load_dwordx4 v5,s[20:23],s47 offen offset:3072 nt lds
v_exp_f32 v245,v245
v_exp_f32 v246,v246
v_exp_f32 v247,v247
v_mfma_f32_16x16x32_bf16 a[64:67],a[192:195],v[196:199],a[64:67]
v_cvt_pk_bf16_f32 v230,v236,v237
v_cvt_pk_bf16_f32 v231,v238,v239
v_mfma_f32_16x16x32_bf16 a[64:67],a[196:199],v[212:215],a[64:67]
v_cvt_pk_bf16_f32 v244,v244,v245
v_cvt_pk_bf16_f32 v245,v246,v247
v_mfma_f32_16x16x32_bf16 a[68:71],a[204:207],v[212:215],a[68:71]
v_exp_f32 v252,v252
v_mfma_f32_16x16x32_bf16 a[68:71],a[200:203],v[196:199],a[68:71]
v_exp_f32 v253,v253
v_mfma_f32_16x16x32_bf16 a[72:75],a[208:211],v[196:199],a[72:75]
v_exp_f32 v254,v254
v_mfma_f32_16x16x32_bf16 a[72:75],a[212:215],v[212:215],a[72:75]
v_exp_f32 v255,v255
v_mfma_f32_16x16x32_bf16 a[76:79],a[220:223],v[212:215],a[76:79]
v_cvt_pk_bf16_f32 v246,v252,v253
v_cvt_pk_bf16_f32 v247,v254,v255
v_mfma_f32_16x16x32_bf16 a[76:79],a[216:219],v[196:199],a[76:79]
s_add_u32 m0,s49,s52
v_mfma_f32_16x16x32_bf16 a[80:83],a[224:227],v[196:199],a[80:83]
s_xor_b32 s49,s50,65536
v_mfma_f32_16x16x32_bf16 a[80:83],a[228:231],v[212:215],a[80:83]
v_mfma_f32_16x16x32_bf16 a[84:87],a[236:239],v[212:215],a[84:87]
buffer_load_dwordx4 v4,s[24:27],s47 offen nt lds
v_mfma_f32_16x16x32_bf16 a[84:87],a[232:235],v[196:199],a[84:87]
v_mfma_f32_16x16x32_bf16 a[88:91],a[240:243],v[196:199],a[88:91]
v_mfma_f32_16x16x32_bf16 a[88:91],a[244:247],v[212:215],a[88:91]
v_mfma_f32_16x16x32_bf16 a[92:95],a[252:255],v[212:215],a[92:95]
v_mfma_f32_16x16x32_bf16 a[92:95],a[248:251],v[196:199],a[92:95]
v_mfma_f32_16x16x32_bf16 v[56:59],v[28:31],v[196:199],v[56:59]
v_mfma_f32_16x16x32_bf16 v[56:59],v[28:31],v[212:215],v[56:59]
)A"
R"A(s_waitcnt lgkmcnt(0)
v_mfma_f32_16x16x32_bf16 v[164:167],a[128:131],v[136:139],v[36:39]
v_mfma_f32_16x16x32_bf16 v[164:167],a[132:135],v[168:171],v[164:167]
v_mfma_f32_16x16x32_bf16 v[164:167],a[136:139],v[200:203],v[164:167]
v_mfma_f32_16x16x32_bf16 v[164:167],a[140:143],v[232:235],v[164:167]
v_mfma_f32_16x16x32_bf16 v[172:175],a[156:159],v[232:235],v[36:39]
v_mfma_f32_16x16x32_bf16 v[172:175],a[152:155],v[200:203],v[172:175]
v_mfma_f32_16x16x32_bf16 v[172:175],a[148:151],v[168:171],v[172:175]
buffer_load_dwordx4 v5,s[24:27],s47 offen offset:1024 nt lds
v_mfma_f32_16x16x32_bf16 v[172:175],a[144:147],v[136:139],v[172:175]
v_mfma_f32_16x16x32_bf16 v[180:183],a[160:163],v[136:139],v[36:39]
v_mfma_f32_16x16x32_bf16 v[180:183],a[164:167],v[168:171],v[180:183]
v_mfma_f32_16x16x32_bf16 v[180:183],a[168:171],v[200:203],v[180:183]
v_mfma_f32_16x16x32_bf16 v[180:183],a[172:175],v[232:235],v[180:183]
v_exp_f32 v164,v164
v_mfma_f32_16x16x32_bf16 v[188:191],a[188:191],v[232:235],v[36:39]
v_exp_f32 v165,v165
v_mfma_f32_16x16x32_bf16 v[188:191],a[184:187],v[200:203],v[188:191]
v_exp_f32 v166,v166
v_mfma_f32_16x16x32_bf16 v[188:191],a[180:183],v[168:171],v[188:191]
v_exp_f32 v167,v167
v_mfma_f32_16x16x32_bf16 v[188:191],a[176:179],v[136:139],v[188:191]
v_cvt_pk_bf16_f32 v164,v164,v165
v_cvt_pk_bf16_f32 v165,v166,v167
v_mfma_f32_16x16x32_bf16 a[96:99],a[192:195],v[228:231],a[96:99]
ds_read_b64_tr_b16 a[192:193],v2
v_mfma_f32_16x16x32_bf16 a[96:99],a[196:199],v[244:247],a[96:99]
ds_read_b64_tr_b16 a[194:195],v2 offset:4096
v_mfma_f32_16x16x32_bf16 a[100:103],a[204:207],v[244:247],a[100:103]
ds_read_b64_tr_b16 a[196:197],v2 offset:8192
v_mfma_f32_16x16x32_bf16 a[100:103],a[200:203],v[228:231],a[100:103]
ds_read_b64_tr_b16 a[198:199],v2 offset:12288
v_mfma_f32_16x16x32_bf16 a[104:107],a[208:211],v[228:231],a[104:107]
buffer_load_dwordx4 v4,s[24:27],s47 offen offset:2048 nt lds
v_mfma_f32_16x16x32_bf16 a[104:107],a[212:215],v[244:247],a[104:107]
ds_read_b64_tr_b16 a[204:205],v3 offset:8192
v_mfma_f32_16x16x32_bf16 a[108:111],a[220:223],v[244:247],a[108:111]
ds_read_b64_tr_b16 a[206:207],v3 offset:12288
v_mfma_f32_16x16x32_bf16 a[108:111],a[216:219],v[228:231],a[108:111]
ds_read_b64_tr_b16 a[200:201],v3
v_mfma_f32_16x16x32_bf16 a[112:115],a[224:227],v[228:231],a[112:115]
ds_read_b64_tr_b16 a[202:203],v3 offset:4096
v_mfma_f32_16x16x32_bf16 a[112:115],a[228:231],v[244:247],a[112:115]
ds_read_b64_tr_b16 a[208:209],v2 offset:256
v_mfma_f32_16x16x32_bf16 a[116:119],a[236:239],v[244:247],a[116:119]
ds_read_b64_tr_b16 a[210:211],v2 offset:4352
v_mfma_f32_16x16x32_bf16 a[116:119],a[232:235],v[228:231],a[116:119]
ds_read_b64_tr_b16 a[212:213],v2 offset:8448
v_mfma_f32_16x16x32_bf16 a[120:123],a[240:243],v[228:231],a[120:123]
ds_read_b64_tr_b16 a[214:215],v2 offset:12544
v_mfma_f32_16x16x32_bf16 a[120:123],a[244:247],v[244:247],a[120:123]
ds_read_b64_tr_b16 a[220:221],v3 offset:8448
v_mfma_f32_16x16x32_bf16 a[124:127],a[252:255],v[244:247],a[124:127]
ds_read_b64_tr_b16 a[222:223],v3 offset:12544
v_mfma_f32_16x16x32_bf16 a[124:127],a[248:251],v[228:231],a[124:127]
ds_read_b64_tr_b16 a[216:217],v3 offset:256
v_mfma_f32_16x16x32_bf16 v[60:63],v[28:31],v[228:231],v[60:63]
ds_read_b64_tr_b16 a[218:219],v3 offset:4352
v_mfma_f32_16x16x32_bf16 v[60:63],v[28:31],v[244:247],v[60:63]
ds_read_b64_tr_b16 a[224:225],v2 offset:512
v_mfma_f32_16x16x32_bf16 v[196:199],a[128:131],v[144:147],v[40:43]
buffer_load_dwordx4 v5,s[24:27],s47 offen offset:3072 nt lds
v_mfma_f32_16x16x32_bf16 v[196:199],a[132:135],v[176:179],v[196:199]
s_add_u32 s47,s47,16384
s_and_b32 s47,s47,0x3ffff
ds_read_b64_tr_b16 a[226:227],v2 offset:4608
v_mfma_f32_16x16x32_bf16 v[196:199],a[136:139],v[208:211],v[196:199]
s_or_b32 s47,s47,s46
ds_read_b64_tr_b16 a[228:229],v2 offset:8704
v_mfma_f32_16x16x32_bf16 v[196:199],a[140:143],v[240:243],v[196:199]
ds_read_b64_tr_b16 a[230:231],v2 offset:12800
v_mfma_f32_16x16x32_bf16 v[204:207],a[156:159],v[240:243],v[40:43]
ds_read_b64_tr_b16 a[236:237],v3 offset:8704
v_mfma_f32_16x16x32_bf16 v[204:207],a[152:155],v[208:211],v[204:207]
ds_read_b64_tr_b16 a[238:239],v3 offset:12800
v_mfma_f32_16x16x32_bf16 v[204:207],a[148:151],v[176:179],v[204:207]
ds_read_b64_tr_b16 a[232:233],v3 offset:512
v_mfma_f32_16x16x32_bf16 v[204:207],a[144:147],v[144:147],v[204:207]
ds_read_b64_tr_b16 a[234:235],v3 offset:4608
v_mfma_f32_16x16x32_bf16 v[212:215],a[160:163],v[144:147],v[40:43]
ds_read_b64_tr_b16 a[240:241],v2 offset:768
v_mfma_f32_16x16x32_bf16 v[212:215],a[164:167],v[176:179],v[212:215]
ds_read_b64_tr_b16 a[242:243],v2 offset:4864
v_mfma_f32_16x16x32_bf16 v[212:215],a[168:171],v[208:211],v[212:215]
ds_read_b64_tr_b16 a[244:245],v2 offset:8960
v_mfma_f32_16x16x32_bf16 v[212:215],a[172:175],v[240:243],v[212:215]
ds_read_b64_tr_b16 a[246:247],v2 offset:13056
v_mfma_f32_16x16x32_bf16 v[220:223],a[188:191],v[240:243],v[40:43]
ds_read_b64_tr_b16 a[252:253],v3 offset:8960
v_mfma_f32_16x16x32_bf16 v[220:223],a[184:187],v[208:211],v[220:223]
ds_read_b64_tr_b16 a[254:255],v3 offset:13056
v_mfma_f32_16x16x32_bf16 v[220:223],a[180:183],v[176:179],v[220:223]
ds_read_b64_tr_b16 a[248:249],v3 offset:768
v_mfma_f32_16x16x32_bf16 v[220:223],a[176:179],v[144:147],v[220:223]
ds_read_b64_tr_b16 a[250:251],v3 offset:4864
buffer_load_dword v26,v24,s[88:91],s93 offen
v_exp_f32 v180,v180
v_exp_f32 v181,v181
v_exp_f32 v182,v182
v_exp_f32 v183,v183
v_exp_f32 v172,v172
v_exp_f32 v173,v173
v_exp_f32 v174,v174
s_bitcmp1_b32 s45,0
s_cbranch_scc0 L_t31v01_%=
s_mov_b32 m0,s80
s_add_u32 s80,s80,4096
buffer_load_dwordx4 v25,s[24:27],s83 offen nt lds
s_add_u32 s83,s83,4096
L_t31v01_%=:
v_exp_f32 v175,v175
v_cvt_pk_bf16_f32 v180,v180,v181
v_cvt_pk_bf16_f32 v181,v182,v183
v_exp_f32 v188,v188
v_exp_f32 v189,v189
v_exp_f32 v190,v190
v_cvt_pk_bf16_f32 v166,v172,v173
v_exp_f32 v191,v191
v_cvt_pk_bf16_f32 v167,v174,v175
v_cvt_pk_bf16_f32 v182,v188,v189
v_cvt_pk_bf16_f32 v183,v190,v191
s_cmp_lg_u32 s53,0
s_cbranch_scc1 L_att1v0_%=
s_waitcnt lgkmcnt(0)
s_nop 7
s_branch L_att1x_%=
.p2align 5
L_att1v1_%=:
v_mfma_f32_16x16x32_bf16 v[228:231],a[128:131],v[152:155],v[44:47]
s_waitcnt vmcnt(8) lgkmcnt(0)
v_exp_f32 v196,v196
v_mfma_f32_16x16x32_bf16 v[228:231],a[132:135],v[184:187],v[228:231]
s_barrier
s_add_u32 m0,s49,s51
ds_read_b128 a[128:131],v1
v_mfma_f32_16x16x32_bf16 v[228:231],a[136:139],v[216:219],v[228:231]
s_mov_b32 s90,L_out_end_%=-L_pc_%=+128
v_exp_f32 v197,v197
v_mfma_f32_16x16x32_bf16 v[228:231],a[140:143],v[248:251],v[228:231]
s_lshr_b32 s45,s45,1
ds_read_b128 a[132:135],v1 offset:256
v_cvt_pk_bf16_f32 v196,v196,v197
v_mfma_f32_16x16x32_bf16 v[236:239],a[156:159],v[248:251],v[44:47]
s_add_u32 s62,s50,32768
buffer_load_dwordx4 v4,s[20:23],s47 offen nt lds
v_mfma_f32_16x16x32_bf16 v[236:239],a[152:155],v[216:219],v[236:239]
s_and_b32 s62,s62,131071
ds_read_b128 a[136:139],v1 offset:512
v_add_u32 v2,s48,v2
v_mfma_f32_16x16x32_bf16 v[236:239],a[148:151],v[184:187],v[236:239]
s_add_u32 s54,s54,16384
v_exp_f32 v198,v198
v_mfma_f32_16x16x32_bf16 v[236:239],a[144:147],v[152:155],v[236:239]
s_sub_u32 s53,s53,1
ds_read_b128 a[140:143],v1 offset:768
v_add_u32 v3,s48,v3
v_mfma_f32_16x16x32_bf16 v[244:247],a[160:163],v[152:155],v[44:47]
s_sub_u32 s48,s62,s50
v_exp_f32 v199,v199
v_mfma_f32_16x16x32_bf16 v[244:247],a[164:167],v[184:187],v[244:247]
s_and_b32 s46,s54,0xfffc0000
ds_read_b128 a[156:159],v1 offset:4864
v_cvt_pk_bf16_f32 v197,v198,v199
v_mfma_f32_16x16x32_bf16 v[244:247],a[168:171],v[216:219],v[244:247]
s_mov_b32 s50,s62
v_exp_f32 v204,v204
v_mfma_f32_16x16x32_bf16 v[244:247],a[172:175],v[248:251],v[244:247]
ds_read_b128 a[152:155],v1 offset:4608
v_mfma_f32_16x16x32_bf16 v[252:255],a[188:191],v[248:251],v[44:47]
v_exp_f32 v205,v205
v_mfma_f32_16x16x32_bf16 v[252:255],a[184:187],v[216:219],v[252:255]
ds_read_b128 a[148:151],v1 offset:4352
v_cvt_pk_bf16_f32 v198,v204,v205
v_mfma_f32_16x16x32_bf16 v[252:255],a[180:183],v[184:187],v[252:255]
v_exp_f32 v206,v206
v_mfma_f32_16x16x32_bf16 v[252:255],a[176:179],v[152:155],v[252:255]
s_waitcnt lgkmcnt(3)
ds_read_b128 a[144:147],v1 offset:4096
v_exp_f32 v207,v207
ds_read_b128 a[160:163],v1 offset:8192
v_cvt_pk_bf16_f32 v199,v206,v207
buffer_load_dwordx4 v4,s[20:23],s47 offen offset:1024 nt lds
ds_read_b128 a[164:167],v1 offset:8448
v_exp_f32 v212,v212
ds_read_b128 a[168:171],v1 offset:8704
v_exp_f32 v213,v213
ds_read_b128 a[172:175],v1 offset:8960
v_cvt_pk_bf16_f32 v212,v212,v213
v_exp_f32 v214,v214
ds_read_b128 a[188:191],v1 offset:13056
v_exp_f32 v215,v215
ds_read_b128 a[184:187],v1 offset:12800
v_cvt_pk_bf16_f32 v213,v214,v215
v_exp_f32 v220,v220
ds_read_b128 a[180:183],v1 offset:12544
v_exp_f32 v221,v221
ds_read_b128 a[176:179],v1 offset:12288
v_cvt_pk_bf16_f32 v214,v220,v221
buffer_load_dwordx4 v5,s[20:23],s47 offen offset:2048 nt lds
v_exp_f32 v222,v222
v_mfma_f32_16x16x32_bf16 v[132:135],a[128:131],v[128:131],v[32:35]
v_exp_f32 v223,v223
v_mfma_f32_16x16x32_bf16 v[132:135],a[132:135],v[160:163],v[132:135]
v_cvt_pk_bf16_f32 v215,v222,v223
v_add_u32 v1,s48,v1
v_mfma_f32_16x16x32_bf16 v[132:135],a[136:139],v[192:195],v[132:135]
v_exp_f32 v228,v228
v_mfma_f32_16x16x32_bf16 v[132:135],a[140:143],v[224:227],v[132:135]
s_waitcnt lgkmcnt(10)
v_exp_f32 v229,v229
v_mfma_f32_16x16x32_bf16 v[140:143],a[156:159],v[224:227],v[32:35]
v_exp_f32 v230,v230
v_mfma_f32_16x16x32_bf16 v[140:143],a[152:155],v[192:195],v[140:143]
s_waitcnt lgkmcnt(9)
v_exp_f32 v231,v231
v_mfma_f32_16x16x32_bf16 v[140:143],a[148:151],v[160:163],v[140:143]
s_waitcnt lgkmcnt(8)
v_cvt_pk_bf16_f32 v228,v228,v229
v_cvt_pk_bf16_f32 v229,v230,v231
v_mfma_f32_16x16x32_bf16 v[140:143],a[144:147],v[128:131],v[140:143]
s_waitcnt lgkmcnt(0)
v_exp_f32 v236,v236
v_mfma_f32_16x16x32_bf16 v[148:151],a[160:163],v[128:131],v[32:35]
v_exp_f32 v237,v237
v_mfma_f32_16x16x32_bf16 v[148:151],a[164:167],v[160:163],v[148:151]
v_exp_f32 v238,v238
v_mfma_f32_16x16x32_bf16 v[148:151],a[168:171],v[192:195],v[148:151]
v_exp_f32 v239,v239
v_mfma_f32_16x16x32_bf16 v[148:151],a[172:175],v[224:227],v[148:151]
v_exp_f32 v244,v244
v_mfma_f32_16x16x32_bf16 v[156:159],a[188:191],v[224:227],v[32:35]
buffer_load_dwordx4 v5,s[20:23],s47 offen offset:3072 nt lds
v_mfma_f32_16x16x32_bf16 v[156:159],a[184:187],v[192:195],v[156:159]
v_exp_f32 v245,v245
v_mfma_f32_16x16x32_bf16 v[156:159],a[180:183],v[160:163],v[156:159]
v_exp_f32 v246,v246
v_mfma_f32_16x16x32_bf16 v[156:159],a[176:179],v[128:131],v[156:159]
v_exp_f32 v247,v247
v_mfma_f32_16x16x32_bf16 a[64:67],a[192:195],v[196:199],a[64:67]
v_cvt_pk_bf16_f32 v230,v236,v237
v_cvt_pk_bf16_f32 v231,v238,v239
v_mfma_f32_16x16x32_bf16 a[64:67],a[196:199],v[212:215],a[64:67]
v_cvt_pk_bf16_f32 v244,v244,v245
v_cvt_pk_bf16_f32 v245,v246,v247
v_mfma_f32_16x16x32_bf16 a[68:71],a[204:207],v[212:215],a[68:71]
v_exp_f32 v252,v252
v_mfma_f32_16x16x32_bf16 a[68:71],a[200:203],v[196:199],a[68:71]
v_exp_f32 v253,v253
v_mfma_f32_16x16x32_bf16 a[72:75],a[208:211],v[196:199],a[72:75]
v_exp_f32 v254,v254
v_mfma_f32_16x16x32_bf16 a[72:75],a[212:215],v[212:215],a[72:75]
v_exp_f32 v255,v255
v_mfma_f32_16x16x32_bf16 a[76:79],a[220:223],v[212:215],a[76:79]
v_cvt_pk_bf16_f32 v246,v252,v253
v_cvt_pk_bf16_f32 v247,v254,v255
v_mfma_f32_16x16x32_bf16 a[76:79],a[216:219],v[196:199],a[76:79]
s_add_u32 m0,s49,s52
v_exp_f32 v132,v132
v_mfma_f32_16x16x32_bf16 a[80:83],a[224:227],v[196:199],a[80:83]
s_xor_b32 s49,s50,65536
v_exp_f32 v133,v133
v_mfma_f32_16x16x32_bf16 a[80:83],a[228:231],v[212:215],a[80:83]
v_exp_f32 v134,v134
v_mfma_f32_16x16x32_bf16 a[84:87],a[236:239],v[212:215],a[84:87]
buffer_load_dwordx4 v4,s[24:27],s47 offen nt lds
v_mfma_f32_16x16x32_bf16 a[84:87],a[232:235],v[196:199],a[84:87]
v_exp_f32 v135,v135
v_mfma_f32_16x16x32_bf16 a[88:91],a[240:243],v[196:199],a[88:91]
v_cvt_pk_bf16_f32 v132,v132,v133
v_cvt_pk_bf16_f32 v133,v134,v135
v_mfma_f32_16x16x32_bf16 a[88:91],a[244:247],v[212:215],a[88:91]
v_exp_f32 v140,v140
v_mfma_f32_16x16x32_bf16 a[92:95],a[252:255],v[212:215],a[92:95]
v_exp_f32 v141,v141
v_mfma_f32_16x16x32_bf16 a[92:95],a[248:251],v[196:199],a[92:95]
)A"
R"A(v_exp_f32 v142,v142
v_mfma_f32_16x16x32_bf16 v[56:59],v[28:31],v[196:199],v[56:59]
v_exp_f32 v143,v143
v_mfma_f32_16x16x32_bf16 v[56:59],v[28:31],v[212:215],v[56:59]
v_exp_f32 v148,v148
v_exp_f32 v149,v149
v_exp_f32 v150,v150
v_exp_f32 v151,v151
v_cvt_pk_bf16_f32 v134,v140,v141
v_cvt_pk_bf16_f32 v135,v142,v143
v_cvt_pk_bf16_f32 v148,v148,v149
v_cvt_pk_bf16_f32 v149,v150,v151
v_exp_f32 v156,v156
buffer_load_dwordx4 v5,s[24:27],s47 offen offset:1024 nt lds
v_exp_f32 v157,v157
v_exp_f32 v158,v158
v_exp_f32 v159,v159
v_cvt_pk_bf16_f32 v150,v156,v157
v_cvt_pk_bf16_f32 v151,v158,v159
v_mfma_f32_16x16x32_bf16 a[96:99],a[192:195],v[228:231],a[96:99]
ds_read_b64_tr_b16 a[192:193],v2
v_mfma_f32_16x16x32_bf16 a[96:99],a[196:199],v[244:247],a[96:99]
ds_read_b64_tr_b16 a[194:195],v2 offset:4096
v_mfma_f32_16x16x32_bf16 a[100:103],a[204:207],v[244:247],a[100:103]
ds_read_b64_tr_b16 a[196:197],v2 offset:8192
v_mfma_f32_16x16x32_bf16 a[100:103],a[200:203],v[228:231],a[100:103]
ds_read_b64_tr_b16 a[198:199],v2 offset:12288
v_mfma_f32_16x16x32_bf16 a[104:107],a[208:211],v[228:231],a[104:107]
buffer_load_dwordx4 v4,s[24:27],s47 offen offset:2048 nt lds
v_mfma_f32_16x16x32_bf16 a[104:107],a[212:215],v[244:247],a[104:107]
ds_read_b64_tr_b16 a[204:205],v3 offset:8192
v_mfma_f32_16x16x32_bf16 a[108:111],a[220:223],v[244:247],a[108:111]
ds_read_b64_tr_b16 a[206:207],v3 offset:12288
v_mfma_f32_16x16x32_bf16 a[108:111],a[216:219],v[228:231],a[108:111]
ds_read_b64_tr_b16 a[200:201],v3
v_mfma_f32_16x16x32_bf16 a[112:115],a[224:227],v[228:231],a[112:115]
ds_read_b64_tr_b16 a[202:203],v3 offset:4096
v_mfma_f32_16x16x32_bf16 a[112:115],a[228:231],v[244:247],a[112:115]
ds_read_b64_tr_b16 a[208:209],v2 offset:256
v_mfma_f32_16x16x32_bf16 a[116:119],a[236:239],v[244:247],a[116:119]
ds_read_b64_tr_b16 a[210:211],v2 offset:4352
v_mfma_f32_16x16x32_bf16 a[116:119],a[232:235],v[228:231],a[116:119]
ds_read_b64_tr_b16 a[212:213],v2 offset:8448
v_mfma_f32_16x16x32_bf16 a[120:123],a[240:243],v[228:231],a[120:123]
ds_read_b64_tr_b16 a[214:215],v2 offset:12544
v_mfma_f32_16x16x32_bf16 a[120:123],a[244:247],v[244:247],a[120:123]
ds_read_b64_tr_b16 a[220:221],v3 offset:8448
v_mfma_f32_16x16x32_bf16 a[124:127],a[252:255],v[244:247],a[124:127]
ds_read_b64_tr_b16 a[222:223],v3 offset:12544
v_mfma_f32_16x16x32_bf16 a[124:127],a[248:251],v[228:231],a[124:127]
ds_read_b64_tr_b16 a[216:217],v3 offset:256
v_mfma_f32_16x16x32_bf16 v[60:63],v[28:31],v[228:231],v[60:63]
ds_read_b64_tr_b16 a[218:219],v3 offset:4352
v_mfma_f32_16x16x32_bf16 v[60:63],v[28:31],v[244:247],v[60:63]
ds_read_b64_tr_b16 a[224:225],v2 offset:512
v_mfma_f32_16x16x32_bf16 v[196:199],a[128:131],v[144:147],v[40:43]
buffer_load_dwordx4 v5,s[24:27],s47 offen offset:3072 nt lds
v_mfma_f32_16x16x32_bf16 v[196:199],a[132:135],v[176:179],v[196:199]
s_add_u32 s47,s47,16384
s_and_b32 s47,s47,0x3ffff
ds_read_b64_tr_b16 a[226:227],v2 offset:4608
v_mfma_f32_16x16x32_bf16 v[196:199],a[136:139],v[208:211],v[196:199]
s_or_b32 s47,s47,s46
ds_read_b64_tr_b16 a[228:229],v2 offset:8704
v_mfma_f32_16x16x32_bf16 v[196:199],a[140:143],v[240:243],v[196:199]
ds_read_b64_tr_b16 a[230:231],v2 offset:12800
v_mfma_f32_16x16x32_bf16 v[204:207],a[156:159],v[240:243],v[40:43]
ds_read_b64_tr_b16 a[236:237],v3 offset:8704
v_mfma_f32_16x16x32_bf16 v[204:207],a[152:155],v[208:211],v[204:207]
ds_read_b64_tr_b16 a[238:239],v3 offset:12800
v_mfma_f32_16x16x32_bf16 v[204:207],a[148:151],v[176:179],v[204:207]
ds_read_b64_tr_b16 a[232:233],v3 offset:512
v_mfma_f32_16x16x32_bf16 v[204:207],a[144:147],v[144:147],v[204:207]
ds_read_b64_tr_b16 a[234:235],v3 offset:4608
v_mfma_f32_16x16x32_bf16 v[212:215],a[160:163],v[144:147],v[40:43]
ds_read_b64_tr_b16 a[240:241],v2 offset:768
v_mfma_f32_16x16x32_bf16 v[212:215],a[164:167],v[176:179],v[212:215]
ds_read_b64_tr_b16 a[242:243],v2 offset:4864
v_mfma_f32_16x16x32_bf16 v[212:215],a[168:171],v[208:211],v[212:215]
ds_read_b64_tr_b16 a[244:245],v2 offset:8960
v_mfma_f32_16x16x32_bf16 v[212:215],a[172:175],v[240:243],v[212:215]
ds_read_b64_tr_b16 a[246:247],v2 offset:13056
v_mfma_f32_16x16x32_bf16 v[220:223],a[188:191],v[240:243],v[40:43]
ds_read_b64_tr_b16 a[252:253],v3 offset:8960
v_mfma_f32_16x16x32_bf16 v[220:223],a[184:187],v[208:211],v[220:223]
ds_read_b64_tr_b16 a[254:255],v3 offset:13056
v_mfma_f32_16x16x32_bf16 v[220:223],a[180:183],v[176:179],v[220:223]
ds_read_b64_tr_b16 a[248:249],v3 offset:768
v_mfma_f32_16x16x32_bf16 v[220:223],a[176:179],v[144:147],v[220:223]
s_waitcnt lgkmcnt(7)
ds_read_b64_tr_b16 a[250:251],v3 offset:4864
v_mfma_f32_16x16x32_bf16 a[0:3],a[192:195],v[132:135],a[0:3]
buffer_load_dword v26,v24,s[88:91],s93 offen
v_mfma_f32_16x16x32_bf16 a[0:3],a[196:199],v[148:151],a[0:3]
v_mfma_f32_16x16x32_bf16 a[4:7],a[204:207],v[148:151],a[4:7]
v_mfma_f32_16x16x32_bf16 a[4:7],a[200:203],v[132:135],a[4:7]
v_mfma_f32_16x16x32_bf16 a[8:11],a[208:211],v[132:135],a[8:11]
v_mfma_f32_16x16x32_bf16 a[8:11],a[212:215],v[148:151],a[8:11]
v_mfma_f32_16x16x32_bf16 a[12:15],a[220:223],v[148:151],a[12:15]
v_mfma_f32_16x16x32_bf16 a[12:15],a[216:219],v[132:135],a[12:15]
v_mfma_f32_16x16x32_bf16 a[16:19],a[224:227],v[132:135],a[16:19]
s_bitcmp1_b32 s45,0
s_cbranch_scc0 L_t31v11_%=
s_mov_b32 m0,s80
s_add_u32 s80,s80,4096
buffer_load_dwordx4 v25,s[24:27],s83 offen nt lds
s_add_u32 s83,s83,4096
L_t31v11_%=:
v_mfma_f32_16x16x32_bf16 a[16:19],a[228:231],v[148:151],a[16:19]
v_mfma_f32_16x16x32_bf16 a[20:23],a[236:239],v[148:151],a[20:23]
v_mfma_f32_16x16x32_bf16 a[20:23],a[232:235],v[132:135],a[20:23]
s_waitcnt lgkmcnt(0)
v_mfma_f32_16x16x32_bf16 a[24:27],a[240:243],v[132:135],a[24:27]
v_mfma_f32_16x16x32_bf16 a[24:27],a[244:247],v[148:151],a[24:27]
v_mfma_f32_16x16x32_bf16 a[28:31],a[252:255],v[148:151],a[28:31]
v_mfma_f32_16x16x32_bf16 a[28:31],a[248:251],v[132:135],a[28:31]
v_mfma_f32_16x16x32_bf16 v[48:51],v[28:31],v[132:135],v[48:51]
s_cmp_lg_u32 s53,0
v_mfma_f32_16x16x32_bf16 v[48:51],v[28:31],v[148:151],v[48:51]
s_cbranch_scc1 L_att1v1_%=
s_waitcnt lgkmcnt(0)
s_nop 7
s_branch L_att1x_%=
.p2align 5
L_att1v2_%=:
v_mfma_f32_16x16x32_bf16 v[228:231],a[128:131],v[152:155],v[44:47]
s_waitcnt vmcnt(8) lgkmcnt(0)
v_mfma_f32_16x16x32_bf16 v[228:231],a[132:135],v[184:187],v[228:231]
s_barrier
s_add_u32 m0,s49,s51
ds_read_b128 a[128:131],v1
v_mfma_f32_16x16x32_bf16 v[228:231],a[136:139],v[216:219],v[228:231]
s_mov_b32 s90,L_out_end_%=-L_pc_%=+128
v_mfma_f32_16x16x32_bf16 v[228:231],a[140:143],v[248:251],v[228:231]
s_lshr_b32 s45,s45,1
ds_read_b128 a[132:135],v1 offset:256
v_mfma_f32_16x16x32_bf16 v[236:239],a[156:159],v[248:251],v[44:47]
s_add_u32 s62,s50,32768
buffer_load_dwordx4 v4,s[20:23],s47 offen nt lds
v_mfma_f32_16x16x32_bf16 v[236:239],a[152:155],v[216:219],v[236:239]
s_and_b32 s62,s62,131071
ds_read_b128 a[136:139],v1 offset:512
v_add_u32 v2,s48,v2
v_mfma_f32_16x16x32_bf16 v[236:239],a[148:151],v[184:187],v[236:239]
s_add_u32 s54,s54,16384
v_mfma_f32_16x16x32_bf16 v[236:239],a[144:147],v[152:155],v[236:239]
s_sub_u32 s53,s53,1
ds_read_b128 a[140:143],v1 offset:768
v_add_u32 v3,s48,v3
v_mfma_f32_16x16x32_bf16 v[244:247],a[160:163],v[152:155],v[44:47]
s_sub_u32 s48,s62,s50
v_mfma_f32_16x16x32_bf16 v[244:247],a[164:167],v[184:187],v[244:247]
s_and_b32 s46,s54,0xfffc0000
ds_read_b128 a[156:159],v1 offset:4864
v_mfma_f32_16x16x32_bf16 v[244:247],a[168:171],v[216:219],v[244:247]
s_mov_b32 s50,s62
v_mfma_f32_16x16x32_bf16 v[244:247],a[172:175],v[248:251],v[244:247]
ds_read_b128 a[152:155],v1 offset:4608
v_mfma_f32_16x16x32_bf16 v[252:255],a[188:191],v[248:251],v[44:47]
v_mfma_f32_16x16x32_bf16 v[252:255],a[184:187],v[216:219],v[252:255]
ds_read_b128 a[148:151],v1 offset:4352
v_mfma_f32_16x16x32_bf16 v[252:255],a[180:183],v[184:187],v[252:255]
v_mfma_f32_16x16x32_bf16 v[252:255],a[176:179],v[152:155],v[252:255]
ds_read_b128 a[144:147],v1 offset:4096
v_mfma_f32_16x16x32_bf16 a[32:35],a[192:195],v[164:167],a[32:35]
v_mfma_f32_16x16x32_bf16 a[32:35],a[196:199],v[180:183],a[32:35]
ds_read_b128 a[160:163],v1 offset:8192
v_mfma_f32_16x16x32_bf16 a[36:39],a[204:207],v[180:183],a[36:39]
buffer_load_dwordx4 v4,s[20:23],s47 offen offset:1024 nt lds
v_mfma_f32_16x16x32_bf16 a[36:39],a[200:203],v[164:167],a[36:39]
ds_read_b128 a[164:167],v1 offset:8448
v_mfma_f32_16x16x32_bf16 a[40:43],a[208:211],v[164:167],a[40:43]
v_mfma_f32_16x16x32_bf16 a[40:43],a[212:215],v[180:183],a[40:43]
ds_read_b128 a[168:171],v1 offset:8704
v_mfma_f32_16x16x32_bf16 a[44:47],a[220:223],v[180:183],a[44:47]
v_mfma_f32_16x16x32_bf16 a[44:47],a[216:219],v[164:167],a[44:47]
ds_read_b128 a[172:175],v1 offset:8960
v_mfma_f32_16x16x32_bf16 a[48:51],a[224:227],v[164:167],a[48:51]
v_mfma_f32_16x16x32_bf16 a[48:51],a[228:231],v[180:183],a[48:51]
ds_read_b128 a[188:191],v1 offset:13056
v_mfma_f32_16x16x32_bf16 a[52:55],a[236:239],v[180:183],a[52:55]
v_mfma_f32_16x16x32_bf16 a[52:55],a[232:235],v[164:167],a[52:55]
ds_read_b128 a[184:187],v1 offset:12800
v_mfma_f32_16x16x32_bf16 a[56:59],a[240:243],v[164:167],a[56:59]
v_mfma_f32_16x16x32_bf16 a[56:59],a[244:247],v[180:183],a[56:59]
ds_read_b128 a[180:183],v1 offset:12544
v_mfma_f32_16x16x32_bf16 a[60:63],a[252:255],v[180:183],a[60:63]
v_mfma_f32_16x16x32_bf16 a[60:63],a[248:251],v[164:167],a[60:63]
ds_read_b128 a[176:179],v1 offset:12288
v_mfma_f32_16x16x32_bf16 v[52:55],v[28:31],v[164:167],v[52:55]
buffer_load_dwordx4 v5,s[20:23],s47 offen offset:2048 nt lds
v_mfma_f32_16x16x32_bf16 v[52:55],v[28:31],v[180:183],v[52:55]
s_waitcnt lgkmcnt(3)
v_mfma_f32_16x16x32_bf16 v[132:135],a[128:131],v[128:131],v[32:35]
v_mfma_f32_16x16x32_bf16 v[132:135],a[132:135],v[160:163],v[132:135]
v_add_u32 v1,s48,v1
v_mfma_f32_16x16x32_bf16 v[132:135],a[136:139],v[192:195],v[132:135]
v_exp_f32 v228,v228
v_mfma_f32_16x16x32_bf16 v[132:135],a[140:143],v[224:227],v[132:135]
v_exp_f32 v229,v229
v_mfma_f32_16x16x32_bf16 v[140:143],a[156:159],v[224:227],v[32:35]
v_exp_f32 v230,v230
v_mfma_f32_16x16x32_bf16 v[140:143],a[152:155],v[192:195],v[140:143]
v_exp_f32 v231,v231
v_mfma_f32_16x16x32_bf16 v[140:143],a[148:151],v[160:163],v[140:143]
v_cvt_pk_bf16_f32 v228,v228,v229
v_cvt_pk_bf16_f32 v229,v230,v231
v_mfma_f32_16x16x32_bf16 v[140:143],a[144:147],v[128:131],v[140:143]
v_exp_f32 v236,v236
v_mfma_f32_16x16x32_bf16 v[148:151],a[160:163],v[128:131],v[32:35]
v_exp_f32 v237,v237
v_mfma_f32_16x16x32_bf16 v[148:151],a[164:167],v[160:163],v[148:151]
v_exp_f32 v238,v238
v_mfma_f32_16x16x32_bf16 v[148:151],a[168:171],v[192:195],v[148:151]
v_exp_f32 v239,v239
v_mfma_f32_16x16x32_bf16 v[148:151],a[172:175],v[224:227],v[148:151]
v_exp_f32 v244,v244
v_mfma_f32_16x16x32_bf16 v[156:159],a[188:191],v[224:227],v[32:35]
s_waitcnt lgkmcnt(0)
buffer_load_dwordx4 v5,s[20:23],s47 offen offset:3072 nt lds
v_mfma_f32_16x16x32_bf16 v[156:159],a[184:187],v[192:195],v[156:159]
v_exp_f32 v245,v245
v_mfma_f32_16x16x32_bf16 v[156:159],a[180:183],v[160:163],v[156:159]
v_exp_f32 v246,v246
v_mfma_f32_16x16x32_bf16 v[156:159],a[176:179],v[128:131],v[156:159]
v_exp_f32 v247,v247
v_cvt_pk_bf16_f32 v230,v236,v237
v_cvt_pk_bf16_f32 v231,v238,v239
v_cvt_pk_bf16_f32 v244,v244,v245
v_cvt_pk_bf16_f32 v245,v246,v247
v_exp_f32 v252,v252
v_exp_f32 v253,v253
v_exp_f32 v254,v254
v_exp_f32 v255,v255
v_cvt_pk_bf16_f32 v246,v252,v253
v_cvt_pk_bf16_f32 v247,v254,v255
s_add_u32 m0,s49,s52
v_exp_f32 v132,v132
s_xor_b32 s49,s50,65536
v_exp_f32 v133,v133
v_exp_f32 v134,v134
buffer_load_dwordx4 v4,s[24:27],s47 offen nt lds
v_exp_f32 v135,v135
v_cvt_pk_bf16_f32 v132,v132,v133
v_cvt_pk_bf16_f32 v133,v134,v135
v_exp_f32 v140,v140
v_exp_f32 v141,v141
v_exp_f32 v142,v142
v_exp_f32 v143,v143
v_exp_f32 v148,v148
v_mfma_f32_16x16x32_bf16 v[164:167],a[128:131],v[136:139],v[36:39]
v_exp_f32 v149,v149
v_mfma_f32_16x16x32_bf16 v[164:167],a[132:135],v[168:171],v[164:167]
v_exp_f32 v150,v150
v_mfma_f32_16x16x32_bf16 v[164:167],a[136:139],v[200:203],v[164:167]
v_exp_f32 v151,v151
v_mfma_f32_16x16x32_bf16 v[164:167],a[140:143],v[232:235],v[164:167]
v_cvt_pk_bf16_f32 v134,v140,v141
v_cvt_pk_bf16_f32 v135,v142,v143
v_mfma_f32_16x16x32_bf16 v[172:175],a[156:159],v[232:235],v[36:39]
)A"
R"A(v_cvt_pk_bf16_f32 v148,v148,v149
v_cvt_pk_bf16_f32 v149,v150,v151
v_mfma_f32_16x16x32_bf16 v[172:175],a[152:155],v[200:203],v[172:175]
v_exp_f32 v156,v156
v_mfma_f32_16x16x32_bf16 v[172:175],a[148:151],v[168:171],v[172:175]
buffer_load_dwordx4 v5,s[24:27],s47 offen offset:1024 nt lds
v_mfma_f32_16x16x32_bf16 v[172:175],a[144:147],v[136:139],v[172:175]
v_exp_f32 v157,v157
v_mfma_f32_16x16x32_bf16 v[180:183],a[160:163],v[136:139],v[36:39]
v_exp_f32 v158,v158
v_mfma_f32_16x16x32_bf16 v[180:183],a[164:167],v[168:171],v[180:183]
v_exp_f32 v159,v159
v_mfma_f32_16x16x32_bf16 v[180:183],a[168:171],v[200:203],v[180:183]
v_cvt_pk_bf16_f32 v150,v156,v157
v_cvt_pk_bf16_f32 v151,v158,v159
v_mfma_f32_16x16x32_bf16 v[180:183],a[172:175],v[232:235],v[180:183]
v_exp_f32 v164,v164
v_mfma_f32_16x16x32_bf16 v[188:191],a[188:191],v[232:235],v[36:39]
v_exp_f32 v165,v165
v_mfma_f32_16x16x32_bf16 v[188:191],a[184:187],v[200:203],v[188:191]
v_exp_f32 v166,v166
v_mfma_f32_16x16x32_bf16 v[188:191],a[180:183],v[168:171],v[188:191]
v_exp_f32 v167,v167
v_mfma_f32_16x16x32_bf16 v[188:191],a[176:179],v[136:139],v[188:191]
v_cvt_pk_bf16_f32 v164,v164,v165
v_cvt_pk_bf16_f32 v165,v166,v167
v_mfma_f32_16x16x32_bf16 a[96:99],a[192:195],v[228:231],a[96:99]
ds_read_b64_tr_b16 a[192:193],v2
v_mfma_f32_16x16x32_bf16 a[96:99],a[196:199],v[244:247],a[96:99]
ds_read_b64_tr_b16 a[194:195],v2 offset:4096
v_mfma_f32_16x16x32_bf16 a[100:103],a[204:207],v[244:247],a[100:103]
ds_read_b64_tr_b16 a[196:197],v2 offset:8192
v_mfma_f32_16x16x32_bf16 a[100:103],a[200:203],v[228:231],a[100:103]
ds_read_b64_tr_b16 a[198:199],v2 offset:12288
v_mfma_f32_16x16x32_bf16 a[104:107],a[208:211],v[228:231],a[104:107]
buffer_load_dwordx4 v4,s[24:27],s47 offen offset:2048 nt lds
v_mfma_f32_16x16x32_bf16 a[104:107],a[212:215],v[244:247],a[104:107]
ds_read_b64_tr_b16 a[204:205],v3 offset:8192
v_mfma_f32_16x16x32_bf16 a[108:111],a[220:223],v[244:247],a[108:111]
ds_read_b64_tr_b16 a[206:207],v3 offset:12288
v_mfma_f32_16x16x32_bf16 a[108:111],a[216:219],v[228:231],a[108:111]
ds_read_b64_tr_b16 a[200:201],v3
v_mfma_f32_16x16x32_bf16 a[112:115],a[224:227],v[228:231],a[112:115]
ds_read_b64_tr_b16 a[202:203],v3 offset:4096
v_mfma_f32_16x16x32_bf16 a[112:115],a[228:231],v[244:247],a[112:115]
ds_read_b64_tr_b16 a[208:209],v2 offset:256
v_mfma_f32_16x16x32_bf16 a[116:119],a[236:239],v[244:247],a[116:119]
ds_read_b64_tr_b16 a[210:211],v2 offset:4352
v_mfma_f32_16x16x32_bf16 a[116:119],a[232:235],v[228:231],a[116:119]
ds_read_b64_tr_b16 a[212:213],v2 offset:8448
v_mfma_f32_16x16x32_bf16 a[120:123],a[240:243],v[228:231],a[120:123]
ds_read_b64_tr_b16 a[214:215],v2 offset:12544
v_mfma_f32_16x16x32_bf16 a[120:123],a[244:247],v[244:247],a[120:123]
ds_read_b64_tr_b16 a[220:221],v3 offset:8448
v_mfma_f32_16x16x32_bf16 a[124:127],a[252:255],v[244:247],a[124:127]
ds_read_b64_tr_b16 a[222:223],v3 offset:12544
v_mfma_f32_16x16x32_bf16 a[124:127],a[248:251],v[228:231],a[124:127]
ds_read_b64_tr_b16 a[216:217],v3 offset:256
v_mfma_f32_16x16x32_bf16 v[60:63],v[28:31],v[228:231],v[60:63]
ds_read_b64_tr_b16 a[218:219],v3 offset:4352
v_mfma_f32_16x16x32_bf16 v[60:63],v[28:31],v[244:247],v[60:63]
s_waitcnt lgkmcnt(7)
ds_read_b64_tr_b16 a[224:225],v2 offset:512
buffer_load_dwordx4 v5,s[24:27],s47 offen offset:3072 nt lds
s_add_u32 s47,s47,16384
s_and_b32 s47,s47,0x3ffff
ds_read_b64_tr_b16 a[226:227],v2 offset:4608
s_or_b32 s47,s47,s46
ds_read_b64_tr_b16 a[228:229],v2 offset:8704
ds_read_b64_tr_b16 a[230:231],v2 offset:12800
ds_read_b64_tr_b16 a[236:237],v3 offset:8704
ds_read_b64_tr_b16 a[238:239],v3 offset:12800
ds_read_b64_tr_b16 a[232:233],v3 offset:512
ds_read_b64_tr_b16 a[234:235],v3 offset:4608
ds_read_b64_tr_b16 a[240:241],v2 offset:768
ds_read_b64_tr_b16 a[242:243],v2 offset:4864
ds_read_b64_tr_b16 a[244:245],v2 offset:8960
ds_read_b64_tr_b16 a[246:247],v2 offset:13056
ds_read_b64_tr_b16 a[252:253],v3 offset:8960
ds_read_b64_tr_b16 a[254:255],v3 offset:13056
ds_read_b64_tr_b16 a[248:249],v3 offset:768
ds_read_b64_tr_b16 a[250:251],v3 offset:4864
v_mfma_f32_16x16x32_bf16 a[0:3],a[192:195],v[132:135],a[0:3]
buffer_load_dword v26,v24,s[88:91],s93 offen
v_mfma_f32_16x16x32_bf16 a[0:3],a[196:199],v[148:151],a[0:3]
v_exp_f32 v180,v180
v_mfma_f32_16x16x32_bf16 a[4:7],a[204:207],v[148:151],a[4:7]
v_exp_f32 v181,v181
v_mfma_f32_16x16x32_bf16 a[4:7],a[200:203],v[132:135],a[4:7]
s_waitcnt lgkmcnt(15)
v_exp_f32 v182,v182
v_mfma_f32_16x16x32_bf16 a[8:11],a[208:211],v[132:135],a[8:11]
v_exp_f32 v183,v183
v_mfma_f32_16x16x32_bf16 a[8:11],a[212:215],v[148:151],a[8:11]
v_exp_f32 v172,v172
v_mfma_f32_16x16x32_bf16 a[12:15],a[220:223],v[148:151],a[12:15]
v_exp_f32 v173,v173
v_mfma_f32_16x16x32_bf16 a[12:15],a[216:219],v[132:135],a[12:15]
s_waitcnt lgkmcnt(0)
v_exp_f32 v174,v174
v_mfma_f32_16x16x32_bf16 a[16:19],a[224:227],v[132:135],a[16:19]
s_bitcmp1_b32 s45,0
s_cbranch_scc0 L_t31v21_%=
s_mov_b32 m0,s80
s_add_u32 s80,s80,4096
buffer_load_dwordx4 v25,s[24:27],s83 offen nt lds
s_add_u32 s83,s83,4096
L_t31v21_%=:
v_mfma_f32_16x16x32_bf16 a[16:19],a[228:231],v[148:151],a[16:19]
v_exp_f32 v175,v175
v_mfma_f32_16x16x32_bf16 a[20:23],a[236:239],v[148:151],a[20:23]
v_cvt_pk_bf16_f32 v180,v180,v181
v_cvt_pk_bf16_f32 v181,v182,v183
v_mfma_f32_16x16x32_bf16 a[20:23],a[232:235],v[132:135],a[20:23]
v_exp_f32 v188,v188
v_mfma_f32_16x16x32_bf16 a[24:27],a[240:243],v[132:135],a[24:27]
v_exp_f32 v189,v189
v_mfma_f32_16x16x32_bf16 a[24:27],a[244:247],v[148:151],a[24:27]
v_exp_f32 v190,v190
v_mfma_f32_16x16x32_bf16 a[28:31],a[252:255],v[148:151],a[28:31]
v_cvt_pk_bf16_f32 v166,v172,v173
v_mfma_f32_16x16x32_bf16 a[28:31],a[248:251],v[132:135],a[28:31]
v_exp_f32 v191,v191
v_mfma_f32_16x16x32_bf16 v[48:51],v[28:31],v[132:135],v[48:51]
v_cvt_pk_bf16_f32 v167,v174,v175
v_cvt_pk_bf16_f32 v182,v188,v189
v_cvt_pk_bf16_f32 v183,v190,v191
s_cmp_lg_u32 s53,0
v_mfma_f32_16x16x32_bf16 v[48:51],v[28:31],v[148:151],v[48:51]
s_cbranch_scc1 L_att1v2_%=
s_waitcnt lgkmcnt(0)
s_nop 7
s_branch L_att1x_%=
.p2align 5
L_att1_%=:
v_mfma_f32_16x16x32_bf16 v[228:231],a[128:131],v[152:155],v[44:47]
s_waitcnt vmcnt(8) lgkmcnt(0)
v_exp_f32 v196,v196
v_mfma_f32_16x16x32_bf16 v[228:231],a[132:135],v[184:187],v[228:231]
s_barrier
s_add_u32 m0,s49,s51
ds_read_b128 a[128:131],v1
v_mfma_f32_16x16x32_bf16 v[228:231],a[136:139],v[216:219],v[228:231]
s_mov_b32 s90,L_out_end_%=-L_pc_%=+128
v_exp_f32 v197,v197
v_mfma_f32_16x16x32_bf16 v[228:231],a[140:143],v[248:251],v[228:231]
s_lshr_b32 s45,s45,1
ds_read_b128 a[132:135],v1 offset:256
v_cvt_pk_bf16_f32 v196,v196,v197
v_mfma_f32_16x16x32_bf16 v[236:239],a[156:159],v[248:251],v[44:47]
s_add_u32 s62,s50,32768
buffer_load_dwordx4 v4,s[20:23],s47 offen nt lds
v_mfma_f32_16x16x32_bf16 v[236:239],a[152:155],v[216:219],v[236:239]
s_and_b32 s62,s62,131071
ds_read_b128 a[136:139],v1 offset:512
v_add_u32 v2,s48,v2
v_mfma_f32_16x16x32_bf16 v[236:239],a[148:151],v[184:187],v[236:239]
s_add_u32 s54,s54,16384
v_exp_f32 v198,v198
v_mfma_f32_16x16x32_bf16 v[236:239],a[144:147],v[152:155],v[236:239]
s_sub_u32 s53,s53,1
ds_read_b128 a[140:143],v1 offset:768
v_add_u32 v3,s48,v3
v_mfma_f32_16x16x32_bf16 v[244:247],a[160:163],v[152:155],v[44:47]
s_sub_u32 s48,s62,s50
v_exp_f32 v199,v199
v_mfma_f32_16x16x32_bf16 v[244:247],a[164:167],v[184:187],v[244:247]
s_and_b32 s46,s54,0xfffc0000
ds_read_b128 a[156:159],v1 offset:4864
v_cvt_pk_bf16_f32 v197,v198,v199
v_mfma_f32_16x16x32_bf16 v[244:247],a[168:171],v[216:219],v[244:247]
s_mov_b32 s50,s62
v_exp_f32 v204,v204
v_mfma_f32_16x16x32_bf16 v[244:247],a[172:175],v[248:251],v[244:247]
ds_read_b128 a[152:155],v1 offset:4608
v_mfma_f32_16x16x32_bf16 v[252:255],a[188:191],v[248:251],v[44:47]
v_exp_f32 v205,v205
v_mfma_f32_16x16x32_bf16 v[252:255],a[184:187],v[216:219],v[252:255]
ds_read_b128 a[148:151],v1 offset:4352
v_cvt_pk_bf16_f32 v198,v204,v205
v_mfma_f32_16x16x32_bf16 v[252:255],a[180:183],v[184:187],v[252:255]
v_exp_f32 v206,v206
v_mfma_f32_16x16x32_bf16 v[252:255],a[176:179],v[152:155],v[252:255]
ds_read_b128 a[144:147],v1 offset:4096
v_mfma_f32_16x16x32_bf16 a[32:35],a[192:195],v[164:167],a[32:35]
v_exp_f32 v207,v207
v_mfma_f32_16x16x32_bf16 a[32:35],a[196:199],v[180:183],a[32:35]
ds_read_b128 a[160:163],v1 offset:8192
v_cvt_pk_bf16_f32 v199,v206,v207
v_mfma_f32_16x16x32_bf16 a[36:39],a[204:207],v[180:183],a[36:39]
buffer_load_dwordx4 v4,s[20:23],s47 offen offset:1024 nt lds
v_mfma_f32_16x16x32_bf16 a[36:39],a[200:203],v[164:167],a[36:39]
ds_read_b128 a[164:167],v1 offset:8448
v_mfma_f32_16x16x32_bf16 a[40:43],a[208:211],v[164:167],a[40:43]
v_exp_f32 v212,v212
v_mfma_f32_16x16x32_bf16 a[40:43],a[212:215],v[180:183],a[40:43]
ds_read_b128 a[168:171],v1 offset:8704
v_mfma_f32_16x16x32_bf16 a[44:47],a[220:223],v[180:183],a[44:47]
v_exp_f32 v213,v213
v_mfma_f32_16x16x32_bf16 a[44:47],a[216:219],v[164:167],a[44:47]
ds_read_b128 a[172:175],v1 offset:8960
v_cvt_pk_bf16_f32 v212,v212,v213
v_mfma_f32_16x16x32_bf16 a[48:51],a[224:227],v[164:167],a[48:51]
v_exp_f32 v214,v214
v_mfma_f32_16x16x32_bf16 a[48:51],a[228:231],v[180:183],a[48:51]
ds_read_b128 a[188:191],v1 offset:13056
v_mfma_f32_16x16x32_bf16 a[52:55],a[236:239],v[180:183],a[52:55]
v_exp_f32 v215,v215
v_mfma_f32_16x16x32_bf16 a[52:55],a[232:235],v[164:167],a[52:55]
ds_read_b128 a[184:187],v1 offset:12800
v_cvt_pk_bf16_f32 v213,v214,v215
v_mfma_f32_16x16x32_bf16 a[56:59],a[240:243],v[164:167],a[56:59]
v_exp_f32 v220,v220
v_mfma_f32_16x16x32_bf16 a[56:59],a[244:247],v[180:183],a[56:59]
ds_read_b128 a[180:183],v1 offset:12544
v_mfma_f32_16x16x32_bf16 a[60:63],a[252:255],v[180:183],a[60:63]
v_exp_f32 v221,v221
v_mfma_f32_16x16x32_bf16 a[60:63],a[248:251],v[164:167],a[60:63]
ds_read_b128 a[176:179],v1 offset:12288
v_cvt_pk_bf16_f32 v214,v220,v221
v_mfma_f32_16x16x32_bf16 v[52:55],v[28:31],v[164:167],v[52:55]
buffer_load_dwordx4 v5,s[20:23],s47 offen offset:2048 nt lds
v_mfma_f32_16x16x32_bf16 v[52:55],v[28:31],v[180:183],v[52:55]
s_waitcnt lgkmcnt(3)
v_exp_f32 v222,v222
v_mfma_f32_16x16x32_bf16 v[132:135],a[128:131],v[128:131],v[32:35]
v_exp_f32 v223,v223
v_mfma_f32_16x16x32_bf16 v[132:135],a[132:135],v[160:163],v[132:135]
v_cvt_pk_bf16_f32 v215,v222,v223
v_add_u32 v1,s48,v1
v_mfma_f32_16x16x32_bf16 v[132:135],a[136:139],v[192:195],v[132:135]
v_exp_f32 v228,v228
v_mfma_f32_16x16x32_bf16 v[132:135],a[140:143],v[224:227],v[132:135]
v_exp_f32 v229,v229
v_mfma_f32_16x16x32_bf16 v[140:143],a[156:159],v[224:227],v[32:35]
v_exp_f32 v230,v230
v_mfma_f32_16x16x32_bf16 v[140:143],a[152:155],v[192:195],v[140:143]
v_exp_f32 v231,v231
v_mfma_f32_16x16x32_bf16 v[140:143],a[148:151],v[160:163],v[140:143]
v_cvt_pk_bf16_f32 v228,v228,v229
v_cvt_pk_bf16_f32 v229,v230,v231
v_mfma_f32_16x16x32_bf16 v[140:143],a[144:147],v[128:131],v[140:143]
v_exp_f32 v236,v236
v_mfma_f32_16x16x32_bf16 v[148:151],a[160:163],v[128:131],v[32:35]
v_exp_f32 v237,v237
v_mfma_f32_16x16x32_bf16 v[148:151],a[164:167],v[160:163],v[148:151]
v_exp_f32 v238,v238
v_mfma_f32_16x16x32_bf16 v[148:151],a[168:171],v[192:195],v[148:151]
v_exp_f32 v239,v239
v_mfma_f32_16x16x32_bf16 v[148:151],a[172:175],v[224:227],v[148:151]
v_exp_f32 v244,v244
v_mfma_f32_16x16x32_bf16 v[156:159],a[188:191],v[224:227],v[32:35]
s_waitcnt lgkmcnt(0)
buffer_load_dwordx4 v5,s[20:23],s47 offen offset:3072 nt lds
v_mfma_f32_16x16x32_bf16 v[156:159],a[184:187],v[192:195],v[156:159]
v_exp_f32 v245,v245
v_mfma_f32_16x16x32_bf16 v[156:159],a[180:183],v[160:163],v[156:159]
v_exp_f32 v246,v246
v_mfma_f32_16x16x32_bf16 v[156:159],a[176:179],v[128:131],v[156:159]
v_exp_f32 v247,v247
v_mfma_f32_16x16x32_bf16 a[64:67],a[192:195],v[196:199],a[64:67]
v_cvt_pk_bf16_f32 v230,v236,v237
v_cvt_pk_bf16_f32 v231,v238,v239
v_mfma_f32_16x16x32_bf16 a[64:67],a[196:199],v[212:215],a[64:67]
v_cvt_pk_bf16_f32 v244,v244,v245
v_cvt_pk_bf16_f32 v245,v246,v247
v_mfma_f32_16x16x32_bf16 a[68:71],a[204:207],v[212:215],a[68:71]
v_exp_f32 v252,v252
v_mfma_f32_16x16x32_bf16 a[68:71],a[200:203],v[196:199],a[68:71]
v_exp_f32 v253,v253
v_mfma_f32_16x16x32_bf16 a[72:75],a[208:211],v[196:199],a[72:75]
v_exp_f32 v254,v254
v_mfma_f32_16x16x32_bf16 a[72:75],a[212:215],v[212:215],a[72:75]
)A"
R"A(v_exp_f32 v255,v255
v_mfma_f32_16x16x32_bf16 a[76:79],a[220:223],v[212:215],a[76:79]
v_cvt_pk_bf16_f32 v246,v252,v253
v_cvt_pk_bf16_f32 v247,v254,v255
v_mfma_f32_16x16x32_bf16 a[76:79],a[216:219],v[196:199],a[76:79]
s_add_u32 m0,s49,s52
v_exp_f32 v132,v132
v_mfma_f32_16x16x32_bf16 a[80:83],a[224:227],v[196:199],a[80:83]
s_xor_b32 s49,s50,65536
v_exp_f32 v133,v133
v_mfma_f32_16x16x32_bf16 a[80:83],a[228:231],v[212:215],a[80:83]
v_exp_f32 v134,v134
v_mfma_f32_16x16x32_bf16 a[84:87],a[236:239],v[212:215],a[84:87]
buffer_load_dwordx4 v4,s[24:27],s47 offen nt lds
v_mfma_f32_16x16x32_bf16 a[84:87],a[232:235],v[196:199],a[84:87]
v_exp_f32 v135,v135
v_mfma_f32_16x16x32_bf16 a[88:91],a[240:243],v[196:199],a[88:91]
v_cvt_pk_bf16_f32 v132,v132,v133
v_cvt_pk_bf16_f32 v133,v134,v135
v_mfma_f32_16x16x32_bf16 a[88:91],a[244:247],v[212:215],a[88:91]
v_exp_f32 v140,v140
v_mfma_f32_16x16x32_bf16 a[92:95],a[252:255],v[212:215],a[92:95]
v_exp_f32 v141,v141
v_mfma_f32_16x16x32_bf16 a[92:95],a[248:251],v[196:199],a[92:95]
v_exp_f32 v142,v142
v_mfma_f32_16x16x32_bf16 v[56:59],v[28:31],v[196:199],v[56:59]
v_exp_f32 v143,v143
v_mfma_f32_16x16x32_bf16 v[56:59],v[28:31],v[212:215],v[56:59]
v_exp_f32 v148,v148
v_mfma_f32_16x16x32_bf16 v[164:167],a[128:131],v[136:139],v[36:39]
v_exp_f32 v149,v149
v_mfma_f32_16x16x32_bf16 v[164:167],a[132:135],v[168:171],v[164:167]
v_exp_f32 v150,v150
v_mfma_f32_16x16x32_bf16 v[164:167],a[136:139],v[200:203],v[164:167]
v_exp_f32 v151,v151
v_mfma_f32_16x16x32_bf16 v[164:167],a[140:143],v[232:235],v[164:167]
v_cvt_pk_bf16_f32 v134,v140,v141
v_cvt_pk_bf16_f32 v135,v142,v143
v_mfma_f32_16x16x32_bf16 v[172:175],a[156:159],v[232:235],v[36:39]
v_cvt_pk_bf16_f32 v148,v148,v149
v_cvt_pk_bf16_f32 v149,v150,v151
v_mfma_f32_16x16x32_bf16 v[172:175],a[152:155],v[200:203],v[172:175]
v_exp_f32 v156,v156
v_mfma_f32_16x16x32_bf16 v[172:175],a[148:151],v[168:171],v[172:175]
buffer_load_dwordx4 v5,s[24:27],s47 offen offset:1024 nt lds
v_mfma_f32_16x16x32_bf16 v[172:175],a[144:147],v[136:139],v[172:175]
v_exp_f32 v157,v157
v_mfma_f32_16x16x32_bf16 v[180:183],a[160:163],v[136:139],v[36:39]
v_exp_f32 v158,v158
v_mfma_f32_16x16x32_bf16 v[180:183],a[164:167],v[168:171],v[180:183]
v_exp_f32 v159,v159
v_mfma_f32_16x16x32_bf16 v[180:183],a[168:171],v[200:203],v[180:183]
v_cvt_pk_bf16_f32 v150,v156,v157
v_cvt_pk_bf16_f32 v151,v158,v159
v_mfma_f32_16x16x32_bf16 v[180:183],a[172:175],v[232:235],v[180:183]
v_exp_f32 v164,v164
v_mfma_f32_16x16x32_bf16 v[188:191],a[188:191],v[232:235],v[36:39]
v_exp_f32 v165,v165
v_mfma_f32_16x16x32_bf16 v[188:191],a[184:187],v[200:203],v[188:191]
v_exp_f32 v166,v166
v_mfma_f32_16x16x32_bf16 v[188:191],a[180:183],v[168:171],v[188:191]
v_exp_f32 v167,v167
v_mfma_f32_16x16x32_bf16 v[188:191],a[176:179],v[136:139],v[188:191]
v_cvt_pk_bf16_f32 v164,v164,v165
v_cvt_pk_bf16_f32 v165,v166,v167
v_mfma_f32_16x16x32_bf16 a[96:99],a[192:195],v[228:231],a[96:99]
ds_read_b64_tr_b16 a[192:193],v2
v_mfma_f32_16x16x32_bf16 a[96:99],a[196:199],v[244:247],a[96:99]
ds_read_b64_tr_b16 a[194:195],v2 offset:4096
v_mfma_f32_16x16x32_bf16 a[100:103],a[204:207],v[244:247],a[100:103]
ds_read_b64_tr_b16 a[196:197],v2 offset:8192
v_mfma_f32_16x16x32_bf16 a[100:103],a[200:203],v[228:231],a[100:103]
ds_read_b64_tr_b16 a[198:199],v2 offset:12288
v_mfma_f32_16x16x32_bf16 a[104:107],a[208:211],v[228:231],a[104:107]
buffer_load_dwordx4 v4,s[24:27],s47 offen offset:2048 nt lds
v_mfma_f32_16x16x32_bf16 a[104:107],a[212:215],v[244:247],a[104:107]
ds_read_b64_tr_b16 a[204:205],v3 offset:8192
v_mfma_f32_16x16x32_bf16 a[108:111],a[220:223],v[244:247],a[108:111]
ds_read_b64_tr_b16 a[206:207],v3 offset:12288
v_mfma_f32_16x16x32_bf16 a[108:111],a[216:219],v[228:231],a[108:111]
ds_read_b64_tr_b16 a[200:201],v3
v_mfma_f32_16x16x32_bf16 a[112:115],a[224:227],v[228:231],a[112:115]
ds_read_b64_tr_b16 a[202:203],v3 offset:4096
v_mfma_f32_16x16x32_bf16 a[112:115],a[228:231],v[244:247],a[112:115]
ds_read_b64_tr_b16 a[208:209],v2 offset:256
v_mfma_f32_16x16x32_bf16 a[116:119],a[236:239],v[244:247],a[116:119]
ds_read_b64_tr_b16 a[210:211],v2 offset:4352
v_mfma_f32_16x16x32_bf16 a[116:119],a[232:235],v[228:231],a[116:119]
ds_read_b64_tr_b16 a[212:213],v2 offset:8448
v_mfma_f32_16x16x32_bf16 a[120:123],a[240:243],v[228:231],a[120:123]
ds_read_b64_tr_b16 a[214:215],v2 offset:12544
v_mfma_f32_16x16x32_bf16 a[120:123],a[244:247],v[244:247],a[120:123]
ds_read_b64_tr_b16 a[220:221],v3 offset:8448
v_mfma_f32_16x16x32_bf16 a[124:127],a[252:255],v[244:247],a[124:127]
ds_read_b64_tr_b16 a[222:223],v3 offset:12544
v_mfma_f32_16x16x32_bf16 a[124:127],a[248:251],v[228:231],a[124:127]
ds_read_b64_tr_b16 a[216:217],v3 offset:256
v_mfma_f32_16x16x32_bf16 v[60:63],v[28:31],v[228:231],v[60:63]
ds_read_b64_tr_b16 a[218:219],v3 offset:4352
v_mfma_f32_16x16x32_bf16 v[60:63],v[28:31],v[244:247],v[60:63]
ds_read_b64_tr_b16 a[224:225],v2 offset:512
v_mfma_f32_16x16x32_bf16 v[196:199],a[128:131],v[144:147],v[40:43]
buffer_load_dwordx4 v5,s[24:27],s47 offen offset:3072 nt lds
v_mfma_f32_16x16x32_bf16 v[196:199],a[132:135],v[176:179],v[196:199]
s_add_u32 s47,s47,16384
s_and_b32 s47,s47,0x3ffff
ds_read_b64_tr_b16 a[226:227],v2 offset:4608
v_mfma_f32_16x16x32_bf16 v[196:199],a[136:139],v[208:211],v[196:199]
s_or_b32 s47,s47,s46
ds_read_b64_tr_b16 a[228:229],v2 offset:8704
v_mfma_f32_16x16x32_bf16 v[196:199],a[140:143],v[240:243],v[196:199]
ds_read_b64_tr_b16 a[230:231],v2 offset:12800
v_mfma_f32_16x16x32_bf16 v[204:207],a[156:159],v[240:243],v[40:43]
ds_read_b64_tr_b16 a[236:237],v3 offset:8704
v_mfma_f32_16x16x32_bf16 v[204:207],a[152:155],v[208:211],v[204:207]
ds_read_b64_tr_b16 a[238:239],v3 offset:12800
v_mfma_f32_16x16x32_bf16 v[204:207],a[148:151],v[176:179],v[204:207]
ds_read_b64_tr_b16 a[232:233],v3 offset:512
v_mfma_f32_16x16x32_bf16 v[204:207],a[144:147],v[144:147],v[204:207]
ds_read_b64_tr_b16 a[234:235],v3 offset:4608
v_mfma_f32_16x16x32_bf16 v[212:215],a[160:163],v[144:147],v[40:43]
ds_read_b64_tr_b16 a[240:241],v2 offset:768
v_mfma_f32_16x16x32_bf16 v[212:215],a[164:167],v[176:179],v[212:215]
ds_read_b64_tr_b16 a[242:243],v2 offset:4864
v_mfma_f32_16x16x32_bf16 v[212:215],a[168:171],v[208:211],v[212:215]
ds_read_b64_tr_b16 a[244:245],v2 offset:8960
v_mfma_f32_16x16x32_bf16 v[212:215],a[172:175],v[240:243],v[212:215]
ds_read_b64_tr_b16 a[246:247],v2 offset:13056
v_mfma_f32_16x16x32_bf16 v[220:223],a[188:191],v[240:243],v[40:43]
ds_read_b64_tr_b16 a[252:253],v3 offset:8960
v_mfma_f32_16x16x32_bf16 v[220:223],a[184:187],v[208:211],v[220:223]
ds_read_b64_tr_b16 a[254:255],v3 offset:13056
v_mfma_f32_16x16x32_bf16 v[220:223],a[180:183],v[176:179],v[220:223]
ds_read_b64_tr_b16 a[248:249],v3 offset:768
v_mfma_f32_16x16x32_bf16 v[220:223],a[176:179],v[144:147],v[220:223]
s_waitcnt lgkmcnt(7)
ds_read_b64_tr_b16 a[250:251],v3 offset:4864
v_mfma_f32_16x16x32_bf16 a[0:3],a[192:195],v[132:135],a[0:3]
buffer_load_dword v26,v24,s[88:91],s93 offen
v_mfma_f32_16x16x32_bf16 a[0:3],a[196:199],v[148:151],a[0:3]
v_exp_f32 v180,v180
v_mfma_f32_16x16x32_bf16 a[4:7],a[204:207],v[148:151],a[4:7]
v_exp_f32 v181,v181
v_mfma_f32_16x16x32_bf16 a[4:7],a[200:203],v[132:135],a[4:7]
v_exp_f32 v182,v182
v_mfma_f32_16x16x32_bf16 a[8:11],a[208:211],v[132:135],a[8:11]
s_memrealtime s[76:77]
v_exp_f32 v183,v183
v_mfma_f32_16x16x32_bf16 a[8:11],a[212:215],v[148:151],a[8:11]
v_exp_f32 v172,v172
v_mfma_f32_16x16x32_bf16 a[12:15],a[220:223],v[148:151],a[12:15]
v_exp_f32 v173,v173
v_mfma_f32_16x16x32_bf16 a[12:15],a[216:219],v[132:135],a[12:15]
v_exp_f32 v174,v174
v_mfma_f32_16x16x32_bf16 a[16:19],a[224:227],v[132:135],a[16:19]
s_bitcmp1_b32 s45,0
s_cbranch_scc0 L_t311_%=
s_mov_b32 m0,s80
s_add_u32 s80,s80,4096
buffer_load_dwordx4 v25,s[24:27],s83 offen nt lds
s_add_u32 s83,s83,4096
L_t311_%=:
v_mfma_f32_16x16x32_bf16 a[16:19],a[228:231],v[148:151],a[16:19]
v_exp_f32 v175,v175
v_mfma_f32_16x16x32_bf16 a[20:23],a[236:239],v[148:151],a[20:23]
v_cvt_pk_bf16_f32 v180,v180,v181
v_cvt_pk_bf16_f32 v181,v182,v183
v_mfma_f32_16x16x32_bf16 a[20:23],a[232:235],v[132:135],a[20:23]
s_waitcnt lgkmcnt(0)
v_exp_f32 v188,v188
v_mfma_f32_16x16x32_bf16 a[24:27],a[240:243],v[132:135],a[24:27]
s_sub_u32 s79,s78,s76
v_exp_f32 v189,v189
v_mfma_f32_16x16x32_bf16 a[24:27],a[244:247],v[148:151],a[24:27]
s_add_u32 s78,s76,s60
v_exp_f32 v190,v190
v_mfma_f32_16x16x32_bf16 a[28:31],a[252:255],v[148:151],a[28:31]
s_cmp_gt_i32 s79,0
s_cbranch_scc1 L_ps7_%=
L_pr7_%=:
v_cvt_pk_bf16_f32 v166,v172,v173
v_mfma_f32_16x16x32_bf16 a[28:31],a[248:251],v[132:135],a[28:31]
v_exp_f32 v191,v191
v_mfma_f32_16x16x32_bf16 v[48:51],v[28:31],v[132:135],v[48:51]
v_cvt_pk_bf16_f32 v167,v174,v175
v_cvt_pk_bf16_f32 v182,v188,v189
v_cvt_pk_bf16_f32 v183,v190,v191
s_cmp_lg_u32 s53,0
v_mfma_f32_16x16x32_bf16 v[48:51],v[28:31],v[148:151],v[48:51]
s_cbranch_scc1 L_att1_%=
L_att1x_%=:
L_endseg_%=:
v_mfma_f32_16x16x32_bf16 v[228:231],a[128:131],v[152:155],v[44:47]
s_waitcnt vmcnt(10)
s_lshl_b32 s43,s41,2
v_exp_f32 v196,v196
v_mfma_f32_16x16x32_bf16 v[228:231],a[132:135],v[184:187],v[228:231]
s_barrier
ds_read_b128 a[128:131],v1
v_add_u32 v2,s48,v2
v_mfma_f32_16x16x32_bf16 v[228:231],a[136:139],v[216:219],v[228:231]
s_add_u32 s43,s43,s70
v_exp_f32 v197,v197
v_mfma_f32_16x16x32_bf16 v[228:231],a[140:143],v[248:251],v[228:231]
s_lshl_b32 s43,s43,19
ds_read_b128 a[132:135],v1 offset:256
v_cvt_pk_bf16_f32 v196,v196,v197
v_mfma_f32_16x16x32_bf16 v[236:239],a[156:159],v[248:251],v[44:47]
s_add_u32 s43,s43,s55
v_exp_f32 v198,v198
v_mfma_f32_16x16x32_bf16 v[236:239],a[152:155],v[216:219],v[236:239]
s_mov_b64 s[28:29],s[12:13]
ds_read_b128 a[136:139],v1 offset:512
v_add_u32 v3,s48,v3
v_mfma_f32_16x16x32_bf16 v[236:239],a[148:151],v[184:187],v[236:239]
s_mov_b32 s48,32768
v_exp_f32 v199,v199
v_mfma_f32_16x16x32_bf16 v[236:239],a[144:147],v[152:155],v[236:239]
s_mov_b32 s30,475136
ds_read_b128 a[140:143],v1 offset:768
v_cvt_pk_bf16_f32 v197,v198,v199
v_mfma_f32_16x16x32_bf16 v[244:247],a[160:163],v[152:155],v[44:47]
s_mov_b32 s31,0x20000
v_exp_f32 v204,v204
v_mfma_f32_16x16x32_bf16 v[244:247],a[164:167],v[184:187],v[244:247]
ds_read_b128 a[156:159],v1 offset:4864
v_lshrrev_b32 v4,6,v6
v_mfma_f32_16x16x32_bf16 v[244:247],a[168:171],v[216:219],v[244:247]
v_exp_f32 v205,v205
v_mfma_f32_16x16x32_bf16 v[244:247],a[172:175],v[248:251],v[244:247]
ds_read_b128 a[152:155],v1 offset:4608
v_cvt_pk_bf16_f32 v198,v204,v205
v_mfma_f32_16x16x32_bf16 v[252:255],a[188:191],v[248:251],v[44:47]
v_exp_f32 v206,v206
v_mfma_f32_16x16x32_bf16 v[252:255],a[184:187],v[216:219],v[252:255]
ds_read_b128 a[148:151],v1 offset:4352
v_mul_u32_u24 v4,320,v4
v_mfma_f32_16x16x32_bf16 v[252:255],a[180:183],v[184:187],v[252:255]
v_exp_f32 v207,v207
v_mfma_f32_16x16x32_bf16 v[252:255],a[176:179],v[152:155],v[252:255]
ds_read_b128 a[144:147],v1 offset:4096
v_cvt_pk_bf16_f32 v199,v206,v207
v_mfma_f32_16x16x32_bf16 a[32:35],a[192:195],v[164:167],a[32:35]
v_exp_f32 v212,v212
v_mfma_f32_16x16x32_bf16 a[32:35],a[196:199],v[180:183],a[32:35]
ds_read_b128 a[160:163],v1 offset:8192
v_bfe_u32 v5,v6,4,2
v_mfma_f32_16x16x32_bf16 a[36:39],a[204:207],v[180:183],a[36:39]
v_exp_f32 v213,v213
v_mfma_f32_16x16x32_bf16 a[36:39],a[200:203],v[164:167],a[36:39]
ds_read_b128 a[164:167],v1 offset:8448
v_cvt_pk_bf16_f32 v212,v212,v213
v_mfma_f32_16x16x32_bf16 a[40:43],a[208:211],v[164:167],a[40:43]
v_exp_f32 v214,v214
v_mfma_f32_16x16x32_bf16 a[40:43],a[212:215],v[180:183],a[40:43]
ds_read_b128 a[168:171],v1 offset:8704
v_lshl_add_u32 v4,v5,3,v4
v_mfma_f32_16x16x32_bf16 a[44:47],a[220:223],v[180:183],a[44:47]
v_exp_f32 v215,v215
v_mfma_f32_16x16x32_bf16 a[44:47],a[216:219],v[164:167],a[44:47]
ds_read_b128 a[172:175],v1 offset:8960
v_cvt_pk_bf16_f32 v213,v214,v215
v_mfma_f32_16x16x32_bf16 a[48:51],a[224:227],v[164:167],a[48:51]
v_exp_f32 v220,v220
v_mfma_f32_16x16x32_bf16 a[48:51],a[228:231],v[180:183],a[48:51]
ds_read_b128 a[188:191],v1 offset:13056
v_bfe_u32 v5,v6,8,1
v_mfma_f32_16x16x32_bf16 a[52:55],a[236:239],v[180:183],a[52:55]
v_exp_f32 v221,v221
v_mfma_f32_16x16x32_bf16 a[52:55],a[232:235],v[164:167],a[52:55]
)A"
R"A(ds_read_b128 a[184:187],v1 offset:12800
v_cvt_pk_bf16_f32 v214,v220,v221
v_mfma_f32_16x16x32_bf16 a[56:59],a[240:243],v[164:167],a[56:59]
v_exp_f32 v222,v222
v_mfma_f32_16x16x32_bf16 a[56:59],a[244:247],v[180:183],a[56:59]
ds_read_b128 a[180:183],v1 offset:12544
v_lshlrev_b32 v5,5,v5
v_mfma_f32_16x16x32_bf16 a[60:63],a[252:255],v[180:183],a[60:63]
v_exp_f32 v223,v223
v_mfma_f32_16x16x32_bf16 a[60:63],a[248:251],v[164:167],a[60:63]
ds_read_b128 a[176:179],v1 offset:12288
v_cvt_pk_bf16_f32 v215,v222,v223
v_mfma_f32_16x16x32_bf16 v[52:55],v[28:31],v[164:167],v[52:55]
v_exp_f32 v228,v228
v_mfma_f32_16x16x32_bf16 v[52:55],v[28:31],v[180:183],v[52:55]
s_waitcnt lgkmcnt(3)
v_exp_f32 v229,v229
v_mfma_f32_16x16x32_bf16 v[132:135],a[128:131],v[128:131],v[32:35]
v_exp_f32 v230,v230
v_mfma_f32_16x16x32_bf16 v[132:135],a[132:135],v[160:163],v[132:135]
v_exp_f32 v231,v231
v_mfma_f32_16x16x32_bf16 v[132:135],a[136:139],v[192:195],v[132:135]
v_cvt_pk_bf16_f32 v228,v228,v229
v_cvt_pk_bf16_f32 v229,v230,v231
v_mfma_f32_16x16x32_bf16 v[132:135],a[140:143],v[224:227],v[132:135]
v_exp_f32 v236,v236
v_mfma_f32_16x16x32_bf16 v[140:143],a[156:159],v[224:227],v[32:35]
s_add_u32 s80,s71,0
s_and_b32 s80,s80,3
s_lshl_b32 s80,s80,1
v_mfma_f32_16x16x32_bf16 v[140:143],a[152:155],v[192:195],v[140:143]
s_mul_i32 s80,s80,163840
s_add_u32 s80,s80,s81
v_add_u32 v1,s48,v1
v_mfma_f32_16x16x32_bf16 v[140:143],a[148:151],v[160:163],v[140:143]
s_add_u32 s83,s80,20480
v_exp_f32 v237,v237
v_mfma_f32_16x16x32_bf16 v[140:143],a[144:147],v[128:131],v[140:143]
v_exp_f32 v238,v238
v_mfma_f32_16x16x32_bf16 v[148:151],a[160:163],v[128:131],v[32:35]
buffer_store_dwordx4 v[64:67],v6,s[36:39],s84 offen sc1
v_mfma_f32_16x16x32_bf16 v[148:151],a[164:167],v[160:163],v[148:151]
v_exp_f32 v239,v239
v_mfma_f32_16x16x32_bf16 v[148:151],a[168:171],v[192:195],v[148:151]
v_exp_f32 v244,v244
v_mfma_f32_16x16x32_bf16 v[148:151],a[172:175],v[224:227],v[148:151]
v_exp_f32 v245,v245
v_mfma_f32_16x16x32_bf16 v[156:159],a[188:191],v[224:227],v[32:35]
s_waitcnt lgkmcnt(0)
v_exp_f32 v246,v246
v_mfma_f32_16x16x32_bf16 v[156:159],a[184:187],v[192:195],v[156:159]
v_exp_f32 v247,v247
v_mfma_f32_16x16x32_bf16 v[156:159],a[180:183],v[160:163],v[156:159]
v_cvt_pk_bf16_f32 v230,v236,v237
v_cvt_pk_bf16_f32 v231,v238,v239
v_mfma_f32_16x16x32_bf16 v[156:159],a[176:179],v[128:131],v[156:159]
v_cvt_pk_bf16_f32 v244,v244,v245
v_cvt_pk_bf16_f32 v245,v246,v247
v_mfma_f32_16x16x32_bf16 a[64:67],a[192:195],v[196:199],a[64:67]
v_exp_f32 v252,v252
v_mfma_f32_16x16x32_bf16 a[64:67],a[196:199],v[212:215],a[64:67]
v_exp_f32 v253,v253
v_mfma_f32_16x16x32_bf16 a[68:71],a[204:207],v[212:215],a[68:71]
v_exp_f32 v254,v254
v_mfma_f32_16x16x32_bf16 a[68:71],a[200:203],v[196:199],a[68:71]
v_exp_f32 v255,v255
v_mfma_f32_16x16x32_bf16 a[72:75],a[208:211],v[196:199],a[72:75]
v_cvt_pk_bf16_f32 v246,v252,v253
v_cvt_pk_bf16_f32 v247,v254,v255
v_mfma_f32_16x16x32_bf16 a[72:75],a[212:215],v[212:215],a[72:75]
v_exp_f32 v132,v132
v_mfma_f32_16x16x32_bf16 a[76:79],a[220:223],v[212:215],a[76:79]
v_exp_f32 v133,v133
v_mfma_f32_16x16x32_bf16 a[76:79],a[216:219],v[196:199],a[76:79]
v_exp_f32 v134,v134
v_mfma_f32_16x16x32_bf16 a[80:83],a[224:227],v[196:199],a[80:83]
v_exp_f32 v135,v135
v_mfma_f32_16x16x32_bf16 a[80:83],a[228:231],v[212:215],a[80:83]
s_add_u32 m0,s82,81920
v_cvt_pk_bf16_f32 v132,v132,v133
v_cvt_pk_bf16_f32 v133,v134,v135
v_mfma_f32_16x16x32_bf16 a[84:87],a[236:239],v[212:215],a[84:87]
v_exp_f32 v140,v140
v_mfma_f32_16x16x32_bf16 a[84:87],a[232:235],v[196:199],a[84:87]
v_exp_f32 v141,v141
v_mfma_f32_16x16x32_bf16 a[88:91],a[240:243],v[196:199],a[88:91]
buffer_load_dwordx4 v7,s[72:75],s80 offen lds
v_mfma_f32_16x16x32_bf16 a[88:91],a[244:247],v[212:215],a[88:91]
v_exp_f32 v142,v142
v_mfma_f32_16x16x32_bf16 a[92:95],a[252:255],v[212:215],a[92:95]
v_exp_f32 v143,v143
v_mfma_f32_16x16x32_bf16 a[92:95],a[248:251],v[196:199],a[92:95]
v_exp_f32 v148,v148
v_mfma_f32_16x16x32_bf16 v[56:59],v[28:31],v[196:199],v[56:59]
buffer_store_dwordx4 v[68:71],v6,s[36:39],s84 offen offset:1024 sc1
v_mfma_f32_16x16x32_bf16 v[56:59],v[28:31],v[212:215],v[56:59]
v_exp_f32 v149,v149
v_mfma_f32_16x16x32_bf16 v[164:167],a[128:131],v[136:139],v[36:39]
v_exp_f32 v150,v150
v_mfma_f32_16x16x32_bf16 v[164:167],a[132:135],v[168:171],v[164:167]
v_exp_f32 v151,v151
v_mfma_f32_16x16x32_bf16 v[164:167],a[136:139],v[200:203],v[164:167]
v_cvt_pk_bf16_f32 v134,v140,v141
v_cvt_pk_bf16_f32 v135,v142,v143
v_mfma_f32_16x16x32_bf16 v[164:167],a[140:143],v[232:235],v[164:167]
v_cvt_pk_bf16_f32 v148,v148,v149
v_cvt_pk_bf16_f32 v149,v150,v151
v_mfma_f32_16x16x32_bf16 v[172:175],a[156:159],v[232:235],v[36:39]
v_exp_f32 v156,v156
v_mfma_f32_16x16x32_bf16 v[172:175],a[152:155],v[200:203],v[172:175]
v_exp_f32 v157,v157
v_mfma_f32_16x16x32_bf16 v[172:175],a[148:151],v[168:171],v[172:175]
v_exp_f32 v158,v158
v_mfma_f32_16x16x32_bf16 v[172:175],a[144:147],v[136:139],v[172:175]
v_exp_f32 v159,v159
v_mfma_f32_16x16x32_bf16 v[180:183],a[160:163],v[136:139],v[36:39]
v_cvt_pk_bf16_f32 v150,v156,v157
v_cvt_pk_bf16_f32 v151,v158,v159
v_mfma_f32_16x16x32_bf16 v[180:183],a[164:167],v[168:171],v[180:183]
v_exp_f32 v164,v164
v_mfma_f32_16x16x32_bf16 v[180:183],a[168:171],v[200:203],v[180:183]
v_exp_f32 v165,v165
v_mfma_f32_16x16x32_bf16 v[180:183],a[172:175],v[232:235],v[180:183]
v_exp_f32 v166,v166
v_mfma_f32_16x16x32_bf16 v[188:191],a[188:191],v[232:235],v[36:39]
v_exp_f32 v167,v167
v_mfma_f32_16x16x32_bf16 v[188:191],a[184:187],v[200:203],v[188:191]
v_cvt_pk_bf16_f32 v164,v164,v165
v_cvt_pk_bf16_f32 v165,v166,v167
v_mfma_f32_16x16x32_bf16 v[188:191],a[180:183],v[168:171],v[188:191]
buffer_load_dwordx4 v8,s[72:75],s80 offen offset:1024 lds
v_mfma_f32_16x16x32_bf16 v[188:191],a[176:179],v[136:139],v[188:191]
v_exp_f32 v172,v172
v_mfma_f32_16x16x32_bf16 a[96:99],a[192:195],v[228:231],a[96:99]
ds_read_b64_tr_b16 a[192:193],v2
ds_read_b64_tr_b16 a[194:195],v2 offset:4096
v_mfma_f32_16x16x32_bf16 a[96:99],a[196:199],v[244:247],a[96:99]
s_add_u32 s28,s28,s43
s_addc_u32 s29,s29,0
ds_read_b64_tr_b16 a[196:197],v2 offset:8192
v_mfma_f32_16x16x32_bf16 a[100:103],a[204:207],v[244:247],a[100:103]
s_and_b32 s29,s29,0xffff
buffer_store_dwordx4 v[72:75],v6,s[36:39],s84 offen offset:2048 sc1
v_mfma_f32_16x16x32_bf16 a[100:103],a[200:203],v[228:231],a[100:103]
ds_read_b64_tr_b16 a[198:199],v2 offset:12288
ds_read_b64_tr_b16 a[204:205],v3 offset:8192
v_mfma_f32_16x16x32_bf16 a[104:107],a[208:211],v[228:231],a[104:107]
ds_read_b64_tr_b16 a[206:207],v3 offset:12288
v_mfma_f32_16x16x32_bf16 a[104:107],a[212:215],v[244:247],a[104:107]
ds_read_b64_tr_b16 a[200:201],v3
v_mfma_f32_16x16x32_bf16 a[108:111],a[220:223],v[244:247],a[108:111]
ds_read_b64_tr_b16 a[202:203],v3 offset:4096
v_mfma_f32_16x16x32_bf16 a[108:111],a[216:219],v[228:231],a[108:111]
ds_read_b64_tr_b16 a[208:209],v2 offset:256
ds_read_b64_tr_b16 a[210:211],v2 offset:4352
v_mfma_f32_16x16x32_bf16 a[112:115],a[224:227],v[228:231],a[112:115]
ds_read_b64_tr_b16 a[212:213],v2 offset:8448
v_mfma_f32_16x16x32_bf16 a[112:115],a[228:231],v[244:247],a[112:115]
ds_read_b64_tr_b16 a[214:215],v2 offset:12544
v_mfma_f32_16x16x32_bf16 a[116:119],a[236:239],v[244:247],a[116:119]
ds_read_b64_tr_b16 a[220:221],v3 offset:8448
v_mfma_f32_16x16x32_bf16 a[116:119],a[232:235],v[228:231],a[116:119]
ds_read_b64_tr_b16 a[222:223],v3 offset:12544
ds_read_b64_tr_b16 a[216:217],v3 offset:256
v_mfma_f32_16x16x32_bf16 a[120:123],a[240:243],v[228:231],a[120:123]
ds_read_b64_tr_b16 a[218:219],v3 offset:4352
v_mfma_f32_16x16x32_bf16 a[120:123],a[244:247],v[244:247],a[120:123]
ds_read_b64_tr_b16 a[224:225],v2 offset:512
v_mfma_f32_16x16x32_bf16 a[124:127],a[252:255],v[244:247],a[124:127]
ds_read_b64_tr_b16 a[226:227],v2 offset:4608
v_mfma_f32_16x16x32_bf16 a[124:127],a[248:251],v[228:231],a[124:127]
ds_read_b64_tr_b16 a[228:229],v2 offset:8704
ds_read_b64_tr_b16 a[230:231],v2 offset:12800
v_mfma_f32_16x16x32_bf16 v[60:63],v[28:31],v[228:231],v[60:63]
ds_read_b64_tr_b16 a[236:237],v3 offset:8704
v_mfma_f32_16x16x32_bf16 v[60:63],v[28:31],v[244:247],v[60:63]
ds_read_b64_tr_b16 a[238:239],v3 offset:12800
v_mfma_f32_16x16x32_bf16 v[196:199],a[128:131],v[144:147],v[40:43]
buffer_load_dwordx4 v9,s[72:75],s80 offen offset:2048 lds
v_mfma_f32_16x16x32_bf16 v[196:199],a[132:135],v[176:179],v[196:199]
ds_read_b64_tr_b16 a[232:233],v3 offset:512
ds_read_b64_tr_b16 a[234:235],v3 offset:4608
v_mfma_f32_16x16x32_bf16 v[196:199],a[136:139],v[208:211],v[196:199]
ds_read_b64_tr_b16 a[240:241],v2 offset:768
v_mfma_f32_16x16x32_bf16 v[196:199],a[140:143],v[240:243],v[196:199]
ds_read_b64_tr_b16 a[242:243],v2 offset:4864
v_mfma_f32_16x16x32_bf16 v[204:207],a[156:159],v[240:243],v[40:43]
ds_read_b64_tr_b16 a[244:245],v2 offset:8960
v_mfma_f32_16x16x32_bf16 v[204:207],a[152:155],v[208:211],v[204:207]
ds_read_b64_tr_b16 a[246:247],v2 offset:13056
ds_read_b64_tr_b16 a[252:253],v3 offset:8960
v_mfma_f32_16x16x32_bf16 v[204:207],a[148:151],v[176:179],v[204:207]
buffer_store_dwordx4 v[76:79],v6,s[36:39],s84 offen offset:3072 sc1
v_mfma_f32_16x16x32_bf16 v[204:207],a[144:147],v[144:147],v[204:207]
s_add_u32 s84,s55,65536
ds_read_b64_tr_b16 a[254:255],v3 offset:13056
ds_read_b64_tr_b16 a[248:249],v3 offset:768
v_mfma_f32_16x16x32_bf16 v[212:215],a[160:163],v[144:147],v[40:43]
s_max_i32 s84,s84,s18
ds_read_b64_tr_b16 a[250:251],v3 offset:4864
v_add_u32 v2,s48,v2
v_mfma_f32_16x16x32_bf16 v[212:215],a[164:167],v[176:179],v[212:215]
v_exp_f32 v173,v173
v_mfma_f32_16x16x32_bf16 v[212:215],a[168:171],v[208:211],v[212:215]
v_exp_f32 v174,v174
v_mfma_f32_16x16x32_bf16 v[212:215],a[172:175],v[240:243],v[212:215]
v_exp_f32 v175,v175
v_mfma_f32_16x16x32_bf16 v[220:223],a[188:191],v[240:243],v[40:43]
v_exp_f32 v180,v180
v_mfma_f32_16x16x32_bf16 v[220:223],a[184:187],v[208:211],v[220:223]
v_exp_f32 v181,v181
v_mfma_f32_16x16x32_bf16 v[220:223],a[180:183],v[176:179],v[220:223]
v_exp_f32 v182,v182
v_mfma_f32_16x16x32_bf16 v[220:223],a[176:179],v[144:147],v[220:223]
s_waitcnt lgkmcnt(1)
v_exp_f32 v183,v183
v_mfma_f32_16x16x32_bf16 a[0:3],a[192:195],v[132:135],a[0:3]
v_cvt_pk_bf16_f32 v166,v172,v173
v_cvt_pk_bf16_f32 v167,v174,v175
v_mfma_f32_16x16x32_bf16 a[0:3],a[196:199],v[148:151],a[0:3]
v_cvt_pk_bf16_f32 v180,v180,v181
v_cvt_pk_bf16_f32 v181,v182,v183
v_mfma_f32_16x16x32_bf16 a[4:7],a[204:207],v[148:151],a[4:7]
v_exp_f32 v188,v188
v_mfma_f32_16x16x32_bf16 a[4:7],a[200:203],v[132:135],a[4:7]
v_exp_f32 v189,v189
v_mfma_f32_16x16x32_bf16 a[8:11],a[208:211],v[132:135],a[8:11]
buffer_load_dwordx4 v10,s[72:75],s80 offen offset:3072 lds
v_mfma_f32_16x16x32_bf16 a[8:11],a[212:215],v[148:151],a[8:11]
v_exp_f32 v190,v190
v_mfma_f32_16x16x32_bf16 a[12:15],a[220:223],v[148:151],a[12:15]
v_exp_f32 v191,v191
v_mfma_f32_16x16x32_bf16 a[12:15],a[216:219],v[132:135],a[12:15]
v_cvt_pk_bf16_f32 v182,v188,v189
v_cvt_pk_bf16_f32 v183,v190,v191
v_mfma_f32_16x16x32_bf16 a[16:19],a[224:227],v[132:135],a[16:19]
v_exp_f32 v196,v196
v_mfma_f32_16x16x32_bf16 a[16:19],a[228:231],v[148:151],a[16:19]
v_exp_f32 v197,v197
v_mfma_f32_16x16x32_bf16 a[20:23],a[236:239],v[148:151],a[20:23]
v_exp_f32 v198,v198
v_mfma_f32_16x16x32_bf16 a[20:23],a[232:235],v[132:135],a[20:23]
v_exp_f32 v199,v199
v_mfma_f32_16x16x32_bf16 a[24:27],a[240:243],v[132:135],a[24:27]
buffer_store_dwordx4 v[80:83],v6,s[36:39],s85 offen sc1
v_mfma_f32_16x16x32_bf16 a[24:27],a[244:247],v[148:151],a[24:27]
v_cvt_pk_bf16_f32 v196,v196,v197
v_cvt_pk_bf16_f32 v197,v198,v199
v_mfma_f32_16x16x32_bf16 a[28:31],a[252:255],v[148:151],a[28:31]
s_waitcnt lgkmcnt(0)
v_exp_f32 v204,v204
v_mfma_f32_16x16x32_bf16 a[28:31],a[248:251],v[132:135],a[28:31]
v_exp_f32 v205,v205
v_mfma_f32_16x16x32_bf16 v[48:51],v[28:31],v[132:135],v[48:51]
v_exp_f32 v206,v206
v_mfma_f32_16x16x32_bf16 v[48:51],v[28:31],v[148:151],v[48:51]
v_exp_f32 v207,v207
v_mfma_f32_16x16x32_bf16 v[228:231],a[128:131],v[152:155],v[44:47]
s_waitcnt vmcnt(10)
v_exp_f32 v212,v212
v_mfma_f32_16x16x32_bf16 v[228:231],a[132:135],v[184:187],v[228:231]
s_barrier
ds_read_b128 a[128:131],v1
v_cvt_pk_bf16_f32 v198,v204,v205
)A"
R"A(v_mfma_f32_16x16x32_bf16 v[228:231],a[136:139],v[216:219],v[228:231]
s_add_u32 m0,s82,86016
v_exp_f32 v213,v213
v_mfma_f32_16x16x32_bf16 v[228:231],a[140:143],v[248:251],v[228:231]
ds_read_b128 a[132:135],v1 offset:256
v_cvt_pk_bf16_f32 v199,v206,v207
v_mfma_f32_16x16x32_bf16 v[236:239],a[156:159],v[248:251],v[44:47]
v_exp_f32 v214,v214
v_mfma_f32_16x16x32_bf16 v[236:239],a[152:155],v[216:219],v[236:239]
buffer_load_dwordx4 v11,s[72:75],s80 offen lds
v_mfma_f32_16x16x32_bf16 v[236:239],a[148:151],v[184:187],v[236:239]
ds_read_b128 a[136:139],v1 offset:512
v_cvt_pk_bf16_f32 v212,v212,v213
v_mfma_f32_16x16x32_bf16 v[236:239],a[144:147],v[152:155],v[236:239]
v_exp_f32 v215,v215
v_mfma_f32_16x16x32_bf16 v[244:247],a[160:163],v[152:155],v[44:47]
ds_read_b128 a[140:143],v1 offset:768
v_cvt_pk_bf16_f32 v213,v214,v215
v_mfma_f32_16x16x32_bf16 v[244:247],a[164:167],v[184:187],v[244:247]
v_exp_f32 v220,v220
v_mfma_f32_16x16x32_bf16 v[244:247],a[168:171],v[216:219],v[244:247]
ds_read_b128 a[156:159],v1 offset:4864
v_add_u32 v3,s48,v3
v_mfma_f32_16x16x32_bf16 v[244:247],a[172:175],v[248:251],v[244:247]
s_mov_b32 s48,98304
v_exp_f32 v221,v221
v_mfma_f32_16x16x32_bf16 v[252:255],a[188:191],v[248:251],v[44:47]
ds_read_b128 a[152:155],v1 offset:4608
v_cvt_pk_bf16_f32 v214,v220,v221
v_mfma_f32_16x16x32_bf16 v[252:255],a[184:187],v[216:219],v[252:255]
v_exp_f32 v222,v222
v_mfma_f32_16x16x32_bf16 v[252:255],a[180:183],v[184:187],v[252:255]
ds_read_b128 a[148:151],v1 offset:4352
v_mfma_f32_16x16x32_bf16 v[252:255],a[176:179],v[152:155],v[252:255]
buffer_store_dwordx4 v[84:87],v6,s[36:39],s85 offen offset:1024 sc1
v_mfma_f32_16x16x32_bf16 a[32:35],a[192:195],v[164:167],a[32:35]
ds_read_b128 a[144:147],v1 offset:4096
v_mfma_f32_16x16x32_bf16 a[32:35],a[196:199],v[180:183],a[32:35]
v_exp_f32 v223,v223
v_mfma_f32_16x16x32_bf16 a[36:39],a[204:207],v[180:183],a[36:39]
ds_read_b128 a[160:163],v1 offset:8192
v_cvt_pk_bf16_f32 v215,v222,v223
v_mfma_f32_16x16x32_bf16 a[36:39],a[200:203],v[164:167],a[36:39]
v_exp_f32 v228,v228
v_mfma_f32_16x16x32_bf16 a[40:43],a[208:211],v[164:167],a[40:43]
ds_read_b128 a[164:167],v1 offset:8448
v_mfma_f32_16x16x32_bf16 a[40:43],a[212:215],v[180:183],a[40:43]
v_exp_f32 v229,v229
v_mfma_f32_16x16x32_bf16 a[44:47],a[220:223],v[180:183],a[44:47]
s_add_u32 m0,s82,87040
ds_read_b128 a[168:171],v1 offset:8704
v_cvt_pk_bf16_f32 v228,v228,v229
v_mfma_f32_16x16x32_bf16 a[44:47],a[216:219],v[164:167],a[44:47]
v_exp_f32 v230,v230
v_mfma_f32_16x16x32_bf16 a[48:51],a[224:227],v[164:167],a[48:51]
ds_read_b128 a[172:175],v1 offset:8960
v_mfma_f32_16x16x32_bf16 a[48:51],a[228:231],v[180:183],a[48:51]
buffer_load_dwordx4 v7,s[72:75],s83 offen lds
v_mfma_f32_16x16x32_bf16 a[52:55],a[236:239],v[180:183],a[52:55]
ds_read_b128 a[188:191],v1 offset:13056
v_mfma_f32_16x16x32_bf16 a[52:55],a[232:235],v[164:167],a[52:55]
v_exp_f32 v231,v231
v_mfma_f32_16x16x32_bf16 a[56:59],a[240:243],v[164:167],a[56:59]
ds_read_b128 a[184:187],v1 offset:12800
v_cvt_pk_bf16_f32 v229,v230,v231
v_mfma_f32_16x16x32_bf16 a[56:59],a[244:247],v[180:183],a[56:59]
v_exp_f32 v236,v236
v_mfma_f32_16x16x32_bf16 a[60:63],a[252:255],v[180:183],a[60:63]
ds_read_b128 a[180:183],v1 offset:12544
v_mfma_f32_16x16x32_bf16 a[60:63],a[248:251],v[164:167],a[60:63]
v_exp_f32 v237,v237
v_mfma_f32_16x16x32_bf16 v[52:55],v[28:31],v[164:167],v[52:55]
ds_read_b128 a[176:179],v1 offset:12288
v_cvt_pk_bf16_f32 v230,v236,v237
v_mfma_f32_16x16x32_bf16 v[52:55],v[28:31],v[180:183],v[52:55]
s_waitcnt lgkmcnt(4)
v_exp_f32 v238,v238
v_mfma_f32_16x16x32_bf16 v[132:135],a[128:131],v[128:131],v[32:35]
v_exp_f32 v239,v239
v_mfma_f32_16x16x32_bf16 v[132:135],a[132:135],v[160:163],v[132:135]
v_exp_f32 v244,v244
v_mfma_f32_16x16x32_bf16 v[132:135],a[136:139],v[192:195],v[132:135]
v_exp_f32 v245,v245
v_mfma_f32_16x16x32_bf16 v[132:135],a[140:143],v[224:227],v[132:135]
buffer_store_dwordx4 v[88:91],v6,s[36:39],s85 offen offset:2048 sc1
v_mfma_f32_16x16x32_bf16 v[140:143],a[156:159],v[224:227],v[32:35]
v_exp_f32 v246,v246
v_mfma_f32_16x16x32_bf16 v[140:143],a[152:155],v[192:195],v[140:143]
v_exp_f32 v247,v247
v_mfma_f32_16x16x32_bf16 v[140:143],a[148:151],v[160:163],v[140:143]
v_cvt_pk_bf16_f32 v231,v238,v239
v_cvt_pk_bf16_f32 v244,v244,v245
v_mfma_f32_16x16x32_bf16 v[140:143],a[144:147],v[128:131],v[140:143]
v_exp_f32 v252,v252
v_mfma_f32_16x16x32_bf16 v[148:151],a[160:163],v[128:131],v[32:35]
v_exp_f32 v253,v253
v_mfma_f32_16x16x32_bf16 v[148:151],a[164:167],v[160:163],v[148:151]
v_exp_f32 v254,v254
v_mfma_f32_16x16x32_bf16 v[148:151],a[168:171],v[192:195],v[148:151]
v_exp_f32 v255,v255
v_mfma_f32_16x16x32_bf16 v[148:151],a[172:175],v[224:227],v[148:151]
s_waitcnt lgkmcnt(0)
buffer_load_dwordx4 v8,s[72:75],s83 offen offset:1024 lds
v_mfma_f32_16x16x32_bf16 v[156:159],a[188:191],v[224:227],v[32:35]
v_cvt_pk_bf16_f32 v245,v246,v247
v_cvt_pk_bf16_f32 v246,v252,v253
v_mfma_f32_16x16x32_bf16 v[156:159],a[184:187],v[192:195],v[156:159]
v_cvt_pk_bf16_f32 v247,v254,v255
v_add_u32 v1,s48,v1
v_mfma_f32_16x16x32_bf16 v[156:159],a[180:183],v[160:163],v[156:159]
v_exp_f32 v132,v132
v_mfma_f32_16x16x32_bf16 v[156:159],a[176:179],v[128:131],v[156:159]
v_exp_f32 v133,v133
v_mfma_f32_16x16x32_bf16 a[64:67],a[192:195],v[196:199],a[64:67]
v_exp_f32 v134,v134
v_mfma_f32_16x16x32_bf16 a[64:67],a[196:199],v[212:215],a[64:67]
v_exp_f32 v135,v135
v_mfma_f32_16x16x32_bf16 a[68:71],a[204:207],v[212:215],a[68:71]
v_cvt_pk_bf16_f32 v132,v132,v133
v_cvt_pk_bf16_f32 v133,v134,v135
v_mfma_f32_16x16x32_bf16 a[68:71],a[200:203],v[196:199],a[68:71]
v_exp_f32 v140,v140
v_mfma_f32_16x16x32_bf16 a[72:75],a[208:211],v[196:199],a[72:75]
v_exp_f32 v141,v141
v_mfma_f32_16x16x32_bf16 a[72:75],a[212:215],v[212:215],a[72:75]
v_exp_f32 v142,v142
v_mfma_f32_16x16x32_bf16 a[76:79],a[220:223],v[212:215],a[76:79]
v_exp_f32 v143,v143
v_mfma_f32_16x16x32_bf16 a[76:79],a[216:219],v[196:199],a[76:79]
v_exp_f32 v148,v148
v_mfma_f32_16x16x32_bf16 a[80:83],a[224:227],v[196:199],a[80:83]
v_exp_f32 v149,v149
v_mfma_f32_16x16x32_bf16 a[80:83],a[228:231],v[212:215],a[80:83]
buffer_store_dwordx4 v[92:95],v6,s[36:39],s85 offen offset:3072 sc1
v_mfma_f32_16x16x32_bf16 a[84:87],a[236:239],v[212:215],a[84:87]
s_add_u32 s85,s55,69632
v_exp_f32 v150,v150
v_mfma_f32_16x16x32_bf16 a[84:87],a[232:235],v[196:199],a[84:87]
s_max_i32 s85,s85,s19
v_exp_f32 v151,v151
v_mfma_f32_16x16x32_bf16 a[88:91],a[240:243],v[196:199],a[88:91]
v_cvt_pk_bf16_f32 v134,v140,v141
v_cvt_pk_bf16_f32 v135,v142,v143
v_mfma_f32_16x16x32_bf16 a[88:91],a[244:247],v[212:215],a[88:91]
v_cvt_pk_bf16_f32 v148,v148,v149
v_cvt_pk_bf16_f32 v149,v150,v151
v_mfma_f32_16x16x32_bf16 a[92:95],a[252:255],v[212:215],a[92:95]
v_exp_f32 v156,v156
v_mfma_f32_16x16x32_bf16 a[92:95],a[248:251],v[196:199],a[92:95]
buffer_load_dwordx4 v9,s[72:75],s83 offen offset:2048 lds
v_mfma_f32_16x16x32_bf16 v[56:59],v[28:31],v[196:199],v[56:59]
v_exp_f32 v157,v157
v_mfma_f32_16x16x32_bf16 v[56:59],v[28:31],v[212:215],v[56:59]
v_exp_f32 v158,v158
v_mfma_f32_16x16x32_bf16 v[164:167],a[128:131],v[136:139],v[36:39]
v_exp_f32 v159,v159
v_mfma_f32_16x16x32_bf16 v[164:167],a[132:135],v[168:171],v[164:167]
v_cvt_pk_bf16_f32 v150,v156,v157
v_cvt_pk_bf16_f32 v151,v158,v159
v_mfma_f32_16x16x32_bf16 v[164:167],a[136:139],v[200:203],v[164:167]
v_mfma_f32_16x16x32_bf16 v[164:167],a[140:143],v[232:235],v[164:167]
v_mfma_f32_16x16x32_bf16 v[172:175],a[156:159],v[232:235],v[36:39]
v_mfma_f32_16x16x32_bf16 v[172:175],a[152:155],v[200:203],v[172:175]
v_mfma_f32_16x16x32_bf16 v[172:175],a[148:151],v[168:171],v[172:175]
v_mfma_f32_16x16x32_bf16 v[172:175],a[144:147],v[136:139],v[172:175]
v_mfma_f32_16x16x32_bf16 v[180:183],a[160:163],v[136:139],v[36:39]
v_mfma_f32_16x16x32_bf16 v[180:183],a[164:167],v[168:171],v[180:183]
s_nop 1
v_exp_f32 v164,v164
v_mfma_f32_16x16x32_bf16 v[180:183],a[168:171],v[200:203],v[180:183]
v_exp_f32 v165,v165
v_mfma_f32_16x16x32_bf16 v[180:183],a[172:175],v[232:235],v[180:183]
v_exp_f32 v166,v166
v_mfma_f32_16x16x32_bf16 v[188:191],a[188:191],v[232:235],v[36:39]
v_exp_f32 v167,v167
v_mfma_f32_16x16x32_bf16 v[188:191],a[184:187],v[200:203],v[188:191]
buffer_store_dwordx4 v[96:99],v6,s[36:39],s86 offen sc1
v_mfma_f32_16x16x32_bf16 v[188:191],a[180:183],v[168:171],v[188:191]
v_cvt_pk_bf16_f32 v164,v164,v165
v_cvt_pk_bf16_f32 v165,v166,v167
v_mfma_f32_16x16x32_bf16 v[188:191],a[176:179],v[136:139],v[188:191]
v_exp_f32 v172,v172
v_mfma_f32_16x16x32_bf16 a[96:99],a[192:195],v[228:231],a[96:99]
ds_read_b64_tr_b16 a[192:193],v2
ds_read_b64_tr_b16 a[194:195],v2 offset:4096
v_mfma_f32_16x16x32_bf16 a[96:99],a[196:199],v[244:247],a[96:99]
buffer_load_dwordx4 v10,s[72:75],s83 offen offset:3072 lds
v_mfma_f32_16x16x32_bf16 a[100:103],a[204:207],v[244:247],a[100:103]
ds_read_b64_tr_b16 a[196:197],v2 offset:8192
ds_read_b64_tr_b16 a[198:199],v2 offset:12288
v_mfma_f32_16x16x32_bf16 a[100:103],a[200:203],v[228:231],a[100:103]
ds_read_b64_tr_b16 a[204:205],v3 offset:8192
v_mfma_f32_16x16x32_bf16 a[104:107],a[208:211],v[228:231],a[104:107]
ds_read_b64_tr_b16 a[206:207],v3 offset:12288
v_mfma_f32_16x16x32_bf16 a[104:107],a[212:215],v[244:247],a[104:107]
ds_read_b64_tr_b16 a[200:201],v3
v_mfma_f32_16x16x32_bf16 a[108:111],a[220:223],v[244:247],a[108:111]
ds_read_b64_tr_b16 a[202:203],v3 offset:4096
ds_read_b64_tr_b16 a[208:209],v2 offset:256
v_mfma_f32_16x16x32_bf16 a[108:111],a[216:219],v[228:231],a[108:111]
ds_read_b64_tr_b16 a[210:211],v2 offset:4352
v_mfma_f32_16x16x32_bf16 a[112:115],a[224:227],v[228:231],a[112:115]
ds_read_b64_tr_b16 a[212:213],v2 offset:8448
v_mfma_f32_16x16x32_bf16 a[112:115],a[228:231],v[244:247],a[112:115]
ds_read_b64_tr_b16 a[214:215],v2 offset:12544
v_mfma_f32_16x16x32_bf16 a[116:119],a[236:239],v[244:247],a[116:119]
ds_read_b64_tr_b16 a[220:221],v3 offset:8448
ds_read_b64_tr_b16 a[222:223],v3 offset:12544
v_mfma_f32_16x16x32_bf16 a[116:119],a[232:235],v[228:231],a[116:119]
ds_read_b64_tr_b16 a[216:217],v3 offset:256
v_mfma_f32_16x16x32_bf16 a[120:123],a[240:243],v[228:231],a[120:123]
ds_read_b64_tr_b16 a[218:219],v3 offset:4352
v_mfma_f32_16x16x32_bf16 a[120:123],a[244:247],v[244:247],a[120:123]
ds_read_b64_tr_b16 a[224:225],v2 offset:512
v_mfma_f32_16x16x32_bf16 a[124:127],a[252:255],v[244:247],a[124:127]
ds_read_b64_tr_b16 a[226:227],v2 offset:4608
ds_read_b64_tr_b16 a[228:229],v2 offset:8704
v_mfma_f32_16x16x32_bf16 a[124:127],a[248:251],v[228:231],a[124:127]
ds_read_b64_tr_b16 a[230:231],v2 offset:12800
v_mfma_f32_16x16x32_bf16 v[60:63],v[28:31],v[228:231],v[60:63]
ds_read_b64_tr_b16 a[236:237],v3 offset:8704
v_mfma_f32_16x16x32_bf16 v[60:63],v[28:31],v[244:247],v[60:63]
ds_read_b64_tr_b16 a[238:239],v3 offset:12800
v_mfma_f32_16x16x32_bf16 v[196:199],a[128:131],v[144:147],v[40:43]
s_add_u32 m0,s82,91136
ds_read_b64_tr_b16 a[232:233],v3 offset:512
ds_read_b64_tr_b16 a[234:235],v3 offset:4608
v_mfma_f32_16x16x32_bf16 v[196:199],a[132:135],v[176:179],v[196:199]
buffer_store_dwordx4 v[100:103],v6,s[36:39],s86 offen offset:1024 sc1
v_mfma_f32_16x16x32_bf16 v[196:199],a[136:139],v[208:211],v[196:199]
ds_read_b64_tr_b16 a[240:241],v2 offset:768
ds_read_b64_tr_b16 a[242:243],v2 offset:4864
v_mfma_f32_16x16x32_bf16 v[196:199],a[140:143],v[240:243],v[196:199]
ds_read_b64_tr_b16 a[244:245],v2 offset:8960
v_mfma_f32_16x16x32_bf16 v[204:207],a[156:159],v[240:243],v[40:43]
ds_read_b64_tr_b16 a[246:247],v2 offset:13056
v_add_u32 v2,s48,v2
v_mfma_f32_16x16x32_bf16 v[204:207],a[152:155],v[208:211],v[204:207]
buffer_load_dwordx4 v11,s[72:75],s83 offen lds
v_mfma_f32_16x16x32_bf16 v[204:207],a[148:151],v[176:179],v[204:207]
ds_read_b64_tr_b16 a[252:253],v3 offset:8960
ds_read_b64_tr_b16 a[254:255],v3 offset:13056
v_mfma_f32_16x16x32_bf16 v[204:207],a[144:147],v[144:147],v[204:207]
ds_read_b64_tr_b16 a[248:249],v3 offset:768
v_mfma_f32_16x16x32_bf16 v[212:215],a[160:163],v[144:147],v[40:43]
ds_read_b64_tr_b16 a[250:251],v3 offset:4864
v_add_u32 v3,s48,v3
v_mfma_f32_16x16x32_bf16 v[212:215],a[164:167],v[176:179],v[212:215]
v_exp_f32 v173,v173
v_mfma_f32_16x16x32_bf16 v[212:215],a[168:171],v[208:211],v[212:215]
)A"
R"A(v_exp_f32 v174,v174
v_mfma_f32_16x16x32_bf16 v[212:215],a[172:175],v[240:243],v[212:215]
v_exp_f32 v175,v175
v_mfma_f32_16x16x32_bf16 v[220:223],a[188:191],v[240:243],v[40:43]
v_exp_f32 v180,v180
v_mfma_f32_16x16x32_bf16 v[220:223],a[184:187],v[208:211],v[220:223]
v_exp_f32 v181,v181
v_mfma_f32_16x16x32_bf16 v[220:223],a[180:183],v[176:179],v[220:223]
v_exp_f32 v182,v182
v_mfma_f32_16x16x32_bf16 v[220:223],a[176:179],v[144:147],v[220:223]
s_waitcnt lgkmcnt(1)
v_exp_f32 v183,v183
v_mfma_f32_16x16x32_bf16 a[0:3],a[192:195],v[132:135],a[0:3]
v_cvt_pk_bf16_f32 v166,v172,v173
v_cvt_pk_bf16_f32 v167,v174,v175
v_mfma_f32_16x16x32_bf16 a[0:3],a[196:199],v[148:151],a[0:3]
v_cvt_pk_bf16_f32 v180,v180,v181
v_cvt_pk_bf16_f32 v181,v182,v183
v_mfma_f32_16x16x32_bf16 a[4:7],a[204:207],v[148:151],a[4:7]
s_add_u32 s80,s71,0
s_and_b32 s80,s80,3
s_lshl_b32 s80,s80,1
v_mfma_f32_16x16x32_bf16 a[4:7],a[200:203],v[132:135],a[4:7]
s_or_b32 s80,s80,1
s_mul_i32 s80,s80,163840
s_add_u32 s80,s80,s81
v_mfma_f32_16x16x32_bf16 a[8:11],a[208:211],v[132:135],a[8:11]
s_add_u32 s83,s80,20480
v_exp_f32 v188,v188
v_mfma_f32_16x16x32_bf16 a[8:11],a[212:215],v[148:151],a[8:11]
v_exp_f32 v189,v189
v_mfma_f32_16x16x32_bf16 a[12:15],a[220:223],v[148:151],a[12:15]
v_exp_f32 v190,v190
v_mfma_f32_16x16x32_bf16 a[12:15],a[216:219],v[132:135],a[12:15]
buffer_store_dwordx4 v[104:107],v6,s[36:39],s86 offen offset:2048 sc1
v_mfma_f32_16x16x32_bf16 a[16:19],a[224:227],v[132:135],a[16:19]
v_exp_f32 v191,v191
v_mfma_f32_16x16x32_bf16 a[16:19],a[228:231],v[148:151],a[16:19]
v_cvt_pk_bf16_f32 v182,v188,v189
v_cvt_pk_bf16_f32 v183,v190,v191
v_mfma_f32_16x16x32_bf16 a[20:23],a[236:239],v[148:151],a[20:23]
v_exp_f32 v196,v196
v_mfma_f32_16x16x32_bf16 a[20:23],a[232:235],v[132:135],a[20:23]
v_exp_f32 v197,v197
v_mfma_f32_16x16x32_bf16 a[24:27],a[240:243],v[132:135],a[24:27]
v_exp_f32 v198,v198
v_mfma_f32_16x16x32_bf16 a[24:27],a[244:247],v[148:151],a[24:27]
v_exp_f32 v199,v199
v_mfma_f32_16x16x32_bf16 a[28:31],a[252:255],v[148:151],a[28:31]
s_waitcnt lgkmcnt(0)
v_cvt_pk_bf16_f32 v196,v196,v197
v_cvt_pk_bf16_f32 v197,v198,v199
v_mfma_f32_16x16x32_bf16 a[28:31],a[248:251],v[132:135],a[28:31]
v_exp_f32 v204,v204
v_mfma_f32_16x16x32_bf16 v[48:51],v[28:31],v[132:135],v[48:51]
v_exp_f32 v205,v205
v_mfma_f32_16x16x32_bf16 v[48:51],v[28:31],v[148:151],v[48:51]
v_exp_f32 v206,v206
v_mfma_f32_16x16x32_bf16 v[228:231],a[128:131],v[152:155],v[44:47]
v_exp_f32 v207,v207
v_mfma_f32_16x16x32_bf16 v[228:231],a[132:135],v[184:187],v[228:231]
s_barrier
ds_read_b128 a[128:131],v1
v_cvt_pk_bf16_f32 v198,v204,v205
v_mfma_f32_16x16x32_bf16 v[228:231],a[136:139],v[216:219],v[228:231]
v_exp_f32 v212,v212
v_mfma_f32_16x16x32_bf16 v[228:231],a[140:143],v[248:251],v[228:231]
ds_read_b128 a[132:135],v1 offset:256
v_cvt_pk_bf16_f32 v199,v206,v207
v_mfma_f32_16x16x32_bf16 v[236:239],a[156:159],v[248:251],v[44:47]
s_add_u32 m0,s82,0
v_exp_f32 v213,v213
v_mfma_f32_16x16x32_bf16 v[236:239],a[152:155],v[216:219],v[236:239]
ds_read_b128 a[136:139],v1 offset:512
v_cvt_pk_bf16_f32 v212,v212,v213
v_mfma_f32_16x16x32_bf16 v[236:239],a[148:151],v[184:187],v[236:239]
v_exp_f32 v214,v214
v_mfma_f32_16x16x32_bf16 v[236:239],a[144:147],v[152:155],v[236:239]
buffer_load_dwordx4 v7,s[72:75],s80 offen lds
v_mfma_f32_16x16x32_bf16 v[244:247],a[160:163],v[152:155],v[44:47]
ds_read_b128 a[140:143],v1 offset:768
v_mfma_f32_16x16x32_bf16 v[244:247],a[164:167],v[184:187],v[244:247]
v_exp_f32 v215,v215
v_mfma_f32_16x16x32_bf16 v[244:247],a[168:171],v[216:219],v[244:247]
ds_read_b128 a[156:159],v1 offset:4864
v_cvt_pk_bf16_f32 v213,v214,v215
v_mfma_f32_16x16x32_bf16 v[244:247],a[172:175],v[248:251],v[244:247]
buffer_store_dwordx4 v[108:111],v6,s[36:39],s86 offen offset:3072 sc1
v_mfma_f32_16x16x32_bf16 v[252:255],a[188:191],v[248:251],v[44:47]
s_add_u32 s86,s55,73728
s_max_i32 s86,s86,s100
ds_read_b128 a[152:155],v1 offset:4608
v_mfma_f32_16x16x32_bf16 v[252:255],a[184:187],v[216:219],v[252:255]
v_exp_f32 v220,v220
v_mfma_f32_16x16x32_bf16 v[252:255],a[180:183],v[184:187],v[252:255]
ds_read_b128 a[148:151],v1 offset:4352
v_mfma_f32_16x16x32_bf16 v[252:255],a[176:179],v[152:155],v[252:255]
v_exp_f32 v221,v221
v_mfma_f32_16x16x32_bf16 a[32:35],a[192:195],v[164:167],a[32:35]
ds_read_b128 a[144:147],v1 offset:4096
v_cvt_pk_bf16_f32 v214,v220,v221
v_mfma_f32_16x16x32_bf16 a[32:35],a[196:199],v[180:183],a[32:35]
v_exp_f32 v222,v222
v_mfma_f32_16x16x32_bf16 a[36:39],a[204:207],v[180:183],a[36:39]
buffer_load_dwordx4 v8,s[72:75],s80 offen offset:1024 lds
v_mfma_f32_16x16x32_bf16 a[36:39],a[200:203],v[164:167],a[36:39]
ds_read_b128 a[160:163],v1 offset:8192
v_mfma_f32_16x16x32_bf16 a[40:43],a[208:211],v[164:167],a[40:43]
v_exp_f32 v223,v223
v_mfma_f32_16x16x32_bf16 a[40:43],a[212:215],v[180:183],a[40:43]
ds_read_b128 a[164:167],v1 offset:8448
v_cvt_pk_bf16_f32 v215,v222,v223
v_mfma_f32_16x16x32_bf16 a[44:47],a[220:223],v[180:183],a[44:47]
v_exp_f32 v228,v228
v_mfma_f32_16x16x32_bf16 a[44:47],a[216:219],v[164:167],a[44:47]
ds_read_b128 a[168:171],v1 offset:8704
v_mfma_f32_16x16x32_bf16 a[48:51],a[224:227],v[164:167],a[48:51]
v_exp_f32 v229,v229
v_mfma_f32_16x16x32_bf16 a[48:51],a[228:231],v[180:183],a[48:51]
ds_read_b128 a[172:175],v1 offset:8960
v_cvt_pk_bf16_f32 v228,v228,v229
v_mfma_f32_16x16x32_bf16 a[52:55],a[236:239],v[180:183],a[52:55]
v_exp_f32 v230,v230
v_mfma_f32_16x16x32_bf16 a[52:55],a[232:235],v[164:167],a[52:55]
ds_read_b128 a[188:191],v1 offset:13056
v_mfma_f32_16x16x32_bf16 a[56:59],a[240:243],v[164:167],a[56:59]
v_exp_f32 v231,v231
v_mfma_f32_16x16x32_bf16 a[56:59],a[244:247],v[180:183],a[56:59]
buffer_load_dwordx4 v9,s[72:75],s80 offen offset:2048 lds
v_mfma_f32_16x16x32_bf16 a[60:63],a[252:255],v[180:183],a[60:63]
ds_read_b128 a[184:187],v1 offset:12800
v_cvt_pk_bf16_f32 v229,v230,v231
v_mfma_f32_16x16x32_bf16 a[60:63],a[248:251],v[164:167],a[60:63]
v_exp_f32 v236,v236
v_mfma_f32_16x16x32_bf16 v[52:55],v[28:31],v[164:167],v[52:55]
ds_read_b128 a[180:183],v1 offset:12544
v_mfma_f32_16x16x32_bf16 v[52:55],v[28:31],v[180:183],v[52:55]
s_waitcnt lgkmcnt(3)
buffer_store_dwordx4 v[112:115],v6,s[36:39],s87 offen sc1
v_mfma_f32_16x16x32_bf16 v[132:135],a[128:131],v[128:131],v[32:35]
ds_read_b128 a[176:179],v1 offset:12288
v_add_u32 v1,v4,v5
v_mfma_f32_16x16x32_bf16 v[132:135],a[132:135],v[160:163],v[132:135]
v_exp_f32 v237,v237
v_mfma_f32_16x16x32_bf16 v[132:135],a[136:139],v[192:195],v[132:135]
v_exp_f32 v238,v238
v_mfma_f32_16x16x32_bf16 v[132:135],a[140:143],v[224:227],v[132:135]
v_exp_f32 v239,v239
v_mfma_f32_16x16x32_bf16 v[140:143],a[156:159],v[224:227],v[32:35]
v_exp_f32 v244,v244
v_mfma_f32_16x16x32_bf16 v[140:143],a[152:155],v[192:195],v[140:143]
v_exp_f32 v245,v245
v_mfma_f32_16x16x32_bf16 v[140:143],a[148:151],v[160:163],v[140:143]
buffer_load_dwordx4 v10,s[72:75],s80 offen offset:3072 lds
v_mfma_f32_16x16x32_bf16 v[140:143],a[144:147],v[128:131],v[140:143]
v_exp_f32 v246,v246
v_mfma_f32_16x16x32_bf16 v[148:151],a[160:163],v[128:131],v[32:35]
v_exp_f32 v247,v247
v_mfma_f32_16x16x32_bf16 v[148:151],a[164:167],v[160:163],v[148:151]
v_cvt_pk_bf16_f32 v230,v236,v237
v_cvt_pk_bf16_f32 v231,v238,v239
v_mfma_f32_16x16x32_bf16 v[148:151],a[168:171],v[192:195],v[148:151]
v_cvt_pk_bf16_f32 v244,v244,v245
v_cvt_pk_bf16_f32 v245,v246,v247
v_mfma_f32_16x16x32_bf16 v[148:151],a[172:175],v[224:227],v[148:151]
s_waitcnt lgkmcnt(0)
v_exp_f32 v252,v252
v_mfma_f32_16x16x32_bf16 v[156:159],a[188:191],v[224:227],v[32:35]
v_exp_f32 v253,v253
v_mfma_f32_16x16x32_bf16 v[156:159],a[184:187],v[192:195],v[156:159]
v_exp_f32 v254,v254
v_mfma_f32_16x16x32_bf16 v[156:159],a[180:183],v[160:163],v[156:159]
s_add_u32 m0,s82,4096
v_exp_f32 v255,v255
v_mfma_f32_16x16x32_bf16 v[156:159],a[176:179],v[128:131],v[156:159]
v_cvt_pk_bf16_f32 v246,v252,v253
v_cvt_pk_bf16_f32 v247,v254,v255
v_mfma_f32_16x16x32_bf16 a[64:67],a[192:195],v[196:199],a[64:67]
v_exp_f32 v132,v132
v_mfma_f32_16x16x32_bf16 a[64:67],a[196:199],v[212:215],a[64:67]
buffer_load_dwordx4 v11,s[72:75],s80 offen lds
v_mfma_f32_16x16x32_bf16 a[68:71],a[204:207],v[212:215],a[68:71]
v_exp_f32 v133,v133
v_mfma_f32_16x16x32_bf16 a[68:71],a[200:203],v[196:199],a[68:71]
v_exp_f32 v134,v134
v_mfma_f32_16x16x32_bf16 a[72:75],a[208:211],v[196:199],a[72:75]
v_exp_f32 v135,v135
v_mfma_f32_16x16x32_bf16 a[72:75],a[212:215],v[212:215],a[72:75]
buffer_store_dwordx4 v[116:119],v6,s[36:39],s87 offen offset:1024 sc1
v_mfma_f32_16x16x32_bf16 a[76:79],a[220:223],v[212:215],a[76:79]
v_cvt_pk_bf16_f32 v132,v132,v133
v_cvt_pk_bf16_f32 v133,v134,v135
v_mfma_f32_16x16x32_bf16 a[76:79],a[216:219],v[196:199],a[76:79]
v_exp_f32 v140,v140
v_mfma_f32_16x16x32_bf16 a[80:83],a[224:227],v[196:199],a[80:83]
v_exp_f32 v141,v141
v_mfma_f32_16x16x32_bf16 a[80:83],a[228:231],v[212:215],a[80:83]
s_add_u32 m0,s82,5120
v_exp_f32 v142,v142
v_mfma_f32_16x16x32_bf16 a[84:87],a[236:239],v[212:215],a[84:87]
v_exp_f32 v143,v143
v_mfma_f32_16x16x32_bf16 a[84:87],a[232:235],v[196:199],a[84:87]
v_exp_f32 v148,v148
v_mfma_f32_16x16x32_bf16 a[88:91],a[240:243],v[196:199],a[88:91]
buffer_load_dwordx4 v7,s[72:75],s83 offen lds
v_mfma_f32_16x16x32_bf16 a[88:91],a[244:247],v[212:215],a[88:91]
v_exp_f32 v149,v149
v_mfma_f32_16x16x32_bf16 a[92:95],a[252:255],v[212:215],a[92:95]
v_exp_f32 v150,v150
v_mfma_f32_16x16x32_bf16 a[92:95],a[248:251],v[196:199],a[92:95]
v_exp_f32 v151,v151
v_mfma_f32_16x16x32_bf16 v[56:59],v[28:31],v[196:199],v[56:59]
v_cvt_pk_bf16_f32 v134,v140,v141
v_cvt_pk_bf16_f32 v135,v142,v143
v_mfma_f32_16x16x32_bf16 v[56:59],v[28:31],v[212:215],v[56:59]
v_cvt_pk_bf16_f32 v148,v148,v149
v_cvt_pk_bf16_f32 v149,v150,v151
v_mfma_f32_16x16x32_bf16 v[164:167],a[128:131],v[136:139],v[36:39]
v_exp_f32 v156,v156
v_mfma_f32_16x16x32_bf16 v[164:167],a[132:135],v[168:171],v[164:167]
v_exp_f32 v157,v157
v_mfma_f32_16x16x32_bf16 v[164:167],a[136:139],v[200:203],v[164:167]
v_exp_f32 v158,v158
v_mfma_f32_16x16x32_bf16 v[164:167],a[140:143],v[232:235],v[164:167]
v_exp_f32 v159,v159
v_mfma_f32_16x16x32_bf16 v[172:175],a[156:159],v[232:235],v[36:39]
v_cvt_pk_bf16_f32 v150,v156,v157
v_cvt_pk_bf16_f32 v151,v158,v159
v_mfma_f32_16x16x32_bf16 v[172:175],a[152:155],v[200:203],v[172:175]
buffer_load_dwordx4 v8,s[72:75],s83 offen offset:1024 lds
v_mfma_f32_16x16x32_bf16 v[172:175],a[148:151],v[168:171],v[172:175]
v_add_u32 v1,81920,v1
v_mfma_f32_16x16x32_bf16 v[172:175],a[144:147],v[136:139],v[172:175]
v_mfma_f32_16x16x32_bf16 v[180:183],a[160:163],v[136:139],v[36:39]
v_mfma_f32_16x16x32_bf16 v[180:183],a[164:167],v[168:171],v[180:183]
buffer_store_dwordx4 v[120:123],v6,s[36:39],s87 offen offset:2048 sc1
v_mfma_f32_16x16x32_bf16 v[180:183],a[168:171],v[200:203],v[180:183]
v_exp_f32 v164,v164
v_mfma_f32_16x16x32_bf16 v[180:183],a[172:175],v[232:235],v[180:183]
v_exp_f32 v165,v165
v_mfma_f32_16x16x32_bf16 v[188:191],a[188:191],v[232:235],v[36:39]
v_exp_f32 v166,v166
v_mfma_f32_16x16x32_bf16 v[188:191],a[184:187],v[200:203],v[188:191]
v_exp_f32 v167,v167
v_mfma_f32_16x16x32_bf16 v[188:191],a[180:183],v[168:171],v[188:191]
v_cvt_pk_bf16_f32 v164,v164,v165
v_cvt_pk_bf16_f32 v165,v166,v167
v_mfma_f32_16x16x32_bf16 v[188:191],a[176:179],v[136:139],v[188:191]
v_exp_f32 v172,v172
v_mfma_f32_16x16x32_bf16 a[96:99],a[192:195],v[228:231],a[96:99]
buffer_load_dwordx4 v9,s[72:75],s83 offen offset:2048 lds
v_mfma_f32_16x16x32_bf16 a[96:99],a[196:199],v[244:247],a[96:99]
ds_read_b64_tr_b16 a[192:193],v2
ds_read_b64_tr_b16 a[194:195],v2 offset:4096
v_mfma_f32_16x16x32_bf16 a[100:103],a[204:207],v[244:247],a[100:103]
ds_read_b64_tr_b16 a[196:197],v2 offset:8192
v_mfma_f32_16x16x32_bf16 a[100:103],a[200:203],v[228:231],a[100:103]
ds_read_b64_tr_b16 a[198:199],v2 offset:12288
v_mfma_f32_16x16x32_bf16 a[104:107],a[208:211],v[228:231],a[104:107]
ds_read_b64_tr_b16 a[204:205],v3 offset:8192
v_mfma_f32_16x16x32_bf16 a[104:107],a[212:215],v[244:247],a[104:107]
ds_read_b64_tr_b16 a[206:207],v3 offset:12288
ds_read_b64_tr_b16 a[200:201],v3
v_mfma_f32_16x16x32_bf16 a[108:111],a[220:223],v[244:247],a[108:111]
ds_read_b64_tr_b16 a[202:203],v3 offset:4096
)A"
R"A(v_mfma_f32_16x16x32_bf16 a[108:111],a[216:219],v[228:231],a[108:111]
ds_read_b64_tr_b16 a[208:209],v2 offset:256
v_mfma_f32_16x16x32_bf16 a[112:115],a[224:227],v[228:231],a[112:115]
ds_read_b64_tr_b16 a[210:211],v2 offset:4352
v_mfma_f32_16x16x32_bf16 a[112:115],a[228:231],v[244:247],a[112:115]
ds_read_b64_tr_b16 a[212:213],v2 offset:8448
ds_read_b64_tr_b16 a[214:215],v2 offset:12544
v_mfma_f32_16x16x32_bf16 a[116:119],a[236:239],v[244:247],a[116:119]
s_cmp_ge_u32 0,s71
s_cselect_b32 s80,2,0
ds_read_b64_tr_b16 a[220:221],v3 offset:8448
v_mfma_f32_16x16x32_bf16 a[116:119],a[232:235],v[228:231],a[116:119]
s_mul_i32 s80,s80,163840
buffer_load_dwordx4 v10,s[72:75],s83 offen offset:3072 lds
v_mfma_f32_16x16x32_bf16 a[120:123],a[240:243],v[228:231],a[120:123]
s_add_u32 s80,s80,s81
ds_read_b64_tr_b16 a[222:223],v3 offset:12544
ds_read_b64_tr_b16 a[216:217],v3 offset:256
v_mfma_f32_16x16x32_bf16 a[120:123],a[244:247],v[244:247],a[120:123]
ds_read_b64_tr_b16 a[218:219],v3 offset:4352
v_mfma_f32_16x16x32_bf16 a[124:127],a[252:255],v[244:247],a[124:127]
ds_read_b64_tr_b16 a[224:225],v2 offset:512
v_mfma_f32_16x16x32_bf16 a[124:127],a[248:251],v[228:231],a[124:127]
buffer_store_dwordx4 v[124:127],v6,s[36:39],s87 offen offset:3072 sc1
v_mfma_f32_16x16x32_bf16 v[60:63],v[28:31],v[228:231],v[60:63]
s_add_u32 s87,s55,77824
ds_read_b64_tr_b16 a[226:227],v2 offset:4608
ds_read_b64_tr_b16 a[228:229],v2 offset:8704
v_mfma_f32_16x16x32_bf16 v[60:63],v[28:31],v[244:247],v[60:63]
s_max_i32 s87,s87,s101
ds_read_b64_tr_b16 a[230:231],v2 offset:12800
v_mfma_f32_16x16x32_bf16 v[196:199],a[128:131],v[144:147],v[40:43]
ds_read_b64_tr_b16 a[236:237],v3 offset:8704
v_mfma_f32_16x16x32_bf16 v[196:199],a[132:135],v[176:179],v[196:199]
s_add_u32 m0,s82,9216
ds_read_b64_tr_b16 a[238:239],v3 offset:12800
v_mfma_f32_16x16x32_bf16 v[196:199],a[136:139],v[208:211],v[196:199]
ds_read_b64_tr_b16 a[232:233],v3 offset:512
ds_read_b64_tr_b16 a[234:235],v3 offset:4608
v_mfma_f32_16x16x32_bf16 v[196:199],a[140:143],v[240:243],v[196:199]
ds_read_b64_tr_b16 a[240:241],v2 offset:768
v_mfma_f32_16x16x32_bf16 v[204:207],a[156:159],v[240:243],v[40:43]
buffer_load_dwordx4 v11,s[72:75],s83 offen lds
v_mfma_f32_16x16x32_bf16 v[204:207],a[152:155],v[208:211],v[204:207]
s_add_u32 s83,s80,20480
ds_read_b64_tr_b16 a[242:243],v2 offset:4864
ds_read_b64_tr_b16 a[244:245],v2 offset:8960
v_mfma_f32_16x16x32_bf16 v[204:207],a[148:151],v[176:179],v[204:207]
ds_read_b64_tr_b16 a[246:247],v2 offset:13056
v_sub_u32 v2,v4,v5
v_mfma_f32_16x16x32_bf16 v[204:207],a[144:147],v[144:147],v[204:207]
ds_read_b64_tr_b16 a[252:253],v3 offset:8960
v_add_u32 v2,81920,v2
v_mfma_f32_16x16x32_bf16 v[212:215],a[160:163],v[144:147],v[40:43]
ds_read_b64_tr_b16 a[254:255],v3 offset:13056
v_mfma_f32_16x16x32_bf16 v[212:215],a[164:167],v[176:179],v[212:215]
ds_read_b64_tr_b16 a[248:249],v3 offset:768
ds_read_b64_tr_b16 a[250:251],v3 offset:4864
v_mfma_f32_16x16x32_bf16 v[212:215],a[168:171],v[208:211],v[212:215]
v_exp_f32 v173,v173
v_mfma_f32_16x16x32_bf16 v[212:215],a[172:175],v[240:243],v[212:215]
v_exp_f32 v174,v174
v_mfma_f32_16x16x32_bf16 v[220:223],a[188:191],v[240:243],v[40:43]
s_add_u32 m0,s82,40960
v_exp_f32 v175,v175
v_mfma_f32_16x16x32_bf16 v[220:223],a[184:187],v[208:211],v[220:223]
v_exp_f32 v180,v180
v_mfma_f32_16x16x32_bf16 v[220:223],a[180:183],v[176:179],v[220:223]
v_exp_f32 v181,v181
v_mfma_f32_16x16x32_bf16 v[220:223],a[176:179],v[144:147],v[220:223]
s_waitcnt lgkmcnt(3)
buffer_load_dwordx4 v7,s[72:75],s80 offen lds
v_mfma_f32_16x16x32_bf16 a[0:3],a[192:195],v[132:135],a[0:3]
v_exp_f32 v182,v182
v_mfma_f32_16x16x32_bf16 a[0:3],a[196:199],v[148:151],a[0:3]
v_exp_f32 v183,v183
v_mfma_f32_16x16x32_bf16 a[4:7],a[204:207],v[148:151],a[4:7]
v_cvt_pk_bf16_f32 v166,v172,v173
v_cvt_pk_bf16_f32 v167,v174,v175
v_mfma_f32_16x16x32_bf16 a[4:7],a[200:203],v[132:135],a[4:7]
v_cvt_pk_bf16_f32 v180,v180,v181
v_cvt_pk_bf16_f32 v181,v182,v183
v_mfma_f32_16x16x32_bf16 a[8:11],a[208:211],v[132:135],a[8:11]
v_exp_f32 v188,v188
v_mfma_f32_16x16x32_bf16 a[8:11],a[212:215],v[148:151],a[8:11]
v_exp_f32 v189,v189
v_mfma_f32_16x16x32_bf16 a[12:15],a[220:223],v[148:151],a[12:15]
v_exp_f32 v190,v190
v_mfma_f32_16x16x32_bf16 a[12:15],a[216:219],v[132:135],a[12:15]
v_exp_f32 v191,v191
v_mfma_f32_16x16x32_bf16 a[16:19],a[224:227],v[132:135],a[16:19]
v_cvt_pk_bf16_f32 v182,v188,v189
v_cvt_pk_bf16_f32 v183,v190,v191
v_mfma_f32_16x16x32_bf16 a[16:19],a[228:231],v[148:151],a[16:19]
v_exp_f32 v196,v196
v_mfma_f32_16x16x32_bf16 a[20:23],a[236:239],v[148:151],a[20:23]
buffer_load_dwordx4 v8,s[72:75],s80 offen offset:1024 lds
v_mfma_f32_16x16x32_bf16 a[20:23],a[232:235],v[132:135],a[20:23]
v_exp_f32 v197,v197
v_mfma_f32_16x16x32_bf16 a[24:27],a[240:243],v[132:135],a[24:27]
v_exp_f32 v198,v198
v_mfma_f32_16x16x32_bf16 a[24:27],a[244:247],v[148:151],a[24:27]
s_waitcnt lgkmcnt(0)
v_exp_f32 v199,v199
v_mfma_f32_16x16x32_bf16 a[28:31],a[252:255],v[148:151],a[28:31]
v_cvt_pk_bf16_f32 v196,v196,v197
v_cvt_pk_bf16_f32 v197,v198,v199
v_mfma_f32_16x16x32_bf16 a[28:31],a[248:251],v[132:135],a[28:31]
v_exp_f32 v204,v204
v_mfma_f32_16x16x32_bf16 v[48:51],v[28:31],v[132:135],v[48:51]
s_waitcnt vmcnt(18)
v_exp_f32 v205,v205
v_mfma_f32_16x16x32_bf16 v[48:51],v[28:31],v[148:151],v[48:51]
s_barrier
v_exp_f32 v206,v206
v_mfma_f32_16x16x32_bf16 v[228:231],a[128:131],v[152:155],v[44:47]
v_exp_f32 v207,v207
v_mfma_f32_16x16x32_bf16 v[228:231],a[132:135],v[184:187],v[228:231]
v_exp_f32 v212,v212
v_mfma_f32_16x16x32_bf16 v[228:231],a[136:139],v[216:219],v[228:231]
buffer_load_dwordx4 v9,s[72:75],s80 offen offset:2048 lds
v_mfma_f32_16x16x32_bf16 v[228:231],a[140:143],v[248:251],v[228:231]
v_exp_f32 v213,v213
v_mfma_f32_16x16x32_bf16 v[236:239],a[156:159],v[248:251],v[44:47]
v_exp_f32 v214,v214
v_mfma_f32_16x16x32_bf16 v[236:239],a[152:155],v[216:219],v[236:239]
v_exp_f32 v215,v215
v_mfma_f32_16x16x32_bf16 v[236:239],a[148:151],v[184:187],v[236:239]
v_cvt_pk_bf16_f32 v198,v204,v205
v_cvt_pk_bf16_f32 v199,v206,v207
v_mfma_f32_16x16x32_bf16 v[236:239],a[144:147],v[152:155],v[236:239]
v_cvt_pk_bf16_f32 v212,v212,v213
v_cvt_pk_bf16_f32 v213,v214,v215
v_mfma_f32_16x16x32_bf16 v[244:247],a[160:163],v[152:155],v[44:47]
v_exp_f32 v220,v220
v_mfma_f32_16x16x32_bf16 v[244:247],a[164:167],v[184:187],v[244:247]
v_exp_f32 v221,v221
v_mfma_f32_16x16x32_bf16 v[244:247],a[168:171],v[216:219],v[244:247]
v_exp_f32 v222,v222
v_mfma_f32_16x16x32_bf16 v[244:247],a[172:175],v[248:251],v[244:247]
s_lshl_b32 s95,s42,2
s_add_u32 s95,s95,s40
s_lshl_b32 s95,s95,4
v_mfma_f32_16x16x32_bf16 v[252:255],a[188:191],v[248:251],v[44:47]
v_mov_b32 v18,s68
v_mov_b32 v19,s69
v_mfma_f32_16x16x32_bf16 v[252:255],a[184:187],v[216:219],v[252:255]
buffer_load_dwordx4 v10,s[72:75],s80 offen offset:3072 lds
v_mfma_f32_16x16x32_bf16 v[252:255],a[180:183],v[184:187],v[252:255]
v_exp_f32 v223,v223
v_mfma_f32_16x16x32_bf16 v[252:255],a[176:179],v[152:155],v[252:255]
v_cvt_pk_bf16_f32 v214,v220,v221
v_cvt_pk_bf16_f32 v215,v222,v223
v_mfma_f32_16x16x32_bf16 a[32:35],a[192:195],v[164:167],a[32:35]
ds_read_b64_tr_b16 a[128:129],v1
v_max_f32 v20,0x00800000,v48
v_mfma_f32_16x16x32_bf16 a[32:35],a[196:199],v[180:183],a[32:35]
v_exp_f32 v228,v228
v_mfma_f32_16x16x32_bf16 a[36:39],a[204:207],v[180:183],a[36:39]
v_exp_f32 v229,v229
v_mfma_f32_16x16x32_bf16 a[36:39],a[200:203],v[164:167],a[36:39]
s_waitcnt vmcnt(5)
s_mov_b64 exec,1
buffer_store_dwordx2 v[18:19],v6,s[64:67],s95 offen sc1
s_mov_b64 exec,-1
v_mfma_f32_16x16x32_bf16 a[40:43],a[208:211],v[164:167],a[40:43]
v_exp_f32 v230,v230
v_mfma_f32_16x16x32_bf16 a[40:43],a[212:215],v[180:183],a[40:43]
s_add_u32 m0,s82,45056
v_exp_f32 v231,v231
v_mfma_f32_16x16x32_bf16 a[44:47],a[220:223],v[180:183],a[44:47]
v_rcp_f32 v20,v20
v_mfma_f32_16x16x32_bf16 a[44:47],a[216:219],v[164:167],a[44:47]
v_cvt_pk_bf16_f32 v228,v228,v229
v_cvt_pk_bf16_f32 v229,v230,v231
v_mfma_f32_16x16x32_bf16 a[48:51],a[224:227],v[164:167],a[48:51]
buffer_load_dwordx4 v11,s[72:75],s80 offen lds
v_mfma_f32_16x16x32_bf16 a[48:51],a[228:231],v[180:183],a[48:51]
v_exp_f32 v236,v236
v_mfma_f32_16x16x32_bf16 a[52:55],a[236:239],v[180:183],a[52:55]
v_exp_f32 v237,v237
v_mfma_f32_16x16x32_bf16 a[52:55],a[232:235],v[164:167],a[52:55]
v_exp_f32 v238,v238
v_mfma_f32_16x16x32_bf16 a[56:59],a[240:243],v[164:167],a[56:59]
v_exp_f32 v239,v239
v_mfma_f32_16x16x32_bf16 a[56:59],a[244:247],v[180:183],a[56:59]
v_exp_f32 v244,v244
v_mfma_f32_16x16x32_bf16 a[60:63],a[252:255],v[180:183],a[60:63]
v_exp_f32 v245,v245
v_mfma_f32_16x16x32_bf16 a[60:63],a[248:251],v[164:167],a[60:63]
v_exp_f32 v246,v246
v_mfma_f32_16x16x32_bf16 v[52:55],v[28:31],v[164:167],v[52:55]
s_add_u32 m0,s82,46080
v_exp_f32 v247,v247
v_mfma_f32_16x16x32_bf16 v[52:55],v[28:31],v[180:183],v[52:55]
v_accvgpr_read_b32 v132,a0
v_accvgpr_read_b32 v133,a1
v_accvgpr_read_b32 v134,a2
v_accvgpr_read_b32 v135,a3
v_accvgpr_read_b32 v12,a4
v_accvgpr_read_b32 v13,a5
v_accvgpr_read_b32 v14,a6
v_accvgpr_read_b32 v15,a7
v_pk_mul_f32 v[132:133],v[20:21],v[132:133] op_sel:[0,0] op_sel_hi:[0,1]
v_pk_mul_f32 v[134:135],v[20:21],v[134:135] op_sel:[0,0] op_sel_hi:[0,1]
v_pk_mul_f32 v[12:13],v[20:21],v[12:13] op_sel:[0,0] op_sel_hi:[0,1]
v_pk_mul_f32 v[14:15],v[20:21],v[14:15] op_sel:[0,0] op_sel_hi:[0,1]
v_cvt_pk_bf16_f32 v132,v132,v133
v_cvt_pk_bf16_f32 v133,v134,v135
v_cvt_pk_bf16_f32 v134,v12,v13
v_cvt_pk_bf16_f32 v135,v14,v15
v_mfma_f32_16x16x32_bf16 a[64:67],a[192:195],v[196:199],a[64:67]
v_cvt_pk_bf16_f32 v230,v236,v237
v_cvt_pk_bf16_f32 v231,v238,v239
v_mfma_f32_16x16x32_bf16 a[64:67],a[196:199],v[212:215],a[64:67]
buffer_load_dwordx4 v7,s[72:75],s83 offen lds
v_mfma_f32_16x16x32_bf16 a[68:71],a[204:207],v[212:215],a[68:71]
v_cvt_pk_bf16_f32 v244,v244,v245
v_cvt_pk_bf16_f32 v245,v246,v247
v_mfma_f32_16x16x32_bf16 a[68:71],a[200:203],v[196:199],a[68:71]
v_exp_f32 v252,v252
v_mfma_f32_16x16x32_bf16 a[72:75],a[208:211],v[196:199],a[72:75]
v_exp_f32 v253,v253
v_mfma_f32_16x16x32_bf16 a[72:75],a[212:215],v[212:215],a[72:75]
v_exp_f32 v254,v254
v_mfma_f32_16x16x32_bf16 a[76:79],a[220:223],v[212:215],a[76:79]
v_exp_f32 v255,v255
v_mfma_f32_16x16x32_bf16 a[76:79],a[216:219],v[196:199],a[76:79]
v_cvt_pk_bf16_f32 v246,v252,v253
v_cvt_pk_bf16_f32 v247,v254,v255
v_mfma_f32_16x16x32_bf16 a[80:83],a[224:227],v[196:199],a[80:83]
v_accvgpr_read_b32 v140,a8
v_accvgpr_read_b32 v141,a9
v_accvgpr_read_b32 v142,a10
v_accvgpr_read_b32 v143,a11
v_accvgpr_read_b32 v12,a12
v_accvgpr_read_b32 v13,a13
v_accvgpr_read_b32 v14,a14
v_accvgpr_read_b32 v15,a15
v_pk_mul_f32 v[140:141],v[20:21],v[140:141] op_sel:[0,0] op_sel_hi:[0,1]
v_pk_mul_f32 v[142:143],v[20:21],v[142:143] op_sel:[0,0] op_sel_hi:[0,1]
v_pk_mul_f32 v[12:13],v[20:21],v[12:13] op_sel:[0,0] op_sel_hi:[0,1]
v_pk_mul_f32 v[14:15],v[20:21],v[14:15] op_sel:[0,0] op_sel_hi:[0,1]
v_cvt_pk_bf16_f32 v140,v140,v141
v_cvt_pk_bf16_f32 v141,v142,v143
v_cvt_pk_bf16_f32 v142,v12,v13
v_cvt_pk_bf16_f32 v143,v14,v15
v_mfma_f32_16x16x32_bf16 a[80:83],a[228:231],v[212:215],a[80:83]
ds_read_b64_tr_b16 a[130:131],v1 offset:5120
ds_read_b64_tr_b16 a[132:133],v1 offset:10240
v_mfma_f32_16x16x32_bf16 a[84:87],a[236:239],v[212:215],a[84:87]
ds_read_b64_tr_b16 a[134:135],v1 offset:15360
v_accvgpr_read_b32 v148,a16
v_mfma_f32_16x16x32_bf16 a[84:87],a[232:235],v[196:199],a[84:87]
ds_read_b64_tr_b16 a[136:137],v1 offset:20480
v_accvgpr_read_b32 v149,a17
v_mfma_f32_16x16x32_bf16 a[88:91],a[240:243],v[196:199],a[88:91]
buffer_load_dwordx4 v8,s[72:75],s83 offen offset:1024 lds
v_mfma_f32_16x16x32_bf16 a[88:91],a[244:247],v[212:215],a[88:91]
ds_read_b64_tr_b16 a[138:139],v1 offset:25600
ds_read_b64_tr_b16 a[140:141],v1 offset:30720
v_mfma_f32_16x16x32_bf16 a[92:95],a[252:255],v[212:215],a[92:95]
ds_read_b64_tr_b16 a[142:143],v1 offset:35840
v_mul_f32 v148,v20,v148
v_mfma_f32_16x16x32_bf16 a[92:95],a[248:251],v[196:199],a[92:95]
v_mul_f32 v149,v20,v149
v_accvgpr_read_b32 v150,a18
v_mfma_f32_16x16x32_bf16 v[56:59],v[28:31],v[196:199],v[56:59]
buffer_store_dwordx4 v[132:135],v6,s[36:39],s84 offen sc1
v_mfma_f32_16x16x32_bf16 v[56:59],v[28:31],v[212:215],v[56:59]
)A"
R"A(v_accvgpr_read_b32 v151,a19
v_cvt_pk_bf16_f32 v148,v148,v149
v_mfma_f32_16x16x32_bf16 a[96:99],a[192:195],v[228:231],a[96:99]
v_mul_f32 v150,v20,v150
v_mul_f32 v151,v20,v151
v_mfma_f32_16x16x32_bf16 a[96:99],a[196:199],v[244:247],a[96:99]
v_cvt_pk_bf16_f32 v149,v150,v151
v_accvgpr_read_b32 v150,a20
v_mfma_f32_16x16x32_bf16 a[100:103],a[204:207],v[244:247],a[100:103]
v_accvgpr_read_b32 v151,a21
v_accvgpr_read_b32 v14,a22
v_mfma_f32_16x16x32_bf16 a[100:103],a[200:203],v[228:231],a[100:103]
v_accvgpr_read_b32 v15,a23
v_mul_f32 v150,v20,v150
v_mfma_f32_16x16x32_bf16 a[104:107],a[208:211],v[228:231],a[104:107]
v_mul_f32 v151,v20,v151
v_mul_f32 v14,v20,v14
v_mfma_f32_16x16x32_bf16 a[104:107],a[212:215],v[244:247],a[104:107]
buffer_load_dwordx4 v9,s[72:75],s83 offen offset:2048 lds
v_mfma_f32_16x16x32_bf16 a[108:111],a[220:223],v[244:247],a[108:111]
v_mul_f32 v15,v20,v15
v_cvt_pk_bf16_f32 v150,v150,v151
v_mfma_f32_16x16x32_bf16 a[108:111],a[216:219],v[228:231],a[108:111]
ds_read_b64_tr_b16 a[144:145],v2 offset:32
v_cvt_pk_bf16_f32 v151,v14,v15
v_mfma_f32_16x16x32_bf16 a[112:115],a[224:227],v[228:231],a[112:115]
ds_read_b64_tr_b16 a[146:147],v2 offset:5152
ds_read_b64_tr_b16 a[148:149],v2 offset:10272
v_mfma_f32_16x16x32_bf16 a[112:115],a[228:231],v[244:247],a[112:115]
ds_read_b64_tr_b16 a[150:151],v2 offset:15392
v_accvgpr_read_b32 v156,a24
v_mfma_f32_16x16x32_bf16 a[116:119],a[236:239],v[244:247],a[116:119]
ds_read_b64_tr_b16 a[152:153],v2 offset:20512
v_accvgpr_read_b32 v157,a25
v_mfma_f32_16x16x32_bf16 a[116:119],a[232:235],v[228:231],a[116:119]
ds_read_b64_tr_b16 a[154:155],v2 offset:25632
v_mul_f32 v156,v20,v156
v_mfma_f32_16x16x32_bf16 a[120:123],a[240:243],v[228:231],a[120:123]
ds_read_b64_tr_b16 a[156:157],v2 offset:30752
ds_read_b64_tr_b16 a[158:159],v2 offset:35872
v_mfma_f32_16x16x32_bf16 a[120:123],a[244:247],v[244:247],a[120:123]
v_mul_f32 v157,v20,v157
v_accvgpr_read_b32 v158,a26
v_mfma_f32_16x16x32_bf16 a[124:127],a[252:255],v[244:247],a[124:127]
v_accvgpr_read_b32 v159,a27
v_cvt_pk_bf16_f32 v156,v156,v157
v_mfma_f32_16x16x32_bf16 a[124:127],a[248:251],v[228:231],a[124:127]
v_mul_f32 v158,v20,v158
v_mul_f32 v159,v20,v159
v_mfma_f32_16x16x32_bf16 v[60:63],v[28:31],v[228:231],v[60:63]
buffer_load_dwordx4 v10,s[72:75],s83 offen offset:3072 lds
v_mfma_f32_16x16x32_bf16 v[60:63],v[28:31],v[244:247],v[60:63]
s_waitcnt lgkmcnt(4)
v_cvt_pk_bf16_f32 v157,v158,v159
v_accvgpr_read_b32 v158,a28
v_mfma_f32_16x16x32_bf16 a[192:195],a[128:131],v[64:67],0
v_accvgpr_read_b32 v159,a29
v_accvgpr_read_b32 v16,a30
v_mfma_f32_16x16x32_bf16 a[192:195],a[132:135],v[68:71],a[192:195]
v_accvgpr_read_b32 v17,a31
v_mul_f32 v158,v20,v158
v_mfma_f32_16x16x32_bf16 a[192:195],a[136:139],v[72:75],a[192:195]
buffer_store_dwordx4 v[140:143],v6,s[36:39],s84 offen offset:1024 sc1
v_mfma_f32_16x16x32_bf16 a[192:195],a[140:143],v[76:79],a[192:195]
v_mul_f32 v159,v20,v159
v_mul_f32 v16,v20,v16
v_mfma_f32_16x16x32_bf16 a[196:199],a[140:143],v[92:95],0
v_mul_f32 v17,v20,v17
v_cvt_pk_bf16_f32 v158,v158,v159
v_mfma_f32_16x16x32_bf16 a[196:199],a[136:139],v[88:91],a[196:199]
ds_read_b64_tr_b16 a[160:161],v1 offset:64
v_cvt_pk_bf16_f32 v159,v16,v17
v_mfma_f32_16x16x32_bf16 a[196:199],a[132:135],v[84:87],a[196:199]
s_add_u32 m0,s82,50176
ds_read_b64_tr_b16 a[162:163],v1 offset:5184
ds_read_b64_tr_b16 a[164:165],v1 offset:10304
v_mfma_f32_16x16x32_bf16 a[196:199],a[128:131],v[80:83],a[196:199]
ds_read_b64_tr_b16 a[166:167],v1 offset:15424
v_max_f32 v21,0x00800000,v52
v_mfma_f32_16x16x32_bf16 a[200:203],a[128:131],v[96:99],0
s_cmp_ge_u32 1,s71
s_cselect_b32 s80,4,2
ds_read_b64_tr_b16 a[168:169],v1 offset:20544
v_mfma_f32_16x16x32_bf16 a[200:203],a[132:135],v[100:103],a[200:203]
s_mul_i32 s80,s80,163840
buffer_load_dwordx4 v11,s[72:75],s83 offen lds
v_mfma_f32_16x16x32_bf16 a[200:203],a[136:139],v[104:107],a[200:203]
s_add_u32 s80,s80,s81
ds_read_b64_tr_b16 a[170:171],v1 offset:25664
ds_read_b64_tr_b16 a[172:173],v1 offset:30784
v_mfma_f32_16x16x32_bf16 a[200:203],a[140:143],v[108:111],a[200:203]
s_add_u32 s83,s80,20480
ds_read_b64_tr_b16 a[174:175],v1 offset:35904
v_accvgpr_read_b32 v180,a48
v_mfma_f32_16x16x32_bf16 a[204:207],a[140:143],v[124:127],0
v_rcp_f32 v21,v21
v_mfma_f32_16x16x32_bf16 a[204:207],a[136:139],v[120:123],a[204:207]
v_accvgpr_read_b32 v164,a32
v_accvgpr_read_b32 v165,a33
v_accvgpr_read_b32 v166,a34
v_accvgpr_read_b32 v167,a35
v_accvgpr_read_b32 v12,a36
v_accvgpr_read_b32 v13,a37
v_accvgpr_read_b32 v14,a38
v_accvgpr_read_b32 v15,a39
v_pk_mul_f32 v[164:165],v[20:21],v[164:165] op_sel:[1,0] op_sel_hi:[1,1]
v_pk_mul_f32 v[166:167],v[20:21],v[166:167] op_sel:[1,0] op_sel_hi:[1,1]
v_pk_mul_f32 v[12:13],v[20:21],v[12:13] op_sel:[1,0] op_sel_hi:[1,1]
v_pk_mul_f32 v[14:15],v[20:21],v[14:15] op_sel:[1,0] op_sel_hi:[1,1]
v_cvt_pk_bf16_f32 v164,v164,v165
v_cvt_pk_bf16_f32 v165,v166,v167
v_cvt_pk_bf16_f32 v166,v12,v13
v_cvt_pk_bf16_f32 v167,v14,v15
v_mfma_f32_16x16x32_bf16 a[204:207],a[132:135],v[116:119],a[204:207]
ds_read_b64_tr_b16 a[176:177],v2 offset:96
ds_read_b64_tr_b16 a[178:179],v2 offset:5216
v_mfma_f32_16x16x32_bf16 a[204:207],a[128:131],v[112:115],a[204:207]
v_accvgpr_read_b32 v172,a40
v_accvgpr_read_b32 v173,a41
v_accvgpr_read_b32 v174,a42
v_accvgpr_read_b32 v175,a43
v_accvgpr_read_b32 v12,a44
v_accvgpr_read_b32 v13,a45
v_accvgpr_read_b32 v14,a46
v_accvgpr_read_b32 v15,a47
v_pk_mul_f32 v[172:173],v[20:21],v[172:173] op_sel:[1,0] op_sel_hi:[1,1]
v_pk_mul_f32 v[174:175],v[20:21],v[174:175] op_sel:[1,0] op_sel_hi:[1,1]
v_pk_mul_f32 v[12:13],v[20:21],v[12:13] op_sel:[1,0] op_sel_hi:[1,1]
v_pk_mul_f32 v[14:15],v[20:21],v[14:15] op_sel:[1,0] op_sel_hi:[1,1]
v_cvt_pk_bf16_f32 v172,v172,v173
v_cvt_pk_bf16_f32 v173,v174,v175
v_cvt_pk_bf16_f32 v174,v12,v13
v_cvt_pk_bf16_f32 v175,v14,v15
v_mfma_f32_16x16x32_bf16 a[208:211],a[144:147],v[64:67],0
ds_read_b64_tr_b16 a[180:181],v2 offset:10336
ds_read_b64_tr_b16 a[182:183],v2 offset:15456
v_mfma_f32_16x16x32_bf16 a[208:211],a[148:151],v[68:71],a[208:211]
s_waitcnt lgkmcnt(7)
ds_read_b64_tr_b16 a[184:185],v2 offset:20576
v_accvgpr_read_b32 v181,a49
v_mfma_f32_16x16x32_bf16 a[208:211],a[152:155],v[72:75],a[208:211]
ds_read_b64_tr_b16 a[186:187],v2 offset:25696
v_mul_f32 v180,v21,v180
v_mfma_f32_16x16x32_bf16 a[208:211],a[156:159],v[76:79],a[208:211]
buffer_store_dwordx4 v[148:151],v6,s[36:39],s84 offen offset:2048 sc1
v_mfma_f32_16x16x32_bf16 a[212:215],a[156:159],v[92:95],0
ds_read_b64_tr_b16 a[188:189],v2 offset:30816
ds_read_b64_tr_b16 a[190:191],v2 offset:35936
v_mfma_f32_16x16x32_bf16 a[212:215],a[152:155],v[88:91],a[212:215]
v_mul_f32 v181,v21,v181
v_accvgpr_read_b32 v182,a50
v_mfma_f32_16x16x32_bf16 a[212:215],a[148:151],v[84:87],a[212:215]
v_accvgpr_read_b32 v183,a51
v_cvt_pk_bf16_f32 v180,v180,v181
v_mfma_f32_16x16x32_bf16 a[212:215],a[144:147],v[80:83],a[212:215]
v_mul_f32 v182,v21,v182
v_mul_f32 v183,v21,v183
v_mfma_f32_16x16x32_bf16 a[216:219],a[144:147],v[96:99],0
v_cvt_pk_bf16_f32 v181,v182,v183
v_accvgpr_read_b32 v182,a52
v_mfma_f32_16x16x32_bf16 a[216:219],a[148:151],v[100:103],a[216:219]
v_accvgpr_read_b32 v183,a53
v_accvgpr_read_b32 v12,a54
v_mfma_f32_16x16x32_bf16 a[216:219],a[152:155],v[104:107],a[216:219]
v_accvgpr_read_b32 v13,a55
v_mul_f32 v182,v21,v182
v_mfma_f32_16x16x32_bf16 a[216:219],a[156:159],v[108:111],a[216:219]
s_add_u32 m0,s82,122880
v_mul_f32 v183,v21,v183
v_mul_f32 v12,v21,v12
v_mfma_f32_16x16x32_bf16 a[220:223],a[156:159],v[124:127],0
v_mul_f32 v13,v21,v13
v_cvt_pk_bf16_f32 v182,v182,v183
v_mfma_f32_16x16x32_bf16 a[220:223],a[152:155],v[120:123],a[220:223]
ds_read_b64_tr_b16 a[128:129],v1 offset:128
v_cvt_pk_bf16_f32 v183,v12,v13
v_mfma_f32_16x16x32_bf16 a[220:223],a[148:151],v[116:119],a[220:223]
buffer_load_dwordx4 v7,s[72:75],s80 offen lds
v_mfma_f32_16x16x32_bf16 a[220:223],a[144:147],v[112:115],a[220:223]
ds_read_b64_tr_b16 a[130:131],v1 offset:5248
ds_read_b64_tr_b16 a[132:133],v1 offset:10368
v_mfma_f32_16x16x32_bf16 a[224:227],a[160:163],v[64:67],0
ds_read_b64_tr_b16 a[134:135],v1 offset:15488
v_accvgpr_read_b32 v188,a56
v_mfma_f32_16x16x32_bf16 a[224:227],a[164:167],v[68:71],a[224:227]
s_waitcnt lgkmcnt(4)
ds_read_b64_tr_b16 a[136:137],v1 offset:20608
v_accvgpr_read_b32 v189,a57
v_mfma_f32_16x16x32_bf16 a[224:227],a[168:171],v[72:75],a[224:227]
ds_read_b64_tr_b16 a[138:139],v1 offset:25728
v_mul_f32 v188,v21,v188
v_mfma_f32_16x16x32_bf16 a[224:227],a[172:175],v[76:79],a[224:227]
ds_read_b64_tr_b16 a[140:141],v1 offset:30848
ds_read_b64_tr_b16 a[142:143],v1 offset:35968
v_mfma_f32_16x16x32_bf16 a[228:231],a[172:175],v[92:95],0
v_mul_f32 v189,v21,v189
v_accvgpr_read_b32 v190,a58
v_mfma_f32_16x16x32_bf16 a[228:231],a[168:171],v[88:91],a[228:231]
v_accvgpr_read_b32 v191,a59
v_cvt_pk_bf16_f32 v188,v188,v189
v_mfma_f32_16x16x32_bf16 a[228:231],a[164:167],v[84:87],a[228:231]
v_mul_f32 v190,v21,v190
v_mul_f32 v191,v21,v191
v_mfma_f32_16x16x32_bf16 a[228:231],a[160:163],v[80:83],a[228:231]
buffer_store_dwordx4 v[156:159],v6,s[36:39],s84 offen offset:3072 sc1
v_mfma_f32_16x16x32_bf16 a[232:235],a[160:163],v[96:99],0
v_cvt_pk_bf16_f32 v189,v190,v191
v_accvgpr_read_b32 v190,a60
v_mfma_f32_16x16x32_bf16 a[232:235],a[164:167],v[100:103],a[232:235]
v_accvgpr_read_b32 v191,a61
v_accvgpr_read_b32 v14,a62
v_mfma_f32_16x16x32_bf16 a[232:235],a[168:171],v[104:107],a[232:235]
v_accvgpr_read_b32 v15,a63
v_mul_f32 v190,v21,v190
v_mfma_f32_16x16x32_bf16 a[232:235],a[172:175],v[108:111],a[232:235]
buffer_load_dwordx4 v8,s[72:75],s80 offen offset:1024 lds
v_mfma_f32_16x16x32_bf16 a[236:239],a[172:175],v[124:127],0
v_mul_f32 v191,v21,v191
v_mul_f32 v14,v21,v14
v_mfma_f32_16x16x32_bf16 a[236:239],a[168:171],v[120:123],a[236:239]
v_mul_f32 v15,v21,v15
v_cvt_pk_bf16_f32 v190,v190,v191
v_mfma_f32_16x16x32_bf16 a[236:239],a[164:167],v[116:119],a[236:239]
v_cvt_pk_bf16_f32 v191,v14,v15
v_max_f32 v22,0x00800000,v56
v_mfma_f32_16x16x32_bf16 a[236:239],a[160:163],v[112:115],a[236:239]
v_rcp_f32 v22,v22
v_mfma_f32_16x16x32_bf16 a[240:243],a[176:179],v[64:67],0
ds_read_b64_tr_b16 a[144:145],v2 offset:160
ds_read_b64_tr_b16 a[146:147],v2 offset:5280
v_mfma_f32_16x16x32_bf16 a[240:243],a[180:183],v[68:71],a[240:243]
v_accvgpr_read_b32 v196,a64
v_accvgpr_read_b32 v197,a65
v_mfma_f32_16x16x32_bf16 a[240:243],a[184:187],v[72:75],a[240:243]
v_mul_f32 v196,v22,v196
v_mul_f32 v197,v22,v197
v_mfma_f32_16x16x32_bf16 a[240:243],a[188:191],v[76:79],a[240:243]
v_accvgpr_read_b32 v198,a66
v_accvgpr_read_b32 v199,a67
v_mfma_f32_16x16x32_bf16 a[244:247],a[188:191],v[92:95],0
ds_read_b64_tr_b16 a[148:149],v2 offset:10400
ds_read_b64_tr_b16 a[150:151],v2 offset:15520
v_mfma_f32_16x16x32_bf16 a[244:247],a[184:187],v[88:91],a[244:247]
v_cvt_pk_bf16_f32 v196,v196,v197
v_mul_f32 v198,v22,v198
v_mfma_f32_16x16x32_bf16 a[244:247],a[180:183],v[84:87],a[244:247]
buffer_load_dwordx4 v9,s[72:75],s80 offen offset:2048 lds
v_mfma_f32_16x16x32_bf16 a[244:247],a[176:179],v[80:83],a[244:247]
v_mul_f32 v199,v22,v199
v_cvt_pk_bf16_f32 v197,v198,v199
v_mfma_f32_16x16x32_bf16 a[248:251],a[176:179],v[96:99],0
ds_read_b64_tr_b16 a[152:153],v2 offset:20640
ds_read_b64_tr_b16 a[154:155],v2 offset:25760
v_mfma_f32_16x16x32_bf16 a[248:251],a[180:183],v[100:103],a[248:251]
v_accvgpr_read_b32 v198,a68
v_accvgpr_read_b32 v199,a69
v_mfma_f32_16x16x32_bf16 a[248:251],a[184:187],v[104:107],a[248:251]
v_accvgpr_read_b32 v16,a70
v_accvgpr_read_b32 v17,a71
v_mfma_f32_16x16x32_bf16 a[248:251],a[188:191],v[108:111],a[248:251]
v_mul_f32 v198,v22,v198
v_mul_f32 v199,v22,v199
v_mfma_f32_16x16x32_bf16 a[252:255],a[188:191],v[124:127],0
buffer_store_dwordx4 v[164:167],v6,s[36:39],s85 offen sc1
v_mfma_f32_16x16x32_bf16 a[252:255],a[184:187],v[120:123],a[252:255]
ds_read_b64_tr_b16 a[156:157],v2 offset:30880
ds_read_b64_tr_b16 a[158:159],v2 offset:36000
v_mfma_f32_16x16x32_bf16 a[252:255],a[180:183],v[116:119],a[252:255]
s_waitcnt vmcnt(19)
v_mul_f32 v16,v22,v16
v_mul_f32 v17,v22,v17
v_mfma_f32_16x16x32_bf16 a[252:255],a[176:179],v[112:115],a[252:255]
s_barrier
v_cvt_pk_bf16_f32 v198,v198,v199
v_cvt_pk_bf16_f32 v199,v16,v17
)A"
R"A(s_waitcnt lgkmcnt(4)
v_mfma_f32_16x16x32_bf16 a[0:3],a[128:131],v[64:67],0
v_accvgpr_read_b32 v204,a72
v_accvgpr_read_b32 v205,a73
v_mfma_f32_16x16x32_bf16 a[0:3],a[132:135],v[68:71],a[0:3]
buffer_load_dwordx4 v10,s[72:75],s80 offen offset:3072 lds
v_mfma_f32_16x16x32_bf16 a[0:3],a[136:139],v[72:75],a[0:3]
v_mul_f32 v204,v22,v204
v_mul_f32 v205,v22,v205
v_mfma_f32_16x16x32_bf16 a[0:3],a[140:143],v[76:79],a[0:3]
v_accvgpr_read_b32 v206,a74
v_accvgpr_read_b32 v207,a75
v_mfma_f32_16x16x32_bf16 a[4:7],a[140:143],v[92:95],0
v_cvt_pk_bf16_f32 v204,v204,v205
v_mul_f32 v206,v22,v206
v_mfma_f32_16x16x32_bf16 a[4:7],a[136:139],v[88:91],a[4:7]
s_lshl_b32 s94,s70,4
v_mul_f32 v207,v22,v207
v_cvt_pk_bf16_f32 v205,v206,v207
v_mfma_f32_16x16x32_bf16 a[4:7],a[132:135],v[84:87],a[4:7]
s_add_u32 s94,s94,s40
v_accvgpr_read_b32 v206,a76
v_accvgpr_read_b32 v207,a77
v_mfma_f32_16x16x32_bf16 a[4:7],a[128:131],v[80:83],a[4:7]
s_lshl_b32 s94,s94,4
v_accvgpr_read_b32 v12,a78
v_accvgpr_read_b32 v13,a79
v_mfma_f32_16x16x32_bf16 a[8:11],a[128:131],v[96:99],0
v_mul_f32 v206,v22,v206
v_mul_f32 v207,v22,v207
v_mfma_f32_16x16x32_bf16 a[8:11],a[132:135],v[100:103],a[8:11]
v_mul_f32 v12,v22,v12
v_mul_f32 v13,v22,v13
v_mfma_f32_16x16x32_bf16 a[8:11],a[136:139],v[104:107],a[8:11]
s_add_u32 m0,s82,126976
v_cvt_pk_bf16_f32 v206,v206,v207
v_cvt_pk_bf16_f32 v207,v12,v13
v_mfma_f32_16x16x32_bf16 a[8:11],a[140:143],v[108:111],a[8:11]
ds_read_b64_tr_b16 a[160:161],v1 offset:192
ds_read_b64_tr_b16 a[162:163],v1 offset:5312
v_mfma_f32_16x16x32_bf16 a[12:15],a[140:143],v[124:127],0
ds_read_b64_tr_b16 a[164:165],v1 offset:10432
v_accvgpr_read_b32 v212,a80
v_mfma_f32_16x16x32_bf16 a[12:15],a[136:139],v[120:123],a[12:15]
buffer_load_dwordx4 v11,s[72:75],s80 offen lds
v_mfma_f32_16x16x32_bf16 a[12:15],a[132:135],v[116:119],a[12:15]
ds_read_b64_tr_b16 a[166:167],v1 offset:15552
ds_read_b64_tr_b16 a[168:169],v1 offset:20672
v_mfma_f32_16x16x32_bf16 a[12:15],a[128:131],v[112:115],a[12:15]
ds_read_b64_tr_b16 a[170:171],v1 offset:25792
v_accvgpr_read_b32 v213,a81
v_mfma_f32_16x16x32_bf16 a[16:19],a[144:147],v[64:67],0
ds_read_b64_tr_b16 a[172:173],v1 offset:30912
v_mul_f32 v212,v22,v212
v_mfma_f32_16x16x32_bf16 a[16:19],a[148:151],v[68:71],a[16:19]
s_waitcnt lgkmcnt(7)
buffer_store_dwordx4 v[172:175],v6,s[36:39],s85 offen offset:1024 sc1
v_mfma_f32_16x16x32_bf16 a[16:19],a[152:155],v[72:75],a[16:19]
ds_read_b64_tr_b16 a[174:175],v1 offset:36032
v_mul_f32 v213,v22,v213
v_mfma_f32_16x16x32_bf16 a[16:19],a[156:159],v[76:79],a[16:19]
v_accvgpr_read_b32 v214,a82
v_accvgpr_read_b32 v215,a83
v_mfma_f32_16x16x32_bf16 a[20:23],a[156:159],v[92:95],0
v_cvt_pk_bf16_f32 v212,v212,v213
v_mul_f32 v214,v22,v214
v_mfma_f32_16x16x32_bf16 a[20:23],a[152:155],v[88:91],a[20:23]
v_mul_f32 v215,v22,v215
v_cvt_pk_bf16_f32 v213,v214,v215
v_mfma_f32_16x16x32_bf16 a[20:23],a[148:151],v[84:87],a[20:23]
s_add_u32 m0,s82,128000
v_accvgpr_read_b32 v214,a84
v_accvgpr_read_b32 v215,a85
v_mfma_f32_16x16x32_bf16 a[20:23],a[144:147],v[80:83],a[20:23]
v_accvgpr_read_b32 v14,a86
v_accvgpr_read_b32 v15,a87
v_mfma_f32_16x16x32_bf16 a[24:27],a[144:147],v[96:99],0
v_mul_f32 v214,v22,v214
v_mul_f32 v215,v22,v215
v_mfma_f32_16x16x32_bf16 a[24:27],a[148:151],v[100:103],a[24:27]
buffer_load_dwordx4 v7,s[72:75],s83 offen lds
v_mfma_f32_16x16x32_bf16 a[24:27],a[152:155],v[104:107],a[24:27]
v_mul_f32 v14,v22,v14
v_mul_f32 v15,v22,v15
v_mfma_f32_16x16x32_bf16 a[24:27],a[156:159],v[108:111],a[24:27]
v_cvt_pk_bf16_f32 v214,v214,v215
v_cvt_pk_bf16_f32 v215,v14,v15
v_mfma_f32_16x16x32_bf16 a[28:31],a[156:159],v[124:127],0
ds_read_b64_tr_b16 a[176:177],v2 offset:224
ds_read_b64_tr_b16 a[178:179],v2 offset:5344
v_mfma_f32_16x16x32_bf16 a[28:31],a[152:155],v[120:123],a[28:31]
ds_read_b64_tr_b16 a[180:181],v2 offset:10464
v_accvgpr_read_b32 v220,a88
v_mfma_f32_16x16x32_bf16 a[28:31],a[148:151],v[116:119],a[28:31]
ds_read_b64_tr_b16 a[182:183],v2 offset:15584
v_accvgpr_read_b32 v221,a89
v_mfma_f32_16x16x32_bf16 a[28:31],a[144:147],v[112:115],a[28:31]
s_waitcnt lgkmcnt(4)
ds_read_b64_tr_b16 a[184:185],v2 offset:20704
v_bfe_u32 v24,v6,4,2
v_mfma_f32_16x16x32_bf16 a[32:35],a[160:163],v[64:67],0
ds_read_b64_tr_b16 a[186:187],v2 offset:25824
ds_read_b64_tr_b16 a[188:189],v2 offset:30944
v_mfma_f32_16x16x32_bf16 a[32:35],a[164:167],v[68:71],a[32:35]
ds_read_b64_tr_b16 a[190:191],v2 offset:36064
v_mul_f32 v220,v22,v220
v_mfma_f32_16x16x32_bf16 a[32:35],a[168:171],v[72:75],a[32:35]
v_mul_f32 v221,v22,v221
v_accvgpr_read_b32 v222,a90
v_mfma_f32_16x16x32_bf16 a[32:35],a[172:175],v[76:79],a[32:35]
buffer_store_dwordx4 v[180:183],v6,s[36:39],s85 offen offset:2048 sc1
v_mfma_f32_16x16x32_bf16 a[36:39],a[172:175],v[92:95],0
v_accvgpr_read_b32 v223,a91
v_min_u32 v24,2,v24
v_mfma_f32_16x16x32_bf16 a[36:39],a[168:171],v[88:91],a[36:39]
v_cvt_pk_bf16_f32 v220,v220,v221
v_mul_f32 v222,v22,v222
v_mfma_f32_16x16x32_bf16 a[36:39],a[164:167],v[84:87],a[36:39]
v_mul_f32 v223,v22,v223
v_add_u32 v24,s71,v24
v_mfma_f32_16x16x32_bf16 a[36:39],a[160:163],v[80:83],a[36:39]
buffer_load_dwordx4 v8,s[72:75],s83 offen offset:1024 lds
v_mfma_f32_16x16x32_bf16 a[40:43],a[160:163],v[96:99],0
v_cvt_pk_bf16_f32 v221,v222,v223
v_add_u32 v24,1,v24
v_mfma_f32_16x16x32_bf16 a[40:43],a[164:167],v[100:103],a[40:43]
v_accvgpr_read_b32 v222,a92
v_accvgpr_read_b32 v223,a93
v_mfma_f32_16x16x32_bf16 a[40:43],a[168:171],v[104:107],a[40:43]
v_and_b32 v24,3,v24
v_accvgpr_read_b32 v16,a94
v_mfma_f32_16x16x32_bf16 a[40:43],a[172:175],v[108:111],a[40:43]
v_accvgpr_read_b32 v17,a95
v_mul_f32 v222,v22,v222
v_mfma_f32_16x16x32_bf16 a[44:47],a[172:175],v[124:127],0
v_mul_f32 v223,v22,v223
v_lshlrev_b32 v24,6,v24
v_mfma_f32_16x16x32_bf16 a[44:47],a[168:171],v[120:123],a[44:47]
v_mul_f32 v16,v22,v16
v_mul_f32 v17,v22,v17
v_mfma_f32_16x16x32_bf16 a[44:47],a[164:167],v[116:119],a[44:47]
v_cvt_pk_bf16_f32 v222,v222,v223
v_cvt_pk_bf16_f32 v223,v16,v17
v_mfma_f32_16x16x32_bf16 a[44:47],a[160:163],v[112:115],a[44:47]
s_waitcnt lgkmcnt(0)
buffer_load_dwordx2 v[26:27],v24,s[64:67],s94 offen sc1
v_mfma_f32_16x16x32_bf16 a[48:51],a[176:179],v[64:67],0
ds_read_b64_tr_b16 a[128:129],v1 offset:256
v_max_f32 v23,0x00800000,v60
v_mfma_f32_16x16x32_bf16 a[48:51],a[180:183],v[68:71],a[48:51]
v_rcp_f32 v23,v23
v_mfma_f32_16x16x32_bf16 a[48:51],a[184:187],v[72:75],a[48:51]
ds_read_b64_tr_b16 a[130:131],v1 offset:5376
v_accvgpr_read_b32 v228,a96
v_mfma_f32_16x16x32_bf16 a[48:51],a[188:191],v[76:79],a[48:51]
buffer_load_dwordx4 v9,s[72:75],s83 offen offset:2048 lds
v_mfma_f32_16x16x32_bf16 a[52:55],a[188:191],v[92:95],0
v_accvgpr_read_b32 v229,a97
v_mul_f32 v228,v23,v228
v_mfma_f32_16x16x32_bf16 a[52:55],a[184:187],v[88:91],a[52:55]
v_mul_f32 v229,v23,v229
v_accvgpr_read_b32 v230,a98
v_mfma_f32_16x16x32_bf16 a[52:55],a[180:183],v[84:87],a[52:55]
ds_read_b64_tr_b16 a[132:133],v1 offset:10496
v_accvgpr_read_b32 v231,a99
v_mfma_f32_16x16x32_bf16 a[52:55],a[176:179],v[80:83],a[52:55]
buffer_store_dwordx4 v[188:191],v6,s[36:39],s85 offen offset:3072 sc1
v_mfma_f32_16x16x32_bf16 a[56:59],a[176:179],v[96:99],0
ds_read_b64_tr_b16 a[134:135],v1 offset:15616
v_cvt_pk_bf16_f32 v228,v228,v229
v_mfma_f32_16x16x32_bf16 a[56:59],a[180:183],v[100:103],a[56:59]
ds_read_b64_tr_b16 a[136:137],v1 offset:20736
ds_read_b64_tr_b16 a[138:139],v1 offset:25856
v_mfma_f32_16x16x32_bf16 a[56:59],a[184:187],v[104:107],a[56:59]
ds_read_b64_tr_b16 a[140:141],v1 offset:30976
ds_read_b64_tr_b16 a[142:143],v1 offset:36096
v_mfma_f32_16x16x32_bf16 a[56:59],a[188:191],v[108:111],a[56:59]
v_mul_f32 v230,v23,v230
v_mul_f32 v231,v23,v231
v_mfma_f32_16x16x32_bf16 a[60:63],a[188:191],v[124:127],0
v_cvt_pk_bf16_f32 v229,v230,v231
v_accvgpr_read_b32 v230,a100
v_mfma_f32_16x16x32_bf16 a[60:63],a[184:187],v[120:123],a[60:63]
v_accvgpr_read_b32 v231,a101
v_accvgpr_read_b32 v12,a102
v_mfma_f32_16x16x32_bf16 a[60:63],a[180:183],v[116:119],a[60:63]
v_accvgpr_read_b32 v13,a103
v_mul_f32 v230,v23,v230
v_mfma_f32_16x16x32_bf16 a[60:63],a[176:179],v[112:115],a[60:63]
s_waitcnt lgkmcnt(5)
v_mul_f32 v231,v23,v231
v_mul_f32 v12,v23,v12
v_mfma_f32_16x16x32_bf16 a[64:67],a[128:131],v[64:67],0
s_waitcnt lgkmcnt(4)
buffer_load_dwordx4 v10,s[72:75],s83 offen offset:3072 lds
v_mfma_f32_16x16x32_bf16 a[64:67],a[132:135],v[68:71],a[64:67]
s_waitcnt lgkmcnt(2)
v_mul_f32 v13,v23,v13
v_cvt_pk_bf16_f32 v230,v230,v231
v_mfma_f32_16x16x32_bf16 a[64:67],a[136:139],v[72:75],a[64:67]
s_waitcnt lgkmcnt(0)
v_cvt_pk_bf16_f32 v231,v12,v13
v_accvgpr_read_b32 v236,a104
v_mfma_f32_16x16x32_bf16 a[64:67],a[140:143],v[76:79],a[64:67]
v_accvgpr_read_b32 v237,a105
v_mul_f32 v236,v23,v236
v_mfma_f32_16x16x32_bf16 a[68:71],a[140:143],v[92:95],0
v_mul_f32 v237,v23,v237
v_accvgpr_read_b32 v238,a106
v_mfma_f32_16x16x32_bf16 a[68:71],a[136:139],v[88:91],a[68:71]
v_accvgpr_read_b32 v239,a107
v_cvt_pk_bf16_f32 v236,v236,v237
v_mfma_f32_16x16x32_bf16 a[68:71],a[132:135],v[84:87],a[68:71]
s_add_u32 m0,s82,132096
v_mul_f32 v238,v23,v238
v_mul_f32 v239,v23,v239
v_mfma_f32_16x16x32_bf16 a[68:71],a[128:131],v[80:83],a[68:71]
ds_read_b64_tr_b16 a[144:145],v2 offset:288
ds_read_b64_tr_b16 a[146:147],v2 offset:5408
v_mfma_f32_16x16x32_bf16 a[72:75],a[128:131],v[96:99],0
ds_read_b64_tr_b16 a[148:149],v2 offset:10528
ds_read_b64_tr_b16 a[150:151],v2 offset:15648
v_mfma_f32_16x16x32_bf16 a[72:75],a[132:135],v[100:103],a[72:75]
ds_read_b64_tr_b16 a[152:153],v2 offset:20768
ds_read_b64_tr_b16 a[154:155],v2 offset:25888
v_mfma_f32_16x16x32_bf16 a[72:75],a[136:139],v[104:107],a[72:75]
ds_read_b64_tr_b16 a[156:157],v2 offset:31008
ds_read_b64_tr_b16 a[158:159],v2 offset:36128
v_mfma_f32_16x16x32_bf16 a[72:75],a[140:143],v[108:111],a[72:75]
v_mfma_f32_16x16x32_bf16 a[76:79],a[140:143],v[124:127],0
buffer_load_dwordx4 v11,s[72:75],s83 offen lds
v_mfma_f32_16x16x32_bf16 a[76:79],a[136:139],v[120:123],a[76:79]
v_cvt_pk_bf16_f32 v237,v238,v239
v_accvgpr_read_b32 v238,a108
v_mfma_f32_16x16x32_bf16 a[76:79],a[132:135],v[116:119],a[76:79]
v_accvgpr_read_b32 v239,a109
v_accvgpr_read_b32 v14,a110
v_mfma_f32_16x16x32_bf16 a[76:79],a[128:131],v[112:115],a[76:79]
s_waitcnt lgkmcnt(6)
v_accvgpr_read_b32 v15,a111
v_mul_f32 v238,v23,v238
v_mfma_f32_16x16x32_bf16 a[80:83],a[144:147],v[64:67],0
s_waitcnt lgkmcnt(4)
v_mul_f32 v239,v23,v239
v_mul_f32 v14,v23,v14
v_mfma_f32_16x16x32_bf16 a[80:83],a[148:151],v[68:71],a[80:83]
s_waitcnt lgkmcnt(2)
v_mul_f32 v15,v23,v15
v_cvt_pk_bf16_f32 v238,v238,v239
v_mfma_f32_16x16x32_bf16 a[80:83],a[152:155],v[72:75],a[80:83]
s_waitcnt lgkmcnt(0)
v_cvt_pk_bf16_f32 v239,v14,v15
v_accvgpr_read_b32 v244,a112
v_mfma_f32_16x16x32_bf16 a[80:83],a[156:159],v[76:79],a[80:83]
v_accvgpr_read_b32 v245,a113
v_mul_f32 v244,v23,v244
v_mfma_f32_16x16x32_bf16 a[84:87],a[156:159],v[92:95],0
v_mul_f32 v245,v23,v245
v_accvgpr_read_b32 v246,a114
v_mfma_f32_16x16x32_bf16 a[84:87],a[152:155],v[88:91],a[84:87]
v_accvgpr_read_b32 v247,a115
v_cvt_pk_bf16_f32 v244,v244,v245
v_mfma_f32_16x16x32_bf16 a[84:87],a[148:151],v[84:87],a[84:87]
v_subrev_u32 v1,81920,v1
v_mul_f32 v246,v23,v246
v_mfma_f32_16x16x32_bf16 a[84:87],a[144:147],v[80:83],a[84:87]
ds_read_b64_tr_b16 a[160:161],v1
ds_read_b64_tr_b16 a[162:163],v1 offset:5120
v_mfma_f32_16x16x32_bf16 a[88:91],a[144:147],v[96:99],0
ds_read_b64_tr_b16 a[164:165],v1 offset:10240
ds_read_b64_tr_b16 a[166:167],v1 offset:15360
v_mfma_f32_16x16x32_bf16 a[88:91],a[148:151],v[100:103],a[88:91]
ds_read_b64_tr_b16 a[168:169],v1 offset:20480
ds_read_b64_tr_b16 a[170:171],v1 offset:25600
v_mfma_f32_16x16x32_bf16 a[88:91],a[152:155],v[104:107],a[88:91]
ds_read_b64_tr_b16 a[172:173],v1 offset:30720
ds_read_b64_tr_b16 a[174:175],v1 offset:35840
v_mfma_f32_16x16x32_bf16 a[88:91],a[156:159],v[108:111],a[88:91]
v_accvgpr_read_b32 v252,a120
v_accvgpr_read_b32 v253,a121
v_cmp_nlt_f32 s[56:57],v48,s63
v_mul_f32 v252,v23,v252
v_mul_f32 v253,v23,v253
v_accvgpr_read_b32 v254,a122
v_accvgpr_read_b32 v255,a123
v_mfma_f32_16x16x32_bf16 a[92:95],a[156:159],v[124:127],0
v_mul_f32 v247,v23,v247
v_cvt_pk_bf16_f32 v245,v246,v247
v_cvt_pk_bf16_f32 v252,v252,v253
v_mul_f32 v254,v23,v254
)A"
R"A(v_mul_f32 v255,v23,v255
v_cmp_nlt_f32 s[58:59],v52,s63
s_nop 1
s_or_b64 s[56:57],s[56:57],s[58:59]
v_cvt_pk_bf16_f32 v253,v254,v255
v_mfma_f32_16x16x32_bf16 a[92:95],a[152:155],v[120:123],a[92:95]
v_accvgpr_read_b32 v246,a116
v_accvgpr_read_b32 v247,a117
v_accvgpr_read_b32 v16,a118
v_accvgpr_read_b32 v17,a119
v_mul_f32 v246,v23,v246
v_mul_f32 v247,v23,v247
v_accvgpr_read_b32 v254,a124
v_accvgpr_read_b32 v255,a125
v_cmp_nlt_f32 s[58:59],v56,s63
s_nop 1
s_or_b64 s[56:57],s[56:57],s[58:59]
v_accvgpr_read_b32 v12,a126
v_accvgpr_read_b32 v13,a127
v_mul_f32 v254,v23,v254
v_mul_f32 v255,v23,v255
s_waitcnt vmcnt(4)
v_cmp_ne_u32 s[90:91],s68,v26
v_cmp_ne_u32 s[92:93],s69,v27
s_nop 0
s_or_b64 s[90:91],s[90:91],s[92:93]
s_cbranch_scc1 L_fr8_%=
L_fo8_%=:
v_mfma_f32_16x16x32_bf16 a[92:95],a[148:151],v[116:119],a[92:95]
v_mul_f32 v16,v23,v16
v_mul_f32 v17,v23,v17
v_cvt_pk_bf16_f32 v246,v246,v247
v_cvt_pk_bf16_f32 v247,v16,v17
v_mul_f32 v12,v23,v12
v_mul_f32 v13,v23,v13
v_cvt_pk_bf16_f32 v254,v254,v255
v_cmp_nlt_f32 s[58:59],v60,s63
s_nop 1
s_or_b64 s[56:57],s[56:57],s[58:59]
v_cvt_pk_bf16_f32 v255,v12,v13
v_mfma_f32_16x16x32_bf16 a[92:95],a[144:147],v[112:115],a[92:95]
s_cmp_lg_u64 s[56:57],0
s_cbranch_scc1 L_slow1_%=
L_sret1_%=:
v_subrev_u32 v2,81920,v2
s_waitcnt lgkmcnt(6)
v_mfma_f32_16x16x32_bf16 a[192:195],a[160:163],v[132:135],a[192:195]
s_cmp_ge_u32 0,s71
s_cselect_b32 s76,2,0
s_lshl_b32 s76,s76,16
s_waitcnt lgkmcnt(4)
v_mfma_f32_16x16x32_bf16 a[192:195],a[164:167],v[140:143],a[192:195]
s_add_u32 s77,s76,4096
s_add_u32 s78,s76,8192
s_add_u32 s79,s76,12288
s_max_i32 s76,s76,s18
s_waitcnt lgkmcnt(2)
v_mfma_f32_16x16x32_bf16 a[192:195],a[168:171],v[148:151],a[192:195]
s_max_i32 s77,s77,s19
buffer_load_dwordx4 v[128:131],v6,s[28:31],s76 offen sc1
s_waitcnt lgkmcnt(0)
v_mfma_f32_16x16x32_bf16 a[192:195],a[172:175],v[156:159],a[192:195]
s_max_i32 s78,s78,s100
ds_read_b64_tr_b16 a[176:177],v2 offset:32
ds_read_b64_tr_b16 a[178:179],v2 offset:5152
v_mfma_f32_16x16x32_bf16 a[196:199],a[172:175],v[188:191],a[196:199]
s_cmp_ge_u32 2,s71
s_cselect_b32 s80,6,4
ds_read_b64_tr_b16 a[180:181],v2 offset:10272
v_mfma_f32_16x16x32_bf16 a[196:199],a[168:171],v[180:183],a[196:199]
s_mul_i32 s80,s80,163840
s_add_u32 s80,s80,s81
ds_read_b64_tr_b16 a[182:183],v2 offset:15392
v_mfma_f32_16x16x32_bf16 a[196:199],a[164:167],v[172:175],a[196:199]
s_max_i32 s79,s79,s101
buffer_store_dwordx4 v[196:199],v6,s[36:39],s86 offen sc1
v_mfma_f32_16x16x32_bf16 a[196:199],a[160:163],v[164:167],a[196:199]
s_add_u32 s83,s80,20480
ds_read_b64_tr_b16 a[184:185],v2 offset:20512
ds_read_b64_tr_b16 a[186:187],v2 offset:25632
v_mfma_f32_16x16x32_bf16 a[200:203],a[160:163],v[196:199],a[200:203]
ds_read_b64_tr_b16 a[188:189],v2 offset:30752
v_mfma_f32_16x16x32_bf16 a[200:203],a[164:167],v[204:207],a[200:203]
ds_read_b64_tr_b16 a[190:191],v2 offset:35872
v_mfma_f32_16x16x32_bf16 a[200:203],a[168:171],v[212:215],a[200:203]
buffer_load_dwordx4 v[160:163],v6,s[28:31],s76 offen offset:1024 sc1
v_mfma_f32_16x16x32_bf16 a[200:203],a[172:175],v[220:223],a[200:203]
ds_read_b64_tr_b16 a[128:129],v1 offset:64
ds_read_b64_tr_b16 a[130:131],v1 offset:5184
v_mfma_f32_16x16x32_bf16 a[204:207],a[172:175],v[252:255],a[204:207]
ds_read_b64_tr_b16 a[132:133],v1 offset:10304
v_mfma_f32_16x16x32_bf16 a[204:207],a[168:171],v[244:247],a[204:207]
ds_read_b64_tr_b16 a[134:135],v1 offset:15424
v_mfma_f32_16x16x32_bf16 a[204:207],a[164:167],v[236:239],a[204:207]
buffer_store_dwordx4 v[204:207],v6,s[36:39],s86 offen offset:1024 sc1
v_mfma_f32_16x16x32_bf16 a[204:207],a[160:163],v[228:231],a[204:207]
s_barrier
ds_read_b64_tr_b16 a[136:137],v1 offset:20544
ds_read_b64_tr_b16 a[138:139],v1 offset:25664
s_waitcnt lgkmcnt(8)
v_mfma_f32_16x16x32_bf16 a[208:211],a[176:179],v[132:135],a[208:211]
ds_read_b64_tr_b16 a[140:141],v1 offset:30784
v_mfma_f32_16x16x32_bf16 a[208:211],a[180:183],v[140:143],a[208:211]
buffer_load_dwordx4 v[192:195],v6,s[28:31],s76 offen offset:2048 sc1
v_mfma_f32_16x16x32_bf16 a[208:211],a[184:187],v[148:151],a[208:211]
s_add_u32 m0,s82,81920
ds_read_b64_tr_b16 a[142:143],v1 offset:35904
ds_read_b64_tr_b16 a[144:145],v2 offset:96
s_waitcnt lgkmcnt(9)
v_mfma_f32_16x16x32_bf16 a[208:211],a[188:191],v[156:159],a[208:211]
ds_read_b64_tr_b16 a[146:147],v2 offset:5216
v_mfma_f32_16x16x32_bf16 a[212:215],a[188:191],v[188:191],a[212:215]
ds_read_b64_tr_b16 a[148:149],v2 offset:10336
v_mfma_f32_16x16x32_bf16 a[212:215],a[184:187],v[180:183],a[212:215]
buffer_store_dwordx4 v[212:215],v6,s[36:39],s86 offen offset:2048 sc1
v_mfma_f32_16x16x32_bf16 a[212:215],a[180:183],v[172:175],a[212:215]
ds_read_b64_tr_b16 a[150:151],v2 offset:15456
ds_read_b64_tr_b16 a[152:153],v2 offset:20576
v_mfma_f32_16x16x32_bf16 a[212:215],a[176:179],v[164:167],a[212:215]
ds_read_b64_tr_b16 a[154:155],v2 offset:25696
v_mfma_f32_16x16x32_bf16 a[216:219],a[176:179],v[196:199],a[216:219]
ds_read_b64_tr_b16 a[156:157],v2 offset:30816
v_mfma_f32_16x16x32_bf16 a[216:219],a[180:183],v[204:207],a[216:219]
buffer_load_dwordx4 v[224:227],v6,s[28:31],s76 offen offset:3072 sc1
v_mfma_f32_16x16x32_bf16 a[216:219],a[184:187],v[212:215],a[216:219]
ds_read_b64_tr_b16 a[158:159],v2 offset:35936
ds_read_b64_tr_b16 a[160:161],v1 offset:128
v_mfma_f32_16x16x32_bf16 a[216:219],a[188:191],v[220:223],a[216:219]
ds_read_b64_tr_b16 a[162:163],v1 offset:5248
v_mfma_f32_16x16x32_bf16 a[220:223],a[188:191],v[252:255],a[220:223]
ds_read_b64_tr_b16 a[164:165],v1 offset:10368
v_mfma_f32_16x16x32_bf16 a[220:223],a[184:187],v[244:247],a[220:223]
buffer_load_dwordx4 v7,s[72:75],s80 offen lds
v_mfma_f32_16x16x32_bf16 a[220:223],a[180:183],v[236:239],a[220:223]
ds_read_b64_tr_b16 a[166:167],v1 offset:15488
ds_read_b64_tr_b16 a[168:169],v1 offset:20608
v_mfma_f32_16x16x32_bf16 a[220:223],a[176:179],v[228:231],a[220:223]
s_waitcnt lgkmcnt(7)
ds_read_b64_tr_b16 a[170:171],v1 offset:25728
v_mfma_f32_16x16x32_bf16 a[224:227],a[128:131],v[132:135],a[224:227]
ds_read_b64_tr_b16 a[172:173],v1 offset:30848
v_mfma_f32_16x16x32_bf16 a[224:227],a[132:135],v[140:143],a[224:227]
buffer_load_dwordx4 v[136:139],v6,s[28:31],s77 offen sc1
v_mfma_f32_16x16x32_bf16 a[224:227],a[136:139],v[148:151],a[224:227]
ds_read_b64_tr_b16 a[174:175],v1 offset:35968
ds_read_b64_tr_b16 a[176:177],v2 offset:160
v_mfma_f32_16x16x32_bf16 a[224:227],a[140:143],v[156:159],a[224:227]
ds_read_b64_tr_b16 a[178:179],v2 offset:5280
v_mfma_f32_16x16x32_bf16 a[228:231],a[140:143],v[188:191],a[228:231]
ds_read_b64_tr_b16 a[180:181],v2 offset:10400
v_mfma_f32_16x16x32_bf16 a[228:231],a[136:139],v[180:183],a[228:231]
buffer_load_dwordx4 v[168:171],v6,s[28:31],s77 offen offset:1024 sc1
v_mfma_f32_16x16x32_bf16 a[228:231],a[132:135],v[172:175],a[228:231]
ds_read_b64_tr_b16 a[182:183],v2 offset:15520
ds_read_b64_tr_b16 a[184:185],v2 offset:20640
v_mfma_f32_16x16x32_bf16 a[228:231],a[128:131],v[164:167],a[228:231]
ds_read_b64_tr_b16 a[186:187],v2 offset:25760
v_mfma_f32_16x16x32_bf16 a[232:235],a[128:131],v[196:199],a[232:235]
ds_read_b64_tr_b16 a[188:189],v2 offset:30880
v_mfma_f32_16x16x32_bf16 a[232:235],a[132:135],v[204:207],a[232:235]
buffer_load_dwordx4 v8,s[72:75],s80 offen offset:1024 lds
v_mfma_f32_16x16x32_bf16 a[232:235],a[136:139],v[212:215],a[232:235]
ds_read_b64_tr_b16 a[190:191],v2 offset:36000
v_mfma_f32_16x16x32_bf16 a[232:235],a[140:143],v[220:223],a[232:235]
v_mfma_f32_16x16x32_bf16 a[236:239],a[140:143],v[252:255],a[236:239]
ds_read_b64_tr_b16 a[140:141],v1 offset:30912
ds_read_b64_tr_b16 a[142:143],v1 offset:36032
v_mfma_f32_16x16x32_bf16 a[236:239],a[136:139],v[244:247],a[236:239]
buffer_load_dwordx4 v[200:203],v6,s[28:31],s77 offen offset:2048 sc1
v_mfma_f32_16x16x32_bf16 a[236:239],a[132:135],v[236:239],a[236:239]
ds_read_b64_tr_b16 a[132:133],v1 offset:10432
ds_read_b64_tr_b16 a[134:135],v1 offset:15552
v_mfma_f32_16x16x32_bf16 a[236:239],a[128:131],v[228:231],a[236:239]
ds_read_b64_tr_b16 a[128:129],v1 offset:192
v_mfma_f32_16x16x32_bf16 a[240:243],a[144:147],v[132:135],a[240:243]
ds_read_b64_tr_b16 a[130:131],v1 offset:5312
v_mfma_f32_16x16x32_bf16 a[240:243],a[148:151],v[140:143],a[240:243]
buffer_store_dwordx4 v[220:223],v6,s[36:39],s86 offen offset:3072 sc1
v_mfma_f32_16x16x32_bf16 a[240:243],a[152:155],v[148:151],a[240:243]
s_waitcnt lgkmcnt(6)
ds_read_b64_tr_b16 a[136:137],v1 offset:20672
ds_read_b64_tr_b16 a[138:139],v1 offset:25792
v_mfma_f32_16x16x32_bf16 a[240:243],a[156:159],v[156:159],a[240:243]
v_mfma_f32_16x16x32_bf16 a[244:247],a[156:159],v[188:191],a[244:247]
v_mfma_f32_16x16x32_bf16 a[244:247],a[152:155],v[180:183],a[244:247]
buffer_load_dwordx4 v[232:235],v6,s[28:31],s77 offen offset:3072 sc1
v_mfma_f32_16x16x32_bf16 a[244:247],a[148:151],v[172:175],a[244:247]
v_mfma_f32_16x16x32_bf16 a[244:247],a[144:147],v[164:167],a[244:247]
v_mfma_f32_16x16x32_bf16 a[248:251],a[144:147],v[196:199],a[248:251]
v_mfma_f32_16x16x32_bf16 a[248:251],a[148:151],v[204:207],a[248:251]
buffer_load_dwordx4 v9,s[72:75],s80 offen offset:2048 lds
v_mfma_f32_16x16x32_bf16 a[248:251],a[152:155],v[212:215],a[248:251]
v_mfma_f32_16x16x32_bf16 a[248:251],a[156:159],v[220:223],a[248:251]
v_mfma_f32_16x16x32_bf16 a[252:255],a[156:159],v[252:255],a[252:255]
ds_read_b64_tr_b16 a[156:157],v2 offset:30944
ds_read_b64_tr_b16 a[158:159],v2 offset:36064
v_mfma_f32_16x16x32_bf16 a[252:255],a[152:155],v[244:247],a[252:255]
buffer_load_dwordx4 v[144:147],v6,s[28:31],s78 offen sc1
v_mfma_f32_16x16x32_bf16 a[252:255],a[148:151],v[236:239],a[252:255]
ds_read_b64_tr_b16 a[148:149],v2 offset:10464
ds_read_b64_tr_b16 a[150:151],v2 offset:15584
v_mfma_f32_16x16x32_bf16 a[252:255],a[144:147],v[228:231],a[252:255]
ds_read_b64_tr_b16 a[144:145],v2 offset:224
v_mfma_f32_16x16x32_bf16 a[0:3],a[160:163],v[132:135],a[0:3]
ds_read_b64_tr_b16 a[146:147],v2 offset:5344
v_mfma_f32_16x16x32_bf16 a[0:3],a[164:167],v[140:143],a[0:3]
buffer_load_dwordx4 v[176:179],v6,s[28:31],s78 offen offset:1024 sc1
v_mfma_f32_16x16x32_bf16 a[0:3],a[168:171],v[148:151],a[0:3]
ds_read_b64_tr_b16 a[152:153],v2 offset:20704
ds_read_b64_tr_b16 a[154:155],v2 offset:25824
v_mfma_f32_16x16x32_bf16 a[0:3],a[172:175],v[156:159],a[0:3]
v_mfma_f32_16x16x32_bf16 a[4:7],a[172:175],v[188:191],a[4:7]
v_mfma_f32_16x16x32_bf16 a[4:7],a[168:171],v[180:183],a[4:7]
buffer_load_dwordx4 v10,s[72:75],s80 offen offset:3072 lds
v_mfma_f32_16x16x32_bf16 a[4:7],a[164:167],v[172:175],a[4:7]
v_mfma_f32_16x16x32_bf16 a[4:7],a[160:163],v[164:167],a[4:7]
v_mfma_f32_16x16x32_bf16 a[8:11],a[160:163],v[196:199],a[8:11]
v_mfma_f32_16x16x32_bf16 a[8:11],a[164:167],v[204:207],a[8:11]
buffer_load_dwordx4 v[208:211],v6,s[28:31],s78 offen offset:2048 sc1
v_mfma_f32_16x16x32_bf16 a[8:11],a[168:171],v[212:215],a[8:11]
v_mfma_f32_16x16x32_bf16 a[8:11],a[172:175],v[220:223],a[8:11]
v_mfma_f32_16x16x32_bf16 a[12:15],a[172:175],v[252:255],a[12:15]
ds_read_b64_tr_b16 a[172:173],v1 offset:30976
ds_read_b64_tr_b16 a[174:175],v1 offset:36096
v_mfma_f32_16x16x32_bf16 a[12:15],a[168:171],v[244:247],a[12:15]
buffer_store_dwordx4 v[228:231],v6,s[36:39],s87 offen sc1
v_mfma_f32_16x16x32_bf16 a[12:15],a[164:167],v[236:239],a[12:15]
s_add_u32 m0,s82,86016
ds_read_b64_tr_b16 a[164:165],v1 offset:10496
ds_read_b64_tr_b16 a[166:167],v1 offset:15616
v_mfma_f32_16x16x32_bf16 a[12:15],a[160:163],v[228:231],a[12:15]
ds_read_b64_tr_b16 a[160:161],v1 offset:256
v_mfma_f32_16x16x32_bf16 a[16:19],a[176:179],v[132:135],a[16:19]
ds_read_b64_tr_b16 a[162:163],v1 offset:5376
v_mfma_f32_16x16x32_bf16 a[16:19],a[180:183],v[140:143],a[16:19]
buffer_load_dwordx4 v[240:243],v6,s[28:31],s78 offen offset:3072 sc1
v_mfma_f32_16x16x32_bf16 a[16:19],a[184:187],v[148:151],a[16:19]
ds_read_b64_tr_b16 a[168:169],v1 offset:20736
ds_read_b64_tr_b16 a[170:171],v1 offset:25856
v_mfma_f32_16x16x32_bf16 a[16:19],a[188:191],v[156:159],a[16:19]
v_add_u32 v1,40960,v1
v_mfma_f32_16x16x32_bf16 a[20:23],a[188:191],v[188:191],a[20:23]
ds_read_b64_tr_b16 a[96:97],v1 offset:128
ds_read_b64_tr_b16 a[98:99],v1 offset:5248
v_mfma_f32_16x16x32_bf16 a[20:23],a[184:187],v[180:183],a[20:23]
buffer_load_dwordx4 v11,s[72:75],s80 offen lds
)A"
R"A(v_mfma_f32_16x16x32_bf16 a[20:23],a[180:183],v[172:175],a[20:23]
ds_read_b64_tr_b16 a[100:101],v1 offset:10368
ds_read_b64_tr_b16 a[102:103],v1 offset:15488
v_mfma_f32_16x16x32_bf16 a[20:23],a[176:179],v[164:167],a[20:23]
ds_read_b64_tr_b16 a[104:105],v1 offset:20608
v_mfma_f32_16x16x32_bf16 a[24:27],a[176:179],v[196:199],a[24:27]
ds_read_b64_tr_b16 a[106:107],v1 offset:25728
v_mfma_f32_16x16x32_bf16 a[24:27],a[180:183],v[204:207],a[24:27]
buffer_load_dwordx4 v[152:155],v6,s[28:31],s79 offen sc1
v_mfma_f32_16x16x32_bf16 a[24:27],a[184:187],v[212:215],a[24:27]
ds_read_b64_tr_b16 a[108:109],v1 offset:30848
ds_read_b64_tr_b16 a[110:111],v1 offset:35968
v_mfma_f32_16x16x32_bf16 a[24:27],a[188:191],v[220:223],a[24:27]
v_mfma_f32_16x16x32_bf16 a[28:31],a[188:191],v[252:255],a[28:31]
ds_read_b64_tr_b16 a[188:189],v2 offset:31008
ds_read_b64_tr_b16 a[190:191],v2 offset:36128
v_mfma_f32_16x16x32_bf16 a[28:31],a[184:187],v[244:247],a[28:31]
s_add_u32 m0,s82,87040
buffer_load_dwordx4 v[184:187],v6,s[28:31],s79 offen offset:1024 sc1
v_mfma_f32_16x16x32_bf16 a[28:31],a[180:183],v[236:239],a[28:31]
ds_read_b64_tr_b16 a[180:181],v2 offset:10528
ds_read_b64_tr_b16 a[182:183],v2 offset:15648
v_mfma_f32_16x16x32_bf16 a[28:31],a[176:179],v[228:231],a[28:31]
s_waitcnt lgkmcnt(7)
ds_read_b64_tr_b16 a[176:177],v2 offset:288
v_mfma_f32_16x16x32_bf16 a[32:35],a[128:131],v[132:135],a[32:35]
ds_read_b64_tr_b16 a[178:179],v2 offset:5408
v_mfma_f32_16x16x32_bf16 a[32:35],a[132:135],v[140:143],a[32:35]
buffer_store_dwordx4 v[236:239],v6,s[36:39],s87 offen offset:1024 sc1
v_mfma_f32_16x16x32_bf16 a[32:35],a[136:139],v[148:151],a[32:35]
ds_read_b64_tr_b16 a[184:185],v2 offset:20768
ds_read_b64_tr_b16 a[186:187],v2 offset:25888
v_mfma_f32_16x16x32_bf16 a[32:35],a[140:143],v[156:159],a[32:35]
v_add_u32 v2,40960,v2
v_mfma_f32_16x16x32_bf16 a[36:39],a[140:143],v[188:191],a[36:39]
ds_read_b64_tr_b16 a[112:113],v2 offset:160
ds_read_b64_tr_b16 a[114:115],v2 offset:5280
v_mfma_f32_16x16x32_bf16 a[36:39],a[136:139],v[180:183],a[36:39]
buffer_load_dwordx4 v[216:219],v6,s[28:31],s79 offen offset:2048 sc1
v_mfma_f32_16x16x32_bf16 a[36:39],a[132:135],v[172:175],a[36:39]
ds_read_b64_tr_b16 a[116:117],v2 offset:10400
ds_read_b64_tr_b16 a[118:119],v2 offset:15520
v_mfma_f32_16x16x32_bf16 a[36:39],a[128:131],v[164:167],a[36:39]
ds_read_b64_tr_b16 a[120:121],v2 offset:20640
v_mfma_f32_16x16x32_bf16 a[40:43],a[128:131],v[196:199],a[40:43]
ds_read_b64_tr_b16 a[122:123],v2 offset:25760
v_mfma_f32_16x16x32_bf16 a[40:43],a[132:135],v[204:207],a[40:43]
buffer_load_dwordx4 v7,s[72:75],s83 offen lds
v_mfma_f32_16x16x32_bf16 a[40:43],a[136:139],v[212:215],a[40:43]
ds_read_b64_tr_b16 a[124:125],v2 offset:30880
ds_read_b64_tr_b16 a[126:127],v2 offset:36000
v_mfma_f32_16x16x32_bf16 a[40:43],a[140:143],v[220:223],a[40:43]
v_mfma_f32_16x16x32_bf16 a[44:47],a[140:143],v[252:255],a[44:47]
ds_read_b64_tr_b16 a[140:141],v1 offset:30720
ds_read_b64_tr_b16 a[142:143],v1 offset:35840
v_mfma_f32_16x16x32_bf16 a[44:47],a[136:139],v[244:247],a[44:47]
buffer_load_dwordx4 v[248:251],v6,s[28:31],s79 offen offset:3072 sc1
v_mfma_f32_16x16x32_bf16 a[44:47],a[132:135],v[236:239],a[44:47]
ds_read_b64_tr_b16 a[132:133],v1 offset:10240
ds_read_b64_tr_b16 a[134:135],v1 offset:15360
v_mfma_f32_16x16x32_bf16 a[44:47],a[128:131],v[228:231],a[44:47]
ds_read_b64_tr_b16 a[128:129],v1
v_mfma_f32_16x16x32_bf16 a[48:51],a[144:147],v[132:135],a[48:51]
ds_read_b64_tr_b16 a[130:131],v1 offset:5120
v_mfma_f32_16x16x32_bf16 a[48:51],a[148:151],v[140:143],a[48:51]
buffer_store_dwordx4 v[244:247],v6,s[36:39],s87 offen offset:2048 sc1
v_mfma_f32_16x16x32_bf16 a[48:51],a[152:155],v[148:151],a[48:51]
ds_read_b64_tr_b16 a[136:137],v1 offset:20480
ds_read_b64_tr_b16 a[138:139],v1 offset:25600
v_mfma_f32_16x16x32_bf16 a[48:51],a[156:159],v[156:159],a[48:51]
s_cmp_ge_u32 1,s71
s_cselect_b32 s76,4,2
s_lshl_b32 s76,s76,16
v_mfma_f32_16x16x32_bf16 a[52:55],a[156:159],v[188:191],a[52:55]
s_add_u32 s77,s76,4096
s_add_u32 s78,s76,8192
s_add_u32 s79,s76,12288
v_mfma_f32_16x16x32_bf16 a[52:55],a[152:155],v[180:183],a[52:55]
s_max_i32 s76,s76,s18
buffer_load_dwordx4 v8,s[72:75],s83 offen offset:1024 lds
v_mfma_f32_16x16x32_bf16 a[52:55],a[148:151],v[172:175],a[52:55]
s_max_i32 s77,s77,s19
s_max_i32 s78,s78,s100
s_max_i32 s79,s79,s101
v_mfma_f32_16x16x32_bf16 a[52:55],a[144:147],v[164:167],a[52:55]
v_mfma_f32_16x16x32_bf16 a[56:59],a[144:147],v[196:199],a[56:59]
v_mfma_f32_16x16x32_bf16 a[56:59],a[148:151],v[204:207],a[56:59]
buffer_store_dwordx4 v[252:255],v6,s[36:39],s87 offen offset:3072 sc1
v_mfma_f32_16x16x32_bf16 a[56:59],a[152:155],v[212:215],a[56:59]
v_mfma_f32_16x16x32_bf16 a[56:59],a[156:159],v[220:223],a[56:59]
v_mfma_f32_16x16x32_bf16 a[60:63],a[156:159],v[252:255],a[60:63]
ds_read_b64_tr_b16 a[156:157],v2 offset:30752
ds_read_b64_tr_b16 a[158:159],v2 offset:35872
v_mfma_f32_16x16x32_bf16 a[60:63],a[152:155],v[244:247],a[60:63]
ds_read_b64_tr_b16 a[152:153],v2 offset:20512
v_mfma_f32_16x16x32_bf16 a[60:63],a[148:151],v[236:239],a[60:63]
buffer_load_dwordx4 v9,s[72:75],s83 offen offset:2048 lds
v_mfma_f32_16x16x32_bf16 a[60:63],a[144:147],v[228:231],a[60:63]
ds_read_b64_tr_b16 a[144:145],v2 offset:32
ds_read_b64_tr_b16 a[146:147],v2 offset:5152
v_mfma_f32_16x16x32_bf16 a[64:67],a[160:163],v[132:135],a[64:67]
ds_read_b64_tr_b16 a[148:149],v2 offset:10272
v_mfma_f32_16x16x32_bf16 a[64:67],a[164:167],v[140:143],a[64:67]
ds_read_b64_tr_b16 a[150:151],v2 offset:15392
v_mfma_f32_16x16x32_bf16 a[64:67],a[168:171],v[148:151],a[64:67]
ds_read_b64_tr_b16 a[154:155],v2 offset:25632
v_mfma_f32_16x16x32_bf16 a[64:67],a[172:175],v[156:159],a[64:67]
v_mfma_f32_16x16x32_bf16 a[68:71],a[172:175],v[188:191],a[68:71]
v_mfma_f32_16x16x32_bf16 a[68:71],a[168:171],v[180:183],a[68:71]
v_mfma_f32_16x16x32_bf16 a[68:71],a[164:167],v[172:175],a[68:71]
v_mfma_f32_16x16x32_bf16 a[68:71],a[160:163],v[164:167],a[68:71]
v_mfma_f32_16x16x32_bf16 a[72:75],a[160:163],v[196:199],a[72:75]
v_mfma_f32_16x16x32_bf16 a[72:75],a[164:167],v[204:207],a[72:75]
v_mfma_f32_16x16x32_bf16 a[72:75],a[168:171],v[212:215],a[72:75]
v_mfma_f32_16x16x32_bf16 a[72:75],a[172:175],v[220:223],a[72:75]
buffer_load_dwordx4 v[64:67],v6,s[28:31],s76 offen sc1
v_mfma_f32_16x16x32_bf16 a[76:79],a[172:175],v[252:255],a[76:79]
ds_read_b64_tr_b16 a[172:173],v1 offset:30784
ds_read_b64_tr_b16 a[174:175],v1 offset:35904
v_mfma_f32_16x16x32_bf16 a[76:79],a[168:171],v[244:247],a[76:79]
ds_read_b64_tr_b16 a[168:169],v1 offset:20544
v_mfma_f32_16x16x32_bf16 a[76:79],a[164:167],v[236:239],a[76:79]
ds_read_b64_tr_b16 a[164:165],v1 offset:10304
v_mfma_f32_16x16x32_bf16 a[76:79],a[160:163],v[228:231],a[76:79]
s_waitcnt lgkmcnt(4)
buffer_load_dwordx4 v10,s[72:75],s83 offen offset:3072 lds
v_mfma_f32_16x16x32_bf16 a[80:83],a[176:179],v[132:135],a[80:83]
ds_read_b64_tr_b16 a[160:161],v1 offset:64
ds_read_b64_tr_b16 a[162:163],v1 offset:5184
v_mfma_f32_16x16x32_bf16 a[80:83],a[180:183],v[140:143],a[80:83]
ds_read_b64_tr_b16 a[166:167],v1 offset:15424
v_mfma_f32_16x16x32_bf16 a[80:83],a[184:187],v[148:151],a[80:83]
ds_read_b64_tr_b16 a[170:171],v1 offset:25664
v_mfma_f32_16x16x32_bf16 a[80:83],a[188:191],v[156:159],a[80:83]
buffer_load_dwordx4 v[68:71],v6,s[28:31],s76 offen offset:1024 sc1
v_mfma_f32_16x16x32_bf16 a[84:87],a[188:191],v[188:191],a[84:87]
v_mfma_f32_16x16x32_bf16 a[84:87],a[184:187],v[180:183],a[84:87]
v_mfma_f32_16x16x32_bf16 a[84:87],a[180:183],v[172:175],a[84:87]
v_mfma_f32_16x16x32_bf16 a[84:87],a[176:179],v[164:167],a[84:87]
v_mfma_f32_16x16x32_bf16 a[88:91],a[176:179],v[196:199],a[88:91]
v_mfma_f32_16x16x32_bf16 a[88:91],a[180:183],v[204:207],a[88:91]
s_add_u32 m0,s82,91136
v_mfma_f32_16x16x32_bf16 a[88:91],a[184:187],v[212:215],a[88:91]
v_mfma_f32_16x16x32_bf16 a[88:91],a[188:191],v[220:223],a[88:91]
buffer_load_dwordx4 v[72:75],v6,s[28:31],s76 offen offset:2048 sc1
v_mfma_f32_16x16x32_bf16 a[92:95],a[188:191],v[252:255],a[92:95]
ds_read_b64_tr_b16 a[188:189],v2 offset:30816
ds_read_b64_tr_b16 a[190:191],v2 offset:35936
v_mfma_f32_16x16x32_bf16 a[92:95],a[184:187],v[244:247],a[92:95]
ds_read_b64_tr_b16 a[184:185],v2 offset:20576
v_mfma_f32_16x16x32_bf16 a[92:95],a[180:183],v[236:239],a[92:95]
s_cmp_ge_u32 0,s71
s_cselect_b32 s80,3,1
s_mul_i32 s80,s80,163840
v_mfma_f32_16x16x32_bf16 a[92:95],a[176:179],v[228:231],a[92:95]
s_add_u32 s80,s80,s81
buffer_load_dwordx4 v11,s[72:75],s83 offen lds
s_waitcnt vmcnt(14)
v_mfma_f32_16x16x32_bf16 a[192:195],a[128:131],v[128:131],a[192:195]
s_add_u32 s83,s80,20480
ds_read_b64_tr_b16 a[176:177],v2 offset:96
ds_read_b64_tr_b16 a[178:179],v2 offset:5216
v_mfma_f32_16x16x32_bf16 a[192:195],a[132:135],v[160:163],a[192:195]
ds_read_b64_tr_b16 a[180:181],v2 offset:10336
v_mfma_f32_16x16x32_bf16 a[192:195],a[136:139],v[192:195],a[192:195]
ds_read_b64_tr_b16 a[182:183],v2 offset:15456
v_mfma_f32_16x16x32_bf16 a[192:195],a[140:143],v[224:227],a[192:195]
buffer_load_dwordx4 v[76:79],v6,s[28:31],s76 offen offset:3072 sc1
v_mfma_f32_16x16x32_bf16 a[196:199],a[140:143],v[232:235],a[196:199]
ds_read_b64_tr_b16 a[186:187],v2 offset:25696
v_mfma_f32_16x16x32_bf16 a[196:199],a[136:139],v[200:203],a[196:199]
v_mfma_f32_16x16x32_bf16 a[196:199],a[132:135],v[168:171],a[196:199]
v_mfma_f32_16x16x32_bf16 a[196:199],a[128:131],v[136:139],a[196:199]
v_mfma_f32_16x16x32_bf16 a[200:203],a[128:131],v[144:147],a[200:203]
s_lshl_b32 s95,s42,2
s_add_u32 s95,s95,s40
s_lshl_b32 s95,s95,4
v_mfma_f32_16x16x32_bf16 a[200:203],a[132:135],v[176:179],a[200:203]
v_mov_b32 v18,s68
v_mov_b32 v19,s69
v_mfma_f32_16x16x32_bf16 a[200:203],a[136:139],v[208:211],a[200:203]
v_mfma_f32_16x16x32_bf16 a[200:203],a[140:143],v[240:243],a[200:203]
s_waitcnt vmcnt(10)
buffer_load_dwordx4 v[80:83],v6,s[28:31],s77 offen sc1
v_mfma_f32_16x16x32_bf16 a[204:207],a[140:143],v[248:251],a[204:207]
ds_read_b64_tr_b16 a[140:141],v1 offset:30912
ds_read_b64_tr_b16 a[142:143],v1 offset:36032
v_mfma_f32_16x16x32_bf16 a[204:207],a[136:139],v[216:219],a[204:207]
ds_read_b64_tr_b16 a[136:137],v1 offset:20672
v_mfma_f32_16x16x32_bf16 a[204:207],a[132:135],v[184:187],a[204:207]
ds_read_b64_tr_b16 a[132:133],v1 offset:10432
v_mfma_f32_16x16x32_bf16 a[204:207],a[128:131],v[152:155],a[204:207]
s_barrier
s_waitcnt vmcnt(8)
s_mov_b64 exec,1
buffer_store_dwordx2 v[18:19],v6,s[64:67],s95 offen offset:1024 sc1
s_mov_b64 exec,-1
v_mfma_f32_16x16x32_bf16 a[208:211],a[144:147],v[128:131],a[208:211]
ds_read_b64_tr_b16 a[128:129],v1 offset:192
ds_read_b64_tr_b16 a[130:131],v1 offset:5312
v_mfma_f32_16x16x32_bf16 a[208:211],a[148:151],v[160:163],a[208:211]
ds_read_b64_tr_b16 a[134:135],v1 offset:15552
v_mfma_f32_16x16x32_bf16 a[208:211],a[152:155],v[192:195],a[208:211]
s_add_u32 m0,s82,0
ds_read_b64_tr_b16 a[138:139],v1 offset:25792
v_mfma_f32_16x16x32_bf16 a[208:211],a[156:159],v[224:227],a[208:211]
buffer_load_dwordx4 v[84:87],v6,s[28:31],s77 offen offset:1024 sc1
v_mfma_f32_16x16x32_bf16 a[212:215],a[156:159],v[232:235],a[212:215]
v_mfma_f32_16x16x32_bf16 a[212:215],a[152:155],v[200:203],a[212:215]
v_mfma_f32_16x16x32_bf16 a[212:215],a[148:151],v[168:171],a[212:215]
v_mfma_f32_16x16x32_bf16 a[212:215],a[144:147],v[136:139],a[212:215]
buffer_load_dwordx4 v7,s[72:75],s80 offen lds
v_mfma_f32_16x16x32_bf16 a[216:219],a[144:147],v[144:147],a[216:219]
v_mfma_f32_16x16x32_bf16 a[216:219],a[148:151],v[176:179],a[216:219]
v_mfma_f32_16x16x32_bf16 a[216:219],a[152:155],v[208:211],a[216:219]
v_mfma_f32_16x16x32_bf16 a[216:219],a[156:159],v[240:243],a[216:219]
buffer_load_dwordx4 v[88:91],v6,s[28:31],s77 offen offset:2048 sc1
v_mfma_f32_16x16x32_bf16 a[220:223],a[156:159],v[248:251],a[220:223]
ds_read_b64_tr_b16 a[156:157],v2 offset:30944
ds_read_b64_tr_b16 a[158:159],v2 offset:36064
v_mfma_f32_16x16x32_bf16 a[220:223],a[152:155],v[216:219],a[220:223]
ds_read_b64_tr_b16 a[152:153],v2 offset:20704
v_mfma_f32_16x16x32_bf16 a[220:223],a[148:151],v[184:187],a[220:223]
ds_read_b64_tr_b16 a[148:149],v2 offset:10464
v_mfma_f32_16x16x32_bf16 a[220:223],a[144:147],v[152:155],a[220:223]
s_waitcnt lgkmcnt(4)
)A"
R"A(ds_read_b64_tr_b16 a[144:145],v2 offset:224
v_mfma_f32_16x16x32_bf16 a[224:227],a[160:163],v[128:131],a[224:227]
ds_read_b64_tr_b16 a[146:147],v2 offset:5344
ds_read_b64_tr_b16 a[150:151],v2 offset:15584
v_mfma_f32_16x16x32_bf16 a[224:227],a[164:167],v[160:163],a[224:227]
ds_read_b64_tr_b16 a[154:155],v2 offset:25824
v_mfma_f32_16x16x32_bf16 a[224:227],a[168:171],v[192:195],a[224:227]
buffer_load_dwordx4 v[92:95],v6,s[28:31],s77 offen offset:3072 sc1
v_mfma_f32_16x16x32_bf16 a[224:227],a[172:175],v[224:227],a[224:227]
v_mfma_f32_16x16x32_bf16 a[228:231],a[172:175],v[232:235],a[228:231]
v_mfma_f32_16x16x32_bf16 a[228:231],a[168:171],v[200:203],a[228:231]
v_mfma_f32_16x16x32_bf16 a[228:231],a[164:167],v[168:171],a[228:231]
buffer_load_dwordx4 v8,s[72:75],s80 offen offset:1024 lds
v_mfma_f32_16x16x32_bf16 a[228:231],a[160:163],v[136:139],a[228:231]
v_mfma_f32_16x16x32_bf16 a[232:235],a[160:163],v[144:147],a[232:235]
v_mfma_f32_16x16x32_bf16 a[232:235],a[164:167],v[176:179],a[232:235]
v_mfma_f32_16x16x32_bf16 a[232:235],a[168:171],v[208:211],a[232:235]
buffer_load_dwordx4 v[96:99],v6,s[28:31],s78 offen sc1
v_mfma_f32_16x16x32_bf16 a[232:235],a[172:175],v[240:243],a[232:235]
v_mfma_f32_16x16x32_bf16 a[236:239],a[172:175],v[248:251],a[236:239]
ds_read_b64_tr_b16 a[172:173],v1 offset:30976
ds_read_b64_tr_b16 a[174:175],v1 offset:36096
v_mfma_f32_16x16x32_bf16 a[236:239],a[168:171],v[216:219],a[236:239]
ds_read_b64_tr_b16 a[168:169],v1 offset:20736
v_mfma_f32_16x16x32_bf16 a[236:239],a[164:167],v[184:187],a[236:239]
ds_read_b64_tr_b16 a[164:165],v1 offset:10496
v_mfma_f32_16x16x32_bf16 a[236:239],a[160:163],v[152:155],a[236:239]
ds_read_b64_tr_b16 a[160:161],v1 offset:256
v_mfma_f32_16x16x32_bf16 a[240:243],a[176:179],v[128:131],a[240:243]
ds_read_b64_tr_b16 a[162:163],v1 offset:5376
ds_read_b64_tr_b16 a[166:167],v1 offset:15616
v_mfma_f32_16x16x32_bf16 a[240:243],a[180:183],v[160:163],a[240:243]
ds_read_b64_tr_b16 a[170:171],v1 offset:25856
v_add_u32 v1,81920,v1
v_mfma_f32_16x16x32_bf16 a[240:243],a[184:187],v[192:195],a[240:243]
buffer_load_dwordx4 v[100:103],v6,s[28:31],s78 offen offset:1024 sc1
v_mfma_f32_16x16x32_bf16 a[240:243],a[188:191],v[224:227],a[240:243]
v_mfma_f32_16x16x32_bf16 a[244:247],a[188:191],v[232:235],a[244:247]
v_mfma_f32_16x16x32_bf16 a[244:247],a[184:187],v[200:203],a[244:247]
v_mfma_f32_16x16x32_bf16 a[244:247],a[180:183],v[168:171],a[244:247]
buffer_load_dwordx4 v9,s[72:75],s80 offen offset:2048 lds
v_mfma_f32_16x16x32_bf16 a[244:247],a[176:179],v[136:139],a[244:247]
v_mfma_f32_16x16x32_bf16 a[248:251],a[176:179],v[144:147],a[248:251]
v_mfma_f32_16x16x32_bf16 a[248:251],a[180:183],v[176:179],a[248:251]
v_mfma_f32_16x16x32_bf16 a[248:251],a[184:187],v[208:211],a[248:251]
buffer_load_dwordx4 v[104:107],v6,s[28:31],s78 offen offset:2048 sc1
v_mfma_f32_16x16x32_bf16 a[248:251],a[188:191],v[240:243],a[248:251]
v_mfma_f32_16x16x32_bf16 a[252:255],a[188:191],v[248:251],a[252:255]
ds_read_b64_tr_b16 a[188:189],v2 offset:31008
ds_read_b64_tr_b16 a[190:191],v2 offset:36128
v_mfma_f32_16x16x32_bf16 a[252:255],a[184:187],v[216:219],a[252:255]
ds_read_b64_tr_b16 a[184:185],v2 offset:20768
v_mfma_f32_16x16x32_bf16 a[252:255],a[180:183],v[184:187],a[252:255]
ds_read_b64_tr_b16 a[180:181],v2 offset:10528
v_mfma_f32_16x16x32_bf16 a[252:255],a[176:179],v[152:155],a[252:255]
ds_read_b64_tr_b16 a[176:177],v2 offset:288
v_mfma_f32_16x16x32_bf16 a[0:3],a[96:99],v[128:131],a[0:3]
ds_read_b64_tr_b16 a[178:179],v2 offset:5408
ds_read_b64_tr_b16 a[182:183],v2 offset:15648
v_mfma_f32_16x16x32_bf16 a[0:3],a[100:103],v[160:163],a[0:3]
ds_read_b64_tr_b16 a[186:187],v2 offset:25888
v_add_u32 v2,81920,v2
v_mfma_f32_16x16x32_bf16 a[0:3],a[104:107],v[192:195],a[0:3]
buffer_load_dwordx4 v[108:111],v6,s[28:31],s78 offen offset:3072 sc1
v_mfma_f32_16x16x32_bf16 a[0:3],a[108:111],v[224:227],a[0:3]
v_mfma_f32_16x16x32_bf16 a[4:7],a[108:111],v[232:235],a[4:7]
v_mfma_f32_16x16x32_bf16 a[4:7],a[104:107],v[200:203],a[4:7]
v_mfma_f32_16x16x32_bf16 a[4:7],a[100:103],v[168:171],a[4:7]
buffer_load_dwordx4 v10,s[72:75],s80 offen offset:3072 lds
v_mfma_f32_16x16x32_bf16 a[4:7],a[96:99],v[136:139],a[4:7]
v_mfma_f32_16x16x32_bf16 a[8:11],a[96:99],v[144:147],a[8:11]
v_mfma_f32_16x16x32_bf16 a[8:11],a[100:103],v[176:179],a[8:11]
v_mfma_f32_16x16x32_bf16 a[8:11],a[104:107],v[208:211],a[8:11]
buffer_load_dwordx4 v[112:115],v6,s[28:31],s79 offen sc1
v_mfma_f32_16x16x32_bf16 a[8:11],a[108:111],v[240:243],a[8:11]
v_mfma_f32_16x16x32_bf16 a[12:15],a[108:111],v[248:251],a[12:15]
ds_read_b64_tr_b16 a[108:109],v1 offset:30720
ds_read_b64_tr_b16 a[110:111],v1 offset:35840
v_mfma_f32_16x16x32_bf16 a[12:15],a[104:107],v[216:219],a[12:15]
ds_read_b64_tr_b16 a[104:105],v1 offset:20480
v_mfma_f32_16x16x32_bf16 a[12:15],a[100:103],v[184:187],a[12:15]
s_add_u32 m0,s82,4096
ds_read_b64_tr_b16 a[100:101],v1 offset:10240
v_mfma_f32_16x16x32_bf16 a[12:15],a[96:99],v[152:155],a[12:15]
ds_read_b64_tr_b16 a[96:97],v1
v_mfma_f32_16x16x32_bf16 a[16:19],a[112:115],v[128:131],a[16:19]
ds_read_b64_tr_b16 a[98:99],v1 offset:5120
ds_read_b64_tr_b16 a[102:103],v1 offset:15360
v_mfma_f32_16x16x32_bf16 a[16:19],a[116:119],v[160:163],a[16:19]
buffer_load_dwordx4 v11,s[72:75],s80 offen lds
v_mfma_f32_16x16x32_bf16 a[16:19],a[120:123],v[192:195],a[16:19]
ds_read_b64_tr_b16 a[106:107],v1 offset:25600
v_mfma_f32_16x16x32_bf16 a[16:19],a[124:127],v[224:227],a[16:19]
v_mfma_f32_16x16x32_bf16 a[20:23],a[124:127],v[232:235],a[20:23]
v_mfma_f32_16x16x32_bf16 a[20:23],a[120:123],v[200:203],a[20:23]
buffer_load_dwordx4 v[116:119],v6,s[28:31],s79 offen offset:1024 sc1
v_mfma_f32_16x16x32_bf16 a[20:23],a[116:119],v[168:171],a[20:23]
v_mfma_f32_16x16x32_bf16 a[20:23],a[112:115],v[136:139],a[20:23]
v_mfma_f32_16x16x32_bf16 a[24:27],a[112:115],v[144:147],a[24:27]
v_mfma_f32_16x16x32_bf16 a[24:27],a[116:119],v[176:179],a[24:27]
v_mfma_f32_16x16x32_bf16 a[24:27],a[120:123],v[208:211],a[24:27]
buffer_load_dwordx4 v[120:123],v6,s[28:31],s79 offen offset:2048 sc1
v_mfma_f32_16x16x32_bf16 a[24:27],a[124:127],v[240:243],a[24:27]
v_mfma_f32_16x16x32_bf16 a[28:31],a[124:127],v[248:251],a[28:31]
ds_read_b64_tr_b16 a[124:125],v2 offset:30752
ds_read_b64_tr_b16 a[126:127],v2 offset:35872
v_mfma_f32_16x16x32_bf16 a[28:31],a[120:123],v[216:219],a[28:31]
s_add_u32 m0,s82,5120
ds_read_b64_tr_b16 a[120:121],v2 offset:20512
v_mfma_f32_16x16x32_bf16 a[28:31],a[116:119],v[184:187],a[28:31]
ds_read_b64_tr_b16 a[116:117],v2 offset:10272
v_mfma_f32_16x16x32_bf16 a[28:31],a[112:115],v[152:155],a[28:31]
s_cmp_ge_u32 2,s71
s_cselect_b32 s76,6,4
s_lshl_b32 s76,s76,16
v_mfma_f32_16x16x32_bf16 a[32:35],a[128:131],v[128:131],a[32:35]
s_add_u32 s77,s76,4096
buffer_load_dwordx4 v7,s[72:75],s83 offen lds
v_mfma_f32_16x16x32_bf16 a[32:35],a[132:135],v[160:163],a[32:35]
s_add_u32 s78,s76,8192
s_max_i32 s77,s77,s19
ds_read_b64_tr_b16 a[112:113],v2 offset:32
v_mfma_f32_16x16x32_bf16 a[32:35],a[136:139],v[192:195],a[32:35]
s_max_i32 s78,s78,s100
ds_read_b64_tr_b16 a[114:115],v2 offset:5152
ds_read_b64_tr_b16 a[118:119],v2 offset:15392
v_mfma_f32_16x16x32_bf16 a[32:35],a[140:143],v[224:227],a[32:35]
ds_read_b64_tr_b16 a[122:123],v2 offset:25632
v_mfma_f32_16x16x32_bf16 a[36:39],a[140:143],v[232:235],a[36:39]
buffer_load_dwordx4 v[124:127],v6,s[28:31],s79 offen offset:3072 sc1
v_mfma_f32_16x16x32_bf16 a[36:39],a[136:139],v[200:203],a[36:39]
s_add_u32 s79,s76,12288
s_max_i32 s76,s76,s18
s_max_i32 s79,s79,s101
v_mfma_f32_16x16x32_bf16 a[36:39],a[132:135],v[168:171],a[36:39]
v_mfma_f32_16x16x32_bf16 a[36:39],a[128:131],v[136:139],a[36:39]
v_mfma_f32_16x16x32_bf16 a[40:43],a[128:131],v[144:147],a[40:43]
v_mfma_f32_16x16x32_bf16 a[40:43],a[132:135],v[176:179],a[40:43]
v_mfma_f32_16x16x32_bf16 a[40:43],a[136:139],v[208:211],a[40:43]
v_mfma_f32_16x16x32_bf16 a[40:43],a[140:143],v[240:243],a[40:43]
v_mfma_f32_16x16x32_bf16 a[44:47],a[140:143],v[248:251],a[44:47]
ds_read_b64_tr_b16 a[140:141],v1 offset:30784
ds_read_b64_tr_b16 a[142:143],v1 offset:35904
v_mfma_f32_16x16x32_bf16 a[44:47],a[136:139],v[216:219],a[44:47]
ds_read_b64_tr_b16 a[136:137],v1 offset:20544
v_mfma_f32_16x16x32_bf16 a[44:47],a[132:135],v[184:187],a[44:47]
ds_read_b64_tr_b16 a[132:133],v1 offset:10304
v_mfma_f32_16x16x32_bf16 a[44:47],a[128:131],v[152:155],a[44:47]
s_waitcnt lgkmcnt(4)
buffer_load_dwordx4 v8,s[72:75],s83 offen offset:1024 lds
v_mfma_f32_16x16x32_bf16 a[48:51],a[144:147],v[128:131],a[48:51]
ds_read_b64_tr_b16 a[128:129],v1 offset:64
ds_read_b64_tr_b16 a[130:131],v1 offset:5184
v_mfma_f32_16x16x32_bf16 a[48:51],a[148:151],v[160:163],a[48:51]
ds_read_b64_tr_b16 a[134:135],v1 offset:15424
v_mfma_f32_16x16x32_bf16 a[48:51],a[152:155],v[192:195],a[48:51]
ds_read_b64_tr_b16 a[138:139],v1 offset:25664
v_mfma_f32_16x16x32_bf16 a[48:51],a[156:159],v[224:227],a[48:51]
s_lshl_b32 s94,s70,4
v_bfe_u32 v24,v6,4,2
v_min_u32 v24,2,v24
v_mfma_f32_16x16x32_bf16 a[52:55],a[156:159],v[232:235],a[52:55]
s_add_u32 s94,s94,s40
v_add_u32 v24,s71,v24
v_add_u32 v24,1,v24
v_mfma_f32_16x16x32_bf16 a[52:55],a[152:155],v[200:203],a[52:55]
s_lshl_b32 s94,s94,4
v_and_b32 v24,3,v24
v_lshlrev_b32 v24,6,v24
v_mfma_f32_16x16x32_bf16 a[52:55],a[148:151],v[168:171],a[52:55]
v_mfma_f32_16x16x32_bf16 a[52:55],a[144:147],v[136:139],a[52:55]
buffer_load_dwordx4 v[132:135],v6,s[28:31],s76 offen sc1
v_mfma_f32_16x16x32_bf16 a[56:59],a[144:147],v[144:147],a[56:59]
v_mfma_f32_16x16x32_bf16 a[56:59],a[148:151],v[176:179],a[56:59]
v_mfma_f32_16x16x32_bf16 a[56:59],a[152:155],v[208:211],a[56:59]
v_mfma_f32_16x16x32_bf16 a[56:59],a[156:159],v[240:243],a[56:59]
v_mfma_f32_16x16x32_bf16 a[60:63],a[156:159],v[248:251],a[60:63]
ds_read_b64_tr_b16 a[156:157],v2 offset:30816
ds_read_b64_tr_b16 a[158:159],v2 offset:35936
v_mfma_f32_16x16x32_bf16 a[60:63],a[152:155],v[216:219],a[60:63]
buffer_load_dwordx4 v[140:143],v6,s[28:31],s76 offen offset:1024 sc1
v_mfma_f32_16x16x32_bf16 a[60:63],a[148:151],v[184:187],a[60:63]
ds_read_b64_tr_b16 a[148:149],v2 offset:10336
ds_read_b64_tr_b16 a[150:151],v2 offset:15456
v_mfma_f32_16x16x32_bf16 a[60:63],a[144:147],v[152:155],a[60:63]
ds_read_b64_tr_b16 a[144:145],v2 offset:96
v_mfma_f32_16x16x32_bf16 a[64:67],a[160:163],v[128:131],a[64:67]
ds_read_b64_tr_b16 a[146:147],v2 offset:5216
v_mfma_f32_16x16x32_bf16 a[64:67],a[164:167],v[160:163],a[64:67]
buffer_load_dwordx4 v9,s[72:75],s83 offen offset:2048 lds
v_mfma_f32_16x16x32_bf16 a[64:67],a[168:171],v[192:195],a[64:67]
ds_read_b64_tr_b16 a[152:153],v2 offset:20576
ds_read_b64_tr_b16 a[154:155],v2 offset:25696
v_mfma_f32_16x16x32_bf16 a[64:67],a[172:175],v[224:227],a[64:67]
v_mfma_f32_16x16x32_bf16 a[68:71],a[172:175],v[232:235],a[68:71]
v_mfma_f32_16x16x32_bf16 a[68:71],a[168:171],v[200:203],a[68:71]
buffer_load_dwordx4 v[148:151],v6,s[28:31],s76 offen offset:2048 sc1
v_mfma_f32_16x16x32_bf16 a[68:71],a[164:167],v[168:171],a[68:71]
v_mfma_f32_16x16x32_bf16 a[68:71],a[160:163],v[136:139],a[68:71]
v_mfma_f32_16x16x32_bf16 a[72:75],a[160:163],v[144:147],a[72:75]
v_mfma_f32_16x16x32_bf16 a[72:75],a[164:167],v[176:179],a[72:75]
buffer_load_dwordx4 v[156:159],v6,s[28:31],s76 offen offset:3072 sc1
v_mfma_f32_16x16x32_bf16 a[72:75],a[168:171],v[208:211],a[72:75]
v_mfma_f32_16x16x32_bf16 a[72:75],a[172:175],v[240:243],a[72:75]
v_mfma_f32_16x16x32_bf16 a[76:79],a[172:175],v[248:251],a[76:79]
ds_read_b64_tr_b16 a[172:173],v1 offset:30848
ds_read_b64_tr_b16 a[174:175],v1 offset:35968
v_mfma_f32_16x16x32_bf16 a[76:79],a[168:171],v[216:219],a[76:79]
buffer_load_dwordx4 v10,s[72:75],s83 offen offset:3072 lds
v_mfma_f32_16x16x32_bf16 a[76:79],a[164:167],v[184:187],a[76:79]
ds_read_b64_tr_b16 a[164:165],v1 offset:10368
ds_read_b64_tr_b16 a[166:167],v1 offset:15488
v_mfma_f32_16x16x32_bf16 a[76:79],a[160:163],v[152:155],a[76:79]
ds_read_b64_tr_b16 a[160:161],v1 offset:128
v_mfma_f32_16x16x32_bf16 a[80:83],a[176:179],v[128:131],a[80:83]
ds_read_b64_tr_b16 a[162:163],v1 offset:5248
v_mfma_f32_16x16x32_bf16 a[80:83],a[180:183],v[160:163],a[80:83]
buffer_load_dwordx4 v[164:167],v6,s[28:31],s77 offen sc1
v_mfma_f32_16x16x32_bf16 a[80:83],a[184:187],v[192:195],a[80:83]
)A"
R"A(ds_read_b64_tr_b16 a[168:169],v1 offset:20608
ds_read_b64_tr_b16 a[170:171],v1 offset:25728
v_mfma_f32_16x16x32_bf16 a[80:83],a[188:191],v[224:227],a[80:83]
v_mfma_f32_16x16x32_bf16 a[84:87],a[188:191],v[232:235],a[84:87]
v_mfma_f32_16x16x32_bf16 a[84:87],a[184:187],v[200:203],a[84:87]
buffer_load_dwordx4 v[172:175],v6,s[28:31],s77 offen offset:1024 sc1
v_mfma_f32_16x16x32_bf16 a[84:87],a[180:183],v[168:171],a[84:87]
v_mfma_f32_16x16x32_bf16 a[84:87],a[176:179],v[136:139],a[84:87]
v_mfma_f32_16x16x32_bf16 a[88:91],a[176:179],v[144:147],a[88:91]
v_mfma_f32_16x16x32_bf16 a[88:91],a[180:183],v[176:179],a[88:91]
s_add_u32 m0,s82,9216
v_mfma_f32_16x16x32_bf16 a[88:91],a[184:187],v[208:211],a[88:91]
v_mfma_f32_16x16x32_bf16 a[88:91],a[188:191],v[240:243],a[88:91]
buffer_load_dwordx2 v[26:27],v24,s[64:67],s94 offen offset:1024 sc1
v_mfma_f32_16x16x32_bf16 a[92:95],a[188:191],v[248:251],a[92:95]
ds_read_b64_tr_b16 a[188:189],v2 offset:30880
ds_read_b64_tr_b16 a[190:191],v2 offset:36000
v_mfma_f32_16x16x32_bf16 a[92:95],a[184:187],v[216:219],a[92:95]
ds_read_b64_tr_b16 a[184:185],v2 offset:20640
v_mfma_f32_16x16x32_bf16 a[92:95],a[180:183],v[184:187],a[92:95]
ds_read_b64_tr_b16 a[180:181],v2 offset:10400
v_mfma_f32_16x16x32_bf16 a[92:95],a[176:179],v[152:155],a[92:95]
s_waitcnt vmcnt(13)
buffer_load_dwordx4 v[180:183],v6,s[28:31],s77 offen offset:2048 sc1
v_mfma_f32_16x16x32_bf16 a[192:195],a[96:99],v[64:67],a[192:195]
s_lshl_b32 s43,s41,2
ds_read_b64_tr_b16 a[176:177],v2 offset:160
ds_read_b64_tr_b16 a[178:179],v2 offset:5280
v_mfma_f32_16x16x32_bf16 a[192:195],a[100:103],v[68:71],a[192:195]
s_add_u32 s84,s70,1
s_add_u32 s85,s70,2
ds_read_b64_tr_b16 a[182:183],v2 offset:15520
v_mfma_f32_16x16x32_bf16 a[192:195],a[104:107],v[72:75],a[192:195]
s_add_u32 s86,s70,3
s_add_u32 s43,s43,s71
ds_read_b64_tr_b16 a[186:187],v2 offset:25760
v_mfma_f32_16x16x32_bf16 a[192:195],a[108:111],v[76:79],a[192:195]
s_and_b32 s84,s84,3
buffer_load_dwordx4 v11,s[72:75],s83 offen lds
v_mfma_f32_16x16x32_bf16 a[196:199],a[108:111],v[92:95],a[196:199]
s_and_b32 s85,s85,3
s_and_b32 s86,s86,3
s_mul_i32 s43,s43,245760
v_mfma_f32_16x16x32_bf16 a[196:199],a[104:107],v[88:91],a[196:199]
s_mul_i32 s84,s84,12
s_mul_i32 s85,s85,12
s_mul_i32 s86,s86,12
v_mfma_f32_16x16x32_bf16 a[196:199],a[100:103],v[84:87],a[196:199]
s_add_u32 s43,s43,134217728
s_mov_b64 s[32:33],s[12:13]
s_add_u32 s84,s84,0
v_mfma_f32_16x16x32_bf16 a[196:199],a[96:99],v[80:83],a[196:199]
s_add_u32 s85,s85,4
buffer_load_dwordx4 v[188:191],v6,s[28:31],s77 offen offset:3072 sc1
v_mfma_f32_16x16x32_bf16 a[200:203],a[96:99],v[96:99],a[200:203]
s_add_u32 s86,s86,8
s_add_u32 s84,s84,s40
s_add_u32 s85,s85,s40
v_mfma_f32_16x16x32_bf16 a[200:203],a[100:103],v[100:103],a[200:203]
s_add_u32 s86,s86,s40
s_add_u32 s32,s32,s43
s_addc_u32 s33,s33,0
v_mfma_f32_16x16x32_bf16 a[200:203],a[104:107],v[104:107],a[200:203]
s_mul_i32 s84,s84,5120
s_mul_i32 s85,s85,5120
s_mul_i32 s86,s86,5120
v_mfma_f32_16x16x32_bf16 a[200:203],a[108:111],v[108:111],a[200:203]
s_and_b32 s33,s33,0xffff
buffer_load_dwordx4 v[196:199],v6,s[28:31],s78 offen sc1
s_waitcnt vmcnt(14)
v_mfma_f32_16x16x32_bf16 a[204:207],a[108:111],v[124:127],a[204:207]
s_mov_b32 s34,245760
ds_read_b64_tr_b16 a[108:109],v1 offset:30912
ds_read_b64_tr_b16 a[110:111],v1 offset:36032
v_mfma_f32_16x16x32_bf16 a[204:207],a[104:107],v[120:123],a[204:207]
s_mov_b32 s35,0x20000
s_max_i32 s84,s84,s18
ds_read_b64_tr_b16 a[104:105],v1 offset:20672
v_mfma_f32_16x16x32_bf16 a[204:207],a[100:103],v[116:119],a[204:207]
s_max_i32 s85,s85,s19
s_max_i32 s86,s86,s100
ds_read_b64_tr_b16 a[100:101],v1 offset:10432
v_mfma_f32_16x16x32_bf16 a[204:207],a[96:99],v[112:115],a[204:207]
s_barrier
buffer_load_dwordx4 v[204:207],v6,s[28:31],s78 offen offset:1024 sc1
v_mfma_f32_16x16x32_bf16 a[208:211],a[112:115],v[64:67],a[208:211]
s_mul_i32 s44,s41,307200
ds_read_b64_tr_b16 a[96:97],v1 offset:192
ds_read_b64_tr_b16 a[98:99],v1 offset:5312
v_mfma_f32_16x16x32_bf16 a[208:211],a[116:119],v[68:71],a[208:211]
s_mov_b64 s[20:21],s[16:17]
ds_read_b64_tr_b16 a[102:103],v1 offset:15552
v_bfe_u32 v18,v6,6,2
v_mfma_f32_16x16x32_bf16 a[208:211],a[120:123],v[72:75],a[208:211]
s_lshl_b32 s43,s40,2
ds_read_b64_tr_b16 a[106:107],v1 offset:25792
v_lshrrev_b32 v19,8,v6
v_mfma_f32_16x16x32_bf16 a[208:211],a[124:127],v[76:79],a[208:211]
s_add_u32 s43,s43,s70
buffer_load_dwordx4 v[212:215],v6,s[28:31],s78 offen offset:2048 sc1
v_mfma_f32_16x16x32_bf16 a[212:215],a[124:127],v[92:95],a[212:215]
s_mul_i32 s43,s43,20480
v_xor_b32 v18,v18,v19
v_lshlrev_b32 v18,2,v18
v_mfma_f32_16x16x32_bf16 a[212:215],a[120:123],v[88:91],a[212:215]
s_mov_b32 s22,307200
v_bfe_u32 v19,v6,4,2
v_or_b32 v18,v18,v19
v_mfma_f32_16x16x32_bf16 a[212:215],a[116:119],v[84:87],a[212:215]
s_mov_b32 s23,0x20000
v_and_b32 v19,16,v6
v_mov_b32 v34,0x3f80
v_mfma_f32_16x16x32_bf16 a[212:215],a[112:115],v[80:83],a[212:215]
s_cmp_ge_u32 0,s71
s_cselect_b32 s76,3,1
s_lshl_b32 s76,s76,16
v_mfma_f32_16x16x32_bf16 a[216:219],a[112:115],v[96:99],a[216:219]
s_add_u32 s77,s76,4096
buffer_load_dwordx4 v[220:223],v6,s[28:31],s78 offen offset:3072 sc1
v_mfma_f32_16x16x32_bf16 a[216:219],a[116:119],v[100:103],a[216:219]
s_add_u32 s78,s76,8192
s_max_i32 s77,s77,s19
s_max_i32 s78,s78,s100
v_mfma_f32_16x16x32_bf16 a[216:219],a[120:123],v[104:107],a[216:219]
v_lshlrev_b32 v19,v19,v34
v_lshrrev_b32 v18,1,v18
v_mfma_f32_16x16x32_bf16 a[216:219],a[124:127],v[108:111],a[216:219]
v_cmp_eq_u32 vcc,0,v18
s_nop 1
v_cndmask_b32 v34,0,v19,vcc
v_mfma_f32_16x16x32_bf16 a[220:223],a[124:127],v[124:127],a[220:223]
ds_read_b64_tr_b16 a[124:125],v2 offset:30944
ds_read_b64_tr_b16 a[126:127],v2 offset:36064
v_mfma_f32_16x16x32_bf16 a[220:223],a[120:123],v[120:123],a[220:223]
ds_read_b64_tr_b16 a[120:121],v2 offset:20704
v_add_u32 v25,4096,v6
v_mfma_f32_16x16x32_bf16 a[220:223],a[116:119],v[116:119],a[220:223]
buffer_load_dwordx4 v[228:231],v6,s[28:31],s79 offen sc1
v_mfma_f32_16x16x32_bf16 a[220:223],a[112:115],v[112:115],a[220:223]
s_waitcnt lgkmcnt(3)
ds_read_b64_tr_b16 a[112:113],v2 offset:224
ds_read_b64_tr_b16 a[114:115],v2 offset:5344
v_mfma_f32_16x16x32_bf16 a[224:227],a[128:131],v[64:67],a[224:227]
ds_read_b64_tr_b16 a[116:117],v2 offset:10464
v_mov_b32 v36,0
v_mfma_f32_16x16x32_bf16 a[224:227],a[132:135],v[68:71],a[224:227]
ds_read_b64_tr_b16 a[118:119],v2 offset:15584
v_mov_b32 v37,0
v_mfma_f32_16x16x32_bf16 a[224:227],a[136:139],v[72:75],a[224:227]
ds_read_b64_tr_b16 a[122:123],v2 offset:25824
v_mov_b32 v32,0
v_mfma_f32_16x16x32_bf16 a[224:227],a[140:143],v[76:79],a[224:227]
v_cmp_eq_u32 vcc,1,v18
s_nop 1
v_cndmask_b32 v35,0,v19,vcc
v_mfma_f32_16x16x32_bf16 a[228:231],a[140:143],v[92:95],a[228:231]
buffer_load_dwordx4 v[236:239],v6,s[28:31],s79 offen offset:1024 sc1
v_mfma_f32_16x16x32_bf16 a[228:231],a[136:139],v[88:91],a[228:231]
s_add_u32 s20,s20,s44
s_addc_u32 s21,s21,0
v_mov_b32 v33,0
v_mfma_f32_16x16x32_bf16 a[228:231],a[132:135],v[84:87],a[228:231]
s_mul_i32 s44,s71,320
v_bfe_u32 v40,v6,4,4
v_mul_u32_u24 v40,1280,v40
v_mfma_f32_16x16x32_bf16 a[228:231],a[128:131],v[80:83],a[228:231]
s_waitcnt vmcnt(9)
v_cmp_ne_u32 s[90:91],s68,v26
v_cmp_ne_u32 s[92:93],s69,v27
s_nop 0
s_or_b64 s[90:91],s[90:91],s[92:93]
s_cbranch_scc1 L_fr9_%=
L_fo9_%=:
v_mfma_f32_16x16x32_bf16 a[232:235],a[128:131],v[96:99],a[232:235]
s_and_b32 s21,s21,0xffff
v_lshrrev_b32 v41,8,v6
v_lshl_add_u32 v40,v41,3,v40
v_mfma_f32_16x16x32_bf16 a[232:235],a[132:135],v[100:103],a[232:235]
s_add_u32 s43,s43,s44
v_mfma_f32_16x16x32_bf16 a[232:235],a[136:139],v[104:107],a[232:235]
buffer_load_dwordx4 v[244:247],v6,s[28:31],s79 offen offset:2048 sc1
v_mfma_f32_16x16x32_bf16 a[232:235],a[140:143],v[108:111],a[232:235]
v_mfma_f32_16x16x32_bf16 a[236:239],a[140:143],v[124:127],a[236:239]
ds_read_b64_tr_b16 a[140:141],v1 offset:30976
ds_read_b64_tr_b16 a[142:143],v1 offset:36096
v_mfma_f32_16x16x32_bf16 a[236:239],a[136:139],v[120:123],a[236:239]
ds_read_b64_tr_b16 a[136:137],v1 offset:20736
v_mfma_f32_16x16x32_bf16 a[236:239],a[132:135],v[116:119],a[236:239]
ds_read_b64_tr_b16 a[132:133],v1 offset:10496
v_mfma_f32_16x16x32_bf16 a[236:239],a[128:131],v[112:115],a[236:239]
ds_read_b64_tr_b16 a[128:129],v1 offset:256
v_mfma_f32_16x16x32_bf16 a[240:243],a[144:147],v[64:67],a[240:243]
buffer_load_dwordx4 v[252:255],v6,s[28:31],s79 offen offset:3072 sc1
v_mfma_f32_16x16x32_bf16 a[240:243],a[148:151],v[68:71],a[240:243]
s_add_u32 s79,s76,12288
s_max_i32 s76,s76,s18
ds_read_b64_tr_b16 a[130:131],v1 offset:5376
v_mfma_f32_16x16x32_bf16 a[240:243],a[152:155],v[72:75],a[240:243]
s_max_i32 s79,s79,s101
ds_read_b64_tr_b16 a[134:135],v1 offset:15616
ds_read_b64_tr_b16 a[138:139],v1 offset:25856
v_mfma_f32_16x16x32_bf16 a[240:243],a[156:159],v[76:79],a[240:243]
v_subrev_u32 v1,40960,v1
v_mfma_f32_16x16x32_bf16 a[244:247],a[156:159],v[92:95],a[244:247]
buffer_load_dwordx4 v[128:131],v6,s[28:31],s76 offen sc1
v_mfma_f32_16x16x32_bf16 a[244:247],a[152:155],v[88:91],a[244:247]
v_mfma_f32_16x16x32_bf16 a[244:247],a[148:151],v[84:87],a[244:247]
v_mfma_f32_16x16x32_bf16 a[244:247],a[144:147],v[80:83],a[244:247]
v_mfma_f32_16x16x32_bf16 a[248:251],a[144:147],v[96:99],a[248:251]
buffer_load_dwordx4 v[160:163],v6,s[28:31],s76 offen offset:1024 sc1
v_mfma_f32_16x16x32_bf16 a[248:251],a[148:151],v[100:103],a[248:251]
v_mfma_f32_16x16x32_bf16 a[248:251],a[152:155],v[104:107],a[248:251]
v_mfma_f32_16x16x32_bf16 a[248:251],a[156:159],v[108:111],a[248:251]
v_mfma_f32_16x16x32_bf16 a[252:255],a[156:159],v[124:127],a[252:255]
buffer_load_dwordx4 v[192:195],v6,s[28:31],s76 offen offset:2048 sc1
v_mfma_f32_16x16x32_bf16 a[252:255],a[152:155],v[120:123],a[252:255]
ds_read_b64_tr_b16 a[152:153],v2 offset:20768
ds_read_b64_tr_b16 a[154:155],v2 offset:25888
v_mfma_f32_16x16x32_bf16 a[252:255],a[148:151],v[116:119],a[252:255]
ds_read_b64_tr_b16 a[148:149],v2 offset:10528
v_mfma_f32_16x16x32_bf16 a[252:255],a[144:147],v[112:115],a[252:255]
ds_read_b64_tr_b16 a[144:145],v2 offset:288
v_mfma_f32_16x16x32_bf16 a[0:3],a[160:163],v[64:67],a[0:3]
ds_read_b64_tr_b16 a[146:147],v2 offset:5408
v_mfma_f32_16x16x32_bf16 a[0:3],a[164:167],v[68:71],a[0:3]
buffer_load_dwordx4 v[224:227],v6,s[28:31],s76 offen offset:3072 sc1
v_mfma_f32_16x16x32_bf16 a[0:3],a[168:171],v[72:75],a[0:3]
ds_read_b64_tr_b16 a[150:151],v2 offset:15648
ds_read_b64_tr_b16 a[156:157],v2 offset:31008
v_mfma_f32_16x16x32_bf16 a[0:3],a[172:175],v[76:79],a[0:3]
ds_read_b64_tr_b16 a[158:159],v2 offset:36128
v_subrev_u32 v2,40960,v2
v_mfma_f32_16x16x32_bf16 a[4:7],a[172:175],v[92:95],a[4:7]
v_mfma_f32_16x16x32_bf16 a[4:7],a[168:171],v[88:91],a[4:7]
v_mfma_f32_16x16x32_bf16 a[4:7],a[164:167],v[84:87],a[4:7]
v_mfma_f32_16x16x32_bf16 a[4:7],a[160:163],v[80:83],a[4:7]
buffer_load_dwordx4 v[136:139],v6,s[28:31],s77 offen sc1
v_mfma_f32_16x16x32_bf16 a[8:11],a[160:163],v[96:99],a[8:11]
v_mfma_f32_16x16x32_bf16 a[8:11],a[164:167],v[100:103],a[8:11]
v_mfma_f32_16x16x32_bf16 a[8:11],a[168:171],v[104:107],a[8:11]
v_mfma_f32_16x16x32_bf16 a[8:11],a[172:175],v[108:111],a[8:11]
v_mfma_f32_16x16x32_bf16 a[12:15],a[172:175],v[124:127],a[12:15]
ds_read_b64_tr_b16 a[172:173],v1 offset:30720
ds_read_b64_tr_b16 a[174:175],v1 offset:35840
v_mfma_f32_16x16x32_bf16 a[12:15],a[168:171],v[120:123],a[12:15]
buffer_load_dwordx4 v[168:171],v6,s[28:31],s77 offen offset:1024 sc1
v_mfma_f32_16x16x32_bf16 a[12:15],a[164:167],v[116:119],a[12:15]
ds_read_b64_tr_b16 a[164:165],v1 offset:10240
ds_read_b64_tr_b16 a[166:167],v1 offset:15360
v_mfma_f32_16x16x32_bf16 a[12:15],a[160:163],v[112:115],a[12:15]
ds_read_b64_tr_b16 a[160:161],v1
v_mfma_f32_16x16x32_bf16 a[16:19],a[176:179],v[64:67],a[16:19]
ds_read_b64_tr_b16 a[162:163],v1 offset:5120
v_mfma_f32_16x16x32_bf16 a[16:19],a[180:183],v[68:71],a[16:19]
ds_read_b64_tr_b16 a[168:169],v1 offset:20480
v_mfma_f32_16x16x32_bf16 a[16:19],a[184:187],v[72:75],a[16:19]
ds_read_b64_tr_b16 a[170:171],v1 offset:25600
v_mfma_f32_16x16x32_bf16 a[16:19],a[188:191],v[76:79],a[16:19]
buffer_load_dwordx4 v[200:203],v6,s[28:31],s77 offen offset:2048 sc1
v_mfma_f32_16x16x32_bf16 a[20:23],a[188:191],v[92:95],a[20:23]
)A"
R"A(v_mfma_f32_16x16x32_bf16 a[20:23],a[184:187],v[88:91],a[20:23]
v_mfma_f32_16x16x32_bf16 a[20:23],a[180:183],v[84:87],a[20:23]
v_mfma_f32_16x16x32_bf16 a[20:23],a[176:179],v[80:83],a[20:23]
v_mfma_f32_16x16x32_bf16 a[24:27],a[176:179],v[96:99],a[24:27]
v_mfma_f32_16x16x32_bf16 a[24:27],a[180:183],v[100:103],a[24:27]
buffer_load_dwordx4 v[232:235],v6,s[28:31],s77 offen offset:3072 sc1
v_mfma_f32_16x16x32_bf16 a[24:27],a[184:187],v[104:107],a[24:27]
v_mfma_f32_16x16x32_bf16 a[24:27],a[188:191],v[108:111],a[24:27]
v_mfma_f32_16x16x32_bf16 a[28:31],a[188:191],v[124:127],a[28:31]
ds_read_b64_tr_b16 a[188:189],v2 offset:30752
ds_read_b64_tr_b16 a[190:191],v2 offset:35872
v_mfma_f32_16x16x32_bf16 a[28:31],a[184:187],v[120:123],a[28:31]
ds_read_b64_tr_b16 a[184:185],v2 offset:20512
v_mfma_f32_16x16x32_bf16 a[28:31],a[180:183],v[116:119],a[28:31]
ds_read_b64_tr_b16 a[180:181],v2 offset:10272
v_mfma_f32_16x16x32_bf16 a[28:31],a[176:179],v[112:115],a[28:31]
buffer_load_dwordx4 v[144:147],v6,s[28:31],s78 offen sc1
v_mfma_f32_16x16x32_bf16 a[32:35],a[96:99],v[64:67],a[32:35]
ds_read_b64_tr_b16 a[176:177],v2 offset:32
ds_read_b64_tr_b16 a[178:179],v2 offset:5152
v_mfma_f32_16x16x32_bf16 a[32:35],a[100:103],v[68:71],a[32:35]
ds_read_b64_tr_b16 a[182:183],v2 offset:15392
v_mfma_f32_16x16x32_bf16 a[32:35],a[104:107],v[72:75],a[32:35]
ds_read_b64_tr_b16 a[186:187],v2 offset:25632
v_mfma_f32_16x16x32_bf16 a[32:35],a[108:111],v[76:79],a[32:35]
v_mfma_f32_16x16x32_bf16 a[36:39],a[108:111],v[92:95],a[36:39]
v_mfma_f32_16x16x32_bf16 a[36:39],a[104:107],v[88:91],a[36:39]
buffer_load_dwordx4 v[176:179],v6,s[28:31],s78 offen offset:1024 sc1
v_mfma_f32_16x16x32_bf16 a[36:39],a[100:103],v[84:87],a[36:39]
v_mfma_f32_16x16x32_bf16 a[36:39],a[96:99],v[80:83],a[36:39]
v_mfma_f32_16x16x32_bf16 a[40:43],a[96:99],v[96:99],a[40:43]
v_mfma_f32_16x16x32_bf16 a[40:43],a[100:103],v[100:103],a[40:43]
v_mfma_f32_16x16x32_bf16 a[40:43],a[104:107],v[104:107],a[40:43]
v_mfma_f32_16x16x32_bf16 a[40:43],a[108:111],v[108:111],a[40:43]
buffer_load_dwordx4 v[208:211],v6,s[28:31],s78 offen offset:2048 sc1
v_mfma_f32_16x16x32_bf16 a[44:47],a[108:111],v[124:127],a[44:47]
ds_read_b64_tr_b16 a[108:109],v1 offset:30784
ds_read_b64_tr_b16 a[110:111],v1 offset:35904
v_mfma_f32_16x16x32_bf16 a[44:47],a[104:107],v[120:123],a[44:47]
ds_read_b64_tr_b16 a[104:105],v1 offset:20544
v_mfma_f32_16x16x32_bf16 a[44:47],a[100:103],v[116:119],a[44:47]
ds_read_b64_tr_b16 a[100:101],v1 offset:10304
v_mfma_f32_16x16x32_bf16 a[44:47],a[96:99],v[112:115],a[44:47]
s_waitcnt lgkmcnt(4)
ds_read_b64_tr_b16 a[96:97],v1 offset:64
v_mfma_f32_16x16x32_bf16 a[48:51],a[112:115],v[64:67],a[48:51]
ds_read_b64_tr_b16 a[98:99],v1 offset:5184
ds_read_b64_tr_b16 a[102:103],v1 offset:15424
v_mfma_f32_16x16x32_bf16 a[48:51],a[116:119],v[68:71],a[48:51]
buffer_load_dwordx4 v[240:243],v6,s[28:31],s78 offen offset:3072 sc1
v_mfma_f32_16x16x32_bf16 a[48:51],a[120:123],v[72:75],a[48:51]
ds_read_b64_tr_b16 a[106:107],v1 offset:25664
v_mfma_f32_16x16x32_bf16 a[48:51],a[124:127],v[76:79],a[48:51]
v_mfma_f32_16x16x32_bf16 a[52:55],a[124:127],v[92:95],a[52:55]
v_mfma_f32_16x16x32_bf16 a[52:55],a[120:123],v[88:91],a[52:55]
v_mfma_f32_16x16x32_bf16 a[52:55],a[116:119],v[84:87],a[52:55]
v_mfma_f32_16x16x32_bf16 a[52:55],a[112:115],v[80:83],a[52:55]
buffer_load_dwordx4 v[152:155],v6,s[28:31],s79 offen sc1
v_mfma_f32_16x16x32_bf16 a[56:59],a[112:115],v[96:99],a[56:59]
v_mfma_f32_16x16x32_bf16 a[56:59],a[116:119],v[100:103],a[56:59]
s_cmp_ge_u32 1,s71
s_cselect_b32 s80,5,3
s_mul_i32 s80,s80,163840
v_mfma_f32_16x16x32_bf16 a[56:59],a[120:123],v[104:107],a[56:59]
s_add_u32 s80,s80,s81
s_add_u32 s83,s80,20480
v_mfma_f32_16x16x32_bf16 a[56:59],a[124:127],v[108:111],a[56:59]
v_mfma_f32_16x16x32_bf16 a[60:63],a[124:127],v[124:127],a[60:63]
ds_read_b64_tr_b16 a[124:125],v2 offset:30816
ds_read_b64_tr_b16 a[126:127],v2 offset:35936
v_mfma_f32_16x16x32_bf16 a[60:63],a[120:123],v[120:123],a[60:63]
buffer_load_dwordx4 v[184:187],v6,s[28:31],s79 offen offset:1024 sc1
v_mfma_f32_16x16x32_bf16 a[60:63],a[116:119],v[116:119],a[60:63]
ds_read_b64_tr_b16 a[116:117],v2 offset:10336
ds_read_b64_tr_b16 a[118:119],v2 offset:15456
v_mfma_f32_16x16x32_bf16 a[60:63],a[112:115],v[112:115],a[60:63]
ds_read_b64_tr_b16 a[112:113],v2 offset:96
v_mfma_f32_16x16x32_bf16 a[64:67],a[128:131],v[64:67],a[64:67]
ds_read_b64_tr_b16 a[114:115],v2 offset:5216
v_mfma_f32_16x16x32_bf16 a[64:67],a[132:135],v[68:71],a[64:67]
ds_read_b64_tr_b16 a[120:121],v2 offset:20576
v_mfma_f32_16x16x32_bf16 a[64:67],a[136:139],v[72:75],a[64:67]
ds_read_b64_tr_b16 a[122:123],v2 offset:25696
v_mfma_f32_16x16x32_bf16 a[64:67],a[140:143],v[76:79],a[64:67]
buffer_load_dwordx4 v[216:219],v6,s[28:31],s79 offen offset:2048 sc1
v_mfma_f32_16x16x32_bf16 a[68:71],a[140:143],v[92:95],a[68:71]
v_mfma_f32_16x16x32_bf16 a[68:71],a[136:139],v[88:91],a[68:71]
v_mfma_f32_16x16x32_bf16 a[68:71],a[132:135],v[84:87],a[68:71]
v_mfma_f32_16x16x32_bf16 a[68:71],a[128:131],v[80:83],a[68:71]
v_mfma_f32_16x16x32_bf16 a[72:75],a[128:131],v[96:99],a[72:75]
v_mfma_f32_16x16x32_bf16 a[72:75],a[132:135],v[100:103],a[72:75]
buffer_load_dwordx4 v[248:251],v6,s[28:31],s79 offen offset:3072 sc1
v_mfma_f32_16x16x32_bf16 a[72:75],a[136:139],v[104:107],a[72:75]
v_mfma_f32_16x16x32_bf16 a[72:75],a[140:143],v[108:111],a[72:75]
v_mfma_f32_16x16x32_bf16 a[76:79],a[140:143],v[124:127],a[76:79]
ds_read_b64_tr_b16 a[140:141],v1 offset:30848
ds_read_b64_tr_b16 a[142:143],v1 offset:35968
v_mfma_f32_16x16x32_bf16 a[76:79],a[136:139],v[120:123],a[76:79]
ds_read_b64_tr_b16 a[136:137],v1 offset:20608
v_mfma_f32_16x16x32_bf16 a[76:79],a[132:135],v[116:119],a[76:79]
s_add_u32 m0,s82,40960
ds_read_b64_tr_b16 a[132:133],v1 offset:10368
v_mfma_f32_16x16x32_bf16 a[76:79],a[128:131],v[112:115],a[76:79]
ds_read_b64_tr_b16 a[128:129],v1 offset:128
v_mfma_f32_16x16x32_bf16 a[80:83],a[144:147],v[64:67],a[80:83]
ds_read_b64_tr_b16 a[130:131],v1 offset:5248
ds_read_b64_tr_b16 a[134:135],v1 offset:15488
v_mfma_f32_16x16x32_bf16 a[80:83],a[148:151],v[68:71],a[80:83]
buffer_load_dwordx4 v7,s[72:75],s80 offen lds
v_mfma_f32_16x16x32_bf16 a[80:83],a[152:155],v[72:75],a[80:83]
ds_read_b64_tr_b16 a[138:139],v1 offset:25728
v_mfma_f32_16x16x32_bf16 a[80:83],a[156:159],v[76:79],a[80:83]
v_mfma_f32_16x16x32_bf16 a[84:87],a[156:159],v[92:95],a[84:87]
v_mfma_f32_16x16x32_bf16 a[84:87],a[152:155],v[88:91],a[84:87]
v_mfma_f32_16x16x32_bf16 a[84:87],a[148:151],v[84:87],a[84:87]
buffer_load_dwordx4 v8,s[72:75],s80 offen offset:1024 lds
v_mfma_f32_16x16x32_bf16 a[84:87],a[144:147],v[80:83],a[84:87]
v_mfma_f32_16x16x32_bf16 a[88:91],a[144:147],v[96:99],a[88:91]
v_mfma_f32_16x16x32_bf16 a[88:91],a[148:151],v[100:103],a[88:91]
v_mfma_f32_16x16x32_bf16 a[88:91],a[152:155],v[104:107],a[88:91]
v_mfma_f32_16x16x32_bf16 a[88:91],a[156:159],v[108:111],a[88:91]
buffer_load_dwordx4 v9,s[72:75],s80 offen offset:2048 lds
v_mfma_f32_16x16x32_bf16 a[92:95],a[156:159],v[124:127],a[92:95]
ds_read_b64_tr_b16 a[156:157],v2 offset:30880
ds_read_b64_tr_b16 a[158:159],v2 offset:36000
v_mfma_f32_16x16x32_bf16 a[92:95],a[152:155],v[120:123],a[92:95]
ds_read_b64_tr_b16 a[152:153],v2 offset:20640
v_mfma_f32_16x16x32_bf16 a[92:95],a[148:151],v[116:119],a[92:95]
ds_read_b64_tr_b16 a[148:149],v2 offset:10400
v_mfma_f32_16x16x32_bf16 a[92:95],a[144:147],v[112:115],a[92:95]
ds_read_b64_tr_b16 a[144:145],v2 offset:160
v_mfma_f32_16x16x32_bf16 a[192:195],a[160:163],v[132:135],a[192:195]
buffer_load_dwordx4 v10,s[72:75],s80 offen offset:3072 lds
v_mfma_f32_16x16x32_bf16 a[192:195],a[164:167],v[140:143],a[192:195]
ds_read_b64_tr_b16 a[146:147],v2 offset:5280
ds_read_b64_tr_b16 a[150:151],v2 offset:15520
v_mfma_f32_16x16x32_bf16 a[192:195],a[168:171],v[148:151],a[192:195]
s_add_u32 m0,s82,45056
ds_read_b64_tr_b16 a[154:155],v2 offset:25760
v_mfma_f32_16x16x32_bf16 a[192:195],a[172:175],v[156:159],a[192:195]
s_waitcnt vmcnt(12)
v_mfma_f32_16x16x32_bf16 a[196:199],a[172:175],v[188:191],a[196:199]
v_mfma_f32_16x16x32_bf16 a[196:199],a[168:171],v[180:183],a[196:199]
buffer_load_dwordx4 v11,s[72:75],s80 offen lds
v_mfma_f32_16x16x32_bf16 a[196:199],a[164:167],v[172:175],a[196:199]
v_mfma_f32_16x16x32_bf16 a[196:199],a[160:163],v[164:167],a[196:199]
s_add_u32 m0,s82,46080
v_mfma_f32_16x16x32_bf16 a[200:203],a[160:163],v[196:199],a[200:203]
v_mfma_f32_16x16x32_bf16 a[200:203],a[164:167],v[204:207],a[200:203]
v_mfma_f32_16x16x32_bf16 a[200:203],a[168:171],v[212:215],a[200:203]
buffer_load_dwordx4 v7,s[72:75],s83 offen lds
v_mfma_f32_16x16x32_bf16 a[200:203],a[172:175],v[220:223],a[200:203]
v_mfma_f32_16x16x32_bf16 a[204:207],a[172:175],v[252:255],a[204:207]
ds_read_b64_tr_b16 a[172:173],v1 offset:30912
ds_read_b64_tr_b16 a[174:175],v1 offset:36032
v_mfma_f32_16x16x32_bf16 a[204:207],a[168:171],v[244:247],a[204:207]
ds_read_b64_tr_b16 a[168:169],v1 offset:20672
v_mfma_f32_16x16x32_bf16 a[204:207],a[164:167],v[236:239],a[204:207]
ds_read_b64_tr_b16 a[164:165],v1 offset:10432
v_mfma_f32_16x16x32_bf16 a[204:207],a[160:163],v[228:231],a[204:207]
s_barrier
buffer_load_dwordx4 v8,s[72:75],s83 offen offset:1024 lds
v_mfma_f32_16x16x32_bf16 a[208:211],a[176:179],v[132:135],a[208:211]
s_cmp_ge_u32 1,s71
s_cselect_b32 s76,5,3
s_lshl_b32 s76,s76,16
v_mfma_f32_16x16x32_bf16 a[208:211],a[180:183],v[140:143],a[208:211]
s_add_u32 s77,s76,4096
s_add_u32 s78,s76,8192
s_add_u32 s79,s76,12288
v_mfma_f32_16x16x32_bf16 a[208:211],a[184:187],v[148:151],a[208:211]
s_max_i32 s76,s76,s18
s_max_i32 s77,s77,s19
s_max_i32 s78,s78,s100
v_mfma_f32_16x16x32_bf16 a[208:211],a[188:191],v[156:159],a[208:211]
s_max_i32 s79,s79,s101
buffer_load_dwordx4 v9,s[72:75],s83 offen offset:2048 lds
v_mfma_f32_16x16x32_bf16 a[212:215],a[188:191],v[188:191],a[212:215]
ds_read_b64_tr_b16 a[160:161],v1 offset:192
ds_read_b64_tr_b16 a[162:163],v1 offset:5312
v_mfma_f32_16x16x32_bf16 a[212:215],a[184:187],v[180:183],a[212:215]
ds_read_b64_tr_b16 a[166:167],v1 offset:15552
v_mfma_f32_16x16x32_bf16 a[212:215],a[180:183],v[172:175],a[212:215]
ds_read_b64_tr_b16 a[170:171],v1 offset:25792
v_mfma_f32_16x16x32_bf16 a[212:215],a[176:179],v[164:167],a[212:215]
v_mfma_f32_16x16x32_bf16 a[216:219],a[176:179],v[196:199],a[216:219]
buffer_load_dwordx4 v10,s[72:75],s83 offen offset:3072 lds
v_mfma_f32_16x16x32_bf16 a[216:219],a[180:183],v[204:207],a[216:219]
v_mfma_f32_16x16x32_bf16 a[216:219],a[184:187],v[212:215],a[216:219]
s_add_u32 m0,s82,50176
v_mfma_f32_16x16x32_bf16 a[216:219],a[188:191],v[220:223],a[216:219]
v_mfma_f32_16x16x32_bf16 a[220:223],a[188:191],v[252:255],a[220:223]
ds_read_b64_tr_b16 a[188:189],v2 offset:30944
ds_read_b64_tr_b16 a[190:191],v2 offset:36064
v_mfma_f32_16x16x32_bf16 a[220:223],a[184:187],v[244:247],a[220:223]
buffer_load_dwordx4 v11,s[72:75],s83 offen lds
v_mfma_f32_16x16x32_bf16 a[220:223],a[180:183],v[236:239],a[220:223]
ds_read_b64_tr_b16 a[180:181],v2 offset:10464
ds_read_b64_tr_b16 a[182:183],v2 offset:15584
v_mfma_f32_16x16x32_bf16 a[220:223],a[176:179],v[228:231],a[220:223]
s_waitcnt lgkmcnt(4)
ds_read_b64_tr_b16 a[176:177],v2 offset:224
v_mfma_f32_16x16x32_bf16 a[224:227],a[96:99],v[132:135],a[224:227]
ds_read_b64_tr_b16 a[178:179],v2 offset:5344
v_mfma_f32_16x16x32_bf16 a[224:227],a[100:103],v[140:143],a[224:227]
ds_read_b64_tr_b16 a[184:185],v2 offset:20704
v_mfma_f32_16x16x32_bf16 a[224:227],a[104:107],v[148:151],a[224:227]
ds_read_b64_tr_b16 a[186:187],v2 offset:25824
v_mfma_f32_16x16x32_bf16 a[224:227],a[108:111],v[156:159],a[224:227]
v_mfma_f32_16x16x32_bf16 a[228:231],a[108:111],v[188:191],a[228:231]
v_mfma_f32_16x16x32_bf16 a[228:231],a[104:107],v[180:183],a[228:231]
v_mfma_f32_16x16x32_bf16 a[228:231],a[100:103],v[172:175],a[228:231]
v_mfma_f32_16x16x32_bf16 a[228:231],a[96:99],v[164:167],a[228:231]
buffer_load_dwordx4 v[64:67],v6,s[28:31],s76 offen sc1
v_mfma_f32_16x16x32_bf16 a[232:235],a[96:99],v[196:199],a[232:235]
v_mfma_f32_16x16x32_bf16 a[232:235],a[100:103],v[204:207],a[232:235]
v_mfma_f32_16x16x32_bf16 a[232:235],a[104:107],v[212:215],a[232:235]
v_mfma_f32_16x16x32_bf16 a[232:235],a[108:111],v[220:223],a[232:235]
)A"
R"A(v_mfma_f32_16x16x32_bf16 a[236:239],a[108:111],v[252:255],a[236:239]
buffer_load_dwordx4 v[68:71],v6,s[28:31],s76 offen offset:1024 sc1
v_mfma_f32_16x16x32_bf16 a[236:239],a[104:107],v[244:247],a[236:239]
ds_read_b64_tr_b16 a[104:105],v1 offset:20736
ds_read_b64_tr_b16 a[106:107],v1 offset:25856
v_mfma_f32_16x16x32_bf16 a[236:239],a[100:103],v[236:239],a[236:239]
ds_read_b64_tr_b16 a[100:101],v1 offset:10496
v_mfma_f32_16x16x32_bf16 a[236:239],a[96:99],v[228:231],a[236:239]
ds_read_b64_tr_b16 a[96:97],v1 offset:256
v_mfma_f32_16x16x32_bf16 a[240:243],a[112:115],v[132:135],a[240:243]
ds_read_b64_tr_b16 a[98:99],v1 offset:5376
v_mfma_f32_16x16x32_bf16 a[240:243],a[116:119],v[140:143],a[240:243]
buffer_load_dwordx4 v[72:75],v6,s[28:31],s76 offen offset:2048 sc1
v_mfma_f32_16x16x32_bf16 a[240:243],a[120:123],v[148:151],a[240:243]
ds_read_b64_tr_b16 a[102:103],v1 offset:15616
ds_read_b64_tr_b16 a[108:109],v1 offset:30976
v_mfma_f32_16x16x32_bf16 a[240:243],a[124:127],v[156:159],a[240:243]
ds_read_b64_tr_b16 a[110:111],v1 offset:36096
v_subrev_u32 v1,81920,v1
v_mfma_f32_16x16x32_bf16 a[244:247],a[124:127],v[188:191],a[244:247]
v_add_u32 v42,40960,v1
v_add_u32 v44,122880,v1
v_mfma_f32_16x16x32_bf16 a[244:247],a[120:123],v[180:183],a[244:247]
v_mfma_f32_16x16x32_bf16 a[244:247],a[116:119],v[172:175],a[244:247]
buffer_load_dwordx4 v[76:79],v6,s[28:31],s76 offen offset:3072 sc1
v_mfma_f32_16x16x32_bf16 a[244:247],a[112:115],v[164:167],a[244:247]
v_mfma_f32_16x16x32_bf16 a[248:251],a[112:115],v[196:199],a[248:251]
v_mfma_f32_16x16x32_bf16 a[248:251],a[116:119],v[204:207],a[248:251]
v_mfma_f32_16x16x32_bf16 a[248:251],a[120:123],v[212:215],a[248:251]
v_mfma_f32_16x16x32_bf16 a[248:251],a[124:127],v[220:223],a[248:251]
buffer_load_dwordx4 v[80:83],v6,s[28:31],s77 offen sc1
v_mfma_f32_16x16x32_bf16 a[252:255],a[124:127],v[252:255],a[252:255]
ds_read_b64_tr_b16 a[124:125],v2 offset:31008
ds_read_b64_tr_b16 a[126:127],v2 offset:36128
v_mfma_f32_16x16x32_bf16 a[252:255],a[120:123],v[244:247],a[252:255]
ds_read_b64_tr_b16 a[120:121],v2 offset:20768
v_mfma_f32_16x16x32_bf16 a[252:255],a[116:119],v[236:239],a[252:255]
ds_read_b64_tr_b16 a[116:117],v2 offset:10528
v_mfma_f32_16x16x32_bf16 a[252:255],a[112:115],v[228:231],a[252:255]
ds_read_b64_tr_b16 a[112:113],v2 offset:288
v_mfma_f32_16x16x32_bf16 a[0:3],a[128:131],v[132:135],a[0:3]
buffer_load_dwordx4 v[84:87],v6,s[28:31],s77 offen offset:1024 sc1
v_mfma_f32_16x16x32_bf16 a[0:3],a[132:135],v[140:143],a[0:3]
ds_read_b64_tr_b16 a[114:115],v2 offset:5408
ds_read_b64_tr_b16 a[118:119],v2 offset:15648
v_mfma_f32_16x16x32_bf16 a[0:3],a[136:139],v[148:151],a[0:3]
ds_read_b64_tr_b16 a[122:123],v2 offset:25888
v_subrev_u32 v2,81920,v2
v_mfma_f32_16x16x32_bf16 a[0:3],a[140:143],v[156:159],a[0:3]
v_add_u32 v43,40960,v2
v_add_u32 v45,122880,v2
v_mfma_f32_16x16x32_bf16 a[4:7],a[140:143],v[188:191],a[4:7]
v_mfma_f32_16x16x32_bf16 a[4:7],a[136:139],v[180:183],a[4:7]
buffer_load_dwordx4 v[88:91],v6,s[28:31],s77 offen offset:2048 sc1
v_mfma_f32_16x16x32_bf16 a[4:7],a[132:135],v[172:175],a[4:7]
v_mfma_f32_16x16x32_bf16 a[4:7],a[128:131],v[164:167],a[4:7]
v_mfma_f32_16x16x32_bf16 a[8:11],a[128:131],v[196:199],a[8:11]
v_mfma_f32_16x16x32_bf16 a[8:11],a[132:135],v[204:207],a[8:11]
v_mfma_f32_16x16x32_bf16 a[8:11],a[136:139],v[212:215],a[8:11]
buffer_load_dwordx4 v[92:95],v6,s[28:31],s77 offen offset:3072 sc1
v_mfma_f32_16x16x32_bf16 a[8:11],a[140:143],v[220:223],a[8:11]
v_mfma_f32_16x16x32_bf16 a[12:15],a[140:143],v[252:255],a[12:15]
ds_read_b64_tr_b16 a[140:141],v1 offset:30720
ds_read_b64_tr_b16 a[142:143],v1 offset:35840
v_mfma_f32_16x16x32_bf16 a[12:15],a[136:139],v[244:247],a[12:15]
ds_read_b64_tr_b16 a[136:137],v1 offset:20480
v_mfma_f32_16x16x32_bf16 a[12:15],a[132:135],v[236:239],a[12:15]
ds_read_b64_tr_b16 a[132:133],v1 offset:10240
v_mfma_f32_16x16x32_bf16 a[12:15],a[128:131],v[228:231],a[12:15]
buffer_load_dwordx4 v[96:99],v6,s[28:31],s78 offen sc1
v_mfma_f32_16x16x32_bf16 a[16:19],a[144:147],v[132:135],a[16:19]
ds_read_b64_tr_b16 a[128:129],v1
ds_read_b64_tr_b16 a[130:131],v1 offset:5120
v_mfma_f32_16x16x32_bf16 a[16:19],a[148:151],v[140:143],a[16:19]
ds_read_b64_tr_b16 a[134:135],v1 offset:15360
v_mfma_f32_16x16x32_bf16 a[16:19],a[152:155],v[148:151],a[16:19]
ds_read_b64_tr_b16 a[138:139],v1 offset:25600
v_mfma_f32_16x16x32_bf16 a[16:19],a[156:159],v[156:159],a[16:19]
v_mfma_f32_16x16x32_bf16 a[20:23],a[156:159],v[188:191],a[20:23]
buffer_load_dwordx4 v[100:103],v6,s[28:31],s78 offen offset:1024 sc1
v_mfma_f32_16x16x32_bf16 a[20:23],a[152:155],v[180:183],a[20:23]
v_mfma_f32_16x16x32_bf16 a[20:23],a[148:151],v[172:175],a[20:23]
v_mfma_f32_16x16x32_bf16 a[20:23],a[144:147],v[164:167],a[20:23]
v_mfma_f32_16x16x32_bf16 a[24:27],a[144:147],v[196:199],a[24:27]
v_mfma_f32_16x16x32_bf16 a[24:27],a[148:151],v[204:207],a[24:27]
buffer_load_dwordx4 v[104:107],v6,s[28:31],s78 offen offset:2048 sc1
v_mfma_f32_16x16x32_bf16 a[24:27],a[152:155],v[212:215],a[24:27]
v_mfma_f32_16x16x32_bf16 a[24:27],a[156:159],v[220:223],a[24:27]
v_mfma_f32_16x16x32_bf16 a[28:31],a[156:159],v[252:255],a[28:31]
ds_read_b64_tr_b16 a[156:157],v2 offset:30752
ds_read_b64_tr_b16 a[158:159],v2 offset:35872
v_mfma_f32_16x16x32_bf16 a[28:31],a[152:155],v[244:247],a[28:31]
ds_read_b64_tr_b16 a[152:153],v2 offset:20512
v_mfma_f32_16x16x32_bf16 a[28:31],a[148:151],v[236:239],a[28:31]
buffer_load_dwordx4 v[108:111],v6,s[28:31],s78 offen offset:3072 sc1
v_mfma_f32_16x16x32_bf16 a[28:31],a[144:147],v[228:231],a[28:31]
s_cmp_ge_u32 2,s71
s_cselect_b32 s80,7,5
s_mul_i32 s80,s80,163840
v_mfma_f32_16x16x32_bf16 a[32:35],a[160:163],v[132:135],a[32:35]
s_add_u32 s80,s80,s81
s_add_u32 s83,s80,20480
ds_read_b64_tr_b16 a[144:145],v2 offset:32
v_mfma_f32_16x16x32_bf16 a[32:35],a[164:167],v[140:143],a[32:35]
ds_read_b64_tr_b16 a[146:147],v2 offset:5152
ds_read_b64_tr_b16 a[148:149],v2 offset:10272
v_mfma_f32_16x16x32_bf16 a[32:35],a[168:171],v[148:151],a[32:35]
ds_read_b64_tr_b16 a[150:151],v2 offset:15392
v_mfma_f32_16x16x32_bf16 a[32:35],a[172:175],v[156:159],a[32:35]
buffer_load_dwordx4 v[112:115],v6,s[28:31],s79 offen sc1
v_mfma_f32_16x16x32_bf16 a[36:39],a[172:175],v[188:191],a[36:39]
ds_read_b64_tr_b16 a[154:155],v2 offset:25632
v_mfma_f32_16x16x32_bf16 a[36:39],a[168:171],v[180:183],a[36:39]
v_mfma_f32_16x16x32_bf16 a[36:39],a[164:167],v[172:175],a[36:39]
v_mfma_f32_16x16x32_bf16 a[36:39],a[160:163],v[164:167],a[36:39]
v_mfma_f32_16x16x32_bf16 a[40:43],a[160:163],v[196:199],a[40:43]
buffer_load_dwordx4 v[116:119],v6,s[28:31],s79 offen offset:1024 sc1
v_mfma_f32_16x16x32_bf16 a[40:43],a[164:167],v[204:207],a[40:43]
v_mfma_f32_16x16x32_bf16 a[40:43],a[168:171],v[212:215],a[40:43]
v_mfma_f32_16x16x32_bf16 a[40:43],a[172:175],v[220:223],a[40:43]
v_mfma_f32_16x16x32_bf16 a[44:47],a[172:175],v[252:255],a[44:47]
ds_read_b64_tr_b16 a[172:173],v1 offset:30784
ds_read_b64_tr_b16 a[174:175],v1 offset:35904
v_mfma_f32_16x16x32_bf16 a[44:47],a[168:171],v[244:247],a[44:47]
buffer_load_dwordx4 v[120:123],v6,s[28:31],s79 offen offset:2048 sc1
v_mfma_f32_16x16x32_bf16 a[44:47],a[164:167],v[236:239],a[44:47]
ds_read_b64_tr_b16 a[164:165],v1 offset:10304
ds_read_b64_tr_b16 a[166:167],v1 offset:15424
v_mfma_f32_16x16x32_bf16 a[44:47],a[160:163],v[228:231],a[44:47]
s_waitcnt lgkmcnt(4)
ds_read_b64_tr_b16 a[160:161],v1 offset:64
v_mfma_f32_16x16x32_bf16 a[48:51],a[176:179],v[132:135],a[48:51]
ds_read_b64_tr_b16 a[162:163],v1 offset:5184
v_mfma_f32_16x16x32_bf16 a[48:51],a[180:183],v[140:143],a[48:51]
ds_read_b64_tr_b16 a[168:169],v1 offset:20544
v_mfma_f32_16x16x32_bf16 a[48:51],a[184:187],v[148:151],a[48:51]
buffer_load_dwordx4 v[124:127],v6,s[28:31],s79 offen offset:3072 sc1
v_mfma_f32_16x16x32_bf16 a[48:51],a[188:191],v[156:159],a[48:51]
ds_read_b64_tr_b16 a[170:171],v1 offset:25664
v_mfma_f32_16x16x32_bf16 a[52:55],a[188:191],v[188:191],a[52:55]
s_add_u32 m0,s82,122880
v_mfma_f32_16x16x32_bf16 a[52:55],a[184:187],v[180:183],a[52:55]
v_mfma_f32_16x16x32_bf16 a[52:55],a[180:183],v[172:175],a[52:55]
v_mfma_f32_16x16x32_bf16 a[52:55],a[176:179],v[164:167],a[52:55]
buffer_load_dwordx4 v7,s[72:75],s80 offen lds
v_mfma_f32_16x16x32_bf16 a[56:59],a[176:179],v[196:199],a[56:59]
v_mfma_f32_16x16x32_bf16 a[56:59],a[180:183],v[204:207],a[56:59]
v_mfma_f32_16x16x32_bf16 a[56:59],a[184:187],v[212:215],a[56:59]
v_mfma_f32_16x16x32_bf16 a[56:59],a[188:191],v[220:223],a[56:59]
v_mfma_f32_16x16x32_bf16 a[60:63],a[188:191],v[252:255],a[60:63]
buffer_load_dwordx4 v8,s[72:75],s80 offen offset:1024 lds
v_mfma_f32_16x16x32_bf16 a[60:63],a[184:187],v[244:247],a[60:63]
ds_read_b64_tr_b16 a[184:185],v2 offset:20576
ds_read_b64_tr_b16 a[186:187],v2 offset:25696
v_mfma_f32_16x16x32_bf16 a[60:63],a[180:183],v[236:239],a[60:63]
ds_read_b64_tr_b16 a[180:181],v2 offset:10336
v_mfma_f32_16x16x32_bf16 a[60:63],a[176:179],v[228:231],a[60:63]
ds_read_b64_tr_b16 a[176:177],v2 offset:96
v_mfma_f32_16x16x32_bf16 a[64:67],a[96:99],v[132:135],a[64:67]
ds_read_b64_tr_b16 a[178:179],v2 offset:5216
v_mfma_f32_16x16x32_bf16 a[64:67],a[100:103],v[140:143],a[64:67]
buffer_load_dwordx4 v9,s[72:75],s80 offen offset:2048 lds
v_mfma_f32_16x16x32_bf16 a[64:67],a[104:107],v[148:151],a[64:67]
ds_read_b64_tr_b16 a[182:183],v2 offset:15456
ds_read_b64_tr_b16 a[188:189],v2 offset:30816
v_mfma_f32_16x16x32_bf16 a[64:67],a[108:111],v[156:159],a[64:67]
ds_read_b64_tr_b16 a[190:191],v2 offset:35936
v_mfma_f32_16x16x32_bf16 a[68:71],a[108:111],v[188:191],a[68:71]
v_mfma_f32_16x16x32_bf16 a[68:71],a[104:107],v[180:183],a[68:71]
v_mfma_f32_16x16x32_bf16 a[68:71],a[100:103],v[172:175],a[68:71]
buffer_load_dwordx4 v10,s[72:75],s80 offen offset:3072 lds
v_mfma_f32_16x16x32_bf16 a[68:71],a[96:99],v[164:167],a[68:71]
v_mfma_f32_16x16x32_bf16 a[72:75],a[96:99],v[196:199],a[72:75]
s_add_u32 m0,s82,126976
v_mfma_f32_16x16x32_bf16 a[72:75],a[100:103],v[204:207],a[72:75]
v_mfma_f32_16x16x32_bf16 a[72:75],a[104:107],v[212:215],a[72:75]
s_cmp_ge_u32 2,s71
s_cselect_b32 s76,7,5
s_lshl_b32 s76,s76,16
v_mfma_f32_16x16x32_bf16 a[72:75],a[108:111],v[220:223],a[72:75]
s_add_u32 s77,s76,4096
buffer_load_dwordx4 v11,s[72:75],s80 offen lds
v_mfma_f32_16x16x32_bf16 a[76:79],a[108:111],v[252:255],a[76:79]
s_add_u32 s78,s76,8192
s_add_u32 s79,s76,12288
s_max_i32 s76,s76,s18
v_mfma_f32_16x16x32_bf16 a[76:79],a[104:107],v[244:247],a[76:79]
s_add_u32 m0,s82,128000
s_max_i32 s77,s77,s19
ds_read_b64_tr_b16 a[104:105],v1 offset:20608
v_mfma_f32_16x16x32_bf16 a[76:79],a[100:103],v[236:239],a[76:79]
s_max_i32 s78,s78,s100
ds_read_b64_tr_b16 a[100:101],v1 offset:10368
ds_read_b64_tr_b16 a[102:103],v1 offset:15488
v_mfma_f32_16x16x32_bf16 a[76:79],a[96:99],v[228:231],a[76:79]
s_max_i32 s79,s79,s101
ds_read_b64_tr_b16 a[96:97],v1 offset:128
v_mfma_f32_16x16x32_bf16 a[80:83],a[112:115],v[132:135],a[80:83]
buffer_load_dwordx4 v7,s[72:75],s83 offen lds
v_mfma_f32_16x16x32_bf16 a[80:83],a[116:119],v[140:143],a[80:83]
ds_read_b64_tr_b16 a[98:99],v1 offset:5248
ds_read_b64_tr_b16 a[106:107],v1 offset:25728
v_mfma_f32_16x16x32_bf16 a[80:83],a[120:123],v[148:151],a[80:83]
ds_read_b64_tr_b16 a[108:109],v1 offset:30848
v_mfma_f32_16x16x32_bf16 a[80:83],a[124:127],v[156:159],a[80:83]
ds_read_b64_tr_b16 a[110:111],v1 offset:35968
v_mfma_f32_16x16x32_bf16 a[84:87],a[124:127],v[188:191],a[84:87]
v_mfma_f32_16x16x32_bf16 a[84:87],a[120:123],v[180:183],a[84:87]
buffer_load_dwordx4 v8,s[72:75],s83 offen offset:1024 lds
v_mfma_f32_16x16x32_bf16 a[84:87],a[116:119],v[172:175],a[84:87]
v_mfma_f32_16x16x32_bf16 a[84:87],a[112:115],v[164:167],a[84:87]
v_mfma_f32_16x16x32_bf16 a[88:91],a[112:115],v[196:199],a[88:91]
v_mfma_f32_16x16x32_bf16 a[88:91],a[116:119],v[204:207],a[88:91]
v_mfma_f32_16x16x32_bf16 a[88:91],a[120:123],v[212:215],a[88:91]
buffer_load_dwordx4 v9,s[72:75],s83 offen offset:2048 lds
v_mfma_f32_16x16x32_bf16 a[88:91],a[124:127],v[220:223],a[88:91]
v_mfma_f32_16x16x32_bf16 a[92:95],a[124:127],v[252:255],a[92:95]
ds_read_b64_tr_b16 a[124:125],v2 offset:30880
ds_read_b64_tr_b16 a[126:127],v2 offset:36000
v_mfma_f32_16x16x32_bf16 a[92:95],a[120:123],v[244:247],a[92:95]
)A"
R"A(ds_read_b64_tr_b16 a[120:121],v2 offset:20640
v_mfma_f32_16x16x32_bf16 a[92:95],a[116:119],v[236:239],a[92:95]
ds_read_b64_tr_b16 a[116:117],v2 offset:10400
v_mfma_f32_16x16x32_bf16 a[92:95],a[112:115],v[228:231],a[92:95]
buffer_load_dwordx4 v10,s[72:75],s83 offen offset:3072 lds
v_mfma_f32_16x16x32_bf16 a[192:195],a[128:131],v[128:131],a[192:195]
ds_read_b64_tr_b16 a[112:113],v2 offset:160
ds_read_b64_tr_b16 a[114:115],v2 offset:5280
v_mfma_f32_16x16x32_bf16 a[192:195],a[132:135],v[160:163],a[192:195]
s_add_u32 m0,s82,132096
ds_read_b64_tr_b16 a[118:119],v2 offset:15520
v_mfma_f32_16x16x32_bf16 a[192:195],a[136:139],v[192:195],a[192:195]
ds_read_b64_tr_b16 a[122:123],v2 offset:25760
v_mfma_f32_16x16x32_bf16 a[192:195],a[140:143],v[224:227],a[192:195]
buffer_load_dwordx4 v[132:135],v6,s[28:31],s76 offen sc1
v_mfma_f32_16x16x32_bf16 a[196:199],a[140:143],v[232:235],a[196:199]
v_mfma_f32_16x16x32_bf16 a[196:199],a[136:139],v[200:203],a[196:199]
v_mfma_f32_16x16x32_bf16 a[196:199],a[132:135],v[168:171],a[196:199]
v_mfma_f32_16x16x32_bf16 a[196:199],a[128:131],v[136:139],a[196:199]
s_waitcnt vmcnt(14)
buffer_load_dwordx4 v11,s[72:75],s83 offen lds
v_mfma_f32_16x16x32_bf16 a[200:203],a[128:131],v[144:147],a[200:203]
ds_read_b64_tr_b16 a[128:129],v1 offset:192
ds_read_b64_tr_b16 a[130:131],v1 offset:5312
v_mfma_f32_16x16x32_bf16 a[200:203],a[132:135],v[176:179],a[200:203]
ds_read_b64_tr_b16 a[132:133],v1 offset:10432
v_mfma_f32_16x16x32_bf16 a[200:203],a[136:139],v[208:211],a[200:203]
ds_read_b64_tr_b16 a[134:135],v1 offset:15552
v_mfma_f32_16x16x32_bf16 a[200:203],a[140:143],v[240:243],a[200:203]
buffer_load_dwordx4 v[140:143],v6,s[28:31],s76 offen offset:1024 sc1
v_mfma_f32_16x16x32_bf16 a[208:211],a[144:147],v[128:131],a[208:211]
ds_read_b64_tr_b16 a[136:137],v1 offset:20672
ds_read_b64_tr_b16 a[138:139],v1 offset:25792
v_mfma_f32_16x16x32_bf16 a[208:211],a[148:151],v[160:163],a[208:211]
ds_read_b64_tr_b16 a[140:141],v1 offset:30912
v_mfma_f32_16x16x32_bf16 a[208:211],a[152:155],v[192:195],a[208:211]
ds_read_b64_tr_b16 a[142:143],v1 offset:36032
v_mfma_f32_16x16x32_bf16 a[208:211],a[156:159],v[224:227],a[208:211]
buffer_load_dwordx4 v[148:151],v6,s[28:31],s76 offen offset:2048 sc1
v_mfma_f32_16x16x32_bf16 a[212:215],a[156:159],v[232:235],a[212:215]
v_mfma_f32_16x16x32_bf16 a[212:215],a[152:155],v[200:203],a[212:215]
v_mfma_f32_16x16x32_bf16 a[212:215],a[148:151],v[168:171],a[212:215]
v_mfma_f32_16x16x32_bf16 a[212:215],a[144:147],v[136:139],a[212:215]
buffer_load_dwordx4 v[156:159],v6,s[28:31],s76 offen offset:3072 sc1
v_mfma_f32_16x16x32_bf16 a[216:219],a[144:147],v[144:147],a[216:219]
s_mul_i32 s76,s70,12
ds_read_b64_tr_b16 a[144:145],v2 offset:224
ds_read_b64_tr_b16 a[146:147],v2 offset:5344
v_mfma_f32_16x16x32_bf16 a[216:219],a[148:151],v[176:179],a[216:219]
s_add_u32 s76,s76,0
s_add_u32 s76,s76,s40
ds_read_b64_tr_b16 a[148:149],v2 offset:10464
v_mfma_f32_16x16x32_bf16 a[216:219],a[152:155],v[208:211],a[216:219]
s_mul_i32 s76,s76,5120
s_max_i32 s76,s76,s101
ds_read_b64_tr_b16 a[150:151],v2 offset:15584
v_mfma_f32_16x16x32_bf16 a[216:219],a[156:159],v[240:243],a[216:219]
s_waitcnt lgkmcnt(4)
buffer_load_dwordx4 v[164:167],v6,s[28:31],s77 offen sc1
v_mfma_f32_16x16x32_bf16 a[224:227],a[160:163],v[128:131],a[224:227]
ds_read_b64_tr_b16 a[152:153],v2 offset:20704
ds_read_b64_tr_b16 a[154:155],v2 offset:25824
v_mfma_f32_16x16x32_bf16 a[224:227],a[164:167],v[160:163],a[224:227]
ds_read_b64_tr_b16 a[156:157],v2 offset:30944
v_mfma_f32_16x16x32_bf16 a[224:227],a[168:171],v[192:195],a[224:227]
ds_read_b64_tr_b16 a[158:159],v2 offset:36064
v_mfma_f32_16x16x32_bf16 a[224:227],a[172:175],v[224:227],a[224:227]
buffer_load_dwordx4 v[172:175],v6,s[28:31],s77 offen offset:1024 sc1
v_mfma_f32_16x16x32_bf16 a[228:231],a[172:175],v[232:235],a[228:231]
v_mfma_f32_16x16x32_bf16 a[228:231],a[168:171],v[200:203],a[228:231]
v_mfma_f32_16x16x32_bf16 a[228:231],a[164:167],v[168:171],a[228:231]
v_mfma_f32_16x16x32_bf16 a[228:231],a[160:163],v[136:139],a[228:231]
v_mfma_f32_16x16x32_bf16 a[232:235],a[160:163],v[144:147],a[232:235]
buffer_load_dwordx4 v[180:183],v6,s[28:31],s77 offen offset:2048 sc1
v_mfma_f32_16x16x32_bf16 a[232:235],a[164:167],v[176:179],a[232:235]
ds_read_b64_tr_b16 a[160:161],v1 offset:256
ds_read_b64_tr_b16 a[162:163],v1 offset:5376
v_mfma_f32_16x16x32_bf16 a[232:235],a[168:171],v[208:211],a[232:235]
ds_read_b64_tr_b16 a[164:165],v1 offset:10496
v_mfma_f32_16x16x32_bf16 a[232:235],a[172:175],v[240:243],a[232:235]
ds_read_b64_tr_b16 a[166:167],v1 offset:15616
v_mfma_f32_16x16x32_bf16 a[240:243],a[176:179],v[128:131],a[240:243]
ds_read_b64_tr_b16 a[168:169],v1 offset:20736
v_mfma_f32_16x16x32_bf16 a[240:243],a[180:183],v[160:163],a[240:243]
buffer_load_dwordx4 v[188:191],v6,s[28:31],s77 offen offset:3072 sc1
v_mfma_f32_16x16x32_bf16 a[240:243],a[184:187],v[192:195],a[240:243]
s_mul_i32 s77,s70,12
ds_read_b64_tr_b16 a[170:171],v1 offset:25856
ds_read_b64_tr_b16 a[172:173],v1 offset:30976
v_mfma_f32_16x16x32_bf16 a[240:243],a[188:191],v[224:227],a[240:243]
s_add_u32 s77,s77,4
s_add_u32 s77,s77,s40
ds_read_b64_tr_b16 a[174:175],v1 offset:36096
v_mfma_f32_16x16x32_bf16 a[244:247],a[188:191],v[232:235],a[244:247]
s_mul_i32 s77,s77,5120
s_max_i32 s77,s77,s101
v_mfma_f32_16x16x32_bf16 a[244:247],a[184:187],v[200:203],a[244:247]
v_mfma_f32_16x16x32_bf16 a[244:247],a[180:183],v[168:171],a[244:247]
buffer_load_dwordx4 v[196:199],v6,s[28:31],s78 offen sc1
v_mfma_f32_16x16x32_bf16 a[244:247],a[176:179],v[136:139],a[244:247]
v_mfma_f32_16x16x32_bf16 a[248:251],a[176:179],v[144:147],a[248:251]
ds_read_b64_tr_b16 a[176:177],v2 offset:288
ds_read_b64_tr_b16 a[178:179],v2 offset:5408
v_mfma_f32_16x16x32_bf16 a[248:251],a[180:183],v[176:179],a[248:251]
ds_read_b64_tr_b16 a[180:181],v2 offset:10528
v_mfma_f32_16x16x32_bf16 a[248:251],a[184:187],v[208:211],a[248:251]
ds_read_b64_tr_b16 a[182:183],v2 offset:15648
v_mfma_f32_16x16x32_bf16 a[248:251],a[188:191],v[240:243],a[248:251]
buffer_load_dwordx4 v[204:207],v6,s[28:31],s78 offen offset:1024 sc1
v_mfma_f32_16x16x32_bf16 a[0:3],a[96:99],v[128:131],a[0:3]
ds_read_b64_tr_b16 a[184:185],v2 offset:20768
ds_read_b64_tr_b16 a[186:187],v2 offset:25888
v_mfma_f32_16x16x32_bf16 a[0:3],a[100:103],v[160:163],a[0:3]
ds_read_b64_tr_b16 a[188:189],v2 offset:31008
v_mfma_f32_16x16x32_bf16 a[0:3],a[104:107],v[192:195],a[0:3]
ds_read_b64_tr_b16 a[190:191],v2 offset:36128
v_mfma_f32_16x16x32_bf16 a[0:3],a[108:111],v[224:227],a[0:3]
v_mfma_f32_16x16x32_bf16 a[4:7],a[108:111],v[232:235],a[4:7]
buffer_load_dwordx4 v[212:215],v6,s[28:31],s78 offen offset:2048 sc1
v_mfma_f32_16x16x32_bf16 a[4:7],a[104:107],v[200:203],a[4:7]
v_mfma_f32_16x16x32_bf16 a[4:7],a[100:103],v[168:171],a[4:7]
v_mfma_f32_16x16x32_bf16 a[4:7],a[96:99],v[136:139],a[4:7]
v_mfma_f32_16x16x32_bf16 a[8:11],a[96:99],v[144:147],a[8:11]
v_mfma_f32_16x16x32_bf16 a[8:11],a[100:103],v[176:179],a[8:11]
buffer_load_dwordx4 v[220:223],v6,s[28:31],s78 offen offset:3072 sc1
v_mfma_f32_16x16x32_bf16 a[8:11],a[104:107],v[208:211],a[8:11]
s_mul_i32 s78,s70,12
s_add_u32 s78,s78,8
s_add_u32 s78,s78,s40
v_mfma_f32_16x16x32_bf16 a[8:11],a[108:111],v[240:243],a[8:11]
s_mul_i32 s78,s78,5120
s_max_i32 s78,s78,s101
v_mfma_f32_16x16x32_bf16 a[16:19],a[112:115],v[128:131],a[16:19]
v_mfma_f32_16x16x32_bf16 a[16:19],a[116:119],v[160:163],a[16:19]
v_mfma_f32_16x16x32_bf16 a[16:19],a[120:123],v[192:195],a[16:19]
buffer_load_dwordx4 v[228:231],v6,s[28:31],s79 offen sc1
v_mfma_f32_16x16x32_bf16 a[16:19],a[124:127],v[224:227],a[16:19]
v_mfma_f32_16x16x32_bf16 a[20:23],a[124:127],v[232:235],a[20:23]
v_mfma_f32_16x16x32_bf16 a[20:23],a[120:123],v[200:203],a[20:23]
v_mfma_f32_16x16x32_bf16 a[20:23],a[116:119],v[168:171],a[20:23]
v_mfma_f32_16x16x32_bf16 a[20:23],a[112:115],v[136:139],a[20:23]
buffer_load_dwordx4 v[236:239],v6,s[28:31],s79 offen offset:1024 sc1
v_mfma_f32_16x16x32_bf16 a[24:27],a[112:115],v[144:147],a[24:27]
v_mfma_f32_16x16x32_bf16 a[24:27],a[116:119],v[176:179],a[24:27]
v_mfma_f32_16x16x32_bf16 a[24:27],a[120:123],v[208:211],a[24:27]
v_mfma_f32_16x16x32_bf16 a[24:27],a[124:127],v[240:243],a[24:27]
v_mfma_f32_16x16x32_bf16 a[32:35],a[128:131],v[128:131],a[32:35]
buffer_load_dwordx4 v[244:247],v6,s[28:31],s79 offen offset:2048 sc1
v_mfma_f32_16x16x32_bf16 a[32:35],a[132:135],v[160:163],a[32:35]
v_mfma_f32_16x16x32_bf16 a[32:35],a[136:139],v[192:195],a[32:35]
v_mfma_f32_16x16x32_bf16 a[32:35],a[140:143],v[224:227],a[32:35]
v_mfma_f32_16x16x32_bf16 a[36:39],a[140:143],v[232:235],a[36:39]
v_mfma_f32_16x16x32_bf16 a[36:39],a[136:139],v[200:203],a[36:39]
buffer_load_dwordx4 v[252:255],v6,s[28:31],s79 offen offset:3072 sc1
v_mfma_f32_16x16x32_bf16 a[36:39],a[132:135],v[168:171],a[36:39]
v_mfma_f32_16x16x32_bf16 a[36:39],a[128:131],v[136:139],a[36:39]
v_mfma_f32_16x16x32_bf16 a[40:43],a[128:131],v[144:147],a[40:43]
v_mfma_f32_16x16x32_bf16 a[40:43],a[132:135],v[176:179],a[40:43]
v_mfma_f32_16x16x32_bf16 a[40:43],a[136:139],v[208:211],a[40:43]
s_waitcnt vmcnt(15)
v_mfma_f32_16x16x32_bf16 a[40:43],a[140:143],v[240:243],a[40:43]
s_barrier
ds_read_b64_tr_b16 a[96:97],v42
ds_read_b64_tr_b16 a[98:99],v42 offset:5120
s_waitcnt lgkmcnt(2)
v_mfma_f32_16x16x32_bf16 a[48:51],a[144:147],v[128:131],a[48:51]
ds_read_b64_tr_b16 a[100:101],v42 offset:10240
v_mfma_f32_16x16x32_bf16 a[48:51],a[148:151],v[160:163],a[48:51]
ds_read_b64_tr_b16 a[102:103],v42 offset:15360
v_mfma_f32_16x16x32_bf16 a[48:51],a[152:155],v[192:195],a[48:51]
ds_read_b64_tr_b16 a[104:105],v42 offset:20480
v_mfma_f32_16x16x32_bf16 a[48:51],a[156:159],v[224:227],a[48:51]
ds_read_b64_tr_b16 a[106:107],v42 offset:25600
ds_read_b64_tr_b16 a[108:109],v42 offset:30720
v_mfma_f32_16x16x32_bf16 a[52:55],a[156:159],v[232:235],a[52:55]
ds_read_b64_tr_b16 a[110:111],v42 offset:35840
v_mfma_f32_16x16x32_bf16 a[52:55],a[152:155],v[200:203],a[52:55]
ds_read_b64_tr_b16 a[112:113],v44
v_mfma_f32_16x16x32_bf16 a[52:55],a[148:151],v[168:171],a[52:55]
ds_read_b64_tr_b16 a[114:115],v44 offset:5120
v_mfma_f32_16x16x32_bf16 a[52:55],a[144:147],v[136:139],a[52:55]
ds_read_b64_tr_b16 a[116:117],v44 offset:10240
ds_read_b64_tr_b16 a[118:119],v44 offset:15360
v_mfma_f32_16x16x32_bf16 a[56:59],a[144:147],v[144:147],a[56:59]
ds_read_b64_tr_b16 a[120:121],v44 offset:20480
v_mfma_f32_16x16x32_bf16 a[56:59],a[148:151],v[176:179],a[56:59]
ds_read_b64_tr_b16 a[122:123],v44 offset:25600
v_mfma_f32_16x16x32_bf16 a[56:59],a[152:155],v[208:211],a[56:59]
ds_read_b64_tr_b16 a[124:125],v44 offset:30720
v_mfma_f32_16x16x32_bf16 a[56:59],a[156:159],v[240:243],a[56:59]
ds_read_b64_tr_b16 a[126:127],v44 offset:35840
ds_read_b64_tr_b16 a[128:129],v43 offset:32
v_mfma_f32_16x16x32_bf16 a[64:67],a[160:163],v[128:131],a[64:67]
ds_read_b64_tr_b16 a[130:131],v43 offset:5152
v_mfma_f32_16x16x32_bf16 a[64:67],a[164:167],v[160:163],a[64:67]
ds_read_b64_tr_b16 a[132:133],v43 offset:10272
v_mfma_f32_16x16x32_bf16 a[64:67],a[168:171],v[192:195],a[64:67]
ds_read_b64_tr_b16 a[134:135],v43 offset:15392
v_mfma_f32_16x16x32_bf16 a[64:67],a[172:175],v[224:227],a[64:67]
ds_read_b64_tr_b16 a[136:137],v43 offset:20512
ds_read_b64_tr_b16 a[138:139],v43 offset:25632
v_mfma_f32_16x16x32_bf16 a[68:71],a[172:175],v[232:235],a[68:71]
ds_read_b64_tr_b16 a[140:141],v43 offset:30752
v_mfma_f32_16x16x32_bf16 a[68:71],a[168:171],v[200:203],a[68:71]
ds_read_b64_tr_b16 a[142:143],v43 offset:35872
v_mfma_f32_16x16x32_bf16 a[68:71],a[164:167],v[168:171],a[68:71]
ds_read_b64_tr_b16 a[144:145],v45 offset:32
v_mfma_f32_16x16x32_bf16 a[68:71],a[160:163],v[136:139],a[68:71]
ds_read_b64_tr_b16 a[146:147],v45 offset:5152
ds_read_b64_tr_b16 a[148:149],v45 offset:10272
v_mfma_f32_16x16x32_bf16 a[72:75],a[160:163],v[144:147],a[72:75]
ds_read_b64_tr_b16 a[150:151],v45 offset:15392
v_mfma_f32_16x16x32_bf16 a[72:75],a[164:167],v[176:179],a[72:75]
ds_read_b64_tr_b16 a[152:153],v45 offset:20512
v_mfma_f32_16x16x32_bf16 a[72:75],a[168:171],v[208:211],a[72:75]
ds_read_b64_tr_b16 a[154:155],v45 offset:25632
v_mfma_f32_16x16x32_bf16 a[72:75],a[172:175],v[240:243],a[72:75]
)A"
R"A(ds_read_b64_tr_b16 a[156:157],v45 offset:30752
ds_read_b64_tr_b16 a[158:159],v45 offset:35872
v_mfma_f32_16x16x32_bf16 a[80:83],a[176:179],v[128:131],a[80:83]
ds_read_b64_tr_b16 a[160:161],v42 offset:64
v_mfma_f32_16x16x32_bf16 a[80:83],a[180:183],v[160:163],a[80:83]
ds_read_b64_tr_b16 a[162:163],v42 offset:5184
v_mfma_f32_16x16x32_bf16 a[80:83],a[184:187],v[192:195],a[80:83]
ds_read_b64_tr_b16 a[164:165],v42 offset:10304
v_mfma_f32_16x16x32_bf16 a[80:83],a[188:191],v[224:227],a[80:83]
ds_read_b64_tr_b16 a[166:167],v42 offset:15424
ds_read_b64_tr_b16 a[168:169],v42 offset:20544
v_mfma_f32_16x16x32_bf16 a[84:87],a[188:191],v[232:235],a[84:87]
ds_read_b64_tr_b16 a[170:171],v42 offset:25664
v_mfma_f32_16x16x32_bf16 a[84:87],a[184:187],v[200:203],a[84:87]
ds_read_b64_tr_b16 a[172:173],v42 offset:30784
v_mfma_f32_16x16x32_bf16 a[84:87],a[180:183],v[168:171],a[84:87]
ds_read_b64_tr_b16 a[174:175],v42 offset:35904
v_mfma_f32_16x16x32_bf16 a[84:87],a[176:179],v[136:139],a[84:87]
v_mfma_f32_16x16x32_bf16 a[88:91],a[176:179],v[144:147],a[88:91]
ds_read_b64_tr_b16 a[176:177],v44 offset:64
ds_read_b64_tr_b16 a[178:179],v44 offset:5184
v_mfma_f32_16x16x32_bf16 a[88:91],a[180:183],v[176:179],a[88:91]
ds_read_b64_tr_b16 a[180:181],v44 offset:10304
v_mfma_f32_16x16x32_bf16 a[88:91],a[184:187],v[208:211],a[88:91]
ds_read_b64_tr_b16 a[182:183],v44 offset:15424
v_mfma_f32_16x16x32_bf16 a[88:91],a[188:191],v[240:243],a[88:91]
s_waitcnt lgkmcnt(7)
ds_read_b64_tr_b16 a[184:185],v44 offset:20544
v_mfma_f32_16x16x32_bf16 a[192:195],a[96:99],v[64:67],a[192:195]
ds_read_b64_tr_b16 a[186:187],v44 offset:25664
ds_read_b64_tr_b16 a[188:189],v44 offset:30784
v_mfma_f32_16x16x32_bf16 a[192:195],a[100:103],v[68:71],a[192:195]
ds_read_b64_tr_b16 a[190:191],v44 offset:35904
v_mfma_f32_16x16x32_bf16 a[192:195],a[104:107],v[72:75],a[192:195]
v_mfma_f32_16x16x32_bf16 a[192:195],a[108:111],v[76:79],a[192:195]
v_mfma_f32_16x16x32_bf16 a[196:199],a[108:111],v[92:95],a[196:199]
v_mfma_f32_16x16x32_bf16 a[196:199],a[104:107],v[88:91],a[196:199]
v_mfma_f32_16x16x32_bf16 a[196:199],a[100:103],v[84:87],a[196:199]
v_mfma_f32_16x16x32_bf16 a[196:199],a[96:99],v[80:83],a[196:199]
v_mfma_f32_16x16x32_bf16 a[200:203],a[96:99],v[96:99],a[200:203]
ds_read_b64_tr_b16 a[96:97],v43 offset:96
ds_read_b64_tr_b16 a[98:99],v43 offset:5216
v_mfma_f32_16x16x32_bf16 a[200:203],a[100:103],v[100:103],a[200:203]
ds_read_b64_tr_b16 a[100:101],v43 offset:10336
v_mfma_f32_16x16x32_bf16 a[200:203],a[104:107],v[104:107],a[200:203]
ds_read_b64_tr_b16 a[102:103],v43 offset:15456
v_mfma_f32_16x16x32_bf16 a[200:203],a[108:111],v[108:111],a[200:203]
ds_read_b64_tr_b16 a[104:105],v43 offset:20576
v_mfma_f32_16x16x32_bf16 a[192:195],a[112:115],v[132:135],a[192:195]
s_waitcnt vmcnt(3)
ds_read_b64_tr_b16 a[106:107],v43 offset:25696
ds_read_b64_tr_b16 a[108:109],v43 offset:30816
v_mfma_f32_16x16x32_bf16 a[192:195],a[116:119],v[140:143],a[192:195]
ds_read_b64_tr_b16 a[110:111],v43 offset:35936
v_mfma_f32_16x16x32_bf16 a[192:195],a[120:123],v[148:151],a[192:195]
v_mfma_f32_16x16x32_bf16 a[192:195],a[124:127],v[156:159],a[192:195]
v_mfma_f32_16x16x32_bf16 a[196:199],a[124:127],v[188:191],a[196:199]
v_mfma_f32_16x16x32_bf16 a[196:199],a[120:123],v[180:183],a[196:199]
v_mfma_f32_16x16x32_bf16 a[196:199],a[116:119],v[172:175],a[196:199]
v_mfma_f32_16x16x32_bf16 a[196:199],a[112:115],v[164:167],a[196:199]
v_mfma_f32_16x16x32_bf16 a[200:203],a[112:115],v[196:199],a[200:203]
ds_read_b64_tr_b16 a[112:113],v45 offset:96
ds_read_b64_tr_b16 a[114:115],v45 offset:5216
v_mfma_f32_16x16x32_bf16 a[200:203],a[116:119],v[204:207],a[200:203]
v_accvgpr_read_b32 v128,a192
v_accvgpr_read_b32 v129,a193
v_mfma_f32_16x16x32_bf16 a[200:203],a[120:123],v[212:215],a[200:203]
v_cvt_pk_bf16_f32 v128,v128,v129
v_accvgpr_read_b32 v130,a194
v_mfma_f32_16x16x32_bf16 a[200:203],a[124:127],v[220:223],a[200:203]
v_accvgpr_read_b32 v131,a195
v_cvt_pk_bf16_f32 v129,v130,v131
v_mfma_f32_16x16x32_bf16 a[208:211],a[128:131],v[64:67],a[208:211]
ds_read_b64_tr_b16 a[116:117],v45 offset:10336
ds_read_b64_tr_b16 a[118:119],v45 offset:15456
v_mfma_f32_16x16x32_bf16 a[208:211],a[132:135],v[68:71],a[208:211]
v_accvgpr_read_b32 v136,a196
v_accvgpr_read_b32 v137,a197
v_mfma_f32_16x16x32_bf16 a[208:211],a[136:139],v[72:75],a[208:211]
v_cvt_pk_bf16_f32 v136,v136,v137
v_accvgpr_read_b32 v138,a198
v_mfma_f32_16x16x32_bf16 a[208:211],a[140:143],v[76:79],a[208:211]
v_accvgpr_read_b32 v139,a199
v_cvt_pk_bf16_f32 v137,v138,v139
v_mfma_f32_16x16x32_bf16 a[212:215],a[140:143],v[92:95],a[212:215]
ds_read_b64_tr_b16 a[120:121],v45 offset:20576
ds_read_b64_tr_b16 a[122:123],v45 offset:25696
v_mfma_f32_16x16x32_bf16 a[212:215],a[136:139],v[88:91],a[212:215]
ds_read_b64_tr_b16 a[124:125],v45 offset:30816
v_accvgpr_read_b32 v144,a200
v_mfma_f32_16x16x32_bf16 a[212:215],a[132:135],v[84:87],a[212:215]
ds_read_b64_tr_b16 a[126:127],v45 offset:35936
v_accvgpr_read_b32 v145,a201
v_mfma_f32_16x16x32_bf16 a[212:215],a[128:131],v[80:83],a[212:215]
v_cvt_pk_bf16_f32 v144,v144,v145
v_accvgpr_read_b32 v146,a202
v_mfma_f32_16x16x32_bf16 a[216:219],a[128:131],v[96:99],a[216:219]
v_accvgpr_read_b32 v147,a203
v_cvt_pk_bf16_f32 v145,v146,v147
v_mfma_f32_16x16x32_bf16 a[216:219],a[132:135],v[100:103],a[216:219]
ds_read_b64_tr_b16 a[128:129],v42 offset:128
ds_read_b64_tr_b16 a[130:131],v42 offset:5248
v_mfma_f32_16x16x32_bf16 a[216:219],a[136:139],v[104:107],a[216:219]
ds_read_b64_tr_b16 a[132:133],v42 offset:10368
v_mfma_f32_16x16x32_bf16 a[216:219],a[140:143],v[108:111],a[216:219]
ds_read_b64_tr_b16 a[134:135],v42 offset:15488
v_mfma_f32_16x16x32_bf16 a[208:211],a[144:147],v[132:135],a[208:211]
ds_read_b64_tr_b16 a[136:137],v42 offset:20608
v_mfma_f32_16x16x32_bf16 a[208:211],a[148:151],v[140:143],a[208:211]
ds_read_b64_tr_b16 a[138:139],v42 offset:25728
ds_read_b64_tr_b16 a[140:141],v42 offset:30848
v_mfma_f32_16x16x32_bf16 a[208:211],a[152:155],v[148:151],a[208:211]
ds_read_b64_tr_b16 a[142:143],v42 offset:35968
v_mfma_f32_16x16x32_bf16 a[208:211],a[156:159],v[156:159],a[208:211]
v_mfma_f32_16x16x32_bf16 a[212:215],a[156:159],v[188:191],a[212:215]
v_mfma_f32_16x16x32_bf16 a[212:215],a[152:155],v[180:183],a[212:215]
v_mfma_f32_16x16x32_bf16 a[212:215],a[148:151],v[172:175],a[212:215]
v_mfma_f32_16x16x32_bf16 a[212:215],a[144:147],v[164:167],a[212:215]
v_mfma_f32_16x16x32_bf16 a[216:219],a[144:147],v[196:199],a[216:219]
ds_read_b64_tr_b16 a[144:145],v44 offset:128
ds_read_b64_tr_b16 a[146:147],v44 offset:5248
v_mfma_f32_16x16x32_bf16 a[216:219],a[148:151],v[204:207],a[216:219]
v_accvgpr_read_b32 v130,a208
v_accvgpr_read_b32 v131,a209
v_mfma_f32_16x16x32_bf16 a[216:219],a[152:155],v[212:215],a[216:219]
v_accvgpr_read_b32 v14,a210
v_accvgpr_read_b32 v15,a211
v_mfma_f32_16x16x32_bf16 a[216:219],a[156:159],v[220:223],a[216:219]
v_cvt_pk_bf16_f32 v130,v130,v131
v_cvt_pk_bf16_f32 v131,v14,v15
v_mfma_f32_16x16x32_bf16 a[224:227],a[160:163],v[64:67],a[224:227]
buffer_store_dwordx4 v[128:131],v6,s[32:35],s84 offen sc1
v_mfma_f32_16x16x32_bf16 a[224:227],a[164:167],v[68:71],a[224:227]
s_waitcnt lgkmcnt(2)
v_accvgpr_read_b32 v138,a212
v_accvgpr_read_b32 v139,a213
v_mfma_f32_16x16x32_bf16 a[224:227],a[168:171],v[72:75],a[224:227]
v_accvgpr_read_b32 v16,a214
v_accvgpr_read_b32 v17,a215
v_mfma_f32_16x16x32_bf16 a[224:227],a[172:175],v[76:79],a[224:227]
v_cvt_pk_bf16_f32 v138,v138,v139
v_cvt_pk_bf16_f32 v139,v16,v17
v_mfma_f32_16x16x32_bf16 a[228:231],a[172:175],v[92:95],a[228:231]
buffer_store_dwordx4 v[136:139],v6,s[32:35],s85 offen sc1
v_mfma_f32_16x16x32_bf16 a[228:231],a[168:171],v[88:91],a[228:231]
v_accvgpr_read_b32 v146,a216
v_accvgpr_read_b32 v147,a217
v_mfma_f32_16x16x32_bf16 a[228:231],a[164:167],v[84:87],a[228:231]
v_accvgpr_read_b32 v12,a218
v_accvgpr_read_b32 v13,a219
v_mfma_f32_16x16x32_bf16 a[228:231],a[160:163],v[80:83],a[228:231]
v_cvt_pk_bf16_f32 v146,v146,v147
v_cvt_pk_bf16_f32 v147,v12,v13
v_mfma_f32_16x16x32_bf16 a[232:235],a[160:163],v[96:99],a[232:235]
buffer_store_dwordx4 v[144:147],v6,s[32:35],s86 offen sc1
v_mfma_f32_16x16x32_bf16 a[232:235],a[164:167],v[100:103],a[232:235]
ds_read_b64_tr_b16 a[148:149],v44 offset:10368
ds_read_b64_tr_b16 a[150:151],v44 offset:15488
v_mfma_f32_16x16x32_bf16 a[232:235],a[168:171],v[104:107],a[232:235]
ds_read_b64_tr_b16 a[152:153],v44 offset:20608
v_mfma_f32_16x16x32_bf16 a[232:235],a[172:175],v[108:111],a[232:235]
ds_read_b64_tr_b16 a[154:155],v44 offset:25728
v_mfma_f32_16x16x32_bf16 a[224:227],a[176:179],v[132:135],a[224:227]
ds_read_b64_tr_b16 a[156:157],v44 offset:30848
v_mfma_f32_16x16x32_bf16 a[224:227],a[180:183],v[140:143],a[224:227]
ds_read_b64_tr_b16 a[158:159],v44 offset:35968
ds_read_b64_tr_b16 a[160:161],v43 offset:160
v_mfma_f32_16x16x32_bf16 a[224:227],a[184:187],v[148:151],a[224:227]
ds_read_b64_tr_b16 a[162:163],v43 offset:5280
v_mfma_f32_16x16x32_bf16 a[224:227],a[188:191],v[156:159],a[224:227]
ds_read_b64_tr_b16 a[164:165],v43 offset:10400
v_mfma_f32_16x16x32_bf16 a[228:231],a[188:191],v[188:191],a[228:231]
ds_read_b64_tr_b16 a[166:167],v43 offset:15520
v_mfma_f32_16x16x32_bf16 a[228:231],a[184:187],v[180:183],a[228:231]
ds_read_b64_tr_b16 a[168:169],v43 offset:20640
ds_read_b64_tr_b16 a[170:171],v43 offset:25760
v_mfma_f32_16x16x32_bf16 a[228:231],a[180:183],v[172:175],a[228:231]
ds_read_b64_tr_b16 a[172:173],v43 offset:30880
v_mfma_f32_16x16x32_bf16 a[228:231],a[176:179],v[164:167],a[228:231]
ds_read_b64_tr_b16 a[174:175],v43 offset:36000
v_mfma_f32_16x16x32_bf16 a[232:235],a[176:179],v[196:199],a[232:235]
ds_read_b64_tr_b16 a[176:177],v45 offset:160
v_mfma_f32_16x16x32_bf16 a[232:235],a[180:183],v[204:207],a[232:235]
v_accvgpr_read_b32 v160,a224
v_accvgpr_read_b32 v161,a225
v_mfma_f32_16x16x32_bf16 a[232:235],a[184:187],v[212:215],a[232:235]
v_cvt_pk_bf16_f32 v160,v160,v161
v_accvgpr_read_b32 v162,a226
v_mfma_f32_16x16x32_bf16 a[232:235],a[188:191],v[220:223],a[232:235]
v_accvgpr_read_b32 v163,a227
v_cvt_pk_bf16_f32 v161,v162,v163
v_mfma_f32_16x16x32_bf16 a[240:243],a[96:99],v[64:67],a[240:243]
ds_read_b64_tr_b16 a[178:179],v45 offset:5280
ds_read_b64_tr_b16 a[180:181],v45 offset:10400
v_mfma_f32_16x16x32_bf16 a[240:243],a[100:103],v[68:71],a[240:243]
v_accvgpr_read_b32 v168,a228
v_accvgpr_read_b32 v169,a229
v_mfma_f32_16x16x32_bf16 a[240:243],a[104:107],v[72:75],a[240:243]
v_cvt_pk_bf16_f32 v168,v168,v169
v_accvgpr_read_b32 v170,a230
v_mfma_f32_16x16x32_bf16 a[240:243],a[108:111],v[76:79],a[240:243]
v_accvgpr_read_b32 v171,a231
v_cvt_pk_bf16_f32 v169,v170,v171
v_mfma_f32_16x16x32_bf16 a[244:247],a[108:111],v[92:95],a[244:247]
ds_read_b64_tr_b16 a[182:183],v45 offset:15520
ds_read_b64_tr_b16 a[184:185],v45 offset:20640
v_mfma_f32_16x16x32_bf16 a[244:247],a[104:107],v[88:91],a[244:247]
ds_read_b64_tr_b16 a[186:187],v45 offset:25760
v_accvgpr_read_b32 v176,a232
v_mfma_f32_16x16x32_bf16 a[244:247],a[100:103],v[84:87],a[244:247]
ds_read_b64_tr_b16 a[188:189],v45 offset:30880
v_accvgpr_read_b32 v177,a233
v_mfma_f32_16x16x32_bf16 a[244:247],a[96:99],v[80:83],a[244:247]
ds_read_b64_tr_b16 a[190:191],v45 offset:36000
v_cvt_pk_bf16_f32 v176,v176,v177
v_mfma_f32_16x16x32_bf16 a[248:251],a[96:99],v[96:99],a[248:251]
v_accvgpr_read_b32 v178,a234
v_accvgpr_read_b32 v179,a235
v_mfma_f32_16x16x32_bf16 a[248:251],a[100:103],v[100:103],a[248:251]
ds_read_b64_tr_b16 a[96:97],v42 offset:192
v_cvt_pk_bf16_f32 v177,v178,v179
v_mfma_f32_16x16x32_bf16 a[248:251],a[104:107],v[104:107],a[248:251]
ds_read_b64_tr_b16 a[98:99],v42 offset:5312
ds_read_b64_tr_b16 a[100:101],v42 offset:10432
v_mfma_f32_16x16x32_bf16 a[248:251],a[108:111],v[108:111],a[248:251]
ds_read_b64_tr_b16 a[102:103],v42 offset:15552
v_mfma_f32_16x16x32_bf16 a[240:243],a[112:115],v[132:135],a[240:243]
ds_read_b64_tr_b16 a[104:105],v42 offset:20672
v_mfma_f32_16x16x32_bf16 a[240:243],a[116:119],v[140:143],a[240:243]
ds_read_b64_tr_b16 a[106:107],v42 offset:25792
v_mfma_f32_16x16x32_bf16 a[240:243],a[120:123],v[148:151],a[240:243]
ds_read_b64_tr_b16 a[108:109],v42 offset:30912
ds_read_b64_tr_b16 a[110:111],v42 offset:36032
v_mfma_f32_16x16x32_bf16 a[240:243],a[124:127],v[156:159],a[240:243]
)A"
R"A(v_mfma_f32_16x16x32_bf16 a[244:247],a[124:127],v[188:191],a[244:247]
v_mfma_f32_16x16x32_bf16 a[244:247],a[120:123],v[180:183],a[244:247]
v_mfma_f32_16x16x32_bf16 a[244:247],a[116:119],v[172:175],a[244:247]
v_mfma_f32_16x16x32_bf16 a[244:247],a[112:115],v[164:167],a[244:247]
v_mfma_f32_16x16x32_bf16 a[248:251],a[112:115],v[196:199],a[248:251]
ds_read_b64_tr_b16 a[112:113],v44 offset:192
ds_read_b64_tr_b16 a[114:115],v44 offset:5312
v_mfma_f32_16x16x32_bf16 a[248:251],a[116:119],v[204:207],a[248:251]
v_accvgpr_read_b32 v162,a240
v_accvgpr_read_b32 v163,a241
v_mfma_f32_16x16x32_bf16 a[248:251],a[120:123],v[212:215],a[248:251]
v_accvgpr_read_b32 v14,a242
v_accvgpr_read_b32 v15,a243
v_mfma_f32_16x16x32_bf16 a[248:251],a[124:127],v[220:223],a[248:251]
v_cvt_pk_bf16_f32 v162,v162,v163
v_cvt_pk_bf16_f32 v163,v14,v15
v_mfma_f32_16x16x32_bf16 a[0:3],a[128:131],v[64:67],a[0:3]
buffer_store_dwordx4 v[160:163],v6,s[32:35],s84 offen offset:1024 sc1
v_mfma_f32_16x16x32_bf16 a[0:3],a[132:135],v[68:71],a[0:3]
v_accvgpr_read_b32 v170,a244
v_accvgpr_read_b32 v171,a245
v_mfma_f32_16x16x32_bf16 a[0:3],a[136:139],v[72:75],a[0:3]
v_accvgpr_read_b32 v16,a246
v_accvgpr_read_b32 v17,a247
v_mfma_f32_16x16x32_bf16 a[0:3],a[140:143],v[76:79],a[0:3]
v_cvt_pk_bf16_f32 v170,v170,v171
v_cvt_pk_bf16_f32 v171,v16,v17
v_mfma_f32_16x16x32_bf16 a[4:7],a[140:143],v[92:95],a[4:7]
buffer_store_dwordx4 v[168:171],v6,s[32:35],s85 offen offset:1024 sc1
v_mfma_f32_16x16x32_bf16 a[4:7],a[136:139],v[88:91],a[4:7]
v_accvgpr_read_b32 v178,a248
v_accvgpr_read_b32 v179,a249
v_mfma_f32_16x16x32_bf16 a[4:7],a[132:135],v[84:87],a[4:7]
v_accvgpr_read_b32 v12,a250
v_accvgpr_read_b32 v13,a251
v_mfma_f32_16x16x32_bf16 a[4:7],a[128:131],v[80:83],a[4:7]
v_cvt_pk_bf16_f32 v178,v178,v179
v_cvt_pk_bf16_f32 v179,v12,v13
v_mfma_f32_16x16x32_bf16 a[8:11],a[128:131],v[96:99],a[8:11]
buffer_store_dwordx4 v[176:179],v6,s[32:35],s86 offen offset:1024 sc1
v_mfma_f32_16x16x32_bf16 a[8:11],a[132:135],v[100:103],a[8:11]
ds_read_b64_tr_b16 a[116:117],v44 offset:10432
ds_read_b64_tr_b16 a[118:119],v44 offset:15552
v_mfma_f32_16x16x32_bf16 a[8:11],a[136:139],v[104:107],a[8:11]
ds_read_b64_tr_b16 a[120:121],v44 offset:20672
v_mfma_f32_16x16x32_bf16 a[8:11],a[140:143],v[108:111],a[8:11]
s_waitcnt lgkmcnt(3)
ds_read_b64_tr_b16 a[122:123],v44 offset:25792
v_mfma_f32_16x16x32_bf16 a[0:3],a[144:147],v[132:135],a[0:3]
ds_read_b64_tr_b16 a[124:125],v44 offset:30912
v_mfma_f32_16x16x32_bf16 a[0:3],a[148:151],v[140:143],a[0:3]
ds_read_b64_tr_b16 a[126:127],v44 offset:36032
ds_read_b64_tr_b16 a[128:129],v43 offset:224
v_mfma_f32_16x16x32_bf16 a[0:3],a[152:155],v[148:151],a[0:3]
ds_read_b64_tr_b16 a[130:131],v43 offset:5344
v_mfma_f32_16x16x32_bf16 a[0:3],a[156:159],v[156:159],a[0:3]
ds_read_b64_tr_b16 a[132:133],v43 offset:10464
v_mfma_f32_16x16x32_bf16 a[4:7],a[156:159],v[188:191],a[4:7]
ds_read_b64_tr_b16 a[134:135],v43 offset:15584
v_mfma_f32_16x16x32_bf16 a[4:7],a[152:155],v[180:183],a[4:7]
ds_read_b64_tr_b16 a[136:137],v43 offset:20704
ds_read_b64_tr_b16 a[138:139],v43 offset:25824
v_mfma_f32_16x16x32_bf16 a[4:7],a[148:151],v[172:175],a[4:7]
ds_read_b64_tr_b16 a[140:141],v43 offset:30944
v_mfma_f32_16x16x32_bf16 a[4:7],a[144:147],v[164:167],a[4:7]
ds_read_b64_tr_b16 a[142:143],v43 offset:36064
v_mfma_f32_16x16x32_bf16 a[8:11],a[144:147],v[196:199],a[8:11]
ds_read_b64_tr_b16 a[144:145],v45 offset:224
v_mfma_f32_16x16x32_bf16 a[8:11],a[148:151],v[204:207],a[8:11]
v_accvgpr_read_b32 v192,a0
v_accvgpr_read_b32 v193,a1
v_mfma_f32_16x16x32_bf16 a[8:11],a[152:155],v[212:215],a[8:11]
v_cvt_pk_bf16_f32 v192,v192,v193
v_accvgpr_read_b32 v194,a2
v_mfma_f32_16x16x32_bf16 a[8:11],a[156:159],v[220:223],a[8:11]
v_accvgpr_read_b32 v195,a3
v_cvt_pk_bf16_f32 v193,v194,v195
v_mfma_f32_16x16x32_bf16 a[16:19],a[160:163],v[64:67],a[16:19]
ds_read_b64_tr_b16 a[146:147],v45 offset:5344
ds_read_b64_tr_b16 a[148:149],v45 offset:10464
v_mfma_f32_16x16x32_bf16 a[16:19],a[164:167],v[68:71],a[16:19]
v_accvgpr_read_b32 v200,a4
v_accvgpr_read_b32 v201,a5
v_mfma_f32_16x16x32_bf16 a[16:19],a[168:171],v[72:75],a[16:19]
v_cvt_pk_bf16_f32 v200,v200,v201
v_accvgpr_read_b32 v202,a6
v_mfma_f32_16x16x32_bf16 a[16:19],a[172:175],v[76:79],a[16:19]
v_accvgpr_read_b32 v203,a7
v_cvt_pk_bf16_f32 v201,v202,v203
v_mfma_f32_16x16x32_bf16 a[20:23],a[172:175],v[92:95],a[20:23]
ds_read_b64_tr_b16 a[150:151],v45 offset:15584
ds_read_b64_tr_b16 a[152:153],v45 offset:20704
v_mfma_f32_16x16x32_bf16 a[20:23],a[168:171],v[88:91],a[20:23]
ds_read_b64_tr_b16 a[154:155],v45 offset:25824
v_accvgpr_read_b32 v208,a8
v_mfma_f32_16x16x32_bf16 a[20:23],a[164:167],v[84:87],a[20:23]
ds_read_b64_tr_b16 a[156:157],v45 offset:30944
v_accvgpr_read_b32 v209,a9
v_mfma_f32_16x16x32_bf16 a[20:23],a[160:163],v[80:83],a[20:23]
ds_read_b64_tr_b16 a[158:159],v45 offset:36064
v_cvt_pk_bf16_f32 v208,v208,v209
v_mfma_f32_16x16x32_bf16 a[24:27],a[160:163],v[96:99],a[24:27]
v_accvgpr_read_b32 v210,a10
v_accvgpr_read_b32 v211,a11
v_mfma_f32_16x16x32_bf16 a[24:27],a[164:167],v[100:103],a[24:27]
ds_read_b64_tr_b16 a[160:161],v42 offset:256
v_cvt_pk_bf16_f32 v209,v210,v211
v_mfma_f32_16x16x32_bf16 a[24:27],a[168:171],v[104:107],a[24:27]
ds_read_b64_tr_b16 a[162:163],v42 offset:5376
ds_read_b64_tr_b16 a[164:165],v42 offset:10496
v_mfma_f32_16x16x32_bf16 a[24:27],a[172:175],v[108:111],a[24:27]
ds_read_b64_tr_b16 a[166:167],v42 offset:15616
v_mfma_f32_16x16x32_bf16 a[16:19],a[176:179],v[132:135],a[16:19]
ds_read_b64_tr_b16 a[168:169],v42 offset:20736
v_mfma_f32_16x16x32_bf16 a[16:19],a[180:183],v[140:143],a[16:19]
ds_read_b64_tr_b16 a[170:171],v42 offset:25856
v_mfma_f32_16x16x32_bf16 a[16:19],a[184:187],v[148:151],a[16:19]
ds_read_b64_tr_b16 a[172:173],v42 offset:30976
ds_read_b64_tr_b16 a[174:175],v42 offset:36096
v_mfma_f32_16x16x32_bf16 a[16:19],a[188:191],v[156:159],a[16:19]
v_mfma_f32_16x16x32_bf16 a[20:23],a[188:191],v[188:191],a[20:23]
v_mfma_f32_16x16x32_bf16 a[20:23],a[184:187],v[180:183],a[20:23]
v_mfma_f32_16x16x32_bf16 a[20:23],a[180:183],v[172:175],a[20:23]
v_mfma_f32_16x16x32_bf16 a[20:23],a[176:179],v[164:167],a[20:23]
v_mfma_f32_16x16x32_bf16 a[24:27],a[176:179],v[196:199],a[24:27]
ds_read_b64_tr_b16 a[176:177],v44 offset:256
ds_read_b64_tr_b16 a[178:179],v44 offset:5376
v_mfma_f32_16x16x32_bf16 a[24:27],a[180:183],v[204:207],a[24:27]
v_accvgpr_read_b32 v194,a16
v_accvgpr_read_b32 v195,a17
v_mfma_f32_16x16x32_bf16 a[24:27],a[184:187],v[212:215],a[24:27]
v_accvgpr_read_b32 v14,a18
v_accvgpr_read_b32 v15,a19
v_mfma_f32_16x16x32_bf16 a[24:27],a[188:191],v[220:223],a[24:27]
v_cvt_pk_bf16_f32 v194,v194,v195
v_cvt_pk_bf16_f32 v195,v14,v15
v_mfma_f32_16x16x32_bf16 a[32:35],a[96:99],v[64:67],a[32:35]
buffer_store_dwordx4 v[192:195],v6,s[32:35],s84 offen offset:2048 sc1
v_mfma_f32_16x16x32_bf16 a[32:35],a[100:103],v[68:71],a[32:35]
v_accvgpr_read_b32 v202,a20
v_accvgpr_read_b32 v203,a21
v_mfma_f32_16x16x32_bf16 a[32:35],a[104:107],v[72:75],a[32:35]
v_accvgpr_read_b32 v16,a22
v_accvgpr_read_b32 v17,a23
v_mfma_f32_16x16x32_bf16 a[32:35],a[108:111],v[76:79],a[32:35]
v_cvt_pk_bf16_f32 v202,v202,v203
v_cvt_pk_bf16_f32 v203,v16,v17
v_mfma_f32_16x16x32_bf16 a[36:39],a[108:111],v[92:95],a[36:39]
buffer_store_dwordx4 v[200:203],v6,s[32:35],s85 offen offset:2048 sc1
v_mfma_f32_16x16x32_bf16 a[36:39],a[104:107],v[88:91],a[36:39]
v_accvgpr_read_b32 v210,a24
v_accvgpr_read_b32 v211,a25
v_mfma_f32_16x16x32_bf16 a[36:39],a[100:103],v[84:87],a[36:39]
v_accvgpr_read_b32 v12,a26
v_accvgpr_read_b32 v13,a27
v_mfma_f32_16x16x32_bf16 a[36:39],a[96:99],v[80:83],a[36:39]
v_cvt_pk_bf16_f32 v210,v210,v211
v_cvt_pk_bf16_f32 v211,v12,v13
v_mfma_f32_16x16x32_bf16 a[40:43],a[96:99],v[96:99],a[40:43]
buffer_store_dwordx4 v[208:211],v6,s[32:35],s86 offen offset:2048 sc1
v_mfma_f32_16x16x32_bf16 a[40:43],a[100:103],v[100:103],a[40:43]
ds_read_b64_tr_b16 a[180:181],v44 offset:10496
ds_read_b64_tr_b16 a[182:183],v44 offset:15616
v_mfma_f32_16x16x32_bf16 a[40:43],a[104:107],v[104:107],a[40:43]
ds_read_b64_tr_b16 a[184:185],v44 offset:20736
v_mfma_f32_16x16x32_bf16 a[40:43],a[108:111],v[108:111],a[40:43]
ds_read_b64_tr_b16 a[186:187],v44 offset:25856
v_mfma_f32_16x16x32_bf16 a[32:35],a[112:115],v[132:135],a[32:35]
s_waitcnt lgkmcnt(4)
ds_read_b64_tr_b16 a[188:189],v44 offset:30976
v_mfma_f32_16x16x32_bf16 a[32:35],a[116:119],v[140:143],a[32:35]
ds_read_b64_tr_b16 a[190:191],v44 offset:36096
ds_read_b64_tr_b16 a[96:97],v43 offset:288
v_mfma_f32_16x16x32_bf16 a[32:35],a[120:123],v[148:151],a[32:35]
ds_read_b64_tr_b16 a[98:99],v43 offset:5408
v_mfma_f32_16x16x32_bf16 a[32:35],a[124:127],v[156:159],a[32:35]
ds_read_b64_tr_b16 a[100:101],v43 offset:10528
v_mfma_f32_16x16x32_bf16 a[36:39],a[124:127],v[188:191],a[36:39]
ds_read_b64_tr_b16 a[102:103],v43 offset:15648
v_mfma_f32_16x16x32_bf16 a[36:39],a[120:123],v[180:183],a[36:39]
ds_read_b64_tr_b16 a[104:105],v43 offset:20768
ds_read_b64_tr_b16 a[106:107],v43 offset:25888
v_mfma_f32_16x16x32_bf16 a[36:39],a[116:119],v[172:175],a[36:39]
ds_read_b64_tr_b16 a[108:109],v43 offset:31008
v_mfma_f32_16x16x32_bf16 a[36:39],a[112:115],v[164:167],a[36:39]
ds_read_b64_tr_b16 a[110:111],v43 offset:36128
v_mfma_f32_16x16x32_bf16 a[40:43],a[112:115],v[196:199],a[40:43]
ds_read_b64_tr_b16 a[112:113],v45 offset:288
v_mfma_f32_16x16x32_bf16 a[40:43],a[116:119],v[204:207],a[40:43]
v_accvgpr_read_b32 v224,a32
v_accvgpr_read_b32 v225,a33
v_mfma_f32_16x16x32_bf16 a[40:43],a[120:123],v[212:215],a[40:43]
v_cvt_pk_bf16_f32 v224,v224,v225
v_accvgpr_read_b32 v226,a34
v_mfma_f32_16x16x32_bf16 a[40:43],a[124:127],v[220:223],a[40:43]
v_accvgpr_read_b32 v227,a35
v_cvt_pk_bf16_f32 v225,v226,v227
v_mfma_f32_16x16x32_bf16 a[48:51],a[128:131],v[64:67],a[48:51]
ds_read_b64_tr_b16 a[114:115],v45 offset:5408
ds_read_b64_tr_b16 a[116:117],v45 offset:10528
v_mfma_f32_16x16x32_bf16 a[48:51],a[132:135],v[68:71],a[48:51]
v_accvgpr_read_b32 v232,a36
v_accvgpr_read_b32 v233,a37
v_mfma_f32_16x16x32_bf16 a[48:51],a[136:139],v[72:75],a[48:51]
v_cvt_pk_bf16_f32 v232,v232,v233
v_accvgpr_read_b32 v234,a38
v_mfma_f32_16x16x32_bf16 a[48:51],a[140:143],v[76:79],a[48:51]
v_accvgpr_read_b32 v235,a39
v_cvt_pk_bf16_f32 v233,v234,v235
v_mfma_f32_16x16x32_bf16 a[52:55],a[140:143],v[92:95],a[52:55]
ds_read_b64_tr_b16 a[118:119],v45 offset:15648
ds_read_b64_tr_b16 a[120:121],v45 offset:20768
v_mfma_f32_16x16x32_bf16 a[52:55],a[136:139],v[88:91],a[52:55]
v_accvgpr_read_b32 v240,a40
v_accvgpr_read_b32 v241,a41
v_mfma_f32_16x16x32_bf16 a[52:55],a[132:135],v[84:87],a[52:55]
v_cvt_pk_bf16_f32 v240,v240,v241
v_accvgpr_read_b32 v242,a42
v_mfma_f32_16x16x32_bf16 a[52:55],a[128:131],v[80:83],a[52:55]
v_accvgpr_read_b32 v243,a43
v_cvt_pk_bf16_f32 v241,v242,v243
v_mfma_f32_16x16x32_bf16 a[56:59],a[128:131],v[96:99],a[56:59]
ds_read_b64_tr_b16 a[122:123],v45 offset:25888
ds_read_b64_tr_b16 a[124:125],v45 offset:31008
v_mfma_f32_16x16x32_bf16 a[56:59],a[132:135],v[100:103],a[56:59]
ds_read_b64_tr_b16 a[126:127],v45 offset:36128
v_mfma_f32_16x16x32_bf16 a[56:59],a[136:139],v[104:107],a[56:59]
ds_read_b64_tr_b16 a[128:129],v1
v_mfma_f32_16x16x32_bf16 a[56:59],a[140:143],v[108:111],a[56:59]
ds_read_b64_tr_b16 a[130:131],v1 offset:5120
v_mfma_f32_16x16x32_bf16 a[48:51],a[144:147],v[132:135],a[48:51]
ds_read_b64_tr_b16 a[132:133],v1 offset:10240
ds_read_b64_tr_b16 a[134:135],v1 offset:15360
v_mfma_f32_16x16x32_bf16 a[48:51],a[148:151],v[140:143],a[48:51]
ds_read_b64_tr_b16 a[136:137],v1 offset:20480
v_mfma_f32_16x16x32_bf16 a[48:51],a[152:155],v[148:151],a[48:51]
ds_read_b64_tr_b16 a[138:139],v1 offset:25600
v_mfma_f32_16x16x32_bf16 a[48:51],a[156:159],v[156:159],a[48:51]
ds_read_b64_tr_b16 a[140:141],v1 offset:30720
v_mfma_f32_16x16x32_bf16 a[52:55],a[156:159],v[188:191],a[52:55]
ds_read_b64_tr_b16 a[142:143],v1 offset:35840
v_mfma_f32_16x16x32_bf16 a[52:55],a[152:155],v[180:183],a[52:55]
v_mfma_f32_16x16x32_bf16 a[52:55],a[148:151],v[172:175],a[52:55]
v_mfma_f32_16x16x32_bf16 a[52:55],a[144:147],v[164:167],a[52:55]
v_mfma_f32_16x16x32_bf16 a[56:59],a[144:147],v[196:199],a[56:59]
ds_read_b64_tr_b16 a[144:145],v42
)A"
R"A(ds_read_b64_tr_b16 a[146:147],v42 offset:5120
v_mfma_f32_16x16x32_bf16 a[56:59],a[148:151],v[204:207],a[56:59]
v_accvgpr_read_b32 v226,a48
v_accvgpr_read_b32 v227,a49
v_mfma_f32_16x16x32_bf16 a[56:59],a[152:155],v[212:215],a[56:59]
v_accvgpr_read_b32 v14,a50
v_accvgpr_read_b32 v15,a51
v_mfma_f32_16x16x32_bf16 a[56:59],a[156:159],v[220:223],a[56:59]
v_cvt_pk_bf16_f32 v226,v226,v227
v_cvt_pk_bf16_f32 v227,v14,v15
v_mfma_f32_16x16x32_bf16 a[64:67],a[160:163],v[64:67],a[64:67]
buffer_store_dwordx4 v[224:227],v6,s[32:35],s84 offen offset:3072 sc1
v_mfma_f32_16x16x32_bf16 a[64:67],a[164:167],v[68:71],a[64:67]
v_accvgpr_read_b32 v234,a52
v_accvgpr_read_b32 v235,a53
v_mfma_f32_16x16x32_bf16 a[64:67],a[168:171],v[72:75],a[64:67]
v_accvgpr_read_b32 v16,a54
v_accvgpr_read_b32 v17,a55
v_mfma_f32_16x16x32_bf16 a[64:67],a[172:175],v[76:79],a[64:67]
v_cvt_pk_bf16_f32 v234,v234,v235
v_cvt_pk_bf16_f32 v235,v16,v17
v_mfma_f32_16x16x32_bf16 a[68:71],a[172:175],v[92:95],a[68:71]
buffer_store_dwordx4 v[232:235],v6,s[32:35],s85 offen offset:3072 sc1
v_mfma_f32_16x16x32_bf16 a[68:71],a[168:171],v[88:91],a[68:71]
v_accvgpr_read_b32 v242,a56
v_accvgpr_read_b32 v243,a57
v_mfma_f32_16x16x32_bf16 a[68:71],a[164:167],v[84:87],a[68:71]
v_accvgpr_read_b32 v12,a58
v_accvgpr_read_b32 v13,a59
v_mfma_f32_16x16x32_bf16 a[68:71],a[160:163],v[80:83],a[68:71]
v_cvt_pk_bf16_f32 v242,v242,v243
v_cvt_pk_bf16_f32 v243,v12,v13
v_mfma_f32_16x16x32_bf16 a[72:75],a[160:163],v[96:99],a[72:75]
buffer_store_dwordx4 v[240:243],v6,s[32:35],s86 offen offset:3072 sc1
v_mfma_f32_16x16x32_bf16 a[72:75],a[164:167],v[100:103],a[72:75]
ds_read_b64_tr_b16 a[148:149],v42 offset:10240
ds_read_b64_tr_b16 a[150:151],v42 offset:15360
v_mfma_f32_16x16x32_bf16 a[72:75],a[168:171],v[104:107],a[72:75]
ds_read_b64_tr_b16 a[152:153],v42 offset:20480
v_mfma_f32_16x16x32_bf16 a[72:75],a[172:175],v[108:111],a[72:75]
ds_read_b64_tr_b16 a[154:155],v42 offset:25600
v_mfma_f32_16x16x32_bf16 a[64:67],a[176:179],v[132:135],a[64:67]
s_waitcnt lgkmcnt(4)
ds_read_b64_tr_b16 a[156:157],v42 offset:30720
v_mfma_f32_16x16x32_bf16 a[64:67],a[180:183],v[140:143],a[64:67]
ds_read_b64_tr_b16 a[158:159],v42 offset:35840
ds_read_b64_tr_b16 a[160:161],v44
v_mfma_f32_16x16x32_bf16 a[64:67],a[184:187],v[148:151],a[64:67]
ds_read_b64_tr_b16 a[162:163],v44 offset:5120
v_mfma_f32_16x16x32_bf16 a[64:67],a[188:191],v[156:159],a[64:67]
ds_read_b64_tr_b16 a[164:165],v44 offset:10240
v_mfma_f32_16x16x32_bf16 a[68:71],a[188:191],v[188:191],a[68:71]
ds_read_b64_tr_b16 a[166:167],v44 offset:15360
v_mfma_f32_16x16x32_bf16 a[68:71],a[184:187],v[180:183],a[68:71]
ds_read_b64_tr_b16 a[168:169],v44 offset:20480
ds_read_b64_tr_b16 a[170:171],v44 offset:25600
v_mfma_f32_16x16x32_bf16 a[68:71],a[180:183],v[172:175],a[68:71]
ds_read_b64_tr_b16 a[172:173],v44 offset:30720
v_mfma_f32_16x16x32_bf16 a[68:71],a[176:179],v[164:167],a[68:71]
ds_read_b64_tr_b16 a[174:175],v44 offset:35840
v_mfma_f32_16x16x32_bf16 a[72:75],a[176:179],v[196:199],a[72:75]
ds_read_b64_tr_b16 a[176:177],v2 offset:32
v_mfma_f32_16x16x32_bf16 a[72:75],a[180:183],v[204:207],a[72:75]
v_accvgpr_read_b32 v128,a64
v_accvgpr_read_b32 v129,a65
v_mfma_f32_16x16x32_bf16 a[72:75],a[184:187],v[212:215],a[72:75]
v_cvt_pk_bf16_f32 v128,v128,v129
v_accvgpr_read_b32 v130,a66
v_mfma_f32_16x16x32_bf16 a[72:75],a[188:191],v[220:223],a[72:75]
v_accvgpr_read_b32 v131,a67
v_cvt_pk_bf16_f32 v129,v130,v131
v_mfma_f32_16x16x32_bf16 a[80:83],a[96:99],v[64:67],a[80:83]
ds_read_b64_tr_b16 a[178:179],v2 offset:5152
ds_read_b64_tr_b16 a[180:181],v2 offset:10272
v_mfma_f32_16x16x32_bf16 a[80:83],a[100:103],v[68:71],a[80:83]
v_accvgpr_read_b32 v136,a68
v_accvgpr_read_b32 v137,a69
v_mfma_f32_16x16x32_bf16 a[80:83],a[104:107],v[72:75],a[80:83]
v_cvt_pk_bf16_f32 v136,v136,v137
v_accvgpr_read_b32 v138,a70
v_mfma_f32_16x16x32_bf16 a[80:83],a[108:111],v[76:79],a[80:83]
ds_read_b64_tr_b16 a[182:183],v2 offset:15392
v_accvgpr_read_b32 v139,a71
v_mfma_f32_16x16x32_bf16 a[84:87],a[108:111],v[92:95],a[84:87]
ds_read_b64_tr_b16 a[184:185],v2 offset:20512
v_cvt_pk_bf16_f32 v137,v138,v139
v_mfma_f32_16x16x32_bf16 a[84:87],a[104:107],v[88:91],a[84:87]
v_accvgpr_read_b32 v144,a72
v_accvgpr_read_b32 v145,a73
v_mfma_f32_16x16x32_bf16 a[84:87],a[100:103],v[84:87],a[84:87]
v_cvt_pk_bf16_f32 v144,v144,v145
v_accvgpr_read_b32 v146,a74
v_mfma_f32_16x16x32_bf16 a[84:87],a[96:99],v[80:83],a[84:87]
v_accvgpr_read_b32 v147,a75
v_cvt_pk_bf16_f32 v145,v146,v147
v_mfma_f32_16x16x32_bf16 a[88:91],a[96:99],v[96:99],a[88:91]
ds_read_b64_tr_b16 a[186:187],v2 offset:25632
ds_read_b64_tr_b16 a[188:189],v2 offset:30752
v_mfma_f32_16x16x32_bf16 a[88:91],a[100:103],v[100:103],a[88:91]
ds_read_b64_tr_b16 a[190:191],v2 offset:35872
v_mfma_f32_16x16x32_bf16 a[88:91],a[104:107],v[104:107],a[88:91]
ds_read_b64_tr_b16 a[96:97],v43 offset:32
v_mfma_f32_16x16x32_bf16 a[88:91],a[108:111],v[108:111],a[88:91]
ds_read_b64_tr_b16 a[98:99],v43 offset:5152
v_mfma_f32_16x16x32_bf16 a[80:83],a[112:115],v[132:135],a[80:83]
ds_read_b64_tr_b16 a[100:101],v43 offset:10272
ds_read_b64_tr_b16 a[102:103],v43 offset:15392
v_mfma_f32_16x16x32_bf16 a[80:83],a[116:119],v[140:143],a[80:83]
ds_read_b64_tr_b16 a[104:105],v43 offset:20512
v_mfma_f32_16x16x32_bf16 a[80:83],a[120:123],v[148:151],a[80:83]
ds_read_b64_tr_b16 a[106:107],v43 offset:25632
v_mfma_f32_16x16x32_bf16 a[80:83],a[124:127],v[156:159],a[80:83]
ds_read_b64_tr_b16 a[108:109],v43 offset:30752
v_mfma_f32_16x16x32_bf16 a[84:87],a[124:127],v[188:191],a[84:87]
ds_read_b64_tr_b16 a[110:111],v43 offset:35872
v_mfma_f32_16x16x32_bf16 a[84:87],a[120:123],v[180:183],a[84:87]
v_mfma_f32_16x16x32_bf16 a[84:87],a[116:119],v[172:175],a[84:87]
v_mfma_f32_16x16x32_bf16 a[84:87],a[112:115],v[164:167],a[84:87]
v_mfma_f32_16x16x32_bf16 a[88:91],a[112:115],v[196:199],a[88:91]
ds_read_b64_tr_b16 a[112:113],v45 offset:32
ds_read_b64_tr_b16 a[114:115],v45 offset:5152
v_mfma_f32_16x16x32_bf16 a[88:91],a[116:119],v[204:207],a[88:91]
v_accvgpr_read_b32 v130,a80
v_accvgpr_read_b32 v131,a81
v_mfma_f32_16x16x32_bf16 a[88:91],a[120:123],v[212:215],a[88:91]
v_accvgpr_read_b32 v14,a82
v_accvgpr_read_b32 v15,a83
v_mfma_f32_16x16x32_bf16 a[88:91],a[124:127],v[220:223],a[88:91]
v_cvt_pk_bf16_f32 v130,v130,v131
v_cvt_pk_bf16_f32 v131,v14,v15
v_mfma_f32_16x16x32_bf16 a[204:207],a[128:131],v[152:155],a[204:207]
s_lshl_b32 s94,s71,2
buffer_store_dwordx4 v[128:131],v25,s[32:35],s84 offen sc1
v_mfma_f32_16x16x32_bf16 a[204:207],a[132:135],v[184:187],a[204:207]
s_add_u32 s94,s94,s40
v_accvgpr_read_b32 v138,a84
v_accvgpr_read_b32 v139,a85
v_mfma_f32_16x16x32_bf16 a[204:207],a[136:139],v[216:219],a[204:207]
s_lshl_b32 s95,s42,2
s_add_u32 s95,s95,s40
s_lshl_b32 s95,s95,4
v_mfma_f32_16x16x32_bf16 a[204:207],a[140:143],v[248:251],a[204:207]
s_lshl_b32 s94,s94,4
v_mov_b32 v18,s68
v_mov_b32 v19,s69
v_mfma_f32_16x16x32_bf16 a[204:207],a[144:147],v[112:115],a[204:207]
s_waitcnt vmcnt(1)
s_mov_b64 exec,1
buffer_store_dwordx2 v[18:19],v6,s[64:67],s95 offen offset:2048 sc1
s_mov_b64 exec,-1
s_waitcnt lgkmcnt(0)
v_mfma_f32_16x16x32_bf16 a[204:207],a[148:151],v[116:119],a[204:207]
v_accvgpr_read_b32 v146,a88
v_accvgpr_read_b32 v147,a89
v_mfma_f32_16x16x32_bf16 a[204:207],a[152:155],v[120:123],a[204:207]
v_accvgpr_read_b32 v16,a86
v_accvgpr_read_b32 v17,a87
v_cvt_pk_bf16_f32 v138,v138,v139
v_accvgpr_read_b32 v12,a90
v_accvgpr_read_b32 v13,a91
v_cvt_pk_bf16_f32 v146,v146,v147
v_cvt_pk_bf16_f32 v139,v16,v17
v_cvt_pk_bf16_f32 v147,v12,v13
v_mfma_f32_16x16x32_bf16 a[204:207],a[156:159],v[124:127],a[204:207]
ds_read_b64_tr_b16 a[116:117],v45 offset:10272
ds_read_b64_tr_b16 a[118:119],v45 offset:15392
v_mfma_f32_16x16x32_bf16 a[204:207],a[160:163],v[228:231],a[204:207]
buffer_store_dwordx4 v[136:139],v25,s[32:35],s85 offen sc1
v_mfma_f32_16x16x32_bf16 a[204:207],a[164:167],v[236:239],a[204:207]
ds_read_b64_tr_b16 a[120:121],v45 offset:20512
ds_read_b64_tr_b16 a[122:123],v45 offset:25632
v_mfma_f32_16x16x32_bf16 a[204:207],a[168:171],v[244:247],a[204:207]
ds_read_b64_tr_b16 a[124:125],v45 offset:30752
ds_read_b64_tr_b16 a[126:127],v45 offset:35872
v_mfma_f32_16x16x32_bf16 a[204:207],a[172:175],v[252:255],a[204:207]
ds_read_b64_tr_b16 a[128:129],v1 offset:64
ds_read_b64_tr_b16 a[130:131],v1 offset:5184
v_mfma_f32_16x16x32_bf16 a[220:223],a[176:179],v[152:155],a[220:223]
buffer_store_dwordx4 v[144:147],v25,s[32:35],s86 offen sc1
v_mfma_f32_16x16x32_bf16 a[220:223],a[180:183],v[184:187],a[220:223]
ds_read_b64_tr_b16 a[132:133],v1 offset:10304
ds_read_b64_tr_b16 a[134:135],v1 offset:15424
v_mfma_f32_16x16x32_bf16 a[220:223],a[184:187],v[216:219],a[220:223]
ds_read_b64_tr_b16 a[136:137],v1 offset:20544
ds_read_b64_tr_b16 a[138:139],v1 offset:25664
v_mfma_f32_16x16x32_bf16 a[220:223],a[188:191],v[248:251],a[220:223]
ds_read_b64_tr_b16 a[140:141],v1 offset:30784
ds_read_b64_tr_b16 a[142:143],v1 offset:35904
v_mfma_f32_16x16x32_bf16 a[220:223],a[96:99],v[112:115],a[220:223]
ds_read_b64_tr_b16 a[144:145],v42 offset:64
ds_read_b64_tr_b16 a[146:147],v42 offset:5184
v_mfma_f32_16x16x32_bf16 a[220:223],a[100:103],v[116:119],a[220:223]
ds_read_b64_tr_b16 a[148:149],v42 offset:10304
ds_read_b64_tr_b16 a[150:151],v42 offset:15424
v_mfma_f32_16x16x32_bf16 a[220:223],a[104:107],v[120:123],a[220:223]
ds_read_b64_tr_b16 a[152:153],v42 offset:20544
ds_read_b64_tr_b16 a[154:155],v42 offset:25664
v_mfma_f32_16x16x32_bf16 a[220:223],a[108:111],v[124:127],a[220:223]
ds_read_b64_tr_b16 a[156:157],v42 offset:30784
ds_read_b64_tr_b16 a[158:159],v42 offset:35904
v_mfma_f32_16x16x32_bf16 a[220:223],a[112:115],v[228:231],a[220:223]
s_waitcnt lgkmcnt(14)
ds_read_b64_tr_b16 a[160:161],v44 offset:64
ds_read_b64_tr_b16 a[162:163],v44 offset:5184
v_mfma_f32_16x16x32_bf16 a[220:223],a[116:119],v[236:239],a[220:223]
ds_read_b64_tr_b16 a[164:165],v44 offset:10304
ds_read_b64_tr_b16 a[166:167],v44 offset:15424
v_mfma_f32_16x16x32_bf16 a[220:223],a[120:123],v[244:247],a[220:223]
ds_read_b64_tr_b16 a[168:169],v44 offset:20544
ds_read_b64_tr_b16 a[170:171],v44 offset:25664
v_mfma_f32_16x16x32_bf16 a[220:223],a[124:127],v[252:255],a[220:223]
ds_read_b64_tr_b16 a[172:173],v44 offset:30784
ds_read_b64_tr_b16 a[174:175],v44 offset:35904
v_mfma_f32_16x16x32_bf16 a[236:239],a[128:131],v[152:155],a[236:239]
s_waitcnt lgkmcnt(14)
ds_read_b64_tr_b16 a[176:177],v2 offset:96
ds_read_b64_tr_b16 a[178:179],v2 offset:5216
v_mfma_f32_16x16x32_bf16 a[236:239],a[132:135],v[184:187],a[236:239]
v_bfe_u32 v24,v6,4,2
v_min_u32 v24,2,v24
v_mfma_f32_16x16x32_bf16 a[236:239],a[136:139],v[216:219],a[236:239]
ds_read_b64_tr_b16 a[180:181],v2 offset:10336
ds_read_b64_tr_b16 a[182:183],v2 offset:15456
v_mfma_f32_16x16x32_bf16 a[236:239],a[140:143],v[248:251],a[236:239]
v_add_u32 v24,s70,v24
v_add_u32 v24,1,v24
v_mfma_f32_16x16x32_bf16 a[236:239],a[144:147],v[112:115],a[236:239]
s_waitcnt lgkmcnt(10)
ds_read_b64_tr_b16 a[184:185],v2 offset:20576
ds_read_b64_tr_b16 a[186:187],v2 offset:25696
v_mfma_f32_16x16x32_bf16 a[236:239],a[148:151],v[116:119],a[236:239]
v_and_b32 v24,3,v24
v_lshlrev_b32 v24,8,v24
v_mfma_f32_16x16x32_bf16 a[236:239],a[152:155],v[120:123],a[236:239]
ds_read_b64_tr_b16 a[188:189],v2 offset:30816
ds_read_b64_tr_b16 a[190:191],v2 offset:35936
v_mfma_f32_16x16x32_bf16 a[236:239],a[156:159],v[124:127],a[236:239]
ds_read_b64_tr_b16 a[96:97],v43 offset:96
ds_read_b64_tr_b16 a[98:99],v43 offset:5216
v_mfma_f32_16x16x32_bf16 a[236:239],a[160:163],v[228:231],a[236:239]
s_waitcnt lgkmcnt(8)
ds_read_b64_tr_b16 a[100:101],v43 offset:10336
ds_read_b64_tr_b16 a[102:103],v43 offset:15456
v_mfma_f32_16x16x32_bf16 a[236:239],a[164:167],v[236:239],a[236:239]
ds_read_b64_tr_b16 a[104:105],v43 offset:20576
ds_read_b64_tr_b16 a[106:107],v43 offset:25696
v_mfma_f32_16x16x32_bf16 a[236:239],a[168:171],v[244:247],a[236:239]
ds_read_b64_tr_b16 a[108:109],v43 offset:30816
ds_read_b64_tr_b16 a[110:111],v43 offset:35936
v_mfma_f32_16x16x32_bf16 a[236:239],a[172:175],v[252:255],a[236:239]
ds_read_b64_tr_b16 a[112:113],v45 offset:96
ds_read_b64_tr_b16 a[114:115],v45 offset:5216
v_mfma_f32_16x16x32_bf16 a[252:255],a[176:179],v[152:155],a[252:255]
)A"
R"A(s_waitcnt lgkmcnt(12)
ds_read_b64_tr_b16 a[116:117],v45 offset:10336
ds_read_b64_tr_b16 a[118:119],v45 offset:15456
v_mfma_f32_16x16x32_bf16 a[252:255],a[180:183],v[184:187],a[252:255]
ds_read_b64_tr_b16 a[120:121],v45 offset:20576
ds_read_b64_tr_b16 a[122:123],v45 offset:25696
v_mfma_f32_16x16x32_bf16 a[252:255],a[184:187],v[216:219],a[252:255]
s_waitcnt lgkmcnt(14)
ds_read_b64_tr_b16 a[124:125],v45 offset:30816
ds_read_b64_tr_b16 a[126:127],v45 offset:35936
v_mfma_f32_16x16x32_bf16 a[252:255],a[188:191],v[248:251],a[252:255]
s_waitcnt lgkmcnt(14)
ds_read_b64_tr_b16 a[128:129],v1 offset:128
ds_read_b64_tr_b16 a[130:131],v1 offset:5248
v_mfma_f32_16x16x32_bf16 a[252:255],a[96:99],v[112:115],a[252:255]
s_waitcnt lgkmcnt(14)
buffer_load_dwordx2 v[26:27],v24,s[64:67],s94 offen offset:2048 sc1
ds_read_b64_tr_b16 a[132:133],v1 offset:10368
ds_read_b64_tr_b16 a[134:135],v1 offset:15488
v_mfma_f32_16x16x32_bf16 a[252:255],a[100:103],v[116:119],a[252:255]
s_waitcnt lgkmcnt(14)
ds_read_b64_tr_b16 a[136:137],v1 offset:20608
ds_read_b64_tr_b16 a[138:139],v1 offset:25728
v_mfma_f32_16x16x32_bf16 a[252:255],a[104:107],v[120:123],a[252:255]
s_waitcnt lgkmcnt(14)
ds_read_b64_tr_b16 a[140:141],v1 offset:30848
ds_read_b64_tr_b16 a[142:143],v1 offset:35968
v_mfma_f32_16x16x32_bf16 a[252:255],a[108:111],v[124:127],a[252:255]
s_waitcnt lgkmcnt(14)
ds_read_b64_tr_b16 a[144:145],v42 offset:128
ds_read_b64_tr_b16 a[146:147],v42 offset:5248
v_mfma_f32_16x16x32_bf16 a[252:255],a[112:115],v[228:231],a[252:255]
s_waitcnt lgkmcnt(14)
ds_read_b64_tr_b16 a[148:149],v42 offset:10368
ds_read_b64_tr_b16 a[150:151],v42 offset:15488
v_mfma_f32_16x16x32_bf16 a[252:255],a[116:119],v[236:239],a[252:255]
s_lshl_b32 s95,s42,2
ds_read_b64_tr_b16 a[152:153],v42 offset:20608
ds_read_b64_tr_b16 a[154:155],v42 offset:25728
s_waitcnt lgkmcnt(15)
v_mfma_f32_16x16x32_bf16 a[252:255],a[120:123],v[244:247],a[252:255]
s_add_u32 s95,s95,s40
s_lshl_b32 s95,s95,4
ds_read_b64_tr_b16 a[156:157],v42 offset:30848
ds_read_b64_tr_b16 a[158:159],v42 offset:35968
v_mov_b32 v18,s68
v_mov_b32 v19,s69
s_waitcnt lgkmcnt(15)
v_mfma_f32_16x16x32_bf16 a[252:255],a[124:127],v[252:255],a[252:255]
s_waitcnt vmcnt(1)
s_mov_b64 exec,1
buffer_store_dwordx2 v[18:19],v6,s[64:67],s95 offen offset:3072 sc1
s_mov_b64 exec,-1
ds_read_b64_tr_b16 a[160:161],v44 offset:128
ds_read_b64_tr_b16 a[162:163],v44 offset:5248
s_waitcnt lgkmcnt(15)
v_mfma_f32_16x16x32_bf16 a[12:15],a[128:131],v[152:155],a[12:15]
s_waitcnt lgkmcnt(14)
ds_read_b64_tr_b16 a[164:165],v44 offset:10368
ds_read_b64_tr_b16 a[166:167],v44 offset:15488
v_mfma_f32_16x16x32_bf16 a[12:15],a[132:135],v[184:187],a[12:15]
s_waitcnt lgkmcnt(14)
ds_read_b64_tr_b16 a[168:169],v44 offset:20608
ds_read_b64_tr_b16 a[170:171],v44 offset:25728
v_mfma_f32_16x16x32_bf16 a[12:15],a[136:139],v[216:219],a[12:15]
s_waitcnt lgkmcnt(14)
ds_read_b64_tr_b16 a[172:173],v44 offset:30848
ds_read_b64_tr_b16 a[174:175],v44 offset:35968
v_mfma_f32_16x16x32_bf16 a[12:15],a[140:143],v[248:251],a[12:15]
s_waitcnt lgkmcnt(14)
ds_read_b64_tr_b16 a[176:177],v2 offset:160
ds_read_b64_tr_b16 a[178:179],v2 offset:5280
v_mfma_f32_16x16x32_bf16 a[12:15],a[144:147],v[112:115],a[12:15]
s_waitcnt lgkmcnt(14)
ds_read_b64_tr_b16 a[180:181],v2 offset:10400
ds_read_b64_tr_b16 a[182:183],v2 offset:15520
v_mfma_f32_16x16x32_bf16 a[12:15],a[148:151],v[116:119],a[12:15]
s_waitcnt lgkmcnt(14)
ds_read_b64_tr_b16 a[184:185],v2 offset:20640
ds_read_b64_tr_b16 a[186:187],v2 offset:25760
v_mfma_f32_16x16x32_bf16 a[12:15],a[152:155],v[120:123],a[12:15]
s_waitcnt lgkmcnt(14)
ds_read_b64_tr_b16 a[188:189],v2 offset:30880
ds_read_b64_tr_b16 a[190:191],v2 offset:36000
v_mfma_f32_16x16x32_bf16 a[12:15],a[156:159],v[124:127],a[12:15]
s_waitcnt lgkmcnt(14)
ds_read_b64_tr_b16 a[96:97],v43 offset:160
ds_read_b64_tr_b16 a[98:99],v43 offset:5280
v_mfma_f32_16x16x32_bf16 a[12:15],a[160:163],v[228:231],a[12:15]
s_waitcnt lgkmcnt(14)
ds_read_b64_tr_b16 a[100:101],v43 offset:10400
ds_read_b64_tr_b16 a[102:103],v43 offset:15520
v_mfma_f32_16x16x32_bf16 a[12:15],a[164:167],v[236:239],a[12:15]
s_waitcnt lgkmcnt(14)
ds_read_b64_tr_b16 a[104:105],v43 offset:20640
ds_read_b64_tr_b16 a[106:107],v43 offset:25760
v_mfma_f32_16x16x32_bf16 a[12:15],a[168:171],v[244:247],a[12:15]
s_waitcnt lgkmcnt(14)
ds_read_b64_tr_b16 a[108:109],v43 offset:30880
ds_read_b64_tr_b16 a[110:111],v43 offset:36000
v_mfma_f32_16x16x32_bf16 a[12:15],a[172:175],v[252:255],a[12:15]
s_waitcnt lgkmcnt(14)
ds_read_b64_tr_b16 a[112:113],v45 offset:160
ds_read_b64_tr_b16 a[114:115],v45 offset:5280
v_mfma_f32_16x16x32_bf16 a[28:31],a[176:179],v[152:155],a[28:31]
s_waitcnt lgkmcnt(14)
ds_read_b64_tr_b16 a[116:117],v45 offset:10400
ds_read_b64_tr_b16 a[118:119],v45 offset:15520
v_mfma_f32_16x16x32_bf16 a[28:31],a[180:183],v[184:187],a[28:31]
s_waitcnt lgkmcnt(14)
ds_read_b64_tr_b16 a[120:121],v45 offset:20640
ds_read_b64_tr_b16 a[122:123],v45 offset:25760
v_mfma_f32_16x16x32_bf16 a[28:31],a[184:187],v[216:219],a[28:31]
s_waitcnt lgkmcnt(14)
ds_read_b64_tr_b16 a[124:125],v45 offset:30880
ds_read_b64_tr_b16 a[126:127],v45 offset:36000
v_mfma_f32_16x16x32_bf16 a[28:31],a[188:191],v[248:251],a[28:31]
s_waitcnt lgkmcnt(14)
ds_read_b64_tr_b16 a[128:129],v1 offset:192
ds_read_b64_tr_b16 a[130:131],v1 offset:5312
v_mfma_f32_16x16x32_bf16 a[28:31],a[96:99],v[112:115],a[28:31]
s_waitcnt lgkmcnt(14)
ds_read_b64_tr_b16 a[132:133],v1 offset:10432
ds_read_b64_tr_b16 a[134:135],v1 offset:15552
v_mfma_f32_16x16x32_bf16 a[28:31],a[100:103],v[116:119],a[28:31]
s_waitcnt lgkmcnt(14)
ds_read_b64_tr_b16 a[136:137],v1 offset:20672
ds_read_b64_tr_b16 a[138:139],v1 offset:25792
v_mfma_f32_16x16x32_bf16 a[28:31],a[104:107],v[120:123],a[28:31]
s_waitcnt lgkmcnt(14)
ds_read_b64_tr_b16 a[140:141],v1 offset:30912
ds_read_b64_tr_b16 a[142:143],v1 offset:36032
v_mfma_f32_16x16x32_bf16 a[28:31],a[108:111],v[124:127],a[28:31]
s_waitcnt lgkmcnt(14)
ds_read_b64_tr_b16 a[144:145],v42 offset:192
ds_read_b64_tr_b16 a[146:147],v42 offset:5312
v_mfma_f32_16x16x32_bf16 a[28:31],a[112:115],v[228:231],a[28:31]
s_waitcnt lgkmcnt(14)
ds_read_b64_tr_b16 a[148:149],v42 offset:10432
ds_read_b64_tr_b16 a[150:151],v42 offset:15552
v_mfma_f32_16x16x32_bf16 a[28:31],a[116:119],v[236:239],a[28:31]
s_waitcnt lgkmcnt(14)
ds_read_b64_tr_b16 a[152:153],v42 offset:20672
ds_read_b64_tr_b16 a[154:155],v42 offset:25792
v_mfma_f32_16x16x32_bf16 a[28:31],a[120:123],v[244:247],a[28:31]
s_waitcnt lgkmcnt(14)
ds_read_b64_tr_b16 a[156:157],v42 offset:30912
ds_read_b64_tr_b16 a[158:159],v42 offset:36032
v_mfma_f32_16x16x32_bf16 a[28:31],a[124:127],v[252:255],a[28:31]
s_waitcnt lgkmcnt(14)
ds_read_b64_tr_b16 a[160:161],v44 offset:192
ds_read_b64_tr_b16 a[162:163],v44 offset:5312
v_mfma_f32_16x16x32_bf16 a[44:47],a[128:131],v[152:155],a[44:47]
ds_read_b64_tr_b16 a[164:165],v44 offset:10432
ds_read_b64_tr_b16 a[166:167],v44 offset:15552
s_waitcnt vmcnt(1)
v_cmp_ne_u32 s[90:91],s68,v26
v_cmp_ne_u32 s[92:93],s69,v27
s_nop 0
s_or_b64 s[90:91],s[90:91],s[92:93]
s_cbranch_scc1 L_fr10_%=
L_fo10_%=:
s_waitcnt lgkmcnt(15)
v_mfma_f32_16x16x32_bf16 a[44:47],a[132:135],v[184:187],a[44:47]
s_waitcnt lgkmcnt(14)
buffer_load_dwordx4 v[64:67],v6,s[32:35],s76 offen sc1
ds_read_b64_tr_b16 a[168:169],v44 offset:20672
ds_read_b64_tr_b16 a[170:171],v44 offset:25792
v_mfma_f32_16x16x32_bf16 a[44:47],a[136:139],v[216:219],a[44:47]
s_lshl_b32 s94,s71,2
ds_read_b64_tr_b16 a[172:173],v44 offset:30912
ds_read_b64_tr_b16 a[174:175],v44 offset:36032
s_waitcnt lgkmcnt(15)
v_mfma_f32_16x16x32_bf16 a[44:47],a[140:143],v[248:251],a[44:47]
s_add_u32 s94,s94,s40
ds_read_b64_tr_b16 a[176:177],v2 offset:224
ds_read_b64_tr_b16 a[178:179],v2 offset:5344
s_waitcnt lgkmcnt(15)
v_mfma_f32_16x16x32_bf16 a[44:47],a[144:147],v[112:115],a[44:47]
s_lshl_b32 s94,s94,4
ds_read_b64_tr_b16 a[180:181],v2 offset:10464
ds_read_b64_tr_b16 a[182:183],v2 offset:15584
s_waitcnt lgkmcnt(15)
v_mfma_f32_16x16x32_bf16 a[44:47],a[148:151],v[116:119],a[44:47]
s_waitcnt lgkmcnt(14)
buffer_load_dwordx4 v[80:83],v6,s[32:35],s77 offen sc1
ds_read_b64_tr_b16 a[184:185],v2 offset:20704
ds_read_b64_tr_b16 a[186:187],v2 offset:25824
v_mfma_f32_16x16x32_bf16 a[44:47],a[152:155],v[120:123],a[44:47]
s_waitcnt lgkmcnt(14)
ds_read_b64_tr_b16 a[188:189],v2 offset:30944
ds_read_b64_tr_b16 a[190:191],v2 offset:36064
v_mfma_f32_16x16x32_bf16 a[44:47],a[156:159],v[124:127],a[44:47]
s_waitcnt lgkmcnt(14)
ds_read_b64_tr_b16 a[96:97],v43 offset:224
ds_read_b64_tr_b16 a[98:99],v43 offset:5344
v_mfma_f32_16x16x32_bf16 a[44:47],a[160:163],v[228:231],a[44:47]
s_waitcnt lgkmcnt(14)
ds_read_b64_tr_b16 a[100:101],v43 offset:10464
ds_read_b64_tr_b16 a[102:103],v43 offset:15584
v_mfma_f32_16x16x32_bf16 a[44:47],a[164:167],v[236:239],a[44:47]
s_waitcnt lgkmcnt(14)
buffer_load_dwordx4 v[96:99],v6,s[32:35],s78 offen sc1
ds_read_b64_tr_b16 a[104:105],v43 offset:20704
ds_read_b64_tr_b16 a[106:107],v43 offset:25824
v_mfma_f32_16x16x32_bf16 a[44:47],a[168:171],v[244:247],a[44:47]
s_waitcnt lgkmcnt(14)
ds_read_b64_tr_b16 a[108:109],v43 offset:30944
ds_read_b64_tr_b16 a[110:111],v43 offset:36064
v_mfma_f32_16x16x32_bf16 a[44:47],a[172:175],v[252:255],a[44:47]
s_waitcnt lgkmcnt(14)
ds_read_b64_tr_b16 a[112:113],v45 offset:224
ds_read_b64_tr_b16 a[114:115],v45 offset:5344
v_mfma_f32_16x16x32_bf16 a[60:63],a[176:179],v[152:155],a[60:63]
s_waitcnt lgkmcnt(14)
ds_read_b64_tr_b16 a[116:117],v45 offset:10464
ds_read_b64_tr_b16 a[118:119],v45 offset:15584
v_mfma_f32_16x16x32_bf16 a[60:63],a[180:183],v[184:187],a[60:63]
s_waitcnt lgkmcnt(14)
buffer_load_dwordx4 v[68:71],v6,s[32:35],s76 offen offset:1024 sc1
ds_read_b64_tr_b16 a[120:121],v45 offset:20704
ds_read_b64_tr_b16 a[122:123],v45 offset:25824
v_mfma_f32_16x16x32_bf16 a[60:63],a[184:187],v[216:219],a[60:63]
s_waitcnt lgkmcnt(14)
ds_read_b64_tr_b16 a[124:125],v45 offset:30944
ds_read_b64_tr_b16 a[126:127],v45 offset:36064
v_mfma_f32_16x16x32_bf16 a[60:63],a[188:191],v[248:251],a[60:63]
s_waitcnt lgkmcnt(14)
ds_read_b64_tr_b16 a[128:129],v1 offset:256
ds_read_b64_tr_b16 a[130:131],v1 offset:5376
v_mfma_f32_16x16x32_bf16 a[60:63],a[96:99],v[112:115],a[60:63]
s_waitcnt lgkmcnt(14)
ds_read_b64_tr_b16 a[132:133],v1 offset:10496
ds_read_b64_tr_b16 a[134:135],v1 offset:15616
v_mfma_f32_16x16x32_bf16 a[60:63],a[100:103],v[116:119],a[60:63]
s_waitcnt lgkmcnt(14)
buffer_load_dwordx4 v[84:87],v6,s[32:35],s77 offen offset:1024 sc1
ds_read_b64_tr_b16 a[136:137],v1 offset:20736
ds_read_b64_tr_b16 a[138:139],v1 offset:25856
v_mfma_f32_16x16x32_bf16 a[60:63],a[104:107],v[120:123],a[60:63]
s_waitcnt lgkmcnt(14)
ds_read_b64_tr_b16 a[140:141],v1 offset:30976
ds_read_b64_tr_b16 a[142:143],v1 offset:36096
v_mfma_f32_16x16x32_bf16 a[60:63],a[108:111],v[124:127],a[60:63]
s_waitcnt lgkmcnt(14)
ds_read_b64_tr_b16 a[144:145],v42 offset:256
ds_read_b64_tr_b16 a[146:147],v42 offset:5376
v_mfma_f32_16x16x32_bf16 a[60:63],a[112:115],v[228:231],a[60:63]
s_waitcnt lgkmcnt(14)
ds_read_b64_tr_b16 a[148:149],v42 offset:10496
ds_read_b64_tr_b16 a[150:151],v42 offset:15616
v_mfma_f32_16x16x32_bf16 a[60:63],a[116:119],v[236:239],a[60:63]
s_waitcnt lgkmcnt(14)
buffer_load_dwordx4 v[100:103],v6,s[32:35],s78 offen offset:1024 sc1
ds_read_b64_tr_b16 a[152:153],v42 offset:20736
ds_read_b64_tr_b16 a[154:155],v42 offset:25856
v_mfma_f32_16x16x32_bf16 a[60:63],a[120:123],v[244:247],a[60:63]
s_waitcnt lgkmcnt(14)
ds_read_b64_tr_b16 a[156:157],v42 offset:30976
ds_read_b64_tr_b16 a[158:159],v42 offset:36096
v_mfma_f32_16x16x32_bf16 a[60:63],a[124:127],v[252:255],a[60:63]
s_waitcnt lgkmcnt(14)
ds_read_b64_tr_b16 a[160:161],v44 offset:256
ds_read_b64_tr_b16 a[162:163],v44 offset:5376
v_mfma_f32_16x16x32_bf16 a[76:79],a[128:131],v[152:155],a[76:79]
s_waitcnt lgkmcnt(14)
ds_read_b64_tr_b16 a[164:165],v44 offset:10496
ds_read_b64_tr_b16 a[166:167],v44 offset:15616
v_mfma_f32_16x16x32_bf16 a[76:79],a[132:135],v[184:187],a[76:79]
s_waitcnt lgkmcnt(14)
buffer_load_dwordx4 v[72:75],v6,s[32:35],s76 offen offset:2048 sc1
ds_read_b64_tr_b16 a[168:169],v44 offset:20736
ds_read_b64_tr_b16 a[170:171],v44 offset:25856
)A"
R"A(v_bfe_u32 v24,v6,4,2
v_min_u32 v24,2,v24
v_mfma_f32_16x16x32_bf16 a[76:79],a[136:139],v[216:219],a[76:79]
s_waitcnt lgkmcnt(14)
ds_read_b64_tr_b16 a[172:173],v44 offset:30976
ds_read_b64_tr_b16 a[174:175],v44 offset:36096
v_add_u32 v24,s70,v24
v_add_u32 v24,1,v24
v_mfma_f32_16x16x32_bf16 a[76:79],a[140:143],v[248:251],a[76:79]
s_waitcnt lgkmcnt(14)
ds_read_b64_tr_b16 a[176:177],v2 offset:288
ds_read_b64_tr_b16 a[178:179],v2 offset:5408
v_and_b32 v24,3,v24
v_lshlrev_b32 v24,8,v24
v_mfma_f32_16x16x32_bf16 a[76:79],a[144:147],v[112:115],a[76:79]
s_waitcnt lgkmcnt(14)
ds_read_b64_tr_b16 a[180:181],v2 offset:10528
ds_read_b64_tr_b16 a[182:183],v2 offset:15648
v_mfma_f32_16x16x32_bf16 a[76:79],a[148:151],v[116:119],a[76:79]
s_waitcnt lgkmcnt(14)
buffer_load_dwordx4 v[88:91],v6,s[32:35],s77 offen offset:2048 sc1
ds_read_b64_tr_b16 a[184:185],v2 offset:20768
ds_read_b64_tr_b16 a[186:187],v2 offset:25888
v_mfma_f32_16x16x32_bf16 a[76:79],a[152:155],v[120:123],a[76:79]
s_waitcnt lgkmcnt(14)
ds_read_b64_tr_b16 a[188:189],v2 offset:31008
ds_read_b64_tr_b16 a[190:191],v2 offset:36128
v_mfma_f32_16x16x32_bf16 a[76:79],a[156:159],v[124:127],a[76:79]
s_waitcnt lgkmcnt(14)
ds_read_b64_tr_b16 a[96:97],v43 offset:288
ds_read_b64_tr_b16 a[98:99],v43 offset:5408
v_mfma_f32_16x16x32_bf16 a[76:79],a[160:163],v[228:231],a[76:79]
s_waitcnt lgkmcnt(14)
ds_read_b64_tr_b16 a[100:101],v43 offset:10528
ds_read_b64_tr_b16 a[102:103],v43 offset:15648
v_mfma_f32_16x16x32_bf16 a[76:79],a[164:167],v[236:239],a[76:79]
s_waitcnt lgkmcnt(14)
buffer_load_dwordx4 v[104:107],v6,s[32:35],s78 offen offset:2048 sc1
ds_read_b64_tr_b16 a[104:105],v43 offset:20768
ds_read_b64_tr_b16 a[106:107],v43 offset:25888
v_mfma_f32_16x16x32_bf16 a[76:79],a[168:171],v[244:247],a[76:79]
s_waitcnt lgkmcnt(14)
ds_read_b64_tr_b16 a[108:109],v43 offset:31008
ds_read_b64_tr_b16 a[110:111],v43 offset:36128
v_mfma_f32_16x16x32_bf16 a[76:79],a[172:175],v[252:255],a[76:79]
s_waitcnt lgkmcnt(14)
ds_read_b64_tr_b16 a[112:113],v45 offset:288
ds_read_b64_tr_b16 a[114:115],v45 offset:5408
v_mfma_f32_16x16x32_bf16 a[92:95],a[176:179],v[152:155],a[92:95]
s_waitcnt lgkmcnt(14)
ds_read_b64_tr_b16 a[116:117],v45 offset:10528
ds_read_b64_tr_b16 a[118:119],v45 offset:15648
v_mfma_f32_16x16x32_bf16 a[92:95],a[180:183],v[184:187],a[92:95]
s_waitcnt lgkmcnt(14)
buffer_load_dwordx4 v[76:79],v6,s[32:35],s76 offen offset:3072 sc1
ds_read_b64_tr_b16 a[120:121],v45 offset:20768
ds_read_b64_tr_b16 a[122:123],v45 offset:25888
v_mfma_f32_16x16x32_bf16 a[92:95],a[184:187],v[216:219],a[92:95]
s_waitcnt lgkmcnt(14)
ds_read_b64_tr_b16 a[124:125],v45 offset:31008
ds_read_b64_tr_b16 a[126:127],v45 offset:36128
v_mfma_f32_16x16x32_bf16 a[92:95],a[188:191],v[248:251],a[92:95]
s_waitcnt lgkmcnt(14)
v_mfma_f32_16x16x32_bf16 a[92:95],a[96:99],v[112:115],a[92:95]
s_waitcnt lgkmcnt(12)
v_mfma_f32_16x16x32_bf16 a[92:95],a[100:103],v[116:119],a[92:95]
s_waitcnt lgkmcnt(10)
buffer_load_dwordx4 v[92:95],v6,s[32:35],s77 offen offset:3072 sc1
v_mfma_f32_16x16x32_bf16 a[92:95],a[104:107],v[120:123],a[92:95]
s_waitcnt lgkmcnt(8)
v_mfma_f32_16x16x32_bf16 a[92:95],a[108:111],v[124:127],a[92:95]
s_waitcnt lgkmcnt(6)
v_mfma_f32_16x16x32_bf16 a[92:95],a[112:115],v[228:231],a[92:95]
s_waitcnt lgkmcnt(4)
v_mfma_f32_16x16x32_bf16 a[92:95],a[116:119],v[236:239],a[92:95]
s_waitcnt lgkmcnt(2)
buffer_load_dwordx4 v[108:111],v6,s[32:35],s78 offen offset:3072 sc1
v_mfma_f32_16x16x32_bf16 a[92:95],a[120:123],v[244:247],a[92:95]
s_waitcnt lgkmcnt(0)
v_mfma_f32_16x16x32_bf16 a[92:95],a[124:127],v[252:255],a[92:95]
buffer_load_dwordx2 v[26:27],v24,s[64:67],s94 offen offset:3072 sc1
s_waitcnt vmcnt(0)
v_cmp_ne_u32 s[90:91],s68,v26
v_cmp_ne_u32 s[92:93],s69,v27
s_nop 0
s_or_b64 s[90:91],s[90:91],s[92:93]
s_cbranch_scc1 L_fr11_%=
L_fo11_%=:
buffer_load_dwordx4 v[132:135],v25,s[32:35],s76 offen sc1
buffer_load_dwordx4 v[164:167],v25,s[32:35],s77 offen sc1
buffer_load_dwordx4 v[196:199],v25,s[32:35],s78 offen sc1
v_mfma_f32_16x16x32_bf16 a[204:207],v[34:37],v[64:67],a[204:207]
v_mfma_f32_16x16x32_bf16 a[204:207],v[34:37],v[80:83],a[204:207]
v_mfma_f32_16x16x32_bf16 a[204:207],v[34:37],v[96:99],a[204:207]
v_mfma_f32_16x16x32_bf16 a[220:223],v[32:35],v[64:67],a[220:223]
v_mfma_f32_16x16x32_bf16 a[220:223],v[32:35],v[80:83],a[220:223]
v_mfma_f32_16x16x32_bf16 a[220:223],v[32:35],v[96:99],a[220:223]
v_mfma_f32_16x16x32_bf16 a[236:239],v[34:37],v[68:71],a[236:239]
v_mfma_f32_16x16x32_bf16 a[236:239],v[34:37],v[84:87],a[236:239]
v_mfma_f32_16x16x32_bf16 a[236:239],v[34:37],v[100:103],a[236:239]
s_nop 1
v_accvgpr_read_b32 v128,a204
v_accvgpr_read_b32 v129,a205
v_mfma_f32_16x16x32_bf16 a[252:255],v[32:35],v[68:71],a[252:255]
v_cvt_pk_bf16_f32 v128,v128,v129
v_accvgpr_read_b32 v130,a206
v_mfma_f32_16x16x32_bf16 a[252:255],v[32:35],v[84:87],a[252:255]
v_accvgpr_read_b32 v131,a207
v_cvt_pk_bf16_f32 v129,v130,v131
v_mfma_f32_16x16x32_bf16 a[252:255],v[32:35],v[100:103],a[252:255]
buffer_store_dwordx2 v[128:129],v40,s[20:23],s43 offen
v_mfma_f32_16x16x32_bf16 a[12:15],v[34:37],v[72:75],a[12:15]
v_accvgpr_read_b32 v130,a220
v_accvgpr_read_b32 v131,a221
v_mfma_f32_16x16x32_bf16 a[12:15],v[34:37],v[88:91],a[12:15]
v_accvgpr_read_b32 v14,a222
v_accvgpr_read_b32 v15,a223
v_mfma_f32_16x16x32_bf16 a[12:15],v[34:37],v[104:107],a[12:15]
v_cvt_pk_bf16_f32 v130,v130,v131
v_cvt_pk_bf16_f32 v131,v14,v15
v_mfma_f32_16x16x32_bf16 a[28:31],v[32:35],v[72:75],a[28:31]
buffer_store_dwordx2 v[130:131],v40,s[20:23],s43 offen offset:32
v_mfma_f32_16x16x32_bf16 a[28:31],v[32:35],v[88:91],a[28:31]
v_accvgpr_read_b32 v136,a236
v_accvgpr_read_b32 v137,a237
v_mfma_f32_16x16x32_bf16 a[28:31],v[32:35],v[104:107],a[28:31]
v_cvt_pk_bf16_f32 v136,v136,v137
v_accvgpr_read_b32 v138,a238
v_mfma_f32_16x16x32_bf16 a[44:47],v[34:37],v[76:79],a[44:47]
v_accvgpr_read_b32 v139,a239
v_cvt_pk_bf16_f32 v137,v138,v139
v_mfma_f32_16x16x32_bf16 a[44:47],v[34:37],v[92:95],a[44:47]
buffer_store_dwordx2 v[136:137],v40,s[20:23],s43 offen offset:64
v_mfma_f32_16x16x32_bf16 a[44:47],v[34:37],v[108:111],a[44:47]
v_accvgpr_read_b32 v144,a12
v_accvgpr_read_b32 v145,a13
v_cvt_pk_bf16_f32 v144,v144,v145
v_accvgpr_read_b32 v146,a14
v_accvgpr_read_b32 v147,a15
v_mfma_f32_16x16x32_bf16 a[60:63],v[32:35],v[76:79],a[60:63]
v_accvgpr_read_b32 v138,a252
v_accvgpr_read_b32 v139,a253
v_cvt_pk_bf16_f32 v145,v146,v147
v_mfma_f32_16x16x32_bf16 a[60:63],v[32:35],v[92:95],a[60:63]
v_accvgpr_read_b32 v16,a254
v_accvgpr_read_b32 v17,a255
v_cvt_pk_bf16_f32 v138,v138,v139
v_cvt_pk_bf16_f32 v139,v16,v17
v_mfma_f32_16x16x32_bf16 a[60:63],v[32:35],v[108:111],a[60:63]
s_waitcnt vmcnt(5)
buffer_store_dwordx2 v[138:139],v40,s[20:23],s43 offen offset:96
buffer_store_dwordx2 v[144:145],v40,s[20:23],s43 offen offset:128
v_accvgpr_read_b32 v146,a28
v_accvgpr_read_b32 v147,a29
v_accvgpr_read_b32 v12,a30
v_accvgpr_read_b32 v13,a31
v_cvt_pk_bf16_f32 v146,v146,v147
v_cvt_pk_bf16_f32 v147,v12,v13
s_nop 3
buffer_store_dwordx2 v[146:147],v40,s[20:23],s43 offen offset:160
v_accvgpr_read_b32 v160,a44
v_accvgpr_read_b32 v161,a45
v_cvt_pk_bf16_f32 v160,v160,v161
v_accvgpr_read_b32 v162,a46
v_accvgpr_read_b32 v163,a47
v_cvt_pk_bf16_f32 v161,v162,v163
buffer_store_dwordx2 v[160:161],v40,s[20:23],s43 offen offset:192
v_accvgpr_read_b32 v162,a60
v_accvgpr_read_b32 v163,a61
v_accvgpr_read_b32 v14,a62
v_accvgpr_read_b32 v15,a63
v_cvt_pk_bf16_f32 v162,v162,v163
v_cvt_pk_bf16_f32 v163,v14,v15
buffer_store_dwordx2 v[162:163],v40,s[20:23],s43 offen offset:224
v_mfma_f32_16x16x32_bf16 a[76:79],v[34:37],v[132:135],a[76:79]
s_waitcnt vmcnt(9)
v_mfma_f32_16x16x32_bf16 a[76:79],v[34:37],v[164:167],a[76:79]
s_waitcnt vmcnt(8)
v_mfma_f32_16x16x32_bf16 a[76:79],v[34:37],v[196:199],a[76:79]
v_mfma_f32_16x16x32_bf16 a[92:95],v[32:35],v[132:135],a[92:95]
v_mfma_f32_16x16x32_bf16 a[92:95],v[32:35],v[164:167],a[92:95]
v_mfma_f32_16x16x32_bf16 a[92:95],v[32:35],v[196:199],a[92:95]
s_nop 3
s_nop 0
v_accvgpr_read_b32 v168,a76
v_accvgpr_read_b32 v169,a77
v_cvt_pk_bf16_f32 v168,v168,v169
v_accvgpr_read_b32 v170,a78
v_accvgpr_read_b32 v171,a79
v_cvt_pk_bf16_f32 v169,v170,v171
buffer_store_dwordx2 v[168:169],v40,s[20:23],s43 offen offset:256
v_accvgpr_read_b32 v170,a92
v_accvgpr_read_b32 v171,a93
v_accvgpr_read_b32 v16,a94
v_accvgpr_read_b32 v17,a95
v_cvt_pk_bf16_f32 v170,v170,v171
v_cvt_pk_bf16_f32 v171,v16,v17
buffer_store_dwordx2 v[170:171],v40,s[20:23],s43 offen offset:288
s_or_b32 s62,s42,s40
s_cmp_lg_u32 s62,0
s_cbranch_scc1 L_noep_%=
v_mov_b32 v12,s68
s_mov_b64 exec,1
buffer_store_dword v12,v25,s[64:67],0 offen sc1
s_mov_b64 exec,-1
L_noep_%=:
s_endpgm
L_ps2_%=:
s_min_i32 s79,s79,100
s_add_u32 s78,s78,s79
L_pl2_%=:
s_sleep 1
s_sub_u32 s79,s79,4
s_cmp_gt_i32 s79,1
s_cbranch_scc1 L_pl2_%=
s_branch L_pr2_%=
L_ps3_%=:
s_min_i32 s79,s79,100
s_add_u32 s78,s78,s79
L_pl3_%=:
s_sleep 1
s_sub_u32 s79,s79,4
s_cmp_gt_i32 s79,1
s_cbranch_scc1 L_pl3_%=
s_branch L_pr3_%=
L_ps4_%=:
s_min_i32 s79,s79,100
s_add_u32 s78,s78,s79
L_pl4_%=:
s_sleep 1
s_sub_u32 s79,s79,4
s_cmp_gt_i32 s79,1
s_cbranch_scc1 L_pl4_%=
s_branch L_pr4_%=
L_ps5_%=:
s_min_i32 s79,s79,100
s_add_u32 s78,s78,s79
L_pl5_%=:
s_sleep 1
s_sub_u32 s79,s79,4
s_cmp_gt_i32 s79,1
s_cbranch_scc1 L_pl5_%=
s_branch L_pr5_%=
L_ps6_%=:
s_min_i32 s79,s79,100
s_add_u32 s78,s78,s79
L_pl6_%=:
s_sleep 1
s_sub_u32 s79,s79,4
s_cmp_gt_i32 s79,1
s_cbranch_scc1 L_pl6_%=
s_branch L_pr6_%=
L_ps7_%=:
s_min_i32 s79,s79,100
s_add_u32 s78,s78,s79
L_pl7_%=:
s_sleep 1
s_sub_u32 s79,s79,4
s_cmp_gt_i32 s79,1
s_cbranch_scc1 L_pl7_%=
s_branch L_pr7_%=
L_fr8_%=:
s_mov_b32 s88,262144
L_fl8_%=:
buffer_load_dwordx2 v[26:27],v24,s[64:67],s94 offen sc1
s_waitcnt vmcnt(0)
s_sub_u32 s88,s88,1
s_cmp_eq_u32 s88,0
s_cbranch_scc1 L_fo8_%=
v_cmp_ne_u32 s[90:91],s68,v26
v_cmp_ne_u32 s[92:93],s69,v27
s_nop 0
s_or_b64 s[90:91],s[90:91],s[92:93]
s_cbranch_scc1 L_fl8_%=
s_branch L_fo8_%=
L_fr9_%=:
s_mov_b32 s88,262144
L_fl9_%=:
buffer_load_dwordx2 v[26:27],v24,s[64:67],s94 offen offset:1024 sc1
s_waitcnt vmcnt(0)
s_sub_u32 s88,s88,1
s_cmp_eq_u32 s88,0
s_cbranch_scc1 L_fo9_%=
v_cmp_ne_u32 s[90:91],s68,v26
v_cmp_ne_u32 s[92:93],s69,v27
s_nop 0
s_or_b64 s[90:91],s[90:91],s[92:93]
s_cbranch_scc1 L_fl9_%=
s_branch L_fo9_%=
L_fr10_%=:
s_mov_b32 s88,262144
L_fl10_%=:
buffer_load_dwordx2 v[26:27],v24,s[64:67],s94 offen offset:2048 sc1
s_waitcnt vmcnt(0)
s_sub_u32 s88,s88,1
s_cmp_eq_u32 s88,0
s_cbranch_scc1 L_fo10_%=
v_cmp_ne_u32 s[90:91],s68,v26
v_cmp_ne_u32 s[92:93],s69,v27
s_nop 0
s_or_b64 s[90:91],s[90:91],s[92:93]
s_cbranch_scc1 L_fl10_%=
s_branch L_fo10_%=
L_fr11_%=:
s_mov_b32 s88,262144
L_fl11_%=:
buffer_load_dwordx2 v[26:27],v24,s[64:67],s94 offen offset:3072 sc1
s_waitcnt vmcnt(0)
s_sub_u32 s88,s88,1
s_cmp_eq_u32 s88,0
s_cbranch_scc1 L_fo11_%=
v_cmp_ne_u32 s[90:91],s68,v26
v_cmp_ne_u32 s[92:93],s69,v27
s_nop 0
s_or_b64 s[90:91],s[90:91],s[92:93]
s_cbranch_scc1 L_fl11_%=
s_branch L_fo11_%=
L_out_end_%=:
L_fast_end_%=:
L_slow0_%=:
s_mov_b32 s98,0
s_branch L_slowc_%=
L_slow1_%=:
s_mov_b32 s98,1
L_slowc_%=:
s_nop 7
s_nop 7
s_waitcnt vmcnt(0) lgkmcnt(0)
s_lshl_b32 s96,%1,2
s_add_u32 s96,s96,s40
s_lshl_b32 s96,s96,15
s_add_u32 s96,s96,167772160
s_mov_b64 s[56:57],s[12:13]
s_add_u32 s56,s56,s96
s_addc_u32 s57,s57,0
s_and_b32 s57,s57,0xffff
s_mov_b32 s58,32768
s_mov_b32 s59,0x20000
s_mov_b32 s96,0
buffer_store_dwordx4 v[64:67],v6,s[56:59],s96 offen
buffer_store_dwordx4 v[68:71],v6,s[56:59],s96 offen offset:1024
buffer_store_dwordx4 v[72:75],v6,s[56:59],s96 offen offset:2048
buffer_store_dwordx4 v[76:79],v6,s[56:59],s96 offen offset:3072
s_mov_b32 s96,4096
buffer_store_dwordx4 v[80:83],v6,s[56:59],s96 offen
buffer_store_dwordx4 v[84:87],v6,s[56:59],s96 offen offset:1024
buffer_store_dwordx4 v[88:91],v6,s[56:59],s96 offen offset:2048
buffer_store_dwordx4 v[92:95],v6,s[56:59],s96 offen offset:3072
s_mov_b32 s96,8192
buffer_store_dwordx4 v[96:99],v6,s[56:59],s96 offen
buffer_store_dwordx4 v[100:103],v6,s[56:59],s96 offen offset:1024
buffer_store_dwordx4 v[104:107],v6,s[56:59],s96 offen offset:2048
buffer_store_dwordx4 v[108:111],v6,s[56:59],s96 offen offset:3072
)A"
R"A(s_mov_b32 s96,12288
buffer_store_dwordx4 v[112:115],v6,s[56:59],s96 offen
buffer_store_dwordx4 v[116:119],v6,s[56:59],s96 offen offset:1024
buffer_store_dwordx4 v[120:123],v6,s[56:59],s96 offen offset:2048
buffer_store_dwordx4 v[124:127],v6,s[56:59],s96 offen offset:3072
s_mov_b32 s96,16384
buffer_store_dwordx4 v[128:131],v6,s[56:59],s96 offen
buffer_store_dwordx4 v[132:135],v6,s[56:59],s96 offen offset:1024
buffer_store_dwordx4 v[136:139],v6,s[56:59],s96 offen offset:2048
buffer_store_dwordx4 v[140:143],v6,s[56:59],s96 offen offset:3072
s_mov_b32 s96,20480
buffer_store_dwordx4 v[144:147],v6,s[56:59],s96 offen
buffer_store_dwordx4 v[148:151],v6,s[56:59],s96 offen offset:1024
buffer_store_dwordx4 v[152:155],v6,s[56:59],s96 offen offset:2048
buffer_store_dwordx4 v[156:159],v6,s[56:59],s96 offen offset:3072
s_mov_b32 s96,24576
buffer_store_dwordx4 v[160:163],v6,s[56:59],s96 offen
buffer_store_dwordx4 v[164:167],v6,s[56:59],s96 offen offset:1024
buffer_store_dwordx4 v[168:171],v6,s[56:59],s96 offen offset:2048
buffer_store_dwordx4 v[172:175],v6,s[56:59],s96 offen offset:3072
s_mov_b32 s96,28672
buffer_store_dwordx4 v[176:179],v6,s[56:59],s96 offen
buffer_store_dwordx4 v[180:183],v6,s[56:59],s96 offen offset:1024
buffer_store_dwordx4 v[184:187],v6,s[56:59],s96 offen offset:2048
buffer_store_dwordx4 v[188:191],v6,s[56:59],s96 offen offset:3072
s_waitcnt vmcnt(0)
v_writelane_b32 v173,s88,0
v_writelane_b32 v173,s89,1
v_writelane_b32 v173,s90,2
v_writelane_b32 v173,s91,3
v_writelane_b32 v173,s92,4
v_writelane_b32 v173,s93,5
v_writelane_b32 v173,s94,6
v_writelane_b32 v173,s95,7
s_mov_b32 s88,s98
s_mul_i32 s90,s41,307200
s_mov_b64 s[92:93],s[4:5]
s_add_u32 s92,s92,s90
s_addc_u32 s93,s93,0
s_and_b32 s93,s93,0xffff
s_mov_b32 s94,307200
s_mov_b32 s95,0x20000
s_mov_b64 s[96:97],s[10:11]
s_and_b32 s97,s97,0xffff
s_mov_b32 s98,5242880
s_mov_b32 s99,0x20000
v_bfe_u32 v171,v6,4,4
v_lshrrev_b32 v172,8,v6
v_lshlrev_b32 v164,8,v171
v_lshl_add_u32 v164,v172,4,v164
v_lshlrev_b32 v165,10,v172
v_lshl_add_u32 v165,v171,1,v165
v_add_u32 v166,4096,v165
v_lshlrev_b32 v168,16,v172
v_lshrrev_b32 v170,2,v171
v_lshl_add_u32 v168,v170,4,v168
v_and_b32 v170,3,v171
v_lshl_add_u32 v168,v170,1,v168
s_lshl_b32 s90,s42,1
s_add_u32 s90,s90,s88
s_lshl_b32 s90,s90,8
v_add_u32 v168,s90,v168
s_mov_b32 s89,0
L_sjb_%=:
s_add_u32 s90,s89,s70
s_add_u32 s90,s90,1
s_and_b32 s90,s90,3
s_lshl_b32 s91,s40,2
s_add_u32 s90,s90,s91
s_lshl_b32 s90,s90,4
v_bfe_u32 v171,v6,4,4
v_lshrrev_b32 v172,8,v6
v_add_u32 v167,s90,v171
v_mul_u32_u24 v167,1280,v167
v_lshl_add_u32 v167,v172,4,v167
s_lshl_b32 s90,s88,4
s_add_u32 s90,s90,s89
s_lshl_b32 s90,s90,12
v_add_u32 v169,s90,v6
v_mov_b32 v64,0
v_mov_b32 v65,0
v_mov_b32 v66,0
v_mov_b32 v67,0
v_mov_b32 v68,0
v_mov_b32 v69,0
v_mov_b32 v70,0
v_mov_b32 v71,0
v_mov_b32 v72,0
v_mov_b32 v73,0
v_mov_b32 v74,0
v_mov_b32 v75,0
v_mov_b32 v76,0
v_mov_b32 v77,0
v_mov_b32 v78,0
v_mov_b32 v79,0
v_mov_b32 v80,0
v_mov_b32 v81,0
v_mov_b32 v82,0
v_mov_b32 v83,0
v_mov_b32 v84,0
v_mov_b32 v85,0
v_mov_b32 v86,0
v_mov_b32 v87,0
v_mov_b32 v88,0
v_mov_b32 v89,0
v_mov_b32 v90,0
v_mov_b32 v91,0
v_mov_b32 v92,0
v_mov_b32 v93,0
v_mov_b32 v94,0
v_mov_b32 v95,0
s_mov_b32 s90,0
L_sq_%=:
s_lshl_b32 s91,s90,6
buffer_load_dwordx4 v[160:163],v167,s[92:95],s91 offen
s_lshl_b32 s91,s90,18
buffer_load_ushort v112,v168,s[96:99],s91 offen
buffer_load_ushort v120,v168,s[96:99],s91 offen offset:8
buffer_load_ushort v128,v168,s[96:99],s91 offen offset:64
buffer_load_ushort v136,v168,s[96:99],s91 offen offset:72
s_add_u32 s91,s91,8192
buffer_load_ushort v113,v168,s[96:99],s91 offen
buffer_load_ushort v121,v168,s[96:99],s91 offen offset:8
buffer_load_ushort v129,v168,s[96:99],s91 offen offset:64
buffer_load_ushort v137,v168,s[96:99],s91 offen offset:72
s_add_u32 s91,s91,8192
buffer_load_ushort v114,v168,s[96:99],s91 offen
buffer_load_ushort v122,v168,s[96:99],s91 offen offset:8
buffer_load_ushort v130,v168,s[96:99],s91 offen offset:64
buffer_load_ushort v138,v168,s[96:99],s91 offen offset:72
s_add_u32 s91,s91,8192
buffer_load_ushort v115,v168,s[96:99],s91 offen
buffer_load_ushort v123,v168,s[96:99],s91 offen offset:8
buffer_load_ushort v131,v168,s[96:99],s91 offen offset:64
buffer_load_ushort v139,v168,s[96:99],s91 offen offset:72
s_add_u32 s91,s91,8192
buffer_load_ushort v116,v168,s[96:99],s91 offen
buffer_load_ushort v124,v168,s[96:99],s91 offen offset:8
buffer_load_ushort v132,v168,s[96:99],s91 offen offset:64
buffer_load_ushort v140,v168,s[96:99],s91 offen offset:72
s_add_u32 s91,s91,8192
buffer_load_ushort v117,v168,s[96:99],s91 offen
buffer_load_ushort v125,v168,s[96:99],s91 offen offset:8
buffer_load_ushort v133,v168,s[96:99],s91 offen offset:64
buffer_load_ushort v141,v168,s[96:99],s91 offen offset:72
s_add_u32 s91,s91,8192
buffer_load_ushort v118,v168,s[96:99],s91 offen
buffer_load_ushort v126,v168,s[96:99],s91 offen offset:8
buffer_load_ushort v134,v168,s[96:99],s91 offen offset:64
buffer_load_ushort v142,v168,s[96:99],s91 offen offset:72
s_add_u32 s91,s91,8192
buffer_load_ushort v119,v168,s[96:99],s91 offen
buffer_load_ushort v127,v168,s[96:99],s91 offen offset:8
buffer_load_ushort v135,v168,s[96:99],s91 offen offset:64
buffer_load_ushort v143,v168,s[96:99],s91 offen offset:72
s_add_u32 s91,s91,8192
s_waitcnt vmcnt(0)
v_lshl_or_b32 v112,v113,16,v112
v_lshl_or_b32 v113,v115,16,v114
v_lshl_or_b32 v114,v117,16,v116
v_lshl_or_b32 v115,v119,16,v118
v_lshl_or_b32 v120,v121,16,v120
v_lshl_or_b32 v121,v123,16,v122
v_lshl_or_b32 v122,v125,16,v124
v_lshl_or_b32 v123,v127,16,v126
v_lshl_or_b32 v128,v129,16,v128
v_lshl_or_b32 v129,v131,16,v130
v_lshl_or_b32 v130,v133,16,v132
v_lshl_or_b32 v131,v135,16,v134
v_lshl_or_b32 v136,v137,16,v136
v_lshl_or_b32 v137,v139,16,v138
v_lshl_or_b32 v138,v141,16,v140
v_lshl_or_b32 v139,v143,16,v142
s_nop 1
v_mfma_f32_16x16x32_bf16 v[64:67],v[112:115],v[160:163],v[64:67]
v_mfma_f32_16x16x32_bf16 v[68:71],v[120:123],v[160:163],v[68:71]
v_mfma_f32_16x16x32_bf16 v[72:75],v[128:131],v[160:163],v[72:75]
v_mfma_f32_16x16x32_bf16 v[76:79],v[136:139],v[160:163],v[76:79]
s_nop 7
s_lshl_b32 s91,s90,18
buffer_load_ushort v112,v168,s[96:99],s91 offen offset:128
buffer_load_ushort v120,v168,s[96:99],s91 offen offset:136
buffer_load_ushort v128,v168,s[96:99],s91 offen offset:192
buffer_load_ushort v136,v168,s[96:99],s91 offen offset:200
s_add_u32 s91,s91,8192
buffer_load_ushort v113,v168,s[96:99],s91 offen offset:128
buffer_load_ushort v121,v168,s[96:99],s91 offen offset:136
buffer_load_ushort v129,v168,s[96:99],s91 offen offset:192
buffer_load_ushort v137,v168,s[96:99],s91 offen offset:200
s_add_u32 s91,s91,8192
buffer_load_ushort v114,v168,s[96:99],s91 offen offset:128
buffer_load_ushort v122,v168,s[96:99],s91 offen offset:136
buffer_load_ushort v130,v168,s[96:99],s91 offen offset:192
buffer_load_ushort v138,v168,s[96:99],s91 offen offset:200
s_add_u32 s91,s91,8192
buffer_load_ushort v115,v168,s[96:99],s91 offen offset:128
buffer_load_ushort v123,v168,s[96:99],s91 offen offset:136
buffer_load_ushort v131,v168,s[96:99],s91 offen offset:192
buffer_load_ushort v139,v168,s[96:99],s91 offen offset:200
s_add_u32 s91,s91,8192
buffer_load_ushort v116,v168,s[96:99],s91 offen offset:128
buffer_load_ushort v124,v168,s[96:99],s91 offen offset:136
buffer_load_ushort v132,v168,s[96:99],s91 offen offset:192
buffer_load_ushort v140,v168,s[96:99],s91 offen offset:200
s_add_u32 s91,s91,8192
buffer_load_ushort v117,v168,s[96:99],s91 offen offset:128
buffer_load_ushort v125,v168,s[96:99],s91 offen offset:136
buffer_load_ushort v133,v168,s[96:99],s91 offen offset:192
buffer_load_ushort v141,v168,s[96:99],s91 offen offset:200
s_add_u32 s91,s91,8192
buffer_load_ushort v118,v168,s[96:99],s91 offen offset:128
buffer_load_ushort v126,v168,s[96:99],s91 offen offset:136
buffer_load_ushort v134,v168,s[96:99],s91 offen offset:192
buffer_load_ushort v142,v168,s[96:99],s91 offen offset:200
s_add_u32 s91,s91,8192
buffer_load_ushort v119,v168,s[96:99],s91 offen offset:128
buffer_load_ushort v127,v168,s[96:99],s91 offen offset:136
buffer_load_ushort v135,v168,s[96:99],s91 offen offset:192
buffer_load_ushort v143,v168,s[96:99],s91 offen offset:200
s_add_u32 s91,s91,8192
s_waitcnt vmcnt(0)
v_lshl_or_b32 v112,v113,16,v112
v_lshl_or_b32 v113,v115,16,v114
v_lshl_or_b32 v114,v117,16,v116
v_lshl_or_b32 v115,v119,16,v118
v_lshl_or_b32 v120,v121,16,v120
v_lshl_or_b32 v121,v123,16,v122
v_lshl_or_b32 v122,v125,16,v124
v_lshl_or_b32 v123,v127,16,v126
v_lshl_or_b32 v128,v129,16,v128
v_lshl_or_b32 v129,v131,16,v130
v_lshl_or_b32 v130,v133,16,v132
v_lshl_or_b32 v131,v135,16,v134
v_lshl_or_b32 v136,v137,16,v136
v_lshl_or_b32 v137,v139,16,v138
v_lshl_or_b32 v138,v141,16,v140
v_lshl_or_b32 v139,v143,16,v142
s_nop 1
v_mfma_f32_16x16x32_bf16 v[80:83],v[112:115],v[160:163],v[80:83]
v_mfma_f32_16x16x32_bf16 v[84:87],v[120:123],v[160:163],v[84:87]
v_mfma_f32_16x16x32_bf16 v[88:91],v[128:131],v[160:163],v[88:91]
v_mfma_f32_16x16x32_bf16 v[92:95],v[136:139],v[160:163],v[92:95]
s_nop 7
s_add_u32 s90,s90,1
s_cmp_lg_u32 s90,20
s_cbranch_scc1 L_sq_%=
s_nop 7
v_mul_f32 v64,s61,v64
v_mul_f32 v65,s61,v65
v_mul_f32 v66,s61,v66
v_mul_f32 v67,s61,v67
v_mul_f32 v68,s61,v68
v_mul_f32 v69,s61,v69
v_mul_f32 v70,s61,v70
v_mul_f32 v71,s61,v71
v_mul_f32 v72,s61,v72
v_mul_f32 v73,s61,v73
v_mul_f32 v74,s61,v74
v_mul_f32 v75,s61,v75
v_mul_f32 v76,s61,v76
v_mul_f32 v77,s61,v77
v_mul_f32 v78,s61,v78
v_mul_f32 v79,s61,v79
v_mul_f32 v80,s61,v80
v_mul_f32 v81,s61,v81
v_mul_f32 v82,s61,v82
v_mul_f32 v83,s61,v83
v_mul_f32 v84,s61,v84
v_mul_f32 v85,s61,v85
v_mul_f32 v86,s61,v86
v_mul_f32 v87,s61,v87
v_mul_f32 v88,s61,v88
v_mul_f32 v89,s61,v89
v_mul_f32 v90,s61,v90
v_mul_f32 v91,s61,v91
v_mul_f32 v92,s61,v92
v_mul_f32 v93,s61,v93
v_mul_f32 v94,s61,v94
v_mul_f32 v95,s61,v95
v_cvt_pk_bf16_f32 v96,v64,v65
v_cvt_pk_bf16_f32 v97,v66,v67
v_cvt_pk_bf16_f32 v98,v68,v69
v_cvt_pk_bf16_f32 v99,v70,v71
v_cvt_pk_bf16_f32 v100,v72,v73
v_cvt_pk_bf16_f32 v101,v74,v75
v_cvt_pk_bf16_f32 v102,v76,v77
v_cvt_pk_bf16_f32 v103,v78,v79
v_cvt_pk_bf16_f32 v104,v80,v81
v_cvt_pk_bf16_f32 v105,v82,v83
v_cvt_pk_bf16_f32 v106,v84,v85
v_cvt_pk_bf16_f32 v107,v86,v87
v_cvt_pk_bf16_f32 v108,v88,v89
v_cvt_pk_bf16_f32 v109,v90,v91
v_cvt_pk_bf16_f32 v110,v92,v93
v_cvt_pk_bf16_f32 v111,v94,v95
v_mov_b32 v170,0xff800000
s_lshl_b32 s91,s88,18
s_mov_b32 s90,64
L_s1_%=:
buffer_load_dwordx4 v[112:115],v164,s[20:23],s91 offen
buffer_load_dwordx4 v[116:119],v164,s[20:23],s91 offen offset:64
buffer_load_dwordx4 v[120:123],v164,s[20:23],s91 offen offset:128
buffer_load_dwordx4 v[124:127],v164,s[20:23],s91 offen offset:192
s_waitcnt vmcnt(0)
v_mfma_f32_16x16x32_bf16 v[144:147],v[112:115],v[96:99],0
v_mfma_f32_16x16x32_bf16 v[144:147],v[116:119],v[100:103],v[144:147]
v_mfma_f32_16x16x32_bf16 v[144:147],v[120:123],v[104:107],v[144:147]
v_mfma_f32_16x16x32_bf16 v[144:147],v[124:127],v[108:111],v[144:147]
s_nop 7
v_max3_f32 v170,v170,v144,v145
v_max3_f32 v170,v170,v146,v147
s_add_u32 s91,s91,4096
s_sub_u32 s90,s90,1
s_cmp_lg_u32 s90,0
s_cbranch_scc1 L_s1_%=
v_mov_b32 v171,v170
s_nop 1
v_permlane32_swap_b32 v170,v171
v_max_f32 v170,v170,v171
v_mov_b32 v171,v170
s_nop 1
v_permlane16_swap_b32 v170,v171
v_max_f32 v170,v170,v171
v_sub_f32 v152,0,v170
v_mov_b32 v156,0
v_sub_f32 v153,0,v170
v_mov_b32 v157,0
v_sub_f32 v154,0,v170
v_mov_b32 v158,0
v_sub_f32 v155,0,v170
v_mov_b32 v159,0
v_mov_b32 v64,0
v_mov_b32 v65,0
v_mov_b32 v66,0
v_mov_b32 v67,0
v_mov_b32 v68,0
v_mov_b32 v69,0
v_mov_b32 v70,0
v_mov_b32 v71,0
v_mov_b32 v72,0
v_mov_b32 v73,0
v_mov_b32 v74,0
v_mov_b32 v75,0
v_mov_b32 v76,0
v_mov_b32 v77,0
v_mov_b32 v78,0
v_mov_b32 v79,0
v_mov_b32 v80,0
v_mov_b32 v81,0
v_mov_b32 v82,0
v_mov_b32 v83,0
v_mov_b32 v84,0
v_mov_b32 v85,0
v_mov_b32 v86,0
v_mov_b32 v87,0
v_mov_b32 v88,0
v_mov_b32 v89,0
v_mov_b32 v90,0
v_mov_b32 v91,0
v_mov_b32 v92,0
v_mov_b32 v93,0
v_mov_b32 v94,0
v_mov_b32 v95,0
s_lshl_b32 s91,s88,18
s_mov_b32 s90,32
v_add_u32 v172,4096,v164
L_s2_%=:
buffer_load_dwordx4 v[112:115],v164,s[20:23],s91 offen
buffer_load_dwordx4 v[116:119],v164,s[20:23],s91 offen offset:64
)A"
R"A(buffer_load_dwordx4 v[120:123],v164,s[20:23],s91 offen offset:128
buffer_load_dwordx4 v[124:127],v164,s[20:23],s91 offen offset:192
buffer_load_dwordx4 v[128:131],v172,s[20:23],s91 offen
buffer_load_dwordx4 v[132:135],v172,s[20:23],s91 offen offset:64
buffer_load_dwordx4 v[136:139],v172,s[20:23],s91 offen offset:128
buffer_load_dwordx4 v[140:143],v172,s[20:23],s91 offen offset:192
s_waitcnt vmcnt(0)
v_mfma_f32_16x16x32_bf16 v[144:147],v[112:115],v[96:99],v[152:155]
v_mfma_f32_16x16x32_bf16 v[144:147],v[116:119],v[100:103],v[144:147]
v_mfma_f32_16x16x32_bf16 v[144:147],v[120:123],v[104:107],v[144:147]
v_mfma_f32_16x16x32_bf16 v[144:147],v[124:127],v[108:111],v[144:147]
v_mfma_f32_16x16x32_bf16 v[148:151],v[128:131],v[96:99],v[152:155]
v_mfma_f32_16x16x32_bf16 v[148:151],v[132:135],v[100:103],v[148:151]
v_mfma_f32_16x16x32_bf16 v[148:151],v[136:139],v[104:107],v[148:151]
v_mfma_f32_16x16x32_bf16 v[148:151],v[140:143],v[108:111],v[148:151]
s_nop 7
v_exp_f32 v144,v144
v_exp_f32 v145,v145
v_exp_f32 v146,v146
v_exp_f32 v147,v147
v_exp_f32 v148,v148
v_exp_f32 v149,v149
v_exp_f32 v150,v150
v_exp_f32 v151,v151
s_nop 0
v_cvt_pk_bf16_f32 v144,v144,v145
v_cvt_pk_bf16_f32 v145,v146,v147
v_cvt_pk_bf16_f32 v146,v148,v149
v_cvt_pk_bf16_f32 v147,v150,v151
buffer_load_ushort v112,v165,s[24:27],s91 offen
buffer_load_ushort v113,v165,s[24:27],s91 offen offset:256
buffer_load_ushort v114,v165,s[24:27],s91 offen offset:512
buffer_load_ushort v115,v165,s[24:27],s91 offen offset:768
buffer_load_ushort v116,v166,s[24:27],s91 offen
buffer_load_ushort v117,v166,s[24:27],s91 offen offset:256
buffer_load_ushort v118,v166,s[24:27],s91 offen offset:512
buffer_load_ushort v119,v166,s[24:27],s91 offen offset:768
buffer_load_ushort v120,v165,s[24:27],s91 offen offset:32
buffer_load_ushort v121,v165,s[24:27],s91 offen offset:288
buffer_load_ushort v122,v165,s[24:27],s91 offen offset:544
buffer_load_ushort v123,v165,s[24:27],s91 offen offset:800
buffer_load_ushort v124,v166,s[24:27],s91 offen offset:32
buffer_load_ushort v125,v166,s[24:27],s91 offen offset:288
buffer_load_ushort v126,v166,s[24:27],s91 offen offset:544
buffer_load_ushort v127,v166,s[24:27],s91 offen offset:800
buffer_load_ushort v128,v165,s[24:27],s91 offen offset:64
buffer_load_ushort v129,v165,s[24:27],s91 offen offset:320
buffer_load_ushort v130,v165,s[24:27],s91 offen offset:576
buffer_load_ushort v131,v165,s[24:27],s91 offen offset:832
buffer_load_ushort v132,v166,s[24:27],s91 offen offset:64
buffer_load_ushort v133,v166,s[24:27],s91 offen offset:320
buffer_load_ushort v134,v166,s[24:27],s91 offen offset:576
buffer_load_ushort v135,v166,s[24:27],s91 offen offset:832
buffer_load_ushort v136,v165,s[24:27],s91 offen offset:96
buffer_load_ushort v137,v165,s[24:27],s91 offen offset:352
buffer_load_ushort v138,v165,s[24:27],s91 offen offset:608
buffer_load_ushort v139,v165,s[24:27],s91 offen offset:864
buffer_load_ushort v140,v166,s[24:27],s91 offen offset:96
buffer_load_ushort v141,v166,s[24:27],s91 offen offset:352
buffer_load_ushort v142,v166,s[24:27],s91 offen offset:608
buffer_load_ushort v143,v166,s[24:27],s91 offen offset:864
s_waitcnt vmcnt(0)
v_lshl_or_b32 v112,v113,16,v112
v_lshl_or_b32 v113,v115,16,v114
v_lshl_or_b32 v114,v117,16,v116
v_lshl_or_b32 v115,v119,16,v118
v_lshl_or_b32 v120,v121,16,v120
v_lshl_or_b32 v121,v123,16,v122
v_lshl_or_b32 v122,v125,16,v124
v_lshl_or_b32 v123,v127,16,v126
v_lshl_or_b32 v128,v129,16,v128
v_lshl_or_b32 v129,v131,16,v130
v_lshl_or_b32 v130,v133,16,v132
v_lshl_or_b32 v131,v135,16,v134
v_lshl_or_b32 v136,v137,16,v136
v_lshl_or_b32 v137,v139,16,v138
v_lshl_or_b32 v138,v141,16,v140
v_lshl_or_b32 v139,v143,16,v142
s_nop 1
v_mfma_f32_16x16x32_bf16 v[64:67],v[112:115],v[144:147],v[64:67]
v_mfma_f32_16x16x32_bf16 v[68:71],v[120:123],v[144:147],v[68:71]
v_mfma_f32_16x16x32_bf16 v[72:75],v[128:131],v[144:147],v[72:75]
v_mfma_f32_16x16x32_bf16 v[76:79],v[136:139],v[144:147],v[76:79]
s_nop 7
buffer_load_ushort v112,v165,s[24:27],s91 offen offset:128
buffer_load_ushort v113,v165,s[24:27],s91 offen offset:384
buffer_load_ushort v114,v165,s[24:27],s91 offen offset:640
buffer_load_ushort v115,v165,s[24:27],s91 offen offset:896
buffer_load_ushort v116,v166,s[24:27],s91 offen offset:128
buffer_load_ushort v117,v166,s[24:27],s91 offen offset:384
buffer_load_ushort v118,v166,s[24:27],s91 offen offset:640
buffer_load_ushort v119,v166,s[24:27],s91 offen offset:896
buffer_load_ushort v120,v165,s[24:27],s91 offen offset:160
buffer_load_ushort v121,v165,s[24:27],s91 offen offset:416
buffer_load_ushort v122,v165,s[24:27],s91 offen offset:672
buffer_load_ushort v123,v165,s[24:27],s91 offen offset:928
buffer_load_ushort v124,v166,s[24:27],s91 offen offset:160
buffer_load_ushort v125,v166,s[24:27],s91 offen offset:416
buffer_load_ushort v126,v166,s[24:27],s91 offen offset:672
buffer_load_ushort v127,v166,s[24:27],s91 offen offset:928
buffer_load_ushort v128,v165,s[24:27],s91 offen offset:192
buffer_load_ushort v129,v165,s[24:27],s91 offen offset:448
buffer_load_ushort v130,v165,s[24:27],s91 offen offset:704
buffer_load_ushort v131,v165,s[24:27],s91 offen offset:960
buffer_load_ushort v132,v166,s[24:27],s91 offen offset:192
buffer_load_ushort v133,v166,s[24:27],s91 offen offset:448
buffer_load_ushort v134,v166,s[24:27],s91 offen offset:704
buffer_load_ushort v135,v166,s[24:27],s91 offen offset:960
buffer_load_ushort v136,v165,s[24:27],s91 offen offset:224
buffer_load_ushort v137,v165,s[24:27],s91 offen offset:480
buffer_load_ushort v138,v165,s[24:27],s91 offen offset:736
buffer_load_ushort v139,v165,s[24:27],s91 offen offset:992
buffer_load_ushort v140,v166,s[24:27],s91 offen offset:224
buffer_load_ushort v141,v166,s[24:27],s91 offen offset:480
buffer_load_ushort v142,v166,s[24:27],s91 offen offset:736
buffer_load_ushort v143,v166,s[24:27],s91 offen offset:992
s_waitcnt vmcnt(0)
v_lshl_or_b32 v112,v113,16,v112
v_lshl_or_b32 v113,v115,16,v114
v_lshl_or_b32 v114,v117,16,v116
v_lshl_or_b32 v115,v119,16,v118
v_lshl_or_b32 v120,v121,16,v120
v_lshl_or_b32 v121,v123,16,v122
v_lshl_or_b32 v122,v125,16,v124
v_lshl_or_b32 v123,v127,16,v126
v_lshl_or_b32 v128,v129,16,v128
v_lshl_or_b32 v129,v131,16,v130
v_lshl_or_b32 v130,v133,16,v132
v_lshl_or_b32 v131,v135,16,v134
v_lshl_or_b32 v136,v137,16,v136
v_lshl_or_b32 v137,v139,16,v138
v_lshl_or_b32 v138,v141,16,v140
v_lshl_or_b32 v139,v143,16,v142
s_nop 1
v_mfma_f32_16x16x32_bf16 v[80:83],v[112:115],v[144:147],v[80:83]
v_mfma_f32_16x16x32_bf16 v[84:87],v[120:123],v[144:147],v[84:87]
v_mfma_f32_16x16x32_bf16 v[88:91],v[128:131],v[144:147],v[88:91]
v_mfma_f32_16x16x32_bf16 v[92:95],v[136:139],v[144:147],v[92:95]
s_nop 7
v_mfma_f32_16x16x32_bf16 v[156:159],v[28:31],v[144:147],v[156:159]
s_nop 7
s_add_u32 s91,s91,8192
s_sub_u32 s90,s90,1
s_cmp_lg_u32 s90,0
s_cbranch_scc1 L_s2_%=
v_rcp_f32 v170,v156
s_nop 0
v_mul_f32 v64,v170,v64
v_mul_f32 v65,v170,v65
v_mul_f32 v66,v170,v66
v_mul_f32 v67,v170,v67
v_mul_f32 v68,v170,v68
v_mul_f32 v69,v170,v69
v_mul_f32 v70,v170,v70
v_mul_f32 v71,v170,v71
v_mul_f32 v72,v170,v72
v_mul_f32 v73,v170,v73
v_mul_f32 v74,v170,v74
v_mul_f32 v75,v170,v75
v_mul_f32 v76,v170,v76
v_mul_f32 v77,v170,v77
v_mul_f32 v78,v170,v78
v_mul_f32 v79,v170,v79
v_mul_f32 v80,v170,v80
v_mul_f32 v81,v170,v81
v_mul_f32 v82,v170,v82
v_mul_f32 v83,v170,v83
v_mul_f32 v84,v170,v84
v_mul_f32 v85,v170,v85
v_mul_f32 v86,v170,v86
v_mul_f32 v87,v170,v87
v_mul_f32 v88,v170,v88
v_mul_f32 v89,v170,v89
v_mul_f32 v90,v170,v90
v_mul_f32 v91,v170,v91
v_mul_f32 v92,v170,v92
v_mul_f32 v93,v170,v93
v_mul_f32 v94,v170,v94
v_mul_f32 v95,v170,v95
v_cvt_pk_bf16_f32 v96,v64,v65
v_cvt_pk_bf16_f32 v97,v66,v67
v_cvt_pk_bf16_f32 v98,v68,v69
v_cvt_pk_bf16_f32 v99,v70,v71
v_cvt_pk_bf16_f32 v100,v72,v73
v_cvt_pk_bf16_f32 v101,v74,v75
v_cvt_pk_bf16_f32 v102,v76,v77
v_cvt_pk_bf16_f32 v103,v78,v79
v_cvt_pk_bf16_f32 v104,v80,v81
v_cvt_pk_bf16_f32 v105,v82,v83
v_cvt_pk_bf16_f32 v106,v84,v85
v_cvt_pk_bf16_f32 v107,v86,v87
v_cvt_pk_bf16_f32 v108,v88,v89
v_cvt_pk_bf16_f32 v109,v90,v91
v_cvt_pk_bf16_f32 v110,v92,v93
v_cvt_pk_bf16_f32 v111,v94,v95
buffer_store_dwordx4 v[96:99],v169,s[36:39],s55 offen sc1
buffer_store_dwordx4 v[100:103],v169,s[36:39],s55 offen offset:1024 sc1
buffer_store_dwordx4 v[104:107],v169,s[36:39],s55 offen offset:2048 sc1
buffer_store_dwordx4 v[108:111],v169,s[36:39],s55 offen offset:3072 sc1
s_waitcnt vmcnt(0)
s_add_u32 s89,s89,1
s_cmp_lg_u32 s89,4
s_cbranch_scc1 L_sjb_%=
s_mov_b32 s98,s88
v_readlane_b32 s88,v173,0
v_readlane_b32 s89,v173,1
v_readlane_b32 s90,v173,2
v_readlane_b32 s91,v173,3
v_readlane_b32 s92,v173,4
v_readlane_b32 s93,v173,5
v_readlane_b32 s94,v173,6
v_readlane_b32 s95,v173,7
s_nop 4
s_mov_b32 s96,0
buffer_load_dwordx4 v[64:67],v6,s[56:59],s96 offen sc1
buffer_load_dwordx4 v[68:71],v6,s[56:59],s96 offen offset:1024 sc1
buffer_load_dwordx4 v[72:75],v6,s[56:59],s96 offen offset:2048 sc1
buffer_load_dwordx4 v[76:79],v6,s[56:59],s96 offen offset:3072 sc1
s_mov_b32 s96,4096
buffer_load_dwordx4 v[80:83],v6,s[56:59],s96 offen sc1
buffer_load_dwordx4 v[84:87],v6,s[56:59],s96 offen offset:1024 sc1
buffer_load_dwordx4 v[88:91],v6,s[56:59],s96 offen offset:2048 sc1
buffer_load_dwordx4 v[92:95],v6,s[56:59],s96 offen offset:3072 sc1
s_mov_b32 s96,8192
buffer_load_dwordx4 v[96:99],v6,s[56:59],s96 offen sc1
buffer_load_dwordx4 v[100:103],v6,s[56:59],s96 offen offset:1024 sc1
buffer_load_dwordx4 v[104:107],v6,s[56:59],s96 offen offset:2048 sc1
buffer_load_dwordx4 v[108:111],v6,s[56:59],s96 offen offset:3072 sc1
s_mov_b32 s96,12288
buffer_load_dwordx4 v[112:115],v6,s[56:59],s96 offen sc1
buffer_load_dwordx4 v[116:119],v6,s[56:59],s96 offen offset:1024 sc1
buffer_load_dwordx4 v[120:123],v6,s[56:59],s96 offen offset:2048 sc1
buffer_load_dwordx4 v[124:127],v6,s[56:59],s96 offen offset:3072 sc1
s_mov_b32 s96,16384
buffer_load_dwordx4 v[128:131],v6,s[56:59],s96 offen sc1
buffer_load_dwordx4 v[132:135],v6,s[56:59],s96 offen offset:1024 sc1
buffer_load_dwordx4 v[136:139],v6,s[56:59],s96 offen offset:2048 sc1
buffer_load_dwordx4 v[140:143],v6,s[56:59],s96 offen offset:3072 sc1
s_mov_b32 s96,20480
buffer_load_dwordx4 v[144:147],v6,s[56:59],s96 offen sc1
buffer_load_dwordx4 v[148:151],v6,s[56:59],s96 offen offset:1024 sc1
buffer_load_dwordx4 v[152:155],v6,s[56:59],s96 offen offset:2048 sc1
buffer_load_dwordx4 v[156:159],v6,s[56:59],s96 offen offset:3072 sc1
s_mov_b32 s96,24576
buffer_load_dwordx4 v[160:163],v6,s[56:59],s96 offen sc1
buffer_load_dwordx4 v[164:167],v6,s[56:59],s96 offen offset:1024 sc1
buffer_load_dwordx4 v[168:171],v6,s[56:59],s96 offen offset:2048 sc1
buffer_load_dwordx4 v[172:175],v6,s[56:59],s96 offen offset:3072 sc1
s_mov_b32 s96,28672
buffer_load_dwordx4 v[176:179],v6,s[56:59],s96 offen sc1
buffer_load_dwordx4 v[180:183],v6,s[56:59],s96 offen offset:1024 sc1
buffer_load_dwordx4 v[184:187],v6,s[56:59],s96 offen offset:2048 sc1
buffer_load_dwordx4 v[188:191],v6,s[56:59],s96 offen offset:3072 sc1
s_waitcnt vmcnt(0)
s_cmp_lg_u32 s98,0
s_cbranch_scc1 L_sr1_%=
buffer_load_dwordx4 v[64:67],v6,s[36:39],s84 offen sc1
buffer_load_dwordx4 v[68:71],v6,s[36:39],s84 offen offset:1024 sc1
buffer_load_dwordx4 v[72:75],v6,s[36:39],s84 offen offset:2048 sc1
buffer_load_dwordx4 v[76:79],v6,s[36:39],s84 offen offset:3072 sc1
buffer_load_dwordx4 v[80:83],v6,s[36:39],s85 offen sc1
buffer_load_dwordx4 v[84:87],v6,s[36:39],s85 offen offset:1024 sc1
buffer_load_dwordx4 v[88:91],v6,s[36:39],s85 offen offset:2048 sc1
buffer_load_dwordx4 v[92:95],v6,s[36:39],s85 offen offset:3072 sc1
buffer_load_dwordx4 v[96:99],v6,s[36:39],s86 offen sc1
buffer_load_dwordx4 v[100:103],v6,s[36:39],s86 offen offset:1024 sc1
buffer_load_dwordx4 v[104:107],v6,s[36:39],s86 offen offset:2048 sc1
buffer_load_dwordx4 v[108:111],v6,s[36:39],s86 offen offset:3072 sc1
buffer_load_dwordx4 v[112:115],v6,s[36:39],s87 offen sc1
buffer_load_dwordx4 v[116:119],v6,s[36:39],s87 offen offset:1024 sc1
buffer_load_dwordx4 v[120:123],v6,s[36:39],s87 offen offset:2048 sc1
buffer_load_dwordx4 v[124:127],v6,s[36:39],s87 offen offset:3072 sc1
s_waitcnt vmcnt(0)
s_nop 7
)A"
R"A(s_branch L_sret0_%=
L_sr1_%=:
buffer_load_dwordx4 v[132:135],v6,s[36:39],s84 offen sc1
buffer_load_dwordx4 v[140:143],v6,s[36:39],s84 offen offset:1024 sc1
buffer_load_dwordx4 v[148:151],v6,s[36:39],s84 offen offset:2048 sc1
buffer_load_dwordx4 v[156:159],v6,s[36:39],s84 offen offset:3072 sc1
buffer_load_dwordx4 v[164:167],v6,s[36:39],s85 offen sc1
buffer_load_dwordx4 v[172:175],v6,s[36:39],s85 offen offset:1024 sc1
buffer_load_dwordx4 v[180:183],v6,s[36:39],s85 offen offset:2048 sc1
buffer_load_dwordx4 v[188:191],v6,s[36:39],s85 offen offset:3072 sc1
buffer_load_dwordx4 v[196:199],v6,s[36:39],s86 offen sc1
buffer_load_dwordx4 v[204:207],v6,s[36:39],s86 offen offset:1024 sc1
buffer_load_dwordx4 v[212:215],v6,s[36:39],s86 offen offset:2048 sc1
buffer_load_dwordx4 v[220:223],v6,s[36:39],s86 offen offset:3072 sc1
buffer_load_dwordx4 v[228:231],v6,s[36:39],s87 offen sc1
buffer_load_dwordx4 v[236:239],v6,s[36:39],s87 offen offset:1024 sc1
buffer_load_dwordx4 v[244:247],v6,s[36:39],s87 offen offset:2048 sc1
buffer_load_dwordx4 v[252:255],v6,s[36:39],s87 offen offset:3072 sc1
s_waitcnt vmcnt(0)
s_nop 7
s_branch L_sret1_%=
L_end_%=:
)A"
    :: "s"(karg), "s"(blk), "s"(wid)
    : "v0", "v1", "v2", "v3", "v4", "v5", "v6", "v7", "v8", "v9", "v10", "v11", "v12", "v13", "v14", "v15", "v16", "v17", "v18", "v19", "v20", "v21", "v22", "v23", "v24", "v25", "v26", "v27", "v28", "v29", "v30", "v31", "v32", "v33", "v34", "v35", "v36", "v37", "v38", "v39", "v40", "v41", "v42", "v43", "v44", "v45", "v46", "v47", "v48", "v49", "v50", "v51", "v52", "v53", "v54", "v55", "v56", "v57", "v58", "v59", "v60", "v61", "v62", "v63", "v64", "v65", "v66", "v67", "v68", "v69", "v70", "v71", "v72", "v73", "v74", "v75", "v76", "v77", "v78", "v79", "v80", "v81", "v82", "v83", "v84", "v85", "v86", "v87", "v88", "v89", "v90", "v91", "v92", "v93", "v94", "v95", "v96", "v97", "v98", "v99", "v100", "v101", "v102", "v103", "v104", "v105", "v106", "v107", "v108", "v109", "v110", "v111", "v112", "v113", "v114", "v115", "v116", "v117", "v118", "v119", "v120", "v121", "v122", "v123", "v124", "v125", "v126", "v127", "v128", "v129", "v130", "v131", "v132", "v133", "v134", "v135", "v136", "v137", "v138", "v139", "v140", "v141", "v142", "v143", "v144", "v145", "v146", "v147", "v148", "v149", "v150", "v151", "v152", "v153", "v154", "v155", "v156", "v157", "v158", "v159", "v160", "v161", "v162", "v163", "v164", "v165", "v166", "v167", "v168", "v169", "v170", "v171", "v172", "v173", "v174", "v175", "v176", "v177", "v178", "v179", "v180", "v181", "v182", "v183", "v184", "v185", "v186", "v187", "v188", "v189", "v190", "v191", "v192", "v193", "v194", "v195", "v196", "v197", "v198", "v199", "v200", "v201", "v202", "v203", "v204", "v205", "v206", "v207", "v208", "v209", "v210", "v211", "v212", "v213", "v214", "v215", "v216", "v217", "v218", "v219", "v220", "v221", "v222", "v223", "v224", "v225", "v226", "v227", "v228", "v229", "v230", "v231", "v232", "v233", "v234", "v235", "v236", "v237", "v238", "v239", "v240", "v241", "v242", "v243", "v244", "v245", "v246", "v247", "v248", "v249", "v250", "v251", "v252", "v253", "v254", "v255", "a0", "a1", "a2", "a3", "a4", "a5", "a6", "a7", "a8", "a9", "a10", "a11", "a12", "a13", "a14", "a15", "a16", "a17", "a18", "a19", "a20", "a21", "a22", "a23", "a24", "a25", "a26", "a27", "a28", "a29", "a30", "a31", "a32", "a33", "a34", "a35", "a36", "a37", "a38", "a39", "a40", "a41", "a42", "a43", "a44", "a45", "a46", "a47", "a48", "a49", "a50", "a51", "a52", "a53", "a54", "a55", "a56", "a57", "a58", "a59", "a60", "a61", "a62", "a63", "a64", "a65", "a66", "a67", "a68", "a69", "a70", "a71", "a72", "a73", "a74", "a75", "a76", "a77", "a78", "a79", "a80", "a81", "a82", "a83", "a84", "a85", "a86", "a87", "a88", "a89", "a90", "a91", "a92", "a93", "a94", "a95", "a96", "a97", "a98", "a99", "a100", "a101", "a102", "a103", "a104", "a105", "a106", "a107", "a108", "a109", "a110", "a111", "a112", "a113", "a114", "a115", "a116", "a117", "a118", "a119", "a120", "a121", "a122", "a123", "a124", "a125", "a126", "a127", "a128", "a129", "a130", "a131", "a132", "a133", "a134", "a135", "a136", "a137", "a138", "a139", "a140", "a141", "a142", "a143", "a144", "a145", "a146", "a147", "a148", "a149", "a150", "a151", "a152", "a153", "a154", "a155", "a156", "a157", "a158", "a159", "a160", "a161", "a162", "a163", "a164", "a165", "a166", "a167", "a168", "a169", "a170", "a171", "a172", "a173", "a174", "a175", "a176", "a177", "a178", "a179", "a180", "a181", "a182", "a183", "a184", "a185", "a186", "a187", "a188", "a189", "a190", "a191", "a192", "a193", "a194", "a195", "a196", "a197", "a198", "a199", "a200", "a201", "a202", "a203", "a204", "a205", "a206", "a207", "a208", "a209", "a210", "a211", "a212", "a213", "a214", "a215", "a216", "a217", "a218", "a219", "a220", "a221", "a222", "a223", "a224", "a225", "a226", "a227", "a228", "a229", "a230", "a231", "a232", "a233", "a234", "a235", "a236", "a237", "a238", "a239", "a240", "a241", "a242", "a243", "a244", "a245", "a246", "a247", "a248", "a249", "a250", "a251", "a252", "a253", "a254", "a255", "s4", "s5", "s6", "s7", "s8", "s9", "s10", "s11", "s12", "s13", "s14", "s15", "s16", "s17", "s18", "s19", "s20", "s21", "s22", "s23", "s24", "s25", "s26", "s27", "s28", "s29", "s30", "s31", "s32", "s33", "s34", "s35", "s36", "s37", "s38", "s39", "s40", "s41", "s42", "s43", "s44", "s45", "s46", "s47", "s48", "s49", "s50", "s51", "s52", "s53", "s54", "s55", "s56", "s57", "s58", "s59", "s60", "s61", "s62", "s63", "s64", "s65", "s66", "s67", "s68", "s69", "s70", "s71", "s72", "s73", "s74", "s75", "s76", "s77", "s78", "s79", "s80", "s81", "s82", "s83", "s84", "s85", "s86", "s87", "s88", "s89", "s90", "s91", "s92", "s93", "s94", "s95", "s96", "s97", "s98", "s99", "s100", "s101", "vcc", "scc", "memory");
#endif
}

extern "C" void kernel_launch(void* const* d_in, const int* in_sizes, int n_in,
                              void* d_out, int out_size, void* d_ws, size_t ws_size,
                              hipStream_t stream) {
  (void)hipFuncSetAttribute((const void*)qattn_kernel, hipFuncAttributeMaxDynamicSharedMemorySize, LDS_ATT);
  qattn_kernel<<<256, 256, LDS_ATT, stream>>>(d_in[0], d_in[1], d_in[2], d_in[3], d_ws, d_in[4], d_out);
}
```

```cpp
#include <hip/hip_runtime.h>
#include <hip/hip_bf16.h>

constexpr int LDS_ATT = 163840;

__global__ __launch_bounds__(256, 1) void qattn_kernel(const void* __restrict__ x, const void* __restrict__ kc, const void* __restrict__ vc,
                                                       const void* __restrict__ wq, void* __restrict__ ws, const void* __restrict__ wo,
                                                       void* __restrict__ out) {
#if defined(__HIP_DEVICE_COMPILE__)
  const auto karg = __builtin_amdgcn_kernarg_segment_ptr();
  const unsigned blk = blockIdx.x;
  const unsigned wid = __builtin_amdgcn_readfirstlane(threadIdx.x >> 6);
  asm volatile(
R"A(s_memtime s[72:73]
s_memrealtime s[74:75]
s_load_dwordx8 s[4:11],%0,0x0
s_load_dwordx4 s[12:15],%0,0x20
s_load_dwordx2 s[16:17],%0,0x30
s_mov_b32 s40,%2
s_mov_b32 s43,%1
s_lshr_b32 s45,s43,3
v_mbcnt_lo_u32_b32 v12,-1,0
v_mbcnt_hi_u32_b32 v12,-1,v12
s_getpc_b64 s[88:89]
L_pc_%=:
s_and_b32 s88,s88,0xffffff80
s_and_b32 s89,s89,0xffff
s_mov_b32 s90,L_fast_end_%=-L_pc_%=
s_mov_b32 s91,0x20000
s_lshl_b32 s44,s45,2
s_add_u32 s44,s44,s40
s_mul_i32 s44,s44,896
v_lshlrev_b32 v67,7,v12
s_mov_b64 exec,127
buffer_load_dword v19,v67,s[88:91],s44 offen
s_mov_b64 exec,-1
s_and_b32 s70,s43,3
s_and_b32 s71,s45,3
s_lshl_b32 s42,s70,2
s_add_u32 s42,s42,s71
s_and_b32 s41,s43,4
s_lshl_b32 s41,s41,1
s_lshr_b32 s46,s45,2
s_add_u32 s41,s41,s46
s_lshl_b32 s56,s42,9
s_mov_b32 s57,0
s_lshl_b32 s59,s40,13
s_add_u32 s58,s59,65536
v_and_b32 v64,3,v12
v_bfe_u32 v65,v12,2,2
v_lshrrev_b32 v66,4,v12
v_lshrrev_b32 v75,3,v12
s_lshl_b32 s44,s40,6
v_add_u32 v75,s44,v75
v_mul_u32_u24 v75,1280,v75
v_and_b32 v76,7,v12
s_add_u32 s84,s70,1
s_and_b32 s84,s84,3
s_mul_i32 s84,s84,20480
s_add_u32 s84,s84,4096
s_add_u32 s85,s70,2
s_and_b32 s85,s85,3
s_mul_i32 s85,s85,20480
s_add_u32 s85,s85,2048
s_add_u32 s86,s70,3
s_and_b32 s86,s86,3
s_mul_i32 s86,s86,20480
s_add_u32 s86,s86,4096
s_add_u32 s87,s70,4
s_and_b32 s87,s87,3
s_mul_i32 s87,s87,20480
s_add_u32 s87,s87,2048
v_or_b32 v77,0,v66
v_xor_b32 v77,v77,v76
v_lshl_add_u32 v77,v77,4,v75
v_add_u32 v48,s84,v77
v_add_u32 v50,s85,v77
v_add_u32 v52,s86,v77
v_add_u32 v54,s87,v77
v_or_b32 v77,4,v66
v_xor_b32 v77,v77,v76
v_lshl_add_u32 v77,v77,4,v75
v_add_u32 v77,9216,v77
v_add_u32 v49,s84,v77
v_add_u32 v51,s85,v77
v_add_u32 v53,s86,v77
v_add_u32 v55,s87,v77
s_lshl_b32 s44,s40,17
v_lshlrev_b32 v75,13,v65
v_lshl_add_u32 v75,v66,6,v75
v_lshl_add_u32 v75,v64,4,v75
v_add_u32 v75,s44,v75
v_add_u32 v56,0,v75
v_add_u32 v57,256,v75
v_add_u32 v58,32768,v75
v_add_u32 v59,33024,v75
v_add_u32 v60,65536,v75
v_add_u32 v61,65792,v75
v_add_u32 v62,98304,v75
v_add_u32 v63,98560,v75
s_mul_i32 s44,s41,307200
s_waitcnt lgkmcnt(0)
s_mov_b64 s[28:29],s[4:5]
s_add_u32 s28,s28,s44
s_addc_u32 s29,s29,0
s_sub_u32 s28,s28,4096
s_subb_u32 s29,s29,0
s_and_b32 s29,s29,0xffff
s_mov_b32 s30,311296
s_mov_b32 s31,0x20000
s_mov_b64 s[32:33],s[10:11]
s_and_b32 s33,s33,0xffff
s_mov_b32 s34,5242880
s_mov_b32 s35,0x20000
s_add_u32 m0,s58,1024
s_nop 0
buffer_load_dwordx4 v57,s[32:35],s56 offen lds
s_add_u32 m0,s58,3072
s_nop 0
buffer_load_dwordx4 v59,s[32:35],s56 offen lds
s_add_u32 m0,s58,5120
s_nop 0
buffer_load_dwordx4 v61,s[32:35],s56 offen lds
s_add_u32 m0,s58,7168
s_nop 0
buffer_load_dwordx4 v63,s[32:35],s56 offen lds
s_add_u32 m0,s59,0
s_nop 0
buffer_load_dwordx4 v48,s[28:31],s57 offen lds
buffer_load_dwordx4 v49,s[28:31],s57 offen offset:1024 lds
buffer_load_dwordx4 v50,s[28:31],s57 offen offset:2048 lds
buffer_load_dwordx4 v51,s[28:31],s57 offen offset:3072 lds
s_add_u32 m0,s59,4096
s_nop 0
buffer_load_dwordx4 v52,s[28:31],s57 offen lds
buffer_load_dwordx4 v53,s[28:31],s57 offen offset:1024 lds
buffer_load_dwordx4 v54,s[28:31],s57 offen offset:2048 lds
buffer_load_dwordx4 v55,s[28:31],s57 offen offset:3072 lds
s_add_u32 s57,s57,128
s_xor_b32 s59,s59,131072
s_add_u32 m0,s58,0
s_nop 0
buffer_load_dwordx4 v56,s[32:35],s56 offen lds
s_add_u32 m0,s58,2048
s_nop 0
buffer_load_dwordx4 v58,s[32:35],s56 offen lds
s_add_u32 m0,s58,4096
s_nop 0
buffer_load_dwordx4 v60,s[32:35],s56 offen lds
s_add_u32 m0,s58,6144
s_nop 0
buffer_load_dwordx4 v62,s[32:35],s56 offen lds
s_add_u32 s56,s56,524288
s_add_u32 s58,s58,32768
s_cmp_ge_u32 s58,131072
s_cselect_b32 s62,98304,0
s_sub_u32 s58,s58,s62
s_add_u32 m0,s58,0
s_nop 0
buffer_load_dwordx4 v56,s[32:35],s56 offen lds
s_add_u32 m0,s58,1024
s_nop 0
buffer_load_dwordx4 v57,s[32:35],s56 offen lds
s_add_u32 m0,s58,2048
s_nop 0
buffer_load_dwordx4 v58,s[32:35],s56 offen lds
s_add_u32 m0,s58,3072
s_nop 0
buffer_load_dwordx4 v59,s[32:35],s56 offen lds
s_add_u32 m0,s58,4096
s_nop 0
buffer_load_dwordx4 v60,s[32:35],s56 offen lds
s_add_u32 m0,s58,5120
s_nop 0
buffer_load_dwordx4 v61,s[32:35],s56 offen lds
s_add_u32 m0,s58,6144
s_nop 0
buffer_load_dwordx4 v62,s[32:35],s56 offen lds
s_add_u32 m0,s58,7168
s_nop 0
buffer_load_dwordx4 v63,s[32:35],s56 offen lds
s_add_u32 s56,s56,524288
s_add_u32 s58,s58,32768
s_cmp_ge_u32 s58,131072
s_cselect_b32 s62,98304,0
s_sub_u32 s58,s58,s62
s_mov_b32 s60,65536
v_lshlrev_b32 v6,4,v12
v_lshlrev_b32 v72,12,v66
v_lshl_add_u32 v72,v65,6,v72
v_lshl_add_u32 v23,v64,4,v72
v_add_u32 v22,65536,v23
v_and_b32 v74,15,v12
v_lshrrev_b32 v76,1,v74
v_lshlrev_b32 v75,7,v74
s_lshl_b32 s44,s40,13
v_add_u32 v75,s44,v75
v_or_b32 v77,0,v66
v_xor_b32 v77,v77,v76
v_lshl_add_u32 v20,v77,4,v75
v_or_b32 v77,4,v66
v_xor_b32 v77,v77,v76
v_lshl_add_u32 v21,v77,4,v75
s_waitcnt vmcnt(12)
s_nop 0
s_memtime s[64:65]
s_memrealtime s[66:67]
s_bfe_u32 s68,%1,327683
s_waitcnt lgkmcnt(0)
s_sub_u32 s64,s64,s72
s_sub_u32 s66,s66,s74
s_mul_i32 s64,s64,10
s_mul_i32 s66,s66,215
s_cmp_lt_u32 s64,s66
s_cbranch_scc1 L_se1_%=
L_sg1_%=:
s_cmp_eq_u32 s68,0
s_cbranch_scc1 L_se1_%=
s_sleep 4
s_sub_u32 s68,s68,1
s_branch L_sg1_%=
L_se1_%=:
s_barrier
ds_read_b128 a[192:195],v20
ds_read_b64_tr_b16 a[128:129],v22 offset:1024
ds_read_b64_tr_b16 a[130:131],v22 offset:3072
ds_read_b128 a[196:199],v21
ds_read_b64_tr_b16 a[132:133],v22 offset:17408
ds_read_b64_tr_b16 a[134:135],v22 offset:19456
ds_read_b128 a[204:207],v21 offset:2048
ds_read_b128 a[200:203],v20 offset:2048
ds_read_b128 a[208:211],v20 offset:4096
ds_read_b128 a[212:215],v21 offset:4096
ds_read_b128 a[220:223],v21 offset:6144
ds_read_b128 a[216:219],v20 offset:6144
ds_read_b64_tr_b16 a[136:137],v22 offset:1032
ds_read_b64_tr_b16 a[138:139],v22 offset:3080
s_waitcnt lgkmcnt(11)
v_mfma_f32_16x16x32_bf16 v[128:131],a[128:131],a[192:195],0
s_add_u32 m0,s59,0
ds_read_b64_tr_b16 a[140:141],v22 offset:17416
ds_read_b64_tr_b16 a[142:143],v22 offset:19464
s_waitcnt lgkmcnt(10)
v_mfma_f32_16x16x32_bf16 v[128:131],a[132:135],a[196:199],v[128:131]
s_waitcnt lgkmcnt(9)
v_mfma_f32_16x16x32_bf16 v[136:139],a[132:135],a[204:207],0
s_waitcnt lgkmcnt(8)
v_mfma_f32_16x16x32_bf16 v[136:139],a[128:131],a[200:203],v[136:139]
s_waitcnt lgkmcnt(7)
v_mfma_f32_16x16x32_bf16 v[144:147],a[128:131],a[208:211],0
s_waitcnt lgkmcnt(6)
buffer_load_dwordx4 v48,s[28:31],s57 offen lds
v_mfma_f32_16x16x32_bf16 v[144:147],a[132:135],a[212:215],v[144:147]
s_waitcnt lgkmcnt(5)
v_mfma_f32_16x16x32_bf16 v[152:155],a[132:135],a[220:223],0
s_waitcnt lgkmcnt(4)
v_mfma_f32_16x16x32_bf16 v[152:155],a[128:131],a[216:219],v[152:155]
s_waitcnt lgkmcnt(2)
ds_read_b64_tr_b16 a[144:145],v22 offset:1280
ds_read_b64_tr_b16 a[146:147],v22 offset:3328
v_mfma_f32_16x16x32_bf16 v[132:135],a[136:139],a[192:195],0
s_waitcnt lgkmcnt(2)
ds_read_b64_tr_b16 a[148:149],v22 offset:17664
ds_read_b64_tr_b16 a[150:151],v22 offset:19712
buffer_load_dwordx4 v49,s[28:31],s57 offen offset:1024 lds
v_mfma_f32_16x16x32_bf16 v[132:135],a[140:143],a[196:199],v[132:135]
v_mfma_f32_16x16x32_bf16 v[140:143],a[140:143],a[204:207],0
v_mfma_f32_16x16x32_bf16 v[140:143],a[136:139],a[200:203],v[140:143]
v_mfma_f32_16x16x32_bf16 v[148:151],a[136:139],a[208:211],0
buffer_load_dwordx4 v50,s[28:31],s57 offen offset:2048 lds
v_mfma_f32_16x16x32_bf16 v[148:151],a[140:143],a[212:215],v[148:151]
v_mfma_f32_16x16x32_bf16 v[156:159],a[140:143],a[220:223],0
v_mfma_f32_16x16x32_bf16 v[156:159],a[136:139],a[216:219],v[156:159]
s_waitcnt lgkmcnt(2)
ds_read_b64_tr_b16 a[152:153],v22 offset:1288
ds_read_b64_tr_b16 a[154:155],v22 offset:3336
v_mfma_f32_16x16x32_bf16 v[160:163],a[144:147],a[192:195],0
s_waitcnt lgkmcnt(2)
buffer_load_dwordx4 v51,s[28:31],s57 offen offset:3072 lds
ds_read_b64_tr_b16 a[156:157],v22 offset:17672
ds_read_b64_tr_b16 a[158:159],v22 offset:19720
v_mfma_f32_16x16x32_bf16 v[160:163],a[148:151],a[196:199],v[160:163]
s_add_u32 m0,s59,4096
v_mfma_f32_16x16x32_bf16 v[168:171],a[148:151],a[204:207],0
v_mfma_f32_16x16x32_bf16 v[168:171],a[144:147],a[200:203],v[168:171]
v_mfma_f32_16x16x32_bf16 v[176:179],a[144:147],a[208:211],0
buffer_load_dwordx4 v52,s[28:31],s57 offen lds
v_mfma_f32_16x16x32_bf16 v[176:179],a[148:151],a[212:215],v[176:179]
v_mfma_f32_16x16x32_bf16 v[184:187],a[148:151],a[220:223],0
v_mfma_f32_16x16x32_bf16 v[184:187],a[144:147],a[216:219],v[184:187]
s_waitcnt lgkmcnt(2)
v_mfma_f32_16x16x32_bf16 v[164:167],a[152:155],a[192:195],0
s_waitcnt lgkmcnt(0)
buffer_load_dwordx4 v53,s[28:31],s57 offen offset:1024 lds
ds_read_b64_tr_b16 a[160:161],v22 offset:1536
ds_read_b64_tr_b16 a[162:163],v22 offset:3584
v_mfma_f32_16x16x32_bf16 v[164:167],a[156:159],a[196:199],v[164:167]
ds_read_b64_tr_b16 a[164:165],v22 offset:17920
ds_read_b64_tr_b16 a[166:167],v22 offset:19968
v_mfma_f32_16x16x32_bf16 v[172:175],a[156:159],a[204:207],0
v_mfma_f32_16x16x32_bf16 v[172:175],a[152:155],a[200:203],v[172:175]
v_mfma_f32_16x16x32_bf16 v[180:183],a[152:155],a[208:211],0
buffer_load_dwordx4 v54,s[28:31],s57 offen offset:2048 lds
v_mfma_f32_16x16x32_bf16 v[180:183],a[156:159],a[212:215],v[180:183]
v_mfma_f32_16x16x32_bf16 v[188:191],a[156:159],a[220:223],0
s_waitcnt vmcnt(15)
v_mfma_f32_16x16x32_bf16 v[188:191],a[152:155],a[216:219],v[188:191]
s_barrier
buffer_load_dwordx4 v55,s[28:31],s57 offen offset:3072 lds
ds_read_b64_tr_b16 a[168:169],v22 offset:1544
ds_read_b64_tr_b16 a[170:171],v22 offset:3592
s_waitcnt lgkmcnt(4)
v_mfma_f32_16x16x32_bf16 v[192:195],a[160:163],a[192:195],0
s_add_u32 m0,s58,0
ds_read_b64_tr_b16 a[172:173],v22 offset:17928
ds_read_b64_tr_b16 a[174:175],v22 offset:19976
s_waitcnt lgkmcnt(4)
v_mfma_f32_16x16x32_bf16 v[192:195],a[164:167],a[196:199],v[192:195]
v_mfma_f32_16x16x32_bf16 v[200:203],a[164:167],a[204:207],0
s_lshl_b32 s44,s41,13
s_add_u32 s44,s44,104857600
s_mov_b64 s[64:65],s[12:13]
v_mfma_f32_16x16x32_bf16 v[200:203],a[160:163],a[200:203],v[200:203]
s_mov_b32 s66,8192
buffer_load_dwordx4 v56,s[32:35],s56 offen lds
v_mfma_f32_16x16x32_bf16 v[208:211],a[160:163],a[208:211],0
s_add_u32 m0,s58,1024
s_add_u32 s64,s64,s44
s_addc_u32 s65,s65,0
v_mfma_f32_16x16x32_bf16 v[208:211],a[164:167],a[212:215],v[208:211]
s_and_b32 s65,s65,0xffff
s_mov_b32 s67,0x20000
v_mov_b32 v14,4096
v_mfma_f32_16x16x32_bf16 v[216:219],a[164:167],a[220:223],0
s_mov_b64 s[72:73],s[14:15]
s_and_b32 s73,s73,0xffff
ds_read_b64_tr_b16 a[176:177],v22 offset:1792
v_mfma_f32_16x16x32_bf16 v[216:219],a[160:163],a[216:219],v[216:219]
s_mov_b32 s74,5242880
s_mov_b32 s75,0x20000
ds_read_b64_tr_b16 a[178:179],v22 offset:3840
s_waitcnt lgkmcnt(4)
v_mfma_f32_16x16x32_bf16 v[196:199],a[168:171],a[192:195],0
s_waitcnt lgkmcnt(2)
buffer_load_dwordx4 v57,s[32:35],s56 offen lds
ds_read_b64_tr_b16 a[180:181],v22 offset:18176
ds_read_b64_tr_b16 a[182:183],v22 offset:20224
v_mfma_f32_16x16x32_bf16 v[196:199],a[172:175],a[196:199],v[196:199]
s_add_u32 m0,s58,2048
v_lshlrev_b32 v15,3,v6
v_add_u32 v16,128,v15
v_mfma_f32_16x16x32_bf16 v[204:207],a[172:175],a[204:207],0
s_lshl_b32 s83,%1,2
ds_read_b64_tr_b16 a[184:185],v22 offset:1800
v_lshrrev_b32 v16,10,v16
v_mfma_f32_16x16x32_bf16 v[204:207],a[168:171],a[200:203],v[204:207]
s_add_u32 s83,s83,s40
ds_read_b64_tr_b16 a[186:187],v22 offset:3848
v_lshl_add_u32 v15,v16,24,v15
v_mfma_f32_16x16x32_bf16 v[212:215],a[168:171],a[208:211],0
s_mul_i32 s83,s83,5120
buffer_load_dwordx4 v58,s[32:35],s56 offen lds
v_mfma_f32_16x16x32_bf16 v[212:215],a[172:175],a[212:215],v[212:215]
s_add_u32 m0,s58,3072
s_lshl_b32 s43,s41,5
ds_read_b64_tr_b16 a[188:189],v22 offset:18184
v_mfma_f32_16x16x32_bf16 v[220:223],a[172:175],a[220:223],0
s_lshl_b32 s44,s42,1
s_add_u32 s43,s43,s44
ds_read_b64_tr_b16 a[190:191],v22 offset:20232
v_mfma_f32_16x16x32_bf16 v[220:223],a[168:171],a[216:219],v[220:223]
s_lshl_b32 s44,s43,18
s_mov_b64 s[20:21],s[6:7]
ds_read_b64_tr_b16 a[128:129],v22
s_waitcnt lgkmcnt(7)
v_mfma_f32_16x16x32_bf16 v[224:227],a[176:179],a[192:195],0
s_mov_b32 s22,524288
buffer_load_dwordx4 v59,s[32:35],s56 offen lds
s_waitcnt lgkmcnt(5)
v_mfma_f32_16x16x32_bf16 v[224:227],a[180:183],a[196:199],v[224:227]
s_add_u32 m0,s58,4096
s_mov_b32 s23,0x20000
)A"
R"A(ds_read_b64_tr_b16 a[130:131],v22 offset:2048
v_mfma_f32_16x16x32_bf16 v[232:235],a[180:183],a[204:207],0
s_add_u32 s20,s20,s44
s_addc_u32 s21,s21,0
ds_read_b64_tr_b16 a[132:133],v22 offset:16384
v_mfma_f32_16x16x32_bf16 v[232:235],a[176:179],a[200:203],v[232:235]
s_and_b32 s21,s21,0xffff
s_mov_b64 s[24:25],s[8:9]
ds_read_b64_tr_b16 a[134:135],v22 offset:18432
v_mfma_f32_16x16x32_bf16 v[240:243],a[176:179],a[208:211],0
s_mov_b32 s26,524288
buffer_load_dwordx4 v60,s[32:35],s56 offen lds
v_mfma_f32_16x16x32_bf16 v[240:243],a[180:183],a[212:215],v[240:243]
s_add_u32 s24,s24,s44
s_addc_u32 s25,s25,0
ds_read_b64_tr_b16 a[136:137],v22 offset:8
v_mfma_f32_16x16x32_bf16 v[248:251],a[180:183],a[220:223],0
s_add_u32 m0,s58,5120
s_and_b32 s25,s25,0xffff
ds_read_b64_tr_b16 a[138:139],v22 offset:2056
v_mfma_f32_16x16x32_bf16 v[248:251],a[176:179],a[216:219],v[248:251]
s_mov_b32 s27,0x20000
s_lshl_b32 s44,s43,16
ds_read_b64_tr_b16 a[140:141],v22 offset:16392
s_waitcnt lgkmcnt(6)
v_mfma_f32_16x16x32_bf16 v[228:231],a[184:187],a[192:195],0
s_xor_b32 s59,s59,131072
s_mov_b64 s[36:37],s[12:13]
ds_read_b64_tr_b16 a[142:143],v22 offset:18440
v_mfma_f32_16x16x32_bf16 v[228:231],a[188:191],a[196:199],v[228:231]
s_add_u32 s57,s57,128
buffer_load_dwordx4 v61,s[32:35],s56 offen lds
v_mfma_f32_16x16x32_bf16 v[236:239],a[188:191],a[204:207],0
s_add_u32 s36,s36,s44
s_addc_u32 s37,s37,0
ds_read_b64_tr_b16 a[144:145],v22 offset:256
v_mfma_f32_16x16x32_bf16 v[236:239],a[184:187],a[200:203],v[236:239]
s_add_u32 m0,s58,6144
s_and_b32 s37,s37,0xffff
ds_read_b64_tr_b16 a[146:147],v22 offset:2304
v_mfma_f32_16x16x32_bf16 v[244:247],a[184:187],a[208:211],0
s_mov_b32 s38,131072
s_mov_b32 s39,0x20000
ds_read_b64_tr_b16 a[148:149],v22 offset:16640
v_mfma_f32_16x16x32_bf16 v[244:247],a[188:191],a[212:215],v[244:247]
s_lshl_b32 s51,s40,12
s_add_u32 s52,s51,16384
ds_read_b64_tr_b16 a[150:151],v22 offset:18688
v_mfma_f32_16x16x32_bf16 v[252:255],a[188:191],a[220:223],0
s_mul_i32 s47,s41,5
buffer_load_dwordx4 v62,s[32:35],s56 offen lds
v_mfma_f32_16x16x32_bf16 v[252:255],a[184:187],a[216:219],v[252:255]
s_add_u32 s47,s47,s42
s_and_b32 s47,s47,15
ds_read_b64_tr_b16 a[152:153],v22 offset:264
s_waitcnt lgkmcnt(6)
v_mfma_f32_16x16x32_bf16 a[0:3],a[128:131],a[192:195],0
s_add_u32 m0,s58,7168
s_lshl_b32 s47,s47,14
ds_read_b64_tr_b16 a[154:155],v22 offset:2312
v_mfma_f32_16x16x32_bf16 a[0:3],a[132:135],a[196:199],a[0:3]
s_mov_b32 s54,0
s_lshl_b32 s55,s40,14
ds_read_b64_tr_b16 a[156:157],v22 offset:16648
v_mfma_f32_16x16x32_bf16 a[32:35],a[132:135],a[204:207],0
s_mov_b32 s61,0x3e0293ee
s_mov_b32 s63,0x5d800000
ds_read_b64_tr_b16 a[158:159],v22 offset:18696
v_mfma_f32_16x16x32_bf16 a[32:35],a[128:131],a[200:203],a[32:35]
s_lshl_b32 s44,s40,2
buffer_load_dwordx4 v63,s[32:35],s56 offen lds
v_mfma_f32_16x16x32_bf16 a[64:67],a[128:131],a[208:211],0
s_add_u32 s44,s44,s70
s_mov_b32 s46,0xff800000
ds_read_b64_tr_b16 a[160:161],v22 offset:512
v_mfma_f32_16x16x32_bf16 a[64:67],a[132:135],a[212:215],a[64:67]
s_add_u32 m0,s59,0
ds_read_b64_tr_b16 a[162:163],v22 offset:2560
v_lshlrev_b32 v70,8,v65
v_mfma_f32_16x16x32_bf16 a[96:99],a[132:135],a[220:223],0
s_cmp_eq_u32 s44,14
s_cselect_b32 s18,0x7149f2ca,s46
ds_read_b64_tr_b16 a[164:165],v22 offset:16896
v_mfma_f32_16x16x32_bf16 a[96:99],a[128:131],a[216:219],a[96:99]
s_cmp_eq_u32 s44,13
s_cselect_b32 s19,0x7149f2ca,s46
ds_read_b64_tr_b16 a[166:167],v22 offset:18944
v_mfma_f32_16x16x32_bf16 a[4:7],a[136:139],a[192:195],0
s_add_u32 s60,s60,32768
buffer_load_dwordx4 v48,s[28:31],s57 offen lds
s_waitcnt lgkmcnt(6)
v_mfma_f32_16x16x32_bf16 a[4:7],a[140:143],a[196:199],a[4:7]
s_cmp_eq_u32 s44,12
s_cselect_b32 s100,0x7149f2ca,s46
ds_read_b64_tr_b16 a[168:169],v22 offset:520
v_mfma_f32_16x16x32_bf16 a[36:39],a[140:143],a[204:207],0
s_cmp_eq_u32 s44,15
s_cselect_b32 s101,0x7149f2ca,s46
ds_read_b64_tr_b16 a[170:171],v22 offset:2568
v_mfma_f32_16x16x32_bf16 a[36:39],a[136:139],a[200:203],a[36:39]
s_add_u32 s58,s58,32768
ds_read_b64_tr_b16 a[172:173],v22 offset:16904
v_lshl_add_u32 v70,v66,6,v70
v_mfma_f32_16x16x32_bf16 a[68:71],a[136:139],a[208:211],0
s_add_u32 s56,s56,524288
ds_read_b64_tr_b16 a[174:175],v22 offset:18952
v_add_u32 v70,s51,v70
v_mfma_f32_16x16x32_bf16 a[68:71],a[140:143],a[212:215],a[68:71]
s_mul_i32 s81,s70,1310720
buffer_load_dwordx4 v49,s[28:31],s57 offen offset:1024 lds
v_mfma_f32_16x16x32_bf16 a[100:103],a[140:143],a[220:223],0
s_mul_i32 s44,s71,320
ds_read_b64_tr_b16 a[176:177],v22 offset:768
v_lshl_add_u32 v4,v64,4,v70
v_mfma_f32_16x16x32_bf16 a[100:103],a[136:139],a[216:219],a[100:103]
s_add_u32 s81,s81,s44
ds_read_b64_tr_b16 a[178:179],v22 offset:2816
v_xor_b32 v71,2,v64
v_mfma_f32_16x16x32_bf16 a[8:11],a[144:147],a[192:195],0
s_mul_i32 s82,s40,10240
ds_read_b64_tr_b16 a[180:181],v22 offset:17152
v_lshl_add_u32 v5,v71,4,v70
v_mfma_f32_16x16x32_bf16 a[8:11],a[148:151],a[196:199],a[8:11]
s_mul_i32 s44,s40,40960
ds_read_b64_tr_b16 a[182:183],v22 offset:19200
v_lshlrev_b32 v72,10,v65
v_mfma_f32_16x16x32_bf16 a[40:43],a[148:151],a[204:207],0
s_lshr_b32 s92,%1,3
buffer_load_dwordx4 v50,s[28:31],s57 offen offset:2048 lds
v_mfma_f32_16x16x32_bf16 a[40:43],a[144:147],a[200:203],a[40:43]
s_lshl_b32 s92,s92,2
ds_read_b64_tr_b16 a[184:185],v22 offset:776
v_lshl_add_u32 v72,v64,6,v72
v_mfma_f32_16x16x32_bf16 a[72:75],a[144:147],a[208:211],0
s_add_u32 s92,s92,s40
ds_read_b64_tr_b16 a[186:187],v22 offset:2824
v_and_b32 v73,2,v65
v_mfma_f32_16x16x32_bf16 a[72:75],a[148:151],a[212:215],a[72:75]
s_lshl_b32 s92,s92,7
ds_read_b64_tr_b16 a[188:189],v22 offset:17160
v_xor_b32 v73,v73,v66
v_mfma_f32_16x16x32_bf16 a[104:107],a[148:151],a[220:223],0
s_add_u32 s93,s92,L_tailseg_%=-L_pc_%=
ds_read_b64_tr_b16 a[190:191],v22 offset:19208
v_lshl_add_u32 v72,v73,4,v72
v_mfma_f32_16x16x32_bf16 a[104:107],a[144:147],a[216:219],a[104:107]
buffer_load_dwordx4 v51,s[28:31],s57 offen offset:3072 lds
v_mfma_f32_16x16x32_bf16 a[12:15],a[152:155],a[192:195],0
s_waitcnt lgkmcnt(5)
v_add_u32 v1,98304,v72
v_lshlrev_b32 v72,10,v66
v_mfma_f32_16x16x32_bf16 a[12:15],a[156:159],a[196:199],a[12:15]
s_add_u32 m0,s59,4096
v_lshl_add_u32 v72,v65,6,v72
v_lshl_add_u32 v72,v64,3,v72
v_mfma_f32_16x16x32_bf16 a[44:47],a[156:159],a[204:207],0
s_xor_b32 s59,s59,131072
v_add_u32 v72,114688,v72
v_and_b32 v73,1,v66
v_mfma_f32_16x16x32_bf16 a[44:47],a[152:155],a[200:203],a[44:47]
s_cmp_eq_u32 s60,131072
s_cselect_b32 s60,32768,s60
v_lshl_add_u32 v2,v73,5,v72
v_mfma_f32_16x16x32_bf16 a[76:79],a[152:155],a[208:211],0
buffer_load_dwordx4 v52,s[28:31],s57 offen lds
v_mfma_f32_16x16x32_bf16 a[76:79],a[156:159],a[212:215],a[76:79]
v_xor_b32 v73,1,v73
v_add_u32 v22,s60,v23
v_mfma_f32_16x16x32_bf16 a[108:111],a[156:159],a[220:223],0
s_add_u32 s60,s60,32768
v_xor_b32 v20,131072,v20
v_xor_b32 v21,131072,v21
v_mfma_f32_16x16x32_bf16 a[108:111],a[152:155],a[216:219],a[108:111]
s_waitcnt vmcnt(13) lgkmcnt(0)
v_lshl_add_u32 v3,v73,5,v72
v_mov_b32 v73,0x3f803f80
v_mfma_f32_16x16x32_bf16 a[16:19],a[160:163],a[192:195],0
s_barrier
buffer_load_dwordx4 v53,s[28:31],s57 offen offset:1024 lds
v_mfma_f32_16x16x32_bf16 a[16:19],a[164:167],a[196:199],a[16:19]
s_cmp_ge_u32 s58,131072
s_cselect_b32 s62,98304,0
ds_read_b128 a[224:227],v20
v_mfma_f32_16x16x32_bf16 a[48:51],a[164:167],a[204:207],0
s_sub_u32 s58,s58,s62
ds_read_b64_tr_b16 a[128:129],v22 offset:1024
v_add_u32 v70,0,v12
v_mfma_f32_16x16x32_bf16 a[48:51],a[160:163],a[200:203],a[48:51]
ds_read_b64_tr_b16 a[130:131],v22 offset:3072
v_mul_u32_u24 v71,3277,v70
v_mfma_f32_16x16x32_bf16 a[80:83],a[160:163],a[208:211],0
ds_read_b128 a[228:231],v21
v_lshrrev_b32 v71,16,v71
v_mfma_f32_16x16x32_bf16 a[80:83],a[164:167],a[212:215],a[80:83]
buffer_load_dwordx4 v54,s[28:31],s57 offen offset:2048 lds
v_mfma_f32_16x16x32_bf16 a[112:115],a[164:167],a[220:223],0
ds_read_b64_tr_b16 a[132:133],v22 offset:17408
v_mul_u32_u24 v72,20,v71
v_mfma_f32_16x16x32_bf16 a[112:115],a[160:163],a[216:219],a[112:115]
ds_read_b64_tr_b16 a[134:135],v22 offset:19456
v_sub_u32 v72,v70,v72
v_mfma_f32_16x16x32_bf16 a[20:23],a[168:171],a[192:195],0
ds_read_b128 a[232:235],v20 offset:2048
v_add_u32 v70,64,v12
v_mfma_f32_16x16x32_bf16 a[20:23],a[172:175],a[196:199],a[20:23]
ds_read_b128 a[236:239],v21 offset:2048
v_lshlrev_b32 v24,10,v6
v_mfma_f32_16x16x32_bf16 a[52:55],a[172:175],a[204:207],0
buffer_load_dwordx4 v55,s[28:31],s57 offen offset:3072 lds
v_mfma_f32_16x16x32_bf16 a[52:55],a[168:171],a[200:203],a[52:55]
s_add_u32 s57,s57,128
ds_read_b128 a[240:243],v20 offset:4096
v_mfma_f32_16x16x32_bf16 a[84:87],a[168:171],a[208:211],0
s_cmp_eq_u32 s60,131072
s_cselect_b32 s60,32768,s60
ds_read_b128 a[244:247],v21 offset:4096
v_mfma_f32_16x16x32_bf16 a[84:87],a[172:175],a[212:215],a[84:87]
ds_read_b128 a[248:251],v20 offset:6144
v_xor_b32 v20,131072,v20
v_mfma_f32_16x16x32_bf16 a[116:119],a[172:175],a[220:223],0
buffer_load_dword v13,v14,s[64:67],0 offen sc1
v_mfma_f32_16x16x32_bf16 a[116:119],a[168:171],a[216:219],a[116:119]
ds_read_b128 a[252:255],v21 offset:6144
v_xor_b32 v21,131072,v21
v_mfma_f32_16x16x32_bf16 a[24:27],a[176:179],a[192:195],0
ds_read_b64_tr_b16 a[136:137],v22 offset:1032
v_mfma_f32_16x16x32_bf16 a[24:27],a[180:183],a[196:199],a[24:27]
ds_read_b64_tr_b16 a[138:139],v22 offset:3080
v_mfma_f32_16x16x32_bf16 a[56:59],a[180:183],a[204:207],0
ds_read_b64_tr_b16 a[140:141],v22 offset:17416
v_mfma_f32_16x16x32_bf16 a[56:59],a[176:179],a[200:203],a[56:59]
ds_read_b64_tr_b16 a[142:143],v22 offset:19464
v_mfma_f32_16x16x32_bf16 a[88:91],a[176:179],a[208:211],0
ds_read_b64_tr_b16 a[144:145],v22 offset:1280
v_mfma_f32_16x16x32_bf16 a[88:91],a[180:183],a[212:215],a[88:91]
ds_read_b64_tr_b16 a[146:147],v22 offset:3328
v_mfma_f32_16x16x32_bf16 a[120:123],a[180:183],a[220:223],0
ds_read_b64_tr_b16 a[148:149],v22 offset:17664
v_mfma_f32_16x16x32_bf16 a[120:123],a[176:179],a[216:219],a[120:123]
ds_read_b64_tr_b16 a[150:151],v22 offset:19712
v_mfma_f32_16x16x32_bf16 a[28:31],a[184:187],a[192:195],0
ds_read_b64_tr_b16 a[152:153],v22 offset:1288
v_mfma_f32_16x16x32_bf16 a[28:31],a[188:191],a[196:199],a[28:31]
ds_read_b64_tr_b16 a[154:155],v22 offset:3336
ds_read_b64_tr_b16 a[156:157],v22 offset:17672
ds_read_b64_tr_b16 a[158:159],v22 offset:19720
v_mfma_f32_16x16x32_bf16 a[60:63],a[188:191],a[204:207],0
v_cmp_gt_u32 vcc,2,v64
s_nop 1
v_cndmask_b32 v28,0,v73,vcc
v_mfma_f32_16x16x32_bf16 a[60:63],a[184:187],a[200:203],a[60:63]
v_and_b32 v73,1,v64
v_mul_u32_u24 v73,0xaaaa,v73
v_mfma_f32_16x16x32_bf16 a[92:95],a[184:187],a[208:211],0
v_add_u32 v73,0x4444,v73
v_lshl_or_b32 v27,v73,16,v73
v_mfma_f32_16x16x32_bf16 a[92:95],a[188:191],a[212:215],a[92:95]
v_mov_b32 v29,v28
v_mov_b32 v30,v28
v_mfma_f32_16x16x32_bf16 a[124:127],a[188:191],a[220:223],0
s_add_u32 m0,s58,0
v_mov_b32 v31,v28
v_bfe_u32 v73,v71,2,1
v_mfma_f32_16x16x32_bf16 a[124:127],a[184:187],a[216:219],a[124:127]
s_waitcnt lgkmcnt(4)
v_lshlrev_b32 v73,1,v73
v_xor_b32 v72,v72,v73
v_mfma_f32_16x16x32_bf16 v[128:131],a[128:131],a[224:227],v[128:131]
v_mul_u32_u24 v71,1280,v71
v_lshl_add_u32 v71,v72,4,v71
v_mfma_f32_16x16x32_bf16 v[128:131],a[132:135],a[228:231],v[128:131]
buffer_load_dwordx4 v56,s[32:35],s56 offen lds
v_mfma_f32_16x16x32_bf16 v[136:139],a[132:135],a[236:239],v[136:139]
v_add_u32 v71,s44,v71
v_subrev_u32 v7,0,v71
v_mfma_f32_16x16x32_bf16 v[136:139],a[128:131],a[232:235],v[136:139]
s_add_u32 m0,s58,1024
ds_read_b64_tr_b16 a[160:161],v22 offset:1536
v_mul_u32_u24 v71,3277,v70
v_mfma_f32_16x16x32_bf16 v[144:147],a[128:131],a[240:243],v[144:147]
ds_read_b64_tr_b16 a[162:163],v22 offset:3584
v_lshrrev_b32 v71,16,v71
v_mfma_f32_16x16x32_bf16 v[144:147],a[132:135],a[244:247],v[144:147]
ds_read_b64_tr_b16 a[164:165],v22 offset:17920
v_mul_u32_u24 v72,20,v71
v_mfma_f32_16x16x32_bf16 v[152:155],a[132:135],a[252:255],v[152:155]
buffer_load_dwordx4 v57,s[32:35],s56 offen lds
v_mfma_f32_16x16x32_bf16 v[152:155],a[128:131],a[248:251],v[152:155]
ds_read_b64_tr_b16 a[166:167],v22 offset:19968
v_sub_u32 v72,v70,v72
v_mfma_f32_16x16x32_bf16 v[132:135],a[136:139],a[224:227],v[132:135]
v_bfe_u32 v73,v71,2,1
)A"
R"A(v_lshlrev_b32 v73,1,v73
v_mfma_f32_16x16x32_bf16 v[132:135],a[140:143],a[228:231],v[132:135]
s_add_u32 m0,s58,2048
ds_read_b64_tr_b16 a[168:169],v22 offset:1544
v_xor_b32 v72,v72,v73
v_mfma_f32_16x16x32_bf16 v[140:143],a[140:143],a[236:239],v[140:143]
ds_read_b64_tr_b16 a[170:171],v22 offset:3592
v_mul_u32_u24 v71,1280,v71
v_mfma_f32_16x16x32_bf16 v[140:143],a[136:139],a[232:235],v[140:143]
ds_read_b64_tr_b16 a[172:173],v22 offset:17928
v_lshl_add_u32 v71,v72,4,v71
v_mfma_f32_16x16x32_bf16 v[148:151],a[136:139],a[240:243],v[148:151]
buffer_load_dwordx4 v58,s[32:35],s56 offen lds
v_mfma_f32_16x16x32_bf16 v[148:151],a[140:143],a[244:247],v[148:151]
ds_read_b64_tr_b16 a[174:175],v22 offset:19976
v_add_u32 v71,s44,v71
v_mfma_f32_16x16x32_bf16 v[156:159],a[140:143],a[252:255],v[156:159]
v_subrev_u32 v8,1024,v71
v_add_u32 v70,128,v12
v_mfma_f32_16x16x32_bf16 v[156:159],a[136:139],a[248:251],v[156:159]
s_add_u32 m0,s58,3072
ds_read_b64_tr_b16 a[176:177],v22 offset:1792
v_mul_u32_u24 v71,3277,v70
v_mfma_f32_16x16x32_bf16 v[160:163],a[144:147],a[224:227],v[160:163]
ds_read_b64_tr_b16 a[178:179],v22 offset:3840
v_lshrrev_b32 v71,16,v71
v_mfma_f32_16x16x32_bf16 v[160:163],a[148:151],a[228:231],v[160:163]
ds_read_b64_tr_b16 a[180:181],v22 offset:18176
v_mul_u32_u24 v72,20,v71
v_mfma_f32_16x16x32_bf16 v[168:171],a[148:151],a[236:239],v[168:171]
buffer_load_dwordx4 v59,s[32:35],s56 offen lds
v_mfma_f32_16x16x32_bf16 v[168:171],a[144:147],a[232:235],v[168:171]
ds_read_b64_tr_b16 a[182:183],v22 offset:20224
v_sub_u32 v72,v70,v72
v_mfma_f32_16x16x32_bf16 v[176:179],a[144:147],a[240:243],v[176:179]
s_add_u32 m0,s58,4096
v_bfe_u32 v73,v71,2,1
v_lshlrev_b32 v73,1,v73
v_mfma_f32_16x16x32_bf16 v[176:179],a[148:151],a[244:247],v[176:179]
ds_read_b64_tr_b16 a[184:185],v22 offset:1800
v_xor_b32 v72,v72,v73
v_mfma_f32_16x16x32_bf16 v[184:187],a[148:151],a[252:255],v[184:187]
ds_read_b64_tr_b16 a[186:187],v22 offset:3848
v_mul_u32_u24 v71,1280,v71
v_mfma_f32_16x16x32_bf16 v[184:187],a[144:147],a[248:251],v[184:187]
s_waitcnt lgkmcnt(5)
buffer_load_dwordx4 v60,s[32:35],s56 offen lds
v_mfma_f32_16x16x32_bf16 v[164:167],a[152:155],a[224:227],v[164:167]
ds_read_b64_tr_b16 a[188:189],v22 offset:18184
v_lshl_add_u32 v71,v72,4,v71
v_mfma_f32_16x16x32_bf16 v[164:167],a[156:159],a[228:231],v[164:167]
ds_read_b64_tr_b16 a[190:191],v22 offset:20232
v_add_u32 v71,s44,v71
v_mfma_f32_16x16x32_bf16 v[172:175],a[156:159],a[236:239],v[172:175]
s_add_u32 m0,s58,5120
v_subrev_u32 v9,2048,v71
v_add_u32 v70,192,v12
v_mfma_f32_16x16x32_bf16 v[172:175],a[152:155],a[232:235],v[172:175]
v_mul_u32_u24 v71,3277,v70
v_lshrrev_b32 v71,16,v71
v_mfma_f32_16x16x32_bf16 v[180:183],a[152:155],a[240:243],v[180:183]
ds_read_b64_tr_b16 a[128:129],v22
v_mul_u32_u24 v72,20,v71
v_mfma_f32_16x16x32_bf16 v[180:183],a[156:159],a[244:247],v[180:183]
buffer_load_dwordx4 v61,s[32:35],s56 offen lds
v_mfma_f32_16x16x32_bf16 v[188:191],a[156:159],a[252:255],v[188:191]
ds_read_b64_tr_b16 a[130:131],v22 offset:2048
v_sub_u32 v72,v70,v72
v_mfma_f32_16x16x32_bf16 v[188:191],a[152:155],a[248:251],v[188:191]
ds_read_b64_tr_b16 a[132:133],v22 offset:16384
v_bfe_u32 v73,v71,2,1
v_mfma_f32_16x16x32_bf16 v[192:195],a[160:163],a[224:227],v[192:195]
s_add_u32 m0,s58,6144
ds_read_b64_tr_b16 a[134:135],v22 offset:18432
v_lshlrev_b32 v73,1,v73
v_mfma_f32_16x16x32_bf16 v[192:195],a[164:167],a[228:231],v[192:195]
v_xor_b32 v72,v72,v73
v_mul_u32_u24 v71,1280,v71
v_mfma_f32_16x16x32_bf16 v[200:203],a[164:167],a[236:239],v[200:203]
ds_read_b64_tr_b16 a[136:137],v22 offset:8
v_lshl_add_u32 v71,v72,4,v71
v_mfma_f32_16x16x32_bf16 v[200:203],a[160:163],a[232:235],v[200:203]
buffer_load_dwordx4 v62,s[32:35],s56 offen lds
v_mfma_f32_16x16x32_bf16 v[208:211],a[160:163],a[240:243],v[208:211]
ds_read_b64_tr_b16 a[138:139],v22 offset:2056
v_add_u32 v71,s44,v71
v_mfma_f32_16x16x32_bf16 v[208:211],a[164:167],a[244:247],v[208:211]
s_add_u32 m0,s58,7168
ds_read_b64_tr_b16 a[140:141],v22 offset:16392
v_subrev_u32 v10,3072,v71
v_mfma_f32_16x16x32_bf16 v[216:219],a[164:167],a[252:255],v[216:219]
s_add_u32 s58,s58,32768
ds_read_b64_tr_b16 a[142:143],v22 offset:18440
v_add_u32 v70,256,v12
v_mfma_f32_16x16x32_bf16 v[216:219],a[160:163],a[248:251],v[216:219]
v_mul_u32_u24 v71,3277,v70
v_lshrrev_b32 v71,16,v71
v_mfma_f32_16x16x32_bf16 v[196:199],a[168:171],a[224:227],v[196:199]
buffer_load_dwordx4 v63,s[32:35],s56 offen lds
v_mfma_f32_16x16x32_bf16 v[196:199],a[172:175],a[228:231],v[196:199]
s_add_u32 s56,s56,524288
ds_read_b64_tr_b16 a[144:145],v22 offset:256
v_mul_u32_u24 v72,20,v71
v_mfma_f32_16x16x32_bf16 v[204:207],a[172:175],a[236:239],v[204:207]
ds_read_b64_tr_b16 a[146:147],v22 offset:2304
v_sub_u32 v72,v70,v72
v_mfma_f32_16x16x32_bf16 v[204:207],a[168:171],a[232:235],v[204:207]
s_add_u32 m0,s59,0
ds_read_b64_tr_b16 a[148:149],v22 offset:16640
v_bfe_u32 v73,v71,2,1
v_mfma_f32_16x16x32_bf16 v[212:215],a[168:171],a[240:243],v[212:215]
ds_read_b64_tr_b16 a[150:151],v22 offset:18688
v_lshlrev_b32 v73,1,v73
v_mfma_f32_16x16x32_bf16 v[212:215],a[172:175],a[244:247],v[212:215]
v_xor_b32 v72,v72,v73
v_mul_u32_u24 v71,1280,v71
v_mfma_f32_16x16x32_bf16 v[220:223],a[172:175],a[252:255],v[220:223]
buffer_load_dwordx4 v48,s[28:31],s57 offen lds
v_mfma_f32_16x16x32_bf16 v[220:223],a[168:171],a[248:251],v[220:223]
s_waitcnt lgkmcnt(4)
v_lshl_add_u32 v71,v72,4,v71
v_add_u32 v71,s44,v71
v_mfma_f32_16x16x32_bf16 v[224:227],a[176:179],a[224:227],v[224:227]
ds_read_b64_tr_b16 a[152:153],v22 offset:264
v_subrev_u32 v11,0,v71
v_mfma_f32_16x16x32_bf16 v[224:227],a[180:183],a[228:231],v[224:227]
s_cmp_ge_u32 s58,131072
s_cselect_b32 s62,98304,0
ds_read_b64_tr_b16 a[154:155],v22 offset:2312
v_mfma_f32_16x16x32_bf16 v[232:235],a[180:183],a[236:239],v[232:235]
s_sub_u32 s58,s58,s62
ds_read_b64_tr_b16 a[156:157],v22 offset:16648
v_mfma_f32_16x16x32_bf16 v[232:235],a[176:179],a[232:235],v[232:235]
ds_read_b64_tr_b16 a[158:159],v22 offset:18696
v_mfma_f32_16x16x32_bf16 v[240:243],a[176:179],a[240:243],v[240:243]
buffer_load_dwordx4 v49,s[28:31],s57 offen offset:1024 lds
v_mfma_f32_16x16x32_bf16 v[240:243],a[180:183],a[244:247],v[240:243]
ds_read_b64_tr_b16 a[160:161],v22 offset:512
v_mfma_f32_16x16x32_bf16 v[248:251],a[180:183],a[252:255],v[248:251]
ds_read_b64_tr_b16 a[162:163],v22 offset:2560
v_mfma_f32_16x16x32_bf16 v[248:251],a[176:179],a[248:251],v[248:251]
ds_read_b64_tr_b16 a[164:165],v22 offset:16896
v_mfma_f32_16x16x32_bf16 v[228:231],a[184:187],a[224:227],v[228:231]
ds_read_b64_tr_b16 a[166:167],v22 offset:18944
v_mfma_f32_16x16x32_bf16 v[228:231],a[188:191],a[228:231],v[228:231]
buffer_load_dwordx4 v50,s[28:31],s57 offen offset:2048 lds
v_mfma_f32_16x16x32_bf16 v[236:239],a[188:191],a[236:239],v[236:239]
ds_read_b64_tr_b16 a[168:169],v22 offset:520
v_mfma_f32_16x16x32_bf16 v[236:239],a[184:187],a[232:235],v[236:239]
ds_read_b64_tr_b16 a[170:171],v22 offset:2568
v_mfma_f32_16x16x32_bf16 v[244:247],a[184:187],a[240:243],v[244:247]
ds_read_b64_tr_b16 a[172:173],v22 offset:16904
v_mfma_f32_16x16x32_bf16 v[244:247],a[188:191],a[244:247],v[244:247]
ds_read_b64_tr_b16 a[174:175],v22 offset:18952
v_mfma_f32_16x16x32_bf16 v[252:255],a[188:191],a[252:255],v[252:255]
ds_read_b64_tr_b16 a[176:177],v22 offset:768
v_mfma_f32_16x16x32_bf16 v[252:255],a[184:187],a[248:251],v[252:255]
buffer_load_dwordx4 v51,s[28:31],s57 offen offset:3072 lds
v_mfma_f32_16x16x32_bf16 a[0:3],a[128:131],a[224:227],a[0:3]
ds_read_b64_tr_b16 a[178:179],v22 offset:2816
v_mfma_f32_16x16x32_bf16 a[0:3],a[132:135],a[228:231],a[0:3]
ds_read_b64_tr_b16 a[180:181],v22 offset:17152
v_mfma_f32_16x16x32_bf16 a[32:35],a[132:135],a[236:239],a[32:35]
s_add_u32 m0,s59,4096
s_xor_b32 s59,s59,131072
ds_read_b64_tr_b16 a[182:183],v22 offset:19200
v_mfma_f32_16x16x32_bf16 a[32:35],a[128:131],a[232:235],a[32:35]
ds_read_b64_tr_b16 a[184:185],v22 offset:776
v_mfma_f32_16x16x32_bf16 a[64:67],a[128:131],a[240:243],a[64:67]
ds_read_b64_tr_b16 a[186:187],v22 offset:2824
v_mfma_f32_16x16x32_bf16 a[64:67],a[132:135],a[244:247],a[64:67]
buffer_load_dwordx4 v52,s[28:31],s57 offen lds
v_mfma_f32_16x16x32_bf16 a[96:99],a[132:135],a[252:255],a[96:99]
ds_read_b64_tr_b16 a[188:189],v22 offset:17160
v_mfma_f32_16x16x32_bf16 a[96:99],a[128:131],a[248:251],a[96:99]
ds_read_b64_tr_b16 a[190:191],v22 offset:19208
v_add_u32 v22,s60,v23
v_mfma_f32_16x16x32_bf16 a[4:7],a[136:139],a[224:227],a[4:7]
v_mfma_f32_16x16x32_bf16 a[4:7],a[140:143],a[228:231],a[4:7]
v_mfma_f32_16x16x32_bf16 a[36:39],a[140:143],a[236:239],a[36:39]
buffer_load_dwordx4 v53,s[28:31],s57 offen offset:1024 lds
v_mfma_f32_16x16x32_bf16 a[36:39],a[136:139],a[232:235],a[36:39]
v_mfma_f32_16x16x32_bf16 a[68:71],a[136:139],a[240:243],a[68:71]
v_mfma_f32_16x16x32_bf16 a[68:71],a[140:143],a[244:247],a[68:71]
v_mfma_f32_16x16x32_bf16 a[100:103],a[140:143],a[252:255],a[100:103]
v_mfma_f32_16x16x32_bf16 a[100:103],a[136:139],a[248:251],a[100:103]
s_waitcnt lgkmcnt(0)
v_mfma_f32_16x16x32_bf16 a[8:11],a[144:147],a[224:227],a[8:11]
buffer_load_dwordx4 v54,s[28:31],s57 offen offset:2048 lds
v_mfma_f32_16x16x32_bf16 a[8:11],a[148:151],a[228:231],a[8:11]
v_mfma_f32_16x16x32_bf16 a[40:43],a[148:151],a[236:239],a[40:43]
v_mfma_f32_16x16x32_bf16 a[40:43],a[144:147],a[232:235],a[40:43]
v_mfma_f32_16x16x32_bf16 a[72:75],a[144:147],a[240:243],a[72:75]
v_mfma_f32_16x16x32_bf16 a[72:75],a[148:151],a[244:247],a[72:75]
v_mfma_f32_16x16x32_bf16 a[104:107],a[148:151],a[252:255],a[104:107]
buffer_load_dwordx4 v55,s[28:31],s57 offen offset:3072 lds
v_mfma_f32_16x16x32_bf16 a[104:107],a[144:147],a[248:251],a[104:107]
s_add_u32 s57,s57,128
v_mfma_f32_16x16x32_bf16 a[12:15],a[152:155],a[224:227],a[12:15]
v_mfma_f32_16x16x32_bf16 a[12:15],a[156:159],a[228:231],a[12:15]
v_mfma_f32_16x16x32_bf16 a[44:47],a[156:159],a[236:239],a[44:47]
v_mfma_f32_16x16x32_bf16 a[44:47],a[152:155],a[232:235],a[44:47]
v_mfma_f32_16x16x32_bf16 a[76:79],a[152:155],a[240:243],a[76:79]
v_mfma_f32_16x16x32_bf16 a[76:79],a[156:159],a[244:247],a[76:79]
v_mfma_f32_16x16x32_bf16 a[108:111],a[156:159],a[252:255],a[108:111]
v_mfma_f32_16x16x32_bf16 a[108:111],a[152:155],a[248:251],a[108:111]
s_waitcnt vmcnt(17)
v_mfma_f32_16x16x32_bf16 a[16:19],a[160:163],a[224:227],a[16:19]
s_barrier
ds_read_b128 a[192:195],v20
v_mfma_f32_16x16x32_bf16 a[16:19],a[164:167],a[228:231],a[16:19]
ds_read_b128 a[196:199],v21
v_mfma_f32_16x16x32_bf16 a[48:51],a[164:167],a[236:239],a[48:51]
ds_read_b128 a[200:203],v20 offset:2048
v_mfma_f32_16x16x32_bf16 a[48:51],a[160:163],a[232:235],a[48:51]
ds_read_b128 a[204:207],v21 offset:2048
v_mfma_f32_16x16x32_bf16 a[80:83],a[160:163],a[240:243],a[80:83]
ds_read_b128 a[208:211],v20 offset:4096
v_mfma_f32_16x16x32_bf16 a[80:83],a[164:167],a[244:247],a[80:83]
ds_read_b128 a[212:215],v21 offset:4096
v_mfma_f32_16x16x32_bf16 a[112:115],a[164:167],a[252:255],a[112:115]
ds_read_b128 a[216:219],v20 offset:6144
v_mfma_f32_16x16x32_bf16 a[112:115],a[160:163],a[248:251],a[112:115]
ds_read_b128 a[220:223],v21 offset:6144
v_mfma_f32_16x16x32_bf16 a[20:23],a[168:171],a[224:227],a[20:23]
ds_read_b64_tr_b16 a[128:129],v22 offset:1024
v_mfma_f32_16x16x32_bf16 a[20:23],a[172:175],a[228:231],a[20:23]
ds_read_b64_tr_b16 a[130:131],v22 offset:3072
v_mfma_f32_16x16x32_bf16 a[52:55],a[172:175],a[236:239],a[52:55]
ds_read_b64_tr_b16 a[132:133],v22 offset:17408
v_mfma_f32_16x16x32_bf16 a[52:55],a[168:171],a[232:235],a[52:55]
ds_read_b64_tr_b16 a[134:135],v22 offset:19456
v_mfma_f32_16x16x32_bf16 a[84:87],a[168:171],a[240:243],a[84:87]
ds_read_b64_tr_b16 a[136:137],v22 offset:1032
v_mfma_f32_16x16x32_bf16 a[84:87],a[172:175],a[244:247],a[84:87]
ds_read_b64_tr_b16 a[138:139],v22 offset:3080
v_mfma_f32_16x16x32_bf16 a[116:119],a[172:175],a[252:255],a[116:119]
ds_read_b64_tr_b16 a[140:141],v22 offset:17416
v_mfma_f32_16x16x32_bf16 a[116:119],a[168:171],a[248:251],a[116:119]
ds_read_b64_tr_b16 a[142:143],v22 offset:19464
v_mfma_f32_16x16x32_bf16 a[24:27],a[176:179],a[224:227],a[24:27]
ds_read_b64_tr_b16 a[144:145],v22 offset:1280
v_mfma_f32_16x16x32_bf16 a[24:27],a[180:183],a[228:231],a[24:27]
)A"
R"A(ds_read_b64_tr_b16 a[146:147],v22 offset:3328
v_mfma_f32_16x16x32_bf16 a[56:59],a[180:183],a[236:239],a[56:59]
ds_read_b64_tr_b16 a[148:149],v22 offset:17664
v_mfma_f32_16x16x32_bf16 a[56:59],a[176:179],a[232:235],a[56:59]
ds_read_b64_tr_b16 a[150:151],v22 offset:19712
v_mfma_f32_16x16x32_bf16 a[88:91],a[176:179],a[240:243],a[88:91]
ds_read_b64_tr_b16 a[152:153],v22 offset:1288
v_mfma_f32_16x16x32_bf16 a[88:91],a[180:183],a[244:247],a[88:91]
ds_read_b64_tr_b16 a[154:155],v22 offset:3336
v_mfma_f32_16x16x32_bf16 a[120:123],a[180:183],a[252:255],a[120:123]
ds_read_b64_tr_b16 a[156:157],v22 offset:17672
v_mfma_f32_16x16x32_bf16 a[120:123],a[176:179],a[248:251],a[120:123]
ds_read_b64_tr_b16 a[158:159],v22 offset:19720
v_mfma_f32_16x16x32_bf16 a[28:31],a[184:187],a[224:227],a[28:31]
v_mfma_f32_16x16x32_bf16 a[28:31],a[188:191],a[228:231],a[28:31]
v_mfma_f32_16x16x32_bf16 a[60:63],a[188:191],a[236:239],a[60:63]
v_mfma_f32_16x16x32_bf16 a[60:63],a[184:187],a[232:235],a[60:63]
v_mfma_f32_16x16x32_bf16 a[92:95],a[184:187],a[240:243],a[92:95]
v_mfma_f32_16x16x32_bf16 a[92:95],a[188:191],a[244:247],a[92:95]
v_mfma_f32_16x16x32_bf16 a[124:127],a[188:191],a[252:255],a[124:127]
v_mfma_f32_16x16x32_bf16 a[124:127],a[184:187],a[248:251],a[124:127]
s_mov_b32 s53,3
.p2align 5
L_qp_%=:
s_waitcnt lgkmcnt(1)
v_mfma_f32_16x16x32_bf16 v[128:131],a[128:131],a[192:195],v[128:131]
s_add_u32 m0,s58,0
s_add_u32 s60,s60,32768
ds_read_b64_tr_b16 a[160:161],v22 offset:1536
v_mfma_f32_16x16x32_bf16 v[128:131],a[132:135],a[196:199],v[128:131]
s_mov_b32 s90,L_att0x_%=-L_pc_%=+128
buffer_load_dwordx4 v56,s[32:35],s56 offen lds
v_mfma_f32_16x16x32_bf16 v[136:139],a[132:135],a[204:207],v[136:139]
s_cmp_eq_u32 s60,131072
s_cselect_b32 s60,32768,s60
ds_read_b64_tr_b16 a[162:163],v22 offset:3584
v_mfma_f32_16x16x32_bf16 v[136:139],a[128:131],a[200:203],v[136:139]
s_add_u32 m0,s58,1024
ds_read_b64_tr_b16 a[164:165],v22 offset:17920
v_xor_b32 v20,131072,v20
v_mfma_f32_16x16x32_bf16 v[144:147],a[128:131],a[208:211],v[144:147]
s_sub_u32 s53,s53,1
ds_read_b64_tr_b16 a[166:167],v22 offset:19968
v_xor_b32 v21,131072,v21
v_mfma_f32_16x16x32_bf16 v[144:147],a[132:135],a[212:215],v[144:147]
ds_read_b64_tr_b16 a[168:169],v22 offset:1544
v_mfma_f32_16x16x32_bf16 v[152:155],a[132:135],a[220:223],v[152:155]
buffer_load_dwordx4 v57,s[32:35],s56 offen lds
v_mfma_f32_16x16x32_bf16 v[152:155],a[128:131],a[216:219],v[152:155]
ds_read_b64_tr_b16 a[170:171],v22 offset:3592
v_mfma_f32_16x16x32_bf16 v[132:135],a[136:139],a[192:195],v[132:135]
ds_read_b64_tr_b16 a[172:173],v22 offset:17928
v_mfma_f32_16x16x32_bf16 v[132:135],a[140:143],a[196:199],v[132:135]
s_add_u32 m0,s58,2048
ds_read_b64_tr_b16 a[174:175],v22 offset:19976
v_mfma_f32_16x16x32_bf16 v[140:143],a[140:143],a[204:207],v[140:143]
ds_read_b64_tr_b16 a[176:177],v22 offset:1792
v_mfma_f32_16x16x32_bf16 v[140:143],a[136:139],a[200:203],v[140:143]
ds_read_b64_tr_b16 a[178:179],v22 offset:3840
v_mfma_f32_16x16x32_bf16 v[148:151],a[136:139],a[208:211],v[148:151]
buffer_load_dwordx4 v58,s[32:35],s56 offen lds
v_mfma_f32_16x16x32_bf16 v[148:151],a[140:143],a[212:215],v[148:151]
ds_read_b64_tr_b16 a[180:181],v22 offset:18176
v_mfma_f32_16x16x32_bf16 v[156:159],a[140:143],a[220:223],v[156:159]
s_add_u32 m0,s58,3072
ds_read_b64_tr_b16 a[182:183],v22 offset:20224
v_mfma_f32_16x16x32_bf16 v[156:159],a[136:139],a[216:219],v[156:159]
ds_read_b64_tr_b16 a[184:185],v22 offset:1800
v_mfma_f32_16x16x32_bf16 v[160:163],a[144:147],a[192:195],v[160:163]
ds_read_b64_tr_b16 a[186:187],v22 offset:3848
v_mfma_f32_16x16x32_bf16 v[160:163],a[148:151],a[196:199],v[160:163]
buffer_load_dwordx4 v59,s[32:35],s56 offen lds
v_mfma_f32_16x16x32_bf16 v[168:171],a[148:151],a[204:207],v[168:171]
ds_read_b64_tr_b16 a[188:189],v22 offset:18184
v_mfma_f32_16x16x32_bf16 v[168:171],a[144:147],a[200:203],v[168:171]
ds_read_b64_tr_b16 a[190:191],v22 offset:20232
v_mfma_f32_16x16x32_bf16 v[176:179],a[144:147],a[208:211],v[176:179]
s_add_u32 m0,s58,4096
ds_read_b64_tr_b16 a[128:129],v22
v_mfma_f32_16x16x32_bf16 v[176:179],a[148:151],a[212:215],v[176:179]
ds_read_b64_tr_b16 a[130:131],v22 offset:2048
v_mfma_f32_16x16x32_bf16 v[184:187],a[148:151],a[220:223],v[184:187]
ds_read_b64_tr_b16 a[132:133],v22 offset:16384
v_mfma_f32_16x16x32_bf16 v[184:187],a[144:147],a[216:219],v[184:187]
buffer_load_dwordx4 v60,s[32:35],s56 offen lds
v_mfma_f32_16x16x32_bf16 v[164:167],a[152:155],a[192:195],v[164:167]
s_waitcnt lgkmcnt(5)
ds_read_b64_tr_b16 a[134:135],v22 offset:18432
v_mfma_f32_16x16x32_bf16 v[164:167],a[156:159],a[196:199],v[164:167]
s_add_u32 m0,s58,5120
ds_read_b64_tr_b16 a[136:137],v22 offset:8
v_mfma_f32_16x16x32_bf16 v[172:175],a[156:159],a[204:207],v[172:175]
ds_read_b64_tr_b16 a[138:139],v22 offset:2056
v_mfma_f32_16x16x32_bf16 v[172:175],a[152:155],a[200:203],v[172:175]
ds_read_b64_tr_b16 a[140:141],v22 offset:16392
v_mfma_f32_16x16x32_bf16 v[180:183],a[152:155],a[208:211],v[180:183]
buffer_load_dwordx4 v61,s[32:35],s56 offen lds
v_mfma_f32_16x16x32_bf16 v[180:183],a[156:159],a[212:215],v[180:183]
ds_read_b64_tr_b16 a[142:143],v22 offset:18440
v_mfma_f32_16x16x32_bf16 v[188:191],a[156:159],a[220:223],v[188:191]
ds_read_b64_tr_b16 a[144:145],v22 offset:256
v_mfma_f32_16x16x32_bf16 v[188:191],a[152:155],a[216:219],v[188:191]
s_add_u32 m0,s58,6144
ds_read_b64_tr_b16 a[146:147],v22 offset:2304
v_mfma_f32_16x16x32_bf16 v[192:195],a[160:163],a[192:195],v[192:195]
ds_read_b64_tr_b16 a[148:149],v22 offset:16640
v_mfma_f32_16x16x32_bf16 v[192:195],a[164:167],a[196:199],v[192:195]
ds_read_b64_tr_b16 a[150:151],v22 offset:18688
v_mfma_f32_16x16x32_bf16 v[200:203],a[164:167],a[204:207],v[200:203]
buffer_load_dwordx4 v62,s[32:35],s56 offen lds
v_mfma_f32_16x16x32_bf16 v[200:203],a[160:163],a[200:203],v[200:203]
ds_read_b64_tr_b16 a[152:153],v22 offset:264
v_mfma_f32_16x16x32_bf16 v[208:211],a[160:163],a[208:211],v[208:211]
s_add_u32 m0,s58,7168
s_add_u32 s58,s58,32768
ds_read_b64_tr_b16 a[154:155],v22 offset:2312
v_mfma_f32_16x16x32_bf16 v[208:211],a[164:167],a[212:215],v[208:211]
s_cmp_ge_u32 s58,131072
s_cselect_b32 s62,98304,0
ds_read_b64_tr_b16 a[156:157],v22 offset:16648
v_mfma_f32_16x16x32_bf16 v[216:219],a[164:167],a[220:223],v[216:219]
s_sub_u32 s58,s58,s62
ds_read_b64_tr_b16 a[158:159],v22 offset:18696
v_mfma_f32_16x16x32_bf16 v[216:219],a[160:163],a[216:219],v[216:219]
buffer_load_dwordx4 v63,s[32:35],s56 offen lds
v_mfma_f32_16x16x32_bf16 v[196:199],a[168:171],a[192:195],v[196:199]
s_add_u32 s56,s56,524288
ds_read_b64_tr_b16 a[160:161],v22 offset:512
v_mfma_f32_16x16x32_bf16 v[196:199],a[172:175],a[196:199],v[196:199]
ds_read_b64_tr_b16 a[162:163],v22 offset:2560
v_mfma_f32_16x16x32_bf16 v[204:207],a[172:175],a[204:207],v[204:207]
s_add_u32 m0,s59,0
ds_read_b64_tr_b16 a[164:165],v22 offset:16896
v_mfma_f32_16x16x32_bf16 v[204:207],a[168:171],a[200:203],v[204:207]
ds_read_b64_tr_b16 a[166:167],v22 offset:18944
v_mfma_f32_16x16x32_bf16 v[212:215],a[168:171],a[208:211],v[212:215]
v_mfma_f32_16x16x32_bf16 v[212:215],a[172:175],a[212:215],v[212:215]
buffer_load_dwordx4 v48,s[28:31],s57 offen lds
v_mfma_f32_16x16x32_bf16 v[220:223],a[172:175],a[220:223],v[220:223]
ds_read_b64_tr_b16 a[172:173],v22 offset:16904
v_mfma_f32_16x16x32_bf16 v[220:223],a[168:171],a[216:219],v[220:223]
ds_read_b64_tr_b16 a[168:169],v22 offset:520
v_mfma_f32_16x16x32_bf16 v[224:227],a[176:179],a[192:195],v[224:227]
ds_read_b64_tr_b16 a[170:171],v22 offset:2568
v_mfma_f32_16x16x32_bf16 v[224:227],a[180:183],a[196:199],v[224:227]
ds_read_b64_tr_b16 a[174:175],v22 offset:18952
v_mfma_f32_16x16x32_bf16 v[232:235],a[180:183],a[204:207],v[232:235]
buffer_load_dwordx4 v49,s[28:31],s57 offen offset:1024 lds
v_mfma_f32_16x16x32_bf16 v[232:235],a[176:179],a[200:203],v[232:235]
v_mfma_f32_16x16x32_bf16 v[240:243],a[176:179],a[208:211],v[240:243]
v_mfma_f32_16x16x32_bf16 v[240:243],a[180:183],a[212:215],v[240:243]
v_mfma_f32_16x16x32_bf16 v[248:251],a[180:183],a[220:223],v[248:251]
ds_read_b64_tr_b16 a[180:181],v22 offset:17152
v_mfma_f32_16x16x32_bf16 v[248:251],a[176:179],a[216:219],v[248:251]
ds_read_b64_tr_b16 a[176:177],v22 offset:768
v_mfma_f32_16x16x32_bf16 v[228:231],a[184:187],a[192:195],v[228:231]
s_waitcnt lgkmcnt(3)
buffer_load_dwordx4 v50,s[28:31],s57 offen offset:2048 lds
v_mfma_f32_16x16x32_bf16 v[228:231],a[188:191],a[196:199],v[228:231]
ds_read_b64_tr_b16 a[178:179],v22 offset:2816
v_mfma_f32_16x16x32_bf16 v[236:239],a[188:191],a[204:207],v[236:239]
ds_read_b64_tr_b16 a[182:183],v22 offset:19200
v_mfma_f32_16x16x32_bf16 v[236:239],a[184:187],a[200:203],v[236:239]
v_mfma_f32_16x16x32_bf16 v[244:247],a[184:187],a[208:211],v[244:247]
v_mfma_f32_16x16x32_bf16 v[244:247],a[188:191],a[212:215],v[244:247]
buffer_load_dwordx4 v51,s[28:31],s57 offen offset:3072 lds
v_mfma_f32_16x16x32_bf16 v[252:255],a[188:191],a[220:223],v[252:255]
ds_read_b64_tr_b16 a[188:189],v22 offset:17160
v_mfma_f32_16x16x32_bf16 v[252:255],a[184:187],a[216:219],v[252:255]
ds_read_b64_tr_b16 a[184:185],v22 offset:776
v_mfma_f32_16x16x32_bf16 a[0:3],a[128:131],a[192:195],a[0:3]
s_add_u32 m0,s59,4096
s_xor_b32 s59,s59,131072
ds_read_b64_tr_b16 a[186:187],v22 offset:2824
v_mfma_f32_16x16x32_bf16 a[0:3],a[132:135],a[196:199],a[0:3]
ds_read_b64_tr_b16 a[190:191],v22 offset:19208
v_add_u32 v22,s60,v23
v_mfma_f32_16x16x32_bf16 a[32:35],a[132:135],a[204:207],a[32:35]
s_add_u32 s60,s60,32768
s_cmp_eq_u32 s60,131072
s_cselect_b32 s60,32768,s60
v_mfma_f32_16x16x32_bf16 a[32:35],a[128:131],a[200:203],a[32:35]
buffer_load_dwordx4 v52,s[28:31],s57 offen lds
v_mfma_f32_16x16x32_bf16 a[64:67],a[128:131],a[208:211],a[64:67]
v_mfma_f32_16x16x32_bf16 a[64:67],a[132:135],a[212:215],a[64:67]
v_mfma_f32_16x16x32_bf16 a[96:99],a[132:135],a[220:223],a[96:99]
v_mfma_f32_16x16x32_bf16 a[96:99],a[128:131],a[216:219],a[96:99]
v_mfma_f32_16x16x32_bf16 a[4:7],a[136:139],a[192:195],a[4:7]
buffer_load_dwordx4 v53,s[28:31],s57 offen offset:1024 lds
v_mfma_f32_16x16x32_bf16 a[4:7],a[140:143],a[196:199],a[4:7]
v_mfma_f32_16x16x32_bf16 a[36:39],a[140:143],a[204:207],a[36:39]
v_mfma_f32_16x16x32_bf16 a[36:39],a[136:139],a[200:203],a[36:39]
v_mfma_f32_16x16x32_bf16 a[68:71],a[136:139],a[208:211],a[68:71]
v_mfma_f32_16x16x32_bf16 a[68:71],a[140:143],a[212:215],a[68:71]
v_mfma_f32_16x16x32_bf16 a[100:103],a[140:143],a[220:223],a[100:103]
buffer_load_dwordx4 v54,s[28:31],s57 offen offset:2048 lds
v_mfma_f32_16x16x32_bf16 a[100:103],a[136:139],a[216:219],a[100:103]
v_mfma_f32_16x16x32_bf16 a[8:11],a[144:147],a[192:195],a[8:11]
v_mfma_f32_16x16x32_bf16 a[8:11],a[148:151],a[196:199],a[8:11]
v_mfma_f32_16x16x32_bf16 a[40:43],a[148:151],a[204:207],a[40:43]
v_mfma_f32_16x16x32_bf16 a[40:43],a[144:147],a[200:203],a[40:43]
buffer_load_dwordx4 v55,s[28:31],s57 offen offset:3072 lds
v_mfma_f32_16x16x32_bf16 a[72:75],a[144:147],a[208:211],a[72:75]
s_add_u32 s57,s57,128
v_mfma_f32_16x16x32_bf16 a[72:75],a[148:151],a[212:215],a[72:75]
v_mfma_f32_16x16x32_bf16 a[104:107],a[148:151],a[220:223],a[104:107]
v_mfma_f32_16x16x32_bf16 a[104:107],a[144:147],a[216:219],a[104:107]
v_mfma_f32_16x16x32_bf16 a[12:15],a[152:155],a[192:195],a[12:15]
v_mfma_f32_16x16x32_bf16 a[12:15],a[156:159],a[196:199],a[12:15]
v_mfma_f32_16x16x32_bf16 a[44:47],a[156:159],a[204:207],a[44:47]
buffer_load_dword v16,v15,s[72:75],s83 offen
v_mfma_f32_16x16x32_bf16 a[44:47],a[152:155],a[200:203],a[44:47]
s_add_u32 s83,s83,896
v_mfma_f32_16x16x32_bf16 a[76:79],a[152:155],a[208:211],a[76:79]
v_mfma_f32_16x16x32_bf16 a[76:79],a[156:159],a[212:215],a[76:79]
v_mfma_f32_16x16x32_bf16 a[108:111],a[156:159],a[220:223],a[108:111]
v_mfma_f32_16x16x32_bf16 a[108:111],a[152:155],a[216:219],a[108:111]
s_waitcnt vmcnt(17) lgkmcnt(0)
v_mfma_f32_16x16x32_bf16 a[16:19],a[160:163],a[192:195],a[16:19]
s_barrier
ds_read_b128 a[224:227],v20
v_mfma_f32_16x16x32_bf16 a[16:19],a[164:167],a[196:199],a[16:19]
ds_read_b64_tr_b16 a[128:129],v22 offset:1024
v_mfma_f32_16x16x32_bf16 a[48:51],a[164:167],a[204:207],a[48:51]
ds_read_b64_tr_b16 a[130:131],v22 offset:3072
v_mfma_f32_16x16x32_bf16 a[48:51],a[160:163],a[200:203],a[48:51]
)A"
R"A(ds_read_b128 a[228:231],v21
v_mfma_f32_16x16x32_bf16 a[80:83],a[160:163],a[208:211],a[80:83]
ds_read_b64_tr_b16 a[132:133],v22 offset:17408
v_mfma_f32_16x16x32_bf16 a[80:83],a[164:167],a[212:215],a[80:83]
ds_read_b64_tr_b16 a[134:135],v22 offset:19456
v_mfma_f32_16x16x32_bf16 a[112:115],a[164:167],a[220:223],a[112:115]
ds_read_b128 a[232:235],v20 offset:2048
v_mfma_f32_16x16x32_bf16 a[112:115],a[160:163],a[216:219],a[112:115]
ds_read_b128 a[236:239],v21 offset:2048
v_mfma_f32_16x16x32_bf16 a[20:23],a[168:171],a[192:195],a[20:23]
ds_read_b128 a[240:243],v20 offset:4096
v_mfma_f32_16x16x32_bf16 a[20:23],a[172:175],a[196:199],a[20:23]
ds_read_b128 a[244:247],v21 offset:4096
v_mfma_f32_16x16x32_bf16 a[52:55],a[172:175],a[204:207],a[52:55]
ds_read_b128 a[248:251],v20 offset:6144
v_xor_b32 v20,131072,v20
v_mfma_f32_16x16x32_bf16 a[52:55],a[168:171],a[200:203],a[52:55]
ds_read_b128 a[252:255],v21 offset:6144
v_xor_b32 v21,131072,v21
v_mfma_f32_16x16x32_bf16 a[84:87],a[168:171],a[208:211],a[84:87]
ds_read_b64_tr_b16 a[136:137],v22 offset:1032
v_mfma_f32_16x16x32_bf16 a[84:87],a[172:175],a[212:215],a[84:87]
ds_read_b64_tr_b16 a[138:139],v22 offset:3080
v_mfma_f32_16x16x32_bf16 a[116:119],a[172:175],a[220:223],a[116:119]
ds_read_b64_tr_b16 a[140:141],v22 offset:17416
v_mfma_f32_16x16x32_bf16 a[116:119],a[168:171],a[216:219],a[116:119]
ds_read_b64_tr_b16 a[142:143],v22 offset:19464
v_mfma_f32_16x16x32_bf16 a[24:27],a[176:179],a[192:195],a[24:27]
ds_read_b64_tr_b16 a[144:145],v22 offset:1280
v_mfma_f32_16x16x32_bf16 a[24:27],a[180:183],a[196:199],a[24:27]
ds_read_b64_tr_b16 a[146:147],v22 offset:3328
v_mfma_f32_16x16x32_bf16 a[56:59],a[180:183],a[204:207],a[56:59]
ds_read_b64_tr_b16 a[148:149],v22 offset:17664
v_mfma_f32_16x16x32_bf16 a[56:59],a[176:179],a[200:203],a[56:59]
ds_read_b64_tr_b16 a[150:151],v22 offset:19712
v_mfma_f32_16x16x32_bf16 a[88:91],a[176:179],a[208:211],a[88:91]
ds_read_b64_tr_b16 a[152:153],v22 offset:1288
v_mfma_f32_16x16x32_bf16 a[88:91],a[180:183],a[212:215],a[88:91]
ds_read_b64_tr_b16 a[154:155],v22 offset:3336
v_mfma_f32_16x16x32_bf16 a[120:123],a[180:183],a[220:223],a[120:123]
ds_read_b64_tr_b16 a[156:157],v22 offset:17672
v_mfma_f32_16x16x32_bf16 a[120:123],a[176:179],a[216:219],a[120:123]
ds_read_b64_tr_b16 a[158:159],v22 offset:19720
v_mfma_f32_16x16x32_bf16 a[28:31],a[184:187],a[192:195],a[28:31]
ds_read_b64_tr_b16 a[160:161],v22 offset:1536
v_mfma_f32_16x16x32_bf16 a[28:31],a[188:191],a[196:199],a[28:31]
ds_read_b64_tr_b16 a[162:163],v22 offset:3584
v_mfma_f32_16x16x32_bf16 a[60:63],a[188:191],a[204:207],a[60:63]
ds_read_b64_tr_b16 a[164:165],v22 offset:17920
v_mfma_f32_16x16x32_bf16 a[60:63],a[184:187],a[200:203],a[60:63]
ds_read_b64_tr_b16 a[166:167],v22 offset:19968
v_mfma_f32_16x16x32_bf16 a[92:95],a[184:187],a[208:211],a[92:95]
ds_read_b64_tr_b16 a[168:169],v22 offset:1544
v_mfma_f32_16x16x32_bf16 a[92:95],a[188:191],a[212:215],a[92:95]
ds_read_b64_tr_b16 a[170:171],v22 offset:3592
v_mfma_f32_16x16x32_bf16 a[124:127],a[188:191],a[220:223],a[124:127]
s_add_u32 m0,s58,0
ds_read_b64_tr_b16 a[172:173],v22 offset:17928
v_mfma_f32_16x16x32_bf16 a[124:127],a[184:187],a[216:219],a[124:127]
s_waitcnt lgkmcnt(7)
ds_read_b64_tr_b16 a[174:175],v22 offset:19976
v_mfma_f32_16x16x32_bf16 v[128:131],a[128:131],a[224:227],v[128:131]
ds_read_b64_tr_b16 a[176:177],v22 offset:1792
v_mfma_f32_16x16x32_bf16 v[128:131],a[132:135],a[228:231],v[128:131]
buffer_load_dwordx4 v56,s[32:35],s56 offen lds
v_mfma_f32_16x16x32_bf16 v[136:139],a[132:135],a[236:239],v[136:139]
ds_read_b64_tr_b16 a[178:179],v22 offset:3840
v_mfma_f32_16x16x32_bf16 v[136:139],a[128:131],a[232:235],v[136:139]
s_add_u32 m0,s58,1024
ds_read_b64_tr_b16 a[180:181],v22 offset:18176
v_mfma_f32_16x16x32_bf16 v[144:147],a[128:131],a[240:243],v[144:147]
ds_read_b64_tr_b16 a[182:183],v22 offset:20224
v_mfma_f32_16x16x32_bf16 v[144:147],a[132:135],a[244:247],v[144:147]
ds_read_b64_tr_b16 a[184:185],v22 offset:1800
v_mfma_f32_16x16x32_bf16 v[152:155],a[132:135],a[252:255],v[152:155]
buffer_load_dwordx4 v57,s[32:35],s56 offen lds
v_mfma_f32_16x16x32_bf16 v[152:155],a[128:131],a[248:251],v[152:155]
ds_read_b64_tr_b16 a[186:187],v22 offset:3848
v_mfma_f32_16x16x32_bf16 v[132:135],a[136:139],a[224:227],v[132:135]
ds_read_b64_tr_b16 a[188:189],v22 offset:18184
v_mfma_f32_16x16x32_bf16 v[132:135],a[140:143],a[228:231],v[132:135]
s_add_u32 m0,s58,2048
ds_read_b64_tr_b16 a[190:191],v22 offset:20232
v_mfma_f32_16x16x32_bf16 v[140:143],a[140:143],a[236:239],v[140:143]
ds_read_b64_tr_b16 a[128:129],v22
v_mfma_f32_16x16x32_bf16 v[140:143],a[136:139],a[232:235],v[140:143]
ds_read_b64_tr_b16 a[130:131],v22 offset:2048
v_mfma_f32_16x16x32_bf16 v[148:151],a[136:139],a[240:243],v[148:151]
buffer_load_dwordx4 v58,s[32:35],s56 offen lds
v_mfma_f32_16x16x32_bf16 v[148:151],a[140:143],a[244:247],v[148:151]
ds_read_b64_tr_b16 a[132:133],v22 offset:16384
v_mfma_f32_16x16x32_bf16 v[156:159],a[140:143],a[252:255],v[156:159]
s_add_u32 m0,s58,3072
ds_read_b64_tr_b16 a[134:135],v22 offset:18432
v_mfma_f32_16x16x32_bf16 v[156:159],a[136:139],a[248:251],v[156:159]
ds_read_b64_tr_b16 a[136:137],v22 offset:8
v_mfma_f32_16x16x32_bf16 v[160:163],a[144:147],a[224:227],v[160:163]
ds_read_b64_tr_b16 a[138:139],v22 offset:2056
v_mfma_f32_16x16x32_bf16 v[160:163],a[148:151],a[228:231],v[160:163]
buffer_load_dwordx4 v59,s[32:35],s56 offen lds
v_mfma_f32_16x16x32_bf16 v[168:171],a[148:151],a[236:239],v[168:171]
ds_read_b64_tr_b16 a[140:141],v22 offset:16392
v_mfma_f32_16x16x32_bf16 v[168:171],a[144:147],a[232:235],v[168:171]
ds_read_b64_tr_b16 a[142:143],v22 offset:18440
v_mfma_f32_16x16x32_bf16 v[176:179],a[144:147],a[240:243],v[176:179]
s_add_u32 m0,s58,4096
v_mfma_f32_16x16x32_bf16 v[176:179],a[148:151],a[244:247],v[176:179]
v_mfma_f32_16x16x32_bf16 v[184:187],a[148:151],a[252:255],v[184:187]
ds_read_b64_tr_b16 a[148:149],v22 offset:16640
v_mfma_f32_16x16x32_bf16 v[184:187],a[144:147],a[248:251],v[184:187]
buffer_load_dwordx4 v60,s[32:35],s56 offen lds
v_mfma_f32_16x16x32_bf16 v[164:167],a[152:155],a[224:227],v[164:167]
ds_read_b64_tr_b16 a[144:145],v22 offset:256
v_mfma_f32_16x16x32_bf16 v[164:167],a[156:159],a[228:231],v[164:167]
s_add_u32 m0,s58,5120
ds_read_b64_tr_b16 a[146:147],v22 offset:2304
v_mfma_f32_16x16x32_bf16 v[172:175],a[156:159],a[236:239],v[172:175]
ds_read_b64_tr_b16 a[150:151],v22 offset:18688
v_mfma_f32_16x16x32_bf16 v[172:175],a[152:155],a[232:235],v[172:175]
v_mfma_f32_16x16x32_bf16 v[180:183],a[152:155],a[240:243],v[180:183]
buffer_load_dwordx4 v61,s[32:35],s56 offen lds
v_mfma_f32_16x16x32_bf16 v[180:183],a[156:159],a[244:247],v[180:183]
v_mfma_f32_16x16x32_bf16 v[188:191],a[156:159],a[252:255],v[188:191]
ds_read_b64_tr_b16 a[156:157],v22 offset:16648
v_mfma_f32_16x16x32_bf16 v[188:191],a[152:155],a[248:251],v[188:191]
s_add_u32 m0,s58,6144
ds_read_b64_tr_b16 a[152:153],v22 offset:264
s_waitcnt lgkmcnt(5)
v_mfma_f32_16x16x32_bf16 v[192:195],a[160:163],a[224:227],v[192:195]
ds_read_b64_tr_b16 a[154:155],v22 offset:2312
v_mfma_f32_16x16x32_bf16 v[192:195],a[164:167],a[228:231],v[192:195]
ds_read_b64_tr_b16 a[158:159],v22 offset:18696
v_mfma_f32_16x16x32_bf16 v[200:203],a[164:167],a[236:239],v[200:203]
buffer_load_dwordx4 v62,s[32:35],s56 offen lds
v_mfma_f32_16x16x32_bf16 v[200:203],a[160:163],a[232:235],v[200:203]
v_mfma_f32_16x16x32_bf16 v[208:211],a[160:163],a[240:243],v[208:211]
s_add_u32 m0,s58,7168
s_add_u32 s58,s58,32768
v_mfma_f32_16x16x32_bf16 v[208:211],a[164:167],a[244:247],v[208:211]
s_cmp_ge_u32 s58,131072
s_cselect_b32 s62,98304,0
s_sub_u32 s58,s58,s62
v_mfma_f32_16x16x32_bf16 v[216:219],a[164:167],a[252:255],v[216:219]
ds_read_b64_tr_b16 a[164:165],v22 offset:16896
v_mfma_f32_16x16x32_bf16 v[216:219],a[160:163],a[248:251],v[216:219]
buffer_load_dwordx4 v63,s[32:35],s56 offen lds
v_mfma_f32_16x16x32_bf16 v[196:199],a[168:171],a[224:227],v[196:199]
s_add_u32 s56,s56,524288
ds_read_b64_tr_b16 a[160:161],v22 offset:512
v_mfma_f32_16x16x32_bf16 v[196:199],a[172:175],a[228:231],v[196:199]
ds_read_b64_tr_b16 a[162:163],v22 offset:2560
v_mfma_f32_16x16x32_bf16 v[204:207],a[172:175],a[236:239],v[204:207]
s_add_u32 m0,s59,0
ds_read_b64_tr_b16 a[166:167],v22 offset:18944
v_mfma_f32_16x16x32_bf16 v[204:207],a[168:171],a[232:235],v[204:207]
v_mfma_f32_16x16x32_bf16 v[212:215],a[168:171],a[240:243],v[212:215]
v_mfma_f32_16x16x32_bf16 v[212:215],a[172:175],a[244:247],v[212:215]
buffer_load_dwordx4 v48,s[28:31],s57 offen lds
v_mfma_f32_16x16x32_bf16 v[220:223],a[172:175],a[252:255],v[220:223]
ds_read_b64_tr_b16 a[172:173],v22 offset:16904
v_mfma_f32_16x16x32_bf16 v[220:223],a[168:171],a[248:251],v[220:223]
ds_read_b64_tr_b16 a[168:169],v22 offset:520
v_mfma_f32_16x16x32_bf16 v[224:227],a[176:179],a[224:227],v[224:227]
ds_read_b64_tr_b16 a[170:171],v22 offset:2568
v_mfma_f32_16x16x32_bf16 v[224:227],a[180:183],a[228:231],v[224:227]
ds_read_b64_tr_b16 a[174:175],v22 offset:18952
v_mfma_f32_16x16x32_bf16 v[232:235],a[180:183],a[236:239],v[232:235]
buffer_load_dwordx4 v49,s[28:31],s57 offen offset:1024 lds
v_mfma_f32_16x16x32_bf16 v[232:235],a[176:179],a[232:235],v[232:235]
v_mfma_f32_16x16x32_bf16 v[240:243],a[176:179],a[240:243],v[240:243]
v_mfma_f32_16x16x32_bf16 v[240:243],a[180:183],a[244:247],v[240:243]
v_mfma_f32_16x16x32_bf16 v[248:251],a[180:183],a[252:255],v[248:251]
ds_read_b64_tr_b16 a[180:181],v22 offset:17152
v_mfma_f32_16x16x32_bf16 v[248:251],a[176:179],a[248:251],v[248:251]
ds_read_b64_tr_b16 a[176:177],v22 offset:768
v_mfma_f32_16x16x32_bf16 v[228:231],a[184:187],a[224:227],v[228:231]
buffer_load_dwordx4 v50,s[28:31],s57 offen offset:2048 lds
v_mfma_f32_16x16x32_bf16 v[228:231],a[188:191],a[228:231],v[228:231]
ds_read_b64_tr_b16 a[178:179],v22 offset:2816
v_mfma_f32_16x16x32_bf16 v[236:239],a[188:191],a[236:239],v[236:239]
ds_read_b64_tr_b16 a[182:183],v22 offset:19200
v_mfma_f32_16x16x32_bf16 v[236:239],a[184:187],a[232:235],v[236:239]
v_mfma_f32_16x16x32_bf16 v[244:247],a[184:187],a[240:243],v[244:247]
v_mfma_f32_16x16x32_bf16 v[244:247],a[188:191],a[244:247],v[244:247]
buffer_load_dwordx4 v51,s[28:31],s57 offen offset:3072 lds
v_mfma_f32_16x16x32_bf16 v[252:255],a[188:191],a[252:255],v[252:255]
ds_read_b64_tr_b16 a[188:189],v22 offset:17160
v_mfma_f32_16x16x32_bf16 v[252:255],a[184:187],a[248:251],v[252:255]
ds_read_b64_tr_b16 a[184:185],v22 offset:776
v_mfma_f32_16x16x32_bf16 a[0:3],a[128:131],a[224:227],a[0:3]
s_add_u32 m0,s59,4096
s_xor_b32 s59,s59,131072
ds_read_b64_tr_b16 a[186:187],v22 offset:2824
v_mfma_f32_16x16x32_bf16 a[0:3],a[132:135],a[228:231],a[0:3]
ds_read_b64_tr_b16 a[190:191],v22 offset:19208
v_add_u32 v22,s60,v23
v_mfma_f32_16x16x32_bf16 a[32:35],a[132:135],a[236:239],a[32:35]
v_mfma_f32_16x16x32_bf16 a[32:35],a[128:131],a[232:235],a[32:35]
buffer_load_dwordx4 v52,s[28:31],s57 offen lds
v_mfma_f32_16x16x32_bf16 a[64:67],a[128:131],a[240:243],a[64:67]
v_mfma_f32_16x16x32_bf16 a[64:67],a[132:135],a[244:247],a[64:67]
v_mfma_f32_16x16x32_bf16 a[96:99],a[132:135],a[252:255],a[96:99]
v_mfma_f32_16x16x32_bf16 a[96:99],a[128:131],a[248:251],a[96:99]
v_mfma_f32_16x16x32_bf16 a[4:7],a[136:139],a[224:227],a[4:7]
buffer_load_dwordx4 v53,s[28:31],s57 offen offset:1024 lds
v_mfma_f32_16x16x32_bf16 a[4:7],a[140:143],a[228:231],a[4:7]
v_mfma_f32_16x16x32_bf16 a[36:39],a[140:143],a[236:239],a[36:39]
v_mfma_f32_16x16x32_bf16 a[36:39],a[136:139],a[232:235],a[36:39]
v_mfma_f32_16x16x32_bf16 a[68:71],a[136:139],a[240:243],a[68:71]
v_mfma_f32_16x16x32_bf16 a[68:71],a[140:143],a[244:247],a[68:71]
v_mfma_f32_16x16x32_bf16 a[100:103],a[140:143],a[252:255],a[100:103]
buffer_load_dwordx4 v54,s[28:31],s57 offen offset:2048 lds
v_mfma_f32_16x16x32_bf16 a[100:103],a[136:139],a[248:251],a[100:103]
s_waitcnt lgkmcnt(0)
v_mfma_f32_16x16x32_bf16 a[8:11],a[144:147],a[224:227],a[8:11]
v_mfma_f32_16x16x32_bf16 a[8:11],a[148:151],a[228:231],a[8:11]
v_mfma_f32_16x16x32_bf16 a[40:43],a[148:151],a[236:239],a[40:43]
v_mfma_f32_16x16x32_bf16 a[40:43],a[144:147],a[232:235],a[40:43]
buffer_load_dwordx4 v55,s[28:31],s57 offen offset:3072 lds
)A"
R"A(v_mfma_f32_16x16x32_bf16 a[72:75],a[144:147],a[240:243],a[72:75]
s_add_u32 s57,s57,128
v_mfma_f32_16x16x32_bf16 a[72:75],a[148:151],a[244:247],a[72:75]
v_mfma_f32_16x16x32_bf16 a[104:107],a[148:151],a[252:255],a[104:107]
v_mfma_f32_16x16x32_bf16 a[104:107],a[144:147],a[248:251],a[104:107]
v_mfma_f32_16x16x32_bf16 a[12:15],a[152:155],a[224:227],a[12:15]
v_mfma_f32_16x16x32_bf16 a[12:15],a[156:159],a[228:231],a[12:15]
v_mfma_f32_16x16x32_bf16 a[44:47],a[156:159],a[236:239],a[44:47]
buffer_load_dword v16,v15,s[72:75],s83 offen
v_mfma_f32_16x16x32_bf16 a[44:47],a[152:155],a[232:235],a[44:47]
s_add_u32 s83,s83,896
v_mfma_f32_16x16x32_bf16 a[76:79],a[152:155],a[240:243],a[76:79]
v_mfma_f32_16x16x32_bf16 a[76:79],a[156:159],a[244:247],a[76:79]
v_mfma_f32_16x16x32_bf16 a[108:111],a[156:159],a[252:255],a[108:111]
v_mfma_f32_16x16x32_bf16 a[108:111],a[152:155],a[248:251],a[108:111]
s_waitcnt vmcnt(17) lgkmcnt(0)
v_mfma_f32_16x16x32_bf16 a[16:19],a[160:163],a[224:227],a[16:19]
s_barrier
ds_read_b128 a[192:195],v20
v_mfma_f32_16x16x32_bf16 a[16:19],a[164:167],a[228:231],a[16:19]
ds_read_b128 a[196:199],v21
v_mfma_f32_16x16x32_bf16 a[48:51],a[164:167],a[236:239],a[48:51]
ds_read_b128 a[200:203],v20 offset:2048
v_mfma_f32_16x16x32_bf16 a[48:51],a[160:163],a[232:235],a[48:51]
ds_read_b128 a[204:207],v21 offset:2048
v_mfma_f32_16x16x32_bf16 a[80:83],a[160:163],a[240:243],a[80:83]
ds_read_b128 a[208:211],v20 offset:4096
v_mfma_f32_16x16x32_bf16 a[80:83],a[164:167],a[244:247],a[80:83]
ds_read_b128 a[212:215],v21 offset:4096
v_mfma_f32_16x16x32_bf16 a[112:115],a[164:167],a[252:255],a[112:115]
buffer_load_dword v26,v24,s[88:91],s93 offen
v_mfma_f32_16x16x32_bf16 a[112:115],a[160:163],a[248:251],a[112:115]
ds_read_b128 a[216:219],v20 offset:6144
v_mfma_f32_16x16x32_bf16 a[20:23],a[168:171],a[224:227],a[20:23]
ds_read_b128 a[220:223],v21 offset:6144
v_mfma_f32_16x16x32_bf16 a[20:23],a[172:175],a[228:231],a[20:23]
ds_read_b64_tr_b16 a[128:129],v22 offset:1024
v_mfma_f32_16x16x32_bf16 a[52:55],a[172:175],a[236:239],a[52:55]
ds_read_b64_tr_b16 a[130:131],v22 offset:3072
v_mfma_f32_16x16x32_bf16 a[52:55],a[168:171],a[232:235],a[52:55]
ds_read_b64_tr_b16 a[132:133],v22 offset:17408
v_mfma_f32_16x16x32_bf16 a[84:87],a[168:171],a[240:243],a[84:87]
ds_read_b64_tr_b16 a[134:135],v22 offset:19456
v_mfma_f32_16x16x32_bf16 a[84:87],a[172:175],a[244:247],a[84:87]
ds_read_b64_tr_b16 a[136:137],v22 offset:1032
v_mfma_f32_16x16x32_bf16 a[116:119],a[172:175],a[252:255],a[116:119]
ds_read_b64_tr_b16 a[138:139],v22 offset:3080
v_mfma_f32_16x16x32_bf16 a[116:119],a[168:171],a[248:251],a[116:119]
ds_read_b64_tr_b16 a[140:141],v22 offset:17416
v_mfma_f32_16x16x32_bf16 a[24:27],a[176:179],a[224:227],a[24:27]
ds_read_b64_tr_b16 a[142:143],v22 offset:19464
v_mfma_f32_16x16x32_bf16 a[24:27],a[180:183],a[228:231],a[24:27]
ds_read_b64_tr_b16 a[144:145],v22 offset:1280
v_mfma_f32_16x16x32_bf16 a[56:59],a[180:183],a[236:239],a[56:59]
ds_read_b64_tr_b16 a[146:147],v22 offset:3328
v_mfma_f32_16x16x32_bf16 a[56:59],a[176:179],a[232:235],a[56:59]
ds_read_b64_tr_b16 a[148:149],v22 offset:17664
v_mfma_f32_16x16x32_bf16 a[88:91],a[176:179],a[240:243],a[88:91]
ds_read_b64_tr_b16 a[150:151],v22 offset:19712
v_mfma_f32_16x16x32_bf16 a[88:91],a[180:183],a[244:247],a[88:91]
ds_read_b64_tr_b16 a[152:153],v22 offset:1288
v_mfma_f32_16x16x32_bf16 a[120:123],a[180:183],a[252:255],a[120:123]
ds_read_b64_tr_b16 a[154:155],v22 offset:3336
v_mfma_f32_16x16x32_bf16 a[120:123],a[176:179],a[248:251],a[120:123]
ds_read_b64_tr_b16 a[156:157],v22 offset:17672
v_mfma_f32_16x16x32_bf16 a[28:31],a[184:187],a[224:227],a[28:31]
ds_read_b64_tr_b16 a[158:159],v22 offset:19720
v_mfma_f32_16x16x32_bf16 a[28:31],a[188:191],a[228:231],a[28:31]
v_mfma_f32_16x16x32_bf16 a[60:63],a[188:191],a[236:239],a[60:63]
v_mfma_f32_16x16x32_bf16 a[60:63],a[184:187],a[232:235],a[60:63]
v_mfma_f32_16x16x32_bf16 a[92:95],a[184:187],a[240:243],a[92:95]
v_mfma_f32_16x16x32_bf16 a[92:95],a[188:191],a[244:247],a[92:95]
v_mfma_f32_16x16x32_bf16 a[124:127],a[188:191],a[252:255],a[124:127]
s_cmp_lg_u32 s53,0
v_mfma_f32_16x16x32_bf16 a[124:127],a[184:187],a[248:251],a[124:127]
s_cbranch_scc1 L_qp_%=
L_qpx_%=:
L_tailseg_%=:
s_waitcnt lgkmcnt(1)
v_mfma_f32_16x16x32_bf16 v[128:131],a[128:131],a[192:195],v[128:131]
s_add_u32 m0,98304,s51
ds_read_b64_tr_b16 a[160:161],v22 offset:1536
v_xor_b32 v20,131072,v20
v_mfma_f32_16x16x32_bf16 v[128:131],a[132:135],a[196:199],v[128:131]
s_add_u32 s54,s54,16384
buffer_load_dwordx4 v4,s[20:23],s47 offen nt lds
v_mfma_f32_16x16x32_bf16 v[136:139],a[132:135],a[204:207],v[136:139]
s_and_b32 s46,s54,0xfffc0000
ds_read_b64_tr_b16 a[162:163],v22 offset:3584
v_xor_b32 v21,131072,v21
v_mfma_f32_16x16x32_bf16 v[136:139],a[128:131],a[200:203],v[136:139]
s_add_u32 s54,s54,16384
ds_read_b64_tr_b16 a[164:165],v22 offset:17920
v_and_b32 v18,48,v6
v_mfma_f32_16x16x32_bf16 v[144:147],a[128:131],a[208:211],v[144:147]
s_mov_b32 s48,-98304
ds_read_b64_tr_b16 a[166:167],v22 offset:19968
v_lshrrev_b32 v19,4,v6
v_mfma_f32_16x16x32_bf16 v[144:147],a[132:135],a[212:215],v[144:147]
s_lshr_b32 s79,%1,3
ds_read_b64_tr_b16 a[168:169],v22 offset:1544
v_lshlrev_b32 v18,2,v18
v_mfma_f32_16x16x32_bf16 v[152:155],a[132:135],a[220:223],v[152:155]
s_mul_i32 s79,s79,7
ds_read_b64_tr_b16 a[170:171],v22 offset:3592
v_and_b32 v19,48,v19
v_mfma_f32_16x16x32_bf16 v[152:155],a[128:131],a[216:219],v[152:155]
s_mov_b32 s60,138
ds_read_b64_tr_b16 a[172:173],v22 offset:17928
v_add_u32 v18,v18,v19
v_mfma_f32_16x16x32_bf16 v[132:135],a[136:139],a[192:195],v[132:135]
s_lshr_b32 s79,s79,5
ds_read_b64_tr_b16 a[174:175],v22 offset:19976
v_add_u32 v18,98304,v18
v_mfma_f32_16x16x32_bf16 v[132:135],a[140:143],a[196:199],v[132:135]
s_sub_u32 s60,s60,s79
s_mul_i32 s83,s41,5
ds_read_b64_tr_b16 a[176:177],v22 offset:1792
v_mfma_f32_16x16x32_bf16 v[140:143],a[140:143],a[204:207],v[140:143]
s_lshl_b32 s44,s40,4
s_add_u32 s83,s83,s42
ds_read_b64_tr_b16 a[178:179],v22 offset:3840
v_mfma_f32_16x16x32_bf16 v[140:143],a[136:139],a[200:203],v[140:143]
s_and_b32 s44,s44,32
ds_read_b64_tr_b16 a[180:181],v22 offset:18176
v_subrev_u32 v25,s51,v4
v_mfma_f32_16x16x32_bf16 v[148:151],a[136:139],a[208:211],v[148:151]
s_add_u32 s83,s83,15
s_lshl_b32 s80,s40,10
ds_read_b64_tr_b16 a[182:183],v22 offset:20224
v_mfma_f32_16x16x32_bf16 v[148:151],a[140:143],a[212:215],v[148:151]
s_and_b32 s83,s83,15
ds_read_b64_tr_b16 a[184:185],v22 offset:1800
v_xor_b32 v25,s44,v25
v_mfma_f32_16x16x32_bf16 v[156:159],a[140:143],a[220:223],v[156:159]
s_add_u32 s83,s83,16
ds_read_b64_tr_b16 a[186:187],v22 offset:3848
v_add_u32 v25,s80,v25
v_mfma_f32_16x16x32_bf16 v[156:159],a[136:139],a[216:219],v[156:159]
s_add_u32 s93,s92,L_att0x_%=-L_pc_%=
v_readfirstlane_b32 s68,v13
s_nop 1
s_add_u32 s68,s68,1
v_mfma_f32_16x16x32_bf16 v[160:163],a[144:147],a[192:195],v[160:163]
s_xor_b32 s69,s68,0x9e3779b9
s_mov_b32 s50,32768
ds_read_b64_tr_b16 a[188:189],v22 offset:18184
v_mfma_f32_16x16x32_bf16 v[160:163],a[148:151],a[196:199],v[160:163]
s_mov_b32 s49,98304
buffer_load_dwordx4 v4,s[20:23],s47 offen offset:1024 nt lds
v_mfma_f32_16x16x32_bf16 v[168:171],a[148:151],a[204:207],v[168:171]
s_mov_b32 s45,0x1249
s_add_u32 s80,s80,131072
s_waitcnt vmcnt(4)
ds_read_b128 a[224:227],v20
v_mfma_f32_16x16x32_bf16 v[168:171],a[144:147],a[200:203],v[168:171]
s_lshl_b32 s83,s83,14
ds_read_b64_tr_b16 a[190:191],v22 offset:20232
v_mfma_f32_16x16x32_bf16 v[176:179],a[144:147],a[208:211],v[176:179]
ds_read_b64_tr_b16 a[128:129],v22
v_mfma_f32_16x16x32_bf16 v[176:179],a[148:151],a[212:215],v[176:179]
ds_read_b64_tr_b16 a[130:131],v22 offset:2048
v_mfma_f32_16x16x32_bf16 v[184:187],a[148:151],a[220:223],v[184:187]
ds_read_b64_tr_b16 a[132:133],v22 offset:16384
v_mfma_f32_16x16x32_bf16 v[184:187],a[144:147],a[216:219],v[184:187]
ds_read_b64_tr_b16 a[134:135],v22 offset:18432
v_mfma_f32_16x16x32_bf16 v[164:167],a[152:155],a[192:195],v[164:167]
s_waitcnt lgkmcnt(6)
ds_read_b64_tr_b16 a[144:145],v22 offset:8
v_mfma_f32_16x16x32_bf16 v[164:167],a[156:159],a[196:199],v[164:167]
ds_read_b64_tr_b16 a[146:147],v22 offset:2056
v_mfma_f32_16x16x32_bf16 v[172:175],a[156:159],a[204:207],v[172:175]
ds_read_b64_tr_b16 a[148:149],v22 offset:16392
v_mfma_f32_16x16x32_bf16 v[172:175],a[152:155],a[200:203],v[172:175]
ds_read_b64_tr_b16 a[150:151],v22 offset:18440
v_mfma_f32_16x16x32_bf16 v[180:183],a[152:155],a[208:211],v[180:183]
v_mfma_f32_16x16x32_bf16 v[180:183],a[156:159],a[212:215],v[180:183]
ds_read_b128 a[228:231],v21
v_mfma_f32_16x16x32_bf16 v[188:191],a[156:159],a[220:223],v[188:191]
v_mfma_f32_16x16x32_bf16 v[188:191],a[152:155],a[216:219],v[188:191]
ds_read_b128 a[232:235],v20 offset:2048
v_mfma_f32_16x16x32_bf16 v[192:195],a[160:163],a[192:195],v[192:195]
ds_read_b128 a[236:239],v21 offset:2048
ds_read_b128 a[240:243],v20 offset:4096
ds_read_b128 a[244:247],v21 offset:4096
ds_read_b128 a[248:251],v20 offset:6144
ds_read_b128 a[252:255],v21 offset:6144
v_mfma_f32_16x16x32_bf16 v[192:195],a[164:167],a[196:199],v[192:195]
v_mfma_f32_16x16x32_bf16 v[200:203],a[164:167],a[204:207],v[200:203]
v_mfma_f32_16x16x32_bf16 v[200:203],a[160:163],a[200:203],v[200:203]
v_mfma_f32_16x16x32_bf16 v[208:211],a[160:163],a[208:211],v[208:211]
buffer_load_dwordx4 v5,s[20:23],s47 offen offset:2048 nt lds
v_mfma_f32_16x16x32_bf16 v[208:211],a[164:167],a[212:215],v[208:211]
v_mfma_f32_16x16x32_bf16 v[216:219],a[164:167],a[220:223],v[216:219]
s_waitcnt vmcnt(4) lgkmcnt(0)
v_mfma_f32_16x16x32_bf16 v[216:219],a[160:163],a[216:219],v[216:219]
s_barrier
ds_read_b64_tr_b16 a[136:137],v22 offset:32768
v_mfma_f32_16x16x32_bf16 v[196:199],a[168:171],a[192:195],v[196:199]
ds_read_b64_tr_b16 a[138:139],v22 offset:34816
v_mfma_f32_16x16x32_bf16 v[196:199],a[172:175],a[196:199],v[196:199]
ds_read_b64_tr_b16 a[140:141],v22 offset:49152
v_mfma_f32_16x16x32_bf16 v[204:207],a[172:175],a[204:207],v[204:207]
ds_read_b64_tr_b16 a[142:143],v22 offset:51200
v_mfma_f32_16x16x32_bf16 v[204:207],a[168:171],a[200:203],v[204:207]
ds_read_b64_tr_b16 a[152:153],v22 offset:32776
v_mfma_f32_16x16x32_bf16 v[212:215],a[168:171],a[208:211],v[212:215]
ds_read_b64_tr_b16 a[154:155],v22 offset:34824
v_mfma_f32_16x16x32_bf16 v[212:215],a[172:175],a[212:215],v[212:215]
ds_read_b64_tr_b16 a[156:157],v22 offset:49160
v_mfma_f32_16x16x32_bf16 v[220:223],a[172:175],a[220:223],v[220:223]
ds_read_b64_tr_b16 a[158:159],v22 offset:51208
v_mfma_f32_16x16x32_bf16 v[220:223],a[168:171],a[216:219],v[220:223]
ds_read_b64_tr_b16 a[160:161],v22 offset:256
v_mfma_f32_16x16x32_bf16 v[224:227],a[176:179],a[192:195],v[224:227]
buffer_load_dwordx4 v5,s[20:23],s47 offen offset:3072 nt lds
v_mfma_f32_16x16x32_bf16 v[224:227],a[180:183],a[196:199],v[224:227]
ds_read_b64_tr_b16 a[162:163],v22 offset:2304
v_mfma_f32_16x16x32_bf16 v[232:235],a[180:183],a[204:207],v[232:235]
ds_read_b64_tr_b16 a[164:165],v22 offset:16640
v_mfma_f32_16x16x32_bf16 v[232:235],a[176:179],a[200:203],v[232:235]
ds_read_b64_tr_b16 a[166:167],v22 offset:18688
v_mfma_f32_16x16x32_bf16 v[240:243],a[176:179],a[208:211],v[240:243]
ds_read_b64_tr_b16 a[168:169],v22 offset:33024
v_mfma_f32_16x16x32_bf16 v[240:243],a[180:183],a[212:215],v[240:243]
ds_read_b64_tr_b16 a[170:171],v22 offset:35072
v_mfma_f32_16x16x32_bf16 v[248:251],a[180:183],a[220:223],v[248:251]
ds_read_b64_tr_b16 a[172:173],v22 offset:49408
v_mfma_f32_16x16x32_bf16 v[248:251],a[176:179],a[216:219],v[248:251]
ds_read_b64_tr_b16 a[174:175],v22 offset:51456
v_mfma_f32_16x16x32_bf16 v[228:231],a[184:187],a[192:195],v[228:231]
ds_read_b64_tr_b16 a[176:177],v22 offset:264
v_mfma_f32_16x16x32_bf16 v[228:231],a[188:191],a[196:199],v[228:231]
ds_read_b64_tr_b16 a[178:179],v22 offset:2312
v_mfma_f32_16x16x32_bf16 v[236:239],a[188:191],a[204:207],v[236:239]
ds_read_b64_tr_b16 a[180:181],v22 offset:16648
v_mfma_f32_16x16x32_bf16 v[236:239],a[184:187],a[200:203],v[236:239]
ds_read_b64_tr_b16 a[182:183],v22 offset:18696
v_mfma_f32_16x16x32_bf16 v[244:247],a[184:187],a[208:211],v[244:247]
v_mfma_f32_16x16x32_bf16 v[244:247],a[188:191],a[212:215],v[244:247]
)A"
R"A(s_add_u32 m0,98304,s52
v_mfma_f32_16x16x32_bf16 v[252:255],a[188:191],a[220:223],v[252:255]
ds_read_b64_tr_b16 a[188:189],v22 offset:49416
v_mfma_f32_16x16x32_bf16 v[252:255],a[184:187],a[216:219],v[252:255]
ds_read_b64_tr_b16 a[184:185],v22 offset:33032
v_mfma_f32_16x16x32_bf16 a[0:3],a[128:131],a[192:195],a[0:3]
buffer_load_dwordx4 v4,s[24:27],s47 offen nt lds
v_mfma_f32_16x16x32_bf16 a[0:3],a[132:135],a[196:199],a[0:3]
s_waitcnt lgkmcnt(4)
ds_read_b64_tr_b16 a[186:187],v22 offset:35080
v_mfma_f32_16x16x32_bf16 a[0:3],a[136:139],a[224:227],a[0:3]
ds_read_b64_tr_b16 a[190:191],v22 offset:51464
v_mfma_f32_16x16x32_bf16 a[0:3],a[140:143],a[228:231],a[0:3]
v_mfma_f32_16x16x32_bf16 a[32:35],a[140:143],a[236:239],a[32:35]
v_mfma_f32_16x16x32_bf16 a[32:35],a[136:139],a[232:235],a[32:35]
v_mfma_f32_16x16x32_bf16 a[32:35],a[132:135],a[204:207],a[32:35]
v_mfma_f32_16x16x32_bf16 a[32:35],a[128:131],a[200:203],a[32:35]
v_mfma_f32_16x16x32_bf16 a[64:67],a[128:131],a[208:211],a[64:67]
v_mfma_f32_16x16x32_bf16 a[64:67],a[132:135],a[212:215],a[64:67]
s_nop 1
v_accvgpr_read_b32 v64,a0
v_accvgpr_read_b32 v65,a1
v_mfma_f32_16x16x32_bf16 a[64:67],a[136:139],a[240:243],a[64:67]
v_mul_f32 v64,s61,v64
v_mul_f32 v65,s61,v65
v_mfma_f32_16x16x32_bf16 a[64:67],a[140:143],a[244:247],a[64:67]
v_accvgpr_read_b32 v66,a2
v_accvgpr_read_b32 v67,a3
v_mfma_f32_16x16x32_bf16 a[96:99],a[140:143],a[252:255],a[96:99]
ds_read_b64_tr_b16 a[140:141],v22 offset:49664
v_cvt_pk_bf16_f32 v64,v64,v65
v_mfma_f32_16x16x32_bf16 a[96:99],a[136:139],a[248:251],a[96:99]
ds_read_b64_tr_b16 a[136:137],v22 offset:33280
v_accvgpr_read_b32 v80,a32
v_mfma_f32_16x16x32_bf16 a[96:99],a[132:135],a[220:223],a[96:99]
ds_read_b64_tr_b16 a[132:133],v22 offset:16896
v_accvgpr_read_b32 v81,a33
v_mfma_f32_16x16x32_bf16 a[96:99],a[128:131],a[216:219],a[96:99]
ds_read_b64_tr_b16 a[128:129],v22 offset:512
v_mul_f32 v80,s61,v80
v_mfma_f32_16x16x32_bf16 a[4:7],a[144:147],a[192:195],a[4:7]
buffer_load_dwordx4 v5,s[24:27],s47 offen offset:1024 nt lds
v_mfma_f32_16x16x32_bf16 a[4:7],a[148:151],a[196:199],a[4:7]
ds_read_b64_tr_b16 a[130:131],v22 offset:2560
v_mul_f32 v81,s61,v81
v_mfma_f32_16x16x32_bf16 a[4:7],a[152:155],a[224:227],a[4:7]
ds_read_b64_tr_b16 a[134:135],v22 offset:18944
v_accvgpr_read_b32 v82,a34
v_mfma_f32_16x16x32_bf16 a[4:7],a[156:159],a[228:231],a[4:7]
ds_read_b64_tr_b16 a[138:139],v22 offset:35328
v_accvgpr_read_b32 v83,a35
v_mfma_f32_16x16x32_bf16 a[36:39],a[156:159],a[236:239],a[36:39]
ds_read_b64_tr_b16 a[142:143],v22 offset:51712
v_mul_f32 v66,s61,v66
v_mfma_f32_16x16x32_bf16 a[36:39],a[152:155],a[232:235],a[36:39]
v_mul_f32 v67,s61,v67
v_cvt_pk_bf16_f32 v80,v80,v81
v_mfma_f32_16x16x32_bf16 a[36:39],a[148:151],a[204:207],a[36:39]
v_mul_f32 v82,s61,v82
v_mul_f32 v83,s61,v83
v_mfma_f32_16x16x32_bf16 a[36:39],a[144:147],a[200:203],a[36:39]
v_cvt_pk_bf16_f32 v65,v66,v67
v_cvt_pk_bf16_f32 v81,v82,v83
v_mfma_f32_16x16x32_bf16 a[68:71],a[144:147],a[208:211],a[68:71]
v_accvgpr_read_b32 v96,a64
v_accvgpr_read_b32 v97,a65
v_mfma_f32_16x16x32_bf16 a[68:71],a[148:151],a[212:215],a[68:71]
v_accvgpr_read_b32 v112,a96
v_accvgpr_read_b32 v113,a97
v_mfma_f32_16x16x32_bf16 a[68:71],a[152:155],a[240:243],a[68:71]
v_accvgpr_read_b32 v66,a4
v_accvgpr_read_b32 v67,a5
v_mfma_f32_16x16x32_bf16 a[68:71],a[156:159],a[244:247],a[68:71]
v_mul_f32 v96,s61,v96
v_mul_f32 v97,s61,v97
v_mfma_f32_16x16x32_bf16 a[100:103],a[156:159],a[252:255],a[100:103]
ds_read_b64_tr_b16 a[156:157],v22 offset:49672
v_accvgpr_read_b32 v98,a66
v_mfma_f32_16x16x32_bf16 a[100:103],a[152:155],a[248:251],a[100:103]
ds_read_b64_tr_b16 a[152:153],v22 offset:33288
v_accvgpr_read_b32 v82,a36
v_mfma_f32_16x16x32_bf16 a[100:103],a[148:151],a[220:223],a[100:103]
ds_read_b64_tr_b16 a[148:149],v22 offset:16904
v_accvgpr_read_b32 v83,a37
v_mfma_f32_16x16x32_bf16 a[100:103],a[144:147],a[216:219],a[100:103]
ds_read_b64_tr_b16 a[144:145],v22 offset:520
v_accvgpr_read_b32 v99,a67
v_mfma_f32_16x16x32_bf16 a[8:11],a[160:163],a[192:195],a[8:11]
buffer_load_dwordx4 v4,s[24:27],s47 offen offset:2048 nt lds
v_mfma_f32_16x16x32_bf16 a[8:11],a[164:167],a[196:199],a[8:11]
ds_read_b64_tr_b16 a[146:147],v22 offset:2568
v_mul_f32 v112,s61,v112
v_mfma_f32_16x16x32_bf16 a[8:11],a[168:171],a[224:227],a[8:11]
ds_read_b64_tr_b16 a[150:151],v22 offset:18952
v_mul_f32 v113,s61,v113
v_mfma_f32_16x16x32_bf16 a[8:11],a[172:175],a[228:231],a[8:11]
ds_read_b64_tr_b16 a[154:155],v22 offset:35336
v_accvgpr_read_b32 v114,a98
v_mfma_f32_16x16x32_bf16 a[40:43],a[172:175],a[236:239],a[40:43]
ds_read_b64_tr_b16 a[158:159],v22 offset:51720
v_accvgpr_read_b32 v115,a99
v_mfma_f32_16x16x32_bf16 a[40:43],a[168:171],a[232:235],a[40:43]
v_accvgpr_read_b32 v12,a6
v_accvgpr_read_b32 v13,a7
v_mfma_f32_16x16x32_bf16 a[40:43],a[164:167],a[204:207],a[40:43]
v_mul_f32 v66,s61,v66
v_mul_f32 v67,s61,v67
v_mfma_f32_16x16x32_bf16 a[40:43],a[160:163],a[200:203],a[40:43]
v_accvgpr_read_b32 v14,a38
v_accvgpr_read_b32 v15,a39
v_mfma_f32_16x16x32_bf16 a[72:75],a[160:163],a[208:211],a[72:75]
v_mul_f32 v82,s61,v82
v_mul_f32 v83,s61,v83
v_mfma_f32_16x16x32_bf16 a[72:75],a[164:167],a[212:215],a[72:75]
v_accvgpr_read_b32 v68,a8
v_accvgpr_read_b32 v69,a9
v_mfma_f32_16x16x32_bf16 a[72:75],a[168:171],a[240:243],a[72:75]
v_mul_f32 v68,s61,v68
v_mul_f32 v69,s61,v69
v_mfma_f32_16x16x32_bf16 a[72:75],a[172:175],a[244:247],a[72:75]
v_accvgpr_read_b32 v70,a10
v_accvgpr_read_b32 v71,a11
v_mfma_f32_16x16x32_bf16 a[104:107],a[172:175],a[252:255],a[104:107]
ds_read_b64_tr_b16 a[172:173],v22 offset:49920
v_cvt_pk_bf16_f32 v96,v96,v97
v_mfma_f32_16x16x32_bf16 a[104:107],a[168:171],a[248:251],a[104:107]
ds_read_b64_tr_b16 a[168:169],v22 offset:33536
v_mul_f32 v98,s61,v98
v_mfma_f32_16x16x32_bf16 a[104:107],a[164:167],a[220:223],a[104:107]
ds_read_b64_tr_b16 a[164:165],v22 offset:17152
v_mul_f32 v99,s61,v99
v_mfma_f32_16x16x32_bf16 a[104:107],a[160:163],a[216:219],a[104:107]
ds_read_b64_tr_b16 a[160:161],v22 offset:768
v_cvt_pk_bf16_f32 v112,v112,v113
v_mfma_f32_16x16x32_bf16 a[12:15],a[176:179],a[192:195],a[12:15]
s_waitcnt lgkmcnt(4)
buffer_load_dwordx4 v5,s[24:27],s47 offen offset:3072 nt lds
v_mfma_f32_16x16x32_bf16 a[12:15],a[180:183],a[196:199],a[12:15]
s_add_u32 s47,s47,16384
s_and_b32 s47,s47,0x3ffff
s_or_b32 s47,s47,s46
v_mfma_f32_16x16x32_bf16 a[12:15],a[184:187],a[224:227],a[12:15]
s_and_b32 s46,s54,0xfffc0000
ds_read_b64_tr_b16 a[162:163],v22 offset:2816
v_mul_f32 v114,s61,v114
v_mfma_f32_16x16x32_bf16 a[12:15],a[188:191],a[228:231],a[12:15]
s_add_u32 s54,s54,16384
ds_read_b64_tr_b16 a[166:167],v22 offset:19200
v_mul_f32 v115,s61,v115
v_mfma_f32_16x16x32_bf16 a[44:47],a[188:191],a[236:239],a[44:47]
ds_read_b64_tr_b16 a[170:171],v22 offset:35584
v_cvt_pk_bf16_f32 v68,v68,v69
v_mfma_f32_16x16x32_bf16 a[44:47],a[184:187],a[232:235],a[44:47]
ds_read_b64_tr_b16 a[174:175],v22 offset:51968
v_mul_f32 v70,s61,v70
v_mfma_f32_16x16x32_bf16 a[44:47],a[180:183],a[204:207],a[44:47]
v_mul_f32 v71,s61,v71
v_mul_f32 v12,s61,v12
v_mfma_f32_16x16x32_bf16 a[44:47],a[176:179],a[200:203],a[44:47]
v_mul_f32 v13,s61,v13
v_cvt_pk_bf16_f32 v66,v66,v67
v_mfma_f32_16x16x32_bf16 a[76:79],a[176:179],a[208:211],a[76:79]
v_mul_f32 v14,s61,v14
v_mul_f32 v15,s61,v15
v_mfma_f32_16x16x32_bf16 a[76:79],a[180:183],a[212:215],a[76:79]
v_cvt_pk_bf16_f32 v82,v82,v83
v_cvt_pk_bf16_f32 v97,v98,v99
v_mfma_f32_16x16x32_bf16 a[76:79],a[184:187],a[240:243],a[76:79]
v_cvt_pk_bf16_f32 v113,v114,v115
v_cvt_pk_bf16_f32 v69,v70,v71
v_mfma_f32_16x16x32_bf16 a[76:79],a[188:191],a[244:247],a[76:79]
v_accvgpr_read_b32 v84,a40
v_accvgpr_read_b32 v85,a41
v_mfma_f32_16x16x32_bf16 a[108:111],a[188:191],a[252:255],a[108:111]
ds_read_b64_tr_b16 a[188:189],v22 offset:49928
v_accvgpr_read_b32 v100,a72
v_mfma_f32_16x16x32_bf16 a[108:111],a[184:187],a[248:251],a[108:111]
ds_read_b64_tr_b16 a[184:185],v22 offset:33544
v_accvgpr_read_b32 v101,a73
v_mfma_f32_16x16x32_bf16 a[108:111],a[180:183],a[220:223],a[108:111]
ds_read_b64_tr_b16 a[180:181],v22 offset:17160
v_accvgpr_read_b32 v116,a104
v_mfma_f32_16x16x32_bf16 a[108:111],a[176:179],a[216:219],a[108:111]
ds_read_b64_tr_b16 a[176:177],v22 offset:776
v_accvgpr_read_b32 v117,a105
v_mfma_f32_16x16x32_bf16 a[16:19],a[128:131],a[192:195],a[16:19]
ds_read_b64_tr_b16 a[178:179],v22 offset:2824
v_cvt_pk_bf16_f32 v67,v12,v13
v_mfma_f32_16x16x32_bf16 a[16:19],a[132:135],a[196:199],a[16:19]
ds_read_b64_tr_b16 a[182:183],v22 offset:19208
v_cvt_pk_bf16_f32 v83,v14,v15
v_mfma_f32_16x16x32_bf16 a[16:19],a[136:139],a[224:227],a[16:19]
ds_read_b64_tr_b16 a[186:187],v22 offset:35592
v_accvgpr_read_b32 v98,a68
v_mfma_f32_16x16x32_bf16 a[16:19],a[140:143],a[228:231],a[16:19]
s_add_u32 m0,0,s51
ds_read_b64_tr_b16 a[190:191],v22 offset:51976
v_accvgpr_read_b32 v99,a69
v_mfma_f32_16x16x32_bf16 a[48:51],a[140:143],a[236:239],a[48:51]
v_accvgpr_read_b32 v114,a100
v_accvgpr_read_b32 v115,a101
v_mfma_f32_16x16x32_bf16 a[48:51],a[136:139],a[232:235],a[48:51]
v_accvgpr_read_b32 v70,a12
v_accvgpr_read_b32 v71,a13
v_mfma_f32_16x16x32_bf16 a[48:51],a[132:135],a[204:207],a[48:51]
buffer_load_dwordx4 v4,s[20:23],s47 offen nt lds
v_mfma_f32_16x16x32_bf16 a[48:51],a[128:131],a[200:203],a[48:51]
v_mul_f32 v84,s61,v84
v_mul_f32 v85,s61,v85
v_mfma_f32_16x16x32_bf16 a[80:83],a[128:131],a[208:211],a[80:83]
v_accvgpr_read_b32 v86,a42
v_accvgpr_read_b32 v87,a43
v_mfma_f32_16x16x32_bf16 a[80:83],a[132:135],a[212:215],a[80:83]
v_mul_f32 v100,s61,v100
v_mul_f32 v101,s61,v101
v_mfma_f32_16x16x32_bf16 a[80:83],a[136:139],a[240:243],a[80:83]
v_accvgpr_read_b32 v102,a74
v_accvgpr_read_b32 v103,a75
v_mfma_f32_16x16x32_bf16 a[80:83],a[140:143],a[244:247],a[80:83]
v_mul_f32 v116,s61,v116
v_mul_f32 v117,s61,v117
v_mfma_f32_16x16x32_bf16 a[112:115],a[140:143],a[252:255],a[112:115]
ds_read_b64_tr_b16 a[140:141],v22 offset:50184
v_accvgpr_read_b32 v118,a106
v_mfma_f32_16x16x32_bf16 a[112:115],a[136:139],a[248:251],a[112:115]
ds_read_b64_tr_b16 a[136:137],v22 offset:33800
v_accvgpr_read_b32 v119,a107
v_mfma_f32_16x16x32_bf16 a[112:115],a[132:135],a[220:223],a[112:115]
ds_read_b64_tr_b16 a[132:133],v22 offset:50176
v_accvgpr_read_b32 v16,a70
v_mfma_f32_16x16x32_bf16 a[112:115],a[128:131],a[216:219],a[112:115]
ds_read_b64_tr_b16 a[128:129],v22 offset:33792
v_accvgpr_read_b32 v17,a71
v_mfma_f32_16x16x32_bf16 a[20:23],a[144:147],a[192:195],a[20:23]
ds_read_b64_tr_b16 a[130:131],v22 offset:35840
v_mul_f32 v98,s61,v98
v_mfma_f32_16x16x32_bf16 a[20:23],a[148:151],a[196:199],a[20:23]
ds_read_b64_tr_b16 a[134:135],v22 offset:52224
v_mul_f32 v99,s61,v99
v_mfma_f32_16x16x32_bf16 a[20:23],a[152:155],a[224:227],a[20:23]
ds_read_b64_tr_b16 a[138:139],v22 offset:35848
v_accvgpr_read_b32 v12,a102
v_mfma_f32_16x16x32_bf16 a[20:23],a[156:159],a[228:231],a[20:23]
ds_read_b64_tr_b16 a[142:143],v22 offset:52232
v_accvgpr_read_b32 v13,a103
v_mfma_f32_16x16x32_bf16 a[52:55],a[156:159],a[236:239],a[52:55]
v_mul_f32 v114,s61,v114
v_mul_f32 v115,s61,v115
v_mfma_f32_16x16x32_bf16 a[52:55],a[152:155],a[232:235],a[52:55]
v_accvgpr_read_b32 v14,a14
v_accvgpr_read_b32 v15,a15
v_mfma_f32_16x16x32_bf16 a[52:55],a[148:151],a[204:207],a[52:55]
buffer_load_dwordx4 v4,s[20:23],s47 offen offset:1024 nt lds
v_mfma_f32_16x16x32_bf16 a[52:55],a[144:147],a[200:203],a[52:55]
v_mul_f32 v70,s61,v70
v_mul_f32 v71,s61,v71
v_mfma_f32_16x16x32_bf16 a[84:87],a[144:147],a[208:211],a[84:87]
v_cvt_pk_bf16_f32 v84,v84,v85
v_mul_f32 v86,s61,v86
v_mfma_f32_16x16x32_bf16 a[84:87],a[148:151],a[212:215],a[84:87]
v_mul_f32 v87,s61,v87
v_cvt_pk_bf16_f32 v100,v100,v101
v_mfma_f32_16x16x32_bf16 a[84:87],a[152:155],a[240:243],a[84:87]
v_mul_f32 v102,s61,v102
v_mul_f32 v103,s61,v103
v_mfma_f32_16x16x32_bf16 a[84:87],a[156:159],a[244:247],a[84:87]
v_cvt_pk_bf16_f32 v116,v116,v117
v_mul_f32 v118,s61,v118
v_mfma_f32_16x16x32_bf16 a[116:119],a[156:159],a[252:255],a[116:119]
ds_read_b64_tr_b16 a[156:157],v22 offset:50440
v_mul_f32 v119,s61,v119
v_mfma_f32_16x16x32_bf16 a[116:119],a[152:155],a[248:251],a[116:119]
ds_read_b64_tr_b16 a[152:153],v22 offset:34056
v_mul_f32 v16,s61,v16
v_mfma_f32_16x16x32_bf16 a[116:119],a[148:151],a[220:223],a[116:119]
)A"
R"A(ds_read_b64_tr_b16 a[148:149],v22 offset:50432
v_mul_f32 v17,s61,v17
v_mfma_f32_16x16x32_bf16 a[116:119],a[144:147],a[216:219],a[116:119]
s_waitcnt lgkmcnt(3)
ds_read_b64_tr_b16 a[144:145],v22 offset:34048
v_cvt_pk_bf16_f32 v98,v98,v99
v_mfma_f32_16x16x32_bf16 a[24:27],a[160:163],a[192:195],a[24:27]
ds_read_b64_tr_b16 a[146:147],v22 offset:36096
v_mul_f32 v12,s61,v12
v_mfma_f32_16x16x32_bf16 a[24:27],a[164:167],a[196:199],a[24:27]
ds_read_b64_tr_b16 a[150:151],v22 offset:52480
v_mul_f32 v13,s61,v13
v_mfma_f32_16x16x32_bf16 a[24:27],a[168:171],a[224:227],a[24:27]
ds_read_b64_tr_b16 a[154:155],v22 offset:36104
v_cvt_pk_bf16_f32 v114,v114,v115
v_mfma_f32_16x16x32_bf16 a[24:27],a[172:175],a[228:231],a[24:27]
ds_read_b64_tr_b16 a[158:159],v22 offset:52488
v_mul_f32 v14,s61,v14
v_mfma_f32_16x16x32_bf16 a[56:59],a[172:175],a[236:239],a[56:59]
v_mul_f32 v15,s61,v15
v_cvt_pk_bf16_f32 v70,v70,v71
v_mfma_f32_16x16x32_bf16 a[56:59],a[168:171],a[232:235],a[56:59]
v_cvt_pk_bf16_f32 v85,v86,v87
v_cvt_pk_bf16_f32 v101,v102,v103
v_mfma_f32_16x16x32_bf16 a[56:59],a[164:167],a[204:207],a[56:59]
buffer_load_dwordx4 v5,s[20:23],s47 offen offset:2048 nt lds
v_mfma_f32_16x16x32_bf16 a[56:59],a[160:163],a[200:203],a[56:59]
v_cvt_pk_bf16_f32 v117,v118,v119
v_accvgpr_read_b32 v72,a16
v_mfma_f32_16x16x32_bf16 a[88:91],a[160:163],a[208:211],a[88:91]
v_accvgpr_read_b32 v73,a17
v_accvgpr_read_b32 v88,a48
v_mfma_f32_16x16x32_bf16 a[88:91],a[164:167],a[212:215],a[88:91]
v_accvgpr_read_b32 v89,a49
v_accvgpr_read_b32 v104,a80
v_mfma_f32_16x16x32_bf16 a[88:91],a[168:171],a[240:243],a[88:91]
v_accvgpr_read_b32 v105,a81
v_cvt_pk_bf16_f32 v99,v16,v17
v_mfma_f32_16x16x32_bf16 a[88:91],a[172:175],a[244:247],a[88:91]
v_cvt_pk_bf16_f32 v115,v12,v13
v_cvt_pk_bf16_f32 v71,v14,v15
v_mfma_f32_16x16x32_bf16 a[120:123],a[172:175],a[252:255],a[120:123]
ds_read_b64_tr_b16 a[172:173],v22 offset:50696
v_accvgpr_read_b32 v86,a44
v_mfma_f32_16x16x32_bf16 a[120:123],a[168:171],a[248:251],a[120:123]
ds_read_b64_tr_b16 a[168:169],v22 offset:34312
v_accvgpr_read_b32 v87,a45
v_mfma_f32_16x16x32_bf16 a[120:123],a[164:167],a[220:223],a[120:123]
ds_read_b64_tr_b16 a[164:165],v22 offset:50688
v_accvgpr_read_b32 v102,a76
v_mfma_f32_16x16x32_bf16 a[120:123],a[160:163],a[216:219],a[120:123]
ds_read_b64_tr_b16 a[160:161],v22 offset:34304
v_accvgpr_read_b32 v103,a77
v_mfma_f32_16x16x32_bf16 a[28:31],a[176:179],a[192:195],a[28:31]
ds_read_b64_tr_b16 a[162:163],v22 offset:36352
v_accvgpr_read_b32 v118,a108
v_mfma_f32_16x16x32_bf16 a[28:31],a[180:183],a[196:199],a[28:31]
ds_read_b64_tr_b16 a[166:167],v22 offset:52736
v_accvgpr_read_b32 v119,a109
v_mfma_f32_16x16x32_bf16 a[28:31],a[184:187],a[224:227],a[28:31]
ds_read_b64_tr_b16 a[170:171],v22 offset:36360
v_mul_f32 v72,s61,v72
v_mfma_f32_16x16x32_bf16 a[28:31],a[188:191],a[228:231],a[28:31]
ds_read_b64_tr_b16 a[174:175],v22 offset:52744
v_mul_f32 v73,s61,v73
v_mfma_f32_16x16x32_bf16 a[60:63],a[188:191],a[236:239],a[60:63]
v_accvgpr_read_b32 v74,a18
v_accvgpr_read_b32 v75,a19
v_mfma_f32_16x16x32_bf16 a[60:63],a[184:187],a[232:235],a[60:63]
v_mul_f32 v88,s61,v88
v_mul_f32 v89,s61,v89
v_mfma_f32_16x16x32_bf16 a[60:63],a[180:183],a[204:207],a[60:63]
buffer_load_dwordx4 v5,s[20:23],s47 offen offset:3072 nt lds
v_mfma_f32_16x16x32_bf16 a[60:63],a[176:179],a[200:203],a[60:63]
v_accvgpr_read_b32 v90,a50
v_accvgpr_read_b32 v91,a51
v_mfma_f32_16x16x32_bf16 a[92:95],a[176:179],a[208:211],a[92:95]
v_mul_f32 v104,s61,v104
v_mul_f32 v105,s61,v105
v_mfma_f32_16x16x32_bf16 a[92:95],a[180:183],a[212:215],a[92:95]
v_accvgpr_read_b32 v106,a82
v_accvgpr_read_b32 v107,a83
v_mfma_f32_16x16x32_bf16 a[92:95],a[184:187],a[240:243],a[92:95]
v_accvgpr_read_b32 v16,a46
v_accvgpr_read_b32 v17,a47
v_mfma_f32_16x16x32_bf16 a[92:95],a[188:191],a[244:247],a[92:95]
v_mul_f32 v86,s61,v86
v_mul_f32 v87,s61,v87
v_mfma_f32_16x16x32_bf16 a[124:127],a[188:191],a[252:255],a[124:127]
ds_read_b64_tr_b16 a[188:189],v22 offset:50952
v_accvgpr_read_b32 v12,a78
v_mfma_f32_16x16x32_bf16 a[124:127],a[184:187],a[248:251],a[124:127]
ds_read_b64_tr_b16 a[184:185],v22 offset:34568
v_accvgpr_read_b32 v13,a79
v_mfma_f32_16x16x32_bf16 a[124:127],a[180:183],a[220:223],a[124:127]
ds_read_b64_tr_b16 a[180:181],v22 offset:50944
v_mul_f32 v102,s61,v102
v_mfma_f32_16x16x32_bf16 a[124:127],a[176:179],a[216:219],a[124:127]
ds_read_b64_tr_b16 a[176:177],v22 offset:34560
v_mul_f32 v103,s61,v103
v_mfma_f32_16x16x32_bf16 v[128:131],a[128:131],a[224:227],v[128:131]
ds_read_b64_tr_b16 a[178:179],v22 offset:36608
v_accvgpr_read_b32 v14,a110
v_mfma_f32_16x16x32_bf16 v[128:131],a[132:135],a[228:231],v[128:131]
ds_read_b64_tr_b16 a[182:183],v22 offset:52992
v_accvgpr_read_b32 v15,a111
v_mfma_f32_16x16x32_bf16 v[136:139],a[132:135],a[236:239],v[136:139]
ds_read_b64_tr_b16 a[186:187],v22 offset:36616
v_mul_f32 v118,s61,v118
v_mfma_f32_16x16x32_bf16 v[136:139],a[128:131],a[232:235],v[136:139]
s_add_u32 m0,0,s52
ds_read_b64_tr_b16 a[190:191],v22 offset:53000
v_mul_f32 v119,s61,v119
v_mfma_f32_16x16x32_bf16 v[144:147],a[128:131],a[240:243],v[144:147]
v_cvt_pk_bf16_f32 v72,v72,v73
v_mul_f32 v74,s61,v74
v_mfma_f32_16x16x32_bf16 v[144:147],a[132:135],a[244:247],v[144:147]
v_mul_f32 v75,s61,v75
v_cvt_pk_bf16_f32 v88,v88,v89
v_mfma_f32_16x16x32_bf16 v[152:155],a[132:135],a[252:255],v[152:155]
buffer_load_dwordx4 v4,s[24:27],s47 offen nt lds
v_mfma_f32_16x16x32_bf16 v[152:155],a[128:131],a[248:251],v[152:155]
v_mul_f32 v90,s61,v90
v_mul_f32 v91,s61,v91
v_mfma_f32_16x16x32_bf16 v[132:135],a[136:139],a[224:227],v[132:135]
v_cvt_pk_bf16_f32 v104,v104,v105
v_mul_f32 v106,s61,v106
v_mfma_f32_16x16x32_bf16 v[132:135],a[140:143],a[228:231],v[132:135]
v_mul_f32 v107,s61,v107
v_mul_f32 v16,s61,v16
v_mfma_f32_16x16x32_bf16 v[140:143],a[140:143],a[236:239],v[140:143]
v_mul_f32 v17,s61,v17
v_cvt_pk_bf16_f32 v86,v86,v87
v_mfma_f32_16x16x32_bf16 v[140:143],a[136:139],a[232:235],v[140:143]
v_mul_f32 v12,s61,v12
v_mul_f32 v13,s61,v13
v_mfma_f32_16x16x32_bf16 v[148:151],a[136:139],a[240:243],v[148:151]
v_cvt_pk_bf16_f32 v102,v102,v103
v_mul_f32 v14,s61,v14
v_mfma_f32_16x16x32_bf16 v[148:151],a[140:143],a[244:247],v[148:151]
v_mul_f32 v15,s61,v15
v_cvt_pk_bf16_f32 v118,v118,v119
v_mfma_f32_16x16x32_bf16 v[156:159],a[140:143],a[252:255],v[156:159]
s_waitcnt vmcnt(5) lgkmcnt(0)
v_cvt_pk_bf16_f32 v73,v74,v75
v_cvt_pk_bf16_f32 v89,v90,v91
v_mfma_f32_16x16x32_bf16 v[156:159],a[136:139],a[248:251],v[156:159]
s_barrier
ds_read_b128 v[48:51],v18
v_cvt_pk_bf16_f32 v105,v106,v107
v_mfma_f32_16x16x32_bf16 v[160:163],a[144:147],a[224:227],v[160:163]
v_cvt_pk_bf16_f32 v87,v16,v17
v_cvt_pk_bf16_f32 v103,v12,v13
v_mfma_f32_16x16x32_bf16 v[160:163],a[148:151],a[228:231],v[160:163]
ds_read_b128 v[52:55],v18 offset:256
v_cvt_pk_bf16_f32 v119,v14,v15
v_mfma_f32_16x16x32_bf16 v[168:171],a[148:151],a[236:239],v[168:171]
v_accvgpr_read_b32 v74,a20
v_accvgpr_read_b32 v75,a21
v_mfma_f32_16x16x32_bf16 v[168:171],a[144:147],a[232:235],v[168:171]
ds_read_b128 v[56:59],v18 offset:512
v_accvgpr_read_b32 v90,a52
v_mfma_f32_16x16x32_bf16 v[176:179],a[144:147],a[240:243],v[176:179]
v_accvgpr_read_b32 v91,a53
v_accvgpr_read_b32 v106,a84
v_mfma_f32_16x16x32_bf16 v[176:179],a[148:151],a[244:247],v[176:179]
buffer_load_dwordx4 v5,s[24:27],s47 offen offset:1024 nt lds
v_mfma_f32_16x16x32_bf16 v[184:187],a[148:151],a[252:255],v[184:187]
ds_read_b128 v[60:63],v18 offset:768
v_accvgpr_read_b32 v107,a85
v_mfma_f32_16x16x32_bf16 v[184:187],a[144:147],a[248:251],v[184:187]
v_accvgpr_read_b32 v16,a22
v_accvgpr_read_b32 v17,a23
v_mfma_f32_16x16x32_bf16 v[164:167],a[152:155],a[224:227],v[164:167]
v_mul_f32 v74,s61,v74
v_mul_f32 v75,s61,v75
v_mfma_f32_16x16x32_bf16 v[164:167],a[156:159],a[228:231],v[164:167]
v_accvgpr_read_b32 v12,a54
v_accvgpr_read_b32 v13,a55
v_mfma_f32_16x16x32_bf16 v[172:175],a[156:159],a[236:239],v[172:175]
v_mul_f32 v90,s61,v90
v_mul_f32 v91,s61,v91
v_mfma_f32_16x16x32_bf16 v[172:175],a[152:155],a[232:235],v[172:175]
v_accvgpr_read_b32 v14,a86
v_accvgpr_read_b32 v15,a87
v_mfma_f32_16x16x32_bf16 v[180:183],a[152:155],a[240:243],v[180:183]
v_mul_f32 v106,s61,v106
v_mul_f32 v107,s61,v107
v_mfma_f32_16x16x32_bf16 v[180:183],a[156:159],a[244:247],v[180:183]
v_mul_f32 v16,s61,v16
v_mul_f32 v17,s61,v17
v_mfma_f32_16x16x32_bf16 v[188:191],a[156:159],a[252:255],v[188:191]
v_cvt_pk_bf16_f32 v74,v74,v75
v_mul_f32 v12,s61,v12
v_mfma_f32_16x16x32_bf16 v[188:191],a[152:155],a[248:251],v[188:191]
v_mul_f32 v13,s61,v13
v_cvt_pk_bf16_f32 v90,v90,v91
v_mfma_f32_16x16x32_bf16 v[192:195],a[160:163],a[224:227],v[192:195]
v_mul_f32 v14,s61,v14
v_mul_f32 v15,s61,v15
v_mfma_f32_16x16x32_bf16 v[192:195],a[164:167],a[228:231],v[192:195]
v_cvt_pk_bf16_f32 v106,v106,v107
v_cvt_pk_bf16_f32 v75,v16,v17
v_mfma_f32_16x16x32_bf16 v[200:203],a[164:167],a[236:239],v[200:203]
v_cvt_pk_bf16_f32 v91,v12,v13
v_cvt_pk_bf16_f32 v107,v14,v15
v_mfma_f32_16x16x32_bf16 v[200:203],a[160:163],a[232:235],v[200:203]
v_accvgpr_read_b32 v76,a24
v_accvgpr_read_b32 v77,a25
v_accvgpr_read_b32 v78,a26
v_accvgpr_read_b32 v79,a27
v_accvgpr_read_b32 v12,a28
v_accvgpr_read_b32 v13,a29
v_accvgpr_read_b32 v14,a30
v_accvgpr_read_b32 v15,a31
v_pk_mul_f32 v[76:77],s[60:61],v[76:77] op_sel:[1,0] op_sel_hi:[1,1]
v_pk_mul_f32 v[78:79],s[60:61],v[78:79] op_sel:[1,0] op_sel_hi:[1,1]
v_pk_mul_f32 v[12:13],s[60:61],v[12:13] op_sel:[1,0] op_sel_hi:[1,1]
v_pk_mul_f32 v[14:15],s[60:61],v[14:15] op_sel:[1,0] op_sel_hi:[1,1]
v_cvt_pk_bf16_f32 v76,v76,v77
v_cvt_pk_bf16_f32 v77,v78,v79
v_cvt_pk_bf16_f32 v78,v12,v13
v_cvt_pk_bf16_f32 v79,v14,v15
v_mfma_f32_16x16x32_bf16 v[208:211],a[160:163],a[240:243],v[208:211]
v_accvgpr_read_b32 v92,a56
v_accvgpr_read_b32 v93,a57
v_accvgpr_read_b32 v94,a58
v_accvgpr_read_b32 v95,a59
v_accvgpr_read_b32 v12,a60
v_accvgpr_read_b32 v13,a61
v_accvgpr_read_b32 v14,a62
v_accvgpr_read_b32 v15,a63
v_pk_mul_f32 v[92:93],s[60:61],v[92:93] op_sel:[1,0] op_sel_hi:[1,1]
v_pk_mul_f32 v[94:95],s[60:61],v[94:95] op_sel:[1,0] op_sel_hi:[1,1]
v_pk_mul_f32 v[12:13],s[60:61],v[12:13] op_sel:[1,0] op_sel_hi:[1,1]
v_pk_mul_f32 v[14:15],s[60:61],v[14:15] op_sel:[1,0] op_sel_hi:[1,1]
v_cvt_pk_bf16_f32 v92,v92,v93
v_cvt_pk_bf16_f32 v93,v94,v95
v_cvt_pk_bf16_f32 v94,v12,v13
v_cvt_pk_bf16_f32 v95,v14,v15
v_mfma_f32_16x16x32_bf16 v[208:211],a[164:167],a[244:247],v[208:211]
buffer_load_dwordx4 v4,s[24:27],s47 offen offset:2048 nt lds
v_mfma_f32_16x16x32_bf16 v[216:219],a[164:167],a[252:255],v[216:219]
ds_read_b128 a[128:131],v1
v_accvgpr_read_b32 v120,a112
v_mfma_f32_16x16x32_bf16 v[216:219],a[160:163],a[248:251],v[216:219]
v_accvgpr_read_b32 v121,a113
v_mul_f32 v120,s61,v120
v_mfma_f32_16x16x32_bf16 v[196:199],a[168:171],a[224:227],v[196:199]
v_mul_f32 v121,s61,v121
v_accvgpr_read_b32 v122,a114
v_mfma_f32_16x16x32_bf16 v[196:199],a[172:175],a[228:231],v[196:199]
ds_read_b128 a[132:135],v1 offset:256
v_accvgpr_read_b32 v123,a115
v_mfma_f32_16x16x32_bf16 v[204:207],a[172:175],a[236:239],v[204:207]
v_accvgpr_read_b32 v108,a88
v_accvgpr_read_b32 v109,a89
v_accvgpr_read_b32 v110,a90
v_accvgpr_read_b32 v111,a91
v_accvgpr_read_b32 v12,a92
v_accvgpr_read_b32 v13,a93
v_accvgpr_read_b32 v14,a94
v_accvgpr_read_b32 v15,a95
v_pk_mul_f32 v[108:109],s[60:61],v[108:109] op_sel:[1,0] op_sel_hi:[1,1]
v_pk_mul_f32 v[110:111],s[60:61],v[110:111] op_sel:[1,0] op_sel_hi:[1,1]
v_pk_mul_f32 v[12:13],s[60:61],v[12:13] op_sel:[1,0] op_sel_hi:[1,1]
v_pk_mul_f32 v[14:15],s[60:61],v[14:15] op_sel:[1,0] op_sel_hi:[1,1]
v_cvt_pk_bf16_f32 v108,v108,v109
v_cvt_pk_bf16_f32 v109,v110,v111
v_cvt_pk_bf16_f32 v110,v12,v13
v_cvt_pk_bf16_f32 v111,v14,v15
v_mfma_f32_16x16x32_bf16 v[204:207],a[168:171],a[232:235],v[204:207]
v_cvt_pk_bf16_f32 v120,v120,v121
v_mul_f32 v122,s61,v122
v_mfma_f32_16x16x32_bf16 v[212:215],a[168:171],a[240:243],v[212:215]
v_mul_f32 v123,s61,v123
v_cvt_pk_bf16_f32 v121,v122,v123
v_mfma_f32_16x16x32_bf16 v[212:215],a[172:175],a[244:247],v[212:215]
ds_read_b128 a[136:139],v1 offset:512
v_accvgpr_read_b32 v122,a116
v_mfma_f32_16x16x32_bf16 v[220:223],a[172:175],a[252:255],v[220:223]
v_accvgpr_read_b32 v123,a117
)A"
R"A(v_accvgpr_read_b32 v16,a118
v_mfma_f32_16x16x32_bf16 v[220:223],a[168:171],a[248:251],v[220:223]
v_accvgpr_read_b32 v17,a119
v_mul_f32 v122,s61,v122
v_mfma_f32_16x16x32_bf16 v[224:227],a[176:179],a[224:227],v[224:227]
ds_read_b128 a[140:143],v1 offset:768
v_mul_f32 v123,s61,v123
v_mfma_f32_16x16x32_bf16 v[224:227],a[180:183],a[228:231],v[224:227]
v_mul_f32 v16,s61,v16
v_mul_f32 v17,s61,v17
v_mfma_f32_16x16x32_bf16 v[232:235],a[180:183],a[236:239],v[232:235]
v_cvt_pk_bf16_f32 v122,v122,v123
v_cvt_pk_bf16_f32 v123,v16,v17
v_mfma_f32_16x16x32_bf16 v[232:235],a[176:179],a[232:235],v[232:235]
ds_read_b128 a[156:159],v1 offset:4864
v_mfma_f32_16x16x32_bf16 v[240:243],a[176:179],a[240:243],v[240:243]
v_accvgpr_read_b32 v124,a120
v_accvgpr_read_b32 v125,a121
v_accvgpr_read_b32 v126,a122
v_accvgpr_read_b32 v127,a123
v_accvgpr_read_b32 v12,a124
v_accvgpr_read_b32 v13,a125
v_accvgpr_read_b32 v14,a126
v_accvgpr_read_b32 v15,a127
v_pk_mul_f32 v[124:125],s[60:61],v[124:125] op_sel:[1,0] op_sel_hi:[1,1]
v_pk_mul_f32 v[126:127],s[60:61],v[126:127] op_sel:[1,0] op_sel_hi:[1,1]
v_pk_mul_f32 v[12:13],s[60:61],v[12:13] op_sel:[1,0] op_sel_hi:[1,1]
v_pk_mul_f32 v[14:15],s[60:61],v[14:15] op_sel:[1,0] op_sel_hi:[1,1]
v_cvt_pk_bf16_f32 v124,v124,v125
v_cvt_pk_bf16_f32 v125,v126,v127
v_cvt_pk_bf16_f32 v126,v12,v13
v_cvt_pk_bf16_f32 v127,v14,v15
v_mfma_f32_16x16x32_bf16 v[240:243],a[180:183],a[244:247],v[240:243]
buffer_load_dwordx4 v5,s[24:27],s47 offen offset:3072 nt lds
v_mfma_f32_16x16x32_bf16 v[248:251],a[180:183],a[252:255],v[248:251]
s_add_u32 s47,s47,16384
s_and_b32 s47,s47,0x3ffff
ds_read_b128 a[152:155],v1 offset:4608
v_mfma_f32_16x16x32_bf16 v[248:251],a[176:179],a[248:251],v[248:251]
s_or_b32 s47,s47,s46
v_mul_f32 v128,s61,v128
v_mul_f32 v129,s61,v129
v_mul_f32 v130,s61,v130
v_mul_f32 v131,s61,v131
v_cvt_pk_bf16_f32 v128,v128,v129
v_cvt_pk_bf16_f32 v129,v130,v131
v_pk_mul_f32 v[132:133],s[60:61],v[132:133] op_sel:[1,0] op_sel_hi:[1,1]
v_pk_mul_f32 v[134:135],s[60:61],v[134:135] op_sel:[1,0] op_sel_hi:[1,1]
v_pk_mul_f32 v[136:137],s[60:61],v[136:137] op_sel:[1,0] op_sel_hi:[1,1]
v_pk_mul_f32 v[138:139],s[60:61],v[138:139] op_sel:[1,0] op_sel_hi:[1,1]
v_pk_mul_f32 v[140:141],s[60:61],v[140:141] op_sel:[1,0] op_sel_hi:[1,1]
v_pk_mul_f32 v[142:143],s[60:61],v[142:143] op_sel:[1,0] op_sel_hi:[1,1]
v_pk_mul_f32 v[144:145],s[60:61],v[144:145] op_sel:[1,0] op_sel_hi:[1,1]
v_pk_mul_f32 v[146:147],s[60:61],v[146:147] op_sel:[1,0] op_sel_hi:[1,1]
v_pk_mul_f32 v[148:149],s[60:61],v[148:149] op_sel:[1,0] op_sel_hi:[1,1]
v_pk_mul_f32 v[150:151],s[60:61],v[150:151] op_sel:[1,0] op_sel_hi:[1,1]
v_pk_mul_f32 v[152:153],s[60:61],v[152:153] op_sel:[1,0] op_sel_hi:[1,1]
v_pk_mul_f32 v[154:155],s[60:61],v[154:155] op_sel:[1,0] op_sel_hi:[1,1]
v_pk_mul_f32 v[156:157],s[60:61],v[156:157] op_sel:[1,0] op_sel_hi:[1,1]
v_pk_mul_f32 v[158:159],s[60:61],v[158:159] op_sel:[1,0] op_sel_hi:[1,1]
v_mfma_f32_16x16x32_bf16 v[228:231],a[184:187],a[224:227],v[228:231]
s_and_b32 s46,s54,0xfffc0000
ds_read_b128 a[148:151],v1 offset:4352
v_cvt_pk_bf16_f32 v130,v132,v133
v_mfma_f32_16x16x32_bf16 v[228:231],a[188:191],a[228:231],v[228:231]
s_add_u32 s54,s54,16384
v_cvt_pk_bf16_f32 v132,v136,v137
v_cvt_pk_bf16_f32 v133,v138,v139
v_mfma_f32_16x16x32_bf16 v[236:239],a[188:191],a[236:239],v[236:239]
ds_read_b128 a[144:147],v1 offset:4096
v_cvt_pk_bf16_f32 v131,v134,v135
v_mfma_f32_16x16x32_bf16 v[236:239],a[184:187],a[232:235],v[236:239]
v_cvt_pk_bf16_f32 v134,v140,v141
v_cvt_pk_bf16_f32 v135,v142,v143
v_mfma_f32_16x16x32_bf16 v[244:247],a[184:187],a[240:243],v[244:247]
ds_read_b128 a[160:163],v1 offset:8192
v_cvt_pk_bf16_f32 v144,v144,v145
v_mfma_f32_16x16x32_bf16 v[244:247],a[188:191],a[244:247],v[244:247]
v_cvt_pk_bf16_f32 v145,v146,v147
v_cvt_pk_bf16_f32 v146,v148,v149
v_mfma_f32_16x16x32_bf16 v[252:255],a[188:191],a[252:255],v[252:255]
ds_read_b128 a[164:167],v1 offset:8448
v_cvt_pk_bf16_f32 v148,v152,v153
v_mfma_f32_16x16x32_bf16 v[252:255],a[184:187],a[248:251],v[252:255]
s_waitcnt lgkmcnt(4)
v_cvt_pk_bf16_f32 v149,v154,v155
v_cvt_pk_bf16_f32 v147,v150,v151
v_mfma_f32_16x16x32_bf16 v[32:35],v[48:51],v[64:67],0
ds_read_b128 a[168:171],v1 offset:8704
v_cvt_pk_bf16_f32 v150,v156,v157
v_mfma_f32_16x16x32_bf16 v[32:35],v[52:55],v[68:71],v[32:35]
ds_read_b64_tr_b16 a[192:193],v2
v_cvt_pk_bf16_f32 v151,v158,v159
v_mfma_f32_16x16x32_bf16 v[32:35],v[56:59],v[72:75],v[32:35]
v_mul_f32 v160,s61,v160
v_mul_f32 v161,s61,v161
v_mul_f32 v162,s61,v162
v_mul_f32 v163,s61,v163
v_cvt_pk_bf16_f32 v160,v160,v161
v_cvt_pk_bf16_f32 v161,v162,v163
v_pk_mul_f32 v[164:165],s[60:61],v[164:165] op_sel:[1,0] op_sel_hi:[1,1]
v_pk_mul_f32 v[166:167],s[60:61],v[166:167] op_sel:[1,0] op_sel_hi:[1,1]
v_pk_mul_f32 v[168:169],s[60:61],v[168:169] op_sel:[1,0] op_sel_hi:[1,1]
v_pk_mul_f32 v[170:171],s[60:61],v[170:171] op_sel:[1,0] op_sel_hi:[1,1]
v_pk_mul_f32 v[172:173],s[60:61],v[172:173] op_sel:[1,0] op_sel_hi:[1,1]
v_pk_mul_f32 v[174:175],s[60:61],v[174:175] op_sel:[1,0] op_sel_hi:[1,1]
v_pk_mul_f32 v[176:177],s[60:61],v[176:177] op_sel:[1,0] op_sel_hi:[1,1]
v_pk_mul_f32 v[178:179],s[60:61],v[178:179] op_sel:[1,0] op_sel_hi:[1,1]
v_pk_mul_f32 v[180:181],s[60:61],v[180:181] op_sel:[1,0] op_sel_hi:[1,1]
v_pk_mul_f32 v[182:183],s[60:61],v[182:183] op_sel:[1,0] op_sel_hi:[1,1]
v_pk_mul_f32 v[184:185],s[60:61],v[184:185] op_sel:[1,0] op_sel_hi:[1,1]
v_pk_mul_f32 v[186:187],s[60:61],v[186:187] op_sel:[1,0] op_sel_hi:[1,1]
v_pk_mul_f32 v[188:189],s[60:61],v[188:189] op_sel:[1,0] op_sel_hi:[1,1]
v_pk_mul_f32 v[190:191],s[60:61],v[190:191] op_sel:[1,0] op_sel_hi:[1,1]
v_mfma_f32_16x16x32_bf16 v[32:35],v[60:63],v[76:79],v[32:35]
ds_read_b128 a[172:175],v1 offset:8960
v_cvt_pk_bf16_f32 v162,v164,v165
v_mfma_f32_16x16x32_bf16 v[36:39],v[48:51],v[80:83],0
v_cvt_pk_bf16_f32 v164,v168,v169
v_cvt_pk_bf16_f32 v165,v170,v171
v_mfma_f32_16x16x32_bf16 v[36:39],v[52:55],v[84:87],v[36:39]
ds_read_b128 a[188:191],v1 offset:13056
v_cvt_pk_bf16_f32 v163,v166,v167
v_mfma_f32_16x16x32_bf16 v[36:39],v[56:59],v[88:91],v[36:39]
v_cvt_pk_bf16_f32 v166,v172,v173
v_cvt_pk_bf16_f32 v167,v174,v175
v_mfma_f32_16x16x32_bf16 v[36:39],v[60:63],v[92:95],v[36:39]
ds_read_b128 a[184:187],v1 offset:12800
v_cvt_pk_bf16_f32 v176,v176,v177
v_mfma_f32_16x16x32_bf16 v[40:43],v[48:51],v[96:99],0
v_cvt_pk_bf16_f32 v177,v178,v179
v_cvt_pk_bf16_f32 v178,v180,v181
v_mfma_f32_16x16x32_bf16 v[40:43],v[52:55],v[100:103],v[40:43]
s_add_u32 m0,32768,s51
v_max3_f32 v32,v32,v33,v34
v_max3_f32 v32,v32,v35,s18
v_mfma_f32_16x16x32_bf16 v[40:43],v[56:59],v[104:107],v[40:43]
v_sub_f32 v35,0,v32
v_sub_f32 v34,0,v32
v_mfma_f32_16x16x32_bf16 v[40:43],v[60:63],v[108:111],v[40:43]
v_sub_f32 v33,0,v32
v_sub_f32 v32,0,v32
v_mfma_f32_16x16x32_bf16 v[44:47],v[48:51],v[112:115],0
buffer_load_dwordx4 v4,s[20:23],s47 offen nt lds
v_mfma_f32_16x16x32_bf16 v[44:47],v[52:55],v[116:119],v[44:47]
ds_read_b128 a[180:183],v1 offset:12544
v_max3_f32 v36,v36,v37,v38
v_mfma_f32_16x16x32_bf16 v[44:47],v[56:59],v[120:123],v[44:47]
v_max3_f32 v36,v36,v39,s19
v_sub_f32 v39,0,v36
v_mfma_f32_16x16x32_bf16 v[44:47],v[60:63],v[124:127],v[44:47]
ds_read_b128 a[176:179],v1 offset:12288
v_sub_f32 v38,0,v36
v_mfma_f32_16x16x32_bf16 v[136:139],a[128:131],v[64:67],v[32:35]
v_sub_f32 v37,0,v36
v_sub_f32 v36,0,v36
v_mfma_f32_16x16x32_bf16 v[136:139],a[132:135],v[68:71],v[136:139]
v_cvt_pk_bf16_f32 v180,v184,v185
v_cvt_pk_bf16_f32 v181,v186,v187
v_mfma_f32_16x16x32_bf16 v[136:139],a[136:139],v[72:75],v[136:139]
v_cvt_pk_bf16_f32 v179,v182,v183
v_cvt_pk_bf16_f32 v182,v188,v189
v_mfma_f32_16x16x32_bf16 v[136:139],a[140:143],v[76:79],v[136:139]
v_cvt_pk_bf16_f32 v183,v190,v191
v_max3_f32 v40,v40,v41,v42
v_mfma_f32_16x16x32_bf16 v[140:143],a[156:159],v[76:79],v[32:35]
v_max3_f32 v40,v40,v43,s100
v_sub_f32 v43,0,v40
v_mfma_f32_16x16x32_bf16 v[140:143],a[152:155],v[72:75],v[140:143]
s_waitcnt lgkmcnt(1)
buffer_load_dwordx4 v4,s[20:23],s47 offen offset:1024 nt lds
v_mfma_f32_16x16x32_bf16 v[140:143],a[148:151],v[68:71],v[140:143]
v_sub_f32 v42,0,v40
v_sub_f32 v41,0,v40
v_mfma_f32_16x16x32_bf16 v[140:143],a[144:147],v[64:67],v[140:143]
ds_read_b64_tr_b16 a[194:195],v2 offset:4096
v_sub_f32 v40,0,v40
v_mfma_f32_16x16x32_bf16 v[152:155],a[160:163],v[64:67],v[32:35]
v_mul_f32 v192,s61,v192
v_mul_f32 v193,s61,v193
v_mul_f32 v194,s61,v194
v_mul_f32 v195,s61,v195
v_cvt_pk_bf16_f32 v192,v192,v193
v_cvt_pk_bf16_f32 v193,v194,v195
v_pk_mul_f32 v[196:197],s[60:61],v[196:197] op_sel:[1,0] op_sel_hi:[1,1]
v_pk_mul_f32 v[198:199],s[60:61],v[198:199] op_sel:[1,0] op_sel_hi:[1,1]
v_pk_mul_f32 v[200:201],s[60:61],v[200:201] op_sel:[1,0] op_sel_hi:[1,1]
v_pk_mul_f32 v[202:203],s[60:61],v[202:203] op_sel:[1,0] op_sel_hi:[1,1]
v_pk_mul_f32 v[204:205],s[60:61],v[204:205] op_sel:[1,0] op_sel_hi:[1,1]
v_pk_mul_f32 v[206:207],s[60:61],v[206:207] op_sel:[1,0] op_sel_hi:[1,1]
v_pk_mul_f32 v[208:209],s[60:61],v[208:209] op_sel:[1,0] op_sel_hi:[1,1]
v_pk_mul_f32 v[210:211],s[60:61],v[210:211] op_sel:[1,0] op_sel_hi:[1,1]
v_pk_mul_f32 v[212:213],s[60:61],v[212:213] op_sel:[1,0] op_sel_hi:[1,1]
v_pk_mul_f32 v[214:215],s[60:61],v[214:215] op_sel:[1,0] op_sel_hi:[1,1]
v_pk_mul_f32 v[216:217],s[60:61],v[216:217] op_sel:[1,0] op_sel_hi:[1,1]
v_pk_mul_f32 v[218:219],s[60:61],v[218:219] op_sel:[1,0] op_sel_hi:[1,1]
v_pk_mul_f32 v[220:221],s[60:61],v[220:221] op_sel:[1,0] op_sel_hi:[1,1]
v_pk_mul_f32 v[222:223],s[60:61],v[222:223] op_sel:[1,0] op_sel_hi:[1,1]
v_mfma_f32_16x16x32_bf16 v[152:155],a[164:167],v[68:71],v[152:155]
v_cvt_pk_bf16_f32 v194,v196,v197
v_cvt_pk_bf16_f32 v196,v200,v201
v_mfma_f32_16x16x32_bf16 v[152:155],a[168:171],v[72:75],v[152:155]
v_cvt_pk_bf16_f32 v197,v202,v203
v_cvt_pk_bf16_f32 v195,v198,v199
v_mfma_f32_16x16x32_bf16 v[152:155],a[172:175],v[76:79],v[152:155]
v_cvt_pk_bf16_f32 v198,v204,v205
v_cvt_pk_bf16_f32 v199,v206,v207
v_mfma_f32_16x16x32_bf16 v[156:159],a[188:191],v[76:79],v[32:35]
v_cvt_pk_bf16_f32 v208,v208,v209
v_cvt_pk_bf16_f32 v209,v210,v211
v_mfma_f32_16x16x32_bf16 v[156:159],a[184:187],v[72:75],v[156:159]
v_cvt_pk_bf16_f32 v210,v212,v213
v_cvt_pk_bf16_f32 v212,v216,v217
v_mfma_f32_16x16x32_bf16 v[156:159],a[180:183],v[68:71],v[156:159]
s_waitcnt lgkmcnt(1)
buffer_load_dwordx4 v5,s[20:23],s47 offen offset:2048 nt lds
v_mfma_f32_16x16x32_bf16 v[156:159],a[176:179],v[64:67],v[156:159]
ds_read_b64_tr_b16 a[196:197],v2 offset:8192
v_cvt_pk_bf16_f32 v213,v218,v219
v_mfma_f32_16x16x32_bf16 v[168:171],a[128:131],v[80:83],v[36:39]
ds_read_b64_tr_b16 a[198:199],v2 offset:12288
ds_read_b64_tr_b16 a[204:205],v3 offset:8192
v_mfma_f32_16x16x32_bf16 v[168:171],a[132:135],v[84:87],v[168:171]
ds_read_b64_tr_b16 a[206:207],v3 offset:12288
v_cvt_pk_bf16_f32 v211,v214,v215
v_mfma_f32_16x16x32_bf16 v[168:171],a[136:139],v[88:91],v[168:171]
ds_read_b64_tr_b16 a[200:201],v3
v_cvt_pk_bf16_f32 v214,v220,v221
v_mfma_f32_16x16x32_bf16 v[168:171],a[140:143],v[92:95],v[168:171]
ds_read_b64_tr_b16 a[202:203],v3 offset:4096
v_cvt_pk_bf16_f32 v215,v222,v223
v_mfma_f32_16x16x32_bf16 v[172:175],a[156:159],v[92:95],v[36:39]
ds_read_b64_tr_b16 a[208:209],v2 offset:256
ds_read_b64_tr_b16 a[210:211],v2 offset:4352
v_mfma_f32_16x16x32_bf16 v[172:175],a[152:155],v[88:91],v[172:175]
v_exp_f32 v136,v136
v_mfma_f32_16x16x32_bf16 v[172:175],a[148:151],v[84:87],v[172:175]
v_exp_f32 v137,v137
v_mfma_f32_16x16x32_bf16 v[172:175],a[144:147],v[80:83],v[172:175]
buffer_load_dwordx4 v5,s[20:23],s47 offen offset:3072 nt lds
v_mfma_f32_16x16x32_bf16 v[184:187],a[160:163],v[80:83],v[36:39]
v_exp_f32 v138,v138
v_mfma_f32_16x16x32_bf16 v[184:187],a[164:167],v[84:87],v[184:187]
v_exp_f32 v139,v139
v_mfma_f32_16x16x32_bf16 v[184:187],a[168:171],v[88:91],v[184:187]
ds_read_b64_tr_b16 a[212:213],v2 offset:8448
ds_read_b64_tr_b16 a[214:215],v2 offset:12544
v_mfma_f32_16x16x32_bf16 v[184:187],a[172:175],v[92:95],v[184:187]
v_cvt_pk_bf16_f32 v136,v136,v137
v_cvt_pk_bf16_f32 v137,v138,v139
v_mfma_f32_16x16x32_bf16 v[188:191],a[188:191],v[92:95],v[36:39]
v_exp_f32 v140,v140
v_mfma_f32_16x16x32_bf16 v[188:191],a[184:187],v[88:91],v[188:191]
)A"
R"A(s_add_u32 m0,32768,s52
v_exp_f32 v141,v141
v_mfma_f32_16x16x32_bf16 v[188:191],a[180:183],v[84:87],v[188:191]
v_exp_f32 v142,v142
v_mfma_f32_16x16x32_bf16 v[188:191],a[176:179],v[80:83],v[188:191]
v_exp_f32 v143,v143
v_mfma_f32_16x16x32_bf16 v[200:203],a[128:131],v[96:99],v[40:43]
buffer_load_dwordx4 v4,s[24:27],s47 offen nt lds
v_mfma_f32_16x16x32_bf16 v[200:203],a[132:135],v[100:103],v[200:203]
ds_read_b64_tr_b16 a[220:221],v3 offset:8448
ds_read_b64_tr_b16 a[222:223],v3 offset:12544
v_mfma_f32_16x16x32_bf16 v[200:203],a[136:139],v[104:107],v[200:203]
v_cvt_pk_bf16_f32 v138,v140,v141
v_cvt_pk_bf16_f32 v139,v142,v143
v_mfma_f32_16x16x32_bf16 v[200:203],a[140:143],v[108:111],v[200:203]
v_exp_f32 v152,v152
v_mfma_f32_16x16x32_bf16 v[204:207],a[156:159],v[108:111],v[40:43]
v_exp_f32 v153,v153
v_mfma_f32_16x16x32_bf16 v[204:207],a[152:155],v[104:107],v[204:207]
v_exp_f32 v154,v154
v_mfma_f32_16x16x32_bf16 v[204:207],a[148:151],v[100:103],v[204:207]
v_exp_f32 v155,v155
v_mfma_f32_16x16x32_bf16 v[204:207],a[144:147],v[96:99],v[204:207]
v_exp_f32 v156,v156
v_mfma_f32_16x16x32_bf16 v[216:219],a[160:163],v[96:99],v[40:43]
v_exp_f32 v157,v157
v_mfma_f32_16x16x32_bf16 v[216:219],a[164:167],v[100:103],v[216:219]
buffer_load_dwordx4 v5,s[24:27],s47 offen offset:1024 nt lds
v_mfma_f32_16x16x32_bf16 v[216:219],a[168:171],v[104:107],v[216:219]
v_exp_f32 v158,v158
v_mfma_f32_16x16x32_bf16 v[216:219],a[172:175],v[108:111],v[216:219]
v_exp_f32 v159,v159
v_mfma_f32_16x16x32_bf16 v[220:223],a[188:191],v[108:111],v[40:43]
ds_read_b64_tr_b16 a[216:217],v3 offset:256
ds_read_b64_tr_b16 a[218:219],v3 offset:4352
v_mfma_f32_16x16x32_bf16 v[220:223],a[184:187],v[104:107],v[220:223]
v_cvt_pk_bf16_f32 v140,v152,v153
v_cvt_pk_bf16_f32 v141,v154,v155
v_mfma_f32_16x16x32_bf16 v[220:223],a[180:183],v[100:103],v[220:223]
v_cvt_pk_bf16_f32 v142,v156,v157
v_cvt_pk_bf16_f32 v143,v158,v159
v_mfma_f32_16x16x32_bf16 v[220:223],a[176:179],v[96:99],v[220:223]
s_waitcnt lgkmcnt(2)
ds_read_b64_tr_b16 a[224:225],v2 offset:512
ds_read_b64_tr_b16 a[226:227],v2 offset:4608
v_mfma_f32_16x16x32_bf16 a[0:3],a[192:195],v[136:139],0
ds_read_b64_tr_b16 a[228:229],v2 offset:8704
ds_read_b64_tr_b16 a[230:231],v2 offset:12800
v_mfma_f32_16x16x32_bf16 a[0:3],a[196:199],v[140:143],a[0:3]
ds_read_b64_tr_b16 a[236:237],v3 offset:8704
ds_read_b64_tr_b16 a[238:239],v3 offset:12800
v_mfma_f32_16x16x32_bf16 a[4:7],a[204:207],v[140:143],0
ds_read_b64_tr_b16 a[232:233],v3 offset:512
ds_read_b64_tr_b16 a[234:235],v3 offset:4608
v_mfma_f32_16x16x32_bf16 a[4:7],a[200:203],v[136:139],a[4:7]
ds_read_b64_tr_b16 a[240:241],v2 offset:768
ds_read_b64_tr_b16 a[242:243],v2 offset:4864
v_mfma_f32_16x16x32_bf16 a[8:11],a[208:211],v[136:139],0
s_memrealtime s[76:77]
ds_read_b64_tr_b16 a[244:245],v2 offset:8960
ds_read_b64_tr_b16 a[246:247],v2 offset:13056
v_mfma_f32_16x16x32_bf16 a[8:11],a[212:215],v[140:143],a[8:11]
buffer_load_dwordx4 v4,s[24:27],s47 offen offset:2048 nt lds
ds_read_b64_tr_b16 a[252:253],v3 offset:8960
ds_read_b64_tr_b16 a[254:255],v3 offset:13056
v_mfma_f32_16x16x32_bf16 a[12:15],a[220:223],v[140:143],0
s_waitcnt lgkmcnt(14)
ds_read_b64_tr_b16 a[248:249],v3 offset:768
ds_read_b64_tr_b16 a[250:251],v3 offset:4864
v_mfma_f32_16x16x32_bf16 a[12:15],a[216:219],v[136:139],a[12:15]
s_waitcnt lgkmcnt(14)
v_mfma_f32_16x16x32_bf16 a[16:19],a[224:227],v[136:139],0
s_waitcnt lgkmcnt(12)
v_mfma_f32_16x16x32_bf16 a[16:19],a[228:231],v[140:143],a[16:19]
s_waitcnt lgkmcnt(10)
v_mfma_f32_16x16x32_bf16 a[20:23],a[236:239],v[140:143],0
s_waitcnt lgkmcnt(0)
v_mfma_f32_16x16x32_bf16 a[20:23],a[232:235],v[136:139],a[20:23]
s_add_u32 s78,s76,s60
v_max3_f32 v44,v44,v45,v46
v_max3_f32 v44,v44,v47,s101
v_mfma_f32_16x16x32_bf16 a[24:27],a[240:243],v[136:139],0
v_mov_b32 v48,0
v_mov_b32 v49,0
v_mfma_f32_16x16x32_bf16 a[24:27],a[244:247],v[140:143],a[24:27]
v_mov_b32 v50,0
v_mov_b32 v51,0
v_mfma_f32_16x16x32_bf16 a[28:31],a[252:255],v[140:143],0
buffer_load_dwordx4 v5,s[24:27],s47 offen offset:3072 nt lds
v_sub_f32 v47,0,v44
v_sub_f32 v46,0,v44
v_sub_f32 v45,0,v44
v_sub_f32 v44,0,v44
v_mfma_f32_16x16x32_bf16 a[28:31],a[248:251],v[136:139],a[28:31]
v_mul_f32 v224,s61,v224
v_mul_f32 v225,s61,v225
v_mul_f32 v226,s61,v226
v_mul_f32 v227,s61,v227
v_cvt_pk_bf16_f32 v224,v224,v225
v_cvt_pk_bf16_f32 v225,v226,v227
v_pk_mul_f32 v[228:229],s[60:61],v[228:229] op_sel:[1,0] op_sel_hi:[1,1]
v_pk_mul_f32 v[230:231],s[60:61],v[230:231] op_sel:[1,0] op_sel_hi:[1,1]
v_pk_mul_f32 v[232:233],s[60:61],v[232:233] op_sel:[1,0] op_sel_hi:[1,1]
v_pk_mul_f32 v[234:235],s[60:61],v[234:235] op_sel:[1,0] op_sel_hi:[1,1]
v_pk_mul_f32 v[236:237],s[60:61],v[236:237] op_sel:[1,0] op_sel_hi:[1,1]
v_pk_mul_f32 v[238:239],s[60:61],v[238:239] op_sel:[1,0] op_sel_hi:[1,1]
v_pk_mul_f32 v[240:241],s[60:61],v[240:241] op_sel:[1,0] op_sel_hi:[1,1]
v_pk_mul_f32 v[242:243],s[60:61],v[242:243] op_sel:[1,0] op_sel_hi:[1,1]
v_pk_mul_f32 v[244:245],s[60:61],v[244:245] op_sel:[1,0] op_sel_hi:[1,1]
v_pk_mul_f32 v[246:247],s[60:61],v[246:247] op_sel:[1,0] op_sel_hi:[1,1]
v_pk_mul_f32 v[248:249],s[60:61],v[248:249] op_sel:[1,0] op_sel_hi:[1,1]
v_pk_mul_f32 v[250:251],s[60:61],v[250:251] op_sel:[1,0] op_sel_hi:[1,1]
v_pk_mul_f32 v[252:253],s[60:61],v[252:253] op_sel:[1,0] op_sel_hi:[1,1]
v_pk_mul_f32 v[254:255],s[60:61],v[254:255] op_sel:[1,0] op_sel_hi:[1,1]
v_smfmac_f32_16x16x64_bf16 v[48:51],v[28:31],v[136:143],v27
v_cvt_pk_bf16_f32 v226,v228,v229
v_cvt_pk_bf16_f32 v228,v232,v233
v_cvt_pk_bf16_f32 v229,v234,v235
v_mfma_f32_16x16x32_bf16 v[232:235],a[128:131],v[112:115],v[44:47]
s_waitcnt vmcnt(8)
s_add_u32 s47,s47,16384
v_cvt_pk_bf16_f32 v227,v230,v231
v_cvt_pk_bf16_f32 v230,v236,v237
v_mfma_f32_16x16x32_bf16 v[232:235],a[132:135],v[116:119],v[232:235]
s_barrier
s_add_u32 m0,65536,s51
v_cvt_pk_bf16_f32 v231,v238,v239
v_mfma_f32_16x16x32_bf16 v[232:235],a[136:139],v[120:123],v[232:235]
s_and_b32 s47,s47,0x3ffff
s_or_b32 s47,s47,s46
v_cvt_pk_bf16_f32 v240,v240,v241
v_mfma_f32_16x16x32_bf16 v[232:235],a[140:143],v[124:127],v[232:235]
s_and_b32 s46,s54,0xfffc0000
v_cvt_pk_bf16_f32 v241,v242,v243
v_cvt_pk_bf16_f32 v242,v244,v245
v_mfma_f32_16x16x32_bf16 v[236:239],a[156:159],v[124:127],v[44:47]
v_cvt_pk_bf16_f32 v244,v248,v249
v_cvt_pk_bf16_f32 v245,v250,v251
v_mfma_f32_16x16x32_bf16 v[236:239],a[152:155],v[120:123],v[236:239]
buffer_load_dwordx4 v4,s[20:23],s47 offen nt lds
v_mfma_f32_16x16x32_bf16 v[236:239],a[148:151],v[116:119],v[236:239]
v_cvt_pk_bf16_f32 v243,v246,v247
v_cvt_pk_bf16_f32 v246,v252,v253
v_mfma_f32_16x16x32_bf16 v[236:239],a[144:147],v[112:115],v[236:239]
v_cvt_pk_bf16_f32 v247,v254,v255
v_add_u32 v1,s48,v1
v_mfma_f32_16x16x32_bf16 v[248:251],a[160:163],v[112:115],v[44:47]
v_exp_f32 v168,v168
v_mfma_f32_16x16x32_bf16 v[248:251],a[164:167],v[116:119],v[248:251]
v_exp_f32 v169,v169
v_mfma_f32_16x16x32_bf16 v[248:251],a[168:171],v[120:123],v[248:251]
v_exp_f32 v170,v170
v_mfma_f32_16x16x32_bf16 v[248:251],a[172:175],v[124:127],v[248:251]
v_exp_f32 v171,v171
v_mfma_f32_16x16x32_bf16 v[252:255],a[188:191],v[124:127],v[44:47]
v_cvt_pk_bf16_f32 v168,v168,v169
v_exp_f32 v172,v172
v_exp_f32 v173,v173
v_exp_f32 v174,v174
v_exp_f32 v175,v175
v_mfma_f32_16x16x32_bf16 v[252:255],a[184:187],v[120:123],v[252:255]
v_cvt_pk_bf16_f32 v169,v170,v171
v_cvt_pk_bf16_f32 v170,v172,v173
v_cvt_pk_bf16_f32 v171,v174,v175
v_exp_f32 v184,v184
v_exp_f32 v185,v185
v_exp_f32 v186,v186
v_exp_f32 v187,v187
v_exp_f32 v188,v188
v_exp_f32 v189,v189
v_exp_f32 v190,v190
v_exp_f32 v191,v191
v_mfma_f32_16x16x32_bf16 v[252:255],a[180:183],v[116:119],v[252:255]
v_cvt_pk_bf16_f32 v172,v184,v185
v_cvt_pk_bf16_f32 v173,v186,v187
v_cvt_pk_bf16_f32 v174,v188,v189
v_cvt_pk_bf16_f32 v175,v190,v191
v_mfma_f32_16x16x32_bf16 v[252:255],a[176:179],v[112:115],v[252:255]
ds_read_b128 a[128:131],v1
v_mov_b32 v52,0
v_mfma_f32_16x16x32_bf16 a[32:35],a[192:195],v[168:171],0
v_mov_b32 v53,0
v_mov_b32 v54,0
v_mfma_f32_16x16x32_bf16 a[32:35],a[196:199],v[172:175],a[32:35]
ds_read_b128 a[132:135],v1 offset:256
v_mov_b32 v55,0
v_mfma_f32_16x16x32_bf16 a[36:39],a[204:207],v[172:175],0
buffer_load_dwordx4 v4,s[20:23],s47 offen offset:1024 nt lds
v_mfma_f32_16x16x32_bf16 a[36:39],a[200:203],v[168:171],a[36:39]
ds_read_b128 a[136:139],v1 offset:512
v_mov_b32 v56,0
v_mfma_f32_16x16x32_bf16 a[40:43],a[208:211],v[168:171],0
v_exp_f32 v200,v200
v_mfma_f32_16x16x32_bf16 a[40:43],a[212:215],v[172:175],a[40:43]
ds_read_b128 a[140:143],v1 offset:768
v_mov_b32 v57,0
v_mfma_f32_16x16x32_bf16 a[44:47],a[220:223],v[172:175],0
v_exp_f32 v201,v201
v_mfma_f32_16x16x32_bf16 a[44:47],a[216:219],v[168:171],a[44:47]
ds_read_b128 a[156:159],v1 offset:4864
v_cvt_pk_bf16_f32 v200,v200,v201
v_mfma_f32_16x16x32_bf16 a[48:51],a[224:227],v[168:171],0
v_exp_f32 v202,v202
v_mfma_f32_16x16x32_bf16 a[48:51],a[228:231],v[172:175],a[48:51]
ds_read_b128 a[152:155],v1 offset:4608
v_mov_b32 v58,0
v_mfma_f32_16x16x32_bf16 a[52:55],a[236:239],v[172:175],0
v_exp_f32 v203,v203
v_mfma_f32_16x16x32_bf16 a[52:55],a[232:235],v[168:171],a[52:55]
ds_read_b128 a[148:151],v1 offset:4352
v_cvt_pk_bf16_f32 v201,v202,v203
v_mfma_f32_16x16x32_bf16 a[56:59],a[240:243],v[168:171],0
v_exp_f32 v204,v204
v_mfma_f32_16x16x32_bf16 a[56:59],a[244:247],v[172:175],a[56:59]
ds_read_b128 a[144:147],v1 offset:4096
v_mov_b32 v59,0
v_mfma_f32_16x16x32_bf16 a[60:63],a[252:255],v[172:175],0
v_exp_f32 v205,v205
v_mfma_f32_16x16x32_bf16 a[60:63],a[248:251],v[168:171],a[60:63]
ds_read_b128 a[160:163],v1 offset:8192
v_cvt_pk_bf16_f32 v202,v204,v205
v_smfmac_f32_16x16x64_bf16 v[52:55],v[28:31],v[168:175],v27
s_waitcnt lgkmcnt(4)
v_mov_b32 v60,0
v_mfma_f32_16x16x32_bf16 v[136:139],a[128:131],v[64:67],v[32:35]
ds_read_b128 a[164:167],v1 offset:8448
v_mov_b32 v61,0
v_mfma_f32_16x16x32_bf16 v[136:139],a[132:135],v[68:71],v[136:139]
ds_read_b128 a[168:171],v1 offset:8704
v_mfma_f32_16x16x32_bf16 v[136:139],a[136:139],v[72:75],v[136:139]
buffer_load_dwordx4 v5,s[20:23],s47 offen offset:2048 nt lds
ds_read_b128 a[172:175],v1 offset:8960
v_mfma_f32_16x16x32_bf16 v[136:139],a[140:143],v[76:79],v[136:139]
ds_read_b128 a[188:191],v1 offset:13056
v_mfma_f32_16x16x32_bf16 v[140:143],a[156:159],v[76:79],v[32:35]
s_waitcnt lgkmcnt(5)
ds_read_b128 a[184:187],v1 offset:12800
v_mfma_f32_16x16x32_bf16 v[140:143],a[152:155],v[72:75],v[140:143]
ds_read_b128 a[180:183],v1 offset:12544
v_mfma_f32_16x16x32_bf16 v[140:143],a[148:151],v[68:71],v[140:143]
ds_read_b128 a[176:179],v1 offset:12288
v_mfma_f32_16x16x32_bf16 v[140:143],a[144:147],v[64:67],v[140:143]
s_waitcnt lgkmcnt(7)
v_mfma_f32_16x16x32_bf16 v[152:155],a[160:163],v[64:67],v[32:35]
s_waitcnt lgkmcnt(6)
v_mfma_f32_16x16x32_bf16 v[152:155],a[164:167],v[68:71],v[152:155]
s_waitcnt lgkmcnt(5)
v_mfma_f32_16x16x32_bf16 v[152:155],a[168:171],v[72:75],v[152:155]
s_waitcnt lgkmcnt(4)
v_exp_f32 v206,v206
v_mfma_f32_16x16x32_bf16 v[152:155],a[172:175],v[76:79],v[152:155]
s_waitcnt lgkmcnt(3)
v_exp_f32 v207,v207
v_mfma_f32_16x16x32_bf16 v[156:159],a[188:191],v[76:79],v[32:35]
s_waitcnt lgkmcnt(2)
v_cvt_pk_bf16_f32 v203,v206,v207
v_mov_b32 v62,0
v_mfma_f32_16x16x32_bf16 v[156:159],a[184:187],v[72:75],v[156:159]
s_waitcnt lgkmcnt(1)
v_exp_f32 v216,v216
v_exp_f32 v217,v217
v_exp_f32 v218,v218
v_exp_f32 v219,v219
v_exp_f32 v220,v220
v_exp_f32 v221,v221
v_exp_f32 v222,v222
v_exp_f32 v223,v223
v_mfma_f32_16x16x32_bf16 v[156:159],a[180:183],v[68:71],v[156:159]
s_waitcnt lgkmcnt(0)
v_cvt_pk_bf16_f32 v204,v216,v217
v_cvt_pk_bf16_f32 v205,v218,v219
v_cvt_pk_bf16_f32 v206,v220,v221
v_cvt_pk_bf16_f32 v207,v222,v223
v_mfma_f32_16x16x32_bf16 v[156:159],a[176:179],v[64:67],v[156:159]
buffer_load_dwordx4 v5,s[20:23],s47 offen offset:3072 nt lds
v_mfma_f32_16x16x32_bf16 a[64:67],a[192:195],v[200:203],0
v_exp_f32 v232,v232
v_mfma_f32_16x16x32_bf16 a[64:67],a[196:199],v[204:207],a[64:67]
v_exp_f32 v233,v233
v_mfma_f32_16x16x32_bf16 a[68:71],a[204:207],v[204:207],0
v_exp_f32 v234,v234
v_mfma_f32_16x16x32_bf16 a[68:71],a[200:203],v[200:203],a[68:71]
v_exp_f32 v235,v235
v_mfma_f32_16x16x32_bf16 a[72:75],a[208:211],v[200:203],0
)A"
R"A(v_cvt_pk_bf16_f32 v232,v232,v233
v_cvt_pk_bf16_f32 v233,v234,v235
v_mfma_f32_16x16x32_bf16 a[72:75],a[212:215],v[204:207],a[72:75]
v_exp_f32 v236,v236
v_mfma_f32_16x16x32_bf16 a[76:79],a[220:223],v[204:207],0
v_exp_f32 v237,v237
v_mfma_f32_16x16x32_bf16 a[76:79],a[216:219],v[200:203],a[76:79]
v_exp_f32 v238,v238
v_mfma_f32_16x16x32_bf16 a[80:83],a[224:227],v[200:203],0
s_add_u32 m0,65536,s52
v_exp_f32 v239,v239
v_mfma_f32_16x16x32_bf16 a[80:83],a[228:231],v[204:207],a[80:83]
v_cvt_pk_bf16_f32 v234,v236,v237
v_cvt_pk_bf16_f32 v235,v238,v239
v_mfma_f32_16x16x32_bf16 a[84:87],a[236:239],v[204:207],0
v_exp_f32 v248,v248
v_mfma_f32_16x16x32_bf16 a[84:87],a[232:235],v[200:203],a[84:87]
buffer_load_dwordx4 v4,s[24:27],s47 offen nt lds
v_mfma_f32_16x16x32_bf16 a[88:91],a[240:243],v[200:203],0
v_exp_f32 v249,v249
v_mfma_f32_16x16x32_bf16 a[88:91],a[244:247],v[204:207],a[88:91]
v_exp_f32 v250,v250
v_mfma_f32_16x16x32_bf16 a[92:95],a[252:255],v[204:207],0
v_exp_f32 v251,v251
v_mfma_f32_16x16x32_bf16 a[92:95],a[248:251],v[200:203],a[92:95]
v_exp_f32 v252,v252
v_smfmac_f32_16x16x64_bf16 v[56:59],v[28:31],v[200:207],v27
v_cvt_pk_bf16_f32 v236,v248,v249
v_mfma_f32_16x16x32_bf16 v[168:171],a[128:131],v[80:83],v[36:39]
v_exp_f32 v253,v253
v_mfma_f32_16x16x32_bf16 v[168:171],a[132:135],v[84:87],v[168:171]
v_exp_f32 v254,v254
v_mfma_f32_16x16x32_bf16 v[168:171],a[136:139],v[88:91],v[168:171]
v_exp_f32 v255,v255
v_mfma_f32_16x16x32_bf16 v[168:171],a[140:143],v[92:95],v[168:171]
v_cvt_pk_bf16_f32 v237,v250,v251
v_cvt_pk_bf16_f32 v238,v252,v253
v_mfma_f32_16x16x32_bf16 v[172:175],a[156:159],v[92:95],v[36:39]
v_cvt_pk_bf16_f32 v239,v254,v255
v_mov_b32 v63,0
v_mfma_f32_16x16x32_bf16 v[172:175],a[152:155],v[88:91],v[172:175]
v_add_u32 v2,s48,v2
v_add_u32 v3,s48,v3
v_mfma_f32_16x16x32_bf16 v[172:175],a[148:151],v[84:87],v[172:175]
s_mov_b32 s48,32768
v_exp_f32 v136,v136
v_mfma_f32_16x16x32_bf16 v[172:175],a[144:147],v[80:83],v[172:175]
v_exp_f32 v137,v137
v_mfma_f32_16x16x32_bf16 v[184:187],a[160:163],v[80:83],v[36:39]
buffer_load_dwordx4 v5,s[24:27],s47 offen offset:1024 nt lds
v_mfma_f32_16x16x32_bf16 v[184:187],a[164:167],v[84:87],v[184:187]
v_exp_f32 v138,v138
v_mfma_f32_16x16x32_bf16 v[184:187],a[168:171],v[88:91],v[184:187]
v_exp_f32 v139,v139
v_mfma_f32_16x16x32_bf16 v[184:187],a[172:175],v[92:95],v[184:187]
v_cvt_pk_bf16_f32 v136,v136,v137
v_cvt_pk_bf16_f32 v137,v138,v139
v_mfma_f32_16x16x32_bf16 v[188:191],a[188:191],v[92:95],v[36:39]
v_exp_f32 v140,v140
v_mfma_f32_16x16x32_bf16 v[188:191],a[184:187],v[88:91],v[188:191]
v_exp_f32 v141,v141
v_mfma_f32_16x16x32_bf16 v[188:191],a[180:183],v[84:87],v[188:191]
v_exp_f32 v142,v142
v_mfma_f32_16x16x32_bf16 v[188:191],a[176:179],v[80:83],v[188:191]
v_exp_f32 v143,v143
v_mfma_f32_16x16x32_bf16 a[96:99],a[192:195],v[232:235],0
ds_read_b64_tr_b16 a[192:193],v2
ds_read_b64_tr_b16 a[194:195],v2 offset:4096
v_mfma_f32_16x16x32_bf16 a[96:99],a[196:199],v[236:239],a[96:99]
ds_read_b64_tr_b16 a[196:197],v2 offset:8192
v_cvt_pk_bf16_f32 v138,v140,v141
v_mfma_f32_16x16x32_bf16 a[100:103],a[204:207],v[236:239],0
ds_read_b64_tr_b16 a[198:199],v2 offset:12288
v_cvt_pk_bf16_f32 v139,v142,v143
v_mfma_f32_16x16x32_bf16 a[100:103],a[200:203],v[232:235],a[100:103]
ds_read_b64_tr_b16 a[204:205],v3 offset:8192
v_add_u32 v1,s48,v1
v_mfma_f32_16x16x32_bf16 a[104:107],a[208:211],v[232:235],0
ds_read_b64_tr_b16 a[206:207],v3 offset:12288
ds_read_b64_tr_b16 a[200:201],v3
v_mfma_f32_16x16x32_bf16 a[104:107],a[212:215],v[236:239],a[104:107]
v_exp_f32 v152,v152
v_mfma_f32_16x16x32_bf16 a[108:111],a[220:223],v[236:239],0
buffer_load_dwordx4 v4,s[24:27],s47 offen offset:2048 nt lds
v_mfma_f32_16x16x32_bf16 a[108:111],a[216:219],v[232:235],a[108:111]
ds_read_b64_tr_b16 a[202:203],v3 offset:4096
ds_read_b64_tr_b16 a[208:209],v2 offset:256
v_mfma_f32_16x16x32_bf16 a[112:115],a[224:227],v[232:235],0
v_exp_f32 v153,v153
v_mfma_f32_16x16x32_bf16 a[112:115],a[228:231],v[236:239],a[112:115]
ds_read_b64_tr_b16 a[210:211],v2 offset:4352
ds_read_b64_tr_b16 a[212:213],v2 offset:8448
v_mfma_f32_16x16x32_bf16 a[116:119],a[236:239],v[236:239],0
ds_read_b64_tr_b16 a[214:215],v2 offset:12544
v_cvt_pk_bf16_f32 v140,v152,v153
v_mfma_f32_16x16x32_bf16 a[116:119],a[232:235],v[232:235],a[116:119]
v_exp_f32 v154,v154
v_mfma_f32_16x16x32_bf16 a[120:123],a[240:243],v[232:235],0
v_exp_f32 v155,v155
v_mfma_f32_16x16x32_bf16 a[120:123],a[244:247],v[236:239],a[120:123]
v_exp_f32 v156,v156
v_mfma_f32_16x16x32_bf16 a[124:127],a[252:255],v[236:239],0
v_exp_f32 v157,v157
v_mfma_f32_16x16x32_bf16 a[124:127],a[248:251],v[232:235],a[124:127]
v_exp_f32 v158,v158
v_smfmac_f32_16x16x64_bf16 v[60:63],v[28:31],v[232:239],v27
ds_read_b64_tr_b16 a[220:221],v3 offset:8448
v_mfma_f32_16x16x32_bf16 v[200:203],a[128:131],v[96:99],v[40:43]
v_exp_f32 v159,v159
v_mfma_f32_16x16x32_bf16 v[200:203],a[132:135],v[100:103],v[200:203]
ds_read_b64_tr_b16 a[222:223],v3 offset:12544
v_cvt_pk_bf16_f32 v141,v154,v155
v_mfma_f32_16x16x32_bf16 v[200:203],a[136:139],v[104:107],v[200:203]
v_cvt_pk_bf16_f32 v142,v156,v157
v_cvt_pk_bf16_f32 v143,v158,v159
v_mfma_f32_16x16x32_bf16 v[200:203],a[140:143],v[108:111],v[200:203]
buffer_load_dwordx4 v5,s[24:27],s47 offen offset:3072 nt lds
v_mfma_f32_16x16x32_bf16 v[204:207],a[156:159],v[108:111],v[40:43]
s_add_u32 s47,s47,16384
ds_read_b64_tr_b16 a[216:217],v3 offset:256
ds_read_b64_tr_b16 a[218:219],v3 offset:4352
v_mfma_f32_16x16x32_bf16 v[204:207],a[152:155],v[104:107],v[204:207]
s_and_b32 s47,s47,0x3ffff
s_or_b32 s47,s47,s46
ds_read_b64_tr_b16 a[224:225],v2 offset:512
v_mfma_f32_16x16x32_bf16 v[204:207],a[148:151],v[100:103],v[204:207]
ds_read_b64_tr_b16 a[226:227],v2 offset:4608
v_mfma_f32_16x16x32_bf16 v[204:207],a[144:147],v[96:99],v[204:207]
ds_read_b64_tr_b16 a[228:229],v2 offset:8704
v_mfma_f32_16x16x32_bf16 v[216:219],a[160:163],v[96:99],v[40:43]
ds_read_b64_tr_b16 a[230:231],v2 offset:12800
ds_read_b64_tr_b16 a[236:237],v3 offset:8704
v_mfma_f32_16x16x32_bf16 v[216:219],a[164:167],v[100:103],v[216:219]
ds_read_b64_tr_b16 a[238:239],v3 offset:12800
v_mfma_f32_16x16x32_bf16 v[216:219],a[168:171],v[104:107],v[216:219]
ds_read_b64_tr_b16 a[232:233],v3 offset:512
v_mfma_f32_16x16x32_bf16 v[216:219],a[172:175],v[108:111],v[216:219]
ds_read_b64_tr_b16 a[234:235],v3 offset:4608
v_mfma_f32_16x16x32_bf16 v[220:223],a[188:191],v[108:111],v[40:43]
ds_read_b64_tr_b16 a[240:241],v2 offset:768
ds_read_b64_tr_b16 a[242:243],v2 offset:4864
v_mfma_f32_16x16x32_bf16 v[220:223],a[184:187],v[104:107],v[220:223]
ds_read_b64_tr_b16 a[244:245],v2 offset:8960
v_mfma_f32_16x16x32_bf16 v[220:223],a[180:183],v[100:103],v[220:223]
ds_read_b64_tr_b16 a[246:247],v2 offset:13056
v_mfma_f32_16x16x32_bf16 v[220:223],a[176:179],v[96:99],v[220:223]
s_waitcnt lgkmcnt(9)
ds_read_b64_tr_b16 a[252:253],v3 offset:8960
v_mfma_f32_16x16x32_bf16 a[0:3],a[192:195],v[136:139],a[0:3]
ds_read_b64_tr_b16 a[254:255],v3 offset:13056
ds_read_b64_tr_b16 a[248:249],v3 offset:768
v_mfma_f32_16x16x32_bf16 a[0:3],a[196:199],v[140:143],a[0:3]
ds_read_b64_tr_b16 a[250:251],v3 offset:4864
v_mfma_f32_16x16x32_bf16 a[4:7],a[204:207],v[140:143],a[4:7]
v_exp_f32 v168,v168
v_mfma_f32_16x16x32_bf16 a[4:7],a[200:203],v[136:139],a[4:7]
s_memrealtime s[76:77]
v_exp_f32 v169,v169
v_mfma_f32_16x16x32_bf16 a[8:11],a[208:211],v[136:139],a[8:11]
v_exp_f32 v170,v170
v_mfma_f32_16x16x32_bf16 a[8:11],a[212:215],v[140:143],a[8:11]
v_exp_f32 v171,v171
v_mfma_f32_16x16x32_bf16 a[12:15],a[220:223],v[140:143],a[12:15]
v_cvt_pk_bf16_f32 v168,v168,v169
v_cvt_pk_bf16_f32 v169,v170,v171
v_mfma_f32_16x16x32_bf16 a[12:15],a[216:219],v[136:139],a[12:15]
v_exp_f32 v172,v172
v_mfma_f32_16x16x32_bf16 a[16:19],a[224:227],v[136:139],a[16:19]
s_waitcnt lgkmcnt(1)
v_exp_f32 v173,v173
v_mfma_f32_16x16x32_bf16 a[16:19],a[228:231],v[140:143],a[16:19]
v_exp_f32 v174,v174
v_mfma_f32_16x16x32_bf16 a[20:23],a[236:239],v[140:143],a[20:23]
s_waitcnt lgkmcnt(0)
v_exp_f32 v175,v175
v_mfma_f32_16x16x32_bf16 a[20:23],a[232:235],v[136:139],a[20:23]
s_sub_u32 s79,s78,s76
v_exp_f32 v184,v184
v_mfma_f32_16x16x32_bf16 a[24:27],a[240:243],v[136:139],a[24:27]
s_add_u32 s78,s76,s60
v_exp_f32 v185,v185
v_mfma_f32_16x16x32_bf16 a[24:27],a[244:247],v[140:143],a[24:27]
s_cmp_gt_i32 s79,0
s_cbranch_scc1 L_ps2_%=
L_pr2_%=:
v_cvt_pk_bf16_f32 v170,v172,v173
v_mfma_f32_16x16x32_bf16 a[28:31],a[252:255],v[140:143],a[28:31]
v_exp_f32 v186,v186
v_exp_f32 v187,v187
v_exp_f32 v188,v188
v_exp_f32 v189,v189
v_exp_f32 v190,v190
v_exp_f32 v191,v191
v_mfma_f32_16x16x32_bf16 a[28:31],a[248:251],v[136:139],a[28:31]
v_cvt_pk_bf16_f32 v171,v174,v175
v_cvt_pk_bf16_f32 v172,v184,v185
v_cvt_pk_bf16_f32 v173,v186,v187
v_cvt_pk_bf16_f32 v174,v188,v189
v_cvt_pk_bf16_f32 v175,v190,v191
v_smfmac_f32_16x16x64_bf16 v[48:51],v[28:31],v[136:143],v27
s_mov_b32 s53,13
s_cmp_eq_u32 s40,3
s_cbranch_scc0 L_att0_%=
s_cmp_gt_i32 s18,0
s_cbranch_scc1 L_att0v0_%=
s_cmp_gt_i32 s19,0
s_cbranch_scc1 L_att0v1_%=
s_cmp_gt_i32 s100,0
s_cbranch_scc1 L_att0v2_%=
.p2align 5
L_att0v3_%=:
s_waitcnt vmcnt(8) lgkmcnt(0)
v_exp_f32 v200,v200
s_barrier
s_add_u32 m0,s49,s51
ds_read_b128 a[128:131],v1
s_mov_b32 s90,L_endseg_%=-L_pc_%=+128
v_exp_f32 v201,v201
s_lshr_b32 s45,s45,1
ds_read_b128 a[132:135],v1 offset:256
v_cvt_pk_bf16_f32 v200,v200,v201
s_add_u32 s62,s50,32768
buffer_load_dwordx4 v4,s[20:23],s47 offen nt lds
s_and_b32 s62,s62,131071
ds_read_b128 a[136:139],v1 offset:512
v_add_u32 v2,s48,v2
s_add_u32 s54,s54,16384
v_exp_f32 v202,v202
s_sub_u32 s53,s53,1
ds_read_b128 a[140:143],v1 offset:768
v_add_u32 v3,s48,v3
s_sub_u32 s48,s62,s50
v_exp_f32 v203,v203
s_and_b32 s46,s54,0xfffc0000
ds_read_b128 a[156:159],v1 offset:4864
v_cvt_pk_bf16_f32 v201,v202,v203
s_mov_b32 s50,s62
v_exp_f32 v204,v204
v_exp_f32 v205,v205
ds_read_b128 a[152:155],v1 offset:4608
v_cvt_pk_bf16_f32 v202,v204,v205
v_exp_f32 v206,v206
ds_read_b128 a[148:151],v1 offset:4352
v_exp_f32 v207,v207
v_mfma_f32_16x16x32_bf16 a[32:35],a[192:195],v[168:171],a[32:35]
ds_read_b128 a[144:147],v1 offset:4096
v_cvt_pk_bf16_f32 v203,v206,v207
v_mfma_f32_16x16x32_bf16 a[32:35],a[196:199],v[172:175],a[32:35]
v_exp_f32 v216,v216
v_mfma_f32_16x16x32_bf16 a[36:39],a[204:207],v[172:175],a[36:39]
buffer_load_dwordx4 v4,s[20:23],s47 offen offset:1024 nt lds
v_mfma_f32_16x16x32_bf16 a[36:39],a[200:203],v[168:171],a[36:39]
ds_read_b128 a[160:163],v1 offset:8192
v_mfma_f32_16x16x32_bf16 a[40:43],a[208:211],v[168:171],a[40:43]
v_exp_f32 v217,v217
v_mfma_f32_16x16x32_bf16 a[40:43],a[212:215],v[172:175],a[40:43]
ds_read_b128 a[164:167],v1 offset:8448
v_cvt_pk_bf16_f32 v204,v216,v217
v_mfma_f32_16x16x32_bf16 a[44:47],a[220:223],v[172:175],a[44:47]
v_exp_f32 v218,v218
v_mfma_f32_16x16x32_bf16 a[44:47],a[216:219],v[168:171],a[44:47]
ds_read_b128 a[168:171],v1 offset:8704
v_mfma_f32_16x16x32_bf16 a[48:51],a[224:227],v[168:171],a[48:51]
v_exp_f32 v219,v219
v_mfma_f32_16x16x32_bf16 a[48:51],a[228:231],v[172:175],a[48:51]
ds_read_b128 a[172:175],v1 offset:8960
v_cvt_pk_bf16_f32 v205,v218,v219
v_mfma_f32_16x16x32_bf16 a[52:55],a[236:239],v[172:175],a[52:55]
v_exp_f32 v220,v220
v_mfma_f32_16x16x32_bf16 a[52:55],a[232:235],v[168:171],a[52:55]
ds_read_b128 a[188:191],v1 offset:13056
v_mfma_f32_16x16x32_bf16 a[56:59],a[240:243],v[168:171],a[56:59]
v_exp_f32 v221,v221
v_mfma_f32_16x16x32_bf16 a[56:59],a[244:247],v[172:175],a[56:59]
ds_read_b128 a[184:187],v1 offset:12800
v_cvt_pk_bf16_f32 v206,v220,v221
v_mfma_f32_16x16x32_bf16 a[60:63],a[252:255],v[172:175],a[60:63]
v_exp_f32 v222,v222
v_mfma_f32_16x16x32_bf16 a[60:63],a[248:251],v[168:171],a[60:63]
ds_read_b128 a[180:183],v1 offset:12544
v_smfmac_f32_16x16x64_bf16 v[52:55],v[28:31],v[168:175],v27
s_waitcnt lgkmcnt(4)
v_mfma_f32_16x16x32_bf16 v[136:139],a[128:131],v[64:67],v[32:35]
buffer_load_dwordx4 v5,s[20:23],s47 offen offset:2048 nt lds
v_mfma_f32_16x16x32_bf16 v[136:139],a[132:135],v[68:71],v[136:139]
ds_read_b128 a[176:179],v1 offset:12288
v_add_u32 v1,s48,v1
v_mfma_f32_16x16x32_bf16 v[136:139],a[136:139],v[72:75],v[136:139]
v_exp_f32 v223,v223
v_mfma_f32_16x16x32_bf16 v[136:139],a[140:143],v[76:79],v[136:139]
)A"
R"A(v_cvt_pk_bf16_f32 v207,v222,v223
v_mfma_f32_16x16x32_bf16 v[140:143],a[156:159],v[76:79],v[32:35]
v_mfma_f32_16x16x32_bf16 v[140:143],a[152:155],v[72:75],v[140:143]
v_mfma_f32_16x16x32_bf16 v[140:143],a[148:151],v[68:71],v[140:143]
v_mfma_f32_16x16x32_bf16 v[140:143],a[144:147],v[64:67],v[140:143]
v_mfma_f32_16x16x32_bf16 v[152:155],a[160:163],v[64:67],v[32:35]
v_mfma_f32_16x16x32_bf16 v[152:155],a[164:167],v[68:71],v[152:155]
v_mfma_f32_16x16x32_bf16 v[152:155],a[168:171],v[72:75],v[152:155]
s_waitcnt lgkmcnt(0)
v_mfma_f32_16x16x32_bf16 v[152:155],a[172:175],v[76:79],v[152:155]
v_mfma_f32_16x16x32_bf16 v[156:159],a[188:191],v[76:79],v[32:35]
v_mfma_f32_16x16x32_bf16 v[156:159],a[184:187],v[72:75],v[156:159]
buffer_load_dwordx4 v5,s[20:23],s47 offen offset:3072 nt lds
v_mfma_f32_16x16x32_bf16 v[156:159],a[180:183],v[68:71],v[156:159]
v_mfma_f32_16x16x32_bf16 v[156:159],a[176:179],v[64:67],v[156:159]
v_mfma_f32_16x16x32_bf16 a[64:67],a[192:195],v[200:203],a[64:67]
v_mfma_f32_16x16x32_bf16 a[64:67],a[196:199],v[204:207],a[64:67]
v_mfma_f32_16x16x32_bf16 a[68:71],a[204:207],v[204:207],a[68:71]
v_mfma_f32_16x16x32_bf16 a[68:71],a[200:203],v[200:203],a[68:71]
v_mfma_f32_16x16x32_bf16 a[72:75],a[208:211],v[200:203],a[72:75]
v_mfma_f32_16x16x32_bf16 a[72:75],a[212:215],v[204:207],a[72:75]
v_exp_f32 v136,v136
v_mfma_f32_16x16x32_bf16 a[76:79],a[220:223],v[204:207],a[76:79]
v_exp_f32 v137,v137
v_mfma_f32_16x16x32_bf16 a[76:79],a[216:219],v[200:203],a[76:79]
v_exp_f32 v138,v138
v_mfma_f32_16x16x32_bf16 a[80:83],a[224:227],v[200:203],a[80:83]
s_add_u32 m0,s49,s52
v_mfma_f32_16x16x32_bf16 a[80:83],a[228:231],v[204:207],a[80:83]
s_xor_b32 s49,s50,65536
v_mfma_f32_16x16x32_bf16 a[84:87],a[236:239],v[204:207],a[84:87]
v_mfma_f32_16x16x32_bf16 a[84:87],a[232:235],v[200:203],a[84:87]
buffer_load_dwordx4 v4,s[24:27],s47 offen nt lds
v_mfma_f32_16x16x32_bf16 a[88:91],a[240:243],v[200:203],a[88:91]
v_mfma_f32_16x16x32_bf16 a[88:91],a[244:247],v[204:207],a[88:91]
v_exp_f32 v139,v139
v_mfma_f32_16x16x32_bf16 a[92:95],a[252:255],v[204:207],a[92:95]
v_exp_f32 v140,v140
v_mfma_f32_16x16x32_bf16 a[92:95],a[248:251],v[200:203],a[92:95]
v_exp_f32 v141,v141
v_smfmac_f32_16x16x64_bf16 v[56:59],v[28:31],v[200:207],v27
v_mfma_f32_16x16x32_bf16 v[168:171],a[128:131],v[80:83],v[36:39]
v_exp_f32 v142,v142
v_mfma_f32_16x16x32_bf16 v[168:171],a[132:135],v[84:87],v[168:171]
v_exp_f32 v143,v143
v_mfma_f32_16x16x32_bf16 v[168:171],a[136:139],v[88:91],v[168:171]
v_exp_f32 v152,v152
v_mfma_f32_16x16x32_bf16 v[168:171],a[140:143],v[92:95],v[168:171]
v_exp_f32 v153,v153
v_mfma_f32_16x16x32_bf16 v[172:175],a[156:159],v[92:95],v[36:39]
v_exp_f32 v154,v154
v_mfma_f32_16x16x32_bf16 v[172:175],a[152:155],v[88:91],v[172:175]
v_exp_f32 v155,v155
v_mfma_f32_16x16x32_bf16 v[172:175],a[148:151],v[84:87],v[172:175]
v_exp_f32 v156,v156
v_mfma_f32_16x16x32_bf16 v[172:175],a[144:147],v[80:83],v[172:175]
v_exp_f32 v157,v157
v_mfma_f32_16x16x32_bf16 v[184:187],a[160:163],v[80:83],v[36:39]
buffer_load_dwordx4 v5,s[24:27],s47 offen offset:1024 nt lds
v_mfma_f32_16x16x32_bf16 v[184:187],a[164:167],v[84:87],v[184:187]
v_exp_f32 v158,v158
v_mfma_f32_16x16x32_bf16 v[184:187],a[168:171],v[88:91],v[184:187]
v_exp_f32 v159,v159
v_mfma_f32_16x16x32_bf16 v[184:187],a[172:175],v[92:95],v[184:187]
v_exp_f32 v168,v168
v_mfma_f32_16x16x32_bf16 v[188:191],a[188:191],v[92:95],v[36:39]
v_exp_f32 v169,v169
v_mfma_f32_16x16x32_bf16 v[188:191],a[184:187],v[88:91],v[188:191]
v_exp_f32 v170,v170
v_mfma_f32_16x16x32_bf16 v[188:191],a[180:183],v[84:87],v[188:191]
v_exp_f32 v171,v171
v_mfma_f32_16x16x32_bf16 v[188:191],a[176:179],v[80:83],v[188:191]
v_exp_f32 v172,v172
ds_read_b64_tr_b16 a[192:193],v2
v_cvt_pk_bf16_f32 v168,v168,v169
ds_read_b64_tr_b16 a[194:195],v2 offset:4096
v_cvt_pk_bf16_f32 v169,v170,v171
v_exp_f32 v173,v173
ds_read_b64_tr_b16 a[196:197],v2 offset:8192
v_cvt_pk_bf16_f32 v170,v172,v173
ds_read_b64_tr_b16 a[198:199],v2 offset:12288
v_cvt_pk_bf16_f32 v136,v136,v137
v_exp_f32 v174,v174
buffer_load_dwordx4 v4,s[24:27],s47 offen offset:2048 nt lds
ds_read_b64_tr_b16 a[204:205],v3 offset:8192
v_cvt_pk_bf16_f32 v137,v138,v139
v_exp_f32 v175,v175
ds_read_b64_tr_b16 a[206:207],v3 offset:12288
v_cvt_pk_bf16_f32 v171,v174,v175
ds_read_b64_tr_b16 a[200:201],v3
v_cvt_pk_bf16_f32 v138,v140,v141
ds_read_b64_tr_b16 a[202:203],v3 offset:4096
ds_read_b64_tr_b16 a[208:209],v2 offset:256
ds_read_b64_tr_b16 a[210:211],v2 offset:4352
v_cvt_pk_bf16_f32 v139,v142,v143
ds_read_b64_tr_b16 a[212:213],v2 offset:8448
ds_read_b64_tr_b16 a[214:215],v2 offset:12544
ds_read_b64_tr_b16 a[220:221],v3 offset:8448
v_mfma_f32_16x16x32_bf16 v[200:203],a[128:131],v[96:99],v[40:43]
ds_read_b64_tr_b16 a[222:223],v3 offset:12544
v_cvt_pk_bf16_f32 v140,v152,v153
v_mfma_f32_16x16x32_bf16 v[200:203],a[132:135],v[100:103],v[200:203]
ds_read_b64_tr_b16 a[216:217],v3 offset:256
v_mfma_f32_16x16x32_bf16 v[200:203],a[136:139],v[104:107],v[200:203]
ds_read_b64_tr_b16 a[218:219],v3 offset:4352
v_cvt_pk_bf16_f32 v141,v154,v155
v_mfma_f32_16x16x32_bf16 v[200:203],a[140:143],v[108:111],v[200:203]
buffer_load_dwordx4 v5,s[24:27],s47 offen offset:3072 nt lds
v_mfma_f32_16x16x32_bf16 v[204:207],a[156:159],v[108:111],v[40:43]
s_add_u32 s47,s47,16384
s_and_b32 s47,s47,0x3ffff
ds_read_b64_tr_b16 a[224:225],v2 offset:512
v_mfma_f32_16x16x32_bf16 v[204:207],a[152:155],v[104:107],v[204:207]
s_or_b32 s47,s47,s46
ds_read_b64_tr_b16 a[226:227],v2 offset:4608
v_cvt_pk_bf16_f32 v142,v156,v157
v_mfma_f32_16x16x32_bf16 v[204:207],a[148:151],v[100:103],v[204:207]
ds_read_b64_tr_b16 a[228:229],v2 offset:8704
v_mfma_f32_16x16x32_bf16 v[204:207],a[144:147],v[96:99],v[204:207]
ds_read_b64_tr_b16 a[230:231],v2 offset:12800
v_mfma_f32_16x16x32_bf16 v[216:219],a[160:163],v[96:99],v[40:43]
ds_read_b64_tr_b16 a[236:237],v3 offset:8704
v_cvt_pk_bf16_f32 v143,v158,v159
v_mfma_f32_16x16x32_bf16 v[216:219],a[164:167],v[100:103],v[216:219]
ds_read_b64_tr_b16 a[238:239],v3 offset:12800
v_mfma_f32_16x16x32_bf16 v[216:219],a[168:171],v[104:107],v[216:219]
ds_read_b64_tr_b16 a[232:233],v3 offset:512
v_mfma_f32_16x16x32_bf16 v[216:219],a[172:175],v[108:111],v[216:219]
ds_read_b64_tr_b16 a[234:235],v3 offset:4608
v_mfma_f32_16x16x32_bf16 v[220:223],a[188:191],v[108:111],v[40:43]
ds_read_b64_tr_b16 a[240:241],v2 offset:768
v_mfma_f32_16x16x32_bf16 v[220:223],a[184:187],v[104:107],v[220:223]
ds_read_b64_tr_b16 a[242:243],v2 offset:4864
v_mfma_f32_16x16x32_bf16 v[220:223],a[180:183],v[100:103],v[220:223]
ds_read_b64_tr_b16 a[244:245],v2 offset:8960
v_mfma_f32_16x16x32_bf16 v[220:223],a[176:179],v[96:99],v[220:223]
s_waitcnt lgkmcnt(7)
buffer_load_dword v26,v24,s[88:91],s93 offen
v_mfma_f32_16x16x32_bf16 a[0:3],a[192:195],v[136:139],a[0:3]
ds_read_b64_tr_b16 a[246:247],v2 offset:13056
v_mfma_f32_16x16x32_bf16 a[0:3],a[196:199],v[140:143],a[0:3]
ds_read_b64_tr_b16 a[252:253],v3 offset:8960
v_mfma_f32_16x16x32_bf16 a[4:7],a[204:207],v[140:143],a[4:7]
ds_read_b64_tr_b16 a[254:255],v3 offset:13056
v_mfma_f32_16x16x32_bf16 a[4:7],a[200:203],v[136:139],a[4:7]
ds_read_b64_tr_b16 a[248:249],v3 offset:768
v_mfma_f32_16x16x32_bf16 a[8:11],a[208:211],v[136:139],a[8:11]
ds_read_b64_tr_b16 a[250:251],v3 offset:4864
v_mfma_f32_16x16x32_bf16 a[8:11],a[212:215],v[140:143],a[8:11]
v_exp_f32 v184,v184
v_mfma_f32_16x16x32_bf16 a[12:15],a[220:223],v[140:143],a[12:15]
v_exp_f32 v185,v185
v_mfma_f32_16x16x32_bf16 a[12:15],a[216:219],v[136:139],a[12:15]
s_bitcmp1_b32 s45,0
s_cbranch_scc0 L_t31v30_%=
s_mov_b32 m0,s80
s_add_u32 s80,s80,4096
buffer_load_dwordx4 v25,s[20:23],s83 offen nt lds
s_add_u32 s83,s83,4096
L_t31v30_%=:
v_mfma_f32_16x16x32_bf16 a[16:19],a[224:227],v[136:139],a[16:19]
v_exp_f32 v186,v186
v_mfma_f32_16x16x32_bf16 a[16:19],a[228:231],v[140:143],a[16:19]
s_waitcnt lgkmcnt(3)
v_exp_f32 v187,v187
v_mfma_f32_16x16x32_bf16 a[20:23],a[236:239],v[140:143],a[20:23]
v_exp_f32 v188,v188
v_mfma_f32_16x16x32_bf16 a[20:23],a[232:235],v[136:139],a[20:23]
v_exp_f32 v189,v189
v_mfma_f32_16x16x32_bf16 a[24:27],a[240:243],v[136:139],a[24:27]
v_exp_f32 v190,v190
v_mfma_f32_16x16x32_bf16 a[24:27],a[244:247],v[140:143],a[24:27]
s_waitcnt lgkmcnt(0)
v_cvt_pk_bf16_f32 v172,v184,v185
v_mfma_f32_16x16x32_bf16 a[28:31],a[252:255],v[140:143],a[28:31]
v_exp_f32 v191,v191
v_mfma_f32_16x16x32_bf16 a[28:31],a[248:251],v[136:139],a[28:31]
v_cvt_pk_bf16_f32 v173,v186,v187
v_cvt_pk_bf16_f32 v174,v188,v189
v_cvt_pk_bf16_f32 v175,v190,v191
s_cmp_lg_u32 s53,0
v_smfmac_f32_16x16x64_bf16 v[48:51],v[28:31],v[136:143],v27
s_cbranch_scc1 L_att0v3_%=
s_waitcnt lgkmcnt(0)
s_nop 7
s_branch L_att0x_%=
.p2align 5
L_att0v0_%=:
v_mfma_f32_16x16x32_bf16 v[232:235],a[128:131],v[112:115],v[44:47]
s_waitcnt vmcnt(8) lgkmcnt(0)
v_exp_f32 v200,v200
v_mfma_f32_16x16x32_bf16 v[232:235],a[132:135],v[116:119],v[232:235]
s_barrier
s_add_u32 m0,s49,s51
ds_read_b128 a[128:131],v1
v_mfma_f32_16x16x32_bf16 v[232:235],a[136:139],v[120:123],v[232:235]
s_mov_b32 s90,L_endseg_%=-L_pc_%=+128
v_exp_f32 v201,v201
v_mfma_f32_16x16x32_bf16 v[232:235],a[140:143],v[124:127],v[232:235]
s_lshr_b32 s45,s45,1
ds_read_b128 a[132:135],v1 offset:256
v_cvt_pk_bf16_f32 v200,v200,v201
v_mfma_f32_16x16x32_bf16 v[236:239],a[156:159],v[124:127],v[44:47]
s_add_u32 s62,s50,32768
buffer_load_dwordx4 v4,s[20:23],s47 offen nt lds
v_mfma_f32_16x16x32_bf16 v[236:239],a[152:155],v[120:123],v[236:239]
s_and_b32 s62,s62,131071
ds_read_b128 a[136:139],v1 offset:512
v_add_u32 v2,s48,v2
v_mfma_f32_16x16x32_bf16 v[236:239],a[148:151],v[116:119],v[236:239]
s_add_u32 s54,s54,16384
v_exp_f32 v202,v202
v_mfma_f32_16x16x32_bf16 v[236:239],a[144:147],v[112:115],v[236:239]
s_sub_u32 s53,s53,1
ds_read_b128 a[140:143],v1 offset:768
v_add_u32 v3,s48,v3
v_mfma_f32_16x16x32_bf16 v[248:251],a[160:163],v[112:115],v[44:47]
s_sub_u32 s48,s62,s50
v_exp_f32 v203,v203
v_mfma_f32_16x16x32_bf16 v[248:251],a[164:167],v[116:119],v[248:251]
s_and_b32 s46,s54,0xfffc0000
ds_read_b128 a[156:159],v1 offset:4864
v_cvt_pk_bf16_f32 v201,v202,v203
v_mfma_f32_16x16x32_bf16 v[248:251],a[168:171],v[120:123],v[248:251]
s_mov_b32 s50,s62
v_exp_f32 v204,v204
v_mfma_f32_16x16x32_bf16 v[248:251],a[172:175],v[124:127],v[248:251]
v_exp_f32 v205,v205
v_mfma_f32_16x16x32_bf16 v[252:255],a[188:191],v[124:127],v[44:47]
ds_read_b128 a[152:155],v1 offset:4608
v_cvt_pk_bf16_f32 v202,v204,v205
v_mfma_f32_16x16x32_bf16 v[252:255],a[184:187],v[120:123],v[252:255]
v_exp_f32 v206,v206
v_mfma_f32_16x16x32_bf16 v[252:255],a[180:183],v[116:119],v[252:255]
ds_read_b128 a[148:151],v1 offset:4352
v_mfma_f32_16x16x32_bf16 v[252:255],a[176:179],v[112:115],v[252:255]
v_exp_f32 v207,v207
v_mfma_f32_16x16x32_bf16 a[32:35],a[192:195],v[168:171],a[32:35]
ds_read_b128 a[144:147],v1 offset:4096
v_cvt_pk_bf16_f32 v203,v206,v207
v_mfma_f32_16x16x32_bf16 a[32:35],a[196:199],v[172:175],a[32:35]
v_exp_f32 v216,v216
v_mfma_f32_16x16x32_bf16 a[36:39],a[204:207],v[172:175],a[36:39]
buffer_load_dwordx4 v4,s[20:23],s47 offen offset:1024 nt lds
v_mfma_f32_16x16x32_bf16 a[36:39],a[200:203],v[168:171],a[36:39]
ds_read_b128 a[160:163],v1 offset:8192
v_mfma_f32_16x16x32_bf16 a[40:43],a[208:211],v[168:171],a[40:43]
v_exp_f32 v217,v217
v_mfma_f32_16x16x32_bf16 a[40:43],a[212:215],v[172:175],a[40:43]
ds_read_b128 a[164:167],v1 offset:8448
v_cvt_pk_bf16_f32 v204,v216,v217
v_mfma_f32_16x16x32_bf16 a[44:47],a[220:223],v[172:175],a[44:47]
v_exp_f32 v218,v218
v_mfma_f32_16x16x32_bf16 a[44:47],a[216:219],v[168:171],a[44:47]
ds_read_b128 a[168:171],v1 offset:8704
v_mfma_f32_16x16x32_bf16 a[48:51],a[224:227],v[168:171],a[48:51]
v_exp_f32 v219,v219
v_mfma_f32_16x16x32_bf16 a[48:51],a[228:231],v[172:175],a[48:51]
ds_read_b128 a[172:175],v1 offset:8960
v_cvt_pk_bf16_f32 v205,v218,v219
v_mfma_f32_16x16x32_bf16 a[52:55],a[236:239],v[172:175],a[52:55]
v_exp_f32 v220,v220
v_mfma_f32_16x16x32_bf16 a[52:55],a[232:235],v[168:171],a[52:55]
ds_read_b128 a[188:191],v1 offset:13056
v_mfma_f32_16x16x32_bf16 a[56:59],a[240:243],v[168:171],a[56:59]
v_exp_f32 v221,v221
v_mfma_f32_16x16x32_bf16 a[56:59],a[244:247],v[172:175],a[56:59]
ds_read_b128 a[184:187],v1 offset:12800
)A"
R"A(v_cvt_pk_bf16_f32 v206,v220,v221
v_mfma_f32_16x16x32_bf16 a[60:63],a[252:255],v[172:175],a[60:63]
v_exp_f32 v222,v222
v_mfma_f32_16x16x32_bf16 a[60:63],a[248:251],v[168:171],a[60:63]
ds_read_b128 a[180:183],v1 offset:12544
v_smfmac_f32_16x16x64_bf16 v[52:55],v[28:31],v[168:175],v27
buffer_load_dwordx4 v5,s[20:23],s47 offen offset:2048 nt lds
ds_read_b128 a[176:179],v1 offset:12288
v_add_u32 v1,s48,v1
v_exp_f32 v223,v223
s_nop 0
v_cvt_pk_bf16_f32 v207,v222,v223
v_exp_f32 v232,v232
v_exp_f32 v233,v233
v_exp_f32 v234,v234
v_exp_f32 v235,v235
v_exp_f32 v236,v236
v_exp_f32 v237,v237
v_exp_f32 v238,v238
v_exp_f32 v239,v239
v_exp_f32 v248,v248
buffer_load_dwordx4 v5,s[20:23],s47 offen offset:3072 nt lds
v_exp_f32 v249,v249
v_exp_f32 v250,v250
v_mfma_f32_16x16x32_bf16 a[64:67],a[192:195],v[200:203],a[64:67]
v_exp_f32 v251,v251
v_mfma_f32_16x16x32_bf16 a[64:67],a[196:199],v[204:207],a[64:67]
v_exp_f32 v252,v252
v_mfma_f32_16x16x32_bf16 a[68:71],a[204:207],v[204:207],a[68:71]
v_exp_f32 v253,v253
v_mfma_f32_16x16x32_bf16 a[68:71],a[200:203],v[200:203],a[68:71]
v_exp_f32 v254,v254
v_mfma_f32_16x16x32_bf16 a[72:75],a[208:211],v[200:203],a[72:75]
v_exp_f32 v255,v255
v_mfma_f32_16x16x32_bf16 a[72:75],a[212:215],v[204:207],a[72:75]
v_mfma_f32_16x16x32_bf16 a[76:79],a[220:223],v[204:207],a[76:79]
v_mfma_f32_16x16x32_bf16 a[76:79],a[216:219],v[200:203],a[76:79]
v_mfma_f32_16x16x32_bf16 a[80:83],a[224:227],v[200:203],a[80:83]
s_add_u32 m0,s49,s52
v_cvt_pk_bf16_f32 v232,v232,v233
v_cvt_pk_bf16_f32 v233,v234,v235
v_mfma_f32_16x16x32_bf16 a[80:83],a[228:231],v[204:207],a[80:83]
s_xor_b32 s49,s50,65536
v_cvt_pk_bf16_f32 v234,v236,v237
v_cvt_pk_bf16_f32 v235,v238,v239
v_mfma_f32_16x16x32_bf16 a[84:87],a[236:239],v[204:207],a[84:87]
v_cvt_pk_bf16_f32 v236,v248,v249
v_cvt_pk_bf16_f32 v237,v250,v251
v_mfma_f32_16x16x32_bf16 a[84:87],a[232:235],v[200:203],a[84:87]
buffer_load_dwordx4 v4,s[24:27],s47 offen nt lds
v_mfma_f32_16x16x32_bf16 a[88:91],a[240:243],v[200:203],a[88:91]
v_cvt_pk_bf16_f32 v238,v252,v253
v_cvt_pk_bf16_f32 v239,v254,v255
v_mfma_f32_16x16x32_bf16 a[88:91],a[244:247],v[204:207],a[88:91]
v_mfma_f32_16x16x32_bf16 a[92:95],a[252:255],v[204:207],a[92:95]
v_mfma_f32_16x16x32_bf16 a[92:95],a[248:251],v[200:203],a[92:95]
v_smfmac_f32_16x16x64_bf16 v[56:59],v[28:31],v[200:207],v27
s_waitcnt lgkmcnt(0)
v_mfma_f32_16x16x32_bf16 v[168:171],a[128:131],v[80:83],v[36:39]
v_mfma_f32_16x16x32_bf16 v[168:171],a[132:135],v[84:87],v[168:171]
v_mfma_f32_16x16x32_bf16 v[168:171],a[136:139],v[88:91],v[168:171]
v_mfma_f32_16x16x32_bf16 v[168:171],a[140:143],v[92:95],v[168:171]
v_mfma_f32_16x16x32_bf16 v[172:175],a[156:159],v[92:95],v[36:39]
v_mfma_f32_16x16x32_bf16 v[172:175],a[152:155],v[88:91],v[172:175]
v_mfma_f32_16x16x32_bf16 v[172:175],a[148:151],v[84:87],v[172:175]
v_mfma_f32_16x16x32_bf16 v[172:175],a[144:147],v[80:83],v[172:175]
v_mfma_f32_16x16x32_bf16 v[184:187],a[160:163],v[80:83],v[36:39]
buffer_load_dwordx4 v5,s[24:27],s47 offen offset:1024 nt lds
v_mfma_f32_16x16x32_bf16 v[184:187],a[164:167],v[84:87],v[184:187]
v_mfma_f32_16x16x32_bf16 v[184:187],a[168:171],v[88:91],v[184:187]
v_mfma_f32_16x16x32_bf16 v[184:187],a[172:175],v[92:95],v[184:187]
v_exp_f32 v168,v168
v_mfma_f32_16x16x32_bf16 v[188:191],a[188:191],v[92:95],v[36:39]
v_exp_f32 v169,v169
v_mfma_f32_16x16x32_bf16 v[188:191],a[184:187],v[88:91],v[188:191]
v_exp_f32 v170,v170
v_mfma_f32_16x16x32_bf16 v[188:191],a[180:183],v[84:87],v[188:191]
v_exp_f32 v171,v171
v_mfma_f32_16x16x32_bf16 v[188:191],a[176:179],v[80:83],v[188:191]
v_exp_f32 v172,v172
v_mfma_f32_16x16x32_bf16 a[96:99],a[192:195],v[232:235],a[96:99]
ds_read_b64_tr_b16 a[192:193],v2
v_cvt_pk_bf16_f32 v168,v168,v169
v_mfma_f32_16x16x32_bf16 a[96:99],a[196:199],v[236:239],a[96:99]
ds_read_b64_tr_b16 a[194:195],v2 offset:4096
v_cvt_pk_bf16_f32 v169,v170,v171
v_mfma_f32_16x16x32_bf16 a[100:103],a[204:207],v[236:239],a[100:103]
v_exp_f32 v173,v173
v_mfma_f32_16x16x32_bf16 a[100:103],a[200:203],v[232:235],a[100:103]
ds_read_b64_tr_b16 a[196:197],v2 offset:8192
v_cvt_pk_bf16_f32 v170,v172,v173
v_mfma_f32_16x16x32_bf16 a[104:107],a[208:211],v[232:235],a[104:107]
ds_read_b64_tr_b16 a[198:199],v2 offset:12288
v_mfma_f32_16x16x32_bf16 a[104:107],a[212:215],v[236:239],a[104:107]
v_exp_f32 v174,v174
v_mfma_f32_16x16x32_bf16 a[108:111],a[220:223],v[236:239],a[108:111]
buffer_load_dwordx4 v4,s[24:27],s47 offen offset:2048 nt lds
v_mfma_f32_16x16x32_bf16 a[108:111],a[216:219],v[232:235],a[108:111]
ds_read_b64_tr_b16 a[204:205],v3 offset:8192
v_mfma_f32_16x16x32_bf16 a[112:115],a[224:227],v[232:235],a[112:115]
v_exp_f32 v175,v175
v_mfma_f32_16x16x32_bf16 a[112:115],a[228:231],v[236:239],a[112:115]
ds_read_b64_tr_b16 a[206:207],v3 offset:12288
v_cvt_pk_bf16_f32 v171,v174,v175
v_mfma_f32_16x16x32_bf16 a[116:119],a[236:239],v[236:239],a[116:119]
ds_read_b64_tr_b16 a[200:201],v3
v_mfma_f32_16x16x32_bf16 a[116:119],a[232:235],v[232:235],a[116:119]
ds_read_b64_tr_b16 a[202:203],v3 offset:4096
v_mfma_f32_16x16x32_bf16 a[120:123],a[240:243],v[232:235],a[120:123]
ds_read_b64_tr_b16 a[208:209],v2 offset:256
v_mfma_f32_16x16x32_bf16 a[120:123],a[244:247],v[236:239],a[120:123]
ds_read_b64_tr_b16 a[210:211],v2 offset:4352
v_mfma_f32_16x16x32_bf16 a[124:127],a[252:255],v[236:239],a[124:127]
ds_read_b64_tr_b16 a[212:213],v2 offset:8448
v_mfma_f32_16x16x32_bf16 a[124:127],a[248:251],v[232:235],a[124:127]
ds_read_b64_tr_b16 a[214:215],v2 offset:12544
v_smfmac_f32_16x16x64_bf16 v[60:63],v[28:31],v[232:239],v27
ds_read_b64_tr_b16 a[220:221],v3 offset:8448
v_mfma_f32_16x16x32_bf16 v[200:203],a[128:131],v[96:99],v[40:43]
ds_read_b64_tr_b16 a[222:223],v3 offset:12544
v_mfma_f32_16x16x32_bf16 v[200:203],a[132:135],v[100:103],v[200:203]
ds_read_b64_tr_b16 a[216:217],v3 offset:256
v_mfma_f32_16x16x32_bf16 v[200:203],a[136:139],v[104:107],v[200:203]
ds_read_b64_tr_b16 a[218:219],v3 offset:4352
v_mfma_f32_16x16x32_bf16 v[200:203],a[140:143],v[108:111],v[200:203]
buffer_load_dwordx4 v5,s[24:27],s47 offen offset:3072 nt lds
v_mfma_f32_16x16x32_bf16 v[204:207],a[156:159],v[108:111],v[40:43]
s_add_u32 s47,s47,16384
s_and_b32 s47,s47,0x3ffff
ds_read_b64_tr_b16 a[224:225],v2 offset:512
v_mfma_f32_16x16x32_bf16 v[204:207],a[152:155],v[104:107],v[204:207]
s_or_b32 s47,s47,s46
ds_read_b64_tr_b16 a[226:227],v2 offset:4608
v_mfma_f32_16x16x32_bf16 v[204:207],a[148:151],v[100:103],v[204:207]
ds_read_b64_tr_b16 a[228:229],v2 offset:8704
v_mfma_f32_16x16x32_bf16 v[204:207],a[144:147],v[96:99],v[204:207]
ds_read_b64_tr_b16 a[230:231],v2 offset:12800
v_mfma_f32_16x16x32_bf16 v[216:219],a[160:163],v[96:99],v[40:43]
ds_read_b64_tr_b16 a[236:237],v3 offset:8704
v_mfma_f32_16x16x32_bf16 v[216:219],a[164:167],v[100:103],v[216:219]
ds_read_b64_tr_b16 a[238:239],v3 offset:12800
v_mfma_f32_16x16x32_bf16 v[216:219],a[168:171],v[104:107],v[216:219]
ds_read_b64_tr_b16 a[232:233],v3 offset:512
v_mfma_f32_16x16x32_bf16 v[216:219],a[172:175],v[108:111],v[216:219]
ds_read_b64_tr_b16 a[234:235],v3 offset:4608
v_mfma_f32_16x16x32_bf16 v[220:223],a[188:191],v[108:111],v[40:43]
ds_read_b64_tr_b16 a[240:241],v2 offset:768
v_mfma_f32_16x16x32_bf16 v[220:223],a[184:187],v[104:107],v[220:223]
ds_read_b64_tr_b16 a[242:243],v2 offset:4864
v_mfma_f32_16x16x32_bf16 v[220:223],a[180:183],v[100:103],v[220:223]
ds_read_b64_tr_b16 a[244:245],v2 offset:8960
v_mfma_f32_16x16x32_bf16 v[220:223],a[176:179],v[96:99],v[220:223]
buffer_load_dword v26,v24,s[88:91],s93 offen
ds_read_b64_tr_b16 a[246:247],v2 offset:13056
ds_read_b64_tr_b16 a[252:253],v3 offset:8960
ds_read_b64_tr_b16 a[254:255],v3 offset:13056
ds_read_b64_tr_b16 a[248:249],v3 offset:768
ds_read_b64_tr_b16 a[250:251],v3 offset:4864
v_exp_f32 v184,v184
v_exp_f32 v185,v185
s_bitcmp1_b32 s45,0
s_cbranch_scc0 L_t31v00_%=
s_mov_b32 m0,s80
s_add_u32 s80,s80,4096
buffer_load_dwordx4 v25,s[20:23],s83 offen nt lds
s_add_u32 s83,s83,4096
L_t31v00_%=:
v_exp_f32 v186,v186
v_exp_f32 v187,v187
v_exp_f32 v188,v188
v_exp_f32 v189,v189
v_exp_f32 v190,v190
v_cvt_pk_bf16_f32 v172,v184,v185
v_exp_f32 v191,v191
v_cvt_pk_bf16_f32 v173,v186,v187
v_cvt_pk_bf16_f32 v174,v188,v189
v_cvt_pk_bf16_f32 v175,v190,v191
s_cmp_lg_u32 s53,0
s_cbranch_scc1 L_att0v0_%=
s_waitcnt lgkmcnt(0)
s_nop 7
s_branch L_att0x_%=
.p2align 5
L_att0v1_%=:
v_mfma_f32_16x16x32_bf16 v[232:235],a[128:131],v[112:115],v[44:47]
s_waitcnt vmcnt(8) lgkmcnt(0)
v_exp_f32 v200,v200
v_mfma_f32_16x16x32_bf16 v[232:235],a[132:135],v[116:119],v[232:235]
s_barrier
s_add_u32 m0,s49,s51
ds_read_b128 a[128:131],v1
v_mfma_f32_16x16x32_bf16 v[232:235],a[136:139],v[120:123],v[232:235]
s_mov_b32 s90,L_endseg_%=-L_pc_%=+128
v_exp_f32 v201,v201
v_mfma_f32_16x16x32_bf16 v[232:235],a[140:143],v[124:127],v[232:235]
s_lshr_b32 s45,s45,1
ds_read_b128 a[132:135],v1 offset:256
v_cvt_pk_bf16_f32 v200,v200,v201
v_mfma_f32_16x16x32_bf16 v[236:239],a[156:159],v[124:127],v[44:47]
s_add_u32 s62,s50,32768
buffer_load_dwordx4 v4,s[20:23],s47 offen nt lds
v_mfma_f32_16x16x32_bf16 v[236:239],a[152:155],v[120:123],v[236:239]
s_and_b32 s62,s62,131071
ds_read_b128 a[136:139],v1 offset:512
v_add_u32 v2,s48,v2
v_mfma_f32_16x16x32_bf16 v[236:239],a[148:151],v[116:119],v[236:239]
s_add_u32 s54,s54,16384
v_exp_f32 v202,v202
v_mfma_f32_16x16x32_bf16 v[236:239],a[144:147],v[112:115],v[236:239]
s_sub_u32 s53,s53,1
ds_read_b128 a[140:143],v1 offset:768
v_add_u32 v3,s48,v3
v_mfma_f32_16x16x32_bf16 v[248:251],a[160:163],v[112:115],v[44:47]
s_sub_u32 s48,s62,s50
v_exp_f32 v203,v203
v_mfma_f32_16x16x32_bf16 v[248:251],a[164:167],v[116:119],v[248:251]
s_and_b32 s46,s54,0xfffc0000
ds_read_b128 a[156:159],v1 offset:4864
v_cvt_pk_bf16_f32 v201,v202,v203
v_mfma_f32_16x16x32_bf16 v[248:251],a[168:171],v[120:123],v[248:251]
s_mov_b32 s50,s62
v_exp_f32 v204,v204
v_mfma_f32_16x16x32_bf16 v[248:251],a[172:175],v[124:127],v[248:251]
v_exp_f32 v205,v205
v_mfma_f32_16x16x32_bf16 v[252:255],a[188:191],v[124:127],v[44:47]
ds_read_b128 a[152:155],v1 offset:4608
v_cvt_pk_bf16_f32 v202,v204,v205
v_mfma_f32_16x16x32_bf16 v[252:255],a[184:187],v[120:123],v[252:255]
v_exp_f32 v206,v206
v_mfma_f32_16x16x32_bf16 v[252:255],a[180:183],v[116:119],v[252:255]
ds_read_b128 a[148:151],v1 offset:4352
v_mfma_f32_16x16x32_bf16 v[252:255],a[176:179],v[112:115],v[252:255]
s_waitcnt lgkmcnt(3)
v_exp_f32 v207,v207
ds_read_b128 a[144:147],v1 offset:4096
v_cvt_pk_bf16_f32 v203,v206,v207
v_exp_f32 v216,v216
buffer_load_dwordx4 v4,s[20:23],s47 offen offset:1024 nt lds
ds_read_b128 a[160:163],v1 offset:8192
v_exp_f32 v217,v217
ds_read_b128 a[164:167],v1 offset:8448
v_cvt_pk_bf16_f32 v204,v216,v217
v_exp_f32 v218,v218
ds_read_b128 a[168:171],v1 offset:8704
v_exp_f32 v219,v219
ds_read_b128 a[172:175],v1 offset:8960
v_cvt_pk_bf16_f32 v205,v218,v219
v_exp_f32 v220,v220
ds_read_b128 a[188:191],v1 offset:13056
v_exp_f32 v221,v221
ds_read_b128 a[184:187],v1 offset:12800
v_cvt_pk_bf16_f32 v206,v220,v221
v_exp_f32 v222,v222
ds_read_b128 a[180:183],v1 offset:12544
v_mfma_f32_16x16x32_bf16 v[136:139],a[128:131],v[64:67],v[32:35]
buffer_load_dwordx4 v5,s[20:23],s47 offen offset:2048 nt lds
v_mfma_f32_16x16x32_bf16 v[136:139],a[132:135],v[68:71],v[136:139]
ds_read_b128 a[176:179],v1 offset:12288
v_add_u32 v1,s48,v1
v_mfma_f32_16x16x32_bf16 v[136:139],a[136:139],v[72:75],v[136:139]
v_exp_f32 v223,v223
v_mfma_f32_16x16x32_bf16 v[136:139],a[140:143],v[76:79],v[136:139]
s_waitcnt lgkmcnt(11)
v_cvt_pk_bf16_f32 v207,v222,v223
v_mfma_f32_16x16x32_bf16 v[140:143],a[156:159],v[76:79],v[32:35]
s_waitcnt lgkmcnt(10)
v_exp_f32 v232,v232
v_mfma_f32_16x16x32_bf16 v[140:143],a[152:155],v[72:75],v[140:143]
s_waitcnt lgkmcnt(9)
v_exp_f32 v233,v233
v_mfma_f32_16x16x32_bf16 v[140:143],a[148:151],v[68:71],v[140:143]
s_waitcnt lgkmcnt(8)
v_exp_f32 v234,v234
v_mfma_f32_16x16x32_bf16 v[140:143],a[144:147],v[64:67],v[140:143]
s_waitcnt lgkmcnt(1)
v_exp_f32 v235,v235
v_mfma_f32_16x16x32_bf16 v[152:155],a[160:163],v[64:67],v[32:35]
v_exp_f32 v236,v236
v_mfma_f32_16x16x32_bf16 v[152:155],a[164:167],v[68:71],v[152:155]
v_exp_f32 v237,v237
v_mfma_f32_16x16x32_bf16 v[152:155],a[168:171],v[72:75],v[152:155]
)A"
R"A(v_exp_f32 v238,v238
v_mfma_f32_16x16x32_bf16 v[152:155],a[172:175],v[76:79],v[152:155]
v_exp_f32 v239,v239
v_mfma_f32_16x16x32_bf16 v[156:159],a[188:191],v[76:79],v[32:35]
v_exp_f32 v248,v248
v_mfma_f32_16x16x32_bf16 v[156:159],a[184:187],v[72:75],v[156:159]
buffer_load_dwordx4 v5,s[20:23],s47 offen offset:3072 nt lds
v_mfma_f32_16x16x32_bf16 v[156:159],a[180:183],v[68:71],v[156:159]
s_waitcnt lgkmcnt(0)
v_exp_f32 v249,v249
v_mfma_f32_16x16x32_bf16 v[156:159],a[176:179],v[64:67],v[156:159]
v_exp_f32 v250,v250
v_mfma_f32_16x16x32_bf16 a[64:67],a[192:195],v[200:203],a[64:67]
v_exp_f32 v251,v251
v_mfma_f32_16x16x32_bf16 a[64:67],a[196:199],v[204:207],a[64:67]
v_exp_f32 v252,v252
v_mfma_f32_16x16x32_bf16 a[68:71],a[204:207],v[204:207],a[68:71]
v_exp_f32 v253,v253
v_mfma_f32_16x16x32_bf16 a[68:71],a[200:203],v[200:203],a[68:71]
v_exp_f32 v254,v254
v_mfma_f32_16x16x32_bf16 a[72:75],a[208:211],v[200:203],a[72:75]
v_exp_f32 v255,v255
v_mfma_f32_16x16x32_bf16 a[72:75],a[212:215],v[204:207],a[72:75]
v_exp_f32 v136,v136
v_mfma_f32_16x16x32_bf16 a[76:79],a[220:223],v[204:207],a[76:79]
v_exp_f32 v137,v137
v_mfma_f32_16x16x32_bf16 a[76:79],a[216:219],v[200:203],a[76:79]
v_exp_f32 v138,v138
v_mfma_f32_16x16x32_bf16 a[80:83],a[224:227],v[200:203],a[80:83]
s_add_u32 m0,s49,s52
v_cvt_pk_bf16_f32 v232,v232,v233
v_cvt_pk_bf16_f32 v233,v234,v235
v_mfma_f32_16x16x32_bf16 a[80:83],a[228:231],v[204:207],a[80:83]
s_xor_b32 s49,s50,65536
v_cvt_pk_bf16_f32 v234,v236,v237
v_cvt_pk_bf16_f32 v235,v238,v239
v_mfma_f32_16x16x32_bf16 a[84:87],a[236:239],v[204:207],a[84:87]
v_cvt_pk_bf16_f32 v236,v248,v249
v_cvt_pk_bf16_f32 v237,v250,v251
v_mfma_f32_16x16x32_bf16 a[84:87],a[232:235],v[200:203],a[84:87]
buffer_load_dwordx4 v4,s[24:27],s47 offen nt lds
v_mfma_f32_16x16x32_bf16 a[88:91],a[240:243],v[200:203],a[88:91]
v_cvt_pk_bf16_f32 v238,v252,v253
v_cvt_pk_bf16_f32 v239,v254,v255
v_mfma_f32_16x16x32_bf16 a[88:91],a[244:247],v[204:207],a[88:91]
v_exp_f32 v139,v139
v_mfma_f32_16x16x32_bf16 a[92:95],a[252:255],v[204:207],a[92:95]
v_exp_f32 v140,v140
v_mfma_f32_16x16x32_bf16 a[92:95],a[248:251],v[200:203],a[92:95]
v_exp_f32 v141,v141
v_smfmac_f32_16x16x64_bf16 v[56:59],v[28:31],v[200:207],v27
v_exp_f32 v142,v142
v_exp_f32 v143,v143
v_exp_f32 v152,v152
v_exp_f32 v153,v153
v_exp_f32 v154,v154
v_exp_f32 v155,v155
v_exp_f32 v156,v156
v_exp_f32 v157,v157
buffer_load_dwordx4 v5,s[24:27],s47 offen offset:1024 nt lds
v_exp_f32 v158,v158
v_exp_f32 v159,v159
v_mfma_f32_16x16x32_bf16 a[96:99],a[192:195],v[232:235],a[96:99]
ds_read_b64_tr_b16 a[192:193],v2
v_mfma_f32_16x16x32_bf16 a[96:99],a[196:199],v[236:239],a[96:99]
ds_read_b64_tr_b16 a[194:195],v2 offset:4096
v_mfma_f32_16x16x32_bf16 a[100:103],a[204:207],v[236:239],a[100:103]
v_mfma_f32_16x16x32_bf16 a[100:103],a[200:203],v[232:235],a[100:103]
ds_read_b64_tr_b16 a[196:197],v2 offset:8192
v_mfma_f32_16x16x32_bf16 a[104:107],a[208:211],v[232:235],a[104:107]
ds_read_b64_tr_b16 a[198:199],v2 offset:12288
v_cvt_pk_bf16_f32 v136,v136,v137
v_mfma_f32_16x16x32_bf16 a[104:107],a[212:215],v[236:239],a[104:107]
v_mfma_f32_16x16x32_bf16 a[108:111],a[220:223],v[236:239],a[108:111]
buffer_load_dwordx4 v4,s[24:27],s47 offen offset:2048 nt lds
v_mfma_f32_16x16x32_bf16 a[108:111],a[216:219],v[232:235],a[108:111]
ds_read_b64_tr_b16 a[204:205],v3 offset:8192
v_cvt_pk_bf16_f32 v137,v138,v139
v_mfma_f32_16x16x32_bf16 a[112:115],a[224:227],v[232:235],a[112:115]
v_mfma_f32_16x16x32_bf16 a[112:115],a[228:231],v[236:239],a[112:115]
ds_read_b64_tr_b16 a[206:207],v3 offset:12288
v_mfma_f32_16x16x32_bf16 a[116:119],a[236:239],v[236:239],a[116:119]
ds_read_b64_tr_b16 a[200:201],v3
v_cvt_pk_bf16_f32 v138,v140,v141
v_mfma_f32_16x16x32_bf16 a[116:119],a[232:235],v[232:235],a[116:119]
ds_read_b64_tr_b16 a[202:203],v3 offset:4096
v_mfma_f32_16x16x32_bf16 a[120:123],a[240:243],v[232:235],a[120:123]
ds_read_b64_tr_b16 a[208:209],v2 offset:256
v_mfma_f32_16x16x32_bf16 a[120:123],a[244:247],v[236:239],a[120:123]
ds_read_b64_tr_b16 a[210:211],v2 offset:4352
v_cvt_pk_bf16_f32 v139,v142,v143
v_mfma_f32_16x16x32_bf16 a[124:127],a[252:255],v[236:239],a[124:127]
ds_read_b64_tr_b16 a[212:213],v2 offset:8448
v_mfma_f32_16x16x32_bf16 a[124:127],a[248:251],v[232:235],a[124:127]
ds_read_b64_tr_b16 a[214:215],v2 offset:12544
v_smfmac_f32_16x16x64_bf16 v[60:63],v[28:31],v[232:239],v27
ds_read_b64_tr_b16 a[220:221],v3 offset:8448
v_mfma_f32_16x16x32_bf16 v[200:203],a[128:131],v[96:99],v[40:43]
ds_read_b64_tr_b16 a[222:223],v3 offset:12544
v_cvt_pk_bf16_f32 v140,v152,v153
v_mfma_f32_16x16x32_bf16 v[200:203],a[132:135],v[100:103],v[200:203]
ds_read_b64_tr_b16 a[216:217],v3 offset:256
v_mfma_f32_16x16x32_bf16 v[200:203],a[136:139],v[104:107],v[200:203]
ds_read_b64_tr_b16 a[218:219],v3 offset:4352
v_cvt_pk_bf16_f32 v141,v154,v155
v_mfma_f32_16x16x32_bf16 v[200:203],a[140:143],v[108:111],v[200:203]
buffer_load_dwordx4 v5,s[24:27],s47 offen offset:3072 nt lds
v_mfma_f32_16x16x32_bf16 v[204:207],a[156:159],v[108:111],v[40:43]
s_add_u32 s47,s47,16384
s_and_b32 s47,s47,0x3ffff
ds_read_b64_tr_b16 a[224:225],v2 offset:512
v_mfma_f32_16x16x32_bf16 v[204:207],a[152:155],v[104:107],v[204:207]
s_or_b32 s47,s47,s46
ds_read_b64_tr_b16 a[226:227],v2 offset:4608
v_cvt_pk_bf16_f32 v142,v156,v157
v_mfma_f32_16x16x32_bf16 v[204:207],a[148:151],v[100:103],v[204:207]
ds_read_b64_tr_b16 a[228:229],v2 offset:8704
v_mfma_f32_16x16x32_bf16 v[204:207],a[144:147],v[96:99],v[204:207]
ds_read_b64_tr_b16 a[230:231],v2 offset:12800
v_mfma_f32_16x16x32_bf16 v[216:219],a[160:163],v[96:99],v[40:43]
ds_read_b64_tr_b16 a[236:237],v3 offset:8704
v_cvt_pk_bf16_f32 v143,v158,v159
v_mfma_f32_16x16x32_bf16 v[216:219],a[164:167],v[100:103],v[216:219]
ds_read_b64_tr_b16 a[238:239],v3 offset:12800
v_mfma_f32_16x16x32_bf16 v[216:219],a[168:171],v[104:107],v[216:219]
ds_read_b64_tr_b16 a[232:233],v3 offset:512
v_mfma_f32_16x16x32_bf16 v[216:219],a[172:175],v[108:111],v[216:219]
ds_read_b64_tr_b16 a[234:235],v3 offset:4608
v_mfma_f32_16x16x32_bf16 v[220:223],a[188:191],v[108:111],v[40:43]
ds_read_b64_tr_b16 a[240:241],v2 offset:768
v_mfma_f32_16x16x32_bf16 v[220:223],a[184:187],v[104:107],v[220:223]
ds_read_b64_tr_b16 a[242:243],v2 offset:4864
v_mfma_f32_16x16x32_bf16 v[220:223],a[180:183],v[100:103],v[220:223]
ds_read_b64_tr_b16 a[244:245],v2 offset:8960
v_mfma_f32_16x16x32_bf16 v[220:223],a[176:179],v[96:99],v[220:223]
s_waitcnt lgkmcnt(7)
buffer_load_dword v26,v24,s[88:91],s93 offen
v_mfma_f32_16x16x32_bf16 a[0:3],a[192:195],v[136:139],a[0:3]
ds_read_b64_tr_b16 a[246:247],v2 offset:13056
v_mfma_f32_16x16x32_bf16 a[0:3],a[196:199],v[140:143],a[0:3]
ds_read_b64_tr_b16 a[252:253],v3 offset:8960
v_mfma_f32_16x16x32_bf16 a[4:7],a[204:207],v[140:143],a[4:7]
ds_read_b64_tr_b16 a[254:255],v3 offset:13056
v_mfma_f32_16x16x32_bf16 a[4:7],a[200:203],v[136:139],a[4:7]
ds_read_b64_tr_b16 a[248:249],v3 offset:768
v_mfma_f32_16x16x32_bf16 a[8:11],a[208:211],v[136:139],a[8:11]
ds_read_b64_tr_b16 a[250:251],v3 offset:4864
v_mfma_f32_16x16x32_bf16 a[8:11],a[212:215],v[140:143],a[8:11]
v_mfma_f32_16x16x32_bf16 a[12:15],a[220:223],v[140:143],a[12:15]
v_mfma_f32_16x16x32_bf16 a[12:15],a[216:219],v[136:139],a[12:15]
s_bitcmp1_b32 s45,0
s_cbranch_scc0 L_t31v10_%=
s_mov_b32 m0,s80
s_add_u32 s80,s80,4096
buffer_load_dwordx4 v25,s[20:23],s83 offen nt lds
s_add_u32 s83,s83,4096
L_t31v10_%=:
v_mfma_f32_16x16x32_bf16 a[16:19],a[224:227],v[136:139],a[16:19]
v_mfma_f32_16x16x32_bf16 a[16:19],a[228:231],v[140:143],a[16:19]
s_waitcnt lgkmcnt(3)
v_mfma_f32_16x16x32_bf16 a[20:23],a[236:239],v[140:143],a[20:23]
v_mfma_f32_16x16x32_bf16 a[20:23],a[232:235],v[136:139],a[20:23]
v_mfma_f32_16x16x32_bf16 a[24:27],a[240:243],v[136:139],a[24:27]
v_mfma_f32_16x16x32_bf16 a[24:27],a[244:247],v[140:143],a[24:27]
s_waitcnt lgkmcnt(0)
v_mfma_f32_16x16x32_bf16 a[28:31],a[252:255],v[140:143],a[28:31]
v_mfma_f32_16x16x32_bf16 a[28:31],a[248:251],v[136:139],a[28:31]
s_cmp_lg_u32 s53,0
v_smfmac_f32_16x16x64_bf16 v[48:51],v[28:31],v[136:143],v27
s_cbranch_scc1 L_att0v1_%=
s_waitcnt lgkmcnt(0)
s_nop 7
s_branch L_att0x_%=
.p2align 5
L_att0v2_%=:
v_mfma_f32_16x16x32_bf16 v[232:235],a[128:131],v[112:115],v[44:47]
s_waitcnt vmcnt(8) lgkmcnt(0)
v_mfma_f32_16x16x32_bf16 v[232:235],a[132:135],v[116:119],v[232:235]
s_barrier
s_add_u32 m0,s49,s51
ds_read_b128 a[128:131],v1
v_mfma_f32_16x16x32_bf16 v[232:235],a[136:139],v[120:123],v[232:235]
s_mov_b32 s90,L_endseg_%=-L_pc_%=+128
v_mfma_f32_16x16x32_bf16 v[232:235],a[140:143],v[124:127],v[232:235]
s_lshr_b32 s45,s45,1
ds_read_b128 a[132:135],v1 offset:256
v_mfma_f32_16x16x32_bf16 v[236:239],a[156:159],v[124:127],v[44:47]
s_add_u32 s62,s50,32768
buffer_load_dwordx4 v4,s[20:23],s47 offen nt lds
v_mfma_f32_16x16x32_bf16 v[236:239],a[152:155],v[120:123],v[236:239]
s_and_b32 s62,s62,131071
ds_read_b128 a[136:139],v1 offset:512
v_add_u32 v2,s48,v2
v_mfma_f32_16x16x32_bf16 v[236:239],a[148:151],v[116:119],v[236:239]
s_add_u32 s54,s54,16384
v_mfma_f32_16x16x32_bf16 v[236:239],a[144:147],v[112:115],v[236:239]
s_sub_u32 s53,s53,1
ds_read_b128 a[140:143],v1 offset:768
v_add_u32 v3,s48,v3
v_mfma_f32_16x16x32_bf16 v[248:251],a[160:163],v[112:115],v[44:47]
s_sub_u32 s48,s62,s50
v_mfma_f32_16x16x32_bf16 v[248:251],a[164:167],v[116:119],v[248:251]
s_and_b32 s46,s54,0xfffc0000
ds_read_b128 a[156:159],v1 offset:4864
v_mfma_f32_16x16x32_bf16 v[248:251],a[168:171],v[120:123],v[248:251]
s_mov_b32 s50,s62
v_mfma_f32_16x16x32_bf16 v[248:251],a[172:175],v[124:127],v[248:251]
v_mfma_f32_16x16x32_bf16 v[252:255],a[188:191],v[124:127],v[44:47]
ds_read_b128 a[152:155],v1 offset:4608
v_mfma_f32_16x16x32_bf16 v[252:255],a[184:187],v[120:123],v[252:255]
v_mfma_f32_16x16x32_bf16 v[252:255],a[180:183],v[116:119],v[252:255]
ds_read_b128 a[148:151],v1 offset:4352
v_mfma_f32_16x16x32_bf16 v[252:255],a[176:179],v[112:115],v[252:255]
v_mfma_f32_16x16x32_bf16 a[32:35],a[192:195],v[168:171],a[32:35]
ds_read_b128 a[144:147],v1 offset:4096
v_mfma_f32_16x16x32_bf16 a[32:35],a[196:199],v[172:175],a[32:35]
v_mfma_f32_16x16x32_bf16 a[36:39],a[204:207],v[172:175],a[36:39]
buffer_load_dwordx4 v4,s[20:23],s47 offen offset:1024 nt lds
v_mfma_f32_16x16x32_bf16 a[36:39],a[200:203],v[168:171],a[36:39]
ds_read_b128 a[160:163],v1 offset:8192
v_mfma_f32_16x16x32_bf16 a[40:43],a[208:211],v[168:171],a[40:43]
v_mfma_f32_16x16x32_bf16 a[40:43],a[212:215],v[172:175],a[40:43]
ds_read_b128 a[164:167],v1 offset:8448
v_mfma_f32_16x16x32_bf16 a[44:47],a[220:223],v[172:175],a[44:47]
v_mfma_f32_16x16x32_bf16 a[44:47],a[216:219],v[168:171],a[44:47]
ds_read_b128 a[168:171],v1 offset:8704
v_mfma_f32_16x16x32_bf16 a[48:51],a[224:227],v[168:171],a[48:51]
v_mfma_f32_16x16x32_bf16 a[48:51],a[228:231],v[172:175],a[48:51]
ds_read_b128 a[172:175],v1 offset:8960
v_mfma_f32_16x16x32_bf16 a[52:55],a[236:239],v[172:175],a[52:55]
v_mfma_f32_16x16x32_bf16 a[52:55],a[232:235],v[168:171],a[52:55]
ds_read_b128 a[188:191],v1 offset:13056
v_mfma_f32_16x16x32_bf16 a[56:59],a[240:243],v[168:171],a[56:59]
v_mfma_f32_16x16x32_bf16 a[56:59],a[244:247],v[172:175],a[56:59]
ds_read_b128 a[184:187],v1 offset:12800
v_mfma_f32_16x16x32_bf16 a[60:63],a[252:255],v[172:175],a[60:63]
v_mfma_f32_16x16x32_bf16 a[60:63],a[248:251],v[168:171],a[60:63]
ds_read_b128 a[180:183],v1 offset:12544
v_smfmac_f32_16x16x64_bf16 v[52:55],v[28:31],v[168:175],v27
s_waitcnt lgkmcnt(4)
v_mfma_f32_16x16x32_bf16 v[136:139],a[128:131],v[64:67],v[32:35]
buffer_load_dwordx4 v5,s[20:23],s47 offen offset:2048 nt lds
v_mfma_f32_16x16x32_bf16 v[136:139],a[132:135],v[68:71],v[136:139]
ds_read_b128 a[176:179],v1 offset:12288
v_add_u32 v1,s48,v1
v_mfma_f32_16x16x32_bf16 v[136:139],a[136:139],v[72:75],v[136:139]
v_mfma_f32_16x16x32_bf16 v[136:139],a[140:143],v[76:79],v[136:139]
v_mfma_f32_16x16x32_bf16 v[140:143],a[156:159],v[76:79],v[32:35]
v_exp_f32 v232,v232
v_mfma_f32_16x16x32_bf16 v[140:143],a[152:155],v[72:75],v[140:143]
v_exp_f32 v233,v233
v_mfma_f32_16x16x32_bf16 v[140:143],a[148:151],v[68:71],v[140:143]
v_exp_f32 v234,v234
v_mfma_f32_16x16x32_bf16 v[140:143],a[144:147],v[64:67],v[140:143]
v_exp_f32 v235,v235
)A"
R"A(v_mfma_f32_16x16x32_bf16 v[152:155],a[160:163],v[64:67],v[32:35]
v_exp_f32 v236,v236
v_mfma_f32_16x16x32_bf16 v[152:155],a[164:167],v[68:71],v[152:155]
v_exp_f32 v237,v237
v_mfma_f32_16x16x32_bf16 v[152:155],a[168:171],v[72:75],v[152:155]
s_waitcnt lgkmcnt(0)
v_exp_f32 v238,v238
v_mfma_f32_16x16x32_bf16 v[152:155],a[172:175],v[76:79],v[152:155]
v_exp_f32 v239,v239
v_mfma_f32_16x16x32_bf16 v[156:159],a[188:191],v[76:79],v[32:35]
v_exp_f32 v248,v248
v_mfma_f32_16x16x32_bf16 v[156:159],a[184:187],v[72:75],v[156:159]
buffer_load_dwordx4 v5,s[20:23],s47 offen offset:3072 nt lds
v_mfma_f32_16x16x32_bf16 v[156:159],a[180:183],v[68:71],v[156:159]
v_exp_f32 v249,v249
v_mfma_f32_16x16x32_bf16 v[156:159],a[176:179],v[64:67],v[156:159]
v_exp_f32 v250,v250
v_exp_f32 v251,v251
v_exp_f32 v252,v252
v_exp_f32 v253,v253
v_exp_f32 v254,v254
v_exp_f32 v255,v255
v_exp_f32 v136,v136
v_exp_f32 v137,v137
v_exp_f32 v138,v138
s_add_u32 m0,s49,s52
v_cvt_pk_bf16_f32 v232,v232,v233
v_cvt_pk_bf16_f32 v233,v234,v235
s_xor_b32 s49,s50,65536
v_cvt_pk_bf16_f32 v234,v236,v237
v_cvt_pk_bf16_f32 v235,v238,v239
v_cvt_pk_bf16_f32 v236,v248,v249
v_cvt_pk_bf16_f32 v237,v250,v251
buffer_load_dwordx4 v4,s[24:27],s47 offen nt lds
v_cvt_pk_bf16_f32 v238,v252,v253
v_cvt_pk_bf16_f32 v239,v254,v255
v_exp_f32 v139,v139
v_exp_f32 v140,v140
v_exp_f32 v141,v141
v_mfma_f32_16x16x32_bf16 v[168:171],a[128:131],v[80:83],v[36:39]
v_exp_f32 v142,v142
v_mfma_f32_16x16x32_bf16 v[168:171],a[132:135],v[84:87],v[168:171]
v_exp_f32 v143,v143
v_mfma_f32_16x16x32_bf16 v[168:171],a[136:139],v[88:91],v[168:171]
v_exp_f32 v152,v152
v_mfma_f32_16x16x32_bf16 v[168:171],a[140:143],v[92:95],v[168:171]
v_exp_f32 v153,v153
v_mfma_f32_16x16x32_bf16 v[172:175],a[156:159],v[92:95],v[36:39]
v_exp_f32 v154,v154
v_mfma_f32_16x16x32_bf16 v[172:175],a[152:155],v[88:91],v[172:175]
v_exp_f32 v155,v155
v_mfma_f32_16x16x32_bf16 v[172:175],a[148:151],v[84:87],v[172:175]
v_exp_f32 v156,v156
v_mfma_f32_16x16x32_bf16 v[172:175],a[144:147],v[80:83],v[172:175]
v_exp_f32 v157,v157
v_mfma_f32_16x16x32_bf16 v[184:187],a[160:163],v[80:83],v[36:39]
buffer_load_dwordx4 v5,s[24:27],s47 offen offset:1024 nt lds
v_mfma_f32_16x16x32_bf16 v[184:187],a[164:167],v[84:87],v[184:187]
v_exp_f32 v158,v158
v_mfma_f32_16x16x32_bf16 v[184:187],a[168:171],v[88:91],v[184:187]
v_exp_f32 v159,v159
v_mfma_f32_16x16x32_bf16 v[184:187],a[172:175],v[92:95],v[184:187]
v_exp_f32 v168,v168
v_mfma_f32_16x16x32_bf16 v[188:191],a[188:191],v[92:95],v[36:39]
v_exp_f32 v169,v169
v_mfma_f32_16x16x32_bf16 v[188:191],a[184:187],v[88:91],v[188:191]
v_exp_f32 v170,v170
v_mfma_f32_16x16x32_bf16 v[188:191],a[180:183],v[84:87],v[188:191]
v_exp_f32 v171,v171
v_mfma_f32_16x16x32_bf16 v[188:191],a[176:179],v[80:83],v[188:191]
v_exp_f32 v172,v172
v_mfma_f32_16x16x32_bf16 a[96:99],a[192:195],v[232:235],a[96:99]
ds_read_b64_tr_b16 a[192:193],v2
v_cvt_pk_bf16_f32 v168,v168,v169
v_mfma_f32_16x16x32_bf16 a[96:99],a[196:199],v[236:239],a[96:99]
ds_read_b64_tr_b16 a[194:195],v2 offset:4096
v_cvt_pk_bf16_f32 v169,v170,v171
v_mfma_f32_16x16x32_bf16 a[100:103],a[204:207],v[236:239],a[100:103]
v_exp_f32 v173,v173
v_mfma_f32_16x16x32_bf16 a[100:103],a[200:203],v[232:235],a[100:103]
ds_read_b64_tr_b16 a[196:197],v2 offset:8192
v_cvt_pk_bf16_f32 v170,v172,v173
v_mfma_f32_16x16x32_bf16 a[104:107],a[208:211],v[232:235],a[104:107]
ds_read_b64_tr_b16 a[198:199],v2 offset:12288
v_cvt_pk_bf16_f32 v136,v136,v137
v_mfma_f32_16x16x32_bf16 a[104:107],a[212:215],v[236:239],a[104:107]
v_exp_f32 v174,v174
v_mfma_f32_16x16x32_bf16 a[108:111],a[220:223],v[236:239],a[108:111]
buffer_load_dwordx4 v4,s[24:27],s47 offen offset:2048 nt lds
v_mfma_f32_16x16x32_bf16 a[108:111],a[216:219],v[232:235],a[108:111]
ds_read_b64_tr_b16 a[204:205],v3 offset:8192
v_cvt_pk_bf16_f32 v137,v138,v139
v_mfma_f32_16x16x32_bf16 a[112:115],a[224:227],v[232:235],a[112:115]
v_exp_f32 v175,v175
v_mfma_f32_16x16x32_bf16 a[112:115],a[228:231],v[236:239],a[112:115]
ds_read_b64_tr_b16 a[206:207],v3 offset:12288
v_cvt_pk_bf16_f32 v171,v174,v175
v_mfma_f32_16x16x32_bf16 a[116:119],a[236:239],v[236:239],a[116:119]
ds_read_b64_tr_b16 a[200:201],v3
v_cvt_pk_bf16_f32 v138,v140,v141
v_mfma_f32_16x16x32_bf16 a[116:119],a[232:235],v[232:235],a[116:119]
ds_read_b64_tr_b16 a[202:203],v3 offset:4096
v_mfma_f32_16x16x32_bf16 a[120:123],a[240:243],v[232:235],a[120:123]
ds_read_b64_tr_b16 a[208:209],v2 offset:256
v_mfma_f32_16x16x32_bf16 a[120:123],a[244:247],v[236:239],a[120:123]
ds_read_b64_tr_b16 a[210:211],v2 offset:4352
v_cvt_pk_bf16_f32 v139,v142,v143
v_mfma_f32_16x16x32_bf16 a[124:127],a[252:255],v[236:239],a[124:127]
ds_read_b64_tr_b16 a[212:213],v2 offset:8448
v_mfma_f32_16x16x32_bf16 a[124:127],a[248:251],v[232:235],a[124:127]
ds_read_b64_tr_b16 a[214:215],v2 offset:12544
v_smfmac_f32_16x16x64_bf16 v[60:63],v[28:31],v[232:239],v27
s_waitcnt lgkmcnt(7)
ds_read_b64_tr_b16 a[220:221],v3 offset:8448
ds_read_b64_tr_b16 a[222:223],v3 offset:12544
v_cvt_pk_bf16_f32 v140,v152,v153
ds_read_b64_tr_b16 a[216:217],v3 offset:256
ds_read_b64_tr_b16 a[218:219],v3 offset:4352
v_cvt_pk_bf16_f32 v141,v154,v155
buffer_load_dwordx4 v5,s[24:27],s47 offen offset:3072 nt lds
s_add_u32 s47,s47,16384
s_and_b32 s47,s47,0x3ffff
ds_read_b64_tr_b16 a[224:225],v2 offset:512
s_or_b32 s47,s47,s46
ds_read_b64_tr_b16 a[226:227],v2 offset:4608
v_cvt_pk_bf16_f32 v142,v156,v157
ds_read_b64_tr_b16 a[228:229],v2 offset:8704
ds_read_b64_tr_b16 a[230:231],v2 offset:12800
ds_read_b64_tr_b16 a[236:237],v3 offset:8704
v_cvt_pk_bf16_f32 v143,v158,v159
ds_read_b64_tr_b16 a[238:239],v3 offset:12800
ds_read_b64_tr_b16 a[232:233],v3 offset:512
ds_read_b64_tr_b16 a[234:235],v3 offset:4608
ds_read_b64_tr_b16 a[240:241],v2 offset:768
ds_read_b64_tr_b16 a[242:243],v2 offset:4864
ds_read_b64_tr_b16 a[244:245],v2 offset:8960
buffer_load_dword v26,v24,s[88:91],s93 offen
v_mfma_f32_16x16x32_bf16 a[0:3],a[192:195],v[136:139],a[0:3]
ds_read_b64_tr_b16 a[246:247],v2 offset:13056
v_mfma_f32_16x16x32_bf16 a[0:3],a[196:199],v[140:143],a[0:3]
s_waitcnt lgkmcnt(15)
ds_read_b64_tr_b16 a[252:253],v3 offset:8960
v_mfma_f32_16x16x32_bf16 a[4:7],a[204:207],v[140:143],a[4:7]
ds_read_b64_tr_b16 a[254:255],v3 offset:13056
v_mfma_f32_16x16x32_bf16 a[4:7],a[200:203],v[136:139],a[4:7]
ds_read_b64_tr_b16 a[248:249],v3 offset:768
v_mfma_f32_16x16x32_bf16 a[8:11],a[208:211],v[136:139],a[8:11]
ds_read_b64_tr_b16 a[250:251],v3 offset:4864
v_mfma_f32_16x16x32_bf16 a[8:11],a[212:215],v[140:143],a[8:11]
s_waitcnt lgkmcnt(15)
v_exp_f32 v184,v184
v_mfma_f32_16x16x32_bf16 a[12:15],a[220:223],v[140:143],a[12:15]
v_exp_f32 v185,v185
v_mfma_f32_16x16x32_bf16 a[12:15],a[216:219],v[136:139],a[12:15]
s_bitcmp1_b32 s45,0
s_cbranch_scc0 L_t31v20_%=
s_mov_b32 m0,s80
s_add_u32 s80,s80,4096
buffer_load_dwordx4 v25,s[20:23],s83 offen nt lds
s_add_u32 s83,s83,4096
L_t31v20_%=:
s_waitcnt lgkmcnt(5)
v_mfma_f32_16x16x32_bf16 a[16:19],a[224:227],v[136:139],a[16:19]
v_exp_f32 v186,v186
v_mfma_f32_16x16x32_bf16 a[16:19],a[228:231],v[140:143],a[16:19]
v_exp_f32 v187,v187
v_mfma_f32_16x16x32_bf16 a[20:23],a[236:239],v[140:143],a[20:23]
v_exp_f32 v188,v188
v_mfma_f32_16x16x32_bf16 a[20:23],a[232:235],v[136:139],a[20:23]
v_exp_f32 v189,v189
v_mfma_f32_16x16x32_bf16 a[24:27],a[240:243],v[136:139],a[24:27]
s_waitcnt lgkmcnt(0)
v_exp_f32 v190,v190
v_mfma_f32_16x16x32_bf16 a[24:27],a[244:247],v[140:143],a[24:27]
v_cvt_pk_bf16_f32 v172,v184,v185
v_mfma_f32_16x16x32_bf16 a[28:31],a[252:255],v[140:143],a[28:31]
v_exp_f32 v191,v191
v_mfma_f32_16x16x32_bf16 a[28:31],a[248:251],v[136:139],a[28:31]
v_cvt_pk_bf16_f32 v173,v186,v187
v_cvt_pk_bf16_f32 v174,v188,v189
v_cvt_pk_bf16_f32 v175,v190,v191
s_cmp_lg_u32 s53,0
v_smfmac_f32_16x16x64_bf16 v[48:51],v[28:31],v[136:143],v27
s_cbranch_scc1 L_att0v2_%=
s_waitcnt lgkmcnt(0)
s_nop 7
s_branch L_att0x_%=
.p2align 5
L_att0_%=:
v_mfma_f32_16x16x32_bf16 v[232:235],a[128:131],v[112:115],v[44:47]
s_waitcnt vmcnt(8) lgkmcnt(0)
v_exp_f32 v200,v200
v_mfma_f32_16x16x32_bf16 v[232:235],a[132:135],v[116:119],v[232:235]
s_barrier
s_add_u32 m0,s49,s51
ds_read_b128 a[128:131],v1
v_mfma_f32_16x16x32_bf16 v[232:235],a[136:139],v[120:123],v[232:235]
s_mov_b32 s90,L_endseg_%=-L_pc_%=+128
v_exp_f32 v201,v201
v_mfma_f32_16x16x32_bf16 v[232:235],a[140:143],v[124:127],v[232:235]
s_lshr_b32 s45,s45,1
ds_read_b128 a[132:135],v1 offset:256
v_cvt_pk_bf16_f32 v200,v200,v201
v_mfma_f32_16x16x32_bf16 v[236:239],a[156:159],v[124:127],v[44:47]
s_add_u32 s62,s50,32768
buffer_load_dwordx4 v4,s[20:23],s47 offen nt lds
v_mfma_f32_16x16x32_bf16 v[236:239],a[152:155],v[120:123],v[236:239]
s_and_b32 s62,s62,131071
ds_read_b128 a[136:139],v1 offset:512
v_add_u32 v2,s48,v2
v_mfma_f32_16x16x32_bf16 v[236:239],a[148:151],v[116:119],v[236:239]
s_add_u32 s54,s54,16384
v_exp_f32 v202,v202
v_mfma_f32_16x16x32_bf16 v[236:239],a[144:147],v[112:115],v[236:239]
s_sub_u32 s53,s53,1
ds_read_b128 a[140:143],v1 offset:768
v_add_u32 v3,s48,v3
v_mfma_f32_16x16x32_bf16 v[248:251],a[160:163],v[112:115],v[44:47]
s_sub_u32 s48,s62,s50
v_exp_f32 v203,v203
v_mfma_f32_16x16x32_bf16 v[248:251],a[164:167],v[116:119],v[248:251]
s_and_b32 s46,s54,0xfffc0000
ds_read_b128 a[156:159],v1 offset:4864
v_cvt_pk_bf16_f32 v201,v202,v203
v_mfma_f32_16x16x32_bf16 v[248:251],a[168:171],v[120:123],v[248:251]
s_mov_b32 s50,s62
v_exp_f32 v204,v204
v_mfma_f32_16x16x32_bf16 v[248:251],a[172:175],v[124:127],v[248:251]
v_exp_f32 v205,v205
v_mfma_f32_16x16x32_bf16 v[252:255],a[188:191],v[124:127],v[44:47]
ds_read_b128 a[152:155],v1 offset:4608
v_cvt_pk_bf16_f32 v202,v204,v205
v_mfma_f32_16x16x32_bf16 v[252:255],a[184:187],v[120:123],v[252:255]
v_exp_f32 v206,v206
v_mfma_f32_16x16x32_bf16 v[252:255],a[180:183],v[116:119],v[252:255]
ds_read_b128 a[148:151],v1 offset:4352
v_mfma_f32_16x16x32_bf16 v[252:255],a[176:179],v[112:115],v[252:255]
v_exp_f32 v207,v207
v_mfma_f32_16x16x32_bf16 a[32:35],a[192:195],v[168:171],a[32:35]
ds_read_b128 a[144:147],v1 offset:4096
v_cvt_pk_bf16_f32 v203,v206,v207
v_mfma_f32_16x16x32_bf16 a[32:35],a[196:199],v[172:175],a[32:35]
v_exp_f32 v216,v216
v_mfma_f32_16x16x32_bf16 a[36:39],a[204:207],v[172:175],a[36:39]
buffer_load_dwordx4 v4,s[20:23],s47 offen offset:1024 nt lds
v_mfma_f32_16x16x32_bf16 a[36:39],a[200:203],v[168:171],a[36:39]
ds_read_b128 a[160:163],v1 offset:8192
v_mfma_f32_16x16x32_bf16 a[40:43],a[208:211],v[168:171],a[40:43]
v_exp_f32 v217,v217
v_mfma_f32_16x16x32_bf16 a[40:43],a[212:215],v[172:175],a[40:43]
ds_read_b128 a[164:167],v1 offset:8448
v_cvt_pk_bf16_f32 v204,v216,v217
v_mfma_f32_16x16x32_bf16 a[44:47],a[220:223],v[172:175],a[44:47]
v_exp_f32 v218,v218
v_mfma_f32_16x16x32_bf16 a[44:47],a[216:219],v[168:171],a[44:47]
ds_read_b128 a[168:171],v1 offset:8704
v_mfma_f32_16x16x32_bf16 a[48:51],a[224:227],v[168:171],a[48:51]
v_exp_f32 v219,v219
v_mfma_f32_16x16x32_bf16 a[48:51],a[228:231],v[172:175],a[48:51]
ds_read_b128 a[172:175],v1 offset:8960
v_cvt_pk_bf16_f32 v205,v218,v219
v_mfma_f32_16x16x32_bf16 a[52:55],a[236:239],v[172:175],a[52:55]
v_exp_f32 v220,v220
v_mfma_f32_16x16x32_bf16 a[52:55],a[232:235],v[168:171],a[52:55]
ds_read_b128 a[188:191],v1 offset:13056
v_mfma_f32_16x16x32_bf16 a[56:59],a[240:243],v[168:171],a[56:59]
v_exp_f32 v221,v221
v_mfma_f32_16x16x32_bf16 a[56:59],a[244:247],v[172:175],a[56:59]
ds_read_b128 a[184:187],v1 offset:12800
v_cvt_pk_bf16_f32 v206,v220,v221
v_mfma_f32_16x16x32_bf16 a[60:63],a[252:255],v[172:175],a[60:63]
v_exp_f32 v222,v222
v_mfma_f32_16x16x32_bf16 a[60:63],a[248:251],v[168:171],a[60:63]
ds_read_b128 a[180:183],v1 offset:12544
v_smfmac_f32_16x16x64_bf16 v[52:55],v[28:31],v[168:175],v27
s_waitcnt lgkmcnt(4)
v_mfma_f32_16x16x32_bf16 v[136:139],a[128:131],v[64:67],v[32:35]
buffer_load_dwordx4 v5,s[20:23],s47 offen offset:2048 nt lds
v_mfma_f32_16x16x32_bf16 v[136:139],a[132:135],v[68:71],v[136:139]
ds_read_b128 a[176:179],v1 offset:12288
v_add_u32 v1,s48,v1
v_mfma_f32_16x16x32_bf16 v[136:139],a[136:139],v[72:75],v[136:139]
v_exp_f32 v223,v223
v_mfma_f32_16x16x32_bf16 v[136:139],a[140:143],v[76:79],v[136:139]
v_cvt_pk_bf16_f32 v207,v222,v223
v_mfma_f32_16x16x32_bf16 v[140:143],a[156:159],v[76:79],v[32:35]
)A"
R"A(v_exp_f32 v232,v232
v_mfma_f32_16x16x32_bf16 v[140:143],a[152:155],v[72:75],v[140:143]
v_exp_f32 v233,v233
v_mfma_f32_16x16x32_bf16 v[140:143],a[148:151],v[68:71],v[140:143]
v_exp_f32 v234,v234
v_mfma_f32_16x16x32_bf16 v[140:143],a[144:147],v[64:67],v[140:143]
v_exp_f32 v235,v235
v_mfma_f32_16x16x32_bf16 v[152:155],a[160:163],v[64:67],v[32:35]
v_exp_f32 v236,v236
v_mfma_f32_16x16x32_bf16 v[152:155],a[164:167],v[68:71],v[152:155]
v_exp_f32 v237,v237
v_mfma_f32_16x16x32_bf16 v[152:155],a[168:171],v[72:75],v[152:155]
s_waitcnt lgkmcnt(0)
v_exp_f32 v238,v238
v_mfma_f32_16x16x32_bf16 v[152:155],a[172:175],v[76:79],v[152:155]
v_exp_f32 v239,v239
v_mfma_f32_16x16x32_bf16 v[156:159],a[188:191],v[76:79],v[32:35]
v_exp_f32 v248,v248
v_mfma_f32_16x16x32_bf16 v[156:159],a[184:187],v[72:75],v[156:159]
buffer_load_dwordx4 v5,s[20:23],s47 offen offset:3072 nt lds
v_mfma_f32_16x16x32_bf16 v[156:159],a[180:183],v[68:71],v[156:159]
v_exp_f32 v249,v249
v_mfma_f32_16x16x32_bf16 v[156:159],a[176:179],v[64:67],v[156:159]
v_exp_f32 v250,v250
v_mfma_f32_16x16x32_bf16 a[64:67],a[192:195],v[200:203],a[64:67]
v_exp_f32 v251,v251
v_mfma_f32_16x16x32_bf16 a[64:67],a[196:199],v[204:207],a[64:67]
v_exp_f32 v252,v252
v_mfma_f32_16x16x32_bf16 a[68:71],a[204:207],v[204:207],a[68:71]
v_exp_f32 v253,v253
v_mfma_f32_16x16x32_bf16 a[68:71],a[200:203],v[200:203],a[68:71]
v_exp_f32 v254,v254
v_mfma_f32_16x16x32_bf16 a[72:75],a[208:211],v[200:203],a[72:75]
v_exp_f32 v255,v255
v_mfma_f32_16x16x32_bf16 a[72:75],a[212:215],v[204:207],a[72:75]
v_exp_f32 v136,v136
v_mfma_f32_16x16x32_bf16 a[76:79],a[220:223],v[204:207],a[76:79]
v_exp_f32 v137,v137
v_mfma_f32_16x16x32_bf16 a[76:79],a[216:219],v[200:203],a[76:79]
v_exp_f32 v138,v138
v_mfma_f32_16x16x32_bf16 a[80:83],a[224:227],v[200:203],a[80:83]
s_add_u32 m0,s49,s52
v_cvt_pk_bf16_f32 v232,v232,v233
v_cvt_pk_bf16_f32 v233,v234,v235
v_mfma_f32_16x16x32_bf16 a[80:83],a[228:231],v[204:207],a[80:83]
s_xor_b32 s49,s50,65536
v_cvt_pk_bf16_f32 v234,v236,v237
v_cvt_pk_bf16_f32 v235,v238,v239
v_mfma_f32_16x16x32_bf16 a[84:87],a[236:239],v[204:207],a[84:87]
v_cvt_pk_bf16_f32 v236,v248,v249
v_cvt_pk_bf16_f32 v237,v250,v251
v_mfma_f32_16x16x32_bf16 a[84:87],a[232:235],v[200:203],a[84:87]
buffer_load_dwordx4 v4,s[24:27],s47 offen nt lds
v_mfma_f32_16x16x32_bf16 a[88:91],a[240:243],v[200:203],a[88:91]
v_cvt_pk_bf16_f32 v238,v252,v253
v_cvt_pk_bf16_f32 v239,v254,v255
v_mfma_f32_16x16x32_bf16 a[88:91],a[244:247],v[204:207],a[88:91]
v_exp_f32 v139,v139
v_mfma_f32_16x16x32_bf16 a[92:95],a[252:255],v[204:207],a[92:95]
v_exp_f32 v140,v140
v_mfma_f32_16x16x32_bf16 a[92:95],a[248:251],v[200:203],a[92:95]
v_exp_f32 v141,v141
v_smfmac_f32_16x16x64_bf16 v[56:59],v[28:31],v[200:207],v27
v_mfma_f32_16x16x32_bf16 v[168:171],a[128:131],v[80:83],v[36:39]
v_exp_f32 v142,v142
v_mfma_f32_16x16x32_bf16 v[168:171],a[132:135],v[84:87],v[168:171]
v_exp_f32 v143,v143
v_mfma_f32_16x16x32_bf16 v[168:171],a[136:139],v[88:91],v[168:171]
v_exp_f32 v152,v152
v_mfma_f32_16x16x32_bf16 v[168:171],a[140:143],v[92:95],v[168:171]
v_exp_f32 v153,v153
v_mfma_f32_16x16x32_bf16 v[172:175],a[156:159],v[92:95],v[36:39]
v_exp_f32 v154,v154
v_mfma_f32_16x16x32_bf16 v[172:175],a[152:155],v[88:91],v[172:175]
v_exp_f32 v155,v155
v_mfma_f32_16x16x32_bf16 v[172:175],a[148:151],v[84:87],v[172:175]
v_exp_f32 v156,v156
v_mfma_f32_16x16x32_bf16 v[172:175],a[144:147],v[80:83],v[172:175]
v_exp_f32 v157,v157
v_mfma_f32_16x16x32_bf16 v[184:187],a[160:163],v[80:83],v[36:39]
buffer_load_dwordx4 v5,s[24:27],s47 offen offset:1024 nt lds
v_mfma_f32_16x16x32_bf16 v[184:187],a[164:167],v[84:87],v[184:187]
v_exp_f32 v158,v158
v_mfma_f32_16x16x32_bf16 v[184:187],a[168:171],v[88:91],v[184:187]
v_exp_f32 v159,v159
v_mfma_f32_16x16x32_bf16 v[184:187],a[172:175],v[92:95],v[184:187]
v_exp_f32 v168,v168
v_mfma_f32_16x16x32_bf16 v[188:191],a[188:191],v[92:95],v[36:39]
v_exp_f32 v169,v169
v_mfma_f32_16x16x32_bf16 v[188:191],a[184:187],v[88:91],v[188:191]
v_exp_f32 v170,v170
v_mfma_f32_16x16x32_bf16 v[188:191],a[180:183],v[84:87],v[188:191]
v_exp_f32 v171,v171
v_mfma_f32_16x16x32_bf16 v[188:191],a[176:179],v[80:83],v[188:191]
v_exp_f32 v172,v172
v_mfma_f32_16x16x32_bf16 a[96:99],a[192:195],v[232:235],a[96:99]
ds_read_b64_tr_b16 a[192:193],v2
v_cvt_pk_bf16_f32 v168,v168,v169
v_mfma_f32_16x16x32_bf16 a[96:99],a[196:199],v[236:239],a[96:99]
ds_read_b64_tr_b16 a[194:195],v2 offset:4096
v_cvt_pk_bf16_f32 v169,v170,v171
v_mfma_f32_16x16x32_bf16 a[100:103],a[204:207],v[236:239],a[100:103]
v_exp_f32 v173,v173
v_mfma_f32_16x16x32_bf16 a[100:103],a[200:203],v[232:235],a[100:103]
ds_read_b64_tr_b16 a[196:197],v2 offset:8192
v_cvt_pk_bf16_f32 v170,v172,v173
v_mfma_f32_16x16x32_bf16 a[104:107],a[208:211],v[232:235],a[104:107]
ds_read_b64_tr_b16 a[198:199],v2 offset:12288
v_cvt_pk_bf16_f32 v136,v136,v137
v_mfma_f32_16x16x32_bf16 a[104:107],a[212:215],v[236:239],a[104:107]
v_exp_f32 v174,v174
v_mfma_f32_16x16x32_bf16 a[108:111],a[220:223],v[236:239],a[108:111]
buffer_load_dwordx4 v4,s[24:27],s47 offen offset:2048 nt lds
v_mfma_f32_16x16x32_bf16 a[108:111],a[216:219],v[232:235],a[108:111]
ds_read_b64_tr_b16 a[204:205],v3 offset:8192
v_cvt_pk_bf16_f32 v137,v138,v139
v_mfma_f32_16x16x32_bf16 a[112:115],a[224:227],v[232:235],a[112:115]
v_exp_f32 v175,v175
v_mfma_f32_16x16x32_bf16 a[112:115],a[228:231],v[236:239],a[112:115]
ds_read_b64_tr_b16 a[206:207],v3 offset:12288
v_cvt_pk_bf16_f32 v171,v174,v175
v_mfma_f32_16x16x32_bf16 a[116:119],a[236:239],v[236:239],a[116:119]
ds_read_b64_tr_b16 a[200:201],v3
v_cvt_pk_bf16_f32 v138,v140,v141
v_mfma_f32_16x16x32_bf16 a[116:119],a[232:235],v[232:235],a[116:119]
ds_read_b64_tr_b16 a[202:203],v3 offset:4096
v_mfma_f32_16x16x32_bf16 a[120:123],a[240:243],v[232:235],a[120:123]
ds_read_b64_tr_b16 a[208:209],v2 offset:256
v_mfma_f32_16x16x32_bf16 a[120:123],a[244:247],v[236:239],a[120:123]
ds_read_b64_tr_b16 a[210:211],v2 offset:4352
v_cvt_pk_bf16_f32 v139,v142,v143
v_mfma_f32_16x16x32_bf16 a[124:127],a[252:255],v[236:239],a[124:127]
ds_read_b64_tr_b16 a[212:213],v2 offset:8448
v_mfma_f32_16x16x32_bf16 a[124:127],a[248:251],v[232:235],a[124:127]
ds_read_b64_tr_b16 a[214:215],v2 offset:12544
v_smfmac_f32_16x16x64_bf16 v[60:63],v[28:31],v[232:239],v27
ds_read_b64_tr_b16 a[220:221],v3 offset:8448
v_mfma_f32_16x16x32_bf16 v[200:203],a[128:131],v[96:99],v[40:43]
ds_read_b64_tr_b16 a[222:223],v3 offset:12544
v_cvt_pk_bf16_f32 v140,v152,v153
v_mfma_f32_16x16x32_bf16 v[200:203],a[132:135],v[100:103],v[200:203]
ds_read_b64_tr_b16 a[216:217],v3 offset:256
v_mfma_f32_16x16x32_bf16 v[200:203],a[136:139],v[104:107],v[200:203]
ds_read_b64_tr_b16 a[218:219],v3 offset:4352
v_cvt_pk_bf16_f32 v141,v154,v155
v_mfma_f32_16x16x32_bf16 v[200:203],a[140:143],v[108:111],v[200:203]
buffer_load_dwordx4 v5,s[24:27],s47 offen offset:3072 nt lds
v_mfma_f32_16x16x32_bf16 v[204:207],a[156:159],v[108:111],v[40:43]
s_add_u32 s47,s47,16384
s_and_b32 s47,s47,0x3ffff
ds_read_b64_tr_b16 a[224:225],v2 offset:512
v_mfma_f32_16x16x32_bf16 v[204:207],a[152:155],v[104:107],v[204:207]
s_or_b32 s47,s47,s46
ds_read_b64_tr_b16 a[226:227],v2 offset:4608
v_cvt_pk_bf16_f32 v142,v156,v157
v_mfma_f32_16x16x32_bf16 v[204:207],a[148:151],v[100:103],v[204:207]
ds_read_b64_tr_b16 a[228:229],v2 offset:8704
v_mfma_f32_16x16x32_bf16 v[204:207],a[144:147],v[96:99],v[204:207]
ds_read_b64_tr_b16 a[230:231],v2 offset:12800
v_mfma_f32_16x16x32_bf16 v[216:219],a[160:163],v[96:99],v[40:43]
ds_read_b64_tr_b16 a[236:237],v3 offset:8704
v_cvt_pk_bf16_f32 v143,v158,v159
v_mfma_f32_16x16x32_bf16 v[216:219],a[164:167],v[100:103],v[216:219]
ds_read_b64_tr_b16 a[238:239],v3 offset:12800
v_mfma_f32_16x16x32_bf16 v[216:219],a[168:171],v[104:107],v[216:219]
ds_read_b64_tr_b16 a[232:233],v3 offset:512
v_mfma_f32_16x16x32_bf16 v[216:219],a[172:175],v[108:111],v[216:219]
ds_read_b64_tr_b16 a[234:235],v3 offset:4608
v_mfma_f32_16x16x32_bf16 v[220:223],a[188:191],v[108:111],v[40:43]
ds_read_b64_tr_b16 a[240:241],v2 offset:768
v_mfma_f32_16x16x32_bf16 v[220:223],a[184:187],v[104:107],v[220:223]
ds_read_b64_tr_b16 a[242:243],v2 offset:4864
v_mfma_f32_16x16x32_bf16 v[220:223],a[180:183],v[100:103],v[220:223]
ds_read_b64_tr_b16 a[244:245],v2 offset:8960
v_mfma_f32_16x16x32_bf16 v[220:223],a[176:179],v[96:99],v[220:223]
s_waitcnt lgkmcnt(7)
buffer_load_dword v26,v24,s[88:91],s93 offen
v_mfma_f32_16x16x32_bf16 a[0:3],a[192:195],v[136:139],a[0:3]
ds_read_b64_tr_b16 a[246:247],v2 offset:13056
v_mfma_f32_16x16x32_bf16 a[0:3],a[196:199],v[140:143],a[0:3]
ds_read_b64_tr_b16 a[252:253],v3 offset:8960
v_mfma_f32_16x16x32_bf16 a[4:7],a[204:207],v[140:143],a[4:7]
ds_read_b64_tr_b16 a[254:255],v3 offset:13056
v_mfma_f32_16x16x32_bf16 a[4:7],a[200:203],v[136:139],a[4:7]
s_memrealtime s[76:77]
ds_read_b64_tr_b16 a[248:249],v3 offset:768
v_mfma_f32_16x16x32_bf16 a[8:11],a[208:211],v[136:139],a[8:11]
ds_read_b64_tr_b16 a[250:251],v3 offset:4864
v_mfma_f32_16x16x32_bf16 a[8:11],a[212:215],v[140:143],a[8:11]
v_exp_f32 v184,v184
v_mfma_f32_16x16x32_bf16 a[12:15],a[220:223],v[140:143],a[12:15]
v_exp_f32 v185,v185
v_mfma_f32_16x16x32_bf16 a[12:15],a[216:219],v[136:139],a[12:15]
s_bitcmp1_b32 s45,0
s_cbranch_scc0 L_t310_%=
s_mov_b32 m0,s80
s_add_u32 s80,s80,4096
buffer_load_dwordx4 v25,s[20:23],s83 offen nt lds
s_add_u32 s83,s83,4096
L_t310_%=:
v_mfma_f32_16x16x32_bf16 a[16:19],a[224:227],v[136:139],a[16:19]
v_exp_f32 v186,v186
v_mfma_f32_16x16x32_bf16 a[16:19],a[228:231],v[140:143],a[16:19]
s_waitcnt lgkmcnt(3)
v_exp_f32 v187,v187
v_mfma_f32_16x16x32_bf16 a[20:23],a[236:239],v[140:143],a[20:23]
s_waitcnt lgkmcnt(0)
v_exp_f32 v188,v188
v_mfma_f32_16x16x32_bf16 a[20:23],a[232:235],v[136:139],a[20:23]
s_sub_u32 s79,s78,s76
v_exp_f32 v189,v189
v_mfma_f32_16x16x32_bf16 a[24:27],a[240:243],v[136:139],a[24:27]
s_add_u32 s78,s76,s60
v_exp_f32 v190,v190
v_mfma_f32_16x16x32_bf16 a[24:27],a[244:247],v[140:143],a[24:27]
s_cmp_gt_i32 s79,0
s_cbranch_scc1 L_ps3_%=
L_pr3_%=:
v_cvt_pk_bf16_f32 v172,v184,v185
v_mfma_f32_16x16x32_bf16 a[28:31],a[252:255],v[140:143],a[28:31]
v_exp_f32 v191,v191
v_mfma_f32_16x16x32_bf16 a[28:31],a[248:251],v[136:139],a[28:31]
v_cvt_pk_bf16_f32 v173,v186,v187
v_cvt_pk_bf16_f32 v174,v188,v189
v_cvt_pk_bf16_f32 v175,v190,v191
s_cmp_lg_u32 s53,0
v_smfmac_f32_16x16x64_bf16 v[48:51],v[28:31],v[136:143],v27
s_cbranch_scc1 L_att0_%=
L_att0x_%=:
v_mfma_f32_16x16x32_bf16 v[232:235],a[128:131],v[112:115],v[44:47]
s_waitcnt vmcnt(10)
s_add_u32 s54,s54,16384
v_exp_f32 v200,v200
v_mfma_f32_16x16x32_bf16 v[232:235],a[132:135],v[116:119],v[232:235]
s_barrier
s_add_u32 m0,0,s51
ds_read_b128 a[128:131],v1
v_mfma_f32_16x16x32_bf16 v[232:235],a[136:139],v[120:123],v[232:235]
s_and_b32 s46,s54,0xfffc0000
ds_read_b128 a[132:135],v1 offset:256
v_and_b32 v18,48,v6
v_mfma_f32_16x16x32_bf16 v[232:235],a[140:143],v[124:127],v[232:235]
s_add_u32 s54,s54,16384
ds_read_b128 a[136:139],v1 offset:512
v_lshrrev_b32 v19,4,v6
v_mfma_f32_16x16x32_bf16 v[236:239],a[156:159],v[124:127],v[44:47]
s_mul_i32 s83,s41,5
buffer_load_dwordx4 v4,s[20:23],s47 offen nt lds
v_mfma_f32_16x16x32_bf16 v[236:239],a[152:155],v[120:123],v[236:239]
s_lshl_b32 s44,s40,5
ds_read_b128 a[140:143],v1 offset:768
v_lshlrev_b32 v18,2,v18
v_mfma_f32_16x16x32_bf16 v[236:239],a[148:151],v[116:119],v[236:239]
s_add_u32 s83,s83,s42
ds_read_b128 a[156:159],v1 offset:4864
v_and_b32 v19,48,v19
v_mfma_f32_16x16x32_bf16 v[236:239],a[144:147],v[112:115],v[236:239]
s_add_u32 s84,s55,0
ds_read_b128 a[152:155],v1 offset:4608
v_add_u32 v18,v18,v19
v_mfma_f32_16x16x32_bf16 v[248:251],a[160:163],v[112:115],v[44:47]
s_add_u32 s85,s55,4096
ds_read_b128 a[148:151],v1 offset:4352
v_add_u32 v18,98304,v18
v_mfma_f32_16x16x32_bf16 v[248:251],a[164:167],v[116:119],v[248:251]
s_add_u32 s86,s55,8192
ds_read_b128 a[144:147],v1 offset:4096
v_add_u32 v2,s48,v2
v_mfma_f32_16x16x32_bf16 v[248:251],a[168:171],v[120:123],v[248:251]
s_add_u32 s87,s55,12288
ds_read_b128 a[160:163],v1 offset:8192
v_add_u32 v3,s48,v3
v_mfma_f32_16x16x32_bf16 v[248:251],a[172:175],v[124:127],v[248:251]
s_mov_b32 s48,32768
s_and_b32 s44,s44,32
ds_read_b128 a[164:167],v1 offset:8448
)A"
R"A(v_mfma_f32_16x16x32_bf16 v[252:255],a[188:191],v[124:127],v[44:47]
s_add_u32 s83,s83,15
ds_read_b128 a[168:171],v1 offset:8704
v_subrev_u32 v25,s51,v4
v_mfma_f32_16x16x32_bf16 v[252:255],a[184:187],v[120:123],v[252:255]
s_max_i32 s84,s84,s18
s_max_i32 s85,s85,s19
ds_read_b128 a[172:175],v1 offset:8960
v_mfma_f32_16x16x32_bf16 v[252:255],a[180:183],v[116:119],v[252:255]
s_max_i32 s86,s86,s100
s_max_i32 s87,s87,s101
ds_read_b128 a[188:191],v1 offset:13056
v_mfma_f32_16x16x32_bf16 v[252:255],a[176:179],v[112:115],v[252:255]
s_lshl_b32 s80,s40,10
ds_read_b128 a[184:187],v1 offset:12800
v_xor_b32 v25,s44,v25
v_mfma_f32_16x16x32_bf16 a[32:35],a[192:195],v[168:171],a[32:35]
s_and_b32 s83,s83,15
ds_read_b128 a[180:183],v1 offset:12544
v_add_u32 v25,s80,v25
v_mfma_f32_16x16x32_bf16 a[32:35],a[196:199],v[172:175],a[32:35]
s_add_u32 s83,s83,16
v_exp_f32 v201,v201
v_mfma_f32_16x16x32_bf16 a[36:39],a[204:207],v[172:175],a[36:39]
s_mov_b32 s50,32768
buffer_load_dwordx4 v4,s[20:23],s47 offen offset:1024 nt lds
v_mfma_f32_16x16x32_bf16 a[36:39],a[200:203],v[168:171],a[36:39]
s_mov_b32 s49,98304
ds_read_b128 a[176:179],v1 offset:12288
v_cvt_pk_bf16_f32 v200,v200,v201
v_mfma_f32_16x16x32_bf16 a[40:43],a[208:211],v[168:171],a[40:43]
s_mov_b32 s45,0x2a8
v_exp_f32 v202,v202
v_mfma_f32_16x16x32_bf16 a[40:43],a[212:215],v[172:175],a[40:43]
s_add_u32 s80,s80,147456
v_exp_f32 v203,v203
v_mfma_f32_16x16x32_bf16 a[44:47],a[220:223],v[172:175],a[44:47]
s_lshl_b32 s83,s83,14
v_exp_f32 v204,v204
v_mfma_f32_16x16x32_bf16 a[44:47],a[216:219],v[168:171],a[44:47]
s_add_u32 s93,s92,L_endseg_%=-L_pc_%=
v_exp_f32 v205,v205
v_mfma_f32_16x16x32_bf16 a[48:51],a[224:227],v[168:171],a[48:51]
v_exp_f32 v206,v206
v_mfma_f32_16x16x32_bf16 a[48:51],a[228:231],v[172:175],a[48:51]
v_exp_f32 v207,v207
v_mfma_f32_16x16x32_bf16 a[52:55],a[236:239],v[172:175],a[52:55]
v_cvt_pk_bf16_f32 v201,v202,v203
v_cvt_pk_bf16_f32 v202,v204,v205
v_mfma_f32_16x16x32_bf16 a[52:55],a[232:235],v[168:171],a[52:55]
v_cvt_pk_bf16_f32 v203,v206,v207
v_add_u32 v1,s48,v1
v_mfma_f32_16x16x32_bf16 a[56:59],a[240:243],v[168:171],a[56:59]
v_exp_f32 v216,v216
v_mfma_f32_16x16x32_bf16 a[56:59],a[244:247],v[172:175],a[56:59]
v_exp_f32 v217,v217
v_mfma_f32_16x16x32_bf16 a[60:63],a[252:255],v[172:175],a[60:63]
v_exp_f32 v218,v218
v_mfma_f32_16x16x32_bf16 a[60:63],a[248:251],v[168:171],a[60:63]
v_exp_f32 v219,v219
v_smfmac_f32_16x16x64_bf16 v[52:55],v[28:31],v[168:175],v27
s_waitcnt lgkmcnt(0)
v_cvt_pk_bf16_f32 v204,v216,v217
v_mfma_f32_16x16x32_bf16 v[136:139],a[128:131],v[64:67],v[32:35]
buffer_load_dwordx4 v5,s[20:23],s47 offen offset:2048 nt lds
v_mfma_f32_16x16x32_bf16 v[136:139],a[132:135],v[68:71],v[136:139]
v_exp_f32 v220,v220
v_mfma_f32_16x16x32_bf16 v[136:139],a[136:139],v[72:75],v[136:139]
v_exp_f32 v221,v221
v_mfma_f32_16x16x32_bf16 v[136:139],a[140:143],v[76:79],v[136:139]
v_exp_f32 v222,v222
v_mfma_f32_16x16x32_bf16 v[140:143],a[156:159],v[76:79],v[32:35]
v_exp_f32 v223,v223
v_mfma_f32_16x16x32_bf16 v[140:143],a[152:155],v[72:75],v[140:143]
v_cvt_pk_bf16_f32 v205,v218,v219
v_cvt_pk_bf16_f32 v206,v220,v221
v_mfma_f32_16x16x32_bf16 v[140:143],a[148:151],v[68:71],v[140:143]
v_cvt_pk_bf16_f32 v207,v222,v223
v_mfma_f32_16x16x32_bf16 v[140:143],a[144:147],v[64:67],v[140:143]
v_exp_f32 v232,v232
v_mfma_f32_16x16x32_bf16 v[152:155],a[160:163],v[64:67],v[32:35]
v_exp_f32 v233,v233
v_mfma_f32_16x16x32_bf16 v[152:155],a[164:167],v[68:71],v[152:155]
v_exp_f32 v234,v234
v_mfma_f32_16x16x32_bf16 v[152:155],a[168:171],v[72:75],v[152:155]
v_exp_f32 v235,v235
v_mfma_f32_16x16x32_bf16 v[152:155],a[172:175],v[76:79],v[152:155]
v_exp_f32 v236,v236
v_mfma_f32_16x16x32_bf16 v[156:159],a[188:191],v[76:79],v[32:35]
v_exp_f32 v237,v237
v_mfma_f32_16x16x32_bf16 v[156:159],a[184:187],v[72:75],v[156:159]
buffer_load_dwordx4 v5,s[20:23],s47 offen offset:3072 nt lds
v_mfma_f32_16x16x32_bf16 v[156:159],a[180:183],v[68:71],v[156:159]
v_exp_f32 v238,v238
v_mfma_f32_16x16x32_bf16 v[156:159],a[176:179],v[64:67],v[156:159]
v_exp_f32 v239,v239
v_mfma_f32_16x16x32_bf16 a[64:67],a[192:195],v[200:203],a[64:67]
v_exp_f32 v248,v248
v_mfma_f32_16x16x32_bf16 a[64:67],a[196:199],v[204:207],a[64:67]
v_exp_f32 v249,v249
v_mfma_f32_16x16x32_bf16 a[68:71],a[204:207],v[204:207],a[68:71]
v_exp_f32 v250,v250
v_mfma_f32_16x16x32_bf16 a[68:71],a[200:203],v[200:203],a[68:71]
v_exp_f32 v251,v251
v_mfma_f32_16x16x32_bf16 a[72:75],a[208:211],v[200:203],a[72:75]
v_exp_f32 v252,v252
v_mfma_f32_16x16x32_bf16 a[72:75],a[212:215],v[204:207],a[72:75]
v_exp_f32 v253,v253
v_mfma_f32_16x16x32_bf16 a[76:79],a[220:223],v[204:207],a[76:79]
v_exp_f32 v254,v254
v_mfma_f32_16x16x32_bf16 a[76:79],a[216:219],v[200:203],a[76:79]
v_exp_f32 v255,v255
v_mfma_f32_16x16x32_bf16 a[80:83],a[224:227],v[200:203],a[80:83]
s_add_u32 m0,0,s52
v_exp_f32 v136,v136
v_mfma_f32_16x16x32_bf16 a[80:83],a[228:231],v[204:207],a[80:83]
v_exp_f32 v137,v137
v_mfma_f32_16x16x32_bf16 a[84:87],a[236:239],v[204:207],a[84:87]
v_cvt_pk_bf16_f32 v232,v232,v233
v_cvt_pk_bf16_f32 v233,v234,v235
v_mfma_f32_16x16x32_bf16 a[84:87],a[232:235],v[200:203],a[84:87]
buffer_load_dwordx4 v4,s[24:27],s47 offen nt lds
v_mfma_f32_16x16x32_bf16 a[88:91],a[240:243],v[200:203],a[88:91]
v_cvt_pk_bf16_f32 v234,v236,v237
v_cvt_pk_bf16_f32 v235,v238,v239
v_mfma_f32_16x16x32_bf16 a[88:91],a[244:247],v[204:207],a[88:91]
v_cvt_pk_bf16_f32 v236,v248,v249
v_cvt_pk_bf16_f32 v237,v250,v251
v_mfma_f32_16x16x32_bf16 a[92:95],a[252:255],v[204:207],a[92:95]
v_cvt_pk_bf16_f32 v238,v252,v253
v_cvt_pk_bf16_f32 v239,v254,v255
v_mfma_f32_16x16x32_bf16 a[92:95],a[248:251],v[200:203],a[92:95]
v_exp_f32 v138,v138
v_smfmac_f32_16x16x64_bf16 v[56:59],v[28:31],v[200:207],v27
v_cvt_pk_bf16_f32 v136,v136,v137
v_mfma_f32_16x16x32_bf16 v[168:171],a[128:131],v[80:83],v[36:39]
v_exp_f32 v139,v139
v_mfma_f32_16x16x32_bf16 v[168:171],a[132:135],v[84:87],v[168:171]
v_exp_f32 v140,v140
v_mfma_f32_16x16x32_bf16 v[168:171],a[136:139],v[88:91],v[168:171]
v_exp_f32 v141,v141
v_mfma_f32_16x16x32_bf16 v[168:171],a[140:143],v[92:95],v[168:171]
v_exp_f32 v142,v142
v_mfma_f32_16x16x32_bf16 v[172:175],a[156:159],v[92:95],v[36:39]
v_exp_f32 v143,v143
v_mfma_f32_16x16x32_bf16 v[172:175],a[152:155],v[88:91],v[172:175]
v_exp_f32 v152,v152
v_mfma_f32_16x16x32_bf16 v[172:175],a[148:151],v[84:87],v[172:175]
v_exp_f32 v153,v153
v_mfma_f32_16x16x32_bf16 v[172:175],a[144:147],v[80:83],v[172:175]
v_exp_f32 v154,v154
v_mfma_f32_16x16x32_bf16 v[184:187],a[160:163],v[80:83],v[36:39]
buffer_load_dwordx4 v5,s[24:27],s47 offen offset:1024 nt lds
v_mfma_f32_16x16x32_bf16 v[184:187],a[164:167],v[84:87],v[184:187]
v_exp_f32 v155,v155
v_mfma_f32_16x16x32_bf16 v[184:187],a[168:171],v[88:91],v[184:187]
v_exp_f32 v156,v156
v_mfma_f32_16x16x32_bf16 v[184:187],a[172:175],v[92:95],v[184:187]
v_exp_f32 v157,v157
v_mfma_f32_16x16x32_bf16 v[188:191],a[188:191],v[92:95],v[36:39]
v_exp_f32 v158,v158
v_mfma_f32_16x16x32_bf16 v[188:191],a[184:187],v[88:91],v[188:191]
v_exp_f32 v159,v159
v_mfma_f32_16x16x32_bf16 v[188:191],a[180:183],v[84:87],v[188:191]
s_waitcnt vmcnt(14)
v_exp_f32 v168,v168
v_mfma_f32_16x16x32_bf16 v[188:191],a[176:179],v[80:83],v[188:191]
s_barrier
ds_read_b128 v[80:83],v18
v_cvt_pk_bf16_f32 v137,v138,v139
v_mfma_f32_16x16x32_bf16 a[96:99],a[192:195],v[232:235],a[96:99]
ds_read_b128 v[84:87],v18 offset:256
v_cvt_pk_bf16_f32 v138,v140,v141
v_mfma_f32_16x16x32_bf16 a[96:99],a[196:199],v[236:239],a[96:99]
ds_read_b128 v[88:91],v18 offset:512
v_cvt_pk_bf16_f32 v139,v142,v143
v_mfma_f32_16x16x32_bf16 a[100:103],a[204:207],v[236:239],a[100:103]
ds_read_b128 v[92:95],v18 offset:768
v_cvt_pk_bf16_f32 v140,v152,v153
v_mfma_f32_16x16x32_bf16 a[100:103],a[200:203],v[232:235],a[100:103]
ds_read_b64_tr_b16 a[192:193],v2
ds_read_b64_tr_b16 a[194:195],v2 offset:4096
v_mfma_f32_16x16x32_bf16 a[104:107],a[208:211],v[232:235],a[104:107]
ds_read_b64_tr_b16 a[196:197],v2 offset:8192
ds_read_b64_tr_b16 a[198:199],v2 offset:12288
v_mfma_f32_16x16x32_bf16 a[104:107],a[212:215],v[236:239],a[104:107]
buffer_load_dwordx4 v4,s[24:27],s47 offen offset:2048 nt lds
v_mfma_f32_16x16x32_bf16 a[108:111],a[220:223],v[236:239],a[108:111]
ds_read_b64_tr_b16 a[204:205],v3 offset:8192
ds_read_b64_tr_b16 a[206:207],v3 offset:12288
v_mfma_f32_16x16x32_bf16 a[108:111],a[216:219],v[232:235],a[108:111]
ds_read_b64_tr_b16 a[200:201],v3
ds_read_b64_tr_b16 a[202:203],v3 offset:4096
v_mfma_f32_16x16x32_bf16 a[112:115],a[224:227],v[232:235],a[112:115]
ds_read_b64_tr_b16 a[208:209],v2 offset:256
ds_read_b64_tr_b16 a[210:211],v2 offset:4352
v_mfma_f32_16x16x32_bf16 a[112:115],a[228:231],v[236:239],a[112:115]
ds_read_b64_tr_b16 a[212:213],v2 offset:8448
ds_read_b64_tr_b16 a[214:215],v2 offset:12544
v_mfma_f32_16x16x32_bf16 a[116:119],a[236:239],v[236:239],a[116:119]
ds_read_b64_tr_b16 a[220:221],v3 offset:8448
ds_read_b64_tr_b16 a[222:223],v3 offset:12544
v_mfma_f32_16x16x32_bf16 a[116:119],a[232:235],v[232:235],a[116:119]
v_cvt_pk_bf16_f32 v141,v154,v155
v_cvt_pk_bf16_f32 v142,v156,v157
v_mfma_f32_16x16x32_bf16 a[120:123],a[240:243],v[232:235],a[120:123]
ds_read_b64_tr_b16 a[216:217],v3 offset:256
v_cvt_pk_bf16_f32 v143,v158,v159
v_mfma_f32_16x16x32_bf16 a[120:123],a[244:247],v[236:239],a[120:123]
ds_read_b64_tr_b16 a[218:219],v3 offset:4352
ds_read_b64_tr_b16 a[224:225],v2 offset:512
v_mfma_f32_16x16x32_bf16 a[124:127],a[252:255],v[236:239],a[124:127]
ds_read_b64_tr_b16 a[226:227],v2 offset:4608
ds_read_b64_tr_b16 a[228:229],v2 offset:8704
v_mfma_f32_16x16x32_bf16 a[124:127],a[248:251],v[232:235],a[124:127]
ds_read_b64_tr_b16 a[230:231],v2 offset:12800
ds_read_b64_tr_b16 a[236:237],v3 offset:8704
v_smfmac_f32_16x16x64_bf16 v[60:63],v[28:31],v[232:239],v27
ds_read_b64_tr_b16 a[238:239],v3 offset:12800
v_mfma_f32_16x16x32_bf16 v[200:203],a[128:131],v[96:99],v[40:43]
ds_read_b64_tr_b16 a[232:233],v3 offset:512
ds_read_b64_tr_b16 a[234:235],v3 offset:4608
v_mfma_f32_16x16x32_bf16 v[200:203],a[132:135],v[100:103],v[200:203]
ds_read_b64_tr_b16 a[240:241],v2 offset:768
ds_read_b64_tr_b16 a[242:243],v2 offset:4864
v_mfma_f32_16x16x32_bf16 v[200:203],a[136:139],v[104:107],v[200:203]
buffer_load_dwordx4 v5,s[24:27],s47 offen offset:3072 nt lds
v_mfma_f32_16x16x32_bf16 v[200:203],a[140:143],v[108:111],v[200:203]
s_add_u32 s47,s47,16384
ds_read_b64_tr_b16 a[244:245],v2 offset:8960
ds_read_b64_tr_b16 a[246:247],v2 offset:13056
v_mfma_f32_16x16x32_bf16 v[204:207],a[156:159],v[108:111],v[40:43]
s_and_b32 s47,s47,0x3ffff
ds_read_b64_tr_b16 a[252:253],v3 offset:8960
ds_read_b64_tr_b16 a[254:255],v3 offset:13056
v_mfma_f32_16x16x32_bf16 v[204:207],a[152:155],v[104:107],v[204:207]
s_or_b32 s47,s47,s46
ds_read_b64_tr_b16 a[248:249],v3 offset:768
ds_read_b64_tr_b16 a[250:251],v3 offset:4864
v_mfma_f32_16x16x32_bf16 v[204:207],a[148:151],v[100:103],v[204:207]
s_and_b32 s46,s54,0xfffc0000
v_exp_f32 v169,v169
v_mfma_f32_16x16x32_bf16 v[204:207],a[144:147],v[96:99],v[204:207]
s_add_u32 s54,s54,16384
v_exp_f32 v170,v170
v_mfma_f32_16x16x32_bf16 v[216:219],a[160:163],v[96:99],v[40:43]
v_exp_f32 v171,v171
v_mfma_f32_16x16x32_bf16 v[216:219],a[164:167],v[100:103],v[216:219]
v_cvt_pk_bf16_f32 v168,v168,v169
v_cvt_pk_bf16_f32 v169,v170,v171
v_mfma_f32_16x16x32_bf16 v[216:219],a[168:171],v[104:107],v[216:219]
v_exp_f32 v172,v172
v_mfma_f32_16x16x32_bf16 v[216:219],a[172:175],v[108:111],v[216:219]
v_exp_f32 v173,v173
v_mfma_f32_16x16x32_bf16 v[220:223],a[188:191],v[108:111],v[40:43]
v_exp_f32 v174,v174
v_mfma_f32_16x16x32_bf16 v[220:223],a[184:187],v[104:107],v[220:223]
v_exp_f32 v175,v175
v_mfma_f32_16x16x32_bf16 v[220:223],a[180:183],v[100:103],v[220:223]
v_cvt_pk_bf16_f32 v170,v172,v173
v_cvt_pk_bf16_f32 v171,v174,v175
v_mfma_f32_16x16x32_bf16 v[220:223],a[176:179],v[96:99],v[220:223]
s_waitcnt lgkmcnt(0)
v_exp_f32 v184,v184
v_mfma_f32_16x16x32_bf16 v[32:35],v[80:83],v[128:131],0
v_exp_f32 v185,v185
v_mfma_f32_16x16x32_bf16 v[32:35],v[84:87],v[160:163],v[32:35]
v_exp_f32 v186,v186
v_mfma_f32_16x16x32_bf16 v[32:35],v[88:91],v[192:195],v[32:35]
v_exp_f32 v187,v187
v_mfma_f32_16x16x32_bf16 v[32:35],v[92:95],v[224:227],v[32:35]
)A"
R"A(v_exp_f32 v188,v188
v_mfma_f32_16x16x32_bf16 v[36:39],v[80:83],v[132:135],0
v_exp_f32 v189,v189
v_mfma_f32_16x16x32_bf16 v[36:39],v[84:87],v[164:167],v[36:39]
v_exp_f32 v190,v190
v_mfma_f32_16x16x32_bf16 v[36:39],v[88:91],v[196:199],v[36:39]
v_exp_f32 v191,v191
v_mfma_f32_16x16x32_bf16 v[36:39],v[92:95],v[228:231],v[36:39]
v_cvt_pk_bf16_f32 v172,v184,v185
v_cvt_pk_bf16_f32 v173,v186,v187
v_mfma_f32_16x16x32_bf16 v[40:43],v[80:83],v[144:147],0
v_cvt_pk_bf16_f32 v174,v188,v189
v_cvt_pk_bf16_f32 v175,v190,v191
v_mfma_f32_16x16x32_bf16 v[40:43],v[84:87],v[176:179],v[40:43]
v_max3_f32 v32,v32,v33,v34
v_max3_f32 v32,v32,v35,s18
v_mfma_f32_16x16x32_bf16 v[40:43],v[88:91],v[208:211],v[40:43]
v_sub_f32 v35,0,v32
v_sub_f32 v34,0,v32
v_mfma_f32_16x16x32_bf16 v[40:43],v[92:95],v[240:243],v[40:43]
v_sub_f32 v33,0,v32
v_sub_f32 v32,0,v32
v_mfma_f32_16x16x32_bf16 a[0:3],a[192:195],v[136:139],a[0:3]
v_exp_f32 v200,v200
v_mfma_f32_16x16x32_bf16 a[0:3],a[196:199],v[140:143],a[0:3]
v_exp_f32 v201,v201
v_mfma_f32_16x16x32_bf16 a[4:7],a[204:207],v[140:143],a[4:7]
v_exp_f32 v202,v202
v_mfma_f32_16x16x32_bf16 a[4:7],a[200:203],v[136:139],a[4:7]
s_memrealtime s[76:77]
v_exp_f32 v203,v203
v_mfma_f32_16x16x32_bf16 a[8:11],a[208:211],v[136:139],a[8:11]
v_cvt_pk_bf16_f32 v200,v200,v201
v_cvt_pk_bf16_f32 v201,v202,v203
v_mfma_f32_16x16x32_bf16 a[8:11],a[212:215],v[140:143],a[8:11]
v_exp_f32 v204,v204
v_mfma_f32_16x16x32_bf16 a[12:15],a[220:223],v[140:143],a[12:15]
v_exp_f32 v205,v205
v_mfma_f32_16x16x32_bf16 a[12:15],a[216:219],v[136:139],a[12:15]
v_exp_f32 v206,v206
v_mfma_f32_16x16x32_bf16 a[16:19],a[224:227],v[136:139],a[16:19]
v_exp_f32 v207,v207
v_mfma_f32_16x16x32_bf16 a[16:19],a[228:231],v[140:143],a[16:19]
v_cvt_pk_bf16_f32 v202,v204,v205
v_cvt_pk_bf16_f32 v203,v206,v207
v_mfma_f32_16x16x32_bf16 a[20:23],a[236:239],v[140:143],a[20:23]
s_waitcnt lgkmcnt(0)
v_exp_f32 v216,v216
v_mfma_f32_16x16x32_bf16 a[20:23],a[232:235],v[136:139],a[20:23]
s_sub_u32 s79,s78,s76
v_exp_f32 v217,v217
v_mfma_f32_16x16x32_bf16 a[24:27],a[240:243],v[136:139],a[24:27]
s_add_u32 s78,s76,s60
v_exp_f32 v218,v218
v_mfma_f32_16x16x32_bf16 a[24:27],a[244:247],v[140:143],a[24:27]
s_cmp_gt_i32 s79,0
s_cbranch_scc1 L_ps4_%=
L_pr4_%=:
v_cvt_pk_bf16_f32 v204,v216,v217
v_mfma_f32_16x16x32_bf16 a[28:31],a[252:255],v[140:143],a[28:31]
v_exp_f32 v219,v219
v_mfma_f32_16x16x32_bf16 a[28:31],a[248:251],v[136:139],a[28:31]
v_exp_f32 v220,v220
v_smfmac_f32_16x16x64_bf16 v[48:51],v[28:31],v[136:143],v27
v_cvt_pk_bf16_f32 v205,v218,v219
v_mfma_f32_16x16x32_bf16 v[232:235],a[128:131],v[112:115],v[44:47]
s_waitcnt vmcnt(9)
v_exp_f32 v221,v221
v_mfma_f32_16x16x32_bf16 v[232:235],a[132:135],v[116:119],v[232:235]
s_barrier
s_add_u32 m0,32768,s51
ds_read_b128 a[128:131],v1
v_mfma_f32_16x16x32_bf16 v[232:235],a[136:139],v[120:123],v[232:235]
ds_read_b128 a[132:135],v1 offset:256
v_cvt_pk_bf16_f32 v206,v220,v221
v_mfma_f32_16x16x32_bf16 v[232:235],a[140:143],v[124:127],v[232:235]
ds_read_b128 a[136:139],v1 offset:512
v_max3_f32 v36,v36,v37,v38
v_mfma_f32_16x16x32_bf16 v[236:239],a[156:159],v[124:127],v[44:47]
buffer_load_dwordx4 v4,s[20:23],s47 offen nt lds
v_mfma_f32_16x16x32_bf16 v[236:239],a[152:155],v[120:123],v[236:239]
ds_read_b128 a[140:143],v1 offset:768
v_max3_f32 v36,v36,v39,s19
v_mfma_f32_16x16x32_bf16 v[236:239],a[148:151],v[116:119],v[236:239]
ds_read_b128 a[156:159],v1 offset:4864
v_sub_f32 v39,0,v36
v_mfma_f32_16x16x32_bf16 v[236:239],a[144:147],v[112:115],v[236:239]
ds_read_b128 a[152:155],v1 offset:4608
v_sub_f32 v38,0,v36
v_mfma_f32_16x16x32_bf16 v[248:251],a[160:163],v[112:115],v[44:47]
ds_read_b128 a[148:151],v1 offset:4352
v_sub_f32 v37,0,v36
v_mfma_f32_16x16x32_bf16 v[248:251],a[164:167],v[116:119],v[248:251]
ds_read_b128 a[144:147],v1 offset:4096
v_sub_f32 v36,0,v36
v_mfma_f32_16x16x32_bf16 v[248:251],a[168:171],v[120:123],v[248:251]
ds_read_b128 a[160:163],v1 offset:8192
v_max3_f32 v40,v40,v41,v42
v_mfma_f32_16x16x32_bf16 v[248:251],a[172:175],v[124:127],v[248:251]
ds_read_b128 a[164:167],v1 offset:8448
v_max3_f32 v40,v40,v43,s100
v_mfma_f32_16x16x32_bf16 v[252:255],a[188:191],v[124:127],v[44:47]
ds_read_b128 a[168:171],v1 offset:8704
v_sub_f32 v43,0,v40
v_mfma_f32_16x16x32_bf16 v[252:255],a[184:187],v[120:123],v[252:255]
ds_read_b128 a[172:175],v1 offset:8960
v_sub_f32 v42,0,v40
v_mfma_f32_16x16x32_bf16 v[252:255],a[180:183],v[116:119],v[252:255]
ds_read_b128 a[188:191],v1 offset:13056
v_sub_f32 v41,0,v40
v_mfma_f32_16x16x32_bf16 v[252:255],a[176:179],v[112:115],v[252:255]
ds_read_b128 a[184:187],v1 offset:12800
v_sub_f32 v40,0,v40
v_mfma_f32_16x16x32_bf16 v[44:47],v[80:83],v[148:151],0
ds_read_b128 a[180:183],v1 offset:12544
v_add_u32 v2,s48,v2
v_mfma_f32_16x16x32_bf16 v[44:47],v[84:87],v[180:183],v[44:47]
ds_read_b128 a[176:179],v1 offset:12288
v_add_u32 v3,s48,v3
v_mfma_f32_16x16x32_bf16 v[44:47],v[88:91],v[212:215],v[44:47]
s_mov_b32 s48,-98304
buffer_load_dwordx4 v4,s[20:23],s47 offen offset:1024 nt lds
v_mfma_f32_16x16x32_bf16 v[44:47],v[92:95],v[244:247],v[44:47]
v_exp_f32 v222,v222
v_mfma_f32_16x16x32_bf16 a[32:35],a[192:195],v[168:171],a[32:35]
v_exp_f32 v223,v223
v_mfma_f32_16x16x32_bf16 a[32:35],a[196:199],v[172:175],a[32:35]
v_cvt_pk_bf16_f32 v207,v222,v223
v_add_f32 v48,v49,v48
v_mfma_f32_16x16x32_bf16 a[36:39],a[204:207],v[172:175],a[36:39]
v_exp_f32 v232,v232
v_mfma_f32_16x16x32_bf16 a[36:39],a[200:203],v[168:171],a[36:39]
v_exp_f32 v233,v233
v_mfma_f32_16x16x32_bf16 a[40:43],a[208:211],v[168:171],a[40:43]
v_exp_f32 v234,v234
v_mfma_f32_16x16x32_bf16 a[40:43],a[212:215],v[172:175],a[40:43]
v_exp_f32 v235,v235
v_mfma_f32_16x16x32_bf16 a[44:47],a[220:223],v[172:175],a[44:47]
v_cvt_pk_bf16_f32 v232,v232,v233
v_cvt_pk_bf16_f32 v233,v234,v235
v_mfma_f32_16x16x32_bf16 a[44:47],a[216:219],v[168:171],a[44:47]
v_exp_f32 v236,v236
v_mfma_f32_16x16x32_bf16 a[48:51],a[224:227],v[168:171],a[48:51]
v_exp_f32 v237,v237
v_mfma_f32_16x16x32_bf16 a[48:51],a[228:231],v[172:175],a[48:51]
v_exp_f32 v238,v238
v_mfma_f32_16x16x32_bf16 a[52:55],a[236:239],v[172:175],a[52:55]
v_exp_f32 v239,v239
v_mfma_f32_16x16x32_bf16 a[52:55],a[232:235],v[168:171],a[52:55]
v_cvt_pk_bf16_f32 v234,v236,v237
v_cvt_pk_bf16_f32 v235,v238,v239
v_mfma_f32_16x16x32_bf16 a[56:59],a[240:243],v[168:171],a[56:59]
buffer_load_dwordx4 v5,s[20:23],s47 offen offset:2048 nt lds
v_mfma_f32_16x16x32_bf16 a[56:59],a[244:247],v[172:175],a[56:59]
v_exp_f32 v248,v248
v_mfma_f32_16x16x32_bf16 a[60:63],a[252:255],v[172:175],a[60:63]
v_exp_f32 v249,v249
v_mfma_f32_16x16x32_bf16 a[60:63],a[248:251],v[168:171],a[60:63]
v_exp_f32 v250,v250
v_smfmac_f32_16x16x64_bf16 v[52:55],v[28:31],v[168:175],v27
s_waitcnt lgkmcnt(0)
v_cvt_pk_bf16_f32 v236,v248,v249
v_mfma_f32_16x16x32_bf16 v[136:139],a[128:131],v[128:131],v[32:35]
v_exp_f32 v251,v251
v_mfma_f32_16x16x32_bf16 v[136:139],a[132:135],v[160:163],v[136:139]
v_exp_f32 v252,v252
v_mfma_f32_16x16x32_bf16 v[136:139],a[136:139],v[192:195],v[136:139]
v_exp_f32 v253,v253
v_mfma_f32_16x16x32_bf16 v[136:139],a[140:143],v[224:227],v[136:139]
v_exp_f32 v254,v254
v_mfma_f32_16x16x32_bf16 v[140:143],a[156:159],v[224:227],v[32:35]
v_exp_f32 v255,v255
v_mfma_f32_16x16x32_bf16 v[140:143],a[152:155],v[192:195],v[140:143]
v_cvt_pk_bf16_f32 v237,v250,v251
v_cvt_pk_bf16_f32 v238,v252,v253
v_mfma_f32_16x16x32_bf16 v[140:143],a[148:151],v[160:163],v[140:143]
v_cvt_pk_bf16_f32 v239,v254,v255
v_max_f32 v20,0x00800000,v48
v_mfma_f32_16x16x32_bf16 v[140:143],a[144:147],v[128:131],v[140:143]
v_rcp_f32 v20,v20
v_mfma_f32_16x16x32_bf16 v[152:155],a[160:163],v[128:131],v[32:35]
v_accvgpr_read_b32 v64,a0
v_accvgpr_read_b32 v65,a1
v_mfma_f32_16x16x32_bf16 v[152:155],a[164:167],v[160:163],v[152:155]
buffer_load_dwordx4 v5,s[20:23],s47 offen offset:3072 nt lds
v_mfma_f32_16x16x32_bf16 v[152:155],a[168:171],v[192:195],v[152:155]
v_exp_f32 v136,v136
v_mfma_f32_16x16x32_bf16 v[152:155],a[172:175],v[224:227],v[152:155]
v_exp_f32 v137,v137
v_mfma_f32_16x16x32_bf16 v[156:159],a[188:191],v[224:227],v[32:35]
v_exp_f32 v138,v138
v_mfma_f32_16x16x32_bf16 v[156:159],a[184:187],v[192:195],v[156:159]
v_exp_f32 v139,v139
v_mfma_f32_16x16x32_bf16 v[156:159],a[180:183],v[160:163],v[156:159]
v_cvt_pk_bf16_f32 v136,v136,v137
v_cvt_pk_bf16_f32 v137,v138,v139
v_mfma_f32_16x16x32_bf16 v[156:159],a[176:179],v[128:131],v[156:159]
v_exp_f32 v140,v140
v_mfma_f32_16x16x32_bf16 a[64:67],a[192:195],v[200:203],a[64:67]
v_exp_f32 v141,v141
v_mfma_f32_16x16x32_bf16 a[64:67],a[196:199],v[204:207],a[64:67]
v_exp_f32 v142,v142
v_mfma_f32_16x16x32_bf16 a[68:71],a[204:207],v[204:207],a[68:71]
v_exp_f32 v143,v143
v_mfma_f32_16x16x32_bf16 a[68:71],a[200:203],v[200:203],a[68:71]
v_cvt_pk_bf16_f32 v138,v140,v141
v_cvt_pk_bf16_f32 v139,v142,v143
v_mfma_f32_16x16x32_bf16 a[72:75],a[208:211],v[200:203],a[72:75]
s_add_u32 m0,32768,s52
v_exp_f32 v152,v152
v_mfma_f32_16x16x32_bf16 a[72:75],a[212:215],v[204:207],a[72:75]
v_exp_f32 v153,v153
v_mfma_f32_16x16x32_bf16 a[76:79],a[220:223],v[204:207],a[76:79]
v_exp_f32 v154,v154
v_mfma_f32_16x16x32_bf16 a[76:79],a[216:219],v[200:203],a[76:79]
buffer_load_dwordx4 v4,s[24:27],s47 offen nt lds
v_mfma_f32_16x16x32_bf16 a[80:83],a[224:227],v[200:203],a[80:83]
v_exp_f32 v155,v155
v_mfma_f32_16x16x32_bf16 a[80:83],a[228:231],v[204:207],a[80:83]
v_exp_f32 v156,v156
v_mfma_f32_16x16x32_bf16 a[84:87],a[236:239],v[204:207],a[84:87]
v_exp_f32 v157,v157
v_mfma_f32_16x16x32_bf16 a[84:87],a[232:235],v[200:203],a[84:87]
v_exp_f32 v158,v158
v_mfma_f32_16x16x32_bf16 a[88:91],a[240:243],v[200:203],a[88:91]
v_exp_f32 v159,v159
v_mfma_f32_16x16x32_bf16 a[88:91],a[244:247],v[204:207],a[88:91]
v_cvt_pk_bf16_f32 v140,v152,v153
v_cvt_pk_bf16_f32 v141,v154,v155
v_mfma_f32_16x16x32_bf16 a[92:95],a[252:255],v[204:207],a[92:95]
v_cvt_pk_bf16_f32 v142,v156,v157
v_cvt_pk_bf16_f32 v143,v158,v159
v_mfma_f32_16x16x32_bf16 a[92:95],a[248:251],v[200:203],a[92:95]
v_mul_f32 v64,v20,v64
v_mul_f32 v65,v20,v65
v_smfmac_f32_16x16x64_bf16 v[56:59],v[28:31],v[200:207],v27
v_accvgpr_read_b32 v66,a2
v_mfma_f32_16x16x32_bf16 v[168:171],a[128:131],v[132:135],v[36:39]
v_accvgpr_read_b32 v67,a3
v_cvt_pk_bf16_f32 v64,v64,v65
v_mfma_f32_16x16x32_bf16 v[168:171],a[132:135],v[164:167],v[168:171]
v_mul_f32 v66,v20,v66
v_mul_f32 v67,v20,v67
v_mfma_f32_16x16x32_bf16 v[168:171],a[136:139],v[196:199],v[168:171]
v_cvt_pk_bf16_f32 v65,v66,v67
v_accvgpr_read_b32 v66,a4
v_mfma_f32_16x16x32_bf16 v[168:171],a[140:143],v[228:231],v[168:171]
v_accvgpr_read_b32 v67,a5
v_accvgpr_read_b32 v12,a6
v_mfma_f32_16x16x32_bf16 v[172:175],a[156:159],v[228:231],v[36:39]
buffer_load_dwordx4 v5,s[24:27],s47 offen offset:1024 nt lds
v_mfma_f32_16x16x32_bf16 v[172:175],a[152:155],v[196:199],v[172:175]
v_accvgpr_read_b32 v13,a7
v_mul_f32 v66,v20,v66
v_mfma_f32_16x16x32_bf16 v[172:175],a[148:151],v[164:167],v[172:175]
v_mul_f32 v67,v20,v67
v_mul_f32 v12,v20,v12
v_mfma_f32_16x16x32_bf16 v[172:175],a[144:147],v[132:135],v[172:175]
v_mul_f32 v13,v20,v13
v_cvt_pk_bf16_f32 v66,v66,v67
v_mfma_f32_16x16x32_bf16 v[184:187],a[160:163],v[132:135],v[36:39]
v_cvt_pk_bf16_f32 v67,v12,v13
v_cmp_nlt_f32 s[56:57],v48,s63
v_mfma_f32_16x16x32_bf16 v[184:187],a[164:167],v[164:167],v[184:187]
v_accvgpr_read_b32 v68,a8
v_accvgpr_read_b32 v69,a9
v_accvgpr_read_b32 v70,a10
v_accvgpr_read_b32 v71,a11
v_accvgpr_read_b32 v12,a12
v_accvgpr_read_b32 v13,a13
v_accvgpr_read_b32 v14,a14
v_accvgpr_read_b32 v15,a15
v_pk_mul_f32 v[68:69],v[20:21],v[68:69] op_sel:[0,0] op_sel_hi:[0,1]
v_pk_mul_f32 v[70:71],v[20:21],v[70:71] op_sel:[0,0] op_sel_hi:[0,1]
v_pk_mul_f32 v[12:13],v[20:21],v[12:13] op_sel:[0,0] op_sel_hi:[0,1]
v_pk_mul_f32 v[14:15],v[20:21],v[14:15] op_sel:[0,0] op_sel_hi:[0,1]
v_cvt_pk_bf16_f32 v68,v68,v69
v_cvt_pk_bf16_f32 v69,v70,v71
v_cvt_pk_bf16_f32 v70,v12,v13
v_cvt_pk_bf16_f32 v71,v14,v15
v_mfma_f32_16x16x32_bf16 v[184:187],a[168:171],v[196:199],v[184:187]
v_accvgpr_read_b32 v72,a16
v_accvgpr_read_b32 v73,a17
v_accvgpr_read_b32 v74,a18
v_accvgpr_read_b32 v75,a19
v_accvgpr_read_b32 v12,a20
v_accvgpr_read_b32 v13,a21
v_accvgpr_read_b32 v14,a22
v_accvgpr_read_b32 v15,a23
)A"
R"A(v_pk_mul_f32 v[72:73],v[20:21],v[72:73] op_sel:[0,0] op_sel_hi:[0,1]
v_pk_mul_f32 v[74:75],v[20:21],v[74:75] op_sel:[0,0] op_sel_hi:[0,1]
v_pk_mul_f32 v[12:13],v[20:21],v[12:13] op_sel:[0,0] op_sel_hi:[0,1]
v_pk_mul_f32 v[14:15],v[20:21],v[14:15] op_sel:[0,0] op_sel_hi:[0,1]
v_cvt_pk_bf16_f32 v72,v72,v73
v_cvt_pk_bf16_f32 v73,v74,v75
v_cvt_pk_bf16_f32 v74,v12,v13
v_cvt_pk_bf16_f32 v75,v14,v15
v_mfma_f32_16x16x32_bf16 v[184:187],a[172:175],v[228:231],v[184:187]
v_accvgpr_read_b32 v76,a24
v_accvgpr_read_b32 v77,a25
v_accvgpr_read_b32 v78,a26
v_accvgpr_read_b32 v79,a27
v_accvgpr_read_b32 v12,a28
v_accvgpr_read_b32 v13,a29
v_accvgpr_read_b32 v14,a30
v_accvgpr_read_b32 v15,a31
v_pk_mul_f32 v[76:77],v[20:21],v[76:77] op_sel:[0,0] op_sel_hi:[0,1]
v_pk_mul_f32 v[78:79],v[20:21],v[78:79] op_sel:[0,0] op_sel_hi:[0,1]
v_pk_mul_f32 v[12:13],v[20:21],v[12:13] op_sel:[0,0] op_sel_hi:[0,1]
v_pk_mul_f32 v[14:15],v[20:21],v[14:15] op_sel:[0,0] op_sel_hi:[0,1]
v_cvt_pk_bf16_f32 v76,v76,v77
v_cvt_pk_bf16_f32 v77,v78,v79
v_cvt_pk_bf16_f32 v78,v12,v13
v_cvt_pk_bf16_f32 v79,v14,v15
v_mfma_f32_16x16x32_bf16 v[188:191],a[188:191],v[228:231],v[36:39]
v_max3_f32 v44,v44,v45,v46
v_max3_f32 v44,v44,v47,s101
v_mfma_f32_16x16x32_bf16 v[188:191],a[184:187],v[196:199],v[188:191]
v_mov_b32 v48,0
v_mov_b32 v49,0
v_mfma_f32_16x16x32_bf16 v[188:191],a[180:183],v[164:167],v[188:191]
v_mov_b32 v50,0
v_mov_b32 v51,0
v_mfma_f32_16x16x32_bf16 v[188:191],a[176:179],v[132:135],v[188:191]
v_sub_f32 v47,0,v44
v_sub_f32 v46,0,v44
v_mfma_f32_16x16x32_bf16 a[96:99],a[192:195],v[232:235],a[96:99]
ds_read_b64_tr_b16 a[192:193],v2
ds_read_b64_tr_b16 a[194:195],v2 offset:4096
v_mfma_f32_16x16x32_bf16 a[96:99],a[196:199],v[236:239],a[96:99]
ds_read_b64_tr_b16 a[196:197],v2 offset:8192
ds_read_b64_tr_b16 a[198:199],v2 offset:12288
v_mfma_f32_16x16x32_bf16 a[100:103],a[204:207],v[236:239],a[100:103]
buffer_load_dwordx4 v4,s[24:27],s47 offen offset:2048 nt lds
v_mfma_f32_16x16x32_bf16 a[100:103],a[200:203],v[232:235],a[100:103]
ds_read_b64_tr_b16 a[204:205],v3 offset:8192
ds_read_b64_tr_b16 a[206:207],v3 offset:12288
v_mfma_f32_16x16x32_bf16 a[104:107],a[208:211],v[232:235],a[104:107]
ds_read_b64_tr_b16 a[200:201],v3
ds_read_b64_tr_b16 a[202:203],v3 offset:4096
v_mfma_f32_16x16x32_bf16 a[104:107],a[212:215],v[236:239],a[104:107]
ds_read_b64_tr_b16 a[208:209],v2 offset:256
ds_read_b64_tr_b16 a[210:211],v2 offset:4352
v_mfma_f32_16x16x32_bf16 a[108:111],a[220:223],v[236:239],a[108:111]
ds_read_b64_tr_b16 a[212:213],v2 offset:8448
ds_read_b64_tr_b16 a[214:215],v2 offset:12544
v_mfma_f32_16x16x32_bf16 a[108:111],a[216:219],v[232:235],a[108:111]
ds_read_b64_tr_b16 a[220:221],v3 offset:8448
ds_read_b64_tr_b16 a[222:223],v3 offset:12544
v_mfma_f32_16x16x32_bf16 a[112:115],a[224:227],v[232:235],a[112:115]
ds_read_b64_tr_b16 a[216:217],v3 offset:256
ds_read_b64_tr_b16 a[218:219],v3 offset:4352
v_mfma_f32_16x16x32_bf16 a[112:115],a[228:231],v[236:239],a[112:115]
ds_read_b64_tr_b16 a[224:225],v2 offset:512
ds_read_b64_tr_b16 a[226:227],v2 offset:4608
v_mfma_f32_16x16x32_bf16 a[116:119],a[236:239],v[236:239],a[116:119]
ds_read_b64_tr_b16 a[228:229],v2 offset:8704
ds_read_b64_tr_b16 a[230:231],v2 offset:12800
v_mfma_f32_16x16x32_bf16 a[116:119],a[232:235],v[232:235],a[116:119]
ds_read_b64_tr_b16 a[236:237],v3 offset:8704
ds_read_b64_tr_b16 a[238:239],v3 offset:12800
v_mfma_f32_16x16x32_bf16 a[120:123],a[240:243],v[232:235],a[120:123]
ds_read_b64_tr_b16 a[232:233],v3 offset:512
ds_read_b64_tr_b16 a[234:235],v3 offset:4608
v_mfma_f32_16x16x32_bf16 a[120:123],a[244:247],v[236:239],a[120:123]
ds_read_b64_tr_b16 a[240:241],v2 offset:768
ds_read_b64_tr_b16 a[242:243],v2 offset:4864
v_mfma_f32_16x16x32_bf16 a[124:127],a[252:255],v[236:239],a[124:127]
ds_read_b64_tr_b16 a[244:245],v2 offset:8960
ds_read_b64_tr_b16 a[246:247],v2 offset:13056
v_mfma_f32_16x16x32_bf16 a[124:127],a[248:251],v[232:235],a[124:127]
ds_read_b64_tr_b16 a[252:253],v3 offset:8960
ds_read_b64_tr_b16 a[254:255],v3 offset:13056
v_smfmac_f32_16x16x64_bf16 v[60:63],v[28:31],v[232:239],v27
ds_read_b64_tr_b16 a[248:249],v3 offset:768
v_mfma_f32_16x16x32_bf16 v[200:203],a[128:131],v[144:147],v[40:43]
buffer_load_dwordx4 v5,s[24:27],s47 offen offset:3072 nt lds
v_mfma_f32_16x16x32_bf16 v[200:203],a[132:135],v[176:179],v[200:203]
s_add_u32 s47,s47,16384
ds_read_b64_tr_b16 a[250:251],v3 offset:4864
v_sub_f32 v45,0,v44
v_mfma_f32_16x16x32_bf16 v[200:203],a[136:139],v[208:211],v[200:203]
s_and_b32 s47,s47,0x3ffff
s_or_b32 s47,s47,s46
v_sub_f32 v44,0,v44
v_mfma_f32_16x16x32_bf16 v[200:203],a[140:143],v[240:243],v[200:203]
s_and_b32 s46,s54,0xfffc0000
v_exp_f32 v168,v168
v_mfma_f32_16x16x32_bf16 v[204:207],a[156:159],v[240:243],v[40:43]
v_exp_f32 v169,v169
v_mfma_f32_16x16x32_bf16 v[204:207],a[152:155],v[208:211],v[204:207]
v_exp_f32 v170,v170
v_mfma_f32_16x16x32_bf16 v[204:207],a[148:151],v[176:179],v[204:207]
v_exp_f32 v171,v171
v_mfma_f32_16x16x32_bf16 v[204:207],a[144:147],v[144:147],v[204:207]
v_cvt_pk_bf16_f32 v168,v168,v169
v_add_f32 v52,v53,v52
v_mfma_f32_16x16x32_bf16 v[216:219],a[160:163],v[144:147],v[40:43]
v_exp_f32 v172,v172
v_mfma_f32_16x16x32_bf16 v[216:219],a[164:167],v[176:179],v[216:219]
v_exp_f32 v173,v173
v_mfma_f32_16x16x32_bf16 v[216:219],a[168:171],v[208:211],v[216:219]
v_exp_f32 v174,v174
v_mfma_f32_16x16x32_bf16 v[216:219],a[172:175],v[240:243],v[216:219]
v_exp_f32 v175,v175
v_mfma_f32_16x16x32_bf16 v[220:223],a[188:191],v[240:243],v[40:43]
v_cvt_pk_bf16_f32 v169,v170,v171
v_max_f32 v21,0x00800000,v52
v_mfma_f32_16x16x32_bf16 v[220:223],a[184:187],v[208:211],v[220:223]
v_cvt_pk_bf16_f32 v170,v172,v173
v_cvt_pk_bf16_f32 v171,v174,v175
v_mfma_f32_16x16x32_bf16 v[220:223],a[180:183],v[176:179],v[220:223]
v_exp_f32 v184,v184
v_mfma_f32_16x16x32_bf16 v[220:223],a[176:179],v[144:147],v[220:223]
s_waitcnt lgkmcnt(0)
v_exp_f32 v185,v185
v_mfma_f32_16x16x32_bf16 a[0:3],a[192:195],v[136:139],0
v_exp_f32 v186,v186
v_mfma_f32_16x16x32_bf16 a[0:3],a[196:199],v[140:143],a[0:3]
v_exp_f32 v187,v187
v_mfma_f32_16x16x32_bf16 a[4:7],a[204:207],v[140:143],0
v_exp_f32 v188,v188
v_mfma_f32_16x16x32_bf16 a[4:7],a[200:203],v[136:139],a[4:7]
s_memrealtime s[76:77]
v_exp_f32 v189,v189
v_mfma_f32_16x16x32_bf16 a[8:11],a[208:211],v[136:139],0
v_exp_f32 v190,v190
v_mfma_f32_16x16x32_bf16 a[8:11],a[212:215],v[140:143],a[8:11]
v_exp_f32 v191,v191
v_mfma_f32_16x16x32_bf16 a[12:15],a[220:223],v[140:143],0
v_rcp_f32 v21,v21
v_mfma_f32_16x16x32_bf16 a[12:15],a[216:219],v[136:139],a[12:15]
v_cvt_pk_bf16_f32 v172,v184,v185
v_cvt_pk_bf16_f32 v173,v186,v187
v_mfma_f32_16x16x32_bf16 a[16:19],a[224:227],v[136:139],0
v_cvt_pk_bf16_f32 v174,v188,v189
v_cvt_pk_bf16_f32 v175,v190,v191
v_mfma_f32_16x16x32_bf16 a[16:19],a[228:231],v[140:143],a[16:19]
v_accvgpr_read_b32 v80,a32
v_accvgpr_read_b32 v81,a33
v_mfma_f32_16x16x32_bf16 a[20:23],a[236:239],v[140:143],0
s_waitcnt lgkmcnt(0)
v_mul_f32 v80,v21,v80
v_mul_f32 v81,v21,v81
v_mfma_f32_16x16x32_bf16 a[20:23],a[232:235],v[136:139],a[20:23]
s_sub_u32 s79,s78,s76
v_accvgpr_read_b32 v82,a34
v_accvgpr_read_b32 v83,a35
v_mfma_f32_16x16x32_bf16 a[24:27],a[240:243],v[136:139],0
s_add_u32 s78,s76,s60
v_cvt_pk_bf16_f32 v80,v80,v81
v_mul_f32 v82,v21,v82
v_mfma_f32_16x16x32_bf16 a[24:27],a[244:247],v[140:143],a[24:27]
s_cmp_gt_i32 s79,0
s_cbranch_scc1 L_ps5_%=
L_pr5_%=:
v_mul_f32 v83,v21,v83
v_mfma_f32_16x16x32_bf16 a[28:31],a[252:255],v[140:143],0
v_cvt_pk_bf16_f32 v81,v82,v83
v_accvgpr_read_b32 v82,a36
v_mfma_f32_16x16x32_bf16 a[28:31],a[248:251],v[136:139],a[28:31]
v_accvgpr_read_b32 v83,a37
v_accvgpr_read_b32 v14,a38
v_smfmac_f32_16x16x64_bf16 v[48:51],v[28:31],v[136:143],v27
v_accvgpr_read_b32 v15,a39
v_mfma_f32_16x16x32_bf16 v[232:235],a[128:131],v[148:151],v[44:47]
s_waitcnt vmcnt(8)
v_mul_f32 v82,v21,v82
v_mul_f32 v83,v21,v83
v_mfma_f32_16x16x32_bf16 v[232:235],a[132:135],v[180:183],v[232:235]
s_barrier
s_add_u32 m0,65536,s51
v_mul_f32 v14,v21,v14
v_mfma_f32_16x16x32_bf16 v[232:235],a[136:139],v[212:215],v[232:235]
v_mul_f32 v15,v21,v15
v_cvt_pk_bf16_f32 v82,v82,v83
v_mfma_f32_16x16x32_bf16 v[232:235],a[140:143],v[244:247],v[232:235]
v_cvt_pk_bf16_f32 v83,v14,v15
v_add_u32 v1,s48,v1
v_mfma_f32_16x16x32_bf16 v[236:239],a[156:159],v[244:247],v[44:47]
buffer_load_dwordx4 v4,s[20:23],s47 offen nt lds
v_mfma_f32_16x16x32_bf16 v[236:239],a[152:155],v[212:215],v[236:239]
v_accvgpr_read_b32 v84,a40
v_accvgpr_read_b32 v85,a41
v_accvgpr_read_b32 v86,a42
v_accvgpr_read_b32 v87,a43
v_accvgpr_read_b32 v12,a44
v_accvgpr_read_b32 v13,a45
v_accvgpr_read_b32 v14,a46
v_accvgpr_read_b32 v15,a47
v_pk_mul_f32 v[84:85],v[20:21],v[84:85] op_sel:[1,0] op_sel_hi:[1,1]
v_pk_mul_f32 v[86:87],v[20:21],v[86:87] op_sel:[1,0] op_sel_hi:[1,1]
v_pk_mul_f32 v[12:13],v[20:21],v[12:13] op_sel:[1,0] op_sel_hi:[1,1]
v_pk_mul_f32 v[14:15],v[20:21],v[14:15] op_sel:[1,0] op_sel_hi:[1,1]
v_cvt_pk_bf16_f32 v84,v84,v85
v_cvt_pk_bf16_f32 v85,v86,v87
v_cvt_pk_bf16_f32 v86,v12,v13
v_cvt_pk_bf16_f32 v87,v14,v15
v_mfma_f32_16x16x32_bf16 v[236:239],a[148:151],v[180:183],v[236:239]
v_accvgpr_read_b32 v88,a48
v_accvgpr_read_b32 v89,a49
v_accvgpr_read_b32 v90,a50
v_accvgpr_read_b32 v91,a51
v_accvgpr_read_b32 v12,a52
v_accvgpr_read_b32 v13,a53
v_accvgpr_read_b32 v14,a54
v_accvgpr_read_b32 v15,a55
v_pk_mul_f32 v[88:89],v[20:21],v[88:89] op_sel:[1,0] op_sel_hi:[1,1]
v_pk_mul_f32 v[90:91],v[20:21],v[90:91] op_sel:[1,0] op_sel_hi:[1,1]
v_pk_mul_f32 v[12:13],v[20:21],v[12:13] op_sel:[1,0] op_sel_hi:[1,1]
v_pk_mul_f32 v[14:15],v[20:21],v[14:15] op_sel:[1,0] op_sel_hi:[1,1]
v_cvt_pk_bf16_f32 v88,v88,v89
v_cvt_pk_bf16_f32 v89,v90,v91
v_cvt_pk_bf16_f32 v90,v12,v13
v_cvt_pk_bf16_f32 v91,v14,v15
v_mfma_f32_16x16x32_bf16 v[236:239],a[144:147],v[148:151],v[236:239]
ds_read_b128 a[128:131],v1
v_mov_b32 v53,0
v_mfma_f32_16x16x32_bf16 v[248:251],a[160:163],v[148:151],v[44:47]
ds_read_b128 a[132:135],v1 offset:256
v_mov_b32 v54,0
v_mfma_f32_16x16x32_bf16 v[248:251],a[164:167],v[180:183],v[248:251]
ds_read_b128 a[136:139],v1 offset:512
v_mov_b32 v55,0
v_mfma_f32_16x16x32_bf16 v[248:251],a[168:171],v[212:215],v[248:251]
v_accvgpr_read_b32 v92,a56
v_accvgpr_read_b32 v93,a57
v_accvgpr_read_b32 v94,a58
v_accvgpr_read_b32 v95,a59
v_accvgpr_read_b32 v12,a60
v_accvgpr_read_b32 v13,a61
v_accvgpr_read_b32 v14,a62
v_accvgpr_read_b32 v15,a63
v_pk_mul_f32 v[92:93],v[20:21],v[92:93] op_sel:[1,0] op_sel_hi:[1,1]
v_pk_mul_f32 v[94:95],v[20:21],v[94:95] op_sel:[1,0] op_sel_hi:[1,1]
v_pk_mul_f32 v[12:13],v[20:21],v[12:13] op_sel:[1,0] op_sel_hi:[1,1]
v_pk_mul_f32 v[14:15],v[20:21],v[14:15] op_sel:[1,0] op_sel_hi:[1,1]
v_cvt_pk_bf16_f32 v92,v92,v93
v_cvt_pk_bf16_f32 v93,v94,v95
v_cvt_pk_bf16_f32 v94,v12,v13
v_cvt_pk_bf16_f32 v95,v14,v15
v_mfma_f32_16x16x32_bf16 v[248:251],a[172:175],v[244:247],v[248:251]
ds_read_b128 a[140:143],v1 offset:768
v_add_f32 v56,v57,v56
v_mfma_f32_16x16x32_bf16 v[252:255],a[188:191],v[244:247],v[44:47]
ds_read_b128 a[156:159],v1 offset:4864
v_max_f32 v22,0x00800000,v56
v_mfma_f32_16x16x32_bf16 v[252:255],a[184:187],v[212:215],v[252:255]
v_cmp_nlt_f32 s[58:59],v52,s63
s_nop 1
s_or_b64 s[56:57],s[56:57],s[58:59]
v_mfma_f32_16x16x32_bf16 v[252:255],a[180:183],v[180:183],v[252:255]
ds_read_b128 a[152:155],v1 offset:4608
v_mov_b32 v52,0
v_mfma_f32_16x16x32_bf16 v[252:255],a[176:179],v[148:151],v[252:255]
ds_read_b128 a[148:151],v1 offset:4352
v_accvgpr_read_b32 v96,a64
v_mfma_f32_16x16x32_bf16 a[32:35],a[192:195],v[168:171],0
ds_read_b128 a[144:147],v1 offset:4096
v_accvgpr_read_b32 v97,a65
v_mfma_f32_16x16x32_bf16 a[32:35],a[196:199],v[172:175],a[32:35]
ds_read_b128 a[160:163],v1 offset:8192
v_accvgpr_read_b32 v98,a66
v_mfma_f32_16x16x32_bf16 a[36:39],a[204:207],v[172:175],0
buffer_load_dwordx4 v4,s[20:23],s47 offen offset:1024 nt lds
v_mfma_f32_16x16x32_bf16 a[36:39],a[200:203],v[168:171],a[36:39]
ds_read_b128 a[164:167],v1 offset:8448
v_accvgpr_read_b32 v99,a67
v_mfma_f32_16x16x32_bf16 a[40:43],a[208:211],v[168:171],0
ds_read_b128 a[168:171],v1 offset:8704
v_accvgpr_read_b32 v16,a70
v_mfma_f32_16x16x32_bf16 a[40:43],a[212:215],v[172:175],a[40:43]
)A"
R"A(ds_read_b128 a[172:175],v1 offset:8960
v_accvgpr_read_b32 v17,a71
v_mfma_f32_16x16x32_bf16 a[44:47],a[220:223],v[172:175],0
ds_read_b128 a[188:191],v1 offset:13056
v_mov_b32 v57,0
v_mfma_f32_16x16x32_bf16 a[44:47],a[216:219],v[168:171],a[44:47]
ds_read_b128 a[184:187],v1 offset:12800
v_mov_b32 v58,0
v_mfma_f32_16x16x32_bf16 a[48:51],a[224:227],v[168:171],0
ds_read_b128 a[180:183],v1 offset:12544
v_mov_b32 v59,0
v_mfma_f32_16x16x32_bf16 a[48:51],a[228:231],v[172:175],a[48:51]
ds_read_b128 a[176:179],v1 offset:12288
v_add_f32 v60,v61,v60
v_mfma_f32_16x16x32_bf16 a[52:55],a[236:239],v[172:175],0
v_exp_f32 v200,v200
v_mfma_f32_16x16x32_bf16 a[52:55],a[232:235],v[168:171],a[52:55]
v_exp_f32 v201,v201
v_mfma_f32_16x16x32_bf16 a[56:59],a[240:243],v[168:171],0
v_exp_f32 v202,v202
v_mfma_f32_16x16x32_bf16 a[56:59],a[244:247],v[172:175],a[56:59]
v_exp_f32 v203,v203
v_mfma_f32_16x16x32_bf16 a[60:63],a[252:255],v[172:175],0
v_cvt_pk_bf16_f32 v200,v200,v201
v_cvt_pk_bf16_f32 v201,v202,v203
v_mfma_f32_16x16x32_bf16 a[60:63],a[248:251],v[168:171],a[60:63]
v_exp_f32 v204,v204
v_smfmac_f32_16x16x64_bf16 v[52:55],v[28:31],v[168:175],v27
s_waitcnt lgkmcnt(1)
v_max_f32 v23,0x00800000,v60
v_mfma_f32_16x16x32_bf16 v[136:139],a[128:131],v[128:131],v[32:35]
buffer_load_dwordx4 v5,s[20:23],s47 offen offset:2048 nt lds
v_mfma_f32_16x16x32_bf16 v[136:139],a[132:135],v[160:163],v[136:139]
v_exp_f32 v205,v205
v_mfma_f32_16x16x32_bf16 v[136:139],a[136:139],v[192:195],v[136:139]
v_exp_f32 v206,v206
v_mfma_f32_16x16x32_bf16 v[136:139],a[140:143],v[224:227],v[136:139]
v_exp_f32 v207,v207
v_mfma_f32_16x16x32_bf16 v[140:143],a[156:159],v[224:227],v[32:35]
v_cvt_pk_bf16_f32 v202,v204,v205
v_cvt_pk_bf16_f32 v203,v206,v207
v_mfma_f32_16x16x32_bf16 v[140:143],a[152:155],v[192:195],v[140:143]
v_exp_f32 v216,v216
v_mfma_f32_16x16x32_bf16 v[140:143],a[148:151],v[160:163],v[140:143]
v_exp_f32 v217,v217
v_mfma_f32_16x16x32_bf16 v[140:143],a[144:147],v[128:131],v[140:143]
v_exp_f32 v218,v218
v_mfma_f32_16x16x32_bf16 v[152:155],a[160:163],v[128:131],v[32:35]
v_exp_f32 v219,v219
v_mfma_f32_16x16x32_bf16 v[152:155],a[164:167],v[160:163],v[152:155]
v_exp_f32 v220,v220
v_mfma_f32_16x16x32_bf16 v[152:155],a[168:171],v[192:195],v[152:155]
v_exp_f32 v221,v221
v_mfma_f32_16x16x32_bf16 v[152:155],a[172:175],v[224:227],v[152:155]
v_exp_f32 v222,v222
v_mfma_f32_16x16x32_bf16 v[156:159],a[188:191],v[224:227],v[32:35]
v_exp_f32 v223,v223
v_mfma_f32_16x16x32_bf16 v[156:159],a[184:187],v[192:195],v[156:159]
v_cvt_pk_bf16_f32 v204,v216,v217
v_cvt_pk_bf16_f32 v205,v218,v219
v_mfma_f32_16x16x32_bf16 v[156:159],a[180:183],v[160:163],v[156:159]
s_waitcnt lgkmcnt(0)
v_cvt_pk_bf16_f32 v206,v220,v221
v_cvt_pk_bf16_f32 v207,v222,v223
v_mfma_f32_16x16x32_bf16 v[156:159],a[176:179],v[128:131],v[156:159]
v_rcp_f32 v22,v22
v_mfma_f32_16x16x32_bf16 a[64:67],a[192:195],v[200:203],0
buffer_load_dwordx4 v5,s[20:23],s47 offen offset:3072 nt lds
v_mul_f32 v96,v22,v96
v_mul_f32 v97,v22,v97
v_cvt_pk_bf16_f32 v96,v96,v97
v_mul_f32 v98,v22,v98
v_mul_f32 v99,v22,v99
v_mfma_f32_16x16x32_bf16 a[64:67],a[196:199],v[204:207],a[64:67]
v_cvt_pk_bf16_f32 v97,v98,v99
v_accvgpr_read_b32 v98,a68
v_accvgpr_read_b32 v99,a69
v_mfma_f32_16x16x32_bf16 a[68:71],a[204:207],v[204:207],0
v_accvgpr_read_b32 v100,a72
v_accvgpr_read_b32 v101,a73
v_accvgpr_read_b32 v102,a74
v_accvgpr_read_b32 v103,a75
v_accvgpr_read_b32 v12,a76
v_accvgpr_read_b32 v13,a77
v_accvgpr_read_b32 v14,a78
v_accvgpr_read_b32 v15,a79
v_pk_mul_f32 v[100:101],v[22:23],v[100:101] op_sel:[0,0] op_sel_hi:[0,1]
v_pk_mul_f32 v[102:103],v[22:23],v[102:103] op_sel:[0,0] op_sel_hi:[0,1]
v_pk_mul_f32 v[12:13],v[22:23],v[12:13] op_sel:[0,0] op_sel_hi:[0,1]
v_pk_mul_f32 v[14:15],v[22:23],v[14:15] op_sel:[0,0] op_sel_hi:[0,1]
v_cvt_pk_bf16_f32 v100,v100,v101
v_cvt_pk_bf16_f32 v101,v102,v103
v_cvt_pk_bf16_f32 v102,v12,v13
v_cvt_pk_bf16_f32 v103,v14,v15
v_mfma_f32_16x16x32_bf16 a[68:71],a[200:203],v[200:203],a[68:71]
v_accvgpr_read_b32 v104,a80
v_accvgpr_read_b32 v105,a81
v_accvgpr_read_b32 v106,a82
v_accvgpr_read_b32 v107,a83
v_accvgpr_read_b32 v12,a84
v_accvgpr_read_b32 v13,a85
v_accvgpr_read_b32 v14,a86
v_accvgpr_read_b32 v15,a87
v_pk_mul_f32 v[104:105],v[22:23],v[104:105] op_sel:[0,0] op_sel_hi:[0,1]
v_pk_mul_f32 v[106:107],v[22:23],v[106:107] op_sel:[0,0] op_sel_hi:[0,1]
v_pk_mul_f32 v[12:13],v[22:23],v[12:13] op_sel:[0,0] op_sel_hi:[0,1]
v_pk_mul_f32 v[14:15],v[22:23],v[14:15] op_sel:[0,0] op_sel_hi:[0,1]
v_cvt_pk_bf16_f32 v104,v104,v105
v_cvt_pk_bf16_f32 v105,v106,v107
v_cvt_pk_bf16_f32 v106,v12,v13
v_cvt_pk_bf16_f32 v107,v14,v15
v_mfma_f32_16x16x32_bf16 a[72:75],a[208:211],v[200:203],0
v_accvgpr_read_b32 v108,a88
v_accvgpr_read_b32 v109,a89
v_accvgpr_read_b32 v110,a90
v_accvgpr_read_b32 v111,a91
v_accvgpr_read_b32 v12,a92
v_accvgpr_read_b32 v13,a93
v_accvgpr_read_b32 v14,a94
v_accvgpr_read_b32 v15,a95
v_pk_mul_f32 v[108:109],v[22:23],v[108:109] op_sel:[0,0] op_sel_hi:[0,1]
v_pk_mul_f32 v[110:111],v[22:23],v[110:111] op_sel:[0,0] op_sel_hi:[0,1]
v_pk_mul_f32 v[12:13],v[22:23],v[12:13] op_sel:[0,0] op_sel_hi:[0,1]
v_pk_mul_f32 v[14:15],v[22:23],v[14:15] op_sel:[0,0] op_sel_hi:[0,1]
v_cvt_pk_bf16_f32 v108,v108,v109
v_cvt_pk_bf16_f32 v109,v110,v111
v_cvt_pk_bf16_f32 v110,v12,v13
v_cvt_pk_bf16_f32 v111,v14,v15
v_mfma_f32_16x16x32_bf16 a[72:75],a[212:215],v[204:207],a[72:75]
v_cmp_nlt_f32 s[58:59],v56,s63
s_nop 1
s_or_b64 s[56:57],s[56:57],s[58:59]
v_mfma_f32_16x16x32_bf16 a[76:79],a[220:223],v[204:207],0
v_mov_b32 v56,0
v_accvgpr_read_b32 v112,a96
v_mfma_f32_16x16x32_bf16 a[76:79],a[216:219],v[200:203],a[76:79]
v_exp_f32 v232,v232
v_mfma_f32_16x16x32_bf16 a[80:83],a[224:227],v[200:203],0
s_add_u32 m0,65536,s52
v_exp_f32 v233,v233
v_mfma_f32_16x16x32_bf16 a[80:83],a[228:231],v[204:207],a[80:83]
v_exp_f32 v234,v234
v_mfma_f32_16x16x32_bf16 a[84:87],a[236:239],v[204:207],0
v_exp_f32 v235,v235
v_mfma_f32_16x16x32_bf16 a[84:87],a[232:235],v[200:203],a[84:87]
buffer_load_dwordx4 v4,s[24:27],s47 offen nt lds
v_mfma_f32_16x16x32_bf16 a[88:91],a[240:243],v[200:203],0
v_cvt_pk_bf16_f32 v232,v232,v233
v_cvt_pk_bf16_f32 v233,v234,v235
v_mfma_f32_16x16x32_bf16 a[88:91],a[244:247],v[204:207],a[88:91]
v_exp_f32 v236,v236
v_mfma_f32_16x16x32_bf16 a[92:95],a[252:255],v[204:207],0
v_exp_f32 v237,v237
v_mfma_f32_16x16x32_bf16 a[92:95],a[248:251],v[200:203],a[92:95]
v_exp_f32 v238,v238
v_smfmac_f32_16x16x64_bf16 v[56:59],v[28:31],v[200:207],v27
v_cvt_pk_bf16_f32 v234,v236,v237
v_mfma_f32_16x16x32_bf16 v[168:171],a[128:131],v[132:135],v[36:39]
v_exp_f32 v239,v239
v_mfma_f32_16x16x32_bf16 v[168:171],a[132:135],v[164:167],v[168:171]
v_cvt_pk_bf16_f32 v235,v238,v239
v_accvgpr_read_b32 v113,a97
v_mfma_f32_16x16x32_bf16 v[168:171],a[136:139],v[196:199],v[168:171]
v_exp_f32 v248,v248
v_mfma_f32_16x16x32_bf16 v[168:171],a[140:143],v[228:231],v[168:171]
v_exp_f32 v249,v249
v_mfma_f32_16x16x32_bf16 v[172:175],a[156:159],v[228:231],v[36:39]
v_exp_f32 v250,v250
v_mfma_f32_16x16x32_bf16 v[172:175],a[152:155],v[196:199],v[172:175]
v_exp_f32 v251,v251
v_mfma_f32_16x16x32_bf16 v[172:175],a[148:151],v[164:167],v[172:175]
v_exp_f32 v252,v252
v_mfma_f32_16x16x32_bf16 v[172:175],a[144:147],v[132:135],v[172:175]
v_exp_f32 v253,v253
v_mfma_f32_16x16x32_bf16 v[184:187],a[160:163],v[132:135],v[36:39]
buffer_load_dwordx4 v5,s[24:27],s47 offen offset:1024 nt lds
v_mfma_f32_16x16x32_bf16 v[184:187],a[164:167],v[164:167],v[184:187]
v_exp_f32 v254,v254
v_mfma_f32_16x16x32_bf16 v[184:187],a[168:171],v[196:199],v[184:187]
v_exp_f32 v255,v255
v_mfma_f32_16x16x32_bf16 v[184:187],a[172:175],v[228:231],v[184:187]
v_rcp_f32 v23,v23
v_mfma_f32_16x16x32_bf16 v[188:191],a[188:191],v[228:231],v[36:39]
v_cvt_pk_bf16_f32 v236,v248,v249
v_cvt_pk_bf16_f32 v237,v250,v251
v_mfma_f32_16x16x32_bf16 v[188:191],a[184:187],v[196:199],v[188:191]
v_cvt_pk_bf16_f32 v238,v252,v253
v_cvt_pk_bf16_f32 v239,v254,v255
v_mfma_f32_16x16x32_bf16 v[188:191],a[180:183],v[164:167],v[188:191]
v_mul_f32 v112,v23,v112
v_mul_f32 v113,v23,v113
v_mfma_f32_16x16x32_bf16 v[188:191],a[176:179],v[132:135],v[188:191]
v_accvgpr_read_b32 v114,a98
v_accvgpr_read_b32 v115,a99
v_mfma_f32_16x16x32_bf16 a[96:99],a[192:195],v[232:235],0
v_cvt_pk_bf16_f32 v112,v112,v113
v_mul_f32 v114,v23,v114
v_mul_f32 v115,v23,v115
v_cvt_pk_bf16_f32 v113,v114,v115
v_mfma_f32_16x16x32_bf16 a[96:99],a[196:199],v[236:239],a[96:99]
v_accvgpr_read_b32 v114,a100
v_accvgpr_read_b32 v115,a101
v_accvgpr_read_b32 v12,a102
v_accvgpr_read_b32 v13,a103
v_mul_f32 v114,v23,v114
v_mul_f32 v115,v23,v115
v_mfma_f32_16x16x32_bf16 a[100:103],a[204:207],v[236:239],0
v_mul_f32 v12,v23,v12
v_mul_f32 v13,v23,v13
v_cvt_pk_bf16_f32 v114,v114,v115
v_cvt_pk_bf16_f32 v115,v12,v13
v_mfma_f32_16x16x32_bf16 a[100:103],a[200:203],v[232:235],a[100:103]
v_accvgpr_read_b32 v116,a104
v_accvgpr_read_b32 v117,a105
v_accvgpr_read_b32 v118,a106
v_accvgpr_read_b32 v119,a107
v_accvgpr_read_b32 v12,a108
v_accvgpr_read_b32 v13,a109
v_accvgpr_read_b32 v14,a110
v_accvgpr_read_b32 v15,a111
v_pk_mul_f32 v[116:117],v[22:23],v[116:117] op_sel:[1,0] op_sel_hi:[1,1]
v_pk_mul_f32 v[118:119],v[22:23],v[118:119] op_sel:[1,0] op_sel_hi:[1,1]
v_pk_mul_f32 v[12:13],v[22:23],v[12:13] op_sel:[1,0] op_sel_hi:[1,1]
v_pk_mul_f32 v[14:15],v[22:23],v[14:15] op_sel:[1,0] op_sel_hi:[1,1]
v_cvt_pk_bf16_f32 v116,v116,v117
v_cvt_pk_bf16_f32 v117,v118,v119
v_cvt_pk_bf16_f32 v118,v12,v13
v_cvt_pk_bf16_f32 v119,v14,v15
v_mfma_f32_16x16x32_bf16 a[104:107],a[208:211],v[232:235],0
v_accvgpr_read_b32 v120,a112
v_accvgpr_read_b32 v121,a113
v_accvgpr_read_b32 v122,a114
v_accvgpr_read_b32 v123,a115
v_accvgpr_read_b32 v12,a116
v_accvgpr_read_b32 v13,a117
v_accvgpr_read_b32 v14,a118
v_accvgpr_read_b32 v15,a119
v_pk_mul_f32 v[120:121],v[22:23],v[120:121] op_sel:[1,0] op_sel_hi:[1,1]
v_pk_mul_f32 v[122:123],v[22:23],v[122:123] op_sel:[1,0] op_sel_hi:[1,1]
v_pk_mul_f32 v[12:13],v[22:23],v[12:13] op_sel:[1,0] op_sel_hi:[1,1]
v_pk_mul_f32 v[14:15],v[22:23],v[14:15] op_sel:[1,0] op_sel_hi:[1,1]
v_cvt_pk_bf16_f32 v120,v120,v121
v_cvt_pk_bf16_f32 v121,v122,v123
v_cvt_pk_bf16_f32 v122,v12,v13
v_cvt_pk_bf16_f32 v123,v14,v15
v_mfma_f32_16x16x32_bf16 a[104:107],a[212:215],v[236:239],a[104:107]
v_accvgpr_read_b32 v124,a120
v_accvgpr_read_b32 v125,a121
v_accvgpr_read_b32 v126,a122
v_accvgpr_read_b32 v127,a123
v_accvgpr_read_b32 v12,a124
v_accvgpr_read_b32 v13,a125
v_accvgpr_read_b32 v14,a126
v_accvgpr_read_b32 v15,a127
v_pk_mul_f32 v[124:125],v[22:23],v[124:125] op_sel:[1,0] op_sel_hi:[1,1]
v_pk_mul_f32 v[126:127],v[22:23],v[126:127] op_sel:[1,0] op_sel_hi:[1,1]
v_pk_mul_f32 v[12:13],v[22:23],v[12:13] op_sel:[1,0] op_sel_hi:[1,1]
v_pk_mul_f32 v[14:15],v[22:23],v[14:15] op_sel:[1,0] op_sel_hi:[1,1]
v_cvt_pk_bf16_f32 v124,v124,v125
v_cvt_pk_bf16_f32 v125,v126,v127
v_cvt_pk_bf16_f32 v126,v12,v13
v_cvt_pk_bf16_f32 v127,v14,v15
v_mfma_f32_16x16x32_bf16 a[108:111],a[220:223],v[236:239],0
buffer_load_dwordx4 v4,s[24:27],s47 offen offset:2048 nt lds
v_mfma_f32_16x16x32_bf16 a[108:111],a[216:219],v[232:235],a[108:111]
v_cmp_nlt_f32 s[58:59],v60,s63
s_nop 1
s_or_b64 s[56:57],s[56:57],s[58:59]
v_mfma_f32_16x16x32_bf16 a[112:115],a[224:227],v[232:235],0
v_mov_b32 v60,0
v_mov_b32 v61,0
v_mfma_f32_16x16x32_bf16 a[112:115],a[228:231],v[236:239],a[112:115]
v_mov_b32 v62,0
v_mov_b32 v63,0
v_mfma_f32_16x16x32_bf16 a[116:119],a[236:239],v[236:239],0
v_add_u32 v2,s48,v2
v_add_u32 v3,s48,v3
v_mfma_f32_16x16x32_bf16 a[116:119],a[232:235],v[232:235],a[116:119]
s_mov_b32 s48,32768
ds_read_b64_tr_b16 a[192:193],v2
ds_read_b64_tr_b16 a[194:195],v2 offset:4096
v_mfma_f32_16x16x32_bf16 a[120:123],a[240:243],v[232:235],0
ds_read_b64_tr_b16 a[196:197],v2 offset:8192
ds_read_b64_tr_b16 a[198:199],v2 offset:12288
v_mfma_f32_16x16x32_bf16 a[120:123],a[244:247],v[236:239],a[120:123]
ds_read_b64_tr_b16 a[204:205],v3 offset:8192
ds_read_b64_tr_b16 a[206:207],v3 offset:12288
v_mfma_f32_16x16x32_bf16 a[124:127],a[252:255],v[236:239],0
ds_read_b64_tr_b16 a[200:201],v3
ds_read_b64_tr_b16 a[202:203],v3 offset:4096
v_mfma_f32_16x16x32_bf16 a[124:127],a[248:251],v[232:235],a[124:127]
)A"
R"A(v_exp_f32 v136,v136
v_smfmac_f32_16x16x64_bf16 v[60:63],v[28:31],v[232:239],v27
ds_read_b64_tr_b16 a[208:209],v2 offset:256
v_mfma_f32_16x16x32_bf16 v[200:203],a[128:131],v[144:147],v[40:43]
v_exp_f32 v137,v137
v_mfma_f32_16x16x32_bf16 v[200:203],a[132:135],v[176:179],v[200:203]
ds_read_b64_tr_b16 a[210:211],v2 offset:4352
v_cvt_pk_bf16_f32 v136,v136,v137
v_mfma_f32_16x16x32_bf16 v[200:203],a[136:139],v[208:211],v[200:203]
v_exp_f32 v138,v138
v_mfma_f32_16x16x32_bf16 v[200:203],a[140:143],v[240:243],v[200:203]
buffer_load_dwordx4 v5,s[24:27],s47 offen offset:3072 nt lds
v_mfma_f32_16x16x32_bf16 v[204:207],a[156:159],v[240:243],v[40:43]
s_add_u32 s47,s47,16384
v_exp_f32 v139,v139
v_mfma_f32_16x16x32_bf16 v[204:207],a[152:155],v[208:211],v[204:207]
s_and_b32 s47,s47,0x3ffff
v_exp_f32 v140,v140
v_mfma_f32_16x16x32_bf16 v[204:207],a[148:151],v[176:179],v[204:207]
s_or_b32 s47,s47,s46
v_exp_f32 v141,v141
v_mfma_f32_16x16x32_bf16 v[204:207],a[144:147],v[144:147],v[204:207]
v_exp_f32 v142,v142
v_mfma_f32_16x16x32_bf16 v[216:219],a[160:163],v[144:147],v[40:43]
v_exp_f32 v143,v143
v_mfma_f32_16x16x32_bf16 v[216:219],a[164:167],v[176:179],v[216:219]
ds_read_b64_tr_b16 a[212:213],v2 offset:8448
ds_read_b64_tr_b16 a[214:215],v2 offset:12544
v_mfma_f32_16x16x32_bf16 v[216:219],a[168:171],v[208:211],v[216:219]
v_cvt_pk_bf16_f32 v137,v138,v139
v_cvt_pk_bf16_f32 v138,v140,v141
v_mfma_f32_16x16x32_bf16 v[216:219],a[172:175],v[240:243],v[216:219]
ds_read_b64_tr_b16 a[220:221],v3 offset:8448
v_cvt_pk_bf16_f32 v139,v142,v143
v_mfma_f32_16x16x32_bf16 v[220:223],a[188:191],v[240:243],v[40:43]
v_exp_f32 v152,v152
v_mfma_f32_16x16x32_bf16 v[220:223],a[184:187],v[208:211],v[220:223]
ds_read_b64_tr_b16 a[222:223],v3 offset:12544
v_exp_f32 v153,v153
v_exp_f32 v154,v154
v_exp_f32 v155,v155
v_exp_f32 v156,v156
v_exp_f32 v157,v157
v_exp_f32 v158,v158
v_exp_f32 v159,v159
v_mfma_f32_16x16x32_bf16 v[220:223],a[180:183],v[176:179],v[220:223]
ds_read_b64_tr_b16 a[216:217],v3 offset:256
ds_read_b64_tr_b16 a[218:219],v3 offset:4352
v_cvt_pk_bf16_f32 v140,v152,v153
v_cvt_pk_bf16_f32 v141,v154,v155
v_cvt_pk_bf16_f32 v142,v156,v157
v_cvt_pk_bf16_f32 v143,v158,v159
v_mfma_f32_16x16x32_bf16 v[220:223],a[176:179],v[144:147],v[220:223]
s_waitcnt lgkmcnt(6)
ds_read_b64_tr_b16 a[224:225],v2 offset:512
ds_read_b64_tr_b16 a[226:227],v2 offset:4608
v_mfma_f32_16x16x32_bf16 a[0:3],a[192:195],v[136:139],a[0:3]
ds_read_b64_tr_b16 a[228:229],v2 offset:8704
ds_read_b64_tr_b16 a[230:231],v2 offset:12800
v_mfma_f32_16x16x32_bf16 a[0:3],a[196:199],v[140:143],a[0:3]
ds_read_b64_tr_b16 a[236:237],v3 offset:8704
ds_read_b64_tr_b16 a[238:239],v3 offset:12800
v_mfma_f32_16x16x32_bf16 a[4:7],a[204:207],v[140:143],a[4:7]
ds_read_b64_tr_b16 a[232:233],v3 offset:512
ds_read_b64_tr_b16 a[234:235],v3 offset:4608
v_mfma_f32_16x16x32_bf16 a[4:7],a[200:203],v[136:139],a[4:7]
s_memrealtime s[76:77]
ds_read_b64_tr_b16 a[240:241],v2 offset:768
ds_read_b64_tr_b16 a[242:243],v2 offset:4864
v_mfma_f32_16x16x32_bf16 a[8:11],a[208:211],v[136:139],a[8:11]
s_waitcnt lgkmcnt(13)
ds_read_b64_tr_b16 a[244:245],v2 offset:8960
ds_read_b64_tr_b16 a[246:247],v2 offset:13056
v_mfma_f32_16x16x32_bf16 a[8:11],a[212:215],v[140:143],a[8:11]
s_waitcnt lgkmcnt(14)
ds_read_b64_tr_b16 a[252:253],v3 offset:8960
ds_read_b64_tr_b16 a[254:255],v3 offset:13056
v_mfma_f32_16x16x32_bf16 a[12:15],a[220:223],v[140:143],a[12:15]
s_waitcnt lgkmcnt(14)
ds_read_b64_tr_b16 a[248:249],v3 offset:768
ds_read_b64_tr_b16 a[250:251],v3 offset:4864
v_mfma_f32_16x16x32_bf16 a[12:15],a[216:219],v[136:139],a[12:15]
s_waitcnt lgkmcnt(14)
v_exp_f32 v168,v168
v_mfma_f32_16x16x32_bf16 a[16:19],a[224:227],v[136:139],a[16:19]
s_waitcnt lgkmcnt(12)
v_exp_f32 v169,v169
v_mfma_f32_16x16x32_bf16 a[16:19],a[228:231],v[140:143],a[16:19]
s_waitcnt lgkmcnt(10)
v_mul_f32 v98,v22,v98
v_mul_f32 v99,v22,v99
v_mfma_f32_16x16x32_bf16 a[20:23],a[236:239],v[140:143],a[20:23]
s_waitcnt lgkmcnt(0)
v_exp_f32 v170,v170
v_mfma_f32_16x16x32_bf16 a[20:23],a[232:235],v[136:139],a[20:23]
s_sub_u32 s79,s78,s76
v_exp_f32 v171,v171
v_mfma_f32_16x16x32_bf16 a[24:27],a[240:243],v[136:139],a[24:27]
s_add_u32 s78,s76,s60
v_mul_f32 v16,v22,v16
v_mul_f32 v17,v22,v17
v_mfma_f32_16x16x32_bf16 a[24:27],a[244:247],v[140:143],a[24:27]
s_cmp_gt_i32 s79,0
s_cbranch_scc1 L_ps6_%=
L_pr6_%=:
v_cvt_pk_bf16_f32 v98,v98,v99
v_mfma_f32_16x16x32_bf16 a[28:31],a[252:255],v[140:143],a[28:31]
v_cvt_pk_bf16_f32 v168,v168,v169
v_exp_f32 v172,v172
v_exp_f32 v173,v173
v_exp_f32 v174,v174
v_exp_f32 v175,v175
v_cvt_pk_bf16_f32 v99,v16,v17
v_cvt_pk_bf16_f32 v169,v170,v171
v_exp_f32 v184,v184
v_exp_f32 v185,v185
v_exp_f32 v186,v186
v_exp_f32 v187,v187
v_exp_f32 v188,v188
v_exp_f32 v189,v189
v_exp_f32 v190,v190
v_exp_f32 v191,v191
v_mfma_f32_16x16x32_bf16 a[28:31],a[248:251],v[136:139],a[28:31]
s_cmp_lg_u64 s[56:57],0
s_cbranch_scc1 L_slow0_%=
L_sret0_%=:
v_cvt_pk_bf16_f32 v170,v172,v173
v_cvt_pk_bf16_f32 v171,v174,v175
v_add_u32 v1,s48,v1
v_cvt_pk_bf16_f32 v172,v184,v185
v_cvt_pk_bf16_f32 v173,v186,v187
v_cvt_pk_bf16_f32 v174,v188,v189
v_cvt_pk_bf16_f32 v175,v190,v191
v_smfmac_f32_16x16x64_bf16 v[48:51],v[28:31],v[136:143],v27
s_mov_b32 s53,11
s_cmp_eq_u32 s40,3
s_cbranch_scc0 L_att1_%=
s_cmp_gt_i32 s18,0
s_cbranch_scc1 L_att1v0_%=
s_cmp_gt_i32 s19,0
s_cbranch_scc1 L_att1v1_%=
s_cmp_gt_i32 s100,0
s_cbranch_scc1 L_att1v2_%=
.p2align 5
L_att1v3_%=:
s_waitcnt vmcnt(8) lgkmcnt(0)
v_exp_f32 v200,v200
s_barrier
s_add_u32 m0,s49,s51
ds_read_b128 a[128:131],v1
s_mov_b32 s90,L_out_end_%=-L_pc_%=+128
v_exp_f32 v201,v201
s_lshr_b32 s45,s45,1
ds_read_b128 a[132:135],v1 offset:256
v_cvt_pk_bf16_f32 v200,v200,v201
s_add_u32 s62,s50,32768
buffer_load_dwordx4 v4,s[20:23],s47 offen nt lds
s_and_b32 s62,s62,131071
ds_read_b128 a[136:139],v1 offset:512
v_add_u32 v2,s48,v2
s_add_u32 s54,s54,16384
v_exp_f32 v202,v202
s_sub_u32 s53,s53,1
ds_read_b128 a[140:143],v1 offset:768
v_add_u32 v3,s48,v3
s_sub_u32 s48,s62,s50
v_exp_f32 v203,v203
s_and_b32 s46,s54,0xfffc0000
ds_read_b128 a[156:159],v1 offset:4864
v_cvt_pk_bf16_f32 v201,v202,v203
s_mov_b32 s50,s62
v_exp_f32 v204,v204
v_exp_f32 v205,v205
ds_read_b128 a[152:155],v1 offset:4608
v_cvt_pk_bf16_f32 v202,v204,v205
v_exp_f32 v206,v206
ds_read_b128 a[148:151],v1 offset:4352
v_exp_f32 v207,v207
v_mfma_f32_16x16x32_bf16 a[32:35],a[192:195],v[168:171],a[32:35]
ds_read_b128 a[144:147],v1 offset:4096
v_cvt_pk_bf16_f32 v203,v206,v207
v_mfma_f32_16x16x32_bf16 a[32:35],a[196:199],v[172:175],a[32:35]
v_exp_f32 v216,v216
v_mfma_f32_16x16x32_bf16 a[36:39],a[204:207],v[172:175],a[36:39]
buffer_load_dwordx4 v4,s[20:23],s47 offen offset:1024 nt lds
v_mfma_f32_16x16x32_bf16 a[36:39],a[200:203],v[168:171],a[36:39]
ds_read_b128 a[160:163],v1 offset:8192
v_mfma_f32_16x16x32_bf16 a[40:43],a[208:211],v[168:171],a[40:43]
v_exp_f32 v217,v217
v_mfma_f32_16x16x32_bf16 a[40:43],a[212:215],v[172:175],a[40:43]
ds_read_b128 a[164:167],v1 offset:8448
v_cvt_pk_bf16_f32 v204,v216,v217
v_mfma_f32_16x16x32_bf16 a[44:47],a[220:223],v[172:175],a[44:47]
v_exp_f32 v218,v218
v_mfma_f32_16x16x32_bf16 a[44:47],a[216:219],v[168:171],a[44:47]
ds_read_b128 a[168:171],v1 offset:8704
v_mfma_f32_16x16x32_bf16 a[48:51],a[224:227],v[168:171],a[48:51]
v_exp_f32 v219,v219
v_mfma_f32_16x16x32_bf16 a[48:51],a[228:231],v[172:175],a[48:51]
ds_read_b128 a[172:175],v1 offset:8960
v_cvt_pk_bf16_f32 v205,v218,v219
v_mfma_f32_16x16x32_bf16 a[52:55],a[236:239],v[172:175],a[52:55]
v_exp_f32 v220,v220
v_mfma_f32_16x16x32_bf16 a[52:55],a[232:235],v[168:171],a[52:55]
ds_read_b128 a[188:191],v1 offset:13056
v_mfma_f32_16x16x32_bf16 a[56:59],a[240:243],v[168:171],a[56:59]
v_exp_f32 v221,v221
v_mfma_f32_16x16x32_bf16 a[56:59],a[244:247],v[172:175],a[56:59]
ds_read_b128 a[184:187],v1 offset:12800
v_cvt_pk_bf16_f32 v206,v220,v221
v_mfma_f32_16x16x32_bf16 a[60:63],a[252:255],v[172:175],a[60:63]
v_exp_f32 v222,v222
v_mfma_f32_16x16x32_bf16 a[60:63],a[248:251],v[168:171],a[60:63]
ds_read_b128 a[180:183],v1 offset:12544
v_smfmac_f32_16x16x64_bf16 v[52:55],v[28:31],v[168:175],v27
s_waitcnt lgkmcnt(4)
v_mfma_f32_16x16x32_bf16 v[136:139],a[128:131],v[128:131],v[32:35]
buffer_load_dwordx4 v5,s[20:23],s47 offen offset:2048 nt lds
v_mfma_f32_16x16x32_bf16 v[136:139],a[132:135],v[160:163],v[136:139]
ds_read_b128 a[176:179],v1 offset:12288
v_add_u32 v1,s48,v1
v_mfma_f32_16x16x32_bf16 v[136:139],a[136:139],v[192:195],v[136:139]
v_exp_f32 v223,v223
v_mfma_f32_16x16x32_bf16 v[136:139],a[140:143],v[224:227],v[136:139]
v_cvt_pk_bf16_f32 v207,v222,v223
v_mfma_f32_16x16x32_bf16 v[140:143],a[156:159],v[224:227],v[32:35]
v_mfma_f32_16x16x32_bf16 v[140:143],a[152:155],v[192:195],v[140:143]
v_mfma_f32_16x16x32_bf16 v[140:143],a[148:151],v[160:163],v[140:143]
v_mfma_f32_16x16x32_bf16 v[140:143],a[144:147],v[128:131],v[140:143]
v_mfma_f32_16x16x32_bf16 v[152:155],a[160:163],v[128:131],v[32:35]
v_mfma_f32_16x16x32_bf16 v[152:155],a[164:167],v[160:163],v[152:155]
v_mfma_f32_16x16x32_bf16 v[152:155],a[168:171],v[192:195],v[152:155]
s_waitcnt lgkmcnt(0)
v_mfma_f32_16x16x32_bf16 v[152:155],a[172:175],v[224:227],v[152:155]
v_mfma_f32_16x16x32_bf16 v[156:159],a[188:191],v[224:227],v[32:35]
v_mfma_f32_16x16x32_bf16 v[156:159],a[184:187],v[192:195],v[156:159]
buffer_load_dwordx4 v5,s[20:23],s47 offen offset:3072 nt lds
v_mfma_f32_16x16x32_bf16 v[156:159],a[180:183],v[160:163],v[156:159]
v_mfma_f32_16x16x32_bf16 v[156:159],a[176:179],v[128:131],v[156:159]
v_mfma_f32_16x16x32_bf16 a[64:67],a[192:195],v[200:203],a[64:67]
v_mfma_f32_16x16x32_bf16 a[64:67],a[196:199],v[204:207],a[64:67]
v_mfma_f32_16x16x32_bf16 a[68:71],a[204:207],v[204:207],a[68:71]
v_mfma_f32_16x16x32_bf16 a[68:71],a[200:203],v[200:203],a[68:71]
v_mfma_f32_16x16x32_bf16 a[72:75],a[208:211],v[200:203],a[72:75]
v_mfma_f32_16x16x32_bf16 a[72:75],a[212:215],v[204:207],a[72:75]
v_exp_f32 v136,v136
v_mfma_f32_16x16x32_bf16 a[76:79],a[220:223],v[204:207],a[76:79]
v_exp_f32 v137,v137
v_mfma_f32_16x16x32_bf16 a[76:79],a[216:219],v[200:203],a[76:79]
v_exp_f32 v138,v138
v_mfma_f32_16x16x32_bf16 a[80:83],a[224:227],v[200:203],a[80:83]
s_add_u32 m0,s49,s52
v_mfma_f32_16x16x32_bf16 a[80:83],a[228:231],v[204:207],a[80:83]
s_xor_b32 s49,s50,65536
v_mfma_f32_16x16x32_bf16 a[84:87],a[236:239],v[204:207],a[84:87]
v_mfma_f32_16x16x32_bf16 a[84:87],a[232:235],v[200:203],a[84:87]
buffer_load_dwordx4 v4,s[24:27],s47 offen nt lds
v_mfma_f32_16x16x32_bf16 a[88:91],a[240:243],v[200:203],a[88:91]
v_mfma_f32_16x16x32_bf16 a[88:91],a[244:247],v[204:207],a[88:91]
v_exp_f32 v139,v139
v_mfma_f32_16x16x32_bf16 a[92:95],a[252:255],v[204:207],a[92:95]
v_exp_f32 v140,v140
v_mfma_f32_16x16x32_bf16 a[92:95],a[248:251],v[200:203],a[92:95]
v_exp_f32 v141,v141
v_smfmac_f32_16x16x64_bf16 v[56:59],v[28:31],v[200:207],v27
v_mfma_f32_16x16x32_bf16 v[168:171],a[128:131],v[132:135],v[36:39]
v_exp_f32 v142,v142
v_mfma_f32_16x16x32_bf16 v[168:171],a[132:135],v[164:167],v[168:171]
v_exp_f32 v143,v143
v_mfma_f32_16x16x32_bf16 v[168:171],a[136:139],v[196:199],v[168:171]
v_exp_f32 v152,v152
v_mfma_f32_16x16x32_bf16 v[168:171],a[140:143],v[228:231],v[168:171]
v_exp_f32 v153,v153
v_mfma_f32_16x16x32_bf16 v[172:175],a[156:159],v[228:231],v[36:39]
v_exp_f32 v154,v154
v_mfma_f32_16x16x32_bf16 v[172:175],a[152:155],v[196:199],v[172:175]
v_exp_f32 v155,v155
v_mfma_f32_16x16x32_bf16 v[172:175],a[148:151],v[164:167],v[172:175]
v_exp_f32 v156,v156
v_mfma_f32_16x16x32_bf16 v[172:175],a[144:147],v[132:135],v[172:175]
v_exp_f32 v157,v157
v_mfma_f32_16x16x32_bf16 v[184:187],a[160:163],v[132:135],v[36:39]
buffer_load_dwordx4 v5,s[24:27],s47 offen offset:1024 nt lds
v_mfma_f32_16x16x32_bf16 v[184:187],a[164:167],v[164:167],v[184:187]
v_exp_f32 v158,v158
v_mfma_f32_16x16x32_bf16 v[184:187],a[168:171],v[196:199],v[184:187]
v_exp_f32 v159,v159
v_mfma_f32_16x16x32_bf16 v[184:187],a[172:175],v[228:231],v[184:187]
v_exp_f32 v168,v168
v_mfma_f32_16x16x32_bf16 v[188:191],a[188:191],v[228:231],v[36:39]
v_exp_f32 v169,v169
v_mfma_f32_16x16x32_bf16 v[188:191],a[184:187],v[196:199],v[188:191]
v_exp_f32 v170,v170
v_mfma_f32_16x16x32_bf16 v[188:191],a[180:183],v[164:167],v[188:191]
)A"
R"A(v_exp_f32 v171,v171
v_mfma_f32_16x16x32_bf16 v[188:191],a[176:179],v[132:135],v[188:191]
v_exp_f32 v172,v172
ds_read_b64_tr_b16 a[192:193],v2
v_cvt_pk_bf16_f32 v168,v168,v169
ds_read_b64_tr_b16 a[194:195],v2 offset:4096
v_cvt_pk_bf16_f32 v169,v170,v171
v_exp_f32 v173,v173
ds_read_b64_tr_b16 a[196:197],v2 offset:8192
v_cvt_pk_bf16_f32 v170,v172,v173
ds_read_b64_tr_b16 a[198:199],v2 offset:12288
v_cvt_pk_bf16_f32 v136,v136,v137
v_exp_f32 v174,v174
buffer_load_dwordx4 v4,s[24:27],s47 offen offset:2048 nt lds
ds_read_b64_tr_b16 a[204:205],v3 offset:8192
v_cvt_pk_bf16_f32 v137,v138,v139
v_exp_f32 v175,v175
ds_read_b64_tr_b16 a[206:207],v3 offset:12288
v_cvt_pk_bf16_f32 v171,v174,v175
ds_read_b64_tr_b16 a[200:201],v3
v_cvt_pk_bf16_f32 v138,v140,v141
ds_read_b64_tr_b16 a[202:203],v3 offset:4096
ds_read_b64_tr_b16 a[208:209],v2 offset:256
ds_read_b64_tr_b16 a[210:211],v2 offset:4352
v_cvt_pk_bf16_f32 v139,v142,v143
ds_read_b64_tr_b16 a[212:213],v2 offset:8448
ds_read_b64_tr_b16 a[214:215],v2 offset:12544
ds_read_b64_tr_b16 a[220:221],v3 offset:8448
v_mfma_f32_16x16x32_bf16 v[200:203],a[128:131],v[144:147],v[40:43]
ds_read_b64_tr_b16 a[222:223],v3 offset:12544
v_cvt_pk_bf16_f32 v140,v152,v153
v_mfma_f32_16x16x32_bf16 v[200:203],a[132:135],v[176:179],v[200:203]
ds_read_b64_tr_b16 a[216:217],v3 offset:256
v_mfma_f32_16x16x32_bf16 v[200:203],a[136:139],v[208:211],v[200:203]
ds_read_b64_tr_b16 a[218:219],v3 offset:4352
v_cvt_pk_bf16_f32 v141,v154,v155
v_mfma_f32_16x16x32_bf16 v[200:203],a[140:143],v[240:243],v[200:203]
buffer_load_dwordx4 v5,s[24:27],s47 offen offset:3072 nt lds
v_mfma_f32_16x16x32_bf16 v[204:207],a[156:159],v[240:243],v[40:43]
s_add_u32 s47,s47,16384
s_and_b32 s47,s47,0x3ffff
ds_read_b64_tr_b16 a[224:225],v2 offset:512
v_mfma_f32_16x16x32_bf16 v[204:207],a[152:155],v[208:211],v[204:207]
s_or_b32 s47,s47,s46
ds_read_b64_tr_b16 a[226:227],v2 offset:4608
v_cvt_pk_bf16_f32 v142,v156,v157
v_mfma_f32_16x16x32_bf16 v[204:207],a[148:151],v[176:179],v[204:207]
ds_read_b64_tr_b16 a[228:229],v2 offset:8704
v_mfma_f32_16x16x32_bf16 v[204:207],a[144:147],v[144:147],v[204:207]
ds_read_b64_tr_b16 a[230:231],v2 offset:12800
v_mfma_f32_16x16x32_bf16 v[216:219],a[160:163],v[144:147],v[40:43]
ds_read_b64_tr_b16 a[236:237],v3 offset:8704
v_cvt_pk_bf16_f32 v143,v158,v159
v_mfma_f32_16x16x32_bf16 v[216:219],a[164:167],v[176:179],v[216:219]
ds_read_b64_tr_b16 a[238:239],v3 offset:12800
v_mfma_f32_16x16x32_bf16 v[216:219],a[168:171],v[208:211],v[216:219]
ds_read_b64_tr_b16 a[232:233],v3 offset:512
v_mfma_f32_16x16x32_bf16 v[216:219],a[172:175],v[240:243],v[216:219]
ds_read_b64_tr_b16 a[234:235],v3 offset:4608
v_mfma_f32_16x16x32_bf16 v[220:223],a[188:191],v[240:243],v[40:43]
ds_read_b64_tr_b16 a[240:241],v2 offset:768
v_mfma_f32_16x16x32_bf16 v[220:223],a[184:187],v[208:211],v[220:223]
ds_read_b64_tr_b16 a[242:243],v2 offset:4864
v_mfma_f32_16x16x32_bf16 v[220:223],a[180:183],v[176:179],v[220:223]
ds_read_b64_tr_b16 a[244:245],v2 offset:8960
v_mfma_f32_16x16x32_bf16 v[220:223],a[176:179],v[144:147],v[220:223]
s_waitcnt lgkmcnt(7)
buffer_load_dword v26,v24,s[88:91],s93 offen
v_mfma_f32_16x16x32_bf16 a[0:3],a[192:195],v[136:139],a[0:3]
ds_read_b64_tr_b16 a[246:247],v2 offset:13056
v_mfma_f32_16x16x32_bf16 a[0:3],a[196:199],v[140:143],a[0:3]
ds_read_b64_tr_b16 a[252:253],v3 offset:8960
v_mfma_f32_16x16x32_bf16 a[4:7],a[204:207],v[140:143],a[4:7]
ds_read_b64_tr_b16 a[254:255],v3 offset:13056
v_mfma_f32_16x16x32_bf16 a[4:7],a[200:203],v[136:139],a[4:7]
ds_read_b64_tr_b16 a[248:249],v3 offset:768
v_mfma_f32_16x16x32_bf16 a[8:11],a[208:211],v[136:139],a[8:11]
ds_read_b64_tr_b16 a[250:251],v3 offset:4864
v_mfma_f32_16x16x32_bf16 a[8:11],a[212:215],v[140:143],a[8:11]
v_exp_f32 v184,v184
v_mfma_f32_16x16x32_bf16 a[12:15],a[220:223],v[140:143],a[12:15]
v_exp_f32 v185,v185
v_mfma_f32_16x16x32_bf16 a[12:15],a[216:219],v[136:139],a[12:15]
s_bitcmp1_b32 s45,0
s_cbranch_scc0 L_t31v31_%=
s_mov_b32 m0,s80
s_add_u32 s80,s80,4096
buffer_load_dwordx4 v25,s[24:27],s83 offen nt lds
s_add_u32 s83,s83,4096
L_t31v31_%=:
v_mfma_f32_16x16x32_bf16 a[16:19],a[224:227],v[136:139],a[16:19]
v_exp_f32 v186,v186
v_mfma_f32_16x16x32_bf16 a[16:19],a[228:231],v[140:143],a[16:19]
s_waitcnt lgkmcnt(3)
v_exp_f32 v187,v187
v_mfma_f32_16x16x32_bf16 a[20:23],a[236:239],v[140:143],a[20:23]
v_exp_f32 v188,v188
v_mfma_f32_16x16x32_bf16 a[20:23],a[232:235],v[136:139],a[20:23]
v_exp_f32 v189,v189
v_mfma_f32_16x16x32_bf16 a[24:27],a[240:243],v[136:139],a[24:27]
v_exp_f32 v190,v190
v_mfma_f32_16x16x32_bf16 a[24:27],a[244:247],v[140:143],a[24:27]
s_waitcnt lgkmcnt(0)
v_cvt_pk_bf16_f32 v172,v184,v185
v_mfma_f32_16x16x32_bf16 a[28:31],a[252:255],v[140:143],a[28:31]
v_exp_f32 v191,v191
v_mfma_f32_16x16x32_bf16 a[28:31],a[248:251],v[136:139],a[28:31]
v_cvt_pk_bf16_f32 v173,v186,v187
v_cvt_pk_bf16_f32 v174,v188,v189
v_cvt_pk_bf16_f32 v175,v190,v191
s_cmp_lg_u32 s53,0
v_smfmac_f32_16x16x64_bf16 v[48:51],v[28:31],v[136:143],v27
s_cbranch_scc1 L_att1v3_%=
s_waitcnt lgkmcnt(0)
s_nop 7
s_branch L_att1x_%=
.p2align 5
L_att1v0_%=:
v_mfma_f32_16x16x32_bf16 v[232:235],a[128:131],v[148:151],v[44:47]
s_waitcnt vmcnt(8) lgkmcnt(0)
v_exp_f32 v200,v200
v_mfma_f32_16x16x32_bf16 v[232:235],a[132:135],v[180:183],v[232:235]
s_barrier
s_add_u32 m0,s49,s51
ds_read_b128 a[128:131],v1
v_mfma_f32_16x16x32_bf16 v[232:235],a[136:139],v[212:215],v[232:235]
s_mov_b32 s90,L_out_end_%=-L_pc_%=+128
v_exp_f32 v201,v201
v_mfma_f32_16x16x32_bf16 v[232:235],a[140:143],v[244:247],v[232:235]
s_lshr_b32 s45,s45,1
ds_read_b128 a[132:135],v1 offset:256
v_cvt_pk_bf16_f32 v200,v200,v201
v_mfma_f32_16x16x32_bf16 v[236:239],a[156:159],v[244:247],v[44:47]
s_add_u32 s62,s50,32768
buffer_load_dwordx4 v4,s[20:23],s47 offen nt lds
v_mfma_f32_16x16x32_bf16 v[236:239],a[152:155],v[212:215],v[236:239]
s_and_b32 s62,s62,131071
ds_read_b128 a[136:139],v1 offset:512
v_add_u32 v2,s48,v2
v_mfma_f32_16x16x32_bf16 v[236:239],a[148:151],v[180:183],v[236:239]
s_add_u32 s54,s54,16384
v_exp_f32 v202,v202
v_mfma_f32_16x16x32_bf16 v[236:239],a[144:147],v[148:151],v[236:239]
s_sub_u32 s53,s53,1
ds_read_b128 a[140:143],v1 offset:768
v_add_u32 v3,s48,v3
v_mfma_f32_16x16x32_bf16 v[248:251],a[160:163],v[148:151],v[44:47]
s_sub_u32 s48,s62,s50
v_exp_f32 v203,v203
v_mfma_f32_16x16x32_bf16 v[248:251],a[164:167],v[180:183],v[248:251]
s_and_b32 s46,s54,0xfffc0000
ds_read_b128 a[156:159],v1 offset:4864
v_cvt_pk_bf16_f32 v201,v202,v203
v_mfma_f32_16x16x32_bf16 v[248:251],a[168:171],v[212:215],v[248:251]
s_mov_b32 s50,s62
v_exp_f32 v204,v204
v_mfma_f32_16x16x32_bf16 v[248:251],a[172:175],v[244:247],v[248:251]
v_exp_f32 v205,v205
v_mfma_f32_16x16x32_bf16 v[252:255],a[188:191],v[244:247],v[44:47]
ds_read_b128 a[152:155],v1 offset:4608
v_cvt_pk_bf16_f32 v202,v204,v205
v_mfma_f32_16x16x32_bf16 v[252:255],a[184:187],v[212:215],v[252:255]
v_exp_f32 v206,v206
v_mfma_f32_16x16x32_bf16 v[252:255],a[180:183],v[180:183],v[252:255]
ds_read_b128 a[148:151],v1 offset:4352
v_mfma_f32_16x16x32_bf16 v[252:255],a[176:179],v[148:151],v[252:255]
v_exp_f32 v207,v207
v_mfma_f32_16x16x32_bf16 a[32:35],a[192:195],v[168:171],a[32:35]
ds_read_b128 a[144:147],v1 offset:4096
v_cvt_pk_bf16_f32 v203,v206,v207
v_mfma_f32_16x16x32_bf16 a[32:35],a[196:199],v[172:175],a[32:35]
v_exp_f32 v216,v216
v_mfma_f32_16x16x32_bf16 a[36:39],a[204:207],v[172:175],a[36:39]
buffer_load_dwordx4 v4,s[20:23],s47 offen offset:1024 nt lds
v_mfma_f32_16x16x32_bf16 a[36:39],a[200:203],v[168:171],a[36:39]
ds_read_b128 a[160:163],v1 offset:8192
v_mfma_f32_16x16x32_bf16 a[40:43],a[208:211],v[168:171],a[40:43]
v_exp_f32 v217,v217
v_mfma_f32_16x16x32_bf16 a[40:43],a[212:215],v[172:175],a[40:43]
ds_read_b128 a[164:167],v1 offset:8448
v_cvt_pk_bf16_f32 v204,v216,v217
v_mfma_f32_16x16x32_bf16 a[44:47],a[220:223],v[172:175],a[44:47]
v_exp_f32 v218,v218
v_mfma_f32_16x16x32_bf16 a[44:47],a[216:219],v[168:171],a[44:47]
ds_read_b128 a[168:171],v1 offset:8704
v_mfma_f32_16x16x32_bf16 a[48:51],a[224:227],v[168:171],a[48:51]
v_exp_f32 v219,v219
v_mfma_f32_16x16x32_bf16 a[48:51],a[228:231],v[172:175],a[48:51]
ds_read_b128 a[172:175],v1 offset:8960
v_cvt_pk_bf16_f32 v205,v218,v219
v_mfma_f32_16x16x32_bf16 a[52:55],a[236:239],v[172:175],a[52:55]
v_exp_f32 v220,v220
v_mfma_f32_16x16x32_bf16 a[52:55],a[232:235],v[168:171],a[52:55]
ds_read_b128 a[188:191],v1 offset:13056
v_mfma_f32_16x16x32_bf16 a[56:59],a[240:243],v[168:171],a[56:59]
v_exp_f32 v221,v221
v_mfma_f32_16x16x32_bf16 a[56:59],a[244:247],v[172:175],a[56:59]
ds_read_b128 a[184:187],v1 offset:12800
v_cvt_pk_bf16_f32 v206,v220,v221
v_mfma_f32_16x16x32_bf16 a[60:63],a[252:255],v[172:175],a[60:63]
v_exp_f32 v222,v222
v_mfma_f32_16x16x32_bf16 a[60:63],a[248:251],v[168:171],a[60:63]
ds_read_b128 a[180:183],v1 offset:12544
v_smfmac_f32_16x16x64_bf16 v[52:55],v[28:31],v[168:175],v27
buffer_load_dwordx4 v5,s[20:23],s47 offen offset:2048 nt lds
ds_read_b128 a[176:179],v1 offset:12288
v_add_u32 v1,s48,v1
v_exp_f32 v223,v223
s_nop 0
v_cvt_pk_bf16_f32 v207,v222,v223
v_exp_f32 v232,v232
v_exp_f32 v233,v233
v_exp_f32 v234,v234
v_exp_f32 v235,v235
v_exp_f32 v236,v236
v_exp_f32 v237,v237
v_exp_f32 v238,v238
v_exp_f32 v239,v239
v_exp_f32 v248,v248
buffer_load_dwordx4 v5,s[20:23],s47 offen offset:3072 nt lds
v_exp_f32 v249,v249
v_exp_f32 v250,v250
v_mfma_f32_16x16x32_bf16 a[64:67],a[192:195],v[200:203],a[64:67]
v_exp_f32 v251,v251
v_mfma_f32_16x16x32_bf16 a[64:67],a[196:199],v[204:207],a[64:67]
v_exp_f32 v252,v252
v_mfma_f32_16x16x32_bf16 a[68:71],a[204:207],v[204:207],a[68:71]
v_exp_f32 v253,v253
v_mfma_f32_16x16x32_bf16 a[68:71],a[200:203],v[200:203],a[68:71]
v_exp_f32 v254,v254
v_mfma_f32_16x16x32_bf16 a[72:75],a[208:211],v[200:203],a[72:75]
v_exp_f32 v255,v255
v_mfma_f32_16x16x32_bf16 a[72:75],a[212:215],v[204:207],a[72:75]
v_mfma_f32_16x16x32_bf16 a[76:79],a[220:223],v[204:207],a[76:79]
v_mfma_f32_16x16x32_bf16 a[76:79],a[216:219],v[200:203],a[76:79]
v_mfma_f32_16x16x32_bf16 a[80:83],a[224:227],v[200:203],a[80:83]
s_add_u32 m0,s49,s52
v_cvt_pk_bf16_f32 v232,v232,v233
v_cvt_pk_bf16_f32 v233,v234,v235
v_mfma_f32_16x16x32_bf16 a[80:83],a[228:231],v[204:207],a[80:83]
s_xor_b32 s49,s50,65536
v_cvt_pk_bf16_f32 v234,v236,v237
v_cvt_pk_bf16_f32 v235,v238,v239
v_mfma_f32_16x16x32_bf16 a[84:87],a[236:239],v[204:207],a[84:87]
v_cvt_pk_bf16_f32 v236,v248,v249
v_cvt_pk_bf16_f32 v237,v250,v251
v_mfma_f32_16x16x32_bf16 a[84:87],a[232:235],v[200:203],a[84:87]
buffer_load_dwordx4 v4,s[24:27],s47 offen nt lds
v_mfma_f32_16x16x32_bf16 a[88:91],a[240:243],v[200:203],a[88:91]
v_cvt_pk_bf16_f32 v238,v252,v253
v_cvt_pk_bf16_f32 v239,v254,v255
v_mfma_f32_16x16x32_bf16 a[88:91],a[244:247],v[204:207],a[88:91]
v_mfma_f32_16x16x32_bf16 a[92:95],a[252:255],v[204:207],a[92:95]
v_mfma_f32_16x16x32_bf16 a[92:95],a[248:251],v[200:203],a[92:95]
v_smfmac_f32_16x16x64_bf16 v[56:59],v[28:31],v[200:207],v27
s_waitcnt lgkmcnt(0)
v_mfma_f32_16x16x32_bf16 v[168:171],a[128:131],v[132:135],v[36:39]
v_mfma_f32_16x16x32_bf16 v[168:171],a[132:135],v[164:167],v[168:171]
v_mfma_f32_16x16x32_bf16 v[168:171],a[136:139],v[196:199],v[168:171]
v_mfma_f32_16x16x32_bf16 v[168:171],a[140:143],v[228:231],v[168:171]
v_mfma_f32_16x16x32_bf16 v[172:175],a[156:159],v[228:231],v[36:39]
v_mfma_f32_16x16x32_bf16 v[172:175],a[152:155],v[196:199],v[172:175]
v_mfma_f32_16x16x32_bf16 v[172:175],a[148:151],v[164:167],v[172:175]
v_mfma_f32_16x16x32_bf16 v[172:175],a[144:147],v[132:135],v[172:175]
v_mfma_f32_16x16x32_bf16 v[184:187],a[160:163],v[132:135],v[36:39]
buffer_load_dwordx4 v5,s[24:27],s47 offen offset:1024 nt lds
v_mfma_f32_16x16x32_bf16 v[184:187],a[164:167],v[164:167],v[184:187]
v_mfma_f32_16x16x32_bf16 v[184:187],a[168:171],v[196:199],v[184:187]
v_mfma_f32_16x16x32_bf16 v[184:187],a[172:175],v[228:231],v[184:187]
v_exp_f32 v168,v168
v_mfma_f32_16x16x32_bf16 v[188:191],a[188:191],v[228:231],v[36:39]
v_exp_f32 v169,v169
v_mfma_f32_16x16x32_bf16 v[188:191],a[184:187],v[196:199],v[188:191]
v_exp_f32 v170,v170
v_mfma_f32_16x16x32_bf16 v[188:191],a[180:183],v[164:167],v[188:191]
v_exp_f32 v171,v171
v_mfma_f32_16x16x32_bf16 v[188:191],a[176:179],v[132:135],v[188:191]
)A"
R"A(v_exp_f32 v172,v172
v_mfma_f32_16x16x32_bf16 a[96:99],a[192:195],v[232:235],a[96:99]
ds_read_b64_tr_b16 a[192:193],v2
v_cvt_pk_bf16_f32 v168,v168,v169
v_mfma_f32_16x16x32_bf16 a[96:99],a[196:199],v[236:239],a[96:99]
ds_read_b64_tr_b16 a[194:195],v2 offset:4096
v_cvt_pk_bf16_f32 v169,v170,v171
v_mfma_f32_16x16x32_bf16 a[100:103],a[204:207],v[236:239],a[100:103]
v_exp_f32 v173,v173
v_mfma_f32_16x16x32_bf16 a[100:103],a[200:203],v[232:235],a[100:103]
ds_read_b64_tr_b16 a[196:197],v2 offset:8192
v_cvt_pk_bf16_f32 v170,v172,v173
v_mfma_f32_16x16x32_bf16 a[104:107],a[208:211],v[232:235],a[104:107]
ds_read_b64_tr_b16 a[198:199],v2 offset:12288
v_mfma_f32_16x16x32_bf16 a[104:107],a[212:215],v[236:239],a[104:107]
v_exp_f32 v174,v174
v_mfma_f32_16x16x32_bf16 a[108:111],a[220:223],v[236:239],a[108:111]
buffer_load_dwordx4 v4,s[24:27],s47 offen offset:2048 nt lds
v_mfma_f32_16x16x32_bf16 a[108:111],a[216:219],v[232:235],a[108:111]
ds_read_b64_tr_b16 a[204:205],v3 offset:8192
v_mfma_f32_16x16x32_bf16 a[112:115],a[224:227],v[232:235],a[112:115]
v_exp_f32 v175,v175
v_mfma_f32_16x16x32_bf16 a[112:115],a[228:231],v[236:239],a[112:115]
ds_read_b64_tr_b16 a[206:207],v3 offset:12288
v_cvt_pk_bf16_f32 v171,v174,v175
v_mfma_f32_16x16x32_bf16 a[116:119],a[236:239],v[236:239],a[116:119]
ds_read_b64_tr_b16 a[200:201],v3
v_mfma_f32_16x16x32_bf16 a[116:119],a[232:235],v[232:235],a[116:119]
ds_read_b64_tr_b16 a[202:203],v3 offset:4096
v_mfma_f32_16x16x32_bf16 a[120:123],a[240:243],v[232:235],a[120:123]
ds_read_b64_tr_b16 a[208:209],v2 offset:256
v_mfma_f32_16x16x32_bf16 a[120:123],a[244:247],v[236:239],a[120:123]
ds_read_b64_tr_b16 a[210:211],v2 offset:4352
v_mfma_f32_16x16x32_bf16 a[124:127],a[252:255],v[236:239],a[124:127]
ds_read_b64_tr_b16 a[212:213],v2 offset:8448
v_mfma_f32_16x16x32_bf16 a[124:127],a[248:251],v[232:235],a[124:127]
ds_read_b64_tr_b16 a[214:215],v2 offset:12544
v_smfmac_f32_16x16x64_bf16 v[60:63],v[28:31],v[232:239],v27
ds_read_b64_tr_b16 a[220:221],v3 offset:8448
v_mfma_f32_16x16x32_bf16 v[200:203],a[128:131],v[144:147],v[40:43]
ds_read_b64_tr_b16 a[222:223],v3 offset:12544
v_mfma_f32_16x16x32_bf16 v[200:203],a[132:135],v[176:179],v[200:203]
ds_read_b64_tr_b16 a[216:217],v3 offset:256
v_mfma_f32_16x16x32_bf16 v[200:203],a[136:139],v[208:211],v[200:203]
ds_read_b64_tr_b16 a[218:219],v3 offset:4352
v_mfma_f32_16x16x32_bf16 v[200:203],a[140:143],v[240:243],v[200:203]
buffer_load_dwordx4 v5,s[24:27],s47 offen offset:3072 nt lds
v_mfma_f32_16x16x32_bf16 v[204:207],a[156:159],v[240:243],v[40:43]
s_add_u32 s47,s47,16384
s_and_b32 s47,s47,0x3ffff
ds_read_b64_tr_b16 a[224:225],v2 offset:512
v_mfma_f32_16x16x32_bf16 v[204:207],a[152:155],v[208:211],v[204:207]
s_or_b32 s47,s47,s46
ds_read_b64_tr_b16 a[226:227],v2 offset:4608
v_mfma_f32_16x16x32_bf16 v[204:207],a[148:151],v[176:179],v[204:207]
ds_read_b64_tr_b16 a[228:229],v2 offset:8704
v_mfma_f32_16x16x32_bf16 v[204:207],a[144:147],v[144:147],v[204:207]
ds_read_b64_tr_b16 a[230:231],v2 offset:12800
v_mfma_f32_16x16x32_bf16 v[216:219],a[160:163],v[144:147],v[40:43]
ds_read_b64_tr_b16 a[236:237],v3 offset:8704
v_mfma_f32_16x16x32_bf16 v[216:219],a[164:167],v[176:179],v[216:219]
ds_read_b64_tr_b16 a[238:239],v3 offset:12800
v_mfma_f32_16x16x32_bf16 v[216:219],a[168:171],v[208:211],v[216:219]
ds_read_b64_tr_b16 a[232:233],v3 offset:512
v_mfma_f32_16x16x32_bf16 v[216:219],a[172:175],v[240:243],v[216:219]
ds_read_b64_tr_b16 a[234:235],v3 offset:4608
v_mfma_f32_16x16x32_bf16 v[220:223],a[188:191],v[240:243],v[40:43]
ds_read_b64_tr_b16 a[240:241],v2 offset:768
v_mfma_f32_16x16x32_bf16 v[220:223],a[184:187],v[208:211],v[220:223]
ds_read_b64_tr_b16 a[242:243],v2 offset:4864
v_mfma_f32_16x16x32_bf16 v[220:223],a[180:183],v[176:179],v[220:223]
ds_read_b64_tr_b16 a[244:245],v2 offset:8960
v_mfma_f32_16x16x32_bf16 v[220:223],a[176:179],v[144:147],v[220:223]
buffer_load_dword v26,v24,s[88:91],s93 offen
ds_read_b64_tr_b16 a[246:247],v2 offset:13056
ds_read_b64_tr_b16 a[252:253],v3 offset:8960
ds_read_b64_tr_b16 a[254:255],v3 offset:13056
ds_read_b64_tr_b16 a[248:249],v3 offset:768
ds_read_b64_tr_b16 a[250:251],v3 offset:4864
v_exp_f32 v184,v184
v_exp_f32 v185,v185
s_bitcmp1_b32 s45,0
s_cbranch_scc0 L_t31v01_%=
s_mov_b32 m0,s80
s_add_u32 s80,s80,4096
buffer_load_dwordx4 v25,s[24:27],s83 offen nt lds
s_add_u32 s83,s83,4096
L_t31v01_%=:
v_exp_f32 v186,v186
v_exp_f32 v187,v187
v_exp_f32 v188,v188
v_exp_f32 v189,v189
v_exp_f32 v190,v190
v_cvt_pk_bf16_f32 v172,v184,v185
v_exp_f32 v191,v191
v_cvt_pk_bf16_f32 v173,v186,v187
v_cvt_pk_bf16_f32 v174,v188,v189
v_cvt_pk_bf16_f32 v175,v190,v191
s_cmp_lg_u32 s53,0
s_cbranch_scc1 L_att1v0_%=
s_waitcnt lgkmcnt(0)
s_nop 7
s_branch L_att1x_%=
.p2align 5
L_att1v1_%=:
v_mfma_f32_16x16x32_bf16 v[232:235],a[128:131],v[148:151],v[44:47]
s_waitcnt vmcnt(8) lgkmcnt(0)
v_exp_f32 v200,v200
v_mfma_f32_16x16x32_bf16 v[232:235],a[132:135],v[180:183],v[232:235]
s_barrier
s_add_u32 m0,s49,s51
ds_read_b128 a[128:131],v1
v_mfma_f32_16x16x32_bf16 v[232:235],a[136:139],v[212:215],v[232:235]
s_mov_b32 s90,L_out_end_%=-L_pc_%=+128
v_exp_f32 v201,v201
v_mfma_f32_16x16x32_bf16 v[232:235],a[140:143],v[244:247],v[232:235]
s_lshr_b32 s45,s45,1
ds_read_b128 a[132:135],v1 offset:256
v_cvt_pk_bf16_f32 v200,v200,v201
v_mfma_f32_16x16x32_bf16 v[236:239],a[156:159],v[244:247],v[44:47]
s_add_u32 s62,s50,32768
buffer_load_dwordx4 v4,s[20:23],s47 offen nt lds
v_mfma_f32_16x16x32_bf16 v[236:239],a[152:155],v[212:215],v[236:239]
s_and_b32 s62,s62,131071
ds_read_b128 a[136:139],v1 offset:512
v_add_u32 v2,s48,v2
v_mfma_f32_16x16x32_bf16 v[236:239],a[148:151],v[180:183],v[236:239]
s_add_u32 s54,s54,16384
v_exp_f32 v202,v202
v_mfma_f32_16x16x32_bf16 v[236:239],a[144:147],v[148:151],v[236:239]
s_sub_u32 s53,s53,1
ds_read_b128 a[140:143],v1 offset:768
v_add_u32 v3,s48,v3
v_mfma_f32_16x16x32_bf16 v[248:251],a[160:163],v[148:151],v[44:47]
s_sub_u32 s48,s62,s50
v_exp_f32 v203,v203
v_mfma_f32_16x16x32_bf16 v[248:251],a[164:167],v[180:183],v[248:251]
s_and_b32 s46,s54,0xfffc0000
ds_read_b128 a[156:159],v1 offset:4864
v_cvt_pk_bf16_f32 v201,v202,v203
v_mfma_f32_16x16x32_bf16 v[248:251],a[168:171],v[212:215],v[248:251]
s_mov_b32 s50,s62
v_exp_f32 v204,v204
v_mfma_f32_16x16x32_bf16 v[248:251],a[172:175],v[244:247],v[248:251]
v_exp_f32 v205,v205
v_mfma_f32_16x16x32_bf16 v[252:255],a[188:191],v[244:247],v[44:47]
ds_read_b128 a[152:155],v1 offset:4608
v_cvt_pk_bf16_f32 v202,v204,v205
v_mfma_f32_16x16x32_bf16 v[252:255],a[184:187],v[212:215],v[252:255]
v_exp_f32 v206,v206
v_mfma_f32_16x16x32_bf16 v[252:255],a[180:183],v[180:183],v[252:255]
ds_read_b128 a[148:151],v1 offset:4352
v_mfma_f32_16x16x32_bf16 v[252:255],a[176:179],v[148:151],v[252:255]
s_waitcnt lgkmcnt(3)
v_exp_f32 v207,v207
ds_read_b128 a[144:147],v1 offset:4096
v_cvt_pk_bf16_f32 v203,v206,v207
v_exp_f32 v216,v216
buffer_load_dwordx4 v4,s[20:23],s47 offen offset:1024 nt lds
ds_read_b128 a[160:163],v1 offset:8192
v_exp_f32 v217,v217
ds_read_b128 a[164:167],v1 offset:8448
v_cvt_pk_bf16_f32 v204,v216,v217
v_exp_f32 v218,v218
ds_read_b128 a[168:171],v1 offset:8704
v_exp_f32 v219,v219
ds_read_b128 a[172:175],v1 offset:8960
v_cvt_pk_bf16_f32 v205,v218,v219
v_exp_f32 v220,v220
ds_read_b128 a[188:191],v1 offset:13056
v_exp_f32 v221,v221
ds_read_b128 a[184:187],v1 offset:12800
v_cvt_pk_bf16_f32 v206,v220,v221
v_exp_f32 v222,v222
ds_read_b128 a[180:183],v1 offset:12544
v_mfma_f32_16x16x32_bf16 v[136:139],a[128:131],v[128:131],v[32:35]
buffer_load_dwordx4 v5,s[20:23],s47 offen offset:2048 nt lds
v_mfma_f32_16x16x32_bf16 v[136:139],a[132:135],v[160:163],v[136:139]
ds_read_b128 a[176:179],v1 offset:12288
v_add_u32 v1,s48,v1
v_mfma_f32_16x16x32_bf16 v[136:139],a[136:139],v[192:195],v[136:139]
v_exp_f32 v223,v223
v_mfma_f32_16x16x32_bf16 v[136:139],a[140:143],v[224:227],v[136:139]
s_waitcnt lgkmcnt(11)
v_cvt_pk_bf16_f32 v207,v222,v223
v_mfma_f32_16x16x32_bf16 v[140:143],a[156:159],v[224:227],v[32:35]
s_waitcnt lgkmcnt(10)
v_exp_f32 v232,v232
v_mfma_f32_16x16x32_bf16 v[140:143],a[152:155],v[192:195],v[140:143]
s_waitcnt lgkmcnt(9)
v_exp_f32 v233,v233
v_mfma_f32_16x16x32_bf16 v[140:143],a[148:151],v[160:163],v[140:143]
s_waitcnt lgkmcnt(8)
v_exp_f32 v234,v234
v_mfma_f32_16x16x32_bf16 v[140:143],a[144:147],v[128:131],v[140:143]
s_waitcnt lgkmcnt(1)
v_exp_f32 v235,v235
v_mfma_f32_16x16x32_bf16 v[152:155],a[160:163],v[128:131],v[32:35]
v_exp_f32 v236,v236
v_mfma_f32_16x16x32_bf16 v[152:155],a[164:167],v[160:163],v[152:155]
v_exp_f32 v237,v237
v_mfma_f32_16x16x32_bf16 v[152:155],a[168:171],v[192:195],v[152:155]
v_exp_f32 v238,v238
v_mfma_f32_16x16x32_bf16 v[152:155],a[172:175],v[224:227],v[152:155]
v_exp_f32 v239,v239
v_mfma_f32_16x16x32_bf16 v[156:159],a[188:191],v[224:227],v[32:35]
v_exp_f32 v248,v248
v_mfma_f32_16x16x32_bf16 v[156:159],a[184:187],v[192:195],v[156:159]
buffer_load_dwordx4 v5,s[20:23],s47 offen offset:3072 nt lds
v_mfma_f32_16x16x32_bf16 v[156:159],a[180:183],v[160:163],v[156:159]
s_waitcnt lgkmcnt(0)
v_exp_f32 v249,v249
v_mfma_f32_16x16x32_bf16 v[156:159],a[176:179],v[128:131],v[156:159]
v_exp_f32 v250,v250
v_mfma_f32_16x16x32_bf16 a[64:67],a[192:195],v[200:203],a[64:67]
v_exp_f32 v251,v251
v_mfma_f32_16x16x32_bf16 a[64:67],a[196:199],v[204:207],a[64:67]
v_exp_f32 v252,v252
v_mfma_f32_16x16x32_bf16 a[68:71],a[204:207],v[204:207],a[68:71]
v_exp_f32 v253,v253
v_mfma_f32_16x16x32_bf16 a[68:71],a[200:203],v[200:203],a[68:71]
v_exp_f32 v254,v254
v_mfma_f32_16x16x32_bf16 a[72:75],a[208:211],v[200:203],a[72:75]
v_exp_f32 v255,v255
v_mfma_f32_16x16x32_bf16 a[72:75],a[212:215],v[204:207],a[72:75]
v_exp_f32 v136,v136
v_mfma_f32_16x16x32_bf16 a[76:79],a[220:223],v[204:207],a[76:79]
v_exp_f32 v137,v137
v_mfma_f32_16x16x32_bf16 a[76:79],a[216:219],v[200:203],a[76:79]
v_exp_f32 v138,v138
v_mfma_f32_16x16x32_bf16 a[80:83],a[224:227],v[200:203],a[80:83]
s_add_u32 m0,s49,s52
v_cvt_pk_bf16_f32 v232,v232,v233
v_cvt_pk_bf16_f32 v233,v234,v235
v_mfma_f32_16x16x32_bf16 a[80:83],a[228:231],v[204:207],a[80:83]
s_xor_b32 s49,s50,65536
v_cvt_pk_bf16_f32 v234,v236,v237
v_cvt_pk_bf16_f32 v235,v238,v239
v_mfma_f32_16x16x32_bf16 a[84:87],a[236:239],v[204:207],a[84:87]
v_cvt_pk_bf16_f32 v236,v248,v249
v_cvt_pk_bf16_f32 v237,v250,v251
v_mfma_f32_16x16x32_bf16 a[84:87],a[232:235],v[200:203],a[84:87]
buffer_load_dwordx4 v4,s[24:27],s47 offen nt lds
v_mfma_f32_16x16x32_bf16 a[88:91],a[240:243],v[200:203],a[88:91]
v_cvt_pk_bf16_f32 v238,v252,v253
v_cvt_pk_bf16_f32 v239,v254,v255
v_mfma_f32_16x16x32_bf16 a[88:91],a[244:247],v[204:207],a[88:91]
v_exp_f32 v139,v139
v_mfma_f32_16x16x32_bf16 a[92:95],a[252:255],v[204:207],a[92:95]
v_exp_f32 v140,v140
v_mfma_f32_16x16x32_bf16 a[92:95],a[248:251],v[200:203],a[92:95]
v_exp_f32 v141,v141
v_smfmac_f32_16x16x64_bf16 v[56:59],v[28:31],v[200:207],v27
v_exp_f32 v142,v142
v_exp_f32 v143,v143
v_exp_f32 v152,v152
v_exp_f32 v153,v153
v_exp_f32 v154,v154
v_exp_f32 v155,v155
v_exp_f32 v156,v156
v_exp_f32 v157,v157
buffer_load_dwordx4 v5,s[24:27],s47 offen offset:1024 nt lds
v_exp_f32 v158,v158
v_exp_f32 v159,v159
v_mfma_f32_16x16x32_bf16 a[96:99],a[192:195],v[232:235],a[96:99]
ds_read_b64_tr_b16 a[192:193],v2
v_mfma_f32_16x16x32_bf16 a[96:99],a[196:199],v[236:239],a[96:99]
ds_read_b64_tr_b16 a[194:195],v2 offset:4096
v_mfma_f32_16x16x32_bf16 a[100:103],a[204:207],v[236:239],a[100:103]
v_mfma_f32_16x16x32_bf16 a[100:103],a[200:203],v[232:235],a[100:103]
ds_read_b64_tr_b16 a[196:197],v2 offset:8192
v_mfma_f32_16x16x32_bf16 a[104:107],a[208:211],v[232:235],a[104:107]
ds_read_b64_tr_b16 a[198:199],v2 offset:12288
v_cvt_pk_bf16_f32 v136,v136,v137
v_mfma_f32_16x16x32_bf16 a[104:107],a[212:215],v[236:239],a[104:107]
v_mfma_f32_16x16x32_bf16 a[108:111],a[220:223],v[236:239],a[108:111]
buffer_load_dwordx4 v4,s[24:27],s47 offen offset:2048 nt lds
v_mfma_f32_16x16x32_bf16 a[108:111],a[216:219],v[232:235],a[108:111]
ds_read_b64_tr_b16 a[204:205],v3 offset:8192
v_cvt_pk_bf16_f32 v137,v138,v139
v_mfma_f32_16x16x32_bf16 a[112:115],a[224:227],v[232:235],a[112:115]
v_mfma_f32_16x16x32_bf16 a[112:115],a[228:231],v[236:239],a[112:115]
ds_read_b64_tr_b16 a[206:207],v3 offset:12288
)A"
R"A(v_mfma_f32_16x16x32_bf16 a[116:119],a[236:239],v[236:239],a[116:119]
ds_read_b64_tr_b16 a[200:201],v3
v_cvt_pk_bf16_f32 v138,v140,v141
v_mfma_f32_16x16x32_bf16 a[116:119],a[232:235],v[232:235],a[116:119]
ds_read_b64_tr_b16 a[202:203],v3 offset:4096
v_mfma_f32_16x16x32_bf16 a[120:123],a[240:243],v[232:235],a[120:123]
ds_read_b64_tr_b16 a[208:209],v2 offset:256
v_mfma_f32_16x16x32_bf16 a[120:123],a[244:247],v[236:239],a[120:123]
ds_read_b64_tr_b16 a[210:211],v2 offset:4352
v_cvt_pk_bf16_f32 v139,v142,v143
v_mfma_f32_16x16x32_bf16 a[124:127],a[252:255],v[236:239],a[124:127]
ds_read_b64_tr_b16 a[212:213],v2 offset:8448
v_mfma_f32_16x16x32_bf16 a[124:127],a[248:251],v[232:235],a[124:127]
ds_read_b64_tr_b16 a[214:215],v2 offset:12544
v_smfmac_f32_16x16x64_bf16 v[60:63],v[28:31],v[232:239],v27
ds_read_b64_tr_b16 a[220:221],v3 offset:8448
v_mfma_f32_16x16x32_bf16 v[200:203],a[128:131],v[144:147],v[40:43]
ds_read_b64_tr_b16 a[222:223],v3 offset:12544
v_cvt_pk_bf16_f32 v140,v152,v153
v_mfma_f32_16x16x32_bf16 v[200:203],a[132:135],v[176:179],v[200:203]
ds_read_b64_tr_b16 a[216:217],v3 offset:256
v_mfma_f32_16x16x32_bf16 v[200:203],a[136:139],v[208:211],v[200:203]
ds_read_b64_tr_b16 a[218:219],v3 offset:4352
v_cvt_pk_bf16_f32 v141,v154,v155
v_mfma_f32_16x16x32_bf16 v[200:203],a[140:143],v[240:243],v[200:203]
buffer_load_dwordx4 v5,s[24:27],s47 offen offset:3072 nt lds
v_mfma_f32_16x16x32_bf16 v[204:207],a[156:159],v[240:243],v[40:43]
s_add_u32 s47,s47,16384
s_and_b32 s47,s47,0x3ffff
ds_read_b64_tr_b16 a[224:225],v2 offset:512
v_mfma_f32_16x16x32_bf16 v[204:207],a[152:155],v[208:211],v[204:207]
s_or_b32 s47,s47,s46
ds_read_b64_tr_b16 a[226:227],v2 offset:4608
v_cvt_pk_bf16_f32 v142,v156,v157
v_mfma_f32_16x16x32_bf16 v[204:207],a[148:151],v[176:179],v[204:207]
ds_read_b64_tr_b16 a[228:229],v2 offset:8704
v_mfma_f32_16x16x32_bf16 v[204:207],a[144:147],v[144:147],v[204:207]
ds_read_b64_tr_b16 a[230:231],v2 offset:12800
v_mfma_f32_16x16x32_bf16 v[216:219],a[160:163],v[144:147],v[40:43]
ds_read_b64_tr_b16 a[236:237],v3 offset:8704
v_cvt_pk_bf16_f32 v143,v158,v159
v_mfma_f32_16x16x32_bf16 v[216:219],a[164:167],v[176:179],v[216:219]
ds_read_b64_tr_b16 a[238:239],v3 offset:12800
v_mfma_f32_16x16x32_bf16 v[216:219],a[168:171],v[208:211],v[216:219]
ds_read_b64_tr_b16 a[232:233],v3 offset:512
v_mfma_f32_16x16x32_bf16 v[216:219],a[172:175],v[240:243],v[216:219]
ds_read_b64_tr_b16 a[234:235],v3 offset:4608
v_mfma_f32_16x16x32_bf16 v[220:223],a[188:191],v[240:243],v[40:43]
ds_read_b64_tr_b16 a[240:241],v2 offset:768
v_mfma_f32_16x16x32_bf16 v[220:223],a[184:187],v[208:211],v[220:223]
ds_read_b64_tr_b16 a[242:243],v2 offset:4864
v_mfma_f32_16x16x32_bf16 v[220:223],a[180:183],v[176:179],v[220:223]
ds_read_b64_tr_b16 a[244:245],v2 offset:8960
v_mfma_f32_16x16x32_bf16 v[220:223],a[176:179],v[144:147],v[220:223]
s_waitcnt lgkmcnt(7)
buffer_load_dword v26,v24,s[88:91],s93 offen
v_mfma_f32_16x16x32_bf16 a[0:3],a[192:195],v[136:139],a[0:3]
ds_read_b64_tr_b16 a[246:247],v2 offset:13056
v_mfma_f32_16x16x32_bf16 a[0:3],a[196:199],v[140:143],a[0:3]
ds_read_b64_tr_b16 a[252:253],v3 offset:8960
v_mfma_f32_16x16x32_bf16 a[4:7],a[204:207],v[140:143],a[4:7]
ds_read_b64_tr_b16 a[254:255],v3 offset:13056
v_mfma_f32_16x16x32_bf16 a[4:7],a[200:203],v[136:139],a[4:7]
ds_read_b64_tr_b16 a[248:249],v3 offset:768
v_mfma_f32_16x16x32_bf16 a[8:11],a[208:211],v[136:139],a[8:11]
ds_read_b64_tr_b16 a[250:251],v3 offset:4864
v_mfma_f32_16x16x32_bf16 a[8:11],a[212:215],v[140:143],a[8:11]
v_mfma_f32_16x16x32_bf16 a[12:15],a[220:223],v[140:143],a[12:15]
v_mfma_f32_16x16x32_bf16 a[12:15],a[216:219],v[136:139],a[12:15]
s_bitcmp1_b32 s45,0
s_cbranch_scc0 L_t31v11_%=
s_mov_b32 m0,s80
s_add_u32 s80,s80,4096
buffer_load_dwordx4 v25,s[24:27],s83 offen nt lds
s_add_u32 s83,s83,4096
L_t31v11_%=:
v_mfma_f32_16x16x32_bf16 a[16:19],a[224:227],v[136:139],a[16:19]
v_mfma_f32_16x16x32_bf16 a[16:19],a[228:231],v[140:143],a[16:19]
s_waitcnt lgkmcnt(3)
v_mfma_f32_16x16x32_bf16 a[20:23],a[236:239],v[140:143],a[20:23]
v_mfma_f32_16x16x32_bf16 a[20:23],a[232:235],v[136:139],a[20:23]
v_mfma_f32_16x16x32_bf16 a[24:27],a[240:243],v[136:139],a[24:27]
v_mfma_f32_16x16x32_bf16 a[24:27],a[244:247],v[140:143],a[24:27]
s_waitcnt lgkmcnt(0)
v_mfma_f32_16x16x32_bf16 a[28:31],a[252:255],v[140:143],a[28:31]
v_mfma_f32_16x16x32_bf16 a[28:31],a[248:251],v[136:139],a[28:31]
s_cmp_lg_u32 s53,0
v_smfmac_f32_16x16x64_bf16 v[48:51],v[28:31],v[136:143],v27
s_cbranch_scc1 L_att1v1_%=
s_waitcnt lgkmcnt(0)
s_nop 7
s_branch L_att1x_%=
.p2align 5
L_att1v2_%=:
v_mfma_f32_16x16x32_bf16 v[232:235],a[128:131],v[148:151],v[44:47]
s_waitcnt vmcnt(8) lgkmcnt(0)
v_mfma_f32_16x16x32_bf16 v[232:235],a[132:135],v[180:183],v[232:235]
s_barrier
s_add_u32 m0,s49,s51
ds_read_b128 a[128:131],v1
v_mfma_f32_16x16x32_bf16 v[232:235],a[136:139],v[212:215],v[232:235]
s_mov_b32 s90,L_out_end_%=-L_pc_%=+128
v_mfma_f32_16x16x32_bf16 v[232:235],a[140:143],v[244:247],v[232:235]
s_lshr_b32 s45,s45,1
ds_read_b128 a[132:135],v1 offset:256
v_mfma_f32_16x16x32_bf16 v[236:239],a[156:159],v[244:247],v[44:47]
s_add_u32 s62,s50,32768
buffer_load_dwordx4 v4,s[20:23],s47 offen nt lds
v_mfma_f32_16x16x32_bf16 v[236:239],a[152:155],v[212:215],v[236:239]
s_and_b32 s62,s62,131071
ds_read_b128 a[136:139],v1 offset:512
v_add_u32 v2,s48,v2
v_mfma_f32_16x16x32_bf16 v[236:239],a[148:151],v[180:183],v[236:239]
s_add_u32 s54,s54,16384
v_mfma_f32_16x16x32_bf16 v[236:239],a[144:147],v[148:151],v[236:239]
s_sub_u32 s53,s53,1
ds_read_b128 a[140:143],v1 offset:768
v_add_u32 v3,s48,v3
v_mfma_f32_16x16x32_bf16 v[248:251],a[160:163],v[148:151],v[44:47]
s_sub_u32 s48,s62,s50
v_mfma_f32_16x16x32_bf16 v[248:251],a[164:167],v[180:183],v[248:251]
s_and_b32 s46,s54,0xfffc0000
ds_read_b128 a[156:159],v1 offset:4864
v_mfma_f32_16x16x32_bf16 v[248:251],a[168:171],v[212:215],v[248:251]
s_mov_b32 s50,s62
v_mfma_f32_16x16x32_bf16 v[248:251],a[172:175],v[244:247],v[248:251]
v_mfma_f32_16x16x32_bf16 v[252:255],a[188:191],v[244:247],v[44:47]
ds_read_b128 a[152:155],v1 offset:4608
v_mfma_f32_16x16x32_bf16 v[252:255],a[184:187],v[212:215],v[252:255]
v_mfma_f32_16x16x32_bf16 v[252:255],a[180:183],v[180:183],v[252:255]
ds_read_b128 a[148:151],v1 offset:4352
v_mfma_f32_16x16x32_bf16 v[252:255],a[176:179],v[148:151],v[252:255]
v_mfma_f32_16x16x32_bf16 a[32:35],a[192:195],v[168:171],a[32:35]
ds_read_b128 a[144:147],v1 offset:4096
v_mfma_f32_16x16x32_bf16 a[32:35],a[196:199],v[172:175],a[32:35]
v_mfma_f32_16x16x32_bf16 a[36:39],a[204:207],v[172:175],a[36:39]
buffer_load_dwordx4 v4,s[20:23],s47 offen offset:1024 nt lds
v_mfma_f32_16x16x32_bf16 a[36:39],a[200:203],v[168:171],a[36:39]
ds_read_b128 a[160:163],v1 offset:8192
v_mfma_f32_16x16x32_bf16 a[40:43],a[208:211],v[168:171],a[40:43]
v_mfma_f32_16x16x32_bf16 a[40:43],a[212:215],v[172:175],a[40:43]
ds_read_b128 a[164:167],v1 offset:8448
v_mfma_f32_16x16x32_bf16 a[44:47],a[220:223],v[172:175],a[44:47]
v_mfma_f32_16x16x32_bf16 a[44:47],a[216:219],v[168:171],a[44:47]
ds_read_b128 a[168:171],v1 offset:8704
v_mfma_f32_16x16x32_bf16 a[48:51],a[224:227],v[168:171],a[48:51]
v_mfma_f32_16x16x32_bf16 a[48:51],a[228:231],v[172:175],a[48:51]
ds_read_b128 a[172:175],v1 offset:8960
v_mfma_f32_16x16x32_bf16 a[52:55],a[236:239],v[172:175],a[52:55]
v_mfma_f32_16x16x32_bf16 a[52:55],a[232:235],v[168:171],a[52:55]
ds_read_b128 a[188:191],v1 offset:13056
v_mfma_f32_16x16x32_bf16 a[56:59],a[240:243],v[168:171],a[56:59]
v_mfma_f32_16x16x32_bf16 a[56:59],a[244:247],v[172:175],a[56:59]
ds_read_b128 a[184:187],v1 offset:12800
v_mfma_f32_16x16x32_bf16 a[60:63],a[252:255],v[172:175],a[60:63]
v_mfma_f32_16x16x32_bf16 a[60:63],a[248:251],v[168:171],a[60:63]
ds_read_b128 a[180:183],v1 offset:12544
v_smfmac_f32_16x16x64_bf16 v[52:55],v[28:31],v[168:175],v27
s_waitcnt lgkmcnt(4)
v_mfma_f32_16x16x32_bf16 v[136:139],a[128:131],v[128:131],v[32:35]
buffer_load_dwordx4 v5,s[20:23],s47 offen offset:2048 nt lds
v_mfma_f32_16x16x32_bf16 v[136:139],a[132:135],v[160:163],v[136:139]
ds_read_b128 a[176:179],v1 offset:12288
v_add_u32 v1,s48,v1
v_mfma_f32_16x16x32_bf16 v[136:139],a[136:139],v[192:195],v[136:139]
v_mfma_f32_16x16x32_bf16 v[136:139],a[140:143],v[224:227],v[136:139]
v_mfma_f32_16x16x32_bf16 v[140:143],a[156:159],v[224:227],v[32:35]
v_exp_f32 v232,v232
v_mfma_f32_16x16x32_bf16 v[140:143],a[152:155],v[192:195],v[140:143]
v_exp_f32 v233,v233
v_mfma_f32_16x16x32_bf16 v[140:143],a[148:151],v[160:163],v[140:143]
v_exp_f32 v234,v234
v_mfma_f32_16x16x32_bf16 v[140:143],a[144:147],v[128:131],v[140:143]
v_exp_f32 v235,v235
v_mfma_f32_16x16x32_bf16 v[152:155],a[160:163],v[128:131],v[32:35]
v_exp_f32 v236,v236
v_mfma_f32_16x16x32_bf16 v[152:155],a[164:167],v[160:163],v[152:155]
v_exp_f32 v237,v237
v_mfma_f32_16x16x32_bf16 v[152:155],a[168:171],v[192:195],v[152:155]
s_waitcnt lgkmcnt(0)
v_exp_f32 v238,v238
v_mfma_f32_16x16x32_bf16 v[152:155],a[172:175],v[224:227],v[152:155]
v_exp_f32 v239,v239
v_mfma_f32_16x16x32_bf16 v[156:159],a[188:191],v[224:227],v[32:35]
v_exp_f32 v248,v248
v_mfma_f32_16x16x32_bf16 v[156:159],a[184:187],v[192:195],v[156:159]
buffer_load_dwordx4 v5,s[20:23],s47 offen offset:3072 nt lds
v_mfma_f32_16x16x32_bf16 v[156:159],a[180:183],v[160:163],v[156:159]
v_exp_f32 v249,v249
v_mfma_f32_16x16x32_bf16 v[156:159],a[176:179],v[128:131],v[156:159]
v_exp_f32 v250,v250
v_exp_f32 v251,v251
v_exp_f32 v252,v252
v_exp_f32 v253,v253
v_exp_f32 v254,v254
v_exp_f32 v255,v255
v_exp_f32 v136,v136
v_exp_f32 v137,v137
v_exp_f32 v138,v138
s_add_u32 m0,s49,s52
v_cvt_pk_bf16_f32 v232,v232,v233
v_cvt_pk_bf16_f32 v233,v234,v235
s_xor_b32 s49,s50,65536
v_cvt_pk_bf16_f32 v234,v236,v237
v_cvt_pk_bf16_f32 v235,v238,v239
v_cvt_pk_bf16_f32 v236,v248,v249
v_cvt_pk_bf16_f32 v237,v250,v251
buffer_load_dwordx4 v4,s[24:27],s47 offen nt lds
v_cvt_pk_bf16_f32 v238,v252,v253
v_cvt_pk_bf16_f32 v239,v254,v255
v_exp_f32 v139,v139
v_exp_f32 v140,v140
v_exp_f32 v141,v141
v_mfma_f32_16x16x32_bf16 v[168:171],a[128:131],v[132:135],v[36:39]
v_exp_f32 v142,v142
v_mfma_f32_16x16x32_bf16 v[168:171],a[132:135],v[164:167],v[168:171]
v_exp_f32 v143,v143
v_mfma_f32_16x16x32_bf16 v[168:171],a[136:139],v[196:199],v[168:171]
v_exp_f32 v152,v152
v_mfma_f32_16x16x32_bf16 v[168:171],a[140:143],v[228:231],v[168:171]
v_exp_f32 v153,v153
v_mfma_f32_16x16x32_bf16 v[172:175],a[156:159],v[228:231],v[36:39]
v_exp_f32 v154,v154
v_mfma_f32_16x16x32_bf16 v[172:175],a[152:155],v[196:199],v[172:175]
v_exp_f32 v155,v155
v_mfma_f32_16x16x32_bf16 v[172:175],a[148:151],v[164:167],v[172:175]
v_exp_f32 v156,v156
v_mfma_f32_16x16x32_bf16 v[172:175],a[144:147],v[132:135],v[172:175]
v_exp_f32 v157,v157
v_mfma_f32_16x16x32_bf16 v[184:187],a[160:163],v[132:135],v[36:39]
buffer_load_dwordx4 v5,s[24:27],s47 offen offset:1024 nt lds
v_mfma_f32_16x16x32_bf16 v[184:187],a[164:167],v[164:167],v[184:187]
v_exp_f32 v158,v158
v_mfma_f32_16x16x32_bf16 v[184:187],a[168:171],v[196:199],v[184:187]
v_exp_f32 v159,v159
v_mfma_f32_16x16x32_bf16 v[184:187],a[172:175],v[228:231],v[184:187]
v_exp_f32 v168,v168
v_mfma_f32_16x16x32_bf16 v[188:191],a[188:191],v[228:231],v[36:39]
v_exp_f32 v169,v169
v_mfma_f32_16x16x32_bf16 v[188:191],a[184:187],v[196:199],v[188:191]
v_exp_f32 v170,v170
v_mfma_f32_16x16x32_bf16 v[188:191],a[180:183],v[164:167],v[188:191]
v_exp_f32 v171,v171
v_mfma_f32_16x16x32_bf16 v[188:191],a[176:179],v[132:135],v[188:191]
v_exp_f32 v172,v172
v_mfma_f32_16x16x32_bf16 a[96:99],a[192:195],v[232:235],a[96:99]
ds_read_b64_tr_b16 a[192:193],v2
v_cvt_pk_bf16_f32 v168,v168,v169
v_mfma_f32_16x16x32_bf16 a[96:99],a[196:199],v[236:239],a[96:99]
ds_read_b64_tr_b16 a[194:195],v2 offset:4096
v_cvt_pk_bf16_f32 v169,v170,v171
v_mfma_f32_16x16x32_bf16 a[100:103],a[204:207],v[236:239],a[100:103]
v_exp_f32 v173,v173
v_mfma_f32_16x16x32_bf16 a[100:103],a[200:203],v[232:235],a[100:103]
ds_read_b64_tr_b16 a[196:197],v2 offset:8192
v_cvt_pk_bf16_f32 v170,v172,v173
v_mfma_f32_16x16x32_bf16 a[104:107],a[208:211],v[232:235],a[104:107]
ds_read_b64_tr_b16 a[198:199],v2 offset:12288
v_cvt_pk_bf16_f32 v136,v136,v137
v_mfma_f32_16x16x32_bf16 a[104:107],a[212:215],v[236:239],a[104:107]
)A"
R"A(v_exp_f32 v174,v174
v_mfma_f32_16x16x32_bf16 a[108:111],a[220:223],v[236:239],a[108:111]
buffer_load_dwordx4 v4,s[24:27],s47 offen offset:2048 nt lds
v_mfma_f32_16x16x32_bf16 a[108:111],a[216:219],v[232:235],a[108:111]
ds_read_b64_tr_b16 a[204:205],v3 offset:8192
v_cvt_pk_bf16_f32 v137,v138,v139
v_mfma_f32_16x16x32_bf16 a[112:115],a[224:227],v[232:235],a[112:115]
v_exp_f32 v175,v175
v_mfma_f32_16x16x32_bf16 a[112:115],a[228:231],v[236:239],a[112:115]
ds_read_b64_tr_b16 a[206:207],v3 offset:12288
v_cvt_pk_bf16_f32 v171,v174,v175
v_mfma_f32_16x16x32_bf16 a[116:119],a[236:239],v[236:239],a[116:119]
ds_read_b64_tr_b16 a[200:201],v3
v_cvt_pk_bf16_f32 v138,v140,v141
v_mfma_f32_16x16x32_bf16 a[116:119],a[232:235],v[232:235],a[116:119]
ds_read_b64_tr_b16 a[202:203],v3 offset:4096
v_mfma_f32_16x16x32_bf16 a[120:123],a[240:243],v[232:235],a[120:123]
ds_read_b64_tr_b16 a[208:209],v2 offset:256
v_mfma_f32_16x16x32_bf16 a[120:123],a[244:247],v[236:239],a[120:123]
ds_read_b64_tr_b16 a[210:211],v2 offset:4352
v_cvt_pk_bf16_f32 v139,v142,v143
v_mfma_f32_16x16x32_bf16 a[124:127],a[252:255],v[236:239],a[124:127]
ds_read_b64_tr_b16 a[212:213],v2 offset:8448
v_mfma_f32_16x16x32_bf16 a[124:127],a[248:251],v[232:235],a[124:127]
ds_read_b64_tr_b16 a[214:215],v2 offset:12544
v_smfmac_f32_16x16x64_bf16 v[60:63],v[28:31],v[232:239],v27
s_waitcnt lgkmcnt(7)
ds_read_b64_tr_b16 a[220:221],v3 offset:8448
ds_read_b64_tr_b16 a[222:223],v3 offset:12544
v_cvt_pk_bf16_f32 v140,v152,v153
ds_read_b64_tr_b16 a[216:217],v3 offset:256
ds_read_b64_tr_b16 a[218:219],v3 offset:4352
v_cvt_pk_bf16_f32 v141,v154,v155
buffer_load_dwordx4 v5,s[24:27],s47 offen offset:3072 nt lds
s_add_u32 s47,s47,16384
s_and_b32 s47,s47,0x3ffff
ds_read_b64_tr_b16 a[224:225],v2 offset:512
s_or_b32 s47,s47,s46
ds_read_b64_tr_b16 a[226:227],v2 offset:4608
v_cvt_pk_bf16_f32 v142,v156,v157
ds_read_b64_tr_b16 a[228:229],v2 offset:8704
ds_read_b64_tr_b16 a[230:231],v2 offset:12800
ds_read_b64_tr_b16 a[236:237],v3 offset:8704
v_cvt_pk_bf16_f32 v143,v158,v159
ds_read_b64_tr_b16 a[238:239],v3 offset:12800
ds_read_b64_tr_b16 a[232:233],v3 offset:512
ds_read_b64_tr_b16 a[234:235],v3 offset:4608
ds_read_b64_tr_b16 a[240:241],v2 offset:768
ds_read_b64_tr_b16 a[242:243],v2 offset:4864
ds_read_b64_tr_b16 a[244:245],v2 offset:8960
buffer_load_dword v26,v24,s[88:91],s93 offen
v_mfma_f32_16x16x32_bf16 a[0:3],a[192:195],v[136:139],a[0:3]
ds_read_b64_tr_b16 a[246:247],v2 offset:13056
v_mfma_f32_16x16x32_bf16 a[0:3],a[196:199],v[140:143],a[0:3]
s_waitcnt lgkmcnt(15)
ds_read_b64_tr_b16 a[252:253],v3 offset:8960
v_mfma_f32_16x16x32_bf16 a[4:7],a[204:207],v[140:143],a[4:7]
ds_read_b64_tr_b16 a[254:255],v3 offset:13056
v_mfma_f32_16x16x32_bf16 a[4:7],a[200:203],v[136:139],a[4:7]
ds_read_b64_tr_b16 a[248:249],v3 offset:768
v_mfma_f32_16x16x32_bf16 a[8:11],a[208:211],v[136:139],a[8:11]
ds_read_b64_tr_b16 a[250:251],v3 offset:4864
v_mfma_f32_16x16x32_bf16 a[8:11],a[212:215],v[140:143],a[8:11]
s_waitcnt lgkmcnt(15)
v_exp_f32 v184,v184
v_mfma_f32_16x16x32_bf16 a[12:15],a[220:223],v[140:143],a[12:15]
v_exp_f32 v185,v185
v_mfma_f32_16x16x32_bf16 a[12:15],a[216:219],v[136:139],a[12:15]
s_bitcmp1_b32 s45,0
s_cbranch_scc0 L_t31v21_%=
s_mov_b32 m0,s80
s_add_u32 s80,s80,4096
buffer_load_dwordx4 v25,s[24:27],s83 offen nt lds
s_add_u32 s83,s83,4096
L_t31v21_%=:
s_waitcnt lgkmcnt(5)
v_mfma_f32_16x16x32_bf16 a[16:19],a[224:227],v[136:139],a[16:19]
v_exp_f32 v186,v186
v_mfma_f32_16x16x32_bf16 a[16:19],a[228:231],v[140:143],a[16:19]
v_exp_f32 v187,v187
v_mfma_f32_16x16x32_bf16 a[20:23],a[236:239],v[140:143],a[20:23]
v_exp_f32 v188,v188
v_mfma_f32_16x16x32_bf16 a[20:23],a[232:235],v[136:139],a[20:23]
v_exp_f32 v189,v189
v_mfma_f32_16x16x32_bf16 a[24:27],a[240:243],v[136:139],a[24:27]
s_waitcnt lgkmcnt(0)
v_exp_f32 v190,v190
v_mfma_f32_16x16x32_bf16 a[24:27],a[244:247],v[140:143],a[24:27]
v_cvt_pk_bf16_f32 v172,v184,v185
v_mfma_f32_16x16x32_bf16 a[28:31],a[252:255],v[140:143],a[28:31]
v_exp_f32 v191,v191
v_mfma_f32_16x16x32_bf16 a[28:31],a[248:251],v[136:139],a[28:31]
v_cvt_pk_bf16_f32 v173,v186,v187
v_cvt_pk_bf16_f32 v174,v188,v189
v_cvt_pk_bf16_f32 v175,v190,v191
s_cmp_lg_u32 s53,0
v_smfmac_f32_16x16x64_bf16 v[48:51],v[28:31],v[136:143],v27
s_cbranch_scc1 L_att1v2_%=
s_waitcnt lgkmcnt(0)
s_nop 7
s_branch L_att1x_%=
.p2align 5
L_att1_%=:
v_mfma_f32_16x16x32_bf16 v[232:235],a[128:131],v[148:151],v[44:47]
s_waitcnt vmcnt(8) lgkmcnt(0)
v_exp_f32 v200,v200
v_mfma_f32_16x16x32_bf16 v[232:235],a[132:135],v[180:183],v[232:235]
s_barrier
s_add_u32 m0,s49,s51
ds_read_b128 a[128:131],v1
v_mfma_f32_16x16x32_bf16 v[232:235],a[136:139],v[212:215],v[232:235]
s_mov_b32 s90,L_out_end_%=-L_pc_%=+128
v_exp_f32 v201,v201
v_mfma_f32_16x16x32_bf16 v[232:235],a[140:143],v[244:247],v[232:235]
s_lshr_b32 s45,s45,1
ds_read_b128 a[132:135],v1 offset:256
v_cvt_pk_bf16_f32 v200,v200,v201
v_mfma_f32_16x16x32_bf16 v[236:239],a[156:159],v[244:247],v[44:47]
s_add_u32 s62,s50,32768
buffer_load_dwordx4 v4,s[20:23],s47 offen nt lds
v_mfma_f32_16x16x32_bf16 v[236:239],a[152:155],v[212:215],v[236:239]
s_and_b32 s62,s62,131071
ds_read_b128 a[136:139],v1 offset:512
v_add_u32 v2,s48,v2
v_mfma_f32_16x16x32_bf16 v[236:239],a[148:151],v[180:183],v[236:239]
s_add_u32 s54,s54,16384
v_exp_f32 v202,v202
v_mfma_f32_16x16x32_bf16 v[236:239],a[144:147],v[148:151],v[236:239]
s_sub_u32 s53,s53,1
ds_read_b128 a[140:143],v1 offset:768
v_add_u32 v3,s48,v3
v_mfma_f32_16x16x32_bf16 v[248:251],a[160:163],v[148:151],v[44:47]
s_sub_u32 s48,s62,s50
v_exp_f32 v203,v203
v_mfma_f32_16x16x32_bf16 v[248:251],a[164:167],v[180:183],v[248:251]
s_and_b32 s46,s54,0xfffc0000
ds_read_b128 a[156:159],v1 offset:4864
v_cvt_pk_bf16_f32 v201,v202,v203
v_mfma_f32_16x16x32_bf16 v[248:251],a[168:171],v[212:215],v[248:251]
s_mov_b32 s50,s62
v_exp_f32 v204,v204
v_mfma_f32_16x16x32_bf16 v[248:251],a[172:175],v[244:247],v[248:251]
v_exp_f32 v205,v205
v_mfma_f32_16x16x32_bf16 v[252:255],a[188:191],v[244:247],v[44:47]
ds_read_b128 a[152:155],v1 offset:4608
v_cvt_pk_bf16_f32 v202,v204,v205
v_mfma_f32_16x16x32_bf16 v[252:255],a[184:187],v[212:215],v[252:255]
v_exp_f32 v206,v206
v_mfma_f32_16x16x32_bf16 v[252:255],a[180:183],v[180:183],v[252:255]
ds_read_b128 a[148:151],v1 offset:4352
v_mfma_f32_16x16x32_bf16 v[252:255],a[176:179],v[148:151],v[252:255]
v_exp_f32 v207,v207
v_mfma_f32_16x16x32_bf16 a[32:35],a[192:195],v[168:171],a[32:35]
ds_read_b128 a[144:147],v1 offset:4096
v_cvt_pk_bf16_f32 v203,v206,v207
v_mfma_f32_16x16x32_bf16 a[32:35],a[196:199],v[172:175],a[32:35]
v_exp_f32 v216,v216
v_mfma_f32_16x16x32_bf16 a[36:39],a[204:207],v[172:175],a[36:39]
buffer_load_dwordx4 v4,s[20:23],s47 offen offset:1024 nt lds
v_mfma_f32_16x16x32_bf16 a[36:39],a[200:203],v[168:171],a[36:39]
ds_read_b128 a[160:163],v1 offset:8192
v_mfma_f32_16x16x32_bf16 a[40:43],a[208:211],v[168:171],a[40:43]
v_exp_f32 v217,v217
v_mfma_f32_16x16x32_bf16 a[40:43],a[212:215],v[172:175],a[40:43]
ds_read_b128 a[164:167],v1 offset:8448
v_cvt_pk_bf16_f32 v204,v216,v217
v_mfma_f32_16x16x32_bf16 a[44:47],a[220:223],v[172:175],a[44:47]
v_exp_f32 v218,v218
v_mfma_f32_16x16x32_bf16 a[44:47],a[216:219],v[168:171],a[44:47]
ds_read_b128 a[168:171],v1 offset:8704
v_mfma_f32_16x16x32_bf16 a[48:51],a[224:227],v[168:171],a[48:51]
v_exp_f32 v219,v219
v_mfma_f32_16x16x32_bf16 a[48:51],a[228:231],v[172:175],a[48:51]
ds_read_b128 a[172:175],v1 offset:8960
v_cvt_pk_bf16_f32 v205,v218,v219
v_mfma_f32_16x16x32_bf16 a[52:55],a[236:239],v[172:175],a[52:55]
v_exp_f32 v220,v220
v_mfma_f32_16x16x32_bf16 a[52:55],a[232:235],v[168:171],a[52:55]
ds_read_b128 a[188:191],v1 offset:13056
v_mfma_f32_16x16x32_bf16 a[56:59],a[240:243],v[168:171],a[56:59]
v_exp_f32 v221,v221
v_mfma_f32_16x16x32_bf16 a[56:59],a[244:247],v[172:175],a[56:59]
ds_read_b128 a[184:187],v1 offset:12800
v_cvt_pk_bf16_f32 v206,v220,v221
v_mfma_f32_16x16x32_bf16 a[60:63],a[252:255],v[172:175],a[60:63]
v_exp_f32 v222,v222
v_mfma_f32_16x16x32_bf16 a[60:63],a[248:251],v[168:171],a[60:63]
ds_read_b128 a[180:183],v1 offset:12544
v_smfmac_f32_16x16x64_bf16 v[52:55],v[28:31],v[168:175],v27
s_waitcnt lgkmcnt(4)
v_mfma_f32_16x16x32_bf16 v[136:139],a[128:131],v[128:131],v[32:35]
buffer_load_dwordx4 v5,s[20:23],s47 offen offset:2048 nt lds
v_mfma_f32_16x16x32_bf16 v[136:139],a[132:135],v[160:163],v[136:139]
ds_read_b128 a[176:179],v1 offset:12288
v_add_u32 v1,s48,v1
v_mfma_f32_16x16x32_bf16 v[136:139],a[136:139],v[192:195],v[136:139]
v_exp_f32 v223,v223
v_mfma_f32_16x16x32_bf16 v[136:139],a[140:143],v[224:227],v[136:139]
v_cvt_pk_bf16_f32 v207,v222,v223
v_mfma_f32_16x16x32_bf16 v[140:143],a[156:159],v[224:227],v[32:35]
v_exp_f32 v232,v232
v_mfma_f32_16x16x32_bf16 v[140:143],a[152:155],v[192:195],v[140:143]
v_exp_f32 v233,v233
v_mfma_f32_16x16x32_bf16 v[140:143],a[148:151],v[160:163],v[140:143]
v_exp_f32 v234,v234
v_mfma_f32_16x16x32_bf16 v[140:143],a[144:147],v[128:131],v[140:143]
v_exp_f32 v235,v235
v_mfma_f32_16x16x32_bf16 v[152:155],a[160:163],v[128:131],v[32:35]
v_exp_f32 v236,v236
v_mfma_f32_16x16x32_bf16 v[152:155],a[164:167],v[160:163],v[152:155]
v_exp_f32 v237,v237
v_mfma_f32_16x16x32_bf16 v[152:155],a[168:171],v[192:195],v[152:155]
s_waitcnt lgkmcnt(0)
v_exp_f32 v238,v238
v_mfma_f32_16x16x32_bf16 v[152:155],a[172:175],v[224:227],v[152:155]
v_exp_f32 v239,v239
v_mfma_f32_16x16x32_bf16 v[156:159],a[188:191],v[224:227],v[32:35]
v_exp_f32 v248,v248
v_mfma_f32_16x16x32_bf16 v[156:159],a[184:187],v[192:195],v[156:159]
buffer_load_dwordx4 v5,s[20:23],s47 offen offset:3072 nt lds
v_mfma_f32_16x16x32_bf16 v[156:159],a[180:183],v[160:163],v[156:159]
v_exp_f32 v249,v249
v_mfma_f32_16x16x32_bf16 v[156:159],a[176:179],v[128:131],v[156:159]
v_exp_f32 v250,v250
v_mfma_f32_16x16x32_bf16 a[64:67],a[192:195],v[200:203],a[64:67]
v_exp_f32 v251,v251
v_mfma_f32_16x16x32_bf16 a[64:67],a[196:199],v[204:207],a[64:67]
v_exp_f32 v252,v252
v_mfma_f32_16x16x32_bf16 a[68:71],a[204:207],v[204:207],a[68:71]
v_exp_f32 v253,v253
v_mfma_f32_16x16x32_bf16 a[68:71],a[200:203],v[200:203],a[68:71]
v_exp_f32 v254,v254
v_mfma_f32_16x16x32_bf16 a[72:75],a[208:211],v[200:203],a[72:75]
v_exp_f32 v255,v255
v_mfma_f32_16x16x32_bf16 a[72:75],a[212:215],v[204:207],a[72:75]
v_exp_f32 v136,v136
v_mfma_f32_16x16x32_bf16 a[76:79],a[220:223],v[204:207],a[76:79]
v_exp_f32 v137,v137
v_mfma_f32_16x16x32_bf16 a[76:79],a[216:219],v[200:203],a[76:79]
v_exp_f32 v138,v138
v_mfma_f32_16x16x32_bf16 a[80:83],a[224:227],v[200:203],a[80:83]
s_add_u32 m0,s49,s52
v_cvt_pk_bf16_f32 v232,v232,v233
v_cvt_pk_bf16_f32 v233,v234,v235
v_mfma_f32_16x16x32_bf16 a[80:83],a[228:231],v[204:207],a[80:83]
s_xor_b32 s49,s50,65536
v_cvt_pk_bf16_f32 v234,v236,v237
v_cvt_pk_bf16_f32 v235,v238,v239
v_mfma_f32_16x16x32_bf16 a[84:87],a[236:239],v[204:207],a[84:87]
v_cvt_pk_bf16_f32 v236,v248,v249
v_cvt_pk_bf16_f32 v237,v250,v251
v_mfma_f32_16x16x32_bf16 a[84:87],a[232:235],v[200:203],a[84:87]
buffer_load_dwordx4 v4,s[24:27],s47 offen nt lds
v_mfma_f32_16x16x32_bf16 a[88:91],a[240:243],v[200:203],a[88:91]
v_cvt_pk_bf16_f32 v238,v252,v253
v_cvt_pk_bf16_f32 v239,v254,v255
v_mfma_f32_16x16x32_bf16 a[88:91],a[244:247],v[204:207],a[88:91]
v_exp_f32 v139,v139
v_mfma_f32_16x16x32_bf16 a[92:95],a[252:255],v[204:207],a[92:95]
v_exp_f32 v140,v140
v_mfma_f32_16x16x32_bf16 a[92:95],a[248:251],v[200:203],a[92:95]
v_exp_f32 v141,v141
v_smfmac_f32_16x16x64_bf16 v[56:59],v[28:31],v[200:207],v27
v_mfma_f32_16x16x32_bf16 v[168:171],a[128:131],v[132:135],v[36:39]
v_exp_f32 v142,v142
v_mfma_f32_16x16x32_bf16 v[168:171],a[132:135],v[164:167],v[168:171]
v_exp_f32 v143,v143
v_mfma_f32_16x16x32_bf16 v[168:171],a[136:139],v[196:199],v[168:171]
v_exp_f32 v152,v152
v_mfma_f32_16x16x32_bf16 v[168:171],a[140:143],v[228:231],v[168:171]
v_exp_f32 v153,v153
v_mfma_f32_16x16x32_bf16 v[172:175],a[156:159],v[228:231],v[36:39]
v_exp_f32 v154,v154
v_mfma_f32_16x16x32_bf16 v[172:175],a[152:155],v[196:199],v[172:175]
v_exp_f32 v155,v155
v_mfma_f32_16x16x32_bf16 v[172:175],a[148:151],v[164:167],v[172:175]
v_exp_f32 v156,v156
v_mfma_f32_16x16x32_bf16 v[172:175],a[144:147],v[132:135],v[172:175]
)A"
R"A(v_exp_f32 v157,v157
v_mfma_f32_16x16x32_bf16 v[184:187],a[160:163],v[132:135],v[36:39]
buffer_load_dwordx4 v5,s[24:27],s47 offen offset:1024 nt lds
v_mfma_f32_16x16x32_bf16 v[184:187],a[164:167],v[164:167],v[184:187]
v_exp_f32 v158,v158
v_mfma_f32_16x16x32_bf16 v[184:187],a[168:171],v[196:199],v[184:187]
v_exp_f32 v159,v159
v_mfma_f32_16x16x32_bf16 v[184:187],a[172:175],v[228:231],v[184:187]
v_exp_f32 v168,v168
v_mfma_f32_16x16x32_bf16 v[188:191],a[188:191],v[228:231],v[36:39]
v_exp_f32 v169,v169
v_mfma_f32_16x16x32_bf16 v[188:191],a[184:187],v[196:199],v[188:191]
v_exp_f32 v170,v170
v_mfma_f32_16x16x32_bf16 v[188:191],a[180:183],v[164:167],v[188:191]
v_exp_f32 v171,v171
v_mfma_f32_16x16x32_bf16 v[188:191],a[176:179],v[132:135],v[188:191]
v_exp_f32 v172,v172
v_mfma_f32_16x16x32_bf16 a[96:99],a[192:195],v[232:235],a[96:99]
ds_read_b64_tr_b16 a[192:193],v2
v_cvt_pk_bf16_f32 v168,v168,v169
v_mfma_f32_16x16x32_bf16 a[96:99],a[196:199],v[236:239],a[96:99]
ds_read_b64_tr_b16 a[194:195],v2 offset:4096
v_cvt_pk_bf16_f32 v169,v170,v171
v_mfma_f32_16x16x32_bf16 a[100:103],a[204:207],v[236:239],a[100:103]
v_exp_f32 v173,v173
v_mfma_f32_16x16x32_bf16 a[100:103],a[200:203],v[232:235],a[100:103]
ds_read_b64_tr_b16 a[196:197],v2 offset:8192
v_cvt_pk_bf16_f32 v170,v172,v173
v_mfma_f32_16x16x32_bf16 a[104:107],a[208:211],v[232:235],a[104:107]
ds_read_b64_tr_b16 a[198:199],v2 offset:12288
v_cvt_pk_bf16_f32 v136,v136,v137
v_mfma_f32_16x16x32_bf16 a[104:107],a[212:215],v[236:239],a[104:107]
v_exp_f32 v174,v174
v_mfma_f32_16x16x32_bf16 a[108:111],a[220:223],v[236:239],a[108:111]
buffer_load_dwordx4 v4,s[24:27],s47 offen offset:2048 nt lds
v_mfma_f32_16x16x32_bf16 a[108:111],a[216:219],v[232:235],a[108:111]
ds_read_b64_tr_b16 a[204:205],v3 offset:8192
v_cvt_pk_bf16_f32 v137,v138,v139
v_mfma_f32_16x16x32_bf16 a[112:115],a[224:227],v[232:235],a[112:115]
v_exp_f32 v175,v175
v_mfma_f32_16x16x32_bf16 a[112:115],a[228:231],v[236:239],a[112:115]
ds_read_b64_tr_b16 a[206:207],v3 offset:12288
v_cvt_pk_bf16_f32 v171,v174,v175
v_mfma_f32_16x16x32_bf16 a[116:119],a[236:239],v[236:239],a[116:119]
ds_read_b64_tr_b16 a[200:201],v3
v_cvt_pk_bf16_f32 v138,v140,v141
v_mfma_f32_16x16x32_bf16 a[116:119],a[232:235],v[232:235],a[116:119]
ds_read_b64_tr_b16 a[202:203],v3 offset:4096
v_mfma_f32_16x16x32_bf16 a[120:123],a[240:243],v[232:235],a[120:123]
ds_read_b64_tr_b16 a[208:209],v2 offset:256
v_mfma_f32_16x16x32_bf16 a[120:123],a[244:247],v[236:239],a[120:123]
ds_read_b64_tr_b16 a[210:211],v2 offset:4352
v_cvt_pk_bf16_f32 v139,v142,v143
v_mfma_f32_16x16x32_bf16 a[124:127],a[252:255],v[236:239],a[124:127]
ds_read_b64_tr_b16 a[212:213],v2 offset:8448
v_mfma_f32_16x16x32_bf16 a[124:127],a[248:251],v[232:235],a[124:127]
ds_read_b64_tr_b16 a[214:215],v2 offset:12544
v_smfmac_f32_16x16x64_bf16 v[60:63],v[28:31],v[232:239],v27
ds_read_b64_tr_b16 a[220:221],v3 offset:8448
v_mfma_f32_16x16x32_bf16 v[200:203],a[128:131],v[144:147],v[40:43]
ds_read_b64_tr_b16 a[222:223],v3 offset:12544
v_cvt_pk_bf16_f32 v140,v152,v153
v_mfma_f32_16x16x32_bf16 v[200:203],a[132:135],v[176:179],v[200:203]
ds_read_b64_tr_b16 a[216:217],v3 offset:256
v_mfma_f32_16x16x32_bf16 v[200:203],a[136:139],v[208:211],v[200:203]
ds_read_b64_tr_b16 a[218:219],v3 offset:4352
v_cvt_pk_bf16_f32 v141,v154,v155
v_mfma_f32_16x16x32_bf16 v[200:203],a[140:143],v[240:243],v[200:203]
buffer_load_dwordx4 v5,s[24:27],s47 offen offset:3072 nt lds
v_mfma_f32_16x16x32_bf16 v[204:207],a[156:159],v[240:243],v[40:43]
s_add_u32 s47,s47,16384
s_and_b32 s47,s47,0x3ffff
ds_read_b64_tr_b16 a[224:225],v2 offset:512
v_mfma_f32_16x16x32_bf16 v[204:207],a[152:155],v[208:211],v[204:207]
s_or_b32 s47,s47,s46
ds_read_b64_tr_b16 a[226:227],v2 offset:4608
v_cvt_pk_bf16_f32 v142,v156,v157
v_mfma_f32_16x16x32_bf16 v[204:207],a[148:151],v[176:179],v[204:207]
ds_read_b64_tr_b16 a[228:229],v2 offset:8704
v_mfma_f32_16x16x32_bf16 v[204:207],a[144:147],v[144:147],v[204:207]
ds_read_b64_tr_b16 a[230:231],v2 offset:12800
v_mfma_f32_16x16x32_bf16 v[216:219],a[160:163],v[144:147],v[40:43]
ds_read_b64_tr_b16 a[236:237],v3 offset:8704
v_cvt_pk_bf16_f32 v143,v158,v159
v_mfma_f32_16x16x32_bf16 v[216:219],a[164:167],v[176:179],v[216:219]
ds_read_b64_tr_b16 a[238:239],v3 offset:12800
v_mfma_f32_16x16x32_bf16 v[216:219],a[168:171],v[208:211],v[216:219]
ds_read_b64_tr_b16 a[232:233],v3 offset:512
v_mfma_f32_16x16x32_bf16 v[216:219],a[172:175],v[240:243],v[216:219]
ds_read_b64_tr_b16 a[234:235],v3 offset:4608
v_mfma_f32_16x16x32_bf16 v[220:223],a[188:191],v[240:243],v[40:43]
ds_read_b64_tr_b16 a[240:241],v2 offset:768
v_mfma_f32_16x16x32_bf16 v[220:223],a[184:187],v[208:211],v[220:223]
ds_read_b64_tr_b16 a[242:243],v2 offset:4864
v_mfma_f32_16x16x32_bf16 v[220:223],a[180:183],v[176:179],v[220:223]
ds_read_b64_tr_b16 a[244:245],v2 offset:8960
v_mfma_f32_16x16x32_bf16 v[220:223],a[176:179],v[144:147],v[220:223]
s_waitcnt lgkmcnt(7)
buffer_load_dword v26,v24,s[88:91],s93 offen
v_mfma_f32_16x16x32_bf16 a[0:3],a[192:195],v[136:139],a[0:3]
ds_read_b64_tr_b16 a[246:247],v2 offset:13056
v_mfma_f32_16x16x32_bf16 a[0:3],a[196:199],v[140:143],a[0:3]
ds_read_b64_tr_b16 a[252:253],v3 offset:8960
v_mfma_f32_16x16x32_bf16 a[4:7],a[204:207],v[140:143],a[4:7]
ds_read_b64_tr_b16 a[254:255],v3 offset:13056
v_mfma_f32_16x16x32_bf16 a[4:7],a[200:203],v[136:139],a[4:7]
s_memrealtime s[76:77]
ds_read_b64_tr_b16 a[248:249],v3 offset:768
v_mfma_f32_16x16x32_bf16 a[8:11],a[208:211],v[136:139],a[8:11]
ds_read_b64_tr_b16 a[250:251],v3 offset:4864
v_mfma_f32_16x16x32_bf16 a[8:11],a[212:215],v[140:143],a[8:11]
v_exp_f32 v184,v184
v_mfma_f32_16x16x32_bf16 a[12:15],a[220:223],v[140:143],a[12:15]
v_exp_f32 v185,v185
v_mfma_f32_16x16x32_bf16 a[12:15],a[216:219],v[136:139],a[12:15]
s_bitcmp1_b32 s45,0
s_cbranch_scc0 L_t311_%=
s_mov_b32 m0,s80
s_add_u32 s80,s80,4096
buffer_load_dwordx4 v25,s[24:27],s83 offen nt lds
s_add_u32 s83,s83,4096
L_t311_%=:
v_mfma_f32_16x16x32_bf16 a[16:19],a[224:227],v[136:139],a[16:19]
v_exp_f32 v186,v186
v_mfma_f32_16x16x32_bf16 a[16:19],a[228:231],v[140:143],a[16:19]
s_waitcnt lgkmcnt(3)
v_exp_f32 v187,v187
v_mfma_f32_16x16x32_bf16 a[20:23],a[236:239],v[140:143],a[20:23]
s_waitcnt lgkmcnt(0)
v_exp_f32 v188,v188
v_mfma_f32_16x16x32_bf16 a[20:23],a[232:235],v[136:139],a[20:23]
s_sub_u32 s79,s78,s76
v_exp_f32 v189,v189
v_mfma_f32_16x16x32_bf16 a[24:27],a[240:243],v[136:139],a[24:27]
s_add_u32 s78,s76,s60
v_exp_f32 v190,v190
v_mfma_f32_16x16x32_bf16 a[24:27],a[244:247],v[140:143],a[24:27]
s_cmp_gt_i32 s79,0
s_cbranch_scc1 L_ps7_%=
L_pr7_%=:
v_cvt_pk_bf16_f32 v172,v184,v185
v_mfma_f32_16x16x32_bf16 a[28:31],a[252:255],v[140:143],a[28:31]
v_exp_f32 v191,v191
v_mfma_f32_16x16x32_bf16 a[28:31],a[248:251],v[136:139],a[28:31]
v_cvt_pk_bf16_f32 v173,v186,v187
v_cvt_pk_bf16_f32 v174,v188,v189
v_cvt_pk_bf16_f32 v175,v190,v191
s_cmp_lg_u32 s53,0
v_smfmac_f32_16x16x64_bf16 v[48:51],v[28:31],v[136:143],v27
s_cbranch_scc1 L_att1_%=
L_att1x_%=:
L_endseg_%=:
v_mfma_f32_16x16x32_bf16 v[232:235],a[128:131],v[148:151],v[44:47]
s_waitcnt vmcnt(10)
s_lshl_b32 s43,s41,2
v_exp_f32 v200,v200
v_mfma_f32_16x16x32_bf16 v[232:235],a[132:135],v[180:183],v[232:235]
s_barrier
ds_read_b128 a[128:131],v1
v_add_u32 v2,s48,v2
v_mfma_f32_16x16x32_bf16 v[232:235],a[136:139],v[212:215],v[232:235]
s_add_u32 s43,s43,s70
v_exp_f32 v201,v201
v_mfma_f32_16x16x32_bf16 v[232:235],a[140:143],v[244:247],v[232:235]
s_lshl_b32 s43,s43,19
ds_read_b128 a[132:135],v1 offset:256
v_cvt_pk_bf16_f32 v200,v200,v201
v_mfma_f32_16x16x32_bf16 v[236:239],a[156:159],v[244:247],v[44:47]
s_add_u32 s43,s43,s55
v_exp_f32 v202,v202
v_mfma_f32_16x16x32_bf16 v[236:239],a[152:155],v[212:215],v[236:239]
s_mov_b64 s[28:29],s[12:13]
ds_read_b128 a[136:139],v1 offset:512
v_add_u32 v3,s48,v3
v_mfma_f32_16x16x32_bf16 v[236:239],a[148:151],v[180:183],v[236:239]
s_mov_b32 s48,32768
v_exp_f32 v203,v203
v_mfma_f32_16x16x32_bf16 v[236:239],a[144:147],v[148:151],v[236:239]
s_mov_b32 s30,475136
ds_read_b128 a[140:143],v1 offset:768
v_cvt_pk_bf16_f32 v201,v202,v203
v_mfma_f32_16x16x32_bf16 v[248:251],a[160:163],v[148:151],v[44:47]
s_mov_b32 s31,0x20000
v_exp_f32 v204,v204
v_mfma_f32_16x16x32_bf16 v[248:251],a[164:167],v[180:183],v[248:251]
ds_read_b128 a[156:159],v1 offset:4864
v_lshrrev_b32 v4,6,v6
v_mfma_f32_16x16x32_bf16 v[248:251],a[168:171],v[212:215],v[248:251]
v_exp_f32 v205,v205
v_mfma_f32_16x16x32_bf16 v[248:251],a[172:175],v[244:247],v[248:251]
ds_read_b128 a[152:155],v1 offset:4608
v_cvt_pk_bf16_f32 v202,v204,v205
v_mfma_f32_16x16x32_bf16 v[252:255],a[188:191],v[244:247],v[44:47]
v_exp_f32 v206,v206
v_mfma_f32_16x16x32_bf16 v[252:255],a[184:187],v[212:215],v[252:255]
ds_read_b128 a[148:151],v1 offset:4352
v_mul_u32_u24 v4,320,v4
v_mfma_f32_16x16x32_bf16 v[252:255],a[180:183],v[180:183],v[252:255]
v_exp_f32 v207,v207
v_mfma_f32_16x16x32_bf16 v[252:255],a[176:179],v[148:151],v[252:255]
ds_read_b128 a[144:147],v1 offset:4096
v_cvt_pk_bf16_f32 v203,v206,v207
v_mfma_f32_16x16x32_bf16 a[32:35],a[192:195],v[168:171],a[32:35]
v_exp_f32 v216,v216
v_mfma_f32_16x16x32_bf16 a[32:35],a[196:199],v[172:175],a[32:35]
ds_read_b128 a[160:163],v1 offset:8192
v_bfe_u32 v5,v6,4,2
v_mfma_f32_16x16x32_bf16 a[36:39],a[204:207],v[172:175],a[36:39]
v_exp_f32 v217,v217
v_mfma_f32_16x16x32_bf16 a[36:39],a[200:203],v[168:171],a[36:39]
ds_read_b128 a[164:167],v1 offset:8448
v_cvt_pk_bf16_f32 v204,v216,v217
v_mfma_f32_16x16x32_bf16 a[40:43],a[208:211],v[168:171],a[40:43]
v_exp_f32 v218,v218
v_mfma_f32_16x16x32_bf16 a[40:43],a[212:215],v[172:175],a[40:43]
ds_read_b128 a[168:171],v1 offset:8704
v_lshl_add_u32 v4,v5,3,v4
v_mfma_f32_16x16x32_bf16 a[44:47],a[220:223],v[172:175],a[44:47]
v_exp_f32 v219,v219
v_mfma_f32_16x16x32_bf16 a[44:47],a[216:219],v[168:171],a[44:47]
ds_read_b128 a[172:175],v1 offset:8960
v_cvt_pk_bf16_f32 v205,v218,v219
v_mfma_f32_16x16x32_bf16 a[48:51],a[224:227],v[168:171],a[48:51]
v_exp_f32 v220,v220
v_mfma_f32_16x16x32_bf16 a[48:51],a[228:231],v[172:175],a[48:51]
ds_read_b128 a[188:191],v1 offset:13056
v_bfe_u32 v5,v6,8,1
v_mfma_f32_16x16x32_bf16 a[52:55],a[236:239],v[172:175],a[52:55]
v_exp_f32 v221,v221
v_mfma_f32_16x16x32_bf16 a[52:55],a[232:235],v[168:171],a[52:55]
ds_read_b128 a[184:187],v1 offset:12800
v_cvt_pk_bf16_f32 v206,v220,v221
v_mfma_f32_16x16x32_bf16 a[56:59],a[240:243],v[168:171],a[56:59]
v_exp_f32 v222,v222
v_mfma_f32_16x16x32_bf16 a[56:59],a[244:247],v[172:175],a[56:59]
ds_read_b128 a[180:183],v1 offset:12544
v_lshlrev_b32 v5,5,v5
v_mfma_f32_16x16x32_bf16 a[60:63],a[252:255],v[172:175],a[60:63]
v_exp_f32 v223,v223
v_mfma_f32_16x16x32_bf16 a[60:63],a[248:251],v[168:171],a[60:63]
ds_read_b128 a[176:179],v1 offset:12288
v_cvt_pk_bf16_f32 v207,v222,v223
v_smfmac_f32_16x16x64_bf16 v[52:55],v[28:31],v[168:175],v27
s_waitcnt lgkmcnt(4)
v_add_u32 v1,s48,v1
v_mfma_f32_16x16x32_bf16 v[136:139],a[128:131],v[128:131],v[32:35]
v_exp_f32 v232,v232
v_mfma_f32_16x16x32_bf16 v[136:139],a[132:135],v[160:163],v[136:139]
v_exp_f32 v233,v233
v_mfma_f32_16x16x32_bf16 v[136:139],a[136:139],v[192:195],v[136:139]
v_exp_f32 v234,v234
v_mfma_f32_16x16x32_bf16 v[136:139],a[140:143],v[224:227],v[136:139]
v_exp_f32 v235,v235
v_mfma_f32_16x16x32_bf16 v[140:143],a[156:159],v[224:227],v[32:35]
v_cvt_pk_bf16_f32 v232,v232,v233
v_cvt_pk_bf16_f32 v233,v234,v235
v_mfma_f32_16x16x32_bf16 v[140:143],a[152:155],v[192:195],v[140:143]
s_add_u32 s80,s71,0
v_exp_f32 v236,v236
v_mfma_f32_16x16x32_bf16 v[140:143],a[148:151],v[160:163],v[140:143]
s_and_b32 s80,s80,3
v_exp_f32 v237,v237
v_mfma_f32_16x16x32_bf16 v[140:143],a[144:147],v[128:131],v[140:143]
s_lshl_b32 s80,s80,1
s_mul_i32 s80,s80,163840
s_add_u32 s80,s80,s81
v_mfma_f32_16x16x32_bf16 v[152:155],a[160:163],v[128:131],v[32:35]
s_add_u32 s83,s80,20480
v_exp_f32 v238,v238
v_mfma_f32_16x16x32_bf16 v[152:155],a[164:167],v[160:163],v[152:155]
buffer_store_dwordx4 v[64:67],v6,s[36:39],s84 offen sc1
v_mfma_f32_16x16x32_bf16 v[152:155],a[168:171],v[192:195],v[152:155]
v_exp_f32 v239,v239
v_mfma_f32_16x16x32_bf16 v[152:155],a[172:175],v[224:227],v[152:155]
s_waitcnt lgkmcnt(0)
v_cvt_pk_bf16_f32 v234,v236,v237
)A"
R"A(v_cvt_pk_bf16_f32 v235,v238,v239
v_mfma_f32_16x16x32_bf16 v[156:159],a[188:191],v[224:227],v[32:35]
v_exp_f32 v248,v248
v_mfma_f32_16x16x32_bf16 v[156:159],a[184:187],v[192:195],v[156:159]
v_exp_f32 v249,v249
v_mfma_f32_16x16x32_bf16 v[156:159],a[180:183],v[160:163],v[156:159]
v_exp_f32 v250,v250
v_mfma_f32_16x16x32_bf16 v[156:159],a[176:179],v[128:131],v[156:159]
v_exp_f32 v251,v251
v_mfma_f32_16x16x32_bf16 a[64:67],a[192:195],v[200:203],a[64:67]
v_exp_f32 v252,v252
v_mfma_f32_16x16x32_bf16 a[64:67],a[196:199],v[204:207],a[64:67]
v_exp_f32 v253,v253
v_mfma_f32_16x16x32_bf16 a[68:71],a[204:207],v[204:207],a[68:71]
v_exp_f32 v254,v254
v_mfma_f32_16x16x32_bf16 a[68:71],a[200:203],v[200:203],a[68:71]
v_exp_f32 v255,v255
v_mfma_f32_16x16x32_bf16 a[72:75],a[208:211],v[200:203],a[72:75]
v_cvt_pk_bf16_f32 v236,v248,v249
v_cvt_pk_bf16_f32 v237,v250,v251
v_mfma_f32_16x16x32_bf16 a[72:75],a[212:215],v[204:207],a[72:75]
v_cvt_pk_bf16_f32 v238,v252,v253
v_cvt_pk_bf16_f32 v239,v254,v255
v_mfma_f32_16x16x32_bf16 a[76:79],a[220:223],v[204:207],a[76:79]
v_exp_f32 v136,v136
v_mfma_f32_16x16x32_bf16 a[76:79],a[216:219],v[200:203],a[76:79]
v_exp_f32 v137,v137
v_mfma_f32_16x16x32_bf16 a[80:83],a[224:227],v[200:203],a[80:83]
v_exp_f32 v138,v138
v_mfma_f32_16x16x32_bf16 a[80:83],a[228:231],v[204:207],a[80:83]
v_exp_f32 v139,v139
v_mfma_f32_16x16x32_bf16 a[84:87],a[236:239],v[204:207],a[84:87]
s_add_u32 m0,s82,81920
v_exp_f32 v140,v140
v_mfma_f32_16x16x32_bf16 a[84:87],a[232:235],v[200:203],a[84:87]
v_cvt_pk_bf16_f32 v136,v136,v137
v_cvt_pk_bf16_f32 v137,v138,v139
v_mfma_f32_16x16x32_bf16 a[88:91],a[240:243],v[200:203],a[88:91]
v_exp_f32 v141,v141
v_mfma_f32_16x16x32_bf16 a[88:91],a[244:247],v[204:207],a[88:91]
buffer_load_dwordx4 v7,s[72:75],s80 offen lds
v_mfma_f32_16x16x32_bf16 a[92:95],a[252:255],v[204:207],a[92:95]
v_exp_f32 v142,v142
v_mfma_f32_16x16x32_bf16 a[92:95],a[248:251],v[200:203],a[92:95]
v_exp_f32 v143,v143
v_smfmac_f32_16x16x64_bf16 v[56:59],v[28:31],v[200:207],v27
v_cvt_pk_bf16_f32 v138,v140,v141
v_mfma_f32_16x16x32_bf16 v[168:171],a[128:131],v[132:135],v[36:39]
buffer_store_dwordx4 v[68:71],v6,s[36:39],s84 offen offset:1024 sc1
v_mfma_f32_16x16x32_bf16 v[168:171],a[132:135],v[164:167],v[168:171]
s_add_u32 s28,s28,s43
s_addc_u32 s29,s29,0
v_cvt_pk_bf16_f32 v139,v142,v143
v_mfma_f32_16x16x32_bf16 v[168:171],a[136:139],v[196:199],v[168:171]
s_and_b32 s29,s29,0xffff
v_exp_f32 v152,v152
v_mfma_f32_16x16x32_bf16 v[168:171],a[140:143],v[228:231],v[168:171]
v_exp_f32 v153,v153
v_mfma_f32_16x16x32_bf16 v[172:175],a[156:159],v[228:231],v[36:39]
v_exp_f32 v154,v154
v_mfma_f32_16x16x32_bf16 v[172:175],a[152:155],v[196:199],v[172:175]
v_exp_f32 v155,v155
v_mfma_f32_16x16x32_bf16 v[172:175],a[148:151],v[164:167],v[172:175]
v_exp_f32 v156,v156
v_mfma_f32_16x16x32_bf16 v[172:175],a[144:147],v[132:135],v[172:175]
v_exp_f32 v157,v157
v_mfma_f32_16x16x32_bf16 v[184:187],a[160:163],v[132:135],v[36:39]
v_exp_f32 v158,v158
v_mfma_f32_16x16x32_bf16 v[184:187],a[164:167],v[164:167],v[184:187]
v_exp_f32 v159,v159
v_mfma_f32_16x16x32_bf16 v[184:187],a[168:171],v[196:199],v[184:187]
v_cvt_pk_bf16_f32 v140,v152,v153
v_cvt_pk_bf16_f32 v141,v154,v155
v_mfma_f32_16x16x32_bf16 v[184:187],a[172:175],v[228:231],v[184:187]
v_cvt_pk_bf16_f32 v142,v156,v157
v_cvt_pk_bf16_f32 v143,v158,v159
v_mfma_f32_16x16x32_bf16 v[188:191],a[188:191],v[228:231],v[36:39]
v_exp_f32 v168,v168
v_mfma_f32_16x16x32_bf16 v[188:191],a[184:187],v[196:199],v[188:191]
v_exp_f32 v169,v169
v_mfma_f32_16x16x32_bf16 v[188:191],a[180:183],v[164:167],v[188:191]
v_exp_f32 v170,v170
v_mfma_f32_16x16x32_bf16 v[188:191],a[176:179],v[132:135],v[188:191]
v_exp_f32 v171,v171
v_mfma_f32_16x16x32_bf16 a[96:99],a[192:195],v[232:235],a[96:99]
buffer_load_dwordx4 v8,s[72:75],s80 offen offset:1024 lds
v_mfma_f32_16x16x32_bf16 a[96:99],a[196:199],v[236:239],a[96:99]
ds_read_b64_tr_b16 a[192:193],v2
ds_read_b64_tr_b16 a[194:195],v2 offset:4096
v_mfma_f32_16x16x32_bf16 a[100:103],a[204:207],v[236:239],a[100:103]
ds_read_b64_tr_b16 a[196:197],v2 offset:8192
v_cvt_pk_bf16_f32 v168,v168,v169
v_mfma_f32_16x16x32_bf16 a[100:103],a[200:203],v[232:235],a[100:103]
ds_read_b64_tr_b16 a[198:199],v2 offset:12288
v_cvt_pk_bf16_f32 v169,v170,v171
v_mfma_f32_16x16x32_bf16 a[104:107],a[208:211],v[232:235],a[104:107]
buffer_store_dwordx4 v[72:75],v6,s[36:39],s84 offen offset:2048 sc1
v_mfma_f32_16x16x32_bf16 a[104:107],a[212:215],v[236:239],a[104:107]
ds_read_b64_tr_b16 a[204:205],v3 offset:8192
ds_read_b64_tr_b16 a[206:207],v3 offset:12288
v_mfma_f32_16x16x32_bf16 a[108:111],a[220:223],v[236:239],a[108:111]
v_exp_f32 v172,v172
v_mfma_f32_16x16x32_bf16 a[108:111],a[216:219],v[232:235],a[108:111]
ds_read_b64_tr_b16 a[200:201],v3
ds_read_b64_tr_b16 a[202:203],v3 offset:4096
v_mfma_f32_16x16x32_bf16 a[112:115],a[224:227],v[232:235],a[112:115]
v_exp_f32 v173,v173
v_mfma_f32_16x16x32_bf16 a[112:115],a[228:231],v[236:239],a[112:115]
ds_read_b64_tr_b16 a[208:209],v2 offset:256
ds_read_b64_tr_b16 a[210:211],v2 offset:4352
v_mfma_f32_16x16x32_bf16 a[116:119],a[236:239],v[236:239],a[116:119]
ds_read_b64_tr_b16 a[212:213],v2 offset:8448
v_cvt_pk_bf16_f32 v170,v172,v173
v_mfma_f32_16x16x32_bf16 a[116:119],a[232:235],v[232:235],a[116:119]
v_exp_f32 v174,v174
v_mfma_f32_16x16x32_bf16 a[120:123],a[240:243],v[232:235],a[120:123]
ds_read_b64_tr_b16 a[214:215],v2 offset:12544
ds_read_b64_tr_b16 a[220:221],v3 offset:8448
v_mfma_f32_16x16x32_bf16 a[120:123],a[244:247],v[236:239],a[120:123]
ds_read_b64_tr_b16 a[222:223],v3 offset:12544
v_mfma_f32_16x16x32_bf16 a[124:127],a[252:255],v[236:239],a[124:127]
ds_read_b64_tr_b16 a[216:217],v3 offset:256
v_mfma_f32_16x16x32_bf16 a[124:127],a[248:251],v[232:235],a[124:127]
ds_read_b64_tr_b16 a[218:219],v3 offset:4352
v_smfmac_f32_16x16x64_bf16 v[60:63],v[28:31],v[232:239],v27
ds_read_b64_tr_b16 a[224:225],v2 offset:512
v_mfma_f32_16x16x32_bf16 v[200:203],a[128:131],v[144:147],v[40:43]
ds_read_b64_tr_b16 a[226:227],v2 offset:4608
ds_read_b64_tr_b16 a[228:229],v2 offset:8704
v_mfma_f32_16x16x32_bf16 v[200:203],a[132:135],v[176:179],v[200:203]
ds_read_b64_tr_b16 a[230:231],v2 offset:12800
v_mfma_f32_16x16x32_bf16 v[200:203],a[136:139],v[208:211],v[200:203]
ds_read_b64_tr_b16 a[236:237],v3 offset:8704
v_mfma_f32_16x16x32_bf16 v[200:203],a[140:143],v[240:243],v[200:203]
buffer_load_dwordx4 v9,s[72:75],s80 offen offset:2048 lds
v_mfma_f32_16x16x32_bf16 v[204:207],a[156:159],v[240:243],v[40:43]
ds_read_b64_tr_b16 a[238:239],v3 offset:12800
ds_read_b64_tr_b16 a[232:233],v3 offset:512
v_mfma_f32_16x16x32_bf16 v[204:207],a[152:155],v[208:211],v[204:207]
ds_read_b64_tr_b16 a[234:235],v3 offset:4608
v_mfma_f32_16x16x32_bf16 v[204:207],a[148:151],v[176:179],v[204:207]
ds_read_b64_tr_b16 a[240:241],v2 offset:768
v_mfma_f32_16x16x32_bf16 v[204:207],a[144:147],v[144:147],v[204:207]
ds_read_b64_tr_b16 a[242:243],v2 offset:4864
v_mfma_f32_16x16x32_bf16 v[216:219],a[160:163],v[144:147],v[40:43]
ds_read_b64_tr_b16 a[244:245],v2 offset:8960
ds_read_b64_tr_b16 a[246:247],v2 offset:13056
v_mfma_f32_16x16x32_bf16 v[216:219],a[164:167],v[176:179],v[216:219]
buffer_store_dwordx4 v[76:79],v6,s[36:39],s84 offen offset:3072 sc1
v_mfma_f32_16x16x32_bf16 v[216:219],a[168:171],v[208:211],v[216:219]
s_add_u32 s84,s55,65536
ds_read_b64_tr_b16 a[252:253],v3 offset:8960
ds_read_b64_tr_b16 a[254:255],v3 offset:13056
v_mfma_f32_16x16x32_bf16 v[216:219],a[172:175],v[240:243],v[216:219]
s_max_i32 s84,s84,s18
ds_read_b64_tr_b16 a[248:249],v3 offset:768
v_add_u32 v2,s48,v2
v_mfma_f32_16x16x32_bf16 v[220:223],a[188:191],v[240:243],v[40:43]
ds_read_b64_tr_b16 a[250:251],v3 offset:4864
v_add_u32 v3,s48,v3
v_mfma_f32_16x16x32_bf16 v[220:223],a[184:187],v[208:211],v[220:223]
s_mov_b32 s48,98304
v_exp_f32 v175,v175
v_mfma_f32_16x16x32_bf16 v[220:223],a[180:183],v[176:179],v[220:223]
v_exp_f32 v184,v184
v_mfma_f32_16x16x32_bf16 v[220:223],a[176:179],v[144:147],v[220:223]
s_waitcnt lgkmcnt(6)
v_exp_f32 v185,v185
v_mfma_f32_16x16x32_bf16 a[0:3],a[192:195],v[136:139],a[0:3]
v_cvt_pk_bf16_f32 v171,v174,v175
v_cvt_pk_bf16_f32 v172,v184,v185
v_mfma_f32_16x16x32_bf16 a[0:3],a[196:199],v[140:143],a[0:3]
v_exp_f32 v186,v186
v_mfma_f32_16x16x32_bf16 a[4:7],a[204:207],v[140:143],a[4:7]
v_exp_f32 v187,v187
v_mfma_f32_16x16x32_bf16 a[4:7],a[200:203],v[136:139],a[4:7]
v_exp_f32 v188,v188
v_mfma_f32_16x16x32_bf16 a[8:11],a[208:211],v[136:139],a[8:11]
v_exp_f32 v189,v189
v_mfma_f32_16x16x32_bf16 a[8:11],a[212:215],v[140:143],a[8:11]
v_exp_f32 v190,v190
v_mfma_f32_16x16x32_bf16 a[12:15],a[220:223],v[140:143],a[12:15]
v_exp_f32 v191,v191
v_mfma_f32_16x16x32_bf16 a[12:15],a[216:219],v[136:139],a[12:15]
buffer_load_dwordx4 v10,s[72:75],s80 offen offset:3072 lds
v_mfma_f32_16x16x32_bf16 a[16:19],a[224:227],v[136:139],a[16:19]
v_cvt_pk_bf16_f32 v173,v186,v187
v_cvt_pk_bf16_f32 v174,v188,v189
v_mfma_f32_16x16x32_bf16 a[16:19],a[228:231],v[140:143],a[16:19]
v_exp_f32 v200,v200
v_mfma_f32_16x16x32_bf16 a[20:23],a[236:239],v[140:143],a[20:23]
v_exp_f32 v201,v201
v_mfma_f32_16x16x32_bf16 a[20:23],a[232:235],v[136:139],a[20:23]
v_cvt_pk_bf16_f32 v175,v190,v191
v_cvt_pk_bf16_f32 v200,v200,v201
v_mfma_f32_16x16x32_bf16 a[24:27],a[240:243],v[136:139],a[24:27]
s_waitcnt lgkmcnt(0)
v_exp_f32 v202,v202
v_mfma_f32_16x16x32_bf16 a[24:27],a[244:247],v[140:143],a[24:27]
v_exp_f32 v203,v203
v_mfma_f32_16x16x32_bf16 a[28:31],a[252:255],v[140:143],a[28:31]
v_exp_f32 v204,v204
v_mfma_f32_16x16x32_bf16 a[28:31],a[248:251],v[136:139],a[28:31]
buffer_store_dwordx4 v[80:83],v6,s[36:39],s85 offen sc1
v_smfmac_f32_16x16x64_bf16 v[48:51],v[28:31],v[136:143],v27
v_cvt_pk_bf16_f32 v201,v202,v203
v_mfma_f32_16x16x32_bf16 v[232:235],a[128:131],v[148:151],v[44:47]
s_waitcnt vmcnt(10)
v_exp_f32 v205,v205
v_mfma_f32_16x16x32_bf16 v[232:235],a[132:135],v[180:183],v[232:235]
s_barrier
ds_read_b128 a[128:131],v1
v_cvt_pk_bf16_f32 v202,v204,v205
v_mfma_f32_16x16x32_bf16 v[232:235],a[136:139],v[212:215],v[232:235]
v_exp_f32 v206,v206
v_mfma_f32_16x16x32_bf16 v[232:235],a[140:143],v[244:247],v[232:235]
v_exp_f32 v207,v207
v_mfma_f32_16x16x32_bf16 v[236:239],a[156:159],v[244:247],v[44:47]
ds_read_b128 a[132:135],v1 offset:256
v_cvt_pk_bf16_f32 v203,v206,v207
v_mfma_f32_16x16x32_bf16 v[236:239],a[152:155],v[212:215],v[236:239]
v_exp_f32 v216,v216
v_mfma_f32_16x16x32_bf16 v[236:239],a[148:151],v[180:183],v[236:239]
s_add_u32 m0,s82,86016
v_exp_f32 v217,v217
v_mfma_f32_16x16x32_bf16 v[236:239],a[144:147],v[148:151],v[236:239]
ds_read_b128 a[136:139],v1 offset:512
v_cvt_pk_bf16_f32 v204,v216,v217
v_mfma_f32_16x16x32_bf16 v[248:251],a[160:163],v[148:151],v[44:47]
v_exp_f32 v218,v218
v_mfma_f32_16x16x32_bf16 v[248:251],a[164:167],v[180:183],v[248:251]
buffer_load_dwordx4 v11,s[72:75],s80 offen lds
v_mfma_f32_16x16x32_bf16 v[248:251],a[168:171],v[212:215],v[248:251]
ds_read_b128 a[140:143],v1 offset:768
v_mfma_f32_16x16x32_bf16 v[248:251],a[172:175],v[244:247],v[248:251]
v_exp_f32 v219,v219
v_mfma_f32_16x16x32_bf16 v[252:255],a[188:191],v[244:247],v[44:47]
ds_read_b128 a[156:159],v1 offset:4864
v_cvt_pk_bf16_f32 v205,v218,v219
v_mfma_f32_16x16x32_bf16 v[252:255],a[184:187],v[212:215],v[252:255]
v_exp_f32 v220,v220
v_mfma_f32_16x16x32_bf16 v[252:255],a[180:183],v[180:183],v[252:255]
ds_read_b128 a[152:155],v1 offset:4608
v_mfma_f32_16x16x32_bf16 v[252:255],a[176:179],v[148:151],v[252:255]
v_exp_f32 v221,v221
v_mfma_f32_16x16x32_bf16 a[32:35],a[192:195],v[168:171],a[32:35]
ds_read_b128 a[148:151],v1 offset:4352
v_cvt_pk_bf16_f32 v206,v220,v221
v_mfma_f32_16x16x32_bf16 a[32:35],a[196:199],v[172:175],a[32:35]
v_exp_f32 v222,v222
v_mfma_f32_16x16x32_bf16 a[36:39],a[204:207],v[172:175],a[36:39]
ds_read_b128 a[144:147],v1 offset:4096
v_mfma_f32_16x16x32_bf16 a[36:39],a[200:203],v[168:171],a[36:39]
buffer_store_dwordx4 v[84:87],v6,s[36:39],s85 offen offset:1024 sc1
v_mfma_f32_16x16x32_bf16 a[40:43],a[208:211],v[168:171],a[40:43]
ds_read_b128 a[160:163],v1 offset:8192
v_mfma_f32_16x16x32_bf16 a[40:43],a[212:215],v[172:175],a[40:43]
v_exp_f32 v223,v223
v_mfma_f32_16x16x32_bf16 a[44:47],a[220:223],v[172:175],a[44:47]
)A"
R"A(ds_read_b128 a[164:167],v1 offset:8448
v_cvt_pk_bf16_f32 v207,v222,v223
v_mfma_f32_16x16x32_bf16 a[44:47],a[216:219],v[168:171],a[44:47]
v_exp_f32 v232,v232
v_mfma_f32_16x16x32_bf16 a[48:51],a[224:227],v[168:171],a[48:51]
ds_read_b128 a[168:171],v1 offset:8704
v_mfma_f32_16x16x32_bf16 a[48:51],a[228:231],v[172:175],a[48:51]
v_exp_f32 v233,v233
v_mfma_f32_16x16x32_bf16 a[52:55],a[236:239],v[172:175],a[52:55]
s_add_u32 m0,s82,87040
ds_read_b128 a[172:175],v1 offset:8960
v_cvt_pk_bf16_f32 v232,v232,v233
v_mfma_f32_16x16x32_bf16 a[52:55],a[232:235],v[168:171],a[52:55]
v_exp_f32 v234,v234
v_mfma_f32_16x16x32_bf16 a[56:59],a[240:243],v[168:171],a[56:59]
ds_read_b128 a[188:191],v1 offset:13056
v_mfma_f32_16x16x32_bf16 a[56:59],a[244:247],v[172:175],a[56:59]
buffer_load_dwordx4 v7,s[72:75],s83 offen lds
v_mfma_f32_16x16x32_bf16 a[60:63],a[252:255],v[172:175],a[60:63]
ds_read_b128 a[184:187],v1 offset:12800
v_mfma_f32_16x16x32_bf16 a[60:63],a[248:251],v[168:171],a[60:63]
v_exp_f32 v235,v235
v_smfmac_f32_16x16x64_bf16 v[52:55],v[28:31],v[168:175],v27
s_waitcnt lgkmcnt(3)
ds_read_b128 a[180:183],v1 offset:12544
v_mfma_f32_16x16x32_bf16 v[136:139],a[128:131],v[128:131],v[32:35]
v_exp_f32 v236,v236
v_mfma_f32_16x16x32_bf16 v[136:139],a[132:135],v[160:163],v[136:139]
ds_read_b128 a[176:179],v1 offset:12288
v_cvt_pk_bf16_f32 v233,v234,v235
v_mfma_f32_16x16x32_bf16 v[136:139],a[136:139],v[192:195],v[136:139]
v_exp_f32 v237,v237
v_mfma_f32_16x16x32_bf16 v[136:139],a[140:143],v[224:227],v[136:139]
v_exp_f32 v238,v238
v_mfma_f32_16x16x32_bf16 v[140:143],a[156:159],v[224:227],v[32:35]
v_exp_f32 v239,v239
v_mfma_f32_16x16x32_bf16 v[140:143],a[152:155],v[192:195],v[140:143]
v_cvt_pk_bf16_f32 v234,v236,v237
v_cvt_pk_bf16_f32 v235,v238,v239
v_mfma_f32_16x16x32_bf16 v[140:143],a[148:151],v[160:163],v[140:143]
v_exp_f32 v248,v248
v_mfma_f32_16x16x32_bf16 v[140:143],a[144:147],v[128:131],v[140:143]
v_exp_f32 v249,v249
v_mfma_f32_16x16x32_bf16 v[152:155],a[160:163],v[128:131],v[32:35]
buffer_store_dwordx4 v[88:91],v6,s[36:39],s85 offen offset:2048 sc1
v_mfma_f32_16x16x32_bf16 v[152:155],a[164:167],v[160:163],v[152:155]
v_exp_f32 v250,v250
v_mfma_f32_16x16x32_bf16 v[152:155],a[168:171],v[192:195],v[152:155]
s_waitcnt lgkmcnt(0)
v_exp_f32 v251,v251
v_mfma_f32_16x16x32_bf16 v[152:155],a[172:175],v[224:227],v[152:155]
v_exp_f32 v252,v252
v_mfma_f32_16x16x32_bf16 v[156:159],a[188:191],v[224:227],v[32:35]
v_exp_f32 v253,v253
v_mfma_f32_16x16x32_bf16 v[156:159],a[184:187],v[192:195],v[156:159]
v_exp_f32 v254,v254
v_mfma_f32_16x16x32_bf16 v[156:159],a[180:183],v[160:163],v[156:159]
v_exp_f32 v255,v255
v_mfma_f32_16x16x32_bf16 v[156:159],a[176:179],v[128:131],v[156:159]
v_cvt_pk_bf16_f32 v236,v248,v249
v_cvt_pk_bf16_f32 v237,v250,v251
v_mfma_f32_16x16x32_bf16 a[64:67],a[192:195],v[200:203],a[64:67]
buffer_load_dwordx4 v8,s[72:75],s83 offen offset:1024 lds
v_mfma_f32_16x16x32_bf16 a[64:67],a[196:199],v[204:207],a[64:67]
v_cvt_pk_bf16_f32 v238,v252,v253
v_cvt_pk_bf16_f32 v239,v254,v255
v_mfma_f32_16x16x32_bf16 a[68:71],a[204:207],v[204:207],a[68:71]
v_exp_f32 v136,v136
v_mfma_f32_16x16x32_bf16 a[68:71],a[200:203],v[200:203],a[68:71]
v_exp_f32 v137,v137
v_mfma_f32_16x16x32_bf16 a[72:75],a[208:211],v[200:203],a[72:75]
v_exp_f32 v138,v138
v_mfma_f32_16x16x32_bf16 a[72:75],a[212:215],v[204:207],a[72:75]
v_exp_f32 v139,v139
v_mfma_f32_16x16x32_bf16 a[76:79],a[220:223],v[204:207],a[76:79]
v_cvt_pk_bf16_f32 v136,v136,v137
v_cvt_pk_bf16_f32 v137,v138,v139
v_mfma_f32_16x16x32_bf16 a[76:79],a[216:219],v[200:203],a[76:79]
v_exp_f32 v140,v140
v_mfma_f32_16x16x32_bf16 a[80:83],a[224:227],v[200:203],a[80:83]
v_exp_f32 v141,v141
v_mfma_f32_16x16x32_bf16 a[80:83],a[228:231],v[204:207],a[80:83]
v_exp_f32 v142,v142
v_mfma_f32_16x16x32_bf16 a[84:87],a[236:239],v[204:207],a[84:87]
v_exp_f32 v143,v143
v_mfma_f32_16x16x32_bf16 a[84:87],a[232:235],v[200:203],a[84:87]
v_cvt_pk_bf16_f32 v138,v140,v141
v_cvt_pk_bf16_f32 v139,v142,v143
v_mfma_f32_16x16x32_bf16 a[88:91],a[240:243],v[200:203],a[88:91]
v_exp_f32 v152,v152
v_mfma_f32_16x16x32_bf16 a[88:91],a[244:247],v[204:207],a[88:91]
v_exp_f32 v153,v153
v_mfma_f32_16x16x32_bf16 a[92:95],a[252:255],v[204:207],a[92:95]
buffer_store_dwordx4 v[92:95],v6,s[36:39],s85 offen offset:3072 sc1
v_mfma_f32_16x16x32_bf16 a[92:95],a[248:251],v[200:203],a[92:95]
s_add_u32 s85,s55,69632
v_exp_f32 v154,v154
v_smfmac_f32_16x16x64_bf16 v[56:59],v[28:31],v[200:207],v27
s_max_i32 s85,s85,s19
v_cvt_pk_bf16_f32 v140,v152,v153
v_mfma_f32_16x16x32_bf16 v[168:171],a[128:131],v[132:135],v[36:39]
v_exp_f32 v155,v155
v_mfma_f32_16x16x32_bf16 v[168:171],a[132:135],v[164:167],v[168:171]
v_exp_f32 v156,v156
v_mfma_f32_16x16x32_bf16 v[168:171],a[136:139],v[196:199],v[168:171]
v_exp_f32 v157,v157
v_mfma_f32_16x16x32_bf16 v[168:171],a[140:143],v[228:231],v[168:171]
buffer_load_dwordx4 v9,s[72:75],s83 offen offset:2048 lds
v_mfma_f32_16x16x32_bf16 v[172:175],a[156:159],v[228:231],v[36:39]
v_exp_f32 v158,v158
v_mfma_f32_16x16x32_bf16 v[172:175],a[152:155],v[196:199],v[172:175]
v_exp_f32 v159,v159
v_mfma_f32_16x16x32_bf16 v[172:175],a[148:151],v[164:167],v[172:175]
v_cvt_pk_bf16_f32 v141,v154,v155
v_cvt_pk_bf16_f32 v142,v156,v157
v_mfma_f32_16x16x32_bf16 v[172:175],a[144:147],v[132:135],v[172:175]
v_cvt_pk_bf16_f32 v143,v158,v159
v_add_u32 v1,s48,v1
v_mfma_f32_16x16x32_bf16 v[184:187],a[160:163],v[132:135],v[36:39]
v_mfma_f32_16x16x32_bf16 v[184:187],a[164:167],v[164:167],v[184:187]
v_exp_f32 v168,v168
v_mfma_f32_16x16x32_bf16 v[184:187],a[168:171],v[196:199],v[184:187]
v_exp_f32 v169,v169
v_mfma_f32_16x16x32_bf16 v[184:187],a[172:175],v[228:231],v[184:187]
v_exp_f32 v170,v170
v_mfma_f32_16x16x32_bf16 v[188:191],a[188:191],v[228:231],v[36:39]
v_exp_f32 v171,v171
v_mfma_f32_16x16x32_bf16 v[188:191],a[184:187],v[196:199],v[188:191]
v_exp_f32 v172,v172
v_mfma_f32_16x16x32_bf16 v[188:191],a[180:183],v[164:167],v[188:191]
v_exp_f32 v173,v173
v_mfma_f32_16x16x32_bf16 v[188:191],a[176:179],v[132:135],v[188:191]
v_exp_f32 v174,v174
v_mfma_f32_16x16x32_bf16 a[96:99],a[192:195],v[232:235],a[96:99]
ds_read_b64_tr_b16 a[192:193],v2
ds_read_b64_tr_b16 a[194:195],v2 offset:4096
v_mfma_f32_16x16x32_bf16 a[96:99],a[196:199],v[236:239],a[96:99]
ds_read_b64_tr_b16 a[196:197],v2 offset:8192
v_cvt_pk_bf16_f32 v168,v168,v169
v_mfma_f32_16x16x32_bf16 a[100:103],a[204:207],v[236:239],a[100:103]
ds_read_b64_tr_b16 a[198:199],v2 offset:12288
v_cvt_pk_bf16_f32 v169,v170,v171
v_mfma_f32_16x16x32_bf16 a[100:103],a[200:203],v[232:235],a[100:103]
buffer_store_dwordx4 v[96:99],v6,s[36:39],s86 offen sc1
v_mfma_f32_16x16x32_bf16 a[104:107],a[208:211],v[232:235],a[104:107]
ds_read_b64_tr_b16 a[204:205],v3 offset:8192
ds_read_b64_tr_b16 a[206:207],v3 offset:12288
v_mfma_f32_16x16x32_bf16 a[104:107],a[212:215],v[236:239],a[104:107]
ds_read_b64_tr_b16 a[200:201],v3
v_cvt_pk_bf16_f32 v170,v172,v173
v_mfma_f32_16x16x32_bf16 a[108:111],a[220:223],v[236:239],a[108:111]
v_exp_f32 v175,v175
v_mfma_f32_16x16x32_bf16 a[108:111],a[216:219],v[232:235],a[108:111]
buffer_load_dwordx4 v10,s[72:75],s83 offen offset:3072 lds
v_mfma_f32_16x16x32_bf16 a[112:115],a[224:227],v[232:235],a[112:115]
ds_read_b64_tr_b16 a[202:203],v3 offset:4096
ds_read_b64_tr_b16 a[208:209],v2 offset:256
v_mfma_f32_16x16x32_bf16 a[112:115],a[228:231],v[236:239],a[112:115]
ds_read_b64_tr_b16 a[210:211],v2 offset:4352
v_cvt_pk_bf16_f32 v171,v174,v175
v_mfma_f32_16x16x32_bf16 a[116:119],a[236:239],v[236:239],a[116:119]
v_exp_f32 v184,v184
v_mfma_f32_16x16x32_bf16 a[116:119],a[232:235],v[232:235],a[116:119]
ds_read_b64_tr_b16 a[212:213],v2 offset:8448
ds_read_b64_tr_b16 a[214:215],v2 offset:12544
v_mfma_f32_16x16x32_bf16 a[120:123],a[240:243],v[232:235],a[120:123]
v_exp_f32 v185,v185
v_mfma_f32_16x16x32_bf16 a[120:123],a[244:247],v[236:239],a[120:123]
ds_read_b64_tr_b16 a[220:221],v3 offset:8448
ds_read_b64_tr_b16 a[222:223],v3 offset:12544
v_mfma_f32_16x16x32_bf16 a[124:127],a[252:255],v[236:239],a[124:127]
ds_read_b64_tr_b16 a[216:217],v3 offset:256
v_cvt_pk_bf16_f32 v172,v184,v185
v_mfma_f32_16x16x32_bf16 a[124:127],a[248:251],v[232:235],a[124:127]
ds_read_b64_tr_b16 a[218:219],v3 offset:4352
v_smfmac_f32_16x16x64_bf16 v[60:63],v[28:31],v[232:239],v27
ds_read_b64_tr_b16 a[224:225],v2 offset:512
v_mfma_f32_16x16x32_bf16 v[200:203],a[128:131],v[144:147],v[40:43]
ds_read_b64_tr_b16 a[226:227],v2 offset:4608
ds_read_b64_tr_b16 a[228:229],v2 offset:8704
v_mfma_f32_16x16x32_bf16 v[200:203],a[132:135],v[176:179],v[200:203]
ds_read_b64_tr_b16 a[230:231],v2 offset:12800
v_mfma_f32_16x16x32_bf16 v[200:203],a[136:139],v[208:211],v[200:203]
ds_read_b64_tr_b16 a[236:237],v3 offset:8704
v_mfma_f32_16x16x32_bf16 v[200:203],a[140:143],v[240:243],v[200:203]
ds_read_b64_tr_b16 a[238:239],v3 offset:12800
v_mfma_f32_16x16x32_bf16 v[204:207],a[156:159],v[240:243],v[40:43]
ds_read_b64_tr_b16 a[232:233],v3 offset:512
ds_read_b64_tr_b16 a[234:235],v3 offset:4608
v_mfma_f32_16x16x32_bf16 v[204:207],a[152:155],v[208:211],v[204:207]
ds_read_b64_tr_b16 a[240:241],v2 offset:768
v_mfma_f32_16x16x32_bf16 v[204:207],a[148:151],v[176:179],v[204:207]
ds_read_b64_tr_b16 a[242:243],v2 offset:4864
v_mfma_f32_16x16x32_bf16 v[204:207],a[144:147],v[144:147],v[204:207]
s_add_u32 m0,s82,91136
ds_read_b64_tr_b16 a[244:245],v2 offset:8960
v_mfma_f32_16x16x32_bf16 v[216:219],a[160:163],v[144:147],v[40:43]
buffer_store_dwordx4 v[100:103],v6,s[36:39],s86 offen offset:1024 sc1
v_mfma_f32_16x16x32_bf16 v[216:219],a[164:167],v[176:179],v[216:219]
ds_read_b64_tr_b16 a[246:247],v2 offset:13056
ds_read_b64_tr_b16 a[252:253],v3 offset:8960
v_mfma_f32_16x16x32_bf16 v[216:219],a[168:171],v[208:211],v[216:219]
ds_read_b64_tr_b16 a[254:255],v3 offset:13056
v_add_u32 v2,s48,v2
v_mfma_f32_16x16x32_bf16 v[216:219],a[172:175],v[240:243],v[216:219]
ds_read_b64_tr_b16 a[248:249],v3 offset:768
v_mfma_f32_16x16x32_bf16 v[220:223],a[188:191],v[240:243],v[40:43]
buffer_load_dwordx4 v11,s[72:75],s83 offen lds
v_mfma_f32_16x16x32_bf16 v[220:223],a[184:187],v[208:211],v[220:223]
ds_read_b64_tr_b16 a[250:251],v3 offset:4864
v_add_u32 v3,s48,v3
v_mfma_f32_16x16x32_bf16 v[220:223],a[180:183],v[176:179],v[220:223]
v_exp_f32 v186,v186
v_mfma_f32_16x16x32_bf16 v[220:223],a[176:179],v[144:147],v[220:223]
s_waitcnt lgkmcnt(5)
v_exp_f32 v187,v187
v_mfma_f32_16x16x32_bf16 a[0:3],a[192:195],v[136:139],a[0:3]
v_exp_f32 v188,v188
v_mfma_f32_16x16x32_bf16 a[0:3],a[196:199],v[140:143],a[0:3]
s_add_u32 s80,s71,0
s_and_b32 s80,s80,3
s_lshl_b32 s80,s80,1
v_mfma_f32_16x16x32_bf16 a[4:7],a[204:207],v[140:143],a[4:7]
s_or_b32 s80,s80,1
s_mul_i32 s80,s80,163840
s_add_u32 s80,s80,s81
v_mfma_f32_16x16x32_bf16 a[4:7],a[200:203],v[136:139],a[4:7]
s_add_u32 s83,s80,20480
v_exp_f32 v189,v189
v_mfma_f32_16x16x32_bf16 a[8:11],a[208:211],v[136:139],a[8:11]
v_exp_f32 v190,v190
v_mfma_f32_16x16x32_bf16 a[8:11],a[212:215],v[140:143],a[8:11]
v_exp_f32 v191,v191
v_mfma_f32_16x16x32_bf16 a[12:15],a[220:223],v[140:143],a[12:15]
v_cvt_pk_bf16_f32 v173,v186,v187
v_cvt_pk_bf16_f32 v174,v188,v189
v_mfma_f32_16x16x32_bf16 a[12:15],a[216:219],v[136:139],a[12:15]
v_exp_f32 v200,v200
v_mfma_f32_16x16x32_bf16 a[16:19],a[224:227],v[136:139],a[16:19]
v_exp_f32 v201,v201
v_mfma_f32_16x16x32_bf16 a[16:19],a[228:231],v[140:143],a[16:19]
v_cvt_pk_bf16_f32 v175,v190,v191
v_cvt_pk_bf16_f32 v200,v200,v201
v_mfma_f32_16x16x32_bf16 a[20:23],a[236:239],v[140:143],a[20:23]
v_exp_f32 v202,v202
v_mfma_f32_16x16x32_bf16 a[20:23],a[232:235],v[136:139],a[20:23]
v_exp_f32 v203,v203
v_mfma_f32_16x16x32_bf16 a[24:27],a[240:243],v[136:139],a[24:27]
s_waitcnt lgkmcnt(0)
v_exp_f32 v204,v204
v_mfma_f32_16x16x32_bf16 a[24:27],a[244:247],v[140:143],a[24:27]
v_exp_f32 v205,v205
v_mfma_f32_16x16x32_bf16 a[28:31],a[252:255],v[140:143],a[28:31]
buffer_store_dwordx4 v[104:107],v6,s[36:39],s86 offen offset:2048 sc1
v_mfma_f32_16x16x32_bf16 a[28:31],a[248:251],v[136:139],a[28:31]
v_exp_f32 v206,v206
v_smfmac_f32_16x16x64_bf16 v[48:51],v[28:31],v[136:143],v27
v_cvt_pk_bf16_f32 v201,v202,v203
v_mfma_f32_16x16x32_bf16 v[232:235],a[128:131],v[148:151],v[44:47]
)A"
R"A(v_exp_f32 v207,v207
v_mfma_f32_16x16x32_bf16 v[232:235],a[132:135],v[180:183],v[232:235]
s_barrier
ds_read_b128 a[128:131],v1
v_cvt_pk_bf16_f32 v202,v204,v205
v_mfma_f32_16x16x32_bf16 v[232:235],a[136:139],v[212:215],v[232:235]
v_exp_f32 v216,v216
v_mfma_f32_16x16x32_bf16 v[232:235],a[140:143],v[244:247],v[232:235]
ds_read_b128 a[132:135],v1 offset:256
v_cvt_pk_bf16_f32 v203,v206,v207
v_mfma_f32_16x16x32_bf16 v[236:239],a[156:159],v[244:247],v[44:47]
s_add_u32 m0,s82,0
v_exp_f32 v217,v217
v_mfma_f32_16x16x32_bf16 v[236:239],a[152:155],v[212:215],v[236:239]
ds_read_b128 a[136:139],v1 offset:512
v_cvt_pk_bf16_f32 v204,v216,v217
v_mfma_f32_16x16x32_bf16 v[236:239],a[148:151],v[180:183],v[236:239]
v_exp_f32 v218,v218
v_mfma_f32_16x16x32_bf16 v[236:239],a[144:147],v[148:151],v[236:239]
buffer_load_dwordx4 v7,s[72:75],s80 offen lds
v_mfma_f32_16x16x32_bf16 v[248:251],a[160:163],v[148:151],v[44:47]
v_exp_f32 v219,v219
v_mfma_f32_16x16x32_bf16 v[248:251],a[164:167],v[180:183],v[248:251]
ds_read_b128 a[140:143],v1 offset:768
v_cvt_pk_bf16_f32 v205,v218,v219
v_mfma_f32_16x16x32_bf16 v[248:251],a[168:171],v[212:215],v[248:251]
v_exp_f32 v220,v220
v_mfma_f32_16x16x32_bf16 v[248:251],a[172:175],v[244:247],v[248:251]
ds_read_b128 a[156:159],v1 offset:4864
v_mfma_f32_16x16x32_bf16 v[252:255],a[188:191],v[244:247],v[44:47]
v_exp_f32 v221,v221
v_mfma_f32_16x16x32_bf16 v[252:255],a[184:187],v[212:215],v[252:255]
ds_read_b128 a[152:155],v1 offset:4608
v_cvt_pk_bf16_f32 v206,v220,v221
v_mfma_f32_16x16x32_bf16 v[252:255],a[180:183],v[180:183],v[252:255]
v_exp_f32 v222,v222
v_mfma_f32_16x16x32_bf16 v[252:255],a[176:179],v[148:151],v[252:255]
ds_read_b128 a[148:151],v1 offset:4352
v_mfma_f32_16x16x32_bf16 a[32:35],a[192:195],v[168:171],a[32:35]
v_exp_f32 v223,v223
v_mfma_f32_16x16x32_bf16 a[32:35],a[196:199],v[172:175],a[32:35]
ds_read_b128 a[144:147],v1 offset:4096
v_cvt_pk_bf16_f32 v207,v222,v223
v_mfma_f32_16x16x32_bf16 a[36:39],a[204:207],v[172:175],a[36:39]
buffer_store_dwordx4 v[108:111],v6,s[36:39],s86 offen offset:3072 sc1
v_mfma_f32_16x16x32_bf16 a[36:39],a[200:203],v[168:171],a[36:39]
s_add_u32 s86,s55,73728
s_max_i32 s86,s86,s100
ds_read_b128 a[160:163],v1 offset:8192
v_mfma_f32_16x16x32_bf16 a[40:43],a[208:211],v[168:171],a[40:43]
v_exp_f32 v232,v232
v_mfma_f32_16x16x32_bf16 a[40:43],a[212:215],v[172:175],a[40:43]
ds_read_b128 a[164:167],v1 offset:8448
v_mfma_f32_16x16x32_bf16 a[44:47],a[220:223],v[172:175],a[44:47]
buffer_load_dwordx4 v8,s[72:75],s80 offen offset:1024 lds
v_mfma_f32_16x16x32_bf16 a[44:47],a[216:219],v[168:171],a[44:47]
ds_read_b128 a[168:171],v1 offset:8704
v_mfma_f32_16x16x32_bf16 a[48:51],a[224:227],v[168:171],a[48:51]
v_exp_f32 v233,v233
v_mfma_f32_16x16x32_bf16 a[48:51],a[228:231],v[172:175],a[48:51]
ds_read_b128 a[172:175],v1 offset:8960
v_cvt_pk_bf16_f32 v232,v232,v233
v_mfma_f32_16x16x32_bf16 a[52:55],a[236:239],v[172:175],a[52:55]
v_exp_f32 v234,v234
v_mfma_f32_16x16x32_bf16 a[52:55],a[232:235],v[168:171],a[52:55]
ds_read_b128 a[188:191],v1 offset:13056
v_mfma_f32_16x16x32_bf16 a[56:59],a[240:243],v[168:171],a[56:59]
v_exp_f32 v235,v235
v_mfma_f32_16x16x32_bf16 a[56:59],a[244:247],v[172:175],a[56:59]
buffer_load_dwordx4 v9,s[72:75],s80 offen offset:2048 lds
v_mfma_f32_16x16x32_bf16 a[60:63],a[252:255],v[172:175],a[60:63]
ds_read_b128 a[184:187],v1 offset:12800
v_cvt_pk_bf16_f32 v233,v234,v235
v_mfma_f32_16x16x32_bf16 a[60:63],a[248:251],v[168:171],a[60:63]
v_exp_f32 v236,v236
v_smfmac_f32_16x16x64_bf16 v[52:55],v[28:31],v[168:175],v27
s_waitcnt lgkmcnt(3)
ds_read_b128 a[180:183],v1 offset:12544
v_mfma_f32_16x16x32_bf16 v[136:139],a[128:131],v[128:131],v[32:35]
v_exp_f32 v237,v237
v_mfma_f32_16x16x32_bf16 v[136:139],a[132:135],v[160:163],v[136:139]
ds_read_b128 a[176:179],v1 offset:12288
v_cvt_pk_bf16_f32 v234,v236,v237
v_mfma_f32_16x16x32_bf16 v[136:139],a[136:139],v[192:195],v[136:139]
v_exp_f32 v238,v238
v_mfma_f32_16x16x32_bf16 v[136:139],a[140:143],v[224:227],v[136:139]
v_exp_f32 v239,v239
v_mfma_f32_16x16x32_bf16 v[140:143],a[156:159],v[224:227],v[32:35]
v_cvt_pk_bf16_f32 v235,v238,v239
v_add_u32 v1,v4,v5
v_mfma_f32_16x16x32_bf16 v[140:143],a[152:155],v[192:195],v[140:143]
v_exp_f32 v248,v248
v_mfma_f32_16x16x32_bf16 v[140:143],a[148:151],v[160:163],v[140:143]
v_exp_f32 v249,v249
v_mfma_f32_16x16x32_bf16 v[140:143],a[144:147],v[128:131],v[140:143]
buffer_store_dwordx4 v[112:115],v6,s[36:39],s87 offen sc1
v_mfma_f32_16x16x32_bf16 v[152:155],a[160:163],v[128:131],v[32:35]
v_exp_f32 v250,v250
v_mfma_f32_16x16x32_bf16 v[152:155],a[164:167],v[160:163],v[152:155]
v_exp_f32 v251,v251
v_mfma_f32_16x16x32_bf16 v[152:155],a[168:171],v[192:195],v[152:155]
s_waitcnt lgkmcnt(0)
v_exp_f32 v252,v252
v_mfma_f32_16x16x32_bf16 v[152:155],a[172:175],v[224:227],v[152:155]
buffer_load_dwordx4 v10,s[72:75],s80 offen offset:3072 lds
v_mfma_f32_16x16x32_bf16 v[156:159],a[188:191],v[224:227],v[32:35]
v_exp_f32 v253,v253
v_mfma_f32_16x16x32_bf16 v[156:159],a[184:187],v[192:195],v[156:159]
v_exp_f32 v254,v254
v_mfma_f32_16x16x32_bf16 v[156:159],a[180:183],v[160:163],v[156:159]
v_exp_f32 v255,v255
v_mfma_f32_16x16x32_bf16 v[156:159],a[176:179],v[128:131],v[156:159]
s_add_u32 m0,s82,4096
v_cvt_pk_bf16_f32 v236,v248,v249
v_cvt_pk_bf16_f32 v237,v250,v251
v_mfma_f32_16x16x32_bf16 a[64:67],a[192:195],v[200:203],a[64:67]
v_cvt_pk_bf16_f32 v238,v252,v253
v_cvt_pk_bf16_f32 v239,v254,v255
v_mfma_f32_16x16x32_bf16 a[64:67],a[196:199],v[204:207],a[64:67]
v_exp_f32 v136,v136
v_mfma_f32_16x16x32_bf16 a[68:71],a[204:207],v[204:207],a[68:71]
buffer_load_dwordx4 v11,s[72:75],s80 offen lds
v_mfma_f32_16x16x32_bf16 a[68:71],a[200:203],v[200:203],a[68:71]
v_exp_f32 v137,v137
v_mfma_f32_16x16x32_bf16 a[72:75],a[208:211],v[200:203],a[72:75]
v_exp_f32 v138,v138
v_mfma_f32_16x16x32_bf16 a[72:75],a[212:215],v[204:207],a[72:75]
v_exp_f32 v139,v139
v_mfma_f32_16x16x32_bf16 a[76:79],a[220:223],v[204:207],a[76:79]
v_cvt_pk_bf16_f32 v136,v136,v137
v_cvt_pk_bf16_f32 v137,v138,v139
v_mfma_f32_16x16x32_bf16 a[76:79],a[216:219],v[200:203],a[76:79]
v_exp_f32 v140,v140
v_mfma_f32_16x16x32_bf16 a[80:83],a[224:227],v[200:203],a[80:83]
v_exp_f32 v141,v141
v_mfma_f32_16x16x32_bf16 a[80:83],a[228:231],v[204:207],a[80:83]
v_exp_f32 v142,v142
v_mfma_f32_16x16x32_bf16 a[84:87],a[236:239],v[204:207],a[84:87]
s_add_u32 m0,s82,5120
v_exp_f32 v143,v143
v_mfma_f32_16x16x32_bf16 a[84:87],a[232:235],v[200:203],a[84:87]
v_cvt_pk_bf16_f32 v138,v140,v141
v_cvt_pk_bf16_f32 v139,v142,v143
v_mfma_f32_16x16x32_bf16 a[88:91],a[240:243],v[200:203],a[88:91]
v_exp_f32 v152,v152
v_mfma_f32_16x16x32_bf16 a[88:91],a[244:247],v[204:207],a[88:91]
buffer_store_dwordx4 v[116:119],v6,s[36:39],s87 offen offset:1024 sc1
v_mfma_f32_16x16x32_bf16 a[92:95],a[252:255],v[204:207],a[92:95]
v_exp_f32 v153,v153
v_mfma_f32_16x16x32_bf16 a[92:95],a[248:251],v[200:203],a[92:95]
v_exp_f32 v154,v154
v_smfmac_f32_16x16x64_bf16 v[56:59],v[28:31],v[200:207],v27
v_cvt_pk_bf16_f32 v140,v152,v153
v_mfma_f32_16x16x32_bf16 v[168:171],a[128:131],v[132:135],v[36:39]
buffer_load_dwordx4 v7,s[72:75],s83 offen lds
v_mfma_f32_16x16x32_bf16 v[168:171],a[132:135],v[164:167],v[168:171]
v_exp_f32 v155,v155
v_mfma_f32_16x16x32_bf16 v[168:171],a[136:139],v[196:199],v[168:171]
v_exp_f32 v156,v156
v_mfma_f32_16x16x32_bf16 v[168:171],a[140:143],v[228:231],v[168:171]
v_exp_f32 v157,v157
v_mfma_f32_16x16x32_bf16 v[172:175],a[156:159],v[228:231],v[36:39]
v_exp_f32 v158,v158
v_mfma_f32_16x16x32_bf16 v[172:175],a[152:155],v[196:199],v[172:175]
v_exp_f32 v159,v159
v_mfma_f32_16x16x32_bf16 v[172:175],a[148:151],v[164:167],v[172:175]
v_cvt_pk_bf16_f32 v141,v154,v155
v_cvt_pk_bf16_f32 v142,v156,v157
v_mfma_f32_16x16x32_bf16 v[172:175],a[144:147],v[132:135],v[172:175]
buffer_load_dwordx4 v8,s[72:75],s83 offen offset:1024 lds
v_mfma_f32_16x16x32_bf16 v[184:187],a[160:163],v[132:135],v[36:39]
v_cvt_pk_bf16_f32 v143,v158,v159
v_add_u32 v1,81920,v1
v_mfma_f32_16x16x32_bf16 v[184:187],a[164:167],v[164:167],v[184:187]
v_exp_f32 v168,v168
v_mfma_f32_16x16x32_bf16 v[184:187],a[168:171],v[196:199],v[184:187]
v_exp_f32 v169,v169
v_mfma_f32_16x16x32_bf16 v[184:187],a[172:175],v[228:231],v[184:187]
v_exp_f32 v170,v170
v_mfma_f32_16x16x32_bf16 v[188:191],a[188:191],v[228:231],v[36:39]
v_exp_f32 v171,v171
v_mfma_f32_16x16x32_bf16 v[188:191],a[184:187],v[196:199],v[188:191]
v_exp_f32 v172,v172
v_mfma_f32_16x16x32_bf16 v[188:191],a[180:183],v[164:167],v[188:191]
v_exp_f32 v173,v173
v_mfma_f32_16x16x32_bf16 v[188:191],a[176:179],v[132:135],v[188:191]
v_exp_f32 v174,v174
v_mfma_f32_16x16x32_bf16 a[96:99],a[192:195],v[232:235],a[96:99]
ds_read_b64_tr_b16 a[192:193],v2
ds_read_b64_tr_b16 a[194:195],v2 offset:4096
v_mfma_f32_16x16x32_bf16 a[96:99],a[196:199],v[236:239],a[96:99]
ds_read_b64_tr_b16 a[196:197],v2 offset:8192
v_cvt_pk_bf16_f32 v168,v168,v169
v_mfma_f32_16x16x32_bf16 a[100:103],a[204:207],v[236:239],a[100:103]
buffer_store_dwordx4 v[120:123],v6,s[36:39],s87 offen offset:2048 sc1
v_mfma_f32_16x16x32_bf16 a[100:103],a[200:203],v[232:235],a[100:103]
ds_read_b64_tr_b16 a[198:199],v2 offset:12288
ds_read_b64_tr_b16 a[204:205],v3 offset:8192
v_mfma_f32_16x16x32_bf16 a[104:107],a[208:211],v[232:235],a[104:107]
ds_read_b64_tr_b16 a[206:207],v3 offset:12288
v_cvt_pk_bf16_f32 v169,v170,v171
v_mfma_f32_16x16x32_bf16 a[104:107],a[212:215],v[236:239],a[104:107]
ds_read_b64_tr_b16 a[200:201],v3
v_cvt_pk_bf16_f32 v170,v172,v173
v_mfma_f32_16x16x32_bf16 a[108:111],a[220:223],v[236:239],a[108:111]
buffer_load_dwordx4 v9,s[72:75],s83 offen offset:2048 lds
v_mfma_f32_16x16x32_bf16 a[108:111],a[216:219],v[232:235],a[108:111]
ds_read_b64_tr_b16 a[202:203],v3 offset:4096
ds_read_b64_tr_b16 a[208:209],v2 offset:256
v_mfma_f32_16x16x32_bf16 a[112:115],a[224:227],v[232:235],a[112:115]
v_exp_f32 v175,v175
v_mfma_f32_16x16x32_bf16 a[112:115],a[228:231],v[236:239],a[112:115]
ds_read_b64_tr_b16 a[210:211],v2 offset:4352
ds_read_b64_tr_b16 a[212:213],v2 offset:8448
v_mfma_f32_16x16x32_bf16 a[116:119],a[236:239],v[236:239],a[116:119]
ds_read_b64_tr_b16 a[214:215],v2 offset:12544
v_cvt_pk_bf16_f32 v171,v174,v175
v_mfma_f32_16x16x32_bf16 a[116:119],a[232:235],v[232:235],a[116:119]
s_cmp_ge_u32 0,s71
s_cselect_b32 s80,2,0
ds_read_b64_tr_b16 a[220:221],v3 offset:8448
v_mfma_f32_16x16x32_bf16 a[120:123],a[240:243],v[232:235],a[120:123]
s_mul_i32 s80,s80,163840
v_exp_f32 v184,v184
v_mfma_f32_16x16x32_bf16 a[120:123],a[244:247],v[236:239],a[120:123]
s_add_u32 s80,s80,s81
buffer_load_dwordx4 v10,s[72:75],s83 offen offset:3072 lds
v_mfma_f32_16x16x32_bf16 a[124:127],a[252:255],v[236:239],a[124:127]
ds_read_b64_tr_b16 a[222:223],v3 offset:12544
ds_read_b64_tr_b16 a[216:217],v3 offset:256
v_mfma_f32_16x16x32_bf16 a[124:127],a[248:251],v[232:235],a[124:127]
ds_read_b64_tr_b16 a[218:219],v3 offset:4352
v_smfmac_f32_16x16x64_bf16 v[60:63],v[28:31],v[232:239],v27
ds_read_b64_tr_b16 a[224:225],v2 offset:512
v_mfma_f32_16x16x32_bf16 v[200:203],a[128:131],v[144:147],v[40:43]
ds_read_b64_tr_b16 a[226:227],v2 offset:4608
v_mfma_f32_16x16x32_bf16 v[200:203],a[132:135],v[176:179],v[200:203]
ds_read_b64_tr_b16 a[228:229],v2 offset:8704
ds_read_b64_tr_b16 a[230:231],v2 offset:12800
v_mfma_f32_16x16x32_bf16 v[200:203],a[136:139],v[208:211],v[200:203]
ds_read_b64_tr_b16 a[236:237],v3 offset:8704
v_mfma_f32_16x16x32_bf16 v[200:203],a[140:143],v[240:243],v[200:203]
ds_read_b64_tr_b16 a[238:239],v3 offset:12800
v_mfma_f32_16x16x32_bf16 v[204:207],a[156:159],v[240:243],v[40:43]
s_add_u32 m0,s82,9216
ds_read_b64_tr_b16 a[232:233],v3 offset:512
v_mfma_f32_16x16x32_bf16 v[204:207],a[152:155],v[208:211],v[204:207]
ds_read_b64_tr_b16 a[234:235],v3 offset:4608
ds_read_b64_tr_b16 a[240:241],v2 offset:768
v_mfma_f32_16x16x32_bf16 v[204:207],a[148:151],v[176:179],v[204:207]
ds_read_b64_tr_b16 a[242:243],v2 offset:4864
v_mfma_f32_16x16x32_bf16 v[204:207],a[144:147],v[144:147],v[204:207]
buffer_store_dwordx4 v[124:127],v6,s[36:39],s87 offen offset:3072 sc1
v_mfma_f32_16x16x32_bf16 v[216:219],a[160:163],v[144:147],v[40:43]
s_add_u32 s87,s55,77824
ds_read_b64_tr_b16 a[244:245],v2 offset:8960
)A"
R"A(ds_read_b64_tr_b16 a[246:247],v2 offset:13056
v_mfma_f32_16x16x32_bf16 v[216:219],a[164:167],v[176:179],v[216:219]
s_max_i32 s87,s87,s101
ds_read_b64_tr_b16 a[252:253],v3 offset:8960
v_sub_u32 v2,v4,v5
v_mfma_f32_16x16x32_bf16 v[216:219],a[168:171],v[208:211],v[216:219]
ds_read_b64_tr_b16 a[254:255],v3 offset:13056
v_add_u32 v2,81920,v2
v_mfma_f32_16x16x32_bf16 v[216:219],a[172:175],v[240:243],v[216:219]
buffer_load_dwordx4 v11,s[72:75],s83 offen lds
v_mfma_f32_16x16x32_bf16 v[220:223],a[188:191],v[240:243],v[40:43]
s_add_u32 s83,s80,20480
ds_read_b64_tr_b16 a[248:249],v3 offset:768
ds_read_b64_tr_b16 a[250:251],v3 offset:4864
v_mfma_f32_16x16x32_bf16 v[220:223],a[184:187],v[208:211],v[220:223]
v_exp_f32 v185,v185
v_mfma_f32_16x16x32_bf16 v[220:223],a[180:183],v[176:179],v[220:223]
v_exp_f32 v186,v186
v_mfma_f32_16x16x32_bf16 v[220:223],a[176:179],v[144:147],v[220:223]
s_add_u32 m0,s82,40960
v_exp_f32 v187,v187
s_waitcnt lgkmcnt(6)
v_mfma_f32_16x16x32_bf16 a[0:3],a[192:195],v[136:139],a[0:3]
v_exp_f32 v188,v188
v_mfma_f32_16x16x32_bf16 a[0:3],a[196:199],v[140:143],a[0:3]
v_exp_f32 v189,v189
v_mfma_f32_16x16x32_bf16 a[4:7],a[204:207],v[140:143],a[4:7]
buffer_load_dwordx4 v7,s[72:75],s80 offen lds
v_mfma_f32_16x16x32_bf16 a[4:7],a[200:203],v[136:139],a[4:7]
v_exp_f32 v190,v190
v_mfma_f32_16x16x32_bf16 a[8:11],a[208:211],v[136:139],a[8:11]
v_exp_f32 v191,v191
v_mfma_f32_16x16x32_bf16 a[8:11],a[212:215],v[140:143],a[8:11]
v_cvt_pk_bf16_f32 v172,v184,v185
v_cvt_pk_bf16_f32 v173,v186,v187
v_mfma_f32_16x16x32_bf16 a[12:15],a[220:223],v[140:143],a[12:15]
v_cvt_pk_bf16_f32 v174,v188,v189
v_cvt_pk_bf16_f32 v175,v190,v191
v_mfma_f32_16x16x32_bf16 a[12:15],a[216:219],v[136:139],a[12:15]
v_exp_f32 v200,v200
v_mfma_f32_16x16x32_bf16 a[16:19],a[224:227],v[136:139],a[16:19]
v_exp_f32 v201,v201
v_mfma_f32_16x16x32_bf16 a[16:19],a[228:231],v[140:143],a[16:19]
v_exp_f32 v202,v202
v_mfma_f32_16x16x32_bf16 a[20:23],a[236:239],v[140:143],a[20:23]
v_exp_f32 v203,v203
v_mfma_f32_16x16x32_bf16 a[20:23],a[232:235],v[136:139],a[20:23]
v_exp_f32 v204,v204
v_mfma_f32_16x16x32_bf16 a[24:27],a[240:243],v[136:139],a[24:27]
s_waitcnt lgkmcnt(0)
v_exp_f32 v205,v205
v_mfma_f32_16x16x32_bf16 a[24:27],a[244:247],v[140:143],a[24:27]
buffer_load_dwordx4 v8,s[72:75],s80 offen offset:1024 lds
v_mfma_f32_16x16x32_bf16 a[28:31],a[252:255],v[140:143],a[28:31]
v_exp_f32 v206,v206
v_mfma_f32_16x16x32_bf16 a[28:31],a[248:251],v[136:139],a[28:31]
s_waitcnt vmcnt(18)
v_cvt_pk_bf16_f32 v200,v200,v201
v_cvt_pk_bf16_f32 v201,v202,v203
v_smfmac_f32_16x16x64_bf16 v[48:51],v[28:31],v[136:143],v27
s_barrier
v_exp_f32 v207,v207
v_mfma_f32_16x16x32_bf16 v[232:235],a[128:131],v[148:151],v[44:47]
v_cvt_pk_bf16_f32 v202,v204,v205
v_cvt_pk_bf16_f32 v203,v206,v207
v_mfma_f32_16x16x32_bf16 v[232:235],a[132:135],v[180:183],v[232:235]
v_exp_f32 v216,v216
v_mfma_f32_16x16x32_bf16 v[232:235],a[136:139],v[212:215],v[232:235]
v_exp_f32 v217,v217
v_mfma_f32_16x16x32_bf16 v[232:235],a[140:143],v[244:247],v[232:235]
v_exp_f32 v218,v218
v_mfma_f32_16x16x32_bf16 v[236:239],a[156:159],v[244:247],v[44:47]
v_exp_f32 v219,v219
v_mfma_f32_16x16x32_bf16 v[236:239],a[152:155],v[212:215],v[236:239]
v_exp_f32 v220,v220
v_mfma_f32_16x16x32_bf16 v[236:239],a[148:151],v[180:183],v[236:239]
buffer_load_dwordx4 v9,s[72:75],s80 offen offset:2048 lds
v_mfma_f32_16x16x32_bf16 v[236:239],a[144:147],v[148:151],v[236:239]
v_exp_f32 v221,v221
v_mfma_f32_16x16x32_bf16 v[248:251],a[160:163],v[148:151],v[44:47]
v_exp_f32 v222,v222
v_mfma_f32_16x16x32_bf16 v[248:251],a[164:167],v[180:183],v[248:251]
v_exp_f32 v223,v223
v_mfma_f32_16x16x32_bf16 v[248:251],a[168:171],v[212:215],v[248:251]
v_cvt_pk_bf16_f32 v204,v216,v217
v_cvt_pk_bf16_f32 v205,v218,v219
v_mfma_f32_16x16x32_bf16 v[248:251],a[172:175],v[244:247],v[248:251]
v_cvt_pk_bf16_f32 v206,v220,v221
v_cvt_pk_bf16_f32 v207,v222,v223
v_mfma_f32_16x16x32_bf16 v[252:255],a[188:191],v[244:247],v[44:47]
v_exp_f32 v232,v232
v_mfma_f32_16x16x32_bf16 v[252:255],a[184:187],v[212:215],v[252:255]
v_exp_f32 v233,v233
v_mfma_f32_16x16x32_bf16 v[252:255],a[180:183],v[180:183],v[252:255]
v_exp_f32 v234,v234
v_mfma_f32_16x16x32_bf16 v[252:255],a[176:179],v[148:151],v[252:255]
v_exp_f32 v235,v235
v_mfma_f32_16x16x32_bf16 a[32:35],a[192:195],v[168:171],a[32:35]
v_cvt_pk_bf16_f32 v232,v232,v233
v_add_f32 v48,v49,v48
v_mfma_f32_16x16x32_bf16 a[32:35],a[196:199],v[172:175],a[32:35]
buffer_load_dwordx4 v10,s[72:75],s80 offen offset:3072 lds
v_mfma_f32_16x16x32_bf16 a[36:39],a[204:207],v[172:175],a[36:39]
v_exp_f32 v236,v236
v_mfma_f32_16x16x32_bf16 a[36:39],a[200:203],v[168:171],a[36:39]
v_exp_f32 v237,v237
v_mfma_f32_16x16x32_bf16 a[40:43],a[208:211],v[168:171],a[40:43]
v_exp_f32 v238,v238
v_mfma_f32_16x16x32_bf16 a[40:43],a[212:215],v[172:175],a[40:43]
v_exp_f32 v239,v239
v_mfma_f32_16x16x32_bf16 a[44:47],a[220:223],v[172:175],a[44:47]
v_cvt_pk_bf16_f32 v233,v234,v235
v_max_f32 v20,0x00800000,v48
v_mfma_f32_16x16x32_bf16 a[44:47],a[216:219],v[168:171],a[44:47]
s_lshl_b32 s95,s42,2
s_add_u32 s95,s95,s40
s_lshl_b32 s95,s95,4
v_mfma_f32_16x16x32_bf16 a[48:51],a[224:227],v[168:171],a[48:51]
v_mov_b32 v18,s68
v_mov_b32 v19,s69
v_mfma_f32_16x16x32_bf16 a[48:51],a[228:231],v[172:175],a[48:51]
s_add_u32 m0,s82,45056
v_cvt_pk_bf16_f32 v234,v236,v237
v_cvt_pk_bf16_f32 v235,v238,v239
v_mfma_f32_16x16x32_bf16 a[52:55],a[236:239],v[172:175],a[52:55]
v_exp_f32 v248,v248
v_mfma_f32_16x16x32_bf16 a[52:55],a[232:235],v[168:171],a[52:55]
v_exp_f32 v249,v249
v_mfma_f32_16x16x32_bf16 a[56:59],a[240:243],v[168:171],a[56:59]
buffer_load_dwordx4 v11,s[72:75],s80 offen lds
v_mfma_f32_16x16x32_bf16 a[56:59],a[244:247],v[172:175],a[56:59]
v_exp_f32 v250,v250
v_mfma_f32_16x16x32_bf16 a[60:63],a[252:255],v[172:175],a[60:63]
v_exp_f32 v251,v251
v_mfma_f32_16x16x32_bf16 a[60:63],a[248:251],v[168:171],a[60:63]
v_exp_f32 v252,v252
v_smfmac_f32_16x16x64_bf16 v[52:55],v[28:31],v[168:175],v27
s_waitcnt vmcnt(6)
s_mov_b64 exec,1
buffer_store_dwordx2 v[18:19],v6,s[64:67],s95 offen sc1
s_mov_b64 exec,-1
v_mfma_f32_16x16x32_bf16 a[64:67],a[192:195],v[200:203],a[64:67]
v_exp_f32 v253,v253
v_mfma_f32_16x16x32_bf16 a[64:67],a[196:199],v[204:207],a[64:67]
v_exp_f32 v254,v254
v_mfma_f32_16x16x32_bf16 a[68:71],a[204:207],v[204:207],a[68:71]
v_exp_f32 v255,v255
v_mfma_f32_16x16x32_bf16 a[68:71],a[200:203],v[200:203],a[68:71]
s_add_u32 m0,s82,46080
v_rcp_f32 v20,v20
v_mfma_f32_16x16x32_bf16 a[72:75],a[208:211],v[200:203],a[72:75]
v_cvt_pk_bf16_f32 v236,v248,v249
v_cvt_pk_bf16_f32 v237,v250,v251
v_mfma_f32_16x16x32_bf16 a[72:75],a[212:215],v[204:207],a[72:75]
v_cvt_pk_bf16_f32 v238,v252,v253
v_cvt_pk_bf16_f32 v239,v254,v255
v_mfma_f32_16x16x32_bf16 a[76:79],a[220:223],v[204:207],a[76:79]
buffer_load_dwordx4 v7,s[72:75],s83 offen lds
v_mfma_f32_16x16x32_bf16 a[76:79],a[216:219],v[200:203],a[76:79]
v_accvgpr_read_b32 v136,a0
v_accvgpr_read_b32 v137,a1
v_accvgpr_read_b32 v138,a2
v_accvgpr_read_b32 v139,a3
v_accvgpr_read_b32 v12,a4
v_accvgpr_read_b32 v13,a5
v_accvgpr_read_b32 v14,a6
v_accvgpr_read_b32 v15,a7
v_pk_mul_f32 v[136:137],v[20:21],v[136:137] op_sel:[0,0] op_sel_hi:[0,1]
v_pk_mul_f32 v[138:139],v[20:21],v[138:139] op_sel:[0,0] op_sel_hi:[0,1]
v_pk_mul_f32 v[12:13],v[20:21],v[12:13] op_sel:[0,0] op_sel_hi:[0,1]
v_pk_mul_f32 v[14:15],v[20:21],v[14:15] op_sel:[0,0] op_sel_hi:[0,1]
v_cvt_pk_bf16_f32 v136,v136,v137
v_cvt_pk_bf16_f32 v137,v138,v139
v_cvt_pk_bf16_f32 v138,v12,v13
v_cvt_pk_bf16_f32 v139,v14,v15
v_mfma_f32_16x16x32_bf16 a[80:83],a[224:227],v[200:203],a[80:83]
ds_read_b64_tr_b16 a[128:129],v1
ds_read_b64_tr_b16 a[130:131],v1 offset:5120
v_mfma_f32_16x16x32_bf16 a[80:83],a[228:231],v[204:207],a[80:83]
ds_read_b64_tr_b16 a[132:133],v1 offset:10240
v_accvgpr_read_b32 v152,a16
v_mfma_f32_16x16x32_bf16 a[84:87],a[236:239],v[204:207],a[84:87]
ds_read_b64_tr_b16 a[134:135],v1 offset:15360
v_accvgpr_read_b32 v153,a17
v_mfma_f32_16x16x32_bf16 a[84:87],a[232:235],v[200:203],a[84:87]
ds_read_b64_tr_b16 a[136:137],v1 offset:20480
v_mul_f32 v152,v20,v152
v_mfma_f32_16x16x32_bf16 a[88:91],a[240:243],v[200:203],a[88:91]
ds_read_b64_tr_b16 a[138:139],v1 offset:25600
ds_read_b64_tr_b16 a[140:141],v1 offset:30720
v_mfma_f32_16x16x32_bf16 a[88:91],a[244:247],v[204:207],a[88:91]
v_accvgpr_read_b32 v140,a8
v_accvgpr_read_b32 v141,a9
v_accvgpr_read_b32 v142,a10
v_accvgpr_read_b32 v143,a11
v_accvgpr_read_b32 v12,a12
v_accvgpr_read_b32 v13,a13
v_accvgpr_read_b32 v14,a14
v_accvgpr_read_b32 v15,a15
v_pk_mul_f32 v[140:141],v[20:21],v[140:141] op_sel:[0,0] op_sel_hi:[0,1]
v_pk_mul_f32 v[142:143],v[20:21],v[142:143] op_sel:[0,0] op_sel_hi:[0,1]
v_pk_mul_f32 v[12:13],v[20:21],v[12:13] op_sel:[0,0] op_sel_hi:[0,1]
v_pk_mul_f32 v[14:15],v[20:21],v[14:15] op_sel:[0,0] op_sel_hi:[0,1]
v_cvt_pk_bf16_f32 v140,v140,v141
v_cvt_pk_bf16_f32 v141,v142,v143
v_cvt_pk_bf16_f32 v142,v12,v13
v_cvt_pk_bf16_f32 v143,v14,v15
v_mfma_f32_16x16x32_bf16 a[92:95],a[252:255],v[204:207],a[92:95]
ds_read_b64_tr_b16 a[142:143],v1 offset:35840
v_mul_f32 v153,v20,v153
v_mfma_f32_16x16x32_bf16 a[92:95],a[248:251],v[200:203],a[92:95]
v_accvgpr_read_b32 v154,a18
v_accvgpr_read_b32 v155,a19
v_smfmac_f32_16x16x64_bf16 v[56:59],v[28:31],v[200:207],v27
v_cvt_pk_bf16_f32 v152,v152,v153
v_mfma_f32_16x16x32_bf16 a[96:99],a[192:195],v[232:235],a[96:99]
buffer_store_dwordx4 v[136:139],v6,s[36:39],s84 offen sc1
v_mfma_f32_16x16x32_bf16 a[96:99],a[196:199],v[236:239],a[96:99]
v_mul_f32 v154,v20,v154
v_mul_f32 v155,v20,v155
v_mfma_f32_16x16x32_bf16 a[100:103],a[204:207],v[236:239],a[100:103]
v_cvt_pk_bf16_f32 v153,v154,v155
v_accvgpr_read_b32 v154,a20
v_mfma_f32_16x16x32_bf16 a[100:103],a[200:203],v[232:235],a[100:103]
v_accvgpr_read_b32 v155,a21
v_accvgpr_read_b32 v14,a22
v_mfma_f32_16x16x32_bf16 a[104:107],a[208:211],v[232:235],a[104:107]
buffer_load_dwordx4 v8,s[72:75],s83 offen offset:1024 lds
v_mfma_f32_16x16x32_bf16 a[104:107],a[212:215],v[236:239],a[104:107]
v_accvgpr_read_b32 v15,a23
v_mul_f32 v154,v20,v154
v_mfma_f32_16x16x32_bf16 a[108:111],a[220:223],v[236:239],a[108:111]
v_mul_f32 v155,v20,v155
v_mul_f32 v14,v20,v14
v_mfma_f32_16x16x32_bf16 a[108:111],a[216:219],v[232:235],a[108:111]
v_mul_f32 v15,v20,v15
v_cvt_pk_bf16_f32 v154,v154,v155
v_mfma_f32_16x16x32_bf16 a[112:115],a[224:227],v[232:235],a[112:115]
ds_read_b64_tr_b16 a[144:145],v2 offset:32
v_cvt_pk_bf16_f32 v155,v14,v15
v_mfma_f32_16x16x32_bf16 a[112:115],a[228:231],v[236:239],a[112:115]
ds_read_b64_tr_b16 a[146:147],v2 offset:5152
ds_read_b64_tr_b16 a[148:149],v2 offset:10272
v_mfma_f32_16x16x32_bf16 a[116:119],a[236:239],v[236:239],a[116:119]
ds_read_b64_tr_b16 a[150:151],v2 offset:15392
v_accvgpr_read_b32 v156,a24
v_mfma_f32_16x16x32_bf16 a[116:119],a[232:235],v[232:235],a[116:119]
buffer_load_dwordx4 v9,s[72:75],s83 offen offset:2048 lds
v_mfma_f32_16x16x32_bf16 a[120:123],a[240:243],v[232:235],a[120:123]
ds_read_b64_tr_b16 a[152:153],v2 offset:20512
ds_read_b64_tr_b16 a[154:155],v2 offset:25632
v_mfma_f32_16x16x32_bf16 a[120:123],a[244:247],v[236:239],a[120:123]
ds_read_b64_tr_b16 a[156:157],v2 offset:30752
v_accvgpr_read_b32 v157,a25
v_mfma_f32_16x16x32_bf16 a[124:127],a[252:255],v[236:239],a[124:127]
ds_read_b64_tr_b16 a[158:159],v2 offset:35872
v_mul_f32 v156,v20,v156
v_mfma_f32_16x16x32_bf16 a[124:127],a[248:251],v[232:235],a[124:127]
v_mul_f32 v157,v20,v157
v_accvgpr_read_b32 v158,a26
v_smfmac_f32_16x16x64_bf16 v[60:63],v[28:31],v[232:239],v27
s_waitcnt lgkmcnt(7)
v_accvgpr_read_b32 v159,a27
v_mfma_f32_16x16x32_bf16 a[192:195],a[128:131],v[64:67],0
v_cvt_pk_bf16_f32 v156,v156,v157
v_mul_f32 v158,v20,v158
v_mfma_f32_16x16x32_bf16 a[192:195],a[132:135],v[68:71],a[192:195]
v_mul_f32 v159,v20,v159
v_cvt_pk_bf16_f32 v157,v158,v159
v_mfma_f32_16x16x32_bf16 a[192:195],a[136:139],v[72:75],a[192:195]
buffer_store_dwordx4 v[140:143],v6,s[36:39],s84 offen offset:1024 sc1
v_mfma_f32_16x16x32_bf16 a[192:195],a[140:143],v[76:79],a[192:195]
v_accvgpr_read_b32 v158,a28
v_accvgpr_read_b32 v159,a29
v_mfma_f32_16x16x32_bf16 a[196:199],a[140:143],v[92:95],0
v_accvgpr_read_b32 v16,a30
v_accvgpr_read_b32 v17,a31
)A"
R"A(v_mfma_f32_16x16x32_bf16 a[196:199],a[136:139],v[88:91],a[196:199]
v_mul_f32 v158,v20,v158
v_mul_f32 v159,v20,v159
v_mfma_f32_16x16x32_bf16 a[196:199],a[132:135],v[84:87],a[196:199]
buffer_load_dwordx4 v10,s[72:75],s83 offen offset:3072 lds
v_mfma_f32_16x16x32_bf16 a[196:199],a[128:131],v[80:83],a[196:199]
v_mul_f32 v16,v20,v16
v_mul_f32 v17,v20,v17
v_mfma_f32_16x16x32_bf16 a[200:203],a[128:131],v[96:99],0
v_cvt_pk_bf16_f32 v158,v158,v159
v_cvt_pk_bf16_f32 v159,v16,v17
v_mfma_f32_16x16x32_bf16 a[200:203],a[132:135],v[100:103],a[200:203]
ds_read_b64_tr_b16 a[160:161],v1 offset:64
ds_read_b64_tr_b16 a[162:163],v1 offset:5184
v_mfma_f32_16x16x32_bf16 a[200:203],a[136:139],v[104:107],a[200:203]
ds_read_b64_tr_b16 a[164:165],v1 offset:10304
v_add_f32 v52,v53,v52
v_mfma_f32_16x16x32_bf16 a[200:203],a[140:143],v[108:111],a[200:203]
ds_read_b64_tr_b16 a[166:167],v1 offset:15424
v_max_f32 v21,0x00800000,v52
v_mfma_f32_16x16x32_bf16 a[204:207],a[140:143],v[124:127],0
s_cmp_ge_u32 1,s71
s_cselect_b32 s80,4,2
ds_read_b64_tr_b16 a[168:169],v1 offset:20544
v_mfma_f32_16x16x32_bf16 a[204:207],a[136:139],v[120:123],a[204:207]
s_add_u32 m0,s82,50176
ds_read_b64_tr_b16 a[170:171],v1 offset:25664
ds_read_b64_tr_b16 a[172:173],v1 offset:30784
v_mfma_f32_16x16x32_bf16 a[204:207],a[132:135],v[116:119],a[204:207]
s_mul_i32 s80,s80,163840
s_add_u32 s80,s80,s81
ds_read_b64_tr_b16 a[174:175],v1 offset:35904
v_mfma_f32_16x16x32_bf16 a[204:207],a[128:131],v[112:115],a[204:207]
s_waitcnt lgkmcnt(8)
v_rcp_f32 v21,v21
v_mfma_f32_16x16x32_bf16 a[208:211],a[144:147],v[64:67],0
buffer_load_dwordx4 v11,s[72:75],s83 offen lds
v_mfma_f32_16x16x32_bf16 a[208:211],a[148:151],v[68:71],a[208:211]
v_accvgpr_read_b32 v168,a32
v_accvgpr_read_b32 v169,a33
v_accvgpr_read_b32 v170,a34
v_accvgpr_read_b32 v171,a35
v_accvgpr_read_b32 v12,a36
v_accvgpr_read_b32 v13,a37
v_accvgpr_read_b32 v14,a38
v_accvgpr_read_b32 v15,a39
v_pk_mul_f32 v[168:169],v[20:21],v[168:169] op_sel:[1,0] op_sel_hi:[1,1]
v_pk_mul_f32 v[170:171],v[20:21],v[170:171] op_sel:[1,0] op_sel_hi:[1,1]
v_pk_mul_f32 v[12:13],v[20:21],v[12:13] op_sel:[1,0] op_sel_hi:[1,1]
v_pk_mul_f32 v[14:15],v[20:21],v[14:15] op_sel:[1,0] op_sel_hi:[1,1]
v_cvt_pk_bf16_f32 v168,v168,v169
v_cvt_pk_bf16_f32 v169,v170,v171
v_cvt_pk_bf16_f32 v170,v12,v13
v_cvt_pk_bf16_f32 v171,v14,v15
v_mfma_f32_16x16x32_bf16 a[208:211],a[152:155],v[72:75],a[208:211]
s_add_u32 s83,s80,20480
ds_read_b64_tr_b16 a[176:177],v2 offset:96
ds_read_b64_tr_b16 a[178:179],v2 offset:5216
v_mfma_f32_16x16x32_bf16 a[208:211],a[156:159],v[76:79],a[208:211]
ds_read_b64_tr_b16 a[180:181],v2 offset:10336
v_accvgpr_read_b32 v184,a48
v_mfma_f32_16x16x32_bf16 a[212:215],a[156:159],v[92:95],0
ds_read_b64_tr_b16 a[182:183],v2 offset:15456
v_accvgpr_read_b32 v185,a49
v_mfma_f32_16x16x32_bf16 a[212:215],a[152:155],v[88:91],a[212:215]
ds_read_b64_tr_b16 a[184:185],v2 offset:20576
v_mul_f32 v184,v21,v184
v_mfma_f32_16x16x32_bf16 a[212:215],a[148:151],v[84:87],a[212:215]
buffer_store_dwordx4 v[152:155],v6,s[36:39],s84 offen offset:2048 sc1
v_mfma_f32_16x16x32_bf16 a[212:215],a[144:147],v[80:83],a[212:215]
ds_read_b64_tr_b16 a[186:187],v2 offset:25696
ds_read_b64_tr_b16 a[188:189],v2 offset:30816
v_mfma_f32_16x16x32_bf16 a[216:219],a[144:147],v[96:99],0
ds_read_b64_tr_b16 a[190:191],v2 offset:35936
v_mul_f32 v185,v21,v185
v_mfma_f32_16x16x32_bf16 a[216:219],a[148:151],v[100:103],a[216:219]
v_accvgpr_read_b32 v172,a40
v_accvgpr_read_b32 v173,a41
v_accvgpr_read_b32 v174,a42
v_accvgpr_read_b32 v175,a43
v_accvgpr_read_b32 v12,a44
v_accvgpr_read_b32 v13,a45
v_accvgpr_read_b32 v14,a46
v_accvgpr_read_b32 v15,a47
v_pk_mul_f32 v[172:173],v[20:21],v[172:173] op_sel:[1,0] op_sel_hi:[1,1]
v_pk_mul_f32 v[174:175],v[20:21],v[174:175] op_sel:[1,0] op_sel_hi:[1,1]
v_pk_mul_f32 v[12:13],v[20:21],v[12:13] op_sel:[1,0] op_sel_hi:[1,1]
v_pk_mul_f32 v[14:15],v[20:21],v[14:15] op_sel:[1,0] op_sel_hi:[1,1]
v_cvt_pk_bf16_f32 v172,v172,v173
v_cvt_pk_bf16_f32 v173,v174,v175
v_cvt_pk_bf16_f32 v174,v12,v13
v_cvt_pk_bf16_f32 v175,v14,v15
v_mfma_f32_16x16x32_bf16 a[216:219],a[152:155],v[104:107],a[216:219]
v_accvgpr_read_b32 v186,a50
v_accvgpr_read_b32 v187,a51
v_mfma_f32_16x16x32_bf16 a[216:219],a[156:159],v[108:111],a[216:219]
v_cvt_pk_bf16_f32 v184,v184,v185
v_mul_f32 v186,v21,v186
v_mfma_f32_16x16x32_bf16 a[220:223],a[156:159],v[124:127],0
v_mul_f32 v187,v21,v187
v_cvt_pk_bf16_f32 v185,v186,v187
v_mfma_f32_16x16x32_bf16 a[220:223],a[152:155],v[120:123],a[220:223]
v_accvgpr_read_b32 v186,a52
v_accvgpr_read_b32 v187,a53
v_mfma_f32_16x16x32_bf16 a[220:223],a[148:151],v[116:119],a[220:223]
s_add_u32 m0,s82,122880
v_accvgpr_read_b32 v12,a54
v_accvgpr_read_b32 v13,a55
v_mfma_f32_16x16x32_bf16 a[220:223],a[144:147],v[112:115],a[220:223]
s_waitcnt lgkmcnt(1)
v_mul_f32 v186,v21,v186
v_mul_f32 v187,v21,v187
v_mfma_f32_16x16x32_bf16 a[224:227],a[160:163],v[64:67],0
v_mul_f32 v12,v21,v12
v_mul_f32 v13,v21,v13
v_mfma_f32_16x16x32_bf16 a[224:227],a[164:167],v[68:71],a[224:227]
buffer_load_dwordx4 v7,s[72:75],s80 offen lds
v_mfma_f32_16x16x32_bf16 a[224:227],a[168:171],v[72:75],a[224:227]
v_cvt_pk_bf16_f32 v186,v186,v187
v_cvt_pk_bf16_f32 v187,v12,v13
v_mfma_f32_16x16x32_bf16 a[224:227],a[172:175],v[76:79],a[224:227]
ds_read_b64_tr_b16 a[128:129],v1 offset:128
ds_read_b64_tr_b16 a[130:131],v1 offset:5248
v_mfma_f32_16x16x32_bf16 a[228:231],a[172:175],v[92:95],0
ds_read_b64_tr_b16 a[132:133],v1 offset:10368
v_accvgpr_read_b32 v188,a56
v_mfma_f32_16x16x32_bf16 a[228:231],a[168:171],v[88:91],a[228:231]
ds_read_b64_tr_b16 a[134:135],v1 offset:15488
v_accvgpr_read_b32 v189,a57
v_mfma_f32_16x16x32_bf16 a[228:231],a[164:167],v[84:87],a[228:231]
ds_read_b64_tr_b16 a[136:137],v1 offset:20608
v_mul_f32 v188,v21,v188
v_mfma_f32_16x16x32_bf16 a[228:231],a[160:163],v[80:83],a[228:231]
ds_read_b64_tr_b16 a[138:139],v1 offset:25728
ds_read_b64_tr_b16 a[140:141],v1 offset:30848
v_mfma_f32_16x16x32_bf16 a[232:235],a[160:163],v[96:99],0
ds_read_b64_tr_b16 a[142:143],v1 offset:35968
v_mul_f32 v189,v21,v189
v_mfma_f32_16x16x32_bf16 a[232:235],a[164:167],v[100:103],a[232:235]
v_accvgpr_read_b32 v190,a58
v_accvgpr_read_b32 v191,a59
v_mfma_f32_16x16x32_bf16 a[232:235],a[168:171],v[104:107],a[232:235]
buffer_store_dwordx4 v[156:159],v6,s[36:39],s84 offen offset:3072 sc1
v_mfma_f32_16x16x32_bf16 a[232:235],a[172:175],v[108:111],a[232:235]
v_cvt_pk_bf16_f32 v188,v188,v189
v_mul_f32 v190,v21,v190
v_mfma_f32_16x16x32_bf16 a[236:239],a[172:175],v[124:127],0
v_mul_f32 v191,v21,v191
v_cvt_pk_bf16_f32 v189,v190,v191
v_mfma_f32_16x16x32_bf16 a[236:239],a[168:171],v[120:123],a[236:239]
v_accvgpr_read_b32 v190,a60
v_accvgpr_read_b32 v191,a61
v_mfma_f32_16x16x32_bf16 a[236:239],a[164:167],v[116:119],a[236:239]
buffer_load_dwordx4 v8,s[72:75],s80 offen offset:1024 lds
v_mfma_f32_16x16x32_bf16 a[236:239],a[160:163],v[112:115],a[236:239]
v_accvgpr_read_b32 v14,a62
v_accvgpr_read_b32 v15,a63
v_mfma_f32_16x16x32_bf16 a[240:243],a[176:179],v[64:67],0
v_mul_f32 v190,v21,v190
v_mul_f32 v191,v21,v191
v_mfma_f32_16x16x32_bf16 a[240:243],a[180:183],v[68:71],a[240:243]
v_mul_f32 v14,v21,v14
v_mul_f32 v15,v21,v15
v_mfma_f32_16x16x32_bf16 a[240:243],a[184:187],v[72:75],a[240:243]
s_waitcnt lgkmcnt(0)
v_cvt_pk_bf16_f32 v190,v190,v191
v_cvt_pk_bf16_f32 v191,v14,v15
v_mfma_f32_16x16x32_bf16 a[240:243],a[188:191],v[76:79],a[240:243]
ds_read_b64_tr_b16 a[144:145],v2 offset:160
ds_read_b64_tr_b16 a[146:147],v2 offset:5280
v_mfma_f32_16x16x32_bf16 a[244:247],a[188:191],v[92:95],0
ds_read_b64_tr_b16 a[148:149],v2 offset:10400
v_add_f32 v56,v57,v56
v_mfma_f32_16x16x32_bf16 a[244:247],a[184:187],v[88:91],a[244:247]
ds_read_b64_tr_b16 a[150:151],v2 offset:15520
v_max_f32 v22,0x00800000,v56
v_mfma_f32_16x16x32_bf16 a[244:247],a[180:183],v[84:87],a[244:247]
ds_read_b64_tr_b16 a[152:153],v2 offset:20640
v_accvgpr_read_b32 v200,a64
v_mfma_f32_16x16x32_bf16 a[244:247],a[176:179],v[80:83],a[244:247]
ds_read_b64_tr_b16 a[154:155],v2 offset:25760
ds_read_b64_tr_b16 a[156:157],v2 offset:30880
v_mfma_f32_16x16x32_bf16 a[248:251],a[176:179],v[96:99],0
v_rcp_f32 v22,v22
v_mfma_f32_16x16x32_bf16 a[248:251],a[180:183],v[100:103],a[248:251]
buffer_load_dwordx4 v9,s[72:75],s80 offen offset:2048 lds
v_mfma_f32_16x16x32_bf16 a[248:251],a[184:187],v[104:107],a[248:251]
ds_read_b64_tr_b16 a[158:159],v2 offset:36000
v_accvgpr_read_b32 v201,a65
v_mfma_f32_16x16x32_bf16 a[248:251],a[188:191],v[108:111],a[248:251]
v_mul_f32 v200,v22,v200
v_mul_f32 v201,v22,v201
v_mfma_f32_16x16x32_bf16 a[252:255],a[188:191],v[124:127],0
v_accvgpr_read_b32 v202,a66
v_accvgpr_read_b32 v203,a67
v_mfma_f32_16x16x32_bf16 a[252:255],a[184:187],v[120:123],a[252:255]
v_cvt_pk_bf16_f32 v200,v200,v201
v_mul_f32 v202,v22,v202
v_mfma_f32_16x16x32_bf16 a[252:255],a[180:183],v[116:119],a[252:255]
s_waitcnt vmcnt(18)
v_mul_f32 v203,v22,v203
v_cvt_pk_bf16_f32 v201,v202,v203
v_mfma_f32_16x16x32_bf16 a[252:255],a[176:179],v[112:115],a[252:255]
s_barrier
buffer_store_dwordx4 v[168:171],v6,s[36:39],s85 offen sc1
v_mfma_f32_16x16x32_bf16 a[0:3],a[128:131],v[64:67],0
v_accvgpr_read_b32 v202,a68
v_accvgpr_read_b32 v203,a69
v_mfma_f32_16x16x32_bf16 a[0:3],a[132:135],v[68:71],a[0:3]
v_accvgpr_read_b32 v16,a70
v_accvgpr_read_b32 v17,a71
v_mfma_f32_16x16x32_bf16 a[0:3],a[136:139],v[72:75],a[0:3]
v_mul_f32 v202,v22,v202
v_mul_f32 v203,v22,v203
v_mfma_f32_16x16x32_bf16 a[0:3],a[140:143],v[76:79],a[0:3]
v_mul_f32 v16,v22,v16
v_mul_f32 v17,v22,v17
v_mfma_f32_16x16x32_bf16 a[4:7],a[140:143],v[92:95],0
buffer_load_dwordx4 v10,s[72:75],s80 offen offset:3072 lds
v_mfma_f32_16x16x32_bf16 a[4:7],a[136:139],v[88:91],a[4:7]
s_lshl_b32 s94,s70,4
v_cvt_pk_bf16_f32 v202,v202,v203
v_cvt_pk_bf16_f32 v203,v16,v17
v_mfma_f32_16x16x32_bf16 a[4:7],a[132:135],v[84:87],a[4:7]
s_add_u32 s94,s94,s40
v_accvgpr_read_b32 v204,a72
v_accvgpr_read_b32 v205,a73
v_mfma_f32_16x16x32_bf16 a[4:7],a[128:131],v[80:83],a[4:7]
s_lshl_b32 s94,s94,4
v_mul_f32 v204,v22,v204
v_mul_f32 v205,v22,v205
v_mfma_f32_16x16x32_bf16 a[8:11],a[128:131],v[96:99],0
v_accvgpr_read_b32 v206,a74
v_accvgpr_read_b32 v207,a75
v_mfma_f32_16x16x32_bf16 a[8:11],a[132:135],v[100:103],a[8:11]
ds_read_b64_tr_b16 a[160:161],v1 offset:192
ds_read_b64_tr_b16 a[162:163],v1 offset:5312
v_mfma_f32_16x16x32_bf16 a[8:11],a[136:139],v[104:107],a[8:11]
v_cvt_pk_bf16_f32 v204,v204,v205
v_mul_f32 v206,v22,v206
v_mfma_f32_16x16x32_bf16 a[8:11],a[140:143],v[108:111],a[8:11]
v_mul_f32 v207,v22,v207
v_cvt_pk_bf16_f32 v205,v206,v207
v_mfma_f32_16x16x32_bf16 a[12:15],a[140:143],v[124:127],0
ds_read_b64_tr_b16 a[164:165],v1 offset:10432
ds_read_b64_tr_b16 a[166:167],v1 offset:15552
v_mfma_f32_16x16x32_bf16 a[12:15],a[136:139],v[120:123],a[12:15]
s_add_u32 m0,s82,126976
v_accvgpr_read_b32 v206,a76
v_accvgpr_read_b32 v207,a77
v_mfma_f32_16x16x32_bf16 a[12:15],a[132:135],v[116:119],a[12:15]
v_accvgpr_read_b32 v12,a78
v_accvgpr_read_b32 v13,a79
v_mfma_f32_16x16x32_bf16 a[12:15],a[128:131],v[112:115],a[12:15]
s_waitcnt lgkmcnt(4)
v_mul_f32 v206,v22,v206
v_mul_f32 v207,v22,v207
v_mfma_f32_16x16x32_bf16 a[16:19],a[144:147],v[64:67],0
buffer_load_dwordx4 v11,s[72:75],s80 offen lds
v_mfma_f32_16x16x32_bf16 a[16:19],a[148:151],v[68:71],a[16:19]
ds_read_b64_tr_b16 a[168:169],v1 offset:20672
ds_read_b64_tr_b16 a[170:171],v1 offset:25792
v_mfma_f32_16x16x32_bf16 a[16:19],a[152:155],v[72:75],a[16:19]
v_mul_f32 v12,v22,v12
v_mul_f32 v13,v22,v13
v_mfma_f32_16x16x32_bf16 a[16:19],a[156:159],v[76:79],a[16:19]
v_cvt_pk_bf16_f32 v206,v206,v207
v_cvt_pk_bf16_f32 v207,v12,v13
v_mfma_f32_16x16x32_bf16 a[20:23],a[156:159],v[92:95],0
buffer_store_dwordx4 v[172:175],v6,s[36:39],s85 offen offset:1024 sc1
v_mfma_f32_16x16x32_bf16 a[20:23],a[152:155],v[88:91],a[20:23]
ds_read_b64_tr_b16 a[172:173],v1 offset:30912
ds_read_b64_tr_b16 a[174:175],v1 offset:36032
v_mfma_f32_16x16x32_bf16 a[20:23],a[148:151],v[84:87],a[20:23]
v_accvgpr_read_b32 v216,a80
v_accvgpr_read_b32 v217,a81
v_mfma_f32_16x16x32_bf16 a[20:23],a[144:147],v[80:83],a[20:23]
v_mul_f32 v216,v22,v216
v_mul_f32 v217,v22,v217
v_mfma_f32_16x16x32_bf16 a[24:27],a[144:147],v[96:99],0
)A"
R"A(v_accvgpr_read_b32 v218,a82
v_accvgpr_read_b32 v219,a83
v_mfma_f32_16x16x32_bf16 a[24:27],a[148:151],v[100:103],a[24:27]
s_add_u32 m0,s82,128000
v_cvt_pk_bf16_f32 v216,v216,v217
v_mul_f32 v218,v22,v218
v_mfma_f32_16x16x32_bf16 a[24:27],a[152:155],v[104:107],a[24:27]
v_mul_f32 v219,v22,v219
v_cvt_pk_bf16_f32 v217,v218,v219
v_mfma_f32_16x16x32_bf16 a[24:27],a[156:159],v[108:111],a[24:27]
v_accvgpr_read_b32 v218,a84
v_accvgpr_read_b32 v219,a85
v_mfma_f32_16x16x32_bf16 a[28:31],a[156:159],v[124:127],0
buffer_load_dwordx4 v7,s[72:75],s83 offen lds
v_mfma_f32_16x16x32_bf16 a[28:31],a[152:155],v[120:123],a[28:31]
v_accvgpr_read_b32 v14,a86
v_accvgpr_read_b32 v15,a87
v_mfma_f32_16x16x32_bf16 a[28:31],a[148:151],v[116:119],a[28:31]
v_mul_f32 v218,v22,v218
v_mul_f32 v219,v22,v219
v_mfma_f32_16x16x32_bf16 a[28:31],a[144:147],v[112:115],a[28:31]
s_waitcnt lgkmcnt(0)
v_mul_f32 v14,v22,v14
v_mul_f32 v15,v22,v15
v_mfma_f32_16x16x32_bf16 a[32:35],a[160:163],v[64:67],0
v_cvt_pk_bf16_f32 v218,v218,v219
v_cvt_pk_bf16_f32 v219,v14,v15
v_mfma_f32_16x16x32_bf16 a[32:35],a[164:167],v[68:71],a[32:35]
ds_read_b64_tr_b16 a[176:177],v2 offset:224
ds_read_b64_tr_b16 a[178:179],v2 offset:5344
v_mfma_f32_16x16x32_bf16 a[32:35],a[168:171],v[72:75],a[32:35]
ds_read_b64_tr_b16 a[180:181],v2 offset:10464
v_bfe_u32 v24,v6,4,2
v_mfma_f32_16x16x32_bf16 a[32:35],a[172:175],v[76:79],a[32:35]
ds_read_b64_tr_b16 a[182:183],v2 offset:15584
v_min_u32 v24,2,v24
v_mfma_f32_16x16x32_bf16 a[36:39],a[172:175],v[92:95],0
ds_read_b64_tr_b16 a[184:185],v2 offset:20704
v_add_u32 v24,s71,v24
v_mfma_f32_16x16x32_bf16 a[36:39],a[168:171],v[88:91],a[36:39]
ds_read_b64_tr_b16 a[186:187],v2 offset:25824
ds_read_b64_tr_b16 a[188:189],v2 offset:30944
v_mfma_f32_16x16x32_bf16 a[36:39],a[164:167],v[84:87],a[36:39]
buffer_store_dwordx4 v[184:187],v6,s[36:39],s85 offen offset:2048 sc1
v_mfma_f32_16x16x32_bf16 a[36:39],a[160:163],v[80:83],a[36:39]
ds_read_b64_tr_b16 a[190:191],v2 offset:36064
v_add_u32 v24,1,v24
v_mfma_f32_16x16x32_bf16 a[40:43],a[160:163],v[96:99],0
v_and_b32 v24,3,v24
v_lshlrev_b32 v24,6,v24
v_mfma_f32_16x16x32_bf16 a[40:43],a[164:167],v[100:103],a[40:43]
v_accvgpr_read_b32 v220,a88
v_accvgpr_read_b32 v221,a89
v_mfma_f32_16x16x32_bf16 a[40:43],a[168:171],v[104:107],a[40:43]
buffer_load_dwordx4 v8,s[72:75],s83 offen offset:1024 lds
v_mfma_f32_16x16x32_bf16 a[40:43],a[172:175],v[108:111],a[40:43]
v_mul_f32 v220,v22,v220
v_mul_f32 v221,v22,v221
v_mfma_f32_16x16x32_bf16 a[44:47],a[172:175],v[124:127],0
v_accvgpr_read_b32 v222,a90
v_accvgpr_read_b32 v223,a91
v_mfma_f32_16x16x32_bf16 a[44:47],a[168:171],v[120:123],a[44:47]
v_cvt_pk_bf16_f32 v220,v220,v221
v_mul_f32 v222,v22,v222
v_mfma_f32_16x16x32_bf16 a[44:47],a[164:167],v[116:119],a[44:47]
buffer_load_dwordx2 v[26:27],v24,s[64:67],s94 offen sc1
v_mfma_f32_16x16x32_bf16 a[44:47],a[160:163],v[112:115],a[44:47]
s_waitcnt lgkmcnt(0)
v_mul_f32 v223,v22,v223
v_cvt_pk_bf16_f32 v221,v222,v223
v_mfma_f32_16x16x32_bf16 a[48:51],a[176:179],v[64:67],0
v_accvgpr_read_b32 v222,a92
v_accvgpr_read_b32 v223,a93
v_mfma_f32_16x16x32_bf16 a[48:51],a[180:183],v[68:71],a[48:51]
v_accvgpr_read_b32 v16,a94
v_accvgpr_read_b32 v17,a95
v_mfma_f32_16x16x32_bf16 a[48:51],a[184:187],v[72:75],a[48:51]
v_mul_f32 v222,v22,v222
v_mul_f32 v223,v22,v223
v_mfma_f32_16x16x32_bf16 a[48:51],a[188:191],v[76:79],a[48:51]
v_mul_f32 v16,v22,v16
v_mul_f32 v17,v22,v17
v_mfma_f32_16x16x32_bf16 a[52:55],a[188:191],v[92:95],0
v_cvt_pk_bf16_f32 v222,v222,v223
v_cvt_pk_bf16_f32 v223,v16,v17
v_mfma_f32_16x16x32_bf16 a[52:55],a[184:187],v[88:91],a[52:55]
buffer_load_dwordx4 v9,s[72:75],s83 offen offset:2048 lds
v_mfma_f32_16x16x32_bf16 a[52:55],a[180:183],v[84:87],a[52:55]
ds_read_b64_tr_b16 a[128:129],v1 offset:256
ds_read_b64_tr_b16 a[130:131],v1 offset:5376
v_mfma_f32_16x16x32_bf16 a[52:55],a[176:179],v[80:83],a[52:55]
ds_read_b64_tr_b16 a[132:133],v1 offset:10496
v_add_f32 v60,v61,v60
v_mfma_f32_16x16x32_bf16 a[56:59],a[176:179],v[96:99],0
ds_read_b64_tr_b16 a[134:135],v1 offset:15616
v_max_f32 v23,0x00800000,v60
v_mfma_f32_16x16x32_bf16 a[56:59],a[180:183],v[100:103],a[56:59]
ds_read_b64_tr_b16 a[136:137],v1 offset:20736
ds_read_b64_tr_b16 a[138:139],v1 offset:25856
v_mfma_f32_16x16x32_bf16 a[56:59],a[184:187],v[104:107],a[56:59]
ds_read_b64_tr_b16 a[140:141],v1 offset:30976
ds_read_b64_tr_b16 a[142:143],v1 offset:36096
v_mfma_f32_16x16x32_bf16 a[56:59],a[188:191],v[108:111],a[56:59]
buffer_store_dwordx4 v[188:191],v6,s[36:39],s85 offen offset:3072 sc1
v_mfma_f32_16x16x32_bf16 a[60:63],a[188:191],v[124:127],0
v_rcp_f32 v23,v23
v_mfma_f32_16x16x32_bf16 a[60:63],a[184:187],v[120:123],a[60:63]
v_accvgpr_read_b32 v232,a96
v_accvgpr_read_b32 v233,a97
v_mfma_f32_16x16x32_bf16 a[60:63],a[180:183],v[116:119],a[60:63]
v_mul_f32 v232,v23,v232
v_mul_f32 v233,v23,v233
v_mfma_f32_16x16x32_bf16 a[60:63],a[176:179],v[112:115],a[60:63]
s_waitcnt lgkmcnt(5)
v_accvgpr_read_b32 v234,a98
v_accvgpr_read_b32 v235,a99
v_mfma_f32_16x16x32_bf16 a[64:67],a[128:131],v[64:67],0
s_waitcnt lgkmcnt(4)
v_cvt_pk_bf16_f32 v232,v232,v233
v_mul_f32 v234,v23,v234
v_mfma_f32_16x16x32_bf16 a[64:67],a[132:135],v[68:71],a[64:67]
s_waitcnt lgkmcnt(2)
v_mul_f32 v235,v23,v235
v_cvt_pk_bf16_f32 v233,v234,v235
v_mfma_f32_16x16x32_bf16 a[64:67],a[136:139],v[72:75],a[64:67]
s_waitcnt lgkmcnt(0)
v_accvgpr_read_b32 v234,a100
v_accvgpr_read_b32 v235,a101
v_mfma_f32_16x16x32_bf16 a[64:67],a[140:143],v[76:79],a[64:67]
buffer_load_dwordx4 v10,s[72:75],s83 offen offset:3072 lds
v_mfma_f32_16x16x32_bf16 a[68:71],a[140:143],v[92:95],0
v_accvgpr_read_b32 v12,a102
v_accvgpr_read_b32 v13,a103
v_mfma_f32_16x16x32_bf16 a[68:71],a[136:139],v[88:91],a[68:71]
v_mul_f32 v234,v23,v234
v_mul_f32 v235,v23,v235
v_mfma_f32_16x16x32_bf16 a[68:71],a[132:135],v[84:87],a[68:71]
v_mul_f32 v12,v23,v12
v_mul_f32 v13,v23,v13
v_mfma_f32_16x16x32_bf16 a[68:71],a[128:131],v[80:83],a[68:71]
ds_read_b64_tr_b16 a[144:145],v2 offset:288
ds_read_b64_tr_b16 a[146:147],v2 offset:5408
v_mfma_f32_16x16x32_bf16 a[72:75],a[128:131],v[96:99],0
ds_read_b64_tr_b16 a[148:149],v2 offset:10528
ds_read_b64_tr_b16 a[150:151],v2 offset:15648
v_mfma_f32_16x16x32_bf16 a[72:75],a[132:135],v[100:103],a[72:75]
ds_read_b64_tr_b16 a[152:153],v2 offset:20768
ds_read_b64_tr_b16 a[154:155],v2 offset:25888
v_mfma_f32_16x16x32_bf16 a[72:75],a[136:139],v[104:107],a[72:75]
ds_read_b64_tr_b16 a[156:157],v2 offset:31008
ds_read_b64_tr_b16 a[158:159],v2 offset:36128
v_mfma_f32_16x16x32_bf16 a[72:75],a[140:143],v[108:111],a[72:75]
s_add_u32 m0,s82,132096
v_mfma_f32_16x16x32_bf16 a[76:79],a[140:143],v[124:127],0
v_cvt_pk_bf16_f32 v234,v234,v235
v_cvt_pk_bf16_f32 v235,v12,v13
v_mfma_f32_16x16x32_bf16 a[76:79],a[136:139],v[120:123],a[76:79]
v_accvgpr_read_b32 v236,a104
v_accvgpr_read_b32 v237,a105
v_mfma_f32_16x16x32_bf16 a[76:79],a[132:135],v[116:119],a[76:79]
v_mul_f32 v236,v23,v236
v_mul_f32 v237,v23,v237
v_mfma_f32_16x16x32_bf16 a[76:79],a[128:131],v[112:115],a[76:79]
s_waitcnt lgkmcnt(6)
buffer_load_dwordx4 v11,s[72:75],s83 offen lds
v_mfma_f32_16x16x32_bf16 a[80:83],a[144:147],v[64:67],0
s_waitcnt lgkmcnt(4)
v_accvgpr_read_b32 v238,a106
v_accvgpr_read_b32 v239,a107
v_mfma_f32_16x16x32_bf16 a[80:83],a[148:151],v[68:71],a[80:83]
s_waitcnt lgkmcnt(2)
v_cvt_pk_bf16_f32 v236,v236,v237
v_mul_f32 v238,v23,v238
v_mfma_f32_16x16x32_bf16 a[80:83],a[152:155],v[72:75],a[80:83]
s_waitcnt lgkmcnt(0)
v_mul_f32 v239,v23,v239
v_cvt_pk_bf16_f32 v237,v238,v239
v_mfma_f32_16x16x32_bf16 a[80:83],a[156:159],v[76:79],a[80:83]
v_accvgpr_read_b32 v238,a108
v_accvgpr_read_b32 v239,a109
v_mfma_f32_16x16x32_bf16 a[84:87],a[156:159],v[92:95],0
v_accvgpr_read_b32 v14,a110
v_accvgpr_read_b32 v15,a111
v_mfma_f32_16x16x32_bf16 a[84:87],a[152:155],v[88:91],a[84:87]
v_mul_f32 v238,v23,v238
v_mul_f32 v239,v23,v239
v_mfma_f32_16x16x32_bf16 a[84:87],a[148:151],v[84:87],a[84:87]
v_subrev_u32 v1,81920,v1
v_mul_f32 v14,v23,v14
v_mfma_f32_16x16x32_bf16 a[84:87],a[144:147],v[80:83],a[84:87]
ds_read_b64_tr_b16 a[160:161],v1
ds_read_b64_tr_b16 a[162:163],v1 offset:5120
v_mfma_f32_16x16x32_bf16 a[88:91],a[144:147],v[96:99],0
ds_read_b64_tr_b16 a[164:165],v1 offset:10240
ds_read_b64_tr_b16 a[166:167],v1 offset:15360
v_mfma_f32_16x16x32_bf16 a[88:91],a[148:151],v[100:103],a[88:91]
ds_read_b64_tr_b16 a[168:169],v1 offset:20480
ds_read_b64_tr_b16 a[170:171],v1 offset:25600
v_mfma_f32_16x16x32_bf16 a[88:91],a[152:155],v[104:107],a[88:91]
ds_read_b64_tr_b16 a[172:173],v1 offset:30720
ds_read_b64_tr_b16 a[174:175],v1 offset:35840
v_mfma_f32_16x16x32_bf16 a[88:91],a[156:159],v[108:111],a[88:91]
v_accvgpr_read_b32 v248,a112
v_accvgpr_read_b32 v249,a113
v_mul_f32 v248,v23,v248
v_mul_f32 v249,v23,v249
v_accvgpr_read_b32 v250,a114
v_accvgpr_read_b32 v251,a115
v_accvgpr_read_b32 v252,a120
v_accvgpr_read_b32 v253,a121
v_cmp_nlt_f32 s[56:57],v48,s63
v_mul_f32 v252,v23,v252
v_mul_f32 v253,v23,v253
v_accvgpr_read_b32 v254,a122
v_accvgpr_read_b32 v255,a123
v_mfma_f32_16x16x32_bf16 a[92:95],a[156:159],v[124:127],0
v_cvt_pk_bf16_f32 v248,v248,v249
v_mul_f32 v250,v23,v250
v_mul_f32 v251,v23,v251
v_cvt_pk_bf16_f32 v249,v250,v251
v_cvt_pk_bf16_f32 v252,v252,v253
v_mul_f32 v254,v23,v254
v_mul_f32 v255,v23,v255
v_cmp_nlt_f32 s[58:59],v52,s63
s_nop 1
s_or_b64 s[56:57],s[56:57],s[58:59]
v_cvt_pk_bf16_f32 v253,v254,v255
v_mfma_f32_16x16x32_bf16 a[92:95],a[152:155],v[120:123],a[92:95]
v_accvgpr_read_b32 v250,a116
v_accvgpr_read_b32 v251,a117
v_accvgpr_read_b32 v16,a118
v_accvgpr_read_b32 v17,a119
v_mul_f32 v250,v23,v250
v_mul_f32 v251,v23,v251
v_accvgpr_read_b32 v254,a124
v_accvgpr_read_b32 v255,a125
v_cmp_nlt_f32 s[58:59],v56,s63
s_nop 1
s_or_b64 s[56:57],s[56:57],s[58:59]
v_accvgpr_read_b32 v12,a126
v_accvgpr_read_b32 v13,a127
v_mul_f32 v254,v23,v254
v_mul_f32 v255,v23,v255
s_waitcnt vmcnt(4)
v_cmp_ne_u32 s[90:91],s68,v26
v_cmp_ne_u32 s[92:93],s69,v27
s_nop 0
s_or_b64 s[90:91],s[90:91],s[92:93]
s_cbranch_scc1 L_fr8_%=
L_fo8_%=:
v_mfma_f32_16x16x32_bf16 a[92:95],a[148:151],v[116:119],a[92:95]
v_mul_f32 v15,v23,v15
v_cvt_pk_bf16_f32 v238,v238,v239
v_cvt_pk_bf16_f32 v239,v14,v15
v_mul_f32 v16,v23,v16
v_mul_f32 v17,v23,v17
v_cvt_pk_bf16_f32 v250,v250,v251
v_cvt_pk_bf16_f32 v251,v16,v17
v_mul_f32 v12,v23,v12
v_mul_f32 v13,v23,v13
v_cvt_pk_bf16_f32 v254,v254,v255
v_cmp_nlt_f32 s[58:59],v60,s63
s_nop 1
s_or_b64 s[56:57],s[56:57],s[58:59]
v_cvt_pk_bf16_f32 v255,v12,v13
v_mfma_f32_16x16x32_bf16 a[92:95],a[144:147],v[112:115],a[92:95]
s_cmp_lg_u64 s[56:57],0
s_cbranch_scc1 L_slow1_%=
L_sret1_%=:
v_subrev_u32 v2,81920,v2
s_waitcnt lgkmcnt(6)
v_mfma_f32_16x16x32_bf16 a[192:195],a[160:163],v[136:139],a[192:195]
s_cmp_ge_u32 0,s71
s_cselect_b32 s76,2,0
s_lshl_b32 s76,s76,16
s_waitcnt lgkmcnt(4)
v_mfma_f32_16x16x32_bf16 a[192:195],a[164:167],v[140:143],a[192:195]
s_add_u32 s77,s76,4096
s_add_u32 s78,s76,8192
s_add_u32 s79,s76,12288
s_max_i32 s76,s76,s18
s_waitcnt lgkmcnt(2)
v_mfma_f32_16x16x32_bf16 a[192:195],a[168:171],v[152:155],a[192:195]
s_max_i32 s77,s77,s19
buffer_load_dwordx4 v[128:131],v6,s[28:31],s76 offen sc1
s_waitcnt lgkmcnt(0)
v_mfma_f32_16x16x32_bf16 a[192:195],a[172:175],v[156:159],a[192:195]
s_max_i32 s78,s78,s100
ds_read_b64_tr_b16 a[176:177],v2 offset:32
ds_read_b64_tr_b16 a[178:179],v2 offset:5152
v_mfma_f32_16x16x32_bf16 a[196:199],a[172:175],v[188:191],a[196:199]
s_cmp_ge_u32 2,s71
s_cselect_b32 s80,6,4
ds_read_b64_tr_b16 a[180:181],v2 offset:10272
v_mfma_f32_16x16x32_bf16 a[196:199],a[168:171],v[184:187],a[196:199]
s_mul_i32 s80,s80,163840
s_add_u32 s80,s80,s81
ds_read_b64_tr_b16 a[182:183],v2 offset:15392
v_mfma_f32_16x16x32_bf16 a[196:199],a[164:167],v[172:175],a[196:199]
s_max_i32 s79,s79,s101
buffer_store_dwordx4 v[200:203],v6,s[36:39],s86 offen sc1
v_mfma_f32_16x16x32_bf16 a[196:199],a[160:163],v[168:171],a[196:199]
s_add_u32 s83,s80,20480
ds_read_b64_tr_b16 a[184:185],v2 offset:20512
ds_read_b64_tr_b16 a[186:187],v2 offset:25632
v_mfma_f32_16x16x32_bf16 a[200:203],a[160:163],v[200:203],a[200:203]
ds_read_b64_tr_b16 a[188:189],v2 offset:30752
v_mfma_f32_16x16x32_bf16 a[200:203],a[164:167],v[204:207],a[200:203]
)A"
R"A(ds_read_b64_tr_b16 a[190:191],v2 offset:35872
v_mfma_f32_16x16x32_bf16 a[200:203],a[168:171],v[216:219],a[200:203]
buffer_load_dwordx4 v[160:163],v6,s[28:31],s76 offen offset:1024 sc1
v_mfma_f32_16x16x32_bf16 a[200:203],a[172:175],v[220:223],a[200:203]
ds_read_b64_tr_b16 a[128:129],v1 offset:64
ds_read_b64_tr_b16 a[130:131],v1 offset:5184
v_mfma_f32_16x16x32_bf16 a[204:207],a[172:175],v[252:255],a[204:207]
ds_read_b64_tr_b16 a[132:133],v1 offset:10304
v_mfma_f32_16x16x32_bf16 a[204:207],a[168:171],v[248:251],a[204:207]
ds_read_b64_tr_b16 a[134:135],v1 offset:15424
v_mfma_f32_16x16x32_bf16 a[204:207],a[164:167],v[236:239],a[204:207]
buffer_store_dwordx4 v[204:207],v6,s[36:39],s86 offen offset:1024 sc1
v_mfma_f32_16x16x32_bf16 a[204:207],a[160:163],v[232:235],a[204:207]
s_barrier
ds_read_b64_tr_b16 a[136:137],v1 offset:20544
ds_read_b64_tr_b16 a[138:139],v1 offset:25664
s_waitcnt lgkmcnt(8)
v_mfma_f32_16x16x32_bf16 a[208:211],a[176:179],v[136:139],a[208:211]
ds_read_b64_tr_b16 a[140:141],v1 offset:30784
v_mfma_f32_16x16x32_bf16 a[208:211],a[180:183],v[140:143],a[208:211]
buffer_load_dwordx4 v[192:195],v6,s[28:31],s76 offen offset:2048 sc1
v_mfma_f32_16x16x32_bf16 a[208:211],a[184:187],v[152:155],a[208:211]
s_add_u32 m0,s82,81920
ds_read_b64_tr_b16 a[142:143],v1 offset:35904
ds_read_b64_tr_b16 a[144:145],v2 offset:96
s_waitcnt lgkmcnt(9)
v_mfma_f32_16x16x32_bf16 a[208:211],a[188:191],v[156:159],a[208:211]
ds_read_b64_tr_b16 a[146:147],v2 offset:5216
v_mfma_f32_16x16x32_bf16 a[212:215],a[188:191],v[188:191],a[212:215]
ds_read_b64_tr_b16 a[148:149],v2 offset:10336
v_mfma_f32_16x16x32_bf16 a[212:215],a[184:187],v[184:187],a[212:215]
buffer_store_dwordx4 v[216:219],v6,s[36:39],s86 offen offset:2048 sc1
v_mfma_f32_16x16x32_bf16 a[212:215],a[180:183],v[172:175],a[212:215]
ds_read_b64_tr_b16 a[150:151],v2 offset:15456
ds_read_b64_tr_b16 a[152:153],v2 offset:20576
v_mfma_f32_16x16x32_bf16 a[212:215],a[176:179],v[168:171],a[212:215]
ds_read_b64_tr_b16 a[154:155],v2 offset:25696
v_mfma_f32_16x16x32_bf16 a[216:219],a[176:179],v[200:203],a[216:219]
ds_read_b64_tr_b16 a[156:157],v2 offset:30816
v_mfma_f32_16x16x32_bf16 a[216:219],a[180:183],v[204:207],a[216:219]
buffer_load_dwordx4 v[224:227],v6,s[28:31],s76 offen offset:3072 sc1
v_mfma_f32_16x16x32_bf16 a[216:219],a[184:187],v[216:219],a[216:219]
ds_read_b64_tr_b16 a[158:159],v2 offset:35936
ds_read_b64_tr_b16 a[160:161],v1 offset:128
v_mfma_f32_16x16x32_bf16 a[216:219],a[188:191],v[220:223],a[216:219]
ds_read_b64_tr_b16 a[162:163],v1 offset:5248
v_mfma_f32_16x16x32_bf16 a[220:223],a[188:191],v[252:255],a[220:223]
ds_read_b64_tr_b16 a[164:165],v1 offset:10368
v_mfma_f32_16x16x32_bf16 a[220:223],a[184:187],v[248:251],a[220:223]
buffer_load_dwordx4 v7,s[72:75],s80 offen lds
v_mfma_f32_16x16x32_bf16 a[220:223],a[180:183],v[236:239],a[220:223]
ds_read_b64_tr_b16 a[166:167],v1 offset:15488
ds_read_b64_tr_b16 a[168:169],v1 offset:20608
v_mfma_f32_16x16x32_bf16 a[220:223],a[176:179],v[232:235],a[220:223]
s_waitcnt lgkmcnt(7)
ds_read_b64_tr_b16 a[170:171],v1 offset:25728
v_mfma_f32_16x16x32_bf16 a[224:227],a[128:131],v[136:139],a[224:227]
ds_read_b64_tr_b16 a[172:173],v1 offset:30848
v_mfma_f32_16x16x32_bf16 a[224:227],a[132:135],v[140:143],a[224:227]
buffer_load_dwordx4 v[132:135],v6,s[28:31],s77 offen sc1
v_mfma_f32_16x16x32_bf16 a[224:227],a[136:139],v[152:155],a[224:227]
ds_read_b64_tr_b16 a[174:175],v1 offset:35968
ds_read_b64_tr_b16 a[176:177],v2 offset:160
v_mfma_f32_16x16x32_bf16 a[224:227],a[140:143],v[156:159],a[224:227]
ds_read_b64_tr_b16 a[178:179],v2 offset:5280
v_mfma_f32_16x16x32_bf16 a[228:231],a[140:143],v[188:191],a[228:231]
ds_read_b64_tr_b16 a[180:181],v2 offset:10400
v_mfma_f32_16x16x32_bf16 a[228:231],a[136:139],v[184:187],a[228:231]
buffer_load_dwordx4 v[164:167],v6,s[28:31],s77 offen offset:1024 sc1
v_mfma_f32_16x16x32_bf16 a[228:231],a[132:135],v[172:175],a[228:231]
ds_read_b64_tr_b16 a[182:183],v2 offset:15520
ds_read_b64_tr_b16 a[184:185],v2 offset:20640
v_mfma_f32_16x16x32_bf16 a[228:231],a[128:131],v[168:171],a[228:231]
ds_read_b64_tr_b16 a[186:187],v2 offset:25760
v_mfma_f32_16x16x32_bf16 a[232:235],a[128:131],v[200:203],a[232:235]
ds_read_b64_tr_b16 a[188:189],v2 offset:30880
v_mfma_f32_16x16x32_bf16 a[232:235],a[132:135],v[204:207],a[232:235]
buffer_load_dwordx4 v8,s[72:75],s80 offen offset:1024 lds
v_mfma_f32_16x16x32_bf16 a[232:235],a[136:139],v[216:219],a[232:235]
ds_read_b64_tr_b16 a[190:191],v2 offset:36000
v_mfma_f32_16x16x32_bf16 a[232:235],a[140:143],v[220:223],a[232:235]
v_mfma_f32_16x16x32_bf16 a[236:239],a[140:143],v[252:255],a[236:239]
ds_read_b64_tr_b16 a[140:141],v1 offset:30912
ds_read_b64_tr_b16 a[142:143],v1 offset:36032
v_mfma_f32_16x16x32_bf16 a[236:239],a[136:139],v[248:251],a[236:239]
buffer_load_dwordx4 v[196:199],v6,s[28:31],s77 offen offset:2048 sc1
v_mfma_f32_16x16x32_bf16 a[236:239],a[132:135],v[236:239],a[236:239]
ds_read_b64_tr_b16 a[132:133],v1 offset:10432
ds_read_b64_tr_b16 a[134:135],v1 offset:15552
v_mfma_f32_16x16x32_bf16 a[236:239],a[128:131],v[232:235],a[236:239]
ds_read_b64_tr_b16 a[128:129],v1 offset:192
v_mfma_f32_16x16x32_bf16 a[240:243],a[144:147],v[136:139],a[240:243]
ds_read_b64_tr_b16 a[130:131],v1 offset:5312
v_mfma_f32_16x16x32_bf16 a[240:243],a[148:151],v[140:143],a[240:243]
buffer_store_dwordx4 v[220:223],v6,s[36:39],s86 offen offset:3072 sc1
v_mfma_f32_16x16x32_bf16 a[240:243],a[152:155],v[152:155],a[240:243]
s_waitcnt lgkmcnt(6)
ds_read_b64_tr_b16 a[136:137],v1 offset:20672
ds_read_b64_tr_b16 a[138:139],v1 offset:25792
v_mfma_f32_16x16x32_bf16 a[240:243],a[156:159],v[156:159],a[240:243]
v_mfma_f32_16x16x32_bf16 a[244:247],a[156:159],v[188:191],a[244:247]
v_mfma_f32_16x16x32_bf16 a[244:247],a[152:155],v[184:187],a[244:247]
buffer_load_dwordx4 v[228:231],v6,s[28:31],s77 offen offset:3072 sc1
v_mfma_f32_16x16x32_bf16 a[244:247],a[148:151],v[172:175],a[244:247]
v_mfma_f32_16x16x32_bf16 a[244:247],a[144:147],v[168:171],a[244:247]
v_mfma_f32_16x16x32_bf16 a[248:251],a[144:147],v[200:203],a[248:251]
v_mfma_f32_16x16x32_bf16 a[248:251],a[148:151],v[204:207],a[248:251]
buffer_load_dwordx4 v9,s[72:75],s80 offen offset:2048 lds
v_mfma_f32_16x16x32_bf16 a[248:251],a[152:155],v[216:219],a[248:251]
v_mfma_f32_16x16x32_bf16 a[248:251],a[156:159],v[220:223],a[248:251]
v_mfma_f32_16x16x32_bf16 a[252:255],a[156:159],v[252:255],a[252:255]
ds_read_b64_tr_b16 a[156:157],v2 offset:30944
ds_read_b64_tr_b16 a[158:159],v2 offset:36064
v_mfma_f32_16x16x32_bf16 a[252:255],a[152:155],v[248:251],a[252:255]
buffer_load_dwordx4 v[144:147],v6,s[28:31],s78 offen sc1
v_mfma_f32_16x16x32_bf16 a[252:255],a[148:151],v[236:239],a[252:255]
ds_read_b64_tr_b16 a[148:149],v2 offset:10464
ds_read_b64_tr_b16 a[150:151],v2 offset:15584
v_mfma_f32_16x16x32_bf16 a[252:255],a[144:147],v[232:235],a[252:255]
ds_read_b64_tr_b16 a[144:145],v2 offset:224
v_mfma_f32_16x16x32_bf16 a[0:3],a[160:163],v[136:139],a[0:3]
ds_read_b64_tr_b16 a[146:147],v2 offset:5344
v_mfma_f32_16x16x32_bf16 a[0:3],a[164:167],v[140:143],a[0:3]
buffer_load_dwordx4 v[176:179],v6,s[28:31],s78 offen offset:1024 sc1
v_mfma_f32_16x16x32_bf16 a[0:3],a[168:171],v[152:155],a[0:3]
ds_read_b64_tr_b16 a[152:153],v2 offset:20704
ds_read_b64_tr_b16 a[154:155],v2 offset:25824
v_mfma_f32_16x16x32_bf16 a[0:3],a[172:175],v[156:159],a[0:3]
v_mfma_f32_16x16x32_bf16 a[4:7],a[172:175],v[188:191],a[4:7]
v_mfma_f32_16x16x32_bf16 a[4:7],a[168:171],v[184:187],a[4:7]
buffer_load_dwordx4 v10,s[72:75],s80 offen offset:3072 lds
v_mfma_f32_16x16x32_bf16 a[4:7],a[164:167],v[172:175],a[4:7]
v_mfma_f32_16x16x32_bf16 a[4:7],a[160:163],v[168:171],a[4:7]
v_mfma_f32_16x16x32_bf16 a[8:11],a[160:163],v[200:203],a[8:11]
v_mfma_f32_16x16x32_bf16 a[8:11],a[164:167],v[204:207],a[8:11]
buffer_load_dwordx4 v[208:211],v6,s[28:31],s78 offen offset:2048 sc1
v_mfma_f32_16x16x32_bf16 a[8:11],a[168:171],v[216:219],a[8:11]
v_mfma_f32_16x16x32_bf16 a[8:11],a[172:175],v[220:223],a[8:11]
v_mfma_f32_16x16x32_bf16 a[12:15],a[172:175],v[252:255],a[12:15]
ds_read_b64_tr_b16 a[172:173],v1 offset:30976
ds_read_b64_tr_b16 a[174:175],v1 offset:36096
v_mfma_f32_16x16x32_bf16 a[12:15],a[168:171],v[248:251],a[12:15]
buffer_store_dwordx4 v[232:235],v6,s[36:39],s87 offen sc1
v_mfma_f32_16x16x32_bf16 a[12:15],a[164:167],v[236:239],a[12:15]
s_add_u32 m0,s82,86016
ds_read_b64_tr_b16 a[164:165],v1 offset:10496
ds_read_b64_tr_b16 a[166:167],v1 offset:15616
v_mfma_f32_16x16x32_bf16 a[12:15],a[160:163],v[232:235],a[12:15]
ds_read_b64_tr_b16 a[160:161],v1 offset:256
v_mfma_f32_16x16x32_bf16 a[16:19],a[176:179],v[136:139],a[16:19]
ds_read_b64_tr_b16 a[162:163],v1 offset:5376
v_mfma_f32_16x16x32_bf16 a[16:19],a[180:183],v[140:143],a[16:19]
buffer_load_dwordx4 v[240:243],v6,s[28:31],s78 offen offset:3072 sc1
v_mfma_f32_16x16x32_bf16 a[16:19],a[184:187],v[152:155],a[16:19]
ds_read_b64_tr_b16 a[168:169],v1 offset:20736
ds_read_b64_tr_b16 a[170:171],v1 offset:25856
v_mfma_f32_16x16x32_bf16 a[16:19],a[188:191],v[156:159],a[16:19]
v_add_u32 v1,40960,v1
v_mfma_f32_16x16x32_bf16 a[20:23],a[188:191],v[188:191],a[20:23]
ds_read_b64_tr_b16 a[96:97],v1 offset:128
ds_read_b64_tr_b16 a[98:99],v1 offset:5248
v_mfma_f32_16x16x32_bf16 a[20:23],a[184:187],v[184:187],a[20:23]
buffer_load_dwordx4 v11,s[72:75],s80 offen lds
v_mfma_f32_16x16x32_bf16 a[20:23],a[180:183],v[172:175],a[20:23]
ds_read_b64_tr_b16 a[100:101],v1 offset:10368
ds_read_b64_tr_b16 a[102:103],v1 offset:15488
v_mfma_f32_16x16x32_bf16 a[20:23],a[176:179],v[168:171],a[20:23]
ds_read_b64_tr_b16 a[104:105],v1 offset:20608
v_mfma_f32_16x16x32_bf16 a[24:27],a[176:179],v[200:203],a[24:27]
ds_read_b64_tr_b16 a[106:107],v1 offset:25728
v_mfma_f32_16x16x32_bf16 a[24:27],a[180:183],v[204:207],a[24:27]
buffer_load_dwordx4 v[148:151],v6,s[28:31],s79 offen sc1
v_mfma_f32_16x16x32_bf16 a[24:27],a[184:187],v[216:219],a[24:27]
ds_read_b64_tr_b16 a[108:109],v1 offset:30848
ds_read_b64_tr_b16 a[110:111],v1 offset:35968
v_mfma_f32_16x16x32_bf16 a[24:27],a[188:191],v[220:223],a[24:27]
v_mfma_f32_16x16x32_bf16 a[28:31],a[188:191],v[252:255],a[28:31]
ds_read_b64_tr_b16 a[188:189],v2 offset:31008
ds_read_b64_tr_b16 a[190:191],v2 offset:36128
v_mfma_f32_16x16x32_bf16 a[28:31],a[184:187],v[248:251],a[28:31]
s_add_u32 m0,s82,87040
buffer_load_dwordx4 v[180:183],v6,s[28:31],s79 offen offset:1024 sc1
v_mfma_f32_16x16x32_bf16 a[28:31],a[180:183],v[236:239],a[28:31]
ds_read_b64_tr_b16 a[180:181],v2 offset:10528
ds_read_b64_tr_b16 a[182:183],v2 offset:15648
v_mfma_f32_16x16x32_bf16 a[28:31],a[176:179],v[232:235],a[28:31]
s_waitcnt lgkmcnt(7)
ds_read_b64_tr_b16 a[176:177],v2 offset:288
v_mfma_f32_16x16x32_bf16 a[32:35],a[128:131],v[136:139],a[32:35]
ds_read_b64_tr_b16 a[178:179],v2 offset:5408
v_mfma_f32_16x16x32_bf16 a[32:35],a[132:135],v[140:143],a[32:35]
buffer_store_dwordx4 v[236:239],v6,s[36:39],s87 offen offset:1024 sc1
v_mfma_f32_16x16x32_bf16 a[32:35],a[136:139],v[152:155],a[32:35]
ds_read_b64_tr_b16 a[184:185],v2 offset:20768
ds_read_b64_tr_b16 a[186:187],v2 offset:25888
v_mfma_f32_16x16x32_bf16 a[32:35],a[140:143],v[156:159],a[32:35]
v_add_u32 v2,40960,v2
v_mfma_f32_16x16x32_bf16 a[36:39],a[140:143],v[188:191],a[36:39]
ds_read_b64_tr_b16 a[112:113],v2 offset:160
ds_read_b64_tr_b16 a[114:115],v2 offset:5280
v_mfma_f32_16x16x32_bf16 a[36:39],a[136:139],v[184:187],a[36:39]
buffer_load_dwordx4 v[212:215],v6,s[28:31],s79 offen offset:2048 sc1
v_mfma_f32_16x16x32_bf16 a[36:39],a[132:135],v[172:175],a[36:39]
ds_read_b64_tr_b16 a[116:117],v2 offset:10400
ds_read_b64_tr_b16 a[118:119],v2 offset:15520
v_mfma_f32_16x16x32_bf16 a[36:39],a[128:131],v[168:171],a[36:39]
ds_read_b64_tr_b16 a[120:121],v2 offset:20640
v_mfma_f32_16x16x32_bf16 a[40:43],a[128:131],v[200:203],a[40:43]
ds_read_b64_tr_b16 a[122:123],v2 offset:25760
v_mfma_f32_16x16x32_bf16 a[40:43],a[132:135],v[204:207],a[40:43]
buffer_load_dwordx4 v7,s[72:75],s83 offen lds
v_mfma_f32_16x16x32_bf16 a[40:43],a[136:139],v[216:219],a[40:43]
ds_read_b64_tr_b16 a[124:125],v2 offset:30880
ds_read_b64_tr_b16 a[126:127],v2 offset:36000
v_mfma_f32_16x16x32_bf16 a[40:43],a[140:143],v[220:223],a[40:43]
)A"
R"A(v_mfma_f32_16x16x32_bf16 a[44:47],a[140:143],v[252:255],a[44:47]
ds_read_b64_tr_b16 a[140:141],v1 offset:30720
ds_read_b64_tr_b16 a[142:143],v1 offset:35840
v_mfma_f32_16x16x32_bf16 a[44:47],a[136:139],v[248:251],a[44:47]
buffer_load_dwordx4 v[244:247],v6,s[28:31],s79 offen offset:3072 sc1
v_mfma_f32_16x16x32_bf16 a[44:47],a[132:135],v[236:239],a[44:47]
ds_read_b64_tr_b16 a[132:133],v1 offset:10240
ds_read_b64_tr_b16 a[134:135],v1 offset:15360
v_mfma_f32_16x16x32_bf16 a[44:47],a[128:131],v[232:235],a[44:47]
ds_read_b64_tr_b16 a[128:129],v1
v_mfma_f32_16x16x32_bf16 a[48:51],a[144:147],v[136:139],a[48:51]
ds_read_b64_tr_b16 a[130:131],v1 offset:5120
v_mfma_f32_16x16x32_bf16 a[48:51],a[148:151],v[140:143],a[48:51]
buffer_store_dwordx4 v[248:251],v6,s[36:39],s87 offen offset:2048 sc1
v_mfma_f32_16x16x32_bf16 a[48:51],a[152:155],v[152:155],a[48:51]
ds_read_b64_tr_b16 a[136:137],v1 offset:20480
ds_read_b64_tr_b16 a[138:139],v1 offset:25600
v_mfma_f32_16x16x32_bf16 a[48:51],a[156:159],v[156:159],a[48:51]
s_cmp_ge_u32 1,s71
s_cselect_b32 s76,4,2
s_lshl_b32 s76,s76,16
v_mfma_f32_16x16x32_bf16 a[52:55],a[156:159],v[188:191],a[52:55]
s_add_u32 s77,s76,4096
s_add_u32 s78,s76,8192
s_add_u32 s79,s76,12288
v_mfma_f32_16x16x32_bf16 a[52:55],a[152:155],v[184:187],a[52:55]
s_max_i32 s76,s76,s18
buffer_load_dwordx4 v8,s[72:75],s83 offen offset:1024 lds
v_mfma_f32_16x16x32_bf16 a[52:55],a[148:151],v[172:175],a[52:55]
s_max_i32 s77,s77,s19
s_max_i32 s78,s78,s100
s_max_i32 s79,s79,s101
v_mfma_f32_16x16x32_bf16 a[52:55],a[144:147],v[168:171],a[52:55]
v_mfma_f32_16x16x32_bf16 a[56:59],a[144:147],v[200:203],a[56:59]
v_mfma_f32_16x16x32_bf16 a[56:59],a[148:151],v[204:207],a[56:59]
buffer_store_dwordx4 v[252:255],v6,s[36:39],s87 offen offset:3072 sc1
v_mfma_f32_16x16x32_bf16 a[56:59],a[152:155],v[216:219],a[56:59]
v_mfma_f32_16x16x32_bf16 a[56:59],a[156:159],v[220:223],a[56:59]
v_mfma_f32_16x16x32_bf16 a[60:63],a[156:159],v[252:255],a[60:63]
ds_read_b64_tr_b16 a[156:157],v2 offset:30752
ds_read_b64_tr_b16 a[158:159],v2 offset:35872
v_mfma_f32_16x16x32_bf16 a[60:63],a[152:155],v[248:251],a[60:63]
ds_read_b64_tr_b16 a[152:153],v2 offset:20512
v_mfma_f32_16x16x32_bf16 a[60:63],a[148:151],v[236:239],a[60:63]
buffer_load_dwordx4 v9,s[72:75],s83 offen offset:2048 lds
v_mfma_f32_16x16x32_bf16 a[60:63],a[144:147],v[232:235],a[60:63]
ds_read_b64_tr_b16 a[144:145],v2 offset:32
ds_read_b64_tr_b16 a[146:147],v2 offset:5152
v_mfma_f32_16x16x32_bf16 a[64:67],a[160:163],v[136:139],a[64:67]
ds_read_b64_tr_b16 a[148:149],v2 offset:10272
v_mfma_f32_16x16x32_bf16 a[64:67],a[164:167],v[140:143],a[64:67]
ds_read_b64_tr_b16 a[150:151],v2 offset:15392
v_mfma_f32_16x16x32_bf16 a[64:67],a[168:171],v[152:155],a[64:67]
ds_read_b64_tr_b16 a[154:155],v2 offset:25632
v_mfma_f32_16x16x32_bf16 a[64:67],a[172:175],v[156:159],a[64:67]
v_mfma_f32_16x16x32_bf16 a[68:71],a[172:175],v[188:191],a[68:71]
v_mfma_f32_16x16x32_bf16 a[68:71],a[168:171],v[184:187],a[68:71]
v_mfma_f32_16x16x32_bf16 a[68:71],a[164:167],v[172:175],a[68:71]
v_mfma_f32_16x16x32_bf16 a[68:71],a[160:163],v[168:171],a[68:71]
v_mfma_f32_16x16x32_bf16 a[72:75],a[160:163],v[200:203],a[72:75]
v_mfma_f32_16x16x32_bf16 a[72:75],a[164:167],v[204:207],a[72:75]
v_mfma_f32_16x16x32_bf16 a[72:75],a[168:171],v[216:219],a[72:75]
v_mfma_f32_16x16x32_bf16 a[72:75],a[172:175],v[220:223],a[72:75]
buffer_load_dwordx4 v[64:67],v6,s[28:31],s76 offen sc1
v_mfma_f32_16x16x32_bf16 a[76:79],a[172:175],v[252:255],a[76:79]
ds_read_b64_tr_b16 a[172:173],v1 offset:30784
ds_read_b64_tr_b16 a[174:175],v1 offset:35904
v_mfma_f32_16x16x32_bf16 a[76:79],a[168:171],v[248:251],a[76:79]
ds_read_b64_tr_b16 a[168:169],v1 offset:20544
v_mfma_f32_16x16x32_bf16 a[76:79],a[164:167],v[236:239],a[76:79]
ds_read_b64_tr_b16 a[164:165],v1 offset:10304
v_mfma_f32_16x16x32_bf16 a[76:79],a[160:163],v[232:235],a[76:79]
s_waitcnt lgkmcnt(4)
buffer_load_dwordx4 v10,s[72:75],s83 offen offset:3072 lds
v_mfma_f32_16x16x32_bf16 a[80:83],a[176:179],v[136:139],a[80:83]
ds_read_b64_tr_b16 a[160:161],v1 offset:64
ds_read_b64_tr_b16 a[162:163],v1 offset:5184
v_mfma_f32_16x16x32_bf16 a[80:83],a[180:183],v[140:143],a[80:83]
ds_read_b64_tr_b16 a[166:167],v1 offset:15424
v_mfma_f32_16x16x32_bf16 a[80:83],a[184:187],v[152:155],a[80:83]
ds_read_b64_tr_b16 a[170:171],v1 offset:25664
v_mfma_f32_16x16x32_bf16 a[80:83],a[188:191],v[156:159],a[80:83]
buffer_load_dwordx4 v[68:71],v6,s[28:31],s76 offen offset:1024 sc1
v_mfma_f32_16x16x32_bf16 a[84:87],a[188:191],v[188:191],a[84:87]
v_mfma_f32_16x16x32_bf16 a[84:87],a[184:187],v[184:187],a[84:87]
v_mfma_f32_16x16x32_bf16 a[84:87],a[180:183],v[172:175],a[84:87]
v_mfma_f32_16x16x32_bf16 a[84:87],a[176:179],v[168:171],a[84:87]
v_mfma_f32_16x16x32_bf16 a[88:91],a[176:179],v[200:203],a[88:91]
v_mfma_f32_16x16x32_bf16 a[88:91],a[180:183],v[204:207],a[88:91]
s_add_u32 m0,s82,91136
v_mfma_f32_16x16x32_bf16 a[88:91],a[184:187],v[216:219],a[88:91]
v_mfma_f32_16x16x32_bf16 a[88:91],a[188:191],v[220:223],a[88:91]
buffer_load_dwordx4 v[72:75],v6,s[28:31],s76 offen offset:2048 sc1
v_mfma_f32_16x16x32_bf16 a[92:95],a[188:191],v[252:255],a[92:95]
ds_read_b64_tr_b16 a[188:189],v2 offset:30816
ds_read_b64_tr_b16 a[190:191],v2 offset:35936
v_mfma_f32_16x16x32_bf16 a[92:95],a[184:187],v[248:251],a[92:95]
ds_read_b64_tr_b16 a[184:185],v2 offset:20576
v_mfma_f32_16x16x32_bf16 a[92:95],a[180:183],v[236:239],a[92:95]
s_cmp_ge_u32 0,s71
s_cselect_b32 s80,3,1
s_mul_i32 s80,s80,163840
v_mfma_f32_16x16x32_bf16 a[92:95],a[176:179],v[232:235],a[92:95]
s_add_u32 s80,s80,s81
buffer_load_dwordx4 v11,s[72:75],s83 offen lds
s_waitcnt vmcnt(14)
v_mfma_f32_16x16x32_bf16 a[192:195],a[128:131],v[128:131],a[192:195]
s_add_u32 s83,s80,20480
ds_read_b64_tr_b16 a[176:177],v2 offset:96
ds_read_b64_tr_b16 a[178:179],v2 offset:5216
v_mfma_f32_16x16x32_bf16 a[192:195],a[132:135],v[160:163],a[192:195]
ds_read_b64_tr_b16 a[180:181],v2 offset:10336
v_mfma_f32_16x16x32_bf16 a[192:195],a[136:139],v[192:195],a[192:195]
ds_read_b64_tr_b16 a[182:183],v2 offset:15456
v_mfma_f32_16x16x32_bf16 a[192:195],a[140:143],v[224:227],a[192:195]
buffer_load_dwordx4 v[76:79],v6,s[28:31],s76 offen offset:3072 sc1
v_mfma_f32_16x16x32_bf16 a[196:199],a[140:143],v[228:231],a[196:199]
ds_read_b64_tr_b16 a[186:187],v2 offset:25696
v_mfma_f32_16x16x32_bf16 a[196:199],a[136:139],v[196:199],a[196:199]
v_mfma_f32_16x16x32_bf16 a[196:199],a[132:135],v[164:167],a[196:199]
v_mfma_f32_16x16x32_bf16 a[196:199],a[128:131],v[132:135],a[196:199]
v_mfma_f32_16x16x32_bf16 a[200:203],a[128:131],v[144:147],a[200:203]
v_mfma_f32_16x16x32_bf16 a[200:203],a[132:135],v[176:179],a[200:203]
v_mfma_f32_16x16x32_bf16 a[200:203],a[136:139],v[208:211],a[200:203]
v_mfma_f32_16x16x32_bf16 a[200:203],a[140:143],v[240:243],a[200:203]
s_lshl_b32 s95,s42,2
buffer_load_dwordx4 v[80:83],v6,s[28:31],s77 offen sc1
s_waitcnt vmcnt(11)
v_mfma_f32_16x16x32_bf16 a[204:207],a[140:143],v[244:247],a[204:207]
s_add_u32 s95,s95,s40
s_lshl_b32 s95,s95,4
v_mov_b32 v18,s68
v_mfma_f32_16x16x32_bf16 a[204:207],a[136:139],v[212:215],a[204:207]
ds_read_b64_tr_b16 a[136:137],v1 offset:20672
v_mov_b32 v19,s69
v_mfma_f32_16x16x32_bf16 a[204:207],a[132:135],v[180:183],a[204:207]
ds_read_b64_tr_b16 a[132:133],v1 offset:10432
ds_read_b64_tr_b16 a[134:135],v1 offset:15552
v_mfma_f32_16x16x32_bf16 a[204:207],a[128:131],v[148:151],a[204:207]
s_barrier
ds_read_b64_tr_b16 a[128:129],v1 offset:192
ds_read_b64_tr_b16 a[130:131],v1 offset:5312
v_mfma_f32_16x16x32_bf16 a[208:211],a[144:147],v[128:131],a[208:211]
ds_read_b64_tr_b16 a[138:139],v1 offset:25792
v_mfma_f32_16x16x32_bf16 a[208:211],a[148:151],v[160:163],a[208:211]
ds_read_b64_tr_b16 a[140:141],v1 offset:30912
v_mfma_f32_16x16x32_bf16 a[208:211],a[152:155],v[192:195],a[208:211]
s_add_u32 m0,s82,0
s_waitcnt vmcnt(8)
s_mov_b64 exec,1
buffer_store_dwordx2 v[18:19],v6,s[64:67],s95 offen offset:1024 sc1
s_mov_b64 exec,-1
v_mfma_f32_16x16x32_bf16 a[208:211],a[156:159],v[224:227],a[208:211]
ds_read_b64_tr_b16 a[142:143],v1 offset:36032
v_mfma_f32_16x16x32_bf16 a[212:215],a[156:159],v[228:231],a[212:215]
v_mfma_f32_16x16x32_bf16 a[212:215],a[152:155],v[196:199],a[212:215]
v_mfma_f32_16x16x32_bf16 a[212:215],a[148:151],v[164:167],a[212:215]
buffer_load_dwordx4 v[84:87],v6,s[28:31],s77 offen offset:1024 sc1
v_mfma_f32_16x16x32_bf16 a[212:215],a[144:147],v[132:135],a[212:215]
v_mfma_f32_16x16x32_bf16 a[216:219],a[144:147],v[144:147],a[216:219]
v_mfma_f32_16x16x32_bf16 a[216:219],a[148:151],v[176:179],a[216:219]
v_mfma_f32_16x16x32_bf16 a[216:219],a[152:155],v[208:211],a[216:219]
buffer_load_dwordx4 v7,s[72:75],s80 offen lds
v_mfma_f32_16x16x32_bf16 a[216:219],a[156:159],v[240:243],a[216:219]
v_mfma_f32_16x16x32_bf16 a[220:223],a[156:159],v[244:247],a[220:223]
ds_read_b64_tr_b16 a[156:157],v2 offset:30944
ds_read_b64_tr_b16 a[158:159],v2 offset:36064
v_mfma_f32_16x16x32_bf16 a[220:223],a[152:155],v[212:215],a[220:223]
ds_read_b64_tr_b16 a[152:153],v2 offset:20704
v_mfma_f32_16x16x32_bf16 a[220:223],a[148:151],v[180:183],a[220:223]
buffer_load_dwordx4 v[88:91],v6,s[28:31],s77 offen offset:2048 sc1
v_mfma_f32_16x16x32_bf16 a[220:223],a[144:147],v[148:151],a[220:223]
s_waitcnt lgkmcnt(3)
ds_read_b64_tr_b16 a[144:145],v2 offset:224
ds_read_b64_tr_b16 a[146:147],v2 offset:5344
v_mfma_f32_16x16x32_bf16 a[224:227],a[160:163],v[128:131],a[224:227]
ds_read_b64_tr_b16 a[148:149],v2 offset:10464
v_mfma_f32_16x16x32_bf16 a[224:227],a[164:167],v[160:163],a[224:227]
ds_read_b64_tr_b16 a[150:151],v2 offset:15584
v_mfma_f32_16x16x32_bf16 a[224:227],a[168:171],v[192:195],a[224:227]
buffer_load_dwordx4 v[92:95],v6,s[28:31],s77 offen offset:3072 sc1
v_mfma_f32_16x16x32_bf16 a[224:227],a[172:175],v[224:227],a[224:227]
ds_read_b64_tr_b16 a[154:155],v2 offset:25824
v_mfma_f32_16x16x32_bf16 a[228:231],a[172:175],v[228:231],a[228:231]
v_mfma_f32_16x16x32_bf16 a[228:231],a[168:171],v[196:199],a[228:231]
v_mfma_f32_16x16x32_bf16 a[228:231],a[164:167],v[164:167],a[228:231]
buffer_load_dwordx4 v8,s[72:75],s80 offen offset:1024 lds
v_mfma_f32_16x16x32_bf16 a[228:231],a[160:163],v[132:135],a[228:231]
v_mfma_f32_16x16x32_bf16 a[232:235],a[160:163],v[144:147],a[232:235]
v_mfma_f32_16x16x32_bf16 a[232:235],a[164:167],v[176:179],a[232:235]
v_mfma_f32_16x16x32_bf16 a[232:235],a[168:171],v[208:211],a[232:235]
buffer_load_dwordx4 v[96:99],v6,s[28:31],s78 offen sc1
v_mfma_f32_16x16x32_bf16 a[232:235],a[172:175],v[240:243],a[232:235]
v_mfma_f32_16x16x32_bf16 a[236:239],a[172:175],v[244:247],a[236:239]
ds_read_b64_tr_b16 a[172:173],v1 offset:30976
ds_read_b64_tr_b16 a[174:175],v1 offset:36096
v_mfma_f32_16x16x32_bf16 a[236:239],a[168:171],v[212:215],a[236:239]
ds_read_b64_tr_b16 a[168:169],v1 offset:20736
v_mfma_f32_16x16x32_bf16 a[236:239],a[164:167],v[180:183],a[236:239]
ds_read_b64_tr_b16 a[164:165],v1 offset:10496
v_mfma_f32_16x16x32_bf16 a[236:239],a[160:163],v[148:151],a[236:239]
ds_read_b64_tr_b16 a[160:161],v1 offset:256
v_mfma_f32_16x16x32_bf16 a[240:243],a[176:179],v[128:131],a[240:243]
ds_read_b64_tr_b16 a[162:163],v1 offset:5376
ds_read_b64_tr_b16 a[166:167],v1 offset:15616
v_mfma_f32_16x16x32_bf16 a[240:243],a[180:183],v[160:163],a[240:243]
ds_read_b64_tr_b16 a[170:171],v1 offset:25856
v_add_u32 v1,81920,v1
v_mfma_f32_16x16x32_bf16 a[240:243],a[184:187],v[192:195],a[240:243]
buffer_load_dwordx4 v[100:103],v6,s[28:31],s78 offen offset:1024 sc1
v_mfma_f32_16x16x32_bf16 a[240:243],a[188:191],v[224:227],a[240:243]
v_mfma_f32_16x16x32_bf16 a[244:247],a[188:191],v[228:231],a[244:247]
v_mfma_f32_16x16x32_bf16 a[244:247],a[184:187],v[196:199],a[244:247]
v_mfma_f32_16x16x32_bf16 a[244:247],a[180:183],v[164:167],a[244:247]
buffer_load_dwordx4 v9,s[72:75],s80 offen offset:2048 lds
v_mfma_f32_16x16x32_bf16 a[244:247],a[176:179],v[132:135],a[244:247]
v_mfma_f32_16x16x32_bf16 a[248:251],a[176:179],v[144:147],a[248:251]
v_mfma_f32_16x16x32_bf16 a[248:251],a[180:183],v[176:179],a[248:251]
v_mfma_f32_16x16x32_bf16 a[248:251],a[184:187],v[208:211],a[248:251]
buffer_load_dwordx4 v[104:107],v6,s[28:31],s78 offen offset:2048 sc1
)A"
R"A(v_mfma_f32_16x16x32_bf16 a[248:251],a[188:191],v[240:243],a[248:251]
v_mfma_f32_16x16x32_bf16 a[252:255],a[188:191],v[244:247],a[252:255]
ds_read_b64_tr_b16 a[188:189],v2 offset:31008
ds_read_b64_tr_b16 a[190:191],v2 offset:36128
v_mfma_f32_16x16x32_bf16 a[252:255],a[184:187],v[212:215],a[252:255]
ds_read_b64_tr_b16 a[184:185],v2 offset:20768
v_mfma_f32_16x16x32_bf16 a[252:255],a[180:183],v[180:183],a[252:255]
ds_read_b64_tr_b16 a[180:181],v2 offset:10528
v_mfma_f32_16x16x32_bf16 a[252:255],a[176:179],v[148:151],a[252:255]
ds_read_b64_tr_b16 a[176:177],v2 offset:288
v_mfma_f32_16x16x32_bf16 a[0:3],a[96:99],v[128:131],a[0:3]
ds_read_b64_tr_b16 a[178:179],v2 offset:5408
ds_read_b64_tr_b16 a[182:183],v2 offset:15648
v_mfma_f32_16x16x32_bf16 a[0:3],a[100:103],v[160:163],a[0:3]
ds_read_b64_tr_b16 a[186:187],v2 offset:25888
v_add_u32 v2,81920,v2
v_mfma_f32_16x16x32_bf16 a[0:3],a[104:107],v[192:195],a[0:3]
buffer_load_dwordx4 v[108:111],v6,s[28:31],s78 offen offset:3072 sc1
v_mfma_f32_16x16x32_bf16 a[0:3],a[108:111],v[224:227],a[0:3]
v_mfma_f32_16x16x32_bf16 a[4:7],a[108:111],v[228:231],a[4:7]
v_mfma_f32_16x16x32_bf16 a[4:7],a[104:107],v[196:199],a[4:7]
v_mfma_f32_16x16x32_bf16 a[4:7],a[100:103],v[164:167],a[4:7]
buffer_load_dwordx4 v10,s[72:75],s80 offen offset:3072 lds
v_mfma_f32_16x16x32_bf16 a[4:7],a[96:99],v[132:135],a[4:7]
v_mfma_f32_16x16x32_bf16 a[8:11],a[96:99],v[144:147],a[8:11]
v_mfma_f32_16x16x32_bf16 a[8:11],a[100:103],v[176:179],a[8:11]
v_mfma_f32_16x16x32_bf16 a[8:11],a[104:107],v[208:211],a[8:11]
buffer_load_dwordx4 v[112:115],v6,s[28:31],s79 offen sc1
v_mfma_f32_16x16x32_bf16 a[8:11],a[108:111],v[240:243],a[8:11]
v_mfma_f32_16x16x32_bf16 a[12:15],a[108:111],v[244:247],a[12:15]
ds_read_b64_tr_b16 a[108:109],v1 offset:30720
ds_read_b64_tr_b16 a[110:111],v1 offset:35840
v_mfma_f32_16x16x32_bf16 a[12:15],a[104:107],v[212:215],a[12:15]
ds_read_b64_tr_b16 a[104:105],v1 offset:20480
v_mfma_f32_16x16x32_bf16 a[12:15],a[100:103],v[180:183],a[12:15]
s_add_u32 m0,s82,4096
ds_read_b64_tr_b16 a[100:101],v1 offset:10240
v_mfma_f32_16x16x32_bf16 a[12:15],a[96:99],v[148:151],a[12:15]
ds_read_b64_tr_b16 a[96:97],v1
v_mfma_f32_16x16x32_bf16 a[16:19],a[112:115],v[128:131],a[16:19]
ds_read_b64_tr_b16 a[98:99],v1 offset:5120
ds_read_b64_tr_b16 a[102:103],v1 offset:15360
v_mfma_f32_16x16x32_bf16 a[16:19],a[116:119],v[160:163],a[16:19]
buffer_load_dwordx4 v11,s[72:75],s80 offen lds
v_mfma_f32_16x16x32_bf16 a[16:19],a[120:123],v[192:195],a[16:19]
ds_read_b64_tr_b16 a[106:107],v1 offset:25600
v_mfma_f32_16x16x32_bf16 a[16:19],a[124:127],v[224:227],a[16:19]
v_mfma_f32_16x16x32_bf16 a[20:23],a[124:127],v[228:231],a[20:23]
v_mfma_f32_16x16x32_bf16 a[20:23],a[120:123],v[196:199],a[20:23]
buffer_load_dwordx4 v[116:119],v6,s[28:31],s79 offen offset:1024 sc1
v_mfma_f32_16x16x32_bf16 a[20:23],a[116:119],v[164:167],a[20:23]
v_mfma_f32_16x16x32_bf16 a[20:23],a[112:115],v[132:135],a[20:23]
v_mfma_f32_16x16x32_bf16 a[24:27],a[112:115],v[144:147],a[24:27]
v_mfma_f32_16x16x32_bf16 a[24:27],a[116:119],v[176:179],a[24:27]
v_mfma_f32_16x16x32_bf16 a[24:27],a[120:123],v[208:211],a[24:27]
buffer_load_dwordx4 v[120:123],v6,s[28:31],s79 offen offset:2048 sc1
v_mfma_f32_16x16x32_bf16 a[24:27],a[124:127],v[240:243],a[24:27]
v_mfma_f32_16x16x32_bf16 a[28:31],a[124:127],v[244:247],a[28:31]
ds_read_b64_tr_b16 a[124:125],v2 offset:30752
ds_read_b64_tr_b16 a[126:127],v2 offset:35872
v_mfma_f32_16x16x32_bf16 a[28:31],a[120:123],v[212:215],a[28:31]
s_add_u32 m0,s82,5120
ds_read_b64_tr_b16 a[120:121],v2 offset:20512
v_mfma_f32_16x16x32_bf16 a[28:31],a[116:119],v[180:183],a[28:31]
ds_read_b64_tr_b16 a[116:117],v2 offset:10272
v_mfma_f32_16x16x32_bf16 a[28:31],a[112:115],v[148:151],a[28:31]
s_cmp_ge_u32 2,s71
s_cselect_b32 s76,6,4
s_lshl_b32 s76,s76,16
v_mfma_f32_16x16x32_bf16 a[32:35],a[128:131],v[128:131],a[32:35]
s_add_u32 s77,s76,4096
buffer_load_dwordx4 v7,s[72:75],s83 offen lds
v_mfma_f32_16x16x32_bf16 a[32:35],a[132:135],v[160:163],a[32:35]
s_add_u32 s78,s76,8192
s_max_i32 s77,s77,s19
ds_read_b64_tr_b16 a[112:113],v2 offset:32
v_mfma_f32_16x16x32_bf16 a[32:35],a[136:139],v[192:195],a[32:35]
s_max_i32 s78,s78,s100
ds_read_b64_tr_b16 a[114:115],v2 offset:5152
ds_read_b64_tr_b16 a[118:119],v2 offset:15392
v_mfma_f32_16x16x32_bf16 a[32:35],a[140:143],v[224:227],a[32:35]
ds_read_b64_tr_b16 a[122:123],v2 offset:25632
v_mfma_f32_16x16x32_bf16 a[36:39],a[140:143],v[228:231],a[36:39]
buffer_load_dwordx4 v[124:127],v6,s[28:31],s79 offen offset:3072 sc1
v_mfma_f32_16x16x32_bf16 a[36:39],a[136:139],v[196:199],a[36:39]
s_add_u32 s79,s76,12288
s_max_i32 s76,s76,s18
s_max_i32 s79,s79,s101
v_mfma_f32_16x16x32_bf16 a[36:39],a[132:135],v[164:167],a[36:39]
v_mfma_f32_16x16x32_bf16 a[36:39],a[128:131],v[132:135],a[36:39]
v_mfma_f32_16x16x32_bf16 a[40:43],a[128:131],v[144:147],a[40:43]
v_mfma_f32_16x16x32_bf16 a[40:43],a[132:135],v[176:179],a[40:43]
v_mfma_f32_16x16x32_bf16 a[40:43],a[136:139],v[208:211],a[40:43]
v_mfma_f32_16x16x32_bf16 a[40:43],a[140:143],v[240:243],a[40:43]
v_mfma_f32_16x16x32_bf16 a[44:47],a[140:143],v[244:247],a[44:47]
ds_read_b64_tr_b16 a[140:141],v1 offset:30784
ds_read_b64_tr_b16 a[142:143],v1 offset:35904
v_mfma_f32_16x16x32_bf16 a[44:47],a[136:139],v[212:215],a[44:47]
ds_read_b64_tr_b16 a[136:137],v1 offset:20544
v_mfma_f32_16x16x32_bf16 a[44:47],a[132:135],v[180:183],a[44:47]
ds_read_b64_tr_b16 a[132:133],v1 offset:10304
v_mfma_f32_16x16x32_bf16 a[44:47],a[128:131],v[148:151],a[44:47]
s_waitcnt lgkmcnt(4)
buffer_load_dwordx4 v8,s[72:75],s83 offen offset:1024 lds
v_mfma_f32_16x16x32_bf16 a[48:51],a[144:147],v[128:131],a[48:51]
ds_read_b64_tr_b16 a[128:129],v1 offset:64
ds_read_b64_tr_b16 a[130:131],v1 offset:5184
v_mfma_f32_16x16x32_bf16 a[48:51],a[148:151],v[160:163],a[48:51]
ds_read_b64_tr_b16 a[134:135],v1 offset:15424
v_mfma_f32_16x16x32_bf16 a[48:51],a[152:155],v[192:195],a[48:51]
ds_read_b64_tr_b16 a[138:139],v1 offset:25664
v_mfma_f32_16x16x32_bf16 a[48:51],a[156:159],v[224:227],a[48:51]
s_lshl_b32 s94,s70,4
v_bfe_u32 v24,v6,4,2
v_min_u32 v24,2,v24
v_mfma_f32_16x16x32_bf16 a[52:55],a[156:159],v[228:231],a[52:55]
s_add_u32 s94,s94,s40
v_add_u32 v24,s71,v24
v_add_u32 v24,1,v24
v_mfma_f32_16x16x32_bf16 a[52:55],a[152:155],v[196:199],a[52:55]
s_lshl_b32 s94,s94,4
v_and_b32 v24,3,v24
v_lshlrev_b32 v24,6,v24
v_mfma_f32_16x16x32_bf16 a[52:55],a[148:151],v[164:167],a[52:55]
v_mfma_f32_16x16x32_bf16 a[52:55],a[144:147],v[132:135],a[52:55]
buffer_load_dwordx4 v[136:139],v6,s[28:31],s76 offen sc1
v_mfma_f32_16x16x32_bf16 a[56:59],a[144:147],v[144:147],a[56:59]
v_mfma_f32_16x16x32_bf16 a[56:59],a[148:151],v[176:179],a[56:59]
v_mfma_f32_16x16x32_bf16 a[56:59],a[152:155],v[208:211],a[56:59]
v_mfma_f32_16x16x32_bf16 a[56:59],a[156:159],v[240:243],a[56:59]
v_mfma_f32_16x16x32_bf16 a[60:63],a[156:159],v[244:247],a[60:63]
ds_read_b64_tr_b16 a[156:157],v2 offset:30816
ds_read_b64_tr_b16 a[158:159],v2 offset:35936
v_mfma_f32_16x16x32_bf16 a[60:63],a[152:155],v[212:215],a[60:63]
buffer_load_dwordx4 v[140:143],v6,s[28:31],s76 offen offset:1024 sc1
v_mfma_f32_16x16x32_bf16 a[60:63],a[148:151],v[180:183],a[60:63]
ds_read_b64_tr_b16 a[148:149],v2 offset:10336
ds_read_b64_tr_b16 a[150:151],v2 offset:15456
v_mfma_f32_16x16x32_bf16 a[60:63],a[144:147],v[148:151],a[60:63]
ds_read_b64_tr_b16 a[144:145],v2 offset:96
v_mfma_f32_16x16x32_bf16 a[64:67],a[160:163],v[128:131],a[64:67]
ds_read_b64_tr_b16 a[146:147],v2 offset:5216
v_mfma_f32_16x16x32_bf16 a[64:67],a[164:167],v[160:163],a[64:67]
buffer_load_dwordx4 v9,s[72:75],s83 offen offset:2048 lds
v_mfma_f32_16x16x32_bf16 a[64:67],a[168:171],v[192:195],a[64:67]
ds_read_b64_tr_b16 a[152:153],v2 offset:20576
ds_read_b64_tr_b16 a[154:155],v2 offset:25696
v_mfma_f32_16x16x32_bf16 a[64:67],a[172:175],v[224:227],a[64:67]
v_mfma_f32_16x16x32_bf16 a[68:71],a[172:175],v[228:231],a[68:71]
v_mfma_f32_16x16x32_bf16 a[68:71],a[168:171],v[196:199],a[68:71]
buffer_load_dwordx4 v[152:155],v6,s[28:31],s76 offen offset:2048 sc1
v_mfma_f32_16x16x32_bf16 a[68:71],a[164:167],v[164:167],a[68:71]
v_mfma_f32_16x16x32_bf16 a[68:71],a[160:163],v[132:135],a[68:71]
v_mfma_f32_16x16x32_bf16 a[72:75],a[160:163],v[144:147],a[72:75]
v_mfma_f32_16x16x32_bf16 a[72:75],a[164:167],v[176:179],a[72:75]
buffer_load_dwordx4 v[156:159],v6,s[28:31],s76 offen offset:3072 sc1
v_mfma_f32_16x16x32_bf16 a[72:75],a[168:171],v[208:211],a[72:75]
v_mfma_f32_16x16x32_bf16 a[72:75],a[172:175],v[240:243],a[72:75]
v_mfma_f32_16x16x32_bf16 a[76:79],a[172:175],v[244:247],a[76:79]
ds_read_b64_tr_b16 a[172:173],v1 offset:30848
ds_read_b64_tr_b16 a[174:175],v1 offset:35968
v_mfma_f32_16x16x32_bf16 a[76:79],a[168:171],v[212:215],a[76:79]
buffer_load_dwordx4 v10,s[72:75],s83 offen offset:3072 lds
v_mfma_f32_16x16x32_bf16 a[76:79],a[164:167],v[180:183],a[76:79]
ds_read_b64_tr_b16 a[164:165],v1 offset:10368
ds_read_b64_tr_b16 a[166:167],v1 offset:15488
v_mfma_f32_16x16x32_bf16 a[76:79],a[160:163],v[148:151],a[76:79]
ds_read_b64_tr_b16 a[160:161],v1 offset:128
v_mfma_f32_16x16x32_bf16 a[80:83],a[176:179],v[128:131],a[80:83]
ds_read_b64_tr_b16 a[162:163],v1 offset:5248
v_mfma_f32_16x16x32_bf16 a[80:83],a[180:183],v[160:163],a[80:83]
buffer_load_dwordx4 v[168:171],v6,s[28:31],s77 offen sc1
v_mfma_f32_16x16x32_bf16 a[80:83],a[184:187],v[192:195],a[80:83]
ds_read_b64_tr_b16 a[168:169],v1 offset:20608
ds_read_b64_tr_b16 a[170:171],v1 offset:25728
v_mfma_f32_16x16x32_bf16 a[80:83],a[188:191],v[224:227],a[80:83]
v_mfma_f32_16x16x32_bf16 a[84:87],a[188:191],v[228:231],a[84:87]
v_mfma_f32_16x16x32_bf16 a[84:87],a[184:187],v[196:199],a[84:87]
buffer_load_dwordx4 v[172:175],v6,s[28:31],s77 offen offset:1024 sc1
v_mfma_f32_16x16x32_bf16 a[84:87],a[180:183],v[164:167],a[84:87]
v_mfma_f32_16x16x32_bf16 a[84:87],a[176:179],v[132:135],a[84:87]
v_mfma_f32_16x16x32_bf16 a[88:91],a[176:179],v[144:147],a[88:91]
v_mfma_f32_16x16x32_bf16 a[88:91],a[180:183],v[176:179],a[88:91]
s_add_u32 m0,s82,9216
v_mfma_f32_16x16x32_bf16 a[88:91],a[184:187],v[208:211],a[88:91]
v_mfma_f32_16x16x32_bf16 a[88:91],a[188:191],v[240:243],a[88:91]
buffer_load_dwordx2 v[26:27],v24,s[64:67],s94 offen offset:1024 sc1
v_mfma_f32_16x16x32_bf16 a[92:95],a[188:191],v[244:247],a[92:95]
ds_read_b64_tr_b16 a[188:189],v2 offset:30880
ds_read_b64_tr_b16 a[190:191],v2 offset:36000
v_mfma_f32_16x16x32_bf16 a[92:95],a[184:187],v[212:215],a[92:95]
ds_read_b64_tr_b16 a[184:185],v2 offset:20640
v_mfma_f32_16x16x32_bf16 a[92:95],a[180:183],v[180:183],a[92:95]
ds_read_b64_tr_b16 a[180:181],v2 offset:10400
v_mfma_f32_16x16x32_bf16 a[92:95],a[176:179],v[148:151],a[92:95]
s_waitcnt vmcnt(13)
buffer_load_dwordx4 v[184:187],v6,s[28:31],s77 offen offset:2048 sc1
v_mfma_f32_16x16x32_bf16 a[192:195],a[96:99],v[64:67],a[192:195]
s_lshl_b32 s43,s41,2
ds_read_b64_tr_b16 a[176:177],v2 offset:160
ds_read_b64_tr_b16 a[178:179],v2 offset:5280
v_mfma_f32_16x16x32_bf16 a[192:195],a[100:103],v[68:71],a[192:195]
s_add_u32 s84,s70,1
s_add_u32 s85,s70,2
ds_read_b64_tr_b16 a[182:183],v2 offset:15520
v_mfma_f32_16x16x32_bf16 a[192:195],a[104:107],v[72:75],a[192:195]
s_add_u32 s86,s70,3
s_add_u32 s43,s43,s71
ds_read_b64_tr_b16 a[186:187],v2 offset:25760
v_mfma_f32_16x16x32_bf16 a[192:195],a[108:111],v[76:79],a[192:195]
s_and_b32 s84,s84,3
buffer_load_dwordx4 v11,s[72:75],s83 offen lds
v_mfma_f32_16x16x32_bf16 a[196:199],a[108:111],v[92:95],a[196:199]
s_and_b32 s85,s85,3
s_and_b32 s86,s86,3
s_mul_i32 s43,s43,245760
v_mfma_f32_16x16x32_bf16 a[196:199],a[104:107],v[88:91],a[196:199]
s_mul_i32 s84,s84,12
s_mul_i32 s85,s85,12
s_mul_i32 s86,s86,12
v_mfma_f32_16x16x32_bf16 a[196:199],a[100:103],v[84:87],a[196:199]
s_add_u32 s43,s43,134217728
s_mov_b64 s[32:33],s[12:13]
s_add_u32 s84,s84,0
v_mfma_f32_16x16x32_bf16 a[196:199],a[96:99],v[80:83],a[196:199]
s_add_u32 s85,s85,4
buffer_load_dwordx4 v[188:191],v6,s[28:31],s77 offen offset:3072 sc1
v_mfma_f32_16x16x32_bf16 a[200:203],a[96:99],v[96:99],a[200:203]
s_add_u32 s86,s86,8
s_add_u32 s84,s84,s40
s_add_u32 s85,s85,s40
v_mfma_f32_16x16x32_bf16 a[200:203],a[100:103],v[100:103],a[200:203]
s_add_u32 s86,s86,s40
)A"
R"A(s_add_u32 s32,s32,s43
s_addc_u32 s33,s33,0
v_mfma_f32_16x16x32_bf16 a[200:203],a[104:107],v[104:107],a[200:203]
s_mul_i32 s84,s84,5120
s_mul_i32 s85,s85,5120
s_mul_i32 s86,s86,5120
v_mfma_f32_16x16x32_bf16 a[200:203],a[108:111],v[108:111],a[200:203]
s_and_b32 s33,s33,0xffff
buffer_load_dwordx4 v[200:203],v6,s[28:31],s78 offen sc1
s_waitcnt vmcnt(14)
v_mfma_f32_16x16x32_bf16 a[204:207],a[108:111],v[124:127],a[204:207]
s_mov_b32 s34,245760
ds_read_b64_tr_b16 a[108:109],v1 offset:30912
ds_read_b64_tr_b16 a[110:111],v1 offset:36032
v_mfma_f32_16x16x32_bf16 a[204:207],a[104:107],v[120:123],a[204:207]
s_mov_b32 s35,0x20000
s_max_i32 s84,s84,s18
ds_read_b64_tr_b16 a[104:105],v1 offset:20672
v_mfma_f32_16x16x32_bf16 a[204:207],a[100:103],v[116:119],a[204:207]
s_max_i32 s85,s85,s19
s_max_i32 s86,s86,s100
ds_read_b64_tr_b16 a[100:101],v1 offset:10432
v_mfma_f32_16x16x32_bf16 a[204:207],a[96:99],v[112:115],a[204:207]
s_barrier
buffer_load_dwordx4 v[204:207],v6,s[28:31],s78 offen offset:1024 sc1
v_mfma_f32_16x16x32_bf16 a[208:211],a[112:115],v[64:67],a[208:211]
s_mul_i32 s44,s41,307200
ds_read_b64_tr_b16 a[96:97],v1 offset:192
ds_read_b64_tr_b16 a[98:99],v1 offset:5312
v_mfma_f32_16x16x32_bf16 a[208:211],a[116:119],v[68:71],a[208:211]
s_mov_b64 s[20:21],s[16:17]
ds_read_b64_tr_b16 a[102:103],v1 offset:15552
v_bfe_u32 v18,v6,6,2
v_mfma_f32_16x16x32_bf16 a[208:211],a[120:123],v[72:75],a[208:211]
s_lshl_b32 s43,s40,2
ds_read_b64_tr_b16 a[106:107],v1 offset:25792
v_lshrrev_b32 v19,8,v6
v_mfma_f32_16x16x32_bf16 a[208:211],a[124:127],v[76:79],a[208:211]
s_add_u32 s43,s43,s70
buffer_load_dwordx4 v[216:219],v6,s[28:31],s78 offen offset:2048 sc1
v_mfma_f32_16x16x32_bf16 a[212:215],a[124:127],v[92:95],a[212:215]
s_mul_i32 s43,s43,20480
v_xor_b32 v18,v18,v19
v_lshlrev_b32 v18,2,v18
v_mfma_f32_16x16x32_bf16 a[212:215],a[120:123],v[88:91],a[212:215]
s_mov_b32 s22,307200
v_bfe_u32 v19,v6,4,2
v_or_b32 v18,v18,v19
v_mfma_f32_16x16x32_bf16 a[212:215],a[116:119],v[84:87],a[212:215]
s_mov_b32 s23,0x20000
v_and_b32 v19,16,v6
v_mov_b32 v34,0x3f80
v_mfma_f32_16x16x32_bf16 a[212:215],a[112:115],v[80:83],a[212:215]
s_cmp_ge_u32 0,s71
s_cselect_b32 s76,3,1
s_lshl_b32 s76,s76,16
v_mfma_f32_16x16x32_bf16 a[216:219],a[112:115],v[96:99],a[216:219]
s_add_u32 s77,s76,4096
buffer_load_dwordx4 v[220:223],v6,s[28:31],s78 offen offset:3072 sc1
v_mfma_f32_16x16x32_bf16 a[216:219],a[116:119],v[100:103],a[216:219]
s_add_u32 s78,s76,8192
s_max_i32 s77,s77,s19
s_max_i32 s78,s78,s100
v_mfma_f32_16x16x32_bf16 a[216:219],a[120:123],v[104:107],a[216:219]
v_lshlrev_b32 v19,v19,v34
v_lshrrev_b32 v18,1,v18
v_mfma_f32_16x16x32_bf16 a[216:219],a[124:127],v[108:111],a[216:219]
v_cmp_eq_u32 vcc,0,v18
s_nop 1
v_cndmask_b32 v34,0,v19,vcc
v_mfma_f32_16x16x32_bf16 a[220:223],a[124:127],v[124:127],a[220:223]
ds_read_b64_tr_b16 a[124:125],v2 offset:30944
ds_read_b64_tr_b16 a[126:127],v2 offset:36064
v_mfma_f32_16x16x32_bf16 a[220:223],a[120:123],v[120:123],a[220:223]
ds_read_b64_tr_b16 a[120:121],v2 offset:20704
v_add_u32 v25,4096,v6
v_mfma_f32_16x16x32_bf16 a[220:223],a[116:119],v[116:119],a[220:223]
buffer_load_dwordx4 v[232:235],v6,s[28:31],s79 offen sc1
v_mfma_f32_16x16x32_bf16 a[220:223],a[112:115],v[112:115],a[220:223]
s_waitcnt lgkmcnt(3)
ds_read_b64_tr_b16 a[112:113],v2 offset:224
ds_read_b64_tr_b16 a[114:115],v2 offset:5344
v_mfma_f32_16x16x32_bf16 a[224:227],a[128:131],v[64:67],a[224:227]
ds_read_b64_tr_b16 a[116:117],v2 offset:10464
v_mov_b32 v36,0
v_mfma_f32_16x16x32_bf16 a[224:227],a[132:135],v[68:71],a[224:227]
ds_read_b64_tr_b16 a[118:119],v2 offset:15584
v_mov_b32 v37,0
v_mfma_f32_16x16x32_bf16 a[224:227],a[136:139],v[72:75],a[224:227]
ds_read_b64_tr_b16 a[122:123],v2 offset:25824
v_mov_b32 v32,0
v_mfma_f32_16x16x32_bf16 a[224:227],a[140:143],v[76:79],a[224:227]
v_cmp_eq_u32 vcc,1,v18
s_nop 1
v_cndmask_b32 v35,0,v19,vcc
v_mfma_f32_16x16x32_bf16 a[228:231],a[140:143],v[92:95],a[228:231]
buffer_load_dwordx4 v[236:239],v6,s[28:31],s79 offen offset:1024 sc1
v_mfma_f32_16x16x32_bf16 a[228:231],a[136:139],v[88:91],a[228:231]
s_add_u32 s20,s20,s44
s_addc_u32 s21,s21,0
v_mov_b32 v33,0
v_mfma_f32_16x16x32_bf16 a[228:231],a[132:135],v[84:87],a[228:231]
s_mul_i32 s44,s71,320
v_bfe_u32 v40,v6,4,4
v_mul_u32_u24 v40,1280,v40
v_mfma_f32_16x16x32_bf16 a[228:231],a[128:131],v[80:83],a[228:231]
s_waitcnt vmcnt(9)
v_cmp_ne_u32 s[90:91],s68,v26
v_cmp_ne_u32 s[92:93],s69,v27
s_nop 0
s_or_b64 s[90:91],s[90:91],s[92:93]
s_cbranch_scc1 L_fr9_%=
L_fo9_%=:
v_mfma_f32_16x16x32_bf16 a[232:235],a[128:131],v[96:99],a[232:235]
s_and_b32 s21,s21,0xffff
v_lshrrev_b32 v41,8,v6
v_lshl_add_u32 v40,v41,3,v40
v_mfma_f32_16x16x32_bf16 a[232:235],a[132:135],v[100:103],a[232:235]
s_add_u32 s43,s43,s44
v_mfma_f32_16x16x32_bf16 a[232:235],a[136:139],v[104:107],a[232:235]
buffer_load_dwordx4 v[248:251],v6,s[28:31],s79 offen offset:2048 sc1
v_mfma_f32_16x16x32_bf16 a[232:235],a[140:143],v[108:111],a[232:235]
v_mfma_f32_16x16x32_bf16 a[236:239],a[140:143],v[124:127],a[236:239]
ds_read_b64_tr_b16 a[140:141],v1 offset:30976
ds_read_b64_tr_b16 a[142:143],v1 offset:36096
v_mfma_f32_16x16x32_bf16 a[236:239],a[136:139],v[120:123],a[236:239]
ds_read_b64_tr_b16 a[136:137],v1 offset:20736
v_mfma_f32_16x16x32_bf16 a[236:239],a[132:135],v[116:119],a[236:239]
ds_read_b64_tr_b16 a[132:133],v1 offset:10496
v_mfma_f32_16x16x32_bf16 a[236:239],a[128:131],v[112:115],a[236:239]
ds_read_b64_tr_b16 a[128:129],v1 offset:256
v_mfma_f32_16x16x32_bf16 a[240:243],a[144:147],v[64:67],a[240:243]
buffer_load_dwordx4 v[252:255],v6,s[28:31],s79 offen offset:3072 sc1
v_mfma_f32_16x16x32_bf16 a[240:243],a[148:151],v[68:71],a[240:243]
s_add_u32 s79,s76,12288
s_max_i32 s76,s76,s18
ds_read_b64_tr_b16 a[130:131],v1 offset:5376
v_mfma_f32_16x16x32_bf16 a[240:243],a[152:155],v[72:75],a[240:243]
s_max_i32 s79,s79,s101
ds_read_b64_tr_b16 a[134:135],v1 offset:15616
ds_read_b64_tr_b16 a[138:139],v1 offset:25856
v_mfma_f32_16x16x32_bf16 a[240:243],a[156:159],v[76:79],a[240:243]
v_subrev_u32 v1,40960,v1
v_mfma_f32_16x16x32_bf16 a[244:247],a[156:159],v[92:95],a[244:247]
buffer_load_dwordx4 v[128:131],v6,s[28:31],s76 offen sc1
v_mfma_f32_16x16x32_bf16 a[244:247],a[152:155],v[88:91],a[244:247]
v_mfma_f32_16x16x32_bf16 a[244:247],a[148:151],v[84:87],a[244:247]
v_mfma_f32_16x16x32_bf16 a[244:247],a[144:147],v[80:83],a[244:247]
v_mfma_f32_16x16x32_bf16 a[248:251],a[144:147],v[96:99],a[248:251]
buffer_load_dwordx4 v[160:163],v6,s[28:31],s76 offen offset:1024 sc1
v_mfma_f32_16x16x32_bf16 a[248:251],a[148:151],v[100:103],a[248:251]
v_mfma_f32_16x16x32_bf16 a[248:251],a[152:155],v[104:107],a[248:251]
v_mfma_f32_16x16x32_bf16 a[248:251],a[156:159],v[108:111],a[248:251]
v_mfma_f32_16x16x32_bf16 a[252:255],a[156:159],v[124:127],a[252:255]
buffer_load_dwordx4 v[192:195],v6,s[28:31],s76 offen offset:2048 sc1
v_mfma_f32_16x16x32_bf16 a[252:255],a[152:155],v[120:123],a[252:255]
ds_read_b64_tr_b16 a[152:153],v2 offset:20768
ds_read_b64_tr_b16 a[154:155],v2 offset:25888
v_mfma_f32_16x16x32_bf16 a[252:255],a[148:151],v[116:119],a[252:255]
ds_read_b64_tr_b16 a[148:149],v2 offset:10528
v_mfma_f32_16x16x32_bf16 a[252:255],a[144:147],v[112:115],a[252:255]
ds_read_b64_tr_b16 a[144:145],v2 offset:288
v_mfma_f32_16x16x32_bf16 a[0:3],a[160:163],v[64:67],a[0:3]
ds_read_b64_tr_b16 a[146:147],v2 offset:5408
v_mfma_f32_16x16x32_bf16 a[0:3],a[164:167],v[68:71],a[0:3]
buffer_load_dwordx4 v[224:227],v6,s[28:31],s76 offen offset:3072 sc1
v_mfma_f32_16x16x32_bf16 a[0:3],a[168:171],v[72:75],a[0:3]
ds_read_b64_tr_b16 a[150:151],v2 offset:15648
ds_read_b64_tr_b16 a[156:157],v2 offset:31008
v_mfma_f32_16x16x32_bf16 a[0:3],a[172:175],v[76:79],a[0:3]
ds_read_b64_tr_b16 a[158:159],v2 offset:36128
v_subrev_u32 v2,40960,v2
v_mfma_f32_16x16x32_bf16 a[4:7],a[172:175],v[92:95],a[4:7]
v_mfma_f32_16x16x32_bf16 a[4:7],a[168:171],v[88:91],a[4:7]
v_mfma_f32_16x16x32_bf16 a[4:7],a[164:167],v[84:87],a[4:7]
v_mfma_f32_16x16x32_bf16 a[4:7],a[160:163],v[80:83],a[4:7]
buffer_load_dwordx4 v[132:135],v6,s[28:31],s77 offen sc1
v_mfma_f32_16x16x32_bf16 a[8:11],a[160:163],v[96:99],a[8:11]
v_mfma_f32_16x16x32_bf16 a[8:11],a[164:167],v[100:103],a[8:11]
v_mfma_f32_16x16x32_bf16 a[8:11],a[168:171],v[104:107],a[8:11]
v_mfma_f32_16x16x32_bf16 a[8:11],a[172:175],v[108:111],a[8:11]
v_mfma_f32_16x16x32_bf16 a[12:15],a[172:175],v[124:127],a[12:15]
ds_read_b64_tr_b16 a[172:173],v1 offset:30720
ds_read_b64_tr_b16 a[174:175],v1 offset:35840
v_mfma_f32_16x16x32_bf16 a[12:15],a[168:171],v[120:123],a[12:15]
buffer_load_dwordx4 v[164:167],v6,s[28:31],s77 offen offset:1024 sc1
v_mfma_f32_16x16x32_bf16 a[12:15],a[164:167],v[116:119],a[12:15]
ds_read_b64_tr_b16 a[164:165],v1 offset:10240
ds_read_b64_tr_b16 a[166:167],v1 offset:15360
v_mfma_f32_16x16x32_bf16 a[12:15],a[160:163],v[112:115],a[12:15]
ds_read_b64_tr_b16 a[160:161],v1
v_mfma_f32_16x16x32_bf16 a[16:19],a[176:179],v[64:67],a[16:19]
ds_read_b64_tr_b16 a[162:163],v1 offset:5120
v_mfma_f32_16x16x32_bf16 a[16:19],a[180:183],v[68:71],a[16:19]
ds_read_b64_tr_b16 a[168:169],v1 offset:20480
v_mfma_f32_16x16x32_bf16 a[16:19],a[184:187],v[72:75],a[16:19]
ds_read_b64_tr_b16 a[170:171],v1 offset:25600
v_mfma_f32_16x16x32_bf16 a[16:19],a[188:191],v[76:79],a[16:19]
buffer_load_dwordx4 v[196:199],v6,s[28:31],s77 offen offset:2048 sc1
v_mfma_f32_16x16x32_bf16 a[20:23],a[188:191],v[92:95],a[20:23]
v_mfma_f32_16x16x32_bf16 a[20:23],a[184:187],v[88:91],a[20:23]
v_mfma_f32_16x16x32_bf16 a[20:23],a[180:183],v[84:87],a[20:23]
v_mfma_f32_16x16x32_bf16 a[20:23],a[176:179],v[80:83],a[20:23]
v_mfma_f32_16x16x32_bf16 a[24:27],a[176:179],v[96:99],a[24:27]
v_mfma_f32_16x16x32_bf16 a[24:27],a[180:183],v[100:103],a[24:27]
buffer_load_dwordx4 v[228:231],v6,s[28:31],s77 offen offset:3072 sc1
v_mfma_f32_16x16x32_bf16 a[24:27],a[184:187],v[104:107],a[24:27]
v_mfma_f32_16x16x32_bf16 a[24:27],a[188:191],v[108:111],a[24:27]
v_mfma_f32_16x16x32_bf16 a[28:31],a[188:191],v[124:127],a[28:31]
ds_read_b64_tr_b16 a[188:189],v2 offset:30752
ds_read_b64_tr_b16 a[190:191],v2 offset:35872
v_mfma_f32_16x16x32_bf16 a[28:31],a[184:187],v[120:123],a[28:31]
ds_read_b64_tr_b16 a[184:185],v2 offset:20512
v_mfma_f32_16x16x32_bf16 a[28:31],a[180:183],v[116:119],a[28:31]
ds_read_b64_tr_b16 a[180:181],v2 offset:10272
v_mfma_f32_16x16x32_bf16 a[28:31],a[176:179],v[112:115],a[28:31]
buffer_load_dwordx4 v[144:147],v6,s[28:31],s78 offen sc1
v_mfma_f32_16x16x32_bf16 a[32:35],a[96:99],v[64:67],a[32:35]
ds_read_b64_tr_b16 a[176:177],v2 offset:32
ds_read_b64_tr_b16 a[178:179],v2 offset:5152
v_mfma_f32_16x16x32_bf16 a[32:35],a[100:103],v[68:71],a[32:35]
ds_read_b64_tr_b16 a[182:183],v2 offset:15392
v_mfma_f32_16x16x32_bf16 a[32:35],a[104:107],v[72:75],a[32:35]
ds_read_b64_tr_b16 a[186:187],v2 offset:25632
v_mfma_f32_16x16x32_bf16 a[32:35],a[108:111],v[76:79],a[32:35]
v_mfma_f32_16x16x32_bf16 a[36:39],a[108:111],v[92:95],a[36:39]
v_mfma_f32_16x16x32_bf16 a[36:39],a[104:107],v[88:91],a[36:39]
buffer_load_dwordx4 v[176:179],v6,s[28:31],s78 offen offset:1024 sc1
v_mfma_f32_16x16x32_bf16 a[36:39],a[100:103],v[84:87],a[36:39]
v_mfma_f32_16x16x32_bf16 a[36:39],a[96:99],v[80:83],a[36:39]
v_mfma_f32_16x16x32_bf16 a[40:43],a[96:99],v[96:99],a[40:43]
v_mfma_f32_16x16x32_bf16 a[40:43],a[100:103],v[100:103],a[40:43]
v_mfma_f32_16x16x32_bf16 a[40:43],a[104:107],v[104:107],a[40:43]
v_mfma_f32_16x16x32_bf16 a[40:43],a[108:111],v[108:111],a[40:43]
buffer_load_dwordx4 v[208:211],v6,s[28:31],s78 offen offset:2048 sc1
v_mfma_f32_16x16x32_bf16 a[44:47],a[108:111],v[124:127],a[44:47]
ds_read_b64_tr_b16 a[108:109],v1 offset:30784
ds_read_b64_tr_b16 a[110:111],v1 offset:35904
v_mfma_f32_16x16x32_bf16 a[44:47],a[104:107],v[120:123],a[44:47]
ds_read_b64_tr_b16 a[104:105],v1 offset:20544
v_mfma_f32_16x16x32_bf16 a[44:47],a[100:103],v[116:119],a[44:47]
ds_read_b64_tr_b16 a[100:101],v1 offset:10304
v_mfma_f32_16x16x32_bf16 a[44:47],a[96:99],v[112:115],a[44:47]
s_waitcnt lgkmcnt(4)
ds_read_b64_tr_b16 a[96:97],v1 offset:64
v_mfma_f32_16x16x32_bf16 a[48:51],a[112:115],v[64:67],a[48:51]
ds_read_b64_tr_b16 a[98:99],v1 offset:5184
ds_read_b64_tr_b16 a[102:103],v1 offset:15424
)A"
R"A(v_mfma_f32_16x16x32_bf16 a[48:51],a[116:119],v[68:71],a[48:51]
buffer_load_dwordx4 v[240:243],v6,s[28:31],s78 offen offset:3072 sc1
v_mfma_f32_16x16x32_bf16 a[48:51],a[120:123],v[72:75],a[48:51]
ds_read_b64_tr_b16 a[106:107],v1 offset:25664
v_mfma_f32_16x16x32_bf16 a[48:51],a[124:127],v[76:79],a[48:51]
v_mfma_f32_16x16x32_bf16 a[52:55],a[124:127],v[92:95],a[52:55]
v_mfma_f32_16x16x32_bf16 a[52:55],a[120:123],v[88:91],a[52:55]
v_mfma_f32_16x16x32_bf16 a[52:55],a[116:119],v[84:87],a[52:55]
v_mfma_f32_16x16x32_bf16 a[52:55],a[112:115],v[80:83],a[52:55]
buffer_load_dwordx4 v[148:151],v6,s[28:31],s79 offen sc1
v_mfma_f32_16x16x32_bf16 a[56:59],a[112:115],v[96:99],a[56:59]
v_mfma_f32_16x16x32_bf16 a[56:59],a[116:119],v[100:103],a[56:59]
s_cmp_ge_u32 1,s71
s_cselect_b32 s80,5,3
s_mul_i32 s80,s80,163840
v_mfma_f32_16x16x32_bf16 a[56:59],a[120:123],v[104:107],a[56:59]
s_add_u32 s80,s80,s81
s_add_u32 s83,s80,20480
v_mfma_f32_16x16x32_bf16 a[56:59],a[124:127],v[108:111],a[56:59]
v_mfma_f32_16x16x32_bf16 a[60:63],a[124:127],v[124:127],a[60:63]
ds_read_b64_tr_b16 a[124:125],v2 offset:30816
ds_read_b64_tr_b16 a[126:127],v2 offset:35936
v_mfma_f32_16x16x32_bf16 a[60:63],a[120:123],v[120:123],a[60:63]
buffer_load_dwordx4 v[180:183],v6,s[28:31],s79 offen offset:1024 sc1
v_mfma_f32_16x16x32_bf16 a[60:63],a[116:119],v[116:119],a[60:63]
ds_read_b64_tr_b16 a[116:117],v2 offset:10336
ds_read_b64_tr_b16 a[118:119],v2 offset:15456
v_mfma_f32_16x16x32_bf16 a[60:63],a[112:115],v[112:115],a[60:63]
ds_read_b64_tr_b16 a[112:113],v2 offset:96
v_mfma_f32_16x16x32_bf16 a[64:67],a[128:131],v[64:67],a[64:67]
ds_read_b64_tr_b16 a[114:115],v2 offset:5216
v_mfma_f32_16x16x32_bf16 a[64:67],a[132:135],v[68:71],a[64:67]
ds_read_b64_tr_b16 a[120:121],v2 offset:20576
v_mfma_f32_16x16x32_bf16 a[64:67],a[136:139],v[72:75],a[64:67]
ds_read_b64_tr_b16 a[122:123],v2 offset:25696
v_mfma_f32_16x16x32_bf16 a[64:67],a[140:143],v[76:79],a[64:67]
buffer_load_dwordx4 v[212:215],v6,s[28:31],s79 offen offset:2048 sc1
v_mfma_f32_16x16x32_bf16 a[68:71],a[140:143],v[92:95],a[68:71]
v_mfma_f32_16x16x32_bf16 a[68:71],a[136:139],v[88:91],a[68:71]
v_mfma_f32_16x16x32_bf16 a[68:71],a[132:135],v[84:87],a[68:71]
v_mfma_f32_16x16x32_bf16 a[68:71],a[128:131],v[80:83],a[68:71]
v_mfma_f32_16x16x32_bf16 a[72:75],a[128:131],v[96:99],a[72:75]
v_mfma_f32_16x16x32_bf16 a[72:75],a[132:135],v[100:103],a[72:75]
buffer_load_dwordx4 v[244:247],v6,s[28:31],s79 offen offset:3072 sc1
v_mfma_f32_16x16x32_bf16 a[72:75],a[136:139],v[104:107],a[72:75]
v_mfma_f32_16x16x32_bf16 a[72:75],a[140:143],v[108:111],a[72:75]
v_mfma_f32_16x16x32_bf16 a[76:79],a[140:143],v[124:127],a[76:79]
ds_read_b64_tr_b16 a[140:141],v1 offset:30848
ds_read_b64_tr_b16 a[142:143],v1 offset:35968
v_mfma_f32_16x16x32_bf16 a[76:79],a[136:139],v[120:123],a[76:79]
ds_read_b64_tr_b16 a[136:137],v1 offset:20608
v_mfma_f32_16x16x32_bf16 a[76:79],a[132:135],v[116:119],a[76:79]
s_add_u32 m0,s82,40960
ds_read_b64_tr_b16 a[132:133],v1 offset:10368
v_mfma_f32_16x16x32_bf16 a[76:79],a[128:131],v[112:115],a[76:79]
ds_read_b64_tr_b16 a[128:129],v1 offset:128
v_mfma_f32_16x16x32_bf16 a[80:83],a[144:147],v[64:67],a[80:83]
ds_read_b64_tr_b16 a[130:131],v1 offset:5248
ds_read_b64_tr_b16 a[134:135],v1 offset:15488
v_mfma_f32_16x16x32_bf16 a[80:83],a[148:151],v[68:71],a[80:83]
buffer_load_dwordx4 v7,s[72:75],s80 offen lds
v_mfma_f32_16x16x32_bf16 a[80:83],a[152:155],v[72:75],a[80:83]
ds_read_b64_tr_b16 a[138:139],v1 offset:25728
v_mfma_f32_16x16x32_bf16 a[80:83],a[156:159],v[76:79],a[80:83]
v_mfma_f32_16x16x32_bf16 a[84:87],a[156:159],v[92:95],a[84:87]
v_mfma_f32_16x16x32_bf16 a[84:87],a[152:155],v[88:91],a[84:87]
v_mfma_f32_16x16x32_bf16 a[84:87],a[148:151],v[84:87],a[84:87]
buffer_load_dwordx4 v8,s[72:75],s80 offen offset:1024 lds
v_mfma_f32_16x16x32_bf16 a[84:87],a[144:147],v[80:83],a[84:87]
v_mfma_f32_16x16x32_bf16 a[88:91],a[144:147],v[96:99],a[88:91]
v_mfma_f32_16x16x32_bf16 a[88:91],a[148:151],v[100:103],a[88:91]
v_mfma_f32_16x16x32_bf16 a[88:91],a[152:155],v[104:107],a[88:91]
v_mfma_f32_16x16x32_bf16 a[88:91],a[156:159],v[108:111],a[88:91]
buffer_load_dwordx4 v9,s[72:75],s80 offen offset:2048 lds
v_mfma_f32_16x16x32_bf16 a[92:95],a[156:159],v[124:127],a[92:95]
ds_read_b64_tr_b16 a[156:157],v2 offset:30880
ds_read_b64_tr_b16 a[158:159],v2 offset:36000
v_mfma_f32_16x16x32_bf16 a[92:95],a[152:155],v[120:123],a[92:95]
ds_read_b64_tr_b16 a[152:153],v2 offset:20640
v_mfma_f32_16x16x32_bf16 a[92:95],a[148:151],v[116:119],a[92:95]
ds_read_b64_tr_b16 a[148:149],v2 offset:10400
v_mfma_f32_16x16x32_bf16 a[92:95],a[144:147],v[112:115],a[92:95]
ds_read_b64_tr_b16 a[144:145],v2 offset:160
v_mfma_f32_16x16x32_bf16 a[192:195],a[160:163],v[136:139],a[192:195]
buffer_load_dwordx4 v10,s[72:75],s80 offen offset:3072 lds
v_mfma_f32_16x16x32_bf16 a[192:195],a[164:167],v[140:143],a[192:195]
ds_read_b64_tr_b16 a[146:147],v2 offset:5280
ds_read_b64_tr_b16 a[150:151],v2 offset:15520
v_mfma_f32_16x16x32_bf16 a[192:195],a[168:171],v[152:155],a[192:195]
s_add_u32 m0,s82,45056
ds_read_b64_tr_b16 a[154:155],v2 offset:25760
v_mfma_f32_16x16x32_bf16 a[192:195],a[172:175],v[156:159],a[192:195]
s_waitcnt vmcnt(12)
v_mfma_f32_16x16x32_bf16 a[196:199],a[172:175],v[188:191],a[196:199]
v_mfma_f32_16x16x32_bf16 a[196:199],a[168:171],v[184:187],a[196:199]
buffer_load_dwordx4 v11,s[72:75],s80 offen lds
v_mfma_f32_16x16x32_bf16 a[196:199],a[164:167],v[172:175],a[196:199]
v_mfma_f32_16x16x32_bf16 a[196:199],a[160:163],v[168:171],a[196:199]
s_add_u32 m0,s82,46080
v_mfma_f32_16x16x32_bf16 a[200:203],a[160:163],v[200:203],a[200:203]
v_mfma_f32_16x16x32_bf16 a[200:203],a[164:167],v[204:207],a[200:203]
v_mfma_f32_16x16x32_bf16 a[200:203],a[168:171],v[216:219],a[200:203]
buffer_load_dwordx4 v7,s[72:75],s83 offen lds
v_mfma_f32_16x16x32_bf16 a[200:203],a[172:175],v[220:223],a[200:203]
v_mfma_f32_16x16x32_bf16 a[204:207],a[172:175],v[252:255],a[204:207]
ds_read_b64_tr_b16 a[172:173],v1 offset:30912
ds_read_b64_tr_b16 a[174:175],v1 offset:36032
v_mfma_f32_16x16x32_bf16 a[204:207],a[168:171],v[248:251],a[204:207]
ds_read_b64_tr_b16 a[168:169],v1 offset:20672
v_mfma_f32_16x16x32_bf16 a[204:207],a[164:167],v[236:239],a[204:207]
ds_read_b64_tr_b16 a[164:165],v1 offset:10432
v_mfma_f32_16x16x32_bf16 a[204:207],a[160:163],v[232:235],a[204:207]
s_barrier
buffer_load_dwordx4 v8,s[72:75],s83 offen offset:1024 lds
v_mfma_f32_16x16x32_bf16 a[208:211],a[176:179],v[136:139],a[208:211]
s_cmp_ge_u32 1,s71
s_cselect_b32 s76,5,3
s_lshl_b32 s76,s76,16
v_mfma_f32_16x16x32_bf16 a[208:211],a[180:183],v[140:143],a[208:211]
s_add_u32 s77,s76,4096
s_add_u32 s78,s76,8192
s_add_u32 s79,s76,12288
v_mfma_f32_16x16x32_bf16 a[208:211],a[184:187],v[152:155],a[208:211]
s_max_i32 s76,s76,s18
s_max_i32 s77,s77,s19
s_max_i32 s78,s78,s100
v_mfma_f32_16x16x32_bf16 a[208:211],a[188:191],v[156:159],a[208:211]
s_max_i32 s79,s79,s101
buffer_load_dwordx4 v9,s[72:75],s83 offen offset:2048 lds
v_mfma_f32_16x16x32_bf16 a[212:215],a[188:191],v[188:191],a[212:215]
ds_read_b64_tr_b16 a[160:161],v1 offset:192
ds_read_b64_tr_b16 a[162:163],v1 offset:5312
v_mfma_f32_16x16x32_bf16 a[212:215],a[184:187],v[184:187],a[212:215]
ds_read_b64_tr_b16 a[166:167],v1 offset:15552
v_mfma_f32_16x16x32_bf16 a[212:215],a[180:183],v[172:175],a[212:215]
ds_read_b64_tr_b16 a[170:171],v1 offset:25792
v_mfma_f32_16x16x32_bf16 a[212:215],a[176:179],v[168:171],a[212:215]
v_mfma_f32_16x16x32_bf16 a[216:219],a[176:179],v[200:203],a[216:219]
buffer_load_dwordx4 v10,s[72:75],s83 offen offset:3072 lds
v_mfma_f32_16x16x32_bf16 a[216:219],a[180:183],v[204:207],a[216:219]
v_mfma_f32_16x16x32_bf16 a[216:219],a[184:187],v[216:219],a[216:219]
s_add_u32 m0,s82,50176
v_mfma_f32_16x16x32_bf16 a[216:219],a[188:191],v[220:223],a[216:219]
v_mfma_f32_16x16x32_bf16 a[220:223],a[188:191],v[252:255],a[220:223]
ds_read_b64_tr_b16 a[188:189],v2 offset:30944
ds_read_b64_tr_b16 a[190:191],v2 offset:36064
v_mfma_f32_16x16x32_bf16 a[220:223],a[184:187],v[248:251],a[220:223]
buffer_load_dwordx4 v11,s[72:75],s83 offen lds
v_mfma_f32_16x16x32_bf16 a[220:223],a[180:183],v[236:239],a[220:223]
ds_read_b64_tr_b16 a[180:181],v2 offset:10464
ds_read_b64_tr_b16 a[182:183],v2 offset:15584
v_mfma_f32_16x16x32_bf16 a[220:223],a[176:179],v[232:235],a[220:223]
s_waitcnt lgkmcnt(4)
ds_read_b64_tr_b16 a[176:177],v2 offset:224
v_mfma_f32_16x16x32_bf16 a[224:227],a[96:99],v[136:139],a[224:227]
ds_read_b64_tr_b16 a[178:179],v2 offset:5344
v_mfma_f32_16x16x32_bf16 a[224:227],a[100:103],v[140:143],a[224:227]
ds_read_b64_tr_b16 a[184:185],v2 offset:20704
v_mfma_f32_16x16x32_bf16 a[224:227],a[104:107],v[152:155],a[224:227]
ds_read_b64_tr_b16 a[186:187],v2 offset:25824
v_mfma_f32_16x16x32_bf16 a[224:227],a[108:111],v[156:159],a[224:227]
v_mfma_f32_16x16x32_bf16 a[228:231],a[108:111],v[188:191],a[228:231]
v_mfma_f32_16x16x32_bf16 a[228:231],a[104:107],v[184:187],a[228:231]
v_mfma_f32_16x16x32_bf16 a[228:231],a[100:103],v[172:175],a[228:231]
v_mfma_f32_16x16x32_bf16 a[228:231],a[96:99],v[168:171],a[228:231]
buffer_load_dwordx4 v[64:67],v6,s[28:31],s76 offen sc1
v_mfma_f32_16x16x32_bf16 a[232:235],a[96:99],v[200:203],a[232:235]
v_mfma_f32_16x16x32_bf16 a[232:235],a[100:103],v[204:207],a[232:235]
v_mfma_f32_16x16x32_bf16 a[232:235],a[104:107],v[216:219],a[232:235]
v_mfma_f32_16x16x32_bf16 a[232:235],a[108:111],v[220:223],a[232:235]
v_mfma_f32_16x16x32_bf16 a[236:239],a[108:111],v[252:255],a[236:239]
buffer_load_dwordx4 v[68:71],v6,s[28:31],s76 offen offset:1024 sc1
v_mfma_f32_16x16x32_bf16 a[236:239],a[104:107],v[248:251],a[236:239]
ds_read_b64_tr_b16 a[104:105],v1 offset:20736
ds_read_b64_tr_b16 a[106:107],v1 offset:25856
v_mfma_f32_16x16x32_bf16 a[236:239],a[100:103],v[236:239],a[236:239]
ds_read_b64_tr_b16 a[100:101],v1 offset:10496
v_mfma_f32_16x16x32_bf16 a[236:239],a[96:99],v[232:235],a[236:239]
ds_read_b64_tr_b16 a[96:97],v1 offset:256
v_mfma_f32_16x16x32_bf16 a[240:243],a[112:115],v[136:139],a[240:243]
ds_read_b64_tr_b16 a[98:99],v1 offset:5376
v_mfma_f32_16x16x32_bf16 a[240:243],a[116:119],v[140:143],a[240:243]
buffer_load_dwordx4 v[72:75],v6,s[28:31],s76 offen offset:2048 sc1
v_mfma_f32_16x16x32_bf16 a[240:243],a[120:123],v[152:155],a[240:243]
ds_read_b64_tr_b16 a[102:103],v1 offset:15616
ds_read_b64_tr_b16 a[108:109],v1 offset:30976
v_mfma_f32_16x16x32_bf16 a[240:243],a[124:127],v[156:159],a[240:243]
ds_read_b64_tr_b16 a[110:111],v1 offset:36096
v_subrev_u32 v1,81920,v1
v_mfma_f32_16x16x32_bf16 a[244:247],a[124:127],v[188:191],a[244:247]
v_add_u32 v42,40960,v1
v_add_u32 v44,122880,v1
v_mfma_f32_16x16x32_bf16 a[244:247],a[120:123],v[184:187],a[244:247]
v_mfma_f32_16x16x32_bf16 a[244:247],a[116:119],v[172:175],a[244:247]
buffer_load_dwordx4 v[76:79],v6,s[28:31],s76 offen offset:3072 sc1
v_mfma_f32_16x16x32_bf16 a[244:247],a[112:115],v[168:171],a[244:247]
v_mfma_f32_16x16x32_bf16 a[248:251],a[112:115],v[200:203],a[248:251]
v_mfma_f32_16x16x32_bf16 a[248:251],a[116:119],v[204:207],a[248:251]
v_mfma_f32_16x16x32_bf16 a[248:251],a[120:123],v[216:219],a[248:251]
v_mfma_f32_16x16x32_bf16 a[248:251],a[124:127],v[220:223],a[248:251]
buffer_load_dwordx4 v[80:83],v6,s[28:31],s77 offen sc1
v_mfma_f32_16x16x32_bf16 a[252:255],a[124:127],v[252:255],a[252:255]
ds_read_b64_tr_b16 a[124:125],v2 offset:31008
ds_read_b64_tr_b16 a[126:127],v2 offset:36128
v_mfma_f32_16x16x32_bf16 a[252:255],a[120:123],v[248:251],a[252:255]
ds_read_b64_tr_b16 a[120:121],v2 offset:20768
v_mfma_f32_16x16x32_bf16 a[252:255],a[116:119],v[236:239],a[252:255]
ds_read_b64_tr_b16 a[116:117],v2 offset:10528
v_mfma_f32_16x16x32_bf16 a[252:255],a[112:115],v[232:235],a[252:255]
ds_read_b64_tr_b16 a[112:113],v2 offset:288
v_mfma_f32_16x16x32_bf16 a[0:3],a[128:131],v[136:139],a[0:3]
buffer_load_dwordx4 v[84:87],v6,s[28:31],s77 offen offset:1024 sc1
v_mfma_f32_16x16x32_bf16 a[0:3],a[132:135],v[140:143],a[0:3]
ds_read_b64_tr_b16 a[114:115],v2 offset:5408
ds_read_b64_tr_b16 a[118:119],v2 offset:15648
v_mfma_f32_16x16x32_bf16 a[0:3],a[136:139],v[152:155],a[0:3]
ds_read_b64_tr_b16 a[122:123],v2 offset:25888
v_subrev_u32 v2,81920,v2
)A"
R"A(v_mfma_f32_16x16x32_bf16 a[0:3],a[140:143],v[156:159],a[0:3]
v_add_u32 v43,40960,v2
v_add_u32 v45,122880,v2
v_mfma_f32_16x16x32_bf16 a[4:7],a[140:143],v[188:191],a[4:7]
v_mfma_f32_16x16x32_bf16 a[4:7],a[136:139],v[184:187],a[4:7]
buffer_load_dwordx4 v[88:91],v6,s[28:31],s77 offen offset:2048 sc1
v_mfma_f32_16x16x32_bf16 a[4:7],a[132:135],v[172:175],a[4:7]
v_mfma_f32_16x16x32_bf16 a[4:7],a[128:131],v[168:171],a[4:7]
v_mfma_f32_16x16x32_bf16 a[8:11],a[128:131],v[200:203],a[8:11]
v_mfma_f32_16x16x32_bf16 a[8:11],a[132:135],v[204:207],a[8:11]
v_mfma_f32_16x16x32_bf16 a[8:11],a[136:139],v[216:219],a[8:11]
buffer_load_dwordx4 v[92:95],v6,s[28:31],s77 offen offset:3072 sc1
v_mfma_f32_16x16x32_bf16 a[8:11],a[140:143],v[220:223],a[8:11]
v_mfma_f32_16x16x32_bf16 a[12:15],a[140:143],v[252:255],a[12:15]
ds_read_b64_tr_b16 a[140:141],v1 offset:30720
ds_read_b64_tr_b16 a[142:143],v1 offset:35840
v_mfma_f32_16x16x32_bf16 a[12:15],a[136:139],v[248:251],a[12:15]
ds_read_b64_tr_b16 a[136:137],v1 offset:20480
v_mfma_f32_16x16x32_bf16 a[12:15],a[132:135],v[236:239],a[12:15]
ds_read_b64_tr_b16 a[132:133],v1 offset:10240
v_mfma_f32_16x16x32_bf16 a[12:15],a[128:131],v[232:235],a[12:15]
buffer_load_dwordx4 v[96:99],v6,s[28:31],s78 offen sc1
v_mfma_f32_16x16x32_bf16 a[16:19],a[144:147],v[136:139],a[16:19]
ds_read_b64_tr_b16 a[128:129],v1
ds_read_b64_tr_b16 a[130:131],v1 offset:5120
v_mfma_f32_16x16x32_bf16 a[16:19],a[148:151],v[140:143],a[16:19]
ds_read_b64_tr_b16 a[134:135],v1 offset:15360
v_mfma_f32_16x16x32_bf16 a[16:19],a[152:155],v[152:155],a[16:19]
ds_read_b64_tr_b16 a[138:139],v1 offset:25600
v_mfma_f32_16x16x32_bf16 a[16:19],a[156:159],v[156:159],a[16:19]
v_mfma_f32_16x16x32_bf16 a[20:23],a[156:159],v[188:191],a[20:23]
buffer_load_dwordx4 v[100:103],v6,s[28:31],s78 offen offset:1024 sc1
v_mfma_f32_16x16x32_bf16 a[20:23],a[152:155],v[184:187],a[20:23]
v_mfma_f32_16x16x32_bf16 a[20:23],a[148:151],v[172:175],a[20:23]
v_mfma_f32_16x16x32_bf16 a[20:23],a[144:147],v[168:171],a[20:23]
v_mfma_f32_16x16x32_bf16 a[24:27],a[144:147],v[200:203],a[24:27]
v_mfma_f32_16x16x32_bf16 a[24:27],a[148:151],v[204:207],a[24:27]
buffer_load_dwordx4 v[104:107],v6,s[28:31],s78 offen offset:2048 sc1
v_mfma_f32_16x16x32_bf16 a[24:27],a[152:155],v[216:219],a[24:27]
v_mfma_f32_16x16x32_bf16 a[24:27],a[156:159],v[220:223],a[24:27]
v_mfma_f32_16x16x32_bf16 a[28:31],a[156:159],v[252:255],a[28:31]
ds_read_b64_tr_b16 a[156:157],v2 offset:30752
ds_read_b64_tr_b16 a[158:159],v2 offset:35872
v_mfma_f32_16x16x32_bf16 a[28:31],a[152:155],v[248:251],a[28:31]
ds_read_b64_tr_b16 a[152:153],v2 offset:20512
v_mfma_f32_16x16x32_bf16 a[28:31],a[148:151],v[236:239],a[28:31]
buffer_load_dwordx4 v[108:111],v6,s[28:31],s78 offen offset:3072 sc1
v_mfma_f32_16x16x32_bf16 a[28:31],a[144:147],v[232:235],a[28:31]
s_cmp_ge_u32 2,s71
s_cselect_b32 s80,7,5
s_mul_i32 s80,s80,163840
v_mfma_f32_16x16x32_bf16 a[32:35],a[160:163],v[136:139],a[32:35]
s_add_u32 s80,s80,s81
s_add_u32 s83,s80,20480
ds_read_b64_tr_b16 a[144:145],v2 offset:32
v_mfma_f32_16x16x32_bf16 a[32:35],a[164:167],v[140:143],a[32:35]
ds_read_b64_tr_b16 a[146:147],v2 offset:5152
ds_read_b64_tr_b16 a[148:149],v2 offset:10272
v_mfma_f32_16x16x32_bf16 a[32:35],a[168:171],v[152:155],a[32:35]
ds_read_b64_tr_b16 a[150:151],v2 offset:15392
v_mfma_f32_16x16x32_bf16 a[32:35],a[172:175],v[156:159],a[32:35]
buffer_load_dwordx4 v[112:115],v6,s[28:31],s79 offen sc1
v_mfma_f32_16x16x32_bf16 a[36:39],a[172:175],v[188:191],a[36:39]
ds_read_b64_tr_b16 a[154:155],v2 offset:25632
v_mfma_f32_16x16x32_bf16 a[36:39],a[168:171],v[184:187],a[36:39]
v_mfma_f32_16x16x32_bf16 a[36:39],a[164:167],v[172:175],a[36:39]
v_mfma_f32_16x16x32_bf16 a[36:39],a[160:163],v[168:171],a[36:39]
v_mfma_f32_16x16x32_bf16 a[40:43],a[160:163],v[200:203],a[40:43]
buffer_load_dwordx4 v[116:119],v6,s[28:31],s79 offen offset:1024 sc1
v_mfma_f32_16x16x32_bf16 a[40:43],a[164:167],v[204:207],a[40:43]
v_mfma_f32_16x16x32_bf16 a[40:43],a[168:171],v[216:219],a[40:43]
v_mfma_f32_16x16x32_bf16 a[40:43],a[172:175],v[220:223],a[40:43]
v_mfma_f32_16x16x32_bf16 a[44:47],a[172:175],v[252:255],a[44:47]
ds_read_b64_tr_b16 a[172:173],v1 offset:30784
ds_read_b64_tr_b16 a[174:175],v1 offset:35904
v_mfma_f32_16x16x32_bf16 a[44:47],a[168:171],v[248:251],a[44:47]
buffer_load_dwordx4 v[120:123],v6,s[28:31],s79 offen offset:2048 sc1
v_mfma_f32_16x16x32_bf16 a[44:47],a[164:167],v[236:239],a[44:47]
ds_read_b64_tr_b16 a[164:165],v1 offset:10304
ds_read_b64_tr_b16 a[166:167],v1 offset:15424
v_mfma_f32_16x16x32_bf16 a[44:47],a[160:163],v[232:235],a[44:47]
s_waitcnt lgkmcnt(4)
ds_read_b64_tr_b16 a[160:161],v1 offset:64
v_mfma_f32_16x16x32_bf16 a[48:51],a[176:179],v[136:139],a[48:51]
ds_read_b64_tr_b16 a[162:163],v1 offset:5184
v_mfma_f32_16x16x32_bf16 a[48:51],a[180:183],v[140:143],a[48:51]
ds_read_b64_tr_b16 a[168:169],v1 offset:20544
v_mfma_f32_16x16x32_bf16 a[48:51],a[184:187],v[152:155],a[48:51]
buffer_load_dwordx4 v[124:127],v6,s[28:31],s79 offen offset:3072 sc1
v_mfma_f32_16x16x32_bf16 a[48:51],a[188:191],v[156:159],a[48:51]
ds_read_b64_tr_b16 a[170:171],v1 offset:25664
v_mfma_f32_16x16x32_bf16 a[52:55],a[188:191],v[188:191],a[52:55]
s_add_u32 m0,s82,122880
v_mfma_f32_16x16x32_bf16 a[52:55],a[184:187],v[184:187],a[52:55]
v_mfma_f32_16x16x32_bf16 a[52:55],a[180:183],v[172:175],a[52:55]
v_mfma_f32_16x16x32_bf16 a[52:55],a[176:179],v[168:171],a[52:55]
buffer_load_dwordx4 v7,s[72:75],s80 offen lds
v_mfma_f32_16x16x32_bf16 a[56:59],a[176:179],v[200:203],a[56:59]
v_mfma_f32_16x16x32_bf16 a[56:59],a[180:183],v[204:207],a[56:59]
v_mfma_f32_16x16x32_bf16 a[56:59],a[184:187],v[216:219],a[56:59]
v_mfma_f32_16x16x32_bf16 a[56:59],a[188:191],v[220:223],a[56:59]
v_mfma_f32_16x16x32_bf16 a[60:63],a[188:191],v[252:255],a[60:63]
buffer_load_dwordx4 v8,s[72:75],s80 offen offset:1024 lds
v_mfma_f32_16x16x32_bf16 a[60:63],a[184:187],v[248:251],a[60:63]
ds_read_b64_tr_b16 a[184:185],v2 offset:20576
ds_read_b64_tr_b16 a[186:187],v2 offset:25696
v_mfma_f32_16x16x32_bf16 a[60:63],a[180:183],v[236:239],a[60:63]
ds_read_b64_tr_b16 a[180:181],v2 offset:10336
v_mfma_f32_16x16x32_bf16 a[60:63],a[176:179],v[232:235],a[60:63]
ds_read_b64_tr_b16 a[176:177],v2 offset:96
v_mfma_f32_16x16x32_bf16 a[64:67],a[96:99],v[136:139],a[64:67]
ds_read_b64_tr_b16 a[178:179],v2 offset:5216
v_mfma_f32_16x16x32_bf16 a[64:67],a[100:103],v[140:143],a[64:67]
buffer_load_dwordx4 v9,s[72:75],s80 offen offset:2048 lds
v_mfma_f32_16x16x32_bf16 a[64:67],a[104:107],v[152:155],a[64:67]
ds_read_b64_tr_b16 a[182:183],v2 offset:15456
ds_read_b64_tr_b16 a[188:189],v2 offset:30816
v_mfma_f32_16x16x32_bf16 a[64:67],a[108:111],v[156:159],a[64:67]
ds_read_b64_tr_b16 a[190:191],v2 offset:35936
v_mfma_f32_16x16x32_bf16 a[68:71],a[108:111],v[188:191],a[68:71]
v_mfma_f32_16x16x32_bf16 a[68:71],a[104:107],v[184:187],a[68:71]
v_mfma_f32_16x16x32_bf16 a[68:71],a[100:103],v[172:175],a[68:71]
buffer_load_dwordx4 v10,s[72:75],s80 offen offset:3072 lds
v_mfma_f32_16x16x32_bf16 a[68:71],a[96:99],v[168:171],a[68:71]
v_mfma_f32_16x16x32_bf16 a[72:75],a[96:99],v[200:203],a[72:75]
s_add_u32 m0,s82,126976
v_mfma_f32_16x16x32_bf16 a[72:75],a[100:103],v[204:207],a[72:75]
v_mfma_f32_16x16x32_bf16 a[72:75],a[104:107],v[216:219],a[72:75]
s_cmp_ge_u32 2,s71
s_cselect_b32 s76,7,5
s_lshl_b32 s76,s76,16
v_mfma_f32_16x16x32_bf16 a[72:75],a[108:111],v[220:223],a[72:75]
s_add_u32 s77,s76,4096
buffer_load_dwordx4 v11,s[72:75],s80 offen lds
v_mfma_f32_16x16x32_bf16 a[76:79],a[108:111],v[252:255],a[76:79]
s_add_u32 s78,s76,8192
s_add_u32 s79,s76,12288
s_max_i32 s76,s76,s18
v_mfma_f32_16x16x32_bf16 a[76:79],a[104:107],v[248:251],a[76:79]
s_add_u32 m0,s82,128000
s_max_i32 s77,s77,s19
ds_read_b64_tr_b16 a[104:105],v1 offset:20608
v_mfma_f32_16x16x32_bf16 a[76:79],a[100:103],v[236:239],a[76:79]
s_max_i32 s78,s78,s100
ds_read_b64_tr_b16 a[100:101],v1 offset:10368
ds_read_b64_tr_b16 a[102:103],v1 offset:15488
v_mfma_f32_16x16x32_bf16 a[76:79],a[96:99],v[232:235],a[76:79]
s_max_i32 s79,s79,s101
ds_read_b64_tr_b16 a[96:97],v1 offset:128
v_mfma_f32_16x16x32_bf16 a[80:83],a[112:115],v[136:139],a[80:83]
buffer_load_dwordx4 v7,s[72:75],s83 offen lds
v_mfma_f32_16x16x32_bf16 a[80:83],a[116:119],v[140:143],a[80:83]
ds_read_b64_tr_b16 a[98:99],v1 offset:5248
ds_read_b64_tr_b16 a[106:107],v1 offset:25728
v_mfma_f32_16x16x32_bf16 a[80:83],a[120:123],v[152:155],a[80:83]
ds_read_b64_tr_b16 a[108:109],v1 offset:30848
v_mfma_f32_16x16x32_bf16 a[80:83],a[124:127],v[156:159],a[80:83]
ds_read_b64_tr_b16 a[110:111],v1 offset:35968
v_mfma_f32_16x16x32_bf16 a[84:87],a[124:127],v[188:191],a[84:87]
v_mfma_f32_16x16x32_bf16 a[84:87],a[120:123],v[184:187],a[84:87]
buffer_load_dwordx4 v8,s[72:75],s83 offen offset:1024 lds
v_mfma_f32_16x16x32_bf16 a[84:87],a[116:119],v[172:175],a[84:87]
v_mfma_f32_16x16x32_bf16 a[84:87],a[112:115],v[168:171],a[84:87]
v_mfma_f32_16x16x32_bf16 a[88:91],a[112:115],v[200:203],a[88:91]
v_mfma_f32_16x16x32_bf16 a[88:91],a[116:119],v[204:207],a[88:91]
v_mfma_f32_16x16x32_bf16 a[88:91],a[120:123],v[216:219],a[88:91]
buffer_load_dwordx4 v9,s[72:75],s83 offen offset:2048 lds
v_mfma_f32_16x16x32_bf16 a[88:91],a[124:127],v[220:223],a[88:91]
v_mfma_f32_16x16x32_bf16 a[92:95],a[124:127],v[252:255],a[92:95]
ds_read_b64_tr_b16 a[124:125],v2 offset:30880
ds_read_b64_tr_b16 a[126:127],v2 offset:36000
v_mfma_f32_16x16x32_bf16 a[92:95],a[120:123],v[248:251],a[92:95]
ds_read_b64_tr_b16 a[120:121],v2 offset:20640
v_mfma_f32_16x16x32_bf16 a[92:95],a[116:119],v[236:239],a[92:95]
ds_read_b64_tr_b16 a[116:117],v2 offset:10400
v_mfma_f32_16x16x32_bf16 a[92:95],a[112:115],v[232:235],a[92:95]
buffer_load_dwordx4 v10,s[72:75],s83 offen offset:3072 lds
v_mfma_f32_16x16x32_bf16 a[192:195],a[128:131],v[128:131],a[192:195]
ds_read_b64_tr_b16 a[112:113],v2 offset:160
ds_read_b64_tr_b16 a[114:115],v2 offset:5280
v_mfma_f32_16x16x32_bf16 a[192:195],a[132:135],v[160:163],a[192:195]
s_add_u32 m0,s82,132096
ds_read_b64_tr_b16 a[118:119],v2 offset:15520
v_mfma_f32_16x16x32_bf16 a[192:195],a[136:139],v[192:195],a[192:195]
ds_read_b64_tr_b16 a[122:123],v2 offset:25760
v_mfma_f32_16x16x32_bf16 a[192:195],a[140:143],v[224:227],a[192:195]
buffer_load_dwordx4 v[136:139],v6,s[28:31],s76 offen sc1
v_mfma_f32_16x16x32_bf16 a[196:199],a[140:143],v[228:231],a[196:199]
v_mfma_f32_16x16x32_bf16 a[196:199],a[136:139],v[196:199],a[196:199]
v_mfma_f32_16x16x32_bf16 a[196:199],a[132:135],v[164:167],a[196:199]
v_mfma_f32_16x16x32_bf16 a[196:199],a[128:131],v[132:135],a[196:199]
s_waitcnt vmcnt(14)
buffer_load_dwordx4 v11,s[72:75],s83 offen lds
v_mfma_f32_16x16x32_bf16 a[200:203],a[128:131],v[144:147],a[200:203]
ds_read_b64_tr_b16 a[128:129],v1 offset:192
ds_read_b64_tr_b16 a[130:131],v1 offset:5312
v_mfma_f32_16x16x32_bf16 a[200:203],a[132:135],v[176:179],a[200:203]
ds_read_b64_tr_b16 a[132:133],v1 offset:10432
v_mfma_f32_16x16x32_bf16 a[200:203],a[136:139],v[208:211],a[200:203]
ds_read_b64_tr_b16 a[134:135],v1 offset:15552
v_mfma_f32_16x16x32_bf16 a[200:203],a[140:143],v[240:243],a[200:203]
buffer_load_dwordx4 v[140:143],v6,s[28:31],s76 offen offset:1024 sc1
v_mfma_f32_16x16x32_bf16 a[208:211],a[144:147],v[128:131],a[208:211]
ds_read_b64_tr_b16 a[136:137],v1 offset:20672
ds_read_b64_tr_b16 a[138:139],v1 offset:25792
v_mfma_f32_16x16x32_bf16 a[208:211],a[148:151],v[160:163],a[208:211]
ds_read_b64_tr_b16 a[140:141],v1 offset:30912
v_mfma_f32_16x16x32_bf16 a[208:211],a[152:155],v[192:195],a[208:211]
ds_read_b64_tr_b16 a[142:143],v1 offset:36032
v_mfma_f32_16x16x32_bf16 a[208:211],a[156:159],v[224:227],a[208:211]
buffer_load_dwordx4 v[152:155],v6,s[28:31],s76 offen offset:2048 sc1
v_mfma_f32_16x16x32_bf16 a[212:215],a[156:159],v[228:231],a[212:215]
v_mfma_f32_16x16x32_bf16 a[212:215],a[152:155],v[196:199],a[212:215]
v_mfma_f32_16x16x32_bf16 a[212:215],a[148:151],v[164:167],a[212:215]
v_mfma_f32_16x16x32_bf16 a[212:215],a[144:147],v[132:135],a[212:215]
buffer_load_dwordx4 v[156:159],v6,s[28:31],s76 offen offset:3072 sc1
v_mfma_f32_16x16x32_bf16 a[216:219],a[144:147],v[144:147],a[216:219]
s_mul_i32 s76,s70,12
ds_read_b64_tr_b16 a[144:145],v2 offset:224
)A"
R"A(ds_read_b64_tr_b16 a[146:147],v2 offset:5344
v_mfma_f32_16x16x32_bf16 a[216:219],a[148:151],v[176:179],a[216:219]
s_add_u32 s76,s76,0
s_add_u32 s76,s76,s40
ds_read_b64_tr_b16 a[148:149],v2 offset:10464
v_mfma_f32_16x16x32_bf16 a[216:219],a[152:155],v[208:211],a[216:219]
s_mul_i32 s76,s76,5120
s_max_i32 s76,s76,s101
ds_read_b64_tr_b16 a[150:151],v2 offset:15584
v_mfma_f32_16x16x32_bf16 a[216:219],a[156:159],v[240:243],a[216:219]
s_waitcnt lgkmcnt(4)
buffer_load_dwordx4 v[168:171],v6,s[28:31],s77 offen sc1
v_mfma_f32_16x16x32_bf16 a[224:227],a[160:163],v[128:131],a[224:227]
ds_read_b64_tr_b16 a[152:153],v2 offset:20704
ds_read_b64_tr_b16 a[154:155],v2 offset:25824
v_mfma_f32_16x16x32_bf16 a[224:227],a[164:167],v[160:163],a[224:227]
ds_read_b64_tr_b16 a[156:157],v2 offset:30944
v_mfma_f32_16x16x32_bf16 a[224:227],a[168:171],v[192:195],a[224:227]
ds_read_b64_tr_b16 a[158:159],v2 offset:36064
v_mfma_f32_16x16x32_bf16 a[224:227],a[172:175],v[224:227],a[224:227]
buffer_load_dwordx4 v[172:175],v6,s[28:31],s77 offen offset:1024 sc1
v_mfma_f32_16x16x32_bf16 a[228:231],a[172:175],v[228:231],a[228:231]
v_mfma_f32_16x16x32_bf16 a[228:231],a[168:171],v[196:199],a[228:231]
v_mfma_f32_16x16x32_bf16 a[228:231],a[164:167],v[164:167],a[228:231]
v_mfma_f32_16x16x32_bf16 a[228:231],a[160:163],v[132:135],a[228:231]
v_mfma_f32_16x16x32_bf16 a[232:235],a[160:163],v[144:147],a[232:235]
buffer_load_dwordx4 v[184:187],v6,s[28:31],s77 offen offset:2048 sc1
v_mfma_f32_16x16x32_bf16 a[232:235],a[164:167],v[176:179],a[232:235]
ds_read_b64_tr_b16 a[160:161],v1 offset:256
ds_read_b64_tr_b16 a[162:163],v1 offset:5376
v_mfma_f32_16x16x32_bf16 a[232:235],a[168:171],v[208:211],a[232:235]
ds_read_b64_tr_b16 a[164:165],v1 offset:10496
v_mfma_f32_16x16x32_bf16 a[232:235],a[172:175],v[240:243],a[232:235]
ds_read_b64_tr_b16 a[166:167],v1 offset:15616
v_mfma_f32_16x16x32_bf16 a[240:243],a[176:179],v[128:131],a[240:243]
ds_read_b64_tr_b16 a[168:169],v1 offset:20736
v_mfma_f32_16x16x32_bf16 a[240:243],a[180:183],v[160:163],a[240:243]
buffer_load_dwordx4 v[188:191],v6,s[28:31],s77 offen offset:3072 sc1
v_mfma_f32_16x16x32_bf16 a[240:243],a[184:187],v[192:195],a[240:243]
s_mul_i32 s77,s70,12
ds_read_b64_tr_b16 a[170:171],v1 offset:25856
ds_read_b64_tr_b16 a[172:173],v1 offset:30976
v_mfma_f32_16x16x32_bf16 a[240:243],a[188:191],v[224:227],a[240:243]
s_add_u32 s77,s77,4
s_add_u32 s77,s77,s40
ds_read_b64_tr_b16 a[174:175],v1 offset:36096
v_mfma_f32_16x16x32_bf16 a[244:247],a[188:191],v[228:231],a[244:247]
s_mul_i32 s77,s77,5120
s_max_i32 s77,s77,s101
v_mfma_f32_16x16x32_bf16 a[244:247],a[184:187],v[196:199],a[244:247]
v_mfma_f32_16x16x32_bf16 a[244:247],a[180:183],v[164:167],a[244:247]
buffer_load_dwordx4 v[200:203],v6,s[28:31],s78 offen sc1
v_mfma_f32_16x16x32_bf16 a[244:247],a[176:179],v[132:135],a[244:247]
v_mfma_f32_16x16x32_bf16 a[248:251],a[176:179],v[144:147],a[248:251]
ds_read_b64_tr_b16 a[176:177],v2 offset:288
ds_read_b64_tr_b16 a[178:179],v2 offset:5408
v_mfma_f32_16x16x32_bf16 a[248:251],a[180:183],v[176:179],a[248:251]
ds_read_b64_tr_b16 a[180:181],v2 offset:10528
v_mfma_f32_16x16x32_bf16 a[248:251],a[184:187],v[208:211],a[248:251]
ds_read_b64_tr_b16 a[182:183],v2 offset:15648
v_mfma_f32_16x16x32_bf16 a[248:251],a[188:191],v[240:243],a[248:251]
buffer_load_dwordx4 v[204:207],v6,s[28:31],s78 offen offset:1024 sc1
v_mfma_f32_16x16x32_bf16 a[0:3],a[96:99],v[128:131],a[0:3]
ds_read_b64_tr_b16 a[184:185],v2 offset:20768
ds_read_b64_tr_b16 a[186:187],v2 offset:25888
v_mfma_f32_16x16x32_bf16 a[0:3],a[100:103],v[160:163],a[0:3]
ds_read_b64_tr_b16 a[188:189],v2 offset:31008
v_mfma_f32_16x16x32_bf16 a[0:3],a[104:107],v[192:195],a[0:3]
ds_read_b64_tr_b16 a[190:191],v2 offset:36128
v_mfma_f32_16x16x32_bf16 a[0:3],a[108:111],v[224:227],a[0:3]
v_mfma_f32_16x16x32_bf16 a[4:7],a[108:111],v[228:231],a[4:7]
buffer_load_dwordx4 v[216:219],v6,s[28:31],s78 offen offset:2048 sc1
v_mfma_f32_16x16x32_bf16 a[4:7],a[104:107],v[196:199],a[4:7]
v_mfma_f32_16x16x32_bf16 a[4:7],a[100:103],v[164:167],a[4:7]
v_mfma_f32_16x16x32_bf16 a[4:7],a[96:99],v[132:135],a[4:7]
v_mfma_f32_16x16x32_bf16 a[8:11],a[96:99],v[144:147],a[8:11]
v_mfma_f32_16x16x32_bf16 a[8:11],a[100:103],v[176:179],a[8:11]
buffer_load_dwordx4 v[220:223],v6,s[28:31],s78 offen offset:3072 sc1
v_mfma_f32_16x16x32_bf16 a[8:11],a[104:107],v[208:211],a[8:11]
s_mul_i32 s78,s70,12
s_add_u32 s78,s78,8
s_add_u32 s78,s78,s40
v_mfma_f32_16x16x32_bf16 a[8:11],a[108:111],v[240:243],a[8:11]
s_mul_i32 s78,s78,5120
s_max_i32 s78,s78,s101
v_mfma_f32_16x16x32_bf16 a[16:19],a[112:115],v[128:131],a[16:19]
v_mfma_f32_16x16x32_bf16 a[16:19],a[116:119],v[160:163],a[16:19]
v_mfma_f32_16x16x32_bf16 a[16:19],a[120:123],v[192:195],a[16:19]
buffer_load_dwordx4 v[232:235],v6,s[28:31],s79 offen sc1
v_mfma_f32_16x16x32_bf16 a[16:19],a[124:127],v[224:227],a[16:19]
v_mfma_f32_16x16x32_bf16 a[20:23],a[124:127],v[228:231],a[20:23]
v_mfma_f32_16x16x32_bf16 a[20:23],a[120:123],v[196:199],a[20:23]
v_mfma_f32_16x16x32_bf16 a[20:23],a[116:119],v[164:167],a[20:23]
v_mfma_f32_16x16x32_bf16 a[20:23],a[112:115],v[132:135],a[20:23]
buffer_load_dwordx4 v[236:239],v6,s[28:31],s79 offen offset:1024 sc1
v_mfma_f32_16x16x32_bf16 a[24:27],a[112:115],v[144:147],a[24:27]
v_mfma_f32_16x16x32_bf16 a[24:27],a[116:119],v[176:179],a[24:27]
v_mfma_f32_16x16x32_bf16 a[24:27],a[120:123],v[208:211],a[24:27]
v_mfma_f32_16x16x32_bf16 a[24:27],a[124:127],v[240:243],a[24:27]
v_mfma_f32_16x16x32_bf16 a[32:35],a[128:131],v[128:131],a[32:35]
buffer_load_dwordx4 v[248:251],v6,s[28:31],s79 offen offset:2048 sc1
v_mfma_f32_16x16x32_bf16 a[32:35],a[132:135],v[160:163],a[32:35]
v_mfma_f32_16x16x32_bf16 a[32:35],a[136:139],v[192:195],a[32:35]
v_mfma_f32_16x16x32_bf16 a[32:35],a[140:143],v[224:227],a[32:35]
v_mfma_f32_16x16x32_bf16 a[36:39],a[140:143],v[228:231],a[36:39]
v_mfma_f32_16x16x32_bf16 a[36:39],a[136:139],v[196:199],a[36:39]
buffer_load_dwordx4 v[252:255],v6,s[28:31],s79 offen offset:3072 sc1
v_mfma_f32_16x16x32_bf16 a[36:39],a[132:135],v[164:167],a[36:39]
v_mfma_f32_16x16x32_bf16 a[36:39],a[128:131],v[132:135],a[36:39]
v_mfma_f32_16x16x32_bf16 a[40:43],a[128:131],v[144:147],a[40:43]
v_mfma_f32_16x16x32_bf16 a[40:43],a[132:135],v[176:179],a[40:43]
v_mfma_f32_16x16x32_bf16 a[40:43],a[136:139],v[208:211],a[40:43]
s_waitcnt vmcnt(15)
v_mfma_f32_16x16x32_bf16 a[40:43],a[140:143],v[240:243],a[40:43]
s_barrier
ds_read_b64_tr_b16 a[96:97],v42
ds_read_b64_tr_b16 a[98:99],v42 offset:5120
s_waitcnt lgkmcnt(2)
v_mfma_f32_16x16x32_bf16 a[48:51],a[144:147],v[128:131],a[48:51]
ds_read_b64_tr_b16 a[100:101],v42 offset:10240
v_mfma_f32_16x16x32_bf16 a[48:51],a[148:151],v[160:163],a[48:51]
ds_read_b64_tr_b16 a[102:103],v42 offset:15360
v_mfma_f32_16x16x32_bf16 a[48:51],a[152:155],v[192:195],a[48:51]
ds_read_b64_tr_b16 a[104:105],v42 offset:20480
v_mfma_f32_16x16x32_bf16 a[48:51],a[156:159],v[224:227],a[48:51]
ds_read_b64_tr_b16 a[106:107],v42 offset:25600
ds_read_b64_tr_b16 a[108:109],v42 offset:30720
v_mfma_f32_16x16x32_bf16 a[52:55],a[156:159],v[228:231],a[52:55]
ds_read_b64_tr_b16 a[110:111],v42 offset:35840
v_mfma_f32_16x16x32_bf16 a[52:55],a[152:155],v[196:199],a[52:55]
ds_read_b64_tr_b16 a[112:113],v44
v_mfma_f32_16x16x32_bf16 a[52:55],a[148:151],v[164:167],a[52:55]
ds_read_b64_tr_b16 a[114:115],v44 offset:5120
v_mfma_f32_16x16x32_bf16 a[52:55],a[144:147],v[132:135],a[52:55]
ds_read_b64_tr_b16 a[116:117],v44 offset:10240
ds_read_b64_tr_b16 a[118:119],v44 offset:15360
v_mfma_f32_16x16x32_bf16 a[56:59],a[144:147],v[144:147],a[56:59]
ds_read_b64_tr_b16 a[120:121],v44 offset:20480
v_mfma_f32_16x16x32_bf16 a[56:59],a[148:151],v[176:179],a[56:59]
ds_read_b64_tr_b16 a[122:123],v44 offset:25600
v_mfma_f32_16x16x32_bf16 a[56:59],a[152:155],v[208:211],a[56:59]
ds_read_b64_tr_b16 a[124:125],v44 offset:30720
v_mfma_f32_16x16x32_bf16 a[56:59],a[156:159],v[240:243],a[56:59]
ds_read_b64_tr_b16 a[126:127],v44 offset:35840
ds_read_b64_tr_b16 a[128:129],v43 offset:32
v_mfma_f32_16x16x32_bf16 a[64:67],a[160:163],v[128:131],a[64:67]
ds_read_b64_tr_b16 a[130:131],v43 offset:5152
v_mfma_f32_16x16x32_bf16 a[64:67],a[164:167],v[160:163],a[64:67]
ds_read_b64_tr_b16 a[132:133],v43 offset:10272
v_mfma_f32_16x16x32_bf16 a[64:67],a[168:171],v[192:195],a[64:67]
ds_read_b64_tr_b16 a[134:135],v43 offset:15392
v_mfma_f32_16x16x32_bf16 a[64:67],a[172:175],v[224:227],a[64:67]
ds_read_b64_tr_b16 a[136:137],v43 offset:20512
ds_read_b64_tr_b16 a[138:139],v43 offset:25632
v_mfma_f32_16x16x32_bf16 a[68:71],a[172:175],v[228:231],a[68:71]
ds_read_b64_tr_b16 a[140:141],v43 offset:30752
v_mfma_f32_16x16x32_bf16 a[68:71],a[168:171],v[196:199],a[68:71]
ds_read_b64_tr_b16 a[142:143],v43 offset:35872
v_mfma_f32_16x16x32_bf16 a[68:71],a[164:167],v[164:167],a[68:71]
ds_read_b64_tr_b16 a[144:145],v45 offset:32
v_mfma_f32_16x16x32_bf16 a[68:71],a[160:163],v[132:135],a[68:71]
ds_read_b64_tr_b16 a[146:147],v45 offset:5152
ds_read_b64_tr_b16 a[148:149],v45 offset:10272
v_mfma_f32_16x16x32_bf16 a[72:75],a[160:163],v[144:147],a[72:75]
ds_read_b64_tr_b16 a[150:151],v45 offset:15392
v_mfma_f32_16x16x32_bf16 a[72:75],a[164:167],v[176:179],a[72:75]
ds_read_b64_tr_b16 a[152:153],v45 offset:20512
v_mfma_f32_16x16x32_bf16 a[72:75],a[168:171],v[208:211],a[72:75]
ds_read_b64_tr_b16 a[154:155],v45 offset:25632
v_mfma_f32_16x16x32_bf16 a[72:75],a[172:175],v[240:243],a[72:75]
ds_read_b64_tr_b16 a[156:157],v45 offset:30752
ds_read_b64_tr_b16 a[158:159],v45 offset:35872
v_mfma_f32_16x16x32_bf16 a[80:83],a[176:179],v[128:131],a[80:83]
ds_read_b64_tr_b16 a[160:161],v42 offset:64
v_mfma_f32_16x16x32_bf16 a[80:83],a[180:183],v[160:163],a[80:83]
ds_read_b64_tr_b16 a[162:163],v42 offset:5184
v_mfma_f32_16x16x32_bf16 a[80:83],a[184:187],v[192:195],a[80:83]
ds_read_b64_tr_b16 a[164:165],v42 offset:10304
v_mfma_f32_16x16x32_bf16 a[80:83],a[188:191],v[224:227],a[80:83]
ds_read_b64_tr_b16 a[166:167],v42 offset:15424
ds_read_b64_tr_b16 a[168:169],v42 offset:20544
v_mfma_f32_16x16x32_bf16 a[84:87],a[188:191],v[228:231],a[84:87]
ds_read_b64_tr_b16 a[170:171],v42 offset:25664
v_mfma_f32_16x16x32_bf16 a[84:87],a[184:187],v[196:199],a[84:87]
ds_read_b64_tr_b16 a[172:173],v42 offset:30784
v_mfma_f32_16x16x32_bf16 a[84:87],a[180:183],v[164:167],a[84:87]
ds_read_b64_tr_b16 a[174:175],v42 offset:35904
v_mfma_f32_16x16x32_bf16 a[84:87],a[176:179],v[132:135],a[84:87]
v_mfma_f32_16x16x32_bf16 a[88:91],a[176:179],v[144:147],a[88:91]
ds_read_b64_tr_b16 a[176:177],v44 offset:64
ds_read_b64_tr_b16 a[178:179],v44 offset:5184
v_mfma_f32_16x16x32_bf16 a[88:91],a[180:183],v[176:179],a[88:91]
ds_read_b64_tr_b16 a[180:181],v44 offset:10304
v_mfma_f32_16x16x32_bf16 a[88:91],a[184:187],v[208:211],a[88:91]
ds_read_b64_tr_b16 a[182:183],v44 offset:15424
v_mfma_f32_16x16x32_bf16 a[88:91],a[188:191],v[240:243],a[88:91]
s_waitcnt lgkmcnt(7)
ds_read_b64_tr_b16 a[184:185],v44 offset:20544
v_mfma_f32_16x16x32_bf16 a[192:195],a[96:99],v[64:67],a[192:195]
ds_read_b64_tr_b16 a[186:187],v44 offset:25664
ds_read_b64_tr_b16 a[188:189],v44 offset:30784
v_mfma_f32_16x16x32_bf16 a[192:195],a[100:103],v[68:71],a[192:195]
ds_read_b64_tr_b16 a[190:191],v44 offset:35904
v_mfma_f32_16x16x32_bf16 a[192:195],a[104:107],v[72:75],a[192:195]
v_mfma_f32_16x16x32_bf16 a[192:195],a[108:111],v[76:79],a[192:195]
v_mfma_f32_16x16x32_bf16 a[196:199],a[108:111],v[92:95],a[196:199]
v_mfma_f32_16x16x32_bf16 a[196:199],a[104:107],v[88:91],a[196:199]
v_mfma_f32_16x16x32_bf16 a[196:199],a[100:103],v[84:87],a[196:199]
v_mfma_f32_16x16x32_bf16 a[196:199],a[96:99],v[80:83],a[196:199]
v_mfma_f32_16x16x32_bf16 a[200:203],a[96:99],v[96:99],a[200:203]
ds_read_b64_tr_b16 a[96:97],v43 offset:96
ds_read_b64_tr_b16 a[98:99],v43 offset:5216
v_mfma_f32_16x16x32_bf16 a[200:203],a[100:103],v[100:103],a[200:203]
ds_read_b64_tr_b16 a[100:101],v43 offset:10336
v_mfma_f32_16x16x32_bf16 a[200:203],a[104:107],v[104:107],a[200:203]
ds_read_b64_tr_b16 a[102:103],v43 offset:15456
v_mfma_f32_16x16x32_bf16 a[200:203],a[108:111],v[108:111],a[200:203]
ds_read_b64_tr_b16 a[104:105],v43 offset:20576
v_mfma_f32_16x16x32_bf16 a[192:195],a[112:115],v[136:139],a[192:195]
)A"
R"A(s_waitcnt vmcnt(3)
ds_read_b64_tr_b16 a[106:107],v43 offset:25696
ds_read_b64_tr_b16 a[108:109],v43 offset:30816
v_mfma_f32_16x16x32_bf16 a[192:195],a[116:119],v[140:143],a[192:195]
ds_read_b64_tr_b16 a[110:111],v43 offset:35936
v_mfma_f32_16x16x32_bf16 a[192:195],a[120:123],v[152:155],a[192:195]
v_mfma_f32_16x16x32_bf16 a[192:195],a[124:127],v[156:159],a[192:195]
v_mfma_f32_16x16x32_bf16 a[196:199],a[124:127],v[188:191],a[196:199]
v_mfma_f32_16x16x32_bf16 a[196:199],a[120:123],v[184:187],a[196:199]
v_mfma_f32_16x16x32_bf16 a[196:199],a[116:119],v[172:175],a[196:199]
v_mfma_f32_16x16x32_bf16 a[196:199],a[112:115],v[168:171],a[196:199]
v_mfma_f32_16x16x32_bf16 a[200:203],a[112:115],v[200:203],a[200:203]
ds_read_b64_tr_b16 a[112:113],v45 offset:96
ds_read_b64_tr_b16 a[114:115],v45 offset:5216
v_mfma_f32_16x16x32_bf16 a[200:203],a[116:119],v[204:207],a[200:203]
v_accvgpr_read_b32 v128,a192
v_accvgpr_read_b32 v129,a193
v_mfma_f32_16x16x32_bf16 a[200:203],a[120:123],v[216:219],a[200:203]
v_cvt_pk_bf16_f32 v128,v128,v129
v_accvgpr_read_b32 v130,a194
v_mfma_f32_16x16x32_bf16 a[200:203],a[124:127],v[220:223],a[200:203]
v_accvgpr_read_b32 v131,a195
v_cvt_pk_bf16_f32 v129,v130,v131
v_mfma_f32_16x16x32_bf16 a[208:211],a[128:131],v[64:67],a[208:211]
ds_read_b64_tr_b16 a[116:117],v45 offset:10336
ds_read_b64_tr_b16 a[118:119],v45 offset:15456
v_mfma_f32_16x16x32_bf16 a[208:211],a[132:135],v[68:71],a[208:211]
v_accvgpr_read_b32 v132,a196
v_accvgpr_read_b32 v133,a197
v_mfma_f32_16x16x32_bf16 a[208:211],a[136:139],v[72:75],a[208:211]
v_cvt_pk_bf16_f32 v132,v132,v133
v_accvgpr_read_b32 v134,a198
v_mfma_f32_16x16x32_bf16 a[208:211],a[140:143],v[76:79],a[208:211]
v_accvgpr_read_b32 v135,a199
v_cvt_pk_bf16_f32 v133,v134,v135
v_mfma_f32_16x16x32_bf16 a[212:215],a[140:143],v[92:95],a[212:215]
ds_read_b64_tr_b16 a[120:121],v45 offset:20576
ds_read_b64_tr_b16 a[122:123],v45 offset:25696
v_mfma_f32_16x16x32_bf16 a[212:215],a[136:139],v[88:91],a[212:215]
ds_read_b64_tr_b16 a[124:125],v45 offset:30816
v_accvgpr_read_b32 v144,a200
v_mfma_f32_16x16x32_bf16 a[212:215],a[132:135],v[84:87],a[212:215]
ds_read_b64_tr_b16 a[126:127],v45 offset:35936
v_accvgpr_read_b32 v145,a201
v_mfma_f32_16x16x32_bf16 a[212:215],a[128:131],v[80:83],a[212:215]
v_cvt_pk_bf16_f32 v144,v144,v145
v_accvgpr_read_b32 v146,a202
v_mfma_f32_16x16x32_bf16 a[216:219],a[128:131],v[96:99],a[216:219]
v_accvgpr_read_b32 v147,a203
v_cvt_pk_bf16_f32 v145,v146,v147
v_mfma_f32_16x16x32_bf16 a[216:219],a[132:135],v[100:103],a[216:219]
ds_read_b64_tr_b16 a[128:129],v42 offset:128
ds_read_b64_tr_b16 a[130:131],v42 offset:5248
v_mfma_f32_16x16x32_bf16 a[216:219],a[136:139],v[104:107],a[216:219]
ds_read_b64_tr_b16 a[132:133],v42 offset:10368
v_mfma_f32_16x16x32_bf16 a[216:219],a[140:143],v[108:111],a[216:219]
ds_read_b64_tr_b16 a[134:135],v42 offset:15488
v_mfma_f32_16x16x32_bf16 a[208:211],a[144:147],v[136:139],a[208:211]
ds_read_b64_tr_b16 a[136:137],v42 offset:20608
v_mfma_f32_16x16x32_bf16 a[208:211],a[148:151],v[140:143],a[208:211]
ds_read_b64_tr_b16 a[138:139],v42 offset:25728
ds_read_b64_tr_b16 a[140:141],v42 offset:30848
v_mfma_f32_16x16x32_bf16 a[208:211],a[152:155],v[152:155],a[208:211]
ds_read_b64_tr_b16 a[142:143],v42 offset:35968
v_mfma_f32_16x16x32_bf16 a[208:211],a[156:159],v[156:159],a[208:211]
v_mfma_f32_16x16x32_bf16 a[212:215],a[156:159],v[188:191],a[212:215]
v_mfma_f32_16x16x32_bf16 a[212:215],a[152:155],v[184:187],a[212:215]
v_mfma_f32_16x16x32_bf16 a[212:215],a[148:151],v[172:175],a[212:215]
v_mfma_f32_16x16x32_bf16 a[212:215],a[144:147],v[168:171],a[212:215]
v_mfma_f32_16x16x32_bf16 a[216:219],a[144:147],v[200:203],a[216:219]
ds_read_b64_tr_b16 a[144:145],v44 offset:128
ds_read_b64_tr_b16 a[146:147],v44 offset:5248
v_mfma_f32_16x16x32_bf16 a[216:219],a[148:151],v[204:207],a[216:219]
v_accvgpr_read_b32 v130,a208
v_accvgpr_read_b32 v131,a209
v_mfma_f32_16x16x32_bf16 a[216:219],a[152:155],v[216:219],a[216:219]
v_accvgpr_read_b32 v14,a210
v_accvgpr_read_b32 v15,a211
v_mfma_f32_16x16x32_bf16 a[216:219],a[156:159],v[220:223],a[216:219]
v_cvt_pk_bf16_f32 v130,v130,v131
v_cvt_pk_bf16_f32 v131,v14,v15
v_mfma_f32_16x16x32_bf16 a[224:227],a[160:163],v[64:67],a[224:227]
buffer_store_dwordx4 v[128:131],v6,s[32:35],s84 offen sc1
v_mfma_f32_16x16x32_bf16 a[224:227],a[164:167],v[68:71],a[224:227]
s_waitcnt lgkmcnt(2)
v_accvgpr_read_b32 v134,a212
v_accvgpr_read_b32 v135,a213
v_mfma_f32_16x16x32_bf16 a[224:227],a[168:171],v[72:75],a[224:227]
v_accvgpr_read_b32 v16,a214
v_accvgpr_read_b32 v17,a215
v_mfma_f32_16x16x32_bf16 a[224:227],a[172:175],v[76:79],a[224:227]
v_cvt_pk_bf16_f32 v134,v134,v135
v_cvt_pk_bf16_f32 v135,v16,v17
v_mfma_f32_16x16x32_bf16 a[228:231],a[172:175],v[92:95],a[228:231]
buffer_store_dwordx4 v[132:135],v6,s[32:35],s85 offen sc1
v_mfma_f32_16x16x32_bf16 a[228:231],a[168:171],v[88:91],a[228:231]
v_accvgpr_read_b32 v146,a216
v_accvgpr_read_b32 v147,a217
v_mfma_f32_16x16x32_bf16 a[228:231],a[164:167],v[84:87],a[228:231]
v_accvgpr_read_b32 v12,a218
v_accvgpr_read_b32 v13,a219
v_mfma_f32_16x16x32_bf16 a[228:231],a[160:163],v[80:83],a[228:231]
v_cvt_pk_bf16_f32 v146,v146,v147
v_cvt_pk_bf16_f32 v147,v12,v13
v_mfma_f32_16x16x32_bf16 a[232:235],a[160:163],v[96:99],a[232:235]
buffer_store_dwordx4 v[144:147],v6,s[32:35],s86 offen sc1
v_mfma_f32_16x16x32_bf16 a[232:235],a[164:167],v[100:103],a[232:235]
ds_read_b64_tr_b16 a[148:149],v44 offset:10368
ds_read_b64_tr_b16 a[150:151],v44 offset:15488
v_mfma_f32_16x16x32_bf16 a[232:235],a[168:171],v[104:107],a[232:235]
ds_read_b64_tr_b16 a[152:153],v44 offset:20608
v_mfma_f32_16x16x32_bf16 a[232:235],a[172:175],v[108:111],a[232:235]
ds_read_b64_tr_b16 a[154:155],v44 offset:25728
v_mfma_f32_16x16x32_bf16 a[224:227],a[176:179],v[136:139],a[224:227]
ds_read_b64_tr_b16 a[156:157],v44 offset:30848
v_mfma_f32_16x16x32_bf16 a[224:227],a[180:183],v[140:143],a[224:227]
ds_read_b64_tr_b16 a[158:159],v44 offset:35968
ds_read_b64_tr_b16 a[160:161],v43 offset:160
v_mfma_f32_16x16x32_bf16 a[224:227],a[184:187],v[152:155],a[224:227]
ds_read_b64_tr_b16 a[162:163],v43 offset:5280
v_mfma_f32_16x16x32_bf16 a[224:227],a[188:191],v[156:159],a[224:227]
ds_read_b64_tr_b16 a[164:165],v43 offset:10400
v_mfma_f32_16x16x32_bf16 a[228:231],a[188:191],v[188:191],a[228:231]
ds_read_b64_tr_b16 a[166:167],v43 offset:15520
v_mfma_f32_16x16x32_bf16 a[228:231],a[184:187],v[184:187],a[228:231]
ds_read_b64_tr_b16 a[168:169],v43 offset:20640
ds_read_b64_tr_b16 a[170:171],v43 offset:25760
v_mfma_f32_16x16x32_bf16 a[228:231],a[180:183],v[172:175],a[228:231]
ds_read_b64_tr_b16 a[172:173],v43 offset:30880
v_mfma_f32_16x16x32_bf16 a[228:231],a[176:179],v[168:171],a[228:231]
ds_read_b64_tr_b16 a[174:175],v43 offset:36000
v_mfma_f32_16x16x32_bf16 a[232:235],a[176:179],v[200:203],a[232:235]
ds_read_b64_tr_b16 a[176:177],v45 offset:160
v_mfma_f32_16x16x32_bf16 a[232:235],a[180:183],v[204:207],a[232:235]
v_accvgpr_read_b32 v160,a224
v_accvgpr_read_b32 v161,a225
v_mfma_f32_16x16x32_bf16 a[232:235],a[184:187],v[216:219],a[232:235]
v_cvt_pk_bf16_f32 v160,v160,v161
v_accvgpr_read_b32 v162,a226
v_mfma_f32_16x16x32_bf16 a[232:235],a[188:191],v[220:223],a[232:235]
v_accvgpr_read_b32 v163,a227
v_cvt_pk_bf16_f32 v161,v162,v163
v_mfma_f32_16x16x32_bf16 a[240:243],a[96:99],v[64:67],a[240:243]
ds_read_b64_tr_b16 a[178:179],v45 offset:5280
ds_read_b64_tr_b16 a[180:181],v45 offset:10400
v_mfma_f32_16x16x32_bf16 a[240:243],a[100:103],v[68:71],a[240:243]
v_accvgpr_read_b32 v164,a228
v_accvgpr_read_b32 v165,a229
v_mfma_f32_16x16x32_bf16 a[240:243],a[104:107],v[72:75],a[240:243]
v_cvt_pk_bf16_f32 v164,v164,v165
v_accvgpr_read_b32 v166,a230
v_mfma_f32_16x16x32_bf16 a[240:243],a[108:111],v[76:79],a[240:243]
v_accvgpr_read_b32 v167,a231
v_cvt_pk_bf16_f32 v165,v166,v167
v_mfma_f32_16x16x32_bf16 a[244:247],a[108:111],v[92:95],a[244:247]
ds_read_b64_tr_b16 a[182:183],v45 offset:15520
ds_read_b64_tr_b16 a[184:185],v45 offset:20640
v_mfma_f32_16x16x32_bf16 a[244:247],a[104:107],v[88:91],a[244:247]
ds_read_b64_tr_b16 a[186:187],v45 offset:25760
v_accvgpr_read_b32 v176,a232
v_mfma_f32_16x16x32_bf16 a[244:247],a[100:103],v[84:87],a[244:247]
ds_read_b64_tr_b16 a[188:189],v45 offset:30880
v_accvgpr_read_b32 v177,a233
v_mfma_f32_16x16x32_bf16 a[244:247],a[96:99],v[80:83],a[244:247]
ds_read_b64_tr_b16 a[190:191],v45 offset:36000
v_cvt_pk_bf16_f32 v176,v176,v177
v_mfma_f32_16x16x32_bf16 a[248:251],a[96:99],v[96:99],a[248:251]
v_accvgpr_read_b32 v178,a234
v_accvgpr_read_b32 v179,a235
v_mfma_f32_16x16x32_bf16 a[248:251],a[100:103],v[100:103],a[248:251]
ds_read_b64_tr_b16 a[96:97],v42 offset:192
v_cvt_pk_bf16_f32 v177,v178,v179
v_mfma_f32_16x16x32_bf16 a[248:251],a[104:107],v[104:107],a[248:251]
ds_read_b64_tr_b16 a[98:99],v42 offset:5312
ds_read_b64_tr_b16 a[100:101],v42 offset:10432
v_mfma_f32_16x16x32_bf16 a[248:251],a[108:111],v[108:111],a[248:251]
ds_read_b64_tr_b16 a[102:103],v42 offset:15552
v_mfma_f32_16x16x32_bf16 a[240:243],a[112:115],v[136:139],a[240:243]
ds_read_b64_tr_b16 a[104:105],v42 offset:20672
v_mfma_f32_16x16x32_bf16 a[240:243],a[116:119],v[140:143],a[240:243]
ds_read_b64_tr_b16 a[106:107],v42 offset:25792
v_mfma_f32_16x16x32_bf16 a[240:243],a[120:123],v[152:155],a[240:243]
ds_read_b64_tr_b16 a[108:109],v42 offset:30912
ds_read_b64_tr_b16 a[110:111],v42 offset:36032
v_mfma_f32_16x16x32_bf16 a[240:243],a[124:127],v[156:159],a[240:243]
v_mfma_f32_16x16x32_bf16 a[244:247],a[124:127],v[188:191],a[244:247]
v_mfma_f32_16x16x32_bf16 a[244:247],a[120:123],v[184:187],a[244:247]
v_mfma_f32_16x16x32_bf16 a[244:247],a[116:119],v[172:175],a[244:247]
v_mfma_f32_16x16x32_bf16 a[244:247],a[112:115],v[168:171],a[244:247]
v_mfma_f32_16x16x32_bf16 a[248:251],a[112:115],v[200:203],a[248:251]
ds_read_b64_tr_b16 a[112:113],v44 offset:192
ds_read_b64_tr_b16 a[114:115],v44 offset:5312
v_mfma_f32_16x16x32_bf16 a[248:251],a[116:119],v[204:207],a[248:251]
v_accvgpr_read_b32 v162,a240
v_accvgpr_read_b32 v163,a241
v_mfma_f32_16x16x32_bf16 a[248:251],a[120:123],v[216:219],a[248:251]
v_accvgpr_read_b32 v14,a242
v_accvgpr_read_b32 v15,a243
v_mfma_f32_16x16x32_bf16 a[248:251],a[124:127],v[220:223],a[248:251]
v_cvt_pk_bf16_f32 v162,v162,v163
v_cvt_pk_bf16_f32 v163,v14,v15
v_mfma_f32_16x16x32_bf16 a[0:3],a[128:131],v[64:67],a[0:3]
buffer_store_dwordx4 v[160:163],v6,s[32:35],s84 offen offset:1024 sc1
v_mfma_f32_16x16x32_bf16 a[0:3],a[132:135],v[68:71],a[0:3]
v_accvgpr_read_b32 v166,a244
v_accvgpr_read_b32 v167,a245
v_mfma_f32_16x16x32_bf16 a[0:3],a[136:139],v[72:75],a[0:3]
v_accvgpr_read_b32 v16,a246
v_accvgpr_read_b32 v17,a247
v_mfma_f32_16x16x32_bf16 a[0:3],a[140:143],v[76:79],a[0:3]
v_cvt_pk_bf16_f32 v166,v166,v167
v_cvt_pk_bf16_f32 v167,v16,v17
v_mfma_f32_16x16x32_bf16 a[4:7],a[140:143],v[92:95],a[4:7]
buffer_store_dwordx4 v[164:167],v6,s[32:35],s85 offen offset:1024 sc1
v_mfma_f32_16x16x32_bf16 a[4:7],a[136:139],v[88:91],a[4:7]
v_accvgpr_read_b32 v178,a248
v_accvgpr_read_b32 v179,a249
v_mfma_f32_16x16x32_bf16 a[4:7],a[132:135],v[84:87],a[4:7]
v_accvgpr_read_b32 v12,a250
v_accvgpr_read_b32 v13,a251
v_mfma_f32_16x16x32_bf16 a[4:7],a[128:131],v[80:83],a[4:7]
v_cvt_pk_bf16_f32 v178,v178,v179
v_cvt_pk_bf16_f32 v179,v12,v13
v_mfma_f32_16x16x32_bf16 a[8:11],a[128:131],v[96:99],a[8:11]
buffer_store_dwordx4 v[176:179],v6,s[32:35],s86 offen offset:1024 sc1
v_mfma_f32_16x16x32_bf16 a[8:11],a[132:135],v[100:103],a[8:11]
ds_read_b64_tr_b16 a[116:117],v44 offset:10432
ds_read_b64_tr_b16 a[118:119],v44 offset:15552
v_mfma_f32_16x16x32_bf16 a[8:11],a[136:139],v[104:107],a[8:11]
ds_read_b64_tr_b16 a[120:121],v44 offset:20672
v_mfma_f32_16x16x32_bf16 a[8:11],a[140:143],v[108:111],a[8:11]
s_waitcnt lgkmcnt(3)
ds_read_b64_tr_b16 a[122:123],v44 offset:25792
v_mfma_f32_16x16x32_bf16 a[0:3],a[144:147],v[136:139],a[0:3]
ds_read_b64_tr_b16 a[124:125],v44 offset:30912
v_mfma_f32_16x16x32_bf16 a[0:3],a[148:151],v[140:143],a[0:3]
ds_read_b64_tr_b16 a[126:127],v44 offset:36032
ds_read_b64_tr_b16 a[128:129],v43 offset:224
v_mfma_f32_16x16x32_bf16 a[0:3],a[152:155],v[152:155],a[0:3]
ds_read_b64_tr_b16 a[130:131],v43 offset:5344
)A"
R"A(v_mfma_f32_16x16x32_bf16 a[0:3],a[156:159],v[156:159],a[0:3]
ds_read_b64_tr_b16 a[132:133],v43 offset:10464
v_mfma_f32_16x16x32_bf16 a[4:7],a[156:159],v[188:191],a[4:7]
ds_read_b64_tr_b16 a[134:135],v43 offset:15584
v_mfma_f32_16x16x32_bf16 a[4:7],a[152:155],v[184:187],a[4:7]
ds_read_b64_tr_b16 a[136:137],v43 offset:20704
ds_read_b64_tr_b16 a[138:139],v43 offset:25824
v_mfma_f32_16x16x32_bf16 a[4:7],a[148:151],v[172:175],a[4:7]
ds_read_b64_tr_b16 a[140:141],v43 offset:30944
v_mfma_f32_16x16x32_bf16 a[4:7],a[144:147],v[168:171],a[4:7]
ds_read_b64_tr_b16 a[142:143],v43 offset:36064
v_mfma_f32_16x16x32_bf16 a[8:11],a[144:147],v[200:203],a[8:11]
ds_read_b64_tr_b16 a[144:145],v45 offset:224
v_mfma_f32_16x16x32_bf16 a[8:11],a[148:151],v[204:207],a[8:11]
v_accvgpr_read_b32 v192,a0
v_accvgpr_read_b32 v193,a1
v_mfma_f32_16x16x32_bf16 a[8:11],a[152:155],v[216:219],a[8:11]
v_cvt_pk_bf16_f32 v192,v192,v193
v_accvgpr_read_b32 v194,a2
v_mfma_f32_16x16x32_bf16 a[8:11],a[156:159],v[220:223],a[8:11]
v_accvgpr_read_b32 v195,a3
v_cvt_pk_bf16_f32 v193,v194,v195
v_mfma_f32_16x16x32_bf16 a[16:19],a[160:163],v[64:67],a[16:19]
ds_read_b64_tr_b16 a[146:147],v45 offset:5344
ds_read_b64_tr_b16 a[148:149],v45 offset:10464
v_mfma_f32_16x16x32_bf16 a[16:19],a[164:167],v[68:71],a[16:19]
v_accvgpr_read_b32 v196,a4
v_accvgpr_read_b32 v197,a5
v_mfma_f32_16x16x32_bf16 a[16:19],a[168:171],v[72:75],a[16:19]
v_cvt_pk_bf16_f32 v196,v196,v197
v_accvgpr_read_b32 v198,a6
v_mfma_f32_16x16x32_bf16 a[16:19],a[172:175],v[76:79],a[16:19]
v_accvgpr_read_b32 v199,a7
v_cvt_pk_bf16_f32 v197,v198,v199
v_mfma_f32_16x16x32_bf16 a[20:23],a[172:175],v[92:95],a[20:23]
ds_read_b64_tr_b16 a[150:151],v45 offset:15584
ds_read_b64_tr_b16 a[152:153],v45 offset:20704
v_mfma_f32_16x16x32_bf16 a[20:23],a[168:171],v[88:91],a[20:23]
ds_read_b64_tr_b16 a[154:155],v45 offset:25824
v_accvgpr_read_b32 v208,a8
v_mfma_f32_16x16x32_bf16 a[20:23],a[164:167],v[84:87],a[20:23]
ds_read_b64_tr_b16 a[156:157],v45 offset:30944
v_accvgpr_read_b32 v209,a9
v_mfma_f32_16x16x32_bf16 a[20:23],a[160:163],v[80:83],a[20:23]
ds_read_b64_tr_b16 a[158:159],v45 offset:36064
v_cvt_pk_bf16_f32 v208,v208,v209
v_mfma_f32_16x16x32_bf16 a[24:27],a[160:163],v[96:99],a[24:27]
v_accvgpr_read_b32 v210,a10
v_accvgpr_read_b32 v211,a11
v_mfma_f32_16x16x32_bf16 a[24:27],a[164:167],v[100:103],a[24:27]
ds_read_b64_tr_b16 a[160:161],v42 offset:256
v_cvt_pk_bf16_f32 v209,v210,v211
v_mfma_f32_16x16x32_bf16 a[24:27],a[168:171],v[104:107],a[24:27]
ds_read_b64_tr_b16 a[162:163],v42 offset:5376
ds_read_b64_tr_b16 a[164:165],v42 offset:10496
v_mfma_f32_16x16x32_bf16 a[24:27],a[172:175],v[108:111],a[24:27]
ds_read_b64_tr_b16 a[166:167],v42 offset:15616
v_mfma_f32_16x16x32_bf16 a[16:19],a[176:179],v[136:139],a[16:19]
ds_read_b64_tr_b16 a[168:169],v42 offset:20736
v_mfma_f32_16x16x32_bf16 a[16:19],a[180:183],v[140:143],a[16:19]
ds_read_b64_tr_b16 a[170:171],v42 offset:25856
v_mfma_f32_16x16x32_bf16 a[16:19],a[184:187],v[152:155],a[16:19]
ds_read_b64_tr_b16 a[172:173],v42 offset:30976
ds_read_b64_tr_b16 a[174:175],v42 offset:36096
v_mfma_f32_16x16x32_bf16 a[16:19],a[188:191],v[156:159],a[16:19]
v_mfma_f32_16x16x32_bf16 a[20:23],a[188:191],v[188:191],a[20:23]
v_mfma_f32_16x16x32_bf16 a[20:23],a[184:187],v[184:187],a[20:23]
v_mfma_f32_16x16x32_bf16 a[20:23],a[180:183],v[172:175],a[20:23]
v_mfma_f32_16x16x32_bf16 a[20:23],a[176:179],v[168:171],a[20:23]
v_mfma_f32_16x16x32_bf16 a[24:27],a[176:179],v[200:203],a[24:27]
ds_read_b64_tr_b16 a[176:177],v44 offset:256
ds_read_b64_tr_b16 a[178:179],v44 offset:5376
v_mfma_f32_16x16x32_bf16 a[24:27],a[180:183],v[204:207],a[24:27]
v_accvgpr_read_b32 v194,a16
v_accvgpr_read_b32 v195,a17
v_mfma_f32_16x16x32_bf16 a[24:27],a[184:187],v[216:219],a[24:27]
v_accvgpr_read_b32 v14,a18
v_accvgpr_read_b32 v15,a19
v_mfma_f32_16x16x32_bf16 a[24:27],a[188:191],v[220:223],a[24:27]
v_cvt_pk_bf16_f32 v194,v194,v195
v_cvt_pk_bf16_f32 v195,v14,v15
v_mfma_f32_16x16x32_bf16 a[32:35],a[96:99],v[64:67],a[32:35]
buffer_store_dwordx4 v[192:195],v6,s[32:35],s84 offen offset:2048 sc1
v_mfma_f32_16x16x32_bf16 a[32:35],a[100:103],v[68:71],a[32:35]
v_accvgpr_read_b32 v198,a20
v_accvgpr_read_b32 v199,a21
v_mfma_f32_16x16x32_bf16 a[32:35],a[104:107],v[72:75],a[32:35]
v_accvgpr_read_b32 v16,a22
v_accvgpr_read_b32 v17,a23
v_mfma_f32_16x16x32_bf16 a[32:35],a[108:111],v[76:79],a[32:35]
v_cvt_pk_bf16_f32 v198,v198,v199
v_cvt_pk_bf16_f32 v199,v16,v17
v_mfma_f32_16x16x32_bf16 a[36:39],a[108:111],v[92:95],a[36:39]
buffer_store_dwordx4 v[196:199],v6,s[32:35],s85 offen offset:2048 sc1
v_mfma_f32_16x16x32_bf16 a[36:39],a[104:107],v[88:91],a[36:39]
v_accvgpr_read_b32 v210,a24
v_accvgpr_read_b32 v211,a25
v_mfma_f32_16x16x32_bf16 a[36:39],a[100:103],v[84:87],a[36:39]
v_accvgpr_read_b32 v12,a26
v_accvgpr_read_b32 v13,a27
v_mfma_f32_16x16x32_bf16 a[36:39],a[96:99],v[80:83],a[36:39]
v_cvt_pk_bf16_f32 v210,v210,v211
v_cvt_pk_bf16_f32 v211,v12,v13
v_mfma_f32_16x16x32_bf16 a[40:43],a[96:99],v[96:99],a[40:43]
buffer_store_dwordx4 v[208:211],v6,s[32:35],s86 offen offset:2048 sc1
v_mfma_f32_16x16x32_bf16 a[40:43],a[100:103],v[100:103],a[40:43]
ds_read_b64_tr_b16 a[180:181],v44 offset:10496
ds_read_b64_tr_b16 a[182:183],v44 offset:15616
v_mfma_f32_16x16x32_bf16 a[40:43],a[104:107],v[104:107],a[40:43]
ds_read_b64_tr_b16 a[184:185],v44 offset:20736
v_mfma_f32_16x16x32_bf16 a[40:43],a[108:111],v[108:111],a[40:43]
ds_read_b64_tr_b16 a[186:187],v44 offset:25856
v_mfma_f32_16x16x32_bf16 a[32:35],a[112:115],v[136:139],a[32:35]
s_waitcnt lgkmcnt(4)
ds_read_b64_tr_b16 a[188:189],v44 offset:30976
v_mfma_f32_16x16x32_bf16 a[32:35],a[116:119],v[140:143],a[32:35]
ds_read_b64_tr_b16 a[190:191],v44 offset:36096
ds_read_b64_tr_b16 a[96:97],v43 offset:288
v_mfma_f32_16x16x32_bf16 a[32:35],a[120:123],v[152:155],a[32:35]
ds_read_b64_tr_b16 a[98:99],v43 offset:5408
v_mfma_f32_16x16x32_bf16 a[32:35],a[124:127],v[156:159],a[32:35]
ds_read_b64_tr_b16 a[100:101],v43 offset:10528
v_mfma_f32_16x16x32_bf16 a[36:39],a[124:127],v[188:191],a[36:39]
ds_read_b64_tr_b16 a[102:103],v43 offset:15648
v_mfma_f32_16x16x32_bf16 a[36:39],a[120:123],v[184:187],a[36:39]
ds_read_b64_tr_b16 a[104:105],v43 offset:20768
ds_read_b64_tr_b16 a[106:107],v43 offset:25888
v_mfma_f32_16x16x32_bf16 a[36:39],a[116:119],v[172:175],a[36:39]
ds_read_b64_tr_b16 a[108:109],v43 offset:31008
v_mfma_f32_16x16x32_bf16 a[36:39],a[112:115],v[168:171],a[36:39]
ds_read_b64_tr_b16 a[110:111],v43 offset:36128
v_mfma_f32_16x16x32_bf16 a[40:43],a[112:115],v[200:203],a[40:43]
ds_read_b64_tr_b16 a[112:113],v45 offset:288
v_mfma_f32_16x16x32_bf16 a[40:43],a[116:119],v[204:207],a[40:43]
v_accvgpr_read_b32 v224,a32
v_accvgpr_read_b32 v225,a33
v_mfma_f32_16x16x32_bf16 a[40:43],a[120:123],v[216:219],a[40:43]
v_cvt_pk_bf16_f32 v224,v224,v225
v_accvgpr_read_b32 v226,a34
v_mfma_f32_16x16x32_bf16 a[40:43],a[124:127],v[220:223],a[40:43]
v_accvgpr_read_b32 v227,a35
v_cvt_pk_bf16_f32 v225,v226,v227
v_mfma_f32_16x16x32_bf16 a[48:51],a[128:131],v[64:67],a[48:51]
ds_read_b64_tr_b16 a[114:115],v45 offset:5408
ds_read_b64_tr_b16 a[116:117],v45 offset:10528
v_mfma_f32_16x16x32_bf16 a[48:51],a[132:135],v[68:71],a[48:51]
v_accvgpr_read_b32 v228,a36
v_accvgpr_read_b32 v229,a37
v_mfma_f32_16x16x32_bf16 a[48:51],a[136:139],v[72:75],a[48:51]
v_cvt_pk_bf16_f32 v228,v228,v229
v_accvgpr_read_b32 v230,a38
v_mfma_f32_16x16x32_bf16 a[48:51],a[140:143],v[76:79],a[48:51]
v_accvgpr_read_b32 v231,a39
v_cvt_pk_bf16_f32 v229,v230,v231
v_mfma_f32_16x16x32_bf16 a[52:55],a[140:143],v[92:95],a[52:55]
ds_read_b64_tr_b16 a[118:119],v45 offset:15648
ds_read_b64_tr_b16 a[120:121],v45 offset:20768
v_mfma_f32_16x16x32_bf16 a[52:55],a[136:139],v[88:91],a[52:55]
v_accvgpr_read_b32 v240,a40
v_accvgpr_read_b32 v241,a41
v_mfma_f32_16x16x32_bf16 a[52:55],a[132:135],v[84:87],a[52:55]
v_cvt_pk_bf16_f32 v240,v240,v241
v_accvgpr_read_b32 v242,a42
v_mfma_f32_16x16x32_bf16 a[52:55],a[128:131],v[80:83],a[52:55]
v_accvgpr_read_b32 v243,a43
v_cvt_pk_bf16_f32 v241,v242,v243
v_mfma_f32_16x16x32_bf16 a[56:59],a[128:131],v[96:99],a[56:59]
ds_read_b64_tr_b16 a[122:123],v45 offset:25888
ds_read_b64_tr_b16 a[124:125],v45 offset:31008
v_mfma_f32_16x16x32_bf16 a[56:59],a[132:135],v[100:103],a[56:59]
ds_read_b64_tr_b16 a[126:127],v45 offset:36128
v_mfma_f32_16x16x32_bf16 a[56:59],a[136:139],v[104:107],a[56:59]
ds_read_b64_tr_b16 a[128:129],v1
v_mfma_f32_16x16x32_bf16 a[56:59],a[140:143],v[108:111],a[56:59]
ds_read_b64_tr_b16 a[130:131],v1 offset:5120
v_mfma_f32_16x16x32_bf16 a[48:51],a[144:147],v[136:139],a[48:51]
ds_read_b64_tr_b16 a[132:133],v1 offset:10240
ds_read_b64_tr_b16 a[134:135],v1 offset:15360
v_mfma_f32_16x16x32_bf16 a[48:51],a[148:151],v[140:143],a[48:51]
ds_read_b64_tr_b16 a[136:137],v1 offset:20480
v_mfma_f32_16x16x32_bf16 a[48:51],a[152:155],v[152:155],a[48:51]
ds_read_b64_tr_b16 a[138:139],v1 offset:25600
v_mfma_f32_16x16x32_bf16 a[48:51],a[156:159],v[156:159],a[48:51]
ds_read_b64_tr_b16 a[140:141],v1 offset:30720
v_mfma_f32_16x16x32_bf16 a[52:55],a[156:159],v[188:191],a[52:55]
ds_read_b64_tr_b16 a[142:143],v1 offset:35840
v_mfma_f32_16x16x32_bf16 a[52:55],a[152:155],v[184:187],a[52:55]
v_mfma_f32_16x16x32_bf16 a[52:55],a[148:151],v[172:175],a[52:55]
v_mfma_f32_16x16x32_bf16 a[52:55],a[144:147],v[168:171],a[52:55]
v_mfma_f32_16x16x32_bf16 a[56:59],a[144:147],v[200:203],a[56:59]
ds_read_b64_tr_b16 a[144:145],v42
ds_read_b64_tr_b16 a[146:147],v42 offset:5120
v_mfma_f32_16x16x32_bf16 a[56:59],a[148:151],v[204:207],a[56:59]
v_accvgpr_read_b32 v226,a48
v_accvgpr_read_b32 v227,a49
v_mfma_f32_16x16x32_bf16 a[56:59],a[152:155],v[216:219],a[56:59]
v_accvgpr_read_b32 v14,a50
v_accvgpr_read_b32 v15,a51
v_mfma_f32_16x16x32_bf16 a[56:59],a[156:159],v[220:223],a[56:59]
v_cvt_pk_bf16_f32 v226,v226,v227
v_cvt_pk_bf16_f32 v227,v14,v15
v_mfma_f32_16x16x32_bf16 a[64:67],a[160:163],v[64:67],a[64:67]
buffer_store_dwordx4 v[224:227],v6,s[32:35],s84 offen offset:3072 sc1
v_mfma_f32_16x16x32_bf16 a[64:67],a[164:167],v[68:71],a[64:67]
v_accvgpr_read_b32 v230,a52
v_accvgpr_read_b32 v231,a53
v_mfma_f32_16x16x32_bf16 a[64:67],a[168:171],v[72:75],a[64:67]
v_accvgpr_read_b32 v16,a54
v_accvgpr_read_b32 v17,a55
v_mfma_f32_16x16x32_bf16 a[64:67],a[172:175],v[76:79],a[64:67]
v_cvt_pk_bf16_f32 v230,v230,v231
v_cvt_pk_bf16_f32 v231,v16,v17
v_mfma_f32_16x16x32_bf16 a[68:71],a[172:175],v[92:95],a[68:71]
buffer_store_dwordx4 v[228:231],v6,s[32:35],s85 offen offset:3072 sc1
v_mfma_f32_16x16x32_bf16 a[68:71],a[168:171],v[88:91],a[68:71]
v_accvgpr_read_b32 v242,a56
v_accvgpr_read_b32 v243,a57
v_mfma_f32_16x16x32_bf16 a[68:71],a[164:167],v[84:87],a[68:71]
v_accvgpr_read_b32 v12,a58
v_accvgpr_read_b32 v13,a59
v_mfma_f32_16x16x32_bf16 a[68:71],a[160:163],v[80:83],a[68:71]
v_cvt_pk_bf16_f32 v242,v242,v243
v_cvt_pk_bf16_f32 v243,v12,v13
v_mfma_f32_16x16x32_bf16 a[72:75],a[160:163],v[96:99],a[72:75]
buffer_store_dwordx4 v[240:243],v6,s[32:35],s86 offen offset:3072 sc1
v_mfma_f32_16x16x32_bf16 a[72:75],a[164:167],v[100:103],a[72:75]
ds_read_b64_tr_b16 a[148:149],v42 offset:10240
ds_read_b64_tr_b16 a[150:151],v42 offset:15360
v_mfma_f32_16x16x32_bf16 a[72:75],a[168:171],v[104:107],a[72:75]
ds_read_b64_tr_b16 a[152:153],v42 offset:20480
v_mfma_f32_16x16x32_bf16 a[72:75],a[172:175],v[108:111],a[72:75]
ds_read_b64_tr_b16 a[154:155],v42 offset:25600
v_mfma_f32_16x16x32_bf16 a[64:67],a[176:179],v[136:139],a[64:67]
s_waitcnt lgkmcnt(4)
ds_read_b64_tr_b16 a[156:157],v42 offset:30720
v_mfma_f32_16x16x32_bf16 a[64:67],a[180:183],v[140:143],a[64:67]
ds_read_b64_tr_b16 a[158:159],v42 offset:35840
ds_read_b64_tr_b16 a[160:161],v44
v_mfma_f32_16x16x32_bf16 a[64:67],a[184:187],v[152:155],a[64:67]
ds_read_b64_tr_b16 a[162:163],v44 offset:5120
v_mfma_f32_16x16x32_bf16 a[64:67],a[188:191],v[156:159],a[64:67]
ds_read_b64_tr_b16 a[164:165],v44 offset:10240
v_mfma_f32_16x16x32_bf16 a[68:71],a[188:191],v[188:191],a[68:71]
ds_read_b64_tr_b16 a[166:167],v44 offset:15360
v_mfma_f32_16x16x32_bf16 a[68:71],a[184:187],v[184:187],a[68:71]
ds_read_b64_tr_b16 a[168:169],v44 offset:20480
ds_read_b64_tr_b16 a[170:171],v44 offset:25600
)A"
R"A(v_mfma_f32_16x16x32_bf16 a[68:71],a[180:183],v[172:175],a[68:71]
ds_read_b64_tr_b16 a[172:173],v44 offset:30720
v_mfma_f32_16x16x32_bf16 a[68:71],a[176:179],v[168:171],a[68:71]
ds_read_b64_tr_b16 a[174:175],v44 offset:35840
v_mfma_f32_16x16x32_bf16 a[72:75],a[176:179],v[200:203],a[72:75]
ds_read_b64_tr_b16 a[176:177],v2 offset:32
v_mfma_f32_16x16x32_bf16 a[72:75],a[180:183],v[204:207],a[72:75]
v_accvgpr_read_b32 v128,a64
v_accvgpr_read_b32 v129,a65
v_mfma_f32_16x16x32_bf16 a[72:75],a[184:187],v[216:219],a[72:75]
v_cvt_pk_bf16_f32 v128,v128,v129
v_accvgpr_read_b32 v130,a66
v_mfma_f32_16x16x32_bf16 a[72:75],a[188:191],v[220:223],a[72:75]
v_accvgpr_read_b32 v131,a67
v_cvt_pk_bf16_f32 v129,v130,v131
v_mfma_f32_16x16x32_bf16 a[80:83],a[96:99],v[64:67],a[80:83]
ds_read_b64_tr_b16 a[178:179],v2 offset:5152
ds_read_b64_tr_b16 a[180:181],v2 offset:10272
v_mfma_f32_16x16x32_bf16 a[80:83],a[100:103],v[68:71],a[80:83]
v_accvgpr_read_b32 v132,a68
v_accvgpr_read_b32 v133,a69
v_mfma_f32_16x16x32_bf16 a[80:83],a[104:107],v[72:75],a[80:83]
v_cvt_pk_bf16_f32 v132,v132,v133
v_accvgpr_read_b32 v134,a70
v_mfma_f32_16x16x32_bf16 a[80:83],a[108:111],v[76:79],a[80:83]
ds_read_b64_tr_b16 a[182:183],v2 offset:15392
v_accvgpr_read_b32 v135,a71
v_mfma_f32_16x16x32_bf16 a[84:87],a[108:111],v[92:95],a[84:87]
ds_read_b64_tr_b16 a[184:185],v2 offset:20512
v_cvt_pk_bf16_f32 v133,v134,v135
v_mfma_f32_16x16x32_bf16 a[84:87],a[104:107],v[88:91],a[84:87]
v_accvgpr_read_b32 v144,a72
v_accvgpr_read_b32 v145,a73
v_mfma_f32_16x16x32_bf16 a[84:87],a[100:103],v[84:87],a[84:87]
v_cvt_pk_bf16_f32 v144,v144,v145
v_accvgpr_read_b32 v146,a74
v_mfma_f32_16x16x32_bf16 a[84:87],a[96:99],v[80:83],a[84:87]
v_accvgpr_read_b32 v147,a75
v_cvt_pk_bf16_f32 v145,v146,v147
v_mfma_f32_16x16x32_bf16 a[88:91],a[96:99],v[96:99],a[88:91]
ds_read_b64_tr_b16 a[186:187],v2 offset:25632
ds_read_b64_tr_b16 a[188:189],v2 offset:30752
v_mfma_f32_16x16x32_bf16 a[88:91],a[100:103],v[100:103],a[88:91]
ds_read_b64_tr_b16 a[190:191],v2 offset:35872
v_mfma_f32_16x16x32_bf16 a[88:91],a[104:107],v[104:107],a[88:91]
ds_read_b64_tr_b16 a[96:97],v43 offset:32
v_mfma_f32_16x16x32_bf16 a[88:91],a[108:111],v[108:111],a[88:91]
ds_read_b64_tr_b16 a[98:99],v43 offset:5152
v_mfma_f32_16x16x32_bf16 a[80:83],a[112:115],v[136:139],a[80:83]
ds_read_b64_tr_b16 a[100:101],v43 offset:10272
ds_read_b64_tr_b16 a[102:103],v43 offset:15392
v_mfma_f32_16x16x32_bf16 a[80:83],a[116:119],v[140:143],a[80:83]
ds_read_b64_tr_b16 a[104:105],v43 offset:20512
v_mfma_f32_16x16x32_bf16 a[80:83],a[120:123],v[152:155],a[80:83]
ds_read_b64_tr_b16 a[106:107],v43 offset:25632
v_mfma_f32_16x16x32_bf16 a[80:83],a[124:127],v[156:159],a[80:83]
ds_read_b64_tr_b16 a[108:109],v43 offset:30752
v_mfma_f32_16x16x32_bf16 a[84:87],a[124:127],v[188:191],a[84:87]
ds_read_b64_tr_b16 a[110:111],v43 offset:35872
v_mfma_f32_16x16x32_bf16 a[84:87],a[120:123],v[184:187],a[84:87]
v_mfma_f32_16x16x32_bf16 a[84:87],a[116:119],v[172:175],a[84:87]
v_mfma_f32_16x16x32_bf16 a[84:87],a[112:115],v[168:171],a[84:87]
v_mfma_f32_16x16x32_bf16 a[88:91],a[112:115],v[200:203],a[88:91]
ds_read_b64_tr_b16 a[112:113],v45 offset:32
ds_read_b64_tr_b16 a[114:115],v45 offset:5152
v_mfma_f32_16x16x32_bf16 a[88:91],a[116:119],v[204:207],a[88:91]
v_accvgpr_read_b32 v130,a80
v_accvgpr_read_b32 v131,a81
v_mfma_f32_16x16x32_bf16 a[88:91],a[120:123],v[216:219],a[88:91]
v_accvgpr_read_b32 v14,a82
v_accvgpr_read_b32 v15,a83
v_mfma_f32_16x16x32_bf16 a[88:91],a[124:127],v[220:223],a[88:91]
v_cvt_pk_bf16_f32 v130,v130,v131
v_cvt_pk_bf16_f32 v131,v14,v15
v_mfma_f32_16x16x32_bf16 a[204:207],a[128:131],v[148:151],a[204:207]
s_lshl_b32 s94,s71,2
buffer_store_dwordx4 v[128:131],v25,s[32:35],s84 offen sc1
v_mfma_f32_16x16x32_bf16 a[204:207],a[132:135],v[180:183],a[204:207]
s_add_u32 s94,s94,s40
v_accvgpr_read_b32 v134,a84
v_accvgpr_read_b32 v135,a85
v_mfma_f32_16x16x32_bf16 a[204:207],a[136:139],v[212:215],a[204:207]
s_lshl_b32 s95,s42,2
s_add_u32 s95,s95,s40
s_lshl_b32 s95,s95,4
v_mfma_f32_16x16x32_bf16 a[204:207],a[140:143],v[244:247],a[204:207]
s_lshl_b32 s94,s94,4
v_mov_b32 v18,s68
v_mov_b32 v19,s69
v_mfma_f32_16x16x32_bf16 a[204:207],a[144:147],v[112:115],a[204:207]
s_waitcnt vmcnt(1)
s_mov_b64 exec,1
buffer_store_dwordx2 v[18:19],v6,s[64:67],s95 offen offset:2048 sc1
s_mov_b64 exec,-1
s_waitcnt lgkmcnt(0)
v_mfma_f32_16x16x32_bf16 a[204:207],a[148:151],v[116:119],a[204:207]
v_accvgpr_read_b32 v146,a88
v_accvgpr_read_b32 v147,a89
v_mfma_f32_16x16x32_bf16 a[204:207],a[152:155],v[120:123],a[204:207]
v_accvgpr_read_b32 v16,a86
v_accvgpr_read_b32 v17,a87
v_cvt_pk_bf16_f32 v134,v134,v135
v_accvgpr_read_b32 v12,a90
v_accvgpr_read_b32 v13,a91
v_cvt_pk_bf16_f32 v146,v146,v147
v_cvt_pk_bf16_f32 v135,v16,v17
v_cvt_pk_bf16_f32 v147,v12,v13
v_mfma_f32_16x16x32_bf16 a[204:207],a[156:159],v[124:127],a[204:207]
ds_read_b64_tr_b16 a[116:117],v45 offset:10272
ds_read_b64_tr_b16 a[118:119],v45 offset:15392
v_mfma_f32_16x16x32_bf16 a[204:207],a[160:163],v[232:235],a[204:207]
buffer_store_dwordx4 v[132:135],v25,s[32:35],s85 offen sc1
v_mfma_f32_16x16x32_bf16 a[204:207],a[164:167],v[236:239],a[204:207]
ds_read_b64_tr_b16 a[120:121],v45 offset:20512
ds_read_b64_tr_b16 a[122:123],v45 offset:25632
v_mfma_f32_16x16x32_bf16 a[204:207],a[168:171],v[248:251],a[204:207]
ds_read_b64_tr_b16 a[124:125],v45 offset:30752
ds_read_b64_tr_b16 a[126:127],v45 offset:35872
v_mfma_f32_16x16x32_bf16 a[204:207],a[172:175],v[252:255],a[204:207]
ds_read_b64_tr_b16 a[128:129],v1 offset:64
ds_read_b64_tr_b16 a[130:131],v1 offset:5184
v_mfma_f32_16x16x32_bf16 a[220:223],a[176:179],v[148:151],a[220:223]
buffer_store_dwordx4 v[144:147],v25,s[32:35],s86 offen sc1
v_mfma_f32_16x16x32_bf16 a[220:223],a[180:183],v[180:183],a[220:223]
ds_read_b64_tr_b16 a[132:133],v1 offset:10304
ds_read_b64_tr_b16 a[134:135],v1 offset:15424
v_mfma_f32_16x16x32_bf16 a[220:223],a[184:187],v[212:215],a[220:223]
ds_read_b64_tr_b16 a[136:137],v1 offset:20544
ds_read_b64_tr_b16 a[138:139],v1 offset:25664
v_mfma_f32_16x16x32_bf16 a[220:223],a[188:191],v[244:247],a[220:223]
ds_read_b64_tr_b16 a[140:141],v1 offset:30784
ds_read_b64_tr_b16 a[142:143],v1 offset:35904
v_mfma_f32_16x16x32_bf16 a[220:223],a[96:99],v[112:115],a[220:223]
ds_read_b64_tr_b16 a[144:145],v42 offset:64
ds_read_b64_tr_b16 a[146:147],v42 offset:5184
v_mfma_f32_16x16x32_bf16 a[220:223],a[100:103],v[116:119],a[220:223]
ds_read_b64_tr_b16 a[148:149],v42 offset:10304
ds_read_b64_tr_b16 a[150:151],v42 offset:15424
v_mfma_f32_16x16x32_bf16 a[220:223],a[104:107],v[120:123],a[220:223]
ds_read_b64_tr_b16 a[152:153],v42 offset:20544
ds_read_b64_tr_b16 a[154:155],v42 offset:25664
v_mfma_f32_16x16x32_bf16 a[220:223],a[108:111],v[124:127],a[220:223]
ds_read_b64_tr_b16 a[156:157],v42 offset:30784
ds_read_b64_tr_b16 a[158:159],v42 offset:35904
v_mfma_f32_16x16x32_bf16 a[220:223],a[112:115],v[232:235],a[220:223]
s_waitcnt lgkmcnt(14)
ds_read_b64_tr_b16 a[160:161],v44 offset:64
ds_read_b64_tr_b16 a[162:163],v44 offset:5184
v_mfma_f32_16x16x32_bf16 a[220:223],a[116:119],v[236:239],a[220:223]
ds_read_b64_tr_b16 a[164:165],v44 offset:10304
ds_read_b64_tr_b16 a[166:167],v44 offset:15424
v_mfma_f32_16x16x32_bf16 a[220:223],a[120:123],v[248:251],a[220:223]
ds_read_b64_tr_b16 a[168:169],v44 offset:20544
ds_read_b64_tr_b16 a[170:171],v44 offset:25664
v_mfma_f32_16x16x32_bf16 a[220:223],a[124:127],v[252:255],a[220:223]
ds_read_b64_tr_b16 a[172:173],v44 offset:30784
ds_read_b64_tr_b16 a[174:175],v44 offset:35904
v_mfma_f32_16x16x32_bf16 a[236:239],a[128:131],v[148:151],a[236:239]
s_waitcnt lgkmcnt(14)
ds_read_b64_tr_b16 a[176:177],v2 offset:96
ds_read_b64_tr_b16 a[178:179],v2 offset:5216
v_mfma_f32_16x16x32_bf16 a[236:239],a[132:135],v[180:183],a[236:239]
v_bfe_u32 v24,v6,4,2
v_min_u32 v24,2,v24
v_mfma_f32_16x16x32_bf16 a[236:239],a[136:139],v[212:215],a[236:239]
ds_read_b64_tr_b16 a[180:181],v2 offset:10336
ds_read_b64_tr_b16 a[182:183],v2 offset:15456
v_mfma_f32_16x16x32_bf16 a[236:239],a[140:143],v[244:247],a[236:239]
v_add_u32 v24,s70,v24
v_add_u32 v24,1,v24
v_mfma_f32_16x16x32_bf16 a[236:239],a[144:147],v[112:115],a[236:239]
s_waitcnt lgkmcnt(10)
ds_read_b64_tr_b16 a[184:185],v2 offset:20576
ds_read_b64_tr_b16 a[186:187],v2 offset:25696
v_mfma_f32_16x16x32_bf16 a[236:239],a[148:151],v[116:119],a[236:239]
v_and_b32 v24,3,v24
v_lshlrev_b32 v24,8,v24
v_mfma_f32_16x16x32_bf16 a[236:239],a[152:155],v[120:123],a[236:239]
ds_read_b64_tr_b16 a[188:189],v2 offset:30816
ds_read_b64_tr_b16 a[190:191],v2 offset:35936
v_mfma_f32_16x16x32_bf16 a[236:239],a[156:159],v[124:127],a[236:239]
ds_read_b64_tr_b16 a[96:97],v43 offset:96
ds_read_b64_tr_b16 a[98:99],v43 offset:5216
v_mfma_f32_16x16x32_bf16 a[236:239],a[160:163],v[232:235],a[236:239]
s_waitcnt lgkmcnt(8)
ds_read_b64_tr_b16 a[100:101],v43 offset:10336
ds_read_b64_tr_b16 a[102:103],v43 offset:15456
v_mfma_f32_16x16x32_bf16 a[236:239],a[164:167],v[236:239],a[236:239]
ds_read_b64_tr_b16 a[104:105],v43 offset:20576
ds_read_b64_tr_b16 a[106:107],v43 offset:25696
v_mfma_f32_16x16x32_bf16 a[236:239],a[168:171],v[248:251],a[236:239]
ds_read_b64_tr_b16 a[108:109],v43 offset:30816
ds_read_b64_tr_b16 a[110:111],v43 offset:35936
v_mfma_f32_16x16x32_bf16 a[236:239],a[172:175],v[252:255],a[236:239]
ds_read_b64_tr_b16 a[112:113],v45 offset:96
ds_read_b64_tr_b16 a[114:115],v45 offset:5216
v_mfma_f32_16x16x32_bf16 a[252:255],a[176:179],v[148:151],a[252:255]
s_waitcnt lgkmcnt(12)
ds_read_b64_tr_b16 a[116:117],v45 offset:10336
ds_read_b64_tr_b16 a[118:119],v45 offset:15456
v_mfma_f32_16x16x32_bf16 a[252:255],a[180:183],v[180:183],a[252:255]
ds_read_b64_tr_b16 a[120:121],v45 offset:20576
ds_read_b64_tr_b16 a[122:123],v45 offset:25696
v_mfma_f32_16x16x32_bf16 a[252:255],a[184:187],v[212:215],a[252:255]
s_waitcnt lgkmcnt(14)
ds_read_b64_tr_b16 a[124:125],v45 offset:30816
ds_read_b64_tr_b16 a[126:127],v45 offset:35936
v_mfma_f32_16x16x32_bf16 a[252:255],a[188:191],v[244:247],a[252:255]
s_waitcnt lgkmcnt(14)
ds_read_b64_tr_b16 a[128:129],v1 offset:128
ds_read_b64_tr_b16 a[130:131],v1 offset:5248
v_mfma_f32_16x16x32_bf16 a[252:255],a[96:99],v[112:115],a[252:255]
s_waitcnt lgkmcnt(14)
buffer_load_dwordx2 v[26:27],v24,s[64:67],s94 offen offset:2048 sc1
ds_read_b64_tr_b16 a[132:133],v1 offset:10368
ds_read_b64_tr_b16 a[134:135],v1 offset:15488
v_mfma_f32_16x16x32_bf16 a[252:255],a[100:103],v[116:119],a[252:255]
s_waitcnt lgkmcnt(14)
ds_read_b64_tr_b16 a[136:137],v1 offset:20608
ds_read_b64_tr_b16 a[138:139],v1 offset:25728
v_mfma_f32_16x16x32_bf16 a[252:255],a[104:107],v[120:123],a[252:255]
s_waitcnt lgkmcnt(14)
ds_read_b64_tr_b16 a[140:141],v1 offset:30848
ds_read_b64_tr_b16 a[142:143],v1 offset:35968
v_mfma_f32_16x16x32_bf16 a[252:255],a[108:111],v[124:127],a[252:255]
s_waitcnt lgkmcnt(14)
ds_read_b64_tr_b16 a[144:145],v42 offset:128
ds_read_b64_tr_b16 a[146:147],v42 offset:5248
v_mfma_f32_16x16x32_bf16 a[252:255],a[112:115],v[232:235],a[252:255]
s_waitcnt lgkmcnt(14)
ds_read_b64_tr_b16 a[148:149],v42 offset:10368
ds_read_b64_tr_b16 a[150:151],v42 offset:15488
v_mfma_f32_16x16x32_bf16 a[252:255],a[116:119],v[236:239],a[252:255]
s_lshl_b32 s95,s42,2
ds_read_b64_tr_b16 a[152:153],v42 offset:20608
ds_read_b64_tr_b16 a[154:155],v42 offset:25728
s_waitcnt lgkmcnt(15)
v_mfma_f32_16x16x32_bf16 a[252:255],a[120:123],v[248:251],a[252:255]
s_add_u32 s95,s95,s40
s_lshl_b32 s95,s95,4
ds_read_b64_tr_b16 a[156:157],v42 offset:30848
ds_read_b64_tr_b16 a[158:159],v42 offset:35968
v_mov_b32 v18,s68
v_mov_b32 v19,s69
s_waitcnt lgkmcnt(15)
v_mfma_f32_16x16x32_bf16 a[252:255],a[124:127],v[252:255],a[252:255]
s_waitcnt vmcnt(1)
s_mov_b64 exec,1
buffer_store_dwordx2 v[18:19],v6,s[64:67],s95 offen offset:3072 sc1
s_mov_b64 exec,-1
ds_read_b64_tr_b16 a[160:161],v44 offset:128
ds_read_b64_tr_b16 a[162:163],v44 offset:5248
s_waitcnt lgkmcnt(15)
v_mfma_f32_16x16x32_bf16 a[12:15],a[128:131],v[148:151],a[12:15]
s_waitcnt lgkmcnt(14)
ds_read_b64_tr_b16 a[164:165],v44 offset:10368
ds_read_b64_tr_b16 a[166:167],v44 offset:15488
v_mfma_f32_16x16x32_bf16 a[12:15],a[132:135],v[180:183],a[12:15]
)A"
R"A(s_waitcnt lgkmcnt(14)
ds_read_b64_tr_b16 a[168:169],v44 offset:20608
ds_read_b64_tr_b16 a[170:171],v44 offset:25728
v_mfma_f32_16x16x32_bf16 a[12:15],a[136:139],v[212:215],a[12:15]
s_waitcnt lgkmcnt(14)
ds_read_b64_tr_b16 a[172:173],v44 offset:30848
ds_read_b64_tr_b16 a[174:175],v44 offset:35968
v_mfma_f32_16x16x32_bf16 a[12:15],a[140:143],v[244:247],a[12:15]
s_waitcnt lgkmcnt(14)
ds_read_b64_tr_b16 a[176:177],v2 offset:160
ds_read_b64_tr_b16 a[178:179],v2 offset:5280
v_mfma_f32_16x16x32_bf16 a[12:15],a[144:147],v[112:115],a[12:15]
s_waitcnt lgkmcnt(14)
ds_read_b64_tr_b16 a[180:181],v2 offset:10400
ds_read_b64_tr_b16 a[182:183],v2 offset:15520
v_mfma_f32_16x16x32_bf16 a[12:15],a[148:151],v[116:119],a[12:15]
s_waitcnt lgkmcnt(14)
ds_read_b64_tr_b16 a[184:185],v2 offset:20640
ds_read_b64_tr_b16 a[186:187],v2 offset:25760
v_mfma_f32_16x16x32_bf16 a[12:15],a[152:155],v[120:123],a[12:15]
s_waitcnt lgkmcnt(14)
ds_read_b64_tr_b16 a[188:189],v2 offset:30880
ds_read_b64_tr_b16 a[190:191],v2 offset:36000
v_mfma_f32_16x16x32_bf16 a[12:15],a[156:159],v[124:127],a[12:15]
s_waitcnt lgkmcnt(14)
ds_read_b64_tr_b16 a[96:97],v43 offset:160
ds_read_b64_tr_b16 a[98:99],v43 offset:5280
v_mfma_f32_16x16x32_bf16 a[12:15],a[160:163],v[232:235],a[12:15]
s_waitcnt lgkmcnt(14)
ds_read_b64_tr_b16 a[100:101],v43 offset:10400
ds_read_b64_tr_b16 a[102:103],v43 offset:15520
v_mfma_f32_16x16x32_bf16 a[12:15],a[164:167],v[236:239],a[12:15]
s_waitcnt lgkmcnt(14)
ds_read_b64_tr_b16 a[104:105],v43 offset:20640
ds_read_b64_tr_b16 a[106:107],v43 offset:25760
v_mfma_f32_16x16x32_bf16 a[12:15],a[168:171],v[248:251],a[12:15]
s_waitcnt lgkmcnt(14)
ds_read_b64_tr_b16 a[108:109],v43 offset:30880
ds_read_b64_tr_b16 a[110:111],v43 offset:36000
v_mfma_f32_16x16x32_bf16 a[12:15],a[172:175],v[252:255],a[12:15]
s_waitcnt lgkmcnt(14)
ds_read_b64_tr_b16 a[112:113],v45 offset:160
ds_read_b64_tr_b16 a[114:115],v45 offset:5280
v_mfma_f32_16x16x32_bf16 a[28:31],a[176:179],v[148:151],a[28:31]
s_waitcnt lgkmcnt(14)
ds_read_b64_tr_b16 a[116:117],v45 offset:10400
ds_read_b64_tr_b16 a[118:119],v45 offset:15520
v_mfma_f32_16x16x32_bf16 a[28:31],a[180:183],v[180:183],a[28:31]
s_waitcnt lgkmcnt(14)
ds_read_b64_tr_b16 a[120:121],v45 offset:20640
ds_read_b64_tr_b16 a[122:123],v45 offset:25760
v_mfma_f32_16x16x32_bf16 a[28:31],a[184:187],v[212:215],a[28:31]
s_waitcnt lgkmcnt(14)
ds_read_b64_tr_b16 a[124:125],v45 offset:30880
ds_read_b64_tr_b16 a[126:127],v45 offset:36000
v_mfma_f32_16x16x32_bf16 a[28:31],a[188:191],v[244:247],a[28:31]
s_waitcnt lgkmcnt(14)
ds_read_b64_tr_b16 a[128:129],v1 offset:192
ds_read_b64_tr_b16 a[130:131],v1 offset:5312
v_mfma_f32_16x16x32_bf16 a[28:31],a[96:99],v[112:115],a[28:31]
s_waitcnt lgkmcnt(14)
ds_read_b64_tr_b16 a[132:133],v1 offset:10432
ds_read_b64_tr_b16 a[134:135],v1 offset:15552
v_mfma_f32_16x16x32_bf16 a[28:31],a[100:103],v[116:119],a[28:31]
s_waitcnt lgkmcnt(14)
ds_read_b64_tr_b16 a[136:137],v1 offset:20672
ds_read_b64_tr_b16 a[138:139],v1 offset:25792
v_mfma_f32_16x16x32_bf16 a[28:31],a[104:107],v[120:123],a[28:31]
s_waitcnt lgkmcnt(14)
ds_read_b64_tr_b16 a[140:141],v1 offset:30912
ds_read_b64_tr_b16 a[142:143],v1 offset:36032
v_mfma_f32_16x16x32_bf16 a[28:31],a[108:111],v[124:127],a[28:31]
s_waitcnt lgkmcnt(14)
ds_read_b64_tr_b16 a[144:145],v42 offset:192
ds_read_b64_tr_b16 a[146:147],v42 offset:5312
v_mfma_f32_16x16x32_bf16 a[28:31],a[112:115],v[232:235],a[28:31]
s_waitcnt lgkmcnt(14)
ds_read_b64_tr_b16 a[148:149],v42 offset:10432
ds_read_b64_tr_b16 a[150:151],v42 offset:15552
v_mfma_f32_16x16x32_bf16 a[28:31],a[116:119],v[236:239],a[28:31]
s_waitcnt lgkmcnt(14)
ds_read_b64_tr_b16 a[152:153],v42 offset:20672
ds_read_b64_tr_b16 a[154:155],v42 offset:25792
v_mfma_f32_16x16x32_bf16 a[28:31],a[120:123],v[248:251],a[28:31]
s_waitcnt lgkmcnt(14)
ds_read_b64_tr_b16 a[156:157],v42 offset:30912
ds_read_b64_tr_b16 a[158:159],v42 offset:36032
v_mfma_f32_16x16x32_bf16 a[28:31],a[124:127],v[252:255],a[28:31]
s_waitcnt lgkmcnt(14)
ds_read_b64_tr_b16 a[160:161],v44 offset:192
ds_read_b64_tr_b16 a[162:163],v44 offset:5312
v_mfma_f32_16x16x32_bf16 a[44:47],a[128:131],v[148:151],a[44:47]
ds_read_b64_tr_b16 a[164:165],v44 offset:10432
ds_read_b64_tr_b16 a[166:167],v44 offset:15552
s_waitcnt vmcnt(1)
v_cmp_ne_u32 s[90:91],s68,v26
v_cmp_ne_u32 s[92:93],s69,v27
s_nop 0
s_or_b64 s[90:91],s[90:91],s[92:93]
s_cbranch_scc1 L_fr10_%=
L_fo10_%=:
s_waitcnt lgkmcnt(15)
v_mfma_f32_16x16x32_bf16 a[44:47],a[132:135],v[180:183],a[44:47]
s_waitcnt lgkmcnt(14)
buffer_load_dwordx4 v[64:67],v6,s[32:35],s76 offen sc1
ds_read_b64_tr_b16 a[168:169],v44 offset:20672
ds_read_b64_tr_b16 a[170:171],v44 offset:25792
v_mfma_f32_16x16x32_bf16 a[44:47],a[136:139],v[212:215],a[44:47]
s_lshl_b32 s94,s71,2
ds_read_b64_tr_b16 a[172:173],v44 offset:30912
ds_read_b64_tr_b16 a[174:175],v44 offset:36032
s_waitcnt lgkmcnt(15)
v_mfma_f32_16x16x32_bf16 a[44:47],a[140:143],v[244:247],a[44:47]
s_add_u32 s94,s94,s40
ds_read_b64_tr_b16 a[176:177],v2 offset:224
ds_read_b64_tr_b16 a[178:179],v2 offset:5344
s_waitcnt lgkmcnt(15)
v_mfma_f32_16x16x32_bf16 a[44:47],a[144:147],v[112:115],a[44:47]
s_lshl_b32 s94,s94,4
ds_read_b64_tr_b16 a[180:181],v2 offset:10464
ds_read_b64_tr_b16 a[182:183],v2 offset:15584
s_waitcnt lgkmcnt(15)
v_mfma_f32_16x16x32_bf16 a[44:47],a[148:151],v[116:119],a[44:47]
s_waitcnt lgkmcnt(14)
buffer_load_dwordx4 v[80:83],v6,s[32:35],s77 offen sc1
ds_read_b64_tr_b16 a[184:185],v2 offset:20704
ds_read_b64_tr_b16 a[186:187],v2 offset:25824
v_mfma_f32_16x16x32_bf16 a[44:47],a[152:155],v[120:123],a[44:47]
s_waitcnt lgkmcnt(14)
ds_read_b64_tr_b16 a[188:189],v2 offset:30944
ds_read_b64_tr_b16 a[190:191],v2 offset:36064
v_mfma_f32_16x16x32_bf16 a[44:47],a[156:159],v[124:127],a[44:47]
s_waitcnt lgkmcnt(14)
ds_read_b64_tr_b16 a[96:97],v43 offset:224
ds_read_b64_tr_b16 a[98:99],v43 offset:5344
v_mfma_f32_16x16x32_bf16 a[44:47],a[160:163],v[232:235],a[44:47]
s_waitcnt lgkmcnt(14)
ds_read_b64_tr_b16 a[100:101],v43 offset:10464
ds_read_b64_tr_b16 a[102:103],v43 offset:15584
v_mfma_f32_16x16x32_bf16 a[44:47],a[164:167],v[236:239],a[44:47]
s_waitcnt lgkmcnt(14)
buffer_load_dwordx4 v[96:99],v6,s[32:35],s78 offen sc1
ds_read_b64_tr_b16 a[104:105],v43 offset:20704
ds_read_b64_tr_b16 a[106:107],v43 offset:25824
v_mfma_f32_16x16x32_bf16 a[44:47],a[168:171],v[248:251],a[44:47]
s_waitcnt lgkmcnt(14)
ds_read_b64_tr_b16 a[108:109],v43 offset:30944
ds_read_b64_tr_b16 a[110:111],v43 offset:36064
v_mfma_f32_16x16x32_bf16 a[44:47],a[172:175],v[252:255],a[44:47]
s_waitcnt lgkmcnt(14)
ds_read_b64_tr_b16 a[112:113],v45 offset:224
ds_read_b64_tr_b16 a[114:115],v45 offset:5344
v_mfma_f32_16x16x32_bf16 a[60:63],a[176:179],v[148:151],a[60:63]
s_waitcnt lgkmcnt(14)
ds_read_b64_tr_b16 a[116:117],v45 offset:10464
ds_read_b64_tr_b16 a[118:119],v45 offset:15584
v_mfma_f32_16x16x32_bf16 a[60:63],a[180:183],v[180:183],a[60:63]
s_waitcnt lgkmcnt(14)
buffer_load_dwordx4 v[68:71],v6,s[32:35],s76 offen offset:1024 sc1
ds_read_b64_tr_b16 a[120:121],v45 offset:20704
ds_read_b64_tr_b16 a[122:123],v45 offset:25824
v_mfma_f32_16x16x32_bf16 a[60:63],a[184:187],v[212:215],a[60:63]
s_waitcnt lgkmcnt(14)
ds_read_b64_tr_b16 a[124:125],v45 offset:30944
ds_read_b64_tr_b16 a[126:127],v45 offset:36064
v_mfma_f32_16x16x32_bf16 a[60:63],a[188:191],v[244:247],a[60:63]
s_waitcnt lgkmcnt(14)
ds_read_b64_tr_b16 a[128:129],v1 offset:256
ds_read_b64_tr_b16 a[130:131],v1 offset:5376
v_mfma_f32_16x16x32_bf16 a[60:63],a[96:99],v[112:115],a[60:63]
s_waitcnt lgkmcnt(14)
ds_read_b64_tr_b16 a[132:133],v1 offset:10496
ds_read_b64_tr_b16 a[134:135],v1 offset:15616
v_mfma_f32_16x16x32_bf16 a[60:63],a[100:103],v[116:119],a[60:63]
s_waitcnt lgkmcnt(14)
buffer_load_dwordx4 v[84:87],v6,s[32:35],s77 offen offset:1024 sc1
ds_read_b64_tr_b16 a[136:137],v1 offset:20736
ds_read_b64_tr_b16 a[138:139],v1 offset:25856
v_mfma_f32_16x16x32_bf16 a[60:63],a[104:107],v[120:123],a[60:63]
s_waitcnt lgkmcnt(14)
ds_read_b64_tr_b16 a[140:141],v1 offset:30976
ds_read_b64_tr_b16 a[142:143],v1 offset:36096
v_mfma_f32_16x16x32_bf16 a[60:63],a[108:111],v[124:127],a[60:63]
s_waitcnt lgkmcnt(14)
ds_read_b64_tr_b16 a[144:145],v42 offset:256
ds_read_b64_tr_b16 a[146:147],v42 offset:5376
v_mfma_f32_16x16x32_bf16 a[60:63],a[112:115],v[232:235],a[60:63]
s_waitcnt lgkmcnt(14)
ds_read_b64_tr_b16 a[148:149],v42 offset:10496
ds_read_b64_tr_b16 a[150:151],v42 offset:15616
v_mfma_f32_16x16x32_bf16 a[60:63],a[116:119],v[236:239],a[60:63]
s_waitcnt lgkmcnt(14)
buffer_load_dwordx4 v[100:103],v6,s[32:35],s78 offen offset:1024 sc1
ds_read_b64_tr_b16 a[152:153],v42 offset:20736
ds_read_b64_tr_b16 a[154:155],v42 offset:25856
v_mfma_f32_16x16x32_bf16 a[60:63],a[120:123],v[248:251],a[60:63]
s_waitcnt lgkmcnt(14)
ds_read_b64_tr_b16 a[156:157],v42 offset:30976
ds_read_b64_tr_b16 a[158:159],v42 offset:36096
v_mfma_f32_16x16x32_bf16 a[60:63],a[124:127],v[252:255],a[60:63]
s_waitcnt lgkmcnt(14)
ds_read_b64_tr_b16 a[160:161],v44 offset:256
ds_read_b64_tr_b16 a[162:163],v44 offset:5376
v_mfma_f32_16x16x32_bf16 a[76:79],a[128:131],v[148:151],a[76:79]
s_waitcnt lgkmcnt(14)
ds_read_b64_tr_b16 a[164:165],v44 offset:10496
ds_read_b64_tr_b16 a[166:167],v44 offset:15616
v_mfma_f32_16x16x32_bf16 a[76:79],a[132:135],v[180:183],a[76:79]
s_waitcnt lgkmcnt(14)
buffer_load_dwordx4 v[72:75],v6,s[32:35],s76 offen offset:2048 sc1
ds_read_b64_tr_b16 a[168:169],v44 offset:20736
ds_read_b64_tr_b16 a[170:171],v44 offset:25856
v_bfe_u32 v24,v6,4,2
v_min_u32 v24,2,v24
v_mfma_f32_16x16x32_bf16 a[76:79],a[136:139],v[212:215],a[76:79]
s_waitcnt lgkmcnt(14)
ds_read_b64_tr_b16 a[172:173],v44 offset:30976
ds_read_b64_tr_b16 a[174:175],v44 offset:36096
v_add_u32 v24,s70,v24
v_add_u32 v24,1,v24
v_mfma_f32_16x16x32_bf16 a[76:79],a[140:143],v[244:247],a[76:79]
s_waitcnt lgkmcnt(14)
ds_read_b64_tr_b16 a[176:177],v2 offset:288
ds_read_b64_tr_b16 a[178:179],v2 offset:5408
v_and_b32 v24,3,v24
v_lshlrev_b32 v24,8,v24
v_mfma_f32_16x16x32_bf16 a[76:79],a[144:147],v[112:115],a[76:79]
s_waitcnt lgkmcnt(14)
ds_read_b64_tr_b16 a[180:181],v2 offset:10528
ds_read_b64_tr_b16 a[182:183],v2 offset:15648
v_mfma_f32_16x16x32_bf16 a[76:79],a[148:151],v[116:119],a[76:79]
s_waitcnt lgkmcnt(14)
buffer_load_dwordx4 v[88:91],v6,s[32:35],s77 offen offset:2048 sc1
ds_read_b64_tr_b16 a[184:185],v2 offset:20768
ds_read_b64_tr_b16 a[186:187],v2 offset:25888
v_mfma_f32_16x16x32_bf16 a[76:79],a[152:155],v[120:123],a[76:79]
s_waitcnt lgkmcnt(14)
ds_read_b64_tr_b16 a[188:189],v2 offset:31008
ds_read_b64_tr_b16 a[190:191],v2 offset:36128
v_mfma_f32_16x16x32_bf16 a[76:79],a[156:159],v[124:127],a[76:79]
s_waitcnt lgkmcnt(14)
ds_read_b64_tr_b16 a[96:97],v43 offset:288
ds_read_b64_tr_b16 a[98:99],v43 offset:5408
v_mfma_f32_16x16x32_bf16 a[76:79],a[160:163],v[232:235],a[76:79]
s_waitcnt lgkmcnt(14)
ds_read_b64_tr_b16 a[100:101],v43 offset:10528
ds_read_b64_tr_b16 a[102:103],v43 offset:15648
v_mfma_f32_16x16x32_bf16 a[76:79],a[164:167],v[236:239],a[76:79]
s_waitcnt lgkmcnt(14)
buffer_load_dwordx4 v[104:107],v6,s[32:35],s78 offen offset:2048 sc1
ds_read_b64_tr_b16 a[104:105],v43 offset:20768
ds_read_b64_tr_b16 a[106:107],v43 offset:25888
v_mfma_f32_16x16x32_bf16 a[76:79],a[168:171],v[248:251],a[76:79]
s_waitcnt lgkmcnt(14)
ds_read_b64_tr_b16 a[108:109],v43 offset:31008
ds_read_b64_tr_b16 a[110:111],v43 offset:36128
v_mfma_f32_16x16x32_bf16 a[76:79],a[172:175],v[252:255],a[76:79]
s_waitcnt lgkmcnt(14)
ds_read_b64_tr_b16 a[112:113],v45 offset:288
ds_read_b64_tr_b16 a[114:115],v45 offset:5408
v_mfma_f32_16x16x32_bf16 a[92:95],a[176:179],v[148:151],a[92:95]
s_waitcnt lgkmcnt(14)
ds_read_b64_tr_b16 a[116:117],v45 offset:10528
ds_read_b64_tr_b16 a[118:119],v45 offset:15648
v_mfma_f32_16x16x32_bf16 a[92:95],a[180:183],v[180:183],a[92:95]
s_waitcnt lgkmcnt(14)
buffer_load_dwordx4 v[76:79],v6,s[32:35],s76 offen offset:3072 sc1
ds_read_b64_tr_b16 a[120:121],v45 offset:20768
ds_read_b64_tr_b16 a[122:123],v45 offset:25888
v_mfma_f32_16x16x32_bf16 a[92:95],a[184:187],v[212:215],a[92:95]
s_waitcnt lgkmcnt(14)
ds_read_b64_tr_b16 a[124:125],v45 offset:31008
ds_read_b64_tr_b16 a[126:127],v45 offset:36128
)A"
R"A(v_mfma_f32_16x16x32_bf16 a[92:95],a[188:191],v[244:247],a[92:95]
s_waitcnt lgkmcnt(14)
v_mfma_f32_16x16x32_bf16 a[92:95],a[96:99],v[112:115],a[92:95]
s_waitcnt lgkmcnt(12)
v_mfma_f32_16x16x32_bf16 a[92:95],a[100:103],v[116:119],a[92:95]
s_waitcnt lgkmcnt(10)
buffer_load_dwordx4 v[92:95],v6,s[32:35],s77 offen offset:3072 sc1
v_mfma_f32_16x16x32_bf16 a[92:95],a[104:107],v[120:123],a[92:95]
s_waitcnt lgkmcnt(8)
v_mfma_f32_16x16x32_bf16 a[92:95],a[108:111],v[124:127],a[92:95]
s_waitcnt lgkmcnt(6)
v_mfma_f32_16x16x32_bf16 a[92:95],a[112:115],v[232:235],a[92:95]
s_waitcnt lgkmcnt(4)
v_mfma_f32_16x16x32_bf16 a[92:95],a[116:119],v[236:239],a[92:95]
s_waitcnt lgkmcnt(2)
buffer_load_dwordx4 v[108:111],v6,s[32:35],s78 offen offset:3072 sc1
v_mfma_f32_16x16x32_bf16 a[92:95],a[120:123],v[248:251],a[92:95]
s_waitcnt lgkmcnt(0)
v_mfma_f32_16x16x32_bf16 a[92:95],a[124:127],v[252:255],a[92:95]
buffer_load_dwordx2 v[26:27],v24,s[64:67],s94 offen offset:3072 sc1
s_waitcnt vmcnt(0)
v_cmp_ne_u32 s[90:91],s68,v26
v_cmp_ne_u32 s[92:93],s69,v27
s_nop 0
s_or_b64 s[90:91],s[90:91],s[92:93]
s_cbranch_scc1 L_fr11_%=
L_fo11_%=:
buffer_load_dwordx4 v[136:139],v25,s[32:35],s76 offen sc1
buffer_load_dwordx4 v[168:171],v25,s[32:35],s77 offen sc1
buffer_load_dwordx4 v[200:203],v25,s[32:35],s78 offen sc1
v_mfma_f32_16x16x32_bf16 a[204:207],v[34:37],v[64:67],a[204:207]
v_mfma_f32_16x16x32_bf16 a[204:207],v[34:37],v[80:83],a[204:207]
v_mfma_f32_16x16x32_bf16 a[204:207],v[34:37],v[96:99],a[204:207]
v_mfma_f32_16x16x32_bf16 a[220:223],v[32:35],v[64:67],a[220:223]
v_mfma_f32_16x16x32_bf16 a[220:223],v[32:35],v[80:83],a[220:223]
v_mfma_f32_16x16x32_bf16 a[220:223],v[32:35],v[96:99],a[220:223]
v_mfma_f32_16x16x32_bf16 a[236:239],v[34:37],v[68:71],a[236:239]
v_mfma_f32_16x16x32_bf16 a[236:239],v[34:37],v[84:87],a[236:239]
v_mfma_f32_16x16x32_bf16 a[236:239],v[34:37],v[100:103],a[236:239]
s_nop 1
v_accvgpr_read_b32 v128,a204
v_accvgpr_read_b32 v129,a205
v_mfma_f32_16x16x32_bf16 a[252:255],v[32:35],v[68:71],a[252:255]
v_cvt_pk_bf16_f32 v128,v128,v129
v_accvgpr_read_b32 v130,a206
v_mfma_f32_16x16x32_bf16 a[252:255],v[32:35],v[84:87],a[252:255]
v_accvgpr_read_b32 v131,a207
v_cvt_pk_bf16_f32 v129,v130,v131
v_mfma_f32_16x16x32_bf16 a[252:255],v[32:35],v[100:103],a[252:255]
buffer_store_dwordx2 v[128:129],v40,s[20:23],s43 offen
v_mfma_f32_16x16x32_bf16 a[12:15],v[34:37],v[72:75],a[12:15]
v_accvgpr_read_b32 v130,a220
v_accvgpr_read_b32 v131,a221
v_mfma_f32_16x16x32_bf16 a[12:15],v[34:37],v[88:91],a[12:15]
v_accvgpr_read_b32 v14,a222
v_accvgpr_read_b32 v15,a223
v_mfma_f32_16x16x32_bf16 a[12:15],v[34:37],v[104:107],a[12:15]
v_cvt_pk_bf16_f32 v130,v130,v131
v_cvt_pk_bf16_f32 v131,v14,v15
v_mfma_f32_16x16x32_bf16 a[28:31],v[32:35],v[72:75],a[28:31]
buffer_store_dwordx2 v[130:131],v40,s[20:23],s43 offen offset:32
v_mfma_f32_16x16x32_bf16 a[28:31],v[32:35],v[88:91],a[28:31]
v_accvgpr_read_b32 v132,a236
v_accvgpr_read_b32 v133,a237
v_mfma_f32_16x16x32_bf16 a[28:31],v[32:35],v[104:107],a[28:31]
v_cvt_pk_bf16_f32 v132,v132,v133
v_accvgpr_read_b32 v134,a238
v_mfma_f32_16x16x32_bf16 a[44:47],v[34:37],v[76:79],a[44:47]
v_accvgpr_read_b32 v135,a239
v_cvt_pk_bf16_f32 v133,v134,v135
v_mfma_f32_16x16x32_bf16 a[44:47],v[34:37],v[92:95],a[44:47]
buffer_store_dwordx2 v[132:133],v40,s[20:23],s43 offen offset:64
v_mfma_f32_16x16x32_bf16 a[44:47],v[34:37],v[108:111],a[44:47]
v_accvgpr_read_b32 v144,a12
v_accvgpr_read_b32 v145,a13
v_cvt_pk_bf16_f32 v144,v144,v145
v_accvgpr_read_b32 v146,a14
v_accvgpr_read_b32 v147,a15
v_mfma_f32_16x16x32_bf16 a[60:63],v[32:35],v[76:79],a[60:63]
v_accvgpr_read_b32 v134,a252
v_accvgpr_read_b32 v135,a253
v_cvt_pk_bf16_f32 v145,v146,v147
v_mfma_f32_16x16x32_bf16 a[60:63],v[32:35],v[92:95],a[60:63]
v_accvgpr_read_b32 v16,a254
v_accvgpr_read_b32 v17,a255
v_cvt_pk_bf16_f32 v134,v134,v135
v_cvt_pk_bf16_f32 v135,v16,v17
v_mfma_f32_16x16x32_bf16 a[60:63],v[32:35],v[108:111],a[60:63]
s_waitcnt vmcnt(5)
buffer_store_dwordx2 v[134:135],v40,s[20:23],s43 offen offset:96
buffer_store_dwordx2 v[144:145],v40,s[20:23],s43 offen offset:128
v_accvgpr_read_b32 v146,a28
v_accvgpr_read_b32 v147,a29
v_accvgpr_read_b32 v12,a30
v_accvgpr_read_b32 v13,a31
v_cvt_pk_bf16_f32 v146,v146,v147
v_cvt_pk_bf16_f32 v147,v12,v13
s_nop 3
buffer_store_dwordx2 v[146:147],v40,s[20:23],s43 offen offset:160
v_accvgpr_read_b32 v160,a44
v_accvgpr_read_b32 v161,a45
v_cvt_pk_bf16_f32 v160,v160,v161
v_accvgpr_read_b32 v162,a46
v_accvgpr_read_b32 v163,a47
v_cvt_pk_bf16_f32 v161,v162,v163
buffer_store_dwordx2 v[160:161],v40,s[20:23],s43 offen offset:192
v_accvgpr_read_b32 v162,a60
v_accvgpr_read_b32 v163,a61
v_accvgpr_read_b32 v14,a62
v_accvgpr_read_b32 v15,a63
v_cvt_pk_bf16_f32 v162,v162,v163
v_cvt_pk_bf16_f32 v163,v14,v15
buffer_store_dwordx2 v[162:163],v40,s[20:23],s43 offen offset:224
v_mfma_f32_16x16x32_bf16 a[76:79],v[34:37],v[136:139],a[76:79]
s_waitcnt vmcnt(9)
v_mfma_f32_16x16x32_bf16 a[76:79],v[34:37],v[168:171],a[76:79]
s_waitcnt vmcnt(8)
v_mfma_f32_16x16x32_bf16 a[76:79],v[34:37],v[200:203],a[76:79]
v_mfma_f32_16x16x32_bf16 a[92:95],v[32:35],v[136:139],a[92:95]
v_mfma_f32_16x16x32_bf16 a[92:95],v[32:35],v[168:171],a[92:95]
v_mfma_f32_16x16x32_bf16 a[92:95],v[32:35],v[200:203],a[92:95]
s_nop 3
s_nop 0
v_accvgpr_read_b32 v164,a76
v_accvgpr_read_b32 v165,a77
v_cvt_pk_bf16_f32 v164,v164,v165
v_accvgpr_read_b32 v166,a78
v_accvgpr_read_b32 v167,a79
v_cvt_pk_bf16_f32 v165,v166,v167
buffer_store_dwordx2 v[164:165],v40,s[20:23],s43 offen offset:256
v_accvgpr_read_b32 v166,a92
v_accvgpr_read_b32 v167,a93
v_accvgpr_read_b32 v16,a94
v_accvgpr_read_b32 v17,a95
v_cvt_pk_bf16_f32 v166,v166,v167
v_cvt_pk_bf16_f32 v167,v16,v17
buffer_store_dwordx2 v[166:167],v40,s[20:23],s43 offen offset:288
s_or_b32 s62,s42,s40
s_cmp_lg_u32 s62,0
s_cbranch_scc1 L_noep_%=
v_mov_b32 v12,s68
s_mov_b64 exec,1
buffer_store_dword v12,v25,s[64:67],0 offen sc1
s_mov_b64 exec,-1
L_noep_%=:
s_endpgm
L_ps2_%=:
s_min_i32 s79,s79,100
s_add_u32 s78,s78,s79
L_pl2_%=:
s_sleep 1
s_sub_u32 s79,s79,4
s_cmp_gt_i32 s79,1
s_cbranch_scc1 L_pl2_%=
s_branch L_pr2_%=
L_ps3_%=:
s_min_i32 s79,s79,100
s_add_u32 s78,s78,s79
L_pl3_%=:
s_sleep 1
s_sub_u32 s79,s79,4
s_cmp_gt_i32 s79,1
s_cbranch_scc1 L_pl3_%=
s_branch L_pr3_%=
L_ps4_%=:
s_min_i32 s79,s79,100
s_add_u32 s78,s78,s79
L_pl4_%=:
s_sleep 1
s_sub_u32 s79,s79,4
s_cmp_gt_i32 s79,1
s_cbranch_scc1 L_pl4_%=
s_branch L_pr4_%=
L_ps5_%=:
s_min_i32 s79,s79,100
s_add_u32 s78,s78,s79
L_pl5_%=:
s_sleep 1
s_sub_u32 s79,s79,4
s_cmp_gt_i32 s79,1
s_cbranch_scc1 L_pl5_%=
s_branch L_pr5_%=
L_ps6_%=:
s_min_i32 s79,s79,100
s_add_u32 s78,s78,s79
L_pl6_%=:
s_sleep 1
s_sub_u32 s79,s79,4
s_cmp_gt_i32 s79,1
s_cbranch_scc1 L_pl6_%=
s_branch L_pr6_%=
L_ps7_%=:
s_min_i32 s79,s79,100
s_add_u32 s78,s78,s79
L_pl7_%=:
s_sleep 1
s_sub_u32 s79,s79,4
s_cmp_gt_i32 s79,1
s_cbranch_scc1 L_pl7_%=
s_branch L_pr7_%=
L_fr8_%=:
s_mov_b32 s88,262144
L_fl8_%=:
buffer_load_dwordx2 v[26:27],v24,s[64:67],s94 offen sc1
s_waitcnt vmcnt(0)
s_sub_u32 s88,s88,1
s_cmp_eq_u32 s88,0
s_cbranch_scc1 L_fo8_%=
v_cmp_ne_u32 s[90:91],s68,v26
v_cmp_ne_u32 s[92:93],s69,v27
s_nop 0
s_or_b64 s[90:91],s[90:91],s[92:93]
s_cbranch_scc1 L_fl8_%=
s_branch L_fo8_%=
L_fr9_%=:
s_mov_b32 s88,262144
L_fl9_%=:
buffer_load_dwordx2 v[26:27],v24,s[64:67],s94 offen offset:1024 sc1
s_waitcnt vmcnt(0)
s_sub_u32 s88,s88,1
s_cmp_eq_u32 s88,0
s_cbranch_scc1 L_fo9_%=
v_cmp_ne_u32 s[90:91],s68,v26
v_cmp_ne_u32 s[92:93],s69,v27
s_nop 0
s_or_b64 s[90:91],s[90:91],s[92:93]
s_cbranch_scc1 L_fl9_%=
s_branch L_fo9_%=
L_fr10_%=:
s_mov_b32 s88,262144
L_fl10_%=:
buffer_load_dwordx2 v[26:27],v24,s[64:67],s94 offen offset:2048 sc1
s_waitcnt vmcnt(0)
s_sub_u32 s88,s88,1
s_cmp_eq_u32 s88,0
s_cbranch_scc1 L_fo10_%=
v_cmp_ne_u32 s[90:91],s68,v26
v_cmp_ne_u32 s[92:93],s69,v27
s_nop 0
s_or_b64 s[90:91],s[90:91],s[92:93]
s_cbranch_scc1 L_fl10_%=
s_branch L_fo10_%=
L_fr11_%=:
s_mov_b32 s88,262144
L_fl11_%=:
buffer_load_dwordx2 v[26:27],v24,s[64:67],s94 offen offset:3072 sc1
s_waitcnt vmcnt(0)
s_sub_u32 s88,s88,1
s_cmp_eq_u32 s88,0
s_cbranch_scc1 L_fo11_%=
v_cmp_ne_u32 s[90:91],s68,v26
v_cmp_ne_u32 s[92:93],s69,v27
s_nop 0
s_or_b64 s[90:91],s[90:91],s[92:93]
s_cbranch_scc1 L_fl11_%=
s_branch L_fo11_%=
L_out_end_%=:
L_fast_end_%=:
L_slow0_%=:
s_mov_b32 s98,0
s_branch L_slowc_%=
L_slow1_%=:
s_mov_b32 s98,1
L_slowc_%=:
s_nop 7
s_nop 7
s_waitcnt vmcnt(0) lgkmcnt(0)
s_lshl_b32 s96,%1,2
s_add_u32 s96,s96,s40
s_lshl_b32 s96,s96,15
s_add_u32 s96,s96,167772160
s_mov_b64 s[56:57],s[12:13]
s_add_u32 s56,s56,s96
s_addc_u32 s57,s57,0
s_and_b32 s57,s57,0xffff
s_mov_b32 s58,32768
s_mov_b32 s59,0x20000
s_mov_b32 s96,0
buffer_store_dwordx4 v[64:67],v6,s[56:59],s96 offen
buffer_store_dwordx4 v[68:71],v6,s[56:59],s96 offen offset:1024
buffer_store_dwordx4 v[72:75],v6,s[56:59],s96 offen offset:2048
buffer_store_dwordx4 v[76:79],v6,s[56:59],s96 offen offset:3072
s_mov_b32 s96,4096
buffer_store_dwordx4 v[80:83],v6,s[56:59],s96 offen
buffer_store_dwordx4 v[84:87],v6,s[56:59],s96 offen offset:1024
buffer_store_dwordx4 v[88:91],v6,s[56:59],s96 offen offset:2048
buffer_store_dwordx4 v[92:95],v6,s[56:59],s96 offen offset:3072
s_mov_b32 s96,8192
buffer_store_dwordx4 v[96:99],v6,s[56:59],s96 offen
buffer_store_dwordx4 v[100:103],v6,s[56:59],s96 offen offset:1024
buffer_store_dwordx4 v[104:107],v6,s[56:59],s96 offen offset:2048
buffer_store_dwordx4 v[108:111],v6,s[56:59],s96 offen offset:3072
s_mov_b32 s96,12288
buffer_store_dwordx4 v[112:115],v6,s[56:59],s96 offen
buffer_store_dwordx4 v[116:119],v6,s[56:59],s96 offen offset:1024
buffer_store_dwordx4 v[120:123],v6,s[56:59],s96 offen offset:2048
buffer_store_dwordx4 v[124:127],v6,s[56:59],s96 offen offset:3072
s_mov_b32 s96,16384
buffer_store_dwordx4 v[128:131],v6,s[56:59],s96 offen
buffer_store_dwordx4 v[132:135],v6,s[56:59],s96 offen offset:1024
buffer_store_dwordx4 v[136:139],v6,s[56:59],s96 offen offset:2048
buffer_store_dwordx4 v[140:143],v6,s[56:59],s96 offen offset:3072
s_mov_b32 s96,20480
buffer_store_dwordx4 v[144:147],v6,s[56:59],s96 offen
buffer_store_dwordx4 v[148:151],v6,s[56:59],s96 offen offset:1024
buffer_store_dwordx4 v[152:155],v6,s[56:59],s96 offen offset:2048
buffer_store_dwordx4 v[156:159],v6,s[56:59],s96 offen offset:3072
s_mov_b32 s96,24576
buffer_store_dwordx4 v[160:163],v6,s[56:59],s96 offen
buffer_store_dwordx4 v[164:167],v6,s[56:59],s96 offen offset:1024
buffer_store_dwordx4 v[168:171],v6,s[56:59],s96 offen offset:2048
buffer_store_dwordx4 v[172:175],v6,s[56:59],s96 offen offset:3072
s_mov_b32 s96,28672
buffer_store_dwordx4 v[176:179],v6,s[56:59],s96 offen
buffer_store_dwordx4 v[180:183],v6,s[56:59],s96 offen offset:1024
buffer_store_dwordx4 v[184:187],v6,s[56:59],s96 offen offset:2048
buffer_store_dwordx4 v[188:191],v6,s[56:59],s96 offen offset:3072
s_waitcnt vmcnt(0)
v_writelane_b32 v173,s88,0
v_writelane_b32 v173,s89,1
v_writelane_b32 v173,s90,2
v_writelane_b32 v173,s91,3
v_writelane_b32 v173,s92,4
v_writelane_b32 v173,s93,5
v_writelane_b32 v173,s94,6
v_writelane_b32 v173,s95,7
s_mov_b32 s88,s98
s_mul_i32 s90,s41,307200
s_mov_b64 s[92:93],s[4:5]
s_add_u32 s92,s92,s90
s_addc_u32 s93,s93,0
s_and_b32 s93,s93,0xffff
s_mov_b32 s94,307200
s_mov_b32 s95,0x20000
s_mov_b64 s[96:97],s[10:11]
s_and_b32 s97,s97,0xffff
s_mov_b32 s98,5242880
s_mov_b32 s99,0x20000
v_bfe_u32 v171,v6,4,4
v_lshrrev_b32 v172,8,v6
v_lshlrev_b32 v164,8,v171
v_lshl_add_u32 v164,v172,4,v164
v_lshlrev_b32 v165,10,v172
v_lshl_add_u32 v165,v171,1,v165
v_add_u32 v166,4096,v165
v_lshlrev_b32 v168,16,v172
v_lshrrev_b32 v170,2,v171
v_lshl_add_u32 v168,v170,4,v168
v_and_b32 v170,3,v171
v_lshl_add_u32 v168,v170,1,v168
s_lshl_b32 s90,s42,1
s_add_u32 s90,s90,s88
s_lshl_b32 s90,s90,8
v_add_u32 v168,s90,v168
s_mov_b32 s89,0
L_sjb_%=:
s_add_u32 s90,s89,s70
s_add_u32 s90,s90,1
s_and_b32 s90,s90,3
s_lshl_b32 s91,s40,2
s_add_u32 s90,s90,s91
s_lshl_b32 s90,s90,4
v_bfe_u32 v171,v6,4,4
v_lshrrev_b32 v172,8,v6
v_add_u32 v167,s90,v171
v_mul_u32_u24 v167,1280,v167
v_lshl_add_u32 v167,v172,4,v167
s_lshl_b32 s90,s88,4
s_add_u32 s90,s90,s89
s_lshl_b32 s90,s90,12
v_add_u32 v169,s90,v6
)A"
R"A(v_mov_b32 v64,0
v_mov_b32 v65,0
v_mov_b32 v66,0
v_mov_b32 v67,0
v_mov_b32 v68,0
v_mov_b32 v69,0
v_mov_b32 v70,0
v_mov_b32 v71,0
v_mov_b32 v72,0
v_mov_b32 v73,0
v_mov_b32 v74,0
v_mov_b32 v75,0
v_mov_b32 v76,0
v_mov_b32 v77,0
v_mov_b32 v78,0
v_mov_b32 v79,0
v_mov_b32 v80,0
v_mov_b32 v81,0
v_mov_b32 v82,0
v_mov_b32 v83,0
v_mov_b32 v84,0
v_mov_b32 v85,0
v_mov_b32 v86,0
v_mov_b32 v87,0
v_mov_b32 v88,0
v_mov_b32 v89,0
v_mov_b32 v90,0
v_mov_b32 v91,0
v_mov_b32 v92,0
v_mov_b32 v93,0
v_mov_b32 v94,0
v_mov_b32 v95,0
s_mov_b32 s90,0
L_sq_%=:
s_lshl_b32 s91,s90,6
buffer_load_dwordx4 v[160:163],v167,s[92:95],s91 offen
s_lshl_b32 s91,s90,18
buffer_load_ushort v112,v168,s[96:99],s91 offen
buffer_load_ushort v120,v168,s[96:99],s91 offen offset:8
buffer_load_ushort v128,v168,s[96:99],s91 offen offset:64
buffer_load_ushort v136,v168,s[96:99],s91 offen offset:72
s_add_u32 s91,s91,8192
buffer_load_ushort v113,v168,s[96:99],s91 offen
buffer_load_ushort v121,v168,s[96:99],s91 offen offset:8
buffer_load_ushort v129,v168,s[96:99],s91 offen offset:64
buffer_load_ushort v137,v168,s[96:99],s91 offen offset:72
s_add_u32 s91,s91,8192
buffer_load_ushort v114,v168,s[96:99],s91 offen
buffer_load_ushort v122,v168,s[96:99],s91 offen offset:8
buffer_load_ushort v130,v168,s[96:99],s91 offen offset:64
buffer_load_ushort v138,v168,s[96:99],s91 offen offset:72
s_add_u32 s91,s91,8192
buffer_load_ushort v115,v168,s[96:99],s91 offen
buffer_load_ushort v123,v168,s[96:99],s91 offen offset:8
buffer_load_ushort v131,v168,s[96:99],s91 offen offset:64
buffer_load_ushort v139,v168,s[96:99],s91 offen offset:72
s_add_u32 s91,s91,8192
buffer_load_ushort v116,v168,s[96:99],s91 offen
buffer_load_ushort v124,v168,s[96:99],s91 offen offset:8
buffer_load_ushort v132,v168,s[96:99],s91 offen offset:64
buffer_load_ushort v140,v168,s[96:99],s91 offen offset:72
s_add_u32 s91,s91,8192
buffer_load_ushort v117,v168,s[96:99],s91 offen
buffer_load_ushort v125,v168,s[96:99],s91 offen offset:8
buffer_load_ushort v133,v168,s[96:99],s91 offen offset:64
buffer_load_ushort v141,v168,s[96:99],s91 offen offset:72
s_add_u32 s91,s91,8192
buffer_load_ushort v118,v168,s[96:99],s91 offen
buffer_load_ushort v126,v168,s[96:99],s91 offen offset:8
buffer_load_ushort v134,v168,s[96:99],s91 offen offset:64
buffer_load_ushort v142,v168,s[96:99],s91 offen offset:72
s_add_u32 s91,s91,8192
buffer_load_ushort v119,v168,s[96:99],s91 offen
buffer_load_ushort v127,v168,s[96:99],s91 offen offset:8
buffer_load_ushort v135,v168,s[96:99],s91 offen offset:64
buffer_load_ushort v143,v168,s[96:99],s91 offen offset:72
s_add_u32 s91,s91,8192
s_waitcnt vmcnt(0)
v_lshl_or_b32 v112,v113,16,v112
v_lshl_or_b32 v113,v115,16,v114
v_lshl_or_b32 v114,v117,16,v116
v_lshl_or_b32 v115,v119,16,v118
v_lshl_or_b32 v120,v121,16,v120
v_lshl_or_b32 v121,v123,16,v122
v_lshl_or_b32 v122,v125,16,v124
v_lshl_or_b32 v123,v127,16,v126
v_lshl_or_b32 v128,v129,16,v128
v_lshl_or_b32 v129,v131,16,v130
v_lshl_or_b32 v130,v133,16,v132
v_lshl_or_b32 v131,v135,16,v134
v_lshl_or_b32 v136,v137,16,v136
v_lshl_or_b32 v137,v139,16,v138
v_lshl_or_b32 v138,v141,16,v140
v_lshl_or_b32 v139,v143,16,v142
s_nop 1
v_mfma_f32_16x16x32_bf16 v[64:67],v[112:115],v[160:163],v[64:67]
v_mfma_f32_16x16x32_bf16 v[68:71],v[120:123],v[160:163],v[68:71]
v_mfma_f32_16x16x32_bf16 v[72:75],v[128:131],v[160:163],v[72:75]
v_mfma_f32_16x16x32_bf16 v[76:79],v[136:139],v[160:163],v[76:79]
s_nop 7
s_lshl_b32 s91,s90,18
buffer_load_ushort v112,v168,s[96:99],s91 offen offset:128
buffer_load_ushort v120,v168,s[96:99],s91 offen offset:136
buffer_load_ushort v128,v168,s[96:99],s91 offen offset:192
buffer_load_ushort v136,v168,s[96:99],s91 offen offset:200
s_add_u32 s91,s91,8192
buffer_load_ushort v113,v168,s[96:99],s91 offen offset:128
buffer_load_ushort v121,v168,s[96:99],s91 offen offset:136
buffer_load_ushort v129,v168,s[96:99],s91 offen offset:192
buffer_load_ushort v137,v168,s[96:99],s91 offen offset:200
s_add_u32 s91,s91,8192
buffer_load_ushort v114,v168,s[96:99],s91 offen offset:128
buffer_load_ushort v122,v168,s[96:99],s91 offen offset:136
buffer_load_ushort v130,v168,s[96:99],s91 offen offset:192
buffer_load_ushort v138,v168,s[96:99],s91 offen offset:200
s_add_u32 s91,s91,8192
buffer_load_ushort v115,v168,s[96:99],s91 offen offset:128
buffer_load_ushort v123,v168,s[96:99],s91 offen offset:136
buffer_load_ushort v131,v168,s[96:99],s91 offen offset:192
buffer_load_ushort v139,v168,s[96:99],s91 offen offset:200
s_add_u32 s91,s91,8192
buffer_load_ushort v116,v168,s[96:99],s91 offen offset:128
buffer_load_ushort v124,v168,s[96:99],s91 offen offset:136
buffer_load_ushort v132,v168,s[96:99],s91 offen offset:192
buffer_load_ushort v140,v168,s[96:99],s91 offen offset:200
s_add_u32 s91,s91,8192
buffer_load_ushort v117,v168,s[96:99],s91 offen offset:128
buffer_load_ushort v125,v168,s[96:99],s91 offen offset:136
buffer_load_ushort v133,v168,s[96:99],s91 offen offset:192
buffer_load_ushort v141,v168,s[96:99],s91 offen offset:200
s_add_u32 s91,s91,8192
buffer_load_ushort v118,v168,s[96:99],s91 offen offset:128
buffer_load_ushort v126,v168,s[96:99],s91 offen offset:136
buffer_load_ushort v134,v168,s[96:99],s91 offen offset:192
buffer_load_ushort v142,v168,s[96:99],s91 offen offset:200
s_add_u32 s91,s91,8192
buffer_load_ushort v119,v168,s[96:99],s91 offen offset:128
buffer_load_ushort v127,v168,s[96:99],s91 offen offset:136
buffer_load_ushort v135,v168,s[96:99],s91 offen offset:192
buffer_load_ushort v143,v168,s[96:99],s91 offen offset:200
s_add_u32 s91,s91,8192
s_waitcnt vmcnt(0)
v_lshl_or_b32 v112,v113,16,v112
v_lshl_or_b32 v113,v115,16,v114
v_lshl_or_b32 v114,v117,16,v116
v_lshl_or_b32 v115,v119,16,v118
v_lshl_or_b32 v120,v121,16,v120
v_lshl_or_b32 v121,v123,16,v122
v_lshl_or_b32 v122,v125,16,v124
v_lshl_or_b32 v123,v127,16,v126
v_lshl_or_b32 v128,v129,16,v128
v_lshl_or_b32 v129,v131,16,v130
v_lshl_or_b32 v130,v133,16,v132
v_lshl_or_b32 v131,v135,16,v134
v_lshl_or_b32 v136,v137,16,v136
v_lshl_or_b32 v137,v139,16,v138
v_lshl_or_b32 v138,v141,16,v140
v_lshl_or_b32 v139,v143,16,v142
s_nop 1
v_mfma_f32_16x16x32_bf16 v[80:83],v[112:115],v[160:163],v[80:83]
v_mfma_f32_16x16x32_bf16 v[84:87],v[120:123],v[160:163],v[84:87]
v_mfma_f32_16x16x32_bf16 v[88:91],v[128:131],v[160:163],v[88:91]
v_mfma_f32_16x16x32_bf16 v[92:95],v[136:139],v[160:163],v[92:95]
s_nop 7
s_add_u32 s90,s90,1
s_cmp_lg_u32 s90,20
s_cbranch_scc1 L_sq_%=
s_nop 7
v_mul_f32 v64,s61,v64
v_mul_f32 v65,s61,v65
v_mul_f32 v66,s61,v66
v_mul_f32 v67,s61,v67
v_mul_f32 v68,s61,v68
v_mul_f32 v69,s61,v69
v_mul_f32 v70,s61,v70
v_mul_f32 v71,s61,v71
v_mul_f32 v72,s61,v72
v_mul_f32 v73,s61,v73
v_mul_f32 v74,s61,v74
v_mul_f32 v75,s61,v75
v_mul_f32 v76,s61,v76
v_mul_f32 v77,s61,v77
v_mul_f32 v78,s61,v78
v_mul_f32 v79,s61,v79
v_mul_f32 v80,s61,v80
v_mul_f32 v81,s61,v81
v_mul_f32 v82,s61,v82
v_mul_f32 v83,s61,v83
v_mul_f32 v84,s61,v84
v_mul_f32 v85,s61,v85
v_mul_f32 v86,s61,v86
v_mul_f32 v87,s61,v87
v_mul_f32 v88,s61,v88
v_mul_f32 v89,s61,v89
v_mul_f32 v90,s61,v90
v_mul_f32 v91,s61,v91
v_mul_f32 v92,s61,v92
v_mul_f32 v93,s61,v93
v_mul_f32 v94,s61,v94
v_mul_f32 v95,s61,v95
v_cvt_pk_bf16_f32 v96,v64,v65
v_cvt_pk_bf16_f32 v97,v66,v67
v_cvt_pk_bf16_f32 v98,v68,v69
v_cvt_pk_bf16_f32 v99,v70,v71
v_cvt_pk_bf16_f32 v100,v72,v73
v_cvt_pk_bf16_f32 v101,v74,v75
v_cvt_pk_bf16_f32 v102,v76,v77
v_cvt_pk_bf16_f32 v103,v78,v79
v_cvt_pk_bf16_f32 v104,v80,v81
v_cvt_pk_bf16_f32 v105,v82,v83
v_cvt_pk_bf16_f32 v106,v84,v85
v_cvt_pk_bf16_f32 v107,v86,v87
v_cvt_pk_bf16_f32 v108,v88,v89
v_cvt_pk_bf16_f32 v109,v90,v91
v_cvt_pk_bf16_f32 v110,v92,v93
v_cvt_pk_bf16_f32 v111,v94,v95
v_mov_b32 v170,0xff800000
s_lshl_b32 s91,s88,18
s_mov_b32 s90,64
L_s1_%=:
buffer_load_dwordx4 v[112:115],v164,s[20:23],s91 offen
buffer_load_dwordx4 v[116:119],v164,s[20:23],s91 offen offset:64
buffer_load_dwordx4 v[120:123],v164,s[20:23],s91 offen offset:128
buffer_load_dwordx4 v[124:127],v164,s[20:23],s91 offen offset:192
s_waitcnt vmcnt(0)
v_mfma_f32_16x16x32_bf16 v[144:147],v[112:115],v[96:99],0
v_mfma_f32_16x16x32_bf16 v[144:147],v[116:119],v[100:103],v[144:147]
v_mfma_f32_16x16x32_bf16 v[144:147],v[120:123],v[104:107],v[144:147]
v_mfma_f32_16x16x32_bf16 v[144:147],v[124:127],v[108:111],v[144:147]
s_nop 7
v_max3_f32 v170,v170,v144,v145
v_max3_f32 v170,v170,v146,v147
s_add_u32 s91,s91,4096
s_sub_u32 s90,s90,1
s_cmp_lg_u32 s90,0
s_cbranch_scc1 L_s1_%=
v_mov_b32 v171,v170
s_nop 1
v_permlane32_swap_b32 v170,v171
v_max_f32 v170,v170,v171
v_mov_b32 v171,v170
s_nop 1
v_permlane16_swap_b32 v170,v171
v_max_f32 v170,v170,v171
v_sub_f32 v152,0,v170
v_mov_b32 v156,0
v_sub_f32 v153,0,v170
v_mov_b32 v157,0
v_sub_f32 v154,0,v170
v_mov_b32 v158,0
v_sub_f32 v155,0,v170
v_mov_b32 v159,0
v_mov_b32 v64,0
v_mov_b32 v65,0
v_mov_b32 v66,0
v_mov_b32 v67,0
v_mov_b32 v68,0
v_mov_b32 v69,0
v_mov_b32 v70,0
v_mov_b32 v71,0
v_mov_b32 v72,0
v_mov_b32 v73,0
v_mov_b32 v74,0
v_mov_b32 v75,0
v_mov_b32 v76,0
v_mov_b32 v77,0
v_mov_b32 v78,0
v_mov_b32 v79,0
v_mov_b32 v80,0
v_mov_b32 v81,0
v_mov_b32 v82,0
v_mov_b32 v83,0
v_mov_b32 v84,0
v_mov_b32 v85,0
v_mov_b32 v86,0
v_mov_b32 v87,0
v_mov_b32 v88,0
v_mov_b32 v89,0
v_mov_b32 v90,0
v_mov_b32 v91,0
v_mov_b32 v92,0
v_mov_b32 v93,0
v_mov_b32 v94,0
v_mov_b32 v95,0
s_lshl_b32 s91,s88,18
s_mov_b32 s90,32
v_add_u32 v172,4096,v164
L_s2_%=:
buffer_load_dwordx4 v[112:115],v164,s[20:23],s91 offen
buffer_load_dwordx4 v[116:119],v164,s[20:23],s91 offen offset:64
buffer_load_dwordx4 v[120:123],v164,s[20:23],s91 offen offset:128
buffer_load_dwordx4 v[124:127],v164,s[20:23],s91 offen offset:192
buffer_load_dwordx4 v[128:131],v172,s[20:23],s91 offen
buffer_load_dwordx4 v[132:135],v172,s[20:23],s91 offen offset:64
buffer_load_dwordx4 v[136:139],v172,s[20:23],s91 offen offset:128
buffer_load_dwordx4 v[140:143],v172,s[20:23],s91 offen offset:192
s_waitcnt vmcnt(0)
v_mfma_f32_16x16x32_bf16 v[144:147],v[112:115],v[96:99],v[152:155]
v_mfma_f32_16x16x32_bf16 v[144:147],v[116:119],v[100:103],v[144:147]
v_mfma_f32_16x16x32_bf16 v[144:147],v[120:123],v[104:107],v[144:147]
v_mfma_f32_16x16x32_bf16 v[144:147],v[124:127],v[108:111],v[144:147]
v_mfma_f32_16x16x32_bf16 v[148:151],v[128:131],v[96:99],v[152:155]
v_mfma_f32_16x16x32_bf16 v[148:151],v[132:135],v[100:103],v[148:151]
v_mfma_f32_16x16x32_bf16 v[148:151],v[136:139],v[104:107],v[148:151]
v_mfma_f32_16x16x32_bf16 v[148:151],v[140:143],v[108:111],v[148:151]
s_nop 7
v_exp_f32 v144,v144
v_exp_f32 v145,v145
v_exp_f32 v146,v146
v_exp_f32 v147,v147
v_exp_f32 v148,v148
v_exp_f32 v149,v149
v_exp_f32 v150,v150
v_exp_f32 v151,v151
s_nop 0
v_cvt_pk_bf16_f32 v144,v144,v145
v_cvt_pk_bf16_f32 v145,v146,v147
v_cvt_pk_bf16_f32 v146,v148,v149
v_cvt_pk_bf16_f32 v147,v150,v151
buffer_load_ushort v112,v165,s[24:27],s91 offen
buffer_load_ushort v113,v165,s[24:27],s91 offen offset:256
buffer_load_ushort v114,v165,s[24:27],s91 offen offset:512
buffer_load_ushort v115,v165,s[24:27],s91 offen offset:768
buffer_load_ushort v116,v166,s[24:27],s91 offen
buffer_load_ushort v117,v166,s[24:27],s91 offen offset:256
buffer_load_ushort v118,v166,s[24:27],s91 offen offset:512
buffer_load_ushort v119,v166,s[24:27],s91 offen offset:768
buffer_load_ushort v120,v165,s[24:27],s91 offen offset:32
buffer_load_ushort v121,v165,s[24:27],s91 offen offset:288
buffer_load_ushort v122,v165,s[24:27],s91 offen offset:544
buffer_load_ushort v123,v165,s[24:27],s91 offen offset:800
buffer_load_ushort v124,v166,s[24:27],s91 offen offset:32
buffer_load_ushort v125,v166,s[24:27],s91 offen offset:288
buffer_load_ushort v126,v166,s[24:27],s91 offen offset:544
buffer_load_ushort v127,v166,s[24:27],s91 offen offset:800
buffer_load_ushort v128,v165,s[24:27],s91 offen offset:64
buffer_load_ushort v129,v165,s[24:27],s91 offen offset:320
buffer_load_ushort v130,v165,s[24:27],s91 offen offset:576
buffer_load_ushort v131,v165,s[24:27],s91 offen offset:832
buffer_load_ushort v132,v166,s[24:27],s91 offen offset:64
buffer_load_ushort v133,v166,s[24:27],s91 offen offset:320
buffer_load_ushort v134,v166,s[24:27],s91 offen offset:576
buffer_load_ushort v135,v166,s[24:27],s91 offen offset:832
)A"
R"A(buffer_load_ushort v136,v165,s[24:27],s91 offen offset:96
buffer_load_ushort v137,v165,s[24:27],s91 offen offset:352
buffer_load_ushort v138,v165,s[24:27],s91 offen offset:608
buffer_load_ushort v139,v165,s[24:27],s91 offen offset:864
buffer_load_ushort v140,v166,s[24:27],s91 offen offset:96
buffer_load_ushort v141,v166,s[24:27],s91 offen offset:352
buffer_load_ushort v142,v166,s[24:27],s91 offen offset:608
buffer_load_ushort v143,v166,s[24:27],s91 offen offset:864
s_waitcnt vmcnt(0)
v_lshl_or_b32 v112,v113,16,v112
v_lshl_or_b32 v113,v115,16,v114
v_lshl_or_b32 v114,v117,16,v116
v_lshl_or_b32 v115,v119,16,v118
v_lshl_or_b32 v120,v121,16,v120
v_lshl_or_b32 v121,v123,16,v122
v_lshl_or_b32 v122,v125,16,v124
v_lshl_or_b32 v123,v127,16,v126
v_lshl_or_b32 v128,v129,16,v128
v_lshl_or_b32 v129,v131,16,v130
v_lshl_or_b32 v130,v133,16,v132
v_lshl_or_b32 v131,v135,16,v134
v_lshl_or_b32 v136,v137,16,v136
v_lshl_or_b32 v137,v139,16,v138
v_lshl_or_b32 v138,v141,16,v140
v_lshl_or_b32 v139,v143,16,v142
s_nop 1
v_mfma_f32_16x16x32_bf16 v[64:67],v[112:115],v[144:147],v[64:67]
v_mfma_f32_16x16x32_bf16 v[68:71],v[120:123],v[144:147],v[68:71]
v_mfma_f32_16x16x32_bf16 v[72:75],v[128:131],v[144:147],v[72:75]
v_mfma_f32_16x16x32_bf16 v[76:79],v[136:139],v[144:147],v[76:79]
s_nop 7
buffer_load_ushort v112,v165,s[24:27],s91 offen offset:128
buffer_load_ushort v113,v165,s[24:27],s91 offen offset:384
buffer_load_ushort v114,v165,s[24:27],s91 offen offset:640
buffer_load_ushort v115,v165,s[24:27],s91 offen offset:896
buffer_load_ushort v116,v166,s[24:27],s91 offen offset:128
buffer_load_ushort v117,v166,s[24:27],s91 offen offset:384
buffer_load_ushort v118,v166,s[24:27],s91 offen offset:640
buffer_load_ushort v119,v166,s[24:27],s91 offen offset:896
buffer_load_ushort v120,v165,s[24:27],s91 offen offset:160
buffer_load_ushort v121,v165,s[24:27],s91 offen offset:416
buffer_load_ushort v122,v165,s[24:27],s91 offen offset:672
buffer_load_ushort v123,v165,s[24:27],s91 offen offset:928
buffer_load_ushort v124,v166,s[24:27],s91 offen offset:160
buffer_load_ushort v125,v166,s[24:27],s91 offen offset:416
buffer_load_ushort v126,v166,s[24:27],s91 offen offset:672
buffer_load_ushort v127,v166,s[24:27],s91 offen offset:928
buffer_load_ushort v128,v165,s[24:27],s91 offen offset:192
buffer_load_ushort v129,v165,s[24:27],s91 offen offset:448
buffer_load_ushort v130,v165,s[24:27],s91 offen offset:704
buffer_load_ushort v131,v165,s[24:27],s91 offen offset:960
buffer_load_ushort v132,v166,s[24:27],s91 offen offset:192
buffer_load_ushort v133,v166,s[24:27],s91 offen offset:448
buffer_load_ushort v134,v166,s[24:27],s91 offen offset:704
buffer_load_ushort v135,v166,s[24:27],s91 offen offset:960
buffer_load_ushort v136,v165,s[24:27],s91 offen offset:224
buffer_load_ushort v137,v165,s[24:27],s91 offen offset:480
buffer_load_ushort v138,v165,s[24:27],s91 offen offset:736
buffer_load_ushort v139,v165,s[24:27],s91 offen offset:992
buffer_load_ushort v140,v166,s[24:27],s91 offen offset:224
buffer_load_ushort v141,v166,s[24:27],s91 offen offset:480
buffer_load_ushort v142,v166,s[24:27],s91 offen offset:736
buffer_load_ushort v143,v166,s[24:27],s91 offen offset:992
s_waitcnt vmcnt(0)
v_lshl_or_b32 v112,v113,16,v112
v_lshl_or_b32 v113,v115,16,v114
v_lshl_or_b32 v114,v117,16,v116
v_lshl_or_b32 v115,v119,16,v118
v_lshl_or_b32 v120,v121,16,v120
v_lshl_or_b32 v121,v123,16,v122
v_lshl_or_b32 v122,v125,16,v124
v_lshl_or_b32 v123,v127,16,v126
v_lshl_or_b32 v128,v129,16,v128
v_lshl_or_b32 v129,v131,16,v130
v_lshl_or_b32 v130,v133,16,v132
v_lshl_or_b32 v131,v135,16,v134
v_lshl_or_b32 v136,v137,16,v136
v_lshl_or_b32 v137,v139,16,v138
v_lshl_or_b32 v138,v141,16,v140
v_lshl_or_b32 v139,v143,16,v142
s_nop 1
v_mfma_f32_16x16x32_bf16 v[80:83],v[112:115],v[144:147],v[80:83]
v_mfma_f32_16x16x32_bf16 v[84:87],v[120:123],v[144:147],v[84:87]
v_mfma_f32_16x16x32_bf16 v[88:91],v[128:131],v[144:147],v[88:91]
v_mfma_f32_16x16x32_bf16 v[92:95],v[136:139],v[144:147],v[92:95]
s_nop 7
v_mfma_f32_16x16x32_bf16 v[156:159],v[28:31],v[144:147],v[156:159]
s_nop 7
s_add_u32 s91,s91,8192
s_sub_u32 s90,s90,1
s_cmp_lg_u32 s90,0
s_cbranch_scc1 L_s2_%=
v_rcp_f32 v170,v156
s_nop 0
v_mul_f32 v64,v170,v64
v_mul_f32 v65,v170,v65
v_mul_f32 v66,v170,v66
v_mul_f32 v67,v170,v67
v_mul_f32 v68,v170,v68
v_mul_f32 v69,v170,v69
v_mul_f32 v70,v170,v70
v_mul_f32 v71,v170,v71
v_mul_f32 v72,v170,v72
v_mul_f32 v73,v170,v73
v_mul_f32 v74,v170,v74
v_mul_f32 v75,v170,v75
v_mul_f32 v76,v170,v76
v_mul_f32 v77,v170,v77
v_mul_f32 v78,v170,v78
v_mul_f32 v79,v170,v79
v_mul_f32 v80,v170,v80
v_mul_f32 v81,v170,v81
v_mul_f32 v82,v170,v82
v_mul_f32 v83,v170,v83
v_mul_f32 v84,v170,v84
v_mul_f32 v85,v170,v85
v_mul_f32 v86,v170,v86
v_mul_f32 v87,v170,v87
v_mul_f32 v88,v170,v88
v_mul_f32 v89,v170,v89
v_mul_f32 v90,v170,v90
v_mul_f32 v91,v170,v91
v_mul_f32 v92,v170,v92
v_mul_f32 v93,v170,v93
v_mul_f32 v94,v170,v94
v_mul_f32 v95,v170,v95
v_cvt_pk_bf16_f32 v96,v64,v65
v_cvt_pk_bf16_f32 v97,v66,v67
v_cvt_pk_bf16_f32 v98,v68,v69
v_cvt_pk_bf16_f32 v99,v70,v71
v_cvt_pk_bf16_f32 v100,v72,v73
v_cvt_pk_bf16_f32 v101,v74,v75
v_cvt_pk_bf16_f32 v102,v76,v77
v_cvt_pk_bf16_f32 v103,v78,v79
v_cvt_pk_bf16_f32 v104,v80,v81
v_cvt_pk_bf16_f32 v105,v82,v83
v_cvt_pk_bf16_f32 v106,v84,v85
v_cvt_pk_bf16_f32 v107,v86,v87
v_cvt_pk_bf16_f32 v108,v88,v89
v_cvt_pk_bf16_f32 v109,v90,v91
v_cvt_pk_bf16_f32 v110,v92,v93
v_cvt_pk_bf16_f32 v111,v94,v95
buffer_store_dwordx4 v[96:99],v169,s[36:39],s55 offen sc1
buffer_store_dwordx4 v[100:103],v169,s[36:39],s55 offen offset:1024 sc1
buffer_store_dwordx4 v[104:107],v169,s[36:39],s55 offen offset:2048 sc1
buffer_store_dwordx4 v[108:111],v169,s[36:39],s55 offen offset:3072 sc1
s_waitcnt vmcnt(0)
s_add_u32 s89,s89,1
s_cmp_lg_u32 s89,4
s_cbranch_scc1 L_sjb_%=
s_mov_b32 s98,s88
v_readlane_b32 s88,v173,0
v_readlane_b32 s89,v173,1
v_readlane_b32 s90,v173,2
v_readlane_b32 s91,v173,3
v_readlane_b32 s92,v173,4
v_readlane_b32 s93,v173,5
v_readlane_b32 s94,v173,6
v_readlane_b32 s95,v173,7
s_nop 4
s_mov_b32 s96,0
buffer_load_dwordx4 v[64:67],v6,s[56:59],s96 offen sc1
buffer_load_dwordx4 v[68:71],v6,s[56:59],s96 offen offset:1024 sc1
buffer_load_dwordx4 v[72:75],v6,s[56:59],s96 offen offset:2048 sc1
buffer_load_dwordx4 v[76:79],v6,s[56:59],s96 offen offset:3072 sc1
s_mov_b32 s96,4096
buffer_load_dwordx4 v[80:83],v6,s[56:59],s96 offen sc1
buffer_load_dwordx4 v[84:87],v6,s[56:59],s96 offen offset:1024 sc1
buffer_load_dwordx4 v[88:91],v6,s[56:59],s96 offen offset:2048 sc1
buffer_load_dwordx4 v[92:95],v6,s[56:59],s96 offen offset:3072 sc1
s_mov_b32 s96,8192
buffer_load_dwordx4 v[96:99],v6,s[56:59],s96 offen sc1
buffer_load_dwordx4 v[100:103],v6,s[56:59],s96 offen offset:1024 sc1
buffer_load_dwordx4 v[104:107],v6,s[56:59],s96 offen offset:2048 sc1
buffer_load_dwordx4 v[108:111],v6,s[56:59],s96 offen offset:3072 sc1
s_mov_b32 s96,12288
buffer_load_dwordx4 v[112:115],v6,s[56:59],s96 offen sc1
buffer_load_dwordx4 v[116:119],v6,s[56:59],s96 offen offset:1024 sc1
buffer_load_dwordx4 v[120:123],v6,s[56:59],s96 offen offset:2048 sc1
buffer_load_dwordx4 v[124:127],v6,s[56:59],s96 offen offset:3072 sc1
s_mov_b32 s96,16384
buffer_load_dwordx4 v[128:131],v6,s[56:59],s96 offen sc1
buffer_load_dwordx4 v[132:135],v6,s[56:59],s96 offen offset:1024 sc1
buffer_load_dwordx4 v[136:139],v6,s[56:59],s96 offen offset:2048 sc1
buffer_load_dwordx4 v[140:143],v6,s[56:59],s96 offen offset:3072 sc1
s_mov_b32 s96,20480
buffer_load_dwordx4 v[144:147],v6,s[56:59],s96 offen sc1
buffer_load_dwordx4 v[148:151],v6,s[56:59],s96 offen offset:1024 sc1
buffer_load_dwordx4 v[152:155],v6,s[56:59],s96 offen offset:2048 sc1
buffer_load_dwordx4 v[156:159],v6,s[56:59],s96 offen offset:3072 sc1
s_mov_b32 s96,24576
buffer_load_dwordx4 v[160:163],v6,s[56:59],s96 offen sc1
buffer_load_dwordx4 v[164:167],v6,s[56:59],s96 offen offset:1024 sc1
buffer_load_dwordx4 v[168:171],v6,s[56:59],s96 offen offset:2048 sc1
buffer_load_dwordx4 v[172:175],v6,s[56:59],s96 offen offset:3072 sc1
s_mov_b32 s96,28672
buffer_load_dwordx4 v[176:179],v6,s[56:59],s96 offen sc1
buffer_load_dwordx4 v[180:183],v6,s[56:59],s96 offen offset:1024 sc1
buffer_load_dwordx4 v[184:187],v6,s[56:59],s96 offen offset:2048 sc1
buffer_load_dwordx4 v[188:191],v6,s[56:59],s96 offen offset:3072 sc1
s_waitcnt vmcnt(0)
s_cmp_lg_u32 s98,0
s_cbranch_scc1 L_sr1_%=
buffer_load_dwordx4 v[64:67],v6,s[36:39],s84 offen sc1
buffer_load_dwordx4 v[68:71],v6,s[36:39],s84 offen offset:1024 sc1
buffer_load_dwordx4 v[72:75],v6,s[36:39],s84 offen offset:2048 sc1
buffer_load_dwordx4 v[76:79],v6,s[36:39],s84 offen offset:3072 sc1
buffer_load_dwordx4 v[80:83],v6,s[36:39],s85 offen sc1
buffer_load_dwordx4 v[84:87],v6,s[36:39],s85 offen offset:1024 sc1
buffer_load_dwordx4 v[88:91],v6,s[36:39],s85 offen offset:2048 sc1
buffer_load_dwordx4 v[92:95],v6,s[36:39],s85 offen offset:3072 sc1
buffer_load_dwordx4 v[96:99],v6,s[36:39],s86 offen sc1
buffer_load_dwordx4 v[100:103],v6,s[36:39],s86 offen offset:1024 sc1
buffer_load_dwordx4 v[104:107],v6,s[36:39],s86 offen offset:2048 sc1
buffer_load_dwordx4 v[108:111],v6,s[36:39],s86 offen offset:3072 sc1
buffer_load_dwordx4 v[112:115],v6,s[36:39],s87 offen sc1
buffer_load_dwordx4 v[116:119],v6,s[36:39],s87 offen offset:1024 sc1
buffer_load_dwordx4 v[120:123],v6,s[36:39],s87 offen offset:2048 sc1
buffer_load_dwordx4 v[124:127],v6,s[36:39],s87 offen offset:3072 sc1
s_waitcnt vmcnt(0)
s_nop 7
s_branch L_sret0_%=
L_sr1_%=:
buffer_load_dwordx4 v[136:139],v6,s[36:39],s84 offen sc1
buffer_load_dwordx4 v[140:143],v6,s[36:39],s84 offen offset:1024 sc1
buffer_load_dwordx4 v[152:155],v6,s[36:39],s84 offen offset:2048 sc1
buffer_load_dwordx4 v[156:159],v6,s[36:39],s84 offen offset:3072 sc1
buffer_load_dwordx4 v[168:171],v6,s[36:39],s85 offen sc1
buffer_load_dwordx4 v[172:175],v6,s[36:39],s85 offen offset:1024 sc1
buffer_load_dwordx4 v[184:187],v6,s[36:39],s85 offen offset:2048 sc1
buffer_load_dwordx4 v[188:191],v6,s[36:39],s85 offen offset:3072 sc1
buffer_load_dwordx4 v[200:203],v6,s[36:39],s86 offen sc1
buffer_load_dwordx4 v[204:207],v6,s[36:39],s86 offen offset:1024 sc1
buffer_load_dwordx4 v[216:219],v6,s[36:39],s86 offen offset:2048 sc1
buffer_load_dwordx4 v[220:223],v6,s[36:39],s86 offen offset:3072 sc1
buffer_load_dwordx4 v[232:235],v6,s[36:39],s87 offen sc1
buffer_load_dwordx4 v[236:239],v6,s[36:39],s87 offen offset:1024 sc1
buffer_load_dwordx4 v[248:251],v6,s[36:39],s87 offen offset:2048 sc1
buffer_load_dwordx4 v[252:255],v6,s[36:39],s87 offen offset:3072 sc1
s_waitcnt vmcnt(0)
s_nop 7
s_branch L_sret1_%=
L_end_%=:
)A"
    :: "s"(karg), "s"(blk), "s"(wid)
    : "v0", "v1", "v2", "v3", "v4", "v5", "v6", "v7", "v8", "v9", "v10", "v11", "v12", "v13", "v14", "v15", "v16", "v17", "v18", "v19", "v20", "v21", "v22", "v23", "v24", "v25", "v26", "v27", "v28", "v29", "v30", "v31", "v32", "v33", "v34", "v35", "v36", "v37", "v38", "v39", "v40", "v41", "v42", "v43", "v44", "v45", "v46", "v47", "v48", "v49", "v50", "v51", "v52", "v53", "v54", "v55", "v56", "v57", "v58", "v59", "v60", "v61", "v62", "v63", "v64", "v65", "v66", "v67", "v68", "v69", "v70", "v71", "v72", "v73", "v74", "v75", "v76", "v77", "v78", "v79", "v80", "v81", "v82", "v83", "v84", "v85", "v86", "v87", "v88", "v89", "v90", "v91", "v92", "v93", "v94", "v95", "v96", "v97", "v98", "v99", "v100", "v101", "v102", "v103", "v104", "v105", "v106", "v107", "v108", "v109", "v110", "v111", "v112", "v113", "v114", "v115", "v116", "v117", "v118", "v119", "v120", "v121", "v122", "v123", "v124", "v125", "v126", "v127", "v128", "v129", "v130", "v131", "v132", "v133", "v134", "v135", "v136", "v137", "v138", "v139", "v140", "v141", "v142", "v143", "v144", "v145", "v146", "v147", "v148", "v149", "v150", "v151", "v152", "v153", "v154", "v155", "v156", "v157", "v158", "v159", "v160", "v161", "v162", "v163", "v164", "v165", "v166", "v167", "v168", "v169", "v170", "v171", "v172", "v173", "v174", "v175", "v176", "v177", "v178", "v179", "v180", "v181", "v182", "v183", "v184", "v185", "v186", "v187", "v188", "v189", "v190", "v191", "v192", "v193", "v194", "v195", "v196", "v197", "v198", "v199", "v200", "v201", "v202", "v203", "v204", "v205", "v206", "v207", "v208", "v209", "v210", "v211", "v212", "v213", "v214", "v215", "v216", "v217", "v218", "v219", "v220", "v221", "v222", "v223", "v224", "v225", "v226", "v227", "v228", "v229", "v230", "v231", "v232", "v233", "v234", "v235", "v236", "v237", "v238", "v239", "v240", "v241", "v242", "v243", "v244", "v245", "v246", "v247", "v248", "v249", "v250", "v251", "v252", "v253", "v254", "v255", "a0", "a1", "a2", "a3", "a4", "a5", "a6", "a7", "a8", "a9", "a10", "a11", "a12", "a13", "a14", "a15", "a16", "a17", "a18", "a19", "a20", "a21", "a22", "a23", "a24", "a25", "a26", "a27", "a28", "a29", "a30", "a31", "a32", "a33", "a34", "a35", "a36", "a37", "a38", "a39", "a40", "a41", "a42", "a43", "a44", "a45", "a46", "a47", "a48", "a49", "a50", "a51", "a52", "a53", "a54", "a55", "a56", "a57", "a58", "a59", "a60", "a61", "a62", "a63", "a64", "a65", "a66", "a67", "a68", "a69", "a70", "a71", "a72", "a73", "a74", "a75", "a76", "a77", "a78", "a79", "a80", "a81", "a82", "a83", "a84", "a85", "a86", "a87", "a88", "a89", "a90", "a91", "a92", "a93", "a94", "a95", "a96", "a97", "a98", "a99", "a100", "a101", "a102", "a103", "a104", "a105", "a106", "a107", "a108", "a109", "a110", "a111", "a112", "a113", "a114", "a115", "a116", "a117", "a118", "a119", "a120", "a121", "a122", "a123", "a124", "a125", "a126", "a127", "a128", "a129", "a130", "a131", "a132", "a133", "a134", "a135", "a136", "a137", "a138", "a139", "a140", "a141", "a142", "a143", "a144", "a145", "a146", "a147", "a148", "a149", "a150", "a151", "a152", "a153", "a154", "a155", "a156", "a157", "a158", "a159", "a160", "a161", "a162", "a163", "a164", "a165", "a166", "a167", "a168", "a169", "a170", "a171", "a172", "a173", "a174", "a175", "a176", "a177", "a178", "a179", "a180", "a181", "a182", "a183", "a184", "a185", "a186", "a187", "a188", "a189", "a190", "a191", "a192", "a193", "a194", "a195", "a196", "a197", "a198", "a199", "a200", "a201", "a202", "a203", "a204", "a205", "a206", "a207", "a208", "a209", "a210", "a211", "a212", "a213", "a214", "a215", "a216", "a217", "a218", "a219", "a220", "a221", "a222", "a223", "a224", "a225", "a226", "a227", "a228", "a229", "a230", "a231", "a232", "a233", "a234", "a235", "a236", "a237", "a238", "a239", "a240", "a241", "a242", "a243", "a244", "a245", "a246", "a247", "a248", "a249", "a250", "a251", "a252", "a253", "a254", "a255", "s4", "s5", "s6", "s7", "s8", "s9", "s10", "s11", "s12", "s13", "s14", "s15", "s16", "s17", "s18", "s19", "s20", "s21", "s22", "s23", "s24", "s25", "s26", "s27", "s28", "s29", "s30", "s31", "s32", "s33", "s34", "s35", "s36", "s37", "s38", "s39", "s40", "s41", "s42", "s43", "s44", "s45", "s46", "s47", "s48", "s49", "s50", "s51", "s52", "s53", "s54", "s55", "s56", "s57", "s58", "s59", "s60", "s61", "s62", "s63", "s64", "s65", "s66", "s67", "s68", "s69", "s70", "s71", "s72", "s73", "s74", "s75", "s76", "s77", "s78", "s79", "s80", "s81", "s82", "s83", "s84", "s85", "s86", "s87", "s88", "s89", "s90", "s91", "s92", "s93", "s94", "s95", "s96", "s97", "s98", "s99", "s100", "s101", "vcc", "scc", "memory");
#endif
}

extern "C" void kernel_launch(void* const* d_in, const int* in_sizes, int n_in,
                              void* d_out, int out_size, void* d_ws, size_t ws_size,
                              hipStream_t stream) {
  (void)hipFuncSetAttribute((const void*)qattn_kernel, hipFuncAttributeMaxDynamicSharedMemorySize, LDS_ATT);
  qattn_kernel<<<256, 256, LDS_ATT, stream>>>(d_in[0], d_in[1], d_in[2], d_in[3], d_ws, d_in[4], d_out);
}
```

```cpp
#include <hip/hip_runtime.h>
#include <hip/hip_bf16.h>

constexpr int LDS_ATT = 163840;

__global__ __launch_bounds__(256, 1) void qattn_kernel(const void* __restrict__ x, const void* __restrict__ kc, const void* __restrict__ vc,
                                                       const void* __restrict__ wq, void* __restrict__ ws, const void* __restrict__ wo,
                                                       void* __restrict__ out) {
#if defined(__HIP_DEVICE_COMPILE__)
  const auto karg = __builtin_amdgcn_kernarg_segment_ptr();
  const unsigned blk = blockIdx.x;
  const unsigned wid = __builtin_amdgcn_readfirstlane(threadIdx.x >> 6);
  asm volatile(
R"A(s_memtime s[72:73]
s_memrealtime s[74:75]
s_load_dwordx8 s[4:11],%0,0x0
s_load_dwordx4 s[12:15],%0,0x20
s_load_dwordx2 s[16:17],%0,0x30
s_mov_b32 s40,%2
s_mov_b32 s43,%1
s_lshr_b32 s45,s43,3
v_mbcnt_lo_u32_b32 v12,-1,0
v_mbcnt_hi_u32_b32 v12,-1,v12
s_getpc_b64 s[88:89]
L_pc_%=:
s_and_b32 s88,s88,0xffffff80
s_and_b32 s89,s89,0xffff
s_mov_b32 s90,L_fast_end_%=-L_pc_%=
s_mov_b32 s91,0x20000
s_lshl_b32 s44,s45,2
s_add_u32 s44,s44,s40
s_mul_i32 s44,s44,896
v_lshlrev_b32 v67,7,v12
s_mov_b64 exec,127
buffer_load_dword v19,v67,s[88:91],s44 offen
s_mov_b64 exec,-1
s_and_b32 s70,s43,3
s_and_b32 s71,s45,3
s_lshl_b32 s42,s70,2
s_add_u32 s42,s42,s71
s_and_b32 s41,s43,4
s_lshl_b32 s41,s41,1
s_lshr_b32 s46,s45,2
s_add_u32 s41,s41,s46
s_lshl_b32 s56,s42,9
s_mov_b32 s57,0
s_lshl_b32 s59,s40,13
s_add_u32 s58,s59,65536
v_and_b32 v64,3,v12
v_bfe_u32 v65,v12,2,2
v_lshrrev_b32 v66,4,v12
v_lshrrev_b32 v75,3,v12
s_lshl_b32 s44,s40,6
v_add_u32 v75,s44,v75
v_mul_u32_u24 v75,1280,v75
v_and_b32 v76,7,v12
s_add_u32 s84,s70,1
s_and_b32 s84,s84,3
s_mul_i32 s84,s84,20480
s_add_u32 s84,s84,4096
s_add_u32 s85,s70,2
s_and_b32 s85,s85,3
s_mul_i32 s85,s85,20480
s_add_u32 s85,s85,2048
s_add_u32 s86,s70,3
s_and_b32 s86,s86,3
s_mul_i32 s86,s86,20480
s_add_u32 s86,s86,4096
s_add_u32 s87,s70,4
s_and_b32 s87,s87,3
s_mul_i32 s87,s87,20480
s_add_u32 s87,s87,2048
v_or_b32 v77,0,v66
v_xor_b32 v77,v77,v76
v_lshl_add_u32 v77,v77,4,v75
v_add_u32 v48,s84,v77
v_add_u32 v50,s85,v77
v_add_u32 v52,s86,v77
v_add_u32 v54,s87,v77
v_or_b32 v77,4,v66
v_xor_b32 v77,v77,v76
v_lshl_add_u32 v77,v77,4,v75
v_add_u32 v77,9216,v77
v_add_u32 v49,s84,v77
v_add_u32 v51,s85,v77
v_add_u32 v53,s86,v77
v_add_u32 v55,s87,v77
s_lshl_b32 s44,s40,17
v_lshlrev_b32 v75,13,v65
v_lshl_add_u32 v75,v66,6,v75
v_lshl_add_u32 v75,v64,4,v75
v_add_u32 v75,s44,v75
v_add_u32 v56,0,v75
v_add_u32 v57,256,v75
v_add_u32 v58,32768,v75
v_add_u32 v59,33024,v75
v_add_u32 v60,65536,v75
v_add_u32 v61,65792,v75
v_add_u32 v62,98304,v75
v_add_u32 v63,98560,v75
s_mul_i32 s44,s41,307200
s_waitcnt lgkmcnt(0)
s_mov_b64 s[28:29],s[4:5]
s_add_u32 s28,s28,s44
s_addc_u32 s29,s29,0
s_sub_u32 s28,s28,4096
s_subb_u32 s29,s29,0
s_and_b32 s29,s29,0xffff
s_mov_b32 s30,311296
s_mov_b32 s31,0x20000
s_mov_b64 s[32:33],s[10:11]
s_and_b32 s33,s33,0xffff
s_mov_b32 s34,5242880
s_mov_b32 s35,0x20000
s_add_u32 m0,s58,1024
s_nop 0
buffer_load_dwordx4 v57,s[32:35],s56 offen lds
s_add_u32 m0,s58,3072
s_nop 0
buffer_load_dwordx4 v59,s[32:35],s56 offen lds
s_add_u32 m0,s58,5120
s_nop 0
buffer_load_dwordx4 v61,s[32:35],s56 offen lds
s_add_u32 m0,s58,7168
s_nop 0
buffer_load_dwordx4 v63,s[32:35],s56 offen lds
s_add_u32 m0,s59,0
s_nop 0
buffer_load_dwordx4 v48,s[28:31],s57 offen lds
buffer_load_dwordx4 v49,s[28:31],s57 offen offset:1024 lds
buffer_load_dwordx4 v50,s[28:31],s57 offen offset:2048 lds
buffer_load_dwordx4 v51,s[28:31],s57 offen offset:3072 lds
s_add_u32 m0,s59,4096
s_nop 0
buffer_load_dwordx4 v52,s[28:31],s57 offen lds
buffer_load_dwordx4 v53,s[28:31],s57 offen offset:1024 lds
buffer_load_dwordx4 v54,s[28:31],s57 offen offset:2048 lds
buffer_load_dwordx4 v55,s[28:31],s57 offen offset:3072 lds
s_add_u32 s57,s57,128
s_xor_b32 s59,s59,131072
s_add_u32 m0,s58,0
s_nop 0
buffer_load_dwordx4 v56,s[32:35],s56 offen lds
s_add_u32 m0,s58,2048
s_nop 0
buffer_load_dwordx4 v58,s[32:35],s56 offen lds
s_add_u32 m0,s58,4096
s_nop 0
buffer_load_dwordx4 v60,s[32:35],s56 offen lds
s_add_u32 m0,s58,6144
s_nop 0
buffer_load_dwordx4 v62,s[32:35],s56 offen lds
s_add_u32 s56,s56,524288
s_add_u32 s58,s58,32768
s_cmp_ge_u32 s58,131072
s_cselect_b32 s62,98304,0
s_sub_u32 s58,s58,s62
s_add_u32 m0,s58,0
s_nop 0
buffer_load_dwordx4 v56,s[32:35],s56 offen lds
s_add_u32 m0,s58,1024
s_nop 0
buffer_load_dwordx4 v57,s[32:35],s56 offen lds
s_add_u32 m0,s58,2048
s_nop 0
buffer_load_dwordx4 v58,s[32:35],s56 offen lds
s_add_u32 m0,s58,3072
s_nop 0
buffer_load_dwordx4 v59,s[32:35],s56 offen lds
s_add_u32 m0,s58,4096
s_nop 0
buffer_load_dwordx4 v60,s[32:35],s56 offen lds
s_add_u32 m0,s58,5120
s_nop 0
buffer_load_dwordx4 v61,s[32:35],s56 offen lds
s_add_u32 m0,s58,6144
s_nop 0
buffer_load_dwordx4 v62,s[32:35],s56 offen lds
s_add_u32 m0,s58,7168
s_nop 0
buffer_load_dwordx4 v63,s[32:35],s56 offen lds
s_add_u32 s56,s56,524288
s_add_u32 s58,s58,32768
s_cmp_ge_u32 s58,131072
s_cselect_b32 s62,98304,0
s_sub_u32 s58,s58,s62
s_mov_b32 s60,65536
v_lshlrev_b32 v6,4,v12
v_lshlrev_b32 v72,12,v66
v_lshl_add_u32 v72,v65,6,v72
v_lshl_add_u32 v23,v64,4,v72
v_add_u32 v22,65536,v23
v_and_b32 v74,15,v12
v_lshrrev_b32 v76,1,v74
v_lshlrev_b32 v75,7,v74
s_lshl_b32 s44,s40,13
v_add_u32 v75,s44,v75
v_or_b32 v77,0,v66
v_xor_b32 v77,v77,v76
v_lshl_add_u32 v20,v77,4,v75
v_or_b32 v77,4,v66
v_xor_b32 v77,v77,v76
v_lshl_add_u32 v21,v77,4,v75
s_waitcnt vmcnt(12)
s_nop 0
s_memtime s[64:65]
s_memrealtime s[66:67]
s_bfe_u32 s68,%1,327683
s_waitcnt lgkmcnt(0)
s_sub_u32 s64,s64,s72
s_sub_u32 s66,s66,s74
s_mul_i32 s64,s64,10
s_mul_i32 s66,s66,215
s_cmp_lt_u32 s64,s66
s_cselect_b32 s95,64,0
s_cbranch_scc1 L_se1_%=
L_sg1_%=:
s_cmp_eq_u32 s68,0
s_cbranch_scc1 L_se1_%=
s_sleep 4
s_sub_u32 s68,s68,1
s_branch L_sg1_%=
L_se1_%=:
s_barrier
ds_read_b128 a[192:195],v20
ds_read_b64_tr_b16 a[128:129],v22 offset:1024
ds_read_b64_tr_b16 a[130:131],v22 offset:3072
ds_read_b128 a[196:199],v21
ds_read_b64_tr_b16 a[132:133],v22 offset:17408
ds_read_b64_tr_b16 a[134:135],v22 offset:19456
ds_read_b128 a[204:207],v21 offset:2048
ds_read_b128 a[200:203],v20 offset:2048
ds_read_b128 a[208:211],v20 offset:4096
ds_read_b128 a[212:215],v21 offset:4096
ds_read_b128 a[220:223],v21 offset:6144
ds_read_b128 a[216:219],v20 offset:6144
ds_read_b64_tr_b16 a[136:137],v22 offset:1032
ds_read_b64_tr_b16 a[138:139],v22 offset:3080
s_waitcnt lgkmcnt(11)
v_mfma_f32_16x16x32_bf16 v[128:131],a[128:131],a[192:195],0
s_add_u32 m0,s59,0
ds_read_b64_tr_b16 a[140:141],v22 offset:17416
ds_read_b64_tr_b16 a[142:143],v22 offset:19464
s_waitcnt lgkmcnt(10)
v_mfma_f32_16x16x32_bf16 v[128:131],a[132:135],a[196:199],v[128:131]
s_waitcnt lgkmcnt(9)
v_mfma_f32_16x16x32_bf16 v[136:139],a[132:135],a[204:207],0
s_waitcnt lgkmcnt(8)
v_mfma_f32_16x16x32_bf16 v[136:139],a[128:131],a[200:203],v[136:139]
s_waitcnt lgkmcnt(7)
v_mfma_f32_16x16x32_bf16 v[144:147],a[128:131],a[208:211],0
s_waitcnt lgkmcnt(6)
buffer_load_dwordx4 v48,s[28:31],s57 offen lds
v_mfma_f32_16x16x32_bf16 v[144:147],a[132:135],a[212:215],v[144:147]
s_waitcnt lgkmcnt(5)
v_mfma_f32_16x16x32_bf16 v[152:155],a[132:135],a[220:223],0
s_waitcnt lgkmcnt(4)
v_mfma_f32_16x16x32_bf16 v[152:155],a[128:131],a[216:219],v[152:155]
s_waitcnt lgkmcnt(2)
ds_read_b64_tr_b16 a[144:145],v22 offset:1280
ds_read_b64_tr_b16 a[146:147],v22 offset:3328
v_mfma_f32_16x16x32_bf16 v[132:135],a[136:139],a[192:195],0
s_waitcnt lgkmcnt(2)
ds_read_b64_tr_b16 a[148:149],v22 offset:17664
ds_read_b64_tr_b16 a[150:151],v22 offset:19712
buffer_load_dwordx4 v49,s[28:31],s57 offen offset:1024 lds
v_mfma_f32_16x16x32_bf16 v[132:135],a[140:143],a[196:199],v[132:135]
v_mfma_f32_16x16x32_bf16 v[140:143],a[140:143],a[204:207],0
v_mfma_f32_16x16x32_bf16 v[140:143],a[136:139],a[200:203],v[140:143]
v_mfma_f32_16x16x32_bf16 v[148:151],a[136:139],a[208:211],0
buffer_load_dwordx4 v50,s[28:31],s57 offen offset:2048 lds
v_mfma_f32_16x16x32_bf16 v[148:151],a[140:143],a[212:215],v[148:151]
v_mfma_f32_16x16x32_bf16 v[156:159],a[140:143],a[220:223],0
v_mfma_f32_16x16x32_bf16 v[156:159],a[136:139],a[216:219],v[156:159]
s_waitcnt lgkmcnt(2)
ds_read_b64_tr_b16 a[152:153],v22 offset:1288
ds_read_b64_tr_b16 a[154:155],v22 offset:3336
v_mfma_f32_16x16x32_bf16 v[160:163],a[144:147],a[192:195],0
s_waitcnt lgkmcnt(2)
buffer_load_dwordx4 v51,s[28:31],s57 offen offset:3072 lds
ds_read_b64_tr_b16 a[156:157],v22 offset:17672
ds_read_b64_tr_b16 a[158:159],v22 offset:19720
v_mfma_f32_16x16x32_bf16 v[160:163],a[148:151],a[196:199],v[160:163]
s_add_u32 m0,s59,4096
v_mfma_f32_16x16x32_bf16 v[168:171],a[148:151],a[204:207],0
v_mfma_f32_16x16x32_bf16 v[168:171],a[144:147],a[200:203],v[168:171]
v_mfma_f32_16x16x32_bf16 v[176:179],a[144:147],a[208:211],0
buffer_load_dwordx4 v52,s[28:31],s57 offen lds
v_mfma_f32_16x16x32_bf16 v[176:179],a[148:151],a[212:215],v[176:179]
v_mfma_f32_16x16x32_bf16 v[184:187],a[148:151],a[220:223],0
v_mfma_f32_16x16x32_bf16 v[184:187],a[144:147],a[216:219],v[184:187]
s_waitcnt lgkmcnt(2)
v_mfma_f32_16x16x32_bf16 v[164:167],a[152:155],a[192:195],0
s_waitcnt lgkmcnt(0)
buffer_load_dwordx4 v53,s[28:31],s57 offen offset:1024 lds
ds_read_b64_tr_b16 a[160:161],v22 offset:1536
ds_read_b64_tr_b16 a[162:163],v22 offset:3584
v_mfma_f32_16x16x32_bf16 v[164:167],a[156:159],a[196:199],v[164:167]
ds_read_b64_tr_b16 a[164:165],v22 offset:17920
ds_read_b64_tr_b16 a[166:167],v22 offset:19968
v_mfma_f32_16x16x32_bf16 v[172:175],a[156:159],a[204:207],0
v_mfma_f32_16x16x32_bf16 v[172:175],a[152:155],a[200:203],v[172:175]
v_mfma_f32_16x16x32_bf16 v[180:183],a[152:155],a[208:211],0
buffer_load_dwordx4 v54,s[28:31],s57 offen offset:2048 lds
v_mfma_f32_16x16x32_bf16 v[180:183],a[156:159],a[212:215],v[180:183]
v_mfma_f32_16x16x32_bf16 v[188:191],a[156:159],a[220:223],0
s_waitcnt vmcnt(15)
v_mfma_f32_16x16x32_bf16 v[188:191],a[152:155],a[216:219],v[188:191]
s_barrier
buffer_load_dwordx4 v55,s[28:31],s57 offen offset:3072 lds
ds_read_b64_tr_b16 a[168:169],v22 offset:1544
ds_read_b64_tr_b16 a[170:171],v22 offset:3592
s_waitcnt lgkmcnt(4)
v_mfma_f32_16x16x32_bf16 v[192:195],a[160:163],a[192:195],0
s_add_u32 m0,s58,0
ds_read_b64_tr_b16 a[172:173],v22 offset:17928
ds_read_b64_tr_b16 a[174:175],v22 offset:19976
s_waitcnt lgkmcnt(4)
v_mfma_f32_16x16x32_bf16 v[192:195],a[164:167],a[196:199],v[192:195]
v_mfma_f32_16x16x32_bf16 v[200:203],a[164:167],a[204:207],0
s_lshl_b32 s44,s41,13
s_add_u32 s44,s44,104857600
s_mov_b64 s[64:65],s[12:13]
v_mfma_f32_16x16x32_bf16 v[200:203],a[160:163],a[200:203],v[200:203]
s_mov_b32 s66,8192
buffer_load_dwordx4 v56,s[32:35],s56 offen lds
v_mfma_f32_16x16x32_bf16 v[208:211],a[160:163],a[208:211],0
s_add_u32 m0,s58,1024
s_add_u32 s64,s64,s44
s_addc_u32 s65,s65,0
v_mfma_f32_16x16x32_bf16 v[208:211],a[164:167],a[212:215],v[208:211]
s_and_b32 s65,s65,0xffff
s_mov_b32 s67,0x20000
v_mov_b32 v14,4096
v_mfma_f32_16x16x32_bf16 v[216:219],a[164:167],a[220:223],0
s_mov_b64 s[72:73],s[14:15]
s_and_b32 s73,s73,0xffff
ds_read_b64_tr_b16 a[176:177],v22 offset:1792
v_mfma_f32_16x16x32_bf16 v[216:219],a[160:163],a[216:219],v[216:219]
s_mov_b32 s74,5242880
s_mov_b32 s75,0x20000
ds_read_b64_tr_b16 a[178:179],v22 offset:3840
s_waitcnt lgkmcnt(4)
v_mfma_f32_16x16x32_bf16 v[196:199],a[168:171],a[192:195],0
s_waitcnt lgkmcnt(2)
buffer_load_dwordx4 v57,s[32:35],s56 offen lds
ds_read_b64_tr_b16 a[180:181],v22 offset:18176
ds_read_b64_tr_b16 a[182:183],v22 offset:20224
v_mfma_f32_16x16x32_bf16 v[196:199],a[172:175],a[196:199],v[196:199]
s_add_u32 m0,s58,2048
v_lshlrev_b32 v15,3,v6
v_add_u32 v16,128,v15
v_mfma_f32_16x16x32_bf16 v[204:207],a[172:175],a[204:207],0
s_lshl_b32 s83,%1,2
ds_read_b64_tr_b16 a[184:185],v22 offset:1800
v_lshrrev_b32 v16,10,v16
v_mfma_f32_16x16x32_bf16 v[204:207],a[168:171],a[200:203],v[204:207]
s_add_u32 s83,s83,s40
ds_read_b64_tr_b16 a[186:187],v22 offset:3848
v_lshl_add_u32 v15,v16,24,v15
v_mfma_f32_16x16x32_bf16 v[212:215],a[168:171],a[208:211],0
s_mul_i32 s83,s83,5120
buffer_load_dwordx4 v58,s[32:35],s56 offen lds
v_mfma_f32_16x16x32_bf16 v[212:215],a[172:175],a[212:215],v[212:215]
s_add_u32 m0,s58,3072
s_lshl_b32 s43,s41,5
ds_read_b64_tr_b16 a[188:189],v22 offset:18184
v_mfma_f32_16x16x32_bf16 v[220:223],a[172:175],a[220:223],0
s_lshl_b32 s44,s42,1
s_add_u32 s43,s43,s44
ds_read_b64_tr_b16 a[190:191],v22 offset:20232
v_mfma_f32_16x16x32_bf16 v[220:223],a[168:171],a[216:219],v[220:223]
s_lshl_b32 s44,s43,18
s_mov_b64 s[20:21],s[6:7]
ds_read_b64_tr_b16 a[128:129],v22
s_waitcnt lgkmcnt(7)
v_mfma_f32_16x16x32_bf16 v[224:227],a[176:179],a[192:195],0
s_mov_b32 s22,524288
buffer_load_dwordx4 v59,s[32:35],s56 offen lds
s_waitcnt lgkmcnt(5)
v_mfma_f32_16x16x32_bf16 v[224:227],a[180:183],a[196:199],v[224:227]
s_add_u32 m0,s58,4096
)A"
R"A(s_mov_b32 s23,0x20000
ds_read_b64_tr_b16 a[130:131],v22 offset:2048
v_mfma_f32_16x16x32_bf16 v[232:235],a[180:183],a[204:207],0
s_add_u32 s20,s20,s44
s_addc_u32 s21,s21,0
ds_read_b64_tr_b16 a[132:133],v22 offset:16384
v_mfma_f32_16x16x32_bf16 v[232:235],a[176:179],a[200:203],v[232:235]
s_and_b32 s21,s21,0xffff
s_mov_b64 s[24:25],s[8:9]
ds_read_b64_tr_b16 a[134:135],v22 offset:18432
v_mfma_f32_16x16x32_bf16 v[240:243],a[176:179],a[208:211],0
s_mov_b32 s26,524288
buffer_load_dwordx4 v60,s[32:35],s56 offen lds
v_mfma_f32_16x16x32_bf16 v[240:243],a[180:183],a[212:215],v[240:243]
s_add_u32 s24,s24,s44
s_addc_u32 s25,s25,0
ds_read_b64_tr_b16 a[136:137],v22 offset:8
v_mfma_f32_16x16x32_bf16 v[248:251],a[180:183],a[220:223],0
s_add_u32 m0,s58,5120
s_and_b32 s25,s25,0xffff
ds_read_b64_tr_b16 a[138:139],v22 offset:2056
v_mfma_f32_16x16x32_bf16 v[248:251],a[176:179],a[216:219],v[248:251]
s_mov_b32 s27,0x20000
s_lshl_b32 s44,s43,16
ds_read_b64_tr_b16 a[140:141],v22 offset:16392
s_waitcnt lgkmcnt(6)
v_mfma_f32_16x16x32_bf16 v[228:231],a[184:187],a[192:195],0
s_xor_b32 s59,s59,131072
s_mov_b64 s[36:37],s[12:13]
ds_read_b64_tr_b16 a[142:143],v22 offset:18440
v_mfma_f32_16x16x32_bf16 v[228:231],a[188:191],a[196:199],v[228:231]
s_add_u32 s57,s57,128
buffer_load_dwordx4 v61,s[32:35],s56 offen lds
v_mfma_f32_16x16x32_bf16 v[236:239],a[188:191],a[204:207],0
s_add_u32 s36,s36,s44
s_addc_u32 s37,s37,0
ds_read_b64_tr_b16 a[144:145],v22 offset:256
v_mfma_f32_16x16x32_bf16 v[236:239],a[184:187],a[200:203],v[236:239]
s_add_u32 m0,s58,6144
s_and_b32 s37,s37,0xffff
ds_read_b64_tr_b16 a[146:147],v22 offset:2304
v_mfma_f32_16x16x32_bf16 v[244:247],a[184:187],a[208:211],0
s_mov_b32 s38,131072
s_mov_b32 s39,0x20000
ds_read_b64_tr_b16 a[148:149],v22 offset:16640
v_mfma_f32_16x16x32_bf16 v[244:247],a[188:191],a[212:215],v[244:247]
s_lshl_b32 s51,s40,12
s_add_u32 s52,s51,16384
ds_read_b64_tr_b16 a[150:151],v22 offset:18688
v_mfma_f32_16x16x32_bf16 v[252:255],a[188:191],a[220:223],0
s_mul_i32 s47,s41,5
buffer_load_dwordx4 v62,s[32:35],s56 offen lds
v_mfma_f32_16x16x32_bf16 v[252:255],a[184:187],a[216:219],v[252:255]
s_add_u32 s47,s47,s42
s_and_b32 s47,s47,15
ds_read_b64_tr_b16 a[152:153],v22 offset:264
s_waitcnt lgkmcnt(6)
v_mfma_f32_16x16x32_bf16 a[0:3],a[128:131],a[192:195],0
s_add_u32 m0,s58,7168
s_lshl_b32 s47,s47,14
ds_read_b64_tr_b16 a[154:155],v22 offset:2312
v_mfma_f32_16x16x32_bf16 a[0:3],a[132:135],a[196:199],a[0:3]
s_mov_b32 s54,0
s_lshl_b32 s55,s40,14
ds_read_b64_tr_b16 a[156:157],v22 offset:16648
v_mfma_f32_16x16x32_bf16 a[32:35],a[132:135],a[204:207],0
s_mov_b32 s61,0x3e0293ee
s_mov_b32 s63,0x5d800000
ds_read_b64_tr_b16 a[158:159],v22 offset:18696
v_mfma_f32_16x16x32_bf16 a[32:35],a[128:131],a[200:203],a[32:35]
s_lshl_b32 s44,s40,2
buffer_load_dwordx4 v63,s[32:35],s56 offen lds
v_mfma_f32_16x16x32_bf16 a[64:67],a[128:131],a[208:211],0
s_add_u32 s44,s44,s70
s_mov_b32 s46,0xff800000
ds_read_b64_tr_b16 a[160:161],v22 offset:512
v_mfma_f32_16x16x32_bf16 a[64:67],a[132:135],a[212:215],a[64:67]
s_add_u32 m0,s59,0
ds_read_b64_tr_b16 a[162:163],v22 offset:2560
v_lshlrev_b32 v70,8,v65
v_mfma_f32_16x16x32_bf16 a[96:99],a[132:135],a[220:223],0
s_cmp_eq_u32 s44,14
s_cselect_b32 s18,0x7149f2ca,s46
ds_read_b64_tr_b16 a[164:165],v22 offset:16896
v_mfma_f32_16x16x32_bf16 a[96:99],a[128:131],a[216:219],a[96:99]
s_cmp_eq_u32 s44,13
s_cselect_b32 s19,0x7149f2ca,s46
ds_read_b64_tr_b16 a[166:167],v22 offset:18944
v_mfma_f32_16x16x32_bf16 a[4:7],a[136:139],a[192:195],0
s_add_u32 s60,s60,32768
buffer_load_dwordx4 v48,s[28:31],s57 offen lds
s_waitcnt lgkmcnt(6)
v_mfma_f32_16x16x32_bf16 a[4:7],a[140:143],a[196:199],a[4:7]
s_cmp_eq_u32 s44,12
s_cselect_b32 s100,0x7149f2ca,s46
ds_read_b64_tr_b16 a[168:169],v22 offset:520
v_mfma_f32_16x16x32_bf16 a[36:39],a[140:143],a[204:207],0
s_cmp_eq_u32 s44,15
s_cselect_b32 s101,0x7149f2ca,s46
ds_read_b64_tr_b16 a[170:171],v22 offset:2568
v_mfma_f32_16x16x32_bf16 a[36:39],a[136:139],a[200:203],a[36:39]
s_add_u32 s58,s58,32768
ds_read_b64_tr_b16 a[172:173],v22 offset:16904
v_lshl_add_u32 v70,v66,6,v70
v_mfma_f32_16x16x32_bf16 a[68:71],a[136:139],a[208:211],0
s_add_u32 s56,s56,524288
ds_read_b64_tr_b16 a[174:175],v22 offset:18952
v_add_u32 v70,s51,v70
v_mfma_f32_16x16x32_bf16 a[68:71],a[140:143],a[212:215],a[68:71]
s_mul_i32 s81,s70,1310720
buffer_load_dwordx4 v49,s[28:31],s57 offen offset:1024 lds
v_mfma_f32_16x16x32_bf16 a[100:103],a[140:143],a[220:223],0
s_mul_i32 s44,s71,320
ds_read_b64_tr_b16 a[176:177],v22 offset:768
v_lshl_add_u32 v4,v64,4,v70
v_mfma_f32_16x16x32_bf16 a[100:103],a[136:139],a[216:219],a[100:103]
s_add_u32 s81,s81,s44
ds_read_b64_tr_b16 a[178:179],v22 offset:2816
v_xor_b32 v71,2,v64
v_mfma_f32_16x16x32_bf16 a[8:11],a[144:147],a[192:195],0
s_mul_i32 s82,s40,10240
ds_read_b64_tr_b16 a[180:181],v22 offset:17152
v_lshl_add_u32 v5,v71,4,v70
v_mfma_f32_16x16x32_bf16 a[8:11],a[148:151],a[196:199],a[8:11]
s_mul_i32 s44,s40,40960
ds_read_b64_tr_b16 a[182:183],v22 offset:19200
v_lshlrev_b32 v72,10,v65
v_mfma_f32_16x16x32_bf16 a[40:43],a[148:151],a[204:207],0
s_lshr_b32 s92,%1,3
buffer_load_dwordx4 v50,s[28:31],s57 offen offset:2048 lds
v_mfma_f32_16x16x32_bf16 a[40:43],a[144:147],a[200:203],a[40:43]
s_lshl_b32 s92,s92,2
ds_read_b64_tr_b16 a[184:185],v22 offset:776
v_lshl_add_u32 v72,v64,6,v72
v_mfma_f32_16x16x32_bf16 a[72:75],a[144:147],a[208:211],0
s_add_u32 s92,s92,s40
ds_read_b64_tr_b16 a[186:187],v22 offset:2824
v_and_b32 v73,2,v65
v_mfma_f32_16x16x32_bf16 a[72:75],a[148:151],a[212:215],a[72:75]
s_lshl_b32 s92,s92,7
ds_read_b64_tr_b16 a[188:189],v22 offset:17160
v_xor_b32 v73,v73,v66
v_mfma_f32_16x16x32_bf16 a[104:107],a[148:151],a[220:223],0
s_add_u32 s93,s92,L_tailseg_%=-L_pc_%=
ds_read_b64_tr_b16 a[190:191],v22 offset:19208
v_lshl_add_u32 v72,v73,4,v72
v_mfma_f32_16x16x32_bf16 a[104:107],a[144:147],a[216:219],a[104:107]
buffer_load_dwordx4 v51,s[28:31],s57 offen offset:3072 lds
v_mfma_f32_16x16x32_bf16 a[12:15],a[152:155],a[192:195],0
s_waitcnt lgkmcnt(5)
v_add_u32 v1,98304,v72
v_lshlrev_b32 v72,10,v66
v_mfma_f32_16x16x32_bf16 a[12:15],a[156:159],a[196:199],a[12:15]
s_add_u32 m0,s59,4096
v_lshl_add_u32 v72,v65,6,v72
v_lshl_add_u32 v72,v64,3,v72
v_mfma_f32_16x16x32_bf16 a[44:47],a[156:159],a[204:207],0
s_xor_b32 s59,s59,131072
v_add_u32 v72,114688,v72
v_and_b32 v73,1,v66
v_mfma_f32_16x16x32_bf16 a[44:47],a[152:155],a[200:203],a[44:47]
s_cmp_eq_u32 s60,131072
s_cselect_b32 s60,32768,s60
v_lshl_add_u32 v2,v73,5,v72
v_mfma_f32_16x16x32_bf16 a[76:79],a[152:155],a[208:211],0
buffer_load_dwordx4 v52,s[28:31],s57 offen lds
v_mfma_f32_16x16x32_bf16 a[76:79],a[156:159],a[212:215],a[76:79]
v_xor_b32 v73,1,v73
v_add_u32 v22,s60,v23
v_mfma_f32_16x16x32_bf16 a[108:111],a[156:159],a[220:223],0
s_add_u32 s60,s60,32768
v_xor_b32 v20,131072,v20
v_xor_b32 v21,131072,v21
v_mfma_f32_16x16x32_bf16 a[108:111],a[152:155],a[216:219],a[108:111]
s_waitcnt vmcnt(13) lgkmcnt(0)
v_lshl_add_u32 v3,v73,5,v72
v_mov_b32 v73,0x3f803f80
v_mfma_f32_16x16x32_bf16 a[16:19],a[160:163],a[192:195],0
s_barrier
buffer_load_dwordx4 v53,s[28:31],s57 offen offset:1024 lds
v_mfma_f32_16x16x32_bf16 a[16:19],a[164:167],a[196:199],a[16:19]
s_cmp_ge_u32 s58,131072
s_cselect_b32 s62,98304,0
ds_read_b128 a[224:227],v20
v_mfma_f32_16x16x32_bf16 a[48:51],a[164:167],a[204:207],0
s_sub_u32 s58,s58,s62
ds_read_b64_tr_b16 a[128:129],v22 offset:1024
v_add_u32 v70,0,v12
v_mfma_f32_16x16x32_bf16 a[48:51],a[160:163],a[200:203],a[48:51]
ds_read_b64_tr_b16 a[130:131],v22 offset:3072
v_mul_u32_u24 v71,3277,v70
v_mfma_f32_16x16x32_bf16 a[80:83],a[160:163],a[208:211],0
ds_read_b128 a[228:231],v21
v_lshrrev_b32 v71,16,v71
v_mfma_f32_16x16x32_bf16 a[80:83],a[164:167],a[212:215],a[80:83]
buffer_load_dwordx4 v54,s[28:31],s57 offen offset:2048 lds
v_mfma_f32_16x16x32_bf16 a[112:115],a[164:167],a[220:223],0
ds_read_b64_tr_b16 a[132:133],v22 offset:17408
v_mul_u32_u24 v72,20,v71
v_mfma_f32_16x16x32_bf16 a[112:115],a[160:163],a[216:219],a[112:115]
ds_read_b64_tr_b16 a[134:135],v22 offset:19456
v_sub_u32 v72,v70,v72
v_mfma_f32_16x16x32_bf16 a[20:23],a[168:171],a[192:195],0
ds_read_b128 a[232:235],v20 offset:2048
v_add_u32 v70,64,v12
v_mfma_f32_16x16x32_bf16 a[20:23],a[172:175],a[196:199],a[20:23]
ds_read_b128 a[236:239],v21 offset:2048
v_lshlrev_b32 v24,10,v6
v_mfma_f32_16x16x32_bf16 a[52:55],a[172:175],a[204:207],0
buffer_load_dwordx4 v55,s[28:31],s57 offen offset:3072 lds
v_mfma_f32_16x16x32_bf16 a[52:55],a[168:171],a[200:203],a[52:55]
s_add_u32 s57,s57,128
ds_read_b128 a[240:243],v20 offset:4096
v_mfma_f32_16x16x32_bf16 a[84:87],a[168:171],a[208:211],0
s_cmp_eq_u32 s60,131072
s_cselect_b32 s60,32768,s60
ds_read_b128 a[244:247],v21 offset:4096
v_mfma_f32_16x16x32_bf16 a[84:87],a[172:175],a[212:215],a[84:87]
ds_read_b128 a[248:251],v20 offset:6144
v_xor_b32 v20,131072,v20
v_mfma_f32_16x16x32_bf16 a[116:119],a[172:175],a[220:223],0
buffer_load_dword v13,v14,s[64:67],0 offen sc1
v_mfma_f32_16x16x32_bf16 a[116:119],a[168:171],a[216:219],a[116:119]
ds_read_b128 a[252:255],v21 offset:6144
v_xor_b32 v21,131072,v21
v_mfma_f32_16x16x32_bf16 a[24:27],a[176:179],a[192:195],0
ds_read_b64_tr_b16 a[136:137],v22 offset:1032
v_mfma_f32_16x16x32_bf16 a[24:27],a[180:183],a[196:199],a[24:27]
ds_read_b64_tr_b16 a[138:139],v22 offset:3080
v_mfma_f32_16x16x32_bf16 a[56:59],a[180:183],a[204:207],0
ds_read_b64_tr_b16 a[140:141],v22 offset:17416
v_mfma_f32_16x16x32_bf16 a[56:59],a[176:179],a[200:203],a[56:59]
ds_read_b64_tr_b16 a[142:143],v22 offset:19464
v_mfma_f32_16x16x32_bf16 a[88:91],a[176:179],a[208:211],0
ds_read_b64_tr_b16 a[144:145],v22 offset:1280
v_mfma_f32_16x16x32_bf16 a[88:91],a[180:183],a[212:215],a[88:91]
ds_read_b64_tr_b16 a[146:147],v22 offset:3328
v_mfma_f32_16x16x32_bf16 a[120:123],a[180:183],a[220:223],0
ds_read_b64_tr_b16 a[148:149],v22 offset:17664
v_mfma_f32_16x16x32_bf16 a[120:123],a[176:179],a[216:219],a[120:123]
ds_read_b64_tr_b16 a[150:151],v22 offset:19712
v_mfma_f32_16x16x32_bf16 a[28:31],a[184:187],a[192:195],0
ds_read_b64_tr_b16 a[152:153],v22 offset:1288
v_mfma_f32_16x16x32_bf16 a[28:31],a[188:191],a[196:199],a[28:31]
ds_read_b64_tr_b16 a[154:155],v22 offset:3336
ds_read_b64_tr_b16 a[156:157],v22 offset:17672
ds_read_b64_tr_b16 a[158:159],v22 offset:19720
v_mfma_f32_16x16x32_bf16 a[60:63],a[188:191],a[204:207],0
v_cmp_gt_u32 vcc,2,v64
s_nop 1
v_cndmask_b32 v28,0,v73,vcc
v_mfma_f32_16x16x32_bf16 a[60:63],a[184:187],a[200:203],a[60:63]
v_and_b32 v73,1,v64
v_mul_u32_u24 v73,0xaaaa,v73
v_mfma_f32_16x16x32_bf16 a[92:95],a[184:187],a[208:211],0
v_add_u32 v73,0x4444,v73
v_lshl_or_b32 v27,v73,16,v73
v_mfma_f32_16x16x32_bf16 a[92:95],a[188:191],a[212:215],a[92:95]
v_mov_b32 v29,v28
v_mov_b32 v30,v28
v_mfma_f32_16x16x32_bf16 a[124:127],a[188:191],a[220:223],0
s_add_u32 m0,s58,0
v_mov_b32 v31,v28
v_bfe_u32 v73,v71,2,1
v_mfma_f32_16x16x32_bf16 a[124:127],a[184:187],a[216:219],a[124:127]
s_waitcnt lgkmcnt(4)
v_lshlrev_b32 v73,1,v73
v_xor_b32 v72,v72,v73
v_mfma_f32_16x16x32_bf16 v[128:131],a[128:131],a[224:227],v[128:131]
v_mul_u32_u24 v71,1280,v71
v_lshl_add_u32 v71,v72,4,v71
v_mfma_f32_16x16x32_bf16 v[128:131],a[132:135],a[228:231],v[128:131]
buffer_load_dwordx4 v56,s[32:35],s56 offen lds
v_mfma_f32_16x16x32_bf16 v[136:139],a[132:135],a[236:239],v[136:139]
v_add_u32 v71,s44,v71
v_subrev_u32 v7,0,v71
v_mfma_f32_16x16x32_bf16 v[136:139],a[128:131],a[232:235],v[136:139]
s_add_u32 m0,s58,1024
ds_read_b64_tr_b16 a[160:161],v22 offset:1536
v_mul_u32_u24 v71,3277,v70
v_mfma_f32_16x16x32_bf16 v[144:147],a[128:131],a[240:243],v[144:147]
ds_read_b64_tr_b16 a[162:163],v22 offset:3584
v_lshrrev_b32 v71,16,v71
v_mfma_f32_16x16x32_bf16 v[144:147],a[132:135],a[244:247],v[144:147]
ds_read_b64_tr_b16 a[164:165],v22 offset:17920
v_mul_u32_u24 v72,20,v71
v_mfma_f32_16x16x32_bf16 v[152:155],a[132:135],a[252:255],v[152:155]
buffer_load_dwordx4 v57,s[32:35],s56 offen lds
v_mfma_f32_16x16x32_bf16 v[152:155],a[128:131],a[248:251],v[152:155]
ds_read_b64_tr_b16 a[166:167],v22 offset:19968
v_sub_u32 v72,v70,v72
v_mfma_f32_16x16x32_bf16 v[132:135],a[136:139],a[224:227],v[132:135]
)A"
R"A(v_bfe_u32 v73,v71,2,1
v_lshlrev_b32 v73,1,v73
v_mfma_f32_16x16x32_bf16 v[132:135],a[140:143],a[228:231],v[132:135]
s_add_u32 m0,s58,2048
ds_read_b64_tr_b16 a[168:169],v22 offset:1544
v_xor_b32 v72,v72,v73
v_mfma_f32_16x16x32_bf16 v[140:143],a[140:143],a[236:239],v[140:143]
ds_read_b64_tr_b16 a[170:171],v22 offset:3592
v_mul_u32_u24 v71,1280,v71
v_mfma_f32_16x16x32_bf16 v[140:143],a[136:139],a[232:235],v[140:143]
ds_read_b64_tr_b16 a[172:173],v22 offset:17928
v_lshl_add_u32 v71,v72,4,v71
v_mfma_f32_16x16x32_bf16 v[148:151],a[136:139],a[240:243],v[148:151]
buffer_load_dwordx4 v58,s[32:35],s56 offen lds
v_mfma_f32_16x16x32_bf16 v[148:151],a[140:143],a[244:247],v[148:151]
ds_read_b64_tr_b16 a[174:175],v22 offset:19976
v_add_u32 v71,s44,v71
v_mfma_f32_16x16x32_bf16 v[156:159],a[140:143],a[252:255],v[156:159]
v_subrev_u32 v8,1024,v71
v_add_u32 v70,128,v12
v_mfma_f32_16x16x32_bf16 v[156:159],a[136:139],a[248:251],v[156:159]
s_add_u32 m0,s58,3072
ds_read_b64_tr_b16 a[176:177],v22 offset:1792
v_mul_u32_u24 v71,3277,v70
v_mfma_f32_16x16x32_bf16 v[160:163],a[144:147],a[224:227],v[160:163]
ds_read_b64_tr_b16 a[178:179],v22 offset:3840
v_lshrrev_b32 v71,16,v71
v_mfma_f32_16x16x32_bf16 v[160:163],a[148:151],a[228:231],v[160:163]
ds_read_b64_tr_b16 a[180:181],v22 offset:18176
v_mul_u32_u24 v72,20,v71
v_mfma_f32_16x16x32_bf16 v[168:171],a[148:151],a[236:239],v[168:171]
buffer_load_dwordx4 v59,s[32:35],s56 offen lds
v_mfma_f32_16x16x32_bf16 v[168:171],a[144:147],a[232:235],v[168:171]
ds_read_b64_tr_b16 a[182:183],v22 offset:20224
v_sub_u32 v72,v70,v72
v_mfma_f32_16x16x32_bf16 v[176:179],a[144:147],a[240:243],v[176:179]
s_add_u32 m0,s58,4096
v_bfe_u32 v73,v71,2,1
v_lshlrev_b32 v73,1,v73
v_mfma_f32_16x16x32_bf16 v[176:179],a[148:151],a[244:247],v[176:179]
ds_read_b64_tr_b16 a[184:185],v22 offset:1800
v_xor_b32 v72,v72,v73
v_mfma_f32_16x16x32_bf16 v[184:187],a[148:151],a[252:255],v[184:187]
ds_read_b64_tr_b16 a[186:187],v22 offset:3848
v_mul_u32_u24 v71,1280,v71
v_mfma_f32_16x16x32_bf16 v[184:187],a[144:147],a[248:251],v[184:187]
s_waitcnt lgkmcnt(5)
buffer_load_dwordx4 v60,s[32:35],s56 offen lds
v_mfma_f32_16x16x32_bf16 v[164:167],a[152:155],a[224:227],v[164:167]
ds_read_b64_tr_b16 a[188:189],v22 offset:18184
v_lshl_add_u32 v71,v72,4,v71
v_mfma_f32_16x16x32_bf16 v[164:167],a[156:159],a[228:231],v[164:167]
ds_read_b64_tr_b16 a[190:191],v22 offset:20232
v_add_u32 v71,s44,v71
v_mfma_f32_16x16x32_bf16 v[172:175],a[156:159],a[236:239],v[172:175]
s_add_u32 m0,s58,5120
v_subrev_u32 v9,2048,v71
v_add_u32 v70,192,v12
v_mfma_f32_16x16x32_bf16 v[172:175],a[152:155],a[232:235],v[172:175]
v_mul_u32_u24 v71,3277,v70
v_lshrrev_b32 v71,16,v71
v_mfma_f32_16x16x32_bf16 v[180:183],a[152:155],a[240:243],v[180:183]
ds_read_b64_tr_b16 a[128:129],v22
v_mul_u32_u24 v72,20,v71
v_mfma_f32_16x16x32_bf16 v[180:183],a[156:159],a[244:247],v[180:183]
buffer_load_dwordx4 v61,s[32:35],s56 offen lds
v_mfma_f32_16x16x32_bf16 v[188:191],a[156:159],a[252:255],v[188:191]
ds_read_b64_tr_b16 a[130:131],v22 offset:2048
v_sub_u32 v72,v70,v72
v_mfma_f32_16x16x32_bf16 v[188:191],a[152:155],a[248:251],v[188:191]
ds_read_b64_tr_b16 a[132:133],v22 offset:16384
v_bfe_u32 v73,v71,2,1
v_mfma_f32_16x16x32_bf16 v[192:195],a[160:163],a[224:227],v[192:195]
s_add_u32 m0,s58,6144
ds_read_b64_tr_b16 a[134:135],v22 offset:18432
v_lshlrev_b32 v73,1,v73
v_mfma_f32_16x16x32_bf16 v[192:195],a[164:167],a[228:231],v[192:195]
v_xor_b32 v72,v72,v73
v_mul_u32_u24 v71,1280,v71
v_mfma_f32_16x16x32_bf16 v[200:203],a[164:167],a[236:239],v[200:203]
ds_read_b64_tr_b16 a[136:137],v22 offset:8
v_lshl_add_u32 v71,v72,4,v71
v_mfma_f32_16x16x32_bf16 v[200:203],a[160:163],a[232:235],v[200:203]
buffer_load_dwordx4 v62,s[32:35],s56 offen lds
v_mfma_f32_16x16x32_bf16 v[208:211],a[160:163],a[240:243],v[208:211]
ds_read_b64_tr_b16 a[138:139],v22 offset:2056
v_add_u32 v71,s44,v71
v_mfma_f32_16x16x32_bf16 v[208:211],a[164:167],a[244:247],v[208:211]
s_add_u32 m0,s58,7168
ds_read_b64_tr_b16 a[140:141],v22 offset:16392
v_subrev_u32 v10,3072,v71
v_mfma_f32_16x16x32_bf16 v[216:219],a[164:167],a[252:255],v[216:219]
s_add_u32 s58,s58,32768
ds_read_b64_tr_b16 a[142:143],v22 offset:18440
v_add_u32 v70,256,v12
v_mfma_f32_16x16x32_bf16 v[216:219],a[160:163],a[248:251],v[216:219]
v_mul_u32_u24 v71,3277,v70
v_lshrrev_b32 v71,16,v71
v_mfma_f32_16x16x32_bf16 v[196:199],a[168:171],a[224:227],v[196:199]
buffer_load_dwordx4 v63,s[32:35],s56 offen lds
v_mfma_f32_16x16x32_bf16 v[196:199],a[172:175],a[228:231],v[196:199]
s_add_u32 s56,s56,524288
ds_read_b64_tr_b16 a[144:145],v22 offset:256
v_mul_u32_u24 v72,20,v71
v_mfma_f32_16x16x32_bf16 v[204:207],a[172:175],a[236:239],v[204:207]
ds_read_b64_tr_b16 a[146:147],v22 offset:2304
v_sub_u32 v72,v70,v72
v_mfma_f32_16x16x32_bf16 v[204:207],a[168:171],a[232:235],v[204:207]
s_add_u32 m0,s59,0
ds_read_b64_tr_b16 a[148:149],v22 offset:16640
v_bfe_u32 v73,v71,2,1
v_mfma_f32_16x16x32_bf16 v[212:215],a[168:171],a[240:243],v[212:215]
ds_read_b64_tr_b16 a[150:151],v22 offset:18688
v_lshlrev_b32 v73,1,v73
v_mfma_f32_16x16x32_bf16 v[212:215],a[172:175],a[244:247],v[212:215]
v_xor_b32 v72,v72,v73
v_mul_u32_u24 v71,1280,v71
v_mfma_f32_16x16x32_bf16 v[220:223],a[172:175],a[252:255],v[220:223]
buffer_load_dwordx4 v48,s[28:31],s57 offen lds
v_mfma_f32_16x16x32_bf16 v[220:223],a[168:171],a[248:251],v[220:223]
s_waitcnt lgkmcnt(4)
v_lshl_add_u32 v71,v72,4,v71
v_add_u32 v71,s44,v71
v_mfma_f32_16x16x32_bf16 v[224:227],a[176:179],a[224:227],v[224:227]
ds_read_b64_tr_b16 a[152:153],v22 offset:264
v_subrev_u32 v11,0,v71
v_mfma_f32_16x16x32_bf16 v[224:227],a[180:183],a[228:231],v[224:227]
s_cmp_ge_u32 s58,131072
s_cselect_b32 s62,98304,0
ds_read_b64_tr_b16 a[154:155],v22 offset:2312
v_mfma_f32_16x16x32_bf16 v[232:235],a[180:183],a[236:239],v[232:235]
s_sub_u32 s58,s58,s62
ds_read_b64_tr_b16 a[156:157],v22 offset:16648
v_mfma_f32_16x16x32_bf16 v[232:235],a[176:179],a[232:235],v[232:235]
ds_read_b64_tr_b16 a[158:159],v22 offset:18696
v_mfma_f32_16x16x32_bf16 v[240:243],a[176:179],a[240:243],v[240:243]
buffer_load_dwordx4 v49,s[28:31],s57 offen offset:1024 lds
v_mfma_f32_16x16x32_bf16 v[240:243],a[180:183],a[244:247],v[240:243]
ds_read_b64_tr_b16 a[160:161],v22 offset:512
v_mfma_f32_16x16x32_bf16 v[248:251],a[180:183],a[252:255],v[248:251]
ds_read_b64_tr_b16 a[162:163],v22 offset:2560
v_mfma_f32_16x16x32_bf16 v[248:251],a[176:179],a[248:251],v[248:251]
ds_read_b64_tr_b16 a[164:165],v22 offset:16896
v_mfma_f32_16x16x32_bf16 v[228:231],a[184:187],a[224:227],v[228:231]
ds_read_b64_tr_b16 a[166:167],v22 offset:18944
v_mfma_f32_16x16x32_bf16 v[228:231],a[188:191],a[228:231],v[228:231]
buffer_load_dwordx4 v50,s[28:31],s57 offen offset:2048 lds
v_mfma_f32_16x16x32_bf16 v[236:239],a[188:191],a[236:239],v[236:239]
ds_read_b64_tr_b16 a[168:169],v22 offset:520
v_mfma_f32_16x16x32_bf16 v[236:239],a[184:187],a[232:235],v[236:239]
ds_read_b64_tr_b16 a[170:171],v22 offset:2568
v_mfma_f32_16x16x32_bf16 v[244:247],a[184:187],a[240:243],v[244:247]
ds_read_b64_tr_b16 a[172:173],v22 offset:16904
v_mfma_f32_16x16x32_bf16 v[244:247],a[188:191],a[244:247],v[244:247]
ds_read_b64_tr_b16 a[174:175],v22 offset:18952
v_mfma_f32_16x16x32_bf16 v[252:255],a[188:191],a[252:255],v[252:255]
ds_read_b64_tr_b16 a[176:177],v22 offset:768
v_mfma_f32_16x16x32_bf16 v[252:255],a[184:187],a[248:251],v[252:255]
buffer_load_dwordx4 v51,s[28:31],s57 offen offset:3072 lds
v_mfma_f32_16x16x32_bf16 a[0:3],a[128:131],a[224:227],a[0:3]
ds_read_b64_tr_b16 a[178:179],v22 offset:2816
v_mfma_f32_16x16x32_bf16 a[0:3],a[132:135],a[228:231],a[0:3]
ds_read_b64_tr_b16 a[180:181],v22 offset:17152
v_mfma_f32_16x16x32_bf16 a[32:35],a[132:135],a[236:239],a[32:35]
s_add_u32 m0,s59,4096
s_xor_b32 s59,s59,131072
ds_read_b64_tr_b16 a[182:183],v22 offset:19200
v_mfma_f32_16x16x32_bf16 a[32:35],a[128:131],a[232:235],a[32:35]
ds_read_b64_tr_b16 a[184:185],v22 offset:776
v_mfma_f32_16x16x32_bf16 a[64:67],a[128:131],a[240:243],a[64:67]
ds_read_b64_tr_b16 a[186:187],v22 offset:2824
v_mfma_f32_16x16x32_bf16 a[64:67],a[132:135],a[244:247],a[64:67]
buffer_load_dwordx4 v52,s[28:31],s57 offen lds
v_mfma_f32_16x16x32_bf16 a[96:99],a[132:135],a[252:255],a[96:99]
ds_read_b64_tr_b16 a[188:189],v22 offset:17160
v_mfma_f32_16x16x32_bf16 a[96:99],a[128:131],a[248:251],a[96:99]
ds_read_b64_tr_b16 a[190:191],v22 offset:19208
v_add_u32 v22,s60,v23
v_mfma_f32_16x16x32_bf16 a[4:7],a[136:139],a[224:227],a[4:7]
v_mfma_f32_16x16x32_bf16 a[4:7],a[140:143],a[228:231],a[4:7]
v_mfma_f32_16x16x32_bf16 a[36:39],a[140:143],a[236:239],a[36:39]
buffer_load_dwordx4 v53,s[28:31],s57 offen offset:1024 lds
v_mfma_f32_16x16x32_bf16 a[36:39],a[136:139],a[232:235],a[36:39]
v_mfma_f32_16x16x32_bf16 a[68:71],a[136:139],a[240:243],a[68:71]
v_mfma_f32_16x16x32_bf16 a[68:71],a[140:143],a[244:247],a[68:71]
v_mfma_f32_16x16x32_bf16 a[100:103],a[140:143],a[252:255],a[100:103]
v_mfma_f32_16x16x32_bf16 a[100:103],a[136:139],a[248:251],a[100:103]
s_waitcnt lgkmcnt(0)
v_mfma_f32_16x16x32_bf16 a[8:11],a[144:147],a[224:227],a[8:11]
buffer_load_dwordx4 v54,s[28:31],s57 offen offset:2048 lds
v_mfma_f32_16x16x32_bf16 a[8:11],a[148:151],a[228:231],a[8:11]
v_mfma_f32_16x16x32_bf16 a[40:43],a[148:151],a[236:239],a[40:43]
v_mfma_f32_16x16x32_bf16 a[40:43],a[144:147],a[232:235],a[40:43]
v_mfma_f32_16x16x32_bf16 a[72:75],a[144:147],a[240:243],a[72:75]
v_mfma_f32_16x16x32_bf16 a[72:75],a[148:151],a[244:247],a[72:75]
v_mfma_f32_16x16x32_bf16 a[104:107],a[148:151],a[252:255],a[104:107]
buffer_load_dwordx4 v55,s[28:31],s57 offen offset:3072 lds
v_mfma_f32_16x16x32_bf16 a[104:107],a[144:147],a[248:251],a[104:107]
s_add_u32 s57,s57,128
v_mfma_f32_16x16x32_bf16 a[12:15],a[152:155],a[224:227],a[12:15]
v_mfma_f32_16x16x32_bf16 a[12:15],a[156:159],a[228:231],a[12:15]
v_mfma_f32_16x16x32_bf16 a[44:47],a[156:159],a[236:239],a[44:47]
v_mfma_f32_16x16x32_bf16 a[44:47],a[152:155],a[232:235],a[44:47]
v_mfma_f32_16x16x32_bf16 a[76:79],a[152:155],a[240:243],a[76:79]
v_mfma_f32_16x16x32_bf16 a[76:79],a[156:159],a[244:247],a[76:79]
v_mfma_f32_16x16x32_bf16 a[108:111],a[156:159],a[252:255],a[108:111]
v_mfma_f32_16x16x32_bf16 a[108:111],a[152:155],a[248:251],a[108:111]
s_waitcnt vmcnt(17)
v_mfma_f32_16x16x32_bf16 a[16:19],a[160:163],a[224:227],a[16:19]
s_barrier
ds_read_b128 a[192:195],v20
v_mfma_f32_16x16x32_bf16 a[16:19],a[164:167],a[228:231],a[16:19]
ds_read_b128 a[196:199],v21
v_mfma_f32_16x16x32_bf16 a[48:51],a[164:167],a[236:239],a[48:51]
ds_read_b128 a[200:203],v20 offset:2048
v_mfma_f32_16x16x32_bf16 a[48:51],a[160:163],a[232:235],a[48:51]
ds_read_b128 a[204:207],v21 offset:2048
v_mfma_f32_16x16x32_bf16 a[80:83],a[160:163],a[240:243],a[80:83]
ds_read_b128 a[208:211],v20 offset:4096
v_mfma_f32_16x16x32_bf16 a[80:83],a[164:167],a[244:247],a[80:83]
ds_read_b128 a[212:215],v21 offset:4096
v_mfma_f32_16x16x32_bf16 a[112:115],a[164:167],a[252:255],a[112:115]
ds_read_b128 a[216:219],v20 offset:6144
v_mfma_f32_16x16x32_bf16 a[112:115],a[160:163],a[248:251],a[112:115]
ds_read_b128 a[220:223],v21 offset:6144
v_mfma_f32_16x16x32_bf16 a[20:23],a[168:171],a[224:227],a[20:23]
ds_read_b64_tr_b16 a[128:129],v22 offset:1024
v_mfma_f32_16x16x32_bf16 a[20:23],a[172:175],a[228:231],a[20:23]
ds_read_b64_tr_b16 a[130:131],v22 offset:3072
v_mfma_f32_16x16x32_bf16 a[52:55],a[172:175],a[236:239],a[52:55]
ds_read_b64_tr_b16 a[132:133],v22 offset:17408
v_mfma_f32_16x16x32_bf16 a[52:55],a[168:171],a[232:235],a[52:55]
ds_read_b64_tr_b16 a[134:135],v22 offset:19456
v_mfma_f32_16x16x32_bf16 a[84:87],a[168:171],a[240:243],a[84:87]
ds_read_b64_tr_b16 a[136:137],v22 offset:1032
v_mfma_f32_16x16x32_bf16 a[84:87],a[172:175],a[244:247],a[84:87]
ds_read_b64_tr_b16 a[138:139],v22 offset:3080
v_mfma_f32_16x16x32_bf16 a[116:119],a[172:175],a[252:255],a[116:119]
ds_read_b64_tr_b16 a[140:141],v22 offset:17416
v_mfma_f32_16x16x32_bf16 a[116:119],a[168:171],a[248:251],a[116:119]
ds_read_b64_tr_b16 a[142:143],v22 offset:19464
v_mfma_f32_16x16x32_bf16 a[24:27],a[176:179],a[224:227],a[24:27]
ds_read_b64_tr_b16 a[144:145],v22 offset:1280
v_mfma_f32_16x16x32_bf16 a[24:27],a[180:183],a[228:231],a[24:27]
)A"
R"A(ds_read_b64_tr_b16 a[146:147],v22 offset:3328
v_mfma_f32_16x16x32_bf16 a[56:59],a[180:183],a[236:239],a[56:59]
ds_read_b64_tr_b16 a[148:149],v22 offset:17664
v_mfma_f32_16x16x32_bf16 a[56:59],a[176:179],a[232:235],a[56:59]
ds_read_b64_tr_b16 a[150:151],v22 offset:19712
v_mfma_f32_16x16x32_bf16 a[88:91],a[176:179],a[240:243],a[88:91]
ds_read_b64_tr_b16 a[152:153],v22 offset:1288
v_mfma_f32_16x16x32_bf16 a[88:91],a[180:183],a[244:247],a[88:91]
ds_read_b64_tr_b16 a[154:155],v22 offset:3336
v_mfma_f32_16x16x32_bf16 a[120:123],a[180:183],a[252:255],a[120:123]
ds_read_b64_tr_b16 a[156:157],v22 offset:17672
v_mfma_f32_16x16x32_bf16 a[120:123],a[176:179],a[248:251],a[120:123]
ds_read_b64_tr_b16 a[158:159],v22 offset:19720
v_mfma_f32_16x16x32_bf16 a[28:31],a[184:187],a[224:227],a[28:31]
v_mfma_f32_16x16x32_bf16 a[28:31],a[188:191],a[228:231],a[28:31]
v_mfma_f32_16x16x32_bf16 a[60:63],a[188:191],a[236:239],a[60:63]
v_mfma_f32_16x16x32_bf16 a[60:63],a[184:187],a[232:235],a[60:63]
v_mfma_f32_16x16x32_bf16 a[92:95],a[184:187],a[240:243],a[92:95]
v_mfma_f32_16x16x32_bf16 a[92:95],a[188:191],a[244:247],a[92:95]
v_mfma_f32_16x16x32_bf16 a[124:127],a[188:191],a[252:255],a[124:127]
v_mfma_f32_16x16x32_bf16 a[124:127],a[184:187],a[248:251],a[124:127]
s_mov_b32 s53,3
.p2align 5
L_qp_%=:
s_waitcnt lgkmcnt(1)
v_mfma_f32_16x16x32_bf16 v[128:131],a[128:131],a[192:195],v[128:131]
s_add_u32 m0,s58,0
s_add_u32 s60,s60,32768
ds_read_b64_tr_b16 a[160:161],v22 offset:1536
v_mfma_f32_16x16x32_bf16 v[128:131],a[132:135],a[196:199],v[128:131]
s_mov_b32 s90,L_att0x_%=-L_pc_%=+128
buffer_load_dwordx4 v56,s[32:35],s56 offen lds
v_mfma_f32_16x16x32_bf16 v[136:139],a[132:135],a[204:207],v[136:139]
s_cmp_eq_u32 s60,131072
s_cselect_b32 s60,32768,s60
ds_read_b64_tr_b16 a[162:163],v22 offset:3584
v_mfma_f32_16x16x32_bf16 v[136:139],a[128:131],a[200:203],v[136:139]
s_add_u32 m0,s58,1024
ds_read_b64_tr_b16 a[164:165],v22 offset:17920
v_xor_b32 v20,131072,v20
v_mfma_f32_16x16x32_bf16 v[144:147],a[128:131],a[208:211],v[144:147]
s_sub_u32 s53,s53,1
ds_read_b64_tr_b16 a[166:167],v22 offset:19968
v_xor_b32 v21,131072,v21
v_mfma_f32_16x16x32_bf16 v[144:147],a[132:135],a[212:215],v[144:147]
ds_read_b64_tr_b16 a[168:169],v22 offset:1544
v_mfma_f32_16x16x32_bf16 v[152:155],a[132:135],a[220:223],v[152:155]
buffer_load_dwordx4 v57,s[32:35],s56 offen lds
v_mfma_f32_16x16x32_bf16 v[152:155],a[128:131],a[216:219],v[152:155]
ds_read_b64_tr_b16 a[170:171],v22 offset:3592
v_mfma_f32_16x16x32_bf16 v[132:135],a[136:139],a[192:195],v[132:135]
ds_read_b64_tr_b16 a[172:173],v22 offset:17928
v_mfma_f32_16x16x32_bf16 v[132:135],a[140:143],a[196:199],v[132:135]
s_add_u32 m0,s58,2048
ds_read_b64_tr_b16 a[174:175],v22 offset:19976
v_mfma_f32_16x16x32_bf16 v[140:143],a[140:143],a[204:207],v[140:143]
ds_read_b64_tr_b16 a[176:177],v22 offset:1792
v_mfma_f32_16x16x32_bf16 v[140:143],a[136:139],a[200:203],v[140:143]
ds_read_b64_tr_b16 a[178:179],v22 offset:3840
v_mfma_f32_16x16x32_bf16 v[148:151],a[136:139],a[208:211],v[148:151]
buffer_load_dwordx4 v58,s[32:35],s56 offen lds
v_mfma_f32_16x16x32_bf16 v[148:151],a[140:143],a[212:215],v[148:151]
ds_read_b64_tr_b16 a[180:181],v22 offset:18176
v_mfma_f32_16x16x32_bf16 v[156:159],a[140:143],a[220:223],v[156:159]
s_add_u32 m0,s58,3072
ds_read_b64_tr_b16 a[182:183],v22 offset:20224
v_mfma_f32_16x16x32_bf16 v[156:159],a[136:139],a[216:219],v[156:159]
ds_read_b64_tr_b16 a[184:185],v22 offset:1800
v_mfma_f32_16x16x32_bf16 v[160:163],a[144:147],a[192:195],v[160:163]
ds_read_b64_tr_b16 a[186:187],v22 offset:3848
v_mfma_f32_16x16x32_bf16 v[160:163],a[148:151],a[196:199],v[160:163]
buffer_load_dwordx4 v59,s[32:35],s56 offen lds
v_mfma_f32_16x16x32_bf16 v[168:171],a[148:151],a[204:207],v[168:171]
ds_read_b64_tr_b16 a[188:189],v22 offset:18184
v_mfma_f32_16x16x32_bf16 v[168:171],a[144:147],a[200:203],v[168:171]
ds_read_b64_tr_b16 a[190:191],v22 offset:20232
v_mfma_f32_16x16x32_bf16 v[176:179],a[144:147],a[208:211],v[176:179]
s_add_u32 m0,s58,4096
ds_read_b64_tr_b16 a[128:129],v22
v_mfma_f32_16x16x32_bf16 v[176:179],a[148:151],a[212:215],v[176:179]
ds_read_b64_tr_b16 a[130:131],v22 offset:2048
v_mfma_f32_16x16x32_bf16 v[184:187],a[148:151],a[220:223],v[184:187]
ds_read_b64_tr_b16 a[132:133],v22 offset:16384
v_mfma_f32_16x16x32_bf16 v[184:187],a[144:147],a[216:219],v[184:187]
buffer_load_dwordx4 v60,s[32:35],s56 offen lds
v_mfma_f32_16x16x32_bf16 v[164:167],a[152:155],a[192:195],v[164:167]
s_waitcnt lgkmcnt(5)
ds_read_b64_tr_b16 a[134:135],v22 offset:18432
v_mfma_f32_16x16x32_bf16 v[164:167],a[156:159],a[196:199],v[164:167]
s_add_u32 m0,s58,5120
ds_read_b64_tr_b16 a[136:137],v22 offset:8
v_mfma_f32_16x16x32_bf16 v[172:175],a[156:159],a[204:207],v[172:175]
ds_read_b64_tr_b16 a[138:139],v22 offset:2056
v_mfma_f32_16x16x32_bf16 v[172:175],a[152:155],a[200:203],v[172:175]
ds_read_b64_tr_b16 a[140:141],v22 offset:16392
v_mfma_f32_16x16x32_bf16 v[180:183],a[152:155],a[208:211],v[180:183]
buffer_load_dwordx4 v61,s[32:35],s56 offen lds
v_mfma_f32_16x16x32_bf16 v[180:183],a[156:159],a[212:215],v[180:183]
ds_read_b64_tr_b16 a[142:143],v22 offset:18440
v_mfma_f32_16x16x32_bf16 v[188:191],a[156:159],a[220:223],v[188:191]
ds_read_b64_tr_b16 a[144:145],v22 offset:256
v_mfma_f32_16x16x32_bf16 v[188:191],a[152:155],a[216:219],v[188:191]
s_add_u32 m0,s58,6144
ds_read_b64_tr_b16 a[146:147],v22 offset:2304
v_mfma_f32_16x16x32_bf16 v[192:195],a[160:163],a[192:195],v[192:195]
ds_read_b64_tr_b16 a[148:149],v22 offset:16640
v_mfma_f32_16x16x32_bf16 v[192:195],a[164:167],a[196:199],v[192:195]
ds_read_b64_tr_b16 a[150:151],v22 offset:18688
v_mfma_f32_16x16x32_bf16 v[200:203],a[164:167],a[204:207],v[200:203]
buffer_load_dwordx4 v62,s[32:35],s56 offen lds
v_mfma_f32_16x16x32_bf16 v[200:203],a[160:163],a[200:203],v[200:203]
ds_read_b64_tr_b16 a[152:153],v22 offset:264
v_mfma_f32_16x16x32_bf16 v[208:211],a[160:163],a[208:211],v[208:211]
s_add_u32 m0,s58,7168
s_add_u32 s58,s58,32768
ds_read_b64_tr_b16 a[154:155],v22 offset:2312
v_mfma_f32_16x16x32_bf16 v[208:211],a[164:167],a[212:215],v[208:211]
s_cmp_ge_u32 s58,131072
s_cselect_b32 s62,98304,0
ds_read_b64_tr_b16 a[156:157],v22 offset:16648
v_mfma_f32_16x16x32_bf16 v[216:219],a[164:167],a[220:223],v[216:219]
s_sub_u32 s58,s58,s62
ds_read_b64_tr_b16 a[158:159],v22 offset:18696
v_mfma_f32_16x16x32_bf16 v[216:219],a[160:163],a[216:219],v[216:219]
buffer_load_dwordx4 v63,s[32:35],s56 offen lds
v_mfma_f32_16x16x32_bf16 v[196:199],a[168:171],a[192:195],v[196:199]
s_add_u32 s56,s56,524288
ds_read_b64_tr_b16 a[160:161],v22 offset:512
v_mfma_f32_16x16x32_bf16 v[196:199],a[172:175],a[196:199],v[196:199]
ds_read_b64_tr_b16 a[162:163],v22 offset:2560
v_mfma_f32_16x16x32_bf16 v[204:207],a[172:175],a[204:207],v[204:207]
s_add_u32 m0,s59,0
ds_read_b64_tr_b16 a[164:165],v22 offset:16896
v_mfma_f32_16x16x32_bf16 v[204:207],a[168:171],a[200:203],v[204:207]
ds_read_b64_tr_b16 a[166:167],v22 offset:18944
v_mfma_f32_16x16x32_bf16 v[212:215],a[168:171],a[208:211],v[212:215]
v_mfma_f32_16x16x32_bf16 v[212:215],a[172:175],a[212:215],v[212:215]
buffer_load_dwordx4 v48,s[28:31],s57 offen lds
v_mfma_f32_16x16x32_bf16 v[220:223],a[172:175],a[220:223],v[220:223]
ds_read_b64_tr_b16 a[172:173],v22 offset:16904
v_mfma_f32_16x16x32_bf16 v[220:223],a[168:171],a[216:219],v[220:223]
ds_read_b64_tr_b16 a[168:169],v22 offset:520
v_mfma_f32_16x16x32_bf16 v[224:227],a[176:179],a[192:195],v[224:227]
ds_read_b64_tr_b16 a[170:171],v22 offset:2568
v_mfma_f32_16x16x32_bf16 v[224:227],a[180:183],a[196:199],v[224:227]
ds_read_b64_tr_b16 a[174:175],v22 offset:18952
v_mfma_f32_16x16x32_bf16 v[232:235],a[180:183],a[204:207],v[232:235]
buffer_load_dwordx4 v49,s[28:31],s57 offen offset:1024 lds
v_mfma_f32_16x16x32_bf16 v[232:235],a[176:179],a[200:203],v[232:235]
v_mfma_f32_16x16x32_bf16 v[240:243],a[176:179],a[208:211],v[240:243]
v_mfma_f32_16x16x32_bf16 v[240:243],a[180:183],a[212:215],v[240:243]
v_mfma_f32_16x16x32_bf16 v[248:251],a[180:183],a[220:223],v[248:251]
ds_read_b64_tr_b16 a[180:181],v22 offset:17152
v_mfma_f32_16x16x32_bf16 v[248:251],a[176:179],a[216:219],v[248:251]
ds_read_b64_tr_b16 a[176:177],v22 offset:768
v_mfma_f32_16x16x32_bf16 v[228:231],a[184:187],a[192:195],v[228:231]
s_waitcnt lgkmcnt(3)
buffer_load_dwordx4 v50,s[28:31],s57 offen offset:2048 lds
v_mfma_f32_16x16x32_bf16 v[228:231],a[188:191],a[196:199],v[228:231]
ds_read_b64_tr_b16 a[178:179],v22 offset:2816
v_mfma_f32_16x16x32_bf16 v[236:239],a[188:191],a[204:207],v[236:239]
ds_read_b64_tr_b16 a[182:183],v22 offset:19200
v_mfma_f32_16x16x32_bf16 v[236:239],a[184:187],a[200:203],v[236:239]
v_mfma_f32_16x16x32_bf16 v[244:247],a[184:187],a[208:211],v[244:247]
v_mfma_f32_16x16x32_bf16 v[244:247],a[188:191],a[212:215],v[244:247]
buffer_load_dwordx4 v51,s[28:31],s57 offen offset:3072 lds
v_mfma_f32_16x16x32_bf16 v[252:255],a[188:191],a[220:223],v[252:255]
ds_read_b64_tr_b16 a[188:189],v22 offset:17160
v_mfma_f32_16x16x32_bf16 v[252:255],a[184:187],a[216:219],v[252:255]
ds_read_b64_tr_b16 a[184:185],v22 offset:776
v_mfma_f32_16x16x32_bf16 a[0:3],a[128:131],a[192:195],a[0:3]
s_add_u32 m0,s59,4096
s_xor_b32 s59,s59,131072
ds_read_b64_tr_b16 a[186:187],v22 offset:2824
v_mfma_f32_16x16x32_bf16 a[0:3],a[132:135],a[196:199],a[0:3]
ds_read_b64_tr_b16 a[190:191],v22 offset:19208
v_add_u32 v22,s60,v23
v_mfma_f32_16x16x32_bf16 a[32:35],a[132:135],a[204:207],a[32:35]
s_add_u32 s60,s60,32768
s_cmp_eq_u32 s60,131072
s_cselect_b32 s60,32768,s60
v_mfma_f32_16x16x32_bf16 a[32:35],a[128:131],a[200:203],a[32:35]
buffer_load_dwordx4 v52,s[28:31],s57 offen lds
v_mfma_f32_16x16x32_bf16 a[64:67],a[128:131],a[208:211],a[64:67]
v_mfma_f32_16x16x32_bf16 a[64:67],a[132:135],a[212:215],a[64:67]
v_mfma_f32_16x16x32_bf16 a[96:99],a[132:135],a[220:223],a[96:99]
v_mfma_f32_16x16x32_bf16 a[96:99],a[128:131],a[216:219],a[96:99]
v_mfma_f32_16x16x32_bf16 a[4:7],a[136:139],a[192:195],a[4:7]
buffer_load_dwordx4 v53,s[28:31],s57 offen offset:1024 lds
v_mfma_f32_16x16x32_bf16 a[4:7],a[140:143],a[196:199],a[4:7]
v_mfma_f32_16x16x32_bf16 a[36:39],a[140:143],a[204:207],a[36:39]
v_mfma_f32_16x16x32_bf16 a[36:39],a[136:139],a[200:203],a[36:39]
v_mfma_f32_16x16x32_bf16 a[68:71],a[136:139],a[208:211],a[68:71]
v_mfma_f32_16x16x32_bf16 a[68:71],a[140:143],a[212:215],a[68:71]
v_mfma_f32_16x16x32_bf16 a[100:103],a[140:143],a[220:223],a[100:103]
buffer_load_dwordx4 v54,s[28:31],s57 offen offset:2048 lds
v_mfma_f32_16x16x32_bf16 a[100:103],a[136:139],a[216:219],a[100:103]
v_mfma_f32_16x16x32_bf16 a[8:11],a[144:147],a[192:195],a[8:11]
v_mfma_f32_16x16x32_bf16 a[8:11],a[148:151],a[196:199],a[8:11]
v_mfma_f32_16x16x32_bf16 a[40:43],a[148:151],a[204:207],a[40:43]
v_mfma_f32_16x16x32_bf16 a[40:43],a[144:147],a[200:203],a[40:43]
buffer_load_dwordx4 v55,s[28:31],s57 offen offset:3072 lds
v_mfma_f32_16x16x32_bf16 a[72:75],a[144:147],a[208:211],a[72:75]
s_add_u32 s57,s57,128
v_mfma_f32_16x16x32_bf16 a[72:75],a[148:151],a[212:215],a[72:75]
v_mfma_f32_16x16x32_bf16 a[104:107],a[148:151],a[220:223],a[104:107]
v_mfma_f32_16x16x32_bf16 a[104:107],a[144:147],a[216:219],a[104:107]
v_mfma_f32_16x16x32_bf16 a[12:15],a[152:155],a[192:195],a[12:15]
v_mfma_f32_16x16x32_bf16 a[12:15],a[156:159],a[196:199],a[12:15]
v_mfma_f32_16x16x32_bf16 a[44:47],a[156:159],a[204:207],a[44:47]
buffer_load_dword v16,v15,s[72:75],s83 offen
v_mfma_f32_16x16x32_bf16 a[44:47],a[152:155],a[200:203],a[44:47]
s_add_u32 s83,s83,896
v_mfma_f32_16x16x32_bf16 a[76:79],a[152:155],a[208:211],a[76:79]
v_mfma_f32_16x16x32_bf16 a[76:79],a[156:159],a[212:215],a[76:79]
v_mfma_f32_16x16x32_bf16 a[108:111],a[156:159],a[220:223],a[108:111]
v_mfma_f32_16x16x32_bf16 a[108:111],a[152:155],a[216:219],a[108:111]
s_waitcnt vmcnt(17) lgkmcnt(0)
v_mfma_f32_16x16x32_bf16 a[16:19],a[160:163],a[192:195],a[16:19]
s_barrier
ds_read_b128 a[224:227],v20
v_mfma_f32_16x16x32_bf16 a[16:19],a[164:167],a[196:199],a[16:19]
ds_read_b64_tr_b16 a[128:129],v22 offset:1024
v_mfma_f32_16x16x32_bf16 a[48:51],a[164:167],a[204:207],a[48:51]
ds_read_b64_tr_b16 a[130:131],v22 offset:3072
v_mfma_f32_16x16x32_bf16 a[48:51],a[160:163],a[200:203],a[48:51]
)A"
R"A(ds_read_b128 a[228:231],v21
v_mfma_f32_16x16x32_bf16 a[80:83],a[160:163],a[208:211],a[80:83]
ds_read_b64_tr_b16 a[132:133],v22 offset:17408
v_mfma_f32_16x16x32_bf16 a[80:83],a[164:167],a[212:215],a[80:83]
ds_read_b64_tr_b16 a[134:135],v22 offset:19456
v_mfma_f32_16x16x32_bf16 a[112:115],a[164:167],a[220:223],a[112:115]
ds_read_b128 a[232:235],v20 offset:2048
v_mfma_f32_16x16x32_bf16 a[112:115],a[160:163],a[216:219],a[112:115]
ds_read_b128 a[236:239],v21 offset:2048
v_mfma_f32_16x16x32_bf16 a[20:23],a[168:171],a[192:195],a[20:23]
ds_read_b128 a[240:243],v20 offset:4096
v_mfma_f32_16x16x32_bf16 a[20:23],a[172:175],a[196:199],a[20:23]
ds_read_b128 a[244:247],v21 offset:4096
v_mfma_f32_16x16x32_bf16 a[52:55],a[172:175],a[204:207],a[52:55]
ds_read_b128 a[248:251],v20 offset:6144
v_xor_b32 v20,131072,v20
v_mfma_f32_16x16x32_bf16 a[52:55],a[168:171],a[200:203],a[52:55]
ds_read_b128 a[252:255],v21 offset:6144
v_xor_b32 v21,131072,v21
v_mfma_f32_16x16x32_bf16 a[84:87],a[168:171],a[208:211],a[84:87]
ds_read_b64_tr_b16 a[136:137],v22 offset:1032
v_mfma_f32_16x16x32_bf16 a[84:87],a[172:175],a[212:215],a[84:87]
ds_read_b64_tr_b16 a[138:139],v22 offset:3080
v_mfma_f32_16x16x32_bf16 a[116:119],a[172:175],a[220:223],a[116:119]
ds_read_b64_tr_b16 a[140:141],v22 offset:17416
v_mfma_f32_16x16x32_bf16 a[116:119],a[168:171],a[216:219],a[116:119]
ds_read_b64_tr_b16 a[142:143],v22 offset:19464
v_mfma_f32_16x16x32_bf16 a[24:27],a[176:179],a[192:195],a[24:27]
ds_read_b64_tr_b16 a[144:145],v22 offset:1280
v_mfma_f32_16x16x32_bf16 a[24:27],a[180:183],a[196:199],a[24:27]
ds_read_b64_tr_b16 a[146:147],v22 offset:3328
v_mfma_f32_16x16x32_bf16 a[56:59],a[180:183],a[204:207],a[56:59]
ds_read_b64_tr_b16 a[148:149],v22 offset:17664
v_mfma_f32_16x16x32_bf16 a[56:59],a[176:179],a[200:203],a[56:59]
ds_read_b64_tr_b16 a[150:151],v22 offset:19712
v_mfma_f32_16x16x32_bf16 a[88:91],a[176:179],a[208:211],a[88:91]
ds_read_b64_tr_b16 a[152:153],v22 offset:1288
v_mfma_f32_16x16x32_bf16 a[88:91],a[180:183],a[212:215],a[88:91]
ds_read_b64_tr_b16 a[154:155],v22 offset:3336
v_mfma_f32_16x16x32_bf16 a[120:123],a[180:183],a[220:223],a[120:123]
ds_read_b64_tr_b16 a[156:157],v22 offset:17672
v_mfma_f32_16x16x32_bf16 a[120:123],a[176:179],a[216:219],a[120:123]
ds_read_b64_tr_b16 a[158:159],v22 offset:19720
v_mfma_f32_16x16x32_bf16 a[28:31],a[184:187],a[192:195],a[28:31]
ds_read_b64_tr_b16 a[160:161],v22 offset:1536
v_mfma_f32_16x16x32_bf16 a[28:31],a[188:191],a[196:199],a[28:31]
ds_read_b64_tr_b16 a[162:163],v22 offset:3584
v_mfma_f32_16x16x32_bf16 a[60:63],a[188:191],a[204:207],a[60:63]
ds_read_b64_tr_b16 a[164:165],v22 offset:17920
v_mfma_f32_16x16x32_bf16 a[60:63],a[184:187],a[200:203],a[60:63]
ds_read_b64_tr_b16 a[166:167],v22 offset:19968
v_mfma_f32_16x16x32_bf16 a[92:95],a[184:187],a[208:211],a[92:95]
ds_read_b64_tr_b16 a[168:169],v22 offset:1544
v_mfma_f32_16x16x32_bf16 a[92:95],a[188:191],a[212:215],a[92:95]
ds_read_b64_tr_b16 a[170:171],v22 offset:3592
v_mfma_f32_16x16x32_bf16 a[124:127],a[188:191],a[220:223],a[124:127]
s_add_u32 m0,s58,0
ds_read_b64_tr_b16 a[172:173],v22 offset:17928
v_mfma_f32_16x16x32_bf16 a[124:127],a[184:187],a[216:219],a[124:127]
s_waitcnt lgkmcnt(7)
ds_read_b64_tr_b16 a[174:175],v22 offset:19976
v_mfma_f32_16x16x32_bf16 v[128:131],a[128:131],a[224:227],v[128:131]
ds_read_b64_tr_b16 a[176:177],v22 offset:1792
v_mfma_f32_16x16x32_bf16 v[128:131],a[132:135],a[228:231],v[128:131]
buffer_load_dwordx4 v56,s[32:35],s56 offen lds
v_mfma_f32_16x16x32_bf16 v[136:139],a[132:135],a[236:239],v[136:139]
ds_read_b64_tr_b16 a[178:179],v22 offset:3840
v_mfma_f32_16x16x32_bf16 v[136:139],a[128:131],a[232:235],v[136:139]
s_add_u32 m0,s58,1024
ds_read_b64_tr_b16 a[180:181],v22 offset:18176
v_mfma_f32_16x16x32_bf16 v[144:147],a[128:131],a[240:243],v[144:147]
ds_read_b64_tr_b16 a[182:183],v22 offset:20224
v_mfma_f32_16x16x32_bf16 v[144:147],a[132:135],a[244:247],v[144:147]
ds_read_b64_tr_b16 a[184:185],v22 offset:1800
v_mfma_f32_16x16x32_bf16 v[152:155],a[132:135],a[252:255],v[152:155]
buffer_load_dwordx4 v57,s[32:35],s56 offen lds
v_mfma_f32_16x16x32_bf16 v[152:155],a[128:131],a[248:251],v[152:155]
ds_read_b64_tr_b16 a[186:187],v22 offset:3848
v_mfma_f32_16x16x32_bf16 v[132:135],a[136:139],a[224:227],v[132:135]
ds_read_b64_tr_b16 a[188:189],v22 offset:18184
v_mfma_f32_16x16x32_bf16 v[132:135],a[140:143],a[228:231],v[132:135]
s_add_u32 m0,s58,2048
ds_read_b64_tr_b16 a[190:191],v22 offset:20232
v_mfma_f32_16x16x32_bf16 v[140:143],a[140:143],a[236:239],v[140:143]
ds_read_b64_tr_b16 a[128:129],v22
v_mfma_f32_16x16x32_bf16 v[140:143],a[136:139],a[232:235],v[140:143]
ds_read_b64_tr_b16 a[130:131],v22 offset:2048
v_mfma_f32_16x16x32_bf16 v[148:151],a[136:139],a[240:243],v[148:151]
buffer_load_dwordx4 v58,s[32:35],s56 offen lds
v_mfma_f32_16x16x32_bf16 v[148:151],a[140:143],a[244:247],v[148:151]
ds_read_b64_tr_b16 a[132:133],v22 offset:16384
v_mfma_f32_16x16x32_bf16 v[156:159],a[140:143],a[252:255],v[156:159]
s_add_u32 m0,s58,3072
ds_read_b64_tr_b16 a[134:135],v22 offset:18432
v_mfma_f32_16x16x32_bf16 v[156:159],a[136:139],a[248:251],v[156:159]
ds_read_b64_tr_b16 a[136:137],v22 offset:8
v_mfma_f32_16x16x32_bf16 v[160:163],a[144:147],a[224:227],v[160:163]
ds_read_b64_tr_b16 a[138:139],v22 offset:2056
v_mfma_f32_16x16x32_bf16 v[160:163],a[148:151],a[228:231],v[160:163]
buffer_load_dwordx4 v59,s[32:35],s56 offen lds
v_mfma_f32_16x16x32_bf16 v[168:171],a[148:151],a[236:239],v[168:171]
ds_read_b64_tr_b16 a[140:141],v22 offset:16392
v_mfma_f32_16x16x32_bf16 v[168:171],a[144:147],a[232:235],v[168:171]
ds_read_b64_tr_b16 a[142:143],v22 offset:18440
v_mfma_f32_16x16x32_bf16 v[176:179],a[144:147],a[240:243],v[176:179]
s_add_u32 m0,s58,4096
v_mfma_f32_16x16x32_bf16 v[176:179],a[148:151],a[244:247],v[176:179]
v_mfma_f32_16x16x32_bf16 v[184:187],a[148:151],a[252:255],v[184:187]
ds_read_b64_tr_b16 a[148:149],v22 offset:16640
v_mfma_f32_16x16x32_bf16 v[184:187],a[144:147],a[248:251],v[184:187]
buffer_load_dwordx4 v60,s[32:35],s56 offen lds
v_mfma_f32_16x16x32_bf16 v[164:167],a[152:155],a[224:227],v[164:167]
ds_read_b64_tr_b16 a[144:145],v22 offset:256
v_mfma_f32_16x16x32_bf16 v[164:167],a[156:159],a[228:231],v[164:167]
s_add_u32 m0,s58,5120
ds_read_b64_tr_b16 a[146:147],v22 offset:2304
v_mfma_f32_16x16x32_bf16 v[172:175],a[156:159],a[236:239],v[172:175]
ds_read_b64_tr_b16 a[150:151],v22 offset:18688
v_mfma_f32_16x16x32_bf16 v[172:175],a[152:155],a[232:235],v[172:175]
v_mfma_f32_16x16x32_bf16 v[180:183],a[152:155],a[240:243],v[180:183]
buffer_load_dwordx4 v61,s[32:35],s56 offen lds
v_mfma_f32_16x16x32_bf16 v[180:183],a[156:159],a[244:247],v[180:183]
v_mfma_f32_16x16x32_bf16 v[188:191],a[156:159],a[252:255],v[188:191]
ds_read_b64_tr_b16 a[156:157],v22 offset:16648
v_mfma_f32_16x16x32_bf16 v[188:191],a[152:155],a[248:251],v[188:191]
s_add_u32 m0,s58,6144
ds_read_b64_tr_b16 a[152:153],v22 offset:264
s_waitcnt lgkmcnt(5)
v_mfma_f32_16x16x32_bf16 v[192:195],a[160:163],a[224:227],v[192:195]
ds_read_b64_tr_b16 a[154:155],v22 offset:2312
v_mfma_f32_16x16x32_bf16 v[192:195],a[164:167],a[228:231],v[192:195]
ds_read_b64_tr_b16 a[158:159],v22 offset:18696
v_mfma_f32_16x16x32_bf16 v[200:203],a[164:167],a[236:239],v[200:203]
buffer_load_dwordx4 v62,s[32:35],s56 offen lds
v_mfma_f32_16x16x32_bf16 v[200:203],a[160:163],a[232:235],v[200:203]
v_mfma_f32_16x16x32_bf16 v[208:211],a[160:163],a[240:243],v[208:211]
s_add_u32 m0,s58,7168
s_add_u32 s58,s58,32768
v_mfma_f32_16x16x32_bf16 v[208:211],a[164:167],a[244:247],v[208:211]
s_cmp_ge_u32 s58,131072
s_cselect_b32 s62,98304,0
s_sub_u32 s58,s58,s62
v_mfma_f32_16x16x32_bf16 v[216:219],a[164:167],a[252:255],v[216:219]
ds_read_b64_tr_b16 a[164:165],v22 offset:16896
v_mfma_f32_16x16x32_bf16 v[216:219],a[160:163],a[248:251],v[216:219]
buffer_load_dwordx4 v63,s[32:35],s56 offen lds
v_mfma_f32_16x16x32_bf16 v[196:199],a[168:171],a[224:227],v[196:199]
s_add_u32 s56,s56,524288
ds_read_b64_tr_b16 a[160:161],v22 offset:512
v_mfma_f32_16x16x32_bf16 v[196:199],a[172:175],a[228:231],v[196:199]
ds_read_b64_tr_b16 a[162:163],v22 offset:2560
v_mfma_f32_16x16x32_bf16 v[204:207],a[172:175],a[236:239],v[204:207]
s_add_u32 m0,s59,0
ds_read_b64_tr_b16 a[166:167],v22 offset:18944
v_mfma_f32_16x16x32_bf16 v[204:207],a[168:171],a[232:235],v[204:207]
v_mfma_f32_16x16x32_bf16 v[212:215],a[168:171],a[240:243],v[212:215]
v_mfma_f32_16x16x32_bf16 v[212:215],a[172:175],a[244:247],v[212:215]
buffer_load_dwordx4 v48,s[28:31],s57 offen lds
v_mfma_f32_16x16x32_bf16 v[220:223],a[172:175],a[252:255],v[220:223]
ds_read_b64_tr_b16 a[172:173],v22 offset:16904
v_mfma_f32_16x16x32_bf16 v[220:223],a[168:171],a[248:251],v[220:223]
ds_read_b64_tr_b16 a[168:169],v22 offset:520
v_mfma_f32_16x16x32_bf16 v[224:227],a[176:179],a[224:227],v[224:227]
ds_read_b64_tr_b16 a[170:171],v22 offset:2568
v_mfma_f32_16x16x32_bf16 v[224:227],a[180:183],a[228:231],v[224:227]
ds_read_b64_tr_b16 a[174:175],v22 offset:18952
v_mfma_f32_16x16x32_bf16 v[232:235],a[180:183],a[236:239],v[232:235]
buffer_load_dwordx4 v49,s[28:31],s57 offen offset:1024 lds
v_mfma_f32_16x16x32_bf16 v[232:235],a[176:179],a[232:235],v[232:235]
v_mfma_f32_16x16x32_bf16 v[240:243],a[176:179],a[240:243],v[240:243]
v_mfma_f32_16x16x32_bf16 v[240:243],a[180:183],a[244:247],v[240:243]
v_mfma_f32_16x16x32_bf16 v[248:251],a[180:183],a[252:255],v[248:251]
ds_read_b64_tr_b16 a[180:181],v22 offset:17152
v_mfma_f32_16x16x32_bf16 v[248:251],a[176:179],a[248:251],v[248:251]
ds_read_b64_tr_b16 a[176:177],v22 offset:768
v_mfma_f32_16x16x32_bf16 v[228:231],a[184:187],a[224:227],v[228:231]
buffer_load_dwordx4 v50,s[28:31],s57 offen offset:2048 lds
v_mfma_f32_16x16x32_bf16 v[228:231],a[188:191],a[228:231],v[228:231]
ds_read_b64_tr_b16 a[178:179],v22 offset:2816
v_mfma_f32_16x16x32_bf16 v[236:239],a[188:191],a[236:239],v[236:239]
ds_read_b64_tr_b16 a[182:183],v22 offset:19200
v_mfma_f32_16x16x32_bf16 v[236:239],a[184:187],a[232:235],v[236:239]
v_mfma_f32_16x16x32_bf16 v[244:247],a[184:187],a[240:243],v[244:247]
v_mfma_f32_16x16x32_bf16 v[244:247],a[188:191],a[244:247],v[244:247]
buffer_load_dwordx4 v51,s[28:31],s57 offen offset:3072 lds
v_mfma_f32_16x16x32_bf16 v[252:255],a[188:191],a[252:255],v[252:255]
ds_read_b64_tr_b16 a[188:189],v22 offset:17160
v_mfma_f32_16x16x32_bf16 v[252:255],a[184:187],a[248:251],v[252:255]
ds_read_b64_tr_b16 a[184:185],v22 offset:776
v_mfma_f32_16x16x32_bf16 a[0:3],a[128:131],a[224:227],a[0:3]
s_add_u32 m0,s59,4096
s_xor_b32 s59,s59,131072
ds_read_b64_tr_b16 a[186:187],v22 offset:2824
v_mfma_f32_16x16x32_bf16 a[0:3],a[132:135],a[228:231],a[0:3]
ds_read_b64_tr_b16 a[190:191],v22 offset:19208
v_add_u32 v22,s60,v23
v_mfma_f32_16x16x32_bf16 a[32:35],a[132:135],a[236:239],a[32:35]
v_mfma_f32_16x16x32_bf16 a[32:35],a[128:131],a[232:235],a[32:35]
buffer_load_dwordx4 v52,s[28:31],s57 offen lds
v_mfma_f32_16x16x32_bf16 a[64:67],a[128:131],a[240:243],a[64:67]
v_mfma_f32_16x16x32_bf16 a[64:67],a[132:135],a[244:247],a[64:67]
v_mfma_f32_16x16x32_bf16 a[96:99],a[132:135],a[252:255],a[96:99]
v_mfma_f32_16x16x32_bf16 a[96:99],a[128:131],a[248:251],a[96:99]
v_mfma_f32_16x16x32_bf16 a[4:7],a[136:139],a[224:227],a[4:7]
buffer_load_dwordx4 v53,s[28:31],s57 offen offset:1024 lds
v_mfma_f32_16x16x32_bf16 a[4:7],a[140:143],a[228:231],a[4:7]
v_mfma_f32_16x16x32_bf16 a[36:39],a[140:143],a[236:239],a[36:39]
v_mfma_f32_16x16x32_bf16 a[36:39],a[136:139],a[232:235],a[36:39]
v_mfma_f32_16x16x32_bf16 a[68:71],a[136:139],a[240:243],a[68:71]
v_mfma_f32_16x16x32_bf16 a[68:71],a[140:143],a[244:247],a[68:71]
v_mfma_f32_16x16x32_bf16 a[100:103],a[140:143],a[252:255],a[100:103]
buffer_load_dwordx4 v54,s[28:31],s57 offen offset:2048 lds
v_mfma_f32_16x16x32_bf16 a[100:103],a[136:139],a[248:251],a[100:103]
s_waitcnt lgkmcnt(0)
v_mfma_f32_16x16x32_bf16 a[8:11],a[144:147],a[224:227],a[8:11]
v_mfma_f32_16x16x32_bf16 a[8:11],a[148:151],a[228:231],a[8:11]
v_mfma_f32_16x16x32_bf16 a[40:43],a[148:151],a[236:239],a[40:43]
v_mfma_f32_16x16x32_bf16 a[40:43],a[144:147],a[232:235],a[40:43]
buffer_load_dwordx4 v55,s[28:31],s57 offen offset:3072 lds
)A"
R"A(v_mfma_f32_16x16x32_bf16 a[72:75],a[144:147],a[240:243],a[72:75]
s_add_u32 s57,s57,128
v_mfma_f32_16x16x32_bf16 a[72:75],a[148:151],a[244:247],a[72:75]
v_mfma_f32_16x16x32_bf16 a[104:107],a[148:151],a[252:255],a[104:107]
v_mfma_f32_16x16x32_bf16 a[104:107],a[144:147],a[248:251],a[104:107]
v_mfma_f32_16x16x32_bf16 a[12:15],a[152:155],a[224:227],a[12:15]
v_mfma_f32_16x16x32_bf16 a[12:15],a[156:159],a[228:231],a[12:15]
v_mfma_f32_16x16x32_bf16 a[44:47],a[156:159],a[236:239],a[44:47]
buffer_load_dword v16,v15,s[72:75],s83 offen
v_mfma_f32_16x16x32_bf16 a[44:47],a[152:155],a[232:235],a[44:47]
s_add_u32 s83,s83,896
v_mfma_f32_16x16x32_bf16 a[76:79],a[152:155],a[240:243],a[76:79]
v_mfma_f32_16x16x32_bf16 a[76:79],a[156:159],a[244:247],a[76:79]
v_mfma_f32_16x16x32_bf16 a[108:111],a[156:159],a[252:255],a[108:111]
v_mfma_f32_16x16x32_bf16 a[108:111],a[152:155],a[248:251],a[108:111]
s_waitcnt vmcnt(17) lgkmcnt(0)
v_mfma_f32_16x16x32_bf16 a[16:19],a[160:163],a[224:227],a[16:19]
s_barrier
ds_read_b128 a[192:195],v20
v_mfma_f32_16x16x32_bf16 a[16:19],a[164:167],a[228:231],a[16:19]
ds_read_b128 a[196:199],v21
v_mfma_f32_16x16x32_bf16 a[48:51],a[164:167],a[236:239],a[48:51]
ds_read_b128 a[200:203],v20 offset:2048
v_mfma_f32_16x16x32_bf16 a[48:51],a[160:163],a[232:235],a[48:51]
ds_read_b128 a[204:207],v21 offset:2048
v_mfma_f32_16x16x32_bf16 a[80:83],a[160:163],a[240:243],a[80:83]
ds_read_b128 a[208:211],v20 offset:4096
v_mfma_f32_16x16x32_bf16 a[80:83],a[164:167],a[244:247],a[80:83]
ds_read_b128 a[212:215],v21 offset:4096
v_mfma_f32_16x16x32_bf16 a[112:115],a[164:167],a[252:255],a[112:115]
buffer_load_dword v26,v24,s[88:91],s93 offen
v_mfma_f32_16x16x32_bf16 a[112:115],a[160:163],a[248:251],a[112:115]
ds_read_b128 a[216:219],v20 offset:6144
v_mfma_f32_16x16x32_bf16 a[20:23],a[168:171],a[224:227],a[20:23]
ds_read_b128 a[220:223],v21 offset:6144
v_mfma_f32_16x16x32_bf16 a[20:23],a[172:175],a[228:231],a[20:23]
ds_read_b64_tr_b16 a[128:129],v22 offset:1024
v_mfma_f32_16x16x32_bf16 a[52:55],a[172:175],a[236:239],a[52:55]
ds_read_b64_tr_b16 a[130:131],v22 offset:3072
v_mfma_f32_16x16x32_bf16 a[52:55],a[168:171],a[232:235],a[52:55]
ds_read_b64_tr_b16 a[132:133],v22 offset:17408
v_mfma_f32_16x16x32_bf16 a[84:87],a[168:171],a[240:243],a[84:87]
ds_read_b64_tr_b16 a[134:135],v22 offset:19456
v_mfma_f32_16x16x32_bf16 a[84:87],a[172:175],a[244:247],a[84:87]
ds_read_b64_tr_b16 a[136:137],v22 offset:1032
v_mfma_f32_16x16x32_bf16 a[116:119],a[172:175],a[252:255],a[116:119]
ds_read_b64_tr_b16 a[138:139],v22 offset:3080
v_mfma_f32_16x16x32_bf16 a[116:119],a[168:171],a[248:251],a[116:119]
ds_read_b64_tr_b16 a[140:141],v22 offset:17416
v_mfma_f32_16x16x32_bf16 a[24:27],a[176:179],a[224:227],a[24:27]
ds_read_b64_tr_b16 a[142:143],v22 offset:19464
v_mfma_f32_16x16x32_bf16 a[24:27],a[180:183],a[228:231],a[24:27]
ds_read_b64_tr_b16 a[144:145],v22 offset:1280
v_mfma_f32_16x16x32_bf16 a[56:59],a[180:183],a[236:239],a[56:59]
ds_read_b64_tr_b16 a[146:147],v22 offset:3328
v_mfma_f32_16x16x32_bf16 a[56:59],a[176:179],a[232:235],a[56:59]
ds_read_b64_tr_b16 a[148:149],v22 offset:17664
v_mfma_f32_16x16x32_bf16 a[88:91],a[176:179],a[240:243],a[88:91]
ds_read_b64_tr_b16 a[150:151],v22 offset:19712
v_mfma_f32_16x16x32_bf16 a[88:91],a[180:183],a[244:247],a[88:91]
ds_read_b64_tr_b16 a[152:153],v22 offset:1288
v_mfma_f32_16x16x32_bf16 a[120:123],a[180:183],a[252:255],a[120:123]
ds_read_b64_tr_b16 a[154:155],v22 offset:3336
v_mfma_f32_16x16x32_bf16 a[120:123],a[176:179],a[248:251],a[120:123]
ds_read_b64_tr_b16 a[156:157],v22 offset:17672
v_mfma_f32_16x16x32_bf16 a[28:31],a[184:187],a[224:227],a[28:31]
ds_read_b64_tr_b16 a[158:159],v22 offset:19720
v_mfma_f32_16x16x32_bf16 a[28:31],a[188:191],a[228:231],a[28:31]
v_mfma_f32_16x16x32_bf16 a[60:63],a[188:191],a[236:239],a[60:63]
v_mfma_f32_16x16x32_bf16 a[60:63],a[184:187],a[232:235],a[60:63]
v_mfma_f32_16x16x32_bf16 a[92:95],a[184:187],a[240:243],a[92:95]
v_mfma_f32_16x16x32_bf16 a[92:95],a[188:191],a[244:247],a[92:95]
v_mfma_f32_16x16x32_bf16 a[124:127],a[188:191],a[252:255],a[124:127]
s_cmp_lg_u32 s53,0
v_mfma_f32_16x16x32_bf16 a[124:127],a[184:187],a[248:251],a[124:127]
s_cbranch_scc1 L_qp_%=
L_qpx_%=:
L_tailseg_%=:
s_waitcnt lgkmcnt(1)
v_mfma_f32_16x16x32_bf16 v[128:131],a[128:131],a[192:195],v[128:131]
s_add_u32 m0,98304,s51
ds_read_b64_tr_b16 a[160:161],v22 offset:1536
v_xor_b32 v20,131072,v20
v_mfma_f32_16x16x32_bf16 v[128:131],a[132:135],a[196:199],v[128:131]
s_add_u32 s54,s54,16384
buffer_load_dwordx4 v4,s[20:23],s47 offen nt lds
v_mfma_f32_16x16x32_bf16 v[136:139],a[132:135],a[204:207],v[136:139]
s_and_b32 s46,s54,0xfffc0000
ds_read_b64_tr_b16 a[162:163],v22 offset:3584
v_xor_b32 v21,131072,v21
v_mfma_f32_16x16x32_bf16 v[136:139],a[128:131],a[200:203],v[136:139]
s_add_u32 s54,s54,16384
ds_read_b64_tr_b16 a[164:165],v22 offset:17920
v_and_b32 v18,48,v6
v_mfma_f32_16x16x32_bf16 v[144:147],a[128:131],a[208:211],v[144:147]
s_mov_b32 s48,-98304
ds_read_b64_tr_b16 a[166:167],v22 offset:19968
v_lshrrev_b32 v19,4,v6
v_mfma_f32_16x16x32_bf16 v[144:147],a[132:135],a[212:215],v[144:147]
s_lshr_b32 s79,%1,3
ds_read_b64_tr_b16 a[168:169],v22 offset:1544
v_lshlrev_b32 v18,2,v18
v_mfma_f32_16x16x32_bf16 v[152:155],a[132:135],a[220:223],v[152:155]
s_mul_i32 s79,s79,7
ds_read_b64_tr_b16 a[170:171],v22 offset:3592
v_and_b32 v19,48,v19
v_mfma_f32_16x16x32_bf16 v[152:155],a[128:131],a[216:219],v[152:155]
s_mov_b32 s60,148
ds_read_b64_tr_b16 a[172:173],v22 offset:17928
v_add_u32 v18,v18,v19
v_mfma_f32_16x16x32_bf16 v[132:135],a[136:139],a[192:195],v[132:135]
s_lshr_b32 s79,s79,5
ds_read_b64_tr_b16 a[174:175],v22 offset:19976
v_add_u32 v18,98304,v18
v_mfma_f32_16x16x32_bf16 v[132:135],a[140:143],a[196:199],v[132:135]
s_sub_u32 s60,s60,s79
s_lshl_b32 s94,s60,2
ds_read_b64_tr_b16 a[176:177],v22 offset:1792
v_mfma_f32_16x16x32_bf16 v[140:143],a[140:143],a[204:207],v[140:143]
s_sub_u32 s94,s94,3
s_mul_i32 s83,s41,5
ds_read_b64_tr_b16 a[178:179],v22 offset:3840
v_mfma_f32_16x16x32_bf16 v[140:143],a[136:139],a[200:203],v[140:143]
s_lshl_b32 s44,s40,4
s_add_u32 s83,s83,s42
ds_read_b64_tr_b16 a[180:181],v22 offset:18176
v_mfma_f32_16x16x32_bf16 v[148:151],a[136:139],a[208:211],v[148:151]
s_and_b32 s44,s44,32
ds_read_b64_tr_b16 a[182:183],v22 offset:20224
v_subrev_u32 v25,s51,v4
v_mfma_f32_16x16x32_bf16 v[148:151],a[140:143],a[212:215],v[148:151]
s_add_u32 s83,s83,15
s_lshl_b32 s80,s40,10
ds_read_b64_tr_b16 a[184:185],v22 offset:1800
v_mfma_f32_16x16x32_bf16 v[156:159],a[140:143],a[220:223],v[156:159]
s_and_b32 s83,s83,15
ds_read_b64_tr_b16 a[186:187],v22 offset:3848
v_xor_b32 v25,s44,v25
v_mfma_f32_16x16x32_bf16 v[156:159],a[136:139],a[216:219],v[156:159]
s_add_u32 s83,s83,16
ds_read_b64_tr_b16 a[188:189],v22 offset:18184
v_add_u32 v25,s80,v25
v_mfma_f32_16x16x32_bf16 v[160:163],a[144:147],a[192:195],v[160:163]
s_add_u32 s93,s92,L_att0x_%=-L_pc_%=
v_readfirstlane_b32 s68,v13
s_nop 1
s_add_u32 s68,s68,1
v_mfma_f32_16x16x32_bf16 v[160:163],a[148:151],a[196:199],v[160:163]
s_xor_b32 s69,s68,0x9e3779b9
buffer_load_dwordx4 v4,s[20:23],s47 offen offset:1024 nt lds
v_mfma_f32_16x16x32_bf16 v[168:171],a[148:151],a[204:207],v[168:171]
s_mov_b32 s50,32768
s_mov_b32 s49,98304
s_waitcnt vmcnt(4)
ds_read_b128 a[224:227],v20
v_mfma_f32_16x16x32_bf16 v[168:171],a[144:147],a[200:203],v[168:171]
s_mov_b32 s45,0x1249
s_add_u32 s80,s80,131072
ds_read_b64_tr_b16 a[190:191],v22 offset:20232
v_mfma_f32_16x16x32_bf16 v[176:179],a[144:147],a[208:211],v[176:179]
s_lshl_b32 s83,s83,14
ds_read_b64_tr_b16 a[128:129],v22
v_mfma_f32_16x16x32_bf16 v[176:179],a[148:151],a[212:215],v[176:179]
ds_read_b64_tr_b16 a[130:131],v22 offset:2048
v_mfma_f32_16x16x32_bf16 v[184:187],a[148:151],a[220:223],v[184:187]
ds_read_b64_tr_b16 a[132:133],v22 offset:16384
v_mfma_f32_16x16x32_bf16 v[184:187],a[144:147],a[216:219],v[184:187]
ds_read_b64_tr_b16 a[134:135],v22 offset:18432
v_mfma_f32_16x16x32_bf16 v[164:167],a[152:155],a[192:195],v[164:167]
s_waitcnt lgkmcnt(6)
ds_read_b64_tr_b16 a[144:145],v22 offset:8
v_mfma_f32_16x16x32_bf16 v[164:167],a[156:159],a[196:199],v[164:167]
ds_read_b64_tr_b16 a[146:147],v22 offset:2056
v_mfma_f32_16x16x32_bf16 v[172:175],a[156:159],a[204:207],v[172:175]
ds_read_b64_tr_b16 a[148:149],v22 offset:16392
v_mfma_f32_16x16x32_bf16 v[172:175],a[152:155],a[200:203],v[172:175]
ds_read_b64_tr_b16 a[150:151],v22 offset:18440
v_mfma_f32_16x16x32_bf16 v[180:183],a[152:155],a[208:211],v[180:183]
v_mfma_f32_16x16x32_bf16 v[180:183],a[156:159],a[212:215],v[180:183]
ds_read_b128 a[228:231],v21
v_mfma_f32_16x16x32_bf16 v[188:191],a[156:159],a[220:223],v[188:191]
v_mfma_f32_16x16x32_bf16 v[188:191],a[152:155],a[216:219],v[188:191]
ds_read_b128 a[232:235],v20 offset:2048
v_mfma_f32_16x16x32_bf16 v[192:195],a[160:163],a[192:195],v[192:195]
ds_read_b128 a[236:239],v21 offset:2048
ds_read_b128 a[240:243],v20 offset:4096
ds_read_b128 a[244:247],v21 offset:4096
ds_read_b128 a[248:251],v20 offset:6144
ds_read_b128 a[252:255],v21 offset:6144
v_mfma_f32_16x16x32_bf16 v[192:195],a[164:167],a[196:199],v[192:195]
v_mfma_f32_16x16x32_bf16 v[200:203],a[164:167],a[204:207],v[200:203]
v_mfma_f32_16x16x32_bf16 v[200:203],a[160:163],a[200:203],v[200:203]
v_mfma_f32_16x16x32_bf16 v[208:211],a[160:163],a[208:211],v[208:211]
buffer_load_dwordx4 v5,s[20:23],s47 offen offset:2048 nt lds
v_mfma_f32_16x16x32_bf16 v[208:211],a[164:167],a[212:215],v[208:211]
v_mfma_f32_16x16x32_bf16 v[216:219],a[164:167],a[220:223],v[216:219]
s_waitcnt vmcnt(4) lgkmcnt(0)
v_mfma_f32_16x16x32_bf16 v[216:219],a[160:163],a[216:219],v[216:219]
s_barrier
ds_read_b64_tr_b16 a[136:137],v22 offset:32768
v_mfma_f32_16x16x32_bf16 v[196:199],a[168:171],a[192:195],v[196:199]
ds_read_b64_tr_b16 a[138:139],v22 offset:34816
v_mfma_f32_16x16x32_bf16 v[196:199],a[172:175],a[196:199],v[196:199]
ds_read_b64_tr_b16 a[140:141],v22 offset:49152
v_mfma_f32_16x16x32_bf16 v[204:207],a[172:175],a[204:207],v[204:207]
ds_read_b64_tr_b16 a[142:143],v22 offset:51200
v_mfma_f32_16x16x32_bf16 v[204:207],a[168:171],a[200:203],v[204:207]
ds_read_b64_tr_b16 a[152:153],v22 offset:32776
v_mfma_f32_16x16x32_bf16 v[212:215],a[168:171],a[208:211],v[212:215]
ds_read_b64_tr_b16 a[154:155],v22 offset:34824
v_mfma_f32_16x16x32_bf16 v[212:215],a[172:175],a[212:215],v[212:215]
ds_read_b64_tr_b16 a[156:157],v22 offset:49160
v_mfma_f32_16x16x32_bf16 v[220:223],a[172:175],a[220:223],v[220:223]
ds_read_b64_tr_b16 a[158:159],v22 offset:51208
v_mfma_f32_16x16x32_bf16 v[220:223],a[168:171],a[216:219],v[220:223]
ds_read_b64_tr_b16 a[160:161],v22 offset:256
v_mfma_f32_16x16x32_bf16 v[224:227],a[176:179],a[192:195],v[224:227]
buffer_load_dwordx4 v5,s[20:23],s47 offen offset:3072 nt lds
v_mfma_f32_16x16x32_bf16 v[224:227],a[180:183],a[196:199],v[224:227]
ds_read_b64_tr_b16 a[162:163],v22 offset:2304
v_mfma_f32_16x16x32_bf16 v[232:235],a[180:183],a[204:207],v[232:235]
ds_read_b64_tr_b16 a[164:165],v22 offset:16640
v_mfma_f32_16x16x32_bf16 v[232:235],a[176:179],a[200:203],v[232:235]
ds_read_b64_tr_b16 a[166:167],v22 offset:18688
v_mfma_f32_16x16x32_bf16 v[240:243],a[176:179],a[208:211],v[240:243]
ds_read_b64_tr_b16 a[168:169],v22 offset:33024
v_mfma_f32_16x16x32_bf16 v[240:243],a[180:183],a[212:215],v[240:243]
ds_read_b64_tr_b16 a[170:171],v22 offset:35072
v_mfma_f32_16x16x32_bf16 v[248:251],a[180:183],a[220:223],v[248:251]
ds_read_b64_tr_b16 a[172:173],v22 offset:49408
v_mfma_f32_16x16x32_bf16 v[248:251],a[176:179],a[216:219],v[248:251]
ds_read_b64_tr_b16 a[174:175],v22 offset:51456
v_mfma_f32_16x16x32_bf16 v[228:231],a[184:187],a[192:195],v[228:231]
ds_read_b64_tr_b16 a[176:177],v22 offset:264
v_mfma_f32_16x16x32_bf16 v[228:231],a[188:191],a[196:199],v[228:231]
ds_read_b64_tr_b16 a[178:179],v22 offset:2312
v_mfma_f32_16x16x32_bf16 v[236:239],a[188:191],a[204:207],v[236:239]
ds_read_b64_tr_b16 a[180:181],v22 offset:16648
v_mfma_f32_16x16x32_bf16 v[236:239],a[184:187],a[200:203],v[236:239]
ds_read_b64_tr_b16 a[182:183],v22 offset:18696
v_mfma_f32_16x16x32_bf16 v[244:247],a[184:187],a[208:211],v[244:247]
v_mfma_f32_16x16x32_bf16 v[244:247],a[188:191],a[212:215],v[244:247]
)A"
R"A(s_add_u32 m0,98304,s52
v_mfma_f32_16x16x32_bf16 v[252:255],a[188:191],a[220:223],v[252:255]
ds_read_b64_tr_b16 a[188:189],v22 offset:49416
v_mfma_f32_16x16x32_bf16 v[252:255],a[184:187],a[216:219],v[252:255]
ds_read_b64_tr_b16 a[184:185],v22 offset:33032
v_mfma_f32_16x16x32_bf16 a[0:3],a[128:131],a[192:195],a[0:3]
buffer_load_dwordx4 v4,s[24:27],s47 offen nt lds
v_mfma_f32_16x16x32_bf16 a[0:3],a[132:135],a[196:199],a[0:3]
s_waitcnt lgkmcnt(4)
ds_read_b64_tr_b16 a[186:187],v22 offset:35080
v_mfma_f32_16x16x32_bf16 a[0:3],a[136:139],a[224:227],a[0:3]
ds_read_b64_tr_b16 a[190:191],v22 offset:51464
v_mfma_f32_16x16x32_bf16 a[0:3],a[140:143],a[228:231],a[0:3]
v_mfma_f32_16x16x32_bf16 a[32:35],a[140:143],a[236:239],a[32:35]
v_mfma_f32_16x16x32_bf16 a[32:35],a[136:139],a[232:235],a[32:35]
v_mfma_f32_16x16x32_bf16 a[32:35],a[132:135],a[204:207],a[32:35]
v_mfma_f32_16x16x32_bf16 a[32:35],a[128:131],a[200:203],a[32:35]
v_mfma_f32_16x16x32_bf16 a[64:67],a[128:131],a[208:211],a[64:67]
v_mfma_f32_16x16x32_bf16 a[64:67],a[132:135],a[212:215],a[64:67]
s_nop 1
v_accvgpr_read_b32 v64,a0
v_accvgpr_read_b32 v65,a1
v_mfma_f32_16x16x32_bf16 a[64:67],a[136:139],a[240:243],a[64:67]
v_mul_f32 v64,s61,v64
v_mul_f32 v65,s61,v65
v_mfma_f32_16x16x32_bf16 a[64:67],a[140:143],a[244:247],a[64:67]
v_accvgpr_read_b32 v66,a2
v_accvgpr_read_b32 v67,a3
v_mfma_f32_16x16x32_bf16 a[96:99],a[140:143],a[252:255],a[96:99]
ds_read_b64_tr_b16 a[140:141],v22 offset:49664
v_cvt_pk_bf16_f32 v64,v64,v65
v_mfma_f32_16x16x32_bf16 a[96:99],a[136:139],a[248:251],a[96:99]
ds_read_b64_tr_b16 a[136:137],v22 offset:33280
v_accvgpr_read_b32 v80,a32
v_mfma_f32_16x16x32_bf16 a[96:99],a[132:135],a[220:223],a[96:99]
ds_read_b64_tr_b16 a[132:133],v22 offset:16896
v_accvgpr_read_b32 v81,a33
v_mfma_f32_16x16x32_bf16 a[96:99],a[128:131],a[216:219],a[96:99]
ds_read_b64_tr_b16 a[128:129],v22 offset:512
v_mul_f32 v80,s61,v80
v_mfma_f32_16x16x32_bf16 a[4:7],a[144:147],a[192:195],a[4:7]
buffer_load_dwordx4 v5,s[24:27],s47 offen offset:1024 nt lds
v_mfma_f32_16x16x32_bf16 a[4:7],a[148:151],a[196:199],a[4:7]
ds_read_b64_tr_b16 a[130:131],v22 offset:2560
v_mul_f32 v81,s61,v81
v_mfma_f32_16x16x32_bf16 a[4:7],a[152:155],a[224:227],a[4:7]
ds_read_b64_tr_b16 a[134:135],v22 offset:18944
v_accvgpr_read_b32 v82,a34
v_mfma_f32_16x16x32_bf16 a[4:7],a[156:159],a[228:231],a[4:7]
ds_read_b64_tr_b16 a[138:139],v22 offset:35328
v_accvgpr_read_b32 v83,a35
v_mfma_f32_16x16x32_bf16 a[36:39],a[156:159],a[236:239],a[36:39]
ds_read_b64_tr_b16 a[142:143],v22 offset:51712
v_mul_f32 v66,s61,v66
v_mfma_f32_16x16x32_bf16 a[36:39],a[152:155],a[232:235],a[36:39]
v_mul_f32 v67,s61,v67
v_cvt_pk_bf16_f32 v80,v80,v81
v_mfma_f32_16x16x32_bf16 a[36:39],a[148:151],a[204:207],a[36:39]
v_mul_f32 v82,s61,v82
v_mul_f32 v83,s61,v83
v_mfma_f32_16x16x32_bf16 a[36:39],a[144:147],a[200:203],a[36:39]
v_cvt_pk_bf16_f32 v65,v66,v67
v_cvt_pk_bf16_f32 v81,v82,v83
v_mfma_f32_16x16x32_bf16 a[68:71],a[144:147],a[208:211],a[68:71]
v_accvgpr_read_b32 v96,a64
v_accvgpr_read_b32 v97,a65
v_mfma_f32_16x16x32_bf16 a[68:71],a[148:151],a[212:215],a[68:71]
v_accvgpr_read_b32 v112,a96
v_accvgpr_read_b32 v113,a97
v_mfma_f32_16x16x32_bf16 a[68:71],a[152:155],a[240:243],a[68:71]
v_accvgpr_read_b32 v66,a4
v_accvgpr_read_b32 v67,a5
v_mfma_f32_16x16x32_bf16 a[68:71],a[156:159],a[244:247],a[68:71]
v_mul_f32 v96,s61,v96
v_mul_f32 v97,s61,v97
v_mfma_f32_16x16x32_bf16 a[100:103],a[156:159],a[252:255],a[100:103]
ds_read_b64_tr_b16 a[156:157],v22 offset:49672
v_accvgpr_read_b32 v98,a66
v_mfma_f32_16x16x32_bf16 a[100:103],a[152:155],a[248:251],a[100:103]
ds_read_b64_tr_b16 a[152:153],v22 offset:33288
v_accvgpr_read_b32 v82,a36
v_mfma_f32_16x16x32_bf16 a[100:103],a[148:151],a[220:223],a[100:103]
ds_read_b64_tr_b16 a[148:149],v22 offset:16904
v_accvgpr_read_b32 v83,a37
v_mfma_f32_16x16x32_bf16 a[100:103],a[144:147],a[216:219],a[100:103]
ds_read_b64_tr_b16 a[144:145],v22 offset:520
v_accvgpr_read_b32 v99,a67
v_mfma_f32_16x16x32_bf16 a[8:11],a[160:163],a[192:195],a[8:11]
buffer_load_dwordx4 v4,s[24:27],s47 offen offset:2048 nt lds
v_mfma_f32_16x16x32_bf16 a[8:11],a[164:167],a[196:199],a[8:11]
ds_read_b64_tr_b16 a[146:147],v22 offset:2568
v_mul_f32 v112,s61,v112
v_mfma_f32_16x16x32_bf16 a[8:11],a[168:171],a[224:227],a[8:11]
ds_read_b64_tr_b16 a[150:151],v22 offset:18952
v_mul_f32 v113,s61,v113
v_mfma_f32_16x16x32_bf16 a[8:11],a[172:175],a[228:231],a[8:11]
ds_read_b64_tr_b16 a[154:155],v22 offset:35336
v_accvgpr_read_b32 v114,a98
v_mfma_f32_16x16x32_bf16 a[40:43],a[172:175],a[236:239],a[40:43]
ds_read_b64_tr_b16 a[158:159],v22 offset:51720
v_accvgpr_read_b32 v115,a99
v_mfma_f32_16x16x32_bf16 a[40:43],a[168:171],a[232:235],a[40:43]
v_accvgpr_read_b32 v12,a6
v_accvgpr_read_b32 v13,a7
v_mfma_f32_16x16x32_bf16 a[40:43],a[164:167],a[204:207],a[40:43]
v_mul_f32 v66,s61,v66
v_mul_f32 v67,s61,v67
v_mfma_f32_16x16x32_bf16 a[40:43],a[160:163],a[200:203],a[40:43]
v_accvgpr_read_b32 v14,a38
v_accvgpr_read_b32 v15,a39
v_mfma_f32_16x16x32_bf16 a[72:75],a[160:163],a[208:211],a[72:75]
v_mul_f32 v82,s61,v82
v_mul_f32 v83,s61,v83
v_mfma_f32_16x16x32_bf16 a[72:75],a[164:167],a[212:215],a[72:75]
v_accvgpr_read_b32 v68,a8
v_accvgpr_read_b32 v69,a9
v_mfma_f32_16x16x32_bf16 a[72:75],a[168:171],a[240:243],a[72:75]
v_mul_f32 v68,s61,v68
v_mul_f32 v69,s61,v69
v_mfma_f32_16x16x32_bf16 a[72:75],a[172:175],a[244:247],a[72:75]
v_accvgpr_read_b32 v70,a10
v_accvgpr_read_b32 v71,a11
v_mfma_f32_16x16x32_bf16 a[104:107],a[172:175],a[252:255],a[104:107]
ds_read_b64_tr_b16 a[172:173],v22 offset:49920
v_cvt_pk_bf16_f32 v96,v96,v97
v_mfma_f32_16x16x32_bf16 a[104:107],a[168:171],a[248:251],a[104:107]
ds_read_b64_tr_b16 a[168:169],v22 offset:33536
v_mul_f32 v98,s61,v98
v_mfma_f32_16x16x32_bf16 a[104:107],a[164:167],a[220:223],a[104:107]
ds_read_b64_tr_b16 a[164:165],v22 offset:17152
v_mul_f32 v99,s61,v99
v_mfma_f32_16x16x32_bf16 a[104:107],a[160:163],a[216:219],a[104:107]
ds_read_b64_tr_b16 a[160:161],v22 offset:768
v_cvt_pk_bf16_f32 v112,v112,v113
v_mfma_f32_16x16x32_bf16 a[12:15],a[176:179],a[192:195],a[12:15]
s_waitcnt lgkmcnt(4)
buffer_load_dwordx4 v5,s[24:27],s47 offen offset:3072 nt lds
v_mfma_f32_16x16x32_bf16 a[12:15],a[180:183],a[196:199],a[12:15]
s_add_u32 s47,s47,16384
s_and_b32 s47,s47,0x3ffff
s_or_b32 s47,s47,s46
v_mfma_f32_16x16x32_bf16 a[12:15],a[184:187],a[224:227],a[12:15]
s_and_b32 s46,s54,0xfffc0000
ds_read_b64_tr_b16 a[162:163],v22 offset:2816
v_mul_f32 v114,s61,v114
v_mfma_f32_16x16x32_bf16 a[12:15],a[188:191],a[228:231],a[12:15]
s_add_u32 s54,s54,16384
ds_read_b64_tr_b16 a[166:167],v22 offset:19200
v_mul_f32 v115,s61,v115
v_mfma_f32_16x16x32_bf16 a[44:47],a[188:191],a[236:239],a[44:47]
ds_read_b64_tr_b16 a[170:171],v22 offset:35584
v_cvt_pk_bf16_f32 v68,v68,v69
v_mfma_f32_16x16x32_bf16 a[44:47],a[184:187],a[232:235],a[44:47]
ds_read_b64_tr_b16 a[174:175],v22 offset:51968
v_mul_f32 v70,s61,v70
v_mfma_f32_16x16x32_bf16 a[44:47],a[180:183],a[204:207],a[44:47]
v_mul_f32 v71,s61,v71
v_mul_f32 v12,s61,v12
v_mfma_f32_16x16x32_bf16 a[44:47],a[176:179],a[200:203],a[44:47]
v_mul_f32 v13,s61,v13
v_cvt_pk_bf16_f32 v66,v66,v67
v_mfma_f32_16x16x32_bf16 a[76:79],a[176:179],a[208:211],a[76:79]
v_mul_f32 v14,s61,v14
v_mul_f32 v15,s61,v15
v_mfma_f32_16x16x32_bf16 a[76:79],a[180:183],a[212:215],a[76:79]
v_cvt_pk_bf16_f32 v82,v82,v83
v_cvt_pk_bf16_f32 v97,v98,v99
v_mfma_f32_16x16x32_bf16 a[76:79],a[184:187],a[240:243],a[76:79]
v_cvt_pk_bf16_f32 v113,v114,v115
v_cvt_pk_bf16_f32 v69,v70,v71
v_mfma_f32_16x16x32_bf16 a[76:79],a[188:191],a[244:247],a[76:79]
v_accvgpr_read_b32 v84,a40
v_accvgpr_read_b32 v85,a41
v_mfma_f32_16x16x32_bf16 a[108:111],a[188:191],a[252:255],a[108:111]
ds_read_b64_tr_b16 a[188:189],v22 offset:49928
v_accvgpr_read_b32 v100,a72
v_mfma_f32_16x16x32_bf16 a[108:111],a[184:187],a[248:251],a[108:111]
ds_read_b64_tr_b16 a[184:185],v22 offset:33544
v_accvgpr_read_b32 v101,a73
v_mfma_f32_16x16x32_bf16 a[108:111],a[180:183],a[220:223],a[108:111]
ds_read_b64_tr_b16 a[180:181],v22 offset:17160
v_accvgpr_read_b32 v116,a104
v_mfma_f32_16x16x32_bf16 a[108:111],a[176:179],a[216:219],a[108:111]
ds_read_b64_tr_b16 a[176:177],v22 offset:776
v_accvgpr_read_b32 v117,a105
v_mfma_f32_16x16x32_bf16 a[16:19],a[128:131],a[192:195],a[16:19]
ds_read_b64_tr_b16 a[178:179],v22 offset:2824
v_cvt_pk_bf16_f32 v67,v12,v13
v_mfma_f32_16x16x32_bf16 a[16:19],a[132:135],a[196:199],a[16:19]
ds_read_b64_tr_b16 a[182:183],v22 offset:19208
v_cvt_pk_bf16_f32 v83,v14,v15
v_mfma_f32_16x16x32_bf16 a[16:19],a[136:139],a[224:227],a[16:19]
ds_read_b64_tr_b16 a[186:187],v22 offset:35592
v_accvgpr_read_b32 v98,a68
v_mfma_f32_16x16x32_bf16 a[16:19],a[140:143],a[228:231],a[16:19]
s_add_u32 m0,0,s51
ds_read_b64_tr_b16 a[190:191],v22 offset:51976
v_accvgpr_read_b32 v99,a69
v_mfma_f32_16x16x32_bf16 a[48:51],a[140:143],a[236:239],a[48:51]
v_accvgpr_read_b32 v114,a100
v_accvgpr_read_b32 v115,a101
v_mfma_f32_16x16x32_bf16 a[48:51],a[136:139],a[232:235],a[48:51]
v_accvgpr_read_b32 v70,a12
v_accvgpr_read_b32 v71,a13
v_mfma_f32_16x16x32_bf16 a[48:51],a[132:135],a[204:207],a[48:51]
buffer_load_dwordx4 v4,s[20:23],s47 offen nt lds
v_mfma_f32_16x16x32_bf16 a[48:51],a[128:131],a[200:203],a[48:51]
v_mul_f32 v84,s61,v84
v_mul_f32 v85,s61,v85
v_mfma_f32_16x16x32_bf16 a[80:83],a[128:131],a[208:211],a[80:83]
v_accvgpr_read_b32 v86,a42
v_accvgpr_read_b32 v87,a43
v_mfma_f32_16x16x32_bf16 a[80:83],a[132:135],a[212:215],a[80:83]
v_mul_f32 v100,s61,v100
v_mul_f32 v101,s61,v101
v_mfma_f32_16x16x32_bf16 a[80:83],a[136:139],a[240:243],a[80:83]
v_accvgpr_read_b32 v102,a74
v_accvgpr_read_b32 v103,a75
v_mfma_f32_16x16x32_bf16 a[80:83],a[140:143],a[244:247],a[80:83]
v_mul_f32 v116,s61,v116
v_mul_f32 v117,s61,v117
v_mfma_f32_16x16x32_bf16 a[112:115],a[140:143],a[252:255],a[112:115]
ds_read_b64_tr_b16 a[140:141],v22 offset:50184
v_accvgpr_read_b32 v118,a106
v_mfma_f32_16x16x32_bf16 a[112:115],a[136:139],a[248:251],a[112:115]
ds_read_b64_tr_b16 a[136:137],v22 offset:33800
v_accvgpr_read_b32 v119,a107
v_mfma_f32_16x16x32_bf16 a[112:115],a[132:135],a[220:223],a[112:115]
ds_read_b64_tr_b16 a[132:133],v22 offset:50176
v_accvgpr_read_b32 v16,a70
v_mfma_f32_16x16x32_bf16 a[112:115],a[128:131],a[216:219],a[112:115]
ds_read_b64_tr_b16 a[128:129],v22 offset:33792
v_accvgpr_read_b32 v17,a71
v_mfma_f32_16x16x32_bf16 a[20:23],a[144:147],a[192:195],a[20:23]
ds_read_b64_tr_b16 a[130:131],v22 offset:35840
v_mul_f32 v98,s61,v98
v_mfma_f32_16x16x32_bf16 a[20:23],a[148:151],a[196:199],a[20:23]
ds_read_b64_tr_b16 a[134:135],v22 offset:52224
v_mul_f32 v99,s61,v99
v_mfma_f32_16x16x32_bf16 a[20:23],a[152:155],a[224:227],a[20:23]
ds_read_b64_tr_b16 a[138:139],v22 offset:35848
v_accvgpr_read_b32 v12,a102
v_mfma_f32_16x16x32_bf16 a[20:23],a[156:159],a[228:231],a[20:23]
ds_read_b64_tr_b16 a[142:143],v22 offset:52232
v_accvgpr_read_b32 v13,a103
v_mfma_f32_16x16x32_bf16 a[52:55],a[156:159],a[236:239],a[52:55]
v_mul_f32 v114,s61,v114
v_mul_f32 v115,s61,v115
v_mfma_f32_16x16x32_bf16 a[52:55],a[152:155],a[232:235],a[52:55]
v_accvgpr_read_b32 v14,a14
v_accvgpr_read_b32 v15,a15
v_mfma_f32_16x16x32_bf16 a[52:55],a[148:151],a[204:207],a[52:55]
buffer_load_dwordx4 v4,s[20:23],s47 offen offset:1024 nt lds
v_mfma_f32_16x16x32_bf16 a[52:55],a[144:147],a[200:203],a[52:55]
v_mul_f32 v70,s61,v70
v_mul_f32 v71,s61,v71
v_mfma_f32_16x16x32_bf16 a[84:87],a[144:147],a[208:211],a[84:87]
v_cvt_pk_bf16_f32 v84,v84,v85
v_mul_f32 v86,s61,v86
v_mfma_f32_16x16x32_bf16 a[84:87],a[148:151],a[212:215],a[84:87]
v_mul_f32 v87,s61,v87
v_cvt_pk_bf16_f32 v100,v100,v101
v_mfma_f32_16x16x32_bf16 a[84:87],a[152:155],a[240:243],a[84:87]
v_mul_f32 v102,s61,v102
v_mul_f32 v103,s61,v103
v_mfma_f32_16x16x32_bf16 a[84:87],a[156:159],a[244:247],a[84:87]
v_cvt_pk_bf16_f32 v116,v116,v117
v_mul_f32 v118,s61,v118
v_mfma_f32_16x16x32_bf16 a[116:119],a[156:159],a[252:255],a[116:119]
ds_read_b64_tr_b16 a[156:157],v22 offset:50440
v_mul_f32 v119,s61,v119
v_mfma_f32_16x16x32_bf16 a[116:119],a[152:155],a[248:251],a[116:119]
ds_read_b64_tr_b16 a[152:153],v22 offset:34056
v_mul_f32 v16,s61,v16
v_mfma_f32_16x16x32_bf16 a[116:119],a[148:151],a[220:223],a[116:119]
)A"
R"A(ds_read_b64_tr_b16 a[148:149],v22 offset:50432
v_mul_f32 v17,s61,v17
v_mfma_f32_16x16x32_bf16 a[116:119],a[144:147],a[216:219],a[116:119]
s_waitcnt lgkmcnt(3)
ds_read_b64_tr_b16 a[144:145],v22 offset:34048
v_cvt_pk_bf16_f32 v98,v98,v99
v_mfma_f32_16x16x32_bf16 a[24:27],a[160:163],a[192:195],a[24:27]
ds_read_b64_tr_b16 a[146:147],v22 offset:36096
v_mul_f32 v12,s61,v12
v_mfma_f32_16x16x32_bf16 a[24:27],a[164:167],a[196:199],a[24:27]
ds_read_b64_tr_b16 a[150:151],v22 offset:52480
v_mul_f32 v13,s61,v13
v_mfma_f32_16x16x32_bf16 a[24:27],a[168:171],a[224:227],a[24:27]
ds_read_b64_tr_b16 a[154:155],v22 offset:36104
v_cvt_pk_bf16_f32 v114,v114,v115
v_mfma_f32_16x16x32_bf16 a[24:27],a[172:175],a[228:231],a[24:27]
ds_read_b64_tr_b16 a[158:159],v22 offset:52488
v_mul_f32 v14,s61,v14
v_mfma_f32_16x16x32_bf16 a[56:59],a[172:175],a[236:239],a[56:59]
v_mul_f32 v15,s61,v15
v_cvt_pk_bf16_f32 v70,v70,v71
v_mfma_f32_16x16x32_bf16 a[56:59],a[168:171],a[232:235],a[56:59]
v_cvt_pk_bf16_f32 v85,v86,v87
v_cvt_pk_bf16_f32 v101,v102,v103
v_mfma_f32_16x16x32_bf16 a[56:59],a[164:167],a[204:207],a[56:59]
buffer_load_dwordx4 v5,s[20:23],s47 offen offset:2048 nt lds
v_mfma_f32_16x16x32_bf16 a[56:59],a[160:163],a[200:203],a[56:59]
v_cvt_pk_bf16_f32 v117,v118,v119
v_accvgpr_read_b32 v72,a16
v_mfma_f32_16x16x32_bf16 a[88:91],a[160:163],a[208:211],a[88:91]
v_accvgpr_read_b32 v73,a17
v_accvgpr_read_b32 v88,a48
v_mfma_f32_16x16x32_bf16 a[88:91],a[164:167],a[212:215],a[88:91]
v_accvgpr_read_b32 v89,a49
v_accvgpr_read_b32 v104,a80
v_mfma_f32_16x16x32_bf16 a[88:91],a[168:171],a[240:243],a[88:91]
v_accvgpr_read_b32 v105,a81
v_cvt_pk_bf16_f32 v99,v16,v17
v_mfma_f32_16x16x32_bf16 a[88:91],a[172:175],a[244:247],a[88:91]
v_cvt_pk_bf16_f32 v115,v12,v13
v_cvt_pk_bf16_f32 v71,v14,v15
v_mfma_f32_16x16x32_bf16 a[120:123],a[172:175],a[252:255],a[120:123]
ds_read_b64_tr_b16 a[172:173],v22 offset:50696
v_accvgpr_read_b32 v86,a44
v_mfma_f32_16x16x32_bf16 a[120:123],a[168:171],a[248:251],a[120:123]
ds_read_b64_tr_b16 a[168:169],v22 offset:34312
v_accvgpr_read_b32 v87,a45
v_mfma_f32_16x16x32_bf16 a[120:123],a[164:167],a[220:223],a[120:123]
ds_read_b64_tr_b16 a[164:165],v22 offset:50688
v_accvgpr_read_b32 v102,a76
v_mfma_f32_16x16x32_bf16 a[120:123],a[160:163],a[216:219],a[120:123]
ds_read_b64_tr_b16 a[160:161],v22 offset:34304
v_accvgpr_read_b32 v103,a77
v_mfma_f32_16x16x32_bf16 a[28:31],a[176:179],a[192:195],a[28:31]
ds_read_b64_tr_b16 a[162:163],v22 offset:36352
v_accvgpr_read_b32 v118,a108
v_mfma_f32_16x16x32_bf16 a[28:31],a[180:183],a[196:199],a[28:31]
ds_read_b64_tr_b16 a[166:167],v22 offset:52736
v_accvgpr_read_b32 v119,a109
v_mfma_f32_16x16x32_bf16 a[28:31],a[184:187],a[224:227],a[28:31]
ds_read_b64_tr_b16 a[170:171],v22 offset:36360
v_mul_f32 v72,s61,v72
v_mfma_f32_16x16x32_bf16 a[28:31],a[188:191],a[228:231],a[28:31]
ds_read_b64_tr_b16 a[174:175],v22 offset:52744
v_mul_f32 v73,s61,v73
v_mfma_f32_16x16x32_bf16 a[60:63],a[188:191],a[236:239],a[60:63]
v_accvgpr_read_b32 v74,a18
v_accvgpr_read_b32 v75,a19
v_mfma_f32_16x16x32_bf16 a[60:63],a[184:187],a[232:235],a[60:63]
v_mul_f32 v88,s61,v88
v_mul_f32 v89,s61,v89
v_mfma_f32_16x16x32_bf16 a[60:63],a[180:183],a[204:207],a[60:63]
buffer_load_dwordx4 v5,s[20:23],s47 offen offset:3072 nt lds
v_mfma_f32_16x16x32_bf16 a[60:63],a[176:179],a[200:203],a[60:63]
v_accvgpr_read_b32 v90,a50
v_accvgpr_read_b32 v91,a51
v_mfma_f32_16x16x32_bf16 a[92:95],a[176:179],a[208:211],a[92:95]
v_mul_f32 v104,s61,v104
v_mul_f32 v105,s61,v105
v_mfma_f32_16x16x32_bf16 a[92:95],a[180:183],a[212:215],a[92:95]
v_accvgpr_read_b32 v106,a82
v_accvgpr_read_b32 v107,a83
v_mfma_f32_16x16x32_bf16 a[92:95],a[184:187],a[240:243],a[92:95]
v_accvgpr_read_b32 v16,a46
v_accvgpr_read_b32 v17,a47
v_mfma_f32_16x16x32_bf16 a[92:95],a[188:191],a[244:247],a[92:95]
v_mul_f32 v86,s61,v86
v_mul_f32 v87,s61,v87
v_mfma_f32_16x16x32_bf16 a[124:127],a[188:191],a[252:255],a[124:127]
ds_read_b64_tr_b16 a[188:189],v22 offset:50952
v_accvgpr_read_b32 v12,a78
v_mfma_f32_16x16x32_bf16 a[124:127],a[184:187],a[248:251],a[124:127]
ds_read_b64_tr_b16 a[184:185],v22 offset:34568
v_accvgpr_read_b32 v13,a79
v_mfma_f32_16x16x32_bf16 a[124:127],a[180:183],a[220:223],a[124:127]
ds_read_b64_tr_b16 a[180:181],v22 offset:50944
v_mul_f32 v102,s61,v102
v_mfma_f32_16x16x32_bf16 a[124:127],a[176:179],a[216:219],a[124:127]
ds_read_b64_tr_b16 a[176:177],v22 offset:34560
v_mul_f32 v103,s61,v103
v_mfma_f32_16x16x32_bf16 v[128:131],a[128:131],a[224:227],v[128:131]
ds_read_b64_tr_b16 a[178:179],v22 offset:36608
v_accvgpr_read_b32 v14,a110
v_mfma_f32_16x16x32_bf16 v[128:131],a[132:135],a[228:231],v[128:131]
ds_read_b64_tr_b16 a[182:183],v22 offset:52992
v_accvgpr_read_b32 v15,a111
v_mfma_f32_16x16x32_bf16 v[136:139],a[132:135],a[236:239],v[136:139]
ds_read_b64_tr_b16 a[186:187],v22 offset:36616
v_mul_f32 v118,s61,v118
v_mfma_f32_16x16x32_bf16 v[136:139],a[128:131],a[232:235],v[136:139]
s_add_u32 m0,0,s52
ds_read_b64_tr_b16 a[190:191],v22 offset:53000
v_mul_f32 v119,s61,v119
v_mfma_f32_16x16x32_bf16 v[144:147],a[128:131],a[240:243],v[144:147]
v_cvt_pk_bf16_f32 v72,v72,v73
v_mul_f32 v74,s61,v74
v_mfma_f32_16x16x32_bf16 v[144:147],a[132:135],a[244:247],v[144:147]
v_mul_f32 v75,s61,v75
v_cvt_pk_bf16_f32 v88,v88,v89
v_mfma_f32_16x16x32_bf16 v[152:155],a[132:135],a[252:255],v[152:155]
buffer_load_dwordx4 v4,s[24:27],s47 offen nt lds
v_mfma_f32_16x16x32_bf16 v[152:155],a[128:131],a[248:251],v[152:155]
v_mul_f32 v90,s61,v90
v_mul_f32 v91,s61,v91
v_mfma_f32_16x16x32_bf16 v[132:135],a[136:139],a[224:227],v[132:135]
v_cvt_pk_bf16_f32 v104,v104,v105
v_mul_f32 v106,s61,v106
v_mfma_f32_16x16x32_bf16 v[132:135],a[140:143],a[228:231],v[132:135]
v_mul_f32 v107,s61,v107
v_mul_f32 v16,s61,v16
v_mfma_f32_16x16x32_bf16 v[140:143],a[140:143],a[236:239],v[140:143]
v_mul_f32 v17,s61,v17
v_cvt_pk_bf16_f32 v86,v86,v87
v_mfma_f32_16x16x32_bf16 v[140:143],a[136:139],a[232:235],v[140:143]
v_mul_f32 v12,s61,v12
v_mul_f32 v13,s61,v13
v_mfma_f32_16x16x32_bf16 v[148:151],a[136:139],a[240:243],v[148:151]
v_cvt_pk_bf16_f32 v102,v102,v103
v_mul_f32 v14,s61,v14
v_mfma_f32_16x16x32_bf16 v[148:151],a[140:143],a[244:247],v[148:151]
v_mul_f32 v15,s61,v15
v_cvt_pk_bf16_f32 v118,v118,v119
v_mfma_f32_16x16x32_bf16 v[156:159],a[140:143],a[252:255],v[156:159]
s_waitcnt vmcnt(5) lgkmcnt(0)
v_cvt_pk_bf16_f32 v73,v74,v75
v_cvt_pk_bf16_f32 v89,v90,v91
v_mfma_f32_16x16x32_bf16 v[156:159],a[136:139],a[248:251],v[156:159]
s_barrier
ds_read_b128 v[48:51],v18
v_cvt_pk_bf16_f32 v105,v106,v107
v_mfma_f32_16x16x32_bf16 v[160:163],a[144:147],a[224:227],v[160:163]
v_cvt_pk_bf16_f32 v87,v16,v17
v_cvt_pk_bf16_f32 v103,v12,v13
v_mfma_f32_16x16x32_bf16 v[160:163],a[148:151],a[228:231],v[160:163]
ds_read_b128 v[52:55],v18 offset:256
v_cvt_pk_bf16_f32 v119,v14,v15
v_mfma_f32_16x16x32_bf16 v[168:171],a[148:151],a[236:239],v[168:171]
v_accvgpr_read_b32 v74,a20
v_accvgpr_read_b32 v75,a21
v_mfma_f32_16x16x32_bf16 v[168:171],a[144:147],a[232:235],v[168:171]
ds_read_b128 v[56:59],v18 offset:512
v_accvgpr_read_b32 v90,a52
v_mfma_f32_16x16x32_bf16 v[176:179],a[144:147],a[240:243],v[176:179]
v_accvgpr_read_b32 v91,a53
v_accvgpr_read_b32 v106,a84
v_mfma_f32_16x16x32_bf16 v[176:179],a[148:151],a[244:247],v[176:179]
buffer_load_dwordx4 v5,s[24:27],s47 offen offset:1024 nt lds
v_mfma_f32_16x16x32_bf16 v[184:187],a[148:151],a[252:255],v[184:187]
ds_read_b128 v[60:63],v18 offset:768
v_accvgpr_read_b32 v107,a85
v_mfma_f32_16x16x32_bf16 v[184:187],a[144:147],a[248:251],v[184:187]
v_accvgpr_read_b32 v16,a22
v_accvgpr_read_b32 v17,a23
v_mfma_f32_16x16x32_bf16 v[164:167],a[152:155],a[224:227],v[164:167]
v_mul_f32 v74,s61,v74
v_mul_f32 v75,s61,v75
v_mfma_f32_16x16x32_bf16 v[164:167],a[156:159],a[228:231],v[164:167]
v_accvgpr_read_b32 v12,a54
v_accvgpr_read_b32 v13,a55
v_mfma_f32_16x16x32_bf16 v[172:175],a[156:159],a[236:239],v[172:175]
v_mul_f32 v90,s61,v90
v_mul_f32 v91,s61,v91
v_mfma_f32_16x16x32_bf16 v[172:175],a[152:155],a[232:235],v[172:175]
v_accvgpr_read_b32 v14,a86
v_accvgpr_read_b32 v15,a87
v_mfma_f32_16x16x32_bf16 v[180:183],a[152:155],a[240:243],v[180:183]
v_mul_f32 v106,s61,v106
v_mul_f32 v107,s61,v107
v_mfma_f32_16x16x32_bf16 v[180:183],a[156:159],a[244:247],v[180:183]
v_mul_f32 v16,s61,v16
v_mul_f32 v17,s61,v17
v_mfma_f32_16x16x32_bf16 v[188:191],a[156:159],a[252:255],v[188:191]
v_cvt_pk_bf16_f32 v74,v74,v75
v_mul_f32 v12,s61,v12
v_mfma_f32_16x16x32_bf16 v[188:191],a[152:155],a[248:251],v[188:191]
v_mul_f32 v13,s61,v13
v_cvt_pk_bf16_f32 v90,v90,v91
v_mfma_f32_16x16x32_bf16 v[192:195],a[160:163],a[224:227],v[192:195]
v_mul_f32 v14,s61,v14
v_mul_f32 v15,s61,v15
v_mfma_f32_16x16x32_bf16 v[192:195],a[164:167],a[228:231],v[192:195]
v_cvt_pk_bf16_f32 v106,v106,v107
v_cvt_pk_bf16_f32 v75,v16,v17
v_mfma_f32_16x16x32_bf16 v[200:203],a[164:167],a[236:239],v[200:203]
v_cvt_pk_bf16_f32 v91,v12,v13
v_cvt_pk_bf16_f32 v107,v14,v15
v_mfma_f32_16x16x32_bf16 v[200:203],a[160:163],a[232:235],v[200:203]
v_accvgpr_read_b32 v76,a24
v_accvgpr_read_b32 v77,a25
v_accvgpr_read_b32 v78,a26
v_accvgpr_read_b32 v79,a27
v_accvgpr_read_b32 v12,a28
v_accvgpr_read_b32 v13,a29
v_accvgpr_read_b32 v14,a30
v_accvgpr_read_b32 v15,a31
v_pk_mul_f32 v[76:77],s[60:61],v[76:77] op_sel:[1,0] op_sel_hi:[1,1]
v_pk_mul_f32 v[78:79],s[60:61],v[78:79] op_sel:[1,0] op_sel_hi:[1,1]
v_pk_mul_f32 v[12:13],s[60:61],v[12:13] op_sel:[1,0] op_sel_hi:[1,1]
v_pk_mul_f32 v[14:15],s[60:61],v[14:15] op_sel:[1,0] op_sel_hi:[1,1]
v_cvt_pk_bf16_f32 v76,v76,v77
v_cvt_pk_bf16_f32 v77,v78,v79
v_cvt_pk_bf16_f32 v78,v12,v13
v_cvt_pk_bf16_f32 v79,v14,v15
v_mfma_f32_16x16x32_bf16 v[208:211],a[160:163],a[240:243],v[208:211]
v_accvgpr_read_b32 v92,a56
v_accvgpr_read_b32 v93,a57
v_accvgpr_read_b32 v94,a58
v_accvgpr_read_b32 v95,a59
v_accvgpr_read_b32 v12,a60
v_accvgpr_read_b32 v13,a61
v_accvgpr_read_b32 v14,a62
v_accvgpr_read_b32 v15,a63
v_pk_mul_f32 v[92:93],s[60:61],v[92:93] op_sel:[1,0] op_sel_hi:[1,1]
v_pk_mul_f32 v[94:95],s[60:61],v[94:95] op_sel:[1,0] op_sel_hi:[1,1]
v_pk_mul_f32 v[12:13],s[60:61],v[12:13] op_sel:[1,0] op_sel_hi:[1,1]
v_pk_mul_f32 v[14:15],s[60:61],v[14:15] op_sel:[1,0] op_sel_hi:[1,1]
v_cvt_pk_bf16_f32 v92,v92,v93
v_cvt_pk_bf16_f32 v93,v94,v95
v_cvt_pk_bf16_f32 v94,v12,v13
v_cvt_pk_bf16_f32 v95,v14,v15
v_mfma_f32_16x16x32_bf16 v[208:211],a[164:167],a[244:247],v[208:211]
buffer_load_dwordx4 v4,s[24:27],s47 offen offset:2048 nt lds
v_mfma_f32_16x16x32_bf16 v[216:219],a[164:167],a[252:255],v[216:219]
ds_read_b128 a[128:131],v1
v_accvgpr_read_b32 v120,a112
v_mfma_f32_16x16x32_bf16 v[216:219],a[160:163],a[248:251],v[216:219]
v_accvgpr_read_b32 v121,a113
v_mul_f32 v120,s61,v120
v_mfma_f32_16x16x32_bf16 v[196:199],a[168:171],a[224:227],v[196:199]
v_mul_f32 v121,s61,v121
v_accvgpr_read_b32 v122,a114
v_mfma_f32_16x16x32_bf16 v[196:199],a[172:175],a[228:231],v[196:199]
ds_read_b128 a[132:135],v1 offset:256
v_accvgpr_read_b32 v123,a115
v_mfma_f32_16x16x32_bf16 v[204:207],a[172:175],a[236:239],v[204:207]
v_accvgpr_read_b32 v108,a88
v_accvgpr_read_b32 v109,a89
v_accvgpr_read_b32 v110,a90
v_accvgpr_read_b32 v111,a91
v_accvgpr_read_b32 v12,a92
v_accvgpr_read_b32 v13,a93
v_accvgpr_read_b32 v14,a94
v_accvgpr_read_b32 v15,a95
v_pk_mul_f32 v[108:109],s[60:61],v[108:109] op_sel:[1,0] op_sel_hi:[1,1]
v_pk_mul_f32 v[110:111],s[60:61],v[110:111] op_sel:[1,0] op_sel_hi:[1,1]
v_pk_mul_f32 v[12:13],s[60:61],v[12:13] op_sel:[1,0] op_sel_hi:[1,1]
v_pk_mul_f32 v[14:15],s[60:61],v[14:15] op_sel:[1,0] op_sel_hi:[1,1]
v_cvt_pk_bf16_f32 v108,v108,v109
v_cvt_pk_bf16_f32 v109,v110,v111
v_cvt_pk_bf16_f32 v110,v12,v13
v_cvt_pk_bf16_f32 v111,v14,v15
v_mfma_f32_16x16x32_bf16 v[204:207],a[168:171],a[232:235],v[204:207]
v_cvt_pk_bf16_f32 v120,v120,v121
v_mul_f32 v122,s61,v122
v_mfma_f32_16x16x32_bf16 v[212:215],a[168:171],a[240:243],v[212:215]
v_mul_f32 v123,s61,v123
v_cvt_pk_bf16_f32 v121,v122,v123
v_mfma_f32_16x16x32_bf16 v[212:215],a[172:175],a[244:247],v[212:215]
ds_read_b128 a[136:139],v1 offset:512
v_accvgpr_read_b32 v122,a116
v_mfma_f32_16x16x32_bf16 v[220:223],a[172:175],a[252:255],v[220:223]
v_accvgpr_read_b32 v123,a117
)A"
R"A(v_accvgpr_read_b32 v16,a118
v_mfma_f32_16x16x32_bf16 v[220:223],a[168:171],a[248:251],v[220:223]
v_accvgpr_read_b32 v17,a119
v_mul_f32 v122,s61,v122
v_mfma_f32_16x16x32_bf16 v[224:227],a[176:179],a[224:227],v[224:227]
ds_read_b128 a[140:143],v1 offset:768
v_mul_f32 v123,s61,v123
v_mfma_f32_16x16x32_bf16 v[224:227],a[180:183],a[228:231],v[224:227]
v_mul_f32 v16,s61,v16
v_mul_f32 v17,s61,v17
v_mfma_f32_16x16x32_bf16 v[232:235],a[180:183],a[236:239],v[232:235]
v_cvt_pk_bf16_f32 v122,v122,v123
v_cvt_pk_bf16_f32 v123,v16,v17
v_mfma_f32_16x16x32_bf16 v[232:235],a[176:179],a[232:235],v[232:235]
ds_read_b128 a[156:159],v1 offset:4864
v_mfma_f32_16x16x32_bf16 v[240:243],a[176:179],a[240:243],v[240:243]
v_accvgpr_read_b32 v124,a120
v_accvgpr_read_b32 v125,a121
v_accvgpr_read_b32 v126,a122
v_accvgpr_read_b32 v127,a123
v_accvgpr_read_b32 v12,a124
v_accvgpr_read_b32 v13,a125
v_accvgpr_read_b32 v14,a126
v_accvgpr_read_b32 v15,a127
v_pk_mul_f32 v[124:125],s[60:61],v[124:125] op_sel:[1,0] op_sel_hi:[1,1]
v_pk_mul_f32 v[126:127],s[60:61],v[126:127] op_sel:[1,0] op_sel_hi:[1,1]
v_pk_mul_f32 v[12:13],s[60:61],v[12:13] op_sel:[1,0] op_sel_hi:[1,1]
v_pk_mul_f32 v[14:15],s[60:61],v[14:15] op_sel:[1,0] op_sel_hi:[1,1]
v_cvt_pk_bf16_f32 v124,v124,v125
v_cvt_pk_bf16_f32 v125,v126,v127
v_cvt_pk_bf16_f32 v126,v12,v13
v_cvt_pk_bf16_f32 v127,v14,v15
v_mfma_f32_16x16x32_bf16 v[240:243],a[180:183],a[244:247],v[240:243]
buffer_load_dwordx4 v5,s[24:27],s47 offen offset:3072 nt lds
v_mfma_f32_16x16x32_bf16 v[248:251],a[180:183],a[252:255],v[248:251]
s_add_u32 s47,s47,16384
s_and_b32 s47,s47,0x3ffff
ds_read_b128 a[152:155],v1 offset:4608
v_mfma_f32_16x16x32_bf16 v[248:251],a[176:179],a[248:251],v[248:251]
s_or_b32 s47,s47,s46
v_mul_f32 v128,s61,v128
v_mul_f32 v129,s61,v129
v_mul_f32 v130,s61,v130
v_mul_f32 v131,s61,v131
v_cvt_pk_bf16_f32 v128,v128,v129
v_cvt_pk_bf16_f32 v129,v130,v131
v_pk_mul_f32 v[132:133],s[60:61],v[132:133] op_sel:[1,0] op_sel_hi:[1,1]
v_pk_mul_f32 v[134:135],s[60:61],v[134:135] op_sel:[1,0] op_sel_hi:[1,1]
v_pk_mul_f32 v[136:137],s[60:61],v[136:137] op_sel:[1,0] op_sel_hi:[1,1]
v_pk_mul_f32 v[138:139],s[60:61],v[138:139] op_sel:[1,0] op_sel_hi:[1,1]
v_pk_mul_f32 v[140:141],s[60:61],v[140:141] op_sel:[1,0] op_sel_hi:[1,1]
v_pk_mul_f32 v[142:143],s[60:61],v[142:143] op_sel:[1,0] op_sel_hi:[1,1]
v_pk_mul_f32 v[144:145],s[60:61],v[144:145] op_sel:[1,0] op_sel_hi:[1,1]
v_pk_mul_f32 v[146:147],s[60:61],v[146:147] op_sel:[1,0] op_sel_hi:[1,1]
v_pk_mul_f32 v[148:149],s[60:61],v[148:149] op_sel:[1,0] op_sel_hi:[1,1]
v_pk_mul_f32 v[150:151],s[60:61],v[150:151] op_sel:[1,0] op_sel_hi:[1,1]
v_pk_mul_f32 v[152:153],s[60:61],v[152:153] op_sel:[1,0] op_sel_hi:[1,1]
v_pk_mul_f32 v[154:155],s[60:61],v[154:155] op_sel:[1,0] op_sel_hi:[1,1]
v_pk_mul_f32 v[156:157],s[60:61],v[156:157] op_sel:[1,0] op_sel_hi:[1,1]
v_pk_mul_f32 v[158:159],s[60:61],v[158:159] op_sel:[1,0] op_sel_hi:[1,1]
v_mfma_f32_16x16x32_bf16 v[228:231],a[184:187],a[224:227],v[228:231]
s_and_b32 s46,s54,0xfffc0000
ds_read_b128 a[148:151],v1 offset:4352
v_cvt_pk_bf16_f32 v130,v132,v133
v_mfma_f32_16x16x32_bf16 v[228:231],a[188:191],a[228:231],v[228:231]
s_add_u32 s54,s54,16384
v_cvt_pk_bf16_f32 v132,v136,v137
v_cvt_pk_bf16_f32 v133,v138,v139
v_mfma_f32_16x16x32_bf16 v[236:239],a[188:191],a[236:239],v[236:239]
ds_read_b128 a[144:147],v1 offset:4096
v_cvt_pk_bf16_f32 v131,v134,v135
v_mfma_f32_16x16x32_bf16 v[236:239],a[184:187],a[232:235],v[236:239]
v_cvt_pk_bf16_f32 v134,v140,v141
v_cvt_pk_bf16_f32 v135,v142,v143
v_mfma_f32_16x16x32_bf16 v[244:247],a[184:187],a[240:243],v[244:247]
ds_read_b128 a[160:163],v1 offset:8192
v_cvt_pk_bf16_f32 v144,v144,v145
v_mfma_f32_16x16x32_bf16 v[244:247],a[188:191],a[244:247],v[244:247]
v_cvt_pk_bf16_f32 v145,v146,v147
v_cvt_pk_bf16_f32 v146,v148,v149
v_mfma_f32_16x16x32_bf16 v[252:255],a[188:191],a[252:255],v[252:255]
ds_read_b128 a[164:167],v1 offset:8448
v_cvt_pk_bf16_f32 v148,v152,v153
v_mfma_f32_16x16x32_bf16 v[252:255],a[184:187],a[248:251],v[252:255]
s_waitcnt lgkmcnt(4)
v_cvt_pk_bf16_f32 v149,v154,v155
v_cvt_pk_bf16_f32 v147,v150,v151
v_mfma_f32_16x16x32_bf16 v[32:35],v[48:51],v[64:67],0
ds_read_b128 a[168:171],v1 offset:8704
v_cvt_pk_bf16_f32 v150,v156,v157
v_mfma_f32_16x16x32_bf16 v[32:35],v[52:55],v[68:71],v[32:35]
ds_read_b64_tr_b16 a[192:193],v2
v_cvt_pk_bf16_f32 v151,v158,v159
v_mfma_f32_16x16x32_bf16 v[32:35],v[56:59],v[72:75],v[32:35]
v_mul_f32 v160,s61,v160
v_mul_f32 v161,s61,v161
v_mul_f32 v162,s61,v162
v_mul_f32 v163,s61,v163
v_cvt_pk_bf16_f32 v160,v160,v161
v_cvt_pk_bf16_f32 v161,v162,v163
v_pk_mul_f32 v[164:165],s[60:61],v[164:165] op_sel:[1,0] op_sel_hi:[1,1]
v_pk_mul_f32 v[166:167],s[60:61],v[166:167] op_sel:[1,0] op_sel_hi:[1,1]
v_pk_mul_f32 v[168:169],s[60:61],v[168:169] op_sel:[1,0] op_sel_hi:[1,1]
v_pk_mul_f32 v[170:171],s[60:61],v[170:171] op_sel:[1,0] op_sel_hi:[1,1]
v_pk_mul_f32 v[172:173],s[60:61],v[172:173] op_sel:[1,0] op_sel_hi:[1,1]
v_pk_mul_f32 v[174:175],s[60:61],v[174:175] op_sel:[1,0] op_sel_hi:[1,1]
v_pk_mul_f32 v[176:177],s[60:61],v[176:177] op_sel:[1,0] op_sel_hi:[1,1]
v_pk_mul_f32 v[178:179],s[60:61],v[178:179] op_sel:[1,0] op_sel_hi:[1,1]
v_pk_mul_f32 v[180:181],s[60:61],v[180:181] op_sel:[1,0] op_sel_hi:[1,1]
v_pk_mul_f32 v[182:183],s[60:61],v[182:183] op_sel:[1,0] op_sel_hi:[1,1]
v_pk_mul_f32 v[184:185],s[60:61],v[184:185] op_sel:[1,0] op_sel_hi:[1,1]
v_pk_mul_f32 v[186:187],s[60:61],v[186:187] op_sel:[1,0] op_sel_hi:[1,1]
v_pk_mul_f32 v[188:189],s[60:61],v[188:189] op_sel:[1,0] op_sel_hi:[1,1]
v_pk_mul_f32 v[190:191],s[60:61],v[190:191] op_sel:[1,0] op_sel_hi:[1,1]
v_mfma_f32_16x16x32_bf16 v[32:35],v[60:63],v[76:79],v[32:35]
ds_read_b128 a[172:175],v1 offset:8960
v_cvt_pk_bf16_f32 v162,v164,v165
v_mfma_f32_16x16x32_bf16 v[36:39],v[48:51],v[80:83],0
v_cvt_pk_bf16_f32 v164,v168,v169
v_cvt_pk_bf16_f32 v165,v170,v171
v_mfma_f32_16x16x32_bf16 v[36:39],v[52:55],v[84:87],v[36:39]
ds_read_b128 a[188:191],v1 offset:13056
v_cvt_pk_bf16_f32 v163,v166,v167
v_mfma_f32_16x16x32_bf16 v[36:39],v[56:59],v[88:91],v[36:39]
v_cvt_pk_bf16_f32 v166,v172,v173
v_cvt_pk_bf16_f32 v167,v174,v175
v_mfma_f32_16x16x32_bf16 v[36:39],v[60:63],v[92:95],v[36:39]
ds_read_b128 a[184:187],v1 offset:12800
v_cvt_pk_bf16_f32 v176,v176,v177
v_mfma_f32_16x16x32_bf16 v[40:43],v[48:51],v[96:99],0
v_cvt_pk_bf16_f32 v177,v178,v179
v_cvt_pk_bf16_f32 v178,v180,v181
v_mfma_f32_16x16x32_bf16 v[40:43],v[52:55],v[100:103],v[40:43]
s_add_u32 m0,32768,s51
v_max3_f32 v32,v32,v33,v34
v_max3_f32 v32,v32,v35,s18
v_mfma_f32_16x16x32_bf16 v[40:43],v[56:59],v[104:107],v[40:43]
v_sub_f32 v35,0,v32
v_sub_f32 v34,0,v32
v_mfma_f32_16x16x32_bf16 v[40:43],v[60:63],v[108:111],v[40:43]
v_sub_f32 v33,0,v32
v_sub_f32 v32,0,v32
v_mfma_f32_16x16x32_bf16 v[44:47],v[48:51],v[112:115],0
buffer_load_dwordx4 v4,s[20:23],s47 offen nt lds
v_mfma_f32_16x16x32_bf16 v[44:47],v[52:55],v[116:119],v[44:47]
ds_read_b128 a[180:183],v1 offset:12544
v_max3_f32 v36,v36,v37,v38
v_mfma_f32_16x16x32_bf16 v[44:47],v[56:59],v[120:123],v[44:47]
v_max3_f32 v36,v36,v39,s19
v_sub_f32 v39,0,v36
v_mfma_f32_16x16x32_bf16 v[44:47],v[60:63],v[124:127],v[44:47]
ds_read_b128 a[176:179],v1 offset:12288
v_sub_f32 v38,0,v36
v_mfma_f32_16x16x32_bf16 v[136:139],a[128:131],v[64:67],v[32:35]
v_sub_f32 v37,0,v36
v_sub_f32 v36,0,v36
v_mfma_f32_16x16x32_bf16 v[136:139],a[132:135],v[68:71],v[136:139]
v_cvt_pk_bf16_f32 v180,v184,v185
v_cvt_pk_bf16_f32 v181,v186,v187
v_mfma_f32_16x16x32_bf16 v[136:139],a[136:139],v[72:75],v[136:139]
v_cvt_pk_bf16_f32 v179,v182,v183
v_cvt_pk_bf16_f32 v182,v188,v189
v_mfma_f32_16x16x32_bf16 v[136:139],a[140:143],v[76:79],v[136:139]
v_cvt_pk_bf16_f32 v183,v190,v191
v_max3_f32 v40,v40,v41,v42
v_mfma_f32_16x16x32_bf16 v[140:143],a[156:159],v[76:79],v[32:35]
v_max3_f32 v40,v40,v43,s100
v_sub_f32 v43,0,v40
v_mfma_f32_16x16x32_bf16 v[140:143],a[152:155],v[72:75],v[140:143]
s_waitcnt lgkmcnt(1)
buffer_load_dwordx4 v4,s[20:23],s47 offen offset:1024 nt lds
v_mfma_f32_16x16x32_bf16 v[140:143],a[148:151],v[68:71],v[140:143]
v_sub_f32 v42,0,v40
v_sub_f32 v41,0,v40
v_mfma_f32_16x16x32_bf16 v[140:143],a[144:147],v[64:67],v[140:143]
ds_read_b64_tr_b16 a[194:195],v2 offset:4096
v_sub_f32 v40,0,v40
v_mfma_f32_16x16x32_bf16 v[152:155],a[160:163],v[64:67],v[32:35]
v_mul_f32 v192,s61,v192
v_mul_f32 v193,s61,v193
v_mul_f32 v194,s61,v194
v_mul_f32 v195,s61,v195
v_cvt_pk_bf16_f32 v192,v192,v193
v_cvt_pk_bf16_f32 v193,v194,v195
v_pk_mul_f32 v[196:197],s[60:61],v[196:197] op_sel:[1,0] op_sel_hi:[1,1]
v_pk_mul_f32 v[198:199],s[60:61],v[198:199] op_sel:[1,0] op_sel_hi:[1,1]
v_pk_mul_f32 v[200:201],s[60:61],v[200:201] op_sel:[1,0] op_sel_hi:[1,1]
v_pk_mul_f32 v[202:203],s[60:61],v[202:203] op_sel:[1,0] op_sel_hi:[1,1]
v_pk_mul_f32 v[204:205],s[60:61],v[204:205] op_sel:[1,0] op_sel_hi:[1,1]
v_pk_mul_f32 v[206:207],s[60:61],v[206:207] op_sel:[1,0] op_sel_hi:[1,1]
v_pk_mul_f32 v[208:209],s[60:61],v[208:209] op_sel:[1,0] op_sel_hi:[1,1]
v_pk_mul_f32 v[210:211],s[60:61],v[210:211] op_sel:[1,0] op_sel_hi:[1,1]
v_pk_mul_f32 v[212:213],s[60:61],v[212:213] op_sel:[1,0] op_sel_hi:[1,1]
v_pk_mul_f32 v[214:215],s[60:61],v[214:215] op_sel:[1,0] op_sel_hi:[1,1]
v_pk_mul_f32 v[216:217],s[60:61],v[216:217] op_sel:[1,0] op_sel_hi:[1,1]
v_pk_mul_f32 v[218:219],s[60:61],v[218:219] op_sel:[1,0] op_sel_hi:[1,1]
v_pk_mul_f32 v[220:221],s[60:61],v[220:221] op_sel:[1,0] op_sel_hi:[1,1]
v_pk_mul_f32 v[222:223],s[60:61],v[222:223] op_sel:[1,0] op_sel_hi:[1,1]
v_mfma_f32_16x16x32_bf16 v[152:155],a[164:167],v[68:71],v[152:155]
v_cvt_pk_bf16_f32 v194,v196,v197
v_cvt_pk_bf16_f32 v196,v200,v201
v_mfma_f32_16x16x32_bf16 v[152:155],a[168:171],v[72:75],v[152:155]
v_cvt_pk_bf16_f32 v197,v202,v203
v_cvt_pk_bf16_f32 v195,v198,v199
v_mfma_f32_16x16x32_bf16 v[152:155],a[172:175],v[76:79],v[152:155]
v_cvt_pk_bf16_f32 v198,v204,v205
v_cvt_pk_bf16_f32 v199,v206,v207
v_mfma_f32_16x16x32_bf16 v[156:159],a[188:191],v[76:79],v[32:35]
v_cvt_pk_bf16_f32 v208,v208,v209
v_cvt_pk_bf16_f32 v209,v210,v211
v_mfma_f32_16x16x32_bf16 v[156:159],a[184:187],v[72:75],v[156:159]
v_cvt_pk_bf16_f32 v210,v212,v213
v_cvt_pk_bf16_f32 v212,v216,v217
v_mfma_f32_16x16x32_bf16 v[156:159],a[180:183],v[68:71],v[156:159]
s_waitcnt lgkmcnt(1)
buffer_load_dwordx4 v5,s[20:23],s47 offen offset:2048 nt lds
v_mfma_f32_16x16x32_bf16 v[156:159],a[176:179],v[64:67],v[156:159]
ds_read_b64_tr_b16 a[196:197],v2 offset:8192
v_cvt_pk_bf16_f32 v213,v218,v219
v_mfma_f32_16x16x32_bf16 v[168:171],a[128:131],v[80:83],v[36:39]
ds_read_b64_tr_b16 a[198:199],v2 offset:12288
ds_read_b64_tr_b16 a[204:205],v3 offset:8192
v_mfma_f32_16x16x32_bf16 v[168:171],a[132:135],v[84:87],v[168:171]
ds_read_b64_tr_b16 a[206:207],v3 offset:12288
v_cvt_pk_bf16_f32 v211,v214,v215
v_mfma_f32_16x16x32_bf16 v[168:171],a[136:139],v[88:91],v[168:171]
ds_read_b64_tr_b16 a[200:201],v3
v_cvt_pk_bf16_f32 v214,v220,v221
v_mfma_f32_16x16x32_bf16 v[168:171],a[140:143],v[92:95],v[168:171]
ds_read_b64_tr_b16 a[202:203],v3 offset:4096
v_cvt_pk_bf16_f32 v215,v222,v223
v_mfma_f32_16x16x32_bf16 v[172:175],a[156:159],v[92:95],v[36:39]
ds_read_b64_tr_b16 a[208:209],v2 offset:256
ds_read_b64_tr_b16 a[210:211],v2 offset:4352
v_mfma_f32_16x16x32_bf16 v[172:175],a[152:155],v[88:91],v[172:175]
v_exp_f32 v136,v136
v_mfma_f32_16x16x32_bf16 v[172:175],a[148:151],v[84:87],v[172:175]
v_exp_f32 v137,v137
v_mfma_f32_16x16x32_bf16 v[172:175],a[144:147],v[80:83],v[172:175]
buffer_load_dwordx4 v5,s[20:23],s47 offen offset:3072 nt lds
v_mfma_f32_16x16x32_bf16 v[184:187],a[160:163],v[80:83],v[36:39]
v_exp_f32 v138,v138
v_mfma_f32_16x16x32_bf16 v[184:187],a[164:167],v[84:87],v[184:187]
v_exp_f32 v139,v139
v_mfma_f32_16x16x32_bf16 v[184:187],a[168:171],v[88:91],v[184:187]
ds_read_b64_tr_b16 a[212:213],v2 offset:8448
ds_read_b64_tr_b16 a[214:215],v2 offset:12544
v_mfma_f32_16x16x32_bf16 v[184:187],a[172:175],v[92:95],v[184:187]
v_cvt_pk_bf16_f32 v136,v136,v137
v_cvt_pk_bf16_f32 v137,v138,v139
v_mfma_f32_16x16x32_bf16 v[188:191],a[188:191],v[92:95],v[36:39]
v_exp_f32 v140,v140
v_mfma_f32_16x16x32_bf16 v[188:191],a[184:187],v[88:91],v[188:191]
)A"
R"A(s_add_u32 m0,32768,s52
v_exp_f32 v141,v141
v_mfma_f32_16x16x32_bf16 v[188:191],a[180:183],v[84:87],v[188:191]
v_exp_f32 v142,v142
v_mfma_f32_16x16x32_bf16 v[188:191],a[176:179],v[80:83],v[188:191]
v_exp_f32 v143,v143
v_mfma_f32_16x16x32_bf16 v[200:203],a[128:131],v[96:99],v[40:43]
buffer_load_dwordx4 v4,s[24:27],s47 offen nt lds
v_mfma_f32_16x16x32_bf16 v[200:203],a[132:135],v[100:103],v[200:203]
ds_read_b64_tr_b16 a[220:221],v3 offset:8448
ds_read_b64_tr_b16 a[222:223],v3 offset:12544
v_mfma_f32_16x16x32_bf16 v[200:203],a[136:139],v[104:107],v[200:203]
v_cvt_pk_bf16_f32 v138,v140,v141
v_cvt_pk_bf16_f32 v139,v142,v143
v_mfma_f32_16x16x32_bf16 v[200:203],a[140:143],v[108:111],v[200:203]
v_exp_f32 v152,v152
v_mfma_f32_16x16x32_bf16 v[204:207],a[156:159],v[108:111],v[40:43]
v_exp_f32 v153,v153
v_mfma_f32_16x16x32_bf16 v[204:207],a[152:155],v[104:107],v[204:207]
v_exp_f32 v154,v154
v_mfma_f32_16x16x32_bf16 v[204:207],a[148:151],v[100:103],v[204:207]
v_exp_f32 v155,v155
v_mfma_f32_16x16x32_bf16 v[204:207],a[144:147],v[96:99],v[204:207]
v_exp_f32 v156,v156
v_mfma_f32_16x16x32_bf16 v[216:219],a[160:163],v[96:99],v[40:43]
v_exp_f32 v157,v157
v_mfma_f32_16x16x32_bf16 v[216:219],a[164:167],v[100:103],v[216:219]
buffer_load_dwordx4 v5,s[24:27],s47 offen offset:1024 nt lds
v_mfma_f32_16x16x32_bf16 v[216:219],a[168:171],v[104:107],v[216:219]
v_exp_f32 v158,v158
v_mfma_f32_16x16x32_bf16 v[216:219],a[172:175],v[108:111],v[216:219]
v_exp_f32 v159,v159
v_mfma_f32_16x16x32_bf16 v[220:223],a[188:191],v[108:111],v[40:43]
ds_read_b64_tr_b16 a[216:217],v3 offset:256
ds_read_b64_tr_b16 a[218:219],v3 offset:4352
v_mfma_f32_16x16x32_bf16 v[220:223],a[184:187],v[104:107],v[220:223]
v_cvt_pk_bf16_f32 v140,v152,v153
v_cvt_pk_bf16_f32 v141,v154,v155
v_mfma_f32_16x16x32_bf16 v[220:223],a[180:183],v[100:103],v[220:223]
v_cvt_pk_bf16_f32 v142,v156,v157
v_cvt_pk_bf16_f32 v143,v158,v159
v_mfma_f32_16x16x32_bf16 v[220:223],a[176:179],v[96:99],v[220:223]
s_waitcnt lgkmcnt(2)
ds_read_b64_tr_b16 a[224:225],v2 offset:512
ds_read_b64_tr_b16 a[226:227],v2 offset:4608
v_mfma_f32_16x16x32_bf16 a[0:3],a[192:195],v[136:139],0
ds_read_b64_tr_b16 a[228:229],v2 offset:8704
ds_read_b64_tr_b16 a[230:231],v2 offset:12800
v_mfma_f32_16x16x32_bf16 a[0:3],a[196:199],v[140:143],a[0:3]
ds_read_b64_tr_b16 a[236:237],v3 offset:8704
ds_read_b64_tr_b16 a[238:239],v3 offset:12800
v_mfma_f32_16x16x32_bf16 a[4:7],a[204:207],v[140:143],0
ds_read_b64_tr_b16 a[232:233],v3 offset:512
ds_read_b64_tr_b16 a[234:235],v3 offset:4608
v_mfma_f32_16x16x32_bf16 a[4:7],a[200:203],v[136:139],a[4:7]
ds_read_b64_tr_b16 a[240:241],v2 offset:768
ds_read_b64_tr_b16 a[242:243],v2 offset:4864
v_mfma_f32_16x16x32_bf16 a[8:11],a[208:211],v[136:139],0
s_memrealtime s[76:77]
ds_read_b64_tr_b16 a[244:245],v2 offset:8960
ds_read_b64_tr_b16 a[246:247],v2 offset:13056
v_mfma_f32_16x16x32_bf16 a[8:11],a[212:215],v[140:143],a[8:11]
buffer_load_dwordx4 v4,s[24:27],s47 offen offset:2048 nt lds
ds_read_b64_tr_b16 a[252:253],v3 offset:8960
ds_read_b64_tr_b16 a[254:255],v3 offset:13056
v_mfma_f32_16x16x32_bf16 a[12:15],a[220:223],v[140:143],0
s_waitcnt lgkmcnt(14)
ds_read_b64_tr_b16 a[248:249],v3 offset:768
ds_read_b64_tr_b16 a[250:251],v3 offset:4864
v_mfma_f32_16x16x32_bf16 a[12:15],a[216:219],v[136:139],a[12:15]
s_waitcnt lgkmcnt(14)
v_mfma_f32_16x16x32_bf16 a[16:19],a[224:227],v[136:139],0
s_waitcnt lgkmcnt(12)
v_mfma_f32_16x16x32_bf16 a[16:19],a[228:231],v[140:143],a[16:19]
s_waitcnt lgkmcnt(10)
v_mfma_f32_16x16x32_bf16 a[20:23],a[236:239],v[140:143],0
s_waitcnt lgkmcnt(0)
v_mfma_f32_16x16x32_bf16 a[20:23],a[232:235],v[136:139],a[20:23]
s_add_u32 s78,s76,s60
v_max3_f32 v44,v44,v45,v46
v_max3_f32 v44,v44,v47,s101
v_mfma_f32_16x16x32_bf16 a[24:27],a[240:243],v[136:139],0
s_lshr_b32 s60,s94,2
v_mov_b32 v48,0
v_mov_b32 v49,0
v_mfma_f32_16x16x32_bf16 a[24:27],a[244:247],v[140:143],a[24:27]
s_max_i32 s60,s60,128
v_mov_b32 v50,0
v_mov_b32 v51,0
v_mfma_f32_16x16x32_bf16 a[28:31],a[252:255],v[140:143],0
buffer_load_dwordx4 v5,s[24:27],s47 offen offset:3072 nt lds
v_sub_f32 v47,0,v44
v_sub_f32 v46,0,v44
v_sub_f32 v45,0,v44
v_sub_f32 v44,0,v44
v_mfma_f32_16x16x32_bf16 a[28:31],a[248:251],v[136:139],a[28:31]
v_mul_f32 v224,s61,v224
v_mul_f32 v225,s61,v225
v_mul_f32 v226,s61,v226
v_mul_f32 v227,s61,v227
v_cvt_pk_bf16_f32 v224,v224,v225
v_cvt_pk_bf16_f32 v225,v226,v227
v_pk_mul_f32 v[228:229],s[60:61],v[228:229] op_sel:[1,0] op_sel_hi:[1,1]
v_pk_mul_f32 v[230:231],s[60:61],v[230:231] op_sel:[1,0] op_sel_hi:[1,1]
v_pk_mul_f32 v[232:233],s[60:61],v[232:233] op_sel:[1,0] op_sel_hi:[1,1]
v_pk_mul_f32 v[234:235],s[60:61],v[234:235] op_sel:[1,0] op_sel_hi:[1,1]
v_pk_mul_f32 v[236:237],s[60:61],v[236:237] op_sel:[1,0] op_sel_hi:[1,1]
v_pk_mul_f32 v[238:239],s[60:61],v[238:239] op_sel:[1,0] op_sel_hi:[1,1]
v_pk_mul_f32 v[240:241],s[60:61],v[240:241] op_sel:[1,0] op_sel_hi:[1,1]
v_pk_mul_f32 v[242:243],s[60:61],v[242:243] op_sel:[1,0] op_sel_hi:[1,1]
v_pk_mul_f32 v[244:245],s[60:61],v[244:245] op_sel:[1,0] op_sel_hi:[1,1]
v_pk_mul_f32 v[246:247],s[60:61],v[246:247] op_sel:[1,0] op_sel_hi:[1,1]
v_pk_mul_f32 v[248:249],s[60:61],v[248:249] op_sel:[1,0] op_sel_hi:[1,1]
v_pk_mul_f32 v[250:251],s[60:61],v[250:251] op_sel:[1,0] op_sel_hi:[1,1]
v_pk_mul_f32 v[252:253],s[60:61],v[252:253] op_sel:[1,0] op_sel_hi:[1,1]
v_pk_mul_f32 v[254:255],s[60:61],v[254:255] op_sel:[1,0] op_sel_hi:[1,1]
v_smfmac_f32_16x16x64_bf16 v[48:51],v[28:31],v[136:143],v27
v_cvt_pk_bf16_f32 v226,v228,v229
v_cvt_pk_bf16_f32 v228,v232,v233
v_cvt_pk_bf16_f32 v229,v234,v235
v_mfma_f32_16x16x32_bf16 v[232:235],a[128:131],v[112:115],v[44:47]
s_waitcnt vmcnt(8)
s_add_u32 s47,s47,16384
v_cvt_pk_bf16_f32 v227,v230,v231
v_cvt_pk_bf16_f32 v230,v236,v237
v_mfma_f32_16x16x32_bf16 v[232:235],a[132:135],v[116:119],v[232:235]
s_barrier
s_add_u32 m0,65536,s51
v_cvt_pk_bf16_f32 v231,v238,v239
v_mfma_f32_16x16x32_bf16 v[232:235],a[136:139],v[120:123],v[232:235]
s_and_b32 s47,s47,0x3ffff
s_or_b32 s47,s47,s46
v_cvt_pk_bf16_f32 v240,v240,v241
v_mfma_f32_16x16x32_bf16 v[232:235],a[140:143],v[124:127],v[232:235]
s_and_b32 s46,s54,0xfffc0000
v_cvt_pk_bf16_f32 v241,v242,v243
v_cvt_pk_bf16_f32 v242,v244,v245
v_mfma_f32_16x16x32_bf16 v[236:239],a[156:159],v[124:127],v[44:47]
v_cvt_pk_bf16_f32 v244,v248,v249
v_cvt_pk_bf16_f32 v245,v250,v251
v_mfma_f32_16x16x32_bf16 v[236:239],a[152:155],v[120:123],v[236:239]
buffer_load_dwordx4 v4,s[20:23],s47 offen nt lds
v_mfma_f32_16x16x32_bf16 v[236:239],a[148:151],v[116:119],v[236:239]
v_cvt_pk_bf16_f32 v243,v246,v247
v_cvt_pk_bf16_f32 v246,v252,v253
v_mfma_f32_16x16x32_bf16 v[236:239],a[144:147],v[112:115],v[236:239]
v_cvt_pk_bf16_f32 v247,v254,v255
v_add_u32 v1,s48,v1
v_mfma_f32_16x16x32_bf16 v[248:251],a[160:163],v[112:115],v[44:47]
v_exp_f32 v168,v168
v_mfma_f32_16x16x32_bf16 v[248:251],a[164:167],v[116:119],v[248:251]
v_exp_f32 v169,v169
v_mfma_f32_16x16x32_bf16 v[248:251],a[168:171],v[120:123],v[248:251]
v_exp_f32 v170,v170
v_mfma_f32_16x16x32_bf16 v[248:251],a[172:175],v[124:127],v[248:251]
v_exp_f32 v171,v171
v_mfma_f32_16x16x32_bf16 v[252:255],a[188:191],v[124:127],v[44:47]
v_cvt_pk_bf16_f32 v168,v168,v169
v_exp_f32 v172,v172
v_exp_f32 v173,v173
v_exp_f32 v174,v174
v_exp_f32 v175,v175
v_mfma_f32_16x16x32_bf16 v[252:255],a[184:187],v[120:123],v[252:255]
v_cvt_pk_bf16_f32 v169,v170,v171
v_cvt_pk_bf16_f32 v170,v172,v173
v_cvt_pk_bf16_f32 v171,v174,v175
v_exp_f32 v184,v184
v_exp_f32 v185,v185
v_exp_f32 v186,v186
v_exp_f32 v187,v187
v_exp_f32 v188,v188
v_exp_f32 v189,v189
v_exp_f32 v190,v190
v_exp_f32 v191,v191
v_mfma_f32_16x16x32_bf16 v[252:255],a[180:183],v[116:119],v[252:255]
v_cvt_pk_bf16_f32 v172,v184,v185
v_cvt_pk_bf16_f32 v173,v186,v187
v_cvt_pk_bf16_f32 v174,v188,v189
v_cvt_pk_bf16_f32 v175,v190,v191
v_mfma_f32_16x16x32_bf16 v[252:255],a[176:179],v[112:115],v[252:255]
ds_read_b128 a[128:131],v1
v_mov_b32 v52,0
v_mfma_f32_16x16x32_bf16 a[32:35],a[192:195],v[168:171],0
v_mov_b32 v53,0
v_mov_b32 v54,0
v_mfma_f32_16x16x32_bf16 a[32:35],a[196:199],v[172:175],a[32:35]
ds_read_b128 a[132:135],v1 offset:256
v_mov_b32 v55,0
v_mfma_f32_16x16x32_bf16 a[36:39],a[204:207],v[172:175],0
buffer_load_dwordx4 v4,s[20:23],s47 offen offset:1024 nt lds
v_mfma_f32_16x16x32_bf16 a[36:39],a[200:203],v[168:171],a[36:39]
ds_read_b128 a[136:139],v1 offset:512
v_mov_b32 v56,0
v_mfma_f32_16x16x32_bf16 a[40:43],a[208:211],v[168:171],0
v_exp_f32 v200,v200
v_mfma_f32_16x16x32_bf16 a[40:43],a[212:215],v[172:175],a[40:43]
ds_read_b128 a[140:143],v1 offset:768
v_mov_b32 v57,0
v_mfma_f32_16x16x32_bf16 a[44:47],a[220:223],v[172:175],0
v_exp_f32 v201,v201
v_mfma_f32_16x16x32_bf16 a[44:47],a[216:219],v[168:171],a[44:47]
ds_read_b128 a[156:159],v1 offset:4864
v_cvt_pk_bf16_f32 v200,v200,v201
v_mfma_f32_16x16x32_bf16 a[48:51],a[224:227],v[168:171],0
v_exp_f32 v202,v202
v_mfma_f32_16x16x32_bf16 a[48:51],a[228:231],v[172:175],a[48:51]
ds_read_b128 a[152:155],v1 offset:4608
v_mov_b32 v58,0
v_mfma_f32_16x16x32_bf16 a[52:55],a[236:239],v[172:175],0
v_exp_f32 v203,v203
v_mfma_f32_16x16x32_bf16 a[52:55],a[232:235],v[168:171],a[52:55]
ds_read_b128 a[148:151],v1 offset:4352
v_cvt_pk_bf16_f32 v201,v202,v203
v_mfma_f32_16x16x32_bf16 a[56:59],a[240:243],v[168:171],0
v_exp_f32 v204,v204
v_mfma_f32_16x16x32_bf16 a[56:59],a[244:247],v[172:175],a[56:59]
ds_read_b128 a[144:147],v1 offset:4096
v_mov_b32 v59,0
v_mfma_f32_16x16x32_bf16 a[60:63],a[252:255],v[172:175],0
v_exp_f32 v205,v205
v_mfma_f32_16x16x32_bf16 a[60:63],a[248:251],v[168:171],a[60:63]
ds_read_b128 a[160:163],v1 offset:8192
v_cvt_pk_bf16_f32 v202,v204,v205
v_smfmac_f32_16x16x64_bf16 v[52:55],v[28:31],v[168:175],v27
s_waitcnt lgkmcnt(4)
v_mov_b32 v60,0
v_mfma_f32_16x16x32_bf16 v[136:139],a[128:131],v[64:67],v[32:35]
ds_read_b128 a[164:167],v1 offset:8448
v_mov_b32 v61,0
v_mfma_f32_16x16x32_bf16 v[136:139],a[132:135],v[68:71],v[136:139]
ds_read_b128 a[168:171],v1 offset:8704
v_mfma_f32_16x16x32_bf16 v[136:139],a[136:139],v[72:75],v[136:139]
buffer_load_dwordx4 v5,s[20:23],s47 offen offset:2048 nt lds
ds_read_b128 a[172:175],v1 offset:8960
v_mfma_f32_16x16x32_bf16 v[136:139],a[140:143],v[76:79],v[136:139]
ds_read_b128 a[188:191],v1 offset:13056
v_mfma_f32_16x16x32_bf16 v[140:143],a[156:159],v[76:79],v[32:35]
s_waitcnt lgkmcnt(5)
ds_read_b128 a[184:187],v1 offset:12800
v_mfma_f32_16x16x32_bf16 v[140:143],a[152:155],v[72:75],v[140:143]
ds_read_b128 a[180:183],v1 offset:12544
v_mfma_f32_16x16x32_bf16 v[140:143],a[148:151],v[68:71],v[140:143]
ds_read_b128 a[176:179],v1 offset:12288
v_mfma_f32_16x16x32_bf16 v[140:143],a[144:147],v[64:67],v[140:143]
s_waitcnt lgkmcnt(7)
v_mfma_f32_16x16x32_bf16 v[152:155],a[160:163],v[64:67],v[32:35]
s_waitcnt lgkmcnt(6)
v_mfma_f32_16x16x32_bf16 v[152:155],a[164:167],v[68:71],v[152:155]
s_waitcnt lgkmcnt(5)
v_mfma_f32_16x16x32_bf16 v[152:155],a[168:171],v[72:75],v[152:155]
s_waitcnt lgkmcnt(4)
v_exp_f32 v206,v206
v_mfma_f32_16x16x32_bf16 v[152:155],a[172:175],v[76:79],v[152:155]
s_waitcnt lgkmcnt(3)
v_exp_f32 v207,v207
v_mfma_f32_16x16x32_bf16 v[156:159],a[188:191],v[76:79],v[32:35]
s_waitcnt lgkmcnt(2)
v_cvt_pk_bf16_f32 v203,v206,v207
v_mov_b32 v62,0
v_mfma_f32_16x16x32_bf16 v[156:159],a[184:187],v[72:75],v[156:159]
s_waitcnt lgkmcnt(1)
v_exp_f32 v216,v216
v_exp_f32 v217,v217
v_exp_f32 v218,v218
v_exp_f32 v219,v219
v_exp_f32 v220,v220
v_exp_f32 v221,v221
v_exp_f32 v222,v222
v_exp_f32 v223,v223
v_mfma_f32_16x16x32_bf16 v[156:159],a[180:183],v[68:71],v[156:159]
s_waitcnt lgkmcnt(0)
v_cvt_pk_bf16_f32 v204,v216,v217
v_cvt_pk_bf16_f32 v205,v218,v219
v_cvt_pk_bf16_f32 v206,v220,v221
v_cvt_pk_bf16_f32 v207,v222,v223
v_mfma_f32_16x16x32_bf16 v[156:159],a[176:179],v[64:67],v[156:159]
buffer_load_dwordx4 v5,s[20:23],s47 offen offset:3072 nt lds
v_mfma_f32_16x16x32_bf16 a[64:67],a[192:195],v[200:203],0
v_exp_f32 v232,v232
v_mfma_f32_16x16x32_bf16 a[64:67],a[196:199],v[204:207],a[64:67]
v_exp_f32 v233,v233
v_mfma_f32_16x16x32_bf16 a[68:71],a[204:207],v[204:207],0
v_exp_f32 v234,v234
v_mfma_f32_16x16x32_bf16 a[68:71],a[200:203],v[200:203],a[68:71]
v_exp_f32 v235,v235
v_mfma_f32_16x16x32_bf16 a[72:75],a[208:211],v[200:203],0
)A"
R"A(v_cvt_pk_bf16_f32 v232,v232,v233
v_cvt_pk_bf16_f32 v233,v234,v235
v_mfma_f32_16x16x32_bf16 a[72:75],a[212:215],v[204:207],a[72:75]
v_exp_f32 v236,v236
v_mfma_f32_16x16x32_bf16 a[76:79],a[220:223],v[204:207],0
v_exp_f32 v237,v237
v_mfma_f32_16x16x32_bf16 a[76:79],a[216:219],v[200:203],a[76:79]
v_exp_f32 v238,v238
v_mfma_f32_16x16x32_bf16 a[80:83],a[224:227],v[200:203],0
s_add_u32 m0,65536,s52
v_exp_f32 v239,v239
v_mfma_f32_16x16x32_bf16 a[80:83],a[228:231],v[204:207],a[80:83]
v_cvt_pk_bf16_f32 v234,v236,v237
v_cvt_pk_bf16_f32 v235,v238,v239
v_mfma_f32_16x16x32_bf16 a[84:87],a[236:239],v[204:207],0
v_exp_f32 v248,v248
v_mfma_f32_16x16x32_bf16 a[84:87],a[232:235],v[200:203],a[84:87]
buffer_load_dwordx4 v4,s[24:27],s47 offen nt lds
v_mfma_f32_16x16x32_bf16 a[88:91],a[240:243],v[200:203],0
v_exp_f32 v249,v249
v_mfma_f32_16x16x32_bf16 a[88:91],a[244:247],v[204:207],a[88:91]
v_exp_f32 v250,v250
v_mfma_f32_16x16x32_bf16 a[92:95],a[252:255],v[204:207],0
v_exp_f32 v251,v251
v_mfma_f32_16x16x32_bf16 a[92:95],a[248:251],v[200:203],a[92:95]
v_exp_f32 v252,v252
v_smfmac_f32_16x16x64_bf16 v[56:59],v[28:31],v[200:207],v27
v_cvt_pk_bf16_f32 v236,v248,v249
v_mfma_f32_16x16x32_bf16 v[168:171],a[128:131],v[80:83],v[36:39]
v_exp_f32 v253,v253
v_mfma_f32_16x16x32_bf16 v[168:171],a[132:135],v[84:87],v[168:171]
v_exp_f32 v254,v254
v_mfma_f32_16x16x32_bf16 v[168:171],a[136:139],v[88:91],v[168:171]
v_exp_f32 v255,v255
v_mfma_f32_16x16x32_bf16 v[168:171],a[140:143],v[92:95],v[168:171]
v_cvt_pk_bf16_f32 v237,v250,v251
v_cvt_pk_bf16_f32 v238,v252,v253
v_mfma_f32_16x16x32_bf16 v[172:175],a[156:159],v[92:95],v[36:39]
v_cvt_pk_bf16_f32 v239,v254,v255
v_mov_b32 v63,0
v_mfma_f32_16x16x32_bf16 v[172:175],a[152:155],v[88:91],v[172:175]
v_add_u32 v2,s48,v2
v_add_u32 v3,s48,v3
v_mfma_f32_16x16x32_bf16 v[172:175],a[148:151],v[84:87],v[172:175]
s_mov_b32 s48,32768
v_exp_f32 v136,v136
v_mfma_f32_16x16x32_bf16 v[172:175],a[144:147],v[80:83],v[172:175]
v_exp_f32 v137,v137
v_mfma_f32_16x16x32_bf16 v[184:187],a[160:163],v[80:83],v[36:39]
buffer_load_dwordx4 v5,s[24:27],s47 offen offset:1024 nt lds
v_mfma_f32_16x16x32_bf16 v[184:187],a[164:167],v[84:87],v[184:187]
v_exp_f32 v138,v138
v_mfma_f32_16x16x32_bf16 v[184:187],a[168:171],v[88:91],v[184:187]
v_exp_f32 v139,v139
v_mfma_f32_16x16x32_bf16 v[184:187],a[172:175],v[92:95],v[184:187]
v_cvt_pk_bf16_f32 v136,v136,v137
v_cvt_pk_bf16_f32 v137,v138,v139
v_mfma_f32_16x16x32_bf16 v[188:191],a[188:191],v[92:95],v[36:39]
v_exp_f32 v140,v140
v_mfma_f32_16x16x32_bf16 v[188:191],a[184:187],v[88:91],v[188:191]
v_exp_f32 v141,v141
v_mfma_f32_16x16x32_bf16 v[188:191],a[180:183],v[84:87],v[188:191]
v_exp_f32 v142,v142
v_mfma_f32_16x16x32_bf16 v[188:191],a[176:179],v[80:83],v[188:191]
v_exp_f32 v143,v143
v_mfma_f32_16x16x32_bf16 a[96:99],a[192:195],v[232:235],0
ds_read_b64_tr_b16 a[192:193],v2
ds_read_b64_tr_b16 a[194:195],v2 offset:4096
v_mfma_f32_16x16x32_bf16 a[96:99],a[196:199],v[236:239],a[96:99]
ds_read_b64_tr_b16 a[196:197],v2 offset:8192
v_cvt_pk_bf16_f32 v138,v140,v141
v_mfma_f32_16x16x32_bf16 a[100:103],a[204:207],v[236:239],0
ds_read_b64_tr_b16 a[198:199],v2 offset:12288
v_cvt_pk_bf16_f32 v139,v142,v143
v_mfma_f32_16x16x32_bf16 a[100:103],a[200:203],v[232:235],a[100:103]
ds_read_b64_tr_b16 a[204:205],v3 offset:8192
v_add_u32 v1,s48,v1
v_mfma_f32_16x16x32_bf16 a[104:107],a[208:211],v[232:235],0
ds_read_b64_tr_b16 a[206:207],v3 offset:12288
ds_read_b64_tr_b16 a[200:201],v3
v_mfma_f32_16x16x32_bf16 a[104:107],a[212:215],v[236:239],a[104:107]
v_exp_f32 v152,v152
v_mfma_f32_16x16x32_bf16 a[108:111],a[220:223],v[236:239],0
buffer_load_dwordx4 v4,s[24:27],s47 offen offset:2048 nt lds
v_mfma_f32_16x16x32_bf16 a[108:111],a[216:219],v[232:235],a[108:111]
ds_read_b64_tr_b16 a[202:203],v3 offset:4096
ds_read_b64_tr_b16 a[208:209],v2 offset:256
v_mfma_f32_16x16x32_bf16 a[112:115],a[224:227],v[232:235],0
v_exp_f32 v153,v153
v_mfma_f32_16x16x32_bf16 a[112:115],a[228:231],v[236:239],a[112:115]
ds_read_b64_tr_b16 a[210:211],v2 offset:4352
ds_read_b64_tr_b16 a[212:213],v2 offset:8448
v_mfma_f32_16x16x32_bf16 a[116:119],a[236:239],v[236:239],0
ds_read_b64_tr_b16 a[214:215],v2 offset:12544
v_cvt_pk_bf16_f32 v140,v152,v153
v_mfma_f32_16x16x32_bf16 a[116:119],a[232:235],v[232:235],a[116:119]
v_exp_f32 v154,v154
v_mfma_f32_16x16x32_bf16 a[120:123],a[240:243],v[232:235],0
v_exp_f32 v155,v155
v_mfma_f32_16x16x32_bf16 a[120:123],a[244:247],v[236:239],a[120:123]
v_exp_f32 v156,v156
v_mfma_f32_16x16x32_bf16 a[124:127],a[252:255],v[236:239],0
v_exp_f32 v157,v157
v_mfma_f32_16x16x32_bf16 a[124:127],a[248:251],v[232:235],a[124:127]
v_exp_f32 v158,v158
v_smfmac_f32_16x16x64_bf16 v[60:63],v[28:31],v[232:239],v27
ds_read_b64_tr_b16 a[220:221],v3 offset:8448
v_mfma_f32_16x16x32_bf16 v[200:203],a[128:131],v[96:99],v[40:43]
v_exp_f32 v159,v159
v_mfma_f32_16x16x32_bf16 v[200:203],a[132:135],v[100:103],v[200:203]
ds_read_b64_tr_b16 a[222:223],v3 offset:12544
v_cvt_pk_bf16_f32 v141,v154,v155
v_mfma_f32_16x16x32_bf16 v[200:203],a[136:139],v[104:107],v[200:203]
v_cvt_pk_bf16_f32 v142,v156,v157
v_cvt_pk_bf16_f32 v143,v158,v159
v_mfma_f32_16x16x32_bf16 v[200:203],a[140:143],v[108:111],v[200:203]
buffer_load_dwordx4 v5,s[24:27],s47 offen offset:3072 nt lds
v_mfma_f32_16x16x32_bf16 v[204:207],a[156:159],v[108:111],v[40:43]
s_add_u32 s47,s47,16384
ds_read_b64_tr_b16 a[216:217],v3 offset:256
ds_read_b64_tr_b16 a[218:219],v3 offset:4352
v_mfma_f32_16x16x32_bf16 v[204:207],a[152:155],v[104:107],v[204:207]
s_and_b32 s47,s47,0x3ffff
s_or_b32 s47,s47,s46
ds_read_b64_tr_b16 a[224:225],v2 offset:512
v_mfma_f32_16x16x32_bf16 v[204:207],a[148:151],v[100:103],v[204:207]
ds_read_b64_tr_b16 a[226:227],v2 offset:4608
v_mfma_f32_16x16x32_bf16 v[204:207],a[144:147],v[96:99],v[204:207]
ds_read_b64_tr_b16 a[228:229],v2 offset:8704
v_mfma_f32_16x16x32_bf16 v[216:219],a[160:163],v[96:99],v[40:43]
ds_read_b64_tr_b16 a[230:231],v2 offset:12800
ds_read_b64_tr_b16 a[236:237],v3 offset:8704
v_mfma_f32_16x16x32_bf16 v[216:219],a[164:167],v[100:103],v[216:219]
ds_read_b64_tr_b16 a[238:239],v3 offset:12800
v_mfma_f32_16x16x32_bf16 v[216:219],a[168:171],v[104:107],v[216:219]
ds_read_b64_tr_b16 a[232:233],v3 offset:512
v_mfma_f32_16x16x32_bf16 v[216:219],a[172:175],v[108:111],v[216:219]
ds_read_b64_tr_b16 a[234:235],v3 offset:4608
v_mfma_f32_16x16x32_bf16 v[220:223],a[188:191],v[108:111],v[40:43]
ds_read_b64_tr_b16 a[240:241],v2 offset:768
ds_read_b64_tr_b16 a[242:243],v2 offset:4864
v_mfma_f32_16x16x32_bf16 v[220:223],a[184:187],v[104:107],v[220:223]
ds_read_b64_tr_b16 a[244:245],v2 offset:8960
v_mfma_f32_16x16x32_bf16 v[220:223],a[180:183],v[100:103],v[220:223]
ds_read_b64_tr_b16 a[246:247],v2 offset:13056
v_mfma_f32_16x16x32_bf16 v[220:223],a[176:179],v[96:99],v[220:223]
s_waitcnt lgkmcnt(9)
ds_read_b64_tr_b16 a[252:253],v3 offset:8960
v_mfma_f32_16x16x32_bf16 a[0:3],a[192:195],v[136:139],a[0:3]
ds_read_b64_tr_b16 a[254:255],v3 offset:13056
ds_read_b64_tr_b16 a[248:249],v3 offset:768
v_mfma_f32_16x16x32_bf16 a[0:3],a[196:199],v[140:143],a[0:3]
ds_read_b64_tr_b16 a[250:251],v3 offset:4864
v_mfma_f32_16x16x32_bf16 a[4:7],a[204:207],v[140:143],a[4:7]
v_exp_f32 v168,v168
v_mfma_f32_16x16x32_bf16 a[4:7],a[200:203],v[136:139],a[4:7]
s_memrealtime s[76:77]
v_exp_f32 v169,v169
v_mfma_f32_16x16x32_bf16 a[8:11],a[208:211],v[136:139],a[8:11]
v_exp_f32 v170,v170
v_mfma_f32_16x16x32_bf16 a[8:11],a[212:215],v[140:143],a[8:11]
v_exp_f32 v171,v171
v_mfma_f32_16x16x32_bf16 a[12:15],a[220:223],v[140:143],a[12:15]
v_cvt_pk_bf16_f32 v168,v168,v169
v_cvt_pk_bf16_f32 v169,v170,v171
v_mfma_f32_16x16x32_bf16 a[12:15],a[216:219],v[136:139],a[12:15]
v_exp_f32 v172,v172
v_mfma_f32_16x16x32_bf16 a[16:19],a[224:227],v[136:139],a[16:19]
s_waitcnt lgkmcnt(1)
v_exp_f32 v173,v173
v_mfma_f32_16x16x32_bf16 a[16:19],a[228:231],v[140:143],a[16:19]
v_exp_f32 v174,v174
v_mfma_f32_16x16x32_bf16 a[20:23],a[236:239],v[140:143],a[20:23]
s_waitcnt lgkmcnt(0)
v_exp_f32 v175,v175
v_mfma_f32_16x16x32_bf16 a[20:23],a[232:235],v[136:139],a[20:23]
s_sub_u32 s79,s78,s76
v_exp_f32 v184,v184
v_mfma_f32_16x16x32_bf16 a[24:27],a[240:243],v[136:139],a[24:27]
s_add_u32 s78,s76,s60
v_exp_f32 v185,v185
v_mfma_f32_16x16x32_bf16 a[24:27],a[244:247],v[140:143],a[24:27]
s_cmp_gt_i32 s79,s95
s_cbranch_scc1 L_ps2_%=
L_pr2_%=:
v_cvt_pk_bf16_f32 v170,v172,v173
v_mfma_f32_16x16x32_bf16 a[28:31],a[252:255],v[140:143],a[28:31]
v_exp_f32 v186,v186
v_exp_f32 v187,v187
v_exp_f32 v188,v188
v_exp_f32 v189,v189
v_exp_f32 v190,v190
v_exp_f32 v191,v191
v_mfma_f32_16x16x32_bf16 a[28:31],a[248:251],v[136:139],a[28:31]
v_cvt_pk_bf16_f32 v171,v174,v175
v_cvt_pk_bf16_f32 v172,v184,v185
v_cvt_pk_bf16_f32 v173,v186,v187
v_cvt_pk_bf16_f32 v174,v188,v189
v_cvt_pk_bf16_f32 v175,v190,v191
v_smfmac_f32_16x16x64_bf16 v[48:51],v[28:31],v[136:143],v27
s_mov_b32 s53,13
s_cmp_eq_u32 s40,3
s_cbranch_scc0 L_att0_%=
s_cmp_gt_i32 s18,0
s_cbranch_scc1 L_att0v0_%=
s_cmp_gt_i32 s19,0
s_cbranch_scc1 L_att0v1_%=
s_cmp_gt_i32 s100,0
s_cbranch_scc1 L_att0v2_%=
.p2align 5
L_att0v3_%=:
s_waitcnt vmcnt(8) lgkmcnt(0)
v_exp_f32 v200,v200
s_barrier
s_add_u32 m0,s49,s51
ds_read_b128 a[128:131],v1
s_mov_b32 s90,L_endseg_%=-L_pc_%=+128
v_exp_f32 v201,v201
s_lshr_b32 s45,s45,1
ds_read_b128 a[132:135],v1 offset:256
v_cvt_pk_bf16_f32 v200,v200,v201
buffer_load_dwordx4 v4,s[20:23],s47 offen nt lds
ds_read_b128 a[136:139],v1 offset:512
v_add_u32 v2,s48,v2
v_exp_f32 v202,v202
s_add_u32 s62,s50,32768
ds_read_b128 a[140:143],v1 offset:768
v_add_u32 v3,s48,v3
s_and_b32 s62,s62,131071
v_exp_f32 v203,v203
s_add_u32 s54,s54,16384
ds_read_b128 a[156:159],v1 offset:4864
v_cvt_pk_bf16_f32 v201,v202,v203
s_sub_u32 s48,s62,s50
v_exp_f32 v204,v204
s_sub_u32 s53,s53,1
v_exp_f32 v205,v205
s_and_b32 s46,s54,0xfffc0000
ds_read_b128 a[152:155],v1 offset:4608
v_cvt_pk_bf16_f32 v202,v204,v205
s_mov_b32 s50,s62
v_exp_f32 v206,v206
ds_read_b128 a[148:151],v1 offset:4352
v_exp_f32 v207,v207
v_mfma_f32_16x16x32_bf16 a[32:35],a[192:195],v[168:171],a[32:35]
ds_read_b128 a[144:147],v1 offset:4096
v_cvt_pk_bf16_f32 v203,v206,v207
v_mfma_f32_16x16x32_bf16 a[32:35],a[196:199],v[172:175],a[32:35]
v_exp_f32 v216,v216
v_mfma_f32_16x16x32_bf16 a[36:39],a[204:207],v[172:175],a[36:39]
buffer_load_dwordx4 v4,s[20:23],s47 offen offset:1024 nt lds
v_mfma_f32_16x16x32_bf16 a[36:39],a[200:203],v[168:171],a[36:39]
ds_read_b128 a[160:163],v1 offset:8192
v_mfma_f32_16x16x32_bf16 a[40:43],a[208:211],v[168:171],a[40:43]
v_exp_f32 v217,v217
v_mfma_f32_16x16x32_bf16 a[40:43],a[212:215],v[172:175],a[40:43]
ds_read_b128 a[164:167],v1 offset:8448
v_cvt_pk_bf16_f32 v204,v216,v217
v_mfma_f32_16x16x32_bf16 a[44:47],a[220:223],v[172:175],a[44:47]
v_exp_f32 v218,v218
v_mfma_f32_16x16x32_bf16 a[44:47],a[216:219],v[168:171],a[44:47]
ds_read_b128 a[168:171],v1 offset:8704
v_mfma_f32_16x16x32_bf16 a[48:51],a[224:227],v[168:171],a[48:51]
v_exp_f32 v219,v219
v_mfma_f32_16x16x32_bf16 a[48:51],a[228:231],v[172:175],a[48:51]
ds_read_b128 a[172:175],v1 offset:8960
v_cvt_pk_bf16_f32 v205,v218,v219
v_mfma_f32_16x16x32_bf16 a[52:55],a[236:239],v[172:175],a[52:55]
v_exp_f32 v220,v220
v_mfma_f32_16x16x32_bf16 a[52:55],a[232:235],v[168:171],a[52:55]
ds_read_b128 a[188:191],v1 offset:13056
v_mfma_f32_16x16x32_bf16 a[56:59],a[240:243],v[168:171],a[56:59]
v_exp_f32 v221,v221
v_mfma_f32_16x16x32_bf16 a[56:59],a[244:247],v[172:175],a[56:59]
ds_read_b128 a[184:187],v1 offset:12800
v_cvt_pk_bf16_f32 v206,v220,v221
v_mfma_f32_16x16x32_bf16 a[60:63],a[252:255],v[172:175],a[60:63]
v_exp_f32 v222,v222
v_mfma_f32_16x16x32_bf16 a[60:63],a[248:251],v[168:171],a[60:63]
ds_read_b128 a[180:183],v1 offset:12544
v_smfmac_f32_16x16x64_bf16 v[52:55],v[28:31],v[168:175],v27
s_waitcnt lgkmcnt(4)
v_mfma_f32_16x16x32_bf16 v[136:139],a[128:131],v[64:67],v[32:35]
buffer_load_dwordx4 v5,s[20:23],s47 offen offset:2048 nt lds
v_mfma_f32_16x16x32_bf16 v[136:139],a[132:135],v[68:71],v[136:139]
ds_read_b128 a[176:179],v1 offset:12288
v_add_u32 v1,s48,v1
v_mfma_f32_16x16x32_bf16 v[136:139],a[136:139],v[72:75],v[136:139]
v_exp_f32 v223,v223
v_mfma_f32_16x16x32_bf16 v[136:139],a[140:143],v[76:79],v[136:139]
)A"
R"A(v_cvt_pk_bf16_f32 v207,v222,v223
v_mfma_f32_16x16x32_bf16 v[140:143],a[156:159],v[76:79],v[32:35]
v_mfma_f32_16x16x32_bf16 v[140:143],a[152:155],v[72:75],v[140:143]
v_mfma_f32_16x16x32_bf16 v[140:143],a[148:151],v[68:71],v[140:143]
v_mfma_f32_16x16x32_bf16 v[140:143],a[144:147],v[64:67],v[140:143]
v_mfma_f32_16x16x32_bf16 v[152:155],a[160:163],v[64:67],v[32:35]
v_mfma_f32_16x16x32_bf16 v[152:155],a[164:167],v[68:71],v[152:155]
v_mfma_f32_16x16x32_bf16 v[152:155],a[168:171],v[72:75],v[152:155]
s_waitcnt lgkmcnt(0)
v_mfma_f32_16x16x32_bf16 v[152:155],a[172:175],v[76:79],v[152:155]
v_mfma_f32_16x16x32_bf16 v[156:159],a[188:191],v[76:79],v[32:35]
v_mfma_f32_16x16x32_bf16 v[156:159],a[184:187],v[72:75],v[156:159]
buffer_load_dwordx4 v5,s[20:23],s47 offen offset:3072 nt lds
v_mfma_f32_16x16x32_bf16 v[156:159],a[180:183],v[68:71],v[156:159]
v_mfma_f32_16x16x32_bf16 v[156:159],a[176:179],v[64:67],v[156:159]
v_mfma_f32_16x16x32_bf16 a[64:67],a[192:195],v[200:203],a[64:67]
v_mfma_f32_16x16x32_bf16 a[64:67],a[196:199],v[204:207],a[64:67]
v_mfma_f32_16x16x32_bf16 a[68:71],a[204:207],v[204:207],a[68:71]
v_mfma_f32_16x16x32_bf16 a[68:71],a[200:203],v[200:203],a[68:71]
v_mfma_f32_16x16x32_bf16 a[72:75],a[208:211],v[200:203],a[72:75]
v_mfma_f32_16x16x32_bf16 a[72:75],a[212:215],v[204:207],a[72:75]
v_exp_f32 v136,v136
v_mfma_f32_16x16x32_bf16 a[76:79],a[220:223],v[204:207],a[76:79]
v_exp_f32 v137,v137
v_mfma_f32_16x16x32_bf16 a[76:79],a[216:219],v[200:203],a[76:79]
v_exp_f32 v138,v138
v_mfma_f32_16x16x32_bf16 a[80:83],a[224:227],v[200:203],a[80:83]
s_add_u32 m0,s49,s52
v_mfma_f32_16x16x32_bf16 a[80:83],a[228:231],v[204:207],a[80:83]
s_xor_b32 s49,s50,65536
v_mfma_f32_16x16x32_bf16 a[84:87],a[236:239],v[204:207],a[84:87]
v_mfma_f32_16x16x32_bf16 a[84:87],a[232:235],v[200:203],a[84:87]
buffer_load_dwordx4 v4,s[24:27],s47 offen nt lds
v_mfma_f32_16x16x32_bf16 a[88:91],a[240:243],v[200:203],a[88:91]
v_mfma_f32_16x16x32_bf16 a[88:91],a[244:247],v[204:207],a[88:91]
v_exp_f32 v139,v139
v_mfma_f32_16x16x32_bf16 a[92:95],a[252:255],v[204:207],a[92:95]
v_exp_f32 v140,v140
v_mfma_f32_16x16x32_bf16 a[92:95],a[248:251],v[200:203],a[92:95]
v_exp_f32 v141,v141
v_smfmac_f32_16x16x64_bf16 v[56:59],v[28:31],v[200:207],v27
v_mfma_f32_16x16x32_bf16 v[168:171],a[128:131],v[80:83],v[36:39]
v_exp_f32 v142,v142
v_mfma_f32_16x16x32_bf16 v[168:171],a[132:135],v[84:87],v[168:171]
v_exp_f32 v143,v143
v_mfma_f32_16x16x32_bf16 v[168:171],a[136:139],v[88:91],v[168:171]
v_exp_f32 v152,v152
v_mfma_f32_16x16x32_bf16 v[168:171],a[140:143],v[92:95],v[168:171]
v_exp_f32 v153,v153
v_mfma_f32_16x16x32_bf16 v[172:175],a[156:159],v[92:95],v[36:39]
v_exp_f32 v154,v154
v_mfma_f32_16x16x32_bf16 v[172:175],a[152:155],v[88:91],v[172:175]
v_exp_f32 v155,v155
v_mfma_f32_16x16x32_bf16 v[172:175],a[148:151],v[84:87],v[172:175]
v_exp_f32 v156,v156
v_mfma_f32_16x16x32_bf16 v[172:175],a[144:147],v[80:83],v[172:175]
v_exp_f32 v157,v157
v_mfma_f32_16x16x32_bf16 v[184:187],a[160:163],v[80:83],v[36:39]
buffer_load_dwordx4 v5,s[24:27],s47 offen offset:1024 nt lds
v_mfma_f32_16x16x32_bf16 v[184:187],a[164:167],v[84:87],v[184:187]
v_exp_f32 v158,v158
v_mfma_f32_16x16x32_bf16 v[184:187],a[168:171],v[88:91],v[184:187]
v_exp_f32 v159,v159
v_mfma_f32_16x16x32_bf16 v[184:187],a[172:175],v[92:95],v[184:187]
v_exp_f32 v168,v168
v_mfma_f32_16x16x32_bf16 v[188:191],a[188:191],v[92:95],v[36:39]
v_exp_f32 v169,v169
v_mfma_f32_16x16x32_bf16 v[188:191],a[184:187],v[88:91],v[188:191]
v_exp_f32 v170,v170
v_mfma_f32_16x16x32_bf16 v[188:191],a[180:183],v[84:87],v[188:191]
v_exp_f32 v171,v171
v_mfma_f32_16x16x32_bf16 v[188:191],a[176:179],v[80:83],v[188:191]
v_exp_f32 v172,v172
ds_read_b64_tr_b16 a[192:193],v2
v_cvt_pk_bf16_f32 v168,v168,v169
ds_read_b64_tr_b16 a[194:195],v2 offset:4096
v_cvt_pk_bf16_f32 v169,v170,v171
v_exp_f32 v173,v173
ds_read_b64_tr_b16 a[196:197],v2 offset:8192
v_cvt_pk_bf16_f32 v170,v172,v173
ds_read_b64_tr_b16 a[198:199],v2 offset:12288
v_cvt_pk_bf16_f32 v136,v136,v137
v_exp_f32 v174,v174
buffer_load_dwordx4 v4,s[24:27],s47 offen offset:2048 nt lds
ds_read_b64_tr_b16 a[204:205],v3 offset:8192
v_cvt_pk_bf16_f32 v137,v138,v139
v_exp_f32 v175,v175
ds_read_b64_tr_b16 a[206:207],v3 offset:12288
v_cvt_pk_bf16_f32 v171,v174,v175
ds_read_b64_tr_b16 a[200:201],v3
v_cvt_pk_bf16_f32 v138,v140,v141
ds_read_b64_tr_b16 a[202:203],v3 offset:4096
ds_read_b64_tr_b16 a[208:209],v2 offset:256
ds_read_b64_tr_b16 a[210:211],v2 offset:4352
v_cvt_pk_bf16_f32 v139,v142,v143
ds_read_b64_tr_b16 a[212:213],v2 offset:8448
ds_read_b64_tr_b16 a[214:215],v2 offset:12544
ds_read_b64_tr_b16 a[220:221],v3 offset:8448
v_mfma_f32_16x16x32_bf16 v[200:203],a[128:131],v[96:99],v[40:43]
ds_read_b64_tr_b16 a[222:223],v3 offset:12544
v_cvt_pk_bf16_f32 v140,v152,v153
v_mfma_f32_16x16x32_bf16 v[200:203],a[132:135],v[100:103],v[200:203]
ds_read_b64_tr_b16 a[216:217],v3 offset:256
v_mfma_f32_16x16x32_bf16 v[200:203],a[136:139],v[104:107],v[200:203]
ds_read_b64_tr_b16 a[218:219],v3 offset:4352
v_cvt_pk_bf16_f32 v141,v154,v155
v_mfma_f32_16x16x32_bf16 v[200:203],a[140:143],v[108:111],v[200:203]
buffer_load_dwordx4 v5,s[24:27],s47 offen offset:3072 nt lds
v_mfma_f32_16x16x32_bf16 v[204:207],a[156:159],v[108:111],v[40:43]
s_add_u32 s47,s47,16384
s_and_b32 s47,s47,0x3ffff
ds_read_b64_tr_b16 a[224:225],v2 offset:512
v_mfma_f32_16x16x32_bf16 v[204:207],a[152:155],v[104:107],v[204:207]
s_or_b32 s47,s47,s46
ds_read_b64_tr_b16 a[226:227],v2 offset:4608
v_cvt_pk_bf16_f32 v142,v156,v157
v_mfma_f32_16x16x32_bf16 v[204:207],a[148:151],v[100:103],v[204:207]
ds_read_b64_tr_b16 a[228:229],v2 offset:8704
v_mfma_f32_16x16x32_bf16 v[204:207],a[144:147],v[96:99],v[204:207]
ds_read_b64_tr_b16 a[230:231],v2 offset:12800
v_mfma_f32_16x16x32_bf16 v[216:219],a[160:163],v[96:99],v[40:43]
ds_read_b64_tr_b16 a[236:237],v3 offset:8704
v_cvt_pk_bf16_f32 v143,v158,v159
v_mfma_f32_16x16x32_bf16 v[216:219],a[164:167],v[100:103],v[216:219]
ds_read_b64_tr_b16 a[238:239],v3 offset:12800
v_mfma_f32_16x16x32_bf16 v[216:219],a[168:171],v[104:107],v[216:219]
ds_read_b64_tr_b16 a[232:233],v3 offset:512
v_mfma_f32_16x16x32_bf16 v[216:219],a[172:175],v[108:111],v[216:219]
ds_read_b64_tr_b16 a[234:235],v3 offset:4608
v_mfma_f32_16x16x32_bf16 v[220:223],a[188:191],v[108:111],v[40:43]
ds_read_b64_tr_b16 a[240:241],v2 offset:768
v_mfma_f32_16x16x32_bf16 v[220:223],a[184:187],v[104:107],v[220:223]
ds_read_b64_tr_b16 a[242:243],v2 offset:4864
v_mfma_f32_16x16x32_bf16 v[220:223],a[180:183],v[100:103],v[220:223]
ds_read_b64_tr_b16 a[244:245],v2 offset:8960
v_mfma_f32_16x16x32_bf16 v[220:223],a[176:179],v[96:99],v[220:223]
s_waitcnt lgkmcnt(7)
buffer_load_dword v26,v24,s[88:91],s93 offen
v_mfma_f32_16x16x32_bf16 a[0:3],a[192:195],v[136:139],a[0:3]
ds_read_b64_tr_b16 a[246:247],v2 offset:13056
v_mfma_f32_16x16x32_bf16 a[0:3],a[196:199],v[140:143],a[0:3]
ds_read_b64_tr_b16 a[252:253],v3 offset:8960
v_mfma_f32_16x16x32_bf16 a[4:7],a[204:207],v[140:143],a[4:7]
ds_read_b64_tr_b16 a[254:255],v3 offset:13056
v_mfma_f32_16x16x32_bf16 a[4:7],a[200:203],v[136:139],a[4:7]
ds_read_b64_tr_b16 a[248:249],v3 offset:768
v_mfma_f32_16x16x32_bf16 a[8:11],a[208:211],v[136:139],a[8:11]
ds_read_b64_tr_b16 a[250:251],v3 offset:4864
v_mfma_f32_16x16x32_bf16 a[8:11],a[212:215],v[140:143],a[8:11]
v_exp_f32 v184,v184
v_mfma_f32_16x16x32_bf16 a[12:15],a[220:223],v[140:143],a[12:15]
v_exp_f32 v185,v185
v_mfma_f32_16x16x32_bf16 a[12:15],a[216:219],v[136:139],a[12:15]
s_bitcmp1_b32 s45,0
s_cbranch_scc0 L_t31v30_%=
s_mov_b32 m0,s80
s_add_u32 s80,s80,4096
buffer_load_dwordx4 v25,s[20:23],s83 offen nt lds
s_add_u32 s83,s83,4096
L_t31v30_%=:
v_mfma_f32_16x16x32_bf16 a[16:19],a[224:227],v[136:139],a[16:19]
v_exp_f32 v186,v186
v_mfma_f32_16x16x32_bf16 a[16:19],a[228:231],v[140:143],a[16:19]
s_waitcnt lgkmcnt(3)
v_exp_f32 v187,v187
v_mfma_f32_16x16x32_bf16 a[20:23],a[236:239],v[140:143],a[20:23]
v_exp_f32 v188,v188
v_mfma_f32_16x16x32_bf16 a[20:23],a[232:235],v[136:139],a[20:23]
v_exp_f32 v189,v189
v_mfma_f32_16x16x32_bf16 a[24:27],a[240:243],v[136:139],a[24:27]
v_exp_f32 v190,v190
v_mfma_f32_16x16x32_bf16 a[24:27],a[244:247],v[140:143],a[24:27]
s_waitcnt lgkmcnt(0)
v_cvt_pk_bf16_f32 v172,v184,v185
v_mfma_f32_16x16x32_bf16 a[28:31],a[252:255],v[140:143],a[28:31]
v_exp_f32 v191,v191
v_mfma_f32_16x16x32_bf16 a[28:31],a[248:251],v[136:139],a[28:31]
v_cvt_pk_bf16_f32 v173,v186,v187
v_cvt_pk_bf16_f32 v174,v188,v189
v_cvt_pk_bf16_f32 v175,v190,v191
s_cmp_lg_u32 s53,0
v_smfmac_f32_16x16x64_bf16 v[48:51],v[28:31],v[136:143],v27
s_cbranch_scc1 L_att0v3_%=
s_waitcnt lgkmcnt(0)
s_nop 7
s_branch L_att0x_%=
.p2align 5
L_att0v0_%=:
v_mfma_f32_16x16x32_bf16 v[232:235],a[128:131],v[112:115],v[44:47]
s_waitcnt vmcnt(8) lgkmcnt(0)
v_exp_f32 v200,v200
v_mfma_f32_16x16x32_bf16 v[232:235],a[132:135],v[116:119],v[232:235]
s_barrier
s_add_u32 m0,s49,s51
ds_read_b128 a[128:131],v1
v_mfma_f32_16x16x32_bf16 v[232:235],a[136:139],v[120:123],v[232:235]
s_mov_b32 s90,L_endseg_%=-L_pc_%=+128
v_exp_f32 v201,v201
v_mfma_f32_16x16x32_bf16 v[232:235],a[140:143],v[124:127],v[232:235]
s_lshr_b32 s45,s45,1
ds_read_b128 a[132:135],v1 offset:256
v_cvt_pk_bf16_f32 v200,v200,v201
v_mfma_f32_16x16x32_bf16 v[236:239],a[156:159],v[124:127],v[44:47]
buffer_load_dwordx4 v4,s[20:23],s47 offen nt lds
v_mfma_f32_16x16x32_bf16 v[236:239],a[152:155],v[120:123],v[236:239]
ds_read_b128 a[136:139],v1 offset:512
v_add_u32 v2,s48,v2
v_mfma_f32_16x16x32_bf16 v[236:239],a[148:151],v[116:119],v[236:239]
v_exp_f32 v202,v202
v_mfma_f32_16x16x32_bf16 v[236:239],a[144:147],v[112:115],v[236:239]
s_add_u32 s62,s50,32768
ds_read_b128 a[140:143],v1 offset:768
v_add_u32 v3,s48,v3
v_mfma_f32_16x16x32_bf16 v[248:251],a[160:163],v[112:115],v[44:47]
s_and_b32 s62,s62,131071
v_exp_f32 v203,v203
v_mfma_f32_16x16x32_bf16 v[248:251],a[164:167],v[116:119],v[248:251]
s_add_u32 s54,s54,16384
ds_read_b128 a[156:159],v1 offset:4864
v_cvt_pk_bf16_f32 v201,v202,v203
v_mfma_f32_16x16x32_bf16 v[248:251],a[168:171],v[120:123],v[248:251]
s_sub_u32 s48,s62,s50
v_exp_f32 v204,v204
v_mfma_f32_16x16x32_bf16 v[248:251],a[172:175],v[124:127],v[248:251]
s_sub_u32 s53,s53,1
v_exp_f32 v205,v205
v_mfma_f32_16x16x32_bf16 v[252:255],a[188:191],v[124:127],v[44:47]
s_and_b32 s46,s54,0xfffc0000
ds_read_b128 a[152:155],v1 offset:4608
v_cvt_pk_bf16_f32 v202,v204,v205
v_mfma_f32_16x16x32_bf16 v[252:255],a[184:187],v[120:123],v[252:255]
s_mov_b32 s50,s62
v_exp_f32 v206,v206
v_mfma_f32_16x16x32_bf16 v[252:255],a[180:183],v[116:119],v[252:255]
ds_read_b128 a[148:151],v1 offset:4352
v_mfma_f32_16x16x32_bf16 v[252:255],a[176:179],v[112:115],v[252:255]
v_exp_f32 v207,v207
v_mfma_f32_16x16x32_bf16 a[32:35],a[192:195],v[168:171],a[32:35]
ds_read_b128 a[144:147],v1 offset:4096
v_cvt_pk_bf16_f32 v203,v206,v207
v_mfma_f32_16x16x32_bf16 a[32:35],a[196:199],v[172:175],a[32:35]
v_exp_f32 v216,v216
v_mfma_f32_16x16x32_bf16 a[36:39],a[204:207],v[172:175],a[36:39]
buffer_load_dwordx4 v4,s[20:23],s47 offen offset:1024 nt lds
v_mfma_f32_16x16x32_bf16 a[36:39],a[200:203],v[168:171],a[36:39]
ds_read_b128 a[160:163],v1 offset:8192
v_mfma_f32_16x16x32_bf16 a[40:43],a[208:211],v[168:171],a[40:43]
v_exp_f32 v217,v217
v_mfma_f32_16x16x32_bf16 a[40:43],a[212:215],v[172:175],a[40:43]
ds_read_b128 a[164:167],v1 offset:8448
v_cvt_pk_bf16_f32 v204,v216,v217
v_mfma_f32_16x16x32_bf16 a[44:47],a[220:223],v[172:175],a[44:47]
v_exp_f32 v218,v218
v_mfma_f32_16x16x32_bf16 a[44:47],a[216:219],v[168:171],a[44:47]
ds_read_b128 a[168:171],v1 offset:8704
v_mfma_f32_16x16x32_bf16 a[48:51],a[224:227],v[168:171],a[48:51]
v_exp_f32 v219,v219
v_mfma_f32_16x16x32_bf16 a[48:51],a[228:231],v[172:175],a[48:51]
ds_read_b128 a[172:175],v1 offset:8960
v_cvt_pk_bf16_f32 v205,v218,v219
v_mfma_f32_16x16x32_bf16 a[52:55],a[236:239],v[172:175],a[52:55]
v_exp_f32 v220,v220
v_mfma_f32_16x16x32_bf16 a[52:55],a[232:235],v[168:171],a[52:55]
ds_read_b128 a[188:191],v1 offset:13056
v_mfma_f32_16x16x32_bf16 a[56:59],a[240:243],v[168:171],a[56:59]
v_exp_f32 v221,v221
v_mfma_f32_16x16x32_bf16 a[56:59],a[244:247],v[172:175],a[56:59]
ds_read_b128 a[184:187],v1 offset:12800
)A"
R"A(v_cvt_pk_bf16_f32 v206,v220,v221
v_mfma_f32_16x16x32_bf16 a[60:63],a[252:255],v[172:175],a[60:63]
v_exp_f32 v222,v222
v_mfma_f32_16x16x32_bf16 a[60:63],a[248:251],v[168:171],a[60:63]
ds_read_b128 a[180:183],v1 offset:12544
v_smfmac_f32_16x16x64_bf16 v[52:55],v[28:31],v[168:175],v27
buffer_load_dwordx4 v5,s[20:23],s47 offen offset:2048 nt lds
ds_read_b128 a[176:179],v1 offset:12288
v_add_u32 v1,s48,v1
v_exp_f32 v223,v223
s_nop 0
v_cvt_pk_bf16_f32 v207,v222,v223
v_exp_f32 v232,v232
v_exp_f32 v233,v233
v_exp_f32 v234,v234
v_exp_f32 v235,v235
v_exp_f32 v236,v236
v_exp_f32 v237,v237
v_exp_f32 v238,v238
v_exp_f32 v239,v239
v_exp_f32 v248,v248
buffer_load_dwordx4 v5,s[20:23],s47 offen offset:3072 nt lds
v_exp_f32 v249,v249
v_exp_f32 v250,v250
v_mfma_f32_16x16x32_bf16 a[64:67],a[192:195],v[200:203],a[64:67]
v_exp_f32 v251,v251
v_mfma_f32_16x16x32_bf16 a[64:67],a[196:199],v[204:207],a[64:67]
v_exp_f32 v252,v252
v_mfma_f32_16x16x32_bf16 a[68:71],a[204:207],v[204:207],a[68:71]
v_exp_f32 v253,v253
v_mfma_f32_16x16x32_bf16 a[68:71],a[200:203],v[200:203],a[68:71]
v_exp_f32 v254,v254
v_mfma_f32_16x16x32_bf16 a[72:75],a[208:211],v[200:203],a[72:75]
v_exp_f32 v255,v255
v_mfma_f32_16x16x32_bf16 a[72:75],a[212:215],v[204:207],a[72:75]
v_mfma_f32_16x16x32_bf16 a[76:79],a[220:223],v[204:207],a[76:79]
v_mfma_f32_16x16x32_bf16 a[76:79],a[216:219],v[200:203],a[76:79]
v_mfma_f32_16x16x32_bf16 a[80:83],a[224:227],v[200:203],a[80:83]
s_add_u32 m0,s49,s52
v_cvt_pk_bf16_f32 v232,v232,v233
v_cvt_pk_bf16_f32 v233,v234,v235
v_mfma_f32_16x16x32_bf16 a[80:83],a[228:231],v[204:207],a[80:83]
s_xor_b32 s49,s50,65536
v_cvt_pk_bf16_f32 v234,v236,v237
v_cvt_pk_bf16_f32 v235,v238,v239
v_mfma_f32_16x16x32_bf16 a[84:87],a[236:239],v[204:207],a[84:87]
v_cvt_pk_bf16_f32 v236,v248,v249
v_cvt_pk_bf16_f32 v237,v250,v251
v_mfma_f32_16x16x32_bf16 a[84:87],a[232:235],v[200:203],a[84:87]
buffer_load_dwordx4 v4,s[24:27],s47 offen nt lds
v_mfma_f32_16x16x32_bf16 a[88:91],a[240:243],v[200:203],a[88:91]
v_cvt_pk_bf16_f32 v238,v252,v253
v_cvt_pk_bf16_f32 v239,v254,v255
v_mfma_f32_16x16x32_bf16 a[88:91],a[244:247],v[204:207],a[88:91]
v_mfma_f32_16x16x32_bf16 a[92:95],a[252:255],v[204:207],a[92:95]
v_mfma_f32_16x16x32_bf16 a[92:95],a[248:251],v[200:203],a[92:95]
v_smfmac_f32_16x16x64_bf16 v[56:59],v[28:31],v[200:207],v27
s_waitcnt lgkmcnt(0)
v_mfma_f32_16x16x32_bf16 v[168:171],a[128:131],v[80:83],v[36:39]
v_mfma_f32_16x16x32_bf16 v[168:171],a[132:135],v[84:87],v[168:171]
v_mfma_f32_16x16x32_bf16 v[168:171],a[136:139],v[88:91],v[168:171]
v_mfma_f32_16x16x32_bf16 v[168:171],a[140:143],v[92:95],v[168:171]
v_mfma_f32_16x16x32_bf16 v[172:175],a[156:159],v[92:95],v[36:39]
v_mfma_f32_16x16x32_bf16 v[172:175],a[152:155],v[88:91],v[172:175]
v_mfma_f32_16x16x32_bf16 v[172:175],a[148:151],v[84:87],v[172:175]
v_mfma_f32_16x16x32_bf16 v[172:175],a[144:147],v[80:83],v[172:175]
v_mfma_f32_16x16x32_bf16 v[184:187],a[160:163],v[80:83],v[36:39]
buffer_load_dwordx4 v5,s[24:27],s47 offen offset:1024 nt lds
v_mfma_f32_16x16x32_bf16 v[184:187],a[164:167],v[84:87],v[184:187]
v_mfma_f32_16x16x32_bf16 v[184:187],a[168:171],v[88:91],v[184:187]
v_mfma_f32_16x16x32_bf16 v[184:187],a[172:175],v[92:95],v[184:187]
v_exp_f32 v168,v168
v_mfma_f32_16x16x32_bf16 v[188:191],a[188:191],v[92:95],v[36:39]
v_exp_f32 v169,v169
v_mfma_f32_16x16x32_bf16 v[188:191],a[184:187],v[88:91],v[188:191]
v_exp_f32 v170,v170
v_mfma_f32_16x16x32_bf16 v[188:191],a[180:183],v[84:87],v[188:191]
v_exp_f32 v171,v171
v_mfma_f32_16x16x32_bf16 v[188:191],a[176:179],v[80:83],v[188:191]
v_exp_f32 v172,v172
v_mfma_f32_16x16x32_bf16 a[96:99],a[192:195],v[232:235],a[96:99]
ds_read_b64_tr_b16 a[192:193],v2
v_cvt_pk_bf16_f32 v168,v168,v169
v_mfma_f32_16x16x32_bf16 a[96:99],a[196:199],v[236:239],a[96:99]
ds_read_b64_tr_b16 a[194:195],v2 offset:4096
v_cvt_pk_bf16_f32 v169,v170,v171
v_mfma_f32_16x16x32_bf16 a[100:103],a[204:207],v[236:239],a[100:103]
v_exp_f32 v173,v173
v_mfma_f32_16x16x32_bf16 a[100:103],a[200:203],v[232:235],a[100:103]
ds_read_b64_tr_b16 a[196:197],v2 offset:8192
v_cvt_pk_bf16_f32 v170,v172,v173
v_mfma_f32_16x16x32_bf16 a[104:107],a[208:211],v[232:235],a[104:107]
ds_read_b64_tr_b16 a[198:199],v2 offset:12288
v_mfma_f32_16x16x32_bf16 a[104:107],a[212:215],v[236:239],a[104:107]
v_exp_f32 v174,v174
v_mfma_f32_16x16x32_bf16 a[108:111],a[220:223],v[236:239],a[108:111]
buffer_load_dwordx4 v4,s[24:27],s47 offen offset:2048 nt lds
v_mfma_f32_16x16x32_bf16 a[108:111],a[216:219],v[232:235],a[108:111]
ds_read_b64_tr_b16 a[204:205],v3 offset:8192
v_mfma_f32_16x16x32_bf16 a[112:115],a[224:227],v[232:235],a[112:115]
v_exp_f32 v175,v175
v_mfma_f32_16x16x32_bf16 a[112:115],a[228:231],v[236:239],a[112:115]
ds_read_b64_tr_b16 a[206:207],v3 offset:12288
v_cvt_pk_bf16_f32 v171,v174,v175
v_mfma_f32_16x16x32_bf16 a[116:119],a[236:239],v[236:239],a[116:119]
ds_read_b64_tr_b16 a[200:201],v3
v_mfma_f32_16x16x32_bf16 a[116:119],a[232:235],v[232:235],a[116:119]
ds_read_b64_tr_b16 a[202:203],v3 offset:4096
v_mfma_f32_16x16x32_bf16 a[120:123],a[240:243],v[232:235],a[120:123]
ds_read_b64_tr_b16 a[208:209],v2 offset:256
v_mfma_f32_16x16x32_bf16 a[120:123],a[244:247],v[236:239],a[120:123]
ds_read_b64_tr_b16 a[210:211],v2 offset:4352
v_mfma_f32_16x16x32_bf16 a[124:127],a[252:255],v[236:239],a[124:127]
ds_read_b64_tr_b16 a[212:213],v2 offset:8448
v_mfma_f32_16x16x32_bf16 a[124:127],a[248:251],v[232:235],a[124:127]
ds_read_b64_tr_b16 a[214:215],v2 offset:12544
v_smfmac_f32_16x16x64_bf16 v[60:63],v[28:31],v[232:239],v27
ds_read_b64_tr_b16 a[220:221],v3 offset:8448
v_mfma_f32_16x16x32_bf16 v[200:203],a[128:131],v[96:99],v[40:43]
ds_read_b64_tr_b16 a[222:223],v3 offset:12544
v_mfma_f32_16x16x32_bf16 v[200:203],a[132:135],v[100:103],v[200:203]
ds_read_b64_tr_b16 a[216:217],v3 offset:256
v_mfma_f32_16x16x32_bf16 v[200:203],a[136:139],v[104:107],v[200:203]
ds_read_b64_tr_b16 a[218:219],v3 offset:4352
v_mfma_f32_16x16x32_bf16 v[200:203],a[140:143],v[108:111],v[200:203]
buffer_load_dwordx4 v5,s[24:27],s47 offen offset:3072 nt lds
v_mfma_f32_16x16x32_bf16 v[204:207],a[156:159],v[108:111],v[40:43]
s_add_u32 s47,s47,16384
s_and_b32 s47,s47,0x3ffff
ds_read_b64_tr_b16 a[224:225],v2 offset:512
v_mfma_f32_16x16x32_bf16 v[204:207],a[152:155],v[104:107],v[204:207]
s_or_b32 s47,s47,s46
ds_read_b64_tr_b16 a[226:227],v2 offset:4608
v_mfma_f32_16x16x32_bf16 v[204:207],a[148:151],v[100:103],v[204:207]
ds_read_b64_tr_b16 a[228:229],v2 offset:8704
v_mfma_f32_16x16x32_bf16 v[204:207],a[144:147],v[96:99],v[204:207]
ds_read_b64_tr_b16 a[230:231],v2 offset:12800
v_mfma_f32_16x16x32_bf16 v[216:219],a[160:163],v[96:99],v[40:43]
ds_read_b64_tr_b16 a[236:237],v3 offset:8704
v_mfma_f32_16x16x32_bf16 v[216:219],a[164:167],v[100:103],v[216:219]
ds_read_b64_tr_b16 a[238:239],v3 offset:12800
v_mfma_f32_16x16x32_bf16 v[216:219],a[168:171],v[104:107],v[216:219]
ds_read_b64_tr_b16 a[232:233],v3 offset:512
v_mfma_f32_16x16x32_bf16 v[216:219],a[172:175],v[108:111],v[216:219]
ds_read_b64_tr_b16 a[234:235],v3 offset:4608
v_mfma_f32_16x16x32_bf16 v[220:223],a[188:191],v[108:111],v[40:43]
ds_read_b64_tr_b16 a[240:241],v2 offset:768
v_mfma_f32_16x16x32_bf16 v[220:223],a[184:187],v[104:107],v[220:223]
ds_read_b64_tr_b16 a[242:243],v2 offset:4864
v_mfma_f32_16x16x32_bf16 v[220:223],a[180:183],v[100:103],v[220:223]
ds_read_b64_tr_b16 a[244:245],v2 offset:8960
v_mfma_f32_16x16x32_bf16 v[220:223],a[176:179],v[96:99],v[220:223]
buffer_load_dword v26,v24,s[88:91],s93 offen
ds_read_b64_tr_b16 a[246:247],v2 offset:13056
ds_read_b64_tr_b16 a[252:253],v3 offset:8960
ds_read_b64_tr_b16 a[254:255],v3 offset:13056
ds_read_b64_tr_b16 a[248:249],v3 offset:768
ds_read_b64_tr_b16 a[250:251],v3 offset:4864
v_exp_f32 v184,v184
v_exp_f32 v185,v185
s_bitcmp1_b32 s45,0
s_cbranch_scc0 L_t31v00_%=
s_mov_b32 m0,s80
s_add_u32 s80,s80,4096
buffer_load_dwordx4 v25,s[20:23],s83 offen nt lds
s_add_u32 s83,s83,4096
L_t31v00_%=:
v_exp_f32 v186,v186
v_exp_f32 v187,v187
v_exp_f32 v188,v188
v_exp_f32 v189,v189
v_exp_f32 v190,v190
v_cvt_pk_bf16_f32 v172,v184,v185
v_exp_f32 v191,v191
v_cvt_pk_bf16_f32 v173,v186,v187
v_cvt_pk_bf16_f32 v174,v188,v189
v_cvt_pk_bf16_f32 v175,v190,v191
s_cmp_lg_u32 s53,0
s_cbranch_scc1 L_att0v0_%=
s_waitcnt lgkmcnt(0)
s_nop 7
s_branch L_att0x_%=
.p2align 5
L_att0v1_%=:
v_mfma_f32_16x16x32_bf16 v[232:235],a[128:131],v[112:115],v[44:47]
s_waitcnt vmcnt(8) lgkmcnt(0)
v_exp_f32 v200,v200
v_mfma_f32_16x16x32_bf16 v[232:235],a[132:135],v[116:119],v[232:235]
s_barrier
s_add_u32 m0,s49,s51
ds_read_b128 a[128:131],v1
v_mfma_f32_16x16x32_bf16 v[232:235],a[136:139],v[120:123],v[232:235]
s_mov_b32 s90,L_endseg_%=-L_pc_%=+128
v_exp_f32 v201,v201
v_mfma_f32_16x16x32_bf16 v[232:235],a[140:143],v[124:127],v[232:235]
s_lshr_b32 s45,s45,1
ds_read_b128 a[132:135],v1 offset:256
v_cvt_pk_bf16_f32 v200,v200,v201
v_mfma_f32_16x16x32_bf16 v[236:239],a[156:159],v[124:127],v[44:47]
buffer_load_dwordx4 v4,s[20:23],s47 offen nt lds
v_mfma_f32_16x16x32_bf16 v[236:239],a[152:155],v[120:123],v[236:239]
ds_read_b128 a[136:139],v1 offset:512
v_add_u32 v2,s48,v2
v_mfma_f32_16x16x32_bf16 v[236:239],a[148:151],v[116:119],v[236:239]
v_exp_f32 v202,v202
v_mfma_f32_16x16x32_bf16 v[236:239],a[144:147],v[112:115],v[236:239]
s_add_u32 s62,s50,32768
ds_read_b128 a[140:143],v1 offset:768
v_add_u32 v3,s48,v3
v_mfma_f32_16x16x32_bf16 v[248:251],a[160:163],v[112:115],v[44:47]
s_and_b32 s62,s62,131071
v_exp_f32 v203,v203
v_mfma_f32_16x16x32_bf16 v[248:251],a[164:167],v[116:119],v[248:251]
s_add_u32 s54,s54,16384
ds_read_b128 a[156:159],v1 offset:4864
v_cvt_pk_bf16_f32 v201,v202,v203
v_mfma_f32_16x16x32_bf16 v[248:251],a[168:171],v[120:123],v[248:251]
s_sub_u32 s48,s62,s50
v_exp_f32 v204,v204
v_mfma_f32_16x16x32_bf16 v[248:251],a[172:175],v[124:127],v[248:251]
s_sub_u32 s53,s53,1
v_exp_f32 v205,v205
v_mfma_f32_16x16x32_bf16 v[252:255],a[188:191],v[124:127],v[44:47]
s_and_b32 s46,s54,0xfffc0000
ds_read_b128 a[152:155],v1 offset:4608
v_cvt_pk_bf16_f32 v202,v204,v205
v_mfma_f32_16x16x32_bf16 v[252:255],a[184:187],v[120:123],v[252:255]
s_mov_b32 s50,s62
v_exp_f32 v206,v206
v_mfma_f32_16x16x32_bf16 v[252:255],a[180:183],v[116:119],v[252:255]
ds_read_b128 a[148:151],v1 offset:4352
v_mfma_f32_16x16x32_bf16 v[252:255],a[176:179],v[112:115],v[252:255]
s_waitcnt lgkmcnt(3)
v_exp_f32 v207,v207
ds_read_b128 a[144:147],v1 offset:4096
v_cvt_pk_bf16_f32 v203,v206,v207
v_exp_f32 v216,v216
buffer_load_dwordx4 v4,s[20:23],s47 offen offset:1024 nt lds
ds_read_b128 a[160:163],v1 offset:8192
v_exp_f32 v217,v217
ds_read_b128 a[164:167],v1 offset:8448
v_cvt_pk_bf16_f32 v204,v216,v217
v_exp_f32 v218,v218
ds_read_b128 a[168:171],v1 offset:8704
v_exp_f32 v219,v219
ds_read_b128 a[172:175],v1 offset:8960
v_cvt_pk_bf16_f32 v205,v218,v219
v_exp_f32 v220,v220
ds_read_b128 a[188:191],v1 offset:13056
v_exp_f32 v221,v221
ds_read_b128 a[184:187],v1 offset:12800
v_cvt_pk_bf16_f32 v206,v220,v221
v_exp_f32 v222,v222
ds_read_b128 a[180:183],v1 offset:12544
v_mfma_f32_16x16x32_bf16 v[136:139],a[128:131],v[64:67],v[32:35]
buffer_load_dwordx4 v5,s[20:23],s47 offen offset:2048 nt lds
v_mfma_f32_16x16x32_bf16 v[136:139],a[132:135],v[68:71],v[136:139]
ds_read_b128 a[176:179],v1 offset:12288
v_add_u32 v1,s48,v1
v_mfma_f32_16x16x32_bf16 v[136:139],a[136:139],v[72:75],v[136:139]
v_exp_f32 v223,v223
v_mfma_f32_16x16x32_bf16 v[136:139],a[140:143],v[76:79],v[136:139]
s_waitcnt lgkmcnt(11)
v_cvt_pk_bf16_f32 v207,v222,v223
v_mfma_f32_16x16x32_bf16 v[140:143],a[156:159],v[76:79],v[32:35]
s_waitcnt lgkmcnt(10)
v_exp_f32 v232,v232
v_mfma_f32_16x16x32_bf16 v[140:143],a[152:155],v[72:75],v[140:143]
s_waitcnt lgkmcnt(9)
v_exp_f32 v233,v233
v_mfma_f32_16x16x32_bf16 v[140:143],a[148:151],v[68:71],v[140:143]
s_waitcnt lgkmcnt(8)
v_exp_f32 v234,v234
v_mfma_f32_16x16x32_bf16 v[140:143],a[144:147],v[64:67],v[140:143]
s_waitcnt lgkmcnt(1)
v_exp_f32 v235,v235
v_mfma_f32_16x16x32_bf16 v[152:155],a[160:163],v[64:67],v[32:35]
v_exp_f32 v236,v236
v_mfma_f32_16x16x32_bf16 v[152:155],a[164:167],v[68:71],v[152:155]
v_exp_f32 v237,v237
v_mfma_f32_16x16x32_bf16 v[152:155],a[168:171],v[72:75],v[152:155]
)A"
R"A(v_exp_f32 v238,v238
v_mfma_f32_16x16x32_bf16 v[152:155],a[172:175],v[76:79],v[152:155]
v_exp_f32 v239,v239
v_mfma_f32_16x16x32_bf16 v[156:159],a[188:191],v[76:79],v[32:35]
v_exp_f32 v248,v248
v_mfma_f32_16x16x32_bf16 v[156:159],a[184:187],v[72:75],v[156:159]
buffer_load_dwordx4 v5,s[20:23],s47 offen offset:3072 nt lds
v_mfma_f32_16x16x32_bf16 v[156:159],a[180:183],v[68:71],v[156:159]
s_waitcnt lgkmcnt(0)
v_exp_f32 v249,v249
v_mfma_f32_16x16x32_bf16 v[156:159],a[176:179],v[64:67],v[156:159]
v_exp_f32 v250,v250
v_mfma_f32_16x16x32_bf16 a[64:67],a[192:195],v[200:203],a[64:67]
v_exp_f32 v251,v251
v_mfma_f32_16x16x32_bf16 a[64:67],a[196:199],v[204:207],a[64:67]
v_exp_f32 v252,v252
v_mfma_f32_16x16x32_bf16 a[68:71],a[204:207],v[204:207],a[68:71]
v_exp_f32 v253,v253
v_mfma_f32_16x16x32_bf16 a[68:71],a[200:203],v[200:203],a[68:71]
v_exp_f32 v254,v254
v_mfma_f32_16x16x32_bf16 a[72:75],a[208:211],v[200:203],a[72:75]
v_exp_f32 v255,v255
v_mfma_f32_16x16x32_bf16 a[72:75],a[212:215],v[204:207],a[72:75]
v_exp_f32 v136,v136
v_mfma_f32_16x16x32_bf16 a[76:79],a[220:223],v[204:207],a[76:79]
v_exp_f32 v137,v137
v_mfma_f32_16x16x32_bf16 a[76:79],a[216:219],v[200:203],a[76:79]
v_exp_f32 v138,v138
v_mfma_f32_16x16x32_bf16 a[80:83],a[224:227],v[200:203],a[80:83]
s_add_u32 m0,s49,s52
v_cvt_pk_bf16_f32 v232,v232,v233
v_cvt_pk_bf16_f32 v233,v234,v235
v_mfma_f32_16x16x32_bf16 a[80:83],a[228:231],v[204:207],a[80:83]
s_xor_b32 s49,s50,65536
v_cvt_pk_bf16_f32 v234,v236,v237
v_cvt_pk_bf16_f32 v235,v238,v239
v_mfma_f32_16x16x32_bf16 a[84:87],a[236:239],v[204:207],a[84:87]
v_cvt_pk_bf16_f32 v236,v248,v249
v_cvt_pk_bf16_f32 v237,v250,v251
v_mfma_f32_16x16x32_bf16 a[84:87],a[232:235],v[200:203],a[84:87]
buffer_load_dwordx4 v4,s[24:27],s47 offen nt lds
v_mfma_f32_16x16x32_bf16 a[88:91],a[240:243],v[200:203],a[88:91]
v_cvt_pk_bf16_f32 v238,v252,v253
v_cvt_pk_bf16_f32 v239,v254,v255
v_mfma_f32_16x16x32_bf16 a[88:91],a[244:247],v[204:207],a[88:91]
v_exp_f32 v139,v139
v_mfma_f32_16x16x32_bf16 a[92:95],a[252:255],v[204:207],a[92:95]
v_exp_f32 v140,v140
v_mfma_f32_16x16x32_bf16 a[92:95],a[248:251],v[200:203],a[92:95]
v_exp_f32 v141,v141
v_smfmac_f32_16x16x64_bf16 v[56:59],v[28:31],v[200:207],v27
v_exp_f32 v142,v142
v_exp_f32 v143,v143
v_exp_f32 v152,v152
v_exp_f32 v153,v153
v_exp_f32 v154,v154
v_exp_f32 v155,v155
v_exp_f32 v156,v156
v_exp_f32 v157,v157
buffer_load_dwordx4 v5,s[24:27],s47 offen offset:1024 nt lds
v_exp_f32 v158,v158
v_exp_f32 v159,v159
v_mfma_f32_16x16x32_bf16 a[96:99],a[192:195],v[232:235],a[96:99]
ds_read_b64_tr_b16 a[192:193],v2
v_mfma_f32_16x16x32_bf16 a[96:99],a[196:199],v[236:239],a[96:99]
ds_read_b64_tr_b16 a[194:195],v2 offset:4096
v_mfma_f32_16x16x32_bf16 a[100:103],a[204:207],v[236:239],a[100:103]
v_mfma_f32_16x16x32_bf16 a[100:103],a[200:203],v[232:235],a[100:103]
ds_read_b64_tr_b16 a[196:197],v2 offset:8192
v_mfma_f32_16x16x32_bf16 a[104:107],a[208:211],v[232:235],a[104:107]
ds_read_b64_tr_b16 a[198:199],v2 offset:12288
v_cvt_pk_bf16_f32 v136,v136,v137
v_mfma_f32_16x16x32_bf16 a[104:107],a[212:215],v[236:239],a[104:107]
v_mfma_f32_16x16x32_bf16 a[108:111],a[220:223],v[236:239],a[108:111]
buffer_load_dwordx4 v4,s[24:27],s47 offen offset:2048 nt lds
v_mfma_f32_16x16x32_bf16 a[108:111],a[216:219],v[232:235],a[108:111]
ds_read_b64_tr_b16 a[204:205],v3 offset:8192
v_cvt_pk_bf16_f32 v137,v138,v139
v_mfma_f32_16x16x32_bf16 a[112:115],a[224:227],v[232:235],a[112:115]
v_mfma_f32_16x16x32_bf16 a[112:115],a[228:231],v[236:239],a[112:115]
ds_read_b64_tr_b16 a[206:207],v3 offset:12288
v_mfma_f32_16x16x32_bf16 a[116:119],a[236:239],v[236:239],a[116:119]
ds_read_b64_tr_b16 a[200:201],v3
v_cvt_pk_bf16_f32 v138,v140,v141
v_mfma_f32_16x16x32_bf16 a[116:119],a[232:235],v[232:235],a[116:119]
ds_read_b64_tr_b16 a[202:203],v3 offset:4096
v_mfma_f32_16x16x32_bf16 a[120:123],a[240:243],v[232:235],a[120:123]
ds_read_b64_tr_b16 a[208:209],v2 offset:256
v_mfma_f32_16x16x32_bf16 a[120:123],a[244:247],v[236:239],a[120:123]
ds_read_b64_tr_b16 a[210:211],v2 offset:4352
v_cvt_pk_bf16_f32 v139,v142,v143
v_mfma_f32_16x16x32_bf16 a[124:127],a[252:255],v[236:239],a[124:127]
ds_read_b64_tr_b16 a[212:213],v2 offset:8448
v_mfma_f32_16x16x32_bf16 a[124:127],a[248:251],v[232:235],a[124:127]
ds_read_b64_tr_b16 a[214:215],v2 offset:12544
v_smfmac_f32_16x16x64_bf16 v[60:63],v[28:31],v[232:239],v27
ds_read_b64_tr_b16 a[220:221],v3 offset:8448
v_mfma_f32_16x16x32_bf16 v[200:203],a[128:131],v[96:99],v[40:43]
ds_read_b64_tr_b16 a[222:223],v3 offset:12544
v_cvt_pk_bf16_f32 v140,v152,v153
v_mfma_f32_16x16x32_bf16 v[200:203],a[132:135],v[100:103],v[200:203]
ds_read_b64_tr_b16 a[216:217],v3 offset:256
v_mfma_f32_16x16x32_bf16 v[200:203],a[136:139],v[104:107],v[200:203]
ds_read_b64_tr_b16 a[218:219],v3 offset:4352
v_cvt_pk_bf16_f32 v141,v154,v155
v_mfma_f32_16x16x32_bf16 v[200:203],a[140:143],v[108:111],v[200:203]
buffer_load_dwordx4 v5,s[24:27],s47 offen offset:3072 nt lds
v_mfma_f32_16x16x32_bf16 v[204:207],a[156:159],v[108:111],v[40:43]
s_add_u32 s47,s47,16384
s_and_b32 s47,s47,0x3ffff
ds_read_b64_tr_b16 a[224:225],v2 offset:512
v_mfma_f32_16x16x32_bf16 v[204:207],a[152:155],v[104:107],v[204:207]
s_or_b32 s47,s47,s46
ds_read_b64_tr_b16 a[226:227],v2 offset:4608
v_cvt_pk_bf16_f32 v142,v156,v157
v_mfma_f32_16x16x32_bf16 v[204:207],a[148:151],v[100:103],v[204:207]
ds_read_b64_tr_b16 a[228:229],v2 offset:8704
v_mfma_f32_16x16x32_bf16 v[204:207],a[144:147],v[96:99],v[204:207]
ds_read_b64_tr_b16 a[230:231],v2 offset:12800
v_mfma_f32_16x16x32_bf16 v[216:219],a[160:163],v[96:99],v[40:43]
ds_read_b64_tr_b16 a[236:237],v3 offset:8704
v_cvt_pk_bf16_f32 v143,v158,v159
v_mfma_f32_16x16x32_bf16 v[216:219],a[164:167],v[100:103],v[216:219]
ds_read_b64_tr_b16 a[238:239],v3 offset:12800
v_mfma_f32_16x16x32_bf16 v[216:219],a[168:171],v[104:107],v[216:219]
ds_read_b64_tr_b16 a[232:233],v3 offset:512
v_mfma_f32_16x16x32_bf16 v[216:219],a[172:175],v[108:111],v[216:219]
ds_read_b64_tr_b16 a[234:235],v3 offset:4608
v_mfma_f32_16x16x32_bf16 v[220:223],a[188:191],v[108:111],v[40:43]
ds_read_b64_tr_b16 a[240:241],v2 offset:768
v_mfma_f32_16x16x32_bf16 v[220:223],a[184:187],v[104:107],v[220:223]
ds_read_b64_tr_b16 a[242:243],v2 offset:4864
v_mfma_f32_16x16x32_bf16 v[220:223],a[180:183],v[100:103],v[220:223]
ds_read_b64_tr_b16 a[244:245],v2 offset:8960
v_mfma_f32_16x16x32_bf16 v[220:223],a[176:179],v[96:99],v[220:223]
s_waitcnt lgkmcnt(7)
buffer_load_dword v26,v24,s[88:91],s93 offen
v_mfma_f32_16x16x32_bf16 a[0:3],a[192:195],v[136:139],a[0:3]
ds_read_b64_tr_b16 a[246:247],v2 offset:13056
v_mfma_f32_16x16x32_bf16 a[0:3],a[196:199],v[140:143],a[0:3]
ds_read_b64_tr_b16 a[252:253],v3 offset:8960
v_mfma_f32_16x16x32_bf16 a[4:7],a[204:207],v[140:143],a[4:7]
ds_read_b64_tr_b16 a[254:255],v3 offset:13056
v_mfma_f32_16x16x32_bf16 a[4:7],a[200:203],v[136:139],a[4:7]
ds_read_b64_tr_b16 a[248:249],v3 offset:768
v_mfma_f32_16x16x32_bf16 a[8:11],a[208:211],v[136:139],a[8:11]
ds_read_b64_tr_b16 a[250:251],v3 offset:4864
v_mfma_f32_16x16x32_bf16 a[8:11],a[212:215],v[140:143],a[8:11]
v_mfma_f32_16x16x32_bf16 a[12:15],a[220:223],v[140:143],a[12:15]
v_mfma_f32_16x16x32_bf16 a[12:15],a[216:219],v[136:139],a[12:15]
s_bitcmp1_b32 s45,0
s_cbranch_scc0 L_t31v10_%=
s_mov_b32 m0,s80
s_add_u32 s80,s80,4096
buffer_load_dwordx4 v25,s[20:23],s83 offen nt lds
s_add_u32 s83,s83,4096
L_t31v10_%=:
v_mfma_f32_16x16x32_bf16 a[16:19],a[224:227],v[136:139],a[16:19]
v_mfma_f32_16x16x32_bf16 a[16:19],a[228:231],v[140:143],a[16:19]
s_waitcnt lgkmcnt(3)
v_mfma_f32_16x16x32_bf16 a[20:23],a[236:239],v[140:143],a[20:23]
v_mfma_f32_16x16x32_bf16 a[20:23],a[232:235],v[136:139],a[20:23]
v_mfma_f32_16x16x32_bf16 a[24:27],a[240:243],v[136:139],a[24:27]
v_mfma_f32_16x16x32_bf16 a[24:27],a[244:247],v[140:143],a[24:27]
s_waitcnt lgkmcnt(0)
v_mfma_f32_16x16x32_bf16 a[28:31],a[252:255],v[140:143],a[28:31]
v_mfma_f32_16x16x32_bf16 a[28:31],a[248:251],v[136:139],a[28:31]
s_cmp_lg_u32 s53,0
v_smfmac_f32_16x16x64_bf16 v[48:51],v[28:31],v[136:143],v27
s_cbranch_scc1 L_att0v1_%=
s_waitcnt lgkmcnt(0)
s_nop 7
s_branch L_att0x_%=
.p2align 5
L_att0v2_%=:
v_mfma_f32_16x16x32_bf16 v[232:235],a[128:131],v[112:115],v[44:47]
s_waitcnt vmcnt(8) lgkmcnt(0)
v_mfma_f32_16x16x32_bf16 v[232:235],a[132:135],v[116:119],v[232:235]
s_barrier
s_add_u32 m0,s49,s51
ds_read_b128 a[128:131],v1
v_mfma_f32_16x16x32_bf16 v[232:235],a[136:139],v[120:123],v[232:235]
s_mov_b32 s90,L_endseg_%=-L_pc_%=+128
v_mfma_f32_16x16x32_bf16 v[232:235],a[140:143],v[124:127],v[232:235]
s_lshr_b32 s45,s45,1
ds_read_b128 a[132:135],v1 offset:256
v_mfma_f32_16x16x32_bf16 v[236:239],a[156:159],v[124:127],v[44:47]
buffer_load_dwordx4 v4,s[20:23],s47 offen nt lds
v_mfma_f32_16x16x32_bf16 v[236:239],a[152:155],v[120:123],v[236:239]
ds_read_b128 a[136:139],v1 offset:512
v_add_u32 v2,s48,v2
v_mfma_f32_16x16x32_bf16 v[236:239],a[148:151],v[116:119],v[236:239]
v_mfma_f32_16x16x32_bf16 v[236:239],a[144:147],v[112:115],v[236:239]
s_add_u32 s62,s50,32768
ds_read_b128 a[140:143],v1 offset:768
v_add_u32 v3,s48,v3
v_mfma_f32_16x16x32_bf16 v[248:251],a[160:163],v[112:115],v[44:47]
s_and_b32 s62,s62,131071
v_mfma_f32_16x16x32_bf16 v[248:251],a[164:167],v[116:119],v[248:251]
s_add_u32 s54,s54,16384
ds_read_b128 a[156:159],v1 offset:4864
v_mfma_f32_16x16x32_bf16 v[248:251],a[168:171],v[120:123],v[248:251]
s_sub_u32 s48,s62,s50
v_mfma_f32_16x16x32_bf16 v[248:251],a[172:175],v[124:127],v[248:251]
s_sub_u32 s53,s53,1
v_mfma_f32_16x16x32_bf16 v[252:255],a[188:191],v[124:127],v[44:47]
s_and_b32 s46,s54,0xfffc0000
ds_read_b128 a[152:155],v1 offset:4608
v_mfma_f32_16x16x32_bf16 v[252:255],a[184:187],v[120:123],v[252:255]
s_mov_b32 s50,s62
v_mfma_f32_16x16x32_bf16 v[252:255],a[180:183],v[116:119],v[252:255]
ds_read_b128 a[148:151],v1 offset:4352
v_mfma_f32_16x16x32_bf16 v[252:255],a[176:179],v[112:115],v[252:255]
v_mfma_f32_16x16x32_bf16 a[32:35],a[192:195],v[168:171],a[32:35]
ds_read_b128 a[144:147],v1 offset:4096
v_mfma_f32_16x16x32_bf16 a[32:35],a[196:199],v[172:175],a[32:35]
v_mfma_f32_16x16x32_bf16 a[36:39],a[204:207],v[172:175],a[36:39]
buffer_load_dwordx4 v4,s[20:23],s47 offen offset:1024 nt lds
v_mfma_f32_16x16x32_bf16 a[36:39],a[200:203],v[168:171],a[36:39]
ds_read_b128 a[160:163],v1 offset:8192
v_mfma_f32_16x16x32_bf16 a[40:43],a[208:211],v[168:171],a[40:43]
v_mfma_f32_16x16x32_bf16 a[40:43],a[212:215],v[172:175],a[40:43]
ds_read_b128 a[164:167],v1 offset:8448
v_mfma_f32_16x16x32_bf16 a[44:47],a[220:223],v[172:175],a[44:47]
v_mfma_f32_16x16x32_bf16 a[44:47],a[216:219],v[168:171],a[44:47]
ds_read_b128 a[168:171],v1 offset:8704
v_mfma_f32_16x16x32_bf16 a[48:51],a[224:227],v[168:171],a[48:51]
v_mfma_f32_16x16x32_bf16 a[48:51],a[228:231],v[172:175],a[48:51]
ds_read_b128 a[172:175],v1 offset:8960
v_mfma_f32_16x16x32_bf16 a[52:55],a[236:239],v[172:175],a[52:55]
v_mfma_f32_16x16x32_bf16 a[52:55],a[232:235],v[168:171],a[52:55]
ds_read_b128 a[188:191],v1 offset:13056
v_mfma_f32_16x16x32_bf16 a[56:59],a[240:243],v[168:171],a[56:59]
v_mfma_f32_16x16x32_bf16 a[56:59],a[244:247],v[172:175],a[56:59]
ds_read_b128 a[184:187],v1 offset:12800
v_mfma_f32_16x16x32_bf16 a[60:63],a[252:255],v[172:175],a[60:63]
v_mfma_f32_16x16x32_bf16 a[60:63],a[248:251],v[168:171],a[60:63]
ds_read_b128 a[180:183],v1 offset:12544
v_smfmac_f32_16x16x64_bf16 v[52:55],v[28:31],v[168:175],v27
s_waitcnt lgkmcnt(4)
v_mfma_f32_16x16x32_bf16 v[136:139],a[128:131],v[64:67],v[32:35]
buffer_load_dwordx4 v5,s[20:23],s47 offen offset:2048 nt lds
v_mfma_f32_16x16x32_bf16 v[136:139],a[132:135],v[68:71],v[136:139]
ds_read_b128 a[176:179],v1 offset:12288
v_add_u32 v1,s48,v1
v_mfma_f32_16x16x32_bf16 v[136:139],a[136:139],v[72:75],v[136:139]
v_mfma_f32_16x16x32_bf16 v[136:139],a[140:143],v[76:79],v[136:139]
v_mfma_f32_16x16x32_bf16 v[140:143],a[156:159],v[76:79],v[32:35]
v_exp_f32 v232,v232
v_mfma_f32_16x16x32_bf16 v[140:143],a[152:155],v[72:75],v[140:143]
v_exp_f32 v233,v233
v_mfma_f32_16x16x32_bf16 v[140:143],a[148:151],v[68:71],v[140:143]
v_exp_f32 v234,v234
v_mfma_f32_16x16x32_bf16 v[140:143],a[144:147],v[64:67],v[140:143]
v_exp_f32 v235,v235
)A"
R"A(v_mfma_f32_16x16x32_bf16 v[152:155],a[160:163],v[64:67],v[32:35]
v_exp_f32 v236,v236
v_mfma_f32_16x16x32_bf16 v[152:155],a[164:167],v[68:71],v[152:155]
v_exp_f32 v237,v237
v_mfma_f32_16x16x32_bf16 v[152:155],a[168:171],v[72:75],v[152:155]
s_waitcnt lgkmcnt(0)
v_exp_f32 v238,v238
v_mfma_f32_16x16x32_bf16 v[152:155],a[172:175],v[76:79],v[152:155]
v_exp_f32 v239,v239
v_mfma_f32_16x16x32_bf16 v[156:159],a[188:191],v[76:79],v[32:35]
v_exp_f32 v248,v248
v_mfma_f32_16x16x32_bf16 v[156:159],a[184:187],v[72:75],v[156:159]
buffer_load_dwordx4 v5,s[20:23],s47 offen offset:3072 nt lds
v_mfma_f32_16x16x32_bf16 v[156:159],a[180:183],v[68:71],v[156:159]
v_exp_f32 v249,v249
v_mfma_f32_16x16x32_bf16 v[156:159],a[176:179],v[64:67],v[156:159]
v_exp_f32 v250,v250
v_exp_f32 v251,v251
v_exp_f32 v252,v252
v_exp_f32 v253,v253
v_exp_f32 v254,v254
v_exp_f32 v255,v255
v_exp_f32 v136,v136
v_exp_f32 v137,v137
v_exp_f32 v138,v138
s_add_u32 m0,s49,s52
v_cvt_pk_bf16_f32 v232,v232,v233
v_cvt_pk_bf16_f32 v233,v234,v235
s_xor_b32 s49,s50,65536
v_cvt_pk_bf16_f32 v234,v236,v237
v_cvt_pk_bf16_f32 v235,v238,v239
v_cvt_pk_bf16_f32 v236,v248,v249
v_cvt_pk_bf16_f32 v237,v250,v251
buffer_load_dwordx4 v4,s[24:27],s47 offen nt lds
v_cvt_pk_bf16_f32 v238,v252,v253
v_cvt_pk_bf16_f32 v239,v254,v255
v_exp_f32 v139,v139
v_exp_f32 v140,v140
v_exp_f32 v141,v141
v_mfma_f32_16x16x32_bf16 v[168:171],a[128:131],v[80:83],v[36:39]
v_exp_f32 v142,v142
v_mfma_f32_16x16x32_bf16 v[168:171],a[132:135],v[84:87],v[168:171]
v_exp_f32 v143,v143
v_mfma_f32_16x16x32_bf16 v[168:171],a[136:139],v[88:91],v[168:171]
v_exp_f32 v152,v152
v_mfma_f32_16x16x32_bf16 v[168:171],a[140:143],v[92:95],v[168:171]
v_exp_f32 v153,v153
v_mfma_f32_16x16x32_bf16 v[172:175],a[156:159],v[92:95],v[36:39]
v_exp_f32 v154,v154
v_mfma_f32_16x16x32_bf16 v[172:175],a[152:155],v[88:91],v[172:175]
v_exp_f32 v155,v155
v_mfma_f32_16x16x32_bf16 v[172:175],a[148:151],v[84:87],v[172:175]
v_exp_f32 v156,v156
v_mfma_f32_16x16x32_bf16 v[172:175],a[144:147],v[80:83],v[172:175]
v_exp_f32 v157,v157
v_mfma_f32_16x16x32_bf16 v[184:187],a[160:163],v[80:83],v[36:39]
buffer_load_dwordx4 v5,s[24:27],s47 offen offset:1024 nt lds
v_mfma_f32_16x16x32_bf16 v[184:187],a[164:167],v[84:87],v[184:187]
v_exp_f32 v158,v158
v_mfma_f32_16x16x32_bf16 v[184:187],a[168:171],v[88:91],v[184:187]
v_exp_f32 v159,v159
v_mfma_f32_16x16x32_bf16 v[184:187],a[172:175],v[92:95],v[184:187]
v_exp_f32 v168,v168
v_mfma_f32_16x16x32_bf16 v[188:191],a[188:191],v[92:95],v[36:39]
v_exp_f32 v169,v169
v_mfma_f32_16x16x32_bf16 v[188:191],a[184:187],v[88:91],v[188:191]
v_exp_f32 v170,v170
v_mfma_f32_16x16x32_bf16 v[188:191],a[180:183],v[84:87],v[188:191]
v_exp_f32 v171,v171
v_mfma_f32_16x16x32_bf16 v[188:191],a[176:179],v[80:83],v[188:191]
v_exp_f32 v172,v172
v_mfma_f32_16x16x32_bf16 a[96:99],a[192:195],v[232:235],a[96:99]
ds_read_b64_tr_b16 a[192:193],v2
v_cvt_pk_bf16_f32 v168,v168,v169
v_mfma_f32_16x16x32_bf16 a[96:99],a[196:199],v[236:239],a[96:99]
ds_read_b64_tr_b16 a[194:195],v2 offset:4096
v_cvt_pk_bf16_f32 v169,v170,v171
v_mfma_f32_16x16x32_bf16 a[100:103],a[204:207],v[236:239],a[100:103]
v_exp_f32 v173,v173
v_mfma_f32_16x16x32_bf16 a[100:103],a[200:203],v[232:235],a[100:103]
ds_read_b64_tr_b16 a[196:197],v2 offset:8192
v_cvt_pk_bf16_f32 v170,v172,v173
v_mfma_f32_16x16x32_bf16 a[104:107],a[208:211],v[232:235],a[104:107]
ds_read_b64_tr_b16 a[198:199],v2 offset:12288
v_cvt_pk_bf16_f32 v136,v136,v137
v_mfma_f32_16x16x32_bf16 a[104:107],a[212:215],v[236:239],a[104:107]
v_exp_f32 v174,v174
v_mfma_f32_16x16x32_bf16 a[108:111],a[220:223],v[236:239],a[108:111]
buffer_load_dwordx4 v4,s[24:27],s47 offen offset:2048 nt lds
v_mfma_f32_16x16x32_bf16 a[108:111],a[216:219],v[232:235],a[108:111]
ds_read_b64_tr_b16 a[204:205],v3 offset:8192
v_cvt_pk_bf16_f32 v137,v138,v139
v_mfma_f32_16x16x32_bf16 a[112:115],a[224:227],v[232:235],a[112:115]
v_exp_f32 v175,v175
v_mfma_f32_16x16x32_bf16 a[112:115],a[228:231],v[236:239],a[112:115]
ds_read_b64_tr_b16 a[206:207],v3 offset:12288
v_cvt_pk_bf16_f32 v171,v174,v175
v_mfma_f32_16x16x32_bf16 a[116:119],a[236:239],v[236:239],a[116:119]
ds_read_b64_tr_b16 a[200:201],v3
v_cvt_pk_bf16_f32 v138,v140,v141
v_mfma_f32_16x16x32_bf16 a[116:119],a[232:235],v[232:235],a[116:119]
ds_read_b64_tr_b16 a[202:203],v3 offset:4096
v_mfma_f32_16x16x32_bf16 a[120:123],a[240:243],v[232:235],a[120:123]
ds_read_b64_tr_b16 a[208:209],v2 offset:256
v_mfma_f32_16x16x32_bf16 a[120:123],a[244:247],v[236:239],a[120:123]
ds_read_b64_tr_b16 a[210:211],v2 offset:4352
v_cvt_pk_bf16_f32 v139,v142,v143
v_mfma_f32_16x16x32_bf16 a[124:127],a[252:255],v[236:239],a[124:127]
ds_read_b64_tr_b16 a[212:213],v2 offset:8448
v_mfma_f32_16x16x32_bf16 a[124:127],a[248:251],v[232:235],a[124:127]
ds_read_b64_tr_b16 a[214:215],v2 offset:12544
v_smfmac_f32_16x16x64_bf16 v[60:63],v[28:31],v[232:239],v27
s_waitcnt lgkmcnt(7)
ds_read_b64_tr_b16 a[220:221],v3 offset:8448
ds_read_b64_tr_b16 a[222:223],v3 offset:12544
v_cvt_pk_bf16_f32 v140,v152,v153
ds_read_b64_tr_b16 a[216:217],v3 offset:256
ds_read_b64_tr_b16 a[218:219],v3 offset:4352
v_cvt_pk_bf16_f32 v141,v154,v155
buffer_load_dwordx4 v5,s[24:27],s47 offen offset:3072 nt lds
s_add_u32 s47,s47,16384
s_and_b32 s47,s47,0x3ffff
ds_read_b64_tr_b16 a[224:225],v2 offset:512
s_or_b32 s47,s47,s46
ds_read_b64_tr_b16 a[226:227],v2 offset:4608
v_cvt_pk_bf16_f32 v142,v156,v157
ds_read_b64_tr_b16 a[228:229],v2 offset:8704
ds_read_b64_tr_b16 a[230:231],v2 offset:12800
ds_read_b64_tr_b16 a[236:237],v3 offset:8704
v_cvt_pk_bf16_f32 v143,v158,v159
ds_read_b64_tr_b16 a[238:239],v3 offset:12800
ds_read_b64_tr_b16 a[232:233],v3 offset:512
ds_read_b64_tr_b16 a[234:235],v3 offset:4608
ds_read_b64_tr_b16 a[240:241],v2 offset:768
ds_read_b64_tr_b16 a[242:243],v2 offset:4864
ds_read_b64_tr_b16 a[244:245],v2 offset:8960
buffer_load_dword v26,v24,s[88:91],s93 offen
v_mfma_f32_16x16x32_bf16 a[0:3],a[192:195],v[136:139],a[0:3]
ds_read_b64_tr_b16 a[246:247],v2 offset:13056
v_mfma_f32_16x16x32_bf16 a[0:3],a[196:199],v[140:143],a[0:3]
s_waitcnt lgkmcnt(15)
ds_read_b64_tr_b16 a[252:253],v3 offset:8960
v_mfma_f32_16x16x32_bf16 a[4:7],a[204:207],v[140:143],a[4:7]
ds_read_b64_tr_b16 a[254:255],v3 offset:13056
v_mfma_f32_16x16x32_bf16 a[4:7],a[200:203],v[136:139],a[4:7]
ds_read_b64_tr_b16 a[248:249],v3 offset:768
v_mfma_f32_16x16x32_bf16 a[8:11],a[208:211],v[136:139],a[8:11]
ds_read_b64_tr_b16 a[250:251],v3 offset:4864
v_mfma_f32_16x16x32_bf16 a[8:11],a[212:215],v[140:143],a[8:11]
s_waitcnt lgkmcnt(15)
v_exp_f32 v184,v184
v_mfma_f32_16x16x32_bf16 a[12:15],a[220:223],v[140:143],a[12:15]
v_exp_f32 v185,v185
v_mfma_f32_16x16x32_bf16 a[12:15],a[216:219],v[136:139],a[12:15]
s_bitcmp1_b32 s45,0
s_cbranch_scc0 L_t31v20_%=
s_mov_b32 m0,s80
s_add_u32 s80,s80,4096
buffer_load_dwordx4 v25,s[20:23],s83 offen nt lds
s_add_u32 s83,s83,4096
L_t31v20_%=:
s_waitcnt lgkmcnt(5)
v_mfma_f32_16x16x32_bf16 a[16:19],a[224:227],v[136:139],a[16:19]
v_exp_f32 v186,v186
v_mfma_f32_16x16x32_bf16 a[16:19],a[228:231],v[140:143],a[16:19]
v_exp_f32 v187,v187
v_mfma_f32_16x16x32_bf16 a[20:23],a[236:239],v[140:143],a[20:23]
v_exp_f32 v188,v188
v_mfma_f32_16x16x32_bf16 a[20:23],a[232:235],v[136:139],a[20:23]
v_exp_f32 v189,v189
v_mfma_f32_16x16x32_bf16 a[24:27],a[240:243],v[136:139],a[24:27]
s_waitcnt lgkmcnt(0)
v_exp_f32 v190,v190
v_mfma_f32_16x16x32_bf16 a[24:27],a[244:247],v[140:143],a[24:27]
v_cvt_pk_bf16_f32 v172,v184,v185
v_mfma_f32_16x16x32_bf16 a[28:31],a[252:255],v[140:143],a[28:31]
v_exp_f32 v191,v191
v_mfma_f32_16x16x32_bf16 a[28:31],a[248:251],v[136:139],a[28:31]
v_cvt_pk_bf16_f32 v173,v186,v187
v_cvt_pk_bf16_f32 v174,v188,v189
v_cvt_pk_bf16_f32 v175,v190,v191
s_cmp_lg_u32 s53,0
v_smfmac_f32_16x16x64_bf16 v[48:51],v[28:31],v[136:143],v27
s_cbranch_scc1 L_att0v2_%=
s_waitcnt lgkmcnt(0)
s_nop 7
s_branch L_att0x_%=
.p2align 5
L_att0_%=:
v_mfma_f32_16x16x32_bf16 v[232:235],a[128:131],v[112:115],v[44:47]
s_waitcnt vmcnt(8) lgkmcnt(0)
v_exp_f32 v200,v200
v_mfma_f32_16x16x32_bf16 v[232:235],a[132:135],v[116:119],v[232:235]
s_barrier
s_add_u32 m0,s49,s51
ds_read_b128 a[128:131],v1
v_mfma_f32_16x16x32_bf16 v[232:235],a[136:139],v[120:123],v[232:235]
s_mov_b32 s90,L_endseg_%=-L_pc_%=+128
v_exp_f32 v201,v201
v_mfma_f32_16x16x32_bf16 v[232:235],a[140:143],v[124:127],v[232:235]
s_lshr_b32 s45,s45,1
ds_read_b128 a[132:135],v1 offset:256
v_cvt_pk_bf16_f32 v200,v200,v201
v_mfma_f32_16x16x32_bf16 v[236:239],a[156:159],v[124:127],v[44:47]
s_sub_u32 s94,s94,3
buffer_load_dwordx4 v4,s[20:23],s47 offen nt lds
v_mfma_f32_16x16x32_bf16 v[236:239],a[152:155],v[120:123],v[236:239]
s_lshr_b32 s60,s94,2
ds_read_b128 a[136:139],v1 offset:512
v_add_u32 v2,s48,v2
v_mfma_f32_16x16x32_bf16 v[236:239],a[148:151],v[116:119],v[236:239]
s_max_i32 s60,s60,128
v_exp_f32 v202,v202
v_mfma_f32_16x16x32_bf16 v[236:239],a[144:147],v[112:115],v[236:239]
s_add_u32 s62,s50,32768
ds_read_b128 a[140:143],v1 offset:768
v_add_u32 v3,s48,v3
v_mfma_f32_16x16x32_bf16 v[248:251],a[160:163],v[112:115],v[44:47]
s_and_b32 s62,s62,131071
v_exp_f32 v203,v203
v_mfma_f32_16x16x32_bf16 v[248:251],a[164:167],v[116:119],v[248:251]
s_add_u32 s54,s54,16384
ds_read_b128 a[156:159],v1 offset:4864
v_cvt_pk_bf16_f32 v201,v202,v203
v_mfma_f32_16x16x32_bf16 v[248:251],a[168:171],v[120:123],v[248:251]
s_sub_u32 s48,s62,s50
v_exp_f32 v204,v204
v_mfma_f32_16x16x32_bf16 v[248:251],a[172:175],v[124:127],v[248:251]
s_sub_u32 s53,s53,1
v_exp_f32 v205,v205
v_mfma_f32_16x16x32_bf16 v[252:255],a[188:191],v[124:127],v[44:47]
s_and_b32 s46,s54,0xfffc0000
ds_read_b128 a[152:155],v1 offset:4608
v_cvt_pk_bf16_f32 v202,v204,v205
v_mfma_f32_16x16x32_bf16 v[252:255],a[184:187],v[120:123],v[252:255]
s_mov_b32 s50,s62
v_exp_f32 v206,v206
v_mfma_f32_16x16x32_bf16 v[252:255],a[180:183],v[116:119],v[252:255]
ds_read_b128 a[148:151],v1 offset:4352
v_mfma_f32_16x16x32_bf16 v[252:255],a[176:179],v[112:115],v[252:255]
v_exp_f32 v207,v207
v_mfma_f32_16x16x32_bf16 a[32:35],a[192:195],v[168:171],a[32:35]
ds_read_b128 a[144:147],v1 offset:4096
v_cvt_pk_bf16_f32 v203,v206,v207
v_mfma_f32_16x16x32_bf16 a[32:35],a[196:199],v[172:175],a[32:35]
v_exp_f32 v216,v216
v_mfma_f32_16x16x32_bf16 a[36:39],a[204:207],v[172:175],a[36:39]
buffer_load_dwordx4 v4,s[20:23],s47 offen offset:1024 nt lds
v_mfma_f32_16x16x32_bf16 a[36:39],a[200:203],v[168:171],a[36:39]
ds_read_b128 a[160:163],v1 offset:8192
v_mfma_f32_16x16x32_bf16 a[40:43],a[208:211],v[168:171],a[40:43]
v_exp_f32 v217,v217
v_mfma_f32_16x16x32_bf16 a[40:43],a[212:215],v[172:175],a[40:43]
ds_read_b128 a[164:167],v1 offset:8448
v_cvt_pk_bf16_f32 v204,v216,v217
v_mfma_f32_16x16x32_bf16 a[44:47],a[220:223],v[172:175],a[44:47]
v_exp_f32 v218,v218
v_mfma_f32_16x16x32_bf16 a[44:47],a[216:219],v[168:171],a[44:47]
ds_read_b128 a[168:171],v1 offset:8704
v_mfma_f32_16x16x32_bf16 a[48:51],a[224:227],v[168:171],a[48:51]
v_exp_f32 v219,v219
v_mfma_f32_16x16x32_bf16 a[48:51],a[228:231],v[172:175],a[48:51]
ds_read_b128 a[172:175],v1 offset:8960
v_cvt_pk_bf16_f32 v205,v218,v219
v_mfma_f32_16x16x32_bf16 a[52:55],a[236:239],v[172:175],a[52:55]
v_exp_f32 v220,v220
v_mfma_f32_16x16x32_bf16 a[52:55],a[232:235],v[168:171],a[52:55]
ds_read_b128 a[188:191],v1 offset:13056
v_mfma_f32_16x16x32_bf16 a[56:59],a[240:243],v[168:171],a[56:59]
v_exp_f32 v221,v221
v_mfma_f32_16x16x32_bf16 a[56:59],a[244:247],v[172:175],a[56:59]
ds_read_b128 a[184:187],v1 offset:12800
v_cvt_pk_bf16_f32 v206,v220,v221
v_mfma_f32_16x16x32_bf16 a[60:63],a[252:255],v[172:175],a[60:63]
v_exp_f32 v222,v222
v_mfma_f32_16x16x32_bf16 a[60:63],a[248:251],v[168:171],a[60:63]
ds_read_b128 a[180:183],v1 offset:12544
v_smfmac_f32_16x16x64_bf16 v[52:55],v[28:31],v[168:175],v27
s_waitcnt lgkmcnt(4)
v_mfma_f32_16x16x32_bf16 v[136:139],a[128:131],v[64:67],v[32:35]
buffer_load_dwordx4 v5,s[20:23],s47 offen offset:2048 nt lds
v_mfma_f32_16x16x32_bf16 v[136:139],a[132:135],v[68:71],v[136:139]
ds_read_b128 a[176:179],v1 offset:12288
v_add_u32 v1,s48,v1
v_mfma_f32_16x16x32_bf16 v[136:139],a[136:139],v[72:75],v[136:139]
v_exp_f32 v223,v223
v_mfma_f32_16x16x32_bf16 v[136:139],a[140:143],v[76:79],v[136:139]
)A"
R"A(v_cvt_pk_bf16_f32 v207,v222,v223
v_mfma_f32_16x16x32_bf16 v[140:143],a[156:159],v[76:79],v[32:35]
v_exp_f32 v232,v232
v_mfma_f32_16x16x32_bf16 v[140:143],a[152:155],v[72:75],v[140:143]
v_exp_f32 v233,v233
v_mfma_f32_16x16x32_bf16 v[140:143],a[148:151],v[68:71],v[140:143]
v_exp_f32 v234,v234
v_mfma_f32_16x16x32_bf16 v[140:143],a[144:147],v[64:67],v[140:143]
v_exp_f32 v235,v235
v_mfma_f32_16x16x32_bf16 v[152:155],a[160:163],v[64:67],v[32:35]
v_exp_f32 v236,v236
v_mfma_f32_16x16x32_bf16 v[152:155],a[164:167],v[68:71],v[152:155]
v_exp_f32 v237,v237
v_mfma_f32_16x16x32_bf16 v[152:155],a[168:171],v[72:75],v[152:155]
s_waitcnt lgkmcnt(0)
v_exp_f32 v238,v238
v_mfma_f32_16x16x32_bf16 v[152:155],a[172:175],v[76:79],v[152:155]
v_exp_f32 v239,v239
v_mfma_f32_16x16x32_bf16 v[156:159],a[188:191],v[76:79],v[32:35]
v_exp_f32 v248,v248
v_mfma_f32_16x16x32_bf16 v[156:159],a[184:187],v[72:75],v[156:159]
buffer_load_dwordx4 v5,s[20:23],s47 offen offset:3072 nt lds
v_mfma_f32_16x16x32_bf16 v[156:159],a[180:183],v[68:71],v[156:159]
v_exp_f32 v249,v249
v_mfma_f32_16x16x32_bf16 v[156:159],a[176:179],v[64:67],v[156:159]
v_exp_f32 v250,v250
v_mfma_f32_16x16x32_bf16 a[64:67],a[192:195],v[200:203],a[64:67]
v_exp_f32 v251,v251
v_mfma_f32_16x16x32_bf16 a[64:67],a[196:199],v[204:207],a[64:67]
v_exp_f32 v252,v252
v_mfma_f32_16x16x32_bf16 a[68:71],a[204:207],v[204:207],a[68:71]
v_exp_f32 v253,v253
v_mfma_f32_16x16x32_bf16 a[68:71],a[200:203],v[200:203],a[68:71]
v_exp_f32 v254,v254
v_mfma_f32_16x16x32_bf16 a[72:75],a[208:211],v[200:203],a[72:75]
v_exp_f32 v255,v255
v_mfma_f32_16x16x32_bf16 a[72:75],a[212:215],v[204:207],a[72:75]
v_exp_f32 v136,v136
v_mfma_f32_16x16x32_bf16 a[76:79],a[220:223],v[204:207],a[76:79]
v_exp_f32 v137,v137
v_mfma_f32_16x16x32_bf16 a[76:79],a[216:219],v[200:203],a[76:79]
v_exp_f32 v138,v138
v_mfma_f32_16x16x32_bf16 a[80:83],a[224:227],v[200:203],a[80:83]
s_add_u32 m0,s49,s52
v_cvt_pk_bf16_f32 v232,v232,v233
v_cvt_pk_bf16_f32 v233,v234,v235
v_mfma_f32_16x16x32_bf16 a[80:83],a[228:231],v[204:207],a[80:83]
s_xor_b32 s49,s50,65536
v_cvt_pk_bf16_f32 v234,v236,v237
v_cvt_pk_bf16_f32 v235,v238,v239
v_mfma_f32_16x16x32_bf16 a[84:87],a[236:239],v[204:207],a[84:87]
v_cvt_pk_bf16_f32 v236,v248,v249
v_cvt_pk_bf16_f32 v237,v250,v251
v_mfma_f32_16x16x32_bf16 a[84:87],a[232:235],v[200:203],a[84:87]
buffer_load_dwordx4 v4,s[24:27],s47 offen nt lds
v_mfma_f32_16x16x32_bf16 a[88:91],a[240:243],v[200:203],a[88:91]
v_cvt_pk_bf16_f32 v238,v252,v253
v_cvt_pk_bf16_f32 v239,v254,v255
v_mfma_f32_16x16x32_bf16 a[88:91],a[244:247],v[204:207],a[88:91]
v_exp_f32 v139,v139
v_mfma_f32_16x16x32_bf16 a[92:95],a[252:255],v[204:207],a[92:95]
v_exp_f32 v140,v140
v_mfma_f32_16x16x32_bf16 a[92:95],a[248:251],v[200:203],a[92:95]
v_exp_f32 v141,v141
v_smfmac_f32_16x16x64_bf16 v[56:59],v[28:31],v[200:207],v27
v_mfma_f32_16x16x32_bf16 v[168:171],a[128:131],v[80:83],v[36:39]
v_exp_f32 v142,v142
v_mfma_f32_16x16x32_bf16 v[168:171],a[132:135],v[84:87],v[168:171]
v_exp_f32 v143,v143
v_mfma_f32_16x16x32_bf16 v[168:171],a[136:139],v[88:91],v[168:171]
v_exp_f32 v152,v152
v_mfma_f32_16x16x32_bf16 v[168:171],a[140:143],v[92:95],v[168:171]
v_exp_f32 v153,v153
v_mfma_f32_16x16x32_bf16 v[172:175],a[156:159],v[92:95],v[36:39]
v_exp_f32 v154,v154
v_mfma_f32_16x16x32_bf16 v[172:175],a[152:155],v[88:91],v[172:175]
v_exp_f32 v155,v155
v_mfma_f32_16x16x32_bf16 v[172:175],a[148:151],v[84:87],v[172:175]
v_exp_f32 v156,v156
v_mfma_f32_16x16x32_bf16 v[172:175],a[144:147],v[80:83],v[172:175]
v_exp_f32 v157,v157
v_mfma_f32_16x16x32_bf16 v[184:187],a[160:163],v[80:83],v[36:39]
buffer_load_dwordx4 v5,s[24:27],s47 offen offset:1024 nt lds
v_mfma_f32_16x16x32_bf16 v[184:187],a[164:167],v[84:87],v[184:187]
v_exp_f32 v158,v158
v_mfma_f32_16x16x32_bf16 v[184:187],a[168:171],v[88:91],v[184:187]
v_exp_f32 v159,v159
v_mfma_f32_16x16x32_bf16 v[184:187],a[172:175],v[92:95],v[184:187]
v_exp_f32 v168,v168
v_mfma_f32_16x16x32_bf16 v[188:191],a[188:191],v[92:95],v[36:39]
v_exp_f32 v169,v169
v_mfma_f32_16x16x32_bf16 v[188:191],a[184:187],v[88:91],v[188:191]
v_exp_f32 v170,v170
v_mfma_f32_16x16x32_bf16 v[188:191],a[180:183],v[84:87],v[188:191]
v_exp_f32 v171,v171
v_mfma_f32_16x16x32_bf16 v[188:191],a[176:179],v[80:83],v[188:191]
v_exp_f32 v172,v172
v_mfma_f32_16x16x32_bf16 a[96:99],a[192:195],v[232:235],a[96:99]
ds_read_b64_tr_b16 a[192:193],v2
v_cvt_pk_bf16_f32 v168,v168,v169
v_mfma_f32_16x16x32_bf16 a[96:99],a[196:199],v[236:239],a[96:99]
ds_read_b64_tr_b16 a[194:195],v2 offset:4096
v_cvt_pk_bf16_f32 v169,v170,v171
v_mfma_f32_16x16x32_bf16 a[100:103],a[204:207],v[236:239],a[100:103]
v_exp_f32 v173,v173
v_mfma_f32_16x16x32_bf16 a[100:103],a[200:203],v[232:235],a[100:103]
ds_read_b64_tr_b16 a[196:197],v2 offset:8192
v_cvt_pk_bf16_f32 v170,v172,v173
v_mfma_f32_16x16x32_bf16 a[104:107],a[208:211],v[232:235],a[104:107]
ds_read_b64_tr_b16 a[198:199],v2 offset:12288
v_cvt_pk_bf16_f32 v136,v136,v137
v_mfma_f32_16x16x32_bf16 a[104:107],a[212:215],v[236:239],a[104:107]
v_exp_f32 v174,v174
v_mfma_f32_16x16x32_bf16 a[108:111],a[220:223],v[236:239],a[108:111]
buffer_load_dwordx4 v4,s[24:27],s47 offen offset:2048 nt lds
v_mfma_f32_16x16x32_bf16 a[108:111],a[216:219],v[232:235],a[108:111]
ds_read_b64_tr_b16 a[204:205],v3 offset:8192
v_cvt_pk_bf16_f32 v137,v138,v139
v_mfma_f32_16x16x32_bf16 a[112:115],a[224:227],v[232:235],a[112:115]
v_exp_f32 v175,v175
v_mfma_f32_16x16x32_bf16 a[112:115],a[228:231],v[236:239],a[112:115]
ds_read_b64_tr_b16 a[206:207],v3 offset:12288
v_cvt_pk_bf16_f32 v171,v174,v175
v_mfma_f32_16x16x32_bf16 a[116:119],a[236:239],v[236:239],a[116:119]
ds_read_b64_tr_b16 a[200:201],v3
v_cvt_pk_bf16_f32 v138,v140,v141
v_mfma_f32_16x16x32_bf16 a[116:119],a[232:235],v[232:235],a[116:119]
ds_read_b64_tr_b16 a[202:203],v3 offset:4096
v_mfma_f32_16x16x32_bf16 a[120:123],a[240:243],v[232:235],a[120:123]
ds_read_b64_tr_b16 a[208:209],v2 offset:256
v_mfma_f32_16x16x32_bf16 a[120:123],a[244:247],v[236:239],a[120:123]
ds_read_b64_tr_b16 a[210:211],v2 offset:4352
v_cvt_pk_bf16_f32 v139,v142,v143
v_mfma_f32_16x16x32_bf16 a[124:127],a[252:255],v[236:239],a[124:127]
ds_read_b64_tr_b16 a[212:213],v2 offset:8448
v_mfma_f32_16x16x32_bf16 a[124:127],a[248:251],v[232:235],a[124:127]
ds_read_b64_tr_b16 a[214:215],v2 offset:12544
v_smfmac_f32_16x16x64_bf16 v[60:63],v[28:31],v[232:239],v27
ds_read_b64_tr_b16 a[220:221],v3 offset:8448
v_mfma_f32_16x16x32_bf16 v[200:203],a[128:131],v[96:99],v[40:43]
ds_read_b64_tr_b16 a[222:223],v3 offset:12544
v_cvt_pk_bf16_f32 v140,v152,v153
v_mfma_f32_16x16x32_bf16 v[200:203],a[132:135],v[100:103],v[200:203]
ds_read_b64_tr_b16 a[216:217],v3 offset:256
v_mfma_f32_16x16x32_bf16 v[200:203],a[136:139],v[104:107],v[200:203]
ds_read_b64_tr_b16 a[218:219],v3 offset:4352
v_cvt_pk_bf16_f32 v141,v154,v155
v_mfma_f32_16x16x32_bf16 v[200:203],a[140:143],v[108:111],v[200:203]
buffer_load_dwordx4 v5,s[24:27],s47 offen offset:3072 nt lds
v_mfma_f32_16x16x32_bf16 v[204:207],a[156:159],v[108:111],v[40:43]
s_add_u32 s47,s47,16384
s_and_b32 s47,s47,0x3ffff
ds_read_b64_tr_b16 a[224:225],v2 offset:512
v_mfma_f32_16x16x32_bf16 v[204:207],a[152:155],v[104:107],v[204:207]
s_or_b32 s47,s47,s46
ds_read_b64_tr_b16 a[226:227],v2 offset:4608
v_cvt_pk_bf16_f32 v142,v156,v157
v_mfma_f32_16x16x32_bf16 v[204:207],a[148:151],v[100:103],v[204:207]
ds_read_b64_tr_b16 a[228:229],v2 offset:8704
v_mfma_f32_16x16x32_bf16 v[204:207],a[144:147],v[96:99],v[204:207]
ds_read_b64_tr_b16 a[230:231],v2 offset:12800
v_mfma_f32_16x16x32_bf16 v[216:219],a[160:163],v[96:99],v[40:43]
ds_read_b64_tr_b16 a[236:237],v3 offset:8704
v_cvt_pk_bf16_f32 v143,v158,v159
v_mfma_f32_16x16x32_bf16 v[216:219],a[164:167],v[100:103],v[216:219]
ds_read_b64_tr_b16 a[238:239],v3 offset:12800
v_mfma_f32_16x16x32_bf16 v[216:219],a[168:171],v[104:107],v[216:219]
ds_read_b64_tr_b16 a[232:233],v3 offset:512
v_mfma_f32_16x16x32_bf16 v[216:219],a[172:175],v[108:111],v[216:219]
ds_read_b64_tr_b16 a[234:235],v3 offset:4608
v_mfma_f32_16x16x32_bf16 v[220:223],a[188:191],v[108:111],v[40:43]
ds_read_b64_tr_b16 a[240:241],v2 offset:768
v_mfma_f32_16x16x32_bf16 v[220:223],a[184:187],v[104:107],v[220:223]
ds_read_b64_tr_b16 a[242:243],v2 offset:4864
v_mfma_f32_16x16x32_bf16 v[220:223],a[180:183],v[100:103],v[220:223]
ds_read_b64_tr_b16 a[244:245],v2 offset:8960
v_mfma_f32_16x16x32_bf16 v[220:223],a[176:179],v[96:99],v[220:223]
s_waitcnt lgkmcnt(7)
buffer_load_dword v26,v24,s[88:91],s93 offen
v_mfma_f32_16x16x32_bf16 a[0:3],a[192:195],v[136:139],a[0:3]
ds_read_b64_tr_b16 a[246:247],v2 offset:13056
v_mfma_f32_16x16x32_bf16 a[0:3],a[196:199],v[140:143],a[0:3]
ds_read_b64_tr_b16 a[252:253],v3 offset:8960
v_mfma_f32_16x16x32_bf16 a[4:7],a[204:207],v[140:143],a[4:7]
ds_read_b64_tr_b16 a[254:255],v3 offset:13056
v_mfma_f32_16x16x32_bf16 a[4:7],a[200:203],v[136:139],a[4:7]
s_memrealtime s[76:77]
ds_read_b64_tr_b16 a[248:249],v3 offset:768
v_mfma_f32_16x16x32_bf16 a[8:11],a[208:211],v[136:139],a[8:11]
ds_read_b64_tr_b16 a[250:251],v3 offset:4864
v_mfma_f32_16x16x32_bf16 a[8:11],a[212:215],v[140:143],a[8:11]
v_exp_f32 v184,v184
v_mfma_f32_16x16x32_bf16 a[12:15],a[220:223],v[140:143],a[12:15]
v_exp_f32 v185,v185
v_mfma_f32_16x16x32_bf16 a[12:15],a[216:219],v[136:139],a[12:15]
s_bitcmp1_b32 s45,0
s_cbranch_scc0 L_t310_%=
s_mov_b32 m0,s80
s_add_u32 s80,s80,4096
buffer_load_dwordx4 v25,s[20:23],s83 offen nt lds
s_add_u32 s83,s83,4096
L_t310_%=:
v_mfma_f32_16x16x32_bf16 a[16:19],a[224:227],v[136:139],a[16:19]
v_exp_f32 v186,v186
v_mfma_f32_16x16x32_bf16 a[16:19],a[228:231],v[140:143],a[16:19]
s_waitcnt lgkmcnt(3)
v_exp_f32 v187,v187
v_mfma_f32_16x16x32_bf16 a[20:23],a[236:239],v[140:143],a[20:23]
s_waitcnt lgkmcnt(0)
v_exp_f32 v188,v188
v_mfma_f32_16x16x32_bf16 a[20:23],a[232:235],v[136:139],a[20:23]
s_sub_u32 s79,s78,s76
v_exp_f32 v189,v189
v_mfma_f32_16x16x32_bf16 a[24:27],a[240:243],v[136:139],a[24:27]
s_add_u32 s78,s76,s60
v_exp_f32 v190,v190
v_mfma_f32_16x16x32_bf16 a[24:27],a[244:247],v[140:143],a[24:27]
s_cmp_gt_i32 s79,s95
s_cbranch_scc1 L_ps3_%=
L_pr3_%=:
v_cvt_pk_bf16_f32 v172,v184,v185
v_mfma_f32_16x16x32_bf16 a[28:31],a[252:255],v[140:143],a[28:31]
v_exp_f32 v191,v191
v_mfma_f32_16x16x32_bf16 a[28:31],a[248:251],v[136:139],a[28:31]
v_cvt_pk_bf16_f32 v173,v186,v187
v_cvt_pk_bf16_f32 v174,v188,v189
v_cvt_pk_bf16_f32 v175,v190,v191
s_cmp_lg_u32 s53,0
v_smfmac_f32_16x16x64_bf16 v[48:51],v[28:31],v[136:143],v27
s_cbranch_scc1 L_att0_%=
L_att0x_%=:
v_mfma_f32_16x16x32_bf16 v[232:235],a[128:131],v[112:115],v[44:47]
s_waitcnt vmcnt(10)
s_sub_u32 s94,s94,3
v_exp_f32 v200,v200
v_mfma_f32_16x16x32_bf16 v[232:235],a[132:135],v[116:119],v[232:235]
s_barrier
s_add_u32 m0,0,s51
ds_read_b128 a[128:131],v1
v_mfma_f32_16x16x32_bf16 v[232:235],a[136:139],v[120:123],v[232:235]
s_lshr_b32 s60,s94,2
ds_read_b128 a[132:135],v1 offset:256
v_and_b32 v18,48,v6
v_mfma_f32_16x16x32_bf16 v[232:235],a[140:143],v[124:127],v[232:235]
s_max_i32 s60,s60,128
ds_read_b128 a[136:139],v1 offset:512
v_lshrrev_b32 v19,4,v6
v_mfma_f32_16x16x32_bf16 v[236:239],a[156:159],v[124:127],v[44:47]
s_add_u32 s54,s54,16384
buffer_load_dwordx4 v4,s[20:23],s47 offen nt lds
v_mfma_f32_16x16x32_bf16 v[236:239],a[152:155],v[120:123],v[236:239]
s_and_b32 s46,s54,0xfffc0000
ds_read_b128 a[140:143],v1 offset:768
v_lshlrev_b32 v18,2,v18
v_mfma_f32_16x16x32_bf16 v[236:239],a[148:151],v[116:119],v[236:239]
s_sub_u32 s94,s94,3
ds_read_b128 a[156:159],v1 offset:4864
v_and_b32 v19,48,v19
v_mfma_f32_16x16x32_bf16 v[236:239],a[144:147],v[112:115],v[236:239]
s_add_u32 s54,s54,16384
ds_read_b128 a[152:155],v1 offset:4608
v_add_u32 v18,v18,v19
v_mfma_f32_16x16x32_bf16 v[248:251],a[160:163],v[112:115],v[44:47]
s_mul_i32 s83,s41,5
ds_read_b128 a[148:151],v1 offset:4352
v_add_u32 v18,98304,v18
v_mfma_f32_16x16x32_bf16 v[248:251],a[164:167],v[116:119],v[248:251]
s_lshl_b32 s44,s40,5
ds_read_b128 a[144:147],v1 offset:4096
v_add_u32 v2,s48,v2
v_mfma_f32_16x16x32_bf16 v[248:251],a[168:171],v[120:123],v[248:251]
s_add_u32 s83,s83,s42
ds_read_b128 a[160:163],v1 offset:8192
v_add_u32 v3,s48,v3
v_mfma_f32_16x16x32_bf16 v[248:251],a[172:175],v[124:127],v[248:251]
)A"
R"A(s_mov_b32 s48,32768
s_add_u32 s84,s55,0
ds_read_b128 a[164:167],v1 offset:8448
v_mfma_f32_16x16x32_bf16 v[252:255],a[188:191],v[124:127],v[44:47]
s_add_u32 s85,s55,4096
s_add_u32 s86,s55,8192
ds_read_b128 a[168:171],v1 offset:8704
v_mfma_f32_16x16x32_bf16 v[252:255],a[184:187],v[120:123],v[252:255]
s_add_u32 s87,s55,12288
s_and_b32 s44,s44,32
ds_read_b128 a[172:175],v1 offset:8960
v_mfma_f32_16x16x32_bf16 v[252:255],a[180:183],v[116:119],v[252:255]
s_add_u32 s83,s83,15
ds_read_b128 a[188:191],v1 offset:13056
v_subrev_u32 v25,s51,v4
v_mfma_f32_16x16x32_bf16 v[252:255],a[176:179],v[112:115],v[252:255]
s_max_i32 s84,s84,s18
s_max_i32 s85,s85,s19
ds_read_b128 a[184:187],v1 offset:12800
v_mfma_f32_16x16x32_bf16 a[32:35],a[192:195],v[168:171],a[32:35]
s_max_i32 s86,s86,s100
s_max_i32 s87,s87,s101
ds_read_b128 a[180:183],v1 offset:12544
v_mfma_f32_16x16x32_bf16 a[32:35],a[196:199],v[172:175],a[32:35]
s_lshl_b32 s80,s40,10
ds_read_b128 a[176:179],v1 offset:12288
v_add_u32 v1,s48,v1
v_mfma_f32_16x16x32_bf16 a[36:39],a[204:207],v[172:175],a[36:39]
s_and_b32 s83,s83,15
buffer_load_dwordx4 v4,s[20:23],s47 offen offset:1024 nt lds
v_mfma_f32_16x16x32_bf16 a[36:39],a[200:203],v[168:171],a[36:39]
s_add_u32 s83,s83,16
v_exp_f32 v201,v201
v_mfma_f32_16x16x32_bf16 a[40:43],a[208:211],v[168:171],a[40:43]
s_mov_b32 s50,32768
v_exp_f32 v202,v202
v_mfma_f32_16x16x32_bf16 a[40:43],a[212:215],v[172:175],a[40:43]
s_mov_b32 s49,98304
v_exp_f32 v203,v203
v_mfma_f32_16x16x32_bf16 a[44:47],a[220:223],v[172:175],a[44:47]
s_mov_b32 s45,0x2a8
v_cvt_pk_bf16_f32 v200,v200,v201
v_cvt_pk_bf16_f32 v201,v202,v203
v_mfma_f32_16x16x32_bf16 a[44:47],a[216:219],v[168:171],a[44:47]
s_lshl_b32 s83,s83,14
v_exp_f32 v204,v204
v_mfma_f32_16x16x32_bf16 a[48:51],a[224:227],v[168:171],a[48:51]
s_add_u32 s93,s92,L_endseg_%=-L_pc_%=
v_exp_f32 v205,v205
v_mfma_f32_16x16x32_bf16 a[48:51],a[228:231],v[172:175],a[48:51]
v_exp_f32 v206,v206
v_mfma_f32_16x16x32_bf16 a[52:55],a[236:239],v[172:175],a[52:55]
v_exp_f32 v207,v207
v_mfma_f32_16x16x32_bf16 a[52:55],a[232:235],v[168:171],a[52:55]
v_cvt_pk_bf16_f32 v202,v204,v205
v_cvt_pk_bf16_f32 v203,v206,v207
v_mfma_f32_16x16x32_bf16 a[56:59],a[240:243],v[168:171],a[56:59]
v_exp_f32 v216,v216
v_mfma_f32_16x16x32_bf16 a[56:59],a[244:247],v[172:175],a[56:59]
v_exp_f32 v217,v217
v_mfma_f32_16x16x32_bf16 a[60:63],a[252:255],v[172:175],a[60:63]
v_exp_f32 v218,v218
v_mfma_f32_16x16x32_bf16 a[60:63],a[248:251],v[168:171],a[60:63]
v_exp_f32 v219,v219
v_smfmac_f32_16x16x64_bf16 v[52:55],v[28:31],v[168:175],v27
s_waitcnt lgkmcnt(0)
v_cvt_pk_bf16_f32 v204,v216,v217
v_mfma_f32_16x16x32_bf16 v[136:139],a[128:131],v[64:67],v[32:35]
buffer_load_dwordx4 v5,s[20:23],s47 offen offset:2048 nt lds
v_mfma_f32_16x16x32_bf16 v[136:139],a[132:135],v[68:71],v[136:139]
v_exp_f32 v220,v220
v_mfma_f32_16x16x32_bf16 v[136:139],a[136:139],v[72:75],v[136:139]
v_exp_f32 v221,v221
v_mfma_f32_16x16x32_bf16 v[136:139],a[140:143],v[76:79],v[136:139]
v_exp_f32 v222,v222
v_mfma_f32_16x16x32_bf16 v[140:143],a[156:159],v[76:79],v[32:35]
v_exp_f32 v223,v223
v_mfma_f32_16x16x32_bf16 v[140:143],a[152:155],v[72:75],v[140:143]
v_cvt_pk_bf16_f32 v205,v218,v219
v_cvt_pk_bf16_f32 v206,v220,v221
v_mfma_f32_16x16x32_bf16 v[140:143],a[148:151],v[68:71],v[140:143]
v_cvt_pk_bf16_f32 v207,v222,v223
v_xor_b32 v25,s44,v25
v_mfma_f32_16x16x32_bf16 v[140:143],a[144:147],v[64:67],v[140:143]
v_exp_f32 v232,v232
v_mfma_f32_16x16x32_bf16 v[152:155],a[160:163],v[64:67],v[32:35]
v_exp_f32 v233,v233
v_mfma_f32_16x16x32_bf16 v[152:155],a[164:167],v[68:71],v[152:155]
v_exp_f32 v234,v234
v_mfma_f32_16x16x32_bf16 v[152:155],a[168:171],v[72:75],v[152:155]
v_exp_f32 v235,v235
v_mfma_f32_16x16x32_bf16 v[152:155],a[172:175],v[76:79],v[152:155]
v_cvt_pk_bf16_f32 v232,v232,v233
v_cvt_pk_bf16_f32 v233,v234,v235
v_mfma_f32_16x16x32_bf16 v[156:159],a[188:191],v[76:79],v[32:35]
v_exp_f32 v236,v236
v_mfma_f32_16x16x32_bf16 v[156:159],a[184:187],v[72:75],v[156:159]
buffer_load_dwordx4 v5,s[20:23],s47 offen offset:3072 nt lds
v_mfma_f32_16x16x32_bf16 v[156:159],a[180:183],v[68:71],v[156:159]
v_exp_f32 v237,v237
v_mfma_f32_16x16x32_bf16 v[156:159],a[176:179],v[64:67],v[156:159]
v_exp_f32 v238,v238
v_mfma_f32_16x16x32_bf16 a[64:67],a[192:195],v[200:203],a[64:67]
v_exp_f32 v239,v239
v_mfma_f32_16x16x32_bf16 a[64:67],a[196:199],v[204:207],a[64:67]
v_cvt_pk_bf16_f32 v234,v236,v237
v_cvt_pk_bf16_f32 v235,v238,v239
v_mfma_f32_16x16x32_bf16 a[68:71],a[204:207],v[204:207],a[68:71]
v_exp_f32 v248,v248
v_mfma_f32_16x16x32_bf16 a[68:71],a[200:203],v[200:203],a[68:71]
v_exp_f32 v249,v249
v_mfma_f32_16x16x32_bf16 a[72:75],a[208:211],v[200:203],a[72:75]
v_exp_f32 v250,v250
v_mfma_f32_16x16x32_bf16 a[72:75],a[212:215],v[204:207],a[72:75]
v_exp_f32 v251,v251
v_mfma_f32_16x16x32_bf16 a[76:79],a[220:223],v[204:207],a[76:79]
v_exp_f32 v252,v252
v_mfma_f32_16x16x32_bf16 a[76:79],a[216:219],v[200:203],a[76:79]
v_exp_f32 v253,v253
v_mfma_f32_16x16x32_bf16 a[80:83],a[224:227],v[200:203],a[80:83]
s_add_u32 m0,0,s52
v_exp_f32 v254,v254
v_mfma_f32_16x16x32_bf16 a[80:83],a[228:231],v[204:207],a[80:83]
v_exp_f32 v255,v255
v_mfma_f32_16x16x32_bf16 a[84:87],a[236:239],v[204:207],a[84:87]
v_cvt_pk_bf16_f32 v236,v248,v249
v_cvt_pk_bf16_f32 v237,v250,v251
v_mfma_f32_16x16x32_bf16 a[84:87],a[232:235],v[200:203],a[84:87]
buffer_load_dwordx4 v4,s[24:27],s47 offen nt lds
v_mfma_f32_16x16x32_bf16 a[88:91],a[240:243],v[200:203],a[88:91]
v_cvt_pk_bf16_f32 v238,v252,v253
v_cvt_pk_bf16_f32 v239,v254,v255
v_mfma_f32_16x16x32_bf16 a[88:91],a[244:247],v[204:207],a[88:91]
v_exp_f32 v136,v136
v_mfma_f32_16x16x32_bf16 a[92:95],a[252:255],v[204:207],a[92:95]
v_exp_f32 v137,v137
v_mfma_f32_16x16x32_bf16 a[92:95],a[248:251],v[200:203],a[92:95]
v_exp_f32 v138,v138
v_smfmac_f32_16x16x64_bf16 v[56:59],v[28:31],v[200:207],v27
v_cvt_pk_bf16_f32 v136,v136,v137
v_mfma_f32_16x16x32_bf16 v[168:171],a[128:131],v[80:83],v[36:39]
v_exp_f32 v139,v139
v_mfma_f32_16x16x32_bf16 v[168:171],a[132:135],v[84:87],v[168:171]
v_exp_f32 v140,v140
v_mfma_f32_16x16x32_bf16 v[168:171],a[136:139],v[88:91],v[168:171]
v_exp_f32 v141,v141
v_mfma_f32_16x16x32_bf16 v[168:171],a[140:143],v[92:95],v[168:171]
v_exp_f32 v142,v142
v_mfma_f32_16x16x32_bf16 v[172:175],a[156:159],v[92:95],v[36:39]
v_exp_f32 v143,v143
v_mfma_f32_16x16x32_bf16 v[172:175],a[152:155],v[88:91],v[172:175]
v_cvt_pk_bf16_f32 v137,v138,v139
v_cvt_pk_bf16_f32 v138,v140,v141
v_mfma_f32_16x16x32_bf16 v[172:175],a[148:151],v[84:87],v[172:175]
v_cvt_pk_bf16_f32 v139,v142,v143
v_add_u32 v25,s80,v25
v_mfma_f32_16x16x32_bf16 v[172:175],a[144:147],v[80:83],v[172:175]
s_add_u32 s80,s80,147456
v_exp_f32 v152,v152
v_mfma_f32_16x16x32_bf16 v[184:187],a[160:163],v[80:83],v[36:39]
buffer_load_dwordx4 v5,s[24:27],s47 offen offset:1024 nt lds
v_mfma_f32_16x16x32_bf16 v[184:187],a[164:167],v[84:87],v[184:187]
v_exp_f32 v153,v153
v_mfma_f32_16x16x32_bf16 v[184:187],a[168:171],v[88:91],v[184:187]
v_exp_f32 v154,v154
v_mfma_f32_16x16x32_bf16 v[184:187],a[172:175],v[92:95],v[184:187]
v_exp_f32 v155,v155
v_mfma_f32_16x16x32_bf16 v[188:191],a[188:191],v[92:95],v[36:39]
v_exp_f32 v156,v156
v_mfma_f32_16x16x32_bf16 v[188:191],a[184:187],v[88:91],v[188:191]
v_exp_f32 v157,v157
v_mfma_f32_16x16x32_bf16 v[188:191],a[180:183],v[84:87],v[188:191]
s_waitcnt vmcnt(14)
v_exp_f32 v158,v158
v_mfma_f32_16x16x32_bf16 v[188:191],a[176:179],v[80:83],v[188:191]
s_barrier
ds_read_b128 v[80:83],v18
v_cvt_pk_bf16_f32 v140,v152,v153
v_mfma_f32_16x16x32_bf16 a[96:99],a[192:195],v[232:235],a[96:99]
ds_read_b128 v[84:87],v18 offset:256
v_cvt_pk_bf16_f32 v141,v154,v155
v_mfma_f32_16x16x32_bf16 a[96:99],a[196:199],v[236:239],a[96:99]
ds_read_b128 v[88:91],v18 offset:512
v_cvt_pk_bf16_f32 v142,v156,v157
v_mfma_f32_16x16x32_bf16 a[100:103],a[204:207],v[236:239],a[100:103]
v_exp_f32 v159,v159
v_mfma_f32_16x16x32_bf16 a[100:103],a[200:203],v[232:235],a[100:103]
ds_read_b128 v[92:95],v18 offset:768
v_cvt_pk_bf16_f32 v143,v158,v159
v_mfma_f32_16x16x32_bf16 a[104:107],a[208:211],v[232:235],a[104:107]
ds_read_b64_tr_b16 a[192:193],v2
ds_read_b64_tr_b16 a[194:195],v2 offset:4096
v_mfma_f32_16x16x32_bf16 a[104:107],a[212:215],v[236:239],a[104:107]
buffer_load_dwordx4 v4,s[24:27],s47 offen offset:2048 nt lds
v_mfma_f32_16x16x32_bf16 a[108:111],a[220:223],v[236:239],a[108:111]
ds_read_b64_tr_b16 a[196:197],v2 offset:8192
ds_read_b64_tr_b16 a[198:199],v2 offset:12288
v_mfma_f32_16x16x32_bf16 a[108:111],a[216:219],v[232:235],a[108:111]
ds_read_b64_tr_b16 a[204:205],v3 offset:8192
ds_read_b64_tr_b16 a[206:207],v3 offset:12288
v_mfma_f32_16x16x32_bf16 a[112:115],a[224:227],v[232:235],a[112:115]
ds_read_b64_tr_b16 a[200:201],v3
ds_read_b64_tr_b16 a[202:203],v3 offset:4096
v_mfma_f32_16x16x32_bf16 a[112:115],a[228:231],v[236:239],a[112:115]
ds_read_b64_tr_b16 a[208:209],v2 offset:256
ds_read_b64_tr_b16 a[210:211],v2 offset:4352
v_mfma_f32_16x16x32_bf16 a[116:119],a[236:239],v[236:239],a[116:119]
ds_read_b64_tr_b16 a[212:213],v2 offset:8448
ds_read_b64_tr_b16 a[214:215],v2 offset:12544
v_mfma_f32_16x16x32_bf16 a[116:119],a[232:235],v[232:235],a[116:119]
ds_read_b64_tr_b16 a[220:221],v3 offset:8448
ds_read_b64_tr_b16 a[222:223],v3 offset:12544
v_mfma_f32_16x16x32_bf16 a[120:123],a[240:243],v[232:235],a[120:123]
ds_read_b64_tr_b16 a[216:217],v3 offset:256
ds_read_b64_tr_b16 a[218:219],v3 offset:4352
v_mfma_f32_16x16x32_bf16 a[120:123],a[244:247],v[236:239],a[120:123]
ds_read_b64_tr_b16 a[224:225],v2 offset:512
ds_read_b64_tr_b16 a[226:227],v2 offset:4608
v_mfma_f32_16x16x32_bf16 a[124:127],a[252:255],v[236:239],a[124:127]
ds_read_b64_tr_b16 a[228:229],v2 offset:8704
ds_read_b64_tr_b16 a[230:231],v2 offset:12800
v_mfma_f32_16x16x32_bf16 a[124:127],a[248:251],v[232:235],a[124:127]
ds_read_b64_tr_b16 a[236:237],v3 offset:8704
ds_read_b64_tr_b16 a[238:239],v3 offset:12800
v_smfmac_f32_16x16x64_bf16 v[60:63],v[28:31],v[232:239],v27
ds_read_b64_tr_b16 a[232:233],v3 offset:512
v_mfma_f32_16x16x32_bf16 v[200:203],a[128:131],v[96:99],v[40:43]
ds_read_b64_tr_b16 a[234:235],v3 offset:4608
ds_read_b64_tr_b16 a[240:241],v2 offset:768
v_mfma_f32_16x16x32_bf16 v[200:203],a[132:135],v[100:103],v[200:203]
ds_read_b64_tr_b16 a[242:243],v2 offset:4864
ds_read_b64_tr_b16 a[244:245],v2 offset:8960
v_mfma_f32_16x16x32_bf16 v[200:203],a[136:139],v[104:107],v[200:203]
buffer_load_dwordx4 v5,s[24:27],s47 offen offset:3072 nt lds
v_mfma_f32_16x16x32_bf16 v[200:203],a[140:143],v[108:111],v[200:203]
s_add_u32 s47,s47,16384
ds_read_b64_tr_b16 a[246:247],v2 offset:13056
ds_read_b64_tr_b16 a[252:253],v3 offset:8960
v_mfma_f32_16x16x32_bf16 v[204:207],a[156:159],v[108:111],v[40:43]
s_and_b32 s47,s47,0x3ffff
ds_read_b64_tr_b16 a[254:255],v3 offset:13056
ds_read_b64_tr_b16 a[248:249],v3 offset:768
v_mfma_f32_16x16x32_bf16 v[204:207],a[152:155],v[104:107],v[204:207]
s_or_b32 s47,s47,s46
ds_read_b64_tr_b16 a[250:251],v3 offset:4864
v_add_u32 v2,s48,v2
v_mfma_f32_16x16x32_bf16 v[204:207],a[148:151],v[100:103],v[204:207]
s_and_b32 s46,s54,0xfffc0000
v_exp_f32 v168,v168
v_mfma_f32_16x16x32_bf16 v[204:207],a[144:147],v[96:99],v[204:207]
s_add_u32 s54,s54,16384
v_exp_f32 v169,v169
v_mfma_f32_16x16x32_bf16 v[216:219],a[160:163],v[96:99],v[40:43]
v_exp_f32 v170,v170
v_mfma_f32_16x16x32_bf16 v[216:219],a[164:167],v[100:103],v[216:219]
v_exp_f32 v171,v171
v_mfma_f32_16x16x32_bf16 v[216:219],a[168:171],v[104:107],v[216:219]
v_cvt_pk_bf16_f32 v168,v168,v169
v_cvt_pk_bf16_f32 v169,v170,v171
v_mfma_f32_16x16x32_bf16 v[216:219],a[172:175],v[108:111],v[216:219]
v_exp_f32 v172,v172
v_mfma_f32_16x16x32_bf16 v[220:223],a[188:191],v[108:111],v[40:43]
v_exp_f32 v173,v173
v_mfma_f32_16x16x32_bf16 v[220:223],a[184:187],v[104:107],v[220:223]
v_exp_f32 v174,v174
v_mfma_f32_16x16x32_bf16 v[220:223],a[180:183],v[100:103],v[220:223]
v_exp_f32 v175,v175
v_mfma_f32_16x16x32_bf16 v[220:223],a[176:179],v[96:99],v[220:223]
s_waitcnt lgkmcnt(0)
v_cvt_pk_bf16_f32 v170,v172,v173
v_cvt_pk_bf16_f32 v171,v174,v175
v_mfma_f32_16x16x32_bf16 v[32:35],v[80:83],v[128:131],0
v_exp_f32 v184,v184
v_mfma_f32_16x16x32_bf16 v[32:35],v[84:87],v[160:163],v[32:35]
)A"
R"A(v_exp_f32 v185,v185
v_mfma_f32_16x16x32_bf16 v[32:35],v[88:91],v[192:195],v[32:35]
v_exp_f32 v186,v186
v_mfma_f32_16x16x32_bf16 v[32:35],v[92:95],v[224:227],v[32:35]
v_exp_f32 v187,v187
v_mfma_f32_16x16x32_bf16 v[36:39],v[80:83],v[132:135],0
v_exp_f32 v188,v188
v_mfma_f32_16x16x32_bf16 v[36:39],v[84:87],v[164:167],v[36:39]
v_exp_f32 v189,v189
v_mfma_f32_16x16x32_bf16 v[36:39],v[88:91],v[196:199],v[36:39]
v_exp_f32 v190,v190
v_mfma_f32_16x16x32_bf16 v[36:39],v[92:95],v[228:231],v[36:39]
v_exp_f32 v191,v191
v_mfma_f32_16x16x32_bf16 v[40:43],v[80:83],v[144:147],0
v_cvt_pk_bf16_f32 v172,v184,v185
v_cvt_pk_bf16_f32 v173,v186,v187
v_mfma_f32_16x16x32_bf16 v[40:43],v[84:87],v[176:179],v[40:43]
v_cvt_pk_bf16_f32 v174,v188,v189
v_cvt_pk_bf16_f32 v175,v190,v191
v_mfma_f32_16x16x32_bf16 v[40:43],v[88:91],v[208:211],v[40:43]
v_max3_f32 v32,v32,v33,v34
v_max3_f32 v32,v32,v35,s18
v_mfma_f32_16x16x32_bf16 v[40:43],v[92:95],v[240:243],v[40:43]
v_sub_f32 v35,0,v32
v_sub_f32 v34,0,v32
v_mfma_f32_16x16x32_bf16 a[0:3],a[192:195],v[136:139],a[0:3]
v_sub_f32 v33,0,v32
v_sub_f32 v32,0,v32
v_mfma_f32_16x16x32_bf16 a[0:3],a[196:199],v[140:143],a[0:3]
v_exp_f32 v200,v200
v_mfma_f32_16x16x32_bf16 a[4:7],a[204:207],v[140:143],a[4:7]
v_exp_f32 v201,v201
v_mfma_f32_16x16x32_bf16 a[4:7],a[200:203],v[136:139],a[4:7]
s_memrealtime s[76:77]
v_exp_f32 v202,v202
v_mfma_f32_16x16x32_bf16 a[8:11],a[208:211],v[136:139],a[8:11]
v_exp_f32 v203,v203
v_mfma_f32_16x16x32_bf16 a[8:11],a[212:215],v[140:143],a[8:11]
v_cvt_pk_bf16_f32 v200,v200,v201
v_cvt_pk_bf16_f32 v201,v202,v203
v_mfma_f32_16x16x32_bf16 a[12:15],a[220:223],v[140:143],a[12:15]
v_exp_f32 v204,v204
v_mfma_f32_16x16x32_bf16 a[12:15],a[216:219],v[136:139],a[12:15]
v_exp_f32 v205,v205
v_mfma_f32_16x16x32_bf16 a[16:19],a[224:227],v[136:139],a[16:19]
v_exp_f32 v206,v206
v_mfma_f32_16x16x32_bf16 a[16:19],a[228:231],v[140:143],a[16:19]
v_exp_f32 v207,v207
v_mfma_f32_16x16x32_bf16 a[20:23],a[236:239],v[140:143],a[20:23]
s_waitcnt lgkmcnt(0)
v_cvt_pk_bf16_f32 v202,v204,v205
v_cvt_pk_bf16_f32 v203,v206,v207
v_mfma_f32_16x16x32_bf16 a[20:23],a[232:235],v[136:139],a[20:23]
s_sub_u32 s79,s78,s76
v_exp_f32 v216,v216
v_mfma_f32_16x16x32_bf16 a[24:27],a[240:243],v[136:139],a[24:27]
s_add_u32 s78,s76,s60
v_exp_f32 v217,v217
v_mfma_f32_16x16x32_bf16 a[24:27],a[244:247],v[140:143],a[24:27]
s_cmp_gt_i32 s79,s95
s_cbranch_scc1 L_ps4_%=
L_pr4_%=:
v_cvt_pk_bf16_f32 v204,v216,v217
v_mfma_f32_16x16x32_bf16 a[28:31],a[252:255],v[140:143],a[28:31]
s_lshr_b32 s60,s94,2
v_exp_f32 v218,v218
v_mfma_f32_16x16x32_bf16 a[28:31],a[248:251],v[136:139],a[28:31]
s_max_i32 s60,s60,128
v_exp_f32 v219,v219
v_smfmac_f32_16x16x64_bf16 v[48:51],v[28:31],v[136:143],v27
s_sub_u32 s94,s94,3
v_cvt_pk_bf16_f32 v205,v218,v219
v_mfma_f32_16x16x32_bf16 v[232:235],a[128:131],v[112:115],v[44:47]
s_waitcnt vmcnt(9)
v_exp_f32 v220,v220
v_mfma_f32_16x16x32_bf16 v[232:235],a[132:135],v[116:119],v[232:235]
s_barrier
s_add_u32 m0,32768,s51
ds_read_b128 a[128:131],v1
v_mfma_f32_16x16x32_bf16 v[232:235],a[136:139],v[120:123],v[232:235]
ds_read_b128 a[132:135],v1 offset:256
v_max3_f32 v36,v36,v37,v38
v_mfma_f32_16x16x32_bf16 v[232:235],a[140:143],v[124:127],v[232:235]
ds_read_b128 a[136:139],v1 offset:512
v_max3_f32 v36,v36,v39,s19
v_mfma_f32_16x16x32_bf16 v[236:239],a[156:159],v[124:127],v[44:47]
buffer_load_dwordx4 v4,s[20:23],s47 offen nt lds
v_mfma_f32_16x16x32_bf16 v[236:239],a[152:155],v[120:123],v[236:239]
ds_read_b128 a[140:143],v1 offset:768
v_sub_f32 v39,0,v36
v_mfma_f32_16x16x32_bf16 v[236:239],a[148:151],v[116:119],v[236:239]
ds_read_b128 a[156:159],v1 offset:4864
v_sub_f32 v38,0,v36
v_mfma_f32_16x16x32_bf16 v[236:239],a[144:147],v[112:115],v[236:239]
ds_read_b128 a[152:155],v1 offset:4608
v_sub_f32 v37,0,v36
v_mfma_f32_16x16x32_bf16 v[248:251],a[160:163],v[112:115],v[44:47]
ds_read_b128 a[148:151],v1 offset:4352
v_sub_f32 v36,0,v36
v_mfma_f32_16x16x32_bf16 v[248:251],a[164:167],v[116:119],v[248:251]
ds_read_b128 a[144:147],v1 offset:4096
v_max3_f32 v40,v40,v41,v42
v_mfma_f32_16x16x32_bf16 v[248:251],a[168:171],v[120:123],v[248:251]
ds_read_b128 a[160:163],v1 offset:8192
v_max3_f32 v40,v40,v43,s100
v_mfma_f32_16x16x32_bf16 v[248:251],a[172:175],v[124:127],v[248:251]
ds_read_b128 a[164:167],v1 offset:8448
v_sub_f32 v43,0,v40
v_mfma_f32_16x16x32_bf16 v[252:255],a[188:191],v[124:127],v[44:47]
ds_read_b128 a[168:171],v1 offset:8704
v_sub_f32 v42,0,v40
v_mfma_f32_16x16x32_bf16 v[252:255],a[184:187],v[120:123],v[252:255]
ds_read_b128 a[172:175],v1 offset:8960
v_sub_f32 v41,0,v40
v_mfma_f32_16x16x32_bf16 v[252:255],a[180:183],v[116:119],v[252:255]
ds_read_b128 a[188:191],v1 offset:13056
v_sub_f32 v40,0,v40
v_mfma_f32_16x16x32_bf16 v[252:255],a[176:179],v[112:115],v[252:255]
ds_read_b128 a[184:187],v1 offset:12800
v_add_u32 v3,s48,v3
v_mfma_f32_16x16x32_bf16 v[44:47],v[80:83],v[148:151],0
s_mov_b32 s48,-98304
ds_read_b128 a[180:183],v1 offset:12544
v_add_f32 v48,v49,v48
v_mfma_f32_16x16x32_bf16 v[44:47],v[84:87],v[180:183],v[44:47]
ds_read_b128 a[176:179],v1 offset:12288
v_max_f32 v20,0x00800000,v48
v_mfma_f32_16x16x32_bf16 v[44:47],v[88:91],v[212:215],v[44:47]
buffer_load_dwordx4 v4,s[20:23],s47 offen offset:1024 nt lds
v_mfma_f32_16x16x32_bf16 v[44:47],v[92:95],v[244:247],v[44:47]
v_exp_f32 v221,v221
v_mfma_f32_16x16x32_bf16 a[32:35],a[192:195],v[168:171],a[32:35]
v_exp_f32 v222,v222
v_mfma_f32_16x16x32_bf16 a[32:35],a[196:199],v[172:175],a[32:35]
v_exp_f32 v223,v223
v_mfma_f32_16x16x32_bf16 a[36:39],a[204:207],v[172:175],a[36:39]
v_cvt_pk_bf16_f32 v206,v220,v221
v_cvt_pk_bf16_f32 v207,v222,v223
v_mfma_f32_16x16x32_bf16 a[36:39],a[200:203],v[168:171],a[36:39]
v_exp_f32 v232,v232
v_mfma_f32_16x16x32_bf16 a[40:43],a[208:211],v[168:171],a[40:43]
v_exp_f32 v233,v233
v_mfma_f32_16x16x32_bf16 a[40:43],a[212:215],v[172:175],a[40:43]
v_exp_f32 v234,v234
v_mfma_f32_16x16x32_bf16 a[44:47],a[220:223],v[172:175],a[44:47]
v_exp_f32 v235,v235
v_mfma_f32_16x16x32_bf16 a[44:47],a[216:219],v[168:171],a[44:47]
v_cvt_pk_bf16_f32 v232,v232,v233
v_cvt_pk_bf16_f32 v233,v234,v235
v_mfma_f32_16x16x32_bf16 a[48:51],a[224:227],v[168:171],a[48:51]
v_exp_f32 v236,v236
v_mfma_f32_16x16x32_bf16 a[48:51],a[228:231],v[172:175],a[48:51]
v_exp_f32 v237,v237
v_mfma_f32_16x16x32_bf16 a[52:55],a[236:239],v[172:175],a[52:55]
v_exp_f32 v238,v238
v_mfma_f32_16x16x32_bf16 a[52:55],a[232:235],v[168:171],a[52:55]
v_exp_f32 v239,v239
v_mfma_f32_16x16x32_bf16 a[56:59],a[240:243],v[168:171],a[56:59]
buffer_load_dwordx4 v5,s[20:23],s47 offen offset:2048 nt lds
v_mfma_f32_16x16x32_bf16 a[56:59],a[244:247],v[172:175],a[56:59]
v_cvt_pk_bf16_f32 v234,v236,v237
v_cvt_pk_bf16_f32 v235,v238,v239
v_mfma_f32_16x16x32_bf16 a[60:63],a[252:255],v[172:175],a[60:63]
v_exp_f32 v248,v248
v_mfma_f32_16x16x32_bf16 a[60:63],a[248:251],v[168:171],a[60:63]
v_exp_f32 v249,v249
v_smfmac_f32_16x16x64_bf16 v[52:55],v[28:31],v[168:175],v27
s_waitcnt lgkmcnt(0)
v_cvt_pk_bf16_f32 v236,v248,v249
v_mfma_f32_16x16x32_bf16 v[136:139],a[128:131],v[128:131],v[32:35]
v_exp_f32 v250,v250
v_mfma_f32_16x16x32_bf16 v[136:139],a[132:135],v[160:163],v[136:139]
v_exp_f32 v251,v251
v_mfma_f32_16x16x32_bf16 v[136:139],a[136:139],v[192:195],v[136:139]
v_exp_f32 v252,v252
v_mfma_f32_16x16x32_bf16 v[136:139],a[140:143],v[224:227],v[136:139]
v_exp_f32 v253,v253
v_mfma_f32_16x16x32_bf16 v[140:143],a[156:159],v[224:227],v[32:35]
v_exp_f32 v254,v254
v_mfma_f32_16x16x32_bf16 v[140:143],a[152:155],v[192:195],v[140:143]
v_exp_f32 v255,v255
v_mfma_f32_16x16x32_bf16 v[140:143],a[148:151],v[160:163],v[140:143]
v_cvt_pk_bf16_f32 v237,v250,v251
v_cvt_pk_bf16_f32 v238,v252,v253
v_mfma_f32_16x16x32_bf16 v[140:143],a[144:147],v[128:131],v[140:143]
v_cvt_pk_bf16_f32 v239,v254,v255
v_accvgpr_read_b32 v64,a0
v_mfma_f32_16x16x32_bf16 v[152:155],a[160:163],v[128:131],v[32:35]
v_rcp_f32 v20,v20
v_mfma_f32_16x16x32_bf16 v[152:155],a[164:167],v[160:163],v[152:155]
buffer_load_dwordx4 v5,s[20:23],s47 offen offset:3072 nt lds
v_mfma_f32_16x16x32_bf16 v[152:155],a[168:171],v[192:195],v[152:155]
v_exp_f32 v136,v136
v_mfma_f32_16x16x32_bf16 v[152:155],a[172:175],v[224:227],v[152:155]
v_exp_f32 v137,v137
v_mfma_f32_16x16x32_bf16 v[156:159],a[188:191],v[224:227],v[32:35]
v_exp_f32 v138,v138
v_mfma_f32_16x16x32_bf16 v[156:159],a[184:187],v[192:195],v[156:159]
v_exp_f32 v139,v139
v_mfma_f32_16x16x32_bf16 v[156:159],a[180:183],v[160:163],v[156:159]
v_cvt_pk_bf16_f32 v136,v136,v137
v_cvt_pk_bf16_f32 v137,v138,v139
v_mfma_f32_16x16x32_bf16 v[156:159],a[176:179],v[128:131],v[156:159]
v_exp_f32 v140,v140
v_mfma_f32_16x16x32_bf16 a[64:67],a[192:195],v[200:203],a[64:67]
v_exp_f32 v141,v141
v_mfma_f32_16x16x32_bf16 a[64:67],a[196:199],v[204:207],a[64:67]
v_exp_f32 v142,v142
v_mfma_f32_16x16x32_bf16 a[68:71],a[204:207],v[204:207],a[68:71]
v_exp_f32 v143,v143
v_mfma_f32_16x16x32_bf16 a[68:71],a[200:203],v[200:203],a[68:71]
v_cvt_pk_bf16_f32 v138,v140,v141
v_cvt_pk_bf16_f32 v139,v142,v143
v_mfma_f32_16x16x32_bf16 a[72:75],a[208:211],v[200:203],a[72:75]
s_add_u32 m0,32768,s52
v_exp_f32 v152,v152
v_mfma_f32_16x16x32_bf16 a[72:75],a[212:215],v[204:207],a[72:75]
v_exp_f32 v153,v153
v_mfma_f32_16x16x32_bf16 a[76:79],a[220:223],v[204:207],a[76:79]
v_exp_f32 v154,v154
v_mfma_f32_16x16x32_bf16 a[76:79],a[216:219],v[200:203],a[76:79]
buffer_load_dwordx4 v4,s[24:27],s47 offen nt lds
v_mfma_f32_16x16x32_bf16 a[80:83],a[224:227],v[200:203],a[80:83]
v_exp_f32 v155,v155
v_mfma_f32_16x16x32_bf16 a[80:83],a[228:231],v[204:207],a[80:83]
v_exp_f32 v156,v156
v_mfma_f32_16x16x32_bf16 a[84:87],a[236:239],v[204:207],a[84:87]
v_exp_f32 v157,v157
v_mfma_f32_16x16x32_bf16 a[84:87],a[232:235],v[200:203],a[84:87]
v_exp_f32 v158,v158
v_mfma_f32_16x16x32_bf16 a[88:91],a[240:243],v[200:203],a[88:91]
v_exp_f32 v159,v159
v_mfma_f32_16x16x32_bf16 a[88:91],a[244:247],v[204:207],a[88:91]
v_cvt_pk_bf16_f32 v140,v152,v153
v_cvt_pk_bf16_f32 v141,v154,v155
v_mfma_f32_16x16x32_bf16 a[92:95],a[252:255],v[204:207],a[92:95]
v_cvt_pk_bf16_f32 v142,v156,v157
v_cvt_pk_bf16_f32 v143,v158,v159
v_mfma_f32_16x16x32_bf16 a[92:95],a[248:251],v[200:203],a[92:95]
v_accvgpr_read_b32 v65,a1
v_mul_f32 v64,v20,v64
v_smfmac_f32_16x16x64_bf16 v[56:59],v[28:31],v[200:207],v27
v_mul_f32 v65,v20,v65
v_mfma_f32_16x16x32_bf16 v[168:171],a[128:131],v[132:135],v[36:39]
v_accvgpr_read_b32 v66,a2
v_accvgpr_read_b32 v67,a3
v_mfma_f32_16x16x32_bf16 v[168:171],a[132:135],v[164:167],v[168:171]
v_cvt_pk_bf16_f32 v64,v64,v65
v_mul_f32 v66,v20,v66
v_mfma_f32_16x16x32_bf16 v[168:171],a[136:139],v[196:199],v[168:171]
v_mul_f32 v67,v20,v67
v_cvt_pk_bf16_f32 v65,v66,v67
v_mfma_f32_16x16x32_bf16 v[168:171],a[140:143],v[228:231],v[168:171]
v_accvgpr_read_b32 v66,a4
v_accvgpr_read_b32 v67,a5
v_mfma_f32_16x16x32_bf16 v[172:175],a[156:159],v[228:231],v[36:39]
buffer_load_dwordx4 v5,s[24:27],s47 offen offset:1024 nt lds
v_mfma_f32_16x16x32_bf16 v[172:175],a[152:155],v[196:199],v[172:175]
v_accvgpr_read_b32 v12,a6
v_accvgpr_read_b32 v13,a7
v_mfma_f32_16x16x32_bf16 v[172:175],a[148:151],v[164:167],v[172:175]
v_mul_f32 v66,v20,v66
v_mul_f32 v67,v20,v67
v_mfma_f32_16x16x32_bf16 v[172:175],a[144:147],v[132:135],v[172:175]
v_mul_f32 v12,v20,v12
v_mul_f32 v13,v20,v13
v_mfma_f32_16x16x32_bf16 v[184:187],a[160:163],v[132:135],v[36:39]
v_cvt_pk_bf16_f32 v66,v66,v67
v_cvt_pk_bf16_f32 v67,v12,v13
v_mfma_f32_16x16x32_bf16 v[184:187],a[164:167],v[164:167],v[184:187]
v_accvgpr_read_b32 v68,a8
v_accvgpr_read_b32 v69,a9
v_accvgpr_read_b32 v70,a10
v_accvgpr_read_b32 v71,a11
v_accvgpr_read_b32 v12,a12
v_accvgpr_read_b32 v13,a13
v_accvgpr_read_b32 v14,a14
v_accvgpr_read_b32 v15,a15
v_pk_mul_f32 v[68:69],v[20:21],v[68:69] op_sel:[0,0] op_sel_hi:[0,1]
v_pk_mul_f32 v[70:71],v[20:21],v[70:71] op_sel:[0,0] op_sel_hi:[0,1]
v_pk_mul_f32 v[12:13],v[20:21],v[12:13] op_sel:[0,0] op_sel_hi:[0,1]
v_pk_mul_f32 v[14:15],v[20:21],v[14:15] op_sel:[0,0] op_sel_hi:[0,1]
v_cvt_pk_bf16_f32 v68,v68,v69
v_cvt_pk_bf16_f32 v69,v70,v71
v_cvt_pk_bf16_f32 v70,v12,v13
v_cvt_pk_bf16_f32 v71,v14,v15
v_mfma_f32_16x16x32_bf16 v[184:187],a[168:171],v[196:199],v[184:187]
v_accvgpr_read_b32 v72,a16
)A"
R"A(v_accvgpr_read_b32 v73,a17
v_accvgpr_read_b32 v74,a18
v_accvgpr_read_b32 v75,a19
v_accvgpr_read_b32 v12,a20
v_accvgpr_read_b32 v13,a21
v_accvgpr_read_b32 v14,a22
v_accvgpr_read_b32 v15,a23
v_pk_mul_f32 v[72:73],v[20:21],v[72:73] op_sel:[0,0] op_sel_hi:[0,1]
v_pk_mul_f32 v[74:75],v[20:21],v[74:75] op_sel:[0,0] op_sel_hi:[0,1]
v_pk_mul_f32 v[12:13],v[20:21],v[12:13] op_sel:[0,0] op_sel_hi:[0,1]
v_pk_mul_f32 v[14:15],v[20:21],v[14:15] op_sel:[0,0] op_sel_hi:[0,1]
v_cvt_pk_bf16_f32 v72,v72,v73
v_cvt_pk_bf16_f32 v73,v74,v75
v_cvt_pk_bf16_f32 v74,v12,v13
v_cvt_pk_bf16_f32 v75,v14,v15
v_mfma_f32_16x16x32_bf16 v[184:187],a[172:175],v[228:231],v[184:187]
v_accvgpr_read_b32 v76,a24
v_accvgpr_read_b32 v77,a25
v_accvgpr_read_b32 v78,a26
v_accvgpr_read_b32 v79,a27
v_accvgpr_read_b32 v12,a28
v_accvgpr_read_b32 v13,a29
v_accvgpr_read_b32 v14,a30
v_accvgpr_read_b32 v15,a31
v_pk_mul_f32 v[76:77],v[20:21],v[76:77] op_sel:[0,0] op_sel_hi:[0,1]
v_pk_mul_f32 v[78:79],v[20:21],v[78:79] op_sel:[0,0] op_sel_hi:[0,1]
v_pk_mul_f32 v[12:13],v[20:21],v[12:13] op_sel:[0,0] op_sel_hi:[0,1]
v_pk_mul_f32 v[14:15],v[20:21],v[14:15] op_sel:[0,0] op_sel_hi:[0,1]
v_cvt_pk_bf16_f32 v76,v76,v77
v_cvt_pk_bf16_f32 v77,v78,v79
v_cvt_pk_bf16_f32 v78,v12,v13
v_cvt_pk_bf16_f32 v79,v14,v15
v_mfma_f32_16x16x32_bf16 v[188:191],a[188:191],v[228:231],v[36:39]
v_cmp_nlt_f32 s[56:57],v48,s63
v_max3_f32 v44,v44,v45,v46
v_mfma_f32_16x16x32_bf16 v[188:191],a[184:187],v[196:199],v[188:191]
v_max3_f32 v44,v44,v47,s101
v_mov_b32 v48,0
v_mfma_f32_16x16x32_bf16 v[188:191],a[180:183],v[164:167],v[188:191]
v_mov_b32 v49,0
v_mov_b32 v50,0
v_mfma_f32_16x16x32_bf16 v[188:191],a[176:179],v[132:135],v[188:191]
v_mov_b32 v51,0
v_sub_f32 v47,0,v44
v_mfma_f32_16x16x32_bf16 a[96:99],a[192:195],v[232:235],a[96:99]
ds_read_b64_tr_b16 a[192:193],v2
ds_read_b64_tr_b16 a[194:195],v2 offset:4096
v_mfma_f32_16x16x32_bf16 a[96:99],a[196:199],v[236:239],a[96:99]
ds_read_b64_tr_b16 a[196:197],v2 offset:8192
ds_read_b64_tr_b16 a[198:199],v2 offset:12288
v_mfma_f32_16x16x32_bf16 a[100:103],a[204:207],v[236:239],a[100:103]
buffer_load_dwordx4 v4,s[24:27],s47 offen offset:2048 nt lds
v_mfma_f32_16x16x32_bf16 a[100:103],a[200:203],v[232:235],a[100:103]
ds_read_b64_tr_b16 a[204:205],v3 offset:8192
ds_read_b64_tr_b16 a[206:207],v3 offset:12288
v_mfma_f32_16x16x32_bf16 a[104:107],a[208:211],v[232:235],a[104:107]
ds_read_b64_tr_b16 a[200:201],v3
ds_read_b64_tr_b16 a[202:203],v3 offset:4096
v_mfma_f32_16x16x32_bf16 a[104:107],a[212:215],v[236:239],a[104:107]
ds_read_b64_tr_b16 a[208:209],v2 offset:256
ds_read_b64_tr_b16 a[210:211],v2 offset:4352
v_mfma_f32_16x16x32_bf16 a[108:111],a[220:223],v[236:239],a[108:111]
ds_read_b64_tr_b16 a[212:213],v2 offset:8448
ds_read_b64_tr_b16 a[214:215],v2 offset:12544
v_mfma_f32_16x16x32_bf16 a[108:111],a[216:219],v[232:235],a[108:111]
ds_read_b64_tr_b16 a[220:221],v3 offset:8448
ds_read_b64_tr_b16 a[222:223],v3 offset:12544
v_mfma_f32_16x16x32_bf16 a[112:115],a[224:227],v[232:235],a[112:115]
ds_read_b64_tr_b16 a[216:217],v3 offset:256
ds_read_b64_tr_b16 a[218:219],v3 offset:4352
v_mfma_f32_16x16x32_bf16 a[112:115],a[228:231],v[236:239],a[112:115]
ds_read_b64_tr_b16 a[224:225],v2 offset:512
ds_read_b64_tr_b16 a[226:227],v2 offset:4608
v_mfma_f32_16x16x32_bf16 a[116:119],a[236:239],v[236:239],a[116:119]
ds_read_b64_tr_b16 a[228:229],v2 offset:8704
ds_read_b64_tr_b16 a[230:231],v2 offset:12800
v_mfma_f32_16x16x32_bf16 a[116:119],a[232:235],v[232:235],a[116:119]
ds_read_b64_tr_b16 a[236:237],v3 offset:8704
ds_read_b64_tr_b16 a[238:239],v3 offset:12800
v_mfma_f32_16x16x32_bf16 a[120:123],a[240:243],v[232:235],a[120:123]
ds_read_b64_tr_b16 a[232:233],v3 offset:512
ds_read_b64_tr_b16 a[234:235],v3 offset:4608
v_mfma_f32_16x16x32_bf16 a[120:123],a[244:247],v[236:239],a[120:123]
ds_read_b64_tr_b16 a[240:241],v2 offset:768
ds_read_b64_tr_b16 a[242:243],v2 offset:4864
v_mfma_f32_16x16x32_bf16 a[124:127],a[252:255],v[236:239],a[124:127]
ds_read_b64_tr_b16 a[244:245],v2 offset:8960
ds_read_b64_tr_b16 a[246:247],v2 offset:13056
v_mfma_f32_16x16x32_bf16 a[124:127],a[248:251],v[232:235],a[124:127]
ds_read_b64_tr_b16 a[252:253],v3 offset:8960
ds_read_b64_tr_b16 a[254:255],v3 offset:13056
v_smfmac_f32_16x16x64_bf16 v[60:63],v[28:31],v[232:239],v27
ds_read_b64_tr_b16 a[248:249],v3 offset:768
v_mfma_f32_16x16x32_bf16 v[200:203],a[128:131],v[144:147],v[40:43]
buffer_load_dwordx4 v5,s[24:27],s47 offen offset:3072 nt lds
v_mfma_f32_16x16x32_bf16 v[200:203],a[132:135],v[176:179],v[200:203]
s_add_u32 s47,s47,16384
ds_read_b64_tr_b16 a[250:251],v3 offset:4864
v_sub_f32 v46,0,v44
v_mfma_f32_16x16x32_bf16 v[200:203],a[136:139],v[208:211],v[200:203]
s_and_b32 s47,s47,0x3ffff
v_sub_f32 v45,0,v44
v_sub_f32 v44,0,v44
v_mfma_f32_16x16x32_bf16 v[200:203],a[140:143],v[240:243],v[200:203]
s_or_b32 s47,s47,s46
v_exp_f32 v168,v168
v_mfma_f32_16x16x32_bf16 v[204:207],a[156:159],v[240:243],v[40:43]
s_and_b32 s46,s54,0xfffc0000
v_exp_f32 v169,v169
v_mfma_f32_16x16x32_bf16 v[204:207],a[152:155],v[208:211],v[204:207]
v_exp_f32 v170,v170
v_mfma_f32_16x16x32_bf16 v[204:207],a[148:151],v[176:179],v[204:207]
v_exp_f32 v171,v171
v_mfma_f32_16x16x32_bf16 v[204:207],a[144:147],v[144:147],v[204:207]
v_cvt_pk_bf16_f32 v168,v168,v169
v_add_f32 v52,v53,v52
v_mfma_f32_16x16x32_bf16 v[216:219],a[160:163],v[144:147],v[40:43]
v_exp_f32 v172,v172
v_mfma_f32_16x16x32_bf16 v[216:219],a[164:167],v[176:179],v[216:219]
v_exp_f32 v173,v173
v_mfma_f32_16x16x32_bf16 v[216:219],a[168:171],v[208:211],v[216:219]
v_exp_f32 v174,v174
v_mfma_f32_16x16x32_bf16 v[216:219],a[172:175],v[240:243],v[216:219]
v_exp_f32 v175,v175
v_mfma_f32_16x16x32_bf16 v[220:223],a[188:191],v[240:243],v[40:43]
v_cvt_pk_bf16_f32 v169,v170,v171
v_max_f32 v21,0x00800000,v52
v_mfma_f32_16x16x32_bf16 v[220:223],a[184:187],v[208:211],v[220:223]
v_cvt_pk_bf16_f32 v170,v172,v173
v_cvt_pk_bf16_f32 v171,v174,v175
v_mfma_f32_16x16x32_bf16 v[220:223],a[180:183],v[176:179],v[220:223]
v_exp_f32 v184,v184
v_mfma_f32_16x16x32_bf16 v[220:223],a[176:179],v[144:147],v[220:223]
s_waitcnt lgkmcnt(0)
v_exp_f32 v185,v185
v_mfma_f32_16x16x32_bf16 a[0:3],a[192:195],v[136:139],0
v_exp_f32 v186,v186
v_mfma_f32_16x16x32_bf16 a[0:3],a[196:199],v[140:143],a[0:3]
v_exp_f32 v187,v187
v_mfma_f32_16x16x32_bf16 a[4:7],a[204:207],v[140:143],0
v_exp_f32 v188,v188
v_mfma_f32_16x16x32_bf16 a[4:7],a[200:203],v[136:139],a[4:7]
s_memrealtime s[76:77]
v_exp_f32 v189,v189
v_mfma_f32_16x16x32_bf16 a[8:11],a[208:211],v[136:139],0
v_exp_f32 v190,v190
v_mfma_f32_16x16x32_bf16 a[8:11],a[212:215],v[140:143],a[8:11]
v_exp_f32 v191,v191
v_mfma_f32_16x16x32_bf16 a[12:15],a[220:223],v[140:143],0
v_rcp_f32 v21,v21
v_mfma_f32_16x16x32_bf16 a[12:15],a[216:219],v[136:139],a[12:15]
v_cvt_pk_bf16_f32 v172,v184,v185
v_cvt_pk_bf16_f32 v173,v186,v187
v_mfma_f32_16x16x32_bf16 a[16:19],a[224:227],v[136:139],0
v_cvt_pk_bf16_f32 v174,v188,v189
v_cvt_pk_bf16_f32 v175,v190,v191
v_mfma_f32_16x16x32_bf16 a[16:19],a[228:231],v[140:143],a[16:19]
v_accvgpr_read_b32 v80,a32
v_accvgpr_read_b32 v81,a33
v_mfma_f32_16x16x32_bf16 a[20:23],a[236:239],v[140:143],0
s_waitcnt lgkmcnt(0)
v_mul_f32 v80,v21,v80
v_mul_f32 v81,v21,v81
v_mfma_f32_16x16x32_bf16 a[20:23],a[232:235],v[136:139],a[20:23]
s_sub_u32 s79,s78,s76
v_accvgpr_read_b32 v82,a34
v_accvgpr_read_b32 v83,a35
v_mfma_f32_16x16x32_bf16 a[24:27],a[240:243],v[136:139],0
s_add_u32 s78,s76,s60
v_cvt_pk_bf16_f32 v80,v80,v81
v_mul_f32 v82,v21,v82
v_mfma_f32_16x16x32_bf16 a[24:27],a[244:247],v[140:143],a[24:27]
s_cmp_gt_i32 s79,s95
s_cbranch_scc1 L_ps5_%=
L_pr5_%=:
v_mul_f32 v83,v21,v83
v_mfma_f32_16x16x32_bf16 a[28:31],a[252:255],v[140:143],0
s_lshr_b32 s60,s94,2
v_cvt_pk_bf16_f32 v81,v82,v83
v_accvgpr_read_b32 v82,a36
v_mfma_f32_16x16x32_bf16 a[28:31],a[248:251],v[136:139],a[28:31]
s_max_i32 s60,s60,128
v_accvgpr_read_b32 v83,a37
v_accvgpr_read_b32 v14,a38
v_smfmac_f32_16x16x64_bf16 v[48:51],v[28:31],v[136:143],v27
v_accvgpr_read_b32 v15,a39
v_mfma_f32_16x16x32_bf16 v[232:235],a[128:131],v[148:151],v[44:47]
s_waitcnt vmcnt(8)
v_mul_f32 v82,v21,v82
v_mul_f32 v83,v21,v83
v_mfma_f32_16x16x32_bf16 v[232:235],a[132:135],v[180:183],v[232:235]
s_barrier
s_add_u32 m0,65536,s51
v_mul_f32 v14,v21,v14
v_mfma_f32_16x16x32_bf16 v[232:235],a[136:139],v[212:215],v[232:235]
v_mul_f32 v15,v21,v15
v_cvt_pk_bf16_f32 v82,v82,v83
v_mfma_f32_16x16x32_bf16 v[232:235],a[140:143],v[244:247],v[232:235]
v_cvt_pk_bf16_f32 v83,v14,v15
v_add_u32 v1,s48,v1
v_mfma_f32_16x16x32_bf16 v[236:239],a[156:159],v[244:247],v[44:47]
buffer_load_dwordx4 v4,s[20:23],s47 offen nt lds
v_mfma_f32_16x16x32_bf16 v[236:239],a[152:155],v[212:215],v[236:239]
v_accvgpr_read_b32 v84,a40
v_accvgpr_read_b32 v85,a41
v_accvgpr_read_b32 v86,a42
v_accvgpr_read_b32 v87,a43
v_accvgpr_read_b32 v12,a44
v_accvgpr_read_b32 v13,a45
v_accvgpr_read_b32 v14,a46
v_accvgpr_read_b32 v15,a47
v_pk_mul_f32 v[84:85],v[20:21],v[84:85] op_sel:[1,0] op_sel_hi:[1,1]
v_pk_mul_f32 v[86:87],v[20:21],v[86:87] op_sel:[1,0] op_sel_hi:[1,1]
v_pk_mul_f32 v[12:13],v[20:21],v[12:13] op_sel:[1,0] op_sel_hi:[1,1]
v_pk_mul_f32 v[14:15],v[20:21],v[14:15] op_sel:[1,0] op_sel_hi:[1,1]
v_cvt_pk_bf16_f32 v84,v84,v85
v_cvt_pk_bf16_f32 v85,v86,v87
v_cvt_pk_bf16_f32 v86,v12,v13
v_cvt_pk_bf16_f32 v87,v14,v15
v_mfma_f32_16x16x32_bf16 v[236:239],a[148:151],v[180:183],v[236:239]
v_accvgpr_read_b32 v88,a48
v_accvgpr_read_b32 v89,a49
v_accvgpr_read_b32 v90,a50
v_accvgpr_read_b32 v91,a51
v_accvgpr_read_b32 v12,a52
v_accvgpr_read_b32 v13,a53
v_accvgpr_read_b32 v14,a54
v_accvgpr_read_b32 v15,a55
v_pk_mul_f32 v[88:89],v[20:21],v[88:89] op_sel:[1,0] op_sel_hi:[1,1]
v_pk_mul_f32 v[90:91],v[20:21],v[90:91] op_sel:[1,0] op_sel_hi:[1,1]
v_pk_mul_f32 v[12:13],v[20:21],v[12:13] op_sel:[1,0] op_sel_hi:[1,1]
v_pk_mul_f32 v[14:15],v[20:21],v[14:15] op_sel:[1,0] op_sel_hi:[1,1]
v_cvt_pk_bf16_f32 v88,v88,v89
v_cvt_pk_bf16_f32 v89,v90,v91
v_cvt_pk_bf16_f32 v90,v12,v13
v_cvt_pk_bf16_f32 v91,v14,v15
v_mfma_f32_16x16x32_bf16 v[236:239],a[144:147],v[148:151],v[236:239]
ds_read_b128 a[128:131],v1
v_mov_b32 v53,0
v_mfma_f32_16x16x32_bf16 v[248:251],a[160:163],v[148:151],v[44:47]
ds_read_b128 a[132:135],v1 offset:256
v_mov_b32 v54,0
v_mfma_f32_16x16x32_bf16 v[248:251],a[164:167],v[180:183],v[248:251]
ds_read_b128 a[136:139],v1 offset:512
v_mov_b32 v55,0
v_mfma_f32_16x16x32_bf16 v[248:251],a[168:171],v[212:215],v[248:251]
v_accvgpr_read_b32 v92,a56
v_accvgpr_read_b32 v93,a57
v_accvgpr_read_b32 v94,a58
v_accvgpr_read_b32 v95,a59
v_accvgpr_read_b32 v12,a60
v_accvgpr_read_b32 v13,a61
v_accvgpr_read_b32 v14,a62
v_accvgpr_read_b32 v15,a63
v_pk_mul_f32 v[92:93],v[20:21],v[92:93] op_sel:[1,0] op_sel_hi:[1,1]
v_pk_mul_f32 v[94:95],v[20:21],v[94:95] op_sel:[1,0] op_sel_hi:[1,1]
v_pk_mul_f32 v[12:13],v[20:21],v[12:13] op_sel:[1,0] op_sel_hi:[1,1]
v_pk_mul_f32 v[14:15],v[20:21],v[14:15] op_sel:[1,0] op_sel_hi:[1,1]
v_cvt_pk_bf16_f32 v92,v92,v93
v_cvt_pk_bf16_f32 v93,v94,v95
v_cvt_pk_bf16_f32 v94,v12,v13
v_cvt_pk_bf16_f32 v95,v14,v15
v_mfma_f32_16x16x32_bf16 v[248:251],a[172:175],v[244:247],v[248:251]
ds_read_b128 a[140:143],v1 offset:768
v_add_f32 v56,v57,v56
v_mfma_f32_16x16x32_bf16 v[252:255],a[188:191],v[244:247],v[44:47]
ds_read_b128 a[156:159],v1 offset:4864
v_max_f32 v22,0x00800000,v56
v_mfma_f32_16x16x32_bf16 v[252:255],a[184:187],v[212:215],v[252:255]
v_cmp_nlt_f32 s[58:59],v52,s63
s_nop 1
s_or_b64 s[56:57],s[56:57],s[58:59]
v_mfma_f32_16x16x32_bf16 v[252:255],a[180:183],v[180:183],v[252:255]
ds_read_b128 a[152:155],v1 offset:4608
v_mov_b32 v52,0
v_mfma_f32_16x16x32_bf16 v[252:255],a[176:179],v[148:151],v[252:255]
ds_read_b128 a[148:151],v1 offset:4352
v_accvgpr_read_b32 v96,a64
v_mfma_f32_16x16x32_bf16 a[32:35],a[192:195],v[168:171],0
ds_read_b128 a[144:147],v1 offset:4096
v_accvgpr_read_b32 v97,a65
v_mfma_f32_16x16x32_bf16 a[32:35],a[196:199],v[172:175],a[32:35]
ds_read_b128 a[160:163],v1 offset:8192
v_accvgpr_read_b32 v98,a66
v_mfma_f32_16x16x32_bf16 a[36:39],a[204:207],v[172:175],0
buffer_load_dwordx4 v4,s[20:23],s47 offen offset:1024 nt lds
v_mfma_f32_16x16x32_bf16 a[36:39],a[200:203],v[168:171],a[36:39]
)A"
R"A(ds_read_b128 a[164:167],v1 offset:8448
v_accvgpr_read_b32 v99,a67
v_mfma_f32_16x16x32_bf16 a[40:43],a[208:211],v[168:171],0
ds_read_b128 a[168:171],v1 offset:8704
v_accvgpr_read_b32 v16,a70
v_mfma_f32_16x16x32_bf16 a[40:43],a[212:215],v[172:175],a[40:43]
ds_read_b128 a[172:175],v1 offset:8960
v_accvgpr_read_b32 v17,a71
v_mfma_f32_16x16x32_bf16 a[44:47],a[220:223],v[172:175],0
ds_read_b128 a[188:191],v1 offset:13056
v_mov_b32 v57,0
v_mfma_f32_16x16x32_bf16 a[44:47],a[216:219],v[168:171],a[44:47]
ds_read_b128 a[184:187],v1 offset:12800
v_mov_b32 v58,0
v_mfma_f32_16x16x32_bf16 a[48:51],a[224:227],v[168:171],0
ds_read_b128 a[180:183],v1 offset:12544
v_mov_b32 v59,0
v_mfma_f32_16x16x32_bf16 a[48:51],a[228:231],v[172:175],a[48:51]
ds_read_b128 a[176:179],v1 offset:12288
v_add_f32 v60,v61,v60
v_mfma_f32_16x16x32_bf16 a[52:55],a[236:239],v[172:175],0
v_exp_f32 v200,v200
v_mfma_f32_16x16x32_bf16 a[52:55],a[232:235],v[168:171],a[52:55]
v_exp_f32 v201,v201
v_mfma_f32_16x16x32_bf16 a[56:59],a[240:243],v[168:171],0
v_exp_f32 v202,v202
v_mfma_f32_16x16x32_bf16 a[56:59],a[244:247],v[172:175],a[56:59]
v_exp_f32 v203,v203
v_mfma_f32_16x16x32_bf16 a[60:63],a[252:255],v[172:175],0
v_cvt_pk_bf16_f32 v200,v200,v201
v_cvt_pk_bf16_f32 v201,v202,v203
v_mfma_f32_16x16x32_bf16 a[60:63],a[248:251],v[168:171],a[60:63]
v_exp_f32 v204,v204
v_smfmac_f32_16x16x64_bf16 v[52:55],v[28:31],v[168:175],v27
s_waitcnt lgkmcnt(1)
v_max_f32 v23,0x00800000,v60
v_mfma_f32_16x16x32_bf16 v[136:139],a[128:131],v[128:131],v[32:35]
buffer_load_dwordx4 v5,s[20:23],s47 offen offset:2048 nt lds
v_mfma_f32_16x16x32_bf16 v[136:139],a[132:135],v[160:163],v[136:139]
v_exp_f32 v205,v205
v_mfma_f32_16x16x32_bf16 v[136:139],a[136:139],v[192:195],v[136:139]
v_exp_f32 v206,v206
v_mfma_f32_16x16x32_bf16 v[136:139],a[140:143],v[224:227],v[136:139]
v_exp_f32 v207,v207
v_mfma_f32_16x16x32_bf16 v[140:143],a[156:159],v[224:227],v[32:35]
v_cvt_pk_bf16_f32 v202,v204,v205
v_cvt_pk_bf16_f32 v203,v206,v207
v_mfma_f32_16x16x32_bf16 v[140:143],a[152:155],v[192:195],v[140:143]
v_exp_f32 v216,v216
v_mfma_f32_16x16x32_bf16 v[140:143],a[148:151],v[160:163],v[140:143]
v_exp_f32 v217,v217
v_mfma_f32_16x16x32_bf16 v[140:143],a[144:147],v[128:131],v[140:143]
v_exp_f32 v218,v218
v_mfma_f32_16x16x32_bf16 v[152:155],a[160:163],v[128:131],v[32:35]
v_exp_f32 v219,v219
v_mfma_f32_16x16x32_bf16 v[152:155],a[164:167],v[160:163],v[152:155]
v_exp_f32 v220,v220
v_mfma_f32_16x16x32_bf16 v[152:155],a[168:171],v[192:195],v[152:155]
v_exp_f32 v221,v221
v_mfma_f32_16x16x32_bf16 v[152:155],a[172:175],v[224:227],v[152:155]
v_exp_f32 v222,v222
v_mfma_f32_16x16x32_bf16 v[156:159],a[188:191],v[224:227],v[32:35]
v_exp_f32 v223,v223
v_mfma_f32_16x16x32_bf16 v[156:159],a[184:187],v[192:195],v[156:159]
v_cvt_pk_bf16_f32 v204,v216,v217
v_cvt_pk_bf16_f32 v205,v218,v219
v_mfma_f32_16x16x32_bf16 v[156:159],a[180:183],v[160:163],v[156:159]
s_waitcnt lgkmcnt(0)
v_cvt_pk_bf16_f32 v206,v220,v221
v_cvt_pk_bf16_f32 v207,v222,v223
v_mfma_f32_16x16x32_bf16 v[156:159],a[176:179],v[128:131],v[156:159]
v_rcp_f32 v22,v22
v_mfma_f32_16x16x32_bf16 a[64:67],a[192:195],v[200:203],0
buffer_load_dwordx4 v5,s[20:23],s47 offen offset:3072 nt lds
v_mul_f32 v96,v22,v96
v_mul_f32 v97,v22,v97
v_cvt_pk_bf16_f32 v96,v96,v97
v_mul_f32 v98,v22,v98
v_mul_f32 v99,v22,v99
v_mfma_f32_16x16x32_bf16 a[64:67],a[196:199],v[204:207],a[64:67]
v_cvt_pk_bf16_f32 v97,v98,v99
v_accvgpr_read_b32 v98,a68
v_accvgpr_read_b32 v99,a69
v_mfma_f32_16x16x32_bf16 a[68:71],a[204:207],v[204:207],0
v_accvgpr_read_b32 v100,a72
v_accvgpr_read_b32 v101,a73
v_accvgpr_read_b32 v102,a74
v_accvgpr_read_b32 v103,a75
v_accvgpr_read_b32 v12,a76
v_accvgpr_read_b32 v13,a77
v_accvgpr_read_b32 v14,a78
v_accvgpr_read_b32 v15,a79
v_pk_mul_f32 v[100:101],v[22:23],v[100:101] op_sel:[0,0] op_sel_hi:[0,1]
v_pk_mul_f32 v[102:103],v[22:23],v[102:103] op_sel:[0,0] op_sel_hi:[0,1]
v_pk_mul_f32 v[12:13],v[22:23],v[12:13] op_sel:[0,0] op_sel_hi:[0,1]
v_pk_mul_f32 v[14:15],v[22:23],v[14:15] op_sel:[0,0] op_sel_hi:[0,1]
v_cvt_pk_bf16_f32 v100,v100,v101
v_cvt_pk_bf16_f32 v101,v102,v103
v_cvt_pk_bf16_f32 v102,v12,v13
v_cvt_pk_bf16_f32 v103,v14,v15
v_mfma_f32_16x16x32_bf16 a[68:71],a[200:203],v[200:203],a[68:71]
v_accvgpr_read_b32 v104,a80
v_accvgpr_read_b32 v105,a81
v_accvgpr_read_b32 v106,a82
v_accvgpr_read_b32 v107,a83
v_accvgpr_read_b32 v12,a84
v_accvgpr_read_b32 v13,a85
v_accvgpr_read_b32 v14,a86
v_accvgpr_read_b32 v15,a87
v_pk_mul_f32 v[104:105],v[22:23],v[104:105] op_sel:[0,0] op_sel_hi:[0,1]
v_pk_mul_f32 v[106:107],v[22:23],v[106:107] op_sel:[0,0] op_sel_hi:[0,1]
v_pk_mul_f32 v[12:13],v[22:23],v[12:13] op_sel:[0,0] op_sel_hi:[0,1]
v_pk_mul_f32 v[14:15],v[22:23],v[14:15] op_sel:[0,0] op_sel_hi:[0,1]
v_cvt_pk_bf16_f32 v104,v104,v105
v_cvt_pk_bf16_f32 v105,v106,v107
v_cvt_pk_bf16_f32 v106,v12,v13
v_cvt_pk_bf16_f32 v107,v14,v15
v_mfma_f32_16x16x32_bf16 a[72:75],a[208:211],v[200:203],0
v_accvgpr_read_b32 v108,a88
v_accvgpr_read_b32 v109,a89
v_accvgpr_read_b32 v110,a90
v_accvgpr_read_b32 v111,a91
v_accvgpr_read_b32 v12,a92
v_accvgpr_read_b32 v13,a93
v_accvgpr_read_b32 v14,a94
v_accvgpr_read_b32 v15,a95
v_pk_mul_f32 v[108:109],v[22:23],v[108:109] op_sel:[0,0] op_sel_hi:[0,1]
v_pk_mul_f32 v[110:111],v[22:23],v[110:111] op_sel:[0,0] op_sel_hi:[0,1]
v_pk_mul_f32 v[12:13],v[22:23],v[12:13] op_sel:[0,0] op_sel_hi:[0,1]
v_pk_mul_f32 v[14:15],v[22:23],v[14:15] op_sel:[0,0] op_sel_hi:[0,1]
v_cvt_pk_bf16_f32 v108,v108,v109
v_cvt_pk_bf16_f32 v109,v110,v111
v_cvt_pk_bf16_f32 v110,v12,v13
v_cvt_pk_bf16_f32 v111,v14,v15
v_mfma_f32_16x16x32_bf16 a[72:75],a[212:215],v[204:207],a[72:75]
v_cmp_nlt_f32 s[58:59],v56,s63
s_nop 1
s_or_b64 s[56:57],s[56:57],s[58:59]
v_mfma_f32_16x16x32_bf16 a[76:79],a[220:223],v[204:207],0
v_mov_b32 v56,0
v_accvgpr_read_b32 v112,a96
v_mfma_f32_16x16x32_bf16 a[76:79],a[216:219],v[200:203],a[76:79]
v_exp_f32 v232,v232
v_mfma_f32_16x16x32_bf16 a[80:83],a[224:227],v[200:203],0
s_add_u32 m0,65536,s52
v_exp_f32 v233,v233
v_mfma_f32_16x16x32_bf16 a[80:83],a[228:231],v[204:207],a[80:83]
v_exp_f32 v234,v234
v_mfma_f32_16x16x32_bf16 a[84:87],a[236:239],v[204:207],0
v_exp_f32 v235,v235
v_mfma_f32_16x16x32_bf16 a[84:87],a[232:235],v[200:203],a[84:87]
buffer_load_dwordx4 v4,s[24:27],s47 offen nt lds
v_mfma_f32_16x16x32_bf16 a[88:91],a[240:243],v[200:203],0
v_cvt_pk_bf16_f32 v232,v232,v233
v_cvt_pk_bf16_f32 v233,v234,v235
v_mfma_f32_16x16x32_bf16 a[88:91],a[244:247],v[204:207],a[88:91]
v_exp_f32 v236,v236
v_mfma_f32_16x16x32_bf16 a[92:95],a[252:255],v[204:207],0
v_exp_f32 v237,v237
v_mfma_f32_16x16x32_bf16 a[92:95],a[248:251],v[200:203],a[92:95]
v_exp_f32 v238,v238
v_smfmac_f32_16x16x64_bf16 v[56:59],v[28:31],v[200:207],v27
v_cvt_pk_bf16_f32 v234,v236,v237
v_mfma_f32_16x16x32_bf16 v[168:171],a[128:131],v[132:135],v[36:39]
v_exp_f32 v239,v239
v_mfma_f32_16x16x32_bf16 v[168:171],a[132:135],v[164:167],v[168:171]
v_cvt_pk_bf16_f32 v235,v238,v239
v_accvgpr_read_b32 v113,a97
v_mfma_f32_16x16x32_bf16 v[168:171],a[136:139],v[196:199],v[168:171]
v_exp_f32 v248,v248
v_mfma_f32_16x16x32_bf16 v[168:171],a[140:143],v[228:231],v[168:171]
v_exp_f32 v249,v249
v_mfma_f32_16x16x32_bf16 v[172:175],a[156:159],v[228:231],v[36:39]
v_exp_f32 v250,v250
v_mfma_f32_16x16x32_bf16 v[172:175],a[152:155],v[196:199],v[172:175]
v_exp_f32 v251,v251
v_mfma_f32_16x16x32_bf16 v[172:175],a[148:151],v[164:167],v[172:175]
v_exp_f32 v252,v252
v_mfma_f32_16x16x32_bf16 v[172:175],a[144:147],v[132:135],v[172:175]
v_exp_f32 v253,v253
v_mfma_f32_16x16x32_bf16 v[184:187],a[160:163],v[132:135],v[36:39]
buffer_load_dwordx4 v5,s[24:27],s47 offen offset:1024 nt lds
v_mfma_f32_16x16x32_bf16 v[184:187],a[164:167],v[164:167],v[184:187]
v_exp_f32 v254,v254
v_mfma_f32_16x16x32_bf16 v[184:187],a[168:171],v[196:199],v[184:187]
v_exp_f32 v255,v255
v_mfma_f32_16x16x32_bf16 v[184:187],a[172:175],v[228:231],v[184:187]
v_rcp_f32 v23,v23
v_mfma_f32_16x16x32_bf16 v[188:191],a[188:191],v[228:231],v[36:39]
v_cvt_pk_bf16_f32 v236,v248,v249
v_cvt_pk_bf16_f32 v237,v250,v251
v_mfma_f32_16x16x32_bf16 v[188:191],a[184:187],v[196:199],v[188:191]
v_cvt_pk_bf16_f32 v238,v252,v253
v_cvt_pk_bf16_f32 v239,v254,v255
v_mfma_f32_16x16x32_bf16 v[188:191],a[180:183],v[164:167],v[188:191]
v_mul_f32 v112,v23,v112
v_mul_f32 v113,v23,v113
v_mfma_f32_16x16x32_bf16 v[188:191],a[176:179],v[132:135],v[188:191]
v_accvgpr_read_b32 v114,a98
v_accvgpr_read_b32 v115,a99
v_mfma_f32_16x16x32_bf16 a[96:99],a[192:195],v[232:235],0
v_cvt_pk_bf16_f32 v112,v112,v113
v_mul_f32 v114,v23,v114
v_mul_f32 v115,v23,v115
v_cvt_pk_bf16_f32 v113,v114,v115
v_mfma_f32_16x16x32_bf16 a[96:99],a[196:199],v[236:239],a[96:99]
v_accvgpr_read_b32 v114,a100
v_accvgpr_read_b32 v115,a101
v_accvgpr_read_b32 v12,a102
v_accvgpr_read_b32 v13,a103
v_mul_f32 v114,v23,v114
v_mul_f32 v115,v23,v115
v_mfma_f32_16x16x32_bf16 a[100:103],a[204:207],v[236:239],0
v_mul_f32 v12,v23,v12
v_mul_f32 v13,v23,v13
v_cvt_pk_bf16_f32 v114,v114,v115
v_cvt_pk_bf16_f32 v115,v12,v13
v_mfma_f32_16x16x32_bf16 a[100:103],a[200:203],v[232:235],a[100:103]
v_accvgpr_read_b32 v116,a104
v_accvgpr_read_b32 v117,a105
v_accvgpr_read_b32 v118,a106
v_accvgpr_read_b32 v119,a107
v_accvgpr_read_b32 v12,a108
v_accvgpr_read_b32 v13,a109
v_accvgpr_read_b32 v14,a110
v_accvgpr_read_b32 v15,a111
v_pk_mul_f32 v[116:117],v[22:23],v[116:117] op_sel:[1,0] op_sel_hi:[1,1]
v_pk_mul_f32 v[118:119],v[22:23],v[118:119] op_sel:[1,0] op_sel_hi:[1,1]
v_pk_mul_f32 v[12:13],v[22:23],v[12:13] op_sel:[1,0] op_sel_hi:[1,1]
v_pk_mul_f32 v[14:15],v[22:23],v[14:15] op_sel:[1,0] op_sel_hi:[1,1]
v_cvt_pk_bf16_f32 v116,v116,v117
v_cvt_pk_bf16_f32 v117,v118,v119
v_cvt_pk_bf16_f32 v118,v12,v13
v_cvt_pk_bf16_f32 v119,v14,v15
v_mfma_f32_16x16x32_bf16 a[104:107],a[208:211],v[232:235],0
v_accvgpr_read_b32 v120,a112
v_accvgpr_read_b32 v121,a113
v_accvgpr_read_b32 v122,a114
v_accvgpr_read_b32 v123,a115
v_accvgpr_read_b32 v12,a116
v_accvgpr_read_b32 v13,a117
v_accvgpr_read_b32 v14,a118
v_accvgpr_read_b32 v15,a119
v_pk_mul_f32 v[120:121],v[22:23],v[120:121] op_sel:[1,0] op_sel_hi:[1,1]
v_pk_mul_f32 v[122:123],v[22:23],v[122:123] op_sel:[1,0] op_sel_hi:[1,1]
v_pk_mul_f32 v[12:13],v[22:23],v[12:13] op_sel:[1,0] op_sel_hi:[1,1]
v_pk_mul_f32 v[14:15],v[22:23],v[14:15] op_sel:[1,0] op_sel_hi:[1,1]
v_cvt_pk_bf16_f32 v120,v120,v121
v_cvt_pk_bf16_f32 v121,v122,v123
v_cvt_pk_bf16_f32 v122,v12,v13
v_cvt_pk_bf16_f32 v123,v14,v15
v_mfma_f32_16x16x32_bf16 a[104:107],a[212:215],v[236:239],a[104:107]
v_accvgpr_read_b32 v124,a120
v_accvgpr_read_b32 v125,a121
v_accvgpr_read_b32 v126,a122
v_accvgpr_read_b32 v127,a123
v_accvgpr_read_b32 v12,a124
v_accvgpr_read_b32 v13,a125
v_accvgpr_read_b32 v14,a126
v_accvgpr_read_b32 v15,a127
v_pk_mul_f32 v[124:125],v[22:23],v[124:125] op_sel:[1,0] op_sel_hi:[1,1]
v_pk_mul_f32 v[126:127],v[22:23],v[126:127] op_sel:[1,0] op_sel_hi:[1,1]
v_pk_mul_f32 v[12:13],v[22:23],v[12:13] op_sel:[1,0] op_sel_hi:[1,1]
v_pk_mul_f32 v[14:15],v[22:23],v[14:15] op_sel:[1,0] op_sel_hi:[1,1]
v_cvt_pk_bf16_f32 v124,v124,v125
v_cvt_pk_bf16_f32 v125,v126,v127
v_cvt_pk_bf16_f32 v126,v12,v13
v_cvt_pk_bf16_f32 v127,v14,v15
v_mfma_f32_16x16x32_bf16 a[108:111],a[220:223],v[236:239],0
buffer_load_dwordx4 v4,s[24:27],s47 offen offset:2048 nt lds
v_mfma_f32_16x16x32_bf16 a[108:111],a[216:219],v[232:235],a[108:111]
v_cmp_nlt_f32 s[58:59],v60,s63
s_nop 1
s_or_b64 s[56:57],s[56:57],s[58:59]
v_mfma_f32_16x16x32_bf16 a[112:115],a[224:227],v[232:235],0
v_mov_b32 v60,0
v_mov_b32 v61,0
v_mfma_f32_16x16x32_bf16 a[112:115],a[228:231],v[236:239],a[112:115]
v_mov_b32 v62,0
v_mov_b32 v63,0
v_mfma_f32_16x16x32_bf16 a[116:119],a[236:239],v[236:239],0
v_add_u32 v2,s48,v2
v_add_u32 v3,s48,v3
v_mfma_f32_16x16x32_bf16 a[116:119],a[232:235],v[232:235],a[116:119]
s_mov_b32 s48,32768
ds_read_b64_tr_b16 a[192:193],v2
ds_read_b64_tr_b16 a[194:195],v2 offset:4096
v_mfma_f32_16x16x32_bf16 a[120:123],a[240:243],v[232:235],0
ds_read_b64_tr_b16 a[196:197],v2 offset:8192
ds_read_b64_tr_b16 a[198:199],v2 offset:12288
v_mfma_f32_16x16x32_bf16 a[120:123],a[244:247],v[236:239],a[120:123]
ds_read_b64_tr_b16 a[204:205],v3 offset:8192
)A"
R"A(ds_read_b64_tr_b16 a[206:207],v3 offset:12288
v_mfma_f32_16x16x32_bf16 a[124:127],a[252:255],v[236:239],0
ds_read_b64_tr_b16 a[200:201],v3
ds_read_b64_tr_b16 a[202:203],v3 offset:4096
v_mfma_f32_16x16x32_bf16 a[124:127],a[248:251],v[232:235],a[124:127]
v_exp_f32 v136,v136
v_smfmac_f32_16x16x64_bf16 v[60:63],v[28:31],v[232:239],v27
ds_read_b64_tr_b16 a[208:209],v2 offset:256
v_mfma_f32_16x16x32_bf16 v[200:203],a[128:131],v[144:147],v[40:43]
v_exp_f32 v137,v137
v_mfma_f32_16x16x32_bf16 v[200:203],a[132:135],v[176:179],v[200:203]
ds_read_b64_tr_b16 a[210:211],v2 offset:4352
v_cvt_pk_bf16_f32 v136,v136,v137
v_mfma_f32_16x16x32_bf16 v[200:203],a[136:139],v[208:211],v[200:203]
v_exp_f32 v138,v138
v_mfma_f32_16x16x32_bf16 v[200:203],a[140:143],v[240:243],v[200:203]
buffer_load_dwordx4 v5,s[24:27],s47 offen offset:3072 nt lds
v_mfma_f32_16x16x32_bf16 v[204:207],a[156:159],v[240:243],v[40:43]
s_add_u32 s47,s47,16384
v_exp_f32 v139,v139
v_mfma_f32_16x16x32_bf16 v[204:207],a[152:155],v[208:211],v[204:207]
s_and_b32 s47,s47,0x3ffff
v_exp_f32 v140,v140
v_mfma_f32_16x16x32_bf16 v[204:207],a[148:151],v[176:179],v[204:207]
s_or_b32 s47,s47,s46
v_exp_f32 v141,v141
v_mfma_f32_16x16x32_bf16 v[204:207],a[144:147],v[144:147],v[204:207]
v_exp_f32 v142,v142
v_mfma_f32_16x16x32_bf16 v[216:219],a[160:163],v[144:147],v[40:43]
v_exp_f32 v143,v143
v_mfma_f32_16x16x32_bf16 v[216:219],a[164:167],v[176:179],v[216:219]
ds_read_b64_tr_b16 a[212:213],v2 offset:8448
ds_read_b64_tr_b16 a[214:215],v2 offset:12544
v_mfma_f32_16x16x32_bf16 v[216:219],a[168:171],v[208:211],v[216:219]
v_cvt_pk_bf16_f32 v137,v138,v139
v_cvt_pk_bf16_f32 v138,v140,v141
v_mfma_f32_16x16x32_bf16 v[216:219],a[172:175],v[240:243],v[216:219]
ds_read_b64_tr_b16 a[220:221],v3 offset:8448
v_cvt_pk_bf16_f32 v139,v142,v143
v_mfma_f32_16x16x32_bf16 v[220:223],a[188:191],v[240:243],v[40:43]
v_exp_f32 v152,v152
v_mfma_f32_16x16x32_bf16 v[220:223],a[184:187],v[208:211],v[220:223]
ds_read_b64_tr_b16 a[222:223],v3 offset:12544
v_exp_f32 v153,v153
v_exp_f32 v154,v154
v_exp_f32 v155,v155
v_exp_f32 v156,v156
v_exp_f32 v157,v157
v_exp_f32 v158,v158
v_exp_f32 v159,v159
v_mfma_f32_16x16x32_bf16 v[220:223],a[180:183],v[176:179],v[220:223]
ds_read_b64_tr_b16 a[216:217],v3 offset:256
ds_read_b64_tr_b16 a[218:219],v3 offset:4352
v_cvt_pk_bf16_f32 v140,v152,v153
v_cvt_pk_bf16_f32 v141,v154,v155
v_cvt_pk_bf16_f32 v142,v156,v157
v_cvt_pk_bf16_f32 v143,v158,v159
v_mfma_f32_16x16x32_bf16 v[220:223],a[176:179],v[144:147],v[220:223]
s_waitcnt lgkmcnt(6)
ds_read_b64_tr_b16 a[224:225],v2 offset:512
ds_read_b64_tr_b16 a[226:227],v2 offset:4608
v_mfma_f32_16x16x32_bf16 a[0:3],a[192:195],v[136:139],a[0:3]
ds_read_b64_tr_b16 a[228:229],v2 offset:8704
ds_read_b64_tr_b16 a[230:231],v2 offset:12800
v_mfma_f32_16x16x32_bf16 a[0:3],a[196:199],v[140:143],a[0:3]
ds_read_b64_tr_b16 a[236:237],v3 offset:8704
ds_read_b64_tr_b16 a[238:239],v3 offset:12800
v_mfma_f32_16x16x32_bf16 a[4:7],a[204:207],v[140:143],a[4:7]
ds_read_b64_tr_b16 a[232:233],v3 offset:512
ds_read_b64_tr_b16 a[234:235],v3 offset:4608
v_mfma_f32_16x16x32_bf16 a[4:7],a[200:203],v[136:139],a[4:7]
s_memrealtime s[76:77]
ds_read_b64_tr_b16 a[240:241],v2 offset:768
ds_read_b64_tr_b16 a[242:243],v2 offset:4864
v_mfma_f32_16x16x32_bf16 a[8:11],a[208:211],v[136:139],a[8:11]
s_waitcnt lgkmcnt(13)
ds_read_b64_tr_b16 a[244:245],v2 offset:8960
ds_read_b64_tr_b16 a[246:247],v2 offset:13056
v_mfma_f32_16x16x32_bf16 a[8:11],a[212:215],v[140:143],a[8:11]
s_waitcnt lgkmcnt(14)
ds_read_b64_tr_b16 a[252:253],v3 offset:8960
ds_read_b64_tr_b16 a[254:255],v3 offset:13056
v_mfma_f32_16x16x32_bf16 a[12:15],a[220:223],v[140:143],a[12:15]
s_waitcnt lgkmcnt(14)
ds_read_b64_tr_b16 a[248:249],v3 offset:768
ds_read_b64_tr_b16 a[250:251],v3 offset:4864
v_mfma_f32_16x16x32_bf16 a[12:15],a[216:219],v[136:139],a[12:15]
s_waitcnt lgkmcnt(14)
v_exp_f32 v168,v168
v_mfma_f32_16x16x32_bf16 a[16:19],a[224:227],v[136:139],a[16:19]
s_waitcnt lgkmcnt(12)
v_exp_f32 v169,v169
v_mfma_f32_16x16x32_bf16 a[16:19],a[228:231],v[140:143],a[16:19]
s_waitcnt lgkmcnt(10)
v_mul_f32 v98,v22,v98
v_mul_f32 v99,v22,v99
v_mfma_f32_16x16x32_bf16 a[20:23],a[236:239],v[140:143],a[20:23]
s_waitcnt lgkmcnt(0)
v_exp_f32 v170,v170
v_mfma_f32_16x16x32_bf16 a[20:23],a[232:235],v[136:139],a[20:23]
s_sub_u32 s79,s78,s76
v_exp_f32 v171,v171
v_mfma_f32_16x16x32_bf16 a[24:27],a[240:243],v[136:139],a[24:27]
s_add_u32 s78,s76,s60
v_mul_f32 v16,v22,v16
v_mul_f32 v17,v22,v17
v_mfma_f32_16x16x32_bf16 a[24:27],a[244:247],v[140:143],a[24:27]
s_cmp_gt_i32 s79,s95
s_cbranch_scc1 L_ps6_%=
L_pr6_%=:
v_cvt_pk_bf16_f32 v98,v98,v99
v_mfma_f32_16x16x32_bf16 a[28:31],a[252:255],v[140:143],a[28:31]
v_cvt_pk_bf16_f32 v168,v168,v169
v_exp_f32 v172,v172
v_exp_f32 v173,v173
v_exp_f32 v174,v174
v_exp_f32 v175,v175
v_cvt_pk_bf16_f32 v99,v16,v17
v_cvt_pk_bf16_f32 v169,v170,v171
v_exp_f32 v184,v184
v_exp_f32 v185,v185
v_exp_f32 v186,v186
v_exp_f32 v187,v187
v_exp_f32 v188,v188
v_exp_f32 v189,v189
v_exp_f32 v190,v190
v_exp_f32 v191,v191
v_mfma_f32_16x16x32_bf16 a[28:31],a[248:251],v[136:139],a[28:31]
s_cmp_lg_u64 s[56:57],0
s_cbranch_scc1 L_slow0_%=
L_sret0_%=:
v_cvt_pk_bf16_f32 v170,v172,v173
v_cvt_pk_bf16_f32 v171,v174,v175
v_add_u32 v1,s48,v1
v_cvt_pk_bf16_f32 v172,v184,v185
v_cvt_pk_bf16_f32 v173,v186,v187
v_cvt_pk_bf16_f32 v174,v188,v189
v_cvt_pk_bf16_f32 v175,v190,v191
v_smfmac_f32_16x16x64_bf16 v[48:51],v[28:31],v[136:143],v27
s_mov_b32 s53,11
s_cmp_eq_u32 s40,3
s_cbranch_scc0 L_att1_%=
s_cmp_gt_i32 s18,0
s_cbranch_scc1 L_att1v0_%=
s_cmp_gt_i32 s19,0
s_cbranch_scc1 L_att1v1_%=
s_cmp_gt_i32 s100,0
s_cbranch_scc1 L_att1v2_%=
.p2align 5
L_att1v3_%=:
s_waitcnt vmcnt(8) lgkmcnt(0)
v_exp_f32 v200,v200
s_barrier
s_add_u32 m0,s49,s51
ds_read_b128 a[128:131],v1
s_mov_b32 s90,L_out_end_%=-L_pc_%=+128
v_exp_f32 v201,v201
s_lshr_b32 s45,s45,1
ds_read_b128 a[132:135],v1 offset:256
v_cvt_pk_bf16_f32 v200,v200,v201
buffer_load_dwordx4 v4,s[20:23],s47 offen nt lds
ds_read_b128 a[136:139],v1 offset:512
v_add_u32 v2,s48,v2
v_exp_f32 v202,v202
s_add_u32 s62,s50,32768
ds_read_b128 a[140:143],v1 offset:768
v_add_u32 v3,s48,v3
s_and_b32 s62,s62,131071
v_exp_f32 v203,v203
s_add_u32 s54,s54,16384
ds_read_b128 a[156:159],v1 offset:4864
v_cvt_pk_bf16_f32 v201,v202,v203
s_sub_u32 s48,s62,s50
v_exp_f32 v204,v204
s_sub_u32 s53,s53,1
v_exp_f32 v205,v205
s_and_b32 s46,s54,0xfffc0000
ds_read_b128 a[152:155],v1 offset:4608
v_cvt_pk_bf16_f32 v202,v204,v205
s_mov_b32 s50,s62
v_exp_f32 v206,v206
ds_read_b128 a[148:151],v1 offset:4352
v_exp_f32 v207,v207
v_mfma_f32_16x16x32_bf16 a[32:35],a[192:195],v[168:171],a[32:35]
ds_read_b128 a[144:147],v1 offset:4096
v_cvt_pk_bf16_f32 v203,v206,v207
v_mfma_f32_16x16x32_bf16 a[32:35],a[196:199],v[172:175],a[32:35]
v_exp_f32 v216,v216
v_mfma_f32_16x16x32_bf16 a[36:39],a[204:207],v[172:175],a[36:39]
buffer_load_dwordx4 v4,s[20:23],s47 offen offset:1024 nt lds
v_mfma_f32_16x16x32_bf16 a[36:39],a[200:203],v[168:171],a[36:39]
ds_read_b128 a[160:163],v1 offset:8192
v_mfma_f32_16x16x32_bf16 a[40:43],a[208:211],v[168:171],a[40:43]
v_exp_f32 v217,v217
v_mfma_f32_16x16x32_bf16 a[40:43],a[212:215],v[172:175],a[40:43]
ds_read_b128 a[164:167],v1 offset:8448
v_cvt_pk_bf16_f32 v204,v216,v217
v_mfma_f32_16x16x32_bf16 a[44:47],a[220:223],v[172:175],a[44:47]
v_exp_f32 v218,v218
v_mfma_f32_16x16x32_bf16 a[44:47],a[216:219],v[168:171],a[44:47]
ds_read_b128 a[168:171],v1 offset:8704
v_mfma_f32_16x16x32_bf16 a[48:51],a[224:227],v[168:171],a[48:51]
v_exp_f32 v219,v219
v_mfma_f32_16x16x32_bf16 a[48:51],a[228:231],v[172:175],a[48:51]
ds_read_b128 a[172:175],v1 offset:8960
v_cvt_pk_bf16_f32 v205,v218,v219
v_mfma_f32_16x16x32_bf16 a[52:55],a[236:239],v[172:175],a[52:55]
v_exp_f32 v220,v220
v_mfma_f32_16x16x32_bf16 a[52:55],a[232:235],v[168:171],a[52:55]
ds_read_b128 a[188:191],v1 offset:13056
v_mfma_f32_16x16x32_bf16 a[56:59],a[240:243],v[168:171],a[56:59]
v_exp_f32 v221,v221
v_mfma_f32_16x16x32_bf16 a[56:59],a[244:247],v[172:175],a[56:59]
ds_read_b128 a[184:187],v1 offset:12800
v_cvt_pk_bf16_f32 v206,v220,v221
v_mfma_f32_16x16x32_bf16 a[60:63],a[252:255],v[172:175],a[60:63]
v_exp_f32 v222,v222
v_mfma_f32_16x16x32_bf16 a[60:63],a[248:251],v[168:171],a[60:63]
ds_read_b128 a[180:183],v1 offset:12544
v_smfmac_f32_16x16x64_bf16 v[52:55],v[28:31],v[168:175],v27
s_waitcnt lgkmcnt(4)
v_mfma_f32_16x16x32_bf16 v[136:139],a[128:131],v[128:131],v[32:35]
buffer_load_dwordx4 v5,s[20:23],s47 offen offset:2048 nt lds
v_mfma_f32_16x16x32_bf16 v[136:139],a[132:135],v[160:163],v[136:139]
ds_read_b128 a[176:179],v1 offset:12288
v_add_u32 v1,s48,v1
v_mfma_f32_16x16x32_bf16 v[136:139],a[136:139],v[192:195],v[136:139]
v_exp_f32 v223,v223
v_mfma_f32_16x16x32_bf16 v[136:139],a[140:143],v[224:227],v[136:139]
v_cvt_pk_bf16_f32 v207,v222,v223
v_mfma_f32_16x16x32_bf16 v[140:143],a[156:159],v[224:227],v[32:35]
v_mfma_f32_16x16x32_bf16 v[140:143],a[152:155],v[192:195],v[140:143]
v_mfma_f32_16x16x32_bf16 v[140:143],a[148:151],v[160:163],v[140:143]
v_mfma_f32_16x16x32_bf16 v[140:143],a[144:147],v[128:131],v[140:143]
v_mfma_f32_16x16x32_bf16 v[152:155],a[160:163],v[128:131],v[32:35]
v_mfma_f32_16x16x32_bf16 v[152:155],a[164:167],v[160:163],v[152:155]
v_mfma_f32_16x16x32_bf16 v[152:155],a[168:171],v[192:195],v[152:155]
s_waitcnt lgkmcnt(0)
v_mfma_f32_16x16x32_bf16 v[152:155],a[172:175],v[224:227],v[152:155]
v_mfma_f32_16x16x32_bf16 v[156:159],a[188:191],v[224:227],v[32:35]
v_mfma_f32_16x16x32_bf16 v[156:159],a[184:187],v[192:195],v[156:159]
buffer_load_dwordx4 v5,s[20:23],s47 offen offset:3072 nt lds
v_mfma_f32_16x16x32_bf16 v[156:159],a[180:183],v[160:163],v[156:159]
v_mfma_f32_16x16x32_bf16 v[156:159],a[176:179],v[128:131],v[156:159]
v_mfma_f32_16x16x32_bf16 a[64:67],a[192:195],v[200:203],a[64:67]
v_mfma_f32_16x16x32_bf16 a[64:67],a[196:199],v[204:207],a[64:67]
v_mfma_f32_16x16x32_bf16 a[68:71],a[204:207],v[204:207],a[68:71]
v_mfma_f32_16x16x32_bf16 a[68:71],a[200:203],v[200:203],a[68:71]
v_mfma_f32_16x16x32_bf16 a[72:75],a[208:211],v[200:203],a[72:75]
v_mfma_f32_16x16x32_bf16 a[72:75],a[212:215],v[204:207],a[72:75]
v_exp_f32 v136,v136
v_mfma_f32_16x16x32_bf16 a[76:79],a[220:223],v[204:207],a[76:79]
v_exp_f32 v137,v137
v_mfma_f32_16x16x32_bf16 a[76:79],a[216:219],v[200:203],a[76:79]
v_exp_f32 v138,v138
v_mfma_f32_16x16x32_bf16 a[80:83],a[224:227],v[200:203],a[80:83]
s_add_u32 m0,s49,s52
v_mfma_f32_16x16x32_bf16 a[80:83],a[228:231],v[204:207],a[80:83]
s_xor_b32 s49,s50,65536
v_mfma_f32_16x16x32_bf16 a[84:87],a[236:239],v[204:207],a[84:87]
v_mfma_f32_16x16x32_bf16 a[84:87],a[232:235],v[200:203],a[84:87]
buffer_load_dwordx4 v4,s[24:27],s47 offen nt lds
v_mfma_f32_16x16x32_bf16 a[88:91],a[240:243],v[200:203],a[88:91]
v_mfma_f32_16x16x32_bf16 a[88:91],a[244:247],v[204:207],a[88:91]
v_exp_f32 v139,v139
v_mfma_f32_16x16x32_bf16 a[92:95],a[252:255],v[204:207],a[92:95]
v_exp_f32 v140,v140
v_mfma_f32_16x16x32_bf16 a[92:95],a[248:251],v[200:203],a[92:95]
v_exp_f32 v141,v141
v_smfmac_f32_16x16x64_bf16 v[56:59],v[28:31],v[200:207],v27
v_mfma_f32_16x16x32_bf16 v[168:171],a[128:131],v[132:135],v[36:39]
v_exp_f32 v142,v142
v_mfma_f32_16x16x32_bf16 v[168:171],a[132:135],v[164:167],v[168:171]
v_exp_f32 v143,v143
v_mfma_f32_16x16x32_bf16 v[168:171],a[136:139],v[196:199],v[168:171]
v_exp_f32 v152,v152
v_mfma_f32_16x16x32_bf16 v[168:171],a[140:143],v[228:231],v[168:171]
v_exp_f32 v153,v153
v_mfma_f32_16x16x32_bf16 v[172:175],a[156:159],v[228:231],v[36:39]
v_exp_f32 v154,v154
v_mfma_f32_16x16x32_bf16 v[172:175],a[152:155],v[196:199],v[172:175]
v_exp_f32 v155,v155
v_mfma_f32_16x16x32_bf16 v[172:175],a[148:151],v[164:167],v[172:175]
v_exp_f32 v156,v156
v_mfma_f32_16x16x32_bf16 v[172:175],a[144:147],v[132:135],v[172:175]
v_exp_f32 v157,v157
v_mfma_f32_16x16x32_bf16 v[184:187],a[160:163],v[132:135],v[36:39]
buffer_load_dwordx4 v5,s[24:27],s47 offen offset:1024 nt lds
v_mfma_f32_16x16x32_bf16 v[184:187],a[164:167],v[164:167],v[184:187]
v_exp_f32 v158,v158
v_mfma_f32_16x16x32_bf16 v[184:187],a[168:171],v[196:199],v[184:187]
v_exp_f32 v159,v159
v_mfma_f32_16x16x32_bf16 v[184:187],a[172:175],v[228:231],v[184:187]
)A"
R"A(v_exp_f32 v168,v168
v_mfma_f32_16x16x32_bf16 v[188:191],a[188:191],v[228:231],v[36:39]
v_exp_f32 v169,v169
v_mfma_f32_16x16x32_bf16 v[188:191],a[184:187],v[196:199],v[188:191]
v_exp_f32 v170,v170
v_mfma_f32_16x16x32_bf16 v[188:191],a[180:183],v[164:167],v[188:191]
v_exp_f32 v171,v171
v_mfma_f32_16x16x32_bf16 v[188:191],a[176:179],v[132:135],v[188:191]
v_exp_f32 v172,v172
ds_read_b64_tr_b16 a[192:193],v2
v_cvt_pk_bf16_f32 v168,v168,v169
ds_read_b64_tr_b16 a[194:195],v2 offset:4096
v_cvt_pk_bf16_f32 v169,v170,v171
v_exp_f32 v173,v173
ds_read_b64_tr_b16 a[196:197],v2 offset:8192
v_cvt_pk_bf16_f32 v170,v172,v173
ds_read_b64_tr_b16 a[198:199],v2 offset:12288
v_cvt_pk_bf16_f32 v136,v136,v137
v_exp_f32 v174,v174
buffer_load_dwordx4 v4,s[24:27],s47 offen offset:2048 nt lds
ds_read_b64_tr_b16 a[204:205],v3 offset:8192
v_cvt_pk_bf16_f32 v137,v138,v139
v_exp_f32 v175,v175
ds_read_b64_tr_b16 a[206:207],v3 offset:12288
v_cvt_pk_bf16_f32 v171,v174,v175
ds_read_b64_tr_b16 a[200:201],v3
v_cvt_pk_bf16_f32 v138,v140,v141
ds_read_b64_tr_b16 a[202:203],v3 offset:4096
ds_read_b64_tr_b16 a[208:209],v2 offset:256
ds_read_b64_tr_b16 a[210:211],v2 offset:4352
v_cvt_pk_bf16_f32 v139,v142,v143
ds_read_b64_tr_b16 a[212:213],v2 offset:8448
ds_read_b64_tr_b16 a[214:215],v2 offset:12544
ds_read_b64_tr_b16 a[220:221],v3 offset:8448
v_mfma_f32_16x16x32_bf16 v[200:203],a[128:131],v[144:147],v[40:43]
ds_read_b64_tr_b16 a[222:223],v3 offset:12544
v_cvt_pk_bf16_f32 v140,v152,v153
v_mfma_f32_16x16x32_bf16 v[200:203],a[132:135],v[176:179],v[200:203]
ds_read_b64_tr_b16 a[216:217],v3 offset:256
v_mfma_f32_16x16x32_bf16 v[200:203],a[136:139],v[208:211],v[200:203]
ds_read_b64_tr_b16 a[218:219],v3 offset:4352
v_cvt_pk_bf16_f32 v141,v154,v155
v_mfma_f32_16x16x32_bf16 v[200:203],a[140:143],v[240:243],v[200:203]
buffer_load_dwordx4 v5,s[24:27],s47 offen offset:3072 nt lds
v_mfma_f32_16x16x32_bf16 v[204:207],a[156:159],v[240:243],v[40:43]
s_add_u32 s47,s47,16384
s_and_b32 s47,s47,0x3ffff
ds_read_b64_tr_b16 a[224:225],v2 offset:512
v_mfma_f32_16x16x32_bf16 v[204:207],a[152:155],v[208:211],v[204:207]
s_or_b32 s47,s47,s46
ds_read_b64_tr_b16 a[226:227],v2 offset:4608
v_cvt_pk_bf16_f32 v142,v156,v157
v_mfma_f32_16x16x32_bf16 v[204:207],a[148:151],v[176:179],v[204:207]
ds_read_b64_tr_b16 a[228:229],v2 offset:8704
v_mfma_f32_16x16x32_bf16 v[204:207],a[144:147],v[144:147],v[204:207]
ds_read_b64_tr_b16 a[230:231],v2 offset:12800
v_mfma_f32_16x16x32_bf16 v[216:219],a[160:163],v[144:147],v[40:43]
ds_read_b64_tr_b16 a[236:237],v3 offset:8704
v_cvt_pk_bf16_f32 v143,v158,v159
v_mfma_f32_16x16x32_bf16 v[216:219],a[164:167],v[176:179],v[216:219]
ds_read_b64_tr_b16 a[238:239],v3 offset:12800
v_mfma_f32_16x16x32_bf16 v[216:219],a[168:171],v[208:211],v[216:219]
ds_read_b64_tr_b16 a[232:233],v3 offset:512
v_mfma_f32_16x16x32_bf16 v[216:219],a[172:175],v[240:243],v[216:219]
ds_read_b64_tr_b16 a[234:235],v3 offset:4608
v_mfma_f32_16x16x32_bf16 v[220:223],a[188:191],v[240:243],v[40:43]
ds_read_b64_tr_b16 a[240:241],v2 offset:768
v_mfma_f32_16x16x32_bf16 v[220:223],a[184:187],v[208:211],v[220:223]
ds_read_b64_tr_b16 a[242:243],v2 offset:4864
v_mfma_f32_16x16x32_bf16 v[220:223],a[180:183],v[176:179],v[220:223]
ds_read_b64_tr_b16 a[244:245],v2 offset:8960
v_mfma_f32_16x16x32_bf16 v[220:223],a[176:179],v[144:147],v[220:223]
s_waitcnt lgkmcnt(7)
buffer_load_dword v26,v24,s[88:91],s93 offen
v_mfma_f32_16x16x32_bf16 a[0:3],a[192:195],v[136:139],a[0:3]
ds_read_b64_tr_b16 a[246:247],v2 offset:13056
v_mfma_f32_16x16x32_bf16 a[0:3],a[196:199],v[140:143],a[0:3]
ds_read_b64_tr_b16 a[252:253],v3 offset:8960
v_mfma_f32_16x16x32_bf16 a[4:7],a[204:207],v[140:143],a[4:7]
ds_read_b64_tr_b16 a[254:255],v3 offset:13056
v_mfma_f32_16x16x32_bf16 a[4:7],a[200:203],v[136:139],a[4:7]
ds_read_b64_tr_b16 a[248:249],v3 offset:768
v_mfma_f32_16x16x32_bf16 a[8:11],a[208:211],v[136:139],a[8:11]
ds_read_b64_tr_b16 a[250:251],v3 offset:4864
v_mfma_f32_16x16x32_bf16 a[8:11],a[212:215],v[140:143],a[8:11]
v_exp_f32 v184,v184
v_mfma_f32_16x16x32_bf16 a[12:15],a[220:223],v[140:143],a[12:15]
v_exp_f32 v185,v185
v_mfma_f32_16x16x32_bf16 a[12:15],a[216:219],v[136:139],a[12:15]
s_bitcmp1_b32 s45,0
s_cbranch_scc0 L_t31v31_%=
s_mov_b32 m0,s80
s_add_u32 s80,s80,4096
buffer_load_dwordx4 v25,s[24:27],s83 offen nt lds
s_add_u32 s83,s83,4096
L_t31v31_%=:
v_mfma_f32_16x16x32_bf16 a[16:19],a[224:227],v[136:139],a[16:19]
v_exp_f32 v186,v186
v_mfma_f32_16x16x32_bf16 a[16:19],a[228:231],v[140:143],a[16:19]
s_waitcnt lgkmcnt(3)
v_exp_f32 v187,v187
v_mfma_f32_16x16x32_bf16 a[20:23],a[236:239],v[140:143],a[20:23]
v_exp_f32 v188,v188
v_mfma_f32_16x16x32_bf16 a[20:23],a[232:235],v[136:139],a[20:23]
v_exp_f32 v189,v189
v_mfma_f32_16x16x32_bf16 a[24:27],a[240:243],v[136:139],a[24:27]
v_exp_f32 v190,v190
v_mfma_f32_16x16x32_bf16 a[24:27],a[244:247],v[140:143],a[24:27]
s_waitcnt lgkmcnt(0)
v_cvt_pk_bf16_f32 v172,v184,v185
v_mfma_f32_16x16x32_bf16 a[28:31],a[252:255],v[140:143],a[28:31]
v_exp_f32 v191,v191
v_mfma_f32_16x16x32_bf16 a[28:31],a[248:251],v[136:139],a[28:31]
v_cvt_pk_bf16_f32 v173,v186,v187
v_cvt_pk_bf16_f32 v174,v188,v189
v_cvt_pk_bf16_f32 v175,v190,v191
s_cmp_lg_u32 s53,0
v_smfmac_f32_16x16x64_bf16 v[48:51],v[28:31],v[136:143],v27
s_cbranch_scc1 L_att1v3_%=
s_waitcnt lgkmcnt(0)
s_nop 7
s_branch L_att1x_%=
.p2align 5
L_att1v0_%=:
v_mfma_f32_16x16x32_bf16 v[232:235],a[128:131],v[148:151],v[44:47]
s_waitcnt vmcnt(8) lgkmcnt(0)
v_exp_f32 v200,v200
v_mfma_f32_16x16x32_bf16 v[232:235],a[132:135],v[180:183],v[232:235]
s_barrier
s_add_u32 m0,s49,s51
ds_read_b128 a[128:131],v1
v_mfma_f32_16x16x32_bf16 v[232:235],a[136:139],v[212:215],v[232:235]
s_mov_b32 s90,L_out_end_%=-L_pc_%=+128
v_exp_f32 v201,v201
v_mfma_f32_16x16x32_bf16 v[232:235],a[140:143],v[244:247],v[232:235]
s_lshr_b32 s45,s45,1
ds_read_b128 a[132:135],v1 offset:256
v_cvt_pk_bf16_f32 v200,v200,v201
v_mfma_f32_16x16x32_bf16 v[236:239],a[156:159],v[244:247],v[44:47]
buffer_load_dwordx4 v4,s[20:23],s47 offen nt lds
v_mfma_f32_16x16x32_bf16 v[236:239],a[152:155],v[212:215],v[236:239]
ds_read_b128 a[136:139],v1 offset:512
v_add_u32 v2,s48,v2
v_mfma_f32_16x16x32_bf16 v[236:239],a[148:151],v[180:183],v[236:239]
v_exp_f32 v202,v202
v_mfma_f32_16x16x32_bf16 v[236:239],a[144:147],v[148:151],v[236:239]
s_add_u32 s62,s50,32768
ds_read_b128 a[140:143],v1 offset:768
v_add_u32 v3,s48,v3
v_mfma_f32_16x16x32_bf16 v[248:251],a[160:163],v[148:151],v[44:47]
s_and_b32 s62,s62,131071
v_exp_f32 v203,v203
v_mfma_f32_16x16x32_bf16 v[248:251],a[164:167],v[180:183],v[248:251]
s_add_u32 s54,s54,16384
ds_read_b128 a[156:159],v1 offset:4864
v_cvt_pk_bf16_f32 v201,v202,v203
v_mfma_f32_16x16x32_bf16 v[248:251],a[168:171],v[212:215],v[248:251]
s_sub_u32 s48,s62,s50
v_exp_f32 v204,v204
v_mfma_f32_16x16x32_bf16 v[248:251],a[172:175],v[244:247],v[248:251]
s_sub_u32 s53,s53,1
v_exp_f32 v205,v205
v_mfma_f32_16x16x32_bf16 v[252:255],a[188:191],v[244:247],v[44:47]
s_and_b32 s46,s54,0xfffc0000
ds_read_b128 a[152:155],v1 offset:4608
v_cvt_pk_bf16_f32 v202,v204,v205
v_mfma_f32_16x16x32_bf16 v[252:255],a[184:187],v[212:215],v[252:255]
s_mov_b32 s50,s62
v_exp_f32 v206,v206
v_mfma_f32_16x16x32_bf16 v[252:255],a[180:183],v[180:183],v[252:255]
ds_read_b128 a[148:151],v1 offset:4352
v_mfma_f32_16x16x32_bf16 v[252:255],a[176:179],v[148:151],v[252:255]
v_exp_f32 v207,v207
v_mfma_f32_16x16x32_bf16 a[32:35],a[192:195],v[168:171],a[32:35]
ds_read_b128 a[144:147],v1 offset:4096
v_cvt_pk_bf16_f32 v203,v206,v207
v_mfma_f32_16x16x32_bf16 a[32:35],a[196:199],v[172:175],a[32:35]
v_exp_f32 v216,v216
v_mfma_f32_16x16x32_bf16 a[36:39],a[204:207],v[172:175],a[36:39]
buffer_load_dwordx4 v4,s[20:23],s47 offen offset:1024 nt lds
v_mfma_f32_16x16x32_bf16 a[36:39],a[200:203],v[168:171],a[36:39]
ds_read_b128 a[160:163],v1 offset:8192
v_mfma_f32_16x16x32_bf16 a[40:43],a[208:211],v[168:171],a[40:43]
v_exp_f32 v217,v217
v_mfma_f32_16x16x32_bf16 a[40:43],a[212:215],v[172:175],a[40:43]
ds_read_b128 a[164:167],v1 offset:8448
v_cvt_pk_bf16_f32 v204,v216,v217
v_mfma_f32_16x16x32_bf16 a[44:47],a[220:223],v[172:175],a[44:47]
v_exp_f32 v218,v218
v_mfma_f32_16x16x32_bf16 a[44:47],a[216:219],v[168:171],a[44:47]
ds_read_b128 a[168:171],v1 offset:8704
v_mfma_f32_16x16x32_bf16 a[48:51],a[224:227],v[168:171],a[48:51]
v_exp_f32 v219,v219
v_mfma_f32_16x16x32_bf16 a[48:51],a[228:231],v[172:175],a[48:51]
ds_read_b128 a[172:175],v1 offset:8960
v_cvt_pk_bf16_f32 v205,v218,v219
v_mfma_f32_16x16x32_bf16 a[52:55],a[236:239],v[172:175],a[52:55]
v_exp_f32 v220,v220
v_mfma_f32_16x16x32_bf16 a[52:55],a[232:235],v[168:171],a[52:55]
ds_read_b128 a[188:191],v1 offset:13056
v_mfma_f32_16x16x32_bf16 a[56:59],a[240:243],v[168:171],a[56:59]
v_exp_f32 v221,v221
v_mfma_f32_16x16x32_bf16 a[56:59],a[244:247],v[172:175],a[56:59]
ds_read_b128 a[184:187],v1 offset:12800
v_cvt_pk_bf16_f32 v206,v220,v221
v_mfma_f32_16x16x32_bf16 a[60:63],a[252:255],v[172:175],a[60:63]
v_exp_f32 v222,v222
v_mfma_f32_16x16x32_bf16 a[60:63],a[248:251],v[168:171],a[60:63]
ds_read_b128 a[180:183],v1 offset:12544
v_smfmac_f32_16x16x64_bf16 v[52:55],v[28:31],v[168:175],v27
buffer_load_dwordx4 v5,s[20:23],s47 offen offset:2048 nt lds
ds_read_b128 a[176:179],v1 offset:12288
v_add_u32 v1,s48,v1
v_exp_f32 v223,v223
s_nop 0
v_cvt_pk_bf16_f32 v207,v222,v223
v_exp_f32 v232,v232
v_exp_f32 v233,v233
v_exp_f32 v234,v234
v_exp_f32 v235,v235
v_exp_f32 v236,v236
v_exp_f32 v237,v237
v_exp_f32 v238,v238
v_exp_f32 v239,v239
v_exp_f32 v248,v248
buffer_load_dwordx4 v5,s[20:23],s47 offen offset:3072 nt lds
v_exp_f32 v249,v249
v_exp_f32 v250,v250
v_mfma_f32_16x16x32_bf16 a[64:67],a[192:195],v[200:203],a[64:67]
v_exp_f32 v251,v251
v_mfma_f32_16x16x32_bf16 a[64:67],a[196:199],v[204:207],a[64:67]
v_exp_f32 v252,v252
v_mfma_f32_16x16x32_bf16 a[68:71],a[204:207],v[204:207],a[68:71]
v_exp_f32 v253,v253
v_mfma_f32_16x16x32_bf16 a[68:71],a[200:203],v[200:203],a[68:71]
v_exp_f32 v254,v254
v_mfma_f32_16x16x32_bf16 a[72:75],a[208:211],v[200:203],a[72:75]
v_exp_f32 v255,v255
v_mfma_f32_16x16x32_bf16 a[72:75],a[212:215],v[204:207],a[72:75]
v_mfma_f32_16x16x32_bf16 a[76:79],a[220:223],v[204:207],a[76:79]
v_mfma_f32_16x16x32_bf16 a[76:79],a[216:219],v[200:203],a[76:79]
v_mfma_f32_16x16x32_bf16 a[80:83],a[224:227],v[200:203],a[80:83]
s_add_u32 m0,s49,s52
v_cvt_pk_bf16_f32 v232,v232,v233
v_cvt_pk_bf16_f32 v233,v234,v235
v_mfma_f32_16x16x32_bf16 a[80:83],a[228:231],v[204:207],a[80:83]
s_xor_b32 s49,s50,65536
v_cvt_pk_bf16_f32 v234,v236,v237
v_cvt_pk_bf16_f32 v235,v238,v239
v_mfma_f32_16x16x32_bf16 a[84:87],a[236:239],v[204:207],a[84:87]
v_cvt_pk_bf16_f32 v236,v248,v249
v_cvt_pk_bf16_f32 v237,v250,v251
v_mfma_f32_16x16x32_bf16 a[84:87],a[232:235],v[200:203],a[84:87]
buffer_load_dwordx4 v4,s[24:27],s47 offen nt lds
v_mfma_f32_16x16x32_bf16 a[88:91],a[240:243],v[200:203],a[88:91]
v_cvt_pk_bf16_f32 v238,v252,v253
v_cvt_pk_bf16_f32 v239,v254,v255
v_mfma_f32_16x16x32_bf16 a[88:91],a[244:247],v[204:207],a[88:91]
v_mfma_f32_16x16x32_bf16 a[92:95],a[252:255],v[204:207],a[92:95]
v_mfma_f32_16x16x32_bf16 a[92:95],a[248:251],v[200:203],a[92:95]
v_smfmac_f32_16x16x64_bf16 v[56:59],v[28:31],v[200:207],v27
s_waitcnt lgkmcnt(0)
v_mfma_f32_16x16x32_bf16 v[168:171],a[128:131],v[132:135],v[36:39]
v_mfma_f32_16x16x32_bf16 v[168:171],a[132:135],v[164:167],v[168:171]
v_mfma_f32_16x16x32_bf16 v[168:171],a[136:139],v[196:199],v[168:171]
v_mfma_f32_16x16x32_bf16 v[168:171],a[140:143],v[228:231],v[168:171]
v_mfma_f32_16x16x32_bf16 v[172:175],a[156:159],v[228:231],v[36:39]
v_mfma_f32_16x16x32_bf16 v[172:175],a[152:155],v[196:199],v[172:175]
v_mfma_f32_16x16x32_bf16 v[172:175],a[148:151],v[164:167],v[172:175]
v_mfma_f32_16x16x32_bf16 v[172:175],a[144:147],v[132:135],v[172:175]
v_mfma_f32_16x16x32_bf16 v[184:187],a[160:163],v[132:135],v[36:39]
buffer_load_dwordx4 v5,s[24:27],s47 offen offset:1024 nt lds
v_mfma_f32_16x16x32_bf16 v[184:187],a[164:167],v[164:167],v[184:187]
v_mfma_f32_16x16x32_bf16 v[184:187],a[168:171],v[196:199],v[184:187]
v_mfma_f32_16x16x32_bf16 v[184:187],a[172:175],v[228:231],v[184:187]
v_exp_f32 v168,v168
v_mfma_f32_16x16x32_bf16 v[188:191],a[188:191],v[228:231],v[36:39]
)A"
R"A(v_exp_f32 v169,v169
v_mfma_f32_16x16x32_bf16 v[188:191],a[184:187],v[196:199],v[188:191]
v_exp_f32 v170,v170
v_mfma_f32_16x16x32_bf16 v[188:191],a[180:183],v[164:167],v[188:191]
v_exp_f32 v171,v171
v_mfma_f32_16x16x32_bf16 v[188:191],a[176:179],v[132:135],v[188:191]
v_exp_f32 v172,v172
v_mfma_f32_16x16x32_bf16 a[96:99],a[192:195],v[232:235],a[96:99]
ds_read_b64_tr_b16 a[192:193],v2
v_cvt_pk_bf16_f32 v168,v168,v169
v_mfma_f32_16x16x32_bf16 a[96:99],a[196:199],v[236:239],a[96:99]
ds_read_b64_tr_b16 a[194:195],v2 offset:4096
v_cvt_pk_bf16_f32 v169,v170,v171
v_mfma_f32_16x16x32_bf16 a[100:103],a[204:207],v[236:239],a[100:103]
v_exp_f32 v173,v173
v_mfma_f32_16x16x32_bf16 a[100:103],a[200:203],v[232:235],a[100:103]
ds_read_b64_tr_b16 a[196:197],v2 offset:8192
v_cvt_pk_bf16_f32 v170,v172,v173
v_mfma_f32_16x16x32_bf16 a[104:107],a[208:211],v[232:235],a[104:107]
ds_read_b64_tr_b16 a[198:199],v2 offset:12288
v_mfma_f32_16x16x32_bf16 a[104:107],a[212:215],v[236:239],a[104:107]
v_exp_f32 v174,v174
v_mfma_f32_16x16x32_bf16 a[108:111],a[220:223],v[236:239],a[108:111]
buffer_load_dwordx4 v4,s[24:27],s47 offen offset:2048 nt lds
v_mfma_f32_16x16x32_bf16 a[108:111],a[216:219],v[232:235],a[108:111]
ds_read_b64_tr_b16 a[204:205],v3 offset:8192
v_mfma_f32_16x16x32_bf16 a[112:115],a[224:227],v[232:235],a[112:115]
v_exp_f32 v175,v175
v_mfma_f32_16x16x32_bf16 a[112:115],a[228:231],v[236:239],a[112:115]
ds_read_b64_tr_b16 a[206:207],v3 offset:12288
v_cvt_pk_bf16_f32 v171,v174,v175
v_mfma_f32_16x16x32_bf16 a[116:119],a[236:239],v[236:239],a[116:119]
ds_read_b64_tr_b16 a[200:201],v3
v_mfma_f32_16x16x32_bf16 a[116:119],a[232:235],v[232:235],a[116:119]
ds_read_b64_tr_b16 a[202:203],v3 offset:4096
v_mfma_f32_16x16x32_bf16 a[120:123],a[240:243],v[232:235],a[120:123]
ds_read_b64_tr_b16 a[208:209],v2 offset:256
v_mfma_f32_16x16x32_bf16 a[120:123],a[244:247],v[236:239],a[120:123]
ds_read_b64_tr_b16 a[210:211],v2 offset:4352
v_mfma_f32_16x16x32_bf16 a[124:127],a[252:255],v[236:239],a[124:127]
ds_read_b64_tr_b16 a[212:213],v2 offset:8448
v_mfma_f32_16x16x32_bf16 a[124:127],a[248:251],v[232:235],a[124:127]
ds_read_b64_tr_b16 a[214:215],v2 offset:12544
v_smfmac_f32_16x16x64_bf16 v[60:63],v[28:31],v[232:239],v27
ds_read_b64_tr_b16 a[220:221],v3 offset:8448
v_mfma_f32_16x16x32_bf16 v[200:203],a[128:131],v[144:147],v[40:43]
ds_read_b64_tr_b16 a[222:223],v3 offset:12544
v_mfma_f32_16x16x32_bf16 v[200:203],a[132:135],v[176:179],v[200:203]
ds_read_b64_tr_b16 a[216:217],v3 offset:256
v_mfma_f32_16x16x32_bf16 v[200:203],a[136:139],v[208:211],v[200:203]
ds_read_b64_tr_b16 a[218:219],v3 offset:4352
v_mfma_f32_16x16x32_bf16 v[200:203],a[140:143],v[240:243],v[200:203]
buffer_load_dwordx4 v5,s[24:27],s47 offen offset:3072 nt lds
v_mfma_f32_16x16x32_bf16 v[204:207],a[156:159],v[240:243],v[40:43]
s_add_u32 s47,s47,16384
s_and_b32 s47,s47,0x3ffff
ds_read_b64_tr_b16 a[224:225],v2 offset:512
v_mfma_f32_16x16x32_bf16 v[204:207],a[152:155],v[208:211],v[204:207]
s_or_b32 s47,s47,s46
ds_read_b64_tr_b16 a[226:227],v2 offset:4608
v_mfma_f32_16x16x32_bf16 v[204:207],a[148:151],v[176:179],v[204:207]
ds_read_b64_tr_b16 a[228:229],v2 offset:8704
v_mfma_f32_16x16x32_bf16 v[204:207],a[144:147],v[144:147],v[204:207]
ds_read_b64_tr_b16 a[230:231],v2 offset:12800
v_mfma_f32_16x16x32_bf16 v[216:219],a[160:163],v[144:147],v[40:43]
ds_read_b64_tr_b16 a[236:237],v3 offset:8704
v_mfma_f32_16x16x32_bf16 v[216:219],a[164:167],v[176:179],v[216:219]
ds_read_b64_tr_b16 a[238:239],v3 offset:12800
v_mfma_f32_16x16x32_bf16 v[216:219],a[168:171],v[208:211],v[216:219]
ds_read_b64_tr_b16 a[232:233],v3 offset:512
v_mfma_f32_16x16x32_bf16 v[216:219],a[172:175],v[240:243],v[216:219]
ds_read_b64_tr_b16 a[234:235],v3 offset:4608
v_mfma_f32_16x16x32_bf16 v[220:223],a[188:191],v[240:243],v[40:43]
ds_read_b64_tr_b16 a[240:241],v2 offset:768
v_mfma_f32_16x16x32_bf16 v[220:223],a[184:187],v[208:211],v[220:223]
ds_read_b64_tr_b16 a[242:243],v2 offset:4864
v_mfma_f32_16x16x32_bf16 v[220:223],a[180:183],v[176:179],v[220:223]
ds_read_b64_tr_b16 a[244:245],v2 offset:8960
v_mfma_f32_16x16x32_bf16 v[220:223],a[176:179],v[144:147],v[220:223]
buffer_load_dword v26,v24,s[88:91],s93 offen
ds_read_b64_tr_b16 a[246:247],v2 offset:13056
ds_read_b64_tr_b16 a[252:253],v3 offset:8960
ds_read_b64_tr_b16 a[254:255],v3 offset:13056
ds_read_b64_tr_b16 a[248:249],v3 offset:768
ds_read_b64_tr_b16 a[250:251],v3 offset:4864
v_exp_f32 v184,v184
v_exp_f32 v185,v185
s_bitcmp1_b32 s45,0
s_cbranch_scc0 L_t31v01_%=
s_mov_b32 m0,s80
s_add_u32 s80,s80,4096
buffer_load_dwordx4 v25,s[24:27],s83 offen nt lds
s_add_u32 s83,s83,4096
L_t31v01_%=:
v_exp_f32 v186,v186
v_exp_f32 v187,v187
v_exp_f32 v188,v188
v_exp_f32 v189,v189
v_exp_f32 v190,v190
v_cvt_pk_bf16_f32 v172,v184,v185
v_exp_f32 v191,v191
v_cvt_pk_bf16_f32 v173,v186,v187
v_cvt_pk_bf16_f32 v174,v188,v189
v_cvt_pk_bf16_f32 v175,v190,v191
s_cmp_lg_u32 s53,0
s_cbranch_scc1 L_att1v0_%=
s_waitcnt lgkmcnt(0)
s_nop 7
s_branch L_att1x_%=
.p2align 5
L_att1v1_%=:
v_mfma_f32_16x16x32_bf16 v[232:235],a[128:131],v[148:151],v[44:47]
s_waitcnt vmcnt(8) lgkmcnt(0)
v_exp_f32 v200,v200
v_mfma_f32_16x16x32_bf16 v[232:235],a[132:135],v[180:183],v[232:235]
s_barrier
s_add_u32 m0,s49,s51
ds_read_b128 a[128:131],v1
v_mfma_f32_16x16x32_bf16 v[232:235],a[136:139],v[212:215],v[232:235]
s_mov_b32 s90,L_out_end_%=-L_pc_%=+128
v_exp_f32 v201,v201
v_mfma_f32_16x16x32_bf16 v[232:235],a[140:143],v[244:247],v[232:235]
s_lshr_b32 s45,s45,1
ds_read_b128 a[132:135],v1 offset:256
v_cvt_pk_bf16_f32 v200,v200,v201
v_mfma_f32_16x16x32_bf16 v[236:239],a[156:159],v[244:247],v[44:47]
buffer_load_dwordx4 v4,s[20:23],s47 offen nt lds
v_mfma_f32_16x16x32_bf16 v[236:239],a[152:155],v[212:215],v[236:239]
ds_read_b128 a[136:139],v1 offset:512
v_add_u32 v2,s48,v2
v_mfma_f32_16x16x32_bf16 v[236:239],a[148:151],v[180:183],v[236:239]
v_exp_f32 v202,v202
v_mfma_f32_16x16x32_bf16 v[236:239],a[144:147],v[148:151],v[236:239]
s_add_u32 s62,s50,32768
ds_read_b128 a[140:143],v1 offset:768
v_add_u32 v3,s48,v3
v_mfma_f32_16x16x32_bf16 v[248:251],a[160:163],v[148:151],v[44:47]
s_and_b32 s62,s62,131071
v_exp_f32 v203,v203
v_mfma_f32_16x16x32_bf16 v[248:251],a[164:167],v[180:183],v[248:251]
s_add_u32 s54,s54,16384
ds_read_b128 a[156:159],v1 offset:4864
v_cvt_pk_bf16_f32 v201,v202,v203
v_mfma_f32_16x16x32_bf16 v[248:251],a[168:171],v[212:215],v[248:251]
s_sub_u32 s48,s62,s50
v_exp_f32 v204,v204
v_mfma_f32_16x16x32_bf16 v[248:251],a[172:175],v[244:247],v[248:251]
s_sub_u32 s53,s53,1
v_exp_f32 v205,v205
v_mfma_f32_16x16x32_bf16 v[252:255],a[188:191],v[244:247],v[44:47]
s_and_b32 s46,s54,0xfffc0000
ds_read_b128 a[152:155],v1 offset:4608
v_cvt_pk_bf16_f32 v202,v204,v205
v_mfma_f32_16x16x32_bf16 v[252:255],a[184:187],v[212:215],v[252:255]
s_mov_b32 s50,s62
v_exp_f32 v206,v206
v_mfma_f32_16x16x32_bf16 v[252:255],a[180:183],v[180:183],v[252:255]
ds_read_b128 a[148:151],v1 offset:4352
v_mfma_f32_16x16x32_bf16 v[252:255],a[176:179],v[148:151],v[252:255]
s_waitcnt lgkmcnt(3)
v_exp_f32 v207,v207
ds_read_b128 a[144:147],v1 offset:4096
v_cvt_pk_bf16_f32 v203,v206,v207
v_exp_f32 v216,v216
buffer_load_dwordx4 v4,s[20:23],s47 offen offset:1024 nt lds
ds_read_b128 a[160:163],v1 offset:8192
v_exp_f32 v217,v217
ds_read_b128 a[164:167],v1 offset:8448
v_cvt_pk_bf16_f32 v204,v216,v217
v_exp_f32 v218,v218
ds_read_b128 a[168:171],v1 offset:8704
v_exp_f32 v219,v219
ds_read_b128 a[172:175],v1 offset:8960
v_cvt_pk_bf16_f32 v205,v218,v219
v_exp_f32 v220,v220
ds_read_b128 a[188:191],v1 offset:13056
v_exp_f32 v221,v221
ds_read_b128 a[184:187],v1 offset:12800
v_cvt_pk_bf16_f32 v206,v220,v221
v_exp_f32 v222,v222
ds_read_b128 a[180:183],v1 offset:12544
v_mfma_f32_16x16x32_bf16 v[136:139],a[128:131],v[128:131],v[32:35]
buffer_load_dwordx4 v5,s[20:23],s47 offen offset:2048 nt lds
v_mfma_f32_16x16x32_bf16 v[136:139],a[132:135],v[160:163],v[136:139]
ds_read_b128 a[176:179],v1 offset:12288
v_add_u32 v1,s48,v1
v_mfma_f32_16x16x32_bf16 v[136:139],a[136:139],v[192:195],v[136:139]
v_exp_f32 v223,v223
v_mfma_f32_16x16x32_bf16 v[136:139],a[140:143],v[224:227],v[136:139]
s_waitcnt lgkmcnt(11)
v_cvt_pk_bf16_f32 v207,v222,v223
v_mfma_f32_16x16x32_bf16 v[140:143],a[156:159],v[224:227],v[32:35]
s_waitcnt lgkmcnt(10)
v_exp_f32 v232,v232
v_mfma_f32_16x16x32_bf16 v[140:143],a[152:155],v[192:195],v[140:143]
s_waitcnt lgkmcnt(9)
v_exp_f32 v233,v233
v_mfma_f32_16x16x32_bf16 v[140:143],a[148:151],v[160:163],v[140:143]
s_waitcnt lgkmcnt(8)
v_exp_f32 v234,v234
v_mfma_f32_16x16x32_bf16 v[140:143],a[144:147],v[128:131],v[140:143]
s_waitcnt lgkmcnt(1)
v_exp_f32 v235,v235
v_mfma_f32_16x16x32_bf16 v[152:155],a[160:163],v[128:131],v[32:35]
v_exp_f32 v236,v236
v_mfma_f32_16x16x32_bf16 v[152:155],a[164:167],v[160:163],v[152:155]
v_exp_f32 v237,v237
v_mfma_f32_16x16x32_bf16 v[152:155],a[168:171],v[192:195],v[152:155]
v_exp_f32 v238,v238
v_mfma_f32_16x16x32_bf16 v[152:155],a[172:175],v[224:227],v[152:155]
v_exp_f32 v239,v239
v_mfma_f32_16x16x32_bf16 v[156:159],a[188:191],v[224:227],v[32:35]
v_exp_f32 v248,v248
v_mfma_f32_16x16x32_bf16 v[156:159],a[184:187],v[192:195],v[156:159]
buffer_load_dwordx4 v5,s[20:23],s47 offen offset:3072 nt lds
v_mfma_f32_16x16x32_bf16 v[156:159],a[180:183],v[160:163],v[156:159]
s_waitcnt lgkmcnt(0)
v_exp_f32 v249,v249
v_mfma_f32_16x16x32_bf16 v[156:159],a[176:179],v[128:131],v[156:159]
v_exp_f32 v250,v250
v_mfma_f32_16x16x32_bf16 a[64:67],a[192:195],v[200:203],a[64:67]
v_exp_f32 v251,v251
v_mfma_f32_16x16x32_bf16 a[64:67],a[196:199],v[204:207],a[64:67]
v_exp_f32 v252,v252
v_mfma_f32_16x16x32_bf16 a[68:71],a[204:207],v[204:207],a[68:71]
v_exp_f32 v253,v253
v_mfma_f32_16x16x32_bf16 a[68:71],a[200:203],v[200:203],a[68:71]
v_exp_f32 v254,v254
v_mfma_f32_16x16x32_bf16 a[72:75],a[208:211],v[200:203],a[72:75]
v_exp_f32 v255,v255
v_mfma_f32_16x16x32_bf16 a[72:75],a[212:215],v[204:207],a[72:75]
v_exp_f32 v136,v136
v_mfma_f32_16x16x32_bf16 a[76:79],a[220:223],v[204:207],a[76:79]
v_exp_f32 v137,v137
v_mfma_f32_16x16x32_bf16 a[76:79],a[216:219],v[200:203],a[76:79]
v_exp_f32 v138,v138
v_mfma_f32_16x16x32_bf16 a[80:83],a[224:227],v[200:203],a[80:83]
s_add_u32 m0,s49,s52
v_cvt_pk_bf16_f32 v232,v232,v233
v_cvt_pk_bf16_f32 v233,v234,v235
v_mfma_f32_16x16x32_bf16 a[80:83],a[228:231],v[204:207],a[80:83]
s_xor_b32 s49,s50,65536
v_cvt_pk_bf16_f32 v234,v236,v237
v_cvt_pk_bf16_f32 v235,v238,v239
v_mfma_f32_16x16x32_bf16 a[84:87],a[236:239],v[204:207],a[84:87]
v_cvt_pk_bf16_f32 v236,v248,v249
v_cvt_pk_bf16_f32 v237,v250,v251
v_mfma_f32_16x16x32_bf16 a[84:87],a[232:235],v[200:203],a[84:87]
buffer_load_dwordx4 v4,s[24:27],s47 offen nt lds
v_mfma_f32_16x16x32_bf16 a[88:91],a[240:243],v[200:203],a[88:91]
v_cvt_pk_bf16_f32 v238,v252,v253
v_cvt_pk_bf16_f32 v239,v254,v255
v_mfma_f32_16x16x32_bf16 a[88:91],a[244:247],v[204:207],a[88:91]
v_exp_f32 v139,v139
v_mfma_f32_16x16x32_bf16 a[92:95],a[252:255],v[204:207],a[92:95]
v_exp_f32 v140,v140
v_mfma_f32_16x16x32_bf16 a[92:95],a[248:251],v[200:203],a[92:95]
v_exp_f32 v141,v141
v_smfmac_f32_16x16x64_bf16 v[56:59],v[28:31],v[200:207],v27
v_exp_f32 v142,v142
v_exp_f32 v143,v143
v_exp_f32 v152,v152
v_exp_f32 v153,v153
v_exp_f32 v154,v154
v_exp_f32 v155,v155
v_exp_f32 v156,v156
v_exp_f32 v157,v157
buffer_load_dwordx4 v5,s[24:27],s47 offen offset:1024 nt lds
v_exp_f32 v158,v158
v_exp_f32 v159,v159
v_mfma_f32_16x16x32_bf16 a[96:99],a[192:195],v[232:235],a[96:99]
ds_read_b64_tr_b16 a[192:193],v2
v_mfma_f32_16x16x32_bf16 a[96:99],a[196:199],v[236:239],a[96:99]
ds_read_b64_tr_b16 a[194:195],v2 offset:4096
v_mfma_f32_16x16x32_bf16 a[100:103],a[204:207],v[236:239],a[100:103]
v_mfma_f32_16x16x32_bf16 a[100:103],a[200:203],v[232:235],a[100:103]
ds_read_b64_tr_b16 a[196:197],v2 offset:8192
v_mfma_f32_16x16x32_bf16 a[104:107],a[208:211],v[232:235],a[104:107]
ds_read_b64_tr_b16 a[198:199],v2 offset:12288
v_cvt_pk_bf16_f32 v136,v136,v137
v_mfma_f32_16x16x32_bf16 a[104:107],a[212:215],v[236:239],a[104:107]
v_mfma_f32_16x16x32_bf16 a[108:111],a[220:223],v[236:239],a[108:111]
buffer_load_dwordx4 v4,s[24:27],s47 offen offset:2048 nt lds
v_mfma_f32_16x16x32_bf16 a[108:111],a[216:219],v[232:235],a[108:111]
)A"
R"A(ds_read_b64_tr_b16 a[204:205],v3 offset:8192
v_cvt_pk_bf16_f32 v137,v138,v139
v_mfma_f32_16x16x32_bf16 a[112:115],a[224:227],v[232:235],a[112:115]
v_mfma_f32_16x16x32_bf16 a[112:115],a[228:231],v[236:239],a[112:115]
ds_read_b64_tr_b16 a[206:207],v3 offset:12288
v_mfma_f32_16x16x32_bf16 a[116:119],a[236:239],v[236:239],a[116:119]
ds_read_b64_tr_b16 a[200:201],v3
v_cvt_pk_bf16_f32 v138,v140,v141
v_mfma_f32_16x16x32_bf16 a[116:119],a[232:235],v[232:235],a[116:119]
ds_read_b64_tr_b16 a[202:203],v3 offset:4096
v_mfma_f32_16x16x32_bf16 a[120:123],a[240:243],v[232:235],a[120:123]
ds_read_b64_tr_b16 a[208:209],v2 offset:256
v_mfma_f32_16x16x32_bf16 a[120:123],a[244:247],v[236:239],a[120:123]
ds_read_b64_tr_b16 a[210:211],v2 offset:4352
v_cvt_pk_bf16_f32 v139,v142,v143
v_mfma_f32_16x16x32_bf16 a[124:127],a[252:255],v[236:239],a[124:127]
ds_read_b64_tr_b16 a[212:213],v2 offset:8448
v_mfma_f32_16x16x32_bf16 a[124:127],a[248:251],v[232:235],a[124:127]
ds_read_b64_tr_b16 a[214:215],v2 offset:12544
v_smfmac_f32_16x16x64_bf16 v[60:63],v[28:31],v[232:239],v27
ds_read_b64_tr_b16 a[220:221],v3 offset:8448
v_mfma_f32_16x16x32_bf16 v[200:203],a[128:131],v[144:147],v[40:43]
ds_read_b64_tr_b16 a[222:223],v3 offset:12544
v_cvt_pk_bf16_f32 v140,v152,v153
v_mfma_f32_16x16x32_bf16 v[200:203],a[132:135],v[176:179],v[200:203]
ds_read_b64_tr_b16 a[216:217],v3 offset:256
v_mfma_f32_16x16x32_bf16 v[200:203],a[136:139],v[208:211],v[200:203]
ds_read_b64_tr_b16 a[218:219],v3 offset:4352
v_cvt_pk_bf16_f32 v141,v154,v155
v_mfma_f32_16x16x32_bf16 v[200:203],a[140:143],v[240:243],v[200:203]
buffer_load_dwordx4 v5,s[24:27],s47 offen offset:3072 nt lds
v_mfma_f32_16x16x32_bf16 v[204:207],a[156:159],v[240:243],v[40:43]
s_add_u32 s47,s47,16384
s_and_b32 s47,s47,0x3ffff
ds_read_b64_tr_b16 a[224:225],v2 offset:512
v_mfma_f32_16x16x32_bf16 v[204:207],a[152:155],v[208:211],v[204:207]
s_or_b32 s47,s47,s46
ds_read_b64_tr_b16 a[226:227],v2 offset:4608
v_cvt_pk_bf16_f32 v142,v156,v157
v_mfma_f32_16x16x32_bf16 v[204:207],a[148:151],v[176:179],v[204:207]
ds_read_b64_tr_b16 a[228:229],v2 offset:8704
v_mfma_f32_16x16x32_bf16 v[204:207],a[144:147],v[144:147],v[204:207]
ds_read_b64_tr_b16 a[230:231],v2 offset:12800
v_mfma_f32_16x16x32_bf16 v[216:219],a[160:163],v[144:147],v[40:43]
ds_read_b64_tr_b16 a[236:237],v3 offset:8704
v_cvt_pk_bf16_f32 v143,v158,v159
v_mfma_f32_16x16x32_bf16 v[216:219],a[164:167],v[176:179],v[216:219]
ds_read_b64_tr_b16 a[238:239],v3 offset:12800
v_mfma_f32_16x16x32_bf16 v[216:219],a[168:171],v[208:211],v[216:219]
ds_read_b64_tr_b16 a[232:233],v3 offset:512
v_mfma_f32_16x16x32_bf16 v[216:219],a[172:175],v[240:243],v[216:219]
ds_read_b64_tr_b16 a[234:235],v3 offset:4608
v_mfma_f32_16x16x32_bf16 v[220:223],a[188:191],v[240:243],v[40:43]
ds_read_b64_tr_b16 a[240:241],v2 offset:768
v_mfma_f32_16x16x32_bf16 v[220:223],a[184:187],v[208:211],v[220:223]
ds_read_b64_tr_b16 a[242:243],v2 offset:4864
v_mfma_f32_16x16x32_bf16 v[220:223],a[180:183],v[176:179],v[220:223]
ds_read_b64_tr_b16 a[244:245],v2 offset:8960
v_mfma_f32_16x16x32_bf16 v[220:223],a[176:179],v[144:147],v[220:223]
s_waitcnt lgkmcnt(7)
buffer_load_dword v26,v24,s[88:91],s93 offen
v_mfma_f32_16x16x32_bf16 a[0:3],a[192:195],v[136:139],a[0:3]
ds_read_b64_tr_b16 a[246:247],v2 offset:13056
v_mfma_f32_16x16x32_bf16 a[0:3],a[196:199],v[140:143],a[0:3]
ds_read_b64_tr_b16 a[252:253],v3 offset:8960
v_mfma_f32_16x16x32_bf16 a[4:7],a[204:207],v[140:143],a[4:7]
ds_read_b64_tr_b16 a[254:255],v3 offset:13056
v_mfma_f32_16x16x32_bf16 a[4:7],a[200:203],v[136:139],a[4:7]
ds_read_b64_tr_b16 a[248:249],v3 offset:768
v_mfma_f32_16x16x32_bf16 a[8:11],a[208:211],v[136:139],a[8:11]
ds_read_b64_tr_b16 a[250:251],v3 offset:4864
v_mfma_f32_16x16x32_bf16 a[8:11],a[212:215],v[140:143],a[8:11]
v_mfma_f32_16x16x32_bf16 a[12:15],a[220:223],v[140:143],a[12:15]
v_mfma_f32_16x16x32_bf16 a[12:15],a[216:219],v[136:139],a[12:15]
s_bitcmp1_b32 s45,0
s_cbranch_scc0 L_t31v11_%=
s_mov_b32 m0,s80
s_add_u32 s80,s80,4096
buffer_load_dwordx4 v25,s[24:27],s83 offen nt lds
s_add_u32 s83,s83,4096
L_t31v11_%=:
v_mfma_f32_16x16x32_bf16 a[16:19],a[224:227],v[136:139],a[16:19]
v_mfma_f32_16x16x32_bf16 a[16:19],a[228:231],v[140:143],a[16:19]
s_waitcnt lgkmcnt(3)
v_mfma_f32_16x16x32_bf16 a[20:23],a[236:239],v[140:143],a[20:23]
v_mfma_f32_16x16x32_bf16 a[20:23],a[232:235],v[136:139],a[20:23]
v_mfma_f32_16x16x32_bf16 a[24:27],a[240:243],v[136:139],a[24:27]
v_mfma_f32_16x16x32_bf16 a[24:27],a[244:247],v[140:143],a[24:27]
s_waitcnt lgkmcnt(0)
v_mfma_f32_16x16x32_bf16 a[28:31],a[252:255],v[140:143],a[28:31]
v_mfma_f32_16x16x32_bf16 a[28:31],a[248:251],v[136:139],a[28:31]
s_cmp_lg_u32 s53,0
v_smfmac_f32_16x16x64_bf16 v[48:51],v[28:31],v[136:143],v27
s_cbranch_scc1 L_att1v1_%=
s_waitcnt lgkmcnt(0)
s_nop 7
s_branch L_att1x_%=
.p2align 5
L_att1v2_%=:
v_mfma_f32_16x16x32_bf16 v[232:235],a[128:131],v[148:151],v[44:47]
s_waitcnt vmcnt(8) lgkmcnt(0)
v_mfma_f32_16x16x32_bf16 v[232:235],a[132:135],v[180:183],v[232:235]
s_barrier
s_add_u32 m0,s49,s51
ds_read_b128 a[128:131],v1
v_mfma_f32_16x16x32_bf16 v[232:235],a[136:139],v[212:215],v[232:235]
s_mov_b32 s90,L_out_end_%=-L_pc_%=+128
v_mfma_f32_16x16x32_bf16 v[232:235],a[140:143],v[244:247],v[232:235]
s_lshr_b32 s45,s45,1
ds_read_b128 a[132:135],v1 offset:256
v_mfma_f32_16x16x32_bf16 v[236:239],a[156:159],v[244:247],v[44:47]
buffer_load_dwordx4 v4,s[20:23],s47 offen nt lds
v_mfma_f32_16x16x32_bf16 v[236:239],a[152:155],v[212:215],v[236:239]
ds_read_b128 a[136:139],v1 offset:512
v_add_u32 v2,s48,v2
v_mfma_f32_16x16x32_bf16 v[236:239],a[148:151],v[180:183],v[236:239]
v_mfma_f32_16x16x32_bf16 v[236:239],a[144:147],v[148:151],v[236:239]
s_add_u32 s62,s50,32768
ds_read_b128 a[140:143],v1 offset:768
v_add_u32 v3,s48,v3
v_mfma_f32_16x16x32_bf16 v[248:251],a[160:163],v[148:151],v[44:47]
s_and_b32 s62,s62,131071
v_mfma_f32_16x16x32_bf16 v[248:251],a[164:167],v[180:183],v[248:251]
s_add_u32 s54,s54,16384
ds_read_b128 a[156:159],v1 offset:4864
v_mfma_f32_16x16x32_bf16 v[248:251],a[168:171],v[212:215],v[248:251]
s_sub_u32 s48,s62,s50
v_mfma_f32_16x16x32_bf16 v[248:251],a[172:175],v[244:247],v[248:251]
s_sub_u32 s53,s53,1
v_mfma_f32_16x16x32_bf16 v[252:255],a[188:191],v[244:247],v[44:47]
s_and_b32 s46,s54,0xfffc0000
ds_read_b128 a[152:155],v1 offset:4608
v_mfma_f32_16x16x32_bf16 v[252:255],a[184:187],v[212:215],v[252:255]
s_mov_b32 s50,s62
v_mfma_f32_16x16x32_bf16 v[252:255],a[180:183],v[180:183],v[252:255]
ds_read_b128 a[148:151],v1 offset:4352
v_mfma_f32_16x16x32_bf16 v[252:255],a[176:179],v[148:151],v[252:255]
v_mfma_f32_16x16x32_bf16 a[32:35],a[192:195],v[168:171],a[32:35]
ds_read_b128 a[144:147],v1 offset:4096
v_mfma_f32_16x16x32_bf16 a[32:35],a[196:199],v[172:175],a[32:35]
v_mfma_f32_16x16x32_bf16 a[36:39],a[204:207],v[172:175],a[36:39]
buffer_load_dwordx4 v4,s[20:23],s47 offen offset:1024 nt lds
v_mfma_f32_16x16x32_bf16 a[36:39],a[200:203],v[168:171],a[36:39]
ds_read_b128 a[160:163],v1 offset:8192
v_mfma_f32_16x16x32_bf16 a[40:43],a[208:211],v[168:171],a[40:43]
v_mfma_f32_16x16x32_bf16 a[40:43],a[212:215],v[172:175],a[40:43]
ds_read_b128 a[164:167],v1 offset:8448
v_mfma_f32_16x16x32_bf16 a[44:47],a[220:223],v[172:175],a[44:47]
v_mfma_f32_16x16x32_bf16 a[44:47],a[216:219],v[168:171],a[44:47]
ds_read_b128 a[168:171],v1 offset:8704
v_mfma_f32_16x16x32_bf16 a[48:51],a[224:227],v[168:171],a[48:51]
v_mfma_f32_16x16x32_bf16 a[48:51],a[228:231],v[172:175],a[48:51]
ds_read_b128 a[172:175],v1 offset:8960
v_mfma_f32_16x16x32_bf16 a[52:55],a[236:239],v[172:175],a[52:55]
v_mfma_f32_16x16x32_bf16 a[52:55],a[232:235],v[168:171],a[52:55]
ds_read_b128 a[188:191],v1 offset:13056
v_mfma_f32_16x16x32_bf16 a[56:59],a[240:243],v[168:171],a[56:59]
v_mfma_f32_16x16x32_bf16 a[56:59],a[244:247],v[172:175],a[56:59]
ds_read_b128 a[184:187],v1 offset:12800
v_mfma_f32_16x16x32_bf16 a[60:63],a[252:255],v[172:175],a[60:63]
v_mfma_f32_16x16x32_bf16 a[60:63],a[248:251],v[168:171],a[60:63]
ds_read_b128 a[180:183],v1 offset:12544
v_smfmac_f32_16x16x64_bf16 v[52:55],v[28:31],v[168:175],v27
s_waitcnt lgkmcnt(4)
v_mfma_f32_16x16x32_bf16 v[136:139],a[128:131],v[128:131],v[32:35]
buffer_load_dwordx4 v5,s[20:23],s47 offen offset:2048 nt lds
v_mfma_f32_16x16x32_bf16 v[136:139],a[132:135],v[160:163],v[136:139]
ds_read_b128 a[176:179],v1 offset:12288
v_add_u32 v1,s48,v1
v_mfma_f32_16x16x32_bf16 v[136:139],a[136:139],v[192:195],v[136:139]
v_mfma_f32_16x16x32_bf16 v[136:139],a[140:143],v[224:227],v[136:139]
v_mfma_f32_16x16x32_bf16 v[140:143],a[156:159],v[224:227],v[32:35]
v_exp_f32 v232,v232
v_mfma_f32_16x16x32_bf16 v[140:143],a[152:155],v[192:195],v[140:143]
v_exp_f32 v233,v233
v_mfma_f32_16x16x32_bf16 v[140:143],a[148:151],v[160:163],v[140:143]
v_exp_f32 v234,v234
v_mfma_f32_16x16x32_bf16 v[140:143],a[144:147],v[128:131],v[140:143]
v_exp_f32 v235,v235
v_mfma_f32_16x16x32_bf16 v[152:155],a[160:163],v[128:131],v[32:35]
v_exp_f32 v236,v236
v_mfma_f32_16x16x32_bf16 v[152:155],a[164:167],v[160:163],v[152:155]
v_exp_f32 v237,v237
v_mfma_f32_16x16x32_bf16 v[152:155],a[168:171],v[192:195],v[152:155]
s_waitcnt lgkmcnt(0)
v_exp_f32 v238,v238
v_mfma_f32_16x16x32_bf16 v[152:155],a[172:175],v[224:227],v[152:155]
v_exp_f32 v239,v239
v_mfma_f32_16x16x32_bf16 v[156:159],a[188:191],v[224:227],v[32:35]
v_exp_f32 v248,v248
v_mfma_f32_16x16x32_bf16 v[156:159],a[184:187],v[192:195],v[156:159]
buffer_load_dwordx4 v5,s[20:23],s47 offen offset:3072 nt lds
v_mfma_f32_16x16x32_bf16 v[156:159],a[180:183],v[160:163],v[156:159]
v_exp_f32 v249,v249
v_mfma_f32_16x16x32_bf16 v[156:159],a[176:179],v[128:131],v[156:159]
v_exp_f32 v250,v250
v_exp_f32 v251,v251
v_exp_f32 v252,v252
v_exp_f32 v253,v253
v_exp_f32 v254,v254
v_exp_f32 v255,v255
v_exp_f32 v136,v136
v_exp_f32 v137,v137
v_exp_f32 v138,v138
s_add_u32 m0,s49,s52
v_cvt_pk_bf16_f32 v232,v232,v233
v_cvt_pk_bf16_f32 v233,v234,v235
s_xor_b32 s49,s50,65536
v_cvt_pk_bf16_f32 v234,v236,v237
v_cvt_pk_bf16_f32 v235,v238,v239
v_cvt_pk_bf16_f32 v236,v248,v249
v_cvt_pk_bf16_f32 v237,v250,v251
buffer_load_dwordx4 v4,s[24:27],s47 offen nt lds
v_cvt_pk_bf16_f32 v238,v252,v253
v_cvt_pk_bf16_f32 v239,v254,v255
v_exp_f32 v139,v139
v_exp_f32 v140,v140
v_exp_f32 v141,v141
v_mfma_f32_16x16x32_bf16 v[168:171],a[128:131],v[132:135],v[36:39]
v_exp_f32 v142,v142
v_mfma_f32_16x16x32_bf16 v[168:171],a[132:135],v[164:167],v[168:171]
v_exp_f32 v143,v143
v_mfma_f32_16x16x32_bf16 v[168:171],a[136:139],v[196:199],v[168:171]
v_exp_f32 v152,v152
v_mfma_f32_16x16x32_bf16 v[168:171],a[140:143],v[228:231],v[168:171]
v_exp_f32 v153,v153
v_mfma_f32_16x16x32_bf16 v[172:175],a[156:159],v[228:231],v[36:39]
v_exp_f32 v154,v154
v_mfma_f32_16x16x32_bf16 v[172:175],a[152:155],v[196:199],v[172:175]
v_exp_f32 v155,v155
v_mfma_f32_16x16x32_bf16 v[172:175],a[148:151],v[164:167],v[172:175]
v_exp_f32 v156,v156
v_mfma_f32_16x16x32_bf16 v[172:175],a[144:147],v[132:135],v[172:175]
v_exp_f32 v157,v157
v_mfma_f32_16x16x32_bf16 v[184:187],a[160:163],v[132:135],v[36:39]
buffer_load_dwordx4 v5,s[24:27],s47 offen offset:1024 nt lds
v_mfma_f32_16x16x32_bf16 v[184:187],a[164:167],v[164:167],v[184:187]
v_exp_f32 v158,v158
v_mfma_f32_16x16x32_bf16 v[184:187],a[168:171],v[196:199],v[184:187]
v_exp_f32 v159,v159
v_mfma_f32_16x16x32_bf16 v[184:187],a[172:175],v[228:231],v[184:187]
v_exp_f32 v168,v168
v_mfma_f32_16x16x32_bf16 v[188:191],a[188:191],v[228:231],v[36:39]
v_exp_f32 v169,v169
v_mfma_f32_16x16x32_bf16 v[188:191],a[184:187],v[196:199],v[188:191]
v_exp_f32 v170,v170
v_mfma_f32_16x16x32_bf16 v[188:191],a[180:183],v[164:167],v[188:191]
v_exp_f32 v171,v171
v_mfma_f32_16x16x32_bf16 v[188:191],a[176:179],v[132:135],v[188:191]
v_exp_f32 v172,v172
v_mfma_f32_16x16x32_bf16 a[96:99],a[192:195],v[232:235],a[96:99]
ds_read_b64_tr_b16 a[192:193],v2
v_cvt_pk_bf16_f32 v168,v168,v169
v_mfma_f32_16x16x32_bf16 a[96:99],a[196:199],v[236:239],a[96:99]
ds_read_b64_tr_b16 a[194:195],v2 offset:4096
v_cvt_pk_bf16_f32 v169,v170,v171
v_mfma_f32_16x16x32_bf16 a[100:103],a[204:207],v[236:239],a[100:103]
v_exp_f32 v173,v173
v_mfma_f32_16x16x32_bf16 a[100:103],a[200:203],v[232:235],a[100:103]
)A"
R"A(ds_read_b64_tr_b16 a[196:197],v2 offset:8192
v_cvt_pk_bf16_f32 v170,v172,v173
v_mfma_f32_16x16x32_bf16 a[104:107],a[208:211],v[232:235],a[104:107]
ds_read_b64_tr_b16 a[198:199],v2 offset:12288
v_cvt_pk_bf16_f32 v136,v136,v137
v_mfma_f32_16x16x32_bf16 a[104:107],a[212:215],v[236:239],a[104:107]
v_exp_f32 v174,v174
v_mfma_f32_16x16x32_bf16 a[108:111],a[220:223],v[236:239],a[108:111]
buffer_load_dwordx4 v4,s[24:27],s47 offen offset:2048 nt lds
v_mfma_f32_16x16x32_bf16 a[108:111],a[216:219],v[232:235],a[108:111]
ds_read_b64_tr_b16 a[204:205],v3 offset:8192
v_cvt_pk_bf16_f32 v137,v138,v139
v_mfma_f32_16x16x32_bf16 a[112:115],a[224:227],v[232:235],a[112:115]
v_exp_f32 v175,v175
v_mfma_f32_16x16x32_bf16 a[112:115],a[228:231],v[236:239],a[112:115]
ds_read_b64_tr_b16 a[206:207],v3 offset:12288
v_cvt_pk_bf16_f32 v171,v174,v175
v_mfma_f32_16x16x32_bf16 a[116:119],a[236:239],v[236:239],a[116:119]
ds_read_b64_tr_b16 a[200:201],v3
v_cvt_pk_bf16_f32 v138,v140,v141
v_mfma_f32_16x16x32_bf16 a[116:119],a[232:235],v[232:235],a[116:119]
ds_read_b64_tr_b16 a[202:203],v3 offset:4096
v_mfma_f32_16x16x32_bf16 a[120:123],a[240:243],v[232:235],a[120:123]
ds_read_b64_tr_b16 a[208:209],v2 offset:256
v_mfma_f32_16x16x32_bf16 a[120:123],a[244:247],v[236:239],a[120:123]
ds_read_b64_tr_b16 a[210:211],v2 offset:4352
v_cvt_pk_bf16_f32 v139,v142,v143
v_mfma_f32_16x16x32_bf16 a[124:127],a[252:255],v[236:239],a[124:127]
ds_read_b64_tr_b16 a[212:213],v2 offset:8448
v_mfma_f32_16x16x32_bf16 a[124:127],a[248:251],v[232:235],a[124:127]
ds_read_b64_tr_b16 a[214:215],v2 offset:12544
v_smfmac_f32_16x16x64_bf16 v[60:63],v[28:31],v[232:239],v27
s_waitcnt lgkmcnt(7)
ds_read_b64_tr_b16 a[220:221],v3 offset:8448
ds_read_b64_tr_b16 a[222:223],v3 offset:12544
v_cvt_pk_bf16_f32 v140,v152,v153
ds_read_b64_tr_b16 a[216:217],v3 offset:256
ds_read_b64_tr_b16 a[218:219],v3 offset:4352
v_cvt_pk_bf16_f32 v141,v154,v155
buffer_load_dwordx4 v5,s[24:27],s47 offen offset:3072 nt lds
s_add_u32 s47,s47,16384
s_and_b32 s47,s47,0x3ffff
ds_read_b64_tr_b16 a[224:225],v2 offset:512
s_or_b32 s47,s47,s46
ds_read_b64_tr_b16 a[226:227],v2 offset:4608
v_cvt_pk_bf16_f32 v142,v156,v157
ds_read_b64_tr_b16 a[228:229],v2 offset:8704
ds_read_b64_tr_b16 a[230:231],v2 offset:12800
ds_read_b64_tr_b16 a[236:237],v3 offset:8704
v_cvt_pk_bf16_f32 v143,v158,v159
ds_read_b64_tr_b16 a[238:239],v3 offset:12800
ds_read_b64_tr_b16 a[232:233],v3 offset:512
ds_read_b64_tr_b16 a[234:235],v3 offset:4608
ds_read_b64_tr_b16 a[240:241],v2 offset:768
ds_read_b64_tr_b16 a[242:243],v2 offset:4864
ds_read_b64_tr_b16 a[244:245],v2 offset:8960
buffer_load_dword v26,v24,s[88:91],s93 offen
v_mfma_f32_16x16x32_bf16 a[0:3],a[192:195],v[136:139],a[0:3]
ds_read_b64_tr_b16 a[246:247],v2 offset:13056
v_mfma_f32_16x16x32_bf16 a[0:3],a[196:199],v[140:143],a[0:3]
s_waitcnt lgkmcnt(15)
ds_read_b64_tr_b16 a[252:253],v3 offset:8960
v_mfma_f32_16x16x32_bf16 a[4:7],a[204:207],v[140:143],a[4:7]
ds_read_b64_tr_b16 a[254:255],v3 offset:13056
v_mfma_f32_16x16x32_bf16 a[4:7],a[200:203],v[136:139],a[4:7]
ds_read_b64_tr_b16 a[248:249],v3 offset:768
v_mfma_f32_16x16x32_bf16 a[8:11],a[208:211],v[136:139],a[8:11]
ds_read_b64_tr_b16 a[250:251],v3 offset:4864
v_mfma_f32_16x16x32_bf16 a[8:11],a[212:215],v[140:143],a[8:11]
s_waitcnt lgkmcnt(15)
v_exp_f32 v184,v184
v_mfma_f32_16x16x32_bf16 a[12:15],a[220:223],v[140:143],a[12:15]
v_exp_f32 v185,v185
v_mfma_f32_16x16x32_bf16 a[12:15],a[216:219],v[136:139],a[12:15]
s_bitcmp1_b32 s45,0
s_cbranch_scc0 L_t31v21_%=
s_mov_b32 m0,s80
s_add_u32 s80,s80,4096
buffer_load_dwordx4 v25,s[24:27],s83 offen nt lds
s_add_u32 s83,s83,4096
L_t31v21_%=:
s_waitcnt lgkmcnt(5)
v_mfma_f32_16x16x32_bf16 a[16:19],a[224:227],v[136:139],a[16:19]
v_exp_f32 v186,v186
v_mfma_f32_16x16x32_bf16 a[16:19],a[228:231],v[140:143],a[16:19]
v_exp_f32 v187,v187
v_mfma_f32_16x16x32_bf16 a[20:23],a[236:239],v[140:143],a[20:23]
v_exp_f32 v188,v188
v_mfma_f32_16x16x32_bf16 a[20:23],a[232:235],v[136:139],a[20:23]
v_exp_f32 v189,v189
v_mfma_f32_16x16x32_bf16 a[24:27],a[240:243],v[136:139],a[24:27]
s_waitcnt lgkmcnt(0)
v_exp_f32 v190,v190
v_mfma_f32_16x16x32_bf16 a[24:27],a[244:247],v[140:143],a[24:27]
v_cvt_pk_bf16_f32 v172,v184,v185
v_mfma_f32_16x16x32_bf16 a[28:31],a[252:255],v[140:143],a[28:31]
v_exp_f32 v191,v191
v_mfma_f32_16x16x32_bf16 a[28:31],a[248:251],v[136:139],a[28:31]
v_cvt_pk_bf16_f32 v173,v186,v187
v_cvt_pk_bf16_f32 v174,v188,v189
v_cvt_pk_bf16_f32 v175,v190,v191
s_cmp_lg_u32 s53,0
v_smfmac_f32_16x16x64_bf16 v[48:51],v[28:31],v[136:143],v27
s_cbranch_scc1 L_att1v2_%=
s_waitcnt lgkmcnt(0)
s_nop 7
s_branch L_att1x_%=
.p2align 5
L_att1_%=:
v_mfma_f32_16x16x32_bf16 v[232:235],a[128:131],v[148:151],v[44:47]
s_waitcnt vmcnt(8) lgkmcnt(0)
v_exp_f32 v200,v200
v_mfma_f32_16x16x32_bf16 v[232:235],a[132:135],v[180:183],v[232:235]
s_barrier
s_add_u32 m0,s49,s51
ds_read_b128 a[128:131],v1
v_mfma_f32_16x16x32_bf16 v[232:235],a[136:139],v[212:215],v[232:235]
s_mov_b32 s90,L_out_end_%=-L_pc_%=+128
v_exp_f32 v201,v201
v_mfma_f32_16x16x32_bf16 v[232:235],a[140:143],v[244:247],v[232:235]
s_lshr_b32 s45,s45,1
ds_read_b128 a[132:135],v1 offset:256
v_cvt_pk_bf16_f32 v200,v200,v201
v_mfma_f32_16x16x32_bf16 v[236:239],a[156:159],v[244:247],v[44:47]
s_sub_u32 s94,s94,3
buffer_load_dwordx4 v4,s[20:23],s47 offen nt lds
v_mfma_f32_16x16x32_bf16 v[236:239],a[152:155],v[212:215],v[236:239]
s_lshr_b32 s60,s94,2
ds_read_b128 a[136:139],v1 offset:512
v_add_u32 v2,s48,v2
v_mfma_f32_16x16x32_bf16 v[236:239],a[148:151],v[180:183],v[236:239]
s_max_i32 s60,s60,128
v_exp_f32 v202,v202
v_mfma_f32_16x16x32_bf16 v[236:239],a[144:147],v[148:151],v[236:239]
s_add_u32 s62,s50,32768
ds_read_b128 a[140:143],v1 offset:768
v_add_u32 v3,s48,v3
v_mfma_f32_16x16x32_bf16 v[248:251],a[160:163],v[148:151],v[44:47]
s_and_b32 s62,s62,131071
v_exp_f32 v203,v203
v_mfma_f32_16x16x32_bf16 v[248:251],a[164:167],v[180:183],v[248:251]
s_add_u32 s54,s54,16384
ds_read_b128 a[156:159],v1 offset:4864
v_cvt_pk_bf16_f32 v201,v202,v203
v_mfma_f32_16x16x32_bf16 v[248:251],a[168:171],v[212:215],v[248:251]
s_sub_u32 s48,s62,s50
v_exp_f32 v204,v204
v_mfma_f32_16x16x32_bf16 v[248:251],a[172:175],v[244:247],v[248:251]
s_sub_u32 s53,s53,1
v_exp_f32 v205,v205
v_mfma_f32_16x16x32_bf16 v[252:255],a[188:191],v[244:247],v[44:47]
s_and_b32 s46,s54,0xfffc0000
ds_read_b128 a[152:155],v1 offset:4608
v_cvt_pk_bf16_f32 v202,v204,v205
v_mfma_f32_16x16x32_bf16 v[252:255],a[184:187],v[212:215],v[252:255]
s_mov_b32 s50,s62
v_exp_f32 v206,v206
v_mfma_f32_16x16x32_bf16 v[252:255],a[180:183],v[180:183],v[252:255]
ds_read_b128 a[148:151],v1 offset:4352
v_mfma_f32_16x16x32_bf16 v[252:255],a[176:179],v[148:151],v[252:255]
v_exp_f32 v207,v207
v_mfma_f32_16x16x32_bf16 a[32:35],a[192:195],v[168:171],a[32:35]
ds_read_b128 a[144:147],v1 offset:4096
v_cvt_pk_bf16_f32 v203,v206,v207
v_mfma_f32_16x16x32_bf16 a[32:35],a[196:199],v[172:175],a[32:35]
v_exp_f32 v216,v216
v_mfma_f32_16x16x32_bf16 a[36:39],a[204:207],v[172:175],a[36:39]
buffer_load_dwordx4 v4,s[20:23],s47 offen offset:1024 nt lds
v_mfma_f32_16x16x32_bf16 a[36:39],a[200:203],v[168:171],a[36:39]
ds_read_b128 a[160:163],v1 offset:8192
v_mfma_f32_16x16x32_bf16 a[40:43],a[208:211],v[168:171],a[40:43]
v_exp_f32 v217,v217
v_mfma_f32_16x16x32_bf16 a[40:43],a[212:215],v[172:175],a[40:43]
ds_read_b128 a[164:167],v1 offset:8448
v_cvt_pk_bf16_f32 v204,v216,v217
v_mfma_f32_16x16x32_bf16 a[44:47],a[220:223],v[172:175],a[44:47]
v_exp_f32 v218,v218
v_mfma_f32_16x16x32_bf16 a[44:47],a[216:219],v[168:171],a[44:47]
ds_read_b128 a[168:171],v1 offset:8704
v_mfma_f32_16x16x32_bf16 a[48:51],a[224:227],v[168:171],a[48:51]
v_exp_f32 v219,v219
v_mfma_f32_16x16x32_bf16 a[48:51],a[228:231],v[172:175],a[48:51]
ds_read_b128 a[172:175],v1 offset:8960
v_cvt_pk_bf16_f32 v205,v218,v219
v_mfma_f32_16x16x32_bf16 a[52:55],a[236:239],v[172:175],a[52:55]
v_exp_f32 v220,v220
v_mfma_f32_16x16x32_bf16 a[52:55],a[232:235],v[168:171],a[52:55]
ds_read_b128 a[188:191],v1 offset:13056
v_mfma_f32_16x16x32_bf16 a[56:59],a[240:243],v[168:171],a[56:59]
v_exp_f32 v221,v221
v_mfma_f32_16x16x32_bf16 a[56:59],a[244:247],v[172:175],a[56:59]
ds_read_b128 a[184:187],v1 offset:12800
v_cvt_pk_bf16_f32 v206,v220,v221
v_mfma_f32_16x16x32_bf16 a[60:63],a[252:255],v[172:175],a[60:63]
v_exp_f32 v222,v222
v_mfma_f32_16x16x32_bf16 a[60:63],a[248:251],v[168:171],a[60:63]
ds_read_b128 a[180:183],v1 offset:12544
v_smfmac_f32_16x16x64_bf16 v[52:55],v[28:31],v[168:175],v27
s_waitcnt lgkmcnt(4)
v_mfma_f32_16x16x32_bf16 v[136:139],a[128:131],v[128:131],v[32:35]
buffer_load_dwordx4 v5,s[20:23],s47 offen offset:2048 nt lds
v_mfma_f32_16x16x32_bf16 v[136:139],a[132:135],v[160:163],v[136:139]
ds_read_b128 a[176:179],v1 offset:12288
v_add_u32 v1,s48,v1
v_mfma_f32_16x16x32_bf16 v[136:139],a[136:139],v[192:195],v[136:139]
v_exp_f32 v223,v223
v_mfma_f32_16x16x32_bf16 v[136:139],a[140:143],v[224:227],v[136:139]
v_cvt_pk_bf16_f32 v207,v222,v223
v_mfma_f32_16x16x32_bf16 v[140:143],a[156:159],v[224:227],v[32:35]
v_exp_f32 v232,v232
v_mfma_f32_16x16x32_bf16 v[140:143],a[152:155],v[192:195],v[140:143]
v_exp_f32 v233,v233
v_mfma_f32_16x16x32_bf16 v[140:143],a[148:151],v[160:163],v[140:143]
v_exp_f32 v234,v234
v_mfma_f32_16x16x32_bf16 v[140:143],a[144:147],v[128:131],v[140:143]
v_exp_f32 v235,v235
v_mfma_f32_16x16x32_bf16 v[152:155],a[160:163],v[128:131],v[32:35]
v_exp_f32 v236,v236
v_mfma_f32_16x16x32_bf16 v[152:155],a[164:167],v[160:163],v[152:155]
v_exp_f32 v237,v237
v_mfma_f32_16x16x32_bf16 v[152:155],a[168:171],v[192:195],v[152:155]
s_waitcnt lgkmcnt(0)
v_exp_f32 v238,v238
v_mfma_f32_16x16x32_bf16 v[152:155],a[172:175],v[224:227],v[152:155]
v_exp_f32 v239,v239
v_mfma_f32_16x16x32_bf16 v[156:159],a[188:191],v[224:227],v[32:35]
v_exp_f32 v248,v248
v_mfma_f32_16x16x32_bf16 v[156:159],a[184:187],v[192:195],v[156:159]
buffer_load_dwordx4 v5,s[20:23],s47 offen offset:3072 nt lds
v_mfma_f32_16x16x32_bf16 v[156:159],a[180:183],v[160:163],v[156:159]
v_exp_f32 v249,v249
v_mfma_f32_16x16x32_bf16 v[156:159],a[176:179],v[128:131],v[156:159]
v_exp_f32 v250,v250
v_mfma_f32_16x16x32_bf16 a[64:67],a[192:195],v[200:203],a[64:67]
v_exp_f32 v251,v251
v_mfma_f32_16x16x32_bf16 a[64:67],a[196:199],v[204:207],a[64:67]
v_exp_f32 v252,v252
v_mfma_f32_16x16x32_bf16 a[68:71],a[204:207],v[204:207],a[68:71]
v_exp_f32 v253,v253
v_mfma_f32_16x16x32_bf16 a[68:71],a[200:203],v[200:203],a[68:71]
v_exp_f32 v254,v254
v_mfma_f32_16x16x32_bf16 a[72:75],a[208:211],v[200:203],a[72:75]
v_exp_f32 v255,v255
v_mfma_f32_16x16x32_bf16 a[72:75],a[212:215],v[204:207],a[72:75]
v_exp_f32 v136,v136
v_mfma_f32_16x16x32_bf16 a[76:79],a[220:223],v[204:207],a[76:79]
v_exp_f32 v137,v137
v_mfma_f32_16x16x32_bf16 a[76:79],a[216:219],v[200:203],a[76:79]
v_exp_f32 v138,v138
v_mfma_f32_16x16x32_bf16 a[80:83],a[224:227],v[200:203],a[80:83]
s_add_u32 m0,s49,s52
v_cvt_pk_bf16_f32 v232,v232,v233
v_cvt_pk_bf16_f32 v233,v234,v235
v_mfma_f32_16x16x32_bf16 a[80:83],a[228:231],v[204:207],a[80:83]
s_xor_b32 s49,s50,65536
v_cvt_pk_bf16_f32 v234,v236,v237
v_cvt_pk_bf16_f32 v235,v238,v239
v_mfma_f32_16x16x32_bf16 a[84:87],a[236:239],v[204:207],a[84:87]
v_cvt_pk_bf16_f32 v236,v248,v249
v_cvt_pk_bf16_f32 v237,v250,v251
v_mfma_f32_16x16x32_bf16 a[84:87],a[232:235],v[200:203],a[84:87]
buffer_load_dwordx4 v4,s[24:27],s47 offen nt lds
v_mfma_f32_16x16x32_bf16 a[88:91],a[240:243],v[200:203],a[88:91]
v_cvt_pk_bf16_f32 v238,v252,v253
v_cvt_pk_bf16_f32 v239,v254,v255
v_mfma_f32_16x16x32_bf16 a[88:91],a[244:247],v[204:207],a[88:91]
v_exp_f32 v139,v139
v_mfma_f32_16x16x32_bf16 a[92:95],a[252:255],v[204:207],a[92:95]
v_exp_f32 v140,v140
v_mfma_f32_16x16x32_bf16 a[92:95],a[248:251],v[200:203],a[92:95]
v_exp_f32 v141,v141
v_smfmac_f32_16x16x64_bf16 v[56:59],v[28:31],v[200:207],v27
v_mfma_f32_16x16x32_bf16 v[168:171],a[128:131],v[132:135],v[36:39]
v_exp_f32 v142,v142
v_mfma_f32_16x16x32_bf16 v[168:171],a[132:135],v[164:167],v[168:171]
v_exp_f32 v143,v143
v_mfma_f32_16x16x32_bf16 v[168:171],a[136:139],v[196:199],v[168:171]
v_exp_f32 v152,v152
v_mfma_f32_16x16x32_bf16 v[168:171],a[140:143],v[228:231],v[168:171]
)A"
R"A(v_exp_f32 v153,v153
v_mfma_f32_16x16x32_bf16 v[172:175],a[156:159],v[228:231],v[36:39]
v_exp_f32 v154,v154
v_mfma_f32_16x16x32_bf16 v[172:175],a[152:155],v[196:199],v[172:175]
v_exp_f32 v155,v155
v_mfma_f32_16x16x32_bf16 v[172:175],a[148:151],v[164:167],v[172:175]
v_exp_f32 v156,v156
v_mfma_f32_16x16x32_bf16 v[172:175],a[144:147],v[132:135],v[172:175]
v_exp_f32 v157,v157
v_mfma_f32_16x16x32_bf16 v[184:187],a[160:163],v[132:135],v[36:39]
buffer_load_dwordx4 v5,s[24:27],s47 offen offset:1024 nt lds
v_mfma_f32_16x16x32_bf16 v[184:187],a[164:167],v[164:167],v[184:187]
v_exp_f32 v158,v158
v_mfma_f32_16x16x32_bf16 v[184:187],a[168:171],v[196:199],v[184:187]
v_exp_f32 v159,v159
v_mfma_f32_16x16x32_bf16 v[184:187],a[172:175],v[228:231],v[184:187]
v_exp_f32 v168,v168
v_mfma_f32_16x16x32_bf16 v[188:191],a[188:191],v[228:231],v[36:39]
v_exp_f32 v169,v169
v_mfma_f32_16x16x32_bf16 v[188:191],a[184:187],v[196:199],v[188:191]
v_exp_f32 v170,v170
v_mfma_f32_16x16x32_bf16 v[188:191],a[180:183],v[164:167],v[188:191]
v_exp_f32 v171,v171
v_mfma_f32_16x16x32_bf16 v[188:191],a[176:179],v[132:135],v[188:191]
v_exp_f32 v172,v172
v_mfma_f32_16x16x32_bf16 a[96:99],a[192:195],v[232:235],a[96:99]
ds_read_b64_tr_b16 a[192:193],v2
v_cvt_pk_bf16_f32 v168,v168,v169
v_mfma_f32_16x16x32_bf16 a[96:99],a[196:199],v[236:239],a[96:99]
ds_read_b64_tr_b16 a[194:195],v2 offset:4096
v_cvt_pk_bf16_f32 v169,v170,v171
v_mfma_f32_16x16x32_bf16 a[100:103],a[204:207],v[236:239],a[100:103]
v_exp_f32 v173,v173
v_mfma_f32_16x16x32_bf16 a[100:103],a[200:203],v[232:235],a[100:103]
ds_read_b64_tr_b16 a[196:197],v2 offset:8192
v_cvt_pk_bf16_f32 v170,v172,v173
v_mfma_f32_16x16x32_bf16 a[104:107],a[208:211],v[232:235],a[104:107]
ds_read_b64_tr_b16 a[198:199],v2 offset:12288
v_cvt_pk_bf16_f32 v136,v136,v137
v_mfma_f32_16x16x32_bf16 a[104:107],a[212:215],v[236:239],a[104:107]
v_exp_f32 v174,v174
v_mfma_f32_16x16x32_bf16 a[108:111],a[220:223],v[236:239],a[108:111]
buffer_load_dwordx4 v4,s[24:27],s47 offen offset:2048 nt lds
v_mfma_f32_16x16x32_bf16 a[108:111],a[216:219],v[232:235],a[108:111]
ds_read_b64_tr_b16 a[204:205],v3 offset:8192
v_cvt_pk_bf16_f32 v137,v138,v139
v_mfma_f32_16x16x32_bf16 a[112:115],a[224:227],v[232:235],a[112:115]
v_exp_f32 v175,v175
v_mfma_f32_16x16x32_bf16 a[112:115],a[228:231],v[236:239],a[112:115]
ds_read_b64_tr_b16 a[206:207],v3 offset:12288
v_cvt_pk_bf16_f32 v171,v174,v175
v_mfma_f32_16x16x32_bf16 a[116:119],a[236:239],v[236:239],a[116:119]
ds_read_b64_tr_b16 a[200:201],v3
v_cvt_pk_bf16_f32 v138,v140,v141
v_mfma_f32_16x16x32_bf16 a[116:119],a[232:235],v[232:235],a[116:119]
ds_read_b64_tr_b16 a[202:203],v3 offset:4096
v_mfma_f32_16x16x32_bf16 a[120:123],a[240:243],v[232:235],a[120:123]
ds_read_b64_tr_b16 a[208:209],v2 offset:256
v_mfma_f32_16x16x32_bf16 a[120:123],a[244:247],v[236:239],a[120:123]
ds_read_b64_tr_b16 a[210:211],v2 offset:4352
v_cvt_pk_bf16_f32 v139,v142,v143
v_mfma_f32_16x16x32_bf16 a[124:127],a[252:255],v[236:239],a[124:127]
ds_read_b64_tr_b16 a[212:213],v2 offset:8448
v_mfma_f32_16x16x32_bf16 a[124:127],a[248:251],v[232:235],a[124:127]
ds_read_b64_tr_b16 a[214:215],v2 offset:12544
v_smfmac_f32_16x16x64_bf16 v[60:63],v[28:31],v[232:239],v27
ds_read_b64_tr_b16 a[220:221],v3 offset:8448
v_mfma_f32_16x16x32_bf16 v[200:203],a[128:131],v[144:147],v[40:43]
ds_read_b64_tr_b16 a[222:223],v3 offset:12544
v_cvt_pk_bf16_f32 v140,v152,v153
v_mfma_f32_16x16x32_bf16 v[200:203],a[132:135],v[176:179],v[200:203]
ds_read_b64_tr_b16 a[216:217],v3 offset:256
v_mfma_f32_16x16x32_bf16 v[200:203],a[136:139],v[208:211],v[200:203]
ds_read_b64_tr_b16 a[218:219],v3 offset:4352
v_cvt_pk_bf16_f32 v141,v154,v155
v_mfma_f32_16x16x32_bf16 v[200:203],a[140:143],v[240:243],v[200:203]
buffer_load_dwordx4 v5,s[24:27],s47 offen offset:3072 nt lds
v_mfma_f32_16x16x32_bf16 v[204:207],a[156:159],v[240:243],v[40:43]
s_add_u32 s47,s47,16384
s_and_b32 s47,s47,0x3ffff
ds_read_b64_tr_b16 a[224:225],v2 offset:512
v_mfma_f32_16x16x32_bf16 v[204:207],a[152:155],v[208:211],v[204:207]
s_or_b32 s47,s47,s46
ds_read_b64_tr_b16 a[226:227],v2 offset:4608
v_cvt_pk_bf16_f32 v142,v156,v157
v_mfma_f32_16x16x32_bf16 v[204:207],a[148:151],v[176:179],v[204:207]
ds_read_b64_tr_b16 a[228:229],v2 offset:8704
v_mfma_f32_16x16x32_bf16 v[204:207],a[144:147],v[144:147],v[204:207]
ds_read_b64_tr_b16 a[230:231],v2 offset:12800
v_mfma_f32_16x16x32_bf16 v[216:219],a[160:163],v[144:147],v[40:43]
ds_read_b64_tr_b16 a[236:237],v3 offset:8704
v_cvt_pk_bf16_f32 v143,v158,v159
v_mfma_f32_16x16x32_bf16 v[216:219],a[164:167],v[176:179],v[216:219]
ds_read_b64_tr_b16 a[238:239],v3 offset:12800
v_mfma_f32_16x16x32_bf16 v[216:219],a[168:171],v[208:211],v[216:219]
ds_read_b64_tr_b16 a[232:233],v3 offset:512
v_mfma_f32_16x16x32_bf16 v[216:219],a[172:175],v[240:243],v[216:219]
ds_read_b64_tr_b16 a[234:235],v3 offset:4608
v_mfma_f32_16x16x32_bf16 v[220:223],a[188:191],v[240:243],v[40:43]
ds_read_b64_tr_b16 a[240:241],v2 offset:768
v_mfma_f32_16x16x32_bf16 v[220:223],a[184:187],v[208:211],v[220:223]
ds_read_b64_tr_b16 a[242:243],v2 offset:4864
v_mfma_f32_16x16x32_bf16 v[220:223],a[180:183],v[176:179],v[220:223]
ds_read_b64_tr_b16 a[244:245],v2 offset:8960
v_mfma_f32_16x16x32_bf16 v[220:223],a[176:179],v[144:147],v[220:223]
s_waitcnt lgkmcnt(7)
buffer_load_dword v26,v24,s[88:91],s93 offen
v_mfma_f32_16x16x32_bf16 a[0:3],a[192:195],v[136:139],a[0:3]
ds_read_b64_tr_b16 a[246:247],v2 offset:13056
v_mfma_f32_16x16x32_bf16 a[0:3],a[196:199],v[140:143],a[0:3]
ds_read_b64_tr_b16 a[252:253],v3 offset:8960
v_mfma_f32_16x16x32_bf16 a[4:7],a[204:207],v[140:143],a[4:7]
ds_read_b64_tr_b16 a[254:255],v3 offset:13056
v_mfma_f32_16x16x32_bf16 a[4:7],a[200:203],v[136:139],a[4:7]
s_memrealtime s[76:77]
ds_read_b64_tr_b16 a[248:249],v3 offset:768
v_mfma_f32_16x16x32_bf16 a[8:11],a[208:211],v[136:139],a[8:11]
ds_read_b64_tr_b16 a[250:251],v3 offset:4864
v_mfma_f32_16x16x32_bf16 a[8:11],a[212:215],v[140:143],a[8:11]
v_exp_f32 v184,v184
v_mfma_f32_16x16x32_bf16 a[12:15],a[220:223],v[140:143],a[12:15]
v_exp_f32 v185,v185
v_mfma_f32_16x16x32_bf16 a[12:15],a[216:219],v[136:139],a[12:15]
s_bitcmp1_b32 s45,0
s_cbranch_scc0 L_t311_%=
s_mov_b32 m0,s80
s_add_u32 s80,s80,4096
buffer_load_dwordx4 v25,s[24:27],s83 offen nt lds
s_add_u32 s83,s83,4096
L_t311_%=:
v_mfma_f32_16x16x32_bf16 a[16:19],a[224:227],v[136:139],a[16:19]
v_exp_f32 v186,v186
v_mfma_f32_16x16x32_bf16 a[16:19],a[228:231],v[140:143],a[16:19]
s_waitcnt lgkmcnt(3)
v_exp_f32 v187,v187
v_mfma_f32_16x16x32_bf16 a[20:23],a[236:239],v[140:143],a[20:23]
s_waitcnt lgkmcnt(0)
v_exp_f32 v188,v188
v_mfma_f32_16x16x32_bf16 a[20:23],a[232:235],v[136:139],a[20:23]
s_sub_u32 s79,s78,s76
v_exp_f32 v189,v189
v_mfma_f32_16x16x32_bf16 a[24:27],a[240:243],v[136:139],a[24:27]
s_add_u32 s78,s76,s60
v_exp_f32 v190,v190
v_mfma_f32_16x16x32_bf16 a[24:27],a[244:247],v[140:143],a[24:27]
s_cmp_gt_i32 s79,s95
s_cbranch_scc1 L_ps7_%=
L_pr7_%=:
v_cvt_pk_bf16_f32 v172,v184,v185
v_mfma_f32_16x16x32_bf16 a[28:31],a[252:255],v[140:143],a[28:31]
v_exp_f32 v191,v191
v_mfma_f32_16x16x32_bf16 a[28:31],a[248:251],v[136:139],a[28:31]
v_cvt_pk_bf16_f32 v173,v186,v187
v_cvt_pk_bf16_f32 v174,v188,v189
v_cvt_pk_bf16_f32 v175,v190,v191
s_cmp_lg_u32 s53,0
v_smfmac_f32_16x16x64_bf16 v[48:51],v[28:31],v[136:143],v27
s_cbranch_scc1 L_att1_%=
L_att1x_%=:
L_endseg_%=:
v_mfma_f32_16x16x32_bf16 v[232:235],a[128:131],v[148:151],v[44:47]
s_waitcnt vmcnt(10)
s_lshl_b32 s43,s41,2
v_exp_f32 v200,v200
v_mfma_f32_16x16x32_bf16 v[232:235],a[132:135],v[180:183],v[232:235]
s_barrier
ds_read_b128 a[128:131],v1
v_add_u32 v2,s48,v2
v_mfma_f32_16x16x32_bf16 v[232:235],a[136:139],v[212:215],v[232:235]
s_add_u32 s43,s43,s70
v_exp_f32 v201,v201
v_mfma_f32_16x16x32_bf16 v[232:235],a[140:143],v[244:247],v[232:235]
s_lshl_b32 s43,s43,19
ds_read_b128 a[132:135],v1 offset:256
v_cvt_pk_bf16_f32 v200,v200,v201
v_mfma_f32_16x16x32_bf16 v[236:239],a[156:159],v[244:247],v[44:47]
s_add_u32 s43,s43,s55
v_exp_f32 v202,v202
v_mfma_f32_16x16x32_bf16 v[236:239],a[152:155],v[212:215],v[236:239]
s_mov_b64 s[28:29],s[12:13]
ds_read_b128 a[136:139],v1 offset:512
v_add_u32 v3,s48,v3
v_mfma_f32_16x16x32_bf16 v[236:239],a[148:151],v[180:183],v[236:239]
s_mov_b32 s48,32768
v_exp_f32 v203,v203
v_mfma_f32_16x16x32_bf16 v[236:239],a[144:147],v[148:151],v[236:239]
s_mov_b32 s30,475136
ds_read_b128 a[140:143],v1 offset:768
v_cvt_pk_bf16_f32 v201,v202,v203
v_mfma_f32_16x16x32_bf16 v[248:251],a[160:163],v[148:151],v[44:47]
s_mov_b32 s31,0x20000
v_exp_f32 v204,v204
v_mfma_f32_16x16x32_bf16 v[248:251],a[164:167],v[180:183],v[248:251]
ds_read_b128 a[156:159],v1 offset:4864
v_lshrrev_b32 v4,6,v6
v_mfma_f32_16x16x32_bf16 v[248:251],a[168:171],v[212:215],v[248:251]
v_exp_f32 v205,v205
v_mfma_f32_16x16x32_bf16 v[248:251],a[172:175],v[244:247],v[248:251]
ds_read_b128 a[152:155],v1 offset:4608
v_cvt_pk_bf16_f32 v202,v204,v205
v_mfma_f32_16x16x32_bf16 v[252:255],a[188:191],v[244:247],v[44:47]
v_exp_f32 v206,v206
v_mfma_f32_16x16x32_bf16 v[252:255],a[184:187],v[212:215],v[252:255]
ds_read_b128 a[148:151],v1 offset:4352
v_mul_u32_u24 v4,320,v4
v_mfma_f32_16x16x32_bf16 v[252:255],a[180:183],v[180:183],v[252:255]
v_exp_f32 v207,v207
v_mfma_f32_16x16x32_bf16 v[252:255],a[176:179],v[148:151],v[252:255]
ds_read_b128 a[144:147],v1 offset:4096
v_cvt_pk_bf16_f32 v203,v206,v207
v_mfma_f32_16x16x32_bf16 a[32:35],a[192:195],v[168:171],a[32:35]
v_exp_f32 v216,v216
v_mfma_f32_16x16x32_bf16 a[32:35],a[196:199],v[172:175],a[32:35]
ds_read_b128 a[160:163],v1 offset:8192
v_bfe_u32 v5,v6,4,2
v_mfma_f32_16x16x32_bf16 a[36:39],a[204:207],v[172:175],a[36:39]
v_exp_f32 v217,v217
v_mfma_f32_16x16x32_bf16 a[36:39],a[200:203],v[168:171],a[36:39]
ds_read_b128 a[164:167],v1 offset:8448
v_cvt_pk_bf16_f32 v204,v216,v217
v_mfma_f32_16x16x32_bf16 a[40:43],a[208:211],v[168:171],a[40:43]
v_exp_f32 v218,v218
v_mfma_f32_16x16x32_bf16 a[40:43],a[212:215],v[172:175],a[40:43]
ds_read_b128 a[168:171],v1 offset:8704
v_lshl_add_u32 v4,v5,3,v4
v_mfma_f32_16x16x32_bf16 a[44:47],a[220:223],v[172:175],a[44:47]
v_exp_f32 v219,v219
v_mfma_f32_16x16x32_bf16 a[44:47],a[216:219],v[168:171],a[44:47]
ds_read_b128 a[172:175],v1 offset:8960
v_cvt_pk_bf16_f32 v205,v218,v219
v_mfma_f32_16x16x32_bf16 a[48:51],a[224:227],v[168:171],a[48:51]
v_exp_f32 v220,v220
v_mfma_f32_16x16x32_bf16 a[48:51],a[228:231],v[172:175],a[48:51]
ds_read_b128 a[188:191],v1 offset:13056
v_bfe_u32 v5,v6,8,1
v_mfma_f32_16x16x32_bf16 a[52:55],a[236:239],v[172:175],a[52:55]
v_exp_f32 v221,v221
v_mfma_f32_16x16x32_bf16 a[52:55],a[232:235],v[168:171],a[52:55]
ds_read_b128 a[184:187],v1 offset:12800
v_cvt_pk_bf16_f32 v206,v220,v221
v_mfma_f32_16x16x32_bf16 a[56:59],a[240:243],v[168:171],a[56:59]
v_exp_f32 v222,v222
v_mfma_f32_16x16x32_bf16 a[56:59],a[244:247],v[172:175],a[56:59]
ds_read_b128 a[180:183],v1 offset:12544
v_lshlrev_b32 v5,5,v5
v_mfma_f32_16x16x32_bf16 a[60:63],a[252:255],v[172:175],a[60:63]
v_exp_f32 v223,v223
v_mfma_f32_16x16x32_bf16 a[60:63],a[248:251],v[168:171],a[60:63]
ds_read_b128 a[176:179],v1 offset:12288
v_cvt_pk_bf16_f32 v207,v222,v223
v_smfmac_f32_16x16x64_bf16 v[52:55],v[28:31],v[168:175],v27
s_waitcnt lgkmcnt(4)
v_add_u32 v1,s48,v1
v_mfma_f32_16x16x32_bf16 v[136:139],a[128:131],v[128:131],v[32:35]
v_exp_f32 v232,v232
v_mfma_f32_16x16x32_bf16 v[136:139],a[132:135],v[160:163],v[136:139]
v_exp_f32 v233,v233
v_mfma_f32_16x16x32_bf16 v[136:139],a[136:139],v[192:195],v[136:139]
v_exp_f32 v234,v234
v_mfma_f32_16x16x32_bf16 v[136:139],a[140:143],v[224:227],v[136:139]
v_exp_f32 v235,v235
v_mfma_f32_16x16x32_bf16 v[140:143],a[156:159],v[224:227],v[32:35]
v_cvt_pk_bf16_f32 v232,v232,v233
v_cvt_pk_bf16_f32 v233,v234,v235
v_mfma_f32_16x16x32_bf16 v[140:143],a[152:155],v[192:195],v[140:143]
s_add_u32 s80,s71,0
v_exp_f32 v236,v236
v_mfma_f32_16x16x32_bf16 v[140:143],a[148:151],v[160:163],v[140:143]
s_and_b32 s80,s80,3
v_exp_f32 v237,v237
v_mfma_f32_16x16x32_bf16 v[140:143],a[144:147],v[128:131],v[140:143]
s_lshl_b32 s80,s80,1
s_mul_i32 s80,s80,163840
s_add_u32 s80,s80,s81
v_mfma_f32_16x16x32_bf16 v[152:155],a[160:163],v[128:131],v[32:35]
)A"
R"A(s_add_u32 s83,s80,20480
v_exp_f32 v238,v238
v_mfma_f32_16x16x32_bf16 v[152:155],a[164:167],v[160:163],v[152:155]
buffer_store_dwordx4 v[64:67],v6,s[36:39],s84 offen sc1
v_mfma_f32_16x16x32_bf16 v[152:155],a[168:171],v[192:195],v[152:155]
v_exp_f32 v239,v239
v_mfma_f32_16x16x32_bf16 v[152:155],a[172:175],v[224:227],v[152:155]
s_waitcnt lgkmcnt(0)
v_cvt_pk_bf16_f32 v234,v236,v237
v_cvt_pk_bf16_f32 v235,v238,v239
v_mfma_f32_16x16x32_bf16 v[156:159],a[188:191],v[224:227],v[32:35]
v_exp_f32 v248,v248
v_mfma_f32_16x16x32_bf16 v[156:159],a[184:187],v[192:195],v[156:159]
v_exp_f32 v249,v249
v_mfma_f32_16x16x32_bf16 v[156:159],a[180:183],v[160:163],v[156:159]
v_exp_f32 v250,v250
v_mfma_f32_16x16x32_bf16 v[156:159],a[176:179],v[128:131],v[156:159]
v_exp_f32 v251,v251
v_mfma_f32_16x16x32_bf16 a[64:67],a[192:195],v[200:203],a[64:67]
v_exp_f32 v252,v252
v_mfma_f32_16x16x32_bf16 a[64:67],a[196:199],v[204:207],a[64:67]
v_exp_f32 v253,v253
v_mfma_f32_16x16x32_bf16 a[68:71],a[204:207],v[204:207],a[68:71]
v_exp_f32 v254,v254
v_mfma_f32_16x16x32_bf16 a[68:71],a[200:203],v[200:203],a[68:71]
v_exp_f32 v255,v255
v_mfma_f32_16x16x32_bf16 a[72:75],a[208:211],v[200:203],a[72:75]
v_cvt_pk_bf16_f32 v236,v248,v249
v_cvt_pk_bf16_f32 v237,v250,v251
v_mfma_f32_16x16x32_bf16 a[72:75],a[212:215],v[204:207],a[72:75]
v_cvt_pk_bf16_f32 v238,v252,v253
v_cvt_pk_bf16_f32 v239,v254,v255
v_mfma_f32_16x16x32_bf16 a[76:79],a[220:223],v[204:207],a[76:79]
v_exp_f32 v136,v136
v_mfma_f32_16x16x32_bf16 a[76:79],a[216:219],v[200:203],a[76:79]
v_exp_f32 v137,v137
v_mfma_f32_16x16x32_bf16 a[80:83],a[224:227],v[200:203],a[80:83]
v_exp_f32 v138,v138
v_mfma_f32_16x16x32_bf16 a[80:83],a[228:231],v[204:207],a[80:83]
v_exp_f32 v139,v139
v_mfma_f32_16x16x32_bf16 a[84:87],a[236:239],v[204:207],a[84:87]
s_add_u32 m0,s82,81920
v_exp_f32 v140,v140
v_mfma_f32_16x16x32_bf16 a[84:87],a[232:235],v[200:203],a[84:87]
v_cvt_pk_bf16_f32 v136,v136,v137
v_cvt_pk_bf16_f32 v137,v138,v139
v_mfma_f32_16x16x32_bf16 a[88:91],a[240:243],v[200:203],a[88:91]
v_exp_f32 v141,v141
v_mfma_f32_16x16x32_bf16 a[88:91],a[244:247],v[204:207],a[88:91]
buffer_load_dwordx4 v7,s[72:75],s80 offen lds
v_mfma_f32_16x16x32_bf16 a[92:95],a[252:255],v[204:207],a[92:95]
v_exp_f32 v142,v142
v_mfma_f32_16x16x32_bf16 a[92:95],a[248:251],v[200:203],a[92:95]
v_exp_f32 v143,v143
v_smfmac_f32_16x16x64_bf16 v[56:59],v[28:31],v[200:207],v27
v_cvt_pk_bf16_f32 v138,v140,v141
v_mfma_f32_16x16x32_bf16 v[168:171],a[128:131],v[132:135],v[36:39]
buffer_store_dwordx4 v[68:71],v6,s[36:39],s84 offen offset:1024 sc1
v_mfma_f32_16x16x32_bf16 v[168:171],a[132:135],v[164:167],v[168:171]
s_add_u32 s28,s28,s43
s_addc_u32 s29,s29,0
v_cvt_pk_bf16_f32 v139,v142,v143
v_mfma_f32_16x16x32_bf16 v[168:171],a[136:139],v[196:199],v[168:171]
s_and_b32 s29,s29,0xffff
v_exp_f32 v152,v152
v_mfma_f32_16x16x32_bf16 v[168:171],a[140:143],v[228:231],v[168:171]
v_exp_f32 v153,v153
v_mfma_f32_16x16x32_bf16 v[172:175],a[156:159],v[228:231],v[36:39]
v_exp_f32 v154,v154
v_mfma_f32_16x16x32_bf16 v[172:175],a[152:155],v[196:199],v[172:175]
v_exp_f32 v155,v155
v_mfma_f32_16x16x32_bf16 v[172:175],a[148:151],v[164:167],v[172:175]
v_exp_f32 v156,v156
v_mfma_f32_16x16x32_bf16 v[172:175],a[144:147],v[132:135],v[172:175]
v_exp_f32 v157,v157
v_mfma_f32_16x16x32_bf16 v[184:187],a[160:163],v[132:135],v[36:39]
v_exp_f32 v158,v158
v_mfma_f32_16x16x32_bf16 v[184:187],a[164:167],v[164:167],v[184:187]
v_exp_f32 v159,v159
v_mfma_f32_16x16x32_bf16 v[184:187],a[168:171],v[196:199],v[184:187]
v_cvt_pk_bf16_f32 v140,v152,v153
v_cvt_pk_bf16_f32 v141,v154,v155
v_mfma_f32_16x16x32_bf16 v[184:187],a[172:175],v[228:231],v[184:187]
v_cvt_pk_bf16_f32 v142,v156,v157
v_cvt_pk_bf16_f32 v143,v158,v159
v_mfma_f32_16x16x32_bf16 v[188:191],a[188:191],v[228:231],v[36:39]
v_exp_f32 v168,v168
v_mfma_f32_16x16x32_bf16 v[188:191],a[184:187],v[196:199],v[188:191]
v_exp_f32 v169,v169
v_mfma_f32_16x16x32_bf16 v[188:191],a[180:183],v[164:167],v[188:191]
v_exp_f32 v170,v170
v_mfma_f32_16x16x32_bf16 v[188:191],a[176:179],v[132:135],v[188:191]
v_exp_f32 v171,v171
v_mfma_f32_16x16x32_bf16 a[96:99],a[192:195],v[232:235],a[96:99]
buffer_load_dwordx4 v8,s[72:75],s80 offen offset:1024 lds
v_mfma_f32_16x16x32_bf16 a[96:99],a[196:199],v[236:239],a[96:99]
ds_read_b64_tr_b16 a[192:193],v2
ds_read_b64_tr_b16 a[194:195],v2 offset:4096
v_mfma_f32_16x16x32_bf16 a[100:103],a[204:207],v[236:239],a[100:103]
ds_read_b64_tr_b16 a[196:197],v2 offset:8192
v_cvt_pk_bf16_f32 v168,v168,v169
v_mfma_f32_16x16x32_bf16 a[100:103],a[200:203],v[232:235],a[100:103]
ds_read_b64_tr_b16 a[198:199],v2 offset:12288
v_cvt_pk_bf16_f32 v169,v170,v171
v_mfma_f32_16x16x32_bf16 a[104:107],a[208:211],v[232:235],a[104:107]
buffer_store_dwordx4 v[72:75],v6,s[36:39],s84 offen offset:2048 sc1
v_mfma_f32_16x16x32_bf16 a[104:107],a[212:215],v[236:239],a[104:107]
ds_read_b64_tr_b16 a[204:205],v3 offset:8192
ds_read_b64_tr_b16 a[206:207],v3 offset:12288
v_mfma_f32_16x16x32_bf16 a[108:111],a[220:223],v[236:239],a[108:111]
v_exp_f32 v172,v172
v_mfma_f32_16x16x32_bf16 a[108:111],a[216:219],v[232:235],a[108:111]
ds_read_b64_tr_b16 a[200:201],v3
ds_read_b64_tr_b16 a[202:203],v3 offset:4096
v_mfma_f32_16x16x32_bf16 a[112:115],a[224:227],v[232:235],a[112:115]
v_exp_f32 v173,v173
v_mfma_f32_16x16x32_bf16 a[112:115],a[228:231],v[236:239],a[112:115]
ds_read_b64_tr_b16 a[208:209],v2 offset:256
ds_read_b64_tr_b16 a[210:211],v2 offset:4352
v_mfma_f32_16x16x32_bf16 a[116:119],a[236:239],v[236:239],a[116:119]
ds_read_b64_tr_b16 a[212:213],v2 offset:8448
v_cvt_pk_bf16_f32 v170,v172,v173
v_mfma_f32_16x16x32_bf16 a[116:119],a[232:235],v[232:235],a[116:119]
v_exp_f32 v174,v174
v_mfma_f32_16x16x32_bf16 a[120:123],a[240:243],v[232:235],a[120:123]
ds_read_b64_tr_b16 a[214:215],v2 offset:12544
ds_read_b64_tr_b16 a[220:221],v3 offset:8448
v_mfma_f32_16x16x32_bf16 a[120:123],a[244:247],v[236:239],a[120:123]
ds_read_b64_tr_b16 a[222:223],v3 offset:12544
v_mfma_f32_16x16x32_bf16 a[124:127],a[252:255],v[236:239],a[124:127]
ds_read_b64_tr_b16 a[216:217],v3 offset:256
v_mfma_f32_16x16x32_bf16 a[124:127],a[248:251],v[232:235],a[124:127]
ds_read_b64_tr_b16 a[218:219],v3 offset:4352
v_smfmac_f32_16x16x64_bf16 v[60:63],v[28:31],v[232:239],v27
ds_read_b64_tr_b16 a[224:225],v2 offset:512
v_mfma_f32_16x16x32_bf16 v[200:203],a[128:131],v[144:147],v[40:43]
ds_read_b64_tr_b16 a[226:227],v2 offset:4608
ds_read_b64_tr_b16 a[228:229],v2 offset:8704
v_mfma_f32_16x16x32_bf16 v[200:203],a[132:135],v[176:179],v[200:203]
ds_read_b64_tr_b16 a[230:231],v2 offset:12800
v_mfma_f32_16x16x32_bf16 v[200:203],a[136:139],v[208:211],v[200:203]
ds_read_b64_tr_b16 a[236:237],v3 offset:8704
v_mfma_f32_16x16x32_bf16 v[200:203],a[140:143],v[240:243],v[200:203]
buffer_load_dwordx4 v9,s[72:75],s80 offen offset:2048 lds
v_mfma_f32_16x16x32_bf16 v[204:207],a[156:159],v[240:243],v[40:43]
ds_read_b64_tr_b16 a[238:239],v3 offset:12800
ds_read_b64_tr_b16 a[232:233],v3 offset:512
v_mfma_f32_16x16x32_bf16 v[204:207],a[152:155],v[208:211],v[204:207]
ds_read_b64_tr_b16 a[234:235],v3 offset:4608
v_mfma_f32_16x16x32_bf16 v[204:207],a[148:151],v[176:179],v[204:207]
ds_read_b64_tr_b16 a[240:241],v2 offset:768
v_mfma_f32_16x16x32_bf16 v[204:207],a[144:147],v[144:147],v[204:207]
ds_read_b64_tr_b16 a[242:243],v2 offset:4864
v_mfma_f32_16x16x32_bf16 v[216:219],a[160:163],v[144:147],v[40:43]
ds_read_b64_tr_b16 a[244:245],v2 offset:8960
ds_read_b64_tr_b16 a[246:247],v2 offset:13056
v_mfma_f32_16x16x32_bf16 v[216:219],a[164:167],v[176:179],v[216:219]
buffer_store_dwordx4 v[76:79],v6,s[36:39],s84 offen offset:3072 sc1
v_mfma_f32_16x16x32_bf16 v[216:219],a[168:171],v[208:211],v[216:219]
s_add_u32 s84,s55,65536
ds_read_b64_tr_b16 a[252:253],v3 offset:8960
ds_read_b64_tr_b16 a[254:255],v3 offset:13056
v_mfma_f32_16x16x32_bf16 v[216:219],a[172:175],v[240:243],v[216:219]
s_max_i32 s84,s84,s18
ds_read_b64_tr_b16 a[248:249],v3 offset:768
v_add_u32 v2,s48,v2
v_mfma_f32_16x16x32_bf16 v[220:223],a[188:191],v[240:243],v[40:43]
ds_read_b64_tr_b16 a[250:251],v3 offset:4864
v_add_u32 v3,s48,v3
v_mfma_f32_16x16x32_bf16 v[220:223],a[184:187],v[208:211],v[220:223]
s_mov_b32 s48,98304
v_exp_f32 v175,v175
v_mfma_f32_16x16x32_bf16 v[220:223],a[180:183],v[176:179],v[220:223]
v_exp_f32 v184,v184
v_mfma_f32_16x16x32_bf16 v[220:223],a[176:179],v[144:147],v[220:223]
s_waitcnt lgkmcnt(6)
v_exp_f32 v185,v185
v_mfma_f32_16x16x32_bf16 a[0:3],a[192:195],v[136:139],a[0:3]
v_cvt_pk_bf16_f32 v171,v174,v175
v_cvt_pk_bf16_f32 v172,v184,v185
v_mfma_f32_16x16x32_bf16 a[0:3],a[196:199],v[140:143],a[0:3]
v_exp_f32 v186,v186
v_mfma_f32_16x16x32_bf16 a[4:7],a[204:207],v[140:143],a[4:7]
v_exp_f32 v187,v187
v_mfma_f32_16x16x32_bf16 a[4:7],a[200:203],v[136:139],a[4:7]
v_exp_f32 v188,v188
v_mfma_f32_16x16x32_bf16 a[8:11],a[208:211],v[136:139],a[8:11]
v_exp_f32 v189,v189
v_mfma_f32_16x16x32_bf16 a[8:11],a[212:215],v[140:143],a[8:11]
v_exp_f32 v190,v190
v_mfma_f32_16x16x32_bf16 a[12:15],a[220:223],v[140:143],a[12:15]
v_exp_f32 v191,v191
v_mfma_f32_16x16x32_bf16 a[12:15],a[216:219],v[136:139],a[12:15]
buffer_load_dwordx4 v10,s[72:75],s80 offen offset:3072 lds
v_mfma_f32_16x16x32_bf16 a[16:19],a[224:227],v[136:139],a[16:19]
v_cvt_pk_bf16_f32 v173,v186,v187
v_cvt_pk_bf16_f32 v174,v188,v189
v_mfma_f32_16x16x32_bf16 a[16:19],a[228:231],v[140:143],a[16:19]
v_exp_f32 v200,v200
v_mfma_f32_16x16x32_bf16 a[20:23],a[236:239],v[140:143],a[20:23]
v_exp_f32 v201,v201
v_mfma_f32_16x16x32_bf16 a[20:23],a[232:235],v[136:139],a[20:23]
v_cvt_pk_bf16_f32 v175,v190,v191
v_cvt_pk_bf16_f32 v200,v200,v201
v_mfma_f32_16x16x32_bf16 a[24:27],a[240:243],v[136:139],a[24:27]
s_waitcnt lgkmcnt(0)
v_exp_f32 v202,v202
v_mfma_f32_16x16x32_bf16 a[24:27],a[244:247],v[140:143],a[24:27]
v_exp_f32 v203,v203
v_mfma_f32_16x16x32_bf16 a[28:31],a[252:255],v[140:143],a[28:31]
v_exp_f32 v204,v204
v_mfma_f32_16x16x32_bf16 a[28:31],a[248:251],v[136:139],a[28:31]
buffer_store_dwordx4 v[80:83],v6,s[36:39],s85 offen sc1
v_smfmac_f32_16x16x64_bf16 v[48:51],v[28:31],v[136:143],v27
v_cvt_pk_bf16_f32 v201,v202,v203
v_mfma_f32_16x16x32_bf16 v[232:235],a[128:131],v[148:151],v[44:47]
s_waitcnt vmcnt(10)
v_exp_f32 v205,v205
v_mfma_f32_16x16x32_bf16 v[232:235],a[132:135],v[180:183],v[232:235]
s_barrier
ds_read_b128 a[128:131],v1
v_cvt_pk_bf16_f32 v202,v204,v205
v_mfma_f32_16x16x32_bf16 v[232:235],a[136:139],v[212:215],v[232:235]
v_exp_f32 v206,v206
v_mfma_f32_16x16x32_bf16 v[232:235],a[140:143],v[244:247],v[232:235]
v_exp_f32 v207,v207
v_mfma_f32_16x16x32_bf16 v[236:239],a[156:159],v[244:247],v[44:47]
ds_read_b128 a[132:135],v1 offset:256
v_cvt_pk_bf16_f32 v203,v206,v207
v_mfma_f32_16x16x32_bf16 v[236:239],a[152:155],v[212:215],v[236:239]
v_exp_f32 v216,v216
v_mfma_f32_16x16x32_bf16 v[236:239],a[148:151],v[180:183],v[236:239]
s_add_u32 m0,s82,86016
v_exp_f32 v217,v217
v_mfma_f32_16x16x32_bf16 v[236:239],a[144:147],v[148:151],v[236:239]
ds_read_b128 a[136:139],v1 offset:512
v_cvt_pk_bf16_f32 v204,v216,v217
v_mfma_f32_16x16x32_bf16 v[248:251],a[160:163],v[148:151],v[44:47]
v_exp_f32 v218,v218
v_mfma_f32_16x16x32_bf16 v[248:251],a[164:167],v[180:183],v[248:251]
buffer_load_dwordx4 v11,s[72:75],s80 offen lds
v_mfma_f32_16x16x32_bf16 v[248:251],a[168:171],v[212:215],v[248:251]
ds_read_b128 a[140:143],v1 offset:768
v_mfma_f32_16x16x32_bf16 v[248:251],a[172:175],v[244:247],v[248:251]
v_exp_f32 v219,v219
v_mfma_f32_16x16x32_bf16 v[252:255],a[188:191],v[244:247],v[44:47]
ds_read_b128 a[156:159],v1 offset:4864
v_cvt_pk_bf16_f32 v205,v218,v219
v_mfma_f32_16x16x32_bf16 v[252:255],a[184:187],v[212:215],v[252:255]
v_exp_f32 v220,v220
v_mfma_f32_16x16x32_bf16 v[252:255],a[180:183],v[180:183],v[252:255]
ds_read_b128 a[152:155],v1 offset:4608
v_mfma_f32_16x16x32_bf16 v[252:255],a[176:179],v[148:151],v[252:255]
v_exp_f32 v221,v221
v_mfma_f32_16x16x32_bf16 a[32:35],a[192:195],v[168:171],a[32:35]
ds_read_b128 a[148:151],v1 offset:4352
v_cvt_pk_bf16_f32 v206,v220,v221
v_mfma_f32_16x16x32_bf16 a[32:35],a[196:199],v[172:175],a[32:35]
v_exp_f32 v222,v222
v_mfma_f32_16x16x32_bf16 a[36:39],a[204:207],v[172:175],a[36:39]
ds_read_b128 a[144:147],v1 offset:4096
)A"
R"A(v_mfma_f32_16x16x32_bf16 a[36:39],a[200:203],v[168:171],a[36:39]
buffer_store_dwordx4 v[84:87],v6,s[36:39],s85 offen offset:1024 sc1
v_mfma_f32_16x16x32_bf16 a[40:43],a[208:211],v[168:171],a[40:43]
ds_read_b128 a[160:163],v1 offset:8192
v_mfma_f32_16x16x32_bf16 a[40:43],a[212:215],v[172:175],a[40:43]
v_exp_f32 v223,v223
v_mfma_f32_16x16x32_bf16 a[44:47],a[220:223],v[172:175],a[44:47]
ds_read_b128 a[164:167],v1 offset:8448
v_cvt_pk_bf16_f32 v207,v222,v223
v_mfma_f32_16x16x32_bf16 a[44:47],a[216:219],v[168:171],a[44:47]
v_exp_f32 v232,v232
v_mfma_f32_16x16x32_bf16 a[48:51],a[224:227],v[168:171],a[48:51]
ds_read_b128 a[168:171],v1 offset:8704
v_mfma_f32_16x16x32_bf16 a[48:51],a[228:231],v[172:175],a[48:51]
v_exp_f32 v233,v233
v_mfma_f32_16x16x32_bf16 a[52:55],a[236:239],v[172:175],a[52:55]
s_add_u32 m0,s82,87040
ds_read_b128 a[172:175],v1 offset:8960
v_cvt_pk_bf16_f32 v232,v232,v233
v_mfma_f32_16x16x32_bf16 a[52:55],a[232:235],v[168:171],a[52:55]
v_exp_f32 v234,v234
v_mfma_f32_16x16x32_bf16 a[56:59],a[240:243],v[168:171],a[56:59]
ds_read_b128 a[188:191],v1 offset:13056
v_mfma_f32_16x16x32_bf16 a[56:59],a[244:247],v[172:175],a[56:59]
buffer_load_dwordx4 v7,s[72:75],s83 offen lds
v_mfma_f32_16x16x32_bf16 a[60:63],a[252:255],v[172:175],a[60:63]
ds_read_b128 a[184:187],v1 offset:12800
v_mfma_f32_16x16x32_bf16 a[60:63],a[248:251],v[168:171],a[60:63]
v_exp_f32 v235,v235
v_smfmac_f32_16x16x64_bf16 v[52:55],v[28:31],v[168:175],v27
s_waitcnt lgkmcnt(3)
ds_read_b128 a[180:183],v1 offset:12544
v_mfma_f32_16x16x32_bf16 v[136:139],a[128:131],v[128:131],v[32:35]
v_exp_f32 v236,v236
v_mfma_f32_16x16x32_bf16 v[136:139],a[132:135],v[160:163],v[136:139]
ds_read_b128 a[176:179],v1 offset:12288
v_cvt_pk_bf16_f32 v233,v234,v235
v_mfma_f32_16x16x32_bf16 v[136:139],a[136:139],v[192:195],v[136:139]
v_exp_f32 v237,v237
v_mfma_f32_16x16x32_bf16 v[136:139],a[140:143],v[224:227],v[136:139]
v_exp_f32 v238,v238
v_mfma_f32_16x16x32_bf16 v[140:143],a[156:159],v[224:227],v[32:35]
v_exp_f32 v239,v239
v_mfma_f32_16x16x32_bf16 v[140:143],a[152:155],v[192:195],v[140:143]
v_cvt_pk_bf16_f32 v234,v236,v237
v_cvt_pk_bf16_f32 v235,v238,v239
v_mfma_f32_16x16x32_bf16 v[140:143],a[148:151],v[160:163],v[140:143]
v_exp_f32 v248,v248
v_mfma_f32_16x16x32_bf16 v[140:143],a[144:147],v[128:131],v[140:143]
v_exp_f32 v249,v249
v_mfma_f32_16x16x32_bf16 v[152:155],a[160:163],v[128:131],v[32:35]
buffer_store_dwordx4 v[88:91],v6,s[36:39],s85 offen offset:2048 sc1
v_mfma_f32_16x16x32_bf16 v[152:155],a[164:167],v[160:163],v[152:155]
v_exp_f32 v250,v250
v_mfma_f32_16x16x32_bf16 v[152:155],a[168:171],v[192:195],v[152:155]
s_waitcnt lgkmcnt(0)
v_exp_f32 v251,v251
v_mfma_f32_16x16x32_bf16 v[152:155],a[172:175],v[224:227],v[152:155]
v_exp_f32 v252,v252
v_mfma_f32_16x16x32_bf16 v[156:159],a[188:191],v[224:227],v[32:35]
v_exp_f32 v253,v253
v_mfma_f32_16x16x32_bf16 v[156:159],a[184:187],v[192:195],v[156:159]
v_exp_f32 v254,v254
v_mfma_f32_16x16x32_bf16 v[156:159],a[180:183],v[160:163],v[156:159]
v_exp_f32 v255,v255
v_mfma_f32_16x16x32_bf16 v[156:159],a[176:179],v[128:131],v[156:159]
v_cvt_pk_bf16_f32 v236,v248,v249
v_cvt_pk_bf16_f32 v237,v250,v251
v_mfma_f32_16x16x32_bf16 a[64:67],a[192:195],v[200:203],a[64:67]
buffer_load_dwordx4 v8,s[72:75],s83 offen offset:1024 lds
v_mfma_f32_16x16x32_bf16 a[64:67],a[196:199],v[204:207],a[64:67]
v_cvt_pk_bf16_f32 v238,v252,v253
v_cvt_pk_bf16_f32 v239,v254,v255
v_mfma_f32_16x16x32_bf16 a[68:71],a[204:207],v[204:207],a[68:71]
v_exp_f32 v136,v136
v_mfma_f32_16x16x32_bf16 a[68:71],a[200:203],v[200:203],a[68:71]
v_exp_f32 v137,v137
v_mfma_f32_16x16x32_bf16 a[72:75],a[208:211],v[200:203],a[72:75]
v_exp_f32 v138,v138
v_mfma_f32_16x16x32_bf16 a[72:75],a[212:215],v[204:207],a[72:75]
v_exp_f32 v139,v139
v_mfma_f32_16x16x32_bf16 a[76:79],a[220:223],v[204:207],a[76:79]
v_cvt_pk_bf16_f32 v136,v136,v137
v_cvt_pk_bf16_f32 v137,v138,v139
v_mfma_f32_16x16x32_bf16 a[76:79],a[216:219],v[200:203],a[76:79]
v_exp_f32 v140,v140
v_mfma_f32_16x16x32_bf16 a[80:83],a[224:227],v[200:203],a[80:83]
v_exp_f32 v141,v141
v_mfma_f32_16x16x32_bf16 a[80:83],a[228:231],v[204:207],a[80:83]
v_exp_f32 v142,v142
v_mfma_f32_16x16x32_bf16 a[84:87],a[236:239],v[204:207],a[84:87]
v_exp_f32 v143,v143
v_mfma_f32_16x16x32_bf16 a[84:87],a[232:235],v[200:203],a[84:87]
v_cvt_pk_bf16_f32 v138,v140,v141
v_cvt_pk_bf16_f32 v139,v142,v143
v_mfma_f32_16x16x32_bf16 a[88:91],a[240:243],v[200:203],a[88:91]
v_exp_f32 v152,v152
v_mfma_f32_16x16x32_bf16 a[88:91],a[244:247],v[204:207],a[88:91]
v_exp_f32 v153,v153
v_mfma_f32_16x16x32_bf16 a[92:95],a[252:255],v[204:207],a[92:95]
buffer_store_dwordx4 v[92:95],v6,s[36:39],s85 offen offset:3072 sc1
v_mfma_f32_16x16x32_bf16 a[92:95],a[248:251],v[200:203],a[92:95]
s_add_u32 s85,s55,69632
v_exp_f32 v154,v154
v_smfmac_f32_16x16x64_bf16 v[56:59],v[28:31],v[200:207],v27
s_max_i32 s85,s85,s19
v_cvt_pk_bf16_f32 v140,v152,v153
v_mfma_f32_16x16x32_bf16 v[168:171],a[128:131],v[132:135],v[36:39]
v_exp_f32 v155,v155
v_mfma_f32_16x16x32_bf16 v[168:171],a[132:135],v[164:167],v[168:171]
v_exp_f32 v156,v156
v_mfma_f32_16x16x32_bf16 v[168:171],a[136:139],v[196:199],v[168:171]
v_exp_f32 v157,v157
v_mfma_f32_16x16x32_bf16 v[168:171],a[140:143],v[228:231],v[168:171]
buffer_load_dwordx4 v9,s[72:75],s83 offen offset:2048 lds
v_mfma_f32_16x16x32_bf16 v[172:175],a[156:159],v[228:231],v[36:39]
v_exp_f32 v158,v158
v_mfma_f32_16x16x32_bf16 v[172:175],a[152:155],v[196:199],v[172:175]
v_exp_f32 v159,v159
v_mfma_f32_16x16x32_bf16 v[172:175],a[148:151],v[164:167],v[172:175]
v_cvt_pk_bf16_f32 v141,v154,v155
v_cvt_pk_bf16_f32 v142,v156,v157
v_mfma_f32_16x16x32_bf16 v[172:175],a[144:147],v[132:135],v[172:175]
v_cvt_pk_bf16_f32 v143,v158,v159
v_add_u32 v1,s48,v1
v_mfma_f32_16x16x32_bf16 v[184:187],a[160:163],v[132:135],v[36:39]
v_mfma_f32_16x16x32_bf16 v[184:187],a[164:167],v[164:167],v[184:187]
v_exp_f32 v168,v168
v_mfma_f32_16x16x32_bf16 v[184:187],a[168:171],v[196:199],v[184:187]
v_exp_f32 v169,v169
v_mfma_f32_16x16x32_bf16 v[184:187],a[172:175],v[228:231],v[184:187]
v_exp_f32 v170,v170
v_mfma_f32_16x16x32_bf16 v[188:191],a[188:191],v[228:231],v[36:39]
v_exp_f32 v171,v171
v_mfma_f32_16x16x32_bf16 v[188:191],a[184:187],v[196:199],v[188:191]
v_exp_f32 v172,v172
v_mfma_f32_16x16x32_bf16 v[188:191],a[180:183],v[164:167],v[188:191]
v_exp_f32 v173,v173
v_mfma_f32_16x16x32_bf16 v[188:191],a[176:179],v[132:135],v[188:191]
v_exp_f32 v174,v174
v_mfma_f32_16x16x32_bf16 a[96:99],a[192:195],v[232:235],a[96:99]
ds_read_b64_tr_b16 a[192:193],v2
ds_read_b64_tr_b16 a[194:195],v2 offset:4096
v_mfma_f32_16x16x32_bf16 a[96:99],a[196:199],v[236:239],a[96:99]
ds_read_b64_tr_b16 a[196:197],v2 offset:8192
v_cvt_pk_bf16_f32 v168,v168,v169
v_mfma_f32_16x16x32_bf16 a[100:103],a[204:207],v[236:239],a[100:103]
ds_read_b64_tr_b16 a[198:199],v2 offset:12288
v_cvt_pk_bf16_f32 v169,v170,v171
v_mfma_f32_16x16x32_bf16 a[100:103],a[200:203],v[232:235],a[100:103]
buffer_store_dwordx4 v[96:99],v6,s[36:39],s86 offen sc1
v_mfma_f32_16x16x32_bf16 a[104:107],a[208:211],v[232:235],a[104:107]
ds_read_b64_tr_b16 a[204:205],v3 offset:8192
ds_read_b64_tr_b16 a[206:207],v3 offset:12288
v_mfma_f32_16x16x32_bf16 a[104:107],a[212:215],v[236:239],a[104:107]
ds_read_b64_tr_b16 a[200:201],v3
v_cvt_pk_bf16_f32 v170,v172,v173
v_mfma_f32_16x16x32_bf16 a[108:111],a[220:223],v[236:239],a[108:111]
v_exp_f32 v175,v175
v_mfma_f32_16x16x32_bf16 a[108:111],a[216:219],v[232:235],a[108:111]
buffer_load_dwordx4 v10,s[72:75],s83 offen offset:3072 lds
v_mfma_f32_16x16x32_bf16 a[112:115],a[224:227],v[232:235],a[112:115]
ds_read_b64_tr_b16 a[202:203],v3 offset:4096
ds_read_b64_tr_b16 a[208:209],v2 offset:256
v_mfma_f32_16x16x32_bf16 a[112:115],a[228:231],v[236:239],a[112:115]
ds_read_b64_tr_b16 a[210:211],v2 offset:4352
v_cvt_pk_bf16_f32 v171,v174,v175
v_mfma_f32_16x16x32_bf16 a[116:119],a[236:239],v[236:239],a[116:119]
v_exp_f32 v184,v184
v_mfma_f32_16x16x32_bf16 a[116:119],a[232:235],v[232:235],a[116:119]
ds_read_b64_tr_b16 a[212:213],v2 offset:8448
ds_read_b64_tr_b16 a[214:215],v2 offset:12544
v_mfma_f32_16x16x32_bf16 a[120:123],a[240:243],v[232:235],a[120:123]
v_exp_f32 v185,v185
v_mfma_f32_16x16x32_bf16 a[120:123],a[244:247],v[236:239],a[120:123]
ds_read_b64_tr_b16 a[220:221],v3 offset:8448
ds_read_b64_tr_b16 a[222:223],v3 offset:12544
v_mfma_f32_16x16x32_bf16 a[124:127],a[252:255],v[236:239],a[124:127]
ds_read_b64_tr_b16 a[216:217],v3 offset:256
v_cvt_pk_bf16_f32 v172,v184,v185
v_mfma_f32_16x16x32_bf16 a[124:127],a[248:251],v[232:235],a[124:127]
ds_read_b64_tr_b16 a[218:219],v3 offset:4352
v_smfmac_f32_16x16x64_bf16 v[60:63],v[28:31],v[232:239],v27
ds_read_b64_tr_b16 a[224:225],v2 offset:512
v_mfma_f32_16x16x32_bf16 v[200:203],a[128:131],v[144:147],v[40:43]
ds_read_b64_tr_b16 a[226:227],v2 offset:4608
ds_read_b64_tr_b16 a[228:229],v2 offset:8704
v_mfma_f32_16x16x32_bf16 v[200:203],a[132:135],v[176:179],v[200:203]
ds_read_b64_tr_b16 a[230:231],v2 offset:12800
v_mfma_f32_16x16x32_bf16 v[200:203],a[136:139],v[208:211],v[200:203]
ds_read_b64_tr_b16 a[236:237],v3 offset:8704
v_mfma_f32_16x16x32_bf16 v[200:203],a[140:143],v[240:243],v[200:203]
ds_read_b64_tr_b16 a[238:239],v3 offset:12800
v_mfma_f32_16x16x32_bf16 v[204:207],a[156:159],v[240:243],v[40:43]
ds_read_b64_tr_b16 a[232:233],v3 offset:512
ds_read_b64_tr_b16 a[234:235],v3 offset:4608
v_mfma_f32_16x16x32_bf16 v[204:207],a[152:155],v[208:211],v[204:207]
ds_read_b64_tr_b16 a[240:241],v2 offset:768
v_mfma_f32_16x16x32_bf16 v[204:207],a[148:151],v[176:179],v[204:207]
ds_read_b64_tr_b16 a[242:243],v2 offset:4864
v_mfma_f32_16x16x32_bf16 v[204:207],a[144:147],v[144:147],v[204:207]
s_add_u32 m0,s82,91136
ds_read_b64_tr_b16 a[244:245],v2 offset:8960
v_mfma_f32_16x16x32_bf16 v[216:219],a[160:163],v[144:147],v[40:43]
buffer_store_dwordx4 v[100:103],v6,s[36:39],s86 offen offset:1024 sc1
v_mfma_f32_16x16x32_bf16 v[216:219],a[164:167],v[176:179],v[216:219]
ds_read_b64_tr_b16 a[246:247],v2 offset:13056
ds_read_b64_tr_b16 a[252:253],v3 offset:8960
v_mfma_f32_16x16x32_bf16 v[216:219],a[168:171],v[208:211],v[216:219]
ds_read_b64_tr_b16 a[254:255],v3 offset:13056
v_add_u32 v2,s48,v2
v_mfma_f32_16x16x32_bf16 v[216:219],a[172:175],v[240:243],v[216:219]
ds_read_b64_tr_b16 a[248:249],v3 offset:768
v_mfma_f32_16x16x32_bf16 v[220:223],a[188:191],v[240:243],v[40:43]
buffer_load_dwordx4 v11,s[72:75],s83 offen lds
v_mfma_f32_16x16x32_bf16 v[220:223],a[184:187],v[208:211],v[220:223]
ds_read_b64_tr_b16 a[250:251],v3 offset:4864
v_add_u32 v3,s48,v3
v_mfma_f32_16x16x32_bf16 v[220:223],a[180:183],v[176:179],v[220:223]
v_exp_f32 v186,v186
v_mfma_f32_16x16x32_bf16 v[220:223],a[176:179],v[144:147],v[220:223]
s_waitcnt lgkmcnt(5)
v_exp_f32 v187,v187
v_mfma_f32_16x16x32_bf16 a[0:3],a[192:195],v[136:139],a[0:3]
v_exp_f32 v188,v188
v_mfma_f32_16x16x32_bf16 a[0:3],a[196:199],v[140:143],a[0:3]
s_add_u32 s80,s71,0
s_and_b32 s80,s80,3
s_lshl_b32 s80,s80,1
v_mfma_f32_16x16x32_bf16 a[4:7],a[204:207],v[140:143],a[4:7]
s_or_b32 s80,s80,1
s_mul_i32 s80,s80,163840
s_add_u32 s80,s80,s81
v_mfma_f32_16x16x32_bf16 a[4:7],a[200:203],v[136:139],a[4:7]
s_add_u32 s83,s80,20480
v_exp_f32 v189,v189
v_mfma_f32_16x16x32_bf16 a[8:11],a[208:211],v[136:139],a[8:11]
v_exp_f32 v190,v190
v_mfma_f32_16x16x32_bf16 a[8:11],a[212:215],v[140:143],a[8:11]
v_exp_f32 v191,v191
v_mfma_f32_16x16x32_bf16 a[12:15],a[220:223],v[140:143],a[12:15]
v_cvt_pk_bf16_f32 v173,v186,v187
v_cvt_pk_bf16_f32 v174,v188,v189
v_mfma_f32_16x16x32_bf16 a[12:15],a[216:219],v[136:139],a[12:15]
v_exp_f32 v200,v200
v_mfma_f32_16x16x32_bf16 a[16:19],a[224:227],v[136:139],a[16:19]
v_exp_f32 v201,v201
v_mfma_f32_16x16x32_bf16 a[16:19],a[228:231],v[140:143],a[16:19]
v_cvt_pk_bf16_f32 v175,v190,v191
v_cvt_pk_bf16_f32 v200,v200,v201
v_mfma_f32_16x16x32_bf16 a[20:23],a[236:239],v[140:143],a[20:23]
v_exp_f32 v202,v202
v_mfma_f32_16x16x32_bf16 a[20:23],a[232:235],v[136:139],a[20:23]
v_exp_f32 v203,v203
v_mfma_f32_16x16x32_bf16 a[24:27],a[240:243],v[136:139],a[24:27]
s_waitcnt lgkmcnt(0)
v_exp_f32 v204,v204
v_mfma_f32_16x16x32_bf16 a[24:27],a[244:247],v[140:143],a[24:27]
v_exp_f32 v205,v205
)A"
R"A(v_mfma_f32_16x16x32_bf16 a[28:31],a[252:255],v[140:143],a[28:31]
buffer_store_dwordx4 v[104:107],v6,s[36:39],s86 offen offset:2048 sc1
v_mfma_f32_16x16x32_bf16 a[28:31],a[248:251],v[136:139],a[28:31]
v_exp_f32 v206,v206
v_smfmac_f32_16x16x64_bf16 v[48:51],v[28:31],v[136:143],v27
v_cvt_pk_bf16_f32 v201,v202,v203
v_mfma_f32_16x16x32_bf16 v[232:235],a[128:131],v[148:151],v[44:47]
v_exp_f32 v207,v207
v_mfma_f32_16x16x32_bf16 v[232:235],a[132:135],v[180:183],v[232:235]
s_barrier
ds_read_b128 a[128:131],v1
v_cvt_pk_bf16_f32 v202,v204,v205
v_mfma_f32_16x16x32_bf16 v[232:235],a[136:139],v[212:215],v[232:235]
v_exp_f32 v216,v216
v_mfma_f32_16x16x32_bf16 v[232:235],a[140:143],v[244:247],v[232:235]
ds_read_b128 a[132:135],v1 offset:256
v_cvt_pk_bf16_f32 v203,v206,v207
v_mfma_f32_16x16x32_bf16 v[236:239],a[156:159],v[244:247],v[44:47]
s_add_u32 m0,s82,0
v_exp_f32 v217,v217
v_mfma_f32_16x16x32_bf16 v[236:239],a[152:155],v[212:215],v[236:239]
ds_read_b128 a[136:139],v1 offset:512
v_cvt_pk_bf16_f32 v204,v216,v217
v_mfma_f32_16x16x32_bf16 v[236:239],a[148:151],v[180:183],v[236:239]
v_exp_f32 v218,v218
v_mfma_f32_16x16x32_bf16 v[236:239],a[144:147],v[148:151],v[236:239]
buffer_load_dwordx4 v7,s[72:75],s80 offen lds
v_mfma_f32_16x16x32_bf16 v[248:251],a[160:163],v[148:151],v[44:47]
v_exp_f32 v219,v219
v_mfma_f32_16x16x32_bf16 v[248:251],a[164:167],v[180:183],v[248:251]
ds_read_b128 a[140:143],v1 offset:768
v_cvt_pk_bf16_f32 v205,v218,v219
v_mfma_f32_16x16x32_bf16 v[248:251],a[168:171],v[212:215],v[248:251]
v_exp_f32 v220,v220
v_mfma_f32_16x16x32_bf16 v[248:251],a[172:175],v[244:247],v[248:251]
ds_read_b128 a[156:159],v1 offset:4864
v_mfma_f32_16x16x32_bf16 v[252:255],a[188:191],v[244:247],v[44:47]
v_exp_f32 v221,v221
v_mfma_f32_16x16x32_bf16 v[252:255],a[184:187],v[212:215],v[252:255]
ds_read_b128 a[152:155],v1 offset:4608
v_cvt_pk_bf16_f32 v206,v220,v221
v_mfma_f32_16x16x32_bf16 v[252:255],a[180:183],v[180:183],v[252:255]
v_exp_f32 v222,v222
v_mfma_f32_16x16x32_bf16 v[252:255],a[176:179],v[148:151],v[252:255]
ds_read_b128 a[148:151],v1 offset:4352
v_mfma_f32_16x16x32_bf16 a[32:35],a[192:195],v[168:171],a[32:35]
v_exp_f32 v223,v223
v_mfma_f32_16x16x32_bf16 a[32:35],a[196:199],v[172:175],a[32:35]
ds_read_b128 a[144:147],v1 offset:4096
v_cvt_pk_bf16_f32 v207,v222,v223
v_mfma_f32_16x16x32_bf16 a[36:39],a[204:207],v[172:175],a[36:39]
buffer_store_dwordx4 v[108:111],v6,s[36:39],s86 offen offset:3072 sc1
v_mfma_f32_16x16x32_bf16 a[36:39],a[200:203],v[168:171],a[36:39]
s_add_u32 s86,s55,73728
s_max_i32 s86,s86,s100
ds_read_b128 a[160:163],v1 offset:8192
v_mfma_f32_16x16x32_bf16 a[40:43],a[208:211],v[168:171],a[40:43]
v_exp_f32 v232,v232
v_mfma_f32_16x16x32_bf16 a[40:43],a[212:215],v[172:175],a[40:43]
ds_read_b128 a[164:167],v1 offset:8448
v_mfma_f32_16x16x32_bf16 a[44:47],a[220:223],v[172:175],a[44:47]
buffer_load_dwordx4 v8,s[72:75],s80 offen offset:1024 lds
v_mfma_f32_16x16x32_bf16 a[44:47],a[216:219],v[168:171],a[44:47]
ds_read_b128 a[168:171],v1 offset:8704
v_mfma_f32_16x16x32_bf16 a[48:51],a[224:227],v[168:171],a[48:51]
v_exp_f32 v233,v233
v_mfma_f32_16x16x32_bf16 a[48:51],a[228:231],v[172:175],a[48:51]
ds_read_b128 a[172:175],v1 offset:8960
v_cvt_pk_bf16_f32 v232,v232,v233
v_mfma_f32_16x16x32_bf16 a[52:55],a[236:239],v[172:175],a[52:55]
v_exp_f32 v234,v234
v_mfma_f32_16x16x32_bf16 a[52:55],a[232:235],v[168:171],a[52:55]
ds_read_b128 a[188:191],v1 offset:13056
v_mfma_f32_16x16x32_bf16 a[56:59],a[240:243],v[168:171],a[56:59]
v_exp_f32 v235,v235
v_mfma_f32_16x16x32_bf16 a[56:59],a[244:247],v[172:175],a[56:59]
buffer_load_dwordx4 v9,s[72:75],s80 offen offset:2048 lds
v_mfma_f32_16x16x32_bf16 a[60:63],a[252:255],v[172:175],a[60:63]
ds_read_b128 a[184:187],v1 offset:12800
v_cvt_pk_bf16_f32 v233,v234,v235
v_mfma_f32_16x16x32_bf16 a[60:63],a[248:251],v[168:171],a[60:63]
v_exp_f32 v236,v236
v_smfmac_f32_16x16x64_bf16 v[52:55],v[28:31],v[168:175],v27
s_waitcnt lgkmcnt(3)
ds_read_b128 a[180:183],v1 offset:12544
v_mfma_f32_16x16x32_bf16 v[136:139],a[128:131],v[128:131],v[32:35]
v_exp_f32 v237,v237
v_mfma_f32_16x16x32_bf16 v[136:139],a[132:135],v[160:163],v[136:139]
ds_read_b128 a[176:179],v1 offset:12288
v_cvt_pk_bf16_f32 v234,v236,v237
v_mfma_f32_16x16x32_bf16 v[136:139],a[136:139],v[192:195],v[136:139]
v_exp_f32 v238,v238
v_mfma_f32_16x16x32_bf16 v[136:139],a[140:143],v[224:227],v[136:139]
v_exp_f32 v239,v239
v_mfma_f32_16x16x32_bf16 v[140:143],a[156:159],v[224:227],v[32:35]
v_cvt_pk_bf16_f32 v235,v238,v239
v_add_u32 v1,v4,v5
v_mfma_f32_16x16x32_bf16 v[140:143],a[152:155],v[192:195],v[140:143]
v_exp_f32 v248,v248
v_mfma_f32_16x16x32_bf16 v[140:143],a[148:151],v[160:163],v[140:143]
v_exp_f32 v249,v249
v_mfma_f32_16x16x32_bf16 v[140:143],a[144:147],v[128:131],v[140:143]
buffer_store_dwordx4 v[112:115],v6,s[36:39],s87 offen sc1
v_mfma_f32_16x16x32_bf16 v[152:155],a[160:163],v[128:131],v[32:35]
v_exp_f32 v250,v250
v_mfma_f32_16x16x32_bf16 v[152:155],a[164:167],v[160:163],v[152:155]
v_exp_f32 v251,v251
v_mfma_f32_16x16x32_bf16 v[152:155],a[168:171],v[192:195],v[152:155]
s_waitcnt lgkmcnt(0)
v_exp_f32 v252,v252
v_mfma_f32_16x16x32_bf16 v[152:155],a[172:175],v[224:227],v[152:155]
buffer_load_dwordx4 v10,s[72:75],s80 offen offset:3072 lds
v_mfma_f32_16x16x32_bf16 v[156:159],a[188:191],v[224:227],v[32:35]
v_exp_f32 v253,v253
v_mfma_f32_16x16x32_bf16 v[156:159],a[184:187],v[192:195],v[156:159]
v_exp_f32 v254,v254
v_mfma_f32_16x16x32_bf16 v[156:159],a[180:183],v[160:163],v[156:159]
v_exp_f32 v255,v255
v_mfma_f32_16x16x32_bf16 v[156:159],a[176:179],v[128:131],v[156:159]
s_add_u32 m0,s82,4096
v_cvt_pk_bf16_f32 v236,v248,v249
v_cvt_pk_bf16_f32 v237,v250,v251
v_mfma_f32_16x16x32_bf16 a[64:67],a[192:195],v[200:203],a[64:67]
v_cvt_pk_bf16_f32 v238,v252,v253
v_cvt_pk_bf16_f32 v239,v254,v255
v_mfma_f32_16x16x32_bf16 a[64:67],a[196:199],v[204:207],a[64:67]
v_exp_f32 v136,v136
v_mfma_f32_16x16x32_bf16 a[68:71],a[204:207],v[204:207],a[68:71]
buffer_load_dwordx4 v11,s[72:75],s80 offen lds
v_mfma_f32_16x16x32_bf16 a[68:71],a[200:203],v[200:203],a[68:71]
v_exp_f32 v137,v137
v_mfma_f32_16x16x32_bf16 a[72:75],a[208:211],v[200:203],a[72:75]
v_exp_f32 v138,v138
v_mfma_f32_16x16x32_bf16 a[72:75],a[212:215],v[204:207],a[72:75]
v_exp_f32 v139,v139
v_mfma_f32_16x16x32_bf16 a[76:79],a[220:223],v[204:207],a[76:79]
v_cvt_pk_bf16_f32 v136,v136,v137
v_cvt_pk_bf16_f32 v137,v138,v139
v_mfma_f32_16x16x32_bf16 a[76:79],a[216:219],v[200:203],a[76:79]
v_exp_f32 v140,v140
v_mfma_f32_16x16x32_bf16 a[80:83],a[224:227],v[200:203],a[80:83]
v_exp_f32 v141,v141
v_mfma_f32_16x16x32_bf16 a[80:83],a[228:231],v[204:207],a[80:83]
v_exp_f32 v142,v142
v_mfma_f32_16x16x32_bf16 a[84:87],a[236:239],v[204:207],a[84:87]
s_add_u32 m0,s82,5120
v_exp_f32 v143,v143
v_mfma_f32_16x16x32_bf16 a[84:87],a[232:235],v[200:203],a[84:87]
v_cvt_pk_bf16_f32 v138,v140,v141
v_cvt_pk_bf16_f32 v139,v142,v143
v_mfma_f32_16x16x32_bf16 a[88:91],a[240:243],v[200:203],a[88:91]
v_exp_f32 v152,v152
v_mfma_f32_16x16x32_bf16 a[88:91],a[244:247],v[204:207],a[88:91]
buffer_store_dwordx4 v[116:119],v6,s[36:39],s87 offen offset:1024 sc1
v_mfma_f32_16x16x32_bf16 a[92:95],a[252:255],v[204:207],a[92:95]
v_exp_f32 v153,v153
v_mfma_f32_16x16x32_bf16 a[92:95],a[248:251],v[200:203],a[92:95]
v_exp_f32 v154,v154
v_smfmac_f32_16x16x64_bf16 v[56:59],v[28:31],v[200:207],v27
v_cvt_pk_bf16_f32 v140,v152,v153
v_mfma_f32_16x16x32_bf16 v[168:171],a[128:131],v[132:135],v[36:39]
buffer_load_dwordx4 v7,s[72:75],s83 offen lds
v_mfma_f32_16x16x32_bf16 v[168:171],a[132:135],v[164:167],v[168:171]
v_exp_f32 v155,v155
v_mfma_f32_16x16x32_bf16 v[168:171],a[136:139],v[196:199],v[168:171]
v_exp_f32 v156,v156
v_mfma_f32_16x16x32_bf16 v[168:171],a[140:143],v[228:231],v[168:171]
v_exp_f32 v157,v157
v_mfma_f32_16x16x32_bf16 v[172:175],a[156:159],v[228:231],v[36:39]
v_exp_f32 v158,v158
v_mfma_f32_16x16x32_bf16 v[172:175],a[152:155],v[196:199],v[172:175]
v_exp_f32 v159,v159
v_mfma_f32_16x16x32_bf16 v[172:175],a[148:151],v[164:167],v[172:175]
v_cvt_pk_bf16_f32 v141,v154,v155
v_cvt_pk_bf16_f32 v142,v156,v157
v_mfma_f32_16x16x32_bf16 v[172:175],a[144:147],v[132:135],v[172:175]
buffer_load_dwordx4 v8,s[72:75],s83 offen offset:1024 lds
v_mfma_f32_16x16x32_bf16 v[184:187],a[160:163],v[132:135],v[36:39]
v_cvt_pk_bf16_f32 v143,v158,v159
v_add_u32 v1,81920,v1
v_mfma_f32_16x16x32_bf16 v[184:187],a[164:167],v[164:167],v[184:187]
v_exp_f32 v168,v168
v_mfma_f32_16x16x32_bf16 v[184:187],a[168:171],v[196:199],v[184:187]
v_exp_f32 v169,v169
v_mfma_f32_16x16x32_bf16 v[184:187],a[172:175],v[228:231],v[184:187]
v_exp_f32 v170,v170
v_mfma_f32_16x16x32_bf16 v[188:191],a[188:191],v[228:231],v[36:39]
v_exp_f32 v171,v171
v_mfma_f32_16x16x32_bf16 v[188:191],a[184:187],v[196:199],v[188:191]
v_exp_f32 v172,v172
v_mfma_f32_16x16x32_bf16 v[188:191],a[180:183],v[164:167],v[188:191]
v_exp_f32 v173,v173
v_mfma_f32_16x16x32_bf16 v[188:191],a[176:179],v[132:135],v[188:191]
v_exp_f32 v174,v174
v_mfma_f32_16x16x32_bf16 a[96:99],a[192:195],v[232:235],a[96:99]
ds_read_b64_tr_b16 a[192:193],v2
ds_read_b64_tr_b16 a[194:195],v2 offset:4096
v_mfma_f32_16x16x32_bf16 a[96:99],a[196:199],v[236:239],a[96:99]
ds_read_b64_tr_b16 a[196:197],v2 offset:8192
v_cvt_pk_bf16_f32 v168,v168,v169
v_mfma_f32_16x16x32_bf16 a[100:103],a[204:207],v[236:239],a[100:103]
buffer_store_dwordx4 v[120:123],v6,s[36:39],s87 offen offset:2048 sc1
v_mfma_f32_16x16x32_bf16 a[100:103],a[200:203],v[232:235],a[100:103]
ds_read_b64_tr_b16 a[198:199],v2 offset:12288
ds_read_b64_tr_b16 a[204:205],v3 offset:8192
v_mfma_f32_16x16x32_bf16 a[104:107],a[208:211],v[232:235],a[104:107]
ds_read_b64_tr_b16 a[206:207],v3 offset:12288
v_cvt_pk_bf16_f32 v169,v170,v171
v_mfma_f32_16x16x32_bf16 a[104:107],a[212:215],v[236:239],a[104:107]
ds_read_b64_tr_b16 a[200:201],v3
v_cvt_pk_bf16_f32 v170,v172,v173
v_mfma_f32_16x16x32_bf16 a[108:111],a[220:223],v[236:239],a[108:111]
buffer_load_dwordx4 v9,s[72:75],s83 offen offset:2048 lds
v_mfma_f32_16x16x32_bf16 a[108:111],a[216:219],v[232:235],a[108:111]
ds_read_b64_tr_b16 a[202:203],v3 offset:4096
ds_read_b64_tr_b16 a[208:209],v2 offset:256
v_mfma_f32_16x16x32_bf16 a[112:115],a[224:227],v[232:235],a[112:115]
v_exp_f32 v175,v175
v_mfma_f32_16x16x32_bf16 a[112:115],a[228:231],v[236:239],a[112:115]
ds_read_b64_tr_b16 a[210:211],v2 offset:4352
ds_read_b64_tr_b16 a[212:213],v2 offset:8448
v_mfma_f32_16x16x32_bf16 a[116:119],a[236:239],v[236:239],a[116:119]
ds_read_b64_tr_b16 a[214:215],v2 offset:12544
v_cvt_pk_bf16_f32 v171,v174,v175
v_mfma_f32_16x16x32_bf16 a[116:119],a[232:235],v[232:235],a[116:119]
s_cmp_ge_u32 0,s71
s_cselect_b32 s80,2,0
ds_read_b64_tr_b16 a[220:221],v3 offset:8448
v_mfma_f32_16x16x32_bf16 a[120:123],a[240:243],v[232:235],a[120:123]
s_mul_i32 s80,s80,163840
v_exp_f32 v184,v184
v_mfma_f32_16x16x32_bf16 a[120:123],a[244:247],v[236:239],a[120:123]
s_add_u32 s80,s80,s81
buffer_load_dwordx4 v10,s[72:75],s83 offen offset:3072 lds
v_mfma_f32_16x16x32_bf16 a[124:127],a[252:255],v[236:239],a[124:127]
ds_read_b64_tr_b16 a[222:223],v3 offset:12544
ds_read_b64_tr_b16 a[216:217],v3 offset:256
v_mfma_f32_16x16x32_bf16 a[124:127],a[248:251],v[232:235],a[124:127]
ds_read_b64_tr_b16 a[218:219],v3 offset:4352
v_smfmac_f32_16x16x64_bf16 v[60:63],v[28:31],v[232:239],v27
ds_read_b64_tr_b16 a[224:225],v2 offset:512
v_mfma_f32_16x16x32_bf16 v[200:203],a[128:131],v[144:147],v[40:43]
ds_read_b64_tr_b16 a[226:227],v2 offset:4608
v_mfma_f32_16x16x32_bf16 v[200:203],a[132:135],v[176:179],v[200:203]
ds_read_b64_tr_b16 a[228:229],v2 offset:8704
ds_read_b64_tr_b16 a[230:231],v2 offset:12800
v_mfma_f32_16x16x32_bf16 v[200:203],a[136:139],v[208:211],v[200:203]
ds_read_b64_tr_b16 a[236:237],v3 offset:8704
v_mfma_f32_16x16x32_bf16 v[200:203],a[140:143],v[240:243],v[200:203]
ds_read_b64_tr_b16 a[238:239],v3 offset:12800
v_mfma_f32_16x16x32_bf16 v[204:207],a[156:159],v[240:243],v[40:43]
s_add_u32 m0,s82,9216
ds_read_b64_tr_b16 a[232:233],v3 offset:512
v_mfma_f32_16x16x32_bf16 v[204:207],a[152:155],v[208:211],v[204:207]
ds_read_b64_tr_b16 a[234:235],v3 offset:4608
ds_read_b64_tr_b16 a[240:241],v2 offset:768
)A"
R"A(v_mfma_f32_16x16x32_bf16 v[204:207],a[148:151],v[176:179],v[204:207]
ds_read_b64_tr_b16 a[242:243],v2 offset:4864
v_mfma_f32_16x16x32_bf16 v[204:207],a[144:147],v[144:147],v[204:207]
buffer_store_dwordx4 v[124:127],v6,s[36:39],s87 offen offset:3072 sc1
v_mfma_f32_16x16x32_bf16 v[216:219],a[160:163],v[144:147],v[40:43]
s_add_u32 s87,s55,77824
ds_read_b64_tr_b16 a[244:245],v2 offset:8960
ds_read_b64_tr_b16 a[246:247],v2 offset:13056
v_mfma_f32_16x16x32_bf16 v[216:219],a[164:167],v[176:179],v[216:219]
s_max_i32 s87,s87,s101
ds_read_b64_tr_b16 a[252:253],v3 offset:8960
v_sub_u32 v2,v4,v5
v_mfma_f32_16x16x32_bf16 v[216:219],a[168:171],v[208:211],v[216:219]
ds_read_b64_tr_b16 a[254:255],v3 offset:13056
v_add_u32 v2,81920,v2
v_mfma_f32_16x16x32_bf16 v[216:219],a[172:175],v[240:243],v[216:219]
buffer_load_dwordx4 v11,s[72:75],s83 offen lds
v_mfma_f32_16x16x32_bf16 v[220:223],a[188:191],v[240:243],v[40:43]
s_add_u32 s83,s80,20480
ds_read_b64_tr_b16 a[248:249],v3 offset:768
ds_read_b64_tr_b16 a[250:251],v3 offset:4864
v_mfma_f32_16x16x32_bf16 v[220:223],a[184:187],v[208:211],v[220:223]
v_exp_f32 v185,v185
v_mfma_f32_16x16x32_bf16 v[220:223],a[180:183],v[176:179],v[220:223]
v_exp_f32 v186,v186
v_mfma_f32_16x16x32_bf16 v[220:223],a[176:179],v[144:147],v[220:223]
s_add_u32 m0,s82,40960
v_exp_f32 v187,v187
s_waitcnt lgkmcnt(6)
v_mfma_f32_16x16x32_bf16 a[0:3],a[192:195],v[136:139],a[0:3]
v_exp_f32 v188,v188
v_mfma_f32_16x16x32_bf16 a[0:3],a[196:199],v[140:143],a[0:3]
v_exp_f32 v189,v189
v_mfma_f32_16x16x32_bf16 a[4:7],a[204:207],v[140:143],a[4:7]
buffer_load_dwordx4 v7,s[72:75],s80 offen lds
v_mfma_f32_16x16x32_bf16 a[4:7],a[200:203],v[136:139],a[4:7]
v_exp_f32 v190,v190
v_mfma_f32_16x16x32_bf16 a[8:11],a[208:211],v[136:139],a[8:11]
v_exp_f32 v191,v191
v_mfma_f32_16x16x32_bf16 a[8:11],a[212:215],v[140:143],a[8:11]
v_cvt_pk_bf16_f32 v172,v184,v185
v_cvt_pk_bf16_f32 v173,v186,v187
v_mfma_f32_16x16x32_bf16 a[12:15],a[220:223],v[140:143],a[12:15]
v_cvt_pk_bf16_f32 v174,v188,v189
v_cvt_pk_bf16_f32 v175,v190,v191
v_mfma_f32_16x16x32_bf16 a[12:15],a[216:219],v[136:139],a[12:15]
v_exp_f32 v200,v200
v_mfma_f32_16x16x32_bf16 a[16:19],a[224:227],v[136:139],a[16:19]
v_exp_f32 v201,v201
v_mfma_f32_16x16x32_bf16 a[16:19],a[228:231],v[140:143],a[16:19]
v_exp_f32 v202,v202
v_mfma_f32_16x16x32_bf16 a[20:23],a[236:239],v[140:143],a[20:23]
v_exp_f32 v203,v203
v_mfma_f32_16x16x32_bf16 a[20:23],a[232:235],v[136:139],a[20:23]
v_exp_f32 v204,v204
v_mfma_f32_16x16x32_bf16 a[24:27],a[240:243],v[136:139],a[24:27]
s_waitcnt lgkmcnt(0)
v_exp_f32 v205,v205
v_mfma_f32_16x16x32_bf16 a[24:27],a[244:247],v[140:143],a[24:27]
buffer_load_dwordx4 v8,s[72:75],s80 offen offset:1024 lds
v_mfma_f32_16x16x32_bf16 a[28:31],a[252:255],v[140:143],a[28:31]
v_exp_f32 v206,v206
v_mfma_f32_16x16x32_bf16 a[28:31],a[248:251],v[136:139],a[28:31]
s_waitcnt vmcnt(18)
v_cvt_pk_bf16_f32 v200,v200,v201
v_cvt_pk_bf16_f32 v201,v202,v203
v_smfmac_f32_16x16x64_bf16 v[48:51],v[28:31],v[136:143],v27
s_barrier
v_exp_f32 v207,v207
v_mfma_f32_16x16x32_bf16 v[232:235],a[128:131],v[148:151],v[44:47]
v_cvt_pk_bf16_f32 v202,v204,v205
v_cvt_pk_bf16_f32 v203,v206,v207
v_mfma_f32_16x16x32_bf16 v[232:235],a[132:135],v[180:183],v[232:235]
v_exp_f32 v216,v216
v_mfma_f32_16x16x32_bf16 v[232:235],a[136:139],v[212:215],v[232:235]
v_exp_f32 v217,v217
v_mfma_f32_16x16x32_bf16 v[232:235],a[140:143],v[244:247],v[232:235]
v_exp_f32 v218,v218
v_mfma_f32_16x16x32_bf16 v[236:239],a[156:159],v[244:247],v[44:47]
v_exp_f32 v219,v219
v_mfma_f32_16x16x32_bf16 v[236:239],a[152:155],v[212:215],v[236:239]
v_exp_f32 v220,v220
v_mfma_f32_16x16x32_bf16 v[236:239],a[148:151],v[180:183],v[236:239]
buffer_load_dwordx4 v9,s[72:75],s80 offen offset:2048 lds
v_mfma_f32_16x16x32_bf16 v[236:239],a[144:147],v[148:151],v[236:239]
v_exp_f32 v221,v221
v_mfma_f32_16x16x32_bf16 v[248:251],a[160:163],v[148:151],v[44:47]
v_exp_f32 v222,v222
v_mfma_f32_16x16x32_bf16 v[248:251],a[164:167],v[180:183],v[248:251]
v_exp_f32 v223,v223
v_mfma_f32_16x16x32_bf16 v[248:251],a[168:171],v[212:215],v[248:251]
v_cvt_pk_bf16_f32 v204,v216,v217
v_cvt_pk_bf16_f32 v205,v218,v219
v_mfma_f32_16x16x32_bf16 v[248:251],a[172:175],v[244:247],v[248:251]
v_cvt_pk_bf16_f32 v206,v220,v221
v_cvt_pk_bf16_f32 v207,v222,v223
v_mfma_f32_16x16x32_bf16 v[252:255],a[188:191],v[244:247],v[44:47]
v_exp_f32 v232,v232
v_mfma_f32_16x16x32_bf16 v[252:255],a[184:187],v[212:215],v[252:255]
v_exp_f32 v233,v233
v_mfma_f32_16x16x32_bf16 v[252:255],a[180:183],v[180:183],v[252:255]
v_exp_f32 v234,v234
v_mfma_f32_16x16x32_bf16 v[252:255],a[176:179],v[148:151],v[252:255]
v_exp_f32 v235,v235
v_mfma_f32_16x16x32_bf16 a[32:35],a[192:195],v[168:171],a[32:35]
v_cvt_pk_bf16_f32 v232,v232,v233
v_add_f32 v48,v49,v48
v_mfma_f32_16x16x32_bf16 a[32:35],a[196:199],v[172:175],a[32:35]
buffer_load_dwordx4 v10,s[72:75],s80 offen offset:3072 lds
v_mfma_f32_16x16x32_bf16 a[36:39],a[204:207],v[172:175],a[36:39]
v_exp_f32 v236,v236
v_mfma_f32_16x16x32_bf16 a[36:39],a[200:203],v[168:171],a[36:39]
v_exp_f32 v237,v237
v_mfma_f32_16x16x32_bf16 a[40:43],a[208:211],v[168:171],a[40:43]
v_exp_f32 v238,v238
v_mfma_f32_16x16x32_bf16 a[40:43],a[212:215],v[172:175],a[40:43]
v_exp_f32 v239,v239
v_mfma_f32_16x16x32_bf16 a[44:47],a[220:223],v[172:175],a[44:47]
v_cvt_pk_bf16_f32 v233,v234,v235
v_max_f32 v20,0x00800000,v48
v_mfma_f32_16x16x32_bf16 a[44:47],a[216:219],v[168:171],a[44:47]
s_lshl_b32 s95,s42,2
s_add_u32 s95,s95,s40
s_lshl_b32 s95,s95,4
v_mfma_f32_16x16x32_bf16 a[48:51],a[224:227],v[168:171],a[48:51]
v_mov_b32 v18,s68
v_mov_b32 v19,s69
v_mfma_f32_16x16x32_bf16 a[48:51],a[228:231],v[172:175],a[48:51]
s_add_u32 m0,s82,45056
v_cvt_pk_bf16_f32 v234,v236,v237
v_cvt_pk_bf16_f32 v235,v238,v239
v_mfma_f32_16x16x32_bf16 a[52:55],a[236:239],v[172:175],a[52:55]
v_exp_f32 v248,v248
v_mfma_f32_16x16x32_bf16 a[52:55],a[232:235],v[168:171],a[52:55]
v_exp_f32 v249,v249
v_mfma_f32_16x16x32_bf16 a[56:59],a[240:243],v[168:171],a[56:59]
buffer_load_dwordx4 v11,s[72:75],s80 offen lds
v_mfma_f32_16x16x32_bf16 a[56:59],a[244:247],v[172:175],a[56:59]
v_exp_f32 v250,v250
v_mfma_f32_16x16x32_bf16 a[60:63],a[252:255],v[172:175],a[60:63]
v_exp_f32 v251,v251
v_mfma_f32_16x16x32_bf16 a[60:63],a[248:251],v[168:171],a[60:63]
v_exp_f32 v252,v252
v_smfmac_f32_16x16x64_bf16 v[52:55],v[28:31],v[168:175],v27
s_waitcnt vmcnt(6)
s_mov_b64 exec,1
buffer_store_dwordx2 v[18:19],v6,s[64:67],s95 offen sc1
s_mov_b64 exec,-1
v_mfma_f32_16x16x32_bf16 a[64:67],a[192:195],v[200:203],a[64:67]
v_exp_f32 v253,v253
v_mfma_f32_16x16x32_bf16 a[64:67],a[196:199],v[204:207],a[64:67]
v_exp_f32 v254,v254
v_mfma_f32_16x16x32_bf16 a[68:71],a[204:207],v[204:207],a[68:71]
v_exp_f32 v255,v255
v_mfma_f32_16x16x32_bf16 a[68:71],a[200:203],v[200:203],a[68:71]
s_add_u32 m0,s82,46080
v_rcp_f32 v20,v20
v_mfma_f32_16x16x32_bf16 a[72:75],a[208:211],v[200:203],a[72:75]
v_cvt_pk_bf16_f32 v236,v248,v249
v_cvt_pk_bf16_f32 v237,v250,v251
v_mfma_f32_16x16x32_bf16 a[72:75],a[212:215],v[204:207],a[72:75]
v_cvt_pk_bf16_f32 v238,v252,v253
v_cvt_pk_bf16_f32 v239,v254,v255
v_mfma_f32_16x16x32_bf16 a[76:79],a[220:223],v[204:207],a[76:79]
buffer_load_dwordx4 v7,s[72:75],s83 offen lds
v_mfma_f32_16x16x32_bf16 a[76:79],a[216:219],v[200:203],a[76:79]
v_accvgpr_read_b32 v136,a0
v_accvgpr_read_b32 v137,a1
v_accvgpr_read_b32 v138,a2
v_accvgpr_read_b32 v139,a3
v_accvgpr_read_b32 v12,a4
v_accvgpr_read_b32 v13,a5
v_accvgpr_read_b32 v14,a6
v_accvgpr_read_b32 v15,a7
v_pk_mul_f32 v[136:137],v[20:21],v[136:137] op_sel:[0,0] op_sel_hi:[0,1]
v_pk_mul_f32 v[138:139],v[20:21],v[138:139] op_sel:[0,0] op_sel_hi:[0,1]
v_pk_mul_f32 v[12:13],v[20:21],v[12:13] op_sel:[0,0] op_sel_hi:[0,1]
v_pk_mul_f32 v[14:15],v[20:21],v[14:15] op_sel:[0,0] op_sel_hi:[0,1]
v_cvt_pk_bf16_f32 v136,v136,v137
v_cvt_pk_bf16_f32 v137,v138,v139
v_cvt_pk_bf16_f32 v138,v12,v13
v_cvt_pk_bf16_f32 v139,v14,v15
v_mfma_f32_16x16x32_bf16 a[80:83],a[224:227],v[200:203],a[80:83]
ds_read_b64_tr_b16 a[128:129],v1
ds_read_b64_tr_b16 a[130:131],v1 offset:5120
v_mfma_f32_16x16x32_bf16 a[80:83],a[228:231],v[204:207],a[80:83]
ds_read_b64_tr_b16 a[132:133],v1 offset:10240
v_accvgpr_read_b32 v152,a16
v_mfma_f32_16x16x32_bf16 a[84:87],a[236:239],v[204:207],a[84:87]
ds_read_b64_tr_b16 a[134:135],v1 offset:15360
v_accvgpr_read_b32 v153,a17
v_mfma_f32_16x16x32_bf16 a[84:87],a[232:235],v[200:203],a[84:87]
ds_read_b64_tr_b16 a[136:137],v1 offset:20480
v_mul_f32 v152,v20,v152
v_mfma_f32_16x16x32_bf16 a[88:91],a[240:243],v[200:203],a[88:91]
ds_read_b64_tr_b16 a[138:139],v1 offset:25600
ds_read_b64_tr_b16 a[140:141],v1 offset:30720
v_mfma_f32_16x16x32_bf16 a[88:91],a[244:247],v[204:207],a[88:91]
v_accvgpr_read_b32 v140,a8
v_accvgpr_read_b32 v141,a9
v_accvgpr_read_b32 v142,a10
v_accvgpr_read_b32 v143,a11
v_accvgpr_read_b32 v12,a12
v_accvgpr_read_b32 v13,a13
v_accvgpr_read_b32 v14,a14
v_accvgpr_read_b32 v15,a15
v_pk_mul_f32 v[140:141],v[20:21],v[140:141] op_sel:[0,0] op_sel_hi:[0,1]
v_pk_mul_f32 v[142:143],v[20:21],v[142:143] op_sel:[0,0] op_sel_hi:[0,1]
v_pk_mul_f32 v[12:13],v[20:21],v[12:13] op_sel:[0,0] op_sel_hi:[0,1]
v_pk_mul_f32 v[14:15],v[20:21],v[14:15] op_sel:[0,0] op_sel_hi:[0,1]
v_cvt_pk_bf16_f32 v140,v140,v141
v_cvt_pk_bf16_f32 v141,v142,v143
v_cvt_pk_bf16_f32 v142,v12,v13
v_cvt_pk_bf16_f32 v143,v14,v15
v_mfma_f32_16x16x32_bf16 a[92:95],a[252:255],v[204:207],a[92:95]
ds_read_b64_tr_b16 a[142:143],v1 offset:35840
v_mul_f32 v153,v20,v153
v_mfma_f32_16x16x32_bf16 a[92:95],a[248:251],v[200:203],a[92:95]
v_accvgpr_read_b32 v154,a18
v_accvgpr_read_b32 v155,a19
v_smfmac_f32_16x16x64_bf16 v[56:59],v[28:31],v[200:207],v27
v_cvt_pk_bf16_f32 v152,v152,v153
v_mfma_f32_16x16x32_bf16 a[96:99],a[192:195],v[232:235],a[96:99]
buffer_store_dwordx4 v[136:139],v6,s[36:39],s84 offen sc1
v_mfma_f32_16x16x32_bf16 a[96:99],a[196:199],v[236:239],a[96:99]
v_mul_f32 v154,v20,v154
v_mul_f32 v155,v20,v155
v_mfma_f32_16x16x32_bf16 a[100:103],a[204:207],v[236:239],a[100:103]
v_cvt_pk_bf16_f32 v153,v154,v155
v_accvgpr_read_b32 v154,a20
v_mfma_f32_16x16x32_bf16 a[100:103],a[200:203],v[232:235],a[100:103]
v_accvgpr_read_b32 v155,a21
v_accvgpr_read_b32 v14,a22
v_mfma_f32_16x16x32_bf16 a[104:107],a[208:211],v[232:235],a[104:107]
buffer_load_dwordx4 v8,s[72:75],s83 offen offset:1024 lds
v_mfma_f32_16x16x32_bf16 a[104:107],a[212:215],v[236:239],a[104:107]
v_accvgpr_read_b32 v15,a23
v_mul_f32 v154,v20,v154
v_mfma_f32_16x16x32_bf16 a[108:111],a[220:223],v[236:239],a[108:111]
v_mul_f32 v155,v20,v155
v_mul_f32 v14,v20,v14
v_mfma_f32_16x16x32_bf16 a[108:111],a[216:219],v[232:235],a[108:111]
v_mul_f32 v15,v20,v15
v_cvt_pk_bf16_f32 v154,v154,v155
v_mfma_f32_16x16x32_bf16 a[112:115],a[224:227],v[232:235],a[112:115]
ds_read_b64_tr_b16 a[144:145],v2 offset:32
v_cvt_pk_bf16_f32 v155,v14,v15
v_mfma_f32_16x16x32_bf16 a[112:115],a[228:231],v[236:239],a[112:115]
ds_read_b64_tr_b16 a[146:147],v2 offset:5152
ds_read_b64_tr_b16 a[148:149],v2 offset:10272
v_mfma_f32_16x16x32_bf16 a[116:119],a[236:239],v[236:239],a[116:119]
ds_read_b64_tr_b16 a[150:151],v2 offset:15392
v_accvgpr_read_b32 v156,a24
v_mfma_f32_16x16x32_bf16 a[116:119],a[232:235],v[232:235],a[116:119]
buffer_load_dwordx4 v9,s[72:75],s83 offen offset:2048 lds
v_mfma_f32_16x16x32_bf16 a[120:123],a[240:243],v[232:235],a[120:123]
ds_read_b64_tr_b16 a[152:153],v2 offset:20512
ds_read_b64_tr_b16 a[154:155],v2 offset:25632
v_mfma_f32_16x16x32_bf16 a[120:123],a[244:247],v[236:239],a[120:123]
ds_read_b64_tr_b16 a[156:157],v2 offset:30752
v_accvgpr_read_b32 v157,a25
v_mfma_f32_16x16x32_bf16 a[124:127],a[252:255],v[236:239],a[124:127]
ds_read_b64_tr_b16 a[158:159],v2 offset:35872
v_mul_f32 v156,v20,v156
v_mfma_f32_16x16x32_bf16 a[124:127],a[248:251],v[232:235],a[124:127]
v_mul_f32 v157,v20,v157
v_accvgpr_read_b32 v158,a26
v_smfmac_f32_16x16x64_bf16 v[60:63],v[28:31],v[232:239],v27
s_waitcnt lgkmcnt(7)
v_accvgpr_read_b32 v159,a27
v_mfma_f32_16x16x32_bf16 a[192:195],a[128:131],v[64:67],0
v_cvt_pk_bf16_f32 v156,v156,v157
v_mul_f32 v158,v20,v158
v_mfma_f32_16x16x32_bf16 a[192:195],a[132:135],v[68:71],a[192:195]
v_mul_f32 v159,v20,v159
)A"
R"A(v_cvt_pk_bf16_f32 v157,v158,v159
v_mfma_f32_16x16x32_bf16 a[192:195],a[136:139],v[72:75],a[192:195]
buffer_store_dwordx4 v[140:143],v6,s[36:39],s84 offen offset:1024 sc1
v_mfma_f32_16x16x32_bf16 a[192:195],a[140:143],v[76:79],a[192:195]
v_accvgpr_read_b32 v158,a28
v_accvgpr_read_b32 v159,a29
v_mfma_f32_16x16x32_bf16 a[196:199],a[140:143],v[92:95],0
v_accvgpr_read_b32 v16,a30
v_accvgpr_read_b32 v17,a31
v_mfma_f32_16x16x32_bf16 a[196:199],a[136:139],v[88:91],a[196:199]
v_mul_f32 v158,v20,v158
v_mul_f32 v159,v20,v159
v_mfma_f32_16x16x32_bf16 a[196:199],a[132:135],v[84:87],a[196:199]
buffer_load_dwordx4 v10,s[72:75],s83 offen offset:3072 lds
v_mfma_f32_16x16x32_bf16 a[196:199],a[128:131],v[80:83],a[196:199]
v_mul_f32 v16,v20,v16
v_mul_f32 v17,v20,v17
v_mfma_f32_16x16x32_bf16 a[200:203],a[128:131],v[96:99],0
v_cvt_pk_bf16_f32 v158,v158,v159
v_cvt_pk_bf16_f32 v159,v16,v17
v_mfma_f32_16x16x32_bf16 a[200:203],a[132:135],v[100:103],a[200:203]
ds_read_b64_tr_b16 a[160:161],v1 offset:64
ds_read_b64_tr_b16 a[162:163],v1 offset:5184
v_mfma_f32_16x16x32_bf16 a[200:203],a[136:139],v[104:107],a[200:203]
ds_read_b64_tr_b16 a[164:165],v1 offset:10304
v_add_f32 v52,v53,v52
v_mfma_f32_16x16x32_bf16 a[200:203],a[140:143],v[108:111],a[200:203]
ds_read_b64_tr_b16 a[166:167],v1 offset:15424
v_max_f32 v21,0x00800000,v52
v_mfma_f32_16x16x32_bf16 a[204:207],a[140:143],v[124:127],0
s_cmp_ge_u32 1,s71
s_cselect_b32 s80,4,2
ds_read_b64_tr_b16 a[168:169],v1 offset:20544
v_mfma_f32_16x16x32_bf16 a[204:207],a[136:139],v[120:123],a[204:207]
s_add_u32 m0,s82,50176
ds_read_b64_tr_b16 a[170:171],v1 offset:25664
ds_read_b64_tr_b16 a[172:173],v1 offset:30784
v_mfma_f32_16x16x32_bf16 a[204:207],a[132:135],v[116:119],a[204:207]
s_mul_i32 s80,s80,163840
s_add_u32 s80,s80,s81
ds_read_b64_tr_b16 a[174:175],v1 offset:35904
v_mfma_f32_16x16x32_bf16 a[204:207],a[128:131],v[112:115],a[204:207]
s_waitcnt lgkmcnt(8)
v_rcp_f32 v21,v21
v_mfma_f32_16x16x32_bf16 a[208:211],a[144:147],v[64:67],0
buffer_load_dwordx4 v11,s[72:75],s83 offen lds
v_mfma_f32_16x16x32_bf16 a[208:211],a[148:151],v[68:71],a[208:211]
v_accvgpr_read_b32 v168,a32
v_accvgpr_read_b32 v169,a33
v_accvgpr_read_b32 v170,a34
v_accvgpr_read_b32 v171,a35
v_accvgpr_read_b32 v12,a36
v_accvgpr_read_b32 v13,a37
v_accvgpr_read_b32 v14,a38
v_accvgpr_read_b32 v15,a39
v_pk_mul_f32 v[168:169],v[20:21],v[168:169] op_sel:[1,0] op_sel_hi:[1,1]
v_pk_mul_f32 v[170:171],v[20:21],v[170:171] op_sel:[1,0] op_sel_hi:[1,1]
v_pk_mul_f32 v[12:13],v[20:21],v[12:13] op_sel:[1,0] op_sel_hi:[1,1]
v_pk_mul_f32 v[14:15],v[20:21],v[14:15] op_sel:[1,0] op_sel_hi:[1,1]
v_cvt_pk_bf16_f32 v168,v168,v169
v_cvt_pk_bf16_f32 v169,v170,v171
v_cvt_pk_bf16_f32 v170,v12,v13
v_cvt_pk_bf16_f32 v171,v14,v15
v_mfma_f32_16x16x32_bf16 a[208:211],a[152:155],v[72:75],a[208:211]
s_add_u32 s83,s80,20480
ds_read_b64_tr_b16 a[176:177],v2 offset:96
ds_read_b64_tr_b16 a[178:179],v2 offset:5216
v_mfma_f32_16x16x32_bf16 a[208:211],a[156:159],v[76:79],a[208:211]
ds_read_b64_tr_b16 a[180:181],v2 offset:10336
v_accvgpr_read_b32 v184,a48
v_mfma_f32_16x16x32_bf16 a[212:215],a[156:159],v[92:95],0
ds_read_b64_tr_b16 a[182:183],v2 offset:15456
v_accvgpr_read_b32 v185,a49
v_mfma_f32_16x16x32_bf16 a[212:215],a[152:155],v[88:91],a[212:215]
ds_read_b64_tr_b16 a[184:185],v2 offset:20576
v_mul_f32 v184,v21,v184
v_mfma_f32_16x16x32_bf16 a[212:215],a[148:151],v[84:87],a[212:215]
buffer_store_dwordx4 v[152:155],v6,s[36:39],s84 offen offset:2048 sc1
v_mfma_f32_16x16x32_bf16 a[212:215],a[144:147],v[80:83],a[212:215]
ds_read_b64_tr_b16 a[186:187],v2 offset:25696
ds_read_b64_tr_b16 a[188:189],v2 offset:30816
v_mfma_f32_16x16x32_bf16 a[216:219],a[144:147],v[96:99],0
ds_read_b64_tr_b16 a[190:191],v2 offset:35936
v_mul_f32 v185,v21,v185
v_mfma_f32_16x16x32_bf16 a[216:219],a[148:151],v[100:103],a[216:219]
v_accvgpr_read_b32 v172,a40
v_accvgpr_read_b32 v173,a41
v_accvgpr_read_b32 v174,a42
v_accvgpr_read_b32 v175,a43
v_accvgpr_read_b32 v12,a44
v_accvgpr_read_b32 v13,a45
v_accvgpr_read_b32 v14,a46
v_accvgpr_read_b32 v15,a47
v_pk_mul_f32 v[172:173],v[20:21],v[172:173] op_sel:[1,0] op_sel_hi:[1,1]
v_pk_mul_f32 v[174:175],v[20:21],v[174:175] op_sel:[1,0] op_sel_hi:[1,1]
v_pk_mul_f32 v[12:13],v[20:21],v[12:13] op_sel:[1,0] op_sel_hi:[1,1]
v_pk_mul_f32 v[14:15],v[20:21],v[14:15] op_sel:[1,0] op_sel_hi:[1,1]
v_cvt_pk_bf16_f32 v172,v172,v173
v_cvt_pk_bf16_f32 v173,v174,v175
v_cvt_pk_bf16_f32 v174,v12,v13
v_cvt_pk_bf16_f32 v175,v14,v15
v_mfma_f32_16x16x32_bf16 a[216:219],a[152:155],v[104:107],a[216:219]
v_accvgpr_read_b32 v186,a50
v_accvgpr_read_b32 v187,a51
v_mfma_f32_16x16x32_bf16 a[216:219],a[156:159],v[108:111],a[216:219]
v_cvt_pk_bf16_f32 v184,v184,v185
v_mul_f32 v186,v21,v186
v_mfma_f32_16x16x32_bf16 a[220:223],a[156:159],v[124:127],0
v_mul_f32 v187,v21,v187
v_cvt_pk_bf16_f32 v185,v186,v187
v_mfma_f32_16x16x32_bf16 a[220:223],a[152:155],v[120:123],a[220:223]
v_accvgpr_read_b32 v186,a52
v_accvgpr_read_b32 v187,a53
v_mfma_f32_16x16x32_bf16 a[220:223],a[148:151],v[116:119],a[220:223]
s_add_u32 m0,s82,122880
v_accvgpr_read_b32 v12,a54
v_accvgpr_read_b32 v13,a55
v_mfma_f32_16x16x32_bf16 a[220:223],a[144:147],v[112:115],a[220:223]
s_waitcnt lgkmcnt(1)
v_mul_f32 v186,v21,v186
v_mul_f32 v187,v21,v187
v_mfma_f32_16x16x32_bf16 a[224:227],a[160:163],v[64:67],0
v_mul_f32 v12,v21,v12
v_mul_f32 v13,v21,v13
v_mfma_f32_16x16x32_bf16 a[224:227],a[164:167],v[68:71],a[224:227]
buffer_load_dwordx4 v7,s[72:75],s80 offen lds
v_mfma_f32_16x16x32_bf16 a[224:227],a[168:171],v[72:75],a[224:227]
v_cvt_pk_bf16_f32 v186,v186,v187
v_cvt_pk_bf16_f32 v187,v12,v13
v_mfma_f32_16x16x32_bf16 a[224:227],a[172:175],v[76:79],a[224:227]
ds_read_b64_tr_b16 a[128:129],v1 offset:128
ds_read_b64_tr_b16 a[130:131],v1 offset:5248
v_mfma_f32_16x16x32_bf16 a[228:231],a[172:175],v[92:95],0
ds_read_b64_tr_b16 a[132:133],v1 offset:10368
v_accvgpr_read_b32 v188,a56
v_mfma_f32_16x16x32_bf16 a[228:231],a[168:171],v[88:91],a[228:231]
ds_read_b64_tr_b16 a[134:135],v1 offset:15488
v_accvgpr_read_b32 v189,a57
v_mfma_f32_16x16x32_bf16 a[228:231],a[164:167],v[84:87],a[228:231]
ds_read_b64_tr_b16 a[136:137],v1 offset:20608
v_mul_f32 v188,v21,v188
v_mfma_f32_16x16x32_bf16 a[228:231],a[160:163],v[80:83],a[228:231]
ds_read_b64_tr_b16 a[138:139],v1 offset:25728
ds_read_b64_tr_b16 a[140:141],v1 offset:30848
v_mfma_f32_16x16x32_bf16 a[232:235],a[160:163],v[96:99],0
ds_read_b64_tr_b16 a[142:143],v1 offset:35968
v_mul_f32 v189,v21,v189
v_mfma_f32_16x16x32_bf16 a[232:235],a[164:167],v[100:103],a[232:235]
v_accvgpr_read_b32 v190,a58
v_accvgpr_read_b32 v191,a59
v_mfma_f32_16x16x32_bf16 a[232:235],a[168:171],v[104:107],a[232:235]
buffer_store_dwordx4 v[156:159],v6,s[36:39],s84 offen offset:3072 sc1
v_mfma_f32_16x16x32_bf16 a[232:235],a[172:175],v[108:111],a[232:235]
v_cvt_pk_bf16_f32 v188,v188,v189
v_mul_f32 v190,v21,v190
v_mfma_f32_16x16x32_bf16 a[236:239],a[172:175],v[124:127],0
v_mul_f32 v191,v21,v191
v_cvt_pk_bf16_f32 v189,v190,v191
v_mfma_f32_16x16x32_bf16 a[236:239],a[168:171],v[120:123],a[236:239]
v_accvgpr_read_b32 v190,a60
v_accvgpr_read_b32 v191,a61
v_mfma_f32_16x16x32_bf16 a[236:239],a[164:167],v[116:119],a[236:239]
buffer_load_dwordx4 v8,s[72:75],s80 offen offset:1024 lds
v_mfma_f32_16x16x32_bf16 a[236:239],a[160:163],v[112:115],a[236:239]
v_accvgpr_read_b32 v14,a62
v_accvgpr_read_b32 v15,a63
v_mfma_f32_16x16x32_bf16 a[240:243],a[176:179],v[64:67],0
v_mul_f32 v190,v21,v190
v_mul_f32 v191,v21,v191
v_mfma_f32_16x16x32_bf16 a[240:243],a[180:183],v[68:71],a[240:243]
v_mul_f32 v14,v21,v14
v_mul_f32 v15,v21,v15
v_mfma_f32_16x16x32_bf16 a[240:243],a[184:187],v[72:75],a[240:243]
s_waitcnt lgkmcnt(0)
v_cvt_pk_bf16_f32 v190,v190,v191
v_cvt_pk_bf16_f32 v191,v14,v15
v_mfma_f32_16x16x32_bf16 a[240:243],a[188:191],v[76:79],a[240:243]
ds_read_b64_tr_b16 a[144:145],v2 offset:160
ds_read_b64_tr_b16 a[146:147],v2 offset:5280
v_mfma_f32_16x16x32_bf16 a[244:247],a[188:191],v[92:95],0
ds_read_b64_tr_b16 a[148:149],v2 offset:10400
v_add_f32 v56,v57,v56
v_mfma_f32_16x16x32_bf16 a[244:247],a[184:187],v[88:91],a[244:247]
ds_read_b64_tr_b16 a[150:151],v2 offset:15520
v_max_f32 v22,0x00800000,v56
v_mfma_f32_16x16x32_bf16 a[244:247],a[180:183],v[84:87],a[244:247]
ds_read_b64_tr_b16 a[152:153],v2 offset:20640
v_accvgpr_read_b32 v200,a64
v_mfma_f32_16x16x32_bf16 a[244:247],a[176:179],v[80:83],a[244:247]
ds_read_b64_tr_b16 a[154:155],v2 offset:25760
ds_read_b64_tr_b16 a[156:157],v2 offset:30880
v_mfma_f32_16x16x32_bf16 a[248:251],a[176:179],v[96:99],0
v_rcp_f32 v22,v22
v_mfma_f32_16x16x32_bf16 a[248:251],a[180:183],v[100:103],a[248:251]
buffer_load_dwordx4 v9,s[72:75],s80 offen offset:2048 lds
v_mfma_f32_16x16x32_bf16 a[248:251],a[184:187],v[104:107],a[248:251]
ds_read_b64_tr_b16 a[158:159],v2 offset:36000
v_accvgpr_read_b32 v201,a65
v_mfma_f32_16x16x32_bf16 a[248:251],a[188:191],v[108:111],a[248:251]
v_mul_f32 v200,v22,v200
v_mul_f32 v201,v22,v201
v_mfma_f32_16x16x32_bf16 a[252:255],a[188:191],v[124:127],0
v_accvgpr_read_b32 v202,a66
v_accvgpr_read_b32 v203,a67
v_mfma_f32_16x16x32_bf16 a[252:255],a[184:187],v[120:123],a[252:255]
v_cvt_pk_bf16_f32 v200,v200,v201
v_mul_f32 v202,v22,v202
v_mfma_f32_16x16x32_bf16 a[252:255],a[180:183],v[116:119],a[252:255]
s_waitcnt vmcnt(18)
v_mul_f32 v203,v22,v203
v_cvt_pk_bf16_f32 v201,v202,v203
v_mfma_f32_16x16x32_bf16 a[252:255],a[176:179],v[112:115],a[252:255]
s_barrier
buffer_store_dwordx4 v[168:171],v6,s[36:39],s85 offen sc1
v_mfma_f32_16x16x32_bf16 a[0:3],a[128:131],v[64:67],0
v_accvgpr_read_b32 v202,a68
v_accvgpr_read_b32 v203,a69
v_mfma_f32_16x16x32_bf16 a[0:3],a[132:135],v[68:71],a[0:3]
v_accvgpr_read_b32 v16,a70
v_accvgpr_read_b32 v17,a71
v_mfma_f32_16x16x32_bf16 a[0:3],a[136:139],v[72:75],a[0:3]
v_mul_f32 v202,v22,v202
v_mul_f32 v203,v22,v203
v_mfma_f32_16x16x32_bf16 a[0:3],a[140:143],v[76:79],a[0:3]
v_mul_f32 v16,v22,v16
v_mul_f32 v17,v22,v17
v_mfma_f32_16x16x32_bf16 a[4:7],a[140:143],v[92:95],0
buffer_load_dwordx4 v10,s[72:75],s80 offen offset:3072 lds
v_mfma_f32_16x16x32_bf16 a[4:7],a[136:139],v[88:91],a[4:7]
s_lshl_b32 s94,s70,4
v_cvt_pk_bf16_f32 v202,v202,v203
v_cvt_pk_bf16_f32 v203,v16,v17
v_mfma_f32_16x16x32_bf16 a[4:7],a[132:135],v[84:87],a[4:7]
s_add_u32 s94,s94,s40
v_accvgpr_read_b32 v204,a72
v_accvgpr_read_b32 v205,a73
v_mfma_f32_16x16x32_bf16 a[4:7],a[128:131],v[80:83],a[4:7]
s_lshl_b32 s94,s94,4
v_mul_f32 v204,v22,v204
v_mul_f32 v205,v22,v205
v_mfma_f32_16x16x32_bf16 a[8:11],a[128:131],v[96:99],0
v_accvgpr_read_b32 v206,a74
v_accvgpr_read_b32 v207,a75
v_mfma_f32_16x16x32_bf16 a[8:11],a[132:135],v[100:103],a[8:11]
ds_read_b64_tr_b16 a[160:161],v1 offset:192
ds_read_b64_tr_b16 a[162:163],v1 offset:5312
v_mfma_f32_16x16x32_bf16 a[8:11],a[136:139],v[104:107],a[8:11]
v_cvt_pk_bf16_f32 v204,v204,v205
v_mul_f32 v206,v22,v206
v_mfma_f32_16x16x32_bf16 a[8:11],a[140:143],v[108:111],a[8:11]
v_mul_f32 v207,v22,v207
v_cvt_pk_bf16_f32 v205,v206,v207
v_mfma_f32_16x16x32_bf16 a[12:15],a[140:143],v[124:127],0
ds_read_b64_tr_b16 a[164:165],v1 offset:10432
ds_read_b64_tr_b16 a[166:167],v1 offset:15552
v_mfma_f32_16x16x32_bf16 a[12:15],a[136:139],v[120:123],a[12:15]
s_add_u32 m0,s82,126976
v_accvgpr_read_b32 v206,a76
v_accvgpr_read_b32 v207,a77
v_mfma_f32_16x16x32_bf16 a[12:15],a[132:135],v[116:119],a[12:15]
v_accvgpr_read_b32 v12,a78
v_accvgpr_read_b32 v13,a79
v_mfma_f32_16x16x32_bf16 a[12:15],a[128:131],v[112:115],a[12:15]
s_waitcnt lgkmcnt(4)
v_mul_f32 v206,v22,v206
v_mul_f32 v207,v22,v207
v_mfma_f32_16x16x32_bf16 a[16:19],a[144:147],v[64:67],0
buffer_load_dwordx4 v11,s[72:75],s80 offen lds
v_mfma_f32_16x16x32_bf16 a[16:19],a[148:151],v[68:71],a[16:19]
ds_read_b64_tr_b16 a[168:169],v1 offset:20672
ds_read_b64_tr_b16 a[170:171],v1 offset:25792
v_mfma_f32_16x16x32_bf16 a[16:19],a[152:155],v[72:75],a[16:19]
v_mul_f32 v12,v22,v12
v_mul_f32 v13,v22,v13
v_mfma_f32_16x16x32_bf16 a[16:19],a[156:159],v[76:79],a[16:19]
v_cvt_pk_bf16_f32 v206,v206,v207
v_cvt_pk_bf16_f32 v207,v12,v13
v_mfma_f32_16x16x32_bf16 a[20:23],a[156:159],v[92:95],0
buffer_store_dwordx4 v[172:175],v6,s[36:39],s85 offen offset:1024 sc1
v_mfma_f32_16x16x32_bf16 a[20:23],a[152:155],v[88:91],a[20:23]
)A"
R"A(ds_read_b64_tr_b16 a[172:173],v1 offset:30912
ds_read_b64_tr_b16 a[174:175],v1 offset:36032
v_mfma_f32_16x16x32_bf16 a[20:23],a[148:151],v[84:87],a[20:23]
v_accvgpr_read_b32 v216,a80
v_accvgpr_read_b32 v217,a81
v_mfma_f32_16x16x32_bf16 a[20:23],a[144:147],v[80:83],a[20:23]
v_mul_f32 v216,v22,v216
v_mul_f32 v217,v22,v217
v_mfma_f32_16x16x32_bf16 a[24:27],a[144:147],v[96:99],0
v_accvgpr_read_b32 v218,a82
v_accvgpr_read_b32 v219,a83
v_mfma_f32_16x16x32_bf16 a[24:27],a[148:151],v[100:103],a[24:27]
s_add_u32 m0,s82,128000
v_cvt_pk_bf16_f32 v216,v216,v217
v_mul_f32 v218,v22,v218
v_mfma_f32_16x16x32_bf16 a[24:27],a[152:155],v[104:107],a[24:27]
v_mul_f32 v219,v22,v219
v_cvt_pk_bf16_f32 v217,v218,v219
v_mfma_f32_16x16x32_bf16 a[24:27],a[156:159],v[108:111],a[24:27]
v_accvgpr_read_b32 v218,a84
v_accvgpr_read_b32 v219,a85
v_mfma_f32_16x16x32_bf16 a[28:31],a[156:159],v[124:127],0
buffer_load_dwordx4 v7,s[72:75],s83 offen lds
v_mfma_f32_16x16x32_bf16 a[28:31],a[152:155],v[120:123],a[28:31]
v_accvgpr_read_b32 v14,a86
v_accvgpr_read_b32 v15,a87
v_mfma_f32_16x16x32_bf16 a[28:31],a[148:151],v[116:119],a[28:31]
v_mul_f32 v218,v22,v218
v_mul_f32 v219,v22,v219
v_mfma_f32_16x16x32_bf16 a[28:31],a[144:147],v[112:115],a[28:31]
s_waitcnt lgkmcnt(0)
v_mul_f32 v14,v22,v14
v_mul_f32 v15,v22,v15
v_mfma_f32_16x16x32_bf16 a[32:35],a[160:163],v[64:67],0
v_cvt_pk_bf16_f32 v218,v218,v219
v_cvt_pk_bf16_f32 v219,v14,v15
v_mfma_f32_16x16x32_bf16 a[32:35],a[164:167],v[68:71],a[32:35]
ds_read_b64_tr_b16 a[176:177],v2 offset:224
ds_read_b64_tr_b16 a[178:179],v2 offset:5344
v_mfma_f32_16x16x32_bf16 a[32:35],a[168:171],v[72:75],a[32:35]
ds_read_b64_tr_b16 a[180:181],v2 offset:10464
v_bfe_u32 v24,v6,4,2
v_mfma_f32_16x16x32_bf16 a[32:35],a[172:175],v[76:79],a[32:35]
ds_read_b64_tr_b16 a[182:183],v2 offset:15584
v_min_u32 v24,2,v24
v_mfma_f32_16x16x32_bf16 a[36:39],a[172:175],v[92:95],0
ds_read_b64_tr_b16 a[184:185],v2 offset:20704
v_add_u32 v24,s71,v24
v_mfma_f32_16x16x32_bf16 a[36:39],a[168:171],v[88:91],a[36:39]
ds_read_b64_tr_b16 a[186:187],v2 offset:25824
ds_read_b64_tr_b16 a[188:189],v2 offset:30944
v_mfma_f32_16x16x32_bf16 a[36:39],a[164:167],v[84:87],a[36:39]
buffer_store_dwordx4 v[184:187],v6,s[36:39],s85 offen offset:2048 sc1
v_mfma_f32_16x16x32_bf16 a[36:39],a[160:163],v[80:83],a[36:39]
ds_read_b64_tr_b16 a[190:191],v2 offset:36064
v_add_u32 v24,1,v24
v_mfma_f32_16x16x32_bf16 a[40:43],a[160:163],v[96:99],0
v_and_b32 v24,3,v24
v_lshlrev_b32 v24,6,v24
v_mfma_f32_16x16x32_bf16 a[40:43],a[164:167],v[100:103],a[40:43]
v_accvgpr_read_b32 v220,a88
v_accvgpr_read_b32 v221,a89
v_mfma_f32_16x16x32_bf16 a[40:43],a[168:171],v[104:107],a[40:43]
buffer_load_dwordx4 v8,s[72:75],s83 offen offset:1024 lds
v_mfma_f32_16x16x32_bf16 a[40:43],a[172:175],v[108:111],a[40:43]
v_mul_f32 v220,v22,v220
v_mul_f32 v221,v22,v221
v_mfma_f32_16x16x32_bf16 a[44:47],a[172:175],v[124:127],0
v_accvgpr_read_b32 v222,a90
v_accvgpr_read_b32 v223,a91
v_mfma_f32_16x16x32_bf16 a[44:47],a[168:171],v[120:123],a[44:47]
v_cvt_pk_bf16_f32 v220,v220,v221
v_mul_f32 v222,v22,v222
v_mfma_f32_16x16x32_bf16 a[44:47],a[164:167],v[116:119],a[44:47]
buffer_load_dwordx2 v[26:27],v24,s[64:67],s94 offen sc1
v_mfma_f32_16x16x32_bf16 a[44:47],a[160:163],v[112:115],a[44:47]
s_waitcnt lgkmcnt(0)
v_mul_f32 v223,v22,v223
v_cvt_pk_bf16_f32 v221,v222,v223
v_mfma_f32_16x16x32_bf16 a[48:51],a[176:179],v[64:67],0
v_accvgpr_read_b32 v222,a92
v_accvgpr_read_b32 v223,a93
v_mfma_f32_16x16x32_bf16 a[48:51],a[180:183],v[68:71],a[48:51]
v_accvgpr_read_b32 v16,a94
v_accvgpr_read_b32 v17,a95
v_mfma_f32_16x16x32_bf16 a[48:51],a[184:187],v[72:75],a[48:51]
v_mul_f32 v222,v22,v222
v_mul_f32 v223,v22,v223
v_mfma_f32_16x16x32_bf16 a[48:51],a[188:191],v[76:79],a[48:51]
v_mul_f32 v16,v22,v16
v_mul_f32 v17,v22,v17
v_mfma_f32_16x16x32_bf16 a[52:55],a[188:191],v[92:95],0
v_cvt_pk_bf16_f32 v222,v222,v223
v_cvt_pk_bf16_f32 v223,v16,v17
v_mfma_f32_16x16x32_bf16 a[52:55],a[184:187],v[88:91],a[52:55]
buffer_load_dwordx4 v9,s[72:75],s83 offen offset:2048 lds
v_mfma_f32_16x16x32_bf16 a[52:55],a[180:183],v[84:87],a[52:55]
ds_read_b64_tr_b16 a[128:129],v1 offset:256
ds_read_b64_tr_b16 a[130:131],v1 offset:5376
v_mfma_f32_16x16x32_bf16 a[52:55],a[176:179],v[80:83],a[52:55]
ds_read_b64_tr_b16 a[132:133],v1 offset:10496
v_add_f32 v60,v61,v60
v_mfma_f32_16x16x32_bf16 a[56:59],a[176:179],v[96:99],0
ds_read_b64_tr_b16 a[134:135],v1 offset:15616
v_max_f32 v23,0x00800000,v60
v_mfma_f32_16x16x32_bf16 a[56:59],a[180:183],v[100:103],a[56:59]
ds_read_b64_tr_b16 a[136:137],v1 offset:20736
ds_read_b64_tr_b16 a[138:139],v1 offset:25856
v_mfma_f32_16x16x32_bf16 a[56:59],a[184:187],v[104:107],a[56:59]
ds_read_b64_tr_b16 a[140:141],v1 offset:30976
ds_read_b64_tr_b16 a[142:143],v1 offset:36096
v_mfma_f32_16x16x32_bf16 a[56:59],a[188:191],v[108:111],a[56:59]
buffer_store_dwordx4 v[188:191],v6,s[36:39],s85 offen offset:3072 sc1
v_mfma_f32_16x16x32_bf16 a[60:63],a[188:191],v[124:127],0
v_rcp_f32 v23,v23
v_mfma_f32_16x16x32_bf16 a[60:63],a[184:187],v[120:123],a[60:63]
v_accvgpr_read_b32 v232,a96
v_accvgpr_read_b32 v233,a97
v_mfma_f32_16x16x32_bf16 a[60:63],a[180:183],v[116:119],a[60:63]
v_mul_f32 v232,v23,v232
v_mul_f32 v233,v23,v233
v_mfma_f32_16x16x32_bf16 a[60:63],a[176:179],v[112:115],a[60:63]
s_waitcnt lgkmcnt(5)
v_accvgpr_read_b32 v234,a98
v_accvgpr_read_b32 v235,a99
v_mfma_f32_16x16x32_bf16 a[64:67],a[128:131],v[64:67],0
s_waitcnt lgkmcnt(4)
v_cvt_pk_bf16_f32 v232,v232,v233
v_mul_f32 v234,v23,v234
v_mfma_f32_16x16x32_bf16 a[64:67],a[132:135],v[68:71],a[64:67]
s_waitcnt lgkmcnt(2)
v_mul_f32 v235,v23,v235
v_cvt_pk_bf16_f32 v233,v234,v235
v_mfma_f32_16x16x32_bf16 a[64:67],a[136:139],v[72:75],a[64:67]
s_waitcnt lgkmcnt(0)
v_accvgpr_read_b32 v234,a100
v_accvgpr_read_b32 v235,a101
v_mfma_f32_16x16x32_bf16 a[64:67],a[140:143],v[76:79],a[64:67]
buffer_load_dwordx4 v10,s[72:75],s83 offen offset:3072 lds
v_mfma_f32_16x16x32_bf16 a[68:71],a[140:143],v[92:95],0
v_accvgpr_read_b32 v12,a102
v_accvgpr_read_b32 v13,a103
v_mfma_f32_16x16x32_bf16 a[68:71],a[136:139],v[88:91],a[68:71]
v_mul_f32 v234,v23,v234
v_mul_f32 v235,v23,v235
v_mfma_f32_16x16x32_bf16 a[68:71],a[132:135],v[84:87],a[68:71]
v_mul_f32 v12,v23,v12
v_mul_f32 v13,v23,v13
v_mfma_f32_16x16x32_bf16 a[68:71],a[128:131],v[80:83],a[68:71]
ds_read_b64_tr_b16 a[144:145],v2 offset:288
ds_read_b64_tr_b16 a[146:147],v2 offset:5408
v_mfma_f32_16x16x32_bf16 a[72:75],a[128:131],v[96:99],0
ds_read_b64_tr_b16 a[148:149],v2 offset:10528
ds_read_b64_tr_b16 a[150:151],v2 offset:15648
v_mfma_f32_16x16x32_bf16 a[72:75],a[132:135],v[100:103],a[72:75]
ds_read_b64_tr_b16 a[152:153],v2 offset:20768
ds_read_b64_tr_b16 a[154:155],v2 offset:25888
v_mfma_f32_16x16x32_bf16 a[72:75],a[136:139],v[104:107],a[72:75]
ds_read_b64_tr_b16 a[156:157],v2 offset:31008
ds_read_b64_tr_b16 a[158:159],v2 offset:36128
v_mfma_f32_16x16x32_bf16 a[72:75],a[140:143],v[108:111],a[72:75]
s_add_u32 m0,s82,132096
v_mfma_f32_16x16x32_bf16 a[76:79],a[140:143],v[124:127],0
v_cvt_pk_bf16_f32 v234,v234,v235
v_cvt_pk_bf16_f32 v235,v12,v13
v_mfma_f32_16x16x32_bf16 a[76:79],a[136:139],v[120:123],a[76:79]
v_accvgpr_read_b32 v236,a104
v_accvgpr_read_b32 v237,a105
v_mfma_f32_16x16x32_bf16 a[76:79],a[132:135],v[116:119],a[76:79]
v_mul_f32 v236,v23,v236
v_mul_f32 v237,v23,v237
v_mfma_f32_16x16x32_bf16 a[76:79],a[128:131],v[112:115],a[76:79]
s_waitcnt lgkmcnt(6)
buffer_load_dwordx4 v11,s[72:75],s83 offen lds
v_mfma_f32_16x16x32_bf16 a[80:83],a[144:147],v[64:67],0
s_waitcnt lgkmcnt(4)
v_accvgpr_read_b32 v238,a106
v_accvgpr_read_b32 v239,a107
v_mfma_f32_16x16x32_bf16 a[80:83],a[148:151],v[68:71],a[80:83]
s_waitcnt lgkmcnt(2)
v_cvt_pk_bf16_f32 v236,v236,v237
v_mul_f32 v238,v23,v238
v_mfma_f32_16x16x32_bf16 a[80:83],a[152:155],v[72:75],a[80:83]
s_waitcnt lgkmcnt(0)
v_mul_f32 v239,v23,v239
v_cvt_pk_bf16_f32 v237,v238,v239
v_mfma_f32_16x16x32_bf16 a[80:83],a[156:159],v[76:79],a[80:83]
v_accvgpr_read_b32 v238,a108
v_accvgpr_read_b32 v239,a109
v_mfma_f32_16x16x32_bf16 a[84:87],a[156:159],v[92:95],0
v_accvgpr_read_b32 v14,a110
v_accvgpr_read_b32 v15,a111
v_mfma_f32_16x16x32_bf16 a[84:87],a[152:155],v[88:91],a[84:87]
v_mul_f32 v238,v23,v238
v_mul_f32 v239,v23,v239
v_mfma_f32_16x16x32_bf16 a[84:87],a[148:151],v[84:87],a[84:87]
v_subrev_u32 v1,81920,v1
v_mul_f32 v14,v23,v14
v_mfma_f32_16x16x32_bf16 a[84:87],a[144:147],v[80:83],a[84:87]
ds_read_b64_tr_b16 a[160:161],v1
ds_read_b64_tr_b16 a[162:163],v1 offset:5120
v_mfma_f32_16x16x32_bf16 a[88:91],a[144:147],v[96:99],0
ds_read_b64_tr_b16 a[164:165],v1 offset:10240
ds_read_b64_tr_b16 a[166:167],v1 offset:15360
v_mfma_f32_16x16x32_bf16 a[88:91],a[148:151],v[100:103],a[88:91]
ds_read_b64_tr_b16 a[168:169],v1 offset:20480
ds_read_b64_tr_b16 a[170:171],v1 offset:25600
v_mfma_f32_16x16x32_bf16 a[88:91],a[152:155],v[104:107],a[88:91]
ds_read_b64_tr_b16 a[172:173],v1 offset:30720
ds_read_b64_tr_b16 a[174:175],v1 offset:35840
v_mfma_f32_16x16x32_bf16 a[88:91],a[156:159],v[108:111],a[88:91]
v_accvgpr_read_b32 v248,a112
v_accvgpr_read_b32 v249,a113
v_mul_f32 v248,v23,v248
v_mul_f32 v249,v23,v249
v_accvgpr_read_b32 v250,a114
v_accvgpr_read_b32 v251,a115
v_accvgpr_read_b32 v252,a120
v_accvgpr_read_b32 v253,a121
v_cmp_nlt_f32 s[56:57],v48,s63
v_mul_f32 v252,v23,v252
v_mul_f32 v253,v23,v253
v_accvgpr_read_b32 v254,a122
v_accvgpr_read_b32 v255,a123
v_mfma_f32_16x16x32_bf16 a[92:95],a[156:159],v[124:127],0
v_cvt_pk_bf16_f32 v248,v248,v249
v_mul_f32 v250,v23,v250
v_mul_f32 v251,v23,v251
v_cvt_pk_bf16_f32 v249,v250,v251
v_cvt_pk_bf16_f32 v252,v252,v253
v_mul_f32 v254,v23,v254
v_mul_f32 v255,v23,v255
v_cmp_nlt_f32 s[58:59],v52,s63
s_nop 1
s_or_b64 s[56:57],s[56:57],s[58:59]
v_cvt_pk_bf16_f32 v253,v254,v255
v_mfma_f32_16x16x32_bf16 a[92:95],a[152:155],v[120:123],a[92:95]
v_accvgpr_read_b32 v250,a116
v_accvgpr_read_b32 v251,a117
v_accvgpr_read_b32 v16,a118
v_accvgpr_read_b32 v17,a119
v_mul_f32 v250,v23,v250
v_mul_f32 v251,v23,v251
v_accvgpr_read_b32 v254,a124
v_accvgpr_read_b32 v255,a125
v_cmp_nlt_f32 s[58:59],v56,s63
s_nop 1
s_or_b64 s[56:57],s[56:57],s[58:59]
v_accvgpr_read_b32 v12,a126
v_accvgpr_read_b32 v13,a127
v_mul_f32 v254,v23,v254
v_mul_f32 v255,v23,v255
s_waitcnt vmcnt(4)
v_cmp_ne_u32 s[90:91],s68,v26
v_cmp_ne_u32 s[92:93],s69,v27
s_nop 0
s_or_b64 s[90:91],s[90:91],s[92:93]
s_cbranch_scc1 L_fr8_%=
L_fo8_%=:
v_mfma_f32_16x16x32_bf16 a[92:95],a[148:151],v[116:119],a[92:95]
v_mul_f32 v15,v23,v15
v_cvt_pk_bf16_f32 v238,v238,v239
v_cvt_pk_bf16_f32 v239,v14,v15
v_mul_f32 v16,v23,v16
v_mul_f32 v17,v23,v17
v_cvt_pk_bf16_f32 v250,v250,v251
v_cvt_pk_bf16_f32 v251,v16,v17
v_mul_f32 v12,v23,v12
v_mul_f32 v13,v23,v13
v_cvt_pk_bf16_f32 v254,v254,v255
v_cmp_nlt_f32 s[58:59],v60,s63
s_nop 1
s_or_b64 s[56:57],s[56:57],s[58:59]
v_cvt_pk_bf16_f32 v255,v12,v13
v_mfma_f32_16x16x32_bf16 a[92:95],a[144:147],v[112:115],a[92:95]
s_cmp_lg_u64 s[56:57],0
s_cbranch_scc1 L_slow1_%=
L_sret1_%=:
v_subrev_u32 v2,81920,v2
s_waitcnt lgkmcnt(6)
v_mfma_f32_16x16x32_bf16 a[192:195],a[160:163],v[136:139],a[192:195]
s_cmp_ge_u32 0,s71
s_cselect_b32 s76,2,0
s_lshl_b32 s76,s76,16
s_waitcnt lgkmcnt(4)
v_mfma_f32_16x16x32_bf16 a[192:195],a[164:167],v[140:143],a[192:195]
s_add_u32 s77,s76,4096
s_add_u32 s78,s76,8192
s_add_u32 s79,s76,12288
s_max_i32 s76,s76,s18
s_waitcnt lgkmcnt(2)
v_mfma_f32_16x16x32_bf16 a[192:195],a[168:171],v[152:155],a[192:195]
s_max_i32 s77,s77,s19
buffer_load_dwordx4 v[128:131],v6,s[28:31],s76 offen sc1
s_waitcnt lgkmcnt(0)
v_mfma_f32_16x16x32_bf16 a[192:195],a[172:175],v[156:159],a[192:195]
s_max_i32 s78,s78,s100
ds_read_b64_tr_b16 a[176:177],v2 offset:32
ds_read_b64_tr_b16 a[178:179],v2 offset:5152
v_mfma_f32_16x16x32_bf16 a[196:199],a[172:175],v[188:191],a[196:199]
s_cmp_ge_u32 2,s71
s_cselect_b32 s80,6,4
ds_read_b64_tr_b16 a[180:181],v2 offset:10272
v_mfma_f32_16x16x32_bf16 a[196:199],a[168:171],v[184:187],a[196:199]
s_mul_i32 s80,s80,163840
s_add_u32 s80,s80,s81
ds_read_b64_tr_b16 a[182:183],v2 offset:15392
v_mfma_f32_16x16x32_bf16 a[196:199],a[164:167],v[172:175],a[196:199]
s_max_i32 s79,s79,s101
buffer_store_dwordx4 v[200:203],v6,s[36:39],s86 offen sc1
)A"
R"A(v_mfma_f32_16x16x32_bf16 a[196:199],a[160:163],v[168:171],a[196:199]
s_add_u32 s83,s80,20480
ds_read_b64_tr_b16 a[184:185],v2 offset:20512
ds_read_b64_tr_b16 a[186:187],v2 offset:25632
v_mfma_f32_16x16x32_bf16 a[200:203],a[160:163],v[200:203],a[200:203]
ds_read_b64_tr_b16 a[188:189],v2 offset:30752
v_mfma_f32_16x16x32_bf16 a[200:203],a[164:167],v[204:207],a[200:203]
ds_read_b64_tr_b16 a[190:191],v2 offset:35872
v_mfma_f32_16x16x32_bf16 a[200:203],a[168:171],v[216:219],a[200:203]
buffer_load_dwordx4 v[160:163],v6,s[28:31],s76 offen offset:1024 sc1
v_mfma_f32_16x16x32_bf16 a[200:203],a[172:175],v[220:223],a[200:203]
ds_read_b64_tr_b16 a[128:129],v1 offset:64
ds_read_b64_tr_b16 a[130:131],v1 offset:5184
v_mfma_f32_16x16x32_bf16 a[204:207],a[172:175],v[252:255],a[204:207]
ds_read_b64_tr_b16 a[132:133],v1 offset:10304
v_mfma_f32_16x16x32_bf16 a[204:207],a[168:171],v[248:251],a[204:207]
ds_read_b64_tr_b16 a[134:135],v1 offset:15424
v_mfma_f32_16x16x32_bf16 a[204:207],a[164:167],v[236:239],a[204:207]
buffer_store_dwordx4 v[204:207],v6,s[36:39],s86 offen offset:1024 sc1
v_mfma_f32_16x16x32_bf16 a[204:207],a[160:163],v[232:235],a[204:207]
s_barrier
ds_read_b64_tr_b16 a[136:137],v1 offset:20544
ds_read_b64_tr_b16 a[138:139],v1 offset:25664
s_waitcnt lgkmcnt(8)
v_mfma_f32_16x16x32_bf16 a[208:211],a[176:179],v[136:139],a[208:211]
ds_read_b64_tr_b16 a[140:141],v1 offset:30784
v_mfma_f32_16x16x32_bf16 a[208:211],a[180:183],v[140:143],a[208:211]
buffer_load_dwordx4 v[192:195],v6,s[28:31],s76 offen offset:2048 sc1
v_mfma_f32_16x16x32_bf16 a[208:211],a[184:187],v[152:155],a[208:211]
s_add_u32 m0,s82,81920
ds_read_b64_tr_b16 a[142:143],v1 offset:35904
ds_read_b64_tr_b16 a[144:145],v2 offset:96
s_waitcnt lgkmcnt(9)
v_mfma_f32_16x16x32_bf16 a[208:211],a[188:191],v[156:159],a[208:211]
ds_read_b64_tr_b16 a[146:147],v2 offset:5216
v_mfma_f32_16x16x32_bf16 a[212:215],a[188:191],v[188:191],a[212:215]
ds_read_b64_tr_b16 a[148:149],v2 offset:10336
v_mfma_f32_16x16x32_bf16 a[212:215],a[184:187],v[184:187],a[212:215]
buffer_store_dwordx4 v[216:219],v6,s[36:39],s86 offen offset:2048 sc1
v_mfma_f32_16x16x32_bf16 a[212:215],a[180:183],v[172:175],a[212:215]
ds_read_b64_tr_b16 a[150:151],v2 offset:15456
ds_read_b64_tr_b16 a[152:153],v2 offset:20576
v_mfma_f32_16x16x32_bf16 a[212:215],a[176:179],v[168:171],a[212:215]
ds_read_b64_tr_b16 a[154:155],v2 offset:25696
v_mfma_f32_16x16x32_bf16 a[216:219],a[176:179],v[200:203],a[216:219]
ds_read_b64_tr_b16 a[156:157],v2 offset:30816
v_mfma_f32_16x16x32_bf16 a[216:219],a[180:183],v[204:207],a[216:219]
buffer_load_dwordx4 v[224:227],v6,s[28:31],s76 offen offset:3072 sc1
v_mfma_f32_16x16x32_bf16 a[216:219],a[184:187],v[216:219],a[216:219]
ds_read_b64_tr_b16 a[158:159],v2 offset:35936
ds_read_b64_tr_b16 a[160:161],v1 offset:128
v_mfma_f32_16x16x32_bf16 a[216:219],a[188:191],v[220:223],a[216:219]
ds_read_b64_tr_b16 a[162:163],v1 offset:5248
v_mfma_f32_16x16x32_bf16 a[220:223],a[188:191],v[252:255],a[220:223]
ds_read_b64_tr_b16 a[164:165],v1 offset:10368
v_mfma_f32_16x16x32_bf16 a[220:223],a[184:187],v[248:251],a[220:223]
buffer_load_dwordx4 v7,s[72:75],s80 offen lds
v_mfma_f32_16x16x32_bf16 a[220:223],a[180:183],v[236:239],a[220:223]
ds_read_b64_tr_b16 a[166:167],v1 offset:15488
ds_read_b64_tr_b16 a[168:169],v1 offset:20608
v_mfma_f32_16x16x32_bf16 a[220:223],a[176:179],v[232:235],a[220:223]
s_waitcnt lgkmcnt(7)
ds_read_b64_tr_b16 a[170:171],v1 offset:25728
v_mfma_f32_16x16x32_bf16 a[224:227],a[128:131],v[136:139],a[224:227]
ds_read_b64_tr_b16 a[172:173],v1 offset:30848
v_mfma_f32_16x16x32_bf16 a[224:227],a[132:135],v[140:143],a[224:227]
buffer_load_dwordx4 v[132:135],v6,s[28:31],s77 offen sc1
v_mfma_f32_16x16x32_bf16 a[224:227],a[136:139],v[152:155],a[224:227]
ds_read_b64_tr_b16 a[174:175],v1 offset:35968
ds_read_b64_tr_b16 a[176:177],v2 offset:160
v_mfma_f32_16x16x32_bf16 a[224:227],a[140:143],v[156:159],a[224:227]
ds_read_b64_tr_b16 a[178:179],v2 offset:5280
v_mfma_f32_16x16x32_bf16 a[228:231],a[140:143],v[188:191],a[228:231]
ds_read_b64_tr_b16 a[180:181],v2 offset:10400
v_mfma_f32_16x16x32_bf16 a[228:231],a[136:139],v[184:187],a[228:231]
buffer_load_dwordx4 v[164:167],v6,s[28:31],s77 offen offset:1024 sc1
v_mfma_f32_16x16x32_bf16 a[228:231],a[132:135],v[172:175],a[228:231]
ds_read_b64_tr_b16 a[182:183],v2 offset:15520
ds_read_b64_tr_b16 a[184:185],v2 offset:20640
v_mfma_f32_16x16x32_bf16 a[228:231],a[128:131],v[168:171],a[228:231]
ds_read_b64_tr_b16 a[186:187],v2 offset:25760
v_mfma_f32_16x16x32_bf16 a[232:235],a[128:131],v[200:203],a[232:235]
ds_read_b64_tr_b16 a[188:189],v2 offset:30880
v_mfma_f32_16x16x32_bf16 a[232:235],a[132:135],v[204:207],a[232:235]
buffer_load_dwordx4 v8,s[72:75],s80 offen offset:1024 lds
v_mfma_f32_16x16x32_bf16 a[232:235],a[136:139],v[216:219],a[232:235]
ds_read_b64_tr_b16 a[190:191],v2 offset:36000
v_mfma_f32_16x16x32_bf16 a[232:235],a[140:143],v[220:223],a[232:235]
v_mfma_f32_16x16x32_bf16 a[236:239],a[140:143],v[252:255],a[236:239]
ds_read_b64_tr_b16 a[140:141],v1 offset:30912
ds_read_b64_tr_b16 a[142:143],v1 offset:36032
v_mfma_f32_16x16x32_bf16 a[236:239],a[136:139],v[248:251],a[236:239]
buffer_load_dwordx4 v[196:199],v6,s[28:31],s77 offen offset:2048 sc1
v_mfma_f32_16x16x32_bf16 a[236:239],a[132:135],v[236:239],a[236:239]
ds_read_b64_tr_b16 a[132:133],v1 offset:10432
ds_read_b64_tr_b16 a[134:135],v1 offset:15552
v_mfma_f32_16x16x32_bf16 a[236:239],a[128:131],v[232:235],a[236:239]
ds_read_b64_tr_b16 a[128:129],v1 offset:192
v_mfma_f32_16x16x32_bf16 a[240:243],a[144:147],v[136:139],a[240:243]
ds_read_b64_tr_b16 a[130:131],v1 offset:5312
v_mfma_f32_16x16x32_bf16 a[240:243],a[148:151],v[140:143],a[240:243]
buffer_store_dwordx4 v[220:223],v6,s[36:39],s86 offen offset:3072 sc1
v_mfma_f32_16x16x32_bf16 a[240:243],a[152:155],v[152:155],a[240:243]
s_waitcnt lgkmcnt(6)
ds_read_b64_tr_b16 a[136:137],v1 offset:20672
ds_read_b64_tr_b16 a[138:139],v1 offset:25792
v_mfma_f32_16x16x32_bf16 a[240:243],a[156:159],v[156:159],a[240:243]
v_mfma_f32_16x16x32_bf16 a[244:247],a[156:159],v[188:191],a[244:247]
v_mfma_f32_16x16x32_bf16 a[244:247],a[152:155],v[184:187],a[244:247]
buffer_load_dwordx4 v[228:231],v6,s[28:31],s77 offen offset:3072 sc1
v_mfma_f32_16x16x32_bf16 a[244:247],a[148:151],v[172:175],a[244:247]
v_mfma_f32_16x16x32_bf16 a[244:247],a[144:147],v[168:171],a[244:247]
v_mfma_f32_16x16x32_bf16 a[248:251],a[144:147],v[200:203],a[248:251]
v_mfma_f32_16x16x32_bf16 a[248:251],a[148:151],v[204:207],a[248:251]
buffer_load_dwordx4 v9,s[72:75],s80 offen offset:2048 lds
v_mfma_f32_16x16x32_bf16 a[248:251],a[152:155],v[216:219],a[248:251]
v_mfma_f32_16x16x32_bf16 a[248:251],a[156:159],v[220:223],a[248:251]
v_mfma_f32_16x16x32_bf16 a[252:255],a[156:159],v[252:255],a[252:255]
ds_read_b64_tr_b16 a[156:157],v2 offset:30944
ds_read_b64_tr_b16 a[158:159],v2 offset:36064
v_mfma_f32_16x16x32_bf16 a[252:255],a[152:155],v[248:251],a[252:255]
buffer_load_dwordx4 v[144:147],v6,s[28:31],s78 offen sc1
v_mfma_f32_16x16x32_bf16 a[252:255],a[148:151],v[236:239],a[252:255]
ds_read_b64_tr_b16 a[148:149],v2 offset:10464
ds_read_b64_tr_b16 a[150:151],v2 offset:15584
v_mfma_f32_16x16x32_bf16 a[252:255],a[144:147],v[232:235],a[252:255]
ds_read_b64_tr_b16 a[144:145],v2 offset:224
v_mfma_f32_16x16x32_bf16 a[0:3],a[160:163],v[136:139],a[0:3]
ds_read_b64_tr_b16 a[146:147],v2 offset:5344
v_mfma_f32_16x16x32_bf16 a[0:3],a[164:167],v[140:143],a[0:3]
buffer_load_dwordx4 v[176:179],v6,s[28:31],s78 offen offset:1024 sc1
v_mfma_f32_16x16x32_bf16 a[0:3],a[168:171],v[152:155],a[0:3]
ds_read_b64_tr_b16 a[152:153],v2 offset:20704
ds_read_b64_tr_b16 a[154:155],v2 offset:25824
v_mfma_f32_16x16x32_bf16 a[0:3],a[172:175],v[156:159],a[0:3]
v_mfma_f32_16x16x32_bf16 a[4:7],a[172:175],v[188:191],a[4:7]
v_mfma_f32_16x16x32_bf16 a[4:7],a[168:171],v[184:187],a[4:7]
buffer_load_dwordx4 v10,s[72:75],s80 offen offset:3072 lds
v_mfma_f32_16x16x32_bf16 a[4:7],a[164:167],v[172:175],a[4:7]
v_mfma_f32_16x16x32_bf16 a[4:7],a[160:163],v[168:171],a[4:7]
v_mfma_f32_16x16x32_bf16 a[8:11],a[160:163],v[200:203],a[8:11]
v_mfma_f32_16x16x32_bf16 a[8:11],a[164:167],v[204:207],a[8:11]
buffer_load_dwordx4 v[208:211],v6,s[28:31],s78 offen offset:2048 sc1
v_mfma_f32_16x16x32_bf16 a[8:11],a[168:171],v[216:219],a[8:11]
v_mfma_f32_16x16x32_bf16 a[8:11],a[172:175],v[220:223],a[8:11]
v_mfma_f32_16x16x32_bf16 a[12:15],a[172:175],v[252:255],a[12:15]
ds_read_b64_tr_b16 a[172:173],v1 offset:30976
ds_read_b64_tr_b16 a[174:175],v1 offset:36096
v_mfma_f32_16x16x32_bf16 a[12:15],a[168:171],v[248:251],a[12:15]
buffer_store_dwordx4 v[232:235],v6,s[36:39],s87 offen sc1
v_mfma_f32_16x16x32_bf16 a[12:15],a[164:167],v[236:239],a[12:15]
s_add_u32 m0,s82,86016
ds_read_b64_tr_b16 a[164:165],v1 offset:10496
ds_read_b64_tr_b16 a[166:167],v1 offset:15616
v_mfma_f32_16x16x32_bf16 a[12:15],a[160:163],v[232:235],a[12:15]
ds_read_b64_tr_b16 a[160:161],v1 offset:256
v_mfma_f32_16x16x32_bf16 a[16:19],a[176:179],v[136:139],a[16:19]
ds_read_b64_tr_b16 a[162:163],v1 offset:5376
v_mfma_f32_16x16x32_bf16 a[16:19],a[180:183],v[140:143],a[16:19]
buffer_load_dwordx4 v[240:243],v6,s[28:31],s78 offen offset:3072 sc1
v_mfma_f32_16x16x32_bf16 a[16:19],a[184:187],v[152:155],a[16:19]
ds_read_b64_tr_b16 a[168:169],v1 offset:20736
ds_read_b64_tr_b16 a[170:171],v1 offset:25856
v_mfma_f32_16x16x32_bf16 a[16:19],a[188:191],v[156:159],a[16:19]
v_add_u32 v1,40960,v1
v_mfma_f32_16x16x32_bf16 a[20:23],a[188:191],v[188:191],a[20:23]
ds_read_b64_tr_b16 a[96:97],v1 offset:128
ds_read_b64_tr_b16 a[98:99],v1 offset:5248
v_mfma_f32_16x16x32_bf16 a[20:23],a[184:187],v[184:187],a[20:23]
buffer_load_dwordx4 v11,s[72:75],s80 offen lds
v_mfma_f32_16x16x32_bf16 a[20:23],a[180:183],v[172:175],a[20:23]
ds_read_b64_tr_b16 a[100:101],v1 offset:10368
ds_read_b64_tr_b16 a[102:103],v1 offset:15488
v_mfma_f32_16x16x32_bf16 a[20:23],a[176:179],v[168:171],a[20:23]
ds_read_b64_tr_b16 a[104:105],v1 offset:20608
v_mfma_f32_16x16x32_bf16 a[24:27],a[176:179],v[200:203],a[24:27]
ds_read_b64_tr_b16 a[106:107],v1 offset:25728
v_mfma_f32_16x16x32_bf16 a[24:27],a[180:183],v[204:207],a[24:27]
buffer_load_dwordx4 v[148:151],v6,s[28:31],s79 offen sc1
v_mfma_f32_16x16x32_bf16 a[24:27],a[184:187],v[216:219],a[24:27]
ds_read_b64_tr_b16 a[108:109],v1 offset:30848
ds_read_b64_tr_b16 a[110:111],v1 offset:35968
v_mfma_f32_16x16x32_bf16 a[24:27],a[188:191],v[220:223],a[24:27]
v_mfma_f32_16x16x32_bf16 a[28:31],a[188:191],v[252:255],a[28:31]
ds_read_b64_tr_b16 a[188:189],v2 offset:31008
ds_read_b64_tr_b16 a[190:191],v2 offset:36128
v_mfma_f32_16x16x32_bf16 a[28:31],a[184:187],v[248:251],a[28:31]
s_add_u32 m0,s82,87040
buffer_load_dwordx4 v[180:183],v6,s[28:31],s79 offen offset:1024 sc1
v_mfma_f32_16x16x32_bf16 a[28:31],a[180:183],v[236:239],a[28:31]
ds_read_b64_tr_b16 a[180:181],v2 offset:10528
ds_read_b64_tr_b16 a[182:183],v2 offset:15648
v_mfma_f32_16x16x32_bf16 a[28:31],a[176:179],v[232:235],a[28:31]
s_waitcnt lgkmcnt(7)
ds_read_b64_tr_b16 a[176:177],v2 offset:288
v_mfma_f32_16x16x32_bf16 a[32:35],a[128:131],v[136:139],a[32:35]
ds_read_b64_tr_b16 a[178:179],v2 offset:5408
v_mfma_f32_16x16x32_bf16 a[32:35],a[132:135],v[140:143],a[32:35]
buffer_store_dwordx4 v[236:239],v6,s[36:39],s87 offen offset:1024 sc1
v_mfma_f32_16x16x32_bf16 a[32:35],a[136:139],v[152:155],a[32:35]
ds_read_b64_tr_b16 a[184:185],v2 offset:20768
ds_read_b64_tr_b16 a[186:187],v2 offset:25888
v_mfma_f32_16x16x32_bf16 a[32:35],a[140:143],v[156:159],a[32:35]
v_add_u32 v2,40960,v2
v_mfma_f32_16x16x32_bf16 a[36:39],a[140:143],v[188:191],a[36:39]
ds_read_b64_tr_b16 a[112:113],v2 offset:160
ds_read_b64_tr_b16 a[114:115],v2 offset:5280
v_mfma_f32_16x16x32_bf16 a[36:39],a[136:139],v[184:187],a[36:39]
buffer_load_dwordx4 v[212:215],v6,s[28:31],s79 offen offset:2048 sc1
v_mfma_f32_16x16x32_bf16 a[36:39],a[132:135],v[172:175],a[36:39]
ds_read_b64_tr_b16 a[116:117],v2 offset:10400
ds_read_b64_tr_b16 a[118:119],v2 offset:15520
v_mfma_f32_16x16x32_bf16 a[36:39],a[128:131],v[168:171],a[36:39]
ds_read_b64_tr_b16 a[120:121],v2 offset:20640
v_mfma_f32_16x16x32_bf16 a[40:43],a[128:131],v[200:203],a[40:43]
)A"
R"A(ds_read_b64_tr_b16 a[122:123],v2 offset:25760
v_mfma_f32_16x16x32_bf16 a[40:43],a[132:135],v[204:207],a[40:43]
buffer_load_dwordx4 v7,s[72:75],s83 offen lds
v_mfma_f32_16x16x32_bf16 a[40:43],a[136:139],v[216:219],a[40:43]
ds_read_b64_tr_b16 a[124:125],v2 offset:30880
ds_read_b64_tr_b16 a[126:127],v2 offset:36000
v_mfma_f32_16x16x32_bf16 a[40:43],a[140:143],v[220:223],a[40:43]
v_mfma_f32_16x16x32_bf16 a[44:47],a[140:143],v[252:255],a[44:47]
ds_read_b64_tr_b16 a[140:141],v1 offset:30720
ds_read_b64_tr_b16 a[142:143],v1 offset:35840
v_mfma_f32_16x16x32_bf16 a[44:47],a[136:139],v[248:251],a[44:47]
buffer_load_dwordx4 v[244:247],v6,s[28:31],s79 offen offset:3072 sc1
v_mfma_f32_16x16x32_bf16 a[44:47],a[132:135],v[236:239],a[44:47]
ds_read_b64_tr_b16 a[132:133],v1 offset:10240
ds_read_b64_tr_b16 a[134:135],v1 offset:15360
v_mfma_f32_16x16x32_bf16 a[44:47],a[128:131],v[232:235],a[44:47]
ds_read_b64_tr_b16 a[128:129],v1
v_mfma_f32_16x16x32_bf16 a[48:51],a[144:147],v[136:139],a[48:51]
ds_read_b64_tr_b16 a[130:131],v1 offset:5120
v_mfma_f32_16x16x32_bf16 a[48:51],a[148:151],v[140:143],a[48:51]
buffer_store_dwordx4 v[248:251],v6,s[36:39],s87 offen offset:2048 sc1
v_mfma_f32_16x16x32_bf16 a[48:51],a[152:155],v[152:155],a[48:51]
ds_read_b64_tr_b16 a[136:137],v1 offset:20480
ds_read_b64_tr_b16 a[138:139],v1 offset:25600
v_mfma_f32_16x16x32_bf16 a[48:51],a[156:159],v[156:159],a[48:51]
s_cmp_ge_u32 1,s71
s_cselect_b32 s76,4,2
s_lshl_b32 s76,s76,16
v_mfma_f32_16x16x32_bf16 a[52:55],a[156:159],v[188:191],a[52:55]
s_add_u32 s77,s76,4096
s_add_u32 s78,s76,8192
s_add_u32 s79,s76,12288
v_mfma_f32_16x16x32_bf16 a[52:55],a[152:155],v[184:187],a[52:55]
s_max_i32 s76,s76,s18
buffer_load_dwordx4 v8,s[72:75],s83 offen offset:1024 lds
v_mfma_f32_16x16x32_bf16 a[52:55],a[148:151],v[172:175],a[52:55]
s_max_i32 s77,s77,s19
s_max_i32 s78,s78,s100
s_max_i32 s79,s79,s101
v_mfma_f32_16x16x32_bf16 a[52:55],a[144:147],v[168:171],a[52:55]
v_mfma_f32_16x16x32_bf16 a[56:59],a[144:147],v[200:203],a[56:59]
v_mfma_f32_16x16x32_bf16 a[56:59],a[148:151],v[204:207],a[56:59]
buffer_store_dwordx4 v[252:255],v6,s[36:39],s87 offen offset:3072 sc1
v_mfma_f32_16x16x32_bf16 a[56:59],a[152:155],v[216:219],a[56:59]
v_mfma_f32_16x16x32_bf16 a[56:59],a[156:159],v[220:223],a[56:59]
v_mfma_f32_16x16x32_bf16 a[60:63],a[156:159],v[252:255],a[60:63]
ds_read_b64_tr_b16 a[156:157],v2 offset:30752
ds_read_b64_tr_b16 a[158:159],v2 offset:35872
v_mfma_f32_16x16x32_bf16 a[60:63],a[152:155],v[248:251],a[60:63]
ds_read_b64_tr_b16 a[152:153],v2 offset:20512
v_mfma_f32_16x16x32_bf16 a[60:63],a[148:151],v[236:239],a[60:63]
buffer_load_dwordx4 v9,s[72:75],s83 offen offset:2048 lds
v_mfma_f32_16x16x32_bf16 a[60:63],a[144:147],v[232:235],a[60:63]
ds_read_b64_tr_b16 a[144:145],v2 offset:32
ds_read_b64_tr_b16 a[146:147],v2 offset:5152
v_mfma_f32_16x16x32_bf16 a[64:67],a[160:163],v[136:139],a[64:67]
ds_read_b64_tr_b16 a[148:149],v2 offset:10272
v_mfma_f32_16x16x32_bf16 a[64:67],a[164:167],v[140:143],a[64:67]
ds_read_b64_tr_b16 a[150:151],v2 offset:15392
v_mfma_f32_16x16x32_bf16 a[64:67],a[168:171],v[152:155],a[64:67]
ds_read_b64_tr_b16 a[154:155],v2 offset:25632
v_mfma_f32_16x16x32_bf16 a[64:67],a[172:175],v[156:159],a[64:67]
v_mfma_f32_16x16x32_bf16 a[68:71],a[172:175],v[188:191],a[68:71]
v_mfma_f32_16x16x32_bf16 a[68:71],a[168:171],v[184:187],a[68:71]
v_mfma_f32_16x16x32_bf16 a[68:71],a[164:167],v[172:175],a[68:71]
v_mfma_f32_16x16x32_bf16 a[68:71],a[160:163],v[168:171],a[68:71]
v_mfma_f32_16x16x32_bf16 a[72:75],a[160:163],v[200:203],a[72:75]
v_mfma_f32_16x16x32_bf16 a[72:75],a[164:167],v[204:207],a[72:75]
v_mfma_f32_16x16x32_bf16 a[72:75],a[168:171],v[216:219],a[72:75]
v_mfma_f32_16x16x32_bf16 a[72:75],a[172:175],v[220:223],a[72:75]
buffer_load_dwordx4 v[64:67],v6,s[28:31],s76 offen sc1
v_mfma_f32_16x16x32_bf16 a[76:79],a[172:175],v[252:255],a[76:79]
ds_read_b64_tr_b16 a[172:173],v1 offset:30784
ds_read_b64_tr_b16 a[174:175],v1 offset:35904
v_mfma_f32_16x16x32_bf16 a[76:79],a[168:171],v[248:251],a[76:79]
ds_read_b64_tr_b16 a[168:169],v1 offset:20544
v_mfma_f32_16x16x32_bf16 a[76:79],a[164:167],v[236:239],a[76:79]
ds_read_b64_tr_b16 a[164:165],v1 offset:10304
v_mfma_f32_16x16x32_bf16 a[76:79],a[160:163],v[232:235],a[76:79]
s_waitcnt lgkmcnt(4)
buffer_load_dwordx4 v10,s[72:75],s83 offen offset:3072 lds
v_mfma_f32_16x16x32_bf16 a[80:83],a[176:179],v[136:139],a[80:83]
ds_read_b64_tr_b16 a[160:161],v1 offset:64
ds_read_b64_tr_b16 a[162:163],v1 offset:5184
v_mfma_f32_16x16x32_bf16 a[80:83],a[180:183],v[140:143],a[80:83]
ds_read_b64_tr_b16 a[166:167],v1 offset:15424
v_mfma_f32_16x16x32_bf16 a[80:83],a[184:187],v[152:155],a[80:83]
ds_read_b64_tr_b16 a[170:171],v1 offset:25664
v_mfma_f32_16x16x32_bf16 a[80:83],a[188:191],v[156:159],a[80:83]
buffer_load_dwordx4 v[68:71],v6,s[28:31],s76 offen offset:1024 sc1
v_mfma_f32_16x16x32_bf16 a[84:87],a[188:191],v[188:191],a[84:87]
v_mfma_f32_16x16x32_bf16 a[84:87],a[184:187],v[184:187],a[84:87]
v_mfma_f32_16x16x32_bf16 a[84:87],a[180:183],v[172:175],a[84:87]
v_mfma_f32_16x16x32_bf16 a[84:87],a[176:179],v[168:171],a[84:87]
v_mfma_f32_16x16x32_bf16 a[88:91],a[176:179],v[200:203],a[88:91]
v_mfma_f32_16x16x32_bf16 a[88:91],a[180:183],v[204:207],a[88:91]
s_add_u32 m0,s82,91136
v_mfma_f32_16x16x32_bf16 a[88:91],a[184:187],v[216:219],a[88:91]
v_mfma_f32_16x16x32_bf16 a[88:91],a[188:191],v[220:223],a[88:91]
buffer_load_dwordx4 v[72:75],v6,s[28:31],s76 offen offset:2048 sc1
v_mfma_f32_16x16x32_bf16 a[92:95],a[188:191],v[252:255],a[92:95]
ds_read_b64_tr_b16 a[188:189],v2 offset:30816
ds_read_b64_tr_b16 a[190:191],v2 offset:35936
v_mfma_f32_16x16x32_bf16 a[92:95],a[184:187],v[248:251],a[92:95]
ds_read_b64_tr_b16 a[184:185],v2 offset:20576
v_mfma_f32_16x16x32_bf16 a[92:95],a[180:183],v[236:239],a[92:95]
s_cmp_ge_u32 0,s71
s_cselect_b32 s80,3,1
s_mul_i32 s80,s80,163840
v_mfma_f32_16x16x32_bf16 a[92:95],a[176:179],v[232:235],a[92:95]
s_add_u32 s80,s80,s81
buffer_load_dwordx4 v11,s[72:75],s83 offen lds
s_waitcnt vmcnt(14)
v_mfma_f32_16x16x32_bf16 a[192:195],a[128:131],v[128:131],a[192:195]
s_add_u32 s83,s80,20480
ds_read_b64_tr_b16 a[176:177],v2 offset:96
ds_read_b64_tr_b16 a[178:179],v2 offset:5216
v_mfma_f32_16x16x32_bf16 a[192:195],a[132:135],v[160:163],a[192:195]
ds_read_b64_tr_b16 a[180:181],v2 offset:10336
v_mfma_f32_16x16x32_bf16 a[192:195],a[136:139],v[192:195],a[192:195]
ds_read_b64_tr_b16 a[182:183],v2 offset:15456
v_mfma_f32_16x16x32_bf16 a[192:195],a[140:143],v[224:227],a[192:195]
buffer_load_dwordx4 v[76:79],v6,s[28:31],s76 offen offset:3072 sc1
v_mfma_f32_16x16x32_bf16 a[196:199],a[140:143],v[228:231],a[196:199]
ds_read_b64_tr_b16 a[186:187],v2 offset:25696
v_mfma_f32_16x16x32_bf16 a[196:199],a[136:139],v[196:199],a[196:199]
v_mfma_f32_16x16x32_bf16 a[196:199],a[132:135],v[164:167],a[196:199]
v_mfma_f32_16x16x32_bf16 a[196:199],a[128:131],v[132:135],a[196:199]
v_mfma_f32_16x16x32_bf16 a[200:203],a[128:131],v[144:147],a[200:203]
v_mfma_f32_16x16x32_bf16 a[200:203],a[132:135],v[176:179],a[200:203]
v_mfma_f32_16x16x32_bf16 a[200:203],a[136:139],v[208:211],a[200:203]
v_mfma_f32_16x16x32_bf16 a[200:203],a[140:143],v[240:243],a[200:203]
s_lshl_b32 s95,s42,2
buffer_load_dwordx4 v[80:83],v6,s[28:31],s77 offen sc1
s_waitcnt vmcnt(11)
v_mfma_f32_16x16x32_bf16 a[204:207],a[140:143],v[244:247],a[204:207]
s_add_u32 s95,s95,s40
s_lshl_b32 s95,s95,4
v_mov_b32 v18,s68
v_mfma_f32_16x16x32_bf16 a[204:207],a[136:139],v[212:215],a[204:207]
ds_read_b64_tr_b16 a[136:137],v1 offset:20672
v_mov_b32 v19,s69
v_mfma_f32_16x16x32_bf16 a[204:207],a[132:135],v[180:183],a[204:207]
ds_read_b64_tr_b16 a[132:133],v1 offset:10432
ds_read_b64_tr_b16 a[134:135],v1 offset:15552
v_mfma_f32_16x16x32_bf16 a[204:207],a[128:131],v[148:151],a[204:207]
s_barrier
ds_read_b64_tr_b16 a[128:129],v1 offset:192
ds_read_b64_tr_b16 a[130:131],v1 offset:5312
v_mfma_f32_16x16x32_bf16 a[208:211],a[144:147],v[128:131],a[208:211]
ds_read_b64_tr_b16 a[138:139],v1 offset:25792
v_mfma_f32_16x16x32_bf16 a[208:211],a[148:151],v[160:163],a[208:211]
ds_read_b64_tr_b16 a[140:141],v1 offset:30912
v_mfma_f32_16x16x32_bf16 a[208:211],a[152:155],v[192:195],a[208:211]
s_add_u32 m0,s82,0
s_waitcnt vmcnt(8)
s_mov_b64 exec,1
buffer_store_dwordx2 v[18:19],v6,s[64:67],s95 offen offset:1024 sc1
s_mov_b64 exec,-1
v_mfma_f32_16x16x32_bf16 a[208:211],a[156:159],v[224:227],a[208:211]
ds_read_b64_tr_b16 a[142:143],v1 offset:36032
v_mfma_f32_16x16x32_bf16 a[212:215],a[156:159],v[228:231],a[212:215]
v_mfma_f32_16x16x32_bf16 a[212:215],a[152:155],v[196:199],a[212:215]
v_mfma_f32_16x16x32_bf16 a[212:215],a[148:151],v[164:167],a[212:215]
buffer_load_dwordx4 v[84:87],v6,s[28:31],s77 offen offset:1024 sc1
v_mfma_f32_16x16x32_bf16 a[212:215],a[144:147],v[132:135],a[212:215]
v_mfma_f32_16x16x32_bf16 a[216:219],a[144:147],v[144:147],a[216:219]
v_mfma_f32_16x16x32_bf16 a[216:219],a[148:151],v[176:179],a[216:219]
v_mfma_f32_16x16x32_bf16 a[216:219],a[152:155],v[208:211],a[216:219]
buffer_load_dwordx4 v7,s[72:75],s80 offen lds
v_mfma_f32_16x16x32_bf16 a[216:219],a[156:159],v[240:243],a[216:219]
v_mfma_f32_16x16x32_bf16 a[220:223],a[156:159],v[244:247],a[220:223]
ds_read_b64_tr_b16 a[156:157],v2 offset:30944
ds_read_b64_tr_b16 a[158:159],v2 offset:36064
v_mfma_f32_16x16x32_bf16 a[220:223],a[152:155],v[212:215],a[220:223]
ds_read_b64_tr_b16 a[152:153],v2 offset:20704
v_mfma_f32_16x16x32_bf16 a[220:223],a[148:151],v[180:183],a[220:223]
buffer_load_dwordx4 v[88:91],v6,s[28:31],s77 offen offset:2048 sc1
v_mfma_f32_16x16x32_bf16 a[220:223],a[144:147],v[148:151],a[220:223]
s_waitcnt lgkmcnt(3)
ds_read_b64_tr_b16 a[144:145],v2 offset:224
ds_read_b64_tr_b16 a[146:147],v2 offset:5344
v_mfma_f32_16x16x32_bf16 a[224:227],a[160:163],v[128:131],a[224:227]
ds_read_b64_tr_b16 a[148:149],v2 offset:10464
v_mfma_f32_16x16x32_bf16 a[224:227],a[164:167],v[160:163],a[224:227]
ds_read_b64_tr_b16 a[150:151],v2 offset:15584
v_mfma_f32_16x16x32_bf16 a[224:227],a[168:171],v[192:195],a[224:227]
buffer_load_dwordx4 v[92:95],v6,s[28:31],s77 offen offset:3072 sc1
v_mfma_f32_16x16x32_bf16 a[224:227],a[172:175],v[224:227],a[224:227]
ds_read_b64_tr_b16 a[154:155],v2 offset:25824
v_mfma_f32_16x16x32_bf16 a[228:231],a[172:175],v[228:231],a[228:231]
v_mfma_f32_16x16x32_bf16 a[228:231],a[168:171],v[196:199],a[228:231]
v_mfma_f32_16x16x32_bf16 a[228:231],a[164:167],v[164:167],a[228:231]
buffer_load_dwordx4 v8,s[72:75],s80 offen offset:1024 lds
v_mfma_f32_16x16x32_bf16 a[228:231],a[160:163],v[132:135],a[228:231]
v_mfma_f32_16x16x32_bf16 a[232:235],a[160:163],v[144:147],a[232:235]
v_mfma_f32_16x16x32_bf16 a[232:235],a[164:167],v[176:179],a[232:235]
v_mfma_f32_16x16x32_bf16 a[232:235],a[168:171],v[208:211],a[232:235]
buffer_load_dwordx4 v[96:99],v6,s[28:31],s78 offen sc1
v_mfma_f32_16x16x32_bf16 a[232:235],a[172:175],v[240:243],a[232:235]
v_mfma_f32_16x16x32_bf16 a[236:239],a[172:175],v[244:247],a[236:239]
ds_read_b64_tr_b16 a[172:173],v1 offset:30976
ds_read_b64_tr_b16 a[174:175],v1 offset:36096
v_mfma_f32_16x16x32_bf16 a[236:239],a[168:171],v[212:215],a[236:239]
ds_read_b64_tr_b16 a[168:169],v1 offset:20736
v_mfma_f32_16x16x32_bf16 a[236:239],a[164:167],v[180:183],a[236:239]
ds_read_b64_tr_b16 a[164:165],v1 offset:10496
v_mfma_f32_16x16x32_bf16 a[236:239],a[160:163],v[148:151],a[236:239]
ds_read_b64_tr_b16 a[160:161],v1 offset:256
v_mfma_f32_16x16x32_bf16 a[240:243],a[176:179],v[128:131],a[240:243]
ds_read_b64_tr_b16 a[162:163],v1 offset:5376
ds_read_b64_tr_b16 a[166:167],v1 offset:15616
v_mfma_f32_16x16x32_bf16 a[240:243],a[180:183],v[160:163],a[240:243]
ds_read_b64_tr_b16 a[170:171],v1 offset:25856
v_add_u32 v1,81920,v1
v_mfma_f32_16x16x32_bf16 a[240:243],a[184:187],v[192:195],a[240:243]
buffer_load_dwordx4 v[100:103],v6,s[28:31],s78 offen offset:1024 sc1
v_mfma_f32_16x16x32_bf16 a[240:243],a[188:191],v[224:227],a[240:243]
v_mfma_f32_16x16x32_bf16 a[244:247],a[188:191],v[228:231],a[244:247]
v_mfma_f32_16x16x32_bf16 a[244:247],a[184:187],v[196:199],a[244:247]
v_mfma_f32_16x16x32_bf16 a[244:247],a[180:183],v[164:167],a[244:247]
buffer_load_dwordx4 v9,s[72:75],s80 offen offset:2048 lds
)A"
R"A(v_mfma_f32_16x16x32_bf16 a[244:247],a[176:179],v[132:135],a[244:247]
v_mfma_f32_16x16x32_bf16 a[248:251],a[176:179],v[144:147],a[248:251]
v_mfma_f32_16x16x32_bf16 a[248:251],a[180:183],v[176:179],a[248:251]
v_mfma_f32_16x16x32_bf16 a[248:251],a[184:187],v[208:211],a[248:251]
buffer_load_dwordx4 v[104:107],v6,s[28:31],s78 offen offset:2048 sc1
v_mfma_f32_16x16x32_bf16 a[248:251],a[188:191],v[240:243],a[248:251]
v_mfma_f32_16x16x32_bf16 a[252:255],a[188:191],v[244:247],a[252:255]
ds_read_b64_tr_b16 a[188:189],v2 offset:31008
ds_read_b64_tr_b16 a[190:191],v2 offset:36128
v_mfma_f32_16x16x32_bf16 a[252:255],a[184:187],v[212:215],a[252:255]
ds_read_b64_tr_b16 a[184:185],v2 offset:20768
v_mfma_f32_16x16x32_bf16 a[252:255],a[180:183],v[180:183],a[252:255]
ds_read_b64_tr_b16 a[180:181],v2 offset:10528
v_mfma_f32_16x16x32_bf16 a[252:255],a[176:179],v[148:151],a[252:255]
ds_read_b64_tr_b16 a[176:177],v2 offset:288
v_mfma_f32_16x16x32_bf16 a[0:3],a[96:99],v[128:131],a[0:3]
ds_read_b64_tr_b16 a[178:179],v2 offset:5408
ds_read_b64_tr_b16 a[182:183],v2 offset:15648
v_mfma_f32_16x16x32_bf16 a[0:3],a[100:103],v[160:163],a[0:3]
ds_read_b64_tr_b16 a[186:187],v2 offset:25888
v_add_u32 v2,81920,v2
v_mfma_f32_16x16x32_bf16 a[0:3],a[104:107],v[192:195],a[0:3]
buffer_load_dwordx4 v[108:111],v6,s[28:31],s78 offen offset:3072 sc1
v_mfma_f32_16x16x32_bf16 a[0:3],a[108:111],v[224:227],a[0:3]
v_mfma_f32_16x16x32_bf16 a[4:7],a[108:111],v[228:231],a[4:7]
v_mfma_f32_16x16x32_bf16 a[4:7],a[104:107],v[196:199],a[4:7]
v_mfma_f32_16x16x32_bf16 a[4:7],a[100:103],v[164:167],a[4:7]
buffer_load_dwordx4 v10,s[72:75],s80 offen offset:3072 lds
v_mfma_f32_16x16x32_bf16 a[4:7],a[96:99],v[132:135],a[4:7]
v_mfma_f32_16x16x32_bf16 a[8:11],a[96:99],v[144:147],a[8:11]
v_mfma_f32_16x16x32_bf16 a[8:11],a[100:103],v[176:179],a[8:11]
v_mfma_f32_16x16x32_bf16 a[8:11],a[104:107],v[208:211],a[8:11]
buffer_load_dwordx4 v[112:115],v6,s[28:31],s79 offen sc1
v_mfma_f32_16x16x32_bf16 a[8:11],a[108:111],v[240:243],a[8:11]
v_mfma_f32_16x16x32_bf16 a[12:15],a[108:111],v[244:247],a[12:15]
ds_read_b64_tr_b16 a[108:109],v1 offset:30720
ds_read_b64_tr_b16 a[110:111],v1 offset:35840
v_mfma_f32_16x16x32_bf16 a[12:15],a[104:107],v[212:215],a[12:15]
ds_read_b64_tr_b16 a[104:105],v1 offset:20480
v_mfma_f32_16x16x32_bf16 a[12:15],a[100:103],v[180:183],a[12:15]
s_add_u32 m0,s82,4096
ds_read_b64_tr_b16 a[100:101],v1 offset:10240
v_mfma_f32_16x16x32_bf16 a[12:15],a[96:99],v[148:151],a[12:15]
ds_read_b64_tr_b16 a[96:97],v1
v_mfma_f32_16x16x32_bf16 a[16:19],a[112:115],v[128:131],a[16:19]
ds_read_b64_tr_b16 a[98:99],v1 offset:5120
ds_read_b64_tr_b16 a[102:103],v1 offset:15360
v_mfma_f32_16x16x32_bf16 a[16:19],a[116:119],v[160:163],a[16:19]
buffer_load_dwordx4 v11,s[72:75],s80 offen lds
v_mfma_f32_16x16x32_bf16 a[16:19],a[120:123],v[192:195],a[16:19]
ds_read_b64_tr_b16 a[106:107],v1 offset:25600
v_mfma_f32_16x16x32_bf16 a[16:19],a[124:127],v[224:227],a[16:19]
v_mfma_f32_16x16x32_bf16 a[20:23],a[124:127],v[228:231],a[20:23]
v_mfma_f32_16x16x32_bf16 a[20:23],a[120:123],v[196:199],a[20:23]
buffer_load_dwordx4 v[116:119],v6,s[28:31],s79 offen offset:1024 sc1
v_mfma_f32_16x16x32_bf16 a[20:23],a[116:119],v[164:167],a[20:23]
v_mfma_f32_16x16x32_bf16 a[20:23],a[112:115],v[132:135],a[20:23]
v_mfma_f32_16x16x32_bf16 a[24:27],a[112:115],v[144:147],a[24:27]
v_mfma_f32_16x16x32_bf16 a[24:27],a[116:119],v[176:179],a[24:27]
v_mfma_f32_16x16x32_bf16 a[24:27],a[120:123],v[208:211],a[24:27]
buffer_load_dwordx4 v[120:123],v6,s[28:31],s79 offen offset:2048 sc1
v_mfma_f32_16x16x32_bf16 a[24:27],a[124:127],v[240:243],a[24:27]
v_mfma_f32_16x16x32_bf16 a[28:31],a[124:127],v[244:247],a[28:31]
ds_read_b64_tr_b16 a[124:125],v2 offset:30752
ds_read_b64_tr_b16 a[126:127],v2 offset:35872
v_mfma_f32_16x16x32_bf16 a[28:31],a[120:123],v[212:215],a[28:31]
s_add_u32 m0,s82,5120
ds_read_b64_tr_b16 a[120:121],v2 offset:20512
v_mfma_f32_16x16x32_bf16 a[28:31],a[116:119],v[180:183],a[28:31]
ds_read_b64_tr_b16 a[116:117],v2 offset:10272
v_mfma_f32_16x16x32_bf16 a[28:31],a[112:115],v[148:151],a[28:31]
s_cmp_ge_u32 2,s71
s_cselect_b32 s76,6,4
s_lshl_b32 s76,s76,16
v_mfma_f32_16x16x32_bf16 a[32:35],a[128:131],v[128:131],a[32:35]
s_add_u32 s77,s76,4096
buffer_load_dwordx4 v7,s[72:75],s83 offen lds
v_mfma_f32_16x16x32_bf16 a[32:35],a[132:135],v[160:163],a[32:35]
s_add_u32 s78,s76,8192
s_max_i32 s77,s77,s19
ds_read_b64_tr_b16 a[112:113],v2 offset:32
v_mfma_f32_16x16x32_bf16 a[32:35],a[136:139],v[192:195],a[32:35]
s_max_i32 s78,s78,s100
ds_read_b64_tr_b16 a[114:115],v2 offset:5152
ds_read_b64_tr_b16 a[118:119],v2 offset:15392
v_mfma_f32_16x16x32_bf16 a[32:35],a[140:143],v[224:227],a[32:35]
ds_read_b64_tr_b16 a[122:123],v2 offset:25632
v_mfma_f32_16x16x32_bf16 a[36:39],a[140:143],v[228:231],a[36:39]
buffer_load_dwordx4 v[124:127],v6,s[28:31],s79 offen offset:3072 sc1
v_mfma_f32_16x16x32_bf16 a[36:39],a[136:139],v[196:199],a[36:39]
s_add_u32 s79,s76,12288
s_max_i32 s76,s76,s18
s_max_i32 s79,s79,s101
v_mfma_f32_16x16x32_bf16 a[36:39],a[132:135],v[164:167],a[36:39]
v_mfma_f32_16x16x32_bf16 a[36:39],a[128:131],v[132:135],a[36:39]
v_mfma_f32_16x16x32_bf16 a[40:43],a[128:131],v[144:147],a[40:43]
v_mfma_f32_16x16x32_bf16 a[40:43],a[132:135],v[176:179],a[40:43]
v_mfma_f32_16x16x32_bf16 a[40:43],a[136:139],v[208:211],a[40:43]
v_mfma_f32_16x16x32_bf16 a[40:43],a[140:143],v[240:243],a[40:43]
v_mfma_f32_16x16x32_bf16 a[44:47],a[140:143],v[244:247],a[44:47]
ds_read_b64_tr_b16 a[140:141],v1 offset:30784
ds_read_b64_tr_b16 a[142:143],v1 offset:35904
v_mfma_f32_16x16x32_bf16 a[44:47],a[136:139],v[212:215],a[44:47]
ds_read_b64_tr_b16 a[136:137],v1 offset:20544
v_mfma_f32_16x16x32_bf16 a[44:47],a[132:135],v[180:183],a[44:47]
ds_read_b64_tr_b16 a[132:133],v1 offset:10304
v_mfma_f32_16x16x32_bf16 a[44:47],a[128:131],v[148:151],a[44:47]
s_waitcnt lgkmcnt(4)
buffer_load_dwordx4 v8,s[72:75],s83 offen offset:1024 lds
v_mfma_f32_16x16x32_bf16 a[48:51],a[144:147],v[128:131],a[48:51]
ds_read_b64_tr_b16 a[128:129],v1 offset:64
ds_read_b64_tr_b16 a[130:131],v1 offset:5184
v_mfma_f32_16x16x32_bf16 a[48:51],a[148:151],v[160:163],a[48:51]
ds_read_b64_tr_b16 a[134:135],v1 offset:15424
v_mfma_f32_16x16x32_bf16 a[48:51],a[152:155],v[192:195],a[48:51]
ds_read_b64_tr_b16 a[138:139],v1 offset:25664
v_mfma_f32_16x16x32_bf16 a[48:51],a[156:159],v[224:227],a[48:51]
s_lshl_b32 s94,s70,4
v_bfe_u32 v24,v6,4,2
v_min_u32 v24,2,v24
v_mfma_f32_16x16x32_bf16 a[52:55],a[156:159],v[228:231],a[52:55]
s_add_u32 s94,s94,s40
v_add_u32 v24,s71,v24
v_add_u32 v24,1,v24
v_mfma_f32_16x16x32_bf16 a[52:55],a[152:155],v[196:199],a[52:55]
s_lshl_b32 s94,s94,4
v_and_b32 v24,3,v24
v_lshlrev_b32 v24,6,v24
v_mfma_f32_16x16x32_bf16 a[52:55],a[148:151],v[164:167],a[52:55]
v_mfma_f32_16x16x32_bf16 a[52:55],a[144:147],v[132:135],a[52:55]
buffer_load_dwordx4 v[136:139],v6,s[28:31],s76 offen sc1
v_mfma_f32_16x16x32_bf16 a[56:59],a[144:147],v[144:147],a[56:59]
v_mfma_f32_16x16x32_bf16 a[56:59],a[148:151],v[176:179],a[56:59]
v_mfma_f32_16x16x32_bf16 a[56:59],a[152:155],v[208:211],a[56:59]
v_mfma_f32_16x16x32_bf16 a[56:59],a[156:159],v[240:243],a[56:59]
v_mfma_f32_16x16x32_bf16 a[60:63],a[156:159],v[244:247],a[60:63]
ds_read_b64_tr_b16 a[156:157],v2 offset:30816
ds_read_b64_tr_b16 a[158:159],v2 offset:35936
v_mfma_f32_16x16x32_bf16 a[60:63],a[152:155],v[212:215],a[60:63]
buffer_load_dwordx4 v[140:143],v6,s[28:31],s76 offen offset:1024 sc1
v_mfma_f32_16x16x32_bf16 a[60:63],a[148:151],v[180:183],a[60:63]
ds_read_b64_tr_b16 a[148:149],v2 offset:10336
ds_read_b64_tr_b16 a[150:151],v2 offset:15456
v_mfma_f32_16x16x32_bf16 a[60:63],a[144:147],v[148:151],a[60:63]
ds_read_b64_tr_b16 a[144:145],v2 offset:96
v_mfma_f32_16x16x32_bf16 a[64:67],a[160:163],v[128:131],a[64:67]
ds_read_b64_tr_b16 a[146:147],v2 offset:5216
v_mfma_f32_16x16x32_bf16 a[64:67],a[164:167],v[160:163],a[64:67]
buffer_load_dwordx4 v9,s[72:75],s83 offen offset:2048 lds
v_mfma_f32_16x16x32_bf16 a[64:67],a[168:171],v[192:195],a[64:67]
ds_read_b64_tr_b16 a[152:153],v2 offset:20576
ds_read_b64_tr_b16 a[154:155],v2 offset:25696
v_mfma_f32_16x16x32_bf16 a[64:67],a[172:175],v[224:227],a[64:67]
v_mfma_f32_16x16x32_bf16 a[68:71],a[172:175],v[228:231],a[68:71]
v_mfma_f32_16x16x32_bf16 a[68:71],a[168:171],v[196:199],a[68:71]
buffer_load_dwordx4 v[152:155],v6,s[28:31],s76 offen offset:2048 sc1
v_mfma_f32_16x16x32_bf16 a[68:71],a[164:167],v[164:167],a[68:71]
v_mfma_f32_16x16x32_bf16 a[68:71],a[160:163],v[132:135],a[68:71]
v_mfma_f32_16x16x32_bf16 a[72:75],a[160:163],v[144:147],a[72:75]
v_mfma_f32_16x16x32_bf16 a[72:75],a[164:167],v[176:179],a[72:75]
buffer_load_dwordx4 v[156:159],v6,s[28:31],s76 offen offset:3072 sc1
v_mfma_f32_16x16x32_bf16 a[72:75],a[168:171],v[208:211],a[72:75]
v_mfma_f32_16x16x32_bf16 a[72:75],a[172:175],v[240:243],a[72:75]
v_mfma_f32_16x16x32_bf16 a[76:79],a[172:175],v[244:247],a[76:79]
ds_read_b64_tr_b16 a[172:173],v1 offset:30848
ds_read_b64_tr_b16 a[174:175],v1 offset:35968
v_mfma_f32_16x16x32_bf16 a[76:79],a[168:171],v[212:215],a[76:79]
buffer_load_dwordx4 v10,s[72:75],s83 offen offset:3072 lds
v_mfma_f32_16x16x32_bf16 a[76:79],a[164:167],v[180:183],a[76:79]
ds_read_b64_tr_b16 a[164:165],v1 offset:10368
ds_read_b64_tr_b16 a[166:167],v1 offset:15488
v_mfma_f32_16x16x32_bf16 a[76:79],a[160:163],v[148:151],a[76:79]
ds_read_b64_tr_b16 a[160:161],v1 offset:128
v_mfma_f32_16x16x32_bf16 a[80:83],a[176:179],v[128:131],a[80:83]
ds_read_b64_tr_b16 a[162:163],v1 offset:5248
v_mfma_f32_16x16x32_bf16 a[80:83],a[180:183],v[160:163],a[80:83]
buffer_load_dwordx4 v[168:171],v6,s[28:31],s77 offen sc1
v_mfma_f32_16x16x32_bf16 a[80:83],a[184:187],v[192:195],a[80:83]
ds_read_b64_tr_b16 a[168:169],v1 offset:20608
ds_read_b64_tr_b16 a[170:171],v1 offset:25728
v_mfma_f32_16x16x32_bf16 a[80:83],a[188:191],v[224:227],a[80:83]
v_mfma_f32_16x16x32_bf16 a[84:87],a[188:191],v[228:231],a[84:87]
v_mfma_f32_16x16x32_bf16 a[84:87],a[184:187],v[196:199],a[84:87]
buffer_load_dwordx4 v[172:175],v6,s[28:31],s77 offen offset:1024 sc1
v_mfma_f32_16x16x32_bf16 a[84:87],a[180:183],v[164:167],a[84:87]
v_mfma_f32_16x16x32_bf16 a[84:87],a[176:179],v[132:135],a[84:87]
v_mfma_f32_16x16x32_bf16 a[88:91],a[176:179],v[144:147],a[88:91]
v_mfma_f32_16x16x32_bf16 a[88:91],a[180:183],v[176:179],a[88:91]
s_add_u32 m0,s82,9216
v_mfma_f32_16x16x32_bf16 a[88:91],a[184:187],v[208:211],a[88:91]
v_mfma_f32_16x16x32_bf16 a[88:91],a[188:191],v[240:243],a[88:91]
buffer_load_dwordx2 v[26:27],v24,s[64:67],s94 offen offset:1024 sc1
v_mfma_f32_16x16x32_bf16 a[92:95],a[188:191],v[244:247],a[92:95]
ds_read_b64_tr_b16 a[188:189],v2 offset:30880
ds_read_b64_tr_b16 a[190:191],v2 offset:36000
v_mfma_f32_16x16x32_bf16 a[92:95],a[184:187],v[212:215],a[92:95]
ds_read_b64_tr_b16 a[184:185],v2 offset:20640
v_mfma_f32_16x16x32_bf16 a[92:95],a[180:183],v[180:183],a[92:95]
ds_read_b64_tr_b16 a[180:181],v2 offset:10400
v_mfma_f32_16x16x32_bf16 a[92:95],a[176:179],v[148:151],a[92:95]
s_waitcnt vmcnt(13)
buffer_load_dwordx4 v[184:187],v6,s[28:31],s77 offen offset:2048 sc1
v_mfma_f32_16x16x32_bf16 a[192:195],a[96:99],v[64:67],a[192:195]
s_lshl_b32 s43,s41,2
ds_read_b64_tr_b16 a[176:177],v2 offset:160
ds_read_b64_tr_b16 a[178:179],v2 offset:5280
v_mfma_f32_16x16x32_bf16 a[192:195],a[100:103],v[68:71],a[192:195]
s_add_u32 s84,s70,1
s_add_u32 s85,s70,2
ds_read_b64_tr_b16 a[182:183],v2 offset:15520
v_mfma_f32_16x16x32_bf16 a[192:195],a[104:107],v[72:75],a[192:195]
s_add_u32 s86,s70,3
s_add_u32 s43,s43,s71
ds_read_b64_tr_b16 a[186:187],v2 offset:25760
v_mfma_f32_16x16x32_bf16 a[192:195],a[108:111],v[76:79],a[192:195]
s_and_b32 s84,s84,3
buffer_load_dwordx4 v11,s[72:75],s83 offen lds
v_mfma_f32_16x16x32_bf16 a[196:199],a[108:111],v[92:95],a[196:199]
s_and_b32 s85,s85,3
s_and_b32 s86,s86,3
s_mul_i32 s43,s43,245760
v_mfma_f32_16x16x32_bf16 a[196:199],a[104:107],v[88:91],a[196:199]
s_mul_i32 s84,s84,12
s_mul_i32 s85,s85,12
s_mul_i32 s86,s86,12
v_mfma_f32_16x16x32_bf16 a[196:199],a[100:103],v[84:87],a[196:199]
s_add_u32 s43,s43,134217728
s_mov_b64 s[32:33],s[12:13]
s_add_u32 s84,s84,0
v_mfma_f32_16x16x32_bf16 a[196:199],a[96:99],v[80:83],a[196:199]
)A"
R"A(s_add_u32 s85,s85,4
buffer_load_dwordx4 v[188:191],v6,s[28:31],s77 offen offset:3072 sc1
v_mfma_f32_16x16x32_bf16 a[200:203],a[96:99],v[96:99],a[200:203]
s_add_u32 s86,s86,8
s_add_u32 s84,s84,s40
s_add_u32 s85,s85,s40
v_mfma_f32_16x16x32_bf16 a[200:203],a[100:103],v[100:103],a[200:203]
s_add_u32 s86,s86,s40
s_add_u32 s32,s32,s43
s_addc_u32 s33,s33,0
v_mfma_f32_16x16x32_bf16 a[200:203],a[104:107],v[104:107],a[200:203]
s_mul_i32 s84,s84,5120
s_mul_i32 s85,s85,5120
s_mul_i32 s86,s86,5120
v_mfma_f32_16x16x32_bf16 a[200:203],a[108:111],v[108:111],a[200:203]
s_and_b32 s33,s33,0xffff
buffer_load_dwordx4 v[200:203],v6,s[28:31],s78 offen sc1
s_waitcnt vmcnt(14)
v_mfma_f32_16x16x32_bf16 a[204:207],a[108:111],v[124:127],a[204:207]
s_mov_b32 s34,245760
ds_read_b64_tr_b16 a[108:109],v1 offset:30912
ds_read_b64_tr_b16 a[110:111],v1 offset:36032
v_mfma_f32_16x16x32_bf16 a[204:207],a[104:107],v[120:123],a[204:207]
s_mov_b32 s35,0x20000
s_max_i32 s84,s84,s18
ds_read_b64_tr_b16 a[104:105],v1 offset:20672
v_mfma_f32_16x16x32_bf16 a[204:207],a[100:103],v[116:119],a[204:207]
s_max_i32 s85,s85,s19
s_max_i32 s86,s86,s100
ds_read_b64_tr_b16 a[100:101],v1 offset:10432
v_mfma_f32_16x16x32_bf16 a[204:207],a[96:99],v[112:115],a[204:207]
s_barrier
buffer_load_dwordx4 v[204:207],v6,s[28:31],s78 offen offset:1024 sc1
v_mfma_f32_16x16x32_bf16 a[208:211],a[112:115],v[64:67],a[208:211]
s_mul_i32 s44,s41,307200
ds_read_b64_tr_b16 a[96:97],v1 offset:192
ds_read_b64_tr_b16 a[98:99],v1 offset:5312
v_mfma_f32_16x16x32_bf16 a[208:211],a[116:119],v[68:71],a[208:211]
s_mov_b64 s[20:21],s[16:17]
ds_read_b64_tr_b16 a[102:103],v1 offset:15552
v_bfe_u32 v18,v6,6,2
v_mfma_f32_16x16x32_bf16 a[208:211],a[120:123],v[72:75],a[208:211]
s_lshl_b32 s43,s40,2
ds_read_b64_tr_b16 a[106:107],v1 offset:25792
v_lshrrev_b32 v19,8,v6
v_mfma_f32_16x16x32_bf16 a[208:211],a[124:127],v[76:79],a[208:211]
s_add_u32 s43,s43,s70
buffer_load_dwordx4 v[216:219],v6,s[28:31],s78 offen offset:2048 sc1
v_mfma_f32_16x16x32_bf16 a[212:215],a[124:127],v[92:95],a[212:215]
s_mul_i32 s43,s43,20480
v_xor_b32 v18,v18,v19
v_lshlrev_b32 v18,2,v18
v_mfma_f32_16x16x32_bf16 a[212:215],a[120:123],v[88:91],a[212:215]
s_mov_b32 s22,307200
v_bfe_u32 v19,v6,4,2
v_or_b32 v18,v18,v19
v_mfma_f32_16x16x32_bf16 a[212:215],a[116:119],v[84:87],a[212:215]
s_mov_b32 s23,0x20000
v_and_b32 v19,16,v6
v_mov_b32 v34,0x3f80
v_mfma_f32_16x16x32_bf16 a[212:215],a[112:115],v[80:83],a[212:215]
s_cmp_ge_u32 0,s71
s_cselect_b32 s76,3,1
s_lshl_b32 s76,s76,16
v_mfma_f32_16x16x32_bf16 a[216:219],a[112:115],v[96:99],a[216:219]
s_add_u32 s77,s76,4096
buffer_load_dwordx4 v[220:223],v6,s[28:31],s78 offen offset:3072 sc1
v_mfma_f32_16x16x32_bf16 a[216:219],a[116:119],v[100:103],a[216:219]
s_add_u32 s78,s76,8192
s_max_i32 s77,s77,s19
s_max_i32 s78,s78,s100
v_mfma_f32_16x16x32_bf16 a[216:219],a[120:123],v[104:107],a[216:219]
v_lshlrev_b32 v19,v19,v34
v_lshrrev_b32 v18,1,v18
v_mfma_f32_16x16x32_bf16 a[216:219],a[124:127],v[108:111],a[216:219]
v_cmp_eq_u32 vcc,0,v18
s_nop 1
v_cndmask_b32 v34,0,v19,vcc
v_mfma_f32_16x16x32_bf16 a[220:223],a[124:127],v[124:127],a[220:223]
ds_read_b64_tr_b16 a[124:125],v2 offset:30944
ds_read_b64_tr_b16 a[126:127],v2 offset:36064
v_mfma_f32_16x16x32_bf16 a[220:223],a[120:123],v[120:123],a[220:223]
ds_read_b64_tr_b16 a[120:121],v2 offset:20704
v_add_u32 v25,4096,v6
v_mfma_f32_16x16x32_bf16 a[220:223],a[116:119],v[116:119],a[220:223]
buffer_load_dwordx4 v[232:235],v6,s[28:31],s79 offen sc1
v_mfma_f32_16x16x32_bf16 a[220:223],a[112:115],v[112:115],a[220:223]
s_waitcnt lgkmcnt(3)
ds_read_b64_tr_b16 a[112:113],v2 offset:224
ds_read_b64_tr_b16 a[114:115],v2 offset:5344
v_mfma_f32_16x16x32_bf16 a[224:227],a[128:131],v[64:67],a[224:227]
ds_read_b64_tr_b16 a[116:117],v2 offset:10464
v_mov_b32 v36,0
v_mfma_f32_16x16x32_bf16 a[224:227],a[132:135],v[68:71],a[224:227]
ds_read_b64_tr_b16 a[118:119],v2 offset:15584
v_mov_b32 v37,0
v_mfma_f32_16x16x32_bf16 a[224:227],a[136:139],v[72:75],a[224:227]
ds_read_b64_tr_b16 a[122:123],v2 offset:25824
v_mov_b32 v32,0
v_mfma_f32_16x16x32_bf16 a[224:227],a[140:143],v[76:79],a[224:227]
v_cmp_eq_u32 vcc,1,v18
s_nop 1
v_cndmask_b32 v35,0,v19,vcc
v_mfma_f32_16x16x32_bf16 a[228:231],a[140:143],v[92:95],a[228:231]
buffer_load_dwordx4 v[236:239],v6,s[28:31],s79 offen offset:1024 sc1
v_mfma_f32_16x16x32_bf16 a[228:231],a[136:139],v[88:91],a[228:231]
s_add_u32 s20,s20,s44
s_addc_u32 s21,s21,0
v_mov_b32 v33,0
v_mfma_f32_16x16x32_bf16 a[228:231],a[132:135],v[84:87],a[228:231]
s_mul_i32 s44,s71,320
v_bfe_u32 v40,v6,4,4
v_mul_u32_u24 v40,1280,v40
v_mfma_f32_16x16x32_bf16 a[228:231],a[128:131],v[80:83],a[228:231]
s_waitcnt vmcnt(9)
v_cmp_ne_u32 s[90:91],s68,v26
v_cmp_ne_u32 s[92:93],s69,v27
s_nop 0
s_or_b64 s[90:91],s[90:91],s[92:93]
s_cbranch_scc1 L_fr9_%=
L_fo9_%=:
v_mfma_f32_16x16x32_bf16 a[232:235],a[128:131],v[96:99],a[232:235]
s_and_b32 s21,s21,0xffff
v_lshrrev_b32 v41,8,v6
v_lshl_add_u32 v40,v41,3,v40
v_mfma_f32_16x16x32_bf16 a[232:235],a[132:135],v[100:103],a[232:235]
s_add_u32 s43,s43,s44
v_mfma_f32_16x16x32_bf16 a[232:235],a[136:139],v[104:107],a[232:235]
buffer_load_dwordx4 v[248:251],v6,s[28:31],s79 offen offset:2048 sc1
v_mfma_f32_16x16x32_bf16 a[232:235],a[140:143],v[108:111],a[232:235]
v_mfma_f32_16x16x32_bf16 a[236:239],a[140:143],v[124:127],a[236:239]
ds_read_b64_tr_b16 a[140:141],v1 offset:30976
ds_read_b64_tr_b16 a[142:143],v1 offset:36096
v_mfma_f32_16x16x32_bf16 a[236:239],a[136:139],v[120:123],a[236:239]
ds_read_b64_tr_b16 a[136:137],v1 offset:20736
v_mfma_f32_16x16x32_bf16 a[236:239],a[132:135],v[116:119],a[236:239]
ds_read_b64_tr_b16 a[132:133],v1 offset:10496
v_mfma_f32_16x16x32_bf16 a[236:239],a[128:131],v[112:115],a[236:239]
ds_read_b64_tr_b16 a[128:129],v1 offset:256
v_mfma_f32_16x16x32_bf16 a[240:243],a[144:147],v[64:67],a[240:243]
buffer_load_dwordx4 v[252:255],v6,s[28:31],s79 offen offset:3072 sc1
v_mfma_f32_16x16x32_bf16 a[240:243],a[148:151],v[68:71],a[240:243]
s_add_u32 s79,s76,12288
s_max_i32 s76,s76,s18
ds_read_b64_tr_b16 a[130:131],v1 offset:5376
v_mfma_f32_16x16x32_bf16 a[240:243],a[152:155],v[72:75],a[240:243]
s_max_i32 s79,s79,s101
ds_read_b64_tr_b16 a[134:135],v1 offset:15616
ds_read_b64_tr_b16 a[138:139],v1 offset:25856
v_mfma_f32_16x16x32_bf16 a[240:243],a[156:159],v[76:79],a[240:243]
v_subrev_u32 v1,40960,v1
v_mfma_f32_16x16x32_bf16 a[244:247],a[156:159],v[92:95],a[244:247]
buffer_load_dwordx4 v[128:131],v6,s[28:31],s76 offen sc1
v_mfma_f32_16x16x32_bf16 a[244:247],a[152:155],v[88:91],a[244:247]
v_mfma_f32_16x16x32_bf16 a[244:247],a[148:151],v[84:87],a[244:247]
v_mfma_f32_16x16x32_bf16 a[244:247],a[144:147],v[80:83],a[244:247]
v_mfma_f32_16x16x32_bf16 a[248:251],a[144:147],v[96:99],a[248:251]
buffer_load_dwordx4 v[160:163],v6,s[28:31],s76 offen offset:1024 sc1
v_mfma_f32_16x16x32_bf16 a[248:251],a[148:151],v[100:103],a[248:251]
v_mfma_f32_16x16x32_bf16 a[248:251],a[152:155],v[104:107],a[248:251]
v_mfma_f32_16x16x32_bf16 a[248:251],a[156:159],v[108:111],a[248:251]
v_mfma_f32_16x16x32_bf16 a[252:255],a[156:159],v[124:127],a[252:255]
buffer_load_dwordx4 v[192:195],v6,s[28:31],s76 offen offset:2048 sc1
v_mfma_f32_16x16x32_bf16 a[252:255],a[152:155],v[120:123],a[252:255]
ds_read_b64_tr_b16 a[152:153],v2 offset:20768
ds_read_b64_tr_b16 a[154:155],v2 offset:25888
v_mfma_f32_16x16x32_bf16 a[252:255],a[148:151],v[116:119],a[252:255]
ds_read_b64_tr_b16 a[148:149],v2 offset:10528
v_mfma_f32_16x16x32_bf16 a[252:255],a[144:147],v[112:115],a[252:255]
ds_read_b64_tr_b16 a[144:145],v2 offset:288
v_mfma_f32_16x16x32_bf16 a[0:3],a[160:163],v[64:67],a[0:3]
ds_read_b64_tr_b16 a[146:147],v2 offset:5408
v_mfma_f32_16x16x32_bf16 a[0:3],a[164:167],v[68:71],a[0:3]
buffer_load_dwordx4 v[224:227],v6,s[28:31],s76 offen offset:3072 sc1
v_mfma_f32_16x16x32_bf16 a[0:3],a[168:171],v[72:75],a[0:3]
ds_read_b64_tr_b16 a[150:151],v2 offset:15648
ds_read_b64_tr_b16 a[156:157],v2 offset:31008
v_mfma_f32_16x16x32_bf16 a[0:3],a[172:175],v[76:79],a[0:3]
ds_read_b64_tr_b16 a[158:159],v2 offset:36128
v_subrev_u32 v2,40960,v2
v_mfma_f32_16x16x32_bf16 a[4:7],a[172:175],v[92:95],a[4:7]
v_mfma_f32_16x16x32_bf16 a[4:7],a[168:171],v[88:91],a[4:7]
v_mfma_f32_16x16x32_bf16 a[4:7],a[164:167],v[84:87],a[4:7]
v_mfma_f32_16x16x32_bf16 a[4:7],a[160:163],v[80:83],a[4:7]
buffer_load_dwordx4 v[132:135],v6,s[28:31],s77 offen sc1
v_mfma_f32_16x16x32_bf16 a[8:11],a[160:163],v[96:99],a[8:11]
v_mfma_f32_16x16x32_bf16 a[8:11],a[164:167],v[100:103],a[8:11]
v_mfma_f32_16x16x32_bf16 a[8:11],a[168:171],v[104:107],a[8:11]
v_mfma_f32_16x16x32_bf16 a[8:11],a[172:175],v[108:111],a[8:11]
v_mfma_f32_16x16x32_bf16 a[12:15],a[172:175],v[124:127],a[12:15]
ds_read_b64_tr_b16 a[172:173],v1 offset:30720
ds_read_b64_tr_b16 a[174:175],v1 offset:35840
v_mfma_f32_16x16x32_bf16 a[12:15],a[168:171],v[120:123],a[12:15]
buffer_load_dwordx4 v[164:167],v6,s[28:31],s77 offen offset:1024 sc1
v_mfma_f32_16x16x32_bf16 a[12:15],a[164:167],v[116:119],a[12:15]
ds_read_b64_tr_b16 a[164:165],v1 offset:10240
ds_read_b64_tr_b16 a[166:167],v1 offset:15360
v_mfma_f32_16x16x32_bf16 a[12:15],a[160:163],v[112:115],a[12:15]
ds_read_b64_tr_b16 a[160:161],v1
v_mfma_f32_16x16x32_bf16 a[16:19],a[176:179],v[64:67],a[16:19]
ds_read_b64_tr_b16 a[162:163],v1 offset:5120
v_mfma_f32_16x16x32_bf16 a[16:19],a[180:183],v[68:71],a[16:19]
ds_read_b64_tr_b16 a[168:169],v1 offset:20480
v_mfma_f32_16x16x32_bf16 a[16:19],a[184:187],v[72:75],a[16:19]
ds_read_b64_tr_b16 a[170:171],v1 offset:25600
v_mfma_f32_16x16x32_bf16 a[16:19],a[188:191],v[76:79],a[16:19]
buffer_load_dwordx4 v[196:199],v6,s[28:31],s77 offen offset:2048 sc1
v_mfma_f32_16x16x32_bf16 a[20:23],a[188:191],v[92:95],a[20:23]
v_mfma_f32_16x16x32_bf16 a[20:23],a[184:187],v[88:91],a[20:23]
v_mfma_f32_16x16x32_bf16 a[20:23],a[180:183],v[84:87],a[20:23]
v_mfma_f32_16x16x32_bf16 a[20:23],a[176:179],v[80:83],a[20:23]
v_mfma_f32_16x16x32_bf16 a[24:27],a[176:179],v[96:99],a[24:27]
v_mfma_f32_16x16x32_bf16 a[24:27],a[180:183],v[100:103],a[24:27]
buffer_load_dwordx4 v[228:231],v6,s[28:31],s77 offen offset:3072 sc1
v_mfma_f32_16x16x32_bf16 a[24:27],a[184:187],v[104:107],a[24:27]
v_mfma_f32_16x16x32_bf16 a[24:27],a[188:191],v[108:111],a[24:27]
v_mfma_f32_16x16x32_bf16 a[28:31],a[188:191],v[124:127],a[28:31]
ds_read_b64_tr_b16 a[188:189],v2 offset:30752
ds_read_b64_tr_b16 a[190:191],v2 offset:35872
v_mfma_f32_16x16x32_bf16 a[28:31],a[184:187],v[120:123],a[28:31]
ds_read_b64_tr_b16 a[184:185],v2 offset:20512
v_mfma_f32_16x16x32_bf16 a[28:31],a[180:183],v[116:119],a[28:31]
ds_read_b64_tr_b16 a[180:181],v2 offset:10272
v_mfma_f32_16x16x32_bf16 a[28:31],a[176:179],v[112:115],a[28:31]
buffer_load_dwordx4 v[144:147],v6,s[28:31],s78 offen sc1
v_mfma_f32_16x16x32_bf16 a[32:35],a[96:99],v[64:67],a[32:35]
ds_read_b64_tr_b16 a[176:177],v2 offset:32
ds_read_b64_tr_b16 a[178:179],v2 offset:5152
v_mfma_f32_16x16x32_bf16 a[32:35],a[100:103],v[68:71],a[32:35]
ds_read_b64_tr_b16 a[182:183],v2 offset:15392
v_mfma_f32_16x16x32_bf16 a[32:35],a[104:107],v[72:75],a[32:35]
ds_read_b64_tr_b16 a[186:187],v2 offset:25632
v_mfma_f32_16x16x32_bf16 a[32:35],a[108:111],v[76:79],a[32:35]
v_mfma_f32_16x16x32_bf16 a[36:39],a[108:111],v[92:95],a[36:39]
v_mfma_f32_16x16x32_bf16 a[36:39],a[104:107],v[88:91],a[36:39]
buffer_load_dwordx4 v[176:179],v6,s[28:31],s78 offen offset:1024 sc1
v_mfma_f32_16x16x32_bf16 a[36:39],a[100:103],v[84:87],a[36:39]
v_mfma_f32_16x16x32_bf16 a[36:39],a[96:99],v[80:83],a[36:39]
v_mfma_f32_16x16x32_bf16 a[40:43],a[96:99],v[96:99],a[40:43]
v_mfma_f32_16x16x32_bf16 a[40:43],a[100:103],v[100:103],a[40:43]
v_mfma_f32_16x16x32_bf16 a[40:43],a[104:107],v[104:107],a[40:43]
v_mfma_f32_16x16x32_bf16 a[40:43],a[108:111],v[108:111],a[40:43]
buffer_load_dwordx4 v[208:211],v6,s[28:31],s78 offen offset:2048 sc1
v_mfma_f32_16x16x32_bf16 a[44:47],a[108:111],v[124:127],a[44:47]
ds_read_b64_tr_b16 a[108:109],v1 offset:30784
ds_read_b64_tr_b16 a[110:111],v1 offset:35904
v_mfma_f32_16x16x32_bf16 a[44:47],a[104:107],v[120:123],a[44:47]
ds_read_b64_tr_b16 a[104:105],v1 offset:20544
v_mfma_f32_16x16x32_bf16 a[44:47],a[100:103],v[116:119],a[44:47]
)A"
R"A(ds_read_b64_tr_b16 a[100:101],v1 offset:10304
v_mfma_f32_16x16x32_bf16 a[44:47],a[96:99],v[112:115],a[44:47]
s_waitcnt lgkmcnt(4)
ds_read_b64_tr_b16 a[96:97],v1 offset:64
v_mfma_f32_16x16x32_bf16 a[48:51],a[112:115],v[64:67],a[48:51]
ds_read_b64_tr_b16 a[98:99],v1 offset:5184
ds_read_b64_tr_b16 a[102:103],v1 offset:15424
v_mfma_f32_16x16x32_bf16 a[48:51],a[116:119],v[68:71],a[48:51]
buffer_load_dwordx4 v[240:243],v6,s[28:31],s78 offen offset:3072 sc1
v_mfma_f32_16x16x32_bf16 a[48:51],a[120:123],v[72:75],a[48:51]
ds_read_b64_tr_b16 a[106:107],v1 offset:25664
v_mfma_f32_16x16x32_bf16 a[48:51],a[124:127],v[76:79],a[48:51]
v_mfma_f32_16x16x32_bf16 a[52:55],a[124:127],v[92:95],a[52:55]
v_mfma_f32_16x16x32_bf16 a[52:55],a[120:123],v[88:91],a[52:55]
v_mfma_f32_16x16x32_bf16 a[52:55],a[116:119],v[84:87],a[52:55]
v_mfma_f32_16x16x32_bf16 a[52:55],a[112:115],v[80:83],a[52:55]
buffer_load_dwordx4 v[148:151],v6,s[28:31],s79 offen sc1
v_mfma_f32_16x16x32_bf16 a[56:59],a[112:115],v[96:99],a[56:59]
v_mfma_f32_16x16x32_bf16 a[56:59],a[116:119],v[100:103],a[56:59]
s_cmp_ge_u32 1,s71
s_cselect_b32 s80,5,3
s_mul_i32 s80,s80,163840
v_mfma_f32_16x16x32_bf16 a[56:59],a[120:123],v[104:107],a[56:59]
s_add_u32 s80,s80,s81
s_add_u32 s83,s80,20480
v_mfma_f32_16x16x32_bf16 a[56:59],a[124:127],v[108:111],a[56:59]
v_mfma_f32_16x16x32_bf16 a[60:63],a[124:127],v[124:127],a[60:63]
ds_read_b64_tr_b16 a[124:125],v2 offset:30816
ds_read_b64_tr_b16 a[126:127],v2 offset:35936
v_mfma_f32_16x16x32_bf16 a[60:63],a[120:123],v[120:123],a[60:63]
buffer_load_dwordx4 v[180:183],v6,s[28:31],s79 offen offset:1024 sc1
v_mfma_f32_16x16x32_bf16 a[60:63],a[116:119],v[116:119],a[60:63]
ds_read_b64_tr_b16 a[116:117],v2 offset:10336
ds_read_b64_tr_b16 a[118:119],v2 offset:15456
v_mfma_f32_16x16x32_bf16 a[60:63],a[112:115],v[112:115],a[60:63]
ds_read_b64_tr_b16 a[112:113],v2 offset:96
v_mfma_f32_16x16x32_bf16 a[64:67],a[128:131],v[64:67],a[64:67]
ds_read_b64_tr_b16 a[114:115],v2 offset:5216
v_mfma_f32_16x16x32_bf16 a[64:67],a[132:135],v[68:71],a[64:67]
ds_read_b64_tr_b16 a[120:121],v2 offset:20576
v_mfma_f32_16x16x32_bf16 a[64:67],a[136:139],v[72:75],a[64:67]
ds_read_b64_tr_b16 a[122:123],v2 offset:25696
v_mfma_f32_16x16x32_bf16 a[64:67],a[140:143],v[76:79],a[64:67]
buffer_load_dwordx4 v[212:215],v6,s[28:31],s79 offen offset:2048 sc1
v_mfma_f32_16x16x32_bf16 a[68:71],a[140:143],v[92:95],a[68:71]
v_mfma_f32_16x16x32_bf16 a[68:71],a[136:139],v[88:91],a[68:71]
v_mfma_f32_16x16x32_bf16 a[68:71],a[132:135],v[84:87],a[68:71]
v_mfma_f32_16x16x32_bf16 a[68:71],a[128:131],v[80:83],a[68:71]
v_mfma_f32_16x16x32_bf16 a[72:75],a[128:131],v[96:99],a[72:75]
v_mfma_f32_16x16x32_bf16 a[72:75],a[132:135],v[100:103],a[72:75]
buffer_load_dwordx4 v[244:247],v6,s[28:31],s79 offen offset:3072 sc1
v_mfma_f32_16x16x32_bf16 a[72:75],a[136:139],v[104:107],a[72:75]
v_mfma_f32_16x16x32_bf16 a[72:75],a[140:143],v[108:111],a[72:75]
v_mfma_f32_16x16x32_bf16 a[76:79],a[140:143],v[124:127],a[76:79]
ds_read_b64_tr_b16 a[140:141],v1 offset:30848
ds_read_b64_tr_b16 a[142:143],v1 offset:35968
v_mfma_f32_16x16x32_bf16 a[76:79],a[136:139],v[120:123],a[76:79]
ds_read_b64_tr_b16 a[136:137],v1 offset:20608
v_mfma_f32_16x16x32_bf16 a[76:79],a[132:135],v[116:119],a[76:79]
s_add_u32 m0,s82,40960
ds_read_b64_tr_b16 a[132:133],v1 offset:10368
v_mfma_f32_16x16x32_bf16 a[76:79],a[128:131],v[112:115],a[76:79]
ds_read_b64_tr_b16 a[128:129],v1 offset:128
v_mfma_f32_16x16x32_bf16 a[80:83],a[144:147],v[64:67],a[80:83]
ds_read_b64_tr_b16 a[130:131],v1 offset:5248
ds_read_b64_tr_b16 a[134:135],v1 offset:15488
v_mfma_f32_16x16x32_bf16 a[80:83],a[148:151],v[68:71],a[80:83]
buffer_load_dwordx4 v7,s[72:75],s80 offen lds
v_mfma_f32_16x16x32_bf16 a[80:83],a[152:155],v[72:75],a[80:83]
ds_read_b64_tr_b16 a[138:139],v1 offset:25728
v_mfma_f32_16x16x32_bf16 a[80:83],a[156:159],v[76:79],a[80:83]
v_mfma_f32_16x16x32_bf16 a[84:87],a[156:159],v[92:95],a[84:87]
v_mfma_f32_16x16x32_bf16 a[84:87],a[152:155],v[88:91],a[84:87]
v_mfma_f32_16x16x32_bf16 a[84:87],a[148:151],v[84:87],a[84:87]
buffer_load_dwordx4 v8,s[72:75],s80 offen offset:1024 lds
v_mfma_f32_16x16x32_bf16 a[84:87],a[144:147],v[80:83],a[84:87]
v_mfma_f32_16x16x32_bf16 a[88:91],a[144:147],v[96:99],a[88:91]
v_mfma_f32_16x16x32_bf16 a[88:91],a[148:151],v[100:103],a[88:91]
v_mfma_f32_16x16x32_bf16 a[88:91],a[152:155],v[104:107],a[88:91]
v_mfma_f32_16x16x32_bf16 a[88:91],a[156:159],v[108:111],a[88:91]
buffer_load_dwordx4 v9,s[72:75],s80 offen offset:2048 lds
v_mfma_f32_16x16x32_bf16 a[92:95],a[156:159],v[124:127],a[92:95]
ds_read_b64_tr_b16 a[156:157],v2 offset:30880
ds_read_b64_tr_b16 a[158:159],v2 offset:36000
v_mfma_f32_16x16x32_bf16 a[92:95],a[152:155],v[120:123],a[92:95]
ds_read_b64_tr_b16 a[152:153],v2 offset:20640
v_mfma_f32_16x16x32_bf16 a[92:95],a[148:151],v[116:119],a[92:95]
ds_read_b64_tr_b16 a[148:149],v2 offset:10400
v_mfma_f32_16x16x32_bf16 a[92:95],a[144:147],v[112:115],a[92:95]
ds_read_b64_tr_b16 a[144:145],v2 offset:160
v_mfma_f32_16x16x32_bf16 a[192:195],a[160:163],v[136:139],a[192:195]
buffer_load_dwordx4 v10,s[72:75],s80 offen offset:3072 lds
v_mfma_f32_16x16x32_bf16 a[192:195],a[164:167],v[140:143],a[192:195]
ds_read_b64_tr_b16 a[146:147],v2 offset:5280
ds_read_b64_tr_b16 a[150:151],v2 offset:15520
v_mfma_f32_16x16x32_bf16 a[192:195],a[168:171],v[152:155],a[192:195]
s_add_u32 m0,s82,45056
ds_read_b64_tr_b16 a[154:155],v2 offset:25760
v_mfma_f32_16x16x32_bf16 a[192:195],a[172:175],v[156:159],a[192:195]
s_waitcnt vmcnt(12)
v_mfma_f32_16x16x32_bf16 a[196:199],a[172:175],v[188:191],a[196:199]
v_mfma_f32_16x16x32_bf16 a[196:199],a[168:171],v[184:187],a[196:199]
buffer_load_dwordx4 v11,s[72:75],s80 offen lds
v_mfma_f32_16x16x32_bf16 a[196:199],a[164:167],v[172:175],a[196:199]
v_mfma_f32_16x16x32_bf16 a[196:199],a[160:163],v[168:171],a[196:199]
s_add_u32 m0,s82,46080
v_mfma_f32_16x16x32_bf16 a[200:203],a[160:163],v[200:203],a[200:203]
v_mfma_f32_16x16x32_bf16 a[200:203],a[164:167],v[204:207],a[200:203]
v_mfma_f32_16x16x32_bf16 a[200:203],a[168:171],v[216:219],a[200:203]
buffer_load_dwordx4 v7,s[72:75],s83 offen lds
v_mfma_f32_16x16x32_bf16 a[200:203],a[172:175],v[220:223],a[200:203]
v_mfma_f32_16x16x32_bf16 a[204:207],a[172:175],v[252:255],a[204:207]
ds_read_b64_tr_b16 a[172:173],v1 offset:30912
ds_read_b64_tr_b16 a[174:175],v1 offset:36032
v_mfma_f32_16x16x32_bf16 a[204:207],a[168:171],v[248:251],a[204:207]
ds_read_b64_tr_b16 a[168:169],v1 offset:20672
v_mfma_f32_16x16x32_bf16 a[204:207],a[164:167],v[236:239],a[204:207]
ds_read_b64_tr_b16 a[164:165],v1 offset:10432
v_mfma_f32_16x16x32_bf16 a[204:207],a[160:163],v[232:235],a[204:207]
s_barrier
buffer_load_dwordx4 v8,s[72:75],s83 offen offset:1024 lds
v_mfma_f32_16x16x32_bf16 a[208:211],a[176:179],v[136:139],a[208:211]
s_cmp_ge_u32 1,s71
s_cselect_b32 s76,5,3
s_lshl_b32 s76,s76,16
v_mfma_f32_16x16x32_bf16 a[208:211],a[180:183],v[140:143],a[208:211]
s_add_u32 s77,s76,4096
s_add_u32 s78,s76,8192
s_add_u32 s79,s76,12288
v_mfma_f32_16x16x32_bf16 a[208:211],a[184:187],v[152:155],a[208:211]
s_max_i32 s76,s76,s18
s_max_i32 s77,s77,s19
s_max_i32 s78,s78,s100
v_mfma_f32_16x16x32_bf16 a[208:211],a[188:191],v[156:159],a[208:211]
s_max_i32 s79,s79,s101
buffer_load_dwordx4 v9,s[72:75],s83 offen offset:2048 lds
v_mfma_f32_16x16x32_bf16 a[212:215],a[188:191],v[188:191],a[212:215]
ds_read_b64_tr_b16 a[160:161],v1 offset:192
ds_read_b64_tr_b16 a[162:163],v1 offset:5312
v_mfma_f32_16x16x32_bf16 a[212:215],a[184:187],v[184:187],a[212:215]
ds_read_b64_tr_b16 a[166:167],v1 offset:15552
v_mfma_f32_16x16x32_bf16 a[212:215],a[180:183],v[172:175],a[212:215]
ds_read_b64_tr_b16 a[170:171],v1 offset:25792
v_mfma_f32_16x16x32_bf16 a[212:215],a[176:179],v[168:171],a[212:215]
v_mfma_f32_16x16x32_bf16 a[216:219],a[176:179],v[200:203],a[216:219]
buffer_load_dwordx4 v10,s[72:75],s83 offen offset:3072 lds
v_mfma_f32_16x16x32_bf16 a[216:219],a[180:183],v[204:207],a[216:219]
v_mfma_f32_16x16x32_bf16 a[216:219],a[184:187],v[216:219],a[216:219]
s_add_u32 m0,s82,50176
v_mfma_f32_16x16x32_bf16 a[216:219],a[188:191],v[220:223],a[216:219]
v_mfma_f32_16x16x32_bf16 a[220:223],a[188:191],v[252:255],a[220:223]
ds_read_b64_tr_b16 a[188:189],v2 offset:30944
ds_read_b64_tr_b16 a[190:191],v2 offset:36064
v_mfma_f32_16x16x32_bf16 a[220:223],a[184:187],v[248:251],a[220:223]
buffer_load_dwordx4 v11,s[72:75],s83 offen lds
v_mfma_f32_16x16x32_bf16 a[220:223],a[180:183],v[236:239],a[220:223]
ds_read_b64_tr_b16 a[180:181],v2 offset:10464
ds_read_b64_tr_b16 a[182:183],v2 offset:15584
v_mfma_f32_16x16x32_bf16 a[220:223],a[176:179],v[232:235],a[220:223]
s_waitcnt lgkmcnt(4)
ds_read_b64_tr_b16 a[176:177],v2 offset:224
v_mfma_f32_16x16x32_bf16 a[224:227],a[96:99],v[136:139],a[224:227]
ds_read_b64_tr_b16 a[178:179],v2 offset:5344
v_mfma_f32_16x16x32_bf16 a[224:227],a[100:103],v[140:143],a[224:227]
ds_read_b64_tr_b16 a[184:185],v2 offset:20704
v_mfma_f32_16x16x32_bf16 a[224:227],a[104:107],v[152:155],a[224:227]
ds_read_b64_tr_b16 a[186:187],v2 offset:25824
v_mfma_f32_16x16x32_bf16 a[224:227],a[108:111],v[156:159],a[224:227]
v_mfma_f32_16x16x32_bf16 a[228:231],a[108:111],v[188:191],a[228:231]
v_mfma_f32_16x16x32_bf16 a[228:231],a[104:107],v[184:187],a[228:231]
v_mfma_f32_16x16x32_bf16 a[228:231],a[100:103],v[172:175],a[228:231]
v_mfma_f32_16x16x32_bf16 a[228:231],a[96:99],v[168:171],a[228:231]
buffer_load_dwordx4 v[64:67],v6,s[28:31],s76 offen sc1
v_mfma_f32_16x16x32_bf16 a[232:235],a[96:99],v[200:203],a[232:235]
v_mfma_f32_16x16x32_bf16 a[232:235],a[100:103],v[204:207],a[232:235]
v_mfma_f32_16x16x32_bf16 a[232:235],a[104:107],v[216:219],a[232:235]
v_mfma_f32_16x16x32_bf16 a[232:235],a[108:111],v[220:223],a[232:235]
v_mfma_f32_16x16x32_bf16 a[236:239],a[108:111],v[252:255],a[236:239]
buffer_load_dwordx4 v[68:71],v6,s[28:31],s76 offen offset:1024 sc1
v_mfma_f32_16x16x32_bf16 a[236:239],a[104:107],v[248:251],a[236:239]
ds_read_b64_tr_b16 a[104:105],v1 offset:20736
ds_read_b64_tr_b16 a[106:107],v1 offset:25856
v_mfma_f32_16x16x32_bf16 a[236:239],a[100:103],v[236:239],a[236:239]
ds_read_b64_tr_b16 a[100:101],v1 offset:10496
v_mfma_f32_16x16x32_bf16 a[236:239],a[96:99],v[232:235],a[236:239]
ds_read_b64_tr_b16 a[96:97],v1 offset:256
v_mfma_f32_16x16x32_bf16 a[240:243],a[112:115],v[136:139],a[240:243]
ds_read_b64_tr_b16 a[98:99],v1 offset:5376
v_mfma_f32_16x16x32_bf16 a[240:243],a[116:119],v[140:143],a[240:243]
buffer_load_dwordx4 v[72:75],v6,s[28:31],s76 offen offset:2048 sc1
v_mfma_f32_16x16x32_bf16 a[240:243],a[120:123],v[152:155],a[240:243]
ds_read_b64_tr_b16 a[102:103],v1 offset:15616
ds_read_b64_tr_b16 a[108:109],v1 offset:30976
v_mfma_f32_16x16x32_bf16 a[240:243],a[124:127],v[156:159],a[240:243]
ds_read_b64_tr_b16 a[110:111],v1 offset:36096
v_subrev_u32 v1,81920,v1
v_mfma_f32_16x16x32_bf16 a[244:247],a[124:127],v[188:191],a[244:247]
v_add_u32 v42,40960,v1
v_add_u32 v44,122880,v1
v_mfma_f32_16x16x32_bf16 a[244:247],a[120:123],v[184:187],a[244:247]
v_mfma_f32_16x16x32_bf16 a[244:247],a[116:119],v[172:175],a[244:247]
buffer_load_dwordx4 v[76:79],v6,s[28:31],s76 offen offset:3072 sc1
v_mfma_f32_16x16x32_bf16 a[244:247],a[112:115],v[168:171],a[244:247]
v_mfma_f32_16x16x32_bf16 a[248:251],a[112:115],v[200:203],a[248:251]
v_mfma_f32_16x16x32_bf16 a[248:251],a[116:119],v[204:207],a[248:251]
v_mfma_f32_16x16x32_bf16 a[248:251],a[120:123],v[216:219],a[248:251]
v_mfma_f32_16x16x32_bf16 a[248:251],a[124:127],v[220:223],a[248:251]
buffer_load_dwordx4 v[80:83],v6,s[28:31],s77 offen sc1
v_mfma_f32_16x16x32_bf16 a[252:255],a[124:127],v[252:255],a[252:255]
ds_read_b64_tr_b16 a[124:125],v2 offset:31008
ds_read_b64_tr_b16 a[126:127],v2 offset:36128
v_mfma_f32_16x16x32_bf16 a[252:255],a[120:123],v[248:251],a[252:255]
ds_read_b64_tr_b16 a[120:121],v2 offset:20768
v_mfma_f32_16x16x32_bf16 a[252:255],a[116:119],v[236:239],a[252:255]
ds_read_b64_tr_b16 a[116:117],v2 offset:10528
v_mfma_f32_16x16x32_bf16 a[252:255],a[112:115],v[232:235],a[252:255]
ds_read_b64_tr_b16 a[112:113],v2 offset:288
v_mfma_f32_16x16x32_bf16 a[0:3],a[128:131],v[136:139],a[0:3]
buffer_load_dwordx4 v[84:87],v6,s[28:31],s77 offen offset:1024 sc1
)A"
R"A(v_mfma_f32_16x16x32_bf16 a[0:3],a[132:135],v[140:143],a[0:3]
ds_read_b64_tr_b16 a[114:115],v2 offset:5408
ds_read_b64_tr_b16 a[118:119],v2 offset:15648
v_mfma_f32_16x16x32_bf16 a[0:3],a[136:139],v[152:155],a[0:3]
ds_read_b64_tr_b16 a[122:123],v2 offset:25888
v_subrev_u32 v2,81920,v2
v_mfma_f32_16x16x32_bf16 a[0:3],a[140:143],v[156:159],a[0:3]
v_add_u32 v43,40960,v2
v_add_u32 v45,122880,v2
v_mfma_f32_16x16x32_bf16 a[4:7],a[140:143],v[188:191],a[4:7]
v_mfma_f32_16x16x32_bf16 a[4:7],a[136:139],v[184:187],a[4:7]
buffer_load_dwordx4 v[88:91],v6,s[28:31],s77 offen offset:2048 sc1
v_mfma_f32_16x16x32_bf16 a[4:7],a[132:135],v[172:175],a[4:7]
v_mfma_f32_16x16x32_bf16 a[4:7],a[128:131],v[168:171],a[4:7]
v_mfma_f32_16x16x32_bf16 a[8:11],a[128:131],v[200:203],a[8:11]
v_mfma_f32_16x16x32_bf16 a[8:11],a[132:135],v[204:207],a[8:11]
v_mfma_f32_16x16x32_bf16 a[8:11],a[136:139],v[216:219],a[8:11]
buffer_load_dwordx4 v[92:95],v6,s[28:31],s77 offen offset:3072 sc1
v_mfma_f32_16x16x32_bf16 a[8:11],a[140:143],v[220:223],a[8:11]
v_mfma_f32_16x16x32_bf16 a[12:15],a[140:143],v[252:255],a[12:15]
ds_read_b64_tr_b16 a[140:141],v1 offset:30720
ds_read_b64_tr_b16 a[142:143],v1 offset:35840
v_mfma_f32_16x16x32_bf16 a[12:15],a[136:139],v[248:251],a[12:15]
ds_read_b64_tr_b16 a[136:137],v1 offset:20480
v_mfma_f32_16x16x32_bf16 a[12:15],a[132:135],v[236:239],a[12:15]
ds_read_b64_tr_b16 a[132:133],v1 offset:10240
v_mfma_f32_16x16x32_bf16 a[12:15],a[128:131],v[232:235],a[12:15]
buffer_load_dwordx4 v[96:99],v6,s[28:31],s78 offen sc1
v_mfma_f32_16x16x32_bf16 a[16:19],a[144:147],v[136:139],a[16:19]
ds_read_b64_tr_b16 a[128:129],v1
ds_read_b64_tr_b16 a[130:131],v1 offset:5120
v_mfma_f32_16x16x32_bf16 a[16:19],a[148:151],v[140:143],a[16:19]
ds_read_b64_tr_b16 a[134:135],v1 offset:15360
v_mfma_f32_16x16x32_bf16 a[16:19],a[152:155],v[152:155],a[16:19]
ds_read_b64_tr_b16 a[138:139],v1 offset:25600
v_mfma_f32_16x16x32_bf16 a[16:19],a[156:159],v[156:159],a[16:19]
v_mfma_f32_16x16x32_bf16 a[20:23],a[156:159],v[188:191],a[20:23]
buffer_load_dwordx4 v[100:103],v6,s[28:31],s78 offen offset:1024 sc1
v_mfma_f32_16x16x32_bf16 a[20:23],a[152:155],v[184:187],a[20:23]
v_mfma_f32_16x16x32_bf16 a[20:23],a[148:151],v[172:175],a[20:23]
v_mfma_f32_16x16x32_bf16 a[20:23],a[144:147],v[168:171],a[20:23]
v_mfma_f32_16x16x32_bf16 a[24:27],a[144:147],v[200:203],a[24:27]
v_mfma_f32_16x16x32_bf16 a[24:27],a[148:151],v[204:207],a[24:27]
buffer_load_dwordx4 v[104:107],v6,s[28:31],s78 offen offset:2048 sc1
v_mfma_f32_16x16x32_bf16 a[24:27],a[152:155],v[216:219],a[24:27]
v_mfma_f32_16x16x32_bf16 a[24:27],a[156:159],v[220:223],a[24:27]
v_mfma_f32_16x16x32_bf16 a[28:31],a[156:159],v[252:255],a[28:31]
ds_read_b64_tr_b16 a[156:157],v2 offset:30752
ds_read_b64_tr_b16 a[158:159],v2 offset:35872
v_mfma_f32_16x16x32_bf16 a[28:31],a[152:155],v[248:251],a[28:31]
ds_read_b64_tr_b16 a[152:153],v2 offset:20512
v_mfma_f32_16x16x32_bf16 a[28:31],a[148:151],v[236:239],a[28:31]
buffer_load_dwordx4 v[108:111],v6,s[28:31],s78 offen offset:3072 sc1
v_mfma_f32_16x16x32_bf16 a[28:31],a[144:147],v[232:235],a[28:31]
s_cmp_ge_u32 2,s71
s_cselect_b32 s80,7,5
s_mul_i32 s80,s80,163840
v_mfma_f32_16x16x32_bf16 a[32:35],a[160:163],v[136:139],a[32:35]
s_add_u32 s80,s80,s81
s_add_u32 s83,s80,20480
ds_read_b64_tr_b16 a[144:145],v2 offset:32
v_mfma_f32_16x16x32_bf16 a[32:35],a[164:167],v[140:143],a[32:35]
ds_read_b64_tr_b16 a[146:147],v2 offset:5152
ds_read_b64_tr_b16 a[148:149],v2 offset:10272
v_mfma_f32_16x16x32_bf16 a[32:35],a[168:171],v[152:155],a[32:35]
ds_read_b64_tr_b16 a[150:151],v2 offset:15392
v_mfma_f32_16x16x32_bf16 a[32:35],a[172:175],v[156:159],a[32:35]
buffer_load_dwordx4 v[112:115],v6,s[28:31],s79 offen sc1
v_mfma_f32_16x16x32_bf16 a[36:39],a[172:175],v[188:191],a[36:39]
ds_read_b64_tr_b16 a[154:155],v2 offset:25632
v_mfma_f32_16x16x32_bf16 a[36:39],a[168:171],v[184:187],a[36:39]
v_mfma_f32_16x16x32_bf16 a[36:39],a[164:167],v[172:175],a[36:39]
v_mfma_f32_16x16x32_bf16 a[36:39],a[160:163],v[168:171],a[36:39]
v_mfma_f32_16x16x32_bf16 a[40:43],a[160:163],v[200:203],a[40:43]
buffer_load_dwordx4 v[116:119],v6,s[28:31],s79 offen offset:1024 sc1
v_mfma_f32_16x16x32_bf16 a[40:43],a[164:167],v[204:207],a[40:43]
v_mfma_f32_16x16x32_bf16 a[40:43],a[168:171],v[216:219],a[40:43]
v_mfma_f32_16x16x32_bf16 a[40:43],a[172:175],v[220:223],a[40:43]
v_mfma_f32_16x16x32_bf16 a[44:47],a[172:175],v[252:255],a[44:47]
ds_read_b64_tr_b16 a[172:173],v1 offset:30784
ds_read_b64_tr_b16 a[174:175],v1 offset:35904
v_mfma_f32_16x16x32_bf16 a[44:47],a[168:171],v[248:251],a[44:47]
buffer_load_dwordx4 v[120:123],v6,s[28:31],s79 offen offset:2048 sc1
v_mfma_f32_16x16x32_bf16 a[44:47],a[164:167],v[236:239],a[44:47]
ds_read_b64_tr_b16 a[164:165],v1 offset:10304
ds_read_b64_tr_b16 a[166:167],v1 offset:15424
v_mfma_f32_16x16x32_bf16 a[44:47],a[160:163],v[232:235],a[44:47]
s_waitcnt lgkmcnt(4)
ds_read_b64_tr_b16 a[160:161],v1 offset:64
v_mfma_f32_16x16x32_bf16 a[48:51],a[176:179],v[136:139],a[48:51]
ds_read_b64_tr_b16 a[162:163],v1 offset:5184
v_mfma_f32_16x16x32_bf16 a[48:51],a[180:183],v[140:143],a[48:51]
ds_read_b64_tr_b16 a[168:169],v1 offset:20544
v_mfma_f32_16x16x32_bf16 a[48:51],a[184:187],v[152:155],a[48:51]
buffer_load_dwordx4 v[124:127],v6,s[28:31],s79 offen offset:3072 sc1
v_mfma_f32_16x16x32_bf16 a[48:51],a[188:191],v[156:159],a[48:51]
ds_read_b64_tr_b16 a[170:171],v1 offset:25664
v_mfma_f32_16x16x32_bf16 a[52:55],a[188:191],v[188:191],a[52:55]
s_add_u32 m0,s82,122880
v_mfma_f32_16x16x32_bf16 a[52:55],a[184:187],v[184:187],a[52:55]
v_mfma_f32_16x16x32_bf16 a[52:55],a[180:183],v[172:175],a[52:55]
v_mfma_f32_16x16x32_bf16 a[52:55],a[176:179],v[168:171],a[52:55]
buffer_load_dwordx4 v7,s[72:75],s80 offen lds
v_mfma_f32_16x16x32_bf16 a[56:59],a[176:179],v[200:203],a[56:59]
v_mfma_f32_16x16x32_bf16 a[56:59],a[180:183],v[204:207],a[56:59]
v_mfma_f32_16x16x32_bf16 a[56:59],a[184:187],v[216:219],a[56:59]
v_mfma_f32_16x16x32_bf16 a[56:59],a[188:191],v[220:223],a[56:59]
v_mfma_f32_16x16x32_bf16 a[60:63],a[188:191],v[252:255],a[60:63]
buffer_load_dwordx4 v8,s[72:75],s80 offen offset:1024 lds
v_mfma_f32_16x16x32_bf16 a[60:63],a[184:187],v[248:251],a[60:63]
ds_read_b64_tr_b16 a[184:185],v2 offset:20576
ds_read_b64_tr_b16 a[186:187],v2 offset:25696
v_mfma_f32_16x16x32_bf16 a[60:63],a[180:183],v[236:239],a[60:63]
ds_read_b64_tr_b16 a[180:181],v2 offset:10336
v_mfma_f32_16x16x32_bf16 a[60:63],a[176:179],v[232:235],a[60:63]
ds_read_b64_tr_b16 a[176:177],v2 offset:96
v_mfma_f32_16x16x32_bf16 a[64:67],a[96:99],v[136:139],a[64:67]
ds_read_b64_tr_b16 a[178:179],v2 offset:5216
v_mfma_f32_16x16x32_bf16 a[64:67],a[100:103],v[140:143],a[64:67]
buffer_load_dwordx4 v9,s[72:75],s80 offen offset:2048 lds
v_mfma_f32_16x16x32_bf16 a[64:67],a[104:107],v[152:155],a[64:67]
ds_read_b64_tr_b16 a[182:183],v2 offset:15456
ds_read_b64_tr_b16 a[188:189],v2 offset:30816
v_mfma_f32_16x16x32_bf16 a[64:67],a[108:111],v[156:159],a[64:67]
ds_read_b64_tr_b16 a[190:191],v2 offset:35936
v_mfma_f32_16x16x32_bf16 a[68:71],a[108:111],v[188:191],a[68:71]
v_mfma_f32_16x16x32_bf16 a[68:71],a[104:107],v[184:187],a[68:71]
v_mfma_f32_16x16x32_bf16 a[68:71],a[100:103],v[172:175],a[68:71]
buffer_load_dwordx4 v10,s[72:75],s80 offen offset:3072 lds
v_mfma_f32_16x16x32_bf16 a[68:71],a[96:99],v[168:171],a[68:71]
v_mfma_f32_16x16x32_bf16 a[72:75],a[96:99],v[200:203],a[72:75]
s_add_u32 m0,s82,126976
v_mfma_f32_16x16x32_bf16 a[72:75],a[100:103],v[204:207],a[72:75]
v_mfma_f32_16x16x32_bf16 a[72:75],a[104:107],v[216:219],a[72:75]
s_cmp_ge_u32 2,s71
s_cselect_b32 s76,7,5
s_lshl_b32 s76,s76,16
v_mfma_f32_16x16x32_bf16 a[72:75],a[108:111],v[220:223],a[72:75]
s_add_u32 s77,s76,4096
buffer_load_dwordx4 v11,s[72:75],s80 offen lds
v_mfma_f32_16x16x32_bf16 a[76:79],a[108:111],v[252:255],a[76:79]
s_add_u32 s78,s76,8192
s_add_u32 s79,s76,12288
s_max_i32 s76,s76,s18
v_mfma_f32_16x16x32_bf16 a[76:79],a[104:107],v[248:251],a[76:79]
s_add_u32 m0,s82,128000
s_max_i32 s77,s77,s19
ds_read_b64_tr_b16 a[104:105],v1 offset:20608
v_mfma_f32_16x16x32_bf16 a[76:79],a[100:103],v[236:239],a[76:79]
s_max_i32 s78,s78,s100
ds_read_b64_tr_b16 a[100:101],v1 offset:10368
ds_read_b64_tr_b16 a[102:103],v1 offset:15488
v_mfma_f32_16x16x32_bf16 a[76:79],a[96:99],v[232:235],a[76:79]
s_max_i32 s79,s79,s101
ds_read_b64_tr_b16 a[96:97],v1 offset:128
v_mfma_f32_16x16x32_bf16 a[80:83],a[112:115],v[136:139],a[80:83]
buffer_load_dwordx4 v7,s[72:75],s83 offen lds
v_mfma_f32_16x16x32_bf16 a[80:83],a[116:119],v[140:143],a[80:83]
ds_read_b64_tr_b16 a[98:99],v1 offset:5248
ds_read_b64_tr_b16 a[106:107],v1 offset:25728
v_mfma_f32_16x16x32_bf16 a[80:83],a[120:123],v[152:155],a[80:83]
ds_read_b64_tr_b16 a[108:109],v1 offset:30848
v_mfma_f32_16x16x32_bf16 a[80:83],a[124:127],v[156:159],a[80:83]
ds_read_b64_tr_b16 a[110:111],v1 offset:35968
v_mfma_f32_16x16x32_bf16 a[84:87],a[124:127],v[188:191],a[84:87]
v_mfma_f32_16x16x32_bf16 a[84:87],a[120:123],v[184:187],a[84:87]
buffer_load_dwordx4 v8,s[72:75],s83 offen offset:1024 lds
v_mfma_f32_16x16x32_bf16 a[84:87],a[116:119],v[172:175],a[84:87]
v_mfma_f32_16x16x32_bf16 a[84:87],a[112:115],v[168:171],a[84:87]
v_mfma_f32_16x16x32_bf16 a[88:91],a[112:115],v[200:203],a[88:91]
v_mfma_f32_16x16x32_bf16 a[88:91],a[116:119],v[204:207],a[88:91]
v_mfma_f32_16x16x32_bf16 a[88:91],a[120:123],v[216:219],a[88:91]
buffer_load_dwordx4 v9,s[72:75],s83 offen offset:2048 lds
v_mfma_f32_16x16x32_bf16 a[88:91],a[124:127],v[220:223],a[88:91]
v_mfma_f32_16x16x32_bf16 a[92:95],a[124:127],v[252:255],a[92:95]
ds_read_b64_tr_b16 a[124:125],v2 offset:30880
ds_read_b64_tr_b16 a[126:127],v2 offset:36000
v_mfma_f32_16x16x32_bf16 a[92:95],a[120:123],v[248:251],a[92:95]
ds_read_b64_tr_b16 a[120:121],v2 offset:20640
v_mfma_f32_16x16x32_bf16 a[92:95],a[116:119],v[236:239],a[92:95]
ds_read_b64_tr_b16 a[116:117],v2 offset:10400
v_mfma_f32_16x16x32_bf16 a[92:95],a[112:115],v[232:235],a[92:95]
buffer_load_dwordx4 v10,s[72:75],s83 offen offset:3072 lds
v_mfma_f32_16x16x32_bf16 a[192:195],a[128:131],v[128:131],a[192:195]
ds_read_b64_tr_b16 a[112:113],v2 offset:160
ds_read_b64_tr_b16 a[114:115],v2 offset:5280
v_mfma_f32_16x16x32_bf16 a[192:195],a[132:135],v[160:163],a[192:195]
s_add_u32 m0,s82,132096
ds_read_b64_tr_b16 a[118:119],v2 offset:15520
v_mfma_f32_16x16x32_bf16 a[192:195],a[136:139],v[192:195],a[192:195]
ds_read_b64_tr_b16 a[122:123],v2 offset:25760
v_mfma_f32_16x16x32_bf16 a[192:195],a[140:143],v[224:227],a[192:195]
buffer_load_dwordx4 v[136:139],v6,s[28:31],s76 offen sc1
v_mfma_f32_16x16x32_bf16 a[196:199],a[140:143],v[228:231],a[196:199]
v_mfma_f32_16x16x32_bf16 a[196:199],a[136:139],v[196:199],a[196:199]
v_mfma_f32_16x16x32_bf16 a[196:199],a[132:135],v[164:167],a[196:199]
v_mfma_f32_16x16x32_bf16 a[196:199],a[128:131],v[132:135],a[196:199]
s_waitcnt vmcnt(14)
buffer_load_dwordx4 v11,s[72:75],s83 offen lds
v_mfma_f32_16x16x32_bf16 a[200:203],a[128:131],v[144:147],a[200:203]
ds_read_b64_tr_b16 a[128:129],v1 offset:192
ds_read_b64_tr_b16 a[130:131],v1 offset:5312
v_mfma_f32_16x16x32_bf16 a[200:203],a[132:135],v[176:179],a[200:203]
ds_read_b64_tr_b16 a[132:133],v1 offset:10432
v_mfma_f32_16x16x32_bf16 a[200:203],a[136:139],v[208:211],a[200:203]
ds_read_b64_tr_b16 a[134:135],v1 offset:15552
v_mfma_f32_16x16x32_bf16 a[200:203],a[140:143],v[240:243],a[200:203]
buffer_load_dwordx4 v[140:143],v6,s[28:31],s76 offen offset:1024 sc1
v_mfma_f32_16x16x32_bf16 a[208:211],a[144:147],v[128:131],a[208:211]
ds_read_b64_tr_b16 a[136:137],v1 offset:20672
ds_read_b64_tr_b16 a[138:139],v1 offset:25792
v_mfma_f32_16x16x32_bf16 a[208:211],a[148:151],v[160:163],a[208:211]
ds_read_b64_tr_b16 a[140:141],v1 offset:30912
v_mfma_f32_16x16x32_bf16 a[208:211],a[152:155],v[192:195],a[208:211]
ds_read_b64_tr_b16 a[142:143],v1 offset:36032
v_mfma_f32_16x16x32_bf16 a[208:211],a[156:159],v[224:227],a[208:211]
buffer_load_dwordx4 v[152:155],v6,s[28:31],s76 offen offset:2048 sc1
v_mfma_f32_16x16x32_bf16 a[212:215],a[156:159],v[228:231],a[212:215]
v_mfma_f32_16x16x32_bf16 a[212:215],a[152:155],v[196:199],a[212:215]
v_mfma_f32_16x16x32_bf16 a[212:215],a[148:151],v[164:167],a[212:215]
)A"
R"A(v_mfma_f32_16x16x32_bf16 a[212:215],a[144:147],v[132:135],a[212:215]
buffer_load_dwordx4 v[156:159],v6,s[28:31],s76 offen offset:3072 sc1
v_mfma_f32_16x16x32_bf16 a[216:219],a[144:147],v[144:147],a[216:219]
s_mul_i32 s76,s70,12
ds_read_b64_tr_b16 a[144:145],v2 offset:224
ds_read_b64_tr_b16 a[146:147],v2 offset:5344
v_mfma_f32_16x16x32_bf16 a[216:219],a[148:151],v[176:179],a[216:219]
s_add_u32 s76,s76,0
s_add_u32 s76,s76,s40
ds_read_b64_tr_b16 a[148:149],v2 offset:10464
v_mfma_f32_16x16x32_bf16 a[216:219],a[152:155],v[208:211],a[216:219]
s_mul_i32 s76,s76,5120
s_max_i32 s76,s76,s101
ds_read_b64_tr_b16 a[150:151],v2 offset:15584
v_mfma_f32_16x16x32_bf16 a[216:219],a[156:159],v[240:243],a[216:219]
s_waitcnt lgkmcnt(4)
buffer_load_dwordx4 v[168:171],v6,s[28:31],s77 offen sc1
v_mfma_f32_16x16x32_bf16 a[224:227],a[160:163],v[128:131],a[224:227]
ds_read_b64_tr_b16 a[152:153],v2 offset:20704
ds_read_b64_tr_b16 a[154:155],v2 offset:25824
v_mfma_f32_16x16x32_bf16 a[224:227],a[164:167],v[160:163],a[224:227]
ds_read_b64_tr_b16 a[156:157],v2 offset:30944
v_mfma_f32_16x16x32_bf16 a[224:227],a[168:171],v[192:195],a[224:227]
ds_read_b64_tr_b16 a[158:159],v2 offset:36064
v_mfma_f32_16x16x32_bf16 a[224:227],a[172:175],v[224:227],a[224:227]
buffer_load_dwordx4 v[172:175],v6,s[28:31],s77 offen offset:1024 sc1
v_mfma_f32_16x16x32_bf16 a[228:231],a[172:175],v[228:231],a[228:231]
v_mfma_f32_16x16x32_bf16 a[228:231],a[168:171],v[196:199],a[228:231]
v_mfma_f32_16x16x32_bf16 a[228:231],a[164:167],v[164:167],a[228:231]
v_mfma_f32_16x16x32_bf16 a[228:231],a[160:163],v[132:135],a[228:231]
v_mfma_f32_16x16x32_bf16 a[232:235],a[160:163],v[144:147],a[232:235]
buffer_load_dwordx4 v[184:187],v6,s[28:31],s77 offen offset:2048 sc1
v_mfma_f32_16x16x32_bf16 a[232:235],a[164:167],v[176:179],a[232:235]
ds_read_b64_tr_b16 a[160:161],v1 offset:256
ds_read_b64_tr_b16 a[162:163],v1 offset:5376
v_mfma_f32_16x16x32_bf16 a[232:235],a[168:171],v[208:211],a[232:235]
ds_read_b64_tr_b16 a[164:165],v1 offset:10496
v_mfma_f32_16x16x32_bf16 a[232:235],a[172:175],v[240:243],a[232:235]
ds_read_b64_tr_b16 a[166:167],v1 offset:15616
v_mfma_f32_16x16x32_bf16 a[240:243],a[176:179],v[128:131],a[240:243]
ds_read_b64_tr_b16 a[168:169],v1 offset:20736
v_mfma_f32_16x16x32_bf16 a[240:243],a[180:183],v[160:163],a[240:243]
buffer_load_dwordx4 v[188:191],v6,s[28:31],s77 offen offset:3072 sc1
v_mfma_f32_16x16x32_bf16 a[240:243],a[184:187],v[192:195],a[240:243]
s_mul_i32 s77,s70,12
ds_read_b64_tr_b16 a[170:171],v1 offset:25856
ds_read_b64_tr_b16 a[172:173],v1 offset:30976
v_mfma_f32_16x16x32_bf16 a[240:243],a[188:191],v[224:227],a[240:243]
s_add_u32 s77,s77,4
s_add_u32 s77,s77,s40
ds_read_b64_tr_b16 a[174:175],v1 offset:36096
v_mfma_f32_16x16x32_bf16 a[244:247],a[188:191],v[228:231],a[244:247]
s_mul_i32 s77,s77,5120
s_max_i32 s77,s77,s101
v_mfma_f32_16x16x32_bf16 a[244:247],a[184:187],v[196:199],a[244:247]
v_mfma_f32_16x16x32_bf16 a[244:247],a[180:183],v[164:167],a[244:247]
buffer_load_dwordx4 v[200:203],v6,s[28:31],s78 offen sc1
v_mfma_f32_16x16x32_bf16 a[244:247],a[176:179],v[132:135],a[244:247]
v_mfma_f32_16x16x32_bf16 a[248:251],a[176:179],v[144:147],a[248:251]
ds_read_b64_tr_b16 a[176:177],v2 offset:288
ds_read_b64_tr_b16 a[178:179],v2 offset:5408
v_mfma_f32_16x16x32_bf16 a[248:251],a[180:183],v[176:179],a[248:251]
ds_read_b64_tr_b16 a[180:181],v2 offset:10528
v_mfma_f32_16x16x32_bf16 a[248:251],a[184:187],v[208:211],a[248:251]
ds_read_b64_tr_b16 a[182:183],v2 offset:15648
v_mfma_f32_16x16x32_bf16 a[248:251],a[188:191],v[240:243],a[248:251]
buffer_load_dwordx4 v[204:207],v6,s[28:31],s78 offen offset:1024 sc1
v_mfma_f32_16x16x32_bf16 a[0:3],a[96:99],v[128:131],a[0:3]
ds_read_b64_tr_b16 a[184:185],v2 offset:20768
ds_read_b64_tr_b16 a[186:187],v2 offset:25888
v_mfma_f32_16x16x32_bf16 a[0:3],a[100:103],v[160:163],a[0:3]
ds_read_b64_tr_b16 a[188:189],v2 offset:31008
v_mfma_f32_16x16x32_bf16 a[0:3],a[104:107],v[192:195],a[0:3]
ds_read_b64_tr_b16 a[190:191],v2 offset:36128
v_mfma_f32_16x16x32_bf16 a[0:3],a[108:111],v[224:227],a[0:3]
v_mfma_f32_16x16x32_bf16 a[4:7],a[108:111],v[228:231],a[4:7]
buffer_load_dwordx4 v[216:219],v6,s[28:31],s78 offen offset:2048 sc1
v_mfma_f32_16x16x32_bf16 a[4:7],a[104:107],v[196:199],a[4:7]
v_mfma_f32_16x16x32_bf16 a[4:7],a[100:103],v[164:167],a[4:7]
v_mfma_f32_16x16x32_bf16 a[4:7],a[96:99],v[132:135],a[4:7]
v_mfma_f32_16x16x32_bf16 a[8:11],a[96:99],v[144:147],a[8:11]
v_mfma_f32_16x16x32_bf16 a[8:11],a[100:103],v[176:179],a[8:11]
buffer_load_dwordx4 v[220:223],v6,s[28:31],s78 offen offset:3072 sc1
v_mfma_f32_16x16x32_bf16 a[8:11],a[104:107],v[208:211],a[8:11]
s_mul_i32 s78,s70,12
s_add_u32 s78,s78,8
s_add_u32 s78,s78,s40
v_mfma_f32_16x16x32_bf16 a[8:11],a[108:111],v[240:243],a[8:11]
s_mul_i32 s78,s78,5120
s_max_i32 s78,s78,s101
v_mfma_f32_16x16x32_bf16 a[16:19],a[112:115],v[128:131],a[16:19]
v_mfma_f32_16x16x32_bf16 a[16:19],a[116:119],v[160:163],a[16:19]
v_mfma_f32_16x16x32_bf16 a[16:19],a[120:123],v[192:195],a[16:19]
buffer_load_dwordx4 v[232:235],v6,s[28:31],s79 offen sc1
v_mfma_f32_16x16x32_bf16 a[16:19],a[124:127],v[224:227],a[16:19]
v_mfma_f32_16x16x32_bf16 a[20:23],a[124:127],v[228:231],a[20:23]
v_mfma_f32_16x16x32_bf16 a[20:23],a[120:123],v[196:199],a[20:23]
v_mfma_f32_16x16x32_bf16 a[20:23],a[116:119],v[164:167],a[20:23]
v_mfma_f32_16x16x32_bf16 a[20:23],a[112:115],v[132:135],a[20:23]
buffer_load_dwordx4 v[236:239],v6,s[28:31],s79 offen offset:1024 sc1
v_mfma_f32_16x16x32_bf16 a[24:27],a[112:115],v[144:147],a[24:27]
v_mfma_f32_16x16x32_bf16 a[24:27],a[116:119],v[176:179],a[24:27]
v_mfma_f32_16x16x32_bf16 a[24:27],a[120:123],v[208:211],a[24:27]
v_mfma_f32_16x16x32_bf16 a[24:27],a[124:127],v[240:243],a[24:27]
v_mfma_f32_16x16x32_bf16 a[32:35],a[128:131],v[128:131],a[32:35]
buffer_load_dwordx4 v[248:251],v6,s[28:31],s79 offen offset:2048 sc1
v_mfma_f32_16x16x32_bf16 a[32:35],a[132:135],v[160:163],a[32:35]
v_mfma_f32_16x16x32_bf16 a[32:35],a[136:139],v[192:195],a[32:35]
v_mfma_f32_16x16x32_bf16 a[32:35],a[140:143],v[224:227],a[32:35]
v_mfma_f32_16x16x32_bf16 a[36:39],a[140:143],v[228:231],a[36:39]
v_mfma_f32_16x16x32_bf16 a[36:39],a[136:139],v[196:199],a[36:39]
buffer_load_dwordx4 v[252:255],v6,s[28:31],s79 offen offset:3072 sc1
v_mfma_f32_16x16x32_bf16 a[36:39],a[132:135],v[164:167],a[36:39]
v_mfma_f32_16x16x32_bf16 a[36:39],a[128:131],v[132:135],a[36:39]
v_mfma_f32_16x16x32_bf16 a[40:43],a[128:131],v[144:147],a[40:43]
v_mfma_f32_16x16x32_bf16 a[40:43],a[132:135],v[176:179],a[40:43]
v_mfma_f32_16x16x32_bf16 a[40:43],a[136:139],v[208:211],a[40:43]
s_waitcnt vmcnt(15)
v_mfma_f32_16x16x32_bf16 a[40:43],a[140:143],v[240:243],a[40:43]
s_barrier
ds_read_b64_tr_b16 a[96:97],v42
ds_read_b64_tr_b16 a[98:99],v42 offset:5120
s_waitcnt lgkmcnt(2)
v_mfma_f32_16x16x32_bf16 a[48:51],a[144:147],v[128:131],a[48:51]
ds_read_b64_tr_b16 a[100:101],v42 offset:10240
v_mfma_f32_16x16x32_bf16 a[48:51],a[148:151],v[160:163],a[48:51]
ds_read_b64_tr_b16 a[102:103],v42 offset:15360
v_mfma_f32_16x16x32_bf16 a[48:51],a[152:155],v[192:195],a[48:51]
ds_read_b64_tr_b16 a[104:105],v42 offset:20480
v_mfma_f32_16x16x32_bf16 a[48:51],a[156:159],v[224:227],a[48:51]
ds_read_b64_tr_b16 a[106:107],v42 offset:25600
ds_read_b64_tr_b16 a[108:109],v42 offset:30720
v_mfma_f32_16x16x32_bf16 a[52:55],a[156:159],v[228:231],a[52:55]
ds_read_b64_tr_b16 a[110:111],v42 offset:35840
v_mfma_f32_16x16x32_bf16 a[52:55],a[152:155],v[196:199],a[52:55]
ds_read_b64_tr_b16 a[112:113],v44
v_mfma_f32_16x16x32_bf16 a[52:55],a[148:151],v[164:167],a[52:55]
ds_read_b64_tr_b16 a[114:115],v44 offset:5120
v_mfma_f32_16x16x32_bf16 a[52:55],a[144:147],v[132:135],a[52:55]
ds_read_b64_tr_b16 a[116:117],v44 offset:10240
ds_read_b64_tr_b16 a[118:119],v44 offset:15360
v_mfma_f32_16x16x32_bf16 a[56:59],a[144:147],v[144:147],a[56:59]
ds_read_b64_tr_b16 a[120:121],v44 offset:20480
v_mfma_f32_16x16x32_bf16 a[56:59],a[148:151],v[176:179],a[56:59]
ds_read_b64_tr_b16 a[122:123],v44 offset:25600
v_mfma_f32_16x16x32_bf16 a[56:59],a[152:155],v[208:211],a[56:59]
ds_read_b64_tr_b16 a[124:125],v44 offset:30720
v_mfma_f32_16x16x32_bf16 a[56:59],a[156:159],v[240:243],a[56:59]
ds_read_b64_tr_b16 a[126:127],v44 offset:35840
ds_read_b64_tr_b16 a[128:129],v43 offset:32
v_mfma_f32_16x16x32_bf16 a[64:67],a[160:163],v[128:131],a[64:67]
ds_read_b64_tr_b16 a[130:131],v43 offset:5152
v_mfma_f32_16x16x32_bf16 a[64:67],a[164:167],v[160:163],a[64:67]
ds_read_b64_tr_b16 a[132:133],v43 offset:10272
v_mfma_f32_16x16x32_bf16 a[64:67],a[168:171],v[192:195],a[64:67]
ds_read_b64_tr_b16 a[134:135],v43 offset:15392
v_mfma_f32_16x16x32_bf16 a[64:67],a[172:175],v[224:227],a[64:67]
ds_read_b64_tr_b16 a[136:137],v43 offset:20512
ds_read_b64_tr_b16 a[138:139],v43 offset:25632
v_mfma_f32_16x16x32_bf16 a[68:71],a[172:175],v[228:231],a[68:71]
ds_read_b64_tr_b16 a[140:141],v43 offset:30752
v_mfma_f32_16x16x32_bf16 a[68:71],a[168:171],v[196:199],a[68:71]
ds_read_b64_tr_b16 a[142:143],v43 offset:35872
v_mfma_f32_16x16x32_bf16 a[68:71],a[164:167],v[164:167],a[68:71]
ds_read_b64_tr_b16 a[144:145],v45 offset:32
v_mfma_f32_16x16x32_bf16 a[68:71],a[160:163],v[132:135],a[68:71]
ds_read_b64_tr_b16 a[146:147],v45 offset:5152
ds_read_b64_tr_b16 a[148:149],v45 offset:10272
v_mfma_f32_16x16x32_bf16 a[72:75],a[160:163],v[144:147],a[72:75]
ds_read_b64_tr_b16 a[150:151],v45 offset:15392
v_mfma_f32_16x16x32_bf16 a[72:75],a[164:167],v[176:179],a[72:75]
ds_read_b64_tr_b16 a[152:153],v45 offset:20512
v_mfma_f32_16x16x32_bf16 a[72:75],a[168:171],v[208:211],a[72:75]
ds_read_b64_tr_b16 a[154:155],v45 offset:25632
v_mfma_f32_16x16x32_bf16 a[72:75],a[172:175],v[240:243],a[72:75]
ds_read_b64_tr_b16 a[156:157],v45 offset:30752
ds_read_b64_tr_b16 a[158:159],v45 offset:35872
v_mfma_f32_16x16x32_bf16 a[80:83],a[176:179],v[128:131],a[80:83]
ds_read_b64_tr_b16 a[160:161],v42 offset:64
v_mfma_f32_16x16x32_bf16 a[80:83],a[180:183],v[160:163],a[80:83]
ds_read_b64_tr_b16 a[162:163],v42 offset:5184
v_mfma_f32_16x16x32_bf16 a[80:83],a[184:187],v[192:195],a[80:83]
ds_read_b64_tr_b16 a[164:165],v42 offset:10304
v_mfma_f32_16x16x32_bf16 a[80:83],a[188:191],v[224:227],a[80:83]
ds_read_b64_tr_b16 a[166:167],v42 offset:15424
ds_read_b64_tr_b16 a[168:169],v42 offset:20544
v_mfma_f32_16x16x32_bf16 a[84:87],a[188:191],v[228:231],a[84:87]
ds_read_b64_tr_b16 a[170:171],v42 offset:25664
v_mfma_f32_16x16x32_bf16 a[84:87],a[184:187],v[196:199],a[84:87]
ds_read_b64_tr_b16 a[172:173],v42 offset:30784
v_mfma_f32_16x16x32_bf16 a[84:87],a[180:183],v[164:167],a[84:87]
ds_read_b64_tr_b16 a[174:175],v42 offset:35904
v_mfma_f32_16x16x32_bf16 a[84:87],a[176:179],v[132:135],a[84:87]
v_mfma_f32_16x16x32_bf16 a[88:91],a[176:179],v[144:147],a[88:91]
ds_read_b64_tr_b16 a[176:177],v44 offset:64
ds_read_b64_tr_b16 a[178:179],v44 offset:5184
v_mfma_f32_16x16x32_bf16 a[88:91],a[180:183],v[176:179],a[88:91]
ds_read_b64_tr_b16 a[180:181],v44 offset:10304
v_mfma_f32_16x16x32_bf16 a[88:91],a[184:187],v[208:211],a[88:91]
ds_read_b64_tr_b16 a[182:183],v44 offset:15424
v_mfma_f32_16x16x32_bf16 a[88:91],a[188:191],v[240:243],a[88:91]
s_waitcnt lgkmcnt(7)
ds_read_b64_tr_b16 a[184:185],v44 offset:20544
v_mfma_f32_16x16x32_bf16 a[192:195],a[96:99],v[64:67],a[192:195]
ds_read_b64_tr_b16 a[186:187],v44 offset:25664
ds_read_b64_tr_b16 a[188:189],v44 offset:30784
v_mfma_f32_16x16x32_bf16 a[192:195],a[100:103],v[68:71],a[192:195]
ds_read_b64_tr_b16 a[190:191],v44 offset:35904
v_mfma_f32_16x16x32_bf16 a[192:195],a[104:107],v[72:75],a[192:195]
v_mfma_f32_16x16x32_bf16 a[192:195],a[108:111],v[76:79],a[192:195]
v_mfma_f32_16x16x32_bf16 a[196:199],a[108:111],v[92:95],a[196:199]
v_mfma_f32_16x16x32_bf16 a[196:199],a[104:107],v[88:91],a[196:199]
v_mfma_f32_16x16x32_bf16 a[196:199],a[100:103],v[84:87],a[196:199]
v_mfma_f32_16x16x32_bf16 a[196:199],a[96:99],v[80:83],a[196:199]
v_mfma_f32_16x16x32_bf16 a[200:203],a[96:99],v[96:99],a[200:203]
ds_read_b64_tr_b16 a[96:97],v43 offset:96
ds_read_b64_tr_b16 a[98:99],v43 offset:5216
v_mfma_f32_16x16x32_bf16 a[200:203],a[100:103],v[100:103],a[200:203]
ds_read_b64_tr_b16 a[100:101],v43 offset:10336
)A"
R"A(v_mfma_f32_16x16x32_bf16 a[200:203],a[104:107],v[104:107],a[200:203]
ds_read_b64_tr_b16 a[102:103],v43 offset:15456
v_mfma_f32_16x16x32_bf16 a[200:203],a[108:111],v[108:111],a[200:203]
ds_read_b64_tr_b16 a[104:105],v43 offset:20576
v_mfma_f32_16x16x32_bf16 a[192:195],a[112:115],v[136:139],a[192:195]
s_waitcnt vmcnt(3)
ds_read_b64_tr_b16 a[106:107],v43 offset:25696
ds_read_b64_tr_b16 a[108:109],v43 offset:30816
v_mfma_f32_16x16x32_bf16 a[192:195],a[116:119],v[140:143],a[192:195]
ds_read_b64_tr_b16 a[110:111],v43 offset:35936
v_mfma_f32_16x16x32_bf16 a[192:195],a[120:123],v[152:155],a[192:195]
v_mfma_f32_16x16x32_bf16 a[192:195],a[124:127],v[156:159],a[192:195]
v_mfma_f32_16x16x32_bf16 a[196:199],a[124:127],v[188:191],a[196:199]
v_mfma_f32_16x16x32_bf16 a[196:199],a[120:123],v[184:187],a[196:199]
v_mfma_f32_16x16x32_bf16 a[196:199],a[116:119],v[172:175],a[196:199]
v_mfma_f32_16x16x32_bf16 a[196:199],a[112:115],v[168:171],a[196:199]
v_mfma_f32_16x16x32_bf16 a[200:203],a[112:115],v[200:203],a[200:203]
ds_read_b64_tr_b16 a[112:113],v45 offset:96
ds_read_b64_tr_b16 a[114:115],v45 offset:5216
v_mfma_f32_16x16x32_bf16 a[200:203],a[116:119],v[204:207],a[200:203]
v_accvgpr_read_b32 v128,a192
v_accvgpr_read_b32 v129,a193
v_mfma_f32_16x16x32_bf16 a[200:203],a[120:123],v[216:219],a[200:203]
v_cvt_pk_bf16_f32 v128,v128,v129
v_accvgpr_read_b32 v130,a194
v_mfma_f32_16x16x32_bf16 a[200:203],a[124:127],v[220:223],a[200:203]
v_accvgpr_read_b32 v131,a195
v_cvt_pk_bf16_f32 v129,v130,v131
v_mfma_f32_16x16x32_bf16 a[208:211],a[128:131],v[64:67],a[208:211]
ds_read_b64_tr_b16 a[116:117],v45 offset:10336
ds_read_b64_tr_b16 a[118:119],v45 offset:15456
v_mfma_f32_16x16x32_bf16 a[208:211],a[132:135],v[68:71],a[208:211]
v_accvgpr_read_b32 v132,a196
v_accvgpr_read_b32 v133,a197
v_mfma_f32_16x16x32_bf16 a[208:211],a[136:139],v[72:75],a[208:211]
v_cvt_pk_bf16_f32 v132,v132,v133
v_accvgpr_read_b32 v134,a198
v_mfma_f32_16x16x32_bf16 a[208:211],a[140:143],v[76:79],a[208:211]
v_accvgpr_read_b32 v135,a199
v_cvt_pk_bf16_f32 v133,v134,v135
v_mfma_f32_16x16x32_bf16 a[212:215],a[140:143],v[92:95],a[212:215]
ds_read_b64_tr_b16 a[120:121],v45 offset:20576
ds_read_b64_tr_b16 a[122:123],v45 offset:25696
v_mfma_f32_16x16x32_bf16 a[212:215],a[136:139],v[88:91],a[212:215]
ds_read_b64_tr_b16 a[124:125],v45 offset:30816
v_accvgpr_read_b32 v144,a200
v_mfma_f32_16x16x32_bf16 a[212:215],a[132:135],v[84:87],a[212:215]
ds_read_b64_tr_b16 a[126:127],v45 offset:35936
v_accvgpr_read_b32 v145,a201
v_mfma_f32_16x16x32_bf16 a[212:215],a[128:131],v[80:83],a[212:215]
v_cvt_pk_bf16_f32 v144,v144,v145
v_accvgpr_read_b32 v146,a202
v_mfma_f32_16x16x32_bf16 a[216:219],a[128:131],v[96:99],a[216:219]
v_accvgpr_read_b32 v147,a203
v_cvt_pk_bf16_f32 v145,v146,v147
v_mfma_f32_16x16x32_bf16 a[216:219],a[132:135],v[100:103],a[216:219]
ds_read_b64_tr_b16 a[128:129],v42 offset:128
ds_read_b64_tr_b16 a[130:131],v42 offset:5248
v_mfma_f32_16x16x32_bf16 a[216:219],a[136:139],v[104:107],a[216:219]
ds_read_b64_tr_b16 a[132:133],v42 offset:10368
v_mfma_f32_16x16x32_bf16 a[216:219],a[140:143],v[108:111],a[216:219]
ds_read_b64_tr_b16 a[134:135],v42 offset:15488
v_mfma_f32_16x16x32_bf16 a[208:211],a[144:147],v[136:139],a[208:211]
ds_read_b64_tr_b16 a[136:137],v42 offset:20608
v_mfma_f32_16x16x32_bf16 a[208:211],a[148:151],v[140:143],a[208:211]
ds_read_b64_tr_b16 a[138:139],v42 offset:25728
ds_read_b64_tr_b16 a[140:141],v42 offset:30848
v_mfma_f32_16x16x32_bf16 a[208:211],a[152:155],v[152:155],a[208:211]
ds_read_b64_tr_b16 a[142:143],v42 offset:35968
v_mfma_f32_16x16x32_bf16 a[208:211],a[156:159],v[156:159],a[208:211]
v_mfma_f32_16x16x32_bf16 a[212:215],a[156:159],v[188:191],a[212:215]
v_mfma_f32_16x16x32_bf16 a[212:215],a[152:155],v[184:187],a[212:215]
v_mfma_f32_16x16x32_bf16 a[212:215],a[148:151],v[172:175],a[212:215]
v_mfma_f32_16x16x32_bf16 a[212:215],a[144:147],v[168:171],a[212:215]
v_mfma_f32_16x16x32_bf16 a[216:219],a[144:147],v[200:203],a[216:219]
ds_read_b64_tr_b16 a[144:145],v44 offset:128
ds_read_b64_tr_b16 a[146:147],v44 offset:5248
v_mfma_f32_16x16x32_bf16 a[216:219],a[148:151],v[204:207],a[216:219]
v_accvgpr_read_b32 v130,a208
v_accvgpr_read_b32 v131,a209
v_mfma_f32_16x16x32_bf16 a[216:219],a[152:155],v[216:219],a[216:219]
v_accvgpr_read_b32 v14,a210
v_accvgpr_read_b32 v15,a211
v_mfma_f32_16x16x32_bf16 a[216:219],a[156:159],v[220:223],a[216:219]
v_cvt_pk_bf16_f32 v130,v130,v131
v_cvt_pk_bf16_f32 v131,v14,v15
v_mfma_f32_16x16x32_bf16 a[224:227],a[160:163],v[64:67],a[224:227]
buffer_store_dwordx4 v[128:131],v6,s[32:35],s84 offen sc1
v_mfma_f32_16x16x32_bf16 a[224:227],a[164:167],v[68:71],a[224:227]
s_waitcnt lgkmcnt(2)
v_accvgpr_read_b32 v134,a212
v_accvgpr_read_b32 v135,a213
v_mfma_f32_16x16x32_bf16 a[224:227],a[168:171],v[72:75],a[224:227]
v_accvgpr_read_b32 v16,a214
v_accvgpr_read_b32 v17,a215
v_mfma_f32_16x16x32_bf16 a[224:227],a[172:175],v[76:79],a[224:227]
v_cvt_pk_bf16_f32 v134,v134,v135
v_cvt_pk_bf16_f32 v135,v16,v17
v_mfma_f32_16x16x32_bf16 a[228:231],a[172:175],v[92:95],a[228:231]
buffer_store_dwordx4 v[132:135],v6,s[32:35],s85 offen sc1
v_mfma_f32_16x16x32_bf16 a[228:231],a[168:171],v[88:91],a[228:231]
v_accvgpr_read_b32 v146,a216
v_accvgpr_read_b32 v147,a217
v_mfma_f32_16x16x32_bf16 a[228:231],a[164:167],v[84:87],a[228:231]
v_accvgpr_read_b32 v12,a218
v_accvgpr_read_b32 v13,a219
v_mfma_f32_16x16x32_bf16 a[228:231],a[160:163],v[80:83],a[228:231]
v_cvt_pk_bf16_f32 v146,v146,v147
v_cvt_pk_bf16_f32 v147,v12,v13
v_mfma_f32_16x16x32_bf16 a[232:235],a[160:163],v[96:99],a[232:235]
buffer_store_dwordx4 v[144:147],v6,s[32:35],s86 offen sc1
v_mfma_f32_16x16x32_bf16 a[232:235],a[164:167],v[100:103],a[232:235]
ds_read_b64_tr_b16 a[148:149],v44 offset:10368
ds_read_b64_tr_b16 a[150:151],v44 offset:15488
v_mfma_f32_16x16x32_bf16 a[232:235],a[168:171],v[104:107],a[232:235]
ds_read_b64_tr_b16 a[152:153],v44 offset:20608
v_mfma_f32_16x16x32_bf16 a[232:235],a[172:175],v[108:111],a[232:235]
ds_read_b64_tr_b16 a[154:155],v44 offset:25728
v_mfma_f32_16x16x32_bf16 a[224:227],a[176:179],v[136:139],a[224:227]
ds_read_b64_tr_b16 a[156:157],v44 offset:30848
v_mfma_f32_16x16x32_bf16 a[224:227],a[180:183],v[140:143],a[224:227]
ds_read_b64_tr_b16 a[158:159],v44 offset:35968
ds_read_b64_tr_b16 a[160:161],v43 offset:160
v_mfma_f32_16x16x32_bf16 a[224:227],a[184:187],v[152:155],a[224:227]
ds_read_b64_tr_b16 a[162:163],v43 offset:5280
v_mfma_f32_16x16x32_bf16 a[224:227],a[188:191],v[156:159],a[224:227]
ds_read_b64_tr_b16 a[164:165],v43 offset:10400
v_mfma_f32_16x16x32_bf16 a[228:231],a[188:191],v[188:191],a[228:231]
ds_read_b64_tr_b16 a[166:167],v43 offset:15520
v_mfma_f32_16x16x32_bf16 a[228:231],a[184:187],v[184:187],a[228:231]
ds_read_b64_tr_b16 a[168:169],v43 offset:20640
ds_read_b64_tr_b16 a[170:171],v43 offset:25760
v_mfma_f32_16x16x32_bf16 a[228:231],a[180:183],v[172:175],a[228:231]
ds_read_b64_tr_b16 a[172:173],v43 offset:30880
v_mfma_f32_16x16x32_bf16 a[228:231],a[176:179],v[168:171],a[228:231]
ds_read_b64_tr_b16 a[174:175],v43 offset:36000
v_mfma_f32_16x16x32_bf16 a[232:235],a[176:179],v[200:203],a[232:235]
ds_read_b64_tr_b16 a[176:177],v45 offset:160
v_mfma_f32_16x16x32_bf16 a[232:235],a[180:183],v[204:207],a[232:235]
v_accvgpr_read_b32 v160,a224
v_accvgpr_read_b32 v161,a225
v_mfma_f32_16x16x32_bf16 a[232:235],a[184:187],v[216:219],a[232:235]
v_cvt_pk_bf16_f32 v160,v160,v161
v_accvgpr_read_b32 v162,a226
v_mfma_f32_16x16x32_bf16 a[232:235],a[188:191],v[220:223],a[232:235]
v_accvgpr_read_b32 v163,a227
v_cvt_pk_bf16_f32 v161,v162,v163
v_mfma_f32_16x16x32_bf16 a[240:243],a[96:99],v[64:67],a[240:243]
ds_read_b64_tr_b16 a[178:179],v45 offset:5280
ds_read_b64_tr_b16 a[180:181],v45 offset:10400
v_mfma_f32_16x16x32_bf16 a[240:243],a[100:103],v[68:71],a[240:243]
v_accvgpr_read_b32 v164,a228
v_accvgpr_read_b32 v165,a229
v_mfma_f32_16x16x32_bf16 a[240:243],a[104:107],v[72:75],a[240:243]
v_cvt_pk_bf16_f32 v164,v164,v165
v_accvgpr_read_b32 v166,a230
v_mfma_f32_16x16x32_bf16 a[240:243],a[108:111],v[76:79],a[240:243]
v_accvgpr_read_b32 v167,a231
v_cvt_pk_bf16_f32 v165,v166,v167
v_mfma_f32_16x16x32_bf16 a[244:247],a[108:111],v[92:95],a[244:247]
ds_read_b64_tr_b16 a[182:183],v45 offset:15520
ds_read_b64_tr_b16 a[184:185],v45 offset:20640
v_mfma_f32_16x16x32_bf16 a[244:247],a[104:107],v[88:91],a[244:247]
ds_read_b64_tr_b16 a[186:187],v45 offset:25760
v_accvgpr_read_b32 v176,a232
v_mfma_f32_16x16x32_bf16 a[244:247],a[100:103],v[84:87],a[244:247]
ds_read_b64_tr_b16 a[188:189],v45 offset:30880
v_accvgpr_read_b32 v177,a233
v_mfma_f32_16x16x32_bf16 a[244:247],a[96:99],v[80:83],a[244:247]
ds_read_b64_tr_b16 a[190:191],v45 offset:36000
v_cvt_pk_bf16_f32 v176,v176,v177
v_mfma_f32_16x16x32_bf16 a[248:251],a[96:99],v[96:99],a[248:251]
v_accvgpr_read_b32 v178,a234
v_accvgpr_read_b32 v179,a235
v_mfma_f32_16x16x32_bf16 a[248:251],a[100:103],v[100:103],a[248:251]
ds_read_b64_tr_b16 a[96:97],v42 offset:192
v_cvt_pk_bf16_f32 v177,v178,v179
v_mfma_f32_16x16x32_bf16 a[248:251],a[104:107],v[104:107],a[248:251]
ds_read_b64_tr_b16 a[98:99],v42 offset:5312
ds_read_b64_tr_b16 a[100:101],v42 offset:10432
v_mfma_f32_16x16x32_bf16 a[248:251],a[108:111],v[108:111],a[248:251]
ds_read_b64_tr_b16 a[102:103],v42 offset:15552
v_mfma_f32_16x16x32_bf16 a[240:243],a[112:115],v[136:139],a[240:243]
ds_read_b64_tr_b16 a[104:105],v42 offset:20672
v_mfma_f32_16x16x32_bf16 a[240:243],a[116:119],v[140:143],a[240:243]
ds_read_b64_tr_b16 a[106:107],v42 offset:25792
v_mfma_f32_16x16x32_bf16 a[240:243],a[120:123],v[152:155],a[240:243]
ds_read_b64_tr_b16 a[108:109],v42 offset:30912
ds_read_b64_tr_b16 a[110:111],v42 offset:36032
v_mfma_f32_16x16x32_bf16 a[240:243],a[124:127],v[156:159],a[240:243]
v_mfma_f32_16x16x32_bf16 a[244:247],a[124:127],v[188:191],a[244:247]
v_mfma_f32_16x16x32_bf16 a[244:247],a[120:123],v[184:187],a[244:247]
v_mfma_f32_16x16x32_bf16 a[244:247],a[116:119],v[172:175],a[244:247]
v_mfma_f32_16x16x32_bf16 a[244:247],a[112:115],v[168:171],a[244:247]
v_mfma_f32_16x16x32_bf16 a[248:251],a[112:115],v[200:203],a[248:251]
ds_read_b64_tr_b16 a[112:113],v44 offset:192
ds_read_b64_tr_b16 a[114:115],v44 offset:5312
v_mfma_f32_16x16x32_bf16 a[248:251],a[116:119],v[204:207],a[248:251]
v_accvgpr_read_b32 v162,a240
v_accvgpr_read_b32 v163,a241
v_mfma_f32_16x16x32_bf16 a[248:251],a[120:123],v[216:219],a[248:251]
v_accvgpr_read_b32 v14,a242
v_accvgpr_read_b32 v15,a243
v_mfma_f32_16x16x32_bf16 a[248:251],a[124:127],v[220:223],a[248:251]
v_cvt_pk_bf16_f32 v162,v162,v163
v_cvt_pk_bf16_f32 v163,v14,v15
v_mfma_f32_16x16x32_bf16 a[0:3],a[128:131],v[64:67],a[0:3]
buffer_store_dwordx4 v[160:163],v6,s[32:35],s84 offen offset:1024 sc1
v_mfma_f32_16x16x32_bf16 a[0:3],a[132:135],v[68:71],a[0:3]
v_accvgpr_read_b32 v166,a244
v_accvgpr_read_b32 v167,a245
v_mfma_f32_16x16x32_bf16 a[0:3],a[136:139],v[72:75],a[0:3]
v_accvgpr_read_b32 v16,a246
v_accvgpr_read_b32 v17,a247
v_mfma_f32_16x16x32_bf16 a[0:3],a[140:143],v[76:79],a[0:3]
v_cvt_pk_bf16_f32 v166,v166,v167
v_cvt_pk_bf16_f32 v167,v16,v17
v_mfma_f32_16x16x32_bf16 a[4:7],a[140:143],v[92:95],a[4:7]
buffer_store_dwordx4 v[164:167],v6,s[32:35],s85 offen offset:1024 sc1
v_mfma_f32_16x16x32_bf16 a[4:7],a[136:139],v[88:91],a[4:7]
v_accvgpr_read_b32 v178,a248
v_accvgpr_read_b32 v179,a249
v_mfma_f32_16x16x32_bf16 a[4:7],a[132:135],v[84:87],a[4:7]
v_accvgpr_read_b32 v12,a250
v_accvgpr_read_b32 v13,a251
v_mfma_f32_16x16x32_bf16 a[4:7],a[128:131],v[80:83],a[4:7]
v_cvt_pk_bf16_f32 v178,v178,v179
v_cvt_pk_bf16_f32 v179,v12,v13
v_mfma_f32_16x16x32_bf16 a[8:11],a[128:131],v[96:99],a[8:11]
buffer_store_dwordx4 v[176:179],v6,s[32:35],s86 offen offset:1024 sc1
v_mfma_f32_16x16x32_bf16 a[8:11],a[132:135],v[100:103],a[8:11]
ds_read_b64_tr_b16 a[116:117],v44 offset:10432
ds_read_b64_tr_b16 a[118:119],v44 offset:15552
v_mfma_f32_16x16x32_bf16 a[8:11],a[136:139],v[104:107],a[8:11]
ds_read_b64_tr_b16 a[120:121],v44 offset:20672
v_mfma_f32_16x16x32_bf16 a[8:11],a[140:143],v[108:111],a[8:11]
s_waitcnt lgkmcnt(3)
ds_read_b64_tr_b16 a[122:123],v44 offset:25792
v_mfma_f32_16x16x32_bf16 a[0:3],a[144:147],v[136:139],a[0:3]
)A"
R"A(ds_read_b64_tr_b16 a[124:125],v44 offset:30912
v_mfma_f32_16x16x32_bf16 a[0:3],a[148:151],v[140:143],a[0:3]
ds_read_b64_tr_b16 a[126:127],v44 offset:36032
ds_read_b64_tr_b16 a[128:129],v43 offset:224
v_mfma_f32_16x16x32_bf16 a[0:3],a[152:155],v[152:155],a[0:3]
ds_read_b64_tr_b16 a[130:131],v43 offset:5344
v_mfma_f32_16x16x32_bf16 a[0:3],a[156:159],v[156:159],a[0:3]
ds_read_b64_tr_b16 a[132:133],v43 offset:10464
v_mfma_f32_16x16x32_bf16 a[4:7],a[156:159],v[188:191],a[4:7]
ds_read_b64_tr_b16 a[134:135],v43 offset:15584
v_mfma_f32_16x16x32_bf16 a[4:7],a[152:155],v[184:187],a[4:7]
ds_read_b64_tr_b16 a[136:137],v43 offset:20704
ds_read_b64_tr_b16 a[138:139],v43 offset:25824
v_mfma_f32_16x16x32_bf16 a[4:7],a[148:151],v[172:175],a[4:7]
ds_read_b64_tr_b16 a[140:141],v43 offset:30944
v_mfma_f32_16x16x32_bf16 a[4:7],a[144:147],v[168:171],a[4:7]
ds_read_b64_tr_b16 a[142:143],v43 offset:36064
v_mfma_f32_16x16x32_bf16 a[8:11],a[144:147],v[200:203],a[8:11]
ds_read_b64_tr_b16 a[144:145],v45 offset:224
v_mfma_f32_16x16x32_bf16 a[8:11],a[148:151],v[204:207],a[8:11]
v_accvgpr_read_b32 v192,a0
v_accvgpr_read_b32 v193,a1
v_mfma_f32_16x16x32_bf16 a[8:11],a[152:155],v[216:219],a[8:11]
v_cvt_pk_bf16_f32 v192,v192,v193
v_accvgpr_read_b32 v194,a2
v_mfma_f32_16x16x32_bf16 a[8:11],a[156:159],v[220:223],a[8:11]
v_accvgpr_read_b32 v195,a3
v_cvt_pk_bf16_f32 v193,v194,v195
v_mfma_f32_16x16x32_bf16 a[16:19],a[160:163],v[64:67],a[16:19]
ds_read_b64_tr_b16 a[146:147],v45 offset:5344
ds_read_b64_tr_b16 a[148:149],v45 offset:10464
v_mfma_f32_16x16x32_bf16 a[16:19],a[164:167],v[68:71],a[16:19]
v_accvgpr_read_b32 v196,a4
v_accvgpr_read_b32 v197,a5
v_mfma_f32_16x16x32_bf16 a[16:19],a[168:171],v[72:75],a[16:19]
v_cvt_pk_bf16_f32 v196,v196,v197
v_accvgpr_read_b32 v198,a6
v_mfma_f32_16x16x32_bf16 a[16:19],a[172:175],v[76:79],a[16:19]
v_accvgpr_read_b32 v199,a7
v_cvt_pk_bf16_f32 v197,v198,v199
v_mfma_f32_16x16x32_bf16 a[20:23],a[172:175],v[92:95],a[20:23]
ds_read_b64_tr_b16 a[150:151],v45 offset:15584
ds_read_b64_tr_b16 a[152:153],v45 offset:20704
v_mfma_f32_16x16x32_bf16 a[20:23],a[168:171],v[88:91],a[20:23]
ds_read_b64_tr_b16 a[154:155],v45 offset:25824
v_accvgpr_read_b32 v208,a8
v_mfma_f32_16x16x32_bf16 a[20:23],a[164:167],v[84:87],a[20:23]
ds_read_b64_tr_b16 a[156:157],v45 offset:30944
v_accvgpr_read_b32 v209,a9
v_mfma_f32_16x16x32_bf16 a[20:23],a[160:163],v[80:83],a[20:23]
ds_read_b64_tr_b16 a[158:159],v45 offset:36064
v_cvt_pk_bf16_f32 v208,v208,v209
v_mfma_f32_16x16x32_bf16 a[24:27],a[160:163],v[96:99],a[24:27]
v_accvgpr_read_b32 v210,a10
v_accvgpr_read_b32 v211,a11
v_mfma_f32_16x16x32_bf16 a[24:27],a[164:167],v[100:103],a[24:27]
ds_read_b64_tr_b16 a[160:161],v42 offset:256
v_cvt_pk_bf16_f32 v209,v210,v211
v_mfma_f32_16x16x32_bf16 a[24:27],a[168:171],v[104:107],a[24:27]
ds_read_b64_tr_b16 a[162:163],v42 offset:5376
ds_read_b64_tr_b16 a[164:165],v42 offset:10496
v_mfma_f32_16x16x32_bf16 a[24:27],a[172:175],v[108:111],a[24:27]
ds_read_b64_tr_b16 a[166:167],v42 offset:15616
v_mfma_f32_16x16x32_bf16 a[16:19],a[176:179],v[136:139],a[16:19]
ds_read_b64_tr_b16 a[168:169],v42 offset:20736
v_mfma_f32_16x16x32_bf16 a[16:19],a[180:183],v[140:143],a[16:19]
ds_read_b64_tr_b16 a[170:171],v42 offset:25856
v_mfma_f32_16x16x32_bf16 a[16:19],a[184:187],v[152:155],a[16:19]
ds_read_b64_tr_b16 a[172:173],v42 offset:30976
ds_read_b64_tr_b16 a[174:175],v42 offset:36096
v_mfma_f32_16x16x32_bf16 a[16:19],a[188:191],v[156:159],a[16:19]
v_mfma_f32_16x16x32_bf16 a[20:23],a[188:191],v[188:191],a[20:23]
v_mfma_f32_16x16x32_bf16 a[20:23],a[184:187],v[184:187],a[20:23]
v_mfma_f32_16x16x32_bf16 a[20:23],a[180:183],v[172:175],a[20:23]
v_mfma_f32_16x16x32_bf16 a[20:23],a[176:179],v[168:171],a[20:23]
v_mfma_f32_16x16x32_bf16 a[24:27],a[176:179],v[200:203],a[24:27]
ds_read_b64_tr_b16 a[176:177],v44 offset:256
ds_read_b64_tr_b16 a[178:179],v44 offset:5376
v_mfma_f32_16x16x32_bf16 a[24:27],a[180:183],v[204:207],a[24:27]
v_accvgpr_read_b32 v194,a16
v_accvgpr_read_b32 v195,a17
v_mfma_f32_16x16x32_bf16 a[24:27],a[184:187],v[216:219],a[24:27]
v_accvgpr_read_b32 v14,a18
v_accvgpr_read_b32 v15,a19
v_mfma_f32_16x16x32_bf16 a[24:27],a[188:191],v[220:223],a[24:27]
v_cvt_pk_bf16_f32 v194,v194,v195
v_cvt_pk_bf16_f32 v195,v14,v15
v_mfma_f32_16x16x32_bf16 a[32:35],a[96:99],v[64:67],a[32:35]
buffer_store_dwordx4 v[192:195],v6,s[32:35],s84 offen offset:2048 sc1
v_mfma_f32_16x16x32_bf16 a[32:35],a[100:103],v[68:71],a[32:35]
v_accvgpr_read_b32 v198,a20
v_accvgpr_read_b32 v199,a21
v_mfma_f32_16x16x32_bf16 a[32:35],a[104:107],v[72:75],a[32:35]
v_accvgpr_read_b32 v16,a22
v_accvgpr_read_b32 v17,a23
v_mfma_f32_16x16x32_bf16 a[32:35],a[108:111],v[76:79],a[32:35]
v_cvt_pk_bf16_f32 v198,v198,v199
v_cvt_pk_bf16_f32 v199,v16,v17
v_mfma_f32_16x16x32_bf16 a[36:39],a[108:111],v[92:95],a[36:39]
buffer_store_dwordx4 v[196:199],v6,s[32:35],s85 offen offset:2048 sc1
v_mfma_f32_16x16x32_bf16 a[36:39],a[104:107],v[88:91],a[36:39]
v_accvgpr_read_b32 v210,a24
v_accvgpr_read_b32 v211,a25
v_mfma_f32_16x16x32_bf16 a[36:39],a[100:103],v[84:87],a[36:39]
v_accvgpr_read_b32 v12,a26
v_accvgpr_read_b32 v13,a27
v_mfma_f32_16x16x32_bf16 a[36:39],a[96:99],v[80:83],a[36:39]
v_cvt_pk_bf16_f32 v210,v210,v211
v_cvt_pk_bf16_f32 v211,v12,v13
v_mfma_f32_16x16x32_bf16 a[40:43],a[96:99],v[96:99],a[40:43]
buffer_store_dwordx4 v[208:211],v6,s[32:35],s86 offen offset:2048 sc1
v_mfma_f32_16x16x32_bf16 a[40:43],a[100:103],v[100:103],a[40:43]
ds_read_b64_tr_b16 a[180:181],v44 offset:10496
ds_read_b64_tr_b16 a[182:183],v44 offset:15616
v_mfma_f32_16x16x32_bf16 a[40:43],a[104:107],v[104:107],a[40:43]
ds_read_b64_tr_b16 a[184:185],v44 offset:20736
v_mfma_f32_16x16x32_bf16 a[40:43],a[108:111],v[108:111],a[40:43]
ds_read_b64_tr_b16 a[186:187],v44 offset:25856
v_mfma_f32_16x16x32_bf16 a[32:35],a[112:115],v[136:139],a[32:35]
s_waitcnt lgkmcnt(4)
ds_read_b64_tr_b16 a[188:189],v44 offset:30976
v_mfma_f32_16x16x32_bf16 a[32:35],a[116:119],v[140:143],a[32:35]
ds_read_b64_tr_b16 a[190:191],v44 offset:36096
ds_read_b64_tr_b16 a[96:97],v43 offset:288
v_mfma_f32_16x16x32_bf16 a[32:35],a[120:123],v[152:155],a[32:35]
ds_read_b64_tr_b16 a[98:99],v43 offset:5408
v_mfma_f32_16x16x32_bf16 a[32:35],a[124:127],v[156:159],a[32:35]
ds_read_b64_tr_b16 a[100:101],v43 offset:10528
v_mfma_f32_16x16x32_bf16 a[36:39],a[124:127],v[188:191],a[36:39]
ds_read_b64_tr_b16 a[102:103],v43 offset:15648
v_mfma_f32_16x16x32_bf16 a[36:39],a[120:123],v[184:187],a[36:39]
ds_read_b64_tr_b16 a[104:105],v43 offset:20768
ds_read_b64_tr_b16 a[106:107],v43 offset:25888
v_mfma_f32_16x16x32_bf16 a[36:39],a[116:119],v[172:175],a[36:39]
ds_read_b64_tr_b16 a[108:109],v43 offset:31008
v_mfma_f32_16x16x32_bf16 a[36:39],a[112:115],v[168:171],a[36:39]
ds_read_b64_tr_b16 a[110:111],v43 offset:36128
v_mfma_f32_16x16x32_bf16 a[40:43],a[112:115],v[200:203],a[40:43]
ds_read_b64_tr_b16 a[112:113],v45 offset:288
v_mfma_f32_16x16x32_bf16 a[40:43],a[116:119],v[204:207],a[40:43]
v_accvgpr_read_b32 v224,a32
v_accvgpr_read_b32 v225,a33
v_mfma_f32_16x16x32_bf16 a[40:43],a[120:123],v[216:219],a[40:43]
v_cvt_pk_bf16_f32 v224,v224,v225
v_accvgpr_read_b32 v226,a34
v_mfma_f32_16x16x32_bf16 a[40:43],a[124:127],v[220:223],a[40:43]
v_accvgpr_read_b32 v227,a35
v_cvt_pk_bf16_f32 v225,v226,v227
v_mfma_f32_16x16x32_bf16 a[48:51],a[128:131],v[64:67],a[48:51]
ds_read_b64_tr_b16 a[114:115],v45 offset:5408
ds_read_b64_tr_b16 a[116:117],v45 offset:10528
v_mfma_f32_16x16x32_bf16 a[48:51],a[132:135],v[68:71],a[48:51]
v_accvgpr_read_b32 v228,a36
v_accvgpr_read_b32 v229,a37
v_mfma_f32_16x16x32_bf16 a[48:51],a[136:139],v[72:75],a[48:51]
v_cvt_pk_bf16_f32 v228,v228,v229
v_accvgpr_read_b32 v230,a38
v_mfma_f32_16x16x32_bf16 a[48:51],a[140:143],v[76:79],a[48:51]
v_accvgpr_read_b32 v231,a39
v_cvt_pk_bf16_f32 v229,v230,v231
v_mfma_f32_16x16x32_bf16 a[52:55],a[140:143],v[92:95],a[52:55]
ds_read_b64_tr_b16 a[118:119],v45 offset:15648
ds_read_b64_tr_b16 a[120:121],v45 offset:20768
v_mfma_f32_16x16x32_bf16 a[52:55],a[136:139],v[88:91],a[52:55]
v_accvgpr_read_b32 v240,a40
v_accvgpr_read_b32 v241,a41
v_mfma_f32_16x16x32_bf16 a[52:55],a[132:135],v[84:87],a[52:55]
v_cvt_pk_bf16_f32 v240,v240,v241
v_accvgpr_read_b32 v242,a42
v_mfma_f32_16x16x32_bf16 a[52:55],a[128:131],v[80:83],a[52:55]
v_accvgpr_read_b32 v243,a43
v_cvt_pk_bf16_f32 v241,v242,v243
v_mfma_f32_16x16x32_bf16 a[56:59],a[128:131],v[96:99],a[56:59]
ds_read_b64_tr_b16 a[122:123],v45 offset:25888
ds_read_b64_tr_b16 a[124:125],v45 offset:31008
v_mfma_f32_16x16x32_bf16 a[56:59],a[132:135],v[100:103],a[56:59]
ds_read_b64_tr_b16 a[126:127],v45 offset:36128
v_mfma_f32_16x16x32_bf16 a[56:59],a[136:139],v[104:107],a[56:59]
ds_read_b64_tr_b16 a[128:129],v1
v_mfma_f32_16x16x32_bf16 a[56:59],a[140:143],v[108:111],a[56:59]
ds_read_b64_tr_b16 a[130:131],v1 offset:5120
v_mfma_f32_16x16x32_bf16 a[48:51],a[144:147],v[136:139],a[48:51]
ds_read_b64_tr_b16 a[132:133],v1 offset:10240
ds_read_b64_tr_b16 a[134:135],v1 offset:15360
v_mfma_f32_16x16x32_bf16 a[48:51],a[148:151],v[140:143],a[48:51]
ds_read_b64_tr_b16 a[136:137],v1 offset:20480
v_mfma_f32_16x16x32_bf16 a[48:51],a[152:155],v[152:155],a[48:51]
ds_read_b64_tr_b16 a[138:139],v1 offset:25600
v_mfma_f32_16x16x32_bf16 a[48:51],a[156:159],v[156:159],a[48:51]
ds_read_b64_tr_b16 a[140:141],v1 offset:30720
v_mfma_f32_16x16x32_bf16 a[52:55],a[156:159],v[188:191],a[52:55]
ds_read_b64_tr_b16 a[142:143],v1 offset:35840
v_mfma_f32_16x16x32_bf16 a[52:55],a[152:155],v[184:187],a[52:55]
v_mfma_f32_16x16x32_bf16 a[52:55],a[148:151],v[172:175],a[52:55]
v_mfma_f32_16x16x32_bf16 a[52:55],a[144:147],v[168:171],a[52:55]
v_mfma_f32_16x16x32_bf16 a[56:59],a[144:147],v[200:203],a[56:59]
ds_read_b64_tr_b16 a[144:145],v42
ds_read_b64_tr_b16 a[146:147],v42 offset:5120
v_mfma_f32_16x16x32_bf16 a[56:59],a[148:151],v[204:207],a[56:59]
v_accvgpr_read_b32 v226,a48
v_accvgpr_read_b32 v227,a49
v_mfma_f32_16x16x32_bf16 a[56:59],a[152:155],v[216:219],a[56:59]
v_accvgpr_read_b32 v14,a50
v_accvgpr_read_b32 v15,a51
v_mfma_f32_16x16x32_bf16 a[56:59],a[156:159],v[220:223],a[56:59]
v_cvt_pk_bf16_f32 v226,v226,v227
v_cvt_pk_bf16_f32 v227,v14,v15
v_mfma_f32_16x16x32_bf16 a[64:67],a[160:163],v[64:67],a[64:67]
buffer_store_dwordx4 v[224:227],v6,s[32:35],s84 offen offset:3072 sc1
v_mfma_f32_16x16x32_bf16 a[64:67],a[164:167],v[68:71],a[64:67]
v_accvgpr_read_b32 v230,a52
v_accvgpr_read_b32 v231,a53
v_mfma_f32_16x16x32_bf16 a[64:67],a[168:171],v[72:75],a[64:67]
v_accvgpr_read_b32 v16,a54
v_accvgpr_read_b32 v17,a55
v_mfma_f32_16x16x32_bf16 a[64:67],a[172:175],v[76:79],a[64:67]
v_cvt_pk_bf16_f32 v230,v230,v231
v_cvt_pk_bf16_f32 v231,v16,v17
v_mfma_f32_16x16x32_bf16 a[68:71],a[172:175],v[92:95],a[68:71]
buffer_store_dwordx4 v[228:231],v6,s[32:35],s85 offen offset:3072 sc1
v_mfma_f32_16x16x32_bf16 a[68:71],a[168:171],v[88:91],a[68:71]
v_accvgpr_read_b32 v242,a56
v_accvgpr_read_b32 v243,a57
v_mfma_f32_16x16x32_bf16 a[68:71],a[164:167],v[84:87],a[68:71]
v_accvgpr_read_b32 v12,a58
v_accvgpr_read_b32 v13,a59
v_mfma_f32_16x16x32_bf16 a[68:71],a[160:163],v[80:83],a[68:71]
v_cvt_pk_bf16_f32 v242,v242,v243
v_cvt_pk_bf16_f32 v243,v12,v13
v_mfma_f32_16x16x32_bf16 a[72:75],a[160:163],v[96:99],a[72:75]
buffer_store_dwordx4 v[240:243],v6,s[32:35],s86 offen offset:3072 sc1
v_mfma_f32_16x16x32_bf16 a[72:75],a[164:167],v[100:103],a[72:75]
ds_read_b64_tr_b16 a[148:149],v42 offset:10240
ds_read_b64_tr_b16 a[150:151],v42 offset:15360
v_mfma_f32_16x16x32_bf16 a[72:75],a[168:171],v[104:107],a[72:75]
ds_read_b64_tr_b16 a[152:153],v42 offset:20480
v_mfma_f32_16x16x32_bf16 a[72:75],a[172:175],v[108:111],a[72:75]
ds_read_b64_tr_b16 a[154:155],v42 offset:25600
v_mfma_f32_16x16x32_bf16 a[64:67],a[176:179],v[136:139],a[64:67]
s_waitcnt lgkmcnt(4)
ds_read_b64_tr_b16 a[156:157],v42 offset:30720
v_mfma_f32_16x16x32_bf16 a[64:67],a[180:183],v[140:143],a[64:67]
ds_read_b64_tr_b16 a[158:159],v42 offset:35840
ds_read_b64_tr_b16 a[160:161],v44
v_mfma_f32_16x16x32_bf16 a[64:67],a[184:187],v[152:155],a[64:67]
ds_read_b64_tr_b16 a[162:163],v44 offset:5120
v_mfma_f32_16x16x32_bf16 a[64:67],a[188:191],v[156:159],a[64:67]
)A"
R"A(ds_read_b64_tr_b16 a[164:165],v44 offset:10240
v_mfma_f32_16x16x32_bf16 a[68:71],a[188:191],v[188:191],a[68:71]
ds_read_b64_tr_b16 a[166:167],v44 offset:15360
v_mfma_f32_16x16x32_bf16 a[68:71],a[184:187],v[184:187],a[68:71]
ds_read_b64_tr_b16 a[168:169],v44 offset:20480
ds_read_b64_tr_b16 a[170:171],v44 offset:25600
v_mfma_f32_16x16x32_bf16 a[68:71],a[180:183],v[172:175],a[68:71]
ds_read_b64_tr_b16 a[172:173],v44 offset:30720
v_mfma_f32_16x16x32_bf16 a[68:71],a[176:179],v[168:171],a[68:71]
ds_read_b64_tr_b16 a[174:175],v44 offset:35840
v_mfma_f32_16x16x32_bf16 a[72:75],a[176:179],v[200:203],a[72:75]
ds_read_b64_tr_b16 a[176:177],v2 offset:32
v_mfma_f32_16x16x32_bf16 a[72:75],a[180:183],v[204:207],a[72:75]
v_accvgpr_read_b32 v128,a64
v_accvgpr_read_b32 v129,a65
v_mfma_f32_16x16x32_bf16 a[72:75],a[184:187],v[216:219],a[72:75]
v_cvt_pk_bf16_f32 v128,v128,v129
v_accvgpr_read_b32 v130,a66
v_mfma_f32_16x16x32_bf16 a[72:75],a[188:191],v[220:223],a[72:75]
v_accvgpr_read_b32 v131,a67
v_cvt_pk_bf16_f32 v129,v130,v131
v_mfma_f32_16x16x32_bf16 a[80:83],a[96:99],v[64:67],a[80:83]
ds_read_b64_tr_b16 a[178:179],v2 offset:5152
ds_read_b64_tr_b16 a[180:181],v2 offset:10272
v_mfma_f32_16x16x32_bf16 a[80:83],a[100:103],v[68:71],a[80:83]
v_accvgpr_read_b32 v132,a68
v_accvgpr_read_b32 v133,a69
v_mfma_f32_16x16x32_bf16 a[80:83],a[104:107],v[72:75],a[80:83]
v_cvt_pk_bf16_f32 v132,v132,v133
v_accvgpr_read_b32 v134,a70
v_mfma_f32_16x16x32_bf16 a[80:83],a[108:111],v[76:79],a[80:83]
ds_read_b64_tr_b16 a[182:183],v2 offset:15392
v_accvgpr_read_b32 v135,a71
v_mfma_f32_16x16x32_bf16 a[84:87],a[108:111],v[92:95],a[84:87]
ds_read_b64_tr_b16 a[184:185],v2 offset:20512
v_cvt_pk_bf16_f32 v133,v134,v135
v_mfma_f32_16x16x32_bf16 a[84:87],a[104:107],v[88:91],a[84:87]
v_accvgpr_read_b32 v144,a72
v_accvgpr_read_b32 v145,a73
v_mfma_f32_16x16x32_bf16 a[84:87],a[100:103],v[84:87],a[84:87]
v_cvt_pk_bf16_f32 v144,v144,v145
v_accvgpr_read_b32 v146,a74
v_mfma_f32_16x16x32_bf16 a[84:87],a[96:99],v[80:83],a[84:87]
v_accvgpr_read_b32 v147,a75
v_cvt_pk_bf16_f32 v145,v146,v147
v_mfma_f32_16x16x32_bf16 a[88:91],a[96:99],v[96:99],a[88:91]
ds_read_b64_tr_b16 a[186:187],v2 offset:25632
ds_read_b64_tr_b16 a[188:189],v2 offset:30752
v_mfma_f32_16x16x32_bf16 a[88:91],a[100:103],v[100:103],a[88:91]
ds_read_b64_tr_b16 a[190:191],v2 offset:35872
v_mfma_f32_16x16x32_bf16 a[88:91],a[104:107],v[104:107],a[88:91]
ds_read_b64_tr_b16 a[96:97],v43 offset:32
v_mfma_f32_16x16x32_bf16 a[88:91],a[108:111],v[108:111],a[88:91]
ds_read_b64_tr_b16 a[98:99],v43 offset:5152
v_mfma_f32_16x16x32_bf16 a[80:83],a[112:115],v[136:139],a[80:83]
ds_read_b64_tr_b16 a[100:101],v43 offset:10272
ds_read_b64_tr_b16 a[102:103],v43 offset:15392
v_mfma_f32_16x16x32_bf16 a[80:83],a[116:119],v[140:143],a[80:83]
ds_read_b64_tr_b16 a[104:105],v43 offset:20512
v_mfma_f32_16x16x32_bf16 a[80:83],a[120:123],v[152:155],a[80:83]
ds_read_b64_tr_b16 a[106:107],v43 offset:25632
v_mfma_f32_16x16x32_bf16 a[80:83],a[124:127],v[156:159],a[80:83]
ds_read_b64_tr_b16 a[108:109],v43 offset:30752
v_mfma_f32_16x16x32_bf16 a[84:87],a[124:127],v[188:191],a[84:87]
ds_read_b64_tr_b16 a[110:111],v43 offset:35872
v_mfma_f32_16x16x32_bf16 a[84:87],a[120:123],v[184:187],a[84:87]
v_mfma_f32_16x16x32_bf16 a[84:87],a[116:119],v[172:175],a[84:87]
v_mfma_f32_16x16x32_bf16 a[84:87],a[112:115],v[168:171],a[84:87]
v_mfma_f32_16x16x32_bf16 a[88:91],a[112:115],v[200:203],a[88:91]
ds_read_b64_tr_b16 a[112:113],v45 offset:32
ds_read_b64_tr_b16 a[114:115],v45 offset:5152
v_mfma_f32_16x16x32_bf16 a[88:91],a[116:119],v[204:207],a[88:91]
v_accvgpr_read_b32 v130,a80
v_accvgpr_read_b32 v131,a81
v_mfma_f32_16x16x32_bf16 a[88:91],a[120:123],v[216:219],a[88:91]
v_accvgpr_read_b32 v14,a82
v_accvgpr_read_b32 v15,a83
v_mfma_f32_16x16x32_bf16 a[88:91],a[124:127],v[220:223],a[88:91]
v_cvt_pk_bf16_f32 v130,v130,v131
v_cvt_pk_bf16_f32 v131,v14,v15
v_mfma_f32_16x16x32_bf16 a[204:207],a[128:131],v[148:151],a[204:207]
s_lshl_b32 s94,s71,2
buffer_store_dwordx4 v[128:131],v25,s[32:35],s84 offen sc1
v_mfma_f32_16x16x32_bf16 a[204:207],a[132:135],v[180:183],a[204:207]
s_add_u32 s94,s94,s40
v_accvgpr_read_b32 v134,a84
v_accvgpr_read_b32 v135,a85
v_mfma_f32_16x16x32_bf16 a[204:207],a[136:139],v[212:215],a[204:207]
s_lshl_b32 s95,s42,2
s_add_u32 s95,s95,s40
s_lshl_b32 s95,s95,4
v_mfma_f32_16x16x32_bf16 a[204:207],a[140:143],v[244:247],a[204:207]
s_lshl_b32 s94,s94,4
v_mov_b32 v18,s68
v_mov_b32 v19,s69
v_mfma_f32_16x16x32_bf16 a[204:207],a[144:147],v[112:115],a[204:207]
s_waitcnt vmcnt(1)
s_mov_b64 exec,1
buffer_store_dwordx2 v[18:19],v6,s[64:67],s95 offen offset:2048 sc1
s_mov_b64 exec,-1
s_waitcnt lgkmcnt(0)
v_mfma_f32_16x16x32_bf16 a[204:207],a[148:151],v[116:119],a[204:207]
v_accvgpr_read_b32 v146,a88
v_accvgpr_read_b32 v147,a89
v_mfma_f32_16x16x32_bf16 a[204:207],a[152:155],v[120:123],a[204:207]
v_accvgpr_read_b32 v16,a86
v_accvgpr_read_b32 v17,a87
v_cvt_pk_bf16_f32 v134,v134,v135
v_accvgpr_read_b32 v12,a90
v_accvgpr_read_b32 v13,a91
v_cvt_pk_bf16_f32 v146,v146,v147
v_cvt_pk_bf16_f32 v135,v16,v17
v_cvt_pk_bf16_f32 v147,v12,v13
v_mfma_f32_16x16x32_bf16 a[204:207],a[156:159],v[124:127],a[204:207]
ds_read_b64_tr_b16 a[116:117],v45 offset:10272
ds_read_b64_tr_b16 a[118:119],v45 offset:15392
v_mfma_f32_16x16x32_bf16 a[204:207],a[160:163],v[232:235],a[204:207]
buffer_store_dwordx4 v[132:135],v25,s[32:35],s85 offen sc1
v_mfma_f32_16x16x32_bf16 a[204:207],a[164:167],v[236:239],a[204:207]
ds_read_b64_tr_b16 a[120:121],v45 offset:20512
ds_read_b64_tr_b16 a[122:123],v45 offset:25632
v_mfma_f32_16x16x32_bf16 a[204:207],a[168:171],v[248:251],a[204:207]
ds_read_b64_tr_b16 a[124:125],v45 offset:30752
ds_read_b64_tr_b16 a[126:127],v45 offset:35872
v_mfma_f32_16x16x32_bf16 a[204:207],a[172:175],v[252:255],a[204:207]
ds_read_b64_tr_b16 a[128:129],v1 offset:64
ds_read_b64_tr_b16 a[130:131],v1 offset:5184
v_mfma_f32_16x16x32_bf16 a[220:223],a[176:179],v[148:151],a[220:223]
buffer_store_dwordx4 v[144:147],v25,s[32:35],s86 offen sc1
v_mfma_f32_16x16x32_bf16 a[220:223],a[180:183],v[180:183],a[220:223]
ds_read_b64_tr_b16 a[132:133],v1 offset:10304
ds_read_b64_tr_b16 a[134:135],v1 offset:15424
v_mfma_f32_16x16x32_bf16 a[220:223],a[184:187],v[212:215],a[220:223]
ds_read_b64_tr_b16 a[136:137],v1 offset:20544
ds_read_b64_tr_b16 a[138:139],v1 offset:25664
v_mfma_f32_16x16x32_bf16 a[220:223],a[188:191],v[244:247],a[220:223]
ds_read_b64_tr_b16 a[140:141],v1 offset:30784
ds_read_b64_tr_b16 a[142:143],v1 offset:35904
v_mfma_f32_16x16x32_bf16 a[220:223],a[96:99],v[112:115],a[220:223]
ds_read_b64_tr_b16 a[144:145],v42 offset:64
ds_read_b64_tr_b16 a[146:147],v42 offset:5184
v_mfma_f32_16x16x32_bf16 a[220:223],a[100:103],v[116:119],a[220:223]
ds_read_b64_tr_b16 a[148:149],v42 offset:10304
ds_read_b64_tr_b16 a[150:151],v42 offset:15424
v_mfma_f32_16x16x32_bf16 a[220:223],a[104:107],v[120:123],a[220:223]
ds_read_b64_tr_b16 a[152:153],v42 offset:20544
ds_read_b64_tr_b16 a[154:155],v42 offset:25664
v_mfma_f32_16x16x32_bf16 a[220:223],a[108:111],v[124:127],a[220:223]
ds_read_b64_tr_b16 a[156:157],v42 offset:30784
ds_read_b64_tr_b16 a[158:159],v42 offset:35904
v_mfma_f32_16x16x32_bf16 a[220:223],a[112:115],v[232:235],a[220:223]
s_waitcnt lgkmcnt(14)
ds_read_b64_tr_b16 a[160:161],v44 offset:64
ds_read_b64_tr_b16 a[162:163],v44 offset:5184
v_mfma_f32_16x16x32_bf16 a[220:223],a[116:119],v[236:239],a[220:223]
ds_read_b64_tr_b16 a[164:165],v44 offset:10304
ds_read_b64_tr_b16 a[166:167],v44 offset:15424
v_mfma_f32_16x16x32_bf16 a[220:223],a[120:123],v[248:251],a[220:223]
ds_read_b64_tr_b16 a[168:169],v44 offset:20544
ds_read_b64_tr_b16 a[170:171],v44 offset:25664
v_mfma_f32_16x16x32_bf16 a[220:223],a[124:127],v[252:255],a[220:223]
ds_read_b64_tr_b16 a[172:173],v44 offset:30784
ds_read_b64_tr_b16 a[174:175],v44 offset:35904
v_mfma_f32_16x16x32_bf16 a[236:239],a[128:131],v[148:151],a[236:239]
s_waitcnt lgkmcnt(14)
ds_read_b64_tr_b16 a[176:177],v2 offset:96
ds_read_b64_tr_b16 a[178:179],v2 offset:5216
v_mfma_f32_16x16x32_bf16 a[236:239],a[132:135],v[180:183],a[236:239]
v_bfe_u32 v24,v6,4,2
v_min_u32 v24,2,v24
v_mfma_f32_16x16x32_bf16 a[236:239],a[136:139],v[212:215],a[236:239]
ds_read_b64_tr_b16 a[180:181],v2 offset:10336
ds_read_b64_tr_b16 a[182:183],v2 offset:15456
v_mfma_f32_16x16x32_bf16 a[236:239],a[140:143],v[244:247],a[236:239]
v_add_u32 v24,s70,v24
v_add_u32 v24,1,v24
v_mfma_f32_16x16x32_bf16 a[236:239],a[144:147],v[112:115],a[236:239]
s_waitcnt lgkmcnt(10)
ds_read_b64_tr_b16 a[184:185],v2 offset:20576
ds_read_b64_tr_b16 a[186:187],v2 offset:25696
v_mfma_f32_16x16x32_bf16 a[236:239],a[148:151],v[116:119],a[236:239]
v_and_b32 v24,3,v24
v_lshlrev_b32 v24,8,v24
v_mfma_f32_16x16x32_bf16 a[236:239],a[152:155],v[120:123],a[236:239]
ds_read_b64_tr_b16 a[188:189],v2 offset:30816
ds_read_b64_tr_b16 a[190:191],v2 offset:35936
v_mfma_f32_16x16x32_bf16 a[236:239],a[156:159],v[124:127],a[236:239]
ds_read_b64_tr_b16 a[96:97],v43 offset:96
ds_read_b64_tr_b16 a[98:99],v43 offset:5216
v_mfma_f32_16x16x32_bf16 a[236:239],a[160:163],v[232:235],a[236:239]
s_waitcnt lgkmcnt(8)
ds_read_b64_tr_b16 a[100:101],v43 offset:10336
ds_read_b64_tr_b16 a[102:103],v43 offset:15456
v_mfma_f32_16x16x32_bf16 a[236:239],a[164:167],v[236:239],a[236:239]
ds_read_b64_tr_b16 a[104:105],v43 offset:20576
ds_read_b64_tr_b16 a[106:107],v43 offset:25696
v_mfma_f32_16x16x32_bf16 a[236:239],a[168:171],v[248:251],a[236:239]
ds_read_b64_tr_b16 a[108:109],v43 offset:30816
ds_read_b64_tr_b16 a[110:111],v43 offset:35936
v_mfma_f32_16x16x32_bf16 a[236:239],a[172:175],v[252:255],a[236:239]
ds_read_b64_tr_b16 a[112:113],v45 offset:96
ds_read_b64_tr_b16 a[114:115],v45 offset:5216
v_mfma_f32_16x16x32_bf16 a[252:255],a[176:179],v[148:151],a[252:255]
s_waitcnt lgkmcnt(12)
ds_read_b64_tr_b16 a[116:117],v45 offset:10336
ds_read_b64_tr_b16 a[118:119],v45 offset:15456
v_mfma_f32_16x16x32_bf16 a[252:255],a[180:183],v[180:183],a[252:255]
ds_read_b64_tr_b16 a[120:121],v45 offset:20576
ds_read_b64_tr_b16 a[122:123],v45 offset:25696
v_mfma_f32_16x16x32_bf16 a[252:255],a[184:187],v[212:215],a[252:255]
s_waitcnt lgkmcnt(14)
ds_read_b64_tr_b16 a[124:125],v45 offset:30816
ds_read_b64_tr_b16 a[126:127],v45 offset:35936
v_mfma_f32_16x16x32_bf16 a[252:255],a[188:191],v[244:247],a[252:255]
s_waitcnt lgkmcnt(14)
ds_read_b64_tr_b16 a[128:129],v1 offset:128
ds_read_b64_tr_b16 a[130:131],v1 offset:5248
v_mfma_f32_16x16x32_bf16 a[252:255],a[96:99],v[112:115],a[252:255]
s_waitcnt lgkmcnt(14)
buffer_load_dwordx2 v[26:27],v24,s[64:67],s94 offen offset:2048 sc1
ds_read_b64_tr_b16 a[132:133],v1 offset:10368
ds_read_b64_tr_b16 a[134:135],v1 offset:15488
v_mfma_f32_16x16x32_bf16 a[252:255],a[100:103],v[116:119],a[252:255]
s_waitcnt lgkmcnt(14)
ds_read_b64_tr_b16 a[136:137],v1 offset:20608
ds_read_b64_tr_b16 a[138:139],v1 offset:25728
v_mfma_f32_16x16x32_bf16 a[252:255],a[104:107],v[120:123],a[252:255]
s_waitcnt lgkmcnt(14)
ds_read_b64_tr_b16 a[140:141],v1 offset:30848
ds_read_b64_tr_b16 a[142:143],v1 offset:35968
v_mfma_f32_16x16x32_bf16 a[252:255],a[108:111],v[124:127],a[252:255]
s_waitcnt lgkmcnt(14)
ds_read_b64_tr_b16 a[144:145],v42 offset:128
ds_read_b64_tr_b16 a[146:147],v42 offset:5248
v_mfma_f32_16x16x32_bf16 a[252:255],a[112:115],v[232:235],a[252:255]
s_waitcnt lgkmcnt(14)
ds_read_b64_tr_b16 a[148:149],v42 offset:10368
ds_read_b64_tr_b16 a[150:151],v42 offset:15488
v_mfma_f32_16x16x32_bf16 a[252:255],a[116:119],v[236:239],a[252:255]
s_lshl_b32 s95,s42,2
ds_read_b64_tr_b16 a[152:153],v42 offset:20608
ds_read_b64_tr_b16 a[154:155],v42 offset:25728
s_waitcnt lgkmcnt(15)
v_mfma_f32_16x16x32_bf16 a[252:255],a[120:123],v[248:251],a[252:255]
s_add_u32 s95,s95,s40
s_lshl_b32 s95,s95,4
ds_read_b64_tr_b16 a[156:157],v42 offset:30848
ds_read_b64_tr_b16 a[158:159],v42 offset:35968
v_mov_b32 v18,s68
v_mov_b32 v19,s69
s_waitcnt lgkmcnt(15)
v_mfma_f32_16x16x32_bf16 a[252:255],a[124:127],v[252:255],a[252:255]
s_waitcnt vmcnt(1)
s_mov_b64 exec,1
buffer_store_dwordx2 v[18:19],v6,s[64:67],s95 offen offset:3072 sc1
s_mov_b64 exec,-1
ds_read_b64_tr_b16 a[160:161],v44 offset:128
)A"
R"A(ds_read_b64_tr_b16 a[162:163],v44 offset:5248
s_waitcnt lgkmcnt(15)
v_mfma_f32_16x16x32_bf16 a[12:15],a[128:131],v[148:151],a[12:15]
s_waitcnt lgkmcnt(14)
ds_read_b64_tr_b16 a[164:165],v44 offset:10368
ds_read_b64_tr_b16 a[166:167],v44 offset:15488
v_mfma_f32_16x16x32_bf16 a[12:15],a[132:135],v[180:183],a[12:15]
s_waitcnt lgkmcnt(14)
ds_read_b64_tr_b16 a[168:169],v44 offset:20608
ds_read_b64_tr_b16 a[170:171],v44 offset:25728
v_mfma_f32_16x16x32_bf16 a[12:15],a[136:139],v[212:215],a[12:15]
s_waitcnt lgkmcnt(14)
ds_read_b64_tr_b16 a[172:173],v44 offset:30848
ds_read_b64_tr_b16 a[174:175],v44 offset:35968
v_mfma_f32_16x16x32_bf16 a[12:15],a[140:143],v[244:247],a[12:15]
s_waitcnt lgkmcnt(14)
ds_read_b64_tr_b16 a[176:177],v2 offset:160
ds_read_b64_tr_b16 a[178:179],v2 offset:5280
v_mfma_f32_16x16x32_bf16 a[12:15],a[144:147],v[112:115],a[12:15]
s_waitcnt lgkmcnt(14)
ds_read_b64_tr_b16 a[180:181],v2 offset:10400
ds_read_b64_tr_b16 a[182:183],v2 offset:15520
v_mfma_f32_16x16x32_bf16 a[12:15],a[148:151],v[116:119],a[12:15]
s_waitcnt lgkmcnt(14)
ds_read_b64_tr_b16 a[184:185],v2 offset:20640
ds_read_b64_tr_b16 a[186:187],v2 offset:25760
v_mfma_f32_16x16x32_bf16 a[12:15],a[152:155],v[120:123],a[12:15]
s_waitcnt lgkmcnt(14)
ds_read_b64_tr_b16 a[188:189],v2 offset:30880
ds_read_b64_tr_b16 a[190:191],v2 offset:36000
v_mfma_f32_16x16x32_bf16 a[12:15],a[156:159],v[124:127],a[12:15]
s_waitcnt lgkmcnt(14)
ds_read_b64_tr_b16 a[96:97],v43 offset:160
ds_read_b64_tr_b16 a[98:99],v43 offset:5280
v_mfma_f32_16x16x32_bf16 a[12:15],a[160:163],v[232:235],a[12:15]
s_waitcnt lgkmcnt(14)
ds_read_b64_tr_b16 a[100:101],v43 offset:10400
ds_read_b64_tr_b16 a[102:103],v43 offset:15520
v_mfma_f32_16x16x32_bf16 a[12:15],a[164:167],v[236:239],a[12:15]
s_waitcnt lgkmcnt(14)
ds_read_b64_tr_b16 a[104:105],v43 offset:20640
ds_read_b64_tr_b16 a[106:107],v43 offset:25760
v_mfma_f32_16x16x32_bf16 a[12:15],a[168:171],v[248:251],a[12:15]
s_waitcnt lgkmcnt(14)
ds_read_b64_tr_b16 a[108:109],v43 offset:30880
ds_read_b64_tr_b16 a[110:111],v43 offset:36000
v_mfma_f32_16x16x32_bf16 a[12:15],a[172:175],v[252:255],a[12:15]
s_waitcnt lgkmcnt(14)
ds_read_b64_tr_b16 a[112:113],v45 offset:160
ds_read_b64_tr_b16 a[114:115],v45 offset:5280
v_mfma_f32_16x16x32_bf16 a[28:31],a[176:179],v[148:151],a[28:31]
s_waitcnt lgkmcnt(14)
ds_read_b64_tr_b16 a[116:117],v45 offset:10400
ds_read_b64_tr_b16 a[118:119],v45 offset:15520
v_mfma_f32_16x16x32_bf16 a[28:31],a[180:183],v[180:183],a[28:31]
s_waitcnt lgkmcnt(14)
ds_read_b64_tr_b16 a[120:121],v45 offset:20640
ds_read_b64_tr_b16 a[122:123],v45 offset:25760
v_mfma_f32_16x16x32_bf16 a[28:31],a[184:187],v[212:215],a[28:31]
s_waitcnt lgkmcnt(14)
ds_read_b64_tr_b16 a[124:125],v45 offset:30880
ds_read_b64_tr_b16 a[126:127],v45 offset:36000
v_mfma_f32_16x16x32_bf16 a[28:31],a[188:191],v[244:247],a[28:31]
s_waitcnt lgkmcnt(14)
ds_read_b64_tr_b16 a[128:129],v1 offset:192
ds_read_b64_tr_b16 a[130:131],v1 offset:5312
v_mfma_f32_16x16x32_bf16 a[28:31],a[96:99],v[112:115],a[28:31]
s_waitcnt lgkmcnt(14)
ds_read_b64_tr_b16 a[132:133],v1 offset:10432
ds_read_b64_tr_b16 a[134:135],v1 offset:15552
v_mfma_f32_16x16x32_bf16 a[28:31],a[100:103],v[116:119],a[28:31]
s_waitcnt lgkmcnt(14)
ds_read_b64_tr_b16 a[136:137],v1 offset:20672
ds_read_b64_tr_b16 a[138:139],v1 offset:25792
v_mfma_f32_16x16x32_bf16 a[28:31],a[104:107],v[120:123],a[28:31]
s_waitcnt lgkmcnt(14)
ds_read_b64_tr_b16 a[140:141],v1 offset:30912
ds_read_b64_tr_b16 a[142:143],v1 offset:36032
v_mfma_f32_16x16x32_bf16 a[28:31],a[108:111],v[124:127],a[28:31]
s_waitcnt lgkmcnt(14)
ds_read_b64_tr_b16 a[144:145],v42 offset:192
ds_read_b64_tr_b16 a[146:147],v42 offset:5312
v_mfma_f32_16x16x32_bf16 a[28:31],a[112:115],v[232:235],a[28:31]
s_waitcnt lgkmcnt(14)
ds_read_b64_tr_b16 a[148:149],v42 offset:10432
ds_read_b64_tr_b16 a[150:151],v42 offset:15552
v_mfma_f32_16x16x32_bf16 a[28:31],a[116:119],v[236:239],a[28:31]
s_waitcnt lgkmcnt(14)
ds_read_b64_tr_b16 a[152:153],v42 offset:20672
ds_read_b64_tr_b16 a[154:155],v42 offset:25792
v_mfma_f32_16x16x32_bf16 a[28:31],a[120:123],v[248:251],a[28:31]
s_waitcnt lgkmcnt(14)
ds_read_b64_tr_b16 a[156:157],v42 offset:30912
ds_read_b64_tr_b16 a[158:159],v42 offset:36032
v_mfma_f32_16x16x32_bf16 a[28:31],a[124:127],v[252:255],a[28:31]
s_waitcnt lgkmcnt(14)
ds_read_b64_tr_b16 a[160:161],v44 offset:192
ds_read_b64_tr_b16 a[162:163],v44 offset:5312
v_mfma_f32_16x16x32_bf16 a[44:47],a[128:131],v[148:151],a[44:47]
ds_read_b64_tr_b16 a[164:165],v44 offset:10432
ds_read_b64_tr_b16 a[166:167],v44 offset:15552
s_waitcnt vmcnt(1)
v_cmp_ne_u32 s[90:91],s68,v26
v_cmp_ne_u32 s[92:93],s69,v27
s_nop 0
s_or_b64 s[90:91],s[90:91],s[92:93]
s_cbranch_scc1 L_fr10_%=
L_fo10_%=:
s_waitcnt lgkmcnt(15)
v_mfma_f32_16x16x32_bf16 a[44:47],a[132:135],v[180:183],a[44:47]
s_waitcnt lgkmcnt(14)
buffer_load_dwordx4 v[64:67],v6,s[32:35],s76 offen sc1
ds_read_b64_tr_b16 a[168:169],v44 offset:20672
ds_read_b64_tr_b16 a[170:171],v44 offset:25792
v_mfma_f32_16x16x32_bf16 a[44:47],a[136:139],v[212:215],a[44:47]
s_lshl_b32 s94,s71,2
ds_read_b64_tr_b16 a[172:173],v44 offset:30912
ds_read_b64_tr_b16 a[174:175],v44 offset:36032
s_waitcnt lgkmcnt(15)
v_mfma_f32_16x16x32_bf16 a[44:47],a[140:143],v[244:247],a[44:47]
s_add_u32 s94,s94,s40
ds_read_b64_tr_b16 a[176:177],v2 offset:224
ds_read_b64_tr_b16 a[178:179],v2 offset:5344
s_waitcnt lgkmcnt(15)
v_mfma_f32_16x16x32_bf16 a[44:47],a[144:147],v[112:115],a[44:47]
s_lshl_b32 s94,s94,4
ds_read_b64_tr_b16 a[180:181],v2 offset:10464
ds_read_b64_tr_b16 a[182:183],v2 offset:15584
s_waitcnt lgkmcnt(15)
v_mfma_f32_16x16x32_bf16 a[44:47],a[148:151],v[116:119],a[44:47]
s_waitcnt lgkmcnt(14)
buffer_load_dwordx4 v[80:83],v6,s[32:35],s77 offen sc1
ds_read_b64_tr_b16 a[184:185],v2 offset:20704
ds_read_b64_tr_b16 a[186:187],v2 offset:25824
v_mfma_f32_16x16x32_bf16 a[44:47],a[152:155],v[120:123],a[44:47]
s_waitcnt lgkmcnt(14)
ds_read_b64_tr_b16 a[188:189],v2 offset:30944
ds_read_b64_tr_b16 a[190:191],v2 offset:36064
v_mfma_f32_16x16x32_bf16 a[44:47],a[156:159],v[124:127],a[44:47]
s_waitcnt lgkmcnt(14)
ds_read_b64_tr_b16 a[96:97],v43 offset:224
ds_read_b64_tr_b16 a[98:99],v43 offset:5344
v_mfma_f32_16x16x32_bf16 a[44:47],a[160:163],v[232:235],a[44:47]
s_waitcnt lgkmcnt(14)
ds_read_b64_tr_b16 a[100:101],v43 offset:10464
ds_read_b64_tr_b16 a[102:103],v43 offset:15584
v_mfma_f32_16x16x32_bf16 a[44:47],a[164:167],v[236:239],a[44:47]
s_waitcnt lgkmcnt(14)
buffer_load_dwordx4 v[96:99],v6,s[32:35],s78 offen sc1
ds_read_b64_tr_b16 a[104:105],v43 offset:20704
ds_read_b64_tr_b16 a[106:107],v43 offset:25824
v_mfma_f32_16x16x32_bf16 a[44:47],a[168:171],v[248:251],a[44:47]
s_waitcnt lgkmcnt(14)
ds_read_b64_tr_b16 a[108:109],v43 offset:30944
ds_read_b64_tr_b16 a[110:111],v43 offset:36064
v_mfma_f32_16x16x32_bf16 a[44:47],a[172:175],v[252:255],a[44:47]
s_waitcnt lgkmcnt(14)
ds_read_b64_tr_b16 a[112:113],v45 offset:224
ds_read_b64_tr_b16 a[114:115],v45 offset:5344
v_mfma_f32_16x16x32_bf16 a[60:63],a[176:179],v[148:151],a[60:63]
s_waitcnt lgkmcnt(14)
ds_read_b64_tr_b16 a[116:117],v45 offset:10464
ds_read_b64_tr_b16 a[118:119],v45 offset:15584
v_mfma_f32_16x16x32_bf16 a[60:63],a[180:183],v[180:183],a[60:63]
s_waitcnt lgkmcnt(14)
buffer_load_dwordx4 v[68:71],v6,s[32:35],s76 offen offset:1024 sc1
ds_read_b64_tr_b16 a[120:121],v45 offset:20704
ds_read_b64_tr_b16 a[122:123],v45 offset:25824
v_mfma_f32_16x16x32_bf16 a[60:63],a[184:187],v[212:215],a[60:63]
s_waitcnt lgkmcnt(14)
ds_read_b64_tr_b16 a[124:125],v45 offset:30944
ds_read_b64_tr_b16 a[126:127],v45 offset:36064
v_mfma_f32_16x16x32_bf16 a[60:63],a[188:191],v[244:247],a[60:63]
s_waitcnt lgkmcnt(14)
ds_read_b64_tr_b16 a[128:129],v1 offset:256
ds_read_b64_tr_b16 a[130:131],v1 offset:5376
v_mfma_f32_16x16x32_bf16 a[60:63],a[96:99],v[112:115],a[60:63]
s_waitcnt lgkmcnt(14)
ds_read_b64_tr_b16 a[132:133],v1 offset:10496
ds_read_b64_tr_b16 a[134:135],v1 offset:15616
v_mfma_f32_16x16x32_bf16 a[60:63],a[100:103],v[116:119],a[60:63]
s_waitcnt lgkmcnt(14)
buffer_load_dwordx4 v[84:87],v6,s[32:35],s77 offen offset:1024 sc1
ds_read_b64_tr_b16 a[136:137],v1 offset:20736
ds_read_b64_tr_b16 a[138:139],v1 offset:25856
v_mfma_f32_16x16x32_bf16 a[60:63],a[104:107],v[120:123],a[60:63]
s_waitcnt lgkmcnt(14)
ds_read_b64_tr_b16 a[140:141],v1 offset:30976
ds_read_b64_tr_b16 a[142:143],v1 offset:36096
v_mfma_f32_16x16x32_bf16 a[60:63],a[108:111],v[124:127],a[60:63]
s_waitcnt lgkmcnt(14)
ds_read_b64_tr_b16 a[144:145],v42 offset:256
ds_read_b64_tr_b16 a[146:147],v42 offset:5376
v_mfma_f32_16x16x32_bf16 a[60:63],a[112:115],v[232:235],a[60:63]
s_waitcnt lgkmcnt(14)
ds_read_b64_tr_b16 a[148:149],v42 offset:10496
ds_read_b64_tr_b16 a[150:151],v42 offset:15616
v_mfma_f32_16x16x32_bf16 a[60:63],a[116:119],v[236:239],a[60:63]
s_waitcnt lgkmcnt(14)
buffer_load_dwordx4 v[100:103],v6,s[32:35],s78 offen offset:1024 sc1
ds_read_b64_tr_b16 a[152:153],v42 offset:20736
ds_read_b64_tr_b16 a[154:155],v42 offset:25856
v_mfma_f32_16x16x32_bf16 a[60:63],a[120:123],v[248:251],a[60:63]
s_waitcnt lgkmcnt(14)
ds_read_b64_tr_b16 a[156:157],v42 offset:30976
ds_read_b64_tr_b16 a[158:159],v42 offset:36096
v_mfma_f32_16x16x32_bf16 a[60:63],a[124:127],v[252:255],a[60:63]
s_waitcnt lgkmcnt(14)
ds_read_b64_tr_b16 a[160:161],v44 offset:256
ds_read_b64_tr_b16 a[162:163],v44 offset:5376
v_mfma_f32_16x16x32_bf16 a[76:79],a[128:131],v[148:151],a[76:79]
s_waitcnt lgkmcnt(14)
ds_read_b64_tr_b16 a[164:165],v44 offset:10496
ds_read_b64_tr_b16 a[166:167],v44 offset:15616
v_mfma_f32_16x16x32_bf16 a[76:79],a[132:135],v[180:183],a[76:79]
s_waitcnt lgkmcnt(14)
buffer_load_dwordx4 v[72:75],v6,s[32:35],s76 offen offset:2048 sc1
ds_read_b64_tr_b16 a[168:169],v44 offset:20736
ds_read_b64_tr_b16 a[170:171],v44 offset:25856
v_bfe_u32 v24,v6,4,2
v_min_u32 v24,2,v24
v_mfma_f32_16x16x32_bf16 a[76:79],a[136:139],v[212:215],a[76:79]
s_waitcnt lgkmcnt(14)
ds_read_b64_tr_b16 a[172:173],v44 offset:30976
ds_read_b64_tr_b16 a[174:175],v44 offset:36096
v_add_u32 v24,s70,v24
v_add_u32 v24,1,v24
v_mfma_f32_16x16x32_bf16 a[76:79],a[140:143],v[244:247],a[76:79]
s_waitcnt lgkmcnt(14)
ds_read_b64_tr_b16 a[176:177],v2 offset:288
ds_read_b64_tr_b16 a[178:179],v2 offset:5408
v_and_b32 v24,3,v24
v_lshlrev_b32 v24,8,v24
v_mfma_f32_16x16x32_bf16 a[76:79],a[144:147],v[112:115],a[76:79]
s_waitcnt lgkmcnt(14)
ds_read_b64_tr_b16 a[180:181],v2 offset:10528
ds_read_b64_tr_b16 a[182:183],v2 offset:15648
v_mfma_f32_16x16x32_bf16 a[76:79],a[148:151],v[116:119],a[76:79]
s_waitcnt lgkmcnt(14)
buffer_load_dwordx4 v[88:91],v6,s[32:35],s77 offen offset:2048 sc1
ds_read_b64_tr_b16 a[184:185],v2 offset:20768
ds_read_b64_tr_b16 a[186:187],v2 offset:25888
v_mfma_f32_16x16x32_bf16 a[76:79],a[152:155],v[120:123],a[76:79]
s_waitcnt lgkmcnt(14)
ds_read_b64_tr_b16 a[188:189],v2 offset:31008
ds_read_b64_tr_b16 a[190:191],v2 offset:36128
v_mfma_f32_16x16x32_bf16 a[76:79],a[156:159],v[124:127],a[76:79]
s_waitcnt lgkmcnt(14)
ds_read_b64_tr_b16 a[96:97],v43 offset:288
ds_read_b64_tr_b16 a[98:99],v43 offset:5408
v_mfma_f32_16x16x32_bf16 a[76:79],a[160:163],v[232:235],a[76:79]
s_waitcnt lgkmcnt(14)
ds_read_b64_tr_b16 a[100:101],v43 offset:10528
ds_read_b64_tr_b16 a[102:103],v43 offset:15648
v_mfma_f32_16x16x32_bf16 a[76:79],a[164:167],v[236:239],a[76:79]
s_waitcnt lgkmcnt(14)
buffer_load_dwordx4 v[104:107],v6,s[32:35],s78 offen offset:2048 sc1
ds_read_b64_tr_b16 a[104:105],v43 offset:20768
ds_read_b64_tr_b16 a[106:107],v43 offset:25888
v_mfma_f32_16x16x32_bf16 a[76:79],a[168:171],v[248:251],a[76:79]
s_waitcnt lgkmcnt(14)
ds_read_b64_tr_b16 a[108:109],v43 offset:31008
ds_read_b64_tr_b16 a[110:111],v43 offset:36128
v_mfma_f32_16x16x32_bf16 a[76:79],a[172:175],v[252:255],a[76:79]
s_waitcnt lgkmcnt(14)
ds_read_b64_tr_b16 a[112:113],v45 offset:288
ds_read_b64_tr_b16 a[114:115],v45 offset:5408
v_mfma_f32_16x16x32_bf16 a[92:95],a[176:179],v[148:151],a[92:95]
s_waitcnt lgkmcnt(14)
ds_read_b64_tr_b16 a[116:117],v45 offset:10528
ds_read_b64_tr_b16 a[118:119],v45 offset:15648
v_mfma_f32_16x16x32_bf16 a[92:95],a[180:183],v[180:183],a[92:95]
s_waitcnt lgkmcnt(14)
)A"
R"A(buffer_load_dwordx4 v[76:79],v6,s[32:35],s76 offen offset:3072 sc1
ds_read_b64_tr_b16 a[120:121],v45 offset:20768
ds_read_b64_tr_b16 a[122:123],v45 offset:25888
v_mfma_f32_16x16x32_bf16 a[92:95],a[184:187],v[212:215],a[92:95]
s_waitcnt lgkmcnt(14)
ds_read_b64_tr_b16 a[124:125],v45 offset:31008
ds_read_b64_tr_b16 a[126:127],v45 offset:36128
v_mfma_f32_16x16x32_bf16 a[92:95],a[188:191],v[244:247],a[92:95]
s_waitcnt lgkmcnt(14)
v_mfma_f32_16x16x32_bf16 a[92:95],a[96:99],v[112:115],a[92:95]
s_waitcnt lgkmcnt(12)
v_mfma_f32_16x16x32_bf16 a[92:95],a[100:103],v[116:119],a[92:95]
s_waitcnt lgkmcnt(10)
buffer_load_dwordx4 v[92:95],v6,s[32:35],s77 offen offset:3072 sc1
v_mfma_f32_16x16x32_bf16 a[92:95],a[104:107],v[120:123],a[92:95]
s_waitcnt lgkmcnt(8)
v_mfma_f32_16x16x32_bf16 a[92:95],a[108:111],v[124:127],a[92:95]
s_waitcnt lgkmcnt(6)
v_mfma_f32_16x16x32_bf16 a[92:95],a[112:115],v[232:235],a[92:95]
s_waitcnt lgkmcnt(4)
v_mfma_f32_16x16x32_bf16 a[92:95],a[116:119],v[236:239],a[92:95]
s_waitcnt lgkmcnt(2)
buffer_load_dwordx4 v[108:111],v6,s[32:35],s78 offen offset:3072 sc1
v_mfma_f32_16x16x32_bf16 a[92:95],a[120:123],v[248:251],a[92:95]
s_waitcnt lgkmcnt(0)
v_mfma_f32_16x16x32_bf16 a[92:95],a[124:127],v[252:255],a[92:95]
buffer_load_dwordx2 v[26:27],v24,s[64:67],s94 offen offset:3072 sc1
s_waitcnt vmcnt(0)
v_cmp_ne_u32 s[90:91],s68,v26
v_cmp_ne_u32 s[92:93],s69,v27
s_nop 0
s_or_b64 s[90:91],s[90:91],s[92:93]
s_cbranch_scc1 L_fr11_%=
L_fo11_%=:
buffer_load_dwordx4 v[136:139],v25,s[32:35],s76 offen sc1
buffer_load_dwordx4 v[168:171],v25,s[32:35],s77 offen sc1
buffer_load_dwordx4 v[200:203],v25,s[32:35],s78 offen sc1
v_mfma_f32_16x16x32_bf16 a[204:207],v[34:37],v[64:67],a[204:207]
v_mfma_f32_16x16x32_bf16 a[204:207],v[34:37],v[80:83],a[204:207]
v_mfma_f32_16x16x32_bf16 a[204:207],v[34:37],v[96:99],a[204:207]
v_mfma_f32_16x16x32_bf16 a[220:223],v[32:35],v[64:67],a[220:223]
v_mfma_f32_16x16x32_bf16 a[220:223],v[32:35],v[80:83],a[220:223]
v_mfma_f32_16x16x32_bf16 a[220:223],v[32:35],v[96:99],a[220:223]
v_mfma_f32_16x16x32_bf16 a[236:239],v[34:37],v[68:71],a[236:239]
v_mfma_f32_16x16x32_bf16 a[236:239],v[34:37],v[84:87],a[236:239]
v_mfma_f32_16x16x32_bf16 a[236:239],v[34:37],v[100:103],a[236:239]
s_nop 1
v_accvgpr_read_b32 v128,a204
v_accvgpr_read_b32 v129,a205
v_mfma_f32_16x16x32_bf16 a[252:255],v[32:35],v[68:71],a[252:255]
v_cvt_pk_bf16_f32 v128,v128,v129
v_accvgpr_read_b32 v130,a206
v_mfma_f32_16x16x32_bf16 a[252:255],v[32:35],v[84:87],a[252:255]
v_accvgpr_read_b32 v131,a207
v_cvt_pk_bf16_f32 v129,v130,v131
v_mfma_f32_16x16x32_bf16 a[252:255],v[32:35],v[100:103],a[252:255]
buffer_store_dwordx2 v[128:129],v40,s[20:23],s43 offen
v_mfma_f32_16x16x32_bf16 a[12:15],v[34:37],v[72:75],a[12:15]
v_accvgpr_read_b32 v130,a220
v_accvgpr_read_b32 v131,a221
v_mfma_f32_16x16x32_bf16 a[12:15],v[34:37],v[88:91],a[12:15]
v_accvgpr_read_b32 v14,a222
v_accvgpr_read_b32 v15,a223
v_mfma_f32_16x16x32_bf16 a[12:15],v[34:37],v[104:107],a[12:15]
v_cvt_pk_bf16_f32 v130,v130,v131
v_cvt_pk_bf16_f32 v131,v14,v15
v_mfma_f32_16x16x32_bf16 a[28:31],v[32:35],v[72:75],a[28:31]
buffer_store_dwordx2 v[130:131],v40,s[20:23],s43 offen offset:32
v_mfma_f32_16x16x32_bf16 a[28:31],v[32:35],v[88:91],a[28:31]
v_accvgpr_read_b32 v132,a236
v_accvgpr_read_b32 v133,a237
v_mfma_f32_16x16x32_bf16 a[28:31],v[32:35],v[104:107],a[28:31]
v_cvt_pk_bf16_f32 v132,v132,v133
v_accvgpr_read_b32 v134,a238
v_mfma_f32_16x16x32_bf16 a[44:47],v[34:37],v[76:79],a[44:47]
v_accvgpr_read_b32 v135,a239
v_cvt_pk_bf16_f32 v133,v134,v135
v_mfma_f32_16x16x32_bf16 a[44:47],v[34:37],v[92:95],a[44:47]
buffer_store_dwordx2 v[132:133],v40,s[20:23],s43 offen offset:64
v_mfma_f32_16x16x32_bf16 a[44:47],v[34:37],v[108:111],a[44:47]
v_accvgpr_read_b32 v144,a12
v_accvgpr_read_b32 v145,a13
v_cvt_pk_bf16_f32 v144,v144,v145
v_accvgpr_read_b32 v146,a14
v_accvgpr_read_b32 v147,a15
v_mfma_f32_16x16x32_bf16 a[60:63],v[32:35],v[76:79],a[60:63]
v_accvgpr_read_b32 v134,a252
v_accvgpr_read_b32 v135,a253
v_cvt_pk_bf16_f32 v145,v146,v147
v_mfma_f32_16x16x32_bf16 a[60:63],v[32:35],v[92:95],a[60:63]
v_accvgpr_read_b32 v16,a254
v_accvgpr_read_b32 v17,a255
v_cvt_pk_bf16_f32 v134,v134,v135
v_cvt_pk_bf16_f32 v135,v16,v17
v_mfma_f32_16x16x32_bf16 a[60:63],v[32:35],v[108:111],a[60:63]
s_waitcnt vmcnt(5)
buffer_store_dwordx2 v[134:135],v40,s[20:23],s43 offen offset:96
buffer_store_dwordx2 v[144:145],v40,s[20:23],s43 offen offset:128
v_accvgpr_read_b32 v146,a28
v_accvgpr_read_b32 v147,a29
v_accvgpr_read_b32 v12,a30
v_accvgpr_read_b32 v13,a31
v_cvt_pk_bf16_f32 v146,v146,v147
v_cvt_pk_bf16_f32 v147,v12,v13
s_nop 3
buffer_store_dwordx2 v[146:147],v40,s[20:23],s43 offen offset:160
v_accvgpr_read_b32 v160,a44
v_accvgpr_read_b32 v161,a45
v_cvt_pk_bf16_f32 v160,v160,v161
v_accvgpr_read_b32 v162,a46
v_accvgpr_read_b32 v163,a47
v_cvt_pk_bf16_f32 v161,v162,v163
buffer_store_dwordx2 v[160:161],v40,s[20:23],s43 offen offset:192
v_accvgpr_read_b32 v162,a60
v_accvgpr_read_b32 v163,a61
v_accvgpr_read_b32 v14,a62
v_accvgpr_read_b32 v15,a63
v_cvt_pk_bf16_f32 v162,v162,v163
v_cvt_pk_bf16_f32 v163,v14,v15
buffer_store_dwordx2 v[162:163],v40,s[20:23],s43 offen offset:224
v_mfma_f32_16x16x32_bf16 a[76:79],v[34:37],v[136:139],a[76:79]
s_waitcnt vmcnt(9)
v_mfma_f32_16x16x32_bf16 a[76:79],v[34:37],v[168:171],a[76:79]
s_waitcnt vmcnt(8)
v_mfma_f32_16x16x32_bf16 a[76:79],v[34:37],v[200:203],a[76:79]
v_mfma_f32_16x16x32_bf16 a[92:95],v[32:35],v[136:139],a[92:95]
v_mfma_f32_16x16x32_bf16 a[92:95],v[32:35],v[168:171],a[92:95]
v_mfma_f32_16x16x32_bf16 a[92:95],v[32:35],v[200:203],a[92:95]
s_nop 3
s_nop 0
v_accvgpr_read_b32 v164,a76
v_accvgpr_read_b32 v165,a77
v_cvt_pk_bf16_f32 v164,v164,v165
v_accvgpr_read_b32 v166,a78
v_accvgpr_read_b32 v167,a79
v_cvt_pk_bf16_f32 v165,v166,v167
buffer_store_dwordx2 v[164:165],v40,s[20:23],s43 offen offset:256
v_accvgpr_read_b32 v166,a92
v_accvgpr_read_b32 v167,a93
v_accvgpr_read_b32 v16,a94
v_accvgpr_read_b32 v17,a95
v_cvt_pk_bf16_f32 v166,v166,v167
v_cvt_pk_bf16_f32 v167,v16,v17
buffer_store_dwordx2 v[166:167],v40,s[20:23],s43 offen offset:288
s_or_b32 s62,s42,s40
s_cmp_lg_u32 s62,0
s_cbranch_scc1 L_noep_%=
v_mov_b32 v12,s68
s_mov_b64 exec,1
buffer_store_dword v12,v25,s[64:67],0 offen sc1
s_mov_b64 exec,-1
L_noep_%=:
s_endpgm
L_ps2_%=:
s_min_i32 s79,s79,100
s_add_u32 s78,s78,s79
L_pl2_%=:
s_sleep 1
s_sub_u32 s79,s79,4
s_cmp_gt_i32 s79,1
s_cbranch_scc1 L_pl2_%=
s_branch L_pr2_%=
L_ps3_%=:
s_min_i32 s79,s79,100
s_add_u32 s78,s78,s79
L_pl3_%=:
s_sleep 1
s_sub_u32 s79,s79,4
s_cmp_gt_i32 s79,1
s_cbranch_scc1 L_pl3_%=
s_branch L_pr3_%=
L_ps4_%=:
s_min_i32 s79,s79,100
s_add_u32 s78,s78,s79
L_pl4_%=:
s_sleep 1
s_sub_u32 s79,s79,4
s_cmp_gt_i32 s79,1
s_cbranch_scc1 L_pl4_%=
s_branch L_pr4_%=
L_ps5_%=:
s_min_i32 s79,s79,100
s_add_u32 s78,s78,s79
L_pl5_%=:
s_sleep 1
s_sub_u32 s79,s79,4
s_cmp_gt_i32 s79,1
s_cbranch_scc1 L_pl5_%=
s_branch L_pr5_%=
L_ps6_%=:
s_min_i32 s79,s79,100
s_add_u32 s78,s78,s79
L_pl6_%=:
s_sleep 1
s_sub_u32 s79,s79,4
s_cmp_gt_i32 s79,1
s_cbranch_scc1 L_pl6_%=
s_branch L_pr6_%=
L_ps7_%=:
s_min_i32 s79,s79,100
s_add_u32 s78,s78,s79
L_pl7_%=:
s_sleep 1
s_sub_u32 s79,s79,4
s_cmp_gt_i32 s79,1
s_cbranch_scc1 L_pl7_%=
s_branch L_pr7_%=
L_fr8_%=:
s_mov_b32 s88,262144
L_fl8_%=:
buffer_load_dwordx2 v[26:27],v24,s[64:67],s94 offen sc1
s_waitcnt vmcnt(0)
s_sub_u32 s88,s88,1
s_cmp_eq_u32 s88,0
s_cbranch_scc1 L_fo8_%=
v_cmp_ne_u32 s[90:91],s68,v26
v_cmp_ne_u32 s[92:93],s69,v27
s_nop 0
s_or_b64 s[90:91],s[90:91],s[92:93]
s_cbranch_scc1 L_fl8_%=
s_branch L_fo8_%=
L_fr9_%=:
s_mov_b32 s88,262144
L_fl9_%=:
buffer_load_dwordx2 v[26:27],v24,s[64:67],s94 offen offset:1024 sc1
s_waitcnt vmcnt(0)
s_sub_u32 s88,s88,1
s_cmp_eq_u32 s88,0
s_cbranch_scc1 L_fo9_%=
v_cmp_ne_u32 s[90:91],s68,v26
v_cmp_ne_u32 s[92:93],s69,v27
s_nop 0
s_or_b64 s[90:91],s[90:91],s[92:93]
s_cbranch_scc1 L_fl9_%=
s_branch L_fo9_%=
L_fr10_%=:
s_mov_b32 s88,262144
L_fl10_%=:
buffer_load_dwordx2 v[26:27],v24,s[64:67],s94 offen offset:2048 sc1
s_waitcnt vmcnt(0)
s_sub_u32 s88,s88,1
s_cmp_eq_u32 s88,0
s_cbranch_scc1 L_fo10_%=
v_cmp_ne_u32 s[90:91],s68,v26
v_cmp_ne_u32 s[92:93],s69,v27
s_nop 0
s_or_b64 s[90:91],s[90:91],s[92:93]
s_cbranch_scc1 L_fl10_%=
s_branch L_fo10_%=
L_fr11_%=:
s_mov_b32 s88,262144
L_fl11_%=:
buffer_load_dwordx2 v[26:27],v24,s[64:67],s94 offen offset:3072 sc1
s_waitcnt vmcnt(0)
s_sub_u32 s88,s88,1
s_cmp_eq_u32 s88,0
s_cbranch_scc1 L_fo11_%=
v_cmp_ne_u32 s[90:91],s68,v26
v_cmp_ne_u32 s[92:93],s69,v27
s_nop 0
s_or_b64 s[90:91],s[90:91],s[92:93]
s_cbranch_scc1 L_fl11_%=
s_branch L_fo11_%=
L_out_end_%=:
L_fast_end_%=:
L_slow0_%=:
s_mov_b32 s98,0
s_branch L_slowc_%=
L_slow1_%=:
s_mov_b32 s98,1
L_slowc_%=:
s_nop 7
s_nop 7
s_waitcnt vmcnt(0) lgkmcnt(0)
s_lshl_b32 s96,%1,2
s_add_u32 s96,s96,s40
s_lshl_b32 s96,s96,15
s_add_u32 s96,s96,167772160
s_mov_b64 s[56:57],s[12:13]
s_add_u32 s56,s56,s96
s_addc_u32 s57,s57,0
s_and_b32 s57,s57,0xffff
s_mov_b32 s58,32768
s_mov_b32 s59,0x20000
s_mov_b32 s96,0
buffer_store_dwordx4 v[64:67],v6,s[56:59],s96 offen
buffer_store_dwordx4 v[68:71],v6,s[56:59],s96 offen offset:1024
buffer_store_dwordx4 v[72:75],v6,s[56:59],s96 offen offset:2048
buffer_store_dwordx4 v[76:79],v6,s[56:59],s96 offen offset:3072
s_mov_b32 s96,4096
buffer_store_dwordx4 v[80:83],v6,s[56:59],s96 offen
buffer_store_dwordx4 v[84:87],v6,s[56:59],s96 offen offset:1024
buffer_store_dwordx4 v[88:91],v6,s[56:59],s96 offen offset:2048
buffer_store_dwordx4 v[92:95],v6,s[56:59],s96 offen offset:3072
s_mov_b32 s96,8192
buffer_store_dwordx4 v[96:99],v6,s[56:59],s96 offen
buffer_store_dwordx4 v[100:103],v6,s[56:59],s96 offen offset:1024
buffer_store_dwordx4 v[104:107],v6,s[56:59],s96 offen offset:2048
buffer_store_dwordx4 v[108:111],v6,s[56:59],s96 offen offset:3072
s_mov_b32 s96,12288
buffer_store_dwordx4 v[112:115],v6,s[56:59],s96 offen
buffer_store_dwordx4 v[116:119],v6,s[56:59],s96 offen offset:1024
buffer_store_dwordx4 v[120:123],v6,s[56:59],s96 offen offset:2048
buffer_store_dwordx4 v[124:127],v6,s[56:59],s96 offen offset:3072
s_mov_b32 s96,16384
buffer_store_dwordx4 v[128:131],v6,s[56:59],s96 offen
buffer_store_dwordx4 v[132:135],v6,s[56:59],s96 offen offset:1024
buffer_store_dwordx4 v[136:139],v6,s[56:59],s96 offen offset:2048
buffer_store_dwordx4 v[140:143],v6,s[56:59],s96 offen offset:3072
s_mov_b32 s96,20480
buffer_store_dwordx4 v[144:147],v6,s[56:59],s96 offen
buffer_store_dwordx4 v[148:151],v6,s[56:59],s96 offen offset:1024
buffer_store_dwordx4 v[152:155],v6,s[56:59],s96 offen offset:2048
buffer_store_dwordx4 v[156:159],v6,s[56:59],s96 offen offset:3072
s_mov_b32 s96,24576
buffer_store_dwordx4 v[160:163],v6,s[56:59],s96 offen
buffer_store_dwordx4 v[164:167],v6,s[56:59],s96 offen offset:1024
buffer_store_dwordx4 v[168:171],v6,s[56:59],s96 offen offset:2048
buffer_store_dwordx4 v[172:175],v6,s[56:59],s96 offen offset:3072
s_mov_b32 s96,28672
buffer_store_dwordx4 v[176:179],v6,s[56:59],s96 offen
buffer_store_dwordx4 v[180:183],v6,s[56:59],s96 offen offset:1024
buffer_store_dwordx4 v[184:187],v6,s[56:59],s96 offen offset:2048
buffer_store_dwordx4 v[188:191],v6,s[56:59],s96 offen offset:3072
s_waitcnt vmcnt(0)
v_writelane_b32 v173,s88,0
v_writelane_b32 v173,s89,1
v_writelane_b32 v173,s90,2
v_writelane_b32 v173,s91,3
v_writelane_b32 v173,s92,4
v_writelane_b32 v173,s93,5
v_writelane_b32 v173,s94,6
v_writelane_b32 v173,s95,7
s_mov_b32 s88,s98
s_mul_i32 s90,s41,307200
s_mov_b64 s[92:93],s[4:5]
s_add_u32 s92,s92,s90
s_addc_u32 s93,s93,0
s_and_b32 s93,s93,0xffff
s_mov_b32 s94,307200
s_mov_b32 s95,0x20000
s_mov_b64 s[96:97],s[10:11]
s_and_b32 s97,s97,0xffff
s_mov_b32 s98,5242880
s_mov_b32 s99,0x20000
v_bfe_u32 v171,v6,4,4
v_lshrrev_b32 v172,8,v6
v_lshlrev_b32 v164,8,v171
v_lshl_add_u32 v164,v172,4,v164
v_lshlrev_b32 v165,10,v172
v_lshl_add_u32 v165,v171,1,v165
v_add_u32 v166,4096,v165
v_lshlrev_b32 v168,16,v172
v_lshrrev_b32 v170,2,v171
v_lshl_add_u32 v168,v170,4,v168
v_and_b32 v170,3,v171
v_lshl_add_u32 v168,v170,1,v168
s_lshl_b32 s90,s42,1
s_add_u32 s90,s90,s88
s_lshl_b32 s90,s90,8
v_add_u32 v168,s90,v168
s_mov_b32 s89,0
L_sjb_%=:
)A"
R"A(s_add_u32 s90,s89,s70
s_add_u32 s90,s90,1
s_and_b32 s90,s90,3
s_lshl_b32 s91,s40,2
s_add_u32 s90,s90,s91
s_lshl_b32 s90,s90,4
v_bfe_u32 v171,v6,4,4
v_lshrrev_b32 v172,8,v6
v_add_u32 v167,s90,v171
v_mul_u32_u24 v167,1280,v167
v_lshl_add_u32 v167,v172,4,v167
s_lshl_b32 s90,s88,4
s_add_u32 s90,s90,s89
s_lshl_b32 s90,s90,12
v_add_u32 v169,s90,v6
v_mov_b32 v64,0
v_mov_b32 v65,0
v_mov_b32 v66,0
v_mov_b32 v67,0
v_mov_b32 v68,0
v_mov_b32 v69,0
v_mov_b32 v70,0
v_mov_b32 v71,0
v_mov_b32 v72,0
v_mov_b32 v73,0
v_mov_b32 v74,0
v_mov_b32 v75,0
v_mov_b32 v76,0
v_mov_b32 v77,0
v_mov_b32 v78,0
v_mov_b32 v79,0
v_mov_b32 v80,0
v_mov_b32 v81,0
v_mov_b32 v82,0
v_mov_b32 v83,0
v_mov_b32 v84,0
v_mov_b32 v85,0
v_mov_b32 v86,0
v_mov_b32 v87,0
v_mov_b32 v88,0
v_mov_b32 v89,0
v_mov_b32 v90,0
v_mov_b32 v91,0
v_mov_b32 v92,0
v_mov_b32 v93,0
v_mov_b32 v94,0
v_mov_b32 v95,0
s_mov_b32 s90,0
L_sq_%=:
s_lshl_b32 s91,s90,6
buffer_load_dwordx4 v[160:163],v167,s[92:95],s91 offen
s_lshl_b32 s91,s90,18
buffer_load_ushort v112,v168,s[96:99],s91 offen
buffer_load_ushort v120,v168,s[96:99],s91 offen offset:8
buffer_load_ushort v128,v168,s[96:99],s91 offen offset:64
buffer_load_ushort v136,v168,s[96:99],s91 offen offset:72
s_add_u32 s91,s91,8192
buffer_load_ushort v113,v168,s[96:99],s91 offen
buffer_load_ushort v121,v168,s[96:99],s91 offen offset:8
buffer_load_ushort v129,v168,s[96:99],s91 offen offset:64
buffer_load_ushort v137,v168,s[96:99],s91 offen offset:72
s_add_u32 s91,s91,8192
buffer_load_ushort v114,v168,s[96:99],s91 offen
buffer_load_ushort v122,v168,s[96:99],s91 offen offset:8
buffer_load_ushort v130,v168,s[96:99],s91 offen offset:64
buffer_load_ushort v138,v168,s[96:99],s91 offen offset:72
s_add_u32 s91,s91,8192
buffer_load_ushort v115,v168,s[96:99],s91 offen
buffer_load_ushort v123,v168,s[96:99],s91 offen offset:8
buffer_load_ushort v131,v168,s[96:99],s91 offen offset:64
buffer_load_ushort v139,v168,s[96:99],s91 offen offset:72
s_add_u32 s91,s91,8192
buffer_load_ushort v116,v168,s[96:99],s91 offen
buffer_load_ushort v124,v168,s[96:99],s91 offen offset:8
buffer_load_ushort v132,v168,s[96:99],s91 offen offset:64
buffer_load_ushort v140,v168,s[96:99],s91 offen offset:72
s_add_u32 s91,s91,8192
buffer_load_ushort v117,v168,s[96:99],s91 offen
buffer_load_ushort v125,v168,s[96:99],s91 offen offset:8
buffer_load_ushort v133,v168,s[96:99],s91 offen offset:64
buffer_load_ushort v141,v168,s[96:99],s91 offen offset:72
s_add_u32 s91,s91,8192
buffer_load_ushort v118,v168,s[96:99],s91 offen
buffer_load_ushort v126,v168,s[96:99],s91 offen offset:8
buffer_load_ushort v134,v168,s[96:99],s91 offen offset:64
buffer_load_ushort v142,v168,s[96:99],s91 offen offset:72
s_add_u32 s91,s91,8192
buffer_load_ushort v119,v168,s[96:99],s91 offen
buffer_load_ushort v127,v168,s[96:99],s91 offen offset:8
buffer_load_ushort v135,v168,s[96:99],s91 offen offset:64
buffer_load_ushort v143,v168,s[96:99],s91 offen offset:72
s_add_u32 s91,s91,8192
s_waitcnt vmcnt(0)
v_lshl_or_b32 v112,v113,16,v112
v_lshl_or_b32 v113,v115,16,v114
v_lshl_or_b32 v114,v117,16,v116
v_lshl_or_b32 v115,v119,16,v118
v_lshl_or_b32 v120,v121,16,v120
v_lshl_or_b32 v121,v123,16,v122
v_lshl_or_b32 v122,v125,16,v124
v_lshl_or_b32 v123,v127,16,v126
v_lshl_or_b32 v128,v129,16,v128
v_lshl_or_b32 v129,v131,16,v130
v_lshl_or_b32 v130,v133,16,v132
v_lshl_or_b32 v131,v135,16,v134
v_lshl_or_b32 v136,v137,16,v136
v_lshl_or_b32 v137,v139,16,v138
v_lshl_or_b32 v138,v141,16,v140
v_lshl_or_b32 v139,v143,16,v142
s_nop 1
v_mfma_f32_16x16x32_bf16 v[64:67],v[112:115],v[160:163],v[64:67]
v_mfma_f32_16x16x32_bf16 v[68:71],v[120:123],v[160:163],v[68:71]
v_mfma_f32_16x16x32_bf16 v[72:75],v[128:131],v[160:163],v[72:75]
v_mfma_f32_16x16x32_bf16 v[76:79],v[136:139],v[160:163],v[76:79]
s_nop 7
s_lshl_b32 s91,s90,18
buffer_load_ushort v112,v168,s[96:99],s91 offen offset:128
buffer_load_ushort v120,v168,s[96:99],s91 offen offset:136
buffer_load_ushort v128,v168,s[96:99],s91 offen offset:192
buffer_load_ushort v136,v168,s[96:99],s91 offen offset:200
s_add_u32 s91,s91,8192
buffer_load_ushort v113,v168,s[96:99],s91 offen offset:128
buffer_load_ushort v121,v168,s[96:99],s91 offen offset:136
buffer_load_ushort v129,v168,s[96:99],s91 offen offset:192
buffer_load_ushort v137,v168,s[96:99],s91 offen offset:200
s_add_u32 s91,s91,8192
buffer_load_ushort v114,v168,s[96:99],s91 offen offset:128
buffer_load_ushort v122,v168,s[96:99],s91 offen offset:136
buffer_load_ushort v130,v168,s[96:99],s91 offen offset:192
buffer_load_ushort v138,v168,s[96:99],s91 offen offset:200
s_add_u32 s91,s91,8192
buffer_load_ushort v115,v168,s[96:99],s91 offen offset:128
buffer_load_ushort v123,v168,s[96:99],s91 offen offset:136
buffer_load_ushort v131,v168,s[96:99],s91 offen offset:192
buffer_load_ushort v139,v168,s[96:99],s91 offen offset:200
s_add_u32 s91,s91,8192
buffer_load_ushort v116,v168,s[96:99],s91 offen offset:128
buffer_load_ushort v124,v168,s[96:99],s91 offen offset:136
buffer_load_ushort v132,v168,s[96:99],s91 offen offset:192
buffer_load_ushort v140,v168,s[96:99],s91 offen offset:200
s_add_u32 s91,s91,8192
buffer_load_ushort v117,v168,s[96:99],s91 offen offset:128
buffer_load_ushort v125,v168,s[96:99],s91 offen offset:136
buffer_load_ushort v133,v168,s[96:99],s91 offen offset:192
buffer_load_ushort v141,v168,s[96:99],s91 offen offset:200
s_add_u32 s91,s91,8192
buffer_load_ushort v118,v168,s[96:99],s91 offen offset:128
buffer_load_ushort v126,v168,s[96:99],s91 offen offset:136
buffer_load_ushort v134,v168,s[96:99],s91 offen offset:192
buffer_load_ushort v142,v168,s[96:99],s91 offen offset:200
s_add_u32 s91,s91,8192
buffer_load_ushort v119,v168,s[96:99],s91 offen offset:128
buffer_load_ushort v127,v168,s[96:99],s91 offen offset:136
buffer_load_ushort v135,v168,s[96:99],s91 offen offset:192
buffer_load_ushort v143,v168,s[96:99],s91 offen offset:200
s_add_u32 s91,s91,8192
s_waitcnt vmcnt(0)
v_lshl_or_b32 v112,v113,16,v112
v_lshl_or_b32 v113,v115,16,v114
v_lshl_or_b32 v114,v117,16,v116
v_lshl_or_b32 v115,v119,16,v118
v_lshl_or_b32 v120,v121,16,v120
v_lshl_or_b32 v121,v123,16,v122
v_lshl_or_b32 v122,v125,16,v124
v_lshl_or_b32 v123,v127,16,v126
v_lshl_or_b32 v128,v129,16,v128
v_lshl_or_b32 v129,v131,16,v130
v_lshl_or_b32 v130,v133,16,v132
v_lshl_or_b32 v131,v135,16,v134
v_lshl_or_b32 v136,v137,16,v136
v_lshl_or_b32 v137,v139,16,v138
v_lshl_or_b32 v138,v141,16,v140
v_lshl_or_b32 v139,v143,16,v142
s_nop 1
v_mfma_f32_16x16x32_bf16 v[80:83],v[112:115],v[160:163],v[80:83]
v_mfma_f32_16x16x32_bf16 v[84:87],v[120:123],v[160:163],v[84:87]
v_mfma_f32_16x16x32_bf16 v[88:91],v[128:131],v[160:163],v[88:91]
v_mfma_f32_16x16x32_bf16 v[92:95],v[136:139],v[160:163],v[92:95]
s_nop 7
s_add_u32 s90,s90,1
s_cmp_lg_u32 s90,20
s_cbranch_scc1 L_sq_%=
s_nop 7
v_mul_f32 v64,s61,v64
v_mul_f32 v65,s61,v65
v_mul_f32 v66,s61,v66
v_mul_f32 v67,s61,v67
v_mul_f32 v68,s61,v68
v_mul_f32 v69,s61,v69
v_mul_f32 v70,s61,v70
v_mul_f32 v71,s61,v71
v_mul_f32 v72,s61,v72
v_mul_f32 v73,s61,v73
v_mul_f32 v74,s61,v74
v_mul_f32 v75,s61,v75
v_mul_f32 v76,s61,v76
v_mul_f32 v77,s61,v77
v_mul_f32 v78,s61,v78
v_mul_f32 v79,s61,v79
v_mul_f32 v80,s61,v80
v_mul_f32 v81,s61,v81
v_mul_f32 v82,s61,v82
v_mul_f32 v83,s61,v83
v_mul_f32 v84,s61,v84
v_mul_f32 v85,s61,v85
v_mul_f32 v86,s61,v86
v_mul_f32 v87,s61,v87
v_mul_f32 v88,s61,v88
v_mul_f32 v89,s61,v89
v_mul_f32 v90,s61,v90
v_mul_f32 v91,s61,v91
v_mul_f32 v92,s61,v92
v_mul_f32 v93,s61,v93
v_mul_f32 v94,s61,v94
v_mul_f32 v95,s61,v95
v_cvt_pk_bf16_f32 v96,v64,v65
v_cvt_pk_bf16_f32 v97,v66,v67
v_cvt_pk_bf16_f32 v98,v68,v69
v_cvt_pk_bf16_f32 v99,v70,v71
v_cvt_pk_bf16_f32 v100,v72,v73
v_cvt_pk_bf16_f32 v101,v74,v75
v_cvt_pk_bf16_f32 v102,v76,v77
v_cvt_pk_bf16_f32 v103,v78,v79
v_cvt_pk_bf16_f32 v104,v80,v81
v_cvt_pk_bf16_f32 v105,v82,v83
v_cvt_pk_bf16_f32 v106,v84,v85
v_cvt_pk_bf16_f32 v107,v86,v87
v_cvt_pk_bf16_f32 v108,v88,v89
v_cvt_pk_bf16_f32 v109,v90,v91
v_cvt_pk_bf16_f32 v110,v92,v93
v_cvt_pk_bf16_f32 v111,v94,v95
v_mov_b32 v170,0xff800000
s_lshl_b32 s91,s88,18
s_mov_b32 s90,64
L_s1_%=:
buffer_load_dwordx4 v[112:115],v164,s[20:23],s91 offen
buffer_load_dwordx4 v[116:119],v164,s[20:23],s91 offen offset:64
buffer_load_dwordx4 v[120:123],v164,s[20:23],s91 offen offset:128
buffer_load_dwordx4 v[124:127],v164,s[20:23],s91 offen offset:192
s_waitcnt vmcnt(0)
v_mfma_f32_16x16x32_bf16 v[144:147],v[112:115],v[96:99],0
v_mfma_f32_16x16x32_bf16 v[144:147],v[116:119],v[100:103],v[144:147]
v_mfma_f32_16x16x32_bf16 v[144:147],v[120:123],v[104:107],v[144:147]
v_mfma_f32_16x16x32_bf16 v[144:147],v[124:127],v[108:111],v[144:147]
s_nop 7
v_max3_f32 v170,v170,v144,v145
v_max3_f32 v170,v170,v146,v147
s_add_u32 s91,s91,4096
s_sub_u32 s90,s90,1
s_cmp_lg_u32 s90,0
s_cbranch_scc1 L_s1_%=
v_mov_b32 v171,v170
s_nop 1
v_permlane32_swap_b32 v170,v171
v_max_f32 v170,v170,v171
v_mov_b32 v171,v170
s_nop 1
v_permlane16_swap_b32 v170,v171
v_max_f32 v170,v170,v171
v_sub_f32 v152,0,v170
v_mov_b32 v156,0
v_sub_f32 v153,0,v170
v_mov_b32 v157,0
v_sub_f32 v154,0,v170
v_mov_b32 v158,0
v_sub_f32 v155,0,v170
v_mov_b32 v159,0
v_mov_b32 v64,0
v_mov_b32 v65,0
v_mov_b32 v66,0
v_mov_b32 v67,0
v_mov_b32 v68,0
v_mov_b32 v69,0
v_mov_b32 v70,0
v_mov_b32 v71,0
v_mov_b32 v72,0
v_mov_b32 v73,0
v_mov_b32 v74,0
v_mov_b32 v75,0
v_mov_b32 v76,0
v_mov_b32 v77,0
v_mov_b32 v78,0
v_mov_b32 v79,0
v_mov_b32 v80,0
v_mov_b32 v81,0
v_mov_b32 v82,0
v_mov_b32 v83,0
v_mov_b32 v84,0
v_mov_b32 v85,0
v_mov_b32 v86,0
v_mov_b32 v87,0
v_mov_b32 v88,0
v_mov_b32 v89,0
v_mov_b32 v90,0
v_mov_b32 v91,0
v_mov_b32 v92,0
v_mov_b32 v93,0
v_mov_b32 v94,0
v_mov_b32 v95,0
s_lshl_b32 s91,s88,18
s_mov_b32 s90,32
v_add_u32 v172,4096,v164
L_s2_%=:
buffer_load_dwordx4 v[112:115],v164,s[20:23],s91 offen
buffer_load_dwordx4 v[116:119],v164,s[20:23],s91 offen offset:64
buffer_load_dwordx4 v[120:123],v164,s[20:23],s91 offen offset:128
buffer_load_dwordx4 v[124:127],v164,s[20:23],s91 offen offset:192
buffer_load_dwordx4 v[128:131],v172,s[20:23],s91 offen
buffer_load_dwordx4 v[132:135],v172,s[20:23],s91 offen offset:64
buffer_load_dwordx4 v[136:139],v172,s[20:23],s91 offen offset:128
buffer_load_dwordx4 v[140:143],v172,s[20:23],s91 offen offset:192
s_waitcnt vmcnt(0)
v_mfma_f32_16x16x32_bf16 v[144:147],v[112:115],v[96:99],v[152:155]
v_mfma_f32_16x16x32_bf16 v[144:147],v[116:119],v[100:103],v[144:147]
v_mfma_f32_16x16x32_bf16 v[144:147],v[120:123],v[104:107],v[144:147]
v_mfma_f32_16x16x32_bf16 v[144:147],v[124:127],v[108:111],v[144:147]
v_mfma_f32_16x16x32_bf16 v[148:151],v[128:131],v[96:99],v[152:155]
v_mfma_f32_16x16x32_bf16 v[148:151],v[132:135],v[100:103],v[148:151]
v_mfma_f32_16x16x32_bf16 v[148:151],v[136:139],v[104:107],v[148:151]
v_mfma_f32_16x16x32_bf16 v[148:151],v[140:143],v[108:111],v[148:151]
s_nop 7
v_exp_f32 v144,v144
v_exp_f32 v145,v145
v_exp_f32 v146,v146
v_exp_f32 v147,v147
v_exp_f32 v148,v148
v_exp_f32 v149,v149
v_exp_f32 v150,v150
v_exp_f32 v151,v151
s_nop 0
v_cvt_pk_bf16_f32 v144,v144,v145
v_cvt_pk_bf16_f32 v145,v146,v147
v_cvt_pk_bf16_f32 v146,v148,v149
v_cvt_pk_bf16_f32 v147,v150,v151
buffer_load_ushort v112,v165,s[24:27],s91 offen
buffer_load_ushort v113,v165,s[24:27],s91 offen offset:256
buffer_load_ushort v114,v165,s[24:27],s91 offen offset:512
buffer_load_ushort v115,v165,s[24:27],s91 offen offset:768
buffer_load_ushort v116,v166,s[24:27],s91 offen
buffer_load_ushort v117,v166,s[24:27],s91 offen offset:256
buffer_load_ushort v118,v166,s[24:27],s91 offen offset:512
buffer_load_ushort v119,v166,s[24:27],s91 offen offset:768
buffer_load_ushort v120,v165,s[24:27],s91 offen offset:32
buffer_load_ushort v121,v165,s[24:27],s91 offen offset:288
buffer_load_ushort v122,v165,s[24:27],s91 offen offset:544
buffer_load_ushort v123,v165,s[24:27],s91 offen offset:800
buffer_load_ushort v124,v166,s[24:27],s91 offen offset:32
buffer_load_ushort v125,v166,s[24:27],s91 offen offset:288
buffer_load_ushort v126,v166,s[24:27],s91 offen offset:544
buffer_load_ushort v127,v166,s[24:27],s91 offen offset:800
buffer_load_ushort v128,v165,s[24:27],s91 offen offset:64
buffer_load_ushort v129,v165,s[24:27],s91 offen offset:320
buffer_load_ushort v130,v165,s[24:27],s91 offen offset:576
)A"
R"A(buffer_load_ushort v131,v165,s[24:27],s91 offen offset:832
buffer_load_ushort v132,v166,s[24:27],s91 offen offset:64
buffer_load_ushort v133,v166,s[24:27],s91 offen offset:320
buffer_load_ushort v134,v166,s[24:27],s91 offen offset:576
buffer_load_ushort v135,v166,s[24:27],s91 offen offset:832
buffer_load_ushort v136,v165,s[24:27],s91 offen offset:96
buffer_load_ushort v137,v165,s[24:27],s91 offen offset:352
buffer_load_ushort v138,v165,s[24:27],s91 offen offset:608
buffer_load_ushort v139,v165,s[24:27],s91 offen offset:864
buffer_load_ushort v140,v166,s[24:27],s91 offen offset:96
buffer_load_ushort v141,v166,s[24:27],s91 offen offset:352
buffer_load_ushort v142,v166,s[24:27],s91 offen offset:608
buffer_load_ushort v143,v166,s[24:27],s91 offen offset:864
s_waitcnt vmcnt(0)
v_lshl_or_b32 v112,v113,16,v112
v_lshl_or_b32 v113,v115,16,v114
v_lshl_or_b32 v114,v117,16,v116
v_lshl_or_b32 v115,v119,16,v118
v_lshl_or_b32 v120,v121,16,v120
v_lshl_or_b32 v121,v123,16,v122
v_lshl_or_b32 v122,v125,16,v124
v_lshl_or_b32 v123,v127,16,v126
v_lshl_or_b32 v128,v129,16,v128
v_lshl_or_b32 v129,v131,16,v130
v_lshl_or_b32 v130,v133,16,v132
v_lshl_or_b32 v131,v135,16,v134
v_lshl_or_b32 v136,v137,16,v136
v_lshl_or_b32 v137,v139,16,v138
v_lshl_or_b32 v138,v141,16,v140
v_lshl_or_b32 v139,v143,16,v142
s_nop 1
v_mfma_f32_16x16x32_bf16 v[64:67],v[112:115],v[144:147],v[64:67]
v_mfma_f32_16x16x32_bf16 v[68:71],v[120:123],v[144:147],v[68:71]
v_mfma_f32_16x16x32_bf16 v[72:75],v[128:131],v[144:147],v[72:75]
v_mfma_f32_16x16x32_bf16 v[76:79],v[136:139],v[144:147],v[76:79]
s_nop 7
buffer_load_ushort v112,v165,s[24:27],s91 offen offset:128
buffer_load_ushort v113,v165,s[24:27],s91 offen offset:384
buffer_load_ushort v114,v165,s[24:27],s91 offen offset:640
buffer_load_ushort v115,v165,s[24:27],s91 offen offset:896
buffer_load_ushort v116,v166,s[24:27],s91 offen offset:128
buffer_load_ushort v117,v166,s[24:27],s91 offen offset:384
buffer_load_ushort v118,v166,s[24:27],s91 offen offset:640
buffer_load_ushort v119,v166,s[24:27],s91 offen offset:896
buffer_load_ushort v120,v165,s[24:27],s91 offen offset:160
buffer_load_ushort v121,v165,s[24:27],s91 offen offset:416
buffer_load_ushort v122,v165,s[24:27],s91 offen offset:672
buffer_load_ushort v123,v165,s[24:27],s91 offen offset:928
buffer_load_ushort v124,v166,s[24:27],s91 offen offset:160
buffer_load_ushort v125,v166,s[24:27],s91 offen offset:416
buffer_load_ushort v126,v166,s[24:27],s91 offen offset:672
buffer_load_ushort v127,v166,s[24:27],s91 offen offset:928
buffer_load_ushort v128,v165,s[24:27],s91 offen offset:192
buffer_load_ushort v129,v165,s[24:27],s91 offen offset:448
buffer_load_ushort v130,v165,s[24:27],s91 offen offset:704
buffer_load_ushort v131,v165,s[24:27],s91 offen offset:960
buffer_load_ushort v132,v166,s[24:27],s91 offen offset:192
buffer_load_ushort v133,v166,s[24:27],s91 offen offset:448
buffer_load_ushort v134,v166,s[24:27],s91 offen offset:704
buffer_load_ushort v135,v166,s[24:27],s91 offen offset:960
buffer_load_ushort v136,v165,s[24:27],s91 offen offset:224
buffer_load_ushort v137,v165,s[24:27],s91 offen offset:480
buffer_load_ushort v138,v165,s[24:27],s91 offen offset:736
buffer_load_ushort v139,v165,s[24:27],s91 offen offset:992
buffer_load_ushort v140,v166,s[24:27],s91 offen offset:224
buffer_load_ushort v141,v166,s[24:27],s91 offen offset:480
buffer_load_ushort v142,v166,s[24:27],s91 offen offset:736
buffer_load_ushort v143,v166,s[24:27],s91 offen offset:992
s_waitcnt vmcnt(0)
v_lshl_or_b32 v112,v113,16,v112
v_lshl_or_b32 v113,v115,16,v114
v_lshl_or_b32 v114,v117,16,v116
v_lshl_or_b32 v115,v119,16,v118
v_lshl_or_b32 v120,v121,16,v120
v_lshl_or_b32 v121,v123,16,v122
v_lshl_or_b32 v122,v125,16,v124
v_lshl_or_b32 v123,v127,16,v126
v_lshl_or_b32 v128,v129,16,v128
v_lshl_or_b32 v129,v131,16,v130
v_lshl_or_b32 v130,v133,16,v132
v_lshl_or_b32 v131,v135,16,v134
v_lshl_or_b32 v136,v137,16,v136
v_lshl_or_b32 v137,v139,16,v138
v_lshl_or_b32 v138,v141,16,v140
v_lshl_or_b32 v139,v143,16,v142
s_nop 1
v_mfma_f32_16x16x32_bf16 v[80:83],v[112:115],v[144:147],v[80:83]
v_mfma_f32_16x16x32_bf16 v[84:87],v[120:123],v[144:147],v[84:87]
v_mfma_f32_16x16x32_bf16 v[88:91],v[128:131],v[144:147],v[88:91]
v_mfma_f32_16x16x32_bf16 v[92:95],v[136:139],v[144:147],v[92:95]
s_nop 7
v_mfma_f32_16x16x32_bf16 v[156:159],v[28:31],v[144:147],v[156:159]
s_nop 7
s_add_u32 s91,s91,8192
s_sub_u32 s90,s90,1
s_cmp_lg_u32 s90,0
s_cbranch_scc1 L_s2_%=
v_rcp_f32 v170,v156
s_nop 0
v_mul_f32 v64,v170,v64
v_mul_f32 v65,v170,v65
v_mul_f32 v66,v170,v66
v_mul_f32 v67,v170,v67
v_mul_f32 v68,v170,v68
v_mul_f32 v69,v170,v69
v_mul_f32 v70,v170,v70
v_mul_f32 v71,v170,v71
v_mul_f32 v72,v170,v72
v_mul_f32 v73,v170,v73
v_mul_f32 v74,v170,v74
v_mul_f32 v75,v170,v75
v_mul_f32 v76,v170,v76
v_mul_f32 v77,v170,v77
v_mul_f32 v78,v170,v78
v_mul_f32 v79,v170,v79
v_mul_f32 v80,v170,v80
v_mul_f32 v81,v170,v81
v_mul_f32 v82,v170,v82
v_mul_f32 v83,v170,v83
v_mul_f32 v84,v170,v84
v_mul_f32 v85,v170,v85
v_mul_f32 v86,v170,v86
v_mul_f32 v87,v170,v87
v_mul_f32 v88,v170,v88
v_mul_f32 v89,v170,v89
v_mul_f32 v90,v170,v90
v_mul_f32 v91,v170,v91
v_mul_f32 v92,v170,v92
v_mul_f32 v93,v170,v93
v_mul_f32 v94,v170,v94
v_mul_f32 v95,v170,v95
v_cvt_pk_bf16_f32 v96,v64,v65
v_cvt_pk_bf16_f32 v97,v66,v67
v_cvt_pk_bf16_f32 v98,v68,v69
v_cvt_pk_bf16_f32 v99,v70,v71
v_cvt_pk_bf16_f32 v100,v72,v73
v_cvt_pk_bf16_f32 v101,v74,v75
v_cvt_pk_bf16_f32 v102,v76,v77
v_cvt_pk_bf16_f32 v103,v78,v79
v_cvt_pk_bf16_f32 v104,v80,v81
v_cvt_pk_bf16_f32 v105,v82,v83
v_cvt_pk_bf16_f32 v106,v84,v85
v_cvt_pk_bf16_f32 v107,v86,v87
v_cvt_pk_bf16_f32 v108,v88,v89
v_cvt_pk_bf16_f32 v109,v90,v91
v_cvt_pk_bf16_f32 v110,v92,v93
v_cvt_pk_bf16_f32 v111,v94,v95
buffer_store_dwordx4 v[96:99],v169,s[36:39],s55 offen sc1
buffer_store_dwordx4 v[100:103],v169,s[36:39],s55 offen offset:1024 sc1
buffer_store_dwordx4 v[104:107],v169,s[36:39],s55 offen offset:2048 sc1
buffer_store_dwordx4 v[108:111],v169,s[36:39],s55 offen offset:3072 sc1
s_waitcnt vmcnt(0)
s_add_u32 s89,s89,1
s_cmp_lg_u32 s89,4
s_cbranch_scc1 L_sjb_%=
s_mov_b32 s98,s88
v_readlane_b32 s88,v173,0
v_readlane_b32 s89,v173,1
v_readlane_b32 s90,v173,2
v_readlane_b32 s91,v173,3
v_readlane_b32 s92,v173,4
v_readlane_b32 s93,v173,5
v_readlane_b32 s94,v173,6
v_readlane_b32 s95,v173,7
s_nop 4
s_mov_b32 s96,0
buffer_load_dwordx4 v[64:67],v6,s[56:59],s96 offen sc1
buffer_load_dwordx4 v[68:71],v6,s[56:59],s96 offen offset:1024 sc1
buffer_load_dwordx4 v[72:75],v6,s[56:59],s96 offen offset:2048 sc1
buffer_load_dwordx4 v[76:79],v6,s[56:59],s96 offen offset:3072 sc1
s_mov_b32 s96,4096
buffer_load_dwordx4 v[80:83],v6,s[56:59],s96 offen sc1
buffer_load_dwordx4 v[84:87],v6,s[56:59],s96 offen offset:1024 sc1
buffer_load_dwordx4 v[88:91],v6,s[56:59],s96 offen offset:2048 sc1
buffer_load_dwordx4 v[92:95],v6,s[56:59],s96 offen offset:3072 sc1
s_mov_b32 s96,8192
buffer_load_dwordx4 v[96:99],v6,s[56:59],s96 offen sc1
buffer_load_dwordx4 v[100:103],v6,s[56:59],s96 offen offset:1024 sc1
buffer_load_dwordx4 v[104:107],v6,s[56:59],s96 offen offset:2048 sc1
buffer_load_dwordx4 v[108:111],v6,s[56:59],s96 offen offset:3072 sc1
s_mov_b32 s96,12288
buffer_load_dwordx4 v[112:115],v6,s[56:59],s96 offen sc1
buffer_load_dwordx4 v[116:119],v6,s[56:59],s96 offen offset:1024 sc1
buffer_load_dwordx4 v[120:123],v6,s[56:59],s96 offen offset:2048 sc1
buffer_load_dwordx4 v[124:127],v6,s[56:59],s96 offen offset:3072 sc1
s_mov_b32 s96,16384
buffer_load_dwordx4 v[128:131],v6,s[56:59],s96 offen sc1
buffer_load_dwordx4 v[132:135],v6,s[56:59],s96 offen offset:1024 sc1
buffer_load_dwordx4 v[136:139],v6,s[56:59],s96 offen offset:2048 sc1
buffer_load_dwordx4 v[140:143],v6,s[56:59],s96 offen offset:3072 sc1
s_mov_b32 s96,20480
buffer_load_dwordx4 v[144:147],v6,s[56:59],s96 offen sc1
buffer_load_dwordx4 v[148:151],v6,s[56:59],s96 offen offset:1024 sc1
buffer_load_dwordx4 v[152:155],v6,s[56:59],s96 offen offset:2048 sc1
buffer_load_dwordx4 v[156:159],v6,s[56:59],s96 offen offset:3072 sc1
s_mov_b32 s96,24576
buffer_load_dwordx4 v[160:163],v6,s[56:59],s96 offen sc1
buffer_load_dwordx4 v[164:167],v6,s[56:59],s96 offen offset:1024 sc1
buffer_load_dwordx4 v[168:171],v6,s[56:59],s96 offen offset:2048 sc1
buffer_load_dwordx4 v[172:175],v6,s[56:59],s96 offen offset:3072 sc1
s_mov_b32 s96,28672
buffer_load_dwordx4 v[176:179],v6,s[56:59],s96 offen sc1
buffer_load_dwordx4 v[180:183],v6,s[56:59],s96 offen offset:1024 sc1
buffer_load_dwordx4 v[184:187],v6,s[56:59],s96 offen offset:2048 sc1
buffer_load_dwordx4 v[188:191],v6,s[56:59],s96 offen offset:3072 sc1
s_waitcnt vmcnt(0)
s_cmp_lg_u32 s98,0
s_cbranch_scc1 L_sr1_%=
buffer_load_dwordx4 v[64:67],v6,s[36:39],s84 offen sc1
buffer_load_dwordx4 v[68:71],v6,s[36:39],s84 offen offset:1024 sc1
buffer_load_dwordx4 v[72:75],v6,s[36:39],s84 offen offset:2048 sc1
buffer_load_dwordx4 v[76:79],v6,s[36:39],s84 offen offset:3072 sc1
buffer_load_dwordx4 v[80:83],v6,s[36:39],s85 offen sc1
buffer_load_dwordx4 v[84:87],v6,s[36:39],s85 offen offset:1024 sc1
buffer_load_dwordx4 v[88:91],v6,s[36:39],s85 offen offset:2048 sc1
buffer_load_dwordx4 v[92:95],v6,s[36:39],s85 offen offset:3072 sc1
buffer_load_dwordx4 v[96:99],v6,s[36:39],s86 offen sc1
buffer_load_dwordx4 v[100:103],v6,s[36:39],s86 offen offset:1024 sc1
buffer_load_dwordx4 v[104:107],v6,s[36:39],s86 offen offset:2048 sc1
buffer_load_dwordx4 v[108:111],v6,s[36:39],s86 offen offset:3072 sc1
buffer_load_dwordx4 v[112:115],v6,s[36:39],s87 offen sc1
buffer_load_dwordx4 v[116:119],v6,s[36:39],s87 offen offset:1024 sc1
buffer_load_dwordx4 v[120:123],v6,s[36:39],s87 offen offset:2048 sc1
buffer_load_dwordx4 v[124:127],v6,s[36:39],s87 offen offset:3072 sc1
s_waitcnt vmcnt(0)
s_nop 7
s_branch L_sret0_%=
L_sr1_%=:
buffer_load_dwordx4 v[136:139],v6,s[36:39],s84 offen sc1
buffer_load_dwordx4 v[140:143],v6,s[36:39],s84 offen offset:1024 sc1
buffer_load_dwordx4 v[152:155],v6,s[36:39],s84 offen offset:2048 sc1
buffer_load_dwordx4 v[156:159],v6,s[36:39],s84 offen offset:3072 sc1
buffer_load_dwordx4 v[168:171],v6,s[36:39],s85 offen sc1
buffer_load_dwordx4 v[172:175],v6,s[36:39],s85 offen offset:1024 sc1
buffer_load_dwordx4 v[184:187],v6,s[36:39],s85 offen offset:2048 sc1
buffer_load_dwordx4 v[188:191],v6,s[36:39],s85 offen offset:3072 sc1
buffer_load_dwordx4 v[200:203],v6,s[36:39],s86 offen sc1
buffer_load_dwordx4 v[204:207],v6,s[36:39],s86 offen offset:1024 sc1
buffer_load_dwordx4 v[216:219],v6,s[36:39],s86 offen offset:2048 sc1
buffer_load_dwordx4 v[220:223],v6,s[36:39],s86 offen offset:3072 sc1
buffer_load_dwordx4 v[232:235],v6,s[36:39],s87 offen sc1
buffer_load_dwordx4 v[236:239],v6,s[36:39],s87 offen offset:1024 sc1
buffer_load_dwordx4 v[248:251],v6,s[36:39],s87 offen offset:2048 sc1
buffer_load_dwordx4 v[252:255],v6,s[36:39],s87 offen offset:3072 sc1
s_waitcnt vmcnt(0)
s_nop 7
s_branch L_sret1_%=
L_end_%=:
)A"
    :: "s"(karg), "s"(blk), "s"(wid)
    : "v0", "v1", "v2", "v3", "v4", "v5", "v6", "v7", "v8", "v9", "v10", "v11", "v12", "v13", "v14", "v15", "v16", "v17", "v18", "v19", "v20", "v21", "v22", "v23", "v24", "v25", "v26", "v27", "v28", "v29", "v30", "v31", "v32", "v33", "v34", "v35", "v36", "v37", "v38", "v39", "v40", "v41", "v42", "v43", "v44", "v45", "v46", "v47", "v48", "v49", "v50", "v51", "v52", "v53", "v54", "v55", "v56", "v57", "v58", "v59", "v60", "v61", "v62", "v63", "v64", "v65", "v66", "v67", "v68", "v69", "v70", "v71", "v72", "v73", "v74", "v75", "v76", "v77", "v78", "v79", "v80", "v81", "v82", "v83", "v84", "v85", "v86", "v87", "v88", "v89", "v90", "v91", "v92", "v93", "v94", "v95", "v96", "v97", "v98", "v99", "v100", "v101", "v102", "v103", "v104", "v105", "v106", "v107", "v108", "v109", "v110", "v111", "v112", "v113", "v114", "v115", "v116", "v117", "v118", "v119", "v120", "v121", "v122", "v123", "v124", "v125", "v126", "v127", "v128", "v129", "v130", "v131", "v132", "v133", "v134", "v135", "v136", "v137", "v138", "v139", "v140", "v141", "v142", "v143", "v144", "v145", "v146", "v147", "v148", "v149", "v150", "v151", "v152", "v153", "v154", "v155", "v156", "v157", "v158", "v159", "v160", "v161", "v162", "v163", "v164", "v165", "v166", "v167", "v168", "v169", "v170", "v171", "v172", "v173", "v174", "v175", "v176", "v177", "v178", "v179", "v180", "v181", "v182", "v183", "v184", "v185", "v186", "v187", "v188", "v189", "v190", "v191", "v192", "v193", "v194", "v195", "v196", "v197", "v198", "v199", "v200", "v201", "v202", "v203", "v204", "v205", "v206", "v207", "v208", "v209", "v210", "v211", "v212", "v213", "v214", "v215", "v216", "v217", "v218", "v219", "v220", "v221", "v222", "v223", "v224", "v225", "v226", "v227", "v228", "v229", "v230", "v231", "v232", "v233", "v234", "v235", "v236", "v237", "v238", "v239", "v240", "v241", "v242", "v243", "v244", "v245", "v246", "v247", "v248", "v249", "v250", "v251", "v252", "v253", "v254", "v255", "a0", "a1", "a2", "a3", "a4", "a5", "a6", "a7", "a8", "a9", "a10", "a11", "a12", "a13", "a14", "a15", "a16", "a17", "a18", "a19", "a20", "a21", "a22", "a23", "a24", "a25", "a26", "a27", "a28", "a29", "a30", "a31", "a32", "a33", "a34", "a35", "a36", "a37", "a38", "a39", "a40", "a41", "a42", "a43", "a44", "a45", "a46", "a47", "a48", "a49", "a50", "a51", "a52", "a53", "a54", "a55", "a56", "a57", "a58", "a59", "a60", "a61", "a62", "a63", "a64", "a65", "a66", "a67", "a68", "a69", "a70", "a71", "a72", "a73", "a74", "a75", "a76", "a77", "a78", "a79", "a80", "a81", "a82", "a83", "a84", "a85", "a86", "a87", "a88", "a89", "a90", "a91", "a92", "a93", "a94", "a95", "a96", "a97", "a98", "a99", "a100", "a101", "a102", "a103", "a104", "a105", "a106", "a107", "a108", "a109", "a110", "a111", "a112", "a113", "a114", "a115", "a116", "a117", "a118", "a119", "a120", "a121", "a122", "a123", "a124", "a125", "a126", "a127", "a128", "a129", "a130", "a131", "a132", "a133", "a134", "a135", "a136", "a137", "a138", "a139", "a140", "a141", "a142", "a143", "a144", "a145", "a146", "a147", "a148", "a149", "a150", "a151", "a152", "a153", "a154", "a155", "a156", "a157", "a158", "a159", "a160", "a161", "a162", "a163", "a164", "a165", "a166", "a167", "a168", "a169", "a170", "a171", "a172", "a173", "a174", "a175", "a176", "a177", "a178", "a179", "a180", "a181", "a182", "a183", "a184", "a185", "a186", "a187", "a188", "a189", "a190", "a191", "a192", "a193", "a194", "a195", "a196", "a197", "a198", "a199", "a200", "a201", "a202", "a203", "a204", "a205", "a206", "a207", "a208", "a209", "a210", "a211", "a212", "a213", "a214", "a215", "a216", "a217", "a218", "a219", "a220", "a221", "a222", "a223", "a224", "a225", "a226", "a227", "a228", "a229", "a230", "a231", "a232", "a233", "a234", "a235", "a236", "a237", "a238", "a239", "a240", "a241", "a242", "a243", "a244", "a245", "a246", "a247", "a248", "a249", "a250", "a251", "a252", "a253", "a254", "a255", "s4", "s5", "s6", "s7", "s8", "s9", "s10", "s11", "s12", "s13", "s14", "s15", "s16", "s17", "s18", "s19", "s20", "s21", "s22", "s23", "s24", "s25", "s26", "s27", "s28", "s29", "s30", "s31", "s32", "s33", "s34", "s35", "s36", "s37", "s38", "s39", "s40", "s41", "s42", "s43", "s44", "s45", "s46", "s47", "s48", "s49", "s50", "s51", "s52", "s53", "s54", "s55", "s56", "s57", "s58", "s59", "s60", "s61", "s62", "s63", "s64", "s65", "s66", "s67", "s68", "s69", "s70", "s71", "s72", "s73", "s74", "s75", "s76", "s77", "s78", "s79", "s80", "s81", "s82", "s83", "s84", "s85", "s86", "s87", "s88", "s89", "s90", "s91", "s92", "s93", "s94", "s95", "s96", "s97", "s98", "s99", "s100", "s101", "vcc", "scc", "memory");
#endif
}

extern "C" void kernel_launch(void* const* d_in, const int* in_sizes, int n_in,
                              void* d_out, int out_size, void* d_ws, size_t ws_size,
                              hipStream_t stream) {
  (void)hipFuncSetAttribute((const void*)qattn_kernel, hipFuncAttributeMaxDynamicSharedMemorySize, LDS_ATT);
  qattn_kernel<<<256, 256, LDS_ATT, stream>>>(d_in[0], d_in[1], d_in[2], d_in[3], d_ws, d_in[4], d_out);
}
```

```cpp
#include <hip/hip_runtime.h>
#include <hip/hip_bf16.h>

constexpr int LDS_ATT = 163840;

__global__ __launch_bounds__(256, 1) void qattn_kernel(const void* __restrict__ x, const void* __restrict__ kc, const void* __restrict__ vc,
                                                       const void* __restrict__ wq, void* __restrict__ ws, const void* __restrict__ wo,
                                                       void* __restrict__ out) {
#if defined(__HIP_DEVICE_COMPILE__)
  const auto karg = __builtin_amdgcn_kernarg_segment_ptr();
  const unsigned blk = blockIdx.x;
  const unsigned wid = __builtin_amdgcn_readfirstlane(threadIdx.x >> 6);
  asm volatile(
R"A(s_memtime s[72:73]
s_memrealtime s[74:75]
s_load_dwordx8 s[4:11],%0,0x0
s_load_dwordx4 s[12:15],%0,0x20
s_load_dwordx2 s[16:17],%0,0x30
s_mov_b32 s40,%2
s_mov_b32 s43,%1
s_lshr_b32 s45,s43,3
v_mbcnt_lo_u32_b32 v12,-1,0
v_mbcnt_hi_u32_b32 v12,-1,v12
s_getpc_b64 s[88:89]
L_pc_%=:
s_and_b32 s88,s88,0xffffff80
s_and_b32 s89,s89,0xffff
s_mov_b32 s90,L_fast_end_%=-L_pc_%=
s_mov_b32 s91,0x20000
s_lshl_b32 s44,s45,2
s_add_u32 s44,s44,s40
s_mul_i32 s44,s44,896
v_lshlrev_b32 v67,7,v12
s_mov_b64 exec,127
buffer_load_dword v19,v67,s[88:91],s44 offen
s_mov_b64 exec,-1
s_and_b32 s70,s43,3
s_and_b32 s71,s45,3
s_lshl_b32 s42,s70,2
s_add_u32 s42,s42,s71
s_and_b32 s41,s43,4
s_lshl_b32 s41,s41,1
s_lshr_b32 s46,s45,2
s_add_u32 s41,s41,s46
s_lshl_b32 s56,s42,9
s_mov_b32 s57,0
s_lshl_b32 s59,s40,13
s_add_u32 s58,s59,65536
v_and_b32 v64,3,v12
v_bfe_u32 v65,v12,2,2
v_lshrrev_b32 v66,4,v12
s_lshl_b32 s44,s40,17
v_lshlrev_b32 v75,13,v65
v_lshl_add_u32 v75,v66,6,v75
v_lshl_add_u32 v75,v64,4,v75
v_add_u32 v75,s44,v75
v_add_u32 v56,0,v75
v_add_u32 v57,256,v75
v_add_u32 v58,32768,v75
v_add_u32 v59,33024,v75
v_add_u32 v60,65536,v75
v_add_u32 v61,65792,v75
v_add_u32 v62,98304,v75
v_add_u32 v63,98560,v75
s_waitcnt lgkmcnt(0)
s_mov_b64 s[32:33],s[10:11]
s_and_b32 s33,s33,0xffff
s_mov_b32 s34,5242880
s_mov_b32 s35,0x20000
s_add_u32 m0,s58,1024
s_nop 0
buffer_load_dwordx4 v57,s[32:35],s56 offen lds
s_add_u32 m0,s58,3072
s_nop 0
buffer_load_dwordx4 v59,s[32:35],s56 offen lds
s_add_u32 m0,s58,5120
s_nop 0
buffer_load_dwordx4 v61,s[32:35],s56 offen lds
s_add_u32 m0,s58,7168
s_nop 0
buffer_load_dwordx4 v63,s[32:35],s56 offen lds
v_lshrrev_b32 v75,3,v12
s_lshl_b32 s44,s40,6
v_add_u32 v75,s44,v75
v_mul_u32_u24 v75,1280,v75
v_and_b32 v76,7,v12
s_add_u32 s84,s70,1
s_and_b32 s84,s84,3
s_mul_i32 s84,s84,20480
s_add_u32 s84,s84,4096
s_add_u32 s85,s70,2
s_and_b32 s85,s85,3
s_mul_i32 s85,s85,20480
s_add_u32 s85,s85,2048
s_add_u32 s86,s70,3
s_and_b32 s86,s86,3
s_mul_i32 s86,s86,20480
s_add_u32 s86,s86,4096
s_add_u32 s87,s70,4
s_and_b32 s87,s87,3
s_mul_i32 s87,s87,20480
s_add_u32 s87,s87,2048
v_or_b32 v77,0,v66
v_xor_b32 v77,v77,v76
v_lshl_add_u32 v77,v77,4,v75
v_add_u32 v48,s84,v77
v_add_u32 v50,s85,v77
v_add_u32 v52,s86,v77
v_add_u32 v54,s87,v77
v_or_b32 v77,4,v66
v_xor_b32 v77,v77,v76
v_lshl_add_u32 v77,v77,4,v75
v_add_u32 v77,9216,v77
v_add_u32 v49,s84,v77
v_add_u32 v51,s85,v77
v_add_u32 v53,s86,v77
v_add_u32 v55,s87,v77
s_mul_i32 s44,s41,307200
s_mov_b64 s[28:29],s[4:5]
s_add_u32 s28,s28,s44
s_addc_u32 s29,s29,0
s_sub_u32 s28,s28,4096
s_subb_u32 s29,s29,0
s_and_b32 s29,s29,0xffff
s_mov_b32 s30,311296
s_mov_b32 s31,0x20000
s_add_u32 m0,s59,0
s_nop 0
buffer_load_dwordx4 v48,s[28:31],s57 offen lds
buffer_load_dwordx4 v49,s[28:31],s57 offen offset:1024 lds
buffer_load_dwordx4 v50,s[28:31],s57 offen offset:2048 lds
buffer_load_dwordx4 v51,s[28:31],s57 offen offset:3072 lds
s_add_u32 m0,s59,4096
s_nop 0
buffer_load_dwordx4 v52,s[28:31],s57 offen lds
buffer_load_dwordx4 v53,s[28:31],s57 offen offset:1024 lds
buffer_load_dwordx4 v54,s[28:31],s57 offen offset:2048 lds
buffer_load_dwordx4 v55,s[28:31],s57 offen offset:3072 lds
s_add_u32 s57,s57,128
s_xor_b32 s59,s59,131072
s_add_u32 m0,s58,0
s_nop 0
buffer_load_dwordx4 v56,s[32:35],s56 offen lds
s_add_u32 m0,s58,2048
s_nop 0
buffer_load_dwordx4 v58,s[32:35],s56 offen lds
s_add_u32 m0,s58,4096
s_nop 0
buffer_load_dwordx4 v60,s[32:35],s56 offen lds
s_add_u32 m0,s58,6144
s_nop 0
buffer_load_dwordx4 v62,s[32:35],s56 offen lds
s_add_u32 s56,s56,524288
s_add_u32 s58,s58,32768
s_cmp_ge_u32 s58,131072
s_cselect_b32 s62,98304,0
s_sub_u32 s58,s58,s62
s_add_u32 m0,s58,0
s_nop 0
buffer_load_dwordx4 v56,s[32:35],s56 offen lds
s_add_u32 m0,s58,1024
s_nop 0
buffer_load_dwordx4 v57,s[32:35],s56 offen lds
s_add_u32 m0,s58,2048
s_nop 0
buffer_load_dwordx4 v58,s[32:35],s56 offen lds
s_add_u32 m0,s58,3072
s_nop 0
buffer_load_dwordx4 v59,s[32:35],s56 offen lds
s_add_u32 m0,s58,4096
s_nop 0
buffer_load_dwordx4 v60,s[32:35],s56 offen lds
s_add_u32 m0,s58,5120
s_nop 0
buffer_load_dwordx4 v61,s[32:35],s56 offen lds
s_add_u32 m0,s58,6144
s_nop 0
buffer_load_dwordx4 v62,s[32:35],s56 offen lds
s_add_u32 m0,s58,7168
s_nop 0
buffer_load_dwordx4 v63,s[32:35],s56 offen lds
s_add_u32 s56,s56,524288
s_add_u32 s58,s58,32768
s_cmp_ge_u32 s58,131072
s_cselect_b32 s62,98304,0
s_sub_u32 s58,s58,s62
s_mov_b32 s60,65536
v_lshlrev_b32 v6,4,v12
v_lshlrev_b32 v72,12,v66
v_lshl_add_u32 v72,v65,6,v72
v_lshl_add_u32 v23,v64,4,v72
v_add_u32 v22,65536,v23
v_and_b32 v74,15,v12
v_lshrrev_b32 v76,1,v74
v_lshlrev_b32 v75,7,v74
s_lshl_b32 s44,s40,13
v_add_u32 v75,s44,v75
v_or_b32 v77,0,v66
v_xor_b32 v77,v77,v76
v_lshl_add_u32 v20,v77,4,v75
v_or_b32 v77,4,v66
v_xor_b32 v77,v77,v76
v_lshl_add_u32 v21,v77,4,v75
s_waitcnt vmcnt(12)
s_nop 0
s_memtime s[64:65]
s_memrealtime s[66:67]
s_bfe_u32 s68,%1,327683
s_waitcnt lgkmcnt(0)
s_sub_u32 s64,s64,s72
s_sub_u32 s66,s66,s74
s_mul_i32 s64,s64,10
s_mul_i32 s66,s66,215
s_cmp_lt_u32 s64,s66
s_cselect_b32 s95,64,0
s_cbranch_scc1 L_se1_%=
L_sg1_%=:
s_cmp_eq_u32 s68,0
s_cbranch_scc1 L_se1_%=
s_sleep 4
s_sub_u32 s68,s68,1
s_branch L_sg1_%=
L_se1_%=:
s_barrier
ds_read_b128 a[192:195],v20
ds_read_b64_tr_b16 a[128:129],v22 offset:1024
ds_read_b64_tr_b16 a[130:131],v22 offset:3072
ds_read_b128 a[196:199],v21
ds_read_b64_tr_b16 a[132:133],v22 offset:17408
ds_read_b64_tr_b16 a[134:135],v22 offset:19456
ds_read_b128 a[204:207],v21 offset:2048
ds_read_b128 a[200:203],v20 offset:2048
ds_read_b128 a[208:211],v20 offset:4096
ds_read_b128 a[212:215],v21 offset:4096
ds_read_b128 a[220:223],v21 offset:6144
ds_read_b128 a[216:219],v20 offset:6144
ds_read_b64_tr_b16 a[136:137],v22 offset:1032
ds_read_b64_tr_b16 a[138:139],v22 offset:3080
s_waitcnt lgkmcnt(11)
v_mfma_f32_16x16x32_bf16 v[128:131],a[128:131],a[192:195],0
s_add_u32 m0,s59,0
ds_read_b64_tr_b16 a[140:141],v22 offset:17416
ds_read_b64_tr_b16 a[142:143],v22 offset:19464
s_waitcnt lgkmcnt(10)
v_mfma_f32_16x16x32_bf16 v[128:131],a[132:135],a[196:199],v[128:131]
s_waitcnt lgkmcnt(9)
v_mfma_f32_16x16x32_bf16 v[136:139],a[132:135],a[204:207],0
s_waitcnt lgkmcnt(8)
v_mfma_f32_16x16x32_bf16 v[136:139],a[128:131],a[200:203],v[136:139]
s_waitcnt lgkmcnt(7)
v_mfma_f32_16x16x32_bf16 v[144:147],a[128:131],a[208:211],0
s_waitcnt lgkmcnt(6)
buffer_load_dwordx4 v48,s[28:31],s57 offen lds
v_mfma_f32_16x16x32_bf16 v[144:147],a[132:135],a[212:215],v[144:147]
s_waitcnt lgkmcnt(5)
v_mfma_f32_16x16x32_bf16 v[152:155],a[132:135],a[220:223],0
s_waitcnt lgkmcnt(4)
v_mfma_f32_16x16x32_bf16 v[152:155],a[128:131],a[216:219],v[152:155]
s_waitcnt lgkmcnt(2)
ds_read_b64_tr_b16 a[144:145],v22 offset:1280
ds_read_b64_tr_b16 a[146:147],v22 offset:3328
v_mfma_f32_16x16x32_bf16 v[132:135],a[136:139],a[192:195],0
s_waitcnt lgkmcnt(2)
ds_read_b64_tr_b16 a[148:149],v22 offset:17664
ds_read_b64_tr_b16 a[150:151],v22 offset:19712
v_mfma_f32_16x16x32_bf16 v[132:135],a[140:143],a[196:199],v[132:135]
buffer_load_dwordx4 v49,s[28:31],s57 offen offset:1024 lds
v_mfma_f32_16x16x32_bf16 v[140:143],a[140:143],a[204:207],0
v_mfma_f32_16x16x32_bf16 v[140:143],a[136:139],a[200:203],v[140:143]
v_mfma_f32_16x16x32_bf16 v[148:151],a[136:139],a[208:211],0
v_mfma_f32_16x16x32_bf16 v[148:151],a[140:143],a[212:215],v[148:151]
v_mfma_f32_16x16x32_bf16 v[156:159],a[140:143],a[220:223],0
buffer_load_dwordx4 v50,s[28:31],s57 offen offset:2048 lds
v_mfma_f32_16x16x32_bf16 v[156:159],a[136:139],a[216:219],v[156:159]
s_waitcnt lgkmcnt(2)
ds_read_b64_tr_b16 a[152:153],v22 offset:1288
ds_read_b64_tr_b16 a[154:155],v22 offset:3336
v_mfma_f32_16x16x32_bf16 v[160:163],a[144:147],a[192:195],0
s_waitcnt lgkmcnt(2)
ds_read_b64_tr_b16 a[156:157],v22 offset:17672
ds_read_b64_tr_b16 a[158:159],v22 offset:19720
v_mfma_f32_16x16x32_bf16 v[160:163],a[148:151],a[196:199],v[160:163]
v_mfma_f32_16x16x32_bf16 v[168:171],a[148:151],a[204:207],0
v_mfma_f32_16x16x32_bf16 v[168:171],a[144:147],a[200:203],v[168:171]
buffer_load_dwordx4 v51,s[28:31],s57 offen offset:3072 lds
v_mfma_f32_16x16x32_bf16 v[176:179],a[144:147],a[208:211],0
s_add_u32 m0,s59,4096
v_mfma_f32_16x16x32_bf16 v[176:179],a[148:151],a[212:215],v[176:179]
v_mfma_f32_16x16x32_bf16 v[184:187],a[148:151],a[220:223],0
v_mfma_f32_16x16x32_bf16 v[184:187],a[144:147],a[216:219],v[184:187]
s_waitcnt lgkmcnt(2)
v_mfma_f32_16x16x32_bf16 v[164:167],a[152:155],a[192:195],0
s_waitcnt lgkmcnt(0)
buffer_load_dwordx4 v52,s[28:31],s57 offen lds
ds_read_b64_tr_b16 a[160:161],v22 offset:1536
ds_read_b64_tr_b16 a[162:163],v22 offset:3584
v_mfma_f32_16x16x32_bf16 v[164:167],a[156:159],a[196:199],v[164:167]
ds_read_b64_tr_b16 a[164:165],v22 offset:17920
ds_read_b64_tr_b16 a[166:167],v22 offset:19968
v_mfma_f32_16x16x32_bf16 v[172:175],a[156:159],a[204:207],0
v_mfma_f32_16x16x32_bf16 v[172:175],a[152:155],a[200:203],v[172:175]
v_mfma_f32_16x16x32_bf16 v[180:183],a[152:155],a[208:211],0
v_mfma_f32_16x16x32_bf16 v[180:183],a[156:159],a[212:215],v[180:183]
buffer_load_dwordx4 v53,s[28:31],s57 offen offset:1024 lds
v_mfma_f32_16x16x32_bf16 v[188:191],a[156:159],a[220:223],0
s_waitcnt vmcnt(14)
v_mfma_f32_16x16x32_bf16 v[188:191],a[152:155],a[216:219],v[188:191]
s_barrier
ds_read_b64_tr_b16 a[168:169],v22 offset:1544
ds_read_b64_tr_b16 a[170:171],v22 offset:3592
s_waitcnt lgkmcnt(4)
v_mfma_f32_16x16x32_bf16 v[192:195],a[160:163],a[192:195],0
s_waitcnt lgkmcnt(2)
ds_read_b64_tr_b16 a[172:173],v22 offset:17928
ds_read_b64_tr_b16 a[174:175],v22 offset:19976
v_mfma_f32_16x16x32_bf16 v[192:195],a[164:167],a[196:199],v[192:195]
buffer_load_dwordx4 v54,s[28:31],s57 offen offset:2048 lds
v_mfma_f32_16x16x32_bf16 v[200:203],a[164:167],a[204:207],0
s_lshl_b32 s44,s41,13
s_add_u32 s44,s44,104857600
s_mov_b64 s[64:65],s[12:13]
v_mfma_f32_16x16x32_bf16 v[200:203],a[160:163],a[200:203],v[200:203]
s_add_u32 s64,s64,s44
s_addc_u32 s65,s65,0
s_and_b32 s65,s65,0xffff
v_mfma_f32_16x16x32_bf16 v[208:211],a[160:163],a[208:211],0
s_mov_b32 s66,8192
s_mov_b32 s67,0x20000
v_mov_b32 v14,4096
v_mfma_f32_16x16x32_bf16 v[208:211],a[164:167],a[212:215],v[208:211]
s_mov_b64 s[72:73],s[14:15]
buffer_load_dwordx4 v55,s[28:31],s57 offen offset:3072 lds
v_mfma_f32_16x16x32_bf16 v[216:219],a[164:167],a[220:223],0
s_add_u32 m0,s58,0
s_and_b32 s73,s73,0xffff
ds_read_b64_tr_b16 a[176:177],v22 offset:1792
v_mfma_f32_16x16x32_bf16 v[216:219],a[160:163],a[216:219],v[216:219]
s_mov_b32 s74,5242880
s_mov_b32 s75,0x20000
ds_read_b64_tr_b16 a[178:179],v22 offset:3840
s_waitcnt lgkmcnt(4)
v_mfma_f32_16x16x32_bf16 v[196:199],a[168:171],a[192:195],0
s_waitcnt lgkmcnt(2)
ds_read_b64_tr_b16 a[180:181],v22 offset:18176
ds_read_b64_tr_b16 a[182:183],v22 offset:20224
v_mfma_f32_16x16x32_bf16 v[196:199],a[172:175],a[196:199],v[196:199]
s_lshl_b32 s83,%1,2
buffer_load_dwordx4 v56,s[32:35],s56 offen lds
v_mfma_f32_16x16x32_bf16 v[204:207],a[172:175],a[204:207],0
s_add_u32 m0,s58,1024
v_lshlrev_b32 v15,3,v6
v_add_u32 v16,128,v15
v_mfma_f32_16x16x32_bf16 v[204:207],a[168:171],a[200:203],v[204:207]
s_add_u32 s83,s83,s40
ds_read_b64_tr_b16 a[184:185],v22 offset:1800
v_lshrrev_b32 v16,10,v16
v_mfma_f32_16x16x32_bf16 v[212:215],a[168:171],a[208:211],0
s_mul_i32 s83,s83,5120
ds_read_b64_tr_b16 a[186:187],v22 offset:3848
v_lshl_add_u32 v15,v16,24,v15
v_mfma_f32_16x16x32_bf16 v[212:215],a[172:175],a[212:215],v[212:215]
s_lshl_b32 s43,s41,5
buffer_load_dwordx4 v57,s[32:35],s56 offen lds
v_mfma_f32_16x16x32_bf16 v[220:223],a[172:175],a[220:223],0
s_lshl_b32 s44,s42,1
s_add_u32 s43,s43,s44
ds_read_b64_tr_b16 a[188:189],v22 offset:18184
v_mfma_f32_16x16x32_bf16 v[220:223],a[168:171],a[216:219],v[220:223]
s_add_u32 m0,s58,2048
s_lshl_b32 s44,s43,18
ds_read_b64_tr_b16 a[190:191],v22 offset:20232
s_waitcnt lgkmcnt(6)
v_mfma_f32_16x16x32_bf16 v[224:227],a[176:179],a[192:195],0
s_mov_b64 s[20:21],s[6:7]
s_mov_b32 s22,524288
ds_read_b64_tr_b16 a[128:129],v22
s_waitcnt lgkmcnt(5)
v_mfma_f32_16x16x32_bf16 v[224:227],a[180:183],a[196:199],v[224:227]
s_add_u32 s20,s20,s44
s_addc_u32 s21,s21,0
ds_read_b64_tr_b16 a[130:131],v22 offset:2048
v_mfma_f32_16x16x32_bf16 v[232:235],a[180:183],a[204:207],0
)A"
R"A(s_and_b32 s21,s21,0xffff
buffer_load_dwordx4 v58,s[32:35],s56 offen lds
v_mfma_f32_16x16x32_bf16 v[232:235],a[176:179],a[200:203],v[232:235]
s_mov_b32 s23,0x20000
s_mov_b64 s[24:25],s[8:9]
ds_read_b64_tr_b16 a[132:133],v22 offset:16384
v_mfma_f32_16x16x32_bf16 v[240:243],a[176:179],a[208:211],0
s_add_u32 m0,s58,3072
s_mov_b32 s26,524288
ds_read_b64_tr_b16 a[134:135],v22 offset:18432
v_mfma_f32_16x16x32_bf16 v[240:243],a[180:183],a[212:215],v[240:243]
s_add_u32 s24,s24,s44
s_addc_u32 s25,s25,0
ds_read_b64_tr_b16 a[136:137],v22 offset:8
v_mfma_f32_16x16x32_bf16 v[248:251],a[180:183],a[220:223],0
s_and_b32 s25,s25,0xffff
s_mov_b32 s27,0x20000
ds_read_b64_tr_b16 a[138:139],v22 offset:2056
v_mfma_f32_16x16x32_bf16 v[248:251],a[176:179],a[216:219],v[248:251]
s_lshl_b32 s44,s43,16
buffer_load_dwordx4 v59,s[32:35],s56 offen lds
s_waitcnt lgkmcnt(6)
v_mfma_f32_16x16x32_bf16 v[228:231],a[184:187],a[192:195],0
s_mov_b64 s[36:37],s[12:13]
s_mov_b32 s38,131072
ds_read_b64_tr_b16 a[140:141],v22 offset:16392
v_mfma_f32_16x16x32_bf16 v[228:231],a[188:191],a[196:199],v[228:231]
s_add_u32 m0,s58,4096
s_mov_b32 s39,0x20000
ds_read_b64_tr_b16 a[142:143],v22 offset:18440
v_mfma_f32_16x16x32_bf16 v[236:239],a[188:191],a[204:207],0
s_add_u32 s36,s36,s44
s_addc_u32 s37,s37,0
ds_read_b64_tr_b16 a[144:145],v22 offset:256
v_mfma_f32_16x16x32_bf16 v[236:239],a[184:187],a[200:203],v[236:239]
s_and_b32 s37,s37,0xffff
s_lshl_b32 s51,s40,12
ds_read_b64_tr_b16 a[146:147],v22 offset:2304
v_mfma_f32_16x16x32_bf16 v[244:247],a[184:187],a[208:211],0
s_add_u32 s52,s51,16384
buffer_load_dwordx4 v60,s[32:35],s56 offen lds
v_mfma_f32_16x16x32_bf16 v[244:247],a[188:191],a[212:215],v[244:247]
s_xor_b32 s59,s59,131072
s_mul_i32 s47,s41,5
ds_read_b64_tr_b16 a[148:149],v22 offset:16640
v_mfma_f32_16x16x32_bf16 v[252:255],a[188:191],a[220:223],0
s_add_u32 m0,s58,5120
s_add_u32 s47,s47,s42
ds_read_b64_tr_b16 a[150:151],v22 offset:18688
v_mfma_f32_16x16x32_bf16 v[252:255],a[184:187],a[216:219],v[252:255]
s_and_b32 s47,s47,15
s_add_u32 s57,s57,128
ds_read_b64_tr_b16 a[152:153],v22 offset:264
s_waitcnt lgkmcnt(7)
v_mfma_f32_16x16x32_bf16 a[0:3],a[128:131],a[192:195],0
s_lshl_b32 s47,s47,14
s_mov_b32 s54,0
ds_read_b64_tr_b16 a[154:155],v22 offset:2312
v_mfma_f32_16x16x32_bf16 a[0:3],a[132:135],a[196:199],a[0:3]
s_lshl_b32 s55,s40,14
buffer_load_dwordx4 v61,s[32:35],s56 offen lds
v_mfma_f32_16x16x32_bf16 a[32:35],a[132:135],a[204:207],0
s_mov_b32 s61,0x3e0293ee
s_mov_b32 s63,0x5d800000
ds_read_b64_tr_b16 a[156:157],v22 offset:16648
v_mfma_f32_16x16x32_bf16 a[32:35],a[128:131],a[200:203],a[32:35]
s_add_u32 m0,s58,6144
s_lshl_b32 s44,s40,2
ds_read_b64_tr_b16 a[158:159],v22 offset:18696
v_mfma_f32_16x16x32_bf16 a[64:67],a[128:131],a[208:211],0
s_add_u32 s44,s44,s70
s_mov_b32 s46,0xff800000
ds_read_b64_tr_b16 a[160:161],v22 offset:512
v_mfma_f32_16x16x32_bf16 a[64:67],a[132:135],a[212:215],a[64:67]
s_cmp_eq_u32 s44,14
s_cselect_b32 s18,0x7149f2ca,s46
ds_read_b64_tr_b16 a[162:163],v22 offset:2560
v_mfma_f32_16x16x32_bf16 a[96:99],a[132:135],a[220:223],0
s_add_u32 s60,s60,32768
buffer_load_dwordx4 v62,s[32:35],s56 offen lds
v_mfma_f32_16x16x32_bf16 a[96:99],a[128:131],a[216:219],a[96:99]
s_cmp_eq_u32 s44,13
s_cselect_b32 s19,0x7149f2ca,s46
ds_read_b64_tr_b16 a[164:165],v22 offset:16896
v_mfma_f32_16x16x32_bf16 a[4:7],a[136:139],a[192:195],0
s_add_u32 m0,s58,7168
ds_read_b64_tr_b16 a[166:167],v22 offset:18944
v_lshlrev_b32 v70,8,v65
s_waitcnt lgkmcnt(6)
v_mfma_f32_16x16x32_bf16 a[4:7],a[140:143],a[196:199],a[4:7]
s_cmp_eq_u32 s44,12
s_cselect_b32 s100,0x7149f2ca,s46
ds_read_b64_tr_b16 a[168:169],v22 offset:520
v_mfma_f32_16x16x32_bf16 a[36:39],a[140:143],a[204:207],0
s_cmp_eq_u32 s44,15
s_cselect_b32 s101,0x7149f2ca,s46
ds_read_b64_tr_b16 a[170:171],v22 offset:2568
v_mfma_f32_16x16x32_bf16 a[36:39],a[136:139],a[200:203],a[36:39]
s_add_u32 s58,s58,32768
buffer_load_dwordx4 v63,s[32:35],s56 offen lds
v_mfma_f32_16x16x32_bf16 a[68:71],a[136:139],a[208:211],0
s_add_u32 s56,s56,524288
ds_read_b64_tr_b16 a[172:173],v22 offset:16904
v_lshl_add_u32 v70,v66,6,v70
v_mfma_f32_16x16x32_bf16 a[68:71],a[140:143],a[212:215],a[68:71]
s_add_u32 m0,s59,0
ds_read_b64_tr_b16 a[174:175],v22 offset:18952
v_add_u32 v70,s51,v70
v_mfma_f32_16x16x32_bf16 a[100:103],a[140:143],a[220:223],0
s_mul_i32 s81,s70,1310720
ds_read_b64_tr_b16 a[176:177],v22 offset:768
v_lshl_add_u32 v4,v64,4,v70
v_mfma_f32_16x16x32_bf16 a[100:103],a[136:139],a[216:219],a[100:103]
s_mul_i32 s44,s71,320
ds_read_b64_tr_b16 a[178:179],v22 offset:2816
v_xor_b32 v71,2,v64
v_mfma_f32_16x16x32_bf16 a[8:11],a[144:147],a[192:195],0
s_add_u32 s81,s81,s44
buffer_load_dwordx4 v48,s[28:31],s57 offen lds
v_mfma_f32_16x16x32_bf16 a[8:11],a[148:151],a[196:199],a[8:11]
s_mul_i32 s82,s40,10240
ds_read_b64_tr_b16 a[180:181],v22 offset:17152
v_lshl_add_u32 v5,v71,4,v70
v_mfma_f32_16x16x32_bf16 a[40:43],a[148:151],a[204:207],0
s_mul_i32 s44,s40,40960
ds_read_b64_tr_b16 a[182:183],v22 offset:19200
v_lshlrev_b32 v72,10,v65
v_mfma_f32_16x16x32_bf16 a[40:43],a[144:147],a[200:203],a[40:43]
s_lshr_b32 s92,%1,3
ds_read_b64_tr_b16 a[184:185],v22 offset:776
v_lshl_add_u32 v72,v64,6,v72
v_mfma_f32_16x16x32_bf16 a[72:75],a[144:147],a[208:211],0
s_lshl_b32 s92,s92,2
ds_read_b64_tr_b16 a[186:187],v22 offset:2824
v_and_b32 v73,2,v65
v_mfma_f32_16x16x32_bf16 a[72:75],a[148:151],a[212:215],a[72:75]
s_add_u32 s92,s92,s40
buffer_load_dwordx4 v49,s[28:31],s57 offen offset:1024 lds
v_mfma_f32_16x16x32_bf16 a[104:107],a[148:151],a[220:223],0
s_lshl_b32 s92,s92,7
ds_read_b64_tr_b16 a[188:189],v22 offset:17160
v_xor_b32 v73,v73,v66
v_mfma_f32_16x16x32_bf16 a[104:107],a[144:147],a[216:219],a[104:107]
s_add_u32 s93,s92,L_tailseg_%=-L_pc_%=
ds_read_b64_tr_b16 a[190:191],v22 offset:19208
v_lshl_add_u32 v72,v73,4,v72
v_mfma_f32_16x16x32_bf16 a[12:15],a[152:155],a[192:195],0
s_waitcnt lgkmcnt(6)
v_add_u32 v1,98304,v72
v_lshlrev_b32 v72,10,v66
v_mfma_f32_16x16x32_bf16 a[12:15],a[156:159],a[196:199],a[12:15]
v_lshl_add_u32 v72,v65,6,v72
v_lshl_add_u32 v72,v64,3,v72
v_mfma_f32_16x16x32_bf16 a[44:47],a[156:159],a[204:207],0
buffer_load_dwordx4 v50,s[28:31],s57 offen offset:2048 lds
v_mfma_f32_16x16x32_bf16 a[44:47],a[152:155],a[200:203],a[44:47]
v_add_u32 v72,114688,v72
v_and_b32 v73,1,v66
v_mfma_f32_16x16x32_bf16 a[76:79],a[152:155],a[208:211],0
s_cmp_eq_u32 s60,131072
s_cselect_b32 s60,32768,s60
v_lshl_add_u32 v2,v73,5,v72
v_mfma_f32_16x16x32_bf16 a[76:79],a[156:159],a[212:215],a[76:79]
v_xor_b32 v73,1,v73
v_add_u32 v22,s60,v23
v_mfma_f32_16x16x32_bf16 a[108:111],a[156:159],a[220:223],0
s_add_u32 s60,s60,32768
v_xor_b32 v20,131072,v20
v_xor_b32 v21,131072,v21
v_mfma_f32_16x16x32_bf16 a[108:111],a[152:155],a[216:219],a[108:111]
s_waitcnt vmcnt(11) lgkmcnt(0)
buffer_load_dwordx4 v51,s[28:31],s57 offen offset:3072 lds
v_mfma_f32_16x16x32_bf16 a[16:19],a[160:163],a[192:195],0
s_barrier
s_add_u32 m0,s59,4096
ds_read_b128 a[224:227],v20
v_mfma_f32_16x16x32_bf16 a[16:19],a[164:167],a[196:199],a[16:19]
s_xor_b32 s59,s59,131072
ds_read_b64_tr_b16 a[128:129],v22 offset:1024
v_lshl_add_u32 v3,v73,5,v72
v_mfma_f32_16x16x32_bf16 a[48:51],a[164:167],a[204:207],0
ds_read_b64_tr_b16 a[130:131],v22 offset:3072
v_mov_b32 v73,0x3f803f80
v_mfma_f32_16x16x32_bf16 a[48:51],a[160:163],a[200:203],a[48:51]
buffer_load_dwordx4 v52,s[28:31],s57 offen lds
v_mfma_f32_16x16x32_bf16 a[80:83],a[160:163],a[208:211],0
s_cmp_ge_u32 s58,131072
s_cselect_b32 s62,98304,0
ds_read_b128 a[228:231],v21
v_mfma_f32_16x16x32_bf16 a[80:83],a[164:167],a[212:215],a[80:83]
s_sub_u32 s58,s58,s62
ds_read_b64_tr_b16 a[132:133],v22 offset:17408
v_add_u32 v70,0,v12
v_mfma_f32_16x16x32_bf16 a[112:115],a[164:167],a[220:223],0
ds_read_b64_tr_b16 a[134:135],v22 offset:19456
v_mul_u32_u24 v71,3277,v70
v_mfma_f32_16x16x32_bf16 a[112:115],a[160:163],a[216:219],a[112:115]
ds_read_b128 a[232:235],v20 offset:2048
v_lshrrev_b32 v71,16,v71
v_mfma_f32_16x16x32_bf16 a[20:23],a[168:171],a[192:195],0
buffer_load_dwordx4 v53,s[28:31],s57 offen offset:1024 lds
v_mfma_f32_16x16x32_bf16 a[20:23],a[172:175],a[196:199],a[20:23]
ds_read_b128 a[236:239],v21 offset:2048
v_mul_u32_u24 v72,20,v71
v_mfma_f32_16x16x32_bf16 a[52:55],a[172:175],a[204:207],0
ds_read_b128 a[240:243],v20 offset:4096
v_sub_u32 v72,v70,v72
v_mfma_f32_16x16x32_bf16 a[52:55],a[168:171],a[200:203],a[52:55]
ds_read_b128 a[244:247],v21 offset:4096
v_add_u32 v70,64,v12
v_mfma_f32_16x16x32_bf16 a[84:87],a[168:171],a[208:211],0
buffer_load_dword v13,v14,s[64:67],0 offen sc1
v_mfma_f32_16x16x32_bf16 a[84:87],a[172:175],a[212:215],a[84:87]
ds_read_b128 a[248:251],v20 offset:6144
v_lshlrev_b32 v24,10,v6
v_mfma_f32_16x16x32_bf16 a[116:119],a[172:175],a[220:223],0
s_cmp_eq_u32 s60,131072
s_cselect_b32 s60,32768,s60
ds_read_b128 a[252:255],v21 offset:6144
v_mfma_f32_16x16x32_bf16 a[116:119],a[168:171],a[216:219],a[116:119]
ds_read_b64_tr_b16 a[136:137],v22 offset:1032
v_xor_b32 v20,131072,v20
v_mfma_f32_16x16x32_bf16 a[24:27],a[176:179],a[192:195],0
buffer_load_dwordx4 v54,s[28:31],s57 offen offset:2048 lds
v_mfma_f32_16x16x32_bf16 a[24:27],a[180:183],a[196:199],a[24:27]
ds_read_b64_tr_b16 a[138:139],v22 offset:3080
v_xor_b32 v21,131072,v21
v_mfma_f32_16x16x32_bf16 a[56:59],a[180:183],a[204:207],0
ds_read_b64_tr_b16 a[140:141],v22 offset:17416
v_mfma_f32_16x16x32_bf16 a[56:59],a[176:179],a[200:203],a[56:59]
ds_read_b64_tr_b16 a[142:143],v22 offset:19464
v_mfma_f32_16x16x32_bf16 a[88:91],a[176:179],a[208:211],0
buffer_load_dwordx4 v55,s[28:31],s57 offen offset:3072 lds
v_mfma_f32_16x16x32_bf16 a[88:91],a[180:183],a[212:215],a[88:91]
s_add_u32 s57,s57,128
ds_read_b64_tr_b16 a[144:145],v22 offset:1280
v_mfma_f32_16x16x32_bf16 a[120:123],a[180:183],a[220:223],0
ds_read_b64_tr_b16 a[146:147],v22 offset:3328
v_mfma_f32_16x16x32_bf16 a[120:123],a[176:179],a[216:219],a[120:123]
ds_read_b64_tr_b16 a[148:149],v22 offset:17664
v_mfma_f32_16x16x32_bf16 a[28:31],a[184:187],a[192:195],0
ds_read_b64_tr_b16 a[150:151],v22 offset:19712
v_mfma_f32_16x16x32_bf16 a[28:31],a[188:191],a[196:199],a[28:31]
ds_read_b64_tr_b16 a[152:153],v22 offset:1288
ds_read_b64_tr_b16 a[154:155],v22 offset:3336
ds_read_b64_tr_b16 a[156:157],v22 offset:17672
ds_read_b64_tr_b16 a[158:159],v22 offset:19720
v_mfma_f32_16x16x32_bf16 a[60:63],a[188:191],a[204:207],0
v_mfma_f32_16x16x32_bf16 a[60:63],a[184:187],a[200:203],a[60:63]
v_cmp_gt_u32 vcc,2,v64
s_nop 1
v_cndmask_b32 v28,0,v73,vcc
v_mfma_f32_16x16x32_bf16 a[92:95],a[184:187],a[208:211],0
v_and_b32 v73,1,v64
v_mul_u32_u24 v73,0xaaaa,v73
v_mfma_f32_16x16x32_bf16 a[92:95],a[188:191],a[212:215],a[92:95]
v_add_u32 v73,0x4444,v73
v_lshl_or_b32 v27,v73,16,v73
v_mfma_f32_16x16x32_bf16 a[124:127],a[188:191],a[220:223],0
s_add_u32 m0,s58,0
v_mov_b32 v29,v28
v_mov_b32 v30,v28
v_mfma_f32_16x16x32_bf16 a[124:127],a[184:187],a[216:219],a[124:127]
s_waitcnt lgkmcnt(5)
v_mov_b32 v31,v28
v_bfe_u32 v73,v71,2,1
v_mfma_f32_16x16x32_bf16 v[128:131],a[128:131],a[224:227],v[128:131]
v_lshlrev_b32 v73,1,v73
v_xor_b32 v72,v72,v73
v_mfma_f32_16x16x32_bf16 v[128:131],a[132:135],a[228:231],v[128:131]
buffer_load_dwordx4 v56,s[32:35],s56 offen lds
v_mfma_f32_16x16x32_bf16 v[136:139],a[132:135],a[236:239],v[136:139]
v_mul_u32_u24 v71,1280,v71
v_lshl_add_u32 v71,v72,4,v71
v_mfma_f32_16x16x32_bf16 v[136:139],a[128:131],a[232:235],v[136:139]
s_add_u32 m0,s58,1024
v_add_u32 v71,s44,v71
v_subrev_u32 v7,0,v71
v_mfma_f32_16x16x32_bf16 v[144:147],a[128:131],a[240:243],v[144:147]
ds_read_b64_tr_b16 a[160:161],v22 offset:1536
v_mul_u32_u24 v71,3277,v70
v_mfma_f32_16x16x32_bf16 v[144:147],a[132:135],a[244:247],v[144:147]
ds_read_b64_tr_b16 a[162:163],v22 offset:3584
v_lshrrev_b32 v71,16,v71
v_mfma_f32_16x16x32_bf16 v[152:155],a[132:135],a[252:255],v[152:155]
buffer_load_dwordx4 v57,s[32:35],s56 offen lds
v_mfma_f32_16x16x32_bf16 v[152:155],a[128:131],a[248:251],v[152:155]
ds_read_b64_tr_b16 a[164:165],v22 offset:17920
v_mul_u32_u24 v72,20,v71
v_mfma_f32_16x16x32_bf16 v[132:135],a[136:139],a[224:227],v[132:135]
ds_read_b64_tr_b16 a[166:167],v22 offset:19968
v_sub_u32 v72,v70,v72
v_mfma_f32_16x16x32_bf16 v[132:135],a[140:143],a[228:231],v[132:135]
)A"
R"A(s_add_u32 m0,s58,2048
v_bfe_u32 v73,v71,2,1
v_lshlrev_b32 v73,1,v73
v_mfma_f32_16x16x32_bf16 v[140:143],a[140:143],a[236:239],v[140:143]
ds_read_b64_tr_b16 a[168:169],v22 offset:1544
v_xor_b32 v72,v72,v73
v_mfma_f32_16x16x32_bf16 v[140:143],a[136:139],a[232:235],v[140:143]
ds_read_b64_tr_b16 a[170:171],v22 offset:3592
v_mul_u32_u24 v71,1280,v71
v_mfma_f32_16x16x32_bf16 v[148:151],a[136:139],a[240:243],v[148:151]
buffer_load_dwordx4 v58,s[32:35],s56 offen lds
v_mfma_f32_16x16x32_bf16 v[148:151],a[140:143],a[244:247],v[148:151]
ds_read_b64_tr_b16 a[172:173],v22 offset:17928
v_lshl_add_u32 v71,v72,4,v71
v_mfma_f32_16x16x32_bf16 v[156:159],a[140:143],a[252:255],v[156:159]
ds_read_b64_tr_b16 a[174:175],v22 offset:19976
v_add_u32 v71,s44,v71
v_mfma_f32_16x16x32_bf16 v[156:159],a[136:139],a[248:251],v[156:159]
s_add_u32 m0,s58,3072
v_subrev_u32 v8,1024,v71
v_add_u32 v70,128,v12
v_mfma_f32_16x16x32_bf16 v[160:163],a[144:147],a[224:227],v[160:163]
s_waitcnt lgkmcnt(4)
ds_read_b64_tr_b16 a[176:177],v22 offset:1792
v_mul_u32_u24 v71,3277,v70
v_mfma_f32_16x16x32_bf16 v[160:163],a[148:151],a[228:231],v[160:163]
ds_read_b64_tr_b16 a[178:179],v22 offset:3840
v_lshrrev_b32 v71,16,v71
v_mfma_f32_16x16x32_bf16 v[168:171],a[148:151],a[236:239],v[168:171]
buffer_load_dwordx4 v59,s[32:35],s56 offen lds
v_mfma_f32_16x16x32_bf16 v[168:171],a[144:147],a[232:235],v[168:171]
ds_read_b64_tr_b16 a[180:181],v22 offset:18176
v_mul_u32_u24 v72,20,v71
v_mfma_f32_16x16x32_bf16 v[176:179],a[144:147],a[240:243],v[176:179]
s_add_u32 m0,s58,4096
ds_read_b64_tr_b16 a[182:183],v22 offset:20224
v_sub_u32 v72,v70,v72
v_mfma_f32_16x16x32_bf16 v[176:179],a[148:151],a[244:247],v[176:179]
v_bfe_u32 v73,v71,2,1
v_lshlrev_b32 v73,1,v73
v_mfma_f32_16x16x32_bf16 v[184:187],a[148:151],a[252:255],v[184:187]
ds_read_b64_tr_b16 a[184:185],v22 offset:1800
v_xor_b32 v72,v72,v73
v_mfma_f32_16x16x32_bf16 v[184:187],a[144:147],a[248:251],v[184:187]
buffer_load_dwordx4 v60,s[32:35],s56 offen lds
v_mfma_f32_16x16x32_bf16 v[164:167],a[152:155],a[224:227],v[164:167]
ds_read_b64_tr_b16 a[186:187],v22 offset:3848
v_mul_u32_u24 v71,1280,v71
v_mfma_f32_16x16x32_bf16 v[164:167],a[156:159],a[228:231],v[164:167]
ds_read_b64_tr_b16 a[188:189],v22 offset:18184
v_lshl_add_u32 v71,v72,4,v71
v_mfma_f32_16x16x32_bf16 v[172:175],a[156:159],a[236:239],v[172:175]
s_add_u32 m0,s58,5120
ds_read_b64_tr_b16 a[190:191],v22 offset:20232
v_add_u32 v71,s44,v71
v_mfma_f32_16x16x32_bf16 v[172:175],a[152:155],a[232:235],v[172:175]
v_subrev_u32 v9,2048,v71
v_add_u32 v70,192,v12
v_mfma_f32_16x16x32_bf16 v[180:183],a[152:155],a[240:243],v[180:183]
v_mul_u32_u24 v71,3277,v70
v_lshrrev_b32 v71,16,v71
v_mfma_f32_16x16x32_bf16 v[180:183],a[156:159],a[244:247],v[180:183]
buffer_load_dwordx4 v61,s[32:35],s56 offen lds
v_mfma_f32_16x16x32_bf16 v[188:191],a[156:159],a[252:255],v[188:191]
ds_read_b64_tr_b16 a[128:129],v22
v_mul_u32_u24 v72,20,v71
v_mfma_f32_16x16x32_bf16 v[188:191],a[152:155],a[248:251],v[188:191]
ds_read_b64_tr_b16 a[130:131],v22 offset:2048
v_sub_u32 v72,v70,v72
v_mfma_f32_16x16x32_bf16 v[192:195],a[160:163],a[224:227],v[192:195]
s_add_u32 m0,s58,6144
ds_read_b64_tr_b16 a[132:133],v22 offset:16384
v_bfe_u32 v73,v71,2,1
v_mfma_f32_16x16x32_bf16 v[192:195],a[164:167],a[228:231],v[192:195]
ds_read_b64_tr_b16 a[134:135],v22 offset:18432
v_lshlrev_b32 v73,1,v73
v_mfma_f32_16x16x32_bf16 v[200:203],a[164:167],a[236:239],v[200:203]
v_xor_b32 v72,v72,v73
v_mul_u32_u24 v71,1280,v71
v_mfma_f32_16x16x32_bf16 v[200:203],a[160:163],a[232:235],v[200:203]
buffer_load_dwordx4 v62,s[32:35],s56 offen lds
v_mfma_f32_16x16x32_bf16 v[208:211],a[160:163],a[240:243],v[208:211]
ds_read_b64_tr_b16 a[136:137],v22 offset:8
v_lshl_add_u32 v71,v72,4,v71
v_mfma_f32_16x16x32_bf16 v[208:211],a[164:167],a[244:247],v[208:211]
s_add_u32 m0,s58,7168
ds_read_b64_tr_b16 a[138:139],v22 offset:2056
v_add_u32 v71,s44,v71
v_mfma_f32_16x16x32_bf16 v[216:219],a[164:167],a[252:255],v[216:219]
s_add_u32 s58,s58,32768
ds_read_b64_tr_b16 a[140:141],v22 offset:16392
v_subrev_u32 v10,3072,v71
v_mfma_f32_16x16x32_bf16 v[216:219],a[160:163],a[248:251],v[216:219]
s_waitcnt lgkmcnt(5)
ds_read_b64_tr_b16 a[142:143],v22 offset:18440
v_add_u32 v70,256,v12
v_mfma_f32_16x16x32_bf16 v[196:199],a[168:171],a[224:227],v[196:199]
buffer_load_dwordx4 v63,s[32:35],s56 offen lds
v_mfma_f32_16x16x32_bf16 v[196:199],a[172:175],a[228:231],v[196:199]
s_add_u32 s56,s56,524288
v_mul_u32_u24 v71,3277,v70
v_lshrrev_b32 v71,16,v71
v_mfma_f32_16x16x32_bf16 v[204:207],a[172:175],a[236:239],v[204:207]
ds_read_b64_tr_b16 a[144:145],v22 offset:256
v_mul_u32_u24 v72,20,v71
v_mfma_f32_16x16x32_bf16 v[204:207],a[168:171],a[232:235],v[204:207]
s_add_u32 m0,s59,0
ds_read_b64_tr_b16 a[146:147],v22 offset:2304
v_sub_u32 v72,v70,v72
v_mfma_f32_16x16x32_bf16 v[212:215],a[168:171],a[240:243],v[212:215]
ds_read_b64_tr_b16 a[148:149],v22 offset:16640
v_bfe_u32 v73,v71,2,1
v_mfma_f32_16x16x32_bf16 v[212:215],a[172:175],a[244:247],v[212:215]
ds_read_b64_tr_b16 a[150:151],v22 offset:18688
v_lshlrev_b32 v73,1,v73
v_mfma_f32_16x16x32_bf16 v[220:223],a[172:175],a[252:255],v[220:223]
buffer_load_dwordx4 v48,s[28:31],s57 offen lds
v_mfma_f32_16x16x32_bf16 v[220:223],a[168:171],a[248:251],v[220:223]
v_xor_b32 v72,v72,v73
v_mul_u32_u24 v71,1280,v71
v_mfma_f32_16x16x32_bf16 v[224:227],a[176:179],a[224:227],v[224:227]
v_lshl_add_u32 v71,v72,4,v71
v_add_u32 v71,s44,v71
v_mfma_f32_16x16x32_bf16 v[224:227],a[180:183],a[228:231],v[224:227]
ds_read_b64_tr_b16 a[152:153],v22 offset:264
v_subrev_u32 v11,0,v71
v_mfma_f32_16x16x32_bf16 v[232:235],a[180:183],a[236:239],v[232:235]
s_cmp_ge_u32 s58,131072
s_cselect_b32 s62,98304,0
ds_read_b64_tr_b16 a[154:155],v22 offset:2312
v_mfma_f32_16x16x32_bf16 v[232:235],a[176:179],a[232:235],v[232:235]
s_sub_u32 s58,s58,s62
ds_read_b64_tr_b16 a[156:157],v22 offset:16648
v_mfma_f32_16x16x32_bf16 v[240:243],a[176:179],a[240:243],v[240:243]
buffer_load_dwordx4 v49,s[28:31],s57 offen offset:1024 lds
v_mfma_f32_16x16x32_bf16 v[240:243],a[180:183],a[244:247],v[240:243]
ds_read_b64_tr_b16 a[158:159],v22 offset:18696
v_mfma_f32_16x16x32_bf16 v[248:251],a[180:183],a[252:255],v[248:251]
ds_read_b64_tr_b16 a[160:161],v22 offset:512
v_mfma_f32_16x16x32_bf16 v[248:251],a[176:179],a[248:251],v[248:251]
ds_read_b64_tr_b16 a[162:163],v22 offset:2560
v_mfma_f32_16x16x32_bf16 v[228:231],a[184:187],a[224:227],v[228:231]
ds_read_b64_tr_b16 a[164:165],v22 offset:16896
v_mfma_f32_16x16x32_bf16 v[228:231],a[188:191],a[228:231],v[228:231]
buffer_load_dwordx4 v50,s[28:31],s57 offen offset:2048 lds
v_mfma_f32_16x16x32_bf16 v[236:239],a[188:191],a[236:239],v[236:239]
ds_read_b64_tr_b16 a[166:167],v22 offset:18944
v_mfma_f32_16x16x32_bf16 v[236:239],a[184:187],a[232:235],v[236:239]
ds_read_b64_tr_b16 a[168:169],v22 offset:520
v_mfma_f32_16x16x32_bf16 v[244:247],a[184:187],a[240:243],v[244:247]
ds_read_b64_tr_b16 a[170:171],v22 offset:2568
v_mfma_f32_16x16x32_bf16 v[244:247],a[188:191],a[244:247],v[244:247]
ds_read_b64_tr_b16 a[172:173],v22 offset:16904
v_mfma_f32_16x16x32_bf16 v[252:255],a[188:191],a[252:255],v[252:255]
ds_read_b64_tr_b16 a[174:175],v22 offset:18952
v_mfma_f32_16x16x32_bf16 v[252:255],a[184:187],a[248:251],v[252:255]
buffer_load_dwordx4 v51,s[28:31],s57 offen offset:3072 lds
v_mfma_f32_16x16x32_bf16 a[0:3],a[128:131],a[224:227],a[0:3]
s_waitcnt lgkmcnt(5)
ds_read_b64_tr_b16 a[176:177],v22 offset:768
v_mfma_f32_16x16x32_bf16 a[0:3],a[132:135],a[228:231],a[0:3]
ds_read_b64_tr_b16 a[178:179],v22 offset:2816
v_mfma_f32_16x16x32_bf16 a[32:35],a[132:135],a[236:239],a[32:35]
s_add_u32 m0,s59,4096
s_xor_b32 s59,s59,131072
ds_read_b64_tr_b16 a[180:181],v22 offset:17152
v_mfma_f32_16x16x32_bf16 a[32:35],a[128:131],a[232:235],a[32:35]
ds_read_b64_tr_b16 a[182:183],v22 offset:19200
v_mfma_f32_16x16x32_bf16 a[64:67],a[128:131],a[240:243],a[64:67]
ds_read_b64_tr_b16 a[184:185],v22 offset:776
v_mfma_f32_16x16x32_bf16 a[64:67],a[132:135],a[244:247],a[64:67]
buffer_load_dwordx4 v52,s[28:31],s57 offen lds
v_mfma_f32_16x16x32_bf16 a[96:99],a[132:135],a[252:255],a[96:99]
ds_read_b64_tr_b16 a[186:187],v22 offset:2824
v_mfma_f32_16x16x32_bf16 a[96:99],a[128:131],a[248:251],a[96:99]
ds_read_b64_tr_b16 a[188:189],v22 offset:17160
v_mfma_f32_16x16x32_bf16 a[4:7],a[136:139],a[224:227],a[4:7]
ds_read_b64_tr_b16 a[190:191],v22 offset:19208
v_add_u32 v22,s60,v23
v_mfma_f32_16x16x32_bf16 a[4:7],a[140:143],a[228:231],a[4:7]
v_mfma_f32_16x16x32_bf16 a[36:39],a[140:143],a[236:239],a[36:39]
buffer_load_dwordx4 v53,s[28:31],s57 offen offset:1024 lds
v_mfma_f32_16x16x32_bf16 a[36:39],a[136:139],a[232:235],a[36:39]
v_mfma_f32_16x16x32_bf16 a[68:71],a[136:139],a[240:243],a[68:71]
v_mfma_f32_16x16x32_bf16 a[68:71],a[140:143],a[244:247],a[68:71]
v_mfma_f32_16x16x32_bf16 a[100:103],a[140:143],a[252:255],a[100:103]
v_mfma_f32_16x16x32_bf16 a[100:103],a[136:139],a[248:251],a[100:103]
v_mfma_f32_16x16x32_bf16 a[8:11],a[144:147],a[224:227],a[8:11]
buffer_load_dwordx4 v54,s[28:31],s57 offen offset:2048 lds
v_mfma_f32_16x16x32_bf16 a[8:11],a[148:151],a[228:231],a[8:11]
v_mfma_f32_16x16x32_bf16 a[40:43],a[148:151],a[236:239],a[40:43]
v_mfma_f32_16x16x32_bf16 a[40:43],a[144:147],a[232:235],a[40:43]
v_mfma_f32_16x16x32_bf16 a[72:75],a[144:147],a[240:243],a[72:75]
v_mfma_f32_16x16x32_bf16 a[72:75],a[148:151],a[244:247],a[72:75]
v_mfma_f32_16x16x32_bf16 a[104:107],a[148:151],a[252:255],a[104:107]
buffer_load_dwordx4 v55,s[28:31],s57 offen offset:3072 lds
v_mfma_f32_16x16x32_bf16 a[104:107],a[144:147],a[248:251],a[104:107]
s_add_u32 s57,s57,128
v_mfma_f32_16x16x32_bf16 a[12:15],a[152:155],a[224:227],a[12:15]
v_mfma_f32_16x16x32_bf16 a[12:15],a[156:159],a[228:231],a[12:15]
v_mfma_f32_16x16x32_bf16 a[44:47],a[156:159],a[236:239],a[44:47]
v_mfma_f32_16x16x32_bf16 a[44:47],a[152:155],a[232:235],a[44:47]
v_mfma_f32_16x16x32_bf16 a[76:79],a[152:155],a[240:243],a[76:79]
v_mfma_f32_16x16x32_bf16 a[76:79],a[156:159],a[244:247],a[76:79]
v_mfma_f32_16x16x32_bf16 a[108:111],a[156:159],a[252:255],a[108:111]
v_mfma_f32_16x16x32_bf16 a[108:111],a[152:155],a[248:251],a[108:111]
s_waitcnt vmcnt(16) lgkmcnt(0)
v_mfma_f32_16x16x32_bf16 a[16:19],a[160:163],a[224:227],a[16:19]
s_barrier
ds_read_b128 a[192:195],v20
v_mfma_f32_16x16x32_bf16 a[16:19],a[164:167],a[228:231],a[16:19]
ds_read_b128 a[196:199],v21
v_mfma_f32_16x16x32_bf16 a[48:51],a[164:167],a[236:239],a[48:51]
ds_read_b128 a[200:203],v20 offset:2048
v_mfma_f32_16x16x32_bf16 a[48:51],a[160:163],a[232:235],a[48:51]
ds_read_b128 a[204:207],v21 offset:2048
v_mfma_f32_16x16x32_bf16 a[80:83],a[160:163],a[240:243],a[80:83]
ds_read_b128 a[208:211],v20 offset:4096
v_mfma_f32_16x16x32_bf16 a[80:83],a[164:167],a[244:247],a[80:83]
ds_read_b128 a[212:215],v21 offset:4096
v_mfma_f32_16x16x32_bf16 a[112:115],a[164:167],a[252:255],a[112:115]
ds_read_b128 a[216:219],v20 offset:6144
v_mfma_f32_16x16x32_bf16 a[112:115],a[160:163],a[248:251],a[112:115]
ds_read_b128 a[220:223],v21 offset:6144
v_mfma_f32_16x16x32_bf16 a[20:23],a[168:171],a[224:227],a[20:23]
ds_read_b64_tr_b16 a[128:129],v22 offset:1024
v_mfma_f32_16x16x32_bf16 a[20:23],a[172:175],a[228:231],a[20:23]
ds_read_b64_tr_b16 a[130:131],v22 offset:3072
v_mfma_f32_16x16x32_bf16 a[52:55],a[172:175],a[236:239],a[52:55]
ds_read_b64_tr_b16 a[132:133],v22 offset:17408
v_mfma_f32_16x16x32_bf16 a[52:55],a[168:171],a[232:235],a[52:55]
ds_read_b64_tr_b16 a[134:135],v22 offset:19456
v_mfma_f32_16x16x32_bf16 a[84:87],a[168:171],a[240:243],a[84:87]
ds_read_b64_tr_b16 a[136:137],v22 offset:1032
v_mfma_f32_16x16x32_bf16 a[84:87],a[172:175],a[244:247],a[84:87]
ds_read_b64_tr_b16 a[138:139],v22 offset:3080
v_mfma_f32_16x16x32_bf16 a[116:119],a[172:175],a[252:255],a[116:119]
ds_read_b64_tr_b16 a[140:141],v22 offset:17416
v_mfma_f32_16x16x32_bf16 a[116:119],a[168:171],a[248:251],a[116:119]
ds_read_b64_tr_b16 a[142:143],v22 offset:19464
v_mfma_f32_16x16x32_bf16 a[24:27],a[176:179],a[224:227],a[24:27]
ds_read_b64_tr_b16 a[144:145],v22 offset:1280
v_mfma_f32_16x16x32_bf16 a[24:27],a[180:183],a[228:231],a[24:27]
ds_read_b64_tr_b16 a[146:147],v22 offset:3328
)A"
R"A(v_mfma_f32_16x16x32_bf16 a[56:59],a[180:183],a[236:239],a[56:59]
ds_read_b64_tr_b16 a[148:149],v22 offset:17664
v_mfma_f32_16x16x32_bf16 a[56:59],a[176:179],a[232:235],a[56:59]
ds_read_b64_tr_b16 a[150:151],v22 offset:19712
v_mfma_f32_16x16x32_bf16 a[88:91],a[176:179],a[240:243],a[88:91]
ds_read_b64_tr_b16 a[152:153],v22 offset:1288
v_mfma_f32_16x16x32_bf16 a[88:91],a[180:183],a[244:247],a[88:91]
ds_read_b64_tr_b16 a[154:155],v22 offset:3336
v_mfma_f32_16x16x32_bf16 a[120:123],a[180:183],a[252:255],a[120:123]
ds_read_b64_tr_b16 a[156:157],v22 offset:17672
v_mfma_f32_16x16x32_bf16 a[120:123],a[176:179],a[248:251],a[120:123]
ds_read_b64_tr_b16 a[158:159],v22 offset:19720
v_mfma_f32_16x16x32_bf16 a[28:31],a[184:187],a[224:227],a[28:31]
v_mfma_f32_16x16x32_bf16 a[28:31],a[188:191],a[228:231],a[28:31]
v_mfma_f32_16x16x32_bf16 a[60:63],a[188:191],a[236:239],a[60:63]
v_mfma_f32_16x16x32_bf16 a[60:63],a[184:187],a[232:235],a[60:63]
v_mfma_f32_16x16x32_bf16 a[92:95],a[184:187],a[240:243],a[92:95]
v_mfma_f32_16x16x32_bf16 a[92:95],a[188:191],a[244:247],a[92:95]
v_mfma_f32_16x16x32_bf16 a[124:127],a[188:191],a[252:255],a[124:127]
v_mfma_f32_16x16x32_bf16 a[124:127],a[184:187],a[248:251],a[124:127]
s_mov_b32 s53,3
.p2align 5
L_qp_%=:
s_waitcnt lgkmcnt(1)
v_mfma_f32_16x16x32_bf16 v[128:131],a[128:131],a[192:195],v[128:131]
s_add_u32 m0,s58,0
s_add_u32 s60,s60,32768
ds_read_b64_tr_b16 a[160:161],v22 offset:1536
v_mfma_f32_16x16x32_bf16 v[128:131],a[132:135],a[196:199],v[128:131]
s_mov_b32 s90,L_att0x_%=-L_pc_%=+128
buffer_load_dwordx4 v56,s[32:35],s56 offen lds
v_mfma_f32_16x16x32_bf16 v[136:139],a[132:135],a[204:207],v[136:139]
s_cmp_eq_u32 s60,131072
s_cselect_b32 s60,32768,s60
ds_read_b64_tr_b16 a[162:163],v22 offset:3584
v_mfma_f32_16x16x32_bf16 v[136:139],a[128:131],a[200:203],v[136:139]
s_add_u32 m0,s58,1024
ds_read_b64_tr_b16 a[164:165],v22 offset:17920
v_xor_b32 v20,131072,v20
v_mfma_f32_16x16x32_bf16 v[144:147],a[128:131],a[208:211],v[144:147]
s_sub_u32 s53,s53,1
ds_read_b64_tr_b16 a[166:167],v22 offset:19968
v_xor_b32 v21,131072,v21
v_mfma_f32_16x16x32_bf16 v[144:147],a[132:135],a[212:215],v[144:147]
ds_read_b64_tr_b16 a[168:169],v22 offset:1544
v_mfma_f32_16x16x32_bf16 v[152:155],a[132:135],a[220:223],v[152:155]
buffer_load_dwordx4 v57,s[32:35],s56 offen lds
v_mfma_f32_16x16x32_bf16 v[152:155],a[128:131],a[216:219],v[152:155]
ds_read_b64_tr_b16 a[170:171],v22 offset:3592
v_mfma_f32_16x16x32_bf16 v[132:135],a[136:139],a[192:195],v[132:135]
ds_read_b64_tr_b16 a[172:173],v22 offset:17928
v_mfma_f32_16x16x32_bf16 v[132:135],a[140:143],a[196:199],v[132:135]
s_add_u32 m0,s58,2048
ds_read_b64_tr_b16 a[174:175],v22 offset:19976
v_mfma_f32_16x16x32_bf16 v[140:143],a[140:143],a[204:207],v[140:143]
ds_read_b64_tr_b16 a[176:177],v22 offset:1792
v_mfma_f32_16x16x32_bf16 v[140:143],a[136:139],a[200:203],v[140:143]
ds_read_b64_tr_b16 a[178:179],v22 offset:3840
v_mfma_f32_16x16x32_bf16 v[148:151],a[136:139],a[208:211],v[148:151]
buffer_load_dwordx4 v58,s[32:35],s56 offen lds
v_mfma_f32_16x16x32_bf16 v[148:151],a[140:143],a[212:215],v[148:151]
ds_read_b64_tr_b16 a[180:181],v22 offset:18176
v_mfma_f32_16x16x32_bf16 v[156:159],a[140:143],a[220:223],v[156:159]
s_add_u32 m0,s58,3072
ds_read_b64_tr_b16 a[182:183],v22 offset:20224
v_mfma_f32_16x16x32_bf16 v[156:159],a[136:139],a[216:219],v[156:159]
ds_read_b64_tr_b16 a[184:185],v22 offset:1800
v_mfma_f32_16x16x32_bf16 v[160:163],a[144:147],a[192:195],v[160:163]
ds_read_b64_tr_b16 a[186:187],v22 offset:3848
v_mfma_f32_16x16x32_bf16 v[160:163],a[148:151],a[196:199],v[160:163]
buffer_load_dwordx4 v59,s[32:35],s56 offen lds
v_mfma_f32_16x16x32_bf16 v[168:171],a[148:151],a[204:207],v[168:171]
ds_read_b64_tr_b16 a[188:189],v22 offset:18184
v_mfma_f32_16x16x32_bf16 v[168:171],a[144:147],a[200:203],v[168:171]
ds_read_b64_tr_b16 a[190:191],v22 offset:20232
v_mfma_f32_16x16x32_bf16 v[176:179],a[144:147],a[208:211],v[176:179]
s_add_u32 m0,s58,4096
ds_read_b64_tr_b16 a[128:129],v22
v_mfma_f32_16x16x32_bf16 v[176:179],a[148:151],a[212:215],v[176:179]
ds_read_b64_tr_b16 a[130:131],v22 offset:2048
v_mfma_f32_16x16x32_bf16 v[184:187],a[148:151],a[220:223],v[184:187]
ds_read_b64_tr_b16 a[132:133],v22 offset:16384
v_mfma_f32_16x16x32_bf16 v[184:187],a[144:147],a[216:219],v[184:187]
buffer_load_dwordx4 v60,s[32:35],s56 offen lds
v_mfma_f32_16x16x32_bf16 v[164:167],a[152:155],a[192:195],v[164:167]
s_waitcnt lgkmcnt(5)
ds_read_b64_tr_b16 a[134:135],v22 offset:18432
v_mfma_f32_16x16x32_bf16 v[164:167],a[156:159],a[196:199],v[164:167]
s_add_u32 m0,s58,5120
ds_read_b64_tr_b16 a[136:137],v22 offset:8
v_mfma_f32_16x16x32_bf16 v[172:175],a[156:159],a[204:207],v[172:175]
ds_read_b64_tr_b16 a[138:139],v22 offset:2056
v_mfma_f32_16x16x32_bf16 v[172:175],a[152:155],a[200:203],v[172:175]
ds_read_b64_tr_b16 a[140:141],v22 offset:16392
v_mfma_f32_16x16x32_bf16 v[180:183],a[152:155],a[208:211],v[180:183]
buffer_load_dwordx4 v61,s[32:35],s56 offen lds
v_mfma_f32_16x16x32_bf16 v[180:183],a[156:159],a[212:215],v[180:183]
ds_read_b64_tr_b16 a[142:143],v22 offset:18440
v_mfma_f32_16x16x32_bf16 v[188:191],a[156:159],a[220:223],v[188:191]
ds_read_b64_tr_b16 a[144:145],v22 offset:256
v_mfma_f32_16x16x32_bf16 v[188:191],a[152:155],a[216:219],v[188:191]
s_add_u32 m0,s58,6144
ds_read_b64_tr_b16 a[146:147],v22 offset:2304
v_mfma_f32_16x16x32_bf16 v[192:195],a[160:163],a[192:195],v[192:195]
ds_read_b64_tr_b16 a[148:149],v22 offset:16640
v_mfma_f32_16x16x32_bf16 v[192:195],a[164:167],a[196:199],v[192:195]
ds_read_b64_tr_b16 a[150:151],v22 offset:18688
v_mfma_f32_16x16x32_bf16 v[200:203],a[164:167],a[204:207],v[200:203]
buffer_load_dwordx4 v62,s[32:35],s56 offen lds
v_mfma_f32_16x16x32_bf16 v[200:203],a[160:163],a[200:203],v[200:203]
ds_read_b64_tr_b16 a[152:153],v22 offset:264
v_mfma_f32_16x16x32_bf16 v[208:211],a[160:163],a[208:211],v[208:211]
s_add_u32 m0,s58,7168
s_add_u32 s58,s58,32768
ds_read_b64_tr_b16 a[154:155],v22 offset:2312
v_mfma_f32_16x16x32_bf16 v[208:211],a[164:167],a[212:215],v[208:211]
s_cmp_ge_u32 s58,131072
s_cselect_b32 s62,98304,0
ds_read_b64_tr_b16 a[156:157],v22 offset:16648
v_mfma_f32_16x16x32_bf16 v[216:219],a[164:167],a[220:223],v[216:219]
s_sub_u32 s58,s58,s62
ds_read_b64_tr_b16 a[158:159],v22 offset:18696
v_mfma_f32_16x16x32_bf16 v[216:219],a[160:163],a[216:219],v[216:219]
buffer_load_dwordx4 v63,s[32:35],s56 offen lds
v_mfma_f32_16x16x32_bf16 v[196:199],a[168:171],a[192:195],v[196:199]
s_add_u32 s56,s56,524288
ds_read_b64_tr_b16 a[160:161],v22 offset:512
v_mfma_f32_16x16x32_bf16 v[196:199],a[172:175],a[196:199],v[196:199]
ds_read_b64_tr_b16 a[162:163],v22 offset:2560
v_mfma_f32_16x16x32_bf16 v[204:207],a[172:175],a[204:207],v[204:207]
s_add_u32 m0,s59,0
ds_read_b64_tr_b16 a[164:165],v22 offset:16896
v_mfma_f32_16x16x32_bf16 v[204:207],a[168:171],a[200:203],v[204:207]
ds_read_b64_tr_b16 a[166:167],v22 offset:18944
v_mfma_f32_16x16x32_bf16 v[212:215],a[168:171],a[208:211],v[212:215]
v_mfma_f32_16x16x32_bf16 v[212:215],a[172:175],a[212:215],v[212:215]
buffer_load_dwordx4 v48,s[28:31],s57 offen lds
v_mfma_f32_16x16x32_bf16 v[220:223],a[172:175],a[220:223],v[220:223]
ds_read_b64_tr_b16 a[172:173],v22 offset:16904
v_mfma_f32_16x16x32_bf16 v[220:223],a[168:171],a[216:219],v[220:223]
ds_read_b64_tr_b16 a[168:169],v22 offset:520
v_mfma_f32_16x16x32_bf16 v[224:227],a[176:179],a[192:195],v[224:227]
ds_read_b64_tr_b16 a[170:171],v22 offset:2568
v_mfma_f32_16x16x32_bf16 v[224:227],a[180:183],a[196:199],v[224:227]
ds_read_b64_tr_b16 a[174:175],v22 offset:18952
v_mfma_f32_16x16x32_bf16 v[232:235],a[180:183],a[204:207],v[232:235]
buffer_load_dwordx4 v49,s[28:31],s57 offen offset:1024 lds
v_mfma_f32_16x16x32_bf16 v[232:235],a[176:179],a[200:203],v[232:235]
v_mfma_f32_16x16x32_bf16 v[240:243],a[176:179],a[208:211],v[240:243]
v_mfma_f32_16x16x32_bf16 v[240:243],a[180:183],a[212:215],v[240:243]
v_mfma_f32_16x16x32_bf16 v[248:251],a[180:183],a[220:223],v[248:251]
ds_read_b64_tr_b16 a[180:181],v22 offset:17152
v_mfma_f32_16x16x32_bf16 v[248:251],a[176:179],a[216:219],v[248:251]
ds_read_b64_tr_b16 a[176:177],v22 offset:768
v_mfma_f32_16x16x32_bf16 v[228:231],a[184:187],a[192:195],v[228:231]
s_waitcnt lgkmcnt(3)
buffer_load_dwordx4 v50,s[28:31],s57 offen offset:2048 lds
v_mfma_f32_16x16x32_bf16 v[228:231],a[188:191],a[196:199],v[228:231]
ds_read_b64_tr_b16 a[178:179],v22 offset:2816
v_mfma_f32_16x16x32_bf16 v[236:239],a[188:191],a[204:207],v[236:239]
ds_read_b64_tr_b16 a[182:183],v22 offset:19200
v_mfma_f32_16x16x32_bf16 v[236:239],a[184:187],a[200:203],v[236:239]
v_mfma_f32_16x16x32_bf16 v[244:247],a[184:187],a[208:211],v[244:247]
v_mfma_f32_16x16x32_bf16 v[244:247],a[188:191],a[212:215],v[244:247]
buffer_load_dwordx4 v51,s[28:31],s57 offen offset:3072 lds
v_mfma_f32_16x16x32_bf16 v[252:255],a[188:191],a[220:223],v[252:255]
ds_read_b64_tr_b16 a[188:189],v22 offset:17160
v_mfma_f32_16x16x32_bf16 v[252:255],a[184:187],a[216:219],v[252:255]
ds_read_b64_tr_b16 a[184:185],v22 offset:776
v_mfma_f32_16x16x32_bf16 a[0:3],a[128:131],a[192:195],a[0:3]
s_add_u32 m0,s59,4096
s_xor_b32 s59,s59,131072
ds_read_b64_tr_b16 a[186:187],v22 offset:2824
v_mfma_f32_16x16x32_bf16 a[0:3],a[132:135],a[196:199],a[0:3]
ds_read_b64_tr_b16 a[190:191],v22 offset:19208
v_add_u32 v22,s60,v23
v_mfma_f32_16x16x32_bf16 a[32:35],a[132:135],a[204:207],a[32:35]
s_add_u32 s60,s60,32768
s_cmp_eq_u32 s60,131072
s_cselect_b32 s60,32768,s60
v_mfma_f32_16x16x32_bf16 a[32:35],a[128:131],a[200:203],a[32:35]
buffer_load_dwordx4 v52,s[28:31],s57 offen lds
v_mfma_f32_16x16x32_bf16 a[64:67],a[128:131],a[208:211],a[64:67]
v_mfma_f32_16x16x32_bf16 a[64:67],a[132:135],a[212:215],a[64:67]
v_mfma_f32_16x16x32_bf16 a[96:99],a[132:135],a[220:223],a[96:99]
v_mfma_f32_16x16x32_bf16 a[96:99],a[128:131],a[216:219],a[96:99]
v_mfma_f32_16x16x32_bf16 a[4:7],a[136:139],a[192:195],a[4:7]
buffer_load_dwordx4 v53,s[28:31],s57 offen offset:1024 lds
v_mfma_f32_16x16x32_bf16 a[4:7],a[140:143],a[196:199],a[4:7]
v_mfma_f32_16x16x32_bf16 a[36:39],a[140:143],a[204:207],a[36:39]
v_mfma_f32_16x16x32_bf16 a[36:39],a[136:139],a[200:203],a[36:39]
v_mfma_f32_16x16x32_bf16 a[68:71],a[136:139],a[208:211],a[68:71]
v_mfma_f32_16x16x32_bf16 a[68:71],a[140:143],a[212:215],a[68:71]
v_mfma_f32_16x16x32_bf16 a[100:103],a[140:143],a[220:223],a[100:103]
buffer_load_dwordx4 v54,s[28:31],s57 offen offset:2048 lds
v_mfma_f32_16x16x32_bf16 a[100:103],a[136:139],a[216:219],a[100:103]
v_mfma_f32_16x16x32_bf16 a[8:11],a[144:147],a[192:195],a[8:11]
v_mfma_f32_16x16x32_bf16 a[8:11],a[148:151],a[196:199],a[8:11]
v_mfma_f32_16x16x32_bf16 a[40:43],a[148:151],a[204:207],a[40:43]
v_mfma_f32_16x16x32_bf16 a[40:43],a[144:147],a[200:203],a[40:43]
buffer_load_dwordx4 v55,s[28:31],s57 offen offset:3072 lds
v_mfma_f32_16x16x32_bf16 a[72:75],a[144:147],a[208:211],a[72:75]
s_add_u32 s57,s57,128
v_mfma_f32_16x16x32_bf16 a[72:75],a[148:151],a[212:215],a[72:75]
v_mfma_f32_16x16x32_bf16 a[104:107],a[148:151],a[220:223],a[104:107]
v_mfma_f32_16x16x32_bf16 a[104:107],a[144:147],a[216:219],a[104:107]
v_mfma_f32_16x16x32_bf16 a[12:15],a[152:155],a[192:195],a[12:15]
v_mfma_f32_16x16x32_bf16 a[12:15],a[156:159],a[196:199],a[12:15]
v_mfma_f32_16x16x32_bf16 a[44:47],a[156:159],a[204:207],a[44:47]
buffer_load_dword v16,v15,s[72:75],s83 offen
v_mfma_f32_16x16x32_bf16 a[44:47],a[152:155],a[200:203],a[44:47]
s_add_u32 s83,s83,896
v_mfma_f32_16x16x32_bf16 a[76:79],a[152:155],a[208:211],a[76:79]
v_mfma_f32_16x16x32_bf16 a[76:79],a[156:159],a[212:215],a[76:79]
v_mfma_f32_16x16x32_bf16 a[108:111],a[156:159],a[220:223],a[108:111]
v_mfma_f32_16x16x32_bf16 a[108:111],a[152:155],a[216:219],a[108:111]
s_waitcnt vmcnt(17) lgkmcnt(0)
v_mfma_f32_16x16x32_bf16 a[16:19],a[160:163],a[192:195],a[16:19]
s_barrier
ds_read_b128 a[224:227],v20
v_mfma_f32_16x16x32_bf16 a[16:19],a[164:167],a[196:199],a[16:19]
ds_read_b64_tr_b16 a[128:129],v22 offset:1024
v_mfma_f32_16x16x32_bf16 a[48:51],a[164:167],a[204:207],a[48:51]
ds_read_b64_tr_b16 a[130:131],v22 offset:3072
v_mfma_f32_16x16x32_bf16 a[48:51],a[160:163],a[200:203],a[48:51]
ds_read_b128 a[228:231],v21
)A"
R"A(v_mfma_f32_16x16x32_bf16 a[80:83],a[160:163],a[208:211],a[80:83]
ds_read_b64_tr_b16 a[132:133],v22 offset:17408
v_mfma_f32_16x16x32_bf16 a[80:83],a[164:167],a[212:215],a[80:83]
ds_read_b64_tr_b16 a[134:135],v22 offset:19456
v_mfma_f32_16x16x32_bf16 a[112:115],a[164:167],a[220:223],a[112:115]
ds_read_b128 a[232:235],v20 offset:2048
v_mfma_f32_16x16x32_bf16 a[112:115],a[160:163],a[216:219],a[112:115]
ds_read_b128 a[236:239],v21 offset:2048
v_mfma_f32_16x16x32_bf16 a[20:23],a[168:171],a[192:195],a[20:23]
ds_read_b128 a[240:243],v20 offset:4096
v_mfma_f32_16x16x32_bf16 a[20:23],a[172:175],a[196:199],a[20:23]
ds_read_b128 a[244:247],v21 offset:4096
v_mfma_f32_16x16x32_bf16 a[52:55],a[172:175],a[204:207],a[52:55]
ds_read_b128 a[248:251],v20 offset:6144
v_xor_b32 v20,131072,v20
v_mfma_f32_16x16x32_bf16 a[52:55],a[168:171],a[200:203],a[52:55]
ds_read_b128 a[252:255],v21 offset:6144
v_xor_b32 v21,131072,v21
v_mfma_f32_16x16x32_bf16 a[84:87],a[168:171],a[208:211],a[84:87]
ds_read_b64_tr_b16 a[136:137],v22 offset:1032
v_mfma_f32_16x16x32_bf16 a[84:87],a[172:175],a[212:215],a[84:87]
ds_read_b64_tr_b16 a[138:139],v22 offset:3080
v_mfma_f32_16x16x32_bf16 a[116:119],a[172:175],a[220:223],a[116:119]
ds_read_b64_tr_b16 a[140:141],v22 offset:17416
v_mfma_f32_16x16x32_bf16 a[116:119],a[168:171],a[216:219],a[116:119]
ds_read_b64_tr_b16 a[142:143],v22 offset:19464
v_mfma_f32_16x16x32_bf16 a[24:27],a[176:179],a[192:195],a[24:27]
ds_read_b64_tr_b16 a[144:145],v22 offset:1280
v_mfma_f32_16x16x32_bf16 a[24:27],a[180:183],a[196:199],a[24:27]
ds_read_b64_tr_b16 a[146:147],v22 offset:3328
v_mfma_f32_16x16x32_bf16 a[56:59],a[180:183],a[204:207],a[56:59]
ds_read_b64_tr_b16 a[148:149],v22 offset:17664
v_mfma_f32_16x16x32_bf16 a[56:59],a[176:179],a[200:203],a[56:59]
ds_read_b64_tr_b16 a[150:151],v22 offset:19712
v_mfma_f32_16x16x32_bf16 a[88:91],a[176:179],a[208:211],a[88:91]
ds_read_b64_tr_b16 a[152:153],v22 offset:1288
v_mfma_f32_16x16x32_bf16 a[88:91],a[180:183],a[212:215],a[88:91]
ds_read_b64_tr_b16 a[154:155],v22 offset:3336
v_mfma_f32_16x16x32_bf16 a[120:123],a[180:183],a[220:223],a[120:123]
ds_read_b64_tr_b16 a[156:157],v22 offset:17672
v_mfma_f32_16x16x32_bf16 a[120:123],a[176:179],a[216:219],a[120:123]
ds_read_b64_tr_b16 a[158:159],v22 offset:19720
v_mfma_f32_16x16x32_bf16 a[28:31],a[184:187],a[192:195],a[28:31]
ds_read_b64_tr_b16 a[160:161],v22 offset:1536
v_mfma_f32_16x16x32_bf16 a[28:31],a[188:191],a[196:199],a[28:31]
ds_read_b64_tr_b16 a[162:163],v22 offset:3584
v_mfma_f32_16x16x32_bf16 a[60:63],a[188:191],a[204:207],a[60:63]
ds_read_b64_tr_b16 a[164:165],v22 offset:17920
v_mfma_f32_16x16x32_bf16 a[60:63],a[184:187],a[200:203],a[60:63]
ds_read_b64_tr_b16 a[166:167],v22 offset:19968
v_mfma_f32_16x16x32_bf16 a[92:95],a[184:187],a[208:211],a[92:95]
ds_read_b64_tr_b16 a[168:169],v22 offset:1544
v_mfma_f32_16x16x32_bf16 a[92:95],a[188:191],a[212:215],a[92:95]
ds_read_b64_tr_b16 a[170:171],v22 offset:3592
v_mfma_f32_16x16x32_bf16 a[124:127],a[188:191],a[220:223],a[124:127]
s_add_u32 m0,s58,0
ds_read_b64_tr_b16 a[172:173],v22 offset:17928
v_mfma_f32_16x16x32_bf16 a[124:127],a[184:187],a[216:219],a[124:127]
s_waitcnt lgkmcnt(7)
ds_read_b64_tr_b16 a[174:175],v22 offset:19976
v_mfma_f32_16x16x32_bf16 v[128:131],a[128:131],a[224:227],v[128:131]
ds_read_b64_tr_b16 a[176:177],v22 offset:1792
v_mfma_f32_16x16x32_bf16 v[128:131],a[132:135],a[228:231],v[128:131]
buffer_load_dwordx4 v56,s[32:35],s56 offen lds
v_mfma_f32_16x16x32_bf16 v[136:139],a[132:135],a[236:239],v[136:139]
ds_read_b64_tr_b16 a[178:179],v22 offset:3840
v_mfma_f32_16x16x32_bf16 v[136:139],a[128:131],a[232:235],v[136:139]
s_add_u32 m0,s58,1024
ds_read_b64_tr_b16 a[180:181],v22 offset:18176
v_mfma_f32_16x16x32_bf16 v[144:147],a[128:131],a[240:243],v[144:147]
ds_read_b64_tr_b16 a[182:183],v22 offset:20224
v_mfma_f32_16x16x32_bf16 v[144:147],a[132:135],a[244:247],v[144:147]
ds_read_b64_tr_b16 a[184:185],v22 offset:1800
v_mfma_f32_16x16x32_bf16 v[152:155],a[132:135],a[252:255],v[152:155]
buffer_load_dwordx4 v57,s[32:35],s56 offen lds
v_mfma_f32_16x16x32_bf16 v[152:155],a[128:131],a[248:251],v[152:155]
ds_read_b64_tr_b16 a[186:187],v22 offset:3848
v_mfma_f32_16x16x32_bf16 v[132:135],a[136:139],a[224:227],v[132:135]
ds_read_b64_tr_b16 a[188:189],v22 offset:18184
v_mfma_f32_16x16x32_bf16 v[132:135],a[140:143],a[228:231],v[132:135]
s_add_u32 m0,s58,2048
ds_read_b64_tr_b16 a[190:191],v22 offset:20232
v_mfma_f32_16x16x32_bf16 v[140:143],a[140:143],a[236:239],v[140:143]
ds_read_b64_tr_b16 a[128:129],v22
v_mfma_f32_16x16x32_bf16 v[140:143],a[136:139],a[232:235],v[140:143]
ds_read_b64_tr_b16 a[130:131],v22 offset:2048
v_mfma_f32_16x16x32_bf16 v[148:151],a[136:139],a[240:243],v[148:151]
buffer_load_dwordx4 v58,s[32:35],s56 offen lds
v_mfma_f32_16x16x32_bf16 v[148:151],a[140:143],a[244:247],v[148:151]
ds_read_b64_tr_b16 a[132:133],v22 offset:16384
v_mfma_f32_16x16x32_bf16 v[156:159],a[140:143],a[252:255],v[156:159]
s_add_u32 m0,s58,3072
ds_read_b64_tr_b16 a[134:135],v22 offset:18432
v_mfma_f32_16x16x32_bf16 v[156:159],a[136:139],a[248:251],v[156:159]
ds_read_b64_tr_b16 a[136:137],v22 offset:8
v_mfma_f32_16x16x32_bf16 v[160:163],a[144:147],a[224:227],v[160:163]
ds_read_b64_tr_b16 a[138:139],v22 offset:2056
v_mfma_f32_16x16x32_bf16 v[160:163],a[148:151],a[228:231],v[160:163]
buffer_load_dwordx4 v59,s[32:35],s56 offen lds
v_mfma_f32_16x16x32_bf16 v[168:171],a[148:151],a[236:239],v[168:171]
ds_read_b64_tr_b16 a[140:141],v22 offset:16392
v_mfma_f32_16x16x32_bf16 v[168:171],a[144:147],a[232:235],v[168:171]
ds_read_b64_tr_b16 a[142:143],v22 offset:18440
v_mfma_f32_16x16x32_bf16 v[176:179],a[144:147],a[240:243],v[176:179]
s_add_u32 m0,s58,4096
v_mfma_f32_16x16x32_bf16 v[176:179],a[148:151],a[244:247],v[176:179]
v_mfma_f32_16x16x32_bf16 v[184:187],a[148:151],a[252:255],v[184:187]
ds_read_b64_tr_b16 a[148:149],v22 offset:16640
v_mfma_f32_16x16x32_bf16 v[184:187],a[144:147],a[248:251],v[184:187]
buffer_load_dwordx4 v60,s[32:35],s56 offen lds
v_mfma_f32_16x16x32_bf16 v[164:167],a[152:155],a[224:227],v[164:167]
ds_read_b64_tr_b16 a[144:145],v22 offset:256
v_mfma_f32_16x16x32_bf16 v[164:167],a[156:159],a[228:231],v[164:167]
s_add_u32 m0,s58,5120
ds_read_b64_tr_b16 a[146:147],v22 offset:2304
v_mfma_f32_16x16x32_bf16 v[172:175],a[156:159],a[236:239],v[172:175]
ds_read_b64_tr_b16 a[150:151],v22 offset:18688
v_mfma_f32_16x16x32_bf16 v[172:175],a[152:155],a[232:235],v[172:175]
v_mfma_f32_16x16x32_bf16 v[180:183],a[152:155],a[240:243],v[180:183]
buffer_load_dwordx4 v61,s[32:35],s56 offen lds
v_mfma_f32_16x16x32_bf16 v[180:183],a[156:159],a[244:247],v[180:183]
v_mfma_f32_16x16x32_bf16 v[188:191],a[156:159],a[252:255],v[188:191]
ds_read_b64_tr_b16 a[156:157],v22 offset:16648
v_mfma_f32_16x16x32_bf16 v[188:191],a[152:155],a[248:251],v[188:191]
s_add_u32 m0,s58,6144
ds_read_b64_tr_b16 a[152:153],v22 offset:264
s_waitcnt lgkmcnt(5)
v_mfma_f32_16x16x32_bf16 v[192:195],a[160:163],a[224:227],v[192:195]
ds_read_b64_tr_b16 a[154:155],v22 offset:2312
v_mfma_f32_16x16x32_bf16 v[192:195],a[164:167],a[228:231],v[192:195]
ds_read_b64_tr_b16 a[158:159],v22 offset:18696
v_mfma_f32_16x16x32_bf16 v[200:203],a[164:167],a[236:239],v[200:203]
buffer_load_dwordx4 v62,s[32:35],s56 offen lds
v_mfma_f32_16x16x32_bf16 v[200:203],a[160:163],a[232:235],v[200:203]
v_mfma_f32_16x16x32_bf16 v[208:211],a[160:163],a[240:243],v[208:211]
s_add_u32 m0,s58,7168
s_add_u32 s58,s58,32768
v_mfma_f32_16x16x32_bf16 v[208:211],a[164:167],a[244:247],v[208:211]
s_cmp_ge_u32 s58,131072
s_cselect_b32 s62,98304,0
s_sub_u32 s58,s58,s62
v_mfma_f32_16x16x32_bf16 v[216:219],a[164:167],a[252:255],v[216:219]
ds_read_b64_tr_b16 a[164:165],v22 offset:16896
v_mfma_f32_16x16x32_bf16 v[216:219],a[160:163],a[248:251],v[216:219]
buffer_load_dwordx4 v63,s[32:35],s56 offen lds
v_mfma_f32_16x16x32_bf16 v[196:199],a[168:171],a[224:227],v[196:199]
s_add_u32 s56,s56,524288
ds_read_b64_tr_b16 a[160:161],v22 offset:512
v_mfma_f32_16x16x32_bf16 v[196:199],a[172:175],a[228:231],v[196:199]
ds_read_b64_tr_b16 a[162:163],v22 offset:2560
v_mfma_f32_16x16x32_bf16 v[204:207],a[172:175],a[236:239],v[204:207]
s_add_u32 m0,s59,0
ds_read_b64_tr_b16 a[166:167],v22 offset:18944
v_mfma_f32_16x16x32_bf16 v[204:207],a[168:171],a[232:235],v[204:207]
v_mfma_f32_16x16x32_bf16 v[212:215],a[168:171],a[240:243],v[212:215]
v_mfma_f32_16x16x32_bf16 v[212:215],a[172:175],a[244:247],v[212:215]
buffer_load_dwordx4 v48,s[28:31],s57 offen lds
v_mfma_f32_16x16x32_bf16 v[220:223],a[172:175],a[252:255],v[220:223]
ds_read_b64_tr_b16 a[172:173],v22 offset:16904
v_mfma_f32_16x16x32_bf16 v[220:223],a[168:171],a[248:251],v[220:223]
ds_read_b64_tr_b16 a[168:169],v22 offset:520
v_mfma_f32_16x16x32_bf16 v[224:227],a[176:179],a[224:227],v[224:227]
ds_read_b64_tr_b16 a[170:171],v22 offset:2568
v_mfma_f32_16x16x32_bf16 v[224:227],a[180:183],a[228:231],v[224:227]
ds_read_b64_tr_b16 a[174:175],v22 offset:18952
v_mfma_f32_16x16x32_bf16 v[232:235],a[180:183],a[236:239],v[232:235]
buffer_load_dwordx4 v49,s[28:31],s57 offen offset:1024 lds
v_mfma_f32_16x16x32_bf16 v[232:235],a[176:179],a[232:235],v[232:235]
v_mfma_f32_16x16x32_bf16 v[240:243],a[176:179],a[240:243],v[240:243]
v_mfma_f32_16x16x32_bf16 v[240:243],a[180:183],a[244:247],v[240:243]
v_mfma_f32_16x16x32_bf16 v[248:251],a[180:183],a[252:255],v[248:251]
ds_read_b64_tr_b16 a[180:181],v22 offset:17152
v_mfma_f32_16x16x32_bf16 v[248:251],a[176:179],a[248:251],v[248:251]
ds_read_b64_tr_b16 a[176:177],v22 offset:768
v_mfma_f32_16x16x32_bf16 v[228:231],a[184:187],a[224:227],v[228:231]
buffer_load_dwordx4 v50,s[28:31],s57 offen offset:2048 lds
v_mfma_f32_16x16x32_bf16 v[228:231],a[188:191],a[228:231],v[228:231]
ds_read_b64_tr_b16 a[178:179],v22 offset:2816
v_mfma_f32_16x16x32_bf16 v[236:239],a[188:191],a[236:239],v[236:239]
ds_read_b64_tr_b16 a[182:183],v22 offset:19200
v_mfma_f32_16x16x32_bf16 v[236:239],a[184:187],a[232:235],v[236:239]
v_mfma_f32_16x16x32_bf16 v[244:247],a[184:187],a[240:243],v[244:247]
v_mfma_f32_16x16x32_bf16 v[244:247],a[188:191],a[244:247],v[244:247]
buffer_load_dwordx4 v51,s[28:31],s57 offen offset:3072 lds
v_mfma_f32_16x16x32_bf16 v[252:255],a[188:191],a[252:255],v[252:255]
ds_read_b64_tr_b16 a[188:189],v22 offset:17160
v_mfma_f32_16x16x32_bf16 v[252:255],a[184:187],a[248:251],v[252:255]
ds_read_b64_tr_b16 a[184:185],v22 offset:776
v_mfma_f32_16x16x32_bf16 a[0:3],a[128:131],a[224:227],a[0:3]
s_add_u32 m0,s59,4096
s_xor_b32 s59,s59,131072
ds_read_b64_tr_b16 a[186:187],v22 offset:2824
v_mfma_f32_16x16x32_bf16 a[0:3],a[132:135],a[228:231],a[0:3]
ds_read_b64_tr_b16 a[190:191],v22 offset:19208
v_add_u32 v22,s60,v23
v_mfma_f32_16x16x32_bf16 a[32:35],a[132:135],a[236:239],a[32:35]
v_mfma_f32_16x16x32_bf16 a[32:35],a[128:131],a[232:235],a[32:35]
buffer_load_dwordx4 v52,s[28:31],s57 offen lds
v_mfma_f32_16x16x32_bf16 a[64:67],a[128:131],a[240:243],a[64:67]
v_mfma_f32_16x16x32_bf16 a[64:67],a[132:135],a[244:247],a[64:67]
v_mfma_f32_16x16x32_bf16 a[96:99],a[132:135],a[252:255],a[96:99]
v_mfma_f32_16x16x32_bf16 a[96:99],a[128:131],a[248:251],a[96:99]
v_mfma_f32_16x16x32_bf16 a[4:7],a[136:139],a[224:227],a[4:7]
buffer_load_dwordx4 v53,s[28:31],s57 offen offset:1024 lds
v_mfma_f32_16x16x32_bf16 a[4:7],a[140:143],a[228:231],a[4:7]
v_mfma_f32_16x16x32_bf16 a[36:39],a[140:143],a[236:239],a[36:39]
v_mfma_f32_16x16x32_bf16 a[36:39],a[136:139],a[232:235],a[36:39]
v_mfma_f32_16x16x32_bf16 a[68:71],a[136:139],a[240:243],a[68:71]
v_mfma_f32_16x16x32_bf16 a[68:71],a[140:143],a[244:247],a[68:71]
v_mfma_f32_16x16x32_bf16 a[100:103],a[140:143],a[252:255],a[100:103]
buffer_load_dwordx4 v54,s[28:31],s57 offen offset:2048 lds
v_mfma_f32_16x16x32_bf16 a[100:103],a[136:139],a[248:251],a[100:103]
s_waitcnt lgkmcnt(0)
v_mfma_f32_16x16x32_bf16 a[8:11],a[144:147],a[224:227],a[8:11]
v_mfma_f32_16x16x32_bf16 a[8:11],a[148:151],a[228:231],a[8:11]
v_mfma_f32_16x16x32_bf16 a[40:43],a[148:151],a[236:239],a[40:43]
v_mfma_f32_16x16x32_bf16 a[40:43],a[144:147],a[232:235],a[40:43]
buffer_load_dwordx4 v55,s[28:31],s57 offen offset:3072 lds
v_mfma_f32_16x16x32_bf16 a[72:75],a[144:147],a[240:243],a[72:75]
)A"
R"A(s_add_u32 s57,s57,128
v_mfma_f32_16x16x32_bf16 a[72:75],a[148:151],a[244:247],a[72:75]
v_mfma_f32_16x16x32_bf16 a[104:107],a[148:151],a[252:255],a[104:107]
v_mfma_f32_16x16x32_bf16 a[104:107],a[144:147],a[248:251],a[104:107]
v_mfma_f32_16x16x32_bf16 a[12:15],a[152:155],a[224:227],a[12:15]
v_mfma_f32_16x16x32_bf16 a[12:15],a[156:159],a[228:231],a[12:15]
v_mfma_f32_16x16x32_bf16 a[44:47],a[156:159],a[236:239],a[44:47]
buffer_load_dword v16,v15,s[72:75],s83 offen
v_mfma_f32_16x16x32_bf16 a[44:47],a[152:155],a[232:235],a[44:47]
s_add_u32 s83,s83,896
v_mfma_f32_16x16x32_bf16 a[76:79],a[152:155],a[240:243],a[76:79]
v_mfma_f32_16x16x32_bf16 a[76:79],a[156:159],a[244:247],a[76:79]
v_mfma_f32_16x16x32_bf16 a[108:111],a[156:159],a[252:255],a[108:111]
v_mfma_f32_16x16x32_bf16 a[108:111],a[152:155],a[248:251],a[108:111]
s_waitcnt vmcnt(17) lgkmcnt(0)
v_mfma_f32_16x16x32_bf16 a[16:19],a[160:163],a[224:227],a[16:19]
s_barrier
ds_read_b128 a[192:195],v20
v_mfma_f32_16x16x32_bf16 a[16:19],a[164:167],a[228:231],a[16:19]
ds_read_b128 a[196:199],v21
v_mfma_f32_16x16x32_bf16 a[48:51],a[164:167],a[236:239],a[48:51]
ds_read_b128 a[200:203],v20 offset:2048
v_mfma_f32_16x16x32_bf16 a[48:51],a[160:163],a[232:235],a[48:51]
ds_read_b128 a[204:207],v21 offset:2048
v_mfma_f32_16x16x32_bf16 a[80:83],a[160:163],a[240:243],a[80:83]
ds_read_b128 a[208:211],v20 offset:4096
v_mfma_f32_16x16x32_bf16 a[80:83],a[164:167],a[244:247],a[80:83]
ds_read_b128 a[212:215],v21 offset:4096
v_mfma_f32_16x16x32_bf16 a[112:115],a[164:167],a[252:255],a[112:115]
buffer_load_dword v26,v24,s[88:91],s93 offen
v_mfma_f32_16x16x32_bf16 a[112:115],a[160:163],a[248:251],a[112:115]
ds_read_b128 a[216:219],v20 offset:6144
v_mfma_f32_16x16x32_bf16 a[20:23],a[168:171],a[224:227],a[20:23]
ds_read_b128 a[220:223],v21 offset:6144
v_mfma_f32_16x16x32_bf16 a[20:23],a[172:175],a[228:231],a[20:23]
ds_read_b64_tr_b16 a[128:129],v22 offset:1024
v_mfma_f32_16x16x32_bf16 a[52:55],a[172:175],a[236:239],a[52:55]
ds_read_b64_tr_b16 a[130:131],v22 offset:3072
v_mfma_f32_16x16x32_bf16 a[52:55],a[168:171],a[232:235],a[52:55]
ds_read_b64_tr_b16 a[132:133],v22 offset:17408
v_mfma_f32_16x16x32_bf16 a[84:87],a[168:171],a[240:243],a[84:87]
ds_read_b64_tr_b16 a[134:135],v22 offset:19456
v_mfma_f32_16x16x32_bf16 a[84:87],a[172:175],a[244:247],a[84:87]
ds_read_b64_tr_b16 a[136:137],v22 offset:1032
v_mfma_f32_16x16x32_bf16 a[116:119],a[172:175],a[252:255],a[116:119]
ds_read_b64_tr_b16 a[138:139],v22 offset:3080
v_mfma_f32_16x16x32_bf16 a[116:119],a[168:171],a[248:251],a[116:119]
ds_read_b64_tr_b16 a[140:141],v22 offset:17416
v_mfma_f32_16x16x32_bf16 a[24:27],a[176:179],a[224:227],a[24:27]
ds_read_b64_tr_b16 a[142:143],v22 offset:19464
v_mfma_f32_16x16x32_bf16 a[24:27],a[180:183],a[228:231],a[24:27]
ds_read_b64_tr_b16 a[144:145],v22 offset:1280
v_mfma_f32_16x16x32_bf16 a[56:59],a[180:183],a[236:239],a[56:59]
ds_read_b64_tr_b16 a[146:147],v22 offset:3328
v_mfma_f32_16x16x32_bf16 a[56:59],a[176:179],a[232:235],a[56:59]
ds_read_b64_tr_b16 a[148:149],v22 offset:17664
v_mfma_f32_16x16x32_bf16 a[88:91],a[176:179],a[240:243],a[88:91]
ds_read_b64_tr_b16 a[150:151],v22 offset:19712
v_mfma_f32_16x16x32_bf16 a[88:91],a[180:183],a[244:247],a[88:91]
ds_read_b64_tr_b16 a[152:153],v22 offset:1288
v_mfma_f32_16x16x32_bf16 a[120:123],a[180:183],a[252:255],a[120:123]
ds_read_b64_tr_b16 a[154:155],v22 offset:3336
v_mfma_f32_16x16x32_bf16 a[120:123],a[176:179],a[248:251],a[120:123]
ds_read_b64_tr_b16 a[156:157],v22 offset:17672
v_mfma_f32_16x16x32_bf16 a[28:31],a[184:187],a[224:227],a[28:31]
ds_read_b64_tr_b16 a[158:159],v22 offset:19720
v_mfma_f32_16x16x32_bf16 a[28:31],a[188:191],a[228:231],a[28:31]
v_mfma_f32_16x16x32_bf16 a[60:63],a[188:191],a[236:239],a[60:63]
v_mfma_f32_16x16x32_bf16 a[60:63],a[184:187],a[232:235],a[60:63]
v_mfma_f32_16x16x32_bf16 a[92:95],a[184:187],a[240:243],a[92:95]
v_mfma_f32_16x16x32_bf16 a[92:95],a[188:191],a[244:247],a[92:95]
v_mfma_f32_16x16x32_bf16 a[124:127],a[188:191],a[252:255],a[124:127]
s_cmp_lg_u32 s53,0
v_mfma_f32_16x16x32_bf16 a[124:127],a[184:187],a[248:251],a[124:127]
s_cbranch_scc1 L_qp_%=
L_qpx_%=:
L_tailseg_%=:
s_waitcnt lgkmcnt(1)
v_mfma_f32_16x16x32_bf16 v[128:131],a[128:131],a[192:195],v[128:131]
s_add_u32 m0,98304,s51
ds_read_b64_tr_b16 a[160:161],v22 offset:1536
v_xor_b32 v20,131072,v20
v_mfma_f32_16x16x32_bf16 v[128:131],a[132:135],a[196:199],v[128:131]
s_add_u32 s54,s54,16384
buffer_load_dwordx4 v4,s[20:23],s47 offen nt lds
v_mfma_f32_16x16x32_bf16 v[136:139],a[132:135],a[204:207],v[136:139]
s_and_b32 s46,s54,0xfffc0000
ds_read_b64_tr_b16 a[162:163],v22 offset:3584
v_xor_b32 v21,131072,v21
v_mfma_f32_16x16x32_bf16 v[136:139],a[128:131],a[200:203],v[136:139]
s_add_u32 s54,s54,16384
ds_read_b64_tr_b16 a[164:165],v22 offset:17920
v_and_b32 v18,48,v6
v_mfma_f32_16x16x32_bf16 v[144:147],a[128:131],a[208:211],v[144:147]
s_mov_b32 s48,-98304
ds_read_b64_tr_b16 a[166:167],v22 offset:19968
v_lshrrev_b32 v19,4,v6
v_mfma_f32_16x16x32_bf16 v[144:147],a[132:135],a[212:215],v[144:147]
s_lshr_b32 s79,%1,3
ds_read_b64_tr_b16 a[168:169],v22 offset:1544
v_lshlrev_b32 v18,2,v18
v_mfma_f32_16x16x32_bf16 v[152:155],a[132:135],a[220:223],v[152:155]
s_mul_i32 s79,s79,7
ds_read_b64_tr_b16 a[170:171],v22 offset:3592
v_and_b32 v19,48,v19
v_mfma_f32_16x16x32_bf16 v[152:155],a[128:131],a[216:219],v[152:155]
s_mov_b32 s60,148
ds_read_b64_tr_b16 a[172:173],v22 offset:17928
v_add_u32 v18,v18,v19
v_mfma_f32_16x16x32_bf16 v[132:135],a[136:139],a[192:195],v[132:135]
s_lshr_b32 s79,s79,5
ds_read_b64_tr_b16 a[174:175],v22 offset:19976
v_add_u32 v18,98304,v18
v_mfma_f32_16x16x32_bf16 v[132:135],a[140:143],a[196:199],v[132:135]
s_sub_u32 s60,s60,s79
s_lshl_b32 s94,s60,2
ds_read_b64_tr_b16 a[176:177],v22 offset:1792
v_mfma_f32_16x16x32_bf16 v[140:143],a[140:143],a[204:207],v[140:143]
s_sub_u32 s94,s94,3
s_mul_i32 s83,s41,5
ds_read_b64_tr_b16 a[178:179],v22 offset:3840
v_mfma_f32_16x16x32_bf16 v[140:143],a[136:139],a[200:203],v[140:143]
s_lshl_b32 s44,s40,4
s_add_u32 s83,s83,s42
ds_read_b64_tr_b16 a[180:181],v22 offset:18176
v_mfma_f32_16x16x32_bf16 v[148:151],a[136:139],a[208:211],v[148:151]
s_and_b32 s44,s44,32
ds_read_b64_tr_b16 a[182:183],v22 offset:20224
v_subrev_u32 v25,s51,v4
v_mfma_f32_16x16x32_bf16 v[148:151],a[140:143],a[212:215],v[148:151]
s_add_u32 s83,s83,15
s_lshl_b32 s80,s40,10
ds_read_b64_tr_b16 a[184:185],v22 offset:1800
v_mfma_f32_16x16x32_bf16 v[156:159],a[140:143],a[220:223],v[156:159]
s_and_b32 s83,s83,15
ds_read_b64_tr_b16 a[186:187],v22 offset:3848
v_xor_b32 v25,s44,v25
v_mfma_f32_16x16x32_bf16 v[156:159],a[136:139],a[216:219],v[156:159]
s_add_u32 s83,s83,16
ds_read_b64_tr_b16 a[188:189],v22 offset:18184
v_add_u32 v25,s80,v25
v_mfma_f32_16x16x32_bf16 v[160:163],a[144:147],a[192:195],v[160:163]
s_add_u32 s93,s92,L_att0x_%=-L_pc_%=
v_readfirstlane_b32 s68,v13
s_nop 1
s_add_u32 s68,s68,1
v_mfma_f32_16x16x32_bf16 v[160:163],a[148:151],a[196:199],v[160:163]
s_xor_b32 s69,s68,0x9e3779b9
buffer_load_dwordx4 v4,s[20:23],s47 offen offset:1024 nt lds
v_mfma_f32_16x16x32_bf16 v[168:171],a[148:151],a[204:207],v[168:171]
s_mov_b32 s50,32768
s_mov_b32 s49,98304
ds_read_b64_tr_b16 a[190:191],v22 offset:20232
v_mfma_f32_16x16x32_bf16 v[168:171],a[144:147],a[200:203],v[168:171]
s_mov_b32 s45,0x1249
s_add_u32 s80,s80,131072
ds_read_b64_tr_b16 a[128:129],v22
v_mfma_f32_16x16x32_bf16 v[176:179],a[144:147],a[208:211],v[176:179]
s_lshl_b32 s83,s83,14
ds_read_b64_tr_b16 a[130:131],v22 offset:2048
v_mfma_f32_16x16x32_bf16 v[176:179],a[148:151],a[212:215],v[176:179]
ds_read_b64_tr_b16 a[132:133],v22 offset:16384
v_mfma_f32_16x16x32_bf16 v[184:187],a[148:151],a[220:223],v[184:187]
ds_read_b64_tr_b16 a[134:135],v22 offset:18432
v_mfma_f32_16x16x32_bf16 v[184:187],a[144:147],a[216:219],v[184:187]
ds_read_b64_tr_b16 a[144:145],v22 offset:8
v_mfma_f32_16x16x32_bf16 v[164:167],a[152:155],a[192:195],v[164:167]
s_waitcnt lgkmcnt(6)
ds_read_b64_tr_b16 a[146:147],v22 offset:2056
v_mfma_f32_16x16x32_bf16 v[164:167],a[156:159],a[196:199],v[164:167]
ds_read_b64_tr_b16 a[148:149],v22 offset:16392
v_mfma_f32_16x16x32_bf16 v[172:175],a[156:159],a[204:207],v[172:175]
s_waitcnt vmcnt(4)
ds_read_b128 a[224:227],v20
v_mfma_f32_16x16x32_bf16 v[172:175],a[152:155],a[200:203],v[172:175]
ds_read_b64_tr_b16 a[150:151],v22 offset:18440
v_mfma_f32_16x16x32_bf16 v[180:183],a[152:155],a[208:211],v[180:183]
v_mfma_f32_16x16x32_bf16 v[180:183],a[156:159],a[212:215],v[180:183]
ds_read_b128 a[228:231],v21
v_mfma_f32_16x16x32_bf16 v[188:191],a[156:159],a[220:223],v[188:191]
v_mfma_f32_16x16x32_bf16 v[188:191],a[152:155],a[216:219],v[188:191]
ds_read_b128 a[232:235],v20 offset:2048
v_mfma_f32_16x16x32_bf16 v[192:195],a[160:163],a[192:195],v[192:195]
v_mfma_f32_16x16x32_bf16 v[192:195],a[164:167],a[196:199],v[192:195]
buffer_load_dwordx4 v5,s[20:23],s47 offen offset:2048 nt lds
v_mfma_f32_16x16x32_bf16 v[200:203],a[164:167],a[204:207],v[200:203]
ds_read_b128 a[236:239],v21 offset:2048
v_mfma_f32_16x16x32_bf16 v[200:203],a[160:163],a[200:203],v[200:203]
v_mfma_f32_16x16x32_bf16 v[208:211],a[160:163],a[208:211],v[208:211]
ds_read_b128 a[240:243],v20 offset:4096
v_mfma_f32_16x16x32_bf16 v[208:211],a[164:167],a[212:215],v[208:211]
v_mfma_f32_16x16x32_bf16 v[216:219],a[164:167],a[220:223],v[216:219]
ds_read_b128 a[244:247],v21 offset:4096
v_mfma_f32_16x16x32_bf16 v[216:219],a[160:163],a[216:219],v[216:219]
ds_read_b64_tr_b16 a[160:161],v22 offset:256
v_mfma_f32_16x16x32_bf16 v[196:199],a[168:171],a[192:195],v[196:199]
ds_read_b128 a[248:251],v20 offset:6144
ds_read_b128 a[252:255],v21 offset:6144
v_mfma_f32_16x16x32_bf16 v[196:199],a[172:175],a[196:199],v[196:199]
v_mfma_f32_16x16x32_bf16 v[204:207],a[172:175],a[204:207],v[204:207]
v_mfma_f32_16x16x32_bf16 v[204:207],a[168:171],a[200:203],v[204:207]
v_mfma_f32_16x16x32_bf16 v[212:215],a[168:171],a[208:211],v[212:215]
ds_read_b64_tr_b16 a[162:163],v22 offset:2304
v_mfma_f32_16x16x32_bf16 v[212:215],a[172:175],a[212:215],v[212:215]
ds_read_b64_tr_b16 a[164:165],v22 offset:16640
v_mfma_f32_16x16x32_bf16 v[220:223],a[172:175],a[220:223],v[220:223]
s_waitcnt vmcnt(4) lgkmcnt(2)
ds_read_b64_tr_b16 a[166:167],v22 offset:18688
v_mfma_f32_16x16x32_bf16 v[220:223],a[168:171],a[216:219],v[220:223]
s_barrier
ds_read_b64_tr_b16 a[136:137],v22 offset:32768
v_mfma_f32_16x16x32_bf16 v[224:227],a[176:179],a[192:195],v[224:227]
buffer_load_dwordx4 v5,s[20:23],s47 offen offset:3072 nt lds
v_mfma_f32_16x16x32_bf16 v[224:227],a[180:183],a[196:199],v[224:227]
ds_read_b64_tr_b16 a[138:139],v22 offset:34816
v_mfma_f32_16x16x32_bf16 v[232:235],a[180:183],a[204:207],v[232:235]
ds_read_b64_tr_b16 a[140:141],v22 offset:49152
v_mfma_f32_16x16x32_bf16 v[232:235],a[176:179],a[200:203],v[232:235]
ds_read_b64_tr_b16 a[142:143],v22 offset:51200
v_mfma_f32_16x16x32_bf16 v[240:243],a[176:179],a[208:211],v[240:243]
ds_read_b64_tr_b16 a[152:153],v22 offset:32776
v_mfma_f32_16x16x32_bf16 v[240:243],a[180:183],a[212:215],v[240:243]
ds_read_b64_tr_b16 a[154:155],v22 offset:34824
v_mfma_f32_16x16x32_bf16 v[248:251],a[180:183],a[220:223],v[248:251]
ds_read_b64_tr_b16 a[156:157],v22 offset:49160
v_mfma_f32_16x16x32_bf16 v[248:251],a[176:179],a[216:219],v[248:251]
ds_read_b64_tr_b16 a[158:159],v22 offset:51208
v_mfma_f32_16x16x32_bf16 v[228:231],a[184:187],a[192:195],v[228:231]
ds_read_b64_tr_b16 a[168:169],v22 offset:33024
v_mfma_f32_16x16x32_bf16 v[228:231],a[188:191],a[196:199],v[228:231]
ds_read_b64_tr_b16 a[170:171],v22 offset:35072
v_mfma_f32_16x16x32_bf16 v[236:239],a[188:191],a[204:207],v[236:239]
ds_read_b64_tr_b16 a[172:173],v22 offset:49408
v_mfma_f32_16x16x32_bf16 v[236:239],a[184:187],a[200:203],v[236:239]
ds_read_b64_tr_b16 a[174:175],v22 offset:51456
v_mfma_f32_16x16x32_bf16 v[244:247],a[184:187],a[208:211],v[244:247]
ds_read_b64_tr_b16 a[176:177],v22 offset:264
v_mfma_f32_16x16x32_bf16 v[244:247],a[188:191],a[212:215],v[244:247]
s_add_u32 m0,98304,s52
ds_read_b64_tr_b16 a[178:179],v22 offset:2312
v_mfma_f32_16x16x32_bf16 v[252:255],a[188:191],a[220:223],v[252:255]
ds_read_b64_tr_b16 a[180:181],v22 offset:16648
)A"
R"A(v_mfma_f32_16x16x32_bf16 v[252:255],a[184:187],a[216:219],v[252:255]
ds_read_b64_tr_b16 a[182:183],v22 offset:18696
v_mfma_f32_16x16x32_bf16 a[0:3],a[128:131],a[192:195],a[0:3]
buffer_load_dwordx4 v4,s[24:27],s47 offen nt lds
v_mfma_f32_16x16x32_bf16 a[0:3],a[132:135],a[196:199],a[0:3]
s_waitcnt lgkmcnt(6)
ds_read_b64_tr_b16 a[184:185],v22 offset:33032
v_mfma_f32_16x16x32_bf16 a[0:3],a[136:139],a[224:227],a[0:3]
ds_read_b64_tr_b16 a[186:187],v22 offset:35080
v_mfma_f32_16x16x32_bf16 a[0:3],a[140:143],a[228:231],a[0:3]
ds_read_b64_tr_b16 a[188:189],v22 offset:49416
v_mfma_f32_16x16x32_bf16 a[32:35],a[140:143],a[236:239],a[32:35]
ds_read_b64_tr_b16 a[190:191],v22 offset:51464
v_mfma_f32_16x16x32_bf16 a[32:35],a[136:139],a[232:235],a[32:35]
v_mfma_f32_16x16x32_bf16 a[32:35],a[132:135],a[204:207],a[32:35]
v_mfma_f32_16x16x32_bf16 a[32:35],a[128:131],a[200:203],a[32:35]
v_mfma_f32_16x16x32_bf16 a[64:67],a[128:131],a[208:211],a[64:67]
v_mfma_f32_16x16x32_bf16 a[64:67],a[132:135],a[212:215],a[64:67]
v_accvgpr_read_b32 v64,a0
v_accvgpr_read_b32 v65,a1
v_mfma_f32_16x16x32_bf16 a[64:67],a[136:139],a[240:243],a[64:67]
v_mul_f32 v64,s61,v64
v_mul_f32 v65,s61,v65
v_mfma_f32_16x16x32_bf16 a[64:67],a[140:143],a[244:247],a[64:67]
v_accvgpr_read_b32 v66,a2
v_accvgpr_read_b32 v67,a3
v_mfma_f32_16x16x32_bf16 a[96:99],a[140:143],a[252:255],a[96:99]
ds_read_b64_tr_b16 a[140:141],v22 offset:49664
v_cvt_pk_bf16_f32 v64,v64,v65
v_mfma_f32_16x16x32_bf16 a[96:99],a[136:139],a[248:251],a[96:99]
ds_read_b64_tr_b16 a[136:137],v22 offset:33280
v_accvgpr_read_b32 v80,a32
v_mfma_f32_16x16x32_bf16 a[96:99],a[132:135],a[220:223],a[96:99]
ds_read_b64_tr_b16 a[132:133],v22 offset:16896
v_accvgpr_read_b32 v81,a33
v_mfma_f32_16x16x32_bf16 a[96:99],a[128:131],a[216:219],a[96:99]
ds_read_b64_tr_b16 a[128:129],v22 offset:512
v_mul_f32 v80,s61,v80
v_mfma_f32_16x16x32_bf16 a[4:7],a[144:147],a[192:195],a[4:7]
buffer_load_dwordx4 v5,s[24:27],s47 offen offset:1024 nt lds
v_mfma_f32_16x16x32_bf16 a[4:7],a[148:151],a[196:199],a[4:7]
ds_read_b64_tr_b16 a[130:131],v22 offset:2560
v_mul_f32 v81,s61,v81
v_mfma_f32_16x16x32_bf16 a[4:7],a[152:155],a[224:227],a[4:7]
ds_read_b64_tr_b16 a[134:135],v22 offset:18944
v_accvgpr_read_b32 v82,a34
v_mfma_f32_16x16x32_bf16 a[4:7],a[156:159],a[228:231],a[4:7]
ds_read_b64_tr_b16 a[138:139],v22 offset:35328
v_accvgpr_read_b32 v83,a35
v_mfma_f32_16x16x32_bf16 a[36:39],a[156:159],a[236:239],a[36:39]
ds_read_b64_tr_b16 a[142:143],v22 offset:51712
v_mul_f32 v66,s61,v66
v_mfma_f32_16x16x32_bf16 a[36:39],a[152:155],a[232:235],a[36:39]
v_mul_f32 v67,s61,v67
v_cvt_pk_bf16_f32 v80,v80,v81
v_mfma_f32_16x16x32_bf16 a[36:39],a[148:151],a[204:207],a[36:39]
v_mul_f32 v82,s61,v82
v_mul_f32 v83,s61,v83
v_mfma_f32_16x16x32_bf16 a[36:39],a[144:147],a[200:203],a[36:39]
v_cvt_pk_bf16_f32 v65,v66,v67
v_cvt_pk_bf16_f32 v81,v82,v83
v_mfma_f32_16x16x32_bf16 a[68:71],a[144:147],a[208:211],a[68:71]
v_accvgpr_read_b32 v112,a96
v_accvgpr_read_b32 v113,a97
v_mfma_f32_16x16x32_bf16 a[68:71],a[148:151],a[212:215],a[68:71]
v_accvgpr_read_b32 v66,a4
v_accvgpr_read_b32 v67,a5
v_mfma_f32_16x16x32_bf16 a[68:71],a[152:155],a[240:243],a[68:71]
v_mul_f32 v112,s61,v112
v_mul_f32 v113,s61,v113
v_mfma_f32_16x16x32_bf16 a[68:71],a[156:159],a[244:247],a[68:71]
v_accvgpr_read_b32 v114,a98
v_accvgpr_read_b32 v115,a99
v_mfma_f32_16x16x32_bf16 a[100:103],a[156:159],a[252:255],a[100:103]
ds_read_b64_tr_b16 a[156:157],v22 offset:49672
v_accvgpr_read_b32 v12,a6
v_mfma_f32_16x16x32_bf16 a[100:103],a[152:155],a[248:251],a[100:103]
ds_read_b64_tr_b16 a[152:153],v22 offset:33288
v_accvgpr_read_b32 v82,a36
v_mfma_f32_16x16x32_bf16 a[100:103],a[148:151],a[220:223],a[100:103]
ds_read_b64_tr_b16 a[148:149],v22 offset:16904
v_accvgpr_read_b32 v83,a37
v_mfma_f32_16x16x32_bf16 a[100:103],a[144:147],a[216:219],a[100:103]
ds_read_b64_tr_b16 a[144:145],v22 offset:520
v_accvgpr_read_b32 v13,a7
v_mfma_f32_16x16x32_bf16 a[8:11],a[160:163],a[192:195],a[8:11]
buffer_load_dwordx4 v4,s[24:27],s47 offen offset:2048 nt lds
v_mfma_f32_16x16x32_bf16 a[8:11],a[164:167],a[196:199],a[8:11]
ds_read_b64_tr_b16 a[146:147],v22 offset:2568
v_mul_f32 v66,s61,v66
v_mfma_f32_16x16x32_bf16 a[8:11],a[168:171],a[224:227],a[8:11]
s_waitcnt lgkmcnt(5)
ds_read_b64_tr_b16 a[150:151],v22 offset:18952
v_mul_f32 v67,s61,v67
v_mfma_f32_16x16x32_bf16 a[8:11],a[172:175],a[228:231],a[8:11]
ds_read_b64_tr_b16 a[154:155],v22 offset:35336
v_accvgpr_read_b32 v14,a38
v_mfma_f32_16x16x32_bf16 a[40:43],a[172:175],a[236:239],a[40:43]
ds_read_b64_tr_b16 a[158:159],v22 offset:51720
v_accvgpr_read_b32 v15,a39
v_mfma_f32_16x16x32_bf16 a[40:43],a[168:171],a[232:235],a[40:43]
v_mul_f32 v82,s61,v82
v_mul_f32 v83,s61,v83
v_mfma_f32_16x16x32_bf16 a[40:43],a[164:167],a[204:207],a[40:43]
v_cvt_pk_bf16_f32 v112,v112,v113
v_mul_f32 v114,s61,v114
v_mfma_f32_16x16x32_bf16 a[40:43],a[160:163],a[200:203],a[40:43]
v_mul_f32 v115,s61,v115
v_mul_f32 v12,s61,v12
v_mfma_f32_16x16x32_bf16 a[72:75],a[160:163],a[208:211],a[72:75]
v_mul_f32 v13,s61,v13
v_cvt_pk_bf16_f32 v66,v66,v67
v_mfma_f32_16x16x32_bf16 a[72:75],a[164:167],a[212:215],a[72:75]
v_accvgpr_read_b32 v68,a8
v_accvgpr_read_b32 v69,a9
v_mfma_f32_16x16x32_bf16 a[72:75],a[168:171],a[240:243],a[72:75]
v_mul_f32 v68,s61,v68
v_mul_f32 v69,s61,v69
v_mfma_f32_16x16x32_bf16 a[72:75],a[172:175],a[244:247],a[72:75]
v_accvgpr_read_b32 v70,a10
v_accvgpr_read_b32 v71,a11
v_mfma_f32_16x16x32_bf16 a[104:107],a[172:175],a[252:255],a[104:107]
ds_read_b64_tr_b16 a[172:173],v22 offset:49920
v_cvt_pk_bf16_f32 v68,v68,v69
v_mfma_f32_16x16x32_bf16 a[104:107],a[168:171],a[248:251],a[104:107]
ds_read_b64_tr_b16 a[168:169],v22 offset:33536
v_mul_f32 v70,s61,v70
v_mfma_f32_16x16x32_bf16 a[104:107],a[164:167],a[220:223],a[104:107]
ds_read_b64_tr_b16 a[164:165],v22 offset:17152
v_mul_f32 v71,s61,v71
v_mfma_f32_16x16x32_bf16 a[104:107],a[160:163],a[216:219],a[104:107]
ds_read_b64_tr_b16 a[160:161],v22 offset:768
v_mul_f32 v14,s61,v14
v_mfma_f32_16x16x32_bf16 a[12:15],a[176:179],a[192:195],a[12:15]
buffer_load_dwordx4 v5,s[24:27],s47 offen offset:3072 nt lds
v_mfma_f32_16x16x32_bf16 a[12:15],a[180:183],a[196:199],a[12:15]
s_add_u32 s47,s47,16384
s_and_b32 s47,s47,0x3ffff
s_or_b32 s47,s47,s46
v_mfma_f32_16x16x32_bf16 a[12:15],a[184:187],a[224:227],a[12:15]
s_and_b32 s46,s54,0xfffc0000
ds_read_b64_tr_b16 a[162:163],v22 offset:2816
v_mul_f32 v15,s61,v15
v_mfma_f32_16x16x32_bf16 a[12:15],a[188:191],a[228:231],a[12:15]
s_add_u32 s54,s54,16384
ds_read_b64_tr_b16 a[166:167],v22 offset:19200
v_cvt_pk_bf16_f32 v82,v82,v83
v_mfma_f32_16x16x32_bf16 a[44:47],a[188:191],a[236:239],a[44:47]
ds_read_b64_tr_b16 a[170:171],v22 offset:35584
v_cvt_pk_bf16_f32 v113,v114,v115
v_mfma_f32_16x16x32_bf16 a[44:47],a[184:187],a[232:235],a[44:47]
ds_read_b64_tr_b16 a[174:175],v22 offset:51968
v_cvt_pk_bf16_f32 v69,v70,v71
v_mfma_f32_16x16x32_bf16 a[44:47],a[180:183],a[204:207],a[44:47]
v_accvgpr_read_b32 v100,a72
v_accvgpr_read_b32 v101,a73
v_mfma_f32_16x16x32_bf16 a[44:47],a[176:179],a[200:203],a[44:47]
v_accvgpr_read_b32 v116,a104
v_accvgpr_read_b32 v117,a105
v_mfma_f32_16x16x32_bf16 a[76:79],a[176:179],a[208:211],a[76:79]
v_cvt_pk_bf16_f32 v67,v12,v13
v_cvt_pk_bf16_f32 v83,v14,v15
v_mfma_f32_16x16x32_bf16 a[76:79],a[180:183],a[212:215],a[76:79]
v_accvgpr_read_b32 v114,a100
v_accvgpr_read_b32 v115,a101
v_mfma_f32_16x16x32_bf16 a[76:79],a[184:187],a[240:243],a[76:79]
v_accvgpr_read_b32 v70,a12
v_accvgpr_read_b32 v71,a13
v_mfma_f32_16x16x32_bf16 a[76:79],a[188:191],a[244:247],a[76:79]
v_mul_f32 v100,s61,v100
v_mul_f32 v101,s61,v101
v_mfma_f32_16x16x32_bf16 a[108:111],a[188:191],a[252:255],a[108:111]
ds_read_b64_tr_b16 a[188:189],v22 offset:49928
v_accvgpr_read_b32 v102,a74
v_mfma_f32_16x16x32_bf16 a[108:111],a[184:187],a[248:251],a[108:111]
ds_read_b64_tr_b16 a[184:185],v22 offset:33544
v_accvgpr_read_b32 v103,a75
v_mfma_f32_16x16x32_bf16 a[108:111],a[180:183],a[220:223],a[108:111]
ds_read_b64_tr_b16 a[180:181],v22 offset:17160
v_mul_f32 v116,s61,v116
v_mfma_f32_16x16x32_bf16 a[108:111],a[176:179],a[216:219],a[108:111]
ds_read_b64_tr_b16 a[176:177],v22 offset:776
v_mul_f32 v117,s61,v117
v_mfma_f32_16x16x32_bf16 a[16:19],a[128:131],a[192:195],a[16:19]
ds_read_b64_tr_b16 a[178:179],v22 offset:2824
v_accvgpr_read_b32 v118,a106
v_mfma_f32_16x16x32_bf16 a[16:19],a[132:135],a[196:199],a[16:19]
ds_read_b64_tr_b16 a[182:183],v22 offset:19208
v_accvgpr_read_b32 v119,a107
v_mfma_f32_16x16x32_bf16 a[16:19],a[136:139],a[224:227],a[16:19]
ds_read_b64_tr_b16 a[186:187],v22 offset:35592
v_accvgpr_read_b32 v12,a102
v_mfma_f32_16x16x32_bf16 a[16:19],a[140:143],a[228:231],a[16:19]
s_add_u32 m0,0,s51
ds_read_b64_tr_b16 a[190:191],v22 offset:51976
v_accvgpr_read_b32 v13,a103
v_mfma_f32_16x16x32_bf16 a[48:51],a[140:143],a[236:239],a[48:51]
v_mul_f32 v114,s61,v114
v_mul_f32 v115,s61,v115
v_mfma_f32_16x16x32_bf16 a[48:51],a[136:139],a[232:235],a[48:51]
v_accvgpr_read_b32 v14,a14
v_accvgpr_read_b32 v15,a15
v_mfma_f32_16x16x32_bf16 a[48:51],a[132:135],a[204:207],a[48:51]
buffer_load_dwordx4 v4,s[20:23],s47 offen nt lds
v_mfma_f32_16x16x32_bf16 a[48:51],a[128:131],a[200:203],a[48:51]
v_mul_f32 v70,s61,v70
v_mul_f32 v71,s61,v71
v_mfma_f32_16x16x32_bf16 a[80:83],a[128:131],a[208:211],a[80:83]
v_cvt_pk_bf16_f32 v100,v100,v101
v_mul_f32 v102,s61,v102
v_mfma_f32_16x16x32_bf16 a[80:83],a[132:135],a[212:215],a[80:83]
v_mul_f32 v103,s61,v103
v_cvt_pk_bf16_f32 v116,v116,v117
v_mfma_f32_16x16x32_bf16 a[80:83],a[136:139],a[240:243],a[80:83]
v_mul_f32 v118,s61,v118
v_mul_f32 v119,s61,v119
v_mfma_f32_16x16x32_bf16 a[80:83],a[140:143],a[244:247],a[80:83]
v_mul_f32 v12,s61,v12
v_mul_f32 v13,s61,v13
v_mfma_f32_16x16x32_bf16 a[112:115],a[140:143],a[252:255],a[112:115]
ds_read_b64_tr_b16 a[140:141],v22 offset:50184
v_cvt_pk_bf16_f32 v114,v114,v115
v_mfma_f32_16x16x32_bf16 a[112:115],a[136:139],a[248:251],a[112:115]
ds_read_b64_tr_b16 a[136:137],v22 offset:33800
v_mul_f32 v14,s61,v14
v_mfma_f32_16x16x32_bf16 a[112:115],a[132:135],a[220:223],a[112:115]
ds_read_b64_tr_b16 a[132:133],v22 offset:50176
v_mul_f32 v15,s61,v15
v_mfma_f32_16x16x32_bf16 a[112:115],a[128:131],a[216:219],a[112:115]
s_waitcnt lgkmcnt(3)
ds_read_b64_tr_b16 a[128:129],v22 offset:33792
v_cvt_pk_bf16_f32 v70,v70,v71
v_mfma_f32_16x16x32_bf16 a[20:23],a[144:147],a[192:195],a[20:23]
ds_read_b64_tr_b16 a[130:131],v22 offset:35840
v_cvt_pk_bf16_f32 v101,v102,v103
v_mfma_f32_16x16x32_bf16 a[20:23],a[148:151],a[196:199],a[20:23]
ds_read_b64_tr_b16 a[134:135],v22 offset:52224
v_cvt_pk_bf16_f32 v117,v118,v119
v_mfma_f32_16x16x32_bf16 a[20:23],a[152:155],a[224:227],a[20:23]
ds_read_b64_tr_b16 a[138:139],v22 offset:35848
v_cvt_pk_bf16_f32 v115,v12,v13
v_mfma_f32_16x16x32_bf16 a[20:23],a[156:159],a[228:231],a[20:23]
ds_read_b64_tr_b16 a[142:143],v22 offset:52232
v_cvt_pk_bf16_f32 v71,v14,v15
v_mfma_f32_16x16x32_bf16 a[52:55],a[156:159],a[236:239],a[52:55]
v_accvgpr_read_b32 v102,a76
v_accvgpr_read_b32 v103,a77
v_mfma_f32_16x16x32_bf16 a[52:55],a[152:155],a[232:235],a[52:55]
v_accvgpr_read_b32 v118,a108
v_accvgpr_read_b32 v119,a109
v_mfma_f32_16x16x32_bf16 a[52:55],a[148:151],a[204:207],a[52:55]
buffer_load_dwordx4 v4,s[20:23],s47 offen offset:1024 nt lds
v_mfma_f32_16x16x32_bf16 a[52:55],a[144:147],a[200:203],a[52:55]
v_accvgpr_read_b32 v12,a78
v_accvgpr_read_b32 v13,a79
v_mfma_f32_16x16x32_bf16 a[84:87],a[144:147],a[208:211],a[84:87]
v_mul_f32 v102,s61,v102
v_mul_f32 v103,s61,v103
v_mfma_f32_16x16x32_bf16 a[84:87],a[148:151],a[212:215],a[84:87]
v_accvgpr_read_b32 v14,a110
v_accvgpr_read_b32 v15,a111
v_mfma_f32_16x16x32_bf16 a[84:87],a[152:155],a[240:243],a[84:87]
v_mul_f32 v118,s61,v118
v_mul_f32 v119,s61,v119
v_mfma_f32_16x16x32_bf16 a[84:87],a[156:159],a[244:247],a[84:87]
v_mul_f32 v12,s61,v12
v_mul_f32 v13,s61,v13
v_mfma_f32_16x16x32_bf16 a[116:119],a[156:159],a[252:255],a[116:119]
ds_read_b64_tr_b16 a[156:157],v22 offset:50440
v_cvt_pk_bf16_f32 v102,v102,v103
v_mfma_f32_16x16x32_bf16 a[116:119],a[152:155],a[248:251],a[116:119]
ds_read_b64_tr_b16 a[152:153],v22 offset:34056
v_mul_f32 v14,s61,v14
v_mfma_f32_16x16x32_bf16 a[116:119],a[148:151],a[220:223],a[116:119]
)A"
R"A(ds_read_b64_tr_b16 a[148:149],v22 offset:50432
v_mul_f32 v15,s61,v15
v_mfma_f32_16x16x32_bf16 a[116:119],a[144:147],a[216:219],a[116:119]
ds_read_b64_tr_b16 a[144:145],v22 offset:34048
v_cvt_pk_bf16_f32 v118,v118,v119
v_mfma_f32_16x16x32_bf16 a[24:27],a[160:163],a[192:195],a[24:27]
ds_read_b64_tr_b16 a[146:147],v22 offset:36096
v_cvt_pk_bf16_f32 v103,v12,v13
v_mfma_f32_16x16x32_bf16 a[24:27],a[164:167],a[196:199],a[24:27]
ds_read_b64_tr_b16 a[150:151],v22 offset:52480
v_cvt_pk_bf16_f32 v119,v14,v15
v_mfma_f32_16x16x32_bf16 a[24:27],a[168:171],a[224:227],a[24:27]
ds_read_b64_tr_b16 a[154:155],v22 offset:36104
v_accvgpr_read_b32 v96,a64
v_mfma_f32_16x16x32_bf16 a[24:27],a[172:175],a[228:231],a[24:27]
ds_read_b64_tr_b16 a[158:159],v22 offset:52488
v_accvgpr_read_b32 v97,a65
v_mfma_f32_16x16x32_bf16 a[56:59],a[172:175],a[236:239],a[56:59]
v_accvgpr_read_b32 v72,a16
v_accvgpr_read_b32 v73,a17
v_accvgpr_read_b32 v74,a18
v_accvgpr_read_b32 v75,a19
v_accvgpr_read_b32 v12,a20
v_accvgpr_read_b32 v13,a21
v_accvgpr_read_b32 v14,a22
v_accvgpr_read_b32 v15,a23
v_pk_mul_f32 v[72:73],s[60:61],v[72:73] op_sel:[1,0] op_sel_hi:[1,1]
v_pk_mul_f32 v[74:75],s[60:61],v[74:75] op_sel:[1,0] op_sel_hi:[1,1]
v_pk_mul_f32 v[12:13],s[60:61],v[12:13] op_sel:[1,0] op_sel_hi:[1,1]
v_pk_mul_f32 v[14:15],s[60:61],v[14:15] op_sel:[1,0] op_sel_hi:[1,1]
v_cvt_pk_bf16_f32 v72,v72,v73
v_cvt_pk_bf16_f32 v73,v74,v75
v_cvt_pk_bf16_f32 v74,v12,v13
v_cvt_pk_bf16_f32 v75,v14,v15
v_mfma_f32_16x16x32_bf16 a[56:59],a[168:171],a[232:235],a[56:59]
v_accvgpr_read_b32 v88,a48
v_accvgpr_read_b32 v89,a49
v_accvgpr_read_b32 v90,a50
v_accvgpr_read_b32 v91,a51
v_accvgpr_read_b32 v12,a52
v_accvgpr_read_b32 v13,a53
v_accvgpr_read_b32 v14,a54
v_accvgpr_read_b32 v15,a55
v_pk_mul_f32 v[88:89],s[60:61],v[88:89] op_sel:[1,0] op_sel_hi:[1,1]
v_pk_mul_f32 v[90:91],s[60:61],v[90:91] op_sel:[1,0] op_sel_hi:[1,1]
v_pk_mul_f32 v[12:13],s[60:61],v[12:13] op_sel:[1,0] op_sel_hi:[1,1]
v_pk_mul_f32 v[14:15],s[60:61],v[14:15] op_sel:[1,0] op_sel_hi:[1,1]
v_cvt_pk_bf16_f32 v88,v88,v89
v_cvt_pk_bf16_f32 v89,v90,v91
v_cvt_pk_bf16_f32 v90,v12,v13
v_cvt_pk_bf16_f32 v91,v14,v15
v_mfma_f32_16x16x32_bf16 a[56:59],a[164:167],a[204:207],a[56:59]
buffer_load_dwordx4 v5,s[20:23],s47 offen offset:2048 nt lds
v_mfma_f32_16x16x32_bf16 a[56:59],a[160:163],a[200:203],a[56:59]
v_mul_f32 v96,s61,v96
v_mul_f32 v97,s61,v97
v_mfma_f32_16x16x32_bf16 a[88:91],a[160:163],a[208:211],a[88:91]
v_accvgpr_read_b32 v98,a66
v_accvgpr_read_b32 v99,a67
v_mfma_f32_16x16x32_bf16 a[88:91],a[164:167],a[212:215],a[88:91]
v_cvt_pk_bf16_f32 v96,v96,v97
v_mul_f32 v98,s61,v98
v_mfma_f32_16x16x32_bf16 a[88:91],a[168:171],a[240:243],a[88:91]
v_mul_f32 v99,s61,v99
v_cvt_pk_bf16_f32 v97,v98,v99
v_mfma_f32_16x16x32_bf16 a[88:91],a[172:175],a[244:247],a[88:91]
v_accvgpr_read_b32 v104,a80
v_accvgpr_read_b32 v105,a81
v_accvgpr_read_b32 v106,a82
v_accvgpr_read_b32 v107,a83
v_accvgpr_read_b32 v12,a84
v_accvgpr_read_b32 v13,a85
v_accvgpr_read_b32 v14,a86
v_accvgpr_read_b32 v15,a87
v_pk_mul_f32 v[104:105],s[60:61],v[104:105] op_sel:[1,0] op_sel_hi:[1,1]
v_pk_mul_f32 v[106:107],s[60:61],v[106:107] op_sel:[1,0] op_sel_hi:[1,1]
v_pk_mul_f32 v[12:13],s[60:61],v[12:13] op_sel:[1,0] op_sel_hi:[1,1]
v_pk_mul_f32 v[14:15],s[60:61],v[14:15] op_sel:[1,0] op_sel_hi:[1,1]
v_cvt_pk_bf16_f32 v104,v104,v105
v_cvt_pk_bf16_f32 v105,v106,v107
v_cvt_pk_bf16_f32 v106,v12,v13
v_cvt_pk_bf16_f32 v107,v14,v15
v_mfma_f32_16x16x32_bf16 a[120:123],a[172:175],a[252:255],a[120:123]
ds_read_b64_tr_b16 a[172:173],v22 offset:50696
v_accvgpr_read_b32 v84,a40
v_mfma_f32_16x16x32_bf16 a[120:123],a[168:171],a[248:251],a[120:123]
ds_read_b64_tr_b16 a[168:169],v22 offset:34312
v_accvgpr_read_b32 v85,a41
v_mfma_f32_16x16x32_bf16 a[120:123],a[164:167],a[220:223],a[120:123]
ds_read_b64_tr_b16 a[164:165],v22 offset:50688
v_accvgpr_read_b32 v98,a68
v_mfma_f32_16x16x32_bf16 a[120:123],a[160:163],a[216:219],a[120:123]
ds_read_b64_tr_b16 a[160:161],v22 offset:34304
v_accvgpr_read_b32 v99,a69
v_mfma_f32_16x16x32_bf16 a[28:31],a[176:179],a[192:195],a[28:31]
ds_read_b64_tr_b16 a[162:163],v22 offset:36352
v_mul_f32 v84,s61,v84
v_mfma_f32_16x16x32_bf16 a[28:31],a[180:183],a[196:199],a[28:31]
ds_read_b64_tr_b16 a[166:167],v22 offset:52736
v_mul_f32 v85,s61,v85
v_mfma_f32_16x16x32_bf16 a[28:31],a[184:187],a[224:227],a[28:31]
ds_read_b64_tr_b16 a[170:171],v22 offset:36360
v_accvgpr_read_b32 v86,a42
v_mfma_f32_16x16x32_bf16 a[28:31],a[188:191],a[228:231],a[28:31]
ds_read_b64_tr_b16 a[174:175],v22 offset:52744
v_accvgpr_read_b32 v87,a43
v_mfma_f32_16x16x32_bf16 a[60:63],a[188:191],a[236:239],a[60:63]
v_accvgpr_read_b32 v120,a112
v_accvgpr_read_b32 v121,a113
v_accvgpr_read_b32 v122,a114
v_accvgpr_read_b32 v123,a115
v_accvgpr_read_b32 v12,a116
v_accvgpr_read_b32 v13,a117
v_accvgpr_read_b32 v14,a118
v_accvgpr_read_b32 v15,a119
v_pk_mul_f32 v[120:121],s[60:61],v[120:121] op_sel:[1,0] op_sel_hi:[1,1]
v_pk_mul_f32 v[122:123],s[60:61],v[122:123] op_sel:[1,0] op_sel_hi:[1,1]
v_pk_mul_f32 v[12:13],s[60:61],v[12:13] op_sel:[1,0] op_sel_hi:[1,1]
v_pk_mul_f32 v[14:15],s[60:61],v[14:15] op_sel:[1,0] op_sel_hi:[1,1]
v_cvt_pk_bf16_f32 v120,v120,v121
v_cvt_pk_bf16_f32 v121,v122,v123
v_cvt_pk_bf16_f32 v122,v12,v13
v_cvt_pk_bf16_f32 v123,v14,v15
v_mfma_f32_16x16x32_bf16 a[60:63],a[184:187],a[232:235],a[60:63]
v_accvgpr_read_b32 v16,a70
v_accvgpr_read_b32 v17,a71
v_mfma_f32_16x16x32_bf16 a[60:63],a[180:183],a[204:207],a[60:63]
buffer_load_dwordx4 v5,s[20:23],s47 offen offset:3072 nt lds
v_mfma_f32_16x16x32_bf16 a[60:63],a[176:179],a[200:203],a[60:63]
v_mul_f32 v98,s61,v98
v_mul_f32 v99,s61,v99
v_mfma_f32_16x16x32_bf16 a[92:95],a[176:179],a[208:211],a[92:95]
v_cvt_pk_bf16_f32 v84,v84,v85
v_mul_f32 v86,s61,v86
v_mfma_f32_16x16x32_bf16 a[92:95],a[180:183],a[212:215],a[92:95]
v_mul_f32 v87,s61,v87
v_mul_f32 v16,s61,v16
v_mfma_f32_16x16x32_bf16 a[92:95],a[184:187],a[240:243],a[92:95]
v_mul_f32 v17,s61,v17
v_cvt_pk_bf16_f32 v98,v98,v99
v_mfma_f32_16x16x32_bf16 a[92:95],a[188:191],a[244:247],a[92:95]
v_cvt_pk_bf16_f32 v85,v86,v87
v_cvt_pk_bf16_f32 v99,v16,v17
v_mfma_f32_16x16x32_bf16 a[124:127],a[188:191],a[252:255],a[124:127]
ds_read_b64_tr_b16 a[188:189],v22 offset:50952
v_accvgpr_read_b32 v86,a44
v_mfma_f32_16x16x32_bf16 a[124:127],a[184:187],a[248:251],a[124:127]
ds_read_b64_tr_b16 a[184:185],v22 offset:34568
v_accvgpr_read_b32 v87,a45
v_mfma_f32_16x16x32_bf16 a[124:127],a[180:183],a[220:223],a[124:127]
ds_read_b64_tr_b16 a[180:181],v22 offset:50944
v_accvgpr_read_b32 v16,a46
v_mfma_f32_16x16x32_bf16 a[124:127],a[176:179],a[216:219],a[124:127]
s_waitcnt lgkmcnt(3)
ds_read_b64_tr_b16 a[176:177],v22 offset:34560
v_accvgpr_read_b32 v17,a47
v_mfma_f32_16x16x32_bf16 v[128:131],a[128:131],a[224:227],v[128:131]
ds_read_b64_tr_b16 a[178:179],v22 offset:36608
v_mul_f32 v86,s61,v86
v_mfma_f32_16x16x32_bf16 v[128:131],a[132:135],a[228:231],v[128:131]
ds_read_b64_tr_b16 a[182:183],v22 offset:52992
v_mul_f32 v87,s61,v87
v_mfma_f32_16x16x32_bf16 v[136:139],a[132:135],a[236:239],v[136:139]
ds_read_b64_tr_b16 a[186:187],v22 offset:36616
v_mul_f32 v16,s61,v16
v_mfma_f32_16x16x32_bf16 v[136:139],a[128:131],a[232:235],v[136:139]
s_add_u32 m0,0,s52
ds_read_b64_tr_b16 a[190:191],v22 offset:53000
v_mul_f32 v17,s61,v17
v_mfma_f32_16x16x32_bf16 v[144:147],a[128:131],a[240:243],v[144:147]
v_accvgpr_read_b32 v76,a24
v_accvgpr_read_b32 v77,a25
v_accvgpr_read_b32 v78,a26
v_accvgpr_read_b32 v79,a27
v_accvgpr_read_b32 v12,a28
v_accvgpr_read_b32 v13,a29
v_accvgpr_read_b32 v14,a30
v_accvgpr_read_b32 v15,a31
v_pk_mul_f32 v[76:77],s[60:61],v[76:77] op_sel:[1,0] op_sel_hi:[1,1]
v_pk_mul_f32 v[78:79],s[60:61],v[78:79] op_sel:[1,0] op_sel_hi:[1,1]
v_pk_mul_f32 v[12:13],s[60:61],v[12:13] op_sel:[1,0] op_sel_hi:[1,1]
v_pk_mul_f32 v[14:15],s[60:61],v[14:15] op_sel:[1,0] op_sel_hi:[1,1]
v_cvt_pk_bf16_f32 v76,v76,v77
v_cvt_pk_bf16_f32 v77,v78,v79
v_cvt_pk_bf16_f32 v78,v12,v13
v_cvt_pk_bf16_f32 v79,v14,v15
v_mfma_f32_16x16x32_bf16 v[144:147],a[132:135],a[244:247],v[144:147]
v_cvt_pk_bf16_f32 v86,v86,v87
v_cvt_pk_bf16_f32 v87,v16,v17
v_mfma_f32_16x16x32_bf16 v[152:155],a[132:135],a[252:255],v[152:155]
buffer_load_dwordx4 v4,s[24:27],s47 offen nt lds
v_mfma_f32_16x16x32_bf16 v[152:155],a[128:131],a[248:251],v[152:155]
v_accvgpr_read_b32 v92,a56
v_accvgpr_read_b32 v93,a57
v_accvgpr_read_b32 v94,a58
v_accvgpr_read_b32 v95,a59
v_accvgpr_read_b32 v12,a60
v_accvgpr_read_b32 v13,a61
v_accvgpr_read_b32 v14,a62
v_accvgpr_read_b32 v15,a63
v_pk_mul_f32 v[92:93],s[60:61],v[92:93] op_sel:[1,0] op_sel_hi:[1,1]
v_pk_mul_f32 v[94:95],s[60:61],v[94:95] op_sel:[1,0] op_sel_hi:[1,1]
v_pk_mul_f32 v[12:13],s[60:61],v[12:13] op_sel:[1,0] op_sel_hi:[1,1]
v_pk_mul_f32 v[14:15],s[60:61],v[14:15] op_sel:[1,0] op_sel_hi:[1,1]
v_cvt_pk_bf16_f32 v92,v92,v93
v_cvt_pk_bf16_f32 v93,v94,v95
v_cvt_pk_bf16_f32 v94,v12,v13
v_cvt_pk_bf16_f32 v95,v14,v15
v_mfma_f32_16x16x32_bf16 v[132:135],a[136:139],a[224:227],v[132:135]
v_accvgpr_read_b32 v108,a88
v_accvgpr_read_b32 v109,a89
v_accvgpr_read_b32 v110,a90
v_accvgpr_read_b32 v111,a91
v_accvgpr_read_b32 v12,a92
v_accvgpr_read_b32 v13,a93
v_accvgpr_read_b32 v14,a94
v_accvgpr_read_b32 v15,a95
v_pk_mul_f32 v[108:109],s[60:61],v[108:109] op_sel:[1,0] op_sel_hi:[1,1]
v_pk_mul_f32 v[110:111],s[60:61],v[110:111] op_sel:[1,0] op_sel_hi:[1,1]
v_pk_mul_f32 v[12:13],s[60:61],v[12:13] op_sel:[1,0] op_sel_hi:[1,1]
v_pk_mul_f32 v[14:15],s[60:61],v[14:15] op_sel:[1,0] op_sel_hi:[1,1]
v_cvt_pk_bf16_f32 v108,v108,v109
v_cvt_pk_bf16_f32 v109,v110,v111
v_cvt_pk_bf16_f32 v110,v12,v13
v_cvt_pk_bf16_f32 v111,v14,v15
v_mfma_f32_16x16x32_bf16 v[132:135],a[140:143],a[228:231],v[132:135]
v_accvgpr_read_b32 v124,a120
v_accvgpr_read_b32 v125,a121
v_accvgpr_read_b32 v126,a122
v_accvgpr_read_b32 v127,a123
v_accvgpr_read_b32 v12,a124
v_accvgpr_read_b32 v13,a125
v_accvgpr_read_b32 v14,a126
v_accvgpr_read_b32 v15,a127
v_pk_mul_f32 v[124:125],s[60:61],v[124:125] op_sel:[1,0] op_sel_hi:[1,1]
v_pk_mul_f32 v[126:127],s[60:61],v[126:127] op_sel:[1,0] op_sel_hi:[1,1]
v_pk_mul_f32 v[12:13],s[60:61],v[12:13] op_sel:[1,0] op_sel_hi:[1,1]
v_pk_mul_f32 v[14:15],s[60:61],v[14:15] op_sel:[1,0] op_sel_hi:[1,1]
v_cvt_pk_bf16_f32 v124,v124,v125
v_cvt_pk_bf16_f32 v125,v126,v127
v_cvt_pk_bf16_f32 v126,v12,v13
v_cvt_pk_bf16_f32 v127,v14,v15
v_mfma_f32_16x16x32_bf16 v[140:143],a[140:143],a[236:239],v[140:143]
v_mfma_f32_16x16x32_bf16 v[140:143],a[136:139],a[232:235],v[140:143]
v_mfma_f32_16x16x32_bf16 v[148:151],a[136:139],a[240:243],v[148:151]
v_mfma_f32_16x16x32_bf16 v[148:151],a[140:143],a[244:247],v[148:151]
v_mfma_f32_16x16x32_bf16 v[156:159],a[140:143],a[252:255],v[156:159]
s_waitcnt vmcnt(5) lgkmcnt(0)
v_mfma_f32_16x16x32_bf16 v[156:159],a[136:139],a[248:251],v[156:159]
s_barrier
ds_read_b128 v[48:51],v18
v_mfma_f32_16x16x32_bf16 v[160:163],a[144:147],a[224:227],v[160:163]
ds_read_b64_tr_b16 a[192:193],v2
v_mfma_f32_16x16x32_bf16 v[160:163],a[148:151],a[228:231],v[160:163]
ds_read_b64_tr_b16 a[194:195],v2 offset:4096
v_mfma_f32_16x16x32_bf16 v[168:171],a[148:151],a[236:239],v[168:171]
ds_read_b64_tr_b16 a[196:197],v2 offset:8192
v_mfma_f32_16x16x32_bf16 v[168:171],a[144:147],a[232:235],v[168:171]
ds_read_b128 v[52:55],v18 offset:256
v_mfma_f32_16x16x32_bf16 v[176:179],a[144:147],a[240:243],v[176:179]
v_mul_f32 v128,s61,v128
v_mul_f32 v129,s61,v129
v_mul_f32 v130,s61,v130
v_mul_f32 v131,s61,v131
v_cvt_pk_bf16_f32 v128,v128,v129
v_cvt_pk_bf16_f32 v129,v130,v131
v_pk_mul_f32 v[132:133],s[60:61],v[132:133] op_sel:[1,0] op_sel_hi:[1,1]
v_pk_mul_f32 v[134:135],s[60:61],v[134:135] op_sel:[1,0] op_sel_hi:[1,1]
v_pk_mul_f32 v[136:137],s[60:61],v[136:137] op_sel:[1,0] op_sel_hi:[1,1]
v_pk_mul_f32 v[138:139],s[60:61],v[138:139] op_sel:[1,0] op_sel_hi:[1,1]
v_pk_mul_f32 v[140:141],s[60:61],v[140:141] op_sel:[1,0] op_sel_hi:[1,1]
v_pk_mul_f32 v[142:143],s[60:61],v[142:143] op_sel:[1,0] op_sel_hi:[1,1]
v_pk_mul_f32 v[144:145],s[60:61],v[144:145] op_sel:[1,0] op_sel_hi:[1,1]
v_pk_mul_f32 v[146:147],s[60:61],v[146:147] op_sel:[1,0] op_sel_hi:[1,1]
v_pk_mul_f32 v[148:149],s[60:61],v[148:149] op_sel:[1,0] op_sel_hi:[1,1]
)A"
R"A(v_pk_mul_f32 v[150:151],s[60:61],v[150:151] op_sel:[1,0] op_sel_hi:[1,1]
v_pk_mul_f32 v[152:153],s[60:61],v[152:153] op_sel:[1,0] op_sel_hi:[1,1]
v_pk_mul_f32 v[154:155],s[60:61],v[154:155] op_sel:[1,0] op_sel_hi:[1,1]
v_pk_mul_f32 v[156:157],s[60:61],v[156:157] op_sel:[1,0] op_sel_hi:[1,1]
v_pk_mul_f32 v[158:159],s[60:61],v[158:159] op_sel:[1,0] op_sel_hi:[1,1]
v_mfma_f32_16x16x32_bf16 v[176:179],a[148:151],a[244:247],v[176:179]
buffer_load_dwordx4 v5,s[24:27],s47 offen offset:1024 nt lds
v_mfma_f32_16x16x32_bf16 v[184:187],a[148:151],a[252:255],v[184:187]
ds_read_b128 v[56:59],v18 offset:512
v_cvt_pk_bf16_f32 v130,v132,v133
v_mfma_f32_16x16x32_bf16 v[184:187],a[144:147],a[248:251],v[184:187]
v_cvt_pk_bf16_f32 v132,v136,v137
v_cvt_pk_bf16_f32 v133,v138,v139
v_mfma_f32_16x16x32_bf16 v[164:167],a[152:155],a[224:227],v[164:167]
ds_read_b128 v[60:63],v18 offset:768
v_cvt_pk_bf16_f32 v131,v134,v135
v_mfma_f32_16x16x32_bf16 v[164:167],a[156:159],a[228:231],v[164:167]
v_cvt_pk_bf16_f32 v134,v140,v141
v_cvt_pk_bf16_f32 v135,v142,v143
v_mfma_f32_16x16x32_bf16 v[172:175],a[156:159],a[236:239],v[172:175]
ds_read_b128 a[128:131],v1
v_cvt_pk_bf16_f32 v144,v144,v145
v_mfma_f32_16x16x32_bf16 v[172:175],a[152:155],a[232:235],v[172:175]
v_cvt_pk_bf16_f32 v145,v146,v147
v_cvt_pk_bf16_f32 v146,v148,v149
v_mfma_f32_16x16x32_bf16 v[180:183],a[152:155],a[240:243],v[180:183]
ds_read_b128 a[132:135],v1 offset:256
v_cvt_pk_bf16_f32 v148,v152,v153
v_mfma_f32_16x16x32_bf16 v[180:183],a[156:159],a[244:247],v[180:183]
v_cvt_pk_bf16_f32 v149,v154,v155
v_cvt_pk_bf16_f32 v147,v150,v151
v_mfma_f32_16x16x32_bf16 v[188:191],a[156:159],a[252:255],v[188:191]
ds_read_b128 a[136:139],v1 offset:512
v_cvt_pk_bf16_f32 v150,v156,v157
v_mfma_f32_16x16x32_bf16 v[188:191],a[152:155],a[248:251],v[188:191]
ds_read_b64_tr_b16 a[198:199],v2 offset:12288
v_cvt_pk_bf16_f32 v151,v158,v159
v_mfma_f32_16x16x32_bf16 v[192:195],a[160:163],a[224:227],v[192:195]
ds_read_b64_tr_b16 a[204:205],v3 offset:8192
v_mfma_f32_16x16x32_bf16 v[192:195],a[164:167],a[228:231],v[192:195]
ds_read_b64_tr_b16 a[206:207],v3 offset:12288
v_mfma_f32_16x16x32_bf16 v[200:203],a[164:167],a[236:239],v[200:203]
ds_read_b128 a[140:143],v1 offset:768
v_mfma_f32_16x16x32_bf16 v[200:203],a[160:163],a[232:235],v[200:203]
ds_read_b64_tr_b16 a[200:201],v3
v_mfma_f32_16x16x32_bf16 v[208:211],a[160:163],a[240:243],v[208:211]
ds_read_b64_tr_b16 a[202:203],v3 offset:4096
v_mfma_f32_16x16x32_bf16 v[208:211],a[164:167],a[244:247],v[208:211]
buffer_load_dwordx4 v4,s[24:27],s47 offen offset:2048 nt lds
v_mfma_f32_16x16x32_bf16 v[216:219],a[164:167],a[252:255],v[216:219]
ds_read_b128 a[156:159],v1 offset:4864
v_mfma_f32_16x16x32_bf16 v[216:219],a[160:163],a[248:251],v[216:219]
v_mul_f32 v160,s61,v160
v_mul_f32 v161,s61,v161
v_mul_f32 v162,s61,v162
v_mul_f32 v163,s61,v163
v_cvt_pk_bf16_f32 v160,v160,v161
v_cvt_pk_bf16_f32 v161,v162,v163
v_pk_mul_f32 v[164:165],s[60:61],v[164:165] op_sel:[1,0] op_sel_hi:[1,1]
v_pk_mul_f32 v[166:167],s[60:61],v[166:167] op_sel:[1,0] op_sel_hi:[1,1]
v_pk_mul_f32 v[168:169],s[60:61],v[168:169] op_sel:[1,0] op_sel_hi:[1,1]
v_pk_mul_f32 v[170:171],s[60:61],v[170:171] op_sel:[1,0] op_sel_hi:[1,1]
v_pk_mul_f32 v[172:173],s[60:61],v[172:173] op_sel:[1,0] op_sel_hi:[1,1]
v_pk_mul_f32 v[174:175],s[60:61],v[174:175] op_sel:[1,0] op_sel_hi:[1,1]
v_pk_mul_f32 v[176:177],s[60:61],v[176:177] op_sel:[1,0] op_sel_hi:[1,1]
v_pk_mul_f32 v[178:179],s[60:61],v[178:179] op_sel:[1,0] op_sel_hi:[1,1]
v_pk_mul_f32 v[180:181],s[60:61],v[180:181] op_sel:[1,0] op_sel_hi:[1,1]
v_pk_mul_f32 v[182:183],s[60:61],v[182:183] op_sel:[1,0] op_sel_hi:[1,1]
v_pk_mul_f32 v[184:185],s[60:61],v[184:185] op_sel:[1,0] op_sel_hi:[1,1]
v_pk_mul_f32 v[186:187],s[60:61],v[186:187] op_sel:[1,0] op_sel_hi:[1,1]
v_pk_mul_f32 v[188:189],s[60:61],v[188:189] op_sel:[1,0] op_sel_hi:[1,1]
v_pk_mul_f32 v[190:191],s[60:61],v[190:191] op_sel:[1,0] op_sel_hi:[1,1]
v_mfma_f32_16x16x32_bf16 v[196:199],a[168:171],a[224:227],v[196:199]
ds_read_b128 a[152:155],v1 offset:4608
v_cvt_pk_bf16_f32 v162,v164,v165
v_mfma_f32_16x16x32_bf16 v[196:199],a[172:175],a[228:231],v[196:199]
v_cvt_pk_bf16_f32 v164,v168,v169
v_cvt_pk_bf16_f32 v165,v170,v171
v_mfma_f32_16x16x32_bf16 v[204:207],a[172:175],a[236:239],v[204:207]
ds_read_b128 a[148:151],v1 offset:4352
v_cvt_pk_bf16_f32 v163,v166,v167
v_mfma_f32_16x16x32_bf16 v[204:207],a[168:171],a[232:235],v[204:207]
v_cvt_pk_bf16_f32 v166,v172,v173
v_cvt_pk_bf16_f32 v167,v174,v175
v_mfma_f32_16x16x32_bf16 v[212:215],a[168:171],a[240:243],v[212:215]
ds_read_b128 a[144:147],v1 offset:4096
v_cvt_pk_bf16_f32 v176,v176,v177
v_mfma_f32_16x16x32_bf16 v[212:215],a[172:175],a[244:247],v[212:215]
v_cvt_pk_bf16_f32 v177,v178,v179
v_cvt_pk_bf16_f32 v178,v180,v181
v_mfma_f32_16x16x32_bf16 v[220:223],a[172:175],a[252:255],v[220:223]
ds_read_b128 a[160:163],v1 offset:8192
v_cvt_pk_bf16_f32 v180,v184,v185
v_mfma_f32_16x16x32_bf16 v[220:223],a[168:171],a[248:251],v[220:223]
v_cvt_pk_bf16_f32 v181,v186,v187
v_cvt_pk_bf16_f32 v179,v182,v183
v_mfma_f32_16x16x32_bf16 v[224:227],a[176:179],a[224:227],v[224:227]
ds_read_b128 a[164:167],v1 offset:8448
v_cvt_pk_bf16_f32 v182,v188,v189
v_mfma_f32_16x16x32_bf16 v[224:227],a[180:183],a[228:231],v[224:227]
ds_read_b64_tr_b16 a[208:209],v2 offset:256
v_cvt_pk_bf16_f32 v183,v190,v191
v_mfma_f32_16x16x32_bf16 v[232:235],a[180:183],a[236:239],v[232:235]
ds_read_b64_tr_b16 a[210:211],v2 offset:4352
v_mfma_f32_16x16x32_bf16 v[232:235],a[176:179],a[232:235],v[232:235]
ds_read_b64_tr_b16 a[212:213],v2 offset:8448
v_mfma_f32_16x16x32_bf16 v[240:243],a[176:179],a[240:243],v[240:243]
ds_read_b128 a[168:171],v1 offset:8704
v_mfma_f32_16x16x32_bf16 v[240:243],a[180:183],a[244:247],v[240:243]
buffer_load_dwordx4 v5,s[24:27],s47 offen offset:3072 nt lds
v_mfma_f32_16x16x32_bf16 v[248:251],a[180:183],a[252:255],v[248:251]
s_add_u32 s47,s47,16384
s_and_b32 s47,s47,0x3ffff
s_or_b32 s47,s47,s46
v_mfma_f32_16x16x32_bf16 v[248:251],a[176:179],a[248:251],v[248:251]
s_and_b32 s46,s54,0xfffc0000
s_add_u32 s54,s54,16384
ds_read_b128 a[172:175],v1 offset:8960
v_mfma_f32_16x16x32_bf16 v[228:231],a[184:187],a[224:227],v[228:231]
v_mul_f32 v192,s61,v192
v_mul_f32 v193,s61,v193
v_mul_f32 v194,s61,v194
v_mul_f32 v195,s61,v195
v_cvt_pk_bf16_f32 v192,v192,v193
v_cvt_pk_bf16_f32 v193,v194,v195
v_pk_mul_f32 v[196:197],s[60:61],v[196:197] op_sel:[1,0] op_sel_hi:[1,1]
v_pk_mul_f32 v[198:199],s[60:61],v[198:199] op_sel:[1,0] op_sel_hi:[1,1]
v_pk_mul_f32 v[200:201],s[60:61],v[200:201] op_sel:[1,0] op_sel_hi:[1,1]
v_pk_mul_f32 v[202:203],s[60:61],v[202:203] op_sel:[1,0] op_sel_hi:[1,1]
v_pk_mul_f32 v[204:205],s[60:61],v[204:205] op_sel:[1,0] op_sel_hi:[1,1]
v_pk_mul_f32 v[206:207],s[60:61],v[206:207] op_sel:[1,0] op_sel_hi:[1,1]
v_pk_mul_f32 v[208:209],s[60:61],v[208:209] op_sel:[1,0] op_sel_hi:[1,1]
v_pk_mul_f32 v[210:211],s[60:61],v[210:211] op_sel:[1,0] op_sel_hi:[1,1]
v_pk_mul_f32 v[212:213],s[60:61],v[212:213] op_sel:[1,0] op_sel_hi:[1,1]
v_pk_mul_f32 v[214:215],s[60:61],v[214:215] op_sel:[1,0] op_sel_hi:[1,1]
v_pk_mul_f32 v[216:217],s[60:61],v[216:217] op_sel:[1,0] op_sel_hi:[1,1]
v_pk_mul_f32 v[218:219],s[60:61],v[218:219] op_sel:[1,0] op_sel_hi:[1,1]
v_pk_mul_f32 v[220:221],s[60:61],v[220:221] op_sel:[1,0] op_sel_hi:[1,1]
v_pk_mul_f32 v[222:223],s[60:61],v[222:223] op_sel:[1,0] op_sel_hi:[1,1]
v_mfma_f32_16x16x32_bf16 v[228:231],a[188:191],a[228:231],v[228:231]
ds_read_b128 a[180:183],v1 offset:12544
v_cvt_pk_bf16_f32 v194,v196,v197
v_mfma_f32_16x16x32_bf16 v[236:239],a[188:191],a[236:239],v[236:239]
v_cvt_pk_bf16_f32 v196,v200,v201
v_cvt_pk_bf16_f32 v197,v202,v203
v_mfma_f32_16x16x32_bf16 v[236:239],a[184:187],a[232:235],v[236:239]
ds_read_b128 a[176:179],v1 offset:12288
v_cvt_pk_bf16_f32 v195,v198,v199
v_mfma_f32_16x16x32_bf16 v[244:247],a[184:187],a[240:243],v[244:247]
v_cvt_pk_bf16_f32 v198,v204,v205
v_cvt_pk_bf16_f32 v199,v206,v207
v_mfma_f32_16x16x32_bf16 v[244:247],a[188:191],a[244:247],v[244:247]
v_cvt_pk_bf16_f32 v208,v208,v209
v_cvt_pk_bf16_f32 v209,v210,v211
v_mfma_f32_16x16x32_bf16 v[252:255],a[188:191],a[252:255],v[252:255]
ds_read_b128 a[188:191],v1 offset:13056
v_cvt_pk_bf16_f32 v210,v212,v213
v_mfma_f32_16x16x32_bf16 v[252:255],a[184:187],a[248:251],v[252:255]
s_waitcnt lgkmcnt(3)
v_cvt_pk_bf16_f32 v212,v216,v217
v_cvt_pk_bf16_f32 v213,v218,v219
v_mfma_f32_16x16x32_bf16 v[32:35],v[48:51],v[64:67],0
ds_read_b128 a[184:187],v1 offset:12800
v_cvt_pk_bf16_f32 v211,v214,v215
v_mfma_f32_16x16x32_bf16 v[32:35],v[52:55],v[68:71],v[32:35]
v_cvt_pk_bf16_f32 v214,v220,v221
v_cvt_pk_bf16_f32 v215,v222,v223
v_mfma_f32_16x16x32_bf16 v[32:35],v[56:59],v[72:75],v[32:35]
ds_read_b64_tr_b16 a[214:215],v2 offset:12544
ds_read_b64_tr_b16 a[220:221],v3 offset:8448
v_mfma_f32_16x16x32_bf16 v[32:35],v[60:63],v[76:79],v[32:35]
ds_read_b64_tr_b16 a[222:223],v3 offset:12544
v_add_u32 v1,s48,v1
v_mfma_f32_16x16x32_bf16 v[36:39],v[48:51],v[80:83],0
ds_read_b64_tr_b16 a[216:217],v3 offset:256
v_mfma_f32_16x16x32_bf16 v[36:39],v[52:55],v[84:87],v[36:39]
ds_read_b64_tr_b16 a[218:219],v3 offset:4352
v_mfma_f32_16x16x32_bf16 v[36:39],v[56:59],v[88:91],v[36:39]
ds_read_b64_tr_b16 a[224:225],v2 offset:512
ds_read_b64_tr_b16 a[226:227],v2 offset:4608
v_mfma_f32_16x16x32_bf16 v[36:39],v[60:63],v[92:95],v[36:39]
ds_read_b64_tr_b16 a[228:229],v2 offset:8704
v_mfma_f32_16x16x32_bf16 v[40:43],v[48:51],v[96:99],0
ds_read_b64_tr_b16 a[230:231],v2 offset:12800
v_mfma_f32_16x16x32_bf16 v[40:43],v[52:55],v[100:103],v[40:43]
s_add_u32 m0,32768,s51
v_max3_f32 v32,v32,v33,v34
v_max3_f32 v32,v32,v35,s18
v_mfma_f32_16x16x32_bf16 v[40:43],v[56:59],v[104:107],v[40:43]
v_sub_f32 v35,0,v32
v_sub_f32 v34,0,v32
v_mfma_f32_16x16x32_bf16 v[40:43],v[60:63],v[108:111],v[40:43]
v_sub_f32 v33,0,v32
v_sub_f32 v32,0,v32
v_mfma_f32_16x16x32_bf16 v[44:47],v[48:51],v[112:115],0
buffer_load_dwordx4 v4,s[20:23],s47 offen nt lds
v_mfma_f32_16x16x32_bf16 v[44:47],v[52:55],v[116:119],v[44:47]
v_max3_f32 v36,v36,v37,v38
v_max3_f32 v36,v36,v39,s19
v_mfma_f32_16x16x32_bf16 v[44:47],v[56:59],v[120:123],v[44:47]
v_sub_f32 v39,0,v36
v_sub_f32 v38,0,v36
v_mfma_f32_16x16x32_bf16 v[44:47],v[60:63],v[124:127],v[44:47]
v_sub_f32 v37,0,v36
v_sub_f32 v36,0,v36
v_mfma_f32_16x16x32_bf16 v[136:139],a[128:131],v[64:67],v[32:35]
ds_read_b64_tr_b16 a[236:237],v3 offset:8704
ds_read_b64_tr_b16 a[238:239],v3 offset:12800
v_mfma_f32_16x16x32_bf16 v[136:139],a[132:135],v[68:71],v[136:139]
v_max3_f32 v40,v40,v41,v42
v_max3_f32 v40,v40,v43,s100
v_mfma_f32_16x16x32_bf16 v[136:139],a[136:139],v[72:75],v[136:139]
v_sub_f32 v43,0,v40
v_sub_f32 v42,0,v40
v_mfma_f32_16x16x32_bf16 v[136:139],a[140:143],v[76:79],v[136:139]
v_sub_f32 v41,0,v40
v_sub_f32 v40,0,v40
v_mfma_f32_16x16x32_bf16 v[140:143],a[156:159],v[76:79],v[32:35]
ds_read_b64_tr_b16 a[232:233],v3 offset:512
ds_read_b64_tr_b16 a[234:235],v3 offset:4608
v_mfma_f32_16x16x32_bf16 v[140:143],a[152:155],v[72:75],v[140:143]
buffer_load_dwordx4 v4,s[20:23],s47 offen offset:1024 nt lds
v_mfma_f32_16x16x32_bf16 v[140:143],a[148:151],v[68:71],v[140:143]
ds_read_b64_tr_b16 a[240:241],v2 offset:768
ds_read_b64_tr_b16 a[242:243],v2 offset:4864
v_mfma_f32_16x16x32_bf16 v[140:143],a[144:147],v[64:67],v[140:143]
ds_read_b64_tr_b16 a[244:245],v2 offset:8960
v_max3_f32 v44,v44,v45,v46
v_mfma_f32_16x16x32_bf16 v[152:155],a[160:163],v[64:67],v[32:35]
ds_read_b64_tr_b16 a[246:247],v2 offset:13056
v_max3_f32 v44,v44,v47,s101
v_mfma_f32_16x16x32_bf16 v[152:155],a[164:167],v[68:71],v[152:155]
v_exp_f32 v136,v136
v_mfma_f32_16x16x32_bf16 v[152:155],a[168:171],v[72:75],v[152:155]
v_exp_f32 v137,v137
v_mfma_f32_16x16x32_bf16 v[152:155],a[172:175],v[76:79],v[152:155]
s_waitcnt lgkmcnt(6)
v_exp_f32 v138,v138
v_mfma_f32_16x16x32_bf16 v[156:159],a[188:191],v[76:79],v[32:35]
v_exp_f32 v139,v139
v_mfma_f32_16x16x32_bf16 v[156:159],a[184:187],v[72:75],v[156:159]
v_cvt_pk_bf16_f32 v136,v136,v137
v_cvt_pk_bf16_f32 v137,v138,v139
v_mfma_f32_16x16x32_bf16 v[156:159],a[180:183],v[68:71],v[156:159]
buffer_load_dwordx4 v5,s[20:23],s47 offen offset:2048 nt lds
)A"
R"A(v_mfma_f32_16x16x32_bf16 v[156:159],a[176:179],v[64:67],v[156:159]
v_exp_f32 v140,v140
v_mfma_f32_16x16x32_bf16 v[168:171],a[128:131],v[80:83],v[36:39]
v_exp_f32 v141,v141
v_mfma_f32_16x16x32_bf16 v[168:171],a[132:135],v[84:87],v[168:171]
v_exp_f32 v142,v142
v_mfma_f32_16x16x32_bf16 v[168:171],a[136:139],v[88:91],v[168:171]
v_exp_f32 v143,v143
v_mfma_f32_16x16x32_bf16 v[168:171],a[140:143],v[92:95],v[168:171]
v_cvt_pk_bf16_f32 v138,v140,v141
v_cvt_pk_bf16_f32 v139,v142,v143
v_mfma_f32_16x16x32_bf16 v[172:175],a[156:159],v[92:95],v[36:39]
v_exp_f32 v152,v152
v_mfma_f32_16x16x32_bf16 v[172:175],a[152:155],v[88:91],v[172:175]
v_exp_f32 v153,v153
v_mfma_f32_16x16x32_bf16 v[172:175],a[148:151],v[84:87],v[172:175]
v_exp_f32 v154,v154
v_mfma_f32_16x16x32_bf16 v[172:175],a[144:147],v[80:83],v[172:175]
buffer_load_dwordx4 v5,s[20:23],s47 offen offset:3072 nt lds
v_mfma_f32_16x16x32_bf16 v[184:187],a[160:163],v[80:83],v[36:39]
v_exp_f32 v155,v155
v_mfma_f32_16x16x32_bf16 v[184:187],a[164:167],v[84:87],v[184:187]
v_exp_f32 v156,v156
v_mfma_f32_16x16x32_bf16 v[184:187],a[168:171],v[88:91],v[184:187]
v_exp_f32 v157,v157
v_mfma_f32_16x16x32_bf16 v[184:187],a[172:175],v[92:95],v[184:187]
v_exp_f32 v158,v158
v_mfma_f32_16x16x32_bf16 v[188:191],a[188:191],v[92:95],v[36:39]
v_exp_f32 v159,v159
v_mfma_f32_16x16x32_bf16 v[188:191],a[184:187],v[88:91],v[188:191]
s_add_u32 m0,32768,s52
v_cvt_pk_bf16_f32 v140,v152,v153
v_cvt_pk_bf16_f32 v141,v154,v155
v_mfma_f32_16x16x32_bf16 v[188:191],a[180:183],v[84:87],v[188:191]
v_cvt_pk_bf16_f32 v142,v156,v157
v_cvt_pk_bf16_f32 v143,v158,v159
v_mfma_f32_16x16x32_bf16 v[188:191],a[176:179],v[80:83],v[188:191]
ds_read_b64_tr_b16 a[252:253],v3 offset:8960
ds_read_b64_tr_b16 a[254:255],v3 offset:13056
v_mfma_f32_16x16x32_bf16 v[200:203],a[128:131],v[96:99],v[40:43]
buffer_load_dwordx4 v4,s[24:27],s47 offen nt lds
v_mfma_f32_16x16x32_bf16 v[200:203],a[132:135],v[100:103],v[200:203]
ds_read_b64_tr_b16 a[248:249],v3 offset:768
ds_read_b64_tr_b16 a[250:251],v3 offset:4864
v_mfma_f32_16x16x32_bf16 v[200:203],a[136:139],v[104:107],v[200:203]
v_mov_b32 v48,0
v_mov_b32 v49,0
v_mfma_f32_16x16x32_bf16 v[200:203],a[140:143],v[108:111],v[200:203]
v_mov_b32 v50,0
v_mov_b32 v51,0
v_mfma_f32_16x16x32_bf16 v[204:207],a[156:159],v[108:111],v[40:43]
v_sub_f32 v47,0,v44
v_sub_f32 v46,0,v44
v_mfma_f32_16x16x32_bf16 v[204:207],a[152:155],v[104:107],v[204:207]
v_sub_f32 v45,0,v44
v_sub_f32 v44,0,v44
v_mfma_f32_16x16x32_bf16 v[204:207],a[148:151],v[100:103],v[204:207]
v_mul_f32 v224,s61,v224
v_mul_f32 v225,s61,v225
v_mul_f32 v226,s61,v226
v_mul_f32 v227,s61,v227
v_cvt_pk_bf16_f32 v224,v224,v225
v_cvt_pk_bf16_f32 v225,v226,v227
v_pk_mul_f32 v[228:229],s[60:61],v[228:229] op_sel:[1,0] op_sel_hi:[1,1]
v_pk_mul_f32 v[230:231],s[60:61],v[230:231] op_sel:[1,0] op_sel_hi:[1,1]
v_pk_mul_f32 v[232:233],s[60:61],v[232:233] op_sel:[1,0] op_sel_hi:[1,1]
v_pk_mul_f32 v[234:235],s[60:61],v[234:235] op_sel:[1,0] op_sel_hi:[1,1]
v_pk_mul_f32 v[236:237],s[60:61],v[236:237] op_sel:[1,0] op_sel_hi:[1,1]
v_pk_mul_f32 v[238:239],s[60:61],v[238:239] op_sel:[1,0] op_sel_hi:[1,1]
v_pk_mul_f32 v[240:241],s[60:61],v[240:241] op_sel:[1,0] op_sel_hi:[1,1]
v_pk_mul_f32 v[242:243],s[60:61],v[242:243] op_sel:[1,0] op_sel_hi:[1,1]
v_pk_mul_f32 v[244:245],s[60:61],v[244:245] op_sel:[1,0] op_sel_hi:[1,1]
v_pk_mul_f32 v[246:247],s[60:61],v[246:247] op_sel:[1,0] op_sel_hi:[1,1]
v_pk_mul_f32 v[248:249],s[60:61],v[248:249] op_sel:[1,0] op_sel_hi:[1,1]
v_pk_mul_f32 v[250:251],s[60:61],v[250:251] op_sel:[1,0] op_sel_hi:[1,1]
v_pk_mul_f32 v[252:253],s[60:61],v[252:253] op_sel:[1,0] op_sel_hi:[1,1]
v_pk_mul_f32 v[254:255],s[60:61],v[254:255] op_sel:[1,0] op_sel_hi:[1,1]
v_mfma_f32_16x16x32_bf16 v[204:207],a[144:147],v[96:99],v[204:207]
v_cvt_pk_bf16_f32 v226,v228,v229
v_cvt_pk_bf16_f32 v228,v232,v233
v_mfma_f32_16x16x32_bf16 v[216:219],a[160:163],v[96:99],v[40:43]
v_cvt_pk_bf16_f32 v229,v234,v235
v_cvt_pk_bf16_f32 v227,v230,v231
v_mfma_f32_16x16x32_bf16 v[216:219],a[164:167],v[100:103],v[216:219]
buffer_load_dwordx4 v5,s[24:27],s47 offen offset:1024 nt lds
v_mfma_f32_16x16x32_bf16 v[216:219],a[168:171],v[104:107],v[216:219]
v_cvt_pk_bf16_f32 v230,v236,v237
v_cvt_pk_bf16_f32 v231,v238,v239
v_mfma_f32_16x16x32_bf16 v[216:219],a[172:175],v[108:111],v[216:219]
v_cvt_pk_bf16_f32 v240,v240,v241
v_cvt_pk_bf16_f32 v241,v242,v243
v_mfma_f32_16x16x32_bf16 v[220:223],a[188:191],v[108:111],v[40:43]
v_cvt_pk_bf16_f32 v242,v244,v245
v_cvt_pk_bf16_f32 v244,v248,v249
v_mfma_f32_16x16x32_bf16 v[220:223],a[184:187],v[104:107],v[220:223]
v_cvt_pk_bf16_f32 v245,v250,v251
v_cvt_pk_bf16_f32 v243,v246,v247
v_mfma_f32_16x16x32_bf16 v[220:223],a[180:183],v[100:103],v[220:223]
v_cvt_pk_bf16_f32 v246,v252,v253
v_cvt_pk_bf16_f32 v247,v254,v255
v_mfma_f32_16x16x32_bf16 v[220:223],a[176:179],v[96:99],v[220:223]
v_exp_f32 v168,v168
v_mfma_f32_16x16x32_bf16 a[0:3],a[192:195],v[136:139],0
v_exp_f32 v169,v169
v_mfma_f32_16x16x32_bf16 a[0:3],a[196:199],v[140:143],a[0:3]
v_exp_f32 v170,v170
v_mfma_f32_16x16x32_bf16 a[4:7],a[204:207],v[140:143],0
buffer_load_dwordx4 v4,s[24:27],s47 offen offset:2048 nt lds
v_mfma_f32_16x16x32_bf16 a[4:7],a[200:203],v[136:139],a[4:7]
s_memrealtime s[76:77]
v_exp_f32 v171,v171
v_mfma_f32_16x16x32_bf16 a[8:11],a[208:211],v[136:139],0
v_cvt_pk_bf16_f32 v168,v168,v169
v_cvt_pk_bf16_f32 v169,v170,v171
v_mfma_f32_16x16x32_bf16 a[8:11],a[212:215],v[140:143],a[8:11]
v_exp_f32 v172,v172
v_mfma_f32_16x16x32_bf16 a[12:15],a[220:223],v[140:143],0
v_exp_f32 v173,v173
v_mfma_f32_16x16x32_bf16 a[12:15],a[216:219],v[136:139],a[12:15]
v_exp_f32 v174,v174
v_mfma_f32_16x16x32_bf16 a[16:19],a[224:227],v[136:139],0
v_exp_f32 v175,v175
v_mfma_f32_16x16x32_bf16 a[16:19],a[228:231],v[140:143],a[16:19]
v_cvt_pk_bf16_f32 v170,v172,v173
v_cvt_pk_bf16_f32 v171,v174,v175
v_mfma_f32_16x16x32_bf16 a[20:23],a[236:239],v[140:143],0
s_waitcnt lgkmcnt(0)
v_exp_f32 v184,v184
v_mfma_f32_16x16x32_bf16 a[20:23],a[232:235],v[136:139],a[20:23]
s_add_u32 s78,s76,s60
buffer_load_dwordx4 v5,s[24:27],s47 offen offset:3072 nt lds
v_mfma_f32_16x16x32_bf16 a[24:27],a[240:243],v[136:139],0
s_add_u32 s47,s47,16384
s_and_b32 s47,s47,0x3ffff
s_or_b32 s47,s47,s46
v_mfma_f32_16x16x32_bf16 a[24:27],a[244:247],v[140:143],a[24:27]
s_lshr_b32 s60,s94,2
v_exp_f32 v185,v185
v_mfma_f32_16x16x32_bf16 a[28:31],a[252:255],v[140:143],0
s_max_i32 s60,s60,128
v_exp_f32 v186,v186
v_mfma_f32_16x16x32_bf16 a[28:31],a[248:251],v[136:139],a[28:31]
s_and_b32 s46,s54,0xfffc0000
v_exp_f32 v187,v187
v_smfmac_f32_16x16x64_bf16 v[48:51],v[28:31],v[136:143],v27
v_cvt_pk_bf16_f32 v172,v184,v185
v_mfma_f32_16x16x32_bf16 v[232:235],a[128:131],v[112:115],v[44:47]
s_waitcnt vmcnt(8)
v_exp_f32 v188,v188
v_mfma_f32_16x16x32_bf16 v[232:235],a[132:135],v[116:119],v[232:235]
s_barrier
s_add_u32 m0,65536,s51
v_cvt_pk_bf16_f32 v173,v186,v187
v_mfma_f32_16x16x32_bf16 v[232:235],a[136:139],v[120:123],v[232:235]
v_exp_f32 v189,v189
v_mfma_f32_16x16x32_bf16 v[232:235],a[140:143],v[124:127],v[232:235]
v_exp_f32 v190,v190
v_mfma_f32_16x16x32_bf16 v[236:239],a[156:159],v[124:127],v[44:47]
buffer_load_dwordx4 v4,s[20:23],s47 offen nt lds
v_mfma_f32_16x16x32_bf16 v[236:239],a[152:155],v[120:123],v[236:239]
v_exp_f32 v191,v191
v_mfma_f32_16x16x32_bf16 v[236:239],a[148:151],v[116:119],v[236:239]
v_cvt_pk_bf16_f32 v174,v188,v189
v_cvt_pk_bf16_f32 v175,v190,v191
v_mfma_f32_16x16x32_bf16 v[236:239],a[144:147],v[112:115],v[236:239]
ds_read_b128 a[128:131],v1
v_mov_b32 v52,0
v_mfma_f32_16x16x32_bf16 v[248:251],a[160:163],v[112:115],v[44:47]
v_mov_b32 v53,0
v_mov_b32 v54,0
v_mfma_f32_16x16x32_bf16 v[248:251],a[164:167],v[116:119],v[248:251]
ds_read_b128 a[132:135],v1 offset:256
v_mov_b32 v55,0
v_mfma_f32_16x16x32_bf16 v[248:251],a[168:171],v[120:123],v[248:251]
v_exp_f32 v200,v200
v_mfma_f32_16x16x32_bf16 v[248:251],a[172:175],v[124:127],v[248:251]
ds_read_b128 a[136:139],v1 offset:512
v_mov_b32 v56,0
v_mfma_f32_16x16x32_bf16 v[252:255],a[188:191],v[124:127],v[44:47]
v_exp_f32 v201,v201
v_mfma_f32_16x16x32_bf16 v[252:255],a[184:187],v[120:123],v[252:255]
ds_read_b128 a[140:143],v1 offset:768
v_cvt_pk_bf16_f32 v200,v200,v201
v_mfma_f32_16x16x32_bf16 v[252:255],a[180:183],v[116:119],v[252:255]
v_exp_f32 v202,v202
v_mfma_f32_16x16x32_bf16 v[252:255],a[176:179],v[112:115],v[252:255]
ds_read_b128 a[156:159],v1 offset:4864
v_mov_b32 v57,0
v_mfma_f32_16x16x32_bf16 a[32:35],a[192:195],v[168:171],0
v_exp_f32 v203,v203
v_mfma_f32_16x16x32_bf16 a[32:35],a[196:199],v[172:175],a[32:35]
ds_read_b128 a[152:155],v1 offset:4608
v_cvt_pk_bf16_f32 v201,v202,v203
v_mfma_f32_16x16x32_bf16 a[36:39],a[204:207],v[172:175],0
buffer_load_dwordx4 v4,s[20:23],s47 offen offset:1024 nt lds
v_mfma_f32_16x16x32_bf16 a[36:39],a[200:203],v[168:171],a[36:39]
ds_read_b128 a[148:151],v1 offset:4352
v_mov_b32 v58,0
v_mfma_f32_16x16x32_bf16 a[40:43],a[208:211],v[168:171],0
v_exp_f32 v204,v204
v_mfma_f32_16x16x32_bf16 a[40:43],a[212:215],v[172:175],a[40:43]
ds_read_b128 a[144:147],v1 offset:4096
v_mov_b32 v59,0
v_mfma_f32_16x16x32_bf16 a[44:47],a[220:223],v[172:175],0
v_exp_f32 v205,v205
v_mfma_f32_16x16x32_bf16 a[44:47],a[216:219],v[168:171],a[44:47]
ds_read_b128 a[160:163],v1 offset:8192
v_cvt_pk_bf16_f32 v202,v204,v205
v_mfma_f32_16x16x32_bf16 a[48:51],a[224:227],v[168:171],0
v_exp_f32 v206,v206
v_mfma_f32_16x16x32_bf16 a[48:51],a[228:231],v[172:175],a[48:51]
ds_read_b128 a[164:167],v1 offset:8448
v_mov_b32 v60,0
v_mfma_f32_16x16x32_bf16 a[52:55],a[236:239],v[172:175],0
v_exp_f32 v207,v207
v_mfma_f32_16x16x32_bf16 a[52:55],a[232:235],v[168:171],a[52:55]
ds_read_b128 a[168:171],v1 offset:8704
v_cvt_pk_bf16_f32 v203,v206,v207
v_mfma_f32_16x16x32_bf16 a[56:59],a[240:243],v[168:171],0
v_exp_f32 v216,v216
v_mfma_f32_16x16x32_bf16 a[56:59],a[244:247],v[172:175],a[56:59]
ds_read_b128 a[172:175],v1 offset:8960
v_mov_b32 v61,0
v_mfma_f32_16x16x32_bf16 a[60:63],a[252:255],v[172:175],0
v_exp_f32 v217,v217
v_mfma_f32_16x16x32_bf16 a[60:63],a[248:251],v[168:171],a[60:63]
ds_read_b128 a[188:191],v1 offset:13056
v_cvt_pk_bf16_f32 v204,v216,v217
v_smfmac_f32_16x16x64_bf16 v[52:55],v[28:31],v[168:175],v27
s_waitcnt lgkmcnt(4)
v_mov_b32 v62,0
v_mfma_f32_16x16x32_bf16 v[136:139],a[128:131],v[64:67],v[32:35]
buffer_load_dwordx4 v5,s[20:23],s47 offen offset:2048 nt lds
v_mfma_f32_16x16x32_bf16 v[136:139],a[132:135],v[68:71],v[136:139]
ds_read_b128 a[184:187],v1 offset:12800
v_mov_b32 v63,0
v_mfma_f32_16x16x32_bf16 v[136:139],a[136:139],v[72:75],v[136:139]
v_exp_f32 v218,v218
v_mfma_f32_16x16x32_bf16 v[136:139],a[140:143],v[76:79],v[136:139]
ds_read_b128 a[180:183],v1 offset:12544
v_add_u32 v2,s48,v2
v_mfma_f32_16x16x32_bf16 v[140:143],a[156:159],v[76:79],v[32:35]
v_exp_f32 v219,v219
v_mfma_f32_16x16x32_bf16 v[140:143],a[152:155],v[72:75],v[140:143]
ds_read_b128 a[176:179],v1 offset:12288
v_cvt_pk_bf16_f32 v205,v218,v219
v_mfma_f32_16x16x32_bf16 v[140:143],a[148:151],v[68:71],v[140:143]
v_exp_f32 v220,v220
v_mfma_f32_16x16x32_bf16 v[140:143],a[144:147],v[64:67],v[140:143]
v_exp_f32 v221,v221
v_mfma_f32_16x16x32_bf16 v[152:155],a[160:163],v[64:67],v[32:35]
s_waitcnt lgkmcnt(3)
v_exp_f32 v222,v222
v_mfma_f32_16x16x32_bf16 v[152:155],a[164:167],v[68:71],v[152:155]
v_exp_f32 v223,v223
v_mfma_f32_16x16x32_bf16 v[152:155],a[168:171],v[72:75],v[152:155]
v_cvt_pk_bf16_f32 v206,v220,v221
v_cvt_pk_bf16_f32 v207,v222,v223
v_mfma_f32_16x16x32_bf16 v[152:155],a[172:175],v[76:79],v[152:155]
v_exp_f32 v232,v232
v_mfma_f32_16x16x32_bf16 v[156:159],a[188:191],v[76:79],v[32:35]
s_waitcnt lgkmcnt(1)
v_exp_f32 v233,v233
v_mfma_f32_16x16x32_bf16 v[156:159],a[184:187],v[72:75],v[156:159]
buffer_load_dwordx4 v5,s[20:23],s47 offen offset:3072 nt lds
v_mfma_f32_16x16x32_bf16 v[156:159],a[180:183],v[68:71],v[156:159]
s_waitcnt lgkmcnt(0)
v_exp_f32 v234,v234
v_mfma_f32_16x16x32_bf16 v[156:159],a[176:179],v[64:67],v[156:159]
v_exp_f32 v235,v235
v_mfma_f32_16x16x32_bf16 a[64:67],a[192:195],v[200:203],0
v_cvt_pk_bf16_f32 v232,v232,v233
v_cvt_pk_bf16_f32 v233,v234,v235
v_mfma_f32_16x16x32_bf16 a[64:67],a[196:199],v[204:207],a[64:67]
)A"
R"A(v_exp_f32 v236,v236
v_mfma_f32_16x16x32_bf16 a[68:71],a[204:207],v[204:207],0
v_exp_f32 v237,v237
v_mfma_f32_16x16x32_bf16 a[68:71],a[200:203],v[200:203],a[68:71]
v_exp_f32 v238,v238
v_mfma_f32_16x16x32_bf16 a[72:75],a[208:211],v[200:203],0
v_exp_f32 v239,v239
v_mfma_f32_16x16x32_bf16 a[72:75],a[212:215],v[204:207],a[72:75]
v_cvt_pk_bf16_f32 v234,v236,v237
v_cvt_pk_bf16_f32 v235,v238,v239
v_mfma_f32_16x16x32_bf16 a[76:79],a[220:223],v[204:207],0
v_exp_f32 v248,v248
v_mfma_f32_16x16x32_bf16 a[76:79],a[216:219],v[200:203],a[76:79]
v_exp_f32 v249,v249
v_mfma_f32_16x16x32_bf16 a[80:83],a[224:227],v[200:203],0
s_add_u32 m0,65536,s52
v_exp_f32 v250,v250
v_mfma_f32_16x16x32_bf16 a[80:83],a[228:231],v[204:207],a[80:83]
v_exp_f32 v251,v251
v_mfma_f32_16x16x32_bf16 a[84:87],a[236:239],v[204:207],0
v_exp_f32 v252,v252
v_mfma_f32_16x16x32_bf16 a[84:87],a[232:235],v[200:203],a[84:87]
buffer_load_dwordx4 v4,s[24:27],s47 offen nt lds
v_mfma_f32_16x16x32_bf16 a[88:91],a[240:243],v[200:203],0
v_exp_f32 v253,v253
v_mfma_f32_16x16x32_bf16 a[88:91],a[244:247],v[204:207],a[88:91]
v_exp_f32 v254,v254
v_mfma_f32_16x16x32_bf16 a[92:95],a[252:255],v[204:207],0
v_exp_f32 v255,v255
v_mfma_f32_16x16x32_bf16 a[92:95],a[248:251],v[200:203],a[92:95]
v_cvt_pk_bf16_f32 v236,v248,v249
v_cvt_pk_bf16_f32 v237,v250,v251
v_smfmac_f32_16x16x64_bf16 v[56:59],v[28:31],v[200:207],v27
v_cvt_pk_bf16_f32 v238,v252,v253
v_mfma_f32_16x16x32_bf16 v[168:171],a[128:131],v[80:83],v[36:39]
v_cvt_pk_bf16_f32 v239,v254,v255
v_add_u32 v3,s48,v3
v_mfma_f32_16x16x32_bf16 v[168:171],a[132:135],v[84:87],v[168:171]
s_mov_b32 s48,32768
v_exp_f32 v136,v136
v_mfma_f32_16x16x32_bf16 v[168:171],a[136:139],v[88:91],v[168:171]
v_exp_f32 v137,v137
v_mfma_f32_16x16x32_bf16 v[168:171],a[140:143],v[92:95],v[168:171]
v_exp_f32 v138,v138
v_mfma_f32_16x16x32_bf16 v[172:175],a[156:159],v[92:95],v[36:39]
v_exp_f32 v139,v139
v_mfma_f32_16x16x32_bf16 v[172:175],a[152:155],v[88:91],v[172:175]
v_cvt_pk_bf16_f32 v136,v136,v137
v_cvt_pk_bf16_f32 v137,v138,v139
v_mfma_f32_16x16x32_bf16 v[172:175],a[148:151],v[84:87],v[172:175]
v_exp_f32 v140,v140
v_mfma_f32_16x16x32_bf16 v[172:175],a[144:147],v[80:83],v[172:175]
v_exp_f32 v141,v141
v_mfma_f32_16x16x32_bf16 v[184:187],a[160:163],v[80:83],v[36:39]
buffer_load_dwordx4 v5,s[24:27],s47 offen offset:1024 nt lds
v_mfma_f32_16x16x32_bf16 v[184:187],a[164:167],v[84:87],v[184:187]
v_exp_f32 v142,v142
v_mfma_f32_16x16x32_bf16 v[184:187],a[168:171],v[88:91],v[184:187]
v_exp_f32 v143,v143
v_mfma_f32_16x16x32_bf16 v[184:187],a[172:175],v[92:95],v[184:187]
v_cvt_pk_bf16_f32 v138,v140,v141
v_cvt_pk_bf16_f32 v139,v142,v143
v_mfma_f32_16x16x32_bf16 v[188:191],a[188:191],v[92:95],v[36:39]
v_exp_f32 v152,v152
v_mfma_f32_16x16x32_bf16 v[188:191],a[184:187],v[88:91],v[188:191]
v_exp_f32 v153,v153
v_mfma_f32_16x16x32_bf16 v[188:191],a[180:183],v[84:87],v[188:191]
v_exp_f32 v154,v154
v_mfma_f32_16x16x32_bf16 v[188:191],a[176:179],v[80:83],v[188:191]
v_exp_f32 v155,v155
v_mfma_f32_16x16x32_bf16 a[96:99],a[192:195],v[232:235],0
ds_read_b64_tr_b16 a[192:193],v2
ds_read_b64_tr_b16 a[194:195],v2 offset:4096
v_mfma_f32_16x16x32_bf16 a[96:99],a[196:199],v[236:239],a[96:99]
ds_read_b64_tr_b16 a[196:197],v2 offset:8192
v_cvt_pk_bf16_f32 v140,v152,v153
v_mfma_f32_16x16x32_bf16 a[100:103],a[204:207],v[236:239],0
ds_read_b64_tr_b16 a[198:199],v2 offset:12288
v_cvt_pk_bf16_f32 v141,v154,v155
v_mfma_f32_16x16x32_bf16 a[100:103],a[200:203],v[232:235],a[100:103]
ds_read_b64_tr_b16 a[204:205],v3 offset:8192
v_add_u32 v1,s48,v1
v_mfma_f32_16x16x32_bf16 a[104:107],a[208:211],v[232:235],0
ds_read_b64_tr_b16 a[206:207],v3 offset:12288
ds_read_b64_tr_b16 a[200:201],v3
v_mfma_f32_16x16x32_bf16 a[104:107],a[212:215],v[236:239],a[104:107]
v_exp_f32 v156,v156
v_mfma_f32_16x16x32_bf16 a[108:111],a[220:223],v[236:239],0
buffer_load_dwordx4 v4,s[24:27],s47 offen offset:2048 nt lds
v_mfma_f32_16x16x32_bf16 a[108:111],a[216:219],v[232:235],a[108:111]
ds_read_b64_tr_b16 a[202:203],v3 offset:4096
ds_read_b64_tr_b16 a[208:209],v2 offset:256
v_mfma_f32_16x16x32_bf16 a[112:115],a[224:227],v[232:235],0
v_exp_f32 v157,v157
v_mfma_f32_16x16x32_bf16 a[112:115],a[228:231],v[236:239],a[112:115]
ds_read_b64_tr_b16 a[210:211],v2 offset:4352
ds_read_b64_tr_b16 a[212:213],v2 offset:8448
v_mfma_f32_16x16x32_bf16 a[116:119],a[236:239],v[236:239],0
ds_read_b64_tr_b16 a[214:215],v2 offset:12544
v_cvt_pk_bf16_f32 v142,v156,v157
v_mfma_f32_16x16x32_bf16 a[116:119],a[232:235],v[232:235],a[116:119]
v_exp_f32 v158,v158
v_mfma_f32_16x16x32_bf16 a[120:123],a[240:243],v[232:235],0
v_exp_f32 v159,v159
v_mfma_f32_16x16x32_bf16 a[120:123],a[244:247],v[236:239],a[120:123]
ds_read_b64_tr_b16 a[220:221],v3 offset:8448
ds_read_b64_tr_b16 a[222:223],v3 offset:12544
v_mfma_f32_16x16x32_bf16 a[124:127],a[252:255],v[236:239],0
ds_read_b64_tr_b16 a[216:217],v3 offset:256
v_cvt_pk_bf16_f32 v143,v158,v159
v_mfma_f32_16x16x32_bf16 a[124:127],a[248:251],v[232:235],a[124:127]
ds_read_b64_tr_b16 a[218:219],v3 offset:4352
v_smfmac_f32_16x16x64_bf16 v[60:63],v[28:31],v[232:239],v27
ds_read_b64_tr_b16 a[224:225],v2 offset:512
v_mfma_f32_16x16x32_bf16 v[200:203],a[128:131],v[96:99],v[40:43]
ds_read_b64_tr_b16 a[226:227],v2 offset:4608
ds_read_b64_tr_b16 a[228:229],v2 offset:8704
v_mfma_f32_16x16x32_bf16 v[200:203],a[132:135],v[100:103],v[200:203]
ds_read_b64_tr_b16 a[230:231],v2 offset:12800
v_mfma_f32_16x16x32_bf16 v[200:203],a[136:139],v[104:107],v[200:203]
ds_read_b64_tr_b16 a[236:237],v3 offset:8704
v_mfma_f32_16x16x32_bf16 v[200:203],a[140:143],v[108:111],v[200:203]
buffer_load_dwordx4 v5,s[24:27],s47 offen offset:3072 nt lds
v_mfma_f32_16x16x32_bf16 v[204:207],a[156:159],v[108:111],v[40:43]
s_add_u32 s47,s47,16384
ds_read_b64_tr_b16 a[238:239],v3 offset:12800
ds_read_b64_tr_b16 a[232:233],v3 offset:512
v_mfma_f32_16x16x32_bf16 v[204:207],a[152:155],v[104:107],v[204:207]
s_and_b32 s47,s47,0x3ffff
s_or_b32 s47,s47,s46
ds_read_b64_tr_b16 a[234:235],v3 offset:4608
v_mfma_f32_16x16x32_bf16 v[204:207],a[148:151],v[100:103],v[204:207]
ds_read_b64_tr_b16 a[240:241],v2 offset:768
v_mfma_f32_16x16x32_bf16 v[204:207],a[144:147],v[96:99],v[204:207]
ds_read_b64_tr_b16 a[242:243],v2 offset:4864
v_mfma_f32_16x16x32_bf16 v[216:219],a[160:163],v[96:99],v[40:43]
ds_read_b64_tr_b16 a[244:245],v2 offset:8960
ds_read_b64_tr_b16 a[246:247],v2 offset:13056
v_mfma_f32_16x16x32_bf16 v[216:219],a[164:167],v[100:103],v[216:219]
ds_read_b64_tr_b16 a[252:253],v3 offset:8960
v_mfma_f32_16x16x32_bf16 v[216:219],a[168:171],v[104:107],v[216:219]
ds_read_b64_tr_b16 a[254:255],v3 offset:13056
v_mfma_f32_16x16x32_bf16 v[216:219],a[172:175],v[108:111],v[216:219]
ds_read_b64_tr_b16 a[248:249],v3 offset:768
v_mfma_f32_16x16x32_bf16 v[220:223],a[188:191],v[108:111],v[40:43]
ds_read_b64_tr_b16 a[250:251],v3 offset:4864
v_mfma_f32_16x16x32_bf16 v[220:223],a[184:187],v[104:107],v[220:223]
v_exp_f32 v168,v168
v_mfma_f32_16x16x32_bf16 v[220:223],a[180:183],v[100:103],v[220:223]
v_exp_f32 v169,v169
v_mfma_f32_16x16x32_bf16 v[220:223],a[176:179],v[96:99],v[220:223]
s_waitcnt lgkmcnt(6)
v_exp_f32 v170,v170
v_mfma_f32_16x16x32_bf16 a[0:3],a[192:195],v[136:139],a[0:3]
v_exp_f32 v171,v171
v_mfma_f32_16x16x32_bf16 a[0:3],a[196:199],v[140:143],a[0:3]
v_cvt_pk_bf16_f32 v168,v168,v169
v_cvt_pk_bf16_f32 v169,v170,v171
v_mfma_f32_16x16x32_bf16 a[4:7],a[204:207],v[140:143],a[4:7]
v_exp_f32 v172,v172
v_mfma_f32_16x16x32_bf16 a[4:7],a[200:203],v[136:139],a[4:7]
s_memrealtime s[76:77]
v_exp_f32 v173,v173
v_mfma_f32_16x16x32_bf16 a[8:11],a[208:211],v[136:139],a[8:11]
v_exp_f32 v174,v174
v_mfma_f32_16x16x32_bf16 a[8:11],a[212:215],v[140:143],a[8:11]
v_exp_f32 v175,v175
v_mfma_f32_16x16x32_bf16 a[12:15],a[220:223],v[140:143],a[12:15]
v_exp_f32 v184,v184
v_mfma_f32_16x16x32_bf16 a[12:15],a[216:219],v[136:139],a[12:15]
v_exp_f32 v185,v185
v_mfma_f32_16x16x32_bf16 a[16:19],a[224:227],v[136:139],a[16:19]
v_exp_f32 v186,v186
v_mfma_f32_16x16x32_bf16 a[16:19],a[228:231],v[140:143],a[16:19]
v_exp_f32 v187,v187
v_mfma_f32_16x16x32_bf16 a[20:23],a[236:239],v[140:143],a[20:23]
s_waitcnt lgkmcnt(0)
v_exp_f32 v188,v188
v_mfma_f32_16x16x32_bf16 a[20:23],a[232:235],v[136:139],a[20:23]
s_sub_u32 s79,s78,s76
v_exp_f32 v189,v189
v_mfma_f32_16x16x32_bf16 a[24:27],a[240:243],v[136:139],a[24:27]
s_add_u32 s78,s76,s60
v_exp_f32 v190,v190
v_mfma_f32_16x16x32_bf16 a[24:27],a[244:247],v[140:143],a[24:27]
s_cmp_gt_i32 s79,s95
s_cbranch_scc1 L_ps2_%=
L_pr2_%=:
v_cvt_pk_bf16_f32 v170,v172,v173
v_mfma_f32_16x16x32_bf16 a[28:31],a[252:255],v[140:143],a[28:31]
v_exp_f32 v191,v191
v_mfma_f32_16x16x32_bf16 a[28:31],a[248:251],v[136:139],a[28:31]
v_cvt_pk_bf16_f32 v171,v174,v175
v_cvt_pk_bf16_f32 v172,v184,v185
v_cvt_pk_bf16_f32 v173,v186,v187
v_cvt_pk_bf16_f32 v174,v188,v189
v_cvt_pk_bf16_f32 v175,v190,v191
v_smfmac_f32_16x16x64_bf16 v[48:51],v[28:31],v[136:143],v27
s_mov_b32 s53,13
s_cmp_eq_u32 s40,3
s_cbranch_scc0 L_att0_%=
s_cmp_gt_i32 s18,0
s_cbranch_scc1 L_att0v0_%=
s_cmp_gt_i32 s19,0
s_cbranch_scc1 L_att0v1_%=
s_cmp_gt_i32 s100,0
s_cbranch_scc1 L_att0v2_%=
.p2align 5
L_att0v3_%=:
s_waitcnt vmcnt(8) lgkmcnt(0)
v_exp_f32 v200,v200
s_barrier
s_add_u32 m0,s49,s51
ds_read_b128 a[128:131],v1
s_mov_b32 s90,L_endseg_%=-L_pc_%=+128
v_exp_f32 v201,v201
s_lshr_b32 s45,s45,1
ds_read_b128 a[132:135],v1 offset:256
v_cvt_pk_bf16_f32 v200,v200,v201
buffer_load_dwordx4 v4,s[20:23],s47 offen nt lds
ds_read_b128 a[136:139],v1 offset:512
v_add_u32 v2,s48,v2
v_exp_f32 v202,v202
s_add_u32 s62,s50,32768
ds_read_b128 a[140:143],v1 offset:768
v_add_u32 v3,s48,v3
s_and_b32 s62,s62,131071
v_exp_f32 v203,v203
s_add_u32 s54,s54,16384
ds_read_b128 a[156:159],v1 offset:4864
v_cvt_pk_bf16_f32 v201,v202,v203
s_sub_u32 s48,s62,s50
v_exp_f32 v204,v204
s_sub_u32 s53,s53,1
v_exp_f32 v205,v205
s_and_b32 s46,s54,0xfffc0000
ds_read_b128 a[152:155],v1 offset:4608
v_cvt_pk_bf16_f32 v202,v204,v205
s_mov_b32 s50,s62
v_exp_f32 v206,v206
v_exp_f32 v207,v207
ds_read_b128 a[148:151],v1 offset:4352
v_cvt_pk_bf16_f32 v203,v206,v207
v_mfma_f32_16x16x32_bf16 a[32:35],a[192:195],v[168:171],a[32:35]
v_exp_f32 v216,v216
v_mfma_f32_16x16x32_bf16 a[32:35],a[196:199],v[172:175],a[32:35]
ds_read_b128 a[144:147],v1 offset:4096
v_mfma_f32_16x16x32_bf16 a[36:39],a[204:207],v[172:175],a[36:39]
buffer_load_dwordx4 v4,s[20:23],s47 offen offset:1024 nt lds
v_mfma_f32_16x16x32_bf16 a[36:39],a[200:203],v[168:171],a[36:39]
ds_read_b128 a[160:163],v1 offset:8192
v_mfma_f32_16x16x32_bf16 a[40:43],a[208:211],v[168:171],a[40:43]
v_exp_f32 v217,v217
v_mfma_f32_16x16x32_bf16 a[40:43],a[212:215],v[172:175],a[40:43]
ds_read_b128 a[164:167],v1 offset:8448
v_cvt_pk_bf16_f32 v204,v216,v217
v_mfma_f32_16x16x32_bf16 a[44:47],a[220:223],v[172:175],a[44:47]
v_exp_f32 v218,v218
v_mfma_f32_16x16x32_bf16 a[44:47],a[216:219],v[168:171],a[44:47]
ds_read_b128 a[168:171],v1 offset:8704
v_mfma_f32_16x16x32_bf16 a[48:51],a[224:227],v[168:171],a[48:51]
v_exp_f32 v219,v219
v_mfma_f32_16x16x32_bf16 a[48:51],a[228:231],v[172:175],a[48:51]
ds_read_b128 a[172:175],v1 offset:8960
v_cvt_pk_bf16_f32 v205,v218,v219
v_mfma_f32_16x16x32_bf16 a[52:55],a[236:239],v[172:175],a[52:55]
v_exp_f32 v220,v220
v_mfma_f32_16x16x32_bf16 a[52:55],a[232:235],v[168:171],a[52:55]
ds_read_b128 a[188:191],v1 offset:13056
v_mfma_f32_16x16x32_bf16 a[56:59],a[240:243],v[168:171],a[56:59]
v_exp_f32 v221,v221
v_mfma_f32_16x16x32_bf16 a[56:59],a[244:247],v[172:175],a[56:59]
ds_read_b128 a[184:187],v1 offset:12800
v_cvt_pk_bf16_f32 v206,v220,v221
v_mfma_f32_16x16x32_bf16 a[60:63],a[252:255],v[172:175],a[60:63]
v_exp_f32 v222,v222
v_mfma_f32_16x16x32_bf16 a[60:63],a[248:251],v[168:171],a[60:63]
ds_read_b128 a[180:183],v1 offset:12544
v_smfmac_f32_16x16x64_bf16 v[52:55],v[28:31],v[168:175],v27
s_waitcnt lgkmcnt(4)
v_mfma_f32_16x16x32_bf16 v[136:139],a[128:131],v[64:67],v[32:35]
buffer_load_dwordx4 v5,s[20:23],s47 offen offset:2048 nt lds
v_mfma_f32_16x16x32_bf16 v[136:139],a[132:135],v[68:71],v[136:139]
ds_read_b128 a[176:179],v1 offset:12288
v_add_u32 v1,s48,v1
v_mfma_f32_16x16x32_bf16 v[136:139],a[136:139],v[72:75],v[136:139]
)A"
R"A(v_exp_f32 v223,v223
v_mfma_f32_16x16x32_bf16 v[136:139],a[140:143],v[76:79],v[136:139]
v_cvt_pk_bf16_f32 v207,v222,v223
v_mfma_f32_16x16x32_bf16 v[140:143],a[156:159],v[76:79],v[32:35]
v_mfma_f32_16x16x32_bf16 v[140:143],a[152:155],v[72:75],v[140:143]
v_mfma_f32_16x16x32_bf16 v[140:143],a[148:151],v[68:71],v[140:143]
v_mfma_f32_16x16x32_bf16 v[140:143],a[144:147],v[64:67],v[140:143]
v_mfma_f32_16x16x32_bf16 v[152:155],a[160:163],v[64:67],v[32:35]
v_mfma_f32_16x16x32_bf16 v[152:155],a[164:167],v[68:71],v[152:155]
v_mfma_f32_16x16x32_bf16 v[152:155],a[168:171],v[72:75],v[152:155]
s_waitcnt lgkmcnt(0)
v_mfma_f32_16x16x32_bf16 v[152:155],a[172:175],v[76:79],v[152:155]
v_mfma_f32_16x16x32_bf16 v[156:159],a[188:191],v[76:79],v[32:35]
v_mfma_f32_16x16x32_bf16 v[156:159],a[184:187],v[72:75],v[156:159]
buffer_load_dwordx4 v5,s[20:23],s47 offen offset:3072 nt lds
v_mfma_f32_16x16x32_bf16 v[156:159],a[180:183],v[68:71],v[156:159]
v_mfma_f32_16x16x32_bf16 v[156:159],a[176:179],v[64:67],v[156:159]
v_mfma_f32_16x16x32_bf16 a[64:67],a[192:195],v[200:203],a[64:67]
v_mfma_f32_16x16x32_bf16 a[64:67],a[196:199],v[204:207],a[64:67]
v_mfma_f32_16x16x32_bf16 a[68:71],a[204:207],v[204:207],a[68:71]
v_mfma_f32_16x16x32_bf16 a[68:71],a[200:203],v[200:203],a[68:71]
v_mfma_f32_16x16x32_bf16 a[72:75],a[208:211],v[200:203],a[72:75]
v_mfma_f32_16x16x32_bf16 a[72:75],a[212:215],v[204:207],a[72:75]
v_exp_f32 v136,v136
v_mfma_f32_16x16x32_bf16 a[76:79],a[220:223],v[204:207],a[76:79]
v_exp_f32 v137,v137
v_mfma_f32_16x16x32_bf16 a[76:79],a[216:219],v[200:203],a[76:79]
v_exp_f32 v138,v138
v_mfma_f32_16x16x32_bf16 a[80:83],a[224:227],v[200:203],a[80:83]
s_add_u32 m0,s49,s52
v_mfma_f32_16x16x32_bf16 a[80:83],a[228:231],v[204:207],a[80:83]
s_xor_b32 s49,s50,65536
v_mfma_f32_16x16x32_bf16 a[84:87],a[236:239],v[204:207],a[84:87]
v_mfma_f32_16x16x32_bf16 a[84:87],a[232:235],v[200:203],a[84:87]
buffer_load_dwordx4 v4,s[24:27],s47 offen nt lds
v_mfma_f32_16x16x32_bf16 a[88:91],a[240:243],v[200:203],a[88:91]
v_mfma_f32_16x16x32_bf16 a[88:91],a[244:247],v[204:207],a[88:91]
v_exp_f32 v139,v139
v_mfma_f32_16x16x32_bf16 a[92:95],a[252:255],v[204:207],a[92:95]
v_exp_f32 v140,v140
v_mfma_f32_16x16x32_bf16 a[92:95],a[248:251],v[200:203],a[92:95]
v_exp_f32 v141,v141
v_smfmac_f32_16x16x64_bf16 v[56:59],v[28:31],v[200:207],v27
v_mfma_f32_16x16x32_bf16 v[168:171],a[128:131],v[80:83],v[36:39]
v_exp_f32 v142,v142
v_mfma_f32_16x16x32_bf16 v[168:171],a[132:135],v[84:87],v[168:171]
v_exp_f32 v143,v143
v_mfma_f32_16x16x32_bf16 v[168:171],a[136:139],v[88:91],v[168:171]
v_exp_f32 v152,v152
v_mfma_f32_16x16x32_bf16 v[168:171],a[140:143],v[92:95],v[168:171]
v_exp_f32 v153,v153
v_mfma_f32_16x16x32_bf16 v[172:175],a[156:159],v[92:95],v[36:39]
v_exp_f32 v154,v154
v_mfma_f32_16x16x32_bf16 v[172:175],a[152:155],v[88:91],v[172:175]
v_exp_f32 v155,v155
v_mfma_f32_16x16x32_bf16 v[172:175],a[148:151],v[84:87],v[172:175]
v_exp_f32 v156,v156
v_mfma_f32_16x16x32_bf16 v[172:175],a[144:147],v[80:83],v[172:175]
v_exp_f32 v157,v157
v_mfma_f32_16x16x32_bf16 v[184:187],a[160:163],v[80:83],v[36:39]
buffer_load_dwordx4 v5,s[24:27],s47 offen offset:1024 nt lds
v_mfma_f32_16x16x32_bf16 v[184:187],a[164:167],v[84:87],v[184:187]
v_exp_f32 v158,v158
v_mfma_f32_16x16x32_bf16 v[184:187],a[168:171],v[88:91],v[184:187]
v_exp_f32 v159,v159
v_mfma_f32_16x16x32_bf16 v[184:187],a[172:175],v[92:95],v[184:187]
v_exp_f32 v168,v168
v_mfma_f32_16x16x32_bf16 v[188:191],a[188:191],v[92:95],v[36:39]
v_exp_f32 v169,v169
v_mfma_f32_16x16x32_bf16 v[188:191],a[184:187],v[88:91],v[188:191]
v_exp_f32 v170,v170
v_mfma_f32_16x16x32_bf16 v[188:191],a[180:183],v[84:87],v[188:191]
v_exp_f32 v171,v171
v_mfma_f32_16x16x32_bf16 v[188:191],a[176:179],v[80:83],v[188:191]
v_exp_f32 v172,v172
ds_read_b64_tr_b16 a[192:193],v2
v_cvt_pk_bf16_f32 v168,v168,v169
ds_read_b64_tr_b16 a[194:195],v2 offset:4096
v_cvt_pk_bf16_f32 v169,v170,v171
v_exp_f32 v173,v173
ds_read_b64_tr_b16 a[196:197],v2 offset:8192
v_cvt_pk_bf16_f32 v170,v172,v173
v_exp_f32 v174,v174
v_exp_f32 v175,v175
buffer_load_dwordx4 v4,s[24:27],s47 offen offset:2048 nt lds
ds_read_b64_tr_b16 a[198:199],v2 offset:12288
v_cvt_pk_bf16_f32 v171,v174,v175
v_exp_f32 v184,v184
v_exp_f32 v185,v185
ds_read_b64_tr_b16 a[204:205],v3 offset:8192
v_cvt_pk_bf16_f32 v136,v136,v137
ds_read_b64_tr_b16 a[206:207],v3 offset:12288
v_cvt_pk_bf16_f32 v172,v184,v185
ds_read_b64_tr_b16 a[200:201],v3
ds_read_b64_tr_b16 a[202:203],v3 offset:4096
v_cvt_pk_bf16_f32 v137,v138,v139
ds_read_b64_tr_b16 a[208:209],v2 offset:256
ds_read_b64_tr_b16 a[210:211],v2 offset:4352
ds_read_b64_tr_b16 a[212:213],v2 offset:8448
v_mfma_f32_16x16x32_bf16 v[200:203],a[128:131],v[96:99],v[40:43]
ds_read_b64_tr_b16 a[214:215],v2 offset:12544
v_cvt_pk_bf16_f32 v138,v140,v141
v_mfma_f32_16x16x32_bf16 v[200:203],a[132:135],v[100:103],v[200:203]
ds_read_b64_tr_b16 a[220:221],v3 offset:8448
v_mfma_f32_16x16x32_bf16 v[200:203],a[136:139],v[104:107],v[200:203]
ds_read_b64_tr_b16 a[222:223],v3 offset:12544
v_cvt_pk_bf16_f32 v139,v142,v143
v_mfma_f32_16x16x32_bf16 v[200:203],a[140:143],v[108:111],v[200:203]
buffer_load_dwordx4 v5,s[24:27],s47 offen offset:3072 nt lds
v_mfma_f32_16x16x32_bf16 v[204:207],a[156:159],v[108:111],v[40:43]
s_add_u32 s47,s47,16384
s_and_b32 s47,s47,0x3ffff
ds_read_b64_tr_b16 a[216:217],v3 offset:256
v_mfma_f32_16x16x32_bf16 v[204:207],a[152:155],v[104:107],v[204:207]
s_or_b32 s47,s47,s46
ds_read_b64_tr_b16 a[218:219],v3 offset:4352
v_cvt_pk_bf16_f32 v140,v152,v153
v_mfma_f32_16x16x32_bf16 v[204:207],a[148:151],v[100:103],v[204:207]
ds_read_b64_tr_b16 a[224:225],v2 offset:512
v_mfma_f32_16x16x32_bf16 v[204:207],a[144:147],v[96:99],v[204:207]
ds_read_b64_tr_b16 a[226:227],v2 offset:4608
v_mfma_f32_16x16x32_bf16 v[216:219],a[160:163],v[96:99],v[40:43]
ds_read_b64_tr_b16 a[228:229],v2 offset:8704
v_cvt_pk_bf16_f32 v141,v154,v155
v_mfma_f32_16x16x32_bf16 v[216:219],a[164:167],v[100:103],v[216:219]
ds_read_b64_tr_b16 a[230:231],v2 offset:12800
v_mfma_f32_16x16x32_bf16 v[216:219],a[168:171],v[104:107],v[216:219]
ds_read_b64_tr_b16 a[236:237],v3 offset:8704
v_mfma_f32_16x16x32_bf16 v[216:219],a[172:175],v[108:111],v[216:219]
ds_read_b64_tr_b16 a[238:239],v3 offset:12800
v_cvt_pk_bf16_f32 v142,v156,v157
v_mfma_f32_16x16x32_bf16 v[220:223],a[188:191],v[108:111],v[40:43]
ds_read_b64_tr_b16 a[232:233],v3 offset:512
v_mfma_f32_16x16x32_bf16 v[220:223],a[184:187],v[104:107],v[220:223]
ds_read_b64_tr_b16 a[234:235],v3 offset:4608
v_mfma_f32_16x16x32_bf16 v[220:223],a[180:183],v[100:103],v[220:223]
ds_read_b64_tr_b16 a[240:241],v2 offset:768
v_cvt_pk_bf16_f32 v143,v158,v159
v_mfma_f32_16x16x32_bf16 v[220:223],a[176:179],v[96:99],v[220:223]
s_waitcnt lgkmcnt(7)
buffer_load_dword v26,v24,s[88:91],s93 offen
v_mfma_f32_16x16x32_bf16 a[0:3],a[192:195],v[136:139],a[0:3]
ds_read_b64_tr_b16 a[242:243],v2 offset:4864
v_mfma_f32_16x16x32_bf16 a[0:3],a[196:199],v[140:143],a[0:3]
ds_read_b64_tr_b16 a[244:245],v2 offset:8960
v_mfma_f32_16x16x32_bf16 a[4:7],a[204:207],v[140:143],a[4:7]
ds_read_b64_tr_b16 a[246:247],v2 offset:13056
v_mfma_f32_16x16x32_bf16 a[4:7],a[200:203],v[136:139],a[4:7]
ds_read_b64_tr_b16 a[252:253],v3 offset:8960
v_mfma_f32_16x16x32_bf16 a[8:11],a[208:211],v[136:139],a[8:11]
ds_read_b64_tr_b16 a[254:255],v3 offset:13056
v_mfma_f32_16x16x32_bf16 a[8:11],a[212:215],v[140:143],a[8:11]
ds_read_b64_tr_b16 a[248:249],v3 offset:768
v_mfma_f32_16x16x32_bf16 a[12:15],a[220:223],v[140:143],a[12:15]
ds_read_b64_tr_b16 a[250:251],v3 offset:4864
v_mfma_f32_16x16x32_bf16 a[12:15],a[216:219],v[136:139],a[12:15]
s_bitcmp1_b32 s45,0
s_cbranch_scc0 L_t31v30_%=
s_mov_b32 m0,s80
s_add_u32 s80,s80,4096
buffer_load_dwordx4 v25,s[20:23],s83 offen nt lds
s_add_u32 s83,s83,4096
L_t31v30_%=:
v_mfma_f32_16x16x32_bf16 a[16:19],a[224:227],v[136:139],a[16:19]
s_waitcnt lgkmcnt(6)
v_exp_f32 v186,v186
v_mfma_f32_16x16x32_bf16 a[16:19],a[228:231],v[140:143],a[16:19]
v_exp_f32 v187,v187
v_mfma_f32_16x16x32_bf16 a[20:23],a[236:239],v[140:143],a[20:23]
v_exp_f32 v188,v188
v_mfma_f32_16x16x32_bf16 a[20:23],a[232:235],v[136:139],a[20:23]
v_exp_f32 v189,v189
v_mfma_f32_16x16x32_bf16 a[24:27],a[240:243],v[136:139],a[24:27]
s_waitcnt lgkmcnt(2)
v_exp_f32 v190,v190
v_mfma_f32_16x16x32_bf16 a[24:27],a[244:247],v[140:143],a[24:27]
v_cvt_pk_bf16_f32 v173,v186,v187
v_mfma_f32_16x16x32_bf16 a[28:31],a[252:255],v[140:143],a[28:31]
s_waitcnt lgkmcnt(0)
v_exp_f32 v191,v191
v_mfma_f32_16x16x32_bf16 a[28:31],a[248:251],v[136:139],a[28:31]
v_cvt_pk_bf16_f32 v174,v188,v189
v_cvt_pk_bf16_f32 v175,v190,v191
s_cmp_lg_u32 s53,0
v_smfmac_f32_16x16x64_bf16 v[48:51],v[28:31],v[136:143],v27
s_cbranch_scc1 L_att0v3_%=
s_waitcnt lgkmcnt(0)
s_nop 7
s_branch L_att0x_%=
.p2align 5
L_att0v0_%=:
v_mfma_f32_16x16x32_bf16 v[232:235],a[128:131],v[112:115],v[44:47]
s_waitcnt vmcnt(8) lgkmcnt(0)
v_exp_f32 v200,v200
v_mfma_f32_16x16x32_bf16 v[232:235],a[132:135],v[116:119],v[232:235]
s_barrier
s_add_u32 m0,s49,s51
ds_read_b128 a[128:131],v1
v_mfma_f32_16x16x32_bf16 v[232:235],a[136:139],v[120:123],v[232:235]
s_mov_b32 s90,L_endseg_%=-L_pc_%=+128
v_exp_f32 v201,v201
v_mfma_f32_16x16x32_bf16 v[232:235],a[140:143],v[124:127],v[232:235]
s_lshr_b32 s45,s45,1
ds_read_b128 a[132:135],v1 offset:256
v_cvt_pk_bf16_f32 v200,v200,v201
v_mfma_f32_16x16x32_bf16 v[236:239],a[156:159],v[124:127],v[44:47]
buffer_load_dwordx4 v4,s[20:23],s47 offen nt lds
v_mfma_f32_16x16x32_bf16 v[236:239],a[152:155],v[120:123],v[236:239]
ds_read_b128 a[136:139],v1 offset:512
v_add_u32 v2,s48,v2
v_mfma_f32_16x16x32_bf16 v[236:239],a[148:151],v[116:119],v[236:239]
v_exp_f32 v202,v202
v_mfma_f32_16x16x32_bf16 v[236:239],a[144:147],v[112:115],v[236:239]
s_add_u32 s62,s50,32768
ds_read_b128 a[140:143],v1 offset:768
v_add_u32 v3,s48,v3
v_mfma_f32_16x16x32_bf16 v[248:251],a[160:163],v[112:115],v[44:47]
s_and_b32 s62,s62,131071
v_exp_f32 v203,v203
v_mfma_f32_16x16x32_bf16 v[248:251],a[164:167],v[116:119],v[248:251]
s_add_u32 s54,s54,16384
ds_read_b128 a[156:159],v1 offset:4864
v_cvt_pk_bf16_f32 v201,v202,v203
v_mfma_f32_16x16x32_bf16 v[248:251],a[168:171],v[120:123],v[248:251]
s_sub_u32 s48,s62,s50
v_exp_f32 v204,v204
v_mfma_f32_16x16x32_bf16 v[248:251],a[172:175],v[124:127],v[248:251]
s_sub_u32 s53,s53,1
v_exp_f32 v205,v205
v_mfma_f32_16x16x32_bf16 v[252:255],a[188:191],v[124:127],v[44:47]
s_and_b32 s46,s54,0xfffc0000
ds_read_b128 a[152:155],v1 offset:4608
v_cvt_pk_bf16_f32 v202,v204,v205
v_mfma_f32_16x16x32_bf16 v[252:255],a[184:187],v[120:123],v[252:255]
s_mov_b32 s50,s62
v_exp_f32 v206,v206
v_mfma_f32_16x16x32_bf16 v[252:255],a[180:183],v[116:119],v[252:255]
v_exp_f32 v207,v207
v_mfma_f32_16x16x32_bf16 v[252:255],a[176:179],v[112:115],v[252:255]
ds_read_b128 a[148:151],v1 offset:4352
v_cvt_pk_bf16_f32 v203,v206,v207
v_mfma_f32_16x16x32_bf16 a[32:35],a[192:195],v[168:171],a[32:35]
v_exp_f32 v216,v216
v_mfma_f32_16x16x32_bf16 a[32:35],a[196:199],v[172:175],a[32:35]
ds_read_b128 a[144:147],v1 offset:4096
v_mfma_f32_16x16x32_bf16 a[36:39],a[204:207],v[172:175],a[36:39]
buffer_load_dwordx4 v4,s[20:23],s47 offen offset:1024 nt lds
v_mfma_f32_16x16x32_bf16 a[36:39],a[200:203],v[168:171],a[36:39]
ds_read_b128 a[160:163],v1 offset:8192
v_mfma_f32_16x16x32_bf16 a[40:43],a[208:211],v[168:171],a[40:43]
v_exp_f32 v217,v217
v_mfma_f32_16x16x32_bf16 a[40:43],a[212:215],v[172:175],a[40:43]
ds_read_b128 a[164:167],v1 offset:8448
v_cvt_pk_bf16_f32 v204,v216,v217
v_mfma_f32_16x16x32_bf16 a[44:47],a[220:223],v[172:175],a[44:47]
v_exp_f32 v218,v218
v_mfma_f32_16x16x32_bf16 a[44:47],a[216:219],v[168:171],a[44:47]
ds_read_b128 a[168:171],v1 offset:8704
v_mfma_f32_16x16x32_bf16 a[48:51],a[224:227],v[168:171],a[48:51]
v_exp_f32 v219,v219
v_mfma_f32_16x16x32_bf16 a[48:51],a[228:231],v[172:175],a[48:51]
ds_read_b128 a[172:175],v1 offset:8960
v_cvt_pk_bf16_f32 v205,v218,v219
v_mfma_f32_16x16x32_bf16 a[52:55],a[236:239],v[172:175],a[52:55]
v_exp_f32 v220,v220
v_mfma_f32_16x16x32_bf16 a[52:55],a[232:235],v[168:171],a[52:55]
ds_read_b128 a[188:191],v1 offset:13056
v_mfma_f32_16x16x32_bf16 a[56:59],a[240:243],v[168:171],a[56:59]
)A"
R"A(v_exp_f32 v221,v221
v_mfma_f32_16x16x32_bf16 a[56:59],a[244:247],v[172:175],a[56:59]
ds_read_b128 a[184:187],v1 offset:12800
v_cvt_pk_bf16_f32 v206,v220,v221
v_mfma_f32_16x16x32_bf16 a[60:63],a[252:255],v[172:175],a[60:63]
v_exp_f32 v222,v222
v_mfma_f32_16x16x32_bf16 a[60:63],a[248:251],v[168:171],a[60:63]
ds_read_b128 a[180:183],v1 offset:12544
v_smfmac_f32_16x16x64_bf16 v[52:55],v[28:31],v[168:175],v27
buffer_load_dwordx4 v5,s[20:23],s47 offen offset:2048 nt lds
ds_read_b128 a[176:179],v1 offset:12288
v_add_u32 v1,s48,v1
v_exp_f32 v223,v223
s_nop 0
v_cvt_pk_bf16_f32 v207,v222,v223
v_exp_f32 v232,v232
v_exp_f32 v233,v233
v_exp_f32 v234,v234
v_exp_f32 v235,v235
v_exp_f32 v236,v236
v_exp_f32 v237,v237
v_exp_f32 v238,v238
v_exp_f32 v239,v239
v_exp_f32 v248,v248
buffer_load_dwordx4 v5,s[20:23],s47 offen offset:3072 nt lds
v_exp_f32 v249,v249
v_exp_f32 v250,v250
v_mfma_f32_16x16x32_bf16 a[64:67],a[192:195],v[200:203],a[64:67]
v_exp_f32 v251,v251
v_mfma_f32_16x16x32_bf16 a[64:67],a[196:199],v[204:207],a[64:67]
v_exp_f32 v252,v252
v_mfma_f32_16x16x32_bf16 a[68:71],a[204:207],v[204:207],a[68:71]
v_exp_f32 v253,v253
v_mfma_f32_16x16x32_bf16 a[68:71],a[200:203],v[200:203],a[68:71]
v_exp_f32 v254,v254
v_mfma_f32_16x16x32_bf16 a[72:75],a[208:211],v[200:203],a[72:75]
v_exp_f32 v255,v255
v_mfma_f32_16x16x32_bf16 a[72:75],a[212:215],v[204:207],a[72:75]
v_mfma_f32_16x16x32_bf16 a[76:79],a[220:223],v[204:207],a[76:79]
v_mfma_f32_16x16x32_bf16 a[76:79],a[216:219],v[200:203],a[76:79]
v_mfma_f32_16x16x32_bf16 a[80:83],a[224:227],v[200:203],a[80:83]
s_add_u32 m0,s49,s52
v_cvt_pk_bf16_f32 v232,v232,v233
v_cvt_pk_bf16_f32 v233,v234,v235
v_mfma_f32_16x16x32_bf16 a[80:83],a[228:231],v[204:207],a[80:83]
s_xor_b32 s49,s50,65536
v_cvt_pk_bf16_f32 v234,v236,v237
v_cvt_pk_bf16_f32 v235,v238,v239
v_mfma_f32_16x16x32_bf16 a[84:87],a[236:239],v[204:207],a[84:87]
v_cvt_pk_bf16_f32 v236,v248,v249
v_cvt_pk_bf16_f32 v237,v250,v251
v_mfma_f32_16x16x32_bf16 a[84:87],a[232:235],v[200:203],a[84:87]
buffer_load_dwordx4 v4,s[24:27],s47 offen nt lds
v_mfma_f32_16x16x32_bf16 a[88:91],a[240:243],v[200:203],a[88:91]
v_cvt_pk_bf16_f32 v238,v252,v253
v_cvt_pk_bf16_f32 v239,v254,v255
v_mfma_f32_16x16x32_bf16 a[88:91],a[244:247],v[204:207],a[88:91]
v_mfma_f32_16x16x32_bf16 a[92:95],a[252:255],v[204:207],a[92:95]
v_mfma_f32_16x16x32_bf16 a[92:95],a[248:251],v[200:203],a[92:95]
v_smfmac_f32_16x16x64_bf16 v[56:59],v[28:31],v[200:207],v27
s_waitcnt lgkmcnt(0)
v_mfma_f32_16x16x32_bf16 v[168:171],a[128:131],v[80:83],v[36:39]
v_mfma_f32_16x16x32_bf16 v[168:171],a[132:135],v[84:87],v[168:171]
v_mfma_f32_16x16x32_bf16 v[168:171],a[136:139],v[88:91],v[168:171]
v_mfma_f32_16x16x32_bf16 v[168:171],a[140:143],v[92:95],v[168:171]
v_mfma_f32_16x16x32_bf16 v[172:175],a[156:159],v[92:95],v[36:39]
v_mfma_f32_16x16x32_bf16 v[172:175],a[152:155],v[88:91],v[172:175]
v_mfma_f32_16x16x32_bf16 v[172:175],a[148:151],v[84:87],v[172:175]
v_mfma_f32_16x16x32_bf16 v[172:175],a[144:147],v[80:83],v[172:175]
v_mfma_f32_16x16x32_bf16 v[184:187],a[160:163],v[80:83],v[36:39]
buffer_load_dwordx4 v5,s[24:27],s47 offen offset:1024 nt lds
v_mfma_f32_16x16x32_bf16 v[184:187],a[164:167],v[84:87],v[184:187]
v_mfma_f32_16x16x32_bf16 v[184:187],a[168:171],v[88:91],v[184:187]
v_mfma_f32_16x16x32_bf16 v[184:187],a[172:175],v[92:95],v[184:187]
v_exp_f32 v168,v168
v_mfma_f32_16x16x32_bf16 v[188:191],a[188:191],v[92:95],v[36:39]
v_exp_f32 v169,v169
v_mfma_f32_16x16x32_bf16 v[188:191],a[184:187],v[88:91],v[188:191]
v_exp_f32 v170,v170
v_mfma_f32_16x16x32_bf16 v[188:191],a[180:183],v[84:87],v[188:191]
v_exp_f32 v171,v171
v_mfma_f32_16x16x32_bf16 v[188:191],a[176:179],v[80:83],v[188:191]
v_exp_f32 v172,v172
v_mfma_f32_16x16x32_bf16 a[96:99],a[192:195],v[232:235],a[96:99]
ds_read_b64_tr_b16 a[192:193],v2
v_cvt_pk_bf16_f32 v168,v168,v169
v_mfma_f32_16x16x32_bf16 a[96:99],a[196:199],v[236:239],a[96:99]
ds_read_b64_tr_b16 a[194:195],v2 offset:4096
v_cvt_pk_bf16_f32 v169,v170,v171
v_mfma_f32_16x16x32_bf16 a[100:103],a[204:207],v[236:239],a[100:103]
v_exp_f32 v173,v173
v_mfma_f32_16x16x32_bf16 a[100:103],a[200:203],v[232:235],a[100:103]
ds_read_b64_tr_b16 a[196:197],v2 offset:8192
v_cvt_pk_bf16_f32 v170,v172,v173
v_mfma_f32_16x16x32_bf16 a[104:107],a[208:211],v[232:235],a[104:107]
v_exp_f32 v174,v174
v_mfma_f32_16x16x32_bf16 a[104:107],a[212:215],v[236:239],a[104:107]
v_exp_f32 v175,v175
v_mfma_f32_16x16x32_bf16 a[108:111],a[220:223],v[236:239],a[108:111]
buffer_load_dwordx4 v4,s[24:27],s47 offen offset:2048 nt lds
v_mfma_f32_16x16x32_bf16 a[108:111],a[216:219],v[232:235],a[108:111]
ds_read_b64_tr_b16 a[198:199],v2 offset:12288
v_cvt_pk_bf16_f32 v171,v174,v175
v_mfma_f32_16x16x32_bf16 a[112:115],a[224:227],v[232:235],a[112:115]
v_exp_f32 v184,v184
v_mfma_f32_16x16x32_bf16 a[112:115],a[228:231],v[236:239],a[112:115]
v_exp_f32 v185,v185
v_mfma_f32_16x16x32_bf16 a[116:119],a[236:239],v[236:239],a[116:119]
ds_read_b64_tr_b16 a[204:205],v3 offset:8192
v_mfma_f32_16x16x32_bf16 a[116:119],a[232:235],v[232:235],a[116:119]
ds_read_b64_tr_b16 a[206:207],v3 offset:12288
v_cvt_pk_bf16_f32 v172,v184,v185
v_mfma_f32_16x16x32_bf16 a[120:123],a[240:243],v[232:235],a[120:123]
ds_read_b64_tr_b16 a[200:201],v3
v_mfma_f32_16x16x32_bf16 a[120:123],a[244:247],v[236:239],a[120:123]
ds_read_b64_tr_b16 a[202:203],v3 offset:4096
v_mfma_f32_16x16x32_bf16 a[124:127],a[252:255],v[236:239],a[124:127]
ds_read_b64_tr_b16 a[208:209],v2 offset:256
v_mfma_f32_16x16x32_bf16 a[124:127],a[248:251],v[232:235],a[124:127]
ds_read_b64_tr_b16 a[210:211],v2 offset:4352
v_smfmac_f32_16x16x64_bf16 v[60:63],v[28:31],v[232:239],v27
ds_read_b64_tr_b16 a[212:213],v2 offset:8448
v_mfma_f32_16x16x32_bf16 v[200:203],a[128:131],v[96:99],v[40:43]
ds_read_b64_tr_b16 a[214:215],v2 offset:12544
v_mfma_f32_16x16x32_bf16 v[200:203],a[132:135],v[100:103],v[200:203]
ds_read_b64_tr_b16 a[220:221],v3 offset:8448
v_mfma_f32_16x16x32_bf16 v[200:203],a[136:139],v[104:107],v[200:203]
ds_read_b64_tr_b16 a[222:223],v3 offset:12544
v_mfma_f32_16x16x32_bf16 v[200:203],a[140:143],v[108:111],v[200:203]
buffer_load_dwordx4 v5,s[24:27],s47 offen offset:3072 nt lds
v_mfma_f32_16x16x32_bf16 v[204:207],a[156:159],v[108:111],v[40:43]
s_add_u32 s47,s47,16384
s_and_b32 s47,s47,0x3ffff
ds_read_b64_tr_b16 a[216:217],v3 offset:256
v_mfma_f32_16x16x32_bf16 v[204:207],a[152:155],v[104:107],v[204:207]
s_or_b32 s47,s47,s46
ds_read_b64_tr_b16 a[218:219],v3 offset:4352
v_mfma_f32_16x16x32_bf16 v[204:207],a[148:151],v[100:103],v[204:207]
ds_read_b64_tr_b16 a[224:225],v2 offset:512
v_mfma_f32_16x16x32_bf16 v[204:207],a[144:147],v[96:99],v[204:207]
ds_read_b64_tr_b16 a[226:227],v2 offset:4608
v_mfma_f32_16x16x32_bf16 v[216:219],a[160:163],v[96:99],v[40:43]
ds_read_b64_tr_b16 a[228:229],v2 offset:8704
v_mfma_f32_16x16x32_bf16 v[216:219],a[164:167],v[100:103],v[216:219]
ds_read_b64_tr_b16 a[230:231],v2 offset:12800
v_mfma_f32_16x16x32_bf16 v[216:219],a[168:171],v[104:107],v[216:219]
ds_read_b64_tr_b16 a[236:237],v3 offset:8704
v_mfma_f32_16x16x32_bf16 v[216:219],a[172:175],v[108:111],v[216:219]
ds_read_b64_tr_b16 a[238:239],v3 offset:12800
v_mfma_f32_16x16x32_bf16 v[220:223],a[188:191],v[108:111],v[40:43]
ds_read_b64_tr_b16 a[232:233],v3 offset:512
v_mfma_f32_16x16x32_bf16 v[220:223],a[184:187],v[104:107],v[220:223]
ds_read_b64_tr_b16 a[234:235],v3 offset:4608
v_mfma_f32_16x16x32_bf16 v[220:223],a[180:183],v[100:103],v[220:223]
ds_read_b64_tr_b16 a[240:241],v2 offset:768
v_mfma_f32_16x16x32_bf16 v[220:223],a[176:179],v[96:99],v[220:223]
buffer_load_dword v26,v24,s[88:91],s93 offen
ds_read_b64_tr_b16 a[242:243],v2 offset:4864
ds_read_b64_tr_b16 a[244:245],v2 offset:8960
ds_read_b64_tr_b16 a[246:247],v2 offset:13056
ds_read_b64_tr_b16 a[252:253],v3 offset:8960
ds_read_b64_tr_b16 a[254:255],v3 offset:13056
ds_read_b64_tr_b16 a[248:249],v3 offset:768
ds_read_b64_tr_b16 a[250:251],v3 offset:4864
s_bitcmp1_b32 s45,0
s_cbranch_scc0 L_t31v00_%=
s_mov_b32 m0,s80
s_add_u32 s80,s80,4096
buffer_load_dwordx4 v25,s[20:23],s83 offen nt lds
s_add_u32 s83,s83,4096
L_t31v00_%=:
v_exp_f32 v186,v186
v_exp_f32 v187,v187
v_exp_f32 v188,v188
v_exp_f32 v189,v189
v_exp_f32 v190,v190
v_cvt_pk_bf16_f32 v173,v186,v187
v_exp_f32 v191,v191
v_cvt_pk_bf16_f32 v174,v188,v189
v_cvt_pk_bf16_f32 v175,v190,v191
s_cmp_lg_u32 s53,0
s_cbranch_scc1 L_att0v0_%=
s_waitcnt lgkmcnt(0)
s_nop 7
s_branch L_att0x_%=
.p2align 5
L_att0v1_%=:
v_mfma_f32_16x16x32_bf16 v[232:235],a[128:131],v[112:115],v[44:47]
s_waitcnt vmcnt(8) lgkmcnt(0)
v_exp_f32 v200,v200
v_mfma_f32_16x16x32_bf16 v[232:235],a[132:135],v[116:119],v[232:235]
s_barrier
s_add_u32 m0,s49,s51
ds_read_b128 a[128:131],v1
v_mfma_f32_16x16x32_bf16 v[232:235],a[136:139],v[120:123],v[232:235]
s_mov_b32 s90,L_endseg_%=-L_pc_%=+128
v_exp_f32 v201,v201
v_mfma_f32_16x16x32_bf16 v[232:235],a[140:143],v[124:127],v[232:235]
s_lshr_b32 s45,s45,1
ds_read_b128 a[132:135],v1 offset:256
v_cvt_pk_bf16_f32 v200,v200,v201
v_mfma_f32_16x16x32_bf16 v[236:239],a[156:159],v[124:127],v[44:47]
buffer_load_dwordx4 v4,s[20:23],s47 offen nt lds
v_mfma_f32_16x16x32_bf16 v[236:239],a[152:155],v[120:123],v[236:239]
ds_read_b128 a[136:139],v1 offset:512
v_add_u32 v2,s48,v2
v_mfma_f32_16x16x32_bf16 v[236:239],a[148:151],v[116:119],v[236:239]
v_exp_f32 v202,v202
v_mfma_f32_16x16x32_bf16 v[236:239],a[144:147],v[112:115],v[236:239]
s_add_u32 s62,s50,32768
ds_read_b128 a[140:143],v1 offset:768
v_add_u32 v3,s48,v3
v_mfma_f32_16x16x32_bf16 v[248:251],a[160:163],v[112:115],v[44:47]
s_and_b32 s62,s62,131071
v_exp_f32 v203,v203
v_mfma_f32_16x16x32_bf16 v[248:251],a[164:167],v[116:119],v[248:251]
s_add_u32 s54,s54,16384
ds_read_b128 a[156:159],v1 offset:4864
v_cvt_pk_bf16_f32 v201,v202,v203
v_mfma_f32_16x16x32_bf16 v[248:251],a[168:171],v[120:123],v[248:251]
s_sub_u32 s48,s62,s50
v_exp_f32 v204,v204
v_mfma_f32_16x16x32_bf16 v[248:251],a[172:175],v[124:127],v[248:251]
s_sub_u32 s53,s53,1
v_exp_f32 v205,v205
v_mfma_f32_16x16x32_bf16 v[252:255],a[188:191],v[124:127],v[44:47]
s_and_b32 s46,s54,0xfffc0000
ds_read_b128 a[152:155],v1 offset:4608
v_cvt_pk_bf16_f32 v202,v204,v205
v_mfma_f32_16x16x32_bf16 v[252:255],a[184:187],v[120:123],v[252:255]
s_mov_b32 s50,s62
v_exp_f32 v206,v206
v_mfma_f32_16x16x32_bf16 v[252:255],a[180:183],v[116:119],v[252:255]
v_exp_f32 v207,v207
v_mfma_f32_16x16x32_bf16 v[252:255],a[176:179],v[112:115],v[252:255]
s_waitcnt lgkmcnt(2)
ds_read_b128 a[148:151],v1 offset:4352
v_cvt_pk_bf16_f32 v203,v206,v207
v_exp_f32 v216,v216
ds_read_b128 a[144:147],v1 offset:4096
buffer_load_dwordx4 v4,s[20:23],s47 offen offset:1024 nt lds
ds_read_b128 a[160:163],v1 offset:8192
v_exp_f32 v217,v217
ds_read_b128 a[164:167],v1 offset:8448
v_cvt_pk_bf16_f32 v204,v216,v217
v_exp_f32 v218,v218
ds_read_b128 a[168:171],v1 offset:8704
v_exp_f32 v219,v219
ds_read_b128 a[172:175],v1 offset:8960
v_cvt_pk_bf16_f32 v205,v218,v219
v_exp_f32 v220,v220
ds_read_b128 a[188:191],v1 offset:13056
v_exp_f32 v221,v221
ds_read_b128 a[184:187],v1 offset:12800
v_cvt_pk_bf16_f32 v206,v220,v221
v_exp_f32 v222,v222
ds_read_b128 a[180:183],v1 offset:12544
v_mfma_f32_16x16x32_bf16 v[136:139],a[128:131],v[64:67],v[32:35]
buffer_load_dwordx4 v5,s[20:23],s47 offen offset:2048 nt lds
v_mfma_f32_16x16x32_bf16 v[136:139],a[132:135],v[68:71],v[136:139]
ds_read_b128 a[176:179],v1 offset:12288
v_add_u32 v1,s48,v1
v_mfma_f32_16x16x32_bf16 v[136:139],a[136:139],v[72:75],v[136:139]
v_exp_f32 v223,v223
v_mfma_f32_16x16x32_bf16 v[136:139],a[140:143],v[76:79],v[136:139]
s_waitcnt lgkmcnt(11)
v_cvt_pk_bf16_f32 v207,v222,v223
v_mfma_f32_16x16x32_bf16 v[140:143],a[156:159],v[76:79],v[32:35]
s_waitcnt lgkmcnt(10)
v_exp_f32 v232,v232
v_mfma_f32_16x16x32_bf16 v[140:143],a[152:155],v[72:75],v[140:143]
s_waitcnt lgkmcnt(9)
v_exp_f32 v233,v233
v_mfma_f32_16x16x32_bf16 v[140:143],a[148:151],v[68:71],v[140:143]
s_waitcnt lgkmcnt(8)
v_exp_f32 v234,v234
v_mfma_f32_16x16x32_bf16 v[140:143],a[144:147],v[64:67],v[140:143]
s_waitcnt lgkmcnt(1)
v_exp_f32 v235,v235
v_mfma_f32_16x16x32_bf16 v[152:155],a[160:163],v[64:67],v[32:35]
v_exp_f32 v236,v236
v_mfma_f32_16x16x32_bf16 v[152:155],a[164:167],v[68:71],v[152:155]
)A"
R"A(v_exp_f32 v237,v237
v_mfma_f32_16x16x32_bf16 v[152:155],a[168:171],v[72:75],v[152:155]
v_exp_f32 v238,v238
v_mfma_f32_16x16x32_bf16 v[152:155],a[172:175],v[76:79],v[152:155]
v_exp_f32 v239,v239
v_mfma_f32_16x16x32_bf16 v[156:159],a[188:191],v[76:79],v[32:35]
v_exp_f32 v248,v248
v_mfma_f32_16x16x32_bf16 v[156:159],a[184:187],v[72:75],v[156:159]
buffer_load_dwordx4 v5,s[20:23],s47 offen offset:3072 nt lds
v_mfma_f32_16x16x32_bf16 v[156:159],a[180:183],v[68:71],v[156:159]
s_waitcnt lgkmcnt(0)
v_exp_f32 v249,v249
v_mfma_f32_16x16x32_bf16 v[156:159],a[176:179],v[64:67],v[156:159]
v_exp_f32 v250,v250
v_mfma_f32_16x16x32_bf16 a[64:67],a[192:195],v[200:203],a[64:67]
v_exp_f32 v251,v251
v_mfma_f32_16x16x32_bf16 a[64:67],a[196:199],v[204:207],a[64:67]
v_exp_f32 v252,v252
v_mfma_f32_16x16x32_bf16 a[68:71],a[204:207],v[204:207],a[68:71]
v_exp_f32 v253,v253
v_mfma_f32_16x16x32_bf16 a[68:71],a[200:203],v[200:203],a[68:71]
v_exp_f32 v254,v254
v_mfma_f32_16x16x32_bf16 a[72:75],a[208:211],v[200:203],a[72:75]
v_exp_f32 v255,v255
v_mfma_f32_16x16x32_bf16 a[72:75],a[212:215],v[204:207],a[72:75]
v_exp_f32 v136,v136
v_mfma_f32_16x16x32_bf16 a[76:79],a[220:223],v[204:207],a[76:79]
v_exp_f32 v137,v137
v_mfma_f32_16x16x32_bf16 a[76:79],a[216:219],v[200:203],a[76:79]
v_exp_f32 v138,v138
v_mfma_f32_16x16x32_bf16 a[80:83],a[224:227],v[200:203],a[80:83]
s_add_u32 m0,s49,s52
v_cvt_pk_bf16_f32 v232,v232,v233
v_cvt_pk_bf16_f32 v233,v234,v235
v_mfma_f32_16x16x32_bf16 a[80:83],a[228:231],v[204:207],a[80:83]
s_xor_b32 s49,s50,65536
v_cvt_pk_bf16_f32 v234,v236,v237
v_cvt_pk_bf16_f32 v235,v238,v239
v_mfma_f32_16x16x32_bf16 a[84:87],a[236:239],v[204:207],a[84:87]
v_cvt_pk_bf16_f32 v236,v248,v249
v_cvt_pk_bf16_f32 v237,v250,v251
v_mfma_f32_16x16x32_bf16 a[84:87],a[232:235],v[200:203],a[84:87]
buffer_load_dwordx4 v4,s[24:27],s47 offen nt lds
v_mfma_f32_16x16x32_bf16 a[88:91],a[240:243],v[200:203],a[88:91]
v_cvt_pk_bf16_f32 v238,v252,v253
v_cvt_pk_bf16_f32 v239,v254,v255
v_mfma_f32_16x16x32_bf16 a[88:91],a[244:247],v[204:207],a[88:91]
v_exp_f32 v139,v139
v_mfma_f32_16x16x32_bf16 a[92:95],a[252:255],v[204:207],a[92:95]
v_exp_f32 v140,v140
v_mfma_f32_16x16x32_bf16 a[92:95],a[248:251],v[200:203],a[92:95]
v_exp_f32 v141,v141
v_smfmac_f32_16x16x64_bf16 v[56:59],v[28:31],v[200:207],v27
v_exp_f32 v142,v142
v_exp_f32 v143,v143
v_exp_f32 v152,v152
v_exp_f32 v153,v153
v_exp_f32 v154,v154
v_exp_f32 v155,v155
v_exp_f32 v156,v156
v_exp_f32 v157,v157
buffer_load_dwordx4 v5,s[24:27],s47 offen offset:1024 nt lds
v_exp_f32 v158,v158
v_exp_f32 v159,v159
v_mfma_f32_16x16x32_bf16 a[96:99],a[192:195],v[232:235],a[96:99]
ds_read_b64_tr_b16 a[192:193],v2
v_mfma_f32_16x16x32_bf16 a[96:99],a[196:199],v[236:239],a[96:99]
ds_read_b64_tr_b16 a[194:195],v2 offset:4096
v_mfma_f32_16x16x32_bf16 a[100:103],a[204:207],v[236:239],a[100:103]
v_mfma_f32_16x16x32_bf16 a[100:103],a[200:203],v[232:235],a[100:103]
ds_read_b64_tr_b16 a[196:197],v2 offset:8192
v_mfma_f32_16x16x32_bf16 a[104:107],a[208:211],v[232:235],a[104:107]
v_mfma_f32_16x16x32_bf16 a[104:107],a[212:215],v[236:239],a[104:107]
v_mfma_f32_16x16x32_bf16 a[108:111],a[220:223],v[236:239],a[108:111]
buffer_load_dwordx4 v4,s[24:27],s47 offen offset:2048 nt lds
v_mfma_f32_16x16x32_bf16 a[108:111],a[216:219],v[232:235],a[108:111]
ds_read_b64_tr_b16 a[198:199],v2 offset:12288
v_mfma_f32_16x16x32_bf16 a[112:115],a[224:227],v[232:235],a[112:115]
v_mfma_f32_16x16x32_bf16 a[112:115],a[228:231],v[236:239],a[112:115]
v_mfma_f32_16x16x32_bf16 a[116:119],a[236:239],v[236:239],a[116:119]
ds_read_b64_tr_b16 a[204:205],v3 offset:8192
v_cvt_pk_bf16_f32 v136,v136,v137
v_mfma_f32_16x16x32_bf16 a[116:119],a[232:235],v[232:235],a[116:119]
ds_read_b64_tr_b16 a[206:207],v3 offset:12288
v_mfma_f32_16x16x32_bf16 a[120:123],a[240:243],v[232:235],a[120:123]
ds_read_b64_tr_b16 a[200:201],v3
v_mfma_f32_16x16x32_bf16 a[120:123],a[244:247],v[236:239],a[120:123]
ds_read_b64_tr_b16 a[202:203],v3 offset:4096
v_cvt_pk_bf16_f32 v137,v138,v139
v_mfma_f32_16x16x32_bf16 a[124:127],a[252:255],v[236:239],a[124:127]
ds_read_b64_tr_b16 a[208:209],v2 offset:256
v_mfma_f32_16x16x32_bf16 a[124:127],a[248:251],v[232:235],a[124:127]
ds_read_b64_tr_b16 a[210:211],v2 offset:4352
v_smfmac_f32_16x16x64_bf16 v[60:63],v[28:31],v[232:239],v27
ds_read_b64_tr_b16 a[212:213],v2 offset:8448
v_mfma_f32_16x16x32_bf16 v[200:203],a[128:131],v[96:99],v[40:43]
ds_read_b64_tr_b16 a[214:215],v2 offset:12544
v_cvt_pk_bf16_f32 v138,v140,v141
v_mfma_f32_16x16x32_bf16 v[200:203],a[132:135],v[100:103],v[200:203]
ds_read_b64_tr_b16 a[220:221],v3 offset:8448
v_mfma_f32_16x16x32_bf16 v[200:203],a[136:139],v[104:107],v[200:203]
ds_read_b64_tr_b16 a[222:223],v3 offset:12544
v_cvt_pk_bf16_f32 v139,v142,v143
v_mfma_f32_16x16x32_bf16 v[200:203],a[140:143],v[108:111],v[200:203]
buffer_load_dwordx4 v5,s[24:27],s47 offen offset:3072 nt lds
v_mfma_f32_16x16x32_bf16 v[204:207],a[156:159],v[108:111],v[40:43]
s_add_u32 s47,s47,16384
s_and_b32 s47,s47,0x3ffff
ds_read_b64_tr_b16 a[216:217],v3 offset:256
v_mfma_f32_16x16x32_bf16 v[204:207],a[152:155],v[104:107],v[204:207]
s_or_b32 s47,s47,s46
ds_read_b64_tr_b16 a[218:219],v3 offset:4352
v_cvt_pk_bf16_f32 v140,v152,v153
v_mfma_f32_16x16x32_bf16 v[204:207],a[148:151],v[100:103],v[204:207]
ds_read_b64_tr_b16 a[224:225],v2 offset:512
v_mfma_f32_16x16x32_bf16 v[204:207],a[144:147],v[96:99],v[204:207]
ds_read_b64_tr_b16 a[226:227],v2 offset:4608
v_mfma_f32_16x16x32_bf16 v[216:219],a[160:163],v[96:99],v[40:43]
ds_read_b64_tr_b16 a[228:229],v2 offset:8704
v_cvt_pk_bf16_f32 v141,v154,v155
v_mfma_f32_16x16x32_bf16 v[216:219],a[164:167],v[100:103],v[216:219]
ds_read_b64_tr_b16 a[230:231],v2 offset:12800
v_mfma_f32_16x16x32_bf16 v[216:219],a[168:171],v[104:107],v[216:219]
ds_read_b64_tr_b16 a[236:237],v3 offset:8704
v_mfma_f32_16x16x32_bf16 v[216:219],a[172:175],v[108:111],v[216:219]
ds_read_b64_tr_b16 a[238:239],v3 offset:12800
v_cvt_pk_bf16_f32 v142,v156,v157
v_mfma_f32_16x16x32_bf16 v[220:223],a[188:191],v[108:111],v[40:43]
ds_read_b64_tr_b16 a[232:233],v3 offset:512
v_mfma_f32_16x16x32_bf16 v[220:223],a[184:187],v[104:107],v[220:223]
ds_read_b64_tr_b16 a[234:235],v3 offset:4608
v_mfma_f32_16x16x32_bf16 v[220:223],a[180:183],v[100:103],v[220:223]
ds_read_b64_tr_b16 a[240:241],v2 offset:768
v_cvt_pk_bf16_f32 v143,v158,v159
v_mfma_f32_16x16x32_bf16 v[220:223],a[176:179],v[96:99],v[220:223]
s_waitcnt lgkmcnt(7)
buffer_load_dword v26,v24,s[88:91],s93 offen
v_mfma_f32_16x16x32_bf16 a[0:3],a[192:195],v[136:139],a[0:3]
ds_read_b64_tr_b16 a[242:243],v2 offset:4864
v_mfma_f32_16x16x32_bf16 a[0:3],a[196:199],v[140:143],a[0:3]
ds_read_b64_tr_b16 a[244:245],v2 offset:8960
v_mfma_f32_16x16x32_bf16 a[4:7],a[204:207],v[140:143],a[4:7]
ds_read_b64_tr_b16 a[246:247],v2 offset:13056
v_mfma_f32_16x16x32_bf16 a[4:7],a[200:203],v[136:139],a[4:7]
ds_read_b64_tr_b16 a[252:253],v3 offset:8960
v_mfma_f32_16x16x32_bf16 a[8:11],a[208:211],v[136:139],a[8:11]
ds_read_b64_tr_b16 a[254:255],v3 offset:13056
v_mfma_f32_16x16x32_bf16 a[8:11],a[212:215],v[140:143],a[8:11]
ds_read_b64_tr_b16 a[248:249],v3 offset:768
v_mfma_f32_16x16x32_bf16 a[12:15],a[220:223],v[140:143],a[12:15]
ds_read_b64_tr_b16 a[250:251],v3 offset:4864
v_mfma_f32_16x16x32_bf16 a[12:15],a[216:219],v[136:139],a[12:15]
s_bitcmp1_b32 s45,0
s_cbranch_scc0 L_t31v10_%=
s_mov_b32 m0,s80
s_add_u32 s80,s80,4096
buffer_load_dwordx4 v25,s[20:23],s83 offen nt lds
s_add_u32 s83,s83,4096
L_t31v10_%=:
v_mfma_f32_16x16x32_bf16 a[16:19],a[224:227],v[136:139],a[16:19]
s_waitcnt lgkmcnt(6)
v_mfma_f32_16x16x32_bf16 a[16:19],a[228:231],v[140:143],a[16:19]
v_mfma_f32_16x16x32_bf16 a[20:23],a[236:239],v[140:143],a[20:23]
v_mfma_f32_16x16x32_bf16 a[20:23],a[232:235],v[136:139],a[20:23]
v_mfma_f32_16x16x32_bf16 a[24:27],a[240:243],v[136:139],a[24:27]
s_waitcnt lgkmcnt(2)
v_mfma_f32_16x16x32_bf16 a[24:27],a[244:247],v[140:143],a[24:27]
v_mfma_f32_16x16x32_bf16 a[28:31],a[252:255],v[140:143],a[28:31]
s_waitcnt lgkmcnt(0)
v_mfma_f32_16x16x32_bf16 a[28:31],a[248:251],v[136:139],a[28:31]
s_cmp_lg_u32 s53,0
v_smfmac_f32_16x16x64_bf16 v[48:51],v[28:31],v[136:143],v27
s_cbranch_scc1 L_att0v1_%=
s_waitcnt lgkmcnt(0)
s_nop 7
s_branch L_att0x_%=
.p2align 5
L_att0v2_%=:
v_mfma_f32_16x16x32_bf16 v[232:235],a[128:131],v[112:115],v[44:47]
s_waitcnt vmcnt(8) lgkmcnt(0)
v_mfma_f32_16x16x32_bf16 v[232:235],a[132:135],v[116:119],v[232:235]
s_barrier
s_add_u32 m0,s49,s51
ds_read_b128 a[128:131],v1
v_mfma_f32_16x16x32_bf16 v[232:235],a[136:139],v[120:123],v[232:235]
s_mov_b32 s90,L_endseg_%=-L_pc_%=+128
v_mfma_f32_16x16x32_bf16 v[232:235],a[140:143],v[124:127],v[232:235]
s_lshr_b32 s45,s45,1
ds_read_b128 a[132:135],v1 offset:256
v_mfma_f32_16x16x32_bf16 v[236:239],a[156:159],v[124:127],v[44:47]
buffer_load_dwordx4 v4,s[20:23],s47 offen nt lds
v_mfma_f32_16x16x32_bf16 v[236:239],a[152:155],v[120:123],v[236:239]
ds_read_b128 a[136:139],v1 offset:512
v_add_u32 v2,s48,v2
v_mfma_f32_16x16x32_bf16 v[236:239],a[148:151],v[116:119],v[236:239]
v_mfma_f32_16x16x32_bf16 v[236:239],a[144:147],v[112:115],v[236:239]
s_add_u32 s62,s50,32768
ds_read_b128 a[140:143],v1 offset:768
v_add_u32 v3,s48,v3
v_mfma_f32_16x16x32_bf16 v[248:251],a[160:163],v[112:115],v[44:47]
s_and_b32 s62,s62,131071
v_mfma_f32_16x16x32_bf16 v[248:251],a[164:167],v[116:119],v[248:251]
s_add_u32 s54,s54,16384
ds_read_b128 a[156:159],v1 offset:4864
v_mfma_f32_16x16x32_bf16 v[248:251],a[168:171],v[120:123],v[248:251]
s_sub_u32 s48,s62,s50
v_mfma_f32_16x16x32_bf16 v[248:251],a[172:175],v[124:127],v[248:251]
s_sub_u32 s53,s53,1
v_mfma_f32_16x16x32_bf16 v[252:255],a[188:191],v[124:127],v[44:47]
s_and_b32 s46,s54,0xfffc0000
ds_read_b128 a[152:155],v1 offset:4608
v_mfma_f32_16x16x32_bf16 v[252:255],a[184:187],v[120:123],v[252:255]
s_mov_b32 s50,s62
v_mfma_f32_16x16x32_bf16 v[252:255],a[180:183],v[116:119],v[252:255]
v_mfma_f32_16x16x32_bf16 v[252:255],a[176:179],v[112:115],v[252:255]
ds_read_b128 a[148:151],v1 offset:4352
v_mfma_f32_16x16x32_bf16 a[32:35],a[192:195],v[168:171],a[32:35]
v_mfma_f32_16x16x32_bf16 a[32:35],a[196:199],v[172:175],a[32:35]
ds_read_b128 a[144:147],v1 offset:4096
v_mfma_f32_16x16x32_bf16 a[36:39],a[204:207],v[172:175],a[36:39]
buffer_load_dwordx4 v4,s[20:23],s47 offen offset:1024 nt lds
v_mfma_f32_16x16x32_bf16 a[36:39],a[200:203],v[168:171],a[36:39]
ds_read_b128 a[160:163],v1 offset:8192
v_mfma_f32_16x16x32_bf16 a[40:43],a[208:211],v[168:171],a[40:43]
v_mfma_f32_16x16x32_bf16 a[40:43],a[212:215],v[172:175],a[40:43]
ds_read_b128 a[164:167],v1 offset:8448
v_mfma_f32_16x16x32_bf16 a[44:47],a[220:223],v[172:175],a[44:47]
v_mfma_f32_16x16x32_bf16 a[44:47],a[216:219],v[168:171],a[44:47]
ds_read_b128 a[168:171],v1 offset:8704
v_mfma_f32_16x16x32_bf16 a[48:51],a[224:227],v[168:171],a[48:51]
v_mfma_f32_16x16x32_bf16 a[48:51],a[228:231],v[172:175],a[48:51]
ds_read_b128 a[172:175],v1 offset:8960
v_mfma_f32_16x16x32_bf16 a[52:55],a[236:239],v[172:175],a[52:55]
v_mfma_f32_16x16x32_bf16 a[52:55],a[232:235],v[168:171],a[52:55]
ds_read_b128 a[188:191],v1 offset:13056
v_mfma_f32_16x16x32_bf16 a[56:59],a[240:243],v[168:171],a[56:59]
v_mfma_f32_16x16x32_bf16 a[56:59],a[244:247],v[172:175],a[56:59]
ds_read_b128 a[184:187],v1 offset:12800
v_mfma_f32_16x16x32_bf16 a[60:63],a[252:255],v[172:175],a[60:63]
v_mfma_f32_16x16x32_bf16 a[60:63],a[248:251],v[168:171],a[60:63]
ds_read_b128 a[180:183],v1 offset:12544
v_smfmac_f32_16x16x64_bf16 v[52:55],v[28:31],v[168:175],v27
s_waitcnt lgkmcnt(4)
v_mfma_f32_16x16x32_bf16 v[136:139],a[128:131],v[64:67],v[32:35]
buffer_load_dwordx4 v5,s[20:23],s47 offen offset:2048 nt lds
v_mfma_f32_16x16x32_bf16 v[136:139],a[132:135],v[68:71],v[136:139]
ds_read_b128 a[176:179],v1 offset:12288
v_add_u32 v1,s48,v1
v_mfma_f32_16x16x32_bf16 v[136:139],a[136:139],v[72:75],v[136:139]
v_mfma_f32_16x16x32_bf16 v[136:139],a[140:143],v[76:79],v[136:139]
v_mfma_f32_16x16x32_bf16 v[140:143],a[156:159],v[76:79],v[32:35]
v_exp_f32 v232,v232
v_mfma_f32_16x16x32_bf16 v[140:143],a[152:155],v[72:75],v[140:143]
v_exp_f32 v233,v233
v_mfma_f32_16x16x32_bf16 v[140:143],a[148:151],v[68:71],v[140:143]
)A"
R"A(v_exp_f32 v234,v234
v_mfma_f32_16x16x32_bf16 v[140:143],a[144:147],v[64:67],v[140:143]
v_exp_f32 v235,v235
v_mfma_f32_16x16x32_bf16 v[152:155],a[160:163],v[64:67],v[32:35]
v_exp_f32 v236,v236
v_mfma_f32_16x16x32_bf16 v[152:155],a[164:167],v[68:71],v[152:155]
v_exp_f32 v237,v237
v_mfma_f32_16x16x32_bf16 v[152:155],a[168:171],v[72:75],v[152:155]
s_waitcnt lgkmcnt(0)
v_exp_f32 v238,v238
v_mfma_f32_16x16x32_bf16 v[152:155],a[172:175],v[76:79],v[152:155]
v_exp_f32 v239,v239
v_mfma_f32_16x16x32_bf16 v[156:159],a[188:191],v[76:79],v[32:35]
v_exp_f32 v248,v248
v_mfma_f32_16x16x32_bf16 v[156:159],a[184:187],v[72:75],v[156:159]
buffer_load_dwordx4 v5,s[20:23],s47 offen offset:3072 nt lds
v_mfma_f32_16x16x32_bf16 v[156:159],a[180:183],v[68:71],v[156:159]
v_exp_f32 v249,v249
v_mfma_f32_16x16x32_bf16 v[156:159],a[176:179],v[64:67],v[156:159]
v_exp_f32 v250,v250
v_exp_f32 v251,v251
v_exp_f32 v252,v252
v_exp_f32 v253,v253
v_exp_f32 v254,v254
v_exp_f32 v255,v255
v_exp_f32 v136,v136
v_exp_f32 v137,v137
v_exp_f32 v138,v138
s_add_u32 m0,s49,s52
v_cvt_pk_bf16_f32 v232,v232,v233
v_cvt_pk_bf16_f32 v233,v234,v235
s_xor_b32 s49,s50,65536
v_cvt_pk_bf16_f32 v234,v236,v237
v_cvt_pk_bf16_f32 v235,v238,v239
v_cvt_pk_bf16_f32 v236,v248,v249
v_cvt_pk_bf16_f32 v237,v250,v251
buffer_load_dwordx4 v4,s[24:27],s47 offen nt lds
v_cvt_pk_bf16_f32 v238,v252,v253
v_cvt_pk_bf16_f32 v239,v254,v255
v_exp_f32 v139,v139
v_exp_f32 v140,v140
v_exp_f32 v141,v141
v_mfma_f32_16x16x32_bf16 v[168:171],a[128:131],v[80:83],v[36:39]
v_exp_f32 v142,v142
v_mfma_f32_16x16x32_bf16 v[168:171],a[132:135],v[84:87],v[168:171]
v_exp_f32 v143,v143
v_mfma_f32_16x16x32_bf16 v[168:171],a[136:139],v[88:91],v[168:171]
v_exp_f32 v152,v152
v_mfma_f32_16x16x32_bf16 v[168:171],a[140:143],v[92:95],v[168:171]
v_exp_f32 v153,v153
v_mfma_f32_16x16x32_bf16 v[172:175],a[156:159],v[92:95],v[36:39]
v_exp_f32 v154,v154
v_mfma_f32_16x16x32_bf16 v[172:175],a[152:155],v[88:91],v[172:175]
v_exp_f32 v155,v155
v_mfma_f32_16x16x32_bf16 v[172:175],a[148:151],v[84:87],v[172:175]
v_exp_f32 v156,v156
v_mfma_f32_16x16x32_bf16 v[172:175],a[144:147],v[80:83],v[172:175]
v_exp_f32 v157,v157
v_mfma_f32_16x16x32_bf16 v[184:187],a[160:163],v[80:83],v[36:39]
buffer_load_dwordx4 v5,s[24:27],s47 offen offset:1024 nt lds
v_mfma_f32_16x16x32_bf16 v[184:187],a[164:167],v[84:87],v[184:187]
v_exp_f32 v158,v158
v_mfma_f32_16x16x32_bf16 v[184:187],a[168:171],v[88:91],v[184:187]
v_exp_f32 v159,v159
v_mfma_f32_16x16x32_bf16 v[184:187],a[172:175],v[92:95],v[184:187]
v_exp_f32 v168,v168
v_mfma_f32_16x16x32_bf16 v[188:191],a[188:191],v[92:95],v[36:39]
v_exp_f32 v169,v169
v_mfma_f32_16x16x32_bf16 v[188:191],a[184:187],v[88:91],v[188:191]
v_exp_f32 v170,v170
v_mfma_f32_16x16x32_bf16 v[188:191],a[180:183],v[84:87],v[188:191]
v_exp_f32 v171,v171
v_mfma_f32_16x16x32_bf16 v[188:191],a[176:179],v[80:83],v[188:191]
v_exp_f32 v172,v172
v_mfma_f32_16x16x32_bf16 a[96:99],a[192:195],v[232:235],a[96:99]
ds_read_b64_tr_b16 a[192:193],v2
v_cvt_pk_bf16_f32 v168,v168,v169
v_mfma_f32_16x16x32_bf16 a[96:99],a[196:199],v[236:239],a[96:99]
ds_read_b64_tr_b16 a[194:195],v2 offset:4096
v_cvt_pk_bf16_f32 v169,v170,v171
v_mfma_f32_16x16x32_bf16 a[100:103],a[204:207],v[236:239],a[100:103]
v_exp_f32 v173,v173
v_mfma_f32_16x16x32_bf16 a[100:103],a[200:203],v[232:235],a[100:103]
ds_read_b64_tr_b16 a[196:197],v2 offset:8192
v_cvt_pk_bf16_f32 v170,v172,v173
v_mfma_f32_16x16x32_bf16 a[104:107],a[208:211],v[232:235],a[104:107]
v_exp_f32 v174,v174
v_mfma_f32_16x16x32_bf16 a[104:107],a[212:215],v[236:239],a[104:107]
v_exp_f32 v175,v175
v_mfma_f32_16x16x32_bf16 a[108:111],a[220:223],v[236:239],a[108:111]
buffer_load_dwordx4 v4,s[24:27],s47 offen offset:2048 nt lds
v_mfma_f32_16x16x32_bf16 a[108:111],a[216:219],v[232:235],a[108:111]
ds_read_b64_tr_b16 a[198:199],v2 offset:12288
v_cvt_pk_bf16_f32 v171,v174,v175
v_mfma_f32_16x16x32_bf16 a[112:115],a[224:227],v[232:235],a[112:115]
v_exp_f32 v184,v184
v_mfma_f32_16x16x32_bf16 a[112:115],a[228:231],v[236:239],a[112:115]
v_exp_f32 v185,v185
v_mfma_f32_16x16x32_bf16 a[116:119],a[236:239],v[236:239],a[116:119]
ds_read_b64_tr_b16 a[204:205],v3 offset:8192
v_cvt_pk_bf16_f32 v136,v136,v137
v_mfma_f32_16x16x32_bf16 a[116:119],a[232:235],v[232:235],a[116:119]
ds_read_b64_tr_b16 a[206:207],v3 offset:12288
v_cvt_pk_bf16_f32 v172,v184,v185
v_mfma_f32_16x16x32_bf16 a[120:123],a[240:243],v[232:235],a[120:123]
ds_read_b64_tr_b16 a[200:201],v3
v_mfma_f32_16x16x32_bf16 a[120:123],a[244:247],v[236:239],a[120:123]
ds_read_b64_tr_b16 a[202:203],v3 offset:4096
v_cvt_pk_bf16_f32 v137,v138,v139
v_mfma_f32_16x16x32_bf16 a[124:127],a[252:255],v[236:239],a[124:127]
ds_read_b64_tr_b16 a[208:209],v2 offset:256
v_mfma_f32_16x16x32_bf16 a[124:127],a[248:251],v[232:235],a[124:127]
ds_read_b64_tr_b16 a[210:211],v2 offset:4352
v_smfmac_f32_16x16x64_bf16 v[60:63],v[28:31],v[232:239],v27
s_waitcnt lgkmcnt(6)
ds_read_b64_tr_b16 a[212:213],v2 offset:8448
ds_read_b64_tr_b16 a[214:215],v2 offset:12544
v_cvt_pk_bf16_f32 v138,v140,v141
ds_read_b64_tr_b16 a[220:221],v3 offset:8448
ds_read_b64_tr_b16 a[222:223],v3 offset:12544
v_cvt_pk_bf16_f32 v139,v142,v143
buffer_load_dwordx4 v5,s[24:27],s47 offen offset:3072 nt lds
s_add_u32 s47,s47,16384
s_and_b32 s47,s47,0x3ffff
ds_read_b64_tr_b16 a[216:217],v3 offset:256
s_or_b32 s47,s47,s46
ds_read_b64_tr_b16 a[218:219],v3 offset:4352
v_cvt_pk_bf16_f32 v140,v152,v153
ds_read_b64_tr_b16 a[224:225],v2 offset:512
ds_read_b64_tr_b16 a[226:227],v2 offset:4608
ds_read_b64_tr_b16 a[228:229],v2 offset:8704
v_cvt_pk_bf16_f32 v141,v154,v155
ds_read_b64_tr_b16 a[230:231],v2 offset:12800
ds_read_b64_tr_b16 a[236:237],v3 offset:8704
ds_read_b64_tr_b16 a[238:239],v3 offset:12800
v_cvt_pk_bf16_f32 v142,v156,v157
ds_read_b64_tr_b16 a[232:233],v3 offset:512
ds_read_b64_tr_b16 a[234:235],v3 offset:4608
ds_read_b64_tr_b16 a[240:241],v2 offset:768
v_cvt_pk_bf16_f32 v143,v158,v159
buffer_load_dword v26,v24,s[88:91],s93 offen
v_mfma_f32_16x16x32_bf16 a[0:3],a[192:195],v[136:139],a[0:3]
ds_read_b64_tr_b16 a[242:243],v2 offset:4864
v_mfma_f32_16x16x32_bf16 a[0:3],a[196:199],v[140:143],a[0:3]
s_waitcnt lgkmcnt(15)
ds_read_b64_tr_b16 a[244:245],v2 offset:8960
v_mfma_f32_16x16x32_bf16 a[4:7],a[204:207],v[140:143],a[4:7]
ds_read_b64_tr_b16 a[246:247],v2 offset:13056
v_mfma_f32_16x16x32_bf16 a[4:7],a[200:203],v[136:139],a[4:7]
ds_read_b64_tr_b16 a[252:253],v3 offset:8960
v_mfma_f32_16x16x32_bf16 a[8:11],a[208:211],v[136:139],a[8:11]
s_waitcnt lgkmcnt(15)
ds_read_b64_tr_b16 a[254:255],v3 offset:13056
v_mfma_f32_16x16x32_bf16 a[8:11],a[212:215],v[140:143],a[8:11]
ds_read_b64_tr_b16 a[248:249],v3 offset:768
v_mfma_f32_16x16x32_bf16 a[12:15],a[220:223],v[140:143],a[12:15]
s_waitcnt lgkmcnt(15)
ds_read_b64_tr_b16 a[250:251],v3 offset:4864
v_mfma_f32_16x16x32_bf16 a[12:15],a[216:219],v[136:139],a[12:15]
s_bitcmp1_b32 s45,0
s_cbranch_scc0 L_t31v20_%=
s_mov_b32 m0,s80
s_add_u32 s80,s80,4096
buffer_load_dwordx4 v25,s[20:23],s83 offen nt lds
s_add_u32 s83,s83,4096
L_t31v20_%=:
s_waitcnt lgkmcnt(7)
v_mfma_f32_16x16x32_bf16 a[16:19],a[224:227],v[136:139],a[16:19]
v_exp_f32 v186,v186
v_mfma_f32_16x16x32_bf16 a[16:19],a[228:231],v[140:143],a[16:19]
v_exp_f32 v187,v187
v_mfma_f32_16x16x32_bf16 a[20:23],a[236:239],v[140:143],a[20:23]
v_exp_f32 v188,v188
v_mfma_f32_16x16x32_bf16 a[20:23],a[232:235],v[136:139],a[20:23]
s_waitcnt lgkmcnt(3)
v_exp_f32 v189,v189
v_mfma_f32_16x16x32_bf16 a[24:27],a[240:243],v[136:139],a[24:27]
v_exp_f32 v190,v190
v_mfma_f32_16x16x32_bf16 a[24:27],a[244:247],v[140:143],a[24:27]
s_waitcnt lgkmcnt(1)
v_cvt_pk_bf16_f32 v173,v186,v187
v_mfma_f32_16x16x32_bf16 a[28:31],a[252:255],v[140:143],a[28:31]
s_waitcnt lgkmcnt(0)
v_exp_f32 v191,v191
v_mfma_f32_16x16x32_bf16 a[28:31],a[248:251],v[136:139],a[28:31]
v_cvt_pk_bf16_f32 v174,v188,v189
v_cvt_pk_bf16_f32 v175,v190,v191
s_cmp_lg_u32 s53,0
v_smfmac_f32_16x16x64_bf16 v[48:51],v[28:31],v[136:143],v27
s_cbranch_scc1 L_att0v2_%=
s_waitcnt lgkmcnt(0)
s_nop 7
s_branch L_att0x_%=
.p2align 5
L_att0_%=:
v_mfma_f32_16x16x32_bf16 v[232:235],a[128:131],v[112:115],v[44:47]
s_waitcnt vmcnt(8) lgkmcnt(0)
v_exp_f32 v200,v200
v_mfma_f32_16x16x32_bf16 v[232:235],a[132:135],v[116:119],v[232:235]
s_barrier
s_add_u32 m0,s49,s51
ds_read_b128 a[128:131],v1
v_mfma_f32_16x16x32_bf16 v[232:235],a[136:139],v[120:123],v[232:235]
s_mov_b32 s90,L_endseg_%=-L_pc_%=+128
v_exp_f32 v201,v201
v_mfma_f32_16x16x32_bf16 v[232:235],a[140:143],v[124:127],v[232:235]
s_lshr_b32 s45,s45,1
ds_read_b128 a[132:135],v1 offset:256
v_cvt_pk_bf16_f32 v200,v200,v201
v_mfma_f32_16x16x32_bf16 v[236:239],a[156:159],v[124:127],v[44:47]
s_sub_u32 s94,s94,3
buffer_load_dwordx4 v4,s[20:23],s47 offen nt lds
v_mfma_f32_16x16x32_bf16 v[236:239],a[152:155],v[120:123],v[236:239]
s_lshr_b32 s60,s94,2
ds_read_b128 a[136:139],v1 offset:512
v_add_u32 v2,s48,v2
v_mfma_f32_16x16x32_bf16 v[236:239],a[148:151],v[116:119],v[236:239]
s_max_i32 s60,s60,128
v_exp_f32 v202,v202
v_mfma_f32_16x16x32_bf16 v[236:239],a[144:147],v[112:115],v[236:239]
s_add_u32 s62,s50,32768
ds_read_b128 a[140:143],v1 offset:768
v_add_u32 v3,s48,v3
v_mfma_f32_16x16x32_bf16 v[248:251],a[160:163],v[112:115],v[44:47]
s_and_b32 s62,s62,131071
v_exp_f32 v203,v203
v_mfma_f32_16x16x32_bf16 v[248:251],a[164:167],v[116:119],v[248:251]
s_add_u32 s54,s54,16384
ds_read_b128 a[156:159],v1 offset:4864
v_cvt_pk_bf16_f32 v201,v202,v203
v_mfma_f32_16x16x32_bf16 v[248:251],a[168:171],v[120:123],v[248:251]
s_sub_u32 s48,s62,s50
v_exp_f32 v204,v204
v_mfma_f32_16x16x32_bf16 v[248:251],a[172:175],v[124:127],v[248:251]
s_sub_u32 s53,s53,1
v_exp_f32 v205,v205
v_mfma_f32_16x16x32_bf16 v[252:255],a[188:191],v[124:127],v[44:47]
s_and_b32 s46,s54,0xfffc0000
ds_read_b128 a[152:155],v1 offset:4608
v_cvt_pk_bf16_f32 v202,v204,v205
v_mfma_f32_16x16x32_bf16 v[252:255],a[184:187],v[120:123],v[252:255]
s_mov_b32 s50,s62
v_exp_f32 v206,v206
v_mfma_f32_16x16x32_bf16 v[252:255],a[180:183],v[116:119],v[252:255]
v_exp_f32 v207,v207
v_mfma_f32_16x16x32_bf16 v[252:255],a[176:179],v[112:115],v[252:255]
ds_read_b128 a[148:151],v1 offset:4352
v_cvt_pk_bf16_f32 v203,v206,v207
v_mfma_f32_16x16x32_bf16 a[32:35],a[192:195],v[168:171],a[32:35]
v_exp_f32 v216,v216
v_mfma_f32_16x16x32_bf16 a[32:35],a[196:199],v[172:175],a[32:35]
ds_read_b128 a[144:147],v1 offset:4096
v_mfma_f32_16x16x32_bf16 a[36:39],a[204:207],v[172:175],a[36:39]
buffer_load_dwordx4 v4,s[20:23],s47 offen offset:1024 nt lds
v_mfma_f32_16x16x32_bf16 a[36:39],a[200:203],v[168:171],a[36:39]
ds_read_b128 a[160:163],v1 offset:8192
v_mfma_f32_16x16x32_bf16 a[40:43],a[208:211],v[168:171],a[40:43]
v_exp_f32 v217,v217
v_mfma_f32_16x16x32_bf16 a[40:43],a[212:215],v[172:175],a[40:43]
ds_read_b128 a[164:167],v1 offset:8448
v_cvt_pk_bf16_f32 v204,v216,v217
v_mfma_f32_16x16x32_bf16 a[44:47],a[220:223],v[172:175],a[44:47]
v_exp_f32 v218,v218
v_mfma_f32_16x16x32_bf16 a[44:47],a[216:219],v[168:171],a[44:47]
ds_read_b128 a[168:171],v1 offset:8704
v_mfma_f32_16x16x32_bf16 a[48:51],a[224:227],v[168:171],a[48:51]
v_exp_f32 v219,v219
v_mfma_f32_16x16x32_bf16 a[48:51],a[228:231],v[172:175],a[48:51]
ds_read_b128 a[172:175],v1 offset:8960
v_cvt_pk_bf16_f32 v205,v218,v219
v_mfma_f32_16x16x32_bf16 a[52:55],a[236:239],v[172:175],a[52:55]
v_exp_f32 v220,v220
v_mfma_f32_16x16x32_bf16 a[52:55],a[232:235],v[168:171],a[52:55]
ds_read_b128 a[188:191],v1 offset:13056
v_mfma_f32_16x16x32_bf16 a[56:59],a[240:243],v[168:171],a[56:59]
v_exp_f32 v221,v221
v_mfma_f32_16x16x32_bf16 a[56:59],a[244:247],v[172:175],a[56:59]
ds_read_b128 a[184:187],v1 offset:12800
v_cvt_pk_bf16_f32 v206,v220,v221
v_mfma_f32_16x16x32_bf16 a[60:63],a[252:255],v[172:175],a[60:63]
v_exp_f32 v222,v222
v_mfma_f32_16x16x32_bf16 a[60:63],a[248:251],v[168:171],a[60:63]
ds_read_b128 a[180:183],v1 offset:12544
v_smfmac_f32_16x16x64_bf16 v[52:55],v[28:31],v[168:175],v27
s_waitcnt lgkmcnt(4)
v_mfma_f32_16x16x32_bf16 v[136:139],a[128:131],v[64:67],v[32:35]
buffer_load_dwordx4 v5,s[20:23],s47 offen offset:2048 nt lds
v_mfma_f32_16x16x32_bf16 v[136:139],a[132:135],v[68:71],v[136:139]
ds_read_b128 a[176:179],v1 offset:12288
)A"
R"A(v_add_u32 v1,s48,v1
v_mfma_f32_16x16x32_bf16 v[136:139],a[136:139],v[72:75],v[136:139]
v_exp_f32 v223,v223
v_mfma_f32_16x16x32_bf16 v[136:139],a[140:143],v[76:79],v[136:139]
v_cvt_pk_bf16_f32 v207,v222,v223
v_mfma_f32_16x16x32_bf16 v[140:143],a[156:159],v[76:79],v[32:35]
v_exp_f32 v232,v232
v_mfma_f32_16x16x32_bf16 v[140:143],a[152:155],v[72:75],v[140:143]
v_exp_f32 v233,v233
v_mfma_f32_16x16x32_bf16 v[140:143],a[148:151],v[68:71],v[140:143]
v_exp_f32 v234,v234
v_mfma_f32_16x16x32_bf16 v[140:143],a[144:147],v[64:67],v[140:143]
v_exp_f32 v235,v235
v_mfma_f32_16x16x32_bf16 v[152:155],a[160:163],v[64:67],v[32:35]
v_exp_f32 v236,v236
v_mfma_f32_16x16x32_bf16 v[152:155],a[164:167],v[68:71],v[152:155]
v_exp_f32 v237,v237
v_mfma_f32_16x16x32_bf16 v[152:155],a[168:171],v[72:75],v[152:155]
s_waitcnt lgkmcnt(0)
v_exp_f32 v238,v238
v_mfma_f32_16x16x32_bf16 v[152:155],a[172:175],v[76:79],v[152:155]
v_exp_f32 v239,v239
v_mfma_f32_16x16x32_bf16 v[156:159],a[188:191],v[76:79],v[32:35]
v_exp_f32 v248,v248
v_mfma_f32_16x16x32_bf16 v[156:159],a[184:187],v[72:75],v[156:159]
buffer_load_dwordx4 v5,s[20:23],s47 offen offset:3072 nt lds
v_mfma_f32_16x16x32_bf16 v[156:159],a[180:183],v[68:71],v[156:159]
v_exp_f32 v249,v249
v_mfma_f32_16x16x32_bf16 v[156:159],a[176:179],v[64:67],v[156:159]
v_exp_f32 v250,v250
v_mfma_f32_16x16x32_bf16 a[64:67],a[192:195],v[200:203],a[64:67]
v_exp_f32 v251,v251
v_mfma_f32_16x16x32_bf16 a[64:67],a[196:199],v[204:207],a[64:67]
v_exp_f32 v252,v252
v_mfma_f32_16x16x32_bf16 a[68:71],a[204:207],v[204:207],a[68:71]
v_exp_f32 v253,v253
v_mfma_f32_16x16x32_bf16 a[68:71],a[200:203],v[200:203],a[68:71]
v_exp_f32 v254,v254
v_mfma_f32_16x16x32_bf16 a[72:75],a[208:211],v[200:203],a[72:75]
v_exp_f32 v255,v255
v_mfma_f32_16x16x32_bf16 a[72:75],a[212:215],v[204:207],a[72:75]
v_exp_f32 v136,v136
v_mfma_f32_16x16x32_bf16 a[76:79],a[220:223],v[204:207],a[76:79]
v_exp_f32 v137,v137
v_mfma_f32_16x16x32_bf16 a[76:79],a[216:219],v[200:203],a[76:79]
v_exp_f32 v138,v138
v_mfma_f32_16x16x32_bf16 a[80:83],a[224:227],v[200:203],a[80:83]
s_add_u32 m0,s49,s52
v_cvt_pk_bf16_f32 v232,v232,v233
v_cvt_pk_bf16_f32 v233,v234,v235
v_mfma_f32_16x16x32_bf16 a[80:83],a[228:231],v[204:207],a[80:83]
s_xor_b32 s49,s50,65536
v_cvt_pk_bf16_f32 v234,v236,v237
v_cvt_pk_bf16_f32 v235,v238,v239
v_mfma_f32_16x16x32_bf16 a[84:87],a[236:239],v[204:207],a[84:87]
v_cvt_pk_bf16_f32 v236,v248,v249
v_cvt_pk_bf16_f32 v237,v250,v251
v_mfma_f32_16x16x32_bf16 a[84:87],a[232:235],v[200:203],a[84:87]
buffer_load_dwordx4 v4,s[24:27],s47 offen nt lds
v_mfma_f32_16x16x32_bf16 a[88:91],a[240:243],v[200:203],a[88:91]
v_cvt_pk_bf16_f32 v238,v252,v253
v_cvt_pk_bf16_f32 v239,v254,v255
v_mfma_f32_16x16x32_bf16 a[88:91],a[244:247],v[204:207],a[88:91]
v_exp_f32 v139,v139
v_mfma_f32_16x16x32_bf16 a[92:95],a[252:255],v[204:207],a[92:95]
v_exp_f32 v140,v140
v_mfma_f32_16x16x32_bf16 a[92:95],a[248:251],v[200:203],a[92:95]
v_exp_f32 v141,v141
v_smfmac_f32_16x16x64_bf16 v[56:59],v[28:31],v[200:207],v27
v_mfma_f32_16x16x32_bf16 v[168:171],a[128:131],v[80:83],v[36:39]
v_exp_f32 v142,v142
v_mfma_f32_16x16x32_bf16 v[168:171],a[132:135],v[84:87],v[168:171]
v_exp_f32 v143,v143
v_mfma_f32_16x16x32_bf16 v[168:171],a[136:139],v[88:91],v[168:171]
v_exp_f32 v152,v152
v_mfma_f32_16x16x32_bf16 v[168:171],a[140:143],v[92:95],v[168:171]
v_exp_f32 v153,v153
v_mfma_f32_16x16x32_bf16 v[172:175],a[156:159],v[92:95],v[36:39]
v_exp_f32 v154,v154
v_mfma_f32_16x16x32_bf16 v[172:175],a[152:155],v[88:91],v[172:175]
v_exp_f32 v155,v155
v_mfma_f32_16x16x32_bf16 v[172:175],a[148:151],v[84:87],v[172:175]
v_exp_f32 v156,v156
v_mfma_f32_16x16x32_bf16 v[172:175],a[144:147],v[80:83],v[172:175]
v_exp_f32 v157,v157
v_mfma_f32_16x16x32_bf16 v[184:187],a[160:163],v[80:83],v[36:39]
buffer_load_dwordx4 v5,s[24:27],s47 offen offset:1024 nt lds
v_mfma_f32_16x16x32_bf16 v[184:187],a[164:167],v[84:87],v[184:187]
v_exp_f32 v158,v158
v_mfma_f32_16x16x32_bf16 v[184:187],a[168:171],v[88:91],v[184:187]
v_exp_f32 v159,v159
v_mfma_f32_16x16x32_bf16 v[184:187],a[172:175],v[92:95],v[184:187]
v_exp_f32 v168,v168
v_mfma_f32_16x16x32_bf16 v[188:191],a[188:191],v[92:95],v[36:39]
v_exp_f32 v169,v169
v_mfma_f32_16x16x32_bf16 v[188:191],a[184:187],v[88:91],v[188:191]
v_exp_f32 v170,v170
v_mfma_f32_16x16x32_bf16 v[188:191],a[180:183],v[84:87],v[188:191]
v_exp_f32 v171,v171
v_mfma_f32_16x16x32_bf16 v[188:191],a[176:179],v[80:83],v[188:191]
v_exp_f32 v172,v172
v_mfma_f32_16x16x32_bf16 a[96:99],a[192:195],v[232:235],a[96:99]
ds_read_b64_tr_b16 a[192:193],v2
v_cvt_pk_bf16_f32 v168,v168,v169
v_mfma_f32_16x16x32_bf16 a[96:99],a[196:199],v[236:239],a[96:99]
ds_read_b64_tr_b16 a[194:195],v2 offset:4096
v_cvt_pk_bf16_f32 v169,v170,v171
v_mfma_f32_16x16x32_bf16 a[100:103],a[204:207],v[236:239],a[100:103]
v_exp_f32 v173,v173
v_mfma_f32_16x16x32_bf16 a[100:103],a[200:203],v[232:235],a[100:103]
ds_read_b64_tr_b16 a[196:197],v2 offset:8192
v_cvt_pk_bf16_f32 v170,v172,v173
v_mfma_f32_16x16x32_bf16 a[104:107],a[208:211],v[232:235],a[104:107]
v_exp_f32 v174,v174
v_mfma_f32_16x16x32_bf16 a[104:107],a[212:215],v[236:239],a[104:107]
v_exp_f32 v175,v175
v_mfma_f32_16x16x32_bf16 a[108:111],a[220:223],v[236:239],a[108:111]
buffer_load_dwordx4 v4,s[24:27],s47 offen offset:2048 nt lds
v_mfma_f32_16x16x32_bf16 a[108:111],a[216:219],v[232:235],a[108:111]
ds_read_b64_tr_b16 a[198:199],v2 offset:12288
v_cvt_pk_bf16_f32 v171,v174,v175
v_mfma_f32_16x16x32_bf16 a[112:115],a[224:227],v[232:235],a[112:115]
v_exp_f32 v184,v184
v_mfma_f32_16x16x32_bf16 a[112:115],a[228:231],v[236:239],a[112:115]
v_exp_f32 v185,v185
v_mfma_f32_16x16x32_bf16 a[116:119],a[236:239],v[236:239],a[116:119]
ds_read_b64_tr_b16 a[204:205],v3 offset:8192
v_cvt_pk_bf16_f32 v136,v136,v137
v_mfma_f32_16x16x32_bf16 a[116:119],a[232:235],v[232:235],a[116:119]
ds_read_b64_tr_b16 a[206:207],v3 offset:12288
v_cvt_pk_bf16_f32 v172,v184,v185
v_mfma_f32_16x16x32_bf16 a[120:123],a[240:243],v[232:235],a[120:123]
ds_read_b64_tr_b16 a[200:201],v3
v_mfma_f32_16x16x32_bf16 a[120:123],a[244:247],v[236:239],a[120:123]
ds_read_b64_tr_b16 a[202:203],v3 offset:4096
v_cvt_pk_bf16_f32 v137,v138,v139
v_mfma_f32_16x16x32_bf16 a[124:127],a[252:255],v[236:239],a[124:127]
ds_read_b64_tr_b16 a[208:209],v2 offset:256
v_mfma_f32_16x16x32_bf16 a[124:127],a[248:251],v[232:235],a[124:127]
ds_read_b64_tr_b16 a[210:211],v2 offset:4352
v_smfmac_f32_16x16x64_bf16 v[60:63],v[28:31],v[232:239],v27
ds_read_b64_tr_b16 a[212:213],v2 offset:8448
v_mfma_f32_16x16x32_bf16 v[200:203],a[128:131],v[96:99],v[40:43]
ds_read_b64_tr_b16 a[214:215],v2 offset:12544
v_cvt_pk_bf16_f32 v138,v140,v141
v_mfma_f32_16x16x32_bf16 v[200:203],a[132:135],v[100:103],v[200:203]
ds_read_b64_tr_b16 a[220:221],v3 offset:8448
v_mfma_f32_16x16x32_bf16 v[200:203],a[136:139],v[104:107],v[200:203]
ds_read_b64_tr_b16 a[222:223],v3 offset:12544
v_cvt_pk_bf16_f32 v139,v142,v143
v_mfma_f32_16x16x32_bf16 v[200:203],a[140:143],v[108:111],v[200:203]
buffer_load_dwordx4 v5,s[24:27],s47 offen offset:3072 nt lds
v_mfma_f32_16x16x32_bf16 v[204:207],a[156:159],v[108:111],v[40:43]
s_add_u32 s47,s47,16384
s_and_b32 s47,s47,0x3ffff
ds_read_b64_tr_b16 a[216:217],v3 offset:256
v_mfma_f32_16x16x32_bf16 v[204:207],a[152:155],v[104:107],v[204:207]
s_or_b32 s47,s47,s46
ds_read_b64_tr_b16 a[218:219],v3 offset:4352
v_cvt_pk_bf16_f32 v140,v152,v153
v_mfma_f32_16x16x32_bf16 v[204:207],a[148:151],v[100:103],v[204:207]
ds_read_b64_tr_b16 a[224:225],v2 offset:512
v_mfma_f32_16x16x32_bf16 v[204:207],a[144:147],v[96:99],v[204:207]
ds_read_b64_tr_b16 a[226:227],v2 offset:4608
v_mfma_f32_16x16x32_bf16 v[216:219],a[160:163],v[96:99],v[40:43]
ds_read_b64_tr_b16 a[228:229],v2 offset:8704
v_cvt_pk_bf16_f32 v141,v154,v155
v_mfma_f32_16x16x32_bf16 v[216:219],a[164:167],v[100:103],v[216:219]
ds_read_b64_tr_b16 a[230:231],v2 offset:12800
v_mfma_f32_16x16x32_bf16 v[216:219],a[168:171],v[104:107],v[216:219]
ds_read_b64_tr_b16 a[236:237],v3 offset:8704
v_mfma_f32_16x16x32_bf16 v[216:219],a[172:175],v[108:111],v[216:219]
ds_read_b64_tr_b16 a[238:239],v3 offset:12800
v_cvt_pk_bf16_f32 v142,v156,v157
v_mfma_f32_16x16x32_bf16 v[220:223],a[188:191],v[108:111],v[40:43]
ds_read_b64_tr_b16 a[232:233],v3 offset:512
v_mfma_f32_16x16x32_bf16 v[220:223],a[184:187],v[104:107],v[220:223]
ds_read_b64_tr_b16 a[234:235],v3 offset:4608
v_mfma_f32_16x16x32_bf16 v[220:223],a[180:183],v[100:103],v[220:223]
ds_read_b64_tr_b16 a[240:241],v2 offset:768
v_cvt_pk_bf16_f32 v143,v158,v159
v_mfma_f32_16x16x32_bf16 v[220:223],a[176:179],v[96:99],v[220:223]
s_waitcnt lgkmcnt(7)
buffer_load_dword v26,v24,s[88:91],s93 offen
v_mfma_f32_16x16x32_bf16 a[0:3],a[192:195],v[136:139],a[0:3]
ds_read_b64_tr_b16 a[242:243],v2 offset:4864
v_mfma_f32_16x16x32_bf16 a[0:3],a[196:199],v[140:143],a[0:3]
ds_read_b64_tr_b16 a[244:245],v2 offset:8960
v_mfma_f32_16x16x32_bf16 a[4:7],a[204:207],v[140:143],a[4:7]
ds_read_b64_tr_b16 a[246:247],v2 offset:13056
v_mfma_f32_16x16x32_bf16 a[4:7],a[200:203],v[136:139],a[4:7]
s_memrealtime s[76:77]
ds_read_b64_tr_b16 a[252:253],v3 offset:8960
v_mfma_f32_16x16x32_bf16 a[8:11],a[208:211],v[136:139],a[8:11]
ds_read_b64_tr_b16 a[254:255],v3 offset:13056
v_mfma_f32_16x16x32_bf16 a[8:11],a[212:215],v[140:143],a[8:11]
ds_read_b64_tr_b16 a[248:249],v3 offset:768
v_mfma_f32_16x16x32_bf16 a[12:15],a[220:223],v[140:143],a[12:15]
ds_read_b64_tr_b16 a[250:251],v3 offset:4864
v_mfma_f32_16x16x32_bf16 a[12:15],a[216:219],v[136:139],a[12:15]
s_bitcmp1_b32 s45,0
s_cbranch_scc0 L_t310_%=
s_mov_b32 m0,s80
s_add_u32 s80,s80,4096
buffer_load_dwordx4 v25,s[20:23],s83 offen nt lds
s_add_u32 s83,s83,4096
L_t310_%=:
v_mfma_f32_16x16x32_bf16 a[16:19],a[224:227],v[136:139],a[16:19]
s_waitcnt lgkmcnt(6)
v_exp_f32 v186,v186
v_mfma_f32_16x16x32_bf16 a[16:19],a[228:231],v[140:143],a[16:19]
v_exp_f32 v187,v187
v_mfma_f32_16x16x32_bf16 a[20:23],a[236:239],v[140:143],a[20:23]
s_waitcnt lgkmcnt(0)
v_exp_f32 v188,v188
v_mfma_f32_16x16x32_bf16 a[20:23],a[232:235],v[136:139],a[20:23]
s_sub_u32 s79,s78,s76
v_exp_f32 v189,v189
v_mfma_f32_16x16x32_bf16 a[24:27],a[240:243],v[136:139],a[24:27]
s_add_u32 s78,s76,s60
v_exp_f32 v190,v190
v_mfma_f32_16x16x32_bf16 a[24:27],a[244:247],v[140:143],a[24:27]
s_cmp_gt_i32 s79,s95
s_cbranch_scc1 L_ps3_%=
L_pr3_%=:
v_cvt_pk_bf16_f32 v173,v186,v187
v_mfma_f32_16x16x32_bf16 a[28:31],a[252:255],v[140:143],a[28:31]
v_exp_f32 v191,v191
v_mfma_f32_16x16x32_bf16 a[28:31],a[248:251],v[136:139],a[28:31]
v_cvt_pk_bf16_f32 v174,v188,v189
v_cvt_pk_bf16_f32 v175,v190,v191
s_cmp_lg_u32 s53,0
v_smfmac_f32_16x16x64_bf16 v[48:51],v[28:31],v[136:143],v27
s_cbranch_scc1 L_att0_%=
L_att0x_%=:
v_mfma_f32_16x16x32_bf16 v[232:235],a[128:131],v[112:115],v[44:47]
s_waitcnt vmcnt(10)
s_sub_u32 s94,s94,3
v_exp_f32 v200,v200
v_mfma_f32_16x16x32_bf16 v[232:235],a[132:135],v[116:119],v[232:235]
s_barrier
s_add_u32 m0,0,s51
ds_read_b128 a[128:131],v1
v_mfma_f32_16x16x32_bf16 v[232:235],a[136:139],v[120:123],v[232:235]
s_lshr_b32 s60,s94,2
v_exp_f32 v201,v201
v_mfma_f32_16x16x32_bf16 v[232:235],a[140:143],v[124:127],v[232:235]
s_max_i32 s60,s60,128
ds_read_b128 a[132:135],v1 offset:256
v_cvt_pk_bf16_f32 v200,v200,v201
v_mfma_f32_16x16x32_bf16 v[236:239],a[156:159],v[124:127],v[44:47]
s_add_u32 s54,s54,16384
buffer_load_dwordx4 v4,s[20:23],s47 offen nt lds
v_mfma_f32_16x16x32_bf16 v[236:239],a[152:155],v[120:123],v[236:239]
s_and_b32 s46,s54,0xfffc0000
ds_read_b128 a[136:139],v1 offset:512
v_and_b32 v18,48,v6
v_mfma_f32_16x16x32_bf16 v[236:239],a[148:151],v[116:119],v[236:239]
s_sub_u32 s94,s94,3
v_exp_f32 v202,v202
v_mfma_f32_16x16x32_bf16 v[236:239],a[144:147],v[112:115],v[236:239]
s_add_u32 s54,s54,16384
ds_read_b128 a[140:143],v1 offset:768
v_lshrrev_b32 v19,4,v6
v_mfma_f32_16x16x32_bf16 v[248:251],a[160:163],v[112:115],v[44:47]
s_mul_i32 s83,s41,5
v_exp_f32 v203,v203
v_mfma_f32_16x16x32_bf16 v[248:251],a[164:167],v[116:119],v[248:251]
s_lshl_b32 s44,s40,5
ds_read_b128 a[156:159],v1 offset:4864
v_cvt_pk_bf16_f32 v201,v202,v203
v_mfma_f32_16x16x32_bf16 v[248:251],a[168:171],v[120:123],v[248:251]
s_add_u32 s83,s83,s42
v_exp_f32 v204,v204
)A"
R"A(v_mfma_f32_16x16x32_bf16 v[248:251],a[172:175],v[124:127],v[248:251]
s_add_u32 s84,s55,0
ds_read_b128 a[152:155],v1 offset:4608
v_lshlrev_b32 v18,2,v18
v_mfma_f32_16x16x32_bf16 v[252:255],a[188:191],v[124:127],v[44:47]
s_add_u32 s85,s55,4096
v_exp_f32 v205,v205
v_mfma_f32_16x16x32_bf16 v[252:255],a[184:187],v[120:123],v[252:255]
s_add_u32 s86,s55,8192
ds_read_b128 a[148:151],v1 offset:4352
v_cvt_pk_bf16_f32 v202,v204,v205
v_mfma_f32_16x16x32_bf16 v[252:255],a[180:183],v[116:119],v[252:255]
s_add_u32 s87,s55,12288
v_exp_f32 v206,v206
v_mfma_f32_16x16x32_bf16 v[252:255],a[176:179],v[112:115],v[252:255]
s_and_b32 s44,s44,32
ds_read_b128 a[144:147],v1 offset:4096
v_and_b32 v19,48,v19
v_mfma_f32_16x16x32_bf16 a[32:35],a[192:195],v[168:171],a[32:35]
s_add_u32 s83,s83,15
v_exp_f32 v207,v207
v_mfma_f32_16x16x32_bf16 a[32:35],a[196:199],v[172:175],a[32:35]
s_max_i32 s84,s84,s18
ds_read_b128 a[160:163],v1 offset:8192
v_cvt_pk_bf16_f32 v203,v206,v207
v_mfma_f32_16x16x32_bf16 a[36:39],a[204:207],v[172:175],a[36:39]
s_max_i32 s85,s85,s19
buffer_load_dwordx4 v4,s[20:23],s47 offen offset:1024 nt lds
v_mfma_f32_16x16x32_bf16 a[36:39],a[200:203],v[168:171],a[36:39]
s_max_i32 s86,s86,s100
ds_read_b128 a[164:167],v1 offset:8448
v_add_u32 v18,v18,v19
v_mfma_f32_16x16x32_bf16 a[40:43],a[208:211],v[168:171],a[40:43]
s_max_i32 s87,s87,s101
v_exp_f32 v216,v216
v_mfma_f32_16x16x32_bf16 a[40:43],a[212:215],v[172:175],a[40:43]
s_lshl_b32 s80,s40,10
ds_read_b128 a[168:171],v1 offset:8704
v_add_u32 v18,98304,v18
v_mfma_f32_16x16x32_bf16 a[44:47],a[220:223],v[172:175],a[44:47]
s_and_b32 s83,s83,15
v_exp_f32 v217,v217
v_mfma_f32_16x16x32_bf16 a[44:47],a[216:219],v[168:171],a[44:47]
s_add_u32 s83,s83,16
ds_read_b128 a[172:175],v1 offset:8960
v_cvt_pk_bf16_f32 v204,v216,v217
v_mfma_f32_16x16x32_bf16 a[48:51],a[224:227],v[168:171],a[48:51]
s_mov_b32 s50,32768
v_exp_f32 v218,v218
v_mfma_f32_16x16x32_bf16 a[48:51],a[228:231],v[172:175],a[48:51]
s_mov_b32 s49,98304
ds_read_b128 a[188:191],v1 offset:13056
v_add_u32 v2,s48,v2
v_mfma_f32_16x16x32_bf16 a[52:55],a[236:239],v[172:175],a[52:55]
s_mov_b32 s45,0x2a8
v_exp_f32 v219,v219
v_mfma_f32_16x16x32_bf16 a[52:55],a[232:235],v[168:171],a[52:55]
s_lshl_b32 s83,s83,14
ds_read_b128 a[184:187],v1 offset:12800
v_cvt_pk_bf16_f32 v205,v218,v219
v_mfma_f32_16x16x32_bf16 a[56:59],a[240:243],v[168:171],a[56:59]
s_add_u32 s93,s92,L_endseg_%=-L_pc_%=
v_exp_f32 v220,v220
v_mfma_f32_16x16x32_bf16 a[56:59],a[244:247],v[172:175],a[56:59]
ds_read_b128 a[180:183],v1 offset:12544
v_add_u32 v3,s48,v3
v_mfma_f32_16x16x32_bf16 a[60:63],a[252:255],v[172:175],a[60:63]
s_mov_b32 s48,32768
v_exp_f32 v221,v221
v_mfma_f32_16x16x32_bf16 a[60:63],a[248:251],v[168:171],a[60:63]
ds_read_b128 a[176:179],v1 offset:12288
v_cvt_pk_bf16_f32 v206,v220,v221
v_smfmac_f32_16x16x64_bf16 v[52:55],v[28:31],v[168:175],v27
s_waitcnt lgkmcnt(4)
v_add_u32 v1,s48,v1
v_mfma_f32_16x16x32_bf16 v[136:139],a[128:131],v[64:67],v[32:35]
buffer_load_dwordx4 v5,s[20:23],s47 offen offset:2048 nt lds
v_mfma_f32_16x16x32_bf16 v[136:139],a[132:135],v[68:71],v[136:139]
v_exp_f32 v222,v222
v_mfma_f32_16x16x32_bf16 v[136:139],a[136:139],v[72:75],v[136:139]
v_exp_f32 v223,v223
v_mfma_f32_16x16x32_bf16 v[136:139],a[140:143],v[76:79],v[136:139]
v_cvt_pk_bf16_f32 v207,v222,v223
v_subrev_u32 v25,s51,v4
v_mfma_f32_16x16x32_bf16 v[140:143],a[156:159],v[76:79],v[32:35]
v_exp_f32 v232,v232
v_mfma_f32_16x16x32_bf16 v[140:143],a[152:155],v[72:75],v[140:143]
v_exp_f32 v233,v233
v_mfma_f32_16x16x32_bf16 v[140:143],a[148:151],v[68:71],v[140:143]
v_exp_f32 v234,v234
v_mfma_f32_16x16x32_bf16 v[140:143],a[144:147],v[64:67],v[140:143]
v_exp_f32 v235,v235
v_mfma_f32_16x16x32_bf16 v[152:155],a[160:163],v[64:67],v[32:35]
v_cvt_pk_bf16_f32 v232,v232,v233
v_cvt_pk_bf16_f32 v233,v234,v235
v_mfma_f32_16x16x32_bf16 v[152:155],a[164:167],v[68:71],v[152:155]
v_exp_f32 v236,v236
v_mfma_f32_16x16x32_bf16 v[152:155],a[168:171],v[72:75],v[152:155]
v_exp_f32 v237,v237
v_mfma_f32_16x16x32_bf16 v[152:155],a[172:175],v[76:79],v[152:155]
s_waitcnt lgkmcnt(0)
v_exp_f32 v238,v238
v_mfma_f32_16x16x32_bf16 v[156:159],a[188:191],v[76:79],v[32:35]
v_exp_f32 v239,v239
v_mfma_f32_16x16x32_bf16 v[156:159],a[184:187],v[72:75],v[156:159]
buffer_load_dwordx4 v5,s[20:23],s47 offen offset:3072 nt lds
v_mfma_f32_16x16x32_bf16 v[156:159],a[180:183],v[68:71],v[156:159]
v_cvt_pk_bf16_f32 v234,v236,v237
v_cvt_pk_bf16_f32 v235,v238,v239
v_mfma_f32_16x16x32_bf16 v[156:159],a[176:179],v[64:67],v[156:159]
v_exp_f32 v248,v248
v_mfma_f32_16x16x32_bf16 a[64:67],a[192:195],v[200:203],a[64:67]
v_exp_f32 v249,v249
v_mfma_f32_16x16x32_bf16 a[64:67],a[196:199],v[204:207],a[64:67]
v_exp_f32 v250,v250
v_mfma_f32_16x16x32_bf16 a[68:71],a[204:207],v[204:207],a[68:71]
v_exp_f32 v251,v251
v_mfma_f32_16x16x32_bf16 a[68:71],a[200:203],v[200:203],a[68:71]
v_exp_f32 v252,v252
v_mfma_f32_16x16x32_bf16 a[72:75],a[208:211],v[200:203],a[72:75]
v_exp_f32 v253,v253
v_mfma_f32_16x16x32_bf16 a[72:75],a[212:215],v[204:207],a[72:75]
v_exp_f32 v254,v254
v_mfma_f32_16x16x32_bf16 a[76:79],a[220:223],v[204:207],a[76:79]
v_exp_f32 v255,v255
v_mfma_f32_16x16x32_bf16 a[76:79],a[216:219],v[200:203],a[76:79]
v_cvt_pk_bf16_f32 v236,v248,v249
v_cvt_pk_bf16_f32 v237,v250,v251
v_mfma_f32_16x16x32_bf16 a[80:83],a[224:227],v[200:203],a[80:83]
s_add_u32 m0,0,s52
v_cvt_pk_bf16_f32 v238,v252,v253
v_cvt_pk_bf16_f32 v239,v254,v255
v_mfma_f32_16x16x32_bf16 a[80:83],a[228:231],v[204:207],a[80:83]
v_exp_f32 v136,v136
v_mfma_f32_16x16x32_bf16 a[84:87],a[236:239],v[204:207],a[84:87]
v_exp_f32 v137,v137
v_mfma_f32_16x16x32_bf16 a[84:87],a[232:235],v[200:203],a[84:87]
buffer_load_dwordx4 v4,s[24:27],s47 offen nt lds
v_mfma_f32_16x16x32_bf16 a[88:91],a[240:243],v[200:203],a[88:91]
v_exp_f32 v138,v138
v_mfma_f32_16x16x32_bf16 a[88:91],a[244:247],v[204:207],a[88:91]
v_exp_f32 v139,v139
v_mfma_f32_16x16x32_bf16 a[92:95],a[252:255],v[204:207],a[92:95]
v_cvt_pk_bf16_f32 v136,v136,v137
v_cvt_pk_bf16_f32 v137,v138,v139
v_mfma_f32_16x16x32_bf16 a[92:95],a[248:251],v[200:203],a[92:95]
v_exp_f32 v140,v140
v_smfmac_f32_16x16x64_bf16 v[56:59],v[28:31],v[200:207],v27
v_xor_b32 v25,s44,v25
v_mfma_f32_16x16x32_bf16 v[168:171],a[128:131],v[80:83],v[36:39]
v_exp_f32 v141,v141
v_mfma_f32_16x16x32_bf16 v[168:171],a[132:135],v[84:87],v[168:171]
v_exp_f32 v142,v142
v_mfma_f32_16x16x32_bf16 v[168:171],a[136:139],v[88:91],v[168:171]
v_exp_f32 v143,v143
v_mfma_f32_16x16x32_bf16 v[168:171],a[140:143],v[92:95],v[168:171]
v_cvt_pk_bf16_f32 v138,v140,v141
v_cvt_pk_bf16_f32 v139,v142,v143
v_mfma_f32_16x16x32_bf16 v[172:175],a[156:159],v[92:95],v[36:39]
v_exp_f32 v152,v152
v_mfma_f32_16x16x32_bf16 v[172:175],a[152:155],v[88:91],v[172:175]
v_exp_f32 v153,v153
v_mfma_f32_16x16x32_bf16 v[172:175],a[148:151],v[84:87],v[172:175]
v_exp_f32 v154,v154
v_mfma_f32_16x16x32_bf16 v[172:175],a[144:147],v[80:83],v[172:175]
v_exp_f32 v155,v155
v_mfma_f32_16x16x32_bf16 v[184:187],a[160:163],v[80:83],v[36:39]
buffer_load_dwordx4 v5,s[24:27],s47 offen offset:1024 nt lds
v_mfma_f32_16x16x32_bf16 v[184:187],a[164:167],v[84:87],v[184:187]
v_exp_f32 v156,v156
v_mfma_f32_16x16x32_bf16 v[184:187],a[168:171],v[88:91],v[184:187]
v_exp_f32 v157,v157
v_mfma_f32_16x16x32_bf16 v[184:187],a[172:175],v[92:95],v[184:187]
v_exp_f32 v158,v158
v_mfma_f32_16x16x32_bf16 v[188:191],a[188:191],v[92:95],v[36:39]
v_exp_f32 v159,v159
v_mfma_f32_16x16x32_bf16 v[188:191],a[184:187],v[88:91],v[188:191]
v_cvt_pk_bf16_f32 v140,v152,v153
v_cvt_pk_bf16_f32 v141,v154,v155
v_mfma_f32_16x16x32_bf16 v[188:191],a[180:183],v[84:87],v[188:191]
s_waitcnt vmcnt(14)
v_cvt_pk_bf16_f32 v142,v156,v157
v_cvt_pk_bf16_f32 v143,v158,v159
v_mfma_f32_16x16x32_bf16 v[188:191],a[176:179],v[80:83],v[188:191]
s_barrier
ds_read_b128 v[80:83],v18
v_add_u32 v25,s80,v25
v_mfma_f32_16x16x32_bf16 a[96:99],a[192:195],v[232:235],a[96:99]
s_add_u32 s80,s80,147456
v_exp_f32 v168,v168
v_mfma_f32_16x16x32_bf16 a[96:99],a[196:199],v[236:239],a[96:99]
v_exp_f32 v169,v169
v_mfma_f32_16x16x32_bf16 a[100:103],a[204:207],v[236:239],a[100:103]
ds_read_b128 v[84:87],v18 offset:256
v_cvt_pk_bf16_f32 v168,v168,v169
v_mfma_f32_16x16x32_bf16 a[100:103],a[200:203],v[232:235],a[100:103]
v_exp_f32 v170,v170
v_mfma_f32_16x16x32_bf16 a[104:107],a[208:211],v[232:235],a[104:107]
v_exp_f32 v171,v171
v_mfma_f32_16x16x32_bf16 a[104:107],a[212:215],v[236:239],a[104:107]
buffer_load_dwordx4 v4,s[24:27],s47 offen offset:2048 nt lds
v_mfma_f32_16x16x32_bf16 a[108:111],a[220:223],v[236:239],a[108:111]
ds_read_b128 v[88:91],v18 offset:512
v_cvt_pk_bf16_f32 v169,v170,v171
v_mfma_f32_16x16x32_bf16 a[108:111],a[216:219],v[232:235],a[108:111]
v_exp_f32 v172,v172
v_mfma_f32_16x16x32_bf16 a[112:115],a[224:227],v[232:235],a[112:115]
v_exp_f32 v173,v173
v_mfma_f32_16x16x32_bf16 a[112:115],a[228:231],v[236:239],a[112:115]
ds_read_b128 v[92:95],v18 offset:768
v_cvt_pk_bf16_f32 v170,v172,v173
v_mfma_f32_16x16x32_bf16 a[116:119],a[236:239],v[236:239],a[116:119]
v_exp_f32 v174,v174
v_mfma_f32_16x16x32_bf16 a[116:119],a[232:235],v[232:235],a[116:119]
ds_read_b64_tr_b16 a[192:193],v2
ds_read_b64_tr_b16 a[194:195],v2 offset:4096
v_mfma_f32_16x16x32_bf16 a[120:123],a[240:243],v[232:235],a[120:123]
v_exp_f32 v175,v175
v_mfma_f32_16x16x32_bf16 a[120:123],a[244:247],v[236:239],a[120:123]
ds_read_b64_tr_b16 a[196:197],v2 offset:8192
ds_read_b64_tr_b16 a[198:199],v2 offset:12288
v_mfma_f32_16x16x32_bf16 a[124:127],a[252:255],v[236:239],a[124:127]
ds_read_b64_tr_b16 a[204:205],v3 offset:8192
v_cvt_pk_bf16_f32 v171,v174,v175
v_mfma_f32_16x16x32_bf16 a[124:127],a[248:251],v[232:235],a[124:127]
v_exp_f32 v184,v184
v_smfmac_f32_16x16x64_bf16 v[60:63],v[28:31],v[232:239],v27
ds_read_b64_tr_b16 a[206:207],v3 offset:12288
v_mfma_f32_16x16x32_bf16 v[200:203],a[128:131],v[96:99],v[40:43]
ds_read_b64_tr_b16 a[200:201],v3
ds_read_b64_tr_b16 a[202:203],v3 offset:4096
v_mfma_f32_16x16x32_bf16 v[200:203],a[132:135],v[100:103],v[200:203]
v_exp_f32 v185,v185
v_mfma_f32_16x16x32_bf16 v[200:203],a[136:139],v[104:107],v[200:203]
buffer_load_dwordx4 v5,s[24:27],s47 offen offset:3072 nt lds
v_mfma_f32_16x16x32_bf16 v[200:203],a[140:143],v[108:111],v[200:203]
s_add_u32 s47,s47,16384
ds_read_b64_tr_b16 a[208:209],v2 offset:256
ds_read_b64_tr_b16 a[210:211],v2 offset:4352
v_mfma_f32_16x16x32_bf16 v[204:207],a[156:159],v[108:111],v[40:43]
s_and_b32 s47,s47,0x3ffff
s_or_b32 s47,s47,s46
ds_read_b64_tr_b16 a[212:213],v2 offset:8448
v_mfma_f32_16x16x32_bf16 v[204:207],a[152:155],v[104:107],v[204:207]
s_and_b32 s46,s54,0xfffc0000
ds_read_b64_tr_b16 a[214:215],v2 offset:12544
v_cvt_pk_bf16_f32 v172,v184,v185
v_mfma_f32_16x16x32_bf16 v[204:207],a[148:151],v[100:103],v[204:207]
s_add_u32 s54,s54,16384
v_exp_f32 v186,v186
v_mfma_f32_16x16x32_bf16 v[204:207],a[144:147],v[96:99],v[204:207]
ds_read_b64_tr_b16 a[220:221],v3 offset:8448
ds_read_b64_tr_b16 a[222:223],v3 offset:12544
v_mfma_f32_16x16x32_bf16 v[216:219],a[160:163],v[96:99],v[40:43]
v_exp_f32 v187,v187
v_mfma_f32_16x16x32_bf16 v[216:219],a[164:167],v[100:103],v[216:219]
ds_read_b64_tr_b16 a[216:217],v3 offset:256
ds_read_b64_tr_b16 a[218:219],v3 offset:4352
v_mfma_f32_16x16x32_bf16 v[216:219],a[168:171],v[104:107],v[216:219]
ds_read_b64_tr_b16 a[224:225],v2 offset:512
v_cvt_pk_bf16_f32 v173,v186,v187
v_mfma_f32_16x16x32_bf16 v[216:219],a[172:175],v[108:111],v[216:219]
ds_read_b64_tr_b16 a[226:227],v2 offset:4608
v_mfma_f32_16x16x32_bf16 v[220:223],a[188:191],v[108:111],v[40:43]
ds_read_b64_tr_b16 a[228:229],v2 offset:8704
v_mfma_f32_16x16x32_bf16 v[220:223],a[184:187],v[104:107],v[220:223]
ds_read_b64_tr_b16 a[230:231],v2 offset:12800
ds_read_b64_tr_b16 a[236:237],v3 offset:8704
v_mfma_f32_16x16x32_bf16 v[220:223],a[180:183],v[100:103],v[220:223]
ds_read_b64_tr_b16 a[238:239],v3 offset:12800
v_mfma_f32_16x16x32_bf16 v[220:223],a[176:179],v[96:99],v[220:223]
s_waitcnt lgkmcnt(8)
ds_read_b64_tr_b16 a[232:233],v3 offset:512
v_mfma_f32_16x16x32_bf16 v[32:35],v[80:83],v[128:131],0
ds_read_b64_tr_b16 a[234:235],v3 offset:4608
v_mfma_f32_16x16x32_bf16 v[32:35],v[84:87],v[160:163],v[32:35]
)A"
R"A(ds_read_b64_tr_b16 a[240:241],v2 offset:768
ds_read_b64_tr_b16 a[242:243],v2 offset:4864
v_mfma_f32_16x16x32_bf16 v[32:35],v[88:91],v[192:195],v[32:35]
ds_read_b64_tr_b16 a[244:245],v2 offset:8960
v_mfma_f32_16x16x32_bf16 v[32:35],v[92:95],v[224:227],v[32:35]
ds_read_b64_tr_b16 a[246:247],v2 offset:13056
v_add_u32 v2,s48,v2
v_mfma_f32_16x16x32_bf16 v[36:39],v[80:83],v[132:135],0
ds_read_b64_tr_b16 a[252:253],v3 offset:8960
v_mfma_f32_16x16x32_bf16 v[36:39],v[84:87],v[164:167],v[36:39]
ds_read_b64_tr_b16 a[254:255],v3 offset:13056
ds_read_b64_tr_b16 a[248:249],v3 offset:768
v_mfma_f32_16x16x32_bf16 v[36:39],v[88:91],v[196:199],v[36:39]
ds_read_b64_tr_b16 a[250:251],v3 offset:4864
v_add_u32 v3,s48,v3
v_mfma_f32_16x16x32_bf16 v[36:39],v[92:95],v[228:231],v[36:39]
s_mov_b32 s48,-98304
v_exp_f32 v188,v188
v_mfma_f32_16x16x32_bf16 v[40:43],v[80:83],v[144:147],0
v_exp_f32 v189,v189
v_mfma_f32_16x16x32_bf16 v[40:43],v[84:87],v[176:179],v[40:43]
v_exp_f32 v190,v190
v_mfma_f32_16x16x32_bf16 v[40:43],v[88:91],v[208:211],v[40:43]
v_exp_f32 v191,v191
v_mfma_f32_16x16x32_bf16 v[40:43],v[92:95],v[240:243],v[40:43]
v_cvt_pk_bf16_f32 v174,v188,v189
v_cvt_pk_bf16_f32 v175,v190,v191
v_mfma_f32_16x16x32_bf16 a[0:3],a[192:195],v[136:139],a[0:3]
v_exp_f32 v200,v200
v_mfma_f32_16x16x32_bf16 a[0:3],a[196:199],v[140:143],a[0:3]
v_max3_f32 v32,v32,v33,v34
v_max3_f32 v32,v32,v35,s18
v_mfma_f32_16x16x32_bf16 a[4:7],a[204:207],v[140:143],a[4:7]
v_sub_f32 v35,0,v32
v_sub_f32 v34,0,v32
v_mfma_f32_16x16x32_bf16 a[4:7],a[200:203],v[136:139],a[4:7]
s_memrealtime s[76:77]
v_sub_f32 v33,0,v32
v_sub_f32 v32,0,v32
v_mfma_f32_16x16x32_bf16 a[8:11],a[208:211],v[136:139],a[8:11]
v_exp_f32 v201,v201
v_mfma_f32_16x16x32_bf16 a[8:11],a[212:215],v[140:143],a[8:11]
v_exp_f32 v202,v202
v_mfma_f32_16x16x32_bf16 a[12:15],a[220:223],v[140:143],a[12:15]
s_waitcnt lgkmcnt(0)
v_exp_f32 v203,v203
v_mfma_f32_16x16x32_bf16 a[12:15],a[216:219],v[136:139],a[12:15]
v_cvt_pk_bf16_f32 v200,v200,v201
v_cvt_pk_bf16_f32 v201,v202,v203
v_mfma_f32_16x16x32_bf16 a[16:19],a[224:227],v[136:139],a[16:19]
v_exp_f32 v204,v204
v_mfma_f32_16x16x32_bf16 a[16:19],a[228:231],v[140:143],a[16:19]
v_exp_f32 v205,v205
v_mfma_f32_16x16x32_bf16 a[20:23],a[236:239],v[140:143],a[20:23]
s_waitcnt lgkmcnt(0)
v_exp_f32 v206,v206
v_mfma_f32_16x16x32_bf16 a[20:23],a[232:235],v[136:139],a[20:23]
s_sub_u32 s79,s78,s76
v_exp_f32 v207,v207
v_mfma_f32_16x16x32_bf16 a[24:27],a[240:243],v[136:139],a[24:27]
s_add_u32 s78,s76,s60
v_cvt_pk_bf16_f32 v202,v204,v205
v_cvt_pk_bf16_f32 v203,v206,v207
v_mfma_f32_16x16x32_bf16 a[24:27],a[244:247],v[140:143],a[24:27]
s_cmp_gt_i32 s79,s95
s_cbranch_scc1 L_ps4_%=
L_pr4_%=:
v_max3_f32 v36,v36,v37,v38
v_mfma_f32_16x16x32_bf16 a[28:31],a[252:255],v[140:143],a[28:31]
s_lshr_b32 s60,s94,2
v_exp_f32 v216,v216
v_mfma_f32_16x16x32_bf16 a[28:31],a[248:251],v[136:139],a[28:31]
s_max_i32 s60,s60,128
v_exp_f32 v217,v217
v_smfmac_f32_16x16x64_bf16 v[48:51],v[28:31],v[136:143],v27
s_sub_u32 s94,s94,3
v_cvt_pk_bf16_f32 v204,v216,v217
v_mfma_f32_16x16x32_bf16 v[232:235],a[128:131],v[112:115],v[44:47]
s_waitcnt vmcnt(9)
v_exp_f32 v218,v218
v_mfma_f32_16x16x32_bf16 v[232:235],a[132:135],v[116:119],v[232:235]
s_barrier
s_add_u32 m0,32768,s51
ds_read_b128 a[128:131],v1
v_mfma_f32_16x16x32_bf16 v[232:235],a[136:139],v[120:123],v[232:235]
v_exp_f32 v219,v219
v_mfma_f32_16x16x32_bf16 v[232:235],a[140:143],v[124:127],v[232:235]
ds_read_b128 a[132:135],v1 offset:256
v_cvt_pk_bf16_f32 v205,v218,v219
v_mfma_f32_16x16x32_bf16 v[236:239],a[156:159],v[124:127],v[44:47]
buffer_load_dwordx4 v4,s[20:23],s47 offen nt lds
v_mfma_f32_16x16x32_bf16 v[236:239],a[152:155],v[120:123],v[236:239]
ds_read_b128 a[136:139],v1 offset:512
v_max3_f32 v36,v36,v39,s19
v_mfma_f32_16x16x32_bf16 v[236:239],a[148:151],v[116:119],v[236:239]
v_exp_f32 v220,v220
v_mfma_f32_16x16x32_bf16 v[236:239],a[144:147],v[112:115],v[236:239]
ds_read_b128 a[140:143],v1 offset:768
v_sub_f32 v39,0,v36
v_mfma_f32_16x16x32_bf16 v[248:251],a[160:163],v[112:115],v[44:47]
v_exp_f32 v221,v221
v_mfma_f32_16x16x32_bf16 v[248:251],a[164:167],v[116:119],v[248:251]
ds_read_b128 a[156:159],v1 offset:4864
v_cvt_pk_bf16_f32 v206,v220,v221
v_mfma_f32_16x16x32_bf16 v[248:251],a[168:171],v[120:123],v[248:251]
v_exp_f32 v222,v222
v_mfma_f32_16x16x32_bf16 v[248:251],a[172:175],v[124:127],v[248:251]
ds_read_b128 a[152:155],v1 offset:4608
v_sub_f32 v38,0,v36
v_mfma_f32_16x16x32_bf16 v[252:255],a[188:191],v[124:127],v[44:47]
v_exp_f32 v223,v223
v_mfma_f32_16x16x32_bf16 v[252:255],a[184:187],v[120:123],v[252:255]
ds_read_b128 a[148:151],v1 offset:4352
v_cvt_pk_bf16_f32 v207,v222,v223
v_mfma_f32_16x16x32_bf16 v[252:255],a[180:183],v[116:119],v[252:255]
v_sub_f32 v37,0,v36
v_sub_f32 v36,0,v36
v_mfma_f32_16x16x32_bf16 v[252:255],a[176:179],v[112:115],v[252:255]
ds_read_b128 a[144:147],v1 offset:4096
v_max3_f32 v40,v40,v41,v42
v_mfma_f32_16x16x32_bf16 v[44:47],v[80:83],v[148:151],0
v_exp_f32 v232,v232
v_mfma_f32_16x16x32_bf16 v[44:47],v[84:87],v[180:183],v[44:47]
ds_read_b128 a[160:163],v1 offset:8192
v_max3_f32 v40,v40,v43,s100
v_mfma_f32_16x16x32_bf16 v[44:47],v[88:91],v[212:215],v[44:47]
buffer_load_dwordx4 v4,s[20:23],s47 offen offset:1024 nt lds
v_mfma_f32_16x16x32_bf16 v[44:47],v[92:95],v[244:247],v[44:47]
ds_read_b128 a[164:167],v1 offset:8448
v_sub_f32 v43,0,v40
v_mfma_f32_16x16x32_bf16 a[32:35],a[192:195],v[168:171],a[32:35]
v_exp_f32 v233,v233
v_mfma_f32_16x16x32_bf16 a[32:35],a[196:199],v[172:175],a[32:35]
ds_read_b128 a[168:171],v1 offset:8704
v_cvt_pk_bf16_f32 v232,v232,v233
v_mfma_f32_16x16x32_bf16 a[36:39],a[204:207],v[172:175],a[36:39]
v_exp_f32 v234,v234
v_mfma_f32_16x16x32_bf16 a[36:39],a[200:203],v[168:171],a[36:39]
ds_read_b128 a[172:175],v1 offset:8960
v_sub_f32 v42,0,v40
v_mfma_f32_16x16x32_bf16 a[40:43],a[208:211],v[168:171],a[40:43]
v_exp_f32 v235,v235
v_mfma_f32_16x16x32_bf16 a[40:43],a[212:215],v[172:175],a[40:43]
ds_read_b128 a[188:191],v1 offset:13056
v_cvt_pk_bf16_f32 v233,v234,v235
v_mfma_f32_16x16x32_bf16 a[44:47],a[220:223],v[172:175],a[44:47]
v_exp_f32 v236,v236
v_mfma_f32_16x16x32_bf16 a[44:47],a[216:219],v[168:171],a[44:47]
ds_read_b128 a[184:187],v1 offset:12800
v_sub_f32 v41,0,v40
v_mfma_f32_16x16x32_bf16 a[48:51],a[224:227],v[168:171],a[48:51]
v_exp_f32 v237,v237
v_mfma_f32_16x16x32_bf16 a[48:51],a[228:231],v[172:175],a[48:51]
ds_read_b128 a[180:183],v1 offset:12544
v_cvt_pk_bf16_f32 v234,v236,v237
v_mfma_f32_16x16x32_bf16 a[52:55],a[236:239],v[172:175],a[52:55]
v_exp_f32 v238,v238
v_mfma_f32_16x16x32_bf16 a[52:55],a[232:235],v[168:171],a[52:55]
ds_read_b128 a[176:179],v1 offset:12288
v_sub_f32 v40,0,v40
v_mfma_f32_16x16x32_bf16 a[56:59],a[240:243],v[168:171],a[56:59]
buffer_load_dwordx4 v5,s[20:23],s47 offen offset:2048 nt lds
v_mfma_f32_16x16x32_bf16 a[56:59],a[244:247],v[172:175],a[56:59]
v_exp_f32 v239,v239
v_mfma_f32_16x16x32_bf16 a[60:63],a[252:255],v[172:175],a[60:63]
v_cvt_pk_bf16_f32 v235,v238,v239
v_add_f32 v48,v49,v48
v_mfma_f32_16x16x32_bf16 a[60:63],a[248:251],v[168:171],a[60:63]
v_exp_f32 v248,v248
v_smfmac_f32_16x16x64_bf16 v[52:55],v[28:31],v[168:175],v27
s_waitcnt lgkmcnt(2)
v_max_f32 v20,0x00800000,v48
v_mfma_f32_16x16x32_bf16 v[136:139],a[128:131],v[128:131],v[32:35]
v_exp_f32 v249,v249
v_mfma_f32_16x16x32_bf16 v[136:139],a[132:135],v[160:163],v[136:139]
v_exp_f32 v250,v250
v_mfma_f32_16x16x32_bf16 v[136:139],a[136:139],v[192:195],v[136:139]
v_exp_f32 v251,v251
v_mfma_f32_16x16x32_bf16 v[136:139],a[140:143],v[224:227],v[136:139]
v_exp_f32 v252,v252
v_mfma_f32_16x16x32_bf16 v[140:143],a[156:159],v[224:227],v[32:35]
v_exp_f32 v253,v253
v_mfma_f32_16x16x32_bf16 v[140:143],a[152:155],v[192:195],v[140:143]
v_exp_f32 v254,v254
v_mfma_f32_16x16x32_bf16 v[140:143],a[148:151],v[160:163],v[140:143]
v_exp_f32 v255,v255
v_mfma_f32_16x16x32_bf16 v[140:143],a[144:147],v[128:131],v[140:143]
v_cvt_pk_bf16_f32 v236,v248,v249
v_cvt_pk_bf16_f32 v237,v250,v251
v_mfma_f32_16x16x32_bf16 v[152:155],a[160:163],v[128:131],v[32:35]
v_cvt_pk_bf16_f32 v238,v252,v253
v_cvt_pk_bf16_f32 v239,v254,v255
v_mfma_f32_16x16x32_bf16 v[152:155],a[164:167],v[160:163],v[152:155]
buffer_load_dwordx4 v5,s[20:23],s47 offen offset:3072 nt lds
v_mfma_f32_16x16x32_bf16 v[152:155],a[168:171],v[192:195],v[152:155]
v_exp_f32 v136,v136
v_mfma_f32_16x16x32_bf16 v[152:155],a[172:175],v[224:227],v[152:155]
v_exp_f32 v137,v137
v_mfma_f32_16x16x32_bf16 v[156:159],a[188:191],v[224:227],v[32:35]
v_exp_f32 v138,v138
v_mfma_f32_16x16x32_bf16 v[156:159],a[184:187],v[192:195],v[156:159]
s_waitcnt lgkmcnt(0)
v_exp_f32 v139,v139
v_mfma_f32_16x16x32_bf16 v[156:159],a[180:183],v[160:163],v[156:159]
v_cvt_pk_bf16_f32 v136,v136,v137
v_cvt_pk_bf16_f32 v137,v138,v139
v_mfma_f32_16x16x32_bf16 v[156:159],a[176:179],v[128:131],v[156:159]
v_exp_f32 v140,v140
v_mfma_f32_16x16x32_bf16 a[64:67],a[192:195],v[200:203],a[64:67]
v_exp_f32 v141,v141
v_mfma_f32_16x16x32_bf16 a[64:67],a[196:199],v[204:207],a[64:67]
v_exp_f32 v142,v142
v_mfma_f32_16x16x32_bf16 a[68:71],a[204:207],v[204:207],a[68:71]
v_exp_f32 v143,v143
v_mfma_f32_16x16x32_bf16 a[68:71],a[200:203],v[200:203],a[68:71]
v_cvt_pk_bf16_f32 v138,v140,v141
v_cvt_pk_bf16_f32 v139,v142,v143
v_mfma_f32_16x16x32_bf16 a[72:75],a[208:211],v[200:203],a[72:75]
s_add_u32 m0,32768,s52
v_exp_f32 v152,v152
v_mfma_f32_16x16x32_bf16 a[72:75],a[212:215],v[204:207],a[72:75]
v_exp_f32 v153,v153
v_mfma_f32_16x16x32_bf16 a[76:79],a[220:223],v[204:207],a[76:79]
v_exp_f32 v154,v154
v_mfma_f32_16x16x32_bf16 a[76:79],a[216:219],v[200:203],a[76:79]
buffer_load_dwordx4 v4,s[24:27],s47 offen nt lds
v_mfma_f32_16x16x32_bf16 a[80:83],a[224:227],v[200:203],a[80:83]
v_exp_f32 v155,v155
v_mfma_f32_16x16x32_bf16 a[80:83],a[228:231],v[204:207],a[80:83]
v_exp_f32 v156,v156
v_mfma_f32_16x16x32_bf16 a[84:87],a[236:239],v[204:207],a[84:87]
v_exp_f32 v157,v157
v_mfma_f32_16x16x32_bf16 a[84:87],a[232:235],v[200:203],a[84:87]
v_exp_f32 v158,v158
v_mfma_f32_16x16x32_bf16 a[88:91],a[240:243],v[200:203],a[88:91]
v_exp_f32 v159,v159
v_mfma_f32_16x16x32_bf16 a[88:91],a[244:247],v[204:207],a[88:91]
v_cvt_pk_bf16_f32 v140,v152,v153
v_cvt_pk_bf16_f32 v141,v154,v155
v_mfma_f32_16x16x32_bf16 a[92:95],a[252:255],v[204:207],a[92:95]
v_cvt_pk_bf16_f32 v142,v156,v157
v_cvt_pk_bf16_f32 v143,v158,v159
v_mfma_f32_16x16x32_bf16 a[92:95],a[248:251],v[200:203],a[92:95]
v_rcp_f32 v20,v20
v_smfmac_f32_16x16x64_bf16 v[56:59],v[28:31],v[200:207],v27
v_accvgpr_read_b32 v64,a0
v_mfma_f32_16x16x32_bf16 v[168:171],a[128:131],v[132:135],v[36:39]
v_accvgpr_read_b32 v65,a1
v_accvgpr_read_b32 v66,a2
v_mfma_f32_16x16x32_bf16 v[168:171],a[132:135],v[164:167],v[168:171]
v_accvgpr_read_b32 v67,a3
v_mul_f32 v64,v20,v64
v_mfma_f32_16x16x32_bf16 v[168:171],a[136:139],v[196:199],v[168:171]
v_mul_f32 v65,v20,v65
v_cvt_pk_bf16_f32 v64,v64,v65
v_mfma_f32_16x16x32_bf16 v[168:171],a[140:143],v[228:231],v[168:171]
v_mul_f32 v66,v20,v66
v_mul_f32 v67,v20,v67
v_mfma_f32_16x16x32_bf16 v[172:175],a[156:159],v[228:231],v[36:39]
buffer_load_dwordx4 v5,s[24:27],s47 offen offset:1024 nt lds
v_mfma_f32_16x16x32_bf16 v[172:175],a[152:155],v[196:199],v[172:175]
v_cvt_pk_bf16_f32 v65,v66,v67
v_accvgpr_read_b32 v16,a6
v_mfma_f32_16x16x32_bf16 v[172:175],a[148:151],v[164:167],v[172:175]
v_accvgpr_read_b32 v17,a7
v_accvgpr_read_b32 v66,a4
v_mfma_f32_16x16x32_bf16 v[172:175],a[144:147],v[132:135],v[172:175]
v_accvgpr_read_b32 v67,a5
v_cmp_nlt_f32 s[56:57],v48,s63
v_mfma_f32_16x16x32_bf16 v[184:187],a[160:163],v[132:135],v[36:39]
v_accvgpr_read_b32 v68,a8
v_accvgpr_read_b32 v69,a9
v_accvgpr_read_b32 v70,a10
v_accvgpr_read_b32 v71,a11
v_accvgpr_read_b32 v12,a12
v_accvgpr_read_b32 v13,a13
v_accvgpr_read_b32 v14,a14
v_accvgpr_read_b32 v15,a15
v_pk_mul_f32 v[68:69],v[20:21],v[68:69] op_sel:[0,0] op_sel_hi:[0,1]
v_pk_mul_f32 v[70:71],v[20:21],v[70:71] op_sel:[0,0] op_sel_hi:[0,1]
v_pk_mul_f32 v[12:13],v[20:21],v[12:13] op_sel:[0,0] op_sel_hi:[0,1]
v_pk_mul_f32 v[14:15],v[20:21],v[14:15] op_sel:[0,0] op_sel_hi:[0,1]
v_cvt_pk_bf16_f32 v68,v68,v69
v_cvt_pk_bf16_f32 v69,v70,v71
v_cvt_pk_bf16_f32 v70,v12,v13
)A"
R"A(v_cvt_pk_bf16_f32 v71,v14,v15
v_mfma_f32_16x16x32_bf16 v[184:187],a[164:167],v[164:167],v[184:187]
v_accvgpr_read_b32 v72,a16
v_accvgpr_read_b32 v73,a17
v_accvgpr_read_b32 v74,a18
v_accvgpr_read_b32 v75,a19
v_accvgpr_read_b32 v12,a20
v_accvgpr_read_b32 v13,a21
v_accvgpr_read_b32 v14,a22
v_accvgpr_read_b32 v15,a23
v_pk_mul_f32 v[72:73],v[20:21],v[72:73] op_sel:[0,0] op_sel_hi:[0,1]
v_pk_mul_f32 v[74:75],v[20:21],v[74:75] op_sel:[0,0] op_sel_hi:[0,1]
v_pk_mul_f32 v[12:13],v[20:21],v[12:13] op_sel:[0,0] op_sel_hi:[0,1]
v_pk_mul_f32 v[14:15],v[20:21],v[14:15] op_sel:[0,0] op_sel_hi:[0,1]
v_cvt_pk_bf16_f32 v72,v72,v73
v_cvt_pk_bf16_f32 v73,v74,v75
v_cvt_pk_bf16_f32 v74,v12,v13
v_cvt_pk_bf16_f32 v75,v14,v15
v_mfma_f32_16x16x32_bf16 v[184:187],a[168:171],v[196:199],v[184:187]
v_accvgpr_read_b32 v76,a24
v_accvgpr_read_b32 v77,a25
v_accvgpr_read_b32 v78,a26
v_accvgpr_read_b32 v79,a27
v_accvgpr_read_b32 v12,a28
v_accvgpr_read_b32 v13,a29
v_accvgpr_read_b32 v14,a30
v_accvgpr_read_b32 v15,a31
v_pk_mul_f32 v[76:77],v[20:21],v[76:77] op_sel:[0,0] op_sel_hi:[0,1]
v_pk_mul_f32 v[78:79],v[20:21],v[78:79] op_sel:[0,0] op_sel_hi:[0,1]
v_pk_mul_f32 v[12:13],v[20:21],v[12:13] op_sel:[0,0] op_sel_hi:[0,1]
v_pk_mul_f32 v[14:15],v[20:21],v[14:15] op_sel:[0,0] op_sel_hi:[0,1]
v_cvt_pk_bf16_f32 v76,v76,v77
v_cvt_pk_bf16_f32 v77,v78,v79
v_cvt_pk_bf16_f32 v78,v12,v13
v_cvt_pk_bf16_f32 v79,v14,v15
v_mfma_f32_16x16x32_bf16 v[184:187],a[172:175],v[228:231],v[184:187]
v_max3_f32 v44,v44,v45,v46
v_max3_f32 v44,v44,v47,s101
v_mfma_f32_16x16x32_bf16 v[188:191],a[188:191],v[228:231],v[36:39]
v_mov_b32 v48,0
v_mov_b32 v49,0
v_mfma_f32_16x16x32_bf16 v[188:191],a[184:187],v[196:199],v[188:191]
v_mov_b32 v50,0
v_mov_b32 v51,0
v_mfma_f32_16x16x32_bf16 v[188:191],a[180:183],v[164:167],v[188:191]
v_sub_f32 v47,0,v44
v_sub_f32 v46,0,v44
v_mfma_f32_16x16x32_bf16 v[188:191],a[176:179],v[132:135],v[188:191]
v_sub_f32 v45,0,v44
v_sub_f32 v44,0,v44
v_mfma_f32_16x16x32_bf16 a[96:99],a[192:195],v[232:235],a[96:99]
ds_read_b64_tr_b16 a[192:193],v2
ds_read_b64_tr_b16 a[194:195],v2 offset:4096
v_mfma_f32_16x16x32_bf16 a[96:99],a[196:199],v[236:239],a[96:99]
ds_read_b64_tr_b16 a[196:197],v2 offset:8192
v_add_f32 v52,v53,v52
v_mfma_f32_16x16x32_bf16 a[100:103],a[204:207],v[236:239],a[100:103]
buffer_load_dwordx4 v4,s[24:27],s47 offen offset:2048 nt lds
v_mfma_f32_16x16x32_bf16 a[100:103],a[200:203],v[232:235],a[100:103]
ds_read_b64_tr_b16 a[198:199],v2 offset:12288
ds_read_b64_tr_b16 a[204:205],v3 offset:8192
v_mfma_f32_16x16x32_bf16 a[104:107],a[208:211],v[232:235],a[104:107]
ds_read_b64_tr_b16 a[206:207],v3 offset:12288
v_max_f32 v21,0x00800000,v52
v_mfma_f32_16x16x32_bf16 a[104:107],a[212:215],v[236:239],a[104:107]
ds_read_b64_tr_b16 a[200:201],v3
v_accvgpr_read_b32 v80,a32
v_mfma_f32_16x16x32_bf16 a[108:111],a[220:223],v[236:239],a[108:111]
ds_read_b64_tr_b16 a[202:203],v3 offset:4096
v_accvgpr_read_b32 v81,a33
v_mfma_f32_16x16x32_bf16 a[108:111],a[216:219],v[232:235],a[108:111]
ds_read_b64_tr_b16 a[208:209],v2 offset:256
ds_read_b64_tr_b16 a[210:211],v2 offset:4352
v_mfma_f32_16x16x32_bf16 a[112:115],a[224:227],v[232:235],a[112:115]
ds_read_b64_tr_b16 a[212:213],v2 offset:8448
v_accvgpr_read_b32 v82,a34
v_mfma_f32_16x16x32_bf16 a[112:115],a[228:231],v[236:239],a[112:115]
ds_read_b64_tr_b16 a[214:215],v2 offset:12544
v_accvgpr_read_b32 v83,a35
v_mfma_f32_16x16x32_bf16 a[116:119],a[236:239],v[236:239],a[116:119]
ds_read_b64_tr_b16 a[220:221],v3 offset:8448
v_accvgpr_read_b32 v12,a38
v_mfma_f32_16x16x32_bf16 a[116:119],a[232:235],v[232:235],a[116:119]
ds_read_b64_tr_b16 a[222:223],v3 offset:12544
ds_read_b64_tr_b16 a[216:217],v3 offset:256
v_mfma_f32_16x16x32_bf16 a[120:123],a[240:243],v[232:235],a[120:123]
ds_read_b64_tr_b16 a[218:219],v3 offset:4352
v_accvgpr_read_b32 v13,a39
v_mfma_f32_16x16x32_bf16 a[120:123],a[244:247],v[236:239],a[120:123]
ds_read_b64_tr_b16 a[224:225],v2 offset:512
v_add_u32 v1,s48,v1
v_mfma_f32_16x16x32_bf16 a[124:127],a[252:255],v[236:239],a[124:127]
ds_read_b64_tr_b16 a[226:227],v2 offset:4608
v_mov_b32 v53,0
v_mfma_f32_16x16x32_bf16 a[124:127],a[248:251],v[232:235],a[124:127]
ds_read_b64_tr_b16 a[228:229],v2 offset:8704
ds_read_b64_tr_b16 a[230:231],v2 offset:12800
v_smfmac_f32_16x16x64_bf16 v[60:63],v[28:31],v[232:239],v27
ds_read_b64_tr_b16 a[236:237],v3 offset:8704
v_mfma_f32_16x16x32_bf16 v[200:203],a[128:131],v[144:147],v[40:43]
buffer_load_dwordx4 v5,s[24:27],s47 offen offset:3072 nt lds
v_mfma_f32_16x16x32_bf16 v[200:203],a[132:135],v[176:179],v[200:203]
s_add_u32 s47,s47,16384
ds_read_b64_tr_b16 a[238:239],v3 offset:12800
ds_read_b64_tr_b16 a[232:233],v3 offset:512
v_mfma_f32_16x16x32_bf16 v[200:203],a[136:139],v[208:211],v[200:203]
s_and_b32 s47,s47,0x3ffff
s_or_b32 s47,s47,s46
ds_read_b64_tr_b16 a[234:235],v3 offset:4608
v_mfma_f32_16x16x32_bf16 v[200:203],a[140:143],v[240:243],v[200:203]
s_and_b32 s46,s54,0xfffc0000
ds_read_b64_tr_b16 a[240:241],v2 offset:768
v_mov_b32 v54,0
v_mfma_f32_16x16x32_bf16 v[204:207],a[156:159],v[240:243],v[40:43]
ds_read_b64_tr_b16 a[242:243],v2 offset:4864
v_mov_b32 v55,0
v_mfma_f32_16x16x32_bf16 v[204:207],a[152:155],v[208:211],v[204:207]
ds_read_b64_tr_b16 a[244:245],v2 offset:8960
ds_read_b64_tr_b16 a[246:247],v2 offset:13056
v_mfma_f32_16x16x32_bf16 v[204:207],a[148:151],v[176:179],v[204:207]
ds_read_b64_tr_b16 a[252:253],v3 offset:8960
v_add_f32 v56,v57,v56
v_mfma_f32_16x16x32_bf16 v[204:207],a[144:147],v[144:147],v[204:207]
ds_read_b64_tr_b16 a[254:255],v3 offset:13056
v_max_f32 v22,0x00800000,v56
v_mfma_f32_16x16x32_bf16 v[216:219],a[160:163],v[144:147],v[40:43]
ds_read_b64_tr_b16 a[248:249],v3 offset:768
v_accvgpr_read_b32 v96,a64
v_mfma_f32_16x16x32_bf16 v[216:219],a[164:167],v[176:179],v[216:219]
ds_read_b64_tr_b16 a[250:251],v3 offset:4864
v_accvgpr_read_b32 v97,a65
v_mfma_f32_16x16x32_bf16 v[216:219],a[168:171],v[208:211],v[216:219]
v_exp_f32 v168,v168
v_mfma_f32_16x16x32_bf16 v[216:219],a[172:175],v[240:243],v[216:219]
v_exp_f32 v169,v169
v_mfma_f32_16x16x32_bf16 v[220:223],a[188:191],v[240:243],v[40:43]
v_exp_f32 v170,v170
v_mfma_f32_16x16x32_bf16 v[220:223],a[184:187],v[208:211],v[220:223]
v_exp_f32 v171,v171
v_mfma_f32_16x16x32_bf16 v[220:223],a[180:183],v[176:179],v[220:223]
v_cvt_pk_bf16_f32 v168,v168,v169
v_cvt_pk_bf16_f32 v169,v170,v171
v_mfma_f32_16x16x32_bf16 v[220:223],a[176:179],v[144:147],v[220:223]
s_waitcnt lgkmcnt(2)
v_exp_f32 v172,v172
v_mfma_f32_16x16x32_bf16 a[0:3],a[192:195],v[136:139],0
v_exp_f32 v173,v173
v_mfma_f32_16x16x32_bf16 a[0:3],a[196:199],v[140:143],a[0:3]
v_exp_f32 v174,v174
v_mfma_f32_16x16x32_bf16 a[4:7],a[204:207],v[140:143],0
v_exp_f32 v175,v175
v_mfma_f32_16x16x32_bf16 a[4:7],a[200:203],v[136:139],a[4:7]
s_memrealtime s[76:77]
v_cvt_pk_bf16_f32 v170,v172,v173
v_cvt_pk_bf16_f32 v171,v174,v175
v_mfma_f32_16x16x32_bf16 a[8:11],a[208:211],v[136:139],0
v_exp_f32 v184,v184
v_mfma_f32_16x16x32_bf16 a[8:11],a[212:215],v[140:143],a[8:11]
v_exp_f32 v185,v185
v_mfma_f32_16x16x32_bf16 a[12:15],a[220:223],v[140:143],0
v_exp_f32 v186,v186
v_mfma_f32_16x16x32_bf16 a[12:15],a[216:219],v[136:139],a[12:15]
v_exp_f32 v187,v187
v_mfma_f32_16x16x32_bf16 a[16:19],a[224:227],v[136:139],0
v_exp_f32 v188,v188
v_mfma_f32_16x16x32_bf16 a[16:19],a[228:231],v[140:143],a[16:19]
v_exp_f32 v189,v189
v_mfma_f32_16x16x32_bf16 a[20:23],a[236:239],v[140:143],0
s_waitcnt lgkmcnt(0)
v_exp_f32 v190,v190
v_mfma_f32_16x16x32_bf16 a[20:23],a[232:235],v[136:139],a[20:23]
s_sub_u32 s79,s78,s76
v_exp_f32 v191,v191
v_mfma_f32_16x16x32_bf16 a[24:27],a[240:243],v[136:139],0
s_add_u32 s78,s76,s60
v_rcp_f32 v21,v21
v_mfma_f32_16x16x32_bf16 a[24:27],a[244:247],v[140:143],a[24:27]
s_cmp_gt_i32 s79,s95
s_cbranch_scc1 L_ps5_%=
L_pr5_%=:
v_cvt_pk_bf16_f32 v172,v184,v185
v_mfma_f32_16x16x32_bf16 a[28:31],a[252:255],v[140:143],0
s_lshr_b32 s60,s94,2
v_cvt_pk_bf16_f32 v173,v186,v187
v_cvt_pk_bf16_f32 v174,v188,v189
v_mfma_f32_16x16x32_bf16 a[28:31],a[248:251],v[136:139],a[28:31]
s_max_i32 s60,s60,128
v_cvt_pk_bf16_f32 v175,v190,v191
v_mul_f32 v80,v21,v80
v_smfmac_f32_16x16x64_bf16 v[48:51],v[28:31],v[136:143],v27
v_mul_f32 v81,v21,v81
v_mfma_f32_16x16x32_bf16 v[232:235],a[128:131],v[148:151],v[44:47]
s_waitcnt vmcnt(8)
v_cvt_pk_bf16_f32 v80,v80,v81
v_mul_f32 v82,v21,v82
v_mfma_f32_16x16x32_bf16 v[232:235],a[132:135],v[180:183],v[232:235]
s_barrier
s_add_u32 m0,65536,s51
v_mul_f32 v83,v21,v83
v_mfma_f32_16x16x32_bf16 v[232:235],a[136:139],v[212:215],v[232:235]
v_cvt_pk_bf16_f32 v81,v82,v83
v_accvgpr_read_b32 v82,a36
v_mfma_f32_16x16x32_bf16 v[232:235],a[140:143],v[244:247],v[232:235]
v_accvgpr_read_b32 v83,a37
v_mul_f32 v82,v21,v82
v_mfma_f32_16x16x32_bf16 v[236:239],a[156:159],v[244:247],v[44:47]
buffer_load_dwordx4 v4,s[20:23],s47 offen nt lds
v_mfma_f32_16x16x32_bf16 v[236:239],a[152:155],v[212:215],v[236:239]
v_mul_f32 v83,v21,v83
v_mul_f32 v12,v21,v12
v_mfma_f32_16x16x32_bf16 v[236:239],a[148:151],v[180:183],v[236:239]
v_mul_f32 v13,v21,v13
v_cvt_pk_bf16_f32 v82,v82,v83
v_mfma_f32_16x16x32_bf16 v[236:239],a[144:147],v[148:151],v[236:239]
ds_read_b128 a[128:131],v1
v_cvt_pk_bf16_f32 v83,v12,v13
v_mfma_f32_16x16x32_bf16 v[248:251],a[160:163],v[148:151],v[44:47]
v_accvgpr_read_b32 v84,a40
v_accvgpr_read_b32 v85,a41
v_accvgpr_read_b32 v86,a42
v_accvgpr_read_b32 v87,a43
v_accvgpr_read_b32 v12,a44
v_accvgpr_read_b32 v13,a45
v_accvgpr_read_b32 v14,a46
v_accvgpr_read_b32 v15,a47
v_pk_mul_f32 v[84:85],v[20:21],v[84:85] op_sel:[1,0] op_sel_hi:[1,1]
v_pk_mul_f32 v[86:87],v[20:21],v[86:87] op_sel:[1,0] op_sel_hi:[1,1]
v_pk_mul_f32 v[12:13],v[20:21],v[12:13] op_sel:[1,0] op_sel_hi:[1,1]
v_pk_mul_f32 v[14:15],v[20:21],v[14:15] op_sel:[1,0] op_sel_hi:[1,1]
v_cvt_pk_bf16_f32 v84,v84,v85
v_cvt_pk_bf16_f32 v85,v86,v87
v_cvt_pk_bf16_f32 v86,v12,v13
v_cvt_pk_bf16_f32 v87,v14,v15
v_mfma_f32_16x16x32_bf16 v[248:251],a[164:167],v[180:183],v[248:251]
v_accvgpr_read_b32 v88,a48
v_accvgpr_read_b32 v89,a49
v_accvgpr_read_b32 v90,a50
v_accvgpr_read_b32 v91,a51
v_accvgpr_read_b32 v12,a52
v_accvgpr_read_b32 v13,a53
v_accvgpr_read_b32 v14,a54
v_accvgpr_read_b32 v15,a55
v_pk_mul_f32 v[88:89],v[20:21],v[88:89] op_sel:[1,0] op_sel_hi:[1,1]
v_pk_mul_f32 v[90:91],v[20:21],v[90:91] op_sel:[1,0] op_sel_hi:[1,1]
v_pk_mul_f32 v[12:13],v[20:21],v[12:13] op_sel:[1,0] op_sel_hi:[1,1]
v_pk_mul_f32 v[14:15],v[20:21],v[14:15] op_sel:[1,0] op_sel_hi:[1,1]
v_cvt_pk_bf16_f32 v88,v88,v89
v_cvt_pk_bf16_f32 v89,v90,v91
v_cvt_pk_bf16_f32 v90,v12,v13
v_cvt_pk_bf16_f32 v91,v14,v15
v_mfma_f32_16x16x32_bf16 v[248:251],a[168:171],v[212:215],v[248:251]
ds_read_b128 a[132:135],v1 offset:256
v_accvgpr_read_b32 v98,a66
v_mfma_f32_16x16x32_bf16 v[248:251],a[172:175],v[244:247],v[248:251]
v_accvgpr_read_b32 v92,a56
v_accvgpr_read_b32 v93,a57
v_accvgpr_read_b32 v94,a58
v_accvgpr_read_b32 v95,a59
v_accvgpr_read_b32 v12,a60
v_accvgpr_read_b32 v13,a61
v_accvgpr_read_b32 v14,a62
v_accvgpr_read_b32 v15,a63
v_pk_mul_f32 v[92:93],v[20:21],v[92:93] op_sel:[1,0] op_sel_hi:[1,1]
v_pk_mul_f32 v[94:95],v[20:21],v[94:95] op_sel:[1,0] op_sel_hi:[1,1]
v_pk_mul_f32 v[12:13],v[20:21],v[12:13] op_sel:[1,0] op_sel_hi:[1,1]
v_pk_mul_f32 v[14:15],v[20:21],v[14:15] op_sel:[1,0] op_sel_hi:[1,1]
v_cvt_pk_bf16_f32 v92,v92,v93
v_cvt_pk_bf16_f32 v93,v94,v95
v_cvt_pk_bf16_f32 v94,v12,v13
v_cvt_pk_bf16_f32 v95,v14,v15
v_mfma_f32_16x16x32_bf16 v[252:255],a[188:191],v[244:247],v[44:47]
ds_read_b128 a[136:139],v1 offset:512
v_accvgpr_read_b32 v99,a67
v_mfma_f32_16x16x32_bf16 v[252:255],a[184:187],v[212:215],v[252:255]
v_cmp_nlt_f32 s[58:59],v52,s63
s_nop 1
s_or_b64 s[56:57],s[56:57],s[58:59]
v_mfma_f32_16x16x32_bf16 v[252:255],a[180:183],v[180:183],v[252:255]
ds_read_b128 a[140:143],v1 offset:768
v_mov_b32 v52,0
v_mfma_f32_16x16x32_bf16 v[252:255],a[176:179],v[148:151],v[252:255]
v_exp_f32 v200,v200
v_mfma_f32_16x16x32_bf16 a[32:35],a[192:195],v[168:171],0
ds_read_b128 a[156:159],v1 offset:4864
v_accvgpr_read_b32 v14,a70
v_mfma_f32_16x16x32_bf16 a[32:35],a[196:199],v[172:175],a[32:35]
v_exp_f32 v201,v201
v_mfma_f32_16x16x32_bf16 a[36:39],a[204:207],v[172:175],0
)A"
R"A(buffer_load_dwordx4 v4,s[20:23],s47 offen offset:1024 nt lds
v_mfma_f32_16x16x32_bf16 a[36:39],a[200:203],v[168:171],a[36:39]
ds_read_b128 a[152:155],v1 offset:4608
v_cvt_pk_bf16_f32 v200,v200,v201
v_mfma_f32_16x16x32_bf16 a[40:43],a[208:211],v[168:171],0
v_exp_f32 v202,v202
v_mfma_f32_16x16x32_bf16 a[40:43],a[212:215],v[172:175],a[40:43]
ds_read_b128 a[148:151],v1 offset:4352
v_accvgpr_read_b32 v15,a71
v_mfma_f32_16x16x32_bf16 a[44:47],a[220:223],v[172:175],0
v_exp_f32 v203,v203
v_mfma_f32_16x16x32_bf16 a[44:47],a[216:219],v[168:171],a[44:47]
ds_read_b128 a[144:147],v1 offset:4096
v_cvt_pk_bf16_f32 v201,v202,v203
v_mfma_f32_16x16x32_bf16 a[48:51],a[224:227],v[168:171],0
v_exp_f32 v204,v204
v_mfma_f32_16x16x32_bf16 a[48:51],a[228:231],v[172:175],a[48:51]
ds_read_b128 a[160:163],v1 offset:8192
v_mov_b32 v57,0
v_mfma_f32_16x16x32_bf16 a[52:55],a[236:239],v[172:175],0
v_exp_f32 v205,v205
v_mfma_f32_16x16x32_bf16 a[52:55],a[232:235],v[168:171],a[52:55]
ds_read_b128 a[164:167],v1 offset:8448
v_cvt_pk_bf16_f32 v202,v204,v205
v_mfma_f32_16x16x32_bf16 a[56:59],a[240:243],v[168:171],0
v_exp_f32 v206,v206
v_mfma_f32_16x16x32_bf16 a[56:59],a[244:247],v[172:175],a[56:59]
ds_read_b128 a[168:171],v1 offset:8704
v_mov_b32 v58,0
v_mfma_f32_16x16x32_bf16 a[60:63],a[252:255],v[172:175],0
v_exp_f32 v207,v207
v_mfma_f32_16x16x32_bf16 a[60:63],a[248:251],v[168:171],a[60:63]
ds_read_b128 a[172:175],v1 offset:8960
v_cvt_pk_bf16_f32 v203,v206,v207
v_smfmac_f32_16x16x64_bf16 v[52:55],v[28:31],v[168:175],v27
s_waitcnt lgkmcnt(4)
v_mov_b32 v59,0
v_mfma_f32_16x16x32_bf16 v[136:139],a[128:131],v[128:131],v[32:35]
buffer_load_dwordx4 v5,s[20:23],s47 offen offset:2048 nt lds
v_mfma_f32_16x16x32_bf16 v[136:139],a[132:135],v[160:163],v[136:139]
ds_read_b128 a[188:191],v1 offset:13056
v_add_f32 v60,v61,v60
v_mfma_f32_16x16x32_bf16 v[136:139],a[136:139],v[192:195],v[136:139]
v_exp_f32 v216,v216
v_mfma_f32_16x16x32_bf16 v[136:139],a[140:143],v[224:227],v[136:139]
ds_read_b128 a[184:187],v1 offset:12800
v_max_f32 v23,0x00800000,v60
v_mfma_f32_16x16x32_bf16 v[140:143],a[156:159],v[224:227],v[32:35]
v_exp_f32 v217,v217
v_mfma_f32_16x16x32_bf16 v[140:143],a[152:155],v[192:195],v[140:143]
ds_read_b128 a[180:183],v1 offset:12544
v_cvt_pk_bf16_f32 v204,v216,v217
v_mfma_f32_16x16x32_bf16 v[140:143],a[148:151],v[160:163],v[140:143]
ds_read_b128 a[176:179],v1 offset:12288
v_mfma_f32_16x16x32_bf16 v[140:143],a[144:147],v[128:131],v[140:143]
s_waitcnt lgkmcnt(4)
v_exp_f32 v218,v218
v_mfma_f32_16x16x32_bf16 v[152:155],a[160:163],v[128:131],v[32:35]
v_exp_f32 v219,v219
v_mfma_f32_16x16x32_bf16 v[152:155],a[164:167],v[160:163],v[152:155]
v_exp_f32 v220,v220
v_mfma_f32_16x16x32_bf16 v[152:155],a[168:171],v[192:195],v[152:155]
v_exp_f32 v221,v221
v_mfma_f32_16x16x32_bf16 v[152:155],a[172:175],v[224:227],v[152:155]
s_waitcnt lgkmcnt(2)
v_exp_f32 v222,v222
v_mfma_f32_16x16x32_bf16 v[156:159],a[188:191],v[224:227],v[32:35]
v_exp_f32 v223,v223
v_mfma_f32_16x16x32_bf16 v[156:159],a[184:187],v[192:195],v[156:159]
s_waitcnt lgkmcnt(1)
v_rcp_f32 v22,v22
v_mfma_f32_16x16x32_bf16 v[156:159],a[180:183],v[160:163],v[156:159]
s_waitcnt lgkmcnt(0)
v_cvt_pk_bf16_f32 v205,v218,v219
v_cvt_pk_bf16_f32 v206,v220,v221
v_cvt_pk_bf16_f32 v207,v222,v223
v_mfma_f32_16x16x32_bf16 v[156:159],a[176:179],v[128:131],v[156:159]
v_mul_f32 v96,v22,v96
v_mul_f32 v97,v22,v97
v_mfma_f32_16x16x32_bf16 a[64:67],a[192:195],v[200:203],0
buffer_load_dwordx4 v5,s[20:23],s47 offen offset:3072 nt lds
v_cvt_pk_bf16_f32 v96,v96,v97
v_mul_f32 v98,v22,v98
v_mul_f32 v99,v22,v99
v_cvt_pk_bf16_f32 v97,v98,v99
v_mfma_f32_16x16x32_bf16 a[64:67],a[196:199],v[204:207],a[64:67]
v_accvgpr_read_b32 v98,a68
v_accvgpr_read_b32 v99,a69
v_mul_f32 v98,v22,v98
v_mul_f32 v99,v22,v99
v_mfma_f32_16x16x32_bf16 a[68:71],a[204:207],v[204:207],0
v_mul_f32 v14,v22,v14
v_mul_f32 v15,v22,v15
v_cvt_pk_bf16_f32 v98,v98,v99
v_cvt_pk_bf16_f32 v99,v14,v15
v_mfma_f32_16x16x32_bf16 a[68:71],a[200:203],v[200:203],a[68:71]
v_accvgpr_read_b32 v100,a72
v_accvgpr_read_b32 v101,a73
v_accvgpr_read_b32 v102,a74
v_accvgpr_read_b32 v103,a75
v_accvgpr_read_b32 v12,a76
v_accvgpr_read_b32 v13,a77
v_accvgpr_read_b32 v14,a78
v_accvgpr_read_b32 v15,a79
v_pk_mul_f32 v[100:101],v[22:23],v[100:101] op_sel:[0,0] op_sel_hi:[0,1]
v_pk_mul_f32 v[102:103],v[22:23],v[102:103] op_sel:[0,0] op_sel_hi:[0,1]
v_pk_mul_f32 v[12:13],v[22:23],v[12:13] op_sel:[0,0] op_sel_hi:[0,1]
v_pk_mul_f32 v[14:15],v[22:23],v[14:15] op_sel:[0,0] op_sel_hi:[0,1]
v_cvt_pk_bf16_f32 v100,v100,v101
v_cvt_pk_bf16_f32 v101,v102,v103
v_cvt_pk_bf16_f32 v102,v12,v13
v_cvt_pk_bf16_f32 v103,v14,v15
v_mfma_f32_16x16x32_bf16 a[72:75],a[208:211],v[200:203],0
v_accvgpr_read_b32 v104,a80
v_accvgpr_read_b32 v105,a81
v_accvgpr_read_b32 v106,a82
v_accvgpr_read_b32 v107,a83
v_accvgpr_read_b32 v12,a84
v_accvgpr_read_b32 v13,a85
v_accvgpr_read_b32 v14,a86
v_accvgpr_read_b32 v15,a87
v_pk_mul_f32 v[104:105],v[22:23],v[104:105] op_sel:[0,0] op_sel_hi:[0,1]
v_pk_mul_f32 v[106:107],v[22:23],v[106:107] op_sel:[0,0] op_sel_hi:[0,1]
v_pk_mul_f32 v[12:13],v[22:23],v[12:13] op_sel:[0,0] op_sel_hi:[0,1]
v_pk_mul_f32 v[14:15],v[22:23],v[14:15] op_sel:[0,0] op_sel_hi:[0,1]
v_cvt_pk_bf16_f32 v104,v104,v105
v_cvt_pk_bf16_f32 v105,v106,v107
v_cvt_pk_bf16_f32 v106,v12,v13
v_cvt_pk_bf16_f32 v107,v14,v15
v_mfma_f32_16x16x32_bf16 a[72:75],a[212:215],v[204:207],a[72:75]
v_accvgpr_read_b32 v108,a88
v_accvgpr_read_b32 v109,a89
v_accvgpr_read_b32 v110,a90
v_accvgpr_read_b32 v111,a91
v_accvgpr_read_b32 v12,a92
v_accvgpr_read_b32 v13,a93
v_accvgpr_read_b32 v14,a94
v_accvgpr_read_b32 v15,a95
v_pk_mul_f32 v[108:109],v[22:23],v[108:109] op_sel:[0,0] op_sel_hi:[0,1]
v_pk_mul_f32 v[110:111],v[22:23],v[110:111] op_sel:[0,0] op_sel_hi:[0,1]
v_pk_mul_f32 v[12:13],v[22:23],v[12:13] op_sel:[0,0] op_sel_hi:[0,1]
v_pk_mul_f32 v[14:15],v[22:23],v[14:15] op_sel:[0,0] op_sel_hi:[0,1]
v_cvt_pk_bf16_f32 v108,v108,v109
v_cvt_pk_bf16_f32 v109,v110,v111
v_cvt_pk_bf16_f32 v110,v12,v13
v_cvt_pk_bf16_f32 v111,v14,v15
v_mfma_f32_16x16x32_bf16 a[76:79],a[220:223],v[204:207],0
v_cmp_nlt_f32 s[58:59],v56,s63
s_nop 1
s_or_b64 s[56:57],s[56:57],s[58:59]
v_mfma_f32_16x16x32_bf16 a[76:79],a[216:219],v[200:203],a[76:79]
v_mov_b32 v56,0
v_accvgpr_read_b32 v112,a96
v_mfma_f32_16x16x32_bf16 a[80:83],a[224:227],v[200:203],0
s_add_u32 m0,65536,s52
v_exp_f32 v232,v232
v_mfma_f32_16x16x32_bf16 a[80:83],a[228:231],v[204:207],a[80:83]
v_exp_f32 v233,v233
v_mfma_f32_16x16x32_bf16 a[84:87],a[236:239],v[204:207],0
v_exp_f32 v234,v234
v_mfma_f32_16x16x32_bf16 a[84:87],a[232:235],v[200:203],a[84:87]
buffer_load_dwordx4 v4,s[24:27],s47 offen nt lds
v_mfma_f32_16x16x32_bf16 a[88:91],a[240:243],v[200:203],0
v_exp_f32 v235,v235
v_mfma_f32_16x16x32_bf16 a[88:91],a[244:247],v[204:207],a[88:91]
v_cvt_pk_bf16_f32 v232,v232,v233
v_cvt_pk_bf16_f32 v233,v234,v235
v_mfma_f32_16x16x32_bf16 a[92:95],a[252:255],v[204:207],0
v_exp_f32 v236,v236
v_mfma_f32_16x16x32_bf16 a[92:95],a[248:251],v[200:203],a[92:95]
v_exp_f32 v237,v237
v_smfmac_f32_16x16x64_bf16 v[56:59],v[28:31],v[200:207],v27
v_cvt_pk_bf16_f32 v234,v236,v237
v_mfma_f32_16x16x32_bf16 v[168:171],a[128:131],v[132:135],v[36:39]
v_exp_f32 v238,v238
v_mfma_f32_16x16x32_bf16 v[168:171],a[132:135],v[164:167],v[168:171]
v_exp_f32 v239,v239
v_mfma_f32_16x16x32_bf16 v[168:171],a[136:139],v[196:199],v[168:171]
v_cvt_pk_bf16_f32 v235,v238,v239
v_accvgpr_read_b32 v113,a97
v_mfma_f32_16x16x32_bf16 v[168:171],a[140:143],v[228:231],v[168:171]
v_exp_f32 v248,v248
v_mfma_f32_16x16x32_bf16 v[172:175],a[156:159],v[228:231],v[36:39]
v_exp_f32 v249,v249
v_mfma_f32_16x16x32_bf16 v[172:175],a[152:155],v[196:199],v[172:175]
v_exp_f32 v250,v250
v_mfma_f32_16x16x32_bf16 v[172:175],a[148:151],v[164:167],v[172:175]
v_exp_f32 v251,v251
v_mfma_f32_16x16x32_bf16 v[172:175],a[144:147],v[132:135],v[172:175]
v_exp_f32 v252,v252
v_mfma_f32_16x16x32_bf16 v[184:187],a[160:163],v[132:135],v[36:39]
buffer_load_dwordx4 v5,s[24:27],s47 offen offset:1024 nt lds
v_mfma_f32_16x16x32_bf16 v[184:187],a[164:167],v[164:167],v[184:187]
v_exp_f32 v253,v253
v_mfma_f32_16x16x32_bf16 v[184:187],a[168:171],v[196:199],v[184:187]
v_exp_f32 v254,v254
v_mfma_f32_16x16x32_bf16 v[184:187],a[172:175],v[228:231],v[184:187]
v_exp_f32 v255,v255
v_mfma_f32_16x16x32_bf16 v[188:191],a[188:191],v[228:231],v[36:39]
v_cvt_pk_bf16_f32 v236,v248,v249
v_cvt_pk_bf16_f32 v237,v250,v251
v_mfma_f32_16x16x32_bf16 v[188:191],a[184:187],v[196:199],v[188:191]
v_cvt_pk_bf16_f32 v238,v252,v253
v_cvt_pk_bf16_f32 v239,v254,v255
v_mfma_f32_16x16x32_bf16 v[188:191],a[180:183],v[164:167],v[188:191]
v_rcp_f32 v23,v23
v_mfma_f32_16x16x32_bf16 v[188:191],a[176:179],v[132:135],v[188:191]
v_accvgpr_read_b32 v114,a98
v_accvgpr_read_b32 v115,a99
v_mfma_f32_16x16x32_bf16 a[96:99],a[192:195],v[232:235],0
v_mul_f32 v66,v20,v66
v_mul_f32 v67,v20,v67
v_mul_f32 v112,v23,v112
v_mul_f32 v113,v23,v113
v_mul_f32 v16,v20,v16
v_mul_f32 v17,v20,v17
v_cvt_pk_bf16_f32 v66,v66,v67
v_cvt_pk_bf16_f32 v112,v112,v113
v_mul_f32 v114,v23,v114
v_mul_f32 v115,v23,v115
v_mfma_f32_16x16x32_bf16 a[96:99],a[196:199],v[236:239],a[96:99]
v_cvt_pk_bf16_f32 v67,v16,v17
v_cvt_pk_bf16_f32 v113,v114,v115
v_accvgpr_read_b32 v16,a102
v_accvgpr_read_b32 v17,a103
v_accvgpr_read_b32 v114,a100
v_accvgpr_read_b32 v115,a101
v_mfma_f32_16x16x32_bf16 a[100:103],a[204:207],v[236:239],0
v_accvgpr_read_b32 v116,a104
v_accvgpr_read_b32 v117,a105
v_accvgpr_read_b32 v118,a106
v_accvgpr_read_b32 v119,a107
v_accvgpr_read_b32 v12,a108
v_accvgpr_read_b32 v13,a109
v_accvgpr_read_b32 v14,a110
v_accvgpr_read_b32 v15,a111
v_pk_mul_f32 v[116:117],v[22:23],v[116:117] op_sel:[1,0] op_sel_hi:[1,1]
v_pk_mul_f32 v[118:119],v[22:23],v[118:119] op_sel:[1,0] op_sel_hi:[1,1]
v_pk_mul_f32 v[12:13],v[22:23],v[12:13] op_sel:[1,0] op_sel_hi:[1,1]
v_pk_mul_f32 v[14:15],v[22:23],v[14:15] op_sel:[1,0] op_sel_hi:[1,1]
v_cvt_pk_bf16_f32 v116,v116,v117
v_cvt_pk_bf16_f32 v117,v118,v119
v_cvt_pk_bf16_f32 v118,v12,v13
v_cvt_pk_bf16_f32 v119,v14,v15
v_mfma_f32_16x16x32_bf16 a[100:103],a[200:203],v[232:235],a[100:103]
v_accvgpr_read_b32 v120,a112
v_accvgpr_read_b32 v121,a113
v_accvgpr_read_b32 v122,a114
v_accvgpr_read_b32 v123,a115
v_accvgpr_read_b32 v12,a116
v_accvgpr_read_b32 v13,a117
v_accvgpr_read_b32 v14,a118
v_accvgpr_read_b32 v15,a119
v_pk_mul_f32 v[120:121],v[22:23],v[120:121] op_sel:[1,0] op_sel_hi:[1,1]
v_pk_mul_f32 v[122:123],v[22:23],v[122:123] op_sel:[1,0] op_sel_hi:[1,1]
v_pk_mul_f32 v[12:13],v[22:23],v[12:13] op_sel:[1,0] op_sel_hi:[1,1]
v_pk_mul_f32 v[14:15],v[22:23],v[14:15] op_sel:[1,0] op_sel_hi:[1,1]
v_cvt_pk_bf16_f32 v120,v120,v121
v_cvt_pk_bf16_f32 v121,v122,v123
v_cvt_pk_bf16_f32 v122,v12,v13
v_cvt_pk_bf16_f32 v123,v14,v15
v_mfma_f32_16x16x32_bf16 a[104:107],a[208:211],v[232:235],0
v_accvgpr_read_b32 v124,a120
v_accvgpr_read_b32 v125,a121
v_accvgpr_read_b32 v126,a122
v_accvgpr_read_b32 v127,a123
v_accvgpr_read_b32 v12,a124
v_accvgpr_read_b32 v13,a125
v_accvgpr_read_b32 v14,a126
v_accvgpr_read_b32 v15,a127
v_pk_mul_f32 v[124:125],v[22:23],v[124:125] op_sel:[1,0] op_sel_hi:[1,1]
v_pk_mul_f32 v[126:127],v[22:23],v[126:127] op_sel:[1,0] op_sel_hi:[1,1]
v_pk_mul_f32 v[12:13],v[22:23],v[12:13] op_sel:[1,0] op_sel_hi:[1,1]
v_pk_mul_f32 v[14:15],v[22:23],v[14:15] op_sel:[1,0] op_sel_hi:[1,1]
v_cvt_pk_bf16_f32 v124,v124,v125
v_cvt_pk_bf16_f32 v125,v126,v127
v_cvt_pk_bf16_f32 v126,v12,v13
v_cvt_pk_bf16_f32 v127,v14,v15
v_mfma_f32_16x16x32_bf16 a[104:107],a[212:215],v[236:239],a[104:107]
v_cmp_nlt_f32 s[58:59],v60,s63
s_nop 1
s_or_b64 s[56:57],s[56:57],s[58:59]
v_mfma_f32_16x16x32_bf16 a[108:111],a[220:223],v[236:239],0
buffer_load_dwordx4 v4,s[24:27],s47 offen offset:2048 nt lds
v_mfma_f32_16x16x32_bf16 a[108:111],a[216:219],v[232:235],a[108:111]
v_mov_b32 v60,0
v_mov_b32 v61,0
v_mfma_f32_16x16x32_bf16 a[112:115],a[224:227],v[232:235],0
v_mov_b32 v62,0
v_mov_b32 v63,0
v_mfma_f32_16x16x32_bf16 a[112:115],a[228:231],v[236:239],a[112:115]
v_add_u32 v2,s48,v2
v_add_u32 v3,s48,v3
v_mfma_f32_16x16x32_bf16 a[116:119],a[236:239],v[236:239],0
s_mov_b32 s48,32768
ds_read_b64_tr_b16 a[192:193],v2
)A"
R"A(ds_read_b64_tr_b16 a[194:195],v2 offset:4096
v_mfma_f32_16x16x32_bf16 a[116:119],a[232:235],v[232:235],a[116:119]
ds_read_b64_tr_b16 a[196:197],v2 offset:8192
v_mul_f32 v114,v23,v114
v_mfma_f32_16x16x32_bf16 a[120:123],a[240:243],v[232:235],0
ds_read_b64_tr_b16 a[198:199],v2 offset:12288
v_mul_f32 v115,v23,v115
v_mfma_f32_16x16x32_bf16 a[120:123],a[244:247],v[236:239],a[120:123]
ds_read_b64_tr_b16 a[204:205],v3 offset:8192
v_mul_f32 v16,v23,v16
v_mfma_f32_16x16x32_bf16 a[124:127],a[252:255],v[236:239],0
ds_read_b64_tr_b16 a[206:207],v3 offset:12288
ds_read_b64_tr_b16 a[200:201],v3
v_mfma_f32_16x16x32_bf16 a[124:127],a[248:251],v[232:235],a[124:127]
ds_read_b64_tr_b16 a[202:203],v3 offset:4096
v_mul_f32 v17,v23,v17
v_smfmac_f32_16x16x64_bf16 v[60:63],v[28:31],v[232:239],v27
ds_read_b64_tr_b16 a[208:209],v2 offset:256
v_mfma_f32_16x16x32_bf16 v[200:203],a[128:131],v[144:147],v[40:43]
v_exp_f32 v136,v136
v_mfma_f32_16x16x32_bf16 v[200:203],a[132:135],v[176:179],v[200:203]
v_exp_f32 v137,v137
v_mfma_f32_16x16x32_bf16 v[200:203],a[136:139],v[208:211],v[200:203]
ds_read_b64_tr_b16 a[210:211],v2 offset:4352
v_cvt_pk_bf16_f32 v136,v136,v137
v_mfma_f32_16x16x32_bf16 v[200:203],a[140:143],v[240:243],v[200:203]
buffer_load_dwordx4 v5,s[24:27],s47 offen offset:3072 nt lds
v_mfma_f32_16x16x32_bf16 v[204:207],a[156:159],v[240:243],v[40:43]
s_add_u32 s47,s47,16384
v_exp_f32 v138,v138
v_mfma_f32_16x16x32_bf16 v[204:207],a[152:155],v[208:211],v[204:207]
s_and_b32 s47,s47,0x3ffff
v_exp_f32 v139,v139
v_mfma_f32_16x16x32_bf16 v[204:207],a[148:151],v[176:179],v[204:207]
s_or_b32 s47,s47,s46
v_exp_f32 v140,v140
v_mfma_f32_16x16x32_bf16 v[204:207],a[144:147],v[144:147],v[204:207]
v_exp_f32 v141,v141
v_mfma_f32_16x16x32_bf16 v[216:219],a[160:163],v[144:147],v[40:43]
v_exp_f32 v142,v142
v_mfma_f32_16x16x32_bf16 v[216:219],a[164:167],v[176:179],v[216:219]
v_exp_f32 v143,v143
v_mfma_f32_16x16x32_bf16 v[216:219],a[168:171],v[208:211],v[216:219]
ds_read_b64_tr_b16 a[212:213],v2 offset:8448
ds_read_b64_tr_b16 a[214:215],v2 offset:12544
v_mfma_f32_16x16x32_bf16 v[216:219],a[172:175],v[240:243],v[216:219]
v_cvt_pk_bf16_f32 v137,v138,v139
v_cvt_pk_bf16_f32 v138,v140,v141
v_mfma_f32_16x16x32_bf16 v[220:223],a[188:191],v[240:243],v[40:43]
ds_read_b64_tr_b16 a[220:221],v3 offset:8448
v_cvt_pk_bf16_f32 v139,v142,v143
v_mfma_f32_16x16x32_bf16 v[220:223],a[184:187],v[208:211],v[220:223]
ds_read_b64_tr_b16 a[222:223],v3 offset:12544
v_exp_f32 v152,v152
v_exp_f32 v153,v153
v_exp_f32 v154,v154
v_exp_f32 v155,v155
v_exp_f32 v156,v156
v_exp_f32 v157,v157
v_exp_f32 v158,v158
v_exp_f32 v159,v159
v_mfma_f32_16x16x32_bf16 v[220:223],a[180:183],v[176:179],v[220:223]
ds_read_b64_tr_b16 a[216:217],v3 offset:256
ds_read_b64_tr_b16 a[218:219],v3 offset:4352
v_cvt_pk_bf16_f32 v140,v152,v153
v_cvt_pk_bf16_f32 v141,v154,v155
v_cvt_pk_bf16_f32 v142,v156,v157
v_cvt_pk_bf16_f32 v143,v158,v159
v_mfma_f32_16x16x32_bf16 v[220:223],a[176:179],v[144:147],v[220:223]
s_waitcnt lgkmcnt(6)
ds_read_b64_tr_b16 a[224:225],v2 offset:512
ds_read_b64_tr_b16 a[226:227],v2 offset:4608
v_mfma_f32_16x16x32_bf16 a[0:3],a[192:195],v[136:139],a[0:3]
ds_read_b64_tr_b16 a[228:229],v2 offset:8704
ds_read_b64_tr_b16 a[230:231],v2 offset:12800
v_mfma_f32_16x16x32_bf16 a[0:3],a[196:199],v[140:143],a[0:3]
ds_read_b64_tr_b16 a[236:237],v3 offset:8704
ds_read_b64_tr_b16 a[238:239],v3 offset:12800
v_mfma_f32_16x16x32_bf16 a[4:7],a[204:207],v[140:143],a[4:7]
ds_read_b64_tr_b16 a[232:233],v3 offset:512
ds_read_b64_tr_b16 a[234:235],v3 offset:4608
v_mfma_f32_16x16x32_bf16 a[4:7],a[200:203],v[136:139],a[4:7]
ds_read_b64_tr_b16 a[240:241],v2 offset:768
ds_read_b64_tr_b16 a[242:243],v2 offset:4864
v_mfma_f32_16x16x32_bf16 a[8:11],a[208:211],v[136:139],a[8:11]
s_memrealtime s[76:77]
ds_read_b64_tr_b16 a[244:245],v2 offset:8960
ds_read_b64_tr_b16 a[246:247],v2 offset:13056
s_waitcnt lgkmcnt(15)
v_mfma_f32_16x16x32_bf16 a[8:11],a[212:215],v[140:143],a[8:11]
s_waitcnt lgkmcnt(14)
ds_read_b64_tr_b16 a[252:253],v3 offset:8960
ds_read_b64_tr_b16 a[254:255],v3 offset:13056
v_mfma_f32_16x16x32_bf16 a[12:15],a[220:223],v[140:143],a[12:15]
s_waitcnt lgkmcnt(14)
ds_read_b64_tr_b16 a[248:249],v3 offset:768
ds_read_b64_tr_b16 a[250:251],v3 offset:4864
v_mfma_f32_16x16x32_bf16 a[12:15],a[216:219],v[136:139],a[12:15]
s_waitcnt lgkmcnt(14)
v_mfma_f32_16x16x32_bf16 a[16:19],a[224:227],v[136:139],a[16:19]
s_waitcnt lgkmcnt(12)
v_mfma_f32_16x16x32_bf16 a[16:19],a[228:231],v[140:143],a[16:19]
s_waitcnt lgkmcnt(10)
v_mfma_f32_16x16x32_bf16 a[20:23],a[236:239],v[140:143],a[20:23]
s_waitcnt lgkmcnt(0)
v_mfma_f32_16x16x32_bf16 a[20:23],a[232:235],v[136:139],a[20:23]
s_sub_u32 s79,s78,s76
v_exp_f32 v168,v168
v_mfma_f32_16x16x32_bf16 a[24:27],a[240:243],v[136:139],a[24:27]
s_add_u32 s78,s76,s60
v_exp_f32 v169,v169
v_mfma_f32_16x16x32_bf16 a[24:27],a[244:247],v[140:143],a[24:27]
s_cmp_gt_i32 s79,s95
s_cbranch_scc1 L_ps6_%=
L_pr6_%=:
v_exp_f32 v170,v170
v_exp_f32 v171,v171
v_mfma_f32_16x16x32_bf16 a[28:31],a[252:255],v[140:143],a[28:31]
v_cvt_pk_bf16_f32 v114,v114,v115
v_cvt_pk_bf16_f32 v168,v168,v169
v_exp_f32 v172,v172
v_exp_f32 v173,v173
v_exp_f32 v174,v174
v_exp_f32 v175,v175
v_cvt_pk_bf16_f32 v115,v16,v17
v_cvt_pk_bf16_f32 v169,v170,v171
v_exp_f32 v184,v184
v_exp_f32 v185,v185
v_exp_f32 v186,v186
v_exp_f32 v187,v187
v_exp_f32 v188,v188
v_exp_f32 v189,v189
v_exp_f32 v190,v190
v_exp_f32 v191,v191
v_mfma_f32_16x16x32_bf16 a[28:31],a[248:251],v[136:139],a[28:31]
s_cmp_lg_u64 s[56:57],0
s_cbranch_scc1 L_slow0_%=
L_sret0_%=:
v_cvt_pk_bf16_f32 v170,v172,v173
v_cvt_pk_bf16_f32 v171,v174,v175
v_add_u32 v1,s48,v1
v_cvt_pk_bf16_f32 v172,v184,v185
v_cvt_pk_bf16_f32 v173,v186,v187
v_cvt_pk_bf16_f32 v174,v188,v189
v_cvt_pk_bf16_f32 v175,v190,v191
v_smfmac_f32_16x16x64_bf16 v[48:51],v[28:31],v[136:143],v27
s_mov_b32 s53,11
s_cmp_eq_u32 s40,3
s_cbranch_scc0 L_att1_%=
s_cmp_gt_i32 s18,0
s_cbranch_scc1 L_att1v0_%=
s_cmp_gt_i32 s19,0
s_cbranch_scc1 L_att1v1_%=
s_cmp_gt_i32 s100,0
s_cbranch_scc1 L_att1v2_%=
.p2align 5
L_att1v3_%=:
s_waitcnt vmcnt(8) lgkmcnt(0)
v_exp_f32 v200,v200
s_barrier
s_add_u32 m0,s49,s51
ds_read_b128 a[128:131],v1
s_mov_b32 s90,L_out_end_%=-L_pc_%=+128
v_exp_f32 v201,v201
s_lshr_b32 s45,s45,1
ds_read_b128 a[132:135],v1 offset:256
v_cvt_pk_bf16_f32 v200,v200,v201
buffer_load_dwordx4 v4,s[20:23],s47 offen nt lds
ds_read_b128 a[136:139],v1 offset:512
v_add_u32 v2,s48,v2
v_exp_f32 v202,v202
s_add_u32 s62,s50,32768
ds_read_b128 a[140:143],v1 offset:768
v_add_u32 v3,s48,v3
s_and_b32 s62,s62,131071
v_exp_f32 v203,v203
s_add_u32 s54,s54,16384
ds_read_b128 a[156:159],v1 offset:4864
v_cvt_pk_bf16_f32 v201,v202,v203
s_sub_u32 s48,s62,s50
v_exp_f32 v204,v204
s_sub_u32 s53,s53,1
v_exp_f32 v205,v205
s_and_b32 s46,s54,0xfffc0000
ds_read_b128 a[152:155],v1 offset:4608
v_cvt_pk_bf16_f32 v202,v204,v205
s_mov_b32 s50,s62
v_exp_f32 v206,v206
v_exp_f32 v207,v207
ds_read_b128 a[148:151],v1 offset:4352
v_cvt_pk_bf16_f32 v203,v206,v207
v_mfma_f32_16x16x32_bf16 a[32:35],a[192:195],v[168:171],a[32:35]
v_exp_f32 v216,v216
v_mfma_f32_16x16x32_bf16 a[32:35],a[196:199],v[172:175],a[32:35]
ds_read_b128 a[144:147],v1 offset:4096
v_mfma_f32_16x16x32_bf16 a[36:39],a[204:207],v[172:175],a[36:39]
buffer_load_dwordx4 v4,s[20:23],s47 offen offset:1024 nt lds
v_mfma_f32_16x16x32_bf16 a[36:39],a[200:203],v[168:171],a[36:39]
ds_read_b128 a[160:163],v1 offset:8192
v_mfma_f32_16x16x32_bf16 a[40:43],a[208:211],v[168:171],a[40:43]
v_exp_f32 v217,v217
v_mfma_f32_16x16x32_bf16 a[40:43],a[212:215],v[172:175],a[40:43]
ds_read_b128 a[164:167],v1 offset:8448
v_cvt_pk_bf16_f32 v204,v216,v217
v_mfma_f32_16x16x32_bf16 a[44:47],a[220:223],v[172:175],a[44:47]
v_exp_f32 v218,v218
v_mfma_f32_16x16x32_bf16 a[44:47],a[216:219],v[168:171],a[44:47]
ds_read_b128 a[168:171],v1 offset:8704
v_mfma_f32_16x16x32_bf16 a[48:51],a[224:227],v[168:171],a[48:51]
v_exp_f32 v219,v219
v_mfma_f32_16x16x32_bf16 a[48:51],a[228:231],v[172:175],a[48:51]
ds_read_b128 a[172:175],v1 offset:8960
v_cvt_pk_bf16_f32 v205,v218,v219
v_mfma_f32_16x16x32_bf16 a[52:55],a[236:239],v[172:175],a[52:55]
v_exp_f32 v220,v220
v_mfma_f32_16x16x32_bf16 a[52:55],a[232:235],v[168:171],a[52:55]
ds_read_b128 a[188:191],v1 offset:13056
v_mfma_f32_16x16x32_bf16 a[56:59],a[240:243],v[168:171],a[56:59]
v_exp_f32 v221,v221
v_mfma_f32_16x16x32_bf16 a[56:59],a[244:247],v[172:175],a[56:59]
ds_read_b128 a[184:187],v1 offset:12800
v_cvt_pk_bf16_f32 v206,v220,v221
v_mfma_f32_16x16x32_bf16 a[60:63],a[252:255],v[172:175],a[60:63]
v_exp_f32 v222,v222
v_mfma_f32_16x16x32_bf16 a[60:63],a[248:251],v[168:171],a[60:63]
ds_read_b128 a[180:183],v1 offset:12544
v_smfmac_f32_16x16x64_bf16 v[52:55],v[28:31],v[168:175],v27
s_waitcnt lgkmcnt(4)
v_mfma_f32_16x16x32_bf16 v[136:139],a[128:131],v[128:131],v[32:35]
buffer_load_dwordx4 v5,s[20:23],s47 offen offset:2048 nt lds
v_mfma_f32_16x16x32_bf16 v[136:139],a[132:135],v[160:163],v[136:139]
ds_read_b128 a[176:179],v1 offset:12288
v_add_u32 v1,s48,v1
v_mfma_f32_16x16x32_bf16 v[136:139],a[136:139],v[192:195],v[136:139]
v_exp_f32 v223,v223
v_mfma_f32_16x16x32_bf16 v[136:139],a[140:143],v[224:227],v[136:139]
v_cvt_pk_bf16_f32 v207,v222,v223
v_mfma_f32_16x16x32_bf16 v[140:143],a[156:159],v[224:227],v[32:35]
v_mfma_f32_16x16x32_bf16 v[140:143],a[152:155],v[192:195],v[140:143]
v_mfma_f32_16x16x32_bf16 v[140:143],a[148:151],v[160:163],v[140:143]
v_mfma_f32_16x16x32_bf16 v[140:143],a[144:147],v[128:131],v[140:143]
v_mfma_f32_16x16x32_bf16 v[152:155],a[160:163],v[128:131],v[32:35]
v_mfma_f32_16x16x32_bf16 v[152:155],a[164:167],v[160:163],v[152:155]
v_mfma_f32_16x16x32_bf16 v[152:155],a[168:171],v[192:195],v[152:155]
s_waitcnt lgkmcnt(0)
v_mfma_f32_16x16x32_bf16 v[152:155],a[172:175],v[224:227],v[152:155]
v_mfma_f32_16x16x32_bf16 v[156:159],a[188:191],v[224:227],v[32:35]
v_mfma_f32_16x16x32_bf16 v[156:159],a[184:187],v[192:195],v[156:159]
buffer_load_dwordx4 v5,s[20:23],s47 offen offset:3072 nt lds
v_mfma_f32_16x16x32_bf16 v[156:159],a[180:183],v[160:163],v[156:159]
v_mfma_f32_16x16x32_bf16 v[156:159],a[176:179],v[128:131],v[156:159]
v_mfma_f32_16x16x32_bf16 a[64:67],a[192:195],v[200:203],a[64:67]
v_mfma_f32_16x16x32_bf16 a[64:67],a[196:199],v[204:207],a[64:67]
v_mfma_f32_16x16x32_bf16 a[68:71],a[204:207],v[204:207],a[68:71]
v_mfma_f32_16x16x32_bf16 a[68:71],a[200:203],v[200:203],a[68:71]
v_mfma_f32_16x16x32_bf16 a[72:75],a[208:211],v[200:203],a[72:75]
v_mfma_f32_16x16x32_bf16 a[72:75],a[212:215],v[204:207],a[72:75]
v_exp_f32 v136,v136
v_mfma_f32_16x16x32_bf16 a[76:79],a[220:223],v[204:207],a[76:79]
v_exp_f32 v137,v137
v_mfma_f32_16x16x32_bf16 a[76:79],a[216:219],v[200:203],a[76:79]
v_exp_f32 v138,v138
v_mfma_f32_16x16x32_bf16 a[80:83],a[224:227],v[200:203],a[80:83]
s_add_u32 m0,s49,s52
v_mfma_f32_16x16x32_bf16 a[80:83],a[228:231],v[204:207],a[80:83]
s_xor_b32 s49,s50,65536
v_mfma_f32_16x16x32_bf16 a[84:87],a[236:239],v[204:207],a[84:87]
v_mfma_f32_16x16x32_bf16 a[84:87],a[232:235],v[200:203],a[84:87]
buffer_load_dwordx4 v4,s[24:27],s47 offen nt lds
v_mfma_f32_16x16x32_bf16 a[88:91],a[240:243],v[200:203],a[88:91]
v_mfma_f32_16x16x32_bf16 a[88:91],a[244:247],v[204:207],a[88:91]
v_exp_f32 v139,v139
v_mfma_f32_16x16x32_bf16 a[92:95],a[252:255],v[204:207],a[92:95]
v_exp_f32 v140,v140
v_mfma_f32_16x16x32_bf16 a[92:95],a[248:251],v[200:203],a[92:95]
v_exp_f32 v141,v141
v_smfmac_f32_16x16x64_bf16 v[56:59],v[28:31],v[200:207],v27
v_mfma_f32_16x16x32_bf16 v[168:171],a[128:131],v[132:135],v[36:39]
v_exp_f32 v142,v142
v_mfma_f32_16x16x32_bf16 v[168:171],a[132:135],v[164:167],v[168:171]
v_exp_f32 v143,v143
v_mfma_f32_16x16x32_bf16 v[168:171],a[136:139],v[196:199],v[168:171]
v_exp_f32 v152,v152
v_mfma_f32_16x16x32_bf16 v[168:171],a[140:143],v[228:231],v[168:171]
v_exp_f32 v153,v153
v_mfma_f32_16x16x32_bf16 v[172:175],a[156:159],v[228:231],v[36:39]
v_exp_f32 v154,v154
v_mfma_f32_16x16x32_bf16 v[172:175],a[152:155],v[196:199],v[172:175]
v_exp_f32 v155,v155
v_mfma_f32_16x16x32_bf16 v[172:175],a[148:151],v[164:167],v[172:175]
v_exp_f32 v156,v156
v_mfma_f32_16x16x32_bf16 v[172:175],a[144:147],v[132:135],v[172:175]
)A"
R"A(v_exp_f32 v157,v157
v_mfma_f32_16x16x32_bf16 v[184:187],a[160:163],v[132:135],v[36:39]
buffer_load_dwordx4 v5,s[24:27],s47 offen offset:1024 nt lds
v_mfma_f32_16x16x32_bf16 v[184:187],a[164:167],v[164:167],v[184:187]
v_exp_f32 v158,v158
v_mfma_f32_16x16x32_bf16 v[184:187],a[168:171],v[196:199],v[184:187]
v_exp_f32 v159,v159
v_mfma_f32_16x16x32_bf16 v[184:187],a[172:175],v[228:231],v[184:187]
v_exp_f32 v168,v168
v_mfma_f32_16x16x32_bf16 v[188:191],a[188:191],v[228:231],v[36:39]
v_exp_f32 v169,v169
v_mfma_f32_16x16x32_bf16 v[188:191],a[184:187],v[196:199],v[188:191]
v_exp_f32 v170,v170
v_mfma_f32_16x16x32_bf16 v[188:191],a[180:183],v[164:167],v[188:191]
v_exp_f32 v171,v171
v_mfma_f32_16x16x32_bf16 v[188:191],a[176:179],v[132:135],v[188:191]
v_exp_f32 v172,v172
ds_read_b64_tr_b16 a[192:193],v2
v_cvt_pk_bf16_f32 v168,v168,v169
ds_read_b64_tr_b16 a[194:195],v2 offset:4096
v_cvt_pk_bf16_f32 v169,v170,v171
v_exp_f32 v173,v173
ds_read_b64_tr_b16 a[196:197],v2 offset:8192
v_cvt_pk_bf16_f32 v170,v172,v173
v_exp_f32 v174,v174
v_exp_f32 v175,v175
buffer_load_dwordx4 v4,s[24:27],s47 offen offset:2048 nt lds
ds_read_b64_tr_b16 a[198:199],v2 offset:12288
v_cvt_pk_bf16_f32 v171,v174,v175
v_exp_f32 v184,v184
v_exp_f32 v185,v185
ds_read_b64_tr_b16 a[204:205],v3 offset:8192
v_cvt_pk_bf16_f32 v136,v136,v137
ds_read_b64_tr_b16 a[206:207],v3 offset:12288
v_cvt_pk_bf16_f32 v172,v184,v185
ds_read_b64_tr_b16 a[200:201],v3
ds_read_b64_tr_b16 a[202:203],v3 offset:4096
v_cvt_pk_bf16_f32 v137,v138,v139
ds_read_b64_tr_b16 a[208:209],v2 offset:256
ds_read_b64_tr_b16 a[210:211],v2 offset:4352
ds_read_b64_tr_b16 a[212:213],v2 offset:8448
v_mfma_f32_16x16x32_bf16 v[200:203],a[128:131],v[144:147],v[40:43]
ds_read_b64_tr_b16 a[214:215],v2 offset:12544
v_cvt_pk_bf16_f32 v138,v140,v141
v_mfma_f32_16x16x32_bf16 v[200:203],a[132:135],v[176:179],v[200:203]
ds_read_b64_tr_b16 a[220:221],v3 offset:8448
v_mfma_f32_16x16x32_bf16 v[200:203],a[136:139],v[208:211],v[200:203]
ds_read_b64_tr_b16 a[222:223],v3 offset:12544
v_cvt_pk_bf16_f32 v139,v142,v143
v_mfma_f32_16x16x32_bf16 v[200:203],a[140:143],v[240:243],v[200:203]
buffer_load_dwordx4 v5,s[24:27],s47 offen offset:3072 nt lds
v_mfma_f32_16x16x32_bf16 v[204:207],a[156:159],v[240:243],v[40:43]
s_add_u32 s47,s47,16384
s_and_b32 s47,s47,0x3ffff
ds_read_b64_tr_b16 a[216:217],v3 offset:256
v_mfma_f32_16x16x32_bf16 v[204:207],a[152:155],v[208:211],v[204:207]
s_or_b32 s47,s47,s46
ds_read_b64_tr_b16 a[218:219],v3 offset:4352
v_cvt_pk_bf16_f32 v140,v152,v153
v_mfma_f32_16x16x32_bf16 v[204:207],a[148:151],v[176:179],v[204:207]
ds_read_b64_tr_b16 a[224:225],v2 offset:512
v_mfma_f32_16x16x32_bf16 v[204:207],a[144:147],v[144:147],v[204:207]
ds_read_b64_tr_b16 a[226:227],v2 offset:4608
v_mfma_f32_16x16x32_bf16 v[216:219],a[160:163],v[144:147],v[40:43]
ds_read_b64_tr_b16 a[228:229],v2 offset:8704
v_cvt_pk_bf16_f32 v141,v154,v155
v_mfma_f32_16x16x32_bf16 v[216:219],a[164:167],v[176:179],v[216:219]
ds_read_b64_tr_b16 a[230:231],v2 offset:12800
v_mfma_f32_16x16x32_bf16 v[216:219],a[168:171],v[208:211],v[216:219]
ds_read_b64_tr_b16 a[236:237],v3 offset:8704
v_mfma_f32_16x16x32_bf16 v[216:219],a[172:175],v[240:243],v[216:219]
ds_read_b64_tr_b16 a[238:239],v3 offset:12800
v_cvt_pk_bf16_f32 v142,v156,v157
v_mfma_f32_16x16x32_bf16 v[220:223],a[188:191],v[240:243],v[40:43]
ds_read_b64_tr_b16 a[232:233],v3 offset:512
v_mfma_f32_16x16x32_bf16 v[220:223],a[184:187],v[208:211],v[220:223]
ds_read_b64_tr_b16 a[234:235],v3 offset:4608
v_mfma_f32_16x16x32_bf16 v[220:223],a[180:183],v[176:179],v[220:223]
ds_read_b64_tr_b16 a[240:241],v2 offset:768
v_cvt_pk_bf16_f32 v143,v158,v159
v_mfma_f32_16x16x32_bf16 v[220:223],a[176:179],v[144:147],v[220:223]
s_waitcnt lgkmcnt(7)
buffer_load_dword v26,v24,s[88:91],s93 offen
v_mfma_f32_16x16x32_bf16 a[0:3],a[192:195],v[136:139],a[0:3]
ds_read_b64_tr_b16 a[242:243],v2 offset:4864
v_mfma_f32_16x16x32_bf16 a[0:3],a[196:199],v[140:143],a[0:3]
ds_read_b64_tr_b16 a[244:245],v2 offset:8960
v_mfma_f32_16x16x32_bf16 a[4:7],a[204:207],v[140:143],a[4:7]
ds_read_b64_tr_b16 a[246:247],v2 offset:13056
v_mfma_f32_16x16x32_bf16 a[4:7],a[200:203],v[136:139],a[4:7]
ds_read_b64_tr_b16 a[252:253],v3 offset:8960
v_mfma_f32_16x16x32_bf16 a[8:11],a[208:211],v[136:139],a[8:11]
ds_read_b64_tr_b16 a[254:255],v3 offset:13056
v_mfma_f32_16x16x32_bf16 a[8:11],a[212:215],v[140:143],a[8:11]
ds_read_b64_tr_b16 a[248:249],v3 offset:768
v_mfma_f32_16x16x32_bf16 a[12:15],a[220:223],v[140:143],a[12:15]
ds_read_b64_tr_b16 a[250:251],v3 offset:4864
v_mfma_f32_16x16x32_bf16 a[12:15],a[216:219],v[136:139],a[12:15]
s_bitcmp1_b32 s45,0
s_cbranch_scc0 L_t31v31_%=
s_mov_b32 m0,s80
s_add_u32 s80,s80,4096
buffer_load_dwordx4 v25,s[24:27],s83 offen nt lds
s_add_u32 s83,s83,4096
L_t31v31_%=:
v_mfma_f32_16x16x32_bf16 a[16:19],a[224:227],v[136:139],a[16:19]
s_waitcnt lgkmcnt(6)
v_exp_f32 v186,v186
v_mfma_f32_16x16x32_bf16 a[16:19],a[228:231],v[140:143],a[16:19]
v_exp_f32 v187,v187
v_mfma_f32_16x16x32_bf16 a[20:23],a[236:239],v[140:143],a[20:23]
v_exp_f32 v188,v188
v_mfma_f32_16x16x32_bf16 a[20:23],a[232:235],v[136:139],a[20:23]
v_exp_f32 v189,v189
v_mfma_f32_16x16x32_bf16 a[24:27],a[240:243],v[136:139],a[24:27]
s_waitcnt lgkmcnt(2)
v_exp_f32 v190,v190
v_mfma_f32_16x16x32_bf16 a[24:27],a[244:247],v[140:143],a[24:27]
v_cvt_pk_bf16_f32 v173,v186,v187
v_mfma_f32_16x16x32_bf16 a[28:31],a[252:255],v[140:143],a[28:31]
s_waitcnt lgkmcnt(0)
v_exp_f32 v191,v191
v_mfma_f32_16x16x32_bf16 a[28:31],a[248:251],v[136:139],a[28:31]
v_cvt_pk_bf16_f32 v174,v188,v189
v_cvt_pk_bf16_f32 v175,v190,v191
s_cmp_lg_u32 s53,0
v_smfmac_f32_16x16x64_bf16 v[48:51],v[28:31],v[136:143],v27
s_cbranch_scc1 L_att1v3_%=
s_waitcnt lgkmcnt(0)
s_nop 7
s_branch L_att1x_%=
.p2align 5
L_att1v0_%=:
v_mfma_f32_16x16x32_bf16 v[232:235],a[128:131],v[148:151],v[44:47]
s_waitcnt vmcnt(8) lgkmcnt(0)
v_exp_f32 v200,v200
v_mfma_f32_16x16x32_bf16 v[232:235],a[132:135],v[180:183],v[232:235]
s_barrier
s_add_u32 m0,s49,s51
ds_read_b128 a[128:131],v1
v_mfma_f32_16x16x32_bf16 v[232:235],a[136:139],v[212:215],v[232:235]
s_mov_b32 s90,L_out_end_%=-L_pc_%=+128
v_exp_f32 v201,v201
v_mfma_f32_16x16x32_bf16 v[232:235],a[140:143],v[244:247],v[232:235]
s_lshr_b32 s45,s45,1
ds_read_b128 a[132:135],v1 offset:256
v_cvt_pk_bf16_f32 v200,v200,v201
v_mfma_f32_16x16x32_bf16 v[236:239],a[156:159],v[244:247],v[44:47]
buffer_load_dwordx4 v4,s[20:23],s47 offen nt lds
v_mfma_f32_16x16x32_bf16 v[236:239],a[152:155],v[212:215],v[236:239]
ds_read_b128 a[136:139],v1 offset:512
v_add_u32 v2,s48,v2
v_mfma_f32_16x16x32_bf16 v[236:239],a[148:151],v[180:183],v[236:239]
v_exp_f32 v202,v202
v_mfma_f32_16x16x32_bf16 v[236:239],a[144:147],v[148:151],v[236:239]
s_add_u32 s62,s50,32768
ds_read_b128 a[140:143],v1 offset:768
v_add_u32 v3,s48,v3
v_mfma_f32_16x16x32_bf16 v[248:251],a[160:163],v[148:151],v[44:47]
s_and_b32 s62,s62,131071
v_exp_f32 v203,v203
v_mfma_f32_16x16x32_bf16 v[248:251],a[164:167],v[180:183],v[248:251]
s_add_u32 s54,s54,16384
ds_read_b128 a[156:159],v1 offset:4864
v_cvt_pk_bf16_f32 v201,v202,v203
v_mfma_f32_16x16x32_bf16 v[248:251],a[168:171],v[212:215],v[248:251]
s_sub_u32 s48,s62,s50
v_exp_f32 v204,v204
v_mfma_f32_16x16x32_bf16 v[248:251],a[172:175],v[244:247],v[248:251]
s_sub_u32 s53,s53,1
v_exp_f32 v205,v205
v_mfma_f32_16x16x32_bf16 v[252:255],a[188:191],v[244:247],v[44:47]
s_and_b32 s46,s54,0xfffc0000
ds_read_b128 a[152:155],v1 offset:4608
v_cvt_pk_bf16_f32 v202,v204,v205
v_mfma_f32_16x16x32_bf16 v[252:255],a[184:187],v[212:215],v[252:255]
s_mov_b32 s50,s62
v_exp_f32 v206,v206
v_mfma_f32_16x16x32_bf16 v[252:255],a[180:183],v[180:183],v[252:255]
v_exp_f32 v207,v207
v_mfma_f32_16x16x32_bf16 v[252:255],a[176:179],v[148:151],v[252:255]
ds_read_b128 a[148:151],v1 offset:4352
v_cvt_pk_bf16_f32 v203,v206,v207
v_mfma_f32_16x16x32_bf16 a[32:35],a[192:195],v[168:171],a[32:35]
v_exp_f32 v216,v216
v_mfma_f32_16x16x32_bf16 a[32:35],a[196:199],v[172:175],a[32:35]
ds_read_b128 a[144:147],v1 offset:4096
v_mfma_f32_16x16x32_bf16 a[36:39],a[204:207],v[172:175],a[36:39]
buffer_load_dwordx4 v4,s[20:23],s47 offen offset:1024 nt lds
v_mfma_f32_16x16x32_bf16 a[36:39],a[200:203],v[168:171],a[36:39]
ds_read_b128 a[160:163],v1 offset:8192
v_mfma_f32_16x16x32_bf16 a[40:43],a[208:211],v[168:171],a[40:43]
v_exp_f32 v217,v217
v_mfma_f32_16x16x32_bf16 a[40:43],a[212:215],v[172:175],a[40:43]
ds_read_b128 a[164:167],v1 offset:8448
v_cvt_pk_bf16_f32 v204,v216,v217
v_mfma_f32_16x16x32_bf16 a[44:47],a[220:223],v[172:175],a[44:47]
v_exp_f32 v218,v218
v_mfma_f32_16x16x32_bf16 a[44:47],a[216:219],v[168:171],a[44:47]
ds_read_b128 a[168:171],v1 offset:8704
v_mfma_f32_16x16x32_bf16 a[48:51],a[224:227],v[168:171],a[48:51]
v_exp_f32 v219,v219
v_mfma_f32_16x16x32_bf16 a[48:51],a[228:231],v[172:175],a[48:51]
ds_read_b128 a[172:175],v1 offset:8960
v_cvt_pk_bf16_f32 v205,v218,v219
v_mfma_f32_16x16x32_bf16 a[52:55],a[236:239],v[172:175],a[52:55]
v_exp_f32 v220,v220
v_mfma_f32_16x16x32_bf16 a[52:55],a[232:235],v[168:171],a[52:55]
ds_read_b128 a[188:191],v1 offset:13056
v_mfma_f32_16x16x32_bf16 a[56:59],a[240:243],v[168:171],a[56:59]
v_exp_f32 v221,v221
v_mfma_f32_16x16x32_bf16 a[56:59],a[244:247],v[172:175],a[56:59]
ds_read_b128 a[184:187],v1 offset:12800
v_cvt_pk_bf16_f32 v206,v220,v221
v_mfma_f32_16x16x32_bf16 a[60:63],a[252:255],v[172:175],a[60:63]
v_exp_f32 v222,v222
v_mfma_f32_16x16x32_bf16 a[60:63],a[248:251],v[168:171],a[60:63]
ds_read_b128 a[180:183],v1 offset:12544
v_smfmac_f32_16x16x64_bf16 v[52:55],v[28:31],v[168:175],v27
buffer_load_dwordx4 v5,s[20:23],s47 offen offset:2048 nt lds
ds_read_b128 a[176:179],v1 offset:12288
v_add_u32 v1,s48,v1
v_exp_f32 v223,v223
s_nop 0
v_cvt_pk_bf16_f32 v207,v222,v223
v_exp_f32 v232,v232
v_exp_f32 v233,v233
v_exp_f32 v234,v234
v_exp_f32 v235,v235
v_exp_f32 v236,v236
v_exp_f32 v237,v237
v_exp_f32 v238,v238
v_exp_f32 v239,v239
v_exp_f32 v248,v248
buffer_load_dwordx4 v5,s[20:23],s47 offen offset:3072 nt lds
v_exp_f32 v249,v249
v_exp_f32 v250,v250
v_mfma_f32_16x16x32_bf16 a[64:67],a[192:195],v[200:203],a[64:67]
v_exp_f32 v251,v251
v_mfma_f32_16x16x32_bf16 a[64:67],a[196:199],v[204:207],a[64:67]
v_exp_f32 v252,v252
v_mfma_f32_16x16x32_bf16 a[68:71],a[204:207],v[204:207],a[68:71]
v_exp_f32 v253,v253
v_mfma_f32_16x16x32_bf16 a[68:71],a[200:203],v[200:203],a[68:71]
v_exp_f32 v254,v254
v_mfma_f32_16x16x32_bf16 a[72:75],a[208:211],v[200:203],a[72:75]
v_exp_f32 v255,v255
v_mfma_f32_16x16x32_bf16 a[72:75],a[212:215],v[204:207],a[72:75]
v_mfma_f32_16x16x32_bf16 a[76:79],a[220:223],v[204:207],a[76:79]
v_mfma_f32_16x16x32_bf16 a[76:79],a[216:219],v[200:203],a[76:79]
v_mfma_f32_16x16x32_bf16 a[80:83],a[224:227],v[200:203],a[80:83]
s_add_u32 m0,s49,s52
v_cvt_pk_bf16_f32 v232,v232,v233
v_cvt_pk_bf16_f32 v233,v234,v235
v_mfma_f32_16x16x32_bf16 a[80:83],a[228:231],v[204:207],a[80:83]
s_xor_b32 s49,s50,65536
v_cvt_pk_bf16_f32 v234,v236,v237
v_cvt_pk_bf16_f32 v235,v238,v239
v_mfma_f32_16x16x32_bf16 a[84:87],a[236:239],v[204:207],a[84:87]
v_cvt_pk_bf16_f32 v236,v248,v249
v_cvt_pk_bf16_f32 v237,v250,v251
v_mfma_f32_16x16x32_bf16 a[84:87],a[232:235],v[200:203],a[84:87]
buffer_load_dwordx4 v4,s[24:27],s47 offen nt lds
v_mfma_f32_16x16x32_bf16 a[88:91],a[240:243],v[200:203],a[88:91]
v_cvt_pk_bf16_f32 v238,v252,v253
v_cvt_pk_bf16_f32 v239,v254,v255
v_mfma_f32_16x16x32_bf16 a[88:91],a[244:247],v[204:207],a[88:91]
v_mfma_f32_16x16x32_bf16 a[92:95],a[252:255],v[204:207],a[92:95]
v_mfma_f32_16x16x32_bf16 a[92:95],a[248:251],v[200:203],a[92:95]
v_smfmac_f32_16x16x64_bf16 v[56:59],v[28:31],v[200:207],v27
s_waitcnt lgkmcnt(0)
v_mfma_f32_16x16x32_bf16 v[168:171],a[128:131],v[132:135],v[36:39]
v_mfma_f32_16x16x32_bf16 v[168:171],a[132:135],v[164:167],v[168:171]
v_mfma_f32_16x16x32_bf16 v[168:171],a[136:139],v[196:199],v[168:171]
v_mfma_f32_16x16x32_bf16 v[168:171],a[140:143],v[228:231],v[168:171]
v_mfma_f32_16x16x32_bf16 v[172:175],a[156:159],v[228:231],v[36:39]
v_mfma_f32_16x16x32_bf16 v[172:175],a[152:155],v[196:199],v[172:175]
v_mfma_f32_16x16x32_bf16 v[172:175],a[148:151],v[164:167],v[172:175]
v_mfma_f32_16x16x32_bf16 v[172:175],a[144:147],v[132:135],v[172:175]
)A"
R"A(v_mfma_f32_16x16x32_bf16 v[184:187],a[160:163],v[132:135],v[36:39]
buffer_load_dwordx4 v5,s[24:27],s47 offen offset:1024 nt lds
v_mfma_f32_16x16x32_bf16 v[184:187],a[164:167],v[164:167],v[184:187]
v_mfma_f32_16x16x32_bf16 v[184:187],a[168:171],v[196:199],v[184:187]
v_mfma_f32_16x16x32_bf16 v[184:187],a[172:175],v[228:231],v[184:187]
v_exp_f32 v168,v168
v_mfma_f32_16x16x32_bf16 v[188:191],a[188:191],v[228:231],v[36:39]
v_exp_f32 v169,v169
v_mfma_f32_16x16x32_bf16 v[188:191],a[184:187],v[196:199],v[188:191]
v_exp_f32 v170,v170
v_mfma_f32_16x16x32_bf16 v[188:191],a[180:183],v[164:167],v[188:191]
v_exp_f32 v171,v171
v_mfma_f32_16x16x32_bf16 v[188:191],a[176:179],v[132:135],v[188:191]
v_exp_f32 v172,v172
v_mfma_f32_16x16x32_bf16 a[96:99],a[192:195],v[232:235],a[96:99]
ds_read_b64_tr_b16 a[192:193],v2
v_cvt_pk_bf16_f32 v168,v168,v169
v_mfma_f32_16x16x32_bf16 a[96:99],a[196:199],v[236:239],a[96:99]
ds_read_b64_tr_b16 a[194:195],v2 offset:4096
v_cvt_pk_bf16_f32 v169,v170,v171
v_mfma_f32_16x16x32_bf16 a[100:103],a[204:207],v[236:239],a[100:103]
v_exp_f32 v173,v173
v_mfma_f32_16x16x32_bf16 a[100:103],a[200:203],v[232:235],a[100:103]
ds_read_b64_tr_b16 a[196:197],v2 offset:8192
v_cvt_pk_bf16_f32 v170,v172,v173
v_mfma_f32_16x16x32_bf16 a[104:107],a[208:211],v[232:235],a[104:107]
v_exp_f32 v174,v174
v_mfma_f32_16x16x32_bf16 a[104:107],a[212:215],v[236:239],a[104:107]
v_exp_f32 v175,v175
v_mfma_f32_16x16x32_bf16 a[108:111],a[220:223],v[236:239],a[108:111]
buffer_load_dwordx4 v4,s[24:27],s47 offen offset:2048 nt lds
v_mfma_f32_16x16x32_bf16 a[108:111],a[216:219],v[232:235],a[108:111]
ds_read_b64_tr_b16 a[198:199],v2 offset:12288
v_cvt_pk_bf16_f32 v171,v174,v175
v_mfma_f32_16x16x32_bf16 a[112:115],a[224:227],v[232:235],a[112:115]
v_exp_f32 v184,v184
v_mfma_f32_16x16x32_bf16 a[112:115],a[228:231],v[236:239],a[112:115]
v_exp_f32 v185,v185
v_mfma_f32_16x16x32_bf16 a[116:119],a[236:239],v[236:239],a[116:119]
ds_read_b64_tr_b16 a[204:205],v3 offset:8192
v_mfma_f32_16x16x32_bf16 a[116:119],a[232:235],v[232:235],a[116:119]
ds_read_b64_tr_b16 a[206:207],v3 offset:12288
v_cvt_pk_bf16_f32 v172,v184,v185
v_mfma_f32_16x16x32_bf16 a[120:123],a[240:243],v[232:235],a[120:123]
ds_read_b64_tr_b16 a[200:201],v3
v_mfma_f32_16x16x32_bf16 a[120:123],a[244:247],v[236:239],a[120:123]
ds_read_b64_tr_b16 a[202:203],v3 offset:4096
v_mfma_f32_16x16x32_bf16 a[124:127],a[252:255],v[236:239],a[124:127]
ds_read_b64_tr_b16 a[208:209],v2 offset:256
v_mfma_f32_16x16x32_bf16 a[124:127],a[248:251],v[232:235],a[124:127]
ds_read_b64_tr_b16 a[210:211],v2 offset:4352
v_smfmac_f32_16x16x64_bf16 v[60:63],v[28:31],v[232:239],v27
ds_read_b64_tr_b16 a[212:213],v2 offset:8448
v_mfma_f32_16x16x32_bf16 v[200:203],a[128:131],v[144:147],v[40:43]
ds_read_b64_tr_b16 a[214:215],v2 offset:12544
v_mfma_f32_16x16x32_bf16 v[200:203],a[132:135],v[176:179],v[200:203]
ds_read_b64_tr_b16 a[220:221],v3 offset:8448
v_mfma_f32_16x16x32_bf16 v[200:203],a[136:139],v[208:211],v[200:203]
ds_read_b64_tr_b16 a[222:223],v3 offset:12544
v_mfma_f32_16x16x32_bf16 v[200:203],a[140:143],v[240:243],v[200:203]
buffer_load_dwordx4 v5,s[24:27],s47 offen offset:3072 nt lds
v_mfma_f32_16x16x32_bf16 v[204:207],a[156:159],v[240:243],v[40:43]
s_add_u32 s47,s47,16384
s_and_b32 s47,s47,0x3ffff
ds_read_b64_tr_b16 a[216:217],v3 offset:256
v_mfma_f32_16x16x32_bf16 v[204:207],a[152:155],v[208:211],v[204:207]
s_or_b32 s47,s47,s46
ds_read_b64_tr_b16 a[218:219],v3 offset:4352
v_mfma_f32_16x16x32_bf16 v[204:207],a[148:151],v[176:179],v[204:207]
ds_read_b64_tr_b16 a[224:225],v2 offset:512
v_mfma_f32_16x16x32_bf16 v[204:207],a[144:147],v[144:147],v[204:207]
ds_read_b64_tr_b16 a[226:227],v2 offset:4608
v_mfma_f32_16x16x32_bf16 v[216:219],a[160:163],v[144:147],v[40:43]
ds_read_b64_tr_b16 a[228:229],v2 offset:8704
v_mfma_f32_16x16x32_bf16 v[216:219],a[164:167],v[176:179],v[216:219]
ds_read_b64_tr_b16 a[230:231],v2 offset:12800
v_mfma_f32_16x16x32_bf16 v[216:219],a[168:171],v[208:211],v[216:219]
ds_read_b64_tr_b16 a[236:237],v3 offset:8704
v_mfma_f32_16x16x32_bf16 v[216:219],a[172:175],v[240:243],v[216:219]
ds_read_b64_tr_b16 a[238:239],v3 offset:12800
v_mfma_f32_16x16x32_bf16 v[220:223],a[188:191],v[240:243],v[40:43]
ds_read_b64_tr_b16 a[232:233],v3 offset:512
v_mfma_f32_16x16x32_bf16 v[220:223],a[184:187],v[208:211],v[220:223]
ds_read_b64_tr_b16 a[234:235],v3 offset:4608
v_mfma_f32_16x16x32_bf16 v[220:223],a[180:183],v[176:179],v[220:223]
ds_read_b64_tr_b16 a[240:241],v2 offset:768
v_mfma_f32_16x16x32_bf16 v[220:223],a[176:179],v[144:147],v[220:223]
buffer_load_dword v26,v24,s[88:91],s93 offen
ds_read_b64_tr_b16 a[242:243],v2 offset:4864
ds_read_b64_tr_b16 a[244:245],v2 offset:8960
ds_read_b64_tr_b16 a[246:247],v2 offset:13056
ds_read_b64_tr_b16 a[252:253],v3 offset:8960
ds_read_b64_tr_b16 a[254:255],v3 offset:13056
ds_read_b64_tr_b16 a[248:249],v3 offset:768
ds_read_b64_tr_b16 a[250:251],v3 offset:4864
s_bitcmp1_b32 s45,0
s_cbranch_scc0 L_t31v01_%=
s_mov_b32 m0,s80
s_add_u32 s80,s80,4096
buffer_load_dwordx4 v25,s[24:27],s83 offen nt lds
s_add_u32 s83,s83,4096
L_t31v01_%=:
v_exp_f32 v186,v186
v_exp_f32 v187,v187
v_exp_f32 v188,v188
v_exp_f32 v189,v189
v_exp_f32 v190,v190
v_cvt_pk_bf16_f32 v173,v186,v187
v_exp_f32 v191,v191
v_cvt_pk_bf16_f32 v174,v188,v189
v_cvt_pk_bf16_f32 v175,v190,v191
s_cmp_lg_u32 s53,0
s_cbranch_scc1 L_att1v0_%=
s_waitcnt lgkmcnt(0)
s_nop 7
s_branch L_att1x_%=
.p2align 5
L_att1v1_%=:
v_mfma_f32_16x16x32_bf16 v[232:235],a[128:131],v[148:151],v[44:47]
s_waitcnt vmcnt(8) lgkmcnt(0)
v_exp_f32 v200,v200
v_mfma_f32_16x16x32_bf16 v[232:235],a[132:135],v[180:183],v[232:235]
s_barrier
s_add_u32 m0,s49,s51
ds_read_b128 a[128:131],v1
v_mfma_f32_16x16x32_bf16 v[232:235],a[136:139],v[212:215],v[232:235]
s_mov_b32 s90,L_out_end_%=-L_pc_%=+128
v_exp_f32 v201,v201
v_mfma_f32_16x16x32_bf16 v[232:235],a[140:143],v[244:247],v[232:235]
s_lshr_b32 s45,s45,1
ds_read_b128 a[132:135],v1 offset:256
v_cvt_pk_bf16_f32 v200,v200,v201
v_mfma_f32_16x16x32_bf16 v[236:239],a[156:159],v[244:247],v[44:47]
buffer_load_dwordx4 v4,s[20:23],s47 offen nt lds
v_mfma_f32_16x16x32_bf16 v[236:239],a[152:155],v[212:215],v[236:239]
ds_read_b128 a[136:139],v1 offset:512
v_add_u32 v2,s48,v2
v_mfma_f32_16x16x32_bf16 v[236:239],a[148:151],v[180:183],v[236:239]
v_exp_f32 v202,v202
v_mfma_f32_16x16x32_bf16 v[236:239],a[144:147],v[148:151],v[236:239]
s_add_u32 s62,s50,32768
ds_read_b128 a[140:143],v1 offset:768
v_add_u32 v3,s48,v3
v_mfma_f32_16x16x32_bf16 v[248:251],a[160:163],v[148:151],v[44:47]
s_and_b32 s62,s62,131071
v_exp_f32 v203,v203
v_mfma_f32_16x16x32_bf16 v[248:251],a[164:167],v[180:183],v[248:251]
s_add_u32 s54,s54,16384
ds_read_b128 a[156:159],v1 offset:4864
v_cvt_pk_bf16_f32 v201,v202,v203
v_mfma_f32_16x16x32_bf16 v[248:251],a[168:171],v[212:215],v[248:251]
s_sub_u32 s48,s62,s50
v_exp_f32 v204,v204
v_mfma_f32_16x16x32_bf16 v[248:251],a[172:175],v[244:247],v[248:251]
s_sub_u32 s53,s53,1
v_exp_f32 v205,v205
v_mfma_f32_16x16x32_bf16 v[252:255],a[188:191],v[244:247],v[44:47]
s_and_b32 s46,s54,0xfffc0000
ds_read_b128 a[152:155],v1 offset:4608
v_cvt_pk_bf16_f32 v202,v204,v205
v_mfma_f32_16x16x32_bf16 v[252:255],a[184:187],v[212:215],v[252:255]
s_mov_b32 s50,s62
v_exp_f32 v206,v206
v_mfma_f32_16x16x32_bf16 v[252:255],a[180:183],v[180:183],v[252:255]
v_exp_f32 v207,v207
v_mfma_f32_16x16x32_bf16 v[252:255],a[176:179],v[148:151],v[252:255]
s_waitcnt lgkmcnt(2)
ds_read_b128 a[148:151],v1 offset:4352
v_cvt_pk_bf16_f32 v203,v206,v207
v_exp_f32 v216,v216
ds_read_b128 a[144:147],v1 offset:4096
buffer_load_dwordx4 v4,s[20:23],s47 offen offset:1024 nt lds
ds_read_b128 a[160:163],v1 offset:8192
v_exp_f32 v217,v217
ds_read_b128 a[164:167],v1 offset:8448
v_cvt_pk_bf16_f32 v204,v216,v217
v_exp_f32 v218,v218
ds_read_b128 a[168:171],v1 offset:8704
v_exp_f32 v219,v219
ds_read_b128 a[172:175],v1 offset:8960
v_cvt_pk_bf16_f32 v205,v218,v219
v_exp_f32 v220,v220
ds_read_b128 a[188:191],v1 offset:13056
v_exp_f32 v221,v221
ds_read_b128 a[184:187],v1 offset:12800
v_cvt_pk_bf16_f32 v206,v220,v221
v_exp_f32 v222,v222
ds_read_b128 a[180:183],v1 offset:12544
v_mfma_f32_16x16x32_bf16 v[136:139],a[128:131],v[128:131],v[32:35]
buffer_load_dwordx4 v5,s[20:23],s47 offen offset:2048 nt lds
v_mfma_f32_16x16x32_bf16 v[136:139],a[132:135],v[160:163],v[136:139]
ds_read_b128 a[176:179],v1 offset:12288
v_add_u32 v1,s48,v1
v_mfma_f32_16x16x32_bf16 v[136:139],a[136:139],v[192:195],v[136:139]
v_exp_f32 v223,v223
v_mfma_f32_16x16x32_bf16 v[136:139],a[140:143],v[224:227],v[136:139]
s_waitcnt lgkmcnt(11)
v_cvt_pk_bf16_f32 v207,v222,v223
v_mfma_f32_16x16x32_bf16 v[140:143],a[156:159],v[224:227],v[32:35]
s_waitcnt lgkmcnt(10)
v_exp_f32 v232,v232
v_mfma_f32_16x16x32_bf16 v[140:143],a[152:155],v[192:195],v[140:143]
s_waitcnt lgkmcnt(9)
v_exp_f32 v233,v233
v_mfma_f32_16x16x32_bf16 v[140:143],a[148:151],v[160:163],v[140:143]
s_waitcnt lgkmcnt(8)
v_exp_f32 v234,v234
v_mfma_f32_16x16x32_bf16 v[140:143],a[144:147],v[128:131],v[140:143]
s_waitcnt lgkmcnt(1)
v_exp_f32 v235,v235
v_mfma_f32_16x16x32_bf16 v[152:155],a[160:163],v[128:131],v[32:35]
v_exp_f32 v236,v236
v_mfma_f32_16x16x32_bf16 v[152:155],a[164:167],v[160:163],v[152:155]
v_exp_f32 v237,v237
v_mfma_f32_16x16x32_bf16 v[152:155],a[168:171],v[192:195],v[152:155]
v_exp_f32 v238,v238
v_mfma_f32_16x16x32_bf16 v[152:155],a[172:175],v[224:227],v[152:155]
v_exp_f32 v239,v239
v_mfma_f32_16x16x32_bf16 v[156:159],a[188:191],v[224:227],v[32:35]
v_exp_f32 v248,v248
v_mfma_f32_16x16x32_bf16 v[156:159],a[184:187],v[192:195],v[156:159]
buffer_load_dwordx4 v5,s[20:23],s47 offen offset:3072 nt lds
v_mfma_f32_16x16x32_bf16 v[156:159],a[180:183],v[160:163],v[156:159]
s_waitcnt lgkmcnt(0)
v_exp_f32 v249,v249
v_mfma_f32_16x16x32_bf16 v[156:159],a[176:179],v[128:131],v[156:159]
v_exp_f32 v250,v250
v_mfma_f32_16x16x32_bf16 a[64:67],a[192:195],v[200:203],a[64:67]
v_exp_f32 v251,v251
v_mfma_f32_16x16x32_bf16 a[64:67],a[196:199],v[204:207],a[64:67]
v_exp_f32 v252,v252
v_mfma_f32_16x16x32_bf16 a[68:71],a[204:207],v[204:207],a[68:71]
v_exp_f32 v253,v253
v_mfma_f32_16x16x32_bf16 a[68:71],a[200:203],v[200:203],a[68:71]
v_exp_f32 v254,v254
v_mfma_f32_16x16x32_bf16 a[72:75],a[208:211],v[200:203],a[72:75]
v_exp_f32 v255,v255
v_mfma_f32_16x16x32_bf16 a[72:75],a[212:215],v[204:207],a[72:75]
v_exp_f32 v136,v136
v_mfma_f32_16x16x32_bf16 a[76:79],a[220:223],v[204:207],a[76:79]
v_exp_f32 v137,v137
v_mfma_f32_16x16x32_bf16 a[76:79],a[216:219],v[200:203],a[76:79]
v_exp_f32 v138,v138
v_mfma_f32_16x16x32_bf16 a[80:83],a[224:227],v[200:203],a[80:83]
s_add_u32 m0,s49,s52
v_cvt_pk_bf16_f32 v232,v232,v233
v_cvt_pk_bf16_f32 v233,v234,v235
v_mfma_f32_16x16x32_bf16 a[80:83],a[228:231],v[204:207],a[80:83]
s_xor_b32 s49,s50,65536
v_cvt_pk_bf16_f32 v234,v236,v237
v_cvt_pk_bf16_f32 v235,v238,v239
v_mfma_f32_16x16x32_bf16 a[84:87],a[236:239],v[204:207],a[84:87]
v_cvt_pk_bf16_f32 v236,v248,v249
v_cvt_pk_bf16_f32 v237,v250,v251
v_mfma_f32_16x16x32_bf16 a[84:87],a[232:235],v[200:203],a[84:87]
buffer_load_dwordx4 v4,s[24:27],s47 offen nt lds
v_mfma_f32_16x16x32_bf16 a[88:91],a[240:243],v[200:203],a[88:91]
v_cvt_pk_bf16_f32 v238,v252,v253
v_cvt_pk_bf16_f32 v239,v254,v255
v_mfma_f32_16x16x32_bf16 a[88:91],a[244:247],v[204:207],a[88:91]
v_exp_f32 v139,v139
v_mfma_f32_16x16x32_bf16 a[92:95],a[252:255],v[204:207],a[92:95]
v_exp_f32 v140,v140
v_mfma_f32_16x16x32_bf16 a[92:95],a[248:251],v[200:203],a[92:95]
v_exp_f32 v141,v141
v_smfmac_f32_16x16x64_bf16 v[56:59],v[28:31],v[200:207],v27
v_exp_f32 v142,v142
v_exp_f32 v143,v143
v_exp_f32 v152,v152
v_exp_f32 v153,v153
v_exp_f32 v154,v154
v_exp_f32 v155,v155
v_exp_f32 v156,v156
v_exp_f32 v157,v157
buffer_load_dwordx4 v5,s[24:27],s47 offen offset:1024 nt lds
v_exp_f32 v158,v158
v_exp_f32 v159,v159
v_mfma_f32_16x16x32_bf16 a[96:99],a[192:195],v[232:235],a[96:99]
ds_read_b64_tr_b16 a[192:193],v2
v_mfma_f32_16x16x32_bf16 a[96:99],a[196:199],v[236:239],a[96:99]
ds_read_b64_tr_b16 a[194:195],v2 offset:4096
v_mfma_f32_16x16x32_bf16 a[100:103],a[204:207],v[236:239],a[100:103]
v_mfma_f32_16x16x32_bf16 a[100:103],a[200:203],v[232:235],a[100:103]
)A"
R"A(ds_read_b64_tr_b16 a[196:197],v2 offset:8192
v_mfma_f32_16x16x32_bf16 a[104:107],a[208:211],v[232:235],a[104:107]
v_mfma_f32_16x16x32_bf16 a[104:107],a[212:215],v[236:239],a[104:107]
v_mfma_f32_16x16x32_bf16 a[108:111],a[220:223],v[236:239],a[108:111]
buffer_load_dwordx4 v4,s[24:27],s47 offen offset:2048 nt lds
v_mfma_f32_16x16x32_bf16 a[108:111],a[216:219],v[232:235],a[108:111]
ds_read_b64_tr_b16 a[198:199],v2 offset:12288
v_mfma_f32_16x16x32_bf16 a[112:115],a[224:227],v[232:235],a[112:115]
v_mfma_f32_16x16x32_bf16 a[112:115],a[228:231],v[236:239],a[112:115]
v_mfma_f32_16x16x32_bf16 a[116:119],a[236:239],v[236:239],a[116:119]
ds_read_b64_tr_b16 a[204:205],v3 offset:8192
v_cvt_pk_bf16_f32 v136,v136,v137
v_mfma_f32_16x16x32_bf16 a[116:119],a[232:235],v[232:235],a[116:119]
ds_read_b64_tr_b16 a[206:207],v3 offset:12288
v_mfma_f32_16x16x32_bf16 a[120:123],a[240:243],v[232:235],a[120:123]
ds_read_b64_tr_b16 a[200:201],v3
v_mfma_f32_16x16x32_bf16 a[120:123],a[244:247],v[236:239],a[120:123]
ds_read_b64_tr_b16 a[202:203],v3 offset:4096
v_cvt_pk_bf16_f32 v137,v138,v139
v_mfma_f32_16x16x32_bf16 a[124:127],a[252:255],v[236:239],a[124:127]
ds_read_b64_tr_b16 a[208:209],v2 offset:256
v_mfma_f32_16x16x32_bf16 a[124:127],a[248:251],v[232:235],a[124:127]
ds_read_b64_tr_b16 a[210:211],v2 offset:4352
v_smfmac_f32_16x16x64_bf16 v[60:63],v[28:31],v[232:239],v27
ds_read_b64_tr_b16 a[212:213],v2 offset:8448
v_mfma_f32_16x16x32_bf16 v[200:203],a[128:131],v[144:147],v[40:43]
ds_read_b64_tr_b16 a[214:215],v2 offset:12544
v_cvt_pk_bf16_f32 v138,v140,v141
v_mfma_f32_16x16x32_bf16 v[200:203],a[132:135],v[176:179],v[200:203]
ds_read_b64_tr_b16 a[220:221],v3 offset:8448
v_mfma_f32_16x16x32_bf16 v[200:203],a[136:139],v[208:211],v[200:203]
ds_read_b64_tr_b16 a[222:223],v3 offset:12544
v_cvt_pk_bf16_f32 v139,v142,v143
v_mfma_f32_16x16x32_bf16 v[200:203],a[140:143],v[240:243],v[200:203]
buffer_load_dwordx4 v5,s[24:27],s47 offen offset:3072 nt lds
v_mfma_f32_16x16x32_bf16 v[204:207],a[156:159],v[240:243],v[40:43]
s_add_u32 s47,s47,16384
s_and_b32 s47,s47,0x3ffff
ds_read_b64_tr_b16 a[216:217],v3 offset:256
v_mfma_f32_16x16x32_bf16 v[204:207],a[152:155],v[208:211],v[204:207]
s_or_b32 s47,s47,s46
ds_read_b64_tr_b16 a[218:219],v3 offset:4352
v_cvt_pk_bf16_f32 v140,v152,v153
v_mfma_f32_16x16x32_bf16 v[204:207],a[148:151],v[176:179],v[204:207]
ds_read_b64_tr_b16 a[224:225],v2 offset:512
v_mfma_f32_16x16x32_bf16 v[204:207],a[144:147],v[144:147],v[204:207]
ds_read_b64_tr_b16 a[226:227],v2 offset:4608
v_mfma_f32_16x16x32_bf16 v[216:219],a[160:163],v[144:147],v[40:43]
ds_read_b64_tr_b16 a[228:229],v2 offset:8704
v_cvt_pk_bf16_f32 v141,v154,v155
v_mfma_f32_16x16x32_bf16 v[216:219],a[164:167],v[176:179],v[216:219]
ds_read_b64_tr_b16 a[230:231],v2 offset:12800
v_mfma_f32_16x16x32_bf16 v[216:219],a[168:171],v[208:211],v[216:219]
ds_read_b64_tr_b16 a[236:237],v3 offset:8704
v_mfma_f32_16x16x32_bf16 v[216:219],a[172:175],v[240:243],v[216:219]
ds_read_b64_tr_b16 a[238:239],v3 offset:12800
v_cvt_pk_bf16_f32 v142,v156,v157
v_mfma_f32_16x16x32_bf16 v[220:223],a[188:191],v[240:243],v[40:43]
ds_read_b64_tr_b16 a[232:233],v3 offset:512
v_mfma_f32_16x16x32_bf16 v[220:223],a[184:187],v[208:211],v[220:223]
ds_read_b64_tr_b16 a[234:235],v3 offset:4608
v_mfma_f32_16x16x32_bf16 v[220:223],a[180:183],v[176:179],v[220:223]
ds_read_b64_tr_b16 a[240:241],v2 offset:768
v_cvt_pk_bf16_f32 v143,v158,v159
v_mfma_f32_16x16x32_bf16 v[220:223],a[176:179],v[144:147],v[220:223]
s_waitcnt lgkmcnt(7)
buffer_load_dword v26,v24,s[88:91],s93 offen
v_mfma_f32_16x16x32_bf16 a[0:3],a[192:195],v[136:139],a[0:3]
ds_read_b64_tr_b16 a[242:243],v2 offset:4864
v_mfma_f32_16x16x32_bf16 a[0:3],a[196:199],v[140:143],a[0:3]
ds_read_b64_tr_b16 a[244:245],v2 offset:8960
v_mfma_f32_16x16x32_bf16 a[4:7],a[204:207],v[140:143],a[4:7]
ds_read_b64_tr_b16 a[246:247],v2 offset:13056
v_mfma_f32_16x16x32_bf16 a[4:7],a[200:203],v[136:139],a[4:7]
ds_read_b64_tr_b16 a[252:253],v3 offset:8960
v_mfma_f32_16x16x32_bf16 a[8:11],a[208:211],v[136:139],a[8:11]
ds_read_b64_tr_b16 a[254:255],v3 offset:13056
v_mfma_f32_16x16x32_bf16 a[8:11],a[212:215],v[140:143],a[8:11]
ds_read_b64_tr_b16 a[248:249],v3 offset:768
v_mfma_f32_16x16x32_bf16 a[12:15],a[220:223],v[140:143],a[12:15]
ds_read_b64_tr_b16 a[250:251],v3 offset:4864
v_mfma_f32_16x16x32_bf16 a[12:15],a[216:219],v[136:139],a[12:15]
s_bitcmp1_b32 s45,0
s_cbranch_scc0 L_t31v11_%=
s_mov_b32 m0,s80
s_add_u32 s80,s80,4096
buffer_load_dwordx4 v25,s[24:27],s83 offen nt lds
s_add_u32 s83,s83,4096
L_t31v11_%=:
v_mfma_f32_16x16x32_bf16 a[16:19],a[224:227],v[136:139],a[16:19]
s_waitcnt lgkmcnt(6)
v_mfma_f32_16x16x32_bf16 a[16:19],a[228:231],v[140:143],a[16:19]
v_mfma_f32_16x16x32_bf16 a[20:23],a[236:239],v[140:143],a[20:23]
v_mfma_f32_16x16x32_bf16 a[20:23],a[232:235],v[136:139],a[20:23]
v_mfma_f32_16x16x32_bf16 a[24:27],a[240:243],v[136:139],a[24:27]
s_waitcnt lgkmcnt(2)
v_mfma_f32_16x16x32_bf16 a[24:27],a[244:247],v[140:143],a[24:27]
v_mfma_f32_16x16x32_bf16 a[28:31],a[252:255],v[140:143],a[28:31]
s_waitcnt lgkmcnt(0)
v_mfma_f32_16x16x32_bf16 a[28:31],a[248:251],v[136:139],a[28:31]
s_cmp_lg_u32 s53,0
v_smfmac_f32_16x16x64_bf16 v[48:51],v[28:31],v[136:143],v27
s_cbranch_scc1 L_att1v1_%=
s_waitcnt lgkmcnt(0)
s_nop 7
s_branch L_att1x_%=
.p2align 5
L_att1v2_%=:
v_mfma_f32_16x16x32_bf16 v[232:235],a[128:131],v[148:151],v[44:47]
s_waitcnt vmcnt(8) lgkmcnt(0)
v_mfma_f32_16x16x32_bf16 v[232:235],a[132:135],v[180:183],v[232:235]
s_barrier
s_add_u32 m0,s49,s51
ds_read_b128 a[128:131],v1
v_mfma_f32_16x16x32_bf16 v[232:235],a[136:139],v[212:215],v[232:235]
s_mov_b32 s90,L_out_end_%=-L_pc_%=+128
v_mfma_f32_16x16x32_bf16 v[232:235],a[140:143],v[244:247],v[232:235]
s_lshr_b32 s45,s45,1
ds_read_b128 a[132:135],v1 offset:256
v_mfma_f32_16x16x32_bf16 v[236:239],a[156:159],v[244:247],v[44:47]
buffer_load_dwordx4 v4,s[20:23],s47 offen nt lds
v_mfma_f32_16x16x32_bf16 v[236:239],a[152:155],v[212:215],v[236:239]
ds_read_b128 a[136:139],v1 offset:512
v_add_u32 v2,s48,v2
v_mfma_f32_16x16x32_bf16 v[236:239],a[148:151],v[180:183],v[236:239]
v_mfma_f32_16x16x32_bf16 v[236:239],a[144:147],v[148:151],v[236:239]
s_add_u32 s62,s50,32768
ds_read_b128 a[140:143],v1 offset:768
v_add_u32 v3,s48,v3
v_mfma_f32_16x16x32_bf16 v[248:251],a[160:163],v[148:151],v[44:47]
s_and_b32 s62,s62,131071
v_mfma_f32_16x16x32_bf16 v[248:251],a[164:167],v[180:183],v[248:251]
s_add_u32 s54,s54,16384
ds_read_b128 a[156:159],v1 offset:4864
v_mfma_f32_16x16x32_bf16 v[248:251],a[168:171],v[212:215],v[248:251]
s_sub_u32 s48,s62,s50
v_mfma_f32_16x16x32_bf16 v[248:251],a[172:175],v[244:247],v[248:251]
s_sub_u32 s53,s53,1
v_mfma_f32_16x16x32_bf16 v[252:255],a[188:191],v[244:247],v[44:47]
s_and_b32 s46,s54,0xfffc0000
ds_read_b128 a[152:155],v1 offset:4608
v_mfma_f32_16x16x32_bf16 v[252:255],a[184:187],v[212:215],v[252:255]
s_mov_b32 s50,s62
v_mfma_f32_16x16x32_bf16 v[252:255],a[180:183],v[180:183],v[252:255]
v_mfma_f32_16x16x32_bf16 v[252:255],a[176:179],v[148:151],v[252:255]
ds_read_b128 a[148:151],v1 offset:4352
v_mfma_f32_16x16x32_bf16 a[32:35],a[192:195],v[168:171],a[32:35]
v_mfma_f32_16x16x32_bf16 a[32:35],a[196:199],v[172:175],a[32:35]
ds_read_b128 a[144:147],v1 offset:4096
v_mfma_f32_16x16x32_bf16 a[36:39],a[204:207],v[172:175],a[36:39]
buffer_load_dwordx4 v4,s[20:23],s47 offen offset:1024 nt lds
v_mfma_f32_16x16x32_bf16 a[36:39],a[200:203],v[168:171],a[36:39]
ds_read_b128 a[160:163],v1 offset:8192
v_mfma_f32_16x16x32_bf16 a[40:43],a[208:211],v[168:171],a[40:43]
v_mfma_f32_16x16x32_bf16 a[40:43],a[212:215],v[172:175],a[40:43]
ds_read_b128 a[164:167],v1 offset:8448
v_mfma_f32_16x16x32_bf16 a[44:47],a[220:223],v[172:175],a[44:47]
v_mfma_f32_16x16x32_bf16 a[44:47],a[216:219],v[168:171],a[44:47]
ds_read_b128 a[168:171],v1 offset:8704
v_mfma_f32_16x16x32_bf16 a[48:51],a[224:227],v[168:171],a[48:51]
v_mfma_f32_16x16x32_bf16 a[48:51],a[228:231],v[172:175],a[48:51]
ds_read_b128 a[172:175],v1 offset:8960
v_mfma_f32_16x16x32_bf16 a[52:55],a[236:239],v[172:175],a[52:55]
v_mfma_f32_16x16x32_bf16 a[52:55],a[232:235],v[168:171],a[52:55]
ds_read_b128 a[188:191],v1 offset:13056
v_mfma_f32_16x16x32_bf16 a[56:59],a[240:243],v[168:171],a[56:59]
v_mfma_f32_16x16x32_bf16 a[56:59],a[244:247],v[172:175],a[56:59]
ds_read_b128 a[184:187],v1 offset:12800
v_mfma_f32_16x16x32_bf16 a[60:63],a[252:255],v[172:175],a[60:63]
v_mfma_f32_16x16x32_bf16 a[60:63],a[248:251],v[168:171],a[60:63]
ds_read_b128 a[180:183],v1 offset:12544
v_smfmac_f32_16x16x64_bf16 v[52:55],v[28:31],v[168:175],v27
s_waitcnt lgkmcnt(4)
v_mfma_f32_16x16x32_bf16 v[136:139],a[128:131],v[128:131],v[32:35]
buffer_load_dwordx4 v5,s[20:23],s47 offen offset:2048 nt lds
v_mfma_f32_16x16x32_bf16 v[136:139],a[132:135],v[160:163],v[136:139]
ds_read_b128 a[176:179],v1 offset:12288
v_add_u32 v1,s48,v1
v_mfma_f32_16x16x32_bf16 v[136:139],a[136:139],v[192:195],v[136:139]
v_mfma_f32_16x16x32_bf16 v[136:139],a[140:143],v[224:227],v[136:139]
v_mfma_f32_16x16x32_bf16 v[140:143],a[156:159],v[224:227],v[32:35]
v_exp_f32 v232,v232
v_mfma_f32_16x16x32_bf16 v[140:143],a[152:155],v[192:195],v[140:143]
v_exp_f32 v233,v233
v_mfma_f32_16x16x32_bf16 v[140:143],a[148:151],v[160:163],v[140:143]
v_exp_f32 v234,v234
v_mfma_f32_16x16x32_bf16 v[140:143],a[144:147],v[128:131],v[140:143]
v_exp_f32 v235,v235
v_mfma_f32_16x16x32_bf16 v[152:155],a[160:163],v[128:131],v[32:35]
v_exp_f32 v236,v236
v_mfma_f32_16x16x32_bf16 v[152:155],a[164:167],v[160:163],v[152:155]
v_exp_f32 v237,v237
v_mfma_f32_16x16x32_bf16 v[152:155],a[168:171],v[192:195],v[152:155]
s_waitcnt lgkmcnt(0)
v_exp_f32 v238,v238
v_mfma_f32_16x16x32_bf16 v[152:155],a[172:175],v[224:227],v[152:155]
v_exp_f32 v239,v239
v_mfma_f32_16x16x32_bf16 v[156:159],a[188:191],v[224:227],v[32:35]
v_exp_f32 v248,v248
v_mfma_f32_16x16x32_bf16 v[156:159],a[184:187],v[192:195],v[156:159]
buffer_load_dwordx4 v5,s[20:23],s47 offen offset:3072 nt lds
v_mfma_f32_16x16x32_bf16 v[156:159],a[180:183],v[160:163],v[156:159]
v_exp_f32 v249,v249
v_mfma_f32_16x16x32_bf16 v[156:159],a[176:179],v[128:131],v[156:159]
v_exp_f32 v250,v250
v_exp_f32 v251,v251
v_exp_f32 v252,v252
v_exp_f32 v253,v253
v_exp_f32 v254,v254
v_exp_f32 v255,v255
v_exp_f32 v136,v136
v_exp_f32 v137,v137
v_exp_f32 v138,v138
s_add_u32 m0,s49,s52
v_cvt_pk_bf16_f32 v232,v232,v233
v_cvt_pk_bf16_f32 v233,v234,v235
s_xor_b32 s49,s50,65536
v_cvt_pk_bf16_f32 v234,v236,v237
v_cvt_pk_bf16_f32 v235,v238,v239
v_cvt_pk_bf16_f32 v236,v248,v249
v_cvt_pk_bf16_f32 v237,v250,v251
buffer_load_dwordx4 v4,s[24:27],s47 offen nt lds
v_cvt_pk_bf16_f32 v238,v252,v253
v_cvt_pk_bf16_f32 v239,v254,v255
v_exp_f32 v139,v139
v_exp_f32 v140,v140
v_exp_f32 v141,v141
v_mfma_f32_16x16x32_bf16 v[168:171],a[128:131],v[132:135],v[36:39]
v_exp_f32 v142,v142
v_mfma_f32_16x16x32_bf16 v[168:171],a[132:135],v[164:167],v[168:171]
v_exp_f32 v143,v143
v_mfma_f32_16x16x32_bf16 v[168:171],a[136:139],v[196:199],v[168:171]
v_exp_f32 v152,v152
v_mfma_f32_16x16x32_bf16 v[168:171],a[140:143],v[228:231],v[168:171]
v_exp_f32 v153,v153
v_mfma_f32_16x16x32_bf16 v[172:175],a[156:159],v[228:231],v[36:39]
v_exp_f32 v154,v154
v_mfma_f32_16x16x32_bf16 v[172:175],a[152:155],v[196:199],v[172:175]
v_exp_f32 v155,v155
v_mfma_f32_16x16x32_bf16 v[172:175],a[148:151],v[164:167],v[172:175]
v_exp_f32 v156,v156
v_mfma_f32_16x16x32_bf16 v[172:175],a[144:147],v[132:135],v[172:175]
v_exp_f32 v157,v157
v_mfma_f32_16x16x32_bf16 v[184:187],a[160:163],v[132:135],v[36:39]
buffer_load_dwordx4 v5,s[24:27],s47 offen offset:1024 nt lds
v_mfma_f32_16x16x32_bf16 v[184:187],a[164:167],v[164:167],v[184:187]
v_exp_f32 v158,v158
v_mfma_f32_16x16x32_bf16 v[184:187],a[168:171],v[196:199],v[184:187]
v_exp_f32 v159,v159
v_mfma_f32_16x16x32_bf16 v[184:187],a[172:175],v[228:231],v[184:187]
v_exp_f32 v168,v168
v_mfma_f32_16x16x32_bf16 v[188:191],a[188:191],v[228:231],v[36:39]
v_exp_f32 v169,v169
v_mfma_f32_16x16x32_bf16 v[188:191],a[184:187],v[196:199],v[188:191]
v_exp_f32 v170,v170
v_mfma_f32_16x16x32_bf16 v[188:191],a[180:183],v[164:167],v[188:191]
v_exp_f32 v171,v171
v_mfma_f32_16x16x32_bf16 v[188:191],a[176:179],v[132:135],v[188:191]
)A"
R"A(v_exp_f32 v172,v172
v_mfma_f32_16x16x32_bf16 a[96:99],a[192:195],v[232:235],a[96:99]
ds_read_b64_tr_b16 a[192:193],v2
v_cvt_pk_bf16_f32 v168,v168,v169
v_mfma_f32_16x16x32_bf16 a[96:99],a[196:199],v[236:239],a[96:99]
ds_read_b64_tr_b16 a[194:195],v2 offset:4096
v_cvt_pk_bf16_f32 v169,v170,v171
v_mfma_f32_16x16x32_bf16 a[100:103],a[204:207],v[236:239],a[100:103]
v_exp_f32 v173,v173
v_mfma_f32_16x16x32_bf16 a[100:103],a[200:203],v[232:235],a[100:103]
ds_read_b64_tr_b16 a[196:197],v2 offset:8192
v_cvt_pk_bf16_f32 v170,v172,v173
v_mfma_f32_16x16x32_bf16 a[104:107],a[208:211],v[232:235],a[104:107]
v_exp_f32 v174,v174
v_mfma_f32_16x16x32_bf16 a[104:107],a[212:215],v[236:239],a[104:107]
v_exp_f32 v175,v175
v_mfma_f32_16x16x32_bf16 a[108:111],a[220:223],v[236:239],a[108:111]
buffer_load_dwordx4 v4,s[24:27],s47 offen offset:2048 nt lds
v_mfma_f32_16x16x32_bf16 a[108:111],a[216:219],v[232:235],a[108:111]
ds_read_b64_tr_b16 a[198:199],v2 offset:12288
v_cvt_pk_bf16_f32 v171,v174,v175
v_mfma_f32_16x16x32_bf16 a[112:115],a[224:227],v[232:235],a[112:115]
v_exp_f32 v184,v184
v_mfma_f32_16x16x32_bf16 a[112:115],a[228:231],v[236:239],a[112:115]
v_exp_f32 v185,v185
v_mfma_f32_16x16x32_bf16 a[116:119],a[236:239],v[236:239],a[116:119]
ds_read_b64_tr_b16 a[204:205],v3 offset:8192
v_cvt_pk_bf16_f32 v136,v136,v137
v_mfma_f32_16x16x32_bf16 a[116:119],a[232:235],v[232:235],a[116:119]
ds_read_b64_tr_b16 a[206:207],v3 offset:12288
v_cvt_pk_bf16_f32 v172,v184,v185
v_mfma_f32_16x16x32_bf16 a[120:123],a[240:243],v[232:235],a[120:123]
ds_read_b64_tr_b16 a[200:201],v3
v_mfma_f32_16x16x32_bf16 a[120:123],a[244:247],v[236:239],a[120:123]
ds_read_b64_tr_b16 a[202:203],v3 offset:4096
v_cvt_pk_bf16_f32 v137,v138,v139
v_mfma_f32_16x16x32_bf16 a[124:127],a[252:255],v[236:239],a[124:127]
ds_read_b64_tr_b16 a[208:209],v2 offset:256
v_mfma_f32_16x16x32_bf16 a[124:127],a[248:251],v[232:235],a[124:127]
ds_read_b64_tr_b16 a[210:211],v2 offset:4352
v_smfmac_f32_16x16x64_bf16 v[60:63],v[28:31],v[232:239],v27
s_waitcnt lgkmcnt(6)
ds_read_b64_tr_b16 a[212:213],v2 offset:8448
ds_read_b64_tr_b16 a[214:215],v2 offset:12544
v_cvt_pk_bf16_f32 v138,v140,v141
ds_read_b64_tr_b16 a[220:221],v3 offset:8448
ds_read_b64_tr_b16 a[222:223],v3 offset:12544
v_cvt_pk_bf16_f32 v139,v142,v143
buffer_load_dwordx4 v5,s[24:27],s47 offen offset:3072 nt lds
s_add_u32 s47,s47,16384
s_and_b32 s47,s47,0x3ffff
ds_read_b64_tr_b16 a[216:217],v3 offset:256
s_or_b32 s47,s47,s46
ds_read_b64_tr_b16 a[218:219],v3 offset:4352
v_cvt_pk_bf16_f32 v140,v152,v153
ds_read_b64_tr_b16 a[224:225],v2 offset:512
ds_read_b64_tr_b16 a[226:227],v2 offset:4608
ds_read_b64_tr_b16 a[228:229],v2 offset:8704
v_cvt_pk_bf16_f32 v141,v154,v155
ds_read_b64_tr_b16 a[230:231],v2 offset:12800
ds_read_b64_tr_b16 a[236:237],v3 offset:8704
ds_read_b64_tr_b16 a[238:239],v3 offset:12800
v_cvt_pk_bf16_f32 v142,v156,v157
ds_read_b64_tr_b16 a[232:233],v3 offset:512
ds_read_b64_tr_b16 a[234:235],v3 offset:4608
ds_read_b64_tr_b16 a[240:241],v2 offset:768
v_cvt_pk_bf16_f32 v143,v158,v159
buffer_load_dword v26,v24,s[88:91],s93 offen
v_mfma_f32_16x16x32_bf16 a[0:3],a[192:195],v[136:139],a[0:3]
ds_read_b64_tr_b16 a[242:243],v2 offset:4864
v_mfma_f32_16x16x32_bf16 a[0:3],a[196:199],v[140:143],a[0:3]
s_waitcnt lgkmcnt(15)
ds_read_b64_tr_b16 a[244:245],v2 offset:8960
v_mfma_f32_16x16x32_bf16 a[4:7],a[204:207],v[140:143],a[4:7]
ds_read_b64_tr_b16 a[246:247],v2 offset:13056
v_mfma_f32_16x16x32_bf16 a[4:7],a[200:203],v[136:139],a[4:7]
ds_read_b64_tr_b16 a[252:253],v3 offset:8960
v_mfma_f32_16x16x32_bf16 a[8:11],a[208:211],v[136:139],a[8:11]
s_waitcnt lgkmcnt(15)
ds_read_b64_tr_b16 a[254:255],v3 offset:13056
v_mfma_f32_16x16x32_bf16 a[8:11],a[212:215],v[140:143],a[8:11]
ds_read_b64_tr_b16 a[248:249],v3 offset:768
v_mfma_f32_16x16x32_bf16 a[12:15],a[220:223],v[140:143],a[12:15]
s_waitcnt lgkmcnt(15)
ds_read_b64_tr_b16 a[250:251],v3 offset:4864
v_mfma_f32_16x16x32_bf16 a[12:15],a[216:219],v[136:139],a[12:15]
s_bitcmp1_b32 s45,0
s_cbranch_scc0 L_t31v21_%=
s_mov_b32 m0,s80
s_add_u32 s80,s80,4096
buffer_load_dwordx4 v25,s[24:27],s83 offen nt lds
s_add_u32 s83,s83,4096
L_t31v21_%=:
s_waitcnt lgkmcnt(7)
v_mfma_f32_16x16x32_bf16 a[16:19],a[224:227],v[136:139],a[16:19]
v_exp_f32 v186,v186
v_mfma_f32_16x16x32_bf16 a[16:19],a[228:231],v[140:143],a[16:19]
v_exp_f32 v187,v187
v_mfma_f32_16x16x32_bf16 a[20:23],a[236:239],v[140:143],a[20:23]
v_exp_f32 v188,v188
v_mfma_f32_16x16x32_bf16 a[20:23],a[232:235],v[136:139],a[20:23]
s_waitcnt lgkmcnt(3)
v_exp_f32 v189,v189
v_mfma_f32_16x16x32_bf16 a[24:27],a[240:243],v[136:139],a[24:27]
v_exp_f32 v190,v190
v_mfma_f32_16x16x32_bf16 a[24:27],a[244:247],v[140:143],a[24:27]
s_waitcnt lgkmcnt(1)
v_cvt_pk_bf16_f32 v173,v186,v187
v_mfma_f32_16x16x32_bf16 a[28:31],a[252:255],v[140:143],a[28:31]
s_waitcnt lgkmcnt(0)
v_exp_f32 v191,v191
v_mfma_f32_16x16x32_bf16 a[28:31],a[248:251],v[136:139],a[28:31]
v_cvt_pk_bf16_f32 v174,v188,v189
v_cvt_pk_bf16_f32 v175,v190,v191
s_cmp_lg_u32 s53,0
v_smfmac_f32_16x16x64_bf16 v[48:51],v[28:31],v[136:143],v27
s_cbranch_scc1 L_att1v2_%=
s_waitcnt lgkmcnt(0)
s_nop 7
s_branch L_att1x_%=
.p2align 5
L_att1_%=:
v_mfma_f32_16x16x32_bf16 v[232:235],a[128:131],v[148:151],v[44:47]
s_waitcnt vmcnt(8) lgkmcnt(0)
v_exp_f32 v200,v200
v_mfma_f32_16x16x32_bf16 v[232:235],a[132:135],v[180:183],v[232:235]
s_barrier
s_add_u32 m0,s49,s51
ds_read_b128 a[128:131],v1
v_mfma_f32_16x16x32_bf16 v[232:235],a[136:139],v[212:215],v[232:235]
s_mov_b32 s90,L_out_end_%=-L_pc_%=+128
v_exp_f32 v201,v201
v_mfma_f32_16x16x32_bf16 v[232:235],a[140:143],v[244:247],v[232:235]
s_lshr_b32 s45,s45,1
ds_read_b128 a[132:135],v1 offset:256
v_cvt_pk_bf16_f32 v200,v200,v201
v_mfma_f32_16x16x32_bf16 v[236:239],a[156:159],v[244:247],v[44:47]
s_sub_u32 s94,s94,3
buffer_load_dwordx4 v4,s[20:23],s47 offen nt lds
v_mfma_f32_16x16x32_bf16 v[236:239],a[152:155],v[212:215],v[236:239]
s_lshr_b32 s60,s94,2
ds_read_b128 a[136:139],v1 offset:512
v_add_u32 v2,s48,v2
v_mfma_f32_16x16x32_bf16 v[236:239],a[148:151],v[180:183],v[236:239]
s_max_i32 s60,s60,128
v_exp_f32 v202,v202
v_mfma_f32_16x16x32_bf16 v[236:239],a[144:147],v[148:151],v[236:239]
s_add_u32 s62,s50,32768
ds_read_b128 a[140:143],v1 offset:768
v_add_u32 v3,s48,v3
v_mfma_f32_16x16x32_bf16 v[248:251],a[160:163],v[148:151],v[44:47]
s_and_b32 s62,s62,131071
v_exp_f32 v203,v203
v_mfma_f32_16x16x32_bf16 v[248:251],a[164:167],v[180:183],v[248:251]
s_add_u32 s54,s54,16384
ds_read_b128 a[156:159],v1 offset:4864
v_cvt_pk_bf16_f32 v201,v202,v203
v_mfma_f32_16x16x32_bf16 v[248:251],a[168:171],v[212:215],v[248:251]
s_sub_u32 s48,s62,s50
v_exp_f32 v204,v204
v_mfma_f32_16x16x32_bf16 v[248:251],a[172:175],v[244:247],v[248:251]
s_sub_u32 s53,s53,1
v_exp_f32 v205,v205
v_mfma_f32_16x16x32_bf16 v[252:255],a[188:191],v[244:247],v[44:47]
s_and_b32 s46,s54,0xfffc0000
ds_read_b128 a[152:155],v1 offset:4608
v_cvt_pk_bf16_f32 v202,v204,v205
v_mfma_f32_16x16x32_bf16 v[252:255],a[184:187],v[212:215],v[252:255]
s_mov_b32 s50,s62
v_exp_f32 v206,v206
v_mfma_f32_16x16x32_bf16 v[252:255],a[180:183],v[180:183],v[252:255]
v_exp_f32 v207,v207
v_mfma_f32_16x16x32_bf16 v[252:255],a[176:179],v[148:151],v[252:255]
ds_read_b128 a[148:151],v1 offset:4352
v_cvt_pk_bf16_f32 v203,v206,v207
v_mfma_f32_16x16x32_bf16 a[32:35],a[192:195],v[168:171],a[32:35]
v_exp_f32 v216,v216
v_mfma_f32_16x16x32_bf16 a[32:35],a[196:199],v[172:175],a[32:35]
ds_read_b128 a[144:147],v1 offset:4096
v_mfma_f32_16x16x32_bf16 a[36:39],a[204:207],v[172:175],a[36:39]
buffer_load_dwordx4 v4,s[20:23],s47 offen offset:1024 nt lds
v_mfma_f32_16x16x32_bf16 a[36:39],a[200:203],v[168:171],a[36:39]
ds_read_b128 a[160:163],v1 offset:8192
v_mfma_f32_16x16x32_bf16 a[40:43],a[208:211],v[168:171],a[40:43]
v_exp_f32 v217,v217
v_mfma_f32_16x16x32_bf16 a[40:43],a[212:215],v[172:175],a[40:43]
ds_read_b128 a[164:167],v1 offset:8448
v_cvt_pk_bf16_f32 v204,v216,v217
v_mfma_f32_16x16x32_bf16 a[44:47],a[220:223],v[172:175],a[44:47]
v_exp_f32 v218,v218
v_mfma_f32_16x16x32_bf16 a[44:47],a[216:219],v[168:171],a[44:47]
ds_read_b128 a[168:171],v1 offset:8704
v_mfma_f32_16x16x32_bf16 a[48:51],a[224:227],v[168:171],a[48:51]
v_exp_f32 v219,v219
v_mfma_f32_16x16x32_bf16 a[48:51],a[228:231],v[172:175],a[48:51]
ds_read_b128 a[172:175],v1 offset:8960
v_cvt_pk_bf16_f32 v205,v218,v219
v_mfma_f32_16x16x32_bf16 a[52:55],a[236:239],v[172:175],a[52:55]
v_exp_f32 v220,v220
v_mfma_f32_16x16x32_bf16 a[52:55],a[232:235],v[168:171],a[52:55]
ds_read_b128 a[188:191],v1 offset:13056
v_mfma_f32_16x16x32_bf16 a[56:59],a[240:243],v[168:171],a[56:59]
v_exp_f32 v221,v221
v_mfma_f32_16x16x32_bf16 a[56:59],a[244:247],v[172:175],a[56:59]
ds_read_b128 a[184:187],v1 offset:12800
v_cvt_pk_bf16_f32 v206,v220,v221
v_mfma_f32_16x16x32_bf16 a[60:63],a[252:255],v[172:175],a[60:63]
v_exp_f32 v222,v222
v_mfma_f32_16x16x32_bf16 a[60:63],a[248:251],v[168:171],a[60:63]
ds_read_b128 a[180:183],v1 offset:12544
v_smfmac_f32_16x16x64_bf16 v[52:55],v[28:31],v[168:175],v27
s_waitcnt lgkmcnt(4)
v_mfma_f32_16x16x32_bf16 v[136:139],a[128:131],v[128:131],v[32:35]
buffer_load_dwordx4 v5,s[20:23],s47 offen offset:2048 nt lds
v_mfma_f32_16x16x32_bf16 v[136:139],a[132:135],v[160:163],v[136:139]
ds_read_b128 a[176:179],v1 offset:12288
v_add_u32 v1,s48,v1
v_mfma_f32_16x16x32_bf16 v[136:139],a[136:139],v[192:195],v[136:139]
v_exp_f32 v223,v223
v_mfma_f32_16x16x32_bf16 v[136:139],a[140:143],v[224:227],v[136:139]
v_cvt_pk_bf16_f32 v207,v222,v223
v_mfma_f32_16x16x32_bf16 v[140:143],a[156:159],v[224:227],v[32:35]
v_exp_f32 v232,v232
v_mfma_f32_16x16x32_bf16 v[140:143],a[152:155],v[192:195],v[140:143]
v_exp_f32 v233,v233
v_mfma_f32_16x16x32_bf16 v[140:143],a[148:151],v[160:163],v[140:143]
v_exp_f32 v234,v234
v_mfma_f32_16x16x32_bf16 v[140:143],a[144:147],v[128:131],v[140:143]
v_exp_f32 v235,v235
v_mfma_f32_16x16x32_bf16 v[152:155],a[160:163],v[128:131],v[32:35]
v_exp_f32 v236,v236
v_mfma_f32_16x16x32_bf16 v[152:155],a[164:167],v[160:163],v[152:155]
v_exp_f32 v237,v237
v_mfma_f32_16x16x32_bf16 v[152:155],a[168:171],v[192:195],v[152:155]
s_waitcnt lgkmcnt(0)
v_exp_f32 v238,v238
v_mfma_f32_16x16x32_bf16 v[152:155],a[172:175],v[224:227],v[152:155]
v_exp_f32 v239,v239
v_mfma_f32_16x16x32_bf16 v[156:159],a[188:191],v[224:227],v[32:35]
v_exp_f32 v248,v248
v_mfma_f32_16x16x32_bf16 v[156:159],a[184:187],v[192:195],v[156:159]
buffer_load_dwordx4 v5,s[20:23],s47 offen offset:3072 nt lds
v_mfma_f32_16x16x32_bf16 v[156:159],a[180:183],v[160:163],v[156:159]
v_exp_f32 v249,v249
v_mfma_f32_16x16x32_bf16 v[156:159],a[176:179],v[128:131],v[156:159]
v_exp_f32 v250,v250
v_mfma_f32_16x16x32_bf16 a[64:67],a[192:195],v[200:203],a[64:67]
v_exp_f32 v251,v251
v_mfma_f32_16x16x32_bf16 a[64:67],a[196:199],v[204:207],a[64:67]
v_exp_f32 v252,v252
v_mfma_f32_16x16x32_bf16 a[68:71],a[204:207],v[204:207],a[68:71]
v_exp_f32 v253,v253
v_mfma_f32_16x16x32_bf16 a[68:71],a[200:203],v[200:203],a[68:71]
v_exp_f32 v254,v254
v_mfma_f32_16x16x32_bf16 a[72:75],a[208:211],v[200:203],a[72:75]
v_exp_f32 v255,v255
v_mfma_f32_16x16x32_bf16 a[72:75],a[212:215],v[204:207],a[72:75]
v_exp_f32 v136,v136
v_mfma_f32_16x16x32_bf16 a[76:79],a[220:223],v[204:207],a[76:79]
v_exp_f32 v137,v137
v_mfma_f32_16x16x32_bf16 a[76:79],a[216:219],v[200:203],a[76:79]
v_exp_f32 v138,v138
v_mfma_f32_16x16x32_bf16 a[80:83],a[224:227],v[200:203],a[80:83]
s_add_u32 m0,s49,s52
v_cvt_pk_bf16_f32 v232,v232,v233
v_cvt_pk_bf16_f32 v233,v234,v235
v_mfma_f32_16x16x32_bf16 a[80:83],a[228:231],v[204:207],a[80:83]
s_xor_b32 s49,s50,65536
v_cvt_pk_bf16_f32 v234,v236,v237
v_cvt_pk_bf16_f32 v235,v238,v239
v_mfma_f32_16x16x32_bf16 a[84:87],a[236:239],v[204:207],a[84:87]
v_cvt_pk_bf16_f32 v236,v248,v249
v_cvt_pk_bf16_f32 v237,v250,v251
v_mfma_f32_16x16x32_bf16 a[84:87],a[232:235],v[200:203],a[84:87]
buffer_load_dwordx4 v4,s[24:27],s47 offen nt lds
v_mfma_f32_16x16x32_bf16 a[88:91],a[240:243],v[200:203],a[88:91]
v_cvt_pk_bf16_f32 v238,v252,v253
v_cvt_pk_bf16_f32 v239,v254,v255
v_mfma_f32_16x16x32_bf16 a[88:91],a[244:247],v[204:207],a[88:91]
v_exp_f32 v139,v139
v_mfma_f32_16x16x32_bf16 a[92:95],a[252:255],v[204:207],a[92:95]
)A"
R"A(v_exp_f32 v140,v140
v_mfma_f32_16x16x32_bf16 a[92:95],a[248:251],v[200:203],a[92:95]
v_exp_f32 v141,v141
v_smfmac_f32_16x16x64_bf16 v[56:59],v[28:31],v[200:207],v27
v_mfma_f32_16x16x32_bf16 v[168:171],a[128:131],v[132:135],v[36:39]
v_exp_f32 v142,v142
v_mfma_f32_16x16x32_bf16 v[168:171],a[132:135],v[164:167],v[168:171]
v_exp_f32 v143,v143
v_mfma_f32_16x16x32_bf16 v[168:171],a[136:139],v[196:199],v[168:171]
v_exp_f32 v152,v152
v_mfma_f32_16x16x32_bf16 v[168:171],a[140:143],v[228:231],v[168:171]
v_exp_f32 v153,v153
v_mfma_f32_16x16x32_bf16 v[172:175],a[156:159],v[228:231],v[36:39]
v_exp_f32 v154,v154
v_mfma_f32_16x16x32_bf16 v[172:175],a[152:155],v[196:199],v[172:175]
v_exp_f32 v155,v155
v_mfma_f32_16x16x32_bf16 v[172:175],a[148:151],v[164:167],v[172:175]
v_exp_f32 v156,v156
v_mfma_f32_16x16x32_bf16 v[172:175],a[144:147],v[132:135],v[172:175]
v_exp_f32 v157,v157
v_mfma_f32_16x16x32_bf16 v[184:187],a[160:163],v[132:135],v[36:39]
buffer_load_dwordx4 v5,s[24:27],s47 offen offset:1024 nt lds
v_mfma_f32_16x16x32_bf16 v[184:187],a[164:167],v[164:167],v[184:187]
v_exp_f32 v158,v158
v_mfma_f32_16x16x32_bf16 v[184:187],a[168:171],v[196:199],v[184:187]
v_exp_f32 v159,v159
v_mfma_f32_16x16x32_bf16 v[184:187],a[172:175],v[228:231],v[184:187]
v_exp_f32 v168,v168
v_mfma_f32_16x16x32_bf16 v[188:191],a[188:191],v[228:231],v[36:39]
v_exp_f32 v169,v169
v_mfma_f32_16x16x32_bf16 v[188:191],a[184:187],v[196:199],v[188:191]
v_exp_f32 v170,v170
v_mfma_f32_16x16x32_bf16 v[188:191],a[180:183],v[164:167],v[188:191]
v_exp_f32 v171,v171
v_mfma_f32_16x16x32_bf16 v[188:191],a[176:179],v[132:135],v[188:191]
v_exp_f32 v172,v172
v_mfma_f32_16x16x32_bf16 a[96:99],a[192:195],v[232:235],a[96:99]
ds_read_b64_tr_b16 a[192:193],v2
v_cvt_pk_bf16_f32 v168,v168,v169
v_mfma_f32_16x16x32_bf16 a[96:99],a[196:199],v[236:239],a[96:99]
ds_read_b64_tr_b16 a[194:195],v2 offset:4096
v_cvt_pk_bf16_f32 v169,v170,v171
v_mfma_f32_16x16x32_bf16 a[100:103],a[204:207],v[236:239],a[100:103]
v_exp_f32 v173,v173
v_mfma_f32_16x16x32_bf16 a[100:103],a[200:203],v[232:235],a[100:103]
ds_read_b64_tr_b16 a[196:197],v2 offset:8192
v_cvt_pk_bf16_f32 v170,v172,v173
v_mfma_f32_16x16x32_bf16 a[104:107],a[208:211],v[232:235],a[104:107]
v_exp_f32 v174,v174
v_mfma_f32_16x16x32_bf16 a[104:107],a[212:215],v[236:239],a[104:107]
v_exp_f32 v175,v175
v_mfma_f32_16x16x32_bf16 a[108:111],a[220:223],v[236:239],a[108:111]
buffer_load_dwordx4 v4,s[24:27],s47 offen offset:2048 nt lds
v_mfma_f32_16x16x32_bf16 a[108:111],a[216:219],v[232:235],a[108:111]
ds_read_b64_tr_b16 a[198:199],v2 offset:12288
v_cvt_pk_bf16_f32 v171,v174,v175
v_mfma_f32_16x16x32_bf16 a[112:115],a[224:227],v[232:235],a[112:115]
v_exp_f32 v184,v184
v_mfma_f32_16x16x32_bf16 a[112:115],a[228:231],v[236:239],a[112:115]
v_exp_f32 v185,v185
v_mfma_f32_16x16x32_bf16 a[116:119],a[236:239],v[236:239],a[116:119]
ds_read_b64_tr_b16 a[204:205],v3 offset:8192
v_cvt_pk_bf16_f32 v136,v136,v137
v_mfma_f32_16x16x32_bf16 a[116:119],a[232:235],v[232:235],a[116:119]
ds_read_b64_tr_b16 a[206:207],v3 offset:12288
v_cvt_pk_bf16_f32 v172,v184,v185
v_mfma_f32_16x16x32_bf16 a[120:123],a[240:243],v[232:235],a[120:123]
ds_read_b64_tr_b16 a[200:201],v3
v_mfma_f32_16x16x32_bf16 a[120:123],a[244:247],v[236:239],a[120:123]
ds_read_b64_tr_b16 a[202:203],v3 offset:4096
v_cvt_pk_bf16_f32 v137,v138,v139
v_mfma_f32_16x16x32_bf16 a[124:127],a[252:255],v[236:239],a[124:127]
ds_read_b64_tr_b16 a[208:209],v2 offset:256
v_mfma_f32_16x16x32_bf16 a[124:127],a[248:251],v[232:235],a[124:127]
ds_read_b64_tr_b16 a[210:211],v2 offset:4352
v_smfmac_f32_16x16x64_bf16 v[60:63],v[28:31],v[232:239],v27
ds_read_b64_tr_b16 a[212:213],v2 offset:8448
v_mfma_f32_16x16x32_bf16 v[200:203],a[128:131],v[144:147],v[40:43]
ds_read_b64_tr_b16 a[214:215],v2 offset:12544
v_cvt_pk_bf16_f32 v138,v140,v141
v_mfma_f32_16x16x32_bf16 v[200:203],a[132:135],v[176:179],v[200:203]
ds_read_b64_tr_b16 a[220:221],v3 offset:8448
v_mfma_f32_16x16x32_bf16 v[200:203],a[136:139],v[208:211],v[200:203]
ds_read_b64_tr_b16 a[222:223],v3 offset:12544
v_cvt_pk_bf16_f32 v139,v142,v143
v_mfma_f32_16x16x32_bf16 v[200:203],a[140:143],v[240:243],v[200:203]
buffer_load_dwordx4 v5,s[24:27],s47 offen offset:3072 nt lds
v_mfma_f32_16x16x32_bf16 v[204:207],a[156:159],v[240:243],v[40:43]
s_add_u32 s47,s47,16384
s_and_b32 s47,s47,0x3ffff
ds_read_b64_tr_b16 a[216:217],v3 offset:256
v_mfma_f32_16x16x32_bf16 v[204:207],a[152:155],v[208:211],v[204:207]
s_or_b32 s47,s47,s46
ds_read_b64_tr_b16 a[218:219],v3 offset:4352
v_cvt_pk_bf16_f32 v140,v152,v153
v_mfma_f32_16x16x32_bf16 v[204:207],a[148:151],v[176:179],v[204:207]
ds_read_b64_tr_b16 a[224:225],v2 offset:512
v_mfma_f32_16x16x32_bf16 v[204:207],a[144:147],v[144:147],v[204:207]
ds_read_b64_tr_b16 a[226:227],v2 offset:4608
v_mfma_f32_16x16x32_bf16 v[216:219],a[160:163],v[144:147],v[40:43]
ds_read_b64_tr_b16 a[228:229],v2 offset:8704
v_cvt_pk_bf16_f32 v141,v154,v155
v_mfma_f32_16x16x32_bf16 v[216:219],a[164:167],v[176:179],v[216:219]
ds_read_b64_tr_b16 a[230:231],v2 offset:12800
v_mfma_f32_16x16x32_bf16 v[216:219],a[168:171],v[208:211],v[216:219]
ds_read_b64_tr_b16 a[236:237],v3 offset:8704
v_mfma_f32_16x16x32_bf16 v[216:219],a[172:175],v[240:243],v[216:219]
ds_read_b64_tr_b16 a[238:239],v3 offset:12800
v_cvt_pk_bf16_f32 v142,v156,v157
v_mfma_f32_16x16x32_bf16 v[220:223],a[188:191],v[240:243],v[40:43]
ds_read_b64_tr_b16 a[232:233],v3 offset:512
v_mfma_f32_16x16x32_bf16 v[220:223],a[184:187],v[208:211],v[220:223]
ds_read_b64_tr_b16 a[234:235],v3 offset:4608
v_mfma_f32_16x16x32_bf16 v[220:223],a[180:183],v[176:179],v[220:223]
ds_read_b64_tr_b16 a[240:241],v2 offset:768
v_cvt_pk_bf16_f32 v143,v158,v159
v_mfma_f32_16x16x32_bf16 v[220:223],a[176:179],v[144:147],v[220:223]
s_waitcnt lgkmcnt(7)
buffer_load_dword v26,v24,s[88:91],s93 offen
v_mfma_f32_16x16x32_bf16 a[0:3],a[192:195],v[136:139],a[0:3]
ds_read_b64_tr_b16 a[242:243],v2 offset:4864
v_mfma_f32_16x16x32_bf16 a[0:3],a[196:199],v[140:143],a[0:3]
ds_read_b64_tr_b16 a[244:245],v2 offset:8960
v_mfma_f32_16x16x32_bf16 a[4:7],a[204:207],v[140:143],a[4:7]
ds_read_b64_tr_b16 a[246:247],v2 offset:13056
v_mfma_f32_16x16x32_bf16 a[4:7],a[200:203],v[136:139],a[4:7]
s_memrealtime s[76:77]
ds_read_b64_tr_b16 a[252:253],v3 offset:8960
v_mfma_f32_16x16x32_bf16 a[8:11],a[208:211],v[136:139],a[8:11]
ds_read_b64_tr_b16 a[254:255],v3 offset:13056
v_mfma_f32_16x16x32_bf16 a[8:11],a[212:215],v[140:143],a[8:11]
ds_read_b64_tr_b16 a[248:249],v3 offset:768
v_mfma_f32_16x16x32_bf16 a[12:15],a[220:223],v[140:143],a[12:15]
ds_read_b64_tr_b16 a[250:251],v3 offset:4864
v_mfma_f32_16x16x32_bf16 a[12:15],a[216:219],v[136:139],a[12:15]
s_bitcmp1_b32 s45,0
s_cbranch_scc0 L_t311_%=
s_mov_b32 m0,s80
s_add_u32 s80,s80,4096
buffer_load_dwordx4 v25,s[24:27],s83 offen nt lds
s_add_u32 s83,s83,4096
L_t311_%=:
v_mfma_f32_16x16x32_bf16 a[16:19],a[224:227],v[136:139],a[16:19]
s_waitcnt lgkmcnt(6)
v_exp_f32 v186,v186
v_mfma_f32_16x16x32_bf16 a[16:19],a[228:231],v[140:143],a[16:19]
v_exp_f32 v187,v187
v_mfma_f32_16x16x32_bf16 a[20:23],a[236:239],v[140:143],a[20:23]
s_waitcnt lgkmcnt(0)
v_exp_f32 v188,v188
v_mfma_f32_16x16x32_bf16 a[20:23],a[232:235],v[136:139],a[20:23]
s_sub_u32 s79,s78,s76
v_exp_f32 v189,v189
v_mfma_f32_16x16x32_bf16 a[24:27],a[240:243],v[136:139],a[24:27]
s_add_u32 s78,s76,s60
v_exp_f32 v190,v190
v_mfma_f32_16x16x32_bf16 a[24:27],a[244:247],v[140:143],a[24:27]
s_cmp_gt_i32 s79,s95
s_cbranch_scc1 L_ps7_%=
L_pr7_%=:
v_cvt_pk_bf16_f32 v173,v186,v187
v_mfma_f32_16x16x32_bf16 a[28:31],a[252:255],v[140:143],a[28:31]
v_exp_f32 v191,v191
v_mfma_f32_16x16x32_bf16 a[28:31],a[248:251],v[136:139],a[28:31]
v_cvt_pk_bf16_f32 v174,v188,v189
v_cvt_pk_bf16_f32 v175,v190,v191
s_cmp_lg_u32 s53,0
v_smfmac_f32_16x16x64_bf16 v[48:51],v[28:31],v[136:143],v27
s_cbranch_scc1 L_att1_%=
L_att1x_%=:
L_endseg_%=:
v_mfma_f32_16x16x32_bf16 v[232:235],a[128:131],v[148:151],v[44:47]
s_waitcnt vmcnt(10)
s_lshl_b32 s43,s41,2
v_exp_f32 v200,v200
v_mfma_f32_16x16x32_bf16 v[232:235],a[132:135],v[180:183],v[232:235]
s_barrier
ds_read_b128 a[128:131],v1
v_add_u32 v2,s48,v2
v_mfma_f32_16x16x32_bf16 v[232:235],a[136:139],v[212:215],v[232:235]
s_add_u32 s43,s43,s70
v_exp_f32 v201,v201
v_mfma_f32_16x16x32_bf16 v[232:235],a[140:143],v[244:247],v[232:235]
s_lshl_b32 s43,s43,19
ds_read_b128 a[132:135],v1 offset:256
v_cvt_pk_bf16_f32 v200,v200,v201
v_mfma_f32_16x16x32_bf16 v[236:239],a[156:159],v[244:247],v[44:47]
s_add_u32 s43,s43,s55
v_exp_f32 v202,v202
v_mfma_f32_16x16x32_bf16 v[236:239],a[152:155],v[212:215],v[236:239]
s_mov_b64 s[28:29],s[12:13]
ds_read_b128 a[136:139],v1 offset:512
v_add_u32 v3,s48,v3
v_mfma_f32_16x16x32_bf16 v[236:239],a[148:151],v[180:183],v[236:239]
s_mov_b32 s48,32768
v_exp_f32 v203,v203
v_mfma_f32_16x16x32_bf16 v[236:239],a[144:147],v[148:151],v[236:239]
s_mov_b32 s30,475136
ds_read_b128 a[140:143],v1 offset:768
v_cvt_pk_bf16_f32 v201,v202,v203
v_mfma_f32_16x16x32_bf16 v[248:251],a[160:163],v[148:151],v[44:47]
s_mov_b32 s31,0x20000
v_exp_f32 v204,v204
v_mfma_f32_16x16x32_bf16 v[248:251],a[164:167],v[180:183],v[248:251]
ds_read_b128 a[156:159],v1 offset:4864
v_lshrrev_b32 v4,6,v6
v_mfma_f32_16x16x32_bf16 v[248:251],a[168:171],v[212:215],v[248:251]
v_exp_f32 v205,v205
v_mfma_f32_16x16x32_bf16 v[248:251],a[172:175],v[244:247],v[248:251]
ds_read_b128 a[152:155],v1 offset:4608
v_cvt_pk_bf16_f32 v202,v204,v205
v_mfma_f32_16x16x32_bf16 v[252:255],a[188:191],v[244:247],v[44:47]
v_exp_f32 v206,v206
v_mfma_f32_16x16x32_bf16 v[252:255],a[184:187],v[212:215],v[252:255]
ds_read_b128 a[148:151],v1 offset:4352
v_mul_u32_u24 v4,320,v4
v_mfma_f32_16x16x32_bf16 v[252:255],a[180:183],v[180:183],v[252:255]
v_exp_f32 v207,v207
v_mfma_f32_16x16x32_bf16 v[252:255],a[176:179],v[148:151],v[252:255]
ds_read_b128 a[144:147],v1 offset:4096
v_cvt_pk_bf16_f32 v203,v206,v207
v_mfma_f32_16x16x32_bf16 a[32:35],a[192:195],v[168:171],a[32:35]
v_exp_f32 v216,v216
v_mfma_f32_16x16x32_bf16 a[32:35],a[196:199],v[172:175],a[32:35]
ds_read_b128 a[160:163],v1 offset:8192
v_bfe_u32 v5,v6,4,2
v_mfma_f32_16x16x32_bf16 a[36:39],a[204:207],v[172:175],a[36:39]
v_exp_f32 v217,v217
v_mfma_f32_16x16x32_bf16 a[36:39],a[200:203],v[168:171],a[36:39]
ds_read_b128 a[164:167],v1 offset:8448
v_cvt_pk_bf16_f32 v204,v216,v217
v_mfma_f32_16x16x32_bf16 a[40:43],a[208:211],v[168:171],a[40:43]
v_exp_f32 v218,v218
v_mfma_f32_16x16x32_bf16 a[40:43],a[212:215],v[172:175],a[40:43]
ds_read_b128 a[168:171],v1 offset:8704
v_lshl_add_u32 v4,v5,3,v4
v_mfma_f32_16x16x32_bf16 a[44:47],a[220:223],v[172:175],a[44:47]
v_exp_f32 v219,v219
v_mfma_f32_16x16x32_bf16 a[44:47],a[216:219],v[168:171],a[44:47]
ds_read_b128 a[172:175],v1 offset:8960
v_cvt_pk_bf16_f32 v205,v218,v219
v_mfma_f32_16x16x32_bf16 a[48:51],a[224:227],v[168:171],a[48:51]
v_exp_f32 v220,v220
v_mfma_f32_16x16x32_bf16 a[48:51],a[228:231],v[172:175],a[48:51]
ds_read_b128 a[188:191],v1 offset:13056
v_bfe_u32 v5,v6,8,1
v_mfma_f32_16x16x32_bf16 a[52:55],a[236:239],v[172:175],a[52:55]
v_exp_f32 v221,v221
v_mfma_f32_16x16x32_bf16 a[52:55],a[232:235],v[168:171],a[52:55]
ds_read_b128 a[184:187],v1 offset:12800
v_cvt_pk_bf16_f32 v206,v220,v221
v_mfma_f32_16x16x32_bf16 a[56:59],a[240:243],v[168:171],a[56:59]
v_exp_f32 v222,v222
v_mfma_f32_16x16x32_bf16 a[56:59],a[244:247],v[172:175],a[56:59]
ds_read_b128 a[180:183],v1 offset:12544
v_lshlrev_b32 v5,5,v5
v_mfma_f32_16x16x32_bf16 a[60:63],a[252:255],v[172:175],a[60:63]
v_exp_f32 v223,v223
v_mfma_f32_16x16x32_bf16 a[60:63],a[248:251],v[168:171],a[60:63]
ds_read_b128 a[176:179],v1 offset:12288
v_cvt_pk_bf16_f32 v207,v222,v223
v_smfmac_f32_16x16x64_bf16 v[52:55],v[28:31],v[168:175],v27
s_waitcnt lgkmcnt(4)
v_add_u32 v1,s48,v1
v_mfma_f32_16x16x32_bf16 v[136:139],a[128:131],v[128:131],v[32:35]
v_exp_f32 v232,v232
v_mfma_f32_16x16x32_bf16 v[136:139],a[132:135],v[160:163],v[136:139]
v_exp_f32 v233,v233
v_mfma_f32_16x16x32_bf16 v[136:139],a[136:139],v[192:195],v[136:139]
v_exp_f32 v234,v234
v_mfma_f32_16x16x32_bf16 v[136:139],a[140:143],v[224:227],v[136:139]
v_exp_f32 v235,v235
v_mfma_f32_16x16x32_bf16 v[140:143],a[156:159],v[224:227],v[32:35]
)A"
R"A(v_cvt_pk_bf16_f32 v232,v232,v233
v_cvt_pk_bf16_f32 v233,v234,v235
v_mfma_f32_16x16x32_bf16 v[140:143],a[152:155],v[192:195],v[140:143]
s_add_u32 s80,s71,0
v_exp_f32 v236,v236
v_mfma_f32_16x16x32_bf16 v[140:143],a[148:151],v[160:163],v[140:143]
s_and_b32 s80,s80,3
v_exp_f32 v237,v237
v_mfma_f32_16x16x32_bf16 v[140:143],a[144:147],v[128:131],v[140:143]
s_lshl_b32 s80,s80,1
s_mul_i32 s80,s80,163840
s_add_u32 s80,s80,s81
v_mfma_f32_16x16x32_bf16 v[152:155],a[160:163],v[128:131],v[32:35]
s_add_u32 s83,s80,20480
v_exp_f32 v238,v238
v_mfma_f32_16x16x32_bf16 v[152:155],a[164:167],v[160:163],v[152:155]
buffer_store_dwordx4 v[64:67],v6,s[36:39],s84 offen sc1
v_mfma_f32_16x16x32_bf16 v[152:155],a[168:171],v[192:195],v[152:155]
v_exp_f32 v239,v239
v_mfma_f32_16x16x32_bf16 v[152:155],a[172:175],v[224:227],v[152:155]
s_waitcnt lgkmcnt(0)
v_cvt_pk_bf16_f32 v234,v236,v237
v_cvt_pk_bf16_f32 v235,v238,v239
v_mfma_f32_16x16x32_bf16 v[156:159],a[188:191],v[224:227],v[32:35]
v_exp_f32 v248,v248
v_mfma_f32_16x16x32_bf16 v[156:159],a[184:187],v[192:195],v[156:159]
v_exp_f32 v249,v249
v_mfma_f32_16x16x32_bf16 v[156:159],a[180:183],v[160:163],v[156:159]
v_exp_f32 v250,v250
v_mfma_f32_16x16x32_bf16 v[156:159],a[176:179],v[128:131],v[156:159]
v_exp_f32 v251,v251
v_mfma_f32_16x16x32_bf16 a[64:67],a[192:195],v[200:203],a[64:67]
v_exp_f32 v252,v252
v_mfma_f32_16x16x32_bf16 a[64:67],a[196:199],v[204:207],a[64:67]
v_exp_f32 v253,v253
v_mfma_f32_16x16x32_bf16 a[68:71],a[204:207],v[204:207],a[68:71]
v_exp_f32 v254,v254
v_mfma_f32_16x16x32_bf16 a[68:71],a[200:203],v[200:203],a[68:71]
v_exp_f32 v255,v255
v_mfma_f32_16x16x32_bf16 a[72:75],a[208:211],v[200:203],a[72:75]
v_cvt_pk_bf16_f32 v236,v248,v249
v_cvt_pk_bf16_f32 v237,v250,v251
v_mfma_f32_16x16x32_bf16 a[72:75],a[212:215],v[204:207],a[72:75]
v_cvt_pk_bf16_f32 v238,v252,v253
v_cvt_pk_bf16_f32 v239,v254,v255
v_mfma_f32_16x16x32_bf16 a[76:79],a[220:223],v[204:207],a[76:79]
v_exp_f32 v136,v136
v_mfma_f32_16x16x32_bf16 a[76:79],a[216:219],v[200:203],a[76:79]
v_exp_f32 v137,v137
v_mfma_f32_16x16x32_bf16 a[80:83],a[224:227],v[200:203],a[80:83]
v_exp_f32 v138,v138
v_mfma_f32_16x16x32_bf16 a[80:83],a[228:231],v[204:207],a[80:83]
v_exp_f32 v139,v139
v_mfma_f32_16x16x32_bf16 a[84:87],a[236:239],v[204:207],a[84:87]
s_add_u32 m0,s82,81920
v_exp_f32 v140,v140
v_mfma_f32_16x16x32_bf16 a[84:87],a[232:235],v[200:203],a[84:87]
v_cvt_pk_bf16_f32 v136,v136,v137
v_cvt_pk_bf16_f32 v137,v138,v139
v_mfma_f32_16x16x32_bf16 a[88:91],a[240:243],v[200:203],a[88:91]
v_exp_f32 v141,v141
v_mfma_f32_16x16x32_bf16 a[88:91],a[244:247],v[204:207],a[88:91]
buffer_load_dwordx4 v7,s[72:75],s80 offen lds
v_mfma_f32_16x16x32_bf16 a[92:95],a[252:255],v[204:207],a[92:95]
v_exp_f32 v142,v142
v_mfma_f32_16x16x32_bf16 a[92:95],a[248:251],v[200:203],a[92:95]
v_exp_f32 v143,v143
v_smfmac_f32_16x16x64_bf16 v[56:59],v[28:31],v[200:207],v27
v_cvt_pk_bf16_f32 v138,v140,v141
v_mfma_f32_16x16x32_bf16 v[168:171],a[128:131],v[132:135],v[36:39]
buffer_store_dwordx4 v[68:71],v6,s[36:39],s84 offen offset:1024 sc1
v_mfma_f32_16x16x32_bf16 v[168:171],a[132:135],v[164:167],v[168:171]
s_add_u32 s28,s28,s43
s_addc_u32 s29,s29,0
v_cvt_pk_bf16_f32 v139,v142,v143
v_mfma_f32_16x16x32_bf16 v[168:171],a[136:139],v[196:199],v[168:171]
s_and_b32 s29,s29,0xffff
v_exp_f32 v152,v152
v_mfma_f32_16x16x32_bf16 v[168:171],a[140:143],v[228:231],v[168:171]
v_exp_f32 v153,v153
v_mfma_f32_16x16x32_bf16 v[172:175],a[156:159],v[228:231],v[36:39]
v_exp_f32 v154,v154
v_mfma_f32_16x16x32_bf16 v[172:175],a[152:155],v[196:199],v[172:175]
v_exp_f32 v155,v155
v_mfma_f32_16x16x32_bf16 v[172:175],a[148:151],v[164:167],v[172:175]
v_exp_f32 v156,v156
v_mfma_f32_16x16x32_bf16 v[172:175],a[144:147],v[132:135],v[172:175]
v_exp_f32 v157,v157
v_mfma_f32_16x16x32_bf16 v[184:187],a[160:163],v[132:135],v[36:39]
v_exp_f32 v158,v158
v_mfma_f32_16x16x32_bf16 v[184:187],a[164:167],v[164:167],v[184:187]
v_exp_f32 v159,v159
v_mfma_f32_16x16x32_bf16 v[184:187],a[168:171],v[196:199],v[184:187]
v_cvt_pk_bf16_f32 v140,v152,v153
v_cvt_pk_bf16_f32 v141,v154,v155
v_mfma_f32_16x16x32_bf16 v[184:187],a[172:175],v[228:231],v[184:187]
v_cvt_pk_bf16_f32 v142,v156,v157
v_cvt_pk_bf16_f32 v143,v158,v159
v_mfma_f32_16x16x32_bf16 v[188:191],a[188:191],v[228:231],v[36:39]
v_exp_f32 v168,v168
v_mfma_f32_16x16x32_bf16 v[188:191],a[184:187],v[196:199],v[188:191]
v_exp_f32 v169,v169
v_mfma_f32_16x16x32_bf16 v[188:191],a[180:183],v[164:167],v[188:191]
v_exp_f32 v170,v170
v_mfma_f32_16x16x32_bf16 v[188:191],a[176:179],v[132:135],v[188:191]
v_exp_f32 v171,v171
v_mfma_f32_16x16x32_bf16 a[96:99],a[192:195],v[232:235],a[96:99]
buffer_load_dwordx4 v8,s[72:75],s80 offen offset:1024 lds
v_mfma_f32_16x16x32_bf16 a[96:99],a[196:199],v[236:239],a[96:99]
ds_read_b64_tr_b16 a[192:193],v2
ds_read_b64_tr_b16 a[194:195],v2 offset:4096
v_mfma_f32_16x16x32_bf16 a[100:103],a[204:207],v[236:239],a[100:103]
ds_read_b64_tr_b16 a[196:197],v2 offset:8192
v_cvt_pk_bf16_f32 v168,v168,v169
v_mfma_f32_16x16x32_bf16 a[100:103],a[200:203],v[232:235],a[100:103]
ds_read_b64_tr_b16 a[198:199],v2 offset:12288
v_cvt_pk_bf16_f32 v169,v170,v171
v_mfma_f32_16x16x32_bf16 a[104:107],a[208:211],v[232:235],a[104:107]
buffer_store_dwordx4 v[72:75],v6,s[36:39],s84 offen offset:2048 sc1
v_mfma_f32_16x16x32_bf16 a[104:107],a[212:215],v[236:239],a[104:107]
ds_read_b64_tr_b16 a[204:205],v3 offset:8192
ds_read_b64_tr_b16 a[206:207],v3 offset:12288
v_mfma_f32_16x16x32_bf16 a[108:111],a[220:223],v[236:239],a[108:111]
v_exp_f32 v172,v172
v_mfma_f32_16x16x32_bf16 a[108:111],a[216:219],v[232:235],a[108:111]
ds_read_b64_tr_b16 a[200:201],v3
ds_read_b64_tr_b16 a[202:203],v3 offset:4096
v_mfma_f32_16x16x32_bf16 a[112:115],a[224:227],v[232:235],a[112:115]
v_exp_f32 v173,v173
v_mfma_f32_16x16x32_bf16 a[112:115],a[228:231],v[236:239],a[112:115]
ds_read_b64_tr_b16 a[208:209],v2 offset:256
ds_read_b64_tr_b16 a[210:211],v2 offset:4352
v_mfma_f32_16x16x32_bf16 a[116:119],a[236:239],v[236:239],a[116:119]
ds_read_b64_tr_b16 a[212:213],v2 offset:8448
v_cvt_pk_bf16_f32 v170,v172,v173
v_mfma_f32_16x16x32_bf16 a[116:119],a[232:235],v[232:235],a[116:119]
v_exp_f32 v174,v174
v_mfma_f32_16x16x32_bf16 a[120:123],a[240:243],v[232:235],a[120:123]
ds_read_b64_tr_b16 a[214:215],v2 offset:12544
ds_read_b64_tr_b16 a[220:221],v3 offset:8448
v_mfma_f32_16x16x32_bf16 a[120:123],a[244:247],v[236:239],a[120:123]
ds_read_b64_tr_b16 a[222:223],v3 offset:12544
v_mfma_f32_16x16x32_bf16 a[124:127],a[252:255],v[236:239],a[124:127]
ds_read_b64_tr_b16 a[216:217],v3 offset:256
v_mfma_f32_16x16x32_bf16 a[124:127],a[248:251],v[232:235],a[124:127]
ds_read_b64_tr_b16 a[218:219],v3 offset:4352
v_smfmac_f32_16x16x64_bf16 v[60:63],v[28:31],v[232:239],v27
ds_read_b64_tr_b16 a[224:225],v2 offset:512
v_mfma_f32_16x16x32_bf16 v[200:203],a[128:131],v[144:147],v[40:43]
ds_read_b64_tr_b16 a[226:227],v2 offset:4608
ds_read_b64_tr_b16 a[228:229],v2 offset:8704
v_mfma_f32_16x16x32_bf16 v[200:203],a[132:135],v[176:179],v[200:203]
ds_read_b64_tr_b16 a[230:231],v2 offset:12800
v_mfma_f32_16x16x32_bf16 v[200:203],a[136:139],v[208:211],v[200:203]
ds_read_b64_tr_b16 a[236:237],v3 offset:8704
v_mfma_f32_16x16x32_bf16 v[200:203],a[140:143],v[240:243],v[200:203]
buffer_load_dwordx4 v9,s[72:75],s80 offen offset:2048 lds
v_mfma_f32_16x16x32_bf16 v[204:207],a[156:159],v[240:243],v[40:43]
ds_read_b64_tr_b16 a[238:239],v3 offset:12800
ds_read_b64_tr_b16 a[232:233],v3 offset:512
v_mfma_f32_16x16x32_bf16 v[204:207],a[152:155],v[208:211],v[204:207]
ds_read_b64_tr_b16 a[234:235],v3 offset:4608
v_mfma_f32_16x16x32_bf16 v[204:207],a[148:151],v[176:179],v[204:207]
ds_read_b64_tr_b16 a[240:241],v2 offset:768
v_mfma_f32_16x16x32_bf16 v[204:207],a[144:147],v[144:147],v[204:207]
ds_read_b64_tr_b16 a[242:243],v2 offset:4864
v_mfma_f32_16x16x32_bf16 v[216:219],a[160:163],v[144:147],v[40:43]
ds_read_b64_tr_b16 a[244:245],v2 offset:8960
ds_read_b64_tr_b16 a[246:247],v2 offset:13056
v_mfma_f32_16x16x32_bf16 v[216:219],a[164:167],v[176:179],v[216:219]
buffer_store_dwordx4 v[76:79],v6,s[36:39],s84 offen offset:3072 sc1
v_mfma_f32_16x16x32_bf16 v[216:219],a[168:171],v[208:211],v[216:219]
s_add_u32 s84,s55,65536
ds_read_b64_tr_b16 a[252:253],v3 offset:8960
ds_read_b64_tr_b16 a[254:255],v3 offset:13056
v_mfma_f32_16x16x32_bf16 v[216:219],a[172:175],v[240:243],v[216:219]
s_max_i32 s84,s84,s18
ds_read_b64_tr_b16 a[248:249],v3 offset:768
v_add_u32 v2,s48,v2
v_mfma_f32_16x16x32_bf16 v[220:223],a[188:191],v[240:243],v[40:43]
ds_read_b64_tr_b16 a[250:251],v3 offset:4864
v_add_u32 v3,s48,v3
v_mfma_f32_16x16x32_bf16 v[220:223],a[184:187],v[208:211],v[220:223]
s_mov_b32 s48,98304
v_exp_f32 v175,v175
v_mfma_f32_16x16x32_bf16 v[220:223],a[180:183],v[176:179],v[220:223]
v_exp_f32 v184,v184
v_mfma_f32_16x16x32_bf16 v[220:223],a[176:179],v[144:147],v[220:223]
s_waitcnt lgkmcnt(6)
v_exp_f32 v185,v185
v_mfma_f32_16x16x32_bf16 a[0:3],a[192:195],v[136:139],a[0:3]
v_cvt_pk_bf16_f32 v171,v174,v175
v_cvt_pk_bf16_f32 v172,v184,v185
v_mfma_f32_16x16x32_bf16 a[0:3],a[196:199],v[140:143],a[0:3]
v_exp_f32 v186,v186
v_mfma_f32_16x16x32_bf16 a[4:7],a[204:207],v[140:143],a[4:7]
v_exp_f32 v187,v187
v_mfma_f32_16x16x32_bf16 a[4:7],a[200:203],v[136:139],a[4:7]
v_exp_f32 v188,v188
v_mfma_f32_16x16x32_bf16 a[8:11],a[208:211],v[136:139],a[8:11]
v_exp_f32 v189,v189
v_mfma_f32_16x16x32_bf16 a[8:11],a[212:215],v[140:143],a[8:11]
v_exp_f32 v190,v190
v_mfma_f32_16x16x32_bf16 a[12:15],a[220:223],v[140:143],a[12:15]
v_exp_f32 v191,v191
v_mfma_f32_16x16x32_bf16 a[12:15],a[216:219],v[136:139],a[12:15]
buffer_load_dwordx4 v10,s[72:75],s80 offen offset:3072 lds
v_mfma_f32_16x16x32_bf16 a[16:19],a[224:227],v[136:139],a[16:19]
v_cvt_pk_bf16_f32 v173,v186,v187
v_cvt_pk_bf16_f32 v174,v188,v189
v_mfma_f32_16x16x32_bf16 a[16:19],a[228:231],v[140:143],a[16:19]
v_exp_f32 v200,v200
v_mfma_f32_16x16x32_bf16 a[20:23],a[236:239],v[140:143],a[20:23]
v_exp_f32 v201,v201
v_mfma_f32_16x16x32_bf16 a[20:23],a[232:235],v[136:139],a[20:23]
v_cvt_pk_bf16_f32 v175,v190,v191
v_cvt_pk_bf16_f32 v200,v200,v201
v_mfma_f32_16x16x32_bf16 a[24:27],a[240:243],v[136:139],a[24:27]
s_waitcnt lgkmcnt(0)
v_exp_f32 v202,v202
v_mfma_f32_16x16x32_bf16 a[24:27],a[244:247],v[140:143],a[24:27]
v_exp_f32 v203,v203
v_mfma_f32_16x16x32_bf16 a[28:31],a[252:255],v[140:143],a[28:31]
v_exp_f32 v204,v204
v_mfma_f32_16x16x32_bf16 a[28:31],a[248:251],v[136:139],a[28:31]
buffer_store_dwordx4 v[80:83],v6,s[36:39],s85 offen sc1
v_smfmac_f32_16x16x64_bf16 v[48:51],v[28:31],v[136:143],v27
v_cvt_pk_bf16_f32 v201,v202,v203
v_mfma_f32_16x16x32_bf16 v[232:235],a[128:131],v[148:151],v[44:47]
s_waitcnt vmcnt(10)
v_exp_f32 v205,v205
v_mfma_f32_16x16x32_bf16 v[232:235],a[132:135],v[180:183],v[232:235]
s_barrier
ds_read_b128 a[128:131],v1
v_cvt_pk_bf16_f32 v202,v204,v205
v_mfma_f32_16x16x32_bf16 v[232:235],a[136:139],v[212:215],v[232:235]
v_exp_f32 v206,v206
v_mfma_f32_16x16x32_bf16 v[232:235],a[140:143],v[244:247],v[232:235]
v_exp_f32 v207,v207
v_mfma_f32_16x16x32_bf16 v[236:239],a[156:159],v[244:247],v[44:47]
ds_read_b128 a[132:135],v1 offset:256
v_cvt_pk_bf16_f32 v203,v206,v207
v_mfma_f32_16x16x32_bf16 v[236:239],a[152:155],v[212:215],v[236:239]
v_exp_f32 v216,v216
v_mfma_f32_16x16x32_bf16 v[236:239],a[148:151],v[180:183],v[236:239]
s_add_u32 m0,s82,86016
v_exp_f32 v217,v217
v_mfma_f32_16x16x32_bf16 v[236:239],a[144:147],v[148:151],v[236:239]
ds_read_b128 a[136:139],v1 offset:512
v_cvt_pk_bf16_f32 v204,v216,v217
v_mfma_f32_16x16x32_bf16 v[248:251],a[160:163],v[148:151],v[44:47]
v_exp_f32 v218,v218
v_mfma_f32_16x16x32_bf16 v[248:251],a[164:167],v[180:183],v[248:251]
buffer_load_dwordx4 v11,s[72:75],s80 offen lds
v_mfma_f32_16x16x32_bf16 v[248:251],a[168:171],v[212:215],v[248:251]
ds_read_b128 a[140:143],v1 offset:768
v_mfma_f32_16x16x32_bf16 v[248:251],a[172:175],v[244:247],v[248:251]
v_exp_f32 v219,v219
v_mfma_f32_16x16x32_bf16 v[252:255],a[188:191],v[244:247],v[44:47]
ds_read_b128 a[156:159],v1 offset:4864
v_cvt_pk_bf16_f32 v205,v218,v219
v_mfma_f32_16x16x32_bf16 v[252:255],a[184:187],v[212:215],v[252:255]
v_exp_f32 v220,v220
v_mfma_f32_16x16x32_bf16 v[252:255],a[180:183],v[180:183],v[252:255]
)A"
R"A(ds_read_b128 a[152:155],v1 offset:4608
v_mfma_f32_16x16x32_bf16 v[252:255],a[176:179],v[148:151],v[252:255]
v_exp_f32 v221,v221
v_mfma_f32_16x16x32_bf16 a[32:35],a[192:195],v[168:171],a[32:35]
ds_read_b128 a[148:151],v1 offset:4352
v_cvt_pk_bf16_f32 v206,v220,v221
v_mfma_f32_16x16x32_bf16 a[32:35],a[196:199],v[172:175],a[32:35]
v_exp_f32 v222,v222
v_mfma_f32_16x16x32_bf16 a[36:39],a[204:207],v[172:175],a[36:39]
ds_read_b128 a[144:147],v1 offset:4096
v_mfma_f32_16x16x32_bf16 a[36:39],a[200:203],v[168:171],a[36:39]
buffer_store_dwordx4 v[84:87],v6,s[36:39],s85 offen offset:1024 sc1
v_mfma_f32_16x16x32_bf16 a[40:43],a[208:211],v[168:171],a[40:43]
ds_read_b128 a[160:163],v1 offset:8192
v_mfma_f32_16x16x32_bf16 a[40:43],a[212:215],v[172:175],a[40:43]
v_exp_f32 v223,v223
v_mfma_f32_16x16x32_bf16 a[44:47],a[220:223],v[172:175],a[44:47]
ds_read_b128 a[164:167],v1 offset:8448
v_cvt_pk_bf16_f32 v207,v222,v223
v_mfma_f32_16x16x32_bf16 a[44:47],a[216:219],v[168:171],a[44:47]
v_exp_f32 v232,v232
v_mfma_f32_16x16x32_bf16 a[48:51],a[224:227],v[168:171],a[48:51]
ds_read_b128 a[168:171],v1 offset:8704
v_mfma_f32_16x16x32_bf16 a[48:51],a[228:231],v[172:175],a[48:51]
v_exp_f32 v233,v233
v_mfma_f32_16x16x32_bf16 a[52:55],a[236:239],v[172:175],a[52:55]
s_add_u32 m0,s82,87040
ds_read_b128 a[172:175],v1 offset:8960
v_cvt_pk_bf16_f32 v232,v232,v233
v_mfma_f32_16x16x32_bf16 a[52:55],a[232:235],v[168:171],a[52:55]
v_exp_f32 v234,v234
v_mfma_f32_16x16x32_bf16 a[56:59],a[240:243],v[168:171],a[56:59]
ds_read_b128 a[188:191],v1 offset:13056
v_mfma_f32_16x16x32_bf16 a[56:59],a[244:247],v[172:175],a[56:59]
buffer_load_dwordx4 v7,s[72:75],s83 offen lds
v_mfma_f32_16x16x32_bf16 a[60:63],a[252:255],v[172:175],a[60:63]
ds_read_b128 a[184:187],v1 offset:12800
v_mfma_f32_16x16x32_bf16 a[60:63],a[248:251],v[168:171],a[60:63]
v_exp_f32 v235,v235
v_smfmac_f32_16x16x64_bf16 v[52:55],v[28:31],v[168:175],v27
s_waitcnt lgkmcnt(3)
ds_read_b128 a[180:183],v1 offset:12544
v_mfma_f32_16x16x32_bf16 v[136:139],a[128:131],v[128:131],v[32:35]
v_exp_f32 v236,v236
v_mfma_f32_16x16x32_bf16 v[136:139],a[132:135],v[160:163],v[136:139]
ds_read_b128 a[176:179],v1 offset:12288
v_cvt_pk_bf16_f32 v233,v234,v235
v_mfma_f32_16x16x32_bf16 v[136:139],a[136:139],v[192:195],v[136:139]
v_exp_f32 v237,v237
v_mfma_f32_16x16x32_bf16 v[136:139],a[140:143],v[224:227],v[136:139]
v_exp_f32 v238,v238
v_mfma_f32_16x16x32_bf16 v[140:143],a[156:159],v[224:227],v[32:35]
v_exp_f32 v239,v239
v_mfma_f32_16x16x32_bf16 v[140:143],a[152:155],v[192:195],v[140:143]
v_cvt_pk_bf16_f32 v234,v236,v237
v_cvt_pk_bf16_f32 v235,v238,v239
v_mfma_f32_16x16x32_bf16 v[140:143],a[148:151],v[160:163],v[140:143]
v_exp_f32 v248,v248
v_mfma_f32_16x16x32_bf16 v[140:143],a[144:147],v[128:131],v[140:143]
v_exp_f32 v249,v249
v_mfma_f32_16x16x32_bf16 v[152:155],a[160:163],v[128:131],v[32:35]
buffer_store_dwordx4 v[88:91],v6,s[36:39],s85 offen offset:2048 sc1
v_mfma_f32_16x16x32_bf16 v[152:155],a[164:167],v[160:163],v[152:155]
v_exp_f32 v250,v250
v_mfma_f32_16x16x32_bf16 v[152:155],a[168:171],v[192:195],v[152:155]
s_waitcnt lgkmcnt(0)
v_exp_f32 v251,v251
v_mfma_f32_16x16x32_bf16 v[152:155],a[172:175],v[224:227],v[152:155]
v_exp_f32 v252,v252
v_mfma_f32_16x16x32_bf16 v[156:159],a[188:191],v[224:227],v[32:35]
v_exp_f32 v253,v253
v_mfma_f32_16x16x32_bf16 v[156:159],a[184:187],v[192:195],v[156:159]
v_exp_f32 v254,v254
v_mfma_f32_16x16x32_bf16 v[156:159],a[180:183],v[160:163],v[156:159]
v_exp_f32 v255,v255
v_mfma_f32_16x16x32_bf16 v[156:159],a[176:179],v[128:131],v[156:159]
v_cvt_pk_bf16_f32 v236,v248,v249
v_cvt_pk_bf16_f32 v237,v250,v251
v_mfma_f32_16x16x32_bf16 a[64:67],a[192:195],v[200:203],a[64:67]
buffer_load_dwordx4 v8,s[72:75],s83 offen offset:1024 lds
v_mfma_f32_16x16x32_bf16 a[64:67],a[196:199],v[204:207],a[64:67]
v_cvt_pk_bf16_f32 v238,v252,v253
v_cvt_pk_bf16_f32 v239,v254,v255
v_mfma_f32_16x16x32_bf16 a[68:71],a[204:207],v[204:207],a[68:71]
v_exp_f32 v136,v136
v_mfma_f32_16x16x32_bf16 a[68:71],a[200:203],v[200:203],a[68:71]
v_exp_f32 v137,v137
v_mfma_f32_16x16x32_bf16 a[72:75],a[208:211],v[200:203],a[72:75]
v_exp_f32 v138,v138
v_mfma_f32_16x16x32_bf16 a[72:75],a[212:215],v[204:207],a[72:75]
v_exp_f32 v139,v139
v_mfma_f32_16x16x32_bf16 a[76:79],a[220:223],v[204:207],a[76:79]
v_cvt_pk_bf16_f32 v136,v136,v137
v_cvt_pk_bf16_f32 v137,v138,v139
v_mfma_f32_16x16x32_bf16 a[76:79],a[216:219],v[200:203],a[76:79]
v_exp_f32 v140,v140
v_mfma_f32_16x16x32_bf16 a[80:83],a[224:227],v[200:203],a[80:83]
v_exp_f32 v141,v141
v_mfma_f32_16x16x32_bf16 a[80:83],a[228:231],v[204:207],a[80:83]
v_exp_f32 v142,v142
v_mfma_f32_16x16x32_bf16 a[84:87],a[236:239],v[204:207],a[84:87]
v_exp_f32 v143,v143
v_mfma_f32_16x16x32_bf16 a[84:87],a[232:235],v[200:203],a[84:87]
v_cvt_pk_bf16_f32 v138,v140,v141
v_cvt_pk_bf16_f32 v139,v142,v143
v_mfma_f32_16x16x32_bf16 a[88:91],a[240:243],v[200:203],a[88:91]
v_exp_f32 v152,v152
v_mfma_f32_16x16x32_bf16 a[88:91],a[244:247],v[204:207],a[88:91]
v_exp_f32 v153,v153
v_mfma_f32_16x16x32_bf16 a[92:95],a[252:255],v[204:207],a[92:95]
buffer_store_dwordx4 v[92:95],v6,s[36:39],s85 offen offset:3072 sc1
v_mfma_f32_16x16x32_bf16 a[92:95],a[248:251],v[200:203],a[92:95]
s_add_u32 s85,s55,69632
v_exp_f32 v154,v154
v_smfmac_f32_16x16x64_bf16 v[56:59],v[28:31],v[200:207],v27
s_max_i32 s85,s85,s19
v_cvt_pk_bf16_f32 v140,v152,v153
v_mfma_f32_16x16x32_bf16 v[168:171],a[128:131],v[132:135],v[36:39]
v_exp_f32 v155,v155
v_mfma_f32_16x16x32_bf16 v[168:171],a[132:135],v[164:167],v[168:171]
v_exp_f32 v156,v156
v_mfma_f32_16x16x32_bf16 v[168:171],a[136:139],v[196:199],v[168:171]
v_exp_f32 v157,v157
v_mfma_f32_16x16x32_bf16 v[168:171],a[140:143],v[228:231],v[168:171]
buffer_load_dwordx4 v9,s[72:75],s83 offen offset:2048 lds
v_mfma_f32_16x16x32_bf16 v[172:175],a[156:159],v[228:231],v[36:39]
v_exp_f32 v158,v158
v_mfma_f32_16x16x32_bf16 v[172:175],a[152:155],v[196:199],v[172:175]
v_exp_f32 v159,v159
v_mfma_f32_16x16x32_bf16 v[172:175],a[148:151],v[164:167],v[172:175]
v_cvt_pk_bf16_f32 v141,v154,v155
v_cvt_pk_bf16_f32 v142,v156,v157
v_mfma_f32_16x16x32_bf16 v[172:175],a[144:147],v[132:135],v[172:175]
v_cvt_pk_bf16_f32 v143,v158,v159
v_add_u32 v1,s48,v1
v_mfma_f32_16x16x32_bf16 v[184:187],a[160:163],v[132:135],v[36:39]
v_mfma_f32_16x16x32_bf16 v[184:187],a[164:167],v[164:167],v[184:187]
v_exp_f32 v168,v168
v_mfma_f32_16x16x32_bf16 v[184:187],a[168:171],v[196:199],v[184:187]
v_exp_f32 v169,v169
v_mfma_f32_16x16x32_bf16 v[184:187],a[172:175],v[228:231],v[184:187]
v_exp_f32 v170,v170
v_mfma_f32_16x16x32_bf16 v[188:191],a[188:191],v[228:231],v[36:39]
v_exp_f32 v171,v171
v_mfma_f32_16x16x32_bf16 v[188:191],a[184:187],v[196:199],v[188:191]
v_exp_f32 v172,v172
v_mfma_f32_16x16x32_bf16 v[188:191],a[180:183],v[164:167],v[188:191]
v_exp_f32 v173,v173
v_mfma_f32_16x16x32_bf16 v[188:191],a[176:179],v[132:135],v[188:191]
v_exp_f32 v174,v174
v_mfma_f32_16x16x32_bf16 a[96:99],a[192:195],v[232:235],a[96:99]
ds_read_b64_tr_b16 a[192:193],v2
ds_read_b64_tr_b16 a[194:195],v2 offset:4096
v_mfma_f32_16x16x32_bf16 a[96:99],a[196:199],v[236:239],a[96:99]
ds_read_b64_tr_b16 a[196:197],v2 offset:8192
v_cvt_pk_bf16_f32 v168,v168,v169
v_mfma_f32_16x16x32_bf16 a[100:103],a[204:207],v[236:239],a[100:103]
ds_read_b64_tr_b16 a[198:199],v2 offset:12288
v_cvt_pk_bf16_f32 v169,v170,v171
v_mfma_f32_16x16x32_bf16 a[100:103],a[200:203],v[232:235],a[100:103]
buffer_store_dwordx4 v[96:99],v6,s[36:39],s86 offen sc1
v_mfma_f32_16x16x32_bf16 a[104:107],a[208:211],v[232:235],a[104:107]
ds_read_b64_tr_b16 a[204:205],v3 offset:8192
ds_read_b64_tr_b16 a[206:207],v3 offset:12288
v_mfma_f32_16x16x32_bf16 a[104:107],a[212:215],v[236:239],a[104:107]
ds_read_b64_tr_b16 a[200:201],v3
v_cvt_pk_bf16_f32 v170,v172,v173
v_mfma_f32_16x16x32_bf16 a[108:111],a[220:223],v[236:239],a[108:111]
v_exp_f32 v175,v175
v_mfma_f32_16x16x32_bf16 a[108:111],a[216:219],v[232:235],a[108:111]
buffer_load_dwordx4 v10,s[72:75],s83 offen offset:3072 lds
v_mfma_f32_16x16x32_bf16 a[112:115],a[224:227],v[232:235],a[112:115]
ds_read_b64_tr_b16 a[202:203],v3 offset:4096
ds_read_b64_tr_b16 a[208:209],v2 offset:256
v_mfma_f32_16x16x32_bf16 a[112:115],a[228:231],v[236:239],a[112:115]
ds_read_b64_tr_b16 a[210:211],v2 offset:4352
v_cvt_pk_bf16_f32 v171,v174,v175
v_mfma_f32_16x16x32_bf16 a[116:119],a[236:239],v[236:239],a[116:119]
v_exp_f32 v184,v184
v_mfma_f32_16x16x32_bf16 a[116:119],a[232:235],v[232:235],a[116:119]
ds_read_b64_tr_b16 a[212:213],v2 offset:8448
ds_read_b64_tr_b16 a[214:215],v2 offset:12544
v_mfma_f32_16x16x32_bf16 a[120:123],a[240:243],v[232:235],a[120:123]
v_exp_f32 v185,v185
v_mfma_f32_16x16x32_bf16 a[120:123],a[244:247],v[236:239],a[120:123]
ds_read_b64_tr_b16 a[220:221],v3 offset:8448
ds_read_b64_tr_b16 a[222:223],v3 offset:12544
v_mfma_f32_16x16x32_bf16 a[124:127],a[252:255],v[236:239],a[124:127]
ds_read_b64_tr_b16 a[216:217],v3 offset:256
v_cvt_pk_bf16_f32 v172,v184,v185
v_mfma_f32_16x16x32_bf16 a[124:127],a[248:251],v[232:235],a[124:127]
ds_read_b64_tr_b16 a[218:219],v3 offset:4352
v_smfmac_f32_16x16x64_bf16 v[60:63],v[28:31],v[232:239],v27
ds_read_b64_tr_b16 a[224:225],v2 offset:512
v_mfma_f32_16x16x32_bf16 v[200:203],a[128:131],v[144:147],v[40:43]
ds_read_b64_tr_b16 a[226:227],v2 offset:4608
ds_read_b64_tr_b16 a[228:229],v2 offset:8704
v_mfma_f32_16x16x32_bf16 v[200:203],a[132:135],v[176:179],v[200:203]
ds_read_b64_tr_b16 a[230:231],v2 offset:12800
v_mfma_f32_16x16x32_bf16 v[200:203],a[136:139],v[208:211],v[200:203]
ds_read_b64_tr_b16 a[236:237],v3 offset:8704
v_mfma_f32_16x16x32_bf16 v[200:203],a[140:143],v[240:243],v[200:203]
ds_read_b64_tr_b16 a[238:239],v3 offset:12800
v_mfma_f32_16x16x32_bf16 v[204:207],a[156:159],v[240:243],v[40:43]
ds_read_b64_tr_b16 a[232:233],v3 offset:512
ds_read_b64_tr_b16 a[234:235],v3 offset:4608
v_mfma_f32_16x16x32_bf16 v[204:207],a[152:155],v[208:211],v[204:207]
ds_read_b64_tr_b16 a[240:241],v2 offset:768
v_mfma_f32_16x16x32_bf16 v[204:207],a[148:151],v[176:179],v[204:207]
ds_read_b64_tr_b16 a[242:243],v2 offset:4864
v_mfma_f32_16x16x32_bf16 v[204:207],a[144:147],v[144:147],v[204:207]
s_add_u32 m0,s82,91136
ds_read_b64_tr_b16 a[244:245],v2 offset:8960
v_mfma_f32_16x16x32_bf16 v[216:219],a[160:163],v[144:147],v[40:43]
buffer_store_dwordx4 v[100:103],v6,s[36:39],s86 offen offset:1024 sc1
v_mfma_f32_16x16x32_bf16 v[216:219],a[164:167],v[176:179],v[216:219]
ds_read_b64_tr_b16 a[246:247],v2 offset:13056
ds_read_b64_tr_b16 a[252:253],v3 offset:8960
v_mfma_f32_16x16x32_bf16 v[216:219],a[168:171],v[208:211],v[216:219]
ds_read_b64_tr_b16 a[254:255],v3 offset:13056
v_add_u32 v2,s48,v2
v_mfma_f32_16x16x32_bf16 v[216:219],a[172:175],v[240:243],v[216:219]
ds_read_b64_tr_b16 a[248:249],v3 offset:768
v_mfma_f32_16x16x32_bf16 v[220:223],a[188:191],v[240:243],v[40:43]
buffer_load_dwordx4 v11,s[72:75],s83 offen lds
v_mfma_f32_16x16x32_bf16 v[220:223],a[184:187],v[208:211],v[220:223]
ds_read_b64_tr_b16 a[250:251],v3 offset:4864
v_add_u32 v3,s48,v3
v_mfma_f32_16x16x32_bf16 v[220:223],a[180:183],v[176:179],v[220:223]
v_exp_f32 v186,v186
v_mfma_f32_16x16x32_bf16 v[220:223],a[176:179],v[144:147],v[220:223]
s_waitcnt lgkmcnt(5)
v_exp_f32 v187,v187
v_mfma_f32_16x16x32_bf16 a[0:3],a[192:195],v[136:139],a[0:3]
v_exp_f32 v188,v188
v_mfma_f32_16x16x32_bf16 a[0:3],a[196:199],v[140:143],a[0:3]
s_add_u32 s80,s71,0
s_and_b32 s80,s80,3
s_lshl_b32 s80,s80,1
v_mfma_f32_16x16x32_bf16 a[4:7],a[204:207],v[140:143],a[4:7]
s_or_b32 s80,s80,1
s_mul_i32 s80,s80,163840
s_add_u32 s80,s80,s81
v_mfma_f32_16x16x32_bf16 a[4:7],a[200:203],v[136:139],a[4:7]
s_add_u32 s83,s80,20480
v_exp_f32 v189,v189
v_mfma_f32_16x16x32_bf16 a[8:11],a[208:211],v[136:139],a[8:11]
v_exp_f32 v190,v190
v_mfma_f32_16x16x32_bf16 a[8:11],a[212:215],v[140:143],a[8:11]
v_exp_f32 v191,v191
v_mfma_f32_16x16x32_bf16 a[12:15],a[220:223],v[140:143],a[12:15]
v_cvt_pk_bf16_f32 v173,v186,v187
v_cvt_pk_bf16_f32 v174,v188,v189
v_mfma_f32_16x16x32_bf16 a[12:15],a[216:219],v[136:139],a[12:15]
v_exp_f32 v200,v200
v_mfma_f32_16x16x32_bf16 a[16:19],a[224:227],v[136:139],a[16:19]
v_exp_f32 v201,v201
v_mfma_f32_16x16x32_bf16 a[16:19],a[228:231],v[140:143],a[16:19]
)A"
R"A(v_cvt_pk_bf16_f32 v175,v190,v191
v_cvt_pk_bf16_f32 v200,v200,v201
v_mfma_f32_16x16x32_bf16 a[20:23],a[236:239],v[140:143],a[20:23]
v_exp_f32 v202,v202
v_mfma_f32_16x16x32_bf16 a[20:23],a[232:235],v[136:139],a[20:23]
v_exp_f32 v203,v203
v_mfma_f32_16x16x32_bf16 a[24:27],a[240:243],v[136:139],a[24:27]
s_waitcnt lgkmcnt(0)
v_exp_f32 v204,v204
v_mfma_f32_16x16x32_bf16 a[24:27],a[244:247],v[140:143],a[24:27]
v_exp_f32 v205,v205
v_mfma_f32_16x16x32_bf16 a[28:31],a[252:255],v[140:143],a[28:31]
buffer_store_dwordx4 v[104:107],v6,s[36:39],s86 offen offset:2048 sc1
v_mfma_f32_16x16x32_bf16 a[28:31],a[248:251],v[136:139],a[28:31]
v_exp_f32 v206,v206
v_smfmac_f32_16x16x64_bf16 v[48:51],v[28:31],v[136:143],v27
v_cvt_pk_bf16_f32 v201,v202,v203
v_mfma_f32_16x16x32_bf16 v[232:235],a[128:131],v[148:151],v[44:47]
v_exp_f32 v207,v207
v_mfma_f32_16x16x32_bf16 v[232:235],a[132:135],v[180:183],v[232:235]
s_barrier
ds_read_b128 a[128:131],v1
v_cvt_pk_bf16_f32 v202,v204,v205
v_mfma_f32_16x16x32_bf16 v[232:235],a[136:139],v[212:215],v[232:235]
v_exp_f32 v216,v216
v_mfma_f32_16x16x32_bf16 v[232:235],a[140:143],v[244:247],v[232:235]
ds_read_b128 a[132:135],v1 offset:256
v_cvt_pk_bf16_f32 v203,v206,v207
v_mfma_f32_16x16x32_bf16 v[236:239],a[156:159],v[244:247],v[44:47]
s_add_u32 m0,s82,0
v_exp_f32 v217,v217
v_mfma_f32_16x16x32_bf16 v[236:239],a[152:155],v[212:215],v[236:239]
ds_read_b128 a[136:139],v1 offset:512
v_cvt_pk_bf16_f32 v204,v216,v217
v_mfma_f32_16x16x32_bf16 v[236:239],a[148:151],v[180:183],v[236:239]
v_exp_f32 v218,v218
v_mfma_f32_16x16x32_bf16 v[236:239],a[144:147],v[148:151],v[236:239]
buffer_load_dwordx4 v7,s[72:75],s80 offen lds
v_mfma_f32_16x16x32_bf16 v[248:251],a[160:163],v[148:151],v[44:47]
v_exp_f32 v219,v219
v_mfma_f32_16x16x32_bf16 v[248:251],a[164:167],v[180:183],v[248:251]
ds_read_b128 a[140:143],v1 offset:768
v_cvt_pk_bf16_f32 v205,v218,v219
v_mfma_f32_16x16x32_bf16 v[248:251],a[168:171],v[212:215],v[248:251]
v_exp_f32 v220,v220
v_mfma_f32_16x16x32_bf16 v[248:251],a[172:175],v[244:247],v[248:251]
ds_read_b128 a[156:159],v1 offset:4864
v_mfma_f32_16x16x32_bf16 v[252:255],a[188:191],v[244:247],v[44:47]
v_exp_f32 v221,v221
v_mfma_f32_16x16x32_bf16 v[252:255],a[184:187],v[212:215],v[252:255]
ds_read_b128 a[152:155],v1 offset:4608
v_cvt_pk_bf16_f32 v206,v220,v221
v_mfma_f32_16x16x32_bf16 v[252:255],a[180:183],v[180:183],v[252:255]
v_exp_f32 v222,v222
v_mfma_f32_16x16x32_bf16 v[252:255],a[176:179],v[148:151],v[252:255]
ds_read_b128 a[148:151],v1 offset:4352
v_mfma_f32_16x16x32_bf16 a[32:35],a[192:195],v[168:171],a[32:35]
v_exp_f32 v223,v223
v_mfma_f32_16x16x32_bf16 a[32:35],a[196:199],v[172:175],a[32:35]
ds_read_b128 a[144:147],v1 offset:4096
v_cvt_pk_bf16_f32 v207,v222,v223
v_mfma_f32_16x16x32_bf16 a[36:39],a[204:207],v[172:175],a[36:39]
buffer_store_dwordx4 v[108:111],v6,s[36:39],s86 offen offset:3072 sc1
v_mfma_f32_16x16x32_bf16 a[36:39],a[200:203],v[168:171],a[36:39]
s_add_u32 s86,s55,73728
s_max_i32 s86,s86,s100
ds_read_b128 a[160:163],v1 offset:8192
v_mfma_f32_16x16x32_bf16 a[40:43],a[208:211],v[168:171],a[40:43]
v_exp_f32 v232,v232
v_mfma_f32_16x16x32_bf16 a[40:43],a[212:215],v[172:175],a[40:43]
ds_read_b128 a[164:167],v1 offset:8448
v_mfma_f32_16x16x32_bf16 a[44:47],a[220:223],v[172:175],a[44:47]
buffer_load_dwordx4 v8,s[72:75],s80 offen offset:1024 lds
v_mfma_f32_16x16x32_bf16 a[44:47],a[216:219],v[168:171],a[44:47]
ds_read_b128 a[168:171],v1 offset:8704
v_mfma_f32_16x16x32_bf16 a[48:51],a[224:227],v[168:171],a[48:51]
v_exp_f32 v233,v233
v_mfma_f32_16x16x32_bf16 a[48:51],a[228:231],v[172:175],a[48:51]
ds_read_b128 a[172:175],v1 offset:8960
v_cvt_pk_bf16_f32 v232,v232,v233
v_mfma_f32_16x16x32_bf16 a[52:55],a[236:239],v[172:175],a[52:55]
v_exp_f32 v234,v234
v_mfma_f32_16x16x32_bf16 a[52:55],a[232:235],v[168:171],a[52:55]
ds_read_b128 a[188:191],v1 offset:13056
v_mfma_f32_16x16x32_bf16 a[56:59],a[240:243],v[168:171],a[56:59]
v_exp_f32 v235,v235
v_mfma_f32_16x16x32_bf16 a[56:59],a[244:247],v[172:175],a[56:59]
buffer_load_dwordx4 v9,s[72:75],s80 offen offset:2048 lds
v_mfma_f32_16x16x32_bf16 a[60:63],a[252:255],v[172:175],a[60:63]
ds_read_b128 a[184:187],v1 offset:12800
v_cvt_pk_bf16_f32 v233,v234,v235
v_mfma_f32_16x16x32_bf16 a[60:63],a[248:251],v[168:171],a[60:63]
v_exp_f32 v236,v236
v_smfmac_f32_16x16x64_bf16 v[52:55],v[28:31],v[168:175],v27
s_waitcnt lgkmcnt(3)
ds_read_b128 a[180:183],v1 offset:12544
v_mfma_f32_16x16x32_bf16 v[136:139],a[128:131],v[128:131],v[32:35]
v_exp_f32 v237,v237
v_mfma_f32_16x16x32_bf16 v[136:139],a[132:135],v[160:163],v[136:139]
ds_read_b128 a[176:179],v1 offset:12288
v_cvt_pk_bf16_f32 v234,v236,v237
v_mfma_f32_16x16x32_bf16 v[136:139],a[136:139],v[192:195],v[136:139]
v_exp_f32 v238,v238
v_mfma_f32_16x16x32_bf16 v[136:139],a[140:143],v[224:227],v[136:139]
v_exp_f32 v239,v239
v_mfma_f32_16x16x32_bf16 v[140:143],a[156:159],v[224:227],v[32:35]
v_cvt_pk_bf16_f32 v235,v238,v239
v_add_u32 v1,v4,v5
v_mfma_f32_16x16x32_bf16 v[140:143],a[152:155],v[192:195],v[140:143]
v_exp_f32 v248,v248
v_mfma_f32_16x16x32_bf16 v[140:143],a[148:151],v[160:163],v[140:143]
v_exp_f32 v249,v249
v_mfma_f32_16x16x32_bf16 v[140:143],a[144:147],v[128:131],v[140:143]
buffer_store_dwordx4 v[112:115],v6,s[36:39],s87 offen sc1
v_mfma_f32_16x16x32_bf16 v[152:155],a[160:163],v[128:131],v[32:35]
v_exp_f32 v250,v250
v_mfma_f32_16x16x32_bf16 v[152:155],a[164:167],v[160:163],v[152:155]
v_exp_f32 v251,v251
v_mfma_f32_16x16x32_bf16 v[152:155],a[168:171],v[192:195],v[152:155]
s_waitcnt lgkmcnt(0)
v_exp_f32 v252,v252
v_mfma_f32_16x16x32_bf16 v[152:155],a[172:175],v[224:227],v[152:155]
buffer_load_dwordx4 v10,s[72:75],s80 offen offset:3072 lds
v_mfma_f32_16x16x32_bf16 v[156:159],a[188:191],v[224:227],v[32:35]
v_exp_f32 v253,v253
v_mfma_f32_16x16x32_bf16 v[156:159],a[184:187],v[192:195],v[156:159]
v_exp_f32 v254,v254
v_mfma_f32_16x16x32_bf16 v[156:159],a[180:183],v[160:163],v[156:159]
v_exp_f32 v255,v255
v_mfma_f32_16x16x32_bf16 v[156:159],a[176:179],v[128:131],v[156:159]
s_add_u32 m0,s82,4096
v_cvt_pk_bf16_f32 v236,v248,v249
v_cvt_pk_bf16_f32 v237,v250,v251
v_mfma_f32_16x16x32_bf16 a[64:67],a[192:195],v[200:203],a[64:67]
v_cvt_pk_bf16_f32 v238,v252,v253
v_cvt_pk_bf16_f32 v239,v254,v255
v_mfma_f32_16x16x32_bf16 a[64:67],a[196:199],v[204:207],a[64:67]
v_exp_f32 v136,v136
v_mfma_f32_16x16x32_bf16 a[68:71],a[204:207],v[204:207],a[68:71]
buffer_load_dwordx4 v11,s[72:75],s80 offen lds
v_mfma_f32_16x16x32_bf16 a[68:71],a[200:203],v[200:203],a[68:71]
v_exp_f32 v137,v137
v_mfma_f32_16x16x32_bf16 a[72:75],a[208:211],v[200:203],a[72:75]
v_exp_f32 v138,v138
v_mfma_f32_16x16x32_bf16 a[72:75],a[212:215],v[204:207],a[72:75]
v_exp_f32 v139,v139
v_mfma_f32_16x16x32_bf16 a[76:79],a[220:223],v[204:207],a[76:79]
v_cvt_pk_bf16_f32 v136,v136,v137
v_cvt_pk_bf16_f32 v137,v138,v139
v_mfma_f32_16x16x32_bf16 a[76:79],a[216:219],v[200:203],a[76:79]
v_exp_f32 v140,v140
v_mfma_f32_16x16x32_bf16 a[80:83],a[224:227],v[200:203],a[80:83]
v_exp_f32 v141,v141
v_mfma_f32_16x16x32_bf16 a[80:83],a[228:231],v[204:207],a[80:83]
v_exp_f32 v142,v142
v_mfma_f32_16x16x32_bf16 a[84:87],a[236:239],v[204:207],a[84:87]
s_add_u32 m0,s82,5120
v_exp_f32 v143,v143
v_mfma_f32_16x16x32_bf16 a[84:87],a[232:235],v[200:203],a[84:87]
v_cvt_pk_bf16_f32 v138,v140,v141
v_cvt_pk_bf16_f32 v139,v142,v143
v_mfma_f32_16x16x32_bf16 a[88:91],a[240:243],v[200:203],a[88:91]
v_exp_f32 v152,v152
v_mfma_f32_16x16x32_bf16 a[88:91],a[244:247],v[204:207],a[88:91]
buffer_store_dwordx4 v[116:119],v6,s[36:39],s87 offen offset:1024 sc1
v_mfma_f32_16x16x32_bf16 a[92:95],a[252:255],v[204:207],a[92:95]
v_exp_f32 v153,v153
v_mfma_f32_16x16x32_bf16 a[92:95],a[248:251],v[200:203],a[92:95]
v_exp_f32 v154,v154
v_smfmac_f32_16x16x64_bf16 v[56:59],v[28:31],v[200:207],v27
v_cvt_pk_bf16_f32 v140,v152,v153
v_mfma_f32_16x16x32_bf16 v[168:171],a[128:131],v[132:135],v[36:39]
buffer_load_dwordx4 v7,s[72:75],s83 offen lds
v_mfma_f32_16x16x32_bf16 v[168:171],a[132:135],v[164:167],v[168:171]
v_exp_f32 v155,v155
v_mfma_f32_16x16x32_bf16 v[168:171],a[136:139],v[196:199],v[168:171]
v_exp_f32 v156,v156
v_mfma_f32_16x16x32_bf16 v[168:171],a[140:143],v[228:231],v[168:171]
v_exp_f32 v157,v157
v_mfma_f32_16x16x32_bf16 v[172:175],a[156:159],v[228:231],v[36:39]
v_exp_f32 v158,v158
v_mfma_f32_16x16x32_bf16 v[172:175],a[152:155],v[196:199],v[172:175]
v_exp_f32 v159,v159
v_mfma_f32_16x16x32_bf16 v[172:175],a[148:151],v[164:167],v[172:175]
v_cvt_pk_bf16_f32 v141,v154,v155
v_cvt_pk_bf16_f32 v142,v156,v157
v_mfma_f32_16x16x32_bf16 v[172:175],a[144:147],v[132:135],v[172:175]
buffer_load_dwordx4 v8,s[72:75],s83 offen offset:1024 lds
v_mfma_f32_16x16x32_bf16 v[184:187],a[160:163],v[132:135],v[36:39]
v_cvt_pk_bf16_f32 v143,v158,v159
v_add_u32 v1,81920,v1
v_mfma_f32_16x16x32_bf16 v[184:187],a[164:167],v[164:167],v[184:187]
v_exp_f32 v168,v168
v_mfma_f32_16x16x32_bf16 v[184:187],a[168:171],v[196:199],v[184:187]
v_exp_f32 v169,v169
v_mfma_f32_16x16x32_bf16 v[184:187],a[172:175],v[228:231],v[184:187]
v_exp_f32 v170,v170
v_mfma_f32_16x16x32_bf16 v[188:191],a[188:191],v[228:231],v[36:39]
v_exp_f32 v171,v171
v_mfma_f32_16x16x32_bf16 v[188:191],a[184:187],v[196:199],v[188:191]
v_exp_f32 v172,v172
v_mfma_f32_16x16x32_bf16 v[188:191],a[180:183],v[164:167],v[188:191]
v_exp_f32 v173,v173
v_mfma_f32_16x16x32_bf16 v[188:191],a[176:179],v[132:135],v[188:191]
v_exp_f32 v174,v174
v_mfma_f32_16x16x32_bf16 a[96:99],a[192:195],v[232:235],a[96:99]
ds_read_b64_tr_b16 a[192:193],v2
ds_read_b64_tr_b16 a[194:195],v2 offset:4096
v_mfma_f32_16x16x32_bf16 a[96:99],a[196:199],v[236:239],a[96:99]
ds_read_b64_tr_b16 a[196:197],v2 offset:8192
v_cvt_pk_bf16_f32 v168,v168,v169
v_mfma_f32_16x16x32_bf16 a[100:103],a[204:207],v[236:239],a[100:103]
buffer_store_dwordx4 v[120:123],v6,s[36:39],s87 offen offset:2048 sc1
v_mfma_f32_16x16x32_bf16 a[100:103],a[200:203],v[232:235],a[100:103]
ds_read_b64_tr_b16 a[198:199],v2 offset:12288
ds_read_b64_tr_b16 a[204:205],v3 offset:8192
v_mfma_f32_16x16x32_bf16 a[104:107],a[208:211],v[232:235],a[104:107]
ds_read_b64_tr_b16 a[206:207],v3 offset:12288
v_cvt_pk_bf16_f32 v169,v170,v171
v_mfma_f32_16x16x32_bf16 a[104:107],a[212:215],v[236:239],a[104:107]
ds_read_b64_tr_b16 a[200:201],v3
v_cvt_pk_bf16_f32 v170,v172,v173
v_mfma_f32_16x16x32_bf16 a[108:111],a[220:223],v[236:239],a[108:111]
buffer_load_dwordx4 v9,s[72:75],s83 offen offset:2048 lds
v_mfma_f32_16x16x32_bf16 a[108:111],a[216:219],v[232:235],a[108:111]
ds_read_b64_tr_b16 a[202:203],v3 offset:4096
ds_read_b64_tr_b16 a[208:209],v2 offset:256
v_mfma_f32_16x16x32_bf16 a[112:115],a[224:227],v[232:235],a[112:115]
v_exp_f32 v175,v175
v_mfma_f32_16x16x32_bf16 a[112:115],a[228:231],v[236:239],a[112:115]
ds_read_b64_tr_b16 a[210:211],v2 offset:4352
ds_read_b64_tr_b16 a[212:213],v2 offset:8448
v_mfma_f32_16x16x32_bf16 a[116:119],a[236:239],v[236:239],a[116:119]
ds_read_b64_tr_b16 a[214:215],v2 offset:12544
v_cvt_pk_bf16_f32 v171,v174,v175
v_mfma_f32_16x16x32_bf16 a[116:119],a[232:235],v[232:235],a[116:119]
s_cmp_ge_u32 0,s71
s_cselect_b32 s80,2,0
ds_read_b64_tr_b16 a[220:221],v3 offset:8448
v_mfma_f32_16x16x32_bf16 a[120:123],a[240:243],v[232:235],a[120:123]
s_mul_i32 s80,s80,163840
v_exp_f32 v184,v184
v_mfma_f32_16x16x32_bf16 a[120:123],a[244:247],v[236:239],a[120:123]
s_add_u32 s80,s80,s81
buffer_load_dwordx4 v10,s[72:75],s83 offen offset:3072 lds
v_mfma_f32_16x16x32_bf16 a[124:127],a[252:255],v[236:239],a[124:127]
ds_read_b64_tr_b16 a[222:223],v3 offset:12544
ds_read_b64_tr_b16 a[216:217],v3 offset:256
v_mfma_f32_16x16x32_bf16 a[124:127],a[248:251],v[232:235],a[124:127]
ds_read_b64_tr_b16 a[218:219],v3 offset:4352
v_smfmac_f32_16x16x64_bf16 v[60:63],v[28:31],v[232:239],v27
ds_read_b64_tr_b16 a[224:225],v2 offset:512
v_mfma_f32_16x16x32_bf16 v[200:203],a[128:131],v[144:147],v[40:43]
ds_read_b64_tr_b16 a[226:227],v2 offset:4608
v_mfma_f32_16x16x32_bf16 v[200:203],a[132:135],v[176:179],v[200:203]
ds_read_b64_tr_b16 a[228:229],v2 offset:8704
ds_read_b64_tr_b16 a[230:231],v2 offset:12800
v_mfma_f32_16x16x32_bf16 v[200:203],a[136:139],v[208:211],v[200:203]
ds_read_b64_tr_b16 a[236:237],v3 offset:8704
)A"
R"A(v_mfma_f32_16x16x32_bf16 v[200:203],a[140:143],v[240:243],v[200:203]
ds_read_b64_tr_b16 a[238:239],v3 offset:12800
v_mfma_f32_16x16x32_bf16 v[204:207],a[156:159],v[240:243],v[40:43]
s_add_u32 m0,s82,9216
ds_read_b64_tr_b16 a[232:233],v3 offset:512
v_mfma_f32_16x16x32_bf16 v[204:207],a[152:155],v[208:211],v[204:207]
ds_read_b64_tr_b16 a[234:235],v3 offset:4608
ds_read_b64_tr_b16 a[240:241],v2 offset:768
v_mfma_f32_16x16x32_bf16 v[204:207],a[148:151],v[176:179],v[204:207]
ds_read_b64_tr_b16 a[242:243],v2 offset:4864
v_mfma_f32_16x16x32_bf16 v[204:207],a[144:147],v[144:147],v[204:207]
buffer_store_dwordx4 v[124:127],v6,s[36:39],s87 offen offset:3072 sc1
v_mfma_f32_16x16x32_bf16 v[216:219],a[160:163],v[144:147],v[40:43]
s_add_u32 s87,s55,77824
ds_read_b64_tr_b16 a[244:245],v2 offset:8960
ds_read_b64_tr_b16 a[246:247],v2 offset:13056
v_mfma_f32_16x16x32_bf16 v[216:219],a[164:167],v[176:179],v[216:219]
s_max_i32 s87,s87,s101
ds_read_b64_tr_b16 a[252:253],v3 offset:8960
v_sub_u32 v2,v4,v5
v_mfma_f32_16x16x32_bf16 v[216:219],a[168:171],v[208:211],v[216:219]
ds_read_b64_tr_b16 a[254:255],v3 offset:13056
v_add_u32 v2,81920,v2
v_mfma_f32_16x16x32_bf16 v[216:219],a[172:175],v[240:243],v[216:219]
buffer_load_dwordx4 v11,s[72:75],s83 offen lds
v_mfma_f32_16x16x32_bf16 v[220:223],a[188:191],v[240:243],v[40:43]
s_add_u32 s83,s80,20480
ds_read_b64_tr_b16 a[248:249],v3 offset:768
ds_read_b64_tr_b16 a[250:251],v3 offset:4864
v_mfma_f32_16x16x32_bf16 v[220:223],a[184:187],v[208:211],v[220:223]
v_exp_f32 v185,v185
v_mfma_f32_16x16x32_bf16 v[220:223],a[180:183],v[176:179],v[220:223]
v_exp_f32 v186,v186
v_mfma_f32_16x16x32_bf16 v[220:223],a[176:179],v[144:147],v[220:223]
s_add_u32 m0,s82,40960
v_exp_f32 v187,v187
s_waitcnt lgkmcnt(6)
v_mfma_f32_16x16x32_bf16 a[0:3],a[192:195],v[136:139],a[0:3]
v_exp_f32 v188,v188
v_mfma_f32_16x16x32_bf16 a[0:3],a[196:199],v[140:143],a[0:3]
v_exp_f32 v189,v189
v_mfma_f32_16x16x32_bf16 a[4:7],a[204:207],v[140:143],a[4:7]
buffer_load_dwordx4 v7,s[72:75],s80 offen lds
v_mfma_f32_16x16x32_bf16 a[4:7],a[200:203],v[136:139],a[4:7]
v_exp_f32 v190,v190
v_mfma_f32_16x16x32_bf16 a[8:11],a[208:211],v[136:139],a[8:11]
v_exp_f32 v191,v191
v_mfma_f32_16x16x32_bf16 a[8:11],a[212:215],v[140:143],a[8:11]
v_cvt_pk_bf16_f32 v172,v184,v185
v_cvt_pk_bf16_f32 v173,v186,v187
v_mfma_f32_16x16x32_bf16 a[12:15],a[220:223],v[140:143],a[12:15]
v_cvt_pk_bf16_f32 v174,v188,v189
v_cvt_pk_bf16_f32 v175,v190,v191
v_mfma_f32_16x16x32_bf16 a[12:15],a[216:219],v[136:139],a[12:15]
v_exp_f32 v200,v200
v_mfma_f32_16x16x32_bf16 a[16:19],a[224:227],v[136:139],a[16:19]
v_exp_f32 v201,v201
v_mfma_f32_16x16x32_bf16 a[16:19],a[228:231],v[140:143],a[16:19]
v_exp_f32 v202,v202
v_mfma_f32_16x16x32_bf16 a[20:23],a[236:239],v[140:143],a[20:23]
v_exp_f32 v203,v203
v_mfma_f32_16x16x32_bf16 a[20:23],a[232:235],v[136:139],a[20:23]
v_exp_f32 v204,v204
v_mfma_f32_16x16x32_bf16 a[24:27],a[240:243],v[136:139],a[24:27]
s_waitcnt lgkmcnt(0)
v_exp_f32 v205,v205
v_mfma_f32_16x16x32_bf16 a[24:27],a[244:247],v[140:143],a[24:27]
buffer_load_dwordx4 v8,s[72:75],s80 offen offset:1024 lds
v_mfma_f32_16x16x32_bf16 a[28:31],a[252:255],v[140:143],a[28:31]
v_exp_f32 v206,v206
v_mfma_f32_16x16x32_bf16 a[28:31],a[248:251],v[136:139],a[28:31]
s_waitcnt vmcnt(18)
v_cvt_pk_bf16_f32 v200,v200,v201
v_cvt_pk_bf16_f32 v201,v202,v203
v_smfmac_f32_16x16x64_bf16 v[48:51],v[28:31],v[136:143],v27
s_barrier
v_exp_f32 v207,v207
v_mfma_f32_16x16x32_bf16 v[232:235],a[128:131],v[148:151],v[44:47]
v_cvt_pk_bf16_f32 v202,v204,v205
v_cvt_pk_bf16_f32 v203,v206,v207
v_mfma_f32_16x16x32_bf16 v[232:235],a[132:135],v[180:183],v[232:235]
v_exp_f32 v216,v216
v_mfma_f32_16x16x32_bf16 v[232:235],a[136:139],v[212:215],v[232:235]
v_exp_f32 v217,v217
v_mfma_f32_16x16x32_bf16 v[232:235],a[140:143],v[244:247],v[232:235]
v_exp_f32 v218,v218
v_mfma_f32_16x16x32_bf16 v[236:239],a[156:159],v[244:247],v[44:47]
v_exp_f32 v219,v219
v_mfma_f32_16x16x32_bf16 v[236:239],a[152:155],v[212:215],v[236:239]
v_exp_f32 v220,v220
v_mfma_f32_16x16x32_bf16 v[236:239],a[148:151],v[180:183],v[236:239]
buffer_load_dwordx4 v9,s[72:75],s80 offen offset:2048 lds
v_mfma_f32_16x16x32_bf16 v[236:239],a[144:147],v[148:151],v[236:239]
v_exp_f32 v221,v221
v_mfma_f32_16x16x32_bf16 v[248:251],a[160:163],v[148:151],v[44:47]
v_exp_f32 v222,v222
v_mfma_f32_16x16x32_bf16 v[248:251],a[164:167],v[180:183],v[248:251]
v_exp_f32 v223,v223
v_mfma_f32_16x16x32_bf16 v[248:251],a[168:171],v[212:215],v[248:251]
v_cvt_pk_bf16_f32 v204,v216,v217
v_cvt_pk_bf16_f32 v205,v218,v219
v_mfma_f32_16x16x32_bf16 v[248:251],a[172:175],v[244:247],v[248:251]
v_cvt_pk_bf16_f32 v206,v220,v221
v_cvt_pk_bf16_f32 v207,v222,v223
v_mfma_f32_16x16x32_bf16 v[252:255],a[188:191],v[244:247],v[44:47]
v_exp_f32 v232,v232
v_mfma_f32_16x16x32_bf16 v[252:255],a[184:187],v[212:215],v[252:255]
v_exp_f32 v233,v233
v_mfma_f32_16x16x32_bf16 v[252:255],a[180:183],v[180:183],v[252:255]
v_exp_f32 v234,v234
v_mfma_f32_16x16x32_bf16 v[252:255],a[176:179],v[148:151],v[252:255]
v_exp_f32 v235,v235
v_mfma_f32_16x16x32_bf16 a[32:35],a[192:195],v[168:171],a[32:35]
v_cvt_pk_bf16_f32 v232,v232,v233
v_add_f32 v48,v49,v48
v_mfma_f32_16x16x32_bf16 a[32:35],a[196:199],v[172:175],a[32:35]
buffer_load_dwordx4 v10,s[72:75],s80 offen offset:3072 lds
v_mfma_f32_16x16x32_bf16 a[36:39],a[204:207],v[172:175],a[36:39]
v_exp_f32 v236,v236
v_mfma_f32_16x16x32_bf16 a[36:39],a[200:203],v[168:171],a[36:39]
v_exp_f32 v237,v237
v_mfma_f32_16x16x32_bf16 a[40:43],a[208:211],v[168:171],a[40:43]
v_exp_f32 v238,v238
v_mfma_f32_16x16x32_bf16 a[40:43],a[212:215],v[172:175],a[40:43]
v_exp_f32 v239,v239
v_mfma_f32_16x16x32_bf16 a[44:47],a[220:223],v[172:175],a[44:47]
v_cvt_pk_bf16_f32 v233,v234,v235
v_max_f32 v20,0x00800000,v48
v_mfma_f32_16x16x32_bf16 a[44:47],a[216:219],v[168:171],a[44:47]
s_lshl_b32 s95,s42,2
s_add_u32 s95,s95,s40
s_lshl_b32 s95,s95,4
v_mfma_f32_16x16x32_bf16 a[48:51],a[224:227],v[168:171],a[48:51]
v_mov_b32 v18,s68
v_mov_b32 v19,s69
v_mfma_f32_16x16x32_bf16 a[48:51],a[228:231],v[172:175],a[48:51]
s_add_u32 m0,s82,45056
v_cvt_pk_bf16_f32 v234,v236,v237
v_cvt_pk_bf16_f32 v235,v238,v239
v_mfma_f32_16x16x32_bf16 a[52:55],a[236:239],v[172:175],a[52:55]
v_exp_f32 v248,v248
v_mfma_f32_16x16x32_bf16 a[52:55],a[232:235],v[168:171],a[52:55]
v_exp_f32 v249,v249
v_mfma_f32_16x16x32_bf16 a[56:59],a[240:243],v[168:171],a[56:59]
buffer_load_dwordx4 v11,s[72:75],s80 offen lds
v_mfma_f32_16x16x32_bf16 a[56:59],a[244:247],v[172:175],a[56:59]
v_exp_f32 v250,v250
v_mfma_f32_16x16x32_bf16 a[60:63],a[252:255],v[172:175],a[60:63]
v_exp_f32 v251,v251
v_mfma_f32_16x16x32_bf16 a[60:63],a[248:251],v[168:171],a[60:63]
v_exp_f32 v252,v252
v_smfmac_f32_16x16x64_bf16 v[52:55],v[28:31],v[168:175],v27
s_waitcnt vmcnt(6)
s_mov_b64 exec,1
buffer_store_dwordx2 v[18:19],v6,s[64:67],s95 offen sc1
s_mov_b64 exec,-1
v_mfma_f32_16x16x32_bf16 a[64:67],a[192:195],v[200:203],a[64:67]
v_exp_f32 v253,v253
v_mfma_f32_16x16x32_bf16 a[64:67],a[196:199],v[204:207],a[64:67]
v_exp_f32 v254,v254
v_mfma_f32_16x16x32_bf16 a[68:71],a[204:207],v[204:207],a[68:71]
v_exp_f32 v255,v255
v_mfma_f32_16x16x32_bf16 a[68:71],a[200:203],v[200:203],a[68:71]
s_add_u32 m0,s82,46080
v_rcp_f32 v20,v20
v_mfma_f32_16x16x32_bf16 a[72:75],a[208:211],v[200:203],a[72:75]
v_cvt_pk_bf16_f32 v236,v248,v249
v_cvt_pk_bf16_f32 v237,v250,v251
v_mfma_f32_16x16x32_bf16 a[72:75],a[212:215],v[204:207],a[72:75]
v_cvt_pk_bf16_f32 v238,v252,v253
v_cvt_pk_bf16_f32 v239,v254,v255
v_mfma_f32_16x16x32_bf16 a[76:79],a[220:223],v[204:207],a[76:79]
buffer_load_dwordx4 v7,s[72:75],s83 offen lds
v_mfma_f32_16x16x32_bf16 a[76:79],a[216:219],v[200:203],a[76:79]
v_accvgpr_read_b32 v136,a0
v_accvgpr_read_b32 v137,a1
v_accvgpr_read_b32 v138,a2
v_accvgpr_read_b32 v139,a3
v_accvgpr_read_b32 v12,a4
v_accvgpr_read_b32 v13,a5
v_accvgpr_read_b32 v14,a6
v_accvgpr_read_b32 v15,a7
v_pk_mul_f32 v[136:137],v[20:21],v[136:137] op_sel:[0,0] op_sel_hi:[0,1]
v_pk_mul_f32 v[138:139],v[20:21],v[138:139] op_sel:[0,0] op_sel_hi:[0,1]
v_pk_mul_f32 v[12:13],v[20:21],v[12:13] op_sel:[0,0] op_sel_hi:[0,1]
v_pk_mul_f32 v[14:15],v[20:21],v[14:15] op_sel:[0,0] op_sel_hi:[0,1]
v_cvt_pk_bf16_f32 v136,v136,v137
v_cvt_pk_bf16_f32 v137,v138,v139
v_cvt_pk_bf16_f32 v138,v12,v13
v_cvt_pk_bf16_f32 v139,v14,v15
v_mfma_f32_16x16x32_bf16 a[80:83],a[224:227],v[200:203],a[80:83]
ds_read_b64_tr_b16 a[128:129],v1
ds_read_b64_tr_b16 a[130:131],v1 offset:5120
v_mfma_f32_16x16x32_bf16 a[80:83],a[228:231],v[204:207],a[80:83]
ds_read_b64_tr_b16 a[132:133],v1 offset:10240
v_accvgpr_read_b32 v152,a16
v_mfma_f32_16x16x32_bf16 a[84:87],a[236:239],v[204:207],a[84:87]
ds_read_b64_tr_b16 a[134:135],v1 offset:15360
v_accvgpr_read_b32 v153,a17
v_mfma_f32_16x16x32_bf16 a[84:87],a[232:235],v[200:203],a[84:87]
ds_read_b64_tr_b16 a[136:137],v1 offset:20480
v_mul_f32 v152,v20,v152
v_mfma_f32_16x16x32_bf16 a[88:91],a[240:243],v[200:203],a[88:91]
ds_read_b64_tr_b16 a[138:139],v1 offset:25600
ds_read_b64_tr_b16 a[140:141],v1 offset:30720
v_mfma_f32_16x16x32_bf16 a[88:91],a[244:247],v[204:207],a[88:91]
v_accvgpr_read_b32 v140,a8
v_accvgpr_read_b32 v141,a9
v_accvgpr_read_b32 v142,a10
v_accvgpr_read_b32 v143,a11
v_accvgpr_read_b32 v12,a12
v_accvgpr_read_b32 v13,a13
v_accvgpr_read_b32 v14,a14
v_accvgpr_read_b32 v15,a15
v_pk_mul_f32 v[140:141],v[20:21],v[140:141] op_sel:[0,0] op_sel_hi:[0,1]
v_pk_mul_f32 v[142:143],v[20:21],v[142:143] op_sel:[0,0] op_sel_hi:[0,1]
v_pk_mul_f32 v[12:13],v[20:21],v[12:13] op_sel:[0,0] op_sel_hi:[0,1]
v_pk_mul_f32 v[14:15],v[20:21],v[14:15] op_sel:[0,0] op_sel_hi:[0,1]
v_cvt_pk_bf16_f32 v140,v140,v141
v_cvt_pk_bf16_f32 v141,v142,v143
v_cvt_pk_bf16_f32 v142,v12,v13
v_cvt_pk_bf16_f32 v143,v14,v15
v_mfma_f32_16x16x32_bf16 a[92:95],a[252:255],v[204:207],a[92:95]
ds_read_b64_tr_b16 a[142:143],v1 offset:35840
v_mul_f32 v153,v20,v153
v_mfma_f32_16x16x32_bf16 a[92:95],a[248:251],v[200:203],a[92:95]
v_accvgpr_read_b32 v154,a18
v_accvgpr_read_b32 v155,a19
v_smfmac_f32_16x16x64_bf16 v[56:59],v[28:31],v[200:207],v27
v_cvt_pk_bf16_f32 v152,v152,v153
v_mfma_f32_16x16x32_bf16 a[96:99],a[192:195],v[232:235],a[96:99]
buffer_store_dwordx4 v[136:139],v6,s[36:39],s84 offen sc1
v_mfma_f32_16x16x32_bf16 a[96:99],a[196:199],v[236:239],a[96:99]
v_mul_f32 v154,v20,v154
v_mul_f32 v155,v20,v155
v_mfma_f32_16x16x32_bf16 a[100:103],a[204:207],v[236:239],a[100:103]
v_cvt_pk_bf16_f32 v153,v154,v155
v_accvgpr_read_b32 v154,a20
v_mfma_f32_16x16x32_bf16 a[100:103],a[200:203],v[232:235],a[100:103]
v_accvgpr_read_b32 v155,a21
v_accvgpr_read_b32 v12,a22
v_mfma_f32_16x16x32_bf16 a[104:107],a[208:211],v[232:235],a[104:107]
buffer_load_dwordx4 v8,s[72:75],s83 offen offset:1024 lds
v_mfma_f32_16x16x32_bf16 a[104:107],a[212:215],v[236:239],a[104:107]
v_accvgpr_read_b32 v13,a23
v_mul_f32 v154,v20,v154
v_mfma_f32_16x16x32_bf16 a[108:111],a[220:223],v[236:239],a[108:111]
v_mul_f32 v155,v20,v155
v_mul_f32 v12,v20,v12
v_mfma_f32_16x16x32_bf16 a[108:111],a[216:219],v[232:235],a[108:111]
v_mul_f32 v13,v20,v13
v_cvt_pk_bf16_f32 v154,v154,v155
v_mfma_f32_16x16x32_bf16 a[112:115],a[224:227],v[232:235],a[112:115]
ds_read_b64_tr_b16 a[144:145],v2 offset:32
v_cvt_pk_bf16_f32 v155,v12,v13
v_mfma_f32_16x16x32_bf16 a[112:115],a[228:231],v[236:239],a[112:115]
ds_read_b64_tr_b16 a[146:147],v2 offset:5152
ds_read_b64_tr_b16 a[148:149],v2 offset:10272
v_mfma_f32_16x16x32_bf16 a[116:119],a[236:239],v[236:239],a[116:119]
ds_read_b64_tr_b16 a[150:151],v2 offset:15392
v_accvgpr_read_b32 v156,a24
v_mfma_f32_16x16x32_bf16 a[116:119],a[232:235],v[232:235],a[116:119]
buffer_load_dwordx4 v9,s[72:75],s83 offen offset:2048 lds
v_mfma_f32_16x16x32_bf16 a[120:123],a[240:243],v[232:235],a[120:123]
ds_read_b64_tr_b16 a[152:153],v2 offset:20512
ds_read_b64_tr_b16 a[154:155],v2 offset:25632
v_mfma_f32_16x16x32_bf16 a[120:123],a[244:247],v[236:239],a[120:123]
ds_read_b64_tr_b16 a[156:157],v2 offset:30752
v_accvgpr_read_b32 v157,a25
v_mfma_f32_16x16x32_bf16 a[124:127],a[252:255],v[236:239],a[124:127]
ds_read_b64_tr_b16 a[158:159],v2 offset:35872
v_mul_f32 v156,v20,v156
v_mfma_f32_16x16x32_bf16 a[124:127],a[248:251],v[232:235],a[124:127]
)A"
R"A(v_mul_f32 v157,v20,v157
v_accvgpr_read_b32 v158,a26
v_smfmac_f32_16x16x64_bf16 v[60:63],v[28:31],v[232:239],v27
s_waitcnt lgkmcnt(7)
v_accvgpr_read_b32 v159,a27
v_mfma_f32_16x16x32_bf16 a[192:195],a[128:131],v[64:67],0
v_cvt_pk_bf16_f32 v156,v156,v157
v_mul_f32 v158,v20,v158
v_mfma_f32_16x16x32_bf16 a[192:195],a[132:135],v[68:71],a[192:195]
v_mul_f32 v159,v20,v159
v_cvt_pk_bf16_f32 v157,v158,v159
v_mfma_f32_16x16x32_bf16 a[192:195],a[136:139],v[72:75],a[192:195]
buffer_store_dwordx4 v[140:143],v6,s[36:39],s84 offen offset:1024 sc1
v_mfma_f32_16x16x32_bf16 a[192:195],a[140:143],v[76:79],a[192:195]
v_accvgpr_read_b32 v158,a28
v_accvgpr_read_b32 v159,a29
v_mfma_f32_16x16x32_bf16 a[196:199],a[140:143],v[92:95],0
v_accvgpr_read_b32 v14,a30
v_accvgpr_read_b32 v15,a31
v_mfma_f32_16x16x32_bf16 a[196:199],a[136:139],v[88:91],a[196:199]
v_mul_f32 v158,v20,v158
v_mul_f32 v159,v20,v159
v_mfma_f32_16x16x32_bf16 a[196:199],a[132:135],v[84:87],a[196:199]
buffer_load_dwordx4 v10,s[72:75],s83 offen offset:3072 lds
v_mfma_f32_16x16x32_bf16 a[196:199],a[128:131],v[80:83],a[196:199]
v_mul_f32 v14,v20,v14
v_mul_f32 v15,v20,v15
v_mfma_f32_16x16x32_bf16 a[200:203],a[128:131],v[96:99],0
v_cvt_pk_bf16_f32 v158,v158,v159
v_cvt_pk_bf16_f32 v159,v14,v15
v_mfma_f32_16x16x32_bf16 a[200:203],a[132:135],v[100:103],a[200:203]
ds_read_b64_tr_b16 a[160:161],v1 offset:64
ds_read_b64_tr_b16 a[162:163],v1 offset:5184
v_mfma_f32_16x16x32_bf16 a[200:203],a[136:139],v[104:107],a[200:203]
ds_read_b64_tr_b16 a[164:165],v1 offset:10304
v_add_f32 v52,v53,v52
v_mfma_f32_16x16x32_bf16 a[200:203],a[140:143],v[108:111],a[200:203]
ds_read_b64_tr_b16 a[166:167],v1 offset:15424
v_max_f32 v21,0x00800000,v52
v_mfma_f32_16x16x32_bf16 a[204:207],a[140:143],v[124:127],0
s_cmp_ge_u32 1,s71
s_cselect_b32 s80,4,2
ds_read_b64_tr_b16 a[168:169],v1 offset:20544
v_mfma_f32_16x16x32_bf16 a[204:207],a[136:139],v[120:123],a[204:207]
s_add_u32 m0,s82,50176
ds_read_b64_tr_b16 a[170:171],v1 offset:25664
ds_read_b64_tr_b16 a[172:173],v1 offset:30784
v_mfma_f32_16x16x32_bf16 a[204:207],a[132:135],v[116:119],a[204:207]
s_mul_i32 s80,s80,163840
s_add_u32 s80,s80,s81
ds_read_b64_tr_b16 a[174:175],v1 offset:35904
v_mfma_f32_16x16x32_bf16 a[204:207],a[128:131],v[112:115],a[204:207]
s_waitcnt lgkmcnt(8)
v_rcp_f32 v21,v21
v_mfma_f32_16x16x32_bf16 a[208:211],a[144:147],v[64:67],0
buffer_load_dwordx4 v11,s[72:75],s83 offen lds
v_mfma_f32_16x16x32_bf16 a[208:211],a[148:151],v[68:71],a[208:211]
v_accvgpr_read_b32 v168,a32
v_accvgpr_read_b32 v169,a33
v_accvgpr_read_b32 v170,a34
v_accvgpr_read_b32 v171,a35
v_accvgpr_read_b32 v12,a36
v_accvgpr_read_b32 v13,a37
v_accvgpr_read_b32 v14,a38
v_accvgpr_read_b32 v15,a39
v_pk_mul_f32 v[168:169],v[20:21],v[168:169] op_sel:[1,0] op_sel_hi:[1,1]
v_pk_mul_f32 v[170:171],v[20:21],v[170:171] op_sel:[1,0] op_sel_hi:[1,1]
v_pk_mul_f32 v[12:13],v[20:21],v[12:13] op_sel:[1,0] op_sel_hi:[1,1]
v_pk_mul_f32 v[14:15],v[20:21],v[14:15] op_sel:[1,0] op_sel_hi:[1,1]
v_cvt_pk_bf16_f32 v168,v168,v169
v_cvt_pk_bf16_f32 v169,v170,v171
v_cvt_pk_bf16_f32 v170,v12,v13
v_cvt_pk_bf16_f32 v171,v14,v15
v_mfma_f32_16x16x32_bf16 a[208:211],a[152:155],v[72:75],a[208:211]
s_add_u32 s83,s80,20480
ds_read_b64_tr_b16 a[176:177],v2 offset:96
ds_read_b64_tr_b16 a[178:179],v2 offset:5216
v_mfma_f32_16x16x32_bf16 a[208:211],a[156:159],v[76:79],a[208:211]
ds_read_b64_tr_b16 a[180:181],v2 offset:10336
v_accvgpr_read_b32 v184,a48
v_mfma_f32_16x16x32_bf16 a[212:215],a[156:159],v[92:95],0
ds_read_b64_tr_b16 a[182:183],v2 offset:15456
v_accvgpr_read_b32 v185,a49
v_mfma_f32_16x16x32_bf16 a[212:215],a[152:155],v[88:91],a[212:215]
ds_read_b64_tr_b16 a[184:185],v2 offset:20576
v_mul_f32 v184,v21,v184
v_mfma_f32_16x16x32_bf16 a[212:215],a[148:151],v[84:87],a[212:215]
buffer_store_dwordx4 v[152:155],v6,s[36:39],s84 offen offset:2048 sc1
v_mfma_f32_16x16x32_bf16 a[212:215],a[144:147],v[80:83],a[212:215]
ds_read_b64_tr_b16 a[186:187],v2 offset:25696
ds_read_b64_tr_b16 a[188:189],v2 offset:30816
v_mfma_f32_16x16x32_bf16 a[216:219],a[144:147],v[96:99],0
ds_read_b64_tr_b16 a[190:191],v2 offset:35936
v_mul_f32 v185,v21,v185
v_mfma_f32_16x16x32_bf16 a[216:219],a[148:151],v[100:103],a[216:219]
v_accvgpr_read_b32 v172,a40
v_accvgpr_read_b32 v173,a41
v_accvgpr_read_b32 v174,a42
v_accvgpr_read_b32 v175,a43
v_accvgpr_read_b32 v12,a44
v_accvgpr_read_b32 v13,a45
v_accvgpr_read_b32 v14,a46
v_accvgpr_read_b32 v15,a47
v_pk_mul_f32 v[172:173],v[20:21],v[172:173] op_sel:[1,0] op_sel_hi:[1,1]
v_pk_mul_f32 v[174:175],v[20:21],v[174:175] op_sel:[1,0] op_sel_hi:[1,1]
v_pk_mul_f32 v[12:13],v[20:21],v[12:13] op_sel:[1,0] op_sel_hi:[1,1]
v_pk_mul_f32 v[14:15],v[20:21],v[14:15] op_sel:[1,0] op_sel_hi:[1,1]
v_cvt_pk_bf16_f32 v172,v172,v173
v_cvt_pk_bf16_f32 v173,v174,v175
v_cvt_pk_bf16_f32 v174,v12,v13
v_cvt_pk_bf16_f32 v175,v14,v15
v_mfma_f32_16x16x32_bf16 a[216:219],a[152:155],v[104:107],a[216:219]
v_accvgpr_read_b32 v186,a50
v_accvgpr_read_b32 v187,a51
v_mfma_f32_16x16x32_bf16 a[216:219],a[156:159],v[108:111],a[216:219]
v_cvt_pk_bf16_f32 v184,v184,v185
v_mul_f32 v186,v21,v186
v_mfma_f32_16x16x32_bf16 a[220:223],a[156:159],v[124:127],0
v_mul_f32 v187,v21,v187
v_cvt_pk_bf16_f32 v185,v186,v187
v_mfma_f32_16x16x32_bf16 a[220:223],a[152:155],v[120:123],a[220:223]
v_accvgpr_read_b32 v186,a52
v_accvgpr_read_b32 v187,a53
v_mfma_f32_16x16x32_bf16 a[220:223],a[148:151],v[116:119],a[220:223]
s_add_u32 m0,s82,122880
v_accvgpr_read_b32 v16,a54
v_accvgpr_read_b32 v17,a55
v_mfma_f32_16x16x32_bf16 a[220:223],a[144:147],v[112:115],a[220:223]
s_waitcnt lgkmcnt(1)
v_mul_f32 v186,v21,v186
v_mul_f32 v187,v21,v187
v_mfma_f32_16x16x32_bf16 a[224:227],a[160:163],v[64:67],0
v_mul_f32 v16,v21,v16
v_mul_f32 v17,v21,v17
v_mfma_f32_16x16x32_bf16 a[224:227],a[164:167],v[68:71],a[224:227]
buffer_load_dwordx4 v7,s[72:75],s80 offen lds
v_mfma_f32_16x16x32_bf16 a[224:227],a[168:171],v[72:75],a[224:227]
v_cvt_pk_bf16_f32 v186,v186,v187
v_cvt_pk_bf16_f32 v187,v16,v17
v_mfma_f32_16x16x32_bf16 a[224:227],a[172:175],v[76:79],a[224:227]
ds_read_b64_tr_b16 a[128:129],v1 offset:128
ds_read_b64_tr_b16 a[130:131],v1 offset:5248
v_mfma_f32_16x16x32_bf16 a[228:231],a[172:175],v[92:95],0
ds_read_b64_tr_b16 a[132:133],v1 offset:10368
v_accvgpr_read_b32 v188,a56
v_mfma_f32_16x16x32_bf16 a[228:231],a[168:171],v[88:91],a[228:231]
ds_read_b64_tr_b16 a[134:135],v1 offset:15488
v_accvgpr_read_b32 v189,a57
v_mfma_f32_16x16x32_bf16 a[228:231],a[164:167],v[84:87],a[228:231]
ds_read_b64_tr_b16 a[136:137],v1 offset:20608
v_mul_f32 v188,v21,v188
v_mfma_f32_16x16x32_bf16 a[228:231],a[160:163],v[80:83],a[228:231]
ds_read_b64_tr_b16 a[138:139],v1 offset:25728
ds_read_b64_tr_b16 a[140:141],v1 offset:30848
v_mfma_f32_16x16x32_bf16 a[232:235],a[160:163],v[96:99],0
ds_read_b64_tr_b16 a[142:143],v1 offset:35968
v_mul_f32 v189,v21,v189
v_mfma_f32_16x16x32_bf16 a[232:235],a[164:167],v[100:103],a[232:235]
v_accvgpr_read_b32 v190,a58
v_accvgpr_read_b32 v191,a59
v_mfma_f32_16x16x32_bf16 a[232:235],a[168:171],v[104:107],a[232:235]
buffer_store_dwordx4 v[156:159],v6,s[36:39],s84 offen offset:3072 sc1
v_mfma_f32_16x16x32_bf16 a[232:235],a[172:175],v[108:111],a[232:235]
v_cvt_pk_bf16_f32 v188,v188,v189
v_mul_f32 v190,v21,v190
v_mfma_f32_16x16x32_bf16 a[236:239],a[172:175],v[124:127],0
v_mul_f32 v191,v21,v191
v_cvt_pk_bf16_f32 v189,v190,v191
v_mfma_f32_16x16x32_bf16 a[236:239],a[168:171],v[120:123],a[236:239]
v_accvgpr_read_b32 v190,a60
v_accvgpr_read_b32 v191,a61
v_mfma_f32_16x16x32_bf16 a[236:239],a[164:167],v[116:119],a[236:239]
buffer_load_dwordx4 v8,s[72:75],s80 offen offset:1024 lds
v_mfma_f32_16x16x32_bf16 a[236:239],a[160:163],v[112:115],a[236:239]
v_accvgpr_read_b32 v12,a62
v_accvgpr_read_b32 v13,a63
v_mfma_f32_16x16x32_bf16 a[240:243],a[176:179],v[64:67],0
v_mul_f32 v190,v21,v190
v_mul_f32 v191,v21,v191
v_mfma_f32_16x16x32_bf16 a[240:243],a[180:183],v[68:71],a[240:243]
v_mul_f32 v12,v21,v12
v_mul_f32 v13,v21,v13
v_mfma_f32_16x16x32_bf16 a[240:243],a[184:187],v[72:75],a[240:243]
s_waitcnt lgkmcnt(0)
v_cvt_pk_bf16_f32 v190,v190,v191
v_cvt_pk_bf16_f32 v191,v12,v13
v_mfma_f32_16x16x32_bf16 a[240:243],a[188:191],v[76:79],a[240:243]
ds_read_b64_tr_b16 a[144:145],v2 offset:160
ds_read_b64_tr_b16 a[146:147],v2 offset:5280
v_mfma_f32_16x16x32_bf16 a[244:247],a[188:191],v[92:95],0
ds_read_b64_tr_b16 a[148:149],v2 offset:10400
v_add_f32 v56,v57,v56
v_mfma_f32_16x16x32_bf16 a[244:247],a[184:187],v[88:91],a[244:247]
ds_read_b64_tr_b16 a[150:151],v2 offset:15520
v_max_f32 v22,0x00800000,v56
v_mfma_f32_16x16x32_bf16 a[244:247],a[180:183],v[84:87],a[244:247]
ds_read_b64_tr_b16 a[152:153],v2 offset:20640
v_accvgpr_read_b32 v200,a64
v_mfma_f32_16x16x32_bf16 a[244:247],a[176:179],v[80:83],a[244:247]
ds_read_b64_tr_b16 a[154:155],v2 offset:25760
ds_read_b64_tr_b16 a[156:157],v2 offset:30880
v_mfma_f32_16x16x32_bf16 a[248:251],a[176:179],v[96:99],0
v_rcp_f32 v22,v22
v_mfma_f32_16x16x32_bf16 a[248:251],a[180:183],v[100:103],a[248:251]
buffer_load_dwordx4 v9,s[72:75],s80 offen offset:2048 lds
v_mfma_f32_16x16x32_bf16 a[248:251],a[184:187],v[104:107],a[248:251]
ds_read_b64_tr_b16 a[158:159],v2 offset:36000
v_accvgpr_read_b32 v201,a65
v_mfma_f32_16x16x32_bf16 a[248:251],a[188:191],v[108:111],a[248:251]
v_mul_f32 v200,v22,v200
v_mul_f32 v201,v22,v201
v_mfma_f32_16x16x32_bf16 a[252:255],a[188:191],v[124:127],0
v_accvgpr_read_b32 v202,a66
v_accvgpr_read_b32 v203,a67
v_mfma_f32_16x16x32_bf16 a[252:255],a[184:187],v[120:123],a[252:255]
v_cvt_pk_bf16_f32 v200,v200,v201
v_mul_f32 v202,v22,v202
v_mfma_f32_16x16x32_bf16 a[252:255],a[180:183],v[116:119],a[252:255]
s_waitcnt vmcnt(18)
v_mul_f32 v203,v22,v203
v_cvt_pk_bf16_f32 v201,v202,v203
v_mfma_f32_16x16x32_bf16 a[252:255],a[176:179],v[112:115],a[252:255]
s_barrier
buffer_store_dwordx4 v[168:171],v6,s[36:39],s85 offen sc1
v_mfma_f32_16x16x32_bf16 a[0:3],a[128:131],v[64:67],0
v_accvgpr_read_b32 v202,a68
v_accvgpr_read_b32 v203,a69
v_mfma_f32_16x16x32_bf16 a[0:3],a[132:135],v[68:71],a[0:3]
v_accvgpr_read_b32 v14,a70
v_accvgpr_read_b32 v15,a71
v_mfma_f32_16x16x32_bf16 a[0:3],a[136:139],v[72:75],a[0:3]
v_mul_f32 v202,v22,v202
v_mul_f32 v203,v22,v203
v_mfma_f32_16x16x32_bf16 a[0:3],a[140:143],v[76:79],a[0:3]
v_mul_f32 v14,v22,v14
v_mul_f32 v15,v22,v15
v_mfma_f32_16x16x32_bf16 a[4:7],a[140:143],v[92:95],0
buffer_load_dwordx4 v10,s[72:75],s80 offen offset:3072 lds
v_mfma_f32_16x16x32_bf16 a[4:7],a[136:139],v[88:91],a[4:7]
s_lshl_b32 s94,s70,4
v_cvt_pk_bf16_f32 v202,v202,v203
v_cvt_pk_bf16_f32 v203,v14,v15
v_mfma_f32_16x16x32_bf16 a[4:7],a[132:135],v[84:87],a[4:7]
s_add_u32 s94,s94,s40
v_accvgpr_read_b32 v204,a72
v_accvgpr_read_b32 v205,a73
v_mfma_f32_16x16x32_bf16 a[4:7],a[128:131],v[80:83],a[4:7]
s_lshl_b32 s94,s94,4
v_mul_f32 v204,v22,v204
v_mul_f32 v205,v22,v205
v_mfma_f32_16x16x32_bf16 a[8:11],a[128:131],v[96:99],0
v_accvgpr_read_b32 v206,a74
v_accvgpr_read_b32 v207,a75
v_mfma_f32_16x16x32_bf16 a[8:11],a[132:135],v[100:103],a[8:11]
ds_read_b64_tr_b16 a[160:161],v1 offset:192
ds_read_b64_tr_b16 a[162:163],v1 offset:5312
v_mfma_f32_16x16x32_bf16 a[8:11],a[136:139],v[104:107],a[8:11]
v_cvt_pk_bf16_f32 v204,v204,v205
v_mul_f32 v206,v22,v206
v_mfma_f32_16x16x32_bf16 a[8:11],a[140:143],v[108:111],a[8:11]
v_mul_f32 v207,v22,v207
v_cvt_pk_bf16_f32 v205,v206,v207
v_mfma_f32_16x16x32_bf16 a[12:15],a[140:143],v[124:127],0
ds_read_b64_tr_b16 a[164:165],v1 offset:10432
ds_read_b64_tr_b16 a[166:167],v1 offset:15552
v_mfma_f32_16x16x32_bf16 a[12:15],a[136:139],v[120:123],a[12:15]
s_add_u32 m0,s82,126976
v_accvgpr_read_b32 v206,a76
v_accvgpr_read_b32 v207,a77
v_mfma_f32_16x16x32_bf16 a[12:15],a[132:135],v[116:119],a[12:15]
v_accvgpr_read_b32 v16,a78
v_accvgpr_read_b32 v17,a79
v_mfma_f32_16x16x32_bf16 a[12:15],a[128:131],v[112:115],a[12:15]
s_waitcnt lgkmcnt(4)
v_mul_f32 v206,v22,v206
v_mul_f32 v207,v22,v207
v_mfma_f32_16x16x32_bf16 a[16:19],a[144:147],v[64:67],0
buffer_load_dwordx4 v11,s[72:75],s80 offen lds
v_mfma_f32_16x16x32_bf16 a[16:19],a[148:151],v[68:71],a[16:19]
ds_read_b64_tr_b16 a[168:169],v1 offset:20672
ds_read_b64_tr_b16 a[170:171],v1 offset:25792
)A"
R"A(v_mfma_f32_16x16x32_bf16 a[16:19],a[152:155],v[72:75],a[16:19]
v_mul_f32 v16,v22,v16
v_mul_f32 v17,v22,v17
v_mfma_f32_16x16x32_bf16 a[16:19],a[156:159],v[76:79],a[16:19]
v_cvt_pk_bf16_f32 v206,v206,v207
v_cvt_pk_bf16_f32 v207,v16,v17
v_mfma_f32_16x16x32_bf16 a[20:23],a[156:159],v[92:95],0
buffer_store_dwordx4 v[172:175],v6,s[36:39],s85 offen offset:1024 sc1
v_mfma_f32_16x16x32_bf16 a[20:23],a[152:155],v[88:91],a[20:23]
ds_read_b64_tr_b16 a[172:173],v1 offset:30912
ds_read_b64_tr_b16 a[174:175],v1 offset:36032
v_mfma_f32_16x16x32_bf16 a[20:23],a[148:151],v[84:87],a[20:23]
v_accvgpr_read_b32 v216,a80
v_accvgpr_read_b32 v217,a81
v_mfma_f32_16x16x32_bf16 a[20:23],a[144:147],v[80:83],a[20:23]
v_mul_f32 v216,v22,v216
v_mul_f32 v217,v22,v217
v_mfma_f32_16x16x32_bf16 a[24:27],a[144:147],v[96:99],0
v_accvgpr_read_b32 v218,a82
v_accvgpr_read_b32 v219,a83
v_mfma_f32_16x16x32_bf16 a[24:27],a[148:151],v[100:103],a[24:27]
s_add_u32 m0,s82,128000
v_cvt_pk_bf16_f32 v216,v216,v217
v_mul_f32 v218,v22,v218
v_mfma_f32_16x16x32_bf16 a[24:27],a[152:155],v[104:107],a[24:27]
v_mul_f32 v219,v22,v219
v_cvt_pk_bf16_f32 v217,v218,v219
v_mfma_f32_16x16x32_bf16 a[24:27],a[156:159],v[108:111],a[24:27]
v_accvgpr_read_b32 v218,a84
v_accvgpr_read_b32 v219,a85
v_mfma_f32_16x16x32_bf16 a[28:31],a[156:159],v[124:127],0
buffer_load_dwordx4 v7,s[72:75],s83 offen lds
v_mfma_f32_16x16x32_bf16 a[28:31],a[152:155],v[120:123],a[28:31]
v_accvgpr_read_b32 v12,a86
v_accvgpr_read_b32 v13,a87
v_mfma_f32_16x16x32_bf16 a[28:31],a[148:151],v[116:119],a[28:31]
v_mul_f32 v218,v22,v218
v_mul_f32 v219,v22,v219
v_mfma_f32_16x16x32_bf16 a[28:31],a[144:147],v[112:115],a[28:31]
s_waitcnt lgkmcnt(0)
v_mul_f32 v12,v22,v12
v_mul_f32 v13,v22,v13
v_mfma_f32_16x16x32_bf16 a[32:35],a[160:163],v[64:67],0
v_cvt_pk_bf16_f32 v218,v218,v219
v_cvt_pk_bf16_f32 v219,v12,v13
v_mfma_f32_16x16x32_bf16 a[32:35],a[164:167],v[68:71],a[32:35]
ds_read_b64_tr_b16 a[176:177],v2 offset:224
ds_read_b64_tr_b16 a[178:179],v2 offset:5344
v_mfma_f32_16x16x32_bf16 a[32:35],a[168:171],v[72:75],a[32:35]
ds_read_b64_tr_b16 a[180:181],v2 offset:10464
v_bfe_u32 v24,v6,4,2
v_mfma_f32_16x16x32_bf16 a[32:35],a[172:175],v[76:79],a[32:35]
ds_read_b64_tr_b16 a[182:183],v2 offset:15584
v_min_u32 v24,2,v24
v_mfma_f32_16x16x32_bf16 a[36:39],a[172:175],v[92:95],0
ds_read_b64_tr_b16 a[184:185],v2 offset:20704
v_add_u32 v24,s71,v24
v_mfma_f32_16x16x32_bf16 a[36:39],a[168:171],v[88:91],a[36:39]
ds_read_b64_tr_b16 a[186:187],v2 offset:25824
ds_read_b64_tr_b16 a[188:189],v2 offset:30944
v_mfma_f32_16x16x32_bf16 a[36:39],a[164:167],v[84:87],a[36:39]
buffer_store_dwordx4 v[184:187],v6,s[36:39],s85 offen offset:2048 sc1
v_mfma_f32_16x16x32_bf16 a[36:39],a[160:163],v[80:83],a[36:39]
ds_read_b64_tr_b16 a[190:191],v2 offset:36064
v_add_u32 v24,1,v24
v_mfma_f32_16x16x32_bf16 a[40:43],a[160:163],v[96:99],0
v_and_b32 v24,3,v24
v_lshlrev_b32 v24,6,v24
v_mfma_f32_16x16x32_bf16 a[40:43],a[164:167],v[100:103],a[40:43]
v_accvgpr_read_b32 v220,a88
v_accvgpr_read_b32 v221,a89
v_mfma_f32_16x16x32_bf16 a[40:43],a[168:171],v[104:107],a[40:43]
buffer_load_dwordx4 v8,s[72:75],s83 offen offset:1024 lds
v_mfma_f32_16x16x32_bf16 a[40:43],a[172:175],v[108:111],a[40:43]
v_mul_f32 v220,v22,v220
v_mul_f32 v221,v22,v221
v_mfma_f32_16x16x32_bf16 a[44:47],a[172:175],v[124:127],0
v_accvgpr_read_b32 v222,a90
v_accvgpr_read_b32 v223,a91
v_mfma_f32_16x16x32_bf16 a[44:47],a[168:171],v[120:123],a[44:47]
v_cvt_pk_bf16_f32 v220,v220,v221
v_mul_f32 v222,v22,v222
v_mfma_f32_16x16x32_bf16 a[44:47],a[164:167],v[116:119],a[44:47]
buffer_load_dwordx2 v[26:27],v24,s[64:67],s94 offen sc1
v_mfma_f32_16x16x32_bf16 a[44:47],a[160:163],v[112:115],a[44:47]
s_waitcnt lgkmcnt(0)
v_mul_f32 v223,v22,v223
v_cvt_pk_bf16_f32 v221,v222,v223
v_mfma_f32_16x16x32_bf16 a[48:51],a[176:179],v[64:67],0
v_accvgpr_read_b32 v222,a92
v_accvgpr_read_b32 v223,a93
v_mfma_f32_16x16x32_bf16 a[48:51],a[180:183],v[68:71],a[48:51]
v_accvgpr_read_b32 v14,a94
v_accvgpr_read_b32 v15,a95
v_mfma_f32_16x16x32_bf16 a[48:51],a[184:187],v[72:75],a[48:51]
v_mul_f32 v222,v22,v222
v_mul_f32 v223,v22,v223
v_mfma_f32_16x16x32_bf16 a[48:51],a[188:191],v[76:79],a[48:51]
v_mul_f32 v14,v22,v14
v_mul_f32 v15,v22,v15
v_mfma_f32_16x16x32_bf16 a[52:55],a[188:191],v[92:95],0
v_cvt_pk_bf16_f32 v222,v222,v223
v_cvt_pk_bf16_f32 v223,v14,v15
v_mfma_f32_16x16x32_bf16 a[52:55],a[184:187],v[88:91],a[52:55]
buffer_load_dwordx4 v9,s[72:75],s83 offen offset:2048 lds
v_mfma_f32_16x16x32_bf16 a[52:55],a[180:183],v[84:87],a[52:55]
ds_read_b64_tr_b16 a[128:129],v1 offset:256
ds_read_b64_tr_b16 a[130:131],v1 offset:5376
v_mfma_f32_16x16x32_bf16 a[52:55],a[176:179],v[80:83],a[52:55]
ds_read_b64_tr_b16 a[132:133],v1 offset:10496
v_add_f32 v60,v61,v60
v_mfma_f32_16x16x32_bf16 a[56:59],a[176:179],v[96:99],0
ds_read_b64_tr_b16 a[134:135],v1 offset:15616
v_max_f32 v23,0x00800000,v60
v_mfma_f32_16x16x32_bf16 a[56:59],a[180:183],v[100:103],a[56:59]
ds_read_b64_tr_b16 a[136:137],v1 offset:20736
ds_read_b64_tr_b16 a[138:139],v1 offset:25856
v_mfma_f32_16x16x32_bf16 a[56:59],a[184:187],v[104:107],a[56:59]
ds_read_b64_tr_b16 a[140:141],v1 offset:30976
ds_read_b64_tr_b16 a[142:143],v1 offset:36096
v_mfma_f32_16x16x32_bf16 a[56:59],a[188:191],v[108:111],a[56:59]
buffer_store_dwordx4 v[188:191],v6,s[36:39],s85 offen offset:3072 sc1
v_mfma_f32_16x16x32_bf16 a[60:63],a[188:191],v[124:127],0
v_rcp_f32 v23,v23
v_mfma_f32_16x16x32_bf16 a[60:63],a[184:187],v[120:123],a[60:63]
v_accvgpr_read_b32 v232,a96
v_accvgpr_read_b32 v233,a97
v_mfma_f32_16x16x32_bf16 a[60:63],a[180:183],v[116:119],a[60:63]
v_mul_f32 v232,v23,v232
v_mul_f32 v233,v23,v233
v_mfma_f32_16x16x32_bf16 a[60:63],a[176:179],v[112:115],a[60:63]
s_waitcnt lgkmcnt(5)
v_accvgpr_read_b32 v234,a98
v_accvgpr_read_b32 v235,a99
v_mfma_f32_16x16x32_bf16 a[64:67],a[128:131],v[64:67],0
s_waitcnt lgkmcnt(4)
v_cvt_pk_bf16_f32 v232,v232,v233
v_mul_f32 v234,v23,v234
v_mfma_f32_16x16x32_bf16 a[64:67],a[132:135],v[68:71],a[64:67]
s_waitcnt lgkmcnt(2)
v_mul_f32 v235,v23,v235
v_cvt_pk_bf16_f32 v233,v234,v235
v_mfma_f32_16x16x32_bf16 a[64:67],a[136:139],v[72:75],a[64:67]
s_waitcnt lgkmcnt(0)
v_accvgpr_read_b32 v234,a100
v_accvgpr_read_b32 v235,a101
v_mfma_f32_16x16x32_bf16 a[64:67],a[140:143],v[76:79],a[64:67]
buffer_load_dwordx4 v10,s[72:75],s83 offen offset:3072 lds
v_mfma_f32_16x16x32_bf16 a[68:71],a[140:143],v[92:95],0
v_accvgpr_read_b32 v16,a102
v_accvgpr_read_b32 v17,a103
v_mfma_f32_16x16x32_bf16 a[68:71],a[136:139],v[88:91],a[68:71]
v_mul_f32 v234,v23,v234
v_mul_f32 v235,v23,v235
v_mfma_f32_16x16x32_bf16 a[68:71],a[132:135],v[84:87],a[68:71]
v_mul_f32 v16,v23,v16
v_mul_f32 v17,v23,v17
v_mfma_f32_16x16x32_bf16 a[68:71],a[128:131],v[80:83],a[68:71]
ds_read_b64_tr_b16 a[144:145],v2 offset:288
ds_read_b64_tr_b16 a[146:147],v2 offset:5408
v_mfma_f32_16x16x32_bf16 a[72:75],a[128:131],v[96:99],0
ds_read_b64_tr_b16 a[148:149],v2 offset:10528
ds_read_b64_tr_b16 a[150:151],v2 offset:15648
v_mfma_f32_16x16x32_bf16 a[72:75],a[132:135],v[100:103],a[72:75]
ds_read_b64_tr_b16 a[152:153],v2 offset:20768
ds_read_b64_tr_b16 a[154:155],v2 offset:25888
v_mfma_f32_16x16x32_bf16 a[72:75],a[136:139],v[104:107],a[72:75]
ds_read_b64_tr_b16 a[156:157],v2 offset:31008
ds_read_b64_tr_b16 a[158:159],v2 offset:36128
v_mfma_f32_16x16x32_bf16 a[72:75],a[140:143],v[108:111],a[72:75]
s_add_u32 m0,s82,132096
v_mfma_f32_16x16x32_bf16 a[76:79],a[140:143],v[124:127],0
v_cvt_pk_bf16_f32 v234,v234,v235
v_cvt_pk_bf16_f32 v235,v16,v17
v_mfma_f32_16x16x32_bf16 a[76:79],a[136:139],v[120:123],a[76:79]
v_accvgpr_read_b32 v236,a104
v_accvgpr_read_b32 v237,a105
v_mfma_f32_16x16x32_bf16 a[76:79],a[132:135],v[116:119],a[76:79]
v_mul_f32 v236,v23,v236
v_mul_f32 v237,v23,v237
v_mfma_f32_16x16x32_bf16 a[76:79],a[128:131],v[112:115],a[76:79]
s_waitcnt lgkmcnt(6)
buffer_load_dwordx4 v11,s[72:75],s83 offen lds
v_mfma_f32_16x16x32_bf16 a[80:83],a[144:147],v[64:67],0
s_waitcnt lgkmcnt(4)
v_accvgpr_read_b32 v238,a106
v_accvgpr_read_b32 v239,a107
v_mfma_f32_16x16x32_bf16 a[80:83],a[148:151],v[68:71],a[80:83]
s_waitcnt lgkmcnt(2)
v_cvt_pk_bf16_f32 v236,v236,v237
v_mul_f32 v238,v23,v238
v_mfma_f32_16x16x32_bf16 a[80:83],a[152:155],v[72:75],a[80:83]
s_waitcnt lgkmcnt(0)
v_mul_f32 v239,v23,v239
v_cvt_pk_bf16_f32 v237,v238,v239
v_mfma_f32_16x16x32_bf16 a[80:83],a[156:159],v[76:79],a[80:83]
v_accvgpr_read_b32 v238,a108
v_accvgpr_read_b32 v239,a109
v_mfma_f32_16x16x32_bf16 a[84:87],a[156:159],v[92:95],0
v_accvgpr_read_b32 v12,a110
v_accvgpr_read_b32 v13,a111
v_mfma_f32_16x16x32_bf16 a[84:87],a[152:155],v[88:91],a[84:87]
v_mul_f32 v238,v23,v238
v_mul_f32 v239,v23,v239
v_mfma_f32_16x16x32_bf16 a[84:87],a[148:151],v[84:87],a[84:87]
v_subrev_u32 v1,81920,v1
v_mul_f32 v12,v23,v12
v_mfma_f32_16x16x32_bf16 a[84:87],a[144:147],v[80:83],a[84:87]
ds_read_b64_tr_b16 a[160:161],v1
ds_read_b64_tr_b16 a[162:163],v1 offset:5120
v_mfma_f32_16x16x32_bf16 a[88:91],a[144:147],v[96:99],0
ds_read_b64_tr_b16 a[164:165],v1 offset:10240
ds_read_b64_tr_b16 a[166:167],v1 offset:15360
v_mfma_f32_16x16x32_bf16 a[88:91],a[148:151],v[100:103],a[88:91]
ds_read_b64_tr_b16 a[168:169],v1 offset:20480
ds_read_b64_tr_b16 a[170:171],v1 offset:25600
v_mfma_f32_16x16x32_bf16 a[88:91],a[152:155],v[104:107],a[88:91]
ds_read_b64_tr_b16 a[172:173],v1 offset:30720
ds_read_b64_tr_b16 a[174:175],v1 offset:35840
v_mfma_f32_16x16x32_bf16 a[88:91],a[156:159],v[108:111],a[88:91]
v_accvgpr_read_b32 v248,a112
v_accvgpr_read_b32 v249,a113
v_mul_f32 v248,v23,v248
v_mul_f32 v249,v23,v249
v_accvgpr_read_b32 v250,a114
v_accvgpr_read_b32 v251,a115
v_accvgpr_read_b32 v252,a120
v_accvgpr_read_b32 v253,a121
v_cmp_nlt_f32 s[56:57],v48,s63
v_mul_f32 v252,v23,v252
v_mul_f32 v253,v23,v253
v_accvgpr_read_b32 v254,a122
v_accvgpr_read_b32 v255,a123
v_mfma_f32_16x16x32_bf16 a[92:95],a[156:159],v[124:127],0
v_cvt_pk_bf16_f32 v248,v248,v249
v_mul_f32 v250,v23,v250
v_mul_f32 v251,v23,v251
v_cvt_pk_bf16_f32 v249,v250,v251
v_cvt_pk_bf16_f32 v252,v252,v253
v_mul_f32 v254,v23,v254
v_mul_f32 v255,v23,v255
v_cmp_nlt_f32 s[58:59],v52,s63
s_nop 1
s_or_b64 s[56:57],s[56:57],s[58:59]
v_cvt_pk_bf16_f32 v253,v254,v255
v_mfma_f32_16x16x32_bf16 a[92:95],a[152:155],v[120:123],a[92:95]
v_accvgpr_read_b32 v250,a116
v_accvgpr_read_b32 v251,a117
v_accvgpr_read_b32 v14,a118
v_accvgpr_read_b32 v15,a119
v_mul_f32 v250,v23,v250
v_mul_f32 v251,v23,v251
v_accvgpr_read_b32 v254,a124
v_accvgpr_read_b32 v255,a125
v_cmp_nlt_f32 s[58:59],v56,s63
s_nop 1
s_or_b64 s[56:57],s[56:57],s[58:59]
v_accvgpr_read_b32 v16,a126
v_accvgpr_read_b32 v17,a127
v_mul_f32 v254,v23,v254
v_mul_f32 v255,v23,v255
s_waitcnt vmcnt(4)
v_cmp_ne_u32 s[90:91],s68,v26
v_cmp_ne_u32 s[92:93],s69,v27
s_nop 0
s_or_b64 s[90:91],s[90:91],s[92:93]
s_cbranch_scc1 L_fr8_%=
L_fo8_%=:
v_mfma_f32_16x16x32_bf16 a[92:95],a[148:151],v[116:119],a[92:95]
v_mul_f32 v13,v23,v13
v_cvt_pk_bf16_f32 v238,v238,v239
v_cvt_pk_bf16_f32 v239,v12,v13
v_mul_f32 v14,v23,v14
v_mul_f32 v15,v23,v15
v_cvt_pk_bf16_f32 v250,v250,v251
v_cvt_pk_bf16_f32 v251,v14,v15
v_mul_f32 v16,v23,v16
v_mul_f32 v17,v23,v17
v_cvt_pk_bf16_f32 v254,v254,v255
v_cmp_nlt_f32 s[58:59],v60,s63
s_nop 1
s_or_b64 s[56:57],s[56:57],s[58:59]
v_cvt_pk_bf16_f32 v255,v16,v17
v_mfma_f32_16x16x32_bf16 a[92:95],a[144:147],v[112:115],a[92:95]
s_cmp_lg_u64 s[56:57],0
s_cbranch_scc1 L_slow1_%=
L_sret1_%=:
v_subrev_u32 v2,81920,v2
s_waitcnt lgkmcnt(6)
v_mfma_f32_16x16x32_bf16 a[192:195],a[160:163],v[136:139],a[192:195]
s_cmp_ge_u32 0,s71
s_cselect_b32 s76,2,0
s_lshl_b32 s76,s76,16
s_waitcnt lgkmcnt(4)
v_mfma_f32_16x16x32_bf16 a[192:195],a[164:167],v[140:143],a[192:195]
s_add_u32 s77,s76,4096
s_add_u32 s78,s76,8192
s_add_u32 s79,s76,12288
s_max_i32 s76,s76,s18
s_waitcnt lgkmcnt(2)
v_mfma_f32_16x16x32_bf16 a[192:195],a[168:171],v[152:155],a[192:195]
s_max_i32 s77,s77,s19
buffer_load_dwordx4 v[128:131],v6,s[28:31],s76 offen sc1
s_waitcnt lgkmcnt(0)
v_mfma_f32_16x16x32_bf16 a[192:195],a[172:175],v[156:159],a[192:195]
s_max_i32 s78,s78,s100
ds_read_b64_tr_b16 a[176:177],v2 offset:32
ds_read_b64_tr_b16 a[178:179],v2 offset:5152
v_mfma_f32_16x16x32_bf16 a[196:199],a[172:175],v[188:191],a[196:199]
)A"
R"A(s_cmp_ge_u32 2,s71
s_cselect_b32 s80,6,4
ds_read_b64_tr_b16 a[180:181],v2 offset:10272
v_mfma_f32_16x16x32_bf16 a[196:199],a[168:171],v[184:187],a[196:199]
s_mul_i32 s80,s80,163840
s_add_u32 s80,s80,s81
ds_read_b64_tr_b16 a[182:183],v2 offset:15392
v_mfma_f32_16x16x32_bf16 a[196:199],a[164:167],v[172:175],a[196:199]
s_max_i32 s79,s79,s101
buffer_store_dwordx4 v[200:203],v6,s[36:39],s86 offen sc1
v_mfma_f32_16x16x32_bf16 a[196:199],a[160:163],v[168:171],a[196:199]
s_add_u32 s83,s80,20480
ds_read_b64_tr_b16 a[184:185],v2 offset:20512
ds_read_b64_tr_b16 a[186:187],v2 offset:25632
v_mfma_f32_16x16x32_bf16 a[200:203],a[160:163],v[200:203],a[200:203]
ds_read_b64_tr_b16 a[188:189],v2 offset:30752
v_mfma_f32_16x16x32_bf16 a[200:203],a[164:167],v[204:207],a[200:203]
ds_read_b64_tr_b16 a[190:191],v2 offset:35872
v_mfma_f32_16x16x32_bf16 a[200:203],a[168:171],v[216:219],a[200:203]
buffer_load_dwordx4 v[160:163],v6,s[28:31],s76 offen offset:1024 sc1
v_mfma_f32_16x16x32_bf16 a[200:203],a[172:175],v[220:223],a[200:203]
ds_read_b64_tr_b16 a[128:129],v1 offset:64
ds_read_b64_tr_b16 a[130:131],v1 offset:5184
v_mfma_f32_16x16x32_bf16 a[204:207],a[172:175],v[252:255],a[204:207]
ds_read_b64_tr_b16 a[132:133],v1 offset:10304
v_mfma_f32_16x16x32_bf16 a[204:207],a[168:171],v[248:251],a[204:207]
ds_read_b64_tr_b16 a[134:135],v1 offset:15424
v_mfma_f32_16x16x32_bf16 a[204:207],a[164:167],v[236:239],a[204:207]
buffer_store_dwordx4 v[204:207],v6,s[36:39],s86 offen offset:1024 sc1
v_mfma_f32_16x16x32_bf16 a[204:207],a[160:163],v[232:235],a[204:207]
s_barrier
ds_read_b64_tr_b16 a[136:137],v1 offset:20544
ds_read_b64_tr_b16 a[138:139],v1 offset:25664
s_waitcnt lgkmcnt(8)
v_mfma_f32_16x16x32_bf16 a[208:211],a[176:179],v[136:139],a[208:211]
ds_read_b64_tr_b16 a[140:141],v1 offset:30784
v_mfma_f32_16x16x32_bf16 a[208:211],a[180:183],v[140:143],a[208:211]
buffer_load_dwordx4 v[192:195],v6,s[28:31],s76 offen offset:2048 sc1
v_mfma_f32_16x16x32_bf16 a[208:211],a[184:187],v[152:155],a[208:211]
s_add_u32 m0,s82,81920
ds_read_b64_tr_b16 a[142:143],v1 offset:35904
ds_read_b64_tr_b16 a[144:145],v2 offset:96
s_waitcnt lgkmcnt(9)
v_mfma_f32_16x16x32_bf16 a[208:211],a[188:191],v[156:159],a[208:211]
ds_read_b64_tr_b16 a[146:147],v2 offset:5216
v_mfma_f32_16x16x32_bf16 a[212:215],a[188:191],v[188:191],a[212:215]
ds_read_b64_tr_b16 a[148:149],v2 offset:10336
v_mfma_f32_16x16x32_bf16 a[212:215],a[184:187],v[184:187],a[212:215]
buffer_store_dwordx4 v[216:219],v6,s[36:39],s86 offen offset:2048 sc1
v_mfma_f32_16x16x32_bf16 a[212:215],a[180:183],v[172:175],a[212:215]
ds_read_b64_tr_b16 a[150:151],v2 offset:15456
ds_read_b64_tr_b16 a[152:153],v2 offset:20576
v_mfma_f32_16x16x32_bf16 a[212:215],a[176:179],v[168:171],a[212:215]
ds_read_b64_tr_b16 a[154:155],v2 offset:25696
v_mfma_f32_16x16x32_bf16 a[216:219],a[176:179],v[200:203],a[216:219]
ds_read_b64_tr_b16 a[156:157],v2 offset:30816
v_mfma_f32_16x16x32_bf16 a[216:219],a[180:183],v[204:207],a[216:219]
buffer_load_dwordx4 v[224:227],v6,s[28:31],s76 offen offset:3072 sc1
v_mfma_f32_16x16x32_bf16 a[216:219],a[184:187],v[216:219],a[216:219]
ds_read_b64_tr_b16 a[158:159],v2 offset:35936
ds_read_b64_tr_b16 a[160:161],v1 offset:128
v_mfma_f32_16x16x32_bf16 a[216:219],a[188:191],v[220:223],a[216:219]
ds_read_b64_tr_b16 a[162:163],v1 offset:5248
v_mfma_f32_16x16x32_bf16 a[220:223],a[188:191],v[252:255],a[220:223]
ds_read_b64_tr_b16 a[164:165],v1 offset:10368
v_mfma_f32_16x16x32_bf16 a[220:223],a[184:187],v[248:251],a[220:223]
buffer_load_dwordx4 v7,s[72:75],s80 offen lds
v_mfma_f32_16x16x32_bf16 a[220:223],a[180:183],v[236:239],a[220:223]
ds_read_b64_tr_b16 a[166:167],v1 offset:15488
ds_read_b64_tr_b16 a[168:169],v1 offset:20608
v_mfma_f32_16x16x32_bf16 a[220:223],a[176:179],v[232:235],a[220:223]
s_waitcnt lgkmcnt(7)
ds_read_b64_tr_b16 a[170:171],v1 offset:25728
v_mfma_f32_16x16x32_bf16 a[224:227],a[128:131],v[136:139],a[224:227]
ds_read_b64_tr_b16 a[172:173],v1 offset:30848
v_mfma_f32_16x16x32_bf16 a[224:227],a[132:135],v[140:143],a[224:227]
buffer_load_dwordx4 v[132:135],v6,s[28:31],s77 offen sc1
v_mfma_f32_16x16x32_bf16 a[224:227],a[136:139],v[152:155],a[224:227]
ds_read_b64_tr_b16 a[174:175],v1 offset:35968
ds_read_b64_tr_b16 a[176:177],v2 offset:160
v_mfma_f32_16x16x32_bf16 a[224:227],a[140:143],v[156:159],a[224:227]
ds_read_b64_tr_b16 a[178:179],v2 offset:5280
v_mfma_f32_16x16x32_bf16 a[228:231],a[140:143],v[188:191],a[228:231]
ds_read_b64_tr_b16 a[180:181],v2 offset:10400
v_mfma_f32_16x16x32_bf16 a[228:231],a[136:139],v[184:187],a[228:231]
buffer_load_dwordx4 v[164:167],v6,s[28:31],s77 offen offset:1024 sc1
v_mfma_f32_16x16x32_bf16 a[228:231],a[132:135],v[172:175],a[228:231]
ds_read_b64_tr_b16 a[182:183],v2 offset:15520
ds_read_b64_tr_b16 a[184:185],v2 offset:20640
v_mfma_f32_16x16x32_bf16 a[228:231],a[128:131],v[168:171],a[228:231]
ds_read_b64_tr_b16 a[186:187],v2 offset:25760
v_mfma_f32_16x16x32_bf16 a[232:235],a[128:131],v[200:203],a[232:235]
ds_read_b64_tr_b16 a[188:189],v2 offset:30880
v_mfma_f32_16x16x32_bf16 a[232:235],a[132:135],v[204:207],a[232:235]
buffer_load_dwordx4 v8,s[72:75],s80 offen offset:1024 lds
v_mfma_f32_16x16x32_bf16 a[232:235],a[136:139],v[216:219],a[232:235]
ds_read_b64_tr_b16 a[190:191],v2 offset:36000
v_mfma_f32_16x16x32_bf16 a[232:235],a[140:143],v[220:223],a[232:235]
v_mfma_f32_16x16x32_bf16 a[236:239],a[140:143],v[252:255],a[236:239]
ds_read_b64_tr_b16 a[140:141],v1 offset:30912
ds_read_b64_tr_b16 a[142:143],v1 offset:36032
v_mfma_f32_16x16x32_bf16 a[236:239],a[136:139],v[248:251],a[236:239]
buffer_load_dwordx4 v[196:199],v6,s[28:31],s77 offen offset:2048 sc1
v_mfma_f32_16x16x32_bf16 a[236:239],a[132:135],v[236:239],a[236:239]
ds_read_b64_tr_b16 a[132:133],v1 offset:10432
ds_read_b64_tr_b16 a[134:135],v1 offset:15552
v_mfma_f32_16x16x32_bf16 a[236:239],a[128:131],v[232:235],a[236:239]
ds_read_b64_tr_b16 a[128:129],v1 offset:192
v_mfma_f32_16x16x32_bf16 a[240:243],a[144:147],v[136:139],a[240:243]
ds_read_b64_tr_b16 a[130:131],v1 offset:5312
v_mfma_f32_16x16x32_bf16 a[240:243],a[148:151],v[140:143],a[240:243]
buffer_store_dwordx4 v[220:223],v6,s[36:39],s86 offen offset:3072 sc1
v_mfma_f32_16x16x32_bf16 a[240:243],a[152:155],v[152:155],a[240:243]
s_waitcnt lgkmcnt(6)
ds_read_b64_tr_b16 a[136:137],v1 offset:20672
ds_read_b64_tr_b16 a[138:139],v1 offset:25792
v_mfma_f32_16x16x32_bf16 a[240:243],a[156:159],v[156:159],a[240:243]
v_mfma_f32_16x16x32_bf16 a[244:247],a[156:159],v[188:191],a[244:247]
v_mfma_f32_16x16x32_bf16 a[244:247],a[152:155],v[184:187],a[244:247]
buffer_load_dwordx4 v[228:231],v6,s[28:31],s77 offen offset:3072 sc1
v_mfma_f32_16x16x32_bf16 a[244:247],a[148:151],v[172:175],a[244:247]
v_mfma_f32_16x16x32_bf16 a[244:247],a[144:147],v[168:171],a[244:247]
v_mfma_f32_16x16x32_bf16 a[248:251],a[144:147],v[200:203],a[248:251]
v_mfma_f32_16x16x32_bf16 a[248:251],a[148:151],v[204:207],a[248:251]
buffer_load_dwordx4 v9,s[72:75],s80 offen offset:2048 lds
v_mfma_f32_16x16x32_bf16 a[248:251],a[152:155],v[216:219],a[248:251]
v_mfma_f32_16x16x32_bf16 a[248:251],a[156:159],v[220:223],a[248:251]
v_mfma_f32_16x16x32_bf16 a[252:255],a[156:159],v[252:255],a[252:255]
ds_read_b64_tr_b16 a[156:157],v2 offset:30944
ds_read_b64_tr_b16 a[158:159],v2 offset:36064
v_mfma_f32_16x16x32_bf16 a[252:255],a[152:155],v[248:251],a[252:255]
buffer_load_dwordx4 v[144:147],v6,s[28:31],s78 offen sc1
v_mfma_f32_16x16x32_bf16 a[252:255],a[148:151],v[236:239],a[252:255]
ds_read_b64_tr_b16 a[148:149],v2 offset:10464
ds_read_b64_tr_b16 a[150:151],v2 offset:15584
v_mfma_f32_16x16x32_bf16 a[252:255],a[144:147],v[232:235],a[252:255]
ds_read_b64_tr_b16 a[144:145],v2 offset:224
v_mfma_f32_16x16x32_bf16 a[0:3],a[160:163],v[136:139],a[0:3]
ds_read_b64_tr_b16 a[146:147],v2 offset:5344
v_mfma_f32_16x16x32_bf16 a[0:3],a[164:167],v[140:143],a[0:3]
buffer_load_dwordx4 v[176:179],v6,s[28:31],s78 offen offset:1024 sc1
v_mfma_f32_16x16x32_bf16 a[0:3],a[168:171],v[152:155],a[0:3]
ds_read_b64_tr_b16 a[152:153],v2 offset:20704
ds_read_b64_tr_b16 a[154:155],v2 offset:25824
v_mfma_f32_16x16x32_bf16 a[0:3],a[172:175],v[156:159],a[0:3]
v_mfma_f32_16x16x32_bf16 a[4:7],a[172:175],v[188:191],a[4:7]
v_mfma_f32_16x16x32_bf16 a[4:7],a[168:171],v[184:187],a[4:7]
buffer_load_dwordx4 v10,s[72:75],s80 offen offset:3072 lds
v_mfma_f32_16x16x32_bf16 a[4:7],a[164:167],v[172:175],a[4:7]
v_mfma_f32_16x16x32_bf16 a[4:7],a[160:163],v[168:171],a[4:7]
v_mfma_f32_16x16x32_bf16 a[8:11],a[160:163],v[200:203],a[8:11]
v_mfma_f32_16x16x32_bf16 a[8:11],a[164:167],v[204:207],a[8:11]
buffer_load_dwordx4 v[208:211],v6,s[28:31],s78 offen offset:2048 sc1
v_mfma_f32_16x16x32_bf16 a[8:11],a[168:171],v[216:219],a[8:11]
v_mfma_f32_16x16x32_bf16 a[8:11],a[172:175],v[220:223],a[8:11]
v_mfma_f32_16x16x32_bf16 a[12:15],a[172:175],v[252:255],a[12:15]
ds_read_b64_tr_b16 a[172:173],v1 offset:30976
ds_read_b64_tr_b16 a[174:175],v1 offset:36096
v_mfma_f32_16x16x32_bf16 a[12:15],a[168:171],v[248:251],a[12:15]
buffer_store_dwordx4 v[232:235],v6,s[36:39],s87 offen sc1
v_mfma_f32_16x16x32_bf16 a[12:15],a[164:167],v[236:239],a[12:15]
s_add_u32 m0,s82,86016
ds_read_b64_tr_b16 a[164:165],v1 offset:10496
ds_read_b64_tr_b16 a[166:167],v1 offset:15616
v_mfma_f32_16x16x32_bf16 a[12:15],a[160:163],v[232:235],a[12:15]
ds_read_b64_tr_b16 a[160:161],v1 offset:256
v_mfma_f32_16x16x32_bf16 a[16:19],a[176:179],v[136:139],a[16:19]
ds_read_b64_tr_b16 a[162:163],v1 offset:5376
v_mfma_f32_16x16x32_bf16 a[16:19],a[180:183],v[140:143],a[16:19]
buffer_load_dwordx4 v[240:243],v6,s[28:31],s78 offen offset:3072 sc1
v_mfma_f32_16x16x32_bf16 a[16:19],a[184:187],v[152:155],a[16:19]
ds_read_b64_tr_b16 a[168:169],v1 offset:20736
ds_read_b64_tr_b16 a[170:171],v1 offset:25856
v_mfma_f32_16x16x32_bf16 a[16:19],a[188:191],v[156:159],a[16:19]
v_add_u32 v1,40960,v1
v_mfma_f32_16x16x32_bf16 a[20:23],a[188:191],v[188:191],a[20:23]
ds_read_b64_tr_b16 a[96:97],v1 offset:128
ds_read_b64_tr_b16 a[98:99],v1 offset:5248
v_mfma_f32_16x16x32_bf16 a[20:23],a[184:187],v[184:187],a[20:23]
buffer_load_dwordx4 v11,s[72:75],s80 offen lds
v_mfma_f32_16x16x32_bf16 a[20:23],a[180:183],v[172:175],a[20:23]
ds_read_b64_tr_b16 a[100:101],v1 offset:10368
ds_read_b64_tr_b16 a[102:103],v1 offset:15488
v_mfma_f32_16x16x32_bf16 a[20:23],a[176:179],v[168:171],a[20:23]
ds_read_b64_tr_b16 a[104:105],v1 offset:20608
v_mfma_f32_16x16x32_bf16 a[24:27],a[176:179],v[200:203],a[24:27]
ds_read_b64_tr_b16 a[106:107],v1 offset:25728
v_mfma_f32_16x16x32_bf16 a[24:27],a[180:183],v[204:207],a[24:27]
buffer_load_dwordx4 v[148:151],v6,s[28:31],s79 offen sc1
v_mfma_f32_16x16x32_bf16 a[24:27],a[184:187],v[216:219],a[24:27]
ds_read_b64_tr_b16 a[108:109],v1 offset:30848
ds_read_b64_tr_b16 a[110:111],v1 offset:35968
v_mfma_f32_16x16x32_bf16 a[24:27],a[188:191],v[220:223],a[24:27]
v_mfma_f32_16x16x32_bf16 a[28:31],a[188:191],v[252:255],a[28:31]
ds_read_b64_tr_b16 a[188:189],v2 offset:31008
ds_read_b64_tr_b16 a[190:191],v2 offset:36128
v_mfma_f32_16x16x32_bf16 a[28:31],a[184:187],v[248:251],a[28:31]
s_add_u32 m0,s82,87040
buffer_load_dwordx4 v[180:183],v6,s[28:31],s79 offen offset:1024 sc1
v_mfma_f32_16x16x32_bf16 a[28:31],a[180:183],v[236:239],a[28:31]
ds_read_b64_tr_b16 a[180:181],v2 offset:10528
ds_read_b64_tr_b16 a[182:183],v2 offset:15648
v_mfma_f32_16x16x32_bf16 a[28:31],a[176:179],v[232:235],a[28:31]
s_waitcnt lgkmcnt(7)
ds_read_b64_tr_b16 a[176:177],v2 offset:288
v_mfma_f32_16x16x32_bf16 a[32:35],a[128:131],v[136:139],a[32:35]
ds_read_b64_tr_b16 a[178:179],v2 offset:5408
v_mfma_f32_16x16x32_bf16 a[32:35],a[132:135],v[140:143],a[32:35]
buffer_store_dwordx4 v[236:239],v6,s[36:39],s87 offen offset:1024 sc1
v_mfma_f32_16x16x32_bf16 a[32:35],a[136:139],v[152:155],a[32:35]
ds_read_b64_tr_b16 a[184:185],v2 offset:20768
ds_read_b64_tr_b16 a[186:187],v2 offset:25888
v_mfma_f32_16x16x32_bf16 a[32:35],a[140:143],v[156:159],a[32:35]
v_add_u32 v2,40960,v2
v_mfma_f32_16x16x32_bf16 a[36:39],a[140:143],v[188:191],a[36:39]
ds_read_b64_tr_b16 a[112:113],v2 offset:160
ds_read_b64_tr_b16 a[114:115],v2 offset:5280
v_mfma_f32_16x16x32_bf16 a[36:39],a[136:139],v[184:187],a[36:39]
)A"
R"A(buffer_load_dwordx4 v[212:215],v6,s[28:31],s79 offen offset:2048 sc1
v_mfma_f32_16x16x32_bf16 a[36:39],a[132:135],v[172:175],a[36:39]
ds_read_b64_tr_b16 a[116:117],v2 offset:10400
ds_read_b64_tr_b16 a[118:119],v2 offset:15520
v_mfma_f32_16x16x32_bf16 a[36:39],a[128:131],v[168:171],a[36:39]
ds_read_b64_tr_b16 a[120:121],v2 offset:20640
v_mfma_f32_16x16x32_bf16 a[40:43],a[128:131],v[200:203],a[40:43]
ds_read_b64_tr_b16 a[122:123],v2 offset:25760
v_mfma_f32_16x16x32_bf16 a[40:43],a[132:135],v[204:207],a[40:43]
buffer_load_dwordx4 v7,s[72:75],s83 offen lds
v_mfma_f32_16x16x32_bf16 a[40:43],a[136:139],v[216:219],a[40:43]
ds_read_b64_tr_b16 a[124:125],v2 offset:30880
ds_read_b64_tr_b16 a[126:127],v2 offset:36000
v_mfma_f32_16x16x32_bf16 a[40:43],a[140:143],v[220:223],a[40:43]
v_mfma_f32_16x16x32_bf16 a[44:47],a[140:143],v[252:255],a[44:47]
ds_read_b64_tr_b16 a[140:141],v1 offset:30720
ds_read_b64_tr_b16 a[142:143],v1 offset:35840
v_mfma_f32_16x16x32_bf16 a[44:47],a[136:139],v[248:251],a[44:47]
buffer_load_dwordx4 v[244:247],v6,s[28:31],s79 offen offset:3072 sc1
v_mfma_f32_16x16x32_bf16 a[44:47],a[132:135],v[236:239],a[44:47]
ds_read_b64_tr_b16 a[132:133],v1 offset:10240
ds_read_b64_tr_b16 a[134:135],v1 offset:15360
v_mfma_f32_16x16x32_bf16 a[44:47],a[128:131],v[232:235],a[44:47]
ds_read_b64_tr_b16 a[128:129],v1
v_mfma_f32_16x16x32_bf16 a[48:51],a[144:147],v[136:139],a[48:51]
ds_read_b64_tr_b16 a[130:131],v1 offset:5120
v_mfma_f32_16x16x32_bf16 a[48:51],a[148:151],v[140:143],a[48:51]
buffer_store_dwordx4 v[248:251],v6,s[36:39],s87 offen offset:2048 sc1
v_mfma_f32_16x16x32_bf16 a[48:51],a[152:155],v[152:155],a[48:51]
ds_read_b64_tr_b16 a[136:137],v1 offset:20480
ds_read_b64_tr_b16 a[138:139],v1 offset:25600
v_mfma_f32_16x16x32_bf16 a[48:51],a[156:159],v[156:159],a[48:51]
s_cmp_ge_u32 1,s71
s_cselect_b32 s76,4,2
s_lshl_b32 s76,s76,16
v_mfma_f32_16x16x32_bf16 a[52:55],a[156:159],v[188:191],a[52:55]
s_add_u32 s77,s76,4096
s_add_u32 s78,s76,8192
s_add_u32 s79,s76,12288
v_mfma_f32_16x16x32_bf16 a[52:55],a[152:155],v[184:187],a[52:55]
s_max_i32 s76,s76,s18
buffer_load_dwordx4 v8,s[72:75],s83 offen offset:1024 lds
v_mfma_f32_16x16x32_bf16 a[52:55],a[148:151],v[172:175],a[52:55]
s_max_i32 s77,s77,s19
s_max_i32 s78,s78,s100
s_max_i32 s79,s79,s101
v_mfma_f32_16x16x32_bf16 a[52:55],a[144:147],v[168:171],a[52:55]
v_mfma_f32_16x16x32_bf16 a[56:59],a[144:147],v[200:203],a[56:59]
v_mfma_f32_16x16x32_bf16 a[56:59],a[148:151],v[204:207],a[56:59]
buffer_store_dwordx4 v[252:255],v6,s[36:39],s87 offen offset:3072 sc1
v_mfma_f32_16x16x32_bf16 a[56:59],a[152:155],v[216:219],a[56:59]
v_mfma_f32_16x16x32_bf16 a[56:59],a[156:159],v[220:223],a[56:59]
v_mfma_f32_16x16x32_bf16 a[60:63],a[156:159],v[252:255],a[60:63]
ds_read_b64_tr_b16 a[156:157],v2 offset:30752
ds_read_b64_tr_b16 a[158:159],v2 offset:35872
v_mfma_f32_16x16x32_bf16 a[60:63],a[152:155],v[248:251],a[60:63]
ds_read_b64_tr_b16 a[152:153],v2 offset:20512
v_mfma_f32_16x16x32_bf16 a[60:63],a[148:151],v[236:239],a[60:63]
buffer_load_dwordx4 v9,s[72:75],s83 offen offset:2048 lds
v_mfma_f32_16x16x32_bf16 a[60:63],a[144:147],v[232:235],a[60:63]
ds_read_b64_tr_b16 a[144:145],v2 offset:32
ds_read_b64_tr_b16 a[146:147],v2 offset:5152
v_mfma_f32_16x16x32_bf16 a[64:67],a[160:163],v[136:139],a[64:67]
ds_read_b64_tr_b16 a[148:149],v2 offset:10272
v_mfma_f32_16x16x32_bf16 a[64:67],a[164:167],v[140:143],a[64:67]
ds_read_b64_tr_b16 a[150:151],v2 offset:15392
v_mfma_f32_16x16x32_bf16 a[64:67],a[168:171],v[152:155],a[64:67]
ds_read_b64_tr_b16 a[154:155],v2 offset:25632
v_mfma_f32_16x16x32_bf16 a[64:67],a[172:175],v[156:159],a[64:67]
v_mfma_f32_16x16x32_bf16 a[68:71],a[172:175],v[188:191],a[68:71]
v_mfma_f32_16x16x32_bf16 a[68:71],a[168:171],v[184:187],a[68:71]
v_mfma_f32_16x16x32_bf16 a[68:71],a[164:167],v[172:175],a[68:71]
v_mfma_f32_16x16x32_bf16 a[68:71],a[160:163],v[168:171],a[68:71]
v_mfma_f32_16x16x32_bf16 a[72:75],a[160:163],v[200:203],a[72:75]
v_mfma_f32_16x16x32_bf16 a[72:75],a[164:167],v[204:207],a[72:75]
v_mfma_f32_16x16x32_bf16 a[72:75],a[168:171],v[216:219],a[72:75]
v_mfma_f32_16x16x32_bf16 a[72:75],a[172:175],v[220:223],a[72:75]
buffer_load_dwordx4 v[64:67],v6,s[28:31],s76 offen sc1
v_mfma_f32_16x16x32_bf16 a[76:79],a[172:175],v[252:255],a[76:79]
ds_read_b64_tr_b16 a[172:173],v1 offset:30784
ds_read_b64_tr_b16 a[174:175],v1 offset:35904
v_mfma_f32_16x16x32_bf16 a[76:79],a[168:171],v[248:251],a[76:79]
ds_read_b64_tr_b16 a[168:169],v1 offset:20544
v_mfma_f32_16x16x32_bf16 a[76:79],a[164:167],v[236:239],a[76:79]
ds_read_b64_tr_b16 a[164:165],v1 offset:10304
v_mfma_f32_16x16x32_bf16 a[76:79],a[160:163],v[232:235],a[76:79]
s_waitcnt lgkmcnt(4)
buffer_load_dwordx4 v10,s[72:75],s83 offen offset:3072 lds
v_mfma_f32_16x16x32_bf16 a[80:83],a[176:179],v[136:139],a[80:83]
ds_read_b64_tr_b16 a[160:161],v1 offset:64
ds_read_b64_tr_b16 a[162:163],v1 offset:5184
v_mfma_f32_16x16x32_bf16 a[80:83],a[180:183],v[140:143],a[80:83]
ds_read_b64_tr_b16 a[166:167],v1 offset:15424
v_mfma_f32_16x16x32_bf16 a[80:83],a[184:187],v[152:155],a[80:83]
ds_read_b64_tr_b16 a[170:171],v1 offset:25664
v_mfma_f32_16x16x32_bf16 a[80:83],a[188:191],v[156:159],a[80:83]
buffer_load_dwordx4 v[68:71],v6,s[28:31],s76 offen offset:1024 sc1
v_mfma_f32_16x16x32_bf16 a[84:87],a[188:191],v[188:191],a[84:87]
v_mfma_f32_16x16x32_bf16 a[84:87],a[184:187],v[184:187],a[84:87]
v_mfma_f32_16x16x32_bf16 a[84:87],a[180:183],v[172:175],a[84:87]
v_mfma_f32_16x16x32_bf16 a[84:87],a[176:179],v[168:171],a[84:87]
v_mfma_f32_16x16x32_bf16 a[88:91],a[176:179],v[200:203],a[88:91]
v_mfma_f32_16x16x32_bf16 a[88:91],a[180:183],v[204:207],a[88:91]
s_add_u32 m0,s82,91136
v_mfma_f32_16x16x32_bf16 a[88:91],a[184:187],v[216:219],a[88:91]
v_mfma_f32_16x16x32_bf16 a[88:91],a[188:191],v[220:223],a[88:91]
buffer_load_dwordx4 v[72:75],v6,s[28:31],s76 offen offset:2048 sc1
v_mfma_f32_16x16x32_bf16 a[92:95],a[188:191],v[252:255],a[92:95]
ds_read_b64_tr_b16 a[188:189],v2 offset:30816
ds_read_b64_tr_b16 a[190:191],v2 offset:35936
v_mfma_f32_16x16x32_bf16 a[92:95],a[184:187],v[248:251],a[92:95]
ds_read_b64_tr_b16 a[184:185],v2 offset:20576
v_mfma_f32_16x16x32_bf16 a[92:95],a[180:183],v[236:239],a[92:95]
s_cmp_ge_u32 0,s71
s_cselect_b32 s80,3,1
s_mul_i32 s80,s80,163840
v_mfma_f32_16x16x32_bf16 a[92:95],a[176:179],v[232:235],a[92:95]
s_add_u32 s80,s80,s81
buffer_load_dwordx4 v11,s[72:75],s83 offen lds
s_waitcnt vmcnt(14)
v_mfma_f32_16x16x32_bf16 a[192:195],a[128:131],v[128:131],a[192:195]
s_add_u32 s83,s80,20480
ds_read_b64_tr_b16 a[176:177],v2 offset:96
ds_read_b64_tr_b16 a[178:179],v2 offset:5216
v_mfma_f32_16x16x32_bf16 a[192:195],a[132:135],v[160:163],a[192:195]
ds_read_b64_tr_b16 a[180:181],v2 offset:10336
v_mfma_f32_16x16x32_bf16 a[192:195],a[136:139],v[192:195],a[192:195]
ds_read_b64_tr_b16 a[182:183],v2 offset:15456
v_mfma_f32_16x16x32_bf16 a[192:195],a[140:143],v[224:227],a[192:195]
buffer_load_dwordx4 v[76:79],v6,s[28:31],s76 offen offset:3072 sc1
v_mfma_f32_16x16x32_bf16 a[196:199],a[140:143],v[228:231],a[196:199]
ds_read_b64_tr_b16 a[186:187],v2 offset:25696
v_mfma_f32_16x16x32_bf16 a[196:199],a[136:139],v[196:199],a[196:199]
v_mfma_f32_16x16x32_bf16 a[196:199],a[132:135],v[164:167],a[196:199]
v_mfma_f32_16x16x32_bf16 a[196:199],a[128:131],v[132:135],a[196:199]
v_mfma_f32_16x16x32_bf16 a[200:203],a[128:131],v[144:147],a[200:203]
v_mfma_f32_16x16x32_bf16 a[200:203],a[132:135],v[176:179],a[200:203]
v_mfma_f32_16x16x32_bf16 a[200:203],a[136:139],v[208:211],a[200:203]
v_mfma_f32_16x16x32_bf16 a[200:203],a[140:143],v[240:243],a[200:203]
s_lshl_b32 s95,s42,2
buffer_load_dwordx4 v[80:83],v6,s[28:31],s77 offen sc1
s_waitcnt vmcnt(11)
v_mfma_f32_16x16x32_bf16 a[204:207],a[140:143],v[244:247],a[204:207]
s_add_u32 s95,s95,s40
s_lshl_b32 s95,s95,4
v_mov_b32 v18,s68
v_mfma_f32_16x16x32_bf16 a[204:207],a[136:139],v[212:215],a[204:207]
ds_read_b64_tr_b16 a[136:137],v1 offset:20672
v_mov_b32 v19,s69
v_mfma_f32_16x16x32_bf16 a[204:207],a[132:135],v[180:183],a[204:207]
ds_read_b64_tr_b16 a[132:133],v1 offset:10432
ds_read_b64_tr_b16 a[134:135],v1 offset:15552
v_mfma_f32_16x16x32_bf16 a[204:207],a[128:131],v[148:151],a[204:207]
s_barrier
ds_read_b64_tr_b16 a[128:129],v1 offset:192
ds_read_b64_tr_b16 a[130:131],v1 offset:5312
v_mfma_f32_16x16x32_bf16 a[208:211],a[144:147],v[128:131],a[208:211]
ds_read_b64_tr_b16 a[138:139],v1 offset:25792
v_mfma_f32_16x16x32_bf16 a[208:211],a[148:151],v[160:163],a[208:211]
ds_read_b64_tr_b16 a[140:141],v1 offset:30912
v_mfma_f32_16x16x32_bf16 a[208:211],a[152:155],v[192:195],a[208:211]
s_add_u32 m0,s82,0
s_waitcnt vmcnt(8)
s_mov_b64 exec,1
buffer_store_dwordx2 v[18:19],v6,s[64:67],s95 offen offset:1024 sc1
s_mov_b64 exec,-1
v_mfma_f32_16x16x32_bf16 a[208:211],a[156:159],v[224:227],a[208:211]
ds_read_b64_tr_b16 a[142:143],v1 offset:36032
v_mfma_f32_16x16x32_bf16 a[212:215],a[156:159],v[228:231],a[212:215]
v_mfma_f32_16x16x32_bf16 a[212:215],a[152:155],v[196:199],a[212:215]
v_mfma_f32_16x16x32_bf16 a[212:215],a[148:151],v[164:167],a[212:215]
buffer_load_dwordx4 v[84:87],v6,s[28:31],s77 offen offset:1024 sc1
v_mfma_f32_16x16x32_bf16 a[212:215],a[144:147],v[132:135],a[212:215]
v_mfma_f32_16x16x32_bf16 a[216:219],a[144:147],v[144:147],a[216:219]
v_mfma_f32_16x16x32_bf16 a[216:219],a[148:151],v[176:179],a[216:219]
v_mfma_f32_16x16x32_bf16 a[216:219],a[152:155],v[208:211],a[216:219]
buffer_load_dwordx4 v7,s[72:75],s80 offen lds
v_mfma_f32_16x16x32_bf16 a[216:219],a[156:159],v[240:243],a[216:219]
v_mfma_f32_16x16x32_bf16 a[220:223],a[156:159],v[244:247],a[220:223]
ds_read_b64_tr_b16 a[156:157],v2 offset:30944
ds_read_b64_tr_b16 a[158:159],v2 offset:36064
v_mfma_f32_16x16x32_bf16 a[220:223],a[152:155],v[212:215],a[220:223]
ds_read_b64_tr_b16 a[152:153],v2 offset:20704
v_mfma_f32_16x16x32_bf16 a[220:223],a[148:151],v[180:183],a[220:223]
buffer_load_dwordx4 v[88:91],v6,s[28:31],s77 offen offset:2048 sc1
v_mfma_f32_16x16x32_bf16 a[220:223],a[144:147],v[148:151],a[220:223]
s_waitcnt lgkmcnt(3)
ds_read_b64_tr_b16 a[144:145],v2 offset:224
ds_read_b64_tr_b16 a[146:147],v2 offset:5344
v_mfma_f32_16x16x32_bf16 a[224:227],a[160:163],v[128:131],a[224:227]
ds_read_b64_tr_b16 a[148:149],v2 offset:10464
v_mfma_f32_16x16x32_bf16 a[224:227],a[164:167],v[160:163],a[224:227]
ds_read_b64_tr_b16 a[150:151],v2 offset:15584
v_mfma_f32_16x16x32_bf16 a[224:227],a[168:171],v[192:195],a[224:227]
buffer_load_dwordx4 v[92:95],v6,s[28:31],s77 offen offset:3072 sc1
v_mfma_f32_16x16x32_bf16 a[224:227],a[172:175],v[224:227],a[224:227]
ds_read_b64_tr_b16 a[154:155],v2 offset:25824
v_mfma_f32_16x16x32_bf16 a[228:231],a[172:175],v[228:231],a[228:231]
v_mfma_f32_16x16x32_bf16 a[228:231],a[168:171],v[196:199],a[228:231]
v_mfma_f32_16x16x32_bf16 a[228:231],a[164:167],v[164:167],a[228:231]
buffer_load_dwordx4 v8,s[72:75],s80 offen offset:1024 lds
v_mfma_f32_16x16x32_bf16 a[228:231],a[160:163],v[132:135],a[228:231]
v_mfma_f32_16x16x32_bf16 a[232:235],a[160:163],v[144:147],a[232:235]
v_mfma_f32_16x16x32_bf16 a[232:235],a[164:167],v[176:179],a[232:235]
v_mfma_f32_16x16x32_bf16 a[232:235],a[168:171],v[208:211],a[232:235]
buffer_load_dwordx4 v[96:99],v6,s[28:31],s78 offen sc1
v_mfma_f32_16x16x32_bf16 a[232:235],a[172:175],v[240:243],a[232:235]
v_mfma_f32_16x16x32_bf16 a[236:239],a[172:175],v[244:247],a[236:239]
ds_read_b64_tr_b16 a[172:173],v1 offset:30976
ds_read_b64_tr_b16 a[174:175],v1 offset:36096
v_mfma_f32_16x16x32_bf16 a[236:239],a[168:171],v[212:215],a[236:239]
ds_read_b64_tr_b16 a[168:169],v1 offset:20736
v_mfma_f32_16x16x32_bf16 a[236:239],a[164:167],v[180:183],a[236:239]
ds_read_b64_tr_b16 a[164:165],v1 offset:10496
v_mfma_f32_16x16x32_bf16 a[236:239],a[160:163],v[148:151],a[236:239]
ds_read_b64_tr_b16 a[160:161],v1 offset:256
v_mfma_f32_16x16x32_bf16 a[240:243],a[176:179],v[128:131],a[240:243]
ds_read_b64_tr_b16 a[162:163],v1 offset:5376
ds_read_b64_tr_b16 a[166:167],v1 offset:15616
v_mfma_f32_16x16x32_bf16 a[240:243],a[180:183],v[160:163],a[240:243]
ds_read_b64_tr_b16 a[170:171],v1 offset:25856
v_add_u32 v1,81920,v1
v_mfma_f32_16x16x32_bf16 a[240:243],a[184:187],v[192:195],a[240:243]
)A"
R"A(buffer_load_dwordx4 v[100:103],v6,s[28:31],s78 offen offset:1024 sc1
v_mfma_f32_16x16x32_bf16 a[240:243],a[188:191],v[224:227],a[240:243]
v_mfma_f32_16x16x32_bf16 a[244:247],a[188:191],v[228:231],a[244:247]
v_mfma_f32_16x16x32_bf16 a[244:247],a[184:187],v[196:199],a[244:247]
v_mfma_f32_16x16x32_bf16 a[244:247],a[180:183],v[164:167],a[244:247]
buffer_load_dwordx4 v9,s[72:75],s80 offen offset:2048 lds
v_mfma_f32_16x16x32_bf16 a[244:247],a[176:179],v[132:135],a[244:247]
v_mfma_f32_16x16x32_bf16 a[248:251],a[176:179],v[144:147],a[248:251]
v_mfma_f32_16x16x32_bf16 a[248:251],a[180:183],v[176:179],a[248:251]
v_mfma_f32_16x16x32_bf16 a[248:251],a[184:187],v[208:211],a[248:251]
buffer_load_dwordx4 v[104:107],v6,s[28:31],s78 offen offset:2048 sc1
v_mfma_f32_16x16x32_bf16 a[248:251],a[188:191],v[240:243],a[248:251]
v_mfma_f32_16x16x32_bf16 a[252:255],a[188:191],v[244:247],a[252:255]
ds_read_b64_tr_b16 a[188:189],v2 offset:31008
ds_read_b64_tr_b16 a[190:191],v2 offset:36128
v_mfma_f32_16x16x32_bf16 a[252:255],a[184:187],v[212:215],a[252:255]
ds_read_b64_tr_b16 a[184:185],v2 offset:20768
v_mfma_f32_16x16x32_bf16 a[252:255],a[180:183],v[180:183],a[252:255]
ds_read_b64_tr_b16 a[180:181],v2 offset:10528
v_mfma_f32_16x16x32_bf16 a[252:255],a[176:179],v[148:151],a[252:255]
ds_read_b64_tr_b16 a[176:177],v2 offset:288
v_mfma_f32_16x16x32_bf16 a[0:3],a[96:99],v[128:131],a[0:3]
ds_read_b64_tr_b16 a[178:179],v2 offset:5408
ds_read_b64_tr_b16 a[182:183],v2 offset:15648
v_mfma_f32_16x16x32_bf16 a[0:3],a[100:103],v[160:163],a[0:3]
ds_read_b64_tr_b16 a[186:187],v2 offset:25888
v_add_u32 v2,81920,v2
v_mfma_f32_16x16x32_bf16 a[0:3],a[104:107],v[192:195],a[0:3]
buffer_load_dwordx4 v[108:111],v6,s[28:31],s78 offen offset:3072 sc1
v_mfma_f32_16x16x32_bf16 a[0:3],a[108:111],v[224:227],a[0:3]
v_mfma_f32_16x16x32_bf16 a[4:7],a[108:111],v[228:231],a[4:7]
v_mfma_f32_16x16x32_bf16 a[4:7],a[104:107],v[196:199],a[4:7]
v_mfma_f32_16x16x32_bf16 a[4:7],a[100:103],v[164:167],a[4:7]
buffer_load_dwordx4 v10,s[72:75],s80 offen offset:3072 lds
v_mfma_f32_16x16x32_bf16 a[4:7],a[96:99],v[132:135],a[4:7]
v_mfma_f32_16x16x32_bf16 a[8:11],a[96:99],v[144:147],a[8:11]
v_mfma_f32_16x16x32_bf16 a[8:11],a[100:103],v[176:179],a[8:11]
v_mfma_f32_16x16x32_bf16 a[8:11],a[104:107],v[208:211],a[8:11]
buffer_load_dwordx4 v[112:115],v6,s[28:31],s79 offen sc1
v_mfma_f32_16x16x32_bf16 a[8:11],a[108:111],v[240:243],a[8:11]
v_mfma_f32_16x16x32_bf16 a[12:15],a[108:111],v[244:247],a[12:15]
ds_read_b64_tr_b16 a[108:109],v1 offset:30720
ds_read_b64_tr_b16 a[110:111],v1 offset:35840
v_mfma_f32_16x16x32_bf16 a[12:15],a[104:107],v[212:215],a[12:15]
ds_read_b64_tr_b16 a[104:105],v1 offset:20480
v_mfma_f32_16x16x32_bf16 a[12:15],a[100:103],v[180:183],a[12:15]
s_add_u32 m0,s82,4096
ds_read_b64_tr_b16 a[100:101],v1 offset:10240
v_mfma_f32_16x16x32_bf16 a[12:15],a[96:99],v[148:151],a[12:15]
ds_read_b64_tr_b16 a[96:97],v1
v_mfma_f32_16x16x32_bf16 a[16:19],a[112:115],v[128:131],a[16:19]
ds_read_b64_tr_b16 a[98:99],v1 offset:5120
ds_read_b64_tr_b16 a[102:103],v1 offset:15360
v_mfma_f32_16x16x32_bf16 a[16:19],a[116:119],v[160:163],a[16:19]
buffer_load_dwordx4 v11,s[72:75],s80 offen lds
v_mfma_f32_16x16x32_bf16 a[16:19],a[120:123],v[192:195],a[16:19]
ds_read_b64_tr_b16 a[106:107],v1 offset:25600
v_mfma_f32_16x16x32_bf16 a[16:19],a[124:127],v[224:227],a[16:19]
v_mfma_f32_16x16x32_bf16 a[20:23],a[124:127],v[228:231],a[20:23]
v_mfma_f32_16x16x32_bf16 a[20:23],a[120:123],v[196:199],a[20:23]
buffer_load_dwordx4 v[116:119],v6,s[28:31],s79 offen offset:1024 sc1
v_mfma_f32_16x16x32_bf16 a[20:23],a[116:119],v[164:167],a[20:23]
v_mfma_f32_16x16x32_bf16 a[20:23],a[112:115],v[132:135],a[20:23]
v_mfma_f32_16x16x32_bf16 a[24:27],a[112:115],v[144:147],a[24:27]
v_mfma_f32_16x16x32_bf16 a[24:27],a[116:119],v[176:179],a[24:27]
v_mfma_f32_16x16x32_bf16 a[24:27],a[120:123],v[208:211],a[24:27]
buffer_load_dwordx4 v[120:123],v6,s[28:31],s79 offen offset:2048 sc1
v_mfma_f32_16x16x32_bf16 a[24:27],a[124:127],v[240:243],a[24:27]
v_mfma_f32_16x16x32_bf16 a[28:31],a[124:127],v[244:247],a[28:31]
ds_read_b64_tr_b16 a[124:125],v2 offset:30752
ds_read_b64_tr_b16 a[126:127],v2 offset:35872
v_mfma_f32_16x16x32_bf16 a[28:31],a[120:123],v[212:215],a[28:31]
s_add_u32 m0,s82,5120
ds_read_b64_tr_b16 a[120:121],v2 offset:20512
v_mfma_f32_16x16x32_bf16 a[28:31],a[116:119],v[180:183],a[28:31]
ds_read_b64_tr_b16 a[116:117],v2 offset:10272
v_mfma_f32_16x16x32_bf16 a[28:31],a[112:115],v[148:151],a[28:31]
s_cmp_ge_u32 2,s71
s_cselect_b32 s76,6,4
s_lshl_b32 s76,s76,16
v_mfma_f32_16x16x32_bf16 a[32:35],a[128:131],v[128:131],a[32:35]
s_add_u32 s77,s76,4096
buffer_load_dwordx4 v7,s[72:75],s83 offen lds
v_mfma_f32_16x16x32_bf16 a[32:35],a[132:135],v[160:163],a[32:35]
s_add_u32 s78,s76,8192
s_max_i32 s77,s77,s19
ds_read_b64_tr_b16 a[112:113],v2 offset:32
v_mfma_f32_16x16x32_bf16 a[32:35],a[136:139],v[192:195],a[32:35]
s_max_i32 s78,s78,s100
ds_read_b64_tr_b16 a[114:115],v2 offset:5152
ds_read_b64_tr_b16 a[118:119],v2 offset:15392
v_mfma_f32_16x16x32_bf16 a[32:35],a[140:143],v[224:227],a[32:35]
ds_read_b64_tr_b16 a[122:123],v2 offset:25632
v_mfma_f32_16x16x32_bf16 a[36:39],a[140:143],v[228:231],a[36:39]
buffer_load_dwordx4 v[124:127],v6,s[28:31],s79 offen offset:3072 sc1
v_mfma_f32_16x16x32_bf16 a[36:39],a[136:139],v[196:199],a[36:39]
s_add_u32 s79,s76,12288
s_max_i32 s76,s76,s18
s_max_i32 s79,s79,s101
v_mfma_f32_16x16x32_bf16 a[36:39],a[132:135],v[164:167],a[36:39]
v_mfma_f32_16x16x32_bf16 a[36:39],a[128:131],v[132:135],a[36:39]
v_mfma_f32_16x16x32_bf16 a[40:43],a[128:131],v[144:147],a[40:43]
v_mfma_f32_16x16x32_bf16 a[40:43],a[132:135],v[176:179],a[40:43]
v_mfma_f32_16x16x32_bf16 a[40:43],a[136:139],v[208:211],a[40:43]
v_mfma_f32_16x16x32_bf16 a[40:43],a[140:143],v[240:243],a[40:43]
v_mfma_f32_16x16x32_bf16 a[44:47],a[140:143],v[244:247],a[44:47]
ds_read_b64_tr_b16 a[140:141],v1 offset:30784
ds_read_b64_tr_b16 a[142:143],v1 offset:35904
v_mfma_f32_16x16x32_bf16 a[44:47],a[136:139],v[212:215],a[44:47]
ds_read_b64_tr_b16 a[136:137],v1 offset:20544
v_mfma_f32_16x16x32_bf16 a[44:47],a[132:135],v[180:183],a[44:47]
ds_read_b64_tr_b16 a[132:133],v1 offset:10304
v_mfma_f32_16x16x32_bf16 a[44:47],a[128:131],v[148:151],a[44:47]
s_waitcnt lgkmcnt(4)
buffer_load_dwordx4 v8,s[72:75],s83 offen offset:1024 lds
v_mfma_f32_16x16x32_bf16 a[48:51],a[144:147],v[128:131],a[48:51]
ds_read_b64_tr_b16 a[128:129],v1 offset:64
ds_read_b64_tr_b16 a[130:131],v1 offset:5184
v_mfma_f32_16x16x32_bf16 a[48:51],a[148:151],v[160:163],a[48:51]
ds_read_b64_tr_b16 a[134:135],v1 offset:15424
v_mfma_f32_16x16x32_bf16 a[48:51],a[152:155],v[192:195],a[48:51]
ds_read_b64_tr_b16 a[138:139],v1 offset:25664
v_mfma_f32_16x16x32_bf16 a[48:51],a[156:159],v[224:227],a[48:51]
s_lshl_b32 s94,s70,4
v_bfe_u32 v24,v6,4,2
v_min_u32 v24,2,v24
v_mfma_f32_16x16x32_bf16 a[52:55],a[156:159],v[228:231],a[52:55]
s_add_u32 s94,s94,s40
v_add_u32 v24,s71,v24
v_add_u32 v24,1,v24
v_mfma_f32_16x16x32_bf16 a[52:55],a[152:155],v[196:199],a[52:55]
s_lshl_b32 s94,s94,4
v_and_b32 v24,3,v24
v_lshlrev_b32 v24,6,v24
v_mfma_f32_16x16x32_bf16 a[52:55],a[148:151],v[164:167],a[52:55]
v_mfma_f32_16x16x32_bf16 a[52:55],a[144:147],v[132:135],a[52:55]
buffer_load_dwordx4 v[136:139],v6,s[28:31],s76 offen sc1
v_mfma_f32_16x16x32_bf16 a[56:59],a[144:147],v[144:147],a[56:59]
v_mfma_f32_16x16x32_bf16 a[56:59],a[148:151],v[176:179],a[56:59]
v_mfma_f32_16x16x32_bf16 a[56:59],a[152:155],v[208:211],a[56:59]
v_mfma_f32_16x16x32_bf16 a[56:59],a[156:159],v[240:243],a[56:59]
v_mfma_f32_16x16x32_bf16 a[60:63],a[156:159],v[244:247],a[60:63]
ds_read_b64_tr_b16 a[156:157],v2 offset:30816
ds_read_b64_tr_b16 a[158:159],v2 offset:35936
v_mfma_f32_16x16x32_bf16 a[60:63],a[152:155],v[212:215],a[60:63]
buffer_load_dwordx4 v[140:143],v6,s[28:31],s76 offen offset:1024 sc1
v_mfma_f32_16x16x32_bf16 a[60:63],a[148:151],v[180:183],a[60:63]
ds_read_b64_tr_b16 a[148:149],v2 offset:10336
ds_read_b64_tr_b16 a[150:151],v2 offset:15456
v_mfma_f32_16x16x32_bf16 a[60:63],a[144:147],v[148:151],a[60:63]
ds_read_b64_tr_b16 a[144:145],v2 offset:96
v_mfma_f32_16x16x32_bf16 a[64:67],a[160:163],v[128:131],a[64:67]
ds_read_b64_tr_b16 a[146:147],v2 offset:5216
v_mfma_f32_16x16x32_bf16 a[64:67],a[164:167],v[160:163],a[64:67]
buffer_load_dwordx4 v9,s[72:75],s83 offen offset:2048 lds
v_mfma_f32_16x16x32_bf16 a[64:67],a[168:171],v[192:195],a[64:67]
ds_read_b64_tr_b16 a[152:153],v2 offset:20576
ds_read_b64_tr_b16 a[154:155],v2 offset:25696
v_mfma_f32_16x16x32_bf16 a[64:67],a[172:175],v[224:227],a[64:67]
v_mfma_f32_16x16x32_bf16 a[68:71],a[172:175],v[228:231],a[68:71]
v_mfma_f32_16x16x32_bf16 a[68:71],a[168:171],v[196:199],a[68:71]
buffer_load_dwordx4 v[152:155],v6,s[28:31],s76 offen offset:2048 sc1
v_mfma_f32_16x16x32_bf16 a[68:71],a[164:167],v[164:167],a[68:71]
v_mfma_f32_16x16x32_bf16 a[68:71],a[160:163],v[132:135],a[68:71]
v_mfma_f32_16x16x32_bf16 a[72:75],a[160:163],v[144:147],a[72:75]
v_mfma_f32_16x16x32_bf16 a[72:75],a[164:167],v[176:179],a[72:75]
buffer_load_dwordx4 v[156:159],v6,s[28:31],s76 offen offset:3072 sc1
v_mfma_f32_16x16x32_bf16 a[72:75],a[168:171],v[208:211],a[72:75]
v_mfma_f32_16x16x32_bf16 a[72:75],a[172:175],v[240:243],a[72:75]
v_mfma_f32_16x16x32_bf16 a[76:79],a[172:175],v[244:247],a[76:79]
ds_read_b64_tr_b16 a[172:173],v1 offset:30848
ds_read_b64_tr_b16 a[174:175],v1 offset:35968
v_mfma_f32_16x16x32_bf16 a[76:79],a[168:171],v[212:215],a[76:79]
buffer_load_dwordx4 v10,s[72:75],s83 offen offset:3072 lds
v_mfma_f32_16x16x32_bf16 a[76:79],a[164:167],v[180:183],a[76:79]
ds_read_b64_tr_b16 a[164:165],v1 offset:10368
ds_read_b64_tr_b16 a[166:167],v1 offset:15488
v_mfma_f32_16x16x32_bf16 a[76:79],a[160:163],v[148:151],a[76:79]
ds_read_b64_tr_b16 a[160:161],v1 offset:128
v_mfma_f32_16x16x32_bf16 a[80:83],a[176:179],v[128:131],a[80:83]
ds_read_b64_tr_b16 a[162:163],v1 offset:5248
v_mfma_f32_16x16x32_bf16 a[80:83],a[180:183],v[160:163],a[80:83]
buffer_load_dwordx4 v[168:171],v6,s[28:31],s77 offen sc1
v_mfma_f32_16x16x32_bf16 a[80:83],a[184:187],v[192:195],a[80:83]
ds_read_b64_tr_b16 a[168:169],v1 offset:20608
ds_read_b64_tr_b16 a[170:171],v1 offset:25728
v_mfma_f32_16x16x32_bf16 a[80:83],a[188:191],v[224:227],a[80:83]
v_mfma_f32_16x16x32_bf16 a[84:87],a[188:191],v[228:231],a[84:87]
v_mfma_f32_16x16x32_bf16 a[84:87],a[184:187],v[196:199],a[84:87]
buffer_load_dwordx4 v[172:175],v6,s[28:31],s77 offen offset:1024 sc1
v_mfma_f32_16x16x32_bf16 a[84:87],a[180:183],v[164:167],a[84:87]
v_mfma_f32_16x16x32_bf16 a[84:87],a[176:179],v[132:135],a[84:87]
v_mfma_f32_16x16x32_bf16 a[88:91],a[176:179],v[144:147],a[88:91]
v_mfma_f32_16x16x32_bf16 a[88:91],a[180:183],v[176:179],a[88:91]
s_add_u32 m0,s82,9216
v_mfma_f32_16x16x32_bf16 a[88:91],a[184:187],v[208:211],a[88:91]
v_mfma_f32_16x16x32_bf16 a[88:91],a[188:191],v[240:243],a[88:91]
buffer_load_dwordx2 v[26:27],v24,s[64:67],s94 offen offset:1024 sc1
v_mfma_f32_16x16x32_bf16 a[92:95],a[188:191],v[244:247],a[92:95]
ds_read_b64_tr_b16 a[188:189],v2 offset:30880
ds_read_b64_tr_b16 a[190:191],v2 offset:36000
v_mfma_f32_16x16x32_bf16 a[92:95],a[184:187],v[212:215],a[92:95]
ds_read_b64_tr_b16 a[184:185],v2 offset:20640
v_mfma_f32_16x16x32_bf16 a[92:95],a[180:183],v[180:183],a[92:95]
ds_read_b64_tr_b16 a[180:181],v2 offset:10400
v_mfma_f32_16x16x32_bf16 a[92:95],a[176:179],v[148:151],a[92:95]
s_waitcnt vmcnt(13)
buffer_load_dwordx4 v[184:187],v6,s[28:31],s77 offen offset:2048 sc1
v_mfma_f32_16x16x32_bf16 a[192:195],a[96:99],v[64:67],a[192:195]
s_lshl_b32 s43,s41,2
ds_read_b64_tr_b16 a[176:177],v2 offset:160
ds_read_b64_tr_b16 a[178:179],v2 offset:5280
v_mfma_f32_16x16x32_bf16 a[192:195],a[100:103],v[68:71],a[192:195]
s_add_u32 s84,s70,1
s_add_u32 s85,s70,2
ds_read_b64_tr_b16 a[182:183],v2 offset:15520
v_mfma_f32_16x16x32_bf16 a[192:195],a[104:107],v[72:75],a[192:195]
s_add_u32 s86,s70,3
s_add_u32 s43,s43,s71
ds_read_b64_tr_b16 a[186:187],v2 offset:25760
v_mfma_f32_16x16x32_bf16 a[192:195],a[108:111],v[76:79],a[192:195]
s_and_b32 s84,s84,3
buffer_load_dwordx4 v11,s[72:75],s83 offen lds
v_mfma_f32_16x16x32_bf16 a[196:199],a[108:111],v[92:95],a[196:199]
)A"
R"A(s_and_b32 s85,s85,3
s_and_b32 s86,s86,3
s_mul_i32 s43,s43,245760
v_mfma_f32_16x16x32_bf16 a[196:199],a[104:107],v[88:91],a[196:199]
s_mul_i32 s84,s84,12
s_mul_i32 s85,s85,12
s_mul_i32 s86,s86,12
v_mfma_f32_16x16x32_bf16 a[196:199],a[100:103],v[84:87],a[196:199]
s_add_u32 s43,s43,134217728
s_mov_b64 s[32:33],s[12:13]
s_add_u32 s84,s84,0
v_mfma_f32_16x16x32_bf16 a[196:199],a[96:99],v[80:83],a[196:199]
s_add_u32 s85,s85,4
buffer_load_dwordx4 v[188:191],v6,s[28:31],s77 offen offset:3072 sc1
v_mfma_f32_16x16x32_bf16 a[200:203],a[96:99],v[96:99],a[200:203]
s_add_u32 s86,s86,8
s_add_u32 s84,s84,s40
s_add_u32 s85,s85,s40
v_mfma_f32_16x16x32_bf16 a[200:203],a[100:103],v[100:103],a[200:203]
s_add_u32 s86,s86,s40
s_add_u32 s32,s32,s43
s_addc_u32 s33,s33,0
v_mfma_f32_16x16x32_bf16 a[200:203],a[104:107],v[104:107],a[200:203]
s_mul_i32 s84,s84,5120
s_mul_i32 s85,s85,5120
s_mul_i32 s86,s86,5120
v_mfma_f32_16x16x32_bf16 a[200:203],a[108:111],v[108:111],a[200:203]
s_and_b32 s33,s33,0xffff
buffer_load_dwordx4 v[200:203],v6,s[28:31],s78 offen sc1
s_waitcnt vmcnt(14)
v_mfma_f32_16x16x32_bf16 a[204:207],a[108:111],v[124:127],a[204:207]
s_mov_b32 s34,245760
ds_read_b64_tr_b16 a[108:109],v1 offset:30912
ds_read_b64_tr_b16 a[110:111],v1 offset:36032
v_mfma_f32_16x16x32_bf16 a[204:207],a[104:107],v[120:123],a[204:207]
s_mov_b32 s35,0x20000
s_max_i32 s84,s84,s18
ds_read_b64_tr_b16 a[104:105],v1 offset:20672
v_mfma_f32_16x16x32_bf16 a[204:207],a[100:103],v[116:119],a[204:207]
s_max_i32 s85,s85,s19
s_max_i32 s86,s86,s100
ds_read_b64_tr_b16 a[100:101],v1 offset:10432
v_mfma_f32_16x16x32_bf16 a[204:207],a[96:99],v[112:115],a[204:207]
s_barrier
buffer_load_dwordx4 v[204:207],v6,s[28:31],s78 offen offset:1024 sc1
v_mfma_f32_16x16x32_bf16 a[208:211],a[112:115],v[64:67],a[208:211]
s_mul_i32 s44,s41,307200
ds_read_b64_tr_b16 a[96:97],v1 offset:192
ds_read_b64_tr_b16 a[98:99],v1 offset:5312
v_mfma_f32_16x16x32_bf16 a[208:211],a[116:119],v[68:71],a[208:211]
s_mov_b64 s[20:21],s[16:17]
ds_read_b64_tr_b16 a[102:103],v1 offset:15552
v_bfe_u32 v18,v6,6,2
v_mfma_f32_16x16x32_bf16 a[208:211],a[120:123],v[72:75],a[208:211]
s_lshl_b32 s43,s40,2
ds_read_b64_tr_b16 a[106:107],v1 offset:25792
v_lshrrev_b32 v19,8,v6
v_mfma_f32_16x16x32_bf16 a[208:211],a[124:127],v[76:79],a[208:211]
s_add_u32 s43,s43,s70
buffer_load_dwordx4 v[216:219],v6,s[28:31],s78 offen offset:2048 sc1
v_mfma_f32_16x16x32_bf16 a[212:215],a[124:127],v[92:95],a[212:215]
s_mul_i32 s43,s43,20480
v_xor_b32 v18,v18,v19
v_lshlrev_b32 v18,2,v18
v_mfma_f32_16x16x32_bf16 a[212:215],a[120:123],v[88:91],a[212:215]
s_mov_b32 s22,307200
v_bfe_u32 v19,v6,4,2
v_or_b32 v18,v18,v19
v_mfma_f32_16x16x32_bf16 a[212:215],a[116:119],v[84:87],a[212:215]
s_mov_b32 s23,0x20000
v_and_b32 v19,16,v6
v_mov_b32 v34,0x3f80
v_mfma_f32_16x16x32_bf16 a[212:215],a[112:115],v[80:83],a[212:215]
s_cmp_ge_u32 0,s71
s_cselect_b32 s76,3,1
s_lshl_b32 s76,s76,16
v_mfma_f32_16x16x32_bf16 a[216:219],a[112:115],v[96:99],a[216:219]
s_add_u32 s77,s76,4096
buffer_load_dwordx4 v[220:223],v6,s[28:31],s78 offen offset:3072 sc1
v_mfma_f32_16x16x32_bf16 a[216:219],a[116:119],v[100:103],a[216:219]
s_add_u32 s78,s76,8192
s_max_i32 s77,s77,s19
s_max_i32 s78,s78,s100
v_mfma_f32_16x16x32_bf16 a[216:219],a[120:123],v[104:107],a[216:219]
v_lshlrev_b32 v19,v19,v34
v_lshrrev_b32 v18,1,v18
v_mfma_f32_16x16x32_bf16 a[216:219],a[124:127],v[108:111],a[216:219]
v_cmp_eq_u32 vcc,0,v18
s_nop 1
v_cndmask_b32 v34,0,v19,vcc
v_mfma_f32_16x16x32_bf16 a[220:223],a[124:127],v[124:127],a[220:223]
ds_read_b64_tr_b16 a[124:125],v2 offset:30944
ds_read_b64_tr_b16 a[126:127],v2 offset:36064
v_mfma_f32_16x16x32_bf16 a[220:223],a[120:123],v[120:123],a[220:223]
ds_read_b64_tr_b16 a[120:121],v2 offset:20704
v_add_u32 v25,4096,v6
v_mfma_f32_16x16x32_bf16 a[220:223],a[116:119],v[116:119],a[220:223]
buffer_load_dwordx4 v[232:235],v6,s[28:31],s79 offen sc1
v_mfma_f32_16x16x32_bf16 a[220:223],a[112:115],v[112:115],a[220:223]
s_waitcnt lgkmcnt(3)
ds_read_b64_tr_b16 a[112:113],v2 offset:224
ds_read_b64_tr_b16 a[114:115],v2 offset:5344
v_mfma_f32_16x16x32_bf16 a[224:227],a[128:131],v[64:67],a[224:227]
ds_read_b64_tr_b16 a[116:117],v2 offset:10464
v_mov_b32 v36,0
v_mfma_f32_16x16x32_bf16 a[224:227],a[132:135],v[68:71],a[224:227]
ds_read_b64_tr_b16 a[118:119],v2 offset:15584
v_mov_b32 v37,0
v_mfma_f32_16x16x32_bf16 a[224:227],a[136:139],v[72:75],a[224:227]
ds_read_b64_tr_b16 a[122:123],v2 offset:25824
v_mov_b32 v32,0
v_mfma_f32_16x16x32_bf16 a[224:227],a[140:143],v[76:79],a[224:227]
v_cmp_eq_u32 vcc,1,v18
s_nop 1
v_cndmask_b32 v35,0,v19,vcc
v_mfma_f32_16x16x32_bf16 a[228:231],a[140:143],v[92:95],a[228:231]
buffer_load_dwordx4 v[236:239],v6,s[28:31],s79 offen offset:1024 sc1
v_mfma_f32_16x16x32_bf16 a[228:231],a[136:139],v[88:91],a[228:231]
s_add_u32 s20,s20,s44
s_addc_u32 s21,s21,0
v_mov_b32 v33,0
v_mfma_f32_16x16x32_bf16 a[228:231],a[132:135],v[84:87],a[228:231]
s_mul_i32 s44,s71,320
v_bfe_u32 v40,v6,4,4
v_mul_u32_u24 v40,1280,v40
v_mfma_f32_16x16x32_bf16 a[228:231],a[128:131],v[80:83],a[228:231]
s_waitcnt vmcnt(9)
v_cmp_ne_u32 s[90:91],s68,v26
v_cmp_ne_u32 s[92:93],s69,v27
s_nop 0
s_or_b64 s[90:91],s[90:91],s[92:93]
s_cbranch_scc1 L_fr9_%=
L_fo9_%=:
v_mfma_f32_16x16x32_bf16 a[232:235],a[128:131],v[96:99],a[232:235]
s_and_b32 s21,s21,0xffff
v_lshrrev_b32 v41,8,v6
v_lshl_add_u32 v40,v41,3,v40
v_mfma_f32_16x16x32_bf16 a[232:235],a[132:135],v[100:103],a[232:235]
s_add_u32 s43,s43,s44
v_mfma_f32_16x16x32_bf16 a[232:235],a[136:139],v[104:107],a[232:235]
buffer_load_dwordx4 v[248:251],v6,s[28:31],s79 offen offset:2048 sc1
v_mfma_f32_16x16x32_bf16 a[232:235],a[140:143],v[108:111],a[232:235]
v_mfma_f32_16x16x32_bf16 a[236:239],a[140:143],v[124:127],a[236:239]
ds_read_b64_tr_b16 a[140:141],v1 offset:30976
ds_read_b64_tr_b16 a[142:143],v1 offset:36096
v_mfma_f32_16x16x32_bf16 a[236:239],a[136:139],v[120:123],a[236:239]
ds_read_b64_tr_b16 a[136:137],v1 offset:20736
v_mfma_f32_16x16x32_bf16 a[236:239],a[132:135],v[116:119],a[236:239]
ds_read_b64_tr_b16 a[132:133],v1 offset:10496
v_mfma_f32_16x16x32_bf16 a[236:239],a[128:131],v[112:115],a[236:239]
ds_read_b64_tr_b16 a[128:129],v1 offset:256
v_mfma_f32_16x16x32_bf16 a[240:243],a[144:147],v[64:67],a[240:243]
buffer_load_dwordx4 v[252:255],v6,s[28:31],s79 offen offset:3072 sc1
v_mfma_f32_16x16x32_bf16 a[240:243],a[148:151],v[68:71],a[240:243]
s_add_u32 s79,s76,12288
s_max_i32 s76,s76,s18
ds_read_b64_tr_b16 a[130:131],v1 offset:5376
v_mfma_f32_16x16x32_bf16 a[240:243],a[152:155],v[72:75],a[240:243]
s_max_i32 s79,s79,s101
ds_read_b64_tr_b16 a[134:135],v1 offset:15616
ds_read_b64_tr_b16 a[138:139],v1 offset:25856
v_mfma_f32_16x16x32_bf16 a[240:243],a[156:159],v[76:79],a[240:243]
v_subrev_u32 v1,40960,v1
v_mfma_f32_16x16x32_bf16 a[244:247],a[156:159],v[92:95],a[244:247]
buffer_load_dwordx4 v[128:131],v6,s[28:31],s76 offen sc1
v_mfma_f32_16x16x32_bf16 a[244:247],a[152:155],v[88:91],a[244:247]
v_mfma_f32_16x16x32_bf16 a[244:247],a[148:151],v[84:87],a[244:247]
v_mfma_f32_16x16x32_bf16 a[244:247],a[144:147],v[80:83],a[244:247]
v_mfma_f32_16x16x32_bf16 a[248:251],a[144:147],v[96:99],a[248:251]
buffer_load_dwordx4 v[160:163],v6,s[28:31],s76 offen offset:1024 sc1
v_mfma_f32_16x16x32_bf16 a[248:251],a[148:151],v[100:103],a[248:251]
v_mfma_f32_16x16x32_bf16 a[248:251],a[152:155],v[104:107],a[248:251]
v_mfma_f32_16x16x32_bf16 a[248:251],a[156:159],v[108:111],a[248:251]
v_mfma_f32_16x16x32_bf16 a[252:255],a[156:159],v[124:127],a[252:255]
buffer_load_dwordx4 v[192:195],v6,s[28:31],s76 offen offset:2048 sc1
v_mfma_f32_16x16x32_bf16 a[252:255],a[152:155],v[120:123],a[252:255]
ds_read_b64_tr_b16 a[152:153],v2 offset:20768
ds_read_b64_tr_b16 a[154:155],v2 offset:25888
v_mfma_f32_16x16x32_bf16 a[252:255],a[148:151],v[116:119],a[252:255]
ds_read_b64_tr_b16 a[148:149],v2 offset:10528
v_mfma_f32_16x16x32_bf16 a[252:255],a[144:147],v[112:115],a[252:255]
ds_read_b64_tr_b16 a[144:145],v2 offset:288
v_mfma_f32_16x16x32_bf16 a[0:3],a[160:163],v[64:67],a[0:3]
ds_read_b64_tr_b16 a[146:147],v2 offset:5408
v_mfma_f32_16x16x32_bf16 a[0:3],a[164:167],v[68:71],a[0:3]
buffer_load_dwordx4 v[224:227],v6,s[28:31],s76 offen offset:3072 sc1
v_mfma_f32_16x16x32_bf16 a[0:3],a[168:171],v[72:75],a[0:3]
ds_read_b64_tr_b16 a[150:151],v2 offset:15648
ds_read_b64_tr_b16 a[156:157],v2 offset:31008
v_mfma_f32_16x16x32_bf16 a[0:3],a[172:175],v[76:79],a[0:3]
ds_read_b64_tr_b16 a[158:159],v2 offset:36128
v_subrev_u32 v2,40960,v2
v_mfma_f32_16x16x32_bf16 a[4:7],a[172:175],v[92:95],a[4:7]
v_mfma_f32_16x16x32_bf16 a[4:7],a[168:171],v[88:91],a[4:7]
v_mfma_f32_16x16x32_bf16 a[4:7],a[164:167],v[84:87],a[4:7]
v_mfma_f32_16x16x32_bf16 a[4:7],a[160:163],v[80:83],a[4:7]
buffer_load_dwordx4 v[132:135],v6,s[28:31],s77 offen sc1
v_mfma_f32_16x16x32_bf16 a[8:11],a[160:163],v[96:99],a[8:11]
v_mfma_f32_16x16x32_bf16 a[8:11],a[164:167],v[100:103],a[8:11]
v_mfma_f32_16x16x32_bf16 a[8:11],a[168:171],v[104:107],a[8:11]
v_mfma_f32_16x16x32_bf16 a[8:11],a[172:175],v[108:111],a[8:11]
v_mfma_f32_16x16x32_bf16 a[12:15],a[172:175],v[124:127],a[12:15]
ds_read_b64_tr_b16 a[172:173],v1 offset:30720
ds_read_b64_tr_b16 a[174:175],v1 offset:35840
v_mfma_f32_16x16x32_bf16 a[12:15],a[168:171],v[120:123],a[12:15]
buffer_load_dwordx4 v[164:167],v6,s[28:31],s77 offen offset:1024 sc1
v_mfma_f32_16x16x32_bf16 a[12:15],a[164:167],v[116:119],a[12:15]
ds_read_b64_tr_b16 a[164:165],v1 offset:10240
ds_read_b64_tr_b16 a[166:167],v1 offset:15360
v_mfma_f32_16x16x32_bf16 a[12:15],a[160:163],v[112:115],a[12:15]
ds_read_b64_tr_b16 a[160:161],v1
v_mfma_f32_16x16x32_bf16 a[16:19],a[176:179],v[64:67],a[16:19]
ds_read_b64_tr_b16 a[162:163],v1 offset:5120
v_mfma_f32_16x16x32_bf16 a[16:19],a[180:183],v[68:71],a[16:19]
ds_read_b64_tr_b16 a[168:169],v1 offset:20480
v_mfma_f32_16x16x32_bf16 a[16:19],a[184:187],v[72:75],a[16:19]
ds_read_b64_tr_b16 a[170:171],v1 offset:25600
v_mfma_f32_16x16x32_bf16 a[16:19],a[188:191],v[76:79],a[16:19]
buffer_load_dwordx4 v[196:199],v6,s[28:31],s77 offen offset:2048 sc1
v_mfma_f32_16x16x32_bf16 a[20:23],a[188:191],v[92:95],a[20:23]
v_mfma_f32_16x16x32_bf16 a[20:23],a[184:187],v[88:91],a[20:23]
v_mfma_f32_16x16x32_bf16 a[20:23],a[180:183],v[84:87],a[20:23]
v_mfma_f32_16x16x32_bf16 a[20:23],a[176:179],v[80:83],a[20:23]
v_mfma_f32_16x16x32_bf16 a[24:27],a[176:179],v[96:99],a[24:27]
v_mfma_f32_16x16x32_bf16 a[24:27],a[180:183],v[100:103],a[24:27]
buffer_load_dwordx4 v[228:231],v6,s[28:31],s77 offen offset:3072 sc1
v_mfma_f32_16x16x32_bf16 a[24:27],a[184:187],v[104:107],a[24:27]
v_mfma_f32_16x16x32_bf16 a[24:27],a[188:191],v[108:111],a[24:27]
v_mfma_f32_16x16x32_bf16 a[28:31],a[188:191],v[124:127],a[28:31]
ds_read_b64_tr_b16 a[188:189],v2 offset:30752
ds_read_b64_tr_b16 a[190:191],v2 offset:35872
v_mfma_f32_16x16x32_bf16 a[28:31],a[184:187],v[120:123],a[28:31]
ds_read_b64_tr_b16 a[184:185],v2 offset:20512
v_mfma_f32_16x16x32_bf16 a[28:31],a[180:183],v[116:119],a[28:31]
ds_read_b64_tr_b16 a[180:181],v2 offset:10272
v_mfma_f32_16x16x32_bf16 a[28:31],a[176:179],v[112:115],a[28:31]
buffer_load_dwordx4 v[144:147],v6,s[28:31],s78 offen sc1
v_mfma_f32_16x16x32_bf16 a[32:35],a[96:99],v[64:67],a[32:35]
ds_read_b64_tr_b16 a[176:177],v2 offset:32
ds_read_b64_tr_b16 a[178:179],v2 offset:5152
v_mfma_f32_16x16x32_bf16 a[32:35],a[100:103],v[68:71],a[32:35]
ds_read_b64_tr_b16 a[182:183],v2 offset:15392
v_mfma_f32_16x16x32_bf16 a[32:35],a[104:107],v[72:75],a[32:35]
ds_read_b64_tr_b16 a[186:187],v2 offset:25632
v_mfma_f32_16x16x32_bf16 a[32:35],a[108:111],v[76:79],a[32:35]
v_mfma_f32_16x16x32_bf16 a[36:39],a[108:111],v[92:95],a[36:39]
v_mfma_f32_16x16x32_bf16 a[36:39],a[104:107],v[88:91],a[36:39]
buffer_load_dwordx4 v[176:179],v6,s[28:31],s78 offen offset:1024 sc1
v_mfma_f32_16x16x32_bf16 a[36:39],a[100:103],v[84:87],a[36:39]
v_mfma_f32_16x16x32_bf16 a[36:39],a[96:99],v[80:83],a[36:39]
v_mfma_f32_16x16x32_bf16 a[40:43],a[96:99],v[96:99],a[40:43]
v_mfma_f32_16x16x32_bf16 a[40:43],a[100:103],v[100:103],a[40:43]
v_mfma_f32_16x16x32_bf16 a[40:43],a[104:107],v[104:107],a[40:43]
v_mfma_f32_16x16x32_bf16 a[40:43],a[108:111],v[108:111],a[40:43]
)A"
R"A(buffer_load_dwordx4 v[208:211],v6,s[28:31],s78 offen offset:2048 sc1
v_mfma_f32_16x16x32_bf16 a[44:47],a[108:111],v[124:127],a[44:47]
ds_read_b64_tr_b16 a[108:109],v1 offset:30784
ds_read_b64_tr_b16 a[110:111],v1 offset:35904
v_mfma_f32_16x16x32_bf16 a[44:47],a[104:107],v[120:123],a[44:47]
ds_read_b64_tr_b16 a[104:105],v1 offset:20544
v_mfma_f32_16x16x32_bf16 a[44:47],a[100:103],v[116:119],a[44:47]
ds_read_b64_tr_b16 a[100:101],v1 offset:10304
v_mfma_f32_16x16x32_bf16 a[44:47],a[96:99],v[112:115],a[44:47]
s_waitcnt lgkmcnt(4)
ds_read_b64_tr_b16 a[96:97],v1 offset:64
v_mfma_f32_16x16x32_bf16 a[48:51],a[112:115],v[64:67],a[48:51]
ds_read_b64_tr_b16 a[98:99],v1 offset:5184
ds_read_b64_tr_b16 a[102:103],v1 offset:15424
v_mfma_f32_16x16x32_bf16 a[48:51],a[116:119],v[68:71],a[48:51]
buffer_load_dwordx4 v[240:243],v6,s[28:31],s78 offen offset:3072 sc1
v_mfma_f32_16x16x32_bf16 a[48:51],a[120:123],v[72:75],a[48:51]
ds_read_b64_tr_b16 a[106:107],v1 offset:25664
v_mfma_f32_16x16x32_bf16 a[48:51],a[124:127],v[76:79],a[48:51]
v_mfma_f32_16x16x32_bf16 a[52:55],a[124:127],v[92:95],a[52:55]
v_mfma_f32_16x16x32_bf16 a[52:55],a[120:123],v[88:91],a[52:55]
v_mfma_f32_16x16x32_bf16 a[52:55],a[116:119],v[84:87],a[52:55]
v_mfma_f32_16x16x32_bf16 a[52:55],a[112:115],v[80:83],a[52:55]
buffer_load_dwordx4 v[148:151],v6,s[28:31],s79 offen sc1
v_mfma_f32_16x16x32_bf16 a[56:59],a[112:115],v[96:99],a[56:59]
v_mfma_f32_16x16x32_bf16 a[56:59],a[116:119],v[100:103],a[56:59]
s_cmp_ge_u32 1,s71
s_cselect_b32 s80,5,3
s_mul_i32 s80,s80,163840
v_mfma_f32_16x16x32_bf16 a[56:59],a[120:123],v[104:107],a[56:59]
s_add_u32 s80,s80,s81
s_add_u32 s83,s80,20480
v_mfma_f32_16x16x32_bf16 a[56:59],a[124:127],v[108:111],a[56:59]
v_mfma_f32_16x16x32_bf16 a[60:63],a[124:127],v[124:127],a[60:63]
ds_read_b64_tr_b16 a[124:125],v2 offset:30816
ds_read_b64_tr_b16 a[126:127],v2 offset:35936
v_mfma_f32_16x16x32_bf16 a[60:63],a[120:123],v[120:123],a[60:63]
buffer_load_dwordx4 v[180:183],v6,s[28:31],s79 offen offset:1024 sc1
v_mfma_f32_16x16x32_bf16 a[60:63],a[116:119],v[116:119],a[60:63]
ds_read_b64_tr_b16 a[116:117],v2 offset:10336
ds_read_b64_tr_b16 a[118:119],v2 offset:15456
v_mfma_f32_16x16x32_bf16 a[60:63],a[112:115],v[112:115],a[60:63]
ds_read_b64_tr_b16 a[112:113],v2 offset:96
v_mfma_f32_16x16x32_bf16 a[64:67],a[128:131],v[64:67],a[64:67]
ds_read_b64_tr_b16 a[114:115],v2 offset:5216
v_mfma_f32_16x16x32_bf16 a[64:67],a[132:135],v[68:71],a[64:67]
ds_read_b64_tr_b16 a[120:121],v2 offset:20576
v_mfma_f32_16x16x32_bf16 a[64:67],a[136:139],v[72:75],a[64:67]
ds_read_b64_tr_b16 a[122:123],v2 offset:25696
v_mfma_f32_16x16x32_bf16 a[64:67],a[140:143],v[76:79],a[64:67]
buffer_load_dwordx4 v[212:215],v6,s[28:31],s79 offen offset:2048 sc1
v_mfma_f32_16x16x32_bf16 a[68:71],a[140:143],v[92:95],a[68:71]
v_mfma_f32_16x16x32_bf16 a[68:71],a[136:139],v[88:91],a[68:71]
v_mfma_f32_16x16x32_bf16 a[68:71],a[132:135],v[84:87],a[68:71]
v_mfma_f32_16x16x32_bf16 a[68:71],a[128:131],v[80:83],a[68:71]
v_mfma_f32_16x16x32_bf16 a[72:75],a[128:131],v[96:99],a[72:75]
v_mfma_f32_16x16x32_bf16 a[72:75],a[132:135],v[100:103],a[72:75]
buffer_load_dwordx4 v[244:247],v6,s[28:31],s79 offen offset:3072 sc1
v_mfma_f32_16x16x32_bf16 a[72:75],a[136:139],v[104:107],a[72:75]
v_mfma_f32_16x16x32_bf16 a[72:75],a[140:143],v[108:111],a[72:75]
v_mfma_f32_16x16x32_bf16 a[76:79],a[140:143],v[124:127],a[76:79]
ds_read_b64_tr_b16 a[140:141],v1 offset:30848
ds_read_b64_tr_b16 a[142:143],v1 offset:35968
v_mfma_f32_16x16x32_bf16 a[76:79],a[136:139],v[120:123],a[76:79]
ds_read_b64_tr_b16 a[136:137],v1 offset:20608
v_mfma_f32_16x16x32_bf16 a[76:79],a[132:135],v[116:119],a[76:79]
s_add_u32 m0,s82,40960
ds_read_b64_tr_b16 a[132:133],v1 offset:10368
v_mfma_f32_16x16x32_bf16 a[76:79],a[128:131],v[112:115],a[76:79]
ds_read_b64_tr_b16 a[128:129],v1 offset:128
v_mfma_f32_16x16x32_bf16 a[80:83],a[144:147],v[64:67],a[80:83]
ds_read_b64_tr_b16 a[130:131],v1 offset:5248
ds_read_b64_tr_b16 a[134:135],v1 offset:15488
v_mfma_f32_16x16x32_bf16 a[80:83],a[148:151],v[68:71],a[80:83]
buffer_load_dwordx4 v7,s[72:75],s80 offen lds
v_mfma_f32_16x16x32_bf16 a[80:83],a[152:155],v[72:75],a[80:83]
ds_read_b64_tr_b16 a[138:139],v1 offset:25728
v_mfma_f32_16x16x32_bf16 a[80:83],a[156:159],v[76:79],a[80:83]
v_mfma_f32_16x16x32_bf16 a[84:87],a[156:159],v[92:95],a[84:87]
v_mfma_f32_16x16x32_bf16 a[84:87],a[152:155],v[88:91],a[84:87]
v_mfma_f32_16x16x32_bf16 a[84:87],a[148:151],v[84:87],a[84:87]
buffer_load_dwordx4 v8,s[72:75],s80 offen offset:1024 lds
v_mfma_f32_16x16x32_bf16 a[84:87],a[144:147],v[80:83],a[84:87]
v_mfma_f32_16x16x32_bf16 a[88:91],a[144:147],v[96:99],a[88:91]
v_mfma_f32_16x16x32_bf16 a[88:91],a[148:151],v[100:103],a[88:91]
v_mfma_f32_16x16x32_bf16 a[88:91],a[152:155],v[104:107],a[88:91]
v_mfma_f32_16x16x32_bf16 a[88:91],a[156:159],v[108:111],a[88:91]
buffer_load_dwordx4 v9,s[72:75],s80 offen offset:2048 lds
v_mfma_f32_16x16x32_bf16 a[92:95],a[156:159],v[124:127],a[92:95]
ds_read_b64_tr_b16 a[156:157],v2 offset:30880
ds_read_b64_tr_b16 a[158:159],v2 offset:36000
v_mfma_f32_16x16x32_bf16 a[92:95],a[152:155],v[120:123],a[92:95]
ds_read_b64_tr_b16 a[152:153],v2 offset:20640
v_mfma_f32_16x16x32_bf16 a[92:95],a[148:151],v[116:119],a[92:95]
ds_read_b64_tr_b16 a[148:149],v2 offset:10400
v_mfma_f32_16x16x32_bf16 a[92:95],a[144:147],v[112:115],a[92:95]
ds_read_b64_tr_b16 a[144:145],v2 offset:160
v_mfma_f32_16x16x32_bf16 a[192:195],a[160:163],v[136:139],a[192:195]
buffer_load_dwordx4 v10,s[72:75],s80 offen offset:3072 lds
v_mfma_f32_16x16x32_bf16 a[192:195],a[164:167],v[140:143],a[192:195]
ds_read_b64_tr_b16 a[146:147],v2 offset:5280
ds_read_b64_tr_b16 a[150:151],v2 offset:15520
v_mfma_f32_16x16x32_bf16 a[192:195],a[168:171],v[152:155],a[192:195]
s_add_u32 m0,s82,45056
ds_read_b64_tr_b16 a[154:155],v2 offset:25760
v_mfma_f32_16x16x32_bf16 a[192:195],a[172:175],v[156:159],a[192:195]
s_waitcnt vmcnt(12)
v_mfma_f32_16x16x32_bf16 a[196:199],a[172:175],v[188:191],a[196:199]
v_mfma_f32_16x16x32_bf16 a[196:199],a[168:171],v[184:187],a[196:199]
buffer_load_dwordx4 v11,s[72:75],s80 offen lds
v_mfma_f32_16x16x32_bf16 a[196:199],a[164:167],v[172:175],a[196:199]
v_mfma_f32_16x16x32_bf16 a[196:199],a[160:163],v[168:171],a[196:199]
s_add_u32 m0,s82,46080
v_mfma_f32_16x16x32_bf16 a[200:203],a[160:163],v[200:203],a[200:203]
v_mfma_f32_16x16x32_bf16 a[200:203],a[164:167],v[204:207],a[200:203]
v_mfma_f32_16x16x32_bf16 a[200:203],a[168:171],v[216:219],a[200:203]
buffer_load_dwordx4 v7,s[72:75],s83 offen lds
v_mfma_f32_16x16x32_bf16 a[200:203],a[172:175],v[220:223],a[200:203]
v_mfma_f32_16x16x32_bf16 a[204:207],a[172:175],v[252:255],a[204:207]
ds_read_b64_tr_b16 a[172:173],v1 offset:30912
ds_read_b64_tr_b16 a[174:175],v1 offset:36032
v_mfma_f32_16x16x32_bf16 a[204:207],a[168:171],v[248:251],a[204:207]
ds_read_b64_tr_b16 a[168:169],v1 offset:20672
v_mfma_f32_16x16x32_bf16 a[204:207],a[164:167],v[236:239],a[204:207]
ds_read_b64_tr_b16 a[164:165],v1 offset:10432
v_mfma_f32_16x16x32_bf16 a[204:207],a[160:163],v[232:235],a[204:207]
s_barrier
buffer_load_dwordx4 v8,s[72:75],s83 offen offset:1024 lds
v_mfma_f32_16x16x32_bf16 a[208:211],a[176:179],v[136:139],a[208:211]
s_cmp_ge_u32 1,s71
s_cselect_b32 s76,5,3
s_lshl_b32 s76,s76,16
v_mfma_f32_16x16x32_bf16 a[208:211],a[180:183],v[140:143],a[208:211]
s_add_u32 s77,s76,4096
s_add_u32 s78,s76,8192
s_add_u32 s79,s76,12288
v_mfma_f32_16x16x32_bf16 a[208:211],a[184:187],v[152:155],a[208:211]
s_max_i32 s76,s76,s18
s_max_i32 s77,s77,s19
s_max_i32 s78,s78,s100
v_mfma_f32_16x16x32_bf16 a[208:211],a[188:191],v[156:159],a[208:211]
s_max_i32 s79,s79,s101
buffer_load_dwordx4 v9,s[72:75],s83 offen offset:2048 lds
v_mfma_f32_16x16x32_bf16 a[212:215],a[188:191],v[188:191],a[212:215]
ds_read_b64_tr_b16 a[160:161],v1 offset:192
ds_read_b64_tr_b16 a[162:163],v1 offset:5312
v_mfma_f32_16x16x32_bf16 a[212:215],a[184:187],v[184:187],a[212:215]
ds_read_b64_tr_b16 a[166:167],v1 offset:15552
v_mfma_f32_16x16x32_bf16 a[212:215],a[180:183],v[172:175],a[212:215]
ds_read_b64_tr_b16 a[170:171],v1 offset:25792
v_mfma_f32_16x16x32_bf16 a[212:215],a[176:179],v[168:171],a[212:215]
v_mfma_f32_16x16x32_bf16 a[216:219],a[176:179],v[200:203],a[216:219]
buffer_load_dwordx4 v10,s[72:75],s83 offen offset:3072 lds
v_mfma_f32_16x16x32_bf16 a[216:219],a[180:183],v[204:207],a[216:219]
v_mfma_f32_16x16x32_bf16 a[216:219],a[184:187],v[216:219],a[216:219]
s_add_u32 m0,s82,50176
v_mfma_f32_16x16x32_bf16 a[216:219],a[188:191],v[220:223],a[216:219]
v_mfma_f32_16x16x32_bf16 a[220:223],a[188:191],v[252:255],a[220:223]
ds_read_b64_tr_b16 a[188:189],v2 offset:30944
ds_read_b64_tr_b16 a[190:191],v2 offset:36064
v_mfma_f32_16x16x32_bf16 a[220:223],a[184:187],v[248:251],a[220:223]
buffer_load_dwordx4 v11,s[72:75],s83 offen lds
v_mfma_f32_16x16x32_bf16 a[220:223],a[180:183],v[236:239],a[220:223]
ds_read_b64_tr_b16 a[180:181],v2 offset:10464
ds_read_b64_tr_b16 a[182:183],v2 offset:15584
v_mfma_f32_16x16x32_bf16 a[220:223],a[176:179],v[232:235],a[220:223]
s_waitcnt lgkmcnt(4)
ds_read_b64_tr_b16 a[176:177],v2 offset:224
v_mfma_f32_16x16x32_bf16 a[224:227],a[96:99],v[136:139],a[224:227]
ds_read_b64_tr_b16 a[178:179],v2 offset:5344
v_mfma_f32_16x16x32_bf16 a[224:227],a[100:103],v[140:143],a[224:227]
ds_read_b64_tr_b16 a[184:185],v2 offset:20704
v_mfma_f32_16x16x32_bf16 a[224:227],a[104:107],v[152:155],a[224:227]
ds_read_b64_tr_b16 a[186:187],v2 offset:25824
v_mfma_f32_16x16x32_bf16 a[224:227],a[108:111],v[156:159],a[224:227]
v_mfma_f32_16x16x32_bf16 a[228:231],a[108:111],v[188:191],a[228:231]
v_mfma_f32_16x16x32_bf16 a[228:231],a[104:107],v[184:187],a[228:231]
v_mfma_f32_16x16x32_bf16 a[228:231],a[100:103],v[172:175],a[228:231]
v_mfma_f32_16x16x32_bf16 a[228:231],a[96:99],v[168:171],a[228:231]
buffer_load_dwordx4 v[64:67],v6,s[28:31],s76 offen sc1
v_mfma_f32_16x16x32_bf16 a[232:235],a[96:99],v[200:203],a[232:235]
v_mfma_f32_16x16x32_bf16 a[232:235],a[100:103],v[204:207],a[232:235]
v_mfma_f32_16x16x32_bf16 a[232:235],a[104:107],v[216:219],a[232:235]
v_mfma_f32_16x16x32_bf16 a[232:235],a[108:111],v[220:223],a[232:235]
v_mfma_f32_16x16x32_bf16 a[236:239],a[108:111],v[252:255],a[236:239]
buffer_load_dwordx4 v[68:71],v6,s[28:31],s76 offen offset:1024 sc1
v_mfma_f32_16x16x32_bf16 a[236:239],a[104:107],v[248:251],a[236:239]
ds_read_b64_tr_b16 a[104:105],v1 offset:20736
ds_read_b64_tr_b16 a[106:107],v1 offset:25856
v_mfma_f32_16x16x32_bf16 a[236:239],a[100:103],v[236:239],a[236:239]
ds_read_b64_tr_b16 a[100:101],v1 offset:10496
v_mfma_f32_16x16x32_bf16 a[236:239],a[96:99],v[232:235],a[236:239]
ds_read_b64_tr_b16 a[96:97],v1 offset:256
v_mfma_f32_16x16x32_bf16 a[240:243],a[112:115],v[136:139],a[240:243]
ds_read_b64_tr_b16 a[98:99],v1 offset:5376
v_mfma_f32_16x16x32_bf16 a[240:243],a[116:119],v[140:143],a[240:243]
buffer_load_dwordx4 v[72:75],v6,s[28:31],s76 offen offset:2048 sc1
v_mfma_f32_16x16x32_bf16 a[240:243],a[120:123],v[152:155],a[240:243]
ds_read_b64_tr_b16 a[102:103],v1 offset:15616
ds_read_b64_tr_b16 a[108:109],v1 offset:30976
v_mfma_f32_16x16x32_bf16 a[240:243],a[124:127],v[156:159],a[240:243]
ds_read_b64_tr_b16 a[110:111],v1 offset:36096
v_subrev_u32 v1,81920,v1
v_mfma_f32_16x16x32_bf16 a[244:247],a[124:127],v[188:191],a[244:247]
v_add_u32 v42,40960,v1
v_add_u32 v44,122880,v1
v_mfma_f32_16x16x32_bf16 a[244:247],a[120:123],v[184:187],a[244:247]
v_mfma_f32_16x16x32_bf16 a[244:247],a[116:119],v[172:175],a[244:247]
buffer_load_dwordx4 v[76:79],v6,s[28:31],s76 offen offset:3072 sc1
v_mfma_f32_16x16x32_bf16 a[244:247],a[112:115],v[168:171],a[244:247]
v_mfma_f32_16x16x32_bf16 a[248:251],a[112:115],v[200:203],a[248:251]
v_mfma_f32_16x16x32_bf16 a[248:251],a[116:119],v[204:207],a[248:251]
v_mfma_f32_16x16x32_bf16 a[248:251],a[120:123],v[216:219],a[248:251]
v_mfma_f32_16x16x32_bf16 a[248:251],a[124:127],v[220:223],a[248:251]
buffer_load_dwordx4 v[80:83],v6,s[28:31],s77 offen sc1
v_mfma_f32_16x16x32_bf16 a[252:255],a[124:127],v[252:255],a[252:255]
ds_read_b64_tr_b16 a[124:125],v2 offset:31008
ds_read_b64_tr_b16 a[126:127],v2 offset:36128
v_mfma_f32_16x16x32_bf16 a[252:255],a[120:123],v[248:251],a[252:255]
)A"
R"A(ds_read_b64_tr_b16 a[120:121],v2 offset:20768
v_mfma_f32_16x16x32_bf16 a[252:255],a[116:119],v[236:239],a[252:255]
ds_read_b64_tr_b16 a[116:117],v2 offset:10528
v_mfma_f32_16x16x32_bf16 a[252:255],a[112:115],v[232:235],a[252:255]
ds_read_b64_tr_b16 a[112:113],v2 offset:288
v_mfma_f32_16x16x32_bf16 a[0:3],a[128:131],v[136:139],a[0:3]
buffer_load_dwordx4 v[84:87],v6,s[28:31],s77 offen offset:1024 sc1
v_mfma_f32_16x16x32_bf16 a[0:3],a[132:135],v[140:143],a[0:3]
ds_read_b64_tr_b16 a[114:115],v2 offset:5408
ds_read_b64_tr_b16 a[118:119],v2 offset:15648
v_mfma_f32_16x16x32_bf16 a[0:3],a[136:139],v[152:155],a[0:3]
ds_read_b64_tr_b16 a[122:123],v2 offset:25888
v_subrev_u32 v2,81920,v2
v_mfma_f32_16x16x32_bf16 a[0:3],a[140:143],v[156:159],a[0:3]
v_add_u32 v43,40960,v2
v_add_u32 v45,122880,v2
v_mfma_f32_16x16x32_bf16 a[4:7],a[140:143],v[188:191],a[4:7]
v_mfma_f32_16x16x32_bf16 a[4:7],a[136:139],v[184:187],a[4:7]
buffer_load_dwordx4 v[88:91],v6,s[28:31],s77 offen offset:2048 sc1
v_mfma_f32_16x16x32_bf16 a[4:7],a[132:135],v[172:175],a[4:7]
v_mfma_f32_16x16x32_bf16 a[4:7],a[128:131],v[168:171],a[4:7]
v_mfma_f32_16x16x32_bf16 a[8:11],a[128:131],v[200:203],a[8:11]
v_mfma_f32_16x16x32_bf16 a[8:11],a[132:135],v[204:207],a[8:11]
v_mfma_f32_16x16x32_bf16 a[8:11],a[136:139],v[216:219],a[8:11]
buffer_load_dwordx4 v[92:95],v6,s[28:31],s77 offen offset:3072 sc1
v_mfma_f32_16x16x32_bf16 a[8:11],a[140:143],v[220:223],a[8:11]
v_mfma_f32_16x16x32_bf16 a[12:15],a[140:143],v[252:255],a[12:15]
ds_read_b64_tr_b16 a[140:141],v1 offset:30720
ds_read_b64_tr_b16 a[142:143],v1 offset:35840
v_mfma_f32_16x16x32_bf16 a[12:15],a[136:139],v[248:251],a[12:15]
ds_read_b64_tr_b16 a[136:137],v1 offset:20480
v_mfma_f32_16x16x32_bf16 a[12:15],a[132:135],v[236:239],a[12:15]
ds_read_b64_tr_b16 a[132:133],v1 offset:10240
v_mfma_f32_16x16x32_bf16 a[12:15],a[128:131],v[232:235],a[12:15]
buffer_load_dwordx4 v[96:99],v6,s[28:31],s78 offen sc1
v_mfma_f32_16x16x32_bf16 a[16:19],a[144:147],v[136:139],a[16:19]
ds_read_b64_tr_b16 a[128:129],v1
ds_read_b64_tr_b16 a[130:131],v1 offset:5120
v_mfma_f32_16x16x32_bf16 a[16:19],a[148:151],v[140:143],a[16:19]
ds_read_b64_tr_b16 a[134:135],v1 offset:15360
v_mfma_f32_16x16x32_bf16 a[16:19],a[152:155],v[152:155],a[16:19]
ds_read_b64_tr_b16 a[138:139],v1 offset:25600
v_mfma_f32_16x16x32_bf16 a[16:19],a[156:159],v[156:159],a[16:19]
v_mfma_f32_16x16x32_bf16 a[20:23],a[156:159],v[188:191],a[20:23]
buffer_load_dwordx4 v[100:103],v6,s[28:31],s78 offen offset:1024 sc1
v_mfma_f32_16x16x32_bf16 a[20:23],a[152:155],v[184:187],a[20:23]
v_mfma_f32_16x16x32_bf16 a[20:23],a[148:151],v[172:175],a[20:23]
v_mfma_f32_16x16x32_bf16 a[20:23],a[144:147],v[168:171],a[20:23]
v_mfma_f32_16x16x32_bf16 a[24:27],a[144:147],v[200:203],a[24:27]
v_mfma_f32_16x16x32_bf16 a[24:27],a[148:151],v[204:207],a[24:27]
buffer_load_dwordx4 v[104:107],v6,s[28:31],s78 offen offset:2048 sc1
v_mfma_f32_16x16x32_bf16 a[24:27],a[152:155],v[216:219],a[24:27]
v_mfma_f32_16x16x32_bf16 a[24:27],a[156:159],v[220:223],a[24:27]
v_mfma_f32_16x16x32_bf16 a[28:31],a[156:159],v[252:255],a[28:31]
ds_read_b64_tr_b16 a[156:157],v2 offset:30752
ds_read_b64_tr_b16 a[158:159],v2 offset:35872
v_mfma_f32_16x16x32_bf16 a[28:31],a[152:155],v[248:251],a[28:31]
ds_read_b64_tr_b16 a[152:153],v2 offset:20512
v_mfma_f32_16x16x32_bf16 a[28:31],a[148:151],v[236:239],a[28:31]
buffer_load_dwordx4 v[108:111],v6,s[28:31],s78 offen offset:3072 sc1
v_mfma_f32_16x16x32_bf16 a[28:31],a[144:147],v[232:235],a[28:31]
s_cmp_ge_u32 2,s71
s_cselect_b32 s80,7,5
s_mul_i32 s80,s80,163840
v_mfma_f32_16x16x32_bf16 a[32:35],a[160:163],v[136:139],a[32:35]
s_add_u32 s80,s80,s81
s_add_u32 s83,s80,20480
ds_read_b64_tr_b16 a[144:145],v2 offset:32
v_mfma_f32_16x16x32_bf16 a[32:35],a[164:167],v[140:143],a[32:35]
ds_read_b64_tr_b16 a[146:147],v2 offset:5152
ds_read_b64_tr_b16 a[148:149],v2 offset:10272
v_mfma_f32_16x16x32_bf16 a[32:35],a[168:171],v[152:155],a[32:35]
ds_read_b64_tr_b16 a[150:151],v2 offset:15392
v_mfma_f32_16x16x32_bf16 a[32:35],a[172:175],v[156:159],a[32:35]
buffer_load_dwordx4 v[112:115],v6,s[28:31],s79 offen sc1
v_mfma_f32_16x16x32_bf16 a[36:39],a[172:175],v[188:191],a[36:39]
ds_read_b64_tr_b16 a[154:155],v2 offset:25632
v_mfma_f32_16x16x32_bf16 a[36:39],a[168:171],v[184:187],a[36:39]
v_mfma_f32_16x16x32_bf16 a[36:39],a[164:167],v[172:175],a[36:39]
v_mfma_f32_16x16x32_bf16 a[36:39],a[160:163],v[168:171],a[36:39]
v_mfma_f32_16x16x32_bf16 a[40:43],a[160:163],v[200:203],a[40:43]
buffer_load_dwordx4 v[116:119],v6,s[28:31],s79 offen offset:1024 sc1
v_mfma_f32_16x16x32_bf16 a[40:43],a[164:167],v[204:207],a[40:43]
v_mfma_f32_16x16x32_bf16 a[40:43],a[168:171],v[216:219],a[40:43]
v_mfma_f32_16x16x32_bf16 a[40:43],a[172:175],v[220:223],a[40:43]
v_mfma_f32_16x16x32_bf16 a[44:47],a[172:175],v[252:255],a[44:47]
ds_read_b64_tr_b16 a[172:173],v1 offset:30784
ds_read_b64_tr_b16 a[174:175],v1 offset:35904
v_mfma_f32_16x16x32_bf16 a[44:47],a[168:171],v[248:251],a[44:47]
buffer_load_dwordx4 v[120:123],v6,s[28:31],s79 offen offset:2048 sc1
v_mfma_f32_16x16x32_bf16 a[44:47],a[164:167],v[236:239],a[44:47]
ds_read_b64_tr_b16 a[164:165],v1 offset:10304
ds_read_b64_tr_b16 a[166:167],v1 offset:15424
v_mfma_f32_16x16x32_bf16 a[44:47],a[160:163],v[232:235],a[44:47]
s_waitcnt lgkmcnt(4)
ds_read_b64_tr_b16 a[160:161],v1 offset:64
v_mfma_f32_16x16x32_bf16 a[48:51],a[176:179],v[136:139],a[48:51]
ds_read_b64_tr_b16 a[162:163],v1 offset:5184
v_mfma_f32_16x16x32_bf16 a[48:51],a[180:183],v[140:143],a[48:51]
ds_read_b64_tr_b16 a[168:169],v1 offset:20544
v_mfma_f32_16x16x32_bf16 a[48:51],a[184:187],v[152:155],a[48:51]
buffer_load_dwordx4 v[124:127],v6,s[28:31],s79 offen offset:3072 sc1
v_mfma_f32_16x16x32_bf16 a[48:51],a[188:191],v[156:159],a[48:51]
ds_read_b64_tr_b16 a[170:171],v1 offset:25664
v_mfma_f32_16x16x32_bf16 a[52:55],a[188:191],v[188:191],a[52:55]
s_add_u32 m0,s82,122880
v_mfma_f32_16x16x32_bf16 a[52:55],a[184:187],v[184:187],a[52:55]
v_mfma_f32_16x16x32_bf16 a[52:55],a[180:183],v[172:175],a[52:55]
v_mfma_f32_16x16x32_bf16 a[52:55],a[176:179],v[168:171],a[52:55]
buffer_load_dwordx4 v7,s[72:75],s80 offen lds
v_mfma_f32_16x16x32_bf16 a[56:59],a[176:179],v[200:203],a[56:59]
v_mfma_f32_16x16x32_bf16 a[56:59],a[180:183],v[204:207],a[56:59]
v_mfma_f32_16x16x32_bf16 a[56:59],a[184:187],v[216:219],a[56:59]
v_mfma_f32_16x16x32_bf16 a[56:59],a[188:191],v[220:223],a[56:59]
v_mfma_f32_16x16x32_bf16 a[60:63],a[188:191],v[252:255],a[60:63]
buffer_load_dwordx4 v8,s[72:75],s80 offen offset:1024 lds
v_mfma_f32_16x16x32_bf16 a[60:63],a[184:187],v[248:251],a[60:63]
ds_read_b64_tr_b16 a[184:185],v2 offset:20576
ds_read_b64_tr_b16 a[186:187],v2 offset:25696
v_mfma_f32_16x16x32_bf16 a[60:63],a[180:183],v[236:239],a[60:63]
ds_read_b64_tr_b16 a[180:181],v2 offset:10336
v_mfma_f32_16x16x32_bf16 a[60:63],a[176:179],v[232:235],a[60:63]
ds_read_b64_tr_b16 a[176:177],v2 offset:96
v_mfma_f32_16x16x32_bf16 a[64:67],a[96:99],v[136:139],a[64:67]
ds_read_b64_tr_b16 a[178:179],v2 offset:5216
v_mfma_f32_16x16x32_bf16 a[64:67],a[100:103],v[140:143],a[64:67]
buffer_load_dwordx4 v9,s[72:75],s80 offen offset:2048 lds
v_mfma_f32_16x16x32_bf16 a[64:67],a[104:107],v[152:155],a[64:67]
ds_read_b64_tr_b16 a[182:183],v2 offset:15456
ds_read_b64_tr_b16 a[188:189],v2 offset:30816
v_mfma_f32_16x16x32_bf16 a[64:67],a[108:111],v[156:159],a[64:67]
ds_read_b64_tr_b16 a[190:191],v2 offset:35936
v_mfma_f32_16x16x32_bf16 a[68:71],a[108:111],v[188:191],a[68:71]
v_mfma_f32_16x16x32_bf16 a[68:71],a[104:107],v[184:187],a[68:71]
v_mfma_f32_16x16x32_bf16 a[68:71],a[100:103],v[172:175],a[68:71]
buffer_load_dwordx4 v10,s[72:75],s80 offen offset:3072 lds
v_mfma_f32_16x16x32_bf16 a[68:71],a[96:99],v[168:171],a[68:71]
v_mfma_f32_16x16x32_bf16 a[72:75],a[96:99],v[200:203],a[72:75]
s_add_u32 m0,s82,126976
v_mfma_f32_16x16x32_bf16 a[72:75],a[100:103],v[204:207],a[72:75]
v_mfma_f32_16x16x32_bf16 a[72:75],a[104:107],v[216:219],a[72:75]
s_cmp_ge_u32 2,s71
s_cselect_b32 s76,7,5
s_lshl_b32 s76,s76,16
v_mfma_f32_16x16x32_bf16 a[72:75],a[108:111],v[220:223],a[72:75]
s_add_u32 s77,s76,4096
buffer_load_dwordx4 v11,s[72:75],s80 offen lds
v_mfma_f32_16x16x32_bf16 a[76:79],a[108:111],v[252:255],a[76:79]
s_add_u32 s78,s76,8192
s_add_u32 s79,s76,12288
s_max_i32 s76,s76,s18
v_mfma_f32_16x16x32_bf16 a[76:79],a[104:107],v[248:251],a[76:79]
s_add_u32 m0,s82,128000
s_max_i32 s77,s77,s19
ds_read_b64_tr_b16 a[104:105],v1 offset:20608
v_mfma_f32_16x16x32_bf16 a[76:79],a[100:103],v[236:239],a[76:79]
s_max_i32 s78,s78,s100
ds_read_b64_tr_b16 a[100:101],v1 offset:10368
ds_read_b64_tr_b16 a[102:103],v1 offset:15488
v_mfma_f32_16x16x32_bf16 a[76:79],a[96:99],v[232:235],a[76:79]
s_max_i32 s79,s79,s101
ds_read_b64_tr_b16 a[96:97],v1 offset:128
v_mfma_f32_16x16x32_bf16 a[80:83],a[112:115],v[136:139],a[80:83]
buffer_load_dwordx4 v7,s[72:75],s83 offen lds
v_mfma_f32_16x16x32_bf16 a[80:83],a[116:119],v[140:143],a[80:83]
ds_read_b64_tr_b16 a[98:99],v1 offset:5248
ds_read_b64_tr_b16 a[106:107],v1 offset:25728
v_mfma_f32_16x16x32_bf16 a[80:83],a[120:123],v[152:155],a[80:83]
ds_read_b64_tr_b16 a[108:109],v1 offset:30848
v_mfma_f32_16x16x32_bf16 a[80:83],a[124:127],v[156:159],a[80:83]
ds_read_b64_tr_b16 a[110:111],v1 offset:35968
v_mfma_f32_16x16x32_bf16 a[84:87],a[124:127],v[188:191],a[84:87]
v_mfma_f32_16x16x32_bf16 a[84:87],a[120:123],v[184:187],a[84:87]
buffer_load_dwordx4 v8,s[72:75],s83 offen offset:1024 lds
v_mfma_f32_16x16x32_bf16 a[84:87],a[116:119],v[172:175],a[84:87]
v_mfma_f32_16x16x32_bf16 a[84:87],a[112:115],v[168:171],a[84:87]
v_mfma_f32_16x16x32_bf16 a[88:91],a[112:115],v[200:203],a[88:91]
v_mfma_f32_16x16x32_bf16 a[88:91],a[116:119],v[204:207],a[88:91]
v_mfma_f32_16x16x32_bf16 a[88:91],a[120:123],v[216:219],a[88:91]
buffer_load_dwordx4 v9,s[72:75],s83 offen offset:2048 lds
v_mfma_f32_16x16x32_bf16 a[88:91],a[124:127],v[220:223],a[88:91]
v_mfma_f32_16x16x32_bf16 a[92:95],a[124:127],v[252:255],a[92:95]
ds_read_b64_tr_b16 a[124:125],v2 offset:30880
ds_read_b64_tr_b16 a[126:127],v2 offset:36000
v_mfma_f32_16x16x32_bf16 a[92:95],a[120:123],v[248:251],a[92:95]
ds_read_b64_tr_b16 a[120:121],v2 offset:20640
v_mfma_f32_16x16x32_bf16 a[92:95],a[116:119],v[236:239],a[92:95]
ds_read_b64_tr_b16 a[116:117],v2 offset:10400
v_mfma_f32_16x16x32_bf16 a[92:95],a[112:115],v[232:235],a[92:95]
buffer_load_dwordx4 v10,s[72:75],s83 offen offset:3072 lds
v_mfma_f32_16x16x32_bf16 a[192:195],a[128:131],v[128:131],a[192:195]
ds_read_b64_tr_b16 a[112:113],v2 offset:160
ds_read_b64_tr_b16 a[114:115],v2 offset:5280
v_mfma_f32_16x16x32_bf16 a[192:195],a[132:135],v[160:163],a[192:195]
s_add_u32 m0,s82,132096
ds_read_b64_tr_b16 a[118:119],v2 offset:15520
v_mfma_f32_16x16x32_bf16 a[192:195],a[136:139],v[192:195],a[192:195]
ds_read_b64_tr_b16 a[122:123],v2 offset:25760
v_mfma_f32_16x16x32_bf16 a[192:195],a[140:143],v[224:227],a[192:195]
buffer_load_dwordx4 v[136:139],v6,s[28:31],s76 offen sc1
v_mfma_f32_16x16x32_bf16 a[196:199],a[140:143],v[228:231],a[196:199]
v_mfma_f32_16x16x32_bf16 a[196:199],a[136:139],v[196:199],a[196:199]
v_mfma_f32_16x16x32_bf16 a[196:199],a[132:135],v[164:167],a[196:199]
v_mfma_f32_16x16x32_bf16 a[196:199],a[128:131],v[132:135],a[196:199]
s_waitcnt vmcnt(14)
buffer_load_dwordx4 v11,s[72:75],s83 offen lds
v_mfma_f32_16x16x32_bf16 a[200:203],a[128:131],v[144:147],a[200:203]
ds_read_b64_tr_b16 a[128:129],v1 offset:192
ds_read_b64_tr_b16 a[130:131],v1 offset:5312
v_mfma_f32_16x16x32_bf16 a[200:203],a[132:135],v[176:179],a[200:203]
ds_read_b64_tr_b16 a[132:133],v1 offset:10432
v_mfma_f32_16x16x32_bf16 a[200:203],a[136:139],v[208:211],a[200:203]
ds_read_b64_tr_b16 a[134:135],v1 offset:15552
v_mfma_f32_16x16x32_bf16 a[200:203],a[140:143],v[240:243],a[200:203]
buffer_load_dwordx4 v[140:143],v6,s[28:31],s76 offen offset:1024 sc1
v_mfma_f32_16x16x32_bf16 a[208:211],a[144:147],v[128:131],a[208:211]
ds_read_b64_tr_b16 a[136:137],v1 offset:20672
ds_read_b64_tr_b16 a[138:139],v1 offset:25792
v_mfma_f32_16x16x32_bf16 a[208:211],a[148:151],v[160:163],a[208:211]
ds_read_b64_tr_b16 a[140:141],v1 offset:30912
v_mfma_f32_16x16x32_bf16 a[208:211],a[152:155],v[192:195],a[208:211]
)A"
R"A(ds_read_b64_tr_b16 a[142:143],v1 offset:36032
v_mfma_f32_16x16x32_bf16 a[208:211],a[156:159],v[224:227],a[208:211]
buffer_load_dwordx4 v[152:155],v6,s[28:31],s76 offen offset:2048 sc1
v_mfma_f32_16x16x32_bf16 a[212:215],a[156:159],v[228:231],a[212:215]
v_mfma_f32_16x16x32_bf16 a[212:215],a[152:155],v[196:199],a[212:215]
v_mfma_f32_16x16x32_bf16 a[212:215],a[148:151],v[164:167],a[212:215]
v_mfma_f32_16x16x32_bf16 a[212:215],a[144:147],v[132:135],a[212:215]
buffer_load_dwordx4 v[156:159],v6,s[28:31],s76 offen offset:3072 sc1
v_mfma_f32_16x16x32_bf16 a[216:219],a[144:147],v[144:147],a[216:219]
s_mul_i32 s76,s70,12
ds_read_b64_tr_b16 a[144:145],v2 offset:224
ds_read_b64_tr_b16 a[146:147],v2 offset:5344
v_mfma_f32_16x16x32_bf16 a[216:219],a[148:151],v[176:179],a[216:219]
s_add_u32 s76,s76,0
s_add_u32 s76,s76,s40
ds_read_b64_tr_b16 a[148:149],v2 offset:10464
v_mfma_f32_16x16x32_bf16 a[216:219],a[152:155],v[208:211],a[216:219]
s_mul_i32 s76,s76,5120
s_max_i32 s76,s76,s101
ds_read_b64_tr_b16 a[150:151],v2 offset:15584
v_mfma_f32_16x16x32_bf16 a[216:219],a[156:159],v[240:243],a[216:219]
s_waitcnt lgkmcnt(4)
buffer_load_dwordx4 v[168:171],v6,s[28:31],s77 offen sc1
v_mfma_f32_16x16x32_bf16 a[224:227],a[160:163],v[128:131],a[224:227]
ds_read_b64_tr_b16 a[152:153],v2 offset:20704
ds_read_b64_tr_b16 a[154:155],v2 offset:25824
v_mfma_f32_16x16x32_bf16 a[224:227],a[164:167],v[160:163],a[224:227]
ds_read_b64_tr_b16 a[156:157],v2 offset:30944
v_mfma_f32_16x16x32_bf16 a[224:227],a[168:171],v[192:195],a[224:227]
ds_read_b64_tr_b16 a[158:159],v2 offset:36064
v_mfma_f32_16x16x32_bf16 a[224:227],a[172:175],v[224:227],a[224:227]
buffer_load_dwordx4 v[172:175],v6,s[28:31],s77 offen offset:1024 sc1
v_mfma_f32_16x16x32_bf16 a[228:231],a[172:175],v[228:231],a[228:231]
v_mfma_f32_16x16x32_bf16 a[228:231],a[168:171],v[196:199],a[228:231]
v_mfma_f32_16x16x32_bf16 a[228:231],a[164:167],v[164:167],a[228:231]
v_mfma_f32_16x16x32_bf16 a[228:231],a[160:163],v[132:135],a[228:231]
v_mfma_f32_16x16x32_bf16 a[232:235],a[160:163],v[144:147],a[232:235]
buffer_load_dwordx4 v[184:187],v6,s[28:31],s77 offen offset:2048 sc1
v_mfma_f32_16x16x32_bf16 a[232:235],a[164:167],v[176:179],a[232:235]
ds_read_b64_tr_b16 a[160:161],v1 offset:256
ds_read_b64_tr_b16 a[162:163],v1 offset:5376
v_mfma_f32_16x16x32_bf16 a[232:235],a[168:171],v[208:211],a[232:235]
ds_read_b64_tr_b16 a[164:165],v1 offset:10496
v_mfma_f32_16x16x32_bf16 a[232:235],a[172:175],v[240:243],a[232:235]
ds_read_b64_tr_b16 a[166:167],v1 offset:15616
v_mfma_f32_16x16x32_bf16 a[240:243],a[176:179],v[128:131],a[240:243]
ds_read_b64_tr_b16 a[168:169],v1 offset:20736
v_mfma_f32_16x16x32_bf16 a[240:243],a[180:183],v[160:163],a[240:243]
buffer_load_dwordx4 v[188:191],v6,s[28:31],s77 offen offset:3072 sc1
v_mfma_f32_16x16x32_bf16 a[240:243],a[184:187],v[192:195],a[240:243]
s_mul_i32 s77,s70,12
ds_read_b64_tr_b16 a[170:171],v1 offset:25856
ds_read_b64_tr_b16 a[172:173],v1 offset:30976
v_mfma_f32_16x16x32_bf16 a[240:243],a[188:191],v[224:227],a[240:243]
s_add_u32 s77,s77,4
s_add_u32 s77,s77,s40
ds_read_b64_tr_b16 a[174:175],v1 offset:36096
v_mfma_f32_16x16x32_bf16 a[244:247],a[188:191],v[228:231],a[244:247]
s_mul_i32 s77,s77,5120
s_max_i32 s77,s77,s101
v_mfma_f32_16x16x32_bf16 a[244:247],a[184:187],v[196:199],a[244:247]
v_mfma_f32_16x16x32_bf16 a[244:247],a[180:183],v[164:167],a[244:247]
buffer_load_dwordx4 v[200:203],v6,s[28:31],s78 offen sc1
v_mfma_f32_16x16x32_bf16 a[244:247],a[176:179],v[132:135],a[244:247]
v_mfma_f32_16x16x32_bf16 a[248:251],a[176:179],v[144:147],a[248:251]
ds_read_b64_tr_b16 a[176:177],v2 offset:288
ds_read_b64_tr_b16 a[178:179],v2 offset:5408
v_mfma_f32_16x16x32_bf16 a[248:251],a[180:183],v[176:179],a[248:251]
ds_read_b64_tr_b16 a[180:181],v2 offset:10528
v_mfma_f32_16x16x32_bf16 a[248:251],a[184:187],v[208:211],a[248:251]
ds_read_b64_tr_b16 a[182:183],v2 offset:15648
v_mfma_f32_16x16x32_bf16 a[248:251],a[188:191],v[240:243],a[248:251]
buffer_load_dwordx4 v[204:207],v6,s[28:31],s78 offen offset:1024 sc1
v_mfma_f32_16x16x32_bf16 a[0:3],a[96:99],v[128:131],a[0:3]
ds_read_b64_tr_b16 a[184:185],v2 offset:20768
ds_read_b64_tr_b16 a[186:187],v2 offset:25888
v_mfma_f32_16x16x32_bf16 a[0:3],a[100:103],v[160:163],a[0:3]
ds_read_b64_tr_b16 a[188:189],v2 offset:31008
v_mfma_f32_16x16x32_bf16 a[0:3],a[104:107],v[192:195],a[0:3]
ds_read_b64_tr_b16 a[190:191],v2 offset:36128
v_mfma_f32_16x16x32_bf16 a[0:3],a[108:111],v[224:227],a[0:3]
v_mfma_f32_16x16x32_bf16 a[4:7],a[108:111],v[228:231],a[4:7]
buffer_load_dwordx4 v[216:219],v6,s[28:31],s78 offen offset:2048 sc1
v_mfma_f32_16x16x32_bf16 a[4:7],a[104:107],v[196:199],a[4:7]
v_mfma_f32_16x16x32_bf16 a[4:7],a[100:103],v[164:167],a[4:7]
v_mfma_f32_16x16x32_bf16 a[4:7],a[96:99],v[132:135],a[4:7]
v_mfma_f32_16x16x32_bf16 a[8:11],a[96:99],v[144:147],a[8:11]
v_mfma_f32_16x16x32_bf16 a[8:11],a[100:103],v[176:179],a[8:11]
buffer_load_dwordx4 v[220:223],v6,s[28:31],s78 offen offset:3072 sc1
v_mfma_f32_16x16x32_bf16 a[8:11],a[104:107],v[208:211],a[8:11]
s_mul_i32 s78,s70,12
s_add_u32 s78,s78,8
s_add_u32 s78,s78,s40
v_mfma_f32_16x16x32_bf16 a[8:11],a[108:111],v[240:243],a[8:11]
s_mul_i32 s78,s78,5120
s_max_i32 s78,s78,s101
v_mfma_f32_16x16x32_bf16 a[16:19],a[112:115],v[128:131],a[16:19]
v_mfma_f32_16x16x32_bf16 a[16:19],a[116:119],v[160:163],a[16:19]
v_mfma_f32_16x16x32_bf16 a[16:19],a[120:123],v[192:195],a[16:19]
buffer_load_dwordx4 v[232:235],v6,s[28:31],s79 offen sc1
v_mfma_f32_16x16x32_bf16 a[16:19],a[124:127],v[224:227],a[16:19]
v_mfma_f32_16x16x32_bf16 a[20:23],a[124:127],v[228:231],a[20:23]
v_mfma_f32_16x16x32_bf16 a[20:23],a[120:123],v[196:199],a[20:23]
v_mfma_f32_16x16x32_bf16 a[20:23],a[116:119],v[164:167],a[20:23]
v_mfma_f32_16x16x32_bf16 a[20:23],a[112:115],v[132:135],a[20:23]
buffer_load_dwordx4 v[236:239],v6,s[28:31],s79 offen offset:1024 sc1
v_mfma_f32_16x16x32_bf16 a[24:27],a[112:115],v[144:147],a[24:27]
v_mfma_f32_16x16x32_bf16 a[24:27],a[116:119],v[176:179],a[24:27]
v_mfma_f32_16x16x32_bf16 a[24:27],a[120:123],v[208:211],a[24:27]
v_mfma_f32_16x16x32_bf16 a[24:27],a[124:127],v[240:243],a[24:27]
v_mfma_f32_16x16x32_bf16 a[32:35],a[128:131],v[128:131],a[32:35]
buffer_load_dwordx4 v[248:251],v6,s[28:31],s79 offen offset:2048 sc1
v_mfma_f32_16x16x32_bf16 a[32:35],a[132:135],v[160:163],a[32:35]
v_mfma_f32_16x16x32_bf16 a[32:35],a[136:139],v[192:195],a[32:35]
v_mfma_f32_16x16x32_bf16 a[32:35],a[140:143],v[224:227],a[32:35]
v_mfma_f32_16x16x32_bf16 a[36:39],a[140:143],v[228:231],a[36:39]
v_mfma_f32_16x16x32_bf16 a[36:39],a[136:139],v[196:199],a[36:39]
buffer_load_dwordx4 v[252:255],v6,s[28:31],s79 offen offset:3072 sc1
v_mfma_f32_16x16x32_bf16 a[36:39],a[132:135],v[164:167],a[36:39]
v_mfma_f32_16x16x32_bf16 a[36:39],a[128:131],v[132:135],a[36:39]
v_mfma_f32_16x16x32_bf16 a[40:43],a[128:131],v[144:147],a[40:43]
v_mfma_f32_16x16x32_bf16 a[40:43],a[132:135],v[176:179],a[40:43]
v_mfma_f32_16x16x32_bf16 a[40:43],a[136:139],v[208:211],a[40:43]
s_waitcnt vmcnt(15)
v_mfma_f32_16x16x32_bf16 a[40:43],a[140:143],v[240:243],a[40:43]
s_barrier
ds_read_b64_tr_b16 a[96:97],v42
ds_read_b64_tr_b16 a[98:99],v42 offset:5120
s_waitcnt lgkmcnt(2)
v_mfma_f32_16x16x32_bf16 a[48:51],a[144:147],v[128:131],a[48:51]
ds_read_b64_tr_b16 a[100:101],v42 offset:10240
v_mfma_f32_16x16x32_bf16 a[48:51],a[148:151],v[160:163],a[48:51]
ds_read_b64_tr_b16 a[102:103],v42 offset:15360
v_mfma_f32_16x16x32_bf16 a[48:51],a[152:155],v[192:195],a[48:51]
ds_read_b64_tr_b16 a[104:105],v42 offset:20480
v_mfma_f32_16x16x32_bf16 a[48:51],a[156:159],v[224:227],a[48:51]
ds_read_b64_tr_b16 a[106:107],v42 offset:25600
ds_read_b64_tr_b16 a[108:109],v42 offset:30720
v_mfma_f32_16x16x32_bf16 a[52:55],a[156:159],v[228:231],a[52:55]
ds_read_b64_tr_b16 a[110:111],v42 offset:35840
v_mfma_f32_16x16x32_bf16 a[52:55],a[152:155],v[196:199],a[52:55]
ds_read_b64_tr_b16 a[112:113],v44
v_mfma_f32_16x16x32_bf16 a[52:55],a[148:151],v[164:167],a[52:55]
ds_read_b64_tr_b16 a[114:115],v44 offset:5120
v_mfma_f32_16x16x32_bf16 a[52:55],a[144:147],v[132:135],a[52:55]
ds_read_b64_tr_b16 a[116:117],v44 offset:10240
ds_read_b64_tr_b16 a[118:119],v44 offset:15360
v_mfma_f32_16x16x32_bf16 a[56:59],a[144:147],v[144:147],a[56:59]
ds_read_b64_tr_b16 a[120:121],v44 offset:20480
v_mfma_f32_16x16x32_bf16 a[56:59],a[148:151],v[176:179],a[56:59]
ds_read_b64_tr_b16 a[122:123],v44 offset:25600
v_mfma_f32_16x16x32_bf16 a[56:59],a[152:155],v[208:211],a[56:59]
ds_read_b64_tr_b16 a[124:125],v44 offset:30720
v_mfma_f32_16x16x32_bf16 a[56:59],a[156:159],v[240:243],a[56:59]
ds_read_b64_tr_b16 a[126:127],v44 offset:35840
ds_read_b64_tr_b16 a[128:129],v43 offset:32
v_mfma_f32_16x16x32_bf16 a[64:67],a[160:163],v[128:131],a[64:67]
ds_read_b64_tr_b16 a[130:131],v43 offset:5152
v_mfma_f32_16x16x32_bf16 a[64:67],a[164:167],v[160:163],a[64:67]
ds_read_b64_tr_b16 a[132:133],v43 offset:10272
v_mfma_f32_16x16x32_bf16 a[64:67],a[168:171],v[192:195],a[64:67]
ds_read_b64_tr_b16 a[134:135],v43 offset:15392
v_mfma_f32_16x16x32_bf16 a[64:67],a[172:175],v[224:227],a[64:67]
ds_read_b64_tr_b16 a[136:137],v43 offset:20512
ds_read_b64_tr_b16 a[138:139],v43 offset:25632
v_mfma_f32_16x16x32_bf16 a[68:71],a[172:175],v[228:231],a[68:71]
ds_read_b64_tr_b16 a[140:141],v43 offset:30752
v_mfma_f32_16x16x32_bf16 a[68:71],a[168:171],v[196:199],a[68:71]
ds_read_b64_tr_b16 a[142:143],v43 offset:35872
v_mfma_f32_16x16x32_bf16 a[68:71],a[164:167],v[164:167],a[68:71]
ds_read_b64_tr_b16 a[144:145],v45 offset:32
v_mfma_f32_16x16x32_bf16 a[68:71],a[160:163],v[132:135],a[68:71]
ds_read_b64_tr_b16 a[146:147],v45 offset:5152
ds_read_b64_tr_b16 a[148:149],v45 offset:10272
v_mfma_f32_16x16x32_bf16 a[72:75],a[160:163],v[144:147],a[72:75]
ds_read_b64_tr_b16 a[150:151],v45 offset:15392
v_mfma_f32_16x16x32_bf16 a[72:75],a[164:167],v[176:179],a[72:75]
ds_read_b64_tr_b16 a[152:153],v45 offset:20512
v_mfma_f32_16x16x32_bf16 a[72:75],a[168:171],v[208:211],a[72:75]
ds_read_b64_tr_b16 a[154:155],v45 offset:25632
v_mfma_f32_16x16x32_bf16 a[72:75],a[172:175],v[240:243],a[72:75]
ds_read_b64_tr_b16 a[156:157],v45 offset:30752
ds_read_b64_tr_b16 a[158:159],v45 offset:35872
v_mfma_f32_16x16x32_bf16 a[80:83],a[176:179],v[128:131],a[80:83]
ds_read_b64_tr_b16 a[160:161],v42 offset:64
v_mfma_f32_16x16x32_bf16 a[80:83],a[180:183],v[160:163],a[80:83]
ds_read_b64_tr_b16 a[162:163],v42 offset:5184
v_mfma_f32_16x16x32_bf16 a[80:83],a[184:187],v[192:195],a[80:83]
ds_read_b64_tr_b16 a[164:165],v42 offset:10304
v_mfma_f32_16x16x32_bf16 a[80:83],a[188:191],v[224:227],a[80:83]
ds_read_b64_tr_b16 a[166:167],v42 offset:15424
ds_read_b64_tr_b16 a[168:169],v42 offset:20544
v_mfma_f32_16x16x32_bf16 a[84:87],a[188:191],v[228:231],a[84:87]
ds_read_b64_tr_b16 a[170:171],v42 offset:25664
v_mfma_f32_16x16x32_bf16 a[84:87],a[184:187],v[196:199],a[84:87]
ds_read_b64_tr_b16 a[172:173],v42 offset:30784
v_mfma_f32_16x16x32_bf16 a[84:87],a[180:183],v[164:167],a[84:87]
ds_read_b64_tr_b16 a[174:175],v42 offset:35904
v_mfma_f32_16x16x32_bf16 a[84:87],a[176:179],v[132:135],a[84:87]
v_mfma_f32_16x16x32_bf16 a[88:91],a[176:179],v[144:147],a[88:91]
ds_read_b64_tr_b16 a[176:177],v44 offset:64
ds_read_b64_tr_b16 a[178:179],v44 offset:5184
v_mfma_f32_16x16x32_bf16 a[88:91],a[180:183],v[176:179],a[88:91]
ds_read_b64_tr_b16 a[180:181],v44 offset:10304
v_mfma_f32_16x16x32_bf16 a[88:91],a[184:187],v[208:211],a[88:91]
ds_read_b64_tr_b16 a[182:183],v44 offset:15424
v_mfma_f32_16x16x32_bf16 a[88:91],a[188:191],v[240:243],a[88:91]
s_waitcnt lgkmcnt(7)
ds_read_b64_tr_b16 a[184:185],v44 offset:20544
v_mfma_f32_16x16x32_bf16 a[192:195],a[96:99],v[64:67],a[192:195]
ds_read_b64_tr_b16 a[186:187],v44 offset:25664
ds_read_b64_tr_b16 a[188:189],v44 offset:30784
v_mfma_f32_16x16x32_bf16 a[192:195],a[100:103],v[68:71],a[192:195]
ds_read_b64_tr_b16 a[190:191],v44 offset:35904
v_mfma_f32_16x16x32_bf16 a[192:195],a[104:107],v[72:75],a[192:195]
v_mfma_f32_16x16x32_bf16 a[192:195],a[108:111],v[76:79],a[192:195]
v_mfma_f32_16x16x32_bf16 a[196:199],a[108:111],v[92:95],a[196:199]
v_mfma_f32_16x16x32_bf16 a[196:199],a[104:107],v[88:91],a[196:199]
)A"
R"A(v_mfma_f32_16x16x32_bf16 a[196:199],a[100:103],v[84:87],a[196:199]
v_mfma_f32_16x16x32_bf16 a[196:199],a[96:99],v[80:83],a[196:199]
v_mfma_f32_16x16x32_bf16 a[200:203],a[96:99],v[96:99],a[200:203]
ds_read_b64_tr_b16 a[96:97],v43 offset:96
ds_read_b64_tr_b16 a[98:99],v43 offset:5216
v_mfma_f32_16x16x32_bf16 a[200:203],a[100:103],v[100:103],a[200:203]
ds_read_b64_tr_b16 a[100:101],v43 offset:10336
v_mfma_f32_16x16x32_bf16 a[200:203],a[104:107],v[104:107],a[200:203]
ds_read_b64_tr_b16 a[102:103],v43 offset:15456
v_mfma_f32_16x16x32_bf16 a[200:203],a[108:111],v[108:111],a[200:203]
ds_read_b64_tr_b16 a[104:105],v43 offset:20576
v_mfma_f32_16x16x32_bf16 a[192:195],a[112:115],v[136:139],a[192:195]
s_waitcnt vmcnt(3)
ds_read_b64_tr_b16 a[106:107],v43 offset:25696
ds_read_b64_tr_b16 a[108:109],v43 offset:30816
v_mfma_f32_16x16x32_bf16 a[192:195],a[116:119],v[140:143],a[192:195]
ds_read_b64_tr_b16 a[110:111],v43 offset:35936
v_mfma_f32_16x16x32_bf16 a[192:195],a[120:123],v[152:155],a[192:195]
v_mfma_f32_16x16x32_bf16 a[192:195],a[124:127],v[156:159],a[192:195]
v_mfma_f32_16x16x32_bf16 a[196:199],a[124:127],v[188:191],a[196:199]
v_mfma_f32_16x16x32_bf16 a[196:199],a[120:123],v[184:187],a[196:199]
v_mfma_f32_16x16x32_bf16 a[196:199],a[116:119],v[172:175],a[196:199]
v_mfma_f32_16x16x32_bf16 a[196:199],a[112:115],v[168:171],a[196:199]
v_mfma_f32_16x16x32_bf16 a[200:203],a[112:115],v[200:203],a[200:203]
ds_read_b64_tr_b16 a[112:113],v45 offset:96
ds_read_b64_tr_b16 a[114:115],v45 offset:5216
v_mfma_f32_16x16x32_bf16 a[200:203],a[116:119],v[204:207],a[200:203]
v_accvgpr_read_b32 v128,a192
v_accvgpr_read_b32 v129,a193
v_mfma_f32_16x16x32_bf16 a[200:203],a[120:123],v[216:219],a[200:203]
v_cvt_pk_bf16_f32 v128,v128,v129
v_accvgpr_read_b32 v130,a194
v_mfma_f32_16x16x32_bf16 a[200:203],a[124:127],v[220:223],a[200:203]
v_accvgpr_read_b32 v131,a195
v_cvt_pk_bf16_f32 v129,v130,v131
v_mfma_f32_16x16x32_bf16 a[208:211],a[128:131],v[64:67],a[208:211]
ds_read_b64_tr_b16 a[116:117],v45 offset:10336
ds_read_b64_tr_b16 a[118:119],v45 offset:15456
v_mfma_f32_16x16x32_bf16 a[208:211],a[132:135],v[68:71],a[208:211]
v_accvgpr_read_b32 v132,a196
v_accvgpr_read_b32 v133,a197
v_mfma_f32_16x16x32_bf16 a[208:211],a[136:139],v[72:75],a[208:211]
v_cvt_pk_bf16_f32 v132,v132,v133
v_accvgpr_read_b32 v134,a198
v_mfma_f32_16x16x32_bf16 a[208:211],a[140:143],v[76:79],a[208:211]
v_accvgpr_read_b32 v135,a199
v_cvt_pk_bf16_f32 v133,v134,v135
v_mfma_f32_16x16x32_bf16 a[212:215],a[140:143],v[92:95],a[212:215]
ds_read_b64_tr_b16 a[120:121],v45 offset:20576
ds_read_b64_tr_b16 a[122:123],v45 offset:25696
v_mfma_f32_16x16x32_bf16 a[212:215],a[136:139],v[88:91],a[212:215]
ds_read_b64_tr_b16 a[124:125],v45 offset:30816
v_accvgpr_read_b32 v144,a200
v_mfma_f32_16x16x32_bf16 a[212:215],a[132:135],v[84:87],a[212:215]
ds_read_b64_tr_b16 a[126:127],v45 offset:35936
v_accvgpr_read_b32 v145,a201
v_mfma_f32_16x16x32_bf16 a[212:215],a[128:131],v[80:83],a[212:215]
v_cvt_pk_bf16_f32 v144,v144,v145
v_accvgpr_read_b32 v146,a202
v_mfma_f32_16x16x32_bf16 a[216:219],a[128:131],v[96:99],a[216:219]
v_accvgpr_read_b32 v147,a203
v_cvt_pk_bf16_f32 v145,v146,v147
v_mfma_f32_16x16x32_bf16 a[216:219],a[132:135],v[100:103],a[216:219]
ds_read_b64_tr_b16 a[128:129],v42 offset:128
ds_read_b64_tr_b16 a[130:131],v42 offset:5248
v_mfma_f32_16x16x32_bf16 a[216:219],a[136:139],v[104:107],a[216:219]
ds_read_b64_tr_b16 a[132:133],v42 offset:10368
v_mfma_f32_16x16x32_bf16 a[216:219],a[140:143],v[108:111],a[216:219]
ds_read_b64_tr_b16 a[134:135],v42 offset:15488
v_mfma_f32_16x16x32_bf16 a[208:211],a[144:147],v[136:139],a[208:211]
ds_read_b64_tr_b16 a[136:137],v42 offset:20608
v_mfma_f32_16x16x32_bf16 a[208:211],a[148:151],v[140:143],a[208:211]
ds_read_b64_tr_b16 a[138:139],v42 offset:25728
ds_read_b64_tr_b16 a[140:141],v42 offset:30848
v_mfma_f32_16x16x32_bf16 a[208:211],a[152:155],v[152:155],a[208:211]
ds_read_b64_tr_b16 a[142:143],v42 offset:35968
v_mfma_f32_16x16x32_bf16 a[208:211],a[156:159],v[156:159],a[208:211]
v_mfma_f32_16x16x32_bf16 a[212:215],a[156:159],v[188:191],a[212:215]
v_mfma_f32_16x16x32_bf16 a[212:215],a[152:155],v[184:187],a[212:215]
v_mfma_f32_16x16x32_bf16 a[212:215],a[148:151],v[172:175],a[212:215]
v_mfma_f32_16x16x32_bf16 a[212:215],a[144:147],v[168:171],a[212:215]
v_mfma_f32_16x16x32_bf16 a[216:219],a[144:147],v[200:203],a[216:219]
ds_read_b64_tr_b16 a[144:145],v44 offset:128
ds_read_b64_tr_b16 a[146:147],v44 offset:5248
v_mfma_f32_16x16x32_bf16 a[216:219],a[148:151],v[204:207],a[216:219]
v_accvgpr_read_b32 v130,a208
v_accvgpr_read_b32 v131,a209
v_mfma_f32_16x16x32_bf16 a[216:219],a[152:155],v[216:219],a[216:219]
v_accvgpr_read_b32 v12,a210
v_accvgpr_read_b32 v13,a211
v_mfma_f32_16x16x32_bf16 a[216:219],a[156:159],v[220:223],a[216:219]
v_cvt_pk_bf16_f32 v130,v130,v131
v_cvt_pk_bf16_f32 v131,v12,v13
v_mfma_f32_16x16x32_bf16 a[224:227],a[160:163],v[64:67],a[224:227]
buffer_store_dwordx4 v[128:131],v6,s[32:35],s84 offen sc1
v_mfma_f32_16x16x32_bf16 a[224:227],a[164:167],v[68:71],a[224:227]
s_waitcnt lgkmcnt(2)
v_accvgpr_read_b32 v134,a212
v_accvgpr_read_b32 v135,a213
v_mfma_f32_16x16x32_bf16 a[224:227],a[168:171],v[72:75],a[224:227]
v_accvgpr_read_b32 v14,a214
v_accvgpr_read_b32 v15,a215
v_mfma_f32_16x16x32_bf16 a[224:227],a[172:175],v[76:79],a[224:227]
v_cvt_pk_bf16_f32 v134,v134,v135
v_cvt_pk_bf16_f32 v135,v14,v15
v_mfma_f32_16x16x32_bf16 a[228:231],a[172:175],v[92:95],a[228:231]
buffer_store_dwordx4 v[132:135],v6,s[32:35],s85 offen sc1
v_mfma_f32_16x16x32_bf16 a[228:231],a[168:171],v[88:91],a[228:231]
v_accvgpr_read_b32 v146,a216
v_accvgpr_read_b32 v147,a217
v_mfma_f32_16x16x32_bf16 a[228:231],a[164:167],v[84:87],a[228:231]
v_accvgpr_read_b32 v16,a218
v_accvgpr_read_b32 v17,a219
v_mfma_f32_16x16x32_bf16 a[228:231],a[160:163],v[80:83],a[228:231]
v_cvt_pk_bf16_f32 v146,v146,v147
v_cvt_pk_bf16_f32 v147,v16,v17
v_mfma_f32_16x16x32_bf16 a[232:235],a[160:163],v[96:99],a[232:235]
buffer_store_dwordx4 v[144:147],v6,s[32:35],s86 offen sc1
v_mfma_f32_16x16x32_bf16 a[232:235],a[164:167],v[100:103],a[232:235]
ds_read_b64_tr_b16 a[148:149],v44 offset:10368
ds_read_b64_tr_b16 a[150:151],v44 offset:15488
v_mfma_f32_16x16x32_bf16 a[232:235],a[168:171],v[104:107],a[232:235]
ds_read_b64_tr_b16 a[152:153],v44 offset:20608
v_mfma_f32_16x16x32_bf16 a[232:235],a[172:175],v[108:111],a[232:235]
ds_read_b64_tr_b16 a[154:155],v44 offset:25728
v_mfma_f32_16x16x32_bf16 a[224:227],a[176:179],v[136:139],a[224:227]
ds_read_b64_tr_b16 a[156:157],v44 offset:30848
v_mfma_f32_16x16x32_bf16 a[224:227],a[180:183],v[140:143],a[224:227]
ds_read_b64_tr_b16 a[158:159],v44 offset:35968
ds_read_b64_tr_b16 a[160:161],v43 offset:160
v_mfma_f32_16x16x32_bf16 a[224:227],a[184:187],v[152:155],a[224:227]
ds_read_b64_tr_b16 a[162:163],v43 offset:5280
v_mfma_f32_16x16x32_bf16 a[224:227],a[188:191],v[156:159],a[224:227]
ds_read_b64_tr_b16 a[164:165],v43 offset:10400
v_mfma_f32_16x16x32_bf16 a[228:231],a[188:191],v[188:191],a[228:231]
ds_read_b64_tr_b16 a[166:167],v43 offset:15520
v_mfma_f32_16x16x32_bf16 a[228:231],a[184:187],v[184:187],a[228:231]
ds_read_b64_tr_b16 a[168:169],v43 offset:20640
ds_read_b64_tr_b16 a[170:171],v43 offset:25760
v_mfma_f32_16x16x32_bf16 a[228:231],a[180:183],v[172:175],a[228:231]
ds_read_b64_tr_b16 a[172:173],v43 offset:30880
v_mfma_f32_16x16x32_bf16 a[228:231],a[176:179],v[168:171],a[228:231]
ds_read_b64_tr_b16 a[174:175],v43 offset:36000
v_mfma_f32_16x16x32_bf16 a[232:235],a[176:179],v[200:203],a[232:235]
ds_read_b64_tr_b16 a[176:177],v45 offset:160
v_mfma_f32_16x16x32_bf16 a[232:235],a[180:183],v[204:207],a[232:235]
v_accvgpr_read_b32 v160,a224
v_accvgpr_read_b32 v161,a225
v_mfma_f32_16x16x32_bf16 a[232:235],a[184:187],v[216:219],a[232:235]
v_cvt_pk_bf16_f32 v160,v160,v161
v_accvgpr_read_b32 v162,a226
v_mfma_f32_16x16x32_bf16 a[232:235],a[188:191],v[220:223],a[232:235]
v_accvgpr_read_b32 v163,a227
v_cvt_pk_bf16_f32 v161,v162,v163
v_mfma_f32_16x16x32_bf16 a[240:243],a[96:99],v[64:67],a[240:243]
ds_read_b64_tr_b16 a[178:179],v45 offset:5280
ds_read_b64_tr_b16 a[180:181],v45 offset:10400
v_mfma_f32_16x16x32_bf16 a[240:243],a[100:103],v[68:71],a[240:243]
v_accvgpr_read_b32 v164,a228
v_accvgpr_read_b32 v165,a229
v_mfma_f32_16x16x32_bf16 a[240:243],a[104:107],v[72:75],a[240:243]
v_cvt_pk_bf16_f32 v164,v164,v165
v_accvgpr_read_b32 v166,a230
v_mfma_f32_16x16x32_bf16 a[240:243],a[108:111],v[76:79],a[240:243]
v_accvgpr_read_b32 v167,a231
v_cvt_pk_bf16_f32 v165,v166,v167
v_mfma_f32_16x16x32_bf16 a[244:247],a[108:111],v[92:95],a[244:247]
ds_read_b64_tr_b16 a[182:183],v45 offset:15520
ds_read_b64_tr_b16 a[184:185],v45 offset:20640
v_mfma_f32_16x16x32_bf16 a[244:247],a[104:107],v[88:91],a[244:247]
ds_read_b64_tr_b16 a[186:187],v45 offset:25760
v_accvgpr_read_b32 v176,a232
v_mfma_f32_16x16x32_bf16 a[244:247],a[100:103],v[84:87],a[244:247]
ds_read_b64_tr_b16 a[188:189],v45 offset:30880
v_accvgpr_read_b32 v177,a233
v_mfma_f32_16x16x32_bf16 a[244:247],a[96:99],v[80:83],a[244:247]
ds_read_b64_tr_b16 a[190:191],v45 offset:36000
v_cvt_pk_bf16_f32 v176,v176,v177
v_mfma_f32_16x16x32_bf16 a[248:251],a[96:99],v[96:99],a[248:251]
v_accvgpr_read_b32 v178,a234
v_accvgpr_read_b32 v179,a235
v_mfma_f32_16x16x32_bf16 a[248:251],a[100:103],v[100:103],a[248:251]
ds_read_b64_tr_b16 a[96:97],v42 offset:192
v_cvt_pk_bf16_f32 v177,v178,v179
v_mfma_f32_16x16x32_bf16 a[248:251],a[104:107],v[104:107],a[248:251]
ds_read_b64_tr_b16 a[98:99],v42 offset:5312
ds_read_b64_tr_b16 a[100:101],v42 offset:10432
v_mfma_f32_16x16x32_bf16 a[248:251],a[108:111],v[108:111],a[248:251]
ds_read_b64_tr_b16 a[102:103],v42 offset:15552
v_mfma_f32_16x16x32_bf16 a[240:243],a[112:115],v[136:139],a[240:243]
ds_read_b64_tr_b16 a[104:105],v42 offset:20672
v_mfma_f32_16x16x32_bf16 a[240:243],a[116:119],v[140:143],a[240:243]
ds_read_b64_tr_b16 a[106:107],v42 offset:25792
v_mfma_f32_16x16x32_bf16 a[240:243],a[120:123],v[152:155],a[240:243]
ds_read_b64_tr_b16 a[108:109],v42 offset:30912
ds_read_b64_tr_b16 a[110:111],v42 offset:36032
v_mfma_f32_16x16x32_bf16 a[240:243],a[124:127],v[156:159],a[240:243]
v_mfma_f32_16x16x32_bf16 a[244:247],a[124:127],v[188:191],a[244:247]
v_mfma_f32_16x16x32_bf16 a[244:247],a[120:123],v[184:187],a[244:247]
v_mfma_f32_16x16x32_bf16 a[244:247],a[116:119],v[172:175],a[244:247]
v_mfma_f32_16x16x32_bf16 a[244:247],a[112:115],v[168:171],a[244:247]
v_mfma_f32_16x16x32_bf16 a[248:251],a[112:115],v[200:203],a[248:251]
ds_read_b64_tr_b16 a[112:113],v44 offset:192
ds_read_b64_tr_b16 a[114:115],v44 offset:5312
v_mfma_f32_16x16x32_bf16 a[248:251],a[116:119],v[204:207],a[248:251]
v_accvgpr_read_b32 v162,a240
v_accvgpr_read_b32 v163,a241
v_mfma_f32_16x16x32_bf16 a[248:251],a[120:123],v[216:219],a[248:251]
v_accvgpr_read_b32 v12,a242
v_accvgpr_read_b32 v13,a243
v_mfma_f32_16x16x32_bf16 a[248:251],a[124:127],v[220:223],a[248:251]
v_cvt_pk_bf16_f32 v162,v162,v163
v_cvt_pk_bf16_f32 v163,v12,v13
v_mfma_f32_16x16x32_bf16 a[0:3],a[128:131],v[64:67],a[0:3]
buffer_store_dwordx4 v[160:163],v6,s[32:35],s84 offen offset:1024 sc1
v_mfma_f32_16x16x32_bf16 a[0:3],a[132:135],v[68:71],a[0:3]
v_accvgpr_read_b32 v166,a244
v_accvgpr_read_b32 v167,a245
v_mfma_f32_16x16x32_bf16 a[0:3],a[136:139],v[72:75],a[0:3]
v_accvgpr_read_b32 v14,a246
v_accvgpr_read_b32 v15,a247
v_mfma_f32_16x16x32_bf16 a[0:3],a[140:143],v[76:79],a[0:3]
v_cvt_pk_bf16_f32 v166,v166,v167
v_cvt_pk_bf16_f32 v167,v14,v15
v_mfma_f32_16x16x32_bf16 a[4:7],a[140:143],v[92:95],a[4:7]
buffer_store_dwordx4 v[164:167],v6,s[32:35],s85 offen offset:1024 sc1
v_mfma_f32_16x16x32_bf16 a[4:7],a[136:139],v[88:91],a[4:7]
v_accvgpr_read_b32 v178,a248
v_accvgpr_read_b32 v179,a249
v_mfma_f32_16x16x32_bf16 a[4:7],a[132:135],v[84:87],a[4:7]
v_accvgpr_read_b32 v16,a250
v_accvgpr_read_b32 v17,a251
v_mfma_f32_16x16x32_bf16 a[4:7],a[128:131],v[80:83],a[4:7]
v_cvt_pk_bf16_f32 v178,v178,v179
v_cvt_pk_bf16_f32 v179,v16,v17
v_mfma_f32_16x16x32_bf16 a[8:11],a[128:131],v[96:99],a[8:11]
buffer_store_dwordx4 v[176:179],v6,s[32:35],s86 offen offset:1024 sc1
v_mfma_f32_16x16x32_bf16 a[8:11],a[132:135],v[100:103],a[8:11]
)A"
R"A(ds_read_b64_tr_b16 a[116:117],v44 offset:10432
ds_read_b64_tr_b16 a[118:119],v44 offset:15552
v_mfma_f32_16x16x32_bf16 a[8:11],a[136:139],v[104:107],a[8:11]
ds_read_b64_tr_b16 a[120:121],v44 offset:20672
v_mfma_f32_16x16x32_bf16 a[8:11],a[140:143],v[108:111],a[8:11]
s_waitcnt lgkmcnt(3)
ds_read_b64_tr_b16 a[122:123],v44 offset:25792
v_mfma_f32_16x16x32_bf16 a[0:3],a[144:147],v[136:139],a[0:3]
ds_read_b64_tr_b16 a[124:125],v44 offset:30912
v_mfma_f32_16x16x32_bf16 a[0:3],a[148:151],v[140:143],a[0:3]
ds_read_b64_tr_b16 a[126:127],v44 offset:36032
ds_read_b64_tr_b16 a[128:129],v43 offset:224
v_mfma_f32_16x16x32_bf16 a[0:3],a[152:155],v[152:155],a[0:3]
ds_read_b64_tr_b16 a[130:131],v43 offset:5344
v_mfma_f32_16x16x32_bf16 a[0:3],a[156:159],v[156:159],a[0:3]
ds_read_b64_tr_b16 a[132:133],v43 offset:10464
v_mfma_f32_16x16x32_bf16 a[4:7],a[156:159],v[188:191],a[4:7]
ds_read_b64_tr_b16 a[134:135],v43 offset:15584
v_mfma_f32_16x16x32_bf16 a[4:7],a[152:155],v[184:187],a[4:7]
ds_read_b64_tr_b16 a[136:137],v43 offset:20704
ds_read_b64_tr_b16 a[138:139],v43 offset:25824
v_mfma_f32_16x16x32_bf16 a[4:7],a[148:151],v[172:175],a[4:7]
ds_read_b64_tr_b16 a[140:141],v43 offset:30944
v_mfma_f32_16x16x32_bf16 a[4:7],a[144:147],v[168:171],a[4:7]
ds_read_b64_tr_b16 a[142:143],v43 offset:36064
v_mfma_f32_16x16x32_bf16 a[8:11],a[144:147],v[200:203],a[8:11]
ds_read_b64_tr_b16 a[144:145],v45 offset:224
v_mfma_f32_16x16x32_bf16 a[8:11],a[148:151],v[204:207],a[8:11]
v_accvgpr_read_b32 v192,a0
v_accvgpr_read_b32 v193,a1
v_mfma_f32_16x16x32_bf16 a[8:11],a[152:155],v[216:219],a[8:11]
v_cvt_pk_bf16_f32 v192,v192,v193
v_accvgpr_read_b32 v194,a2
v_mfma_f32_16x16x32_bf16 a[8:11],a[156:159],v[220:223],a[8:11]
v_accvgpr_read_b32 v195,a3
v_cvt_pk_bf16_f32 v193,v194,v195
v_mfma_f32_16x16x32_bf16 a[16:19],a[160:163],v[64:67],a[16:19]
ds_read_b64_tr_b16 a[146:147],v45 offset:5344
ds_read_b64_tr_b16 a[148:149],v45 offset:10464
v_mfma_f32_16x16x32_bf16 a[16:19],a[164:167],v[68:71],a[16:19]
v_accvgpr_read_b32 v196,a4
v_accvgpr_read_b32 v197,a5
v_mfma_f32_16x16x32_bf16 a[16:19],a[168:171],v[72:75],a[16:19]
v_cvt_pk_bf16_f32 v196,v196,v197
v_accvgpr_read_b32 v198,a6
v_mfma_f32_16x16x32_bf16 a[16:19],a[172:175],v[76:79],a[16:19]
v_accvgpr_read_b32 v199,a7
v_cvt_pk_bf16_f32 v197,v198,v199
v_mfma_f32_16x16x32_bf16 a[20:23],a[172:175],v[92:95],a[20:23]
ds_read_b64_tr_b16 a[150:151],v45 offset:15584
ds_read_b64_tr_b16 a[152:153],v45 offset:20704
v_mfma_f32_16x16x32_bf16 a[20:23],a[168:171],v[88:91],a[20:23]
ds_read_b64_tr_b16 a[154:155],v45 offset:25824
v_accvgpr_read_b32 v208,a8
v_mfma_f32_16x16x32_bf16 a[20:23],a[164:167],v[84:87],a[20:23]
ds_read_b64_tr_b16 a[156:157],v45 offset:30944
v_accvgpr_read_b32 v209,a9
v_mfma_f32_16x16x32_bf16 a[20:23],a[160:163],v[80:83],a[20:23]
ds_read_b64_tr_b16 a[158:159],v45 offset:36064
v_cvt_pk_bf16_f32 v208,v208,v209
v_mfma_f32_16x16x32_bf16 a[24:27],a[160:163],v[96:99],a[24:27]
v_accvgpr_read_b32 v210,a10
v_accvgpr_read_b32 v211,a11
v_mfma_f32_16x16x32_bf16 a[24:27],a[164:167],v[100:103],a[24:27]
ds_read_b64_tr_b16 a[160:161],v42 offset:256
v_cvt_pk_bf16_f32 v209,v210,v211
v_mfma_f32_16x16x32_bf16 a[24:27],a[168:171],v[104:107],a[24:27]
ds_read_b64_tr_b16 a[162:163],v42 offset:5376
ds_read_b64_tr_b16 a[164:165],v42 offset:10496
v_mfma_f32_16x16x32_bf16 a[24:27],a[172:175],v[108:111],a[24:27]
ds_read_b64_tr_b16 a[166:167],v42 offset:15616
v_mfma_f32_16x16x32_bf16 a[16:19],a[176:179],v[136:139],a[16:19]
ds_read_b64_tr_b16 a[168:169],v42 offset:20736
v_mfma_f32_16x16x32_bf16 a[16:19],a[180:183],v[140:143],a[16:19]
ds_read_b64_tr_b16 a[170:171],v42 offset:25856
v_mfma_f32_16x16x32_bf16 a[16:19],a[184:187],v[152:155],a[16:19]
ds_read_b64_tr_b16 a[172:173],v42 offset:30976
ds_read_b64_tr_b16 a[174:175],v42 offset:36096
v_mfma_f32_16x16x32_bf16 a[16:19],a[188:191],v[156:159],a[16:19]
v_mfma_f32_16x16x32_bf16 a[20:23],a[188:191],v[188:191],a[20:23]
v_mfma_f32_16x16x32_bf16 a[20:23],a[184:187],v[184:187],a[20:23]
v_mfma_f32_16x16x32_bf16 a[20:23],a[180:183],v[172:175],a[20:23]
v_mfma_f32_16x16x32_bf16 a[20:23],a[176:179],v[168:171],a[20:23]
v_mfma_f32_16x16x32_bf16 a[24:27],a[176:179],v[200:203],a[24:27]
ds_read_b64_tr_b16 a[176:177],v44 offset:256
ds_read_b64_tr_b16 a[178:179],v44 offset:5376
v_mfma_f32_16x16x32_bf16 a[24:27],a[180:183],v[204:207],a[24:27]
v_accvgpr_read_b32 v194,a16
v_accvgpr_read_b32 v195,a17
v_mfma_f32_16x16x32_bf16 a[24:27],a[184:187],v[216:219],a[24:27]
v_accvgpr_read_b32 v12,a18
v_accvgpr_read_b32 v13,a19
v_mfma_f32_16x16x32_bf16 a[24:27],a[188:191],v[220:223],a[24:27]
v_cvt_pk_bf16_f32 v194,v194,v195
v_cvt_pk_bf16_f32 v195,v12,v13
v_mfma_f32_16x16x32_bf16 a[32:35],a[96:99],v[64:67],a[32:35]
buffer_store_dwordx4 v[192:195],v6,s[32:35],s84 offen offset:2048 sc1
v_mfma_f32_16x16x32_bf16 a[32:35],a[100:103],v[68:71],a[32:35]
v_accvgpr_read_b32 v198,a20
v_accvgpr_read_b32 v199,a21
v_mfma_f32_16x16x32_bf16 a[32:35],a[104:107],v[72:75],a[32:35]
v_accvgpr_read_b32 v14,a22
v_accvgpr_read_b32 v15,a23
v_mfma_f32_16x16x32_bf16 a[32:35],a[108:111],v[76:79],a[32:35]
v_cvt_pk_bf16_f32 v198,v198,v199
v_cvt_pk_bf16_f32 v199,v14,v15
v_mfma_f32_16x16x32_bf16 a[36:39],a[108:111],v[92:95],a[36:39]
buffer_store_dwordx4 v[196:199],v6,s[32:35],s85 offen offset:2048 sc1
v_mfma_f32_16x16x32_bf16 a[36:39],a[104:107],v[88:91],a[36:39]
v_accvgpr_read_b32 v210,a24
v_accvgpr_read_b32 v211,a25
v_mfma_f32_16x16x32_bf16 a[36:39],a[100:103],v[84:87],a[36:39]
v_accvgpr_read_b32 v16,a26
v_accvgpr_read_b32 v17,a27
v_mfma_f32_16x16x32_bf16 a[36:39],a[96:99],v[80:83],a[36:39]
v_cvt_pk_bf16_f32 v210,v210,v211
v_cvt_pk_bf16_f32 v211,v16,v17
v_mfma_f32_16x16x32_bf16 a[40:43],a[96:99],v[96:99],a[40:43]
buffer_store_dwordx4 v[208:211],v6,s[32:35],s86 offen offset:2048 sc1
v_mfma_f32_16x16x32_bf16 a[40:43],a[100:103],v[100:103],a[40:43]
ds_read_b64_tr_b16 a[180:181],v44 offset:10496
ds_read_b64_tr_b16 a[182:183],v44 offset:15616
v_mfma_f32_16x16x32_bf16 a[40:43],a[104:107],v[104:107],a[40:43]
ds_read_b64_tr_b16 a[184:185],v44 offset:20736
v_mfma_f32_16x16x32_bf16 a[40:43],a[108:111],v[108:111],a[40:43]
ds_read_b64_tr_b16 a[186:187],v44 offset:25856
v_mfma_f32_16x16x32_bf16 a[32:35],a[112:115],v[136:139],a[32:35]
s_waitcnt lgkmcnt(4)
ds_read_b64_tr_b16 a[188:189],v44 offset:30976
v_mfma_f32_16x16x32_bf16 a[32:35],a[116:119],v[140:143],a[32:35]
ds_read_b64_tr_b16 a[190:191],v44 offset:36096
ds_read_b64_tr_b16 a[96:97],v43 offset:288
v_mfma_f32_16x16x32_bf16 a[32:35],a[120:123],v[152:155],a[32:35]
ds_read_b64_tr_b16 a[98:99],v43 offset:5408
v_mfma_f32_16x16x32_bf16 a[32:35],a[124:127],v[156:159],a[32:35]
ds_read_b64_tr_b16 a[100:101],v43 offset:10528
v_mfma_f32_16x16x32_bf16 a[36:39],a[124:127],v[188:191],a[36:39]
ds_read_b64_tr_b16 a[102:103],v43 offset:15648
v_mfma_f32_16x16x32_bf16 a[36:39],a[120:123],v[184:187],a[36:39]
ds_read_b64_tr_b16 a[104:105],v43 offset:20768
ds_read_b64_tr_b16 a[106:107],v43 offset:25888
v_mfma_f32_16x16x32_bf16 a[36:39],a[116:119],v[172:175],a[36:39]
ds_read_b64_tr_b16 a[108:109],v43 offset:31008
v_mfma_f32_16x16x32_bf16 a[36:39],a[112:115],v[168:171],a[36:39]
ds_read_b64_tr_b16 a[110:111],v43 offset:36128
v_mfma_f32_16x16x32_bf16 a[40:43],a[112:115],v[200:203],a[40:43]
ds_read_b64_tr_b16 a[112:113],v45 offset:288
v_mfma_f32_16x16x32_bf16 a[40:43],a[116:119],v[204:207],a[40:43]
v_accvgpr_read_b32 v224,a32
v_accvgpr_read_b32 v225,a33
v_mfma_f32_16x16x32_bf16 a[40:43],a[120:123],v[216:219],a[40:43]
v_cvt_pk_bf16_f32 v224,v224,v225
v_accvgpr_read_b32 v226,a34
v_mfma_f32_16x16x32_bf16 a[40:43],a[124:127],v[220:223],a[40:43]
v_accvgpr_read_b32 v227,a35
v_cvt_pk_bf16_f32 v225,v226,v227
v_mfma_f32_16x16x32_bf16 a[48:51],a[128:131],v[64:67],a[48:51]
ds_read_b64_tr_b16 a[114:115],v45 offset:5408
ds_read_b64_tr_b16 a[116:117],v45 offset:10528
v_mfma_f32_16x16x32_bf16 a[48:51],a[132:135],v[68:71],a[48:51]
v_accvgpr_read_b32 v228,a36
v_accvgpr_read_b32 v229,a37
v_mfma_f32_16x16x32_bf16 a[48:51],a[136:139],v[72:75],a[48:51]
v_cvt_pk_bf16_f32 v228,v228,v229
v_accvgpr_read_b32 v230,a38
v_mfma_f32_16x16x32_bf16 a[48:51],a[140:143],v[76:79],a[48:51]
v_accvgpr_read_b32 v231,a39
v_cvt_pk_bf16_f32 v229,v230,v231
v_mfma_f32_16x16x32_bf16 a[52:55],a[140:143],v[92:95],a[52:55]
ds_read_b64_tr_b16 a[118:119],v45 offset:15648
ds_read_b64_tr_b16 a[120:121],v45 offset:20768
v_mfma_f32_16x16x32_bf16 a[52:55],a[136:139],v[88:91],a[52:55]
v_accvgpr_read_b32 v240,a40
v_accvgpr_read_b32 v241,a41
v_mfma_f32_16x16x32_bf16 a[52:55],a[132:135],v[84:87],a[52:55]
v_cvt_pk_bf16_f32 v240,v240,v241
v_accvgpr_read_b32 v242,a42
v_mfma_f32_16x16x32_bf16 a[52:55],a[128:131],v[80:83],a[52:55]
v_accvgpr_read_b32 v243,a43
v_cvt_pk_bf16_f32 v241,v242,v243
v_mfma_f32_16x16x32_bf16 a[56:59],a[128:131],v[96:99],a[56:59]
ds_read_b64_tr_b16 a[122:123],v45 offset:25888
ds_read_b64_tr_b16 a[124:125],v45 offset:31008
v_mfma_f32_16x16x32_bf16 a[56:59],a[132:135],v[100:103],a[56:59]
ds_read_b64_tr_b16 a[126:127],v45 offset:36128
v_mfma_f32_16x16x32_bf16 a[56:59],a[136:139],v[104:107],a[56:59]
ds_read_b64_tr_b16 a[128:129],v1
v_mfma_f32_16x16x32_bf16 a[56:59],a[140:143],v[108:111],a[56:59]
ds_read_b64_tr_b16 a[130:131],v1 offset:5120
v_mfma_f32_16x16x32_bf16 a[48:51],a[144:147],v[136:139],a[48:51]
ds_read_b64_tr_b16 a[132:133],v1 offset:10240
ds_read_b64_tr_b16 a[134:135],v1 offset:15360
v_mfma_f32_16x16x32_bf16 a[48:51],a[148:151],v[140:143],a[48:51]
ds_read_b64_tr_b16 a[136:137],v1 offset:20480
v_mfma_f32_16x16x32_bf16 a[48:51],a[152:155],v[152:155],a[48:51]
ds_read_b64_tr_b16 a[138:139],v1 offset:25600
v_mfma_f32_16x16x32_bf16 a[48:51],a[156:159],v[156:159],a[48:51]
ds_read_b64_tr_b16 a[140:141],v1 offset:30720
v_mfma_f32_16x16x32_bf16 a[52:55],a[156:159],v[188:191],a[52:55]
ds_read_b64_tr_b16 a[142:143],v1 offset:35840
v_mfma_f32_16x16x32_bf16 a[52:55],a[152:155],v[184:187],a[52:55]
v_mfma_f32_16x16x32_bf16 a[52:55],a[148:151],v[172:175],a[52:55]
v_mfma_f32_16x16x32_bf16 a[52:55],a[144:147],v[168:171],a[52:55]
v_mfma_f32_16x16x32_bf16 a[56:59],a[144:147],v[200:203],a[56:59]
ds_read_b64_tr_b16 a[144:145],v42
ds_read_b64_tr_b16 a[146:147],v42 offset:5120
v_mfma_f32_16x16x32_bf16 a[56:59],a[148:151],v[204:207],a[56:59]
v_accvgpr_read_b32 v226,a48
v_accvgpr_read_b32 v227,a49
v_mfma_f32_16x16x32_bf16 a[56:59],a[152:155],v[216:219],a[56:59]
v_accvgpr_read_b32 v12,a50
v_accvgpr_read_b32 v13,a51
v_mfma_f32_16x16x32_bf16 a[56:59],a[156:159],v[220:223],a[56:59]
v_cvt_pk_bf16_f32 v226,v226,v227
v_cvt_pk_bf16_f32 v227,v12,v13
v_mfma_f32_16x16x32_bf16 a[64:67],a[160:163],v[64:67],a[64:67]
buffer_store_dwordx4 v[224:227],v6,s[32:35],s84 offen offset:3072 sc1
v_mfma_f32_16x16x32_bf16 a[64:67],a[164:167],v[68:71],a[64:67]
v_accvgpr_read_b32 v230,a52
v_accvgpr_read_b32 v231,a53
v_mfma_f32_16x16x32_bf16 a[64:67],a[168:171],v[72:75],a[64:67]
v_accvgpr_read_b32 v14,a54
v_accvgpr_read_b32 v15,a55
v_mfma_f32_16x16x32_bf16 a[64:67],a[172:175],v[76:79],a[64:67]
v_cvt_pk_bf16_f32 v230,v230,v231
v_cvt_pk_bf16_f32 v231,v14,v15
v_mfma_f32_16x16x32_bf16 a[68:71],a[172:175],v[92:95],a[68:71]
buffer_store_dwordx4 v[228:231],v6,s[32:35],s85 offen offset:3072 sc1
v_mfma_f32_16x16x32_bf16 a[68:71],a[168:171],v[88:91],a[68:71]
v_accvgpr_read_b32 v242,a56
v_accvgpr_read_b32 v243,a57
v_mfma_f32_16x16x32_bf16 a[68:71],a[164:167],v[84:87],a[68:71]
v_accvgpr_read_b32 v16,a58
v_accvgpr_read_b32 v17,a59
v_mfma_f32_16x16x32_bf16 a[68:71],a[160:163],v[80:83],a[68:71]
v_cvt_pk_bf16_f32 v242,v242,v243
v_cvt_pk_bf16_f32 v243,v16,v17
v_mfma_f32_16x16x32_bf16 a[72:75],a[160:163],v[96:99],a[72:75]
buffer_store_dwordx4 v[240:243],v6,s[32:35],s86 offen offset:3072 sc1
v_mfma_f32_16x16x32_bf16 a[72:75],a[164:167],v[100:103],a[72:75]
ds_read_b64_tr_b16 a[148:149],v42 offset:10240
ds_read_b64_tr_b16 a[150:151],v42 offset:15360
v_mfma_f32_16x16x32_bf16 a[72:75],a[168:171],v[104:107],a[72:75]
ds_read_b64_tr_b16 a[152:153],v42 offset:20480
v_mfma_f32_16x16x32_bf16 a[72:75],a[172:175],v[108:111],a[72:75]
ds_read_b64_tr_b16 a[154:155],v42 offset:25600
v_mfma_f32_16x16x32_bf16 a[64:67],a[176:179],v[136:139],a[64:67]
)A"
R"A(s_waitcnt lgkmcnt(4)
ds_read_b64_tr_b16 a[156:157],v42 offset:30720
v_mfma_f32_16x16x32_bf16 a[64:67],a[180:183],v[140:143],a[64:67]
ds_read_b64_tr_b16 a[158:159],v42 offset:35840
ds_read_b64_tr_b16 a[160:161],v44
v_mfma_f32_16x16x32_bf16 a[64:67],a[184:187],v[152:155],a[64:67]
ds_read_b64_tr_b16 a[162:163],v44 offset:5120
v_mfma_f32_16x16x32_bf16 a[64:67],a[188:191],v[156:159],a[64:67]
ds_read_b64_tr_b16 a[164:165],v44 offset:10240
v_mfma_f32_16x16x32_bf16 a[68:71],a[188:191],v[188:191],a[68:71]
ds_read_b64_tr_b16 a[166:167],v44 offset:15360
v_mfma_f32_16x16x32_bf16 a[68:71],a[184:187],v[184:187],a[68:71]
ds_read_b64_tr_b16 a[168:169],v44 offset:20480
ds_read_b64_tr_b16 a[170:171],v44 offset:25600
v_mfma_f32_16x16x32_bf16 a[68:71],a[180:183],v[172:175],a[68:71]
ds_read_b64_tr_b16 a[172:173],v44 offset:30720
v_mfma_f32_16x16x32_bf16 a[68:71],a[176:179],v[168:171],a[68:71]
ds_read_b64_tr_b16 a[174:175],v44 offset:35840
v_mfma_f32_16x16x32_bf16 a[72:75],a[176:179],v[200:203],a[72:75]
ds_read_b64_tr_b16 a[176:177],v2 offset:32
v_mfma_f32_16x16x32_bf16 a[72:75],a[180:183],v[204:207],a[72:75]
v_accvgpr_read_b32 v128,a64
v_accvgpr_read_b32 v129,a65
v_mfma_f32_16x16x32_bf16 a[72:75],a[184:187],v[216:219],a[72:75]
v_cvt_pk_bf16_f32 v128,v128,v129
v_accvgpr_read_b32 v130,a66
v_mfma_f32_16x16x32_bf16 a[72:75],a[188:191],v[220:223],a[72:75]
v_accvgpr_read_b32 v131,a67
v_cvt_pk_bf16_f32 v129,v130,v131
v_mfma_f32_16x16x32_bf16 a[80:83],a[96:99],v[64:67],a[80:83]
ds_read_b64_tr_b16 a[178:179],v2 offset:5152
ds_read_b64_tr_b16 a[180:181],v2 offset:10272
v_mfma_f32_16x16x32_bf16 a[80:83],a[100:103],v[68:71],a[80:83]
v_accvgpr_read_b32 v132,a68
v_accvgpr_read_b32 v133,a69
v_mfma_f32_16x16x32_bf16 a[80:83],a[104:107],v[72:75],a[80:83]
v_cvt_pk_bf16_f32 v132,v132,v133
v_accvgpr_read_b32 v134,a70
v_mfma_f32_16x16x32_bf16 a[80:83],a[108:111],v[76:79],a[80:83]
ds_read_b64_tr_b16 a[182:183],v2 offset:15392
v_accvgpr_read_b32 v135,a71
v_mfma_f32_16x16x32_bf16 a[84:87],a[108:111],v[92:95],a[84:87]
ds_read_b64_tr_b16 a[184:185],v2 offset:20512
v_cvt_pk_bf16_f32 v133,v134,v135
v_mfma_f32_16x16x32_bf16 a[84:87],a[104:107],v[88:91],a[84:87]
v_accvgpr_read_b32 v144,a72
v_accvgpr_read_b32 v145,a73
v_mfma_f32_16x16x32_bf16 a[84:87],a[100:103],v[84:87],a[84:87]
v_cvt_pk_bf16_f32 v144,v144,v145
v_accvgpr_read_b32 v146,a74
v_mfma_f32_16x16x32_bf16 a[84:87],a[96:99],v[80:83],a[84:87]
v_accvgpr_read_b32 v147,a75
v_cvt_pk_bf16_f32 v145,v146,v147
v_mfma_f32_16x16x32_bf16 a[88:91],a[96:99],v[96:99],a[88:91]
ds_read_b64_tr_b16 a[186:187],v2 offset:25632
ds_read_b64_tr_b16 a[188:189],v2 offset:30752
v_mfma_f32_16x16x32_bf16 a[88:91],a[100:103],v[100:103],a[88:91]
ds_read_b64_tr_b16 a[190:191],v2 offset:35872
v_mfma_f32_16x16x32_bf16 a[88:91],a[104:107],v[104:107],a[88:91]
ds_read_b64_tr_b16 a[96:97],v43 offset:32
v_mfma_f32_16x16x32_bf16 a[88:91],a[108:111],v[108:111],a[88:91]
ds_read_b64_tr_b16 a[98:99],v43 offset:5152
v_mfma_f32_16x16x32_bf16 a[80:83],a[112:115],v[136:139],a[80:83]
ds_read_b64_tr_b16 a[100:101],v43 offset:10272
ds_read_b64_tr_b16 a[102:103],v43 offset:15392
v_mfma_f32_16x16x32_bf16 a[80:83],a[116:119],v[140:143],a[80:83]
ds_read_b64_tr_b16 a[104:105],v43 offset:20512
v_mfma_f32_16x16x32_bf16 a[80:83],a[120:123],v[152:155],a[80:83]
ds_read_b64_tr_b16 a[106:107],v43 offset:25632
v_mfma_f32_16x16x32_bf16 a[80:83],a[124:127],v[156:159],a[80:83]
ds_read_b64_tr_b16 a[108:109],v43 offset:30752
v_mfma_f32_16x16x32_bf16 a[84:87],a[124:127],v[188:191],a[84:87]
ds_read_b64_tr_b16 a[110:111],v43 offset:35872
v_mfma_f32_16x16x32_bf16 a[84:87],a[120:123],v[184:187],a[84:87]
v_mfma_f32_16x16x32_bf16 a[84:87],a[116:119],v[172:175],a[84:87]
v_mfma_f32_16x16x32_bf16 a[84:87],a[112:115],v[168:171],a[84:87]
v_mfma_f32_16x16x32_bf16 a[88:91],a[112:115],v[200:203],a[88:91]
ds_read_b64_tr_b16 a[112:113],v45 offset:32
ds_read_b64_tr_b16 a[114:115],v45 offset:5152
v_mfma_f32_16x16x32_bf16 a[88:91],a[116:119],v[204:207],a[88:91]
v_accvgpr_read_b32 v130,a80
v_accvgpr_read_b32 v131,a81
v_mfma_f32_16x16x32_bf16 a[88:91],a[120:123],v[216:219],a[88:91]
v_accvgpr_read_b32 v12,a82
v_accvgpr_read_b32 v13,a83
v_mfma_f32_16x16x32_bf16 a[88:91],a[124:127],v[220:223],a[88:91]
v_cvt_pk_bf16_f32 v130,v130,v131
v_cvt_pk_bf16_f32 v131,v12,v13
v_mfma_f32_16x16x32_bf16 a[204:207],a[128:131],v[148:151],a[204:207]
s_lshl_b32 s94,s71,2
buffer_store_dwordx4 v[128:131],v25,s[32:35],s84 offen sc1
v_mfma_f32_16x16x32_bf16 a[204:207],a[132:135],v[180:183],a[204:207]
s_add_u32 s94,s94,s40
v_accvgpr_read_b32 v134,a84
v_accvgpr_read_b32 v135,a85
v_mfma_f32_16x16x32_bf16 a[204:207],a[136:139],v[212:215],a[204:207]
s_lshl_b32 s95,s42,2
s_add_u32 s95,s95,s40
s_lshl_b32 s95,s95,4
v_mfma_f32_16x16x32_bf16 a[204:207],a[140:143],v[244:247],a[204:207]
s_lshl_b32 s94,s94,4
v_mov_b32 v18,s68
v_mov_b32 v19,s69
v_mfma_f32_16x16x32_bf16 a[204:207],a[144:147],v[112:115],a[204:207]
s_waitcnt vmcnt(1)
s_mov_b64 exec,1
buffer_store_dwordx2 v[18:19],v6,s[64:67],s95 offen offset:2048 sc1
s_mov_b64 exec,-1
s_waitcnt lgkmcnt(0)
v_mfma_f32_16x16x32_bf16 a[204:207],a[148:151],v[116:119],a[204:207]
v_accvgpr_read_b32 v146,a88
v_accvgpr_read_b32 v147,a89
v_mfma_f32_16x16x32_bf16 a[204:207],a[152:155],v[120:123],a[204:207]
v_accvgpr_read_b32 v14,a86
v_accvgpr_read_b32 v15,a87
v_cvt_pk_bf16_f32 v134,v134,v135
v_accvgpr_read_b32 v16,a90
v_accvgpr_read_b32 v17,a91
v_cvt_pk_bf16_f32 v146,v146,v147
v_cvt_pk_bf16_f32 v135,v14,v15
v_cvt_pk_bf16_f32 v147,v16,v17
v_mfma_f32_16x16x32_bf16 a[204:207],a[156:159],v[124:127],a[204:207]
ds_read_b64_tr_b16 a[116:117],v45 offset:10272
ds_read_b64_tr_b16 a[118:119],v45 offset:15392
v_mfma_f32_16x16x32_bf16 a[204:207],a[160:163],v[232:235],a[204:207]
buffer_store_dwordx4 v[132:135],v25,s[32:35],s85 offen sc1
v_mfma_f32_16x16x32_bf16 a[204:207],a[164:167],v[236:239],a[204:207]
ds_read_b64_tr_b16 a[120:121],v45 offset:20512
ds_read_b64_tr_b16 a[122:123],v45 offset:25632
v_mfma_f32_16x16x32_bf16 a[204:207],a[168:171],v[248:251],a[204:207]
ds_read_b64_tr_b16 a[124:125],v45 offset:30752
ds_read_b64_tr_b16 a[126:127],v45 offset:35872
v_mfma_f32_16x16x32_bf16 a[204:207],a[172:175],v[252:255],a[204:207]
ds_read_b64_tr_b16 a[128:129],v1 offset:64
ds_read_b64_tr_b16 a[130:131],v1 offset:5184
v_mfma_f32_16x16x32_bf16 a[220:223],a[176:179],v[148:151],a[220:223]
buffer_store_dwordx4 v[144:147],v25,s[32:35],s86 offen sc1
v_mfma_f32_16x16x32_bf16 a[220:223],a[180:183],v[180:183],a[220:223]
ds_read_b64_tr_b16 a[132:133],v1 offset:10304
ds_read_b64_tr_b16 a[134:135],v1 offset:15424
v_mfma_f32_16x16x32_bf16 a[220:223],a[184:187],v[212:215],a[220:223]
ds_read_b64_tr_b16 a[136:137],v1 offset:20544
ds_read_b64_tr_b16 a[138:139],v1 offset:25664
v_mfma_f32_16x16x32_bf16 a[220:223],a[188:191],v[244:247],a[220:223]
ds_read_b64_tr_b16 a[140:141],v1 offset:30784
ds_read_b64_tr_b16 a[142:143],v1 offset:35904
v_mfma_f32_16x16x32_bf16 a[220:223],a[96:99],v[112:115],a[220:223]
ds_read_b64_tr_b16 a[144:145],v42 offset:64
ds_read_b64_tr_b16 a[146:147],v42 offset:5184
v_mfma_f32_16x16x32_bf16 a[220:223],a[100:103],v[116:119],a[220:223]
ds_read_b64_tr_b16 a[148:149],v42 offset:10304
ds_read_b64_tr_b16 a[150:151],v42 offset:15424
v_mfma_f32_16x16x32_bf16 a[220:223],a[104:107],v[120:123],a[220:223]
ds_read_b64_tr_b16 a[152:153],v42 offset:20544
ds_read_b64_tr_b16 a[154:155],v42 offset:25664
v_mfma_f32_16x16x32_bf16 a[220:223],a[108:111],v[124:127],a[220:223]
ds_read_b64_tr_b16 a[156:157],v42 offset:30784
ds_read_b64_tr_b16 a[158:159],v42 offset:35904
v_mfma_f32_16x16x32_bf16 a[220:223],a[112:115],v[232:235],a[220:223]
s_waitcnt lgkmcnt(14)
ds_read_b64_tr_b16 a[160:161],v44 offset:64
ds_read_b64_tr_b16 a[162:163],v44 offset:5184
v_mfma_f32_16x16x32_bf16 a[220:223],a[116:119],v[236:239],a[220:223]
ds_read_b64_tr_b16 a[164:165],v44 offset:10304
ds_read_b64_tr_b16 a[166:167],v44 offset:15424
v_mfma_f32_16x16x32_bf16 a[220:223],a[120:123],v[248:251],a[220:223]
ds_read_b64_tr_b16 a[168:169],v44 offset:20544
ds_read_b64_tr_b16 a[170:171],v44 offset:25664
v_mfma_f32_16x16x32_bf16 a[220:223],a[124:127],v[252:255],a[220:223]
ds_read_b64_tr_b16 a[172:173],v44 offset:30784
ds_read_b64_tr_b16 a[174:175],v44 offset:35904
v_mfma_f32_16x16x32_bf16 a[236:239],a[128:131],v[148:151],a[236:239]
s_waitcnt lgkmcnt(14)
ds_read_b64_tr_b16 a[176:177],v2 offset:96
ds_read_b64_tr_b16 a[178:179],v2 offset:5216
v_mfma_f32_16x16x32_bf16 a[236:239],a[132:135],v[180:183],a[236:239]
v_bfe_u32 v24,v6,4,2
v_min_u32 v24,2,v24
v_mfma_f32_16x16x32_bf16 a[236:239],a[136:139],v[212:215],a[236:239]
ds_read_b64_tr_b16 a[180:181],v2 offset:10336
ds_read_b64_tr_b16 a[182:183],v2 offset:15456
v_mfma_f32_16x16x32_bf16 a[236:239],a[140:143],v[244:247],a[236:239]
v_add_u32 v24,s70,v24
v_add_u32 v24,1,v24
v_mfma_f32_16x16x32_bf16 a[236:239],a[144:147],v[112:115],a[236:239]
s_waitcnt lgkmcnt(10)
ds_read_b64_tr_b16 a[184:185],v2 offset:20576
ds_read_b64_tr_b16 a[186:187],v2 offset:25696
v_mfma_f32_16x16x32_bf16 a[236:239],a[148:151],v[116:119],a[236:239]
v_and_b32 v24,3,v24
v_lshlrev_b32 v24,8,v24
v_mfma_f32_16x16x32_bf16 a[236:239],a[152:155],v[120:123],a[236:239]
ds_read_b64_tr_b16 a[188:189],v2 offset:30816
ds_read_b64_tr_b16 a[190:191],v2 offset:35936
v_mfma_f32_16x16x32_bf16 a[236:239],a[156:159],v[124:127],a[236:239]
ds_read_b64_tr_b16 a[96:97],v43 offset:96
ds_read_b64_tr_b16 a[98:99],v43 offset:5216
v_mfma_f32_16x16x32_bf16 a[236:239],a[160:163],v[232:235],a[236:239]
s_waitcnt lgkmcnt(8)
ds_read_b64_tr_b16 a[100:101],v43 offset:10336
ds_read_b64_tr_b16 a[102:103],v43 offset:15456
v_mfma_f32_16x16x32_bf16 a[236:239],a[164:167],v[236:239],a[236:239]
ds_read_b64_tr_b16 a[104:105],v43 offset:20576
ds_read_b64_tr_b16 a[106:107],v43 offset:25696
v_mfma_f32_16x16x32_bf16 a[236:239],a[168:171],v[248:251],a[236:239]
ds_read_b64_tr_b16 a[108:109],v43 offset:30816
ds_read_b64_tr_b16 a[110:111],v43 offset:35936
v_mfma_f32_16x16x32_bf16 a[236:239],a[172:175],v[252:255],a[236:239]
ds_read_b64_tr_b16 a[112:113],v45 offset:96
ds_read_b64_tr_b16 a[114:115],v45 offset:5216
v_mfma_f32_16x16x32_bf16 a[252:255],a[176:179],v[148:151],a[252:255]
s_waitcnt lgkmcnt(12)
ds_read_b64_tr_b16 a[116:117],v45 offset:10336
ds_read_b64_tr_b16 a[118:119],v45 offset:15456
v_mfma_f32_16x16x32_bf16 a[252:255],a[180:183],v[180:183],a[252:255]
ds_read_b64_tr_b16 a[120:121],v45 offset:20576
ds_read_b64_tr_b16 a[122:123],v45 offset:25696
v_mfma_f32_16x16x32_bf16 a[252:255],a[184:187],v[212:215],a[252:255]
s_waitcnt lgkmcnt(14)
ds_read_b64_tr_b16 a[124:125],v45 offset:30816
ds_read_b64_tr_b16 a[126:127],v45 offset:35936
v_mfma_f32_16x16x32_bf16 a[252:255],a[188:191],v[244:247],a[252:255]
s_waitcnt lgkmcnt(14)
ds_read_b64_tr_b16 a[128:129],v1 offset:128
ds_read_b64_tr_b16 a[130:131],v1 offset:5248
v_mfma_f32_16x16x32_bf16 a[252:255],a[96:99],v[112:115],a[252:255]
s_waitcnt lgkmcnt(14)
buffer_load_dwordx2 v[26:27],v24,s[64:67],s94 offen offset:2048 sc1
ds_read_b64_tr_b16 a[132:133],v1 offset:10368
ds_read_b64_tr_b16 a[134:135],v1 offset:15488
v_mfma_f32_16x16x32_bf16 a[252:255],a[100:103],v[116:119],a[252:255]
s_waitcnt lgkmcnt(14)
ds_read_b64_tr_b16 a[136:137],v1 offset:20608
ds_read_b64_tr_b16 a[138:139],v1 offset:25728
v_mfma_f32_16x16x32_bf16 a[252:255],a[104:107],v[120:123],a[252:255]
s_waitcnt lgkmcnt(14)
ds_read_b64_tr_b16 a[140:141],v1 offset:30848
ds_read_b64_tr_b16 a[142:143],v1 offset:35968
v_mfma_f32_16x16x32_bf16 a[252:255],a[108:111],v[124:127],a[252:255]
s_waitcnt lgkmcnt(14)
ds_read_b64_tr_b16 a[144:145],v42 offset:128
ds_read_b64_tr_b16 a[146:147],v42 offset:5248
v_mfma_f32_16x16x32_bf16 a[252:255],a[112:115],v[232:235],a[252:255]
s_waitcnt lgkmcnt(14)
ds_read_b64_tr_b16 a[148:149],v42 offset:10368
ds_read_b64_tr_b16 a[150:151],v42 offset:15488
v_mfma_f32_16x16x32_bf16 a[252:255],a[116:119],v[236:239],a[252:255]
s_lshl_b32 s95,s42,2
ds_read_b64_tr_b16 a[152:153],v42 offset:20608
ds_read_b64_tr_b16 a[154:155],v42 offset:25728
s_waitcnt lgkmcnt(15)
v_mfma_f32_16x16x32_bf16 a[252:255],a[120:123],v[248:251],a[252:255]
s_add_u32 s95,s95,s40
s_lshl_b32 s95,s95,4
)A"
R"A(ds_read_b64_tr_b16 a[156:157],v42 offset:30848
ds_read_b64_tr_b16 a[158:159],v42 offset:35968
v_mov_b32 v18,s68
v_mov_b32 v19,s69
s_waitcnt lgkmcnt(15)
v_mfma_f32_16x16x32_bf16 a[252:255],a[124:127],v[252:255],a[252:255]
s_waitcnt vmcnt(1)
s_mov_b64 exec,1
buffer_store_dwordx2 v[18:19],v6,s[64:67],s95 offen offset:3072 sc1
s_mov_b64 exec,-1
ds_read_b64_tr_b16 a[160:161],v44 offset:128
ds_read_b64_tr_b16 a[162:163],v44 offset:5248
s_waitcnt lgkmcnt(15)
v_mfma_f32_16x16x32_bf16 a[12:15],a[128:131],v[148:151],a[12:15]
s_waitcnt lgkmcnt(14)
ds_read_b64_tr_b16 a[164:165],v44 offset:10368
ds_read_b64_tr_b16 a[166:167],v44 offset:15488
v_mfma_f32_16x16x32_bf16 a[12:15],a[132:135],v[180:183],a[12:15]
s_waitcnt lgkmcnt(14)
ds_read_b64_tr_b16 a[168:169],v44 offset:20608
ds_read_b64_tr_b16 a[170:171],v44 offset:25728
v_mfma_f32_16x16x32_bf16 a[12:15],a[136:139],v[212:215],a[12:15]
s_waitcnt lgkmcnt(14)
ds_read_b64_tr_b16 a[172:173],v44 offset:30848
ds_read_b64_tr_b16 a[174:175],v44 offset:35968
v_mfma_f32_16x16x32_bf16 a[12:15],a[140:143],v[244:247],a[12:15]
s_waitcnt lgkmcnt(14)
ds_read_b64_tr_b16 a[176:177],v2 offset:160
ds_read_b64_tr_b16 a[178:179],v2 offset:5280
v_mfma_f32_16x16x32_bf16 a[12:15],a[144:147],v[112:115],a[12:15]
s_waitcnt lgkmcnt(14)
ds_read_b64_tr_b16 a[180:181],v2 offset:10400
ds_read_b64_tr_b16 a[182:183],v2 offset:15520
v_mfma_f32_16x16x32_bf16 a[12:15],a[148:151],v[116:119],a[12:15]
s_waitcnt lgkmcnt(14)
ds_read_b64_tr_b16 a[184:185],v2 offset:20640
ds_read_b64_tr_b16 a[186:187],v2 offset:25760
v_mfma_f32_16x16x32_bf16 a[12:15],a[152:155],v[120:123],a[12:15]
s_waitcnt lgkmcnt(14)
ds_read_b64_tr_b16 a[188:189],v2 offset:30880
ds_read_b64_tr_b16 a[190:191],v2 offset:36000
v_mfma_f32_16x16x32_bf16 a[12:15],a[156:159],v[124:127],a[12:15]
s_waitcnt lgkmcnt(14)
ds_read_b64_tr_b16 a[96:97],v43 offset:160
ds_read_b64_tr_b16 a[98:99],v43 offset:5280
v_mfma_f32_16x16x32_bf16 a[12:15],a[160:163],v[232:235],a[12:15]
s_waitcnt lgkmcnt(14)
ds_read_b64_tr_b16 a[100:101],v43 offset:10400
ds_read_b64_tr_b16 a[102:103],v43 offset:15520
v_mfma_f32_16x16x32_bf16 a[12:15],a[164:167],v[236:239],a[12:15]
s_waitcnt lgkmcnt(14)
ds_read_b64_tr_b16 a[104:105],v43 offset:20640
ds_read_b64_tr_b16 a[106:107],v43 offset:25760
v_mfma_f32_16x16x32_bf16 a[12:15],a[168:171],v[248:251],a[12:15]
s_waitcnt lgkmcnt(14)
ds_read_b64_tr_b16 a[108:109],v43 offset:30880
ds_read_b64_tr_b16 a[110:111],v43 offset:36000
v_mfma_f32_16x16x32_bf16 a[12:15],a[172:175],v[252:255],a[12:15]
s_waitcnt lgkmcnt(14)
ds_read_b64_tr_b16 a[112:113],v45 offset:160
ds_read_b64_tr_b16 a[114:115],v45 offset:5280
v_mfma_f32_16x16x32_bf16 a[28:31],a[176:179],v[148:151],a[28:31]
s_waitcnt lgkmcnt(14)
ds_read_b64_tr_b16 a[116:117],v45 offset:10400
ds_read_b64_tr_b16 a[118:119],v45 offset:15520
v_mfma_f32_16x16x32_bf16 a[28:31],a[180:183],v[180:183],a[28:31]
s_waitcnt lgkmcnt(14)
ds_read_b64_tr_b16 a[120:121],v45 offset:20640
ds_read_b64_tr_b16 a[122:123],v45 offset:25760
v_mfma_f32_16x16x32_bf16 a[28:31],a[184:187],v[212:215],a[28:31]
s_waitcnt lgkmcnt(14)
ds_read_b64_tr_b16 a[124:125],v45 offset:30880
ds_read_b64_tr_b16 a[126:127],v45 offset:36000
v_mfma_f32_16x16x32_bf16 a[28:31],a[188:191],v[244:247],a[28:31]
s_waitcnt lgkmcnt(14)
ds_read_b64_tr_b16 a[128:129],v1 offset:192
ds_read_b64_tr_b16 a[130:131],v1 offset:5312
v_mfma_f32_16x16x32_bf16 a[28:31],a[96:99],v[112:115],a[28:31]
s_waitcnt lgkmcnt(14)
ds_read_b64_tr_b16 a[132:133],v1 offset:10432
ds_read_b64_tr_b16 a[134:135],v1 offset:15552
v_mfma_f32_16x16x32_bf16 a[28:31],a[100:103],v[116:119],a[28:31]
s_waitcnt lgkmcnt(14)
ds_read_b64_tr_b16 a[136:137],v1 offset:20672
ds_read_b64_tr_b16 a[138:139],v1 offset:25792
v_mfma_f32_16x16x32_bf16 a[28:31],a[104:107],v[120:123],a[28:31]
s_waitcnt lgkmcnt(14)
ds_read_b64_tr_b16 a[140:141],v1 offset:30912
ds_read_b64_tr_b16 a[142:143],v1 offset:36032
v_mfma_f32_16x16x32_bf16 a[28:31],a[108:111],v[124:127],a[28:31]
s_waitcnt lgkmcnt(14)
ds_read_b64_tr_b16 a[144:145],v42 offset:192
ds_read_b64_tr_b16 a[146:147],v42 offset:5312
v_mfma_f32_16x16x32_bf16 a[28:31],a[112:115],v[232:235],a[28:31]
s_waitcnt lgkmcnt(14)
ds_read_b64_tr_b16 a[148:149],v42 offset:10432
ds_read_b64_tr_b16 a[150:151],v42 offset:15552
v_mfma_f32_16x16x32_bf16 a[28:31],a[116:119],v[236:239],a[28:31]
s_waitcnt lgkmcnt(14)
ds_read_b64_tr_b16 a[152:153],v42 offset:20672
ds_read_b64_tr_b16 a[154:155],v42 offset:25792
v_mfma_f32_16x16x32_bf16 a[28:31],a[120:123],v[248:251],a[28:31]
s_waitcnt lgkmcnt(14)
ds_read_b64_tr_b16 a[156:157],v42 offset:30912
ds_read_b64_tr_b16 a[158:159],v42 offset:36032
v_mfma_f32_16x16x32_bf16 a[28:31],a[124:127],v[252:255],a[28:31]
s_waitcnt lgkmcnt(14)
ds_read_b64_tr_b16 a[160:161],v44 offset:192
ds_read_b64_tr_b16 a[162:163],v44 offset:5312
v_mfma_f32_16x16x32_bf16 a[44:47],a[128:131],v[148:151],a[44:47]
ds_read_b64_tr_b16 a[164:165],v44 offset:10432
ds_read_b64_tr_b16 a[166:167],v44 offset:15552
s_waitcnt vmcnt(1)
v_cmp_ne_u32 s[90:91],s68,v26
v_cmp_ne_u32 s[92:93],s69,v27
s_nop 0
s_or_b64 s[90:91],s[90:91],s[92:93]
s_cbranch_scc1 L_fr10_%=
L_fo10_%=:
s_waitcnt lgkmcnt(15)
v_mfma_f32_16x16x32_bf16 a[44:47],a[132:135],v[180:183],a[44:47]
s_waitcnt lgkmcnt(14)
buffer_load_dwordx4 v[64:67],v6,s[32:35],s76 offen sc1
ds_read_b64_tr_b16 a[168:169],v44 offset:20672
ds_read_b64_tr_b16 a[170:171],v44 offset:25792
v_mfma_f32_16x16x32_bf16 a[44:47],a[136:139],v[212:215],a[44:47]
s_lshl_b32 s94,s71,2
ds_read_b64_tr_b16 a[172:173],v44 offset:30912
ds_read_b64_tr_b16 a[174:175],v44 offset:36032
s_waitcnt lgkmcnt(15)
v_mfma_f32_16x16x32_bf16 a[44:47],a[140:143],v[244:247],a[44:47]
s_add_u32 s94,s94,s40
ds_read_b64_tr_b16 a[176:177],v2 offset:224
ds_read_b64_tr_b16 a[178:179],v2 offset:5344
s_waitcnt lgkmcnt(15)
v_mfma_f32_16x16x32_bf16 a[44:47],a[144:147],v[112:115],a[44:47]
s_lshl_b32 s94,s94,4
ds_read_b64_tr_b16 a[180:181],v2 offset:10464
ds_read_b64_tr_b16 a[182:183],v2 offset:15584
s_waitcnt lgkmcnt(15)
v_mfma_f32_16x16x32_bf16 a[44:47],a[148:151],v[116:119],a[44:47]
s_waitcnt lgkmcnt(14)
buffer_load_dwordx4 v[80:83],v6,s[32:35],s77 offen sc1
ds_read_b64_tr_b16 a[184:185],v2 offset:20704
ds_read_b64_tr_b16 a[186:187],v2 offset:25824
v_mfma_f32_16x16x32_bf16 a[44:47],a[152:155],v[120:123],a[44:47]
s_waitcnt lgkmcnt(14)
ds_read_b64_tr_b16 a[188:189],v2 offset:30944
ds_read_b64_tr_b16 a[190:191],v2 offset:36064
v_mfma_f32_16x16x32_bf16 a[44:47],a[156:159],v[124:127],a[44:47]
s_waitcnt lgkmcnt(14)
ds_read_b64_tr_b16 a[96:97],v43 offset:224
ds_read_b64_tr_b16 a[98:99],v43 offset:5344
v_mfma_f32_16x16x32_bf16 a[44:47],a[160:163],v[232:235],a[44:47]
s_waitcnt lgkmcnt(14)
ds_read_b64_tr_b16 a[100:101],v43 offset:10464
ds_read_b64_tr_b16 a[102:103],v43 offset:15584
v_mfma_f32_16x16x32_bf16 a[44:47],a[164:167],v[236:239],a[44:47]
s_waitcnt lgkmcnt(14)
buffer_load_dwordx4 v[96:99],v6,s[32:35],s78 offen sc1
ds_read_b64_tr_b16 a[104:105],v43 offset:20704
ds_read_b64_tr_b16 a[106:107],v43 offset:25824
v_mfma_f32_16x16x32_bf16 a[44:47],a[168:171],v[248:251],a[44:47]
s_waitcnt lgkmcnt(14)
ds_read_b64_tr_b16 a[108:109],v43 offset:30944
ds_read_b64_tr_b16 a[110:111],v43 offset:36064
v_mfma_f32_16x16x32_bf16 a[44:47],a[172:175],v[252:255],a[44:47]
s_waitcnt lgkmcnt(14)
ds_read_b64_tr_b16 a[112:113],v45 offset:224
ds_read_b64_tr_b16 a[114:115],v45 offset:5344
v_mfma_f32_16x16x32_bf16 a[60:63],a[176:179],v[148:151],a[60:63]
s_waitcnt lgkmcnt(14)
ds_read_b64_tr_b16 a[116:117],v45 offset:10464
ds_read_b64_tr_b16 a[118:119],v45 offset:15584
v_mfma_f32_16x16x32_bf16 a[60:63],a[180:183],v[180:183],a[60:63]
s_waitcnt lgkmcnt(14)
buffer_load_dwordx4 v[68:71],v6,s[32:35],s76 offen offset:1024 sc1
ds_read_b64_tr_b16 a[120:121],v45 offset:20704
ds_read_b64_tr_b16 a[122:123],v45 offset:25824
v_mfma_f32_16x16x32_bf16 a[60:63],a[184:187],v[212:215],a[60:63]
s_waitcnt lgkmcnt(14)
ds_read_b64_tr_b16 a[124:125],v45 offset:30944
ds_read_b64_tr_b16 a[126:127],v45 offset:36064
v_mfma_f32_16x16x32_bf16 a[60:63],a[188:191],v[244:247],a[60:63]
s_waitcnt lgkmcnt(14)
ds_read_b64_tr_b16 a[128:129],v1 offset:256
ds_read_b64_tr_b16 a[130:131],v1 offset:5376
v_mfma_f32_16x16x32_bf16 a[60:63],a[96:99],v[112:115],a[60:63]
s_waitcnt lgkmcnt(14)
ds_read_b64_tr_b16 a[132:133],v1 offset:10496
ds_read_b64_tr_b16 a[134:135],v1 offset:15616
v_mfma_f32_16x16x32_bf16 a[60:63],a[100:103],v[116:119],a[60:63]
s_waitcnt lgkmcnt(14)
buffer_load_dwordx4 v[84:87],v6,s[32:35],s77 offen offset:1024 sc1
ds_read_b64_tr_b16 a[136:137],v1 offset:20736
ds_read_b64_tr_b16 a[138:139],v1 offset:25856
v_mfma_f32_16x16x32_bf16 a[60:63],a[104:107],v[120:123],a[60:63]
s_waitcnt lgkmcnt(14)
ds_read_b64_tr_b16 a[140:141],v1 offset:30976
ds_read_b64_tr_b16 a[142:143],v1 offset:36096
v_mfma_f32_16x16x32_bf16 a[60:63],a[108:111],v[124:127],a[60:63]
s_waitcnt lgkmcnt(14)
ds_read_b64_tr_b16 a[144:145],v42 offset:256
ds_read_b64_tr_b16 a[146:147],v42 offset:5376
v_mfma_f32_16x16x32_bf16 a[60:63],a[112:115],v[232:235],a[60:63]
s_waitcnt lgkmcnt(14)
ds_read_b64_tr_b16 a[148:149],v42 offset:10496
ds_read_b64_tr_b16 a[150:151],v42 offset:15616
v_mfma_f32_16x16x32_bf16 a[60:63],a[116:119],v[236:239],a[60:63]
s_waitcnt lgkmcnt(14)
buffer_load_dwordx4 v[100:103],v6,s[32:35],s78 offen offset:1024 sc1
ds_read_b64_tr_b16 a[152:153],v42 offset:20736
ds_read_b64_tr_b16 a[154:155],v42 offset:25856
v_mfma_f32_16x16x32_bf16 a[60:63],a[120:123],v[248:251],a[60:63]
s_waitcnt lgkmcnt(14)
ds_read_b64_tr_b16 a[156:157],v42 offset:30976
ds_read_b64_tr_b16 a[158:159],v42 offset:36096
v_mfma_f32_16x16x32_bf16 a[60:63],a[124:127],v[252:255],a[60:63]
s_waitcnt lgkmcnt(14)
ds_read_b64_tr_b16 a[160:161],v44 offset:256
ds_read_b64_tr_b16 a[162:163],v44 offset:5376
v_mfma_f32_16x16x32_bf16 a[76:79],a[128:131],v[148:151],a[76:79]
s_waitcnt lgkmcnt(14)
ds_read_b64_tr_b16 a[164:165],v44 offset:10496
ds_read_b64_tr_b16 a[166:167],v44 offset:15616
v_mfma_f32_16x16x32_bf16 a[76:79],a[132:135],v[180:183],a[76:79]
s_waitcnt lgkmcnt(14)
buffer_load_dwordx4 v[72:75],v6,s[32:35],s76 offen offset:2048 sc1
ds_read_b64_tr_b16 a[168:169],v44 offset:20736
ds_read_b64_tr_b16 a[170:171],v44 offset:25856
v_bfe_u32 v24,v6,4,2
v_min_u32 v24,2,v24
v_mfma_f32_16x16x32_bf16 a[76:79],a[136:139],v[212:215],a[76:79]
s_waitcnt lgkmcnt(14)
ds_read_b64_tr_b16 a[172:173],v44 offset:30976
ds_read_b64_tr_b16 a[174:175],v44 offset:36096
v_add_u32 v24,s70,v24
v_add_u32 v24,1,v24
v_mfma_f32_16x16x32_bf16 a[76:79],a[140:143],v[244:247],a[76:79]
s_waitcnt lgkmcnt(14)
ds_read_b64_tr_b16 a[176:177],v2 offset:288
ds_read_b64_tr_b16 a[178:179],v2 offset:5408
v_and_b32 v24,3,v24
v_lshlrev_b32 v24,8,v24
v_mfma_f32_16x16x32_bf16 a[76:79],a[144:147],v[112:115],a[76:79]
s_waitcnt lgkmcnt(14)
ds_read_b64_tr_b16 a[180:181],v2 offset:10528
ds_read_b64_tr_b16 a[182:183],v2 offset:15648
v_mfma_f32_16x16x32_bf16 a[76:79],a[148:151],v[116:119],a[76:79]
s_waitcnt lgkmcnt(14)
buffer_load_dwordx4 v[88:91],v6,s[32:35],s77 offen offset:2048 sc1
ds_read_b64_tr_b16 a[184:185],v2 offset:20768
ds_read_b64_tr_b16 a[186:187],v2 offset:25888
v_mfma_f32_16x16x32_bf16 a[76:79],a[152:155],v[120:123],a[76:79]
s_waitcnt lgkmcnt(14)
ds_read_b64_tr_b16 a[188:189],v2 offset:31008
ds_read_b64_tr_b16 a[190:191],v2 offset:36128
v_mfma_f32_16x16x32_bf16 a[76:79],a[156:159],v[124:127],a[76:79]
s_waitcnt lgkmcnt(14)
ds_read_b64_tr_b16 a[96:97],v43 offset:288
ds_read_b64_tr_b16 a[98:99],v43 offset:5408
v_mfma_f32_16x16x32_bf16 a[76:79],a[160:163],v[232:235],a[76:79]
s_waitcnt lgkmcnt(14)
ds_read_b64_tr_b16 a[100:101],v43 offset:10528
ds_read_b64_tr_b16 a[102:103],v43 offset:15648
v_mfma_f32_16x16x32_bf16 a[76:79],a[164:167],v[236:239],a[76:79]
s_waitcnt lgkmcnt(14)
buffer_load_dwordx4 v[104:107],v6,s[32:35],s78 offen offset:2048 sc1
ds_read_b64_tr_b16 a[104:105],v43 offset:20768
ds_read_b64_tr_b16 a[106:107],v43 offset:25888
v_mfma_f32_16x16x32_bf16 a[76:79],a[168:171],v[248:251],a[76:79]
s_waitcnt lgkmcnt(14)
ds_read_b64_tr_b16 a[108:109],v43 offset:31008
ds_read_b64_tr_b16 a[110:111],v43 offset:36128
v_mfma_f32_16x16x32_bf16 a[76:79],a[172:175],v[252:255],a[76:79]
)A"
R"A(s_waitcnt lgkmcnt(14)
ds_read_b64_tr_b16 a[112:113],v45 offset:288
ds_read_b64_tr_b16 a[114:115],v45 offset:5408
v_mfma_f32_16x16x32_bf16 a[92:95],a[176:179],v[148:151],a[92:95]
s_waitcnt lgkmcnt(14)
ds_read_b64_tr_b16 a[116:117],v45 offset:10528
ds_read_b64_tr_b16 a[118:119],v45 offset:15648
v_mfma_f32_16x16x32_bf16 a[92:95],a[180:183],v[180:183],a[92:95]
s_waitcnt lgkmcnt(14)
buffer_load_dwordx4 v[76:79],v6,s[32:35],s76 offen offset:3072 sc1
ds_read_b64_tr_b16 a[120:121],v45 offset:20768
ds_read_b64_tr_b16 a[122:123],v45 offset:25888
v_mfma_f32_16x16x32_bf16 a[92:95],a[184:187],v[212:215],a[92:95]
s_waitcnt lgkmcnt(14)
ds_read_b64_tr_b16 a[124:125],v45 offset:31008
ds_read_b64_tr_b16 a[126:127],v45 offset:36128
v_mfma_f32_16x16x32_bf16 a[92:95],a[188:191],v[244:247],a[92:95]
s_waitcnt lgkmcnt(14)
v_mfma_f32_16x16x32_bf16 a[92:95],a[96:99],v[112:115],a[92:95]
s_waitcnt lgkmcnt(12)
v_mfma_f32_16x16x32_bf16 a[92:95],a[100:103],v[116:119],a[92:95]
s_waitcnt lgkmcnt(10)
buffer_load_dwordx4 v[92:95],v6,s[32:35],s77 offen offset:3072 sc1
v_mfma_f32_16x16x32_bf16 a[92:95],a[104:107],v[120:123],a[92:95]
s_waitcnt lgkmcnt(8)
v_mfma_f32_16x16x32_bf16 a[92:95],a[108:111],v[124:127],a[92:95]
s_waitcnt lgkmcnt(6)
v_mfma_f32_16x16x32_bf16 a[92:95],a[112:115],v[232:235],a[92:95]
s_waitcnt lgkmcnt(4)
v_mfma_f32_16x16x32_bf16 a[92:95],a[116:119],v[236:239],a[92:95]
s_waitcnt lgkmcnt(2)
buffer_load_dwordx4 v[108:111],v6,s[32:35],s78 offen offset:3072 sc1
v_mfma_f32_16x16x32_bf16 a[92:95],a[120:123],v[248:251],a[92:95]
s_waitcnt lgkmcnt(0)
v_mfma_f32_16x16x32_bf16 a[92:95],a[124:127],v[252:255],a[92:95]
buffer_load_dwordx2 v[26:27],v24,s[64:67],s94 offen offset:3072 sc1
s_waitcnt vmcnt(0)
v_cmp_ne_u32 s[90:91],s68,v26
v_cmp_ne_u32 s[92:93],s69,v27
s_nop 0
s_or_b64 s[90:91],s[90:91],s[92:93]
s_cbranch_scc1 L_fr11_%=
L_fo11_%=:
buffer_load_dwordx4 v[136:139],v25,s[32:35],s76 offen sc1
buffer_load_dwordx4 v[168:171],v25,s[32:35],s77 offen sc1
buffer_load_dwordx4 v[200:203],v25,s[32:35],s78 offen sc1
v_mfma_f32_16x16x32_bf16 a[204:207],v[34:37],v[64:67],a[204:207]
v_mfma_f32_16x16x32_bf16 a[204:207],v[34:37],v[80:83],a[204:207]
v_mfma_f32_16x16x32_bf16 a[204:207],v[34:37],v[96:99],a[204:207]
v_mfma_f32_16x16x32_bf16 a[220:223],v[32:35],v[64:67],a[220:223]
v_mfma_f32_16x16x32_bf16 a[220:223],v[32:35],v[80:83],a[220:223]
v_mfma_f32_16x16x32_bf16 a[220:223],v[32:35],v[96:99],a[220:223]
v_mfma_f32_16x16x32_bf16 a[236:239],v[34:37],v[68:71],a[236:239]
v_mfma_f32_16x16x32_bf16 a[236:239],v[34:37],v[84:87],a[236:239]
v_mfma_f32_16x16x32_bf16 a[236:239],v[34:37],v[100:103],a[236:239]
s_nop 1
v_accvgpr_read_b32 v128,a204
v_accvgpr_read_b32 v129,a205
v_mfma_f32_16x16x32_bf16 a[252:255],v[32:35],v[68:71],a[252:255]
v_cvt_pk_bf16_f32 v128,v128,v129
v_accvgpr_read_b32 v130,a206
v_mfma_f32_16x16x32_bf16 a[252:255],v[32:35],v[84:87],a[252:255]
v_accvgpr_read_b32 v131,a207
v_cvt_pk_bf16_f32 v129,v130,v131
v_mfma_f32_16x16x32_bf16 a[252:255],v[32:35],v[100:103],a[252:255]
buffer_store_dwordx2 v[128:129],v40,s[20:23],s43 offen
v_mfma_f32_16x16x32_bf16 a[12:15],v[34:37],v[72:75],a[12:15]
v_accvgpr_read_b32 v130,a220
v_accvgpr_read_b32 v131,a221
v_mfma_f32_16x16x32_bf16 a[12:15],v[34:37],v[88:91],a[12:15]
v_accvgpr_read_b32 v12,a222
v_accvgpr_read_b32 v13,a223
v_mfma_f32_16x16x32_bf16 a[12:15],v[34:37],v[104:107],a[12:15]
v_cvt_pk_bf16_f32 v130,v130,v131
v_cvt_pk_bf16_f32 v131,v12,v13
v_mfma_f32_16x16x32_bf16 a[28:31],v[32:35],v[72:75],a[28:31]
buffer_store_dwordx2 v[130:131],v40,s[20:23],s43 offen offset:32
v_mfma_f32_16x16x32_bf16 a[28:31],v[32:35],v[88:91],a[28:31]
v_accvgpr_read_b32 v132,a236
v_accvgpr_read_b32 v133,a237
v_mfma_f32_16x16x32_bf16 a[28:31],v[32:35],v[104:107],a[28:31]
v_cvt_pk_bf16_f32 v132,v132,v133
v_accvgpr_read_b32 v134,a238
v_mfma_f32_16x16x32_bf16 a[44:47],v[34:37],v[76:79],a[44:47]
v_accvgpr_read_b32 v135,a239
v_cvt_pk_bf16_f32 v133,v134,v135
v_mfma_f32_16x16x32_bf16 a[44:47],v[34:37],v[92:95],a[44:47]
buffer_store_dwordx2 v[132:133],v40,s[20:23],s43 offen offset:64
v_mfma_f32_16x16x32_bf16 a[44:47],v[34:37],v[108:111],a[44:47]
v_accvgpr_read_b32 v144,a12
v_accvgpr_read_b32 v145,a13
v_cvt_pk_bf16_f32 v144,v144,v145
v_accvgpr_read_b32 v146,a14
v_accvgpr_read_b32 v147,a15
v_mfma_f32_16x16x32_bf16 a[60:63],v[32:35],v[76:79],a[60:63]
v_accvgpr_read_b32 v134,a252
v_accvgpr_read_b32 v135,a253
v_cvt_pk_bf16_f32 v145,v146,v147
v_mfma_f32_16x16x32_bf16 a[60:63],v[32:35],v[92:95],a[60:63]
v_accvgpr_read_b32 v14,a254
v_accvgpr_read_b32 v15,a255
v_cvt_pk_bf16_f32 v134,v134,v135
v_cvt_pk_bf16_f32 v135,v14,v15
v_mfma_f32_16x16x32_bf16 a[60:63],v[32:35],v[108:111],a[60:63]
s_waitcnt vmcnt(5)
buffer_store_dwordx2 v[134:135],v40,s[20:23],s43 offen offset:96
buffer_store_dwordx2 v[144:145],v40,s[20:23],s43 offen offset:128
v_accvgpr_read_b32 v146,a28
v_accvgpr_read_b32 v147,a29
v_accvgpr_read_b32 v16,a30
v_accvgpr_read_b32 v17,a31
v_cvt_pk_bf16_f32 v146,v146,v147
v_cvt_pk_bf16_f32 v147,v16,v17
s_nop 3
buffer_store_dwordx2 v[146:147],v40,s[20:23],s43 offen offset:160
v_accvgpr_read_b32 v160,a44
v_accvgpr_read_b32 v161,a45
v_cvt_pk_bf16_f32 v160,v160,v161
v_accvgpr_read_b32 v162,a46
v_accvgpr_read_b32 v163,a47
v_cvt_pk_bf16_f32 v161,v162,v163
buffer_store_dwordx2 v[160:161],v40,s[20:23],s43 offen offset:192
v_accvgpr_read_b32 v162,a60
v_accvgpr_read_b32 v163,a61
v_accvgpr_read_b32 v12,a62
v_accvgpr_read_b32 v13,a63
v_cvt_pk_bf16_f32 v162,v162,v163
v_cvt_pk_bf16_f32 v163,v12,v13
buffer_store_dwordx2 v[162:163],v40,s[20:23],s43 offen offset:224
v_mfma_f32_16x16x32_bf16 a[76:79],v[34:37],v[136:139],a[76:79]
s_waitcnt vmcnt(9)
v_mfma_f32_16x16x32_bf16 a[76:79],v[34:37],v[168:171],a[76:79]
s_waitcnt vmcnt(8)
v_mfma_f32_16x16x32_bf16 a[76:79],v[34:37],v[200:203],a[76:79]
v_mfma_f32_16x16x32_bf16 a[92:95],v[32:35],v[136:139],a[92:95]
v_mfma_f32_16x16x32_bf16 a[92:95],v[32:35],v[168:171],a[92:95]
v_mfma_f32_16x16x32_bf16 a[92:95],v[32:35],v[200:203],a[92:95]
s_nop 3
s_nop 0
v_accvgpr_read_b32 v164,a76
v_accvgpr_read_b32 v165,a77
v_cvt_pk_bf16_f32 v164,v164,v165
v_accvgpr_read_b32 v166,a78
v_accvgpr_read_b32 v167,a79
v_cvt_pk_bf16_f32 v165,v166,v167
buffer_store_dwordx2 v[164:165],v40,s[20:23],s43 offen offset:256
v_accvgpr_read_b32 v166,a92
v_accvgpr_read_b32 v167,a93
v_accvgpr_read_b32 v14,a94
v_accvgpr_read_b32 v15,a95
v_cvt_pk_bf16_f32 v166,v166,v167
v_cvt_pk_bf16_f32 v167,v14,v15
buffer_store_dwordx2 v[166:167],v40,s[20:23],s43 offen offset:288
s_or_b32 s62,s42,s40
s_cmp_lg_u32 s62,0
s_cbranch_scc1 L_noep_%=
v_mov_b32 v12,s68
s_mov_b64 exec,1
buffer_store_dword v12,v25,s[64:67],0 offen sc1
s_mov_b64 exec,-1
L_noep_%=:
s_endpgm
L_ps2_%=:
s_min_i32 s79,s79,100
s_add_u32 s78,s78,s79
L_pl2_%=:
s_sleep 1
s_sub_u32 s79,s79,4
s_cmp_gt_i32 s79,1
s_cbranch_scc1 L_pl2_%=
s_branch L_pr2_%=
L_ps3_%=:
s_min_i32 s79,s79,100
s_add_u32 s78,s78,s79
L_pl3_%=:
s_sleep 1
s_sub_u32 s79,s79,4
s_cmp_gt_i32 s79,1
s_cbranch_scc1 L_pl3_%=
s_branch L_pr3_%=
L_ps4_%=:
s_min_i32 s79,s79,100
s_add_u32 s78,s78,s79
L_pl4_%=:
s_sleep 1
s_sub_u32 s79,s79,4
s_cmp_gt_i32 s79,1
s_cbranch_scc1 L_pl4_%=
s_branch L_pr4_%=
L_ps5_%=:
s_min_i32 s79,s79,100
s_add_u32 s78,s78,s79
L_pl5_%=:
s_sleep 1
s_sub_u32 s79,s79,4
s_cmp_gt_i32 s79,1
s_cbranch_scc1 L_pl5_%=
s_branch L_pr5_%=
L_ps6_%=:
s_min_i32 s79,s79,100
s_add_u32 s78,s78,s79
L_pl6_%=:
s_sleep 1
s_sub_u32 s79,s79,4
s_cmp_gt_i32 s79,1
s_cbranch_scc1 L_pl6_%=
s_branch L_pr6_%=
L_ps7_%=:
s_min_i32 s79,s79,100
s_add_u32 s78,s78,s79
L_pl7_%=:
s_sleep 1
s_sub_u32 s79,s79,4
s_cmp_gt_i32 s79,1
s_cbranch_scc1 L_pl7_%=
s_branch L_pr7_%=
L_fr8_%=:
s_mov_b32 s88,262144
L_fl8_%=:
buffer_load_dwordx2 v[26:27],v24,s[64:67],s94 offen sc1
s_waitcnt vmcnt(0)
s_sub_u32 s88,s88,1
s_cmp_eq_u32 s88,0
s_cbranch_scc1 L_fo8_%=
v_cmp_ne_u32 s[90:91],s68,v26
v_cmp_ne_u32 s[92:93],s69,v27
s_nop 0
s_or_b64 s[90:91],s[90:91],s[92:93]
s_cbranch_scc1 L_fl8_%=
s_branch L_fo8_%=
L_fr9_%=:
s_mov_b32 s88,262144
L_fl9_%=:
buffer_load_dwordx2 v[26:27],v24,s[64:67],s94 offen offset:1024 sc1
s_waitcnt vmcnt(0)
s_sub_u32 s88,s88,1
s_cmp_eq_u32 s88,0
s_cbranch_scc1 L_fo9_%=
v_cmp_ne_u32 s[90:91],s68,v26
v_cmp_ne_u32 s[92:93],s69,v27
s_nop 0
s_or_b64 s[90:91],s[90:91],s[92:93]
s_cbranch_scc1 L_fl9_%=
s_branch L_fo9_%=
L_fr10_%=:
s_mov_b32 s88,262144
L_fl10_%=:
buffer_load_dwordx2 v[26:27],v24,s[64:67],s94 offen offset:2048 sc1
s_waitcnt vmcnt(0)
s_sub_u32 s88,s88,1
s_cmp_eq_u32 s88,0
s_cbranch_scc1 L_fo10_%=
v_cmp_ne_u32 s[90:91],s68,v26
v_cmp_ne_u32 s[92:93],s69,v27
s_nop 0
s_or_b64 s[90:91],s[90:91],s[92:93]
s_cbranch_scc1 L_fl10_%=
s_branch L_fo10_%=
L_fr11_%=:
s_mov_b32 s88,262144
L_fl11_%=:
buffer_load_dwordx2 v[26:27],v24,s[64:67],s94 offen offset:3072 sc1
s_waitcnt vmcnt(0)
s_sub_u32 s88,s88,1
s_cmp_eq_u32 s88,0
s_cbranch_scc1 L_fo11_%=
v_cmp_ne_u32 s[90:91],s68,v26
v_cmp_ne_u32 s[92:93],s69,v27
s_nop 0
s_or_b64 s[90:91],s[90:91],s[92:93]
s_cbranch_scc1 L_fl11_%=
s_branch L_fo11_%=
L_out_end_%=:
L_fast_end_%=:
L_slow0_%=:
s_mov_b32 s98,0
s_branch L_slowc_%=
L_slow1_%=:
s_mov_b32 s98,1
L_slowc_%=:
s_nop 7
s_nop 7
s_waitcnt vmcnt(0) lgkmcnt(0)
s_lshl_b32 s96,%1,2
s_add_u32 s96,s96,s40
s_lshl_b32 s96,s96,15
s_add_u32 s96,s96,167772160
s_mov_b64 s[56:57],s[12:13]
s_add_u32 s56,s56,s96
s_addc_u32 s57,s57,0
s_and_b32 s57,s57,0xffff
s_mov_b32 s58,32768
s_mov_b32 s59,0x20000
s_mov_b32 s96,0
buffer_store_dwordx4 v[64:67],v6,s[56:59],s96 offen
buffer_store_dwordx4 v[68:71],v6,s[56:59],s96 offen offset:1024
buffer_store_dwordx4 v[72:75],v6,s[56:59],s96 offen offset:2048
buffer_store_dwordx4 v[76:79],v6,s[56:59],s96 offen offset:3072
s_mov_b32 s96,4096
buffer_store_dwordx4 v[80:83],v6,s[56:59],s96 offen
buffer_store_dwordx4 v[84:87],v6,s[56:59],s96 offen offset:1024
buffer_store_dwordx4 v[88:91],v6,s[56:59],s96 offen offset:2048
buffer_store_dwordx4 v[92:95],v6,s[56:59],s96 offen offset:3072
s_mov_b32 s96,8192
buffer_store_dwordx4 v[96:99],v6,s[56:59],s96 offen
buffer_store_dwordx4 v[100:103],v6,s[56:59],s96 offen offset:1024
buffer_store_dwordx4 v[104:107],v6,s[56:59],s96 offen offset:2048
buffer_store_dwordx4 v[108:111],v6,s[56:59],s96 offen offset:3072
s_mov_b32 s96,12288
buffer_store_dwordx4 v[112:115],v6,s[56:59],s96 offen
buffer_store_dwordx4 v[116:119],v6,s[56:59],s96 offen offset:1024
buffer_store_dwordx4 v[120:123],v6,s[56:59],s96 offen offset:2048
buffer_store_dwordx4 v[124:127],v6,s[56:59],s96 offen offset:3072
s_mov_b32 s96,16384
buffer_store_dwordx4 v[128:131],v6,s[56:59],s96 offen
buffer_store_dwordx4 v[132:135],v6,s[56:59],s96 offen offset:1024
buffer_store_dwordx4 v[136:139],v6,s[56:59],s96 offen offset:2048
buffer_store_dwordx4 v[140:143],v6,s[56:59],s96 offen offset:3072
s_mov_b32 s96,20480
buffer_store_dwordx4 v[144:147],v6,s[56:59],s96 offen
buffer_store_dwordx4 v[148:151],v6,s[56:59],s96 offen offset:1024
buffer_store_dwordx4 v[152:155],v6,s[56:59],s96 offen offset:2048
buffer_store_dwordx4 v[156:159],v6,s[56:59],s96 offen offset:3072
s_mov_b32 s96,24576
buffer_store_dwordx4 v[160:163],v6,s[56:59],s96 offen
buffer_store_dwordx4 v[164:167],v6,s[56:59],s96 offen offset:1024
buffer_store_dwordx4 v[168:171],v6,s[56:59],s96 offen offset:2048
buffer_store_dwordx4 v[172:175],v6,s[56:59],s96 offen offset:3072
s_mov_b32 s96,28672
buffer_store_dwordx4 v[176:179],v6,s[56:59],s96 offen
buffer_store_dwordx4 v[180:183],v6,s[56:59],s96 offen offset:1024
buffer_store_dwordx4 v[184:187],v6,s[56:59],s96 offen offset:2048
buffer_store_dwordx4 v[188:191],v6,s[56:59],s96 offen offset:3072
s_waitcnt vmcnt(0)
v_writelane_b32 v173,s88,0
v_writelane_b32 v173,s89,1
v_writelane_b32 v173,s90,2
v_writelane_b32 v173,s91,3
v_writelane_b32 v173,s92,4
v_writelane_b32 v173,s93,5
v_writelane_b32 v173,s94,6
v_writelane_b32 v173,s95,7
s_mov_b32 s88,s98
s_mul_i32 s90,s41,307200
s_mov_b64 s[92:93],s[4:5]
s_add_u32 s92,s92,s90
s_addc_u32 s93,s93,0
s_and_b32 s93,s93,0xffff
s_mov_b32 s94,307200
s_mov_b32 s95,0x20000
s_mov_b64 s[96:97],s[10:11]
s_and_b32 s97,s97,0xffff
s_mov_b32 s98,5242880
s_mov_b32 s99,0x20000
v_bfe_u32 v171,v6,4,4
v_lshrrev_b32 v172,8,v6
v_lshlrev_b32 v164,8,v171
)A"
R"A(v_lshl_add_u32 v164,v172,4,v164
v_lshlrev_b32 v165,10,v172
v_lshl_add_u32 v165,v171,1,v165
v_add_u32 v166,4096,v165
v_lshlrev_b32 v168,16,v172
v_lshrrev_b32 v170,2,v171
v_lshl_add_u32 v168,v170,4,v168
v_and_b32 v170,3,v171
v_lshl_add_u32 v168,v170,1,v168
s_lshl_b32 s90,s42,1
s_add_u32 s90,s90,s88
s_lshl_b32 s90,s90,8
v_add_u32 v168,s90,v168
s_mov_b32 s89,0
L_sjb_%=:
s_add_u32 s90,s89,s70
s_add_u32 s90,s90,1
s_and_b32 s90,s90,3
s_lshl_b32 s91,s40,2
s_add_u32 s90,s90,s91
s_lshl_b32 s90,s90,4
v_bfe_u32 v171,v6,4,4
v_lshrrev_b32 v172,8,v6
v_add_u32 v167,s90,v171
v_mul_u32_u24 v167,1280,v167
v_lshl_add_u32 v167,v172,4,v167
s_lshl_b32 s90,s88,4
s_add_u32 s90,s90,s89
s_lshl_b32 s90,s90,12
v_add_u32 v169,s90,v6
v_mov_b32 v64,0
v_mov_b32 v65,0
v_mov_b32 v66,0
v_mov_b32 v67,0
v_mov_b32 v68,0
v_mov_b32 v69,0
v_mov_b32 v70,0
v_mov_b32 v71,0
v_mov_b32 v72,0
v_mov_b32 v73,0
v_mov_b32 v74,0
v_mov_b32 v75,0
v_mov_b32 v76,0
v_mov_b32 v77,0
v_mov_b32 v78,0
v_mov_b32 v79,0
v_mov_b32 v80,0
v_mov_b32 v81,0
v_mov_b32 v82,0
v_mov_b32 v83,0
v_mov_b32 v84,0
v_mov_b32 v85,0
v_mov_b32 v86,0
v_mov_b32 v87,0
v_mov_b32 v88,0
v_mov_b32 v89,0
v_mov_b32 v90,0
v_mov_b32 v91,0
v_mov_b32 v92,0
v_mov_b32 v93,0
v_mov_b32 v94,0
v_mov_b32 v95,0
s_mov_b32 s90,0
L_sq_%=:
s_lshl_b32 s91,s90,6
buffer_load_dwordx4 v[160:163],v167,s[92:95],s91 offen
s_lshl_b32 s91,s90,18
buffer_load_ushort v112,v168,s[96:99],s91 offen
buffer_load_ushort v120,v168,s[96:99],s91 offen offset:8
buffer_load_ushort v128,v168,s[96:99],s91 offen offset:64
buffer_load_ushort v136,v168,s[96:99],s91 offen offset:72
s_add_u32 s91,s91,8192
buffer_load_ushort v113,v168,s[96:99],s91 offen
buffer_load_ushort v121,v168,s[96:99],s91 offen offset:8
buffer_load_ushort v129,v168,s[96:99],s91 offen offset:64
buffer_load_ushort v137,v168,s[96:99],s91 offen offset:72
s_add_u32 s91,s91,8192
buffer_load_ushort v114,v168,s[96:99],s91 offen
buffer_load_ushort v122,v168,s[96:99],s91 offen offset:8
buffer_load_ushort v130,v168,s[96:99],s91 offen offset:64
buffer_load_ushort v138,v168,s[96:99],s91 offen offset:72
s_add_u32 s91,s91,8192
buffer_load_ushort v115,v168,s[96:99],s91 offen
buffer_load_ushort v123,v168,s[96:99],s91 offen offset:8
buffer_load_ushort v131,v168,s[96:99],s91 offen offset:64
buffer_load_ushort v139,v168,s[96:99],s91 offen offset:72
s_add_u32 s91,s91,8192
buffer_load_ushort v116,v168,s[96:99],s91 offen
buffer_load_ushort v124,v168,s[96:99],s91 offen offset:8
buffer_load_ushort v132,v168,s[96:99],s91 offen offset:64
buffer_load_ushort v140,v168,s[96:99],s91 offen offset:72
s_add_u32 s91,s91,8192
buffer_load_ushort v117,v168,s[96:99],s91 offen
buffer_load_ushort v125,v168,s[96:99],s91 offen offset:8
buffer_load_ushort v133,v168,s[96:99],s91 offen offset:64
buffer_load_ushort v141,v168,s[96:99],s91 offen offset:72
s_add_u32 s91,s91,8192
buffer_load_ushort v118,v168,s[96:99],s91 offen
buffer_load_ushort v126,v168,s[96:99],s91 offen offset:8
buffer_load_ushort v134,v168,s[96:99],s91 offen offset:64
buffer_load_ushort v142,v168,s[96:99],s91 offen offset:72
s_add_u32 s91,s91,8192
buffer_load_ushort v119,v168,s[96:99],s91 offen
buffer_load_ushort v127,v168,s[96:99],s91 offen offset:8
buffer_load_ushort v135,v168,s[96:99],s91 offen offset:64
buffer_load_ushort v143,v168,s[96:99],s91 offen offset:72
s_add_u32 s91,s91,8192
s_waitcnt vmcnt(0)
v_lshl_or_b32 v112,v113,16,v112
v_lshl_or_b32 v113,v115,16,v114
v_lshl_or_b32 v114,v117,16,v116
v_lshl_or_b32 v115,v119,16,v118
v_lshl_or_b32 v120,v121,16,v120
v_lshl_or_b32 v121,v123,16,v122
v_lshl_or_b32 v122,v125,16,v124
v_lshl_or_b32 v123,v127,16,v126
v_lshl_or_b32 v128,v129,16,v128
v_lshl_or_b32 v129,v131,16,v130
v_lshl_or_b32 v130,v133,16,v132
v_lshl_or_b32 v131,v135,16,v134
v_lshl_or_b32 v136,v137,16,v136
v_lshl_or_b32 v137,v139,16,v138
v_lshl_or_b32 v138,v141,16,v140
v_lshl_or_b32 v139,v143,16,v142
s_nop 1
v_mfma_f32_16x16x32_bf16 v[64:67],v[112:115],v[160:163],v[64:67]
v_mfma_f32_16x16x32_bf16 v[68:71],v[120:123],v[160:163],v[68:71]
v_mfma_f32_16x16x32_bf16 v[72:75],v[128:131],v[160:163],v[72:75]
v_mfma_f32_16x16x32_bf16 v[76:79],v[136:139],v[160:163],v[76:79]
s_nop 7
s_lshl_b32 s91,s90,18
buffer_load_ushort v112,v168,s[96:99],s91 offen offset:128
buffer_load_ushort v120,v168,s[96:99],s91 offen offset:136
buffer_load_ushort v128,v168,s[96:99],s91 offen offset:192
buffer_load_ushort v136,v168,s[96:99],s91 offen offset:200
s_add_u32 s91,s91,8192
buffer_load_ushort v113,v168,s[96:99],s91 offen offset:128
buffer_load_ushort v121,v168,s[96:99],s91 offen offset:136
buffer_load_ushort v129,v168,s[96:99],s91 offen offset:192
buffer_load_ushort v137,v168,s[96:99],s91 offen offset:200
s_add_u32 s91,s91,8192
buffer_load_ushort v114,v168,s[96:99],s91 offen offset:128
buffer_load_ushort v122,v168,s[96:99],s91 offen offset:136
buffer_load_ushort v130,v168,s[96:99],s91 offen offset:192
buffer_load_ushort v138,v168,s[96:99],s91 offen offset:200
s_add_u32 s91,s91,8192
buffer_load_ushort v115,v168,s[96:99],s91 offen offset:128
buffer_load_ushort v123,v168,s[96:99],s91 offen offset:136
buffer_load_ushort v131,v168,s[96:99],s91 offen offset:192
buffer_load_ushort v139,v168,s[96:99],s91 offen offset:200
s_add_u32 s91,s91,8192
buffer_load_ushort v116,v168,s[96:99],s91 offen offset:128
buffer_load_ushort v124,v168,s[96:99],s91 offen offset:136
buffer_load_ushort v132,v168,s[96:99],s91 offen offset:192
buffer_load_ushort v140,v168,s[96:99],s91 offen offset:200
s_add_u32 s91,s91,8192
buffer_load_ushort v117,v168,s[96:99],s91 offen offset:128
buffer_load_ushort v125,v168,s[96:99],s91 offen offset:136
buffer_load_ushort v133,v168,s[96:99],s91 offen offset:192
buffer_load_ushort v141,v168,s[96:99],s91 offen offset:200
s_add_u32 s91,s91,8192
buffer_load_ushort v118,v168,s[96:99],s91 offen offset:128
buffer_load_ushort v126,v168,s[96:99],s91 offen offset:136
buffer_load_ushort v134,v168,s[96:99],s91 offen offset:192
buffer_load_ushort v142,v168,s[96:99],s91 offen offset:200
s_add_u32 s91,s91,8192
buffer_load_ushort v119,v168,s[96:99],s91 offen offset:128
buffer_load_ushort v127,v168,s[96:99],s91 offen offset:136
buffer_load_ushort v135,v168,s[96:99],s91 offen offset:192
buffer_load_ushort v143,v168,s[96:99],s91 offen offset:200
s_add_u32 s91,s91,8192
s_waitcnt vmcnt(0)
v_lshl_or_b32 v112,v113,16,v112
v_lshl_or_b32 v113,v115,16,v114
v_lshl_or_b32 v114,v117,16,v116
v_lshl_or_b32 v115,v119,16,v118
v_lshl_or_b32 v120,v121,16,v120
v_lshl_or_b32 v121,v123,16,v122
v_lshl_or_b32 v122,v125,16,v124
v_lshl_or_b32 v123,v127,16,v126
v_lshl_or_b32 v128,v129,16,v128
v_lshl_or_b32 v129,v131,16,v130
v_lshl_or_b32 v130,v133,16,v132
v_lshl_or_b32 v131,v135,16,v134
v_lshl_or_b32 v136,v137,16,v136
v_lshl_or_b32 v137,v139,16,v138
v_lshl_or_b32 v138,v141,16,v140
v_lshl_or_b32 v139,v143,16,v142
s_nop 1
v_mfma_f32_16x16x32_bf16 v[80:83],v[112:115],v[160:163],v[80:83]
v_mfma_f32_16x16x32_bf16 v[84:87],v[120:123],v[160:163],v[84:87]
v_mfma_f32_16x16x32_bf16 v[88:91],v[128:131],v[160:163],v[88:91]
v_mfma_f32_16x16x32_bf16 v[92:95],v[136:139],v[160:163],v[92:95]
s_nop 7
s_add_u32 s90,s90,1
s_cmp_lg_u32 s90,20
s_cbranch_scc1 L_sq_%=
s_nop 7
v_mul_f32 v64,s61,v64
v_mul_f32 v65,s61,v65
v_mul_f32 v66,s61,v66
v_mul_f32 v67,s61,v67
v_mul_f32 v68,s61,v68
v_mul_f32 v69,s61,v69
v_mul_f32 v70,s61,v70
v_mul_f32 v71,s61,v71
v_mul_f32 v72,s61,v72
v_mul_f32 v73,s61,v73
v_mul_f32 v74,s61,v74
v_mul_f32 v75,s61,v75
v_mul_f32 v76,s61,v76
v_mul_f32 v77,s61,v77
v_mul_f32 v78,s61,v78
v_mul_f32 v79,s61,v79
v_mul_f32 v80,s61,v80
v_mul_f32 v81,s61,v81
v_mul_f32 v82,s61,v82
v_mul_f32 v83,s61,v83
v_mul_f32 v84,s61,v84
v_mul_f32 v85,s61,v85
v_mul_f32 v86,s61,v86
v_mul_f32 v87,s61,v87
v_mul_f32 v88,s61,v88
v_mul_f32 v89,s61,v89
v_mul_f32 v90,s61,v90
v_mul_f32 v91,s61,v91
v_mul_f32 v92,s61,v92
v_mul_f32 v93,s61,v93
v_mul_f32 v94,s61,v94
v_mul_f32 v95,s61,v95
v_cvt_pk_bf16_f32 v96,v64,v65
v_cvt_pk_bf16_f32 v97,v66,v67
v_cvt_pk_bf16_f32 v98,v68,v69
v_cvt_pk_bf16_f32 v99,v70,v71
v_cvt_pk_bf16_f32 v100,v72,v73
v_cvt_pk_bf16_f32 v101,v74,v75
v_cvt_pk_bf16_f32 v102,v76,v77
v_cvt_pk_bf16_f32 v103,v78,v79
v_cvt_pk_bf16_f32 v104,v80,v81
v_cvt_pk_bf16_f32 v105,v82,v83
v_cvt_pk_bf16_f32 v106,v84,v85
v_cvt_pk_bf16_f32 v107,v86,v87
v_cvt_pk_bf16_f32 v108,v88,v89
v_cvt_pk_bf16_f32 v109,v90,v91
v_cvt_pk_bf16_f32 v110,v92,v93
v_cvt_pk_bf16_f32 v111,v94,v95
v_mov_b32 v170,0xff800000
s_lshl_b32 s91,s88,18
s_mov_b32 s90,64
L_s1_%=:
buffer_load_dwordx4 v[112:115],v164,s[20:23],s91 offen
buffer_load_dwordx4 v[116:119],v164,s[20:23],s91 offen offset:64
buffer_load_dwordx4 v[120:123],v164,s[20:23],s91 offen offset:128
buffer_load_dwordx4 v[124:127],v164,s[20:23],s91 offen offset:192
s_waitcnt vmcnt(0)
v_mfma_f32_16x16x32_bf16 v[144:147],v[112:115],v[96:99],0
v_mfma_f32_16x16x32_bf16 v[144:147],v[116:119],v[100:103],v[144:147]
v_mfma_f32_16x16x32_bf16 v[144:147],v[120:123],v[104:107],v[144:147]
v_mfma_f32_16x16x32_bf16 v[144:147],v[124:127],v[108:111],v[144:147]
s_nop 7
v_max3_f32 v170,v170,v144,v145
v_max3_f32 v170,v170,v146,v147
s_add_u32 s91,s91,4096
s_sub_u32 s90,s90,1
s_cmp_lg_u32 s90,0
s_cbranch_scc1 L_s1_%=
v_mov_b32 v171,v170
s_nop 1
v_permlane32_swap_b32 v170,v171
v_max_f32 v170,v170,v171
v_mov_b32 v171,v170
s_nop 1
v_permlane16_swap_b32 v170,v171
v_max_f32 v170,v170,v171
v_sub_f32 v152,0,v170
v_mov_b32 v156,0
v_sub_f32 v153,0,v170
v_mov_b32 v157,0
v_sub_f32 v154,0,v170
v_mov_b32 v158,0
v_sub_f32 v155,0,v170
v_mov_b32 v159,0
v_mov_b32 v64,0
v_mov_b32 v65,0
v_mov_b32 v66,0
v_mov_b32 v67,0
v_mov_b32 v68,0
v_mov_b32 v69,0
v_mov_b32 v70,0
v_mov_b32 v71,0
v_mov_b32 v72,0
v_mov_b32 v73,0
v_mov_b32 v74,0
v_mov_b32 v75,0
v_mov_b32 v76,0
v_mov_b32 v77,0
v_mov_b32 v78,0
v_mov_b32 v79,0
v_mov_b32 v80,0
v_mov_b32 v81,0
v_mov_b32 v82,0
v_mov_b32 v83,0
v_mov_b32 v84,0
v_mov_b32 v85,0
v_mov_b32 v86,0
v_mov_b32 v87,0
v_mov_b32 v88,0
v_mov_b32 v89,0
v_mov_b32 v90,0
v_mov_b32 v91,0
v_mov_b32 v92,0
v_mov_b32 v93,0
v_mov_b32 v94,0
v_mov_b32 v95,0
s_lshl_b32 s91,s88,18
s_mov_b32 s90,32
v_add_u32 v172,4096,v164
L_s2_%=:
buffer_load_dwordx4 v[112:115],v164,s[20:23],s91 offen
buffer_load_dwordx4 v[116:119],v164,s[20:23],s91 offen offset:64
buffer_load_dwordx4 v[120:123],v164,s[20:23],s91 offen offset:128
buffer_load_dwordx4 v[124:127],v164,s[20:23],s91 offen offset:192
buffer_load_dwordx4 v[128:131],v172,s[20:23],s91 offen
buffer_load_dwordx4 v[132:135],v172,s[20:23],s91 offen offset:64
buffer_load_dwordx4 v[136:139],v172,s[20:23],s91 offen offset:128
buffer_load_dwordx4 v[140:143],v172,s[20:23],s91 offen offset:192
s_waitcnt vmcnt(0)
v_mfma_f32_16x16x32_bf16 v[144:147],v[112:115],v[96:99],v[152:155]
v_mfma_f32_16x16x32_bf16 v[144:147],v[116:119],v[100:103],v[144:147]
v_mfma_f32_16x16x32_bf16 v[144:147],v[120:123],v[104:107],v[144:147]
v_mfma_f32_16x16x32_bf16 v[144:147],v[124:127],v[108:111],v[144:147]
v_mfma_f32_16x16x32_bf16 v[148:151],v[128:131],v[96:99],v[152:155]
v_mfma_f32_16x16x32_bf16 v[148:151],v[132:135],v[100:103],v[148:151]
v_mfma_f32_16x16x32_bf16 v[148:151],v[136:139],v[104:107],v[148:151]
v_mfma_f32_16x16x32_bf16 v[148:151],v[140:143],v[108:111],v[148:151]
s_nop 7
v_exp_f32 v144,v144
v_exp_f32 v145,v145
v_exp_f32 v146,v146
v_exp_f32 v147,v147
v_exp_f32 v148,v148
v_exp_f32 v149,v149
v_exp_f32 v150,v150
v_exp_f32 v151,v151
s_nop 0
v_cvt_pk_bf16_f32 v144,v144,v145
v_cvt_pk_bf16_f32 v145,v146,v147
v_cvt_pk_bf16_f32 v146,v148,v149
v_cvt_pk_bf16_f32 v147,v150,v151
buffer_load_ushort v112,v165,s[24:27],s91 offen
buffer_load_ushort v113,v165,s[24:27],s91 offen offset:256
buffer_load_ushort v114,v165,s[24:27],s91 offen offset:512
buffer_load_ushort v115,v165,s[24:27],s91 offen offset:768
buffer_load_ushort v116,v166,s[24:27],s91 offen
buffer_load_ushort v117,v166,s[24:27],s91 offen offset:256
buffer_load_ushort v118,v166,s[24:27],s91 offen offset:512
buffer_load_ushort v119,v166,s[24:27],s91 offen offset:768
buffer_load_ushort v120,v165,s[24:27],s91 offen offset:32
buffer_load_ushort v121,v165,s[24:27],s91 offen offset:288
buffer_load_ushort v122,v165,s[24:27],s91 offen offset:544
buffer_load_ushort v123,v165,s[24:27],s91 offen offset:800
)A"
R"A(buffer_load_ushort v124,v166,s[24:27],s91 offen offset:32
buffer_load_ushort v125,v166,s[24:27],s91 offen offset:288
buffer_load_ushort v126,v166,s[24:27],s91 offen offset:544
buffer_load_ushort v127,v166,s[24:27],s91 offen offset:800
buffer_load_ushort v128,v165,s[24:27],s91 offen offset:64
buffer_load_ushort v129,v165,s[24:27],s91 offen offset:320
buffer_load_ushort v130,v165,s[24:27],s91 offen offset:576
buffer_load_ushort v131,v165,s[24:27],s91 offen offset:832
buffer_load_ushort v132,v166,s[24:27],s91 offen offset:64
buffer_load_ushort v133,v166,s[24:27],s91 offen offset:320
buffer_load_ushort v134,v166,s[24:27],s91 offen offset:576
buffer_load_ushort v135,v166,s[24:27],s91 offen offset:832
buffer_load_ushort v136,v165,s[24:27],s91 offen offset:96
buffer_load_ushort v137,v165,s[24:27],s91 offen offset:352
buffer_load_ushort v138,v165,s[24:27],s91 offen offset:608
buffer_load_ushort v139,v165,s[24:27],s91 offen offset:864
buffer_load_ushort v140,v166,s[24:27],s91 offen offset:96
buffer_load_ushort v141,v166,s[24:27],s91 offen offset:352
buffer_load_ushort v142,v166,s[24:27],s91 offen offset:608
buffer_load_ushort v143,v166,s[24:27],s91 offen offset:864
s_waitcnt vmcnt(0)
v_lshl_or_b32 v112,v113,16,v112
v_lshl_or_b32 v113,v115,16,v114
v_lshl_or_b32 v114,v117,16,v116
v_lshl_or_b32 v115,v119,16,v118
v_lshl_or_b32 v120,v121,16,v120
v_lshl_or_b32 v121,v123,16,v122
v_lshl_or_b32 v122,v125,16,v124
v_lshl_or_b32 v123,v127,16,v126
v_lshl_or_b32 v128,v129,16,v128
v_lshl_or_b32 v129,v131,16,v130
v_lshl_or_b32 v130,v133,16,v132
v_lshl_or_b32 v131,v135,16,v134
v_lshl_or_b32 v136,v137,16,v136
v_lshl_or_b32 v137,v139,16,v138
v_lshl_or_b32 v138,v141,16,v140
v_lshl_or_b32 v139,v143,16,v142
s_nop 1
v_mfma_f32_16x16x32_bf16 v[64:67],v[112:115],v[144:147],v[64:67]
v_mfma_f32_16x16x32_bf16 v[68:71],v[120:123],v[144:147],v[68:71]
v_mfma_f32_16x16x32_bf16 v[72:75],v[128:131],v[144:147],v[72:75]
v_mfma_f32_16x16x32_bf16 v[76:79],v[136:139],v[144:147],v[76:79]
s_nop 7
buffer_load_ushort v112,v165,s[24:27],s91 offen offset:128
buffer_load_ushort v113,v165,s[24:27],s91 offen offset:384
buffer_load_ushort v114,v165,s[24:27],s91 offen offset:640
buffer_load_ushort v115,v165,s[24:27],s91 offen offset:896
buffer_load_ushort v116,v166,s[24:27],s91 offen offset:128
buffer_load_ushort v117,v166,s[24:27],s91 offen offset:384
buffer_load_ushort v118,v166,s[24:27],s91 offen offset:640
buffer_load_ushort v119,v166,s[24:27],s91 offen offset:896
buffer_load_ushort v120,v165,s[24:27],s91 offen offset:160
buffer_load_ushort v121,v165,s[24:27],s91 offen offset:416
buffer_load_ushort v122,v165,s[24:27],s91 offen offset:672
buffer_load_ushort v123,v165,s[24:27],s91 offen offset:928
buffer_load_ushort v124,v166,s[24:27],s91 offen offset:160
buffer_load_ushort v125,v166,s[24:27],s91 offen offset:416
buffer_load_ushort v126,v166,s[24:27],s91 offen offset:672
buffer_load_ushort v127,v166,s[24:27],s91 offen offset:928
buffer_load_ushort v128,v165,s[24:27],s91 offen offset:192
buffer_load_ushort v129,v165,s[24:27],s91 offen offset:448
buffer_load_ushort v130,v165,s[24:27],s91 offen offset:704
buffer_load_ushort v131,v165,s[24:27],s91 offen offset:960
buffer_load_ushort v132,v166,s[24:27],s91 offen offset:192
buffer_load_ushort v133,v166,s[24:27],s91 offen offset:448
buffer_load_ushort v134,v166,s[24:27],s91 offen offset:704
buffer_load_ushort v135,v166,s[24:27],s91 offen offset:960
buffer_load_ushort v136,v165,s[24:27],s91 offen offset:224
buffer_load_ushort v137,v165,s[24:27],s91 offen offset:480
buffer_load_ushort v138,v165,s[24:27],s91 offen offset:736
buffer_load_ushort v139,v165,s[24:27],s91 offen offset:992
buffer_load_ushort v140,v166,s[24:27],s91 offen offset:224
buffer_load_ushort v141,v166,s[24:27],s91 offen offset:480
buffer_load_ushort v142,v166,s[24:27],s91 offen offset:736
buffer_load_ushort v143,v166,s[24:27],s91 offen offset:992
s_waitcnt vmcnt(0)
v_lshl_or_b32 v112,v113,16,v112
v_lshl_or_b32 v113,v115,16,v114
v_lshl_or_b32 v114,v117,16,v116
v_lshl_or_b32 v115,v119,16,v118
v_lshl_or_b32 v120,v121,16,v120
v_lshl_or_b32 v121,v123,16,v122
v_lshl_or_b32 v122,v125,16,v124
v_lshl_or_b32 v123,v127,16,v126
v_lshl_or_b32 v128,v129,16,v128
v_lshl_or_b32 v129,v131,16,v130
v_lshl_or_b32 v130,v133,16,v132
v_lshl_or_b32 v131,v135,16,v134
v_lshl_or_b32 v136,v137,16,v136
v_lshl_or_b32 v137,v139,16,v138
v_lshl_or_b32 v138,v141,16,v140
v_lshl_or_b32 v139,v143,16,v142
s_nop 1
v_mfma_f32_16x16x32_bf16 v[80:83],v[112:115],v[144:147],v[80:83]
v_mfma_f32_16x16x32_bf16 v[84:87],v[120:123],v[144:147],v[84:87]
v_mfma_f32_16x16x32_bf16 v[88:91],v[128:131],v[144:147],v[88:91]
v_mfma_f32_16x16x32_bf16 v[92:95],v[136:139],v[144:147],v[92:95]
s_nop 7
v_mfma_f32_16x16x32_bf16 v[156:159],v[28:31],v[144:147],v[156:159]
s_nop 7
s_add_u32 s91,s91,8192
s_sub_u32 s90,s90,1
s_cmp_lg_u32 s90,0
s_cbranch_scc1 L_s2_%=
v_rcp_f32 v170,v156
s_nop 0
v_mul_f32 v64,v170,v64
v_mul_f32 v65,v170,v65
v_mul_f32 v66,v170,v66
v_mul_f32 v67,v170,v67
v_mul_f32 v68,v170,v68
v_mul_f32 v69,v170,v69
v_mul_f32 v70,v170,v70
v_mul_f32 v71,v170,v71
v_mul_f32 v72,v170,v72
v_mul_f32 v73,v170,v73
v_mul_f32 v74,v170,v74
v_mul_f32 v75,v170,v75
v_mul_f32 v76,v170,v76
v_mul_f32 v77,v170,v77
v_mul_f32 v78,v170,v78
v_mul_f32 v79,v170,v79
v_mul_f32 v80,v170,v80
v_mul_f32 v81,v170,v81
v_mul_f32 v82,v170,v82
v_mul_f32 v83,v170,v83
v_mul_f32 v84,v170,v84
v_mul_f32 v85,v170,v85
v_mul_f32 v86,v170,v86
v_mul_f32 v87,v170,v87
v_mul_f32 v88,v170,v88
v_mul_f32 v89,v170,v89
v_mul_f32 v90,v170,v90
v_mul_f32 v91,v170,v91
v_mul_f32 v92,v170,v92
v_mul_f32 v93,v170,v93
v_mul_f32 v94,v170,v94
v_mul_f32 v95,v170,v95
v_cvt_pk_bf16_f32 v96,v64,v65
v_cvt_pk_bf16_f32 v97,v66,v67
v_cvt_pk_bf16_f32 v98,v68,v69
v_cvt_pk_bf16_f32 v99,v70,v71
v_cvt_pk_bf16_f32 v100,v72,v73
v_cvt_pk_bf16_f32 v101,v74,v75
v_cvt_pk_bf16_f32 v102,v76,v77
v_cvt_pk_bf16_f32 v103,v78,v79
v_cvt_pk_bf16_f32 v104,v80,v81
v_cvt_pk_bf16_f32 v105,v82,v83
v_cvt_pk_bf16_f32 v106,v84,v85
v_cvt_pk_bf16_f32 v107,v86,v87
v_cvt_pk_bf16_f32 v108,v88,v89
v_cvt_pk_bf16_f32 v109,v90,v91
v_cvt_pk_bf16_f32 v110,v92,v93
v_cvt_pk_bf16_f32 v111,v94,v95
buffer_store_dwordx4 v[96:99],v169,s[36:39],s55 offen sc1
buffer_store_dwordx4 v[100:103],v169,s[36:39],s55 offen offset:1024 sc1
buffer_store_dwordx4 v[104:107],v169,s[36:39],s55 offen offset:2048 sc1
buffer_store_dwordx4 v[108:111],v169,s[36:39],s55 offen offset:3072 sc1
s_waitcnt vmcnt(0)
s_add_u32 s89,s89,1
s_cmp_lg_u32 s89,4
s_cbranch_scc1 L_sjb_%=
s_mov_b32 s98,s88
v_readlane_b32 s88,v173,0
v_readlane_b32 s89,v173,1
v_readlane_b32 s90,v173,2
v_readlane_b32 s91,v173,3
v_readlane_b32 s92,v173,4
v_readlane_b32 s93,v173,5
v_readlane_b32 s94,v173,6
v_readlane_b32 s95,v173,7
s_nop 4
s_mov_b32 s96,0
buffer_load_dwordx4 v[64:67],v6,s[56:59],s96 offen sc1
buffer_load_dwordx4 v[68:71],v6,s[56:59],s96 offen offset:1024 sc1
buffer_load_dwordx4 v[72:75],v6,s[56:59],s96 offen offset:2048 sc1
buffer_load_dwordx4 v[76:79],v6,s[56:59],s96 offen offset:3072 sc1
s_mov_b32 s96,4096
buffer_load_dwordx4 v[80:83],v6,s[56:59],s96 offen sc1
buffer_load_dwordx4 v[84:87],v6,s[56:59],s96 offen offset:1024 sc1
buffer_load_dwordx4 v[88:91],v6,s[56:59],s96 offen offset:2048 sc1
buffer_load_dwordx4 v[92:95],v6,s[56:59],s96 offen offset:3072 sc1
s_mov_b32 s96,8192
buffer_load_dwordx4 v[96:99],v6,s[56:59],s96 offen sc1
buffer_load_dwordx4 v[100:103],v6,s[56:59],s96 offen offset:1024 sc1
buffer_load_dwordx4 v[104:107],v6,s[56:59],s96 offen offset:2048 sc1
buffer_load_dwordx4 v[108:111],v6,s[56:59],s96 offen offset:3072 sc1
s_mov_b32 s96,12288
buffer_load_dwordx4 v[112:115],v6,s[56:59],s96 offen sc1
buffer_load_dwordx4 v[116:119],v6,s[56:59],s96 offen offset:1024 sc1
buffer_load_dwordx4 v[120:123],v6,s[56:59],s96 offen offset:2048 sc1
buffer_load_dwordx4 v[124:127],v6,s[56:59],s96 offen offset:3072 sc1
s_mov_b32 s96,16384
buffer_load_dwordx4 v[128:131],v6,s[56:59],s96 offen sc1
buffer_load_dwordx4 v[132:135],v6,s[56:59],s96 offen offset:1024 sc1
buffer_load_dwordx4 v[136:139],v6,s[56:59],s96 offen offset:2048 sc1
buffer_load_dwordx4 v[140:143],v6,s[56:59],s96 offen offset:3072 sc1
s_mov_b32 s96,20480
buffer_load_dwordx4 v[144:147],v6,s[56:59],s96 offen sc1
buffer_load_dwordx4 v[148:151],v6,s[56:59],s96 offen offset:1024 sc1
buffer_load_dwordx4 v[152:155],v6,s[56:59],s96 offen offset:2048 sc1
buffer_load_dwordx4 v[156:159],v6,s[56:59],s96 offen offset:3072 sc1
s_mov_b32 s96,24576
buffer_load_dwordx4 v[160:163],v6,s[56:59],s96 offen sc1
buffer_load_dwordx4 v[164:167],v6,s[56:59],s96 offen offset:1024 sc1
buffer_load_dwordx4 v[168:171],v6,s[56:59],s96 offen offset:2048 sc1
buffer_load_dwordx4 v[172:175],v6,s[56:59],s96 offen offset:3072 sc1
s_mov_b32 s96,28672
buffer_load_dwordx4 v[176:179],v6,s[56:59],s96 offen sc1
buffer_load_dwordx4 v[180:183],v6,s[56:59],s96 offen offset:1024 sc1
buffer_load_dwordx4 v[184:187],v6,s[56:59],s96 offen offset:2048 sc1
buffer_load_dwordx4 v[188:191],v6,s[56:59],s96 offen offset:3072 sc1
s_waitcnt vmcnt(0)
s_cmp_lg_u32 s98,0
s_cbranch_scc1 L_sr1_%=
buffer_load_dwordx4 v[64:67],v6,s[36:39],s84 offen sc1
buffer_load_dwordx4 v[68:71],v6,s[36:39],s84 offen offset:1024 sc1
buffer_load_dwordx4 v[72:75],v6,s[36:39],s84 offen offset:2048 sc1
buffer_load_dwordx4 v[76:79],v6,s[36:39],s84 offen offset:3072 sc1
buffer_load_dwordx4 v[80:83],v6,s[36:39],s85 offen sc1
buffer_load_dwordx4 v[84:87],v6,s[36:39],s85 offen offset:1024 sc1
buffer_load_dwordx4 v[88:91],v6,s[36:39],s85 offen offset:2048 sc1
buffer_load_dwordx4 v[92:95],v6,s[36:39],s85 offen offset:3072 sc1
buffer_load_dwordx4 v[96:99],v6,s[36:39],s86 offen sc1
buffer_load_dwordx4 v[100:103],v6,s[36:39],s86 offen offset:1024 sc1
buffer_load_dwordx4 v[104:107],v6,s[36:39],s86 offen offset:2048 sc1
buffer_load_dwordx4 v[108:111],v6,s[36:39],s86 offen offset:3072 sc1
buffer_load_dwordx4 v[112:115],v6,s[36:39],s87 offen sc1
buffer_load_dwordx4 v[116:119],v6,s[36:39],s87 offen offset:1024 sc1
buffer_load_dwordx4 v[120:123],v6,s[36:39],s87 offen offset:2048 sc1
buffer_load_dwordx4 v[124:127],v6,s[36:39],s87 offen offset:3072 sc1
s_waitcnt vmcnt(0)
s_nop 7
s_branch L_sret0_%=
L_sr1_%=:
buffer_load_dwordx4 v[136:139],v6,s[36:39],s84 offen sc1
buffer_load_dwordx4 v[140:143],v6,s[36:39],s84 offen offset:1024 sc1
buffer_load_dwordx4 v[152:155],v6,s[36:39],s84 offen offset:2048 sc1
buffer_load_dwordx4 v[156:159],v6,s[36:39],s84 offen offset:3072 sc1
buffer_load_dwordx4 v[168:171],v6,s[36:39],s85 offen sc1
buffer_load_dwordx4 v[172:175],v6,s[36:39],s85 offen offset:1024 sc1
buffer_load_dwordx4 v[184:187],v6,s[36:39],s85 offen offset:2048 sc1
buffer_load_dwordx4 v[188:191],v6,s[36:39],s85 offen offset:3072 sc1
buffer_load_dwordx4 v[200:203],v6,s[36:39],s86 offen sc1
buffer_load_dwordx4 v[204:207],v6,s[36:39],s86 offen offset:1024 sc1
buffer_load_dwordx4 v[216:219],v6,s[36:39],s86 offen offset:2048 sc1
buffer_load_dwordx4 v[220:223],v6,s[36:39],s86 offen offset:3072 sc1
buffer_load_dwordx4 v[232:235],v6,s[36:39],s87 offen sc1
buffer_load_dwordx4 v[236:239],v6,s[36:39],s87 offen offset:1024 sc1
buffer_load_dwordx4 v[248:251],v6,s[36:39],s87 offen offset:2048 sc1
buffer_load_dwordx4 v[252:255],v6,s[36:39],s87 offen offset:3072 sc1
s_waitcnt vmcnt(0)
s_nop 7
s_branch L_sret1_%=
L_end_%=:
)A"
    :: "s"(karg), "s"(blk), "s"(wid)
    : "v0", "v1", "v2", "v3", "v4", "v5", "v6", "v7", "v8", "v9", "v10", "v11", "v12", "v13", "v14", "v15", "v16", "v17", "v18", "v19", "v20", "v21", "v22", "v23", "v24", "v25", "v26", "v27", "v28", "v29", "v30", "v31", "v32", "v33", "v34", "v35", "v36", "v37", "v38", "v39", "v40", "v41", "v42", "v43", "v44", "v45", "v46", "v47", "v48", "v49", "v50", "v51", "v52", "v53", "v54", "v55", "v56", "v57", "v58", "v59", "v60", "v61", "v62", "v63", "v64", "v65", "v66", "v67", "v68", "v69", "v70", "v71", "v72", "v73", "v74", "v75", "v76", "v77", "v78", "v79", "v80", "v81", "v82", "v83", "v84", "v85", "v86", "v87", "v88", "v89", "v90", "v91", "v92", "v93", "v94", "v95", "v96", "v97", "v98", "v99", "v100", "v101", "v102", "v103", "v104", "v105", "v106", "v107", "v108", "v109", "v110", "v111", "v112", "v113", "v114", "v115", "v116", "v117", "v118", "v119", "v120", "v121", "v122", "v123", "v124", "v125", "v126", "v127", "v128", "v129", "v130", "v131", "v132", "v133", "v134", "v135", "v136", "v137", "v138", "v139", "v140", "v141", "v142", "v143", "v144", "v145", "v146", "v147", "v148", "v149", "v150", "v151", "v152", "v153", "v154", "v155", "v156", "v157", "v158", "v159", "v160", "v161", "v162", "v163", "v164", "v165", "v166", "v167", "v168", "v169", "v170", "v171", "v172", "v173", "v174", "v175", "v176", "v177", "v178", "v179", "v180", "v181", "v182", "v183", "v184", "v185", "v186", "v187", "v188", "v189", "v190", "v191", "v192", "v193", "v194", "v195", "v196", "v197", "v198", "v199", "v200", "v201", "v202", "v203", "v204", "v205", "v206", "v207", "v208", "v209", "v210", "v211", "v212", "v213", "v214", "v215", "v216", "v217", "v218", "v219", "v220", "v221", "v222", "v223", "v224", "v225", "v226", "v227", "v228", "v229", "v230", "v231", "v232", "v233", "v234", "v235", "v236", "v237", "v238", "v239", "v240", "v241", "v242", "v243", "v244", "v245", "v246", "v247", "v248", "v249", "v250", "v251", "v252", "v253", "v254", "v255", "a0", "a1", "a2", "a3", "a4", "a5", "a6", "a7", "a8", "a9", "a10", "a11", "a12", "a13", "a14", "a15", "a16", "a17", "a18", "a19", "a20", "a21", "a22", "a23", "a24", "a25", "a26", "a27", "a28", "a29", "a30", "a31", "a32", "a33", "a34", "a35", "a36", "a37", "a38", "a39", "a40", "a41", "a42", "a43", "a44", "a45", "a46", "a47", "a48", "a49", "a50", "a51", "a52", "a53", "a54", "a55", "a56", "a57", "a58", "a59", "a60", "a61", "a62", "a63", "a64", "a65", "a66", "a67", "a68", "a69", "a70", "a71", "a72", "a73", "a74", "a75", "a76", "a77", "a78", "a79", "a80", "a81", "a82", "a83", "a84", "a85", "a86", "a87", "a88", "a89", "a90", "a91", "a92", "a93", "a94", "a95", "a96", "a97", "a98", "a99", "a100", "a101", "a102", "a103", "a104", "a105", "a106", "a107", "a108", "a109", "a110", "a111", "a112", "a113", "a114", "a115", "a116", "a117", "a118", "a119", "a120", "a121", "a122", "a123", "a124", "a125", "a126", "a127", "a128", "a129", "a130", "a131", "a132", "a133", "a134", "a135", "a136", "a137", "a138", "a139", "a140", "a141", "a142", "a143", "a144", "a145", "a146", "a147", "a148", "a149", "a150", "a151", "a152", "a153", "a154", "a155", "a156", "a157", "a158", "a159", "a160", "a161", "a162", "a163", "a164", "a165", "a166", "a167", "a168", "a169", "a170", "a171", "a172", "a173", "a174", "a175", "a176", "a177", "a178", "a179", "a180", "a181", "a182", "a183", "a184", "a185", "a186", "a187", "a188", "a189", "a190", "a191", "a192", "a193", "a194", "a195", "a196", "a197", "a198", "a199", "a200", "a201", "a202", "a203", "a204", "a205", "a206", "a207", "a208", "a209", "a210", "a211", "a212", "a213", "a214", "a215", "a216", "a217", "a218", "a219", "a220", "a221", "a222", "a223", "a224", "a225", "a226", "a227", "a228", "a229", "a230", "a231", "a232", "a233", "a234", "a235", "a236", "a237", "a238", "a239", "a240", "a241", "a242", "a243", "a244", "a245", "a246", "a247", "a248", "a249", "a250", "a251", "a252", "a253", "a254", "a255", "s4", "s5", "s6", "s7", "s8", "s9", "s10", "s11", "s12", "s13", "s14", "s15", "s16", "s17", "s18", "s19", "s20", "s21", "s22", "s23", "s24", "s25", "s26", "s27", "s28", "s29", "s30", "s31", "s32", "s33", "s34", "s35", "s36", "s37", "s38", "s39", "s40", "s41", "s42", "s43", "s44", "s45", "s46", "s47", "s48", "s49", "s50", "s51", "s52", "s53", "s54", "s55", "s56", "s57", "s58", "s59", "s60", "s61", "s62", "s63", "s64", "s65", "s66", "s67", "s68", "s69", "s70", "s71", "s72", "s73", "s74", "s75", "s76", "s77", "s78", "s79", "s80", "s81", "s82", "s83", "s84", "s85", "s86", "s87", "s88", "s89", "s90", "s91", "s92", "s93", "s94", "s95", "s96", "s97", "s98", "s99", "s100", "s101", "vcc", "scc", "memory");
#endif
}

extern "C" void kernel_launch(void* const* d_in, const int* in_sizes, int n_in,
                              void* d_out, int out_size, void* d_ws, size_t ws_size,
                              hipStream_t stream) {
  (void)hipFuncSetAttribute((const void*)qattn_kernel, hipFuncAttributeMaxDynamicSharedMemorySize, LDS_ATT);
  qattn_kernel<<<256, 256, LDS_ATT, stream>>>(d_in[0], d_in[1], d_in[2], d_in[3], d_ws, d_in[4], d_out);
}
```

```cpp
#include <hip/hip_runtime.h>
#include <hip/hip_bf16.h>

constexpr int LDS_ATT = 163840;

__global__ __launch_bounds__(256, 1) void qattn_kernel(const void* __restrict__ x, const void* __restrict__ kc, const void* __restrict__ vc,
                                                       const void* __restrict__ wq, void* __restrict__ ws, const void* __restrict__ wo,
                                                       void* __restrict__ out) {
#if defined(__HIP_DEVICE_COMPILE__)
  const auto karg = __builtin_amdgcn_kernarg_segment_ptr();
  const unsigned blk = blockIdx.x;
  const unsigned wid = __builtin_amdgcn_readfirstlane(threadIdx.x >> 6);
  asm volatile(
R"A(s_memtime s[72:73]
s_memrealtime s[74:75]
s_load_dwordx8 s[4:11],%0,0x0
s_load_dwordx4 s[12:15],%0,0x20
s_load_dwordx2 s[16:17],%0,0x30
s_mov_b32 s40,%2
s_mov_b32 s43,%1
s_lshr_b32 s45,s43,3
v_mbcnt_lo_u32_b32 v12,-1,0
v_mbcnt_hi_u32_b32 v12,-1,v12
s_getpc_b64 s[88:89]
L_pc_%=:
s_and_b32 s88,s88,0xffffff80
s_and_b32 s89,s89,0xffff
s_mov_b32 s90,L_fast_end_%=-L_pc_%=
s_mov_b32 s91,0x20000
s_lshl_b32 s44,s45,2
s_add_u32 s44,s44,s40
s_mul_i32 s44,s44,896
v_lshlrev_b32 v67,7,v12
s_mov_b64 exec,127
buffer_load_dword v19,v67,s[88:91],s44 offen
s_mov_b64 exec,-1
s_and_b32 s70,s43,3
s_and_b32 s71,s45,3
s_lshl_b32 s42,s70,2
s_add_u32 s42,s42,s71
s_and_b32 s41,s43,4
s_lshl_b32 s41,s41,1
s_lshr_b32 s46,s45,2
s_add_u32 s41,s41,s46
s_lshl_b32 s56,s42,9
s_mov_b32 s57,0
s_lshl_b32 s59,s40,13
s_add_u32 s58,s59,65536
v_and_b32 v64,3,v12
v_bfe_u32 v65,v12,2,2
v_lshrrev_b32 v66,4,v12
s_lshl_b32 s44,s40,17
v_lshlrev_b32 v75,13,v65
v_lshl_add_u32 v75,v66,6,v75
v_lshl_add_u32 v75,v64,4,v75
v_add_u32 v75,s44,v75
v_add_u32 v56,0,v75
v_add_u32 v57,256,v75
v_add_u32 v58,32768,v75
v_add_u32 v59,33024,v75
v_add_u32 v60,65536,v75
v_add_u32 v61,65792,v75
v_add_u32 v62,98304,v75
v_add_u32 v63,98560,v75
s_waitcnt lgkmcnt(0)
s_mov_b64 s[32:33],s[10:11]
s_and_b32 s33,s33,0xffff
s_mov_b32 s34,5242880
s_mov_b32 s35,0x20000
s_add_u32 m0,s58,1024
s_nop 0
buffer_load_dwordx4 v57,s[32:35],s56 offen lds
s_add_u32 m0,s58,3072
s_nop 0
buffer_load_dwordx4 v59,s[32:35],s56 offen lds
s_add_u32 m0,s58,5120
s_nop 0
buffer_load_dwordx4 v61,s[32:35],s56 offen lds
s_add_u32 m0,s58,7168
s_nop 0
buffer_load_dwordx4 v63,s[32:35],s56 offen lds
v_lshrrev_b32 v75,3,v12
s_lshl_b32 s44,s40,6
v_add_u32 v75,s44,v75
v_mul_u32_u24 v75,1280,v75
v_and_b32 v76,7,v12
s_add_u32 s84,s70,1
s_and_b32 s84,s84,3
s_mul_i32 s84,s84,20480
s_add_u32 s84,s84,4096
s_add_u32 s85,s70,2
s_and_b32 s85,s85,3
s_mul_i32 s85,s85,20480
s_add_u32 s85,s85,2048
s_add_u32 s86,s70,3
s_and_b32 s86,s86,3
s_mul_i32 s86,s86,20480
s_add_u32 s86,s86,4096
s_add_u32 s87,s70,4
s_and_b32 s87,s87,3
s_mul_i32 s87,s87,20480
s_add_u32 s87,s87,2048
v_or_b32 v77,0,v66
v_xor_b32 v77,v77,v76
v_lshl_add_u32 v77,v77,4,v75
v_add_u32 v48,s84,v77
v_add_u32 v50,s85,v77
v_add_u32 v52,s86,v77
v_add_u32 v54,s87,v77
v_or_b32 v77,4,v66
v_xor_b32 v77,v77,v76
v_lshl_add_u32 v77,v77,4,v75
v_add_u32 v77,9216,v77
v_add_u32 v49,s84,v77
v_add_u32 v51,s85,v77
v_add_u32 v53,s86,v77
v_add_u32 v55,s87,v77
s_mul_i32 s44,s41,307200
s_mov_b64 s[28:29],s[4:5]
s_add_u32 s28,s28,s44
s_addc_u32 s29,s29,0
s_sub_u32 s28,s28,4096
s_subb_u32 s29,s29,0
s_and_b32 s29,s29,0xffff
s_mov_b32 s30,311296
s_mov_b32 s31,0x20000
s_add_u32 m0,s59,0
s_nop 0
buffer_load_dwordx4 v48,s[28:31],s57 offen lds
buffer_load_dwordx4 v49,s[28:31],s57 offen offset:1024 lds
buffer_load_dwordx4 v50,s[28:31],s57 offen offset:2048 lds
buffer_load_dwordx4 v51,s[28:31],s57 offen offset:3072 lds
s_add_u32 m0,s59,4096
s_nop 0
buffer_load_dwordx4 v52,s[28:31],s57 offen lds
buffer_load_dwordx4 v53,s[28:31],s57 offen offset:1024 lds
buffer_load_dwordx4 v54,s[28:31],s57 offen offset:2048 lds
buffer_load_dwordx4 v55,s[28:31],s57 offen offset:3072 lds
s_add_u32 s57,s57,128
s_xor_b32 s59,s59,131072
s_add_u32 m0,s58,0
s_nop 0
buffer_load_dwordx4 v56,s[32:35],s56 offen lds
s_add_u32 m0,s58,2048
s_nop 0
buffer_load_dwordx4 v58,s[32:35],s56 offen lds
s_add_u32 m0,s58,4096
s_nop 0
buffer_load_dwordx4 v60,s[32:35],s56 offen lds
s_add_u32 m0,s58,6144
s_nop 0
buffer_load_dwordx4 v62,s[32:35],s56 offen lds
s_add_u32 s56,s56,524288
s_add_u32 s58,s58,32768
s_cmp_ge_u32 s58,131072
s_cselect_b32 s62,98304,0
s_sub_u32 s58,s58,s62
s_add_u32 m0,s58,0
s_nop 0
buffer_load_dwordx4 v56,s[32:35],s56 offen lds
s_add_u32 m0,s58,1024
s_nop 0
buffer_load_dwordx4 v57,s[32:35],s56 offen lds
s_add_u32 m0,s58,2048
s_nop 0
buffer_load_dwordx4 v58,s[32:35],s56 offen lds
s_add_u32 m0,s58,3072
s_nop 0
buffer_load_dwordx4 v59,s[32:35],s56 offen lds
s_add_u32 m0,s58,4096
s_nop 0
buffer_load_dwordx4 v60,s[32:35],s56 offen lds
s_add_u32 m0,s58,5120
s_nop 0
buffer_load_dwordx4 v61,s[32:35],s56 offen lds
s_add_u32 m0,s58,6144
s_nop 0
buffer_load_dwordx4 v62,s[32:35],s56 offen lds
s_add_u32 m0,s58,7168
s_nop 0
buffer_load_dwordx4 v63,s[32:35],s56 offen lds
s_add_u32 s56,s56,524288
s_add_u32 s58,s58,32768
s_cmp_ge_u32 s58,131072
s_cselect_b32 s62,98304,0
s_sub_u32 s58,s58,s62
s_mov_b32 s60,65536
v_lshlrev_b32 v6,4,v12
v_lshlrev_b32 v72,12,v66
v_lshl_add_u32 v72,v65,6,v72
v_lshl_add_u32 v23,v64,4,v72
v_add_u32 v22,65536,v23
v_and_b32 v74,15,v12
v_lshrrev_b32 v76,1,v74
v_lshlrev_b32 v75,7,v74
s_lshl_b32 s44,s40,13
v_add_u32 v75,s44,v75
v_or_b32 v77,0,v66
v_xor_b32 v77,v77,v76
v_lshl_add_u32 v20,v77,4,v75
v_or_b32 v77,4,v66
v_xor_b32 v77,v77,v76
v_lshl_add_u32 v21,v77,4,v75
s_waitcnt vmcnt(12)
s_nop 0
s_memtime s[64:65]
s_memrealtime s[66:67]
s_bfe_u32 s68,%1,327683
s_waitcnt lgkmcnt(0)
s_sub_u32 s64,s64,s72
s_sub_u32 s66,s66,s74
s_mul_i32 s64,s64,10
s_mul_i32 s66,s66,215
s_cmp_lt_u32 s64,s66
s_cselect_b32 s95,64,0
s_cbranch_scc1 L_se1_%=
L_sg1_%=:
s_cmp_eq_u32 s68,0
s_cbranch_scc1 L_se1_%=
s_sleep 4
s_sub_u32 s68,s68,1
s_branch L_sg1_%=
L_se1_%=:
s_barrier
ds_read_b128 a[192:195],v20
ds_read_b64_tr_b16 a[128:129],v22 offset:1024
ds_read_b64_tr_b16 a[130:131],v22 offset:3072
ds_read_b128 a[196:199],v21
ds_read_b64_tr_b16 a[132:133],v22 offset:17408
ds_read_b64_tr_b16 a[134:135],v22 offset:19456
ds_read_b128 a[204:207],v21 offset:2048
ds_read_b128 a[200:203],v20 offset:2048
ds_read_b128 a[208:211],v20 offset:4096
ds_read_b128 a[212:215],v21 offset:4096
ds_read_b128 a[220:223],v21 offset:6144
ds_read_b128 a[216:219],v20 offset:6144
ds_read_b64_tr_b16 a[136:137],v22 offset:1032
ds_read_b64_tr_b16 a[138:139],v22 offset:3080
s_waitcnt lgkmcnt(11)
v_mfma_f32_16x16x32_bf16 v[128:131],a[128:131],a[192:195],0
s_add_u32 m0,s59,0
ds_read_b64_tr_b16 a[140:141],v22 offset:17416
ds_read_b64_tr_b16 a[142:143],v22 offset:19464
s_waitcnt lgkmcnt(10)
v_mfma_f32_16x16x32_bf16 v[128:131],a[132:135],a[196:199],v[128:131]
s_waitcnt lgkmcnt(9)
v_mfma_f32_16x16x32_bf16 v[136:139],a[132:135],a[204:207],0
s_waitcnt lgkmcnt(8)
v_mfma_f32_16x16x32_bf16 v[136:139],a[128:131],a[200:203],v[136:139]
s_waitcnt lgkmcnt(7)
v_mfma_f32_16x16x32_bf16 v[144:147],a[128:131],a[208:211],0
s_waitcnt lgkmcnt(6)
buffer_load_dwordx4 v48,s[28:31],s57 offen lds
v_mfma_f32_16x16x32_bf16 v[144:147],a[132:135],a[212:215],v[144:147]
s_waitcnt lgkmcnt(5)
v_mfma_f32_16x16x32_bf16 v[152:155],a[132:135],a[220:223],0
s_waitcnt lgkmcnt(4)
v_mfma_f32_16x16x32_bf16 v[152:155],a[128:131],a[216:219],v[152:155]
s_waitcnt lgkmcnt(2)
ds_read_b64_tr_b16 a[144:145],v22 offset:1280
ds_read_b64_tr_b16 a[146:147],v22 offset:3328
v_mfma_f32_16x16x32_bf16 v[132:135],a[136:139],a[192:195],0
s_waitcnt lgkmcnt(2)
ds_read_b64_tr_b16 a[148:149],v22 offset:17664
ds_read_b64_tr_b16 a[150:151],v22 offset:19712
v_mfma_f32_16x16x32_bf16 v[132:135],a[140:143],a[196:199],v[132:135]
buffer_load_dwordx4 v49,s[28:31],s57 offen offset:1024 lds
v_mfma_f32_16x16x32_bf16 v[140:143],a[140:143],a[204:207],0
v_mfma_f32_16x16x32_bf16 v[140:143],a[136:139],a[200:203],v[140:143]
v_mfma_f32_16x16x32_bf16 v[148:151],a[136:139],a[208:211],0
v_mfma_f32_16x16x32_bf16 v[148:151],a[140:143],a[212:215],v[148:151]
v_mfma_f32_16x16x32_bf16 v[156:159],a[140:143],a[220:223],0
buffer_load_dwordx4 v50,s[28:31],s57 offen offset:2048 lds
v_mfma_f32_16x16x32_bf16 v[156:159],a[136:139],a[216:219],v[156:159]
s_waitcnt lgkmcnt(2)
ds_read_b64_tr_b16 a[152:153],v22 offset:1288
ds_read_b64_tr_b16 a[154:155],v22 offset:3336
v_mfma_f32_16x16x32_bf16 v[160:163],a[144:147],a[192:195],0
s_waitcnt lgkmcnt(2)
ds_read_b64_tr_b16 a[156:157],v22 offset:17672
ds_read_b64_tr_b16 a[158:159],v22 offset:19720
v_mfma_f32_16x16x32_bf16 v[160:163],a[148:151],a[196:199],v[160:163]
v_mfma_f32_16x16x32_bf16 v[168:171],a[148:151],a[204:207],0
v_mfma_f32_16x16x32_bf16 v[168:171],a[144:147],a[200:203],v[168:171]
buffer_load_dwordx4 v51,s[28:31],s57 offen offset:3072 lds
v_mfma_f32_16x16x32_bf16 v[176:179],a[144:147],a[208:211],0
s_add_u32 m0,s59,4096
v_mfma_f32_16x16x32_bf16 v[176:179],a[148:151],a[212:215],v[176:179]
v_mfma_f32_16x16x32_bf16 v[184:187],a[148:151],a[220:223],0
v_mfma_f32_16x16x32_bf16 v[184:187],a[144:147],a[216:219],v[184:187]
s_waitcnt lgkmcnt(2)
v_mfma_f32_16x16x32_bf16 v[164:167],a[152:155],a[192:195],0
s_waitcnt lgkmcnt(0)
buffer_load_dwordx4 v52,s[28:31],s57 offen lds
ds_read_b64_tr_b16 a[160:161],v22 offset:1536
ds_read_b64_tr_b16 a[162:163],v22 offset:3584
v_mfma_f32_16x16x32_bf16 v[164:167],a[156:159],a[196:199],v[164:167]
ds_read_b64_tr_b16 a[164:165],v22 offset:17920
ds_read_b64_tr_b16 a[166:167],v22 offset:19968
v_mfma_f32_16x16x32_bf16 v[172:175],a[156:159],a[204:207],0
v_mfma_f32_16x16x32_bf16 v[172:175],a[152:155],a[200:203],v[172:175]
v_mfma_f32_16x16x32_bf16 v[180:183],a[152:155],a[208:211],0
v_mfma_f32_16x16x32_bf16 v[180:183],a[156:159],a[212:215],v[180:183]
buffer_load_dwordx4 v53,s[28:31],s57 offen offset:1024 lds
v_mfma_f32_16x16x32_bf16 v[188:191],a[156:159],a[220:223],0
s_waitcnt vmcnt(14)
v_mfma_f32_16x16x32_bf16 v[188:191],a[152:155],a[216:219],v[188:191]
s_barrier
ds_read_b64_tr_b16 a[168:169],v22 offset:1544
ds_read_b64_tr_b16 a[170:171],v22 offset:3592
s_waitcnt lgkmcnt(4)
v_mfma_f32_16x16x32_bf16 v[192:195],a[160:163],a[192:195],0
s_waitcnt lgkmcnt(2)
ds_read_b64_tr_b16 a[172:173],v22 offset:17928
ds_read_b64_tr_b16 a[174:175],v22 offset:19976
v_mfma_f32_16x16x32_bf16 v[192:195],a[164:167],a[196:199],v[192:195]
buffer_load_dwordx4 v54,s[28:31],s57 offen offset:2048 lds
v_mfma_f32_16x16x32_bf16 v[200:203],a[164:167],a[204:207],0
s_lshl_b32 s44,s41,13
s_add_u32 s44,s44,104857600
s_mov_b64 s[64:65],s[12:13]
v_mfma_f32_16x16x32_bf16 v[200:203],a[160:163],a[200:203],v[200:203]
s_add_u32 s64,s64,s44
s_addc_u32 s65,s65,0
s_and_b32 s65,s65,0xffff
v_mfma_f32_16x16x32_bf16 v[208:211],a[160:163],a[208:211],0
s_mov_b32 s66,8192
s_mov_b32 s67,0x20000
v_mov_b32 v14,4096
v_mfma_f32_16x16x32_bf16 v[208:211],a[164:167],a[212:215],v[208:211]
s_mov_b64 s[72:73],s[14:15]
buffer_load_dwordx4 v55,s[28:31],s57 offen offset:3072 lds
v_mfma_f32_16x16x32_bf16 v[216:219],a[164:167],a[220:223],0
s_add_u32 m0,s58,0
s_and_b32 s73,s73,0xffff
ds_read_b64_tr_b16 a[176:177],v22 offset:1792
v_mfma_f32_16x16x32_bf16 v[216:219],a[160:163],a[216:219],v[216:219]
s_mov_b32 s74,5242880
s_mov_b32 s75,0x20000
ds_read_b64_tr_b16 a[178:179],v22 offset:3840
s_waitcnt lgkmcnt(4)
v_mfma_f32_16x16x32_bf16 v[196:199],a[168:171],a[192:195],0
s_waitcnt lgkmcnt(2)
ds_read_b64_tr_b16 a[180:181],v22 offset:18176
ds_read_b64_tr_b16 a[182:183],v22 offset:20224
v_mfma_f32_16x16x32_bf16 v[196:199],a[172:175],a[196:199],v[196:199]
s_lshl_b32 s83,%1,2
buffer_load_dwordx4 v56,s[32:35],s56 offen lds
v_mfma_f32_16x16x32_bf16 v[204:207],a[172:175],a[204:207],0
s_add_u32 m0,s58,1024
v_lshlrev_b32 v15,3,v6
v_add_u32 v16,128,v15
v_mfma_f32_16x16x32_bf16 v[204:207],a[168:171],a[200:203],v[204:207]
s_add_u32 s83,s83,s40
ds_read_b64_tr_b16 a[184:185],v22 offset:1800
v_lshrrev_b32 v16,10,v16
v_mfma_f32_16x16x32_bf16 v[212:215],a[168:171],a[208:211],0
s_mul_i32 s83,s83,5120
ds_read_b64_tr_b16 a[186:187],v22 offset:3848
v_lshl_add_u32 v15,v16,24,v15
v_mfma_f32_16x16x32_bf16 v[212:215],a[172:175],a[212:215],v[212:215]
s_lshl_b32 s43,s41,5
buffer_load_dwordx4 v57,s[32:35],s56 offen lds
v_mfma_f32_16x16x32_bf16 v[220:223],a[172:175],a[220:223],0
s_lshl_b32 s44,s42,1
s_add_u32 s43,s43,s44
ds_read_b64_tr_b16 a[188:189],v22 offset:18184
v_mfma_f32_16x16x32_bf16 v[220:223],a[168:171],a[216:219],v[220:223]
s_add_u32 m0,s58,2048
s_lshl_b32 s44,s43,18
ds_read_b64_tr_b16 a[190:191],v22 offset:20232
s_waitcnt lgkmcnt(6)
v_mfma_f32_16x16x32_bf16 v[224:227],a[176:179],a[192:195],0
s_mov_b64 s[20:21],s[6:7]
s_mov_b32 s22,524288
ds_read_b64_tr_b16 a[128:129],v22
s_waitcnt lgkmcnt(5)
v_mfma_f32_16x16x32_bf16 v[224:227],a[180:183],a[196:199],v[224:227]
s_add_u32 s20,s20,s44
s_addc_u32 s21,s21,0
ds_read_b64_tr_b16 a[130:131],v22 offset:2048
v_mfma_f32_16x16x32_bf16 v[232:235],a[180:183],a[204:207],0
)A"
R"A(s_and_b32 s21,s21,0xffff
buffer_load_dwordx4 v58,s[32:35],s56 offen lds
v_mfma_f32_16x16x32_bf16 v[232:235],a[176:179],a[200:203],v[232:235]
s_mov_b32 s23,0x20000
s_mov_b64 s[24:25],s[8:9]
ds_read_b64_tr_b16 a[132:133],v22 offset:16384
v_mfma_f32_16x16x32_bf16 v[240:243],a[176:179],a[208:211],0
s_add_u32 m0,s58,3072
s_mov_b32 s26,524288
ds_read_b64_tr_b16 a[134:135],v22 offset:18432
v_mfma_f32_16x16x32_bf16 v[240:243],a[180:183],a[212:215],v[240:243]
s_add_u32 s24,s24,s44
s_addc_u32 s25,s25,0
ds_read_b64_tr_b16 a[136:137],v22 offset:8
v_mfma_f32_16x16x32_bf16 v[248:251],a[180:183],a[220:223],0
s_and_b32 s25,s25,0xffff
s_mov_b32 s27,0x20000
ds_read_b64_tr_b16 a[138:139],v22 offset:2056
v_mfma_f32_16x16x32_bf16 v[248:251],a[176:179],a[216:219],v[248:251]
s_lshl_b32 s44,s43,16
buffer_load_dwordx4 v59,s[32:35],s56 offen lds
s_waitcnt lgkmcnt(6)
v_mfma_f32_16x16x32_bf16 v[228:231],a[184:187],a[192:195],0
s_mov_b64 s[36:37],s[12:13]
s_mov_b32 s38,131072
ds_read_b64_tr_b16 a[140:141],v22 offset:16392
v_mfma_f32_16x16x32_bf16 v[228:231],a[188:191],a[196:199],v[228:231]
s_add_u32 m0,s58,4096
s_mov_b32 s39,0x20000
ds_read_b64_tr_b16 a[142:143],v22 offset:18440
v_mfma_f32_16x16x32_bf16 v[236:239],a[188:191],a[204:207],0
s_add_u32 s36,s36,s44
s_addc_u32 s37,s37,0
ds_read_b64_tr_b16 a[144:145],v22 offset:256
v_mfma_f32_16x16x32_bf16 v[236:239],a[184:187],a[200:203],v[236:239]
s_and_b32 s37,s37,0xffff
s_lshl_b32 s51,s40,12
ds_read_b64_tr_b16 a[146:147],v22 offset:2304
v_mfma_f32_16x16x32_bf16 v[244:247],a[184:187],a[208:211],0
s_add_u32 s52,s51,16384
buffer_load_dwordx4 v60,s[32:35],s56 offen lds
v_mfma_f32_16x16x32_bf16 v[244:247],a[188:191],a[212:215],v[244:247]
s_xor_b32 s59,s59,131072
s_mul_i32 s47,s41,5
ds_read_b64_tr_b16 a[148:149],v22 offset:16640
v_mfma_f32_16x16x32_bf16 v[252:255],a[188:191],a[220:223],0
s_add_u32 m0,s58,5120
s_add_u32 s47,s47,s42
ds_read_b64_tr_b16 a[150:151],v22 offset:18688
v_mfma_f32_16x16x32_bf16 v[252:255],a[184:187],a[216:219],v[252:255]
s_and_b32 s47,s47,15
s_add_u32 s57,s57,128
ds_read_b64_tr_b16 a[152:153],v22 offset:264
s_waitcnt lgkmcnt(7)
v_mfma_f32_16x16x32_bf16 a[0:3],a[128:131],a[192:195],0
s_lshl_b32 s47,s47,14
s_mov_b32 s54,0
ds_read_b64_tr_b16 a[154:155],v22 offset:2312
v_mfma_f32_16x16x32_bf16 a[0:3],a[132:135],a[196:199],a[0:3]
s_lshl_b32 s55,s40,14
buffer_load_dwordx4 v61,s[32:35],s56 offen lds
v_mfma_f32_16x16x32_bf16 a[32:35],a[132:135],a[204:207],0
s_mov_b32 s61,0x3e0293ee
s_mov_b32 s63,0x5d800000
ds_read_b64_tr_b16 a[156:157],v22 offset:16648
v_mfma_f32_16x16x32_bf16 a[32:35],a[128:131],a[200:203],a[32:35]
s_add_u32 m0,s58,6144
s_lshl_b32 s44,s40,2
ds_read_b64_tr_b16 a[158:159],v22 offset:18696
v_mfma_f32_16x16x32_bf16 a[64:67],a[128:131],a[208:211],0
s_add_u32 s44,s44,s70
s_mov_b32 s46,0xff800000
ds_read_b64_tr_b16 a[160:161],v22 offset:512
v_mfma_f32_16x16x32_bf16 a[64:67],a[132:135],a[212:215],a[64:67]
s_cmp_eq_u32 s44,14
s_cselect_b32 s18,0x7149f2ca,s46
ds_read_b64_tr_b16 a[162:163],v22 offset:2560
v_mfma_f32_16x16x32_bf16 a[96:99],a[132:135],a[220:223],0
s_add_u32 s60,s60,32768
buffer_load_dwordx4 v62,s[32:35],s56 offen lds
v_mfma_f32_16x16x32_bf16 a[96:99],a[128:131],a[216:219],a[96:99]
s_cmp_eq_u32 s44,13
s_cselect_b32 s19,0x7149f2ca,s46
ds_read_b64_tr_b16 a[164:165],v22 offset:16896
v_mfma_f32_16x16x32_bf16 a[4:7],a[136:139],a[192:195],0
s_add_u32 m0,s58,7168
ds_read_b64_tr_b16 a[166:167],v22 offset:18944
v_lshlrev_b32 v70,8,v65
s_waitcnt lgkmcnt(6)
v_mfma_f32_16x16x32_bf16 a[4:7],a[140:143],a[196:199],a[4:7]
s_cmp_eq_u32 s44,12
s_cselect_b32 s100,0x7149f2ca,s46
ds_read_b64_tr_b16 a[168:169],v22 offset:520
v_mfma_f32_16x16x32_bf16 a[36:39],a[140:143],a[204:207],0
s_cmp_eq_u32 s44,15
s_cselect_b32 s101,0x7149f2ca,s46
ds_read_b64_tr_b16 a[170:171],v22 offset:2568
v_mfma_f32_16x16x32_bf16 a[36:39],a[136:139],a[200:203],a[36:39]
s_add_u32 s58,s58,32768
buffer_load_dwordx4 v63,s[32:35],s56 offen lds
v_mfma_f32_16x16x32_bf16 a[68:71],a[136:139],a[208:211],0
s_add_u32 s56,s56,524288
ds_read_b64_tr_b16 a[172:173],v22 offset:16904
v_lshl_add_u32 v70,v66,6,v70
v_mfma_f32_16x16x32_bf16 a[68:71],a[140:143],a[212:215],a[68:71]
s_add_u32 m0,s59,0
ds_read_b64_tr_b16 a[174:175],v22 offset:18952
v_add_u32 v70,s51,v70
v_mfma_f32_16x16x32_bf16 a[100:103],a[140:143],a[220:223],0
s_mul_i32 s81,s70,1310720
ds_read_b64_tr_b16 a[176:177],v22 offset:768
v_lshl_add_u32 v4,v64,4,v70
v_mfma_f32_16x16x32_bf16 a[100:103],a[136:139],a[216:219],a[100:103]
s_mul_i32 s44,s71,320
ds_read_b64_tr_b16 a[178:179],v22 offset:2816
v_xor_b32 v71,2,v64
v_mfma_f32_16x16x32_bf16 a[8:11],a[144:147],a[192:195],0
s_add_u32 s81,s81,s44
buffer_load_dwordx4 v48,s[28:31],s57 offen lds
v_mfma_f32_16x16x32_bf16 a[8:11],a[148:151],a[196:199],a[8:11]
s_mul_i32 s82,s40,10240
ds_read_b64_tr_b16 a[180:181],v22 offset:17152
v_lshl_add_u32 v5,v71,4,v70
v_mfma_f32_16x16x32_bf16 a[40:43],a[148:151],a[204:207],0
s_mul_i32 s44,s40,40960
ds_read_b64_tr_b16 a[182:183],v22 offset:19200
v_lshlrev_b32 v72,10,v65
v_mfma_f32_16x16x32_bf16 a[40:43],a[144:147],a[200:203],a[40:43]
s_lshr_b32 s92,%1,3
ds_read_b64_tr_b16 a[184:185],v22 offset:776
v_lshl_add_u32 v72,v64,6,v72
v_mfma_f32_16x16x32_bf16 a[72:75],a[144:147],a[208:211],0
s_lshl_b32 s92,s92,2
ds_read_b64_tr_b16 a[186:187],v22 offset:2824
v_and_b32 v73,2,v65
v_mfma_f32_16x16x32_bf16 a[72:75],a[148:151],a[212:215],a[72:75]
s_add_u32 s92,s92,s40
buffer_load_dwordx4 v49,s[28:31],s57 offen offset:1024 lds
v_mfma_f32_16x16x32_bf16 a[104:107],a[148:151],a[220:223],0
s_lshl_b32 s92,s92,7
ds_read_b64_tr_b16 a[188:189],v22 offset:17160
v_xor_b32 v73,v73,v66
v_mfma_f32_16x16x32_bf16 a[104:107],a[144:147],a[216:219],a[104:107]
s_add_u32 s93,s92,L_tailseg_%=-L_pc_%=
ds_read_b64_tr_b16 a[190:191],v22 offset:19208
v_lshl_add_u32 v72,v73,4,v72
v_mfma_f32_16x16x32_bf16 a[12:15],a[152:155],a[192:195],0
s_waitcnt lgkmcnt(6)
v_add_u32 v1,98304,v72
v_lshlrev_b32 v72,10,v66
v_mfma_f32_16x16x32_bf16 a[12:15],a[156:159],a[196:199],a[12:15]
v_lshl_add_u32 v72,v65,6,v72
v_lshl_add_u32 v72,v64,3,v72
v_mfma_f32_16x16x32_bf16 a[44:47],a[156:159],a[204:207],0
buffer_load_dwordx4 v50,s[28:31],s57 offen offset:2048 lds
v_mfma_f32_16x16x32_bf16 a[44:47],a[152:155],a[200:203],a[44:47]
v_add_u32 v72,114688,v72
v_and_b32 v73,1,v66
v_mfma_f32_16x16x32_bf16 a[76:79],a[152:155],a[208:211],0
s_cmp_eq_u32 s60,131072
s_cselect_b32 s60,32768,s60
v_lshl_add_u32 v2,v73,5,v72
v_mfma_f32_16x16x32_bf16 a[76:79],a[156:159],a[212:215],a[76:79]
v_xor_b32 v73,1,v73
v_add_u32 v22,s60,v23
v_mfma_f32_16x16x32_bf16 a[108:111],a[156:159],a[220:223],0
s_add_u32 s60,s60,32768
v_xor_b32 v20,131072,v20
v_xor_b32 v21,131072,v21
v_mfma_f32_16x16x32_bf16 a[108:111],a[152:155],a[216:219],a[108:111]
s_waitcnt vmcnt(11) lgkmcnt(0)
buffer_load_dwordx4 v51,s[28:31],s57 offen offset:3072 lds
v_mfma_f32_16x16x32_bf16 a[16:19],a[160:163],a[192:195],0
s_barrier
s_add_u32 m0,s59,4096
ds_read_b128 a[224:227],v20
v_mfma_f32_16x16x32_bf16 a[16:19],a[164:167],a[196:199],a[16:19]
s_xor_b32 s59,s59,131072
ds_read_b64_tr_b16 a[128:129],v22 offset:1024
v_lshl_add_u32 v3,v73,5,v72
v_mfma_f32_16x16x32_bf16 a[48:51],a[164:167],a[204:207],0
ds_read_b64_tr_b16 a[130:131],v22 offset:3072
v_mov_b32 v73,0x3f803f80
v_mfma_f32_16x16x32_bf16 a[48:51],a[160:163],a[200:203],a[48:51]
buffer_load_dwordx4 v52,s[28:31],s57 offen lds
v_mfma_f32_16x16x32_bf16 a[80:83],a[160:163],a[208:211],0
s_cmp_ge_u32 s58,131072
s_cselect_b32 s62,98304,0
ds_read_b128 a[228:231],v21
v_mfma_f32_16x16x32_bf16 a[80:83],a[164:167],a[212:215],a[80:83]
s_sub_u32 s58,s58,s62
ds_read_b64_tr_b16 a[132:133],v22 offset:17408
v_add_u32 v70,0,v12
v_mfma_f32_16x16x32_bf16 a[112:115],a[164:167],a[220:223],0
ds_read_b64_tr_b16 a[134:135],v22 offset:19456
v_mul_u32_u24 v71,3277,v70
v_mfma_f32_16x16x32_bf16 a[112:115],a[160:163],a[216:219],a[112:115]
ds_read_b128 a[232:235],v20 offset:2048
v_lshrrev_b32 v71,16,v71
v_mfma_f32_16x16x32_bf16 a[20:23],a[168:171],a[192:195],0
buffer_load_dwordx4 v53,s[28:31],s57 offen offset:1024 lds
v_mfma_f32_16x16x32_bf16 a[20:23],a[172:175],a[196:199],a[20:23]
ds_read_b128 a[236:239],v21 offset:2048
v_mul_u32_u24 v72,20,v71
v_mfma_f32_16x16x32_bf16 a[52:55],a[172:175],a[204:207],0
ds_read_b128 a[240:243],v20 offset:4096
v_sub_u32 v72,v70,v72
v_mfma_f32_16x16x32_bf16 a[52:55],a[168:171],a[200:203],a[52:55]
ds_read_b128 a[244:247],v21 offset:4096
v_add_u32 v70,64,v12
v_mfma_f32_16x16x32_bf16 a[84:87],a[168:171],a[208:211],0
buffer_load_dword v13,v14,s[64:67],0 offen sc1
v_mfma_f32_16x16x32_bf16 a[84:87],a[172:175],a[212:215],a[84:87]
ds_read_b128 a[248:251],v20 offset:6144
v_lshlrev_b32 v24,10,v6
v_mfma_f32_16x16x32_bf16 a[116:119],a[172:175],a[220:223],0
s_cmp_eq_u32 s60,131072
s_cselect_b32 s60,32768,s60
ds_read_b128 a[252:255],v21 offset:6144
v_mfma_f32_16x16x32_bf16 a[116:119],a[168:171],a[216:219],a[116:119]
ds_read_b64_tr_b16 a[136:137],v22 offset:1032
v_xor_b32 v20,131072,v20
v_mfma_f32_16x16x32_bf16 a[24:27],a[176:179],a[192:195],0
buffer_load_dwordx4 v54,s[28:31],s57 offen offset:2048 lds
v_mfma_f32_16x16x32_bf16 a[24:27],a[180:183],a[196:199],a[24:27]
ds_read_b64_tr_b16 a[138:139],v22 offset:3080
v_xor_b32 v21,131072,v21
v_mfma_f32_16x16x32_bf16 a[56:59],a[180:183],a[204:207],0
ds_read_b64_tr_b16 a[140:141],v22 offset:17416
v_mfma_f32_16x16x32_bf16 a[56:59],a[176:179],a[200:203],a[56:59]
ds_read_b64_tr_b16 a[142:143],v22 offset:19464
v_mfma_f32_16x16x32_bf16 a[88:91],a[176:179],a[208:211],0
buffer_load_dwordx4 v55,s[28:31],s57 offen offset:3072 lds
v_mfma_f32_16x16x32_bf16 a[88:91],a[180:183],a[212:215],a[88:91]
s_add_u32 s57,s57,128
ds_read_b64_tr_b16 a[144:145],v22 offset:1280
v_mfma_f32_16x16x32_bf16 a[120:123],a[180:183],a[220:223],0
ds_read_b64_tr_b16 a[146:147],v22 offset:3328
v_mfma_f32_16x16x32_bf16 a[120:123],a[176:179],a[216:219],a[120:123]
ds_read_b64_tr_b16 a[148:149],v22 offset:17664
v_mfma_f32_16x16x32_bf16 a[28:31],a[184:187],a[192:195],0
ds_read_b64_tr_b16 a[150:151],v22 offset:19712
v_mfma_f32_16x16x32_bf16 a[28:31],a[188:191],a[196:199],a[28:31]
ds_read_b64_tr_b16 a[152:153],v22 offset:1288
ds_read_b64_tr_b16 a[154:155],v22 offset:3336
ds_read_b64_tr_b16 a[156:157],v22 offset:17672
ds_read_b64_tr_b16 a[158:159],v22 offset:19720
v_mfma_f32_16x16x32_bf16 a[60:63],a[188:191],a[204:207],0
v_mfma_f32_16x16x32_bf16 a[60:63],a[184:187],a[200:203],a[60:63]
v_cmp_gt_u32 vcc,2,v64
s_nop 1
v_cndmask_b32 v28,0,v73,vcc
v_mfma_f32_16x16x32_bf16 a[92:95],a[184:187],a[208:211],0
v_and_b32 v73,1,v64
v_mul_u32_u24 v73,0xaaaa,v73
v_mfma_f32_16x16x32_bf16 a[92:95],a[188:191],a[212:215],a[92:95]
v_add_u32 v73,0x4444,v73
v_lshl_or_b32 v27,v73,16,v73
v_mfma_f32_16x16x32_bf16 a[124:127],a[188:191],a[220:223],0
s_add_u32 m0,s58,0
v_mov_b32 v29,v28
v_mov_b32 v30,v28
v_mfma_f32_16x16x32_bf16 a[124:127],a[184:187],a[216:219],a[124:127]
s_waitcnt lgkmcnt(5)
v_mov_b32 v31,v28
v_bfe_u32 v73,v71,2,1
v_mfma_f32_16x16x32_bf16 v[128:131],a[128:131],a[224:227],v[128:131]
v_lshlrev_b32 v73,1,v73
v_xor_b32 v72,v72,v73
v_mfma_f32_16x16x32_bf16 v[128:131],a[132:135],a[228:231],v[128:131]
buffer_load_dwordx4 v56,s[32:35],s56 offen lds
v_mfma_f32_16x16x32_bf16 v[136:139],a[132:135],a[236:239],v[136:139]
v_mul_u32_u24 v71,1280,v71
v_lshl_add_u32 v71,v72,4,v71
v_mfma_f32_16x16x32_bf16 v[136:139],a[128:131],a[232:235],v[136:139]
s_add_u32 m0,s58,1024
v_add_u32 v71,s44,v71
v_subrev_u32 v7,0,v71
v_mfma_f32_16x16x32_bf16 v[144:147],a[128:131],a[240:243],v[144:147]
ds_read_b64_tr_b16 a[160:161],v22 offset:1536
v_mul_u32_u24 v71,3277,v70
v_mfma_f32_16x16x32_bf16 v[144:147],a[132:135],a[244:247],v[144:147]
ds_read_b64_tr_b16 a[162:163],v22 offset:3584
v_lshrrev_b32 v71,16,v71
v_mfma_f32_16x16x32_bf16 v[152:155],a[132:135],a[252:255],v[152:155]
buffer_load_dwordx4 v57,s[32:35],s56 offen lds
v_mfma_f32_16x16x32_bf16 v[152:155],a[128:131],a[248:251],v[152:155]
ds_read_b64_tr_b16 a[164:165],v22 offset:17920
v_mul_u32_u24 v72,20,v71
v_mfma_f32_16x16x32_bf16 v[132:135],a[136:139],a[224:227],v[132:135]
ds_read_b64_tr_b16 a[166:167],v22 offset:19968
v_sub_u32 v72,v70,v72
v_mfma_f32_16x16x32_bf16 v[132:135],a[140:143],a[228:231],v[132:135]
)A"
R"A(s_add_u32 m0,s58,2048
v_bfe_u32 v73,v71,2,1
v_lshlrev_b32 v73,1,v73
v_mfma_f32_16x16x32_bf16 v[140:143],a[140:143],a[236:239],v[140:143]
ds_read_b64_tr_b16 a[168:169],v22 offset:1544
v_xor_b32 v72,v72,v73
v_mfma_f32_16x16x32_bf16 v[140:143],a[136:139],a[232:235],v[140:143]
ds_read_b64_tr_b16 a[170:171],v22 offset:3592
v_mul_u32_u24 v71,1280,v71
v_mfma_f32_16x16x32_bf16 v[148:151],a[136:139],a[240:243],v[148:151]
buffer_load_dwordx4 v58,s[32:35],s56 offen lds
v_mfma_f32_16x16x32_bf16 v[148:151],a[140:143],a[244:247],v[148:151]
ds_read_b64_tr_b16 a[172:173],v22 offset:17928
v_lshl_add_u32 v71,v72,4,v71
v_mfma_f32_16x16x32_bf16 v[156:159],a[140:143],a[252:255],v[156:159]
ds_read_b64_tr_b16 a[174:175],v22 offset:19976
v_add_u32 v71,s44,v71
v_mfma_f32_16x16x32_bf16 v[156:159],a[136:139],a[248:251],v[156:159]
s_add_u32 m0,s58,3072
v_subrev_u32 v8,1024,v71
v_add_u32 v70,128,v12
v_mfma_f32_16x16x32_bf16 v[160:163],a[144:147],a[224:227],v[160:163]
s_waitcnt lgkmcnt(4)
ds_read_b64_tr_b16 a[176:177],v22 offset:1792
v_mul_u32_u24 v71,3277,v70
v_mfma_f32_16x16x32_bf16 v[160:163],a[148:151],a[228:231],v[160:163]
ds_read_b64_tr_b16 a[178:179],v22 offset:3840
v_lshrrev_b32 v71,16,v71
v_mfma_f32_16x16x32_bf16 v[168:171],a[148:151],a[236:239],v[168:171]
buffer_load_dwordx4 v59,s[32:35],s56 offen lds
v_mfma_f32_16x16x32_bf16 v[168:171],a[144:147],a[232:235],v[168:171]
ds_read_b64_tr_b16 a[180:181],v22 offset:18176
v_mul_u32_u24 v72,20,v71
v_mfma_f32_16x16x32_bf16 v[176:179],a[144:147],a[240:243],v[176:179]
s_add_u32 m0,s58,4096
ds_read_b64_tr_b16 a[182:183],v22 offset:20224
v_sub_u32 v72,v70,v72
v_mfma_f32_16x16x32_bf16 v[176:179],a[148:151],a[244:247],v[176:179]
v_bfe_u32 v73,v71,2,1
v_lshlrev_b32 v73,1,v73
v_mfma_f32_16x16x32_bf16 v[184:187],a[148:151],a[252:255],v[184:187]
ds_read_b64_tr_b16 a[184:185],v22 offset:1800
v_xor_b32 v72,v72,v73
v_mfma_f32_16x16x32_bf16 v[184:187],a[144:147],a[248:251],v[184:187]
buffer_load_dwordx4 v60,s[32:35],s56 offen lds
v_mfma_f32_16x16x32_bf16 v[164:167],a[152:155],a[224:227],v[164:167]
ds_read_b64_tr_b16 a[186:187],v22 offset:3848
v_mul_u32_u24 v71,1280,v71
v_mfma_f32_16x16x32_bf16 v[164:167],a[156:159],a[228:231],v[164:167]
ds_read_b64_tr_b16 a[188:189],v22 offset:18184
v_lshl_add_u32 v71,v72,4,v71
v_mfma_f32_16x16x32_bf16 v[172:175],a[156:159],a[236:239],v[172:175]
s_add_u32 m0,s58,5120
ds_read_b64_tr_b16 a[190:191],v22 offset:20232
v_add_u32 v71,s44,v71
v_mfma_f32_16x16x32_bf16 v[172:175],a[152:155],a[232:235],v[172:175]
v_subrev_u32 v9,2048,v71
v_add_u32 v70,192,v12
v_mfma_f32_16x16x32_bf16 v[180:183],a[152:155],a[240:243],v[180:183]
v_mul_u32_u24 v71,3277,v70
v_lshrrev_b32 v71,16,v71
v_mfma_f32_16x16x32_bf16 v[180:183],a[156:159],a[244:247],v[180:183]
buffer_load_dwordx4 v61,s[32:35],s56 offen lds
v_mfma_f32_16x16x32_bf16 v[188:191],a[156:159],a[252:255],v[188:191]
ds_read_b64_tr_b16 a[128:129],v22
v_mul_u32_u24 v72,20,v71
v_mfma_f32_16x16x32_bf16 v[188:191],a[152:155],a[248:251],v[188:191]
ds_read_b64_tr_b16 a[130:131],v22 offset:2048
v_sub_u32 v72,v70,v72
v_mfma_f32_16x16x32_bf16 v[192:195],a[160:163],a[224:227],v[192:195]
s_add_u32 m0,s58,6144
ds_read_b64_tr_b16 a[132:133],v22 offset:16384
v_bfe_u32 v73,v71,2,1
v_mfma_f32_16x16x32_bf16 v[192:195],a[164:167],a[228:231],v[192:195]
ds_read_b64_tr_b16 a[134:135],v22 offset:18432
v_lshlrev_b32 v73,1,v73
v_mfma_f32_16x16x32_bf16 v[200:203],a[164:167],a[236:239],v[200:203]
v_xor_b32 v72,v72,v73
v_mul_u32_u24 v71,1280,v71
v_mfma_f32_16x16x32_bf16 v[200:203],a[160:163],a[232:235],v[200:203]
buffer_load_dwordx4 v62,s[32:35],s56 offen lds
v_mfma_f32_16x16x32_bf16 v[208:211],a[160:163],a[240:243],v[208:211]
ds_read_b64_tr_b16 a[136:137],v22 offset:8
v_lshl_add_u32 v71,v72,4,v71
v_mfma_f32_16x16x32_bf16 v[208:211],a[164:167],a[244:247],v[208:211]
s_add_u32 m0,s58,7168
ds_read_b64_tr_b16 a[138:139],v22 offset:2056
v_add_u32 v71,s44,v71
v_mfma_f32_16x16x32_bf16 v[216:219],a[164:167],a[252:255],v[216:219]
s_add_u32 s58,s58,32768
ds_read_b64_tr_b16 a[140:141],v22 offset:16392
v_subrev_u32 v10,3072,v71
v_mfma_f32_16x16x32_bf16 v[216:219],a[160:163],a[248:251],v[216:219]
s_waitcnt lgkmcnt(5)
ds_read_b64_tr_b16 a[142:143],v22 offset:18440
v_add_u32 v70,256,v12
v_mfma_f32_16x16x32_bf16 v[196:199],a[168:171],a[224:227],v[196:199]
buffer_load_dwordx4 v63,s[32:35],s56 offen lds
v_mfma_f32_16x16x32_bf16 v[196:199],a[172:175],a[228:231],v[196:199]
s_add_u32 s56,s56,524288
v_mul_u32_u24 v71,3277,v70
v_lshrrev_b32 v71,16,v71
v_mfma_f32_16x16x32_bf16 v[204:207],a[172:175],a[236:239],v[204:207]
ds_read_b64_tr_b16 a[144:145],v22 offset:256
v_mul_u32_u24 v72,20,v71
v_mfma_f32_16x16x32_bf16 v[204:207],a[168:171],a[232:235],v[204:207]
s_add_u32 m0,s59,0
ds_read_b64_tr_b16 a[146:147],v22 offset:2304
v_sub_u32 v72,v70,v72
v_mfma_f32_16x16x32_bf16 v[212:215],a[168:171],a[240:243],v[212:215]
ds_read_b64_tr_b16 a[148:149],v22 offset:16640
v_bfe_u32 v73,v71,2,1
v_mfma_f32_16x16x32_bf16 v[212:215],a[172:175],a[244:247],v[212:215]
ds_read_b64_tr_b16 a[150:151],v22 offset:18688
v_lshlrev_b32 v73,1,v73
v_mfma_f32_16x16x32_bf16 v[220:223],a[172:175],a[252:255],v[220:223]
buffer_load_dwordx4 v48,s[28:31],s57 offen lds
v_mfma_f32_16x16x32_bf16 v[220:223],a[168:171],a[248:251],v[220:223]
v_xor_b32 v72,v72,v73
v_mul_u32_u24 v71,1280,v71
v_mfma_f32_16x16x32_bf16 v[224:227],a[176:179],a[224:227],v[224:227]
v_lshl_add_u32 v71,v72,4,v71
v_add_u32 v71,s44,v71
v_mfma_f32_16x16x32_bf16 v[224:227],a[180:183],a[228:231],v[224:227]
ds_read_b64_tr_b16 a[152:153],v22 offset:264
v_subrev_u32 v11,0,v71
v_mfma_f32_16x16x32_bf16 v[232:235],a[180:183],a[236:239],v[232:235]
s_cmp_ge_u32 s58,131072
s_cselect_b32 s62,98304,0
ds_read_b64_tr_b16 a[154:155],v22 offset:2312
v_mfma_f32_16x16x32_bf16 v[232:235],a[176:179],a[232:235],v[232:235]
s_sub_u32 s58,s58,s62
ds_read_b64_tr_b16 a[156:157],v22 offset:16648
v_mfma_f32_16x16x32_bf16 v[240:243],a[176:179],a[240:243],v[240:243]
buffer_load_dwordx4 v49,s[28:31],s57 offen offset:1024 lds
v_mfma_f32_16x16x32_bf16 v[240:243],a[180:183],a[244:247],v[240:243]
ds_read_b64_tr_b16 a[158:159],v22 offset:18696
v_mfma_f32_16x16x32_bf16 v[248:251],a[180:183],a[252:255],v[248:251]
ds_read_b64_tr_b16 a[160:161],v22 offset:512
v_mfma_f32_16x16x32_bf16 v[248:251],a[176:179],a[248:251],v[248:251]
ds_read_b64_tr_b16 a[162:163],v22 offset:2560
v_mfma_f32_16x16x32_bf16 v[228:231],a[184:187],a[224:227],v[228:231]
ds_read_b64_tr_b16 a[164:165],v22 offset:16896
v_mfma_f32_16x16x32_bf16 v[228:231],a[188:191],a[228:231],v[228:231]
buffer_load_dwordx4 v50,s[28:31],s57 offen offset:2048 lds
v_mfma_f32_16x16x32_bf16 v[236:239],a[188:191],a[236:239],v[236:239]
ds_read_b64_tr_b16 a[166:167],v22 offset:18944
v_mfma_f32_16x16x32_bf16 v[236:239],a[184:187],a[232:235],v[236:239]
ds_read_b64_tr_b16 a[168:169],v22 offset:520
v_mfma_f32_16x16x32_bf16 v[244:247],a[184:187],a[240:243],v[244:247]
ds_read_b64_tr_b16 a[170:171],v22 offset:2568
v_mfma_f32_16x16x32_bf16 v[244:247],a[188:191],a[244:247],v[244:247]
ds_read_b64_tr_b16 a[172:173],v22 offset:16904
v_mfma_f32_16x16x32_bf16 v[252:255],a[188:191],a[252:255],v[252:255]
ds_read_b64_tr_b16 a[174:175],v22 offset:18952
v_mfma_f32_16x16x32_bf16 v[252:255],a[184:187],a[248:251],v[252:255]
buffer_load_dwordx4 v51,s[28:31],s57 offen offset:3072 lds
v_mfma_f32_16x16x32_bf16 a[0:3],a[128:131],a[224:227],a[0:3]
s_waitcnt lgkmcnt(5)
ds_read_b64_tr_b16 a[176:177],v22 offset:768
v_mfma_f32_16x16x32_bf16 a[0:3],a[132:135],a[228:231],a[0:3]
ds_read_b64_tr_b16 a[178:179],v22 offset:2816
v_mfma_f32_16x16x32_bf16 a[32:35],a[132:135],a[236:239],a[32:35]
s_add_u32 m0,s59,4096
s_xor_b32 s59,s59,131072
ds_read_b64_tr_b16 a[180:181],v22 offset:17152
v_mfma_f32_16x16x32_bf16 a[32:35],a[128:131],a[232:235],a[32:35]
ds_read_b64_tr_b16 a[182:183],v22 offset:19200
v_mfma_f32_16x16x32_bf16 a[64:67],a[128:131],a[240:243],a[64:67]
ds_read_b64_tr_b16 a[184:185],v22 offset:776
v_mfma_f32_16x16x32_bf16 a[64:67],a[132:135],a[244:247],a[64:67]
buffer_load_dwordx4 v52,s[28:31],s57 offen lds
v_mfma_f32_16x16x32_bf16 a[96:99],a[132:135],a[252:255],a[96:99]
ds_read_b64_tr_b16 a[186:187],v22 offset:2824
v_mfma_f32_16x16x32_bf16 a[96:99],a[128:131],a[248:251],a[96:99]
ds_read_b64_tr_b16 a[188:189],v22 offset:17160
v_mfma_f32_16x16x32_bf16 a[4:7],a[136:139],a[224:227],a[4:7]
ds_read_b64_tr_b16 a[190:191],v22 offset:19208
v_add_u32 v22,s60,v23
v_mfma_f32_16x16x32_bf16 a[4:7],a[140:143],a[228:231],a[4:7]
v_mfma_f32_16x16x32_bf16 a[36:39],a[140:143],a[236:239],a[36:39]
buffer_load_dwordx4 v53,s[28:31],s57 offen offset:1024 lds
v_mfma_f32_16x16x32_bf16 a[36:39],a[136:139],a[232:235],a[36:39]
v_mfma_f32_16x16x32_bf16 a[68:71],a[136:139],a[240:243],a[68:71]
v_mfma_f32_16x16x32_bf16 a[68:71],a[140:143],a[244:247],a[68:71]
v_mfma_f32_16x16x32_bf16 a[100:103],a[140:143],a[252:255],a[100:103]
v_mfma_f32_16x16x32_bf16 a[100:103],a[136:139],a[248:251],a[100:103]
v_mfma_f32_16x16x32_bf16 a[8:11],a[144:147],a[224:227],a[8:11]
buffer_load_dwordx4 v54,s[28:31],s57 offen offset:2048 lds
v_mfma_f32_16x16x32_bf16 a[8:11],a[148:151],a[228:231],a[8:11]
v_mfma_f32_16x16x32_bf16 a[40:43],a[148:151],a[236:239],a[40:43]
v_mfma_f32_16x16x32_bf16 a[40:43],a[144:147],a[232:235],a[40:43]
v_mfma_f32_16x16x32_bf16 a[72:75],a[144:147],a[240:243],a[72:75]
v_mfma_f32_16x16x32_bf16 a[72:75],a[148:151],a[244:247],a[72:75]
v_mfma_f32_16x16x32_bf16 a[104:107],a[148:151],a[252:255],a[104:107]
buffer_load_dwordx4 v55,s[28:31],s57 offen offset:3072 lds
v_mfma_f32_16x16x32_bf16 a[104:107],a[144:147],a[248:251],a[104:107]
s_add_u32 s57,s57,128
v_mfma_f32_16x16x32_bf16 a[12:15],a[152:155],a[224:227],a[12:15]
v_mfma_f32_16x16x32_bf16 a[12:15],a[156:159],a[228:231],a[12:15]
v_mfma_f32_16x16x32_bf16 a[44:47],a[156:159],a[236:239],a[44:47]
v_mfma_f32_16x16x32_bf16 a[44:47],a[152:155],a[232:235],a[44:47]
v_mfma_f32_16x16x32_bf16 a[76:79],a[152:155],a[240:243],a[76:79]
v_mfma_f32_16x16x32_bf16 a[76:79],a[156:159],a[244:247],a[76:79]
v_mfma_f32_16x16x32_bf16 a[108:111],a[156:159],a[252:255],a[108:111]
v_mfma_f32_16x16x32_bf16 a[108:111],a[152:155],a[248:251],a[108:111]
s_waitcnt vmcnt(16) lgkmcnt(0)
v_mfma_f32_16x16x32_bf16 a[16:19],a[160:163],a[224:227],a[16:19]
s_barrier
ds_read_b128 a[192:195],v20
v_mfma_f32_16x16x32_bf16 a[16:19],a[164:167],a[228:231],a[16:19]
ds_read_b128 a[196:199],v21
v_mfma_f32_16x16x32_bf16 a[48:51],a[164:167],a[236:239],a[48:51]
ds_read_b128 a[200:203],v20 offset:2048
v_mfma_f32_16x16x32_bf16 a[48:51],a[160:163],a[232:235],a[48:51]
ds_read_b128 a[204:207],v21 offset:2048
v_mfma_f32_16x16x32_bf16 a[80:83],a[160:163],a[240:243],a[80:83]
ds_read_b128 a[208:211],v20 offset:4096
v_mfma_f32_16x16x32_bf16 a[80:83],a[164:167],a[244:247],a[80:83]
ds_read_b128 a[212:215],v21 offset:4096
v_mfma_f32_16x16x32_bf16 a[112:115],a[164:167],a[252:255],a[112:115]
ds_read_b128 a[216:219],v20 offset:6144
v_mfma_f32_16x16x32_bf16 a[112:115],a[160:163],a[248:251],a[112:115]
ds_read_b128 a[220:223],v21 offset:6144
v_mfma_f32_16x16x32_bf16 a[20:23],a[168:171],a[224:227],a[20:23]
ds_read_b64_tr_b16 a[128:129],v22 offset:1024
v_mfma_f32_16x16x32_bf16 a[20:23],a[172:175],a[228:231],a[20:23]
ds_read_b64_tr_b16 a[130:131],v22 offset:3072
v_mfma_f32_16x16x32_bf16 a[52:55],a[172:175],a[236:239],a[52:55]
ds_read_b64_tr_b16 a[132:133],v22 offset:17408
v_mfma_f32_16x16x32_bf16 a[52:55],a[168:171],a[232:235],a[52:55]
ds_read_b64_tr_b16 a[134:135],v22 offset:19456
v_mfma_f32_16x16x32_bf16 a[84:87],a[168:171],a[240:243],a[84:87]
ds_read_b64_tr_b16 a[136:137],v22 offset:1032
v_mfma_f32_16x16x32_bf16 a[84:87],a[172:175],a[244:247],a[84:87]
ds_read_b64_tr_b16 a[138:139],v22 offset:3080
v_mfma_f32_16x16x32_bf16 a[116:119],a[172:175],a[252:255],a[116:119]
ds_read_b64_tr_b16 a[140:141],v22 offset:17416
v_mfma_f32_16x16x32_bf16 a[116:119],a[168:171],a[248:251],a[116:119]
ds_read_b64_tr_b16 a[142:143],v22 offset:19464
v_mfma_f32_16x16x32_bf16 a[24:27],a[176:179],a[224:227],a[24:27]
ds_read_b64_tr_b16 a[144:145],v22 offset:1280
v_mfma_f32_16x16x32_bf16 a[24:27],a[180:183],a[228:231],a[24:27]
ds_read_b64_tr_b16 a[146:147],v22 offset:3328
)A"
R"A(v_mfma_f32_16x16x32_bf16 a[56:59],a[180:183],a[236:239],a[56:59]
ds_read_b64_tr_b16 a[148:149],v22 offset:17664
v_mfma_f32_16x16x32_bf16 a[56:59],a[176:179],a[232:235],a[56:59]
ds_read_b64_tr_b16 a[150:151],v22 offset:19712
v_mfma_f32_16x16x32_bf16 a[88:91],a[176:179],a[240:243],a[88:91]
ds_read_b64_tr_b16 a[152:153],v22 offset:1288
v_mfma_f32_16x16x32_bf16 a[88:91],a[180:183],a[244:247],a[88:91]
ds_read_b64_tr_b16 a[154:155],v22 offset:3336
v_mfma_f32_16x16x32_bf16 a[120:123],a[180:183],a[252:255],a[120:123]
ds_read_b64_tr_b16 a[156:157],v22 offset:17672
v_mfma_f32_16x16x32_bf16 a[120:123],a[176:179],a[248:251],a[120:123]
ds_read_b64_tr_b16 a[158:159],v22 offset:19720
v_mfma_f32_16x16x32_bf16 a[28:31],a[184:187],a[224:227],a[28:31]
v_mfma_f32_16x16x32_bf16 a[28:31],a[188:191],a[228:231],a[28:31]
v_mfma_f32_16x16x32_bf16 a[60:63],a[188:191],a[236:239],a[60:63]
v_mfma_f32_16x16x32_bf16 a[60:63],a[184:187],a[232:235],a[60:63]
v_mfma_f32_16x16x32_bf16 a[92:95],a[184:187],a[240:243],a[92:95]
v_mfma_f32_16x16x32_bf16 a[92:95],a[188:191],a[244:247],a[92:95]
v_mfma_f32_16x16x32_bf16 a[124:127],a[188:191],a[252:255],a[124:127]
v_mfma_f32_16x16x32_bf16 a[124:127],a[184:187],a[248:251],a[124:127]
s_mov_b32 s53,3
.p2align 5
L_qp_%=:
s_waitcnt lgkmcnt(1)
v_mfma_f32_16x16x32_bf16 v[128:131],a[128:131],a[192:195],v[128:131]
s_add_u32 m0,s58,0
s_add_u32 s60,s60,32768
ds_read_b64_tr_b16 a[160:161],v22 offset:1536
v_mfma_f32_16x16x32_bf16 v[128:131],a[132:135],a[196:199],v[128:131]
s_mov_b32 s90,L_att0x_%=-L_pc_%=+128
buffer_load_dwordx4 v56,s[32:35],s56 offen lds
v_mfma_f32_16x16x32_bf16 v[136:139],a[132:135],a[204:207],v[136:139]
s_cmp_eq_u32 s60,131072
s_cselect_b32 s60,32768,s60
ds_read_b64_tr_b16 a[162:163],v22 offset:3584
v_mfma_f32_16x16x32_bf16 v[136:139],a[128:131],a[200:203],v[136:139]
s_add_u32 m0,s58,1024
ds_read_b64_tr_b16 a[164:165],v22 offset:17920
v_xor_b32 v20,131072,v20
v_mfma_f32_16x16x32_bf16 v[144:147],a[128:131],a[208:211],v[144:147]
s_sub_u32 s53,s53,1
ds_read_b64_tr_b16 a[166:167],v22 offset:19968
v_xor_b32 v21,131072,v21
v_mfma_f32_16x16x32_bf16 v[144:147],a[132:135],a[212:215],v[144:147]
ds_read_b64_tr_b16 a[168:169],v22 offset:1544
v_mfma_f32_16x16x32_bf16 v[152:155],a[132:135],a[220:223],v[152:155]
buffer_load_dwordx4 v57,s[32:35],s56 offen lds
v_mfma_f32_16x16x32_bf16 v[152:155],a[128:131],a[216:219],v[152:155]
ds_read_b64_tr_b16 a[170:171],v22 offset:3592
v_mfma_f32_16x16x32_bf16 v[132:135],a[136:139],a[192:195],v[132:135]
ds_read_b64_tr_b16 a[172:173],v22 offset:17928
v_mfma_f32_16x16x32_bf16 v[132:135],a[140:143],a[196:199],v[132:135]
s_add_u32 m0,s58,2048
ds_read_b64_tr_b16 a[174:175],v22 offset:19976
v_mfma_f32_16x16x32_bf16 v[140:143],a[140:143],a[204:207],v[140:143]
ds_read_b64_tr_b16 a[176:177],v22 offset:1792
v_mfma_f32_16x16x32_bf16 v[140:143],a[136:139],a[200:203],v[140:143]
ds_read_b64_tr_b16 a[178:179],v22 offset:3840
v_mfma_f32_16x16x32_bf16 v[148:151],a[136:139],a[208:211],v[148:151]
buffer_load_dwordx4 v58,s[32:35],s56 offen lds
v_mfma_f32_16x16x32_bf16 v[148:151],a[140:143],a[212:215],v[148:151]
ds_read_b64_tr_b16 a[180:181],v22 offset:18176
v_mfma_f32_16x16x32_bf16 v[156:159],a[140:143],a[220:223],v[156:159]
s_add_u32 m0,s58,3072
ds_read_b64_tr_b16 a[182:183],v22 offset:20224
v_mfma_f32_16x16x32_bf16 v[156:159],a[136:139],a[216:219],v[156:159]
ds_read_b64_tr_b16 a[184:185],v22 offset:1800
v_mfma_f32_16x16x32_bf16 v[160:163],a[144:147],a[192:195],v[160:163]
ds_read_b64_tr_b16 a[186:187],v22 offset:3848
v_mfma_f32_16x16x32_bf16 v[160:163],a[148:151],a[196:199],v[160:163]
buffer_load_dwordx4 v59,s[32:35],s56 offen lds
v_mfma_f32_16x16x32_bf16 v[168:171],a[148:151],a[204:207],v[168:171]
ds_read_b64_tr_b16 a[188:189],v22 offset:18184
v_mfma_f32_16x16x32_bf16 v[168:171],a[144:147],a[200:203],v[168:171]
ds_read_b64_tr_b16 a[190:191],v22 offset:20232
v_mfma_f32_16x16x32_bf16 v[176:179],a[144:147],a[208:211],v[176:179]
s_add_u32 m0,s58,4096
ds_read_b64_tr_b16 a[128:129],v22
v_mfma_f32_16x16x32_bf16 v[176:179],a[148:151],a[212:215],v[176:179]
ds_read_b64_tr_b16 a[130:131],v22 offset:2048
v_mfma_f32_16x16x32_bf16 v[184:187],a[148:151],a[220:223],v[184:187]
ds_read_b64_tr_b16 a[132:133],v22 offset:16384
v_mfma_f32_16x16x32_bf16 v[184:187],a[144:147],a[216:219],v[184:187]
buffer_load_dwordx4 v60,s[32:35],s56 offen lds
v_mfma_f32_16x16x32_bf16 v[164:167],a[152:155],a[192:195],v[164:167]
s_waitcnt lgkmcnt(5)
ds_read_b64_tr_b16 a[134:135],v22 offset:18432
v_mfma_f32_16x16x32_bf16 v[164:167],a[156:159],a[196:199],v[164:167]
s_add_u32 m0,s58,5120
ds_read_b64_tr_b16 a[136:137],v22 offset:8
v_mfma_f32_16x16x32_bf16 v[172:175],a[156:159],a[204:207],v[172:175]
ds_read_b64_tr_b16 a[138:139],v22 offset:2056
v_mfma_f32_16x16x32_bf16 v[172:175],a[152:155],a[200:203],v[172:175]
ds_read_b64_tr_b16 a[140:141],v22 offset:16392
v_mfma_f32_16x16x32_bf16 v[180:183],a[152:155],a[208:211],v[180:183]
buffer_load_dwordx4 v61,s[32:35],s56 offen lds
v_mfma_f32_16x16x32_bf16 v[180:183],a[156:159],a[212:215],v[180:183]
ds_read_b64_tr_b16 a[142:143],v22 offset:18440
v_mfma_f32_16x16x32_bf16 v[188:191],a[156:159],a[220:223],v[188:191]
ds_read_b64_tr_b16 a[144:145],v22 offset:256
v_mfma_f32_16x16x32_bf16 v[188:191],a[152:155],a[216:219],v[188:191]
s_add_u32 m0,s58,6144
ds_read_b64_tr_b16 a[146:147],v22 offset:2304
v_mfma_f32_16x16x32_bf16 v[192:195],a[160:163],a[192:195],v[192:195]
ds_read_b64_tr_b16 a[148:149],v22 offset:16640
v_mfma_f32_16x16x32_bf16 v[192:195],a[164:167],a[196:199],v[192:195]
ds_read_b64_tr_b16 a[150:151],v22 offset:18688
v_mfma_f32_16x16x32_bf16 v[200:203],a[164:167],a[204:207],v[200:203]
buffer_load_dwordx4 v62,s[32:35],s56 offen lds
v_mfma_f32_16x16x32_bf16 v[200:203],a[160:163],a[200:203],v[200:203]
ds_read_b64_tr_b16 a[152:153],v22 offset:264
v_mfma_f32_16x16x32_bf16 v[208:211],a[160:163],a[208:211],v[208:211]
s_add_u32 m0,s58,7168
s_add_u32 s58,s58,32768
ds_read_b64_tr_b16 a[154:155],v22 offset:2312
v_mfma_f32_16x16x32_bf16 v[208:211],a[164:167],a[212:215],v[208:211]
s_cmp_ge_u32 s58,131072
s_cselect_b32 s62,98304,0
ds_read_b64_tr_b16 a[156:157],v22 offset:16648
v_mfma_f32_16x16x32_bf16 v[216:219],a[164:167],a[220:223],v[216:219]
s_sub_u32 s58,s58,s62
ds_read_b64_tr_b16 a[158:159],v22 offset:18696
v_mfma_f32_16x16x32_bf16 v[216:219],a[160:163],a[216:219],v[216:219]
buffer_load_dwordx4 v63,s[32:35],s56 offen lds
v_mfma_f32_16x16x32_bf16 v[196:199],a[168:171],a[192:195],v[196:199]
s_add_u32 s56,s56,524288
ds_read_b64_tr_b16 a[160:161],v22 offset:512
v_mfma_f32_16x16x32_bf16 v[196:199],a[172:175],a[196:199],v[196:199]
ds_read_b64_tr_b16 a[162:163],v22 offset:2560
v_mfma_f32_16x16x32_bf16 v[204:207],a[172:175],a[204:207],v[204:207]
s_add_u32 m0,s59,0
ds_read_b64_tr_b16 a[164:165],v22 offset:16896
v_mfma_f32_16x16x32_bf16 v[204:207],a[168:171],a[200:203],v[204:207]
ds_read_b64_tr_b16 a[166:167],v22 offset:18944
v_mfma_f32_16x16x32_bf16 v[212:215],a[168:171],a[208:211],v[212:215]
v_mfma_f32_16x16x32_bf16 v[212:215],a[172:175],a[212:215],v[212:215]
buffer_load_dwordx4 v48,s[28:31],s57 offen lds
v_mfma_f32_16x16x32_bf16 v[220:223],a[172:175],a[220:223],v[220:223]
ds_read_b64_tr_b16 a[172:173],v22 offset:16904
v_mfma_f32_16x16x32_bf16 v[220:223],a[168:171],a[216:219],v[220:223]
ds_read_b64_tr_b16 a[168:169],v22 offset:520
v_mfma_f32_16x16x32_bf16 v[224:227],a[176:179],a[192:195],v[224:227]
ds_read_b64_tr_b16 a[170:171],v22 offset:2568
v_mfma_f32_16x16x32_bf16 v[224:227],a[180:183],a[196:199],v[224:227]
ds_read_b64_tr_b16 a[174:175],v22 offset:18952
v_mfma_f32_16x16x32_bf16 v[232:235],a[180:183],a[204:207],v[232:235]
buffer_load_dwordx4 v49,s[28:31],s57 offen offset:1024 lds
v_mfma_f32_16x16x32_bf16 v[232:235],a[176:179],a[200:203],v[232:235]
v_mfma_f32_16x16x32_bf16 v[240:243],a[176:179],a[208:211],v[240:243]
v_mfma_f32_16x16x32_bf16 v[240:243],a[180:183],a[212:215],v[240:243]
v_mfma_f32_16x16x32_bf16 v[248:251],a[180:183],a[220:223],v[248:251]
ds_read_b64_tr_b16 a[180:181],v22 offset:17152
v_mfma_f32_16x16x32_bf16 v[248:251],a[176:179],a[216:219],v[248:251]
ds_read_b64_tr_b16 a[176:177],v22 offset:768
v_mfma_f32_16x16x32_bf16 v[228:231],a[184:187],a[192:195],v[228:231]
s_waitcnt lgkmcnt(3)
buffer_load_dwordx4 v50,s[28:31],s57 offen offset:2048 lds
v_mfma_f32_16x16x32_bf16 v[228:231],a[188:191],a[196:199],v[228:231]
ds_read_b64_tr_b16 a[178:179],v22 offset:2816
v_mfma_f32_16x16x32_bf16 v[236:239],a[188:191],a[204:207],v[236:239]
ds_read_b64_tr_b16 a[182:183],v22 offset:19200
v_mfma_f32_16x16x32_bf16 v[236:239],a[184:187],a[200:203],v[236:239]
v_mfma_f32_16x16x32_bf16 v[244:247],a[184:187],a[208:211],v[244:247]
v_mfma_f32_16x16x32_bf16 v[244:247],a[188:191],a[212:215],v[244:247]
buffer_load_dwordx4 v51,s[28:31],s57 offen offset:3072 lds
v_mfma_f32_16x16x32_bf16 v[252:255],a[188:191],a[220:223],v[252:255]
ds_read_b64_tr_b16 a[188:189],v22 offset:17160
v_mfma_f32_16x16x32_bf16 v[252:255],a[184:187],a[216:219],v[252:255]
ds_read_b64_tr_b16 a[184:185],v22 offset:776
v_mfma_f32_16x16x32_bf16 a[0:3],a[128:131],a[192:195],a[0:3]
s_add_u32 m0,s59,4096
s_xor_b32 s59,s59,131072
ds_read_b64_tr_b16 a[186:187],v22 offset:2824
v_mfma_f32_16x16x32_bf16 a[0:3],a[132:135],a[196:199],a[0:3]
ds_read_b64_tr_b16 a[190:191],v22 offset:19208
v_add_u32 v22,s60,v23
v_mfma_f32_16x16x32_bf16 a[32:35],a[132:135],a[204:207],a[32:35]
s_add_u32 s60,s60,32768
s_cmp_eq_u32 s60,131072
s_cselect_b32 s60,32768,s60
v_mfma_f32_16x16x32_bf16 a[32:35],a[128:131],a[200:203],a[32:35]
buffer_load_dwordx4 v52,s[28:31],s57 offen lds
v_mfma_f32_16x16x32_bf16 a[64:67],a[128:131],a[208:211],a[64:67]
v_mfma_f32_16x16x32_bf16 a[64:67],a[132:135],a[212:215],a[64:67]
v_mfma_f32_16x16x32_bf16 a[96:99],a[132:135],a[220:223],a[96:99]
v_mfma_f32_16x16x32_bf16 a[96:99],a[128:131],a[216:219],a[96:99]
v_mfma_f32_16x16x32_bf16 a[4:7],a[136:139],a[192:195],a[4:7]
buffer_load_dwordx4 v53,s[28:31],s57 offen offset:1024 lds
v_mfma_f32_16x16x32_bf16 a[4:7],a[140:143],a[196:199],a[4:7]
v_mfma_f32_16x16x32_bf16 a[36:39],a[140:143],a[204:207],a[36:39]
v_mfma_f32_16x16x32_bf16 a[36:39],a[136:139],a[200:203],a[36:39]
v_mfma_f32_16x16x32_bf16 a[68:71],a[136:139],a[208:211],a[68:71]
v_mfma_f32_16x16x32_bf16 a[68:71],a[140:143],a[212:215],a[68:71]
v_mfma_f32_16x16x32_bf16 a[100:103],a[140:143],a[220:223],a[100:103]
buffer_load_dwordx4 v54,s[28:31],s57 offen offset:2048 lds
v_mfma_f32_16x16x32_bf16 a[100:103],a[136:139],a[216:219],a[100:103]
v_mfma_f32_16x16x32_bf16 a[8:11],a[144:147],a[192:195],a[8:11]
v_mfma_f32_16x16x32_bf16 a[8:11],a[148:151],a[196:199],a[8:11]
v_mfma_f32_16x16x32_bf16 a[40:43],a[148:151],a[204:207],a[40:43]
v_mfma_f32_16x16x32_bf16 a[40:43],a[144:147],a[200:203],a[40:43]
buffer_load_dwordx4 v55,s[28:31],s57 offen offset:3072 lds
v_mfma_f32_16x16x32_bf16 a[72:75],a[144:147],a[208:211],a[72:75]
s_add_u32 s57,s57,128
v_mfma_f32_16x16x32_bf16 a[72:75],a[148:151],a[212:215],a[72:75]
v_mfma_f32_16x16x32_bf16 a[104:107],a[148:151],a[220:223],a[104:107]
v_mfma_f32_16x16x32_bf16 a[104:107],a[144:147],a[216:219],a[104:107]
v_mfma_f32_16x16x32_bf16 a[12:15],a[152:155],a[192:195],a[12:15]
v_mfma_f32_16x16x32_bf16 a[12:15],a[156:159],a[196:199],a[12:15]
v_mfma_f32_16x16x32_bf16 a[44:47],a[156:159],a[204:207],a[44:47]
buffer_load_dword v16,v15,s[72:75],s83 offen
v_mfma_f32_16x16x32_bf16 a[44:47],a[152:155],a[200:203],a[44:47]
s_add_u32 s83,s83,896
v_mfma_f32_16x16x32_bf16 a[76:79],a[152:155],a[208:211],a[76:79]
v_mfma_f32_16x16x32_bf16 a[76:79],a[156:159],a[212:215],a[76:79]
v_mfma_f32_16x16x32_bf16 a[108:111],a[156:159],a[220:223],a[108:111]
v_mfma_f32_16x16x32_bf16 a[108:111],a[152:155],a[216:219],a[108:111]
s_waitcnt vmcnt(17) lgkmcnt(0)
v_mfma_f32_16x16x32_bf16 a[16:19],a[160:163],a[192:195],a[16:19]
s_barrier
ds_read_b128 a[224:227],v20
v_mfma_f32_16x16x32_bf16 a[16:19],a[164:167],a[196:199],a[16:19]
ds_read_b64_tr_b16 a[128:129],v22 offset:1024
v_mfma_f32_16x16x32_bf16 a[48:51],a[164:167],a[204:207],a[48:51]
ds_read_b64_tr_b16 a[130:131],v22 offset:3072
v_mfma_f32_16x16x32_bf16 a[48:51],a[160:163],a[200:203],a[48:51]
ds_read_b128 a[228:231],v21
)A"
R"A(v_mfma_f32_16x16x32_bf16 a[80:83],a[160:163],a[208:211],a[80:83]
ds_read_b64_tr_b16 a[132:133],v22 offset:17408
v_mfma_f32_16x16x32_bf16 a[80:83],a[164:167],a[212:215],a[80:83]
ds_read_b64_tr_b16 a[134:135],v22 offset:19456
v_mfma_f32_16x16x32_bf16 a[112:115],a[164:167],a[220:223],a[112:115]
ds_read_b128 a[232:235],v20 offset:2048
v_mfma_f32_16x16x32_bf16 a[112:115],a[160:163],a[216:219],a[112:115]
ds_read_b128 a[236:239],v21 offset:2048
v_mfma_f32_16x16x32_bf16 a[20:23],a[168:171],a[192:195],a[20:23]
ds_read_b128 a[240:243],v20 offset:4096
v_mfma_f32_16x16x32_bf16 a[20:23],a[172:175],a[196:199],a[20:23]
ds_read_b128 a[244:247],v21 offset:4096
v_mfma_f32_16x16x32_bf16 a[52:55],a[172:175],a[204:207],a[52:55]
ds_read_b128 a[248:251],v20 offset:6144
v_xor_b32 v20,131072,v20
v_mfma_f32_16x16x32_bf16 a[52:55],a[168:171],a[200:203],a[52:55]
ds_read_b128 a[252:255],v21 offset:6144
v_xor_b32 v21,131072,v21
v_mfma_f32_16x16x32_bf16 a[84:87],a[168:171],a[208:211],a[84:87]
ds_read_b64_tr_b16 a[136:137],v22 offset:1032
v_mfma_f32_16x16x32_bf16 a[84:87],a[172:175],a[212:215],a[84:87]
ds_read_b64_tr_b16 a[138:139],v22 offset:3080
v_mfma_f32_16x16x32_bf16 a[116:119],a[172:175],a[220:223],a[116:119]
ds_read_b64_tr_b16 a[140:141],v22 offset:17416
v_mfma_f32_16x16x32_bf16 a[116:119],a[168:171],a[216:219],a[116:119]
ds_read_b64_tr_b16 a[142:143],v22 offset:19464
v_mfma_f32_16x16x32_bf16 a[24:27],a[176:179],a[192:195],a[24:27]
ds_read_b64_tr_b16 a[144:145],v22 offset:1280
v_mfma_f32_16x16x32_bf16 a[24:27],a[180:183],a[196:199],a[24:27]
ds_read_b64_tr_b16 a[146:147],v22 offset:3328
v_mfma_f32_16x16x32_bf16 a[56:59],a[180:183],a[204:207],a[56:59]
ds_read_b64_tr_b16 a[148:149],v22 offset:17664
v_mfma_f32_16x16x32_bf16 a[56:59],a[176:179],a[200:203],a[56:59]
ds_read_b64_tr_b16 a[150:151],v22 offset:19712
v_mfma_f32_16x16x32_bf16 a[88:91],a[176:179],a[208:211],a[88:91]
ds_read_b64_tr_b16 a[152:153],v22 offset:1288
v_mfma_f32_16x16x32_bf16 a[88:91],a[180:183],a[212:215],a[88:91]
ds_read_b64_tr_b16 a[154:155],v22 offset:3336
v_mfma_f32_16x16x32_bf16 a[120:123],a[180:183],a[220:223],a[120:123]
ds_read_b64_tr_b16 a[156:157],v22 offset:17672
v_mfma_f32_16x16x32_bf16 a[120:123],a[176:179],a[216:219],a[120:123]
ds_read_b64_tr_b16 a[158:159],v22 offset:19720
v_mfma_f32_16x16x32_bf16 a[28:31],a[184:187],a[192:195],a[28:31]
ds_read_b64_tr_b16 a[160:161],v22 offset:1536
v_mfma_f32_16x16x32_bf16 a[28:31],a[188:191],a[196:199],a[28:31]
ds_read_b64_tr_b16 a[162:163],v22 offset:3584
v_mfma_f32_16x16x32_bf16 a[60:63],a[188:191],a[204:207],a[60:63]
ds_read_b64_tr_b16 a[164:165],v22 offset:17920
v_mfma_f32_16x16x32_bf16 a[60:63],a[184:187],a[200:203],a[60:63]
ds_read_b64_tr_b16 a[166:167],v22 offset:19968
v_mfma_f32_16x16x32_bf16 a[92:95],a[184:187],a[208:211],a[92:95]
ds_read_b64_tr_b16 a[168:169],v22 offset:1544
v_mfma_f32_16x16x32_bf16 a[92:95],a[188:191],a[212:215],a[92:95]
ds_read_b64_tr_b16 a[170:171],v22 offset:3592
v_mfma_f32_16x16x32_bf16 a[124:127],a[188:191],a[220:223],a[124:127]
s_add_u32 m0,s58,0
ds_read_b64_tr_b16 a[172:173],v22 offset:17928
v_mfma_f32_16x16x32_bf16 a[124:127],a[184:187],a[216:219],a[124:127]
s_waitcnt lgkmcnt(7)
ds_read_b64_tr_b16 a[174:175],v22 offset:19976
v_mfma_f32_16x16x32_bf16 v[128:131],a[128:131],a[224:227],v[128:131]
ds_read_b64_tr_b16 a[176:177],v22 offset:1792
v_mfma_f32_16x16x32_bf16 v[128:131],a[132:135],a[228:231],v[128:131]
buffer_load_dwordx4 v56,s[32:35],s56 offen lds
v_mfma_f32_16x16x32_bf16 v[136:139],a[132:135],a[236:239],v[136:139]
ds_read_b64_tr_b16 a[178:179],v22 offset:3840
v_mfma_f32_16x16x32_bf16 v[136:139],a[128:131],a[232:235],v[136:139]
s_add_u32 m0,s58,1024
ds_read_b64_tr_b16 a[180:181],v22 offset:18176
v_mfma_f32_16x16x32_bf16 v[144:147],a[128:131],a[240:243],v[144:147]
ds_read_b64_tr_b16 a[182:183],v22 offset:20224
v_mfma_f32_16x16x32_bf16 v[144:147],a[132:135],a[244:247],v[144:147]
ds_read_b64_tr_b16 a[184:185],v22 offset:1800
v_mfma_f32_16x16x32_bf16 v[152:155],a[132:135],a[252:255],v[152:155]
buffer_load_dwordx4 v57,s[32:35],s56 offen lds
v_mfma_f32_16x16x32_bf16 v[152:155],a[128:131],a[248:251],v[152:155]
ds_read_b64_tr_b16 a[186:187],v22 offset:3848
v_mfma_f32_16x16x32_bf16 v[132:135],a[136:139],a[224:227],v[132:135]
ds_read_b64_tr_b16 a[188:189],v22 offset:18184
v_mfma_f32_16x16x32_bf16 v[132:135],a[140:143],a[228:231],v[132:135]
s_add_u32 m0,s58,2048
ds_read_b64_tr_b16 a[190:191],v22 offset:20232
v_mfma_f32_16x16x32_bf16 v[140:143],a[140:143],a[236:239],v[140:143]
ds_read_b64_tr_b16 a[128:129],v22
v_mfma_f32_16x16x32_bf16 v[140:143],a[136:139],a[232:235],v[140:143]
ds_read_b64_tr_b16 a[130:131],v22 offset:2048
v_mfma_f32_16x16x32_bf16 v[148:151],a[136:139],a[240:243],v[148:151]
buffer_load_dwordx4 v58,s[32:35],s56 offen lds
v_mfma_f32_16x16x32_bf16 v[148:151],a[140:143],a[244:247],v[148:151]
ds_read_b64_tr_b16 a[132:133],v22 offset:16384
v_mfma_f32_16x16x32_bf16 v[156:159],a[140:143],a[252:255],v[156:159]
s_add_u32 m0,s58,3072
ds_read_b64_tr_b16 a[134:135],v22 offset:18432
v_mfma_f32_16x16x32_bf16 v[156:159],a[136:139],a[248:251],v[156:159]
ds_read_b64_tr_b16 a[136:137],v22 offset:8
v_mfma_f32_16x16x32_bf16 v[160:163],a[144:147],a[224:227],v[160:163]
ds_read_b64_tr_b16 a[138:139],v22 offset:2056
v_mfma_f32_16x16x32_bf16 v[160:163],a[148:151],a[228:231],v[160:163]
buffer_load_dwordx4 v59,s[32:35],s56 offen lds
v_mfma_f32_16x16x32_bf16 v[168:171],a[148:151],a[236:239],v[168:171]
ds_read_b64_tr_b16 a[140:141],v22 offset:16392
v_mfma_f32_16x16x32_bf16 v[168:171],a[144:147],a[232:235],v[168:171]
ds_read_b64_tr_b16 a[142:143],v22 offset:18440
v_mfma_f32_16x16x32_bf16 v[176:179],a[144:147],a[240:243],v[176:179]
s_add_u32 m0,s58,4096
v_mfma_f32_16x16x32_bf16 v[176:179],a[148:151],a[244:247],v[176:179]
v_mfma_f32_16x16x32_bf16 v[184:187],a[148:151],a[252:255],v[184:187]
ds_read_b64_tr_b16 a[148:149],v22 offset:16640
v_mfma_f32_16x16x32_bf16 v[184:187],a[144:147],a[248:251],v[184:187]
buffer_load_dwordx4 v60,s[32:35],s56 offen lds
v_mfma_f32_16x16x32_bf16 v[164:167],a[152:155],a[224:227],v[164:167]
ds_read_b64_tr_b16 a[144:145],v22 offset:256
v_mfma_f32_16x16x32_bf16 v[164:167],a[156:159],a[228:231],v[164:167]
s_add_u32 m0,s58,5120
ds_read_b64_tr_b16 a[146:147],v22 offset:2304
v_mfma_f32_16x16x32_bf16 v[172:175],a[156:159],a[236:239],v[172:175]
ds_read_b64_tr_b16 a[150:151],v22 offset:18688
v_mfma_f32_16x16x32_bf16 v[172:175],a[152:155],a[232:235],v[172:175]
v_mfma_f32_16x16x32_bf16 v[180:183],a[152:155],a[240:243],v[180:183]
buffer_load_dwordx4 v61,s[32:35],s56 offen lds
v_mfma_f32_16x16x32_bf16 v[180:183],a[156:159],a[244:247],v[180:183]
v_mfma_f32_16x16x32_bf16 v[188:191],a[156:159],a[252:255],v[188:191]
ds_read_b64_tr_b16 a[156:157],v22 offset:16648
v_mfma_f32_16x16x32_bf16 v[188:191],a[152:155],a[248:251],v[188:191]
s_add_u32 m0,s58,6144
ds_read_b64_tr_b16 a[152:153],v22 offset:264
s_waitcnt lgkmcnt(5)
v_mfma_f32_16x16x32_bf16 v[192:195],a[160:163],a[224:227],v[192:195]
ds_read_b64_tr_b16 a[154:155],v22 offset:2312
v_mfma_f32_16x16x32_bf16 v[192:195],a[164:167],a[228:231],v[192:195]
ds_read_b64_tr_b16 a[158:159],v22 offset:18696
v_mfma_f32_16x16x32_bf16 v[200:203],a[164:167],a[236:239],v[200:203]
buffer_load_dwordx4 v62,s[32:35],s56 offen lds
v_mfma_f32_16x16x32_bf16 v[200:203],a[160:163],a[232:235],v[200:203]
v_mfma_f32_16x16x32_bf16 v[208:211],a[160:163],a[240:243],v[208:211]
s_add_u32 m0,s58,7168
s_add_u32 s58,s58,32768
v_mfma_f32_16x16x32_bf16 v[208:211],a[164:167],a[244:247],v[208:211]
s_cmp_ge_u32 s58,131072
s_cselect_b32 s62,98304,0
s_sub_u32 s58,s58,s62
v_mfma_f32_16x16x32_bf16 v[216:219],a[164:167],a[252:255],v[216:219]
ds_read_b64_tr_b16 a[164:165],v22 offset:16896
v_mfma_f32_16x16x32_bf16 v[216:219],a[160:163],a[248:251],v[216:219]
buffer_load_dwordx4 v63,s[32:35],s56 offen lds
v_mfma_f32_16x16x32_bf16 v[196:199],a[168:171],a[224:227],v[196:199]
s_add_u32 s56,s56,524288
ds_read_b64_tr_b16 a[160:161],v22 offset:512
v_mfma_f32_16x16x32_bf16 v[196:199],a[172:175],a[228:231],v[196:199]
ds_read_b64_tr_b16 a[162:163],v22 offset:2560
v_mfma_f32_16x16x32_bf16 v[204:207],a[172:175],a[236:239],v[204:207]
s_add_u32 m0,s59,0
ds_read_b64_tr_b16 a[166:167],v22 offset:18944
v_mfma_f32_16x16x32_bf16 v[204:207],a[168:171],a[232:235],v[204:207]
v_mfma_f32_16x16x32_bf16 v[212:215],a[168:171],a[240:243],v[212:215]
v_mfma_f32_16x16x32_bf16 v[212:215],a[172:175],a[244:247],v[212:215]
buffer_load_dwordx4 v48,s[28:31],s57 offen lds
v_mfma_f32_16x16x32_bf16 v[220:223],a[172:175],a[252:255],v[220:223]
ds_read_b64_tr_b16 a[172:173],v22 offset:16904
v_mfma_f32_16x16x32_bf16 v[220:223],a[168:171],a[248:251],v[220:223]
ds_read_b64_tr_b16 a[168:169],v22 offset:520
v_mfma_f32_16x16x32_bf16 v[224:227],a[176:179],a[224:227],v[224:227]
ds_read_b64_tr_b16 a[170:171],v22 offset:2568
v_mfma_f32_16x16x32_bf16 v[224:227],a[180:183],a[228:231],v[224:227]
ds_read_b64_tr_b16 a[174:175],v22 offset:18952
v_mfma_f32_16x16x32_bf16 v[232:235],a[180:183],a[236:239],v[232:235]
buffer_load_dwordx4 v49,s[28:31],s57 offen offset:1024 lds
v_mfma_f32_16x16x32_bf16 v[232:235],a[176:179],a[232:235],v[232:235]
v_mfma_f32_16x16x32_bf16 v[240:243],a[176:179],a[240:243],v[240:243]
v_mfma_f32_16x16x32_bf16 v[240:243],a[180:183],a[244:247],v[240:243]
v_mfma_f32_16x16x32_bf16 v[248:251],a[180:183],a[252:255],v[248:251]
ds_read_b64_tr_b16 a[180:181],v22 offset:17152
v_mfma_f32_16x16x32_bf16 v[248:251],a[176:179],a[248:251],v[248:251]
ds_read_b64_tr_b16 a[176:177],v22 offset:768
v_mfma_f32_16x16x32_bf16 v[228:231],a[184:187],a[224:227],v[228:231]
buffer_load_dwordx4 v50,s[28:31],s57 offen offset:2048 lds
v_mfma_f32_16x16x32_bf16 v[228:231],a[188:191],a[228:231],v[228:231]
ds_read_b64_tr_b16 a[178:179],v22 offset:2816
v_mfma_f32_16x16x32_bf16 v[236:239],a[188:191],a[236:239],v[236:239]
ds_read_b64_tr_b16 a[182:183],v22 offset:19200
v_mfma_f32_16x16x32_bf16 v[236:239],a[184:187],a[232:235],v[236:239]
v_mfma_f32_16x16x32_bf16 v[244:247],a[184:187],a[240:243],v[244:247]
v_mfma_f32_16x16x32_bf16 v[244:247],a[188:191],a[244:247],v[244:247]
buffer_load_dwordx4 v51,s[28:31],s57 offen offset:3072 lds
v_mfma_f32_16x16x32_bf16 v[252:255],a[188:191],a[252:255],v[252:255]
ds_read_b64_tr_b16 a[188:189],v22 offset:17160
v_mfma_f32_16x16x32_bf16 v[252:255],a[184:187],a[248:251],v[252:255]
ds_read_b64_tr_b16 a[184:185],v22 offset:776
v_mfma_f32_16x16x32_bf16 a[0:3],a[128:131],a[224:227],a[0:3]
s_add_u32 m0,s59,4096
s_xor_b32 s59,s59,131072
ds_read_b64_tr_b16 a[186:187],v22 offset:2824
v_mfma_f32_16x16x32_bf16 a[0:3],a[132:135],a[228:231],a[0:3]
ds_read_b64_tr_b16 a[190:191],v22 offset:19208
v_add_u32 v22,s60,v23
v_mfma_f32_16x16x32_bf16 a[32:35],a[132:135],a[236:239],a[32:35]
v_mfma_f32_16x16x32_bf16 a[32:35],a[128:131],a[232:235],a[32:35]
buffer_load_dwordx4 v52,s[28:31],s57 offen lds
v_mfma_f32_16x16x32_bf16 a[64:67],a[128:131],a[240:243],a[64:67]
v_mfma_f32_16x16x32_bf16 a[64:67],a[132:135],a[244:247],a[64:67]
v_mfma_f32_16x16x32_bf16 a[96:99],a[132:135],a[252:255],a[96:99]
v_mfma_f32_16x16x32_bf16 a[96:99],a[128:131],a[248:251],a[96:99]
v_mfma_f32_16x16x32_bf16 a[4:7],a[136:139],a[224:227],a[4:7]
buffer_load_dwordx4 v53,s[28:31],s57 offen offset:1024 lds
v_mfma_f32_16x16x32_bf16 a[4:7],a[140:143],a[228:231],a[4:7]
v_mfma_f32_16x16x32_bf16 a[36:39],a[140:143],a[236:239],a[36:39]
v_mfma_f32_16x16x32_bf16 a[36:39],a[136:139],a[232:235],a[36:39]
v_mfma_f32_16x16x32_bf16 a[68:71],a[136:139],a[240:243],a[68:71]
v_mfma_f32_16x16x32_bf16 a[68:71],a[140:143],a[244:247],a[68:71]
v_mfma_f32_16x16x32_bf16 a[100:103],a[140:143],a[252:255],a[100:103]
buffer_load_dwordx4 v54,s[28:31],s57 offen offset:2048 lds
v_mfma_f32_16x16x32_bf16 a[100:103],a[136:139],a[248:251],a[100:103]
s_waitcnt lgkmcnt(0)
v_mfma_f32_16x16x32_bf16 a[8:11],a[144:147],a[224:227],a[8:11]
v_mfma_f32_16x16x32_bf16 a[8:11],a[148:151],a[228:231],a[8:11]
v_mfma_f32_16x16x32_bf16 a[40:43],a[148:151],a[236:239],a[40:43]
v_mfma_f32_16x16x32_bf16 a[40:43],a[144:147],a[232:235],a[40:43]
buffer_load_dwordx4 v55,s[28:31],s57 offen offset:3072 lds
v_mfma_f32_16x16x32_bf16 a[72:75],a[144:147],a[240:243],a[72:75]
)A"
R"A(s_add_u32 s57,s57,128
v_mfma_f32_16x16x32_bf16 a[72:75],a[148:151],a[244:247],a[72:75]
v_mfma_f32_16x16x32_bf16 a[104:107],a[148:151],a[252:255],a[104:107]
v_mfma_f32_16x16x32_bf16 a[104:107],a[144:147],a[248:251],a[104:107]
v_mfma_f32_16x16x32_bf16 a[12:15],a[152:155],a[224:227],a[12:15]
v_mfma_f32_16x16x32_bf16 a[12:15],a[156:159],a[228:231],a[12:15]
v_mfma_f32_16x16x32_bf16 a[44:47],a[156:159],a[236:239],a[44:47]
buffer_load_dword v16,v15,s[72:75],s83 offen
v_mfma_f32_16x16x32_bf16 a[44:47],a[152:155],a[232:235],a[44:47]
s_add_u32 s83,s83,896
v_mfma_f32_16x16x32_bf16 a[76:79],a[152:155],a[240:243],a[76:79]
v_mfma_f32_16x16x32_bf16 a[76:79],a[156:159],a[244:247],a[76:79]
v_mfma_f32_16x16x32_bf16 a[108:111],a[156:159],a[252:255],a[108:111]
v_mfma_f32_16x16x32_bf16 a[108:111],a[152:155],a[248:251],a[108:111]
s_waitcnt vmcnt(17) lgkmcnt(0)
v_mfma_f32_16x16x32_bf16 a[16:19],a[160:163],a[224:227],a[16:19]
s_barrier
ds_read_b128 a[192:195],v20
v_mfma_f32_16x16x32_bf16 a[16:19],a[164:167],a[228:231],a[16:19]
ds_read_b128 a[196:199],v21
v_mfma_f32_16x16x32_bf16 a[48:51],a[164:167],a[236:239],a[48:51]
ds_read_b128 a[200:203],v20 offset:2048
v_mfma_f32_16x16x32_bf16 a[48:51],a[160:163],a[232:235],a[48:51]
ds_read_b128 a[204:207],v21 offset:2048
v_mfma_f32_16x16x32_bf16 a[80:83],a[160:163],a[240:243],a[80:83]
ds_read_b128 a[208:211],v20 offset:4096
v_mfma_f32_16x16x32_bf16 a[80:83],a[164:167],a[244:247],a[80:83]
ds_read_b128 a[212:215],v21 offset:4096
v_mfma_f32_16x16x32_bf16 a[112:115],a[164:167],a[252:255],a[112:115]
buffer_load_dword v26,v24,s[88:91],s93 offen
v_mfma_f32_16x16x32_bf16 a[112:115],a[160:163],a[248:251],a[112:115]
ds_read_b128 a[216:219],v20 offset:6144
v_mfma_f32_16x16x32_bf16 a[20:23],a[168:171],a[224:227],a[20:23]
ds_read_b128 a[220:223],v21 offset:6144
v_mfma_f32_16x16x32_bf16 a[20:23],a[172:175],a[228:231],a[20:23]
ds_read_b64_tr_b16 a[128:129],v22 offset:1024
v_mfma_f32_16x16x32_bf16 a[52:55],a[172:175],a[236:239],a[52:55]
ds_read_b64_tr_b16 a[130:131],v22 offset:3072
v_mfma_f32_16x16x32_bf16 a[52:55],a[168:171],a[232:235],a[52:55]
ds_read_b64_tr_b16 a[132:133],v22 offset:17408
v_mfma_f32_16x16x32_bf16 a[84:87],a[168:171],a[240:243],a[84:87]
ds_read_b64_tr_b16 a[134:135],v22 offset:19456
v_mfma_f32_16x16x32_bf16 a[84:87],a[172:175],a[244:247],a[84:87]
ds_read_b64_tr_b16 a[136:137],v22 offset:1032
v_mfma_f32_16x16x32_bf16 a[116:119],a[172:175],a[252:255],a[116:119]
ds_read_b64_tr_b16 a[138:139],v22 offset:3080
v_mfma_f32_16x16x32_bf16 a[116:119],a[168:171],a[248:251],a[116:119]
ds_read_b64_tr_b16 a[140:141],v22 offset:17416
v_mfma_f32_16x16x32_bf16 a[24:27],a[176:179],a[224:227],a[24:27]
ds_read_b64_tr_b16 a[142:143],v22 offset:19464
v_mfma_f32_16x16x32_bf16 a[24:27],a[180:183],a[228:231],a[24:27]
ds_read_b64_tr_b16 a[144:145],v22 offset:1280
v_mfma_f32_16x16x32_bf16 a[56:59],a[180:183],a[236:239],a[56:59]
ds_read_b64_tr_b16 a[146:147],v22 offset:3328
v_mfma_f32_16x16x32_bf16 a[56:59],a[176:179],a[232:235],a[56:59]
ds_read_b64_tr_b16 a[148:149],v22 offset:17664
v_mfma_f32_16x16x32_bf16 a[88:91],a[176:179],a[240:243],a[88:91]
ds_read_b64_tr_b16 a[150:151],v22 offset:19712
v_mfma_f32_16x16x32_bf16 a[88:91],a[180:183],a[244:247],a[88:91]
ds_read_b64_tr_b16 a[152:153],v22 offset:1288
v_mfma_f32_16x16x32_bf16 a[120:123],a[180:183],a[252:255],a[120:123]
ds_read_b64_tr_b16 a[154:155],v22 offset:3336
v_mfma_f32_16x16x32_bf16 a[120:123],a[176:179],a[248:251],a[120:123]
ds_read_b64_tr_b16 a[156:157],v22 offset:17672
v_mfma_f32_16x16x32_bf16 a[28:31],a[184:187],a[224:227],a[28:31]
ds_read_b64_tr_b16 a[158:159],v22 offset:19720
v_mfma_f32_16x16x32_bf16 a[28:31],a[188:191],a[228:231],a[28:31]
v_mfma_f32_16x16x32_bf16 a[60:63],a[188:191],a[236:239],a[60:63]
v_mfma_f32_16x16x32_bf16 a[60:63],a[184:187],a[232:235],a[60:63]
v_mfma_f32_16x16x32_bf16 a[92:95],a[184:187],a[240:243],a[92:95]
v_mfma_f32_16x16x32_bf16 a[92:95],a[188:191],a[244:247],a[92:95]
v_mfma_f32_16x16x32_bf16 a[124:127],a[188:191],a[252:255],a[124:127]
s_cmp_lg_u32 s53,0
v_mfma_f32_16x16x32_bf16 a[124:127],a[184:187],a[248:251],a[124:127]
s_cbranch_scc1 L_qp_%=
L_qpx_%=:
L_tailseg_%=:
s_waitcnt lgkmcnt(1)
v_mfma_f32_16x16x32_bf16 v[128:131],a[128:131],a[192:195],v[128:131]
s_add_u32 m0,98304,s51
ds_read_b64_tr_b16 a[160:161],v22 offset:1536
v_xor_b32 v20,131072,v20
v_mfma_f32_16x16x32_bf16 v[128:131],a[132:135],a[196:199],v[128:131]
s_add_u32 s54,s54,16384
buffer_load_dwordx4 v4,s[20:23],s47 offen nt lds
v_mfma_f32_16x16x32_bf16 v[136:139],a[132:135],a[204:207],v[136:139]
s_and_b32 s46,s54,0xfffc0000
ds_read_b64_tr_b16 a[162:163],v22 offset:3584
v_xor_b32 v21,131072,v21
v_mfma_f32_16x16x32_bf16 v[136:139],a[128:131],a[200:203],v[136:139]
s_add_u32 s54,s54,16384
ds_read_b64_tr_b16 a[164:165],v22 offset:17920
v_and_b32 v18,48,v6
v_mfma_f32_16x16x32_bf16 v[144:147],a[128:131],a[208:211],v[144:147]
s_mov_b32 s48,-98304
ds_read_b64_tr_b16 a[166:167],v22 offset:19968
v_lshrrev_b32 v19,4,v6
v_mfma_f32_16x16x32_bf16 v[144:147],a[132:135],a[212:215],v[144:147]
s_lshr_b32 s79,%1,3
ds_read_b64_tr_b16 a[168:169],v22 offset:1544
v_lshlrev_b32 v18,2,v18
v_mfma_f32_16x16x32_bf16 v[152:155],a[132:135],a[220:223],v[152:155]
s_mul_i32 s79,s79,7
ds_read_b64_tr_b16 a[170:171],v22 offset:3592
v_and_b32 v19,48,v19
v_mfma_f32_16x16x32_bf16 v[152:155],a[128:131],a[216:219],v[152:155]
s_mov_b32 s60,148
ds_read_b64_tr_b16 a[172:173],v22 offset:17928
v_add_u32 v18,v18,v19
v_mfma_f32_16x16x32_bf16 v[132:135],a[136:139],a[192:195],v[132:135]
s_lshr_b32 s79,s79,5
ds_read_b64_tr_b16 a[174:175],v22 offset:19976
v_add_u32 v18,98304,v18
v_mfma_f32_16x16x32_bf16 v[132:135],a[140:143],a[196:199],v[132:135]
s_sub_u32 s60,s60,s79
s_lshl_b32 s94,s60,2
ds_read_b64_tr_b16 a[176:177],v22 offset:1792
v_mfma_f32_16x16x32_bf16 v[140:143],a[140:143],a[204:207],v[140:143]
s_sub_u32 s94,s94,3
s_mul_i32 s83,s41,5
ds_read_b64_tr_b16 a[178:179],v22 offset:3840
v_mfma_f32_16x16x32_bf16 v[140:143],a[136:139],a[200:203],v[140:143]
s_lshl_b32 s44,s40,4
s_add_u32 s83,s83,s42
ds_read_b64_tr_b16 a[180:181],v22 offset:18176
v_mfma_f32_16x16x32_bf16 v[148:151],a[136:139],a[208:211],v[148:151]
s_and_b32 s44,s44,32
ds_read_b64_tr_b16 a[182:183],v22 offset:20224
v_subrev_u32 v25,s51,v4
v_mfma_f32_16x16x32_bf16 v[148:151],a[140:143],a[212:215],v[148:151]
s_add_u32 s83,s83,15
buffer_load_dwordx4 v4,s[20:23],s47 offen offset:1024 nt lds
v_mfma_f32_16x16x32_bf16 v[156:159],a[140:143],a[220:223],v[156:159]
s_lshl_b32 s80,s40,10
ds_read_b64_tr_b16 a[184:185],v22 offset:1800
v_xor_b32 v25,s44,v25
v_mfma_f32_16x16x32_bf16 v[156:159],a[136:139],a[216:219],v[156:159]
s_and_b32 s83,s83,15
ds_read_b64_tr_b16 a[186:187],v22 offset:3848
v_add_u32 v25,s80,v25
v_mfma_f32_16x16x32_bf16 v[160:163],a[144:147],a[192:195],v[160:163]
s_add_u32 s83,s83,16
v_readfirstlane_b32 s68,v13
s_nop 1
s_add_u32 s68,s68,1
v_mfma_f32_16x16x32_bf16 v[160:163],a[148:151],a[196:199],v[160:163]
s_xor_b32 s69,s68,0x9e3779b9
s_add_u32 s93,s92,L_att0x_%=-L_pc_%=
ds_read_b64_tr_b16 a[188:189],v22 offset:18184
v_mfma_f32_16x16x32_bf16 v[168:171],a[148:151],a[204:207],v[168:171]
s_mov_b32 s50,32768
s_mov_b32 s49,98304
ds_read_b64_tr_b16 a[190:191],v22 offset:20232
v_mfma_f32_16x16x32_bf16 v[168:171],a[144:147],a[200:203],v[168:171]
s_mov_b32 s45,0x1249
s_add_u32 s80,s80,131072
ds_read_b64_tr_b16 a[128:129],v22
v_mfma_f32_16x16x32_bf16 v[176:179],a[144:147],a[208:211],v[176:179]
s_lshl_b32 s83,s83,14
ds_read_b64_tr_b16 a[130:131],v22 offset:2048
v_mfma_f32_16x16x32_bf16 v[176:179],a[148:151],a[212:215],v[176:179]
ds_read_b64_tr_b16 a[132:133],v22 offset:16384
v_mfma_f32_16x16x32_bf16 v[184:187],a[148:151],a[220:223],v[184:187]
ds_read_b64_tr_b16 a[134:135],v22 offset:18432
v_mfma_f32_16x16x32_bf16 v[184:187],a[144:147],a[216:219],v[184:187]
ds_read_b64_tr_b16 a[144:145],v22 offset:8
v_mfma_f32_16x16x32_bf16 v[164:167],a[152:155],a[192:195],v[164:167]
s_waitcnt lgkmcnt(7)
ds_read_b64_tr_b16 a[146:147],v22 offset:2056
v_mfma_f32_16x16x32_bf16 v[164:167],a[156:159],a[196:199],v[164:167]
buffer_load_dwordx4 v5,s[20:23],s47 offen offset:2048 nt lds
v_mfma_f32_16x16x32_bf16 v[172:175],a[156:159],a[204:207],v[172:175]
s_waitcnt vmcnt(5)
ds_read_b128 a[224:227],v20
v_mfma_f32_16x16x32_bf16 v[172:175],a[152:155],a[200:203],v[172:175]
ds_read_b64_tr_b16 a[148:149],v22 offset:16392
v_mfma_f32_16x16x32_bf16 v[180:183],a[152:155],a[208:211],v[180:183]
ds_read_b64_tr_b16 a[150:151],v22 offset:18440
v_mfma_f32_16x16x32_bf16 v[180:183],a[156:159],a[212:215],v[180:183]
v_mfma_f32_16x16x32_bf16 v[188:191],a[156:159],a[220:223],v[188:191]
ds_read_b128 a[228:231],v21
v_mfma_f32_16x16x32_bf16 v[188:191],a[152:155],a[216:219],v[188:191]
v_mfma_f32_16x16x32_bf16 v[192:195],a[160:163],a[192:195],v[192:195]
ds_read_b128 a[232:235],v20 offset:2048
v_mfma_f32_16x16x32_bf16 v[192:195],a[164:167],a[196:199],v[192:195]
v_mfma_f32_16x16x32_bf16 v[200:203],a[164:167],a[204:207],v[200:203]
ds_read_b128 a[236:239],v21 offset:2048
v_mfma_f32_16x16x32_bf16 v[200:203],a[160:163],a[200:203],v[200:203]
v_mfma_f32_16x16x32_bf16 v[208:211],a[160:163],a[208:211],v[208:211]
ds_read_b128 a[240:243],v20 offset:4096
v_mfma_f32_16x16x32_bf16 v[208:211],a[164:167],a[212:215],v[208:211]
buffer_load_dwordx4 v5,s[20:23],s47 offen offset:3072 nt lds
v_mfma_f32_16x16x32_bf16 v[216:219],a[164:167],a[220:223],v[216:219]
ds_read_b128 a[244:247],v21 offset:4096
v_mfma_f32_16x16x32_bf16 v[216:219],a[160:163],a[216:219],v[216:219]
ds_read_b64_tr_b16 a[160:161],v22 offset:256
v_mfma_f32_16x16x32_bf16 v[196:199],a[168:171],a[192:195],v[196:199]
ds_read_b128 a[248:251],v20 offset:6144
ds_read_b128 a[252:255],v21 offset:6144
v_mfma_f32_16x16x32_bf16 v[196:199],a[172:175],a[196:199],v[196:199]
v_mfma_f32_16x16x32_bf16 v[204:207],a[172:175],a[204:207],v[204:207]
v_mfma_f32_16x16x32_bf16 v[204:207],a[168:171],a[200:203],v[204:207]
v_mfma_f32_16x16x32_bf16 v[212:215],a[168:171],a[208:211],v[212:215]
ds_read_b64_tr_b16 a[162:163],v22 offset:2304
v_mfma_f32_16x16x32_bf16 v[212:215],a[172:175],a[212:215],v[212:215]
ds_read_b64_tr_b16 a[164:165],v22 offset:16640
v_mfma_f32_16x16x32_bf16 v[220:223],a[172:175],a[220:223],v[220:223]
s_waitcnt vmcnt(5) lgkmcnt(2)
s_add_u32 m0,98304,s52
ds_read_b64_tr_b16 a[166:167],v22 offset:18688
v_mfma_f32_16x16x32_bf16 v[220:223],a[168:171],a[216:219],v[220:223]
s_barrier
ds_read_b64_tr_b16 a[136:137],v22 offset:32768
v_mfma_f32_16x16x32_bf16 v[224:227],a[176:179],a[192:195],v[224:227]
buffer_load_dwordx4 v4,s[24:27],s47 offen nt lds
v_mfma_f32_16x16x32_bf16 v[224:227],a[180:183],a[196:199],v[224:227]
ds_read_b64_tr_b16 a[138:139],v22 offset:34816
v_mfma_f32_16x16x32_bf16 v[232:235],a[180:183],a[204:207],v[232:235]
ds_read_b64_tr_b16 a[140:141],v22 offset:49152
v_mfma_f32_16x16x32_bf16 v[232:235],a[176:179],a[200:203],v[232:235]
ds_read_b64_tr_b16 a[142:143],v22 offset:51200
v_mfma_f32_16x16x32_bf16 v[240:243],a[176:179],a[208:211],v[240:243]
ds_read_b64_tr_b16 a[152:153],v22 offset:32776
v_mfma_f32_16x16x32_bf16 v[240:243],a[180:183],a[212:215],v[240:243]
ds_read_b64_tr_b16 a[154:155],v22 offset:34824
v_mfma_f32_16x16x32_bf16 v[248:251],a[180:183],a[220:223],v[248:251]
ds_read_b64_tr_b16 a[156:157],v22 offset:49160
v_mfma_f32_16x16x32_bf16 v[248:251],a[176:179],a[216:219],v[248:251]
ds_read_b64_tr_b16 a[158:159],v22 offset:51208
v_mfma_f32_16x16x32_bf16 v[228:231],a[184:187],a[192:195],v[228:231]
ds_read_b64_tr_b16 a[168:169],v22 offset:33024
v_mfma_f32_16x16x32_bf16 v[228:231],a[188:191],a[196:199],v[228:231]
ds_read_b64_tr_b16 a[170:171],v22 offset:35072
v_mfma_f32_16x16x32_bf16 v[236:239],a[188:191],a[204:207],v[236:239]
ds_read_b64_tr_b16 a[172:173],v22 offset:49408
v_mfma_f32_16x16x32_bf16 v[236:239],a[184:187],a[200:203],v[236:239]
ds_read_b64_tr_b16 a[174:175],v22 offset:51456
v_mfma_f32_16x16x32_bf16 v[244:247],a[184:187],a[208:211],v[244:247]
buffer_load_dwordx4 v5,s[24:27],s47 offen offset:1024 nt lds
v_mfma_f32_16x16x32_bf16 v[244:247],a[188:191],a[212:215],v[244:247]
ds_read_b64_tr_b16 a[176:177],v22 offset:264
v_mfma_f32_16x16x32_bf16 v[252:255],a[188:191],a[220:223],v[252:255]
)A"
R"A(ds_read_b64_tr_b16 a[178:179],v22 offset:2312
v_mfma_f32_16x16x32_bf16 v[252:255],a[184:187],a[216:219],v[252:255]
ds_read_b64_tr_b16 a[180:181],v22 offset:16648
v_mfma_f32_16x16x32_bf16 a[0:3],a[128:131],a[192:195],a[0:3]
ds_read_b64_tr_b16 a[182:183],v22 offset:18696
v_mfma_f32_16x16x32_bf16 a[0:3],a[132:135],a[196:199],a[0:3]
s_waitcnt lgkmcnt(6)
ds_read_b64_tr_b16 a[184:185],v22 offset:33032
v_mfma_f32_16x16x32_bf16 a[0:3],a[136:139],a[224:227],a[0:3]
ds_read_b64_tr_b16 a[186:187],v22 offset:35080
v_mfma_f32_16x16x32_bf16 a[0:3],a[140:143],a[228:231],a[0:3]
ds_read_b64_tr_b16 a[188:189],v22 offset:49416
v_mfma_f32_16x16x32_bf16 a[32:35],a[140:143],a[236:239],a[32:35]
ds_read_b64_tr_b16 a[190:191],v22 offset:51464
v_mfma_f32_16x16x32_bf16 a[32:35],a[136:139],a[232:235],a[32:35]
v_mfma_f32_16x16x32_bf16 a[32:35],a[132:135],a[204:207],a[32:35]
v_mfma_f32_16x16x32_bf16 a[32:35],a[128:131],a[200:203],a[32:35]
v_mfma_f32_16x16x32_bf16 a[64:67],a[128:131],a[208:211],a[64:67]
buffer_load_dwordx4 v4,s[24:27],s47 offen offset:2048 nt lds
v_mfma_f32_16x16x32_bf16 a[64:67],a[132:135],a[212:215],a[64:67]
v_accvgpr_read_b32 v64,a0
v_accvgpr_read_b32 v65,a1
v_mfma_f32_16x16x32_bf16 a[64:67],a[136:139],a[240:243],a[64:67]
v_mul_f32 v64,s61,v64
v_mul_f32 v65,s61,v65
v_mfma_f32_16x16x32_bf16 a[64:67],a[140:143],a[244:247],a[64:67]
v_accvgpr_read_b32 v66,a2
v_accvgpr_read_b32 v67,a3
v_mfma_f32_16x16x32_bf16 a[96:99],a[140:143],a[252:255],a[96:99]
ds_read_b64_tr_b16 a[140:141],v22 offset:49664
v_cvt_pk_bf16_f32 v64,v64,v65
v_mfma_f32_16x16x32_bf16 a[96:99],a[136:139],a[248:251],a[96:99]
ds_read_b64_tr_b16 a[136:137],v22 offset:33280
v_accvgpr_read_b32 v80,a32
v_mfma_f32_16x16x32_bf16 a[96:99],a[132:135],a[220:223],a[96:99]
ds_read_b64_tr_b16 a[132:133],v22 offset:16896
v_accvgpr_read_b32 v81,a33
v_mfma_f32_16x16x32_bf16 a[96:99],a[128:131],a[216:219],a[96:99]
ds_read_b64_tr_b16 a[128:129],v22 offset:512
v_mul_f32 v80,s61,v80
v_mfma_f32_16x16x32_bf16 a[4:7],a[144:147],a[192:195],a[4:7]
ds_read_b64_tr_b16 a[130:131],v22 offset:2560
v_mul_f32 v81,s61,v81
v_mfma_f32_16x16x32_bf16 a[4:7],a[148:151],a[196:199],a[4:7]
ds_read_b64_tr_b16 a[134:135],v22 offset:18944
v_accvgpr_read_b32 v82,a34
v_mfma_f32_16x16x32_bf16 a[4:7],a[152:155],a[224:227],a[4:7]
ds_read_b64_tr_b16 a[138:139],v22 offset:35328
v_accvgpr_read_b32 v83,a35
v_mfma_f32_16x16x32_bf16 a[4:7],a[156:159],a[228:231],a[4:7]
ds_read_b64_tr_b16 a[142:143],v22 offset:51712
v_mul_f32 v66,s61,v66
v_mfma_f32_16x16x32_bf16 a[36:39],a[156:159],a[236:239],a[36:39]
buffer_load_dwordx4 v5,s[24:27],s47 offen offset:3072 nt lds
v_mfma_f32_16x16x32_bf16 a[36:39],a[152:155],a[232:235],a[36:39]
s_add_u32 s47,s47,16384
s_and_b32 s47,s47,0x3ffff
s_or_b32 s47,s47,s46
v_mfma_f32_16x16x32_bf16 a[36:39],a[148:151],a[204:207],a[36:39]
s_and_b32 s46,s54,0xfffc0000
v_mul_f32 v67,s61,v67
v_cvt_pk_bf16_f32 v80,v80,v81
v_mfma_f32_16x16x32_bf16 a[36:39],a[144:147],a[200:203],a[36:39]
s_add_u32 s54,s54,16384
v_mul_f32 v82,s61,v82
v_mul_f32 v83,s61,v83
v_mfma_f32_16x16x32_bf16 a[68:71],a[144:147],a[208:211],a[68:71]
v_cvt_pk_bf16_f32 v65,v66,v67
v_cvt_pk_bf16_f32 v81,v82,v83
v_mfma_f32_16x16x32_bf16 a[68:71],a[148:151],a[212:215],a[68:71]
v_accvgpr_read_b32 v112,a96
v_accvgpr_read_b32 v113,a97
v_mfma_f32_16x16x32_bf16 a[68:71],a[152:155],a[240:243],a[68:71]
v_accvgpr_read_b32 v66,a4
v_accvgpr_read_b32 v67,a5
v_mfma_f32_16x16x32_bf16 a[68:71],a[156:159],a[244:247],a[68:71]
v_mul_f32 v112,s61,v112
v_mul_f32 v113,s61,v113
v_mfma_f32_16x16x32_bf16 a[100:103],a[156:159],a[252:255],a[100:103]
ds_read_b64_tr_b16 a[156:157],v22 offset:49672
v_accvgpr_read_b32 v114,a98
v_mfma_f32_16x16x32_bf16 a[100:103],a[152:155],a[248:251],a[100:103]
ds_read_b64_tr_b16 a[152:153],v22 offset:33288
v_accvgpr_read_b32 v82,a36
v_mfma_f32_16x16x32_bf16 a[100:103],a[148:151],a[220:223],a[100:103]
ds_read_b64_tr_b16 a[148:149],v22 offset:16904
v_accvgpr_read_b32 v83,a37
v_mfma_f32_16x16x32_bf16 a[100:103],a[144:147],a[216:219],a[100:103]
ds_read_b64_tr_b16 a[144:145],v22 offset:520
v_accvgpr_read_b32 v115,a99
v_mfma_f32_16x16x32_bf16 a[8:11],a[160:163],a[192:195],a[8:11]
s_add_u32 m0,0,s51
ds_read_b64_tr_b16 a[146:147],v22 offset:2568
v_accvgpr_read_b32 v12,a6
v_mfma_f32_16x16x32_bf16 a[8:11],a[164:167],a[196:199],a[8:11]
ds_read_b64_tr_b16 a[150:151],v22 offset:18952
v_accvgpr_read_b32 v13,a7
v_mfma_f32_16x16x32_bf16 a[8:11],a[168:171],a[224:227],a[8:11]
s_waitcnt lgkmcnt(6)
ds_read_b64_tr_b16 a[154:155],v22 offset:35336
v_mul_f32 v66,s61,v66
v_mfma_f32_16x16x32_bf16 a[8:11],a[172:175],a[228:231],a[8:11]
buffer_load_dwordx4 v4,s[20:23],s47 offen nt lds
v_mfma_f32_16x16x32_bf16 a[40:43],a[172:175],a[236:239],a[40:43]
ds_read_b64_tr_b16 a[158:159],v22 offset:51720
v_mul_f32 v67,s61,v67
v_mfma_f32_16x16x32_bf16 a[40:43],a[168:171],a[232:235],a[40:43]
v_accvgpr_read_b32 v14,a38
v_accvgpr_read_b32 v15,a39
v_mfma_f32_16x16x32_bf16 a[40:43],a[164:167],a[204:207],a[40:43]
v_mul_f32 v82,s61,v82
v_mul_f32 v83,s61,v83
v_mfma_f32_16x16x32_bf16 a[40:43],a[160:163],a[200:203],a[40:43]
v_cvt_pk_bf16_f32 v112,v112,v113
v_mul_f32 v114,s61,v114
v_mfma_f32_16x16x32_bf16 a[72:75],a[160:163],a[208:211],a[72:75]
v_mul_f32 v115,s61,v115
v_mul_f32 v12,s61,v12
v_mfma_f32_16x16x32_bf16 a[72:75],a[164:167],a[212:215],a[72:75]
v_accvgpr_read_b32 v68,a8
v_accvgpr_read_b32 v69,a9
v_mfma_f32_16x16x32_bf16 a[72:75],a[168:171],a[240:243],a[72:75]
v_mul_f32 v68,s61,v68
v_mul_f32 v69,s61,v69
v_mfma_f32_16x16x32_bf16 a[72:75],a[172:175],a[244:247],a[72:75]
v_accvgpr_read_b32 v70,a10
v_accvgpr_read_b32 v71,a11
v_mfma_f32_16x16x32_bf16 a[104:107],a[172:175],a[252:255],a[104:107]
ds_read_b64_tr_b16 a[172:173],v22 offset:49920
v_cvt_pk_bf16_f32 v68,v68,v69
v_mfma_f32_16x16x32_bf16 a[104:107],a[168:171],a[248:251],a[104:107]
ds_read_b64_tr_b16 a[168:169],v22 offset:33536
v_mul_f32 v70,s61,v70
v_mfma_f32_16x16x32_bf16 a[104:107],a[164:167],a[220:223],a[104:107]
ds_read_b64_tr_b16 a[164:165],v22 offset:17152
v_mul_f32 v71,s61,v71
v_mfma_f32_16x16x32_bf16 a[104:107],a[160:163],a[216:219],a[104:107]
ds_read_b64_tr_b16 a[160:161],v22 offset:768
v_mul_f32 v13,s61,v13
v_mfma_f32_16x16x32_bf16 a[12:15],a[176:179],a[192:195],a[12:15]
ds_read_b64_tr_b16 a[162:163],v22 offset:2816
v_cvt_pk_bf16_f32 v66,v66,v67
v_mfma_f32_16x16x32_bf16 a[12:15],a[180:183],a[196:199],a[12:15]
buffer_load_dwordx4 v4,s[20:23],s47 offen offset:1024 nt lds
v_mfma_f32_16x16x32_bf16 a[12:15],a[184:187],a[224:227],a[12:15]
ds_read_b64_tr_b16 a[166:167],v22 offset:19200
v_mul_f32 v14,s61,v14
v_mfma_f32_16x16x32_bf16 a[12:15],a[188:191],a[228:231],a[12:15]
ds_read_b64_tr_b16 a[170:171],v22 offset:35584
v_mul_f32 v15,s61,v15
v_mfma_f32_16x16x32_bf16 a[44:47],a[188:191],a[236:239],a[44:47]
ds_read_b64_tr_b16 a[174:175],v22 offset:51968
v_cvt_pk_bf16_f32 v82,v82,v83
v_mfma_f32_16x16x32_bf16 a[44:47],a[184:187],a[232:235],a[44:47]
v_cvt_pk_bf16_f32 v113,v114,v115
v_cvt_pk_bf16_f32 v69,v70,v71
v_mfma_f32_16x16x32_bf16 a[44:47],a[180:183],a[204:207],a[44:47]
v_accvgpr_read_b32 v100,a72
v_accvgpr_read_b32 v101,a73
v_mfma_f32_16x16x32_bf16 a[44:47],a[176:179],a[200:203],a[44:47]
v_accvgpr_read_b32 v116,a104
v_accvgpr_read_b32 v117,a105
v_mfma_f32_16x16x32_bf16 a[76:79],a[176:179],a[208:211],a[76:79]
v_cvt_pk_bf16_f32 v67,v12,v13
v_cvt_pk_bf16_f32 v83,v14,v15
v_mfma_f32_16x16x32_bf16 a[76:79],a[180:183],a[212:215],a[76:79]
v_accvgpr_read_b32 v114,a100
v_accvgpr_read_b32 v115,a101
v_mfma_f32_16x16x32_bf16 a[76:79],a[184:187],a[240:243],a[76:79]
v_accvgpr_read_b32 v70,a12
v_accvgpr_read_b32 v71,a13
v_mfma_f32_16x16x32_bf16 a[76:79],a[188:191],a[244:247],a[76:79]
v_mul_f32 v100,s61,v100
v_mul_f32 v101,s61,v101
v_mfma_f32_16x16x32_bf16 a[108:111],a[188:191],a[252:255],a[108:111]
ds_read_b64_tr_b16 a[188:189],v22 offset:49928
v_accvgpr_read_b32 v102,a74
v_mfma_f32_16x16x32_bf16 a[108:111],a[184:187],a[248:251],a[108:111]
ds_read_b64_tr_b16 a[184:185],v22 offset:33544
v_accvgpr_read_b32 v103,a75
v_mfma_f32_16x16x32_bf16 a[108:111],a[180:183],a[220:223],a[108:111]
ds_read_b64_tr_b16 a[180:181],v22 offset:17160
v_mul_f32 v116,s61,v116
v_mfma_f32_16x16x32_bf16 a[108:111],a[176:179],a[216:219],a[108:111]
buffer_load_dwordx4 v5,s[20:23],s47 offen offset:2048 nt lds
v_mfma_f32_16x16x32_bf16 a[16:19],a[128:131],a[192:195],a[16:19]
ds_read_b64_tr_b16 a[176:177],v22 offset:776
v_mul_f32 v117,s61,v117
v_mfma_f32_16x16x32_bf16 a[16:19],a[132:135],a[196:199],a[16:19]
ds_read_b64_tr_b16 a[178:179],v22 offset:2824
v_accvgpr_read_b32 v118,a106
v_mfma_f32_16x16x32_bf16 a[16:19],a[136:139],a[224:227],a[16:19]
ds_read_b64_tr_b16 a[182:183],v22 offset:19208
v_accvgpr_read_b32 v119,a107
v_mfma_f32_16x16x32_bf16 a[16:19],a[140:143],a[228:231],a[16:19]
ds_read_b64_tr_b16 a[186:187],v22 offset:35592
v_accvgpr_read_b32 v12,a102
v_mfma_f32_16x16x32_bf16 a[48:51],a[140:143],a[236:239],a[48:51]
ds_read_b64_tr_b16 a[190:191],v22 offset:51976
v_accvgpr_read_b32 v13,a103
v_mfma_f32_16x16x32_bf16 a[48:51],a[136:139],a[232:235],a[48:51]
v_mul_f32 v114,s61,v114
v_mul_f32 v115,s61,v115
v_mfma_f32_16x16x32_bf16 a[48:51],a[132:135],a[204:207],a[48:51]
v_accvgpr_read_b32 v14,a14
v_accvgpr_read_b32 v15,a15
v_mfma_f32_16x16x32_bf16 a[48:51],a[128:131],a[200:203],a[48:51]
v_mul_f32 v70,s61,v70
v_mul_f32 v71,s61,v71
v_mfma_f32_16x16x32_bf16 a[80:83],a[128:131],a[208:211],a[80:83]
v_cvt_pk_bf16_f32 v100,v100,v101
v_mul_f32 v102,s61,v102
v_mfma_f32_16x16x32_bf16 a[80:83],a[132:135],a[212:215],a[80:83]
v_mul_f32 v103,s61,v103
v_cvt_pk_bf16_f32 v116,v116,v117
v_mfma_f32_16x16x32_bf16 a[80:83],a[136:139],a[240:243],a[80:83]
v_mul_f32 v118,s61,v118
v_mul_f32 v119,s61,v119
v_mfma_f32_16x16x32_bf16 a[80:83],a[140:143],a[244:247],a[80:83]
v_mul_f32 v12,s61,v12
v_mul_f32 v13,s61,v13
v_mfma_f32_16x16x32_bf16 a[112:115],a[140:143],a[252:255],a[112:115]
ds_read_b64_tr_b16 a[140:141],v22 offset:50184
v_cvt_pk_bf16_f32 v114,v114,v115
v_mfma_f32_16x16x32_bf16 a[112:115],a[136:139],a[248:251],a[112:115]
buffer_load_dwordx4 v5,s[20:23],s47 offen offset:3072 nt lds
v_mfma_f32_16x16x32_bf16 a[112:115],a[132:135],a[220:223],a[112:115]
ds_read_b64_tr_b16 a[132:133],v22 offset:50176
v_mul_f32 v14,s61,v14
v_mfma_f32_16x16x32_bf16 a[112:115],a[128:131],a[216:219],a[112:115]
s_waitcnt lgkmcnt(2)
ds_read_b64_tr_b16 a[128:129],v22 offset:33792
v_mul_f32 v15,s61,v15
v_mfma_f32_16x16x32_bf16 a[20:23],a[144:147],a[192:195],a[20:23]
ds_read_b64_tr_b16 a[130:131],v22 offset:35840
v_cvt_pk_bf16_f32 v70,v70,v71
v_mfma_f32_16x16x32_bf16 a[20:23],a[148:151],a[196:199],a[20:23]
ds_read_b64_tr_b16 a[134:135],v22 offset:52224
v_cvt_pk_bf16_f32 v101,v102,v103
v_mfma_f32_16x16x32_bf16 a[20:23],a[152:155],a[224:227],a[20:23]
ds_read_b64_tr_b16 a[136:137],v22 offset:33800
v_cvt_pk_bf16_f32 v117,v118,v119
v_mfma_f32_16x16x32_bf16 a[20:23],a[156:159],a[228:231],a[20:23]
ds_read_b64_tr_b16 a[138:139],v22 offset:35848
v_cvt_pk_bf16_f32 v115,v12,v13
v_mfma_f32_16x16x32_bf16 a[52:55],a[156:159],a[236:239],a[52:55]
ds_read_b64_tr_b16 a[142:143],v22 offset:52232
v_cvt_pk_bf16_f32 v71,v14,v15
v_mfma_f32_16x16x32_bf16 a[52:55],a[152:155],a[232:235],a[52:55]
v_accvgpr_read_b32 v102,a76
v_accvgpr_read_b32 v103,a77
v_mfma_f32_16x16x32_bf16 a[52:55],a[148:151],a[204:207],a[52:55]
v_accvgpr_read_b32 v118,a108
v_accvgpr_read_b32 v119,a109
v_mfma_f32_16x16x32_bf16 a[52:55],a[144:147],a[200:203],a[52:55]
v_accvgpr_read_b32 v12,a78
v_accvgpr_read_b32 v13,a79
v_mfma_f32_16x16x32_bf16 a[84:87],a[144:147],a[208:211],a[84:87]
s_add_u32 m0,0,s52
v_mul_f32 v102,s61,v102
v_mul_f32 v103,s61,v103
v_mfma_f32_16x16x32_bf16 a[84:87],a[148:151],a[212:215],a[84:87]
v_accvgpr_read_b32 v14,a110
v_accvgpr_read_b32 v15,a111
v_mfma_f32_16x16x32_bf16 a[84:87],a[152:155],a[240:243],a[84:87]
v_mul_f32 v118,s61,v118
v_mul_f32 v119,s61,v119
v_mfma_f32_16x16x32_bf16 a[84:87],a[156:159],a[244:247],a[84:87]
buffer_load_dwordx4 v4,s[24:27],s47 offen nt lds
v_mfma_f32_16x16x32_bf16 a[116:119],a[156:159],a[252:255],a[116:119]
ds_read_b64_tr_b16 a[156:157],v22 offset:50440
v_mul_f32 v12,s61,v12
v_mfma_f32_16x16x32_bf16 a[116:119],a[152:155],a[248:251],a[116:119]
)A"
R"A(ds_read_b64_tr_b16 a[152:153],v22 offset:34056
v_mul_f32 v13,s61,v13
v_mfma_f32_16x16x32_bf16 a[116:119],a[148:151],a[220:223],a[116:119]
ds_read_b64_tr_b16 a[148:149],v22 offset:50432
v_cvt_pk_bf16_f32 v102,v102,v103
v_mfma_f32_16x16x32_bf16 a[116:119],a[144:147],a[216:219],a[116:119]
ds_read_b64_tr_b16 a[144:145],v22 offset:34048
v_mul_f32 v14,s61,v14
v_mfma_f32_16x16x32_bf16 a[24:27],a[160:163],a[192:195],a[24:27]
ds_read_b64_tr_b16 a[146:147],v22 offset:36096
v_mul_f32 v15,s61,v15
v_mfma_f32_16x16x32_bf16 a[24:27],a[164:167],a[196:199],a[24:27]
ds_read_b64_tr_b16 a[150:151],v22 offset:52480
v_cvt_pk_bf16_f32 v118,v118,v119
v_mfma_f32_16x16x32_bf16 a[24:27],a[168:171],a[224:227],a[24:27]
ds_read_b64_tr_b16 a[154:155],v22 offset:36104
v_cvt_pk_bf16_f32 v103,v12,v13
v_mfma_f32_16x16x32_bf16 a[24:27],a[172:175],a[228:231],a[24:27]
ds_read_b64_tr_b16 a[158:159],v22 offset:52488
v_cvt_pk_bf16_f32 v119,v14,v15
v_mfma_f32_16x16x32_bf16 a[56:59],a[172:175],a[236:239],a[56:59]
v_accvgpr_read_b32 v72,a16
v_accvgpr_read_b32 v73,a17
v_accvgpr_read_b32 v74,a18
v_accvgpr_read_b32 v75,a19
v_accvgpr_read_b32 v12,a20
v_accvgpr_read_b32 v13,a21
v_accvgpr_read_b32 v14,a22
v_accvgpr_read_b32 v15,a23
v_pk_mul_f32 v[72:73],s[60:61],v[72:73] op_sel:[1,0] op_sel_hi:[1,1]
v_pk_mul_f32 v[74:75],s[60:61],v[74:75] op_sel:[1,0] op_sel_hi:[1,1]
v_pk_mul_f32 v[12:13],s[60:61],v[12:13] op_sel:[1,0] op_sel_hi:[1,1]
v_pk_mul_f32 v[14:15],s[60:61],v[14:15] op_sel:[1,0] op_sel_hi:[1,1]
v_cvt_pk_bf16_f32 v72,v72,v73
v_cvt_pk_bf16_f32 v73,v74,v75
v_cvt_pk_bf16_f32 v74,v12,v13
v_cvt_pk_bf16_f32 v75,v14,v15
v_mfma_f32_16x16x32_bf16 a[56:59],a[168:171],a[232:235],a[56:59]
v_accvgpr_read_b32 v96,a64
v_accvgpr_read_b32 v97,a65
v_mfma_f32_16x16x32_bf16 a[56:59],a[164:167],a[204:207],a[56:59]
v_accvgpr_read_b32 v88,a48
v_accvgpr_read_b32 v89,a49
v_accvgpr_read_b32 v90,a50
v_accvgpr_read_b32 v91,a51
v_accvgpr_read_b32 v12,a52
v_accvgpr_read_b32 v13,a53
v_accvgpr_read_b32 v14,a54
v_accvgpr_read_b32 v15,a55
v_pk_mul_f32 v[88:89],s[60:61],v[88:89] op_sel:[1,0] op_sel_hi:[1,1]
v_pk_mul_f32 v[90:91],s[60:61],v[90:91] op_sel:[1,0] op_sel_hi:[1,1]
v_pk_mul_f32 v[12:13],s[60:61],v[12:13] op_sel:[1,0] op_sel_hi:[1,1]
v_pk_mul_f32 v[14:15],s[60:61],v[14:15] op_sel:[1,0] op_sel_hi:[1,1]
v_cvt_pk_bf16_f32 v88,v88,v89
v_cvt_pk_bf16_f32 v89,v90,v91
v_cvt_pk_bf16_f32 v90,v12,v13
v_cvt_pk_bf16_f32 v91,v14,v15
v_mfma_f32_16x16x32_bf16 a[56:59],a[160:163],a[200:203],a[56:59]
v_mul_f32 v96,s61,v96
v_mul_f32 v97,s61,v97
v_mfma_f32_16x16x32_bf16 a[88:91],a[160:163],a[208:211],a[88:91]
v_accvgpr_read_b32 v98,a66
v_accvgpr_read_b32 v99,a67
v_mfma_f32_16x16x32_bf16 a[88:91],a[164:167],a[212:215],a[88:91]
buffer_load_dwordx4 v5,s[24:27],s47 offen offset:1024 nt lds
v_mfma_f32_16x16x32_bf16 a[88:91],a[168:171],a[240:243],a[88:91]
v_cvt_pk_bf16_f32 v96,v96,v97
v_mul_f32 v98,s61,v98
v_mfma_f32_16x16x32_bf16 a[88:91],a[172:175],a[244:247],a[88:91]
v_mul_f32 v99,s61,v99
v_cvt_pk_bf16_f32 v97,v98,v99
v_mfma_f32_16x16x32_bf16 a[120:123],a[172:175],a[252:255],a[120:123]
ds_read_b64_tr_b16 a[172:173],v22 offset:50696
v_accvgpr_read_b32 v84,a40
v_mfma_f32_16x16x32_bf16 a[120:123],a[168:171],a[248:251],a[120:123]
ds_read_b64_tr_b16 a[168:169],v22 offset:34312
v_accvgpr_read_b32 v85,a41
v_mfma_f32_16x16x32_bf16 a[120:123],a[164:167],a[220:223],a[120:123]
ds_read_b64_tr_b16 a[164:165],v22 offset:50688
v_accvgpr_read_b32 v98,a68
v_mfma_f32_16x16x32_bf16 a[120:123],a[160:163],a[216:219],a[120:123]
ds_read_b64_tr_b16 a[160:161],v22 offset:34304
v_accvgpr_read_b32 v99,a69
v_mfma_f32_16x16x32_bf16 a[28:31],a[176:179],a[192:195],a[28:31]
ds_read_b64_tr_b16 a[162:163],v22 offset:36352
v_mul_f32 v84,s61,v84
v_mfma_f32_16x16x32_bf16 a[28:31],a[180:183],a[196:199],a[28:31]
ds_read_b64_tr_b16 a[166:167],v22 offset:52736
v_mul_f32 v85,s61,v85
v_mfma_f32_16x16x32_bf16 a[28:31],a[184:187],a[224:227],a[28:31]
ds_read_b64_tr_b16 a[170:171],v22 offset:36360
v_accvgpr_read_b32 v86,a42
v_mfma_f32_16x16x32_bf16 a[28:31],a[188:191],a[228:231],a[28:31]
ds_read_b64_tr_b16 a[174:175],v22 offset:52744
v_accvgpr_read_b32 v87,a43
v_mfma_f32_16x16x32_bf16 a[60:63],a[188:191],a[236:239],a[60:63]
v_accvgpr_read_b32 v104,a80
v_accvgpr_read_b32 v105,a81
v_accvgpr_read_b32 v106,a82
v_accvgpr_read_b32 v107,a83
v_accvgpr_read_b32 v12,a84
v_accvgpr_read_b32 v13,a85
v_accvgpr_read_b32 v14,a86
v_accvgpr_read_b32 v15,a87
v_pk_mul_f32 v[104:105],s[60:61],v[104:105] op_sel:[1,0] op_sel_hi:[1,1]
v_pk_mul_f32 v[106:107],s[60:61],v[106:107] op_sel:[1,0] op_sel_hi:[1,1]
v_pk_mul_f32 v[12:13],s[60:61],v[12:13] op_sel:[1,0] op_sel_hi:[1,1]
v_pk_mul_f32 v[14:15],s[60:61],v[14:15] op_sel:[1,0] op_sel_hi:[1,1]
v_cvt_pk_bf16_f32 v104,v104,v105
v_cvt_pk_bf16_f32 v105,v106,v107
v_cvt_pk_bf16_f32 v106,v12,v13
v_cvt_pk_bf16_f32 v107,v14,v15
v_mfma_f32_16x16x32_bf16 a[60:63],a[184:187],a[232:235],a[60:63]
v_accvgpr_read_b32 v120,a112
v_accvgpr_read_b32 v121,a113
v_accvgpr_read_b32 v122,a114
v_accvgpr_read_b32 v123,a115
v_accvgpr_read_b32 v12,a116
v_accvgpr_read_b32 v13,a117
v_accvgpr_read_b32 v14,a118
v_accvgpr_read_b32 v15,a119
v_pk_mul_f32 v[120:121],s[60:61],v[120:121] op_sel:[1,0] op_sel_hi:[1,1]
v_pk_mul_f32 v[122:123],s[60:61],v[122:123] op_sel:[1,0] op_sel_hi:[1,1]
v_pk_mul_f32 v[12:13],s[60:61],v[12:13] op_sel:[1,0] op_sel_hi:[1,1]
v_pk_mul_f32 v[14:15],s[60:61],v[14:15] op_sel:[1,0] op_sel_hi:[1,1]
v_cvt_pk_bf16_f32 v120,v120,v121
v_cvt_pk_bf16_f32 v121,v122,v123
v_cvt_pk_bf16_f32 v122,v12,v13
v_cvt_pk_bf16_f32 v123,v14,v15
v_mfma_f32_16x16x32_bf16 a[60:63],a[180:183],a[204:207],a[60:63]
v_accvgpr_read_b32 v16,a70
v_accvgpr_read_b32 v17,a71
v_mfma_f32_16x16x32_bf16 a[60:63],a[176:179],a[200:203],a[60:63]
buffer_load_dwordx4 v4,s[24:27],s47 offen offset:2048 nt lds
v_mfma_f32_16x16x32_bf16 a[92:95],a[176:179],a[208:211],a[92:95]
v_mul_f32 v98,s61,v98
v_mul_f32 v99,s61,v99
v_mfma_f32_16x16x32_bf16 a[92:95],a[180:183],a[212:215],a[92:95]
v_cvt_pk_bf16_f32 v84,v84,v85
v_mul_f32 v86,s61,v86
v_mfma_f32_16x16x32_bf16 a[92:95],a[184:187],a[240:243],a[92:95]
v_mul_f32 v87,s61,v87
v_mul_f32 v16,s61,v16
v_mfma_f32_16x16x32_bf16 a[92:95],a[188:191],a[244:247],a[92:95]
v_mul_f32 v17,s61,v17
v_cvt_pk_bf16_f32 v98,v98,v99
v_mfma_f32_16x16x32_bf16 a[124:127],a[188:191],a[252:255],a[124:127]
ds_read_b64_tr_b16 a[188:189],v22 offset:50952
v_cvt_pk_bf16_f32 v85,v86,v87
v_mfma_f32_16x16x32_bf16 a[124:127],a[184:187],a[248:251],a[124:127]
ds_read_b64_tr_b16 a[184:185],v22 offset:34568
v_cvt_pk_bf16_f32 v99,v16,v17
v_mfma_f32_16x16x32_bf16 a[124:127],a[180:183],a[220:223],a[124:127]
ds_read_b64_tr_b16 a[180:181],v22 offset:50944
v_accvgpr_read_b32 v86,a44
v_mfma_f32_16x16x32_bf16 a[124:127],a[176:179],a[216:219],a[124:127]
s_waitcnt lgkmcnt(3)
ds_read_b64_tr_b16 a[176:177],v22 offset:34560
v_accvgpr_read_b32 v87,a45
v_mfma_f32_16x16x32_bf16 v[128:131],a[128:131],a[224:227],v[128:131]
ds_read_b64_tr_b16 a[178:179],v22 offset:36608
v_accvgpr_read_b32 v16,a46
v_mfma_f32_16x16x32_bf16 v[128:131],a[132:135],a[228:231],v[128:131]
ds_read_b64_tr_b16 a[182:183],v22 offset:52992
v_accvgpr_read_b32 v17,a47
v_mfma_f32_16x16x32_bf16 v[136:139],a[132:135],a[236:239],v[136:139]
ds_read_b64_tr_b16 a[186:187],v22 offset:36616
v_mul_f32 v86,s61,v86
v_mfma_f32_16x16x32_bf16 v[136:139],a[128:131],a[232:235],v[136:139]
ds_read_b64_tr_b16 a[190:191],v22 offset:53000
v_mul_f32 v87,s61,v87
v_mfma_f32_16x16x32_bf16 v[144:147],a[128:131],a[240:243],v[144:147]
v_accvgpr_read_b32 v76,a24
v_accvgpr_read_b32 v77,a25
v_accvgpr_read_b32 v78,a26
v_accvgpr_read_b32 v79,a27
v_accvgpr_read_b32 v12,a28
v_accvgpr_read_b32 v13,a29
v_accvgpr_read_b32 v14,a30
v_accvgpr_read_b32 v15,a31
v_pk_mul_f32 v[76:77],s[60:61],v[76:77] op_sel:[1,0] op_sel_hi:[1,1]
v_pk_mul_f32 v[78:79],s[60:61],v[78:79] op_sel:[1,0] op_sel_hi:[1,1]
v_pk_mul_f32 v[12:13],s[60:61],v[12:13] op_sel:[1,0] op_sel_hi:[1,1]
v_pk_mul_f32 v[14:15],s[60:61],v[14:15] op_sel:[1,0] op_sel_hi:[1,1]
v_cvt_pk_bf16_f32 v76,v76,v77
v_cvt_pk_bf16_f32 v77,v78,v79
v_cvt_pk_bf16_f32 v78,v12,v13
v_cvt_pk_bf16_f32 v79,v14,v15
v_mfma_f32_16x16x32_bf16 v[144:147],a[132:135],a[244:247],v[144:147]
buffer_load_dwordx4 v5,s[24:27],s47 offen offset:3072 nt lds
v_mfma_f32_16x16x32_bf16 v[152:155],a[132:135],a[252:255],v[152:155]
s_add_u32 s47,s47,16384
s_and_b32 s47,s47,0x3ffff
s_or_b32 s47,s47,s46
v_mfma_f32_16x16x32_bf16 v[152:155],a[128:131],a[248:251],v[152:155]
s_and_b32 s46,s54,0xfffc0000
v_mul_f32 v16,s61,v16
v_mul_f32 v17,s61,v17
v_mfma_f32_16x16x32_bf16 v[132:135],a[136:139],a[224:227],v[132:135]
s_add_u32 s54,s54,16384
v_cvt_pk_bf16_f32 v86,v86,v87
v_cvt_pk_bf16_f32 v87,v16,v17
v_mfma_f32_16x16x32_bf16 v[132:135],a[140:143],a[228:231],v[132:135]
v_accvgpr_read_b32 v92,a56
v_accvgpr_read_b32 v93,a57
v_accvgpr_read_b32 v94,a58
v_accvgpr_read_b32 v95,a59
v_accvgpr_read_b32 v12,a60
v_accvgpr_read_b32 v13,a61
v_accvgpr_read_b32 v14,a62
v_accvgpr_read_b32 v15,a63
v_pk_mul_f32 v[92:93],s[60:61],v[92:93] op_sel:[1,0] op_sel_hi:[1,1]
v_pk_mul_f32 v[94:95],s[60:61],v[94:95] op_sel:[1,0] op_sel_hi:[1,1]
v_pk_mul_f32 v[12:13],s[60:61],v[12:13] op_sel:[1,0] op_sel_hi:[1,1]
v_pk_mul_f32 v[14:15],s[60:61],v[14:15] op_sel:[1,0] op_sel_hi:[1,1]
v_cvt_pk_bf16_f32 v92,v92,v93
v_cvt_pk_bf16_f32 v93,v94,v95
v_cvt_pk_bf16_f32 v94,v12,v13
v_cvt_pk_bf16_f32 v95,v14,v15
v_mfma_f32_16x16x32_bf16 v[140:143],a[140:143],a[236:239],v[140:143]
v_accvgpr_read_b32 v108,a88
v_accvgpr_read_b32 v109,a89
v_accvgpr_read_b32 v110,a90
v_accvgpr_read_b32 v111,a91
v_accvgpr_read_b32 v12,a92
v_accvgpr_read_b32 v13,a93
v_accvgpr_read_b32 v14,a94
v_accvgpr_read_b32 v15,a95
v_pk_mul_f32 v[108:109],s[60:61],v[108:109] op_sel:[1,0] op_sel_hi:[1,1]
v_pk_mul_f32 v[110:111],s[60:61],v[110:111] op_sel:[1,0] op_sel_hi:[1,1]
v_pk_mul_f32 v[12:13],s[60:61],v[12:13] op_sel:[1,0] op_sel_hi:[1,1]
v_pk_mul_f32 v[14:15],s[60:61],v[14:15] op_sel:[1,0] op_sel_hi:[1,1]
v_cvt_pk_bf16_f32 v108,v108,v109
v_cvt_pk_bf16_f32 v109,v110,v111
v_cvt_pk_bf16_f32 v110,v12,v13
v_cvt_pk_bf16_f32 v111,v14,v15
v_mfma_f32_16x16x32_bf16 v[140:143],a[136:139],a[232:235],v[140:143]
v_accvgpr_read_b32 v124,a120
v_accvgpr_read_b32 v125,a121
v_accvgpr_read_b32 v126,a122
v_accvgpr_read_b32 v127,a123
v_accvgpr_read_b32 v12,a124
v_accvgpr_read_b32 v13,a125
v_accvgpr_read_b32 v14,a126
v_accvgpr_read_b32 v15,a127
v_pk_mul_f32 v[124:125],s[60:61],v[124:125] op_sel:[1,0] op_sel_hi:[1,1]
v_pk_mul_f32 v[126:127],s[60:61],v[126:127] op_sel:[1,0] op_sel_hi:[1,1]
v_pk_mul_f32 v[12:13],s[60:61],v[12:13] op_sel:[1,0] op_sel_hi:[1,1]
v_pk_mul_f32 v[14:15],s[60:61],v[14:15] op_sel:[1,0] op_sel_hi:[1,1]
v_cvt_pk_bf16_f32 v124,v124,v125
v_cvt_pk_bf16_f32 v125,v126,v127
v_cvt_pk_bf16_f32 v126,v12,v13
v_cvt_pk_bf16_f32 v127,v14,v15
v_mfma_f32_16x16x32_bf16 v[148:151],a[136:139],a[240:243],v[148:151]
v_mfma_f32_16x16x32_bf16 v[148:151],a[140:143],a[244:247],v[148:151]
v_mfma_f32_16x16x32_bf16 v[156:159],a[140:143],a[252:255],v[156:159]
s_waitcnt vmcnt(8) lgkmcnt(0)
v_mfma_f32_16x16x32_bf16 v[156:159],a[136:139],a[248:251],v[156:159]
s_barrier
ds_read_b128 v[48:51],v18
v_mfma_f32_16x16x32_bf16 v[160:163],a[144:147],a[224:227],v[160:163]
ds_read_b64_tr_b16 a[192:193],v2
v_mfma_f32_16x16x32_bf16 v[160:163],a[148:151],a[228:231],v[160:163]
ds_read_b64_tr_b16 a[194:195],v2 offset:4096
v_mfma_f32_16x16x32_bf16 v[168:171],a[148:151],a[236:239],v[168:171]
ds_read_b64_tr_b16 a[196:197],v2 offset:8192
v_mfma_f32_16x16x32_bf16 v[168:171],a[144:147],a[232:235],v[168:171]
ds_read_b128 v[52:55],v18 offset:256
v_mfma_f32_16x16x32_bf16 v[176:179],a[144:147],a[240:243],v[176:179]
v_mul_f32 v128,s61,v128
v_mul_f32 v129,s61,v129
v_mul_f32 v130,s61,v130
v_mul_f32 v131,s61,v131
v_cvt_pk_bf16_f32 v128,v128,v129
v_cvt_pk_bf16_f32 v129,v130,v131
v_pk_mul_f32 v[132:133],s[60:61],v[132:133] op_sel:[1,0] op_sel_hi:[1,1]
v_pk_mul_f32 v[134:135],s[60:61],v[134:135] op_sel:[1,0] op_sel_hi:[1,1]
v_pk_mul_f32 v[136:137],s[60:61],v[136:137] op_sel:[1,0] op_sel_hi:[1,1]
v_pk_mul_f32 v[138:139],s[60:61],v[138:139] op_sel:[1,0] op_sel_hi:[1,1]
v_pk_mul_f32 v[140:141],s[60:61],v[140:141] op_sel:[1,0] op_sel_hi:[1,1]
)A"
R"A(v_pk_mul_f32 v[142:143],s[60:61],v[142:143] op_sel:[1,0] op_sel_hi:[1,1]
v_pk_mul_f32 v[144:145],s[60:61],v[144:145] op_sel:[1,0] op_sel_hi:[1,1]
v_pk_mul_f32 v[146:147],s[60:61],v[146:147] op_sel:[1,0] op_sel_hi:[1,1]
v_pk_mul_f32 v[148:149],s[60:61],v[148:149] op_sel:[1,0] op_sel_hi:[1,1]
v_pk_mul_f32 v[150:151],s[60:61],v[150:151] op_sel:[1,0] op_sel_hi:[1,1]
v_pk_mul_f32 v[152:153],s[60:61],v[152:153] op_sel:[1,0] op_sel_hi:[1,1]
v_pk_mul_f32 v[154:155],s[60:61],v[154:155] op_sel:[1,0] op_sel_hi:[1,1]
v_pk_mul_f32 v[156:157],s[60:61],v[156:157] op_sel:[1,0] op_sel_hi:[1,1]
v_pk_mul_f32 v[158:159],s[60:61],v[158:159] op_sel:[1,0] op_sel_hi:[1,1]
v_mfma_f32_16x16x32_bf16 v[176:179],a[148:151],a[244:247],v[176:179]
ds_read_b128 v[56:59],v18 offset:512
v_cvt_pk_bf16_f32 v130,v132,v133
v_mfma_f32_16x16x32_bf16 v[184:187],a[148:151],a[252:255],v[184:187]
v_cvt_pk_bf16_f32 v132,v136,v137
v_cvt_pk_bf16_f32 v133,v138,v139
v_mfma_f32_16x16x32_bf16 v[184:187],a[144:147],a[248:251],v[184:187]
ds_read_b128 v[60:63],v18 offset:768
v_cvt_pk_bf16_f32 v131,v134,v135
v_mfma_f32_16x16x32_bf16 v[164:167],a[152:155],a[224:227],v[164:167]
v_cvt_pk_bf16_f32 v134,v140,v141
v_cvt_pk_bf16_f32 v135,v142,v143
v_mfma_f32_16x16x32_bf16 v[164:167],a[156:159],a[228:231],v[164:167]
ds_read_b128 a[128:131],v1
v_cvt_pk_bf16_f32 v144,v144,v145
v_mfma_f32_16x16x32_bf16 v[172:175],a[156:159],a[236:239],v[172:175]
v_cvt_pk_bf16_f32 v145,v146,v147
v_cvt_pk_bf16_f32 v146,v148,v149
v_mfma_f32_16x16x32_bf16 v[172:175],a[152:155],a[232:235],v[172:175]
ds_read_b128 a[132:135],v1 offset:256
v_cvt_pk_bf16_f32 v148,v152,v153
v_mfma_f32_16x16x32_bf16 v[180:183],a[152:155],a[240:243],v[180:183]
v_cvt_pk_bf16_f32 v149,v154,v155
v_cvt_pk_bf16_f32 v147,v150,v151
v_mfma_f32_16x16x32_bf16 v[180:183],a[156:159],a[244:247],v[180:183]
ds_read_b128 a[136:139],v1 offset:512
v_cvt_pk_bf16_f32 v150,v156,v157
v_mfma_f32_16x16x32_bf16 v[188:191],a[156:159],a[252:255],v[188:191]
ds_read_b64_tr_b16 a[198:199],v2 offset:12288
v_cvt_pk_bf16_f32 v151,v158,v159
v_mfma_f32_16x16x32_bf16 v[188:191],a[152:155],a[248:251],v[188:191]
ds_read_b64_tr_b16 a[204:205],v3 offset:8192
v_mfma_f32_16x16x32_bf16 v[192:195],a[160:163],a[224:227],v[192:195]
ds_read_b64_tr_b16 a[206:207],v3 offset:12288
v_mfma_f32_16x16x32_bf16 v[192:195],a[164:167],a[228:231],v[192:195]
s_add_u32 m0,32768,s51
ds_read_b128 a[140:143],v1 offset:768
v_mfma_f32_16x16x32_bf16 v[200:203],a[164:167],a[236:239],v[200:203]
ds_read_b64_tr_b16 a[200:201],v3
v_mfma_f32_16x16x32_bf16 v[200:203],a[160:163],a[232:235],v[200:203]
ds_read_b64_tr_b16 a[202:203],v3 offset:4096
v_mfma_f32_16x16x32_bf16 v[208:211],a[160:163],a[240:243],v[208:211]
buffer_load_dwordx4 v4,s[20:23],s47 offen nt lds
v_mfma_f32_16x16x32_bf16 v[208:211],a[164:167],a[244:247],v[208:211]
ds_read_b128 a[156:159],v1 offset:4864
v_mfma_f32_16x16x32_bf16 v[216:219],a[164:167],a[252:255],v[216:219]
v_mul_f32 v160,s61,v160
v_mul_f32 v161,s61,v161
v_mul_f32 v162,s61,v162
v_mul_f32 v163,s61,v163
v_cvt_pk_bf16_f32 v160,v160,v161
v_cvt_pk_bf16_f32 v161,v162,v163
v_pk_mul_f32 v[164:165],s[60:61],v[164:165] op_sel:[1,0] op_sel_hi:[1,1]
v_pk_mul_f32 v[166:167],s[60:61],v[166:167] op_sel:[1,0] op_sel_hi:[1,1]
v_pk_mul_f32 v[168:169],s[60:61],v[168:169] op_sel:[1,0] op_sel_hi:[1,1]
v_pk_mul_f32 v[170:171],s[60:61],v[170:171] op_sel:[1,0] op_sel_hi:[1,1]
v_pk_mul_f32 v[172:173],s[60:61],v[172:173] op_sel:[1,0] op_sel_hi:[1,1]
v_pk_mul_f32 v[174:175],s[60:61],v[174:175] op_sel:[1,0] op_sel_hi:[1,1]
v_pk_mul_f32 v[176:177],s[60:61],v[176:177] op_sel:[1,0] op_sel_hi:[1,1]
v_pk_mul_f32 v[178:179],s[60:61],v[178:179] op_sel:[1,0] op_sel_hi:[1,1]
v_pk_mul_f32 v[180:181],s[60:61],v[180:181] op_sel:[1,0] op_sel_hi:[1,1]
v_pk_mul_f32 v[182:183],s[60:61],v[182:183] op_sel:[1,0] op_sel_hi:[1,1]
v_pk_mul_f32 v[184:185],s[60:61],v[184:185] op_sel:[1,0] op_sel_hi:[1,1]
v_pk_mul_f32 v[186:187],s[60:61],v[186:187] op_sel:[1,0] op_sel_hi:[1,1]
v_pk_mul_f32 v[188:189],s[60:61],v[188:189] op_sel:[1,0] op_sel_hi:[1,1]
v_pk_mul_f32 v[190:191],s[60:61],v[190:191] op_sel:[1,0] op_sel_hi:[1,1]
v_mfma_f32_16x16x32_bf16 v[216:219],a[160:163],a[248:251],v[216:219]
ds_read_b128 a[152:155],v1 offset:4608
v_cvt_pk_bf16_f32 v162,v164,v165
v_mfma_f32_16x16x32_bf16 v[196:199],a[168:171],a[224:227],v[196:199]
v_cvt_pk_bf16_f32 v164,v168,v169
v_cvt_pk_bf16_f32 v165,v170,v171
v_mfma_f32_16x16x32_bf16 v[196:199],a[172:175],a[228:231],v[196:199]
ds_read_b128 a[148:151],v1 offset:4352
v_cvt_pk_bf16_f32 v163,v166,v167
v_mfma_f32_16x16x32_bf16 v[204:207],a[172:175],a[236:239],v[204:207]
v_cvt_pk_bf16_f32 v166,v172,v173
v_cvt_pk_bf16_f32 v167,v174,v175
v_mfma_f32_16x16x32_bf16 v[204:207],a[168:171],a[232:235],v[204:207]
ds_read_b128 a[144:147],v1 offset:4096
v_cvt_pk_bf16_f32 v176,v176,v177
v_mfma_f32_16x16x32_bf16 v[212:215],a[168:171],a[240:243],v[212:215]
v_cvt_pk_bf16_f32 v177,v178,v179
v_cvt_pk_bf16_f32 v178,v180,v181
v_mfma_f32_16x16x32_bf16 v[212:215],a[172:175],a[244:247],v[212:215]
ds_read_b128 a[160:163],v1 offset:8192
v_cvt_pk_bf16_f32 v180,v184,v185
v_mfma_f32_16x16x32_bf16 v[220:223],a[172:175],a[252:255],v[220:223]
v_cvt_pk_bf16_f32 v181,v186,v187
v_cvt_pk_bf16_f32 v179,v182,v183
v_mfma_f32_16x16x32_bf16 v[220:223],a[168:171],a[248:251],v[220:223]
ds_read_b128 a[164:167],v1 offset:8448
v_cvt_pk_bf16_f32 v182,v188,v189
v_mfma_f32_16x16x32_bf16 v[224:227],a[176:179],a[224:227],v[224:227]
ds_read_b64_tr_b16 a[208:209],v2 offset:256
v_cvt_pk_bf16_f32 v183,v190,v191
v_mfma_f32_16x16x32_bf16 v[224:227],a[180:183],a[228:231],v[224:227]
buffer_load_dwordx4 v4,s[20:23],s47 offen offset:1024 nt lds
v_mfma_f32_16x16x32_bf16 v[232:235],a[180:183],a[236:239],v[232:235]
ds_read_b128 a[168:171],v1 offset:8704
v_mfma_f32_16x16x32_bf16 v[232:235],a[176:179],a[232:235],v[232:235]
ds_read_b64_tr_b16 a[210:211],v2 offset:4352
v_mfma_f32_16x16x32_bf16 v[240:243],a[176:179],a[240:243],v[240:243]
ds_read_b64_tr_b16 a[212:213],v2 offset:8448
v_mfma_f32_16x16x32_bf16 v[240:243],a[180:183],a[244:247],v[240:243]
v_mul_f32 v192,s61,v192
v_mul_f32 v193,s61,v193
v_mul_f32 v194,s61,v194
v_mul_f32 v195,s61,v195
v_cvt_pk_bf16_f32 v192,v192,v193
v_cvt_pk_bf16_f32 v193,v194,v195
v_pk_mul_f32 v[196:197],s[60:61],v[196:197] op_sel:[1,0] op_sel_hi:[1,1]
v_pk_mul_f32 v[198:199],s[60:61],v[198:199] op_sel:[1,0] op_sel_hi:[1,1]
v_pk_mul_f32 v[200:201],s[60:61],v[200:201] op_sel:[1,0] op_sel_hi:[1,1]
v_pk_mul_f32 v[202:203],s[60:61],v[202:203] op_sel:[1,0] op_sel_hi:[1,1]
v_pk_mul_f32 v[204:205],s[60:61],v[204:205] op_sel:[1,0] op_sel_hi:[1,1]
v_pk_mul_f32 v[206:207],s[60:61],v[206:207] op_sel:[1,0] op_sel_hi:[1,1]
v_pk_mul_f32 v[208:209],s[60:61],v[208:209] op_sel:[1,0] op_sel_hi:[1,1]
v_pk_mul_f32 v[210:211],s[60:61],v[210:211] op_sel:[1,0] op_sel_hi:[1,1]
v_pk_mul_f32 v[212:213],s[60:61],v[212:213] op_sel:[1,0] op_sel_hi:[1,1]
v_pk_mul_f32 v[214:215],s[60:61],v[214:215] op_sel:[1,0] op_sel_hi:[1,1]
v_pk_mul_f32 v[216:217],s[60:61],v[216:217] op_sel:[1,0] op_sel_hi:[1,1]
v_pk_mul_f32 v[218:219],s[60:61],v[218:219] op_sel:[1,0] op_sel_hi:[1,1]
v_pk_mul_f32 v[220:221],s[60:61],v[220:221] op_sel:[1,0] op_sel_hi:[1,1]
v_pk_mul_f32 v[222:223],s[60:61],v[222:223] op_sel:[1,0] op_sel_hi:[1,1]
v_mfma_f32_16x16x32_bf16 v[248:251],a[180:183],a[252:255],v[248:251]
ds_read_b128 a[172:175],v1 offset:8960
v_cvt_pk_bf16_f32 v194,v196,v197
v_mfma_f32_16x16x32_bf16 v[248:251],a[176:179],a[248:251],v[248:251]
v_cvt_pk_bf16_f32 v196,v200,v201
v_cvt_pk_bf16_f32 v197,v202,v203
v_mfma_f32_16x16x32_bf16 v[228:231],a[184:187],a[224:227],v[228:231]
ds_read_b128 a[180:183],v1 offset:12544
v_cvt_pk_bf16_f32 v195,v198,v199
v_mfma_f32_16x16x32_bf16 v[228:231],a[188:191],a[228:231],v[228:231]
v_cvt_pk_bf16_f32 v198,v204,v205
v_cvt_pk_bf16_f32 v199,v206,v207
v_mfma_f32_16x16x32_bf16 v[236:239],a[188:191],a[236:239],v[236:239]
ds_read_b128 a[176:179],v1 offset:12288
v_cvt_pk_bf16_f32 v208,v208,v209
v_mfma_f32_16x16x32_bf16 v[236:239],a[184:187],a[232:235],v[236:239]
v_cvt_pk_bf16_f32 v209,v210,v211
v_cvt_pk_bf16_f32 v210,v212,v213
v_mfma_f32_16x16x32_bf16 v[244:247],a[184:187],a[240:243],v[244:247]
v_cvt_pk_bf16_f32 v212,v216,v217
v_cvt_pk_bf16_f32 v213,v218,v219
v_mfma_f32_16x16x32_bf16 v[244:247],a[188:191],a[244:247],v[244:247]
v_cvt_pk_bf16_f32 v211,v214,v215
v_cvt_pk_bf16_f32 v214,v220,v221
v_mfma_f32_16x16x32_bf16 v[252:255],a[188:191],a[252:255],v[252:255]
buffer_load_dwordx4 v5,s[20:23],s47 offen offset:2048 nt lds
v_mfma_f32_16x16x32_bf16 v[252:255],a[184:187],a[248:251],v[252:255]
s_waitcnt lgkmcnt(2)
ds_read_b128 a[188:191],v1 offset:13056
v_cvt_pk_bf16_f32 v215,v222,v223
v_mfma_f32_16x16x32_bf16 v[32:35],v[48:51],v[64:67],0
ds_read_b64_tr_b16 a[214:215],v2 offset:12544
v_mfma_f32_16x16x32_bf16 v[32:35],v[52:55],v[68:71],v[32:35]
ds_read_b64_tr_b16 a[220:221],v3 offset:8448
v_mfma_f32_16x16x32_bf16 v[32:35],v[56:59],v[72:75],v[32:35]
ds_read_b64_tr_b16 a[222:223],v3 offset:12544
v_mfma_f32_16x16x32_bf16 v[32:35],v[60:63],v[76:79],v[32:35]
ds_read_b128 a[184:187],v1 offset:12800
v_add_u32 v1,s48,v1
v_mfma_f32_16x16x32_bf16 v[36:39],v[48:51],v[80:83],0
ds_read_b64_tr_b16 a[216:217],v3 offset:256
v_mfma_f32_16x16x32_bf16 v[36:39],v[52:55],v[84:87],v[36:39]
ds_read_b64_tr_b16 a[218:219],v3 offset:4352
v_mfma_f32_16x16x32_bf16 v[36:39],v[56:59],v[88:91],v[36:39]
ds_read_b64_tr_b16 a[224:225],v2 offset:512
v_mfma_f32_16x16x32_bf16 v[36:39],v[60:63],v[92:95],v[36:39]
ds_read_b64_tr_b16 a[226:227],v2 offset:4608
ds_read_b64_tr_b16 a[228:229],v2 offset:8704
v_mfma_f32_16x16x32_bf16 v[40:43],v[48:51],v[96:99],0
ds_read_b64_tr_b16 a[230:231],v2 offset:12800
v_mfma_f32_16x16x32_bf16 v[40:43],v[52:55],v[100:103],v[40:43]
v_max3_f32 v32,v32,v33,v34
v_max3_f32 v32,v32,v35,s18
v_mfma_f32_16x16x32_bf16 v[40:43],v[56:59],v[104:107],v[40:43]
v_sub_f32 v35,0,v32
v_sub_f32 v34,0,v32
v_mfma_f32_16x16x32_bf16 v[40:43],v[60:63],v[108:111],v[40:43]
v_sub_f32 v33,0,v32
v_sub_f32 v32,0,v32
v_mfma_f32_16x16x32_bf16 v[44:47],v[48:51],v[112:115],0
buffer_load_dwordx4 v5,s[20:23],s47 offen offset:3072 nt lds
v_mfma_f32_16x16x32_bf16 v[44:47],v[52:55],v[116:119],v[44:47]
v_max3_f32 v36,v36,v37,v38
v_max3_f32 v36,v36,v39,s19
v_mfma_f32_16x16x32_bf16 v[44:47],v[56:59],v[120:123],v[44:47]
v_sub_f32 v39,0,v36
v_sub_f32 v38,0,v36
v_mfma_f32_16x16x32_bf16 v[44:47],v[60:63],v[124:127],v[44:47]
v_sub_f32 v37,0,v36
v_sub_f32 v36,0,v36
v_mfma_f32_16x16x32_bf16 v[136:139],a[128:131],v[64:67],v[32:35]
ds_read_b64_tr_b16 a[236:237],v3 offset:8704
ds_read_b64_tr_b16 a[238:239],v3 offset:12800
v_mfma_f32_16x16x32_bf16 v[136:139],a[132:135],v[68:71],v[136:139]
v_max3_f32 v40,v40,v41,v42
v_max3_f32 v40,v40,v43,s100
v_mfma_f32_16x16x32_bf16 v[136:139],a[136:139],v[72:75],v[136:139]
v_sub_f32 v43,0,v40
v_sub_f32 v42,0,v40
v_mfma_f32_16x16x32_bf16 v[136:139],a[140:143],v[76:79],v[136:139]
v_sub_f32 v41,0,v40
v_sub_f32 v40,0,v40
v_mfma_f32_16x16x32_bf16 v[140:143],a[156:159],v[76:79],v[32:35]
ds_read_b64_tr_b16 a[232:233],v3 offset:512
ds_read_b64_tr_b16 a[234:235],v3 offset:4608
v_mfma_f32_16x16x32_bf16 v[140:143],a[152:155],v[72:75],v[140:143]
s_add_u32 m0,32768,s52
ds_read_b64_tr_b16 a[240:241],v2 offset:768
v_max3_f32 v44,v44,v45,v46
v_mfma_f32_16x16x32_bf16 v[140:143],a[148:151],v[68:71],v[140:143]
ds_read_b64_tr_b16 a[242:243],v2 offset:4864
v_max3_f32 v44,v44,v47,s101
v_mfma_f32_16x16x32_bf16 v[140:143],a[144:147],v[64:67],v[140:143]
ds_read_b64_tr_b16 a[244:245],v2 offset:8960
v_mov_b32 v48,0
v_mfma_f32_16x16x32_bf16 v[152:155],a[160:163],v[64:67],v[32:35]
buffer_load_dwordx4 v4,s[24:27],s47 offen nt lds
v_mfma_f32_16x16x32_bf16 v[152:155],a[164:167],v[68:71],v[152:155]
v_exp_f32 v136,v136
v_mfma_f32_16x16x32_bf16 v[152:155],a[168:171],v[72:75],v[152:155]
v_exp_f32 v137,v137
v_mfma_f32_16x16x32_bf16 v[152:155],a[172:175],v[76:79],v[152:155]
s_waitcnt lgkmcnt(5)
v_exp_f32 v138,v138
v_mfma_f32_16x16x32_bf16 v[156:159],a[188:191],v[76:79],v[32:35]
v_exp_f32 v139,v139
v_mfma_f32_16x16x32_bf16 v[156:159],a[184:187],v[72:75],v[156:159]
v_cvt_pk_bf16_f32 v136,v136,v137
)A"
R"A(v_cvt_pk_bf16_f32 v137,v138,v139
v_mfma_f32_16x16x32_bf16 v[156:159],a[180:183],v[68:71],v[156:159]
v_exp_f32 v140,v140
v_mfma_f32_16x16x32_bf16 v[156:159],a[176:179],v[64:67],v[156:159]
v_exp_f32 v141,v141
v_mfma_f32_16x16x32_bf16 v[168:171],a[128:131],v[80:83],v[36:39]
v_exp_f32 v142,v142
v_mfma_f32_16x16x32_bf16 v[168:171],a[132:135],v[84:87],v[168:171]
v_exp_f32 v143,v143
v_mfma_f32_16x16x32_bf16 v[168:171],a[136:139],v[88:91],v[168:171]
v_cvt_pk_bf16_f32 v138,v140,v141
v_cvt_pk_bf16_f32 v139,v142,v143
v_mfma_f32_16x16x32_bf16 v[168:171],a[140:143],v[92:95],v[168:171]
v_exp_f32 v152,v152
v_mfma_f32_16x16x32_bf16 v[172:175],a[156:159],v[92:95],v[36:39]
v_exp_f32 v153,v153
v_mfma_f32_16x16x32_bf16 v[172:175],a[152:155],v[88:91],v[172:175]
buffer_load_dwordx4 v5,s[24:27],s47 offen offset:1024 nt lds
v_mfma_f32_16x16x32_bf16 v[172:175],a[148:151],v[84:87],v[172:175]
v_exp_f32 v154,v154
v_mfma_f32_16x16x32_bf16 v[172:175],a[144:147],v[80:83],v[172:175]
v_exp_f32 v155,v155
v_mfma_f32_16x16x32_bf16 v[184:187],a[160:163],v[80:83],v[36:39]
v_exp_f32 v156,v156
v_mfma_f32_16x16x32_bf16 v[184:187],a[164:167],v[84:87],v[184:187]
v_exp_f32 v157,v157
v_mfma_f32_16x16x32_bf16 v[184:187],a[168:171],v[88:91],v[184:187]
v_exp_f32 v158,v158
v_mfma_f32_16x16x32_bf16 v[184:187],a[172:175],v[92:95],v[184:187]
v_exp_f32 v159,v159
v_mfma_f32_16x16x32_bf16 v[188:191],a[188:191],v[92:95],v[36:39]
v_cvt_pk_bf16_f32 v140,v152,v153
v_cvt_pk_bf16_f32 v141,v154,v155
v_mfma_f32_16x16x32_bf16 v[188:191],a[184:187],v[88:91],v[188:191]
v_cvt_pk_bf16_f32 v142,v156,v157
v_cvt_pk_bf16_f32 v143,v158,v159
v_mfma_f32_16x16x32_bf16 v[188:191],a[180:183],v[84:87],v[188:191]
ds_read_b64_tr_b16 a[246:247],v2 offset:13056
ds_read_b64_tr_b16 a[252:253],v3 offset:8960
v_mfma_f32_16x16x32_bf16 v[188:191],a[176:179],v[80:83],v[188:191]
ds_read_b64_tr_b16 a[254:255],v3 offset:13056
v_mov_b32 v49,0
v_mfma_f32_16x16x32_bf16 v[200:203],a[128:131],v[96:99],v[40:43]
ds_read_b64_tr_b16 a[248:249],v3 offset:768
v_mov_b32 v50,0
v_mfma_f32_16x16x32_bf16 v[200:203],a[132:135],v[100:103],v[200:203]
ds_read_b64_tr_b16 a[250:251],v3 offset:4864
v_mov_b32 v51,0
v_mfma_f32_16x16x32_bf16 v[200:203],a[136:139],v[104:107],v[200:203]
buffer_load_dwordx4 v4,s[24:27],s47 offen offset:2048 nt lds
v_mfma_f32_16x16x32_bf16 v[200:203],a[140:143],v[108:111],v[200:203]
v_sub_f32 v47,0,v44
v_sub_f32 v46,0,v44
v_mfma_f32_16x16x32_bf16 v[204:207],a[156:159],v[108:111],v[40:43]
v_sub_f32 v45,0,v44
v_sub_f32 v44,0,v44
v_mfma_f32_16x16x32_bf16 v[204:207],a[152:155],v[104:107],v[204:207]
v_mul_f32 v224,s61,v224
v_mul_f32 v225,s61,v225
v_mul_f32 v226,s61,v226
v_mul_f32 v227,s61,v227
v_cvt_pk_bf16_f32 v224,v224,v225
v_cvt_pk_bf16_f32 v225,v226,v227
v_pk_mul_f32 v[228:229],s[60:61],v[228:229] op_sel:[1,0] op_sel_hi:[1,1]
v_pk_mul_f32 v[230:231],s[60:61],v[230:231] op_sel:[1,0] op_sel_hi:[1,1]
v_pk_mul_f32 v[232:233],s[60:61],v[232:233] op_sel:[1,0] op_sel_hi:[1,1]
v_pk_mul_f32 v[234:235],s[60:61],v[234:235] op_sel:[1,0] op_sel_hi:[1,1]
v_pk_mul_f32 v[236:237],s[60:61],v[236:237] op_sel:[1,0] op_sel_hi:[1,1]
v_pk_mul_f32 v[238:239],s[60:61],v[238:239] op_sel:[1,0] op_sel_hi:[1,1]
v_pk_mul_f32 v[240:241],s[60:61],v[240:241] op_sel:[1,0] op_sel_hi:[1,1]
v_pk_mul_f32 v[242:243],s[60:61],v[242:243] op_sel:[1,0] op_sel_hi:[1,1]
v_pk_mul_f32 v[244:245],s[60:61],v[244:245] op_sel:[1,0] op_sel_hi:[1,1]
v_pk_mul_f32 v[246:247],s[60:61],v[246:247] op_sel:[1,0] op_sel_hi:[1,1]
v_pk_mul_f32 v[248:249],s[60:61],v[248:249] op_sel:[1,0] op_sel_hi:[1,1]
v_pk_mul_f32 v[250:251],s[60:61],v[250:251] op_sel:[1,0] op_sel_hi:[1,1]
v_pk_mul_f32 v[252:253],s[60:61],v[252:253] op_sel:[1,0] op_sel_hi:[1,1]
v_pk_mul_f32 v[254:255],s[60:61],v[254:255] op_sel:[1,0] op_sel_hi:[1,1]
v_mfma_f32_16x16x32_bf16 v[204:207],a[148:151],v[100:103],v[204:207]
v_cvt_pk_bf16_f32 v226,v228,v229
v_cvt_pk_bf16_f32 v228,v232,v233
v_mfma_f32_16x16x32_bf16 v[204:207],a[144:147],v[96:99],v[204:207]
v_cvt_pk_bf16_f32 v229,v234,v235
v_cvt_pk_bf16_f32 v227,v230,v231
v_mfma_f32_16x16x32_bf16 v[216:219],a[160:163],v[96:99],v[40:43]
v_cvt_pk_bf16_f32 v230,v236,v237
v_cvt_pk_bf16_f32 v231,v238,v239
v_mfma_f32_16x16x32_bf16 v[216:219],a[164:167],v[100:103],v[216:219]
v_cvt_pk_bf16_f32 v240,v240,v241
v_cvt_pk_bf16_f32 v241,v242,v243
v_mfma_f32_16x16x32_bf16 v[216:219],a[168:171],v[104:107],v[216:219]
v_cvt_pk_bf16_f32 v242,v244,v245
v_cvt_pk_bf16_f32 v244,v248,v249
v_mfma_f32_16x16x32_bf16 v[216:219],a[172:175],v[108:111],v[216:219]
v_cvt_pk_bf16_f32 v245,v250,v251
v_cvt_pk_bf16_f32 v243,v246,v247
v_mfma_f32_16x16x32_bf16 v[220:223],a[188:191],v[108:111],v[40:43]
v_cvt_pk_bf16_f32 v246,v252,v253
v_cvt_pk_bf16_f32 v247,v254,v255
v_mfma_f32_16x16x32_bf16 v[220:223],a[184:187],v[104:107],v[220:223]
v_exp_f32 v168,v168
v_mfma_f32_16x16x32_bf16 v[220:223],a[180:183],v[100:103],v[220:223]
v_exp_f32 v169,v169
v_mfma_f32_16x16x32_bf16 v[220:223],a[176:179],v[96:99],v[220:223]
buffer_load_dwordx4 v5,s[24:27],s47 offen offset:3072 nt lds
v_mfma_f32_16x16x32_bf16 a[0:3],a[192:195],v[136:139],0
s_add_u32 s47,s47,16384
s_and_b32 s47,s47,0x3ffff
s_or_b32 s47,s47,s46
v_mfma_f32_16x16x32_bf16 a[0:3],a[196:199],v[140:143],a[0:3]
s_and_b32 s46,s54,0xfffc0000
v_exp_f32 v170,v170
v_mfma_f32_16x16x32_bf16 a[4:7],a[204:207],v[140:143],0
v_exp_f32 v171,v171
v_mfma_f32_16x16x32_bf16 a[4:7],a[200:203],v[136:139],a[4:7]
s_memrealtime s[76:77]
v_cvt_pk_bf16_f32 v168,v168,v169
v_cvt_pk_bf16_f32 v169,v170,v171
v_mfma_f32_16x16x32_bf16 a[8:11],a[208:211],v[136:139],0
v_exp_f32 v172,v172
v_mfma_f32_16x16x32_bf16 a[8:11],a[212:215],v[140:143],a[8:11]
v_exp_f32 v173,v173
v_mfma_f32_16x16x32_bf16 a[12:15],a[220:223],v[140:143],0
v_exp_f32 v174,v174
v_mfma_f32_16x16x32_bf16 a[12:15],a[216:219],v[136:139],a[12:15]
v_exp_f32 v175,v175
v_mfma_f32_16x16x32_bf16 a[16:19],a[224:227],v[136:139],0
v_cvt_pk_bf16_f32 v170,v172,v173
v_cvt_pk_bf16_f32 v171,v174,v175
v_mfma_f32_16x16x32_bf16 a[16:19],a[228:231],v[140:143],a[16:19]
v_exp_f32 v184,v184
v_mfma_f32_16x16x32_bf16 a[20:23],a[236:239],v[140:143],0
s_waitcnt lgkmcnt(0)
v_exp_f32 v185,v185
v_mfma_f32_16x16x32_bf16 a[20:23],a[232:235],v[136:139],a[20:23]
s_add_u32 s78,s76,s60
v_exp_f32 v186,v186
v_mfma_f32_16x16x32_bf16 a[24:27],a[240:243],v[136:139],0
s_lshr_b32 s60,s94,2
v_exp_f32 v187,v187
v_mfma_f32_16x16x32_bf16 a[24:27],a[244:247],v[140:143],a[24:27]
s_max_i32 s60,s60,128
v_exp_f32 v188,v188
v_mfma_f32_16x16x32_bf16 a[28:31],a[252:255],v[140:143],0
v_exp_f32 v189,v189
v_mfma_f32_16x16x32_bf16 a[28:31],a[248:251],v[136:139],a[28:31]
v_exp_f32 v190,v190
v_smfmac_f32_16x16x64_bf16 v[48:51],v[28:31],v[136:143],v27
v_cvt_pk_bf16_f32 v172,v184,v185
v_mfma_f32_16x16x32_bf16 v[232:235],a[128:131],v[112:115],v[44:47]
s_waitcnt vmcnt(8)
v_exp_f32 v191,v191
v_mfma_f32_16x16x32_bf16 v[232:235],a[132:135],v[116:119],v[232:235]
s_barrier
s_add_u32 m0,65536,s51
v_cvt_pk_bf16_f32 v173,v186,v187
v_mfma_f32_16x16x32_bf16 v[232:235],a[136:139],v[120:123],v[232:235]
v_cvt_pk_bf16_f32 v174,v188,v189
v_cvt_pk_bf16_f32 v175,v190,v191
v_mfma_f32_16x16x32_bf16 v[232:235],a[140:143],v[124:127],v[232:235]
ds_read_b128 a[128:131],v1
v_mov_b32 v52,0
v_mfma_f32_16x16x32_bf16 v[236:239],a[156:159],v[124:127],v[44:47]
buffer_load_dwordx4 v4,s[20:23],s47 offen nt lds
v_mfma_f32_16x16x32_bf16 v[236:239],a[152:155],v[120:123],v[236:239]
ds_read_b128 a[132:135],v1 offset:256
v_mov_b32 v53,0
v_mfma_f32_16x16x32_bf16 v[236:239],a[148:151],v[116:119],v[236:239]
v_mov_b32 v54,0
v_mov_b32 v55,0
v_mfma_f32_16x16x32_bf16 v[236:239],a[144:147],v[112:115],v[236:239]
ds_read_b128 a[136:139],v1 offset:512
v_mov_b32 v56,0
v_mfma_f32_16x16x32_bf16 v[248:251],a[160:163],v[112:115],v[44:47]
v_exp_f32 v200,v200
v_mfma_f32_16x16x32_bf16 v[248:251],a[164:167],v[116:119],v[248:251]
ds_read_b128 a[140:143],v1 offset:768
v_mov_b32 v57,0
v_mfma_f32_16x16x32_bf16 v[248:251],a[168:171],v[120:123],v[248:251]
v_exp_f32 v201,v201
v_mfma_f32_16x16x32_bf16 v[248:251],a[172:175],v[124:127],v[248:251]
ds_read_b128 a[156:159],v1 offset:4864
v_cvt_pk_bf16_f32 v200,v200,v201
v_mfma_f32_16x16x32_bf16 v[252:255],a[188:191],v[124:127],v[44:47]
v_exp_f32 v202,v202
v_mfma_f32_16x16x32_bf16 v[252:255],a[184:187],v[120:123],v[252:255]
ds_read_b128 a[152:155],v1 offset:4608
v_mov_b32 v58,0
v_mfma_f32_16x16x32_bf16 v[252:255],a[180:183],v[116:119],v[252:255]
v_exp_f32 v203,v203
v_mfma_f32_16x16x32_bf16 v[252:255],a[176:179],v[112:115],v[252:255]
ds_read_b128 a[148:151],v1 offset:4352
v_cvt_pk_bf16_f32 v201,v202,v203
v_mfma_f32_16x16x32_bf16 a[32:35],a[192:195],v[168:171],0
v_exp_f32 v204,v204
v_mfma_f32_16x16x32_bf16 a[32:35],a[196:199],v[172:175],a[32:35]
ds_read_b128 a[144:147],v1 offset:4096
v_mov_b32 v59,0
v_mfma_f32_16x16x32_bf16 a[36:39],a[204:207],v[172:175],0
buffer_load_dwordx4 v4,s[20:23],s47 offen offset:1024 nt lds
v_mfma_f32_16x16x32_bf16 a[36:39],a[200:203],v[168:171],a[36:39]
ds_read_b128 a[160:163],v1 offset:8192
v_mov_b32 v60,0
v_mfma_f32_16x16x32_bf16 a[40:43],a[208:211],v[168:171],0
v_exp_f32 v205,v205
v_mfma_f32_16x16x32_bf16 a[40:43],a[212:215],v[172:175],a[40:43]
ds_read_b128 a[164:167],v1 offset:8448
v_cvt_pk_bf16_f32 v202,v204,v205
v_mfma_f32_16x16x32_bf16 a[44:47],a[220:223],v[172:175],0
v_exp_f32 v206,v206
v_mfma_f32_16x16x32_bf16 a[44:47],a[216:219],v[168:171],a[44:47]
ds_read_b128 a[168:171],v1 offset:8704
v_mov_b32 v61,0
v_mfma_f32_16x16x32_bf16 a[48:51],a[224:227],v[168:171],0
v_exp_f32 v207,v207
v_mfma_f32_16x16x32_bf16 a[48:51],a[228:231],v[172:175],a[48:51]
ds_read_b128 a[172:175],v1 offset:8960
v_cvt_pk_bf16_f32 v203,v206,v207
v_mfma_f32_16x16x32_bf16 a[52:55],a[236:239],v[172:175],0
v_exp_f32 v216,v216
v_mfma_f32_16x16x32_bf16 a[52:55],a[232:235],v[168:171],a[52:55]
ds_read_b128 a[188:191],v1 offset:13056
v_mov_b32 v62,0
v_mfma_f32_16x16x32_bf16 a[56:59],a[240:243],v[168:171],0
v_exp_f32 v217,v217
v_mfma_f32_16x16x32_bf16 a[56:59],a[244:247],v[172:175],a[56:59]
ds_read_b128 a[184:187],v1 offset:12800
v_cvt_pk_bf16_f32 v204,v216,v217
v_mfma_f32_16x16x32_bf16 a[60:63],a[252:255],v[172:175],0
v_exp_f32 v218,v218
v_mfma_f32_16x16x32_bf16 a[60:63],a[248:251],v[168:171],a[60:63]
ds_read_b128 a[180:183],v1 offset:12544
v_mov_b32 v63,0
v_smfmac_f32_16x16x64_bf16 v[52:55],v[28:31],v[168:175],v27
s_waitcnt lgkmcnt(4)
v_add_u32 v2,s48,v2
v_mfma_f32_16x16x32_bf16 v[136:139],a[128:131],v[64:67],v[32:35]
buffer_load_dwordx4 v5,s[20:23],s47 offen offset:2048 nt lds
v_mfma_f32_16x16x32_bf16 v[136:139],a[132:135],v[68:71],v[136:139]
ds_read_b128 a[176:179],v1 offset:12288
v_add_u32 v3,s48,v3
v_mfma_f32_16x16x32_bf16 v[136:139],a[136:139],v[72:75],v[136:139]
s_mov_b32 s48,32768
v_exp_f32 v219,v219
v_mfma_f32_16x16x32_bf16 v[136:139],a[140:143],v[76:79],v[136:139]
v_exp_f32 v220,v220
v_mfma_f32_16x16x32_bf16 v[140:143],a[156:159],v[76:79],v[32:35]
v_exp_f32 v221,v221
v_mfma_f32_16x16x32_bf16 v[140:143],a[152:155],v[72:75],v[140:143]
v_exp_f32 v222,v222
v_mfma_f32_16x16x32_bf16 v[140:143],a[148:151],v[68:71],v[140:143]
v_exp_f32 v223,v223
v_mfma_f32_16x16x32_bf16 v[140:143],a[144:147],v[64:67],v[140:143]
v_cvt_pk_bf16_f32 v205,v218,v219
v_cvt_pk_bf16_f32 v206,v220,v221
v_mfma_f32_16x16x32_bf16 v[152:155],a[160:163],v[64:67],v[32:35]
v_cvt_pk_bf16_f32 v207,v222,v223
v_add_u32 v1,s48,v1
v_mfma_f32_16x16x32_bf16 v[152:155],a[164:167],v[68:71],v[152:155]
v_exp_f32 v232,v232
v_mfma_f32_16x16x32_bf16 v[152:155],a[168:171],v[72:75],v[152:155]
s_waitcnt lgkmcnt(0)
v_exp_f32 v233,v233
v_mfma_f32_16x16x32_bf16 v[152:155],a[172:175],v[76:79],v[152:155]
v_exp_f32 v234,v234
v_mfma_f32_16x16x32_bf16 v[156:159],a[188:191],v[76:79],v[32:35]
v_exp_f32 v235,v235
v_mfma_f32_16x16x32_bf16 v[156:159],a[184:187],v[72:75],v[156:159]
buffer_load_dwordx4 v5,s[20:23],s47 offen offset:3072 nt lds
v_mfma_f32_16x16x32_bf16 v[156:159],a[180:183],v[68:71],v[156:159]
v_exp_f32 v236,v236
v_mfma_f32_16x16x32_bf16 v[156:159],a[176:179],v[64:67],v[156:159]
v_exp_f32 v237,v237
v_mfma_f32_16x16x32_bf16 a[64:67],a[192:195],v[200:203],0
v_exp_f32 v238,v238
v_mfma_f32_16x16x32_bf16 a[64:67],a[196:199],v[204:207],a[64:67]
)A"
R"A(v_exp_f32 v239,v239
v_mfma_f32_16x16x32_bf16 a[68:71],a[204:207],v[204:207],0
v_exp_f32 v248,v248
v_mfma_f32_16x16x32_bf16 a[68:71],a[200:203],v[200:203],a[68:71]
v_exp_f32 v249,v249
v_mfma_f32_16x16x32_bf16 a[72:75],a[208:211],v[200:203],0
v_exp_f32 v250,v250
v_mfma_f32_16x16x32_bf16 a[72:75],a[212:215],v[204:207],a[72:75]
v_exp_f32 v251,v251
v_mfma_f32_16x16x32_bf16 a[76:79],a[220:223],v[204:207],0
v_exp_f32 v252,v252
v_mfma_f32_16x16x32_bf16 a[76:79],a[216:219],v[200:203],a[76:79]
v_exp_f32 v253,v253
v_mfma_f32_16x16x32_bf16 a[80:83],a[224:227],v[200:203],0
s_add_u32 m0,65536,s52
v_exp_f32 v254,v254
v_mfma_f32_16x16x32_bf16 a[80:83],a[228:231],v[204:207],a[80:83]
v_exp_f32 v255,v255
v_mfma_f32_16x16x32_bf16 a[84:87],a[236:239],v[204:207],0
v_exp_f32 v136,v136
v_mfma_f32_16x16x32_bf16 a[84:87],a[232:235],v[200:203],a[84:87]
buffer_load_dwordx4 v4,s[24:27],s47 offen nt lds
v_mfma_f32_16x16x32_bf16 a[88:91],a[240:243],v[200:203],0
v_cvt_pk_bf16_f32 v232,v232,v233
v_cvt_pk_bf16_f32 v233,v234,v235
v_mfma_f32_16x16x32_bf16 a[88:91],a[244:247],v[204:207],a[88:91]
v_cvt_pk_bf16_f32 v234,v236,v237
v_cvt_pk_bf16_f32 v235,v238,v239
v_mfma_f32_16x16x32_bf16 a[92:95],a[252:255],v[204:207],0
v_cvt_pk_bf16_f32 v236,v248,v249
v_cvt_pk_bf16_f32 v237,v250,v251
v_mfma_f32_16x16x32_bf16 a[92:95],a[248:251],v[200:203],a[92:95]
v_cvt_pk_bf16_f32 v238,v252,v253
v_cvt_pk_bf16_f32 v239,v254,v255
v_smfmac_f32_16x16x64_bf16 v[56:59],v[28:31],v[200:207],v27
v_mfma_f32_16x16x32_bf16 v[168:171],a[128:131],v[80:83],v[36:39]
v_exp_f32 v137,v137
v_mfma_f32_16x16x32_bf16 v[168:171],a[132:135],v[84:87],v[168:171]
v_exp_f32 v138,v138
v_mfma_f32_16x16x32_bf16 v[168:171],a[136:139],v[88:91],v[168:171]
v_exp_f32 v139,v139
v_mfma_f32_16x16x32_bf16 v[168:171],a[140:143],v[92:95],v[168:171]
v_exp_f32 v140,v140
v_mfma_f32_16x16x32_bf16 v[172:175],a[156:159],v[92:95],v[36:39]
v_exp_f32 v141,v141
v_mfma_f32_16x16x32_bf16 v[172:175],a[152:155],v[88:91],v[172:175]
v_exp_f32 v142,v142
v_mfma_f32_16x16x32_bf16 v[172:175],a[148:151],v[84:87],v[172:175]
v_exp_f32 v143,v143
v_mfma_f32_16x16x32_bf16 v[172:175],a[144:147],v[80:83],v[172:175]
v_exp_f32 v152,v152
v_mfma_f32_16x16x32_bf16 v[184:187],a[160:163],v[80:83],v[36:39]
buffer_load_dwordx4 v5,s[24:27],s47 offen offset:1024 nt lds
v_mfma_f32_16x16x32_bf16 v[184:187],a[164:167],v[84:87],v[184:187]
v_exp_f32 v153,v153
v_mfma_f32_16x16x32_bf16 v[184:187],a[168:171],v[88:91],v[184:187]
v_exp_f32 v154,v154
v_mfma_f32_16x16x32_bf16 v[184:187],a[172:175],v[92:95],v[184:187]
v_exp_f32 v155,v155
v_mfma_f32_16x16x32_bf16 v[188:191],a[188:191],v[92:95],v[36:39]
v_exp_f32 v156,v156
v_mfma_f32_16x16x32_bf16 v[188:191],a[184:187],v[88:91],v[188:191]
v_exp_f32 v157,v157
v_mfma_f32_16x16x32_bf16 v[188:191],a[180:183],v[84:87],v[188:191]
v_exp_f32 v158,v158
v_mfma_f32_16x16x32_bf16 v[188:191],a[176:179],v[80:83],v[188:191]
v_exp_f32 v159,v159
v_mfma_f32_16x16x32_bf16 a[96:99],a[192:195],v[232:235],0
ds_read_b64_tr_b16 a[192:193],v2
ds_read_b64_tr_b16 a[194:195],v2 offset:4096
v_mfma_f32_16x16x32_bf16 a[96:99],a[196:199],v[236:239],a[96:99]
ds_read_b64_tr_b16 a[196:197],v2 offset:8192
v_cvt_pk_bf16_f32 v136,v136,v137
v_mfma_f32_16x16x32_bf16 a[100:103],a[204:207],v[236:239],0
ds_read_b64_tr_b16 a[198:199],v2 offset:12288
v_cvt_pk_bf16_f32 v137,v138,v139
v_mfma_f32_16x16x32_bf16 a[100:103],a[200:203],v[232:235],a[100:103]
ds_read_b64_tr_b16 a[204:205],v3 offset:8192
v_cvt_pk_bf16_f32 v138,v140,v141
v_mfma_f32_16x16x32_bf16 a[104:107],a[208:211],v[232:235],0
ds_read_b64_tr_b16 a[206:207],v3 offset:12288
ds_read_b64_tr_b16 a[200:201],v3
v_mfma_f32_16x16x32_bf16 a[104:107],a[212:215],v[236:239],a[104:107]
ds_read_b64_tr_b16 a[202:203],v3 offset:4096
v_cvt_pk_bf16_f32 v139,v142,v143
v_mfma_f32_16x16x32_bf16 a[108:111],a[220:223],v[236:239],0
buffer_load_dwordx4 v4,s[24:27],s47 offen offset:2048 nt lds
v_mfma_f32_16x16x32_bf16 a[108:111],a[216:219],v[232:235],a[108:111]
ds_read_b64_tr_b16 a[208:209],v2 offset:256
ds_read_b64_tr_b16 a[210:211],v2 offset:4352
v_mfma_f32_16x16x32_bf16 a[112:115],a[224:227],v[232:235],0
ds_read_b64_tr_b16 a[212:213],v2 offset:8448
v_cvt_pk_bf16_f32 v140,v152,v153
v_mfma_f32_16x16x32_bf16 a[112:115],a[228:231],v[236:239],a[112:115]
ds_read_b64_tr_b16 a[214:215],v2 offset:12544
v_cvt_pk_bf16_f32 v141,v154,v155
v_mfma_f32_16x16x32_bf16 a[116:119],a[236:239],v[236:239],0
ds_read_b64_tr_b16 a[220:221],v3 offset:8448
v_cvt_pk_bf16_f32 v142,v156,v157
v_mfma_f32_16x16x32_bf16 a[116:119],a[232:235],v[232:235],a[116:119]
ds_read_b64_tr_b16 a[222:223],v3 offset:12544
v_cvt_pk_bf16_f32 v143,v158,v159
v_mfma_f32_16x16x32_bf16 a[120:123],a[240:243],v[232:235],0
ds_read_b64_tr_b16 a[216:217],v3 offset:256
ds_read_b64_tr_b16 a[218:219],v3 offset:4352
v_mfma_f32_16x16x32_bf16 a[120:123],a[244:247],v[236:239],a[120:123]
v_exp_f32 v168,v168
v_mfma_f32_16x16x32_bf16 a[124:127],a[252:255],v[236:239],0
ds_read_b64_tr_b16 a[224:225],v2 offset:512
ds_read_b64_tr_b16 a[226:227],v2 offset:4608
v_mfma_f32_16x16x32_bf16 a[124:127],a[248:251],v[232:235],a[124:127]
v_exp_f32 v169,v169
v_smfmac_f32_16x16x64_bf16 v[60:63],v[28:31],v[232:239],v27
ds_read_b64_tr_b16 a[228:229],v2 offset:8704
v_mfma_f32_16x16x32_bf16 v[200:203],a[128:131],v[96:99],v[40:43]
ds_read_b64_tr_b16 a[230:231],v2 offset:12800
ds_read_b64_tr_b16 a[236:237],v3 offset:8704
v_mfma_f32_16x16x32_bf16 v[200:203],a[132:135],v[100:103],v[200:203]
ds_read_b64_tr_b16 a[238:239],v3 offset:12800
v_cvt_pk_bf16_f32 v168,v168,v169
v_mfma_f32_16x16x32_bf16 v[200:203],a[136:139],v[104:107],v[200:203]
ds_read_b64_tr_b16 a[232:233],v3 offset:512
v_mfma_f32_16x16x32_bf16 v[200:203],a[140:143],v[108:111],v[200:203]
buffer_load_dwordx4 v5,s[24:27],s47 offen offset:3072 nt lds
v_mfma_f32_16x16x32_bf16 v[204:207],a[156:159],v[108:111],v[40:43]
s_add_u32 s47,s47,16384
ds_read_b64_tr_b16 a[234:235],v3 offset:4608
ds_read_b64_tr_b16 a[240:241],v2 offset:768
v_mfma_f32_16x16x32_bf16 v[204:207],a[152:155],v[104:107],v[204:207]
s_and_b32 s47,s47,0x3ffff
s_or_b32 s47,s47,s46
ds_read_b64_tr_b16 a[242:243],v2 offset:4864
v_mfma_f32_16x16x32_bf16 v[204:207],a[148:151],v[100:103],v[204:207]
ds_read_b64_tr_b16 a[244:245],v2 offset:8960
v_mfma_f32_16x16x32_bf16 v[204:207],a[144:147],v[96:99],v[204:207]
ds_read_b64_tr_b16 a[246:247],v2 offset:13056
v_mfma_f32_16x16x32_bf16 v[216:219],a[160:163],v[96:99],v[40:43]
ds_read_b64_tr_b16 a[252:253],v3 offset:8960
ds_read_b64_tr_b16 a[254:255],v3 offset:13056
v_mfma_f32_16x16x32_bf16 v[216:219],a[164:167],v[100:103],v[216:219]
ds_read_b64_tr_b16 a[248:249],v3 offset:768
v_mfma_f32_16x16x32_bf16 v[216:219],a[168:171],v[104:107],v[216:219]
ds_read_b64_tr_b16 a[250:251],v3 offset:4864
v_mfma_f32_16x16x32_bf16 v[216:219],a[172:175],v[108:111],v[216:219]
v_exp_f32 v170,v170
v_mfma_f32_16x16x32_bf16 v[220:223],a[188:191],v[108:111],v[40:43]
v_exp_f32 v171,v171
v_mfma_f32_16x16x32_bf16 v[220:223],a[184:187],v[104:107],v[220:223]
v_exp_f32 v172,v172
v_mfma_f32_16x16x32_bf16 v[220:223],a[180:183],v[100:103],v[220:223]
v_exp_f32 v173,v173
v_mfma_f32_16x16x32_bf16 v[220:223],a[176:179],v[96:99],v[220:223]
s_waitcnt lgkmcnt(4)
v_exp_f32 v174,v174
v_mfma_f32_16x16x32_bf16 a[0:3],a[192:195],v[136:139],a[0:3]
v_exp_f32 v175,v175
v_mfma_f32_16x16x32_bf16 a[0:3],a[196:199],v[140:143],a[0:3]
v_cvt_pk_bf16_f32 v169,v170,v171
v_cvt_pk_bf16_f32 v170,v172,v173
v_mfma_f32_16x16x32_bf16 a[4:7],a[204:207],v[140:143],a[4:7]
v_exp_f32 v184,v184
v_mfma_f32_16x16x32_bf16 a[4:7],a[200:203],v[136:139],a[4:7]
s_memrealtime s[76:77]
v_exp_f32 v185,v185
v_mfma_f32_16x16x32_bf16 a[8:11],a[208:211],v[136:139],a[8:11]
v_exp_f32 v186,v186
v_mfma_f32_16x16x32_bf16 a[8:11],a[212:215],v[140:143],a[8:11]
v_exp_f32 v187,v187
v_mfma_f32_16x16x32_bf16 a[12:15],a[220:223],v[140:143],a[12:15]
v_exp_f32 v188,v188
v_mfma_f32_16x16x32_bf16 a[12:15],a[216:219],v[136:139],a[12:15]
v_exp_f32 v189,v189
v_mfma_f32_16x16x32_bf16 a[16:19],a[224:227],v[136:139],a[16:19]
v_exp_f32 v190,v190
v_mfma_f32_16x16x32_bf16 a[16:19],a[228:231],v[140:143],a[16:19]
v_exp_f32 v191,v191
v_mfma_f32_16x16x32_bf16 a[20:23],a[236:239],v[140:143],a[20:23]
s_waitcnt lgkmcnt(0)
v_cvt_pk_bf16_f32 v171,v174,v175
v_cvt_pk_bf16_f32 v172,v184,v185
v_mfma_f32_16x16x32_bf16 a[20:23],a[232:235],v[136:139],a[20:23]
s_sub_u32 s79,s78,s76
v_cvt_pk_bf16_f32 v173,v186,v187
v_cvt_pk_bf16_f32 v174,v188,v189
v_mfma_f32_16x16x32_bf16 a[24:27],a[240:243],v[136:139],a[24:27]
s_add_u32 s78,s76,s60
v_cvt_pk_bf16_f32 v175,v190,v191
v_mfma_f32_16x16x32_bf16 a[24:27],a[244:247],v[140:143],a[24:27]
s_cmp_gt_i32 s79,s95
s_cbranch_scc1 L_ps2_%=
L_pr2_%=:
v_mfma_f32_16x16x32_bf16 a[28:31],a[252:255],v[140:143],a[28:31]
v_mfma_f32_16x16x32_bf16 a[28:31],a[248:251],v[136:139],a[28:31]
v_smfmac_f32_16x16x64_bf16 v[48:51],v[28:31],v[136:143],v27
s_mov_b32 s53,13
s_cmp_eq_u32 s40,3
s_cbranch_scc0 L_att0_%=
s_cmp_gt_i32 s18,0
s_cbranch_scc1 L_att0v0_%=
s_cmp_gt_i32 s19,0
s_cbranch_scc1 L_att0v1_%=
s_cmp_gt_i32 s100,0
s_cbranch_scc1 L_att0v2_%=
.p2align 5
L_att0v3_%=:
s_waitcnt vmcnt(8) lgkmcnt(0)
v_exp_f32 v200,v200
s_barrier
s_add_u32 m0,s49,s51
ds_read_b128 a[128:131],v1
s_mov_b32 s90,L_endseg_%=-L_pc_%=+128
v_exp_f32 v201,v201
s_lshr_b32 s45,s45,1
ds_read_b128 a[132:135],v1 offset:256
v_cvt_pk_bf16_f32 v200,v200,v201
buffer_load_dwordx4 v4,s[20:23],s47 offen nt lds
ds_read_b128 a[136:139],v1 offset:512
v_add_u32 v2,s48,v2
v_exp_f32 v202,v202
s_add_u32 s62,s50,32768
ds_read_b128 a[140:143],v1 offset:768
v_add_u32 v3,s48,v3
s_and_b32 s62,s62,131071
v_exp_f32 v203,v203
s_add_u32 s54,s54,16384
ds_read_b128 a[156:159],v1 offset:4864
v_cvt_pk_bf16_f32 v201,v202,v203
s_sub_u32 s48,s62,s50
v_exp_f32 v204,v204
s_sub_u32 s53,s53,1
v_exp_f32 v205,v205
s_and_b32 s46,s54,0xfffc0000
ds_read_b128 a[152:155],v1 offset:4608
v_cvt_pk_bf16_f32 v202,v204,v205
s_mov_b32 s50,s62
v_exp_f32 v206,v206
v_exp_f32 v207,v207
ds_read_b128 a[148:151],v1 offset:4352
v_cvt_pk_bf16_f32 v203,v206,v207
v_mfma_f32_16x16x32_bf16 a[32:35],a[192:195],v[168:171],a[32:35]
v_exp_f32 v216,v216
v_mfma_f32_16x16x32_bf16 a[32:35],a[196:199],v[172:175],a[32:35]
ds_read_b128 a[144:147],v1 offset:4096
v_mfma_f32_16x16x32_bf16 a[36:39],a[204:207],v[172:175],a[36:39]
buffer_load_dwordx4 v4,s[20:23],s47 offen offset:1024 nt lds
v_mfma_f32_16x16x32_bf16 a[36:39],a[200:203],v[168:171],a[36:39]
ds_read_b128 a[160:163],v1 offset:8192
v_mfma_f32_16x16x32_bf16 a[40:43],a[208:211],v[168:171],a[40:43]
v_exp_f32 v217,v217
v_mfma_f32_16x16x32_bf16 a[40:43],a[212:215],v[172:175],a[40:43]
ds_read_b128 a[164:167],v1 offset:8448
v_cvt_pk_bf16_f32 v204,v216,v217
v_mfma_f32_16x16x32_bf16 a[44:47],a[220:223],v[172:175],a[44:47]
v_exp_f32 v218,v218
v_mfma_f32_16x16x32_bf16 a[44:47],a[216:219],v[168:171],a[44:47]
ds_read_b128 a[168:171],v1 offset:8704
v_mfma_f32_16x16x32_bf16 a[48:51],a[224:227],v[168:171],a[48:51]
v_exp_f32 v219,v219
v_mfma_f32_16x16x32_bf16 a[48:51],a[228:231],v[172:175],a[48:51]
ds_read_b128 a[172:175],v1 offset:8960
v_cvt_pk_bf16_f32 v205,v218,v219
v_mfma_f32_16x16x32_bf16 a[52:55],a[236:239],v[172:175],a[52:55]
v_exp_f32 v220,v220
v_mfma_f32_16x16x32_bf16 a[52:55],a[232:235],v[168:171],a[52:55]
ds_read_b128 a[188:191],v1 offset:13056
v_mfma_f32_16x16x32_bf16 a[56:59],a[240:243],v[168:171],a[56:59]
v_exp_f32 v221,v221
v_mfma_f32_16x16x32_bf16 a[56:59],a[244:247],v[172:175],a[56:59]
ds_read_b128 a[184:187],v1 offset:12800
v_cvt_pk_bf16_f32 v206,v220,v221
v_mfma_f32_16x16x32_bf16 a[60:63],a[252:255],v[172:175],a[60:63]
v_exp_f32 v222,v222
v_mfma_f32_16x16x32_bf16 a[60:63],a[248:251],v[168:171],a[60:63]
ds_read_b128 a[180:183],v1 offset:12544
v_smfmac_f32_16x16x64_bf16 v[52:55],v[28:31],v[168:175],v27
s_waitcnt lgkmcnt(4)
v_mfma_f32_16x16x32_bf16 v[136:139],a[128:131],v[64:67],v[32:35]
buffer_load_dwordx4 v5,s[20:23],s47 offen offset:2048 nt lds
v_mfma_f32_16x16x32_bf16 v[136:139],a[132:135],v[68:71],v[136:139]
ds_read_b128 a[176:179],v1 offset:12288
v_add_u32 v1,s48,v1
v_mfma_f32_16x16x32_bf16 v[136:139],a[136:139],v[72:75],v[136:139]
v_exp_f32 v223,v223
v_mfma_f32_16x16x32_bf16 v[136:139],a[140:143],v[76:79],v[136:139]
)A"
R"A(v_cvt_pk_bf16_f32 v207,v222,v223
v_mfma_f32_16x16x32_bf16 v[140:143],a[156:159],v[76:79],v[32:35]
v_mfma_f32_16x16x32_bf16 v[140:143],a[152:155],v[72:75],v[140:143]
v_mfma_f32_16x16x32_bf16 v[140:143],a[148:151],v[68:71],v[140:143]
v_mfma_f32_16x16x32_bf16 v[140:143],a[144:147],v[64:67],v[140:143]
v_mfma_f32_16x16x32_bf16 v[152:155],a[160:163],v[64:67],v[32:35]
v_mfma_f32_16x16x32_bf16 v[152:155],a[164:167],v[68:71],v[152:155]
v_mfma_f32_16x16x32_bf16 v[152:155],a[168:171],v[72:75],v[152:155]
s_waitcnt lgkmcnt(0)
v_mfma_f32_16x16x32_bf16 v[152:155],a[172:175],v[76:79],v[152:155]
v_mfma_f32_16x16x32_bf16 v[156:159],a[188:191],v[76:79],v[32:35]
v_mfma_f32_16x16x32_bf16 v[156:159],a[184:187],v[72:75],v[156:159]
buffer_load_dwordx4 v5,s[20:23],s47 offen offset:3072 nt lds
v_mfma_f32_16x16x32_bf16 v[156:159],a[180:183],v[68:71],v[156:159]
v_mfma_f32_16x16x32_bf16 v[156:159],a[176:179],v[64:67],v[156:159]
v_mfma_f32_16x16x32_bf16 a[64:67],a[192:195],v[200:203],a[64:67]
v_mfma_f32_16x16x32_bf16 a[64:67],a[196:199],v[204:207],a[64:67]
v_mfma_f32_16x16x32_bf16 a[68:71],a[204:207],v[204:207],a[68:71]
v_mfma_f32_16x16x32_bf16 a[68:71],a[200:203],v[200:203],a[68:71]
v_mfma_f32_16x16x32_bf16 a[72:75],a[208:211],v[200:203],a[72:75]
v_mfma_f32_16x16x32_bf16 a[72:75],a[212:215],v[204:207],a[72:75]
v_exp_f32 v136,v136
v_mfma_f32_16x16x32_bf16 a[76:79],a[220:223],v[204:207],a[76:79]
v_exp_f32 v137,v137
v_mfma_f32_16x16x32_bf16 a[76:79],a[216:219],v[200:203],a[76:79]
v_exp_f32 v138,v138
v_mfma_f32_16x16x32_bf16 a[80:83],a[224:227],v[200:203],a[80:83]
s_add_u32 m0,s49,s52
v_mfma_f32_16x16x32_bf16 a[80:83],a[228:231],v[204:207],a[80:83]
s_xor_b32 s49,s50,65536
v_mfma_f32_16x16x32_bf16 a[84:87],a[236:239],v[204:207],a[84:87]
v_mfma_f32_16x16x32_bf16 a[84:87],a[232:235],v[200:203],a[84:87]
buffer_load_dwordx4 v4,s[24:27],s47 offen nt lds
v_mfma_f32_16x16x32_bf16 a[88:91],a[240:243],v[200:203],a[88:91]
v_mfma_f32_16x16x32_bf16 a[88:91],a[244:247],v[204:207],a[88:91]
v_exp_f32 v139,v139
v_mfma_f32_16x16x32_bf16 a[92:95],a[252:255],v[204:207],a[92:95]
v_exp_f32 v140,v140
v_mfma_f32_16x16x32_bf16 a[92:95],a[248:251],v[200:203],a[92:95]
v_exp_f32 v141,v141
v_smfmac_f32_16x16x64_bf16 v[56:59],v[28:31],v[200:207],v27
v_mfma_f32_16x16x32_bf16 v[168:171],a[128:131],v[80:83],v[36:39]
v_exp_f32 v142,v142
v_mfma_f32_16x16x32_bf16 v[168:171],a[132:135],v[84:87],v[168:171]
v_exp_f32 v143,v143
v_mfma_f32_16x16x32_bf16 v[168:171],a[136:139],v[88:91],v[168:171]
v_exp_f32 v152,v152
v_mfma_f32_16x16x32_bf16 v[168:171],a[140:143],v[92:95],v[168:171]
v_exp_f32 v153,v153
v_mfma_f32_16x16x32_bf16 v[172:175],a[156:159],v[92:95],v[36:39]
v_exp_f32 v154,v154
v_mfma_f32_16x16x32_bf16 v[172:175],a[152:155],v[88:91],v[172:175]
v_exp_f32 v155,v155
v_mfma_f32_16x16x32_bf16 v[172:175],a[148:151],v[84:87],v[172:175]
v_exp_f32 v156,v156
v_mfma_f32_16x16x32_bf16 v[172:175],a[144:147],v[80:83],v[172:175]
v_exp_f32 v157,v157
v_mfma_f32_16x16x32_bf16 v[184:187],a[160:163],v[80:83],v[36:39]
buffer_load_dwordx4 v5,s[24:27],s47 offen offset:1024 nt lds
v_mfma_f32_16x16x32_bf16 v[184:187],a[164:167],v[84:87],v[184:187]
v_exp_f32 v158,v158
v_mfma_f32_16x16x32_bf16 v[184:187],a[168:171],v[88:91],v[184:187]
v_exp_f32 v159,v159
v_mfma_f32_16x16x32_bf16 v[184:187],a[172:175],v[92:95],v[184:187]
v_exp_f32 v168,v168
v_mfma_f32_16x16x32_bf16 v[188:191],a[188:191],v[92:95],v[36:39]
v_exp_f32 v169,v169
v_mfma_f32_16x16x32_bf16 v[188:191],a[184:187],v[88:91],v[188:191]
v_exp_f32 v170,v170
v_mfma_f32_16x16x32_bf16 v[188:191],a[180:183],v[84:87],v[188:191]
v_exp_f32 v171,v171
v_mfma_f32_16x16x32_bf16 v[188:191],a[176:179],v[80:83],v[188:191]
v_exp_f32 v172,v172
ds_read_b64_tr_b16 a[192:193],v2
v_cvt_pk_bf16_f32 v168,v168,v169
ds_read_b64_tr_b16 a[194:195],v2 offset:4096
v_cvt_pk_bf16_f32 v169,v170,v171
v_exp_f32 v173,v173
ds_read_b64_tr_b16 a[196:197],v2 offset:8192
v_cvt_pk_bf16_f32 v170,v172,v173
v_exp_f32 v174,v174
v_exp_f32 v175,v175
buffer_load_dwordx4 v4,s[24:27],s47 offen offset:2048 nt lds
ds_read_b64_tr_b16 a[198:199],v2 offset:12288
v_cvt_pk_bf16_f32 v171,v174,v175
v_exp_f32 v184,v184
v_exp_f32 v185,v185
ds_read_b64_tr_b16 a[204:205],v3 offset:8192
v_cvt_pk_bf16_f32 v136,v136,v137
ds_read_b64_tr_b16 a[206:207],v3 offset:12288
v_cvt_pk_bf16_f32 v172,v184,v185
ds_read_b64_tr_b16 a[200:201],v3
ds_read_b64_tr_b16 a[202:203],v3 offset:4096
v_cvt_pk_bf16_f32 v137,v138,v139
ds_read_b64_tr_b16 a[208:209],v2 offset:256
ds_read_b64_tr_b16 a[210:211],v2 offset:4352
ds_read_b64_tr_b16 a[212:213],v2 offset:8448
v_mfma_f32_16x16x32_bf16 v[200:203],a[128:131],v[96:99],v[40:43]
ds_read_b64_tr_b16 a[214:215],v2 offset:12544
v_cvt_pk_bf16_f32 v138,v140,v141
v_mfma_f32_16x16x32_bf16 v[200:203],a[132:135],v[100:103],v[200:203]
ds_read_b64_tr_b16 a[220:221],v3 offset:8448
v_mfma_f32_16x16x32_bf16 v[200:203],a[136:139],v[104:107],v[200:203]
ds_read_b64_tr_b16 a[222:223],v3 offset:12544
v_cvt_pk_bf16_f32 v139,v142,v143
v_mfma_f32_16x16x32_bf16 v[200:203],a[140:143],v[108:111],v[200:203]
buffer_load_dwordx4 v5,s[24:27],s47 offen offset:3072 nt lds
v_mfma_f32_16x16x32_bf16 v[204:207],a[156:159],v[108:111],v[40:43]
s_add_u32 s47,s47,16384
s_and_b32 s47,s47,0x3ffff
ds_read_b64_tr_b16 a[216:217],v3 offset:256
v_mfma_f32_16x16x32_bf16 v[204:207],a[152:155],v[104:107],v[204:207]
s_or_b32 s47,s47,s46
ds_read_b64_tr_b16 a[218:219],v3 offset:4352
v_cvt_pk_bf16_f32 v140,v152,v153
v_mfma_f32_16x16x32_bf16 v[204:207],a[148:151],v[100:103],v[204:207]
ds_read_b64_tr_b16 a[224:225],v2 offset:512
v_mfma_f32_16x16x32_bf16 v[204:207],a[144:147],v[96:99],v[204:207]
ds_read_b64_tr_b16 a[226:227],v2 offset:4608
v_mfma_f32_16x16x32_bf16 v[216:219],a[160:163],v[96:99],v[40:43]
ds_read_b64_tr_b16 a[228:229],v2 offset:8704
v_cvt_pk_bf16_f32 v141,v154,v155
v_mfma_f32_16x16x32_bf16 v[216:219],a[164:167],v[100:103],v[216:219]
ds_read_b64_tr_b16 a[230:231],v2 offset:12800
v_mfma_f32_16x16x32_bf16 v[216:219],a[168:171],v[104:107],v[216:219]
ds_read_b64_tr_b16 a[236:237],v3 offset:8704
v_mfma_f32_16x16x32_bf16 v[216:219],a[172:175],v[108:111],v[216:219]
ds_read_b64_tr_b16 a[238:239],v3 offset:12800
v_cvt_pk_bf16_f32 v142,v156,v157
v_mfma_f32_16x16x32_bf16 v[220:223],a[188:191],v[108:111],v[40:43]
ds_read_b64_tr_b16 a[232:233],v3 offset:512
v_mfma_f32_16x16x32_bf16 v[220:223],a[184:187],v[104:107],v[220:223]
ds_read_b64_tr_b16 a[234:235],v3 offset:4608
v_mfma_f32_16x16x32_bf16 v[220:223],a[180:183],v[100:103],v[220:223]
ds_read_b64_tr_b16 a[240:241],v2 offset:768
v_cvt_pk_bf16_f32 v143,v158,v159
v_mfma_f32_16x16x32_bf16 v[220:223],a[176:179],v[96:99],v[220:223]
s_waitcnt lgkmcnt(7)
buffer_load_dword v26,v24,s[88:91],s93 offen
v_mfma_f32_16x16x32_bf16 a[0:3],a[192:195],v[136:139],a[0:3]
ds_read_b64_tr_b16 a[242:243],v2 offset:4864
v_mfma_f32_16x16x32_bf16 a[0:3],a[196:199],v[140:143],a[0:3]
ds_read_b64_tr_b16 a[244:245],v2 offset:8960
v_mfma_f32_16x16x32_bf16 a[4:7],a[204:207],v[140:143],a[4:7]
ds_read_b64_tr_b16 a[246:247],v2 offset:13056
v_mfma_f32_16x16x32_bf16 a[4:7],a[200:203],v[136:139],a[4:7]
ds_read_b64_tr_b16 a[252:253],v3 offset:8960
v_mfma_f32_16x16x32_bf16 a[8:11],a[208:211],v[136:139],a[8:11]
ds_read_b64_tr_b16 a[254:255],v3 offset:13056
v_mfma_f32_16x16x32_bf16 a[8:11],a[212:215],v[140:143],a[8:11]
ds_read_b64_tr_b16 a[248:249],v3 offset:768
v_mfma_f32_16x16x32_bf16 a[12:15],a[220:223],v[140:143],a[12:15]
ds_read_b64_tr_b16 a[250:251],v3 offset:4864
v_mfma_f32_16x16x32_bf16 a[12:15],a[216:219],v[136:139],a[12:15]
s_bitcmp1_b32 s45,0
s_cbranch_scc0 L_t31v30_%=
s_mov_b32 m0,s80
s_add_u32 s80,s80,4096
buffer_load_dwordx4 v25,s[20:23],s83 offen nt lds
s_add_u32 s83,s83,4096
L_t31v30_%=:
v_mfma_f32_16x16x32_bf16 a[16:19],a[224:227],v[136:139],a[16:19]
s_waitcnt lgkmcnt(6)
v_exp_f32 v186,v186
v_mfma_f32_16x16x32_bf16 a[16:19],a[228:231],v[140:143],a[16:19]
v_exp_f32 v187,v187
v_mfma_f32_16x16x32_bf16 a[20:23],a[236:239],v[140:143],a[20:23]
v_exp_f32 v188,v188
v_mfma_f32_16x16x32_bf16 a[20:23],a[232:235],v[136:139],a[20:23]
v_exp_f32 v189,v189
v_mfma_f32_16x16x32_bf16 a[24:27],a[240:243],v[136:139],a[24:27]
s_waitcnt lgkmcnt(2)
v_exp_f32 v190,v190
v_mfma_f32_16x16x32_bf16 a[24:27],a[244:247],v[140:143],a[24:27]
v_cvt_pk_bf16_f32 v173,v186,v187
v_mfma_f32_16x16x32_bf16 a[28:31],a[252:255],v[140:143],a[28:31]
s_waitcnt lgkmcnt(0)
v_exp_f32 v191,v191
v_mfma_f32_16x16x32_bf16 a[28:31],a[248:251],v[136:139],a[28:31]
v_cvt_pk_bf16_f32 v174,v188,v189
v_cvt_pk_bf16_f32 v175,v190,v191
s_cmp_lg_u32 s53,0
v_smfmac_f32_16x16x64_bf16 v[48:51],v[28:31],v[136:143],v27
s_cbranch_scc1 L_att0v3_%=
s_waitcnt lgkmcnt(0)
s_nop 7
s_branch L_att0x_%=
.p2align 5
L_att0v0_%=:
v_mfma_f32_16x16x32_bf16 v[232:235],a[128:131],v[112:115],v[44:47]
s_waitcnt vmcnt(8) lgkmcnt(0)
v_exp_f32 v200,v200
v_mfma_f32_16x16x32_bf16 v[232:235],a[132:135],v[116:119],v[232:235]
s_barrier
s_add_u32 m0,s49,s51
ds_read_b128 a[128:131],v1
v_mfma_f32_16x16x32_bf16 v[232:235],a[136:139],v[120:123],v[232:235]
s_mov_b32 s90,L_endseg_%=-L_pc_%=+128
v_exp_f32 v201,v201
v_mfma_f32_16x16x32_bf16 v[232:235],a[140:143],v[124:127],v[232:235]
s_lshr_b32 s45,s45,1
ds_read_b128 a[132:135],v1 offset:256
v_cvt_pk_bf16_f32 v200,v200,v201
v_mfma_f32_16x16x32_bf16 v[236:239],a[156:159],v[124:127],v[44:47]
buffer_load_dwordx4 v4,s[20:23],s47 offen nt lds
v_mfma_f32_16x16x32_bf16 v[236:239],a[152:155],v[120:123],v[236:239]
ds_read_b128 a[136:139],v1 offset:512
v_add_u32 v2,s48,v2
v_mfma_f32_16x16x32_bf16 v[236:239],a[148:151],v[116:119],v[236:239]
v_exp_f32 v202,v202
v_mfma_f32_16x16x32_bf16 v[236:239],a[144:147],v[112:115],v[236:239]
s_add_u32 s62,s50,32768
ds_read_b128 a[140:143],v1 offset:768
v_add_u32 v3,s48,v3
v_mfma_f32_16x16x32_bf16 v[248:251],a[160:163],v[112:115],v[44:47]
s_and_b32 s62,s62,131071
v_exp_f32 v203,v203
v_mfma_f32_16x16x32_bf16 v[248:251],a[164:167],v[116:119],v[248:251]
s_add_u32 s54,s54,16384
ds_read_b128 a[156:159],v1 offset:4864
v_cvt_pk_bf16_f32 v201,v202,v203
v_mfma_f32_16x16x32_bf16 v[248:251],a[168:171],v[120:123],v[248:251]
s_sub_u32 s48,s62,s50
v_exp_f32 v204,v204
v_mfma_f32_16x16x32_bf16 v[248:251],a[172:175],v[124:127],v[248:251]
s_sub_u32 s53,s53,1
v_exp_f32 v205,v205
v_mfma_f32_16x16x32_bf16 v[252:255],a[188:191],v[124:127],v[44:47]
s_and_b32 s46,s54,0xfffc0000
ds_read_b128 a[152:155],v1 offset:4608
v_cvt_pk_bf16_f32 v202,v204,v205
v_mfma_f32_16x16x32_bf16 v[252:255],a[184:187],v[120:123],v[252:255]
s_mov_b32 s50,s62
v_exp_f32 v206,v206
v_mfma_f32_16x16x32_bf16 v[252:255],a[180:183],v[116:119],v[252:255]
v_exp_f32 v207,v207
v_mfma_f32_16x16x32_bf16 v[252:255],a[176:179],v[112:115],v[252:255]
ds_read_b128 a[148:151],v1 offset:4352
v_cvt_pk_bf16_f32 v203,v206,v207
v_mfma_f32_16x16x32_bf16 a[32:35],a[192:195],v[168:171],a[32:35]
v_exp_f32 v216,v216
v_mfma_f32_16x16x32_bf16 a[32:35],a[196:199],v[172:175],a[32:35]
ds_read_b128 a[144:147],v1 offset:4096
v_mfma_f32_16x16x32_bf16 a[36:39],a[204:207],v[172:175],a[36:39]
buffer_load_dwordx4 v4,s[20:23],s47 offen offset:1024 nt lds
v_mfma_f32_16x16x32_bf16 a[36:39],a[200:203],v[168:171],a[36:39]
ds_read_b128 a[160:163],v1 offset:8192
v_mfma_f32_16x16x32_bf16 a[40:43],a[208:211],v[168:171],a[40:43]
v_exp_f32 v217,v217
v_mfma_f32_16x16x32_bf16 a[40:43],a[212:215],v[172:175],a[40:43]
ds_read_b128 a[164:167],v1 offset:8448
v_cvt_pk_bf16_f32 v204,v216,v217
v_mfma_f32_16x16x32_bf16 a[44:47],a[220:223],v[172:175],a[44:47]
v_exp_f32 v218,v218
v_mfma_f32_16x16x32_bf16 a[44:47],a[216:219],v[168:171],a[44:47]
ds_read_b128 a[168:171],v1 offset:8704
v_mfma_f32_16x16x32_bf16 a[48:51],a[224:227],v[168:171],a[48:51]
v_exp_f32 v219,v219
v_mfma_f32_16x16x32_bf16 a[48:51],a[228:231],v[172:175],a[48:51]
ds_read_b128 a[172:175],v1 offset:8960
v_cvt_pk_bf16_f32 v205,v218,v219
v_mfma_f32_16x16x32_bf16 a[52:55],a[236:239],v[172:175],a[52:55]
v_exp_f32 v220,v220
v_mfma_f32_16x16x32_bf16 a[52:55],a[232:235],v[168:171],a[52:55]
ds_read_b128 a[188:191],v1 offset:13056
v_mfma_f32_16x16x32_bf16 a[56:59],a[240:243],v[168:171],a[56:59]
v_exp_f32 v221,v221
v_mfma_f32_16x16x32_bf16 a[56:59],a[244:247],v[172:175],a[56:59]
)A"
R"A(ds_read_b128 a[184:187],v1 offset:12800
v_cvt_pk_bf16_f32 v206,v220,v221
v_mfma_f32_16x16x32_bf16 a[60:63],a[252:255],v[172:175],a[60:63]
v_exp_f32 v222,v222
v_mfma_f32_16x16x32_bf16 a[60:63],a[248:251],v[168:171],a[60:63]
ds_read_b128 a[180:183],v1 offset:12544
v_smfmac_f32_16x16x64_bf16 v[52:55],v[28:31],v[168:175],v27
buffer_load_dwordx4 v5,s[20:23],s47 offen offset:2048 nt lds
ds_read_b128 a[176:179],v1 offset:12288
v_add_u32 v1,s48,v1
v_exp_f32 v223,v223
s_nop 0
v_cvt_pk_bf16_f32 v207,v222,v223
v_exp_f32 v232,v232
v_exp_f32 v233,v233
v_exp_f32 v234,v234
v_exp_f32 v235,v235
v_exp_f32 v236,v236
v_exp_f32 v237,v237
v_exp_f32 v238,v238
v_exp_f32 v239,v239
v_exp_f32 v248,v248
buffer_load_dwordx4 v5,s[20:23],s47 offen offset:3072 nt lds
v_exp_f32 v249,v249
v_exp_f32 v250,v250
v_mfma_f32_16x16x32_bf16 a[64:67],a[192:195],v[200:203],a[64:67]
v_exp_f32 v251,v251
v_mfma_f32_16x16x32_bf16 a[64:67],a[196:199],v[204:207],a[64:67]
v_exp_f32 v252,v252
v_mfma_f32_16x16x32_bf16 a[68:71],a[204:207],v[204:207],a[68:71]
v_exp_f32 v253,v253
v_mfma_f32_16x16x32_bf16 a[68:71],a[200:203],v[200:203],a[68:71]
v_exp_f32 v254,v254
v_mfma_f32_16x16x32_bf16 a[72:75],a[208:211],v[200:203],a[72:75]
v_exp_f32 v255,v255
v_mfma_f32_16x16x32_bf16 a[72:75],a[212:215],v[204:207],a[72:75]
v_mfma_f32_16x16x32_bf16 a[76:79],a[220:223],v[204:207],a[76:79]
v_mfma_f32_16x16x32_bf16 a[76:79],a[216:219],v[200:203],a[76:79]
v_mfma_f32_16x16x32_bf16 a[80:83],a[224:227],v[200:203],a[80:83]
s_add_u32 m0,s49,s52
v_cvt_pk_bf16_f32 v232,v232,v233
v_cvt_pk_bf16_f32 v233,v234,v235
v_mfma_f32_16x16x32_bf16 a[80:83],a[228:231],v[204:207],a[80:83]
s_xor_b32 s49,s50,65536
v_cvt_pk_bf16_f32 v234,v236,v237
v_cvt_pk_bf16_f32 v235,v238,v239
v_mfma_f32_16x16x32_bf16 a[84:87],a[236:239],v[204:207],a[84:87]
v_cvt_pk_bf16_f32 v236,v248,v249
v_cvt_pk_bf16_f32 v237,v250,v251
v_mfma_f32_16x16x32_bf16 a[84:87],a[232:235],v[200:203],a[84:87]
buffer_load_dwordx4 v4,s[24:27],s47 offen nt lds
v_mfma_f32_16x16x32_bf16 a[88:91],a[240:243],v[200:203],a[88:91]
v_cvt_pk_bf16_f32 v238,v252,v253
v_cvt_pk_bf16_f32 v239,v254,v255
v_mfma_f32_16x16x32_bf16 a[88:91],a[244:247],v[204:207],a[88:91]
v_mfma_f32_16x16x32_bf16 a[92:95],a[252:255],v[204:207],a[92:95]
v_mfma_f32_16x16x32_bf16 a[92:95],a[248:251],v[200:203],a[92:95]
v_smfmac_f32_16x16x64_bf16 v[56:59],v[28:31],v[200:207],v27
s_waitcnt lgkmcnt(0)
v_mfma_f32_16x16x32_bf16 v[168:171],a[128:131],v[80:83],v[36:39]
v_mfma_f32_16x16x32_bf16 v[168:171],a[132:135],v[84:87],v[168:171]
v_mfma_f32_16x16x32_bf16 v[168:171],a[136:139],v[88:91],v[168:171]
v_mfma_f32_16x16x32_bf16 v[168:171],a[140:143],v[92:95],v[168:171]
v_mfma_f32_16x16x32_bf16 v[172:175],a[156:159],v[92:95],v[36:39]
v_mfma_f32_16x16x32_bf16 v[172:175],a[152:155],v[88:91],v[172:175]
v_mfma_f32_16x16x32_bf16 v[172:175],a[148:151],v[84:87],v[172:175]
v_mfma_f32_16x16x32_bf16 v[172:175],a[144:147],v[80:83],v[172:175]
v_mfma_f32_16x16x32_bf16 v[184:187],a[160:163],v[80:83],v[36:39]
buffer_load_dwordx4 v5,s[24:27],s47 offen offset:1024 nt lds
v_mfma_f32_16x16x32_bf16 v[184:187],a[164:167],v[84:87],v[184:187]
v_mfma_f32_16x16x32_bf16 v[184:187],a[168:171],v[88:91],v[184:187]
v_mfma_f32_16x16x32_bf16 v[184:187],a[172:175],v[92:95],v[184:187]
v_exp_f32 v168,v168
v_mfma_f32_16x16x32_bf16 v[188:191],a[188:191],v[92:95],v[36:39]
v_exp_f32 v169,v169
v_mfma_f32_16x16x32_bf16 v[188:191],a[184:187],v[88:91],v[188:191]
v_exp_f32 v170,v170
v_mfma_f32_16x16x32_bf16 v[188:191],a[180:183],v[84:87],v[188:191]
v_exp_f32 v171,v171
v_mfma_f32_16x16x32_bf16 v[188:191],a[176:179],v[80:83],v[188:191]
v_exp_f32 v172,v172
v_mfma_f32_16x16x32_bf16 a[96:99],a[192:195],v[232:235],a[96:99]
ds_read_b64_tr_b16 a[192:193],v2
v_cvt_pk_bf16_f32 v168,v168,v169
v_mfma_f32_16x16x32_bf16 a[96:99],a[196:199],v[236:239],a[96:99]
ds_read_b64_tr_b16 a[194:195],v2 offset:4096
v_cvt_pk_bf16_f32 v169,v170,v171
v_mfma_f32_16x16x32_bf16 a[100:103],a[204:207],v[236:239],a[100:103]
v_exp_f32 v173,v173
v_mfma_f32_16x16x32_bf16 a[100:103],a[200:203],v[232:235],a[100:103]
ds_read_b64_tr_b16 a[196:197],v2 offset:8192
v_cvt_pk_bf16_f32 v170,v172,v173
v_mfma_f32_16x16x32_bf16 a[104:107],a[208:211],v[232:235],a[104:107]
v_exp_f32 v174,v174
v_mfma_f32_16x16x32_bf16 a[104:107],a[212:215],v[236:239],a[104:107]
v_exp_f32 v175,v175
v_mfma_f32_16x16x32_bf16 a[108:111],a[220:223],v[236:239],a[108:111]
buffer_load_dwordx4 v4,s[24:27],s47 offen offset:2048 nt lds
v_mfma_f32_16x16x32_bf16 a[108:111],a[216:219],v[232:235],a[108:111]
ds_read_b64_tr_b16 a[198:199],v2 offset:12288
v_cvt_pk_bf16_f32 v171,v174,v175
v_mfma_f32_16x16x32_bf16 a[112:115],a[224:227],v[232:235],a[112:115]
v_exp_f32 v184,v184
v_mfma_f32_16x16x32_bf16 a[112:115],a[228:231],v[236:239],a[112:115]
v_exp_f32 v185,v185
v_mfma_f32_16x16x32_bf16 a[116:119],a[236:239],v[236:239],a[116:119]
ds_read_b64_tr_b16 a[204:205],v3 offset:8192
v_mfma_f32_16x16x32_bf16 a[116:119],a[232:235],v[232:235],a[116:119]
ds_read_b64_tr_b16 a[206:207],v3 offset:12288
v_cvt_pk_bf16_f32 v172,v184,v185
v_mfma_f32_16x16x32_bf16 a[120:123],a[240:243],v[232:235],a[120:123]
ds_read_b64_tr_b16 a[200:201],v3
v_mfma_f32_16x16x32_bf16 a[120:123],a[244:247],v[236:239],a[120:123]
ds_read_b64_tr_b16 a[202:203],v3 offset:4096
v_mfma_f32_16x16x32_bf16 a[124:127],a[252:255],v[236:239],a[124:127]
ds_read_b64_tr_b16 a[208:209],v2 offset:256
v_mfma_f32_16x16x32_bf16 a[124:127],a[248:251],v[232:235],a[124:127]
ds_read_b64_tr_b16 a[210:211],v2 offset:4352
v_smfmac_f32_16x16x64_bf16 v[60:63],v[28:31],v[232:239],v27
ds_read_b64_tr_b16 a[212:213],v2 offset:8448
v_mfma_f32_16x16x32_bf16 v[200:203],a[128:131],v[96:99],v[40:43]
ds_read_b64_tr_b16 a[214:215],v2 offset:12544
v_mfma_f32_16x16x32_bf16 v[200:203],a[132:135],v[100:103],v[200:203]
ds_read_b64_tr_b16 a[220:221],v3 offset:8448
v_mfma_f32_16x16x32_bf16 v[200:203],a[136:139],v[104:107],v[200:203]
ds_read_b64_tr_b16 a[222:223],v3 offset:12544
v_mfma_f32_16x16x32_bf16 v[200:203],a[140:143],v[108:111],v[200:203]
buffer_load_dwordx4 v5,s[24:27],s47 offen offset:3072 nt lds
v_mfma_f32_16x16x32_bf16 v[204:207],a[156:159],v[108:111],v[40:43]
s_add_u32 s47,s47,16384
s_and_b32 s47,s47,0x3ffff
ds_read_b64_tr_b16 a[216:217],v3 offset:256
v_mfma_f32_16x16x32_bf16 v[204:207],a[152:155],v[104:107],v[204:207]
s_or_b32 s47,s47,s46
ds_read_b64_tr_b16 a[218:219],v3 offset:4352
v_mfma_f32_16x16x32_bf16 v[204:207],a[148:151],v[100:103],v[204:207]
ds_read_b64_tr_b16 a[224:225],v2 offset:512
v_mfma_f32_16x16x32_bf16 v[204:207],a[144:147],v[96:99],v[204:207]
ds_read_b64_tr_b16 a[226:227],v2 offset:4608
v_mfma_f32_16x16x32_bf16 v[216:219],a[160:163],v[96:99],v[40:43]
ds_read_b64_tr_b16 a[228:229],v2 offset:8704
v_mfma_f32_16x16x32_bf16 v[216:219],a[164:167],v[100:103],v[216:219]
ds_read_b64_tr_b16 a[230:231],v2 offset:12800
v_mfma_f32_16x16x32_bf16 v[216:219],a[168:171],v[104:107],v[216:219]
ds_read_b64_tr_b16 a[236:237],v3 offset:8704
v_mfma_f32_16x16x32_bf16 v[216:219],a[172:175],v[108:111],v[216:219]
ds_read_b64_tr_b16 a[238:239],v3 offset:12800
v_mfma_f32_16x16x32_bf16 v[220:223],a[188:191],v[108:111],v[40:43]
ds_read_b64_tr_b16 a[232:233],v3 offset:512
v_mfma_f32_16x16x32_bf16 v[220:223],a[184:187],v[104:107],v[220:223]
ds_read_b64_tr_b16 a[234:235],v3 offset:4608
v_mfma_f32_16x16x32_bf16 v[220:223],a[180:183],v[100:103],v[220:223]
ds_read_b64_tr_b16 a[240:241],v2 offset:768
v_mfma_f32_16x16x32_bf16 v[220:223],a[176:179],v[96:99],v[220:223]
buffer_load_dword v26,v24,s[88:91],s93 offen
ds_read_b64_tr_b16 a[242:243],v2 offset:4864
ds_read_b64_tr_b16 a[244:245],v2 offset:8960
ds_read_b64_tr_b16 a[246:247],v2 offset:13056
ds_read_b64_tr_b16 a[252:253],v3 offset:8960
ds_read_b64_tr_b16 a[254:255],v3 offset:13056
ds_read_b64_tr_b16 a[248:249],v3 offset:768
ds_read_b64_tr_b16 a[250:251],v3 offset:4864
s_bitcmp1_b32 s45,0
s_cbranch_scc0 L_t31v00_%=
s_mov_b32 m0,s80
s_add_u32 s80,s80,4096
buffer_load_dwordx4 v25,s[20:23],s83 offen nt lds
s_add_u32 s83,s83,4096
L_t31v00_%=:
v_exp_f32 v186,v186
v_exp_f32 v187,v187
v_exp_f32 v188,v188
v_exp_f32 v189,v189
v_exp_f32 v190,v190
v_cvt_pk_bf16_f32 v173,v186,v187
v_exp_f32 v191,v191
v_cvt_pk_bf16_f32 v174,v188,v189
v_cvt_pk_bf16_f32 v175,v190,v191
s_cmp_lg_u32 s53,0
s_cbranch_scc1 L_att0v0_%=
s_waitcnt lgkmcnt(0)
s_nop 7
s_branch L_att0x_%=
.p2align 5
L_att0v1_%=:
v_mfma_f32_16x16x32_bf16 v[232:235],a[128:131],v[112:115],v[44:47]
s_waitcnt vmcnt(8) lgkmcnt(0)
v_exp_f32 v200,v200
v_mfma_f32_16x16x32_bf16 v[232:235],a[132:135],v[116:119],v[232:235]
s_barrier
s_add_u32 m0,s49,s51
ds_read_b128 a[128:131],v1
v_mfma_f32_16x16x32_bf16 v[232:235],a[136:139],v[120:123],v[232:235]
s_mov_b32 s90,L_endseg_%=-L_pc_%=+128
v_exp_f32 v201,v201
v_mfma_f32_16x16x32_bf16 v[232:235],a[140:143],v[124:127],v[232:235]
s_lshr_b32 s45,s45,1
ds_read_b128 a[132:135],v1 offset:256
v_cvt_pk_bf16_f32 v200,v200,v201
v_mfma_f32_16x16x32_bf16 v[236:239],a[156:159],v[124:127],v[44:47]
buffer_load_dwordx4 v4,s[20:23],s47 offen nt lds
v_mfma_f32_16x16x32_bf16 v[236:239],a[152:155],v[120:123],v[236:239]
ds_read_b128 a[136:139],v1 offset:512
v_add_u32 v2,s48,v2
v_mfma_f32_16x16x32_bf16 v[236:239],a[148:151],v[116:119],v[236:239]
v_exp_f32 v202,v202
v_mfma_f32_16x16x32_bf16 v[236:239],a[144:147],v[112:115],v[236:239]
s_add_u32 s62,s50,32768
ds_read_b128 a[140:143],v1 offset:768
v_add_u32 v3,s48,v3
v_mfma_f32_16x16x32_bf16 v[248:251],a[160:163],v[112:115],v[44:47]
s_and_b32 s62,s62,131071
v_exp_f32 v203,v203
v_mfma_f32_16x16x32_bf16 v[248:251],a[164:167],v[116:119],v[248:251]
s_add_u32 s54,s54,16384
ds_read_b128 a[156:159],v1 offset:4864
v_cvt_pk_bf16_f32 v201,v202,v203
v_mfma_f32_16x16x32_bf16 v[248:251],a[168:171],v[120:123],v[248:251]
s_sub_u32 s48,s62,s50
v_exp_f32 v204,v204
v_mfma_f32_16x16x32_bf16 v[248:251],a[172:175],v[124:127],v[248:251]
s_sub_u32 s53,s53,1
v_exp_f32 v205,v205
v_mfma_f32_16x16x32_bf16 v[252:255],a[188:191],v[124:127],v[44:47]
s_and_b32 s46,s54,0xfffc0000
ds_read_b128 a[152:155],v1 offset:4608
v_cvt_pk_bf16_f32 v202,v204,v205
v_mfma_f32_16x16x32_bf16 v[252:255],a[184:187],v[120:123],v[252:255]
s_mov_b32 s50,s62
v_exp_f32 v206,v206
v_mfma_f32_16x16x32_bf16 v[252:255],a[180:183],v[116:119],v[252:255]
v_exp_f32 v207,v207
v_mfma_f32_16x16x32_bf16 v[252:255],a[176:179],v[112:115],v[252:255]
s_waitcnt lgkmcnt(2)
ds_read_b128 a[148:151],v1 offset:4352
v_cvt_pk_bf16_f32 v203,v206,v207
v_exp_f32 v216,v216
ds_read_b128 a[144:147],v1 offset:4096
buffer_load_dwordx4 v4,s[20:23],s47 offen offset:1024 nt lds
ds_read_b128 a[160:163],v1 offset:8192
v_exp_f32 v217,v217
ds_read_b128 a[164:167],v1 offset:8448
v_cvt_pk_bf16_f32 v204,v216,v217
v_exp_f32 v218,v218
ds_read_b128 a[168:171],v1 offset:8704
v_exp_f32 v219,v219
ds_read_b128 a[172:175],v1 offset:8960
v_cvt_pk_bf16_f32 v205,v218,v219
v_exp_f32 v220,v220
ds_read_b128 a[188:191],v1 offset:13056
v_exp_f32 v221,v221
ds_read_b128 a[184:187],v1 offset:12800
v_cvt_pk_bf16_f32 v206,v220,v221
v_exp_f32 v222,v222
ds_read_b128 a[180:183],v1 offset:12544
v_mfma_f32_16x16x32_bf16 v[136:139],a[128:131],v[64:67],v[32:35]
buffer_load_dwordx4 v5,s[20:23],s47 offen offset:2048 nt lds
v_mfma_f32_16x16x32_bf16 v[136:139],a[132:135],v[68:71],v[136:139]
ds_read_b128 a[176:179],v1 offset:12288
v_add_u32 v1,s48,v1
v_mfma_f32_16x16x32_bf16 v[136:139],a[136:139],v[72:75],v[136:139]
v_exp_f32 v223,v223
v_mfma_f32_16x16x32_bf16 v[136:139],a[140:143],v[76:79],v[136:139]
s_waitcnt lgkmcnt(11)
v_cvt_pk_bf16_f32 v207,v222,v223
v_mfma_f32_16x16x32_bf16 v[140:143],a[156:159],v[76:79],v[32:35]
s_waitcnt lgkmcnt(10)
v_exp_f32 v232,v232
v_mfma_f32_16x16x32_bf16 v[140:143],a[152:155],v[72:75],v[140:143]
s_waitcnt lgkmcnt(9)
v_exp_f32 v233,v233
v_mfma_f32_16x16x32_bf16 v[140:143],a[148:151],v[68:71],v[140:143]
s_waitcnt lgkmcnt(8)
v_exp_f32 v234,v234
v_mfma_f32_16x16x32_bf16 v[140:143],a[144:147],v[64:67],v[140:143]
s_waitcnt lgkmcnt(1)
v_exp_f32 v235,v235
v_mfma_f32_16x16x32_bf16 v[152:155],a[160:163],v[64:67],v[32:35]
v_exp_f32 v236,v236
v_mfma_f32_16x16x32_bf16 v[152:155],a[164:167],v[68:71],v[152:155]
v_exp_f32 v237,v237
v_mfma_f32_16x16x32_bf16 v[152:155],a[168:171],v[72:75],v[152:155]
)A"
R"A(v_exp_f32 v238,v238
v_mfma_f32_16x16x32_bf16 v[152:155],a[172:175],v[76:79],v[152:155]
v_exp_f32 v239,v239
v_mfma_f32_16x16x32_bf16 v[156:159],a[188:191],v[76:79],v[32:35]
v_exp_f32 v248,v248
v_mfma_f32_16x16x32_bf16 v[156:159],a[184:187],v[72:75],v[156:159]
buffer_load_dwordx4 v5,s[20:23],s47 offen offset:3072 nt lds
v_mfma_f32_16x16x32_bf16 v[156:159],a[180:183],v[68:71],v[156:159]
s_waitcnt lgkmcnt(0)
v_exp_f32 v249,v249
v_mfma_f32_16x16x32_bf16 v[156:159],a[176:179],v[64:67],v[156:159]
v_exp_f32 v250,v250
v_mfma_f32_16x16x32_bf16 a[64:67],a[192:195],v[200:203],a[64:67]
v_exp_f32 v251,v251
v_mfma_f32_16x16x32_bf16 a[64:67],a[196:199],v[204:207],a[64:67]
v_exp_f32 v252,v252
v_mfma_f32_16x16x32_bf16 a[68:71],a[204:207],v[204:207],a[68:71]
v_exp_f32 v253,v253
v_mfma_f32_16x16x32_bf16 a[68:71],a[200:203],v[200:203],a[68:71]
v_exp_f32 v254,v254
v_mfma_f32_16x16x32_bf16 a[72:75],a[208:211],v[200:203],a[72:75]
v_exp_f32 v255,v255
v_mfma_f32_16x16x32_bf16 a[72:75],a[212:215],v[204:207],a[72:75]
v_exp_f32 v136,v136
v_mfma_f32_16x16x32_bf16 a[76:79],a[220:223],v[204:207],a[76:79]
v_exp_f32 v137,v137
v_mfma_f32_16x16x32_bf16 a[76:79],a[216:219],v[200:203],a[76:79]
v_exp_f32 v138,v138
v_mfma_f32_16x16x32_bf16 a[80:83],a[224:227],v[200:203],a[80:83]
s_add_u32 m0,s49,s52
v_cvt_pk_bf16_f32 v232,v232,v233
v_cvt_pk_bf16_f32 v233,v234,v235
v_mfma_f32_16x16x32_bf16 a[80:83],a[228:231],v[204:207],a[80:83]
s_xor_b32 s49,s50,65536
v_cvt_pk_bf16_f32 v234,v236,v237
v_cvt_pk_bf16_f32 v235,v238,v239
v_mfma_f32_16x16x32_bf16 a[84:87],a[236:239],v[204:207],a[84:87]
v_cvt_pk_bf16_f32 v236,v248,v249
v_cvt_pk_bf16_f32 v237,v250,v251
v_mfma_f32_16x16x32_bf16 a[84:87],a[232:235],v[200:203],a[84:87]
buffer_load_dwordx4 v4,s[24:27],s47 offen nt lds
v_mfma_f32_16x16x32_bf16 a[88:91],a[240:243],v[200:203],a[88:91]
v_cvt_pk_bf16_f32 v238,v252,v253
v_cvt_pk_bf16_f32 v239,v254,v255
v_mfma_f32_16x16x32_bf16 a[88:91],a[244:247],v[204:207],a[88:91]
v_exp_f32 v139,v139
v_mfma_f32_16x16x32_bf16 a[92:95],a[252:255],v[204:207],a[92:95]
v_exp_f32 v140,v140
v_mfma_f32_16x16x32_bf16 a[92:95],a[248:251],v[200:203],a[92:95]
v_exp_f32 v141,v141
v_smfmac_f32_16x16x64_bf16 v[56:59],v[28:31],v[200:207],v27
v_exp_f32 v142,v142
v_exp_f32 v143,v143
v_exp_f32 v152,v152
v_exp_f32 v153,v153
v_exp_f32 v154,v154
v_exp_f32 v155,v155
v_exp_f32 v156,v156
v_exp_f32 v157,v157
buffer_load_dwordx4 v5,s[24:27],s47 offen offset:1024 nt lds
v_exp_f32 v158,v158
v_exp_f32 v159,v159
v_mfma_f32_16x16x32_bf16 a[96:99],a[192:195],v[232:235],a[96:99]
ds_read_b64_tr_b16 a[192:193],v2
v_mfma_f32_16x16x32_bf16 a[96:99],a[196:199],v[236:239],a[96:99]
ds_read_b64_tr_b16 a[194:195],v2 offset:4096
v_mfma_f32_16x16x32_bf16 a[100:103],a[204:207],v[236:239],a[100:103]
v_mfma_f32_16x16x32_bf16 a[100:103],a[200:203],v[232:235],a[100:103]
ds_read_b64_tr_b16 a[196:197],v2 offset:8192
v_mfma_f32_16x16x32_bf16 a[104:107],a[208:211],v[232:235],a[104:107]
v_mfma_f32_16x16x32_bf16 a[104:107],a[212:215],v[236:239],a[104:107]
v_mfma_f32_16x16x32_bf16 a[108:111],a[220:223],v[236:239],a[108:111]
buffer_load_dwordx4 v4,s[24:27],s47 offen offset:2048 nt lds
v_mfma_f32_16x16x32_bf16 a[108:111],a[216:219],v[232:235],a[108:111]
ds_read_b64_tr_b16 a[198:199],v2 offset:12288
v_mfma_f32_16x16x32_bf16 a[112:115],a[224:227],v[232:235],a[112:115]
v_mfma_f32_16x16x32_bf16 a[112:115],a[228:231],v[236:239],a[112:115]
v_mfma_f32_16x16x32_bf16 a[116:119],a[236:239],v[236:239],a[116:119]
ds_read_b64_tr_b16 a[204:205],v3 offset:8192
v_cvt_pk_bf16_f32 v136,v136,v137
v_mfma_f32_16x16x32_bf16 a[116:119],a[232:235],v[232:235],a[116:119]
ds_read_b64_tr_b16 a[206:207],v3 offset:12288
v_mfma_f32_16x16x32_bf16 a[120:123],a[240:243],v[232:235],a[120:123]
ds_read_b64_tr_b16 a[200:201],v3
v_mfma_f32_16x16x32_bf16 a[120:123],a[244:247],v[236:239],a[120:123]
ds_read_b64_tr_b16 a[202:203],v3 offset:4096
v_cvt_pk_bf16_f32 v137,v138,v139
v_mfma_f32_16x16x32_bf16 a[124:127],a[252:255],v[236:239],a[124:127]
ds_read_b64_tr_b16 a[208:209],v2 offset:256
v_mfma_f32_16x16x32_bf16 a[124:127],a[248:251],v[232:235],a[124:127]
ds_read_b64_tr_b16 a[210:211],v2 offset:4352
v_smfmac_f32_16x16x64_bf16 v[60:63],v[28:31],v[232:239],v27
ds_read_b64_tr_b16 a[212:213],v2 offset:8448
v_mfma_f32_16x16x32_bf16 v[200:203],a[128:131],v[96:99],v[40:43]
ds_read_b64_tr_b16 a[214:215],v2 offset:12544
v_cvt_pk_bf16_f32 v138,v140,v141
v_mfma_f32_16x16x32_bf16 v[200:203],a[132:135],v[100:103],v[200:203]
ds_read_b64_tr_b16 a[220:221],v3 offset:8448
v_mfma_f32_16x16x32_bf16 v[200:203],a[136:139],v[104:107],v[200:203]
ds_read_b64_tr_b16 a[222:223],v3 offset:12544
v_cvt_pk_bf16_f32 v139,v142,v143
v_mfma_f32_16x16x32_bf16 v[200:203],a[140:143],v[108:111],v[200:203]
buffer_load_dwordx4 v5,s[24:27],s47 offen offset:3072 nt lds
v_mfma_f32_16x16x32_bf16 v[204:207],a[156:159],v[108:111],v[40:43]
s_add_u32 s47,s47,16384
s_and_b32 s47,s47,0x3ffff
ds_read_b64_tr_b16 a[216:217],v3 offset:256
v_mfma_f32_16x16x32_bf16 v[204:207],a[152:155],v[104:107],v[204:207]
s_or_b32 s47,s47,s46
ds_read_b64_tr_b16 a[218:219],v3 offset:4352
v_cvt_pk_bf16_f32 v140,v152,v153
v_mfma_f32_16x16x32_bf16 v[204:207],a[148:151],v[100:103],v[204:207]
ds_read_b64_tr_b16 a[224:225],v2 offset:512
v_mfma_f32_16x16x32_bf16 v[204:207],a[144:147],v[96:99],v[204:207]
ds_read_b64_tr_b16 a[226:227],v2 offset:4608
v_mfma_f32_16x16x32_bf16 v[216:219],a[160:163],v[96:99],v[40:43]
ds_read_b64_tr_b16 a[228:229],v2 offset:8704
v_cvt_pk_bf16_f32 v141,v154,v155
v_mfma_f32_16x16x32_bf16 v[216:219],a[164:167],v[100:103],v[216:219]
ds_read_b64_tr_b16 a[230:231],v2 offset:12800
v_mfma_f32_16x16x32_bf16 v[216:219],a[168:171],v[104:107],v[216:219]
ds_read_b64_tr_b16 a[236:237],v3 offset:8704
v_mfma_f32_16x16x32_bf16 v[216:219],a[172:175],v[108:111],v[216:219]
ds_read_b64_tr_b16 a[238:239],v3 offset:12800
v_cvt_pk_bf16_f32 v142,v156,v157
v_mfma_f32_16x16x32_bf16 v[220:223],a[188:191],v[108:111],v[40:43]
ds_read_b64_tr_b16 a[232:233],v3 offset:512
v_mfma_f32_16x16x32_bf16 v[220:223],a[184:187],v[104:107],v[220:223]
ds_read_b64_tr_b16 a[234:235],v3 offset:4608
v_mfma_f32_16x16x32_bf16 v[220:223],a[180:183],v[100:103],v[220:223]
ds_read_b64_tr_b16 a[240:241],v2 offset:768
v_cvt_pk_bf16_f32 v143,v158,v159
v_mfma_f32_16x16x32_bf16 v[220:223],a[176:179],v[96:99],v[220:223]
s_waitcnt lgkmcnt(7)
buffer_load_dword v26,v24,s[88:91],s93 offen
v_mfma_f32_16x16x32_bf16 a[0:3],a[192:195],v[136:139],a[0:3]
ds_read_b64_tr_b16 a[242:243],v2 offset:4864
v_mfma_f32_16x16x32_bf16 a[0:3],a[196:199],v[140:143],a[0:3]
ds_read_b64_tr_b16 a[244:245],v2 offset:8960
v_mfma_f32_16x16x32_bf16 a[4:7],a[204:207],v[140:143],a[4:7]
ds_read_b64_tr_b16 a[246:247],v2 offset:13056
v_mfma_f32_16x16x32_bf16 a[4:7],a[200:203],v[136:139],a[4:7]
ds_read_b64_tr_b16 a[252:253],v3 offset:8960
v_mfma_f32_16x16x32_bf16 a[8:11],a[208:211],v[136:139],a[8:11]
ds_read_b64_tr_b16 a[254:255],v3 offset:13056
v_mfma_f32_16x16x32_bf16 a[8:11],a[212:215],v[140:143],a[8:11]
ds_read_b64_tr_b16 a[248:249],v3 offset:768
v_mfma_f32_16x16x32_bf16 a[12:15],a[220:223],v[140:143],a[12:15]
ds_read_b64_tr_b16 a[250:251],v3 offset:4864
v_mfma_f32_16x16x32_bf16 a[12:15],a[216:219],v[136:139],a[12:15]
s_bitcmp1_b32 s45,0
s_cbranch_scc0 L_t31v10_%=
s_mov_b32 m0,s80
s_add_u32 s80,s80,4096
buffer_load_dwordx4 v25,s[20:23],s83 offen nt lds
s_add_u32 s83,s83,4096
L_t31v10_%=:
v_mfma_f32_16x16x32_bf16 a[16:19],a[224:227],v[136:139],a[16:19]
s_waitcnt lgkmcnt(6)
v_mfma_f32_16x16x32_bf16 a[16:19],a[228:231],v[140:143],a[16:19]
v_mfma_f32_16x16x32_bf16 a[20:23],a[236:239],v[140:143],a[20:23]
v_mfma_f32_16x16x32_bf16 a[20:23],a[232:235],v[136:139],a[20:23]
v_mfma_f32_16x16x32_bf16 a[24:27],a[240:243],v[136:139],a[24:27]
s_waitcnt lgkmcnt(2)
v_mfma_f32_16x16x32_bf16 a[24:27],a[244:247],v[140:143],a[24:27]
v_mfma_f32_16x16x32_bf16 a[28:31],a[252:255],v[140:143],a[28:31]
s_waitcnt lgkmcnt(0)
v_mfma_f32_16x16x32_bf16 a[28:31],a[248:251],v[136:139],a[28:31]
s_cmp_lg_u32 s53,0
v_smfmac_f32_16x16x64_bf16 v[48:51],v[28:31],v[136:143],v27
s_cbranch_scc1 L_att0v1_%=
s_waitcnt lgkmcnt(0)
s_nop 7
s_branch L_att0x_%=
.p2align 5
L_att0v2_%=:
v_mfma_f32_16x16x32_bf16 v[232:235],a[128:131],v[112:115],v[44:47]
s_waitcnt vmcnt(8) lgkmcnt(0)
v_mfma_f32_16x16x32_bf16 v[232:235],a[132:135],v[116:119],v[232:235]
s_barrier
s_add_u32 m0,s49,s51
ds_read_b128 a[128:131],v1
v_mfma_f32_16x16x32_bf16 v[232:235],a[136:139],v[120:123],v[232:235]
s_mov_b32 s90,L_endseg_%=-L_pc_%=+128
v_mfma_f32_16x16x32_bf16 v[232:235],a[140:143],v[124:127],v[232:235]
s_lshr_b32 s45,s45,1
ds_read_b128 a[132:135],v1 offset:256
v_mfma_f32_16x16x32_bf16 v[236:239],a[156:159],v[124:127],v[44:47]
buffer_load_dwordx4 v4,s[20:23],s47 offen nt lds
v_mfma_f32_16x16x32_bf16 v[236:239],a[152:155],v[120:123],v[236:239]
ds_read_b128 a[136:139],v1 offset:512
v_add_u32 v2,s48,v2
v_mfma_f32_16x16x32_bf16 v[236:239],a[148:151],v[116:119],v[236:239]
v_mfma_f32_16x16x32_bf16 v[236:239],a[144:147],v[112:115],v[236:239]
s_add_u32 s62,s50,32768
ds_read_b128 a[140:143],v1 offset:768
v_add_u32 v3,s48,v3
v_mfma_f32_16x16x32_bf16 v[248:251],a[160:163],v[112:115],v[44:47]
s_and_b32 s62,s62,131071
v_mfma_f32_16x16x32_bf16 v[248:251],a[164:167],v[116:119],v[248:251]
s_add_u32 s54,s54,16384
ds_read_b128 a[156:159],v1 offset:4864
v_mfma_f32_16x16x32_bf16 v[248:251],a[168:171],v[120:123],v[248:251]
s_sub_u32 s48,s62,s50
v_mfma_f32_16x16x32_bf16 v[248:251],a[172:175],v[124:127],v[248:251]
s_sub_u32 s53,s53,1
v_mfma_f32_16x16x32_bf16 v[252:255],a[188:191],v[124:127],v[44:47]
s_and_b32 s46,s54,0xfffc0000
ds_read_b128 a[152:155],v1 offset:4608
v_mfma_f32_16x16x32_bf16 v[252:255],a[184:187],v[120:123],v[252:255]
s_mov_b32 s50,s62
v_mfma_f32_16x16x32_bf16 v[252:255],a[180:183],v[116:119],v[252:255]
v_mfma_f32_16x16x32_bf16 v[252:255],a[176:179],v[112:115],v[252:255]
ds_read_b128 a[148:151],v1 offset:4352
v_mfma_f32_16x16x32_bf16 a[32:35],a[192:195],v[168:171],a[32:35]
v_mfma_f32_16x16x32_bf16 a[32:35],a[196:199],v[172:175],a[32:35]
ds_read_b128 a[144:147],v1 offset:4096
v_mfma_f32_16x16x32_bf16 a[36:39],a[204:207],v[172:175],a[36:39]
buffer_load_dwordx4 v4,s[20:23],s47 offen offset:1024 nt lds
v_mfma_f32_16x16x32_bf16 a[36:39],a[200:203],v[168:171],a[36:39]
ds_read_b128 a[160:163],v1 offset:8192
v_mfma_f32_16x16x32_bf16 a[40:43],a[208:211],v[168:171],a[40:43]
v_mfma_f32_16x16x32_bf16 a[40:43],a[212:215],v[172:175],a[40:43]
ds_read_b128 a[164:167],v1 offset:8448
v_mfma_f32_16x16x32_bf16 a[44:47],a[220:223],v[172:175],a[44:47]
v_mfma_f32_16x16x32_bf16 a[44:47],a[216:219],v[168:171],a[44:47]
ds_read_b128 a[168:171],v1 offset:8704
v_mfma_f32_16x16x32_bf16 a[48:51],a[224:227],v[168:171],a[48:51]
v_mfma_f32_16x16x32_bf16 a[48:51],a[228:231],v[172:175],a[48:51]
ds_read_b128 a[172:175],v1 offset:8960
v_mfma_f32_16x16x32_bf16 a[52:55],a[236:239],v[172:175],a[52:55]
v_mfma_f32_16x16x32_bf16 a[52:55],a[232:235],v[168:171],a[52:55]
ds_read_b128 a[188:191],v1 offset:13056
v_mfma_f32_16x16x32_bf16 a[56:59],a[240:243],v[168:171],a[56:59]
v_mfma_f32_16x16x32_bf16 a[56:59],a[244:247],v[172:175],a[56:59]
ds_read_b128 a[184:187],v1 offset:12800
v_mfma_f32_16x16x32_bf16 a[60:63],a[252:255],v[172:175],a[60:63]
v_mfma_f32_16x16x32_bf16 a[60:63],a[248:251],v[168:171],a[60:63]
ds_read_b128 a[180:183],v1 offset:12544
v_smfmac_f32_16x16x64_bf16 v[52:55],v[28:31],v[168:175],v27
s_waitcnt lgkmcnt(4)
v_mfma_f32_16x16x32_bf16 v[136:139],a[128:131],v[64:67],v[32:35]
buffer_load_dwordx4 v5,s[20:23],s47 offen offset:2048 nt lds
v_mfma_f32_16x16x32_bf16 v[136:139],a[132:135],v[68:71],v[136:139]
ds_read_b128 a[176:179],v1 offset:12288
v_add_u32 v1,s48,v1
v_mfma_f32_16x16x32_bf16 v[136:139],a[136:139],v[72:75],v[136:139]
v_mfma_f32_16x16x32_bf16 v[136:139],a[140:143],v[76:79],v[136:139]
v_mfma_f32_16x16x32_bf16 v[140:143],a[156:159],v[76:79],v[32:35]
v_exp_f32 v232,v232
v_mfma_f32_16x16x32_bf16 v[140:143],a[152:155],v[72:75],v[140:143]
v_exp_f32 v233,v233
v_mfma_f32_16x16x32_bf16 v[140:143],a[148:151],v[68:71],v[140:143]
v_exp_f32 v234,v234
v_mfma_f32_16x16x32_bf16 v[140:143],a[144:147],v[64:67],v[140:143]
)A"
R"A(v_exp_f32 v235,v235
v_mfma_f32_16x16x32_bf16 v[152:155],a[160:163],v[64:67],v[32:35]
v_exp_f32 v236,v236
v_mfma_f32_16x16x32_bf16 v[152:155],a[164:167],v[68:71],v[152:155]
v_exp_f32 v237,v237
v_mfma_f32_16x16x32_bf16 v[152:155],a[168:171],v[72:75],v[152:155]
s_waitcnt lgkmcnt(0)
v_exp_f32 v238,v238
v_mfma_f32_16x16x32_bf16 v[152:155],a[172:175],v[76:79],v[152:155]
v_exp_f32 v239,v239
v_mfma_f32_16x16x32_bf16 v[156:159],a[188:191],v[76:79],v[32:35]
v_exp_f32 v248,v248
v_mfma_f32_16x16x32_bf16 v[156:159],a[184:187],v[72:75],v[156:159]
buffer_load_dwordx4 v5,s[20:23],s47 offen offset:3072 nt lds
v_mfma_f32_16x16x32_bf16 v[156:159],a[180:183],v[68:71],v[156:159]
v_exp_f32 v249,v249
v_mfma_f32_16x16x32_bf16 v[156:159],a[176:179],v[64:67],v[156:159]
v_exp_f32 v250,v250
v_exp_f32 v251,v251
v_exp_f32 v252,v252
v_exp_f32 v253,v253
v_exp_f32 v254,v254
v_exp_f32 v255,v255
v_exp_f32 v136,v136
v_exp_f32 v137,v137
v_exp_f32 v138,v138
s_add_u32 m0,s49,s52
v_cvt_pk_bf16_f32 v232,v232,v233
v_cvt_pk_bf16_f32 v233,v234,v235
s_xor_b32 s49,s50,65536
v_cvt_pk_bf16_f32 v234,v236,v237
v_cvt_pk_bf16_f32 v235,v238,v239
v_cvt_pk_bf16_f32 v236,v248,v249
v_cvt_pk_bf16_f32 v237,v250,v251
buffer_load_dwordx4 v4,s[24:27],s47 offen nt lds
v_cvt_pk_bf16_f32 v238,v252,v253
v_cvt_pk_bf16_f32 v239,v254,v255
v_exp_f32 v139,v139
v_exp_f32 v140,v140
v_exp_f32 v141,v141
v_mfma_f32_16x16x32_bf16 v[168:171],a[128:131],v[80:83],v[36:39]
v_exp_f32 v142,v142
v_mfma_f32_16x16x32_bf16 v[168:171],a[132:135],v[84:87],v[168:171]
v_exp_f32 v143,v143
v_mfma_f32_16x16x32_bf16 v[168:171],a[136:139],v[88:91],v[168:171]
v_exp_f32 v152,v152
v_mfma_f32_16x16x32_bf16 v[168:171],a[140:143],v[92:95],v[168:171]
v_exp_f32 v153,v153
v_mfma_f32_16x16x32_bf16 v[172:175],a[156:159],v[92:95],v[36:39]
v_exp_f32 v154,v154
v_mfma_f32_16x16x32_bf16 v[172:175],a[152:155],v[88:91],v[172:175]
v_exp_f32 v155,v155
v_mfma_f32_16x16x32_bf16 v[172:175],a[148:151],v[84:87],v[172:175]
v_exp_f32 v156,v156
v_mfma_f32_16x16x32_bf16 v[172:175],a[144:147],v[80:83],v[172:175]
v_exp_f32 v157,v157
v_mfma_f32_16x16x32_bf16 v[184:187],a[160:163],v[80:83],v[36:39]
buffer_load_dwordx4 v5,s[24:27],s47 offen offset:1024 nt lds
v_mfma_f32_16x16x32_bf16 v[184:187],a[164:167],v[84:87],v[184:187]
v_exp_f32 v158,v158
v_mfma_f32_16x16x32_bf16 v[184:187],a[168:171],v[88:91],v[184:187]
v_exp_f32 v159,v159
v_mfma_f32_16x16x32_bf16 v[184:187],a[172:175],v[92:95],v[184:187]
v_exp_f32 v168,v168
v_mfma_f32_16x16x32_bf16 v[188:191],a[188:191],v[92:95],v[36:39]
v_exp_f32 v169,v169
v_mfma_f32_16x16x32_bf16 v[188:191],a[184:187],v[88:91],v[188:191]
v_exp_f32 v170,v170
v_mfma_f32_16x16x32_bf16 v[188:191],a[180:183],v[84:87],v[188:191]
v_exp_f32 v171,v171
v_mfma_f32_16x16x32_bf16 v[188:191],a[176:179],v[80:83],v[188:191]
v_exp_f32 v172,v172
v_mfma_f32_16x16x32_bf16 a[96:99],a[192:195],v[232:235],a[96:99]
ds_read_b64_tr_b16 a[192:193],v2
v_cvt_pk_bf16_f32 v168,v168,v169
v_mfma_f32_16x16x32_bf16 a[96:99],a[196:199],v[236:239],a[96:99]
ds_read_b64_tr_b16 a[194:195],v2 offset:4096
v_cvt_pk_bf16_f32 v169,v170,v171
v_mfma_f32_16x16x32_bf16 a[100:103],a[204:207],v[236:239],a[100:103]
v_exp_f32 v173,v173
v_mfma_f32_16x16x32_bf16 a[100:103],a[200:203],v[232:235],a[100:103]
ds_read_b64_tr_b16 a[196:197],v2 offset:8192
v_cvt_pk_bf16_f32 v170,v172,v173
v_mfma_f32_16x16x32_bf16 a[104:107],a[208:211],v[232:235],a[104:107]
v_exp_f32 v174,v174
v_mfma_f32_16x16x32_bf16 a[104:107],a[212:215],v[236:239],a[104:107]
v_exp_f32 v175,v175
v_mfma_f32_16x16x32_bf16 a[108:111],a[220:223],v[236:239],a[108:111]
buffer_load_dwordx4 v4,s[24:27],s47 offen offset:2048 nt lds
v_mfma_f32_16x16x32_bf16 a[108:111],a[216:219],v[232:235],a[108:111]
ds_read_b64_tr_b16 a[198:199],v2 offset:12288
v_cvt_pk_bf16_f32 v171,v174,v175
v_mfma_f32_16x16x32_bf16 a[112:115],a[224:227],v[232:235],a[112:115]
v_exp_f32 v184,v184
v_mfma_f32_16x16x32_bf16 a[112:115],a[228:231],v[236:239],a[112:115]
v_exp_f32 v185,v185
v_mfma_f32_16x16x32_bf16 a[116:119],a[236:239],v[236:239],a[116:119]
ds_read_b64_tr_b16 a[204:205],v3 offset:8192
v_cvt_pk_bf16_f32 v136,v136,v137
v_mfma_f32_16x16x32_bf16 a[116:119],a[232:235],v[232:235],a[116:119]
ds_read_b64_tr_b16 a[206:207],v3 offset:12288
v_cvt_pk_bf16_f32 v172,v184,v185
v_mfma_f32_16x16x32_bf16 a[120:123],a[240:243],v[232:235],a[120:123]
ds_read_b64_tr_b16 a[200:201],v3
v_mfma_f32_16x16x32_bf16 a[120:123],a[244:247],v[236:239],a[120:123]
ds_read_b64_tr_b16 a[202:203],v3 offset:4096
v_cvt_pk_bf16_f32 v137,v138,v139
v_mfma_f32_16x16x32_bf16 a[124:127],a[252:255],v[236:239],a[124:127]
ds_read_b64_tr_b16 a[208:209],v2 offset:256
v_mfma_f32_16x16x32_bf16 a[124:127],a[248:251],v[232:235],a[124:127]
ds_read_b64_tr_b16 a[210:211],v2 offset:4352
v_smfmac_f32_16x16x64_bf16 v[60:63],v[28:31],v[232:239],v27
s_waitcnt lgkmcnt(6)
ds_read_b64_tr_b16 a[212:213],v2 offset:8448
ds_read_b64_tr_b16 a[214:215],v2 offset:12544
v_cvt_pk_bf16_f32 v138,v140,v141
ds_read_b64_tr_b16 a[220:221],v3 offset:8448
ds_read_b64_tr_b16 a[222:223],v3 offset:12544
v_cvt_pk_bf16_f32 v139,v142,v143
buffer_load_dwordx4 v5,s[24:27],s47 offen offset:3072 nt lds
s_add_u32 s47,s47,16384
s_and_b32 s47,s47,0x3ffff
ds_read_b64_tr_b16 a[216:217],v3 offset:256
s_or_b32 s47,s47,s46
ds_read_b64_tr_b16 a[218:219],v3 offset:4352
v_cvt_pk_bf16_f32 v140,v152,v153
ds_read_b64_tr_b16 a[224:225],v2 offset:512
ds_read_b64_tr_b16 a[226:227],v2 offset:4608
ds_read_b64_tr_b16 a[228:229],v2 offset:8704
v_cvt_pk_bf16_f32 v141,v154,v155
ds_read_b64_tr_b16 a[230:231],v2 offset:12800
ds_read_b64_tr_b16 a[236:237],v3 offset:8704
ds_read_b64_tr_b16 a[238:239],v3 offset:12800
v_cvt_pk_bf16_f32 v142,v156,v157
ds_read_b64_tr_b16 a[232:233],v3 offset:512
ds_read_b64_tr_b16 a[234:235],v3 offset:4608
ds_read_b64_tr_b16 a[240:241],v2 offset:768
v_cvt_pk_bf16_f32 v143,v158,v159
buffer_load_dword v26,v24,s[88:91],s93 offen
v_mfma_f32_16x16x32_bf16 a[0:3],a[192:195],v[136:139],a[0:3]
ds_read_b64_tr_b16 a[242:243],v2 offset:4864
v_mfma_f32_16x16x32_bf16 a[0:3],a[196:199],v[140:143],a[0:3]
s_waitcnt lgkmcnt(15)
ds_read_b64_tr_b16 a[244:245],v2 offset:8960
v_mfma_f32_16x16x32_bf16 a[4:7],a[204:207],v[140:143],a[4:7]
ds_read_b64_tr_b16 a[246:247],v2 offset:13056
v_mfma_f32_16x16x32_bf16 a[4:7],a[200:203],v[136:139],a[4:7]
ds_read_b64_tr_b16 a[252:253],v3 offset:8960
v_mfma_f32_16x16x32_bf16 a[8:11],a[208:211],v[136:139],a[8:11]
s_waitcnt lgkmcnt(15)
ds_read_b64_tr_b16 a[254:255],v3 offset:13056
v_mfma_f32_16x16x32_bf16 a[8:11],a[212:215],v[140:143],a[8:11]
ds_read_b64_tr_b16 a[248:249],v3 offset:768
v_mfma_f32_16x16x32_bf16 a[12:15],a[220:223],v[140:143],a[12:15]
s_waitcnt lgkmcnt(15)
ds_read_b64_tr_b16 a[250:251],v3 offset:4864
v_mfma_f32_16x16x32_bf16 a[12:15],a[216:219],v[136:139],a[12:15]
s_bitcmp1_b32 s45,0
s_cbranch_scc0 L_t31v20_%=
s_mov_b32 m0,s80
s_add_u32 s80,s80,4096
buffer_load_dwordx4 v25,s[20:23],s83 offen nt lds
s_add_u32 s83,s83,4096
L_t31v20_%=:
s_waitcnt lgkmcnt(7)
v_mfma_f32_16x16x32_bf16 a[16:19],a[224:227],v[136:139],a[16:19]
v_exp_f32 v186,v186
v_mfma_f32_16x16x32_bf16 a[16:19],a[228:231],v[140:143],a[16:19]
v_exp_f32 v187,v187
v_mfma_f32_16x16x32_bf16 a[20:23],a[236:239],v[140:143],a[20:23]
v_exp_f32 v188,v188
v_mfma_f32_16x16x32_bf16 a[20:23],a[232:235],v[136:139],a[20:23]
s_waitcnt lgkmcnt(3)
v_exp_f32 v189,v189
v_mfma_f32_16x16x32_bf16 a[24:27],a[240:243],v[136:139],a[24:27]
v_exp_f32 v190,v190
v_mfma_f32_16x16x32_bf16 a[24:27],a[244:247],v[140:143],a[24:27]
s_waitcnt lgkmcnt(1)
v_cvt_pk_bf16_f32 v173,v186,v187
v_mfma_f32_16x16x32_bf16 a[28:31],a[252:255],v[140:143],a[28:31]
s_waitcnt lgkmcnt(0)
v_exp_f32 v191,v191
v_mfma_f32_16x16x32_bf16 a[28:31],a[248:251],v[136:139],a[28:31]
v_cvt_pk_bf16_f32 v174,v188,v189
v_cvt_pk_bf16_f32 v175,v190,v191
s_cmp_lg_u32 s53,0
v_smfmac_f32_16x16x64_bf16 v[48:51],v[28:31],v[136:143],v27
s_cbranch_scc1 L_att0v2_%=
s_waitcnt lgkmcnt(0)
s_nop 7
s_branch L_att0x_%=
.p2align 5
L_att0_%=:
v_mfma_f32_16x16x32_bf16 v[232:235],a[128:131],v[112:115],v[44:47]
s_waitcnt vmcnt(8) lgkmcnt(0)
v_exp_f32 v200,v200
v_mfma_f32_16x16x32_bf16 v[232:235],a[132:135],v[116:119],v[232:235]
s_barrier
s_add_u32 m0,s49,s51
ds_read_b128 a[128:131],v1
v_mfma_f32_16x16x32_bf16 v[232:235],a[136:139],v[120:123],v[232:235]
s_mov_b32 s90,L_endseg_%=-L_pc_%=+128
v_exp_f32 v201,v201
v_mfma_f32_16x16x32_bf16 v[232:235],a[140:143],v[124:127],v[232:235]
s_lshr_b32 s45,s45,1
ds_read_b128 a[132:135],v1 offset:256
v_cvt_pk_bf16_f32 v200,v200,v201
v_mfma_f32_16x16x32_bf16 v[236:239],a[156:159],v[124:127],v[44:47]
s_sub_u32 s94,s94,3
buffer_load_dwordx4 v4,s[20:23],s47 offen nt lds
v_mfma_f32_16x16x32_bf16 v[236:239],a[152:155],v[120:123],v[236:239]
s_lshr_b32 s60,s94,2
ds_read_b128 a[136:139],v1 offset:512
v_add_u32 v2,s48,v2
v_mfma_f32_16x16x32_bf16 v[236:239],a[148:151],v[116:119],v[236:239]
s_max_i32 s60,s60,128
v_exp_f32 v202,v202
v_mfma_f32_16x16x32_bf16 v[236:239],a[144:147],v[112:115],v[236:239]
s_add_u32 s62,s50,32768
ds_read_b128 a[140:143],v1 offset:768
v_add_u32 v3,s48,v3
v_mfma_f32_16x16x32_bf16 v[248:251],a[160:163],v[112:115],v[44:47]
s_and_b32 s62,s62,131071
v_exp_f32 v203,v203
v_mfma_f32_16x16x32_bf16 v[248:251],a[164:167],v[116:119],v[248:251]
s_add_u32 s54,s54,16384
ds_read_b128 a[156:159],v1 offset:4864
v_cvt_pk_bf16_f32 v201,v202,v203
v_mfma_f32_16x16x32_bf16 v[248:251],a[168:171],v[120:123],v[248:251]
s_sub_u32 s48,s62,s50
v_exp_f32 v204,v204
v_mfma_f32_16x16x32_bf16 v[248:251],a[172:175],v[124:127],v[248:251]
s_sub_u32 s53,s53,1
v_exp_f32 v205,v205
v_mfma_f32_16x16x32_bf16 v[252:255],a[188:191],v[124:127],v[44:47]
s_and_b32 s46,s54,0xfffc0000
ds_read_b128 a[152:155],v1 offset:4608
v_cvt_pk_bf16_f32 v202,v204,v205
v_mfma_f32_16x16x32_bf16 v[252:255],a[184:187],v[120:123],v[252:255]
s_mov_b32 s50,s62
v_exp_f32 v206,v206
v_mfma_f32_16x16x32_bf16 v[252:255],a[180:183],v[116:119],v[252:255]
v_exp_f32 v207,v207
v_mfma_f32_16x16x32_bf16 v[252:255],a[176:179],v[112:115],v[252:255]
ds_read_b128 a[148:151],v1 offset:4352
v_cvt_pk_bf16_f32 v203,v206,v207
v_mfma_f32_16x16x32_bf16 a[32:35],a[192:195],v[168:171],a[32:35]
v_exp_f32 v216,v216
v_mfma_f32_16x16x32_bf16 a[32:35],a[196:199],v[172:175],a[32:35]
ds_read_b128 a[144:147],v1 offset:4096
v_mfma_f32_16x16x32_bf16 a[36:39],a[204:207],v[172:175],a[36:39]
buffer_load_dwordx4 v4,s[20:23],s47 offen offset:1024 nt lds
v_mfma_f32_16x16x32_bf16 a[36:39],a[200:203],v[168:171],a[36:39]
ds_read_b128 a[160:163],v1 offset:8192
v_mfma_f32_16x16x32_bf16 a[40:43],a[208:211],v[168:171],a[40:43]
v_exp_f32 v217,v217
v_mfma_f32_16x16x32_bf16 a[40:43],a[212:215],v[172:175],a[40:43]
ds_read_b128 a[164:167],v1 offset:8448
v_cvt_pk_bf16_f32 v204,v216,v217
v_mfma_f32_16x16x32_bf16 a[44:47],a[220:223],v[172:175],a[44:47]
v_exp_f32 v218,v218
v_mfma_f32_16x16x32_bf16 a[44:47],a[216:219],v[168:171],a[44:47]
ds_read_b128 a[168:171],v1 offset:8704
v_mfma_f32_16x16x32_bf16 a[48:51],a[224:227],v[168:171],a[48:51]
v_exp_f32 v219,v219
v_mfma_f32_16x16x32_bf16 a[48:51],a[228:231],v[172:175],a[48:51]
ds_read_b128 a[172:175],v1 offset:8960
v_cvt_pk_bf16_f32 v205,v218,v219
v_mfma_f32_16x16x32_bf16 a[52:55],a[236:239],v[172:175],a[52:55]
v_exp_f32 v220,v220
v_mfma_f32_16x16x32_bf16 a[52:55],a[232:235],v[168:171],a[52:55]
ds_read_b128 a[188:191],v1 offset:13056
v_mfma_f32_16x16x32_bf16 a[56:59],a[240:243],v[168:171],a[56:59]
v_exp_f32 v221,v221
v_mfma_f32_16x16x32_bf16 a[56:59],a[244:247],v[172:175],a[56:59]
ds_read_b128 a[184:187],v1 offset:12800
v_cvt_pk_bf16_f32 v206,v220,v221
v_mfma_f32_16x16x32_bf16 a[60:63],a[252:255],v[172:175],a[60:63]
v_exp_f32 v222,v222
v_mfma_f32_16x16x32_bf16 a[60:63],a[248:251],v[168:171],a[60:63]
ds_read_b128 a[180:183],v1 offset:12544
v_smfmac_f32_16x16x64_bf16 v[52:55],v[28:31],v[168:175],v27
s_waitcnt lgkmcnt(4)
v_mfma_f32_16x16x32_bf16 v[136:139],a[128:131],v[64:67],v[32:35]
buffer_load_dwordx4 v5,s[20:23],s47 offen offset:2048 nt lds
v_mfma_f32_16x16x32_bf16 v[136:139],a[132:135],v[68:71],v[136:139]
ds_read_b128 a[176:179],v1 offset:12288
v_add_u32 v1,s48,v1
v_mfma_f32_16x16x32_bf16 v[136:139],a[136:139],v[72:75],v[136:139]
)A"
R"A(v_exp_f32 v223,v223
v_mfma_f32_16x16x32_bf16 v[136:139],a[140:143],v[76:79],v[136:139]
v_cvt_pk_bf16_f32 v207,v222,v223
v_mfma_f32_16x16x32_bf16 v[140:143],a[156:159],v[76:79],v[32:35]
v_exp_f32 v232,v232
v_mfma_f32_16x16x32_bf16 v[140:143],a[152:155],v[72:75],v[140:143]
v_exp_f32 v233,v233
v_mfma_f32_16x16x32_bf16 v[140:143],a[148:151],v[68:71],v[140:143]
v_exp_f32 v234,v234
v_mfma_f32_16x16x32_bf16 v[140:143],a[144:147],v[64:67],v[140:143]
v_exp_f32 v235,v235
v_mfma_f32_16x16x32_bf16 v[152:155],a[160:163],v[64:67],v[32:35]
v_exp_f32 v236,v236
v_mfma_f32_16x16x32_bf16 v[152:155],a[164:167],v[68:71],v[152:155]
v_exp_f32 v237,v237
v_mfma_f32_16x16x32_bf16 v[152:155],a[168:171],v[72:75],v[152:155]
s_waitcnt lgkmcnt(0)
v_exp_f32 v238,v238
v_mfma_f32_16x16x32_bf16 v[152:155],a[172:175],v[76:79],v[152:155]
v_exp_f32 v239,v239
v_mfma_f32_16x16x32_bf16 v[156:159],a[188:191],v[76:79],v[32:35]
v_exp_f32 v248,v248
v_mfma_f32_16x16x32_bf16 v[156:159],a[184:187],v[72:75],v[156:159]
buffer_load_dwordx4 v5,s[20:23],s47 offen offset:3072 nt lds
v_mfma_f32_16x16x32_bf16 v[156:159],a[180:183],v[68:71],v[156:159]
v_exp_f32 v249,v249
v_mfma_f32_16x16x32_bf16 v[156:159],a[176:179],v[64:67],v[156:159]
v_exp_f32 v250,v250
v_mfma_f32_16x16x32_bf16 a[64:67],a[192:195],v[200:203],a[64:67]
v_exp_f32 v251,v251
v_mfma_f32_16x16x32_bf16 a[64:67],a[196:199],v[204:207],a[64:67]
v_exp_f32 v252,v252
v_mfma_f32_16x16x32_bf16 a[68:71],a[204:207],v[204:207],a[68:71]
v_exp_f32 v253,v253
v_mfma_f32_16x16x32_bf16 a[68:71],a[200:203],v[200:203],a[68:71]
v_exp_f32 v254,v254
v_mfma_f32_16x16x32_bf16 a[72:75],a[208:211],v[200:203],a[72:75]
v_exp_f32 v255,v255
v_mfma_f32_16x16x32_bf16 a[72:75],a[212:215],v[204:207],a[72:75]
v_exp_f32 v136,v136
v_mfma_f32_16x16x32_bf16 a[76:79],a[220:223],v[204:207],a[76:79]
v_exp_f32 v137,v137
v_mfma_f32_16x16x32_bf16 a[76:79],a[216:219],v[200:203],a[76:79]
v_exp_f32 v138,v138
v_mfma_f32_16x16x32_bf16 a[80:83],a[224:227],v[200:203],a[80:83]
s_add_u32 m0,s49,s52
v_cvt_pk_bf16_f32 v232,v232,v233
v_cvt_pk_bf16_f32 v233,v234,v235
v_mfma_f32_16x16x32_bf16 a[80:83],a[228:231],v[204:207],a[80:83]
s_xor_b32 s49,s50,65536
v_cvt_pk_bf16_f32 v234,v236,v237
v_cvt_pk_bf16_f32 v235,v238,v239
v_mfma_f32_16x16x32_bf16 a[84:87],a[236:239],v[204:207],a[84:87]
v_cvt_pk_bf16_f32 v236,v248,v249
v_cvt_pk_bf16_f32 v237,v250,v251
v_mfma_f32_16x16x32_bf16 a[84:87],a[232:235],v[200:203],a[84:87]
buffer_load_dwordx4 v4,s[24:27],s47 offen nt lds
v_mfma_f32_16x16x32_bf16 a[88:91],a[240:243],v[200:203],a[88:91]
v_cvt_pk_bf16_f32 v238,v252,v253
v_cvt_pk_bf16_f32 v239,v254,v255
v_mfma_f32_16x16x32_bf16 a[88:91],a[244:247],v[204:207],a[88:91]
v_exp_f32 v139,v139
v_mfma_f32_16x16x32_bf16 a[92:95],a[252:255],v[204:207],a[92:95]
v_exp_f32 v140,v140
v_mfma_f32_16x16x32_bf16 a[92:95],a[248:251],v[200:203],a[92:95]
v_exp_f32 v141,v141
v_smfmac_f32_16x16x64_bf16 v[56:59],v[28:31],v[200:207],v27
v_mfma_f32_16x16x32_bf16 v[168:171],a[128:131],v[80:83],v[36:39]
v_exp_f32 v142,v142
v_mfma_f32_16x16x32_bf16 v[168:171],a[132:135],v[84:87],v[168:171]
v_exp_f32 v143,v143
v_mfma_f32_16x16x32_bf16 v[168:171],a[136:139],v[88:91],v[168:171]
v_exp_f32 v152,v152
v_mfma_f32_16x16x32_bf16 v[168:171],a[140:143],v[92:95],v[168:171]
v_exp_f32 v153,v153
v_mfma_f32_16x16x32_bf16 v[172:175],a[156:159],v[92:95],v[36:39]
v_exp_f32 v154,v154
v_mfma_f32_16x16x32_bf16 v[172:175],a[152:155],v[88:91],v[172:175]
v_exp_f32 v155,v155
v_mfma_f32_16x16x32_bf16 v[172:175],a[148:151],v[84:87],v[172:175]
v_exp_f32 v156,v156
v_mfma_f32_16x16x32_bf16 v[172:175],a[144:147],v[80:83],v[172:175]
v_exp_f32 v157,v157
v_mfma_f32_16x16x32_bf16 v[184:187],a[160:163],v[80:83],v[36:39]
buffer_load_dwordx4 v5,s[24:27],s47 offen offset:1024 nt lds
v_mfma_f32_16x16x32_bf16 v[184:187],a[164:167],v[84:87],v[184:187]
v_exp_f32 v158,v158
v_mfma_f32_16x16x32_bf16 v[184:187],a[168:171],v[88:91],v[184:187]
v_exp_f32 v159,v159
v_mfma_f32_16x16x32_bf16 v[184:187],a[172:175],v[92:95],v[184:187]
v_exp_f32 v168,v168
v_mfma_f32_16x16x32_bf16 v[188:191],a[188:191],v[92:95],v[36:39]
v_exp_f32 v169,v169
v_mfma_f32_16x16x32_bf16 v[188:191],a[184:187],v[88:91],v[188:191]
v_exp_f32 v170,v170
v_mfma_f32_16x16x32_bf16 v[188:191],a[180:183],v[84:87],v[188:191]
v_exp_f32 v171,v171
v_mfma_f32_16x16x32_bf16 v[188:191],a[176:179],v[80:83],v[188:191]
v_exp_f32 v172,v172
v_mfma_f32_16x16x32_bf16 a[96:99],a[192:195],v[232:235],a[96:99]
ds_read_b64_tr_b16 a[192:193],v2
v_cvt_pk_bf16_f32 v168,v168,v169
v_mfma_f32_16x16x32_bf16 a[96:99],a[196:199],v[236:239],a[96:99]
ds_read_b64_tr_b16 a[194:195],v2 offset:4096
v_cvt_pk_bf16_f32 v169,v170,v171
v_mfma_f32_16x16x32_bf16 a[100:103],a[204:207],v[236:239],a[100:103]
v_exp_f32 v173,v173
v_mfma_f32_16x16x32_bf16 a[100:103],a[200:203],v[232:235],a[100:103]
ds_read_b64_tr_b16 a[196:197],v2 offset:8192
v_cvt_pk_bf16_f32 v170,v172,v173
v_mfma_f32_16x16x32_bf16 a[104:107],a[208:211],v[232:235],a[104:107]
v_exp_f32 v174,v174
v_mfma_f32_16x16x32_bf16 a[104:107],a[212:215],v[236:239],a[104:107]
v_exp_f32 v175,v175
v_mfma_f32_16x16x32_bf16 a[108:111],a[220:223],v[236:239],a[108:111]
buffer_load_dwordx4 v4,s[24:27],s47 offen offset:2048 nt lds
v_mfma_f32_16x16x32_bf16 a[108:111],a[216:219],v[232:235],a[108:111]
ds_read_b64_tr_b16 a[198:199],v2 offset:12288
v_cvt_pk_bf16_f32 v171,v174,v175
v_mfma_f32_16x16x32_bf16 a[112:115],a[224:227],v[232:235],a[112:115]
v_exp_f32 v184,v184
v_mfma_f32_16x16x32_bf16 a[112:115],a[228:231],v[236:239],a[112:115]
v_exp_f32 v185,v185
v_mfma_f32_16x16x32_bf16 a[116:119],a[236:239],v[236:239],a[116:119]
ds_read_b64_tr_b16 a[204:205],v3 offset:8192
v_cvt_pk_bf16_f32 v136,v136,v137
v_mfma_f32_16x16x32_bf16 a[116:119],a[232:235],v[232:235],a[116:119]
ds_read_b64_tr_b16 a[206:207],v3 offset:12288
v_cvt_pk_bf16_f32 v172,v184,v185
v_mfma_f32_16x16x32_bf16 a[120:123],a[240:243],v[232:235],a[120:123]
ds_read_b64_tr_b16 a[200:201],v3
v_mfma_f32_16x16x32_bf16 a[120:123],a[244:247],v[236:239],a[120:123]
ds_read_b64_tr_b16 a[202:203],v3 offset:4096
v_cvt_pk_bf16_f32 v137,v138,v139
v_mfma_f32_16x16x32_bf16 a[124:127],a[252:255],v[236:239],a[124:127]
ds_read_b64_tr_b16 a[208:209],v2 offset:256
v_mfma_f32_16x16x32_bf16 a[124:127],a[248:251],v[232:235],a[124:127]
ds_read_b64_tr_b16 a[210:211],v2 offset:4352
v_smfmac_f32_16x16x64_bf16 v[60:63],v[28:31],v[232:239],v27
ds_read_b64_tr_b16 a[212:213],v2 offset:8448
v_mfma_f32_16x16x32_bf16 v[200:203],a[128:131],v[96:99],v[40:43]
ds_read_b64_tr_b16 a[214:215],v2 offset:12544
v_cvt_pk_bf16_f32 v138,v140,v141
v_mfma_f32_16x16x32_bf16 v[200:203],a[132:135],v[100:103],v[200:203]
ds_read_b64_tr_b16 a[220:221],v3 offset:8448
v_mfma_f32_16x16x32_bf16 v[200:203],a[136:139],v[104:107],v[200:203]
ds_read_b64_tr_b16 a[222:223],v3 offset:12544
v_cvt_pk_bf16_f32 v139,v142,v143
v_mfma_f32_16x16x32_bf16 v[200:203],a[140:143],v[108:111],v[200:203]
buffer_load_dwordx4 v5,s[24:27],s47 offen offset:3072 nt lds
v_mfma_f32_16x16x32_bf16 v[204:207],a[156:159],v[108:111],v[40:43]
s_add_u32 s47,s47,16384
s_and_b32 s47,s47,0x3ffff
ds_read_b64_tr_b16 a[216:217],v3 offset:256
v_mfma_f32_16x16x32_bf16 v[204:207],a[152:155],v[104:107],v[204:207]
s_or_b32 s47,s47,s46
ds_read_b64_tr_b16 a[218:219],v3 offset:4352
v_cvt_pk_bf16_f32 v140,v152,v153
v_mfma_f32_16x16x32_bf16 v[204:207],a[148:151],v[100:103],v[204:207]
ds_read_b64_tr_b16 a[224:225],v2 offset:512
v_mfma_f32_16x16x32_bf16 v[204:207],a[144:147],v[96:99],v[204:207]
ds_read_b64_tr_b16 a[226:227],v2 offset:4608
v_mfma_f32_16x16x32_bf16 v[216:219],a[160:163],v[96:99],v[40:43]
ds_read_b64_tr_b16 a[228:229],v2 offset:8704
v_cvt_pk_bf16_f32 v141,v154,v155
v_mfma_f32_16x16x32_bf16 v[216:219],a[164:167],v[100:103],v[216:219]
ds_read_b64_tr_b16 a[230:231],v2 offset:12800
v_mfma_f32_16x16x32_bf16 v[216:219],a[168:171],v[104:107],v[216:219]
ds_read_b64_tr_b16 a[236:237],v3 offset:8704
v_mfma_f32_16x16x32_bf16 v[216:219],a[172:175],v[108:111],v[216:219]
ds_read_b64_tr_b16 a[238:239],v3 offset:12800
v_cvt_pk_bf16_f32 v142,v156,v157
v_mfma_f32_16x16x32_bf16 v[220:223],a[188:191],v[108:111],v[40:43]
ds_read_b64_tr_b16 a[232:233],v3 offset:512
v_mfma_f32_16x16x32_bf16 v[220:223],a[184:187],v[104:107],v[220:223]
ds_read_b64_tr_b16 a[234:235],v3 offset:4608
v_mfma_f32_16x16x32_bf16 v[220:223],a[180:183],v[100:103],v[220:223]
ds_read_b64_tr_b16 a[240:241],v2 offset:768
v_cvt_pk_bf16_f32 v143,v158,v159
v_mfma_f32_16x16x32_bf16 v[220:223],a[176:179],v[96:99],v[220:223]
s_waitcnt lgkmcnt(7)
buffer_load_dword v26,v24,s[88:91],s93 offen
v_mfma_f32_16x16x32_bf16 a[0:3],a[192:195],v[136:139],a[0:3]
ds_read_b64_tr_b16 a[242:243],v2 offset:4864
v_mfma_f32_16x16x32_bf16 a[0:3],a[196:199],v[140:143],a[0:3]
ds_read_b64_tr_b16 a[244:245],v2 offset:8960
v_mfma_f32_16x16x32_bf16 a[4:7],a[204:207],v[140:143],a[4:7]
ds_read_b64_tr_b16 a[246:247],v2 offset:13056
v_mfma_f32_16x16x32_bf16 a[4:7],a[200:203],v[136:139],a[4:7]
s_memrealtime s[76:77]
ds_read_b64_tr_b16 a[252:253],v3 offset:8960
v_mfma_f32_16x16x32_bf16 a[8:11],a[208:211],v[136:139],a[8:11]
ds_read_b64_tr_b16 a[254:255],v3 offset:13056
v_mfma_f32_16x16x32_bf16 a[8:11],a[212:215],v[140:143],a[8:11]
ds_read_b64_tr_b16 a[248:249],v3 offset:768
v_mfma_f32_16x16x32_bf16 a[12:15],a[220:223],v[140:143],a[12:15]
ds_read_b64_tr_b16 a[250:251],v3 offset:4864
v_mfma_f32_16x16x32_bf16 a[12:15],a[216:219],v[136:139],a[12:15]
s_bitcmp1_b32 s45,0
s_cbranch_scc0 L_t310_%=
s_mov_b32 m0,s80
s_add_u32 s80,s80,4096
buffer_load_dwordx4 v25,s[20:23],s83 offen nt lds
s_add_u32 s83,s83,4096
L_t310_%=:
v_mfma_f32_16x16x32_bf16 a[16:19],a[224:227],v[136:139],a[16:19]
s_waitcnt lgkmcnt(6)
v_exp_f32 v186,v186
v_mfma_f32_16x16x32_bf16 a[16:19],a[228:231],v[140:143],a[16:19]
v_exp_f32 v187,v187
v_mfma_f32_16x16x32_bf16 a[20:23],a[236:239],v[140:143],a[20:23]
s_waitcnt lgkmcnt(0)
v_exp_f32 v188,v188
v_mfma_f32_16x16x32_bf16 a[20:23],a[232:235],v[136:139],a[20:23]
s_sub_u32 s79,s78,s76
v_exp_f32 v189,v189
v_mfma_f32_16x16x32_bf16 a[24:27],a[240:243],v[136:139],a[24:27]
s_add_u32 s78,s76,s60
v_exp_f32 v190,v190
v_mfma_f32_16x16x32_bf16 a[24:27],a[244:247],v[140:143],a[24:27]
s_cmp_gt_i32 s79,s95
s_cbranch_scc1 L_ps3_%=
L_pr3_%=:
v_cvt_pk_bf16_f32 v173,v186,v187
v_mfma_f32_16x16x32_bf16 a[28:31],a[252:255],v[140:143],a[28:31]
v_exp_f32 v191,v191
v_mfma_f32_16x16x32_bf16 a[28:31],a[248:251],v[136:139],a[28:31]
v_cvt_pk_bf16_f32 v174,v188,v189
v_cvt_pk_bf16_f32 v175,v190,v191
s_cmp_lg_u32 s53,0
v_smfmac_f32_16x16x64_bf16 v[48:51],v[28:31],v[136:143],v27
s_cbranch_scc1 L_att0_%=
L_att0x_%=:
v_mfma_f32_16x16x32_bf16 v[232:235],a[128:131],v[112:115],v[44:47]
s_waitcnt vmcnt(10)
s_sub_u32 s94,s94,3
v_exp_f32 v200,v200
v_mfma_f32_16x16x32_bf16 v[232:235],a[132:135],v[116:119],v[232:235]
s_barrier
s_add_u32 m0,0,s51
ds_read_b128 a[128:131],v1
v_mfma_f32_16x16x32_bf16 v[232:235],a[136:139],v[120:123],v[232:235]
s_lshr_b32 s60,s94,2
v_exp_f32 v201,v201
v_mfma_f32_16x16x32_bf16 v[232:235],a[140:143],v[124:127],v[232:235]
s_max_i32 s60,s60,128
ds_read_b128 a[132:135],v1 offset:256
v_cvt_pk_bf16_f32 v200,v200,v201
v_mfma_f32_16x16x32_bf16 v[236:239],a[156:159],v[124:127],v[44:47]
s_add_u32 s54,s54,16384
buffer_load_dwordx4 v4,s[20:23],s47 offen nt lds
v_mfma_f32_16x16x32_bf16 v[236:239],a[152:155],v[120:123],v[236:239]
s_and_b32 s46,s54,0xfffc0000
ds_read_b128 a[136:139],v1 offset:512
v_and_b32 v18,48,v6
v_mfma_f32_16x16x32_bf16 v[236:239],a[148:151],v[116:119],v[236:239]
s_sub_u32 s94,s94,3
v_exp_f32 v202,v202
v_mfma_f32_16x16x32_bf16 v[236:239],a[144:147],v[112:115],v[236:239]
s_add_u32 s54,s54,16384
ds_read_b128 a[140:143],v1 offset:768
v_lshrrev_b32 v19,4,v6
v_mfma_f32_16x16x32_bf16 v[248:251],a[160:163],v[112:115],v[44:47]
s_mul_i32 s83,s41,5
v_exp_f32 v203,v203
v_mfma_f32_16x16x32_bf16 v[248:251],a[164:167],v[116:119],v[248:251]
s_lshl_b32 s44,s40,5
ds_read_b128 a[156:159],v1 offset:4864
v_cvt_pk_bf16_f32 v201,v202,v203
v_mfma_f32_16x16x32_bf16 v[248:251],a[168:171],v[120:123],v[248:251]
s_add_u32 s83,s83,s42
v_exp_f32 v204,v204
v_mfma_f32_16x16x32_bf16 v[248:251],a[172:175],v[124:127],v[248:251]
s_add_u32 s84,s55,0
)A"
R"A(ds_read_b128 a[152:155],v1 offset:4608
v_lshlrev_b32 v18,2,v18
v_mfma_f32_16x16x32_bf16 v[252:255],a[188:191],v[124:127],v[44:47]
s_add_u32 s85,s55,4096
v_exp_f32 v205,v205
v_mfma_f32_16x16x32_bf16 v[252:255],a[184:187],v[120:123],v[252:255]
s_add_u32 s86,s55,8192
ds_read_b128 a[148:151],v1 offset:4352
v_cvt_pk_bf16_f32 v202,v204,v205
v_mfma_f32_16x16x32_bf16 v[252:255],a[180:183],v[116:119],v[252:255]
s_add_u32 s87,s55,12288
v_exp_f32 v206,v206
v_mfma_f32_16x16x32_bf16 v[252:255],a[176:179],v[112:115],v[252:255]
s_and_b32 s44,s44,32
ds_read_b128 a[144:147],v1 offset:4096
v_and_b32 v19,48,v19
v_mfma_f32_16x16x32_bf16 a[32:35],a[192:195],v[168:171],a[32:35]
s_add_u32 s83,s83,15
v_exp_f32 v207,v207
v_mfma_f32_16x16x32_bf16 a[32:35],a[196:199],v[172:175],a[32:35]
s_max_i32 s84,s84,s18
ds_read_b128 a[160:163],v1 offset:8192
v_cvt_pk_bf16_f32 v203,v206,v207
v_mfma_f32_16x16x32_bf16 a[36:39],a[204:207],v[172:175],a[36:39]
s_max_i32 s85,s85,s19
buffer_load_dwordx4 v4,s[20:23],s47 offen offset:1024 nt lds
v_mfma_f32_16x16x32_bf16 a[36:39],a[200:203],v[168:171],a[36:39]
s_max_i32 s86,s86,s100
ds_read_b128 a[164:167],v1 offset:8448
v_add_u32 v18,v18,v19
v_mfma_f32_16x16x32_bf16 a[40:43],a[208:211],v[168:171],a[40:43]
s_max_i32 s87,s87,s101
v_exp_f32 v216,v216
v_mfma_f32_16x16x32_bf16 a[40:43],a[212:215],v[172:175],a[40:43]
s_lshl_b32 s80,s40,10
ds_read_b128 a[168:171],v1 offset:8704
v_add_u32 v18,98304,v18
v_mfma_f32_16x16x32_bf16 a[44:47],a[220:223],v[172:175],a[44:47]
s_and_b32 s83,s83,15
v_exp_f32 v217,v217
v_mfma_f32_16x16x32_bf16 a[44:47],a[216:219],v[168:171],a[44:47]
s_add_u32 s83,s83,16
ds_read_b128 a[172:175],v1 offset:8960
v_cvt_pk_bf16_f32 v204,v216,v217
v_mfma_f32_16x16x32_bf16 a[48:51],a[224:227],v[168:171],a[48:51]
s_mov_b32 s50,32768
v_exp_f32 v218,v218
v_mfma_f32_16x16x32_bf16 a[48:51],a[228:231],v[172:175],a[48:51]
s_mov_b32 s49,98304
ds_read_b128 a[188:191],v1 offset:13056
v_add_u32 v2,s48,v2
v_mfma_f32_16x16x32_bf16 a[52:55],a[236:239],v[172:175],a[52:55]
s_mov_b32 s45,0x2a8
v_exp_f32 v219,v219
v_mfma_f32_16x16x32_bf16 a[52:55],a[232:235],v[168:171],a[52:55]
s_lshl_b32 s83,s83,14
ds_read_b128 a[184:187],v1 offset:12800
v_cvt_pk_bf16_f32 v205,v218,v219
v_mfma_f32_16x16x32_bf16 a[56:59],a[240:243],v[168:171],a[56:59]
s_add_u32 s93,s92,L_endseg_%=-L_pc_%=
v_exp_f32 v220,v220
v_mfma_f32_16x16x32_bf16 a[56:59],a[244:247],v[172:175],a[56:59]
ds_read_b128 a[180:183],v1 offset:12544
v_add_u32 v3,s48,v3
v_mfma_f32_16x16x32_bf16 a[60:63],a[252:255],v[172:175],a[60:63]
s_mov_b32 s48,32768
v_exp_f32 v221,v221
v_mfma_f32_16x16x32_bf16 a[60:63],a[248:251],v[168:171],a[60:63]
ds_read_b128 a[176:179],v1 offset:12288
v_cvt_pk_bf16_f32 v206,v220,v221
v_smfmac_f32_16x16x64_bf16 v[52:55],v[28:31],v[168:175],v27
s_waitcnt lgkmcnt(4)
v_add_u32 v1,s48,v1
v_mfma_f32_16x16x32_bf16 v[136:139],a[128:131],v[64:67],v[32:35]
buffer_load_dwordx4 v5,s[20:23],s47 offen offset:2048 nt lds
v_mfma_f32_16x16x32_bf16 v[136:139],a[132:135],v[68:71],v[136:139]
v_exp_f32 v222,v222
v_mfma_f32_16x16x32_bf16 v[136:139],a[136:139],v[72:75],v[136:139]
v_exp_f32 v223,v223
v_mfma_f32_16x16x32_bf16 v[136:139],a[140:143],v[76:79],v[136:139]
v_cvt_pk_bf16_f32 v207,v222,v223
v_subrev_u32 v25,s51,v4
v_mfma_f32_16x16x32_bf16 v[140:143],a[156:159],v[76:79],v[32:35]
v_exp_f32 v232,v232
v_mfma_f32_16x16x32_bf16 v[140:143],a[152:155],v[72:75],v[140:143]
v_exp_f32 v233,v233
v_mfma_f32_16x16x32_bf16 v[140:143],a[148:151],v[68:71],v[140:143]
v_exp_f32 v234,v234
v_mfma_f32_16x16x32_bf16 v[140:143],a[144:147],v[64:67],v[140:143]
v_exp_f32 v235,v235
v_mfma_f32_16x16x32_bf16 v[152:155],a[160:163],v[64:67],v[32:35]
v_cvt_pk_bf16_f32 v232,v232,v233
v_cvt_pk_bf16_f32 v233,v234,v235
v_mfma_f32_16x16x32_bf16 v[152:155],a[164:167],v[68:71],v[152:155]
v_exp_f32 v236,v236
v_mfma_f32_16x16x32_bf16 v[152:155],a[168:171],v[72:75],v[152:155]
v_exp_f32 v237,v237
v_mfma_f32_16x16x32_bf16 v[152:155],a[172:175],v[76:79],v[152:155]
s_waitcnt lgkmcnt(0)
v_exp_f32 v238,v238
v_mfma_f32_16x16x32_bf16 v[156:159],a[188:191],v[76:79],v[32:35]
v_exp_f32 v239,v239
v_mfma_f32_16x16x32_bf16 v[156:159],a[184:187],v[72:75],v[156:159]
buffer_load_dwordx4 v5,s[20:23],s47 offen offset:3072 nt lds
v_mfma_f32_16x16x32_bf16 v[156:159],a[180:183],v[68:71],v[156:159]
v_cvt_pk_bf16_f32 v234,v236,v237
v_cvt_pk_bf16_f32 v235,v238,v239
v_mfma_f32_16x16x32_bf16 v[156:159],a[176:179],v[64:67],v[156:159]
v_exp_f32 v248,v248
v_mfma_f32_16x16x32_bf16 a[64:67],a[192:195],v[200:203],a[64:67]
v_exp_f32 v249,v249
v_mfma_f32_16x16x32_bf16 a[64:67],a[196:199],v[204:207],a[64:67]
v_exp_f32 v250,v250
v_mfma_f32_16x16x32_bf16 a[68:71],a[204:207],v[204:207],a[68:71]
v_exp_f32 v251,v251
v_mfma_f32_16x16x32_bf16 a[68:71],a[200:203],v[200:203],a[68:71]
v_exp_f32 v252,v252
v_mfma_f32_16x16x32_bf16 a[72:75],a[208:211],v[200:203],a[72:75]
v_exp_f32 v253,v253
v_mfma_f32_16x16x32_bf16 a[72:75],a[212:215],v[204:207],a[72:75]
v_exp_f32 v254,v254
v_mfma_f32_16x16x32_bf16 a[76:79],a[220:223],v[204:207],a[76:79]
v_exp_f32 v255,v255
v_mfma_f32_16x16x32_bf16 a[76:79],a[216:219],v[200:203],a[76:79]
v_cvt_pk_bf16_f32 v236,v248,v249
v_cvt_pk_bf16_f32 v237,v250,v251
v_mfma_f32_16x16x32_bf16 a[80:83],a[224:227],v[200:203],a[80:83]
s_add_u32 m0,0,s52
v_cvt_pk_bf16_f32 v238,v252,v253
v_cvt_pk_bf16_f32 v239,v254,v255
v_mfma_f32_16x16x32_bf16 a[80:83],a[228:231],v[204:207],a[80:83]
v_exp_f32 v136,v136
v_mfma_f32_16x16x32_bf16 a[84:87],a[236:239],v[204:207],a[84:87]
v_exp_f32 v137,v137
v_mfma_f32_16x16x32_bf16 a[84:87],a[232:235],v[200:203],a[84:87]
buffer_load_dwordx4 v4,s[24:27],s47 offen nt lds
v_mfma_f32_16x16x32_bf16 a[88:91],a[240:243],v[200:203],a[88:91]
v_exp_f32 v138,v138
v_mfma_f32_16x16x32_bf16 a[88:91],a[244:247],v[204:207],a[88:91]
v_exp_f32 v139,v139
v_mfma_f32_16x16x32_bf16 a[92:95],a[252:255],v[204:207],a[92:95]
v_cvt_pk_bf16_f32 v136,v136,v137
v_cvt_pk_bf16_f32 v137,v138,v139
v_mfma_f32_16x16x32_bf16 a[92:95],a[248:251],v[200:203],a[92:95]
v_exp_f32 v140,v140
v_smfmac_f32_16x16x64_bf16 v[56:59],v[28:31],v[200:207],v27
v_xor_b32 v25,s44,v25
v_mfma_f32_16x16x32_bf16 v[168:171],a[128:131],v[80:83],v[36:39]
v_exp_f32 v141,v141
v_mfma_f32_16x16x32_bf16 v[168:171],a[132:135],v[84:87],v[168:171]
v_exp_f32 v142,v142
v_mfma_f32_16x16x32_bf16 v[168:171],a[136:139],v[88:91],v[168:171]
v_exp_f32 v143,v143
v_mfma_f32_16x16x32_bf16 v[168:171],a[140:143],v[92:95],v[168:171]
v_cvt_pk_bf16_f32 v138,v140,v141
v_cvt_pk_bf16_f32 v139,v142,v143
v_mfma_f32_16x16x32_bf16 v[172:175],a[156:159],v[92:95],v[36:39]
v_exp_f32 v152,v152
v_mfma_f32_16x16x32_bf16 v[172:175],a[152:155],v[88:91],v[172:175]
v_exp_f32 v153,v153
v_mfma_f32_16x16x32_bf16 v[172:175],a[148:151],v[84:87],v[172:175]
v_exp_f32 v154,v154
v_mfma_f32_16x16x32_bf16 v[172:175],a[144:147],v[80:83],v[172:175]
v_exp_f32 v155,v155
v_mfma_f32_16x16x32_bf16 v[184:187],a[160:163],v[80:83],v[36:39]
buffer_load_dwordx4 v5,s[24:27],s47 offen offset:1024 nt lds
v_mfma_f32_16x16x32_bf16 v[184:187],a[164:167],v[84:87],v[184:187]
v_exp_f32 v156,v156
v_mfma_f32_16x16x32_bf16 v[184:187],a[168:171],v[88:91],v[184:187]
v_exp_f32 v157,v157
v_mfma_f32_16x16x32_bf16 v[184:187],a[172:175],v[92:95],v[184:187]
v_exp_f32 v158,v158
v_mfma_f32_16x16x32_bf16 v[188:191],a[188:191],v[92:95],v[36:39]
v_exp_f32 v159,v159
v_mfma_f32_16x16x32_bf16 v[188:191],a[184:187],v[88:91],v[188:191]
v_cvt_pk_bf16_f32 v140,v152,v153
v_cvt_pk_bf16_f32 v141,v154,v155
v_mfma_f32_16x16x32_bf16 v[188:191],a[180:183],v[84:87],v[188:191]
s_waitcnt vmcnt(14)
v_cvt_pk_bf16_f32 v142,v156,v157
v_cvt_pk_bf16_f32 v143,v158,v159
v_mfma_f32_16x16x32_bf16 v[188:191],a[176:179],v[80:83],v[188:191]
s_barrier
ds_read_b128 v[80:83],v18
v_add_u32 v25,s80,v25
v_mfma_f32_16x16x32_bf16 a[96:99],a[192:195],v[232:235],a[96:99]
s_add_u32 s80,s80,147456
v_exp_f32 v168,v168
v_mfma_f32_16x16x32_bf16 a[96:99],a[196:199],v[236:239],a[96:99]
v_exp_f32 v169,v169
v_mfma_f32_16x16x32_bf16 a[100:103],a[204:207],v[236:239],a[100:103]
ds_read_b128 v[84:87],v18 offset:256
v_cvt_pk_bf16_f32 v168,v168,v169
v_mfma_f32_16x16x32_bf16 a[100:103],a[200:203],v[232:235],a[100:103]
v_exp_f32 v170,v170
v_mfma_f32_16x16x32_bf16 a[104:107],a[208:211],v[232:235],a[104:107]
v_exp_f32 v171,v171
v_mfma_f32_16x16x32_bf16 a[104:107],a[212:215],v[236:239],a[104:107]
buffer_load_dwordx4 v4,s[24:27],s47 offen offset:2048 nt lds
v_mfma_f32_16x16x32_bf16 a[108:111],a[220:223],v[236:239],a[108:111]
ds_read_b128 v[88:91],v18 offset:512
v_cvt_pk_bf16_f32 v169,v170,v171
v_mfma_f32_16x16x32_bf16 a[108:111],a[216:219],v[232:235],a[108:111]
v_exp_f32 v172,v172
v_mfma_f32_16x16x32_bf16 a[112:115],a[224:227],v[232:235],a[112:115]
v_exp_f32 v173,v173
v_mfma_f32_16x16x32_bf16 a[112:115],a[228:231],v[236:239],a[112:115]
ds_read_b128 v[92:95],v18 offset:768
v_cvt_pk_bf16_f32 v170,v172,v173
v_mfma_f32_16x16x32_bf16 a[116:119],a[236:239],v[236:239],a[116:119]
v_exp_f32 v174,v174
v_mfma_f32_16x16x32_bf16 a[116:119],a[232:235],v[232:235],a[116:119]
ds_read_b64_tr_b16 a[192:193],v2
ds_read_b64_tr_b16 a[194:195],v2 offset:4096
v_mfma_f32_16x16x32_bf16 a[120:123],a[240:243],v[232:235],a[120:123]
v_exp_f32 v175,v175
v_mfma_f32_16x16x32_bf16 a[120:123],a[244:247],v[236:239],a[120:123]
ds_read_b64_tr_b16 a[196:197],v2 offset:8192
ds_read_b64_tr_b16 a[198:199],v2 offset:12288
v_mfma_f32_16x16x32_bf16 a[124:127],a[252:255],v[236:239],a[124:127]
ds_read_b64_tr_b16 a[204:205],v3 offset:8192
v_cvt_pk_bf16_f32 v171,v174,v175
v_mfma_f32_16x16x32_bf16 a[124:127],a[248:251],v[232:235],a[124:127]
v_exp_f32 v184,v184
v_smfmac_f32_16x16x64_bf16 v[60:63],v[28:31],v[232:239],v27
ds_read_b64_tr_b16 a[206:207],v3 offset:12288
v_mfma_f32_16x16x32_bf16 v[200:203],a[128:131],v[96:99],v[40:43]
ds_read_b64_tr_b16 a[200:201],v3
ds_read_b64_tr_b16 a[202:203],v3 offset:4096
v_mfma_f32_16x16x32_bf16 v[200:203],a[132:135],v[100:103],v[200:203]
v_exp_f32 v185,v185
v_mfma_f32_16x16x32_bf16 v[200:203],a[136:139],v[104:107],v[200:203]
buffer_load_dwordx4 v5,s[24:27],s47 offen offset:3072 nt lds
v_mfma_f32_16x16x32_bf16 v[200:203],a[140:143],v[108:111],v[200:203]
s_add_u32 s47,s47,16384
ds_read_b64_tr_b16 a[208:209],v2 offset:256
ds_read_b64_tr_b16 a[210:211],v2 offset:4352
v_mfma_f32_16x16x32_bf16 v[204:207],a[156:159],v[108:111],v[40:43]
s_and_b32 s47,s47,0x3ffff
s_or_b32 s47,s47,s46
ds_read_b64_tr_b16 a[212:213],v2 offset:8448
v_mfma_f32_16x16x32_bf16 v[204:207],a[152:155],v[104:107],v[204:207]
s_and_b32 s46,s54,0xfffc0000
ds_read_b64_tr_b16 a[214:215],v2 offset:12544
v_cvt_pk_bf16_f32 v172,v184,v185
v_mfma_f32_16x16x32_bf16 v[204:207],a[148:151],v[100:103],v[204:207]
s_add_u32 s54,s54,16384
v_exp_f32 v186,v186
v_mfma_f32_16x16x32_bf16 v[204:207],a[144:147],v[96:99],v[204:207]
ds_read_b64_tr_b16 a[220:221],v3 offset:8448
ds_read_b64_tr_b16 a[222:223],v3 offset:12544
v_mfma_f32_16x16x32_bf16 v[216:219],a[160:163],v[96:99],v[40:43]
v_exp_f32 v187,v187
v_mfma_f32_16x16x32_bf16 v[216:219],a[164:167],v[100:103],v[216:219]
ds_read_b64_tr_b16 a[216:217],v3 offset:256
ds_read_b64_tr_b16 a[218:219],v3 offset:4352
v_mfma_f32_16x16x32_bf16 v[216:219],a[168:171],v[104:107],v[216:219]
ds_read_b64_tr_b16 a[224:225],v2 offset:512
v_cvt_pk_bf16_f32 v173,v186,v187
v_mfma_f32_16x16x32_bf16 v[216:219],a[172:175],v[108:111],v[216:219]
ds_read_b64_tr_b16 a[226:227],v2 offset:4608
v_mfma_f32_16x16x32_bf16 v[220:223],a[188:191],v[108:111],v[40:43]
ds_read_b64_tr_b16 a[228:229],v2 offset:8704
v_mfma_f32_16x16x32_bf16 v[220:223],a[184:187],v[104:107],v[220:223]
ds_read_b64_tr_b16 a[230:231],v2 offset:12800
ds_read_b64_tr_b16 a[236:237],v3 offset:8704
v_mfma_f32_16x16x32_bf16 v[220:223],a[180:183],v[100:103],v[220:223]
ds_read_b64_tr_b16 a[238:239],v3 offset:12800
v_mfma_f32_16x16x32_bf16 v[220:223],a[176:179],v[96:99],v[220:223]
s_waitcnt lgkmcnt(8)
ds_read_b64_tr_b16 a[232:233],v3 offset:512
v_mfma_f32_16x16x32_bf16 v[32:35],v[80:83],v[128:131],0
ds_read_b64_tr_b16 a[234:235],v3 offset:4608
v_mfma_f32_16x16x32_bf16 v[32:35],v[84:87],v[160:163],v[32:35]
ds_read_b64_tr_b16 a[240:241],v2 offset:768
ds_read_b64_tr_b16 a[242:243],v2 offset:4864
)A"
R"A(v_mfma_f32_16x16x32_bf16 v[32:35],v[88:91],v[192:195],v[32:35]
ds_read_b64_tr_b16 a[244:245],v2 offset:8960
v_mfma_f32_16x16x32_bf16 v[32:35],v[92:95],v[224:227],v[32:35]
ds_read_b64_tr_b16 a[246:247],v2 offset:13056
v_add_u32 v2,s48,v2
v_mfma_f32_16x16x32_bf16 v[36:39],v[80:83],v[132:135],0
ds_read_b64_tr_b16 a[252:253],v3 offset:8960
v_mfma_f32_16x16x32_bf16 v[36:39],v[84:87],v[164:167],v[36:39]
ds_read_b64_tr_b16 a[254:255],v3 offset:13056
ds_read_b64_tr_b16 a[248:249],v3 offset:768
v_mfma_f32_16x16x32_bf16 v[36:39],v[88:91],v[196:199],v[36:39]
ds_read_b64_tr_b16 a[250:251],v3 offset:4864
v_add_u32 v3,s48,v3
v_mfma_f32_16x16x32_bf16 v[36:39],v[92:95],v[228:231],v[36:39]
s_mov_b32 s48,-98304
v_exp_f32 v188,v188
v_mfma_f32_16x16x32_bf16 v[40:43],v[80:83],v[144:147],0
v_exp_f32 v189,v189
v_mfma_f32_16x16x32_bf16 v[40:43],v[84:87],v[176:179],v[40:43]
v_exp_f32 v190,v190
v_mfma_f32_16x16x32_bf16 v[40:43],v[88:91],v[208:211],v[40:43]
v_exp_f32 v191,v191
v_mfma_f32_16x16x32_bf16 v[40:43],v[92:95],v[240:243],v[40:43]
v_cvt_pk_bf16_f32 v174,v188,v189
v_cvt_pk_bf16_f32 v175,v190,v191
v_mfma_f32_16x16x32_bf16 a[0:3],a[192:195],v[136:139],a[0:3]
v_exp_f32 v200,v200
v_mfma_f32_16x16x32_bf16 a[0:3],a[196:199],v[140:143],a[0:3]
v_max3_f32 v32,v32,v33,v34
v_max3_f32 v32,v32,v35,s18
v_mfma_f32_16x16x32_bf16 a[4:7],a[204:207],v[140:143],a[4:7]
v_sub_f32 v35,0,v32
v_sub_f32 v34,0,v32
v_mfma_f32_16x16x32_bf16 a[4:7],a[200:203],v[136:139],a[4:7]
s_memrealtime s[76:77]
v_sub_f32 v33,0,v32
v_sub_f32 v32,0,v32
v_mfma_f32_16x16x32_bf16 a[8:11],a[208:211],v[136:139],a[8:11]
v_exp_f32 v201,v201
v_mfma_f32_16x16x32_bf16 a[8:11],a[212:215],v[140:143],a[8:11]
v_exp_f32 v202,v202
v_mfma_f32_16x16x32_bf16 a[12:15],a[220:223],v[140:143],a[12:15]
s_waitcnt lgkmcnt(0)
v_exp_f32 v203,v203
v_mfma_f32_16x16x32_bf16 a[12:15],a[216:219],v[136:139],a[12:15]
v_cvt_pk_bf16_f32 v200,v200,v201
v_cvt_pk_bf16_f32 v201,v202,v203
v_mfma_f32_16x16x32_bf16 a[16:19],a[224:227],v[136:139],a[16:19]
v_exp_f32 v204,v204
v_mfma_f32_16x16x32_bf16 a[16:19],a[228:231],v[140:143],a[16:19]
v_exp_f32 v205,v205
v_mfma_f32_16x16x32_bf16 a[20:23],a[236:239],v[140:143],a[20:23]
s_waitcnt lgkmcnt(0)
v_exp_f32 v206,v206
v_mfma_f32_16x16x32_bf16 a[20:23],a[232:235],v[136:139],a[20:23]
s_sub_u32 s79,s78,s76
v_exp_f32 v207,v207
v_mfma_f32_16x16x32_bf16 a[24:27],a[240:243],v[136:139],a[24:27]
s_add_u32 s78,s76,s60
v_cvt_pk_bf16_f32 v202,v204,v205
v_cvt_pk_bf16_f32 v203,v206,v207
v_mfma_f32_16x16x32_bf16 a[24:27],a[244:247],v[140:143],a[24:27]
s_cmp_gt_i32 s79,s95
s_cbranch_scc1 L_ps4_%=
L_pr4_%=:
v_max3_f32 v36,v36,v37,v38
v_mfma_f32_16x16x32_bf16 a[28:31],a[252:255],v[140:143],a[28:31]
s_lshr_b32 s60,s94,2
v_exp_f32 v216,v216
v_mfma_f32_16x16x32_bf16 a[28:31],a[248:251],v[136:139],a[28:31]
s_max_i32 s60,s60,128
v_exp_f32 v217,v217
v_smfmac_f32_16x16x64_bf16 v[48:51],v[28:31],v[136:143],v27
s_sub_u32 s94,s94,3
v_cvt_pk_bf16_f32 v204,v216,v217
v_mfma_f32_16x16x32_bf16 v[232:235],a[128:131],v[112:115],v[44:47]
s_waitcnt vmcnt(9)
v_exp_f32 v218,v218
v_mfma_f32_16x16x32_bf16 v[232:235],a[132:135],v[116:119],v[232:235]
s_barrier
s_add_u32 m0,32768,s51
ds_read_b128 a[128:131],v1
v_mfma_f32_16x16x32_bf16 v[232:235],a[136:139],v[120:123],v[232:235]
v_exp_f32 v219,v219
v_mfma_f32_16x16x32_bf16 v[232:235],a[140:143],v[124:127],v[232:235]
ds_read_b128 a[132:135],v1 offset:256
v_cvt_pk_bf16_f32 v205,v218,v219
v_mfma_f32_16x16x32_bf16 v[236:239],a[156:159],v[124:127],v[44:47]
buffer_load_dwordx4 v4,s[20:23],s47 offen nt lds
v_mfma_f32_16x16x32_bf16 v[236:239],a[152:155],v[120:123],v[236:239]
ds_read_b128 a[136:139],v1 offset:512
v_max3_f32 v36,v36,v39,s19
v_mfma_f32_16x16x32_bf16 v[236:239],a[148:151],v[116:119],v[236:239]
v_exp_f32 v220,v220
v_mfma_f32_16x16x32_bf16 v[236:239],a[144:147],v[112:115],v[236:239]
ds_read_b128 a[140:143],v1 offset:768
v_sub_f32 v39,0,v36
v_mfma_f32_16x16x32_bf16 v[248:251],a[160:163],v[112:115],v[44:47]
v_exp_f32 v221,v221
v_mfma_f32_16x16x32_bf16 v[248:251],a[164:167],v[116:119],v[248:251]
ds_read_b128 a[156:159],v1 offset:4864
v_cvt_pk_bf16_f32 v206,v220,v221
v_mfma_f32_16x16x32_bf16 v[248:251],a[168:171],v[120:123],v[248:251]
v_exp_f32 v222,v222
v_mfma_f32_16x16x32_bf16 v[248:251],a[172:175],v[124:127],v[248:251]
ds_read_b128 a[152:155],v1 offset:4608
v_sub_f32 v38,0,v36
v_mfma_f32_16x16x32_bf16 v[252:255],a[188:191],v[124:127],v[44:47]
v_exp_f32 v223,v223
v_mfma_f32_16x16x32_bf16 v[252:255],a[184:187],v[120:123],v[252:255]
ds_read_b128 a[148:151],v1 offset:4352
v_cvt_pk_bf16_f32 v207,v222,v223
v_mfma_f32_16x16x32_bf16 v[252:255],a[180:183],v[116:119],v[252:255]
v_sub_f32 v37,0,v36
v_sub_f32 v36,0,v36
v_mfma_f32_16x16x32_bf16 v[252:255],a[176:179],v[112:115],v[252:255]
ds_read_b128 a[144:147],v1 offset:4096
v_max3_f32 v40,v40,v41,v42
v_mfma_f32_16x16x32_bf16 v[44:47],v[80:83],v[148:151],0
v_exp_f32 v232,v232
v_mfma_f32_16x16x32_bf16 v[44:47],v[84:87],v[180:183],v[44:47]
ds_read_b128 a[160:163],v1 offset:8192
v_max3_f32 v40,v40,v43,s100
v_mfma_f32_16x16x32_bf16 v[44:47],v[88:91],v[212:215],v[44:47]
buffer_load_dwordx4 v4,s[20:23],s47 offen offset:1024 nt lds
v_mfma_f32_16x16x32_bf16 v[44:47],v[92:95],v[244:247],v[44:47]
ds_read_b128 a[164:167],v1 offset:8448
v_sub_f32 v43,0,v40
v_mfma_f32_16x16x32_bf16 a[32:35],a[192:195],v[168:171],a[32:35]
v_exp_f32 v233,v233
v_mfma_f32_16x16x32_bf16 a[32:35],a[196:199],v[172:175],a[32:35]
ds_read_b128 a[168:171],v1 offset:8704
v_cvt_pk_bf16_f32 v232,v232,v233
v_mfma_f32_16x16x32_bf16 a[36:39],a[204:207],v[172:175],a[36:39]
v_exp_f32 v234,v234
v_mfma_f32_16x16x32_bf16 a[36:39],a[200:203],v[168:171],a[36:39]
ds_read_b128 a[172:175],v1 offset:8960
v_sub_f32 v42,0,v40
v_mfma_f32_16x16x32_bf16 a[40:43],a[208:211],v[168:171],a[40:43]
v_exp_f32 v235,v235
v_mfma_f32_16x16x32_bf16 a[40:43],a[212:215],v[172:175],a[40:43]
ds_read_b128 a[188:191],v1 offset:13056
v_cvt_pk_bf16_f32 v233,v234,v235
v_mfma_f32_16x16x32_bf16 a[44:47],a[220:223],v[172:175],a[44:47]
v_exp_f32 v236,v236
v_mfma_f32_16x16x32_bf16 a[44:47],a[216:219],v[168:171],a[44:47]
ds_read_b128 a[184:187],v1 offset:12800
v_sub_f32 v41,0,v40
v_mfma_f32_16x16x32_bf16 a[48:51],a[224:227],v[168:171],a[48:51]
v_exp_f32 v237,v237
v_mfma_f32_16x16x32_bf16 a[48:51],a[228:231],v[172:175],a[48:51]
ds_read_b128 a[180:183],v1 offset:12544
v_cvt_pk_bf16_f32 v234,v236,v237
v_mfma_f32_16x16x32_bf16 a[52:55],a[236:239],v[172:175],a[52:55]
v_exp_f32 v238,v238
v_mfma_f32_16x16x32_bf16 a[52:55],a[232:235],v[168:171],a[52:55]
ds_read_b128 a[176:179],v1 offset:12288
v_sub_f32 v40,0,v40
v_mfma_f32_16x16x32_bf16 a[56:59],a[240:243],v[168:171],a[56:59]
buffer_load_dwordx4 v5,s[20:23],s47 offen offset:2048 nt lds
v_mfma_f32_16x16x32_bf16 a[56:59],a[244:247],v[172:175],a[56:59]
v_exp_f32 v239,v239
v_mfma_f32_16x16x32_bf16 a[60:63],a[252:255],v[172:175],a[60:63]
v_cvt_pk_bf16_f32 v235,v238,v239
v_add_f32 v48,v49,v48
v_mfma_f32_16x16x32_bf16 a[60:63],a[248:251],v[168:171],a[60:63]
v_exp_f32 v248,v248
v_smfmac_f32_16x16x64_bf16 v[52:55],v[28:31],v[168:175],v27
s_waitcnt lgkmcnt(2)
v_max_f32 v20,0x00800000,v48
v_mfma_f32_16x16x32_bf16 v[136:139],a[128:131],v[128:131],v[32:35]
v_exp_f32 v249,v249
v_mfma_f32_16x16x32_bf16 v[136:139],a[132:135],v[160:163],v[136:139]
v_exp_f32 v250,v250
v_mfma_f32_16x16x32_bf16 v[136:139],a[136:139],v[192:195],v[136:139]
v_exp_f32 v251,v251
v_mfma_f32_16x16x32_bf16 v[136:139],a[140:143],v[224:227],v[136:139]
v_exp_f32 v252,v252
v_mfma_f32_16x16x32_bf16 v[140:143],a[156:159],v[224:227],v[32:35]
v_exp_f32 v253,v253
v_mfma_f32_16x16x32_bf16 v[140:143],a[152:155],v[192:195],v[140:143]
v_exp_f32 v254,v254
v_mfma_f32_16x16x32_bf16 v[140:143],a[148:151],v[160:163],v[140:143]
v_exp_f32 v255,v255
v_mfma_f32_16x16x32_bf16 v[140:143],a[144:147],v[128:131],v[140:143]
v_cvt_pk_bf16_f32 v236,v248,v249
v_cvt_pk_bf16_f32 v237,v250,v251
v_mfma_f32_16x16x32_bf16 v[152:155],a[160:163],v[128:131],v[32:35]
v_cvt_pk_bf16_f32 v238,v252,v253
v_cvt_pk_bf16_f32 v239,v254,v255
v_mfma_f32_16x16x32_bf16 v[152:155],a[164:167],v[160:163],v[152:155]
buffer_load_dwordx4 v5,s[20:23],s47 offen offset:3072 nt lds
v_mfma_f32_16x16x32_bf16 v[152:155],a[168:171],v[192:195],v[152:155]
v_exp_f32 v136,v136
v_mfma_f32_16x16x32_bf16 v[152:155],a[172:175],v[224:227],v[152:155]
v_exp_f32 v137,v137
v_mfma_f32_16x16x32_bf16 v[156:159],a[188:191],v[224:227],v[32:35]
v_exp_f32 v138,v138
v_mfma_f32_16x16x32_bf16 v[156:159],a[184:187],v[192:195],v[156:159]
s_waitcnt lgkmcnt(0)
v_exp_f32 v139,v139
v_mfma_f32_16x16x32_bf16 v[156:159],a[180:183],v[160:163],v[156:159]
v_cvt_pk_bf16_f32 v136,v136,v137
v_cvt_pk_bf16_f32 v137,v138,v139
v_mfma_f32_16x16x32_bf16 v[156:159],a[176:179],v[128:131],v[156:159]
v_exp_f32 v140,v140
v_mfma_f32_16x16x32_bf16 a[64:67],a[192:195],v[200:203],a[64:67]
v_exp_f32 v141,v141
v_mfma_f32_16x16x32_bf16 a[64:67],a[196:199],v[204:207],a[64:67]
v_exp_f32 v142,v142
v_mfma_f32_16x16x32_bf16 a[68:71],a[204:207],v[204:207],a[68:71]
v_exp_f32 v143,v143
v_mfma_f32_16x16x32_bf16 a[68:71],a[200:203],v[200:203],a[68:71]
v_cvt_pk_bf16_f32 v138,v140,v141
v_cvt_pk_bf16_f32 v139,v142,v143
v_mfma_f32_16x16x32_bf16 a[72:75],a[208:211],v[200:203],a[72:75]
s_add_u32 m0,32768,s52
v_exp_f32 v152,v152
v_mfma_f32_16x16x32_bf16 a[72:75],a[212:215],v[204:207],a[72:75]
v_exp_f32 v153,v153
v_mfma_f32_16x16x32_bf16 a[76:79],a[220:223],v[204:207],a[76:79]
v_exp_f32 v154,v154
v_mfma_f32_16x16x32_bf16 a[76:79],a[216:219],v[200:203],a[76:79]
buffer_load_dwordx4 v4,s[24:27],s47 offen nt lds
v_mfma_f32_16x16x32_bf16 a[80:83],a[224:227],v[200:203],a[80:83]
v_exp_f32 v155,v155
v_mfma_f32_16x16x32_bf16 a[80:83],a[228:231],v[204:207],a[80:83]
v_exp_f32 v156,v156
v_mfma_f32_16x16x32_bf16 a[84:87],a[236:239],v[204:207],a[84:87]
v_exp_f32 v157,v157
v_mfma_f32_16x16x32_bf16 a[84:87],a[232:235],v[200:203],a[84:87]
v_exp_f32 v158,v158
v_mfma_f32_16x16x32_bf16 a[88:91],a[240:243],v[200:203],a[88:91]
v_exp_f32 v159,v159
v_mfma_f32_16x16x32_bf16 a[88:91],a[244:247],v[204:207],a[88:91]
v_cvt_pk_bf16_f32 v140,v152,v153
v_cvt_pk_bf16_f32 v141,v154,v155
v_mfma_f32_16x16x32_bf16 a[92:95],a[252:255],v[204:207],a[92:95]
v_cvt_pk_bf16_f32 v142,v156,v157
v_cvt_pk_bf16_f32 v143,v158,v159
v_mfma_f32_16x16x32_bf16 a[92:95],a[248:251],v[200:203],a[92:95]
v_rcp_f32 v20,v20
v_smfmac_f32_16x16x64_bf16 v[56:59],v[28:31],v[200:207],v27
v_accvgpr_read_b32 v64,a0
v_mfma_f32_16x16x32_bf16 v[168:171],a[128:131],v[132:135],v[36:39]
v_accvgpr_read_b32 v65,a1
v_accvgpr_read_b32 v66,a2
v_mfma_f32_16x16x32_bf16 v[168:171],a[132:135],v[164:167],v[168:171]
v_accvgpr_read_b32 v67,a3
v_mul_f32 v64,v20,v64
v_mfma_f32_16x16x32_bf16 v[168:171],a[136:139],v[196:199],v[168:171]
v_mul_f32 v65,v20,v65
v_cvt_pk_bf16_f32 v64,v64,v65
v_mfma_f32_16x16x32_bf16 v[168:171],a[140:143],v[228:231],v[168:171]
v_mul_f32 v66,v20,v66
v_mul_f32 v67,v20,v67
v_mfma_f32_16x16x32_bf16 v[172:175],a[156:159],v[228:231],v[36:39]
buffer_load_dwordx4 v5,s[24:27],s47 offen offset:1024 nt lds
v_mfma_f32_16x16x32_bf16 v[172:175],a[152:155],v[196:199],v[172:175]
v_cvt_pk_bf16_f32 v65,v66,v67
v_accvgpr_read_b32 v16,a6
v_mfma_f32_16x16x32_bf16 v[172:175],a[148:151],v[164:167],v[172:175]
v_accvgpr_read_b32 v17,a7
v_accvgpr_read_b32 v66,a4
v_mfma_f32_16x16x32_bf16 v[172:175],a[144:147],v[132:135],v[172:175]
v_accvgpr_read_b32 v67,a5
v_cmp_nlt_f32 s[56:57],v48,s63
v_mfma_f32_16x16x32_bf16 v[184:187],a[160:163],v[132:135],v[36:39]
v_accvgpr_read_b32 v68,a8
v_accvgpr_read_b32 v69,a9
v_accvgpr_read_b32 v70,a10
v_accvgpr_read_b32 v71,a11
v_accvgpr_read_b32 v12,a12
v_accvgpr_read_b32 v13,a13
v_accvgpr_read_b32 v14,a14
v_accvgpr_read_b32 v15,a15
v_pk_mul_f32 v[68:69],v[20:21],v[68:69] op_sel:[0,0] op_sel_hi:[0,1]
v_pk_mul_f32 v[70:71],v[20:21],v[70:71] op_sel:[0,0] op_sel_hi:[0,1]
v_pk_mul_f32 v[12:13],v[20:21],v[12:13] op_sel:[0,0] op_sel_hi:[0,1]
v_pk_mul_f32 v[14:15],v[20:21],v[14:15] op_sel:[0,0] op_sel_hi:[0,1]
v_cvt_pk_bf16_f32 v68,v68,v69
v_cvt_pk_bf16_f32 v69,v70,v71
v_cvt_pk_bf16_f32 v70,v12,v13
v_cvt_pk_bf16_f32 v71,v14,v15
v_mfma_f32_16x16x32_bf16 v[184:187],a[164:167],v[164:167],v[184:187]
)A"
R"A(v_accvgpr_read_b32 v72,a16
v_accvgpr_read_b32 v73,a17
v_accvgpr_read_b32 v74,a18
v_accvgpr_read_b32 v75,a19
v_accvgpr_read_b32 v12,a20
v_accvgpr_read_b32 v13,a21
v_accvgpr_read_b32 v14,a22
v_accvgpr_read_b32 v15,a23
v_pk_mul_f32 v[72:73],v[20:21],v[72:73] op_sel:[0,0] op_sel_hi:[0,1]
v_pk_mul_f32 v[74:75],v[20:21],v[74:75] op_sel:[0,0] op_sel_hi:[0,1]
v_pk_mul_f32 v[12:13],v[20:21],v[12:13] op_sel:[0,0] op_sel_hi:[0,1]
v_pk_mul_f32 v[14:15],v[20:21],v[14:15] op_sel:[0,0] op_sel_hi:[0,1]
v_cvt_pk_bf16_f32 v72,v72,v73
v_cvt_pk_bf16_f32 v73,v74,v75
v_cvt_pk_bf16_f32 v74,v12,v13
v_cvt_pk_bf16_f32 v75,v14,v15
v_mfma_f32_16x16x32_bf16 v[184:187],a[168:171],v[196:199],v[184:187]
v_accvgpr_read_b32 v76,a24
v_accvgpr_read_b32 v77,a25
v_accvgpr_read_b32 v78,a26
v_accvgpr_read_b32 v79,a27
v_accvgpr_read_b32 v12,a28
v_accvgpr_read_b32 v13,a29
v_accvgpr_read_b32 v14,a30
v_accvgpr_read_b32 v15,a31
v_pk_mul_f32 v[76:77],v[20:21],v[76:77] op_sel:[0,0] op_sel_hi:[0,1]
v_pk_mul_f32 v[78:79],v[20:21],v[78:79] op_sel:[0,0] op_sel_hi:[0,1]
v_pk_mul_f32 v[12:13],v[20:21],v[12:13] op_sel:[0,0] op_sel_hi:[0,1]
v_pk_mul_f32 v[14:15],v[20:21],v[14:15] op_sel:[0,0] op_sel_hi:[0,1]
v_cvt_pk_bf16_f32 v76,v76,v77
v_cvt_pk_bf16_f32 v77,v78,v79
v_cvt_pk_bf16_f32 v78,v12,v13
v_cvt_pk_bf16_f32 v79,v14,v15
v_mfma_f32_16x16x32_bf16 v[184:187],a[172:175],v[228:231],v[184:187]
v_max3_f32 v44,v44,v45,v46
v_max3_f32 v44,v44,v47,s101
v_mfma_f32_16x16x32_bf16 v[188:191],a[188:191],v[228:231],v[36:39]
v_mov_b32 v48,0
v_mov_b32 v49,0
v_mfma_f32_16x16x32_bf16 v[188:191],a[184:187],v[196:199],v[188:191]
v_mov_b32 v50,0
v_mov_b32 v51,0
v_mfma_f32_16x16x32_bf16 v[188:191],a[180:183],v[164:167],v[188:191]
v_sub_f32 v47,0,v44
v_sub_f32 v46,0,v44
v_mfma_f32_16x16x32_bf16 v[188:191],a[176:179],v[132:135],v[188:191]
v_sub_f32 v45,0,v44
v_sub_f32 v44,0,v44
v_mfma_f32_16x16x32_bf16 a[96:99],a[192:195],v[232:235],a[96:99]
ds_read_b64_tr_b16 a[192:193],v2
ds_read_b64_tr_b16 a[194:195],v2 offset:4096
v_mfma_f32_16x16x32_bf16 a[96:99],a[196:199],v[236:239],a[96:99]
ds_read_b64_tr_b16 a[196:197],v2 offset:8192
v_add_f32 v52,v53,v52
v_mfma_f32_16x16x32_bf16 a[100:103],a[204:207],v[236:239],a[100:103]
buffer_load_dwordx4 v4,s[24:27],s47 offen offset:2048 nt lds
v_mfma_f32_16x16x32_bf16 a[100:103],a[200:203],v[232:235],a[100:103]
ds_read_b64_tr_b16 a[198:199],v2 offset:12288
ds_read_b64_tr_b16 a[204:205],v3 offset:8192
v_mfma_f32_16x16x32_bf16 a[104:107],a[208:211],v[232:235],a[104:107]
ds_read_b64_tr_b16 a[206:207],v3 offset:12288
v_max_f32 v21,0x00800000,v52
v_mfma_f32_16x16x32_bf16 a[104:107],a[212:215],v[236:239],a[104:107]
ds_read_b64_tr_b16 a[200:201],v3
v_accvgpr_read_b32 v80,a32
v_mfma_f32_16x16x32_bf16 a[108:111],a[220:223],v[236:239],a[108:111]
ds_read_b64_tr_b16 a[202:203],v3 offset:4096
v_accvgpr_read_b32 v81,a33
v_mfma_f32_16x16x32_bf16 a[108:111],a[216:219],v[232:235],a[108:111]
ds_read_b64_tr_b16 a[208:209],v2 offset:256
ds_read_b64_tr_b16 a[210:211],v2 offset:4352
v_mfma_f32_16x16x32_bf16 a[112:115],a[224:227],v[232:235],a[112:115]
ds_read_b64_tr_b16 a[212:213],v2 offset:8448
v_accvgpr_read_b32 v82,a34
v_mfma_f32_16x16x32_bf16 a[112:115],a[228:231],v[236:239],a[112:115]
ds_read_b64_tr_b16 a[214:215],v2 offset:12544
v_accvgpr_read_b32 v83,a35
v_mfma_f32_16x16x32_bf16 a[116:119],a[236:239],v[236:239],a[116:119]
ds_read_b64_tr_b16 a[220:221],v3 offset:8448
v_accvgpr_read_b32 v12,a38
v_mfma_f32_16x16x32_bf16 a[116:119],a[232:235],v[232:235],a[116:119]
ds_read_b64_tr_b16 a[222:223],v3 offset:12544
ds_read_b64_tr_b16 a[216:217],v3 offset:256
v_mfma_f32_16x16x32_bf16 a[120:123],a[240:243],v[232:235],a[120:123]
ds_read_b64_tr_b16 a[218:219],v3 offset:4352
v_accvgpr_read_b32 v13,a39
v_mfma_f32_16x16x32_bf16 a[120:123],a[244:247],v[236:239],a[120:123]
ds_read_b64_tr_b16 a[224:225],v2 offset:512
v_add_u32 v1,s48,v1
v_mfma_f32_16x16x32_bf16 a[124:127],a[252:255],v[236:239],a[124:127]
ds_read_b64_tr_b16 a[226:227],v2 offset:4608
v_mov_b32 v53,0
v_mfma_f32_16x16x32_bf16 a[124:127],a[248:251],v[232:235],a[124:127]
ds_read_b64_tr_b16 a[228:229],v2 offset:8704
ds_read_b64_tr_b16 a[230:231],v2 offset:12800
v_smfmac_f32_16x16x64_bf16 v[60:63],v[28:31],v[232:239],v27
ds_read_b64_tr_b16 a[236:237],v3 offset:8704
v_mfma_f32_16x16x32_bf16 v[200:203],a[128:131],v[144:147],v[40:43]
buffer_load_dwordx4 v5,s[24:27],s47 offen offset:3072 nt lds
v_mfma_f32_16x16x32_bf16 v[200:203],a[132:135],v[176:179],v[200:203]
s_add_u32 s47,s47,16384
ds_read_b64_tr_b16 a[238:239],v3 offset:12800
ds_read_b64_tr_b16 a[232:233],v3 offset:512
v_mfma_f32_16x16x32_bf16 v[200:203],a[136:139],v[208:211],v[200:203]
s_and_b32 s47,s47,0x3ffff
s_or_b32 s47,s47,s46
ds_read_b64_tr_b16 a[234:235],v3 offset:4608
v_mfma_f32_16x16x32_bf16 v[200:203],a[140:143],v[240:243],v[200:203]
s_and_b32 s46,s54,0xfffc0000
ds_read_b64_tr_b16 a[240:241],v2 offset:768
v_mov_b32 v54,0
v_mfma_f32_16x16x32_bf16 v[204:207],a[156:159],v[240:243],v[40:43]
ds_read_b64_tr_b16 a[242:243],v2 offset:4864
v_mov_b32 v55,0
v_mfma_f32_16x16x32_bf16 v[204:207],a[152:155],v[208:211],v[204:207]
ds_read_b64_tr_b16 a[244:245],v2 offset:8960
ds_read_b64_tr_b16 a[246:247],v2 offset:13056
v_mfma_f32_16x16x32_bf16 v[204:207],a[148:151],v[176:179],v[204:207]
ds_read_b64_tr_b16 a[252:253],v3 offset:8960
v_add_f32 v56,v57,v56
v_mfma_f32_16x16x32_bf16 v[204:207],a[144:147],v[144:147],v[204:207]
ds_read_b64_tr_b16 a[254:255],v3 offset:13056
v_max_f32 v22,0x00800000,v56
v_mfma_f32_16x16x32_bf16 v[216:219],a[160:163],v[144:147],v[40:43]
ds_read_b64_tr_b16 a[248:249],v3 offset:768
v_accvgpr_read_b32 v96,a64
v_mfma_f32_16x16x32_bf16 v[216:219],a[164:167],v[176:179],v[216:219]
ds_read_b64_tr_b16 a[250:251],v3 offset:4864
v_accvgpr_read_b32 v97,a65
v_mfma_f32_16x16x32_bf16 v[216:219],a[168:171],v[208:211],v[216:219]
v_exp_f32 v168,v168
v_mfma_f32_16x16x32_bf16 v[216:219],a[172:175],v[240:243],v[216:219]
v_exp_f32 v169,v169
v_mfma_f32_16x16x32_bf16 v[220:223],a[188:191],v[240:243],v[40:43]
v_exp_f32 v170,v170
v_mfma_f32_16x16x32_bf16 v[220:223],a[184:187],v[208:211],v[220:223]
v_exp_f32 v171,v171
v_mfma_f32_16x16x32_bf16 v[220:223],a[180:183],v[176:179],v[220:223]
v_cvt_pk_bf16_f32 v168,v168,v169
v_cvt_pk_bf16_f32 v169,v170,v171
v_mfma_f32_16x16x32_bf16 v[220:223],a[176:179],v[144:147],v[220:223]
s_waitcnt lgkmcnt(2)
v_exp_f32 v172,v172
v_mfma_f32_16x16x32_bf16 a[0:3],a[192:195],v[136:139],0
v_exp_f32 v173,v173
v_mfma_f32_16x16x32_bf16 a[0:3],a[196:199],v[140:143],a[0:3]
v_exp_f32 v174,v174
v_mfma_f32_16x16x32_bf16 a[4:7],a[204:207],v[140:143],0
v_exp_f32 v175,v175
v_mfma_f32_16x16x32_bf16 a[4:7],a[200:203],v[136:139],a[4:7]
s_memrealtime s[76:77]
v_cvt_pk_bf16_f32 v170,v172,v173
v_cvt_pk_bf16_f32 v171,v174,v175
v_mfma_f32_16x16x32_bf16 a[8:11],a[208:211],v[136:139],0
v_exp_f32 v184,v184
v_mfma_f32_16x16x32_bf16 a[8:11],a[212:215],v[140:143],a[8:11]
v_exp_f32 v185,v185
v_mfma_f32_16x16x32_bf16 a[12:15],a[220:223],v[140:143],0
v_exp_f32 v186,v186
v_mfma_f32_16x16x32_bf16 a[12:15],a[216:219],v[136:139],a[12:15]
v_exp_f32 v187,v187
v_mfma_f32_16x16x32_bf16 a[16:19],a[224:227],v[136:139],0
v_exp_f32 v188,v188
v_mfma_f32_16x16x32_bf16 a[16:19],a[228:231],v[140:143],a[16:19]
v_exp_f32 v189,v189
v_mfma_f32_16x16x32_bf16 a[20:23],a[236:239],v[140:143],0
s_waitcnt lgkmcnt(0)
v_exp_f32 v190,v190
v_mfma_f32_16x16x32_bf16 a[20:23],a[232:235],v[136:139],a[20:23]
s_sub_u32 s79,s78,s76
v_exp_f32 v191,v191
v_mfma_f32_16x16x32_bf16 a[24:27],a[240:243],v[136:139],0
s_add_u32 s78,s76,s60
v_rcp_f32 v21,v21
v_mfma_f32_16x16x32_bf16 a[24:27],a[244:247],v[140:143],a[24:27]
s_cmp_gt_i32 s79,s95
s_cbranch_scc1 L_ps5_%=
L_pr5_%=:
v_cvt_pk_bf16_f32 v172,v184,v185
v_mfma_f32_16x16x32_bf16 a[28:31],a[252:255],v[140:143],0
s_lshr_b32 s60,s94,2
v_cvt_pk_bf16_f32 v173,v186,v187
v_cvt_pk_bf16_f32 v174,v188,v189
v_mfma_f32_16x16x32_bf16 a[28:31],a[248:251],v[136:139],a[28:31]
s_max_i32 s60,s60,128
v_cvt_pk_bf16_f32 v175,v190,v191
v_mul_f32 v80,v21,v80
v_smfmac_f32_16x16x64_bf16 v[48:51],v[28:31],v[136:143],v27
v_mul_f32 v81,v21,v81
v_mfma_f32_16x16x32_bf16 v[232:235],a[128:131],v[148:151],v[44:47]
s_waitcnt vmcnt(8)
v_cvt_pk_bf16_f32 v80,v80,v81
v_mul_f32 v82,v21,v82
v_mfma_f32_16x16x32_bf16 v[232:235],a[132:135],v[180:183],v[232:235]
s_barrier
s_add_u32 m0,65536,s51
v_mul_f32 v83,v21,v83
v_mfma_f32_16x16x32_bf16 v[232:235],a[136:139],v[212:215],v[232:235]
v_cvt_pk_bf16_f32 v81,v82,v83
v_accvgpr_read_b32 v82,a36
v_mfma_f32_16x16x32_bf16 v[232:235],a[140:143],v[244:247],v[232:235]
v_accvgpr_read_b32 v83,a37
v_mul_f32 v82,v21,v82
v_mfma_f32_16x16x32_bf16 v[236:239],a[156:159],v[244:247],v[44:47]
buffer_load_dwordx4 v4,s[20:23],s47 offen nt lds
v_mfma_f32_16x16x32_bf16 v[236:239],a[152:155],v[212:215],v[236:239]
v_mul_f32 v83,v21,v83
v_mul_f32 v12,v21,v12
v_mfma_f32_16x16x32_bf16 v[236:239],a[148:151],v[180:183],v[236:239]
v_mul_f32 v13,v21,v13
v_cvt_pk_bf16_f32 v82,v82,v83
v_mfma_f32_16x16x32_bf16 v[236:239],a[144:147],v[148:151],v[236:239]
ds_read_b128 a[128:131],v1
v_cvt_pk_bf16_f32 v83,v12,v13
v_mfma_f32_16x16x32_bf16 v[248:251],a[160:163],v[148:151],v[44:47]
v_accvgpr_read_b32 v84,a40
v_accvgpr_read_b32 v85,a41
v_accvgpr_read_b32 v86,a42
v_accvgpr_read_b32 v87,a43
v_accvgpr_read_b32 v12,a44
v_accvgpr_read_b32 v13,a45
v_accvgpr_read_b32 v14,a46
v_accvgpr_read_b32 v15,a47
v_pk_mul_f32 v[84:85],v[20:21],v[84:85] op_sel:[1,0] op_sel_hi:[1,1]
v_pk_mul_f32 v[86:87],v[20:21],v[86:87] op_sel:[1,0] op_sel_hi:[1,1]
v_pk_mul_f32 v[12:13],v[20:21],v[12:13] op_sel:[1,0] op_sel_hi:[1,1]
v_pk_mul_f32 v[14:15],v[20:21],v[14:15] op_sel:[1,0] op_sel_hi:[1,1]
v_cvt_pk_bf16_f32 v84,v84,v85
v_cvt_pk_bf16_f32 v85,v86,v87
v_cvt_pk_bf16_f32 v86,v12,v13
v_cvt_pk_bf16_f32 v87,v14,v15
v_mfma_f32_16x16x32_bf16 v[248:251],a[164:167],v[180:183],v[248:251]
v_accvgpr_read_b32 v88,a48
v_accvgpr_read_b32 v89,a49
v_accvgpr_read_b32 v90,a50
v_accvgpr_read_b32 v91,a51
v_accvgpr_read_b32 v12,a52
v_accvgpr_read_b32 v13,a53
v_accvgpr_read_b32 v14,a54
v_accvgpr_read_b32 v15,a55
v_pk_mul_f32 v[88:89],v[20:21],v[88:89] op_sel:[1,0] op_sel_hi:[1,1]
v_pk_mul_f32 v[90:91],v[20:21],v[90:91] op_sel:[1,0] op_sel_hi:[1,1]
v_pk_mul_f32 v[12:13],v[20:21],v[12:13] op_sel:[1,0] op_sel_hi:[1,1]
v_pk_mul_f32 v[14:15],v[20:21],v[14:15] op_sel:[1,0] op_sel_hi:[1,1]
v_cvt_pk_bf16_f32 v88,v88,v89
v_cvt_pk_bf16_f32 v89,v90,v91
v_cvt_pk_bf16_f32 v90,v12,v13
v_cvt_pk_bf16_f32 v91,v14,v15
v_mfma_f32_16x16x32_bf16 v[248:251],a[168:171],v[212:215],v[248:251]
ds_read_b128 a[132:135],v1 offset:256
v_accvgpr_read_b32 v98,a66
v_mfma_f32_16x16x32_bf16 v[248:251],a[172:175],v[244:247],v[248:251]
v_accvgpr_read_b32 v92,a56
v_accvgpr_read_b32 v93,a57
v_accvgpr_read_b32 v94,a58
v_accvgpr_read_b32 v95,a59
v_accvgpr_read_b32 v12,a60
v_accvgpr_read_b32 v13,a61
v_accvgpr_read_b32 v14,a62
v_accvgpr_read_b32 v15,a63
v_pk_mul_f32 v[92:93],v[20:21],v[92:93] op_sel:[1,0] op_sel_hi:[1,1]
v_pk_mul_f32 v[94:95],v[20:21],v[94:95] op_sel:[1,0] op_sel_hi:[1,1]
v_pk_mul_f32 v[12:13],v[20:21],v[12:13] op_sel:[1,0] op_sel_hi:[1,1]
v_pk_mul_f32 v[14:15],v[20:21],v[14:15] op_sel:[1,0] op_sel_hi:[1,1]
v_cvt_pk_bf16_f32 v92,v92,v93
v_cvt_pk_bf16_f32 v93,v94,v95
v_cvt_pk_bf16_f32 v94,v12,v13
v_cvt_pk_bf16_f32 v95,v14,v15
v_mfma_f32_16x16x32_bf16 v[252:255],a[188:191],v[244:247],v[44:47]
ds_read_b128 a[136:139],v1 offset:512
v_accvgpr_read_b32 v99,a67
v_mfma_f32_16x16x32_bf16 v[252:255],a[184:187],v[212:215],v[252:255]
v_cmp_nlt_f32 s[58:59],v52,s63
s_nop 1
s_or_b64 s[56:57],s[56:57],s[58:59]
v_mfma_f32_16x16x32_bf16 v[252:255],a[180:183],v[180:183],v[252:255]
ds_read_b128 a[140:143],v1 offset:768
v_mov_b32 v52,0
v_mfma_f32_16x16x32_bf16 v[252:255],a[176:179],v[148:151],v[252:255]
v_exp_f32 v200,v200
v_mfma_f32_16x16x32_bf16 a[32:35],a[192:195],v[168:171],0
ds_read_b128 a[156:159],v1 offset:4864
v_accvgpr_read_b32 v14,a70
v_mfma_f32_16x16x32_bf16 a[32:35],a[196:199],v[172:175],a[32:35]
v_exp_f32 v201,v201
v_mfma_f32_16x16x32_bf16 a[36:39],a[204:207],v[172:175],0
buffer_load_dwordx4 v4,s[20:23],s47 offen offset:1024 nt lds
v_mfma_f32_16x16x32_bf16 a[36:39],a[200:203],v[168:171],a[36:39]
)A"
R"A(ds_read_b128 a[152:155],v1 offset:4608
v_cvt_pk_bf16_f32 v200,v200,v201
v_mfma_f32_16x16x32_bf16 a[40:43],a[208:211],v[168:171],0
v_exp_f32 v202,v202
v_mfma_f32_16x16x32_bf16 a[40:43],a[212:215],v[172:175],a[40:43]
ds_read_b128 a[148:151],v1 offset:4352
v_accvgpr_read_b32 v15,a71
v_mfma_f32_16x16x32_bf16 a[44:47],a[220:223],v[172:175],0
v_exp_f32 v203,v203
v_mfma_f32_16x16x32_bf16 a[44:47],a[216:219],v[168:171],a[44:47]
ds_read_b128 a[144:147],v1 offset:4096
v_cvt_pk_bf16_f32 v201,v202,v203
v_mfma_f32_16x16x32_bf16 a[48:51],a[224:227],v[168:171],0
v_exp_f32 v204,v204
v_mfma_f32_16x16x32_bf16 a[48:51],a[228:231],v[172:175],a[48:51]
ds_read_b128 a[160:163],v1 offset:8192
v_mov_b32 v57,0
v_mfma_f32_16x16x32_bf16 a[52:55],a[236:239],v[172:175],0
v_exp_f32 v205,v205
v_mfma_f32_16x16x32_bf16 a[52:55],a[232:235],v[168:171],a[52:55]
ds_read_b128 a[164:167],v1 offset:8448
v_cvt_pk_bf16_f32 v202,v204,v205
v_mfma_f32_16x16x32_bf16 a[56:59],a[240:243],v[168:171],0
v_exp_f32 v206,v206
v_mfma_f32_16x16x32_bf16 a[56:59],a[244:247],v[172:175],a[56:59]
ds_read_b128 a[168:171],v1 offset:8704
v_mov_b32 v58,0
v_mfma_f32_16x16x32_bf16 a[60:63],a[252:255],v[172:175],0
v_exp_f32 v207,v207
v_mfma_f32_16x16x32_bf16 a[60:63],a[248:251],v[168:171],a[60:63]
ds_read_b128 a[172:175],v1 offset:8960
v_cvt_pk_bf16_f32 v203,v206,v207
v_smfmac_f32_16x16x64_bf16 v[52:55],v[28:31],v[168:175],v27
s_waitcnt lgkmcnt(4)
v_mov_b32 v59,0
v_mfma_f32_16x16x32_bf16 v[136:139],a[128:131],v[128:131],v[32:35]
buffer_load_dwordx4 v5,s[20:23],s47 offen offset:2048 nt lds
v_mfma_f32_16x16x32_bf16 v[136:139],a[132:135],v[160:163],v[136:139]
ds_read_b128 a[188:191],v1 offset:13056
v_add_f32 v60,v61,v60
v_mfma_f32_16x16x32_bf16 v[136:139],a[136:139],v[192:195],v[136:139]
v_exp_f32 v216,v216
v_mfma_f32_16x16x32_bf16 v[136:139],a[140:143],v[224:227],v[136:139]
ds_read_b128 a[184:187],v1 offset:12800
v_max_f32 v23,0x00800000,v60
v_mfma_f32_16x16x32_bf16 v[140:143],a[156:159],v[224:227],v[32:35]
v_exp_f32 v217,v217
v_mfma_f32_16x16x32_bf16 v[140:143],a[152:155],v[192:195],v[140:143]
ds_read_b128 a[180:183],v1 offset:12544
v_cvt_pk_bf16_f32 v204,v216,v217
v_mfma_f32_16x16x32_bf16 v[140:143],a[148:151],v[160:163],v[140:143]
ds_read_b128 a[176:179],v1 offset:12288
v_mfma_f32_16x16x32_bf16 v[140:143],a[144:147],v[128:131],v[140:143]
s_waitcnt lgkmcnt(4)
v_exp_f32 v218,v218
v_mfma_f32_16x16x32_bf16 v[152:155],a[160:163],v[128:131],v[32:35]
v_exp_f32 v219,v219
v_mfma_f32_16x16x32_bf16 v[152:155],a[164:167],v[160:163],v[152:155]
v_exp_f32 v220,v220
v_mfma_f32_16x16x32_bf16 v[152:155],a[168:171],v[192:195],v[152:155]
v_exp_f32 v221,v221
v_mfma_f32_16x16x32_bf16 v[152:155],a[172:175],v[224:227],v[152:155]
s_waitcnt lgkmcnt(2)
v_exp_f32 v222,v222
v_mfma_f32_16x16x32_bf16 v[156:159],a[188:191],v[224:227],v[32:35]
v_exp_f32 v223,v223
v_mfma_f32_16x16x32_bf16 v[156:159],a[184:187],v[192:195],v[156:159]
s_waitcnt lgkmcnt(1)
v_rcp_f32 v22,v22
v_mfma_f32_16x16x32_bf16 v[156:159],a[180:183],v[160:163],v[156:159]
s_waitcnt lgkmcnt(0)
v_cvt_pk_bf16_f32 v205,v218,v219
v_cvt_pk_bf16_f32 v206,v220,v221
v_cvt_pk_bf16_f32 v207,v222,v223
v_mfma_f32_16x16x32_bf16 v[156:159],a[176:179],v[128:131],v[156:159]
v_mul_f32 v96,v22,v96
v_mul_f32 v97,v22,v97
v_mfma_f32_16x16x32_bf16 a[64:67],a[192:195],v[200:203],0
buffer_load_dwordx4 v5,s[20:23],s47 offen offset:3072 nt lds
v_cvt_pk_bf16_f32 v96,v96,v97
v_mul_f32 v98,v22,v98
v_mul_f32 v99,v22,v99
v_cvt_pk_bf16_f32 v97,v98,v99
v_mfma_f32_16x16x32_bf16 a[64:67],a[196:199],v[204:207],a[64:67]
v_accvgpr_read_b32 v98,a68
v_accvgpr_read_b32 v99,a69
v_mul_f32 v98,v22,v98
v_mul_f32 v99,v22,v99
v_mfma_f32_16x16x32_bf16 a[68:71],a[204:207],v[204:207],0
v_mul_f32 v14,v22,v14
v_mul_f32 v15,v22,v15
v_cvt_pk_bf16_f32 v98,v98,v99
v_cvt_pk_bf16_f32 v99,v14,v15
v_mfma_f32_16x16x32_bf16 a[68:71],a[200:203],v[200:203],a[68:71]
v_accvgpr_read_b32 v100,a72
v_accvgpr_read_b32 v101,a73
v_accvgpr_read_b32 v102,a74
v_accvgpr_read_b32 v103,a75
v_accvgpr_read_b32 v12,a76
v_accvgpr_read_b32 v13,a77
v_accvgpr_read_b32 v14,a78
v_accvgpr_read_b32 v15,a79
v_pk_mul_f32 v[100:101],v[22:23],v[100:101] op_sel:[0,0] op_sel_hi:[0,1]
v_pk_mul_f32 v[102:103],v[22:23],v[102:103] op_sel:[0,0] op_sel_hi:[0,1]
v_pk_mul_f32 v[12:13],v[22:23],v[12:13] op_sel:[0,0] op_sel_hi:[0,1]
v_pk_mul_f32 v[14:15],v[22:23],v[14:15] op_sel:[0,0] op_sel_hi:[0,1]
v_cvt_pk_bf16_f32 v100,v100,v101
v_cvt_pk_bf16_f32 v101,v102,v103
v_cvt_pk_bf16_f32 v102,v12,v13
v_cvt_pk_bf16_f32 v103,v14,v15
v_mfma_f32_16x16x32_bf16 a[72:75],a[208:211],v[200:203],0
v_accvgpr_read_b32 v104,a80
v_accvgpr_read_b32 v105,a81
v_accvgpr_read_b32 v106,a82
v_accvgpr_read_b32 v107,a83
v_accvgpr_read_b32 v12,a84
v_accvgpr_read_b32 v13,a85
v_accvgpr_read_b32 v14,a86
v_accvgpr_read_b32 v15,a87
v_pk_mul_f32 v[104:105],v[22:23],v[104:105] op_sel:[0,0] op_sel_hi:[0,1]
v_pk_mul_f32 v[106:107],v[22:23],v[106:107] op_sel:[0,0] op_sel_hi:[0,1]
v_pk_mul_f32 v[12:13],v[22:23],v[12:13] op_sel:[0,0] op_sel_hi:[0,1]
v_pk_mul_f32 v[14:15],v[22:23],v[14:15] op_sel:[0,0] op_sel_hi:[0,1]
v_cvt_pk_bf16_f32 v104,v104,v105
v_cvt_pk_bf16_f32 v105,v106,v107
v_cvt_pk_bf16_f32 v106,v12,v13
v_cvt_pk_bf16_f32 v107,v14,v15
v_mfma_f32_16x16x32_bf16 a[72:75],a[212:215],v[204:207],a[72:75]
v_accvgpr_read_b32 v108,a88
v_accvgpr_read_b32 v109,a89
v_accvgpr_read_b32 v110,a90
v_accvgpr_read_b32 v111,a91
v_accvgpr_read_b32 v12,a92
v_accvgpr_read_b32 v13,a93
v_accvgpr_read_b32 v14,a94
v_accvgpr_read_b32 v15,a95
v_pk_mul_f32 v[108:109],v[22:23],v[108:109] op_sel:[0,0] op_sel_hi:[0,1]
v_pk_mul_f32 v[110:111],v[22:23],v[110:111] op_sel:[0,0] op_sel_hi:[0,1]
v_pk_mul_f32 v[12:13],v[22:23],v[12:13] op_sel:[0,0] op_sel_hi:[0,1]
v_pk_mul_f32 v[14:15],v[22:23],v[14:15] op_sel:[0,0] op_sel_hi:[0,1]
v_cvt_pk_bf16_f32 v108,v108,v109
v_cvt_pk_bf16_f32 v109,v110,v111
v_cvt_pk_bf16_f32 v110,v12,v13
v_cvt_pk_bf16_f32 v111,v14,v15
v_mfma_f32_16x16x32_bf16 a[76:79],a[220:223],v[204:207],0
v_cmp_nlt_f32 s[58:59],v56,s63
s_nop 1
s_or_b64 s[56:57],s[56:57],s[58:59]
v_mfma_f32_16x16x32_bf16 a[76:79],a[216:219],v[200:203],a[76:79]
v_mov_b32 v56,0
v_accvgpr_read_b32 v112,a96
v_mfma_f32_16x16x32_bf16 a[80:83],a[224:227],v[200:203],0
s_add_u32 m0,65536,s52
v_exp_f32 v232,v232
v_mfma_f32_16x16x32_bf16 a[80:83],a[228:231],v[204:207],a[80:83]
v_exp_f32 v233,v233
v_mfma_f32_16x16x32_bf16 a[84:87],a[236:239],v[204:207],0
v_exp_f32 v234,v234
v_mfma_f32_16x16x32_bf16 a[84:87],a[232:235],v[200:203],a[84:87]
buffer_load_dwordx4 v4,s[24:27],s47 offen nt lds
v_mfma_f32_16x16x32_bf16 a[88:91],a[240:243],v[200:203],0
v_exp_f32 v235,v235
v_mfma_f32_16x16x32_bf16 a[88:91],a[244:247],v[204:207],a[88:91]
v_cvt_pk_bf16_f32 v232,v232,v233
v_cvt_pk_bf16_f32 v233,v234,v235
v_mfma_f32_16x16x32_bf16 a[92:95],a[252:255],v[204:207],0
v_exp_f32 v236,v236
v_mfma_f32_16x16x32_bf16 a[92:95],a[248:251],v[200:203],a[92:95]
v_exp_f32 v237,v237
v_smfmac_f32_16x16x64_bf16 v[56:59],v[28:31],v[200:207],v27
v_cvt_pk_bf16_f32 v234,v236,v237
v_mfma_f32_16x16x32_bf16 v[168:171],a[128:131],v[132:135],v[36:39]
v_exp_f32 v238,v238
v_mfma_f32_16x16x32_bf16 v[168:171],a[132:135],v[164:167],v[168:171]
v_exp_f32 v239,v239
v_mfma_f32_16x16x32_bf16 v[168:171],a[136:139],v[196:199],v[168:171]
v_cvt_pk_bf16_f32 v235,v238,v239
v_accvgpr_read_b32 v113,a97
v_mfma_f32_16x16x32_bf16 v[168:171],a[140:143],v[228:231],v[168:171]
v_exp_f32 v248,v248
v_mfma_f32_16x16x32_bf16 v[172:175],a[156:159],v[228:231],v[36:39]
v_exp_f32 v249,v249
v_mfma_f32_16x16x32_bf16 v[172:175],a[152:155],v[196:199],v[172:175]
v_exp_f32 v250,v250
v_mfma_f32_16x16x32_bf16 v[172:175],a[148:151],v[164:167],v[172:175]
v_exp_f32 v251,v251
v_mfma_f32_16x16x32_bf16 v[172:175],a[144:147],v[132:135],v[172:175]
v_exp_f32 v252,v252
v_mfma_f32_16x16x32_bf16 v[184:187],a[160:163],v[132:135],v[36:39]
buffer_load_dwordx4 v5,s[24:27],s47 offen offset:1024 nt lds
v_mfma_f32_16x16x32_bf16 v[184:187],a[164:167],v[164:167],v[184:187]
v_exp_f32 v253,v253
v_mfma_f32_16x16x32_bf16 v[184:187],a[168:171],v[196:199],v[184:187]
v_exp_f32 v254,v254
v_mfma_f32_16x16x32_bf16 v[184:187],a[172:175],v[228:231],v[184:187]
v_exp_f32 v255,v255
v_mfma_f32_16x16x32_bf16 v[188:191],a[188:191],v[228:231],v[36:39]
v_cvt_pk_bf16_f32 v236,v248,v249
v_cvt_pk_bf16_f32 v237,v250,v251
v_mfma_f32_16x16x32_bf16 v[188:191],a[184:187],v[196:199],v[188:191]
v_cvt_pk_bf16_f32 v238,v252,v253
v_cvt_pk_bf16_f32 v239,v254,v255
v_mfma_f32_16x16x32_bf16 v[188:191],a[180:183],v[164:167],v[188:191]
v_rcp_f32 v23,v23
v_mfma_f32_16x16x32_bf16 v[188:191],a[176:179],v[132:135],v[188:191]
v_accvgpr_read_b32 v114,a98
v_accvgpr_read_b32 v115,a99
v_mfma_f32_16x16x32_bf16 a[96:99],a[192:195],v[232:235],0
v_mul_f32 v66,v20,v66
v_mul_f32 v67,v20,v67
v_mul_f32 v112,v23,v112
v_mul_f32 v113,v23,v113
v_mul_f32 v16,v20,v16
v_mul_f32 v17,v20,v17
v_cvt_pk_bf16_f32 v66,v66,v67
v_cvt_pk_bf16_f32 v112,v112,v113
v_mul_f32 v114,v23,v114
v_mul_f32 v115,v23,v115
v_mfma_f32_16x16x32_bf16 a[96:99],a[196:199],v[236:239],a[96:99]
v_cvt_pk_bf16_f32 v67,v16,v17
v_cvt_pk_bf16_f32 v113,v114,v115
v_accvgpr_read_b32 v16,a102
v_accvgpr_read_b32 v17,a103
v_accvgpr_read_b32 v114,a100
v_accvgpr_read_b32 v115,a101
v_mfma_f32_16x16x32_bf16 a[100:103],a[204:207],v[236:239],0
v_accvgpr_read_b32 v116,a104
v_accvgpr_read_b32 v117,a105
v_accvgpr_read_b32 v118,a106
v_accvgpr_read_b32 v119,a107
v_accvgpr_read_b32 v12,a108
v_accvgpr_read_b32 v13,a109
v_accvgpr_read_b32 v14,a110
v_accvgpr_read_b32 v15,a111
v_pk_mul_f32 v[116:117],v[22:23],v[116:117] op_sel:[1,0] op_sel_hi:[1,1]
v_pk_mul_f32 v[118:119],v[22:23],v[118:119] op_sel:[1,0] op_sel_hi:[1,1]
v_pk_mul_f32 v[12:13],v[22:23],v[12:13] op_sel:[1,0] op_sel_hi:[1,1]
v_pk_mul_f32 v[14:15],v[22:23],v[14:15] op_sel:[1,0] op_sel_hi:[1,1]
v_cvt_pk_bf16_f32 v116,v116,v117
v_cvt_pk_bf16_f32 v117,v118,v119
v_cvt_pk_bf16_f32 v118,v12,v13
v_cvt_pk_bf16_f32 v119,v14,v15
v_mfma_f32_16x16x32_bf16 a[100:103],a[200:203],v[232:235],a[100:103]
v_accvgpr_read_b32 v120,a112
v_accvgpr_read_b32 v121,a113
v_accvgpr_read_b32 v122,a114
v_accvgpr_read_b32 v123,a115
v_accvgpr_read_b32 v12,a116
v_accvgpr_read_b32 v13,a117
v_accvgpr_read_b32 v14,a118
v_accvgpr_read_b32 v15,a119
v_pk_mul_f32 v[120:121],v[22:23],v[120:121] op_sel:[1,0] op_sel_hi:[1,1]
v_pk_mul_f32 v[122:123],v[22:23],v[122:123] op_sel:[1,0] op_sel_hi:[1,1]
v_pk_mul_f32 v[12:13],v[22:23],v[12:13] op_sel:[1,0] op_sel_hi:[1,1]
v_pk_mul_f32 v[14:15],v[22:23],v[14:15] op_sel:[1,0] op_sel_hi:[1,1]
v_cvt_pk_bf16_f32 v120,v120,v121
v_cvt_pk_bf16_f32 v121,v122,v123
v_cvt_pk_bf16_f32 v122,v12,v13
v_cvt_pk_bf16_f32 v123,v14,v15
v_mfma_f32_16x16x32_bf16 a[104:107],a[208:211],v[232:235],0
v_accvgpr_read_b32 v124,a120
v_accvgpr_read_b32 v125,a121
v_accvgpr_read_b32 v126,a122
v_accvgpr_read_b32 v127,a123
v_accvgpr_read_b32 v12,a124
v_accvgpr_read_b32 v13,a125
v_accvgpr_read_b32 v14,a126
v_accvgpr_read_b32 v15,a127
v_pk_mul_f32 v[124:125],v[22:23],v[124:125] op_sel:[1,0] op_sel_hi:[1,1]
v_pk_mul_f32 v[126:127],v[22:23],v[126:127] op_sel:[1,0] op_sel_hi:[1,1]
v_pk_mul_f32 v[12:13],v[22:23],v[12:13] op_sel:[1,0] op_sel_hi:[1,1]
v_pk_mul_f32 v[14:15],v[22:23],v[14:15] op_sel:[1,0] op_sel_hi:[1,1]
v_cvt_pk_bf16_f32 v124,v124,v125
v_cvt_pk_bf16_f32 v125,v126,v127
v_cvt_pk_bf16_f32 v126,v12,v13
v_cvt_pk_bf16_f32 v127,v14,v15
v_mfma_f32_16x16x32_bf16 a[104:107],a[212:215],v[236:239],a[104:107]
v_cmp_nlt_f32 s[58:59],v60,s63
s_nop 1
s_or_b64 s[56:57],s[56:57],s[58:59]
v_mfma_f32_16x16x32_bf16 a[108:111],a[220:223],v[236:239],0
buffer_load_dwordx4 v4,s[24:27],s47 offen offset:2048 nt lds
v_mfma_f32_16x16x32_bf16 a[108:111],a[216:219],v[232:235],a[108:111]
v_mov_b32 v60,0
v_mov_b32 v61,0
v_mfma_f32_16x16x32_bf16 a[112:115],a[224:227],v[232:235],0
v_mov_b32 v62,0
v_mov_b32 v63,0
v_mfma_f32_16x16x32_bf16 a[112:115],a[228:231],v[236:239],a[112:115]
v_add_u32 v2,s48,v2
v_add_u32 v3,s48,v3
v_mfma_f32_16x16x32_bf16 a[116:119],a[236:239],v[236:239],0
s_mov_b32 s48,32768
ds_read_b64_tr_b16 a[192:193],v2
ds_read_b64_tr_b16 a[194:195],v2 offset:4096
v_mfma_f32_16x16x32_bf16 a[116:119],a[232:235],v[232:235],a[116:119]
)A"
R"A(ds_read_b64_tr_b16 a[196:197],v2 offset:8192
v_mul_f32 v114,v23,v114
v_mfma_f32_16x16x32_bf16 a[120:123],a[240:243],v[232:235],0
ds_read_b64_tr_b16 a[198:199],v2 offset:12288
v_mul_f32 v115,v23,v115
v_mfma_f32_16x16x32_bf16 a[120:123],a[244:247],v[236:239],a[120:123]
ds_read_b64_tr_b16 a[204:205],v3 offset:8192
v_mul_f32 v16,v23,v16
v_mfma_f32_16x16x32_bf16 a[124:127],a[252:255],v[236:239],0
ds_read_b64_tr_b16 a[206:207],v3 offset:12288
ds_read_b64_tr_b16 a[200:201],v3
v_mfma_f32_16x16x32_bf16 a[124:127],a[248:251],v[232:235],a[124:127]
ds_read_b64_tr_b16 a[202:203],v3 offset:4096
v_mul_f32 v17,v23,v17
v_smfmac_f32_16x16x64_bf16 v[60:63],v[28:31],v[232:239],v27
ds_read_b64_tr_b16 a[208:209],v2 offset:256
v_mfma_f32_16x16x32_bf16 v[200:203],a[128:131],v[144:147],v[40:43]
v_exp_f32 v136,v136
v_mfma_f32_16x16x32_bf16 v[200:203],a[132:135],v[176:179],v[200:203]
v_exp_f32 v137,v137
v_mfma_f32_16x16x32_bf16 v[200:203],a[136:139],v[208:211],v[200:203]
ds_read_b64_tr_b16 a[210:211],v2 offset:4352
v_cvt_pk_bf16_f32 v136,v136,v137
v_mfma_f32_16x16x32_bf16 v[200:203],a[140:143],v[240:243],v[200:203]
buffer_load_dwordx4 v5,s[24:27],s47 offen offset:3072 nt lds
v_mfma_f32_16x16x32_bf16 v[204:207],a[156:159],v[240:243],v[40:43]
s_add_u32 s47,s47,16384
v_exp_f32 v138,v138
v_mfma_f32_16x16x32_bf16 v[204:207],a[152:155],v[208:211],v[204:207]
s_and_b32 s47,s47,0x3ffff
v_exp_f32 v139,v139
v_mfma_f32_16x16x32_bf16 v[204:207],a[148:151],v[176:179],v[204:207]
s_or_b32 s47,s47,s46
v_exp_f32 v140,v140
v_mfma_f32_16x16x32_bf16 v[204:207],a[144:147],v[144:147],v[204:207]
v_exp_f32 v141,v141
v_mfma_f32_16x16x32_bf16 v[216:219],a[160:163],v[144:147],v[40:43]
v_exp_f32 v142,v142
v_mfma_f32_16x16x32_bf16 v[216:219],a[164:167],v[176:179],v[216:219]
v_exp_f32 v143,v143
v_mfma_f32_16x16x32_bf16 v[216:219],a[168:171],v[208:211],v[216:219]
ds_read_b64_tr_b16 a[212:213],v2 offset:8448
ds_read_b64_tr_b16 a[214:215],v2 offset:12544
v_mfma_f32_16x16x32_bf16 v[216:219],a[172:175],v[240:243],v[216:219]
v_cvt_pk_bf16_f32 v137,v138,v139
v_cvt_pk_bf16_f32 v138,v140,v141
v_mfma_f32_16x16x32_bf16 v[220:223],a[188:191],v[240:243],v[40:43]
ds_read_b64_tr_b16 a[220:221],v3 offset:8448
v_cvt_pk_bf16_f32 v139,v142,v143
v_mfma_f32_16x16x32_bf16 v[220:223],a[184:187],v[208:211],v[220:223]
ds_read_b64_tr_b16 a[222:223],v3 offset:12544
v_exp_f32 v152,v152
v_exp_f32 v153,v153
v_exp_f32 v154,v154
v_exp_f32 v155,v155
v_exp_f32 v156,v156
v_exp_f32 v157,v157
v_exp_f32 v158,v158
v_exp_f32 v159,v159
v_mfma_f32_16x16x32_bf16 v[220:223],a[180:183],v[176:179],v[220:223]
ds_read_b64_tr_b16 a[216:217],v3 offset:256
ds_read_b64_tr_b16 a[218:219],v3 offset:4352
v_cvt_pk_bf16_f32 v140,v152,v153
v_cvt_pk_bf16_f32 v141,v154,v155
v_cvt_pk_bf16_f32 v142,v156,v157
v_cvt_pk_bf16_f32 v143,v158,v159
v_mfma_f32_16x16x32_bf16 v[220:223],a[176:179],v[144:147],v[220:223]
s_waitcnt lgkmcnt(6)
ds_read_b64_tr_b16 a[224:225],v2 offset:512
ds_read_b64_tr_b16 a[226:227],v2 offset:4608
v_mfma_f32_16x16x32_bf16 a[0:3],a[192:195],v[136:139],a[0:3]
ds_read_b64_tr_b16 a[228:229],v2 offset:8704
ds_read_b64_tr_b16 a[230:231],v2 offset:12800
v_mfma_f32_16x16x32_bf16 a[0:3],a[196:199],v[140:143],a[0:3]
ds_read_b64_tr_b16 a[236:237],v3 offset:8704
ds_read_b64_tr_b16 a[238:239],v3 offset:12800
v_mfma_f32_16x16x32_bf16 a[4:7],a[204:207],v[140:143],a[4:7]
ds_read_b64_tr_b16 a[232:233],v3 offset:512
ds_read_b64_tr_b16 a[234:235],v3 offset:4608
v_mfma_f32_16x16x32_bf16 a[4:7],a[200:203],v[136:139],a[4:7]
ds_read_b64_tr_b16 a[240:241],v2 offset:768
ds_read_b64_tr_b16 a[242:243],v2 offset:4864
v_mfma_f32_16x16x32_bf16 a[8:11],a[208:211],v[136:139],a[8:11]
s_memrealtime s[76:77]
ds_read_b64_tr_b16 a[244:245],v2 offset:8960
ds_read_b64_tr_b16 a[246:247],v2 offset:13056
s_waitcnt lgkmcnt(15)
v_mfma_f32_16x16x32_bf16 a[8:11],a[212:215],v[140:143],a[8:11]
s_waitcnt lgkmcnt(14)
ds_read_b64_tr_b16 a[252:253],v3 offset:8960
ds_read_b64_tr_b16 a[254:255],v3 offset:13056
v_mfma_f32_16x16x32_bf16 a[12:15],a[220:223],v[140:143],a[12:15]
s_waitcnt lgkmcnt(14)
ds_read_b64_tr_b16 a[248:249],v3 offset:768
ds_read_b64_tr_b16 a[250:251],v3 offset:4864
v_mfma_f32_16x16x32_bf16 a[12:15],a[216:219],v[136:139],a[12:15]
s_waitcnt lgkmcnt(14)
v_mfma_f32_16x16x32_bf16 a[16:19],a[224:227],v[136:139],a[16:19]
s_waitcnt lgkmcnt(12)
v_mfma_f32_16x16x32_bf16 a[16:19],a[228:231],v[140:143],a[16:19]
s_waitcnt lgkmcnt(10)
v_mfma_f32_16x16x32_bf16 a[20:23],a[236:239],v[140:143],a[20:23]
s_waitcnt lgkmcnt(0)
v_mfma_f32_16x16x32_bf16 a[20:23],a[232:235],v[136:139],a[20:23]
s_sub_u32 s79,s78,s76
v_exp_f32 v168,v168
v_mfma_f32_16x16x32_bf16 a[24:27],a[240:243],v[136:139],a[24:27]
s_add_u32 s78,s76,s60
v_exp_f32 v169,v169
v_mfma_f32_16x16x32_bf16 a[24:27],a[244:247],v[140:143],a[24:27]
s_cmp_gt_i32 s79,s95
s_cbranch_scc1 L_ps6_%=
L_pr6_%=:
v_exp_f32 v170,v170
v_exp_f32 v171,v171
v_mfma_f32_16x16x32_bf16 a[28:31],a[252:255],v[140:143],a[28:31]
v_cvt_pk_bf16_f32 v114,v114,v115
v_cvt_pk_bf16_f32 v168,v168,v169
v_exp_f32 v172,v172
v_exp_f32 v173,v173
v_exp_f32 v174,v174
v_exp_f32 v175,v175
v_cvt_pk_bf16_f32 v115,v16,v17
v_cvt_pk_bf16_f32 v169,v170,v171
v_exp_f32 v184,v184
v_exp_f32 v185,v185
v_exp_f32 v186,v186
v_exp_f32 v187,v187
v_exp_f32 v188,v188
v_exp_f32 v189,v189
v_exp_f32 v190,v190
v_exp_f32 v191,v191
v_mfma_f32_16x16x32_bf16 a[28:31],a[248:251],v[136:139],a[28:31]
s_cmp_lg_u64 s[56:57],0
s_cbranch_scc1 L_slow0_%=
L_sret0_%=:
v_cvt_pk_bf16_f32 v170,v172,v173
v_cvt_pk_bf16_f32 v171,v174,v175
v_add_u32 v1,s48,v1
v_cvt_pk_bf16_f32 v172,v184,v185
v_cvt_pk_bf16_f32 v173,v186,v187
v_cvt_pk_bf16_f32 v174,v188,v189
v_cvt_pk_bf16_f32 v175,v190,v191
v_smfmac_f32_16x16x64_bf16 v[48:51],v[28:31],v[136:143],v27
s_mov_b32 s53,11
s_cmp_eq_u32 s40,3
s_cbranch_scc0 L_att1_%=
s_cmp_gt_i32 s18,0
s_cbranch_scc1 L_att1v0_%=
s_cmp_gt_i32 s19,0
s_cbranch_scc1 L_att1v1_%=
s_cmp_gt_i32 s100,0
s_cbranch_scc1 L_att1v2_%=
.p2align 5
L_att1v3_%=:
s_waitcnt vmcnt(8) lgkmcnt(0)
v_exp_f32 v200,v200
s_barrier
s_add_u32 m0,s49,s51
ds_read_b128 a[128:131],v1
s_mov_b32 s90,L_out_end_%=-L_pc_%=+128
v_exp_f32 v201,v201
s_lshr_b32 s45,s45,1
ds_read_b128 a[132:135],v1 offset:256
v_cvt_pk_bf16_f32 v200,v200,v201
buffer_load_dwordx4 v4,s[20:23],s47 offen nt lds
ds_read_b128 a[136:139],v1 offset:512
v_add_u32 v2,s48,v2
v_exp_f32 v202,v202
s_add_u32 s62,s50,32768
ds_read_b128 a[140:143],v1 offset:768
v_add_u32 v3,s48,v3
s_and_b32 s62,s62,131071
v_exp_f32 v203,v203
s_add_u32 s54,s54,16384
ds_read_b128 a[156:159],v1 offset:4864
v_cvt_pk_bf16_f32 v201,v202,v203
s_sub_u32 s48,s62,s50
v_exp_f32 v204,v204
s_sub_u32 s53,s53,1
v_exp_f32 v205,v205
s_and_b32 s46,s54,0xfffc0000
ds_read_b128 a[152:155],v1 offset:4608
v_cvt_pk_bf16_f32 v202,v204,v205
s_mov_b32 s50,s62
v_exp_f32 v206,v206
v_exp_f32 v207,v207
ds_read_b128 a[148:151],v1 offset:4352
v_cvt_pk_bf16_f32 v203,v206,v207
v_mfma_f32_16x16x32_bf16 a[32:35],a[192:195],v[168:171],a[32:35]
v_exp_f32 v216,v216
v_mfma_f32_16x16x32_bf16 a[32:35],a[196:199],v[172:175],a[32:35]
ds_read_b128 a[144:147],v1 offset:4096
v_mfma_f32_16x16x32_bf16 a[36:39],a[204:207],v[172:175],a[36:39]
buffer_load_dwordx4 v4,s[20:23],s47 offen offset:1024 nt lds
v_mfma_f32_16x16x32_bf16 a[36:39],a[200:203],v[168:171],a[36:39]
ds_read_b128 a[160:163],v1 offset:8192
v_mfma_f32_16x16x32_bf16 a[40:43],a[208:211],v[168:171],a[40:43]
v_exp_f32 v217,v217
v_mfma_f32_16x16x32_bf16 a[40:43],a[212:215],v[172:175],a[40:43]
ds_read_b128 a[164:167],v1 offset:8448
v_cvt_pk_bf16_f32 v204,v216,v217
v_mfma_f32_16x16x32_bf16 a[44:47],a[220:223],v[172:175],a[44:47]
v_exp_f32 v218,v218
v_mfma_f32_16x16x32_bf16 a[44:47],a[216:219],v[168:171],a[44:47]
ds_read_b128 a[168:171],v1 offset:8704
v_mfma_f32_16x16x32_bf16 a[48:51],a[224:227],v[168:171],a[48:51]
v_exp_f32 v219,v219
v_mfma_f32_16x16x32_bf16 a[48:51],a[228:231],v[172:175],a[48:51]
ds_read_b128 a[172:175],v1 offset:8960
v_cvt_pk_bf16_f32 v205,v218,v219
v_mfma_f32_16x16x32_bf16 a[52:55],a[236:239],v[172:175],a[52:55]
v_exp_f32 v220,v220
v_mfma_f32_16x16x32_bf16 a[52:55],a[232:235],v[168:171],a[52:55]
ds_read_b128 a[188:191],v1 offset:13056
v_mfma_f32_16x16x32_bf16 a[56:59],a[240:243],v[168:171],a[56:59]
v_exp_f32 v221,v221
v_mfma_f32_16x16x32_bf16 a[56:59],a[244:247],v[172:175],a[56:59]
ds_read_b128 a[184:187],v1 offset:12800
v_cvt_pk_bf16_f32 v206,v220,v221
v_mfma_f32_16x16x32_bf16 a[60:63],a[252:255],v[172:175],a[60:63]
v_exp_f32 v222,v222
v_mfma_f32_16x16x32_bf16 a[60:63],a[248:251],v[168:171],a[60:63]
ds_read_b128 a[180:183],v1 offset:12544
v_smfmac_f32_16x16x64_bf16 v[52:55],v[28:31],v[168:175],v27
s_waitcnt lgkmcnt(4)
v_mfma_f32_16x16x32_bf16 v[136:139],a[128:131],v[128:131],v[32:35]
buffer_load_dwordx4 v5,s[20:23],s47 offen offset:2048 nt lds
v_mfma_f32_16x16x32_bf16 v[136:139],a[132:135],v[160:163],v[136:139]
ds_read_b128 a[176:179],v1 offset:12288
v_add_u32 v1,s48,v1
v_mfma_f32_16x16x32_bf16 v[136:139],a[136:139],v[192:195],v[136:139]
v_exp_f32 v223,v223
v_mfma_f32_16x16x32_bf16 v[136:139],a[140:143],v[224:227],v[136:139]
v_cvt_pk_bf16_f32 v207,v222,v223
v_mfma_f32_16x16x32_bf16 v[140:143],a[156:159],v[224:227],v[32:35]
v_mfma_f32_16x16x32_bf16 v[140:143],a[152:155],v[192:195],v[140:143]
v_mfma_f32_16x16x32_bf16 v[140:143],a[148:151],v[160:163],v[140:143]
v_mfma_f32_16x16x32_bf16 v[140:143],a[144:147],v[128:131],v[140:143]
v_mfma_f32_16x16x32_bf16 v[152:155],a[160:163],v[128:131],v[32:35]
v_mfma_f32_16x16x32_bf16 v[152:155],a[164:167],v[160:163],v[152:155]
v_mfma_f32_16x16x32_bf16 v[152:155],a[168:171],v[192:195],v[152:155]
s_waitcnt lgkmcnt(0)
v_mfma_f32_16x16x32_bf16 v[152:155],a[172:175],v[224:227],v[152:155]
v_mfma_f32_16x16x32_bf16 v[156:159],a[188:191],v[224:227],v[32:35]
v_mfma_f32_16x16x32_bf16 v[156:159],a[184:187],v[192:195],v[156:159]
buffer_load_dwordx4 v5,s[20:23],s47 offen offset:3072 nt lds
v_mfma_f32_16x16x32_bf16 v[156:159],a[180:183],v[160:163],v[156:159]
v_mfma_f32_16x16x32_bf16 v[156:159],a[176:179],v[128:131],v[156:159]
v_mfma_f32_16x16x32_bf16 a[64:67],a[192:195],v[200:203],a[64:67]
v_mfma_f32_16x16x32_bf16 a[64:67],a[196:199],v[204:207],a[64:67]
v_mfma_f32_16x16x32_bf16 a[68:71],a[204:207],v[204:207],a[68:71]
v_mfma_f32_16x16x32_bf16 a[68:71],a[200:203],v[200:203],a[68:71]
v_mfma_f32_16x16x32_bf16 a[72:75],a[208:211],v[200:203],a[72:75]
v_mfma_f32_16x16x32_bf16 a[72:75],a[212:215],v[204:207],a[72:75]
v_exp_f32 v136,v136
v_mfma_f32_16x16x32_bf16 a[76:79],a[220:223],v[204:207],a[76:79]
v_exp_f32 v137,v137
v_mfma_f32_16x16x32_bf16 a[76:79],a[216:219],v[200:203],a[76:79]
v_exp_f32 v138,v138
v_mfma_f32_16x16x32_bf16 a[80:83],a[224:227],v[200:203],a[80:83]
s_add_u32 m0,s49,s52
v_mfma_f32_16x16x32_bf16 a[80:83],a[228:231],v[204:207],a[80:83]
s_xor_b32 s49,s50,65536
v_mfma_f32_16x16x32_bf16 a[84:87],a[236:239],v[204:207],a[84:87]
v_mfma_f32_16x16x32_bf16 a[84:87],a[232:235],v[200:203],a[84:87]
buffer_load_dwordx4 v4,s[24:27],s47 offen nt lds
v_mfma_f32_16x16x32_bf16 a[88:91],a[240:243],v[200:203],a[88:91]
v_mfma_f32_16x16x32_bf16 a[88:91],a[244:247],v[204:207],a[88:91]
v_exp_f32 v139,v139
v_mfma_f32_16x16x32_bf16 a[92:95],a[252:255],v[204:207],a[92:95]
v_exp_f32 v140,v140
v_mfma_f32_16x16x32_bf16 a[92:95],a[248:251],v[200:203],a[92:95]
v_exp_f32 v141,v141
v_smfmac_f32_16x16x64_bf16 v[56:59],v[28:31],v[200:207],v27
v_mfma_f32_16x16x32_bf16 v[168:171],a[128:131],v[132:135],v[36:39]
v_exp_f32 v142,v142
v_mfma_f32_16x16x32_bf16 v[168:171],a[132:135],v[164:167],v[168:171]
v_exp_f32 v143,v143
v_mfma_f32_16x16x32_bf16 v[168:171],a[136:139],v[196:199],v[168:171]
v_exp_f32 v152,v152
v_mfma_f32_16x16x32_bf16 v[168:171],a[140:143],v[228:231],v[168:171]
v_exp_f32 v153,v153
v_mfma_f32_16x16x32_bf16 v[172:175],a[156:159],v[228:231],v[36:39]
v_exp_f32 v154,v154
v_mfma_f32_16x16x32_bf16 v[172:175],a[152:155],v[196:199],v[172:175]
v_exp_f32 v155,v155
v_mfma_f32_16x16x32_bf16 v[172:175],a[148:151],v[164:167],v[172:175]
v_exp_f32 v156,v156
v_mfma_f32_16x16x32_bf16 v[172:175],a[144:147],v[132:135],v[172:175]
v_exp_f32 v157,v157
v_mfma_f32_16x16x32_bf16 v[184:187],a[160:163],v[132:135],v[36:39]
buffer_load_dwordx4 v5,s[24:27],s47 offen offset:1024 nt lds
)A"
R"A(v_mfma_f32_16x16x32_bf16 v[184:187],a[164:167],v[164:167],v[184:187]
v_exp_f32 v158,v158
v_mfma_f32_16x16x32_bf16 v[184:187],a[168:171],v[196:199],v[184:187]
v_exp_f32 v159,v159
v_mfma_f32_16x16x32_bf16 v[184:187],a[172:175],v[228:231],v[184:187]
v_exp_f32 v168,v168
v_mfma_f32_16x16x32_bf16 v[188:191],a[188:191],v[228:231],v[36:39]
v_exp_f32 v169,v169
v_mfma_f32_16x16x32_bf16 v[188:191],a[184:187],v[196:199],v[188:191]
v_exp_f32 v170,v170
v_mfma_f32_16x16x32_bf16 v[188:191],a[180:183],v[164:167],v[188:191]
v_exp_f32 v171,v171
v_mfma_f32_16x16x32_bf16 v[188:191],a[176:179],v[132:135],v[188:191]
v_exp_f32 v172,v172
ds_read_b64_tr_b16 a[192:193],v2
v_cvt_pk_bf16_f32 v168,v168,v169
ds_read_b64_tr_b16 a[194:195],v2 offset:4096
v_cvt_pk_bf16_f32 v169,v170,v171
v_exp_f32 v173,v173
ds_read_b64_tr_b16 a[196:197],v2 offset:8192
v_cvt_pk_bf16_f32 v170,v172,v173
v_exp_f32 v174,v174
v_exp_f32 v175,v175
buffer_load_dwordx4 v4,s[24:27],s47 offen offset:2048 nt lds
ds_read_b64_tr_b16 a[198:199],v2 offset:12288
v_cvt_pk_bf16_f32 v171,v174,v175
v_exp_f32 v184,v184
v_exp_f32 v185,v185
ds_read_b64_tr_b16 a[204:205],v3 offset:8192
v_cvt_pk_bf16_f32 v136,v136,v137
ds_read_b64_tr_b16 a[206:207],v3 offset:12288
v_cvt_pk_bf16_f32 v172,v184,v185
ds_read_b64_tr_b16 a[200:201],v3
ds_read_b64_tr_b16 a[202:203],v3 offset:4096
v_cvt_pk_bf16_f32 v137,v138,v139
ds_read_b64_tr_b16 a[208:209],v2 offset:256
ds_read_b64_tr_b16 a[210:211],v2 offset:4352
ds_read_b64_tr_b16 a[212:213],v2 offset:8448
v_mfma_f32_16x16x32_bf16 v[200:203],a[128:131],v[144:147],v[40:43]
ds_read_b64_tr_b16 a[214:215],v2 offset:12544
v_cvt_pk_bf16_f32 v138,v140,v141
v_mfma_f32_16x16x32_bf16 v[200:203],a[132:135],v[176:179],v[200:203]
ds_read_b64_tr_b16 a[220:221],v3 offset:8448
v_mfma_f32_16x16x32_bf16 v[200:203],a[136:139],v[208:211],v[200:203]
ds_read_b64_tr_b16 a[222:223],v3 offset:12544
v_cvt_pk_bf16_f32 v139,v142,v143
v_mfma_f32_16x16x32_bf16 v[200:203],a[140:143],v[240:243],v[200:203]
buffer_load_dwordx4 v5,s[24:27],s47 offen offset:3072 nt lds
v_mfma_f32_16x16x32_bf16 v[204:207],a[156:159],v[240:243],v[40:43]
s_add_u32 s47,s47,16384
s_and_b32 s47,s47,0x3ffff
ds_read_b64_tr_b16 a[216:217],v3 offset:256
v_mfma_f32_16x16x32_bf16 v[204:207],a[152:155],v[208:211],v[204:207]
s_or_b32 s47,s47,s46
ds_read_b64_tr_b16 a[218:219],v3 offset:4352
v_cvt_pk_bf16_f32 v140,v152,v153
v_mfma_f32_16x16x32_bf16 v[204:207],a[148:151],v[176:179],v[204:207]
ds_read_b64_tr_b16 a[224:225],v2 offset:512
v_mfma_f32_16x16x32_bf16 v[204:207],a[144:147],v[144:147],v[204:207]
ds_read_b64_tr_b16 a[226:227],v2 offset:4608
v_mfma_f32_16x16x32_bf16 v[216:219],a[160:163],v[144:147],v[40:43]
ds_read_b64_tr_b16 a[228:229],v2 offset:8704
v_cvt_pk_bf16_f32 v141,v154,v155
v_mfma_f32_16x16x32_bf16 v[216:219],a[164:167],v[176:179],v[216:219]
ds_read_b64_tr_b16 a[230:231],v2 offset:12800
v_mfma_f32_16x16x32_bf16 v[216:219],a[168:171],v[208:211],v[216:219]
ds_read_b64_tr_b16 a[236:237],v3 offset:8704
v_mfma_f32_16x16x32_bf16 v[216:219],a[172:175],v[240:243],v[216:219]
ds_read_b64_tr_b16 a[238:239],v3 offset:12800
v_cvt_pk_bf16_f32 v142,v156,v157
v_mfma_f32_16x16x32_bf16 v[220:223],a[188:191],v[240:243],v[40:43]
ds_read_b64_tr_b16 a[232:233],v3 offset:512
v_mfma_f32_16x16x32_bf16 v[220:223],a[184:187],v[208:211],v[220:223]
ds_read_b64_tr_b16 a[234:235],v3 offset:4608
v_mfma_f32_16x16x32_bf16 v[220:223],a[180:183],v[176:179],v[220:223]
ds_read_b64_tr_b16 a[240:241],v2 offset:768
v_cvt_pk_bf16_f32 v143,v158,v159
v_mfma_f32_16x16x32_bf16 v[220:223],a[176:179],v[144:147],v[220:223]
s_waitcnt lgkmcnt(7)
buffer_load_dword v26,v24,s[88:91],s93 offen
v_mfma_f32_16x16x32_bf16 a[0:3],a[192:195],v[136:139],a[0:3]
ds_read_b64_tr_b16 a[242:243],v2 offset:4864
v_mfma_f32_16x16x32_bf16 a[0:3],a[196:199],v[140:143],a[0:3]
ds_read_b64_tr_b16 a[244:245],v2 offset:8960
v_mfma_f32_16x16x32_bf16 a[4:7],a[204:207],v[140:143],a[4:7]
ds_read_b64_tr_b16 a[246:247],v2 offset:13056
v_mfma_f32_16x16x32_bf16 a[4:7],a[200:203],v[136:139],a[4:7]
ds_read_b64_tr_b16 a[252:253],v3 offset:8960
v_mfma_f32_16x16x32_bf16 a[8:11],a[208:211],v[136:139],a[8:11]
ds_read_b64_tr_b16 a[254:255],v3 offset:13056
v_mfma_f32_16x16x32_bf16 a[8:11],a[212:215],v[140:143],a[8:11]
ds_read_b64_tr_b16 a[248:249],v3 offset:768
v_mfma_f32_16x16x32_bf16 a[12:15],a[220:223],v[140:143],a[12:15]
ds_read_b64_tr_b16 a[250:251],v3 offset:4864
v_mfma_f32_16x16x32_bf16 a[12:15],a[216:219],v[136:139],a[12:15]
s_bitcmp1_b32 s45,0
s_cbranch_scc0 L_t31v31_%=
s_mov_b32 m0,s80
s_add_u32 s80,s80,4096
buffer_load_dwordx4 v25,s[24:27],s83 offen nt lds
s_add_u32 s83,s83,4096
L_t31v31_%=:
v_mfma_f32_16x16x32_bf16 a[16:19],a[224:227],v[136:139],a[16:19]
s_waitcnt lgkmcnt(6)
v_exp_f32 v186,v186
v_mfma_f32_16x16x32_bf16 a[16:19],a[228:231],v[140:143],a[16:19]
v_exp_f32 v187,v187
v_mfma_f32_16x16x32_bf16 a[20:23],a[236:239],v[140:143],a[20:23]
v_exp_f32 v188,v188
v_mfma_f32_16x16x32_bf16 a[20:23],a[232:235],v[136:139],a[20:23]
v_exp_f32 v189,v189
v_mfma_f32_16x16x32_bf16 a[24:27],a[240:243],v[136:139],a[24:27]
s_waitcnt lgkmcnt(2)
v_exp_f32 v190,v190
v_mfma_f32_16x16x32_bf16 a[24:27],a[244:247],v[140:143],a[24:27]
v_cvt_pk_bf16_f32 v173,v186,v187
v_mfma_f32_16x16x32_bf16 a[28:31],a[252:255],v[140:143],a[28:31]
s_waitcnt lgkmcnt(0)
v_exp_f32 v191,v191
v_mfma_f32_16x16x32_bf16 a[28:31],a[248:251],v[136:139],a[28:31]
v_cvt_pk_bf16_f32 v174,v188,v189
v_cvt_pk_bf16_f32 v175,v190,v191
s_cmp_lg_u32 s53,0
v_smfmac_f32_16x16x64_bf16 v[48:51],v[28:31],v[136:143],v27
s_cbranch_scc1 L_att1v3_%=
s_waitcnt lgkmcnt(0)
s_nop 7
s_branch L_att1x_%=
.p2align 5
L_att1v0_%=:
v_mfma_f32_16x16x32_bf16 v[232:235],a[128:131],v[148:151],v[44:47]
s_waitcnt vmcnt(8) lgkmcnt(0)
v_exp_f32 v200,v200
v_mfma_f32_16x16x32_bf16 v[232:235],a[132:135],v[180:183],v[232:235]
s_barrier
s_add_u32 m0,s49,s51
ds_read_b128 a[128:131],v1
v_mfma_f32_16x16x32_bf16 v[232:235],a[136:139],v[212:215],v[232:235]
s_mov_b32 s90,L_out_end_%=-L_pc_%=+128
v_exp_f32 v201,v201
v_mfma_f32_16x16x32_bf16 v[232:235],a[140:143],v[244:247],v[232:235]
s_lshr_b32 s45,s45,1
ds_read_b128 a[132:135],v1 offset:256
v_cvt_pk_bf16_f32 v200,v200,v201
v_mfma_f32_16x16x32_bf16 v[236:239],a[156:159],v[244:247],v[44:47]
buffer_load_dwordx4 v4,s[20:23],s47 offen nt lds
v_mfma_f32_16x16x32_bf16 v[236:239],a[152:155],v[212:215],v[236:239]
ds_read_b128 a[136:139],v1 offset:512
v_add_u32 v2,s48,v2
v_mfma_f32_16x16x32_bf16 v[236:239],a[148:151],v[180:183],v[236:239]
v_exp_f32 v202,v202
v_mfma_f32_16x16x32_bf16 v[236:239],a[144:147],v[148:151],v[236:239]
s_add_u32 s62,s50,32768
ds_read_b128 a[140:143],v1 offset:768
v_add_u32 v3,s48,v3
v_mfma_f32_16x16x32_bf16 v[248:251],a[160:163],v[148:151],v[44:47]
s_and_b32 s62,s62,131071
v_exp_f32 v203,v203
v_mfma_f32_16x16x32_bf16 v[248:251],a[164:167],v[180:183],v[248:251]
s_add_u32 s54,s54,16384
ds_read_b128 a[156:159],v1 offset:4864
v_cvt_pk_bf16_f32 v201,v202,v203
v_mfma_f32_16x16x32_bf16 v[248:251],a[168:171],v[212:215],v[248:251]
s_sub_u32 s48,s62,s50
v_exp_f32 v204,v204
v_mfma_f32_16x16x32_bf16 v[248:251],a[172:175],v[244:247],v[248:251]
s_sub_u32 s53,s53,1
v_exp_f32 v205,v205
v_mfma_f32_16x16x32_bf16 v[252:255],a[188:191],v[244:247],v[44:47]
s_and_b32 s46,s54,0xfffc0000
ds_read_b128 a[152:155],v1 offset:4608
v_cvt_pk_bf16_f32 v202,v204,v205
v_mfma_f32_16x16x32_bf16 v[252:255],a[184:187],v[212:215],v[252:255]
s_mov_b32 s50,s62
v_exp_f32 v206,v206
v_mfma_f32_16x16x32_bf16 v[252:255],a[180:183],v[180:183],v[252:255]
v_exp_f32 v207,v207
v_mfma_f32_16x16x32_bf16 v[252:255],a[176:179],v[148:151],v[252:255]
ds_read_b128 a[148:151],v1 offset:4352
v_cvt_pk_bf16_f32 v203,v206,v207
v_mfma_f32_16x16x32_bf16 a[32:35],a[192:195],v[168:171],a[32:35]
v_exp_f32 v216,v216
v_mfma_f32_16x16x32_bf16 a[32:35],a[196:199],v[172:175],a[32:35]
ds_read_b128 a[144:147],v1 offset:4096
v_mfma_f32_16x16x32_bf16 a[36:39],a[204:207],v[172:175],a[36:39]
buffer_load_dwordx4 v4,s[20:23],s47 offen offset:1024 nt lds
v_mfma_f32_16x16x32_bf16 a[36:39],a[200:203],v[168:171],a[36:39]
ds_read_b128 a[160:163],v1 offset:8192
v_mfma_f32_16x16x32_bf16 a[40:43],a[208:211],v[168:171],a[40:43]
v_exp_f32 v217,v217
v_mfma_f32_16x16x32_bf16 a[40:43],a[212:215],v[172:175],a[40:43]
ds_read_b128 a[164:167],v1 offset:8448
v_cvt_pk_bf16_f32 v204,v216,v217
v_mfma_f32_16x16x32_bf16 a[44:47],a[220:223],v[172:175],a[44:47]
v_exp_f32 v218,v218
v_mfma_f32_16x16x32_bf16 a[44:47],a[216:219],v[168:171],a[44:47]
ds_read_b128 a[168:171],v1 offset:8704
v_mfma_f32_16x16x32_bf16 a[48:51],a[224:227],v[168:171],a[48:51]
v_exp_f32 v219,v219
v_mfma_f32_16x16x32_bf16 a[48:51],a[228:231],v[172:175],a[48:51]
ds_read_b128 a[172:175],v1 offset:8960
v_cvt_pk_bf16_f32 v205,v218,v219
v_mfma_f32_16x16x32_bf16 a[52:55],a[236:239],v[172:175],a[52:55]
v_exp_f32 v220,v220
v_mfma_f32_16x16x32_bf16 a[52:55],a[232:235],v[168:171],a[52:55]
ds_read_b128 a[188:191],v1 offset:13056
v_mfma_f32_16x16x32_bf16 a[56:59],a[240:243],v[168:171],a[56:59]
v_exp_f32 v221,v221
v_mfma_f32_16x16x32_bf16 a[56:59],a[244:247],v[172:175],a[56:59]
ds_read_b128 a[184:187],v1 offset:12800
v_cvt_pk_bf16_f32 v206,v220,v221
v_mfma_f32_16x16x32_bf16 a[60:63],a[252:255],v[172:175],a[60:63]
v_exp_f32 v222,v222
v_mfma_f32_16x16x32_bf16 a[60:63],a[248:251],v[168:171],a[60:63]
ds_read_b128 a[180:183],v1 offset:12544
v_smfmac_f32_16x16x64_bf16 v[52:55],v[28:31],v[168:175],v27
buffer_load_dwordx4 v5,s[20:23],s47 offen offset:2048 nt lds
ds_read_b128 a[176:179],v1 offset:12288
v_add_u32 v1,s48,v1
v_exp_f32 v223,v223
s_nop 0
v_cvt_pk_bf16_f32 v207,v222,v223
v_exp_f32 v232,v232
v_exp_f32 v233,v233
v_exp_f32 v234,v234
v_exp_f32 v235,v235
v_exp_f32 v236,v236
v_exp_f32 v237,v237
v_exp_f32 v238,v238
v_exp_f32 v239,v239
v_exp_f32 v248,v248
buffer_load_dwordx4 v5,s[20:23],s47 offen offset:3072 nt lds
v_exp_f32 v249,v249
v_exp_f32 v250,v250
v_mfma_f32_16x16x32_bf16 a[64:67],a[192:195],v[200:203],a[64:67]
v_exp_f32 v251,v251
v_mfma_f32_16x16x32_bf16 a[64:67],a[196:199],v[204:207],a[64:67]
v_exp_f32 v252,v252
v_mfma_f32_16x16x32_bf16 a[68:71],a[204:207],v[204:207],a[68:71]
v_exp_f32 v253,v253
v_mfma_f32_16x16x32_bf16 a[68:71],a[200:203],v[200:203],a[68:71]
v_exp_f32 v254,v254
v_mfma_f32_16x16x32_bf16 a[72:75],a[208:211],v[200:203],a[72:75]
v_exp_f32 v255,v255
v_mfma_f32_16x16x32_bf16 a[72:75],a[212:215],v[204:207],a[72:75]
v_mfma_f32_16x16x32_bf16 a[76:79],a[220:223],v[204:207],a[76:79]
v_mfma_f32_16x16x32_bf16 a[76:79],a[216:219],v[200:203],a[76:79]
v_mfma_f32_16x16x32_bf16 a[80:83],a[224:227],v[200:203],a[80:83]
s_add_u32 m0,s49,s52
v_cvt_pk_bf16_f32 v232,v232,v233
v_cvt_pk_bf16_f32 v233,v234,v235
v_mfma_f32_16x16x32_bf16 a[80:83],a[228:231],v[204:207],a[80:83]
s_xor_b32 s49,s50,65536
v_cvt_pk_bf16_f32 v234,v236,v237
v_cvt_pk_bf16_f32 v235,v238,v239
v_mfma_f32_16x16x32_bf16 a[84:87],a[236:239],v[204:207],a[84:87]
v_cvt_pk_bf16_f32 v236,v248,v249
v_cvt_pk_bf16_f32 v237,v250,v251
v_mfma_f32_16x16x32_bf16 a[84:87],a[232:235],v[200:203],a[84:87]
buffer_load_dwordx4 v4,s[24:27],s47 offen nt lds
v_mfma_f32_16x16x32_bf16 a[88:91],a[240:243],v[200:203],a[88:91]
v_cvt_pk_bf16_f32 v238,v252,v253
v_cvt_pk_bf16_f32 v239,v254,v255
v_mfma_f32_16x16x32_bf16 a[88:91],a[244:247],v[204:207],a[88:91]
v_mfma_f32_16x16x32_bf16 a[92:95],a[252:255],v[204:207],a[92:95]
v_mfma_f32_16x16x32_bf16 a[92:95],a[248:251],v[200:203],a[92:95]
v_smfmac_f32_16x16x64_bf16 v[56:59],v[28:31],v[200:207],v27
s_waitcnt lgkmcnt(0)
v_mfma_f32_16x16x32_bf16 v[168:171],a[128:131],v[132:135],v[36:39]
v_mfma_f32_16x16x32_bf16 v[168:171],a[132:135],v[164:167],v[168:171]
v_mfma_f32_16x16x32_bf16 v[168:171],a[136:139],v[196:199],v[168:171]
v_mfma_f32_16x16x32_bf16 v[168:171],a[140:143],v[228:231],v[168:171]
v_mfma_f32_16x16x32_bf16 v[172:175],a[156:159],v[228:231],v[36:39]
v_mfma_f32_16x16x32_bf16 v[172:175],a[152:155],v[196:199],v[172:175]
v_mfma_f32_16x16x32_bf16 v[172:175],a[148:151],v[164:167],v[172:175]
v_mfma_f32_16x16x32_bf16 v[172:175],a[144:147],v[132:135],v[172:175]
v_mfma_f32_16x16x32_bf16 v[184:187],a[160:163],v[132:135],v[36:39]
buffer_load_dwordx4 v5,s[24:27],s47 offen offset:1024 nt lds
)A"
R"A(v_mfma_f32_16x16x32_bf16 v[184:187],a[164:167],v[164:167],v[184:187]
v_mfma_f32_16x16x32_bf16 v[184:187],a[168:171],v[196:199],v[184:187]
v_mfma_f32_16x16x32_bf16 v[184:187],a[172:175],v[228:231],v[184:187]
v_exp_f32 v168,v168
v_mfma_f32_16x16x32_bf16 v[188:191],a[188:191],v[228:231],v[36:39]
v_exp_f32 v169,v169
v_mfma_f32_16x16x32_bf16 v[188:191],a[184:187],v[196:199],v[188:191]
v_exp_f32 v170,v170
v_mfma_f32_16x16x32_bf16 v[188:191],a[180:183],v[164:167],v[188:191]
v_exp_f32 v171,v171
v_mfma_f32_16x16x32_bf16 v[188:191],a[176:179],v[132:135],v[188:191]
v_exp_f32 v172,v172
v_mfma_f32_16x16x32_bf16 a[96:99],a[192:195],v[232:235],a[96:99]
ds_read_b64_tr_b16 a[192:193],v2
v_cvt_pk_bf16_f32 v168,v168,v169
v_mfma_f32_16x16x32_bf16 a[96:99],a[196:199],v[236:239],a[96:99]
ds_read_b64_tr_b16 a[194:195],v2 offset:4096
v_cvt_pk_bf16_f32 v169,v170,v171
v_mfma_f32_16x16x32_bf16 a[100:103],a[204:207],v[236:239],a[100:103]
v_exp_f32 v173,v173
v_mfma_f32_16x16x32_bf16 a[100:103],a[200:203],v[232:235],a[100:103]
ds_read_b64_tr_b16 a[196:197],v2 offset:8192
v_cvt_pk_bf16_f32 v170,v172,v173
v_mfma_f32_16x16x32_bf16 a[104:107],a[208:211],v[232:235],a[104:107]
v_exp_f32 v174,v174
v_mfma_f32_16x16x32_bf16 a[104:107],a[212:215],v[236:239],a[104:107]
v_exp_f32 v175,v175
v_mfma_f32_16x16x32_bf16 a[108:111],a[220:223],v[236:239],a[108:111]
buffer_load_dwordx4 v4,s[24:27],s47 offen offset:2048 nt lds
v_mfma_f32_16x16x32_bf16 a[108:111],a[216:219],v[232:235],a[108:111]
ds_read_b64_tr_b16 a[198:199],v2 offset:12288
v_cvt_pk_bf16_f32 v171,v174,v175
v_mfma_f32_16x16x32_bf16 a[112:115],a[224:227],v[232:235],a[112:115]
v_exp_f32 v184,v184
v_mfma_f32_16x16x32_bf16 a[112:115],a[228:231],v[236:239],a[112:115]
v_exp_f32 v185,v185
v_mfma_f32_16x16x32_bf16 a[116:119],a[236:239],v[236:239],a[116:119]
ds_read_b64_tr_b16 a[204:205],v3 offset:8192
v_mfma_f32_16x16x32_bf16 a[116:119],a[232:235],v[232:235],a[116:119]
ds_read_b64_tr_b16 a[206:207],v3 offset:12288
v_cvt_pk_bf16_f32 v172,v184,v185
v_mfma_f32_16x16x32_bf16 a[120:123],a[240:243],v[232:235],a[120:123]
ds_read_b64_tr_b16 a[200:201],v3
v_mfma_f32_16x16x32_bf16 a[120:123],a[244:247],v[236:239],a[120:123]
ds_read_b64_tr_b16 a[202:203],v3 offset:4096
v_mfma_f32_16x16x32_bf16 a[124:127],a[252:255],v[236:239],a[124:127]
ds_read_b64_tr_b16 a[208:209],v2 offset:256
v_mfma_f32_16x16x32_bf16 a[124:127],a[248:251],v[232:235],a[124:127]
ds_read_b64_tr_b16 a[210:211],v2 offset:4352
v_smfmac_f32_16x16x64_bf16 v[60:63],v[28:31],v[232:239],v27
ds_read_b64_tr_b16 a[212:213],v2 offset:8448
v_mfma_f32_16x16x32_bf16 v[200:203],a[128:131],v[144:147],v[40:43]
ds_read_b64_tr_b16 a[214:215],v2 offset:12544
v_mfma_f32_16x16x32_bf16 v[200:203],a[132:135],v[176:179],v[200:203]
ds_read_b64_tr_b16 a[220:221],v3 offset:8448
v_mfma_f32_16x16x32_bf16 v[200:203],a[136:139],v[208:211],v[200:203]
ds_read_b64_tr_b16 a[222:223],v3 offset:12544
v_mfma_f32_16x16x32_bf16 v[200:203],a[140:143],v[240:243],v[200:203]
buffer_load_dwordx4 v5,s[24:27],s47 offen offset:3072 nt lds
v_mfma_f32_16x16x32_bf16 v[204:207],a[156:159],v[240:243],v[40:43]
s_add_u32 s47,s47,16384
s_and_b32 s47,s47,0x3ffff
ds_read_b64_tr_b16 a[216:217],v3 offset:256
v_mfma_f32_16x16x32_bf16 v[204:207],a[152:155],v[208:211],v[204:207]
s_or_b32 s47,s47,s46
ds_read_b64_tr_b16 a[218:219],v3 offset:4352
v_mfma_f32_16x16x32_bf16 v[204:207],a[148:151],v[176:179],v[204:207]
ds_read_b64_tr_b16 a[224:225],v2 offset:512
v_mfma_f32_16x16x32_bf16 v[204:207],a[144:147],v[144:147],v[204:207]
ds_read_b64_tr_b16 a[226:227],v2 offset:4608
v_mfma_f32_16x16x32_bf16 v[216:219],a[160:163],v[144:147],v[40:43]
ds_read_b64_tr_b16 a[228:229],v2 offset:8704
v_mfma_f32_16x16x32_bf16 v[216:219],a[164:167],v[176:179],v[216:219]
ds_read_b64_tr_b16 a[230:231],v2 offset:12800
v_mfma_f32_16x16x32_bf16 v[216:219],a[168:171],v[208:211],v[216:219]
ds_read_b64_tr_b16 a[236:237],v3 offset:8704
v_mfma_f32_16x16x32_bf16 v[216:219],a[172:175],v[240:243],v[216:219]
ds_read_b64_tr_b16 a[238:239],v3 offset:12800
v_mfma_f32_16x16x32_bf16 v[220:223],a[188:191],v[240:243],v[40:43]
ds_read_b64_tr_b16 a[232:233],v3 offset:512
v_mfma_f32_16x16x32_bf16 v[220:223],a[184:187],v[208:211],v[220:223]
ds_read_b64_tr_b16 a[234:235],v3 offset:4608
v_mfma_f32_16x16x32_bf16 v[220:223],a[180:183],v[176:179],v[220:223]
ds_read_b64_tr_b16 a[240:241],v2 offset:768
v_mfma_f32_16x16x32_bf16 v[220:223],a[176:179],v[144:147],v[220:223]
buffer_load_dword v26,v24,s[88:91],s93 offen
ds_read_b64_tr_b16 a[242:243],v2 offset:4864
ds_read_b64_tr_b16 a[244:245],v2 offset:8960
ds_read_b64_tr_b16 a[246:247],v2 offset:13056
ds_read_b64_tr_b16 a[252:253],v3 offset:8960
ds_read_b64_tr_b16 a[254:255],v3 offset:13056
ds_read_b64_tr_b16 a[248:249],v3 offset:768
ds_read_b64_tr_b16 a[250:251],v3 offset:4864
s_bitcmp1_b32 s45,0
s_cbranch_scc0 L_t31v01_%=
s_mov_b32 m0,s80
s_add_u32 s80,s80,4096
buffer_load_dwordx4 v25,s[24:27],s83 offen nt lds
s_add_u32 s83,s83,4096
L_t31v01_%=:
v_exp_f32 v186,v186
v_exp_f32 v187,v187
v_exp_f32 v188,v188
v_exp_f32 v189,v189
v_exp_f32 v190,v190
v_cvt_pk_bf16_f32 v173,v186,v187
v_exp_f32 v191,v191
v_cvt_pk_bf16_f32 v174,v188,v189
v_cvt_pk_bf16_f32 v175,v190,v191
s_cmp_lg_u32 s53,0
s_cbranch_scc1 L_att1v0_%=
s_waitcnt lgkmcnt(0)
s_nop 7
s_branch L_att1x_%=
.p2align 5
L_att1v1_%=:
v_mfma_f32_16x16x32_bf16 v[232:235],a[128:131],v[148:151],v[44:47]
s_waitcnt vmcnt(8) lgkmcnt(0)
v_exp_f32 v200,v200
v_mfma_f32_16x16x32_bf16 v[232:235],a[132:135],v[180:183],v[232:235]
s_barrier
s_add_u32 m0,s49,s51
ds_read_b128 a[128:131],v1
v_mfma_f32_16x16x32_bf16 v[232:235],a[136:139],v[212:215],v[232:235]
s_mov_b32 s90,L_out_end_%=-L_pc_%=+128
v_exp_f32 v201,v201
v_mfma_f32_16x16x32_bf16 v[232:235],a[140:143],v[244:247],v[232:235]
s_lshr_b32 s45,s45,1
ds_read_b128 a[132:135],v1 offset:256
v_cvt_pk_bf16_f32 v200,v200,v201
v_mfma_f32_16x16x32_bf16 v[236:239],a[156:159],v[244:247],v[44:47]
buffer_load_dwordx4 v4,s[20:23],s47 offen nt lds
v_mfma_f32_16x16x32_bf16 v[236:239],a[152:155],v[212:215],v[236:239]
ds_read_b128 a[136:139],v1 offset:512
v_add_u32 v2,s48,v2
v_mfma_f32_16x16x32_bf16 v[236:239],a[148:151],v[180:183],v[236:239]
v_exp_f32 v202,v202
v_mfma_f32_16x16x32_bf16 v[236:239],a[144:147],v[148:151],v[236:239]
s_add_u32 s62,s50,32768
ds_read_b128 a[140:143],v1 offset:768
v_add_u32 v3,s48,v3
v_mfma_f32_16x16x32_bf16 v[248:251],a[160:163],v[148:151],v[44:47]
s_and_b32 s62,s62,131071
v_exp_f32 v203,v203
v_mfma_f32_16x16x32_bf16 v[248:251],a[164:167],v[180:183],v[248:251]
s_add_u32 s54,s54,16384
ds_read_b128 a[156:159],v1 offset:4864
v_cvt_pk_bf16_f32 v201,v202,v203
v_mfma_f32_16x16x32_bf16 v[248:251],a[168:171],v[212:215],v[248:251]
s_sub_u32 s48,s62,s50
v_exp_f32 v204,v204
v_mfma_f32_16x16x32_bf16 v[248:251],a[172:175],v[244:247],v[248:251]
s_sub_u32 s53,s53,1
v_exp_f32 v205,v205
v_mfma_f32_16x16x32_bf16 v[252:255],a[188:191],v[244:247],v[44:47]
s_and_b32 s46,s54,0xfffc0000
ds_read_b128 a[152:155],v1 offset:4608
v_cvt_pk_bf16_f32 v202,v204,v205
v_mfma_f32_16x16x32_bf16 v[252:255],a[184:187],v[212:215],v[252:255]
s_mov_b32 s50,s62
v_exp_f32 v206,v206
v_mfma_f32_16x16x32_bf16 v[252:255],a[180:183],v[180:183],v[252:255]
v_exp_f32 v207,v207
v_mfma_f32_16x16x32_bf16 v[252:255],a[176:179],v[148:151],v[252:255]
s_waitcnt lgkmcnt(2)
ds_read_b128 a[148:151],v1 offset:4352
v_cvt_pk_bf16_f32 v203,v206,v207
v_exp_f32 v216,v216
ds_read_b128 a[144:147],v1 offset:4096
buffer_load_dwordx4 v4,s[20:23],s47 offen offset:1024 nt lds
ds_read_b128 a[160:163],v1 offset:8192
v_exp_f32 v217,v217
ds_read_b128 a[164:167],v1 offset:8448
v_cvt_pk_bf16_f32 v204,v216,v217
v_exp_f32 v218,v218
ds_read_b128 a[168:171],v1 offset:8704
v_exp_f32 v219,v219
ds_read_b128 a[172:175],v1 offset:8960
v_cvt_pk_bf16_f32 v205,v218,v219
v_exp_f32 v220,v220
ds_read_b128 a[188:191],v1 offset:13056
v_exp_f32 v221,v221
ds_read_b128 a[184:187],v1 offset:12800
v_cvt_pk_bf16_f32 v206,v220,v221
v_exp_f32 v222,v222
ds_read_b128 a[180:183],v1 offset:12544
v_mfma_f32_16x16x32_bf16 v[136:139],a[128:131],v[128:131],v[32:35]
buffer_load_dwordx4 v5,s[20:23],s47 offen offset:2048 nt lds
v_mfma_f32_16x16x32_bf16 v[136:139],a[132:135],v[160:163],v[136:139]
ds_read_b128 a[176:179],v1 offset:12288
v_add_u32 v1,s48,v1
v_mfma_f32_16x16x32_bf16 v[136:139],a[136:139],v[192:195],v[136:139]
v_exp_f32 v223,v223
v_mfma_f32_16x16x32_bf16 v[136:139],a[140:143],v[224:227],v[136:139]
s_waitcnt lgkmcnt(11)
v_cvt_pk_bf16_f32 v207,v222,v223
v_mfma_f32_16x16x32_bf16 v[140:143],a[156:159],v[224:227],v[32:35]
s_waitcnt lgkmcnt(10)
v_exp_f32 v232,v232
v_mfma_f32_16x16x32_bf16 v[140:143],a[152:155],v[192:195],v[140:143]
s_waitcnt lgkmcnt(9)
v_exp_f32 v233,v233
v_mfma_f32_16x16x32_bf16 v[140:143],a[148:151],v[160:163],v[140:143]
s_waitcnt lgkmcnt(8)
v_exp_f32 v234,v234
v_mfma_f32_16x16x32_bf16 v[140:143],a[144:147],v[128:131],v[140:143]
s_waitcnt lgkmcnt(1)
v_exp_f32 v235,v235
v_mfma_f32_16x16x32_bf16 v[152:155],a[160:163],v[128:131],v[32:35]
v_exp_f32 v236,v236
v_mfma_f32_16x16x32_bf16 v[152:155],a[164:167],v[160:163],v[152:155]
v_exp_f32 v237,v237
v_mfma_f32_16x16x32_bf16 v[152:155],a[168:171],v[192:195],v[152:155]
v_exp_f32 v238,v238
v_mfma_f32_16x16x32_bf16 v[152:155],a[172:175],v[224:227],v[152:155]
v_exp_f32 v239,v239
v_mfma_f32_16x16x32_bf16 v[156:159],a[188:191],v[224:227],v[32:35]
v_exp_f32 v248,v248
v_mfma_f32_16x16x32_bf16 v[156:159],a[184:187],v[192:195],v[156:159]
buffer_load_dwordx4 v5,s[20:23],s47 offen offset:3072 nt lds
v_mfma_f32_16x16x32_bf16 v[156:159],a[180:183],v[160:163],v[156:159]
s_waitcnt lgkmcnt(0)
v_exp_f32 v249,v249
v_mfma_f32_16x16x32_bf16 v[156:159],a[176:179],v[128:131],v[156:159]
v_exp_f32 v250,v250
v_mfma_f32_16x16x32_bf16 a[64:67],a[192:195],v[200:203],a[64:67]
v_exp_f32 v251,v251
v_mfma_f32_16x16x32_bf16 a[64:67],a[196:199],v[204:207],a[64:67]
v_exp_f32 v252,v252
v_mfma_f32_16x16x32_bf16 a[68:71],a[204:207],v[204:207],a[68:71]
v_exp_f32 v253,v253
v_mfma_f32_16x16x32_bf16 a[68:71],a[200:203],v[200:203],a[68:71]
v_exp_f32 v254,v254
v_mfma_f32_16x16x32_bf16 a[72:75],a[208:211],v[200:203],a[72:75]
v_exp_f32 v255,v255
v_mfma_f32_16x16x32_bf16 a[72:75],a[212:215],v[204:207],a[72:75]
v_exp_f32 v136,v136
v_mfma_f32_16x16x32_bf16 a[76:79],a[220:223],v[204:207],a[76:79]
v_exp_f32 v137,v137
v_mfma_f32_16x16x32_bf16 a[76:79],a[216:219],v[200:203],a[76:79]
v_exp_f32 v138,v138
v_mfma_f32_16x16x32_bf16 a[80:83],a[224:227],v[200:203],a[80:83]
s_add_u32 m0,s49,s52
v_cvt_pk_bf16_f32 v232,v232,v233
v_cvt_pk_bf16_f32 v233,v234,v235
v_mfma_f32_16x16x32_bf16 a[80:83],a[228:231],v[204:207],a[80:83]
s_xor_b32 s49,s50,65536
v_cvt_pk_bf16_f32 v234,v236,v237
v_cvt_pk_bf16_f32 v235,v238,v239
v_mfma_f32_16x16x32_bf16 a[84:87],a[236:239],v[204:207],a[84:87]
v_cvt_pk_bf16_f32 v236,v248,v249
v_cvt_pk_bf16_f32 v237,v250,v251
v_mfma_f32_16x16x32_bf16 a[84:87],a[232:235],v[200:203],a[84:87]
buffer_load_dwordx4 v4,s[24:27],s47 offen nt lds
v_mfma_f32_16x16x32_bf16 a[88:91],a[240:243],v[200:203],a[88:91]
v_cvt_pk_bf16_f32 v238,v252,v253
v_cvt_pk_bf16_f32 v239,v254,v255
v_mfma_f32_16x16x32_bf16 a[88:91],a[244:247],v[204:207],a[88:91]
v_exp_f32 v139,v139
v_mfma_f32_16x16x32_bf16 a[92:95],a[252:255],v[204:207],a[92:95]
v_exp_f32 v140,v140
v_mfma_f32_16x16x32_bf16 a[92:95],a[248:251],v[200:203],a[92:95]
v_exp_f32 v141,v141
v_smfmac_f32_16x16x64_bf16 v[56:59],v[28:31],v[200:207],v27
v_exp_f32 v142,v142
v_exp_f32 v143,v143
v_exp_f32 v152,v152
v_exp_f32 v153,v153
v_exp_f32 v154,v154
v_exp_f32 v155,v155
v_exp_f32 v156,v156
v_exp_f32 v157,v157
buffer_load_dwordx4 v5,s[24:27],s47 offen offset:1024 nt lds
v_exp_f32 v158,v158
v_exp_f32 v159,v159
v_mfma_f32_16x16x32_bf16 a[96:99],a[192:195],v[232:235],a[96:99]
ds_read_b64_tr_b16 a[192:193],v2
v_mfma_f32_16x16x32_bf16 a[96:99],a[196:199],v[236:239],a[96:99]
ds_read_b64_tr_b16 a[194:195],v2 offset:4096
v_mfma_f32_16x16x32_bf16 a[100:103],a[204:207],v[236:239],a[100:103]
v_mfma_f32_16x16x32_bf16 a[100:103],a[200:203],v[232:235],a[100:103]
ds_read_b64_tr_b16 a[196:197],v2 offset:8192
v_mfma_f32_16x16x32_bf16 a[104:107],a[208:211],v[232:235],a[104:107]
v_mfma_f32_16x16x32_bf16 a[104:107],a[212:215],v[236:239],a[104:107]
)A"
R"A(v_mfma_f32_16x16x32_bf16 a[108:111],a[220:223],v[236:239],a[108:111]
buffer_load_dwordx4 v4,s[24:27],s47 offen offset:2048 nt lds
v_mfma_f32_16x16x32_bf16 a[108:111],a[216:219],v[232:235],a[108:111]
ds_read_b64_tr_b16 a[198:199],v2 offset:12288
v_mfma_f32_16x16x32_bf16 a[112:115],a[224:227],v[232:235],a[112:115]
v_mfma_f32_16x16x32_bf16 a[112:115],a[228:231],v[236:239],a[112:115]
v_mfma_f32_16x16x32_bf16 a[116:119],a[236:239],v[236:239],a[116:119]
ds_read_b64_tr_b16 a[204:205],v3 offset:8192
v_cvt_pk_bf16_f32 v136,v136,v137
v_mfma_f32_16x16x32_bf16 a[116:119],a[232:235],v[232:235],a[116:119]
ds_read_b64_tr_b16 a[206:207],v3 offset:12288
v_mfma_f32_16x16x32_bf16 a[120:123],a[240:243],v[232:235],a[120:123]
ds_read_b64_tr_b16 a[200:201],v3
v_mfma_f32_16x16x32_bf16 a[120:123],a[244:247],v[236:239],a[120:123]
ds_read_b64_tr_b16 a[202:203],v3 offset:4096
v_cvt_pk_bf16_f32 v137,v138,v139
v_mfma_f32_16x16x32_bf16 a[124:127],a[252:255],v[236:239],a[124:127]
ds_read_b64_tr_b16 a[208:209],v2 offset:256
v_mfma_f32_16x16x32_bf16 a[124:127],a[248:251],v[232:235],a[124:127]
ds_read_b64_tr_b16 a[210:211],v2 offset:4352
v_smfmac_f32_16x16x64_bf16 v[60:63],v[28:31],v[232:239],v27
ds_read_b64_tr_b16 a[212:213],v2 offset:8448
v_mfma_f32_16x16x32_bf16 v[200:203],a[128:131],v[144:147],v[40:43]
ds_read_b64_tr_b16 a[214:215],v2 offset:12544
v_cvt_pk_bf16_f32 v138,v140,v141
v_mfma_f32_16x16x32_bf16 v[200:203],a[132:135],v[176:179],v[200:203]
ds_read_b64_tr_b16 a[220:221],v3 offset:8448
v_mfma_f32_16x16x32_bf16 v[200:203],a[136:139],v[208:211],v[200:203]
ds_read_b64_tr_b16 a[222:223],v3 offset:12544
v_cvt_pk_bf16_f32 v139,v142,v143
v_mfma_f32_16x16x32_bf16 v[200:203],a[140:143],v[240:243],v[200:203]
buffer_load_dwordx4 v5,s[24:27],s47 offen offset:3072 nt lds
v_mfma_f32_16x16x32_bf16 v[204:207],a[156:159],v[240:243],v[40:43]
s_add_u32 s47,s47,16384
s_and_b32 s47,s47,0x3ffff
ds_read_b64_tr_b16 a[216:217],v3 offset:256
v_mfma_f32_16x16x32_bf16 v[204:207],a[152:155],v[208:211],v[204:207]
s_or_b32 s47,s47,s46
ds_read_b64_tr_b16 a[218:219],v3 offset:4352
v_cvt_pk_bf16_f32 v140,v152,v153
v_mfma_f32_16x16x32_bf16 v[204:207],a[148:151],v[176:179],v[204:207]
ds_read_b64_tr_b16 a[224:225],v2 offset:512
v_mfma_f32_16x16x32_bf16 v[204:207],a[144:147],v[144:147],v[204:207]
ds_read_b64_tr_b16 a[226:227],v2 offset:4608
v_mfma_f32_16x16x32_bf16 v[216:219],a[160:163],v[144:147],v[40:43]
ds_read_b64_tr_b16 a[228:229],v2 offset:8704
v_cvt_pk_bf16_f32 v141,v154,v155
v_mfma_f32_16x16x32_bf16 v[216:219],a[164:167],v[176:179],v[216:219]
ds_read_b64_tr_b16 a[230:231],v2 offset:12800
v_mfma_f32_16x16x32_bf16 v[216:219],a[168:171],v[208:211],v[216:219]
ds_read_b64_tr_b16 a[236:237],v3 offset:8704
v_mfma_f32_16x16x32_bf16 v[216:219],a[172:175],v[240:243],v[216:219]
ds_read_b64_tr_b16 a[238:239],v3 offset:12800
v_cvt_pk_bf16_f32 v142,v156,v157
v_mfma_f32_16x16x32_bf16 v[220:223],a[188:191],v[240:243],v[40:43]
ds_read_b64_tr_b16 a[232:233],v3 offset:512
v_mfma_f32_16x16x32_bf16 v[220:223],a[184:187],v[208:211],v[220:223]
ds_read_b64_tr_b16 a[234:235],v3 offset:4608
v_mfma_f32_16x16x32_bf16 v[220:223],a[180:183],v[176:179],v[220:223]
ds_read_b64_tr_b16 a[240:241],v2 offset:768
v_cvt_pk_bf16_f32 v143,v158,v159
v_mfma_f32_16x16x32_bf16 v[220:223],a[176:179],v[144:147],v[220:223]
s_waitcnt lgkmcnt(7)
buffer_load_dword v26,v24,s[88:91],s93 offen
v_mfma_f32_16x16x32_bf16 a[0:3],a[192:195],v[136:139],a[0:3]
ds_read_b64_tr_b16 a[242:243],v2 offset:4864
v_mfma_f32_16x16x32_bf16 a[0:3],a[196:199],v[140:143],a[0:3]
ds_read_b64_tr_b16 a[244:245],v2 offset:8960
v_mfma_f32_16x16x32_bf16 a[4:7],a[204:207],v[140:143],a[4:7]
ds_read_b64_tr_b16 a[246:247],v2 offset:13056
v_mfma_f32_16x16x32_bf16 a[4:7],a[200:203],v[136:139],a[4:7]
ds_read_b64_tr_b16 a[252:253],v3 offset:8960
v_mfma_f32_16x16x32_bf16 a[8:11],a[208:211],v[136:139],a[8:11]
ds_read_b64_tr_b16 a[254:255],v3 offset:13056
v_mfma_f32_16x16x32_bf16 a[8:11],a[212:215],v[140:143],a[8:11]
ds_read_b64_tr_b16 a[248:249],v3 offset:768
v_mfma_f32_16x16x32_bf16 a[12:15],a[220:223],v[140:143],a[12:15]
ds_read_b64_tr_b16 a[250:251],v3 offset:4864
v_mfma_f32_16x16x32_bf16 a[12:15],a[216:219],v[136:139],a[12:15]
s_bitcmp1_b32 s45,0
s_cbranch_scc0 L_t31v11_%=
s_mov_b32 m0,s80
s_add_u32 s80,s80,4096
buffer_load_dwordx4 v25,s[24:27],s83 offen nt lds
s_add_u32 s83,s83,4096
L_t31v11_%=:
v_mfma_f32_16x16x32_bf16 a[16:19],a[224:227],v[136:139],a[16:19]
s_waitcnt lgkmcnt(6)
v_mfma_f32_16x16x32_bf16 a[16:19],a[228:231],v[140:143],a[16:19]
v_mfma_f32_16x16x32_bf16 a[20:23],a[236:239],v[140:143],a[20:23]
v_mfma_f32_16x16x32_bf16 a[20:23],a[232:235],v[136:139],a[20:23]
v_mfma_f32_16x16x32_bf16 a[24:27],a[240:243],v[136:139],a[24:27]
s_waitcnt lgkmcnt(2)
v_mfma_f32_16x16x32_bf16 a[24:27],a[244:247],v[140:143],a[24:27]
v_mfma_f32_16x16x32_bf16 a[28:31],a[252:255],v[140:143],a[28:31]
s_waitcnt lgkmcnt(0)
v_mfma_f32_16x16x32_bf16 a[28:31],a[248:251],v[136:139],a[28:31]
s_cmp_lg_u32 s53,0
v_smfmac_f32_16x16x64_bf16 v[48:51],v[28:31],v[136:143],v27
s_cbranch_scc1 L_att1v1_%=
s_waitcnt lgkmcnt(0)
s_nop 7
s_branch L_att1x_%=
.p2align 5
L_att1v2_%=:
v_mfma_f32_16x16x32_bf16 v[232:235],a[128:131],v[148:151],v[44:47]
s_waitcnt vmcnt(8) lgkmcnt(0)
v_mfma_f32_16x16x32_bf16 v[232:235],a[132:135],v[180:183],v[232:235]
s_barrier
s_add_u32 m0,s49,s51
ds_read_b128 a[128:131],v1
v_mfma_f32_16x16x32_bf16 v[232:235],a[136:139],v[212:215],v[232:235]
s_mov_b32 s90,L_out_end_%=-L_pc_%=+128
v_mfma_f32_16x16x32_bf16 v[232:235],a[140:143],v[244:247],v[232:235]
s_lshr_b32 s45,s45,1
ds_read_b128 a[132:135],v1 offset:256
v_mfma_f32_16x16x32_bf16 v[236:239],a[156:159],v[244:247],v[44:47]
buffer_load_dwordx4 v4,s[20:23],s47 offen nt lds
v_mfma_f32_16x16x32_bf16 v[236:239],a[152:155],v[212:215],v[236:239]
ds_read_b128 a[136:139],v1 offset:512
v_add_u32 v2,s48,v2
v_mfma_f32_16x16x32_bf16 v[236:239],a[148:151],v[180:183],v[236:239]
v_mfma_f32_16x16x32_bf16 v[236:239],a[144:147],v[148:151],v[236:239]
s_add_u32 s62,s50,32768
ds_read_b128 a[140:143],v1 offset:768
v_add_u32 v3,s48,v3
v_mfma_f32_16x16x32_bf16 v[248:251],a[160:163],v[148:151],v[44:47]
s_and_b32 s62,s62,131071
v_mfma_f32_16x16x32_bf16 v[248:251],a[164:167],v[180:183],v[248:251]
s_add_u32 s54,s54,16384
ds_read_b128 a[156:159],v1 offset:4864
v_mfma_f32_16x16x32_bf16 v[248:251],a[168:171],v[212:215],v[248:251]
s_sub_u32 s48,s62,s50
v_mfma_f32_16x16x32_bf16 v[248:251],a[172:175],v[244:247],v[248:251]
s_sub_u32 s53,s53,1
v_mfma_f32_16x16x32_bf16 v[252:255],a[188:191],v[244:247],v[44:47]
s_and_b32 s46,s54,0xfffc0000
ds_read_b128 a[152:155],v1 offset:4608
v_mfma_f32_16x16x32_bf16 v[252:255],a[184:187],v[212:215],v[252:255]
s_mov_b32 s50,s62
v_mfma_f32_16x16x32_bf16 v[252:255],a[180:183],v[180:183],v[252:255]
v_mfma_f32_16x16x32_bf16 v[252:255],a[176:179],v[148:151],v[252:255]
ds_read_b128 a[148:151],v1 offset:4352
v_mfma_f32_16x16x32_bf16 a[32:35],a[192:195],v[168:171],a[32:35]
v_mfma_f32_16x16x32_bf16 a[32:35],a[196:199],v[172:175],a[32:35]
ds_read_b128 a[144:147],v1 offset:4096
v_mfma_f32_16x16x32_bf16 a[36:39],a[204:207],v[172:175],a[36:39]
buffer_load_dwordx4 v4,s[20:23],s47 offen offset:1024 nt lds
v_mfma_f32_16x16x32_bf16 a[36:39],a[200:203],v[168:171],a[36:39]
ds_read_b128 a[160:163],v1 offset:8192
v_mfma_f32_16x16x32_bf16 a[40:43],a[208:211],v[168:171],a[40:43]
v_mfma_f32_16x16x32_bf16 a[40:43],a[212:215],v[172:175],a[40:43]
ds_read_b128 a[164:167],v1 offset:8448
v_mfma_f32_16x16x32_bf16 a[44:47],a[220:223],v[172:175],a[44:47]
v_mfma_f32_16x16x32_bf16 a[44:47],a[216:219],v[168:171],a[44:47]
ds_read_b128 a[168:171],v1 offset:8704
v_mfma_f32_16x16x32_bf16 a[48:51],a[224:227],v[168:171],a[48:51]
v_mfma_f32_16x16x32_bf16 a[48:51],a[228:231],v[172:175],a[48:51]
ds_read_b128 a[172:175],v1 offset:8960
v_mfma_f32_16x16x32_bf16 a[52:55],a[236:239],v[172:175],a[52:55]
v_mfma_f32_16x16x32_bf16 a[52:55],a[232:235],v[168:171],a[52:55]
ds_read_b128 a[188:191],v1 offset:13056
v_mfma_f32_16x16x32_bf16 a[56:59],a[240:243],v[168:171],a[56:59]
v_mfma_f32_16x16x32_bf16 a[56:59],a[244:247],v[172:175],a[56:59]
ds_read_b128 a[184:187],v1 offset:12800
v_mfma_f32_16x16x32_bf16 a[60:63],a[252:255],v[172:175],a[60:63]
v_mfma_f32_16x16x32_bf16 a[60:63],a[248:251],v[168:171],a[60:63]
ds_read_b128 a[180:183],v1 offset:12544
v_smfmac_f32_16x16x64_bf16 v[52:55],v[28:31],v[168:175],v27
s_waitcnt lgkmcnt(4)
v_mfma_f32_16x16x32_bf16 v[136:139],a[128:131],v[128:131],v[32:35]
buffer_load_dwordx4 v5,s[20:23],s47 offen offset:2048 nt lds
v_mfma_f32_16x16x32_bf16 v[136:139],a[132:135],v[160:163],v[136:139]
ds_read_b128 a[176:179],v1 offset:12288
v_add_u32 v1,s48,v1
v_mfma_f32_16x16x32_bf16 v[136:139],a[136:139],v[192:195],v[136:139]
v_mfma_f32_16x16x32_bf16 v[136:139],a[140:143],v[224:227],v[136:139]
v_mfma_f32_16x16x32_bf16 v[140:143],a[156:159],v[224:227],v[32:35]
v_exp_f32 v232,v232
v_mfma_f32_16x16x32_bf16 v[140:143],a[152:155],v[192:195],v[140:143]
v_exp_f32 v233,v233
v_mfma_f32_16x16x32_bf16 v[140:143],a[148:151],v[160:163],v[140:143]
v_exp_f32 v234,v234
v_mfma_f32_16x16x32_bf16 v[140:143],a[144:147],v[128:131],v[140:143]
v_exp_f32 v235,v235
v_mfma_f32_16x16x32_bf16 v[152:155],a[160:163],v[128:131],v[32:35]
v_exp_f32 v236,v236
v_mfma_f32_16x16x32_bf16 v[152:155],a[164:167],v[160:163],v[152:155]
v_exp_f32 v237,v237
v_mfma_f32_16x16x32_bf16 v[152:155],a[168:171],v[192:195],v[152:155]
s_waitcnt lgkmcnt(0)
v_exp_f32 v238,v238
v_mfma_f32_16x16x32_bf16 v[152:155],a[172:175],v[224:227],v[152:155]
v_exp_f32 v239,v239
v_mfma_f32_16x16x32_bf16 v[156:159],a[188:191],v[224:227],v[32:35]
v_exp_f32 v248,v248
v_mfma_f32_16x16x32_bf16 v[156:159],a[184:187],v[192:195],v[156:159]
buffer_load_dwordx4 v5,s[20:23],s47 offen offset:3072 nt lds
v_mfma_f32_16x16x32_bf16 v[156:159],a[180:183],v[160:163],v[156:159]
v_exp_f32 v249,v249
v_mfma_f32_16x16x32_bf16 v[156:159],a[176:179],v[128:131],v[156:159]
v_exp_f32 v250,v250
v_exp_f32 v251,v251
v_exp_f32 v252,v252
v_exp_f32 v253,v253
v_exp_f32 v254,v254
v_exp_f32 v255,v255
v_exp_f32 v136,v136
v_exp_f32 v137,v137
v_exp_f32 v138,v138
s_add_u32 m0,s49,s52
v_cvt_pk_bf16_f32 v232,v232,v233
v_cvt_pk_bf16_f32 v233,v234,v235
s_xor_b32 s49,s50,65536
v_cvt_pk_bf16_f32 v234,v236,v237
v_cvt_pk_bf16_f32 v235,v238,v239
v_cvt_pk_bf16_f32 v236,v248,v249
v_cvt_pk_bf16_f32 v237,v250,v251
buffer_load_dwordx4 v4,s[24:27],s47 offen nt lds
v_cvt_pk_bf16_f32 v238,v252,v253
v_cvt_pk_bf16_f32 v239,v254,v255
v_exp_f32 v139,v139
v_exp_f32 v140,v140
v_exp_f32 v141,v141
v_mfma_f32_16x16x32_bf16 v[168:171],a[128:131],v[132:135],v[36:39]
v_exp_f32 v142,v142
v_mfma_f32_16x16x32_bf16 v[168:171],a[132:135],v[164:167],v[168:171]
v_exp_f32 v143,v143
v_mfma_f32_16x16x32_bf16 v[168:171],a[136:139],v[196:199],v[168:171]
v_exp_f32 v152,v152
v_mfma_f32_16x16x32_bf16 v[168:171],a[140:143],v[228:231],v[168:171]
v_exp_f32 v153,v153
v_mfma_f32_16x16x32_bf16 v[172:175],a[156:159],v[228:231],v[36:39]
v_exp_f32 v154,v154
v_mfma_f32_16x16x32_bf16 v[172:175],a[152:155],v[196:199],v[172:175]
v_exp_f32 v155,v155
v_mfma_f32_16x16x32_bf16 v[172:175],a[148:151],v[164:167],v[172:175]
v_exp_f32 v156,v156
v_mfma_f32_16x16x32_bf16 v[172:175],a[144:147],v[132:135],v[172:175]
v_exp_f32 v157,v157
v_mfma_f32_16x16x32_bf16 v[184:187],a[160:163],v[132:135],v[36:39]
buffer_load_dwordx4 v5,s[24:27],s47 offen offset:1024 nt lds
v_mfma_f32_16x16x32_bf16 v[184:187],a[164:167],v[164:167],v[184:187]
v_exp_f32 v158,v158
v_mfma_f32_16x16x32_bf16 v[184:187],a[168:171],v[196:199],v[184:187]
v_exp_f32 v159,v159
v_mfma_f32_16x16x32_bf16 v[184:187],a[172:175],v[228:231],v[184:187]
v_exp_f32 v168,v168
v_mfma_f32_16x16x32_bf16 v[188:191],a[188:191],v[228:231],v[36:39]
v_exp_f32 v169,v169
v_mfma_f32_16x16x32_bf16 v[188:191],a[184:187],v[196:199],v[188:191]
v_exp_f32 v170,v170
v_mfma_f32_16x16x32_bf16 v[188:191],a[180:183],v[164:167],v[188:191]
v_exp_f32 v171,v171
v_mfma_f32_16x16x32_bf16 v[188:191],a[176:179],v[132:135],v[188:191]
v_exp_f32 v172,v172
v_mfma_f32_16x16x32_bf16 a[96:99],a[192:195],v[232:235],a[96:99]
ds_read_b64_tr_b16 a[192:193],v2
v_cvt_pk_bf16_f32 v168,v168,v169
)A"
R"A(v_mfma_f32_16x16x32_bf16 a[96:99],a[196:199],v[236:239],a[96:99]
ds_read_b64_tr_b16 a[194:195],v2 offset:4096
v_cvt_pk_bf16_f32 v169,v170,v171
v_mfma_f32_16x16x32_bf16 a[100:103],a[204:207],v[236:239],a[100:103]
v_exp_f32 v173,v173
v_mfma_f32_16x16x32_bf16 a[100:103],a[200:203],v[232:235],a[100:103]
ds_read_b64_tr_b16 a[196:197],v2 offset:8192
v_cvt_pk_bf16_f32 v170,v172,v173
v_mfma_f32_16x16x32_bf16 a[104:107],a[208:211],v[232:235],a[104:107]
v_exp_f32 v174,v174
v_mfma_f32_16x16x32_bf16 a[104:107],a[212:215],v[236:239],a[104:107]
v_exp_f32 v175,v175
v_mfma_f32_16x16x32_bf16 a[108:111],a[220:223],v[236:239],a[108:111]
buffer_load_dwordx4 v4,s[24:27],s47 offen offset:2048 nt lds
v_mfma_f32_16x16x32_bf16 a[108:111],a[216:219],v[232:235],a[108:111]
ds_read_b64_tr_b16 a[198:199],v2 offset:12288
v_cvt_pk_bf16_f32 v171,v174,v175
v_mfma_f32_16x16x32_bf16 a[112:115],a[224:227],v[232:235],a[112:115]
v_exp_f32 v184,v184
v_mfma_f32_16x16x32_bf16 a[112:115],a[228:231],v[236:239],a[112:115]
v_exp_f32 v185,v185
v_mfma_f32_16x16x32_bf16 a[116:119],a[236:239],v[236:239],a[116:119]
ds_read_b64_tr_b16 a[204:205],v3 offset:8192
v_cvt_pk_bf16_f32 v136,v136,v137
v_mfma_f32_16x16x32_bf16 a[116:119],a[232:235],v[232:235],a[116:119]
ds_read_b64_tr_b16 a[206:207],v3 offset:12288
v_cvt_pk_bf16_f32 v172,v184,v185
v_mfma_f32_16x16x32_bf16 a[120:123],a[240:243],v[232:235],a[120:123]
ds_read_b64_tr_b16 a[200:201],v3
v_mfma_f32_16x16x32_bf16 a[120:123],a[244:247],v[236:239],a[120:123]
ds_read_b64_tr_b16 a[202:203],v3 offset:4096
v_cvt_pk_bf16_f32 v137,v138,v139
v_mfma_f32_16x16x32_bf16 a[124:127],a[252:255],v[236:239],a[124:127]
ds_read_b64_tr_b16 a[208:209],v2 offset:256
v_mfma_f32_16x16x32_bf16 a[124:127],a[248:251],v[232:235],a[124:127]
ds_read_b64_tr_b16 a[210:211],v2 offset:4352
v_smfmac_f32_16x16x64_bf16 v[60:63],v[28:31],v[232:239],v27
s_waitcnt lgkmcnt(6)
ds_read_b64_tr_b16 a[212:213],v2 offset:8448
ds_read_b64_tr_b16 a[214:215],v2 offset:12544
v_cvt_pk_bf16_f32 v138,v140,v141
ds_read_b64_tr_b16 a[220:221],v3 offset:8448
ds_read_b64_tr_b16 a[222:223],v3 offset:12544
v_cvt_pk_bf16_f32 v139,v142,v143
buffer_load_dwordx4 v5,s[24:27],s47 offen offset:3072 nt lds
s_add_u32 s47,s47,16384
s_and_b32 s47,s47,0x3ffff
ds_read_b64_tr_b16 a[216:217],v3 offset:256
s_or_b32 s47,s47,s46
ds_read_b64_tr_b16 a[218:219],v3 offset:4352
v_cvt_pk_bf16_f32 v140,v152,v153
ds_read_b64_tr_b16 a[224:225],v2 offset:512
ds_read_b64_tr_b16 a[226:227],v2 offset:4608
ds_read_b64_tr_b16 a[228:229],v2 offset:8704
v_cvt_pk_bf16_f32 v141,v154,v155
ds_read_b64_tr_b16 a[230:231],v2 offset:12800
ds_read_b64_tr_b16 a[236:237],v3 offset:8704
ds_read_b64_tr_b16 a[238:239],v3 offset:12800
v_cvt_pk_bf16_f32 v142,v156,v157
ds_read_b64_tr_b16 a[232:233],v3 offset:512
ds_read_b64_tr_b16 a[234:235],v3 offset:4608
ds_read_b64_tr_b16 a[240:241],v2 offset:768
v_cvt_pk_bf16_f32 v143,v158,v159
buffer_load_dword v26,v24,s[88:91],s93 offen
v_mfma_f32_16x16x32_bf16 a[0:3],a[192:195],v[136:139],a[0:3]
ds_read_b64_tr_b16 a[242:243],v2 offset:4864
v_mfma_f32_16x16x32_bf16 a[0:3],a[196:199],v[140:143],a[0:3]
s_waitcnt lgkmcnt(15)
ds_read_b64_tr_b16 a[244:245],v2 offset:8960
v_mfma_f32_16x16x32_bf16 a[4:7],a[204:207],v[140:143],a[4:7]
ds_read_b64_tr_b16 a[246:247],v2 offset:13056
v_mfma_f32_16x16x32_bf16 a[4:7],a[200:203],v[136:139],a[4:7]
ds_read_b64_tr_b16 a[252:253],v3 offset:8960
v_mfma_f32_16x16x32_bf16 a[8:11],a[208:211],v[136:139],a[8:11]
s_waitcnt lgkmcnt(15)
ds_read_b64_tr_b16 a[254:255],v3 offset:13056
v_mfma_f32_16x16x32_bf16 a[8:11],a[212:215],v[140:143],a[8:11]
ds_read_b64_tr_b16 a[248:249],v3 offset:768
v_mfma_f32_16x16x32_bf16 a[12:15],a[220:223],v[140:143],a[12:15]
s_waitcnt lgkmcnt(15)
ds_read_b64_tr_b16 a[250:251],v3 offset:4864
v_mfma_f32_16x16x32_bf16 a[12:15],a[216:219],v[136:139],a[12:15]
s_bitcmp1_b32 s45,0
s_cbranch_scc0 L_t31v21_%=
s_mov_b32 m0,s80
s_add_u32 s80,s80,4096
buffer_load_dwordx4 v25,s[24:27],s83 offen nt lds
s_add_u32 s83,s83,4096
L_t31v21_%=:
s_waitcnt lgkmcnt(7)
v_mfma_f32_16x16x32_bf16 a[16:19],a[224:227],v[136:139],a[16:19]
v_exp_f32 v186,v186
v_mfma_f32_16x16x32_bf16 a[16:19],a[228:231],v[140:143],a[16:19]
v_exp_f32 v187,v187
v_mfma_f32_16x16x32_bf16 a[20:23],a[236:239],v[140:143],a[20:23]
v_exp_f32 v188,v188
v_mfma_f32_16x16x32_bf16 a[20:23],a[232:235],v[136:139],a[20:23]
s_waitcnt lgkmcnt(3)
v_exp_f32 v189,v189
v_mfma_f32_16x16x32_bf16 a[24:27],a[240:243],v[136:139],a[24:27]
v_exp_f32 v190,v190
v_mfma_f32_16x16x32_bf16 a[24:27],a[244:247],v[140:143],a[24:27]
s_waitcnt lgkmcnt(1)
v_cvt_pk_bf16_f32 v173,v186,v187
v_mfma_f32_16x16x32_bf16 a[28:31],a[252:255],v[140:143],a[28:31]
s_waitcnt lgkmcnt(0)
v_exp_f32 v191,v191
v_mfma_f32_16x16x32_bf16 a[28:31],a[248:251],v[136:139],a[28:31]
v_cvt_pk_bf16_f32 v174,v188,v189
v_cvt_pk_bf16_f32 v175,v190,v191
s_cmp_lg_u32 s53,0
v_smfmac_f32_16x16x64_bf16 v[48:51],v[28:31],v[136:143],v27
s_cbranch_scc1 L_att1v2_%=
s_waitcnt lgkmcnt(0)
s_nop 7
s_branch L_att1x_%=
.p2align 5
L_att1_%=:
v_mfma_f32_16x16x32_bf16 v[232:235],a[128:131],v[148:151],v[44:47]
s_waitcnt vmcnt(8) lgkmcnt(0)
v_exp_f32 v200,v200
v_mfma_f32_16x16x32_bf16 v[232:235],a[132:135],v[180:183],v[232:235]
s_barrier
s_add_u32 m0,s49,s51
ds_read_b128 a[128:131],v1
v_mfma_f32_16x16x32_bf16 v[232:235],a[136:139],v[212:215],v[232:235]
s_mov_b32 s90,L_out_end_%=-L_pc_%=+128
v_exp_f32 v201,v201
v_mfma_f32_16x16x32_bf16 v[232:235],a[140:143],v[244:247],v[232:235]
s_lshr_b32 s45,s45,1
ds_read_b128 a[132:135],v1 offset:256
v_cvt_pk_bf16_f32 v200,v200,v201
v_mfma_f32_16x16x32_bf16 v[236:239],a[156:159],v[244:247],v[44:47]
s_sub_u32 s94,s94,3
buffer_load_dwordx4 v4,s[20:23],s47 offen nt lds
v_mfma_f32_16x16x32_bf16 v[236:239],a[152:155],v[212:215],v[236:239]
s_lshr_b32 s60,s94,2
ds_read_b128 a[136:139],v1 offset:512
v_add_u32 v2,s48,v2
v_mfma_f32_16x16x32_bf16 v[236:239],a[148:151],v[180:183],v[236:239]
s_max_i32 s60,s60,128
v_exp_f32 v202,v202
v_mfma_f32_16x16x32_bf16 v[236:239],a[144:147],v[148:151],v[236:239]
s_add_u32 s62,s50,32768
ds_read_b128 a[140:143],v1 offset:768
v_add_u32 v3,s48,v3
v_mfma_f32_16x16x32_bf16 v[248:251],a[160:163],v[148:151],v[44:47]
s_and_b32 s62,s62,131071
v_exp_f32 v203,v203
v_mfma_f32_16x16x32_bf16 v[248:251],a[164:167],v[180:183],v[248:251]
s_add_u32 s54,s54,16384
ds_read_b128 a[156:159],v1 offset:4864
v_cvt_pk_bf16_f32 v201,v202,v203
v_mfma_f32_16x16x32_bf16 v[248:251],a[168:171],v[212:215],v[248:251]
s_sub_u32 s48,s62,s50
v_exp_f32 v204,v204
v_mfma_f32_16x16x32_bf16 v[248:251],a[172:175],v[244:247],v[248:251]
s_sub_u32 s53,s53,1
v_exp_f32 v205,v205
v_mfma_f32_16x16x32_bf16 v[252:255],a[188:191],v[244:247],v[44:47]
s_and_b32 s46,s54,0xfffc0000
ds_read_b128 a[152:155],v1 offset:4608
v_cvt_pk_bf16_f32 v202,v204,v205
v_mfma_f32_16x16x32_bf16 v[252:255],a[184:187],v[212:215],v[252:255]
s_mov_b32 s50,s62
v_exp_f32 v206,v206
v_mfma_f32_16x16x32_bf16 v[252:255],a[180:183],v[180:183],v[252:255]
v_exp_f32 v207,v207
v_mfma_f32_16x16x32_bf16 v[252:255],a[176:179],v[148:151],v[252:255]
ds_read_b128 a[148:151],v1 offset:4352
v_cvt_pk_bf16_f32 v203,v206,v207
v_mfma_f32_16x16x32_bf16 a[32:35],a[192:195],v[168:171],a[32:35]
v_exp_f32 v216,v216
v_mfma_f32_16x16x32_bf16 a[32:35],a[196:199],v[172:175],a[32:35]
ds_read_b128 a[144:147],v1 offset:4096
v_mfma_f32_16x16x32_bf16 a[36:39],a[204:207],v[172:175],a[36:39]
buffer_load_dwordx4 v4,s[20:23],s47 offen offset:1024 nt lds
v_mfma_f32_16x16x32_bf16 a[36:39],a[200:203],v[168:171],a[36:39]
ds_read_b128 a[160:163],v1 offset:8192
v_mfma_f32_16x16x32_bf16 a[40:43],a[208:211],v[168:171],a[40:43]
v_exp_f32 v217,v217
v_mfma_f32_16x16x32_bf16 a[40:43],a[212:215],v[172:175],a[40:43]
ds_read_b128 a[164:167],v1 offset:8448
v_cvt_pk_bf16_f32 v204,v216,v217
v_mfma_f32_16x16x32_bf16 a[44:47],a[220:223],v[172:175],a[44:47]
v_exp_f32 v218,v218
v_mfma_f32_16x16x32_bf16 a[44:47],a[216:219],v[168:171],a[44:47]
ds_read_b128 a[168:171],v1 offset:8704
v_mfma_f32_16x16x32_bf16 a[48:51],a[224:227],v[168:171],a[48:51]
v_exp_f32 v219,v219
v_mfma_f32_16x16x32_bf16 a[48:51],a[228:231],v[172:175],a[48:51]
ds_read_b128 a[172:175],v1 offset:8960
v_cvt_pk_bf16_f32 v205,v218,v219
v_mfma_f32_16x16x32_bf16 a[52:55],a[236:239],v[172:175],a[52:55]
v_exp_f32 v220,v220
v_mfma_f32_16x16x32_bf16 a[52:55],a[232:235],v[168:171],a[52:55]
ds_read_b128 a[188:191],v1 offset:13056
v_mfma_f32_16x16x32_bf16 a[56:59],a[240:243],v[168:171],a[56:59]
v_exp_f32 v221,v221
v_mfma_f32_16x16x32_bf16 a[56:59],a[244:247],v[172:175],a[56:59]
ds_read_b128 a[184:187],v1 offset:12800
v_cvt_pk_bf16_f32 v206,v220,v221
v_mfma_f32_16x16x32_bf16 a[60:63],a[252:255],v[172:175],a[60:63]
v_exp_f32 v222,v222
v_mfma_f32_16x16x32_bf16 a[60:63],a[248:251],v[168:171],a[60:63]
ds_read_b128 a[180:183],v1 offset:12544
v_smfmac_f32_16x16x64_bf16 v[52:55],v[28:31],v[168:175],v27
s_waitcnt lgkmcnt(4)
v_mfma_f32_16x16x32_bf16 v[136:139],a[128:131],v[128:131],v[32:35]
buffer_load_dwordx4 v5,s[20:23],s47 offen offset:2048 nt lds
v_mfma_f32_16x16x32_bf16 v[136:139],a[132:135],v[160:163],v[136:139]
ds_read_b128 a[176:179],v1 offset:12288
v_add_u32 v1,s48,v1
v_mfma_f32_16x16x32_bf16 v[136:139],a[136:139],v[192:195],v[136:139]
v_exp_f32 v223,v223
v_mfma_f32_16x16x32_bf16 v[136:139],a[140:143],v[224:227],v[136:139]
v_cvt_pk_bf16_f32 v207,v222,v223
v_mfma_f32_16x16x32_bf16 v[140:143],a[156:159],v[224:227],v[32:35]
v_exp_f32 v232,v232
v_mfma_f32_16x16x32_bf16 v[140:143],a[152:155],v[192:195],v[140:143]
v_exp_f32 v233,v233
v_mfma_f32_16x16x32_bf16 v[140:143],a[148:151],v[160:163],v[140:143]
v_exp_f32 v234,v234
v_mfma_f32_16x16x32_bf16 v[140:143],a[144:147],v[128:131],v[140:143]
v_exp_f32 v235,v235
v_mfma_f32_16x16x32_bf16 v[152:155],a[160:163],v[128:131],v[32:35]
v_exp_f32 v236,v236
v_mfma_f32_16x16x32_bf16 v[152:155],a[164:167],v[160:163],v[152:155]
v_exp_f32 v237,v237
v_mfma_f32_16x16x32_bf16 v[152:155],a[168:171],v[192:195],v[152:155]
s_waitcnt lgkmcnt(0)
v_exp_f32 v238,v238
v_mfma_f32_16x16x32_bf16 v[152:155],a[172:175],v[224:227],v[152:155]
v_exp_f32 v239,v239
v_mfma_f32_16x16x32_bf16 v[156:159],a[188:191],v[224:227],v[32:35]
v_exp_f32 v248,v248
v_mfma_f32_16x16x32_bf16 v[156:159],a[184:187],v[192:195],v[156:159]
buffer_load_dwordx4 v5,s[20:23],s47 offen offset:3072 nt lds
v_mfma_f32_16x16x32_bf16 v[156:159],a[180:183],v[160:163],v[156:159]
v_exp_f32 v249,v249
v_mfma_f32_16x16x32_bf16 v[156:159],a[176:179],v[128:131],v[156:159]
v_exp_f32 v250,v250
v_mfma_f32_16x16x32_bf16 a[64:67],a[192:195],v[200:203],a[64:67]
v_exp_f32 v251,v251
v_mfma_f32_16x16x32_bf16 a[64:67],a[196:199],v[204:207],a[64:67]
v_exp_f32 v252,v252
v_mfma_f32_16x16x32_bf16 a[68:71],a[204:207],v[204:207],a[68:71]
v_exp_f32 v253,v253
v_mfma_f32_16x16x32_bf16 a[68:71],a[200:203],v[200:203],a[68:71]
v_exp_f32 v254,v254
v_mfma_f32_16x16x32_bf16 a[72:75],a[208:211],v[200:203],a[72:75]
v_exp_f32 v255,v255
v_mfma_f32_16x16x32_bf16 a[72:75],a[212:215],v[204:207],a[72:75]
v_exp_f32 v136,v136
v_mfma_f32_16x16x32_bf16 a[76:79],a[220:223],v[204:207],a[76:79]
v_exp_f32 v137,v137
v_mfma_f32_16x16x32_bf16 a[76:79],a[216:219],v[200:203],a[76:79]
v_exp_f32 v138,v138
v_mfma_f32_16x16x32_bf16 a[80:83],a[224:227],v[200:203],a[80:83]
s_add_u32 m0,s49,s52
v_cvt_pk_bf16_f32 v232,v232,v233
v_cvt_pk_bf16_f32 v233,v234,v235
v_mfma_f32_16x16x32_bf16 a[80:83],a[228:231],v[204:207],a[80:83]
s_xor_b32 s49,s50,65536
v_cvt_pk_bf16_f32 v234,v236,v237
v_cvt_pk_bf16_f32 v235,v238,v239
v_mfma_f32_16x16x32_bf16 a[84:87],a[236:239],v[204:207],a[84:87]
v_cvt_pk_bf16_f32 v236,v248,v249
v_cvt_pk_bf16_f32 v237,v250,v251
v_mfma_f32_16x16x32_bf16 a[84:87],a[232:235],v[200:203],a[84:87]
buffer_load_dwordx4 v4,s[24:27],s47 offen nt lds
v_mfma_f32_16x16x32_bf16 a[88:91],a[240:243],v[200:203],a[88:91]
v_cvt_pk_bf16_f32 v238,v252,v253
v_cvt_pk_bf16_f32 v239,v254,v255
v_mfma_f32_16x16x32_bf16 a[88:91],a[244:247],v[204:207],a[88:91]
v_exp_f32 v139,v139
v_mfma_f32_16x16x32_bf16 a[92:95],a[252:255],v[204:207],a[92:95]
v_exp_f32 v140,v140
v_mfma_f32_16x16x32_bf16 a[92:95],a[248:251],v[200:203],a[92:95]
v_exp_f32 v141,v141
v_smfmac_f32_16x16x64_bf16 v[56:59],v[28:31],v[200:207],v27
)A"
R"A(v_mfma_f32_16x16x32_bf16 v[168:171],a[128:131],v[132:135],v[36:39]
v_exp_f32 v142,v142
v_mfma_f32_16x16x32_bf16 v[168:171],a[132:135],v[164:167],v[168:171]
v_exp_f32 v143,v143
v_mfma_f32_16x16x32_bf16 v[168:171],a[136:139],v[196:199],v[168:171]
v_exp_f32 v152,v152
v_mfma_f32_16x16x32_bf16 v[168:171],a[140:143],v[228:231],v[168:171]
v_exp_f32 v153,v153
v_mfma_f32_16x16x32_bf16 v[172:175],a[156:159],v[228:231],v[36:39]
v_exp_f32 v154,v154
v_mfma_f32_16x16x32_bf16 v[172:175],a[152:155],v[196:199],v[172:175]
v_exp_f32 v155,v155
v_mfma_f32_16x16x32_bf16 v[172:175],a[148:151],v[164:167],v[172:175]
v_exp_f32 v156,v156
v_mfma_f32_16x16x32_bf16 v[172:175],a[144:147],v[132:135],v[172:175]
v_exp_f32 v157,v157
v_mfma_f32_16x16x32_bf16 v[184:187],a[160:163],v[132:135],v[36:39]
buffer_load_dwordx4 v5,s[24:27],s47 offen offset:1024 nt lds
v_mfma_f32_16x16x32_bf16 v[184:187],a[164:167],v[164:167],v[184:187]
v_exp_f32 v158,v158
v_mfma_f32_16x16x32_bf16 v[184:187],a[168:171],v[196:199],v[184:187]
v_exp_f32 v159,v159
v_mfma_f32_16x16x32_bf16 v[184:187],a[172:175],v[228:231],v[184:187]
v_exp_f32 v168,v168
v_mfma_f32_16x16x32_bf16 v[188:191],a[188:191],v[228:231],v[36:39]
v_exp_f32 v169,v169
v_mfma_f32_16x16x32_bf16 v[188:191],a[184:187],v[196:199],v[188:191]
v_exp_f32 v170,v170
v_mfma_f32_16x16x32_bf16 v[188:191],a[180:183],v[164:167],v[188:191]
v_exp_f32 v171,v171
v_mfma_f32_16x16x32_bf16 v[188:191],a[176:179],v[132:135],v[188:191]
v_exp_f32 v172,v172
v_mfma_f32_16x16x32_bf16 a[96:99],a[192:195],v[232:235],a[96:99]
ds_read_b64_tr_b16 a[192:193],v2
v_cvt_pk_bf16_f32 v168,v168,v169
v_mfma_f32_16x16x32_bf16 a[96:99],a[196:199],v[236:239],a[96:99]
ds_read_b64_tr_b16 a[194:195],v2 offset:4096
v_cvt_pk_bf16_f32 v169,v170,v171
v_mfma_f32_16x16x32_bf16 a[100:103],a[204:207],v[236:239],a[100:103]
v_exp_f32 v173,v173
v_mfma_f32_16x16x32_bf16 a[100:103],a[200:203],v[232:235],a[100:103]
ds_read_b64_tr_b16 a[196:197],v2 offset:8192
v_cvt_pk_bf16_f32 v170,v172,v173
v_mfma_f32_16x16x32_bf16 a[104:107],a[208:211],v[232:235],a[104:107]
v_exp_f32 v174,v174
v_mfma_f32_16x16x32_bf16 a[104:107],a[212:215],v[236:239],a[104:107]
v_exp_f32 v175,v175
v_mfma_f32_16x16x32_bf16 a[108:111],a[220:223],v[236:239],a[108:111]
buffer_load_dwordx4 v4,s[24:27],s47 offen offset:2048 nt lds
v_mfma_f32_16x16x32_bf16 a[108:111],a[216:219],v[232:235],a[108:111]
ds_read_b64_tr_b16 a[198:199],v2 offset:12288
v_cvt_pk_bf16_f32 v171,v174,v175
v_mfma_f32_16x16x32_bf16 a[112:115],a[224:227],v[232:235],a[112:115]
v_exp_f32 v184,v184
v_mfma_f32_16x16x32_bf16 a[112:115],a[228:231],v[236:239],a[112:115]
v_exp_f32 v185,v185
v_mfma_f32_16x16x32_bf16 a[116:119],a[236:239],v[236:239],a[116:119]
ds_read_b64_tr_b16 a[204:205],v3 offset:8192
v_cvt_pk_bf16_f32 v136,v136,v137
v_mfma_f32_16x16x32_bf16 a[116:119],a[232:235],v[232:235],a[116:119]
ds_read_b64_tr_b16 a[206:207],v3 offset:12288
v_cvt_pk_bf16_f32 v172,v184,v185
v_mfma_f32_16x16x32_bf16 a[120:123],a[240:243],v[232:235],a[120:123]
ds_read_b64_tr_b16 a[200:201],v3
v_mfma_f32_16x16x32_bf16 a[120:123],a[244:247],v[236:239],a[120:123]
ds_read_b64_tr_b16 a[202:203],v3 offset:4096
v_cvt_pk_bf16_f32 v137,v138,v139
v_mfma_f32_16x16x32_bf16 a[124:127],a[252:255],v[236:239],a[124:127]
ds_read_b64_tr_b16 a[208:209],v2 offset:256
v_mfma_f32_16x16x32_bf16 a[124:127],a[248:251],v[232:235],a[124:127]
ds_read_b64_tr_b16 a[210:211],v2 offset:4352
v_smfmac_f32_16x16x64_bf16 v[60:63],v[28:31],v[232:239],v27
ds_read_b64_tr_b16 a[212:213],v2 offset:8448
v_mfma_f32_16x16x32_bf16 v[200:203],a[128:131],v[144:147],v[40:43]
ds_read_b64_tr_b16 a[214:215],v2 offset:12544
v_cvt_pk_bf16_f32 v138,v140,v141
v_mfma_f32_16x16x32_bf16 v[200:203],a[132:135],v[176:179],v[200:203]
ds_read_b64_tr_b16 a[220:221],v3 offset:8448
v_mfma_f32_16x16x32_bf16 v[200:203],a[136:139],v[208:211],v[200:203]
ds_read_b64_tr_b16 a[222:223],v3 offset:12544
v_cvt_pk_bf16_f32 v139,v142,v143
v_mfma_f32_16x16x32_bf16 v[200:203],a[140:143],v[240:243],v[200:203]
buffer_load_dwordx4 v5,s[24:27],s47 offen offset:3072 nt lds
v_mfma_f32_16x16x32_bf16 v[204:207],a[156:159],v[240:243],v[40:43]
s_add_u32 s47,s47,16384
s_and_b32 s47,s47,0x3ffff
ds_read_b64_tr_b16 a[216:217],v3 offset:256
v_mfma_f32_16x16x32_bf16 v[204:207],a[152:155],v[208:211],v[204:207]
s_or_b32 s47,s47,s46
ds_read_b64_tr_b16 a[218:219],v3 offset:4352
v_cvt_pk_bf16_f32 v140,v152,v153
v_mfma_f32_16x16x32_bf16 v[204:207],a[148:151],v[176:179],v[204:207]
ds_read_b64_tr_b16 a[224:225],v2 offset:512
v_mfma_f32_16x16x32_bf16 v[204:207],a[144:147],v[144:147],v[204:207]
ds_read_b64_tr_b16 a[226:227],v2 offset:4608
v_mfma_f32_16x16x32_bf16 v[216:219],a[160:163],v[144:147],v[40:43]
ds_read_b64_tr_b16 a[228:229],v2 offset:8704
v_cvt_pk_bf16_f32 v141,v154,v155
v_mfma_f32_16x16x32_bf16 v[216:219],a[164:167],v[176:179],v[216:219]
ds_read_b64_tr_b16 a[230:231],v2 offset:12800
v_mfma_f32_16x16x32_bf16 v[216:219],a[168:171],v[208:211],v[216:219]
ds_read_b64_tr_b16 a[236:237],v3 offset:8704
v_mfma_f32_16x16x32_bf16 v[216:219],a[172:175],v[240:243],v[216:219]
ds_read_b64_tr_b16 a[238:239],v3 offset:12800
v_cvt_pk_bf16_f32 v142,v156,v157
v_mfma_f32_16x16x32_bf16 v[220:223],a[188:191],v[240:243],v[40:43]
ds_read_b64_tr_b16 a[232:233],v3 offset:512
v_mfma_f32_16x16x32_bf16 v[220:223],a[184:187],v[208:211],v[220:223]
ds_read_b64_tr_b16 a[234:235],v3 offset:4608
v_mfma_f32_16x16x32_bf16 v[220:223],a[180:183],v[176:179],v[220:223]
ds_read_b64_tr_b16 a[240:241],v2 offset:768
v_cvt_pk_bf16_f32 v143,v158,v159
v_mfma_f32_16x16x32_bf16 v[220:223],a[176:179],v[144:147],v[220:223]
s_waitcnt lgkmcnt(7)
buffer_load_dword v26,v24,s[88:91],s93 offen
v_mfma_f32_16x16x32_bf16 a[0:3],a[192:195],v[136:139],a[0:3]
ds_read_b64_tr_b16 a[242:243],v2 offset:4864
v_mfma_f32_16x16x32_bf16 a[0:3],a[196:199],v[140:143],a[0:3]
ds_read_b64_tr_b16 a[244:245],v2 offset:8960
v_mfma_f32_16x16x32_bf16 a[4:7],a[204:207],v[140:143],a[4:7]
ds_read_b64_tr_b16 a[246:247],v2 offset:13056
v_mfma_f32_16x16x32_bf16 a[4:7],a[200:203],v[136:139],a[4:7]
s_memrealtime s[76:77]
ds_read_b64_tr_b16 a[252:253],v3 offset:8960
v_mfma_f32_16x16x32_bf16 a[8:11],a[208:211],v[136:139],a[8:11]
ds_read_b64_tr_b16 a[254:255],v3 offset:13056
v_mfma_f32_16x16x32_bf16 a[8:11],a[212:215],v[140:143],a[8:11]
ds_read_b64_tr_b16 a[248:249],v3 offset:768
v_mfma_f32_16x16x32_bf16 a[12:15],a[220:223],v[140:143],a[12:15]
ds_read_b64_tr_b16 a[250:251],v3 offset:4864
v_mfma_f32_16x16x32_bf16 a[12:15],a[216:219],v[136:139],a[12:15]
s_bitcmp1_b32 s45,0
s_cbranch_scc0 L_t311_%=
s_mov_b32 m0,s80
s_add_u32 s80,s80,4096
buffer_load_dwordx4 v25,s[24:27],s83 offen nt lds
s_add_u32 s83,s83,4096
L_t311_%=:
v_mfma_f32_16x16x32_bf16 a[16:19],a[224:227],v[136:139],a[16:19]
s_waitcnt lgkmcnt(6)
v_exp_f32 v186,v186
v_mfma_f32_16x16x32_bf16 a[16:19],a[228:231],v[140:143],a[16:19]
v_exp_f32 v187,v187
v_mfma_f32_16x16x32_bf16 a[20:23],a[236:239],v[140:143],a[20:23]
s_waitcnt lgkmcnt(0)
v_exp_f32 v188,v188
v_mfma_f32_16x16x32_bf16 a[20:23],a[232:235],v[136:139],a[20:23]
s_sub_u32 s79,s78,s76
v_exp_f32 v189,v189
v_mfma_f32_16x16x32_bf16 a[24:27],a[240:243],v[136:139],a[24:27]
s_add_u32 s78,s76,s60
v_exp_f32 v190,v190
v_mfma_f32_16x16x32_bf16 a[24:27],a[244:247],v[140:143],a[24:27]
s_cmp_gt_i32 s79,s95
s_cbranch_scc1 L_ps7_%=
L_pr7_%=:
v_cvt_pk_bf16_f32 v173,v186,v187
v_mfma_f32_16x16x32_bf16 a[28:31],a[252:255],v[140:143],a[28:31]
v_exp_f32 v191,v191
v_mfma_f32_16x16x32_bf16 a[28:31],a[248:251],v[136:139],a[28:31]
v_cvt_pk_bf16_f32 v174,v188,v189
v_cvt_pk_bf16_f32 v175,v190,v191
s_cmp_lg_u32 s53,0
v_smfmac_f32_16x16x64_bf16 v[48:51],v[28:31],v[136:143],v27
s_cbranch_scc1 L_att1_%=
L_att1x_%=:
L_endseg_%=:
v_mfma_f32_16x16x32_bf16 v[232:235],a[128:131],v[148:151],v[44:47]
s_waitcnt vmcnt(10)
s_lshl_b32 s43,s41,2
v_exp_f32 v200,v200
v_mfma_f32_16x16x32_bf16 v[232:235],a[132:135],v[180:183],v[232:235]
s_barrier
ds_read_b128 a[128:131],v1
v_add_u32 v2,s48,v2
v_mfma_f32_16x16x32_bf16 v[232:235],a[136:139],v[212:215],v[232:235]
s_add_u32 s43,s43,s70
v_exp_f32 v201,v201
v_mfma_f32_16x16x32_bf16 v[232:235],a[140:143],v[244:247],v[232:235]
s_lshl_b32 s43,s43,19
ds_read_b128 a[132:135],v1 offset:256
v_cvt_pk_bf16_f32 v200,v200,v201
v_mfma_f32_16x16x32_bf16 v[236:239],a[156:159],v[244:247],v[44:47]
s_add_u32 s43,s43,s55
v_exp_f32 v202,v202
v_mfma_f32_16x16x32_bf16 v[236:239],a[152:155],v[212:215],v[236:239]
s_mov_b64 s[28:29],s[12:13]
ds_read_b128 a[136:139],v1 offset:512
v_add_u32 v3,s48,v3
v_mfma_f32_16x16x32_bf16 v[236:239],a[148:151],v[180:183],v[236:239]
s_mov_b32 s48,32768
v_exp_f32 v203,v203
v_mfma_f32_16x16x32_bf16 v[236:239],a[144:147],v[148:151],v[236:239]
s_mov_b32 s30,475136
ds_read_b128 a[140:143],v1 offset:768
v_cvt_pk_bf16_f32 v201,v202,v203
v_mfma_f32_16x16x32_bf16 v[248:251],a[160:163],v[148:151],v[44:47]
s_mov_b32 s31,0x20000
v_exp_f32 v204,v204
v_mfma_f32_16x16x32_bf16 v[248:251],a[164:167],v[180:183],v[248:251]
ds_read_b128 a[156:159],v1 offset:4864
v_lshrrev_b32 v4,6,v6
v_mfma_f32_16x16x32_bf16 v[248:251],a[168:171],v[212:215],v[248:251]
v_exp_f32 v205,v205
v_mfma_f32_16x16x32_bf16 v[248:251],a[172:175],v[244:247],v[248:251]
ds_read_b128 a[152:155],v1 offset:4608
v_cvt_pk_bf16_f32 v202,v204,v205
v_mfma_f32_16x16x32_bf16 v[252:255],a[188:191],v[244:247],v[44:47]
v_exp_f32 v206,v206
v_mfma_f32_16x16x32_bf16 v[252:255],a[184:187],v[212:215],v[252:255]
ds_read_b128 a[148:151],v1 offset:4352
v_mul_u32_u24 v4,320,v4
v_mfma_f32_16x16x32_bf16 v[252:255],a[180:183],v[180:183],v[252:255]
v_exp_f32 v207,v207
v_mfma_f32_16x16x32_bf16 v[252:255],a[176:179],v[148:151],v[252:255]
ds_read_b128 a[144:147],v1 offset:4096
v_cvt_pk_bf16_f32 v203,v206,v207
v_mfma_f32_16x16x32_bf16 a[32:35],a[192:195],v[168:171],a[32:35]
v_exp_f32 v216,v216
v_mfma_f32_16x16x32_bf16 a[32:35],a[196:199],v[172:175],a[32:35]
ds_read_b128 a[160:163],v1 offset:8192
v_bfe_u32 v5,v6,4,2
v_mfma_f32_16x16x32_bf16 a[36:39],a[204:207],v[172:175],a[36:39]
v_exp_f32 v217,v217
v_mfma_f32_16x16x32_bf16 a[36:39],a[200:203],v[168:171],a[36:39]
ds_read_b128 a[164:167],v1 offset:8448
v_cvt_pk_bf16_f32 v204,v216,v217
v_mfma_f32_16x16x32_bf16 a[40:43],a[208:211],v[168:171],a[40:43]
v_exp_f32 v218,v218
v_mfma_f32_16x16x32_bf16 a[40:43],a[212:215],v[172:175],a[40:43]
ds_read_b128 a[168:171],v1 offset:8704
v_lshl_add_u32 v4,v5,3,v4
v_mfma_f32_16x16x32_bf16 a[44:47],a[220:223],v[172:175],a[44:47]
v_exp_f32 v219,v219
v_mfma_f32_16x16x32_bf16 a[44:47],a[216:219],v[168:171],a[44:47]
ds_read_b128 a[172:175],v1 offset:8960
v_cvt_pk_bf16_f32 v205,v218,v219
v_mfma_f32_16x16x32_bf16 a[48:51],a[224:227],v[168:171],a[48:51]
v_exp_f32 v220,v220
v_mfma_f32_16x16x32_bf16 a[48:51],a[228:231],v[172:175],a[48:51]
ds_read_b128 a[188:191],v1 offset:13056
v_bfe_u32 v5,v6,8,1
v_mfma_f32_16x16x32_bf16 a[52:55],a[236:239],v[172:175],a[52:55]
v_exp_f32 v221,v221
v_mfma_f32_16x16x32_bf16 a[52:55],a[232:235],v[168:171],a[52:55]
ds_read_b128 a[184:187],v1 offset:12800
v_cvt_pk_bf16_f32 v206,v220,v221
v_mfma_f32_16x16x32_bf16 a[56:59],a[240:243],v[168:171],a[56:59]
v_exp_f32 v222,v222
v_mfma_f32_16x16x32_bf16 a[56:59],a[244:247],v[172:175],a[56:59]
ds_read_b128 a[180:183],v1 offset:12544
v_lshlrev_b32 v5,5,v5
v_mfma_f32_16x16x32_bf16 a[60:63],a[252:255],v[172:175],a[60:63]
v_exp_f32 v223,v223
v_mfma_f32_16x16x32_bf16 a[60:63],a[248:251],v[168:171],a[60:63]
ds_read_b128 a[176:179],v1 offset:12288
v_cvt_pk_bf16_f32 v207,v222,v223
v_smfmac_f32_16x16x64_bf16 v[52:55],v[28:31],v[168:175],v27
s_waitcnt lgkmcnt(4)
v_add_u32 v1,s48,v1
v_mfma_f32_16x16x32_bf16 v[136:139],a[128:131],v[128:131],v[32:35]
v_exp_f32 v232,v232
v_mfma_f32_16x16x32_bf16 v[136:139],a[132:135],v[160:163],v[136:139]
v_exp_f32 v233,v233
v_mfma_f32_16x16x32_bf16 v[136:139],a[136:139],v[192:195],v[136:139]
v_exp_f32 v234,v234
v_mfma_f32_16x16x32_bf16 v[136:139],a[140:143],v[224:227],v[136:139]
v_exp_f32 v235,v235
v_mfma_f32_16x16x32_bf16 v[140:143],a[156:159],v[224:227],v[32:35]
v_cvt_pk_bf16_f32 v232,v232,v233
v_cvt_pk_bf16_f32 v233,v234,v235
v_mfma_f32_16x16x32_bf16 v[140:143],a[152:155],v[192:195],v[140:143]
s_add_u32 s80,s71,0
)A"
R"A(v_exp_f32 v236,v236
v_mfma_f32_16x16x32_bf16 v[140:143],a[148:151],v[160:163],v[140:143]
s_and_b32 s80,s80,3
v_exp_f32 v237,v237
v_mfma_f32_16x16x32_bf16 v[140:143],a[144:147],v[128:131],v[140:143]
s_lshl_b32 s80,s80,1
s_mul_i32 s80,s80,163840
s_add_u32 s80,s80,s81
v_mfma_f32_16x16x32_bf16 v[152:155],a[160:163],v[128:131],v[32:35]
s_add_u32 s83,s80,20480
v_exp_f32 v238,v238
v_mfma_f32_16x16x32_bf16 v[152:155],a[164:167],v[160:163],v[152:155]
buffer_store_dwordx4 v[64:67],v6,s[36:39],s84 offen sc1
v_mfma_f32_16x16x32_bf16 v[152:155],a[168:171],v[192:195],v[152:155]
v_exp_f32 v239,v239
v_mfma_f32_16x16x32_bf16 v[152:155],a[172:175],v[224:227],v[152:155]
s_waitcnt lgkmcnt(0)
v_cvt_pk_bf16_f32 v234,v236,v237
v_cvt_pk_bf16_f32 v235,v238,v239
v_mfma_f32_16x16x32_bf16 v[156:159],a[188:191],v[224:227],v[32:35]
v_exp_f32 v248,v248
v_mfma_f32_16x16x32_bf16 v[156:159],a[184:187],v[192:195],v[156:159]
v_exp_f32 v249,v249
v_mfma_f32_16x16x32_bf16 v[156:159],a[180:183],v[160:163],v[156:159]
v_exp_f32 v250,v250
v_mfma_f32_16x16x32_bf16 v[156:159],a[176:179],v[128:131],v[156:159]
v_exp_f32 v251,v251
v_mfma_f32_16x16x32_bf16 a[64:67],a[192:195],v[200:203],a[64:67]
v_exp_f32 v252,v252
v_mfma_f32_16x16x32_bf16 a[64:67],a[196:199],v[204:207],a[64:67]
v_exp_f32 v253,v253
v_mfma_f32_16x16x32_bf16 a[68:71],a[204:207],v[204:207],a[68:71]
v_exp_f32 v254,v254
v_mfma_f32_16x16x32_bf16 a[68:71],a[200:203],v[200:203],a[68:71]
v_exp_f32 v255,v255
v_mfma_f32_16x16x32_bf16 a[72:75],a[208:211],v[200:203],a[72:75]
v_cvt_pk_bf16_f32 v236,v248,v249
v_cvt_pk_bf16_f32 v237,v250,v251
v_mfma_f32_16x16x32_bf16 a[72:75],a[212:215],v[204:207],a[72:75]
v_cvt_pk_bf16_f32 v238,v252,v253
v_cvt_pk_bf16_f32 v239,v254,v255
v_mfma_f32_16x16x32_bf16 a[76:79],a[220:223],v[204:207],a[76:79]
v_exp_f32 v136,v136
v_mfma_f32_16x16x32_bf16 a[76:79],a[216:219],v[200:203],a[76:79]
v_exp_f32 v137,v137
v_mfma_f32_16x16x32_bf16 a[80:83],a[224:227],v[200:203],a[80:83]
v_exp_f32 v138,v138
v_mfma_f32_16x16x32_bf16 a[80:83],a[228:231],v[204:207],a[80:83]
v_exp_f32 v139,v139
v_mfma_f32_16x16x32_bf16 a[84:87],a[236:239],v[204:207],a[84:87]
s_add_u32 m0,s82,81920
v_exp_f32 v140,v140
v_mfma_f32_16x16x32_bf16 a[84:87],a[232:235],v[200:203],a[84:87]
v_cvt_pk_bf16_f32 v136,v136,v137
v_cvt_pk_bf16_f32 v137,v138,v139
v_mfma_f32_16x16x32_bf16 a[88:91],a[240:243],v[200:203],a[88:91]
v_exp_f32 v141,v141
v_mfma_f32_16x16x32_bf16 a[88:91],a[244:247],v[204:207],a[88:91]
buffer_load_dwordx4 v7,s[72:75],s80 offen lds
v_mfma_f32_16x16x32_bf16 a[92:95],a[252:255],v[204:207],a[92:95]
v_exp_f32 v142,v142
v_mfma_f32_16x16x32_bf16 a[92:95],a[248:251],v[200:203],a[92:95]
v_exp_f32 v143,v143
v_smfmac_f32_16x16x64_bf16 v[56:59],v[28:31],v[200:207],v27
v_cvt_pk_bf16_f32 v138,v140,v141
v_mfma_f32_16x16x32_bf16 v[168:171],a[128:131],v[132:135],v[36:39]
buffer_store_dwordx4 v[68:71],v6,s[36:39],s84 offen offset:1024 sc1
v_mfma_f32_16x16x32_bf16 v[168:171],a[132:135],v[164:167],v[168:171]
s_add_u32 s28,s28,s43
s_addc_u32 s29,s29,0
v_cvt_pk_bf16_f32 v139,v142,v143
v_mfma_f32_16x16x32_bf16 v[168:171],a[136:139],v[196:199],v[168:171]
s_and_b32 s29,s29,0xffff
v_exp_f32 v152,v152
v_mfma_f32_16x16x32_bf16 v[168:171],a[140:143],v[228:231],v[168:171]
v_exp_f32 v153,v153
v_mfma_f32_16x16x32_bf16 v[172:175],a[156:159],v[228:231],v[36:39]
v_exp_f32 v154,v154
v_mfma_f32_16x16x32_bf16 v[172:175],a[152:155],v[196:199],v[172:175]
v_exp_f32 v155,v155
v_mfma_f32_16x16x32_bf16 v[172:175],a[148:151],v[164:167],v[172:175]
v_exp_f32 v156,v156
v_mfma_f32_16x16x32_bf16 v[172:175],a[144:147],v[132:135],v[172:175]
v_exp_f32 v157,v157
v_mfma_f32_16x16x32_bf16 v[184:187],a[160:163],v[132:135],v[36:39]
v_exp_f32 v158,v158
v_mfma_f32_16x16x32_bf16 v[184:187],a[164:167],v[164:167],v[184:187]
v_exp_f32 v159,v159
v_mfma_f32_16x16x32_bf16 v[184:187],a[168:171],v[196:199],v[184:187]
v_cvt_pk_bf16_f32 v140,v152,v153
v_cvt_pk_bf16_f32 v141,v154,v155
v_mfma_f32_16x16x32_bf16 v[184:187],a[172:175],v[228:231],v[184:187]
v_cvt_pk_bf16_f32 v142,v156,v157
v_cvt_pk_bf16_f32 v143,v158,v159
v_mfma_f32_16x16x32_bf16 v[188:191],a[188:191],v[228:231],v[36:39]
v_exp_f32 v168,v168
v_mfma_f32_16x16x32_bf16 v[188:191],a[184:187],v[196:199],v[188:191]
v_exp_f32 v169,v169
v_mfma_f32_16x16x32_bf16 v[188:191],a[180:183],v[164:167],v[188:191]
v_exp_f32 v170,v170
v_mfma_f32_16x16x32_bf16 v[188:191],a[176:179],v[132:135],v[188:191]
v_exp_f32 v171,v171
v_mfma_f32_16x16x32_bf16 a[96:99],a[192:195],v[232:235],a[96:99]
buffer_load_dwordx4 v8,s[72:75],s80 offen offset:1024 lds
v_mfma_f32_16x16x32_bf16 a[96:99],a[196:199],v[236:239],a[96:99]
ds_read_b64_tr_b16 a[192:193],v2
ds_read_b64_tr_b16 a[194:195],v2 offset:4096
v_mfma_f32_16x16x32_bf16 a[100:103],a[204:207],v[236:239],a[100:103]
ds_read_b64_tr_b16 a[196:197],v2 offset:8192
v_cvt_pk_bf16_f32 v168,v168,v169
v_mfma_f32_16x16x32_bf16 a[100:103],a[200:203],v[232:235],a[100:103]
ds_read_b64_tr_b16 a[198:199],v2 offset:12288
v_cvt_pk_bf16_f32 v169,v170,v171
v_mfma_f32_16x16x32_bf16 a[104:107],a[208:211],v[232:235],a[104:107]
buffer_store_dwordx4 v[72:75],v6,s[36:39],s84 offen offset:2048 sc1
v_mfma_f32_16x16x32_bf16 a[104:107],a[212:215],v[236:239],a[104:107]
ds_read_b64_tr_b16 a[204:205],v3 offset:8192
ds_read_b64_tr_b16 a[206:207],v3 offset:12288
v_mfma_f32_16x16x32_bf16 a[108:111],a[220:223],v[236:239],a[108:111]
v_exp_f32 v172,v172
v_mfma_f32_16x16x32_bf16 a[108:111],a[216:219],v[232:235],a[108:111]
ds_read_b64_tr_b16 a[200:201],v3
ds_read_b64_tr_b16 a[202:203],v3 offset:4096
v_mfma_f32_16x16x32_bf16 a[112:115],a[224:227],v[232:235],a[112:115]
v_exp_f32 v173,v173
v_mfma_f32_16x16x32_bf16 a[112:115],a[228:231],v[236:239],a[112:115]
ds_read_b64_tr_b16 a[208:209],v2 offset:256
ds_read_b64_tr_b16 a[210:211],v2 offset:4352
v_mfma_f32_16x16x32_bf16 a[116:119],a[236:239],v[236:239],a[116:119]
ds_read_b64_tr_b16 a[212:213],v2 offset:8448
v_cvt_pk_bf16_f32 v170,v172,v173
v_mfma_f32_16x16x32_bf16 a[116:119],a[232:235],v[232:235],a[116:119]
v_exp_f32 v174,v174
v_mfma_f32_16x16x32_bf16 a[120:123],a[240:243],v[232:235],a[120:123]
ds_read_b64_tr_b16 a[214:215],v2 offset:12544
ds_read_b64_tr_b16 a[220:221],v3 offset:8448
v_mfma_f32_16x16x32_bf16 a[120:123],a[244:247],v[236:239],a[120:123]
ds_read_b64_tr_b16 a[222:223],v3 offset:12544
v_mfma_f32_16x16x32_bf16 a[124:127],a[252:255],v[236:239],a[124:127]
ds_read_b64_tr_b16 a[216:217],v3 offset:256
v_mfma_f32_16x16x32_bf16 a[124:127],a[248:251],v[232:235],a[124:127]
ds_read_b64_tr_b16 a[218:219],v3 offset:4352
v_smfmac_f32_16x16x64_bf16 v[60:63],v[28:31],v[232:239],v27
ds_read_b64_tr_b16 a[224:225],v2 offset:512
v_mfma_f32_16x16x32_bf16 v[200:203],a[128:131],v[144:147],v[40:43]
ds_read_b64_tr_b16 a[226:227],v2 offset:4608
ds_read_b64_tr_b16 a[228:229],v2 offset:8704
v_mfma_f32_16x16x32_bf16 v[200:203],a[132:135],v[176:179],v[200:203]
ds_read_b64_tr_b16 a[230:231],v2 offset:12800
v_mfma_f32_16x16x32_bf16 v[200:203],a[136:139],v[208:211],v[200:203]
ds_read_b64_tr_b16 a[236:237],v3 offset:8704
v_mfma_f32_16x16x32_bf16 v[200:203],a[140:143],v[240:243],v[200:203]
buffer_load_dwordx4 v9,s[72:75],s80 offen offset:2048 lds
v_mfma_f32_16x16x32_bf16 v[204:207],a[156:159],v[240:243],v[40:43]
ds_read_b64_tr_b16 a[238:239],v3 offset:12800
ds_read_b64_tr_b16 a[232:233],v3 offset:512
v_mfma_f32_16x16x32_bf16 v[204:207],a[152:155],v[208:211],v[204:207]
ds_read_b64_tr_b16 a[234:235],v3 offset:4608
v_mfma_f32_16x16x32_bf16 v[204:207],a[148:151],v[176:179],v[204:207]
ds_read_b64_tr_b16 a[240:241],v2 offset:768
v_mfma_f32_16x16x32_bf16 v[204:207],a[144:147],v[144:147],v[204:207]
ds_read_b64_tr_b16 a[242:243],v2 offset:4864
v_mfma_f32_16x16x32_bf16 v[216:219],a[160:163],v[144:147],v[40:43]
ds_read_b64_tr_b16 a[244:245],v2 offset:8960
ds_read_b64_tr_b16 a[246:247],v2 offset:13056
v_mfma_f32_16x16x32_bf16 v[216:219],a[164:167],v[176:179],v[216:219]
buffer_store_dwordx4 v[76:79],v6,s[36:39],s84 offen offset:3072 sc1
v_mfma_f32_16x16x32_bf16 v[216:219],a[168:171],v[208:211],v[216:219]
s_add_u32 s84,s55,65536
ds_read_b64_tr_b16 a[252:253],v3 offset:8960
ds_read_b64_tr_b16 a[254:255],v3 offset:13056
v_mfma_f32_16x16x32_bf16 v[216:219],a[172:175],v[240:243],v[216:219]
s_max_i32 s84,s84,s18
ds_read_b64_tr_b16 a[248:249],v3 offset:768
v_add_u32 v2,s48,v2
v_mfma_f32_16x16x32_bf16 v[220:223],a[188:191],v[240:243],v[40:43]
ds_read_b64_tr_b16 a[250:251],v3 offset:4864
v_add_u32 v3,s48,v3
v_mfma_f32_16x16x32_bf16 v[220:223],a[184:187],v[208:211],v[220:223]
s_mov_b32 s48,98304
v_exp_f32 v175,v175
v_mfma_f32_16x16x32_bf16 v[220:223],a[180:183],v[176:179],v[220:223]
v_exp_f32 v184,v184
v_mfma_f32_16x16x32_bf16 v[220:223],a[176:179],v[144:147],v[220:223]
s_waitcnt lgkmcnt(6)
v_exp_f32 v185,v185
v_mfma_f32_16x16x32_bf16 a[0:3],a[192:195],v[136:139],a[0:3]
v_cvt_pk_bf16_f32 v171,v174,v175
v_cvt_pk_bf16_f32 v172,v184,v185
v_mfma_f32_16x16x32_bf16 a[0:3],a[196:199],v[140:143],a[0:3]
v_exp_f32 v186,v186
v_mfma_f32_16x16x32_bf16 a[4:7],a[204:207],v[140:143],a[4:7]
v_exp_f32 v187,v187
v_mfma_f32_16x16x32_bf16 a[4:7],a[200:203],v[136:139],a[4:7]
v_exp_f32 v188,v188
v_mfma_f32_16x16x32_bf16 a[8:11],a[208:211],v[136:139],a[8:11]
v_exp_f32 v189,v189
v_mfma_f32_16x16x32_bf16 a[8:11],a[212:215],v[140:143],a[8:11]
v_exp_f32 v190,v190
v_mfma_f32_16x16x32_bf16 a[12:15],a[220:223],v[140:143],a[12:15]
v_exp_f32 v191,v191
v_mfma_f32_16x16x32_bf16 a[12:15],a[216:219],v[136:139],a[12:15]
buffer_load_dwordx4 v10,s[72:75],s80 offen offset:3072 lds
v_mfma_f32_16x16x32_bf16 a[16:19],a[224:227],v[136:139],a[16:19]
v_cvt_pk_bf16_f32 v173,v186,v187
v_cvt_pk_bf16_f32 v174,v188,v189
v_mfma_f32_16x16x32_bf16 a[16:19],a[228:231],v[140:143],a[16:19]
v_exp_f32 v200,v200
v_mfma_f32_16x16x32_bf16 a[20:23],a[236:239],v[140:143],a[20:23]
v_exp_f32 v201,v201
v_mfma_f32_16x16x32_bf16 a[20:23],a[232:235],v[136:139],a[20:23]
v_cvt_pk_bf16_f32 v175,v190,v191
v_cvt_pk_bf16_f32 v200,v200,v201
v_mfma_f32_16x16x32_bf16 a[24:27],a[240:243],v[136:139],a[24:27]
s_waitcnt lgkmcnt(0)
v_exp_f32 v202,v202
v_mfma_f32_16x16x32_bf16 a[24:27],a[244:247],v[140:143],a[24:27]
v_exp_f32 v203,v203
v_mfma_f32_16x16x32_bf16 a[28:31],a[252:255],v[140:143],a[28:31]
v_exp_f32 v204,v204
v_mfma_f32_16x16x32_bf16 a[28:31],a[248:251],v[136:139],a[28:31]
buffer_store_dwordx4 v[80:83],v6,s[36:39],s85 offen sc1
v_smfmac_f32_16x16x64_bf16 v[48:51],v[28:31],v[136:143],v27
v_cvt_pk_bf16_f32 v201,v202,v203
v_mfma_f32_16x16x32_bf16 v[232:235],a[128:131],v[148:151],v[44:47]
s_waitcnt vmcnt(10)
v_exp_f32 v205,v205
v_mfma_f32_16x16x32_bf16 v[232:235],a[132:135],v[180:183],v[232:235]
s_barrier
ds_read_b128 a[128:131],v1
v_cvt_pk_bf16_f32 v202,v204,v205
v_mfma_f32_16x16x32_bf16 v[232:235],a[136:139],v[212:215],v[232:235]
v_exp_f32 v206,v206
v_mfma_f32_16x16x32_bf16 v[232:235],a[140:143],v[244:247],v[232:235]
v_exp_f32 v207,v207
v_mfma_f32_16x16x32_bf16 v[236:239],a[156:159],v[244:247],v[44:47]
ds_read_b128 a[132:135],v1 offset:256
v_cvt_pk_bf16_f32 v203,v206,v207
v_mfma_f32_16x16x32_bf16 v[236:239],a[152:155],v[212:215],v[236:239]
v_exp_f32 v216,v216
v_mfma_f32_16x16x32_bf16 v[236:239],a[148:151],v[180:183],v[236:239]
s_add_u32 m0,s82,86016
v_exp_f32 v217,v217
v_mfma_f32_16x16x32_bf16 v[236:239],a[144:147],v[148:151],v[236:239]
ds_read_b128 a[136:139],v1 offset:512
v_cvt_pk_bf16_f32 v204,v216,v217
v_mfma_f32_16x16x32_bf16 v[248:251],a[160:163],v[148:151],v[44:47]
v_exp_f32 v218,v218
v_mfma_f32_16x16x32_bf16 v[248:251],a[164:167],v[180:183],v[248:251]
buffer_load_dwordx4 v11,s[72:75],s80 offen lds
v_mfma_f32_16x16x32_bf16 v[248:251],a[168:171],v[212:215],v[248:251]
ds_read_b128 a[140:143],v1 offset:768
v_mfma_f32_16x16x32_bf16 v[248:251],a[172:175],v[244:247],v[248:251]
v_exp_f32 v219,v219
v_mfma_f32_16x16x32_bf16 v[252:255],a[188:191],v[244:247],v[44:47]
ds_read_b128 a[156:159],v1 offset:4864
v_cvt_pk_bf16_f32 v205,v218,v219
v_mfma_f32_16x16x32_bf16 v[252:255],a[184:187],v[212:215],v[252:255]
v_exp_f32 v220,v220
v_mfma_f32_16x16x32_bf16 v[252:255],a[180:183],v[180:183],v[252:255]
ds_read_b128 a[152:155],v1 offset:4608
v_mfma_f32_16x16x32_bf16 v[252:255],a[176:179],v[148:151],v[252:255]
)A"
R"A(v_exp_f32 v221,v221
v_mfma_f32_16x16x32_bf16 a[32:35],a[192:195],v[168:171],a[32:35]
ds_read_b128 a[148:151],v1 offset:4352
v_cvt_pk_bf16_f32 v206,v220,v221
v_mfma_f32_16x16x32_bf16 a[32:35],a[196:199],v[172:175],a[32:35]
v_exp_f32 v222,v222
v_mfma_f32_16x16x32_bf16 a[36:39],a[204:207],v[172:175],a[36:39]
ds_read_b128 a[144:147],v1 offset:4096
v_mfma_f32_16x16x32_bf16 a[36:39],a[200:203],v[168:171],a[36:39]
buffer_store_dwordx4 v[84:87],v6,s[36:39],s85 offen offset:1024 sc1
v_mfma_f32_16x16x32_bf16 a[40:43],a[208:211],v[168:171],a[40:43]
ds_read_b128 a[160:163],v1 offset:8192
v_mfma_f32_16x16x32_bf16 a[40:43],a[212:215],v[172:175],a[40:43]
v_exp_f32 v223,v223
v_mfma_f32_16x16x32_bf16 a[44:47],a[220:223],v[172:175],a[44:47]
ds_read_b128 a[164:167],v1 offset:8448
v_cvt_pk_bf16_f32 v207,v222,v223
v_mfma_f32_16x16x32_bf16 a[44:47],a[216:219],v[168:171],a[44:47]
v_exp_f32 v232,v232
v_mfma_f32_16x16x32_bf16 a[48:51],a[224:227],v[168:171],a[48:51]
ds_read_b128 a[168:171],v1 offset:8704
v_mfma_f32_16x16x32_bf16 a[48:51],a[228:231],v[172:175],a[48:51]
v_exp_f32 v233,v233
v_mfma_f32_16x16x32_bf16 a[52:55],a[236:239],v[172:175],a[52:55]
s_add_u32 m0,s82,87040
ds_read_b128 a[172:175],v1 offset:8960
v_cvt_pk_bf16_f32 v232,v232,v233
v_mfma_f32_16x16x32_bf16 a[52:55],a[232:235],v[168:171],a[52:55]
v_exp_f32 v234,v234
v_mfma_f32_16x16x32_bf16 a[56:59],a[240:243],v[168:171],a[56:59]
ds_read_b128 a[188:191],v1 offset:13056
v_mfma_f32_16x16x32_bf16 a[56:59],a[244:247],v[172:175],a[56:59]
buffer_load_dwordx4 v7,s[72:75],s83 offen lds
v_mfma_f32_16x16x32_bf16 a[60:63],a[252:255],v[172:175],a[60:63]
ds_read_b128 a[184:187],v1 offset:12800
v_mfma_f32_16x16x32_bf16 a[60:63],a[248:251],v[168:171],a[60:63]
v_exp_f32 v235,v235
v_smfmac_f32_16x16x64_bf16 v[52:55],v[28:31],v[168:175],v27
s_waitcnt lgkmcnt(3)
ds_read_b128 a[180:183],v1 offset:12544
v_mfma_f32_16x16x32_bf16 v[136:139],a[128:131],v[128:131],v[32:35]
v_exp_f32 v236,v236
v_mfma_f32_16x16x32_bf16 v[136:139],a[132:135],v[160:163],v[136:139]
ds_read_b128 a[176:179],v1 offset:12288
v_cvt_pk_bf16_f32 v233,v234,v235
v_mfma_f32_16x16x32_bf16 v[136:139],a[136:139],v[192:195],v[136:139]
v_exp_f32 v237,v237
v_mfma_f32_16x16x32_bf16 v[136:139],a[140:143],v[224:227],v[136:139]
v_exp_f32 v238,v238
v_mfma_f32_16x16x32_bf16 v[140:143],a[156:159],v[224:227],v[32:35]
v_exp_f32 v239,v239
v_mfma_f32_16x16x32_bf16 v[140:143],a[152:155],v[192:195],v[140:143]
v_cvt_pk_bf16_f32 v234,v236,v237
v_cvt_pk_bf16_f32 v235,v238,v239
v_mfma_f32_16x16x32_bf16 v[140:143],a[148:151],v[160:163],v[140:143]
v_exp_f32 v248,v248
v_mfma_f32_16x16x32_bf16 v[140:143],a[144:147],v[128:131],v[140:143]
v_exp_f32 v249,v249
v_mfma_f32_16x16x32_bf16 v[152:155],a[160:163],v[128:131],v[32:35]
buffer_store_dwordx4 v[88:91],v6,s[36:39],s85 offen offset:2048 sc1
v_mfma_f32_16x16x32_bf16 v[152:155],a[164:167],v[160:163],v[152:155]
v_exp_f32 v250,v250
v_mfma_f32_16x16x32_bf16 v[152:155],a[168:171],v[192:195],v[152:155]
s_waitcnt lgkmcnt(0)
v_exp_f32 v251,v251
v_mfma_f32_16x16x32_bf16 v[152:155],a[172:175],v[224:227],v[152:155]
v_exp_f32 v252,v252
v_mfma_f32_16x16x32_bf16 v[156:159],a[188:191],v[224:227],v[32:35]
v_exp_f32 v253,v253
v_mfma_f32_16x16x32_bf16 v[156:159],a[184:187],v[192:195],v[156:159]
v_exp_f32 v254,v254
v_mfma_f32_16x16x32_bf16 v[156:159],a[180:183],v[160:163],v[156:159]
v_exp_f32 v255,v255
v_mfma_f32_16x16x32_bf16 v[156:159],a[176:179],v[128:131],v[156:159]
v_cvt_pk_bf16_f32 v236,v248,v249
v_cvt_pk_bf16_f32 v237,v250,v251
v_mfma_f32_16x16x32_bf16 a[64:67],a[192:195],v[200:203],a[64:67]
buffer_load_dwordx4 v8,s[72:75],s83 offen offset:1024 lds
v_mfma_f32_16x16x32_bf16 a[64:67],a[196:199],v[204:207],a[64:67]
v_cvt_pk_bf16_f32 v238,v252,v253
v_cvt_pk_bf16_f32 v239,v254,v255
v_mfma_f32_16x16x32_bf16 a[68:71],a[204:207],v[204:207],a[68:71]
v_exp_f32 v136,v136
v_mfma_f32_16x16x32_bf16 a[68:71],a[200:203],v[200:203],a[68:71]
v_exp_f32 v137,v137
v_mfma_f32_16x16x32_bf16 a[72:75],a[208:211],v[200:203],a[72:75]
v_exp_f32 v138,v138
v_mfma_f32_16x16x32_bf16 a[72:75],a[212:215],v[204:207],a[72:75]
v_exp_f32 v139,v139
v_mfma_f32_16x16x32_bf16 a[76:79],a[220:223],v[204:207],a[76:79]
v_cvt_pk_bf16_f32 v136,v136,v137
v_cvt_pk_bf16_f32 v137,v138,v139
v_mfma_f32_16x16x32_bf16 a[76:79],a[216:219],v[200:203],a[76:79]
v_exp_f32 v140,v140
v_mfma_f32_16x16x32_bf16 a[80:83],a[224:227],v[200:203],a[80:83]
v_exp_f32 v141,v141
v_mfma_f32_16x16x32_bf16 a[80:83],a[228:231],v[204:207],a[80:83]
v_exp_f32 v142,v142
v_mfma_f32_16x16x32_bf16 a[84:87],a[236:239],v[204:207],a[84:87]
v_exp_f32 v143,v143
v_mfma_f32_16x16x32_bf16 a[84:87],a[232:235],v[200:203],a[84:87]
v_cvt_pk_bf16_f32 v138,v140,v141
v_cvt_pk_bf16_f32 v139,v142,v143
v_mfma_f32_16x16x32_bf16 a[88:91],a[240:243],v[200:203],a[88:91]
v_exp_f32 v152,v152
v_mfma_f32_16x16x32_bf16 a[88:91],a[244:247],v[204:207],a[88:91]
v_exp_f32 v153,v153
v_mfma_f32_16x16x32_bf16 a[92:95],a[252:255],v[204:207],a[92:95]
buffer_store_dwordx4 v[92:95],v6,s[36:39],s85 offen offset:3072 sc1
v_mfma_f32_16x16x32_bf16 a[92:95],a[248:251],v[200:203],a[92:95]
s_add_u32 s85,s55,69632
v_exp_f32 v154,v154
v_smfmac_f32_16x16x64_bf16 v[56:59],v[28:31],v[200:207],v27
s_max_i32 s85,s85,s19
v_cvt_pk_bf16_f32 v140,v152,v153
v_mfma_f32_16x16x32_bf16 v[168:171],a[128:131],v[132:135],v[36:39]
v_exp_f32 v155,v155
v_mfma_f32_16x16x32_bf16 v[168:171],a[132:135],v[164:167],v[168:171]
v_exp_f32 v156,v156
v_mfma_f32_16x16x32_bf16 v[168:171],a[136:139],v[196:199],v[168:171]
v_exp_f32 v157,v157
v_mfma_f32_16x16x32_bf16 v[168:171],a[140:143],v[228:231],v[168:171]
buffer_load_dwordx4 v9,s[72:75],s83 offen offset:2048 lds
v_mfma_f32_16x16x32_bf16 v[172:175],a[156:159],v[228:231],v[36:39]
v_exp_f32 v158,v158
v_mfma_f32_16x16x32_bf16 v[172:175],a[152:155],v[196:199],v[172:175]
v_exp_f32 v159,v159
v_mfma_f32_16x16x32_bf16 v[172:175],a[148:151],v[164:167],v[172:175]
v_cvt_pk_bf16_f32 v141,v154,v155
v_cvt_pk_bf16_f32 v142,v156,v157
v_mfma_f32_16x16x32_bf16 v[172:175],a[144:147],v[132:135],v[172:175]
v_cvt_pk_bf16_f32 v143,v158,v159
v_add_u32 v1,s48,v1
v_mfma_f32_16x16x32_bf16 v[184:187],a[160:163],v[132:135],v[36:39]
v_mfma_f32_16x16x32_bf16 v[184:187],a[164:167],v[164:167],v[184:187]
v_exp_f32 v168,v168
v_mfma_f32_16x16x32_bf16 v[184:187],a[168:171],v[196:199],v[184:187]
v_exp_f32 v169,v169
v_mfma_f32_16x16x32_bf16 v[184:187],a[172:175],v[228:231],v[184:187]
v_exp_f32 v170,v170
v_mfma_f32_16x16x32_bf16 v[188:191],a[188:191],v[228:231],v[36:39]
v_exp_f32 v171,v171
v_mfma_f32_16x16x32_bf16 v[188:191],a[184:187],v[196:199],v[188:191]
v_exp_f32 v172,v172
v_mfma_f32_16x16x32_bf16 v[188:191],a[180:183],v[164:167],v[188:191]
v_exp_f32 v173,v173
v_mfma_f32_16x16x32_bf16 v[188:191],a[176:179],v[132:135],v[188:191]
v_exp_f32 v174,v174
v_mfma_f32_16x16x32_bf16 a[96:99],a[192:195],v[232:235],a[96:99]
ds_read_b64_tr_b16 a[192:193],v2
ds_read_b64_tr_b16 a[194:195],v2 offset:4096
v_mfma_f32_16x16x32_bf16 a[96:99],a[196:199],v[236:239],a[96:99]
ds_read_b64_tr_b16 a[196:197],v2 offset:8192
v_cvt_pk_bf16_f32 v168,v168,v169
v_mfma_f32_16x16x32_bf16 a[100:103],a[204:207],v[236:239],a[100:103]
ds_read_b64_tr_b16 a[198:199],v2 offset:12288
v_cvt_pk_bf16_f32 v169,v170,v171
v_mfma_f32_16x16x32_bf16 a[100:103],a[200:203],v[232:235],a[100:103]
buffer_store_dwordx4 v[96:99],v6,s[36:39],s86 offen sc1
v_mfma_f32_16x16x32_bf16 a[104:107],a[208:211],v[232:235],a[104:107]
ds_read_b64_tr_b16 a[204:205],v3 offset:8192
ds_read_b64_tr_b16 a[206:207],v3 offset:12288
v_mfma_f32_16x16x32_bf16 a[104:107],a[212:215],v[236:239],a[104:107]
ds_read_b64_tr_b16 a[200:201],v3
v_cvt_pk_bf16_f32 v170,v172,v173
v_mfma_f32_16x16x32_bf16 a[108:111],a[220:223],v[236:239],a[108:111]
v_exp_f32 v175,v175
v_mfma_f32_16x16x32_bf16 a[108:111],a[216:219],v[232:235],a[108:111]
buffer_load_dwordx4 v10,s[72:75],s83 offen offset:3072 lds
v_mfma_f32_16x16x32_bf16 a[112:115],a[224:227],v[232:235],a[112:115]
ds_read_b64_tr_b16 a[202:203],v3 offset:4096
ds_read_b64_tr_b16 a[208:209],v2 offset:256
v_mfma_f32_16x16x32_bf16 a[112:115],a[228:231],v[236:239],a[112:115]
ds_read_b64_tr_b16 a[210:211],v2 offset:4352
v_cvt_pk_bf16_f32 v171,v174,v175
v_mfma_f32_16x16x32_bf16 a[116:119],a[236:239],v[236:239],a[116:119]
v_exp_f32 v184,v184
v_mfma_f32_16x16x32_bf16 a[116:119],a[232:235],v[232:235],a[116:119]
ds_read_b64_tr_b16 a[212:213],v2 offset:8448
ds_read_b64_tr_b16 a[214:215],v2 offset:12544
v_mfma_f32_16x16x32_bf16 a[120:123],a[240:243],v[232:235],a[120:123]
v_exp_f32 v185,v185
v_mfma_f32_16x16x32_bf16 a[120:123],a[244:247],v[236:239],a[120:123]
ds_read_b64_tr_b16 a[220:221],v3 offset:8448
ds_read_b64_tr_b16 a[222:223],v3 offset:12544
v_mfma_f32_16x16x32_bf16 a[124:127],a[252:255],v[236:239],a[124:127]
ds_read_b64_tr_b16 a[216:217],v3 offset:256
v_cvt_pk_bf16_f32 v172,v184,v185
v_mfma_f32_16x16x32_bf16 a[124:127],a[248:251],v[232:235],a[124:127]
ds_read_b64_tr_b16 a[218:219],v3 offset:4352
v_smfmac_f32_16x16x64_bf16 v[60:63],v[28:31],v[232:239],v27
ds_read_b64_tr_b16 a[224:225],v2 offset:512
v_mfma_f32_16x16x32_bf16 v[200:203],a[128:131],v[144:147],v[40:43]
ds_read_b64_tr_b16 a[226:227],v2 offset:4608
ds_read_b64_tr_b16 a[228:229],v2 offset:8704
v_mfma_f32_16x16x32_bf16 v[200:203],a[132:135],v[176:179],v[200:203]
ds_read_b64_tr_b16 a[230:231],v2 offset:12800
v_mfma_f32_16x16x32_bf16 v[200:203],a[136:139],v[208:211],v[200:203]
ds_read_b64_tr_b16 a[236:237],v3 offset:8704
v_mfma_f32_16x16x32_bf16 v[200:203],a[140:143],v[240:243],v[200:203]
ds_read_b64_tr_b16 a[238:239],v3 offset:12800
v_mfma_f32_16x16x32_bf16 v[204:207],a[156:159],v[240:243],v[40:43]
ds_read_b64_tr_b16 a[232:233],v3 offset:512
ds_read_b64_tr_b16 a[234:235],v3 offset:4608
v_mfma_f32_16x16x32_bf16 v[204:207],a[152:155],v[208:211],v[204:207]
ds_read_b64_tr_b16 a[240:241],v2 offset:768
v_mfma_f32_16x16x32_bf16 v[204:207],a[148:151],v[176:179],v[204:207]
ds_read_b64_tr_b16 a[242:243],v2 offset:4864
v_mfma_f32_16x16x32_bf16 v[204:207],a[144:147],v[144:147],v[204:207]
s_add_u32 m0,s82,91136
ds_read_b64_tr_b16 a[244:245],v2 offset:8960
v_mfma_f32_16x16x32_bf16 v[216:219],a[160:163],v[144:147],v[40:43]
buffer_store_dwordx4 v[100:103],v6,s[36:39],s86 offen offset:1024 sc1
v_mfma_f32_16x16x32_bf16 v[216:219],a[164:167],v[176:179],v[216:219]
ds_read_b64_tr_b16 a[246:247],v2 offset:13056
ds_read_b64_tr_b16 a[252:253],v3 offset:8960
v_mfma_f32_16x16x32_bf16 v[216:219],a[168:171],v[208:211],v[216:219]
ds_read_b64_tr_b16 a[254:255],v3 offset:13056
v_add_u32 v2,s48,v2
v_mfma_f32_16x16x32_bf16 v[216:219],a[172:175],v[240:243],v[216:219]
ds_read_b64_tr_b16 a[248:249],v3 offset:768
v_mfma_f32_16x16x32_bf16 v[220:223],a[188:191],v[240:243],v[40:43]
buffer_load_dwordx4 v11,s[72:75],s83 offen lds
v_mfma_f32_16x16x32_bf16 v[220:223],a[184:187],v[208:211],v[220:223]
ds_read_b64_tr_b16 a[250:251],v3 offset:4864
v_add_u32 v3,s48,v3
v_mfma_f32_16x16x32_bf16 v[220:223],a[180:183],v[176:179],v[220:223]
v_exp_f32 v186,v186
v_mfma_f32_16x16x32_bf16 v[220:223],a[176:179],v[144:147],v[220:223]
s_waitcnt lgkmcnt(5)
v_exp_f32 v187,v187
v_mfma_f32_16x16x32_bf16 a[0:3],a[192:195],v[136:139],a[0:3]
v_exp_f32 v188,v188
v_mfma_f32_16x16x32_bf16 a[0:3],a[196:199],v[140:143],a[0:3]
s_add_u32 s80,s71,0
s_and_b32 s80,s80,3
s_lshl_b32 s80,s80,1
v_mfma_f32_16x16x32_bf16 a[4:7],a[204:207],v[140:143],a[4:7]
s_or_b32 s80,s80,1
s_mul_i32 s80,s80,163840
s_add_u32 s80,s80,s81
v_mfma_f32_16x16x32_bf16 a[4:7],a[200:203],v[136:139],a[4:7]
s_add_u32 s83,s80,20480
v_exp_f32 v189,v189
v_mfma_f32_16x16x32_bf16 a[8:11],a[208:211],v[136:139],a[8:11]
v_exp_f32 v190,v190
v_mfma_f32_16x16x32_bf16 a[8:11],a[212:215],v[140:143],a[8:11]
v_exp_f32 v191,v191
v_mfma_f32_16x16x32_bf16 a[12:15],a[220:223],v[140:143],a[12:15]
v_cvt_pk_bf16_f32 v173,v186,v187
v_cvt_pk_bf16_f32 v174,v188,v189
v_mfma_f32_16x16x32_bf16 a[12:15],a[216:219],v[136:139],a[12:15]
v_exp_f32 v200,v200
v_mfma_f32_16x16x32_bf16 a[16:19],a[224:227],v[136:139],a[16:19]
v_exp_f32 v201,v201
v_mfma_f32_16x16x32_bf16 a[16:19],a[228:231],v[140:143],a[16:19]
v_cvt_pk_bf16_f32 v175,v190,v191
v_cvt_pk_bf16_f32 v200,v200,v201
)A"
R"A(v_mfma_f32_16x16x32_bf16 a[20:23],a[236:239],v[140:143],a[20:23]
v_exp_f32 v202,v202
v_mfma_f32_16x16x32_bf16 a[20:23],a[232:235],v[136:139],a[20:23]
v_exp_f32 v203,v203
v_mfma_f32_16x16x32_bf16 a[24:27],a[240:243],v[136:139],a[24:27]
s_waitcnt lgkmcnt(0)
v_exp_f32 v204,v204
v_mfma_f32_16x16x32_bf16 a[24:27],a[244:247],v[140:143],a[24:27]
v_exp_f32 v205,v205
v_mfma_f32_16x16x32_bf16 a[28:31],a[252:255],v[140:143],a[28:31]
buffer_store_dwordx4 v[104:107],v6,s[36:39],s86 offen offset:2048 sc1
v_mfma_f32_16x16x32_bf16 a[28:31],a[248:251],v[136:139],a[28:31]
v_exp_f32 v206,v206
v_smfmac_f32_16x16x64_bf16 v[48:51],v[28:31],v[136:143],v27
v_cvt_pk_bf16_f32 v201,v202,v203
v_mfma_f32_16x16x32_bf16 v[232:235],a[128:131],v[148:151],v[44:47]
v_exp_f32 v207,v207
v_mfma_f32_16x16x32_bf16 v[232:235],a[132:135],v[180:183],v[232:235]
s_barrier
ds_read_b128 a[128:131],v1
v_cvt_pk_bf16_f32 v202,v204,v205
v_mfma_f32_16x16x32_bf16 v[232:235],a[136:139],v[212:215],v[232:235]
v_exp_f32 v216,v216
v_mfma_f32_16x16x32_bf16 v[232:235],a[140:143],v[244:247],v[232:235]
ds_read_b128 a[132:135],v1 offset:256
v_cvt_pk_bf16_f32 v203,v206,v207
v_mfma_f32_16x16x32_bf16 v[236:239],a[156:159],v[244:247],v[44:47]
s_add_u32 m0,s82,0
v_exp_f32 v217,v217
v_mfma_f32_16x16x32_bf16 v[236:239],a[152:155],v[212:215],v[236:239]
ds_read_b128 a[136:139],v1 offset:512
v_cvt_pk_bf16_f32 v204,v216,v217
v_mfma_f32_16x16x32_bf16 v[236:239],a[148:151],v[180:183],v[236:239]
v_exp_f32 v218,v218
v_mfma_f32_16x16x32_bf16 v[236:239],a[144:147],v[148:151],v[236:239]
buffer_load_dwordx4 v7,s[72:75],s80 offen lds
v_mfma_f32_16x16x32_bf16 v[248:251],a[160:163],v[148:151],v[44:47]
v_exp_f32 v219,v219
v_mfma_f32_16x16x32_bf16 v[248:251],a[164:167],v[180:183],v[248:251]
ds_read_b128 a[140:143],v1 offset:768
v_cvt_pk_bf16_f32 v205,v218,v219
v_mfma_f32_16x16x32_bf16 v[248:251],a[168:171],v[212:215],v[248:251]
v_exp_f32 v220,v220
v_mfma_f32_16x16x32_bf16 v[248:251],a[172:175],v[244:247],v[248:251]
ds_read_b128 a[156:159],v1 offset:4864
v_mfma_f32_16x16x32_bf16 v[252:255],a[188:191],v[244:247],v[44:47]
v_exp_f32 v221,v221
v_mfma_f32_16x16x32_bf16 v[252:255],a[184:187],v[212:215],v[252:255]
ds_read_b128 a[152:155],v1 offset:4608
v_cvt_pk_bf16_f32 v206,v220,v221
v_mfma_f32_16x16x32_bf16 v[252:255],a[180:183],v[180:183],v[252:255]
v_exp_f32 v222,v222
v_mfma_f32_16x16x32_bf16 v[252:255],a[176:179],v[148:151],v[252:255]
ds_read_b128 a[148:151],v1 offset:4352
v_mfma_f32_16x16x32_bf16 a[32:35],a[192:195],v[168:171],a[32:35]
v_exp_f32 v223,v223
v_mfma_f32_16x16x32_bf16 a[32:35],a[196:199],v[172:175],a[32:35]
ds_read_b128 a[144:147],v1 offset:4096
v_cvt_pk_bf16_f32 v207,v222,v223
v_mfma_f32_16x16x32_bf16 a[36:39],a[204:207],v[172:175],a[36:39]
buffer_store_dwordx4 v[108:111],v6,s[36:39],s86 offen offset:3072 sc1
v_mfma_f32_16x16x32_bf16 a[36:39],a[200:203],v[168:171],a[36:39]
s_add_u32 s86,s55,73728
s_max_i32 s86,s86,s100
ds_read_b128 a[160:163],v1 offset:8192
v_mfma_f32_16x16x32_bf16 a[40:43],a[208:211],v[168:171],a[40:43]
v_exp_f32 v232,v232
v_mfma_f32_16x16x32_bf16 a[40:43],a[212:215],v[172:175],a[40:43]
ds_read_b128 a[164:167],v1 offset:8448
v_mfma_f32_16x16x32_bf16 a[44:47],a[220:223],v[172:175],a[44:47]
buffer_load_dwordx4 v8,s[72:75],s80 offen offset:1024 lds
v_mfma_f32_16x16x32_bf16 a[44:47],a[216:219],v[168:171],a[44:47]
ds_read_b128 a[168:171],v1 offset:8704
v_mfma_f32_16x16x32_bf16 a[48:51],a[224:227],v[168:171],a[48:51]
v_exp_f32 v233,v233
v_mfma_f32_16x16x32_bf16 a[48:51],a[228:231],v[172:175],a[48:51]
ds_read_b128 a[172:175],v1 offset:8960
v_cvt_pk_bf16_f32 v232,v232,v233
v_mfma_f32_16x16x32_bf16 a[52:55],a[236:239],v[172:175],a[52:55]
v_exp_f32 v234,v234
v_mfma_f32_16x16x32_bf16 a[52:55],a[232:235],v[168:171],a[52:55]
ds_read_b128 a[188:191],v1 offset:13056
v_mfma_f32_16x16x32_bf16 a[56:59],a[240:243],v[168:171],a[56:59]
v_exp_f32 v235,v235
v_mfma_f32_16x16x32_bf16 a[56:59],a[244:247],v[172:175],a[56:59]
buffer_load_dwordx4 v9,s[72:75],s80 offen offset:2048 lds
v_mfma_f32_16x16x32_bf16 a[60:63],a[252:255],v[172:175],a[60:63]
ds_read_b128 a[184:187],v1 offset:12800
v_cvt_pk_bf16_f32 v233,v234,v235
v_mfma_f32_16x16x32_bf16 a[60:63],a[248:251],v[168:171],a[60:63]
v_exp_f32 v236,v236
v_smfmac_f32_16x16x64_bf16 v[52:55],v[28:31],v[168:175],v27
s_waitcnt lgkmcnt(3)
ds_read_b128 a[180:183],v1 offset:12544
v_mfma_f32_16x16x32_bf16 v[136:139],a[128:131],v[128:131],v[32:35]
v_exp_f32 v237,v237
v_mfma_f32_16x16x32_bf16 v[136:139],a[132:135],v[160:163],v[136:139]
ds_read_b128 a[176:179],v1 offset:12288
v_cvt_pk_bf16_f32 v234,v236,v237
v_mfma_f32_16x16x32_bf16 v[136:139],a[136:139],v[192:195],v[136:139]
v_exp_f32 v238,v238
v_mfma_f32_16x16x32_bf16 v[136:139],a[140:143],v[224:227],v[136:139]
v_exp_f32 v239,v239
v_mfma_f32_16x16x32_bf16 v[140:143],a[156:159],v[224:227],v[32:35]
v_cvt_pk_bf16_f32 v235,v238,v239
v_add_u32 v1,v4,v5
v_mfma_f32_16x16x32_bf16 v[140:143],a[152:155],v[192:195],v[140:143]
v_exp_f32 v248,v248
v_mfma_f32_16x16x32_bf16 v[140:143],a[148:151],v[160:163],v[140:143]
v_exp_f32 v249,v249
v_mfma_f32_16x16x32_bf16 v[140:143],a[144:147],v[128:131],v[140:143]
buffer_store_dwordx4 v[112:115],v6,s[36:39],s87 offen sc1
v_mfma_f32_16x16x32_bf16 v[152:155],a[160:163],v[128:131],v[32:35]
v_exp_f32 v250,v250
v_mfma_f32_16x16x32_bf16 v[152:155],a[164:167],v[160:163],v[152:155]
v_exp_f32 v251,v251
v_mfma_f32_16x16x32_bf16 v[152:155],a[168:171],v[192:195],v[152:155]
s_waitcnt lgkmcnt(0)
v_exp_f32 v252,v252
v_mfma_f32_16x16x32_bf16 v[152:155],a[172:175],v[224:227],v[152:155]
buffer_load_dwordx4 v10,s[72:75],s80 offen offset:3072 lds
v_mfma_f32_16x16x32_bf16 v[156:159],a[188:191],v[224:227],v[32:35]
v_exp_f32 v253,v253
v_mfma_f32_16x16x32_bf16 v[156:159],a[184:187],v[192:195],v[156:159]
v_exp_f32 v254,v254
v_mfma_f32_16x16x32_bf16 v[156:159],a[180:183],v[160:163],v[156:159]
v_exp_f32 v255,v255
v_mfma_f32_16x16x32_bf16 v[156:159],a[176:179],v[128:131],v[156:159]
s_add_u32 m0,s82,4096
v_cvt_pk_bf16_f32 v236,v248,v249
v_cvt_pk_bf16_f32 v237,v250,v251
v_mfma_f32_16x16x32_bf16 a[64:67],a[192:195],v[200:203],a[64:67]
v_cvt_pk_bf16_f32 v238,v252,v253
v_cvt_pk_bf16_f32 v239,v254,v255
v_mfma_f32_16x16x32_bf16 a[64:67],a[196:199],v[204:207],a[64:67]
v_exp_f32 v136,v136
v_mfma_f32_16x16x32_bf16 a[68:71],a[204:207],v[204:207],a[68:71]
buffer_load_dwordx4 v11,s[72:75],s80 offen lds
v_mfma_f32_16x16x32_bf16 a[68:71],a[200:203],v[200:203],a[68:71]
v_exp_f32 v137,v137
v_mfma_f32_16x16x32_bf16 a[72:75],a[208:211],v[200:203],a[72:75]
v_exp_f32 v138,v138
v_mfma_f32_16x16x32_bf16 a[72:75],a[212:215],v[204:207],a[72:75]
v_exp_f32 v139,v139
v_mfma_f32_16x16x32_bf16 a[76:79],a[220:223],v[204:207],a[76:79]
v_cvt_pk_bf16_f32 v136,v136,v137
v_cvt_pk_bf16_f32 v137,v138,v139
v_mfma_f32_16x16x32_bf16 a[76:79],a[216:219],v[200:203],a[76:79]
v_exp_f32 v140,v140
v_mfma_f32_16x16x32_bf16 a[80:83],a[224:227],v[200:203],a[80:83]
v_exp_f32 v141,v141
v_mfma_f32_16x16x32_bf16 a[80:83],a[228:231],v[204:207],a[80:83]
v_exp_f32 v142,v142
v_mfma_f32_16x16x32_bf16 a[84:87],a[236:239],v[204:207],a[84:87]
s_add_u32 m0,s82,5120
v_exp_f32 v143,v143
v_mfma_f32_16x16x32_bf16 a[84:87],a[232:235],v[200:203],a[84:87]
v_cvt_pk_bf16_f32 v138,v140,v141
v_cvt_pk_bf16_f32 v139,v142,v143
v_mfma_f32_16x16x32_bf16 a[88:91],a[240:243],v[200:203],a[88:91]
v_exp_f32 v152,v152
v_mfma_f32_16x16x32_bf16 a[88:91],a[244:247],v[204:207],a[88:91]
buffer_store_dwordx4 v[116:119],v6,s[36:39],s87 offen offset:1024 sc1
v_mfma_f32_16x16x32_bf16 a[92:95],a[252:255],v[204:207],a[92:95]
v_exp_f32 v153,v153
v_mfma_f32_16x16x32_bf16 a[92:95],a[248:251],v[200:203],a[92:95]
v_exp_f32 v154,v154
v_smfmac_f32_16x16x64_bf16 v[56:59],v[28:31],v[200:207],v27
v_cvt_pk_bf16_f32 v140,v152,v153
v_mfma_f32_16x16x32_bf16 v[168:171],a[128:131],v[132:135],v[36:39]
buffer_load_dwordx4 v7,s[72:75],s83 offen lds
v_mfma_f32_16x16x32_bf16 v[168:171],a[132:135],v[164:167],v[168:171]
v_exp_f32 v155,v155
v_mfma_f32_16x16x32_bf16 v[168:171],a[136:139],v[196:199],v[168:171]
v_exp_f32 v156,v156
v_mfma_f32_16x16x32_bf16 v[168:171],a[140:143],v[228:231],v[168:171]
v_exp_f32 v157,v157
v_mfma_f32_16x16x32_bf16 v[172:175],a[156:159],v[228:231],v[36:39]
v_exp_f32 v158,v158
v_mfma_f32_16x16x32_bf16 v[172:175],a[152:155],v[196:199],v[172:175]
v_exp_f32 v159,v159
v_mfma_f32_16x16x32_bf16 v[172:175],a[148:151],v[164:167],v[172:175]
v_cvt_pk_bf16_f32 v141,v154,v155
v_cvt_pk_bf16_f32 v142,v156,v157
v_mfma_f32_16x16x32_bf16 v[172:175],a[144:147],v[132:135],v[172:175]
buffer_load_dwordx4 v8,s[72:75],s83 offen offset:1024 lds
v_mfma_f32_16x16x32_bf16 v[184:187],a[160:163],v[132:135],v[36:39]
v_cvt_pk_bf16_f32 v143,v158,v159
v_add_u32 v1,81920,v1
v_mfma_f32_16x16x32_bf16 v[184:187],a[164:167],v[164:167],v[184:187]
v_exp_f32 v168,v168
v_mfma_f32_16x16x32_bf16 v[184:187],a[168:171],v[196:199],v[184:187]
v_exp_f32 v169,v169
v_mfma_f32_16x16x32_bf16 v[184:187],a[172:175],v[228:231],v[184:187]
v_exp_f32 v170,v170
v_mfma_f32_16x16x32_bf16 v[188:191],a[188:191],v[228:231],v[36:39]
v_exp_f32 v171,v171
v_mfma_f32_16x16x32_bf16 v[188:191],a[184:187],v[196:199],v[188:191]
v_exp_f32 v172,v172
v_mfma_f32_16x16x32_bf16 v[188:191],a[180:183],v[164:167],v[188:191]
v_exp_f32 v173,v173
v_mfma_f32_16x16x32_bf16 v[188:191],a[176:179],v[132:135],v[188:191]
v_exp_f32 v174,v174
v_mfma_f32_16x16x32_bf16 a[96:99],a[192:195],v[232:235],a[96:99]
ds_read_b64_tr_b16 a[192:193],v2
ds_read_b64_tr_b16 a[194:195],v2 offset:4096
v_mfma_f32_16x16x32_bf16 a[96:99],a[196:199],v[236:239],a[96:99]
ds_read_b64_tr_b16 a[196:197],v2 offset:8192
v_cvt_pk_bf16_f32 v168,v168,v169
v_mfma_f32_16x16x32_bf16 a[100:103],a[204:207],v[236:239],a[100:103]
buffer_store_dwordx4 v[120:123],v6,s[36:39],s87 offen offset:2048 sc1
v_mfma_f32_16x16x32_bf16 a[100:103],a[200:203],v[232:235],a[100:103]
ds_read_b64_tr_b16 a[198:199],v2 offset:12288
ds_read_b64_tr_b16 a[204:205],v3 offset:8192
v_mfma_f32_16x16x32_bf16 a[104:107],a[208:211],v[232:235],a[104:107]
ds_read_b64_tr_b16 a[206:207],v3 offset:12288
v_cvt_pk_bf16_f32 v169,v170,v171
v_mfma_f32_16x16x32_bf16 a[104:107],a[212:215],v[236:239],a[104:107]
ds_read_b64_tr_b16 a[200:201],v3
v_cvt_pk_bf16_f32 v170,v172,v173
v_mfma_f32_16x16x32_bf16 a[108:111],a[220:223],v[236:239],a[108:111]
buffer_load_dwordx4 v9,s[72:75],s83 offen offset:2048 lds
v_mfma_f32_16x16x32_bf16 a[108:111],a[216:219],v[232:235],a[108:111]
ds_read_b64_tr_b16 a[202:203],v3 offset:4096
ds_read_b64_tr_b16 a[208:209],v2 offset:256
v_mfma_f32_16x16x32_bf16 a[112:115],a[224:227],v[232:235],a[112:115]
v_exp_f32 v175,v175
v_mfma_f32_16x16x32_bf16 a[112:115],a[228:231],v[236:239],a[112:115]
ds_read_b64_tr_b16 a[210:211],v2 offset:4352
ds_read_b64_tr_b16 a[212:213],v2 offset:8448
v_mfma_f32_16x16x32_bf16 a[116:119],a[236:239],v[236:239],a[116:119]
ds_read_b64_tr_b16 a[214:215],v2 offset:12544
v_cvt_pk_bf16_f32 v171,v174,v175
v_mfma_f32_16x16x32_bf16 a[116:119],a[232:235],v[232:235],a[116:119]
s_cmp_ge_u32 0,s71
s_cselect_b32 s80,2,0
ds_read_b64_tr_b16 a[220:221],v3 offset:8448
v_mfma_f32_16x16x32_bf16 a[120:123],a[240:243],v[232:235],a[120:123]
s_mul_i32 s80,s80,163840
v_exp_f32 v184,v184
v_mfma_f32_16x16x32_bf16 a[120:123],a[244:247],v[236:239],a[120:123]
s_add_u32 s80,s80,s81
buffer_load_dwordx4 v10,s[72:75],s83 offen offset:3072 lds
v_mfma_f32_16x16x32_bf16 a[124:127],a[252:255],v[236:239],a[124:127]
ds_read_b64_tr_b16 a[222:223],v3 offset:12544
ds_read_b64_tr_b16 a[216:217],v3 offset:256
v_mfma_f32_16x16x32_bf16 a[124:127],a[248:251],v[232:235],a[124:127]
ds_read_b64_tr_b16 a[218:219],v3 offset:4352
v_smfmac_f32_16x16x64_bf16 v[60:63],v[28:31],v[232:239],v27
ds_read_b64_tr_b16 a[224:225],v2 offset:512
v_mfma_f32_16x16x32_bf16 v[200:203],a[128:131],v[144:147],v[40:43]
ds_read_b64_tr_b16 a[226:227],v2 offset:4608
v_mfma_f32_16x16x32_bf16 v[200:203],a[132:135],v[176:179],v[200:203]
ds_read_b64_tr_b16 a[228:229],v2 offset:8704
ds_read_b64_tr_b16 a[230:231],v2 offset:12800
v_mfma_f32_16x16x32_bf16 v[200:203],a[136:139],v[208:211],v[200:203]
ds_read_b64_tr_b16 a[236:237],v3 offset:8704
v_mfma_f32_16x16x32_bf16 v[200:203],a[140:143],v[240:243],v[200:203]
)A"
R"A(ds_read_b64_tr_b16 a[238:239],v3 offset:12800
v_mfma_f32_16x16x32_bf16 v[204:207],a[156:159],v[240:243],v[40:43]
s_add_u32 m0,s82,9216
ds_read_b64_tr_b16 a[232:233],v3 offset:512
v_mfma_f32_16x16x32_bf16 v[204:207],a[152:155],v[208:211],v[204:207]
ds_read_b64_tr_b16 a[234:235],v3 offset:4608
ds_read_b64_tr_b16 a[240:241],v2 offset:768
v_mfma_f32_16x16x32_bf16 v[204:207],a[148:151],v[176:179],v[204:207]
ds_read_b64_tr_b16 a[242:243],v2 offset:4864
v_mfma_f32_16x16x32_bf16 v[204:207],a[144:147],v[144:147],v[204:207]
buffer_store_dwordx4 v[124:127],v6,s[36:39],s87 offen offset:3072 sc1
v_mfma_f32_16x16x32_bf16 v[216:219],a[160:163],v[144:147],v[40:43]
s_add_u32 s87,s55,77824
ds_read_b64_tr_b16 a[244:245],v2 offset:8960
ds_read_b64_tr_b16 a[246:247],v2 offset:13056
v_mfma_f32_16x16x32_bf16 v[216:219],a[164:167],v[176:179],v[216:219]
s_max_i32 s87,s87,s101
ds_read_b64_tr_b16 a[252:253],v3 offset:8960
v_sub_u32 v2,v4,v5
v_mfma_f32_16x16x32_bf16 v[216:219],a[168:171],v[208:211],v[216:219]
ds_read_b64_tr_b16 a[254:255],v3 offset:13056
v_add_u32 v2,81920,v2
v_mfma_f32_16x16x32_bf16 v[216:219],a[172:175],v[240:243],v[216:219]
buffer_load_dwordx4 v11,s[72:75],s83 offen lds
v_mfma_f32_16x16x32_bf16 v[220:223],a[188:191],v[240:243],v[40:43]
s_add_u32 s83,s80,20480
ds_read_b64_tr_b16 a[248:249],v3 offset:768
ds_read_b64_tr_b16 a[250:251],v3 offset:4864
v_mfma_f32_16x16x32_bf16 v[220:223],a[184:187],v[208:211],v[220:223]
v_exp_f32 v185,v185
v_mfma_f32_16x16x32_bf16 v[220:223],a[180:183],v[176:179],v[220:223]
v_exp_f32 v186,v186
v_mfma_f32_16x16x32_bf16 v[220:223],a[176:179],v[144:147],v[220:223]
s_add_u32 m0,s82,40960
v_exp_f32 v187,v187
s_waitcnt lgkmcnt(6)
v_mfma_f32_16x16x32_bf16 a[0:3],a[192:195],v[136:139],a[0:3]
v_exp_f32 v188,v188
v_mfma_f32_16x16x32_bf16 a[0:3],a[196:199],v[140:143],a[0:3]
v_exp_f32 v189,v189
v_mfma_f32_16x16x32_bf16 a[4:7],a[204:207],v[140:143],a[4:7]
buffer_load_dwordx4 v7,s[72:75],s80 offen lds
v_mfma_f32_16x16x32_bf16 a[4:7],a[200:203],v[136:139],a[4:7]
v_exp_f32 v190,v190
v_mfma_f32_16x16x32_bf16 a[8:11],a[208:211],v[136:139],a[8:11]
v_exp_f32 v191,v191
v_mfma_f32_16x16x32_bf16 a[8:11],a[212:215],v[140:143],a[8:11]
v_cvt_pk_bf16_f32 v172,v184,v185
v_cvt_pk_bf16_f32 v173,v186,v187
v_mfma_f32_16x16x32_bf16 a[12:15],a[220:223],v[140:143],a[12:15]
v_cvt_pk_bf16_f32 v174,v188,v189
v_cvt_pk_bf16_f32 v175,v190,v191
v_mfma_f32_16x16x32_bf16 a[12:15],a[216:219],v[136:139],a[12:15]
v_exp_f32 v200,v200
v_mfma_f32_16x16x32_bf16 a[16:19],a[224:227],v[136:139],a[16:19]
v_exp_f32 v201,v201
v_mfma_f32_16x16x32_bf16 a[16:19],a[228:231],v[140:143],a[16:19]
v_exp_f32 v202,v202
v_mfma_f32_16x16x32_bf16 a[20:23],a[236:239],v[140:143],a[20:23]
v_exp_f32 v203,v203
v_mfma_f32_16x16x32_bf16 a[20:23],a[232:235],v[136:139],a[20:23]
v_exp_f32 v204,v204
v_mfma_f32_16x16x32_bf16 a[24:27],a[240:243],v[136:139],a[24:27]
s_waitcnt lgkmcnt(0)
v_exp_f32 v205,v205
v_mfma_f32_16x16x32_bf16 a[24:27],a[244:247],v[140:143],a[24:27]
buffer_load_dwordx4 v8,s[72:75],s80 offen offset:1024 lds
v_mfma_f32_16x16x32_bf16 a[28:31],a[252:255],v[140:143],a[28:31]
v_exp_f32 v206,v206
v_mfma_f32_16x16x32_bf16 a[28:31],a[248:251],v[136:139],a[28:31]
s_waitcnt vmcnt(18)
v_cvt_pk_bf16_f32 v200,v200,v201
v_cvt_pk_bf16_f32 v201,v202,v203
v_smfmac_f32_16x16x64_bf16 v[48:51],v[28:31],v[136:143],v27
s_barrier
v_exp_f32 v207,v207
v_mfma_f32_16x16x32_bf16 v[232:235],a[128:131],v[148:151],v[44:47]
v_cvt_pk_bf16_f32 v202,v204,v205
v_cvt_pk_bf16_f32 v203,v206,v207
v_mfma_f32_16x16x32_bf16 v[232:235],a[132:135],v[180:183],v[232:235]
v_exp_f32 v216,v216
v_mfma_f32_16x16x32_bf16 v[232:235],a[136:139],v[212:215],v[232:235]
v_exp_f32 v217,v217
v_mfma_f32_16x16x32_bf16 v[232:235],a[140:143],v[244:247],v[232:235]
v_exp_f32 v218,v218
v_mfma_f32_16x16x32_bf16 v[236:239],a[156:159],v[244:247],v[44:47]
v_exp_f32 v219,v219
v_mfma_f32_16x16x32_bf16 v[236:239],a[152:155],v[212:215],v[236:239]
v_exp_f32 v220,v220
v_mfma_f32_16x16x32_bf16 v[236:239],a[148:151],v[180:183],v[236:239]
buffer_load_dwordx4 v9,s[72:75],s80 offen offset:2048 lds
v_mfma_f32_16x16x32_bf16 v[236:239],a[144:147],v[148:151],v[236:239]
v_exp_f32 v221,v221
v_mfma_f32_16x16x32_bf16 v[248:251],a[160:163],v[148:151],v[44:47]
v_exp_f32 v222,v222
v_mfma_f32_16x16x32_bf16 v[248:251],a[164:167],v[180:183],v[248:251]
v_exp_f32 v223,v223
v_mfma_f32_16x16x32_bf16 v[248:251],a[168:171],v[212:215],v[248:251]
v_cvt_pk_bf16_f32 v204,v216,v217
v_cvt_pk_bf16_f32 v205,v218,v219
v_mfma_f32_16x16x32_bf16 v[248:251],a[172:175],v[244:247],v[248:251]
v_cvt_pk_bf16_f32 v206,v220,v221
v_cvt_pk_bf16_f32 v207,v222,v223
v_mfma_f32_16x16x32_bf16 v[252:255],a[188:191],v[244:247],v[44:47]
v_exp_f32 v232,v232
v_mfma_f32_16x16x32_bf16 v[252:255],a[184:187],v[212:215],v[252:255]
v_exp_f32 v233,v233
v_mfma_f32_16x16x32_bf16 v[252:255],a[180:183],v[180:183],v[252:255]
v_exp_f32 v234,v234
v_mfma_f32_16x16x32_bf16 v[252:255],a[176:179],v[148:151],v[252:255]
v_exp_f32 v235,v235
v_mfma_f32_16x16x32_bf16 a[32:35],a[192:195],v[168:171],a[32:35]
v_cvt_pk_bf16_f32 v232,v232,v233
v_add_f32 v48,v49,v48
v_mfma_f32_16x16x32_bf16 a[32:35],a[196:199],v[172:175],a[32:35]
buffer_load_dwordx4 v10,s[72:75],s80 offen offset:3072 lds
v_mfma_f32_16x16x32_bf16 a[36:39],a[204:207],v[172:175],a[36:39]
v_exp_f32 v236,v236
v_mfma_f32_16x16x32_bf16 a[36:39],a[200:203],v[168:171],a[36:39]
v_exp_f32 v237,v237
v_mfma_f32_16x16x32_bf16 a[40:43],a[208:211],v[168:171],a[40:43]
v_exp_f32 v238,v238
v_mfma_f32_16x16x32_bf16 a[40:43],a[212:215],v[172:175],a[40:43]
v_exp_f32 v239,v239
v_mfma_f32_16x16x32_bf16 a[44:47],a[220:223],v[172:175],a[44:47]
v_cvt_pk_bf16_f32 v233,v234,v235
v_max_f32 v20,0x00800000,v48
v_mfma_f32_16x16x32_bf16 a[44:47],a[216:219],v[168:171],a[44:47]
s_lshl_b32 s95,s42,2
s_add_u32 s95,s95,s40
s_lshl_b32 s95,s95,4
v_mfma_f32_16x16x32_bf16 a[48:51],a[224:227],v[168:171],a[48:51]
v_mov_b32 v18,s68
v_mov_b32 v19,s69
v_mfma_f32_16x16x32_bf16 a[48:51],a[228:231],v[172:175],a[48:51]
s_add_u32 m0,s82,45056
v_cvt_pk_bf16_f32 v234,v236,v237
v_cvt_pk_bf16_f32 v235,v238,v239
v_mfma_f32_16x16x32_bf16 a[52:55],a[236:239],v[172:175],a[52:55]
v_exp_f32 v248,v248
v_mfma_f32_16x16x32_bf16 a[52:55],a[232:235],v[168:171],a[52:55]
v_exp_f32 v249,v249
v_mfma_f32_16x16x32_bf16 a[56:59],a[240:243],v[168:171],a[56:59]
buffer_load_dwordx4 v11,s[72:75],s80 offen lds
v_mfma_f32_16x16x32_bf16 a[56:59],a[244:247],v[172:175],a[56:59]
v_exp_f32 v250,v250
v_mfma_f32_16x16x32_bf16 a[60:63],a[252:255],v[172:175],a[60:63]
v_exp_f32 v251,v251
v_mfma_f32_16x16x32_bf16 a[60:63],a[248:251],v[168:171],a[60:63]
v_exp_f32 v252,v252
v_smfmac_f32_16x16x64_bf16 v[52:55],v[28:31],v[168:175],v27
s_waitcnt vmcnt(6)
s_mov_b64 exec,1
buffer_store_dwordx2 v[18:19],v6,s[64:67],s95 offen sc1
s_mov_b64 exec,-1
v_mfma_f32_16x16x32_bf16 a[64:67],a[192:195],v[200:203],a[64:67]
v_exp_f32 v253,v253
v_mfma_f32_16x16x32_bf16 a[64:67],a[196:199],v[204:207],a[64:67]
v_exp_f32 v254,v254
v_mfma_f32_16x16x32_bf16 a[68:71],a[204:207],v[204:207],a[68:71]
v_exp_f32 v255,v255
v_mfma_f32_16x16x32_bf16 a[68:71],a[200:203],v[200:203],a[68:71]
s_add_u32 m0,s82,46080
v_rcp_f32 v20,v20
v_mfma_f32_16x16x32_bf16 a[72:75],a[208:211],v[200:203],a[72:75]
v_cvt_pk_bf16_f32 v236,v248,v249
v_cvt_pk_bf16_f32 v237,v250,v251
v_mfma_f32_16x16x32_bf16 a[72:75],a[212:215],v[204:207],a[72:75]
v_cvt_pk_bf16_f32 v238,v252,v253
v_cvt_pk_bf16_f32 v239,v254,v255
v_mfma_f32_16x16x32_bf16 a[76:79],a[220:223],v[204:207],a[76:79]
buffer_load_dwordx4 v7,s[72:75],s83 offen lds
v_mfma_f32_16x16x32_bf16 a[76:79],a[216:219],v[200:203],a[76:79]
v_accvgpr_read_b32 v136,a0
v_accvgpr_read_b32 v137,a1
v_accvgpr_read_b32 v138,a2
v_accvgpr_read_b32 v139,a3
v_accvgpr_read_b32 v12,a4
v_accvgpr_read_b32 v13,a5
v_accvgpr_read_b32 v14,a6
v_accvgpr_read_b32 v15,a7
v_pk_mul_f32 v[136:137],v[20:21],v[136:137] op_sel:[0,0] op_sel_hi:[0,1]
v_pk_mul_f32 v[138:139],v[20:21],v[138:139] op_sel:[0,0] op_sel_hi:[0,1]
v_pk_mul_f32 v[12:13],v[20:21],v[12:13] op_sel:[0,0] op_sel_hi:[0,1]
v_pk_mul_f32 v[14:15],v[20:21],v[14:15] op_sel:[0,0] op_sel_hi:[0,1]
v_cvt_pk_bf16_f32 v136,v136,v137
v_cvt_pk_bf16_f32 v137,v138,v139
v_cvt_pk_bf16_f32 v138,v12,v13
v_cvt_pk_bf16_f32 v139,v14,v15
v_mfma_f32_16x16x32_bf16 a[80:83],a[224:227],v[200:203],a[80:83]
ds_read_b64_tr_b16 a[128:129],v1
ds_read_b64_tr_b16 a[130:131],v1 offset:5120
v_mfma_f32_16x16x32_bf16 a[80:83],a[228:231],v[204:207],a[80:83]
ds_read_b64_tr_b16 a[132:133],v1 offset:10240
v_accvgpr_read_b32 v152,a16
v_mfma_f32_16x16x32_bf16 a[84:87],a[236:239],v[204:207],a[84:87]
ds_read_b64_tr_b16 a[134:135],v1 offset:15360
v_accvgpr_read_b32 v153,a17
v_mfma_f32_16x16x32_bf16 a[84:87],a[232:235],v[200:203],a[84:87]
ds_read_b64_tr_b16 a[136:137],v1 offset:20480
v_mul_f32 v152,v20,v152
v_mfma_f32_16x16x32_bf16 a[88:91],a[240:243],v[200:203],a[88:91]
ds_read_b64_tr_b16 a[138:139],v1 offset:25600
ds_read_b64_tr_b16 a[140:141],v1 offset:30720
v_mfma_f32_16x16x32_bf16 a[88:91],a[244:247],v[204:207],a[88:91]
v_accvgpr_read_b32 v140,a8
v_accvgpr_read_b32 v141,a9
v_accvgpr_read_b32 v142,a10
v_accvgpr_read_b32 v143,a11
v_accvgpr_read_b32 v12,a12
v_accvgpr_read_b32 v13,a13
v_accvgpr_read_b32 v14,a14
v_accvgpr_read_b32 v15,a15
v_pk_mul_f32 v[140:141],v[20:21],v[140:141] op_sel:[0,0] op_sel_hi:[0,1]
v_pk_mul_f32 v[142:143],v[20:21],v[142:143] op_sel:[0,0] op_sel_hi:[0,1]
v_pk_mul_f32 v[12:13],v[20:21],v[12:13] op_sel:[0,0] op_sel_hi:[0,1]
v_pk_mul_f32 v[14:15],v[20:21],v[14:15] op_sel:[0,0] op_sel_hi:[0,1]
v_cvt_pk_bf16_f32 v140,v140,v141
v_cvt_pk_bf16_f32 v141,v142,v143
v_cvt_pk_bf16_f32 v142,v12,v13
v_cvt_pk_bf16_f32 v143,v14,v15
v_mfma_f32_16x16x32_bf16 a[92:95],a[252:255],v[204:207],a[92:95]
ds_read_b64_tr_b16 a[142:143],v1 offset:35840
v_mul_f32 v153,v20,v153
v_mfma_f32_16x16x32_bf16 a[92:95],a[248:251],v[200:203],a[92:95]
v_accvgpr_read_b32 v154,a18
v_accvgpr_read_b32 v155,a19
v_smfmac_f32_16x16x64_bf16 v[56:59],v[28:31],v[200:207],v27
v_cvt_pk_bf16_f32 v152,v152,v153
v_mfma_f32_16x16x32_bf16 a[96:99],a[192:195],v[232:235],a[96:99]
buffer_store_dwordx4 v[136:139],v6,s[36:39],s84 offen sc1
v_mfma_f32_16x16x32_bf16 a[96:99],a[196:199],v[236:239],a[96:99]
v_mul_f32 v154,v20,v154
v_mul_f32 v155,v20,v155
v_mfma_f32_16x16x32_bf16 a[100:103],a[204:207],v[236:239],a[100:103]
v_cvt_pk_bf16_f32 v153,v154,v155
v_accvgpr_read_b32 v154,a20
v_mfma_f32_16x16x32_bf16 a[100:103],a[200:203],v[232:235],a[100:103]
v_accvgpr_read_b32 v155,a21
v_accvgpr_read_b32 v12,a22
v_mfma_f32_16x16x32_bf16 a[104:107],a[208:211],v[232:235],a[104:107]
buffer_load_dwordx4 v8,s[72:75],s83 offen offset:1024 lds
v_mfma_f32_16x16x32_bf16 a[104:107],a[212:215],v[236:239],a[104:107]
v_accvgpr_read_b32 v13,a23
v_mul_f32 v154,v20,v154
v_mfma_f32_16x16x32_bf16 a[108:111],a[220:223],v[236:239],a[108:111]
v_mul_f32 v155,v20,v155
v_mul_f32 v12,v20,v12
v_mfma_f32_16x16x32_bf16 a[108:111],a[216:219],v[232:235],a[108:111]
v_mul_f32 v13,v20,v13
v_cvt_pk_bf16_f32 v154,v154,v155
v_mfma_f32_16x16x32_bf16 a[112:115],a[224:227],v[232:235],a[112:115]
ds_read_b64_tr_b16 a[144:145],v2 offset:32
v_cvt_pk_bf16_f32 v155,v12,v13
v_mfma_f32_16x16x32_bf16 a[112:115],a[228:231],v[236:239],a[112:115]
ds_read_b64_tr_b16 a[146:147],v2 offset:5152
ds_read_b64_tr_b16 a[148:149],v2 offset:10272
v_mfma_f32_16x16x32_bf16 a[116:119],a[236:239],v[236:239],a[116:119]
ds_read_b64_tr_b16 a[150:151],v2 offset:15392
v_accvgpr_read_b32 v156,a24
v_mfma_f32_16x16x32_bf16 a[116:119],a[232:235],v[232:235],a[116:119]
buffer_load_dwordx4 v9,s[72:75],s83 offen offset:2048 lds
v_mfma_f32_16x16x32_bf16 a[120:123],a[240:243],v[232:235],a[120:123]
ds_read_b64_tr_b16 a[152:153],v2 offset:20512
ds_read_b64_tr_b16 a[154:155],v2 offset:25632
v_mfma_f32_16x16x32_bf16 a[120:123],a[244:247],v[236:239],a[120:123]
ds_read_b64_tr_b16 a[156:157],v2 offset:30752
v_accvgpr_read_b32 v157,a25
v_mfma_f32_16x16x32_bf16 a[124:127],a[252:255],v[236:239],a[124:127]
ds_read_b64_tr_b16 a[158:159],v2 offset:35872
v_mul_f32 v156,v20,v156
v_mfma_f32_16x16x32_bf16 a[124:127],a[248:251],v[232:235],a[124:127]
v_mul_f32 v157,v20,v157
)A"
R"A(v_accvgpr_read_b32 v158,a26
v_smfmac_f32_16x16x64_bf16 v[60:63],v[28:31],v[232:239],v27
s_waitcnt lgkmcnt(7)
v_accvgpr_read_b32 v159,a27
v_mfma_f32_16x16x32_bf16 a[192:195],a[128:131],v[64:67],0
v_cvt_pk_bf16_f32 v156,v156,v157
v_mul_f32 v158,v20,v158
v_mfma_f32_16x16x32_bf16 a[192:195],a[132:135],v[68:71],a[192:195]
v_mul_f32 v159,v20,v159
v_cvt_pk_bf16_f32 v157,v158,v159
v_mfma_f32_16x16x32_bf16 a[192:195],a[136:139],v[72:75],a[192:195]
buffer_store_dwordx4 v[140:143],v6,s[36:39],s84 offen offset:1024 sc1
v_mfma_f32_16x16x32_bf16 a[192:195],a[140:143],v[76:79],a[192:195]
v_accvgpr_read_b32 v158,a28
v_accvgpr_read_b32 v159,a29
v_mfma_f32_16x16x32_bf16 a[196:199],a[140:143],v[92:95],0
v_accvgpr_read_b32 v14,a30
v_accvgpr_read_b32 v15,a31
v_mfma_f32_16x16x32_bf16 a[196:199],a[136:139],v[88:91],a[196:199]
v_mul_f32 v158,v20,v158
v_mul_f32 v159,v20,v159
v_mfma_f32_16x16x32_bf16 a[196:199],a[132:135],v[84:87],a[196:199]
buffer_load_dwordx4 v10,s[72:75],s83 offen offset:3072 lds
v_mfma_f32_16x16x32_bf16 a[196:199],a[128:131],v[80:83],a[196:199]
v_mul_f32 v14,v20,v14
v_mul_f32 v15,v20,v15
v_mfma_f32_16x16x32_bf16 a[200:203],a[128:131],v[96:99],0
v_cvt_pk_bf16_f32 v158,v158,v159
v_cvt_pk_bf16_f32 v159,v14,v15
v_mfma_f32_16x16x32_bf16 a[200:203],a[132:135],v[100:103],a[200:203]
ds_read_b64_tr_b16 a[160:161],v1 offset:64
ds_read_b64_tr_b16 a[162:163],v1 offset:5184
v_mfma_f32_16x16x32_bf16 a[200:203],a[136:139],v[104:107],a[200:203]
ds_read_b64_tr_b16 a[164:165],v1 offset:10304
v_add_f32 v52,v53,v52
v_mfma_f32_16x16x32_bf16 a[200:203],a[140:143],v[108:111],a[200:203]
ds_read_b64_tr_b16 a[166:167],v1 offset:15424
v_max_f32 v21,0x00800000,v52
v_mfma_f32_16x16x32_bf16 a[204:207],a[140:143],v[124:127],0
s_cmp_ge_u32 1,s71
s_cselect_b32 s80,4,2
ds_read_b64_tr_b16 a[168:169],v1 offset:20544
v_mfma_f32_16x16x32_bf16 a[204:207],a[136:139],v[120:123],a[204:207]
s_add_u32 m0,s82,50176
ds_read_b64_tr_b16 a[170:171],v1 offset:25664
ds_read_b64_tr_b16 a[172:173],v1 offset:30784
v_mfma_f32_16x16x32_bf16 a[204:207],a[132:135],v[116:119],a[204:207]
s_mul_i32 s80,s80,163840
s_add_u32 s80,s80,s81
ds_read_b64_tr_b16 a[174:175],v1 offset:35904
v_mfma_f32_16x16x32_bf16 a[204:207],a[128:131],v[112:115],a[204:207]
s_waitcnt lgkmcnt(8)
v_rcp_f32 v21,v21
v_mfma_f32_16x16x32_bf16 a[208:211],a[144:147],v[64:67],0
buffer_load_dwordx4 v11,s[72:75],s83 offen lds
v_mfma_f32_16x16x32_bf16 a[208:211],a[148:151],v[68:71],a[208:211]
v_accvgpr_read_b32 v168,a32
v_accvgpr_read_b32 v169,a33
v_accvgpr_read_b32 v170,a34
v_accvgpr_read_b32 v171,a35
v_accvgpr_read_b32 v12,a36
v_accvgpr_read_b32 v13,a37
v_accvgpr_read_b32 v14,a38
v_accvgpr_read_b32 v15,a39
v_pk_mul_f32 v[168:169],v[20:21],v[168:169] op_sel:[1,0] op_sel_hi:[1,1]
v_pk_mul_f32 v[170:171],v[20:21],v[170:171] op_sel:[1,0] op_sel_hi:[1,1]
v_pk_mul_f32 v[12:13],v[20:21],v[12:13] op_sel:[1,0] op_sel_hi:[1,1]
v_pk_mul_f32 v[14:15],v[20:21],v[14:15] op_sel:[1,0] op_sel_hi:[1,1]
v_cvt_pk_bf16_f32 v168,v168,v169
v_cvt_pk_bf16_f32 v169,v170,v171
v_cvt_pk_bf16_f32 v170,v12,v13
v_cvt_pk_bf16_f32 v171,v14,v15
v_mfma_f32_16x16x32_bf16 a[208:211],a[152:155],v[72:75],a[208:211]
s_add_u32 s83,s80,20480
ds_read_b64_tr_b16 a[176:177],v2 offset:96
ds_read_b64_tr_b16 a[178:179],v2 offset:5216
v_mfma_f32_16x16x32_bf16 a[208:211],a[156:159],v[76:79],a[208:211]
ds_read_b64_tr_b16 a[180:181],v2 offset:10336
v_accvgpr_read_b32 v184,a48
v_mfma_f32_16x16x32_bf16 a[212:215],a[156:159],v[92:95],0
ds_read_b64_tr_b16 a[182:183],v2 offset:15456
v_accvgpr_read_b32 v185,a49
v_mfma_f32_16x16x32_bf16 a[212:215],a[152:155],v[88:91],a[212:215]
ds_read_b64_tr_b16 a[184:185],v2 offset:20576
v_mul_f32 v184,v21,v184
v_mfma_f32_16x16x32_bf16 a[212:215],a[148:151],v[84:87],a[212:215]
buffer_store_dwordx4 v[152:155],v6,s[36:39],s84 offen offset:2048 sc1
v_mfma_f32_16x16x32_bf16 a[212:215],a[144:147],v[80:83],a[212:215]
ds_read_b64_tr_b16 a[186:187],v2 offset:25696
ds_read_b64_tr_b16 a[188:189],v2 offset:30816
v_mfma_f32_16x16x32_bf16 a[216:219],a[144:147],v[96:99],0
ds_read_b64_tr_b16 a[190:191],v2 offset:35936
v_mul_f32 v185,v21,v185
v_mfma_f32_16x16x32_bf16 a[216:219],a[148:151],v[100:103],a[216:219]
v_accvgpr_read_b32 v172,a40
v_accvgpr_read_b32 v173,a41
v_accvgpr_read_b32 v174,a42
v_accvgpr_read_b32 v175,a43
v_accvgpr_read_b32 v12,a44
v_accvgpr_read_b32 v13,a45
v_accvgpr_read_b32 v14,a46
v_accvgpr_read_b32 v15,a47
v_pk_mul_f32 v[172:173],v[20:21],v[172:173] op_sel:[1,0] op_sel_hi:[1,1]
v_pk_mul_f32 v[174:175],v[20:21],v[174:175] op_sel:[1,0] op_sel_hi:[1,1]
v_pk_mul_f32 v[12:13],v[20:21],v[12:13] op_sel:[1,0] op_sel_hi:[1,1]
v_pk_mul_f32 v[14:15],v[20:21],v[14:15] op_sel:[1,0] op_sel_hi:[1,1]
v_cvt_pk_bf16_f32 v172,v172,v173
v_cvt_pk_bf16_f32 v173,v174,v175
v_cvt_pk_bf16_f32 v174,v12,v13
v_cvt_pk_bf16_f32 v175,v14,v15
v_mfma_f32_16x16x32_bf16 a[216:219],a[152:155],v[104:107],a[216:219]
v_accvgpr_read_b32 v186,a50
v_accvgpr_read_b32 v187,a51
v_mfma_f32_16x16x32_bf16 a[216:219],a[156:159],v[108:111],a[216:219]
v_cvt_pk_bf16_f32 v184,v184,v185
v_mul_f32 v186,v21,v186
v_mfma_f32_16x16x32_bf16 a[220:223],a[156:159],v[124:127],0
v_mul_f32 v187,v21,v187
v_cvt_pk_bf16_f32 v185,v186,v187
v_mfma_f32_16x16x32_bf16 a[220:223],a[152:155],v[120:123],a[220:223]
v_accvgpr_read_b32 v186,a52
v_accvgpr_read_b32 v187,a53
v_mfma_f32_16x16x32_bf16 a[220:223],a[148:151],v[116:119],a[220:223]
s_add_u32 m0,s82,122880
v_accvgpr_read_b32 v16,a54
v_accvgpr_read_b32 v17,a55
v_mfma_f32_16x16x32_bf16 a[220:223],a[144:147],v[112:115],a[220:223]
s_waitcnt lgkmcnt(1)
v_mul_f32 v186,v21,v186
v_mul_f32 v187,v21,v187
v_mfma_f32_16x16x32_bf16 a[224:227],a[160:163],v[64:67],0
v_mul_f32 v16,v21,v16
v_mul_f32 v17,v21,v17
v_mfma_f32_16x16x32_bf16 a[224:227],a[164:167],v[68:71],a[224:227]
buffer_load_dwordx4 v7,s[72:75],s80 offen lds
v_mfma_f32_16x16x32_bf16 a[224:227],a[168:171],v[72:75],a[224:227]
v_cvt_pk_bf16_f32 v186,v186,v187
v_cvt_pk_bf16_f32 v187,v16,v17
v_mfma_f32_16x16x32_bf16 a[224:227],a[172:175],v[76:79],a[224:227]
ds_read_b64_tr_b16 a[128:129],v1 offset:128
ds_read_b64_tr_b16 a[130:131],v1 offset:5248
v_mfma_f32_16x16x32_bf16 a[228:231],a[172:175],v[92:95],0
ds_read_b64_tr_b16 a[132:133],v1 offset:10368
v_accvgpr_read_b32 v188,a56
v_mfma_f32_16x16x32_bf16 a[228:231],a[168:171],v[88:91],a[228:231]
ds_read_b64_tr_b16 a[134:135],v1 offset:15488
v_accvgpr_read_b32 v189,a57
v_mfma_f32_16x16x32_bf16 a[228:231],a[164:167],v[84:87],a[228:231]
ds_read_b64_tr_b16 a[136:137],v1 offset:20608
v_mul_f32 v188,v21,v188
v_mfma_f32_16x16x32_bf16 a[228:231],a[160:163],v[80:83],a[228:231]
ds_read_b64_tr_b16 a[138:139],v1 offset:25728
ds_read_b64_tr_b16 a[140:141],v1 offset:30848
v_mfma_f32_16x16x32_bf16 a[232:235],a[160:163],v[96:99],0
ds_read_b64_tr_b16 a[142:143],v1 offset:35968
v_mul_f32 v189,v21,v189
v_mfma_f32_16x16x32_bf16 a[232:235],a[164:167],v[100:103],a[232:235]
v_accvgpr_read_b32 v190,a58
v_accvgpr_read_b32 v191,a59
v_mfma_f32_16x16x32_bf16 a[232:235],a[168:171],v[104:107],a[232:235]
buffer_store_dwordx4 v[156:159],v6,s[36:39],s84 offen offset:3072 sc1
v_mfma_f32_16x16x32_bf16 a[232:235],a[172:175],v[108:111],a[232:235]
v_cvt_pk_bf16_f32 v188,v188,v189
v_mul_f32 v190,v21,v190
v_mfma_f32_16x16x32_bf16 a[236:239],a[172:175],v[124:127],0
v_mul_f32 v191,v21,v191
v_cvt_pk_bf16_f32 v189,v190,v191
v_mfma_f32_16x16x32_bf16 a[236:239],a[168:171],v[120:123],a[236:239]
v_accvgpr_read_b32 v190,a60
v_accvgpr_read_b32 v191,a61
v_mfma_f32_16x16x32_bf16 a[236:239],a[164:167],v[116:119],a[236:239]
buffer_load_dwordx4 v8,s[72:75],s80 offen offset:1024 lds
v_mfma_f32_16x16x32_bf16 a[236:239],a[160:163],v[112:115],a[236:239]
v_accvgpr_read_b32 v12,a62
v_accvgpr_read_b32 v13,a63
v_mfma_f32_16x16x32_bf16 a[240:243],a[176:179],v[64:67],0
v_mul_f32 v190,v21,v190
v_mul_f32 v191,v21,v191
v_mfma_f32_16x16x32_bf16 a[240:243],a[180:183],v[68:71],a[240:243]
v_mul_f32 v12,v21,v12
v_mul_f32 v13,v21,v13
v_mfma_f32_16x16x32_bf16 a[240:243],a[184:187],v[72:75],a[240:243]
s_waitcnt lgkmcnt(0)
v_cvt_pk_bf16_f32 v190,v190,v191
v_cvt_pk_bf16_f32 v191,v12,v13
v_mfma_f32_16x16x32_bf16 a[240:243],a[188:191],v[76:79],a[240:243]
ds_read_b64_tr_b16 a[144:145],v2 offset:160
ds_read_b64_tr_b16 a[146:147],v2 offset:5280
v_mfma_f32_16x16x32_bf16 a[244:247],a[188:191],v[92:95],0
ds_read_b64_tr_b16 a[148:149],v2 offset:10400
v_add_f32 v56,v57,v56
v_mfma_f32_16x16x32_bf16 a[244:247],a[184:187],v[88:91],a[244:247]
ds_read_b64_tr_b16 a[150:151],v2 offset:15520
v_max_f32 v22,0x00800000,v56
v_mfma_f32_16x16x32_bf16 a[244:247],a[180:183],v[84:87],a[244:247]
ds_read_b64_tr_b16 a[152:153],v2 offset:20640
v_accvgpr_read_b32 v200,a64
v_mfma_f32_16x16x32_bf16 a[244:247],a[176:179],v[80:83],a[244:247]
ds_read_b64_tr_b16 a[154:155],v2 offset:25760
ds_read_b64_tr_b16 a[156:157],v2 offset:30880
v_mfma_f32_16x16x32_bf16 a[248:251],a[176:179],v[96:99],0
v_rcp_f32 v22,v22
v_mfma_f32_16x16x32_bf16 a[248:251],a[180:183],v[100:103],a[248:251]
buffer_load_dwordx4 v9,s[72:75],s80 offen offset:2048 lds
v_mfma_f32_16x16x32_bf16 a[248:251],a[184:187],v[104:107],a[248:251]
ds_read_b64_tr_b16 a[158:159],v2 offset:36000
v_accvgpr_read_b32 v201,a65
v_mfma_f32_16x16x32_bf16 a[248:251],a[188:191],v[108:111],a[248:251]
v_mul_f32 v200,v22,v200
v_mul_f32 v201,v22,v201
v_mfma_f32_16x16x32_bf16 a[252:255],a[188:191],v[124:127],0
v_accvgpr_read_b32 v202,a66
v_accvgpr_read_b32 v203,a67
v_mfma_f32_16x16x32_bf16 a[252:255],a[184:187],v[120:123],a[252:255]
v_cvt_pk_bf16_f32 v200,v200,v201
v_mul_f32 v202,v22,v202
v_mfma_f32_16x16x32_bf16 a[252:255],a[180:183],v[116:119],a[252:255]
s_waitcnt vmcnt(18)
v_mul_f32 v203,v22,v203
v_cvt_pk_bf16_f32 v201,v202,v203
v_mfma_f32_16x16x32_bf16 a[252:255],a[176:179],v[112:115],a[252:255]
s_barrier
buffer_store_dwordx4 v[168:171],v6,s[36:39],s85 offen sc1
v_mfma_f32_16x16x32_bf16 a[0:3],a[128:131],v[64:67],0
v_accvgpr_read_b32 v202,a68
v_accvgpr_read_b32 v203,a69
v_mfma_f32_16x16x32_bf16 a[0:3],a[132:135],v[68:71],a[0:3]
v_accvgpr_read_b32 v14,a70
v_accvgpr_read_b32 v15,a71
v_mfma_f32_16x16x32_bf16 a[0:3],a[136:139],v[72:75],a[0:3]
v_mul_f32 v202,v22,v202
v_mul_f32 v203,v22,v203
v_mfma_f32_16x16x32_bf16 a[0:3],a[140:143],v[76:79],a[0:3]
v_mul_f32 v14,v22,v14
v_mul_f32 v15,v22,v15
v_mfma_f32_16x16x32_bf16 a[4:7],a[140:143],v[92:95],0
buffer_load_dwordx4 v10,s[72:75],s80 offen offset:3072 lds
v_mfma_f32_16x16x32_bf16 a[4:7],a[136:139],v[88:91],a[4:7]
s_lshl_b32 s94,s70,4
v_cvt_pk_bf16_f32 v202,v202,v203
v_cvt_pk_bf16_f32 v203,v14,v15
v_mfma_f32_16x16x32_bf16 a[4:7],a[132:135],v[84:87],a[4:7]
s_add_u32 s94,s94,s40
v_accvgpr_read_b32 v204,a72
v_accvgpr_read_b32 v205,a73
v_mfma_f32_16x16x32_bf16 a[4:7],a[128:131],v[80:83],a[4:7]
s_lshl_b32 s94,s94,4
v_mul_f32 v204,v22,v204
v_mul_f32 v205,v22,v205
v_mfma_f32_16x16x32_bf16 a[8:11],a[128:131],v[96:99],0
v_accvgpr_read_b32 v206,a74
v_accvgpr_read_b32 v207,a75
v_mfma_f32_16x16x32_bf16 a[8:11],a[132:135],v[100:103],a[8:11]
ds_read_b64_tr_b16 a[160:161],v1 offset:192
ds_read_b64_tr_b16 a[162:163],v1 offset:5312
v_mfma_f32_16x16x32_bf16 a[8:11],a[136:139],v[104:107],a[8:11]
v_cvt_pk_bf16_f32 v204,v204,v205
v_mul_f32 v206,v22,v206
v_mfma_f32_16x16x32_bf16 a[8:11],a[140:143],v[108:111],a[8:11]
v_mul_f32 v207,v22,v207
v_cvt_pk_bf16_f32 v205,v206,v207
v_mfma_f32_16x16x32_bf16 a[12:15],a[140:143],v[124:127],0
ds_read_b64_tr_b16 a[164:165],v1 offset:10432
ds_read_b64_tr_b16 a[166:167],v1 offset:15552
v_mfma_f32_16x16x32_bf16 a[12:15],a[136:139],v[120:123],a[12:15]
s_add_u32 m0,s82,126976
v_accvgpr_read_b32 v206,a76
v_accvgpr_read_b32 v207,a77
v_mfma_f32_16x16x32_bf16 a[12:15],a[132:135],v[116:119],a[12:15]
v_accvgpr_read_b32 v16,a78
v_accvgpr_read_b32 v17,a79
v_mfma_f32_16x16x32_bf16 a[12:15],a[128:131],v[112:115],a[12:15]
s_waitcnt lgkmcnt(4)
v_mul_f32 v206,v22,v206
v_mul_f32 v207,v22,v207
v_mfma_f32_16x16x32_bf16 a[16:19],a[144:147],v[64:67],0
buffer_load_dwordx4 v11,s[72:75],s80 offen lds
v_mfma_f32_16x16x32_bf16 a[16:19],a[148:151],v[68:71],a[16:19]
ds_read_b64_tr_b16 a[168:169],v1 offset:20672
ds_read_b64_tr_b16 a[170:171],v1 offset:25792
v_mfma_f32_16x16x32_bf16 a[16:19],a[152:155],v[72:75],a[16:19]
)A"
R"A(v_mul_f32 v16,v22,v16
v_mul_f32 v17,v22,v17
v_mfma_f32_16x16x32_bf16 a[16:19],a[156:159],v[76:79],a[16:19]
v_cvt_pk_bf16_f32 v206,v206,v207
v_cvt_pk_bf16_f32 v207,v16,v17
v_mfma_f32_16x16x32_bf16 a[20:23],a[156:159],v[92:95],0
buffer_store_dwordx4 v[172:175],v6,s[36:39],s85 offen offset:1024 sc1
v_mfma_f32_16x16x32_bf16 a[20:23],a[152:155],v[88:91],a[20:23]
ds_read_b64_tr_b16 a[172:173],v1 offset:30912
ds_read_b64_tr_b16 a[174:175],v1 offset:36032
v_mfma_f32_16x16x32_bf16 a[20:23],a[148:151],v[84:87],a[20:23]
v_accvgpr_read_b32 v216,a80
v_accvgpr_read_b32 v217,a81
v_mfma_f32_16x16x32_bf16 a[20:23],a[144:147],v[80:83],a[20:23]
v_mul_f32 v216,v22,v216
v_mul_f32 v217,v22,v217
v_mfma_f32_16x16x32_bf16 a[24:27],a[144:147],v[96:99],0
v_accvgpr_read_b32 v218,a82
v_accvgpr_read_b32 v219,a83
v_mfma_f32_16x16x32_bf16 a[24:27],a[148:151],v[100:103],a[24:27]
s_add_u32 m0,s82,128000
v_cvt_pk_bf16_f32 v216,v216,v217
v_mul_f32 v218,v22,v218
v_mfma_f32_16x16x32_bf16 a[24:27],a[152:155],v[104:107],a[24:27]
v_mul_f32 v219,v22,v219
v_cvt_pk_bf16_f32 v217,v218,v219
v_mfma_f32_16x16x32_bf16 a[24:27],a[156:159],v[108:111],a[24:27]
v_accvgpr_read_b32 v218,a84
v_accvgpr_read_b32 v219,a85
v_mfma_f32_16x16x32_bf16 a[28:31],a[156:159],v[124:127],0
buffer_load_dwordx4 v7,s[72:75],s83 offen lds
v_mfma_f32_16x16x32_bf16 a[28:31],a[152:155],v[120:123],a[28:31]
v_accvgpr_read_b32 v12,a86
v_accvgpr_read_b32 v13,a87
v_mfma_f32_16x16x32_bf16 a[28:31],a[148:151],v[116:119],a[28:31]
v_mul_f32 v218,v22,v218
v_mul_f32 v219,v22,v219
v_mfma_f32_16x16x32_bf16 a[28:31],a[144:147],v[112:115],a[28:31]
s_waitcnt lgkmcnt(0)
v_mul_f32 v12,v22,v12
v_mul_f32 v13,v22,v13
v_mfma_f32_16x16x32_bf16 a[32:35],a[160:163],v[64:67],0
v_cvt_pk_bf16_f32 v218,v218,v219
v_cvt_pk_bf16_f32 v219,v12,v13
v_mfma_f32_16x16x32_bf16 a[32:35],a[164:167],v[68:71],a[32:35]
ds_read_b64_tr_b16 a[176:177],v2 offset:224
ds_read_b64_tr_b16 a[178:179],v2 offset:5344
v_mfma_f32_16x16x32_bf16 a[32:35],a[168:171],v[72:75],a[32:35]
ds_read_b64_tr_b16 a[180:181],v2 offset:10464
v_bfe_u32 v24,v6,4,2
v_mfma_f32_16x16x32_bf16 a[32:35],a[172:175],v[76:79],a[32:35]
ds_read_b64_tr_b16 a[182:183],v2 offset:15584
v_min_u32 v24,2,v24
v_mfma_f32_16x16x32_bf16 a[36:39],a[172:175],v[92:95],0
ds_read_b64_tr_b16 a[184:185],v2 offset:20704
v_add_u32 v24,s71,v24
v_mfma_f32_16x16x32_bf16 a[36:39],a[168:171],v[88:91],a[36:39]
ds_read_b64_tr_b16 a[186:187],v2 offset:25824
ds_read_b64_tr_b16 a[188:189],v2 offset:30944
v_mfma_f32_16x16x32_bf16 a[36:39],a[164:167],v[84:87],a[36:39]
buffer_store_dwordx4 v[184:187],v6,s[36:39],s85 offen offset:2048 sc1
v_mfma_f32_16x16x32_bf16 a[36:39],a[160:163],v[80:83],a[36:39]
ds_read_b64_tr_b16 a[190:191],v2 offset:36064
v_add_u32 v24,1,v24
v_mfma_f32_16x16x32_bf16 a[40:43],a[160:163],v[96:99],0
v_and_b32 v24,3,v24
v_lshlrev_b32 v24,6,v24
v_mfma_f32_16x16x32_bf16 a[40:43],a[164:167],v[100:103],a[40:43]
v_accvgpr_read_b32 v220,a88
v_accvgpr_read_b32 v221,a89
v_mfma_f32_16x16x32_bf16 a[40:43],a[168:171],v[104:107],a[40:43]
buffer_load_dwordx4 v8,s[72:75],s83 offen offset:1024 lds
v_mfma_f32_16x16x32_bf16 a[40:43],a[172:175],v[108:111],a[40:43]
v_mul_f32 v220,v22,v220
v_mul_f32 v221,v22,v221
v_mfma_f32_16x16x32_bf16 a[44:47],a[172:175],v[124:127],0
v_accvgpr_read_b32 v222,a90
v_accvgpr_read_b32 v223,a91
v_mfma_f32_16x16x32_bf16 a[44:47],a[168:171],v[120:123],a[44:47]
v_cvt_pk_bf16_f32 v220,v220,v221
v_mul_f32 v222,v22,v222
v_mfma_f32_16x16x32_bf16 a[44:47],a[164:167],v[116:119],a[44:47]
buffer_load_dwordx2 v[26:27],v24,s[64:67],s94 offen sc1
v_mfma_f32_16x16x32_bf16 a[44:47],a[160:163],v[112:115],a[44:47]
s_waitcnt lgkmcnt(0)
v_mul_f32 v223,v22,v223
v_cvt_pk_bf16_f32 v221,v222,v223
v_mfma_f32_16x16x32_bf16 a[48:51],a[176:179],v[64:67],0
v_accvgpr_read_b32 v222,a92
v_accvgpr_read_b32 v223,a93
v_mfma_f32_16x16x32_bf16 a[48:51],a[180:183],v[68:71],a[48:51]
v_accvgpr_read_b32 v14,a94
v_accvgpr_read_b32 v15,a95
v_mfma_f32_16x16x32_bf16 a[48:51],a[184:187],v[72:75],a[48:51]
v_mul_f32 v222,v22,v222
v_mul_f32 v223,v22,v223
v_mfma_f32_16x16x32_bf16 a[48:51],a[188:191],v[76:79],a[48:51]
v_mul_f32 v14,v22,v14
v_mul_f32 v15,v22,v15
v_mfma_f32_16x16x32_bf16 a[52:55],a[188:191],v[92:95],0
v_cvt_pk_bf16_f32 v222,v222,v223
v_cvt_pk_bf16_f32 v223,v14,v15
v_mfma_f32_16x16x32_bf16 a[52:55],a[184:187],v[88:91],a[52:55]
buffer_load_dwordx4 v9,s[72:75],s83 offen offset:2048 lds
v_mfma_f32_16x16x32_bf16 a[52:55],a[180:183],v[84:87],a[52:55]
ds_read_b64_tr_b16 a[128:129],v1 offset:256
ds_read_b64_tr_b16 a[130:131],v1 offset:5376
v_mfma_f32_16x16x32_bf16 a[52:55],a[176:179],v[80:83],a[52:55]
ds_read_b64_tr_b16 a[132:133],v1 offset:10496
v_add_f32 v60,v61,v60
v_mfma_f32_16x16x32_bf16 a[56:59],a[176:179],v[96:99],0
ds_read_b64_tr_b16 a[134:135],v1 offset:15616
v_max_f32 v23,0x00800000,v60
v_mfma_f32_16x16x32_bf16 a[56:59],a[180:183],v[100:103],a[56:59]
ds_read_b64_tr_b16 a[136:137],v1 offset:20736
ds_read_b64_tr_b16 a[138:139],v1 offset:25856
v_mfma_f32_16x16x32_bf16 a[56:59],a[184:187],v[104:107],a[56:59]
ds_read_b64_tr_b16 a[140:141],v1 offset:30976
ds_read_b64_tr_b16 a[142:143],v1 offset:36096
v_mfma_f32_16x16x32_bf16 a[56:59],a[188:191],v[108:111],a[56:59]
buffer_store_dwordx4 v[188:191],v6,s[36:39],s85 offen offset:3072 sc1
v_mfma_f32_16x16x32_bf16 a[60:63],a[188:191],v[124:127],0
v_rcp_f32 v23,v23
v_mfma_f32_16x16x32_bf16 a[60:63],a[184:187],v[120:123],a[60:63]
v_accvgpr_read_b32 v232,a96
v_accvgpr_read_b32 v233,a97
v_mfma_f32_16x16x32_bf16 a[60:63],a[180:183],v[116:119],a[60:63]
v_mul_f32 v232,v23,v232
v_mul_f32 v233,v23,v233
v_mfma_f32_16x16x32_bf16 a[60:63],a[176:179],v[112:115],a[60:63]
s_waitcnt lgkmcnt(5)
v_accvgpr_read_b32 v234,a98
v_accvgpr_read_b32 v235,a99
v_mfma_f32_16x16x32_bf16 a[64:67],a[128:131],v[64:67],0
s_waitcnt lgkmcnt(4)
v_cvt_pk_bf16_f32 v232,v232,v233
v_mul_f32 v234,v23,v234
v_mfma_f32_16x16x32_bf16 a[64:67],a[132:135],v[68:71],a[64:67]
s_waitcnt lgkmcnt(2)
v_mul_f32 v235,v23,v235
v_cvt_pk_bf16_f32 v233,v234,v235
v_mfma_f32_16x16x32_bf16 a[64:67],a[136:139],v[72:75],a[64:67]
s_waitcnt lgkmcnt(0)
v_accvgpr_read_b32 v234,a100
v_accvgpr_read_b32 v235,a101
v_mfma_f32_16x16x32_bf16 a[64:67],a[140:143],v[76:79],a[64:67]
buffer_load_dwordx4 v10,s[72:75],s83 offen offset:3072 lds
v_mfma_f32_16x16x32_bf16 a[68:71],a[140:143],v[92:95],0
v_accvgpr_read_b32 v16,a102
v_accvgpr_read_b32 v17,a103
v_mfma_f32_16x16x32_bf16 a[68:71],a[136:139],v[88:91],a[68:71]
v_mul_f32 v234,v23,v234
v_mul_f32 v235,v23,v235
v_mfma_f32_16x16x32_bf16 a[68:71],a[132:135],v[84:87],a[68:71]
v_mul_f32 v16,v23,v16
v_mul_f32 v17,v23,v17
v_mfma_f32_16x16x32_bf16 a[68:71],a[128:131],v[80:83],a[68:71]
ds_read_b64_tr_b16 a[144:145],v2 offset:288
ds_read_b64_tr_b16 a[146:147],v2 offset:5408
v_mfma_f32_16x16x32_bf16 a[72:75],a[128:131],v[96:99],0
ds_read_b64_tr_b16 a[148:149],v2 offset:10528
ds_read_b64_tr_b16 a[150:151],v2 offset:15648
v_mfma_f32_16x16x32_bf16 a[72:75],a[132:135],v[100:103],a[72:75]
ds_read_b64_tr_b16 a[152:153],v2 offset:20768
ds_read_b64_tr_b16 a[154:155],v2 offset:25888
v_mfma_f32_16x16x32_bf16 a[72:75],a[136:139],v[104:107],a[72:75]
ds_read_b64_tr_b16 a[156:157],v2 offset:31008
ds_read_b64_tr_b16 a[158:159],v2 offset:36128
v_mfma_f32_16x16x32_bf16 a[72:75],a[140:143],v[108:111],a[72:75]
s_add_u32 m0,s82,132096
v_mfma_f32_16x16x32_bf16 a[76:79],a[140:143],v[124:127],0
v_cvt_pk_bf16_f32 v234,v234,v235
v_cvt_pk_bf16_f32 v235,v16,v17
v_mfma_f32_16x16x32_bf16 a[76:79],a[136:139],v[120:123],a[76:79]
v_accvgpr_read_b32 v236,a104
v_accvgpr_read_b32 v237,a105
v_mfma_f32_16x16x32_bf16 a[76:79],a[132:135],v[116:119],a[76:79]
v_mul_f32 v236,v23,v236
v_mul_f32 v237,v23,v237
v_mfma_f32_16x16x32_bf16 a[76:79],a[128:131],v[112:115],a[76:79]
s_waitcnt lgkmcnt(6)
buffer_load_dwordx4 v11,s[72:75],s83 offen lds
v_mfma_f32_16x16x32_bf16 a[80:83],a[144:147],v[64:67],0
s_waitcnt lgkmcnt(4)
v_accvgpr_read_b32 v238,a106
v_accvgpr_read_b32 v239,a107
v_mfma_f32_16x16x32_bf16 a[80:83],a[148:151],v[68:71],a[80:83]
s_waitcnt lgkmcnt(2)
v_cvt_pk_bf16_f32 v236,v236,v237
v_mul_f32 v238,v23,v238
v_mfma_f32_16x16x32_bf16 a[80:83],a[152:155],v[72:75],a[80:83]
s_waitcnt lgkmcnt(0)
v_mul_f32 v239,v23,v239
v_cvt_pk_bf16_f32 v237,v238,v239
v_mfma_f32_16x16x32_bf16 a[80:83],a[156:159],v[76:79],a[80:83]
v_accvgpr_read_b32 v238,a108
v_accvgpr_read_b32 v239,a109
v_mfma_f32_16x16x32_bf16 a[84:87],a[156:159],v[92:95],0
v_accvgpr_read_b32 v12,a110
v_accvgpr_read_b32 v13,a111
v_mfma_f32_16x16x32_bf16 a[84:87],a[152:155],v[88:91],a[84:87]
v_mul_f32 v238,v23,v238
v_mul_f32 v239,v23,v239
v_mfma_f32_16x16x32_bf16 a[84:87],a[148:151],v[84:87],a[84:87]
v_subrev_u32 v1,81920,v1
v_mul_f32 v12,v23,v12
v_mfma_f32_16x16x32_bf16 a[84:87],a[144:147],v[80:83],a[84:87]
ds_read_b64_tr_b16 a[160:161],v1
ds_read_b64_tr_b16 a[162:163],v1 offset:5120
v_mfma_f32_16x16x32_bf16 a[88:91],a[144:147],v[96:99],0
ds_read_b64_tr_b16 a[164:165],v1 offset:10240
ds_read_b64_tr_b16 a[166:167],v1 offset:15360
v_mfma_f32_16x16x32_bf16 a[88:91],a[148:151],v[100:103],a[88:91]
ds_read_b64_tr_b16 a[168:169],v1 offset:20480
ds_read_b64_tr_b16 a[170:171],v1 offset:25600
v_mfma_f32_16x16x32_bf16 a[88:91],a[152:155],v[104:107],a[88:91]
ds_read_b64_tr_b16 a[172:173],v1 offset:30720
ds_read_b64_tr_b16 a[174:175],v1 offset:35840
v_mfma_f32_16x16x32_bf16 a[88:91],a[156:159],v[108:111],a[88:91]
v_accvgpr_read_b32 v248,a112
v_accvgpr_read_b32 v249,a113
v_mul_f32 v248,v23,v248
v_mul_f32 v249,v23,v249
v_accvgpr_read_b32 v250,a114
v_accvgpr_read_b32 v251,a115
v_accvgpr_read_b32 v252,a120
v_accvgpr_read_b32 v253,a121
v_cmp_nlt_f32 s[56:57],v48,s63
v_mul_f32 v252,v23,v252
v_mul_f32 v253,v23,v253
v_accvgpr_read_b32 v254,a122
v_accvgpr_read_b32 v255,a123
v_mfma_f32_16x16x32_bf16 a[92:95],a[156:159],v[124:127],0
v_cvt_pk_bf16_f32 v248,v248,v249
v_mul_f32 v250,v23,v250
v_mul_f32 v251,v23,v251
v_cvt_pk_bf16_f32 v249,v250,v251
v_cvt_pk_bf16_f32 v252,v252,v253
v_mul_f32 v254,v23,v254
v_mul_f32 v255,v23,v255
v_cmp_nlt_f32 s[58:59],v52,s63
s_nop 1
s_or_b64 s[56:57],s[56:57],s[58:59]
v_cvt_pk_bf16_f32 v253,v254,v255
v_mfma_f32_16x16x32_bf16 a[92:95],a[152:155],v[120:123],a[92:95]
v_accvgpr_read_b32 v250,a116
v_accvgpr_read_b32 v251,a117
v_accvgpr_read_b32 v14,a118
v_accvgpr_read_b32 v15,a119
v_mul_f32 v250,v23,v250
v_mul_f32 v251,v23,v251
v_accvgpr_read_b32 v254,a124
v_accvgpr_read_b32 v255,a125
v_cmp_nlt_f32 s[58:59],v56,s63
s_nop 1
s_or_b64 s[56:57],s[56:57],s[58:59]
v_accvgpr_read_b32 v16,a126
v_accvgpr_read_b32 v17,a127
v_mul_f32 v254,v23,v254
v_mul_f32 v255,v23,v255
s_waitcnt vmcnt(4)
v_cmp_ne_u32 s[90:91],s68,v26
v_cmp_ne_u32 s[92:93],s69,v27
s_nop 0
s_or_b64 s[90:91],s[90:91],s[92:93]
s_cbranch_scc1 L_fr8_%=
L_fo8_%=:
v_mfma_f32_16x16x32_bf16 a[92:95],a[148:151],v[116:119],a[92:95]
v_mul_f32 v13,v23,v13
v_cvt_pk_bf16_f32 v238,v238,v239
v_cvt_pk_bf16_f32 v239,v12,v13
v_mul_f32 v14,v23,v14
v_mul_f32 v15,v23,v15
v_cvt_pk_bf16_f32 v250,v250,v251
v_cvt_pk_bf16_f32 v251,v14,v15
v_mul_f32 v16,v23,v16
v_mul_f32 v17,v23,v17
v_cvt_pk_bf16_f32 v254,v254,v255
v_cmp_nlt_f32 s[58:59],v60,s63
s_nop 1
s_or_b64 s[56:57],s[56:57],s[58:59]
v_cvt_pk_bf16_f32 v255,v16,v17
v_mfma_f32_16x16x32_bf16 a[92:95],a[144:147],v[112:115],a[92:95]
s_cmp_lg_u64 s[56:57],0
s_cbranch_scc1 L_slow1_%=
L_sret1_%=:
v_subrev_u32 v2,81920,v2
s_waitcnt lgkmcnt(6)
v_mfma_f32_16x16x32_bf16 a[192:195],a[160:163],v[136:139],a[192:195]
s_cmp_ge_u32 0,s71
s_cselect_b32 s76,2,0
s_lshl_b32 s76,s76,16
s_waitcnt lgkmcnt(4)
v_mfma_f32_16x16x32_bf16 a[192:195],a[164:167],v[140:143],a[192:195]
s_add_u32 s77,s76,4096
s_add_u32 s78,s76,8192
s_add_u32 s79,s76,12288
s_max_i32 s76,s76,s18
s_waitcnt lgkmcnt(2)
v_mfma_f32_16x16x32_bf16 a[192:195],a[168:171],v[152:155],a[192:195]
s_max_i32 s77,s77,s19
buffer_load_dwordx4 v[128:131],v6,s[28:31],s76 offen sc1
s_waitcnt lgkmcnt(0)
v_mfma_f32_16x16x32_bf16 a[192:195],a[172:175],v[156:159],a[192:195]
s_max_i32 s78,s78,s100
ds_read_b64_tr_b16 a[176:177],v2 offset:32
ds_read_b64_tr_b16 a[178:179],v2 offset:5152
v_mfma_f32_16x16x32_bf16 a[196:199],a[172:175],v[188:191],a[196:199]
s_cmp_ge_u32 2,s71
)A"
R"A(s_cselect_b32 s80,6,4
ds_read_b64_tr_b16 a[180:181],v2 offset:10272
v_mfma_f32_16x16x32_bf16 a[196:199],a[168:171],v[184:187],a[196:199]
s_mul_i32 s80,s80,163840
s_add_u32 s80,s80,s81
ds_read_b64_tr_b16 a[182:183],v2 offset:15392
v_mfma_f32_16x16x32_bf16 a[196:199],a[164:167],v[172:175],a[196:199]
s_max_i32 s79,s79,s101
buffer_store_dwordx4 v[200:203],v6,s[36:39],s86 offen sc1
v_mfma_f32_16x16x32_bf16 a[196:199],a[160:163],v[168:171],a[196:199]
s_add_u32 s83,s80,20480
ds_read_b64_tr_b16 a[184:185],v2 offset:20512
ds_read_b64_tr_b16 a[186:187],v2 offset:25632
v_mfma_f32_16x16x32_bf16 a[200:203],a[160:163],v[200:203],a[200:203]
ds_read_b64_tr_b16 a[188:189],v2 offset:30752
v_mfma_f32_16x16x32_bf16 a[200:203],a[164:167],v[204:207],a[200:203]
ds_read_b64_tr_b16 a[190:191],v2 offset:35872
v_mfma_f32_16x16x32_bf16 a[200:203],a[168:171],v[216:219],a[200:203]
buffer_load_dwordx4 v[160:163],v6,s[28:31],s76 offen offset:1024 sc1
v_mfma_f32_16x16x32_bf16 a[200:203],a[172:175],v[220:223],a[200:203]
ds_read_b64_tr_b16 a[128:129],v1 offset:64
ds_read_b64_tr_b16 a[130:131],v1 offset:5184
v_mfma_f32_16x16x32_bf16 a[204:207],a[172:175],v[252:255],a[204:207]
ds_read_b64_tr_b16 a[132:133],v1 offset:10304
v_mfma_f32_16x16x32_bf16 a[204:207],a[168:171],v[248:251],a[204:207]
ds_read_b64_tr_b16 a[134:135],v1 offset:15424
v_mfma_f32_16x16x32_bf16 a[204:207],a[164:167],v[236:239],a[204:207]
buffer_store_dwordx4 v[204:207],v6,s[36:39],s86 offen offset:1024 sc1
v_mfma_f32_16x16x32_bf16 a[204:207],a[160:163],v[232:235],a[204:207]
s_barrier
ds_read_b64_tr_b16 a[136:137],v1 offset:20544
ds_read_b64_tr_b16 a[138:139],v1 offset:25664
s_waitcnt lgkmcnt(8)
v_mfma_f32_16x16x32_bf16 a[208:211],a[176:179],v[136:139],a[208:211]
ds_read_b64_tr_b16 a[140:141],v1 offset:30784
v_mfma_f32_16x16x32_bf16 a[208:211],a[180:183],v[140:143],a[208:211]
buffer_load_dwordx4 v[192:195],v6,s[28:31],s76 offen offset:2048 sc1
v_mfma_f32_16x16x32_bf16 a[208:211],a[184:187],v[152:155],a[208:211]
s_add_u32 m0,s82,81920
ds_read_b64_tr_b16 a[142:143],v1 offset:35904
ds_read_b64_tr_b16 a[144:145],v2 offset:96
s_waitcnt lgkmcnt(9)
v_mfma_f32_16x16x32_bf16 a[208:211],a[188:191],v[156:159],a[208:211]
ds_read_b64_tr_b16 a[146:147],v2 offset:5216
v_mfma_f32_16x16x32_bf16 a[212:215],a[188:191],v[188:191],a[212:215]
ds_read_b64_tr_b16 a[148:149],v2 offset:10336
v_mfma_f32_16x16x32_bf16 a[212:215],a[184:187],v[184:187],a[212:215]
buffer_store_dwordx4 v[216:219],v6,s[36:39],s86 offen offset:2048 sc1
v_mfma_f32_16x16x32_bf16 a[212:215],a[180:183],v[172:175],a[212:215]
ds_read_b64_tr_b16 a[150:151],v2 offset:15456
ds_read_b64_tr_b16 a[152:153],v2 offset:20576
v_mfma_f32_16x16x32_bf16 a[212:215],a[176:179],v[168:171],a[212:215]
ds_read_b64_tr_b16 a[154:155],v2 offset:25696
v_mfma_f32_16x16x32_bf16 a[216:219],a[176:179],v[200:203],a[216:219]
ds_read_b64_tr_b16 a[156:157],v2 offset:30816
v_mfma_f32_16x16x32_bf16 a[216:219],a[180:183],v[204:207],a[216:219]
buffer_load_dwordx4 v[224:227],v6,s[28:31],s76 offen offset:3072 sc1
v_mfma_f32_16x16x32_bf16 a[216:219],a[184:187],v[216:219],a[216:219]
ds_read_b64_tr_b16 a[158:159],v2 offset:35936
ds_read_b64_tr_b16 a[160:161],v1 offset:128
v_mfma_f32_16x16x32_bf16 a[216:219],a[188:191],v[220:223],a[216:219]
ds_read_b64_tr_b16 a[162:163],v1 offset:5248
v_mfma_f32_16x16x32_bf16 a[220:223],a[188:191],v[252:255],a[220:223]
ds_read_b64_tr_b16 a[164:165],v1 offset:10368
v_mfma_f32_16x16x32_bf16 a[220:223],a[184:187],v[248:251],a[220:223]
buffer_load_dwordx4 v7,s[72:75],s80 offen lds
v_mfma_f32_16x16x32_bf16 a[220:223],a[180:183],v[236:239],a[220:223]
ds_read_b64_tr_b16 a[166:167],v1 offset:15488
ds_read_b64_tr_b16 a[168:169],v1 offset:20608
v_mfma_f32_16x16x32_bf16 a[220:223],a[176:179],v[232:235],a[220:223]
s_waitcnt lgkmcnt(7)
ds_read_b64_tr_b16 a[170:171],v1 offset:25728
v_mfma_f32_16x16x32_bf16 a[224:227],a[128:131],v[136:139],a[224:227]
ds_read_b64_tr_b16 a[172:173],v1 offset:30848
v_mfma_f32_16x16x32_bf16 a[224:227],a[132:135],v[140:143],a[224:227]
buffer_load_dwordx4 v[132:135],v6,s[28:31],s77 offen sc1
v_mfma_f32_16x16x32_bf16 a[224:227],a[136:139],v[152:155],a[224:227]
ds_read_b64_tr_b16 a[174:175],v1 offset:35968
ds_read_b64_tr_b16 a[176:177],v2 offset:160
v_mfma_f32_16x16x32_bf16 a[224:227],a[140:143],v[156:159],a[224:227]
ds_read_b64_tr_b16 a[178:179],v2 offset:5280
v_mfma_f32_16x16x32_bf16 a[228:231],a[140:143],v[188:191],a[228:231]
ds_read_b64_tr_b16 a[180:181],v2 offset:10400
v_mfma_f32_16x16x32_bf16 a[228:231],a[136:139],v[184:187],a[228:231]
buffer_load_dwordx4 v[164:167],v6,s[28:31],s77 offen offset:1024 sc1
v_mfma_f32_16x16x32_bf16 a[228:231],a[132:135],v[172:175],a[228:231]
ds_read_b64_tr_b16 a[182:183],v2 offset:15520
ds_read_b64_tr_b16 a[184:185],v2 offset:20640
v_mfma_f32_16x16x32_bf16 a[228:231],a[128:131],v[168:171],a[228:231]
ds_read_b64_tr_b16 a[186:187],v2 offset:25760
v_mfma_f32_16x16x32_bf16 a[232:235],a[128:131],v[200:203],a[232:235]
ds_read_b64_tr_b16 a[188:189],v2 offset:30880
v_mfma_f32_16x16x32_bf16 a[232:235],a[132:135],v[204:207],a[232:235]
buffer_load_dwordx4 v8,s[72:75],s80 offen offset:1024 lds
v_mfma_f32_16x16x32_bf16 a[232:235],a[136:139],v[216:219],a[232:235]
ds_read_b64_tr_b16 a[190:191],v2 offset:36000
v_mfma_f32_16x16x32_bf16 a[232:235],a[140:143],v[220:223],a[232:235]
v_mfma_f32_16x16x32_bf16 a[236:239],a[140:143],v[252:255],a[236:239]
ds_read_b64_tr_b16 a[140:141],v1 offset:30912
ds_read_b64_tr_b16 a[142:143],v1 offset:36032
v_mfma_f32_16x16x32_bf16 a[236:239],a[136:139],v[248:251],a[236:239]
buffer_load_dwordx4 v[196:199],v6,s[28:31],s77 offen offset:2048 sc1
v_mfma_f32_16x16x32_bf16 a[236:239],a[132:135],v[236:239],a[236:239]
ds_read_b64_tr_b16 a[132:133],v1 offset:10432
ds_read_b64_tr_b16 a[134:135],v1 offset:15552
v_mfma_f32_16x16x32_bf16 a[236:239],a[128:131],v[232:235],a[236:239]
ds_read_b64_tr_b16 a[128:129],v1 offset:192
v_mfma_f32_16x16x32_bf16 a[240:243],a[144:147],v[136:139],a[240:243]
ds_read_b64_tr_b16 a[130:131],v1 offset:5312
v_mfma_f32_16x16x32_bf16 a[240:243],a[148:151],v[140:143],a[240:243]
buffer_store_dwordx4 v[220:223],v6,s[36:39],s86 offen offset:3072 sc1
v_mfma_f32_16x16x32_bf16 a[240:243],a[152:155],v[152:155],a[240:243]
s_waitcnt lgkmcnt(6)
ds_read_b64_tr_b16 a[136:137],v1 offset:20672
ds_read_b64_tr_b16 a[138:139],v1 offset:25792
v_mfma_f32_16x16x32_bf16 a[240:243],a[156:159],v[156:159],a[240:243]
v_mfma_f32_16x16x32_bf16 a[244:247],a[156:159],v[188:191],a[244:247]
v_mfma_f32_16x16x32_bf16 a[244:247],a[152:155],v[184:187],a[244:247]
buffer_load_dwordx4 v[228:231],v6,s[28:31],s77 offen offset:3072 sc1
v_mfma_f32_16x16x32_bf16 a[244:247],a[148:151],v[172:175],a[244:247]
v_mfma_f32_16x16x32_bf16 a[244:247],a[144:147],v[168:171],a[244:247]
v_mfma_f32_16x16x32_bf16 a[248:251],a[144:147],v[200:203],a[248:251]
v_mfma_f32_16x16x32_bf16 a[248:251],a[148:151],v[204:207],a[248:251]
buffer_load_dwordx4 v9,s[72:75],s80 offen offset:2048 lds
v_mfma_f32_16x16x32_bf16 a[248:251],a[152:155],v[216:219],a[248:251]
v_mfma_f32_16x16x32_bf16 a[248:251],a[156:159],v[220:223],a[248:251]
v_mfma_f32_16x16x32_bf16 a[252:255],a[156:159],v[252:255],a[252:255]
ds_read_b64_tr_b16 a[156:157],v2 offset:30944
ds_read_b64_tr_b16 a[158:159],v2 offset:36064
v_mfma_f32_16x16x32_bf16 a[252:255],a[152:155],v[248:251],a[252:255]
buffer_load_dwordx4 v[144:147],v6,s[28:31],s78 offen sc1
v_mfma_f32_16x16x32_bf16 a[252:255],a[148:151],v[236:239],a[252:255]
ds_read_b64_tr_b16 a[148:149],v2 offset:10464
ds_read_b64_tr_b16 a[150:151],v2 offset:15584
v_mfma_f32_16x16x32_bf16 a[252:255],a[144:147],v[232:235],a[252:255]
ds_read_b64_tr_b16 a[144:145],v2 offset:224
v_mfma_f32_16x16x32_bf16 a[0:3],a[160:163],v[136:139],a[0:3]
ds_read_b64_tr_b16 a[146:147],v2 offset:5344
v_mfma_f32_16x16x32_bf16 a[0:3],a[164:167],v[140:143],a[0:3]
buffer_load_dwordx4 v[176:179],v6,s[28:31],s78 offen offset:1024 sc1
v_mfma_f32_16x16x32_bf16 a[0:3],a[168:171],v[152:155],a[0:3]
ds_read_b64_tr_b16 a[152:153],v2 offset:20704
ds_read_b64_tr_b16 a[154:155],v2 offset:25824
v_mfma_f32_16x16x32_bf16 a[0:3],a[172:175],v[156:159],a[0:3]
v_mfma_f32_16x16x32_bf16 a[4:7],a[172:175],v[188:191],a[4:7]
v_mfma_f32_16x16x32_bf16 a[4:7],a[168:171],v[184:187],a[4:7]
buffer_load_dwordx4 v10,s[72:75],s80 offen offset:3072 lds
v_mfma_f32_16x16x32_bf16 a[4:7],a[164:167],v[172:175],a[4:7]
v_mfma_f32_16x16x32_bf16 a[4:7],a[160:163],v[168:171],a[4:7]
v_mfma_f32_16x16x32_bf16 a[8:11],a[160:163],v[200:203],a[8:11]
v_mfma_f32_16x16x32_bf16 a[8:11],a[164:167],v[204:207],a[8:11]
buffer_load_dwordx4 v[208:211],v6,s[28:31],s78 offen offset:2048 sc1
v_mfma_f32_16x16x32_bf16 a[8:11],a[168:171],v[216:219],a[8:11]
v_mfma_f32_16x16x32_bf16 a[8:11],a[172:175],v[220:223],a[8:11]
v_mfma_f32_16x16x32_bf16 a[12:15],a[172:175],v[252:255],a[12:15]
ds_read_b64_tr_b16 a[172:173],v1 offset:30976
ds_read_b64_tr_b16 a[174:175],v1 offset:36096
v_mfma_f32_16x16x32_bf16 a[12:15],a[168:171],v[248:251],a[12:15]
buffer_store_dwordx4 v[232:235],v6,s[36:39],s87 offen sc1
v_mfma_f32_16x16x32_bf16 a[12:15],a[164:167],v[236:239],a[12:15]
s_add_u32 m0,s82,86016
ds_read_b64_tr_b16 a[164:165],v1 offset:10496
ds_read_b64_tr_b16 a[166:167],v1 offset:15616
v_mfma_f32_16x16x32_bf16 a[12:15],a[160:163],v[232:235],a[12:15]
ds_read_b64_tr_b16 a[160:161],v1 offset:256
v_mfma_f32_16x16x32_bf16 a[16:19],a[176:179],v[136:139],a[16:19]
ds_read_b64_tr_b16 a[162:163],v1 offset:5376
v_mfma_f32_16x16x32_bf16 a[16:19],a[180:183],v[140:143],a[16:19]
buffer_load_dwordx4 v[240:243],v6,s[28:31],s78 offen offset:3072 sc1
v_mfma_f32_16x16x32_bf16 a[16:19],a[184:187],v[152:155],a[16:19]
ds_read_b64_tr_b16 a[168:169],v1 offset:20736
ds_read_b64_tr_b16 a[170:171],v1 offset:25856
v_mfma_f32_16x16x32_bf16 a[16:19],a[188:191],v[156:159],a[16:19]
v_add_u32 v1,40960,v1
v_mfma_f32_16x16x32_bf16 a[20:23],a[188:191],v[188:191],a[20:23]
ds_read_b64_tr_b16 a[96:97],v1 offset:128
ds_read_b64_tr_b16 a[98:99],v1 offset:5248
v_mfma_f32_16x16x32_bf16 a[20:23],a[184:187],v[184:187],a[20:23]
buffer_load_dwordx4 v11,s[72:75],s80 offen lds
v_mfma_f32_16x16x32_bf16 a[20:23],a[180:183],v[172:175],a[20:23]
ds_read_b64_tr_b16 a[100:101],v1 offset:10368
ds_read_b64_tr_b16 a[102:103],v1 offset:15488
v_mfma_f32_16x16x32_bf16 a[20:23],a[176:179],v[168:171],a[20:23]
ds_read_b64_tr_b16 a[104:105],v1 offset:20608
v_mfma_f32_16x16x32_bf16 a[24:27],a[176:179],v[200:203],a[24:27]
ds_read_b64_tr_b16 a[106:107],v1 offset:25728
v_mfma_f32_16x16x32_bf16 a[24:27],a[180:183],v[204:207],a[24:27]
buffer_load_dwordx4 v[148:151],v6,s[28:31],s79 offen sc1
v_mfma_f32_16x16x32_bf16 a[24:27],a[184:187],v[216:219],a[24:27]
ds_read_b64_tr_b16 a[108:109],v1 offset:30848
ds_read_b64_tr_b16 a[110:111],v1 offset:35968
v_mfma_f32_16x16x32_bf16 a[24:27],a[188:191],v[220:223],a[24:27]
v_mfma_f32_16x16x32_bf16 a[28:31],a[188:191],v[252:255],a[28:31]
ds_read_b64_tr_b16 a[188:189],v2 offset:31008
ds_read_b64_tr_b16 a[190:191],v2 offset:36128
v_mfma_f32_16x16x32_bf16 a[28:31],a[184:187],v[248:251],a[28:31]
s_add_u32 m0,s82,87040
buffer_load_dwordx4 v[180:183],v6,s[28:31],s79 offen offset:1024 sc1
v_mfma_f32_16x16x32_bf16 a[28:31],a[180:183],v[236:239],a[28:31]
ds_read_b64_tr_b16 a[180:181],v2 offset:10528
ds_read_b64_tr_b16 a[182:183],v2 offset:15648
v_mfma_f32_16x16x32_bf16 a[28:31],a[176:179],v[232:235],a[28:31]
s_waitcnt lgkmcnt(7)
ds_read_b64_tr_b16 a[176:177],v2 offset:288
v_mfma_f32_16x16x32_bf16 a[32:35],a[128:131],v[136:139],a[32:35]
ds_read_b64_tr_b16 a[178:179],v2 offset:5408
v_mfma_f32_16x16x32_bf16 a[32:35],a[132:135],v[140:143],a[32:35]
buffer_store_dwordx4 v[236:239],v6,s[36:39],s87 offen offset:1024 sc1
v_mfma_f32_16x16x32_bf16 a[32:35],a[136:139],v[152:155],a[32:35]
ds_read_b64_tr_b16 a[184:185],v2 offset:20768
ds_read_b64_tr_b16 a[186:187],v2 offset:25888
v_mfma_f32_16x16x32_bf16 a[32:35],a[140:143],v[156:159],a[32:35]
v_add_u32 v2,40960,v2
v_mfma_f32_16x16x32_bf16 a[36:39],a[140:143],v[188:191],a[36:39]
ds_read_b64_tr_b16 a[112:113],v2 offset:160
ds_read_b64_tr_b16 a[114:115],v2 offset:5280
v_mfma_f32_16x16x32_bf16 a[36:39],a[136:139],v[184:187],a[36:39]
)A"
R"A(buffer_load_dwordx4 v[212:215],v6,s[28:31],s79 offen offset:2048 sc1
v_mfma_f32_16x16x32_bf16 a[36:39],a[132:135],v[172:175],a[36:39]
ds_read_b64_tr_b16 a[116:117],v2 offset:10400
ds_read_b64_tr_b16 a[118:119],v2 offset:15520
v_mfma_f32_16x16x32_bf16 a[36:39],a[128:131],v[168:171],a[36:39]
ds_read_b64_tr_b16 a[120:121],v2 offset:20640
v_mfma_f32_16x16x32_bf16 a[40:43],a[128:131],v[200:203],a[40:43]
ds_read_b64_tr_b16 a[122:123],v2 offset:25760
v_mfma_f32_16x16x32_bf16 a[40:43],a[132:135],v[204:207],a[40:43]
buffer_load_dwordx4 v7,s[72:75],s83 offen lds
v_mfma_f32_16x16x32_bf16 a[40:43],a[136:139],v[216:219],a[40:43]
ds_read_b64_tr_b16 a[124:125],v2 offset:30880
ds_read_b64_tr_b16 a[126:127],v2 offset:36000
v_mfma_f32_16x16x32_bf16 a[40:43],a[140:143],v[220:223],a[40:43]
v_mfma_f32_16x16x32_bf16 a[44:47],a[140:143],v[252:255],a[44:47]
ds_read_b64_tr_b16 a[140:141],v1 offset:30720
ds_read_b64_tr_b16 a[142:143],v1 offset:35840
v_mfma_f32_16x16x32_bf16 a[44:47],a[136:139],v[248:251],a[44:47]
buffer_load_dwordx4 v[244:247],v6,s[28:31],s79 offen offset:3072 sc1
v_mfma_f32_16x16x32_bf16 a[44:47],a[132:135],v[236:239],a[44:47]
ds_read_b64_tr_b16 a[132:133],v1 offset:10240
ds_read_b64_tr_b16 a[134:135],v1 offset:15360
v_mfma_f32_16x16x32_bf16 a[44:47],a[128:131],v[232:235],a[44:47]
ds_read_b64_tr_b16 a[128:129],v1
v_mfma_f32_16x16x32_bf16 a[48:51],a[144:147],v[136:139],a[48:51]
ds_read_b64_tr_b16 a[130:131],v1 offset:5120
v_mfma_f32_16x16x32_bf16 a[48:51],a[148:151],v[140:143],a[48:51]
buffer_store_dwordx4 v[248:251],v6,s[36:39],s87 offen offset:2048 sc1
v_mfma_f32_16x16x32_bf16 a[48:51],a[152:155],v[152:155],a[48:51]
ds_read_b64_tr_b16 a[136:137],v1 offset:20480
ds_read_b64_tr_b16 a[138:139],v1 offset:25600
v_mfma_f32_16x16x32_bf16 a[48:51],a[156:159],v[156:159],a[48:51]
s_cmp_ge_u32 1,s71
s_cselect_b32 s76,4,2
s_lshl_b32 s76,s76,16
v_mfma_f32_16x16x32_bf16 a[52:55],a[156:159],v[188:191],a[52:55]
s_add_u32 s77,s76,4096
s_add_u32 s78,s76,8192
s_add_u32 s79,s76,12288
v_mfma_f32_16x16x32_bf16 a[52:55],a[152:155],v[184:187],a[52:55]
s_max_i32 s76,s76,s18
buffer_load_dwordx4 v8,s[72:75],s83 offen offset:1024 lds
v_mfma_f32_16x16x32_bf16 a[52:55],a[148:151],v[172:175],a[52:55]
s_max_i32 s77,s77,s19
s_max_i32 s78,s78,s100
s_max_i32 s79,s79,s101
v_mfma_f32_16x16x32_bf16 a[52:55],a[144:147],v[168:171],a[52:55]
v_mfma_f32_16x16x32_bf16 a[56:59],a[144:147],v[200:203],a[56:59]
v_mfma_f32_16x16x32_bf16 a[56:59],a[148:151],v[204:207],a[56:59]
buffer_store_dwordx4 v[252:255],v6,s[36:39],s87 offen offset:3072 sc1
v_mfma_f32_16x16x32_bf16 a[56:59],a[152:155],v[216:219],a[56:59]
v_mfma_f32_16x16x32_bf16 a[56:59],a[156:159],v[220:223],a[56:59]
v_mfma_f32_16x16x32_bf16 a[60:63],a[156:159],v[252:255],a[60:63]
ds_read_b64_tr_b16 a[156:157],v2 offset:30752
ds_read_b64_tr_b16 a[158:159],v2 offset:35872
v_mfma_f32_16x16x32_bf16 a[60:63],a[152:155],v[248:251],a[60:63]
ds_read_b64_tr_b16 a[152:153],v2 offset:20512
v_mfma_f32_16x16x32_bf16 a[60:63],a[148:151],v[236:239],a[60:63]
buffer_load_dwordx4 v9,s[72:75],s83 offen offset:2048 lds
v_mfma_f32_16x16x32_bf16 a[60:63],a[144:147],v[232:235],a[60:63]
ds_read_b64_tr_b16 a[144:145],v2 offset:32
ds_read_b64_tr_b16 a[146:147],v2 offset:5152
v_mfma_f32_16x16x32_bf16 a[64:67],a[160:163],v[136:139],a[64:67]
ds_read_b64_tr_b16 a[148:149],v2 offset:10272
v_mfma_f32_16x16x32_bf16 a[64:67],a[164:167],v[140:143],a[64:67]
ds_read_b64_tr_b16 a[150:151],v2 offset:15392
v_mfma_f32_16x16x32_bf16 a[64:67],a[168:171],v[152:155],a[64:67]
ds_read_b64_tr_b16 a[154:155],v2 offset:25632
v_mfma_f32_16x16x32_bf16 a[64:67],a[172:175],v[156:159],a[64:67]
v_mfma_f32_16x16x32_bf16 a[68:71],a[172:175],v[188:191],a[68:71]
v_mfma_f32_16x16x32_bf16 a[68:71],a[168:171],v[184:187],a[68:71]
v_mfma_f32_16x16x32_bf16 a[68:71],a[164:167],v[172:175],a[68:71]
v_mfma_f32_16x16x32_bf16 a[68:71],a[160:163],v[168:171],a[68:71]
v_mfma_f32_16x16x32_bf16 a[72:75],a[160:163],v[200:203],a[72:75]
v_mfma_f32_16x16x32_bf16 a[72:75],a[164:167],v[204:207],a[72:75]
v_mfma_f32_16x16x32_bf16 a[72:75],a[168:171],v[216:219],a[72:75]
v_mfma_f32_16x16x32_bf16 a[72:75],a[172:175],v[220:223],a[72:75]
buffer_load_dwordx4 v[64:67],v6,s[28:31],s76 offen sc1
v_mfma_f32_16x16x32_bf16 a[76:79],a[172:175],v[252:255],a[76:79]
ds_read_b64_tr_b16 a[172:173],v1 offset:30784
ds_read_b64_tr_b16 a[174:175],v1 offset:35904
v_mfma_f32_16x16x32_bf16 a[76:79],a[168:171],v[248:251],a[76:79]
ds_read_b64_tr_b16 a[168:169],v1 offset:20544
v_mfma_f32_16x16x32_bf16 a[76:79],a[164:167],v[236:239],a[76:79]
ds_read_b64_tr_b16 a[164:165],v1 offset:10304
v_mfma_f32_16x16x32_bf16 a[76:79],a[160:163],v[232:235],a[76:79]
s_waitcnt lgkmcnt(4)
buffer_load_dwordx4 v10,s[72:75],s83 offen offset:3072 lds
v_mfma_f32_16x16x32_bf16 a[80:83],a[176:179],v[136:139],a[80:83]
ds_read_b64_tr_b16 a[160:161],v1 offset:64
ds_read_b64_tr_b16 a[162:163],v1 offset:5184
v_mfma_f32_16x16x32_bf16 a[80:83],a[180:183],v[140:143],a[80:83]
ds_read_b64_tr_b16 a[166:167],v1 offset:15424
v_mfma_f32_16x16x32_bf16 a[80:83],a[184:187],v[152:155],a[80:83]
ds_read_b64_tr_b16 a[170:171],v1 offset:25664
v_mfma_f32_16x16x32_bf16 a[80:83],a[188:191],v[156:159],a[80:83]
buffer_load_dwordx4 v[68:71],v6,s[28:31],s76 offen offset:1024 sc1
v_mfma_f32_16x16x32_bf16 a[84:87],a[188:191],v[188:191],a[84:87]
v_mfma_f32_16x16x32_bf16 a[84:87],a[184:187],v[184:187],a[84:87]
v_mfma_f32_16x16x32_bf16 a[84:87],a[180:183],v[172:175],a[84:87]
v_mfma_f32_16x16x32_bf16 a[84:87],a[176:179],v[168:171],a[84:87]
v_mfma_f32_16x16x32_bf16 a[88:91],a[176:179],v[200:203],a[88:91]
v_mfma_f32_16x16x32_bf16 a[88:91],a[180:183],v[204:207],a[88:91]
s_add_u32 m0,s82,91136
v_mfma_f32_16x16x32_bf16 a[88:91],a[184:187],v[216:219],a[88:91]
v_mfma_f32_16x16x32_bf16 a[88:91],a[188:191],v[220:223],a[88:91]
buffer_load_dwordx4 v[72:75],v6,s[28:31],s76 offen offset:2048 sc1
v_mfma_f32_16x16x32_bf16 a[92:95],a[188:191],v[252:255],a[92:95]
ds_read_b64_tr_b16 a[188:189],v2 offset:30816
ds_read_b64_tr_b16 a[190:191],v2 offset:35936
v_mfma_f32_16x16x32_bf16 a[92:95],a[184:187],v[248:251],a[92:95]
ds_read_b64_tr_b16 a[184:185],v2 offset:20576
v_mfma_f32_16x16x32_bf16 a[92:95],a[180:183],v[236:239],a[92:95]
s_cmp_ge_u32 0,s71
s_cselect_b32 s80,3,1
s_mul_i32 s80,s80,163840
v_mfma_f32_16x16x32_bf16 a[92:95],a[176:179],v[232:235],a[92:95]
s_add_u32 s80,s80,s81
buffer_load_dwordx4 v11,s[72:75],s83 offen lds
s_waitcnt vmcnt(14)
v_mfma_f32_16x16x32_bf16 a[192:195],a[128:131],v[128:131],a[192:195]
s_add_u32 s83,s80,20480
ds_read_b64_tr_b16 a[176:177],v2 offset:96
ds_read_b64_tr_b16 a[178:179],v2 offset:5216
v_mfma_f32_16x16x32_bf16 a[192:195],a[132:135],v[160:163],a[192:195]
ds_read_b64_tr_b16 a[180:181],v2 offset:10336
v_mfma_f32_16x16x32_bf16 a[192:195],a[136:139],v[192:195],a[192:195]
ds_read_b64_tr_b16 a[182:183],v2 offset:15456
v_mfma_f32_16x16x32_bf16 a[192:195],a[140:143],v[224:227],a[192:195]
buffer_load_dwordx4 v[76:79],v6,s[28:31],s76 offen offset:3072 sc1
v_mfma_f32_16x16x32_bf16 a[196:199],a[140:143],v[228:231],a[196:199]
ds_read_b64_tr_b16 a[186:187],v2 offset:25696
v_mfma_f32_16x16x32_bf16 a[196:199],a[136:139],v[196:199],a[196:199]
v_mfma_f32_16x16x32_bf16 a[196:199],a[132:135],v[164:167],a[196:199]
v_mfma_f32_16x16x32_bf16 a[196:199],a[128:131],v[132:135],a[196:199]
v_mfma_f32_16x16x32_bf16 a[200:203],a[128:131],v[144:147],a[200:203]
v_mfma_f32_16x16x32_bf16 a[200:203],a[132:135],v[176:179],a[200:203]
v_mfma_f32_16x16x32_bf16 a[200:203],a[136:139],v[208:211],a[200:203]
v_mfma_f32_16x16x32_bf16 a[200:203],a[140:143],v[240:243],a[200:203]
s_lshl_b32 s95,s42,2
buffer_load_dwordx4 v[80:83],v6,s[28:31],s77 offen sc1
s_waitcnt vmcnt(11)
v_mfma_f32_16x16x32_bf16 a[204:207],a[140:143],v[244:247],a[204:207]
s_add_u32 s95,s95,s40
s_lshl_b32 s95,s95,4
v_mov_b32 v18,s68
v_mfma_f32_16x16x32_bf16 a[204:207],a[136:139],v[212:215],a[204:207]
ds_read_b64_tr_b16 a[136:137],v1 offset:20672
v_mov_b32 v19,s69
v_mfma_f32_16x16x32_bf16 a[204:207],a[132:135],v[180:183],a[204:207]
ds_read_b64_tr_b16 a[132:133],v1 offset:10432
ds_read_b64_tr_b16 a[134:135],v1 offset:15552
v_mfma_f32_16x16x32_bf16 a[204:207],a[128:131],v[148:151],a[204:207]
s_barrier
ds_read_b64_tr_b16 a[128:129],v1 offset:192
ds_read_b64_tr_b16 a[130:131],v1 offset:5312
v_mfma_f32_16x16x32_bf16 a[208:211],a[144:147],v[128:131],a[208:211]
ds_read_b64_tr_b16 a[138:139],v1 offset:25792
v_mfma_f32_16x16x32_bf16 a[208:211],a[148:151],v[160:163],a[208:211]
ds_read_b64_tr_b16 a[140:141],v1 offset:30912
v_mfma_f32_16x16x32_bf16 a[208:211],a[152:155],v[192:195],a[208:211]
s_add_u32 m0,s82,0
s_waitcnt vmcnt(8)
s_mov_b64 exec,1
buffer_store_dwordx2 v[18:19],v6,s[64:67],s95 offen offset:1024 sc1
s_mov_b64 exec,-1
v_mfma_f32_16x16x32_bf16 a[208:211],a[156:159],v[224:227],a[208:211]
ds_read_b64_tr_b16 a[142:143],v1 offset:36032
v_mfma_f32_16x16x32_bf16 a[212:215],a[156:159],v[228:231],a[212:215]
v_mfma_f32_16x16x32_bf16 a[212:215],a[152:155],v[196:199],a[212:215]
v_mfma_f32_16x16x32_bf16 a[212:215],a[148:151],v[164:167],a[212:215]
buffer_load_dwordx4 v[84:87],v6,s[28:31],s77 offen offset:1024 sc1
v_mfma_f32_16x16x32_bf16 a[212:215],a[144:147],v[132:135],a[212:215]
v_mfma_f32_16x16x32_bf16 a[216:219],a[144:147],v[144:147],a[216:219]
v_mfma_f32_16x16x32_bf16 a[216:219],a[148:151],v[176:179],a[216:219]
v_mfma_f32_16x16x32_bf16 a[216:219],a[152:155],v[208:211],a[216:219]
buffer_load_dwordx4 v7,s[72:75],s80 offen lds
v_mfma_f32_16x16x32_bf16 a[216:219],a[156:159],v[240:243],a[216:219]
v_mfma_f32_16x16x32_bf16 a[220:223],a[156:159],v[244:247],a[220:223]
ds_read_b64_tr_b16 a[156:157],v2 offset:30944
ds_read_b64_tr_b16 a[158:159],v2 offset:36064
v_mfma_f32_16x16x32_bf16 a[220:223],a[152:155],v[212:215],a[220:223]
ds_read_b64_tr_b16 a[152:153],v2 offset:20704
v_mfma_f32_16x16x32_bf16 a[220:223],a[148:151],v[180:183],a[220:223]
buffer_load_dwordx4 v[88:91],v6,s[28:31],s77 offen offset:2048 sc1
v_mfma_f32_16x16x32_bf16 a[220:223],a[144:147],v[148:151],a[220:223]
s_waitcnt lgkmcnt(3)
ds_read_b64_tr_b16 a[144:145],v2 offset:224
ds_read_b64_tr_b16 a[146:147],v2 offset:5344
v_mfma_f32_16x16x32_bf16 a[224:227],a[160:163],v[128:131],a[224:227]
ds_read_b64_tr_b16 a[148:149],v2 offset:10464
v_mfma_f32_16x16x32_bf16 a[224:227],a[164:167],v[160:163],a[224:227]
ds_read_b64_tr_b16 a[150:151],v2 offset:15584
v_mfma_f32_16x16x32_bf16 a[224:227],a[168:171],v[192:195],a[224:227]
buffer_load_dwordx4 v[92:95],v6,s[28:31],s77 offen offset:3072 sc1
v_mfma_f32_16x16x32_bf16 a[224:227],a[172:175],v[224:227],a[224:227]
ds_read_b64_tr_b16 a[154:155],v2 offset:25824
v_mfma_f32_16x16x32_bf16 a[228:231],a[172:175],v[228:231],a[228:231]
v_mfma_f32_16x16x32_bf16 a[228:231],a[168:171],v[196:199],a[228:231]
v_mfma_f32_16x16x32_bf16 a[228:231],a[164:167],v[164:167],a[228:231]
buffer_load_dwordx4 v8,s[72:75],s80 offen offset:1024 lds
v_mfma_f32_16x16x32_bf16 a[228:231],a[160:163],v[132:135],a[228:231]
v_mfma_f32_16x16x32_bf16 a[232:235],a[160:163],v[144:147],a[232:235]
v_mfma_f32_16x16x32_bf16 a[232:235],a[164:167],v[176:179],a[232:235]
v_mfma_f32_16x16x32_bf16 a[232:235],a[168:171],v[208:211],a[232:235]
buffer_load_dwordx4 v[96:99],v6,s[28:31],s78 offen sc1
v_mfma_f32_16x16x32_bf16 a[232:235],a[172:175],v[240:243],a[232:235]
v_mfma_f32_16x16x32_bf16 a[236:239],a[172:175],v[244:247],a[236:239]
ds_read_b64_tr_b16 a[172:173],v1 offset:30976
ds_read_b64_tr_b16 a[174:175],v1 offset:36096
v_mfma_f32_16x16x32_bf16 a[236:239],a[168:171],v[212:215],a[236:239]
ds_read_b64_tr_b16 a[168:169],v1 offset:20736
v_mfma_f32_16x16x32_bf16 a[236:239],a[164:167],v[180:183],a[236:239]
ds_read_b64_tr_b16 a[164:165],v1 offset:10496
v_mfma_f32_16x16x32_bf16 a[236:239],a[160:163],v[148:151],a[236:239]
ds_read_b64_tr_b16 a[160:161],v1 offset:256
v_mfma_f32_16x16x32_bf16 a[240:243],a[176:179],v[128:131],a[240:243]
ds_read_b64_tr_b16 a[162:163],v1 offset:5376
ds_read_b64_tr_b16 a[166:167],v1 offset:15616
v_mfma_f32_16x16x32_bf16 a[240:243],a[180:183],v[160:163],a[240:243]
ds_read_b64_tr_b16 a[170:171],v1 offset:25856
v_add_u32 v1,81920,v1
v_mfma_f32_16x16x32_bf16 a[240:243],a[184:187],v[192:195],a[240:243]
)A"
R"A(buffer_load_dwordx4 v[100:103],v6,s[28:31],s78 offen offset:1024 sc1
v_mfma_f32_16x16x32_bf16 a[240:243],a[188:191],v[224:227],a[240:243]
v_mfma_f32_16x16x32_bf16 a[244:247],a[188:191],v[228:231],a[244:247]
v_mfma_f32_16x16x32_bf16 a[244:247],a[184:187],v[196:199],a[244:247]
v_mfma_f32_16x16x32_bf16 a[244:247],a[180:183],v[164:167],a[244:247]
buffer_load_dwordx4 v9,s[72:75],s80 offen offset:2048 lds
v_mfma_f32_16x16x32_bf16 a[244:247],a[176:179],v[132:135],a[244:247]
v_mfma_f32_16x16x32_bf16 a[248:251],a[176:179],v[144:147],a[248:251]
v_mfma_f32_16x16x32_bf16 a[248:251],a[180:183],v[176:179],a[248:251]
v_mfma_f32_16x16x32_bf16 a[248:251],a[184:187],v[208:211],a[248:251]
buffer_load_dwordx4 v[104:107],v6,s[28:31],s78 offen offset:2048 sc1
v_mfma_f32_16x16x32_bf16 a[248:251],a[188:191],v[240:243],a[248:251]
v_mfma_f32_16x16x32_bf16 a[252:255],a[188:191],v[244:247],a[252:255]
ds_read_b64_tr_b16 a[188:189],v2 offset:31008
ds_read_b64_tr_b16 a[190:191],v2 offset:36128
v_mfma_f32_16x16x32_bf16 a[252:255],a[184:187],v[212:215],a[252:255]
ds_read_b64_tr_b16 a[184:185],v2 offset:20768
v_mfma_f32_16x16x32_bf16 a[252:255],a[180:183],v[180:183],a[252:255]
ds_read_b64_tr_b16 a[180:181],v2 offset:10528
v_mfma_f32_16x16x32_bf16 a[252:255],a[176:179],v[148:151],a[252:255]
ds_read_b64_tr_b16 a[176:177],v2 offset:288
v_mfma_f32_16x16x32_bf16 a[0:3],a[96:99],v[128:131],a[0:3]
ds_read_b64_tr_b16 a[178:179],v2 offset:5408
ds_read_b64_tr_b16 a[182:183],v2 offset:15648
v_mfma_f32_16x16x32_bf16 a[0:3],a[100:103],v[160:163],a[0:3]
ds_read_b64_tr_b16 a[186:187],v2 offset:25888
v_add_u32 v2,81920,v2
v_mfma_f32_16x16x32_bf16 a[0:3],a[104:107],v[192:195],a[0:3]
buffer_load_dwordx4 v[108:111],v6,s[28:31],s78 offen offset:3072 sc1
v_mfma_f32_16x16x32_bf16 a[0:3],a[108:111],v[224:227],a[0:3]
v_mfma_f32_16x16x32_bf16 a[4:7],a[108:111],v[228:231],a[4:7]
v_mfma_f32_16x16x32_bf16 a[4:7],a[104:107],v[196:199],a[4:7]
v_mfma_f32_16x16x32_bf16 a[4:7],a[100:103],v[164:167],a[4:7]
buffer_load_dwordx4 v10,s[72:75],s80 offen offset:3072 lds
v_mfma_f32_16x16x32_bf16 a[4:7],a[96:99],v[132:135],a[4:7]
v_mfma_f32_16x16x32_bf16 a[8:11],a[96:99],v[144:147],a[8:11]
v_mfma_f32_16x16x32_bf16 a[8:11],a[100:103],v[176:179],a[8:11]
v_mfma_f32_16x16x32_bf16 a[8:11],a[104:107],v[208:211],a[8:11]
buffer_load_dwordx4 v[112:115],v6,s[28:31],s79 offen sc1
v_mfma_f32_16x16x32_bf16 a[8:11],a[108:111],v[240:243],a[8:11]
v_mfma_f32_16x16x32_bf16 a[12:15],a[108:111],v[244:247],a[12:15]
ds_read_b64_tr_b16 a[108:109],v1 offset:30720
ds_read_b64_tr_b16 a[110:111],v1 offset:35840
v_mfma_f32_16x16x32_bf16 a[12:15],a[104:107],v[212:215],a[12:15]
ds_read_b64_tr_b16 a[104:105],v1 offset:20480
v_mfma_f32_16x16x32_bf16 a[12:15],a[100:103],v[180:183],a[12:15]
s_add_u32 m0,s82,4096
ds_read_b64_tr_b16 a[100:101],v1 offset:10240
v_mfma_f32_16x16x32_bf16 a[12:15],a[96:99],v[148:151],a[12:15]
ds_read_b64_tr_b16 a[96:97],v1
v_mfma_f32_16x16x32_bf16 a[16:19],a[112:115],v[128:131],a[16:19]
ds_read_b64_tr_b16 a[98:99],v1 offset:5120
ds_read_b64_tr_b16 a[102:103],v1 offset:15360
v_mfma_f32_16x16x32_bf16 a[16:19],a[116:119],v[160:163],a[16:19]
buffer_load_dwordx4 v11,s[72:75],s80 offen lds
v_mfma_f32_16x16x32_bf16 a[16:19],a[120:123],v[192:195],a[16:19]
ds_read_b64_tr_b16 a[106:107],v1 offset:25600
v_mfma_f32_16x16x32_bf16 a[16:19],a[124:127],v[224:227],a[16:19]
v_mfma_f32_16x16x32_bf16 a[20:23],a[124:127],v[228:231],a[20:23]
v_mfma_f32_16x16x32_bf16 a[20:23],a[120:123],v[196:199],a[20:23]
buffer_load_dwordx4 v[116:119],v6,s[28:31],s79 offen offset:1024 sc1
v_mfma_f32_16x16x32_bf16 a[20:23],a[116:119],v[164:167],a[20:23]
v_mfma_f32_16x16x32_bf16 a[20:23],a[112:115],v[132:135],a[20:23]
v_mfma_f32_16x16x32_bf16 a[24:27],a[112:115],v[144:147],a[24:27]
v_mfma_f32_16x16x32_bf16 a[24:27],a[116:119],v[176:179],a[24:27]
v_mfma_f32_16x16x32_bf16 a[24:27],a[120:123],v[208:211],a[24:27]
buffer_load_dwordx4 v[120:123],v6,s[28:31],s79 offen offset:2048 sc1
v_mfma_f32_16x16x32_bf16 a[24:27],a[124:127],v[240:243],a[24:27]
v_mfma_f32_16x16x32_bf16 a[28:31],a[124:127],v[244:247],a[28:31]
ds_read_b64_tr_b16 a[124:125],v2 offset:30752
ds_read_b64_tr_b16 a[126:127],v2 offset:35872
v_mfma_f32_16x16x32_bf16 a[28:31],a[120:123],v[212:215],a[28:31]
s_add_u32 m0,s82,5120
ds_read_b64_tr_b16 a[120:121],v2 offset:20512
v_mfma_f32_16x16x32_bf16 a[28:31],a[116:119],v[180:183],a[28:31]
ds_read_b64_tr_b16 a[116:117],v2 offset:10272
v_mfma_f32_16x16x32_bf16 a[28:31],a[112:115],v[148:151],a[28:31]
s_cmp_ge_u32 2,s71
s_cselect_b32 s76,6,4
s_lshl_b32 s76,s76,16
v_mfma_f32_16x16x32_bf16 a[32:35],a[128:131],v[128:131],a[32:35]
s_add_u32 s77,s76,4096
buffer_load_dwordx4 v7,s[72:75],s83 offen lds
v_mfma_f32_16x16x32_bf16 a[32:35],a[132:135],v[160:163],a[32:35]
s_add_u32 s78,s76,8192
s_max_i32 s77,s77,s19
ds_read_b64_tr_b16 a[112:113],v2 offset:32
v_mfma_f32_16x16x32_bf16 a[32:35],a[136:139],v[192:195],a[32:35]
s_max_i32 s78,s78,s100
ds_read_b64_tr_b16 a[114:115],v2 offset:5152
ds_read_b64_tr_b16 a[118:119],v2 offset:15392
v_mfma_f32_16x16x32_bf16 a[32:35],a[140:143],v[224:227],a[32:35]
ds_read_b64_tr_b16 a[122:123],v2 offset:25632
v_mfma_f32_16x16x32_bf16 a[36:39],a[140:143],v[228:231],a[36:39]
buffer_load_dwordx4 v[124:127],v6,s[28:31],s79 offen offset:3072 sc1
v_mfma_f32_16x16x32_bf16 a[36:39],a[136:139],v[196:199],a[36:39]
s_add_u32 s79,s76,12288
s_max_i32 s76,s76,s18
s_max_i32 s79,s79,s101
v_mfma_f32_16x16x32_bf16 a[36:39],a[132:135],v[164:167],a[36:39]
v_mfma_f32_16x16x32_bf16 a[36:39],a[128:131],v[132:135],a[36:39]
v_mfma_f32_16x16x32_bf16 a[40:43],a[128:131],v[144:147],a[40:43]
v_mfma_f32_16x16x32_bf16 a[40:43],a[132:135],v[176:179],a[40:43]
v_mfma_f32_16x16x32_bf16 a[40:43],a[136:139],v[208:211],a[40:43]
v_mfma_f32_16x16x32_bf16 a[40:43],a[140:143],v[240:243],a[40:43]
v_mfma_f32_16x16x32_bf16 a[44:47],a[140:143],v[244:247],a[44:47]
ds_read_b64_tr_b16 a[140:141],v1 offset:30784
ds_read_b64_tr_b16 a[142:143],v1 offset:35904
v_mfma_f32_16x16x32_bf16 a[44:47],a[136:139],v[212:215],a[44:47]
ds_read_b64_tr_b16 a[136:137],v1 offset:20544
v_mfma_f32_16x16x32_bf16 a[44:47],a[132:135],v[180:183],a[44:47]
ds_read_b64_tr_b16 a[132:133],v1 offset:10304
v_mfma_f32_16x16x32_bf16 a[44:47],a[128:131],v[148:151],a[44:47]
s_waitcnt lgkmcnt(4)
buffer_load_dwordx4 v8,s[72:75],s83 offen offset:1024 lds
v_mfma_f32_16x16x32_bf16 a[48:51],a[144:147],v[128:131],a[48:51]
ds_read_b64_tr_b16 a[128:129],v1 offset:64
ds_read_b64_tr_b16 a[130:131],v1 offset:5184
v_mfma_f32_16x16x32_bf16 a[48:51],a[148:151],v[160:163],a[48:51]
ds_read_b64_tr_b16 a[134:135],v1 offset:15424
v_mfma_f32_16x16x32_bf16 a[48:51],a[152:155],v[192:195],a[48:51]
ds_read_b64_tr_b16 a[138:139],v1 offset:25664
v_mfma_f32_16x16x32_bf16 a[48:51],a[156:159],v[224:227],a[48:51]
s_lshl_b32 s94,s70,4
v_bfe_u32 v24,v6,4,2
v_min_u32 v24,2,v24
v_mfma_f32_16x16x32_bf16 a[52:55],a[156:159],v[228:231],a[52:55]
s_add_u32 s94,s94,s40
v_add_u32 v24,s71,v24
v_add_u32 v24,1,v24
v_mfma_f32_16x16x32_bf16 a[52:55],a[152:155],v[196:199],a[52:55]
s_lshl_b32 s94,s94,4
v_and_b32 v24,3,v24
v_lshlrev_b32 v24,6,v24
v_mfma_f32_16x16x32_bf16 a[52:55],a[148:151],v[164:167],a[52:55]
v_mfma_f32_16x16x32_bf16 a[52:55],a[144:147],v[132:135],a[52:55]
buffer_load_dwordx4 v[136:139],v6,s[28:31],s76 offen sc1
v_mfma_f32_16x16x32_bf16 a[56:59],a[144:147],v[144:147],a[56:59]
v_mfma_f32_16x16x32_bf16 a[56:59],a[148:151],v[176:179],a[56:59]
v_mfma_f32_16x16x32_bf16 a[56:59],a[152:155],v[208:211],a[56:59]
v_mfma_f32_16x16x32_bf16 a[56:59],a[156:159],v[240:243],a[56:59]
v_mfma_f32_16x16x32_bf16 a[60:63],a[156:159],v[244:247],a[60:63]
ds_read_b64_tr_b16 a[156:157],v2 offset:30816
ds_read_b64_tr_b16 a[158:159],v2 offset:35936
v_mfma_f32_16x16x32_bf16 a[60:63],a[152:155],v[212:215],a[60:63]
buffer_load_dwordx4 v[140:143],v6,s[28:31],s76 offen offset:1024 sc1
v_mfma_f32_16x16x32_bf16 a[60:63],a[148:151],v[180:183],a[60:63]
ds_read_b64_tr_b16 a[148:149],v2 offset:10336
ds_read_b64_tr_b16 a[150:151],v2 offset:15456
v_mfma_f32_16x16x32_bf16 a[60:63],a[144:147],v[148:151],a[60:63]
ds_read_b64_tr_b16 a[144:145],v2 offset:96
v_mfma_f32_16x16x32_bf16 a[64:67],a[160:163],v[128:131],a[64:67]
ds_read_b64_tr_b16 a[146:147],v2 offset:5216
v_mfma_f32_16x16x32_bf16 a[64:67],a[164:167],v[160:163],a[64:67]
buffer_load_dwordx4 v9,s[72:75],s83 offen offset:2048 lds
v_mfma_f32_16x16x32_bf16 a[64:67],a[168:171],v[192:195],a[64:67]
ds_read_b64_tr_b16 a[152:153],v2 offset:20576
ds_read_b64_tr_b16 a[154:155],v2 offset:25696
v_mfma_f32_16x16x32_bf16 a[64:67],a[172:175],v[224:227],a[64:67]
v_mfma_f32_16x16x32_bf16 a[68:71],a[172:175],v[228:231],a[68:71]
v_mfma_f32_16x16x32_bf16 a[68:71],a[168:171],v[196:199],a[68:71]
buffer_load_dwordx4 v[152:155],v6,s[28:31],s76 offen offset:2048 sc1
v_mfma_f32_16x16x32_bf16 a[68:71],a[164:167],v[164:167],a[68:71]
v_mfma_f32_16x16x32_bf16 a[68:71],a[160:163],v[132:135],a[68:71]
v_mfma_f32_16x16x32_bf16 a[72:75],a[160:163],v[144:147],a[72:75]
v_mfma_f32_16x16x32_bf16 a[72:75],a[164:167],v[176:179],a[72:75]
buffer_load_dwordx4 v[156:159],v6,s[28:31],s76 offen offset:3072 sc1
v_mfma_f32_16x16x32_bf16 a[72:75],a[168:171],v[208:211],a[72:75]
v_mfma_f32_16x16x32_bf16 a[72:75],a[172:175],v[240:243],a[72:75]
v_mfma_f32_16x16x32_bf16 a[76:79],a[172:175],v[244:247],a[76:79]
ds_read_b64_tr_b16 a[172:173],v1 offset:30848
ds_read_b64_tr_b16 a[174:175],v1 offset:35968
v_mfma_f32_16x16x32_bf16 a[76:79],a[168:171],v[212:215],a[76:79]
buffer_load_dwordx4 v10,s[72:75],s83 offen offset:3072 lds
v_mfma_f32_16x16x32_bf16 a[76:79],a[164:167],v[180:183],a[76:79]
ds_read_b64_tr_b16 a[164:165],v1 offset:10368
ds_read_b64_tr_b16 a[166:167],v1 offset:15488
v_mfma_f32_16x16x32_bf16 a[76:79],a[160:163],v[148:151],a[76:79]
ds_read_b64_tr_b16 a[160:161],v1 offset:128
v_mfma_f32_16x16x32_bf16 a[80:83],a[176:179],v[128:131],a[80:83]
ds_read_b64_tr_b16 a[162:163],v1 offset:5248
v_mfma_f32_16x16x32_bf16 a[80:83],a[180:183],v[160:163],a[80:83]
buffer_load_dwordx4 v[168:171],v6,s[28:31],s77 offen sc1
v_mfma_f32_16x16x32_bf16 a[80:83],a[184:187],v[192:195],a[80:83]
ds_read_b64_tr_b16 a[168:169],v1 offset:20608
ds_read_b64_tr_b16 a[170:171],v1 offset:25728
v_mfma_f32_16x16x32_bf16 a[80:83],a[188:191],v[224:227],a[80:83]
v_mfma_f32_16x16x32_bf16 a[84:87],a[188:191],v[228:231],a[84:87]
v_mfma_f32_16x16x32_bf16 a[84:87],a[184:187],v[196:199],a[84:87]
buffer_load_dwordx4 v[172:175],v6,s[28:31],s77 offen offset:1024 sc1
v_mfma_f32_16x16x32_bf16 a[84:87],a[180:183],v[164:167],a[84:87]
v_mfma_f32_16x16x32_bf16 a[84:87],a[176:179],v[132:135],a[84:87]
v_mfma_f32_16x16x32_bf16 a[88:91],a[176:179],v[144:147],a[88:91]
v_mfma_f32_16x16x32_bf16 a[88:91],a[180:183],v[176:179],a[88:91]
s_add_u32 m0,s82,9216
v_mfma_f32_16x16x32_bf16 a[88:91],a[184:187],v[208:211],a[88:91]
v_mfma_f32_16x16x32_bf16 a[88:91],a[188:191],v[240:243],a[88:91]
buffer_load_dwordx2 v[26:27],v24,s[64:67],s94 offen offset:1024 sc1
v_mfma_f32_16x16x32_bf16 a[92:95],a[188:191],v[244:247],a[92:95]
ds_read_b64_tr_b16 a[188:189],v2 offset:30880
ds_read_b64_tr_b16 a[190:191],v2 offset:36000
v_mfma_f32_16x16x32_bf16 a[92:95],a[184:187],v[212:215],a[92:95]
ds_read_b64_tr_b16 a[184:185],v2 offset:20640
v_mfma_f32_16x16x32_bf16 a[92:95],a[180:183],v[180:183],a[92:95]
ds_read_b64_tr_b16 a[180:181],v2 offset:10400
v_mfma_f32_16x16x32_bf16 a[92:95],a[176:179],v[148:151],a[92:95]
s_waitcnt vmcnt(13)
buffer_load_dwordx4 v[184:187],v6,s[28:31],s77 offen offset:2048 sc1
v_mfma_f32_16x16x32_bf16 a[192:195],a[96:99],v[64:67],a[192:195]
s_lshl_b32 s43,s41,2
ds_read_b64_tr_b16 a[176:177],v2 offset:160
ds_read_b64_tr_b16 a[178:179],v2 offset:5280
v_mfma_f32_16x16x32_bf16 a[192:195],a[100:103],v[68:71],a[192:195]
s_add_u32 s84,s70,1
s_add_u32 s85,s70,2
ds_read_b64_tr_b16 a[182:183],v2 offset:15520
v_mfma_f32_16x16x32_bf16 a[192:195],a[104:107],v[72:75],a[192:195]
s_add_u32 s86,s70,3
s_add_u32 s43,s43,s71
ds_read_b64_tr_b16 a[186:187],v2 offset:25760
v_mfma_f32_16x16x32_bf16 a[192:195],a[108:111],v[76:79],a[192:195]
s_and_b32 s84,s84,3
buffer_load_dwordx4 v11,s[72:75],s83 offen lds
v_mfma_f32_16x16x32_bf16 a[196:199],a[108:111],v[92:95],a[196:199]
)A"
R"A(s_and_b32 s85,s85,3
s_and_b32 s86,s86,3
s_mul_i32 s43,s43,245760
v_mfma_f32_16x16x32_bf16 a[196:199],a[104:107],v[88:91],a[196:199]
s_mul_i32 s84,s84,12
s_mul_i32 s85,s85,12
s_mul_i32 s86,s86,12
v_mfma_f32_16x16x32_bf16 a[196:199],a[100:103],v[84:87],a[196:199]
s_add_u32 s43,s43,134217728
s_mov_b64 s[32:33],s[12:13]
s_add_u32 s84,s84,0
v_mfma_f32_16x16x32_bf16 a[196:199],a[96:99],v[80:83],a[196:199]
s_add_u32 s85,s85,4
buffer_load_dwordx4 v[188:191],v6,s[28:31],s77 offen offset:3072 sc1
v_mfma_f32_16x16x32_bf16 a[200:203],a[96:99],v[96:99],a[200:203]
s_add_u32 s86,s86,8
s_add_u32 s84,s84,s40
s_add_u32 s85,s85,s40
v_mfma_f32_16x16x32_bf16 a[200:203],a[100:103],v[100:103],a[200:203]
s_add_u32 s86,s86,s40
s_add_u32 s32,s32,s43
s_addc_u32 s33,s33,0
v_mfma_f32_16x16x32_bf16 a[200:203],a[104:107],v[104:107],a[200:203]
s_mul_i32 s84,s84,5120
s_mul_i32 s85,s85,5120
s_mul_i32 s86,s86,5120
v_mfma_f32_16x16x32_bf16 a[200:203],a[108:111],v[108:111],a[200:203]
s_and_b32 s33,s33,0xffff
buffer_load_dwordx4 v[200:203],v6,s[28:31],s78 offen sc1
s_waitcnt vmcnt(14)
v_mfma_f32_16x16x32_bf16 a[204:207],a[108:111],v[124:127],a[204:207]
s_mov_b32 s34,245760
ds_read_b64_tr_b16 a[108:109],v1 offset:30912
ds_read_b64_tr_b16 a[110:111],v1 offset:36032
v_mfma_f32_16x16x32_bf16 a[204:207],a[104:107],v[120:123],a[204:207]
s_mov_b32 s35,0x20000
s_max_i32 s84,s84,s18
ds_read_b64_tr_b16 a[104:105],v1 offset:20672
v_mfma_f32_16x16x32_bf16 a[204:207],a[100:103],v[116:119],a[204:207]
s_max_i32 s85,s85,s19
s_max_i32 s86,s86,s100
ds_read_b64_tr_b16 a[100:101],v1 offset:10432
v_mfma_f32_16x16x32_bf16 a[204:207],a[96:99],v[112:115],a[204:207]
s_barrier
buffer_load_dwordx4 v[204:207],v6,s[28:31],s78 offen offset:1024 sc1
v_mfma_f32_16x16x32_bf16 a[208:211],a[112:115],v[64:67],a[208:211]
s_mul_i32 s44,s41,307200
ds_read_b64_tr_b16 a[96:97],v1 offset:192
ds_read_b64_tr_b16 a[98:99],v1 offset:5312
v_mfma_f32_16x16x32_bf16 a[208:211],a[116:119],v[68:71],a[208:211]
s_mov_b64 s[20:21],s[16:17]
ds_read_b64_tr_b16 a[102:103],v1 offset:15552
v_bfe_u32 v18,v6,6,2
v_mfma_f32_16x16x32_bf16 a[208:211],a[120:123],v[72:75],a[208:211]
s_lshl_b32 s43,s40,2
ds_read_b64_tr_b16 a[106:107],v1 offset:25792
v_lshrrev_b32 v19,8,v6
v_mfma_f32_16x16x32_bf16 a[208:211],a[124:127],v[76:79],a[208:211]
s_add_u32 s43,s43,s70
buffer_load_dwordx4 v[216:219],v6,s[28:31],s78 offen offset:2048 sc1
v_mfma_f32_16x16x32_bf16 a[212:215],a[124:127],v[92:95],a[212:215]
s_mul_i32 s43,s43,20480
v_xor_b32 v18,v18,v19
v_lshlrev_b32 v18,2,v18
v_mfma_f32_16x16x32_bf16 a[212:215],a[120:123],v[88:91],a[212:215]
s_mov_b32 s22,307200
v_bfe_u32 v19,v6,4,2
v_or_b32 v18,v18,v19
v_mfma_f32_16x16x32_bf16 a[212:215],a[116:119],v[84:87],a[212:215]
s_mov_b32 s23,0x20000
v_and_b32 v19,16,v6
v_mov_b32 v34,0x3f80
v_mfma_f32_16x16x32_bf16 a[212:215],a[112:115],v[80:83],a[212:215]
s_cmp_ge_u32 0,s71
s_cselect_b32 s76,3,1
s_lshl_b32 s76,s76,16
v_mfma_f32_16x16x32_bf16 a[216:219],a[112:115],v[96:99],a[216:219]
s_add_u32 s77,s76,4096
buffer_load_dwordx4 v[220:223],v6,s[28:31],s78 offen offset:3072 sc1
v_mfma_f32_16x16x32_bf16 a[216:219],a[116:119],v[100:103],a[216:219]
s_add_u32 s78,s76,8192
s_max_i32 s77,s77,s19
s_max_i32 s78,s78,s100
v_mfma_f32_16x16x32_bf16 a[216:219],a[120:123],v[104:107],a[216:219]
v_lshlrev_b32 v19,v19,v34
v_lshrrev_b32 v18,1,v18
v_mfma_f32_16x16x32_bf16 a[216:219],a[124:127],v[108:111],a[216:219]
v_cmp_eq_u32 vcc,0,v18
s_nop 1
v_cndmask_b32 v34,0,v19,vcc
v_mfma_f32_16x16x32_bf16 a[220:223],a[124:127],v[124:127],a[220:223]
ds_read_b64_tr_b16 a[124:125],v2 offset:30944
ds_read_b64_tr_b16 a[126:127],v2 offset:36064
v_mfma_f32_16x16x32_bf16 a[220:223],a[120:123],v[120:123],a[220:223]
ds_read_b64_tr_b16 a[120:121],v2 offset:20704
v_add_u32 v25,4096,v6
v_mfma_f32_16x16x32_bf16 a[220:223],a[116:119],v[116:119],a[220:223]
buffer_load_dwordx4 v[232:235],v6,s[28:31],s79 offen sc1
v_mfma_f32_16x16x32_bf16 a[220:223],a[112:115],v[112:115],a[220:223]
s_waitcnt lgkmcnt(3)
ds_read_b64_tr_b16 a[112:113],v2 offset:224
ds_read_b64_tr_b16 a[114:115],v2 offset:5344
v_mfma_f32_16x16x32_bf16 a[224:227],a[128:131],v[64:67],a[224:227]
ds_read_b64_tr_b16 a[116:117],v2 offset:10464
v_mov_b32 v36,0
v_mfma_f32_16x16x32_bf16 a[224:227],a[132:135],v[68:71],a[224:227]
ds_read_b64_tr_b16 a[118:119],v2 offset:15584
v_mov_b32 v37,0
v_mfma_f32_16x16x32_bf16 a[224:227],a[136:139],v[72:75],a[224:227]
ds_read_b64_tr_b16 a[122:123],v2 offset:25824
v_mov_b32 v32,0
v_mfma_f32_16x16x32_bf16 a[224:227],a[140:143],v[76:79],a[224:227]
v_cmp_eq_u32 vcc,1,v18
s_nop 1
v_cndmask_b32 v35,0,v19,vcc
v_mfma_f32_16x16x32_bf16 a[228:231],a[140:143],v[92:95],a[228:231]
buffer_load_dwordx4 v[236:239],v6,s[28:31],s79 offen offset:1024 sc1
v_mfma_f32_16x16x32_bf16 a[228:231],a[136:139],v[88:91],a[228:231]
s_add_u32 s20,s20,s44
s_addc_u32 s21,s21,0
v_mov_b32 v33,0
v_mfma_f32_16x16x32_bf16 a[228:231],a[132:135],v[84:87],a[228:231]
s_mul_i32 s44,s71,320
v_bfe_u32 v40,v6,4,4
v_mul_u32_u24 v40,1280,v40
v_mfma_f32_16x16x32_bf16 a[228:231],a[128:131],v[80:83],a[228:231]
s_waitcnt vmcnt(9)
v_cmp_ne_u32 s[90:91],s68,v26
v_cmp_ne_u32 s[92:93],s69,v27
s_nop 0
s_or_b64 s[90:91],s[90:91],s[92:93]
s_cbranch_scc1 L_fr9_%=
L_fo9_%=:
v_mfma_f32_16x16x32_bf16 a[232:235],a[128:131],v[96:99],a[232:235]
s_and_b32 s21,s21,0xffff
v_lshrrev_b32 v41,8,v6
v_lshl_add_u32 v40,v41,3,v40
v_mfma_f32_16x16x32_bf16 a[232:235],a[132:135],v[100:103],a[232:235]
s_add_u32 s43,s43,s44
v_mfma_f32_16x16x32_bf16 a[232:235],a[136:139],v[104:107],a[232:235]
buffer_load_dwordx4 v[248:251],v6,s[28:31],s79 offen offset:2048 sc1
v_mfma_f32_16x16x32_bf16 a[232:235],a[140:143],v[108:111],a[232:235]
v_mfma_f32_16x16x32_bf16 a[236:239],a[140:143],v[124:127],a[236:239]
ds_read_b64_tr_b16 a[140:141],v1 offset:30976
ds_read_b64_tr_b16 a[142:143],v1 offset:36096
v_mfma_f32_16x16x32_bf16 a[236:239],a[136:139],v[120:123],a[236:239]
ds_read_b64_tr_b16 a[136:137],v1 offset:20736
v_mfma_f32_16x16x32_bf16 a[236:239],a[132:135],v[116:119],a[236:239]
ds_read_b64_tr_b16 a[132:133],v1 offset:10496
v_mfma_f32_16x16x32_bf16 a[236:239],a[128:131],v[112:115],a[236:239]
ds_read_b64_tr_b16 a[128:129],v1 offset:256
v_mfma_f32_16x16x32_bf16 a[240:243],a[144:147],v[64:67],a[240:243]
buffer_load_dwordx4 v[252:255],v6,s[28:31],s79 offen offset:3072 sc1
v_mfma_f32_16x16x32_bf16 a[240:243],a[148:151],v[68:71],a[240:243]
s_add_u32 s79,s76,12288
s_max_i32 s76,s76,s18
ds_read_b64_tr_b16 a[130:131],v1 offset:5376
v_mfma_f32_16x16x32_bf16 a[240:243],a[152:155],v[72:75],a[240:243]
s_max_i32 s79,s79,s101
ds_read_b64_tr_b16 a[134:135],v1 offset:15616
ds_read_b64_tr_b16 a[138:139],v1 offset:25856
v_mfma_f32_16x16x32_bf16 a[240:243],a[156:159],v[76:79],a[240:243]
v_subrev_u32 v1,40960,v1
v_mfma_f32_16x16x32_bf16 a[244:247],a[156:159],v[92:95],a[244:247]
buffer_load_dwordx4 v[128:131],v6,s[28:31],s76 offen sc1
v_mfma_f32_16x16x32_bf16 a[244:247],a[152:155],v[88:91],a[244:247]
v_mfma_f32_16x16x32_bf16 a[244:247],a[148:151],v[84:87],a[244:247]
v_mfma_f32_16x16x32_bf16 a[244:247],a[144:147],v[80:83],a[244:247]
v_mfma_f32_16x16x32_bf16 a[248:251],a[144:147],v[96:99],a[248:251]
buffer_load_dwordx4 v[160:163],v6,s[28:31],s76 offen offset:1024 sc1
v_mfma_f32_16x16x32_bf16 a[248:251],a[148:151],v[100:103],a[248:251]
v_mfma_f32_16x16x32_bf16 a[248:251],a[152:155],v[104:107],a[248:251]
v_mfma_f32_16x16x32_bf16 a[248:251],a[156:159],v[108:111],a[248:251]
v_mfma_f32_16x16x32_bf16 a[252:255],a[156:159],v[124:127],a[252:255]
buffer_load_dwordx4 v[192:195],v6,s[28:31],s76 offen offset:2048 sc1
v_mfma_f32_16x16x32_bf16 a[252:255],a[152:155],v[120:123],a[252:255]
ds_read_b64_tr_b16 a[152:153],v2 offset:20768
ds_read_b64_tr_b16 a[154:155],v2 offset:25888
v_mfma_f32_16x16x32_bf16 a[252:255],a[148:151],v[116:119],a[252:255]
ds_read_b64_tr_b16 a[148:149],v2 offset:10528
v_mfma_f32_16x16x32_bf16 a[252:255],a[144:147],v[112:115],a[252:255]
ds_read_b64_tr_b16 a[144:145],v2 offset:288
v_mfma_f32_16x16x32_bf16 a[0:3],a[160:163],v[64:67],a[0:3]
ds_read_b64_tr_b16 a[146:147],v2 offset:5408
v_mfma_f32_16x16x32_bf16 a[0:3],a[164:167],v[68:71],a[0:3]
buffer_load_dwordx4 v[224:227],v6,s[28:31],s76 offen offset:3072 sc1
v_mfma_f32_16x16x32_bf16 a[0:3],a[168:171],v[72:75],a[0:3]
ds_read_b64_tr_b16 a[150:151],v2 offset:15648
ds_read_b64_tr_b16 a[156:157],v2 offset:31008
v_mfma_f32_16x16x32_bf16 a[0:3],a[172:175],v[76:79],a[0:3]
ds_read_b64_tr_b16 a[158:159],v2 offset:36128
v_subrev_u32 v2,40960,v2
v_mfma_f32_16x16x32_bf16 a[4:7],a[172:175],v[92:95],a[4:7]
v_mfma_f32_16x16x32_bf16 a[4:7],a[168:171],v[88:91],a[4:7]
v_mfma_f32_16x16x32_bf16 a[4:7],a[164:167],v[84:87],a[4:7]
v_mfma_f32_16x16x32_bf16 a[4:7],a[160:163],v[80:83],a[4:7]
buffer_load_dwordx4 v[132:135],v6,s[28:31],s77 offen sc1
v_mfma_f32_16x16x32_bf16 a[8:11],a[160:163],v[96:99],a[8:11]
v_mfma_f32_16x16x32_bf16 a[8:11],a[164:167],v[100:103],a[8:11]
v_mfma_f32_16x16x32_bf16 a[8:11],a[168:171],v[104:107],a[8:11]
v_mfma_f32_16x16x32_bf16 a[8:11],a[172:175],v[108:111],a[8:11]
v_mfma_f32_16x16x32_bf16 a[12:15],a[172:175],v[124:127],a[12:15]
ds_read_b64_tr_b16 a[172:173],v1 offset:30720
ds_read_b64_tr_b16 a[174:175],v1 offset:35840
v_mfma_f32_16x16x32_bf16 a[12:15],a[168:171],v[120:123],a[12:15]
buffer_load_dwordx4 v[164:167],v6,s[28:31],s77 offen offset:1024 sc1
v_mfma_f32_16x16x32_bf16 a[12:15],a[164:167],v[116:119],a[12:15]
ds_read_b64_tr_b16 a[164:165],v1 offset:10240
ds_read_b64_tr_b16 a[166:167],v1 offset:15360
v_mfma_f32_16x16x32_bf16 a[12:15],a[160:163],v[112:115],a[12:15]
ds_read_b64_tr_b16 a[160:161],v1
v_mfma_f32_16x16x32_bf16 a[16:19],a[176:179],v[64:67],a[16:19]
ds_read_b64_tr_b16 a[162:163],v1 offset:5120
v_mfma_f32_16x16x32_bf16 a[16:19],a[180:183],v[68:71],a[16:19]
ds_read_b64_tr_b16 a[168:169],v1 offset:20480
v_mfma_f32_16x16x32_bf16 a[16:19],a[184:187],v[72:75],a[16:19]
ds_read_b64_tr_b16 a[170:171],v1 offset:25600
v_mfma_f32_16x16x32_bf16 a[16:19],a[188:191],v[76:79],a[16:19]
buffer_load_dwordx4 v[196:199],v6,s[28:31],s77 offen offset:2048 sc1
v_mfma_f32_16x16x32_bf16 a[20:23],a[188:191],v[92:95],a[20:23]
v_mfma_f32_16x16x32_bf16 a[20:23],a[184:187],v[88:91],a[20:23]
v_mfma_f32_16x16x32_bf16 a[20:23],a[180:183],v[84:87],a[20:23]
v_mfma_f32_16x16x32_bf16 a[20:23],a[176:179],v[80:83],a[20:23]
v_mfma_f32_16x16x32_bf16 a[24:27],a[176:179],v[96:99],a[24:27]
v_mfma_f32_16x16x32_bf16 a[24:27],a[180:183],v[100:103],a[24:27]
buffer_load_dwordx4 v[228:231],v6,s[28:31],s77 offen offset:3072 sc1
v_mfma_f32_16x16x32_bf16 a[24:27],a[184:187],v[104:107],a[24:27]
v_mfma_f32_16x16x32_bf16 a[24:27],a[188:191],v[108:111],a[24:27]
v_mfma_f32_16x16x32_bf16 a[28:31],a[188:191],v[124:127],a[28:31]
ds_read_b64_tr_b16 a[188:189],v2 offset:30752
ds_read_b64_tr_b16 a[190:191],v2 offset:35872
v_mfma_f32_16x16x32_bf16 a[28:31],a[184:187],v[120:123],a[28:31]
ds_read_b64_tr_b16 a[184:185],v2 offset:20512
v_mfma_f32_16x16x32_bf16 a[28:31],a[180:183],v[116:119],a[28:31]
ds_read_b64_tr_b16 a[180:181],v2 offset:10272
v_mfma_f32_16x16x32_bf16 a[28:31],a[176:179],v[112:115],a[28:31]
buffer_load_dwordx4 v[144:147],v6,s[28:31],s78 offen sc1
v_mfma_f32_16x16x32_bf16 a[32:35],a[96:99],v[64:67],a[32:35]
ds_read_b64_tr_b16 a[176:177],v2 offset:32
ds_read_b64_tr_b16 a[178:179],v2 offset:5152
v_mfma_f32_16x16x32_bf16 a[32:35],a[100:103],v[68:71],a[32:35]
ds_read_b64_tr_b16 a[182:183],v2 offset:15392
v_mfma_f32_16x16x32_bf16 a[32:35],a[104:107],v[72:75],a[32:35]
ds_read_b64_tr_b16 a[186:187],v2 offset:25632
v_mfma_f32_16x16x32_bf16 a[32:35],a[108:111],v[76:79],a[32:35]
v_mfma_f32_16x16x32_bf16 a[36:39],a[108:111],v[92:95],a[36:39]
v_mfma_f32_16x16x32_bf16 a[36:39],a[104:107],v[88:91],a[36:39]
buffer_load_dwordx4 v[176:179],v6,s[28:31],s78 offen offset:1024 sc1
v_mfma_f32_16x16x32_bf16 a[36:39],a[100:103],v[84:87],a[36:39]
v_mfma_f32_16x16x32_bf16 a[36:39],a[96:99],v[80:83],a[36:39]
v_mfma_f32_16x16x32_bf16 a[40:43],a[96:99],v[96:99],a[40:43]
v_mfma_f32_16x16x32_bf16 a[40:43],a[100:103],v[100:103],a[40:43]
v_mfma_f32_16x16x32_bf16 a[40:43],a[104:107],v[104:107],a[40:43]
v_mfma_f32_16x16x32_bf16 a[40:43],a[108:111],v[108:111],a[40:43]
)A"
R"A(buffer_load_dwordx4 v[208:211],v6,s[28:31],s78 offen offset:2048 sc1
v_mfma_f32_16x16x32_bf16 a[44:47],a[108:111],v[124:127],a[44:47]
ds_read_b64_tr_b16 a[108:109],v1 offset:30784
ds_read_b64_tr_b16 a[110:111],v1 offset:35904
v_mfma_f32_16x16x32_bf16 a[44:47],a[104:107],v[120:123],a[44:47]
ds_read_b64_tr_b16 a[104:105],v1 offset:20544
v_mfma_f32_16x16x32_bf16 a[44:47],a[100:103],v[116:119],a[44:47]
ds_read_b64_tr_b16 a[100:101],v1 offset:10304
v_mfma_f32_16x16x32_bf16 a[44:47],a[96:99],v[112:115],a[44:47]
s_waitcnt lgkmcnt(4)
ds_read_b64_tr_b16 a[96:97],v1 offset:64
v_mfma_f32_16x16x32_bf16 a[48:51],a[112:115],v[64:67],a[48:51]
ds_read_b64_tr_b16 a[98:99],v1 offset:5184
ds_read_b64_tr_b16 a[102:103],v1 offset:15424
v_mfma_f32_16x16x32_bf16 a[48:51],a[116:119],v[68:71],a[48:51]
buffer_load_dwordx4 v[240:243],v6,s[28:31],s78 offen offset:3072 sc1
v_mfma_f32_16x16x32_bf16 a[48:51],a[120:123],v[72:75],a[48:51]
ds_read_b64_tr_b16 a[106:107],v1 offset:25664
v_mfma_f32_16x16x32_bf16 a[48:51],a[124:127],v[76:79],a[48:51]
v_mfma_f32_16x16x32_bf16 a[52:55],a[124:127],v[92:95],a[52:55]
v_mfma_f32_16x16x32_bf16 a[52:55],a[120:123],v[88:91],a[52:55]
v_mfma_f32_16x16x32_bf16 a[52:55],a[116:119],v[84:87],a[52:55]
v_mfma_f32_16x16x32_bf16 a[52:55],a[112:115],v[80:83],a[52:55]
buffer_load_dwordx4 v[148:151],v6,s[28:31],s79 offen sc1
v_mfma_f32_16x16x32_bf16 a[56:59],a[112:115],v[96:99],a[56:59]
v_mfma_f32_16x16x32_bf16 a[56:59],a[116:119],v[100:103],a[56:59]
s_cmp_ge_u32 1,s71
s_cselect_b32 s80,5,3
s_mul_i32 s80,s80,163840
v_mfma_f32_16x16x32_bf16 a[56:59],a[120:123],v[104:107],a[56:59]
s_add_u32 s80,s80,s81
s_add_u32 s83,s80,20480
v_mfma_f32_16x16x32_bf16 a[56:59],a[124:127],v[108:111],a[56:59]
v_mfma_f32_16x16x32_bf16 a[60:63],a[124:127],v[124:127],a[60:63]
ds_read_b64_tr_b16 a[124:125],v2 offset:30816
ds_read_b64_tr_b16 a[126:127],v2 offset:35936
v_mfma_f32_16x16x32_bf16 a[60:63],a[120:123],v[120:123],a[60:63]
buffer_load_dwordx4 v[180:183],v6,s[28:31],s79 offen offset:1024 sc1
v_mfma_f32_16x16x32_bf16 a[60:63],a[116:119],v[116:119],a[60:63]
ds_read_b64_tr_b16 a[116:117],v2 offset:10336
ds_read_b64_tr_b16 a[118:119],v2 offset:15456
v_mfma_f32_16x16x32_bf16 a[60:63],a[112:115],v[112:115],a[60:63]
ds_read_b64_tr_b16 a[112:113],v2 offset:96
v_mfma_f32_16x16x32_bf16 a[64:67],a[128:131],v[64:67],a[64:67]
ds_read_b64_tr_b16 a[114:115],v2 offset:5216
v_mfma_f32_16x16x32_bf16 a[64:67],a[132:135],v[68:71],a[64:67]
ds_read_b64_tr_b16 a[120:121],v2 offset:20576
v_mfma_f32_16x16x32_bf16 a[64:67],a[136:139],v[72:75],a[64:67]
ds_read_b64_tr_b16 a[122:123],v2 offset:25696
v_mfma_f32_16x16x32_bf16 a[64:67],a[140:143],v[76:79],a[64:67]
buffer_load_dwordx4 v[212:215],v6,s[28:31],s79 offen offset:2048 sc1
v_mfma_f32_16x16x32_bf16 a[68:71],a[140:143],v[92:95],a[68:71]
v_mfma_f32_16x16x32_bf16 a[68:71],a[136:139],v[88:91],a[68:71]
v_mfma_f32_16x16x32_bf16 a[68:71],a[132:135],v[84:87],a[68:71]
v_mfma_f32_16x16x32_bf16 a[68:71],a[128:131],v[80:83],a[68:71]
v_mfma_f32_16x16x32_bf16 a[72:75],a[128:131],v[96:99],a[72:75]
v_mfma_f32_16x16x32_bf16 a[72:75],a[132:135],v[100:103],a[72:75]
buffer_load_dwordx4 v[244:247],v6,s[28:31],s79 offen offset:3072 sc1
v_mfma_f32_16x16x32_bf16 a[72:75],a[136:139],v[104:107],a[72:75]
v_mfma_f32_16x16x32_bf16 a[72:75],a[140:143],v[108:111],a[72:75]
v_mfma_f32_16x16x32_bf16 a[76:79],a[140:143],v[124:127],a[76:79]
ds_read_b64_tr_b16 a[140:141],v1 offset:30848
ds_read_b64_tr_b16 a[142:143],v1 offset:35968
v_mfma_f32_16x16x32_bf16 a[76:79],a[136:139],v[120:123],a[76:79]
ds_read_b64_tr_b16 a[136:137],v1 offset:20608
v_mfma_f32_16x16x32_bf16 a[76:79],a[132:135],v[116:119],a[76:79]
s_add_u32 m0,s82,40960
ds_read_b64_tr_b16 a[132:133],v1 offset:10368
v_mfma_f32_16x16x32_bf16 a[76:79],a[128:131],v[112:115],a[76:79]
ds_read_b64_tr_b16 a[128:129],v1 offset:128
v_mfma_f32_16x16x32_bf16 a[80:83],a[144:147],v[64:67],a[80:83]
ds_read_b64_tr_b16 a[130:131],v1 offset:5248
ds_read_b64_tr_b16 a[134:135],v1 offset:15488
v_mfma_f32_16x16x32_bf16 a[80:83],a[148:151],v[68:71],a[80:83]
buffer_load_dwordx4 v7,s[72:75],s80 offen lds
v_mfma_f32_16x16x32_bf16 a[80:83],a[152:155],v[72:75],a[80:83]
ds_read_b64_tr_b16 a[138:139],v1 offset:25728
v_mfma_f32_16x16x32_bf16 a[80:83],a[156:159],v[76:79],a[80:83]
v_mfma_f32_16x16x32_bf16 a[84:87],a[156:159],v[92:95],a[84:87]
v_mfma_f32_16x16x32_bf16 a[84:87],a[152:155],v[88:91],a[84:87]
v_mfma_f32_16x16x32_bf16 a[84:87],a[148:151],v[84:87],a[84:87]
buffer_load_dwordx4 v8,s[72:75],s80 offen offset:1024 lds
v_mfma_f32_16x16x32_bf16 a[84:87],a[144:147],v[80:83],a[84:87]
v_mfma_f32_16x16x32_bf16 a[88:91],a[144:147],v[96:99],a[88:91]
v_mfma_f32_16x16x32_bf16 a[88:91],a[148:151],v[100:103],a[88:91]
v_mfma_f32_16x16x32_bf16 a[88:91],a[152:155],v[104:107],a[88:91]
v_mfma_f32_16x16x32_bf16 a[88:91],a[156:159],v[108:111],a[88:91]
buffer_load_dwordx4 v9,s[72:75],s80 offen offset:2048 lds
v_mfma_f32_16x16x32_bf16 a[92:95],a[156:159],v[124:127],a[92:95]
ds_read_b64_tr_b16 a[156:157],v2 offset:30880
ds_read_b64_tr_b16 a[158:159],v2 offset:36000
v_mfma_f32_16x16x32_bf16 a[92:95],a[152:155],v[120:123],a[92:95]
ds_read_b64_tr_b16 a[152:153],v2 offset:20640
v_mfma_f32_16x16x32_bf16 a[92:95],a[148:151],v[116:119],a[92:95]
ds_read_b64_tr_b16 a[148:149],v2 offset:10400
v_mfma_f32_16x16x32_bf16 a[92:95],a[144:147],v[112:115],a[92:95]
ds_read_b64_tr_b16 a[144:145],v2 offset:160
v_mfma_f32_16x16x32_bf16 a[192:195],a[160:163],v[136:139],a[192:195]
buffer_load_dwordx4 v10,s[72:75],s80 offen offset:3072 lds
v_mfma_f32_16x16x32_bf16 a[192:195],a[164:167],v[140:143],a[192:195]
ds_read_b64_tr_b16 a[146:147],v2 offset:5280
ds_read_b64_tr_b16 a[150:151],v2 offset:15520
v_mfma_f32_16x16x32_bf16 a[192:195],a[168:171],v[152:155],a[192:195]
s_add_u32 m0,s82,45056
ds_read_b64_tr_b16 a[154:155],v2 offset:25760
v_mfma_f32_16x16x32_bf16 a[192:195],a[172:175],v[156:159],a[192:195]
s_waitcnt vmcnt(12)
v_mfma_f32_16x16x32_bf16 a[196:199],a[172:175],v[188:191],a[196:199]
v_mfma_f32_16x16x32_bf16 a[196:199],a[168:171],v[184:187],a[196:199]
buffer_load_dwordx4 v11,s[72:75],s80 offen lds
v_mfma_f32_16x16x32_bf16 a[196:199],a[164:167],v[172:175],a[196:199]
v_mfma_f32_16x16x32_bf16 a[196:199],a[160:163],v[168:171],a[196:199]
s_add_u32 m0,s82,46080
v_mfma_f32_16x16x32_bf16 a[200:203],a[160:163],v[200:203],a[200:203]
v_mfma_f32_16x16x32_bf16 a[200:203],a[164:167],v[204:207],a[200:203]
v_mfma_f32_16x16x32_bf16 a[200:203],a[168:171],v[216:219],a[200:203]
buffer_load_dwordx4 v7,s[72:75],s83 offen lds
v_mfma_f32_16x16x32_bf16 a[200:203],a[172:175],v[220:223],a[200:203]
v_mfma_f32_16x16x32_bf16 a[204:207],a[172:175],v[252:255],a[204:207]
ds_read_b64_tr_b16 a[172:173],v1 offset:30912
ds_read_b64_tr_b16 a[174:175],v1 offset:36032
v_mfma_f32_16x16x32_bf16 a[204:207],a[168:171],v[248:251],a[204:207]
ds_read_b64_tr_b16 a[168:169],v1 offset:20672
v_mfma_f32_16x16x32_bf16 a[204:207],a[164:167],v[236:239],a[204:207]
ds_read_b64_tr_b16 a[164:165],v1 offset:10432
v_mfma_f32_16x16x32_bf16 a[204:207],a[160:163],v[232:235],a[204:207]
s_barrier
buffer_load_dwordx4 v8,s[72:75],s83 offen offset:1024 lds
v_mfma_f32_16x16x32_bf16 a[208:211],a[176:179],v[136:139],a[208:211]
s_cmp_ge_u32 1,s71
s_cselect_b32 s76,5,3
s_lshl_b32 s76,s76,16
v_mfma_f32_16x16x32_bf16 a[208:211],a[180:183],v[140:143],a[208:211]
s_add_u32 s77,s76,4096
s_add_u32 s78,s76,8192
s_add_u32 s79,s76,12288
v_mfma_f32_16x16x32_bf16 a[208:211],a[184:187],v[152:155],a[208:211]
s_max_i32 s76,s76,s18
s_max_i32 s77,s77,s19
s_max_i32 s78,s78,s100
v_mfma_f32_16x16x32_bf16 a[208:211],a[188:191],v[156:159],a[208:211]
s_max_i32 s79,s79,s101
buffer_load_dwordx4 v9,s[72:75],s83 offen offset:2048 lds
v_mfma_f32_16x16x32_bf16 a[212:215],a[188:191],v[188:191],a[212:215]
ds_read_b64_tr_b16 a[160:161],v1 offset:192
ds_read_b64_tr_b16 a[162:163],v1 offset:5312
v_mfma_f32_16x16x32_bf16 a[212:215],a[184:187],v[184:187],a[212:215]
ds_read_b64_tr_b16 a[166:167],v1 offset:15552
v_mfma_f32_16x16x32_bf16 a[212:215],a[180:183],v[172:175],a[212:215]
ds_read_b64_tr_b16 a[170:171],v1 offset:25792
v_mfma_f32_16x16x32_bf16 a[212:215],a[176:179],v[168:171],a[212:215]
v_mfma_f32_16x16x32_bf16 a[216:219],a[176:179],v[200:203],a[216:219]
buffer_load_dwordx4 v10,s[72:75],s83 offen offset:3072 lds
v_mfma_f32_16x16x32_bf16 a[216:219],a[180:183],v[204:207],a[216:219]
v_mfma_f32_16x16x32_bf16 a[216:219],a[184:187],v[216:219],a[216:219]
s_add_u32 m0,s82,50176
v_mfma_f32_16x16x32_bf16 a[216:219],a[188:191],v[220:223],a[216:219]
v_mfma_f32_16x16x32_bf16 a[220:223],a[188:191],v[252:255],a[220:223]
ds_read_b64_tr_b16 a[188:189],v2 offset:30944
ds_read_b64_tr_b16 a[190:191],v2 offset:36064
v_mfma_f32_16x16x32_bf16 a[220:223],a[184:187],v[248:251],a[220:223]
buffer_load_dwordx4 v11,s[72:75],s83 offen lds
v_mfma_f32_16x16x32_bf16 a[220:223],a[180:183],v[236:239],a[220:223]
ds_read_b64_tr_b16 a[180:181],v2 offset:10464
ds_read_b64_tr_b16 a[182:183],v2 offset:15584
v_mfma_f32_16x16x32_bf16 a[220:223],a[176:179],v[232:235],a[220:223]
s_waitcnt lgkmcnt(4)
ds_read_b64_tr_b16 a[176:177],v2 offset:224
v_mfma_f32_16x16x32_bf16 a[224:227],a[96:99],v[136:139],a[224:227]
ds_read_b64_tr_b16 a[178:179],v2 offset:5344
v_mfma_f32_16x16x32_bf16 a[224:227],a[100:103],v[140:143],a[224:227]
ds_read_b64_tr_b16 a[184:185],v2 offset:20704
v_mfma_f32_16x16x32_bf16 a[224:227],a[104:107],v[152:155],a[224:227]
ds_read_b64_tr_b16 a[186:187],v2 offset:25824
v_mfma_f32_16x16x32_bf16 a[224:227],a[108:111],v[156:159],a[224:227]
v_mfma_f32_16x16x32_bf16 a[228:231],a[108:111],v[188:191],a[228:231]
v_mfma_f32_16x16x32_bf16 a[228:231],a[104:107],v[184:187],a[228:231]
v_mfma_f32_16x16x32_bf16 a[228:231],a[100:103],v[172:175],a[228:231]
v_mfma_f32_16x16x32_bf16 a[228:231],a[96:99],v[168:171],a[228:231]
buffer_load_dwordx4 v[64:67],v6,s[28:31],s76 offen sc1
v_mfma_f32_16x16x32_bf16 a[232:235],a[96:99],v[200:203],a[232:235]
v_mfma_f32_16x16x32_bf16 a[232:235],a[100:103],v[204:207],a[232:235]
v_mfma_f32_16x16x32_bf16 a[232:235],a[104:107],v[216:219],a[232:235]
v_mfma_f32_16x16x32_bf16 a[232:235],a[108:111],v[220:223],a[232:235]
v_mfma_f32_16x16x32_bf16 a[236:239],a[108:111],v[252:255],a[236:239]
buffer_load_dwordx4 v[68:71],v6,s[28:31],s76 offen offset:1024 sc1
v_mfma_f32_16x16x32_bf16 a[236:239],a[104:107],v[248:251],a[236:239]
ds_read_b64_tr_b16 a[104:105],v1 offset:20736
ds_read_b64_tr_b16 a[106:107],v1 offset:25856
v_mfma_f32_16x16x32_bf16 a[236:239],a[100:103],v[236:239],a[236:239]
ds_read_b64_tr_b16 a[100:101],v1 offset:10496
v_mfma_f32_16x16x32_bf16 a[236:239],a[96:99],v[232:235],a[236:239]
ds_read_b64_tr_b16 a[96:97],v1 offset:256
v_mfma_f32_16x16x32_bf16 a[240:243],a[112:115],v[136:139],a[240:243]
ds_read_b64_tr_b16 a[98:99],v1 offset:5376
v_mfma_f32_16x16x32_bf16 a[240:243],a[116:119],v[140:143],a[240:243]
buffer_load_dwordx4 v[72:75],v6,s[28:31],s76 offen offset:2048 sc1
v_mfma_f32_16x16x32_bf16 a[240:243],a[120:123],v[152:155],a[240:243]
ds_read_b64_tr_b16 a[102:103],v1 offset:15616
ds_read_b64_tr_b16 a[108:109],v1 offset:30976
v_mfma_f32_16x16x32_bf16 a[240:243],a[124:127],v[156:159],a[240:243]
ds_read_b64_tr_b16 a[110:111],v1 offset:36096
v_subrev_u32 v1,81920,v1
v_mfma_f32_16x16x32_bf16 a[244:247],a[124:127],v[188:191],a[244:247]
v_add_u32 v42,40960,v1
v_add_u32 v44,122880,v1
v_mfma_f32_16x16x32_bf16 a[244:247],a[120:123],v[184:187],a[244:247]
v_mfma_f32_16x16x32_bf16 a[244:247],a[116:119],v[172:175],a[244:247]
buffer_load_dwordx4 v[76:79],v6,s[28:31],s76 offen offset:3072 sc1
v_mfma_f32_16x16x32_bf16 a[244:247],a[112:115],v[168:171],a[244:247]
v_mfma_f32_16x16x32_bf16 a[248:251],a[112:115],v[200:203],a[248:251]
v_mfma_f32_16x16x32_bf16 a[248:251],a[116:119],v[204:207],a[248:251]
v_mfma_f32_16x16x32_bf16 a[248:251],a[120:123],v[216:219],a[248:251]
v_mfma_f32_16x16x32_bf16 a[248:251],a[124:127],v[220:223],a[248:251]
buffer_load_dwordx4 v[80:83],v6,s[28:31],s77 offen sc1
v_mfma_f32_16x16x32_bf16 a[252:255],a[124:127],v[252:255],a[252:255]
ds_read_b64_tr_b16 a[124:125],v2 offset:31008
ds_read_b64_tr_b16 a[126:127],v2 offset:36128
v_mfma_f32_16x16x32_bf16 a[252:255],a[120:123],v[248:251],a[252:255]
)A"
R"A(ds_read_b64_tr_b16 a[120:121],v2 offset:20768
v_mfma_f32_16x16x32_bf16 a[252:255],a[116:119],v[236:239],a[252:255]
ds_read_b64_tr_b16 a[116:117],v2 offset:10528
v_mfma_f32_16x16x32_bf16 a[252:255],a[112:115],v[232:235],a[252:255]
ds_read_b64_tr_b16 a[112:113],v2 offset:288
v_mfma_f32_16x16x32_bf16 a[0:3],a[128:131],v[136:139],a[0:3]
buffer_load_dwordx4 v[84:87],v6,s[28:31],s77 offen offset:1024 sc1
v_mfma_f32_16x16x32_bf16 a[0:3],a[132:135],v[140:143],a[0:3]
ds_read_b64_tr_b16 a[114:115],v2 offset:5408
ds_read_b64_tr_b16 a[118:119],v2 offset:15648
v_mfma_f32_16x16x32_bf16 a[0:3],a[136:139],v[152:155],a[0:3]
ds_read_b64_tr_b16 a[122:123],v2 offset:25888
v_subrev_u32 v2,81920,v2
v_mfma_f32_16x16x32_bf16 a[0:3],a[140:143],v[156:159],a[0:3]
v_add_u32 v43,40960,v2
v_add_u32 v45,122880,v2
v_mfma_f32_16x16x32_bf16 a[4:7],a[140:143],v[188:191],a[4:7]
v_mfma_f32_16x16x32_bf16 a[4:7],a[136:139],v[184:187],a[4:7]
buffer_load_dwordx4 v[88:91],v6,s[28:31],s77 offen offset:2048 sc1
v_mfma_f32_16x16x32_bf16 a[4:7],a[132:135],v[172:175],a[4:7]
v_mfma_f32_16x16x32_bf16 a[4:7],a[128:131],v[168:171],a[4:7]
v_mfma_f32_16x16x32_bf16 a[8:11],a[128:131],v[200:203],a[8:11]
v_mfma_f32_16x16x32_bf16 a[8:11],a[132:135],v[204:207],a[8:11]
v_mfma_f32_16x16x32_bf16 a[8:11],a[136:139],v[216:219],a[8:11]
buffer_load_dwordx4 v[92:95],v6,s[28:31],s77 offen offset:3072 sc1
v_mfma_f32_16x16x32_bf16 a[8:11],a[140:143],v[220:223],a[8:11]
v_mfma_f32_16x16x32_bf16 a[12:15],a[140:143],v[252:255],a[12:15]
ds_read_b64_tr_b16 a[140:141],v1 offset:30720
ds_read_b64_tr_b16 a[142:143],v1 offset:35840
v_mfma_f32_16x16x32_bf16 a[12:15],a[136:139],v[248:251],a[12:15]
ds_read_b64_tr_b16 a[136:137],v1 offset:20480
v_mfma_f32_16x16x32_bf16 a[12:15],a[132:135],v[236:239],a[12:15]
ds_read_b64_tr_b16 a[132:133],v1 offset:10240
v_mfma_f32_16x16x32_bf16 a[12:15],a[128:131],v[232:235],a[12:15]
buffer_load_dwordx4 v[96:99],v6,s[28:31],s78 offen sc1
v_mfma_f32_16x16x32_bf16 a[16:19],a[144:147],v[136:139],a[16:19]
ds_read_b64_tr_b16 a[128:129],v1
ds_read_b64_tr_b16 a[130:131],v1 offset:5120
v_mfma_f32_16x16x32_bf16 a[16:19],a[148:151],v[140:143],a[16:19]
ds_read_b64_tr_b16 a[134:135],v1 offset:15360
v_mfma_f32_16x16x32_bf16 a[16:19],a[152:155],v[152:155],a[16:19]
ds_read_b64_tr_b16 a[138:139],v1 offset:25600
v_mfma_f32_16x16x32_bf16 a[16:19],a[156:159],v[156:159],a[16:19]
v_mfma_f32_16x16x32_bf16 a[20:23],a[156:159],v[188:191],a[20:23]
buffer_load_dwordx4 v[100:103],v6,s[28:31],s78 offen offset:1024 sc1
v_mfma_f32_16x16x32_bf16 a[20:23],a[152:155],v[184:187],a[20:23]
v_mfma_f32_16x16x32_bf16 a[20:23],a[148:151],v[172:175],a[20:23]
v_mfma_f32_16x16x32_bf16 a[20:23],a[144:147],v[168:171],a[20:23]
v_mfma_f32_16x16x32_bf16 a[24:27],a[144:147],v[200:203],a[24:27]
v_mfma_f32_16x16x32_bf16 a[24:27],a[148:151],v[204:207],a[24:27]
buffer_load_dwordx4 v[104:107],v6,s[28:31],s78 offen offset:2048 sc1
v_mfma_f32_16x16x32_bf16 a[24:27],a[152:155],v[216:219],a[24:27]
v_mfma_f32_16x16x32_bf16 a[24:27],a[156:159],v[220:223],a[24:27]
v_mfma_f32_16x16x32_bf16 a[28:31],a[156:159],v[252:255],a[28:31]
ds_read_b64_tr_b16 a[156:157],v2 offset:30752
ds_read_b64_tr_b16 a[158:159],v2 offset:35872
v_mfma_f32_16x16x32_bf16 a[28:31],a[152:155],v[248:251],a[28:31]
ds_read_b64_tr_b16 a[152:153],v2 offset:20512
v_mfma_f32_16x16x32_bf16 a[28:31],a[148:151],v[236:239],a[28:31]
buffer_load_dwordx4 v[108:111],v6,s[28:31],s78 offen offset:3072 sc1
v_mfma_f32_16x16x32_bf16 a[28:31],a[144:147],v[232:235],a[28:31]
s_cmp_ge_u32 2,s71
s_cselect_b32 s80,7,5
s_mul_i32 s80,s80,163840
v_mfma_f32_16x16x32_bf16 a[32:35],a[160:163],v[136:139],a[32:35]
s_add_u32 s80,s80,s81
s_add_u32 s83,s80,20480
ds_read_b64_tr_b16 a[144:145],v2 offset:32
v_mfma_f32_16x16x32_bf16 a[32:35],a[164:167],v[140:143],a[32:35]
ds_read_b64_tr_b16 a[146:147],v2 offset:5152
ds_read_b64_tr_b16 a[148:149],v2 offset:10272
v_mfma_f32_16x16x32_bf16 a[32:35],a[168:171],v[152:155],a[32:35]
ds_read_b64_tr_b16 a[150:151],v2 offset:15392
v_mfma_f32_16x16x32_bf16 a[32:35],a[172:175],v[156:159],a[32:35]
buffer_load_dwordx4 v[112:115],v6,s[28:31],s79 offen sc1
v_mfma_f32_16x16x32_bf16 a[36:39],a[172:175],v[188:191],a[36:39]
ds_read_b64_tr_b16 a[154:155],v2 offset:25632
v_mfma_f32_16x16x32_bf16 a[36:39],a[168:171],v[184:187],a[36:39]
v_mfma_f32_16x16x32_bf16 a[36:39],a[164:167],v[172:175],a[36:39]
v_mfma_f32_16x16x32_bf16 a[36:39],a[160:163],v[168:171],a[36:39]
v_mfma_f32_16x16x32_bf16 a[40:43],a[160:163],v[200:203],a[40:43]
buffer_load_dwordx4 v[116:119],v6,s[28:31],s79 offen offset:1024 sc1
v_mfma_f32_16x16x32_bf16 a[40:43],a[164:167],v[204:207],a[40:43]
v_mfma_f32_16x16x32_bf16 a[40:43],a[168:171],v[216:219],a[40:43]
v_mfma_f32_16x16x32_bf16 a[40:43],a[172:175],v[220:223],a[40:43]
v_mfma_f32_16x16x32_bf16 a[44:47],a[172:175],v[252:255],a[44:47]
ds_read_b64_tr_b16 a[172:173],v1 offset:30784
ds_read_b64_tr_b16 a[174:175],v1 offset:35904
v_mfma_f32_16x16x32_bf16 a[44:47],a[168:171],v[248:251],a[44:47]
buffer_load_dwordx4 v[120:123],v6,s[28:31],s79 offen offset:2048 sc1
v_mfma_f32_16x16x32_bf16 a[44:47],a[164:167],v[236:239],a[44:47]
ds_read_b64_tr_b16 a[164:165],v1 offset:10304
ds_read_b64_tr_b16 a[166:167],v1 offset:15424
v_mfma_f32_16x16x32_bf16 a[44:47],a[160:163],v[232:235],a[44:47]
s_waitcnt lgkmcnt(4)
ds_read_b64_tr_b16 a[160:161],v1 offset:64
v_mfma_f32_16x16x32_bf16 a[48:51],a[176:179],v[136:139],a[48:51]
ds_read_b64_tr_b16 a[162:163],v1 offset:5184
v_mfma_f32_16x16x32_bf16 a[48:51],a[180:183],v[140:143],a[48:51]
ds_read_b64_tr_b16 a[168:169],v1 offset:20544
v_mfma_f32_16x16x32_bf16 a[48:51],a[184:187],v[152:155],a[48:51]
buffer_load_dwordx4 v[124:127],v6,s[28:31],s79 offen offset:3072 sc1
v_mfma_f32_16x16x32_bf16 a[48:51],a[188:191],v[156:159],a[48:51]
ds_read_b64_tr_b16 a[170:171],v1 offset:25664
v_mfma_f32_16x16x32_bf16 a[52:55],a[188:191],v[188:191],a[52:55]
s_add_u32 m0,s82,122880
v_mfma_f32_16x16x32_bf16 a[52:55],a[184:187],v[184:187],a[52:55]
v_mfma_f32_16x16x32_bf16 a[52:55],a[180:183],v[172:175],a[52:55]
v_mfma_f32_16x16x32_bf16 a[52:55],a[176:179],v[168:171],a[52:55]
buffer_load_dwordx4 v7,s[72:75],s80 offen lds
v_mfma_f32_16x16x32_bf16 a[56:59],a[176:179],v[200:203],a[56:59]
v_mfma_f32_16x16x32_bf16 a[56:59],a[180:183],v[204:207],a[56:59]
v_mfma_f32_16x16x32_bf16 a[56:59],a[184:187],v[216:219],a[56:59]
v_mfma_f32_16x16x32_bf16 a[56:59],a[188:191],v[220:223],a[56:59]
v_mfma_f32_16x16x32_bf16 a[60:63],a[188:191],v[252:255],a[60:63]
buffer_load_dwordx4 v8,s[72:75],s80 offen offset:1024 lds
v_mfma_f32_16x16x32_bf16 a[60:63],a[184:187],v[248:251],a[60:63]
ds_read_b64_tr_b16 a[184:185],v2 offset:20576
ds_read_b64_tr_b16 a[186:187],v2 offset:25696
v_mfma_f32_16x16x32_bf16 a[60:63],a[180:183],v[236:239],a[60:63]
ds_read_b64_tr_b16 a[180:181],v2 offset:10336
v_mfma_f32_16x16x32_bf16 a[60:63],a[176:179],v[232:235],a[60:63]
ds_read_b64_tr_b16 a[176:177],v2 offset:96
v_mfma_f32_16x16x32_bf16 a[64:67],a[96:99],v[136:139],a[64:67]
ds_read_b64_tr_b16 a[178:179],v2 offset:5216
v_mfma_f32_16x16x32_bf16 a[64:67],a[100:103],v[140:143],a[64:67]
buffer_load_dwordx4 v9,s[72:75],s80 offen offset:2048 lds
v_mfma_f32_16x16x32_bf16 a[64:67],a[104:107],v[152:155],a[64:67]
ds_read_b64_tr_b16 a[182:183],v2 offset:15456
ds_read_b64_tr_b16 a[188:189],v2 offset:30816
v_mfma_f32_16x16x32_bf16 a[64:67],a[108:111],v[156:159],a[64:67]
ds_read_b64_tr_b16 a[190:191],v2 offset:35936
v_mfma_f32_16x16x32_bf16 a[68:71],a[108:111],v[188:191],a[68:71]
v_mfma_f32_16x16x32_bf16 a[68:71],a[104:107],v[184:187],a[68:71]
v_mfma_f32_16x16x32_bf16 a[68:71],a[100:103],v[172:175],a[68:71]
buffer_load_dwordx4 v10,s[72:75],s80 offen offset:3072 lds
v_mfma_f32_16x16x32_bf16 a[68:71],a[96:99],v[168:171],a[68:71]
v_mfma_f32_16x16x32_bf16 a[72:75],a[96:99],v[200:203],a[72:75]
s_add_u32 m0,s82,126976
v_mfma_f32_16x16x32_bf16 a[72:75],a[100:103],v[204:207],a[72:75]
v_mfma_f32_16x16x32_bf16 a[72:75],a[104:107],v[216:219],a[72:75]
s_cmp_ge_u32 2,s71
s_cselect_b32 s76,7,5
s_lshl_b32 s76,s76,16
v_mfma_f32_16x16x32_bf16 a[72:75],a[108:111],v[220:223],a[72:75]
s_add_u32 s77,s76,4096
buffer_load_dwordx4 v11,s[72:75],s80 offen lds
v_mfma_f32_16x16x32_bf16 a[76:79],a[108:111],v[252:255],a[76:79]
s_add_u32 s78,s76,8192
s_add_u32 s79,s76,12288
s_max_i32 s76,s76,s18
v_mfma_f32_16x16x32_bf16 a[76:79],a[104:107],v[248:251],a[76:79]
s_add_u32 m0,s82,128000
s_max_i32 s77,s77,s19
ds_read_b64_tr_b16 a[104:105],v1 offset:20608
v_mfma_f32_16x16x32_bf16 a[76:79],a[100:103],v[236:239],a[76:79]
s_max_i32 s78,s78,s100
ds_read_b64_tr_b16 a[100:101],v1 offset:10368
ds_read_b64_tr_b16 a[102:103],v1 offset:15488
v_mfma_f32_16x16x32_bf16 a[76:79],a[96:99],v[232:235],a[76:79]
s_max_i32 s79,s79,s101
ds_read_b64_tr_b16 a[96:97],v1 offset:128
v_mfma_f32_16x16x32_bf16 a[80:83],a[112:115],v[136:139],a[80:83]
buffer_load_dwordx4 v7,s[72:75],s83 offen lds
v_mfma_f32_16x16x32_bf16 a[80:83],a[116:119],v[140:143],a[80:83]
ds_read_b64_tr_b16 a[98:99],v1 offset:5248
ds_read_b64_tr_b16 a[106:107],v1 offset:25728
v_mfma_f32_16x16x32_bf16 a[80:83],a[120:123],v[152:155],a[80:83]
ds_read_b64_tr_b16 a[108:109],v1 offset:30848
v_mfma_f32_16x16x32_bf16 a[80:83],a[124:127],v[156:159],a[80:83]
ds_read_b64_tr_b16 a[110:111],v1 offset:35968
v_mfma_f32_16x16x32_bf16 a[84:87],a[124:127],v[188:191],a[84:87]
v_mfma_f32_16x16x32_bf16 a[84:87],a[120:123],v[184:187],a[84:87]
buffer_load_dwordx4 v8,s[72:75],s83 offen offset:1024 lds
v_mfma_f32_16x16x32_bf16 a[84:87],a[116:119],v[172:175],a[84:87]
v_mfma_f32_16x16x32_bf16 a[84:87],a[112:115],v[168:171],a[84:87]
v_mfma_f32_16x16x32_bf16 a[88:91],a[112:115],v[200:203],a[88:91]
v_mfma_f32_16x16x32_bf16 a[88:91],a[116:119],v[204:207],a[88:91]
v_mfma_f32_16x16x32_bf16 a[88:91],a[120:123],v[216:219],a[88:91]
buffer_load_dwordx4 v9,s[72:75],s83 offen offset:2048 lds
v_mfma_f32_16x16x32_bf16 a[88:91],a[124:127],v[220:223],a[88:91]
v_mfma_f32_16x16x32_bf16 a[92:95],a[124:127],v[252:255],a[92:95]
ds_read_b64_tr_b16 a[124:125],v2 offset:30880
ds_read_b64_tr_b16 a[126:127],v2 offset:36000
v_mfma_f32_16x16x32_bf16 a[92:95],a[120:123],v[248:251],a[92:95]
ds_read_b64_tr_b16 a[120:121],v2 offset:20640
v_mfma_f32_16x16x32_bf16 a[92:95],a[116:119],v[236:239],a[92:95]
ds_read_b64_tr_b16 a[116:117],v2 offset:10400
v_mfma_f32_16x16x32_bf16 a[92:95],a[112:115],v[232:235],a[92:95]
buffer_load_dwordx4 v10,s[72:75],s83 offen offset:3072 lds
v_mfma_f32_16x16x32_bf16 a[192:195],a[128:131],v[128:131],a[192:195]
ds_read_b64_tr_b16 a[112:113],v2 offset:160
ds_read_b64_tr_b16 a[114:115],v2 offset:5280
v_mfma_f32_16x16x32_bf16 a[192:195],a[132:135],v[160:163],a[192:195]
s_add_u32 m0,s82,132096
ds_read_b64_tr_b16 a[118:119],v2 offset:15520
v_mfma_f32_16x16x32_bf16 a[192:195],a[136:139],v[192:195],a[192:195]
ds_read_b64_tr_b16 a[122:123],v2 offset:25760
v_mfma_f32_16x16x32_bf16 a[192:195],a[140:143],v[224:227],a[192:195]
buffer_load_dwordx4 v[136:139],v6,s[28:31],s76 offen sc1
v_mfma_f32_16x16x32_bf16 a[196:199],a[140:143],v[228:231],a[196:199]
v_mfma_f32_16x16x32_bf16 a[196:199],a[136:139],v[196:199],a[196:199]
v_mfma_f32_16x16x32_bf16 a[196:199],a[132:135],v[164:167],a[196:199]
v_mfma_f32_16x16x32_bf16 a[196:199],a[128:131],v[132:135],a[196:199]
s_waitcnt vmcnt(14)
buffer_load_dwordx4 v11,s[72:75],s83 offen lds
v_mfma_f32_16x16x32_bf16 a[200:203],a[128:131],v[144:147],a[200:203]
ds_read_b64_tr_b16 a[128:129],v1 offset:192
ds_read_b64_tr_b16 a[130:131],v1 offset:5312
v_mfma_f32_16x16x32_bf16 a[200:203],a[132:135],v[176:179],a[200:203]
ds_read_b64_tr_b16 a[132:133],v1 offset:10432
v_mfma_f32_16x16x32_bf16 a[200:203],a[136:139],v[208:211],a[200:203]
ds_read_b64_tr_b16 a[134:135],v1 offset:15552
v_mfma_f32_16x16x32_bf16 a[200:203],a[140:143],v[240:243],a[200:203]
buffer_load_dwordx4 v[140:143],v6,s[28:31],s76 offen offset:1024 sc1
v_mfma_f32_16x16x32_bf16 a[208:211],a[144:147],v[128:131],a[208:211]
ds_read_b64_tr_b16 a[136:137],v1 offset:20672
ds_read_b64_tr_b16 a[138:139],v1 offset:25792
v_mfma_f32_16x16x32_bf16 a[208:211],a[148:151],v[160:163],a[208:211]
ds_read_b64_tr_b16 a[140:141],v1 offset:30912
v_mfma_f32_16x16x32_bf16 a[208:211],a[152:155],v[192:195],a[208:211]
)A"
R"A(ds_read_b64_tr_b16 a[142:143],v1 offset:36032
v_mfma_f32_16x16x32_bf16 a[208:211],a[156:159],v[224:227],a[208:211]
buffer_load_dwordx4 v[152:155],v6,s[28:31],s76 offen offset:2048 sc1
v_mfma_f32_16x16x32_bf16 a[212:215],a[156:159],v[228:231],a[212:215]
v_mfma_f32_16x16x32_bf16 a[212:215],a[152:155],v[196:199],a[212:215]
v_mfma_f32_16x16x32_bf16 a[212:215],a[148:151],v[164:167],a[212:215]
v_mfma_f32_16x16x32_bf16 a[212:215],a[144:147],v[132:135],a[212:215]
buffer_load_dwordx4 v[156:159],v6,s[28:31],s76 offen offset:3072 sc1
v_mfma_f32_16x16x32_bf16 a[216:219],a[144:147],v[144:147],a[216:219]
s_mul_i32 s76,s70,12
ds_read_b64_tr_b16 a[144:145],v2 offset:224
ds_read_b64_tr_b16 a[146:147],v2 offset:5344
v_mfma_f32_16x16x32_bf16 a[216:219],a[148:151],v[176:179],a[216:219]
s_add_u32 s76,s76,0
s_add_u32 s76,s76,s40
ds_read_b64_tr_b16 a[148:149],v2 offset:10464
v_mfma_f32_16x16x32_bf16 a[216:219],a[152:155],v[208:211],a[216:219]
s_mul_i32 s76,s76,5120
s_max_i32 s76,s76,s101
ds_read_b64_tr_b16 a[150:151],v2 offset:15584
v_mfma_f32_16x16x32_bf16 a[216:219],a[156:159],v[240:243],a[216:219]
s_waitcnt lgkmcnt(4)
buffer_load_dwordx4 v[168:171],v6,s[28:31],s77 offen sc1
v_mfma_f32_16x16x32_bf16 a[224:227],a[160:163],v[128:131],a[224:227]
ds_read_b64_tr_b16 a[152:153],v2 offset:20704
ds_read_b64_tr_b16 a[154:155],v2 offset:25824
v_mfma_f32_16x16x32_bf16 a[224:227],a[164:167],v[160:163],a[224:227]
ds_read_b64_tr_b16 a[156:157],v2 offset:30944
v_mfma_f32_16x16x32_bf16 a[224:227],a[168:171],v[192:195],a[224:227]
ds_read_b64_tr_b16 a[158:159],v2 offset:36064
v_mfma_f32_16x16x32_bf16 a[224:227],a[172:175],v[224:227],a[224:227]
buffer_load_dwordx4 v[172:175],v6,s[28:31],s77 offen offset:1024 sc1
v_mfma_f32_16x16x32_bf16 a[228:231],a[172:175],v[228:231],a[228:231]
v_mfma_f32_16x16x32_bf16 a[228:231],a[168:171],v[196:199],a[228:231]
v_mfma_f32_16x16x32_bf16 a[228:231],a[164:167],v[164:167],a[228:231]
v_mfma_f32_16x16x32_bf16 a[228:231],a[160:163],v[132:135],a[228:231]
v_mfma_f32_16x16x32_bf16 a[232:235],a[160:163],v[144:147],a[232:235]
buffer_load_dwordx4 v[184:187],v6,s[28:31],s77 offen offset:2048 sc1
v_mfma_f32_16x16x32_bf16 a[232:235],a[164:167],v[176:179],a[232:235]
ds_read_b64_tr_b16 a[160:161],v1 offset:256
ds_read_b64_tr_b16 a[162:163],v1 offset:5376
v_mfma_f32_16x16x32_bf16 a[232:235],a[168:171],v[208:211],a[232:235]
ds_read_b64_tr_b16 a[164:165],v1 offset:10496
v_mfma_f32_16x16x32_bf16 a[232:235],a[172:175],v[240:243],a[232:235]
ds_read_b64_tr_b16 a[166:167],v1 offset:15616
v_mfma_f32_16x16x32_bf16 a[240:243],a[176:179],v[128:131],a[240:243]
ds_read_b64_tr_b16 a[168:169],v1 offset:20736
v_mfma_f32_16x16x32_bf16 a[240:243],a[180:183],v[160:163],a[240:243]
buffer_load_dwordx4 v[188:191],v6,s[28:31],s77 offen offset:3072 sc1
v_mfma_f32_16x16x32_bf16 a[240:243],a[184:187],v[192:195],a[240:243]
s_mul_i32 s77,s70,12
ds_read_b64_tr_b16 a[170:171],v1 offset:25856
ds_read_b64_tr_b16 a[172:173],v1 offset:30976
v_mfma_f32_16x16x32_bf16 a[240:243],a[188:191],v[224:227],a[240:243]
s_add_u32 s77,s77,4
s_add_u32 s77,s77,s40
ds_read_b64_tr_b16 a[174:175],v1 offset:36096
v_mfma_f32_16x16x32_bf16 a[244:247],a[188:191],v[228:231],a[244:247]
s_mul_i32 s77,s77,5120
s_max_i32 s77,s77,s101
v_mfma_f32_16x16x32_bf16 a[244:247],a[184:187],v[196:199],a[244:247]
v_mfma_f32_16x16x32_bf16 a[244:247],a[180:183],v[164:167],a[244:247]
buffer_load_dwordx4 v[200:203],v6,s[28:31],s78 offen sc1
v_mfma_f32_16x16x32_bf16 a[244:247],a[176:179],v[132:135],a[244:247]
v_mfma_f32_16x16x32_bf16 a[248:251],a[176:179],v[144:147],a[248:251]
ds_read_b64_tr_b16 a[176:177],v2 offset:288
ds_read_b64_tr_b16 a[178:179],v2 offset:5408
v_mfma_f32_16x16x32_bf16 a[248:251],a[180:183],v[176:179],a[248:251]
ds_read_b64_tr_b16 a[180:181],v2 offset:10528
v_mfma_f32_16x16x32_bf16 a[248:251],a[184:187],v[208:211],a[248:251]
ds_read_b64_tr_b16 a[182:183],v2 offset:15648
v_mfma_f32_16x16x32_bf16 a[248:251],a[188:191],v[240:243],a[248:251]
buffer_load_dwordx4 v[204:207],v6,s[28:31],s78 offen offset:1024 sc1
v_mfma_f32_16x16x32_bf16 a[0:3],a[96:99],v[128:131],a[0:3]
ds_read_b64_tr_b16 a[184:185],v2 offset:20768
ds_read_b64_tr_b16 a[186:187],v2 offset:25888
v_mfma_f32_16x16x32_bf16 a[0:3],a[100:103],v[160:163],a[0:3]
ds_read_b64_tr_b16 a[188:189],v2 offset:31008
v_mfma_f32_16x16x32_bf16 a[0:3],a[104:107],v[192:195],a[0:3]
ds_read_b64_tr_b16 a[190:191],v2 offset:36128
v_mfma_f32_16x16x32_bf16 a[0:3],a[108:111],v[224:227],a[0:3]
v_mfma_f32_16x16x32_bf16 a[4:7],a[108:111],v[228:231],a[4:7]
buffer_load_dwordx4 v[216:219],v6,s[28:31],s78 offen offset:2048 sc1
v_mfma_f32_16x16x32_bf16 a[4:7],a[104:107],v[196:199],a[4:7]
v_mfma_f32_16x16x32_bf16 a[4:7],a[100:103],v[164:167],a[4:7]
v_mfma_f32_16x16x32_bf16 a[4:7],a[96:99],v[132:135],a[4:7]
v_mfma_f32_16x16x32_bf16 a[8:11],a[96:99],v[144:147],a[8:11]
v_mfma_f32_16x16x32_bf16 a[8:11],a[100:103],v[176:179],a[8:11]
buffer_load_dwordx4 v[220:223],v6,s[28:31],s78 offen offset:3072 sc1
v_mfma_f32_16x16x32_bf16 a[8:11],a[104:107],v[208:211],a[8:11]
s_mul_i32 s78,s70,12
s_add_u32 s78,s78,8
s_add_u32 s78,s78,s40
v_mfma_f32_16x16x32_bf16 a[8:11],a[108:111],v[240:243],a[8:11]
s_mul_i32 s78,s78,5120
s_max_i32 s78,s78,s101
v_mfma_f32_16x16x32_bf16 a[16:19],a[112:115],v[128:131],a[16:19]
v_mfma_f32_16x16x32_bf16 a[16:19],a[116:119],v[160:163],a[16:19]
v_mfma_f32_16x16x32_bf16 a[16:19],a[120:123],v[192:195],a[16:19]
buffer_load_dwordx4 v[232:235],v6,s[28:31],s79 offen sc1
v_mfma_f32_16x16x32_bf16 a[16:19],a[124:127],v[224:227],a[16:19]
v_mfma_f32_16x16x32_bf16 a[20:23],a[124:127],v[228:231],a[20:23]
v_mfma_f32_16x16x32_bf16 a[20:23],a[120:123],v[196:199],a[20:23]
v_mfma_f32_16x16x32_bf16 a[20:23],a[116:119],v[164:167],a[20:23]
v_mfma_f32_16x16x32_bf16 a[20:23],a[112:115],v[132:135],a[20:23]
buffer_load_dwordx4 v[236:239],v6,s[28:31],s79 offen offset:1024 sc1
v_mfma_f32_16x16x32_bf16 a[24:27],a[112:115],v[144:147],a[24:27]
v_mfma_f32_16x16x32_bf16 a[24:27],a[116:119],v[176:179],a[24:27]
v_mfma_f32_16x16x32_bf16 a[24:27],a[120:123],v[208:211],a[24:27]
v_mfma_f32_16x16x32_bf16 a[24:27],a[124:127],v[240:243],a[24:27]
v_mfma_f32_16x16x32_bf16 a[32:35],a[128:131],v[128:131],a[32:35]
buffer_load_dwordx4 v[248:251],v6,s[28:31],s79 offen offset:2048 sc1
v_mfma_f32_16x16x32_bf16 a[32:35],a[132:135],v[160:163],a[32:35]
v_mfma_f32_16x16x32_bf16 a[32:35],a[136:139],v[192:195],a[32:35]
v_mfma_f32_16x16x32_bf16 a[32:35],a[140:143],v[224:227],a[32:35]
v_mfma_f32_16x16x32_bf16 a[36:39],a[140:143],v[228:231],a[36:39]
v_mfma_f32_16x16x32_bf16 a[36:39],a[136:139],v[196:199],a[36:39]
buffer_load_dwordx4 v[252:255],v6,s[28:31],s79 offen offset:3072 sc1
v_mfma_f32_16x16x32_bf16 a[36:39],a[132:135],v[164:167],a[36:39]
v_mfma_f32_16x16x32_bf16 a[36:39],a[128:131],v[132:135],a[36:39]
v_mfma_f32_16x16x32_bf16 a[40:43],a[128:131],v[144:147],a[40:43]
v_mfma_f32_16x16x32_bf16 a[40:43],a[132:135],v[176:179],a[40:43]
v_mfma_f32_16x16x32_bf16 a[40:43],a[136:139],v[208:211],a[40:43]
s_waitcnt vmcnt(15)
v_mfma_f32_16x16x32_bf16 a[40:43],a[140:143],v[240:243],a[40:43]
s_barrier
ds_read_b64_tr_b16 a[96:97],v42
ds_read_b64_tr_b16 a[98:99],v42 offset:5120
s_waitcnt lgkmcnt(2)
v_mfma_f32_16x16x32_bf16 a[48:51],a[144:147],v[128:131],a[48:51]
ds_read_b64_tr_b16 a[100:101],v42 offset:10240
v_mfma_f32_16x16x32_bf16 a[48:51],a[148:151],v[160:163],a[48:51]
ds_read_b64_tr_b16 a[102:103],v42 offset:15360
v_mfma_f32_16x16x32_bf16 a[48:51],a[152:155],v[192:195],a[48:51]
ds_read_b64_tr_b16 a[104:105],v42 offset:20480
v_mfma_f32_16x16x32_bf16 a[48:51],a[156:159],v[224:227],a[48:51]
ds_read_b64_tr_b16 a[106:107],v42 offset:25600
ds_read_b64_tr_b16 a[108:109],v42 offset:30720
v_mfma_f32_16x16x32_bf16 a[52:55],a[156:159],v[228:231],a[52:55]
ds_read_b64_tr_b16 a[110:111],v42 offset:35840
v_mfma_f32_16x16x32_bf16 a[52:55],a[152:155],v[196:199],a[52:55]
ds_read_b64_tr_b16 a[112:113],v44
v_mfma_f32_16x16x32_bf16 a[52:55],a[148:151],v[164:167],a[52:55]
ds_read_b64_tr_b16 a[114:115],v44 offset:5120
v_mfma_f32_16x16x32_bf16 a[52:55],a[144:147],v[132:135],a[52:55]
ds_read_b64_tr_b16 a[116:117],v44 offset:10240
ds_read_b64_tr_b16 a[118:119],v44 offset:15360
v_mfma_f32_16x16x32_bf16 a[56:59],a[144:147],v[144:147],a[56:59]
ds_read_b64_tr_b16 a[120:121],v44 offset:20480
v_mfma_f32_16x16x32_bf16 a[56:59],a[148:151],v[176:179],a[56:59]
ds_read_b64_tr_b16 a[122:123],v44 offset:25600
v_mfma_f32_16x16x32_bf16 a[56:59],a[152:155],v[208:211],a[56:59]
ds_read_b64_tr_b16 a[124:125],v44 offset:30720
v_mfma_f32_16x16x32_bf16 a[56:59],a[156:159],v[240:243],a[56:59]
ds_read_b64_tr_b16 a[126:127],v44 offset:35840
ds_read_b64_tr_b16 a[128:129],v43 offset:32
v_mfma_f32_16x16x32_bf16 a[64:67],a[160:163],v[128:131],a[64:67]
ds_read_b64_tr_b16 a[130:131],v43 offset:5152
v_mfma_f32_16x16x32_bf16 a[64:67],a[164:167],v[160:163],a[64:67]
ds_read_b64_tr_b16 a[132:133],v43 offset:10272
v_mfma_f32_16x16x32_bf16 a[64:67],a[168:171],v[192:195],a[64:67]
ds_read_b64_tr_b16 a[134:135],v43 offset:15392
v_mfma_f32_16x16x32_bf16 a[64:67],a[172:175],v[224:227],a[64:67]
ds_read_b64_tr_b16 a[136:137],v43 offset:20512
ds_read_b64_tr_b16 a[138:139],v43 offset:25632
v_mfma_f32_16x16x32_bf16 a[68:71],a[172:175],v[228:231],a[68:71]
ds_read_b64_tr_b16 a[140:141],v43 offset:30752
v_mfma_f32_16x16x32_bf16 a[68:71],a[168:171],v[196:199],a[68:71]
ds_read_b64_tr_b16 a[142:143],v43 offset:35872
v_mfma_f32_16x16x32_bf16 a[68:71],a[164:167],v[164:167],a[68:71]
ds_read_b64_tr_b16 a[144:145],v45 offset:32
v_mfma_f32_16x16x32_bf16 a[68:71],a[160:163],v[132:135],a[68:71]
ds_read_b64_tr_b16 a[146:147],v45 offset:5152
ds_read_b64_tr_b16 a[148:149],v45 offset:10272
v_mfma_f32_16x16x32_bf16 a[72:75],a[160:163],v[144:147],a[72:75]
ds_read_b64_tr_b16 a[150:151],v45 offset:15392
v_mfma_f32_16x16x32_bf16 a[72:75],a[164:167],v[176:179],a[72:75]
ds_read_b64_tr_b16 a[152:153],v45 offset:20512
v_mfma_f32_16x16x32_bf16 a[72:75],a[168:171],v[208:211],a[72:75]
ds_read_b64_tr_b16 a[154:155],v45 offset:25632
v_mfma_f32_16x16x32_bf16 a[72:75],a[172:175],v[240:243],a[72:75]
ds_read_b64_tr_b16 a[156:157],v45 offset:30752
ds_read_b64_tr_b16 a[158:159],v45 offset:35872
v_mfma_f32_16x16x32_bf16 a[80:83],a[176:179],v[128:131],a[80:83]
ds_read_b64_tr_b16 a[160:161],v42 offset:64
v_mfma_f32_16x16x32_bf16 a[80:83],a[180:183],v[160:163],a[80:83]
ds_read_b64_tr_b16 a[162:163],v42 offset:5184
v_mfma_f32_16x16x32_bf16 a[80:83],a[184:187],v[192:195],a[80:83]
ds_read_b64_tr_b16 a[164:165],v42 offset:10304
v_mfma_f32_16x16x32_bf16 a[80:83],a[188:191],v[224:227],a[80:83]
ds_read_b64_tr_b16 a[166:167],v42 offset:15424
ds_read_b64_tr_b16 a[168:169],v42 offset:20544
v_mfma_f32_16x16x32_bf16 a[84:87],a[188:191],v[228:231],a[84:87]
ds_read_b64_tr_b16 a[170:171],v42 offset:25664
v_mfma_f32_16x16x32_bf16 a[84:87],a[184:187],v[196:199],a[84:87]
ds_read_b64_tr_b16 a[172:173],v42 offset:30784
v_mfma_f32_16x16x32_bf16 a[84:87],a[180:183],v[164:167],a[84:87]
ds_read_b64_tr_b16 a[174:175],v42 offset:35904
v_mfma_f32_16x16x32_bf16 a[84:87],a[176:179],v[132:135],a[84:87]
v_mfma_f32_16x16x32_bf16 a[88:91],a[176:179],v[144:147],a[88:91]
ds_read_b64_tr_b16 a[176:177],v44 offset:64
ds_read_b64_tr_b16 a[178:179],v44 offset:5184
v_mfma_f32_16x16x32_bf16 a[88:91],a[180:183],v[176:179],a[88:91]
ds_read_b64_tr_b16 a[180:181],v44 offset:10304
v_mfma_f32_16x16x32_bf16 a[88:91],a[184:187],v[208:211],a[88:91]
ds_read_b64_tr_b16 a[182:183],v44 offset:15424
v_mfma_f32_16x16x32_bf16 a[88:91],a[188:191],v[240:243],a[88:91]
s_waitcnt lgkmcnt(7)
ds_read_b64_tr_b16 a[184:185],v44 offset:20544
v_mfma_f32_16x16x32_bf16 a[192:195],a[96:99],v[64:67],a[192:195]
ds_read_b64_tr_b16 a[186:187],v44 offset:25664
ds_read_b64_tr_b16 a[188:189],v44 offset:30784
v_mfma_f32_16x16x32_bf16 a[192:195],a[100:103],v[68:71],a[192:195]
ds_read_b64_tr_b16 a[190:191],v44 offset:35904
v_mfma_f32_16x16x32_bf16 a[192:195],a[104:107],v[72:75],a[192:195]
v_mfma_f32_16x16x32_bf16 a[192:195],a[108:111],v[76:79],a[192:195]
v_mfma_f32_16x16x32_bf16 a[196:199],a[108:111],v[92:95],a[196:199]
v_mfma_f32_16x16x32_bf16 a[196:199],a[104:107],v[88:91],a[196:199]
)A"
R"A(v_mfma_f32_16x16x32_bf16 a[196:199],a[100:103],v[84:87],a[196:199]
v_mfma_f32_16x16x32_bf16 a[196:199],a[96:99],v[80:83],a[196:199]
v_mfma_f32_16x16x32_bf16 a[200:203],a[96:99],v[96:99],a[200:203]
ds_read_b64_tr_b16 a[96:97],v43 offset:96
ds_read_b64_tr_b16 a[98:99],v43 offset:5216
v_mfma_f32_16x16x32_bf16 a[200:203],a[100:103],v[100:103],a[200:203]
ds_read_b64_tr_b16 a[100:101],v43 offset:10336
v_mfma_f32_16x16x32_bf16 a[200:203],a[104:107],v[104:107],a[200:203]
ds_read_b64_tr_b16 a[102:103],v43 offset:15456
v_mfma_f32_16x16x32_bf16 a[200:203],a[108:111],v[108:111],a[200:203]
ds_read_b64_tr_b16 a[104:105],v43 offset:20576
v_mfma_f32_16x16x32_bf16 a[192:195],a[112:115],v[136:139],a[192:195]
s_waitcnt vmcnt(3)
ds_read_b64_tr_b16 a[106:107],v43 offset:25696
ds_read_b64_tr_b16 a[108:109],v43 offset:30816
v_mfma_f32_16x16x32_bf16 a[192:195],a[116:119],v[140:143],a[192:195]
ds_read_b64_tr_b16 a[110:111],v43 offset:35936
v_mfma_f32_16x16x32_bf16 a[192:195],a[120:123],v[152:155],a[192:195]
v_mfma_f32_16x16x32_bf16 a[192:195],a[124:127],v[156:159],a[192:195]
v_mfma_f32_16x16x32_bf16 a[196:199],a[124:127],v[188:191],a[196:199]
v_mfma_f32_16x16x32_bf16 a[196:199],a[120:123],v[184:187],a[196:199]
v_mfma_f32_16x16x32_bf16 a[196:199],a[116:119],v[172:175],a[196:199]
v_mfma_f32_16x16x32_bf16 a[196:199],a[112:115],v[168:171],a[196:199]
v_mfma_f32_16x16x32_bf16 a[200:203],a[112:115],v[200:203],a[200:203]
ds_read_b64_tr_b16 a[112:113],v45 offset:96
ds_read_b64_tr_b16 a[114:115],v45 offset:5216
v_mfma_f32_16x16x32_bf16 a[200:203],a[116:119],v[204:207],a[200:203]
v_accvgpr_read_b32 v128,a192
v_accvgpr_read_b32 v129,a193
v_mfma_f32_16x16x32_bf16 a[200:203],a[120:123],v[216:219],a[200:203]
v_cvt_pk_bf16_f32 v128,v128,v129
v_accvgpr_read_b32 v130,a194
v_mfma_f32_16x16x32_bf16 a[200:203],a[124:127],v[220:223],a[200:203]
v_accvgpr_read_b32 v131,a195
v_cvt_pk_bf16_f32 v129,v130,v131
v_mfma_f32_16x16x32_bf16 a[208:211],a[128:131],v[64:67],a[208:211]
ds_read_b64_tr_b16 a[116:117],v45 offset:10336
ds_read_b64_tr_b16 a[118:119],v45 offset:15456
v_mfma_f32_16x16x32_bf16 a[208:211],a[132:135],v[68:71],a[208:211]
v_accvgpr_read_b32 v132,a196
v_accvgpr_read_b32 v133,a197
v_mfma_f32_16x16x32_bf16 a[208:211],a[136:139],v[72:75],a[208:211]
v_cvt_pk_bf16_f32 v132,v132,v133
v_accvgpr_read_b32 v134,a198
v_mfma_f32_16x16x32_bf16 a[208:211],a[140:143],v[76:79],a[208:211]
v_accvgpr_read_b32 v135,a199
v_cvt_pk_bf16_f32 v133,v134,v135
v_mfma_f32_16x16x32_bf16 a[212:215],a[140:143],v[92:95],a[212:215]
ds_read_b64_tr_b16 a[120:121],v45 offset:20576
ds_read_b64_tr_b16 a[122:123],v45 offset:25696
v_mfma_f32_16x16x32_bf16 a[212:215],a[136:139],v[88:91],a[212:215]
ds_read_b64_tr_b16 a[124:125],v45 offset:30816
v_accvgpr_read_b32 v144,a200
v_mfma_f32_16x16x32_bf16 a[212:215],a[132:135],v[84:87],a[212:215]
ds_read_b64_tr_b16 a[126:127],v45 offset:35936
v_accvgpr_read_b32 v145,a201
v_mfma_f32_16x16x32_bf16 a[212:215],a[128:131],v[80:83],a[212:215]
v_cvt_pk_bf16_f32 v144,v144,v145
v_accvgpr_read_b32 v146,a202
v_mfma_f32_16x16x32_bf16 a[216:219],a[128:131],v[96:99],a[216:219]
v_accvgpr_read_b32 v147,a203
v_cvt_pk_bf16_f32 v145,v146,v147
v_mfma_f32_16x16x32_bf16 a[216:219],a[132:135],v[100:103],a[216:219]
ds_read_b64_tr_b16 a[128:129],v42 offset:128
ds_read_b64_tr_b16 a[130:131],v42 offset:5248
v_mfma_f32_16x16x32_bf16 a[216:219],a[136:139],v[104:107],a[216:219]
ds_read_b64_tr_b16 a[132:133],v42 offset:10368
v_mfma_f32_16x16x32_bf16 a[216:219],a[140:143],v[108:111],a[216:219]
ds_read_b64_tr_b16 a[134:135],v42 offset:15488
v_mfma_f32_16x16x32_bf16 a[208:211],a[144:147],v[136:139],a[208:211]
ds_read_b64_tr_b16 a[136:137],v42 offset:20608
v_mfma_f32_16x16x32_bf16 a[208:211],a[148:151],v[140:143],a[208:211]
ds_read_b64_tr_b16 a[138:139],v42 offset:25728
ds_read_b64_tr_b16 a[140:141],v42 offset:30848
v_mfma_f32_16x16x32_bf16 a[208:211],a[152:155],v[152:155],a[208:211]
ds_read_b64_tr_b16 a[142:143],v42 offset:35968
v_mfma_f32_16x16x32_bf16 a[208:211],a[156:159],v[156:159],a[208:211]
v_mfma_f32_16x16x32_bf16 a[212:215],a[156:159],v[188:191],a[212:215]
v_mfma_f32_16x16x32_bf16 a[212:215],a[152:155],v[184:187],a[212:215]
v_mfma_f32_16x16x32_bf16 a[212:215],a[148:151],v[172:175],a[212:215]
v_mfma_f32_16x16x32_bf16 a[212:215],a[144:147],v[168:171],a[212:215]
v_mfma_f32_16x16x32_bf16 a[216:219],a[144:147],v[200:203],a[216:219]
ds_read_b64_tr_b16 a[144:145],v44 offset:128
ds_read_b64_tr_b16 a[146:147],v44 offset:5248
v_mfma_f32_16x16x32_bf16 a[216:219],a[148:151],v[204:207],a[216:219]
v_accvgpr_read_b32 v130,a208
v_accvgpr_read_b32 v131,a209
v_mfma_f32_16x16x32_bf16 a[216:219],a[152:155],v[216:219],a[216:219]
v_accvgpr_read_b32 v12,a210
v_accvgpr_read_b32 v13,a211
v_mfma_f32_16x16x32_bf16 a[216:219],a[156:159],v[220:223],a[216:219]
v_cvt_pk_bf16_f32 v130,v130,v131
v_cvt_pk_bf16_f32 v131,v12,v13
v_mfma_f32_16x16x32_bf16 a[224:227],a[160:163],v[64:67],a[224:227]
buffer_store_dwordx4 v[128:131],v6,s[32:35],s84 offen sc1
v_mfma_f32_16x16x32_bf16 a[224:227],a[164:167],v[68:71],a[224:227]
s_waitcnt lgkmcnt(2)
v_accvgpr_read_b32 v134,a212
v_accvgpr_read_b32 v135,a213
v_mfma_f32_16x16x32_bf16 a[224:227],a[168:171],v[72:75],a[224:227]
v_accvgpr_read_b32 v14,a214
v_accvgpr_read_b32 v15,a215
v_mfma_f32_16x16x32_bf16 a[224:227],a[172:175],v[76:79],a[224:227]
v_cvt_pk_bf16_f32 v134,v134,v135
v_cvt_pk_bf16_f32 v135,v14,v15
v_mfma_f32_16x16x32_bf16 a[228:231],a[172:175],v[92:95],a[228:231]
buffer_store_dwordx4 v[132:135],v6,s[32:35],s85 offen sc1
v_mfma_f32_16x16x32_bf16 a[228:231],a[168:171],v[88:91],a[228:231]
v_accvgpr_read_b32 v146,a216
v_accvgpr_read_b32 v147,a217
v_mfma_f32_16x16x32_bf16 a[228:231],a[164:167],v[84:87],a[228:231]
v_accvgpr_read_b32 v16,a218
v_accvgpr_read_b32 v17,a219
v_mfma_f32_16x16x32_bf16 a[228:231],a[160:163],v[80:83],a[228:231]
v_cvt_pk_bf16_f32 v146,v146,v147
v_cvt_pk_bf16_f32 v147,v16,v17
v_mfma_f32_16x16x32_bf16 a[232:235],a[160:163],v[96:99],a[232:235]
buffer_store_dwordx4 v[144:147],v6,s[32:35],s86 offen sc1
v_mfma_f32_16x16x32_bf16 a[232:235],a[164:167],v[100:103],a[232:235]
ds_read_b64_tr_b16 a[148:149],v44 offset:10368
ds_read_b64_tr_b16 a[150:151],v44 offset:15488
v_mfma_f32_16x16x32_bf16 a[232:235],a[168:171],v[104:107],a[232:235]
ds_read_b64_tr_b16 a[152:153],v44 offset:20608
v_mfma_f32_16x16x32_bf16 a[232:235],a[172:175],v[108:111],a[232:235]
ds_read_b64_tr_b16 a[154:155],v44 offset:25728
v_mfma_f32_16x16x32_bf16 a[224:227],a[176:179],v[136:139],a[224:227]
ds_read_b64_tr_b16 a[156:157],v44 offset:30848
v_mfma_f32_16x16x32_bf16 a[224:227],a[180:183],v[140:143],a[224:227]
ds_read_b64_tr_b16 a[158:159],v44 offset:35968
ds_read_b64_tr_b16 a[160:161],v43 offset:160
v_mfma_f32_16x16x32_bf16 a[224:227],a[184:187],v[152:155],a[224:227]
ds_read_b64_tr_b16 a[162:163],v43 offset:5280
v_mfma_f32_16x16x32_bf16 a[224:227],a[188:191],v[156:159],a[224:227]
ds_read_b64_tr_b16 a[164:165],v43 offset:10400
v_mfma_f32_16x16x32_bf16 a[228:231],a[188:191],v[188:191],a[228:231]
ds_read_b64_tr_b16 a[166:167],v43 offset:15520
v_mfma_f32_16x16x32_bf16 a[228:231],a[184:187],v[184:187],a[228:231]
ds_read_b64_tr_b16 a[168:169],v43 offset:20640
ds_read_b64_tr_b16 a[170:171],v43 offset:25760
v_mfma_f32_16x16x32_bf16 a[228:231],a[180:183],v[172:175],a[228:231]
ds_read_b64_tr_b16 a[172:173],v43 offset:30880
v_mfma_f32_16x16x32_bf16 a[228:231],a[176:179],v[168:171],a[228:231]
ds_read_b64_tr_b16 a[174:175],v43 offset:36000
v_mfma_f32_16x16x32_bf16 a[232:235],a[176:179],v[200:203],a[232:235]
ds_read_b64_tr_b16 a[176:177],v45 offset:160
v_mfma_f32_16x16x32_bf16 a[232:235],a[180:183],v[204:207],a[232:235]
v_accvgpr_read_b32 v160,a224
v_accvgpr_read_b32 v161,a225
v_mfma_f32_16x16x32_bf16 a[232:235],a[184:187],v[216:219],a[232:235]
v_cvt_pk_bf16_f32 v160,v160,v161
v_accvgpr_read_b32 v162,a226
v_mfma_f32_16x16x32_bf16 a[232:235],a[188:191],v[220:223],a[232:235]
v_accvgpr_read_b32 v163,a227
v_cvt_pk_bf16_f32 v161,v162,v163
v_mfma_f32_16x16x32_bf16 a[240:243],a[96:99],v[64:67],a[240:243]
ds_read_b64_tr_b16 a[178:179],v45 offset:5280
ds_read_b64_tr_b16 a[180:181],v45 offset:10400
v_mfma_f32_16x16x32_bf16 a[240:243],a[100:103],v[68:71],a[240:243]
v_accvgpr_read_b32 v164,a228
v_accvgpr_read_b32 v165,a229
v_mfma_f32_16x16x32_bf16 a[240:243],a[104:107],v[72:75],a[240:243]
v_cvt_pk_bf16_f32 v164,v164,v165
v_accvgpr_read_b32 v166,a230
v_mfma_f32_16x16x32_bf16 a[240:243],a[108:111],v[76:79],a[240:243]
v_accvgpr_read_b32 v167,a231
v_cvt_pk_bf16_f32 v165,v166,v167
v_mfma_f32_16x16x32_bf16 a[244:247],a[108:111],v[92:95],a[244:247]
ds_read_b64_tr_b16 a[182:183],v45 offset:15520
ds_read_b64_tr_b16 a[184:185],v45 offset:20640
v_mfma_f32_16x16x32_bf16 a[244:247],a[104:107],v[88:91],a[244:247]
ds_read_b64_tr_b16 a[186:187],v45 offset:25760
v_accvgpr_read_b32 v176,a232
v_mfma_f32_16x16x32_bf16 a[244:247],a[100:103],v[84:87],a[244:247]
ds_read_b64_tr_b16 a[188:189],v45 offset:30880
v_accvgpr_read_b32 v177,a233
v_mfma_f32_16x16x32_bf16 a[244:247],a[96:99],v[80:83],a[244:247]
ds_read_b64_tr_b16 a[190:191],v45 offset:36000
v_cvt_pk_bf16_f32 v176,v176,v177
v_mfma_f32_16x16x32_bf16 a[248:251],a[96:99],v[96:99],a[248:251]
v_accvgpr_read_b32 v178,a234
v_accvgpr_read_b32 v179,a235
v_mfma_f32_16x16x32_bf16 a[248:251],a[100:103],v[100:103],a[248:251]
ds_read_b64_tr_b16 a[96:97],v42 offset:192
v_cvt_pk_bf16_f32 v177,v178,v179
v_mfma_f32_16x16x32_bf16 a[248:251],a[104:107],v[104:107],a[248:251]
ds_read_b64_tr_b16 a[98:99],v42 offset:5312
ds_read_b64_tr_b16 a[100:101],v42 offset:10432
v_mfma_f32_16x16x32_bf16 a[248:251],a[108:111],v[108:111],a[248:251]
ds_read_b64_tr_b16 a[102:103],v42 offset:15552
v_mfma_f32_16x16x32_bf16 a[240:243],a[112:115],v[136:139],a[240:243]
ds_read_b64_tr_b16 a[104:105],v42 offset:20672
v_mfma_f32_16x16x32_bf16 a[240:243],a[116:119],v[140:143],a[240:243]
ds_read_b64_tr_b16 a[106:107],v42 offset:25792
v_mfma_f32_16x16x32_bf16 a[240:243],a[120:123],v[152:155],a[240:243]
ds_read_b64_tr_b16 a[108:109],v42 offset:30912
ds_read_b64_tr_b16 a[110:111],v42 offset:36032
v_mfma_f32_16x16x32_bf16 a[240:243],a[124:127],v[156:159],a[240:243]
v_mfma_f32_16x16x32_bf16 a[244:247],a[124:127],v[188:191],a[244:247]
v_mfma_f32_16x16x32_bf16 a[244:247],a[120:123],v[184:187],a[244:247]
v_mfma_f32_16x16x32_bf16 a[244:247],a[116:119],v[172:175],a[244:247]
v_mfma_f32_16x16x32_bf16 a[244:247],a[112:115],v[168:171],a[244:247]
v_mfma_f32_16x16x32_bf16 a[248:251],a[112:115],v[200:203],a[248:251]
ds_read_b64_tr_b16 a[112:113],v44 offset:192
ds_read_b64_tr_b16 a[114:115],v44 offset:5312
v_mfma_f32_16x16x32_bf16 a[248:251],a[116:119],v[204:207],a[248:251]
v_accvgpr_read_b32 v162,a240
v_accvgpr_read_b32 v163,a241
v_mfma_f32_16x16x32_bf16 a[248:251],a[120:123],v[216:219],a[248:251]
v_accvgpr_read_b32 v12,a242
v_accvgpr_read_b32 v13,a243
v_mfma_f32_16x16x32_bf16 a[248:251],a[124:127],v[220:223],a[248:251]
v_cvt_pk_bf16_f32 v162,v162,v163
v_cvt_pk_bf16_f32 v163,v12,v13
v_mfma_f32_16x16x32_bf16 a[0:3],a[128:131],v[64:67],a[0:3]
buffer_store_dwordx4 v[160:163],v6,s[32:35],s84 offen offset:1024 sc1
v_mfma_f32_16x16x32_bf16 a[0:3],a[132:135],v[68:71],a[0:3]
v_accvgpr_read_b32 v166,a244
v_accvgpr_read_b32 v167,a245
v_mfma_f32_16x16x32_bf16 a[0:3],a[136:139],v[72:75],a[0:3]
v_accvgpr_read_b32 v14,a246
v_accvgpr_read_b32 v15,a247
v_mfma_f32_16x16x32_bf16 a[0:3],a[140:143],v[76:79],a[0:3]
v_cvt_pk_bf16_f32 v166,v166,v167
v_cvt_pk_bf16_f32 v167,v14,v15
v_mfma_f32_16x16x32_bf16 a[4:7],a[140:143],v[92:95],a[4:7]
buffer_store_dwordx4 v[164:167],v6,s[32:35],s85 offen offset:1024 sc1
v_mfma_f32_16x16x32_bf16 a[4:7],a[136:139],v[88:91],a[4:7]
v_accvgpr_read_b32 v178,a248
v_accvgpr_read_b32 v179,a249
v_mfma_f32_16x16x32_bf16 a[4:7],a[132:135],v[84:87],a[4:7]
v_accvgpr_read_b32 v16,a250
v_accvgpr_read_b32 v17,a251
v_mfma_f32_16x16x32_bf16 a[4:7],a[128:131],v[80:83],a[4:7]
v_cvt_pk_bf16_f32 v178,v178,v179
v_cvt_pk_bf16_f32 v179,v16,v17
v_mfma_f32_16x16x32_bf16 a[8:11],a[128:131],v[96:99],a[8:11]
buffer_store_dwordx4 v[176:179],v6,s[32:35],s86 offen offset:1024 sc1
v_mfma_f32_16x16x32_bf16 a[8:11],a[132:135],v[100:103],a[8:11]
)A"
R"A(ds_read_b64_tr_b16 a[116:117],v44 offset:10432
ds_read_b64_tr_b16 a[118:119],v44 offset:15552
v_mfma_f32_16x16x32_bf16 a[8:11],a[136:139],v[104:107],a[8:11]
ds_read_b64_tr_b16 a[120:121],v44 offset:20672
v_mfma_f32_16x16x32_bf16 a[8:11],a[140:143],v[108:111],a[8:11]
s_waitcnt lgkmcnt(3)
ds_read_b64_tr_b16 a[122:123],v44 offset:25792
v_mfma_f32_16x16x32_bf16 a[0:3],a[144:147],v[136:139],a[0:3]
ds_read_b64_tr_b16 a[124:125],v44 offset:30912
v_mfma_f32_16x16x32_bf16 a[0:3],a[148:151],v[140:143],a[0:3]
ds_read_b64_tr_b16 a[126:127],v44 offset:36032
ds_read_b64_tr_b16 a[128:129],v43 offset:224
v_mfma_f32_16x16x32_bf16 a[0:3],a[152:155],v[152:155],a[0:3]
ds_read_b64_tr_b16 a[130:131],v43 offset:5344
v_mfma_f32_16x16x32_bf16 a[0:3],a[156:159],v[156:159],a[0:3]
ds_read_b64_tr_b16 a[132:133],v43 offset:10464
v_mfma_f32_16x16x32_bf16 a[4:7],a[156:159],v[188:191],a[4:7]
ds_read_b64_tr_b16 a[134:135],v43 offset:15584
v_mfma_f32_16x16x32_bf16 a[4:7],a[152:155],v[184:187],a[4:7]
ds_read_b64_tr_b16 a[136:137],v43 offset:20704
ds_read_b64_tr_b16 a[138:139],v43 offset:25824
v_mfma_f32_16x16x32_bf16 a[4:7],a[148:151],v[172:175],a[4:7]
ds_read_b64_tr_b16 a[140:141],v43 offset:30944
v_mfma_f32_16x16x32_bf16 a[4:7],a[144:147],v[168:171],a[4:7]
ds_read_b64_tr_b16 a[142:143],v43 offset:36064
v_mfma_f32_16x16x32_bf16 a[8:11],a[144:147],v[200:203],a[8:11]
ds_read_b64_tr_b16 a[144:145],v45 offset:224
v_mfma_f32_16x16x32_bf16 a[8:11],a[148:151],v[204:207],a[8:11]
v_accvgpr_read_b32 v192,a0
v_accvgpr_read_b32 v193,a1
v_mfma_f32_16x16x32_bf16 a[8:11],a[152:155],v[216:219],a[8:11]
v_cvt_pk_bf16_f32 v192,v192,v193
v_accvgpr_read_b32 v194,a2
v_mfma_f32_16x16x32_bf16 a[8:11],a[156:159],v[220:223],a[8:11]
v_accvgpr_read_b32 v195,a3
v_cvt_pk_bf16_f32 v193,v194,v195
v_mfma_f32_16x16x32_bf16 a[16:19],a[160:163],v[64:67],a[16:19]
ds_read_b64_tr_b16 a[146:147],v45 offset:5344
ds_read_b64_tr_b16 a[148:149],v45 offset:10464
v_mfma_f32_16x16x32_bf16 a[16:19],a[164:167],v[68:71],a[16:19]
v_accvgpr_read_b32 v196,a4
v_accvgpr_read_b32 v197,a5
v_mfma_f32_16x16x32_bf16 a[16:19],a[168:171],v[72:75],a[16:19]
v_cvt_pk_bf16_f32 v196,v196,v197
v_accvgpr_read_b32 v198,a6
v_mfma_f32_16x16x32_bf16 a[16:19],a[172:175],v[76:79],a[16:19]
v_accvgpr_read_b32 v199,a7
v_cvt_pk_bf16_f32 v197,v198,v199
v_mfma_f32_16x16x32_bf16 a[20:23],a[172:175],v[92:95],a[20:23]
ds_read_b64_tr_b16 a[150:151],v45 offset:15584
ds_read_b64_tr_b16 a[152:153],v45 offset:20704
v_mfma_f32_16x16x32_bf16 a[20:23],a[168:171],v[88:91],a[20:23]
ds_read_b64_tr_b16 a[154:155],v45 offset:25824
v_accvgpr_read_b32 v208,a8
v_mfma_f32_16x16x32_bf16 a[20:23],a[164:167],v[84:87],a[20:23]
ds_read_b64_tr_b16 a[156:157],v45 offset:30944
v_accvgpr_read_b32 v209,a9
v_mfma_f32_16x16x32_bf16 a[20:23],a[160:163],v[80:83],a[20:23]
ds_read_b64_tr_b16 a[158:159],v45 offset:36064
v_cvt_pk_bf16_f32 v208,v208,v209
v_mfma_f32_16x16x32_bf16 a[24:27],a[160:163],v[96:99],a[24:27]
v_accvgpr_read_b32 v210,a10
v_accvgpr_read_b32 v211,a11
v_mfma_f32_16x16x32_bf16 a[24:27],a[164:167],v[100:103],a[24:27]
ds_read_b64_tr_b16 a[160:161],v42 offset:256
v_cvt_pk_bf16_f32 v209,v210,v211
v_mfma_f32_16x16x32_bf16 a[24:27],a[168:171],v[104:107],a[24:27]
ds_read_b64_tr_b16 a[162:163],v42 offset:5376
ds_read_b64_tr_b16 a[164:165],v42 offset:10496
v_mfma_f32_16x16x32_bf16 a[24:27],a[172:175],v[108:111],a[24:27]
ds_read_b64_tr_b16 a[166:167],v42 offset:15616
v_mfma_f32_16x16x32_bf16 a[16:19],a[176:179],v[136:139],a[16:19]
ds_read_b64_tr_b16 a[168:169],v42 offset:20736
v_mfma_f32_16x16x32_bf16 a[16:19],a[180:183],v[140:143],a[16:19]
ds_read_b64_tr_b16 a[170:171],v42 offset:25856
v_mfma_f32_16x16x32_bf16 a[16:19],a[184:187],v[152:155],a[16:19]
ds_read_b64_tr_b16 a[172:173],v42 offset:30976
ds_read_b64_tr_b16 a[174:175],v42 offset:36096
v_mfma_f32_16x16x32_bf16 a[16:19],a[188:191],v[156:159],a[16:19]
v_mfma_f32_16x16x32_bf16 a[20:23],a[188:191],v[188:191],a[20:23]
v_mfma_f32_16x16x32_bf16 a[20:23],a[184:187],v[184:187],a[20:23]
v_mfma_f32_16x16x32_bf16 a[20:23],a[180:183],v[172:175],a[20:23]
v_mfma_f32_16x16x32_bf16 a[20:23],a[176:179],v[168:171],a[20:23]
v_mfma_f32_16x16x32_bf16 a[24:27],a[176:179],v[200:203],a[24:27]
ds_read_b64_tr_b16 a[176:177],v44 offset:256
ds_read_b64_tr_b16 a[178:179],v44 offset:5376
v_mfma_f32_16x16x32_bf16 a[24:27],a[180:183],v[204:207],a[24:27]
v_accvgpr_read_b32 v194,a16
v_accvgpr_read_b32 v195,a17
v_mfma_f32_16x16x32_bf16 a[24:27],a[184:187],v[216:219],a[24:27]
v_accvgpr_read_b32 v12,a18
v_accvgpr_read_b32 v13,a19
v_mfma_f32_16x16x32_bf16 a[24:27],a[188:191],v[220:223],a[24:27]
v_cvt_pk_bf16_f32 v194,v194,v195
v_cvt_pk_bf16_f32 v195,v12,v13
v_mfma_f32_16x16x32_bf16 a[32:35],a[96:99],v[64:67],a[32:35]
buffer_store_dwordx4 v[192:195],v6,s[32:35],s84 offen offset:2048 sc1
v_mfma_f32_16x16x32_bf16 a[32:35],a[100:103],v[68:71],a[32:35]
v_accvgpr_read_b32 v198,a20
v_accvgpr_read_b32 v199,a21
v_mfma_f32_16x16x32_bf16 a[32:35],a[104:107],v[72:75],a[32:35]
v_accvgpr_read_b32 v14,a22
v_accvgpr_read_b32 v15,a23
v_mfma_f32_16x16x32_bf16 a[32:35],a[108:111],v[76:79],a[32:35]
v_cvt_pk_bf16_f32 v198,v198,v199
v_cvt_pk_bf16_f32 v199,v14,v15
v_mfma_f32_16x16x32_bf16 a[36:39],a[108:111],v[92:95],a[36:39]
buffer_store_dwordx4 v[196:199],v6,s[32:35],s85 offen offset:2048 sc1
v_mfma_f32_16x16x32_bf16 a[36:39],a[104:107],v[88:91],a[36:39]
v_accvgpr_read_b32 v210,a24
v_accvgpr_read_b32 v211,a25
v_mfma_f32_16x16x32_bf16 a[36:39],a[100:103],v[84:87],a[36:39]
v_accvgpr_read_b32 v16,a26
v_accvgpr_read_b32 v17,a27
v_mfma_f32_16x16x32_bf16 a[36:39],a[96:99],v[80:83],a[36:39]
v_cvt_pk_bf16_f32 v210,v210,v211
v_cvt_pk_bf16_f32 v211,v16,v17
v_mfma_f32_16x16x32_bf16 a[40:43],a[96:99],v[96:99],a[40:43]
buffer_store_dwordx4 v[208:211],v6,s[32:35],s86 offen offset:2048 sc1
v_mfma_f32_16x16x32_bf16 a[40:43],a[100:103],v[100:103],a[40:43]
ds_read_b64_tr_b16 a[180:181],v44 offset:10496
ds_read_b64_tr_b16 a[182:183],v44 offset:15616
v_mfma_f32_16x16x32_bf16 a[40:43],a[104:107],v[104:107],a[40:43]
ds_read_b64_tr_b16 a[184:185],v44 offset:20736
v_mfma_f32_16x16x32_bf16 a[40:43],a[108:111],v[108:111],a[40:43]
ds_read_b64_tr_b16 a[186:187],v44 offset:25856
v_mfma_f32_16x16x32_bf16 a[32:35],a[112:115],v[136:139],a[32:35]
s_waitcnt lgkmcnt(4)
ds_read_b64_tr_b16 a[188:189],v44 offset:30976
v_mfma_f32_16x16x32_bf16 a[32:35],a[116:119],v[140:143],a[32:35]
ds_read_b64_tr_b16 a[190:191],v44 offset:36096
ds_read_b64_tr_b16 a[96:97],v43 offset:288
v_mfma_f32_16x16x32_bf16 a[32:35],a[120:123],v[152:155],a[32:35]
ds_read_b64_tr_b16 a[98:99],v43 offset:5408
v_mfma_f32_16x16x32_bf16 a[32:35],a[124:127],v[156:159],a[32:35]
ds_read_b64_tr_b16 a[100:101],v43 offset:10528
v_mfma_f32_16x16x32_bf16 a[36:39],a[124:127],v[188:191],a[36:39]
ds_read_b64_tr_b16 a[102:103],v43 offset:15648
v_mfma_f32_16x16x32_bf16 a[36:39],a[120:123],v[184:187],a[36:39]
ds_read_b64_tr_b16 a[104:105],v43 offset:20768
ds_read_b64_tr_b16 a[106:107],v43 offset:25888
v_mfma_f32_16x16x32_bf16 a[36:39],a[116:119],v[172:175],a[36:39]
ds_read_b64_tr_b16 a[108:109],v43 offset:31008
v_mfma_f32_16x16x32_bf16 a[36:39],a[112:115],v[168:171],a[36:39]
ds_read_b64_tr_b16 a[110:111],v43 offset:36128
v_mfma_f32_16x16x32_bf16 a[40:43],a[112:115],v[200:203],a[40:43]
ds_read_b64_tr_b16 a[112:113],v45 offset:288
v_mfma_f32_16x16x32_bf16 a[40:43],a[116:119],v[204:207],a[40:43]
v_accvgpr_read_b32 v224,a32
v_accvgpr_read_b32 v225,a33
v_mfma_f32_16x16x32_bf16 a[40:43],a[120:123],v[216:219],a[40:43]
v_cvt_pk_bf16_f32 v224,v224,v225
v_accvgpr_read_b32 v226,a34
v_mfma_f32_16x16x32_bf16 a[40:43],a[124:127],v[220:223],a[40:43]
v_accvgpr_read_b32 v227,a35
v_cvt_pk_bf16_f32 v225,v226,v227
v_mfma_f32_16x16x32_bf16 a[48:51],a[128:131],v[64:67],a[48:51]
ds_read_b64_tr_b16 a[114:115],v45 offset:5408
ds_read_b64_tr_b16 a[116:117],v45 offset:10528
v_mfma_f32_16x16x32_bf16 a[48:51],a[132:135],v[68:71],a[48:51]
v_accvgpr_read_b32 v228,a36
v_accvgpr_read_b32 v229,a37
v_mfma_f32_16x16x32_bf16 a[48:51],a[136:139],v[72:75],a[48:51]
v_cvt_pk_bf16_f32 v228,v228,v229
v_accvgpr_read_b32 v230,a38
v_mfma_f32_16x16x32_bf16 a[48:51],a[140:143],v[76:79],a[48:51]
v_accvgpr_read_b32 v231,a39
v_cvt_pk_bf16_f32 v229,v230,v231
v_mfma_f32_16x16x32_bf16 a[52:55],a[140:143],v[92:95],a[52:55]
ds_read_b64_tr_b16 a[118:119],v45 offset:15648
ds_read_b64_tr_b16 a[120:121],v45 offset:20768
v_mfma_f32_16x16x32_bf16 a[52:55],a[136:139],v[88:91],a[52:55]
v_accvgpr_read_b32 v240,a40
v_accvgpr_read_b32 v241,a41
v_mfma_f32_16x16x32_bf16 a[52:55],a[132:135],v[84:87],a[52:55]
v_cvt_pk_bf16_f32 v240,v240,v241
v_accvgpr_read_b32 v242,a42
v_mfma_f32_16x16x32_bf16 a[52:55],a[128:131],v[80:83],a[52:55]
v_accvgpr_read_b32 v243,a43
v_cvt_pk_bf16_f32 v241,v242,v243
v_mfma_f32_16x16x32_bf16 a[56:59],a[128:131],v[96:99],a[56:59]
ds_read_b64_tr_b16 a[122:123],v45 offset:25888
ds_read_b64_tr_b16 a[124:125],v45 offset:31008
v_mfma_f32_16x16x32_bf16 a[56:59],a[132:135],v[100:103],a[56:59]
ds_read_b64_tr_b16 a[126:127],v45 offset:36128
v_mfma_f32_16x16x32_bf16 a[56:59],a[136:139],v[104:107],a[56:59]
ds_read_b64_tr_b16 a[128:129],v1
v_mfma_f32_16x16x32_bf16 a[56:59],a[140:143],v[108:111],a[56:59]
ds_read_b64_tr_b16 a[130:131],v1 offset:5120
v_mfma_f32_16x16x32_bf16 a[48:51],a[144:147],v[136:139],a[48:51]
ds_read_b64_tr_b16 a[132:133],v1 offset:10240
ds_read_b64_tr_b16 a[134:135],v1 offset:15360
v_mfma_f32_16x16x32_bf16 a[48:51],a[148:151],v[140:143],a[48:51]
ds_read_b64_tr_b16 a[136:137],v1 offset:20480
v_mfma_f32_16x16x32_bf16 a[48:51],a[152:155],v[152:155],a[48:51]
ds_read_b64_tr_b16 a[138:139],v1 offset:25600
v_mfma_f32_16x16x32_bf16 a[48:51],a[156:159],v[156:159],a[48:51]
ds_read_b64_tr_b16 a[140:141],v1 offset:30720
v_mfma_f32_16x16x32_bf16 a[52:55],a[156:159],v[188:191],a[52:55]
ds_read_b64_tr_b16 a[142:143],v1 offset:35840
v_mfma_f32_16x16x32_bf16 a[52:55],a[152:155],v[184:187],a[52:55]
v_mfma_f32_16x16x32_bf16 a[52:55],a[148:151],v[172:175],a[52:55]
v_mfma_f32_16x16x32_bf16 a[52:55],a[144:147],v[168:171],a[52:55]
v_mfma_f32_16x16x32_bf16 a[56:59],a[144:147],v[200:203],a[56:59]
ds_read_b64_tr_b16 a[144:145],v42
ds_read_b64_tr_b16 a[146:147],v42 offset:5120
v_mfma_f32_16x16x32_bf16 a[56:59],a[148:151],v[204:207],a[56:59]
v_accvgpr_read_b32 v226,a48
v_accvgpr_read_b32 v227,a49
v_mfma_f32_16x16x32_bf16 a[56:59],a[152:155],v[216:219],a[56:59]
v_accvgpr_read_b32 v12,a50
v_accvgpr_read_b32 v13,a51
v_mfma_f32_16x16x32_bf16 a[56:59],a[156:159],v[220:223],a[56:59]
v_cvt_pk_bf16_f32 v226,v226,v227
v_cvt_pk_bf16_f32 v227,v12,v13
v_mfma_f32_16x16x32_bf16 a[64:67],a[160:163],v[64:67],a[64:67]
buffer_store_dwordx4 v[224:227],v6,s[32:35],s84 offen offset:3072 sc1
v_mfma_f32_16x16x32_bf16 a[64:67],a[164:167],v[68:71],a[64:67]
v_accvgpr_read_b32 v230,a52
v_accvgpr_read_b32 v231,a53
v_mfma_f32_16x16x32_bf16 a[64:67],a[168:171],v[72:75],a[64:67]
v_accvgpr_read_b32 v14,a54
v_accvgpr_read_b32 v15,a55
v_mfma_f32_16x16x32_bf16 a[64:67],a[172:175],v[76:79],a[64:67]
v_cvt_pk_bf16_f32 v230,v230,v231
v_cvt_pk_bf16_f32 v231,v14,v15
v_mfma_f32_16x16x32_bf16 a[68:71],a[172:175],v[92:95],a[68:71]
buffer_store_dwordx4 v[228:231],v6,s[32:35],s85 offen offset:3072 sc1
v_mfma_f32_16x16x32_bf16 a[68:71],a[168:171],v[88:91],a[68:71]
v_accvgpr_read_b32 v242,a56
v_accvgpr_read_b32 v243,a57
v_mfma_f32_16x16x32_bf16 a[68:71],a[164:167],v[84:87],a[68:71]
v_accvgpr_read_b32 v16,a58
v_accvgpr_read_b32 v17,a59
v_mfma_f32_16x16x32_bf16 a[68:71],a[160:163],v[80:83],a[68:71]
v_cvt_pk_bf16_f32 v242,v242,v243
v_cvt_pk_bf16_f32 v243,v16,v17
v_mfma_f32_16x16x32_bf16 a[72:75],a[160:163],v[96:99],a[72:75]
buffer_store_dwordx4 v[240:243],v6,s[32:35],s86 offen offset:3072 sc1
v_mfma_f32_16x16x32_bf16 a[72:75],a[164:167],v[100:103],a[72:75]
ds_read_b64_tr_b16 a[148:149],v42 offset:10240
ds_read_b64_tr_b16 a[150:151],v42 offset:15360
v_mfma_f32_16x16x32_bf16 a[72:75],a[168:171],v[104:107],a[72:75]
ds_read_b64_tr_b16 a[152:153],v42 offset:20480
v_mfma_f32_16x16x32_bf16 a[72:75],a[172:175],v[108:111],a[72:75]
ds_read_b64_tr_b16 a[154:155],v42 offset:25600
v_mfma_f32_16x16x32_bf16 a[64:67],a[176:179],v[136:139],a[64:67]
)A"
R"A(s_waitcnt lgkmcnt(4)
ds_read_b64_tr_b16 a[156:157],v42 offset:30720
v_mfma_f32_16x16x32_bf16 a[64:67],a[180:183],v[140:143],a[64:67]
ds_read_b64_tr_b16 a[158:159],v42 offset:35840
ds_read_b64_tr_b16 a[160:161],v44
v_mfma_f32_16x16x32_bf16 a[64:67],a[184:187],v[152:155],a[64:67]
ds_read_b64_tr_b16 a[162:163],v44 offset:5120
v_mfma_f32_16x16x32_bf16 a[64:67],a[188:191],v[156:159],a[64:67]
ds_read_b64_tr_b16 a[164:165],v44 offset:10240
v_mfma_f32_16x16x32_bf16 a[68:71],a[188:191],v[188:191],a[68:71]
ds_read_b64_tr_b16 a[166:167],v44 offset:15360
v_mfma_f32_16x16x32_bf16 a[68:71],a[184:187],v[184:187],a[68:71]
ds_read_b64_tr_b16 a[168:169],v44 offset:20480
ds_read_b64_tr_b16 a[170:171],v44 offset:25600
v_mfma_f32_16x16x32_bf16 a[68:71],a[180:183],v[172:175],a[68:71]
ds_read_b64_tr_b16 a[172:173],v44 offset:30720
v_mfma_f32_16x16x32_bf16 a[68:71],a[176:179],v[168:171],a[68:71]
ds_read_b64_tr_b16 a[174:175],v44 offset:35840
v_mfma_f32_16x16x32_bf16 a[72:75],a[176:179],v[200:203],a[72:75]
ds_read_b64_tr_b16 a[176:177],v2 offset:32
v_mfma_f32_16x16x32_bf16 a[72:75],a[180:183],v[204:207],a[72:75]
v_accvgpr_read_b32 v128,a64
v_accvgpr_read_b32 v129,a65
v_mfma_f32_16x16x32_bf16 a[72:75],a[184:187],v[216:219],a[72:75]
v_cvt_pk_bf16_f32 v128,v128,v129
v_accvgpr_read_b32 v130,a66
v_mfma_f32_16x16x32_bf16 a[72:75],a[188:191],v[220:223],a[72:75]
v_accvgpr_read_b32 v131,a67
v_cvt_pk_bf16_f32 v129,v130,v131
v_mfma_f32_16x16x32_bf16 a[80:83],a[96:99],v[64:67],a[80:83]
ds_read_b64_tr_b16 a[178:179],v2 offset:5152
ds_read_b64_tr_b16 a[180:181],v2 offset:10272
v_mfma_f32_16x16x32_bf16 a[80:83],a[100:103],v[68:71],a[80:83]
v_accvgpr_read_b32 v132,a68
v_accvgpr_read_b32 v133,a69
v_mfma_f32_16x16x32_bf16 a[80:83],a[104:107],v[72:75],a[80:83]
v_cvt_pk_bf16_f32 v132,v132,v133
v_accvgpr_read_b32 v134,a70
v_mfma_f32_16x16x32_bf16 a[80:83],a[108:111],v[76:79],a[80:83]
ds_read_b64_tr_b16 a[182:183],v2 offset:15392
v_accvgpr_read_b32 v135,a71
v_mfma_f32_16x16x32_bf16 a[84:87],a[108:111],v[92:95],a[84:87]
ds_read_b64_tr_b16 a[184:185],v2 offset:20512
v_cvt_pk_bf16_f32 v133,v134,v135
v_mfma_f32_16x16x32_bf16 a[84:87],a[104:107],v[88:91],a[84:87]
v_accvgpr_read_b32 v144,a72
v_accvgpr_read_b32 v145,a73
v_mfma_f32_16x16x32_bf16 a[84:87],a[100:103],v[84:87],a[84:87]
v_cvt_pk_bf16_f32 v144,v144,v145
v_accvgpr_read_b32 v146,a74
v_mfma_f32_16x16x32_bf16 a[84:87],a[96:99],v[80:83],a[84:87]
v_accvgpr_read_b32 v147,a75
v_cvt_pk_bf16_f32 v145,v146,v147
v_mfma_f32_16x16x32_bf16 a[88:91],a[96:99],v[96:99],a[88:91]
ds_read_b64_tr_b16 a[186:187],v2 offset:25632
ds_read_b64_tr_b16 a[188:189],v2 offset:30752
v_mfma_f32_16x16x32_bf16 a[88:91],a[100:103],v[100:103],a[88:91]
ds_read_b64_tr_b16 a[190:191],v2 offset:35872
v_mfma_f32_16x16x32_bf16 a[88:91],a[104:107],v[104:107],a[88:91]
ds_read_b64_tr_b16 a[96:97],v43 offset:32
v_mfma_f32_16x16x32_bf16 a[88:91],a[108:111],v[108:111],a[88:91]
ds_read_b64_tr_b16 a[98:99],v43 offset:5152
v_mfma_f32_16x16x32_bf16 a[80:83],a[112:115],v[136:139],a[80:83]
ds_read_b64_tr_b16 a[100:101],v43 offset:10272
ds_read_b64_tr_b16 a[102:103],v43 offset:15392
v_mfma_f32_16x16x32_bf16 a[80:83],a[116:119],v[140:143],a[80:83]
ds_read_b64_tr_b16 a[104:105],v43 offset:20512
v_mfma_f32_16x16x32_bf16 a[80:83],a[120:123],v[152:155],a[80:83]
ds_read_b64_tr_b16 a[106:107],v43 offset:25632
v_mfma_f32_16x16x32_bf16 a[80:83],a[124:127],v[156:159],a[80:83]
ds_read_b64_tr_b16 a[108:109],v43 offset:30752
v_mfma_f32_16x16x32_bf16 a[84:87],a[124:127],v[188:191],a[84:87]
ds_read_b64_tr_b16 a[110:111],v43 offset:35872
v_mfma_f32_16x16x32_bf16 a[84:87],a[120:123],v[184:187],a[84:87]
v_mfma_f32_16x16x32_bf16 a[84:87],a[116:119],v[172:175],a[84:87]
v_mfma_f32_16x16x32_bf16 a[84:87],a[112:115],v[168:171],a[84:87]
v_mfma_f32_16x16x32_bf16 a[88:91],a[112:115],v[200:203],a[88:91]
ds_read_b64_tr_b16 a[112:113],v45 offset:32
ds_read_b64_tr_b16 a[114:115],v45 offset:5152
v_mfma_f32_16x16x32_bf16 a[88:91],a[116:119],v[204:207],a[88:91]
v_accvgpr_read_b32 v130,a80
v_accvgpr_read_b32 v131,a81
v_mfma_f32_16x16x32_bf16 a[88:91],a[120:123],v[216:219],a[88:91]
v_accvgpr_read_b32 v12,a82
v_accvgpr_read_b32 v13,a83
v_mfma_f32_16x16x32_bf16 a[88:91],a[124:127],v[220:223],a[88:91]
v_cvt_pk_bf16_f32 v130,v130,v131
v_cvt_pk_bf16_f32 v131,v12,v13
v_mfma_f32_16x16x32_bf16 a[204:207],a[128:131],v[148:151],a[204:207]
s_lshl_b32 s94,s71,2
buffer_store_dwordx4 v[128:131],v25,s[32:35],s84 offen sc1
v_mfma_f32_16x16x32_bf16 a[204:207],a[132:135],v[180:183],a[204:207]
s_add_u32 s94,s94,s40
v_accvgpr_read_b32 v134,a84
v_accvgpr_read_b32 v135,a85
v_mfma_f32_16x16x32_bf16 a[204:207],a[136:139],v[212:215],a[204:207]
s_lshl_b32 s95,s42,2
s_add_u32 s95,s95,s40
s_lshl_b32 s95,s95,4
v_mfma_f32_16x16x32_bf16 a[204:207],a[140:143],v[244:247],a[204:207]
s_lshl_b32 s94,s94,4
v_mov_b32 v18,s68
v_mov_b32 v19,s69
v_mfma_f32_16x16x32_bf16 a[204:207],a[144:147],v[112:115],a[204:207]
s_waitcnt vmcnt(1)
s_mov_b64 exec,1
buffer_store_dwordx2 v[18:19],v6,s[64:67],s95 offen offset:2048 sc1
s_mov_b64 exec,-1
s_waitcnt lgkmcnt(0)
v_mfma_f32_16x16x32_bf16 a[204:207],a[148:151],v[116:119],a[204:207]
v_accvgpr_read_b32 v146,a88
v_accvgpr_read_b32 v147,a89
v_mfma_f32_16x16x32_bf16 a[204:207],a[152:155],v[120:123],a[204:207]
v_accvgpr_read_b32 v14,a86
v_accvgpr_read_b32 v15,a87
v_cvt_pk_bf16_f32 v134,v134,v135
v_accvgpr_read_b32 v16,a90
v_accvgpr_read_b32 v17,a91
v_cvt_pk_bf16_f32 v146,v146,v147
v_cvt_pk_bf16_f32 v135,v14,v15
v_cvt_pk_bf16_f32 v147,v16,v17
v_mfma_f32_16x16x32_bf16 a[204:207],a[156:159],v[124:127],a[204:207]
ds_read_b64_tr_b16 a[116:117],v45 offset:10272
ds_read_b64_tr_b16 a[118:119],v45 offset:15392
v_mfma_f32_16x16x32_bf16 a[204:207],a[160:163],v[232:235],a[204:207]
buffer_store_dwordx4 v[132:135],v25,s[32:35],s85 offen sc1
v_mfma_f32_16x16x32_bf16 a[204:207],a[164:167],v[236:239],a[204:207]
ds_read_b64_tr_b16 a[120:121],v45 offset:20512
ds_read_b64_tr_b16 a[122:123],v45 offset:25632
v_mfma_f32_16x16x32_bf16 a[204:207],a[168:171],v[248:251],a[204:207]
ds_read_b64_tr_b16 a[124:125],v45 offset:30752
ds_read_b64_tr_b16 a[126:127],v45 offset:35872
v_mfma_f32_16x16x32_bf16 a[204:207],a[172:175],v[252:255],a[204:207]
ds_read_b64_tr_b16 a[128:129],v1 offset:64
ds_read_b64_tr_b16 a[130:131],v1 offset:5184
v_mfma_f32_16x16x32_bf16 a[220:223],a[176:179],v[148:151],a[220:223]
buffer_store_dwordx4 v[144:147],v25,s[32:35],s86 offen sc1
v_mfma_f32_16x16x32_bf16 a[220:223],a[180:183],v[180:183],a[220:223]
ds_read_b64_tr_b16 a[132:133],v1 offset:10304
ds_read_b64_tr_b16 a[134:135],v1 offset:15424
v_mfma_f32_16x16x32_bf16 a[220:223],a[184:187],v[212:215],a[220:223]
ds_read_b64_tr_b16 a[136:137],v1 offset:20544
ds_read_b64_tr_b16 a[138:139],v1 offset:25664
v_mfma_f32_16x16x32_bf16 a[220:223],a[188:191],v[244:247],a[220:223]
ds_read_b64_tr_b16 a[140:141],v1 offset:30784
ds_read_b64_tr_b16 a[142:143],v1 offset:35904
v_mfma_f32_16x16x32_bf16 a[220:223],a[96:99],v[112:115],a[220:223]
ds_read_b64_tr_b16 a[144:145],v42 offset:64
ds_read_b64_tr_b16 a[146:147],v42 offset:5184
v_mfma_f32_16x16x32_bf16 a[220:223],a[100:103],v[116:119],a[220:223]
ds_read_b64_tr_b16 a[148:149],v42 offset:10304
ds_read_b64_tr_b16 a[150:151],v42 offset:15424
v_mfma_f32_16x16x32_bf16 a[220:223],a[104:107],v[120:123],a[220:223]
ds_read_b64_tr_b16 a[152:153],v42 offset:20544
ds_read_b64_tr_b16 a[154:155],v42 offset:25664
v_mfma_f32_16x16x32_bf16 a[220:223],a[108:111],v[124:127],a[220:223]
ds_read_b64_tr_b16 a[156:157],v42 offset:30784
ds_read_b64_tr_b16 a[158:159],v42 offset:35904
v_mfma_f32_16x16x32_bf16 a[220:223],a[112:115],v[232:235],a[220:223]
s_waitcnt lgkmcnt(14)
ds_read_b64_tr_b16 a[160:161],v44 offset:64
ds_read_b64_tr_b16 a[162:163],v44 offset:5184
v_mfma_f32_16x16x32_bf16 a[220:223],a[116:119],v[236:239],a[220:223]
ds_read_b64_tr_b16 a[164:165],v44 offset:10304
ds_read_b64_tr_b16 a[166:167],v44 offset:15424
v_mfma_f32_16x16x32_bf16 a[220:223],a[120:123],v[248:251],a[220:223]
ds_read_b64_tr_b16 a[168:169],v44 offset:20544
ds_read_b64_tr_b16 a[170:171],v44 offset:25664
v_mfma_f32_16x16x32_bf16 a[220:223],a[124:127],v[252:255],a[220:223]
ds_read_b64_tr_b16 a[172:173],v44 offset:30784
ds_read_b64_tr_b16 a[174:175],v44 offset:35904
v_mfma_f32_16x16x32_bf16 a[236:239],a[128:131],v[148:151],a[236:239]
s_waitcnt lgkmcnt(14)
ds_read_b64_tr_b16 a[176:177],v2 offset:96
ds_read_b64_tr_b16 a[178:179],v2 offset:5216
v_mfma_f32_16x16x32_bf16 a[236:239],a[132:135],v[180:183],a[236:239]
v_bfe_u32 v24,v6,4,2
v_min_u32 v24,2,v24
v_mfma_f32_16x16x32_bf16 a[236:239],a[136:139],v[212:215],a[236:239]
ds_read_b64_tr_b16 a[180:181],v2 offset:10336
ds_read_b64_tr_b16 a[182:183],v2 offset:15456
v_mfma_f32_16x16x32_bf16 a[236:239],a[140:143],v[244:247],a[236:239]
v_add_u32 v24,s70,v24
v_add_u32 v24,1,v24
v_mfma_f32_16x16x32_bf16 a[236:239],a[144:147],v[112:115],a[236:239]
s_waitcnt lgkmcnt(10)
ds_read_b64_tr_b16 a[184:185],v2 offset:20576
ds_read_b64_tr_b16 a[186:187],v2 offset:25696
v_mfma_f32_16x16x32_bf16 a[236:239],a[148:151],v[116:119],a[236:239]
v_and_b32 v24,3,v24
v_lshlrev_b32 v24,8,v24
v_mfma_f32_16x16x32_bf16 a[236:239],a[152:155],v[120:123],a[236:239]
ds_read_b64_tr_b16 a[188:189],v2 offset:30816
ds_read_b64_tr_b16 a[190:191],v2 offset:35936
v_mfma_f32_16x16x32_bf16 a[236:239],a[156:159],v[124:127],a[236:239]
ds_read_b64_tr_b16 a[96:97],v43 offset:96
ds_read_b64_tr_b16 a[98:99],v43 offset:5216
v_mfma_f32_16x16x32_bf16 a[236:239],a[160:163],v[232:235],a[236:239]
s_waitcnt lgkmcnt(8)
ds_read_b64_tr_b16 a[100:101],v43 offset:10336
ds_read_b64_tr_b16 a[102:103],v43 offset:15456
v_mfma_f32_16x16x32_bf16 a[236:239],a[164:167],v[236:239],a[236:239]
ds_read_b64_tr_b16 a[104:105],v43 offset:20576
ds_read_b64_tr_b16 a[106:107],v43 offset:25696
v_mfma_f32_16x16x32_bf16 a[236:239],a[168:171],v[248:251],a[236:239]
ds_read_b64_tr_b16 a[108:109],v43 offset:30816
ds_read_b64_tr_b16 a[110:111],v43 offset:35936
v_mfma_f32_16x16x32_bf16 a[236:239],a[172:175],v[252:255],a[236:239]
ds_read_b64_tr_b16 a[112:113],v45 offset:96
ds_read_b64_tr_b16 a[114:115],v45 offset:5216
v_mfma_f32_16x16x32_bf16 a[252:255],a[176:179],v[148:151],a[252:255]
s_waitcnt lgkmcnt(12)
ds_read_b64_tr_b16 a[116:117],v45 offset:10336
ds_read_b64_tr_b16 a[118:119],v45 offset:15456
v_mfma_f32_16x16x32_bf16 a[252:255],a[180:183],v[180:183],a[252:255]
ds_read_b64_tr_b16 a[120:121],v45 offset:20576
ds_read_b64_tr_b16 a[122:123],v45 offset:25696
v_mfma_f32_16x16x32_bf16 a[252:255],a[184:187],v[212:215],a[252:255]
s_waitcnt lgkmcnt(14)
ds_read_b64_tr_b16 a[124:125],v45 offset:30816
ds_read_b64_tr_b16 a[126:127],v45 offset:35936
v_mfma_f32_16x16x32_bf16 a[252:255],a[188:191],v[244:247],a[252:255]
s_waitcnt lgkmcnt(14)
ds_read_b64_tr_b16 a[128:129],v1 offset:128
ds_read_b64_tr_b16 a[130:131],v1 offset:5248
v_mfma_f32_16x16x32_bf16 a[252:255],a[96:99],v[112:115],a[252:255]
s_waitcnt lgkmcnt(14)
buffer_load_dwordx2 v[26:27],v24,s[64:67],s94 offen offset:2048 sc1
ds_read_b64_tr_b16 a[132:133],v1 offset:10368
ds_read_b64_tr_b16 a[134:135],v1 offset:15488
v_mfma_f32_16x16x32_bf16 a[252:255],a[100:103],v[116:119],a[252:255]
s_waitcnt lgkmcnt(14)
ds_read_b64_tr_b16 a[136:137],v1 offset:20608
ds_read_b64_tr_b16 a[138:139],v1 offset:25728
v_mfma_f32_16x16x32_bf16 a[252:255],a[104:107],v[120:123],a[252:255]
s_waitcnt lgkmcnt(14)
ds_read_b64_tr_b16 a[140:141],v1 offset:30848
ds_read_b64_tr_b16 a[142:143],v1 offset:35968
v_mfma_f32_16x16x32_bf16 a[252:255],a[108:111],v[124:127],a[252:255]
s_waitcnt lgkmcnt(14)
ds_read_b64_tr_b16 a[144:145],v42 offset:128
ds_read_b64_tr_b16 a[146:147],v42 offset:5248
v_mfma_f32_16x16x32_bf16 a[252:255],a[112:115],v[232:235],a[252:255]
s_waitcnt lgkmcnt(14)
ds_read_b64_tr_b16 a[148:149],v42 offset:10368
ds_read_b64_tr_b16 a[150:151],v42 offset:15488
v_mfma_f32_16x16x32_bf16 a[252:255],a[116:119],v[236:239],a[252:255]
s_lshl_b32 s95,s42,2
ds_read_b64_tr_b16 a[152:153],v42 offset:20608
ds_read_b64_tr_b16 a[154:155],v42 offset:25728
s_waitcnt lgkmcnt(15)
v_mfma_f32_16x16x32_bf16 a[252:255],a[120:123],v[248:251],a[252:255]
s_add_u32 s95,s95,s40
s_lshl_b32 s95,s95,4
)A"
R"A(ds_read_b64_tr_b16 a[156:157],v42 offset:30848
ds_read_b64_tr_b16 a[158:159],v42 offset:35968
v_mov_b32 v18,s68
v_mov_b32 v19,s69
s_waitcnt lgkmcnt(15)
v_mfma_f32_16x16x32_bf16 a[252:255],a[124:127],v[252:255],a[252:255]
s_waitcnt vmcnt(1)
s_mov_b64 exec,1
buffer_store_dwordx2 v[18:19],v6,s[64:67],s95 offen offset:3072 sc1
s_mov_b64 exec,-1
ds_read_b64_tr_b16 a[160:161],v44 offset:128
ds_read_b64_tr_b16 a[162:163],v44 offset:5248
s_waitcnt lgkmcnt(15)
v_mfma_f32_16x16x32_bf16 a[12:15],a[128:131],v[148:151],a[12:15]
s_waitcnt lgkmcnt(14)
ds_read_b64_tr_b16 a[164:165],v44 offset:10368
ds_read_b64_tr_b16 a[166:167],v44 offset:15488
v_mfma_f32_16x16x32_bf16 a[12:15],a[132:135],v[180:183],a[12:15]
s_waitcnt lgkmcnt(14)
ds_read_b64_tr_b16 a[168:169],v44 offset:20608
ds_read_b64_tr_b16 a[170:171],v44 offset:25728
v_mfma_f32_16x16x32_bf16 a[12:15],a[136:139],v[212:215],a[12:15]
s_waitcnt lgkmcnt(14)
ds_read_b64_tr_b16 a[172:173],v44 offset:30848
ds_read_b64_tr_b16 a[174:175],v44 offset:35968
v_mfma_f32_16x16x32_bf16 a[12:15],a[140:143],v[244:247],a[12:15]
s_waitcnt lgkmcnt(14)
ds_read_b64_tr_b16 a[176:177],v2 offset:160
ds_read_b64_tr_b16 a[178:179],v2 offset:5280
v_mfma_f32_16x16x32_bf16 a[12:15],a[144:147],v[112:115],a[12:15]
s_waitcnt lgkmcnt(14)
ds_read_b64_tr_b16 a[180:181],v2 offset:10400
ds_read_b64_tr_b16 a[182:183],v2 offset:15520
v_mfma_f32_16x16x32_bf16 a[12:15],a[148:151],v[116:119],a[12:15]
s_waitcnt lgkmcnt(14)
ds_read_b64_tr_b16 a[184:185],v2 offset:20640
ds_read_b64_tr_b16 a[186:187],v2 offset:25760
v_mfma_f32_16x16x32_bf16 a[12:15],a[152:155],v[120:123],a[12:15]
s_waitcnt lgkmcnt(14)
ds_read_b64_tr_b16 a[188:189],v2 offset:30880
ds_read_b64_tr_b16 a[190:191],v2 offset:36000
v_mfma_f32_16x16x32_bf16 a[12:15],a[156:159],v[124:127],a[12:15]
s_waitcnt lgkmcnt(14)
ds_read_b64_tr_b16 a[96:97],v43 offset:160
ds_read_b64_tr_b16 a[98:99],v43 offset:5280
v_mfma_f32_16x16x32_bf16 a[12:15],a[160:163],v[232:235],a[12:15]
s_waitcnt lgkmcnt(14)
ds_read_b64_tr_b16 a[100:101],v43 offset:10400
ds_read_b64_tr_b16 a[102:103],v43 offset:15520
v_mfma_f32_16x16x32_bf16 a[12:15],a[164:167],v[236:239],a[12:15]
s_waitcnt lgkmcnt(14)
ds_read_b64_tr_b16 a[104:105],v43 offset:20640
ds_read_b64_tr_b16 a[106:107],v43 offset:25760
v_mfma_f32_16x16x32_bf16 a[12:15],a[168:171],v[248:251],a[12:15]
s_waitcnt lgkmcnt(14)
ds_read_b64_tr_b16 a[108:109],v43 offset:30880
ds_read_b64_tr_b16 a[110:111],v43 offset:36000
v_mfma_f32_16x16x32_bf16 a[12:15],a[172:175],v[252:255],a[12:15]
s_waitcnt lgkmcnt(14)
ds_read_b64_tr_b16 a[112:113],v45 offset:160
ds_read_b64_tr_b16 a[114:115],v45 offset:5280
v_mfma_f32_16x16x32_bf16 a[28:31],a[176:179],v[148:151],a[28:31]
s_waitcnt lgkmcnt(14)
ds_read_b64_tr_b16 a[116:117],v45 offset:10400
ds_read_b64_tr_b16 a[118:119],v45 offset:15520
v_mfma_f32_16x16x32_bf16 a[28:31],a[180:183],v[180:183],a[28:31]
s_waitcnt lgkmcnt(14)
ds_read_b64_tr_b16 a[120:121],v45 offset:20640
ds_read_b64_tr_b16 a[122:123],v45 offset:25760
v_mfma_f32_16x16x32_bf16 a[28:31],a[184:187],v[212:215],a[28:31]
s_waitcnt lgkmcnt(14)
ds_read_b64_tr_b16 a[124:125],v45 offset:30880
ds_read_b64_tr_b16 a[126:127],v45 offset:36000
v_mfma_f32_16x16x32_bf16 a[28:31],a[188:191],v[244:247],a[28:31]
s_waitcnt lgkmcnt(14)
ds_read_b64_tr_b16 a[128:129],v1 offset:192
ds_read_b64_tr_b16 a[130:131],v1 offset:5312
v_mfma_f32_16x16x32_bf16 a[28:31],a[96:99],v[112:115],a[28:31]
s_waitcnt lgkmcnt(14)
ds_read_b64_tr_b16 a[132:133],v1 offset:10432
ds_read_b64_tr_b16 a[134:135],v1 offset:15552
v_mfma_f32_16x16x32_bf16 a[28:31],a[100:103],v[116:119],a[28:31]
s_waitcnt lgkmcnt(14)
ds_read_b64_tr_b16 a[136:137],v1 offset:20672
ds_read_b64_tr_b16 a[138:139],v1 offset:25792
v_mfma_f32_16x16x32_bf16 a[28:31],a[104:107],v[120:123],a[28:31]
s_waitcnt lgkmcnt(14)
ds_read_b64_tr_b16 a[140:141],v1 offset:30912
ds_read_b64_tr_b16 a[142:143],v1 offset:36032
v_mfma_f32_16x16x32_bf16 a[28:31],a[108:111],v[124:127],a[28:31]
s_waitcnt lgkmcnt(14)
ds_read_b64_tr_b16 a[144:145],v42 offset:192
ds_read_b64_tr_b16 a[146:147],v42 offset:5312
v_mfma_f32_16x16x32_bf16 a[28:31],a[112:115],v[232:235],a[28:31]
s_waitcnt lgkmcnt(14)
ds_read_b64_tr_b16 a[148:149],v42 offset:10432
ds_read_b64_tr_b16 a[150:151],v42 offset:15552
v_mfma_f32_16x16x32_bf16 a[28:31],a[116:119],v[236:239],a[28:31]
s_waitcnt lgkmcnt(14)
ds_read_b64_tr_b16 a[152:153],v42 offset:20672
ds_read_b64_tr_b16 a[154:155],v42 offset:25792
v_mfma_f32_16x16x32_bf16 a[28:31],a[120:123],v[248:251],a[28:31]
s_waitcnt lgkmcnt(14)
ds_read_b64_tr_b16 a[156:157],v42 offset:30912
ds_read_b64_tr_b16 a[158:159],v42 offset:36032
v_mfma_f32_16x16x32_bf16 a[28:31],a[124:127],v[252:255],a[28:31]
s_waitcnt lgkmcnt(14)
ds_read_b64_tr_b16 a[160:161],v44 offset:192
ds_read_b64_tr_b16 a[162:163],v44 offset:5312
v_mfma_f32_16x16x32_bf16 a[44:47],a[128:131],v[148:151],a[44:47]
ds_read_b64_tr_b16 a[164:165],v44 offset:10432
ds_read_b64_tr_b16 a[166:167],v44 offset:15552
s_waitcnt vmcnt(1)
v_cmp_ne_u32 s[90:91],s68,v26
v_cmp_ne_u32 s[92:93],s69,v27
s_nop 0
s_or_b64 s[90:91],s[90:91],s[92:93]
s_cbranch_scc1 L_fr10_%=
L_fo10_%=:
s_waitcnt lgkmcnt(15)
v_mfma_f32_16x16x32_bf16 a[44:47],a[132:135],v[180:183],a[44:47]
s_waitcnt lgkmcnt(14)
buffer_load_dwordx4 v[64:67],v6,s[32:35],s76 offen sc1
ds_read_b64_tr_b16 a[168:169],v44 offset:20672
ds_read_b64_tr_b16 a[170:171],v44 offset:25792
v_mfma_f32_16x16x32_bf16 a[44:47],a[136:139],v[212:215],a[44:47]
s_lshl_b32 s94,s71,2
ds_read_b64_tr_b16 a[172:173],v44 offset:30912
ds_read_b64_tr_b16 a[174:175],v44 offset:36032
s_waitcnt lgkmcnt(15)
v_mfma_f32_16x16x32_bf16 a[44:47],a[140:143],v[244:247],a[44:47]
s_add_u32 s94,s94,s40
ds_read_b64_tr_b16 a[176:177],v2 offset:224
ds_read_b64_tr_b16 a[178:179],v2 offset:5344
s_waitcnt lgkmcnt(15)
v_mfma_f32_16x16x32_bf16 a[44:47],a[144:147],v[112:115],a[44:47]
s_lshl_b32 s94,s94,4
ds_read_b64_tr_b16 a[180:181],v2 offset:10464
ds_read_b64_tr_b16 a[182:183],v2 offset:15584
s_waitcnt lgkmcnt(15)
v_mfma_f32_16x16x32_bf16 a[44:47],a[148:151],v[116:119],a[44:47]
s_waitcnt lgkmcnt(14)
buffer_load_dwordx4 v[80:83],v6,s[32:35],s77 offen sc1
ds_read_b64_tr_b16 a[184:185],v2 offset:20704
ds_read_b64_tr_b16 a[186:187],v2 offset:25824
v_mfma_f32_16x16x32_bf16 a[44:47],a[152:155],v[120:123],a[44:47]
s_waitcnt lgkmcnt(14)
ds_read_b64_tr_b16 a[188:189],v2 offset:30944
ds_read_b64_tr_b16 a[190:191],v2 offset:36064
v_mfma_f32_16x16x32_bf16 a[44:47],a[156:159],v[124:127],a[44:47]
s_waitcnt lgkmcnt(14)
ds_read_b64_tr_b16 a[96:97],v43 offset:224
ds_read_b64_tr_b16 a[98:99],v43 offset:5344
v_mfma_f32_16x16x32_bf16 a[44:47],a[160:163],v[232:235],a[44:47]
s_waitcnt lgkmcnt(14)
ds_read_b64_tr_b16 a[100:101],v43 offset:10464
ds_read_b64_tr_b16 a[102:103],v43 offset:15584
v_mfma_f32_16x16x32_bf16 a[44:47],a[164:167],v[236:239],a[44:47]
s_waitcnt lgkmcnt(14)
buffer_load_dwordx4 v[96:99],v6,s[32:35],s78 offen sc1
ds_read_b64_tr_b16 a[104:105],v43 offset:20704
ds_read_b64_tr_b16 a[106:107],v43 offset:25824
v_mfma_f32_16x16x32_bf16 a[44:47],a[168:171],v[248:251],a[44:47]
s_waitcnt lgkmcnt(14)
ds_read_b64_tr_b16 a[108:109],v43 offset:30944
ds_read_b64_tr_b16 a[110:111],v43 offset:36064
v_mfma_f32_16x16x32_bf16 a[44:47],a[172:175],v[252:255],a[44:47]
s_waitcnt lgkmcnt(14)
ds_read_b64_tr_b16 a[112:113],v45 offset:224
ds_read_b64_tr_b16 a[114:115],v45 offset:5344
v_mfma_f32_16x16x32_bf16 a[60:63],a[176:179],v[148:151],a[60:63]
s_waitcnt lgkmcnt(14)
ds_read_b64_tr_b16 a[116:117],v45 offset:10464
ds_read_b64_tr_b16 a[118:119],v45 offset:15584
v_mfma_f32_16x16x32_bf16 a[60:63],a[180:183],v[180:183],a[60:63]
s_waitcnt lgkmcnt(14)
buffer_load_dwordx4 v[68:71],v6,s[32:35],s76 offen offset:1024 sc1
ds_read_b64_tr_b16 a[120:121],v45 offset:20704
ds_read_b64_tr_b16 a[122:123],v45 offset:25824
v_mfma_f32_16x16x32_bf16 a[60:63],a[184:187],v[212:215],a[60:63]
s_waitcnt lgkmcnt(14)
ds_read_b64_tr_b16 a[124:125],v45 offset:30944
ds_read_b64_tr_b16 a[126:127],v45 offset:36064
v_mfma_f32_16x16x32_bf16 a[60:63],a[188:191],v[244:247],a[60:63]
s_waitcnt lgkmcnt(14)
ds_read_b64_tr_b16 a[128:129],v1 offset:256
ds_read_b64_tr_b16 a[130:131],v1 offset:5376
v_mfma_f32_16x16x32_bf16 a[60:63],a[96:99],v[112:115],a[60:63]
s_waitcnt lgkmcnt(14)
ds_read_b64_tr_b16 a[132:133],v1 offset:10496
ds_read_b64_tr_b16 a[134:135],v1 offset:15616
v_mfma_f32_16x16x32_bf16 a[60:63],a[100:103],v[116:119],a[60:63]
s_waitcnt lgkmcnt(14)
buffer_load_dwordx4 v[84:87],v6,s[32:35],s77 offen offset:1024 sc1
ds_read_b64_tr_b16 a[136:137],v1 offset:20736
ds_read_b64_tr_b16 a[138:139],v1 offset:25856
v_mfma_f32_16x16x32_bf16 a[60:63],a[104:107],v[120:123],a[60:63]
s_waitcnt lgkmcnt(14)
ds_read_b64_tr_b16 a[140:141],v1 offset:30976
ds_read_b64_tr_b16 a[142:143],v1 offset:36096
v_mfma_f32_16x16x32_bf16 a[60:63],a[108:111],v[124:127],a[60:63]
s_waitcnt lgkmcnt(14)
ds_read_b64_tr_b16 a[144:145],v42 offset:256
ds_read_b64_tr_b16 a[146:147],v42 offset:5376
v_mfma_f32_16x16x32_bf16 a[60:63],a[112:115],v[232:235],a[60:63]
s_waitcnt lgkmcnt(14)
ds_read_b64_tr_b16 a[148:149],v42 offset:10496
ds_read_b64_tr_b16 a[150:151],v42 offset:15616
v_mfma_f32_16x16x32_bf16 a[60:63],a[116:119],v[236:239],a[60:63]
s_waitcnt lgkmcnt(14)
buffer_load_dwordx4 v[100:103],v6,s[32:35],s78 offen offset:1024 sc1
ds_read_b64_tr_b16 a[152:153],v42 offset:20736
ds_read_b64_tr_b16 a[154:155],v42 offset:25856
v_mfma_f32_16x16x32_bf16 a[60:63],a[120:123],v[248:251],a[60:63]
s_waitcnt lgkmcnt(14)
ds_read_b64_tr_b16 a[156:157],v42 offset:30976
ds_read_b64_tr_b16 a[158:159],v42 offset:36096
v_mfma_f32_16x16x32_bf16 a[60:63],a[124:127],v[252:255],a[60:63]
s_waitcnt lgkmcnt(14)
ds_read_b64_tr_b16 a[160:161],v44 offset:256
ds_read_b64_tr_b16 a[162:163],v44 offset:5376
v_mfma_f32_16x16x32_bf16 a[76:79],a[128:131],v[148:151],a[76:79]
s_waitcnt lgkmcnt(14)
ds_read_b64_tr_b16 a[164:165],v44 offset:10496
ds_read_b64_tr_b16 a[166:167],v44 offset:15616
v_mfma_f32_16x16x32_bf16 a[76:79],a[132:135],v[180:183],a[76:79]
s_waitcnt lgkmcnt(14)
buffer_load_dwordx4 v[72:75],v6,s[32:35],s76 offen offset:2048 sc1
ds_read_b64_tr_b16 a[168:169],v44 offset:20736
ds_read_b64_tr_b16 a[170:171],v44 offset:25856
v_bfe_u32 v24,v6,4,2
v_min_u32 v24,2,v24
v_mfma_f32_16x16x32_bf16 a[76:79],a[136:139],v[212:215],a[76:79]
s_waitcnt lgkmcnt(14)
ds_read_b64_tr_b16 a[172:173],v44 offset:30976
ds_read_b64_tr_b16 a[174:175],v44 offset:36096
v_add_u32 v24,s70,v24
v_add_u32 v24,1,v24
v_mfma_f32_16x16x32_bf16 a[76:79],a[140:143],v[244:247],a[76:79]
s_waitcnt lgkmcnt(14)
ds_read_b64_tr_b16 a[176:177],v2 offset:288
ds_read_b64_tr_b16 a[178:179],v2 offset:5408
v_and_b32 v24,3,v24
v_lshlrev_b32 v24,8,v24
v_mfma_f32_16x16x32_bf16 a[76:79],a[144:147],v[112:115],a[76:79]
s_waitcnt lgkmcnt(14)
ds_read_b64_tr_b16 a[180:181],v2 offset:10528
ds_read_b64_tr_b16 a[182:183],v2 offset:15648
v_mfma_f32_16x16x32_bf16 a[76:79],a[148:151],v[116:119],a[76:79]
s_waitcnt lgkmcnt(14)
buffer_load_dwordx4 v[88:91],v6,s[32:35],s77 offen offset:2048 sc1
ds_read_b64_tr_b16 a[184:185],v2 offset:20768
ds_read_b64_tr_b16 a[186:187],v2 offset:25888
v_mfma_f32_16x16x32_bf16 a[76:79],a[152:155],v[120:123],a[76:79]
s_waitcnt lgkmcnt(14)
ds_read_b64_tr_b16 a[188:189],v2 offset:31008
ds_read_b64_tr_b16 a[190:191],v2 offset:36128
v_mfma_f32_16x16x32_bf16 a[76:79],a[156:159],v[124:127],a[76:79]
s_waitcnt lgkmcnt(14)
ds_read_b64_tr_b16 a[96:97],v43 offset:288
ds_read_b64_tr_b16 a[98:99],v43 offset:5408
v_mfma_f32_16x16x32_bf16 a[76:79],a[160:163],v[232:235],a[76:79]
s_waitcnt lgkmcnt(14)
ds_read_b64_tr_b16 a[100:101],v43 offset:10528
ds_read_b64_tr_b16 a[102:103],v43 offset:15648
v_mfma_f32_16x16x32_bf16 a[76:79],a[164:167],v[236:239],a[76:79]
s_waitcnt lgkmcnt(14)
buffer_load_dwordx4 v[104:107],v6,s[32:35],s78 offen offset:2048 sc1
ds_read_b64_tr_b16 a[104:105],v43 offset:20768
ds_read_b64_tr_b16 a[106:107],v43 offset:25888
v_mfma_f32_16x16x32_bf16 a[76:79],a[168:171],v[248:251],a[76:79]
s_waitcnt lgkmcnt(14)
ds_read_b64_tr_b16 a[108:109],v43 offset:31008
ds_read_b64_tr_b16 a[110:111],v43 offset:36128
v_mfma_f32_16x16x32_bf16 a[76:79],a[172:175],v[252:255],a[76:79]
)A"
R"A(s_waitcnt lgkmcnt(14)
ds_read_b64_tr_b16 a[112:113],v45 offset:288
ds_read_b64_tr_b16 a[114:115],v45 offset:5408
v_mfma_f32_16x16x32_bf16 a[92:95],a[176:179],v[148:151],a[92:95]
s_waitcnt lgkmcnt(14)
ds_read_b64_tr_b16 a[116:117],v45 offset:10528
ds_read_b64_tr_b16 a[118:119],v45 offset:15648
v_mfma_f32_16x16x32_bf16 a[92:95],a[180:183],v[180:183],a[92:95]
s_waitcnt lgkmcnt(14)
buffer_load_dwordx4 v[76:79],v6,s[32:35],s76 offen offset:3072 sc1
ds_read_b64_tr_b16 a[120:121],v45 offset:20768
ds_read_b64_tr_b16 a[122:123],v45 offset:25888
v_mfma_f32_16x16x32_bf16 a[92:95],a[184:187],v[212:215],a[92:95]
s_waitcnt lgkmcnt(14)
ds_read_b64_tr_b16 a[124:125],v45 offset:31008
ds_read_b64_tr_b16 a[126:127],v45 offset:36128
v_mfma_f32_16x16x32_bf16 a[92:95],a[188:191],v[244:247],a[92:95]
s_waitcnt lgkmcnt(14)
v_mfma_f32_16x16x32_bf16 a[92:95],a[96:99],v[112:115],a[92:95]
s_waitcnt lgkmcnt(12)
v_mfma_f32_16x16x32_bf16 a[92:95],a[100:103],v[116:119],a[92:95]
s_waitcnt lgkmcnt(10)
buffer_load_dwordx4 v[92:95],v6,s[32:35],s77 offen offset:3072 sc1
v_mfma_f32_16x16x32_bf16 a[92:95],a[104:107],v[120:123],a[92:95]
s_waitcnt lgkmcnt(8)
v_mfma_f32_16x16x32_bf16 a[92:95],a[108:111],v[124:127],a[92:95]
s_waitcnt lgkmcnt(6)
v_mfma_f32_16x16x32_bf16 a[92:95],a[112:115],v[232:235],a[92:95]
s_waitcnt lgkmcnt(4)
v_mfma_f32_16x16x32_bf16 a[92:95],a[116:119],v[236:239],a[92:95]
s_waitcnt lgkmcnt(2)
buffer_load_dwordx4 v[108:111],v6,s[32:35],s78 offen offset:3072 sc1
v_mfma_f32_16x16x32_bf16 a[92:95],a[120:123],v[248:251],a[92:95]
s_waitcnt lgkmcnt(0)
v_mfma_f32_16x16x32_bf16 a[92:95],a[124:127],v[252:255],a[92:95]
buffer_load_dwordx2 v[26:27],v24,s[64:67],s94 offen offset:3072 sc1
s_waitcnt vmcnt(0)
v_cmp_ne_u32 s[90:91],s68,v26
v_cmp_ne_u32 s[92:93],s69,v27
s_nop 0
s_or_b64 s[90:91],s[90:91],s[92:93]
s_cbranch_scc1 L_fr11_%=
L_fo11_%=:
buffer_load_dwordx4 v[136:139],v25,s[32:35],s76 offen sc1
buffer_load_dwordx4 v[168:171],v25,s[32:35],s77 offen sc1
buffer_load_dwordx4 v[200:203],v25,s[32:35],s78 offen sc1
v_mfma_f32_16x16x32_bf16 a[204:207],v[34:37],v[64:67],a[204:207]
v_mfma_f32_16x16x32_bf16 a[204:207],v[34:37],v[80:83],a[204:207]
v_mfma_f32_16x16x32_bf16 a[204:207],v[34:37],v[96:99],a[204:207]
v_mfma_f32_16x16x32_bf16 a[220:223],v[32:35],v[64:67],a[220:223]
v_mfma_f32_16x16x32_bf16 a[220:223],v[32:35],v[80:83],a[220:223]
v_mfma_f32_16x16x32_bf16 a[220:223],v[32:35],v[96:99],a[220:223]
v_mfma_f32_16x16x32_bf16 a[236:239],v[34:37],v[68:71],a[236:239]
v_mfma_f32_16x16x32_bf16 a[236:239],v[34:37],v[84:87],a[236:239]
v_mfma_f32_16x16x32_bf16 a[236:239],v[34:37],v[100:103],a[236:239]
s_nop 1
v_accvgpr_read_b32 v128,a204
v_accvgpr_read_b32 v129,a205
v_mfma_f32_16x16x32_bf16 a[252:255],v[32:35],v[68:71],a[252:255]
v_cvt_pk_bf16_f32 v128,v128,v129
v_accvgpr_read_b32 v130,a206
v_mfma_f32_16x16x32_bf16 a[252:255],v[32:35],v[84:87],a[252:255]
v_accvgpr_read_b32 v131,a207
v_cvt_pk_bf16_f32 v129,v130,v131
v_mfma_f32_16x16x32_bf16 a[252:255],v[32:35],v[100:103],a[252:255]
buffer_store_dwordx2 v[128:129],v40,s[20:23],s43 offen
v_mfma_f32_16x16x32_bf16 a[12:15],v[34:37],v[72:75],a[12:15]
v_accvgpr_read_b32 v130,a220
v_accvgpr_read_b32 v131,a221
v_mfma_f32_16x16x32_bf16 a[12:15],v[34:37],v[88:91],a[12:15]
v_accvgpr_read_b32 v12,a222
v_accvgpr_read_b32 v13,a223
v_mfma_f32_16x16x32_bf16 a[12:15],v[34:37],v[104:107],a[12:15]
v_cvt_pk_bf16_f32 v130,v130,v131
v_cvt_pk_bf16_f32 v131,v12,v13
v_mfma_f32_16x16x32_bf16 a[28:31],v[32:35],v[72:75],a[28:31]
buffer_store_dwordx2 v[130:131],v40,s[20:23],s43 offen offset:32
v_mfma_f32_16x16x32_bf16 a[28:31],v[32:35],v[88:91],a[28:31]
v_accvgpr_read_b32 v132,a236
v_accvgpr_read_b32 v133,a237
v_mfma_f32_16x16x32_bf16 a[28:31],v[32:35],v[104:107],a[28:31]
v_cvt_pk_bf16_f32 v132,v132,v133
v_accvgpr_read_b32 v134,a238
v_mfma_f32_16x16x32_bf16 a[44:47],v[34:37],v[76:79],a[44:47]
v_accvgpr_read_b32 v135,a239
v_cvt_pk_bf16_f32 v133,v134,v135
v_mfma_f32_16x16x32_bf16 a[44:47],v[34:37],v[92:95],a[44:47]
buffer_store_dwordx2 v[132:133],v40,s[20:23],s43 offen offset:64
v_mfma_f32_16x16x32_bf16 a[44:47],v[34:37],v[108:111],a[44:47]
v_accvgpr_read_b32 v144,a12
v_accvgpr_read_b32 v145,a13
v_cvt_pk_bf16_f32 v144,v144,v145
v_accvgpr_read_b32 v146,a14
v_accvgpr_read_b32 v147,a15
v_mfma_f32_16x16x32_bf16 a[60:63],v[32:35],v[76:79],a[60:63]
v_accvgpr_read_b32 v134,a252
v_accvgpr_read_b32 v135,a253
v_cvt_pk_bf16_f32 v145,v146,v147
v_mfma_f32_16x16x32_bf16 a[60:63],v[32:35],v[92:95],a[60:63]
v_accvgpr_read_b32 v14,a254
v_accvgpr_read_b32 v15,a255
v_cvt_pk_bf16_f32 v134,v134,v135
v_cvt_pk_bf16_f32 v135,v14,v15
v_mfma_f32_16x16x32_bf16 a[60:63],v[32:35],v[108:111],a[60:63]
s_waitcnt vmcnt(5)
buffer_store_dwordx2 v[134:135],v40,s[20:23],s43 offen offset:96
buffer_store_dwordx2 v[144:145],v40,s[20:23],s43 offen offset:128
v_accvgpr_read_b32 v146,a28
v_accvgpr_read_b32 v147,a29
v_accvgpr_read_b32 v16,a30
v_accvgpr_read_b32 v17,a31
v_cvt_pk_bf16_f32 v146,v146,v147
v_cvt_pk_bf16_f32 v147,v16,v17
s_nop 3
buffer_store_dwordx2 v[146:147],v40,s[20:23],s43 offen offset:160
v_accvgpr_read_b32 v160,a44
v_accvgpr_read_b32 v161,a45
v_cvt_pk_bf16_f32 v160,v160,v161
v_accvgpr_read_b32 v162,a46
v_accvgpr_read_b32 v163,a47
v_cvt_pk_bf16_f32 v161,v162,v163
buffer_store_dwordx2 v[160:161],v40,s[20:23],s43 offen offset:192
v_accvgpr_read_b32 v162,a60
v_accvgpr_read_b32 v163,a61
v_accvgpr_read_b32 v12,a62
v_accvgpr_read_b32 v13,a63
v_cvt_pk_bf16_f32 v162,v162,v163
v_cvt_pk_bf16_f32 v163,v12,v13
buffer_store_dwordx2 v[162:163],v40,s[20:23],s43 offen offset:224
v_mfma_f32_16x16x32_bf16 a[76:79],v[34:37],v[136:139],a[76:79]
s_waitcnt vmcnt(9)
v_mfma_f32_16x16x32_bf16 a[76:79],v[34:37],v[168:171],a[76:79]
s_waitcnt vmcnt(8)
v_mfma_f32_16x16x32_bf16 a[76:79],v[34:37],v[200:203],a[76:79]
v_mfma_f32_16x16x32_bf16 a[92:95],v[32:35],v[136:139],a[92:95]
v_mfma_f32_16x16x32_bf16 a[92:95],v[32:35],v[168:171],a[92:95]
v_mfma_f32_16x16x32_bf16 a[92:95],v[32:35],v[200:203],a[92:95]
s_nop 3
s_nop 0
v_accvgpr_read_b32 v164,a76
v_accvgpr_read_b32 v165,a77
v_cvt_pk_bf16_f32 v164,v164,v165
v_accvgpr_read_b32 v166,a78
v_accvgpr_read_b32 v167,a79
v_cvt_pk_bf16_f32 v165,v166,v167
buffer_store_dwordx2 v[164:165],v40,s[20:23],s43 offen offset:256
v_accvgpr_read_b32 v166,a92
v_accvgpr_read_b32 v167,a93
v_accvgpr_read_b32 v14,a94
v_accvgpr_read_b32 v15,a95
v_cvt_pk_bf16_f32 v166,v166,v167
v_cvt_pk_bf16_f32 v167,v14,v15
buffer_store_dwordx2 v[166:167],v40,s[20:23],s43 offen offset:288
s_or_b32 s62,s42,s40
s_cmp_lg_u32 s62,0
s_cbranch_scc1 L_noep_%=
v_mov_b32 v12,s68
s_mov_b64 exec,1
buffer_store_dword v12,v25,s[64:67],0 offen sc1
s_mov_b64 exec,-1
L_noep_%=:
s_endpgm
L_ps2_%=:
s_min_i32 s79,s79,100
s_add_u32 s78,s78,s79
L_pl2_%=:
s_sleep 1
s_sub_u32 s79,s79,4
s_cmp_gt_i32 s79,1
s_cbranch_scc1 L_pl2_%=
s_branch L_pr2_%=
L_ps3_%=:
s_min_i32 s79,s79,100
s_add_u32 s78,s78,s79
L_pl3_%=:
s_sleep 1
s_sub_u32 s79,s79,4
s_cmp_gt_i32 s79,1
s_cbranch_scc1 L_pl3_%=
s_branch L_pr3_%=
L_ps4_%=:
s_min_i32 s79,s79,100
s_add_u32 s78,s78,s79
L_pl4_%=:
s_sleep 1
s_sub_u32 s79,s79,4
s_cmp_gt_i32 s79,1
s_cbranch_scc1 L_pl4_%=
s_branch L_pr4_%=
L_ps5_%=:
s_min_i32 s79,s79,100
s_add_u32 s78,s78,s79
L_pl5_%=:
s_sleep 1
s_sub_u32 s79,s79,4
s_cmp_gt_i32 s79,1
s_cbranch_scc1 L_pl5_%=
s_branch L_pr5_%=
L_ps6_%=:
s_min_i32 s79,s79,100
s_add_u32 s78,s78,s79
L_pl6_%=:
s_sleep 1
s_sub_u32 s79,s79,4
s_cmp_gt_i32 s79,1
s_cbranch_scc1 L_pl6_%=
s_branch L_pr6_%=
L_ps7_%=:
s_min_i32 s79,s79,100
s_add_u32 s78,s78,s79
L_pl7_%=:
s_sleep 1
s_sub_u32 s79,s79,4
s_cmp_gt_i32 s79,1
s_cbranch_scc1 L_pl7_%=
s_branch L_pr7_%=
L_fr8_%=:
s_mov_b32 s88,262144
L_fl8_%=:
buffer_load_dwordx2 v[26:27],v24,s[64:67],s94 offen sc1
s_waitcnt vmcnt(0)
s_sub_u32 s88,s88,1
s_cmp_eq_u32 s88,0
s_cbranch_scc1 L_fo8_%=
v_cmp_ne_u32 s[90:91],s68,v26
v_cmp_ne_u32 s[92:93],s69,v27
s_nop 0
s_or_b64 s[90:91],s[90:91],s[92:93]
s_cbranch_scc1 L_fl8_%=
s_branch L_fo8_%=
L_fr9_%=:
s_mov_b32 s88,262144
L_fl9_%=:
buffer_load_dwordx2 v[26:27],v24,s[64:67],s94 offen offset:1024 sc1
s_waitcnt vmcnt(0)
s_sub_u32 s88,s88,1
s_cmp_eq_u32 s88,0
s_cbranch_scc1 L_fo9_%=
v_cmp_ne_u32 s[90:91],s68,v26
v_cmp_ne_u32 s[92:93],s69,v27
s_nop 0
s_or_b64 s[90:91],s[90:91],s[92:93]
s_cbranch_scc1 L_fl9_%=
s_branch L_fo9_%=
L_fr10_%=:
s_mov_b32 s88,262144
L_fl10_%=:
buffer_load_dwordx2 v[26:27],v24,s[64:67],s94 offen offset:2048 sc1
s_waitcnt vmcnt(0)
s_sub_u32 s88,s88,1
s_cmp_eq_u32 s88,0
s_cbranch_scc1 L_fo10_%=
v_cmp_ne_u32 s[90:91],s68,v26
v_cmp_ne_u32 s[92:93],s69,v27
s_nop 0
s_or_b64 s[90:91],s[90:91],s[92:93]
s_cbranch_scc1 L_fl10_%=
s_branch L_fo10_%=
L_fr11_%=:
s_mov_b32 s88,262144
L_fl11_%=:
buffer_load_dwordx2 v[26:27],v24,s[64:67],s94 offen offset:3072 sc1
s_waitcnt vmcnt(0)
s_sub_u32 s88,s88,1
s_cmp_eq_u32 s88,0
s_cbranch_scc1 L_fo11_%=
v_cmp_ne_u32 s[90:91],s68,v26
v_cmp_ne_u32 s[92:93],s69,v27
s_nop 0
s_or_b64 s[90:91],s[90:91],s[92:93]
s_cbranch_scc1 L_fl11_%=
s_branch L_fo11_%=
L_out_end_%=:
L_fast_end_%=:
L_slow0_%=:
s_mov_b32 s98,0
s_branch L_slowc_%=
L_slow1_%=:
s_mov_b32 s98,1
L_slowc_%=:
s_nop 7
s_nop 7
s_waitcnt vmcnt(0) lgkmcnt(0)
s_lshl_b32 s96,%1,2
s_add_u32 s96,s96,s40
s_lshl_b32 s96,s96,15
s_add_u32 s96,s96,167772160
s_mov_b64 s[56:57],s[12:13]
s_add_u32 s56,s56,s96
s_addc_u32 s57,s57,0
s_and_b32 s57,s57,0xffff
s_mov_b32 s58,32768
s_mov_b32 s59,0x20000
s_mov_b32 s96,0
buffer_store_dwordx4 v[64:67],v6,s[56:59],s96 offen
buffer_store_dwordx4 v[68:71],v6,s[56:59],s96 offen offset:1024
buffer_store_dwordx4 v[72:75],v6,s[56:59],s96 offen offset:2048
buffer_store_dwordx4 v[76:79],v6,s[56:59],s96 offen offset:3072
s_mov_b32 s96,4096
buffer_store_dwordx4 v[80:83],v6,s[56:59],s96 offen
buffer_store_dwordx4 v[84:87],v6,s[56:59],s96 offen offset:1024
buffer_store_dwordx4 v[88:91],v6,s[56:59],s96 offen offset:2048
buffer_store_dwordx4 v[92:95],v6,s[56:59],s96 offen offset:3072
s_mov_b32 s96,8192
buffer_store_dwordx4 v[96:99],v6,s[56:59],s96 offen
buffer_store_dwordx4 v[100:103],v6,s[56:59],s96 offen offset:1024
buffer_store_dwordx4 v[104:107],v6,s[56:59],s96 offen offset:2048
buffer_store_dwordx4 v[108:111],v6,s[56:59],s96 offen offset:3072
s_mov_b32 s96,12288
buffer_store_dwordx4 v[112:115],v6,s[56:59],s96 offen
buffer_store_dwordx4 v[116:119],v6,s[56:59],s96 offen offset:1024
buffer_store_dwordx4 v[120:123],v6,s[56:59],s96 offen offset:2048
buffer_store_dwordx4 v[124:127],v6,s[56:59],s96 offen offset:3072
s_mov_b32 s96,16384
buffer_store_dwordx4 v[128:131],v6,s[56:59],s96 offen
buffer_store_dwordx4 v[132:135],v6,s[56:59],s96 offen offset:1024
buffer_store_dwordx4 v[136:139],v6,s[56:59],s96 offen offset:2048
buffer_store_dwordx4 v[140:143],v6,s[56:59],s96 offen offset:3072
s_mov_b32 s96,20480
buffer_store_dwordx4 v[144:147],v6,s[56:59],s96 offen
buffer_store_dwordx4 v[148:151],v6,s[56:59],s96 offen offset:1024
buffer_store_dwordx4 v[152:155],v6,s[56:59],s96 offen offset:2048
buffer_store_dwordx4 v[156:159],v6,s[56:59],s96 offen offset:3072
s_mov_b32 s96,24576
buffer_store_dwordx4 v[160:163],v6,s[56:59],s96 offen
buffer_store_dwordx4 v[164:167],v6,s[56:59],s96 offen offset:1024
buffer_store_dwordx4 v[168:171],v6,s[56:59],s96 offen offset:2048
buffer_store_dwordx4 v[172:175],v6,s[56:59],s96 offen offset:3072
s_mov_b32 s96,28672
buffer_store_dwordx4 v[176:179],v6,s[56:59],s96 offen
buffer_store_dwordx4 v[180:183],v6,s[56:59],s96 offen offset:1024
buffer_store_dwordx4 v[184:187],v6,s[56:59],s96 offen offset:2048
buffer_store_dwordx4 v[188:191],v6,s[56:59],s96 offen offset:3072
s_waitcnt vmcnt(0)
v_writelane_b32 v173,s88,0
v_writelane_b32 v173,s89,1
v_writelane_b32 v173,s90,2
v_writelane_b32 v173,s91,3
v_writelane_b32 v173,s92,4
v_writelane_b32 v173,s93,5
v_writelane_b32 v173,s94,6
v_writelane_b32 v173,s95,7
s_mov_b32 s88,s98
s_mul_i32 s90,s41,307200
s_mov_b64 s[92:93],s[4:5]
s_add_u32 s92,s92,s90
s_addc_u32 s93,s93,0
s_and_b32 s93,s93,0xffff
s_mov_b32 s94,307200
s_mov_b32 s95,0x20000
s_mov_b64 s[96:97],s[10:11]
s_and_b32 s97,s97,0xffff
s_mov_b32 s98,5242880
s_mov_b32 s99,0x20000
v_bfe_u32 v171,v6,4,4
v_lshrrev_b32 v172,8,v6
v_lshlrev_b32 v164,8,v171
)A"
R"A(v_lshl_add_u32 v164,v172,4,v164
v_lshlrev_b32 v165,10,v172
v_lshl_add_u32 v165,v171,1,v165
v_add_u32 v166,4096,v165
v_lshlrev_b32 v168,16,v172
v_lshrrev_b32 v170,2,v171
v_lshl_add_u32 v168,v170,4,v168
v_and_b32 v170,3,v171
v_lshl_add_u32 v168,v170,1,v168
s_lshl_b32 s90,s42,1
s_add_u32 s90,s90,s88
s_lshl_b32 s90,s90,8
v_add_u32 v168,s90,v168
s_mov_b32 s89,0
L_sjb_%=:
s_add_u32 s90,s89,s70
s_add_u32 s90,s90,1
s_and_b32 s90,s90,3
s_lshl_b32 s91,s40,2
s_add_u32 s90,s90,s91
s_lshl_b32 s90,s90,4
v_bfe_u32 v171,v6,4,4
v_lshrrev_b32 v172,8,v6
v_add_u32 v167,s90,v171
v_mul_u32_u24 v167,1280,v167
v_lshl_add_u32 v167,v172,4,v167
s_lshl_b32 s90,s88,4
s_add_u32 s90,s90,s89
s_lshl_b32 s90,s90,12
v_add_u32 v169,s90,v6
v_mov_b32 v64,0
v_mov_b32 v65,0
v_mov_b32 v66,0
v_mov_b32 v67,0
v_mov_b32 v68,0
v_mov_b32 v69,0
v_mov_b32 v70,0
v_mov_b32 v71,0
v_mov_b32 v72,0
v_mov_b32 v73,0
v_mov_b32 v74,0
v_mov_b32 v75,0
v_mov_b32 v76,0
v_mov_b32 v77,0
v_mov_b32 v78,0
v_mov_b32 v79,0
v_mov_b32 v80,0
v_mov_b32 v81,0
v_mov_b32 v82,0
v_mov_b32 v83,0
v_mov_b32 v84,0
v_mov_b32 v85,0
v_mov_b32 v86,0
v_mov_b32 v87,0
v_mov_b32 v88,0
v_mov_b32 v89,0
v_mov_b32 v90,0
v_mov_b32 v91,0
v_mov_b32 v92,0
v_mov_b32 v93,0
v_mov_b32 v94,0
v_mov_b32 v95,0
s_mov_b32 s90,0
L_sq_%=:
s_lshl_b32 s91,s90,6
buffer_load_dwordx4 v[160:163],v167,s[92:95],s91 offen
s_lshl_b32 s91,s90,18
buffer_load_ushort v112,v168,s[96:99],s91 offen
buffer_load_ushort v120,v168,s[96:99],s91 offen offset:8
buffer_load_ushort v128,v168,s[96:99],s91 offen offset:64
buffer_load_ushort v136,v168,s[96:99],s91 offen offset:72
s_add_u32 s91,s91,8192
buffer_load_ushort v113,v168,s[96:99],s91 offen
buffer_load_ushort v121,v168,s[96:99],s91 offen offset:8
buffer_load_ushort v129,v168,s[96:99],s91 offen offset:64
buffer_load_ushort v137,v168,s[96:99],s91 offen offset:72
s_add_u32 s91,s91,8192
buffer_load_ushort v114,v168,s[96:99],s91 offen
buffer_load_ushort v122,v168,s[96:99],s91 offen offset:8
buffer_load_ushort v130,v168,s[96:99],s91 offen offset:64
buffer_load_ushort v138,v168,s[96:99],s91 offen offset:72
s_add_u32 s91,s91,8192
buffer_load_ushort v115,v168,s[96:99],s91 offen
buffer_load_ushort v123,v168,s[96:99],s91 offen offset:8
buffer_load_ushort v131,v168,s[96:99],s91 offen offset:64
buffer_load_ushort v139,v168,s[96:99],s91 offen offset:72
s_add_u32 s91,s91,8192
buffer_load_ushort v116,v168,s[96:99],s91 offen
buffer_load_ushort v124,v168,s[96:99],s91 offen offset:8
buffer_load_ushort v132,v168,s[96:99],s91 offen offset:64
buffer_load_ushort v140,v168,s[96:99],s91 offen offset:72
s_add_u32 s91,s91,8192
buffer_load_ushort v117,v168,s[96:99],s91 offen
buffer_load_ushort v125,v168,s[96:99],s91 offen offset:8
buffer_load_ushort v133,v168,s[96:99],s91 offen offset:64
buffer_load_ushort v141,v168,s[96:99],s91 offen offset:72
s_add_u32 s91,s91,8192
buffer_load_ushort v118,v168,s[96:99],s91 offen
buffer_load_ushort v126,v168,s[96:99],s91 offen offset:8
buffer_load_ushort v134,v168,s[96:99],s91 offen offset:64
buffer_load_ushort v142,v168,s[96:99],s91 offen offset:72
s_add_u32 s91,s91,8192
buffer_load_ushort v119,v168,s[96:99],s91 offen
buffer_load_ushort v127,v168,s[96:99],s91 offen offset:8
buffer_load_ushort v135,v168,s[96:99],s91 offen offset:64
buffer_load_ushort v143,v168,s[96:99],s91 offen offset:72
s_add_u32 s91,s91,8192
s_waitcnt vmcnt(0)
v_lshl_or_b32 v112,v113,16,v112
v_lshl_or_b32 v113,v115,16,v114
v_lshl_or_b32 v114,v117,16,v116
v_lshl_or_b32 v115,v119,16,v118
v_lshl_or_b32 v120,v121,16,v120
v_lshl_or_b32 v121,v123,16,v122
v_lshl_or_b32 v122,v125,16,v124
v_lshl_or_b32 v123,v127,16,v126
v_lshl_or_b32 v128,v129,16,v128
v_lshl_or_b32 v129,v131,16,v130
v_lshl_or_b32 v130,v133,16,v132
v_lshl_or_b32 v131,v135,16,v134
v_lshl_or_b32 v136,v137,16,v136
v_lshl_or_b32 v137,v139,16,v138
v_lshl_or_b32 v138,v141,16,v140
v_lshl_or_b32 v139,v143,16,v142
s_nop 1
v_mfma_f32_16x16x32_bf16 v[64:67],v[112:115],v[160:163],v[64:67]
v_mfma_f32_16x16x32_bf16 v[68:71],v[120:123],v[160:163],v[68:71]
v_mfma_f32_16x16x32_bf16 v[72:75],v[128:131],v[160:163],v[72:75]
v_mfma_f32_16x16x32_bf16 v[76:79],v[136:139],v[160:163],v[76:79]
s_nop 7
s_lshl_b32 s91,s90,18
buffer_load_ushort v112,v168,s[96:99],s91 offen offset:128
buffer_load_ushort v120,v168,s[96:99],s91 offen offset:136
buffer_load_ushort v128,v168,s[96:99],s91 offen offset:192
buffer_load_ushort v136,v168,s[96:99],s91 offen offset:200
s_add_u32 s91,s91,8192
buffer_load_ushort v113,v168,s[96:99],s91 offen offset:128
buffer_load_ushort v121,v168,s[96:99],s91 offen offset:136
buffer_load_ushort v129,v168,s[96:99],s91 offen offset:192
buffer_load_ushort v137,v168,s[96:99],s91 offen offset:200
s_add_u32 s91,s91,8192
buffer_load_ushort v114,v168,s[96:99],s91 offen offset:128
buffer_load_ushort v122,v168,s[96:99],s91 offen offset:136
buffer_load_ushort v130,v168,s[96:99],s91 offen offset:192
buffer_load_ushort v138,v168,s[96:99],s91 offen offset:200
s_add_u32 s91,s91,8192
buffer_load_ushort v115,v168,s[96:99],s91 offen offset:128
buffer_load_ushort v123,v168,s[96:99],s91 offen offset:136
buffer_load_ushort v131,v168,s[96:99],s91 offen offset:192
buffer_load_ushort v139,v168,s[96:99],s91 offen offset:200
s_add_u32 s91,s91,8192
buffer_load_ushort v116,v168,s[96:99],s91 offen offset:128
buffer_load_ushort v124,v168,s[96:99],s91 offen offset:136
buffer_load_ushort v132,v168,s[96:99],s91 offen offset:192
buffer_load_ushort v140,v168,s[96:99],s91 offen offset:200
s_add_u32 s91,s91,8192
buffer_load_ushort v117,v168,s[96:99],s91 offen offset:128
buffer_load_ushort v125,v168,s[96:99],s91 offen offset:136
buffer_load_ushort v133,v168,s[96:99],s91 offen offset:192
buffer_load_ushort v141,v168,s[96:99],s91 offen offset:200
s_add_u32 s91,s91,8192
buffer_load_ushort v118,v168,s[96:99],s91 offen offset:128
buffer_load_ushort v126,v168,s[96:99],s91 offen offset:136
buffer_load_ushort v134,v168,s[96:99],s91 offen offset:192
buffer_load_ushort v142,v168,s[96:99],s91 offen offset:200
s_add_u32 s91,s91,8192
buffer_load_ushort v119,v168,s[96:99],s91 offen offset:128
buffer_load_ushort v127,v168,s[96:99],s91 offen offset:136
buffer_load_ushort v135,v168,s[96:99],s91 offen offset:192
buffer_load_ushort v143,v168,s[96:99],s91 offen offset:200
s_add_u32 s91,s91,8192
s_waitcnt vmcnt(0)
v_lshl_or_b32 v112,v113,16,v112
v_lshl_or_b32 v113,v115,16,v114
v_lshl_or_b32 v114,v117,16,v116
v_lshl_or_b32 v115,v119,16,v118
v_lshl_or_b32 v120,v121,16,v120
v_lshl_or_b32 v121,v123,16,v122
v_lshl_or_b32 v122,v125,16,v124
v_lshl_or_b32 v123,v127,16,v126
v_lshl_or_b32 v128,v129,16,v128
v_lshl_or_b32 v129,v131,16,v130
v_lshl_or_b32 v130,v133,16,v132
v_lshl_or_b32 v131,v135,16,v134
v_lshl_or_b32 v136,v137,16,v136
v_lshl_or_b32 v137,v139,16,v138
v_lshl_or_b32 v138,v141,16,v140
v_lshl_or_b32 v139,v143,16,v142
s_nop 1
v_mfma_f32_16x16x32_bf16 v[80:83],v[112:115],v[160:163],v[80:83]
v_mfma_f32_16x16x32_bf16 v[84:87],v[120:123],v[160:163],v[84:87]
v_mfma_f32_16x16x32_bf16 v[88:91],v[128:131],v[160:163],v[88:91]
v_mfma_f32_16x16x32_bf16 v[92:95],v[136:139],v[160:163],v[92:95]
s_nop 7
s_add_u32 s90,s90,1
s_cmp_lg_u32 s90,20
s_cbranch_scc1 L_sq_%=
s_nop 7
v_mul_f32 v64,s61,v64
v_mul_f32 v65,s61,v65
v_mul_f32 v66,s61,v66
v_mul_f32 v67,s61,v67
v_mul_f32 v68,s61,v68
v_mul_f32 v69,s61,v69
v_mul_f32 v70,s61,v70
v_mul_f32 v71,s61,v71
v_mul_f32 v72,s61,v72
v_mul_f32 v73,s61,v73
v_mul_f32 v74,s61,v74
v_mul_f32 v75,s61,v75
v_mul_f32 v76,s61,v76
v_mul_f32 v77,s61,v77
v_mul_f32 v78,s61,v78
v_mul_f32 v79,s61,v79
v_mul_f32 v80,s61,v80
v_mul_f32 v81,s61,v81
v_mul_f32 v82,s61,v82
v_mul_f32 v83,s61,v83
v_mul_f32 v84,s61,v84
v_mul_f32 v85,s61,v85
v_mul_f32 v86,s61,v86
v_mul_f32 v87,s61,v87
v_mul_f32 v88,s61,v88
v_mul_f32 v89,s61,v89
v_mul_f32 v90,s61,v90
v_mul_f32 v91,s61,v91
v_mul_f32 v92,s61,v92
v_mul_f32 v93,s61,v93
v_mul_f32 v94,s61,v94
v_mul_f32 v95,s61,v95
v_cvt_pk_bf16_f32 v96,v64,v65
v_cvt_pk_bf16_f32 v97,v66,v67
v_cvt_pk_bf16_f32 v98,v68,v69
v_cvt_pk_bf16_f32 v99,v70,v71
v_cvt_pk_bf16_f32 v100,v72,v73
v_cvt_pk_bf16_f32 v101,v74,v75
v_cvt_pk_bf16_f32 v102,v76,v77
v_cvt_pk_bf16_f32 v103,v78,v79
v_cvt_pk_bf16_f32 v104,v80,v81
v_cvt_pk_bf16_f32 v105,v82,v83
v_cvt_pk_bf16_f32 v106,v84,v85
v_cvt_pk_bf16_f32 v107,v86,v87
v_cvt_pk_bf16_f32 v108,v88,v89
v_cvt_pk_bf16_f32 v109,v90,v91
v_cvt_pk_bf16_f32 v110,v92,v93
v_cvt_pk_bf16_f32 v111,v94,v95
v_mov_b32 v170,0xff800000
s_lshl_b32 s91,s88,18
s_mov_b32 s90,64
L_s1_%=:
buffer_load_dwordx4 v[112:115],v164,s[20:23],s91 offen
buffer_load_dwordx4 v[116:119],v164,s[20:23],s91 offen offset:64
buffer_load_dwordx4 v[120:123],v164,s[20:23],s91 offen offset:128
buffer_load_dwordx4 v[124:127],v164,s[20:23],s91 offen offset:192
s_waitcnt vmcnt(0)
v_mfma_f32_16x16x32_bf16 v[144:147],v[112:115],v[96:99],0
v_mfma_f32_16x16x32_bf16 v[144:147],v[116:119],v[100:103],v[144:147]
v_mfma_f32_16x16x32_bf16 v[144:147],v[120:123],v[104:107],v[144:147]
v_mfma_f32_16x16x32_bf16 v[144:147],v[124:127],v[108:111],v[144:147]
s_nop 7
v_max3_f32 v170,v170,v144,v145
v_max3_f32 v170,v170,v146,v147
s_add_u32 s91,s91,4096
s_sub_u32 s90,s90,1
s_cmp_lg_u32 s90,0
s_cbranch_scc1 L_s1_%=
v_mov_b32 v171,v170
s_nop 1
v_permlane32_swap_b32 v170,v171
v_max_f32 v170,v170,v171
v_mov_b32 v171,v170
s_nop 1
v_permlane16_swap_b32 v170,v171
v_max_f32 v170,v170,v171
v_sub_f32 v152,0,v170
v_mov_b32 v156,0
v_sub_f32 v153,0,v170
v_mov_b32 v157,0
v_sub_f32 v154,0,v170
v_mov_b32 v158,0
v_sub_f32 v155,0,v170
v_mov_b32 v159,0
v_mov_b32 v64,0
v_mov_b32 v65,0
v_mov_b32 v66,0
v_mov_b32 v67,0
v_mov_b32 v68,0
v_mov_b32 v69,0
v_mov_b32 v70,0
v_mov_b32 v71,0
v_mov_b32 v72,0
v_mov_b32 v73,0
v_mov_b32 v74,0
v_mov_b32 v75,0
v_mov_b32 v76,0
v_mov_b32 v77,0
v_mov_b32 v78,0
v_mov_b32 v79,0
v_mov_b32 v80,0
v_mov_b32 v81,0
v_mov_b32 v82,0
v_mov_b32 v83,0
v_mov_b32 v84,0
v_mov_b32 v85,0
v_mov_b32 v86,0
v_mov_b32 v87,0
v_mov_b32 v88,0
v_mov_b32 v89,0
v_mov_b32 v90,0
v_mov_b32 v91,0
v_mov_b32 v92,0
v_mov_b32 v93,0
v_mov_b32 v94,0
v_mov_b32 v95,0
s_lshl_b32 s91,s88,18
s_mov_b32 s90,32
v_add_u32 v172,4096,v164
L_s2_%=:
buffer_load_dwordx4 v[112:115],v164,s[20:23],s91 offen
buffer_load_dwordx4 v[116:119],v164,s[20:23],s91 offen offset:64
buffer_load_dwordx4 v[120:123],v164,s[20:23],s91 offen offset:128
buffer_load_dwordx4 v[124:127],v164,s[20:23],s91 offen offset:192
buffer_load_dwordx4 v[128:131],v172,s[20:23],s91 offen
buffer_load_dwordx4 v[132:135],v172,s[20:23],s91 offen offset:64
buffer_load_dwordx4 v[136:139],v172,s[20:23],s91 offen offset:128
buffer_load_dwordx4 v[140:143],v172,s[20:23],s91 offen offset:192
s_waitcnt vmcnt(0)
v_mfma_f32_16x16x32_bf16 v[144:147],v[112:115],v[96:99],v[152:155]
v_mfma_f32_16x16x32_bf16 v[144:147],v[116:119],v[100:103],v[144:147]
v_mfma_f32_16x16x32_bf16 v[144:147],v[120:123],v[104:107],v[144:147]
v_mfma_f32_16x16x32_bf16 v[144:147],v[124:127],v[108:111],v[144:147]
v_mfma_f32_16x16x32_bf16 v[148:151],v[128:131],v[96:99],v[152:155]
v_mfma_f32_16x16x32_bf16 v[148:151],v[132:135],v[100:103],v[148:151]
v_mfma_f32_16x16x32_bf16 v[148:151],v[136:139],v[104:107],v[148:151]
v_mfma_f32_16x16x32_bf16 v[148:151],v[140:143],v[108:111],v[148:151]
s_nop 7
v_exp_f32 v144,v144
v_exp_f32 v145,v145
v_exp_f32 v146,v146
v_exp_f32 v147,v147
v_exp_f32 v148,v148
v_exp_f32 v149,v149
v_exp_f32 v150,v150
v_exp_f32 v151,v151
s_nop 0
v_cvt_pk_bf16_f32 v144,v144,v145
v_cvt_pk_bf16_f32 v145,v146,v147
v_cvt_pk_bf16_f32 v146,v148,v149
v_cvt_pk_bf16_f32 v147,v150,v151
buffer_load_ushort v112,v165,s[24:27],s91 offen
buffer_load_ushort v113,v165,s[24:27],s91 offen offset:256
buffer_load_ushort v114,v165,s[24:27],s91 offen offset:512
buffer_load_ushort v115,v165,s[24:27],s91 offen offset:768
buffer_load_ushort v116,v166,s[24:27],s91 offen
buffer_load_ushort v117,v166,s[24:27],s91 offen offset:256
buffer_load_ushort v118,v166,s[24:27],s91 offen offset:512
buffer_load_ushort v119,v166,s[24:27],s91 offen offset:768
buffer_load_ushort v120,v165,s[24:27],s91 offen offset:32
buffer_load_ushort v121,v165,s[24:27],s91 offen offset:288
buffer_load_ushort v122,v165,s[24:27],s91 offen offset:544
buffer_load_ushort v123,v165,s[24:27],s91 offen offset:800
)A"
R"A(buffer_load_ushort v124,v166,s[24:27],s91 offen offset:32
buffer_load_ushort v125,v166,s[24:27],s91 offen offset:288
buffer_load_ushort v126,v166,s[24:27],s91 offen offset:544
buffer_load_ushort v127,v166,s[24:27],s91 offen offset:800
buffer_load_ushort v128,v165,s[24:27],s91 offen offset:64
buffer_load_ushort v129,v165,s[24:27],s91 offen offset:320
buffer_load_ushort v130,v165,s[24:27],s91 offen offset:576
buffer_load_ushort v131,v165,s[24:27],s91 offen offset:832
buffer_load_ushort v132,v166,s[24:27],s91 offen offset:64
buffer_load_ushort v133,v166,s[24:27],s91 offen offset:320
buffer_load_ushort v134,v166,s[24:27],s91 offen offset:576
buffer_load_ushort v135,v166,s[24:27],s91 offen offset:832
buffer_load_ushort v136,v165,s[24:27],s91 offen offset:96
buffer_load_ushort v137,v165,s[24:27],s91 offen offset:352
buffer_load_ushort v138,v165,s[24:27],s91 offen offset:608
buffer_load_ushort v139,v165,s[24:27],s91 offen offset:864
buffer_load_ushort v140,v166,s[24:27],s91 offen offset:96
buffer_load_ushort v141,v166,s[24:27],s91 offen offset:352
buffer_load_ushort v142,v166,s[24:27],s91 offen offset:608
buffer_load_ushort v143,v166,s[24:27],s91 offen offset:864
s_waitcnt vmcnt(0)
v_lshl_or_b32 v112,v113,16,v112
v_lshl_or_b32 v113,v115,16,v114
v_lshl_or_b32 v114,v117,16,v116
v_lshl_or_b32 v115,v119,16,v118
v_lshl_or_b32 v120,v121,16,v120
v_lshl_or_b32 v121,v123,16,v122
v_lshl_or_b32 v122,v125,16,v124
v_lshl_or_b32 v123,v127,16,v126
v_lshl_or_b32 v128,v129,16,v128
v_lshl_or_b32 v129,v131,16,v130
v_lshl_or_b32 v130,v133,16,v132
v_lshl_or_b32 v131,v135,16,v134
v_lshl_or_b32 v136,v137,16,v136
v_lshl_or_b32 v137,v139,16,v138
v_lshl_or_b32 v138,v141,16,v140
v_lshl_or_b32 v139,v143,16,v142
s_nop 1
v_mfma_f32_16x16x32_bf16 v[64:67],v[112:115],v[144:147],v[64:67]
v_mfma_f32_16x16x32_bf16 v[68:71],v[120:123],v[144:147],v[68:71]
v_mfma_f32_16x16x32_bf16 v[72:75],v[128:131],v[144:147],v[72:75]
v_mfma_f32_16x16x32_bf16 v[76:79],v[136:139],v[144:147],v[76:79]
s_nop 7
buffer_load_ushort v112,v165,s[24:27],s91 offen offset:128
buffer_load_ushort v113,v165,s[24:27],s91 offen offset:384
buffer_load_ushort v114,v165,s[24:27],s91 offen offset:640
buffer_load_ushort v115,v165,s[24:27],s91 offen offset:896
buffer_load_ushort v116,v166,s[24:27],s91 offen offset:128
buffer_load_ushort v117,v166,s[24:27],s91 offen offset:384
buffer_load_ushort v118,v166,s[24:27],s91 offen offset:640
buffer_load_ushort v119,v166,s[24:27],s91 offen offset:896
buffer_load_ushort v120,v165,s[24:27],s91 offen offset:160
buffer_load_ushort v121,v165,s[24:27],s91 offen offset:416
buffer_load_ushort v122,v165,s[24:27],s91 offen offset:672
buffer_load_ushort v123,v165,s[24:27],s91 offen offset:928
buffer_load_ushort v124,v166,s[24:27],s91 offen offset:160
buffer_load_ushort v125,v166,s[24:27],s91 offen offset:416
buffer_load_ushort v126,v166,s[24:27],s91 offen offset:672
buffer_load_ushort v127,v166,s[24:27],s91 offen offset:928
buffer_load_ushort v128,v165,s[24:27],s91 offen offset:192
buffer_load_ushort v129,v165,s[24:27],s91 offen offset:448
buffer_load_ushort v130,v165,s[24:27],s91 offen offset:704
buffer_load_ushort v131,v165,s[24:27],s91 offen offset:960
buffer_load_ushort v132,v166,s[24:27],s91 offen offset:192
buffer_load_ushort v133,v166,s[24:27],s91 offen offset:448
buffer_load_ushort v134,v166,s[24:27],s91 offen offset:704
buffer_load_ushort v135,v166,s[24:27],s91 offen offset:960
buffer_load_ushort v136,v165,s[24:27],s91 offen offset:224
buffer_load_ushort v137,v165,s[24:27],s91 offen offset:480
buffer_load_ushort v138,v165,s[24:27],s91 offen offset:736
buffer_load_ushort v139,v165,s[24:27],s91 offen offset:992
buffer_load_ushort v140,v166,s[24:27],s91 offen offset:224
buffer_load_ushort v141,v166,s[24:27],s91 offen offset:480
buffer_load_ushort v142,v166,s[24:27],s91 offen offset:736
buffer_load_ushort v143,v166,s[24:27],s91 offen offset:992
s_waitcnt vmcnt(0)
v_lshl_or_b32 v112,v113,16,v112
v_lshl_or_b32 v113,v115,16,v114
v_lshl_or_b32 v114,v117,16,v116
v_lshl_or_b32 v115,v119,16,v118
v_lshl_or_b32 v120,v121,16,v120
v_lshl_or_b32 v121,v123,16,v122
v_lshl_or_b32 v122,v125,16,v124
v_lshl_or_b32 v123,v127,16,v126
v_lshl_or_b32 v128,v129,16,v128
v_lshl_or_b32 v129,v131,16,v130
v_lshl_or_b32 v130,v133,16,v132
v_lshl_or_b32 v131,v135,16,v134
v_lshl_or_b32 v136,v137,16,v136
v_lshl_or_b32 v137,v139,16,v138
v_lshl_or_b32 v138,v141,16,v140
v_lshl_or_b32 v139,v143,16,v142
s_nop 1
v_mfma_f32_16x16x32_bf16 v[80:83],v[112:115],v[144:147],v[80:83]
v_mfma_f32_16x16x32_bf16 v[84:87],v[120:123],v[144:147],v[84:87]
v_mfma_f32_16x16x32_bf16 v[88:91],v[128:131],v[144:147],v[88:91]
v_mfma_f32_16x16x32_bf16 v[92:95],v[136:139],v[144:147],v[92:95]
s_nop 7
v_mfma_f32_16x16x32_bf16 v[156:159],v[28:31],v[144:147],v[156:159]
s_nop 7
s_add_u32 s91,s91,8192
s_sub_u32 s90,s90,1
s_cmp_lg_u32 s90,0
s_cbranch_scc1 L_s2_%=
v_rcp_f32 v170,v156
s_nop 0
v_mul_f32 v64,v170,v64
v_mul_f32 v65,v170,v65
v_mul_f32 v66,v170,v66
v_mul_f32 v67,v170,v67
v_mul_f32 v68,v170,v68
v_mul_f32 v69,v170,v69
v_mul_f32 v70,v170,v70
v_mul_f32 v71,v170,v71
v_mul_f32 v72,v170,v72
v_mul_f32 v73,v170,v73
v_mul_f32 v74,v170,v74
v_mul_f32 v75,v170,v75
v_mul_f32 v76,v170,v76
v_mul_f32 v77,v170,v77
v_mul_f32 v78,v170,v78
v_mul_f32 v79,v170,v79
v_mul_f32 v80,v170,v80
v_mul_f32 v81,v170,v81
v_mul_f32 v82,v170,v82
v_mul_f32 v83,v170,v83
v_mul_f32 v84,v170,v84
v_mul_f32 v85,v170,v85
v_mul_f32 v86,v170,v86
v_mul_f32 v87,v170,v87
v_mul_f32 v88,v170,v88
v_mul_f32 v89,v170,v89
v_mul_f32 v90,v170,v90
v_mul_f32 v91,v170,v91
v_mul_f32 v92,v170,v92
v_mul_f32 v93,v170,v93
v_mul_f32 v94,v170,v94
v_mul_f32 v95,v170,v95
v_cvt_pk_bf16_f32 v96,v64,v65
v_cvt_pk_bf16_f32 v97,v66,v67
v_cvt_pk_bf16_f32 v98,v68,v69
v_cvt_pk_bf16_f32 v99,v70,v71
v_cvt_pk_bf16_f32 v100,v72,v73
v_cvt_pk_bf16_f32 v101,v74,v75
v_cvt_pk_bf16_f32 v102,v76,v77
v_cvt_pk_bf16_f32 v103,v78,v79
v_cvt_pk_bf16_f32 v104,v80,v81
v_cvt_pk_bf16_f32 v105,v82,v83
v_cvt_pk_bf16_f32 v106,v84,v85
v_cvt_pk_bf16_f32 v107,v86,v87
v_cvt_pk_bf16_f32 v108,v88,v89
v_cvt_pk_bf16_f32 v109,v90,v91
v_cvt_pk_bf16_f32 v110,v92,v93
v_cvt_pk_bf16_f32 v111,v94,v95
buffer_store_dwordx4 v[96:99],v169,s[36:39],s55 offen sc1
buffer_store_dwordx4 v[100:103],v169,s[36:39],s55 offen offset:1024 sc1
buffer_store_dwordx4 v[104:107],v169,s[36:39],s55 offen offset:2048 sc1
buffer_store_dwordx4 v[108:111],v169,s[36:39],s55 offen offset:3072 sc1
s_waitcnt vmcnt(0)
s_add_u32 s89,s89,1
s_cmp_lg_u32 s89,4
s_cbranch_scc1 L_sjb_%=
s_mov_b32 s98,s88
v_readlane_b32 s88,v173,0
v_readlane_b32 s89,v173,1
v_readlane_b32 s90,v173,2
v_readlane_b32 s91,v173,3
v_readlane_b32 s92,v173,4
v_readlane_b32 s93,v173,5
v_readlane_b32 s94,v173,6
v_readlane_b32 s95,v173,7
s_nop 4
s_mov_b32 s96,0
buffer_load_dwordx4 v[64:67],v6,s[56:59],s96 offen sc1
buffer_load_dwordx4 v[68:71],v6,s[56:59],s96 offen offset:1024 sc1
buffer_load_dwordx4 v[72:75],v6,s[56:59],s96 offen offset:2048 sc1
buffer_load_dwordx4 v[76:79],v6,s[56:59],s96 offen offset:3072 sc1
s_mov_b32 s96,4096
buffer_load_dwordx4 v[80:83],v6,s[56:59],s96 offen sc1
buffer_load_dwordx4 v[84:87],v6,s[56:59],s96 offen offset:1024 sc1
buffer_load_dwordx4 v[88:91],v6,s[56:59],s96 offen offset:2048 sc1
buffer_load_dwordx4 v[92:95],v6,s[56:59],s96 offen offset:3072 sc1
s_mov_b32 s96,8192
buffer_load_dwordx4 v[96:99],v6,s[56:59],s96 offen sc1
buffer_load_dwordx4 v[100:103],v6,s[56:59],s96 offen offset:1024 sc1
buffer_load_dwordx4 v[104:107],v6,s[56:59],s96 offen offset:2048 sc1
buffer_load_dwordx4 v[108:111],v6,s[56:59],s96 offen offset:3072 sc1
s_mov_b32 s96,12288
buffer_load_dwordx4 v[112:115],v6,s[56:59],s96 offen sc1
buffer_load_dwordx4 v[116:119],v6,s[56:59],s96 offen offset:1024 sc1
buffer_load_dwordx4 v[120:123],v6,s[56:59],s96 offen offset:2048 sc1
buffer_load_dwordx4 v[124:127],v6,s[56:59],s96 offen offset:3072 sc1
s_mov_b32 s96,16384
buffer_load_dwordx4 v[128:131],v6,s[56:59],s96 offen sc1
buffer_load_dwordx4 v[132:135],v6,s[56:59],s96 offen offset:1024 sc1
buffer_load_dwordx4 v[136:139],v6,s[56:59],s96 offen offset:2048 sc1
buffer_load_dwordx4 v[140:143],v6,s[56:59],s96 offen offset:3072 sc1
s_mov_b32 s96,20480
buffer_load_dwordx4 v[144:147],v6,s[56:59],s96 offen sc1
buffer_load_dwordx4 v[148:151],v6,s[56:59],s96 offen offset:1024 sc1
buffer_load_dwordx4 v[152:155],v6,s[56:59],s96 offen offset:2048 sc1
buffer_load_dwordx4 v[156:159],v6,s[56:59],s96 offen offset:3072 sc1
s_mov_b32 s96,24576
buffer_load_dwordx4 v[160:163],v6,s[56:59],s96 offen sc1
buffer_load_dwordx4 v[164:167],v6,s[56:59],s96 offen offset:1024 sc1
buffer_load_dwordx4 v[168:171],v6,s[56:59],s96 offen offset:2048 sc1
buffer_load_dwordx4 v[172:175],v6,s[56:59],s96 offen offset:3072 sc1
s_mov_b32 s96,28672
buffer_load_dwordx4 v[176:179],v6,s[56:59],s96 offen sc1
buffer_load_dwordx4 v[180:183],v6,s[56:59],s96 offen offset:1024 sc1
buffer_load_dwordx4 v[184:187],v6,s[56:59],s96 offen offset:2048 sc1
buffer_load_dwordx4 v[188:191],v6,s[56:59],s96 offen offset:3072 sc1
s_waitcnt vmcnt(0)
s_cmp_lg_u32 s98,0
s_cbranch_scc1 L_sr1_%=
buffer_load_dwordx4 v[64:67],v6,s[36:39],s84 offen sc1
buffer_load_dwordx4 v[68:71],v6,s[36:39],s84 offen offset:1024 sc1
buffer_load_dwordx4 v[72:75],v6,s[36:39],s84 offen offset:2048 sc1
buffer_load_dwordx4 v[76:79],v6,s[36:39],s84 offen offset:3072 sc1
buffer_load_dwordx4 v[80:83],v6,s[36:39],s85 offen sc1
buffer_load_dwordx4 v[84:87],v6,s[36:39],s85 offen offset:1024 sc1
buffer_load_dwordx4 v[88:91],v6,s[36:39],s85 offen offset:2048 sc1
buffer_load_dwordx4 v[92:95],v6,s[36:39],s85 offen offset:3072 sc1
buffer_load_dwordx4 v[96:99],v6,s[36:39],s86 offen sc1
buffer_load_dwordx4 v[100:103],v6,s[36:39],s86 offen offset:1024 sc1
buffer_load_dwordx4 v[104:107],v6,s[36:39],s86 offen offset:2048 sc1
buffer_load_dwordx4 v[108:111],v6,s[36:39],s86 offen offset:3072 sc1
buffer_load_dwordx4 v[112:115],v6,s[36:39],s87 offen sc1
buffer_load_dwordx4 v[116:119],v6,s[36:39],s87 offen offset:1024 sc1
buffer_load_dwordx4 v[120:123],v6,s[36:39],s87 offen offset:2048 sc1
buffer_load_dwordx4 v[124:127],v6,s[36:39],s87 offen offset:3072 sc1
s_waitcnt vmcnt(0)
s_nop 7
s_branch L_sret0_%=
L_sr1_%=:
buffer_load_dwordx4 v[136:139],v6,s[36:39],s84 offen sc1
buffer_load_dwordx4 v[140:143],v6,s[36:39],s84 offen offset:1024 sc1
buffer_load_dwordx4 v[152:155],v6,s[36:39],s84 offen offset:2048 sc1
buffer_load_dwordx4 v[156:159],v6,s[36:39],s84 offen offset:3072 sc1
buffer_load_dwordx4 v[168:171],v6,s[36:39],s85 offen sc1
buffer_load_dwordx4 v[172:175],v6,s[36:39],s85 offen offset:1024 sc1
buffer_load_dwordx4 v[184:187],v6,s[36:39],s85 offen offset:2048 sc1
buffer_load_dwordx4 v[188:191],v6,s[36:39],s85 offen offset:3072 sc1
buffer_load_dwordx4 v[200:203],v6,s[36:39],s86 offen sc1
buffer_load_dwordx4 v[204:207],v6,s[36:39],s86 offen offset:1024 sc1
buffer_load_dwordx4 v[216:219],v6,s[36:39],s86 offen offset:2048 sc1
buffer_load_dwordx4 v[220:223],v6,s[36:39],s86 offen offset:3072 sc1
buffer_load_dwordx4 v[232:235],v6,s[36:39],s87 offen sc1
buffer_load_dwordx4 v[236:239],v6,s[36:39],s87 offen offset:1024 sc1
buffer_load_dwordx4 v[248:251],v6,s[36:39],s87 offen offset:2048 sc1
buffer_load_dwordx4 v[252:255],v6,s[36:39],s87 offen offset:3072 sc1
s_waitcnt vmcnt(0)
s_nop 7
s_branch L_sret1_%=
L_end_%=:
)A"
    :: "s"(karg), "s"(blk), "s"(wid)
    : "v0", "v1", "v2", "v3", "v4", "v5", "v6", "v7", "v8", "v9", "v10", "v11", "v12", "v13", "v14", "v15", "v16", "v17", "v18", "v19", "v20", "v21", "v22", "v23", "v24", "v25", "v26", "v27", "v28", "v29", "v30", "v31", "v32", "v33", "v34", "v35", "v36", "v37", "v38", "v39", "v40", "v41", "v42", "v43", "v44", "v45", "v46", "v47", "v48", "v49", "v50", "v51", "v52", "v53", "v54", "v55", "v56", "v57", "v58", "v59", "v60", "v61", "v62", "v63", "v64", "v65", "v66", "v67", "v68", "v69", "v70", "v71", "v72", "v73", "v74", "v75", "v76", "v77", "v78", "v79", "v80", "v81", "v82", "v83", "v84", "v85", "v86", "v87", "v88", "v89", "v90", "v91", "v92", "v93", "v94", "v95", "v96", "v97", "v98", "v99", "v100", "v101", "v102", "v103", "v104", "v105", "v106", "v107", "v108", "v109", "v110", "v111", "v112", "v113", "v114", "v115", "v116", "v117", "v118", "v119", "v120", "v121", "v122", "v123", "v124", "v125", "v126", "v127", "v128", "v129", "v130", "v131", "v132", "v133", "v134", "v135", "v136", "v137", "v138", "v139", "v140", "v141", "v142", "v143", "v144", "v145", "v146", "v147", "v148", "v149", "v150", "v151", "v152", "v153", "v154", "v155", "v156", "v157", "v158", "v159", "v160", "v161", "v162", "v163", "v164", "v165", "v166", "v167", "v168", "v169", "v170", "v171", "v172", "v173", "v174", "v175", "v176", "v177", "v178", "v179", "v180", "v181", "v182", "v183", "v184", "v185", "v186", "v187", "v188", "v189", "v190", "v191", "v192", "v193", "v194", "v195", "v196", "v197", "v198", "v199", "v200", "v201", "v202", "v203", "v204", "v205", "v206", "v207", "v208", "v209", "v210", "v211", "v212", "v213", "v214", "v215", "v216", "v217", "v218", "v219", "v220", "v221", "v222", "v223", "v224", "v225", "v226", "v227", "v228", "v229", "v230", "v231", "v232", "v233", "v234", "v235", "v236", "v237", "v238", "v239", "v240", "v241", "v242", "v243", "v244", "v245", "v246", "v247", "v248", "v249", "v250", "v251", "v252", "v253", "v254", "v255", "a0", "a1", "a2", "a3", "a4", "a5", "a6", "a7", "a8", "a9", "a10", "a11", "a12", "a13", "a14", "a15", "a16", "a17", "a18", "a19", "a20", "a21", "a22", "a23", "a24", "a25", "a26", "a27", "a28", "a29", "a30", "a31", "a32", "a33", "a34", "a35", "a36", "a37", "a38", "a39", "a40", "a41", "a42", "a43", "a44", "a45", "a46", "a47", "a48", "a49", "a50", "a51", "a52", "a53", "a54", "a55", "a56", "a57", "a58", "a59", "a60", "a61", "a62", "a63", "a64", "a65", "a66", "a67", "a68", "a69", "a70", "a71", "a72", "a73", "a74", "a75", "a76", "a77", "a78", "a79", "a80", "a81", "a82", "a83", "a84", "a85", "a86", "a87", "a88", "a89", "a90", "a91", "a92", "a93", "a94", "a95", "a96", "a97", "a98", "a99", "a100", "a101", "a102", "a103", "a104", "a105", "a106", "a107", "a108", "a109", "a110", "a111", "a112", "a113", "a114", "a115", "a116", "a117", "a118", "a119", "a120", "a121", "a122", "a123", "a124", "a125", "a126", "a127", "a128", "a129", "a130", "a131", "a132", "a133", "a134", "a135", "a136", "a137", "a138", "a139", "a140", "a141", "a142", "a143", "a144", "a145", "a146", "a147", "a148", "a149", "a150", "a151", "a152", "a153", "a154", "a155", "a156", "a157", "a158", "a159", "a160", "a161", "a162", "a163", "a164", "a165", "a166", "a167", "a168", "a169", "a170", "a171", "a172", "a173", "a174", "a175", "a176", "a177", "a178", "a179", "a180", "a181", "a182", "a183", "a184", "a185", "a186", "a187", "a188", "a189", "a190", "a191", "a192", "a193", "a194", "a195", "a196", "a197", "a198", "a199", "a200", "a201", "a202", "a203", "a204", "a205", "a206", "a207", "a208", "a209", "a210", "a211", "a212", "a213", "a214", "a215", "a216", "a217", "a218", "a219", "a220", "a221", "a222", "a223", "a224", "a225", "a226", "a227", "a228", "a229", "a230", "a231", "a232", "a233", "a234", "a235", "a236", "a237", "a238", "a239", "a240", "a241", "a242", "a243", "a244", "a245", "a246", "a247", "a248", "a249", "a250", "a251", "a252", "a253", "a254", "a255", "s4", "s5", "s6", "s7", "s8", "s9", "s10", "s11", "s12", "s13", "s14", "s15", "s16", "s17", "s18", "s19", "s20", "s21", "s22", "s23", "s24", "s25", "s26", "s27", "s28", "s29", "s30", "s31", "s32", "s33", "s34", "s35", "s36", "s37", "s38", "s39", "s40", "s41", "s42", "s43", "s44", "s45", "s46", "s47", "s48", "s49", "s50", "s51", "s52", "s53", "s54", "s55", "s56", "s57", "s58", "s59", "s60", "s61", "s62", "s63", "s64", "s65", "s66", "s67", "s68", "s69", "s70", "s71", "s72", "s73", "s74", "s75", "s76", "s77", "s78", "s79", "s80", "s81", "s82", "s83", "s84", "s85", "s86", "s87", "s88", "s89", "s90", "s91", "s92", "s93", "s94", "s95", "s96", "s97", "s98", "s99", "s100", "s101", "vcc", "scc", "memory");
#endif
}

extern "C" void kernel_launch(void* const* d_in, const int* in_sizes, int n_in,
                              void* d_out, int out_size, void* d_ws, size_t ws_size,
                              hipStream_t stream) {
  (void)hipFuncSetAttribute((const void*)qattn_kernel, hipFuncAttributeMaxDynamicSharedMemorySize, LDS_ATT);
  qattn_kernel<<<256, 256, LDS_ATT, stream>>>(d_in[0], d_in[1], d_in[2], d_in[3], d_ws, d_in[4], d_out);
}
```

```cpp
#include <hip/hip_runtime.h>
#include <hip/hip_bf16.h>

constexpr int LDS_ATT = 163840;

__global__ __launch_bounds__(256, 1) void qattn_kernel(const void* __restrict__ x, const void* __restrict__ kc, const void* __restrict__ vc,
                                                       const void* __restrict__ wq, void* __restrict__ ws, const void* __restrict__ wo,
                                                       void* __restrict__ out) {
#if defined(__HIP_DEVICE_COMPILE__)
  const auto karg = __builtin_amdgcn_kernarg_segment_ptr();
  const unsigned blk = blockIdx.x;
  const unsigned wid = __builtin_amdgcn_readfirstlane(threadIdx.x >> 6);
  asm volatile(
R"A(s_memtime s[72:73]
s_memrealtime s[74:75]
s_load_dwordx8 s[4:11],%0,0x0
s_load_dwordx4 s[12:15],%0,0x20
s_load_dwordx2 s[16:17],%0,0x30
s_mov_b32 s40,%2
s_mov_b32 s43,%1
s_lshr_b32 s45,s43,3
v_mbcnt_lo_u32_b32 v12,-1,0
v_mbcnt_hi_u32_b32 v12,-1,v12
s_getpc_b64 s[88:89]
L_pc_%=:
s_and_b32 s88,s88,0xffffff80
s_and_b32 s89,s89,0xffff
s_mov_b32 s90,L_fast_end_%=-L_pc_%=
s_mov_b32 s91,0x20000
s_lshl_b32 s44,s45,2
s_add_u32 s44,s44,s40
s_mul_i32 s44,s44,896
v_lshlrev_b32 v67,7,v12
s_mov_b64 exec,127
buffer_load_dword v19,v67,s[88:91],s44 offen
s_mov_b64 exec,-1
s_and_b32 s70,s43,3
s_and_b32 s71,s45,3
s_lshl_b32 s42,s70,2
s_add_u32 s42,s42,s71
s_and_b32 s41,s43,4
s_lshl_b32 s41,s41,1
s_lshr_b32 s46,s45,2
s_add_u32 s41,s41,s46
s_lshl_b32 s56,s42,9
s_mov_b32 s57,0
s_lshl_b32 s59,s40,13
s_add_u32 s58,s59,65536
v_and_b32 v64,3,v12
v_bfe_u32 v65,v12,2,2
v_lshrrev_b32 v66,4,v12
s_lshl_b32 s44,s40,17
v_lshlrev_b32 v75,13,v65
v_lshl_add_u32 v75,v66,6,v75
v_lshl_add_u32 v75,v64,4,v75
v_add_u32 v75,s44,v75
v_add_u32 v56,0,v75
v_add_u32 v57,256,v75
v_add_u32 v58,32768,v75
v_add_u32 v59,33024,v75
v_add_u32 v60,65536,v75
v_add_u32 v61,65792,v75
v_add_u32 v62,98304,v75
v_add_u32 v63,98560,v75
s_waitcnt lgkmcnt(0)
s_mov_b64 s[32:33],s[10:11]
s_and_b32 s33,s33,0xffff
s_mov_b32 s34,5242880
s_mov_b32 s35,0x20000
s_add_u32 m0,s58,1024
s_nop 0
buffer_load_dwordx4 v57,s[32:35],s56 offen lds
s_add_u32 m0,s58,3072
s_nop 0
buffer_load_dwordx4 v59,s[32:35],s56 offen lds
s_add_u32 m0,s58,5120
s_nop 0
buffer_load_dwordx4 v61,s[32:35],s56 offen lds
s_add_u32 m0,s58,7168
s_nop 0
buffer_load_dwordx4 v63,s[32:35],s56 offen lds
v_lshrrev_b32 v75,3,v12
s_lshl_b32 s44,s40,6
v_add_u32 v75,s44,v75
v_mul_u32_u24 v75,1280,v75
v_and_b32 v76,7,v12
s_add_u32 s84,s70,1
s_and_b32 s84,s84,3
s_mul_i32 s84,s84,20480
s_add_u32 s84,s84,4096
s_add_u32 s85,s70,2
s_and_b32 s85,s85,3
s_mul_i32 s85,s85,20480
s_add_u32 s85,s85,2048
s_add_u32 s86,s70,3
s_and_b32 s86,s86,3
s_mul_i32 s86,s86,20480
s_add_u32 s86,s86,4096
s_add_u32 s87,s70,4
s_and_b32 s87,s87,3
s_mul_i32 s87,s87,20480
s_add_u32 s87,s87,2048
v_or_b32 v77,0,v66
v_xor_b32 v77,v77,v76
v_lshl_add_u32 v77,v77,4,v75
v_add_u32 v48,s84,v77
v_add_u32 v50,s85,v77
v_add_u32 v52,s86,v77
v_add_u32 v54,s87,v77
v_or_b32 v77,4,v66
v_xor_b32 v77,v77,v76
v_lshl_add_u32 v77,v77,4,v75
v_add_u32 v77,9216,v77
v_add_u32 v49,s84,v77
v_add_u32 v51,s85,v77
v_add_u32 v53,s86,v77
v_add_u32 v55,s87,v77
s_mul_i32 s44,s41,307200
s_mov_b64 s[28:29],s[4:5]
s_add_u32 s28,s28,s44
s_addc_u32 s29,s29,0
s_sub_u32 s28,s28,4096
s_subb_u32 s29,s29,0
s_and_b32 s29,s29,0xffff
s_mov_b32 s30,311296
s_mov_b32 s31,0x20000
s_add_u32 m0,s59,0
s_nop 0
buffer_load_dwordx4 v48,s[28:31],s57 offen lds
buffer_load_dwordx4 v49,s[28:31],s57 offen offset:1024 lds
buffer_load_dwordx4 v50,s[28:31],s57 offen offset:2048 lds
buffer_load_dwordx4 v51,s[28:31],s57 offen offset:3072 lds
s_add_u32 m0,s59,4096
s_nop 0
buffer_load_dwordx4 v52,s[28:31],s57 offen lds
buffer_load_dwordx4 v53,s[28:31],s57 offen offset:1024 lds
buffer_load_dwordx4 v54,s[28:31],s57 offen offset:2048 lds
buffer_load_dwordx4 v55,s[28:31],s57 offen offset:3072 lds
s_add_u32 s57,s57,128
s_xor_b32 s59,s59,131072
s_add_u32 m0,s58,0
s_nop 0
buffer_load_dwordx4 v56,s[32:35],s56 offen lds
s_add_u32 m0,s58,2048
s_nop 0
buffer_load_dwordx4 v58,s[32:35],s56 offen lds
s_add_u32 m0,s58,4096
s_nop 0
buffer_load_dwordx4 v60,s[32:35],s56 offen lds
s_add_u32 m0,s58,6144
s_nop 0
buffer_load_dwordx4 v62,s[32:35],s56 offen lds
s_add_u32 s56,s56,524288
s_add_u32 s58,s58,32768
s_cmp_ge_u32 s58,131072
s_cselect_b32 s62,98304,0
s_sub_u32 s58,s58,s62
s_add_u32 m0,s58,0
s_nop 0
buffer_load_dwordx4 v56,s[32:35],s56 offen lds
s_add_u32 m0,s58,1024
s_nop 0
buffer_load_dwordx4 v57,s[32:35],s56 offen lds
s_add_u32 m0,s58,2048
s_nop 0
buffer_load_dwordx4 v58,s[32:35],s56 offen lds
s_add_u32 m0,s58,3072
s_nop 0
buffer_load_dwordx4 v59,s[32:35],s56 offen lds
s_add_u32 m0,s58,4096
s_nop 0
buffer_load_dwordx4 v60,s[32:35],s56 offen lds
s_add_u32 m0,s58,5120
s_nop 0
buffer_load_dwordx4 v61,s[32:35],s56 offen lds
s_add_u32 m0,s58,6144
s_nop 0
buffer_load_dwordx4 v62,s[32:35],s56 offen lds
s_add_u32 m0,s58,7168
s_nop 0
buffer_load_dwordx4 v63,s[32:35],s56 offen lds
s_add_u32 s56,s56,524288
s_add_u32 s58,s58,32768
s_cmp_ge_u32 s58,131072
s_cselect_b32 s62,98304,0
s_sub_u32 s58,s58,s62
s_mov_b32 s60,65536
v_lshlrev_b32 v6,4,v12
v_lshlrev_b32 v72,12,v66
v_lshl_add_u32 v72,v65,6,v72
v_lshl_add_u32 v23,v64,4,v72
v_add_u32 v22,65536,v23
v_and_b32 v74,15,v12
v_lshrrev_b32 v76,1,v74
v_lshlrev_b32 v75,7,v74
s_lshl_b32 s44,s40,13
v_add_u32 v75,s44,v75
v_or_b32 v77,0,v66
v_xor_b32 v77,v77,v76
v_lshl_add_u32 v20,v77,4,v75
v_or_b32 v77,4,v66
v_xor_b32 v77,v77,v76
v_lshl_add_u32 v21,v77,4,v75
s_waitcnt vmcnt(12)
s_nop 0
s_memtime s[64:65]
s_memrealtime s[66:67]
s_bfe_u32 s68,%1,327683
s_waitcnt lgkmcnt(0)
s_sub_u32 s64,s64,s72
s_sub_u32 s66,s66,s74
s_mul_i32 s64,s64,10
s_mul_i32 s66,s66,215
s_cmp_lt_u32 s64,s66
s_cselect_b32 s95,64,0
s_mov_b32 %2,s95
s_cbranch_scc1 L_se1_%=
L_sg1_%=:
s_cmp_eq_u32 s68,0
s_cbranch_scc1 L_se1_%=
s_sleep 4
s_sub_u32 s68,s68,1
s_branch L_sg1_%=
L_se1_%=:
s_barrier
ds_read_b128 a[192:195],v20
ds_read_b64_tr_b16 a[128:129],v22 offset:1024
ds_read_b64_tr_b16 a[130:131],v22 offset:3072
ds_read_b128 a[196:199],v21
ds_read_b64_tr_b16 a[132:133],v22 offset:17408
ds_read_b64_tr_b16 a[134:135],v22 offset:19456
ds_read_b128 a[204:207],v21 offset:2048
ds_read_b128 a[200:203],v20 offset:2048
ds_read_b128 a[208:211],v20 offset:4096
ds_read_b128 a[212:215],v21 offset:4096
ds_read_b128 a[220:223],v21 offset:6144
ds_read_b128 a[216:219],v20 offset:6144
ds_read_b64_tr_b16 a[136:137],v22 offset:1032
ds_read_b64_tr_b16 a[138:139],v22 offset:3080
s_waitcnt lgkmcnt(11)
v_mfma_f32_16x16x32_bf16 v[128:131],a[128:131],a[192:195],0
s_add_u32 m0,s59,0
ds_read_b64_tr_b16 a[140:141],v22 offset:17416
ds_read_b64_tr_b16 a[142:143],v22 offset:19464
s_waitcnt lgkmcnt(10)
v_mfma_f32_16x16x32_bf16 v[128:131],a[132:135],a[196:199],v[128:131]
s_waitcnt lgkmcnt(9)
v_mfma_f32_16x16x32_bf16 v[136:139],a[132:135],a[204:207],0
s_waitcnt lgkmcnt(8)
v_mfma_f32_16x16x32_bf16 v[136:139],a[128:131],a[200:203],v[136:139]
s_waitcnt lgkmcnt(7)
v_mfma_f32_16x16x32_bf16 v[144:147],a[128:131],a[208:211],0
s_waitcnt lgkmcnt(6)
buffer_load_dwordx4 v48,s[28:31],s57 offen lds
v_mfma_f32_16x16x32_bf16 v[144:147],a[132:135],a[212:215],v[144:147]
s_waitcnt lgkmcnt(5)
v_mfma_f32_16x16x32_bf16 v[152:155],a[132:135],a[220:223],0
s_waitcnt lgkmcnt(4)
v_mfma_f32_16x16x32_bf16 v[152:155],a[128:131],a[216:219],v[152:155]
s_waitcnt lgkmcnt(2)
ds_read_b64_tr_b16 a[144:145],v22 offset:1280
ds_read_b64_tr_b16 a[146:147],v22 offset:3328
v_mfma_f32_16x16x32_bf16 v[132:135],a[136:139],a[192:195],0
s_waitcnt lgkmcnt(2)
ds_read_b64_tr_b16 a[148:149],v22 offset:17664
ds_read_b64_tr_b16 a[150:151],v22 offset:19712
v_mfma_f32_16x16x32_bf16 v[132:135],a[140:143],a[196:199],v[132:135]
buffer_load_dwordx4 v49,s[28:31],s57 offen offset:1024 lds
v_mfma_f32_16x16x32_bf16 v[140:143],a[140:143],a[204:207],0
v_mfma_f32_16x16x32_bf16 v[140:143],a[136:139],a[200:203],v[140:143]
v_mfma_f32_16x16x32_bf16 v[148:151],a[136:139],a[208:211],0
v_mfma_f32_16x16x32_bf16 v[148:151],a[140:143],a[212:215],v[148:151]
v_mfma_f32_16x16x32_bf16 v[156:159],a[140:143],a[220:223],0
buffer_load_dwordx4 v50,s[28:31],s57 offen offset:2048 lds
v_mfma_f32_16x16x32_bf16 v[156:159],a[136:139],a[216:219],v[156:159]
s_waitcnt lgkmcnt(2)
ds_read_b64_tr_b16 a[152:153],v22 offset:1288
ds_read_b64_tr_b16 a[154:155],v22 offset:3336
v_mfma_f32_16x16x32_bf16 v[160:163],a[144:147],a[192:195],0
s_waitcnt lgkmcnt(2)
ds_read_b64_tr_b16 a[156:157],v22 offset:17672
ds_read_b64_tr_b16 a[158:159],v22 offset:19720
v_mfma_f32_16x16x32_bf16 v[160:163],a[148:151],a[196:199],v[160:163]
v_mfma_f32_16x16x32_bf16 v[168:171],a[148:151],a[204:207],0
v_mfma_f32_16x16x32_bf16 v[168:171],a[144:147],a[200:203],v[168:171]
buffer_load_dwordx4 v51,s[28:31],s57 offen offset:3072 lds
v_mfma_f32_16x16x32_bf16 v[176:179],a[144:147],a[208:211],0
s_add_u32 m0,s59,4096
v_mfma_f32_16x16x32_bf16 v[176:179],a[148:151],a[212:215],v[176:179]
v_mfma_f32_16x16x32_bf16 v[184:187],a[148:151],a[220:223],0
v_mfma_f32_16x16x32_bf16 v[184:187],a[144:147],a[216:219],v[184:187]
s_waitcnt lgkmcnt(2)
v_mfma_f32_16x16x32_bf16 v[164:167],a[152:155],a[192:195],0
s_waitcnt lgkmcnt(0)
buffer_load_dwordx4 v52,s[28:31],s57 offen lds
ds_read_b64_tr_b16 a[160:161],v22 offset:1536
ds_read_b64_tr_b16 a[162:163],v22 offset:3584
v_mfma_f32_16x16x32_bf16 v[164:167],a[156:159],a[196:199],v[164:167]
ds_read_b64_tr_b16 a[164:165],v22 offset:17920
ds_read_b64_tr_b16 a[166:167],v22 offset:19968
v_mfma_f32_16x16x32_bf16 v[172:175],a[156:159],a[204:207],0
v_mfma_f32_16x16x32_bf16 v[172:175],a[152:155],a[200:203],v[172:175]
v_mfma_f32_16x16x32_bf16 v[180:183],a[152:155],a[208:211],0
v_mfma_f32_16x16x32_bf16 v[180:183],a[156:159],a[212:215],v[180:183]
buffer_load_dwordx4 v53,s[28:31],s57 offen offset:1024 lds
v_mfma_f32_16x16x32_bf16 v[188:191],a[156:159],a[220:223],0
s_waitcnt vmcnt(14)
v_mfma_f32_16x16x32_bf16 v[188:191],a[152:155],a[216:219],v[188:191]
s_barrier
ds_read_b64_tr_b16 a[168:169],v22 offset:1544
ds_read_b64_tr_b16 a[170:171],v22 offset:3592
s_waitcnt lgkmcnt(4)
v_mfma_f32_16x16x32_bf16 v[192:195],a[160:163],a[192:195],0
s_waitcnt lgkmcnt(2)
ds_read_b64_tr_b16 a[172:173],v22 offset:17928
ds_read_b64_tr_b16 a[174:175],v22 offset:19976
v_mfma_f32_16x16x32_bf16 v[192:195],a[164:167],a[196:199],v[192:195]
buffer_load_dwordx4 v54,s[28:31],s57 offen offset:2048 lds
v_mfma_f32_16x16x32_bf16 v[200:203],a[164:167],a[204:207],0
s_lshl_b32 s44,s41,13
s_add_u32 s44,s44,104857600
s_mov_b64 s[64:65],s[12:13]
v_mfma_f32_16x16x32_bf16 v[200:203],a[160:163],a[200:203],v[200:203]
s_add_u32 s64,s64,s44
s_addc_u32 s65,s65,0
s_and_b32 s65,s65,0xffff
v_mfma_f32_16x16x32_bf16 v[208:211],a[160:163],a[208:211],0
s_mov_b32 s66,8192
s_mov_b32 s67,0x20000
v_mov_b32 v14,4096
v_mfma_f32_16x16x32_bf16 v[208:211],a[164:167],a[212:215],v[208:211]
s_mov_b64 s[72:73],s[14:15]
buffer_load_dwordx4 v55,s[28:31],s57 offen offset:3072 lds
v_mfma_f32_16x16x32_bf16 v[216:219],a[164:167],a[220:223],0
s_add_u32 m0,s58,0
s_and_b32 s73,s73,0xffff
ds_read_b64_tr_b16 a[176:177],v22 offset:1792
v_mfma_f32_16x16x32_bf16 v[216:219],a[160:163],a[216:219],v[216:219]
s_mov_b32 s74,5242880
s_mov_b32 s75,0x20000
ds_read_b64_tr_b16 a[178:179],v22 offset:3840
s_waitcnt lgkmcnt(4)
v_mfma_f32_16x16x32_bf16 v[196:199],a[168:171],a[192:195],0
s_waitcnt lgkmcnt(2)
ds_read_b64_tr_b16 a[180:181],v22 offset:18176
ds_read_b64_tr_b16 a[182:183],v22 offset:20224
v_mfma_f32_16x16x32_bf16 v[196:199],a[172:175],a[196:199],v[196:199]
s_lshl_b32 s83,%1,2
buffer_load_dwordx4 v56,s[32:35],s56 offen lds
v_mfma_f32_16x16x32_bf16 v[204:207],a[172:175],a[204:207],0
s_add_u32 m0,s58,1024
v_lshlrev_b32 v15,3,v6
v_add_u32 v16,128,v15
v_mfma_f32_16x16x32_bf16 v[204:207],a[168:171],a[200:203],v[204:207]
s_add_u32 s83,s83,s40
ds_read_b64_tr_b16 a[184:185],v22 offset:1800
v_lshrrev_b32 v16,10,v16
v_mfma_f32_16x16x32_bf16 v[212:215],a[168:171],a[208:211],0
s_mul_i32 s83,s83,5120
ds_read_b64_tr_b16 a[186:187],v22 offset:3848
v_lshl_add_u32 v15,v16,24,v15
v_mfma_f32_16x16x32_bf16 v[212:215],a[172:175],a[212:215],v[212:215]
s_lshl_b32 s43,s41,5
buffer_load_dwordx4 v57,s[32:35],s56 offen lds
v_mfma_f32_16x16x32_bf16 v[220:223],a[172:175],a[220:223],0
s_lshl_b32 s44,s42,1
s_add_u32 s43,s43,s44
ds_read_b64_tr_b16 a[188:189],v22 offset:18184
v_mfma_f32_16x16x32_bf16 v[220:223],a[168:171],a[216:219],v[220:223]
s_add_u32 m0,s58,2048
s_lshl_b32 s44,s43,18
ds_read_b64_tr_b16 a[190:191],v22 offset:20232
s_waitcnt lgkmcnt(6)
v_mfma_f32_16x16x32_bf16 v[224:227],a[176:179],a[192:195],0
s_mov_b64 s[20:21],s[6:7]
s_mov_b32 s22,524288
ds_read_b64_tr_b16 a[128:129],v22
s_waitcnt lgkmcnt(5)
v_mfma_f32_16x16x32_bf16 v[224:227],a[180:183],a[196:199],v[224:227]
s_add_u32 s20,s20,s44
s_addc_u32 s21,s21,0
ds_read_b64_tr_b16 a[130:131],v22 offset:2048
v_mfma_f32_16x16x32_bf16 v[232:235],a[180:183],a[204:207],0
)A"
R"A(s_and_b32 s21,s21,0xffff
buffer_load_dwordx4 v58,s[32:35],s56 offen lds
v_mfma_f32_16x16x32_bf16 v[232:235],a[176:179],a[200:203],v[232:235]
s_mov_b32 s23,0x20000
s_mov_b64 s[24:25],s[8:9]
ds_read_b64_tr_b16 a[132:133],v22 offset:16384
v_mfma_f32_16x16x32_bf16 v[240:243],a[176:179],a[208:211],0
s_add_u32 m0,s58,3072
s_mov_b32 s26,524288
ds_read_b64_tr_b16 a[134:135],v22 offset:18432
v_mfma_f32_16x16x32_bf16 v[240:243],a[180:183],a[212:215],v[240:243]
s_add_u32 s24,s24,s44
s_addc_u32 s25,s25,0
ds_read_b64_tr_b16 a[136:137],v22 offset:8
v_mfma_f32_16x16x32_bf16 v[248:251],a[180:183],a[220:223],0
s_and_b32 s25,s25,0xffff
s_mov_b32 s27,0x20000
ds_read_b64_tr_b16 a[138:139],v22 offset:2056
v_mfma_f32_16x16x32_bf16 v[248:251],a[176:179],a[216:219],v[248:251]
s_lshl_b32 s44,s43,16
buffer_load_dwordx4 v59,s[32:35],s56 offen lds
s_waitcnt lgkmcnt(6)
v_mfma_f32_16x16x32_bf16 v[228:231],a[184:187],a[192:195],0
s_mov_b64 s[36:37],s[12:13]
s_mov_b32 s38,131072
ds_read_b64_tr_b16 a[140:141],v22 offset:16392
v_mfma_f32_16x16x32_bf16 v[228:231],a[188:191],a[196:199],v[228:231]
s_add_u32 m0,s58,4096
s_mov_b32 s39,0x20000
ds_read_b64_tr_b16 a[142:143],v22 offset:18440
v_mfma_f32_16x16x32_bf16 v[236:239],a[188:191],a[204:207],0
s_add_u32 s36,s36,s44
s_addc_u32 s37,s37,0
ds_read_b64_tr_b16 a[144:145],v22 offset:256
v_mfma_f32_16x16x32_bf16 v[236:239],a[184:187],a[200:203],v[236:239]
s_and_b32 s37,s37,0xffff
s_lshl_b32 s51,s40,12
ds_read_b64_tr_b16 a[146:147],v22 offset:2304
v_mfma_f32_16x16x32_bf16 v[244:247],a[184:187],a[208:211],0
s_add_u32 s52,s51,16384
buffer_load_dwordx4 v60,s[32:35],s56 offen lds
v_mfma_f32_16x16x32_bf16 v[244:247],a[188:191],a[212:215],v[244:247]
s_xor_b32 s59,s59,131072
s_mul_i32 s47,s41,5
ds_read_b64_tr_b16 a[148:149],v22 offset:16640
v_mfma_f32_16x16x32_bf16 v[252:255],a[188:191],a[220:223],0
s_add_u32 m0,s58,5120
s_add_u32 s47,s47,s42
ds_read_b64_tr_b16 a[150:151],v22 offset:18688
v_mfma_f32_16x16x32_bf16 v[252:255],a[184:187],a[216:219],v[252:255]
s_and_b32 s47,s47,15
s_add_u32 s57,s57,128
ds_read_b64_tr_b16 a[152:153],v22 offset:264
s_waitcnt lgkmcnt(7)
v_mfma_f32_16x16x32_bf16 a[0:3],a[128:131],a[192:195],0
s_lshl_b32 s47,s47,14
s_mov_b32 s54,0
ds_read_b64_tr_b16 a[154:155],v22 offset:2312
v_mfma_f32_16x16x32_bf16 a[0:3],a[132:135],a[196:199],a[0:3]
s_lshl_b32 s55,s40,14
buffer_load_dwordx4 v61,s[32:35],s56 offen lds
v_mfma_f32_16x16x32_bf16 a[32:35],a[132:135],a[204:207],0
s_mov_b32 s61,0x3e0293ee
s_mov_b32 s63,0x5d800000
ds_read_b64_tr_b16 a[156:157],v22 offset:16648
v_mfma_f32_16x16x32_bf16 a[32:35],a[128:131],a[200:203],a[32:35]
s_add_u32 m0,s58,6144
s_lshl_b32 s44,s40,2
ds_read_b64_tr_b16 a[158:159],v22 offset:18696
v_mfma_f32_16x16x32_bf16 a[64:67],a[128:131],a[208:211],0
s_add_u32 s44,s44,s70
s_mov_b32 s46,0xff800000
ds_read_b64_tr_b16 a[160:161],v22 offset:512
v_mfma_f32_16x16x32_bf16 a[64:67],a[132:135],a[212:215],a[64:67]
s_cmp_eq_u32 s44,14
s_cselect_b32 s18,0x7149f2ca,s46
ds_read_b64_tr_b16 a[162:163],v22 offset:2560
v_mfma_f32_16x16x32_bf16 a[96:99],a[132:135],a[220:223],0
s_add_u32 s60,s60,32768
buffer_load_dwordx4 v62,s[32:35],s56 offen lds
v_mfma_f32_16x16x32_bf16 a[96:99],a[128:131],a[216:219],a[96:99]
s_cmp_eq_u32 s44,13
s_cselect_b32 s19,0x7149f2ca,s46
ds_read_b64_tr_b16 a[164:165],v22 offset:16896
v_mfma_f32_16x16x32_bf16 a[4:7],a[136:139],a[192:195],0
s_add_u32 m0,s58,7168
ds_read_b64_tr_b16 a[166:167],v22 offset:18944
v_lshlrev_b32 v70,8,v65
s_waitcnt lgkmcnt(6)
v_mfma_f32_16x16x32_bf16 a[4:7],a[140:143],a[196:199],a[4:7]
s_cmp_eq_u32 s44,12
s_cselect_b32 s100,0x7149f2ca,s46
ds_read_b64_tr_b16 a[168:169],v22 offset:520
v_mfma_f32_16x16x32_bf16 a[36:39],a[140:143],a[204:207],0
s_cmp_eq_u32 s44,15
s_cselect_b32 s101,0x7149f2ca,s46
ds_read_b64_tr_b16 a[170:171],v22 offset:2568
v_mfma_f32_16x16x32_bf16 a[36:39],a[136:139],a[200:203],a[36:39]
s_add_u32 s58,s58,32768
buffer_load_dwordx4 v63,s[32:35],s56 offen lds
v_mfma_f32_16x16x32_bf16 a[68:71],a[136:139],a[208:211],0
s_add_u32 s56,s56,524288
ds_read_b64_tr_b16 a[172:173],v22 offset:16904
v_lshl_add_u32 v70,v66,6,v70
v_mfma_f32_16x16x32_bf16 a[68:71],a[140:143],a[212:215],a[68:71]
s_add_u32 m0,s59,0
ds_read_b64_tr_b16 a[174:175],v22 offset:18952
v_add_u32 v70,s51,v70
v_mfma_f32_16x16x32_bf16 a[100:103],a[140:143],a[220:223],0
s_mul_i32 s81,s70,1310720
ds_read_b64_tr_b16 a[176:177],v22 offset:768
v_lshl_add_u32 v4,v64,4,v70
v_mfma_f32_16x16x32_bf16 a[100:103],a[136:139],a[216:219],a[100:103]
s_mul_i32 s44,s71,320
ds_read_b64_tr_b16 a[178:179],v22 offset:2816
v_xor_b32 v71,2,v64
v_mfma_f32_16x16x32_bf16 a[8:11],a[144:147],a[192:195],0
s_add_u32 s81,s81,s44
buffer_load_dwordx4 v48,s[28:31],s57 offen lds
v_mfma_f32_16x16x32_bf16 a[8:11],a[148:151],a[196:199],a[8:11]
s_mul_i32 s82,s40,10240
ds_read_b64_tr_b16 a[180:181],v22 offset:17152
v_lshl_add_u32 v5,v71,4,v70
v_mfma_f32_16x16x32_bf16 a[40:43],a[148:151],a[204:207],0
s_mul_i32 s44,s40,40960
ds_read_b64_tr_b16 a[182:183],v22 offset:19200
v_lshlrev_b32 v72,10,v65
v_mfma_f32_16x16x32_bf16 a[40:43],a[144:147],a[200:203],a[40:43]
s_lshr_b32 s92,%1,3
ds_read_b64_tr_b16 a[184:185],v22 offset:776
v_lshl_add_u32 v72,v64,6,v72
v_mfma_f32_16x16x32_bf16 a[72:75],a[144:147],a[208:211],0
s_lshl_b32 s92,s92,2
ds_read_b64_tr_b16 a[186:187],v22 offset:2824
v_and_b32 v73,2,v65
v_mfma_f32_16x16x32_bf16 a[72:75],a[148:151],a[212:215],a[72:75]
s_add_u32 s92,s92,s40
buffer_load_dwordx4 v49,s[28:31],s57 offen offset:1024 lds
v_mfma_f32_16x16x32_bf16 a[104:107],a[148:151],a[220:223],0
s_lshl_b32 s92,s92,7
ds_read_b64_tr_b16 a[188:189],v22 offset:17160
v_xor_b32 v73,v73,v66
v_mfma_f32_16x16x32_bf16 a[104:107],a[144:147],a[216:219],a[104:107]
s_add_u32 s93,s92,L_tailseg_%=-L_pc_%=
ds_read_b64_tr_b16 a[190:191],v22 offset:19208
v_lshl_add_u32 v72,v73,4,v72
v_mfma_f32_16x16x32_bf16 a[12:15],a[152:155],a[192:195],0
s_waitcnt lgkmcnt(6)
v_add_u32 v1,98304,v72
v_lshlrev_b32 v72,10,v66
v_mfma_f32_16x16x32_bf16 a[12:15],a[156:159],a[196:199],a[12:15]
v_lshl_add_u32 v72,v65,6,v72
v_lshl_add_u32 v72,v64,3,v72
v_mfma_f32_16x16x32_bf16 a[44:47],a[156:159],a[204:207],0
buffer_load_dwordx4 v50,s[28:31],s57 offen offset:2048 lds
v_mfma_f32_16x16x32_bf16 a[44:47],a[152:155],a[200:203],a[44:47]
v_add_u32 v72,114688,v72
v_and_b32 v73,1,v66
v_mfma_f32_16x16x32_bf16 a[76:79],a[152:155],a[208:211],0
s_cmp_eq_u32 s60,131072
s_cselect_b32 s60,32768,s60
v_lshl_add_u32 v2,v73,5,v72
v_mfma_f32_16x16x32_bf16 a[76:79],a[156:159],a[212:215],a[76:79]
v_xor_b32 v73,1,v73
v_add_u32 v22,s60,v23
v_mfma_f32_16x16x32_bf16 a[108:111],a[156:159],a[220:223],0
s_add_u32 s60,s60,32768
v_xor_b32 v20,131072,v20
v_xor_b32 v21,131072,v21
v_mfma_f32_16x16x32_bf16 a[108:111],a[152:155],a[216:219],a[108:111]
s_waitcnt vmcnt(11) lgkmcnt(0)
buffer_load_dwordx4 v51,s[28:31],s57 offen offset:3072 lds
v_mfma_f32_16x16x32_bf16 a[16:19],a[160:163],a[192:195],0
s_barrier
s_add_u32 m0,s59,4096
ds_read_b128 a[224:227],v20
v_mfma_f32_16x16x32_bf16 a[16:19],a[164:167],a[196:199],a[16:19]
s_xor_b32 s59,s59,131072
ds_read_b64_tr_b16 a[128:129],v22 offset:1024
v_lshl_add_u32 v3,v73,5,v72
v_mfma_f32_16x16x32_bf16 a[48:51],a[164:167],a[204:207],0
ds_read_b64_tr_b16 a[130:131],v22 offset:3072
v_mov_b32 v73,0x3f803f80
v_mfma_f32_16x16x32_bf16 a[48:51],a[160:163],a[200:203],a[48:51]
buffer_load_dwordx4 v52,s[28:31],s57 offen lds
v_mfma_f32_16x16x32_bf16 a[80:83],a[160:163],a[208:211],0
s_cmp_ge_u32 s58,131072
s_cselect_b32 s62,98304,0
ds_read_b128 a[228:231],v21
v_mfma_f32_16x16x32_bf16 a[80:83],a[164:167],a[212:215],a[80:83]
s_sub_u32 s58,s58,s62
ds_read_b64_tr_b16 a[132:133],v22 offset:17408
v_add_u32 v70,0,v12
v_mfma_f32_16x16x32_bf16 a[112:115],a[164:167],a[220:223],0
ds_read_b64_tr_b16 a[134:135],v22 offset:19456
v_mul_u32_u24 v71,3277,v70
v_mfma_f32_16x16x32_bf16 a[112:115],a[160:163],a[216:219],a[112:115]
ds_read_b128 a[232:235],v20 offset:2048
v_lshrrev_b32 v71,16,v71
v_mfma_f32_16x16x32_bf16 a[20:23],a[168:171],a[192:195],0
buffer_load_dwordx4 v53,s[28:31],s57 offen offset:1024 lds
v_mfma_f32_16x16x32_bf16 a[20:23],a[172:175],a[196:199],a[20:23]
ds_read_b128 a[236:239],v21 offset:2048
v_mul_u32_u24 v72,20,v71
v_mfma_f32_16x16x32_bf16 a[52:55],a[172:175],a[204:207],0
ds_read_b128 a[240:243],v20 offset:4096
v_sub_u32 v72,v70,v72
v_mfma_f32_16x16x32_bf16 a[52:55],a[168:171],a[200:203],a[52:55]
ds_read_b128 a[244:247],v21 offset:4096
v_add_u32 v70,64,v12
v_mfma_f32_16x16x32_bf16 a[84:87],a[168:171],a[208:211],0
buffer_load_dword v13,v14,s[64:67],0 offen sc1
v_mfma_f32_16x16x32_bf16 a[84:87],a[172:175],a[212:215],a[84:87]
ds_read_b128 a[248:251],v20 offset:6144
v_lshlrev_b32 v24,10,v6
v_mfma_f32_16x16x32_bf16 a[116:119],a[172:175],a[220:223],0
s_cmp_eq_u32 s60,131072
s_cselect_b32 s60,32768,s60
ds_read_b128 a[252:255],v21 offset:6144
v_mfma_f32_16x16x32_bf16 a[116:119],a[168:171],a[216:219],a[116:119]
ds_read_b64_tr_b16 a[136:137],v22 offset:1032
v_xor_b32 v20,131072,v20
v_mfma_f32_16x16x32_bf16 a[24:27],a[176:179],a[192:195],0
buffer_load_dwordx4 v54,s[28:31],s57 offen offset:2048 lds
v_mfma_f32_16x16x32_bf16 a[24:27],a[180:183],a[196:199],a[24:27]
ds_read_b64_tr_b16 a[138:139],v22 offset:3080
v_xor_b32 v21,131072,v21
v_mfma_f32_16x16x32_bf16 a[56:59],a[180:183],a[204:207],0
ds_read_b64_tr_b16 a[140:141],v22 offset:17416
v_mfma_f32_16x16x32_bf16 a[56:59],a[176:179],a[200:203],a[56:59]
ds_read_b64_tr_b16 a[142:143],v22 offset:19464
v_mfma_f32_16x16x32_bf16 a[88:91],a[176:179],a[208:211],0
buffer_load_dwordx4 v55,s[28:31],s57 offen offset:3072 lds
v_mfma_f32_16x16x32_bf16 a[88:91],a[180:183],a[212:215],a[88:91]
s_add_u32 s57,s57,128
ds_read_b64_tr_b16 a[144:145],v22 offset:1280
v_mfma_f32_16x16x32_bf16 a[120:123],a[180:183],a[220:223],0
ds_read_b64_tr_b16 a[146:147],v22 offset:3328
v_mfma_f32_16x16x32_bf16 a[120:123],a[176:179],a[216:219],a[120:123]
ds_read_b64_tr_b16 a[148:149],v22 offset:17664
v_mfma_f32_16x16x32_bf16 a[28:31],a[184:187],a[192:195],0
ds_read_b64_tr_b16 a[150:151],v22 offset:19712
v_mfma_f32_16x16x32_bf16 a[28:31],a[188:191],a[196:199],a[28:31]
ds_read_b64_tr_b16 a[152:153],v22 offset:1288
ds_read_b64_tr_b16 a[154:155],v22 offset:3336
ds_read_b64_tr_b16 a[156:157],v22 offset:17672
ds_read_b64_tr_b16 a[158:159],v22 offset:19720
v_mfma_f32_16x16x32_bf16 a[60:63],a[188:191],a[204:207],0
v_mfma_f32_16x16x32_bf16 a[60:63],a[184:187],a[200:203],a[60:63]
v_cmp_gt_u32 vcc,2,v64
s_nop 1
v_cndmask_b32 v28,0,v73,vcc
v_mfma_f32_16x16x32_bf16 a[92:95],a[184:187],a[208:211],0
v_and_b32 v73,1,v64
v_mul_u32_u24 v73,0xaaaa,v73
v_mfma_f32_16x16x32_bf16 a[92:95],a[188:191],a[212:215],a[92:95]
v_add_u32 v73,0x4444,v73
v_lshl_or_b32 v27,v73,16,v73
v_mfma_f32_16x16x32_bf16 a[124:127],a[188:191],a[220:223],0
s_add_u32 m0,s58,0
v_mov_b32 v29,v28
v_mov_b32 v30,v28
v_mfma_f32_16x16x32_bf16 a[124:127],a[184:187],a[216:219],a[124:127]
s_waitcnt lgkmcnt(5)
v_mov_b32 v31,v28
v_bfe_u32 v73,v71,2,1
v_mfma_f32_16x16x32_bf16 v[128:131],a[128:131],a[224:227],v[128:131]
v_lshlrev_b32 v73,1,v73
v_xor_b32 v72,v72,v73
v_mfma_f32_16x16x32_bf16 v[128:131],a[132:135],a[228:231],v[128:131]
buffer_load_dwordx4 v56,s[32:35],s56 offen lds
v_mfma_f32_16x16x32_bf16 v[136:139],a[132:135],a[236:239],v[136:139]
v_mul_u32_u24 v71,1280,v71
v_lshl_add_u32 v71,v72,4,v71
v_mfma_f32_16x16x32_bf16 v[136:139],a[128:131],a[232:235],v[136:139]
s_add_u32 m0,s58,1024
v_add_u32 v71,s44,v71
v_subrev_u32 v7,0,v71
v_mfma_f32_16x16x32_bf16 v[144:147],a[128:131],a[240:243],v[144:147]
ds_read_b64_tr_b16 a[160:161],v22 offset:1536
v_mul_u32_u24 v71,3277,v70
v_mfma_f32_16x16x32_bf16 v[144:147],a[132:135],a[244:247],v[144:147]
ds_read_b64_tr_b16 a[162:163],v22 offset:3584
v_lshrrev_b32 v71,16,v71
v_mfma_f32_16x16x32_bf16 v[152:155],a[132:135],a[252:255],v[152:155]
buffer_load_dwordx4 v57,s[32:35],s56 offen lds
v_mfma_f32_16x16x32_bf16 v[152:155],a[128:131],a[248:251],v[152:155]
ds_read_b64_tr_b16 a[164:165],v22 offset:17920
v_mul_u32_u24 v72,20,v71
v_mfma_f32_16x16x32_bf16 v[132:135],a[136:139],a[224:227],v[132:135]
ds_read_b64_tr_b16 a[166:167],v22 offset:19968
v_sub_u32 v72,v70,v72
v_mfma_f32_16x16x32_bf16 v[132:135],a[140:143],a[228:231],v[132:135]
)A"
R"A(s_add_u32 m0,s58,2048
v_bfe_u32 v73,v71,2,1
v_lshlrev_b32 v73,1,v73
v_mfma_f32_16x16x32_bf16 v[140:143],a[140:143],a[236:239],v[140:143]
ds_read_b64_tr_b16 a[168:169],v22 offset:1544
v_xor_b32 v72,v72,v73
v_mfma_f32_16x16x32_bf16 v[140:143],a[136:139],a[232:235],v[140:143]
ds_read_b64_tr_b16 a[170:171],v22 offset:3592
v_mul_u32_u24 v71,1280,v71
v_mfma_f32_16x16x32_bf16 v[148:151],a[136:139],a[240:243],v[148:151]
buffer_load_dwordx4 v58,s[32:35],s56 offen lds
v_mfma_f32_16x16x32_bf16 v[148:151],a[140:143],a[244:247],v[148:151]
ds_read_b64_tr_b16 a[172:173],v22 offset:17928
v_lshl_add_u32 v71,v72,4,v71
v_mfma_f32_16x16x32_bf16 v[156:159],a[140:143],a[252:255],v[156:159]
ds_read_b64_tr_b16 a[174:175],v22 offset:19976
v_add_u32 v71,s44,v71
v_mfma_f32_16x16x32_bf16 v[156:159],a[136:139],a[248:251],v[156:159]
s_add_u32 m0,s58,3072
v_subrev_u32 v8,1024,v71
v_add_u32 v70,128,v12
v_mfma_f32_16x16x32_bf16 v[160:163],a[144:147],a[224:227],v[160:163]
s_waitcnt lgkmcnt(4)
ds_read_b64_tr_b16 a[176:177],v22 offset:1792
v_mul_u32_u24 v71,3277,v70
v_mfma_f32_16x16x32_bf16 v[160:163],a[148:151],a[228:231],v[160:163]
ds_read_b64_tr_b16 a[178:179],v22 offset:3840
v_lshrrev_b32 v71,16,v71
v_mfma_f32_16x16x32_bf16 v[168:171],a[148:151],a[236:239],v[168:171]
buffer_load_dwordx4 v59,s[32:35],s56 offen lds
v_mfma_f32_16x16x32_bf16 v[168:171],a[144:147],a[232:235],v[168:171]
ds_read_b64_tr_b16 a[180:181],v22 offset:18176
v_mul_u32_u24 v72,20,v71
v_mfma_f32_16x16x32_bf16 v[176:179],a[144:147],a[240:243],v[176:179]
s_add_u32 m0,s58,4096
ds_read_b64_tr_b16 a[182:183],v22 offset:20224
v_sub_u32 v72,v70,v72
v_mfma_f32_16x16x32_bf16 v[176:179],a[148:151],a[244:247],v[176:179]
v_bfe_u32 v73,v71,2,1
v_lshlrev_b32 v73,1,v73
v_mfma_f32_16x16x32_bf16 v[184:187],a[148:151],a[252:255],v[184:187]
ds_read_b64_tr_b16 a[184:185],v22 offset:1800
v_xor_b32 v72,v72,v73
v_mfma_f32_16x16x32_bf16 v[184:187],a[144:147],a[248:251],v[184:187]
buffer_load_dwordx4 v60,s[32:35],s56 offen lds
v_mfma_f32_16x16x32_bf16 v[164:167],a[152:155],a[224:227],v[164:167]
ds_read_b64_tr_b16 a[186:187],v22 offset:3848
v_mul_u32_u24 v71,1280,v71
v_mfma_f32_16x16x32_bf16 v[164:167],a[156:159],a[228:231],v[164:167]
ds_read_b64_tr_b16 a[188:189],v22 offset:18184
v_lshl_add_u32 v71,v72,4,v71
v_mfma_f32_16x16x32_bf16 v[172:175],a[156:159],a[236:239],v[172:175]
s_add_u32 m0,s58,5120
ds_read_b64_tr_b16 a[190:191],v22 offset:20232
v_add_u32 v71,s44,v71
v_mfma_f32_16x16x32_bf16 v[172:175],a[152:155],a[232:235],v[172:175]
v_subrev_u32 v9,2048,v71
v_add_u32 v70,192,v12
v_mfma_f32_16x16x32_bf16 v[180:183],a[152:155],a[240:243],v[180:183]
v_mul_u32_u24 v71,3277,v70
v_lshrrev_b32 v71,16,v71
v_mfma_f32_16x16x32_bf16 v[180:183],a[156:159],a[244:247],v[180:183]
buffer_load_dwordx4 v61,s[32:35],s56 offen lds
v_mfma_f32_16x16x32_bf16 v[188:191],a[156:159],a[252:255],v[188:191]
ds_read_b64_tr_b16 a[128:129],v22
v_mul_u32_u24 v72,20,v71
v_mfma_f32_16x16x32_bf16 v[188:191],a[152:155],a[248:251],v[188:191]
ds_read_b64_tr_b16 a[130:131],v22 offset:2048
v_sub_u32 v72,v70,v72
v_mfma_f32_16x16x32_bf16 v[192:195],a[160:163],a[224:227],v[192:195]
s_add_u32 m0,s58,6144
ds_read_b64_tr_b16 a[132:133],v22 offset:16384
v_bfe_u32 v73,v71,2,1
v_mfma_f32_16x16x32_bf16 v[192:195],a[164:167],a[228:231],v[192:195]
ds_read_b64_tr_b16 a[134:135],v22 offset:18432
v_lshlrev_b32 v73,1,v73
v_mfma_f32_16x16x32_bf16 v[200:203],a[164:167],a[236:239],v[200:203]
v_xor_b32 v72,v72,v73
v_mul_u32_u24 v71,1280,v71
v_mfma_f32_16x16x32_bf16 v[200:203],a[160:163],a[232:235],v[200:203]
buffer_load_dwordx4 v62,s[32:35],s56 offen lds
v_mfma_f32_16x16x32_bf16 v[208:211],a[160:163],a[240:243],v[208:211]
ds_read_b64_tr_b16 a[136:137],v22 offset:8
v_lshl_add_u32 v71,v72,4,v71
v_mfma_f32_16x16x32_bf16 v[208:211],a[164:167],a[244:247],v[208:211]
s_add_u32 m0,s58,7168
ds_read_b64_tr_b16 a[138:139],v22 offset:2056
v_add_u32 v71,s44,v71
v_mfma_f32_16x16x32_bf16 v[216:219],a[164:167],a[252:255],v[216:219]
s_add_u32 s58,s58,32768
ds_read_b64_tr_b16 a[140:141],v22 offset:16392
v_subrev_u32 v10,3072,v71
v_mfma_f32_16x16x32_bf16 v[216:219],a[160:163],a[248:251],v[216:219]
s_waitcnt lgkmcnt(5)
ds_read_b64_tr_b16 a[142:143],v22 offset:18440
v_add_u32 v70,256,v12
v_mfma_f32_16x16x32_bf16 v[196:199],a[168:171],a[224:227],v[196:199]
buffer_load_dwordx4 v63,s[32:35],s56 offen lds
v_mfma_f32_16x16x32_bf16 v[196:199],a[172:175],a[228:231],v[196:199]
s_add_u32 s56,s56,524288
v_mul_u32_u24 v71,3277,v70
v_lshrrev_b32 v71,16,v71
v_mfma_f32_16x16x32_bf16 v[204:207],a[172:175],a[236:239],v[204:207]
ds_read_b64_tr_b16 a[144:145],v22 offset:256
v_mul_u32_u24 v72,20,v71
v_mfma_f32_16x16x32_bf16 v[204:207],a[168:171],a[232:235],v[204:207]
s_add_u32 m0,s59,0
ds_read_b64_tr_b16 a[146:147],v22 offset:2304
v_sub_u32 v72,v70,v72
v_mfma_f32_16x16x32_bf16 v[212:215],a[168:171],a[240:243],v[212:215]
ds_read_b64_tr_b16 a[148:149],v22 offset:16640
v_bfe_u32 v73,v71,2,1
v_mfma_f32_16x16x32_bf16 v[212:215],a[172:175],a[244:247],v[212:215]
ds_read_b64_tr_b16 a[150:151],v22 offset:18688
v_lshlrev_b32 v73,1,v73
v_mfma_f32_16x16x32_bf16 v[220:223],a[172:175],a[252:255],v[220:223]
buffer_load_dwordx4 v48,s[28:31],s57 offen lds
v_mfma_f32_16x16x32_bf16 v[220:223],a[168:171],a[248:251],v[220:223]
v_xor_b32 v72,v72,v73
v_mul_u32_u24 v71,1280,v71
v_mfma_f32_16x16x32_bf16 v[224:227],a[176:179],a[224:227],v[224:227]
v_lshl_add_u32 v71,v72,4,v71
v_add_u32 v71,s44,v71
v_mfma_f32_16x16x32_bf16 v[224:227],a[180:183],a[228:231],v[224:227]
ds_read_b64_tr_b16 a[152:153],v22 offset:264
v_subrev_u32 v11,0,v71
v_mfma_f32_16x16x32_bf16 v[232:235],a[180:183],a[236:239],v[232:235]
s_cmp_ge_u32 s58,131072
s_cselect_b32 s62,98304,0
ds_read_b64_tr_b16 a[154:155],v22 offset:2312
v_mfma_f32_16x16x32_bf16 v[232:235],a[176:179],a[232:235],v[232:235]
s_sub_u32 s58,s58,s62
ds_read_b64_tr_b16 a[156:157],v22 offset:16648
v_mfma_f32_16x16x32_bf16 v[240:243],a[176:179],a[240:243],v[240:243]
buffer_load_dwordx4 v49,s[28:31],s57 offen offset:1024 lds
v_mfma_f32_16x16x32_bf16 v[240:243],a[180:183],a[244:247],v[240:243]
ds_read_b64_tr_b16 a[158:159],v22 offset:18696
v_mfma_f32_16x16x32_bf16 v[248:251],a[180:183],a[252:255],v[248:251]
ds_read_b64_tr_b16 a[160:161],v22 offset:512
v_mfma_f32_16x16x32_bf16 v[248:251],a[176:179],a[248:251],v[248:251]
ds_read_b64_tr_b16 a[162:163],v22 offset:2560
v_mfma_f32_16x16x32_bf16 v[228:231],a[184:187],a[224:227],v[228:231]
ds_read_b64_tr_b16 a[164:165],v22 offset:16896
v_mfma_f32_16x16x32_bf16 v[228:231],a[188:191],a[228:231],v[228:231]
buffer_load_dwordx4 v50,s[28:31],s57 offen offset:2048 lds
v_mfma_f32_16x16x32_bf16 v[236:239],a[188:191],a[236:239],v[236:239]
ds_read_b64_tr_b16 a[166:167],v22 offset:18944
v_mfma_f32_16x16x32_bf16 v[236:239],a[184:187],a[232:235],v[236:239]
ds_read_b64_tr_b16 a[168:169],v22 offset:520
v_mfma_f32_16x16x32_bf16 v[244:247],a[184:187],a[240:243],v[244:247]
ds_read_b64_tr_b16 a[170:171],v22 offset:2568
v_mfma_f32_16x16x32_bf16 v[244:247],a[188:191],a[244:247],v[244:247]
ds_read_b64_tr_b16 a[172:173],v22 offset:16904
v_mfma_f32_16x16x32_bf16 v[252:255],a[188:191],a[252:255],v[252:255]
ds_read_b64_tr_b16 a[174:175],v22 offset:18952
v_mfma_f32_16x16x32_bf16 v[252:255],a[184:187],a[248:251],v[252:255]
buffer_load_dwordx4 v51,s[28:31],s57 offen offset:3072 lds
v_mfma_f32_16x16x32_bf16 a[0:3],a[128:131],a[224:227],a[0:3]
s_waitcnt lgkmcnt(5)
ds_read_b64_tr_b16 a[176:177],v22 offset:768
v_mfma_f32_16x16x32_bf16 a[0:3],a[132:135],a[228:231],a[0:3]
ds_read_b64_tr_b16 a[178:179],v22 offset:2816
v_mfma_f32_16x16x32_bf16 a[32:35],a[132:135],a[236:239],a[32:35]
s_add_u32 m0,s59,4096
s_xor_b32 s59,s59,131072
ds_read_b64_tr_b16 a[180:181],v22 offset:17152
v_mfma_f32_16x16x32_bf16 a[32:35],a[128:131],a[232:235],a[32:35]
ds_read_b64_tr_b16 a[182:183],v22 offset:19200
v_mfma_f32_16x16x32_bf16 a[64:67],a[128:131],a[240:243],a[64:67]
ds_read_b64_tr_b16 a[184:185],v22 offset:776
v_mfma_f32_16x16x32_bf16 a[64:67],a[132:135],a[244:247],a[64:67]
buffer_load_dwordx4 v52,s[28:31],s57 offen lds
v_mfma_f32_16x16x32_bf16 a[96:99],a[132:135],a[252:255],a[96:99]
ds_read_b64_tr_b16 a[186:187],v22 offset:2824
v_mfma_f32_16x16x32_bf16 a[96:99],a[128:131],a[248:251],a[96:99]
ds_read_b64_tr_b16 a[188:189],v22 offset:17160
v_mfma_f32_16x16x32_bf16 a[4:7],a[136:139],a[224:227],a[4:7]
ds_read_b64_tr_b16 a[190:191],v22 offset:19208
v_add_u32 v22,s60,v23
v_mfma_f32_16x16x32_bf16 a[4:7],a[140:143],a[228:231],a[4:7]
v_mfma_f32_16x16x32_bf16 a[36:39],a[140:143],a[236:239],a[36:39]
buffer_load_dwordx4 v53,s[28:31],s57 offen offset:1024 lds
v_mfma_f32_16x16x32_bf16 a[36:39],a[136:139],a[232:235],a[36:39]
v_mfma_f32_16x16x32_bf16 a[68:71],a[136:139],a[240:243],a[68:71]
v_mfma_f32_16x16x32_bf16 a[68:71],a[140:143],a[244:247],a[68:71]
v_mfma_f32_16x16x32_bf16 a[100:103],a[140:143],a[252:255],a[100:103]
v_mfma_f32_16x16x32_bf16 a[100:103],a[136:139],a[248:251],a[100:103]
v_mfma_f32_16x16x32_bf16 a[8:11],a[144:147],a[224:227],a[8:11]
buffer_load_dwordx4 v54,s[28:31],s57 offen offset:2048 lds
v_mfma_f32_16x16x32_bf16 a[8:11],a[148:151],a[228:231],a[8:11]
v_mfma_f32_16x16x32_bf16 a[40:43],a[148:151],a[236:239],a[40:43]
v_mfma_f32_16x16x32_bf16 a[40:43],a[144:147],a[232:235],a[40:43]
v_mfma_f32_16x16x32_bf16 a[72:75],a[144:147],a[240:243],a[72:75]
v_mfma_f32_16x16x32_bf16 a[72:75],a[148:151],a[244:247],a[72:75]
v_mfma_f32_16x16x32_bf16 a[104:107],a[148:151],a[252:255],a[104:107]
buffer_load_dwordx4 v55,s[28:31],s57 offen offset:3072 lds
v_mfma_f32_16x16x32_bf16 a[104:107],a[144:147],a[248:251],a[104:107]
s_add_u32 s57,s57,128
v_mfma_f32_16x16x32_bf16 a[12:15],a[152:155],a[224:227],a[12:15]
v_mfma_f32_16x16x32_bf16 a[12:15],a[156:159],a[228:231],a[12:15]
v_mfma_f32_16x16x32_bf16 a[44:47],a[156:159],a[236:239],a[44:47]
v_mfma_f32_16x16x32_bf16 a[44:47],a[152:155],a[232:235],a[44:47]
v_mfma_f32_16x16x32_bf16 a[76:79],a[152:155],a[240:243],a[76:79]
v_mfma_f32_16x16x32_bf16 a[76:79],a[156:159],a[244:247],a[76:79]
v_mfma_f32_16x16x32_bf16 a[108:111],a[156:159],a[252:255],a[108:111]
v_mfma_f32_16x16x32_bf16 a[108:111],a[152:155],a[248:251],a[108:111]
s_waitcnt vmcnt(16) lgkmcnt(0)
v_mfma_f32_16x16x32_bf16 a[16:19],a[160:163],a[224:227],a[16:19]
s_barrier
ds_read_b128 a[192:195],v20
v_mfma_f32_16x16x32_bf16 a[16:19],a[164:167],a[228:231],a[16:19]
ds_read_b128 a[196:199],v21
v_mfma_f32_16x16x32_bf16 a[48:51],a[164:167],a[236:239],a[48:51]
ds_read_b128 a[200:203],v20 offset:2048
v_mfma_f32_16x16x32_bf16 a[48:51],a[160:163],a[232:235],a[48:51]
ds_read_b128 a[204:207],v21 offset:2048
v_mfma_f32_16x16x32_bf16 a[80:83],a[160:163],a[240:243],a[80:83]
ds_read_b128 a[208:211],v20 offset:4096
v_mfma_f32_16x16x32_bf16 a[80:83],a[164:167],a[244:247],a[80:83]
ds_read_b128 a[212:215],v21 offset:4096
v_mfma_f32_16x16x32_bf16 a[112:115],a[164:167],a[252:255],a[112:115]
ds_read_b128 a[216:219],v20 offset:6144
v_mfma_f32_16x16x32_bf16 a[112:115],a[160:163],a[248:251],a[112:115]
ds_read_b128 a[220:223],v21 offset:6144
v_mfma_f32_16x16x32_bf16 a[20:23],a[168:171],a[224:227],a[20:23]
ds_read_b64_tr_b16 a[128:129],v22 offset:1024
v_mfma_f32_16x16x32_bf16 a[20:23],a[172:175],a[228:231],a[20:23]
ds_read_b64_tr_b16 a[130:131],v22 offset:3072
v_mfma_f32_16x16x32_bf16 a[52:55],a[172:175],a[236:239],a[52:55]
ds_read_b64_tr_b16 a[132:133],v22 offset:17408
v_mfma_f32_16x16x32_bf16 a[52:55],a[168:171],a[232:235],a[52:55]
ds_read_b64_tr_b16 a[134:135],v22 offset:19456
v_mfma_f32_16x16x32_bf16 a[84:87],a[168:171],a[240:243],a[84:87]
ds_read_b64_tr_b16 a[136:137],v22 offset:1032
v_mfma_f32_16x16x32_bf16 a[84:87],a[172:175],a[244:247],a[84:87]
ds_read_b64_tr_b16 a[138:139],v22 offset:3080
v_mfma_f32_16x16x32_bf16 a[116:119],a[172:175],a[252:255],a[116:119]
ds_read_b64_tr_b16 a[140:141],v22 offset:17416
v_mfma_f32_16x16x32_bf16 a[116:119],a[168:171],a[248:251],a[116:119]
ds_read_b64_tr_b16 a[142:143],v22 offset:19464
v_mfma_f32_16x16x32_bf16 a[24:27],a[176:179],a[224:227],a[24:27]
ds_read_b64_tr_b16 a[144:145],v22 offset:1280
v_mfma_f32_16x16x32_bf16 a[24:27],a[180:183],a[228:231],a[24:27]
ds_read_b64_tr_b16 a[146:147],v22 offset:3328
)A"
R"A(v_mfma_f32_16x16x32_bf16 a[56:59],a[180:183],a[236:239],a[56:59]
ds_read_b64_tr_b16 a[148:149],v22 offset:17664
v_mfma_f32_16x16x32_bf16 a[56:59],a[176:179],a[232:235],a[56:59]
ds_read_b64_tr_b16 a[150:151],v22 offset:19712
v_mfma_f32_16x16x32_bf16 a[88:91],a[176:179],a[240:243],a[88:91]
ds_read_b64_tr_b16 a[152:153],v22 offset:1288
v_mfma_f32_16x16x32_bf16 a[88:91],a[180:183],a[244:247],a[88:91]
ds_read_b64_tr_b16 a[154:155],v22 offset:3336
v_mfma_f32_16x16x32_bf16 a[120:123],a[180:183],a[252:255],a[120:123]
ds_read_b64_tr_b16 a[156:157],v22 offset:17672
v_mfma_f32_16x16x32_bf16 a[120:123],a[176:179],a[248:251],a[120:123]
ds_read_b64_tr_b16 a[158:159],v22 offset:19720
v_mfma_f32_16x16x32_bf16 a[28:31],a[184:187],a[224:227],a[28:31]
v_mfma_f32_16x16x32_bf16 a[28:31],a[188:191],a[228:231],a[28:31]
v_mfma_f32_16x16x32_bf16 a[60:63],a[188:191],a[236:239],a[60:63]
v_mfma_f32_16x16x32_bf16 a[60:63],a[184:187],a[232:235],a[60:63]
v_mfma_f32_16x16x32_bf16 a[92:95],a[184:187],a[240:243],a[92:95]
v_mfma_f32_16x16x32_bf16 a[92:95],a[188:191],a[244:247],a[92:95]
v_mfma_f32_16x16x32_bf16 a[124:127],a[188:191],a[252:255],a[124:127]
v_mfma_f32_16x16x32_bf16 a[124:127],a[184:187],a[248:251],a[124:127]
s_mov_b32 s53,3
.p2align 5
L_qp_%=:
s_waitcnt lgkmcnt(1)
v_mfma_f32_16x16x32_bf16 v[128:131],a[128:131],a[192:195],v[128:131]
s_add_u32 m0,s58,0
s_add_u32 s60,s60,32768
ds_read_b64_tr_b16 a[160:161],v22 offset:1536
v_mfma_f32_16x16x32_bf16 v[128:131],a[132:135],a[196:199],v[128:131]
s_mov_b32 s90,L_att0x_%=-L_pc_%=+128
buffer_load_dwordx4 v56,s[32:35],s56 offen lds
v_mfma_f32_16x16x32_bf16 v[136:139],a[132:135],a[204:207],v[136:139]
s_cmp_eq_u32 s60,131072
s_cselect_b32 s60,32768,s60
ds_read_b64_tr_b16 a[162:163],v22 offset:3584
v_mfma_f32_16x16x32_bf16 v[136:139],a[128:131],a[200:203],v[136:139]
s_add_u32 m0,s58,1024
ds_read_b64_tr_b16 a[164:165],v22 offset:17920
v_xor_b32 v20,131072,v20
v_mfma_f32_16x16x32_bf16 v[144:147],a[128:131],a[208:211],v[144:147]
s_sub_u32 s53,s53,1
ds_read_b64_tr_b16 a[166:167],v22 offset:19968
v_xor_b32 v21,131072,v21
v_mfma_f32_16x16x32_bf16 v[144:147],a[132:135],a[212:215],v[144:147]
ds_read_b64_tr_b16 a[168:169],v22 offset:1544
v_mfma_f32_16x16x32_bf16 v[152:155],a[132:135],a[220:223],v[152:155]
buffer_load_dwordx4 v57,s[32:35],s56 offen lds
v_mfma_f32_16x16x32_bf16 v[152:155],a[128:131],a[216:219],v[152:155]
ds_read_b64_tr_b16 a[170:171],v22 offset:3592
v_mfma_f32_16x16x32_bf16 v[132:135],a[136:139],a[192:195],v[132:135]
ds_read_b64_tr_b16 a[172:173],v22 offset:17928
v_mfma_f32_16x16x32_bf16 v[132:135],a[140:143],a[196:199],v[132:135]
s_add_u32 m0,s58,2048
ds_read_b64_tr_b16 a[174:175],v22 offset:19976
v_mfma_f32_16x16x32_bf16 v[140:143],a[140:143],a[204:207],v[140:143]
ds_read_b64_tr_b16 a[176:177],v22 offset:1792
v_mfma_f32_16x16x32_bf16 v[140:143],a[136:139],a[200:203],v[140:143]
ds_read_b64_tr_b16 a[178:179],v22 offset:3840
v_mfma_f32_16x16x32_bf16 v[148:151],a[136:139],a[208:211],v[148:151]
buffer_load_dwordx4 v58,s[32:35],s56 offen lds
v_mfma_f32_16x16x32_bf16 v[148:151],a[140:143],a[212:215],v[148:151]
ds_read_b64_tr_b16 a[180:181],v22 offset:18176
v_mfma_f32_16x16x32_bf16 v[156:159],a[140:143],a[220:223],v[156:159]
s_add_u32 m0,s58,3072
ds_read_b64_tr_b16 a[182:183],v22 offset:20224
v_mfma_f32_16x16x32_bf16 v[156:159],a[136:139],a[216:219],v[156:159]
ds_read_b64_tr_b16 a[184:185],v22 offset:1800
v_mfma_f32_16x16x32_bf16 v[160:163],a[144:147],a[192:195],v[160:163]
ds_read_b64_tr_b16 a[186:187],v22 offset:3848
v_mfma_f32_16x16x32_bf16 v[160:163],a[148:151],a[196:199],v[160:163]
buffer_load_dwordx4 v59,s[32:35],s56 offen lds
v_mfma_f32_16x16x32_bf16 v[168:171],a[148:151],a[204:207],v[168:171]
ds_read_b64_tr_b16 a[188:189],v22 offset:18184
v_mfma_f32_16x16x32_bf16 v[168:171],a[144:147],a[200:203],v[168:171]
ds_read_b64_tr_b16 a[190:191],v22 offset:20232
v_mfma_f32_16x16x32_bf16 v[176:179],a[144:147],a[208:211],v[176:179]
s_add_u32 m0,s58,4096
ds_read_b64_tr_b16 a[128:129],v22
v_mfma_f32_16x16x32_bf16 v[176:179],a[148:151],a[212:215],v[176:179]
ds_read_b64_tr_b16 a[130:131],v22 offset:2048
v_mfma_f32_16x16x32_bf16 v[184:187],a[148:151],a[220:223],v[184:187]
ds_read_b64_tr_b16 a[132:133],v22 offset:16384
v_mfma_f32_16x16x32_bf16 v[184:187],a[144:147],a[216:219],v[184:187]
buffer_load_dwordx4 v60,s[32:35],s56 offen lds
v_mfma_f32_16x16x32_bf16 v[164:167],a[152:155],a[192:195],v[164:167]
s_waitcnt lgkmcnt(5)
ds_read_b64_tr_b16 a[134:135],v22 offset:18432
v_mfma_f32_16x16x32_bf16 v[164:167],a[156:159],a[196:199],v[164:167]
s_add_u32 m0,s58,5120
ds_read_b64_tr_b16 a[136:137],v22 offset:8
v_mfma_f32_16x16x32_bf16 v[172:175],a[156:159],a[204:207],v[172:175]
ds_read_b64_tr_b16 a[138:139],v22 offset:2056
v_mfma_f32_16x16x32_bf16 v[172:175],a[152:155],a[200:203],v[172:175]
ds_read_b64_tr_b16 a[140:141],v22 offset:16392
v_mfma_f32_16x16x32_bf16 v[180:183],a[152:155],a[208:211],v[180:183]
buffer_load_dwordx4 v61,s[32:35],s56 offen lds
v_mfma_f32_16x16x32_bf16 v[180:183],a[156:159],a[212:215],v[180:183]
ds_read_b64_tr_b16 a[142:143],v22 offset:18440
v_mfma_f32_16x16x32_bf16 v[188:191],a[156:159],a[220:223],v[188:191]
ds_read_b64_tr_b16 a[144:145],v22 offset:256
v_mfma_f32_16x16x32_bf16 v[188:191],a[152:155],a[216:219],v[188:191]
s_add_u32 m0,s58,6144
ds_read_b64_tr_b16 a[146:147],v22 offset:2304
v_mfma_f32_16x16x32_bf16 v[192:195],a[160:163],a[192:195],v[192:195]
ds_read_b64_tr_b16 a[148:149],v22 offset:16640
v_mfma_f32_16x16x32_bf16 v[192:195],a[164:167],a[196:199],v[192:195]
ds_read_b64_tr_b16 a[150:151],v22 offset:18688
v_mfma_f32_16x16x32_bf16 v[200:203],a[164:167],a[204:207],v[200:203]
buffer_load_dwordx4 v62,s[32:35],s56 offen lds
v_mfma_f32_16x16x32_bf16 v[200:203],a[160:163],a[200:203],v[200:203]
ds_read_b64_tr_b16 a[152:153],v22 offset:264
v_mfma_f32_16x16x32_bf16 v[208:211],a[160:163],a[208:211],v[208:211]
s_add_u32 m0,s58,7168
s_add_u32 s58,s58,32768
ds_read_b64_tr_b16 a[154:155],v22 offset:2312
v_mfma_f32_16x16x32_bf16 v[208:211],a[164:167],a[212:215],v[208:211]
s_cmp_ge_u32 s58,131072
s_cselect_b32 s62,98304,0
ds_read_b64_tr_b16 a[156:157],v22 offset:16648
v_mfma_f32_16x16x32_bf16 v[216:219],a[164:167],a[220:223],v[216:219]
s_sub_u32 s58,s58,s62
ds_read_b64_tr_b16 a[158:159],v22 offset:18696
v_mfma_f32_16x16x32_bf16 v[216:219],a[160:163],a[216:219],v[216:219]
buffer_load_dwordx4 v63,s[32:35],s56 offen lds
v_mfma_f32_16x16x32_bf16 v[196:199],a[168:171],a[192:195],v[196:199]
s_add_u32 s56,s56,524288
ds_read_b64_tr_b16 a[160:161],v22 offset:512
v_mfma_f32_16x16x32_bf16 v[196:199],a[172:175],a[196:199],v[196:199]
ds_read_b64_tr_b16 a[162:163],v22 offset:2560
v_mfma_f32_16x16x32_bf16 v[204:207],a[172:175],a[204:207],v[204:207]
s_add_u32 m0,s59,0
ds_read_b64_tr_b16 a[164:165],v22 offset:16896
v_mfma_f32_16x16x32_bf16 v[204:207],a[168:171],a[200:203],v[204:207]
ds_read_b64_tr_b16 a[166:167],v22 offset:18944
v_mfma_f32_16x16x32_bf16 v[212:215],a[168:171],a[208:211],v[212:215]
v_mfma_f32_16x16x32_bf16 v[212:215],a[172:175],a[212:215],v[212:215]
buffer_load_dwordx4 v48,s[28:31],s57 offen lds
v_mfma_f32_16x16x32_bf16 v[220:223],a[172:175],a[220:223],v[220:223]
ds_read_b64_tr_b16 a[172:173],v22 offset:16904
v_mfma_f32_16x16x32_bf16 v[220:223],a[168:171],a[216:219],v[220:223]
ds_read_b64_tr_b16 a[168:169],v22 offset:520
v_mfma_f32_16x16x32_bf16 v[224:227],a[176:179],a[192:195],v[224:227]
ds_read_b64_tr_b16 a[170:171],v22 offset:2568
v_mfma_f32_16x16x32_bf16 v[224:227],a[180:183],a[196:199],v[224:227]
ds_read_b64_tr_b16 a[174:175],v22 offset:18952
v_mfma_f32_16x16x32_bf16 v[232:235],a[180:183],a[204:207],v[232:235]
buffer_load_dwordx4 v49,s[28:31],s57 offen offset:1024 lds
v_mfma_f32_16x16x32_bf16 v[232:235],a[176:179],a[200:203],v[232:235]
v_mfma_f32_16x16x32_bf16 v[240:243],a[176:179],a[208:211],v[240:243]
v_mfma_f32_16x16x32_bf16 v[240:243],a[180:183],a[212:215],v[240:243]
v_mfma_f32_16x16x32_bf16 v[248:251],a[180:183],a[220:223],v[248:251]
ds_read_b64_tr_b16 a[180:181],v22 offset:17152
v_mfma_f32_16x16x32_bf16 v[248:251],a[176:179],a[216:219],v[248:251]
ds_read_b64_tr_b16 a[176:177],v22 offset:768
v_mfma_f32_16x16x32_bf16 v[228:231],a[184:187],a[192:195],v[228:231]
s_waitcnt lgkmcnt(3)
buffer_load_dwordx4 v50,s[28:31],s57 offen offset:2048 lds
v_mfma_f32_16x16x32_bf16 v[228:231],a[188:191],a[196:199],v[228:231]
ds_read_b64_tr_b16 a[178:179],v22 offset:2816
v_mfma_f32_16x16x32_bf16 v[236:239],a[188:191],a[204:207],v[236:239]
ds_read_b64_tr_b16 a[182:183],v22 offset:19200
v_mfma_f32_16x16x32_bf16 v[236:239],a[184:187],a[200:203],v[236:239]
v_mfma_f32_16x16x32_bf16 v[244:247],a[184:187],a[208:211],v[244:247]
v_mfma_f32_16x16x32_bf16 v[244:247],a[188:191],a[212:215],v[244:247]
buffer_load_dwordx4 v51,s[28:31],s57 offen offset:3072 lds
v_mfma_f32_16x16x32_bf16 v[252:255],a[188:191],a[220:223],v[252:255]
ds_read_b64_tr_b16 a[188:189],v22 offset:17160
v_mfma_f32_16x16x32_bf16 v[252:255],a[184:187],a[216:219],v[252:255]
ds_read_b64_tr_b16 a[184:185],v22 offset:776
v_mfma_f32_16x16x32_bf16 a[0:3],a[128:131],a[192:195],a[0:3]
s_add_u32 m0,s59,4096
s_xor_b32 s59,s59,131072
ds_read_b64_tr_b16 a[186:187],v22 offset:2824
v_mfma_f32_16x16x32_bf16 a[0:3],a[132:135],a[196:199],a[0:3]
ds_read_b64_tr_b16 a[190:191],v22 offset:19208
v_add_u32 v22,s60,v23
v_mfma_f32_16x16x32_bf16 a[32:35],a[132:135],a[204:207],a[32:35]
s_add_u32 s60,s60,32768
s_cmp_eq_u32 s60,131072
s_cselect_b32 s60,32768,s60
v_mfma_f32_16x16x32_bf16 a[32:35],a[128:131],a[200:203],a[32:35]
buffer_load_dwordx4 v52,s[28:31],s57 offen lds
v_mfma_f32_16x16x32_bf16 a[64:67],a[128:131],a[208:211],a[64:67]
v_mfma_f32_16x16x32_bf16 a[64:67],a[132:135],a[212:215],a[64:67]
v_mfma_f32_16x16x32_bf16 a[96:99],a[132:135],a[220:223],a[96:99]
v_mfma_f32_16x16x32_bf16 a[96:99],a[128:131],a[216:219],a[96:99]
v_mfma_f32_16x16x32_bf16 a[4:7],a[136:139],a[192:195],a[4:7]
buffer_load_dwordx4 v53,s[28:31],s57 offen offset:1024 lds
v_mfma_f32_16x16x32_bf16 a[4:7],a[140:143],a[196:199],a[4:7]
v_mfma_f32_16x16x32_bf16 a[36:39],a[140:143],a[204:207],a[36:39]
v_mfma_f32_16x16x32_bf16 a[36:39],a[136:139],a[200:203],a[36:39]
v_mfma_f32_16x16x32_bf16 a[68:71],a[136:139],a[208:211],a[68:71]
v_mfma_f32_16x16x32_bf16 a[68:71],a[140:143],a[212:215],a[68:71]
v_mfma_f32_16x16x32_bf16 a[100:103],a[140:143],a[220:223],a[100:103]
buffer_load_dwordx4 v54,s[28:31],s57 offen offset:2048 lds
v_mfma_f32_16x16x32_bf16 a[100:103],a[136:139],a[216:219],a[100:103]
v_mfma_f32_16x16x32_bf16 a[8:11],a[144:147],a[192:195],a[8:11]
v_mfma_f32_16x16x32_bf16 a[8:11],a[148:151],a[196:199],a[8:11]
v_mfma_f32_16x16x32_bf16 a[40:43],a[148:151],a[204:207],a[40:43]
v_mfma_f32_16x16x32_bf16 a[40:43],a[144:147],a[200:203],a[40:43]
buffer_load_dwordx4 v55,s[28:31],s57 offen offset:3072 lds
v_mfma_f32_16x16x32_bf16 a[72:75],a[144:147],a[208:211],a[72:75]
s_add_u32 s57,s57,128
v_mfma_f32_16x16x32_bf16 a[72:75],a[148:151],a[212:215],a[72:75]
v_mfma_f32_16x16x32_bf16 a[104:107],a[148:151],a[220:223],a[104:107]
v_mfma_f32_16x16x32_bf16 a[104:107],a[144:147],a[216:219],a[104:107]
v_mfma_f32_16x16x32_bf16 a[12:15],a[152:155],a[192:195],a[12:15]
v_mfma_f32_16x16x32_bf16 a[12:15],a[156:159],a[196:199],a[12:15]
v_mfma_f32_16x16x32_bf16 a[44:47],a[156:159],a[204:207],a[44:47]
buffer_load_dword v16,v15,s[72:75],s83 offen
v_mfma_f32_16x16x32_bf16 a[44:47],a[152:155],a[200:203],a[44:47]
s_add_u32 s83,s83,896
v_mfma_f32_16x16x32_bf16 a[76:79],a[152:155],a[208:211],a[76:79]
v_mfma_f32_16x16x32_bf16 a[76:79],a[156:159],a[212:215],a[76:79]
v_mfma_f32_16x16x32_bf16 a[108:111],a[156:159],a[220:223],a[108:111]
v_mfma_f32_16x16x32_bf16 a[108:111],a[152:155],a[216:219],a[108:111]
s_waitcnt vmcnt(17) lgkmcnt(0)
v_mfma_f32_16x16x32_bf16 a[16:19],a[160:163],a[192:195],a[16:19]
s_barrier
ds_read_b128 a[224:227],v20
v_mfma_f32_16x16x32_bf16 a[16:19],a[164:167],a[196:199],a[16:19]
ds_read_b64_tr_b16 a[128:129],v22 offset:1024
v_mfma_f32_16x16x32_bf16 a[48:51],a[164:167],a[204:207],a[48:51]
ds_read_b64_tr_b16 a[130:131],v22 offset:3072
v_mfma_f32_16x16x32_bf16 a[48:51],a[160:163],a[200:203],a[48:51]
ds_read_b128 a[228:231],v21
)A"
R"A(v_mfma_f32_16x16x32_bf16 a[80:83],a[160:163],a[208:211],a[80:83]
ds_read_b64_tr_b16 a[132:133],v22 offset:17408
v_mfma_f32_16x16x32_bf16 a[80:83],a[164:167],a[212:215],a[80:83]
ds_read_b64_tr_b16 a[134:135],v22 offset:19456
v_mfma_f32_16x16x32_bf16 a[112:115],a[164:167],a[220:223],a[112:115]
ds_read_b128 a[232:235],v20 offset:2048
v_mfma_f32_16x16x32_bf16 a[112:115],a[160:163],a[216:219],a[112:115]
ds_read_b128 a[236:239],v21 offset:2048
v_mfma_f32_16x16x32_bf16 a[20:23],a[168:171],a[192:195],a[20:23]
ds_read_b128 a[240:243],v20 offset:4096
v_mfma_f32_16x16x32_bf16 a[20:23],a[172:175],a[196:199],a[20:23]
ds_read_b128 a[244:247],v21 offset:4096
v_mfma_f32_16x16x32_bf16 a[52:55],a[172:175],a[204:207],a[52:55]
ds_read_b128 a[248:251],v20 offset:6144
v_xor_b32 v20,131072,v20
v_mfma_f32_16x16x32_bf16 a[52:55],a[168:171],a[200:203],a[52:55]
ds_read_b128 a[252:255],v21 offset:6144
v_xor_b32 v21,131072,v21
v_mfma_f32_16x16x32_bf16 a[84:87],a[168:171],a[208:211],a[84:87]
ds_read_b64_tr_b16 a[136:137],v22 offset:1032
v_mfma_f32_16x16x32_bf16 a[84:87],a[172:175],a[212:215],a[84:87]
ds_read_b64_tr_b16 a[138:139],v22 offset:3080
v_mfma_f32_16x16x32_bf16 a[116:119],a[172:175],a[220:223],a[116:119]
ds_read_b64_tr_b16 a[140:141],v22 offset:17416
v_mfma_f32_16x16x32_bf16 a[116:119],a[168:171],a[216:219],a[116:119]
ds_read_b64_tr_b16 a[142:143],v22 offset:19464
v_mfma_f32_16x16x32_bf16 a[24:27],a[176:179],a[192:195],a[24:27]
ds_read_b64_tr_b16 a[144:145],v22 offset:1280
v_mfma_f32_16x16x32_bf16 a[24:27],a[180:183],a[196:199],a[24:27]
ds_read_b64_tr_b16 a[146:147],v22 offset:3328
v_mfma_f32_16x16x32_bf16 a[56:59],a[180:183],a[204:207],a[56:59]
ds_read_b64_tr_b16 a[148:149],v22 offset:17664
v_mfma_f32_16x16x32_bf16 a[56:59],a[176:179],a[200:203],a[56:59]
ds_read_b64_tr_b16 a[150:151],v22 offset:19712
v_mfma_f32_16x16x32_bf16 a[88:91],a[176:179],a[208:211],a[88:91]
ds_read_b64_tr_b16 a[152:153],v22 offset:1288
v_mfma_f32_16x16x32_bf16 a[88:91],a[180:183],a[212:215],a[88:91]
ds_read_b64_tr_b16 a[154:155],v22 offset:3336
v_mfma_f32_16x16x32_bf16 a[120:123],a[180:183],a[220:223],a[120:123]
ds_read_b64_tr_b16 a[156:157],v22 offset:17672
v_mfma_f32_16x16x32_bf16 a[120:123],a[176:179],a[216:219],a[120:123]
ds_read_b64_tr_b16 a[158:159],v22 offset:19720
v_mfma_f32_16x16x32_bf16 a[28:31],a[184:187],a[192:195],a[28:31]
ds_read_b64_tr_b16 a[160:161],v22 offset:1536
v_mfma_f32_16x16x32_bf16 a[28:31],a[188:191],a[196:199],a[28:31]
ds_read_b64_tr_b16 a[162:163],v22 offset:3584
v_mfma_f32_16x16x32_bf16 a[60:63],a[188:191],a[204:207],a[60:63]
ds_read_b64_tr_b16 a[164:165],v22 offset:17920
v_mfma_f32_16x16x32_bf16 a[60:63],a[184:187],a[200:203],a[60:63]
ds_read_b64_tr_b16 a[166:167],v22 offset:19968
v_mfma_f32_16x16x32_bf16 a[92:95],a[184:187],a[208:211],a[92:95]
ds_read_b64_tr_b16 a[168:169],v22 offset:1544
v_mfma_f32_16x16x32_bf16 a[92:95],a[188:191],a[212:215],a[92:95]
ds_read_b64_tr_b16 a[170:171],v22 offset:3592
v_mfma_f32_16x16x32_bf16 a[124:127],a[188:191],a[220:223],a[124:127]
s_add_u32 m0,s58,0
ds_read_b64_tr_b16 a[172:173],v22 offset:17928
v_mfma_f32_16x16x32_bf16 a[124:127],a[184:187],a[216:219],a[124:127]
s_waitcnt lgkmcnt(7)
ds_read_b64_tr_b16 a[174:175],v22 offset:19976
v_mfma_f32_16x16x32_bf16 v[128:131],a[128:131],a[224:227],v[128:131]
ds_read_b64_tr_b16 a[176:177],v22 offset:1792
v_mfma_f32_16x16x32_bf16 v[128:131],a[132:135],a[228:231],v[128:131]
buffer_load_dwordx4 v56,s[32:35],s56 offen lds
v_mfma_f32_16x16x32_bf16 v[136:139],a[132:135],a[236:239],v[136:139]
ds_read_b64_tr_b16 a[178:179],v22 offset:3840
v_mfma_f32_16x16x32_bf16 v[136:139],a[128:131],a[232:235],v[136:139]
s_add_u32 m0,s58,1024
ds_read_b64_tr_b16 a[180:181],v22 offset:18176
v_mfma_f32_16x16x32_bf16 v[144:147],a[128:131],a[240:243],v[144:147]
ds_read_b64_tr_b16 a[182:183],v22 offset:20224
v_mfma_f32_16x16x32_bf16 v[144:147],a[132:135],a[244:247],v[144:147]
ds_read_b64_tr_b16 a[184:185],v22 offset:1800
v_mfma_f32_16x16x32_bf16 v[152:155],a[132:135],a[252:255],v[152:155]
buffer_load_dwordx4 v57,s[32:35],s56 offen lds
v_mfma_f32_16x16x32_bf16 v[152:155],a[128:131],a[248:251],v[152:155]
ds_read_b64_tr_b16 a[186:187],v22 offset:3848
v_mfma_f32_16x16x32_bf16 v[132:135],a[136:139],a[224:227],v[132:135]
ds_read_b64_tr_b16 a[188:189],v22 offset:18184
v_mfma_f32_16x16x32_bf16 v[132:135],a[140:143],a[228:231],v[132:135]
s_add_u32 m0,s58,2048
ds_read_b64_tr_b16 a[190:191],v22 offset:20232
v_mfma_f32_16x16x32_bf16 v[140:143],a[140:143],a[236:239],v[140:143]
ds_read_b64_tr_b16 a[128:129],v22
v_mfma_f32_16x16x32_bf16 v[140:143],a[136:139],a[232:235],v[140:143]
ds_read_b64_tr_b16 a[130:131],v22 offset:2048
v_mfma_f32_16x16x32_bf16 v[148:151],a[136:139],a[240:243],v[148:151]
buffer_load_dwordx4 v58,s[32:35],s56 offen lds
v_mfma_f32_16x16x32_bf16 v[148:151],a[140:143],a[244:247],v[148:151]
ds_read_b64_tr_b16 a[132:133],v22 offset:16384
v_mfma_f32_16x16x32_bf16 v[156:159],a[140:143],a[252:255],v[156:159]
s_add_u32 m0,s58,3072
ds_read_b64_tr_b16 a[134:135],v22 offset:18432
v_mfma_f32_16x16x32_bf16 v[156:159],a[136:139],a[248:251],v[156:159]
ds_read_b64_tr_b16 a[136:137],v22 offset:8
v_mfma_f32_16x16x32_bf16 v[160:163],a[144:147],a[224:227],v[160:163]
ds_read_b64_tr_b16 a[138:139],v22 offset:2056
v_mfma_f32_16x16x32_bf16 v[160:163],a[148:151],a[228:231],v[160:163]
buffer_load_dwordx4 v59,s[32:35],s56 offen lds
v_mfma_f32_16x16x32_bf16 v[168:171],a[148:151],a[236:239],v[168:171]
ds_read_b64_tr_b16 a[140:141],v22 offset:16392
v_mfma_f32_16x16x32_bf16 v[168:171],a[144:147],a[232:235],v[168:171]
ds_read_b64_tr_b16 a[142:143],v22 offset:18440
v_mfma_f32_16x16x32_bf16 v[176:179],a[144:147],a[240:243],v[176:179]
s_add_u32 m0,s58,4096
v_mfma_f32_16x16x32_bf16 v[176:179],a[148:151],a[244:247],v[176:179]
v_mfma_f32_16x16x32_bf16 v[184:187],a[148:151],a[252:255],v[184:187]
ds_read_b64_tr_b16 a[148:149],v22 offset:16640
v_mfma_f32_16x16x32_bf16 v[184:187],a[144:147],a[248:251],v[184:187]
buffer_load_dwordx4 v60,s[32:35],s56 offen lds
v_mfma_f32_16x16x32_bf16 v[164:167],a[152:155],a[224:227],v[164:167]
ds_read_b64_tr_b16 a[144:145],v22 offset:256
v_mfma_f32_16x16x32_bf16 v[164:167],a[156:159],a[228:231],v[164:167]
s_add_u32 m0,s58,5120
ds_read_b64_tr_b16 a[146:147],v22 offset:2304
v_mfma_f32_16x16x32_bf16 v[172:175],a[156:159],a[236:239],v[172:175]
ds_read_b64_tr_b16 a[150:151],v22 offset:18688
v_mfma_f32_16x16x32_bf16 v[172:175],a[152:155],a[232:235],v[172:175]
v_mfma_f32_16x16x32_bf16 v[180:183],a[152:155],a[240:243],v[180:183]
buffer_load_dwordx4 v61,s[32:35],s56 offen lds
v_mfma_f32_16x16x32_bf16 v[180:183],a[156:159],a[244:247],v[180:183]
v_mfma_f32_16x16x32_bf16 v[188:191],a[156:159],a[252:255],v[188:191]
ds_read_b64_tr_b16 a[156:157],v22 offset:16648
v_mfma_f32_16x16x32_bf16 v[188:191],a[152:155],a[248:251],v[188:191]
s_add_u32 m0,s58,6144
ds_read_b64_tr_b16 a[152:153],v22 offset:264
s_waitcnt lgkmcnt(5)
v_mfma_f32_16x16x32_bf16 v[192:195],a[160:163],a[224:227],v[192:195]
ds_read_b64_tr_b16 a[154:155],v22 offset:2312
v_mfma_f32_16x16x32_bf16 v[192:195],a[164:167],a[228:231],v[192:195]
ds_read_b64_tr_b16 a[158:159],v22 offset:18696
v_mfma_f32_16x16x32_bf16 v[200:203],a[164:167],a[236:239],v[200:203]
buffer_load_dwordx4 v62,s[32:35],s56 offen lds
v_mfma_f32_16x16x32_bf16 v[200:203],a[160:163],a[232:235],v[200:203]
v_mfma_f32_16x16x32_bf16 v[208:211],a[160:163],a[240:243],v[208:211]
s_add_u32 m0,s58,7168
s_add_u32 s58,s58,32768
v_mfma_f32_16x16x32_bf16 v[208:211],a[164:167],a[244:247],v[208:211]
s_cmp_ge_u32 s58,131072
s_cselect_b32 s62,98304,0
s_sub_u32 s58,s58,s62
v_mfma_f32_16x16x32_bf16 v[216:219],a[164:167],a[252:255],v[216:219]
ds_read_b64_tr_b16 a[164:165],v22 offset:16896
v_mfma_f32_16x16x32_bf16 v[216:219],a[160:163],a[248:251],v[216:219]
buffer_load_dwordx4 v63,s[32:35],s56 offen lds
v_mfma_f32_16x16x32_bf16 v[196:199],a[168:171],a[224:227],v[196:199]
s_add_u32 s56,s56,524288
ds_read_b64_tr_b16 a[160:161],v22 offset:512
v_mfma_f32_16x16x32_bf16 v[196:199],a[172:175],a[228:231],v[196:199]
ds_read_b64_tr_b16 a[162:163],v22 offset:2560
v_mfma_f32_16x16x32_bf16 v[204:207],a[172:175],a[236:239],v[204:207]
s_add_u32 m0,s59,0
ds_read_b64_tr_b16 a[166:167],v22 offset:18944
v_mfma_f32_16x16x32_bf16 v[204:207],a[168:171],a[232:235],v[204:207]
v_mfma_f32_16x16x32_bf16 v[212:215],a[168:171],a[240:243],v[212:215]
v_mfma_f32_16x16x32_bf16 v[212:215],a[172:175],a[244:247],v[212:215]
buffer_load_dwordx4 v48,s[28:31],s57 offen lds
v_mfma_f32_16x16x32_bf16 v[220:223],a[172:175],a[252:255],v[220:223]
ds_read_b64_tr_b16 a[172:173],v22 offset:16904
v_mfma_f32_16x16x32_bf16 v[220:223],a[168:171],a[248:251],v[220:223]
ds_read_b64_tr_b16 a[168:169],v22 offset:520
v_mfma_f32_16x16x32_bf16 v[224:227],a[176:179],a[224:227],v[224:227]
ds_read_b64_tr_b16 a[170:171],v22 offset:2568
v_mfma_f32_16x16x32_bf16 v[224:227],a[180:183],a[228:231],v[224:227]
ds_read_b64_tr_b16 a[174:175],v22 offset:18952
v_mfma_f32_16x16x32_bf16 v[232:235],a[180:183],a[236:239],v[232:235]
buffer_load_dwordx4 v49,s[28:31],s57 offen offset:1024 lds
v_mfma_f32_16x16x32_bf16 v[232:235],a[176:179],a[232:235],v[232:235]
v_mfma_f32_16x16x32_bf16 v[240:243],a[176:179],a[240:243],v[240:243]
v_mfma_f32_16x16x32_bf16 v[240:243],a[180:183],a[244:247],v[240:243]
v_mfma_f32_16x16x32_bf16 v[248:251],a[180:183],a[252:255],v[248:251]
ds_read_b64_tr_b16 a[180:181],v22 offset:17152
v_mfma_f32_16x16x32_bf16 v[248:251],a[176:179],a[248:251],v[248:251]
ds_read_b64_tr_b16 a[176:177],v22 offset:768
v_mfma_f32_16x16x32_bf16 v[228:231],a[184:187],a[224:227],v[228:231]
buffer_load_dwordx4 v50,s[28:31],s57 offen offset:2048 lds
v_mfma_f32_16x16x32_bf16 v[228:231],a[188:191],a[228:231],v[228:231]
ds_read_b64_tr_b16 a[178:179],v22 offset:2816
v_mfma_f32_16x16x32_bf16 v[236:239],a[188:191],a[236:239],v[236:239]
ds_read_b64_tr_b16 a[182:183],v22 offset:19200
v_mfma_f32_16x16x32_bf16 v[236:239],a[184:187],a[232:235],v[236:239]
v_mfma_f32_16x16x32_bf16 v[244:247],a[184:187],a[240:243],v[244:247]
v_mfma_f32_16x16x32_bf16 v[244:247],a[188:191],a[244:247],v[244:247]
buffer_load_dwordx4 v51,s[28:31],s57 offen offset:3072 lds
v_mfma_f32_16x16x32_bf16 v[252:255],a[188:191],a[252:255],v[252:255]
ds_read_b64_tr_b16 a[188:189],v22 offset:17160
v_mfma_f32_16x16x32_bf16 v[252:255],a[184:187],a[248:251],v[252:255]
ds_read_b64_tr_b16 a[184:185],v22 offset:776
v_mfma_f32_16x16x32_bf16 a[0:3],a[128:131],a[224:227],a[0:3]
s_add_u32 m0,s59,4096
s_xor_b32 s59,s59,131072
ds_read_b64_tr_b16 a[186:187],v22 offset:2824
v_mfma_f32_16x16x32_bf16 a[0:3],a[132:135],a[228:231],a[0:3]
ds_read_b64_tr_b16 a[190:191],v22 offset:19208
v_add_u32 v22,s60,v23
v_mfma_f32_16x16x32_bf16 a[32:35],a[132:135],a[236:239],a[32:35]
v_mfma_f32_16x16x32_bf16 a[32:35],a[128:131],a[232:235],a[32:35]
buffer_load_dwordx4 v52,s[28:31],s57 offen lds
v_mfma_f32_16x16x32_bf16 a[64:67],a[128:131],a[240:243],a[64:67]
v_mfma_f32_16x16x32_bf16 a[64:67],a[132:135],a[244:247],a[64:67]
v_mfma_f32_16x16x32_bf16 a[96:99],a[132:135],a[252:255],a[96:99]
v_mfma_f32_16x16x32_bf16 a[96:99],a[128:131],a[248:251],a[96:99]
v_mfma_f32_16x16x32_bf16 a[4:7],a[136:139],a[224:227],a[4:7]
buffer_load_dwordx4 v53,s[28:31],s57 offen offset:1024 lds
v_mfma_f32_16x16x32_bf16 a[4:7],a[140:143],a[228:231],a[4:7]
v_mfma_f32_16x16x32_bf16 a[36:39],a[140:143],a[236:239],a[36:39]
v_mfma_f32_16x16x32_bf16 a[36:39],a[136:139],a[232:235],a[36:39]
v_mfma_f32_16x16x32_bf16 a[68:71],a[136:139],a[240:243],a[68:71]
v_mfma_f32_16x16x32_bf16 a[68:71],a[140:143],a[244:247],a[68:71]
v_mfma_f32_16x16x32_bf16 a[100:103],a[140:143],a[252:255],a[100:103]
buffer_load_dwordx4 v54,s[28:31],s57 offen offset:2048 lds
v_mfma_f32_16x16x32_bf16 a[100:103],a[136:139],a[248:251],a[100:103]
s_waitcnt lgkmcnt(0)
v_mfma_f32_16x16x32_bf16 a[8:11],a[144:147],a[224:227],a[8:11]
v_mfma_f32_16x16x32_bf16 a[8:11],a[148:151],a[228:231],a[8:11]
v_mfma_f32_16x16x32_bf16 a[40:43],a[148:151],a[236:239],a[40:43]
v_mfma_f32_16x16x32_bf16 a[40:43],a[144:147],a[232:235],a[40:43]
buffer_load_dwordx4 v55,s[28:31],s57 offen offset:3072 lds
v_mfma_f32_16x16x32_bf16 a[72:75],a[144:147],a[240:243],a[72:75]
)A"
R"A(s_add_u32 s57,s57,128
v_mfma_f32_16x16x32_bf16 a[72:75],a[148:151],a[244:247],a[72:75]
v_mfma_f32_16x16x32_bf16 a[104:107],a[148:151],a[252:255],a[104:107]
v_mfma_f32_16x16x32_bf16 a[104:107],a[144:147],a[248:251],a[104:107]
v_mfma_f32_16x16x32_bf16 a[12:15],a[152:155],a[224:227],a[12:15]
v_mfma_f32_16x16x32_bf16 a[12:15],a[156:159],a[228:231],a[12:15]
v_mfma_f32_16x16x32_bf16 a[44:47],a[156:159],a[236:239],a[44:47]
buffer_load_dword v16,v15,s[72:75],s83 offen
v_mfma_f32_16x16x32_bf16 a[44:47],a[152:155],a[232:235],a[44:47]
s_add_u32 s83,s83,896
v_mfma_f32_16x16x32_bf16 a[76:79],a[152:155],a[240:243],a[76:79]
v_mfma_f32_16x16x32_bf16 a[76:79],a[156:159],a[244:247],a[76:79]
v_mfma_f32_16x16x32_bf16 a[108:111],a[156:159],a[252:255],a[108:111]
v_mfma_f32_16x16x32_bf16 a[108:111],a[152:155],a[248:251],a[108:111]
s_waitcnt vmcnt(17) lgkmcnt(0)
v_mfma_f32_16x16x32_bf16 a[16:19],a[160:163],a[224:227],a[16:19]
s_barrier
ds_read_b128 a[192:195],v20
v_mfma_f32_16x16x32_bf16 a[16:19],a[164:167],a[228:231],a[16:19]
ds_read_b128 a[196:199],v21
v_mfma_f32_16x16x32_bf16 a[48:51],a[164:167],a[236:239],a[48:51]
ds_read_b128 a[200:203],v20 offset:2048
v_mfma_f32_16x16x32_bf16 a[48:51],a[160:163],a[232:235],a[48:51]
ds_read_b128 a[204:207],v21 offset:2048
v_mfma_f32_16x16x32_bf16 a[80:83],a[160:163],a[240:243],a[80:83]
ds_read_b128 a[208:211],v20 offset:4096
v_mfma_f32_16x16x32_bf16 a[80:83],a[164:167],a[244:247],a[80:83]
ds_read_b128 a[212:215],v21 offset:4096
v_mfma_f32_16x16x32_bf16 a[112:115],a[164:167],a[252:255],a[112:115]
buffer_load_dword v26,v24,s[88:91],s93 offen
v_mfma_f32_16x16x32_bf16 a[112:115],a[160:163],a[248:251],a[112:115]
ds_read_b128 a[216:219],v20 offset:6144
v_mfma_f32_16x16x32_bf16 a[20:23],a[168:171],a[224:227],a[20:23]
ds_read_b128 a[220:223],v21 offset:6144
v_mfma_f32_16x16x32_bf16 a[20:23],a[172:175],a[228:231],a[20:23]
ds_read_b64_tr_b16 a[128:129],v22 offset:1024
v_mfma_f32_16x16x32_bf16 a[52:55],a[172:175],a[236:239],a[52:55]
ds_read_b64_tr_b16 a[130:131],v22 offset:3072
v_mfma_f32_16x16x32_bf16 a[52:55],a[168:171],a[232:235],a[52:55]
ds_read_b64_tr_b16 a[132:133],v22 offset:17408
v_mfma_f32_16x16x32_bf16 a[84:87],a[168:171],a[240:243],a[84:87]
ds_read_b64_tr_b16 a[134:135],v22 offset:19456
v_mfma_f32_16x16x32_bf16 a[84:87],a[172:175],a[244:247],a[84:87]
ds_read_b64_tr_b16 a[136:137],v22 offset:1032
v_mfma_f32_16x16x32_bf16 a[116:119],a[172:175],a[252:255],a[116:119]
ds_read_b64_tr_b16 a[138:139],v22 offset:3080
v_mfma_f32_16x16x32_bf16 a[116:119],a[168:171],a[248:251],a[116:119]
ds_read_b64_tr_b16 a[140:141],v22 offset:17416
v_mfma_f32_16x16x32_bf16 a[24:27],a[176:179],a[224:227],a[24:27]
ds_read_b64_tr_b16 a[142:143],v22 offset:19464
v_mfma_f32_16x16x32_bf16 a[24:27],a[180:183],a[228:231],a[24:27]
ds_read_b64_tr_b16 a[144:145],v22 offset:1280
v_mfma_f32_16x16x32_bf16 a[56:59],a[180:183],a[236:239],a[56:59]
ds_read_b64_tr_b16 a[146:147],v22 offset:3328
v_mfma_f32_16x16x32_bf16 a[56:59],a[176:179],a[232:235],a[56:59]
ds_read_b64_tr_b16 a[148:149],v22 offset:17664
v_mfma_f32_16x16x32_bf16 a[88:91],a[176:179],a[240:243],a[88:91]
ds_read_b64_tr_b16 a[150:151],v22 offset:19712
v_mfma_f32_16x16x32_bf16 a[88:91],a[180:183],a[244:247],a[88:91]
ds_read_b64_tr_b16 a[152:153],v22 offset:1288
v_mfma_f32_16x16x32_bf16 a[120:123],a[180:183],a[252:255],a[120:123]
ds_read_b64_tr_b16 a[154:155],v22 offset:3336
v_mfma_f32_16x16x32_bf16 a[120:123],a[176:179],a[248:251],a[120:123]
ds_read_b64_tr_b16 a[156:157],v22 offset:17672
v_mfma_f32_16x16x32_bf16 a[28:31],a[184:187],a[224:227],a[28:31]
ds_read_b64_tr_b16 a[158:159],v22 offset:19720
v_mfma_f32_16x16x32_bf16 a[28:31],a[188:191],a[228:231],a[28:31]
v_mfma_f32_16x16x32_bf16 a[60:63],a[188:191],a[236:239],a[60:63]
v_mfma_f32_16x16x32_bf16 a[60:63],a[184:187],a[232:235],a[60:63]
v_mfma_f32_16x16x32_bf16 a[92:95],a[184:187],a[240:243],a[92:95]
v_mfma_f32_16x16x32_bf16 a[92:95],a[188:191],a[244:247],a[92:95]
v_mfma_f32_16x16x32_bf16 a[124:127],a[188:191],a[252:255],a[124:127]
s_cmp_lg_u32 s53,0
v_mfma_f32_16x16x32_bf16 a[124:127],a[184:187],a[248:251],a[124:127]
s_cbranch_scc1 L_qp_%=
L_qpx_%=:
L_tailseg_%=:
s_waitcnt lgkmcnt(1)
v_mfma_f32_16x16x32_bf16 v[128:131],a[128:131],a[192:195],v[128:131]
s_add_u32 m0,98304,s51
ds_read_b64_tr_b16 a[160:161],v22 offset:1536
v_xor_b32 v20,131072,v20
v_mfma_f32_16x16x32_bf16 v[128:131],a[132:135],a[196:199],v[128:131]
s_add_u32 s54,s54,16384
buffer_load_dwordx4 v4,s[20:23],s47 offen nt lds
v_mfma_f32_16x16x32_bf16 v[136:139],a[132:135],a[204:207],v[136:139]
s_and_b32 s46,s54,0xfffc0000
ds_read_b64_tr_b16 a[162:163],v22 offset:3584
v_xor_b32 v21,131072,v21
v_mfma_f32_16x16x32_bf16 v[136:139],a[128:131],a[200:203],v[136:139]
s_add_u32 s54,s54,16384
ds_read_b64_tr_b16 a[164:165],v22 offset:17920
v_and_b32 v18,48,v6
v_mfma_f32_16x16x32_bf16 v[144:147],a[128:131],a[208:211],v[144:147]
s_mov_b32 s48,-98304
ds_read_b64_tr_b16 a[166:167],v22 offset:19968
v_lshrrev_b32 v19,4,v6
v_mfma_f32_16x16x32_bf16 v[144:147],a[132:135],a[212:215],v[144:147]
s_lshr_b32 s79,%1,3
ds_read_b64_tr_b16 a[168:169],v22 offset:1544
v_lshlrev_b32 v18,2,v18
v_mfma_f32_16x16x32_bf16 v[152:155],a[132:135],a[220:223],v[152:155]
s_mul_i32 s79,s79,7
ds_read_b64_tr_b16 a[170:171],v22 offset:3592
v_and_b32 v19,48,v19
v_mfma_f32_16x16x32_bf16 v[152:155],a[128:131],a[216:219],v[152:155]
s_mov_b32 s60,148
ds_read_b64_tr_b16 a[172:173],v22 offset:17928
v_add_u32 v18,v18,v19
v_mfma_f32_16x16x32_bf16 v[132:135],a[136:139],a[192:195],v[132:135]
s_lshr_b32 s79,s79,5
ds_read_b64_tr_b16 a[174:175],v22 offset:19976
v_add_u32 v18,98304,v18
v_mfma_f32_16x16x32_bf16 v[132:135],a[140:143],a[196:199],v[132:135]
s_sub_u32 s60,s60,s79
s_lshl_b32 s94,s60,2
ds_read_b64_tr_b16 a[176:177],v22 offset:1792
v_mfma_f32_16x16x32_bf16 v[140:143],a[140:143],a[204:207],v[140:143]
s_sub_u32 s94,s94,3
s_mul_i32 s83,s41,5
ds_read_b64_tr_b16 a[178:179],v22 offset:3840
v_mfma_f32_16x16x32_bf16 v[140:143],a[136:139],a[200:203],v[140:143]
s_lshl_b32 s44,s40,4
s_add_u32 s83,s83,s42
ds_read_b64_tr_b16 a[180:181],v22 offset:18176
v_mfma_f32_16x16x32_bf16 v[148:151],a[136:139],a[208:211],v[148:151]
s_and_b32 s44,s44,32
ds_read_b64_tr_b16 a[182:183],v22 offset:20224
v_subrev_u32 v25,s51,v4
v_mfma_f32_16x16x32_bf16 v[148:151],a[140:143],a[212:215],v[148:151]
s_add_u32 s83,s83,15
buffer_load_dwordx4 v4,s[20:23],s47 offen offset:1024 nt lds
v_mfma_f32_16x16x32_bf16 v[156:159],a[140:143],a[220:223],v[156:159]
s_lshl_b32 s80,s40,10
ds_read_b64_tr_b16 a[184:185],v22 offset:1800
v_xor_b32 v25,s44,v25
v_mfma_f32_16x16x32_bf16 v[156:159],a[136:139],a[216:219],v[156:159]
s_and_b32 s83,s83,15
ds_read_b64_tr_b16 a[186:187],v22 offset:3848
v_add_u32 v25,s80,v25
v_mfma_f32_16x16x32_bf16 v[160:163],a[144:147],a[192:195],v[160:163]
s_add_u32 s83,s83,16
v_readfirstlane_b32 s68,v13
s_nop 1
s_add_u32 s68,s68,1
v_mfma_f32_16x16x32_bf16 v[160:163],a[148:151],a[196:199],v[160:163]
s_xor_b32 s69,s68,0x9e3779b9
s_add_u32 s93,s92,L_att0x_%=-L_pc_%=
ds_read_b64_tr_b16 a[188:189],v22 offset:18184
v_mfma_f32_16x16x32_bf16 v[168:171],a[148:151],a[204:207],v[168:171]
s_mov_b32 s50,32768
s_mov_b32 s49,98304
ds_read_b64_tr_b16 a[190:191],v22 offset:20232
v_mfma_f32_16x16x32_bf16 v[168:171],a[144:147],a[200:203],v[168:171]
s_mov_b32 s45,0x1249
s_add_u32 s80,s80,131072
ds_read_b64_tr_b16 a[128:129],v22
v_mfma_f32_16x16x32_bf16 v[176:179],a[144:147],a[208:211],v[176:179]
s_lshl_b32 s83,s83,14
ds_read_b64_tr_b16 a[130:131],v22 offset:2048
v_mfma_f32_16x16x32_bf16 v[176:179],a[148:151],a[212:215],v[176:179]
ds_read_b64_tr_b16 a[132:133],v22 offset:16384
v_mfma_f32_16x16x32_bf16 v[184:187],a[148:151],a[220:223],v[184:187]
ds_read_b64_tr_b16 a[134:135],v22 offset:18432
v_mfma_f32_16x16x32_bf16 v[184:187],a[144:147],a[216:219],v[184:187]
ds_read_b64_tr_b16 a[144:145],v22 offset:8
v_mfma_f32_16x16x32_bf16 v[164:167],a[152:155],a[192:195],v[164:167]
s_waitcnt lgkmcnt(7)
ds_read_b64_tr_b16 a[146:147],v22 offset:2056
v_mfma_f32_16x16x32_bf16 v[164:167],a[156:159],a[196:199],v[164:167]
buffer_load_dwordx4 v5,s[20:23],s47 offen offset:2048 nt lds
v_mfma_f32_16x16x32_bf16 v[172:175],a[156:159],a[204:207],v[172:175]
s_waitcnt vmcnt(5)
ds_read_b128 a[224:227],v20
v_mfma_f32_16x16x32_bf16 v[172:175],a[152:155],a[200:203],v[172:175]
ds_read_b64_tr_b16 a[148:149],v22 offset:16392
v_mfma_f32_16x16x32_bf16 v[180:183],a[152:155],a[208:211],v[180:183]
ds_read_b64_tr_b16 a[150:151],v22 offset:18440
v_mfma_f32_16x16x32_bf16 v[180:183],a[156:159],a[212:215],v[180:183]
v_mfma_f32_16x16x32_bf16 v[188:191],a[156:159],a[220:223],v[188:191]
ds_read_b128 a[228:231],v21
v_mfma_f32_16x16x32_bf16 v[188:191],a[152:155],a[216:219],v[188:191]
v_mfma_f32_16x16x32_bf16 v[192:195],a[160:163],a[192:195],v[192:195]
ds_read_b128 a[232:235],v20 offset:2048
v_mfma_f32_16x16x32_bf16 v[192:195],a[164:167],a[196:199],v[192:195]
v_mfma_f32_16x16x32_bf16 v[200:203],a[164:167],a[204:207],v[200:203]
ds_read_b128 a[236:239],v21 offset:2048
v_mfma_f32_16x16x32_bf16 v[200:203],a[160:163],a[200:203],v[200:203]
v_mfma_f32_16x16x32_bf16 v[208:211],a[160:163],a[208:211],v[208:211]
ds_read_b128 a[240:243],v20 offset:4096
v_mfma_f32_16x16x32_bf16 v[208:211],a[164:167],a[212:215],v[208:211]
buffer_load_dwordx4 v5,s[20:23],s47 offen offset:3072 nt lds
v_mfma_f32_16x16x32_bf16 v[216:219],a[164:167],a[220:223],v[216:219]
ds_read_b128 a[244:247],v21 offset:4096
v_mfma_f32_16x16x32_bf16 v[216:219],a[160:163],a[216:219],v[216:219]
ds_read_b64_tr_b16 a[160:161],v22 offset:256
v_mfma_f32_16x16x32_bf16 v[196:199],a[168:171],a[192:195],v[196:199]
ds_read_b128 a[248:251],v20 offset:6144
ds_read_b128 a[252:255],v21 offset:6144
v_mfma_f32_16x16x32_bf16 v[196:199],a[172:175],a[196:199],v[196:199]
v_mfma_f32_16x16x32_bf16 v[204:207],a[172:175],a[204:207],v[204:207]
v_mfma_f32_16x16x32_bf16 v[204:207],a[168:171],a[200:203],v[204:207]
v_mfma_f32_16x16x32_bf16 v[212:215],a[168:171],a[208:211],v[212:215]
ds_read_b64_tr_b16 a[162:163],v22 offset:2304
v_mfma_f32_16x16x32_bf16 v[212:215],a[172:175],a[212:215],v[212:215]
ds_read_b64_tr_b16 a[164:165],v22 offset:16640
v_mfma_f32_16x16x32_bf16 v[220:223],a[172:175],a[220:223],v[220:223]
s_waitcnt vmcnt(5) lgkmcnt(2)
s_add_u32 m0,98304,s52
ds_read_b64_tr_b16 a[166:167],v22 offset:18688
v_mfma_f32_16x16x32_bf16 v[220:223],a[168:171],a[216:219],v[220:223]
s_barrier
ds_read_b64_tr_b16 a[136:137],v22 offset:32768
v_mfma_f32_16x16x32_bf16 v[224:227],a[176:179],a[192:195],v[224:227]
buffer_load_dwordx4 v4,s[24:27],s47 offen nt lds
v_mfma_f32_16x16x32_bf16 v[224:227],a[180:183],a[196:199],v[224:227]
ds_read_b64_tr_b16 a[138:139],v22 offset:34816
v_mfma_f32_16x16x32_bf16 v[232:235],a[180:183],a[204:207],v[232:235]
ds_read_b64_tr_b16 a[140:141],v22 offset:49152
v_mfma_f32_16x16x32_bf16 v[232:235],a[176:179],a[200:203],v[232:235]
ds_read_b64_tr_b16 a[142:143],v22 offset:51200
v_mfma_f32_16x16x32_bf16 v[240:243],a[176:179],a[208:211],v[240:243]
ds_read_b64_tr_b16 a[152:153],v22 offset:32776
v_mfma_f32_16x16x32_bf16 v[240:243],a[180:183],a[212:215],v[240:243]
ds_read_b64_tr_b16 a[154:155],v22 offset:34824
v_mfma_f32_16x16x32_bf16 v[248:251],a[180:183],a[220:223],v[248:251]
ds_read_b64_tr_b16 a[156:157],v22 offset:49160
v_mfma_f32_16x16x32_bf16 v[248:251],a[176:179],a[216:219],v[248:251]
ds_read_b64_tr_b16 a[158:159],v22 offset:51208
v_mfma_f32_16x16x32_bf16 v[228:231],a[184:187],a[192:195],v[228:231]
ds_read_b64_tr_b16 a[168:169],v22 offset:33024
v_mfma_f32_16x16x32_bf16 v[228:231],a[188:191],a[196:199],v[228:231]
ds_read_b64_tr_b16 a[170:171],v22 offset:35072
v_mfma_f32_16x16x32_bf16 v[236:239],a[188:191],a[204:207],v[236:239]
ds_read_b64_tr_b16 a[172:173],v22 offset:49408
v_mfma_f32_16x16x32_bf16 v[236:239],a[184:187],a[200:203],v[236:239]
ds_read_b64_tr_b16 a[174:175],v22 offset:51456
v_mfma_f32_16x16x32_bf16 v[244:247],a[184:187],a[208:211],v[244:247]
buffer_load_dwordx4 v5,s[24:27],s47 offen offset:1024 nt lds
v_mfma_f32_16x16x32_bf16 v[244:247],a[188:191],a[212:215],v[244:247]
ds_read_b64_tr_b16 a[176:177],v22 offset:264
v_mfma_f32_16x16x32_bf16 v[252:255],a[188:191],a[220:223],v[252:255]
)A"
R"A(ds_read_b64_tr_b16 a[178:179],v22 offset:2312
v_mfma_f32_16x16x32_bf16 v[252:255],a[184:187],a[216:219],v[252:255]
ds_read_b64_tr_b16 a[180:181],v22 offset:16648
v_mfma_f32_16x16x32_bf16 a[0:3],a[128:131],a[192:195],a[0:3]
ds_read_b64_tr_b16 a[182:183],v22 offset:18696
v_mfma_f32_16x16x32_bf16 a[0:3],a[132:135],a[196:199],a[0:3]
s_waitcnt lgkmcnt(6)
ds_read_b64_tr_b16 a[184:185],v22 offset:33032
v_mfma_f32_16x16x32_bf16 a[0:3],a[136:139],a[224:227],a[0:3]
ds_read_b64_tr_b16 a[186:187],v22 offset:35080
v_mfma_f32_16x16x32_bf16 a[0:3],a[140:143],a[228:231],a[0:3]
ds_read_b64_tr_b16 a[188:189],v22 offset:49416
v_mfma_f32_16x16x32_bf16 a[32:35],a[140:143],a[236:239],a[32:35]
ds_read_b64_tr_b16 a[190:191],v22 offset:51464
v_mfma_f32_16x16x32_bf16 a[32:35],a[136:139],a[232:235],a[32:35]
v_mfma_f32_16x16x32_bf16 a[32:35],a[132:135],a[204:207],a[32:35]
v_mfma_f32_16x16x32_bf16 a[32:35],a[128:131],a[200:203],a[32:35]
v_mfma_f32_16x16x32_bf16 a[64:67],a[128:131],a[208:211],a[64:67]
buffer_load_dwordx4 v4,s[24:27],s47 offen offset:2048 nt lds
v_mfma_f32_16x16x32_bf16 a[64:67],a[132:135],a[212:215],a[64:67]
v_accvgpr_read_b32 v64,a0
v_accvgpr_read_b32 v65,a1
v_mfma_f32_16x16x32_bf16 a[64:67],a[136:139],a[240:243],a[64:67]
v_mul_f32 v64,s61,v64
v_mul_f32 v65,s61,v65
v_mfma_f32_16x16x32_bf16 a[64:67],a[140:143],a[244:247],a[64:67]
v_accvgpr_read_b32 v66,a2
v_accvgpr_read_b32 v67,a3
v_mfma_f32_16x16x32_bf16 a[96:99],a[140:143],a[252:255],a[96:99]
ds_read_b64_tr_b16 a[140:141],v22 offset:49664
v_cvt_pk_bf16_f32 v64,v64,v65
v_mfma_f32_16x16x32_bf16 a[96:99],a[136:139],a[248:251],a[96:99]
ds_read_b64_tr_b16 a[136:137],v22 offset:33280
v_accvgpr_read_b32 v80,a32
v_mfma_f32_16x16x32_bf16 a[96:99],a[132:135],a[220:223],a[96:99]
ds_read_b64_tr_b16 a[132:133],v22 offset:16896
v_accvgpr_read_b32 v81,a33
v_mfma_f32_16x16x32_bf16 a[96:99],a[128:131],a[216:219],a[96:99]
ds_read_b64_tr_b16 a[128:129],v22 offset:512
v_mul_f32 v80,s61,v80
v_mfma_f32_16x16x32_bf16 a[4:7],a[144:147],a[192:195],a[4:7]
ds_read_b64_tr_b16 a[130:131],v22 offset:2560
v_mul_f32 v81,s61,v81
v_mfma_f32_16x16x32_bf16 a[4:7],a[148:151],a[196:199],a[4:7]
ds_read_b64_tr_b16 a[134:135],v22 offset:18944
v_accvgpr_read_b32 v82,a34
v_mfma_f32_16x16x32_bf16 a[4:7],a[152:155],a[224:227],a[4:7]
ds_read_b64_tr_b16 a[138:139],v22 offset:35328
v_accvgpr_read_b32 v83,a35
v_mfma_f32_16x16x32_bf16 a[4:7],a[156:159],a[228:231],a[4:7]
ds_read_b64_tr_b16 a[142:143],v22 offset:51712
v_mul_f32 v66,s61,v66
v_mfma_f32_16x16x32_bf16 a[36:39],a[156:159],a[236:239],a[36:39]
buffer_load_dwordx4 v5,s[24:27],s47 offen offset:3072 nt lds
v_mfma_f32_16x16x32_bf16 a[36:39],a[152:155],a[232:235],a[36:39]
s_add_u32 s47,s47,16384
s_and_b32 s47,s47,0x3ffff
s_or_b32 s47,s47,s46
v_mfma_f32_16x16x32_bf16 a[36:39],a[148:151],a[204:207],a[36:39]
s_and_b32 s46,s54,0xfffc0000
v_mul_f32 v67,s61,v67
v_cvt_pk_bf16_f32 v80,v80,v81
v_mfma_f32_16x16x32_bf16 a[36:39],a[144:147],a[200:203],a[36:39]
s_add_u32 s54,s54,16384
v_mul_f32 v82,s61,v82
v_mul_f32 v83,s61,v83
v_mfma_f32_16x16x32_bf16 a[68:71],a[144:147],a[208:211],a[68:71]
v_cvt_pk_bf16_f32 v65,v66,v67
v_cvt_pk_bf16_f32 v81,v82,v83
v_mfma_f32_16x16x32_bf16 a[68:71],a[148:151],a[212:215],a[68:71]
v_accvgpr_read_b32 v112,a96
v_accvgpr_read_b32 v113,a97
v_mfma_f32_16x16x32_bf16 a[68:71],a[152:155],a[240:243],a[68:71]
v_accvgpr_read_b32 v66,a4
v_accvgpr_read_b32 v67,a5
v_mfma_f32_16x16x32_bf16 a[68:71],a[156:159],a[244:247],a[68:71]
v_mul_f32 v112,s61,v112
v_mul_f32 v113,s61,v113
v_mfma_f32_16x16x32_bf16 a[100:103],a[156:159],a[252:255],a[100:103]
ds_read_b64_tr_b16 a[156:157],v22 offset:49672
v_accvgpr_read_b32 v114,a98
v_mfma_f32_16x16x32_bf16 a[100:103],a[152:155],a[248:251],a[100:103]
ds_read_b64_tr_b16 a[152:153],v22 offset:33288
v_accvgpr_read_b32 v82,a36
v_mfma_f32_16x16x32_bf16 a[100:103],a[148:151],a[220:223],a[100:103]
ds_read_b64_tr_b16 a[148:149],v22 offset:16904
v_accvgpr_read_b32 v83,a37
v_mfma_f32_16x16x32_bf16 a[100:103],a[144:147],a[216:219],a[100:103]
ds_read_b64_tr_b16 a[144:145],v22 offset:520
v_accvgpr_read_b32 v115,a99
v_mfma_f32_16x16x32_bf16 a[8:11],a[160:163],a[192:195],a[8:11]
s_add_u32 m0,0,s51
ds_read_b64_tr_b16 a[146:147],v22 offset:2568
v_accvgpr_read_b32 v12,a6
v_mfma_f32_16x16x32_bf16 a[8:11],a[164:167],a[196:199],a[8:11]
ds_read_b64_tr_b16 a[150:151],v22 offset:18952
v_accvgpr_read_b32 v13,a7
v_mfma_f32_16x16x32_bf16 a[8:11],a[168:171],a[224:227],a[8:11]
s_waitcnt lgkmcnt(6)
ds_read_b64_tr_b16 a[154:155],v22 offset:35336
v_mul_f32 v66,s61,v66
v_mfma_f32_16x16x32_bf16 a[8:11],a[172:175],a[228:231],a[8:11]
buffer_load_dwordx4 v4,s[20:23],s47 offen nt lds
v_mfma_f32_16x16x32_bf16 a[40:43],a[172:175],a[236:239],a[40:43]
ds_read_b64_tr_b16 a[158:159],v22 offset:51720
v_mul_f32 v67,s61,v67
v_mfma_f32_16x16x32_bf16 a[40:43],a[168:171],a[232:235],a[40:43]
v_accvgpr_read_b32 v14,a38
v_accvgpr_read_b32 v15,a39
v_mfma_f32_16x16x32_bf16 a[40:43],a[164:167],a[204:207],a[40:43]
v_mul_f32 v82,s61,v82
v_mul_f32 v83,s61,v83
v_mfma_f32_16x16x32_bf16 a[40:43],a[160:163],a[200:203],a[40:43]
v_cvt_pk_bf16_f32 v112,v112,v113
v_mul_f32 v114,s61,v114
v_mfma_f32_16x16x32_bf16 a[72:75],a[160:163],a[208:211],a[72:75]
v_mul_f32 v115,s61,v115
v_mul_f32 v12,s61,v12
v_mfma_f32_16x16x32_bf16 a[72:75],a[164:167],a[212:215],a[72:75]
v_accvgpr_read_b32 v68,a8
v_accvgpr_read_b32 v69,a9
v_mfma_f32_16x16x32_bf16 a[72:75],a[168:171],a[240:243],a[72:75]
v_mul_f32 v68,s61,v68
v_mul_f32 v69,s61,v69
v_mfma_f32_16x16x32_bf16 a[72:75],a[172:175],a[244:247],a[72:75]
v_accvgpr_read_b32 v70,a10
v_accvgpr_read_b32 v71,a11
v_mfma_f32_16x16x32_bf16 a[104:107],a[172:175],a[252:255],a[104:107]
ds_read_b64_tr_b16 a[172:173],v22 offset:49920
v_cvt_pk_bf16_f32 v68,v68,v69
v_mfma_f32_16x16x32_bf16 a[104:107],a[168:171],a[248:251],a[104:107]
ds_read_b64_tr_b16 a[168:169],v22 offset:33536
v_mul_f32 v70,s61,v70
v_mfma_f32_16x16x32_bf16 a[104:107],a[164:167],a[220:223],a[104:107]
ds_read_b64_tr_b16 a[164:165],v22 offset:17152
v_mul_f32 v71,s61,v71
v_mfma_f32_16x16x32_bf16 a[104:107],a[160:163],a[216:219],a[104:107]
ds_read_b64_tr_b16 a[160:161],v22 offset:768
v_mul_f32 v13,s61,v13
v_mfma_f32_16x16x32_bf16 a[12:15],a[176:179],a[192:195],a[12:15]
ds_read_b64_tr_b16 a[162:163],v22 offset:2816
v_cvt_pk_bf16_f32 v66,v66,v67
v_mfma_f32_16x16x32_bf16 a[12:15],a[180:183],a[196:199],a[12:15]
buffer_load_dwordx4 v4,s[20:23],s47 offen offset:1024 nt lds
v_mfma_f32_16x16x32_bf16 a[12:15],a[184:187],a[224:227],a[12:15]
ds_read_b64_tr_b16 a[166:167],v22 offset:19200
v_mul_f32 v14,s61,v14
v_mfma_f32_16x16x32_bf16 a[12:15],a[188:191],a[228:231],a[12:15]
ds_read_b64_tr_b16 a[170:171],v22 offset:35584
v_mul_f32 v15,s61,v15
v_mfma_f32_16x16x32_bf16 a[44:47],a[188:191],a[236:239],a[44:47]
ds_read_b64_tr_b16 a[174:175],v22 offset:51968
v_cvt_pk_bf16_f32 v82,v82,v83
v_mfma_f32_16x16x32_bf16 a[44:47],a[184:187],a[232:235],a[44:47]
v_cvt_pk_bf16_f32 v113,v114,v115
v_cvt_pk_bf16_f32 v69,v70,v71
v_mfma_f32_16x16x32_bf16 a[44:47],a[180:183],a[204:207],a[44:47]
v_accvgpr_read_b32 v100,a72
v_accvgpr_read_b32 v101,a73
v_mfma_f32_16x16x32_bf16 a[44:47],a[176:179],a[200:203],a[44:47]
v_accvgpr_read_b32 v116,a104
v_accvgpr_read_b32 v117,a105
v_mfma_f32_16x16x32_bf16 a[76:79],a[176:179],a[208:211],a[76:79]
v_cvt_pk_bf16_f32 v67,v12,v13
v_cvt_pk_bf16_f32 v83,v14,v15
v_mfma_f32_16x16x32_bf16 a[76:79],a[180:183],a[212:215],a[76:79]
v_accvgpr_read_b32 v114,a100
v_accvgpr_read_b32 v115,a101
v_mfma_f32_16x16x32_bf16 a[76:79],a[184:187],a[240:243],a[76:79]
v_accvgpr_read_b32 v70,a12
v_accvgpr_read_b32 v71,a13
v_mfma_f32_16x16x32_bf16 a[76:79],a[188:191],a[244:247],a[76:79]
v_mul_f32 v100,s61,v100
v_mul_f32 v101,s61,v101
v_mfma_f32_16x16x32_bf16 a[108:111],a[188:191],a[252:255],a[108:111]
ds_read_b64_tr_b16 a[188:189],v22 offset:49928
v_accvgpr_read_b32 v102,a74
v_mfma_f32_16x16x32_bf16 a[108:111],a[184:187],a[248:251],a[108:111]
ds_read_b64_tr_b16 a[184:185],v22 offset:33544
v_accvgpr_read_b32 v103,a75
v_mfma_f32_16x16x32_bf16 a[108:111],a[180:183],a[220:223],a[108:111]
ds_read_b64_tr_b16 a[180:181],v22 offset:17160
v_mul_f32 v116,s61,v116
v_mfma_f32_16x16x32_bf16 a[108:111],a[176:179],a[216:219],a[108:111]
buffer_load_dwordx4 v5,s[20:23],s47 offen offset:2048 nt lds
v_mfma_f32_16x16x32_bf16 a[16:19],a[128:131],a[192:195],a[16:19]
ds_read_b64_tr_b16 a[176:177],v22 offset:776
v_mul_f32 v117,s61,v117
v_mfma_f32_16x16x32_bf16 a[16:19],a[132:135],a[196:199],a[16:19]
ds_read_b64_tr_b16 a[178:179],v22 offset:2824
v_accvgpr_read_b32 v118,a106
v_mfma_f32_16x16x32_bf16 a[16:19],a[136:139],a[224:227],a[16:19]
ds_read_b64_tr_b16 a[182:183],v22 offset:19208
v_accvgpr_read_b32 v119,a107
v_mfma_f32_16x16x32_bf16 a[16:19],a[140:143],a[228:231],a[16:19]
ds_read_b64_tr_b16 a[186:187],v22 offset:35592
v_accvgpr_read_b32 v12,a102
v_mfma_f32_16x16x32_bf16 a[48:51],a[140:143],a[236:239],a[48:51]
ds_read_b64_tr_b16 a[190:191],v22 offset:51976
v_accvgpr_read_b32 v13,a103
v_mfma_f32_16x16x32_bf16 a[48:51],a[136:139],a[232:235],a[48:51]
v_mul_f32 v114,s61,v114
v_mul_f32 v115,s61,v115
v_mfma_f32_16x16x32_bf16 a[48:51],a[132:135],a[204:207],a[48:51]
v_accvgpr_read_b32 v14,a14
v_accvgpr_read_b32 v15,a15
v_mfma_f32_16x16x32_bf16 a[48:51],a[128:131],a[200:203],a[48:51]
v_mul_f32 v70,s61,v70
v_mul_f32 v71,s61,v71
v_mfma_f32_16x16x32_bf16 a[80:83],a[128:131],a[208:211],a[80:83]
v_cvt_pk_bf16_f32 v100,v100,v101
v_mul_f32 v102,s61,v102
v_mfma_f32_16x16x32_bf16 a[80:83],a[132:135],a[212:215],a[80:83]
v_mul_f32 v103,s61,v103
v_cvt_pk_bf16_f32 v116,v116,v117
v_mfma_f32_16x16x32_bf16 a[80:83],a[136:139],a[240:243],a[80:83]
v_mul_f32 v118,s61,v118
v_mul_f32 v119,s61,v119
v_mfma_f32_16x16x32_bf16 a[80:83],a[140:143],a[244:247],a[80:83]
v_mul_f32 v12,s61,v12
v_mul_f32 v13,s61,v13
v_mfma_f32_16x16x32_bf16 a[112:115],a[140:143],a[252:255],a[112:115]
ds_read_b64_tr_b16 a[140:141],v22 offset:50184
v_cvt_pk_bf16_f32 v114,v114,v115
v_mfma_f32_16x16x32_bf16 a[112:115],a[136:139],a[248:251],a[112:115]
buffer_load_dwordx4 v5,s[20:23],s47 offen offset:3072 nt lds
v_mfma_f32_16x16x32_bf16 a[112:115],a[132:135],a[220:223],a[112:115]
ds_read_b64_tr_b16 a[132:133],v22 offset:50176
v_mul_f32 v14,s61,v14
v_mfma_f32_16x16x32_bf16 a[112:115],a[128:131],a[216:219],a[112:115]
s_waitcnt lgkmcnt(2)
ds_read_b64_tr_b16 a[128:129],v22 offset:33792
v_mul_f32 v15,s61,v15
v_mfma_f32_16x16x32_bf16 a[20:23],a[144:147],a[192:195],a[20:23]
ds_read_b64_tr_b16 a[130:131],v22 offset:35840
v_cvt_pk_bf16_f32 v70,v70,v71
v_mfma_f32_16x16x32_bf16 a[20:23],a[148:151],a[196:199],a[20:23]
ds_read_b64_tr_b16 a[134:135],v22 offset:52224
v_cvt_pk_bf16_f32 v101,v102,v103
v_mfma_f32_16x16x32_bf16 a[20:23],a[152:155],a[224:227],a[20:23]
ds_read_b64_tr_b16 a[136:137],v22 offset:33800
v_cvt_pk_bf16_f32 v117,v118,v119
v_mfma_f32_16x16x32_bf16 a[20:23],a[156:159],a[228:231],a[20:23]
ds_read_b64_tr_b16 a[138:139],v22 offset:35848
v_cvt_pk_bf16_f32 v115,v12,v13
v_mfma_f32_16x16x32_bf16 a[52:55],a[156:159],a[236:239],a[52:55]
ds_read_b64_tr_b16 a[142:143],v22 offset:52232
v_cvt_pk_bf16_f32 v71,v14,v15
v_mfma_f32_16x16x32_bf16 a[52:55],a[152:155],a[232:235],a[52:55]
v_accvgpr_read_b32 v102,a76
v_accvgpr_read_b32 v103,a77
v_mfma_f32_16x16x32_bf16 a[52:55],a[148:151],a[204:207],a[52:55]
v_accvgpr_read_b32 v118,a108
v_accvgpr_read_b32 v119,a109
v_mfma_f32_16x16x32_bf16 a[52:55],a[144:147],a[200:203],a[52:55]
v_accvgpr_read_b32 v12,a78
v_accvgpr_read_b32 v13,a79
v_mfma_f32_16x16x32_bf16 a[84:87],a[144:147],a[208:211],a[84:87]
s_add_u32 m0,0,s52
v_mul_f32 v102,s61,v102
v_mul_f32 v103,s61,v103
v_mfma_f32_16x16x32_bf16 a[84:87],a[148:151],a[212:215],a[84:87]
v_accvgpr_read_b32 v14,a110
v_accvgpr_read_b32 v15,a111
v_mfma_f32_16x16x32_bf16 a[84:87],a[152:155],a[240:243],a[84:87]
v_mul_f32 v118,s61,v118
v_mul_f32 v119,s61,v119
v_mfma_f32_16x16x32_bf16 a[84:87],a[156:159],a[244:247],a[84:87]
buffer_load_dwordx4 v4,s[24:27],s47 offen nt lds
v_mfma_f32_16x16x32_bf16 a[116:119],a[156:159],a[252:255],a[116:119]
ds_read_b64_tr_b16 a[156:157],v22 offset:50440
v_mul_f32 v12,s61,v12
v_mfma_f32_16x16x32_bf16 a[116:119],a[152:155],a[248:251],a[116:119]
)A"
R"A(ds_read_b64_tr_b16 a[152:153],v22 offset:34056
v_mul_f32 v13,s61,v13
v_mfma_f32_16x16x32_bf16 a[116:119],a[148:151],a[220:223],a[116:119]
ds_read_b64_tr_b16 a[148:149],v22 offset:50432
v_cvt_pk_bf16_f32 v102,v102,v103
v_mfma_f32_16x16x32_bf16 a[116:119],a[144:147],a[216:219],a[116:119]
ds_read_b64_tr_b16 a[144:145],v22 offset:34048
v_mul_f32 v14,s61,v14
v_mfma_f32_16x16x32_bf16 a[24:27],a[160:163],a[192:195],a[24:27]
ds_read_b64_tr_b16 a[146:147],v22 offset:36096
v_mul_f32 v15,s61,v15
v_mfma_f32_16x16x32_bf16 a[24:27],a[164:167],a[196:199],a[24:27]
ds_read_b64_tr_b16 a[150:151],v22 offset:52480
v_cvt_pk_bf16_f32 v118,v118,v119
v_mfma_f32_16x16x32_bf16 a[24:27],a[168:171],a[224:227],a[24:27]
ds_read_b64_tr_b16 a[154:155],v22 offset:36104
v_cvt_pk_bf16_f32 v103,v12,v13
v_mfma_f32_16x16x32_bf16 a[24:27],a[172:175],a[228:231],a[24:27]
ds_read_b64_tr_b16 a[158:159],v22 offset:52488
v_cvt_pk_bf16_f32 v119,v14,v15
v_mfma_f32_16x16x32_bf16 a[56:59],a[172:175],a[236:239],a[56:59]
v_accvgpr_read_b32 v72,a16
v_accvgpr_read_b32 v73,a17
v_accvgpr_read_b32 v74,a18
v_accvgpr_read_b32 v75,a19
v_accvgpr_read_b32 v12,a20
v_accvgpr_read_b32 v13,a21
v_accvgpr_read_b32 v14,a22
v_accvgpr_read_b32 v15,a23
v_pk_mul_f32 v[72:73],s[60:61],v[72:73] op_sel:[1,0] op_sel_hi:[1,1]
v_pk_mul_f32 v[74:75],s[60:61],v[74:75] op_sel:[1,0] op_sel_hi:[1,1]
v_pk_mul_f32 v[12:13],s[60:61],v[12:13] op_sel:[1,0] op_sel_hi:[1,1]
v_pk_mul_f32 v[14:15],s[60:61],v[14:15] op_sel:[1,0] op_sel_hi:[1,1]
v_cvt_pk_bf16_f32 v72,v72,v73
v_cvt_pk_bf16_f32 v73,v74,v75
v_cvt_pk_bf16_f32 v74,v12,v13
v_cvt_pk_bf16_f32 v75,v14,v15
v_mfma_f32_16x16x32_bf16 a[56:59],a[168:171],a[232:235],a[56:59]
v_accvgpr_read_b32 v96,a64
v_accvgpr_read_b32 v97,a65
v_mfma_f32_16x16x32_bf16 a[56:59],a[164:167],a[204:207],a[56:59]
v_accvgpr_read_b32 v88,a48
v_accvgpr_read_b32 v89,a49
v_accvgpr_read_b32 v90,a50
v_accvgpr_read_b32 v91,a51
v_accvgpr_read_b32 v12,a52
v_accvgpr_read_b32 v13,a53
v_accvgpr_read_b32 v14,a54
v_accvgpr_read_b32 v15,a55
v_pk_mul_f32 v[88:89],s[60:61],v[88:89] op_sel:[1,0] op_sel_hi:[1,1]
v_pk_mul_f32 v[90:91],s[60:61],v[90:91] op_sel:[1,0] op_sel_hi:[1,1]
v_pk_mul_f32 v[12:13],s[60:61],v[12:13] op_sel:[1,0] op_sel_hi:[1,1]
v_pk_mul_f32 v[14:15],s[60:61],v[14:15] op_sel:[1,0] op_sel_hi:[1,1]
v_cvt_pk_bf16_f32 v88,v88,v89
v_cvt_pk_bf16_f32 v89,v90,v91
v_cvt_pk_bf16_f32 v90,v12,v13
v_cvt_pk_bf16_f32 v91,v14,v15
v_mfma_f32_16x16x32_bf16 a[56:59],a[160:163],a[200:203],a[56:59]
v_mul_f32 v96,s61,v96
v_mul_f32 v97,s61,v97
v_mfma_f32_16x16x32_bf16 a[88:91],a[160:163],a[208:211],a[88:91]
v_accvgpr_read_b32 v98,a66
v_accvgpr_read_b32 v99,a67
v_mfma_f32_16x16x32_bf16 a[88:91],a[164:167],a[212:215],a[88:91]
buffer_load_dwordx4 v5,s[24:27],s47 offen offset:1024 nt lds
v_mfma_f32_16x16x32_bf16 a[88:91],a[168:171],a[240:243],a[88:91]
v_cvt_pk_bf16_f32 v96,v96,v97
v_mul_f32 v98,s61,v98
v_mfma_f32_16x16x32_bf16 a[88:91],a[172:175],a[244:247],a[88:91]
v_mul_f32 v99,s61,v99
v_cvt_pk_bf16_f32 v97,v98,v99
v_mfma_f32_16x16x32_bf16 a[120:123],a[172:175],a[252:255],a[120:123]
ds_read_b64_tr_b16 a[172:173],v22 offset:50696
v_accvgpr_read_b32 v84,a40
v_mfma_f32_16x16x32_bf16 a[120:123],a[168:171],a[248:251],a[120:123]
ds_read_b64_tr_b16 a[168:169],v22 offset:34312
v_accvgpr_read_b32 v85,a41
v_mfma_f32_16x16x32_bf16 a[120:123],a[164:167],a[220:223],a[120:123]
ds_read_b64_tr_b16 a[164:165],v22 offset:50688
v_accvgpr_read_b32 v98,a68
v_mfma_f32_16x16x32_bf16 a[120:123],a[160:163],a[216:219],a[120:123]
ds_read_b64_tr_b16 a[160:161],v22 offset:34304
v_accvgpr_read_b32 v99,a69
v_mfma_f32_16x16x32_bf16 a[28:31],a[176:179],a[192:195],a[28:31]
ds_read_b64_tr_b16 a[162:163],v22 offset:36352
v_mul_f32 v84,s61,v84
v_mfma_f32_16x16x32_bf16 a[28:31],a[180:183],a[196:199],a[28:31]
ds_read_b64_tr_b16 a[166:167],v22 offset:52736
v_mul_f32 v85,s61,v85
v_mfma_f32_16x16x32_bf16 a[28:31],a[184:187],a[224:227],a[28:31]
ds_read_b64_tr_b16 a[170:171],v22 offset:36360
v_accvgpr_read_b32 v86,a42
v_mfma_f32_16x16x32_bf16 a[28:31],a[188:191],a[228:231],a[28:31]
ds_read_b64_tr_b16 a[174:175],v22 offset:52744
v_accvgpr_read_b32 v87,a43
v_mfma_f32_16x16x32_bf16 a[60:63],a[188:191],a[236:239],a[60:63]
v_accvgpr_read_b32 v104,a80
v_accvgpr_read_b32 v105,a81
v_accvgpr_read_b32 v106,a82
v_accvgpr_read_b32 v107,a83
v_accvgpr_read_b32 v12,a84
v_accvgpr_read_b32 v13,a85
v_accvgpr_read_b32 v14,a86
v_accvgpr_read_b32 v15,a87
v_pk_mul_f32 v[104:105],s[60:61],v[104:105] op_sel:[1,0] op_sel_hi:[1,1]
v_pk_mul_f32 v[106:107],s[60:61],v[106:107] op_sel:[1,0] op_sel_hi:[1,1]
v_pk_mul_f32 v[12:13],s[60:61],v[12:13] op_sel:[1,0] op_sel_hi:[1,1]
v_pk_mul_f32 v[14:15],s[60:61],v[14:15] op_sel:[1,0] op_sel_hi:[1,1]
v_cvt_pk_bf16_f32 v104,v104,v105
v_cvt_pk_bf16_f32 v105,v106,v107
v_cvt_pk_bf16_f32 v106,v12,v13
v_cvt_pk_bf16_f32 v107,v14,v15
v_mfma_f32_16x16x32_bf16 a[60:63],a[184:187],a[232:235],a[60:63]
v_accvgpr_read_b32 v120,a112
v_accvgpr_read_b32 v121,a113
v_accvgpr_read_b32 v122,a114
v_accvgpr_read_b32 v123,a115
v_accvgpr_read_b32 v12,a116
v_accvgpr_read_b32 v13,a117
v_accvgpr_read_b32 v14,a118
v_accvgpr_read_b32 v15,a119
v_pk_mul_f32 v[120:121],s[60:61],v[120:121] op_sel:[1,0] op_sel_hi:[1,1]
v_pk_mul_f32 v[122:123],s[60:61],v[122:123] op_sel:[1,0] op_sel_hi:[1,1]
v_pk_mul_f32 v[12:13],s[60:61],v[12:13] op_sel:[1,0] op_sel_hi:[1,1]
v_pk_mul_f32 v[14:15],s[60:61],v[14:15] op_sel:[1,0] op_sel_hi:[1,1]
v_cvt_pk_bf16_f32 v120,v120,v121
v_cvt_pk_bf16_f32 v121,v122,v123
v_cvt_pk_bf16_f32 v122,v12,v13
v_cvt_pk_bf16_f32 v123,v14,v15
v_mfma_f32_16x16x32_bf16 a[60:63],a[180:183],a[204:207],a[60:63]
v_accvgpr_read_b32 v16,a70
v_accvgpr_read_b32 v17,a71
v_mfma_f32_16x16x32_bf16 a[60:63],a[176:179],a[200:203],a[60:63]
buffer_load_dwordx4 v4,s[24:27],s47 offen offset:2048 nt lds
v_mfma_f32_16x16x32_bf16 a[92:95],a[176:179],a[208:211],a[92:95]
v_mul_f32 v98,s61,v98
v_mul_f32 v99,s61,v99
v_mfma_f32_16x16x32_bf16 a[92:95],a[180:183],a[212:215],a[92:95]
v_cvt_pk_bf16_f32 v84,v84,v85
v_mul_f32 v86,s61,v86
v_mfma_f32_16x16x32_bf16 a[92:95],a[184:187],a[240:243],a[92:95]
v_mul_f32 v87,s61,v87
v_mul_f32 v16,s61,v16
v_mfma_f32_16x16x32_bf16 a[92:95],a[188:191],a[244:247],a[92:95]
v_mul_f32 v17,s61,v17
v_cvt_pk_bf16_f32 v98,v98,v99
v_mfma_f32_16x16x32_bf16 a[124:127],a[188:191],a[252:255],a[124:127]
ds_read_b64_tr_b16 a[188:189],v22 offset:50952
v_cvt_pk_bf16_f32 v85,v86,v87
v_mfma_f32_16x16x32_bf16 a[124:127],a[184:187],a[248:251],a[124:127]
ds_read_b64_tr_b16 a[184:185],v22 offset:34568
v_cvt_pk_bf16_f32 v99,v16,v17
v_mfma_f32_16x16x32_bf16 a[124:127],a[180:183],a[220:223],a[124:127]
ds_read_b64_tr_b16 a[180:181],v22 offset:50944
v_accvgpr_read_b32 v86,a44
v_mfma_f32_16x16x32_bf16 a[124:127],a[176:179],a[216:219],a[124:127]
s_waitcnt lgkmcnt(3)
ds_read_b64_tr_b16 a[176:177],v22 offset:34560
v_accvgpr_read_b32 v87,a45
v_mfma_f32_16x16x32_bf16 v[128:131],a[128:131],a[224:227],v[128:131]
ds_read_b64_tr_b16 a[178:179],v22 offset:36608
v_accvgpr_read_b32 v16,a46
v_mfma_f32_16x16x32_bf16 v[128:131],a[132:135],a[228:231],v[128:131]
ds_read_b64_tr_b16 a[182:183],v22 offset:52992
v_accvgpr_read_b32 v17,a47
v_mfma_f32_16x16x32_bf16 v[136:139],a[132:135],a[236:239],v[136:139]
ds_read_b64_tr_b16 a[186:187],v22 offset:36616
v_mul_f32 v86,s61,v86
v_mfma_f32_16x16x32_bf16 v[136:139],a[128:131],a[232:235],v[136:139]
ds_read_b64_tr_b16 a[190:191],v22 offset:53000
v_mul_f32 v87,s61,v87
v_mfma_f32_16x16x32_bf16 v[144:147],a[128:131],a[240:243],v[144:147]
v_accvgpr_read_b32 v76,a24
v_accvgpr_read_b32 v77,a25
v_accvgpr_read_b32 v78,a26
v_accvgpr_read_b32 v79,a27
v_accvgpr_read_b32 v12,a28
v_accvgpr_read_b32 v13,a29
v_accvgpr_read_b32 v14,a30
v_accvgpr_read_b32 v15,a31
v_pk_mul_f32 v[76:77],s[60:61],v[76:77] op_sel:[1,0] op_sel_hi:[1,1]
v_pk_mul_f32 v[78:79],s[60:61],v[78:79] op_sel:[1,0] op_sel_hi:[1,1]
v_pk_mul_f32 v[12:13],s[60:61],v[12:13] op_sel:[1,0] op_sel_hi:[1,1]
v_pk_mul_f32 v[14:15],s[60:61],v[14:15] op_sel:[1,0] op_sel_hi:[1,1]
v_cvt_pk_bf16_f32 v76,v76,v77
v_cvt_pk_bf16_f32 v77,v78,v79
v_cvt_pk_bf16_f32 v78,v12,v13
v_cvt_pk_bf16_f32 v79,v14,v15
v_mfma_f32_16x16x32_bf16 v[144:147],a[132:135],a[244:247],v[144:147]
buffer_load_dwordx4 v5,s[24:27],s47 offen offset:3072 nt lds
v_mfma_f32_16x16x32_bf16 v[152:155],a[132:135],a[252:255],v[152:155]
s_add_u32 s47,s47,16384
s_and_b32 s47,s47,0x3ffff
s_or_b32 s47,s47,s46
v_mfma_f32_16x16x32_bf16 v[152:155],a[128:131],a[248:251],v[152:155]
s_and_b32 s46,s54,0xfffc0000
v_mul_f32 v16,s61,v16
v_mul_f32 v17,s61,v17
v_mfma_f32_16x16x32_bf16 v[132:135],a[136:139],a[224:227],v[132:135]
s_add_u32 s54,s54,16384
v_cvt_pk_bf16_f32 v86,v86,v87
v_cvt_pk_bf16_f32 v87,v16,v17
v_mfma_f32_16x16x32_bf16 v[132:135],a[140:143],a[228:231],v[132:135]
v_accvgpr_read_b32 v92,a56
v_accvgpr_read_b32 v93,a57
v_accvgpr_read_b32 v94,a58
v_accvgpr_read_b32 v95,a59
v_accvgpr_read_b32 v12,a60
v_accvgpr_read_b32 v13,a61
v_accvgpr_read_b32 v14,a62
v_accvgpr_read_b32 v15,a63
v_pk_mul_f32 v[92:93],s[60:61],v[92:93] op_sel:[1,0] op_sel_hi:[1,1]
v_pk_mul_f32 v[94:95],s[60:61],v[94:95] op_sel:[1,0] op_sel_hi:[1,1]
v_pk_mul_f32 v[12:13],s[60:61],v[12:13] op_sel:[1,0] op_sel_hi:[1,1]
v_pk_mul_f32 v[14:15],s[60:61],v[14:15] op_sel:[1,0] op_sel_hi:[1,1]
v_cvt_pk_bf16_f32 v92,v92,v93
v_cvt_pk_bf16_f32 v93,v94,v95
v_cvt_pk_bf16_f32 v94,v12,v13
v_cvt_pk_bf16_f32 v95,v14,v15
v_mfma_f32_16x16x32_bf16 v[140:143],a[140:143],a[236:239],v[140:143]
v_accvgpr_read_b32 v108,a88
v_accvgpr_read_b32 v109,a89
v_accvgpr_read_b32 v110,a90
v_accvgpr_read_b32 v111,a91
v_accvgpr_read_b32 v12,a92
v_accvgpr_read_b32 v13,a93
v_accvgpr_read_b32 v14,a94
v_accvgpr_read_b32 v15,a95
v_pk_mul_f32 v[108:109],s[60:61],v[108:109] op_sel:[1,0] op_sel_hi:[1,1]
v_pk_mul_f32 v[110:111],s[60:61],v[110:111] op_sel:[1,0] op_sel_hi:[1,1]
v_pk_mul_f32 v[12:13],s[60:61],v[12:13] op_sel:[1,0] op_sel_hi:[1,1]
v_pk_mul_f32 v[14:15],s[60:61],v[14:15] op_sel:[1,0] op_sel_hi:[1,1]
v_cvt_pk_bf16_f32 v108,v108,v109
v_cvt_pk_bf16_f32 v109,v110,v111
v_cvt_pk_bf16_f32 v110,v12,v13
v_cvt_pk_bf16_f32 v111,v14,v15
v_mfma_f32_16x16x32_bf16 v[140:143],a[136:139],a[232:235],v[140:143]
v_accvgpr_read_b32 v124,a120
v_accvgpr_read_b32 v125,a121
v_accvgpr_read_b32 v126,a122
v_accvgpr_read_b32 v127,a123
v_accvgpr_read_b32 v12,a124
v_accvgpr_read_b32 v13,a125
v_accvgpr_read_b32 v14,a126
v_accvgpr_read_b32 v15,a127
v_pk_mul_f32 v[124:125],s[60:61],v[124:125] op_sel:[1,0] op_sel_hi:[1,1]
v_pk_mul_f32 v[126:127],s[60:61],v[126:127] op_sel:[1,0] op_sel_hi:[1,1]
v_pk_mul_f32 v[12:13],s[60:61],v[12:13] op_sel:[1,0] op_sel_hi:[1,1]
v_pk_mul_f32 v[14:15],s[60:61],v[14:15] op_sel:[1,0] op_sel_hi:[1,1]
v_cvt_pk_bf16_f32 v124,v124,v125
v_cvt_pk_bf16_f32 v125,v126,v127
v_cvt_pk_bf16_f32 v126,v12,v13
v_cvt_pk_bf16_f32 v127,v14,v15
v_mfma_f32_16x16x32_bf16 v[148:151],a[136:139],a[240:243],v[148:151]
v_mfma_f32_16x16x32_bf16 v[148:151],a[140:143],a[244:247],v[148:151]
v_mfma_f32_16x16x32_bf16 v[156:159],a[140:143],a[252:255],v[156:159]
s_waitcnt vmcnt(8) lgkmcnt(0)
v_mfma_f32_16x16x32_bf16 v[156:159],a[136:139],a[248:251],v[156:159]
s_barrier
ds_read_b128 v[48:51],v18
v_mfma_f32_16x16x32_bf16 v[160:163],a[144:147],a[224:227],v[160:163]
ds_read_b64_tr_b16 a[192:193],v2
v_mfma_f32_16x16x32_bf16 v[160:163],a[148:151],a[228:231],v[160:163]
ds_read_b64_tr_b16 a[194:195],v2 offset:4096
v_mfma_f32_16x16x32_bf16 v[168:171],a[148:151],a[236:239],v[168:171]
ds_read_b64_tr_b16 a[196:197],v2 offset:8192
v_mfma_f32_16x16x32_bf16 v[168:171],a[144:147],a[232:235],v[168:171]
ds_read_b128 v[52:55],v18 offset:256
v_mfma_f32_16x16x32_bf16 v[176:179],a[144:147],a[240:243],v[176:179]
v_mul_f32 v128,s61,v128
v_mul_f32 v129,s61,v129
v_mul_f32 v130,s61,v130
v_mul_f32 v131,s61,v131
v_cvt_pk_bf16_f32 v128,v128,v129
v_cvt_pk_bf16_f32 v129,v130,v131
v_pk_mul_f32 v[132:133],s[60:61],v[132:133] op_sel:[1,0] op_sel_hi:[1,1]
v_pk_mul_f32 v[134:135],s[60:61],v[134:135] op_sel:[1,0] op_sel_hi:[1,1]
v_pk_mul_f32 v[136:137],s[60:61],v[136:137] op_sel:[1,0] op_sel_hi:[1,1]
v_pk_mul_f32 v[138:139],s[60:61],v[138:139] op_sel:[1,0] op_sel_hi:[1,1]
v_pk_mul_f32 v[140:141],s[60:61],v[140:141] op_sel:[1,0] op_sel_hi:[1,1]
)A"
R"A(v_pk_mul_f32 v[142:143],s[60:61],v[142:143] op_sel:[1,0] op_sel_hi:[1,1]
v_pk_mul_f32 v[144:145],s[60:61],v[144:145] op_sel:[1,0] op_sel_hi:[1,1]
v_pk_mul_f32 v[146:147],s[60:61],v[146:147] op_sel:[1,0] op_sel_hi:[1,1]
v_pk_mul_f32 v[148:149],s[60:61],v[148:149] op_sel:[1,0] op_sel_hi:[1,1]
v_pk_mul_f32 v[150:151],s[60:61],v[150:151] op_sel:[1,0] op_sel_hi:[1,1]
v_pk_mul_f32 v[152:153],s[60:61],v[152:153] op_sel:[1,0] op_sel_hi:[1,1]
v_pk_mul_f32 v[154:155],s[60:61],v[154:155] op_sel:[1,0] op_sel_hi:[1,1]
v_pk_mul_f32 v[156:157],s[60:61],v[156:157] op_sel:[1,0] op_sel_hi:[1,1]
v_pk_mul_f32 v[158:159],s[60:61],v[158:159] op_sel:[1,0] op_sel_hi:[1,1]
v_mfma_f32_16x16x32_bf16 v[176:179],a[148:151],a[244:247],v[176:179]
ds_read_b128 v[56:59],v18 offset:512
v_cvt_pk_bf16_f32 v130,v132,v133
v_mfma_f32_16x16x32_bf16 v[184:187],a[148:151],a[252:255],v[184:187]
v_cvt_pk_bf16_f32 v132,v136,v137
v_cvt_pk_bf16_f32 v133,v138,v139
v_mfma_f32_16x16x32_bf16 v[184:187],a[144:147],a[248:251],v[184:187]
ds_read_b128 v[60:63],v18 offset:768
v_cvt_pk_bf16_f32 v131,v134,v135
v_mfma_f32_16x16x32_bf16 v[164:167],a[152:155],a[224:227],v[164:167]
v_cvt_pk_bf16_f32 v134,v140,v141
v_cvt_pk_bf16_f32 v135,v142,v143
v_mfma_f32_16x16x32_bf16 v[164:167],a[156:159],a[228:231],v[164:167]
ds_read_b128 a[128:131],v1
v_cvt_pk_bf16_f32 v144,v144,v145
v_mfma_f32_16x16x32_bf16 v[172:175],a[156:159],a[236:239],v[172:175]
v_cvt_pk_bf16_f32 v145,v146,v147
v_cvt_pk_bf16_f32 v146,v148,v149
v_mfma_f32_16x16x32_bf16 v[172:175],a[152:155],a[232:235],v[172:175]
ds_read_b128 a[132:135],v1 offset:256
v_cvt_pk_bf16_f32 v148,v152,v153
v_mfma_f32_16x16x32_bf16 v[180:183],a[152:155],a[240:243],v[180:183]
v_cvt_pk_bf16_f32 v149,v154,v155
v_cvt_pk_bf16_f32 v147,v150,v151
v_mfma_f32_16x16x32_bf16 v[180:183],a[156:159],a[244:247],v[180:183]
ds_read_b128 a[136:139],v1 offset:512
v_cvt_pk_bf16_f32 v150,v156,v157
v_mfma_f32_16x16x32_bf16 v[188:191],a[156:159],a[252:255],v[188:191]
ds_read_b64_tr_b16 a[198:199],v2 offset:12288
v_cvt_pk_bf16_f32 v151,v158,v159
v_mfma_f32_16x16x32_bf16 v[188:191],a[152:155],a[248:251],v[188:191]
ds_read_b64_tr_b16 a[204:205],v3 offset:8192
v_mfma_f32_16x16x32_bf16 v[192:195],a[160:163],a[224:227],v[192:195]
ds_read_b64_tr_b16 a[206:207],v3 offset:12288
v_mfma_f32_16x16x32_bf16 v[192:195],a[164:167],a[228:231],v[192:195]
s_add_u32 m0,32768,s51
ds_read_b128 a[140:143],v1 offset:768
v_mfma_f32_16x16x32_bf16 v[200:203],a[164:167],a[236:239],v[200:203]
ds_read_b64_tr_b16 a[200:201],v3
v_mfma_f32_16x16x32_bf16 v[200:203],a[160:163],a[232:235],v[200:203]
ds_read_b64_tr_b16 a[202:203],v3 offset:4096
v_mfma_f32_16x16x32_bf16 v[208:211],a[160:163],a[240:243],v[208:211]
buffer_load_dwordx4 v4,s[20:23],s47 offen nt lds
v_mfma_f32_16x16x32_bf16 v[208:211],a[164:167],a[244:247],v[208:211]
ds_read_b128 a[156:159],v1 offset:4864
v_mfma_f32_16x16x32_bf16 v[216:219],a[164:167],a[252:255],v[216:219]
v_mul_f32 v160,s61,v160
v_mul_f32 v161,s61,v161
v_mul_f32 v162,s61,v162
v_mul_f32 v163,s61,v163
v_cvt_pk_bf16_f32 v160,v160,v161
v_cvt_pk_bf16_f32 v161,v162,v163
v_pk_mul_f32 v[164:165],s[60:61],v[164:165] op_sel:[1,0] op_sel_hi:[1,1]
v_pk_mul_f32 v[166:167],s[60:61],v[166:167] op_sel:[1,0] op_sel_hi:[1,1]
v_pk_mul_f32 v[168:169],s[60:61],v[168:169] op_sel:[1,0] op_sel_hi:[1,1]
v_pk_mul_f32 v[170:171],s[60:61],v[170:171] op_sel:[1,0] op_sel_hi:[1,1]
v_pk_mul_f32 v[172:173],s[60:61],v[172:173] op_sel:[1,0] op_sel_hi:[1,1]
v_pk_mul_f32 v[174:175],s[60:61],v[174:175] op_sel:[1,0] op_sel_hi:[1,1]
v_pk_mul_f32 v[176:177],s[60:61],v[176:177] op_sel:[1,0] op_sel_hi:[1,1]
v_pk_mul_f32 v[178:179],s[60:61],v[178:179] op_sel:[1,0] op_sel_hi:[1,1]
v_pk_mul_f32 v[180:181],s[60:61],v[180:181] op_sel:[1,0] op_sel_hi:[1,1]
v_pk_mul_f32 v[182:183],s[60:61],v[182:183] op_sel:[1,0] op_sel_hi:[1,1]
v_pk_mul_f32 v[184:185],s[60:61],v[184:185] op_sel:[1,0] op_sel_hi:[1,1]
v_pk_mul_f32 v[186:187],s[60:61],v[186:187] op_sel:[1,0] op_sel_hi:[1,1]
v_pk_mul_f32 v[188:189],s[60:61],v[188:189] op_sel:[1,0] op_sel_hi:[1,1]
v_pk_mul_f32 v[190:191],s[60:61],v[190:191] op_sel:[1,0] op_sel_hi:[1,1]
v_mfma_f32_16x16x32_bf16 v[216:219],a[160:163],a[248:251],v[216:219]
ds_read_b128 a[152:155],v1 offset:4608
v_cvt_pk_bf16_f32 v162,v164,v165
v_mfma_f32_16x16x32_bf16 v[196:199],a[168:171],a[224:227],v[196:199]
v_cvt_pk_bf16_f32 v164,v168,v169
v_cvt_pk_bf16_f32 v165,v170,v171
v_mfma_f32_16x16x32_bf16 v[196:199],a[172:175],a[228:231],v[196:199]
ds_read_b128 a[148:151],v1 offset:4352
v_cvt_pk_bf16_f32 v163,v166,v167
v_mfma_f32_16x16x32_bf16 v[204:207],a[172:175],a[236:239],v[204:207]
v_cvt_pk_bf16_f32 v166,v172,v173
v_cvt_pk_bf16_f32 v167,v174,v175
v_mfma_f32_16x16x32_bf16 v[204:207],a[168:171],a[232:235],v[204:207]
ds_read_b128 a[144:147],v1 offset:4096
v_cvt_pk_bf16_f32 v176,v176,v177
v_mfma_f32_16x16x32_bf16 v[212:215],a[168:171],a[240:243],v[212:215]
v_cvt_pk_bf16_f32 v177,v178,v179
v_cvt_pk_bf16_f32 v178,v180,v181
v_mfma_f32_16x16x32_bf16 v[212:215],a[172:175],a[244:247],v[212:215]
ds_read_b128 a[160:163],v1 offset:8192
v_cvt_pk_bf16_f32 v180,v184,v185
v_mfma_f32_16x16x32_bf16 v[220:223],a[172:175],a[252:255],v[220:223]
v_cvt_pk_bf16_f32 v181,v186,v187
v_cvt_pk_bf16_f32 v179,v182,v183
v_mfma_f32_16x16x32_bf16 v[220:223],a[168:171],a[248:251],v[220:223]
ds_read_b128 a[164:167],v1 offset:8448
v_cvt_pk_bf16_f32 v182,v188,v189
v_mfma_f32_16x16x32_bf16 v[224:227],a[176:179],a[224:227],v[224:227]
ds_read_b64_tr_b16 a[208:209],v2 offset:256
v_cvt_pk_bf16_f32 v183,v190,v191
v_mfma_f32_16x16x32_bf16 v[224:227],a[180:183],a[228:231],v[224:227]
buffer_load_dwordx4 v4,s[20:23],s47 offen offset:1024 nt lds
v_mfma_f32_16x16x32_bf16 v[232:235],a[180:183],a[236:239],v[232:235]
ds_read_b128 a[168:171],v1 offset:8704
v_mfma_f32_16x16x32_bf16 v[232:235],a[176:179],a[232:235],v[232:235]
ds_read_b64_tr_b16 a[210:211],v2 offset:4352
v_mfma_f32_16x16x32_bf16 v[240:243],a[176:179],a[240:243],v[240:243]
ds_read_b64_tr_b16 a[212:213],v2 offset:8448
v_mfma_f32_16x16x32_bf16 v[240:243],a[180:183],a[244:247],v[240:243]
v_mul_f32 v192,s61,v192
v_mul_f32 v193,s61,v193
v_mul_f32 v194,s61,v194
v_mul_f32 v195,s61,v195
v_cvt_pk_bf16_f32 v192,v192,v193
v_cvt_pk_bf16_f32 v193,v194,v195
v_pk_mul_f32 v[196:197],s[60:61],v[196:197] op_sel:[1,0] op_sel_hi:[1,1]
v_pk_mul_f32 v[198:199],s[60:61],v[198:199] op_sel:[1,0] op_sel_hi:[1,1]
v_pk_mul_f32 v[200:201],s[60:61],v[200:201] op_sel:[1,0] op_sel_hi:[1,1]
v_pk_mul_f32 v[202:203],s[60:61],v[202:203] op_sel:[1,0] op_sel_hi:[1,1]
v_pk_mul_f32 v[204:205],s[60:61],v[204:205] op_sel:[1,0] op_sel_hi:[1,1]
v_pk_mul_f32 v[206:207],s[60:61],v[206:207] op_sel:[1,0] op_sel_hi:[1,1]
v_pk_mul_f32 v[208:209],s[60:61],v[208:209] op_sel:[1,0] op_sel_hi:[1,1]
v_pk_mul_f32 v[210:211],s[60:61],v[210:211] op_sel:[1,0] op_sel_hi:[1,1]
v_pk_mul_f32 v[212:213],s[60:61],v[212:213] op_sel:[1,0] op_sel_hi:[1,1]
v_pk_mul_f32 v[214:215],s[60:61],v[214:215] op_sel:[1,0] op_sel_hi:[1,1]
v_pk_mul_f32 v[216:217],s[60:61],v[216:217] op_sel:[1,0] op_sel_hi:[1,1]
v_pk_mul_f32 v[218:219],s[60:61],v[218:219] op_sel:[1,0] op_sel_hi:[1,1]
v_pk_mul_f32 v[220:221],s[60:61],v[220:221] op_sel:[1,0] op_sel_hi:[1,1]
v_pk_mul_f32 v[222:223],s[60:61],v[222:223] op_sel:[1,0] op_sel_hi:[1,1]
v_mfma_f32_16x16x32_bf16 v[248:251],a[180:183],a[252:255],v[248:251]
ds_read_b128 a[172:175],v1 offset:8960
v_cvt_pk_bf16_f32 v194,v196,v197
v_mfma_f32_16x16x32_bf16 v[248:251],a[176:179],a[248:251],v[248:251]
v_cvt_pk_bf16_f32 v196,v200,v201
v_cvt_pk_bf16_f32 v197,v202,v203
v_mfma_f32_16x16x32_bf16 v[228:231],a[184:187],a[224:227],v[228:231]
ds_read_b128 a[180:183],v1 offset:12544
v_cvt_pk_bf16_f32 v195,v198,v199
v_mfma_f32_16x16x32_bf16 v[228:231],a[188:191],a[228:231],v[228:231]
v_cvt_pk_bf16_f32 v198,v204,v205
v_cvt_pk_bf16_f32 v199,v206,v207
v_mfma_f32_16x16x32_bf16 v[236:239],a[188:191],a[236:239],v[236:239]
ds_read_b128 a[176:179],v1 offset:12288
v_cvt_pk_bf16_f32 v208,v208,v209
v_mfma_f32_16x16x32_bf16 v[236:239],a[184:187],a[232:235],v[236:239]
v_cvt_pk_bf16_f32 v209,v210,v211
v_cvt_pk_bf16_f32 v210,v212,v213
v_mfma_f32_16x16x32_bf16 v[244:247],a[184:187],a[240:243],v[244:247]
v_cvt_pk_bf16_f32 v212,v216,v217
v_cvt_pk_bf16_f32 v213,v218,v219
v_mfma_f32_16x16x32_bf16 v[244:247],a[188:191],a[244:247],v[244:247]
v_cvt_pk_bf16_f32 v211,v214,v215
v_cvt_pk_bf16_f32 v214,v220,v221
v_mfma_f32_16x16x32_bf16 v[252:255],a[188:191],a[252:255],v[252:255]
buffer_load_dwordx4 v5,s[20:23],s47 offen offset:2048 nt lds
v_mfma_f32_16x16x32_bf16 v[252:255],a[184:187],a[248:251],v[252:255]
s_waitcnt lgkmcnt(2)
ds_read_b128 a[188:191],v1 offset:13056
v_cvt_pk_bf16_f32 v215,v222,v223
v_mfma_f32_16x16x32_bf16 v[32:35],v[48:51],v[64:67],0
ds_read_b64_tr_b16 a[214:215],v2 offset:12544
v_mfma_f32_16x16x32_bf16 v[32:35],v[52:55],v[68:71],v[32:35]
ds_read_b64_tr_b16 a[220:221],v3 offset:8448
v_mfma_f32_16x16x32_bf16 v[32:35],v[56:59],v[72:75],v[32:35]
ds_read_b64_tr_b16 a[222:223],v3 offset:12544
v_mfma_f32_16x16x32_bf16 v[32:35],v[60:63],v[76:79],v[32:35]
ds_read_b128 a[184:187],v1 offset:12800
v_add_u32 v1,s48,v1
v_mfma_f32_16x16x32_bf16 v[36:39],v[48:51],v[80:83],0
ds_read_b64_tr_b16 a[216:217],v3 offset:256
v_mfma_f32_16x16x32_bf16 v[36:39],v[52:55],v[84:87],v[36:39]
ds_read_b64_tr_b16 a[218:219],v3 offset:4352
v_mfma_f32_16x16x32_bf16 v[36:39],v[56:59],v[88:91],v[36:39]
ds_read_b64_tr_b16 a[224:225],v2 offset:512
v_mfma_f32_16x16x32_bf16 v[36:39],v[60:63],v[92:95],v[36:39]
ds_read_b64_tr_b16 a[226:227],v2 offset:4608
ds_read_b64_tr_b16 a[228:229],v2 offset:8704
v_mfma_f32_16x16x32_bf16 v[40:43],v[48:51],v[96:99],0
ds_read_b64_tr_b16 a[230:231],v2 offset:12800
v_mfma_f32_16x16x32_bf16 v[40:43],v[52:55],v[100:103],v[40:43]
v_max3_f32 v32,v32,v33,v34
v_max3_f32 v32,v32,v35,s18
v_mfma_f32_16x16x32_bf16 v[40:43],v[56:59],v[104:107],v[40:43]
v_sub_f32 v35,0,v32
v_sub_f32 v34,0,v32
v_mfma_f32_16x16x32_bf16 v[40:43],v[60:63],v[108:111],v[40:43]
v_sub_f32 v33,0,v32
v_sub_f32 v32,0,v32
v_mfma_f32_16x16x32_bf16 v[44:47],v[48:51],v[112:115],0
buffer_load_dwordx4 v5,s[20:23],s47 offen offset:3072 nt lds
v_mfma_f32_16x16x32_bf16 v[44:47],v[52:55],v[116:119],v[44:47]
v_max3_f32 v36,v36,v37,v38
v_max3_f32 v36,v36,v39,s19
v_mfma_f32_16x16x32_bf16 v[44:47],v[56:59],v[120:123],v[44:47]
v_sub_f32 v39,0,v36
v_sub_f32 v38,0,v36
v_mfma_f32_16x16x32_bf16 v[44:47],v[60:63],v[124:127],v[44:47]
v_sub_f32 v37,0,v36
v_sub_f32 v36,0,v36
v_mfma_f32_16x16x32_bf16 v[136:139],a[128:131],v[64:67],v[32:35]
ds_read_b64_tr_b16 a[236:237],v3 offset:8704
ds_read_b64_tr_b16 a[238:239],v3 offset:12800
v_mfma_f32_16x16x32_bf16 v[136:139],a[132:135],v[68:71],v[136:139]
v_max3_f32 v40,v40,v41,v42
v_max3_f32 v40,v40,v43,s100
v_mfma_f32_16x16x32_bf16 v[136:139],a[136:139],v[72:75],v[136:139]
v_sub_f32 v43,0,v40
v_sub_f32 v42,0,v40
v_mfma_f32_16x16x32_bf16 v[136:139],a[140:143],v[76:79],v[136:139]
v_sub_f32 v41,0,v40
v_sub_f32 v40,0,v40
v_mfma_f32_16x16x32_bf16 v[140:143],a[156:159],v[76:79],v[32:35]
ds_read_b64_tr_b16 a[232:233],v3 offset:512
ds_read_b64_tr_b16 a[234:235],v3 offset:4608
v_mfma_f32_16x16x32_bf16 v[140:143],a[152:155],v[72:75],v[140:143]
s_add_u32 m0,32768,s52
ds_read_b64_tr_b16 a[240:241],v2 offset:768
v_max3_f32 v44,v44,v45,v46
v_mfma_f32_16x16x32_bf16 v[140:143],a[148:151],v[68:71],v[140:143]
ds_read_b64_tr_b16 a[242:243],v2 offset:4864
v_max3_f32 v44,v44,v47,s101
v_mfma_f32_16x16x32_bf16 v[140:143],a[144:147],v[64:67],v[140:143]
ds_read_b64_tr_b16 a[244:245],v2 offset:8960
v_mov_b32 v48,0
v_mfma_f32_16x16x32_bf16 v[152:155],a[160:163],v[64:67],v[32:35]
buffer_load_dwordx4 v4,s[24:27],s47 offen nt lds
v_mfma_f32_16x16x32_bf16 v[152:155],a[164:167],v[68:71],v[152:155]
v_exp_f32 v136,v136
v_mfma_f32_16x16x32_bf16 v[152:155],a[168:171],v[72:75],v[152:155]
v_exp_f32 v137,v137
v_mfma_f32_16x16x32_bf16 v[152:155],a[172:175],v[76:79],v[152:155]
s_waitcnt lgkmcnt(5)
v_exp_f32 v138,v138
v_mfma_f32_16x16x32_bf16 v[156:159],a[188:191],v[76:79],v[32:35]
v_exp_f32 v139,v139
v_mfma_f32_16x16x32_bf16 v[156:159],a[184:187],v[72:75],v[156:159]
v_cvt_pk_bf16_f32 v136,v136,v137
)A"
R"A(v_cvt_pk_bf16_f32 v137,v138,v139
v_mfma_f32_16x16x32_bf16 v[156:159],a[180:183],v[68:71],v[156:159]
v_exp_f32 v140,v140
v_mfma_f32_16x16x32_bf16 v[156:159],a[176:179],v[64:67],v[156:159]
v_exp_f32 v141,v141
v_mfma_f32_16x16x32_bf16 v[168:171],a[128:131],v[80:83],v[36:39]
v_exp_f32 v142,v142
v_mfma_f32_16x16x32_bf16 v[168:171],a[132:135],v[84:87],v[168:171]
v_exp_f32 v143,v143
v_mfma_f32_16x16x32_bf16 v[168:171],a[136:139],v[88:91],v[168:171]
v_cvt_pk_bf16_f32 v138,v140,v141
v_cvt_pk_bf16_f32 v139,v142,v143
v_mfma_f32_16x16x32_bf16 v[168:171],a[140:143],v[92:95],v[168:171]
v_exp_f32 v152,v152
v_mfma_f32_16x16x32_bf16 v[172:175],a[156:159],v[92:95],v[36:39]
v_exp_f32 v153,v153
v_mfma_f32_16x16x32_bf16 v[172:175],a[152:155],v[88:91],v[172:175]
buffer_load_dwordx4 v5,s[24:27],s47 offen offset:1024 nt lds
v_mfma_f32_16x16x32_bf16 v[172:175],a[148:151],v[84:87],v[172:175]
v_exp_f32 v154,v154
v_mfma_f32_16x16x32_bf16 v[172:175],a[144:147],v[80:83],v[172:175]
v_exp_f32 v155,v155
v_mfma_f32_16x16x32_bf16 v[184:187],a[160:163],v[80:83],v[36:39]
v_exp_f32 v156,v156
v_mfma_f32_16x16x32_bf16 v[184:187],a[164:167],v[84:87],v[184:187]
v_exp_f32 v157,v157
v_mfma_f32_16x16x32_bf16 v[184:187],a[168:171],v[88:91],v[184:187]
v_exp_f32 v158,v158
v_mfma_f32_16x16x32_bf16 v[184:187],a[172:175],v[92:95],v[184:187]
v_exp_f32 v159,v159
v_mfma_f32_16x16x32_bf16 v[188:191],a[188:191],v[92:95],v[36:39]
v_cvt_pk_bf16_f32 v140,v152,v153
v_cvt_pk_bf16_f32 v141,v154,v155
v_mfma_f32_16x16x32_bf16 v[188:191],a[184:187],v[88:91],v[188:191]
v_cvt_pk_bf16_f32 v142,v156,v157
v_cvt_pk_bf16_f32 v143,v158,v159
v_mfma_f32_16x16x32_bf16 v[188:191],a[180:183],v[84:87],v[188:191]
ds_read_b64_tr_b16 a[246:247],v2 offset:13056
ds_read_b64_tr_b16 a[252:253],v3 offset:8960
v_mfma_f32_16x16x32_bf16 v[188:191],a[176:179],v[80:83],v[188:191]
ds_read_b64_tr_b16 a[254:255],v3 offset:13056
v_mov_b32 v49,0
v_mfma_f32_16x16x32_bf16 v[200:203],a[128:131],v[96:99],v[40:43]
ds_read_b64_tr_b16 a[248:249],v3 offset:768
v_mov_b32 v50,0
v_mfma_f32_16x16x32_bf16 v[200:203],a[132:135],v[100:103],v[200:203]
ds_read_b64_tr_b16 a[250:251],v3 offset:4864
v_mov_b32 v51,0
v_mfma_f32_16x16x32_bf16 v[200:203],a[136:139],v[104:107],v[200:203]
buffer_load_dwordx4 v4,s[24:27],s47 offen offset:2048 nt lds
v_mfma_f32_16x16x32_bf16 v[200:203],a[140:143],v[108:111],v[200:203]
v_sub_f32 v47,0,v44
v_sub_f32 v46,0,v44
v_mfma_f32_16x16x32_bf16 v[204:207],a[156:159],v[108:111],v[40:43]
v_sub_f32 v45,0,v44
v_sub_f32 v44,0,v44
v_mfma_f32_16x16x32_bf16 v[204:207],a[152:155],v[104:107],v[204:207]
v_mul_f32 v224,s61,v224
v_mul_f32 v225,s61,v225
v_mul_f32 v226,s61,v226
v_mul_f32 v227,s61,v227
v_cvt_pk_bf16_f32 v224,v224,v225
v_cvt_pk_bf16_f32 v225,v226,v227
v_pk_mul_f32 v[228:229],s[60:61],v[228:229] op_sel:[1,0] op_sel_hi:[1,1]
v_pk_mul_f32 v[230:231],s[60:61],v[230:231] op_sel:[1,0] op_sel_hi:[1,1]
v_pk_mul_f32 v[232:233],s[60:61],v[232:233] op_sel:[1,0] op_sel_hi:[1,1]
v_pk_mul_f32 v[234:235],s[60:61],v[234:235] op_sel:[1,0] op_sel_hi:[1,1]
v_pk_mul_f32 v[236:237],s[60:61],v[236:237] op_sel:[1,0] op_sel_hi:[1,1]
v_pk_mul_f32 v[238:239],s[60:61],v[238:239] op_sel:[1,0] op_sel_hi:[1,1]
v_pk_mul_f32 v[240:241],s[60:61],v[240:241] op_sel:[1,0] op_sel_hi:[1,1]
v_pk_mul_f32 v[242:243],s[60:61],v[242:243] op_sel:[1,0] op_sel_hi:[1,1]
v_pk_mul_f32 v[244:245],s[60:61],v[244:245] op_sel:[1,0] op_sel_hi:[1,1]
v_pk_mul_f32 v[246:247],s[60:61],v[246:247] op_sel:[1,0] op_sel_hi:[1,1]
v_pk_mul_f32 v[248:249],s[60:61],v[248:249] op_sel:[1,0] op_sel_hi:[1,1]
v_pk_mul_f32 v[250:251],s[60:61],v[250:251] op_sel:[1,0] op_sel_hi:[1,1]
v_pk_mul_f32 v[252:253],s[60:61],v[252:253] op_sel:[1,0] op_sel_hi:[1,1]
v_pk_mul_f32 v[254:255],s[60:61],v[254:255] op_sel:[1,0] op_sel_hi:[1,1]
v_mfma_f32_16x16x32_bf16 v[204:207],a[148:151],v[100:103],v[204:207]
v_cvt_pk_bf16_f32 v226,v228,v229
v_cvt_pk_bf16_f32 v228,v232,v233
v_mfma_f32_16x16x32_bf16 v[204:207],a[144:147],v[96:99],v[204:207]
v_cvt_pk_bf16_f32 v229,v234,v235
v_cvt_pk_bf16_f32 v227,v230,v231
v_mfma_f32_16x16x32_bf16 v[216:219],a[160:163],v[96:99],v[40:43]
v_cvt_pk_bf16_f32 v230,v236,v237
v_cvt_pk_bf16_f32 v231,v238,v239
v_mfma_f32_16x16x32_bf16 v[216:219],a[164:167],v[100:103],v[216:219]
v_cvt_pk_bf16_f32 v240,v240,v241
v_cvt_pk_bf16_f32 v241,v242,v243
v_mfma_f32_16x16x32_bf16 v[216:219],a[168:171],v[104:107],v[216:219]
v_cvt_pk_bf16_f32 v242,v244,v245
v_cvt_pk_bf16_f32 v244,v248,v249
v_mfma_f32_16x16x32_bf16 v[216:219],a[172:175],v[108:111],v[216:219]
v_cvt_pk_bf16_f32 v245,v250,v251
v_cvt_pk_bf16_f32 v243,v246,v247
v_mfma_f32_16x16x32_bf16 v[220:223],a[188:191],v[108:111],v[40:43]
v_cvt_pk_bf16_f32 v246,v252,v253
v_cvt_pk_bf16_f32 v247,v254,v255
v_mfma_f32_16x16x32_bf16 v[220:223],a[184:187],v[104:107],v[220:223]
v_exp_f32 v168,v168
v_mfma_f32_16x16x32_bf16 v[220:223],a[180:183],v[100:103],v[220:223]
v_exp_f32 v169,v169
v_mfma_f32_16x16x32_bf16 v[220:223],a[176:179],v[96:99],v[220:223]
buffer_load_dwordx4 v5,s[24:27],s47 offen offset:3072 nt lds
v_mfma_f32_16x16x32_bf16 a[0:3],a[192:195],v[136:139],0
s_add_u32 s47,s47,16384
s_and_b32 s47,s47,0x3ffff
s_or_b32 s47,s47,s46
v_mfma_f32_16x16x32_bf16 a[0:3],a[196:199],v[140:143],a[0:3]
s_and_b32 s46,s54,0xfffc0000
v_exp_f32 v170,v170
v_mfma_f32_16x16x32_bf16 a[4:7],a[204:207],v[140:143],0
v_exp_f32 v171,v171
v_mfma_f32_16x16x32_bf16 a[4:7],a[200:203],v[136:139],a[4:7]
s_memrealtime s[76:77]
v_cvt_pk_bf16_f32 v168,v168,v169
v_cvt_pk_bf16_f32 v169,v170,v171
v_mfma_f32_16x16x32_bf16 a[8:11],a[208:211],v[136:139],0
v_exp_f32 v172,v172
v_mfma_f32_16x16x32_bf16 a[8:11],a[212:215],v[140:143],a[8:11]
v_exp_f32 v173,v173
v_mfma_f32_16x16x32_bf16 a[12:15],a[220:223],v[140:143],0
v_exp_f32 v174,v174
v_mfma_f32_16x16x32_bf16 a[12:15],a[216:219],v[136:139],a[12:15]
v_exp_f32 v175,v175
v_mfma_f32_16x16x32_bf16 a[16:19],a[224:227],v[136:139],0
v_cvt_pk_bf16_f32 v170,v172,v173
v_cvt_pk_bf16_f32 v171,v174,v175
v_mfma_f32_16x16x32_bf16 a[16:19],a[228:231],v[140:143],a[16:19]
v_exp_f32 v184,v184
v_mfma_f32_16x16x32_bf16 a[20:23],a[236:239],v[140:143],0
s_waitcnt lgkmcnt(0)
v_exp_f32 v185,v185
v_mfma_f32_16x16x32_bf16 a[20:23],a[232:235],v[136:139],a[20:23]
s_add_u32 s78,s76,s60
v_exp_f32 v186,v186
v_mfma_f32_16x16x32_bf16 a[24:27],a[240:243],v[136:139],0
s_lshr_b32 s60,s94,2
v_exp_f32 v187,v187
v_mfma_f32_16x16x32_bf16 a[24:27],a[244:247],v[140:143],a[24:27]
s_max_i32 s60,s60,128
v_exp_f32 v188,v188
v_mfma_f32_16x16x32_bf16 a[28:31],a[252:255],v[140:143],0
v_exp_f32 v189,v189
v_mfma_f32_16x16x32_bf16 a[28:31],a[248:251],v[136:139],a[28:31]
v_exp_f32 v190,v190
v_smfmac_f32_16x16x64_bf16 v[48:51],v[28:31],v[136:143],v27
v_cvt_pk_bf16_f32 v172,v184,v185
v_mfma_f32_16x16x32_bf16 v[232:235],a[128:131],v[112:115],v[44:47]
s_waitcnt vmcnt(8)
v_exp_f32 v191,v191
v_mfma_f32_16x16x32_bf16 v[232:235],a[132:135],v[116:119],v[232:235]
s_barrier
s_add_u32 m0,65536,s51
v_cvt_pk_bf16_f32 v173,v186,v187
v_mfma_f32_16x16x32_bf16 v[232:235],a[136:139],v[120:123],v[232:235]
v_cvt_pk_bf16_f32 v174,v188,v189
v_cvt_pk_bf16_f32 v175,v190,v191
v_mfma_f32_16x16x32_bf16 v[232:235],a[140:143],v[124:127],v[232:235]
ds_read_b128 a[128:131],v1
v_mov_b32 v52,0
v_mfma_f32_16x16x32_bf16 v[236:239],a[156:159],v[124:127],v[44:47]
buffer_load_dwordx4 v4,s[20:23],s47 offen nt lds
v_mfma_f32_16x16x32_bf16 v[236:239],a[152:155],v[120:123],v[236:239]
ds_read_b128 a[132:135],v1 offset:256
v_mov_b32 v53,0
v_mfma_f32_16x16x32_bf16 v[236:239],a[148:151],v[116:119],v[236:239]
v_mov_b32 v54,0
v_mov_b32 v55,0
v_mfma_f32_16x16x32_bf16 v[236:239],a[144:147],v[112:115],v[236:239]
ds_read_b128 a[136:139],v1 offset:512
v_mov_b32 v56,0
v_mfma_f32_16x16x32_bf16 v[248:251],a[160:163],v[112:115],v[44:47]
v_exp_f32 v200,v200
v_mfma_f32_16x16x32_bf16 v[248:251],a[164:167],v[116:119],v[248:251]
ds_read_b128 a[140:143],v1 offset:768
v_mov_b32 v57,0
v_mfma_f32_16x16x32_bf16 v[248:251],a[168:171],v[120:123],v[248:251]
v_exp_f32 v201,v201
v_mfma_f32_16x16x32_bf16 v[248:251],a[172:175],v[124:127],v[248:251]
ds_read_b128 a[156:159],v1 offset:4864
v_cvt_pk_bf16_f32 v200,v200,v201
v_mfma_f32_16x16x32_bf16 v[252:255],a[188:191],v[124:127],v[44:47]
v_exp_f32 v202,v202
v_mfma_f32_16x16x32_bf16 v[252:255],a[184:187],v[120:123],v[252:255]
ds_read_b128 a[152:155],v1 offset:4608
v_mov_b32 v58,0
v_mfma_f32_16x16x32_bf16 v[252:255],a[180:183],v[116:119],v[252:255]
v_exp_f32 v203,v203
v_mfma_f32_16x16x32_bf16 v[252:255],a[176:179],v[112:115],v[252:255]
ds_read_b128 a[148:151],v1 offset:4352
v_cvt_pk_bf16_f32 v201,v202,v203
v_mfma_f32_16x16x32_bf16 a[32:35],a[192:195],v[168:171],0
v_exp_f32 v204,v204
v_mfma_f32_16x16x32_bf16 a[32:35],a[196:199],v[172:175],a[32:35]
ds_read_b128 a[144:147],v1 offset:4096
v_mov_b32 v59,0
v_mfma_f32_16x16x32_bf16 a[36:39],a[204:207],v[172:175],0
buffer_load_dwordx4 v4,s[20:23],s47 offen offset:1024 nt lds
v_mfma_f32_16x16x32_bf16 a[36:39],a[200:203],v[168:171],a[36:39]
ds_read_b128 a[160:163],v1 offset:8192
v_mov_b32 v60,0
v_mfma_f32_16x16x32_bf16 a[40:43],a[208:211],v[168:171],0
v_exp_f32 v205,v205
v_mfma_f32_16x16x32_bf16 a[40:43],a[212:215],v[172:175],a[40:43]
ds_read_b128 a[164:167],v1 offset:8448
v_cvt_pk_bf16_f32 v202,v204,v205
v_mfma_f32_16x16x32_bf16 a[44:47],a[220:223],v[172:175],0
v_exp_f32 v206,v206
v_mfma_f32_16x16x32_bf16 a[44:47],a[216:219],v[168:171],a[44:47]
ds_read_b128 a[168:171],v1 offset:8704
v_mov_b32 v61,0
v_mfma_f32_16x16x32_bf16 a[48:51],a[224:227],v[168:171],0
v_exp_f32 v207,v207
v_mfma_f32_16x16x32_bf16 a[48:51],a[228:231],v[172:175],a[48:51]
ds_read_b128 a[172:175],v1 offset:8960
v_cvt_pk_bf16_f32 v203,v206,v207
v_mfma_f32_16x16x32_bf16 a[52:55],a[236:239],v[172:175],0
v_exp_f32 v216,v216
v_mfma_f32_16x16x32_bf16 a[52:55],a[232:235],v[168:171],a[52:55]
ds_read_b128 a[188:191],v1 offset:13056
v_mov_b32 v62,0
v_mfma_f32_16x16x32_bf16 a[56:59],a[240:243],v[168:171],0
v_exp_f32 v217,v217
v_mfma_f32_16x16x32_bf16 a[56:59],a[244:247],v[172:175],a[56:59]
ds_read_b128 a[184:187],v1 offset:12800
v_cvt_pk_bf16_f32 v204,v216,v217
v_mfma_f32_16x16x32_bf16 a[60:63],a[252:255],v[172:175],0
v_exp_f32 v218,v218
v_mfma_f32_16x16x32_bf16 a[60:63],a[248:251],v[168:171],a[60:63]
ds_read_b128 a[180:183],v1 offset:12544
v_mov_b32 v63,0
v_smfmac_f32_16x16x64_bf16 v[52:55],v[28:31],v[168:175],v27
s_waitcnt lgkmcnt(4)
v_add_u32 v2,s48,v2
v_mfma_f32_16x16x32_bf16 v[136:139],a[128:131],v[64:67],v[32:35]
buffer_load_dwordx4 v5,s[20:23],s47 offen offset:2048 nt lds
v_mfma_f32_16x16x32_bf16 v[136:139],a[132:135],v[68:71],v[136:139]
ds_read_b128 a[176:179],v1 offset:12288
v_add_u32 v3,s48,v3
v_mfma_f32_16x16x32_bf16 v[136:139],a[136:139],v[72:75],v[136:139]
s_mov_b32 s48,32768
v_exp_f32 v219,v219
v_mfma_f32_16x16x32_bf16 v[136:139],a[140:143],v[76:79],v[136:139]
v_exp_f32 v220,v220
v_mfma_f32_16x16x32_bf16 v[140:143],a[156:159],v[76:79],v[32:35]
v_exp_f32 v221,v221
v_mfma_f32_16x16x32_bf16 v[140:143],a[152:155],v[72:75],v[140:143]
v_exp_f32 v222,v222
v_mfma_f32_16x16x32_bf16 v[140:143],a[148:151],v[68:71],v[140:143]
v_exp_f32 v223,v223
v_mfma_f32_16x16x32_bf16 v[140:143],a[144:147],v[64:67],v[140:143]
v_cvt_pk_bf16_f32 v205,v218,v219
v_cvt_pk_bf16_f32 v206,v220,v221
v_mfma_f32_16x16x32_bf16 v[152:155],a[160:163],v[64:67],v[32:35]
v_cvt_pk_bf16_f32 v207,v222,v223
v_add_u32 v1,s48,v1
v_mfma_f32_16x16x32_bf16 v[152:155],a[164:167],v[68:71],v[152:155]
v_exp_f32 v232,v232
v_mfma_f32_16x16x32_bf16 v[152:155],a[168:171],v[72:75],v[152:155]
s_waitcnt lgkmcnt(0)
v_exp_f32 v233,v233
v_mfma_f32_16x16x32_bf16 v[152:155],a[172:175],v[76:79],v[152:155]
v_exp_f32 v234,v234
v_mfma_f32_16x16x32_bf16 v[156:159],a[188:191],v[76:79],v[32:35]
v_exp_f32 v235,v235
v_mfma_f32_16x16x32_bf16 v[156:159],a[184:187],v[72:75],v[156:159]
buffer_load_dwordx4 v5,s[20:23],s47 offen offset:3072 nt lds
v_mfma_f32_16x16x32_bf16 v[156:159],a[180:183],v[68:71],v[156:159]
v_exp_f32 v236,v236
v_mfma_f32_16x16x32_bf16 v[156:159],a[176:179],v[64:67],v[156:159]
v_exp_f32 v237,v237
v_mfma_f32_16x16x32_bf16 a[64:67],a[192:195],v[200:203],0
v_exp_f32 v238,v238
v_mfma_f32_16x16x32_bf16 a[64:67],a[196:199],v[204:207],a[64:67]
)A"
R"A(v_exp_f32 v239,v239
v_mfma_f32_16x16x32_bf16 a[68:71],a[204:207],v[204:207],0
v_exp_f32 v248,v248
v_mfma_f32_16x16x32_bf16 a[68:71],a[200:203],v[200:203],a[68:71]
v_exp_f32 v249,v249
v_mfma_f32_16x16x32_bf16 a[72:75],a[208:211],v[200:203],0
v_exp_f32 v250,v250
v_mfma_f32_16x16x32_bf16 a[72:75],a[212:215],v[204:207],a[72:75]
v_exp_f32 v251,v251
v_mfma_f32_16x16x32_bf16 a[76:79],a[220:223],v[204:207],0
v_exp_f32 v252,v252
v_mfma_f32_16x16x32_bf16 a[76:79],a[216:219],v[200:203],a[76:79]
v_exp_f32 v253,v253
v_mfma_f32_16x16x32_bf16 a[80:83],a[224:227],v[200:203],0
s_add_u32 m0,65536,s52
v_exp_f32 v254,v254
v_mfma_f32_16x16x32_bf16 a[80:83],a[228:231],v[204:207],a[80:83]
v_exp_f32 v255,v255
v_mfma_f32_16x16x32_bf16 a[84:87],a[236:239],v[204:207],0
v_exp_f32 v136,v136
v_mfma_f32_16x16x32_bf16 a[84:87],a[232:235],v[200:203],a[84:87]
buffer_load_dwordx4 v4,s[24:27],s47 offen nt lds
v_mfma_f32_16x16x32_bf16 a[88:91],a[240:243],v[200:203],0
v_cvt_pk_bf16_f32 v232,v232,v233
v_cvt_pk_bf16_f32 v233,v234,v235
v_mfma_f32_16x16x32_bf16 a[88:91],a[244:247],v[204:207],a[88:91]
v_cvt_pk_bf16_f32 v234,v236,v237
v_cvt_pk_bf16_f32 v235,v238,v239
v_mfma_f32_16x16x32_bf16 a[92:95],a[252:255],v[204:207],0
v_cvt_pk_bf16_f32 v236,v248,v249
v_cvt_pk_bf16_f32 v237,v250,v251
v_mfma_f32_16x16x32_bf16 a[92:95],a[248:251],v[200:203],a[92:95]
v_cvt_pk_bf16_f32 v238,v252,v253
v_cvt_pk_bf16_f32 v239,v254,v255
v_smfmac_f32_16x16x64_bf16 v[56:59],v[28:31],v[200:207],v27
v_mfma_f32_16x16x32_bf16 v[168:171],a[128:131],v[80:83],v[36:39]
v_exp_f32 v137,v137
v_mfma_f32_16x16x32_bf16 v[168:171],a[132:135],v[84:87],v[168:171]
v_exp_f32 v138,v138
v_mfma_f32_16x16x32_bf16 v[168:171],a[136:139],v[88:91],v[168:171]
v_exp_f32 v139,v139
v_mfma_f32_16x16x32_bf16 v[168:171],a[140:143],v[92:95],v[168:171]
v_exp_f32 v140,v140
v_mfma_f32_16x16x32_bf16 v[172:175],a[156:159],v[92:95],v[36:39]
v_exp_f32 v141,v141
v_mfma_f32_16x16x32_bf16 v[172:175],a[152:155],v[88:91],v[172:175]
v_exp_f32 v142,v142
v_mfma_f32_16x16x32_bf16 v[172:175],a[148:151],v[84:87],v[172:175]
v_exp_f32 v143,v143
v_mfma_f32_16x16x32_bf16 v[172:175],a[144:147],v[80:83],v[172:175]
v_exp_f32 v152,v152
v_mfma_f32_16x16x32_bf16 v[184:187],a[160:163],v[80:83],v[36:39]
buffer_load_dwordx4 v5,s[24:27],s47 offen offset:1024 nt lds
v_mfma_f32_16x16x32_bf16 v[184:187],a[164:167],v[84:87],v[184:187]
v_exp_f32 v153,v153
v_mfma_f32_16x16x32_bf16 v[184:187],a[168:171],v[88:91],v[184:187]
v_exp_f32 v154,v154
v_mfma_f32_16x16x32_bf16 v[184:187],a[172:175],v[92:95],v[184:187]
v_exp_f32 v155,v155
v_mfma_f32_16x16x32_bf16 v[188:191],a[188:191],v[92:95],v[36:39]
v_exp_f32 v156,v156
v_mfma_f32_16x16x32_bf16 v[188:191],a[184:187],v[88:91],v[188:191]
v_exp_f32 v157,v157
v_mfma_f32_16x16x32_bf16 v[188:191],a[180:183],v[84:87],v[188:191]
v_exp_f32 v158,v158
v_mfma_f32_16x16x32_bf16 v[188:191],a[176:179],v[80:83],v[188:191]
v_exp_f32 v159,v159
v_mfma_f32_16x16x32_bf16 a[96:99],a[192:195],v[232:235],0
ds_read_b64_tr_b16 a[192:193],v2
ds_read_b64_tr_b16 a[194:195],v2 offset:4096
v_mfma_f32_16x16x32_bf16 a[96:99],a[196:199],v[236:239],a[96:99]
ds_read_b64_tr_b16 a[196:197],v2 offset:8192
v_cvt_pk_bf16_f32 v136,v136,v137
v_mfma_f32_16x16x32_bf16 a[100:103],a[204:207],v[236:239],0
ds_read_b64_tr_b16 a[198:199],v2 offset:12288
v_cvt_pk_bf16_f32 v137,v138,v139
v_mfma_f32_16x16x32_bf16 a[100:103],a[200:203],v[232:235],a[100:103]
ds_read_b64_tr_b16 a[204:205],v3 offset:8192
v_cvt_pk_bf16_f32 v138,v140,v141
v_mfma_f32_16x16x32_bf16 a[104:107],a[208:211],v[232:235],0
ds_read_b64_tr_b16 a[206:207],v3 offset:12288
ds_read_b64_tr_b16 a[200:201],v3
v_mfma_f32_16x16x32_bf16 a[104:107],a[212:215],v[236:239],a[104:107]
ds_read_b64_tr_b16 a[202:203],v3 offset:4096
v_cvt_pk_bf16_f32 v139,v142,v143
v_mfma_f32_16x16x32_bf16 a[108:111],a[220:223],v[236:239],0
buffer_load_dwordx4 v4,s[24:27],s47 offen offset:2048 nt lds
v_mfma_f32_16x16x32_bf16 a[108:111],a[216:219],v[232:235],a[108:111]
ds_read_b64_tr_b16 a[208:209],v2 offset:256
ds_read_b64_tr_b16 a[210:211],v2 offset:4352
v_mfma_f32_16x16x32_bf16 a[112:115],a[224:227],v[232:235],0
ds_read_b64_tr_b16 a[212:213],v2 offset:8448
v_cvt_pk_bf16_f32 v140,v152,v153
v_mfma_f32_16x16x32_bf16 a[112:115],a[228:231],v[236:239],a[112:115]
ds_read_b64_tr_b16 a[214:215],v2 offset:12544
v_cvt_pk_bf16_f32 v141,v154,v155
v_mfma_f32_16x16x32_bf16 a[116:119],a[236:239],v[236:239],0
ds_read_b64_tr_b16 a[220:221],v3 offset:8448
v_cvt_pk_bf16_f32 v142,v156,v157
v_mfma_f32_16x16x32_bf16 a[116:119],a[232:235],v[232:235],a[116:119]
ds_read_b64_tr_b16 a[222:223],v3 offset:12544
v_cvt_pk_bf16_f32 v143,v158,v159
v_mfma_f32_16x16x32_bf16 a[120:123],a[240:243],v[232:235],0
ds_read_b64_tr_b16 a[216:217],v3 offset:256
ds_read_b64_tr_b16 a[218:219],v3 offset:4352
v_mfma_f32_16x16x32_bf16 a[120:123],a[244:247],v[236:239],a[120:123]
v_exp_f32 v168,v168
v_mfma_f32_16x16x32_bf16 a[124:127],a[252:255],v[236:239],0
ds_read_b64_tr_b16 a[224:225],v2 offset:512
ds_read_b64_tr_b16 a[226:227],v2 offset:4608
v_mfma_f32_16x16x32_bf16 a[124:127],a[248:251],v[232:235],a[124:127]
v_exp_f32 v169,v169
v_smfmac_f32_16x16x64_bf16 v[60:63],v[28:31],v[232:239],v27
ds_read_b64_tr_b16 a[228:229],v2 offset:8704
v_mfma_f32_16x16x32_bf16 v[200:203],a[128:131],v[96:99],v[40:43]
ds_read_b64_tr_b16 a[230:231],v2 offset:12800
ds_read_b64_tr_b16 a[236:237],v3 offset:8704
v_mfma_f32_16x16x32_bf16 v[200:203],a[132:135],v[100:103],v[200:203]
ds_read_b64_tr_b16 a[238:239],v3 offset:12800
v_cvt_pk_bf16_f32 v168,v168,v169
v_mfma_f32_16x16x32_bf16 v[200:203],a[136:139],v[104:107],v[200:203]
ds_read_b64_tr_b16 a[232:233],v3 offset:512
v_mfma_f32_16x16x32_bf16 v[200:203],a[140:143],v[108:111],v[200:203]
buffer_load_dwordx4 v5,s[24:27],s47 offen offset:3072 nt lds
v_mfma_f32_16x16x32_bf16 v[204:207],a[156:159],v[108:111],v[40:43]
s_add_u32 s47,s47,16384
ds_read_b64_tr_b16 a[234:235],v3 offset:4608
ds_read_b64_tr_b16 a[240:241],v2 offset:768
v_mfma_f32_16x16x32_bf16 v[204:207],a[152:155],v[104:107],v[204:207]
s_and_b32 s47,s47,0x3ffff
s_or_b32 s47,s47,s46
ds_read_b64_tr_b16 a[242:243],v2 offset:4864
v_mfma_f32_16x16x32_bf16 v[204:207],a[148:151],v[100:103],v[204:207]
ds_read_b64_tr_b16 a[244:245],v2 offset:8960
v_mfma_f32_16x16x32_bf16 v[204:207],a[144:147],v[96:99],v[204:207]
ds_read_b64_tr_b16 a[246:247],v2 offset:13056
v_mfma_f32_16x16x32_bf16 v[216:219],a[160:163],v[96:99],v[40:43]
ds_read_b64_tr_b16 a[252:253],v3 offset:8960
ds_read_b64_tr_b16 a[254:255],v3 offset:13056
v_mfma_f32_16x16x32_bf16 v[216:219],a[164:167],v[100:103],v[216:219]
ds_read_b64_tr_b16 a[248:249],v3 offset:768
v_mfma_f32_16x16x32_bf16 v[216:219],a[168:171],v[104:107],v[216:219]
ds_read_b64_tr_b16 a[250:251],v3 offset:4864
v_mfma_f32_16x16x32_bf16 v[216:219],a[172:175],v[108:111],v[216:219]
v_exp_f32 v170,v170
v_mfma_f32_16x16x32_bf16 v[220:223],a[188:191],v[108:111],v[40:43]
v_exp_f32 v171,v171
v_mfma_f32_16x16x32_bf16 v[220:223],a[184:187],v[104:107],v[220:223]
v_exp_f32 v172,v172
v_mfma_f32_16x16x32_bf16 v[220:223],a[180:183],v[100:103],v[220:223]
v_exp_f32 v173,v173
v_mfma_f32_16x16x32_bf16 v[220:223],a[176:179],v[96:99],v[220:223]
s_waitcnt lgkmcnt(4)
v_exp_f32 v174,v174
v_mfma_f32_16x16x32_bf16 a[0:3],a[192:195],v[136:139],a[0:3]
v_exp_f32 v175,v175
v_mfma_f32_16x16x32_bf16 a[0:3],a[196:199],v[140:143],a[0:3]
v_cvt_pk_bf16_f32 v169,v170,v171
v_cvt_pk_bf16_f32 v170,v172,v173
v_mfma_f32_16x16x32_bf16 a[4:7],a[204:207],v[140:143],a[4:7]
v_exp_f32 v184,v184
v_mfma_f32_16x16x32_bf16 a[4:7],a[200:203],v[136:139],a[4:7]
s_memrealtime s[76:77]
v_exp_f32 v185,v185
v_mfma_f32_16x16x32_bf16 a[8:11],a[208:211],v[136:139],a[8:11]
v_exp_f32 v186,v186
v_mfma_f32_16x16x32_bf16 a[8:11],a[212:215],v[140:143],a[8:11]
v_exp_f32 v187,v187
v_mfma_f32_16x16x32_bf16 a[12:15],a[220:223],v[140:143],a[12:15]
v_exp_f32 v188,v188
v_mfma_f32_16x16x32_bf16 a[12:15],a[216:219],v[136:139],a[12:15]
v_exp_f32 v189,v189
v_mfma_f32_16x16x32_bf16 a[16:19],a[224:227],v[136:139],a[16:19]
v_exp_f32 v190,v190
v_mfma_f32_16x16x32_bf16 a[16:19],a[228:231],v[140:143],a[16:19]
v_exp_f32 v191,v191
v_mfma_f32_16x16x32_bf16 a[20:23],a[236:239],v[140:143],a[20:23]
s_waitcnt lgkmcnt(0)
v_cvt_pk_bf16_f32 v171,v174,v175
v_cvt_pk_bf16_f32 v172,v184,v185
v_mfma_f32_16x16x32_bf16 a[20:23],a[232:235],v[136:139],a[20:23]
s_sub_u32 s79,s78,s76
v_cvt_pk_bf16_f32 v173,v186,v187
v_cvt_pk_bf16_f32 v174,v188,v189
v_mfma_f32_16x16x32_bf16 a[24:27],a[240:243],v[136:139],a[24:27]
s_add_u32 s78,s76,s60
v_cvt_pk_bf16_f32 v175,v190,v191
v_mfma_f32_16x16x32_bf16 a[24:27],a[244:247],v[140:143],a[24:27]
s_cmp_gt_i32 s79,s95
s_cbranch_scc1 L_ps2_%=
L_pr2_%=:
v_mfma_f32_16x16x32_bf16 a[28:31],a[252:255],v[140:143],a[28:31]
v_mfma_f32_16x16x32_bf16 a[28:31],a[248:251],v[136:139],a[28:31]
v_smfmac_f32_16x16x64_bf16 v[48:51],v[28:31],v[136:143],v27
s_mov_b32 s53,13
s_cmp_eq_u32 s40,3
s_cbranch_scc0 L_att0_%=
s_cmp_gt_i32 s18,0
s_cbranch_scc1 L_att0v0_%=
s_cmp_gt_i32 s19,0
s_cbranch_scc1 L_att0v1_%=
s_cmp_gt_i32 s100,0
s_cbranch_scc1 L_att0v2_%=
.p2align 5
L_att0v3_%=:
s_waitcnt vmcnt(8) lgkmcnt(0)
v_exp_f32 v200,v200
s_barrier
s_add_u32 m0,s49,s51
ds_read_b128 a[128:131],v1
s_mov_b32 s90,L_endseg_%=-L_pc_%=+128
v_exp_f32 v201,v201
s_lshr_b32 s45,s45,1
ds_read_b128 a[132:135],v1 offset:256
v_cvt_pk_bf16_f32 v200,v200,v201
buffer_load_dwordx4 v4,s[20:23],s47 offen nt lds
ds_read_b128 a[136:139],v1 offset:512
v_add_u32 v2,s48,v2
v_exp_f32 v202,v202
s_add_u32 s62,s50,32768
ds_read_b128 a[140:143],v1 offset:768
v_add_u32 v3,s48,v3
s_and_b32 s62,s62,131071
v_exp_f32 v203,v203
s_add_u32 s54,s54,16384
ds_read_b128 a[156:159],v1 offset:4864
v_cvt_pk_bf16_f32 v201,v202,v203
s_sub_u32 s48,s62,s50
v_exp_f32 v204,v204
s_sub_u32 s53,s53,1
v_exp_f32 v205,v205
s_and_b32 s46,s54,0xfffc0000
ds_read_b128 a[152:155],v1 offset:4608
v_cvt_pk_bf16_f32 v202,v204,v205
s_mov_b32 s50,s62
v_exp_f32 v206,v206
v_exp_f32 v207,v207
ds_read_b128 a[148:151],v1 offset:4352
v_cvt_pk_bf16_f32 v203,v206,v207
v_mfma_f32_16x16x32_bf16 a[32:35],a[192:195],v[168:171],a[32:35]
v_exp_f32 v216,v216
v_mfma_f32_16x16x32_bf16 a[32:35],a[196:199],v[172:175],a[32:35]
ds_read_b128 a[144:147],v1 offset:4096
v_mfma_f32_16x16x32_bf16 a[36:39],a[204:207],v[172:175],a[36:39]
buffer_load_dwordx4 v4,s[20:23],s47 offen offset:1024 nt lds
v_mfma_f32_16x16x32_bf16 a[36:39],a[200:203],v[168:171],a[36:39]
ds_read_b128 a[160:163],v1 offset:8192
v_mfma_f32_16x16x32_bf16 a[40:43],a[208:211],v[168:171],a[40:43]
v_exp_f32 v217,v217
v_mfma_f32_16x16x32_bf16 a[40:43],a[212:215],v[172:175],a[40:43]
ds_read_b128 a[164:167],v1 offset:8448
v_cvt_pk_bf16_f32 v204,v216,v217
v_mfma_f32_16x16x32_bf16 a[44:47],a[220:223],v[172:175],a[44:47]
v_exp_f32 v218,v218
v_mfma_f32_16x16x32_bf16 a[44:47],a[216:219],v[168:171],a[44:47]
ds_read_b128 a[168:171],v1 offset:8704
v_mfma_f32_16x16x32_bf16 a[48:51],a[224:227],v[168:171],a[48:51]
v_exp_f32 v219,v219
v_mfma_f32_16x16x32_bf16 a[48:51],a[228:231],v[172:175],a[48:51]
ds_read_b128 a[172:175],v1 offset:8960
v_cvt_pk_bf16_f32 v205,v218,v219
v_mfma_f32_16x16x32_bf16 a[52:55],a[236:239],v[172:175],a[52:55]
v_exp_f32 v220,v220
v_mfma_f32_16x16x32_bf16 a[52:55],a[232:235],v[168:171],a[52:55]
ds_read_b128 a[188:191],v1 offset:13056
v_mfma_f32_16x16x32_bf16 a[56:59],a[240:243],v[168:171],a[56:59]
v_exp_f32 v221,v221
v_mfma_f32_16x16x32_bf16 a[56:59],a[244:247],v[172:175],a[56:59]
ds_read_b128 a[184:187],v1 offset:12800
v_cvt_pk_bf16_f32 v206,v220,v221
v_mfma_f32_16x16x32_bf16 a[60:63],a[252:255],v[172:175],a[60:63]
v_exp_f32 v222,v222
v_mfma_f32_16x16x32_bf16 a[60:63],a[248:251],v[168:171],a[60:63]
ds_read_b128 a[180:183],v1 offset:12544
v_smfmac_f32_16x16x64_bf16 v[52:55],v[28:31],v[168:175],v27
s_waitcnt lgkmcnt(4)
v_mfma_f32_16x16x32_bf16 v[136:139],a[128:131],v[64:67],v[32:35]
buffer_load_dwordx4 v5,s[20:23],s47 offen offset:2048 nt lds
v_mfma_f32_16x16x32_bf16 v[136:139],a[132:135],v[68:71],v[136:139]
ds_read_b128 a[176:179],v1 offset:12288
v_add_u32 v1,s48,v1
v_mfma_f32_16x16x32_bf16 v[136:139],a[136:139],v[72:75],v[136:139]
v_exp_f32 v223,v223
v_mfma_f32_16x16x32_bf16 v[136:139],a[140:143],v[76:79],v[136:139]
)A"
R"A(v_cvt_pk_bf16_f32 v207,v222,v223
v_mfma_f32_16x16x32_bf16 v[140:143],a[156:159],v[76:79],v[32:35]
v_mfma_f32_16x16x32_bf16 v[140:143],a[152:155],v[72:75],v[140:143]
v_mfma_f32_16x16x32_bf16 v[140:143],a[148:151],v[68:71],v[140:143]
v_mfma_f32_16x16x32_bf16 v[140:143],a[144:147],v[64:67],v[140:143]
v_mfma_f32_16x16x32_bf16 v[152:155],a[160:163],v[64:67],v[32:35]
v_mfma_f32_16x16x32_bf16 v[152:155],a[164:167],v[68:71],v[152:155]
v_mfma_f32_16x16x32_bf16 v[152:155],a[168:171],v[72:75],v[152:155]
s_waitcnt lgkmcnt(0)
v_mfma_f32_16x16x32_bf16 v[152:155],a[172:175],v[76:79],v[152:155]
v_mfma_f32_16x16x32_bf16 v[156:159],a[188:191],v[76:79],v[32:35]
v_mfma_f32_16x16x32_bf16 v[156:159],a[184:187],v[72:75],v[156:159]
buffer_load_dwordx4 v5,s[20:23],s47 offen offset:3072 nt lds
v_mfma_f32_16x16x32_bf16 v[156:159],a[180:183],v[68:71],v[156:159]
v_mfma_f32_16x16x32_bf16 v[156:159],a[176:179],v[64:67],v[156:159]
v_mfma_f32_16x16x32_bf16 a[64:67],a[192:195],v[200:203],a[64:67]
v_mfma_f32_16x16x32_bf16 a[64:67],a[196:199],v[204:207],a[64:67]
v_mfma_f32_16x16x32_bf16 a[68:71],a[204:207],v[204:207],a[68:71]
v_mfma_f32_16x16x32_bf16 a[68:71],a[200:203],v[200:203],a[68:71]
v_mfma_f32_16x16x32_bf16 a[72:75],a[208:211],v[200:203],a[72:75]
v_mfma_f32_16x16x32_bf16 a[72:75],a[212:215],v[204:207],a[72:75]
v_exp_f32 v136,v136
v_mfma_f32_16x16x32_bf16 a[76:79],a[220:223],v[204:207],a[76:79]
v_exp_f32 v137,v137
v_mfma_f32_16x16x32_bf16 a[76:79],a[216:219],v[200:203],a[76:79]
v_exp_f32 v138,v138
v_mfma_f32_16x16x32_bf16 a[80:83],a[224:227],v[200:203],a[80:83]
s_add_u32 m0,s49,s52
v_mfma_f32_16x16x32_bf16 a[80:83],a[228:231],v[204:207],a[80:83]
s_xor_b32 s49,s50,65536
v_mfma_f32_16x16x32_bf16 a[84:87],a[236:239],v[204:207],a[84:87]
v_mfma_f32_16x16x32_bf16 a[84:87],a[232:235],v[200:203],a[84:87]
buffer_load_dwordx4 v4,s[24:27],s47 offen nt lds
v_mfma_f32_16x16x32_bf16 a[88:91],a[240:243],v[200:203],a[88:91]
v_mfma_f32_16x16x32_bf16 a[88:91],a[244:247],v[204:207],a[88:91]
v_exp_f32 v139,v139
v_mfma_f32_16x16x32_bf16 a[92:95],a[252:255],v[204:207],a[92:95]
v_exp_f32 v140,v140
v_mfma_f32_16x16x32_bf16 a[92:95],a[248:251],v[200:203],a[92:95]
v_exp_f32 v141,v141
v_smfmac_f32_16x16x64_bf16 v[56:59],v[28:31],v[200:207],v27
v_mfma_f32_16x16x32_bf16 v[168:171],a[128:131],v[80:83],v[36:39]
v_exp_f32 v142,v142
v_mfma_f32_16x16x32_bf16 v[168:171],a[132:135],v[84:87],v[168:171]
v_exp_f32 v143,v143
v_mfma_f32_16x16x32_bf16 v[168:171],a[136:139],v[88:91],v[168:171]
v_exp_f32 v152,v152
v_mfma_f32_16x16x32_bf16 v[168:171],a[140:143],v[92:95],v[168:171]
v_exp_f32 v153,v153
v_mfma_f32_16x16x32_bf16 v[172:175],a[156:159],v[92:95],v[36:39]
v_exp_f32 v154,v154
v_mfma_f32_16x16x32_bf16 v[172:175],a[152:155],v[88:91],v[172:175]
v_exp_f32 v155,v155
v_mfma_f32_16x16x32_bf16 v[172:175],a[148:151],v[84:87],v[172:175]
v_exp_f32 v156,v156
v_mfma_f32_16x16x32_bf16 v[172:175],a[144:147],v[80:83],v[172:175]
v_exp_f32 v157,v157
v_mfma_f32_16x16x32_bf16 v[184:187],a[160:163],v[80:83],v[36:39]
buffer_load_dwordx4 v5,s[24:27],s47 offen offset:1024 nt lds
v_mfma_f32_16x16x32_bf16 v[184:187],a[164:167],v[84:87],v[184:187]
v_exp_f32 v158,v158
v_mfma_f32_16x16x32_bf16 v[184:187],a[168:171],v[88:91],v[184:187]
v_exp_f32 v159,v159
v_mfma_f32_16x16x32_bf16 v[184:187],a[172:175],v[92:95],v[184:187]
v_exp_f32 v168,v168
v_mfma_f32_16x16x32_bf16 v[188:191],a[188:191],v[92:95],v[36:39]
v_exp_f32 v169,v169
v_mfma_f32_16x16x32_bf16 v[188:191],a[184:187],v[88:91],v[188:191]
v_exp_f32 v170,v170
v_mfma_f32_16x16x32_bf16 v[188:191],a[180:183],v[84:87],v[188:191]
v_exp_f32 v171,v171
v_mfma_f32_16x16x32_bf16 v[188:191],a[176:179],v[80:83],v[188:191]
v_exp_f32 v172,v172
ds_read_b64_tr_b16 a[192:193],v2
v_cvt_pk_bf16_f32 v168,v168,v169
ds_read_b64_tr_b16 a[194:195],v2 offset:4096
v_cvt_pk_bf16_f32 v169,v170,v171
v_exp_f32 v173,v173
ds_read_b64_tr_b16 a[196:197],v2 offset:8192
v_cvt_pk_bf16_f32 v170,v172,v173
v_exp_f32 v174,v174
v_exp_f32 v175,v175
buffer_load_dwordx4 v4,s[24:27],s47 offen offset:2048 nt lds
ds_read_b64_tr_b16 a[198:199],v2 offset:12288
v_cvt_pk_bf16_f32 v171,v174,v175
v_exp_f32 v184,v184
v_exp_f32 v185,v185
ds_read_b64_tr_b16 a[204:205],v3 offset:8192
v_cvt_pk_bf16_f32 v136,v136,v137
ds_read_b64_tr_b16 a[206:207],v3 offset:12288
v_cvt_pk_bf16_f32 v172,v184,v185
ds_read_b64_tr_b16 a[200:201],v3
ds_read_b64_tr_b16 a[202:203],v3 offset:4096
v_cvt_pk_bf16_f32 v137,v138,v139
ds_read_b64_tr_b16 a[208:209],v2 offset:256
ds_read_b64_tr_b16 a[210:211],v2 offset:4352
ds_read_b64_tr_b16 a[212:213],v2 offset:8448
v_mfma_f32_16x16x32_bf16 v[200:203],a[128:131],v[96:99],v[40:43]
ds_read_b64_tr_b16 a[214:215],v2 offset:12544
v_cvt_pk_bf16_f32 v138,v140,v141
v_mfma_f32_16x16x32_bf16 v[200:203],a[132:135],v[100:103],v[200:203]
ds_read_b64_tr_b16 a[220:221],v3 offset:8448
v_mfma_f32_16x16x32_bf16 v[200:203],a[136:139],v[104:107],v[200:203]
ds_read_b64_tr_b16 a[222:223],v3 offset:12544
v_cvt_pk_bf16_f32 v139,v142,v143
v_mfma_f32_16x16x32_bf16 v[200:203],a[140:143],v[108:111],v[200:203]
buffer_load_dwordx4 v5,s[24:27],s47 offen offset:3072 nt lds
v_mfma_f32_16x16x32_bf16 v[204:207],a[156:159],v[108:111],v[40:43]
s_add_u32 s47,s47,16384
s_and_b32 s47,s47,0x3ffff
ds_read_b64_tr_b16 a[216:217],v3 offset:256
v_mfma_f32_16x16x32_bf16 v[204:207],a[152:155],v[104:107],v[204:207]
s_or_b32 s47,s47,s46
ds_read_b64_tr_b16 a[218:219],v3 offset:4352
v_cvt_pk_bf16_f32 v140,v152,v153
v_mfma_f32_16x16x32_bf16 v[204:207],a[148:151],v[100:103],v[204:207]
ds_read_b64_tr_b16 a[224:225],v2 offset:512
v_mfma_f32_16x16x32_bf16 v[204:207],a[144:147],v[96:99],v[204:207]
ds_read_b64_tr_b16 a[226:227],v2 offset:4608
v_mfma_f32_16x16x32_bf16 v[216:219],a[160:163],v[96:99],v[40:43]
ds_read_b64_tr_b16 a[228:229],v2 offset:8704
v_cvt_pk_bf16_f32 v141,v154,v155
v_mfma_f32_16x16x32_bf16 v[216:219],a[164:167],v[100:103],v[216:219]
ds_read_b64_tr_b16 a[230:231],v2 offset:12800
v_mfma_f32_16x16x32_bf16 v[216:219],a[168:171],v[104:107],v[216:219]
ds_read_b64_tr_b16 a[236:237],v3 offset:8704
v_mfma_f32_16x16x32_bf16 v[216:219],a[172:175],v[108:111],v[216:219]
ds_read_b64_tr_b16 a[238:239],v3 offset:12800
v_cvt_pk_bf16_f32 v142,v156,v157
v_mfma_f32_16x16x32_bf16 v[220:223],a[188:191],v[108:111],v[40:43]
ds_read_b64_tr_b16 a[232:233],v3 offset:512
v_mfma_f32_16x16x32_bf16 v[220:223],a[184:187],v[104:107],v[220:223]
ds_read_b64_tr_b16 a[234:235],v3 offset:4608
v_mfma_f32_16x16x32_bf16 v[220:223],a[180:183],v[100:103],v[220:223]
ds_read_b64_tr_b16 a[240:241],v2 offset:768
v_cvt_pk_bf16_f32 v143,v158,v159
v_mfma_f32_16x16x32_bf16 v[220:223],a[176:179],v[96:99],v[220:223]
s_waitcnt lgkmcnt(7)
buffer_load_dword v26,v24,s[88:91],s93 offen
v_mfma_f32_16x16x32_bf16 a[0:3],a[192:195],v[136:139],a[0:3]
ds_read_b64_tr_b16 a[242:243],v2 offset:4864
v_mfma_f32_16x16x32_bf16 a[0:3],a[196:199],v[140:143],a[0:3]
ds_read_b64_tr_b16 a[244:245],v2 offset:8960
v_mfma_f32_16x16x32_bf16 a[4:7],a[204:207],v[140:143],a[4:7]
ds_read_b64_tr_b16 a[246:247],v2 offset:13056
v_mfma_f32_16x16x32_bf16 a[4:7],a[200:203],v[136:139],a[4:7]
ds_read_b64_tr_b16 a[252:253],v3 offset:8960
v_mfma_f32_16x16x32_bf16 a[8:11],a[208:211],v[136:139],a[8:11]
ds_read_b64_tr_b16 a[254:255],v3 offset:13056
v_mfma_f32_16x16x32_bf16 a[8:11],a[212:215],v[140:143],a[8:11]
ds_read_b64_tr_b16 a[248:249],v3 offset:768
v_mfma_f32_16x16x32_bf16 a[12:15],a[220:223],v[140:143],a[12:15]
ds_read_b64_tr_b16 a[250:251],v3 offset:4864
v_mfma_f32_16x16x32_bf16 a[12:15],a[216:219],v[136:139],a[12:15]
s_bitcmp1_b32 s45,0
s_cbranch_scc0 L_t31v30_%=
s_mov_b32 m0,s80
s_add_u32 s80,s80,4096
buffer_load_dwordx4 v25,s[20:23],s83 offen nt lds
s_add_u32 s83,s83,4096
L_t31v30_%=:
v_mfma_f32_16x16x32_bf16 a[16:19],a[224:227],v[136:139],a[16:19]
s_waitcnt lgkmcnt(6)
v_exp_f32 v186,v186
v_mfma_f32_16x16x32_bf16 a[16:19],a[228:231],v[140:143],a[16:19]
v_exp_f32 v187,v187
v_mfma_f32_16x16x32_bf16 a[20:23],a[236:239],v[140:143],a[20:23]
v_exp_f32 v188,v188
v_mfma_f32_16x16x32_bf16 a[20:23],a[232:235],v[136:139],a[20:23]
v_exp_f32 v189,v189
v_mfma_f32_16x16x32_bf16 a[24:27],a[240:243],v[136:139],a[24:27]
s_waitcnt lgkmcnt(2)
v_exp_f32 v190,v190
v_mfma_f32_16x16x32_bf16 a[24:27],a[244:247],v[140:143],a[24:27]
v_cvt_pk_bf16_f32 v173,v186,v187
v_mfma_f32_16x16x32_bf16 a[28:31],a[252:255],v[140:143],a[28:31]
s_waitcnt lgkmcnt(0)
v_exp_f32 v191,v191
v_mfma_f32_16x16x32_bf16 a[28:31],a[248:251],v[136:139],a[28:31]
v_cvt_pk_bf16_f32 v174,v188,v189
v_cvt_pk_bf16_f32 v175,v190,v191
s_cmp_lg_u32 s53,0
v_smfmac_f32_16x16x64_bf16 v[48:51],v[28:31],v[136:143],v27
s_cbranch_scc1 L_att0v3_%=
s_waitcnt lgkmcnt(0)
s_nop 7
s_branch L_att0x_%=
.p2align 5
L_att0v0_%=:
v_mfma_f32_16x16x32_bf16 v[232:235],a[128:131],v[112:115],v[44:47]
s_waitcnt vmcnt(8) lgkmcnt(0)
v_exp_f32 v200,v200
v_mfma_f32_16x16x32_bf16 v[232:235],a[132:135],v[116:119],v[232:235]
s_barrier
s_add_u32 m0,s49,s51
ds_read_b128 a[128:131],v1
v_mfma_f32_16x16x32_bf16 v[232:235],a[136:139],v[120:123],v[232:235]
s_mov_b32 s90,L_endseg_%=-L_pc_%=+128
v_exp_f32 v201,v201
v_mfma_f32_16x16x32_bf16 v[232:235],a[140:143],v[124:127],v[232:235]
s_lshr_b32 s45,s45,1
ds_read_b128 a[132:135],v1 offset:256
v_cvt_pk_bf16_f32 v200,v200,v201
v_mfma_f32_16x16x32_bf16 v[236:239],a[156:159],v[124:127],v[44:47]
buffer_load_dwordx4 v4,s[20:23],s47 offen nt lds
v_mfma_f32_16x16x32_bf16 v[236:239],a[152:155],v[120:123],v[236:239]
ds_read_b128 a[136:139],v1 offset:512
v_add_u32 v2,s48,v2
v_mfma_f32_16x16x32_bf16 v[236:239],a[148:151],v[116:119],v[236:239]
v_exp_f32 v202,v202
v_mfma_f32_16x16x32_bf16 v[236:239],a[144:147],v[112:115],v[236:239]
s_add_u32 s62,s50,32768
ds_read_b128 a[140:143],v1 offset:768
v_add_u32 v3,s48,v3
v_mfma_f32_16x16x32_bf16 v[248:251],a[160:163],v[112:115],v[44:47]
s_and_b32 s62,s62,131071
v_exp_f32 v203,v203
v_mfma_f32_16x16x32_bf16 v[248:251],a[164:167],v[116:119],v[248:251]
s_add_u32 s54,s54,16384
ds_read_b128 a[156:159],v1 offset:4864
v_cvt_pk_bf16_f32 v201,v202,v203
v_mfma_f32_16x16x32_bf16 v[248:251],a[168:171],v[120:123],v[248:251]
s_sub_u32 s48,s62,s50
v_exp_f32 v204,v204
v_mfma_f32_16x16x32_bf16 v[248:251],a[172:175],v[124:127],v[248:251]
s_sub_u32 s53,s53,1
v_exp_f32 v205,v205
v_mfma_f32_16x16x32_bf16 v[252:255],a[188:191],v[124:127],v[44:47]
s_and_b32 s46,s54,0xfffc0000
ds_read_b128 a[152:155],v1 offset:4608
v_cvt_pk_bf16_f32 v202,v204,v205
v_mfma_f32_16x16x32_bf16 v[252:255],a[184:187],v[120:123],v[252:255]
s_mov_b32 s50,s62
v_exp_f32 v206,v206
v_mfma_f32_16x16x32_bf16 v[252:255],a[180:183],v[116:119],v[252:255]
v_exp_f32 v207,v207
v_mfma_f32_16x16x32_bf16 v[252:255],a[176:179],v[112:115],v[252:255]
ds_read_b128 a[148:151],v1 offset:4352
v_cvt_pk_bf16_f32 v203,v206,v207
v_mfma_f32_16x16x32_bf16 a[32:35],a[192:195],v[168:171],a[32:35]
v_exp_f32 v216,v216
v_mfma_f32_16x16x32_bf16 a[32:35],a[196:199],v[172:175],a[32:35]
ds_read_b128 a[144:147],v1 offset:4096
v_mfma_f32_16x16x32_bf16 a[36:39],a[204:207],v[172:175],a[36:39]
buffer_load_dwordx4 v4,s[20:23],s47 offen offset:1024 nt lds
v_mfma_f32_16x16x32_bf16 a[36:39],a[200:203],v[168:171],a[36:39]
ds_read_b128 a[160:163],v1 offset:8192
v_mfma_f32_16x16x32_bf16 a[40:43],a[208:211],v[168:171],a[40:43]
v_exp_f32 v217,v217
v_mfma_f32_16x16x32_bf16 a[40:43],a[212:215],v[172:175],a[40:43]
ds_read_b128 a[164:167],v1 offset:8448
v_cvt_pk_bf16_f32 v204,v216,v217
v_mfma_f32_16x16x32_bf16 a[44:47],a[220:223],v[172:175],a[44:47]
v_exp_f32 v218,v218
v_mfma_f32_16x16x32_bf16 a[44:47],a[216:219],v[168:171],a[44:47]
ds_read_b128 a[168:171],v1 offset:8704
v_mfma_f32_16x16x32_bf16 a[48:51],a[224:227],v[168:171],a[48:51]
v_exp_f32 v219,v219
v_mfma_f32_16x16x32_bf16 a[48:51],a[228:231],v[172:175],a[48:51]
ds_read_b128 a[172:175],v1 offset:8960
v_cvt_pk_bf16_f32 v205,v218,v219
v_mfma_f32_16x16x32_bf16 a[52:55],a[236:239],v[172:175],a[52:55]
v_exp_f32 v220,v220
v_mfma_f32_16x16x32_bf16 a[52:55],a[232:235],v[168:171],a[52:55]
ds_read_b128 a[188:191],v1 offset:13056
v_mfma_f32_16x16x32_bf16 a[56:59],a[240:243],v[168:171],a[56:59]
v_exp_f32 v221,v221
v_mfma_f32_16x16x32_bf16 a[56:59],a[244:247],v[172:175],a[56:59]
)A"
R"A(ds_read_b128 a[184:187],v1 offset:12800
v_cvt_pk_bf16_f32 v206,v220,v221
v_mfma_f32_16x16x32_bf16 a[60:63],a[252:255],v[172:175],a[60:63]
v_exp_f32 v222,v222
v_mfma_f32_16x16x32_bf16 a[60:63],a[248:251],v[168:171],a[60:63]
ds_read_b128 a[180:183],v1 offset:12544
v_smfmac_f32_16x16x64_bf16 v[52:55],v[28:31],v[168:175],v27
buffer_load_dwordx4 v5,s[20:23],s47 offen offset:2048 nt lds
ds_read_b128 a[176:179],v1 offset:12288
v_add_u32 v1,s48,v1
v_exp_f32 v223,v223
s_nop 0
v_cvt_pk_bf16_f32 v207,v222,v223
v_exp_f32 v232,v232
v_exp_f32 v233,v233
v_exp_f32 v234,v234
v_exp_f32 v235,v235
v_exp_f32 v236,v236
v_exp_f32 v237,v237
v_exp_f32 v238,v238
v_exp_f32 v239,v239
v_exp_f32 v248,v248
buffer_load_dwordx4 v5,s[20:23],s47 offen offset:3072 nt lds
v_exp_f32 v249,v249
v_exp_f32 v250,v250
v_mfma_f32_16x16x32_bf16 a[64:67],a[192:195],v[200:203],a[64:67]
v_exp_f32 v251,v251
v_mfma_f32_16x16x32_bf16 a[64:67],a[196:199],v[204:207],a[64:67]
v_exp_f32 v252,v252
v_mfma_f32_16x16x32_bf16 a[68:71],a[204:207],v[204:207],a[68:71]
v_exp_f32 v253,v253
v_mfma_f32_16x16x32_bf16 a[68:71],a[200:203],v[200:203],a[68:71]
v_exp_f32 v254,v254
v_mfma_f32_16x16x32_bf16 a[72:75],a[208:211],v[200:203],a[72:75]
v_exp_f32 v255,v255
v_mfma_f32_16x16x32_bf16 a[72:75],a[212:215],v[204:207],a[72:75]
v_mfma_f32_16x16x32_bf16 a[76:79],a[220:223],v[204:207],a[76:79]
v_mfma_f32_16x16x32_bf16 a[76:79],a[216:219],v[200:203],a[76:79]
v_mfma_f32_16x16x32_bf16 a[80:83],a[224:227],v[200:203],a[80:83]
s_add_u32 m0,s49,s52
v_cvt_pk_bf16_f32 v232,v232,v233
v_cvt_pk_bf16_f32 v233,v234,v235
v_mfma_f32_16x16x32_bf16 a[80:83],a[228:231],v[204:207],a[80:83]
s_xor_b32 s49,s50,65536
v_cvt_pk_bf16_f32 v234,v236,v237
v_cvt_pk_bf16_f32 v235,v238,v239
v_mfma_f32_16x16x32_bf16 a[84:87],a[236:239],v[204:207],a[84:87]
v_cvt_pk_bf16_f32 v236,v248,v249
v_cvt_pk_bf16_f32 v237,v250,v251
v_mfma_f32_16x16x32_bf16 a[84:87],a[232:235],v[200:203],a[84:87]
buffer_load_dwordx4 v4,s[24:27],s47 offen nt lds
v_mfma_f32_16x16x32_bf16 a[88:91],a[240:243],v[200:203],a[88:91]
v_cvt_pk_bf16_f32 v238,v252,v253
v_cvt_pk_bf16_f32 v239,v254,v255
v_mfma_f32_16x16x32_bf16 a[88:91],a[244:247],v[204:207],a[88:91]
v_mfma_f32_16x16x32_bf16 a[92:95],a[252:255],v[204:207],a[92:95]
v_mfma_f32_16x16x32_bf16 a[92:95],a[248:251],v[200:203],a[92:95]
v_smfmac_f32_16x16x64_bf16 v[56:59],v[28:31],v[200:207],v27
s_waitcnt lgkmcnt(0)
v_mfma_f32_16x16x32_bf16 v[168:171],a[128:131],v[80:83],v[36:39]
v_mfma_f32_16x16x32_bf16 v[168:171],a[132:135],v[84:87],v[168:171]
v_mfma_f32_16x16x32_bf16 v[168:171],a[136:139],v[88:91],v[168:171]
v_mfma_f32_16x16x32_bf16 v[168:171],a[140:143],v[92:95],v[168:171]
v_mfma_f32_16x16x32_bf16 v[172:175],a[156:159],v[92:95],v[36:39]
v_mfma_f32_16x16x32_bf16 v[172:175],a[152:155],v[88:91],v[172:175]
v_mfma_f32_16x16x32_bf16 v[172:175],a[148:151],v[84:87],v[172:175]
v_mfma_f32_16x16x32_bf16 v[172:175],a[144:147],v[80:83],v[172:175]
v_mfma_f32_16x16x32_bf16 v[184:187],a[160:163],v[80:83],v[36:39]
buffer_load_dwordx4 v5,s[24:27],s47 offen offset:1024 nt lds
v_mfma_f32_16x16x32_bf16 v[184:187],a[164:167],v[84:87],v[184:187]
v_mfma_f32_16x16x32_bf16 v[184:187],a[168:171],v[88:91],v[184:187]
v_mfma_f32_16x16x32_bf16 v[184:187],a[172:175],v[92:95],v[184:187]
v_exp_f32 v168,v168
v_mfma_f32_16x16x32_bf16 v[188:191],a[188:191],v[92:95],v[36:39]
v_exp_f32 v169,v169
v_mfma_f32_16x16x32_bf16 v[188:191],a[184:187],v[88:91],v[188:191]
v_exp_f32 v170,v170
v_mfma_f32_16x16x32_bf16 v[188:191],a[180:183],v[84:87],v[188:191]
v_exp_f32 v171,v171
v_mfma_f32_16x16x32_bf16 v[188:191],a[176:179],v[80:83],v[188:191]
v_exp_f32 v172,v172
v_mfma_f32_16x16x32_bf16 a[96:99],a[192:195],v[232:235],a[96:99]
ds_read_b64_tr_b16 a[192:193],v2
v_cvt_pk_bf16_f32 v168,v168,v169
v_mfma_f32_16x16x32_bf16 a[96:99],a[196:199],v[236:239],a[96:99]
ds_read_b64_tr_b16 a[194:195],v2 offset:4096
v_cvt_pk_bf16_f32 v169,v170,v171
v_mfma_f32_16x16x32_bf16 a[100:103],a[204:207],v[236:239],a[100:103]
v_exp_f32 v173,v173
v_mfma_f32_16x16x32_bf16 a[100:103],a[200:203],v[232:235],a[100:103]
ds_read_b64_tr_b16 a[196:197],v2 offset:8192
v_cvt_pk_bf16_f32 v170,v172,v173
v_mfma_f32_16x16x32_bf16 a[104:107],a[208:211],v[232:235],a[104:107]
v_exp_f32 v174,v174
v_mfma_f32_16x16x32_bf16 a[104:107],a[212:215],v[236:239],a[104:107]
v_exp_f32 v175,v175
v_mfma_f32_16x16x32_bf16 a[108:111],a[220:223],v[236:239],a[108:111]
buffer_load_dwordx4 v4,s[24:27],s47 offen offset:2048 nt lds
v_mfma_f32_16x16x32_bf16 a[108:111],a[216:219],v[232:235],a[108:111]
ds_read_b64_tr_b16 a[198:199],v2 offset:12288
v_cvt_pk_bf16_f32 v171,v174,v175
v_mfma_f32_16x16x32_bf16 a[112:115],a[224:227],v[232:235],a[112:115]
v_exp_f32 v184,v184
v_mfma_f32_16x16x32_bf16 a[112:115],a[228:231],v[236:239],a[112:115]
v_exp_f32 v185,v185
v_mfma_f32_16x16x32_bf16 a[116:119],a[236:239],v[236:239],a[116:119]
ds_read_b64_tr_b16 a[204:205],v3 offset:8192
v_mfma_f32_16x16x32_bf16 a[116:119],a[232:235],v[232:235],a[116:119]
ds_read_b64_tr_b16 a[206:207],v3 offset:12288
v_cvt_pk_bf16_f32 v172,v184,v185
v_mfma_f32_16x16x32_bf16 a[120:123],a[240:243],v[232:235],a[120:123]
ds_read_b64_tr_b16 a[200:201],v3
v_mfma_f32_16x16x32_bf16 a[120:123],a[244:247],v[236:239],a[120:123]
ds_read_b64_tr_b16 a[202:203],v3 offset:4096
v_mfma_f32_16x16x32_bf16 a[124:127],a[252:255],v[236:239],a[124:127]
ds_read_b64_tr_b16 a[208:209],v2 offset:256
v_mfma_f32_16x16x32_bf16 a[124:127],a[248:251],v[232:235],a[124:127]
ds_read_b64_tr_b16 a[210:211],v2 offset:4352
v_smfmac_f32_16x16x64_bf16 v[60:63],v[28:31],v[232:239],v27
ds_read_b64_tr_b16 a[212:213],v2 offset:8448
v_mfma_f32_16x16x32_bf16 v[200:203],a[128:131],v[96:99],v[40:43]
ds_read_b64_tr_b16 a[214:215],v2 offset:12544
v_mfma_f32_16x16x32_bf16 v[200:203],a[132:135],v[100:103],v[200:203]
ds_read_b64_tr_b16 a[220:221],v3 offset:8448
v_mfma_f32_16x16x32_bf16 v[200:203],a[136:139],v[104:107],v[200:203]
ds_read_b64_tr_b16 a[222:223],v3 offset:12544
v_mfma_f32_16x16x32_bf16 v[200:203],a[140:143],v[108:111],v[200:203]
buffer_load_dwordx4 v5,s[24:27],s47 offen offset:3072 nt lds
v_mfma_f32_16x16x32_bf16 v[204:207],a[156:159],v[108:111],v[40:43]
s_add_u32 s47,s47,16384
s_and_b32 s47,s47,0x3ffff
ds_read_b64_tr_b16 a[216:217],v3 offset:256
v_mfma_f32_16x16x32_bf16 v[204:207],a[152:155],v[104:107],v[204:207]
s_or_b32 s47,s47,s46
ds_read_b64_tr_b16 a[218:219],v3 offset:4352
v_mfma_f32_16x16x32_bf16 v[204:207],a[148:151],v[100:103],v[204:207]
ds_read_b64_tr_b16 a[224:225],v2 offset:512
v_mfma_f32_16x16x32_bf16 v[204:207],a[144:147],v[96:99],v[204:207]
ds_read_b64_tr_b16 a[226:227],v2 offset:4608
v_mfma_f32_16x16x32_bf16 v[216:219],a[160:163],v[96:99],v[40:43]
ds_read_b64_tr_b16 a[228:229],v2 offset:8704
v_mfma_f32_16x16x32_bf16 v[216:219],a[164:167],v[100:103],v[216:219]
ds_read_b64_tr_b16 a[230:231],v2 offset:12800
v_mfma_f32_16x16x32_bf16 v[216:219],a[168:171],v[104:107],v[216:219]
ds_read_b64_tr_b16 a[236:237],v3 offset:8704
v_mfma_f32_16x16x32_bf16 v[216:219],a[172:175],v[108:111],v[216:219]
ds_read_b64_tr_b16 a[238:239],v3 offset:12800
v_mfma_f32_16x16x32_bf16 v[220:223],a[188:191],v[108:111],v[40:43]
ds_read_b64_tr_b16 a[232:233],v3 offset:512
v_mfma_f32_16x16x32_bf16 v[220:223],a[184:187],v[104:107],v[220:223]
ds_read_b64_tr_b16 a[234:235],v3 offset:4608
v_mfma_f32_16x16x32_bf16 v[220:223],a[180:183],v[100:103],v[220:223]
ds_read_b64_tr_b16 a[240:241],v2 offset:768
v_mfma_f32_16x16x32_bf16 v[220:223],a[176:179],v[96:99],v[220:223]
buffer_load_dword v26,v24,s[88:91],s93 offen
ds_read_b64_tr_b16 a[242:243],v2 offset:4864
ds_read_b64_tr_b16 a[244:245],v2 offset:8960
ds_read_b64_tr_b16 a[246:247],v2 offset:13056
ds_read_b64_tr_b16 a[252:253],v3 offset:8960
ds_read_b64_tr_b16 a[254:255],v3 offset:13056
ds_read_b64_tr_b16 a[248:249],v3 offset:768
ds_read_b64_tr_b16 a[250:251],v3 offset:4864
s_bitcmp1_b32 s45,0
s_cbranch_scc0 L_t31v00_%=
s_mov_b32 m0,s80
s_add_u32 s80,s80,4096
buffer_load_dwordx4 v25,s[20:23],s83 offen nt lds
s_add_u32 s83,s83,4096
L_t31v00_%=:
v_exp_f32 v186,v186
v_exp_f32 v187,v187
v_exp_f32 v188,v188
v_exp_f32 v189,v189
v_exp_f32 v190,v190
v_cvt_pk_bf16_f32 v173,v186,v187
v_exp_f32 v191,v191
v_cvt_pk_bf16_f32 v174,v188,v189
v_cvt_pk_bf16_f32 v175,v190,v191
s_cmp_lg_u32 s53,0
s_cbranch_scc1 L_att0v0_%=
s_waitcnt lgkmcnt(0)
s_nop 7
s_branch L_att0x_%=
.p2align 5
L_att0v1_%=:
v_mfma_f32_16x16x32_bf16 v[232:235],a[128:131],v[112:115],v[44:47]
s_waitcnt vmcnt(8) lgkmcnt(0)
v_exp_f32 v200,v200
v_mfma_f32_16x16x32_bf16 v[232:235],a[132:135],v[116:119],v[232:235]
s_barrier
s_add_u32 m0,s49,s51
ds_read_b128 a[128:131],v1
v_mfma_f32_16x16x32_bf16 v[232:235],a[136:139],v[120:123],v[232:235]
s_mov_b32 s90,L_endseg_%=-L_pc_%=+128
v_exp_f32 v201,v201
v_mfma_f32_16x16x32_bf16 v[232:235],a[140:143],v[124:127],v[232:235]
s_lshr_b32 s45,s45,1
ds_read_b128 a[132:135],v1 offset:256
v_cvt_pk_bf16_f32 v200,v200,v201
v_mfma_f32_16x16x32_bf16 v[236:239],a[156:159],v[124:127],v[44:47]
buffer_load_dwordx4 v4,s[20:23],s47 offen nt lds
v_mfma_f32_16x16x32_bf16 v[236:239],a[152:155],v[120:123],v[236:239]
ds_read_b128 a[136:139],v1 offset:512
v_add_u32 v2,s48,v2
v_mfma_f32_16x16x32_bf16 v[236:239],a[148:151],v[116:119],v[236:239]
v_exp_f32 v202,v202
v_mfma_f32_16x16x32_bf16 v[236:239],a[144:147],v[112:115],v[236:239]
s_add_u32 s62,s50,32768
ds_read_b128 a[140:143],v1 offset:768
v_add_u32 v3,s48,v3
v_mfma_f32_16x16x32_bf16 v[248:251],a[160:163],v[112:115],v[44:47]
s_and_b32 s62,s62,131071
v_exp_f32 v203,v203
v_mfma_f32_16x16x32_bf16 v[248:251],a[164:167],v[116:119],v[248:251]
s_add_u32 s54,s54,16384
ds_read_b128 a[156:159],v1 offset:4864
v_cvt_pk_bf16_f32 v201,v202,v203
v_mfma_f32_16x16x32_bf16 v[248:251],a[168:171],v[120:123],v[248:251]
s_sub_u32 s48,s62,s50
v_exp_f32 v204,v204
v_mfma_f32_16x16x32_bf16 v[248:251],a[172:175],v[124:127],v[248:251]
s_sub_u32 s53,s53,1
v_exp_f32 v205,v205
v_mfma_f32_16x16x32_bf16 v[252:255],a[188:191],v[124:127],v[44:47]
s_and_b32 s46,s54,0xfffc0000
ds_read_b128 a[152:155],v1 offset:4608
v_cvt_pk_bf16_f32 v202,v204,v205
v_mfma_f32_16x16x32_bf16 v[252:255],a[184:187],v[120:123],v[252:255]
s_mov_b32 s50,s62
v_exp_f32 v206,v206
v_mfma_f32_16x16x32_bf16 v[252:255],a[180:183],v[116:119],v[252:255]
v_exp_f32 v207,v207
v_mfma_f32_16x16x32_bf16 v[252:255],a[176:179],v[112:115],v[252:255]
ds_read_b128 a[148:151],v1 offset:4352
v_cvt_pk_bf16_f32 v203,v206,v207
v_exp_f32 v216,v216
ds_read_b128 a[144:147],v1 offset:4096
buffer_load_dwordx4 v4,s[20:23],s47 offen offset:1024 nt lds
ds_read_b128 a[160:163],v1 offset:8192
v_exp_f32 v217,v217
ds_read_b128 a[164:167],v1 offset:8448
v_cvt_pk_bf16_f32 v204,v216,v217
v_exp_f32 v218,v218
ds_read_b128 a[168:171],v1 offset:8704
v_exp_f32 v219,v219
ds_read_b128 a[172:175],v1 offset:8960
v_cvt_pk_bf16_f32 v205,v218,v219
v_exp_f32 v220,v220
ds_read_b128 a[188:191],v1 offset:13056
v_exp_f32 v221,v221
ds_read_b128 a[184:187],v1 offset:12800
v_cvt_pk_bf16_f32 v206,v220,v221
v_exp_f32 v222,v222
ds_read_b128 a[180:183],v1 offset:12544
s_waitcnt lgkmcnt(11)
v_mfma_f32_16x16x32_bf16 v[136:139],a[128:131],v[64:67],v[32:35]
buffer_load_dwordx4 v5,s[20:23],s47 offen offset:2048 nt lds
v_mfma_f32_16x16x32_bf16 v[136:139],a[132:135],v[68:71],v[136:139]
ds_read_b128 a[176:179],v1 offset:12288
v_add_u32 v1,s48,v1
v_mfma_f32_16x16x32_bf16 v[136:139],a[136:139],v[72:75],v[136:139]
v_exp_f32 v223,v223
v_mfma_f32_16x16x32_bf16 v[136:139],a[140:143],v[76:79],v[136:139]
s_waitcnt lgkmcnt(11)
v_cvt_pk_bf16_f32 v207,v222,v223
v_mfma_f32_16x16x32_bf16 v[140:143],a[156:159],v[76:79],v[32:35]
s_waitcnt lgkmcnt(10)
v_exp_f32 v232,v232
v_mfma_f32_16x16x32_bf16 v[140:143],a[152:155],v[72:75],v[140:143]
s_waitcnt lgkmcnt(9)
v_exp_f32 v233,v233
v_mfma_f32_16x16x32_bf16 v[140:143],a[148:151],v[68:71],v[140:143]
s_waitcnt lgkmcnt(8)
v_exp_f32 v234,v234
v_mfma_f32_16x16x32_bf16 v[140:143],a[144:147],v[64:67],v[140:143]
s_waitcnt lgkmcnt(1)
v_exp_f32 v235,v235
v_mfma_f32_16x16x32_bf16 v[152:155],a[160:163],v[64:67],v[32:35]
v_exp_f32 v236,v236
v_mfma_f32_16x16x32_bf16 v[152:155],a[164:167],v[68:71],v[152:155]
v_exp_f32 v237,v237
v_mfma_f32_16x16x32_bf16 v[152:155],a[168:171],v[72:75],v[152:155]
)A"
R"A(v_exp_f32 v238,v238
v_mfma_f32_16x16x32_bf16 v[152:155],a[172:175],v[76:79],v[152:155]
v_exp_f32 v239,v239
v_mfma_f32_16x16x32_bf16 v[156:159],a[188:191],v[76:79],v[32:35]
v_exp_f32 v248,v248
v_mfma_f32_16x16x32_bf16 v[156:159],a[184:187],v[72:75],v[156:159]
buffer_load_dwordx4 v5,s[20:23],s47 offen offset:3072 nt lds
v_mfma_f32_16x16x32_bf16 v[156:159],a[180:183],v[68:71],v[156:159]
s_waitcnt lgkmcnt(0)
v_exp_f32 v249,v249
v_mfma_f32_16x16x32_bf16 v[156:159],a[176:179],v[64:67],v[156:159]
v_exp_f32 v250,v250
v_mfma_f32_16x16x32_bf16 a[64:67],a[192:195],v[200:203],a[64:67]
v_exp_f32 v251,v251
v_mfma_f32_16x16x32_bf16 a[64:67],a[196:199],v[204:207],a[64:67]
v_exp_f32 v252,v252
v_mfma_f32_16x16x32_bf16 a[68:71],a[204:207],v[204:207],a[68:71]
v_exp_f32 v253,v253
v_mfma_f32_16x16x32_bf16 a[68:71],a[200:203],v[200:203],a[68:71]
v_exp_f32 v254,v254
v_mfma_f32_16x16x32_bf16 a[72:75],a[208:211],v[200:203],a[72:75]
v_exp_f32 v255,v255
v_mfma_f32_16x16x32_bf16 a[72:75],a[212:215],v[204:207],a[72:75]
v_exp_f32 v136,v136
v_mfma_f32_16x16x32_bf16 a[76:79],a[220:223],v[204:207],a[76:79]
v_exp_f32 v137,v137
v_mfma_f32_16x16x32_bf16 a[76:79],a[216:219],v[200:203],a[76:79]
v_exp_f32 v138,v138
v_mfma_f32_16x16x32_bf16 a[80:83],a[224:227],v[200:203],a[80:83]
s_add_u32 m0,s49,s52
v_cvt_pk_bf16_f32 v232,v232,v233
v_cvt_pk_bf16_f32 v233,v234,v235
v_mfma_f32_16x16x32_bf16 a[80:83],a[228:231],v[204:207],a[80:83]
s_xor_b32 s49,s50,65536
v_cvt_pk_bf16_f32 v234,v236,v237
v_cvt_pk_bf16_f32 v235,v238,v239
v_mfma_f32_16x16x32_bf16 a[84:87],a[236:239],v[204:207],a[84:87]
v_cvt_pk_bf16_f32 v236,v248,v249
v_cvt_pk_bf16_f32 v237,v250,v251
v_mfma_f32_16x16x32_bf16 a[84:87],a[232:235],v[200:203],a[84:87]
buffer_load_dwordx4 v4,s[24:27],s47 offen nt lds
v_mfma_f32_16x16x32_bf16 a[88:91],a[240:243],v[200:203],a[88:91]
v_cvt_pk_bf16_f32 v238,v252,v253
v_cvt_pk_bf16_f32 v239,v254,v255
v_mfma_f32_16x16x32_bf16 a[88:91],a[244:247],v[204:207],a[88:91]
v_exp_f32 v139,v139
v_mfma_f32_16x16x32_bf16 a[92:95],a[252:255],v[204:207],a[92:95]
v_exp_f32 v140,v140
v_mfma_f32_16x16x32_bf16 a[92:95],a[248:251],v[200:203],a[92:95]
v_exp_f32 v141,v141
v_smfmac_f32_16x16x64_bf16 v[56:59],v[28:31],v[200:207],v27
v_exp_f32 v142,v142
v_exp_f32 v143,v143
v_exp_f32 v152,v152
v_exp_f32 v153,v153
v_exp_f32 v154,v154
v_exp_f32 v155,v155
v_exp_f32 v156,v156
v_exp_f32 v157,v157
buffer_load_dwordx4 v5,s[24:27],s47 offen offset:1024 nt lds
v_exp_f32 v158,v158
v_exp_f32 v159,v159
v_mfma_f32_16x16x32_bf16 a[96:99],a[192:195],v[232:235],a[96:99]
ds_read_b64_tr_b16 a[192:193],v2
v_mfma_f32_16x16x32_bf16 a[96:99],a[196:199],v[236:239],a[96:99]
ds_read_b64_tr_b16 a[194:195],v2 offset:4096
v_mfma_f32_16x16x32_bf16 a[100:103],a[204:207],v[236:239],a[100:103]
v_mfma_f32_16x16x32_bf16 a[100:103],a[200:203],v[232:235],a[100:103]
ds_read_b64_tr_b16 a[196:197],v2 offset:8192
v_mfma_f32_16x16x32_bf16 a[104:107],a[208:211],v[232:235],a[104:107]
v_mfma_f32_16x16x32_bf16 a[104:107],a[212:215],v[236:239],a[104:107]
v_mfma_f32_16x16x32_bf16 a[108:111],a[220:223],v[236:239],a[108:111]
buffer_load_dwordx4 v4,s[24:27],s47 offen offset:2048 nt lds
v_mfma_f32_16x16x32_bf16 a[108:111],a[216:219],v[232:235],a[108:111]
ds_read_b64_tr_b16 a[198:199],v2 offset:12288
v_mfma_f32_16x16x32_bf16 a[112:115],a[224:227],v[232:235],a[112:115]
v_mfma_f32_16x16x32_bf16 a[112:115],a[228:231],v[236:239],a[112:115]
v_mfma_f32_16x16x32_bf16 a[116:119],a[236:239],v[236:239],a[116:119]
ds_read_b64_tr_b16 a[204:205],v3 offset:8192
v_cvt_pk_bf16_f32 v136,v136,v137
v_mfma_f32_16x16x32_bf16 a[116:119],a[232:235],v[232:235],a[116:119]
ds_read_b64_tr_b16 a[206:207],v3 offset:12288
v_mfma_f32_16x16x32_bf16 a[120:123],a[240:243],v[232:235],a[120:123]
ds_read_b64_tr_b16 a[200:201],v3
v_mfma_f32_16x16x32_bf16 a[120:123],a[244:247],v[236:239],a[120:123]
ds_read_b64_tr_b16 a[202:203],v3 offset:4096
v_cvt_pk_bf16_f32 v137,v138,v139
v_mfma_f32_16x16x32_bf16 a[124:127],a[252:255],v[236:239],a[124:127]
ds_read_b64_tr_b16 a[208:209],v2 offset:256
v_mfma_f32_16x16x32_bf16 a[124:127],a[248:251],v[232:235],a[124:127]
ds_read_b64_tr_b16 a[210:211],v2 offset:4352
v_smfmac_f32_16x16x64_bf16 v[60:63],v[28:31],v[232:239],v27
ds_read_b64_tr_b16 a[212:213],v2 offset:8448
v_mfma_f32_16x16x32_bf16 v[200:203],a[128:131],v[96:99],v[40:43]
ds_read_b64_tr_b16 a[214:215],v2 offset:12544
v_cvt_pk_bf16_f32 v138,v140,v141
v_mfma_f32_16x16x32_bf16 v[200:203],a[132:135],v[100:103],v[200:203]
ds_read_b64_tr_b16 a[220:221],v3 offset:8448
v_mfma_f32_16x16x32_bf16 v[200:203],a[136:139],v[104:107],v[200:203]
ds_read_b64_tr_b16 a[222:223],v3 offset:12544
v_cvt_pk_bf16_f32 v139,v142,v143
v_mfma_f32_16x16x32_bf16 v[200:203],a[140:143],v[108:111],v[200:203]
buffer_load_dwordx4 v5,s[24:27],s47 offen offset:3072 nt lds
v_mfma_f32_16x16x32_bf16 v[204:207],a[156:159],v[108:111],v[40:43]
s_add_u32 s47,s47,16384
s_and_b32 s47,s47,0x3ffff
ds_read_b64_tr_b16 a[216:217],v3 offset:256
v_mfma_f32_16x16x32_bf16 v[204:207],a[152:155],v[104:107],v[204:207]
s_or_b32 s47,s47,s46
ds_read_b64_tr_b16 a[218:219],v3 offset:4352
v_cvt_pk_bf16_f32 v140,v152,v153
v_mfma_f32_16x16x32_bf16 v[204:207],a[148:151],v[100:103],v[204:207]
ds_read_b64_tr_b16 a[224:225],v2 offset:512
v_mfma_f32_16x16x32_bf16 v[204:207],a[144:147],v[96:99],v[204:207]
ds_read_b64_tr_b16 a[226:227],v2 offset:4608
v_mfma_f32_16x16x32_bf16 v[216:219],a[160:163],v[96:99],v[40:43]
ds_read_b64_tr_b16 a[228:229],v2 offset:8704
v_cvt_pk_bf16_f32 v141,v154,v155
v_mfma_f32_16x16x32_bf16 v[216:219],a[164:167],v[100:103],v[216:219]
ds_read_b64_tr_b16 a[230:231],v2 offset:12800
v_mfma_f32_16x16x32_bf16 v[216:219],a[168:171],v[104:107],v[216:219]
ds_read_b64_tr_b16 a[236:237],v3 offset:8704
v_mfma_f32_16x16x32_bf16 v[216:219],a[172:175],v[108:111],v[216:219]
ds_read_b64_tr_b16 a[238:239],v3 offset:12800
v_cvt_pk_bf16_f32 v142,v156,v157
v_mfma_f32_16x16x32_bf16 v[220:223],a[188:191],v[108:111],v[40:43]
ds_read_b64_tr_b16 a[232:233],v3 offset:512
v_mfma_f32_16x16x32_bf16 v[220:223],a[184:187],v[104:107],v[220:223]
ds_read_b64_tr_b16 a[234:235],v3 offset:4608
v_mfma_f32_16x16x32_bf16 v[220:223],a[180:183],v[100:103],v[220:223]
ds_read_b64_tr_b16 a[240:241],v2 offset:768
v_cvt_pk_bf16_f32 v143,v158,v159
v_mfma_f32_16x16x32_bf16 v[220:223],a[176:179],v[96:99],v[220:223]
s_waitcnt lgkmcnt(7)
buffer_load_dword v26,v24,s[88:91],s93 offen
v_mfma_f32_16x16x32_bf16 a[0:3],a[192:195],v[136:139],a[0:3]
ds_read_b64_tr_b16 a[242:243],v2 offset:4864
v_mfma_f32_16x16x32_bf16 a[0:3],a[196:199],v[140:143],a[0:3]
ds_read_b64_tr_b16 a[244:245],v2 offset:8960
v_mfma_f32_16x16x32_bf16 a[4:7],a[204:207],v[140:143],a[4:7]
ds_read_b64_tr_b16 a[246:247],v2 offset:13056
v_mfma_f32_16x16x32_bf16 a[4:7],a[200:203],v[136:139],a[4:7]
ds_read_b64_tr_b16 a[252:253],v3 offset:8960
v_mfma_f32_16x16x32_bf16 a[8:11],a[208:211],v[136:139],a[8:11]
ds_read_b64_tr_b16 a[254:255],v3 offset:13056
v_mfma_f32_16x16x32_bf16 a[8:11],a[212:215],v[140:143],a[8:11]
ds_read_b64_tr_b16 a[248:249],v3 offset:768
v_mfma_f32_16x16x32_bf16 a[12:15],a[220:223],v[140:143],a[12:15]
ds_read_b64_tr_b16 a[250:251],v3 offset:4864
v_mfma_f32_16x16x32_bf16 a[12:15],a[216:219],v[136:139],a[12:15]
s_bitcmp1_b32 s45,0
s_cbranch_scc0 L_t31v10_%=
s_mov_b32 m0,s80
s_add_u32 s80,s80,4096
buffer_load_dwordx4 v25,s[20:23],s83 offen nt lds
s_add_u32 s83,s83,4096
L_t31v10_%=:
v_mfma_f32_16x16x32_bf16 a[16:19],a[224:227],v[136:139],a[16:19]
s_waitcnt lgkmcnt(6)
v_mfma_f32_16x16x32_bf16 a[16:19],a[228:231],v[140:143],a[16:19]
v_mfma_f32_16x16x32_bf16 a[20:23],a[236:239],v[140:143],a[20:23]
v_mfma_f32_16x16x32_bf16 a[20:23],a[232:235],v[136:139],a[20:23]
v_mfma_f32_16x16x32_bf16 a[24:27],a[240:243],v[136:139],a[24:27]
s_waitcnt lgkmcnt(2)
v_mfma_f32_16x16x32_bf16 a[24:27],a[244:247],v[140:143],a[24:27]
v_mfma_f32_16x16x32_bf16 a[28:31],a[252:255],v[140:143],a[28:31]
s_waitcnt lgkmcnt(0)
v_mfma_f32_16x16x32_bf16 a[28:31],a[248:251],v[136:139],a[28:31]
s_cmp_lg_u32 s53,0
v_smfmac_f32_16x16x64_bf16 v[48:51],v[28:31],v[136:143],v27
s_cbranch_scc1 L_att0v1_%=
s_waitcnt lgkmcnt(0)
s_nop 7
s_branch L_att0x_%=
.p2align 5
L_att0v2_%=:
v_mfma_f32_16x16x32_bf16 v[232:235],a[128:131],v[112:115],v[44:47]
s_waitcnt vmcnt(8) lgkmcnt(0)
v_mfma_f32_16x16x32_bf16 v[232:235],a[132:135],v[116:119],v[232:235]
s_barrier
s_add_u32 m0,s49,s51
ds_read_b128 a[128:131],v1
v_mfma_f32_16x16x32_bf16 v[232:235],a[136:139],v[120:123],v[232:235]
s_mov_b32 s90,L_endseg_%=-L_pc_%=+128
v_mfma_f32_16x16x32_bf16 v[232:235],a[140:143],v[124:127],v[232:235]
s_lshr_b32 s45,s45,1
ds_read_b128 a[132:135],v1 offset:256
v_mfma_f32_16x16x32_bf16 v[236:239],a[156:159],v[124:127],v[44:47]
buffer_load_dwordx4 v4,s[20:23],s47 offen nt lds
v_mfma_f32_16x16x32_bf16 v[236:239],a[152:155],v[120:123],v[236:239]
ds_read_b128 a[136:139],v1 offset:512
v_add_u32 v2,s48,v2
v_mfma_f32_16x16x32_bf16 v[236:239],a[148:151],v[116:119],v[236:239]
v_mfma_f32_16x16x32_bf16 v[236:239],a[144:147],v[112:115],v[236:239]
s_add_u32 s62,s50,32768
ds_read_b128 a[140:143],v1 offset:768
v_add_u32 v3,s48,v3
v_mfma_f32_16x16x32_bf16 v[248:251],a[160:163],v[112:115],v[44:47]
s_and_b32 s62,s62,131071
v_mfma_f32_16x16x32_bf16 v[248:251],a[164:167],v[116:119],v[248:251]
s_add_u32 s54,s54,16384
ds_read_b128 a[156:159],v1 offset:4864
v_mfma_f32_16x16x32_bf16 v[248:251],a[168:171],v[120:123],v[248:251]
s_sub_u32 s48,s62,s50
v_mfma_f32_16x16x32_bf16 v[248:251],a[172:175],v[124:127],v[248:251]
s_sub_u32 s53,s53,1
v_mfma_f32_16x16x32_bf16 v[252:255],a[188:191],v[124:127],v[44:47]
s_and_b32 s46,s54,0xfffc0000
ds_read_b128 a[152:155],v1 offset:4608
v_mfma_f32_16x16x32_bf16 v[252:255],a[184:187],v[120:123],v[252:255]
s_mov_b32 s50,s62
v_mfma_f32_16x16x32_bf16 v[252:255],a[180:183],v[116:119],v[252:255]
v_mfma_f32_16x16x32_bf16 v[252:255],a[176:179],v[112:115],v[252:255]
ds_read_b128 a[148:151],v1 offset:4352
v_mfma_f32_16x16x32_bf16 a[32:35],a[192:195],v[168:171],a[32:35]
v_mfma_f32_16x16x32_bf16 a[32:35],a[196:199],v[172:175],a[32:35]
ds_read_b128 a[144:147],v1 offset:4096
v_mfma_f32_16x16x32_bf16 a[36:39],a[204:207],v[172:175],a[36:39]
buffer_load_dwordx4 v4,s[20:23],s47 offen offset:1024 nt lds
v_mfma_f32_16x16x32_bf16 a[36:39],a[200:203],v[168:171],a[36:39]
ds_read_b128 a[160:163],v1 offset:8192
v_mfma_f32_16x16x32_bf16 a[40:43],a[208:211],v[168:171],a[40:43]
v_mfma_f32_16x16x32_bf16 a[40:43],a[212:215],v[172:175],a[40:43]
ds_read_b128 a[164:167],v1 offset:8448
v_mfma_f32_16x16x32_bf16 a[44:47],a[220:223],v[172:175],a[44:47]
v_mfma_f32_16x16x32_bf16 a[44:47],a[216:219],v[168:171],a[44:47]
ds_read_b128 a[168:171],v1 offset:8704
v_mfma_f32_16x16x32_bf16 a[48:51],a[224:227],v[168:171],a[48:51]
v_mfma_f32_16x16x32_bf16 a[48:51],a[228:231],v[172:175],a[48:51]
ds_read_b128 a[172:175],v1 offset:8960
v_mfma_f32_16x16x32_bf16 a[52:55],a[236:239],v[172:175],a[52:55]
v_mfma_f32_16x16x32_bf16 a[52:55],a[232:235],v[168:171],a[52:55]
ds_read_b128 a[188:191],v1 offset:13056
v_mfma_f32_16x16x32_bf16 a[56:59],a[240:243],v[168:171],a[56:59]
v_mfma_f32_16x16x32_bf16 a[56:59],a[244:247],v[172:175],a[56:59]
ds_read_b128 a[184:187],v1 offset:12800
v_mfma_f32_16x16x32_bf16 a[60:63],a[252:255],v[172:175],a[60:63]
v_mfma_f32_16x16x32_bf16 a[60:63],a[248:251],v[168:171],a[60:63]
ds_read_b128 a[180:183],v1 offset:12544
v_smfmac_f32_16x16x64_bf16 v[52:55],v[28:31],v[168:175],v27
s_waitcnt lgkmcnt(4)
v_mfma_f32_16x16x32_bf16 v[136:139],a[128:131],v[64:67],v[32:35]
buffer_load_dwordx4 v5,s[20:23],s47 offen offset:2048 nt lds
v_mfma_f32_16x16x32_bf16 v[136:139],a[132:135],v[68:71],v[136:139]
ds_read_b128 a[176:179],v1 offset:12288
v_add_u32 v1,s48,v1
v_mfma_f32_16x16x32_bf16 v[136:139],a[136:139],v[72:75],v[136:139]
v_mfma_f32_16x16x32_bf16 v[136:139],a[140:143],v[76:79],v[136:139]
v_mfma_f32_16x16x32_bf16 v[140:143],a[156:159],v[76:79],v[32:35]
v_exp_f32 v232,v232
v_mfma_f32_16x16x32_bf16 v[140:143],a[152:155],v[72:75],v[140:143]
v_exp_f32 v233,v233
v_mfma_f32_16x16x32_bf16 v[140:143],a[148:151],v[68:71],v[140:143]
v_exp_f32 v234,v234
v_mfma_f32_16x16x32_bf16 v[140:143],a[144:147],v[64:67],v[140:143]
)A"
R"A(v_exp_f32 v235,v235
v_mfma_f32_16x16x32_bf16 v[152:155],a[160:163],v[64:67],v[32:35]
v_exp_f32 v236,v236
v_mfma_f32_16x16x32_bf16 v[152:155],a[164:167],v[68:71],v[152:155]
v_exp_f32 v237,v237
v_mfma_f32_16x16x32_bf16 v[152:155],a[168:171],v[72:75],v[152:155]
s_waitcnt lgkmcnt(0)
v_exp_f32 v238,v238
v_mfma_f32_16x16x32_bf16 v[152:155],a[172:175],v[76:79],v[152:155]
v_exp_f32 v239,v239
v_mfma_f32_16x16x32_bf16 v[156:159],a[188:191],v[76:79],v[32:35]
v_exp_f32 v248,v248
v_mfma_f32_16x16x32_bf16 v[156:159],a[184:187],v[72:75],v[156:159]
buffer_load_dwordx4 v5,s[20:23],s47 offen offset:3072 nt lds
v_mfma_f32_16x16x32_bf16 v[156:159],a[180:183],v[68:71],v[156:159]
v_exp_f32 v249,v249
v_mfma_f32_16x16x32_bf16 v[156:159],a[176:179],v[64:67],v[156:159]
v_exp_f32 v250,v250
v_exp_f32 v251,v251
v_exp_f32 v252,v252
v_exp_f32 v253,v253
v_exp_f32 v254,v254
v_exp_f32 v255,v255
v_exp_f32 v136,v136
v_exp_f32 v137,v137
v_exp_f32 v138,v138
s_add_u32 m0,s49,s52
v_cvt_pk_bf16_f32 v232,v232,v233
v_cvt_pk_bf16_f32 v233,v234,v235
s_xor_b32 s49,s50,65536
v_cvt_pk_bf16_f32 v234,v236,v237
v_cvt_pk_bf16_f32 v235,v238,v239
v_cvt_pk_bf16_f32 v236,v248,v249
v_cvt_pk_bf16_f32 v237,v250,v251
buffer_load_dwordx4 v4,s[24:27],s47 offen nt lds
v_cvt_pk_bf16_f32 v238,v252,v253
v_cvt_pk_bf16_f32 v239,v254,v255
v_exp_f32 v139,v139
v_exp_f32 v140,v140
v_exp_f32 v141,v141
v_mfma_f32_16x16x32_bf16 v[168:171],a[128:131],v[80:83],v[36:39]
v_exp_f32 v142,v142
v_mfma_f32_16x16x32_bf16 v[168:171],a[132:135],v[84:87],v[168:171]
v_exp_f32 v143,v143
v_mfma_f32_16x16x32_bf16 v[168:171],a[136:139],v[88:91],v[168:171]
v_exp_f32 v152,v152
v_mfma_f32_16x16x32_bf16 v[168:171],a[140:143],v[92:95],v[168:171]
v_exp_f32 v153,v153
v_mfma_f32_16x16x32_bf16 v[172:175],a[156:159],v[92:95],v[36:39]
v_exp_f32 v154,v154
v_mfma_f32_16x16x32_bf16 v[172:175],a[152:155],v[88:91],v[172:175]
v_exp_f32 v155,v155
v_mfma_f32_16x16x32_bf16 v[172:175],a[148:151],v[84:87],v[172:175]
v_exp_f32 v156,v156
v_mfma_f32_16x16x32_bf16 v[172:175],a[144:147],v[80:83],v[172:175]
v_exp_f32 v157,v157
v_mfma_f32_16x16x32_bf16 v[184:187],a[160:163],v[80:83],v[36:39]
buffer_load_dwordx4 v5,s[24:27],s47 offen offset:1024 nt lds
v_mfma_f32_16x16x32_bf16 v[184:187],a[164:167],v[84:87],v[184:187]
v_exp_f32 v158,v158
v_mfma_f32_16x16x32_bf16 v[184:187],a[168:171],v[88:91],v[184:187]
v_exp_f32 v159,v159
v_mfma_f32_16x16x32_bf16 v[184:187],a[172:175],v[92:95],v[184:187]
v_exp_f32 v168,v168
v_mfma_f32_16x16x32_bf16 v[188:191],a[188:191],v[92:95],v[36:39]
v_exp_f32 v169,v169
v_mfma_f32_16x16x32_bf16 v[188:191],a[184:187],v[88:91],v[188:191]
v_exp_f32 v170,v170
v_mfma_f32_16x16x32_bf16 v[188:191],a[180:183],v[84:87],v[188:191]
v_exp_f32 v171,v171
v_mfma_f32_16x16x32_bf16 v[188:191],a[176:179],v[80:83],v[188:191]
v_exp_f32 v172,v172
v_mfma_f32_16x16x32_bf16 a[96:99],a[192:195],v[232:235],a[96:99]
ds_read_b64_tr_b16 a[192:193],v2
v_cvt_pk_bf16_f32 v168,v168,v169
v_mfma_f32_16x16x32_bf16 a[96:99],a[196:199],v[236:239],a[96:99]
ds_read_b64_tr_b16 a[194:195],v2 offset:4096
v_cvt_pk_bf16_f32 v169,v170,v171
v_mfma_f32_16x16x32_bf16 a[100:103],a[204:207],v[236:239],a[100:103]
v_exp_f32 v173,v173
v_mfma_f32_16x16x32_bf16 a[100:103],a[200:203],v[232:235],a[100:103]
ds_read_b64_tr_b16 a[196:197],v2 offset:8192
v_cvt_pk_bf16_f32 v170,v172,v173
v_mfma_f32_16x16x32_bf16 a[104:107],a[208:211],v[232:235],a[104:107]
v_exp_f32 v174,v174
v_mfma_f32_16x16x32_bf16 a[104:107],a[212:215],v[236:239],a[104:107]
v_exp_f32 v175,v175
v_mfma_f32_16x16x32_bf16 a[108:111],a[220:223],v[236:239],a[108:111]
buffer_load_dwordx4 v4,s[24:27],s47 offen offset:2048 nt lds
v_mfma_f32_16x16x32_bf16 a[108:111],a[216:219],v[232:235],a[108:111]
ds_read_b64_tr_b16 a[198:199],v2 offset:12288
v_cvt_pk_bf16_f32 v171,v174,v175
v_mfma_f32_16x16x32_bf16 a[112:115],a[224:227],v[232:235],a[112:115]
v_exp_f32 v184,v184
v_mfma_f32_16x16x32_bf16 a[112:115],a[228:231],v[236:239],a[112:115]
v_exp_f32 v185,v185
v_mfma_f32_16x16x32_bf16 a[116:119],a[236:239],v[236:239],a[116:119]
ds_read_b64_tr_b16 a[204:205],v3 offset:8192
v_cvt_pk_bf16_f32 v136,v136,v137
v_mfma_f32_16x16x32_bf16 a[116:119],a[232:235],v[232:235],a[116:119]
ds_read_b64_tr_b16 a[206:207],v3 offset:12288
v_cvt_pk_bf16_f32 v172,v184,v185
v_mfma_f32_16x16x32_bf16 a[120:123],a[240:243],v[232:235],a[120:123]
ds_read_b64_tr_b16 a[200:201],v3
v_mfma_f32_16x16x32_bf16 a[120:123],a[244:247],v[236:239],a[120:123]
ds_read_b64_tr_b16 a[202:203],v3 offset:4096
v_cvt_pk_bf16_f32 v137,v138,v139
v_mfma_f32_16x16x32_bf16 a[124:127],a[252:255],v[236:239],a[124:127]
ds_read_b64_tr_b16 a[208:209],v2 offset:256
v_mfma_f32_16x16x32_bf16 a[124:127],a[248:251],v[232:235],a[124:127]
ds_read_b64_tr_b16 a[210:211],v2 offset:4352
v_smfmac_f32_16x16x64_bf16 v[60:63],v[28:31],v[232:239],v27
ds_read_b64_tr_b16 a[212:213],v2 offset:8448
ds_read_b64_tr_b16 a[214:215],v2 offset:12544
v_cvt_pk_bf16_f32 v138,v140,v141
ds_read_b64_tr_b16 a[220:221],v3 offset:8448
ds_read_b64_tr_b16 a[222:223],v3 offset:12544
v_cvt_pk_bf16_f32 v139,v142,v143
buffer_load_dwordx4 v5,s[24:27],s47 offen offset:3072 nt lds
s_add_u32 s47,s47,16384
s_and_b32 s47,s47,0x3ffff
ds_read_b64_tr_b16 a[216:217],v3 offset:256
s_or_b32 s47,s47,s46
ds_read_b64_tr_b16 a[218:219],v3 offset:4352
v_cvt_pk_bf16_f32 v140,v152,v153
ds_read_b64_tr_b16 a[224:225],v2 offset:512
ds_read_b64_tr_b16 a[226:227],v2 offset:4608
ds_read_b64_tr_b16 a[228:229],v2 offset:8704
v_cvt_pk_bf16_f32 v141,v154,v155
ds_read_b64_tr_b16 a[230:231],v2 offset:12800
ds_read_b64_tr_b16 a[236:237],v3 offset:8704
ds_read_b64_tr_b16 a[238:239],v3 offset:12800
v_cvt_pk_bf16_f32 v142,v156,v157
ds_read_b64_tr_b16 a[232:233],v3 offset:512
ds_read_b64_tr_b16 a[234:235],v3 offset:4608
ds_read_b64_tr_b16 a[240:241],v2 offset:768
v_cvt_pk_bf16_f32 v143,v158,v159
buffer_load_dword v26,v24,s[88:91],s93 offen
s_waitcnt lgkmcnt(15)
v_mfma_f32_16x16x32_bf16 a[0:3],a[192:195],v[136:139],a[0:3]
ds_read_b64_tr_b16 a[242:243],v2 offset:4864
v_mfma_f32_16x16x32_bf16 a[0:3],a[196:199],v[140:143],a[0:3]
ds_read_b64_tr_b16 a[244:245],v2 offset:8960
v_mfma_f32_16x16x32_bf16 a[4:7],a[204:207],v[140:143],a[4:7]
ds_read_b64_tr_b16 a[246:247],v2 offset:13056
v_mfma_f32_16x16x32_bf16 a[4:7],a[200:203],v[136:139],a[4:7]
ds_read_b64_tr_b16 a[252:253],v3 offset:8960
v_mfma_f32_16x16x32_bf16 a[8:11],a[208:211],v[136:139],a[8:11]
s_waitcnt lgkmcnt(15)
ds_read_b64_tr_b16 a[254:255],v3 offset:13056
v_mfma_f32_16x16x32_bf16 a[8:11],a[212:215],v[140:143],a[8:11]
ds_read_b64_tr_b16 a[248:249],v3 offset:768
v_mfma_f32_16x16x32_bf16 a[12:15],a[220:223],v[140:143],a[12:15]
s_waitcnt lgkmcnt(15)
ds_read_b64_tr_b16 a[250:251],v3 offset:4864
v_mfma_f32_16x16x32_bf16 a[12:15],a[216:219],v[136:139],a[12:15]
s_bitcmp1_b32 s45,0
s_cbranch_scc0 L_t31v20_%=
s_mov_b32 m0,s80
s_add_u32 s80,s80,4096
buffer_load_dwordx4 v25,s[20:23],s83 offen nt lds
s_add_u32 s83,s83,4096
L_t31v20_%=:
s_waitcnt lgkmcnt(7)
v_mfma_f32_16x16x32_bf16 a[16:19],a[224:227],v[136:139],a[16:19]
v_exp_f32 v186,v186
v_mfma_f32_16x16x32_bf16 a[16:19],a[228:231],v[140:143],a[16:19]
v_exp_f32 v187,v187
v_mfma_f32_16x16x32_bf16 a[20:23],a[236:239],v[140:143],a[20:23]
v_exp_f32 v188,v188
v_mfma_f32_16x16x32_bf16 a[20:23],a[232:235],v[136:139],a[20:23]
s_waitcnt lgkmcnt(3)
v_exp_f32 v189,v189
v_mfma_f32_16x16x32_bf16 a[24:27],a[240:243],v[136:139],a[24:27]
v_exp_f32 v190,v190
v_mfma_f32_16x16x32_bf16 a[24:27],a[244:247],v[140:143],a[24:27]
s_waitcnt lgkmcnt(1)
v_cvt_pk_bf16_f32 v173,v186,v187
v_mfma_f32_16x16x32_bf16 a[28:31],a[252:255],v[140:143],a[28:31]
s_waitcnt lgkmcnt(0)
v_exp_f32 v191,v191
v_mfma_f32_16x16x32_bf16 a[28:31],a[248:251],v[136:139],a[28:31]
v_cvt_pk_bf16_f32 v174,v188,v189
v_cvt_pk_bf16_f32 v175,v190,v191
s_cmp_lg_u32 s53,0
v_smfmac_f32_16x16x64_bf16 v[48:51],v[28:31],v[136:143],v27
s_cbranch_scc1 L_att0v2_%=
s_waitcnt lgkmcnt(0)
s_nop 7
s_branch L_att0x_%=
.p2align 5
L_att0_%=:
v_mfma_f32_16x16x32_bf16 v[232:235],a[128:131],v[112:115],v[44:47]
s_waitcnt vmcnt(8) lgkmcnt(0)
v_exp_f32 v200,v200
v_mfma_f32_16x16x32_bf16 v[232:235],a[132:135],v[116:119],v[232:235]
s_barrier
s_add_u32 m0,s49,s51
ds_read_b128 a[128:131],v1
v_mfma_f32_16x16x32_bf16 v[232:235],a[136:139],v[120:123],v[232:235]
s_mov_b32 s90,L_endseg_%=-L_pc_%=+128
v_exp_f32 v201,v201
v_mfma_f32_16x16x32_bf16 v[232:235],a[140:143],v[124:127],v[232:235]
s_lshr_b32 s45,s45,1
ds_read_b128 a[132:135],v1 offset:256
v_cvt_pk_bf16_f32 v200,v200,v201
v_mfma_f32_16x16x32_bf16 v[236:239],a[156:159],v[124:127],v[44:47]
s_sub_u32 s94,s94,3
buffer_load_dwordx4 v4,s[20:23],s47 offen nt lds
v_mfma_f32_16x16x32_bf16 v[236:239],a[152:155],v[120:123],v[236:239]
s_lshr_b32 s60,s94,2
ds_read_b128 a[136:139],v1 offset:512
v_add_u32 v2,s48,v2
v_mfma_f32_16x16x32_bf16 v[236:239],a[148:151],v[116:119],v[236:239]
s_max_i32 s60,s60,128
v_exp_f32 v202,v202
v_mfma_f32_16x16x32_bf16 v[236:239],a[144:147],v[112:115],v[236:239]
s_add_u32 s62,s50,32768
ds_read_b128 a[140:143],v1 offset:768
v_add_u32 v3,s48,v3
v_mfma_f32_16x16x32_bf16 v[248:251],a[160:163],v[112:115],v[44:47]
s_and_b32 s62,s62,131071
v_exp_f32 v203,v203
v_mfma_f32_16x16x32_bf16 v[248:251],a[164:167],v[116:119],v[248:251]
s_add_u32 s54,s54,16384
ds_read_b128 a[156:159],v1 offset:4864
v_cvt_pk_bf16_f32 v201,v202,v203
v_mfma_f32_16x16x32_bf16 v[248:251],a[168:171],v[120:123],v[248:251]
s_sub_u32 s48,s62,s50
v_exp_f32 v204,v204
v_mfma_f32_16x16x32_bf16 v[248:251],a[172:175],v[124:127],v[248:251]
s_sub_u32 s53,s53,1
v_exp_f32 v205,v205
v_mfma_f32_16x16x32_bf16 v[252:255],a[188:191],v[124:127],v[44:47]
s_and_b32 s46,s54,0xfffc0000
ds_read_b128 a[152:155],v1 offset:4608
v_cvt_pk_bf16_f32 v202,v204,v205
v_mfma_f32_16x16x32_bf16 v[252:255],a[184:187],v[120:123],v[252:255]
s_mov_b32 s50,s62
v_exp_f32 v206,v206
v_mfma_f32_16x16x32_bf16 v[252:255],a[180:183],v[116:119],v[252:255]
v_exp_f32 v207,v207
v_mfma_f32_16x16x32_bf16 v[252:255],a[176:179],v[112:115],v[252:255]
ds_read_b128 a[148:151],v1 offset:4352
v_cvt_pk_bf16_f32 v203,v206,v207
v_mfma_f32_16x16x32_bf16 a[32:35],a[192:195],v[168:171],a[32:35]
v_exp_f32 v216,v216
v_mfma_f32_16x16x32_bf16 a[32:35],a[196:199],v[172:175],a[32:35]
ds_read_b128 a[144:147],v1 offset:4096
v_mfma_f32_16x16x32_bf16 a[36:39],a[204:207],v[172:175],a[36:39]
buffer_load_dwordx4 v4,s[20:23],s47 offen offset:1024 nt lds
v_mfma_f32_16x16x32_bf16 a[36:39],a[200:203],v[168:171],a[36:39]
ds_read_b128 a[160:163],v1 offset:8192
v_mfma_f32_16x16x32_bf16 a[40:43],a[208:211],v[168:171],a[40:43]
v_exp_f32 v217,v217
v_mfma_f32_16x16x32_bf16 a[40:43],a[212:215],v[172:175],a[40:43]
ds_read_b128 a[164:167],v1 offset:8448
v_cvt_pk_bf16_f32 v204,v216,v217
v_mfma_f32_16x16x32_bf16 a[44:47],a[220:223],v[172:175],a[44:47]
v_exp_f32 v218,v218
v_mfma_f32_16x16x32_bf16 a[44:47],a[216:219],v[168:171],a[44:47]
ds_read_b128 a[168:171],v1 offset:8704
v_mfma_f32_16x16x32_bf16 a[48:51],a[224:227],v[168:171],a[48:51]
v_exp_f32 v219,v219
v_mfma_f32_16x16x32_bf16 a[48:51],a[228:231],v[172:175],a[48:51]
ds_read_b128 a[172:175],v1 offset:8960
v_cvt_pk_bf16_f32 v205,v218,v219
v_mfma_f32_16x16x32_bf16 a[52:55],a[236:239],v[172:175],a[52:55]
v_exp_f32 v220,v220
v_mfma_f32_16x16x32_bf16 a[52:55],a[232:235],v[168:171],a[52:55]
ds_read_b128 a[188:191],v1 offset:13056
v_mfma_f32_16x16x32_bf16 a[56:59],a[240:243],v[168:171],a[56:59]
v_exp_f32 v221,v221
v_mfma_f32_16x16x32_bf16 a[56:59],a[244:247],v[172:175],a[56:59]
ds_read_b128 a[184:187],v1 offset:12800
v_cvt_pk_bf16_f32 v206,v220,v221
v_mfma_f32_16x16x32_bf16 a[60:63],a[252:255],v[172:175],a[60:63]
v_exp_f32 v222,v222
v_mfma_f32_16x16x32_bf16 a[60:63],a[248:251],v[168:171],a[60:63]
ds_read_b128 a[180:183],v1 offset:12544
v_smfmac_f32_16x16x64_bf16 v[52:55],v[28:31],v[168:175],v27
s_waitcnt lgkmcnt(4)
v_mfma_f32_16x16x32_bf16 v[136:139],a[128:131],v[64:67],v[32:35]
buffer_load_dwordx4 v5,s[20:23],s47 offen offset:2048 nt lds
v_mfma_f32_16x16x32_bf16 v[136:139],a[132:135],v[68:71],v[136:139]
ds_read_b128 a[176:179],v1 offset:12288
v_add_u32 v1,s48,v1
v_mfma_f32_16x16x32_bf16 v[136:139],a[136:139],v[72:75],v[136:139]
v_exp_f32 v223,v223
)A"
R"A(v_mfma_f32_16x16x32_bf16 v[136:139],a[140:143],v[76:79],v[136:139]
v_cvt_pk_bf16_f32 v207,v222,v223
v_mfma_f32_16x16x32_bf16 v[140:143],a[156:159],v[76:79],v[32:35]
v_exp_f32 v232,v232
v_mfma_f32_16x16x32_bf16 v[140:143],a[152:155],v[72:75],v[140:143]
v_exp_f32 v233,v233
v_mfma_f32_16x16x32_bf16 v[140:143],a[148:151],v[68:71],v[140:143]
v_exp_f32 v234,v234
v_mfma_f32_16x16x32_bf16 v[140:143],a[144:147],v[64:67],v[140:143]
v_exp_f32 v235,v235
v_mfma_f32_16x16x32_bf16 v[152:155],a[160:163],v[64:67],v[32:35]
v_exp_f32 v236,v236
v_mfma_f32_16x16x32_bf16 v[152:155],a[164:167],v[68:71],v[152:155]
v_exp_f32 v237,v237
v_mfma_f32_16x16x32_bf16 v[152:155],a[168:171],v[72:75],v[152:155]
s_waitcnt lgkmcnt(0)
v_exp_f32 v238,v238
v_mfma_f32_16x16x32_bf16 v[152:155],a[172:175],v[76:79],v[152:155]
v_exp_f32 v239,v239
v_mfma_f32_16x16x32_bf16 v[156:159],a[188:191],v[76:79],v[32:35]
v_exp_f32 v248,v248
v_mfma_f32_16x16x32_bf16 v[156:159],a[184:187],v[72:75],v[156:159]
buffer_load_dwordx4 v5,s[20:23],s47 offen offset:3072 nt lds
v_mfma_f32_16x16x32_bf16 v[156:159],a[180:183],v[68:71],v[156:159]
v_exp_f32 v249,v249
v_mfma_f32_16x16x32_bf16 v[156:159],a[176:179],v[64:67],v[156:159]
v_exp_f32 v250,v250
v_mfma_f32_16x16x32_bf16 a[64:67],a[192:195],v[200:203],a[64:67]
v_exp_f32 v251,v251
v_mfma_f32_16x16x32_bf16 a[64:67],a[196:199],v[204:207],a[64:67]
v_exp_f32 v252,v252
v_mfma_f32_16x16x32_bf16 a[68:71],a[204:207],v[204:207],a[68:71]
v_exp_f32 v253,v253
v_mfma_f32_16x16x32_bf16 a[68:71],a[200:203],v[200:203],a[68:71]
v_exp_f32 v254,v254
v_mfma_f32_16x16x32_bf16 a[72:75],a[208:211],v[200:203],a[72:75]
v_exp_f32 v255,v255
v_mfma_f32_16x16x32_bf16 a[72:75],a[212:215],v[204:207],a[72:75]
v_exp_f32 v136,v136
v_mfma_f32_16x16x32_bf16 a[76:79],a[220:223],v[204:207],a[76:79]
v_exp_f32 v137,v137
v_mfma_f32_16x16x32_bf16 a[76:79],a[216:219],v[200:203],a[76:79]
v_exp_f32 v138,v138
v_mfma_f32_16x16x32_bf16 a[80:83],a[224:227],v[200:203],a[80:83]
s_add_u32 m0,s49,s52
v_cvt_pk_bf16_f32 v232,v232,v233
v_cvt_pk_bf16_f32 v233,v234,v235
v_mfma_f32_16x16x32_bf16 a[80:83],a[228:231],v[204:207],a[80:83]
s_xor_b32 s49,s50,65536
v_cvt_pk_bf16_f32 v234,v236,v237
v_cvt_pk_bf16_f32 v235,v238,v239
v_mfma_f32_16x16x32_bf16 a[84:87],a[236:239],v[204:207],a[84:87]
v_cvt_pk_bf16_f32 v236,v248,v249
v_cvt_pk_bf16_f32 v237,v250,v251
v_mfma_f32_16x16x32_bf16 a[84:87],a[232:235],v[200:203],a[84:87]
buffer_load_dwordx4 v4,s[24:27],s47 offen nt lds
v_mfma_f32_16x16x32_bf16 a[88:91],a[240:243],v[200:203],a[88:91]
v_cvt_pk_bf16_f32 v238,v252,v253
v_cvt_pk_bf16_f32 v239,v254,v255
v_mfma_f32_16x16x32_bf16 a[88:91],a[244:247],v[204:207],a[88:91]
v_exp_f32 v139,v139
v_mfma_f32_16x16x32_bf16 a[92:95],a[252:255],v[204:207],a[92:95]
v_exp_f32 v140,v140
v_mfma_f32_16x16x32_bf16 a[92:95],a[248:251],v[200:203],a[92:95]
v_exp_f32 v141,v141
v_smfmac_f32_16x16x64_bf16 v[56:59],v[28:31],v[200:207],v27
v_mfma_f32_16x16x32_bf16 v[168:171],a[128:131],v[80:83],v[36:39]
v_exp_f32 v142,v142
v_mfma_f32_16x16x32_bf16 v[168:171],a[132:135],v[84:87],v[168:171]
v_exp_f32 v143,v143
v_mfma_f32_16x16x32_bf16 v[168:171],a[136:139],v[88:91],v[168:171]
v_exp_f32 v152,v152
v_mfma_f32_16x16x32_bf16 v[168:171],a[140:143],v[92:95],v[168:171]
v_exp_f32 v153,v153
v_mfma_f32_16x16x32_bf16 v[172:175],a[156:159],v[92:95],v[36:39]
v_exp_f32 v154,v154
v_mfma_f32_16x16x32_bf16 v[172:175],a[152:155],v[88:91],v[172:175]
v_exp_f32 v155,v155
v_mfma_f32_16x16x32_bf16 v[172:175],a[148:151],v[84:87],v[172:175]
v_exp_f32 v156,v156
v_mfma_f32_16x16x32_bf16 v[172:175],a[144:147],v[80:83],v[172:175]
v_exp_f32 v157,v157
v_mfma_f32_16x16x32_bf16 v[184:187],a[160:163],v[80:83],v[36:39]
buffer_load_dwordx4 v5,s[24:27],s47 offen offset:1024 nt lds
v_mfma_f32_16x16x32_bf16 v[184:187],a[164:167],v[84:87],v[184:187]
v_exp_f32 v158,v158
v_mfma_f32_16x16x32_bf16 v[184:187],a[168:171],v[88:91],v[184:187]
v_exp_f32 v159,v159
v_mfma_f32_16x16x32_bf16 v[184:187],a[172:175],v[92:95],v[184:187]
v_exp_f32 v168,v168
v_mfma_f32_16x16x32_bf16 v[188:191],a[188:191],v[92:95],v[36:39]
v_exp_f32 v169,v169
v_mfma_f32_16x16x32_bf16 v[188:191],a[184:187],v[88:91],v[188:191]
v_exp_f32 v170,v170
v_mfma_f32_16x16x32_bf16 v[188:191],a[180:183],v[84:87],v[188:191]
v_exp_f32 v171,v171
v_mfma_f32_16x16x32_bf16 v[188:191],a[176:179],v[80:83],v[188:191]
v_exp_f32 v172,v172
v_mfma_f32_16x16x32_bf16 a[96:99],a[192:195],v[232:235],a[96:99]
ds_read_b64_tr_b16 a[192:193],v2
v_cvt_pk_bf16_f32 v168,v168,v169
v_mfma_f32_16x16x32_bf16 a[96:99],a[196:199],v[236:239],a[96:99]
ds_read_b64_tr_b16 a[194:195],v2 offset:4096
v_cvt_pk_bf16_f32 v169,v170,v171
v_mfma_f32_16x16x32_bf16 a[100:103],a[204:207],v[236:239],a[100:103]
v_exp_f32 v173,v173
v_mfma_f32_16x16x32_bf16 a[100:103],a[200:203],v[232:235],a[100:103]
ds_read_b64_tr_b16 a[196:197],v2 offset:8192
v_cvt_pk_bf16_f32 v170,v172,v173
v_mfma_f32_16x16x32_bf16 a[104:107],a[208:211],v[232:235],a[104:107]
v_exp_f32 v174,v174
v_mfma_f32_16x16x32_bf16 a[104:107],a[212:215],v[236:239],a[104:107]
v_exp_f32 v175,v175
v_mfma_f32_16x16x32_bf16 a[108:111],a[220:223],v[236:239],a[108:111]
buffer_load_dwordx4 v4,s[24:27],s47 offen offset:2048 nt lds
v_mfma_f32_16x16x32_bf16 a[108:111],a[216:219],v[232:235],a[108:111]
ds_read_b64_tr_b16 a[198:199],v2 offset:12288
v_cvt_pk_bf16_f32 v171,v174,v175
v_mfma_f32_16x16x32_bf16 a[112:115],a[224:227],v[232:235],a[112:115]
v_exp_f32 v184,v184
v_mfma_f32_16x16x32_bf16 a[112:115],a[228:231],v[236:239],a[112:115]
v_exp_f32 v185,v185
v_mfma_f32_16x16x32_bf16 a[116:119],a[236:239],v[236:239],a[116:119]
ds_read_b64_tr_b16 a[204:205],v3 offset:8192
v_cvt_pk_bf16_f32 v136,v136,v137
v_mfma_f32_16x16x32_bf16 a[116:119],a[232:235],v[232:235],a[116:119]
ds_read_b64_tr_b16 a[206:207],v3 offset:12288
v_cvt_pk_bf16_f32 v172,v184,v185
v_mfma_f32_16x16x32_bf16 a[120:123],a[240:243],v[232:235],a[120:123]
ds_read_b64_tr_b16 a[200:201],v3
v_mfma_f32_16x16x32_bf16 a[120:123],a[244:247],v[236:239],a[120:123]
ds_read_b64_tr_b16 a[202:203],v3 offset:4096
v_cvt_pk_bf16_f32 v137,v138,v139
v_mfma_f32_16x16x32_bf16 a[124:127],a[252:255],v[236:239],a[124:127]
ds_read_b64_tr_b16 a[208:209],v2 offset:256
v_mfma_f32_16x16x32_bf16 a[124:127],a[248:251],v[232:235],a[124:127]
ds_read_b64_tr_b16 a[210:211],v2 offset:4352
v_smfmac_f32_16x16x64_bf16 v[60:63],v[28:31],v[232:239],v27
ds_read_b64_tr_b16 a[212:213],v2 offset:8448
v_mfma_f32_16x16x32_bf16 v[200:203],a[128:131],v[96:99],v[40:43]
ds_read_b64_tr_b16 a[214:215],v2 offset:12544
v_cvt_pk_bf16_f32 v138,v140,v141
v_mfma_f32_16x16x32_bf16 v[200:203],a[132:135],v[100:103],v[200:203]
ds_read_b64_tr_b16 a[220:221],v3 offset:8448
v_mfma_f32_16x16x32_bf16 v[200:203],a[136:139],v[104:107],v[200:203]
ds_read_b64_tr_b16 a[222:223],v3 offset:12544
v_cvt_pk_bf16_f32 v139,v142,v143
v_mfma_f32_16x16x32_bf16 v[200:203],a[140:143],v[108:111],v[200:203]
buffer_load_dwordx4 v5,s[24:27],s47 offen offset:3072 nt lds
v_mfma_f32_16x16x32_bf16 v[204:207],a[156:159],v[108:111],v[40:43]
s_add_u32 s47,s47,16384
s_and_b32 s47,s47,0x3ffff
ds_read_b64_tr_b16 a[216:217],v3 offset:256
v_mfma_f32_16x16x32_bf16 v[204:207],a[152:155],v[104:107],v[204:207]
s_or_b32 s47,s47,s46
ds_read_b64_tr_b16 a[218:219],v3 offset:4352
v_cvt_pk_bf16_f32 v140,v152,v153
v_mfma_f32_16x16x32_bf16 v[204:207],a[148:151],v[100:103],v[204:207]
ds_read_b64_tr_b16 a[224:225],v2 offset:512
v_mfma_f32_16x16x32_bf16 v[204:207],a[144:147],v[96:99],v[204:207]
ds_read_b64_tr_b16 a[226:227],v2 offset:4608
v_mfma_f32_16x16x32_bf16 v[216:219],a[160:163],v[96:99],v[40:43]
ds_read_b64_tr_b16 a[228:229],v2 offset:8704
v_cvt_pk_bf16_f32 v141,v154,v155
v_mfma_f32_16x16x32_bf16 v[216:219],a[164:167],v[100:103],v[216:219]
ds_read_b64_tr_b16 a[230:231],v2 offset:12800
v_mfma_f32_16x16x32_bf16 v[216:219],a[168:171],v[104:107],v[216:219]
ds_read_b64_tr_b16 a[236:237],v3 offset:8704
v_mfma_f32_16x16x32_bf16 v[216:219],a[172:175],v[108:111],v[216:219]
ds_read_b64_tr_b16 a[238:239],v3 offset:12800
v_cvt_pk_bf16_f32 v142,v156,v157
v_mfma_f32_16x16x32_bf16 v[220:223],a[188:191],v[108:111],v[40:43]
ds_read_b64_tr_b16 a[232:233],v3 offset:512
v_mfma_f32_16x16x32_bf16 v[220:223],a[184:187],v[104:107],v[220:223]
ds_read_b64_tr_b16 a[234:235],v3 offset:4608
v_mfma_f32_16x16x32_bf16 v[220:223],a[180:183],v[100:103],v[220:223]
ds_read_b64_tr_b16 a[240:241],v2 offset:768
v_cvt_pk_bf16_f32 v143,v158,v159
v_mfma_f32_16x16x32_bf16 v[220:223],a[176:179],v[96:99],v[220:223]
s_waitcnt lgkmcnt(7)
buffer_load_dword v26,v24,s[88:91],s93 offen
v_mfma_f32_16x16x32_bf16 a[0:3],a[192:195],v[136:139],a[0:3]
ds_read_b64_tr_b16 a[242:243],v2 offset:4864
v_mfma_f32_16x16x32_bf16 a[0:3],a[196:199],v[140:143],a[0:3]
ds_read_b64_tr_b16 a[244:245],v2 offset:8960
v_mfma_f32_16x16x32_bf16 a[4:7],a[204:207],v[140:143],a[4:7]
ds_read_b64_tr_b16 a[246:247],v2 offset:13056
v_mfma_f32_16x16x32_bf16 a[4:7],a[200:203],v[136:139],a[4:7]
s_memrealtime s[76:77]
ds_read_b64_tr_b16 a[252:253],v3 offset:8960
v_mfma_f32_16x16x32_bf16 a[8:11],a[208:211],v[136:139],a[8:11]
ds_read_b64_tr_b16 a[254:255],v3 offset:13056
v_mfma_f32_16x16x32_bf16 a[8:11],a[212:215],v[140:143],a[8:11]
ds_read_b64_tr_b16 a[248:249],v3 offset:768
v_mfma_f32_16x16x32_bf16 a[12:15],a[220:223],v[140:143],a[12:15]
ds_read_b64_tr_b16 a[250:251],v3 offset:4864
v_mfma_f32_16x16x32_bf16 a[12:15],a[216:219],v[136:139],a[12:15]
s_bitcmp1_b32 s45,0
s_cbranch_scc0 L_t310_%=
s_mov_b32 m0,s80
s_add_u32 s80,s80,4096
buffer_load_dwordx4 v25,s[20:23],s83 offen nt lds
s_add_u32 s83,s83,4096
L_t310_%=:
v_mfma_f32_16x16x32_bf16 a[16:19],a[224:227],v[136:139],a[16:19]
s_waitcnt lgkmcnt(6)
v_exp_f32 v186,v186
v_mfma_f32_16x16x32_bf16 a[16:19],a[228:231],v[140:143],a[16:19]
v_exp_f32 v187,v187
v_mfma_f32_16x16x32_bf16 a[20:23],a[236:239],v[140:143],a[20:23]
s_waitcnt lgkmcnt(0)
v_exp_f32 v188,v188
v_mfma_f32_16x16x32_bf16 a[20:23],a[232:235],v[136:139],a[20:23]
s_sub_u32 s79,s78,s76
v_exp_f32 v189,v189
v_mfma_f32_16x16x32_bf16 a[24:27],a[240:243],v[136:139],a[24:27]
s_add_u32 s78,s76,s60
v_exp_f32 v190,v190
v_mfma_f32_16x16x32_bf16 a[24:27],a[244:247],v[140:143],a[24:27]
s_cmp_gt_i32 s79,s95
s_cbranch_scc1 L_ps3_%=
L_pr3_%=:
v_cvt_pk_bf16_f32 v173,v186,v187
v_mfma_f32_16x16x32_bf16 a[28:31],a[252:255],v[140:143],a[28:31]
v_exp_f32 v191,v191
v_mfma_f32_16x16x32_bf16 a[28:31],a[248:251],v[136:139],a[28:31]
v_cvt_pk_bf16_f32 v174,v188,v189
v_cvt_pk_bf16_f32 v175,v190,v191
s_cmp_lg_u32 s53,0
v_smfmac_f32_16x16x64_bf16 v[48:51],v[28:31],v[136:143],v27
s_cbranch_scc1 L_att0_%=
L_att0x_%=:
v_mfma_f32_16x16x32_bf16 v[232:235],a[128:131],v[112:115],v[44:47]
s_waitcnt vmcnt(10)
s_sub_u32 s94,s94,3
v_exp_f32 v200,v200
v_mfma_f32_16x16x32_bf16 v[232:235],a[132:135],v[116:119],v[232:235]
s_barrier
s_add_u32 m0,0,s51
ds_read_b128 a[128:131],v1
v_mfma_f32_16x16x32_bf16 v[232:235],a[136:139],v[120:123],v[232:235]
s_lshr_b32 s60,s94,2
v_exp_f32 v201,v201
v_mfma_f32_16x16x32_bf16 v[232:235],a[140:143],v[124:127],v[232:235]
s_max_i32 s60,s60,128
ds_read_b128 a[132:135],v1 offset:256
v_cvt_pk_bf16_f32 v200,v200,v201
v_mfma_f32_16x16x32_bf16 v[236:239],a[156:159],v[124:127],v[44:47]
s_add_u32 s54,s54,16384
buffer_load_dwordx4 v4,s[20:23],s47 offen nt lds
v_mfma_f32_16x16x32_bf16 v[236:239],a[152:155],v[120:123],v[236:239]
s_and_b32 s46,s54,0xfffc0000
ds_read_b128 a[136:139],v1 offset:512
v_and_b32 v18,48,v6
v_mfma_f32_16x16x32_bf16 v[236:239],a[148:151],v[116:119],v[236:239]
s_sub_u32 s94,s94,3
v_exp_f32 v202,v202
v_mfma_f32_16x16x32_bf16 v[236:239],a[144:147],v[112:115],v[236:239]
s_add_u32 s54,s54,16384
ds_read_b128 a[140:143],v1 offset:768
v_lshrrev_b32 v19,4,v6
v_mfma_f32_16x16x32_bf16 v[248:251],a[160:163],v[112:115],v[44:47]
s_mul_i32 s83,s41,5
v_exp_f32 v203,v203
v_mfma_f32_16x16x32_bf16 v[248:251],a[164:167],v[116:119],v[248:251]
s_lshl_b32 s44,s40,5
ds_read_b128 a[156:159],v1 offset:4864
v_cvt_pk_bf16_f32 v201,v202,v203
v_mfma_f32_16x16x32_bf16 v[248:251],a[168:171],v[120:123],v[248:251]
s_add_u32 s83,s83,s42
v_exp_f32 v204,v204
v_mfma_f32_16x16x32_bf16 v[248:251],a[172:175],v[124:127],v[248:251]
s_add_u32 s84,s55,0
ds_read_b128 a[152:155],v1 offset:4608
)A"
R"A(v_lshlrev_b32 v18,2,v18
v_mfma_f32_16x16x32_bf16 v[252:255],a[188:191],v[124:127],v[44:47]
s_add_u32 s85,s55,4096
v_exp_f32 v205,v205
v_mfma_f32_16x16x32_bf16 v[252:255],a[184:187],v[120:123],v[252:255]
s_add_u32 s86,s55,8192
ds_read_b128 a[148:151],v1 offset:4352
v_cvt_pk_bf16_f32 v202,v204,v205
v_mfma_f32_16x16x32_bf16 v[252:255],a[180:183],v[116:119],v[252:255]
s_add_u32 s87,s55,12288
v_exp_f32 v206,v206
v_mfma_f32_16x16x32_bf16 v[252:255],a[176:179],v[112:115],v[252:255]
s_and_b32 s44,s44,32
ds_read_b128 a[144:147],v1 offset:4096
v_and_b32 v19,48,v19
v_mfma_f32_16x16x32_bf16 a[32:35],a[192:195],v[168:171],a[32:35]
s_add_u32 s83,s83,15
v_exp_f32 v207,v207
v_mfma_f32_16x16x32_bf16 a[32:35],a[196:199],v[172:175],a[32:35]
s_max_i32 s84,s84,s18
ds_read_b128 a[160:163],v1 offset:8192
v_cvt_pk_bf16_f32 v203,v206,v207
v_mfma_f32_16x16x32_bf16 a[36:39],a[204:207],v[172:175],a[36:39]
s_max_i32 s85,s85,s19
buffer_load_dwordx4 v4,s[20:23],s47 offen offset:1024 nt lds
v_mfma_f32_16x16x32_bf16 a[36:39],a[200:203],v[168:171],a[36:39]
s_max_i32 s86,s86,s100
ds_read_b128 a[164:167],v1 offset:8448
v_add_u32 v18,v18,v19
v_mfma_f32_16x16x32_bf16 a[40:43],a[208:211],v[168:171],a[40:43]
s_max_i32 s87,s87,s101
v_exp_f32 v216,v216
v_mfma_f32_16x16x32_bf16 a[40:43],a[212:215],v[172:175],a[40:43]
s_lshl_b32 s80,s40,10
ds_read_b128 a[168:171],v1 offset:8704
v_add_u32 v18,98304,v18
v_mfma_f32_16x16x32_bf16 a[44:47],a[220:223],v[172:175],a[44:47]
s_and_b32 s83,s83,15
v_exp_f32 v217,v217
v_mfma_f32_16x16x32_bf16 a[44:47],a[216:219],v[168:171],a[44:47]
s_add_u32 s83,s83,16
ds_read_b128 a[172:175],v1 offset:8960
v_cvt_pk_bf16_f32 v204,v216,v217
v_mfma_f32_16x16x32_bf16 a[48:51],a[224:227],v[168:171],a[48:51]
s_mov_b32 s50,32768
v_exp_f32 v218,v218
v_mfma_f32_16x16x32_bf16 a[48:51],a[228:231],v[172:175],a[48:51]
s_mov_b32 s49,98304
ds_read_b128 a[188:191],v1 offset:13056
v_add_u32 v2,s48,v2
v_mfma_f32_16x16x32_bf16 a[52:55],a[236:239],v[172:175],a[52:55]
s_mov_b32 s45,0x2a8
v_exp_f32 v219,v219
v_mfma_f32_16x16x32_bf16 a[52:55],a[232:235],v[168:171],a[52:55]
s_lshl_b32 s83,s83,14
ds_read_b128 a[184:187],v1 offset:12800
v_cvt_pk_bf16_f32 v205,v218,v219
v_mfma_f32_16x16x32_bf16 a[56:59],a[240:243],v[168:171],a[56:59]
s_add_u32 s93,s92,L_endseg_%=-L_pc_%=
v_exp_f32 v220,v220
v_mfma_f32_16x16x32_bf16 a[56:59],a[244:247],v[172:175],a[56:59]
ds_read_b128 a[180:183],v1 offset:12544
v_add_u32 v3,s48,v3
v_mfma_f32_16x16x32_bf16 a[60:63],a[252:255],v[172:175],a[60:63]
s_mov_b32 s48,32768
v_exp_f32 v221,v221
v_mfma_f32_16x16x32_bf16 a[60:63],a[248:251],v[168:171],a[60:63]
ds_read_b128 a[176:179],v1 offset:12288
v_cvt_pk_bf16_f32 v206,v220,v221
v_smfmac_f32_16x16x64_bf16 v[52:55],v[28:31],v[168:175],v27
s_waitcnt lgkmcnt(4)
v_add_u32 v1,s48,v1
v_mfma_f32_16x16x32_bf16 v[136:139],a[128:131],v[64:67],v[32:35]
buffer_load_dwordx4 v5,s[20:23],s47 offen offset:2048 nt lds
v_mfma_f32_16x16x32_bf16 v[136:139],a[132:135],v[68:71],v[136:139]
v_exp_f32 v222,v222
v_mfma_f32_16x16x32_bf16 v[136:139],a[136:139],v[72:75],v[136:139]
v_exp_f32 v223,v223
v_mfma_f32_16x16x32_bf16 v[136:139],a[140:143],v[76:79],v[136:139]
v_cvt_pk_bf16_f32 v207,v222,v223
v_subrev_u32 v25,s51,v4
v_mfma_f32_16x16x32_bf16 v[140:143],a[156:159],v[76:79],v[32:35]
v_exp_f32 v232,v232
v_mfma_f32_16x16x32_bf16 v[140:143],a[152:155],v[72:75],v[140:143]
v_exp_f32 v233,v233
v_mfma_f32_16x16x32_bf16 v[140:143],a[148:151],v[68:71],v[140:143]
v_exp_f32 v234,v234
v_mfma_f32_16x16x32_bf16 v[140:143],a[144:147],v[64:67],v[140:143]
v_exp_f32 v235,v235
v_mfma_f32_16x16x32_bf16 v[152:155],a[160:163],v[64:67],v[32:35]
v_cvt_pk_bf16_f32 v232,v232,v233
v_cvt_pk_bf16_f32 v233,v234,v235
v_mfma_f32_16x16x32_bf16 v[152:155],a[164:167],v[68:71],v[152:155]
v_exp_f32 v236,v236
v_mfma_f32_16x16x32_bf16 v[152:155],a[168:171],v[72:75],v[152:155]
v_exp_f32 v237,v237
v_mfma_f32_16x16x32_bf16 v[152:155],a[172:175],v[76:79],v[152:155]
s_waitcnt lgkmcnt(0)
v_exp_f32 v238,v238
v_mfma_f32_16x16x32_bf16 v[156:159],a[188:191],v[76:79],v[32:35]
v_exp_f32 v239,v239
v_mfma_f32_16x16x32_bf16 v[156:159],a[184:187],v[72:75],v[156:159]
buffer_load_dwordx4 v5,s[20:23],s47 offen offset:3072 nt lds
v_mfma_f32_16x16x32_bf16 v[156:159],a[180:183],v[68:71],v[156:159]
v_cvt_pk_bf16_f32 v234,v236,v237
v_cvt_pk_bf16_f32 v235,v238,v239
v_mfma_f32_16x16x32_bf16 v[156:159],a[176:179],v[64:67],v[156:159]
v_exp_f32 v248,v248
v_mfma_f32_16x16x32_bf16 a[64:67],a[192:195],v[200:203],a[64:67]
v_exp_f32 v249,v249
v_mfma_f32_16x16x32_bf16 a[64:67],a[196:199],v[204:207],a[64:67]
v_exp_f32 v250,v250
v_mfma_f32_16x16x32_bf16 a[68:71],a[204:207],v[204:207],a[68:71]
v_exp_f32 v251,v251
v_mfma_f32_16x16x32_bf16 a[68:71],a[200:203],v[200:203],a[68:71]
v_exp_f32 v252,v252
v_mfma_f32_16x16x32_bf16 a[72:75],a[208:211],v[200:203],a[72:75]
v_exp_f32 v253,v253
v_mfma_f32_16x16x32_bf16 a[72:75],a[212:215],v[204:207],a[72:75]
v_exp_f32 v254,v254
v_mfma_f32_16x16x32_bf16 a[76:79],a[220:223],v[204:207],a[76:79]
v_exp_f32 v255,v255
v_mfma_f32_16x16x32_bf16 a[76:79],a[216:219],v[200:203],a[76:79]
v_cvt_pk_bf16_f32 v236,v248,v249
v_cvt_pk_bf16_f32 v237,v250,v251
v_mfma_f32_16x16x32_bf16 a[80:83],a[224:227],v[200:203],a[80:83]
s_add_u32 m0,0,s52
v_cvt_pk_bf16_f32 v238,v252,v253
v_cvt_pk_bf16_f32 v239,v254,v255
v_mfma_f32_16x16x32_bf16 a[80:83],a[228:231],v[204:207],a[80:83]
v_exp_f32 v136,v136
v_mfma_f32_16x16x32_bf16 a[84:87],a[236:239],v[204:207],a[84:87]
v_exp_f32 v137,v137
v_mfma_f32_16x16x32_bf16 a[84:87],a[232:235],v[200:203],a[84:87]
buffer_load_dwordx4 v4,s[24:27],s47 offen nt lds
v_mfma_f32_16x16x32_bf16 a[88:91],a[240:243],v[200:203],a[88:91]
v_exp_f32 v138,v138
v_mfma_f32_16x16x32_bf16 a[88:91],a[244:247],v[204:207],a[88:91]
v_exp_f32 v139,v139
v_mfma_f32_16x16x32_bf16 a[92:95],a[252:255],v[204:207],a[92:95]
v_cvt_pk_bf16_f32 v136,v136,v137
v_cvt_pk_bf16_f32 v137,v138,v139
v_mfma_f32_16x16x32_bf16 a[92:95],a[248:251],v[200:203],a[92:95]
v_exp_f32 v140,v140
v_smfmac_f32_16x16x64_bf16 v[56:59],v[28:31],v[200:207],v27
v_xor_b32 v25,s44,v25
v_mfma_f32_16x16x32_bf16 v[168:171],a[128:131],v[80:83],v[36:39]
v_exp_f32 v141,v141
v_mfma_f32_16x16x32_bf16 v[168:171],a[132:135],v[84:87],v[168:171]
v_exp_f32 v142,v142
v_mfma_f32_16x16x32_bf16 v[168:171],a[136:139],v[88:91],v[168:171]
v_exp_f32 v143,v143
v_mfma_f32_16x16x32_bf16 v[168:171],a[140:143],v[92:95],v[168:171]
v_cvt_pk_bf16_f32 v138,v140,v141
v_cvt_pk_bf16_f32 v139,v142,v143
v_mfma_f32_16x16x32_bf16 v[172:175],a[156:159],v[92:95],v[36:39]
v_exp_f32 v152,v152
v_mfma_f32_16x16x32_bf16 v[172:175],a[152:155],v[88:91],v[172:175]
v_exp_f32 v153,v153
v_mfma_f32_16x16x32_bf16 v[172:175],a[148:151],v[84:87],v[172:175]
v_exp_f32 v154,v154
v_mfma_f32_16x16x32_bf16 v[172:175],a[144:147],v[80:83],v[172:175]
v_exp_f32 v155,v155
v_mfma_f32_16x16x32_bf16 v[184:187],a[160:163],v[80:83],v[36:39]
buffer_load_dwordx4 v5,s[24:27],s47 offen offset:1024 nt lds
v_mfma_f32_16x16x32_bf16 v[184:187],a[164:167],v[84:87],v[184:187]
v_exp_f32 v156,v156
v_mfma_f32_16x16x32_bf16 v[184:187],a[168:171],v[88:91],v[184:187]
v_exp_f32 v157,v157
v_mfma_f32_16x16x32_bf16 v[184:187],a[172:175],v[92:95],v[184:187]
v_exp_f32 v158,v158
v_mfma_f32_16x16x32_bf16 v[188:191],a[188:191],v[92:95],v[36:39]
v_exp_f32 v159,v159
v_mfma_f32_16x16x32_bf16 v[188:191],a[184:187],v[88:91],v[188:191]
v_cvt_pk_bf16_f32 v140,v152,v153
v_cvt_pk_bf16_f32 v141,v154,v155
v_mfma_f32_16x16x32_bf16 v[188:191],a[180:183],v[84:87],v[188:191]
s_waitcnt vmcnt(14)
v_cvt_pk_bf16_f32 v142,v156,v157
v_cvt_pk_bf16_f32 v143,v158,v159
v_mfma_f32_16x16x32_bf16 v[188:191],a[176:179],v[80:83],v[188:191]
s_barrier
ds_read_b128 v[80:83],v18
v_add_u32 v25,s80,v25
v_mfma_f32_16x16x32_bf16 a[96:99],a[192:195],v[232:235],a[96:99]
s_add_u32 s80,s80,147456
v_exp_f32 v168,v168
v_mfma_f32_16x16x32_bf16 a[96:99],a[196:199],v[236:239],a[96:99]
v_exp_f32 v169,v169
v_mfma_f32_16x16x32_bf16 a[100:103],a[204:207],v[236:239],a[100:103]
ds_read_b128 v[84:87],v18 offset:256
v_cvt_pk_bf16_f32 v168,v168,v169
v_mfma_f32_16x16x32_bf16 a[100:103],a[200:203],v[232:235],a[100:103]
v_exp_f32 v170,v170
v_mfma_f32_16x16x32_bf16 a[104:107],a[208:211],v[232:235],a[104:107]
v_exp_f32 v171,v171
v_mfma_f32_16x16x32_bf16 a[104:107],a[212:215],v[236:239],a[104:107]
buffer_load_dwordx4 v4,s[24:27],s47 offen offset:2048 nt lds
v_mfma_f32_16x16x32_bf16 a[108:111],a[220:223],v[236:239],a[108:111]
ds_read_b128 v[88:91],v18 offset:512
v_cvt_pk_bf16_f32 v169,v170,v171
v_mfma_f32_16x16x32_bf16 a[108:111],a[216:219],v[232:235],a[108:111]
v_exp_f32 v172,v172
v_mfma_f32_16x16x32_bf16 a[112:115],a[224:227],v[232:235],a[112:115]
v_exp_f32 v173,v173
v_mfma_f32_16x16x32_bf16 a[112:115],a[228:231],v[236:239],a[112:115]
ds_read_b128 v[92:95],v18 offset:768
v_cvt_pk_bf16_f32 v170,v172,v173
v_mfma_f32_16x16x32_bf16 a[116:119],a[236:239],v[236:239],a[116:119]
v_exp_f32 v174,v174
v_mfma_f32_16x16x32_bf16 a[116:119],a[232:235],v[232:235],a[116:119]
ds_read_b64_tr_b16 a[192:193],v2
ds_read_b64_tr_b16 a[194:195],v2 offset:4096
v_mfma_f32_16x16x32_bf16 a[120:123],a[240:243],v[232:235],a[120:123]
v_exp_f32 v175,v175
v_mfma_f32_16x16x32_bf16 a[120:123],a[244:247],v[236:239],a[120:123]
ds_read_b64_tr_b16 a[196:197],v2 offset:8192
ds_read_b64_tr_b16 a[198:199],v2 offset:12288
v_mfma_f32_16x16x32_bf16 a[124:127],a[252:255],v[236:239],a[124:127]
ds_read_b64_tr_b16 a[204:205],v3 offset:8192
v_cvt_pk_bf16_f32 v171,v174,v175
v_mfma_f32_16x16x32_bf16 a[124:127],a[248:251],v[232:235],a[124:127]
v_exp_f32 v184,v184
v_smfmac_f32_16x16x64_bf16 v[60:63],v[28:31],v[232:239],v27
ds_read_b64_tr_b16 a[206:207],v3 offset:12288
v_mfma_f32_16x16x32_bf16 v[200:203],a[128:131],v[96:99],v[40:43]
ds_read_b64_tr_b16 a[200:201],v3
ds_read_b64_tr_b16 a[202:203],v3 offset:4096
v_mfma_f32_16x16x32_bf16 v[200:203],a[132:135],v[100:103],v[200:203]
v_exp_f32 v185,v185
v_mfma_f32_16x16x32_bf16 v[200:203],a[136:139],v[104:107],v[200:203]
buffer_load_dwordx4 v5,s[24:27],s47 offen offset:3072 nt lds
v_mfma_f32_16x16x32_bf16 v[200:203],a[140:143],v[108:111],v[200:203]
s_add_u32 s47,s47,16384
ds_read_b64_tr_b16 a[208:209],v2 offset:256
ds_read_b64_tr_b16 a[210:211],v2 offset:4352
v_mfma_f32_16x16x32_bf16 v[204:207],a[156:159],v[108:111],v[40:43]
s_and_b32 s47,s47,0x3ffff
s_or_b32 s47,s47,s46
ds_read_b64_tr_b16 a[212:213],v2 offset:8448
v_mfma_f32_16x16x32_bf16 v[204:207],a[152:155],v[104:107],v[204:207]
s_and_b32 s46,s54,0xfffc0000
ds_read_b64_tr_b16 a[214:215],v2 offset:12544
v_cvt_pk_bf16_f32 v172,v184,v185
v_mfma_f32_16x16x32_bf16 v[204:207],a[148:151],v[100:103],v[204:207]
s_add_u32 s54,s54,16384
v_exp_f32 v186,v186
v_mfma_f32_16x16x32_bf16 v[204:207],a[144:147],v[96:99],v[204:207]
ds_read_b64_tr_b16 a[220:221],v3 offset:8448
ds_read_b64_tr_b16 a[222:223],v3 offset:12544
v_mfma_f32_16x16x32_bf16 v[216:219],a[160:163],v[96:99],v[40:43]
v_exp_f32 v187,v187
v_mfma_f32_16x16x32_bf16 v[216:219],a[164:167],v[100:103],v[216:219]
ds_read_b64_tr_b16 a[216:217],v3 offset:256
ds_read_b64_tr_b16 a[218:219],v3 offset:4352
v_mfma_f32_16x16x32_bf16 v[216:219],a[168:171],v[104:107],v[216:219]
ds_read_b64_tr_b16 a[224:225],v2 offset:512
v_cvt_pk_bf16_f32 v173,v186,v187
v_mfma_f32_16x16x32_bf16 v[216:219],a[172:175],v[108:111],v[216:219]
ds_read_b64_tr_b16 a[226:227],v2 offset:4608
v_mfma_f32_16x16x32_bf16 v[220:223],a[188:191],v[108:111],v[40:43]
ds_read_b64_tr_b16 a[228:229],v2 offset:8704
v_mfma_f32_16x16x32_bf16 v[220:223],a[184:187],v[104:107],v[220:223]
ds_read_b64_tr_b16 a[230:231],v2 offset:12800
ds_read_b64_tr_b16 a[236:237],v3 offset:8704
v_mfma_f32_16x16x32_bf16 v[220:223],a[180:183],v[100:103],v[220:223]
ds_read_b64_tr_b16 a[238:239],v3 offset:12800
v_mfma_f32_16x16x32_bf16 v[220:223],a[176:179],v[96:99],v[220:223]
s_waitcnt lgkmcnt(8)
ds_read_b64_tr_b16 a[232:233],v3 offset:512
v_mfma_f32_16x16x32_bf16 v[32:35],v[80:83],v[128:131],0
ds_read_b64_tr_b16 a[234:235],v3 offset:4608
v_mfma_f32_16x16x32_bf16 v[32:35],v[84:87],v[160:163],v[32:35]
ds_read_b64_tr_b16 a[240:241],v2 offset:768
ds_read_b64_tr_b16 a[242:243],v2 offset:4864
v_mfma_f32_16x16x32_bf16 v[32:35],v[88:91],v[192:195],v[32:35]
)A"
R"A(ds_read_b64_tr_b16 a[244:245],v2 offset:8960
v_mfma_f32_16x16x32_bf16 v[32:35],v[92:95],v[224:227],v[32:35]
ds_read_b64_tr_b16 a[246:247],v2 offset:13056
v_add_u32 v2,s48,v2
v_mfma_f32_16x16x32_bf16 v[36:39],v[80:83],v[132:135],0
ds_read_b64_tr_b16 a[252:253],v3 offset:8960
v_mfma_f32_16x16x32_bf16 v[36:39],v[84:87],v[164:167],v[36:39]
ds_read_b64_tr_b16 a[254:255],v3 offset:13056
ds_read_b64_tr_b16 a[248:249],v3 offset:768
v_mfma_f32_16x16x32_bf16 v[36:39],v[88:91],v[196:199],v[36:39]
ds_read_b64_tr_b16 a[250:251],v3 offset:4864
v_add_u32 v3,s48,v3
v_mfma_f32_16x16x32_bf16 v[36:39],v[92:95],v[228:231],v[36:39]
s_mov_b32 s48,-98304
v_exp_f32 v188,v188
v_mfma_f32_16x16x32_bf16 v[40:43],v[80:83],v[144:147],0
v_exp_f32 v189,v189
v_mfma_f32_16x16x32_bf16 v[40:43],v[84:87],v[176:179],v[40:43]
v_exp_f32 v190,v190
v_mfma_f32_16x16x32_bf16 v[40:43],v[88:91],v[208:211],v[40:43]
v_exp_f32 v191,v191
v_mfma_f32_16x16x32_bf16 v[40:43],v[92:95],v[240:243],v[40:43]
v_cvt_pk_bf16_f32 v174,v188,v189
v_cvt_pk_bf16_f32 v175,v190,v191
v_mfma_f32_16x16x32_bf16 a[0:3],a[192:195],v[136:139],a[0:3]
v_exp_f32 v200,v200
v_mfma_f32_16x16x32_bf16 a[0:3],a[196:199],v[140:143],a[0:3]
v_max3_f32 v32,v32,v33,v34
v_max3_f32 v32,v32,v35,s18
v_mfma_f32_16x16x32_bf16 a[4:7],a[204:207],v[140:143],a[4:7]
v_sub_f32 v35,0,v32
v_sub_f32 v34,0,v32
v_mfma_f32_16x16x32_bf16 a[4:7],a[200:203],v[136:139],a[4:7]
s_memrealtime s[76:77]
v_sub_f32 v33,0,v32
v_sub_f32 v32,0,v32
v_mfma_f32_16x16x32_bf16 a[8:11],a[208:211],v[136:139],a[8:11]
v_exp_f32 v201,v201
v_mfma_f32_16x16x32_bf16 a[8:11],a[212:215],v[140:143],a[8:11]
v_exp_f32 v202,v202
v_mfma_f32_16x16x32_bf16 a[12:15],a[220:223],v[140:143],a[12:15]
s_waitcnt lgkmcnt(0)
v_exp_f32 v203,v203
v_mfma_f32_16x16x32_bf16 a[12:15],a[216:219],v[136:139],a[12:15]
v_cvt_pk_bf16_f32 v200,v200,v201
v_cvt_pk_bf16_f32 v201,v202,v203
v_mfma_f32_16x16x32_bf16 a[16:19],a[224:227],v[136:139],a[16:19]
v_exp_f32 v204,v204
v_mfma_f32_16x16x32_bf16 a[16:19],a[228:231],v[140:143],a[16:19]
v_exp_f32 v205,v205
v_mfma_f32_16x16x32_bf16 a[20:23],a[236:239],v[140:143],a[20:23]
s_waitcnt lgkmcnt(0)
v_exp_f32 v206,v206
v_mfma_f32_16x16x32_bf16 a[20:23],a[232:235],v[136:139],a[20:23]
s_sub_u32 s79,s78,s76
v_exp_f32 v207,v207
v_mfma_f32_16x16x32_bf16 a[24:27],a[240:243],v[136:139],a[24:27]
s_add_u32 s78,s76,s60
v_cvt_pk_bf16_f32 v202,v204,v205
v_cvt_pk_bf16_f32 v203,v206,v207
v_mfma_f32_16x16x32_bf16 a[24:27],a[244:247],v[140:143],a[24:27]
s_cmp_gt_i32 s79,s95
s_cbranch_scc1 L_ps4_%=
L_pr4_%=:
v_max3_f32 v36,v36,v37,v38
v_mfma_f32_16x16x32_bf16 a[28:31],a[252:255],v[140:143],a[28:31]
s_lshr_b32 s60,s94,2
v_exp_f32 v216,v216
v_mfma_f32_16x16x32_bf16 a[28:31],a[248:251],v[136:139],a[28:31]
s_max_i32 s60,s60,128
v_exp_f32 v217,v217
v_smfmac_f32_16x16x64_bf16 v[48:51],v[28:31],v[136:143],v27
s_sub_u32 s94,s94,3
v_cvt_pk_bf16_f32 v204,v216,v217
v_mfma_f32_16x16x32_bf16 v[232:235],a[128:131],v[112:115],v[44:47]
s_waitcnt vmcnt(9)
v_exp_f32 v218,v218
v_mfma_f32_16x16x32_bf16 v[232:235],a[132:135],v[116:119],v[232:235]
s_barrier
s_add_u32 m0,32768,s51
ds_read_b128 a[128:131],v1
v_mfma_f32_16x16x32_bf16 v[232:235],a[136:139],v[120:123],v[232:235]
v_exp_f32 v219,v219
v_mfma_f32_16x16x32_bf16 v[232:235],a[140:143],v[124:127],v[232:235]
ds_read_b128 a[132:135],v1 offset:256
v_cvt_pk_bf16_f32 v205,v218,v219
v_mfma_f32_16x16x32_bf16 v[236:239],a[156:159],v[124:127],v[44:47]
buffer_load_dwordx4 v4,s[20:23],s47 offen nt lds
v_mfma_f32_16x16x32_bf16 v[236:239],a[152:155],v[120:123],v[236:239]
ds_read_b128 a[136:139],v1 offset:512
v_max3_f32 v36,v36,v39,s19
v_mfma_f32_16x16x32_bf16 v[236:239],a[148:151],v[116:119],v[236:239]
v_exp_f32 v220,v220
v_mfma_f32_16x16x32_bf16 v[236:239],a[144:147],v[112:115],v[236:239]
ds_read_b128 a[140:143],v1 offset:768
v_sub_f32 v39,0,v36
v_mfma_f32_16x16x32_bf16 v[248:251],a[160:163],v[112:115],v[44:47]
v_exp_f32 v221,v221
v_mfma_f32_16x16x32_bf16 v[248:251],a[164:167],v[116:119],v[248:251]
ds_read_b128 a[156:159],v1 offset:4864
v_cvt_pk_bf16_f32 v206,v220,v221
v_mfma_f32_16x16x32_bf16 v[248:251],a[168:171],v[120:123],v[248:251]
v_exp_f32 v222,v222
v_mfma_f32_16x16x32_bf16 v[248:251],a[172:175],v[124:127],v[248:251]
ds_read_b128 a[152:155],v1 offset:4608
v_sub_f32 v38,0,v36
v_mfma_f32_16x16x32_bf16 v[252:255],a[188:191],v[124:127],v[44:47]
v_exp_f32 v223,v223
v_mfma_f32_16x16x32_bf16 v[252:255],a[184:187],v[120:123],v[252:255]
ds_read_b128 a[148:151],v1 offset:4352
v_cvt_pk_bf16_f32 v207,v222,v223
v_mfma_f32_16x16x32_bf16 v[252:255],a[180:183],v[116:119],v[252:255]
v_sub_f32 v37,0,v36
v_sub_f32 v36,0,v36
v_mfma_f32_16x16x32_bf16 v[252:255],a[176:179],v[112:115],v[252:255]
ds_read_b128 a[144:147],v1 offset:4096
v_max3_f32 v40,v40,v41,v42
v_mfma_f32_16x16x32_bf16 v[44:47],v[80:83],v[148:151],0
v_exp_f32 v232,v232
v_mfma_f32_16x16x32_bf16 v[44:47],v[84:87],v[180:183],v[44:47]
ds_read_b128 a[160:163],v1 offset:8192
v_max3_f32 v40,v40,v43,s100
v_mfma_f32_16x16x32_bf16 v[44:47],v[88:91],v[212:215],v[44:47]
buffer_load_dwordx4 v4,s[20:23],s47 offen offset:1024 nt lds
v_mfma_f32_16x16x32_bf16 v[44:47],v[92:95],v[244:247],v[44:47]
ds_read_b128 a[164:167],v1 offset:8448
v_sub_f32 v43,0,v40
v_mfma_f32_16x16x32_bf16 a[32:35],a[192:195],v[168:171],a[32:35]
v_exp_f32 v233,v233
v_mfma_f32_16x16x32_bf16 a[32:35],a[196:199],v[172:175],a[32:35]
ds_read_b128 a[168:171],v1 offset:8704
v_cvt_pk_bf16_f32 v232,v232,v233
v_mfma_f32_16x16x32_bf16 a[36:39],a[204:207],v[172:175],a[36:39]
v_exp_f32 v234,v234
v_mfma_f32_16x16x32_bf16 a[36:39],a[200:203],v[168:171],a[36:39]
ds_read_b128 a[172:175],v1 offset:8960
v_sub_f32 v42,0,v40
v_mfma_f32_16x16x32_bf16 a[40:43],a[208:211],v[168:171],a[40:43]
v_exp_f32 v235,v235
v_mfma_f32_16x16x32_bf16 a[40:43],a[212:215],v[172:175],a[40:43]
ds_read_b128 a[188:191],v1 offset:13056
v_cvt_pk_bf16_f32 v233,v234,v235
v_mfma_f32_16x16x32_bf16 a[44:47],a[220:223],v[172:175],a[44:47]
v_exp_f32 v236,v236
v_mfma_f32_16x16x32_bf16 a[44:47],a[216:219],v[168:171],a[44:47]
ds_read_b128 a[184:187],v1 offset:12800
v_sub_f32 v41,0,v40
v_mfma_f32_16x16x32_bf16 a[48:51],a[224:227],v[168:171],a[48:51]
v_exp_f32 v237,v237
v_mfma_f32_16x16x32_bf16 a[48:51],a[228:231],v[172:175],a[48:51]
ds_read_b128 a[180:183],v1 offset:12544
v_cvt_pk_bf16_f32 v234,v236,v237
v_mfma_f32_16x16x32_bf16 a[52:55],a[236:239],v[172:175],a[52:55]
v_exp_f32 v238,v238
v_mfma_f32_16x16x32_bf16 a[52:55],a[232:235],v[168:171],a[52:55]
ds_read_b128 a[176:179],v1 offset:12288
v_sub_f32 v40,0,v40
v_mfma_f32_16x16x32_bf16 a[56:59],a[240:243],v[168:171],a[56:59]
buffer_load_dwordx4 v5,s[20:23],s47 offen offset:2048 nt lds
v_mfma_f32_16x16x32_bf16 a[56:59],a[244:247],v[172:175],a[56:59]
v_exp_f32 v239,v239
v_mfma_f32_16x16x32_bf16 a[60:63],a[252:255],v[172:175],a[60:63]
v_cvt_pk_bf16_f32 v235,v238,v239
v_add_f32 v48,v49,v48
v_mfma_f32_16x16x32_bf16 a[60:63],a[248:251],v[168:171],a[60:63]
v_exp_f32 v248,v248
v_smfmac_f32_16x16x64_bf16 v[52:55],v[28:31],v[168:175],v27
s_waitcnt lgkmcnt(2)
v_max_f32 v20,0x00800000,v48
v_mfma_f32_16x16x32_bf16 v[136:139],a[128:131],v[128:131],v[32:35]
v_exp_f32 v249,v249
v_mfma_f32_16x16x32_bf16 v[136:139],a[132:135],v[160:163],v[136:139]
v_exp_f32 v250,v250
v_mfma_f32_16x16x32_bf16 v[136:139],a[136:139],v[192:195],v[136:139]
v_exp_f32 v251,v251
v_mfma_f32_16x16x32_bf16 v[136:139],a[140:143],v[224:227],v[136:139]
v_exp_f32 v252,v252
v_mfma_f32_16x16x32_bf16 v[140:143],a[156:159],v[224:227],v[32:35]
v_exp_f32 v253,v253
v_mfma_f32_16x16x32_bf16 v[140:143],a[152:155],v[192:195],v[140:143]
v_exp_f32 v254,v254
v_mfma_f32_16x16x32_bf16 v[140:143],a[148:151],v[160:163],v[140:143]
v_exp_f32 v255,v255
v_mfma_f32_16x16x32_bf16 v[140:143],a[144:147],v[128:131],v[140:143]
v_cvt_pk_bf16_f32 v236,v248,v249
v_cvt_pk_bf16_f32 v237,v250,v251
v_mfma_f32_16x16x32_bf16 v[152:155],a[160:163],v[128:131],v[32:35]
v_cvt_pk_bf16_f32 v238,v252,v253
v_cvt_pk_bf16_f32 v239,v254,v255
v_mfma_f32_16x16x32_bf16 v[152:155],a[164:167],v[160:163],v[152:155]
buffer_load_dwordx4 v5,s[20:23],s47 offen offset:3072 nt lds
v_mfma_f32_16x16x32_bf16 v[152:155],a[168:171],v[192:195],v[152:155]
v_exp_f32 v136,v136
v_mfma_f32_16x16x32_bf16 v[152:155],a[172:175],v[224:227],v[152:155]
v_exp_f32 v137,v137
v_mfma_f32_16x16x32_bf16 v[156:159],a[188:191],v[224:227],v[32:35]
v_exp_f32 v138,v138
v_mfma_f32_16x16x32_bf16 v[156:159],a[184:187],v[192:195],v[156:159]
s_waitcnt lgkmcnt(0)
v_exp_f32 v139,v139
v_mfma_f32_16x16x32_bf16 v[156:159],a[180:183],v[160:163],v[156:159]
v_cvt_pk_bf16_f32 v136,v136,v137
v_cvt_pk_bf16_f32 v137,v138,v139
v_mfma_f32_16x16x32_bf16 v[156:159],a[176:179],v[128:131],v[156:159]
v_exp_f32 v140,v140
v_mfma_f32_16x16x32_bf16 a[64:67],a[192:195],v[200:203],a[64:67]
v_exp_f32 v141,v141
v_mfma_f32_16x16x32_bf16 a[64:67],a[196:199],v[204:207],a[64:67]
v_exp_f32 v142,v142
v_mfma_f32_16x16x32_bf16 a[68:71],a[204:207],v[204:207],a[68:71]
v_exp_f32 v143,v143
v_mfma_f32_16x16x32_bf16 a[68:71],a[200:203],v[200:203],a[68:71]
v_cvt_pk_bf16_f32 v138,v140,v141
v_cvt_pk_bf16_f32 v139,v142,v143
v_mfma_f32_16x16x32_bf16 a[72:75],a[208:211],v[200:203],a[72:75]
s_add_u32 m0,32768,s52
v_exp_f32 v152,v152
v_mfma_f32_16x16x32_bf16 a[72:75],a[212:215],v[204:207],a[72:75]
v_exp_f32 v153,v153
v_mfma_f32_16x16x32_bf16 a[76:79],a[220:223],v[204:207],a[76:79]
v_exp_f32 v154,v154
v_mfma_f32_16x16x32_bf16 a[76:79],a[216:219],v[200:203],a[76:79]
buffer_load_dwordx4 v4,s[24:27],s47 offen nt lds
v_mfma_f32_16x16x32_bf16 a[80:83],a[224:227],v[200:203],a[80:83]
v_exp_f32 v155,v155
v_mfma_f32_16x16x32_bf16 a[80:83],a[228:231],v[204:207],a[80:83]
v_exp_f32 v156,v156
v_mfma_f32_16x16x32_bf16 a[84:87],a[236:239],v[204:207],a[84:87]
v_exp_f32 v157,v157
v_mfma_f32_16x16x32_bf16 a[84:87],a[232:235],v[200:203],a[84:87]
v_exp_f32 v158,v158
v_mfma_f32_16x16x32_bf16 a[88:91],a[240:243],v[200:203],a[88:91]
v_exp_f32 v159,v159
v_mfma_f32_16x16x32_bf16 a[88:91],a[244:247],v[204:207],a[88:91]
v_cvt_pk_bf16_f32 v140,v152,v153
v_cvt_pk_bf16_f32 v141,v154,v155
v_mfma_f32_16x16x32_bf16 a[92:95],a[252:255],v[204:207],a[92:95]
v_cvt_pk_bf16_f32 v142,v156,v157
v_cvt_pk_bf16_f32 v143,v158,v159
v_mfma_f32_16x16x32_bf16 a[92:95],a[248:251],v[200:203],a[92:95]
v_rcp_f32 v20,v20
v_smfmac_f32_16x16x64_bf16 v[56:59],v[28:31],v[200:207],v27
v_accvgpr_read_b32 v64,a0
v_mfma_f32_16x16x32_bf16 v[168:171],a[128:131],v[132:135],v[36:39]
v_accvgpr_read_b32 v65,a1
v_accvgpr_read_b32 v66,a2
v_mfma_f32_16x16x32_bf16 v[168:171],a[132:135],v[164:167],v[168:171]
v_accvgpr_read_b32 v67,a3
v_mul_f32 v64,v20,v64
v_mfma_f32_16x16x32_bf16 v[168:171],a[136:139],v[196:199],v[168:171]
v_mul_f32 v65,v20,v65
v_cvt_pk_bf16_f32 v64,v64,v65
v_mfma_f32_16x16x32_bf16 v[168:171],a[140:143],v[228:231],v[168:171]
v_mul_f32 v66,v20,v66
v_mul_f32 v67,v20,v67
v_mfma_f32_16x16x32_bf16 v[172:175],a[156:159],v[228:231],v[36:39]
buffer_load_dwordx4 v5,s[24:27],s47 offen offset:1024 nt lds
v_mfma_f32_16x16x32_bf16 v[172:175],a[152:155],v[196:199],v[172:175]
v_cvt_pk_bf16_f32 v65,v66,v67
v_accvgpr_read_b32 v16,a6
v_mfma_f32_16x16x32_bf16 v[172:175],a[148:151],v[164:167],v[172:175]
v_accvgpr_read_b32 v17,a7
v_accvgpr_read_b32 v66,a4
v_mfma_f32_16x16x32_bf16 v[172:175],a[144:147],v[132:135],v[172:175]
v_accvgpr_read_b32 v67,a5
v_cmp_nlt_f32 s[56:57],v48,s63
v_mfma_f32_16x16x32_bf16 v[184:187],a[160:163],v[132:135],v[36:39]
v_accvgpr_read_b32 v68,a8
v_accvgpr_read_b32 v69,a9
v_accvgpr_read_b32 v70,a10
v_accvgpr_read_b32 v71,a11
v_accvgpr_read_b32 v12,a12
v_accvgpr_read_b32 v13,a13
v_accvgpr_read_b32 v14,a14
v_accvgpr_read_b32 v15,a15
v_pk_mul_f32 v[68:69],v[20:21],v[68:69] op_sel:[0,0] op_sel_hi:[0,1]
v_pk_mul_f32 v[70:71],v[20:21],v[70:71] op_sel:[0,0] op_sel_hi:[0,1]
v_pk_mul_f32 v[12:13],v[20:21],v[12:13] op_sel:[0,0] op_sel_hi:[0,1]
v_pk_mul_f32 v[14:15],v[20:21],v[14:15] op_sel:[0,0] op_sel_hi:[0,1]
v_cvt_pk_bf16_f32 v68,v68,v69
v_cvt_pk_bf16_f32 v69,v70,v71
v_cvt_pk_bf16_f32 v70,v12,v13
v_cvt_pk_bf16_f32 v71,v14,v15
v_mfma_f32_16x16x32_bf16 v[184:187],a[164:167],v[164:167],v[184:187]
v_accvgpr_read_b32 v72,a16
v_accvgpr_read_b32 v73,a17
)A"
R"A(v_accvgpr_read_b32 v74,a18
v_accvgpr_read_b32 v75,a19
v_accvgpr_read_b32 v12,a20
v_accvgpr_read_b32 v13,a21
v_accvgpr_read_b32 v14,a22
v_accvgpr_read_b32 v15,a23
v_pk_mul_f32 v[72:73],v[20:21],v[72:73] op_sel:[0,0] op_sel_hi:[0,1]
v_pk_mul_f32 v[74:75],v[20:21],v[74:75] op_sel:[0,0] op_sel_hi:[0,1]
v_pk_mul_f32 v[12:13],v[20:21],v[12:13] op_sel:[0,0] op_sel_hi:[0,1]
v_pk_mul_f32 v[14:15],v[20:21],v[14:15] op_sel:[0,0] op_sel_hi:[0,1]
v_cvt_pk_bf16_f32 v72,v72,v73
v_cvt_pk_bf16_f32 v73,v74,v75
v_cvt_pk_bf16_f32 v74,v12,v13
v_cvt_pk_bf16_f32 v75,v14,v15
v_mfma_f32_16x16x32_bf16 v[184:187],a[168:171],v[196:199],v[184:187]
v_accvgpr_read_b32 v76,a24
v_accvgpr_read_b32 v77,a25
v_accvgpr_read_b32 v78,a26
v_accvgpr_read_b32 v79,a27
v_accvgpr_read_b32 v12,a28
v_accvgpr_read_b32 v13,a29
v_accvgpr_read_b32 v14,a30
v_accvgpr_read_b32 v15,a31
v_pk_mul_f32 v[76:77],v[20:21],v[76:77] op_sel:[0,0] op_sel_hi:[0,1]
v_pk_mul_f32 v[78:79],v[20:21],v[78:79] op_sel:[0,0] op_sel_hi:[0,1]
v_pk_mul_f32 v[12:13],v[20:21],v[12:13] op_sel:[0,0] op_sel_hi:[0,1]
v_pk_mul_f32 v[14:15],v[20:21],v[14:15] op_sel:[0,0] op_sel_hi:[0,1]
v_cvt_pk_bf16_f32 v76,v76,v77
v_cvt_pk_bf16_f32 v77,v78,v79
v_cvt_pk_bf16_f32 v78,v12,v13
v_cvt_pk_bf16_f32 v79,v14,v15
v_mfma_f32_16x16x32_bf16 v[184:187],a[172:175],v[228:231],v[184:187]
v_max3_f32 v44,v44,v45,v46
v_max3_f32 v44,v44,v47,s101
v_mfma_f32_16x16x32_bf16 v[188:191],a[188:191],v[228:231],v[36:39]
v_mov_b32 v48,0
v_mov_b32 v49,0
v_mfma_f32_16x16x32_bf16 v[188:191],a[184:187],v[196:199],v[188:191]
v_mov_b32 v50,0
v_mov_b32 v51,0
v_mfma_f32_16x16x32_bf16 v[188:191],a[180:183],v[164:167],v[188:191]
v_sub_f32 v47,0,v44
v_sub_f32 v46,0,v44
v_mfma_f32_16x16x32_bf16 v[188:191],a[176:179],v[132:135],v[188:191]
v_sub_f32 v45,0,v44
v_sub_f32 v44,0,v44
v_mfma_f32_16x16x32_bf16 a[96:99],a[192:195],v[232:235],a[96:99]
ds_read_b64_tr_b16 a[192:193],v2
ds_read_b64_tr_b16 a[194:195],v2 offset:4096
v_mfma_f32_16x16x32_bf16 a[96:99],a[196:199],v[236:239],a[96:99]
ds_read_b64_tr_b16 a[196:197],v2 offset:8192
v_add_f32 v52,v53,v52
v_mfma_f32_16x16x32_bf16 a[100:103],a[204:207],v[236:239],a[100:103]
buffer_load_dwordx4 v4,s[24:27],s47 offen offset:2048 nt lds
v_mfma_f32_16x16x32_bf16 a[100:103],a[200:203],v[232:235],a[100:103]
ds_read_b64_tr_b16 a[198:199],v2 offset:12288
ds_read_b64_tr_b16 a[204:205],v3 offset:8192
v_mfma_f32_16x16x32_bf16 a[104:107],a[208:211],v[232:235],a[104:107]
ds_read_b64_tr_b16 a[206:207],v3 offset:12288
v_max_f32 v21,0x00800000,v52
v_mfma_f32_16x16x32_bf16 a[104:107],a[212:215],v[236:239],a[104:107]
ds_read_b64_tr_b16 a[200:201],v3
v_accvgpr_read_b32 v80,a32
v_mfma_f32_16x16x32_bf16 a[108:111],a[220:223],v[236:239],a[108:111]
ds_read_b64_tr_b16 a[202:203],v3 offset:4096
v_accvgpr_read_b32 v81,a33
v_mfma_f32_16x16x32_bf16 a[108:111],a[216:219],v[232:235],a[108:111]
ds_read_b64_tr_b16 a[208:209],v2 offset:256
ds_read_b64_tr_b16 a[210:211],v2 offset:4352
v_mfma_f32_16x16x32_bf16 a[112:115],a[224:227],v[232:235],a[112:115]
ds_read_b64_tr_b16 a[212:213],v2 offset:8448
v_accvgpr_read_b32 v82,a34
v_mfma_f32_16x16x32_bf16 a[112:115],a[228:231],v[236:239],a[112:115]
ds_read_b64_tr_b16 a[214:215],v2 offset:12544
v_accvgpr_read_b32 v83,a35
v_mfma_f32_16x16x32_bf16 a[116:119],a[236:239],v[236:239],a[116:119]
ds_read_b64_tr_b16 a[220:221],v3 offset:8448
v_accvgpr_read_b32 v12,a38
v_mfma_f32_16x16x32_bf16 a[116:119],a[232:235],v[232:235],a[116:119]
ds_read_b64_tr_b16 a[222:223],v3 offset:12544
ds_read_b64_tr_b16 a[216:217],v3 offset:256
v_mfma_f32_16x16x32_bf16 a[120:123],a[240:243],v[232:235],a[120:123]
ds_read_b64_tr_b16 a[218:219],v3 offset:4352
v_accvgpr_read_b32 v13,a39
v_mfma_f32_16x16x32_bf16 a[120:123],a[244:247],v[236:239],a[120:123]
ds_read_b64_tr_b16 a[224:225],v2 offset:512
v_add_u32 v1,s48,v1
v_mfma_f32_16x16x32_bf16 a[124:127],a[252:255],v[236:239],a[124:127]
ds_read_b64_tr_b16 a[226:227],v2 offset:4608
v_mov_b32 v53,0
v_mfma_f32_16x16x32_bf16 a[124:127],a[248:251],v[232:235],a[124:127]
ds_read_b64_tr_b16 a[228:229],v2 offset:8704
ds_read_b64_tr_b16 a[230:231],v2 offset:12800
v_smfmac_f32_16x16x64_bf16 v[60:63],v[28:31],v[232:239],v27
ds_read_b64_tr_b16 a[236:237],v3 offset:8704
v_mfma_f32_16x16x32_bf16 v[200:203],a[128:131],v[144:147],v[40:43]
buffer_load_dwordx4 v5,s[24:27],s47 offen offset:3072 nt lds
v_mfma_f32_16x16x32_bf16 v[200:203],a[132:135],v[176:179],v[200:203]
s_add_u32 s47,s47,16384
ds_read_b64_tr_b16 a[238:239],v3 offset:12800
ds_read_b64_tr_b16 a[232:233],v3 offset:512
v_mfma_f32_16x16x32_bf16 v[200:203],a[136:139],v[208:211],v[200:203]
s_and_b32 s47,s47,0x3ffff
s_or_b32 s47,s47,s46
ds_read_b64_tr_b16 a[234:235],v3 offset:4608
v_mfma_f32_16x16x32_bf16 v[200:203],a[140:143],v[240:243],v[200:203]
s_and_b32 s46,s54,0xfffc0000
ds_read_b64_tr_b16 a[240:241],v2 offset:768
v_mov_b32 v54,0
v_mfma_f32_16x16x32_bf16 v[204:207],a[156:159],v[240:243],v[40:43]
ds_read_b64_tr_b16 a[242:243],v2 offset:4864
v_mov_b32 v55,0
v_mfma_f32_16x16x32_bf16 v[204:207],a[152:155],v[208:211],v[204:207]
ds_read_b64_tr_b16 a[244:245],v2 offset:8960
ds_read_b64_tr_b16 a[246:247],v2 offset:13056
v_mfma_f32_16x16x32_bf16 v[204:207],a[148:151],v[176:179],v[204:207]
ds_read_b64_tr_b16 a[252:253],v3 offset:8960
v_add_f32 v56,v57,v56
v_mfma_f32_16x16x32_bf16 v[204:207],a[144:147],v[144:147],v[204:207]
ds_read_b64_tr_b16 a[254:255],v3 offset:13056
v_max_f32 v22,0x00800000,v56
v_mfma_f32_16x16x32_bf16 v[216:219],a[160:163],v[144:147],v[40:43]
ds_read_b64_tr_b16 a[248:249],v3 offset:768
v_accvgpr_read_b32 v96,a64
v_mfma_f32_16x16x32_bf16 v[216:219],a[164:167],v[176:179],v[216:219]
ds_read_b64_tr_b16 a[250:251],v3 offset:4864
v_accvgpr_read_b32 v97,a65
v_mfma_f32_16x16x32_bf16 v[216:219],a[168:171],v[208:211],v[216:219]
v_exp_f32 v168,v168
v_mfma_f32_16x16x32_bf16 v[216:219],a[172:175],v[240:243],v[216:219]
v_exp_f32 v169,v169
v_mfma_f32_16x16x32_bf16 v[220:223],a[188:191],v[240:243],v[40:43]
v_exp_f32 v170,v170
v_mfma_f32_16x16x32_bf16 v[220:223],a[184:187],v[208:211],v[220:223]
v_exp_f32 v171,v171
v_mfma_f32_16x16x32_bf16 v[220:223],a[180:183],v[176:179],v[220:223]
v_cvt_pk_bf16_f32 v168,v168,v169
v_cvt_pk_bf16_f32 v169,v170,v171
v_mfma_f32_16x16x32_bf16 v[220:223],a[176:179],v[144:147],v[220:223]
s_waitcnt lgkmcnt(2)
v_exp_f32 v172,v172
v_mfma_f32_16x16x32_bf16 a[0:3],a[192:195],v[136:139],0
v_exp_f32 v173,v173
v_mfma_f32_16x16x32_bf16 a[0:3],a[196:199],v[140:143],a[0:3]
v_exp_f32 v174,v174
v_mfma_f32_16x16x32_bf16 a[4:7],a[204:207],v[140:143],0
v_exp_f32 v175,v175
v_mfma_f32_16x16x32_bf16 a[4:7],a[200:203],v[136:139],a[4:7]
s_memrealtime s[76:77]
v_cvt_pk_bf16_f32 v170,v172,v173
v_cvt_pk_bf16_f32 v171,v174,v175
v_mfma_f32_16x16x32_bf16 a[8:11],a[208:211],v[136:139],0
v_exp_f32 v184,v184
v_mfma_f32_16x16x32_bf16 a[8:11],a[212:215],v[140:143],a[8:11]
v_exp_f32 v185,v185
v_mfma_f32_16x16x32_bf16 a[12:15],a[220:223],v[140:143],0
v_exp_f32 v186,v186
v_mfma_f32_16x16x32_bf16 a[12:15],a[216:219],v[136:139],a[12:15]
v_exp_f32 v187,v187
v_mfma_f32_16x16x32_bf16 a[16:19],a[224:227],v[136:139],0
v_exp_f32 v188,v188
v_mfma_f32_16x16x32_bf16 a[16:19],a[228:231],v[140:143],a[16:19]
v_exp_f32 v189,v189
v_mfma_f32_16x16x32_bf16 a[20:23],a[236:239],v[140:143],0
s_waitcnt lgkmcnt(0)
v_exp_f32 v190,v190
v_mfma_f32_16x16x32_bf16 a[20:23],a[232:235],v[136:139],a[20:23]
s_sub_u32 s79,s78,s76
v_exp_f32 v191,v191
v_mfma_f32_16x16x32_bf16 a[24:27],a[240:243],v[136:139],0
s_add_u32 s78,s76,s60
v_rcp_f32 v21,v21
v_mfma_f32_16x16x32_bf16 a[24:27],a[244:247],v[140:143],a[24:27]
s_cmp_gt_i32 s79,s95
s_cbranch_scc1 L_ps5_%=
L_pr5_%=:
v_cvt_pk_bf16_f32 v172,v184,v185
v_mfma_f32_16x16x32_bf16 a[28:31],a[252:255],v[140:143],0
s_lshr_b32 s60,s94,2
v_cvt_pk_bf16_f32 v173,v186,v187
v_cvt_pk_bf16_f32 v174,v188,v189
v_mfma_f32_16x16x32_bf16 a[28:31],a[248:251],v[136:139],a[28:31]
s_max_i32 s60,s60,128
v_cvt_pk_bf16_f32 v175,v190,v191
v_mul_f32 v80,v21,v80
v_smfmac_f32_16x16x64_bf16 v[48:51],v[28:31],v[136:143],v27
v_mul_f32 v81,v21,v81
v_mfma_f32_16x16x32_bf16 v[232:235],a[128:131],v[148:151],v[44:47]
s_waitcnt vmcnt(8)
v_cvt_pk_bf16_f32 v80,v80,v81
v_mul_f32 v82,v21,v82
v_mfma_f32_16x16x32_bf16 v[232:235],a[132:135],v[180:183],v[232:235]
s_barrier
s_add_u32 m0,65536,s51
v_mul_f32 v83,v21,v83
v_mfma_f32_16x16x32_bf16 v[232:235],a[136:139],v[212:215],v[232:235]
v_cvt_pk_bf16_f32 v81,v82,v83
v_accvgpr_read_b32 v82,a36
v_mfma_f32_16x16x32_bf16 v[232:235],a[140:143],v[244:247],v[232:235]
v_accvgpr_read_b32 v83,a37
v_mul_f32 v82,v21,v82
v_mfma_f32_16x16x32_bf16 v[236:239],a[156:159],v[244:247],v[44:47]
buffer_load_dwordx4 v4,s[20:23],s47 offen nt lds
v_mfma_f32_16x16x32_bf16 v[236:239],a[152:155],v[212:215],v[236:239]
v_mul_f32 v83,v21,v83
v_mul_f32 v12,v21,v12
v_mfma_f32_16x16x32_bf16 v[236:239],a[148:151],v[180:183],v[236:239]
v_mul_f32 v13,v21,v13
v_cvt_pk_bf16_f32 v82,v82,v83
v_mfma_f32_16x16x32_bf16 v[236:239],a[144:147],v[148:151],v[236:239]
ds_read_b128 a[128:131],v1
v_cvt_pk_bf16_f32 v83,v12,v13
v_mfma_f32_16x16x32_bf16 v[248:251],a[160:163],v[148:151],v[44:47]
v_accvgpr_read_b32 v84,a40
v_accvgpr_read_b32 v85,a41
v_accvgpr_read_b32 v86,a42
v_accvgpr_read_b32 v87,a43
v_accvgpr_read_b32 v12,a44
v_accvgpr_read_b32 v13,a45
v_accvgpr_read_b32 v14,a46
v_accvgpr_read_b32 v15,a47
v_pk_mul_f32 v[84:85],v[20:21],v[84:85] op_sel:[1,0] op_sel_hi:[1,1]
v_pk_mul_f32 v[86:87],v[20:21],v[86:87] op_sel:[1,0] op_sel_hi:[1,1]
v_pk_mul_f32 v[12:13],v[20:21],v[12:13] op_sel:[1,0] op_sel_hi:[1,1]
v_pk_mul_f32 v[14:15],v[20:21],v[14:15] op_sel:[1,0] op_sel_hi:[1,1]
v_cvt_pk_bf16_f32 v84,v84,v85
v_cvt_pk_bf16_f32 v85,v86,v87
v_cvt_pk_bf16_f32 v86,v12,v13
v_cvt_pk_bf16_f32 v87,v14,v15
v_mfma_f32_16x16x32_bf16 v[248:251],a[164:167],v[180:183],v[248:251]
v_accvgpr_read_b32 v88,a48
v_accvgpr_read_b32 v89,a49
v_accvgpr_read_b32 v90,a50
v_accvgpr_read_b32 v91,a51
v_accvgpr_read_b32 v12,a52
v_accvgpr_read_b32 v13,a53
v_accvgpr_read_b32 v14,a54
v_accvgpr_read_b32 v15,a55
v_pk_mul_f32 v[88:89],v[20:21],v[88:89] op_sel:[1,0] op_sel_hi:[1,1]
v_pk_mul_f32 v[90:91],v[20:21],v[90:91] op_sel:[1,0] op_sel_hi:[1,1]
v_pk_mul_f32 v[12:13],v[20:21],v[12:13] op_sel:[1,0] op_sel_hi:[1,1]
v_pk_mul_f32 v[14:15],v[20:21],v[14:15] op_sel:[1,0] op_sel_hi:[1,1]
v_cvt_pk_bf16_f32 v88,v88,v89
v_cvt_pk_bf16_f32 v89,v90,v91
v_cvt_pk_bf16_f32 v90,v12,v13
v_cvt_pk_bf16_f32 v91,v14,v15
v_mfma_f32_16x16x32_bf16 v[248:251],a[168:171],v[212:215],v[248:251]
ds_read_b128 a[132:135],v1 offset:256
v_accvgpr_read_b32 v98,a66
v_mfma_f32_16x16x32_bf16 v[248:251],a[172:175],v[244:247],v[248:251]
v_accvgpr_read_b32 v92,a56
v_accvgpr_read_b32 v93,a57
v_accvgpr_read_b32 v94,a58
v_accvgpr_read_b32 v95,a59
v_accvgpr_read_b32 v12,a60
v_accvgpr_read_b32 v13,a61
v_accvgpr_read_b32 v14,a62
v_accvgpr_read_b32 v15,a63
v_pk_mul_f32 v[92:93],v[20:21],v[92:93] op_sel:[1,0] op_sel_hi:[1,1]
v_pk_mul_f32 v[94:95],v[20:21],v[94:95] op_sel:[1,0] op_sel_hi:[1,1]
v_pk_mul_f32 v[12:13],v[20:21],v[12:13] op_sel:[1,0] op_sel_hi:[1,1]
v_pk_mul_f32 v[14:15],v[20:21],v[14:15] op_sel:[1,0] op_sel_hi:[1,1]
v_cvt_pk_bf16_f32 v92,v92,v93
v_cvt_pk_bf16_f32 v93,v94,v95
v_cvt_pk_bf16_f32 v94,v12,v13
v_cvt_pk_bf16_f32 v95,v14,v15
v_mfma_f32_16x16x32_bf16 v[252:255],a[188:191],v[244:247],v[44:47]
ds_read_b128 a[136:139],v1 offset:512
v_accvgpr_read_b32 v99,a67
v_mfma_f32_16x16x32_bf16 v[252:255],a[184:187],v[212:215],v[252:255]
v_cmp_nlt_f32 s[58:59],v52,s63
s_nop 1
s_or_b64 s[56:57],s[56:57],s[58:59]
v_mfma_f32_16x16x32_bf16 v[252:255],a[180:183],v[180:183],v[252:255]
ds_read_b128 a[140:143],v1 offset:768
v_mov_b32 v52,0
v_mfma_f32_16x16x32_bf16 v[252:255],a[176:179],v[148:151],v[252:255]
v_exp_f32 v200,v200
v_mfma_f32_16x16x32_bf16 a[32:35],a[192:195],v[168:171],0
ds_read_b128 a[156:159],v1 offset:4864
v_accvgpr_read_b32 v14,a70
v_mfma_f32_16x16x32_bf16 a[32:35],a[196:199],v[172:175],a[32:35]
v_exp_f32 v201,v201
v_mfma_f32_16x16x32_bf16 a[36:39],a[204:207],v[172:175],0
buffer_load_dwordx4 v4,s[20:23],s47 offen offset:1024 nt lds
v_mfma_f32_16x16x32_bf16 a[36:39],a[200:203],v[168:171],a[36:39]
ds_read_b128 a[152:155],v1 offset:4608
)A"
R"A(v_cvt_pk_bf16_f32 v200,v200,v201
v_mfma_f32_16x16x32_bf16 a[40:43],a[208:211],v[168:171],0
v_exp_f32 v202,v202
v_mfma_f32_16x16x32_bf16 a[40:43],a[212:215],v[172:175],a[40:43]
ds_read_b128 a[148:151],v1 offset:4352
v_accvgpr_read_b32 v15,a71
v_mfma_f32_16x16x32_bf16 a[44:47],a[220:223],v[172:175],0
v_exp_f32 v203,v203
v_mfma_f32_16x16x32_bf16 a[44:47],a[216:219],v[168:171],a[44:47]
ds_read_b128 a[144:147],v1 offset:4096
v_cvt_pk_bf16_f32 v201,v202,v203
v_mfma_f32_16x16x32_bf16 a[48:51],a[224:227],v[168:171],0
v_exp_f32 v204,v204
v_mfma_f32_16x16x32_bf16 a[48:51],a[228:231],v[172:175],a[48:51]
ds_read_b128 a[160:163],v1 offset:8192
v_mov_b32 v57,0
v_mfma_f32_16x16x32_bf16 a[52:55],a[236:239],v[172:175],0
v_exp_f32 v205,v205
v_mfma_f32_16x16x32_bf16 a[52:55],a[232:235],v[168:171],a[52:55]
ds_read_b128 a[164:167],v1 offset:8448
v_cvt_pk_bf16_f32 v202,v204,v205
v_mfma_f32_16x16x32_bf16 a[56:59],a[240:243],v[168:171],0
v_exp_f32 v206,v206
v_mfma_f32_16x16x32_bf16 a[56:59],a[244:247],v[172:175],a[56:59]
ds_read_b128 a[168:171],v1 offset:8704
v_mov_b32 v58,0
v_mfma_f32_16x16x32_bf16 a[60:63],a[252:255],v[172:175],0
v_exp_f32 v207,v207
v_mfma_f32_16x16x32_bf16 a[60:63],a[248:251],v[168:171],a[60:63]
ds_read_b128 a[172:175],v1 offset:8960
v_cvt_pk_bf16_f32 v203,v206,v207
v_smfmac_f32_16x16x64_bf16 v[52:55],v[28:31],v[168:175],v27
s_waitcnt lgkmcnt(4)
v_mov_b32 v59,0
v_mfma_f32_16x16x32_bf16 v[136:139],a[128:131],v[128:131],v[32:35]
buffer_load_dwordx4 v5,s[20:23],s47 offen offset:2048 nt lds
v_mfma_f32_16x16x32_bf16 v[136:139],a[132:135],v[160:163],v[136:139]
ds_read_b128 a[188:191],v1 offset:13056
v_add_f32 v60,v61,v60
v_mfma_f32_16x16x32_bf16 v[136:139],a[136:139],v[192:195],v[136:139]
v_exp_f32 v216,v216
v_mfma_f32_16x16x32_bf16 v[136:139],a[140:143],v[224:227],v[136:139]
ds_read_b128 a[184:187],v1 offset:12800
v_max_f32 v23,0x00800000,v60
v_mfma_f32_16x16x32_bf16 v[140:143],a[156:159],v[224:227],v[32:35]
v_exp_f32 v217,v217
v_mfma_f32_16x16x32_bf16 v[140:143],a[152:155],v[192:195],v[140:143]
ds_read_b128 a[180:183],v1 offset:12544
v_cvt_pk_bf16_f32 v204,v216,v217
v_mfma_f32_16x16x32_bf16 v[140:143],a[148:151],v[160:163],v[140:143]
ds_read_b128 a[176:179],v1 offset:12288
v_mfma_f32_16x16x32_bf16 v[140:143],a[144:147],v[128:131],v[140:143]
s_waitcnt lgkmcnt(4)
v_exp_f32 v218,v218
v_mfma_f32_16x16x32_bf16 v[152:155],a[160:163],v[128:131],v[32:35]
v_exp_f32 v219,v219
v_mfma_f32_16x16x32_bf16 v[152:155],a[164:167],v[160:163],v[152:155]
v_exp_f32 v220,v220
v_mfma_f32_16x16x32_bf16 v[152:155],a[168:171],v[192:195],v[152:155]
v_exp_f32 v221,v221
v_mfma_f32_16x16x32_bf16 v[152:155],a[172:175],v[224:227],v[152:155]
s_waitcnt lgkmcnt(2)
v_exp_f32 v222,v222
v_mfma_f32_16x16x32_bf16 v[156:159],a[188:191],v[224:227],v[32:35]
v_exp_f32 v223,v223
v_mfma_f32_16x16x32_bf16 v[156:159],a[184:187],v[192:195],v[156:159]
s_waitcnt lgkmcnt(1)
v_rcp_f32 v22,v22
v_mfma_f32_16x16x32_bf16 v[156:159],a[180:183],v[160:163],v[156:159]
s_waitcnt lgkmcnt(0)
v_cvt_pk_bf16_f32 v205,v218,v219
v_cvt_pk_bf16_f32 v206,v220,v221
v_cvt_pk_bf16_f32 v207,v222,v223
v_mfma_f32_16x16x32_bf16 v[156:159],a[176:179],v[128:131],v[156:159]
v_mul_f32 v96,v22,v96
v_mul_f32 v97,v22,v97
v_mfma_f32_16x16x32_bf16 a[64:67],a[192:195],v[200:203],0
buffer_load_dwordx4 v5,s[20:23],s47 offen offset:3072 nt lds
v_cvt_pk_bf16_f32 v96,v96,v97
v_mul_f32 v98,v22,v98
v_mul_f32 v99,v22,v99
v_cvt_pk_bf16_f32 v97,v98,v99
v_mfma_f32_16x16x32_bf16 a[64:67],a[196:199],v[204:207],a[64:67]
v_accvgpr_read_b32 v98,a68
v_accvgpr_read_b32 v99,a69
v_mul_f32 v98,v22,v98
v_mul_f32 v99,v22,v99
v_mfma_f32_16x16x32_bf16 a[68:71],a[204:207],v[204:207],0
v_mul_f32 v14,v22,v14
v_mul_f32 v15,v22,v15
v_cvt_pk_bf16_f32 v98,v98,v99
v_cvt_pk_bf16_f32 v99,v14,v15
v_mfma_f32_16x16x32_bf16 a[68:71],a[200:203],v[200:203],a[68:71]
v_accvgpr_read_b32 v100,a72
v_accvgpr_read_b32 v101,a73
v_accvgpr_read_b32 v102,a74
v_accvgpr_read_b32 v103,a75
v_accvgpr_read_b32 v12,a76
v_accvgpr_read_b32 v13,a77
v_accvgpr_read_b32 v14,a78
v_accvgpr_read_b32 v15,a79
v_pk_mul_f32 v[100:101],v[22:23],v[100:101] op_sel:[0,0] op_sel_hi:[0,1]
v_pk_mul_f32 v[102:103],v[22:23],v[102:103] op_sel:[0,0] op_sel_hi:[0,1]
v_pk_mul_f32 v[12:13],v[22:23],v[12:13] op_sel:[0,0] op_sel_hi:[0,1]
v_pk_mul_f32 v[14:15],v[22:23],v[14:15] op_sel:[0,0] op_sel_hi:[0,1]
v_cvt_pk_bf16_f32 v100,v100,v101
v_cvt_pk_bf16_f32 v101,v102,v103
v_cvt_pk_bf16_f32 v102,v12,v13
v_cvt_pk_bf16_f32 v103,v14,v15
v_mfma_f32_16x16x32_bf16 a[72:75],a[208:211],v[200:203],0
v_accvgpr_read_b32 v104,a80
v_accvgpr_read_b32 v105,a81
v_accvgpr_read_b32 v106,a82
v_accvgpr_read_b32 v107,a83
v_accvgpr_read_b32 v12,a84
v_accvgpr_read_b32 v13,a85
v_accvgpr_read_b32 v14,a86
v_accvgpr_read_b32 v15,a87
v_pk_mul_f32 v[104:105],v[22:23],v[104:105] op_sel:[0,0] op_sel_hi:[0,1]
v_pk_mul_f32 v[106:107],v[22:23],v[106:107] op_sel:[0,0] op_sel_hi:[0,1]
v_pk_mul_f32 v[12:13],v[22:23],v[12:13] op_sel:[0,0] op_sel_hi:[0,1]
v_pk_mul_f32 v[14:15],v[22:23],v[14:15] op_sel:[0,0] op_sel_hi:[0,1]
v_cvt_pk_bf16_f32 v104,v104,v105
v_cvt_pk_bf16_f32 v105,v106,v107
v_cvt_pk_bf16_f32 v106,v12,v13
v_cvt_pk_bf16_f32 v107,v14,v15
v_mfma_f32_16x16x32_bf16 a[72:75],a[212:215],v[204:207],a[72:75]
v_accvgpr_read_b32 v108,a88
v_accvgpr_read_b32 v109,a89
v_accvgpr_read_b32 v110,a90
v_accvgpr_read_b32 v111,a91
v_accvgpr_read_b32 v12,a92
v_accvgpr_read_b32 v13,a93
v_accvgpr_read_b32 v14,a94
v_accvgpr_read_b32 v15,a95
v_pk_mul_f32 v[108:109],v[22:23],v[108:109] op_sel:[0,0] op_sel_hi:[0,1]
v_pk_mul_f32 v[110:111],v[22:23],v[110:111] op_sel:[0,0] op_sel_hi:[0,1]
v_pk_mul_f32 v[12:13],v[22:23],v[12:13] op_sel:[0,0] op_sel_hi:[0,1]
v_pk_mul_f32 v[14:15],v[22:23],v[14:15] op_sel:[0,0] op_sel_hi:[0,1]
v_cvt_pk_bf16_f32 v108,v108,v109
v_cvt_pk_bf16_f32 v109,v110,v111
v_cvt_pk_bf16_f32 v110,v12,v13
v_cvt_pk_bf16_f32 v111,v14,v15
v_mfma_f32_16x16x32_bf16 a[76:79],a[220:223],v[204:207],0
v_cmp_nlt_f32 s[58:59],v56,s63
s_nop 1
s_or_b64 s[56:57],s[56:57],s[58:59]
v_mfma_f32_16x16x32_bf16 a[76:79],a[216:219],v[200:203],a[76:79]
v_mov_b32 v56,0
v_accvgpr_read_b32 v112,a96
v_mfma_f32_16x16x32_bf16 a[80:83],a[224:227],v[200:203],0
s_add_u32 m0,65536,s52
v_exp_f32 v232,v232
v_mfma_f32_16x16x32_bf16 a[80:83],a[228:231],v[204:207],a[80:83]
v_exp_f32 v233,v233
v_mfma_f32_16x16x32_bf16 a[84:87],a[236:239],v[204:207],0
v_exp_f32 v234,v234
v_mfma_f32_16x16x32_bf16 a[84:87],a[232:235],v[200:203],a[84:87]
buffer_load_dwordx4 v4,s[24:27],s47 offen nt lds
v_mfma_f32_16x16x32_bf16 a[88:91],a[240:243],v[200:203],0
v_exp_f32 v235,v235
v_mfma_f32_16x16x32_bf16 a[88:91],a[244:247],v[204:207],a[88:91]
v_cvt_pk_bf16_f32 v232,v232,v233
v_cvt_pk_bf16_f32 v233,v234,v235
v_mfma_f32_16x16x32_bf16 a[92:95],a[252:255],v[204:207],0
v_exp_f32 v236,v236
v_mfma_f32_16x16x32_bf16 a[92:95],a[248:251],v[200:203],a[92:95]
v_exp_f32 v237,v237
v_smfmac_f32_16x16x64_bf16 v[56:59],v[28:31],v[200:207],v27
v_cvt_pk_bf16_f32 v234,v236,v237
v_mfma_f32_16x16x32_bf16 v[168:171],a[128:131],v[132:135],v[36:39]
v_exp_f32 v238,v238
v_mfma_f32_16x16x32_bf16 v[168:171],a[132:135],v[164:167],v[168:171]
v_exp_f32 v239,v239
v_mfma_f32_16x16x32_bf16 v[168:171],a[136:139],v[196:199],v[168:171]
v_cvt_pk_bf16_f32 v235,v238,v239
v_accvgpr_read_b32 v113,a97
v_mfma_f32_16x16x32_bf16 v[168:171],a[140:143],v[228:231],v[168:171]
v_exp_f32 v248,v248
v_mfma_f32_16x16x32_bf16 v[172:175],a[156:159],v[228:231],v[36:39]
v_exp_f32 v249,v249
v_mfma_f32_16x16x32_bf16 v[172:175],a[152:155],v[196:199],v[172:175]
v_exp_f32 v250,v250
v_mfma_f32_16x16x32_bf16 v[172:175],a[148:151],v[164:167],v[172:175]
v_exp_f32 v251,v251
v_mfma_f32_16x16x32_bf16 v[172:175],a[144:147],v[132:135],v[172:175]
v_exp_f32 v252,v252
v_mfma_f32_16x16x32_bf16 v[184:187],a[160:163],v[132:135],v[36:39]
buffer_load_dwordx4 v5,s[24:27],s47 offen offset:1024 nt lds
v_mfma_f32_16x16x32_bf16 v[184:187],a[164:167],v[164:167],v[184:187]
v_exp_f32 v253,v253
v_mfma_f32_16x16x32_bf16 v[184:187],a[168:171],v[196:199],v[184:187]
v_exp_f32 v254,v254
v_mfma_f32_16x16x32_bf16 v[184:187],a[172:175],v[228:231],v[184:187]
v_exp_f32 v255,v255
v_mfma_f32_16x16x32_bf16 v[188:191],a[188:191],v[228:231],v[36:39]
v_cvt_pk_bf16_f32 v236,v248,v249
v_cvt_pk_bf16_f32 v237,v250,v251
v_mfma_f32_16x16x32_bf16 v[188:191],a[184:187],v[196:199],v[188:191]
v_cvt_pk_bf16_f32 v238,v252,v253
v_cvt_pk_bf16_f32 v239,v254,v255
v_mfma_f32_16x16x32_bf16 v[188:191],a[180:183],v[164:167],v[188:191]
v_rcp_f32 v23,v23
v_mfma_f32_16x16x32_bf16 v[188:191],a[176:179],v[132:135],v[188:191]
v_accvgpr_read_b32 v114,a98
v_accvgpr_read_b32 v115,a99
v_mfma_f32_16x16x32_bf16 a[96:99],a[192:195],v[232:235],0
v_mul_f32 v66,v20,v66
v_mul_f32 v67,v20,v67
v_mul_f32 v112,v23,v112
v_mul_f32 v113,v23,v113
v_mul_f32 v16,v20,v16
v_mul_f32 v17,v20,v17
v_cvt_pk_bf16_f32 v66,v66,v67
v_cvt_pk_bf16_f32 v112,v112,v113
v_mul_f32 v114,v23,v114
v_mul_f32 v115,v23,v115
v_mfma_f32_16x16x32_bf16 a[96:99],a[196:199],v[236:239],a[96:99]
v_cvt_pk_bf16_f32 v67,v16,v17
v_cvt_pk_bf16_f32 v113,v114,v115
v_accvgpr_read_b32 v16,a102
v_accvgpr_read_b32 v17,a103
v_accvgpr_read_b32 v114,a100
v_accvgpr_read_b32 v115,a101
v_mfma_f32_16x16x32_bf16 a[100:103],a[204:207],v[236:239],0
v_accvgpr_read_b32 v116,a104
v_accvgpr_read_b32 v117,a105
v_accvgpr_read_b32 v118,a106
v_accvgpr_read_b32 v119,a107
v_accvgpr_read_b32 v12,a108
v_accvgpr_read_b32 v13,a109
v_accvgpr_read_b32 v14,a110
v_accvgpr_read_b32 v15,a111
v_pk_mul_f32 v[116:117],v[22:23],v[116:117] op_sel:[1,0] op_sel_hi:[1,1]
v_pk_mul_f32 v[118:119],v[22:23],v[118:119] op_sel:[1,0] op_sel_hi:[1,1]
v_pk_mul_f32 v[12:13],v[22:23],v[12:13] op_sel:[1,0] op_sel_hi:[1,1]
v_pk_mul_f32 v[14:15],v[22:23],v[14:15] op_sel:[1,0] op_sel_hi:[1,1]
v_cvt_pk_bf16_f32 v116,v116,v117
v_cvt_pk_bf16_f32 v117,v118,v119
v_cvt_pk_bf16_f32 v118,v12,v13
v_cvt_pk_bf16_f32 v119,v14,v15
v_mfma_f32_16x16x32_bf16 a[100:103],a[200:203],v[232:235],a[100:103]
v_accvgpr_read_b32 v120,a112
v_accvgpr_read_b32 v121,a113
v_accvgpr_read_b32 v122,a114
v_accvgpr_read_b32 v123,a115
v_accvgpr_read_b32 v12,a116
v_accvgpr_read_b32 v13,a117
v_accvgpr_read_b32 v14,a118
v_accvgpr_read_b32 v15,a119
v_pk_mul_f32 v[120:121],v[22:23],v[120:121] op_sel:[1,0] op_sel_hi:[1,1]
v_pk_mul_f32 v[122:123],v[22:23],v[122:123] op_sel:[1,0] op_sel_hi:[1,1]
v_pk_mul_f32 v[12:13],v[22:23],v[12:13] op_sel:[1,0] op_sel_hi:[1,1]
v_pk_mul_f32 v[14:15],v[22:23],v[14:15] op_sel:[1,0] op_sel_hi:[1,1]
v_cvt_pk_bf16_f32 v120,v120,v121
v_cvt_pk_bf16_f32 v121,v122,v123
v_cvt_pk_bf16_f32 v122,v12,v13
v_cvt_pk_bf16_f32 v123,v14,v15
v_mfma_f32_16x16x32_bf16 a[104:107],a[208:211],v[232:235],0
v_accvgpr_read_b32 v124,a120
v_accvgpr_read_b32 v125,a121
v_accvgpr_read_b32 v126,a122
v_accvgpr_read_b32 v127,a123
v_accvgpr_read_b32 v12,a124
v_accvgpr_read_b32 v13,a125
v_accvgpr_read_b32 v14,a126
v_accvgpr_read_b32 v15,a127
v_pk_mul_f32 v[124:125],v[22:23],v[124:125] op_sel:[1,0] op_sel_hi:[1,1]
v_pk_mul_f32 v[126:127],v[22:23],v[126:127] op_sel:[1,0] op_sel_hi:[1,1]
v_pk_mul_f32 v[12:13],v[22:23],v[12:13] op_sel:[1,0] op_sel_hi:[1,1]
v_pk_mul_f32 v[14:15],v[22:23],v[14:15] op_sel:[1,0] op_sel_hi:[1,1]
v_cvt_pk_bf16_f32 v124,v124,v125
v_cvt_pk_bf16_f32 v125,v126,v127
v_cvt_pk_bf16_f32 v126,v12,v13
v_cvt_pk_bf16_f32 v127,v14,v15
v_mfma_f32_16x16x32_bf16 a[104:107],a[212:215],v[236:239],a[104:107]
v_cmp_nlt_f32 s[58:59],v60,s63
s_nop 1
s_or_b64 s[56:57],s[56:57],s[58:59]
v_mfma_f32_16x16x32_bf16 a[108:111],a[220:223],v[236:239],0
buffer_load_dwordx4 v4,s[24:27],s47 offen offset:2048 nt lds
v_mfma_f32_16x16x32_bf16 a[108:111],a[216:219],v[232:235],a[108:111]
v_mov_b32 v60,0
v_mov_b32 v61,0
v_mfma_f32_16x16x32_bf16 a[112:115],a[224:227],v[232:235],0
v_mov_b32 v62,0
v_mov_b32 v63,0
v_mfma_f32_16x16x32_bf16 a[112:115],a[228:231],v[236:239],a[112:115]
v_add_u32 v2,s48,v2
v_add_u32 v3,s48,v3
v_mfma_f32_16x16x32_bf16 a[116:119],a[236:239],v[236:239],0
s_mov_b32 s48,32768
ds_read_b64_tr_b16 a[192:193],v2
ds_read_b64_tr_b16 a[194:195],v2 offset:4096
v_mfma_f32_16x16x32_bf16 a[116:119],a[232:235],v[232:235],a[116:119]
ds_read_b64_tr_b16 a[196:197],v2 offset:8192
)A"
R"A(v_mul_f32 v114,v23,v114
v_mfma_f32_16x16x32_bf16 a[120:123],a[240:243],v[232:235],0
ds_read_b64_tr_b16 a[198:199],v2 offset:12288
v_mul_f32 v115,v23,v115
v_mfma_f32_16x16x32_bf16 a[120:123],a[244:247],v[236:239],a[120:123]
ds_read_b64_tr_b16 a[204:205],v3 offset:8192
v_mul_f32 v16,v23,v16
v_mfma_f32_16x16x32_bf16 a[124:127],a[252:255],v[236:239],0
ds_read_b64_tr_b16 a[206:207],v3 offset:12288
ds_read_b64_tr_b16 a[200:201],v3
v_mfma_f32_16x16x32_bf16 a[124:127],a[248:251],v[232:235],a[124:127]
ds_read_b64_tr_b16 a[202:203],v3 offset:4096
v_mul_f32 v17,v23,v17
v_smfmac_f32_16x16x64_bf16 v[60:63],v[28:31],v[232:239],v27
ds_read_b64_tr_b16 a[208:209],v2 offset:256
v_mfma_f32_16x16x32_bf16 v[200:203],a[128:131],v[144:147],v[40:43]
v_exp_f32 v136,v136
v_mfma_f32_16x16x32_bf16 v[200:203],a[132:135],v[176:179],v[200:203]
v_exp_f32 v137,v137
v_mfma_f32_16x16x32_bf16 v[200:203],a[136:139],v[208:211],v[200:203]
ds_read_b64_tr_b16 a[210:211],v2 offset:4352
v_cvt_pk_bf16_f32 v136,v136,v137
v_mfma_f32_16x16x32_bf16 v[200:203],a[140:143],v[240:243],v[200:203]
buffer_load_dwordx4 v5,s[24:27],s47 offen offset:3072 nt lds
v_mfma_f32_16x16x32_bf16 v[204:207],a[156:159],v[240:243],v[40:43]
s_add_u32 s47,s47,16384
v_exp_f32 v138,v138
v_mfma_f32_16x16x32_bf16 v[204:207],a[152:155],v[208:211],v[204:207]
s_and_b32 s47,s47,0x3ffff
v_exp_f32 v139,v139
v_mfma_f32_16x16x32_bf16 v[204:207],a[148:151],v[176:179],v[204:207]
s_or_b32 s47,s47,s46
v_exp_f32 v140,v140
v_mfma_f32_16x16x32_bf16 v[204:207],a[144:147],v[144:147],v[204:207]
v_exp_f32 v141,v141
v_mfma_f32_16x16x32_bf16 v[216:219],a[160:163],v[144:147],v[40:43]
v_exp_f32 v142,v142
v_mfma_f32_16x16x32_bf16 v[216:219],a[164:167],v[176:179],v[216:219]
v_exp_f32 v143,v143
v_mfma_f32_16x16x32_bf16 v[216:219],a[168:171],v[208:211],v[216:219]
ds_read_b64_tr_b16 a[212:213],v2 offset:8448
ds_read_b64_tr_b16 a[214:215],v2 offset:12544
v_mfma_f32_16x16x32_bf16 v[216:219],a[172:175],v[240:243],v[216:219]
v_cvt_pk_bf16_f32 v137,v138,v139
v_cvt_pk_bf16_f32 v138,v140,v141
v_mfma_f32_16x16x32_bf16 v[220:223],a[188:191],v[240:243],v[40:43]
ds_read_b64_tr_b16 a[220:221],v3 offset:8448
v_cvt_pk_bf16_f32 v139,v142,v143
v_mfma_f32_16x16x32_bf16 v[220:223],a[184:187],v[208:211],v[220:223]
ds_read_b64_tr_b16 a[222:223],v3 offset:12544
v_exp_f32 v152,v152
v_exp_f32 v153,v153
v_exp_f32 v154,v154
v_exp_f32 v155,v155
v_exp_f32 v156,v156
v_exp_f32 v157,v157
v_exp_f32 v158,v158
v_exp_f32 v159,v159
v_mfma_f32_16x16x32_bf16 v[220:223],a[180:183],v[176:179],v[220:223]
ds_read_b64_tr_b16 a[216:217],v3 offset:256
ds_read_b64_tr_b16 a[218:219],v3 offset:4352
v_cvt_pk_bf16_f32 v140,v152,v153
v_cvt_pk_bf16_f32 v141,v154,v155
v_cvt_pk_bf16_f32 v142,v156,v157
v_cvt_pk_bf16_f32 v143,v158,v159
v_mfma_f32_16x16x32_bf16 v[220:223],a[176:179],v[144:147],v[220:223]
s_waitcnt lgkmcnt(6)
ds_read_b64_tr_b16 a[224:225],v2 offset:512
ds_read_b64_tr_b16 a[226:227],v2 offset:4608
v_mfma_f32_16x16x32_bf16 a[0:3],a[192:195],v[136:139],a[0:3]
ds_read_b64_tr_b16 a[228:229],v2 offset:8704
ds_read_b64_tr_b16 a[230:231],v2 offset:12800
v_mfma_f32_16x16x32_bf16 a[0:3],a[196:199],v[140:143],a[0:3]
ds_read_b64_tr_b16 a[236:237],v3 offset:8704
ds_read_b64_tr_b16 a[238:239],v3 offset:12800
v_mfma_f32_16x16x32_bf16 a[4:7],a[204:207],v[140:143],a[4:7]
ds_read_b64_tr_b16 a[232:233],v3 offset:512
ds_read_b64_tr_b16 a[234:235],v3 offset:4608
v_mfma_f32_16x16x32_bf16 a[4:7],a[200:203],v[136:139],a[4:7]
ds_read_b64_tr_b16 a[240:241],v2 offset:768
ds_read_b64_tr_b16 a[242:243],v2 offset:4864
v_mfma_f32_16x16x32_bf16 a[8:11],a[208:211],v[136:139],a[8:11]
s_memrealtime s[76:77]
ds_read_b64_tr_b16 a[244:245],v2 offset:8960
ds_read_b64_tr_b16 a[246:247],v2 offset:13056
s_waitcnt lgkmcnt(15)
v_mfma_f32_16x16x32_bf16 a[8:11],a[212:215],v[140:143],a[8:11]
s_waitcnt lgkmcnt(14)
ds_read_b64_tr_b16 a[252:253],v3 offset:8960
ds_read_b64_tr_b16 a[254:255],v3 offset:13056
v_mfma_f32_16x16x32_bf16 a[12:15],a[220:223],v[140:143],a[12:15]
s_waitcnt lgkmcnt(14)
ds_read_b64_tr_b16 a[248:249],v3 offset:768
ds_read_b64_tr_b16 a[250:251],v3 offset:4864
v_mfma_f32_16x16x32_bf16 a[12:15],a[216:219],v[136:139],a[12:15]
s_waitcnt lgkmcnt(14)
v_mfma_f32_16x16x32_bf16 a[16:19],a[224:227],v[136:139],a[16:19]
s_waitcnt lgkmcnt(12)
v_mfma_f32_16x16x32_bf16 a[16:19],a[228:231],v[140:143],a[16:19]
s_waitcnt lgkmcnt(10)
v_mfma_f32_16x16x32_bf16 a[20:23],a[236:239],v[140:143],a[20:23]
s_waitcnt lgkmcnt(0)
v_mfma_f32_16x16x32_bf16 a[20:23],a[232:235],v[136:139],a[20:23]
s_sub_u32 s79,s78,s76
v_exp_f32 v168,v168
v_mfma_f32_16x16x32_bf16 a[24:27],a[240:243],v[136:139],a[24:27]
s_add_u32 s78,s76,s60
v_exp_f32 v169,v169
v_mfma_f32_16x16x32_bf16 a[24:27],a[244:247],v[140:143],a[24:27]
s_cmp_gt_i32 s79,s95
s_cbranch_scc1 L_ps6_%=
L_pr6_%=:
v_exp_f32 v170,v170
v_exp_f32 v171,v171
v_mfma_f32_16x16x32_bf16 a[28:31],a[252:255],v[140:143],a[28:31]
v_cvt_pk_bf16_f32 v114,v114,v115
v_cvt_pk_bf16_f32 v168,v168,v169
v_exp_f32 v172,v172
v_exp_f32 v173,v173
v_exp_f32 v174,v174
v_exp_f32 v175,v175
v_cvt_pk_bf16_f32 v115,v16,v17
v_cvt_pk_bf16_f32 v169,v170,v171
v_exp_f32 v184,v184
v_exp_f32 v185,v185
v_exp_f32 v186,v186
v_exp_f32 v187,v187
v_exp_f32 v188,v188
v_exp_f32 v189,v189
v_exp_f32 v190,v190
v_exp_f32 v191,v191
v_mfma_f32_16x16x32_bf16 a[28:31],a[248:251],v[136:139],a[28:31]
s_cmp_lg_u64 s[56:57],0
s_cbranch_scc1 L_slow0_%=
L_sret0_%=:
v_cvt_pk_bf16_f32 v170,v172,v173
v_cvt_pk_bf16_f32 v171,v174,v175
v_add_u32 v1,s48,v1
v_cvt_pk_bf16_f32 v172,v184,v185
v_cvt_pk_bf16_f32 v173,v186,v187
v_cvt_pk_bf16_f32 v174,v188,v189
v_cvt_pk_bf16_f32 v175,v190,v191
v_smfmac_f32_16x16x64_bf16 v[48:51],v[28:31],v[136:143],v27
s_mov_b32 s53,11
s_cmp_eq_u32 s40,3
s_cbranch_scc0 L_att1_%=
s_cmp_gt_i32 s18,0
s_cbranch_scc1 L_att1v0_%=
s_cmp_gt_i32 s19,0
s_cbranch_scc1 L_att1v1_%=
s_cmp_gt_i32 s100,0
s_cbranch_scc1 L_att1v2_%=
.p2align 5
L_att1v3_%=:
s_waitcnt vmcnt(8) lgkmcnt(0)
v_exp_f32 v200,v200
s_barrier
s_add_u32 m0,s49,s51
ds_read_b128 a[128:131],v1
s_mov_b32 s90,L_out_end_%=-L_pc_%=+128
v_exp_f32 v201,v201
s_lshr_b32 s45,s45,1
ds_read_b128 a[132:135],v1 offset:256
v_cvt_pk_bf16_f32 v200,v200,v201
buffer_load_dwordx4 v4,s[20:23],s47 offen nt lds
ds_read_b128 a[136:139],v1 offset:512
v_add_u32 v2,s48,v2
v_exp_f32 v202,v202
s_add_u32 s62,s50,32768
ds_read_b128 a[140:143],v1 offset:768
v_add_u32 v3,s48,v3
s_and_b32 s62,s62,131071
v_exp_f32 v203,v203
s_add_u32 s54,s54,16384
ds_read_b128 a[156:159],v1 offset:4864
v_cvt_pk_bf16_f32 v201,v202,v203
s_sub_u32 s48,s62,s50
v_exp_f32 v204,v204
s_sub_u32 s53,s53,1
v_exp_f32 v205,v205
s_and_b32 s46,s54,0xfffc0000
ds_read_b128 a[152:155],v1 offset:4608
v_cvt_pk_bf16_f32 v202,v204,v205
s_mov_b32 s50,s62
v_exp_f32 v206,v206
v_exp_f32 v207,v207
ds_read_b128 a[148:151],v1 offset:4352
v_cvt_pk_bf16_f32 v203,v206,v207
v_mfma_f32_16x16x32_bf16 a[32:35],a[192:195],v[168:171],a[32:35]
v_exp_f32 v216,v216
v_mfma_f32_16x16x32_bf16 a[32:35],a[196:199],v[172:175],a[32:35]
ds_read_b128 a[144:147],v1 offset:4096
v_mfma_f32_16x16x32_bf16 a[36:39],a[204:207],v[172:175],a[36:39]
buffer_load_dwordx4 v4,s[20:23],s47 offen offset:1024 nt lds
v_mfma_f32_16x16x32_bf16 a[36:39],a[200:203],v[168:171],a[36:39]
ds_read_b128 a[160:163],v1 offset:8192
v_mfma_f32_16x16x32_bf16 a[40:43],a[208:211],v[168:171],a[40:43]
v_exp_f32 v217,v217
v_mfma_f32_16x16x32_bf16 a[40:43],a[212:215],v[172:175],a[40:43]
ds_read_b128 a[164:167],v1 offset:8448
v_cvt_pk_bf16_f32 v204,v216,v217
v_mfma_f32_16x16x32_bf16 a[44:47],a[220:223],v[172:175],a[44:47]
v_exp_f32 v218,v218
v_mfma_f32_16x16x32_bf16 a[44:47],a[216:219],v[168:171],a[44:47]
ds_read_b128 a[168:171],v1 offset:8704
v_mfma_f32_16x16x32_bf16 a[48:51],a[224:227],v[168:171],a[48:51]
v_exp_f32 v219,v219
v_mfma_f32_16x16x32_bf16 a[48:51],a[228:231],v[172:175],a[48:51]
ds_read_b128 a[172:175],v1 offset:8960
v_cvt_pk_bf16_f32 v205,v218,v219
v_mfma_f32_16x16x32_bf16 a[52:55],a[236:239],v[172:175],a[52:55]
v_exp_f32 v220,v220
v_mfma_f32_16x16x32_bf16 a[52:55],a[232:235],v[168:171],a[52:55]
ds_read_b128 a[188:191],v1 offset:13056
v_mfma_f32_16x16x32_bf16 a[56:59],a[240:243],v[168:171],a[56:59]
v_exp_f32 v221,v221
v_mfma_f32_16x16x32_bf16 a[56:59],a[244:247],v[172:175],a[56:59]
ds_read_b128 a[184:187],v1 offset:12800
v_cvt_pk_bf16_f32 v206,v220,v221
v_mfma_f32_16x16x32_bf16 a[60:63],a[252:255],v[172:175],a[60:63]
v_exp_f32 v222,v222
v_mfma_f32_16x16x32_bf16 a[60:63],a[248:251],v[168:171],a[60:63]
ds_read_b128 a[180:183],v1 offset:12544
v_smfmac_f32_16x16x64_bf16 v[52:55],v[28:31],v[168:175],v27
s_waitcnt lgkmcnt(4)
v_mfma_f32_16x16x32_bf16 v[136:139],a[128:131],v[128:131],v[32:35]
buffer_load_dwordx4 v5,s[20:23],s47 offen offset:2048 nt lds
v_mfma_f32_16x16x32_bf16 v[136:139],a[132:135],v[160:163],v[136:139]
ds_read_b128 a[176:179],v1 offset:12288
v_add_u32 v1,s48,v1
v_mfma_f32_16x16x32_bf16 v[136:139],a[136:139],v[192:195],v[136:139]
v_exp_f32 v223,v223
v_mfma_f32_16x16x32_bf16 v[136:139],a[140:143],v[224:227],v[136:139]
v_cvt_pk_bf16_f32 v207,v222,v223
v_mfma_f32_16x16x32_bf16 v[140:143],a[156:159],v[224:227],v[32:35]
v_mfma_f32_16x16x32_bf16 v[140:143],a[152:155],v[192:195],v[140:143]
v_mfma_f32_16x16x32_bf16 v[140:143],a[148:151],v[160:163],v[140:143]
v_mfma_f32_16x16x32_bf16 v[140:143],a[144:147],v[128:131],v[140:143]
v_mfma_f32_16x16x32_bf16 v[152:155],a[160:163],v[128:131],v[32:35]
v_mfma_f32_16x16x32_bf16 v[152:155],a[164:167],v[160:163],v[152:155]
v_mfma_f32_16x16x32_bf16 v[152:155],a[168:171],v[192:195],v[152:155]
s_waitcnt lgkmcnt(0)
v_mfma_f32_16x16x32_bf16 v[152:155],a[172:175],v[224:227],v[152:155]
v_mfma_f32_16x16x32_bf16 v[156:159],a[188:191],v[224:227],v[32:35]
v_mfma_f32_16x16x32_bf16 v[156:159],a[184:187],v[192:195],v[156:159]
buffer_load_dwordx4 v5,s[20:23],s47 offen offset:3072 nt lds
v_mfma_f32_16x16x32_bf16 v[156:159],a[180:183],v[160:163],v[156:159]
v_mfma_f32_16x16x32_bf16 v[156:159],a[176:179],v[128:131],v[156:159]
v_mfma_f32_16x16x32_bf16 a[64:67],a[192:195],v[200:203],a[64:67]
v_mfma_f32_16x16x32_bf16 a[64:67],a[196:199],v[204:207],a[64:67]
v_mfma_f32_16x16x32_bf16 a[68:71],a[204:207],v[204:207],a[68:71]
v_mfma_f32_16x16x32_bf16 a[68:71],a[200:203],v[200:203],a[68:71]
v_mfma_f32_16x16x32_bf16 a[72:75],a[208:211],v[200:203],a[72:75]
v_mfma_f32_16x16x32_bf16 a[72:75],a[212:215],v[204:207],a[72:75]
v_exp_f32 v136,v136
v_mfma_f32_16x16x32_bf16 a[76:79],a[220:223],v[204:207],a[76:79]
v_exp_f32 v137,v137
v_mfma_f32_16x16x32_bf16 a[76:79],a[216:219],v[200:203],a[76:79]
v_exp_f32 v138,v138
v_mfma_f32_16x16x32_bf16 a[80:83],a[224:227],v[200:203],a[80:83]
s_add_u32 m0,s49,s52
v_mfma_f32_16x16x32_bf16 a[80:83],a[228:231],v[204:207],a[80:83]
s_xor_b32 s49,s50,65536
v_mfma_f32_16x16x32_bf16 a[84:87],a[236:239],v[204:207],a[84:87]
v_mfma_f32_16x16x32_bf16 a[84:87],a[232:235],v[200:203],a[84:87]
buffer_load_dwordx4 v4,s[24:27],s47 offen nt lds
v_mfma_f32_16x16x32_bf16 a[88:91],a[240:243],v[200:203],a[88:91]
v_mfma_f32_16x16x32_bf16 a[88:91],a[244:247],v[204:207],a[88:91]
v_exp_f32 v139,v139
v_mfma_f32_16x16x32_bf16 a[92:95],a[252:255],v[204:207],a[92:95]
v_exp_f32 v140,v140
v_mfma_f32_16x16x32_bf16 a[92:95],a[248:251],v[200:203],a[92:95]
v_exp_f32 v141,v141
v_smfmac_f32_16x16x64_bf16 v[56:59],v[28:31],v[200:207],v27
v_mfma_f32_16x16x32_bf16 v[168:171],a[128:131],v[132:135],v[36:39]
v_exp_f32 v142,v142
v_mfma_f32_16x16x32_bf16 v[168:171],a[132:135],v[164:167],v[168:171]
v_exp_f32 v143,v143
v_mfma_f32_16x16x32_bf16 v[168:171],a[136:139],v[196:199],v[168:171]
v_exp_f32 v152,v152
v_mfma_f32_16x16x32_bf16 v[168:171],a[140:143],v[228:231],v[168:171]
v_exp_f32 v153,v153
v_mfma_f32_16x16x32_bf16 v[172:175],a[156:159],v[228:231],v[36:39]
v_exp_f32 v154,v154
v_mfma_f32_16x16x32_bf16 v[172:175],a[152:155],v[196:199],v[172:175]
v_exp_f32 v155,v155
v_mfma_f32_16x16x32_bf16 v[172:175],a[148:151],v[164:167],v[172:175]
v_exp_f32 v156,v156
v_mfma_f32_16x16x32_bf16 v[172:175],a[144:147],v[132:135],v[172:175]
v_exp_f32 v157,v157
v_mfma_f32_16x16x32_bf16 v[184:187],a[160:163],v[132:135],v[36:39]
buffer_load_dwordx4 v5,s[24:27],s47 offen offset:1024 nt lds
)A"
R"A(v_mfma_f32_16x16x32_bf16 v[184:187],a[164:167],v[164:167],v[184:187]
v_exp_f32 v158,v158
v_mfma_f32_16x16x32_bf16 v[184:187],a[168:171],v[196:199],v[184:187]
v_exp_f32 v159,v159
v_mfma_f32_16x16x32_bf16 v[184:187],a[172:175],v[228:231],v[184:187]
v_exp_f32 v168,v168
v_mfma_f32_16x16x32_bf16 v[188:191],a[188:191],v[228:231],v[36:39]
v_exp_f32 v169,v169
v_mfma_f32_16x16x32_bf16 v[188:191],a[184:187],v[196:199],v[188:191]
v_exp_f32 v170,v170
v_mfma_f32_16x16x32_bf16 v[188:191],a[180:183],v[164:167],v[188:191]
v_exp_f32 v171,v171
v_mfma_f32_16x16x32_bf16 v[188:191],a[176:179],v[132:135],v[188:191]
v_exp_f32 v172,v172
ds_read_b64_tr_b16 a[192:193],v2
v_cvt_pk_bf16_f32 v168,v168,v169
ds_read_b64_tr_b16 a[194:195],v2 offset:4096
v_cvt_pk_bf16_f32 v169,v170,v171
v_exp_f32 v173,v173
ds_read_b64_tr_b16 a[196:197],v2 offset:8192
v_cvt_pk_bf16_f32 v170,v172,v173
v_exp_f32 v174,v174
v_exp_f32 v175,v175
buffer_load_dwordx4 v4,s[24:27],s47 offen offset:2048 nt lds
ds_read_b64_tr_b16 a[198:199],v2 offset:12288
v_cvt_pk_bf16_f32 v171,v174,v175
v_exp_f32 v184,v184
v_exp_f32 v185,v185
ds_read_b64_tr_b16 a[204:205],v3 offset:8192
v_cvt_pk_bf16_f32 v136,v136,v137
ds_read_b64_tr_b16 a[206:207],v3 offset:12288
v_cvt_pk_bf16_f32 v172,v184,v185
ds_read_b64_tr_b16 a[200:201],v3
ds_read_b64_tr_b16 a[202:203],v3 offset:4096
v_cvt_pk_bf16_f32 v137,v138,v139
ds_read_b64_tr_b16 a[208:209],v2 offset:256
ds_read_b64_tr_b16 a[210:211],v2 offset:4352
ds_read_b64_tr_b16 a[212:213],v2 offset:8448
v_mfma_f32_16x16x32_bf16 v[200:203],a[128:131],v[144:147],v[40:43]
ds_read_b64_tr_b16 a[214:215],v2 offset:12544
v_cvt_pk_bf16_f32 v138,v140,v141
v_mfma_f32_16x16x32_bf16 v[200:203],a[132:135],v[176:179],v[200:203]
ds_read_b64_tr_b16 a[220:221],v3 offset:8448
v_mfma_f32_16x16x32_bf16 v[200:203],a[136:139],v[208:211],v[200:203]
ds_read_b64_tr_b16 a[222:223],v3 offset:12544
v_cvt_pk_bf16_f32 v139,v142,v143
v_mfma_f32_16x16x32_bf16 v[200:203],a[140:143],v[240:243],v[200:203]
buffer_load_dwordx4 v5,s[24:27],s47 offen offset:3072 nt lds
v_mfma_f32_16x16x32_bf16 v[204:207],a[156:159],v[240:243],v[40:43]
s_add_u32 s47,s47,16384
s_and_b32 s47,s47,0x3ffff
ds_read_b64_tr_b16 a[216:217],v3 offset:256
v_mfma_f32_16x16x32_bf16 v[204:207],a[152:155],v[208:211],v[204:207]
s_or_b32 s47,s47,s46
ds_read_b64_tr_b16 a[218:219],v3 offset:4352
v_cvt_pk_bf16_f32 v140,v152,v153
v_mfma_f32_16x16x32_bf16 v[204:207],a[148:151],v[176:179],v[204:207]
ds_read_b64_tr_b16 a[224:225],v2 offset:512
v_mfma_f32_16x16x32_bf16 v[204:207],a[144:147],v[144:147],v[204:207]
ds_read_b64_tr_b16 a[226:227],v2 offset:4608
v_mfma_f32_16x16x32_bf16 v[216:219],a[160:163],v[144:147],v[40:43]
ds_read_b64_tr_b16 a[228:229],v2 offset:8704
v_cvt_pk_bf16_f32 v141,v154,v155
v_mfma_f32_16x16x32_bf16 v[216:219],a[164:167],v[176:179],v[216:219]
ds_read_b64_tr_b16 a[230:231],v2 offset:12800
v_mfma_f32_16x16x32_bf16 v[216:219],a[168:171],v[208:211],v[216:219]
ds_read_b64_tr_b16 a[236:237],v3 offset:8704
v_mfma_f32_16x16x32_bf16 v[216:219],a[172:175],v[240:243],v[216:219]
ds_read_b64_tr_b16 a[238:239],v3 offset:12800
v_cvt_pk_bf16_f32 v142,v156,v157
v_mfma_f32_16x16x32_bf16 v[220:223],a[188:191],v[240:243],v[40:43]
ds_read_b64_tr_b16 a[232:233],v3 offset:512
v_mfma_f32_16x16x32_bf16 v[220:223],a[184:187],v[208:211],v[220:223]
ds_read_b64_tr_b16 a[234:235],v3 offset:4608
v_mfma_f32_16x16x32_bf16 v[220:223],a[180:183],v[176:179],v[220:223]
ds_read_b64_tr_b16 a[240:241],v2 offset:768
v_cvt_pk_bf16_f32 v143,v158,v159
v_mfma_f32_16x16x32_bf16 v[220:223],a[176:179],v[144:147],v[220:223]
s_waitcnt lgkmcnt(7)
buffer_load_dword v26,v24,s[88:91],s93 offen
v_mfma_f32_16x16x32_bf16 a[0:3],a[192:195],v[136:139],a[0:3]
ds_read_b64_tr_b16 a[242:243],v2 offset:4864
v_mfma_f32_16x16x32_bf16 a[0:3],a[196:199],v[140:143],a[0:3]
ds_read_b64_tr_b16 a[244:245],v2 offset:8960
v_mfma_f32_16x16x32_bf16 a[4:7],a[204:207],v[140:143],a[4:7]
ds_read_b64_tr_b16 a[246:247],v2 offset:13056
v_mfma_f32_16x16x32_bf16 a[4:7],a[200:203],v[136:139],a[4:7]
ds_read_b64_tr_b16 a[252:253],v3 offset:8960
v_mfma_f32_16x16x32_bf16 a[8:11],a[208:211],v[136:139],a[8:11]
ds_read_b64_tr_b16 a[254:255],v3 offset:13056
v_mfma_f32_16x16x32_bf16 a[8:11],a[212:215],v[140:143],a[8:11]
ds_read_b64_tr_b16 a[248:249],v3 offset:768
v_mfma_f32_16x16x32_bf16 a[12:15],a[220:223],v[140:143],a[12:15]
ds_read_b64_tr_b16 a[250:251],v3 offset:4864
v_mfma_f32_16x16x32_bf16 a[12:15],a[216:219],v[136:139],a[12:15]
s_bitcmp1_b32 s45,0
s_cbranch_scc0 L_t31v31_%=
s_mov_b32 m0,s80
s_add_u32 s80,s80,4096
buffer_load_dwordx4 v25,s[24:27],s83 offen nt lds
s_add_u32 s83,s83,4096
L_t31v31_%=:
v_mfma_f32_16x16x32_bf16 a[16:19],a[224:227],v[136:139],a[16:19]
s_waitcnt lgkmcnt(6)
v_exp_f32 v186,v186
v_mfma_f32_16x16x32_bf16 a[16:19],a[228:231],v[140:143],a[16:19]
v_exp_f32 v187,v187
v_mfma_f32_16x16x32_bf16 a[20:23],a[236:239],v[140:143],a[20:23]
v_exp_f32 v188,v188
v_mfma_f32_16x16x32_bf16 a[20:23],a[232:235],v[136:139],a[20:23]
v_exp_f32 v189,v189
v_mfma_f32_16x16x32_bf16 a[24:27],a[240:243],v[136:139],a[24:27]
s_waitcnt lgkmcnt(2)
v_exp_f32 v190,v190
v_mfma_f32_16x16x32_bf16 a[24:27],a[244:247],v[140:143],a[24:27]
v_cvt_pk_bf16_f32 v173,v186,v187
v_mfma_f32_16x16x32_bf16 a[28:31],a[252:255],v[140:143],a[28:31]
s_waitcnt lgkmcnt(0)
v_exp_f32 v191,v191
v_mfma_f32_16x16x32_bf16 a[28:31],a[248:251],v[136:139],a[28:31]
v_cvt_pk_bf16_f32 v174,v188,v189
v_cvt_pk_bf16_f32 v175,v190,v191
s_cmp_lg_u32 s53,0
v_smfmac_f32_16x16x64_bf16 v[48:51],v[28:31],v[136:143],v27
s_cbranch_scc1 L_att1v3_%=
s_waitcnt lgkmcnt(0)
s_nop 7
s_branch L_att1x_%=
.p2align 5
L_att1v0_%=:
v_mfma_f32_16x16x32_bf16 v[232:235],a[128:131],v[148:151],v[44:47]
s_waitcnt vmcnt(8) lgkmcnt(0)
v_exp_f32 v200,v200
v_mfma_f32_16x16x32_bf16 v[232:235],a[132:135],v[180:183],v[232:235]
s_barrier
s_add_u32 m0,s49,s51
ds_read_b128 a[128:131],v1
v_mfma_f32_16x16x32_bf16 v[232:235],a[136:139],v[212:215],v[232:235]
s_mov_b32 s90,L_out_end_%=-L_pc_%=+128
v_exp_f32 v201,v201
v_mfma_f32_16x16x32_bf16 v[232:235],a[140:143],v[244:247],v[232:235]
s_lshr_b32 s45,s45,1
ds_read_b128 a[132:135],v1 offset:256
v_cvt_pk_bf16_f32 v200,v200,v201
v_mfma_f32_16x16x32_bf16 v[236:239],a[156:159],v[244:247],v[44:47]
buffer_load_dwordx4 v4,s[20:23],s47 offen nt lds
v_mfma_f32_16x16x32_bf16 v[236:239],a[152:155],v[212:215],v[236:239]
ds_read_b128 a[136:139],v1 offset:512
v_add_u32 v2,s48,v2
v_mfma_f32_16x16x32_bf16 v[236:239],a[148:151],v[180:183],v[236:239]
v_exp_f32 v202,v202
v_mfma_f32_16x16x32_bf16 v[236:239],a[144:147],v[148:151],v[236:239]
s_add_u32 s62,s50,32768
ds_read_b128 a[140:143],v1 offset:768
v_add_u32 v3,s48,v3
v_mfma_f32_16x16x32_bf16 v[248:251],a[160:163],v[148:151],v[44:47]
s_and_b32 s62,s62,131071
v_exp_f32 v203,v203
v_mfma_f32_16x16x32_bf16 v[248:251],a[164:167],v[180:183],v[248:251]
s_add_u32 s54,s54,16384
ds_read_b128 a[156:159],v1 offset:4864
v_cvt_pk_bf16_f32 v201,v202,v203
v_mfma_f32_16x16x32_bf16 v[248:251],a[168:171],v[212:215],v[248:251]
s_sub_u32 s48,s62,s50
v_exp_f32 v204,v204
v_mfma_f32_16x16x32_bf16 v[248:251],a[172:175],v[244:247],v[248:251]
s_sub_u32 s53,s53,1
v_exp_f32 v205,v205
v_mfma_f32_16x16x32_bf16 v[252:255],a[188:191],v[244:247],v[44:47]
s_and_b32 s46,s54,0xfffc0000
ds_read_b128 a[152:155],v1 offset:4608
v_cvt_pk_bf16_f32 v202,v204,v205
v_mfma_f32_16x16x32_bf16 v[252:255],a[184:187],v[212:215],v[252:255]
s_mov_b32 s50,s62
v_exp_f32 v206,v206
v_mfma_f32_16x16x32_bf16 v[252:255],a[180:183],v[180:183],v[252:255]
v_exp_f32 v207,v207
v_mfma_f32_16x16x32_bf16 v[252:255],a[176:179],v[148:151],v[252:255]
ds_read_b128 a[148:151],v1 offset:4352
v_cvt_pk_bf16_f32 v203,v206,v207
v_mfma_f32_16x16x32_bf16 a[32:35],a[192:195],v[168:171],a[32:35]
v_exp_f32 v216,v216
v_mfma_f32_16x16x32_bf16 a[32:35],a[196:199],v[172:175],a[32:35]
ds_read_b128 a[144:147],v1 offset:4096
v_mfma_f32_16x16x32_bf16 a[36:39],a[204:207],v[172:175],a[36:39]
buffer_load_dwordx4 v4,s[20:23],s47 offen offset:1024 nt lds
v_mfma_f32_16x16x32_bf16 a[36:39],a[200:203],v[168:171],a[36:39]
ds_read_b128 a[160:163],v1 offset:8192
v_mfma_f32_16x16x32_bf16 a[40:43],a[208:211],v[168:171],a[40:43]
v_exp_f32 v217,v217
v_mfma_f32_16x16x32_bf16 a[40:43],a[212:215],v[172:175],a[40:43]
ds_read_b128 a[164:167],v1 offset:8448
v_cvt_pk_bf16_f32 v204,v216,v217
v_mfma_f32_16x16x32_bf16 a[44:47],a[220:223],v[172:175],a[44:47]
v_exp_f32 v218,v218
v_mfma_f32_16x16x32_bf16 a[44:47],a[216:219],v[168:171],a[44:47]
ds_read_b128 a[168:171],v1 offset:8704
v_mfma_f32_16x16x32_bf16 a[48:51],a[224:227],v[168:171],a[48:51]
v_exp_f32 v219,v219
v_mfma_f32_16x16x32_bf16 a[48:51],a[228:231],v[172:175],a[48:51]
ds_read_b128 a[172:175],v1 offset:8960
v_cvt_pk_bf16_f32 v205,v218,v219
v_mfma_f32_16x16x32_bf16 a[52:55],a[236:239],v[172:175],a[52:55]
v_exp_f32 v220,v220
v_mfma_f32_16x16x32_bf16 a[52:55],a[232:235],v[168:171],a[52:55]
ds_read_b128 a[188:191],v1 offset:13056
v_mfma_f32_16x16x32_bf16 a[56:59],a[240:243],v[168:171],a[56:59]
v_exp_f32 v221,v221
v_mfma_f32_16x16x32_bf16 a[56:59],a[244:247],v[172:175],a[56:59]
ds_read_b128 a[184:187],v1 offset:12800
v_cvt_pk_bf16_f32 v206,v220,v221
v_mfma_f32_16x16x32_bf16 a[60:63],a[252:255],v[172:175],a[60:63]
v_exp_f32 v222,v222
v_mfma_f32_16x16x32_bf16 a[60:63],a[248:251],v[168:171],a[60:63]
ds_read_b128 a[180:183],v1 offset:12544
v_smfmac_f32_16x16x64_bf16 v[52:55],v[28:31],v[168:175],v27
buffer_load_dwordx4 v5,s[20:23],s47 offen offset:2048 nt lds
ds_read_b128 a[176:179],v1 offset:12288
v_add_u32 v1,s48,v1
v_exp_f32 v223,v223
s_nop 0
v_cvt_pk_bf16_f32 v207,v222,v223
v_exp_f32 v232,v232
v_exp_f32 v233,v233
v_exp_f32 v234,v234
v_exp_f32 v235,v235
v_exp_f32 v236,v236
v_exp_f32 v237,v237
v_exp_f32 v238,v238
v_exp_f32 v239,v239
v_exp_f32 v248,v248
buffer_load_dwordx4 v5,s[20:23],s47 offen offset:3072 nt lds
v_exp_f32 v249,v249
v_exp_f32 v250,v250
v_mfma_f32_16x16x32_bf16 a[64:67],a[192:195],v[200:203],a[64:67]
v_exp_f32 v251,v251
v_mfma_f32_16x16x32_bf16 a[64:67],a[196:199],v[204:207],a[64:67]
v_exp_f32 v252,v252
v_mfma_f32_16x16x32_bf16 a[68:71],a[204:207],v[204:207],a[68:71]
v_exp_f32 v253,v253
v_mfma_f32_16x16x32_bf16 a[68:71],a[200:203],v[200:203],a[68:71]
v_exp_f32 v254,v254
v_mfma_f32_16x16x32_bf16 a[72:75],a[208:211],v[200:203],a[72:75]
v_exp_f32 v255,v255
v_mfma_f32_16x16x32_bf16 a[72:75],a[212:215],v[204:207],a[72:75]
v_mfma_f32_16x16x32_bf16 a[76:79],a[220:223],v[204:207],a[76:79]
v_mfma_f32_16x16x32_bf16 a[76:79],a[216:219],v[200:203],a[76:79]
v_mfma_f32_16x16x32_bf16 a[80:83],a[224:227],v[200:203],a[80:83]
s_add_u32 m0,s49,s52
v_cvt_pk_bf16_f32 v232,v232,v233
v_cvt_pk_bf16_f32 v233,v234,v235
v_mfma_f32_16x16x32_bf16 a[80:83],a[228:231],v[204:207],a[80:83]
s_xor_b32 s49,s50,65536
v_cvt_pk_bf16_f32 v234,v236,v237
v_cvt_pk_bf16_f32 v235,v238,v239
v_mfma_f32_16x16x32_bf16 a[84:87],a[236:239],v[204:207],a[84:87]
v_cvt_pk_bf16_f32 v236,v248,v249
v_cvt_pk_bf16_f32 v237,v250,v251
v_mfma_f32_16x16x32_bf16 a[84:87],a[232:235],v[200:203],a[84:87]
buffer_load_dwordx4 v4,s[24:27],s47 offen nt lds
v_mfma_f32_16x16x32_bf16 a[88:91],a[240:243],v[200:203],a[88:91]
v_cvt_pk_bf16_f32 v238,v252,v253
v_cvt_pk_bf16_f32 v239,v254,v255
v_mfma_f32_16x16x32_bf16 a[88:91],a[244:247],v[204:207],a[88:91]
v_mfma_f32_16x16x32_bf16 a[92:95],a[252:255],v[204:207],a[92:95]
v_mfma_f32_16x16x32_bf16 a[92:95],a[248:251],v[200:203],a[92:95]
v_smfmac_f32_16x16x64_bf16 v[56:59],v[28:31],v[200:207],v27
s_waitcnt lgkmcnt(0)
v_mfma_f32_16x16x32_bf16 v[168:171],a[128:131],v[132:135],v[36:39]
v_mfma_f32_16x16x32_bf16 v[168:171],a[132:135],v[164:167],v[168:171]
v_mfma_f32_16x16x32_bf16 v[168:171],a[136:139],v[196:199],v[168:171]
v_mfma_f32_16x16x32_bf16 v[168:171],a[140:143],v[228:231],v[168:171]
v_mfma_f32_16x16x32_bf16 v[172:175],a[156:159],v[228:231],v[36:39]
v_mfma_f32_16x16x32_bf16 v[172:175],a[152:155],v[196:199],v[172:175]
v_mfma_f32_16x16x32_bf16 v[172:175],a[148:151],v[164:167],v[172:175]
v_mfma_f32_16x16x32_bf16 v[172:175],a[144:147],v[132:135],v[172:175]
v_mfma_f32_16x16x32_bf16 v[184:187],a[160:163],v[132:135],v[36:39]
buffer_load_dwordx4 v5,s[24:27],s47 offen offset:1024 nt lds
)A"
R"A(v_mfma_f32_16x16x32_bf16 v[184:187],a[164:167],v[164:167],v[184:187]
v_mfma_f32_16x16x32_bf16 v[184:187],a[168:171],v[196:199],v[184:187]
v_mfma_f32_16x16x32_bf16 v[184:187],a[172:175],v[228:231],v[184:187]
v_exp_f32 v168,v168
v_mfma_f32_16x16x32_bf16 v[188:191],a[188:191],v[228:231],v[36:39]
v_exp_f32 v169,v169
v_mfma_f32_16x16x32_bf16 v[188:191],a[184:187],v[196:199],v[188:191]
v_exp_f32 v170,v170
v_mfma_f32_16x16x32_bf16 v[188:191],a[180:183],v[164:167],v[188:191]
v_exp_f32 v171,v171
v_mfma_f32_16x16x32_bf16 v[188:191],a[176:179],v[132:135],v[188:191]
v_exp_f32 v172,v172
v_mfma_f32_16x16x32_bf16 a[96:99],a[192:195],v[232:235],a[96:99]
ds_read_b64_tr_b16 a[192:193],v2
v_cvt_pk_bf16_f32 v168,v168,v169
v_mfma_f32_16x16x32_bf16 a[96:99],a[196:199],v[236:239],a[96:99]
ds_read_b64_tr_b16 a[194:195],v2 offset:4096
v_cvt_pk_bf16_f32 v169,v170,v171
v_mfma_f32_16x16x32_bf16 a[100:103],a[204:207],v[236:239],a[100:103]
v_exp_f32 v173,v173
v_mfma_f32_16x16x32_bf16 a[100:103],a[200:203],v[232:235],a[100:103]
ds_read_b64_tr_b16 a[196:197],v2 offset:8192
v_cvt_pk_bf16_f32 v170,v172,v173
v_mfma_f32_16x16x32_bf16 a[104:107],a[208:211],v[232:235],a[104:107]
v_exp_f32 v174,v174
v_mfma_f32_16x16x32_bf16 a[104:107],a[212:215],v[236:239],a[104:107]
v_exp_f32 v175,v175
v_mfma_f32_16x16x32_bf16 a[108:111],a[220:223],v[236:239],a[108:111]
buffer_load_dwordx4 v4,s[24:27],s47 offen offset:2048 nt lds
v_mfma_f32_16x16x32_bf16 a[108:111],a[216:219],v[232:235],a[108:111]
ds_read_b64_tr_b16 a[198:199],v2 offset:12288
v_cvt_pk_bf16_f32 v171,v174,v175
v_mfma_f32_16x16x32_bf16 a[112:115],a[224:227],v[232:235],a[112:115]
v_exp_f32 v184,v184
v_mfma_f32_16x16x32_bf16 a[112:115],a[228:231],v[236:239],a[112:115]
v_exp_f32 v185,v185
v_mfma_f32_16x16x32_bf16 a[116:119],a[236:239],v[236:239],a[116:119]
ds_read_b64_tr_b16 a[204:205],v3 offset:8192
v_mfma_f32_16x16x32_bf16 a[116:119],a[232:235],v[232:235],a[116:119]
ds_read_b64_tr_b16 a[206:207],v3 offset:12288
v_cvt_pk_bf16_f32 v172,v184,v185
v_mfma_f32_16x16x32_bf16 a[120:123],a[240:243],v[232:235],a[120:123]
ds_read_b64_tr_b16 a[200:201],v3
v_mfma_f32_16x16x32_bf16 a[120:123],a[244:247],v[236:239],a[120:123]
ds_read_b64_tr_b16 a[202:203],v3 offset:4096
v_mfma_f32_16x16x32_bf16 a[124:127],a[252:255],v[236:239],a[124:127]
ds_read_b64_tr_b16 a[208:209],v2 offset:256
v_mfma_f32_16x16x32_bf16 a[124:127],a[248:251],v[232:235],a[124:127]
ds_read_b64_tr_b16 a[210:211],v2 offset:4352
v_smfmac_f32_16x16x64_bf16 v[60:63],v[28:31],v[232:239],v27
ds_read_b64_tr_b16 a[212:213],v2 offset:8448
v_mfma_f32_16x16x32_bf16 v[200:203],a[128:131],v[144:147],v[40:43]
ds_read_b64_tr_b16 a[214:215],v2 offset:12544
v_mfma_f32_16x16x32_bf16 v[200:203],a[132:135],v[176:179],v[200:203]
ds_read_b64_tr_b16 a[220:221],v3 offset:8448
v_mfma_f32_16x16x32_bf16 v[200:203],a[136:139],v[208:211],v[200:203]
ds_read_b64_tr_b16 a[222:223],v3 offset:12544
v_mfma_f32_16x16x32_bf16 v[200:203],a[140:143],v[240:243],v[200:203]
buffer_load_dwordx4 v5,s[24:27],s47 offen offset:3072 nt lds
v_mfma_f32_16x16x32_bf16 v[204:207],a[156:159],v[240:243],v[40:43]
s_add_u32 s47,s47,16384
s_and_b32 s47,s47,0x3ffff
ds_read_b64_tr_b16 a[216:217],v3 offset:256
v_mfma_f32_16x16x32_bf16 v[204:207],a[152:155],v[208:211],v[204:207]
s_or_b32 s47,s47,s46
ds_read_b64_tr_b16 a[218:219],v3 offset:4352
v_mfma_f32_16x16x32_bf16 v[204:207],a[148:151],v[176:179],v[204:207]
ds_read_b64_tr_b16 a[224:225],v2 offset:512
v_mfma_f32_16x16x32_bf16 v[204:207],a[144:147],v[144:147],v[204:207]
ds_read_b64_tr_b16 a[226:227],v2 offset:4608
v_mfma_f32_16x16x32_bf16 v[216:219],a[160:163],v[144:147],v[40:43]
ds_read_b64_tr_b16 a[228:229],v2 offset:8704
v_mfma_f32_16x16x32_bf16 v[216:219],a[164:167],v[176:179],v[216:219]
ds_read_b64_tr_b16 a[230:231],v2 offset:12800
v_mfma_f32_16x16x32_bf16 v[216:219],a[168:171],v[208:211],v[216:219]
ds_read_b64_tr_b16 a[236:237],v3 offset:8704
v_mfma_f32_16x16x32_bf16 v[216:219],a[172:175],v[240:243],v[216:219]
ds_read_b64_tr_b16 a[238:239],v3 offset:12800
v_mfma_f32_16x16x32_bf16 v[220:223],a[188:191],v[240:243],v[40:43]
ds_read_b64_tr_b16 a[232:233],v3 offset:512
v_mfma_f32_16x16x32_bf16 v[220:223],a[184:187],v[208:211],v[220:223]
ds_read_b64_tr_b16 a[234:235],v3 offset:4608
v_mfma_f32_16x16x32_bf16 v[220:223],a[180:183],v[176:179],v[220:223]
ds_read_b64_tr_b16 a[240:241],v2 offset:768
v_mfma_f32_16x16x32_bf16 v[220:223],a[176:179],v[144:147],v[220:223]
buffer_load_dword v26,v24,s[88:91],s93 offen
ds_read_b64_tr_b16 a[242:243],v2 offset:4864
ds_read_b64_tr_b16 a[244:245],v2 offset:8960
ds_read_b64_tr_b16 a[246:247],v2 offset:13056
ds_read_b64_tr_b16 a[252:253],v3 offset:8960
ds_read_b64_tr_b16 a[254:255],v3 offset:13056
ds_read_b64_tr_b16 a[248:249],v3 offset:768
ds_read_b64_tr_b16 a[250:251],v3 offset:4864
s_bitcmp1_b32 s45,0
s_cbranch_scc0 L_t31v01_%=
s_mov_b32 m0,s80
s_add_u32 s80,s80,4096
buffer_load_dwordx4 v25,s[24:27],s83 offen nt lds
s_add_u32 s83,s83,4096
L_t31v01_%=:
v_exp_f32 v186,v186
v_exp_f32 v187,v187
v_exp_f32 v188,v188
v_exp_f32 v189,v189
v_exp_f32 v190,v190
v_cvt_pk_bf16_f32 v173,v186,v187
v_exp_f32 v191,v191
v_cvt_pk_bf16_f32 v174,v188,v189
v_cvt_pk_bf16_f32 v175,v190,v191
s_cmp_lg_u32 s53,0
s_cbranch_scc1 L_att1v0_%=
s_waitcnt lgkmcnt(0)
s_nop 7
s_branch L_att1x_%=
.p2align 5
L_att1v1_%=:
v_mfma_f32_16x16x32_bf16 v[232:235],a[128:131],v[148:151],v[44:47]
s_waitcnt vmcnt(8) lgkmcnt(0)
v_exp_f32 v200,v200
v_mfma_f32_16x16x32_bf16 v[232:235],a[132:135],v[180:183],v[232:235]
s_barrier
s_add_u32 m0,s49,s51
ds_read_b128 a[128:131],v1
v_mfma_f32_16x16x32_bf16 v[232:235],a[136:139],v[212:215],v[232:235]
s_mov_b32 s90,L_out_end_%=-L_pc_%=+128
v_exp_f32 v201,v201
v_mfma_f32_16x16x32_bf16 v[232:235],a[140:143],v[244:247],v[232:235]
s_lshr_b32 s45,s45,1
ds_read_b128 a[132:135],v1 offset:256
v_cvt_pk_bf16_f32 v200,v200,v201
v_mfma_f32_16x16x32_bf16 v[236:239],a[156:159],v[244:247],v[44:47]
buffer_load_dwordx4 v4,s[20:23],s47 offen nt lds
v_mfma_f32_16x16x32_bf16 v[236:239],a[152:155],v[212:215],v[236:239]
ds_read_b128 a[136:139],v1 offset:512
v_add_u32 v2,s48,v2
v_mfma_f32_16x16x32_bf16 v[236:239],a[148:151],v[180:183],v[236:239]
v_exp_f32 v202,v202
v_mfma_f32_16x16x32_bf16 v[236:239],a[144:147],v[148:151],v[236:239]
s_add_u32 s62,s50,32768
ds_read_b128 a[140:143],v1 offset:768
v_add_u32 v3,s48,v3
v_mfma_f32_16x16x32_bf16 v[248:251],a[160:163],v[148:151],v[44:47]
s_and_b32 s62,s62,131071
v_exp_f32 v203,v203
v_mfma_f32_16x16x32_bf16 v[248:251],a[164:167],v[180:183],v[248:251]
s_add_u32 s54,s54,16384
ds_read_b128 a[156:159],v1 offset:4864
v_cvt_pk_bf16_f32 v201,v202,v203
v_mfma_f32_16x16x32_bf16 v[248:251],a[168:171],v[212:215],v[248:251]
s_sub_u32 s48,s62,s50
v_exp_f32 v204,v204
v_mfma_f32_16x16x32_bf16 v[248:251],a[172:175],v[244:247],v[248:251]
s_sub_u32 s53,s53,1
v_exp_f32 v205,v205
v_mfma_f32_16x16x32_bf16 v[252:255],a[188:191],v[244:247],v[44:47]
s_and_b32 s46,s54,0xfffc0000
ds_read_b128 a[152:155],v1 offset:4608
v_cvt_pk_bf16_f32 v202,v204,v205
v_mfma_f32_16x16x32_bf16 v[252:255],a[184:187],v[212:215],v[252:255]
s_mov_b32 s50,s62
v_exp_f32 v206,v206
v_mfma_f32_16x16x32_bf16 v[252:255],a[180:183],v[180:183],v[252:255]
v_exp_f32 v207,v207
v_mfma_f32_16x16x32_bf16 v[252:255],a[176:179],v[148:151],v[252:255]
ds_read_b128 a[148:151],v1 offset:4352
v_cvt_pk_bf16_f32 v203,v206,v207
v_exp_f32 v216,v216
ds_read_b128 a[144:147],v1 offset:4096
buffer_load_dwordx4 v4,s[20:23],s47 offen offset:1024 nt lds
ds_read_b128 a[160:163],v1 offset:8192
v_exp_f32 v217,v217
ds_read_b128 a[164:167],v1 offset:8448
v_cvt_pk_bf16_f32 v204,v216,v217
v_exp_f32 v218,v218
ds_read_b128 a[168:171],v1 offset:8704
v_exp_f32 v219,v219
ds_read_b128 a[172:175],v1 offset:8960
v_cvt_pk_bf16_f32 v205,v218,v219
v_exp_f32 v220,v220
ds_read_b128 a[188:191],v1 offset:13056
v_exp_f32 v221,v221
ds_read_b128 a[184:187],v1 offset:12800
v_cvt_pk_bf16_f32 v206,v220,v221
v_exp_f32 v222,v222
ds_read_b128 a[180:183],v1 offset:12544
s_waitcnt lgkmcnt(11)
v_mfma_f32_16x16x32_bf16 v[136:139],a[128:131],v[128:131],v[32:35]
buffer_load_dwordx4 v5,s[20:23],s47 offen offset:2048 nt lds
v_mfma_f32_16x16x32_bf16 v[136:139],a[132:135],v[160:163],v[136:139]
ds_read_b128 a[176:179],v1 offset:12288
v_add_u32 v1,s48,v1
v_mfma_f32_16x16x32_bf16 v[136:139],a[136:139],v[192:195],v[136:139]
v_exp_f32 v223,v223
v_mfma_f32_16x16x32_bf16 v[136:139],a[140:143],v[224:227],v[136:139]
s_waitcnt lgkmcnt(11)
v_cvt_pk_bf16_f32 v207,v222,v223
v_mfma_f32_16x16x32_bf16 v[140:143],a[156:159],v[224:227],v[32:35]
s_waitcnt lgkmcnt(10)
v_exp_f32 v232,v232
v_mfma_f32_16x16x32_bf16 v[140:143],a[152:155],v[192:195],v[140:143]
s_waitcnt lgkmcnt(9)
v_exp_f32 v233,v233
v_mfma_f32_16x16x32_bf16 v[140:143],a[148:151],v[160:163],v[140:143]
s_waitcnt lgkmcnt(8)
v_exp_f32 v234,v234
v_mfma_f32_16x16x32_bf16 v[140:143],a[144:147],v[128:131],v[140:143]
s_waitcnt lgkmcnt(1)
v_exp_f32 v235,v235
v_mfma_f32_16x16x32_bf16 v[152:155],a[160:163],v[128:131],v[32:35]
v_exp_f32 v236,v236
v_mfma_f32_16x16x32_bf16 v[152:155],a[164:167],v[160:163],v[152:155]
v_exp_f32 v237,v237
v_mfma_f32_16x16x32_bf16 v[152:155],a[168:171],v[192:195],v[152:155]
v_exp_f32 v238,v238
v_mfma_f32_16x16x32_bf16 v[152:155],a[172:175],v[224:227],v[152:155]
v_exp_f32 v239,v239
v_mfma_f32_16x16x32_bf16 v[156:159],a[188:191],v[224:227],v[32:35]
v_exp_f32 v248,v248
v_mfma_f32_16x16x32_bf16 v[156:159],a[184:187],v[192:195],v[156:159]
buffer_load_dwordx4 v5,s[20:23],s47 offen offset:3072 nt lds
v_mfma_f32_16x16x32_bf16 v[156:159],a[180:183],v[160:163],v[156:159]
s_waitcnt lgkmcnt(0)
v_exp_f32 v249,v249
v_mfma_f32_16x16x32_bf16 v[156:159],a[176:179],v[128:131],v[156:159]
v_exp_f32 v250,v250
v_mfma_f32_16x16x32_bf16 a[64:67],a[192:195],v[200:203],a[64:67]
v_exp_f32 v251,v251
v_mfma_f32_16x16x32_bf16 a[64:67],a[196:199],v[204:207],a[64:67]
v_exp_f32 v252,v252
v_mfma_f32_16x16x32_bf16 a[68:71],a[204:207],v[204:207],a[68:71]
v_exp_f32 v253,v253
v_mfma_f32_16x16x32_bf16 a[68:71],a[200:203],v[200:203],a[68:71]
v_exp_f32 v254,v254
v_mfma_f32_16x16x32_bf16 a[72:75],a[208:211],v[200:203],a[72:75]
v_exp_f32 v255,v255
v_mfma_f32_16x16x32_bf16 a[72:75],a[212:215],v[204:207],a[72:75]
v_exp_f32 v136,v136
v_mfma_f32_16x16x32_bf16 a[76:79],a[220:223],v[204:207],a[76:79]
v_exp_f32 v137,v137
v_mfma_f32_16x16x32_bf16 a[76:79],a[216:219],v[200:203],a[76:79]
v_exp_f32 v138,v138
v_mfma_f32_16x16x32_bf16 a[80:83],a[224:227],v[200:203],a[80:83]
s_add_u32 m0,s49,s52
v_cvt_pk_bf16_f32 v232,v232,v233
v_cvt_pk_bf16_f32 v233,v234,v235
v_mfma_f32_16x16x32_bf16 a[80:83],a[228:231],v[204:207],a[80:83]
s_xor_b32 s49,s50,65536
v_cvt_pk_bf16_f32 v234,v236,v237
v_cvt_pk_bf16_f32 v235,v238,v239
v_mfma_f32_16x16x32_bf16 a[84:87],a[236:239],v[204:207],a[84:87]
v_cvt_pk_bf16_f32 v236,v248,v249
v_cvt_pk_bf16_f32 v237,v250,v251
v_mfma_f32_16x16x32_bf16 a[84:87],a[232:235],v[200:203],a[84:87]
buffer_load_dwordx4 v4,s[24:27],s47 offen nt lds
v_mfma_f32_16x16x32_bf16 a[88:91],a[240:243],v[200:203],a[88:91]
v_cvt_pk_bf16_f32 v238,v252,v253
v_cvt_pk_bf16_f32 v239,v254,v255
v_mfma_f32_16x16x32_bf16 a[88:91],a[244:247],v[204:207],a[88:91]
v_exp_f32 v139,v139
v_mfma_f32_16x16x32_bf16 a[92:95],a[252:255],v[204:207],a[92:95]
v_exp_f32 v140,v140
v_mfma_f32_16x16x32_bf16 a[92:95],a[248:251],v[200:203],a[92:95]
v_exp_f32 v141,v141
v_smfmac_f32_16x16x64_bf16 v[56:59],v[28:31],v[200:207],v27
v_exp_f32 v142,v142
v_exp_f32 v143,v143
v_exp_f32 v152,v152
v_exp_f32 v153,v153
v_exp_f32 v154,v154
v_exp_f32 v155,v155
v_exp_f32 v156,v156
v_exp_f32 v157,v157
buffer_load_dwordx4 v5,s[24:27],s47 offen offset:1024 nt lds
v_exp_f32 v158,v158
v_exp_f32 v159,v159
v_mfma_f32_16x16x32_bf16 a[96:99],a[192:195],v[232:235],a[96:99]
ds_read_b64_tr_b16 a[192:193],v2
v_mfma_f32_16x16x32_bf16 a[96:99],a[196:199],v[236:239],a[96:99]
ds_read_b64_tr_b16 a[194:195],v2 offset:4096
v_mfma_f32_16x16x32_bf16 a[100:103],a[204:207],v[236:239],a[100:103]
v_mfma_f32_16x16x32_bf16 a[100:103],a[200:203],v[232:235],a[100:103]
ds_read_b64_tr_b16 a[196:197],v2 offset:8192
v_mfma_f32_16x16x32_bf16 a[104:107],a[208:211],v[232:235],a[104:107]
v_mfma_f32_16x16x32_bf16 a[104:107],a[212:215],v[236:239],a[104:107]
)A"
R"A(v_mfma_f32_16x16x32_bf16 a[108:111],a[220:223],v[236:239],a[108:111]
buffer_load_dwordx4 v4,s[24:27],s47 offen offset:2048 nt lds
v_mfma_f32_16x16x32_bf16 a[108:111],a[216:219],v[232:235],a[108:111]
ds_read_b64_tr_b16 a[198:199],v2 offset:12288
v_mfma_f32_16x16x32_bf16 a[112:115],a[224:227],v[232:235],a[112:115]
v_mfma_f32_16x16x32_bf16 a[112:115],a[228:231],v[236:239],a[112:115]
v_mfma_f32_16x16x32_bf16 a[116:119],a[236:239],v[236:239],a[116:119]
ds_read_b64_tr_b16 a[204:205],v3 offset:8192
v_cvt_pk_bf16_f32 v136,v136,v137
v_mfma_f32_16x16x32_bf16 a[116:119],a[232:235],v[232:235],a[116:119]
ds_read_b64_tr_b16 a[206:207],v3 offset:12288
v_mfma_f32_16x16x32_bf16 a[120:123],a[240:243],v[232:235],a[120:123]
ds_read_b64_tr_b16 a[200:201],v3
v_mfma_f32_16x16x32_bf16 a[120:123],a[244:247],v[236:239],a[120:123]
ds_read_b64_tr_b16 a[202:203],v3 offset:4096
v_cvt_pk_bf16_f32 v137,v138,v139
v_mfma_f32_16x16x32_bf16 a[124:127],a[252:255],v[236:239],a[124:127]
ds_read_b64_tr_b16 a[208:209],v2 offset:256
v_mfma_f32_16x16x32_bf16 a[124:127],a[248:251],v[232:235],a[124:127]
ds_read_b64_tr_b16 a[210:211],v2 offset:4352
v_smfmac_f32_16x16x64_bf16 v[60:63],v[28:31],v[232:239],v27
ds_read_b64_tr_b16 a[212:213],v2 offset:8448
v_mfma_f32_16x16x32_bf16 v[200:203],a[128:131],v[144:147],v[40:43]
ds_read_b64_tr_b16 a[214:215],v2 offset:12544
v_cvt_pk_bf16_f32 v138,v140,v141
v_mfma_f32_16x16x32_bf16 v[200:203],a[132:135],v[176:179],v[200:203]
ds_read_b64_tr_b16 a[220:221],v3 offset:8448
v_mfma_f32_16x16x32_bf16 v[200:203],a[136:139],v[208:211],v[200:203]
ds_read_b64_tr_b16 a[222:223],v3 offset:12544
v_cvt_pk_bf16_f32 v139,v142,v143
v_mfma_f32_16x16x32_bf16 v[200:203],a[140:143],v[240:243],v[200:203]
buffer_load_dwordx4 v5,s[24:27],s47 offen offset:3072 nt lds
v_mfma_f32_16x16x32_bf16 v[204:207],a[156:159],v[240:243],v[40:43]
s_add_u32 s47,s47,16384
s_and_b32 s47,s47,0x3ffff
ds_read_b64_tr_b16 a[216:217],v3 offset:256
v_mfma_f32_16x16x32_bf16 v[204:207],a[152:155],v[208:211],v[204:207]
s_or_b32 s47,s47,s46
ds_read_b64_tr_b16 a[218:219],v3 offset:4352
v_cvt_pk_bf16_f32 v140,v152,v153
v_mfma_f32_16x16x32_bf16 v[204:207],a[148:151],v[176:179],v[204:207]
ds_read_b64_tr_b16 a[224:225],v2 offset:512
v_mfma_f32_16x16x32_bf16 v[204:207],a[144:147],v[144:147],v[204:207]
ds_read_b64_tr_b16 a[226:227],v2 offset:4608
v_mfma_f32_16x16x32_bf16 v[216:219],a[160:163],v[144:147],v[40:43]
ds_read_b64_tr_b16 a[228:229],v2 offset:8704
v_cvt_pk_bf16_f32 v141,v154,v155
v_mfma_f32_16x16x32_bf16 v[216:219],a[164:167],v[176:179],v[216:219]
ds_read_b64_tr_b16 a[230:231],v2 offset:12800
v_mfma_f32_16x16x32_bf16 v[216:219],a[168:171],v[208:211],v[216:219]
ds_read_b64_tr_b16 a[236:237],v3 offset:8704
v_mfma_f32_16x16x32_bf16 v[216:219],a[172:175],v[240:243],v[216:219]
ds_read_b64_tr_b16 a[238:239],v3 offset:12800
v_cvt_pk_bf16_f32 v142,v156,v157
v_mfma_f32_16x16x32_bf16 v[220:223],a[188:191],v[240:243],v[40:43]
ds_read_b64_tr_b16 a[232:233],v3 offset:512
v_mfma_f32_16x16x32_bf16 v[220:223],a[184:187],v[208:211],v[220:223]
ds_read_b64_tr_b16 a[234:235],v3 offset:4608
v_mfma_f32_16x16x32_bf16 v[220:223],a[180:183],v[176:179],v[220:223]
ds_read_b64_tr_b16 a[240:241],v2 offset:768
v_cvt_pk_bf16_f32 v143,v158,v159
v_mfma_f32_16x16x32_bf16 v[220:223],a[176:179],v[144:147],v[220:223]
s_waitcnt lgkmcnt(7)
buffer_load_dword v26,v24,s[88:91],s93 offen
v_mfma_f32_16x16x32_bf16 a[0:3],a[192:195],v[136:139],a[0:3]
ds_read_b64_tr_b16 a[242:243],v2 offset:4864
v_mfma_f32_16x16x32_bf16 a[0:3],a[196:199],v[140:143],a[0:3]
ds_read_b64_tr_b16 a[244:245],v2 offset:8960
v_mfma_f32_16x16x32_bf16 a[4:7],a[204:207],v[140:143],a[4:7]
ds_read_b64_tr_b16 a[246:247],v2 offset:13056
v_mfma_f32_16x16x32_bf16 a[4:7],a[200:203],v[136:139],a[4:7]
ds_read_b64_tr_b16 a[252:253],v3 offset:8960
v_mfma_f32_16x16x32_bf16 a[8:11],a[208:211],v[136:139],a[8:11]
ds_read_b64_tr_b16 a[254:255],v3 offset:13056
v_mfma_f32_16x16x32_bf16 a[8:11],a[212:215],v[140:143],a[8:11]
ds_read_b64_tr_b16 a[248:249],v3 offset:768
v_mfma_f32_16x16x32_bf16 a[12:15],a[220:223],v[140:143],a[12:15]
ds_read_b64_tr_b16 a[250:251],v3 offset:4864
v_mfma_f32_16x16x32_bf16 a[12:15],a[216:219],v[136:139],a[12:15]
s_bitcmp1_b32 s45,0
s_cbranch_scc0 L_t31v11_%=
s_mov_b32 m0,s80
s_add_u32 s80,s80,4096
buffer_load_dwordx4 v25,s[24:27],s83 offen nt lds
s_add_u32 s83,s83,4096
L_t31v11_%=:
v_mfma_f32_16x16x32_bf16 a[16:19],a[224:227],v[136:139],a[16:19]
s_waitcnt lgkmcnt(6)
v_mfma_f32_16x16x32_bf16 a[16:19],a[228:231],v[140:143],a[16:19]
v_mfma_f32_16x16x32_bf16 a[20:23],a[236:239],v[140:143],a[20:23]
v_mfma_f32_16x16x32_bf16 a[20:23],a[232:235],v[136:139],a[20:23]
v_mfma_f32_16x16x32_bf16 a[24:27],a[240:243],v[136:139],a[24:27]
s_waitcnt lgkmcnt(2)
v_mfma_f32_16x16x32_bf16 a[24:27],a[244:247],v[140:143],a[24:27]
v_mfma_f32_16x16x32_bf16 a[28:31],a[252:255],v[140:143],a[28:31]
s_waitcnt lgkmcnt(0)
v_mfma_f32_16x16x32_bf16 a[28:31],a[248:251],v[136:139],a[28:31]
s_cmp_lg_u32 s53,0
v_smfmac_f32_16x16x64_bf16 v[48:51],v[28:31],v[136:143],v27
s_cbranch_scc1 L_att1v1_%=
s_waitcnt lgkmcnt(0)
s_nop 7
s_branch L_att1x_%=
.p2align 5
L_att1v2_%=:
v_mfma_f32_16x16x32_bf16 v[232:235],a[128:131],v[148:151],v[44:47]
s_waitcnt vmcnt(8) lgkmcnt(0)
v_mfma_f32_16x16x32_bf16 v[232:235],a[132:135],v[180:183],v[232:235]
s_barrier
s_add_u32 m0,s49,s51
ds_read_b128 a[128:131],v1
v_mfma_f32_16x16x32_bf16 v[232:235],a[136:139],v[212:215],v[232:235]
s_mov_b32 s90,L_out_end_%=-L_pc_%=+128
v_mfma_f32_16x16x32_bf16 v[232:235],a[140:143],v[244:247],v[232:235]
s_lshr_b32 s45,s45,1
ds_read_b128 a[132:135],v1 offset:256
v_mfma_f32_16x16x32_bf16 v[236:239],a[156:159],v[244:247],v[44:47]
buffer_load_dwordx4 v4,s[20:23],s47 offen nt lds
v_mfma_f32_16x16x32_bf16 v[236:239],a[152:155],v[212:215],v[236:239]
ds_read_b128 a[136:139],v1 offset:512
v_add_u32 v2,s48,v2
v_mfma_f32_16x16x32_bf16 v[236:239],a[148:151],v[180:183],v[236:239]
v_mfma_f32_16x16x32_bf16 v[236:239],a[144:147],v[148:151],v[236:239]
s_add_u32 s62,s50,32768
ds_read_b128 a[140:143],v1 offset:768
v_add_u32 v3,s48,v3
v_mfma_f32_16x16x32_bf16 v[248:251],a[160:163],v[148:151],v[44:47]
s_and_b32 s62,s62,131071
v_mfma_f32_16x16x32_bf16 v[248:251],a[164:167],v[180:183],v[248:251]
s_add_u32 s54,s54,16384
ds_read_b128 a[156:159],v1 offset:4864
v_mfma_f32_16x16x32_bf16 v[248:251],a[168:171],v[212:215],v[248:251]
s_sub_u32 s48,s62,s50
v_mfma_f32_16x16x32_bf16 v[248:251],a[172:175],v[244:247],v[248:251]
s_sub_u32 s53,s53,1
v_mfma_f32_16x16x32_bf16 v[252:255],a[188:191],v[244:247],v[44:47]
s_and_b32 s46,s54,0xfffc0000
ds_read_b128 a[152:155],v1 offset:4608
v_mfma_f32_16x16x32_bf16 v[252:255],a[184:187],v[212:215],v[252:255]
s_mov_b32 s50,s62
v_mfma_f32_16x16x32_bf16 v[252:255],a[180:183],v[180:183],v[252:255]
v_mfma_f32_16x16x32_bf16 v[252:255],a[176:179],v[148:151],v[252:255]
ds_read_b128 a[148:151],v1 offset:4352
v_mfma_f32_16x16x32_bf16 a[32:35],a[192:195],v[168:171],a[32:35]
v_mfma_f32_16x16x32_bf16 a[32:35],a[196:199],v[172:175],a[32:35]
ds_read_b128 a[144:147],v1 offset:4096
v_mfma_f32_16x16x32_bf16 a[36:39],a[204:207],v[172:175],a[36:39]
buffer_load_dwordx4 v4,s[20:23],s47 offen offset:1024 nt lds
v_mfma_f32_16x16x32_bf16 a[36:39],a[200:203],v[168:171],a[36:39]
ds_read_b128 a[160:163],v1 offset:8192
v_mfma_f32_16x16x32_bf16 a[40:43],a[208:211],v[168:171],a[40:43]
v_mfma_f32_16x16x32_bf16 a[40:43],a[212:215],v[172:175],a[40:43]
ds_read_b128 a[164:167],v1 offset:8448
v_mfma_f32_16x16x32_bf16 a[44:47],a[220:223],v[172:175],a[44:47]
v_mfma_f32_16x16x32_bf16 a[44:47],a[216:219],v[168:171],a[44:47]
ds_read_b128 a[168:171],v1 offset:8704
v_mfma_f32_16x16x32_bf16 a[48:51],a[224:227],v[168:171],a[48:51]
v_mfma_f32_16x16x32_bf16 a[48:51],a[228:231],v[172:175],a[48:51]
ds_read_b128 a[172:175],v1 offset:8960
v_mfma_f32_16x16x32_bf16 a[52:55],a[236:239],v[172:175],a[52:55]
v_mfma_f32_16x16x32_bf16 a[52:55],a[232:235],v[168:171],a[52:55]
ds_read_b128 a[188:191],v1 offset:13056
v_mfma_f32_16x16x32_bf16 a[56:59],a[240:243],v[168:171],a[56:59]
v_mfma_f32_16x16x32_bf16 a[56:59],a[244:247],v[172:175],a[56:59]
ds_read_b128 a[184:187],v1 offset:12800
v_mfma_f32_16x16x32_bf16 a[60:63],a[252:255],v[172:175],a[60:63]
v_mfma_f32_16x16x32_bf16 a[60:63],a[248:251],v[168:171],a[60:63]
ds_read_b128 a[180:183],v1 offset:12544
v_smfmac_f32_16x16x64_bf16 v[52:55],v[28:31],v[168:175],v27
s_waitcnt lgkmcnt(4)
v_mfma_f32_16x16x32_bf16 v[136:139],a[128:131],v[128:131],v[32:35]
buffer_load_dwordx4 v5,s[20:23],s47 offen offset:2048 nt lds
v_mfma_f32_16x16x32_bf16 v[136:139],a[132:135],v[160:163],v[136:139]
ds_read_b128 a[176:179],v1 offset:12288
v_add_u32 v1,s48,v1
v_mfma_f32_16x16x32_bf16 v[136:139],a[136:139],v[192:195],v[136:139]
v_mfma_f32_16x16x32_bf16 v[136:139],a[140:143],v[224:227],v[136:139]
v_mfma_f32_16x16x32_bf16 v[140:143],a[156:159],v[224:227],v[32:35]
v_exp_f32 v232,v232
v_mfma_f32_16x16x32_bf16 v[140:143],a[152:155],v[192:195],v[140:143]
v_exp_f32 v233,v233
v_mfma_f32_16x16x32_bf16 v[140:143],a[148:151],v[160:163],v[140:143]
v_exp_f32 v234,v234
v_mfma_f32_16x16x32_bf16 v[140:143],a[144:147],v[128:131],v[140:143]
v_exp_f32 v235,v235
v_mfma_f32_16x16x32_bf16 v[152:155],a[160:163],v[128:131],v[32:35]
v_exp_f32 v236,v236
v_mfma_f32_16x16x32_bf16 v[152:155],a[164:167],v[160:163],v[152:155]
v_exp_f32 v237,v237
v_mfma_f32_16x16x32_bf16 v[152:155],a[168:171],v[192:195],v[152:155]
s_waitcnt lgkmcnt(0)
v_exp_f32 v238,v238
v_mfma_f32_16x16x32_bf16 v[152:155],a[172:175],v[224:227],v[152:155]
v_exp_f32 v239,v239
v_mfma_f32_16x16x32_bf16 v[156:159],a[188:191],v[224:227],v[32:35]
v_exp_f32 v248,v248
v_mfma_f32_16x16x32_bf16 v[156:159],a[184:187],v[192:195],v[156:159]
buffer_load_dwordx4 v5,s[20:23],s47 offen offset:3072 nt lds
v_mfma_f32_16x16x32_bf16 v[156:159],a[180:183],v[160:163],v[156:159]
v_exp_f32 v249,v249
v_mfma_f32_16x16x32_bf16 v[156:159],a[176:179],v[128:131],v[156:159]
v_exp_f32 v250,v250
v_exp_f32 v251,v251
v_exp_f32 v252,v252
v_exp_f32 v253,v253
v_exp_f32 v254,v254
v_exp_f32 v255,v255
v_exp_f32 v136,v136
v_exp_f32 v137,v137
v_exp_f32 v138,v138
s_add_u32 m0,s49,s52
v_cvt_pk_bf16_f32 v232,v232,v233
v_cvt_pk_bf16_f32 v233,v234,v235
s_xor_b32 s49,s50,65536
v_cvt_pk_bf16_f32 v234,v236,v237
v_cvt_pk_bf16_f32 v235,v238,v239
v_cvt_pk_bf16_f32 v236,v248,v249
v_cvt_pk_bf16_f32 v237,v250,v251
buffer_load_dwordx4 v4,s[24:27],s47 offen nt lds
v_cvt_pk_bf16_f32 v238,v252,v253
v_cvt_pk_bf16_f32 v239,v254,v255
v_exp_f32 v139,v139
v_exp_f32 v140,v140
v_exp_f32 v141,v141
v_mfma_f32_16x16x32_bf16 v[168:171],a[128:131],v[132:135],v[36:39]
v_exp_f32 v142,v142
v_mfma_f32_16x16x32_bf16 v[168:171],a[132:135],v[164:167],v[168:171]
v_exp_f32 v143,v143
v_mfma_f32_16x16x32_bf16 v[168:171],a[136:139],v[196:199],v[168:171]
v_exp_f32 v152,v152
v_mfma_f32_16x16x32_bf16 v[168:171],a[140:143],v[228:231],v[168:171]
v_exp_f32 v153,v153
v_mfma_f32_16x16x32_bf16 v[172:175],a[156:159],v[228:231],v[36:39]
v_exp_f32 v154,v154
v_mfma_f32_16x16x32_bf16 v[172:175],a[152:155],v[196:199],v[172:175]
v_exp_f32 v155,v155
v_mfma_f32_16x16x32_bf16 v[172:175],a[148:151],v[164:167],v[172:175]
v_exp_f32 v156,v156
v_mfma_f32_16x16x32_bf16 v[172:175],a[144:147],v[132:135],v[172:175]
v_exp_f32 v157,v157
v_mfma_f32_16x16x32_bf16 v[184:187],a[160:163],v[132:135],v[36:39]
buffer_load_dwordx4 v5,s[24:27],s47 offen offset:1024 nt lds
v_mfma_f32_16x16x32_bf16 v[184:187],a[164:167],v[164:167],v[184:187]
v_exp_f32 v158,v158
v_mfma_f32_16x16x32_bf16 v[184:187],a[168:171],v[196:199],v[184:187]
v_exp_f32 v159,v159
v_mfma_f32_16x16x32_bf16 v[184:187],a[172:175],v[228:231],v[184:187]
v_exp_f32 v168,v168
v_mfma_f32_16x16x32_bf16 v[188:191],a[188:191],v[228:231],v[36:39]
v_exp_f32 v169,v169
v_mfma_f32_16x16x32_bf16 v[188:191],a[184:187],v[196:199],v[188:191]
v_exp_f32 v170,v170
v_mfma_f32_16x16x32_bf16 v[188:191],a[180:183],v[164:167],v[188:191]
v_exp_f32 v171,v171
v_mfma_f32_16x16x32_bf16 v[188:191],a[176:179],v[132:135],v[188:191]
v_exp_f32 v172,v172
v_mfma_f32_16x16x32_bf16 a[96:99],a[192:195],v[232:235],a[96:99]
ds_read_b64_tr_b16 a[192:193],v2
v_cvt_pk_bf16_f32 v168,v168,v169
)A"
R"A(v_mfma_f32_16x16x32_bf16 a[96:99],a[196:199],v[236:239],a[96:99]
ds_read_b64_tr_b16 a[194:195],v2 offset:4096
v_cvt_pk_bf16_f32 v169,v170,v171
v_mfma_f32_16x16x32_bf16 a[100:103],a[204:207],v[236:239],a[100:103]
v_exp_f32 v173,v173
v_mfma_f32_16x16x32_bf16 a[100:103],a[200:203],v[232:235],a[100:103]
ds_read_b64_tr_b16 a[196:197],v2 offset:8192
v_cvt_pk_bf16_f32 v170,v172,v173
v_mfma_f32_16x16x32_bf16 a[104:107],a[208:211],v[232:235],a[104:107]
v_exp_f32 v174,v174
v_mfma_f32_16x16x32_bf16 a[104:107],a[212:215],v[236:239],a[104:107]
v_exp_f32 v175,v175
v_mfma_f32_16x16x32_bf16 a[108:111],a[220:223],v[236:239],a[108:111]
buffer_load_dwordx4 v4,s[24:27],s47 offen offset:2048 nt lds
v_mfma_f32_16x16x32_bf16 a[108:111],a[216:219],v[232:235],a[108:111]
ds_read_b64_tr_b16 a[198:199],v2 offset:12288
v_cvt_pk_bf16_f32 v171,v174,v175
v_mfma_f32_16x16x32_bf16 a[112:115],a[224:227],v[232:235],a[112:115]
v_exp_f32 v184,v184
v_mfma_f32_16x16x32_bf16 a[112:115],a[228:231],v[236:239],a[112:115]
v_exp_f32 v185,v185
v_mfma_f32_16x16x32_bf16 a[116:119],a[236:239],v[236:239],a[116:119]
ds_read_b64_tr_b16 a[204:205],v3 offset:8192
v_cvt_pk_bf16_f32 v136,v136,v137
v_mfma_f32_16x16x32_bf16 a[116:119],a[232:235],v[232:235],a[116:119]
ds_read_b64_tr_b16 a[206:207],v3 offset:12288
v_cvt_pk_bf16_f32 v172,v184,v185
v_mfma_f32_16x16x32_bf16 a[120:123],a[240:243],v[232:235],a[120:123]
ds_read_b64_tr_b16 a[200:201],v3
v_mfma_f32_16x16x32_bf16 a[120:123],a[244:247],v[236:239],a[120:123]
ds_read_b64_tr_b16 a[202:203],v3 offset:4096
v_cvt_pk_bf16_f32 v137,v138,v139
v_mfma_f32_16x16x32_bf16 a[124:127],a[252:255],v[236:239],a[124:127]
ds_read_b64_tr_b16 a[208:209],v2 offset:256
v_mfma_f32_16x16x32_bf16 a[124:127],a[248:251],v[232:235],a[124:127]
ds_read_b64_tr_b16 a[210:211],v2 offset:4352
v_smfmac_f32_16x16x64_bf16 v[60:63],v[28:31],v[232:239],v27
ds_read_b64_tr_b16 a[212:213],v2 offset:8448
ds_read_b64_tr_b16 a[214:215],v2 offset:12544
v_cvt_pk_bf16_f32 v138,v140,v141
ds_read_b64_tr_b16 a[220:221],v3 offset:8448
ds_read_b64_tr_b16 a[222:223],v3 offset:12544
v_cvt_pk_bf16_f32 v139,v142,v143
buffer_load_dwordx4 v5,s[24:27],s47 offen offset:3072 nt lds
s_add_u32 s47,s47,16384
s_and_b32 s47,s47,0x3ffff
ds_read_b64_tr_b16 a[216:217],v3 offset:256
s_or_b32 s47,s47,s46
ds_read_b64_tr_b16 a[218:219],v3 offset:4352
v_cvt_pk_bf16_f32 v140,v152,v153
ds_read_b64_tr_b16 a[224:225],v2 offset:512
ds_read_b64_tr_b16 a[226:227],v2 offset:4608
ds_read_b64_tr_b16 a[228:229],v2 offset:8704
v_cvt_pk_bf16_f32 v141,v154,v155
ds_read_b64_tr_b16 a[230:231],v2 offset:12800
ds_read_b64_tr_b16 a[236:237],v3 offset:8704
ds_read_b64_tr_b16 a[238:239],v3 offset:12800
v_cvt_pk_bf16_f32 v142,v156,v157
ds_read_b64_tr_b16 a[232:233],v3 offset:512
ds_read_b64_tr_b16 a[234:235],v3 offset:4608
ds_read_b64_tr_b16 a[240:241],v2 offset:768
v_cvt_pk_bf16_f32 v143,v158,v159
buffer_load_dword v26,v24,s[88:91],s93 offen
s_waitcnt lgkmcnt(15)
v_mfma_f32_16x16x32_bf16 a[0:3],a[192:195],v[136:139],a[0:3]
ds_read_b64_tr_b16 a[242:243],v2 offset:4864
v_mfma_f32_16x16x32_bf16 a[0:3],a[196:199],v[140:143],a[0:3]
ds_read_b64_tr_b16 a[244:245],v2 offset:8960
v_mfma_f32_16x16x32_bf16 a[4:7],a[204:207],v[140:143],a[4:7]
ds_read_b64_tr_b16 a[246:247],v2 offset:13056
v_mfma_f32_16x16x32_bf16 a[4:7],a[200:203],v[136:139],a[4:7]
ds_read_b64_tr_b16 a[252:253],v3 offset:8960
v_mfma_f32_16x16x32_bf16 a[8:11],a[208:211],v[136:139],a[8:11]
s_waitcnt lgkmcnt(15)
ds_read_b64_tr_b16 a[254:255],v3 offset:13056
v_mfma_f32_16x16x32_bf16 a[8:11],a[212:215],v[140:143],a[8:11]
ds_read_b64_tr_b16 a[248:249],v3 offset:768
v_mfma_f32_16x16x32_bf16 a[12:15],a[220:223],v[140:143],a[12:15]
s_waitcnt lgkmcnt(15)
ds_read_b64_tr_b16 a[250:251],v3 offset:4864
v_mfma_f32_16x16x32_bf16 a[12:15],a[216:219],v[136:139],a[12:15]
s_bitcmp1_b32 s45,0
s_cbranch_scc0 L_t31v21_%=
s_mov_b32 m0,s80
s_add_u32 s80,s80,4096
buffer_load_dwordx4 v25,s[24:27],s83 offen nt lds
s_add_u32 s83,s83,4096
L_t31v21_%=:
s_waitcnt lgkmcnt(7)
v_mfma_f32_16x16x32_bf16 a[16:19],a[224:227],v[136:139],a[16:19]
v_exp_f32 v186,v186
v_mfma_f32_16x16x32_bf16 a[16:19],a[228:231],v[140:143],a[16:19]
v_exp_f32 v187,v187
v_mfma_f32_16x16x32_bf16 a[20:23],a[236:239],v[140:143],a[20:23]
v_exp_f32 v188,v188
v_mfma_f32_16x16x32_bf16 a[20:23],a[232:235],v[136:139],a[20:23]
s_waitcnt lgkmcnt(3)
v_exp_f32 v189,v189
v_mfma_f32_16x16x32_bf16 a[24:27],a[240:243],v[136:139],a[24:27]
v_exp_f32 v190,v190
v_mfma_f32_16x16x32_bf16 a[24:27],a[244:247],v[140:143],a[24:27]
s_waitcnt lgkmcnt(1)
v_cvt_pk_bf16_f32 v173,v186,v187
v_mfma_f32_16x16x32_bf16 a[28:31],a[252:255],v[140:143],a[28:31]
s_waitcnt lgkmcnt(0)
v_exp_f32 v191,v191
v_mfma_f32_16x16x32_bf16 a[28:31],a[248:251],v[136:139],a[28:31]
v_cvt_pk_bf16_f32 v174,v188,v189
v_cvt_pk_bf16_f32 v175,v190,v191
s_cmp_lg_u32 s53,0
v_smfmac_f32_16x16x64_bf16 v[48:51],v[28:31],v[136:143],v27
s_cbranch_scc1 L_att1v2_%=
s_waitcnt lgkmcnt(0)
s_nop 7
s_branch L_att1x_%=
.p2align 5
L_att1_%=:
v_mfma_f32_16x16x32_bf16 v[232:235],a[128:131],v[148:151],v[44:47]
s_waitcnt vmcnt(8) lgkmcnt(0)
v_exp_f32 v200,v200
v_mfma_f32_16x16x32_bf16 v[232:235],a[132:135],v[180:183],v[232:235]
s_barrier
s_add_u32 m0,s49,s51
ds_read_b128 a[128:131],v1
v_mfma_f32_16x16x32_bf16 v[232:235],a[136:139],v[212:215],v[232:235]
s_mov_b32 s90,L_out_end_%=-L_pc_%=+128
v_exp_f32 v201,v201
v_mfma_f32_16x16x32_bf16 v[232:235],a[140:143],v[244:247],v[232:235]
s_lshr_b32 s45,s45,1
ds_read_b128 a[132:135],v1 offset:256
v_cvt_pk_bf16_f32 v200,v200,v201
v_mfma_f32_16x16x32_bf16 v[236:239],a[156:159],v[244:247],v[44:47]
s_sub_u32 s94,s94,3
buffer_load_dwordx4 v4,s[20:23],s47 offen nt lds
v_mfma_f32_16x16x32_bf16 v[236:239],a[152:155],v[212:215],v[236:239]
s_lshr_b32 s60,s94,2
ds_read_b128 a[136:139],v1 offset:512
v_add_u32 v2,s48,v2
v_mfma_f32_16x16x32_bf16 v[236:239],a[148:151],v[180:183],v[236:239]
s_max_i32 s60,s60,128
v_exp_f32 v202,v202
v_mfma_f32_16x16x32_bf16 v[236:239],a[144:147],v[148:151],v[236:239]
s_add_u32 s62,s50,32768
ds_read_b128 a[140:143],v1 offset:768
v_add_u32 v3,s48,v3
v_mfma_f32_16x16x32_bf16 v[248:251],a[160:163],v[148:151],v[44:47]
s_and_b32 s62,s62,131071
v_exp_f32 v203,v203
v_mfma_f32_16x16x32_bf16 v[248:251],a[164:167],v[180:183],v[248:251]
s_add_u32 s54,s54,16384
ds_read_b128 a[156:159],v1 offset:4864
v_cvt_pk_bf16_f32 v201,v202,v203
v_mfma_f32_16x16x32_bf16 v[248:251],a[168:171],v[212:215],v[248:251]
s_sub_u32 s48,s62,s50
v_exp_f32 v204,v204
v_mfma_f32_16x16x32_bf16 v[248:251],a[172:175],v[244:247],v[248:251]
s_sub_u32 s53,s53,1
v_exp_f32 v205,v205
v_mfma_f32_16x16x32_bf16 v[252:255],a[188:191],v[244:247],v[44:47]
s_and_b32 s46,s54,0xfffc0000
ds_read_b128 a[152:155],v1 offset:4608
v_cvt_pk_bf16_f32 v202,v204,v205
v_mfma_f32_16x16x32_bf16 v[252:255],a[184:187],v[212:215],v[252:255]
s_mov_b32 s50,s62
v_exp_f32 v206,v206
v_mfma_f32_16x16x32_bf16 v[252:255],a[180:183],v[180:183],v[252:255]
v_exp_f32 v207,v207
v_mfma_f32_16x16x32_bf16 v[252:255],a[176:179],v[148:151],v[252:255]
ds_read_b128 a[148:151],v1 offset:4352
v_cvt_pk_bf16_f32 v203,v206,v207
v_mfma_f32_16x16x32_bf16 a[32:35],a[192:195],v[168:171],a[32:35]
v_exp_f32 v216,v216
v_mfma_f32_16x16x32_bf16 a[32:35],a[196:199],v[172:175],a[32:35]
ds_read_b128 a[144:147],v1 offset:4096
v_mfma_f32_16x16x32_bf16 a[36:39],a[204:207],v[172:175],a[36:39]
buffer_load_dwordx4 v4,s[20:23],s47 offen offset:1024 nt lds
v_mfma_f32_16x16x32_bf16 a[36:39],a[200:203],v[168:171],a[36:39]
ds_read_b128 a[160:163],v1 offset:8192
v_mfma_f32_16x16x32_bf16 a[40:43],a[208:211],v[168:171],a[40:43]
v_exp_f32 v217,v217
v_mfma_f32_16x16x32_bf16 a[40:43],a[212:215],v[172:175],a[40:43]
ds_read_b128 a[164:167],v1 offset:8448
v_cvt_pk_bf16_f32 v204,v216,v217
v_mfma_f32_16x16x32_bf16 a[44:47],a[220:223],v[172:175],a[44:47]
v_exp_f32 v218,v218
v_mfma_f32_16x16x32_bf16 a[44:47],a[216:219],v[168:171],a[44:47]
ds_read_b128 a[168:171],v1 offset:8704
v_mfma_f32_16x16x32_bf16 a[48:51],a[224:227],v[168:171],a[48:51]
v_exp_f32 v219,v219
v_mfma_f32_16x16x32_bf16 a[48:51],a[228:231],v[172:175],a[48:51]
ds_read_b128 a[172:175],v1 offset:8960
v_cvt_pk_bf16_f32 v205,v218,v219
v_mfma_f32_16x16x32_bf16 a[52:55],a[236:239],v[172:175],a[52:55]
v_exp_f32 v220,v220
v_mfma_f32_16x16x32_bf16 a[52:55],a[232:235],v[168:171],a[52:55]
ds_read_b128 a[188:191],v1 offset:13056
v_mfma_f32_16x16x32_bf16 a[56:59],a[240:243],v[168:171],a[56:59]
v_exp_f32 v221,v221
v_mfma_f32_16x16x32_bf16 a[56:59],a[244:247],v[172:175],a[56:59]
ds_read_b128 a[184:187],v1 offset:12800
v_cvt_pk_bf16_f32 v206,v220,v221
v_mfma_f32_16x16x32_bf16 a[60:63],a[252:255],v[172:175],a[60:63]
v_exp_f32 v222,v222
v_mfma_f32_16x16x32_bf16 a[60:63],a[248:251],v[168:171],a[60:63]
ds_read_b128 a[180:183],v1 offset:12544
v_smfmac_f32_16x16x64_bf16 v[52:55],v[28:31],v[168:175],v27
s_waitcnt lgkmcnt(4)
v_mfma_f32_16x16x32_bf16 v[136:139],a[128:131],v[128:131],v[32:35]
buffer_load_dwordx4 v5,s[20:23],s47 offen offset:2048 nt lds
v_mfma_f32_16x16x32_bf16 v[136:139],a[132:135],v[160:163],v[136:139]
ds_read_b128 a[176:179],v1 offset:12288
v_add_u32 v1,s48,v1
v_mfma_f32_16x16x32_bf16 v[136:139],a[136:139],v[192:195],v[136:139]
v_exp_f32 v223,v223
v_mfma_f32_16x16x32_bf16 v[136:139],a[140:143],v[224:227],v[136:139]
v_cvt_pk_bf16_f32 v207,v222,v223
v_mfma_f32_16x16x32_bf16 v[140:143],a[156:159],v[224:227],v[32:35]
v_exp_f32 v232,v232
v_mfma_f32_16x16x32_bf16 v[140:143],a[152:155],v[192:195],v[140:143]
v_exp_f32 v233,v233
v_mfma_f32_16x16x32_bf16 v[140:143],a[148:151],v[160:163],v[140:143]
v_exp_f32 v234,v234
v_mfma_f32_16x16x32_bf16 v[140:143],a[144:147],v[128:131],v[140:143]
v_exp_f32 v235,v235
v_mfma_f32_16x16x32_bf16 v[152:155],a[160:163],v[128:131],v[32:35]
v_exp_f32 v236,v236
v_mfma_f32_16x16x32_bf16 v[152:155],a[164:167],v[160:163],v[152:155]
v_exp_f32 v237,v237
v_mfma_f32_16x16x32_bf16 v[152:155],a[168:171],v[192:195],v[152:155]
s_waitcnt lgkmcnt(0)
v_exp_f32 v238,v238
v_mfma_f32_16x16x32_bf16 v[152:155],a[172:175],v[224:227],v[152:155]
v_exp_f32 v239,v239
v_mfma_f32_16x16x32_bf16 v[156:159],a[188:191],v[224:227],v[32:35]
v_exp_f32 v248,v248
v_mfma_f32_16x16x32_bf16 v[156:159],a[184:187],v[192:195],v[156:159]
buffer_load_dwordx4 v5,s[20:23],s47 offen offset:3072 nt lds
v_mfma_f32_16x16x32_bf16 v[156:159],a[180:183],v[160:163],v[156:159]
v_exp_f32 v249,v249
v_mfma_f32_16x16x32_bf16 v[156:159],a[176:179],v[128:131],v[156:159]
v_exp_f32 v250,v250
v_mfma_f32_16x16x32_bf16 a[64:67],a[192:195],v[200:203],a[64:67]
v_exp_f32 v251,v251
v_mfma_f32_16x16x32_bf16 a[64:67],a[196:199],v[204:207],a[64:67]
v_exp_f32 v252,v252
v_mfma_f32_16x16x32_bf16 a[68:71],a[204:207],v[204:207],a[68:71]
v_exp_f32 v253,v253
v_mfma_f32_16x16x32_bf16 a[68:71],a[200:203],v[200:203],a[68:71]
v_exp_f32 v254,v254
v_mfma_f32_16x16x32_bf16 a[72:75],a[208:211],v[200:203],a[72:75]
v_exp_f32 v255,v255
v_mfma_f32_16x16x32_bf16 a[72:75],a[212:215],v[204:207],a[72:75]
v_exp_f32 v136,v136
v_mfma_f32_16x16x32_bf16 a[76:79],a[220:223],v[204:207],a[76:79]
v_exp_f32 v137,v137
v_mfma_f32_16x16x32_bf16 a[76:79],a[216:219],v[200:203],a[76:79]
v_exp_f32 v138,v138
v_mfma_f32_16x16x32_bf16 a[80:83],a[224:227],v[200:203],a[80:83]
s_add_u32 m0,s49,s52
v_cvt_pk_bf16_f32 v232,v232,v233
v_cvt_pk_bf16_f32 v233,v234,v235
v_mfma_f32_16x16x32_bf16 a[80:83],a[228:231],v[204:207],a[80:83]
s_xor_b32 s49,s50,65536
v_cvt_pk_bf16_f32 v234,v236,v237
v_cvt_pk_bf16_f32 v235,v238,v239
v_mfma_f32_16x16x32_bf16 a[84:87],a[236:239],v[204:207],a[84:87]
v_cvt_pk_bf16_f32 v236,v248,v249
v_cvt_pk_bf16_f32 v237,v250,v251
v_mfma_f32_16x16x32_bf16 a[84:87],a[232:235],v[200:203],a[84:87]
buffer_load_dwordx4 v4,s[24:27],s47 offen nt lds
v_mfma_f32_16x16x32_bf16 a[88:91],a[240:243],v[200:203],a[88:91]
v_cvt_pk_bf16_f32 v238,v252,v253
v_cvt_pk_bf16_f32 v239,v254,v255
v_mfma_f32_16x16x32_bf16 a[88:91],a[244:247],v[204:207],a[88:91]
v_exp_f32 v139,v139
v_mfma_f32_16x16x32_bf16 a[92:95],a[252:255],v[204:207],a[92:95]
v_exp_f32 v140,v140
v_mfma_f32_16x16x32_bf16 a[92:95],a[248:251],v[200:203],a[92:95]
v_exp_f32 v141,v141
v_smfmac_f32_16x16x64_bf16 v[56:59],v[28:31],v[200:207],v27
)A"
R"A(v_mfma_f32_16x16x32_bf16 v[168:171],a[128:131],v[132:135],v[36:39]
v_exp_f32 v142,v142
v_mfma_f32_16x16x32_bf16 v[168:171],a[132:135],v[164:167],v[168:171]
v_exp_f32 v143,v143
v_mfma_f32_16x16x32_bf16 v[168:171],a[136:139],v[196:199],v[168:171]
v_exp_f32 v152,v152
v_mfma_f32_16x16x32_bf16 v[168:171],a[140:143],v[228:231],v[168:171]
v_exp_f32 v153,v153
v_mfma_f32_16x16x32_bf16 v[172:175],a[156:159],v[228:231],v[36:39]
v_exp_f32 v154,v154
v_mfma_f32_16x16x32_bf16 v[172:175],a[152:155],v[196:199],v[172:175]
v_exp_f32 v155,v155
v_mfma_f32_16x16x32_bf16 v[172:175],a[148:151],v[164:167],v[172:175]
v_exp_f32 v156,v156
v_mfma_f32_16x16x32_bf16 v[172:175],a[144:147],v[132:135],v[172:175]
v_exp_f32 v157,v157
v_mfma_f32_16x16x32_bf16 v[184:187],a[160:163],v[132:135],v[36:39]
buffer_load_dwordx4 v5,s[24:27],s47 offen offset:1024 nt lds
v_mfma_f32_16x16x32_bf16 v[184:187],a[164:167],v[164:167],v[184:187]
v_exp_f32 v158,v158
v_mfma_f32_16x16x32_bf16 v[184:187],a[168:171],v[196:199],v[184:187]
v_exp_f32 v159,v159
v_mfma_f32_16x16x32_bf16 v[184:187],a[172:175],v[228:231],v[184:187]
v_exp_f32 v168,v168
v_mfma_f32_16x16x32_bf16 v[188:191],a[188:191],v[228:231],v[36:39]
v_exp_f32 v169,v169
v_mfma_f32_16x16x32_bf16 v[188:191],a[184:187],v[196:199],v[188:191]
v_exp_f32 v170,v170
v_mfma_f32_16x16x32_bf16 v[188:191],a[180:183],v[164:167],v[188:191]
v_exp_f32 v171,v171
v_mfma_f32_16x16x32_bf16 v[188:191],a[176:179],v[132:135],v[188:191]
v_exp_f32 v172,v172
v_mfma_f32_16x16x32_bf16 a[96:99],a[192:195],v[232:235],a[96:99]
ds_read_b64_tr_b16 a[192:193],v2
v_cvt_pk_bf16_f32 v168,v168,v169
v_mfma_f32_16x16x32_bf16 a[96:99],a[196:199],v[236:239],a[96:99]
ds_read_b64_tr_b16 a[194:195],v2 offset:4096
v_cvt_pk_bf16_f32 v169,v170,v171
v_mfma_f32_16x16x32_bf16 a[100:103],a[204:207],v[236:239],a[100:103]
v_exp_f32 v173,v173
v_mfma_f32_16x16x32_bf16 a[100:103],a[200:203],v[232:235],a[100:103]
ds_read_b64_tr_b16 a[196:197],v2 offset:8192
v_cvt_pk_bf16_f32 v170,v172,v173
v_mfma_f32_16x16x32_bf16 a[104:107],a[208:211],v[232:235],a[104:107]
v_exp_f32 v174,v174
v_mfma_f32_16x16x32_bf16 a[104:107],a[212:215],v[236:239],a[104:107]
v_exp_f32 v175,v175
v_mfma_f32_16x16x32_bf16 a[108:111],a[220:223],v[236:239],a[108:111]
buffer_load_dwordx4 v4,s[24:27],s47 offen offset:2048 nt lds
v_mfma_f32_16x16x32_bf16 a[108:111],a[216:219],v[232:235],a[108:111]
ds_read_b64_tr_b16 a[198:199],v2 offset:12288
v_cvt_pk_bf16_f32 v171,v174,v175
v_mfma_f32_16x16x32_bf16 a[112:115],a[224:227],v[232:235],a[112:115]
v_exp_f32 v184,v184
v_mfma_f32_16x16x32_bf16 a[112:115],a[228:231],v[236:239],a[112:115]
v_exp_f32 v185,v185
v_mfma_f32_16x16x32_bf16 a[116:119],a[236:239],v[236:239],a[116:119]
ds_read_b64_tr_b16 a[204:205],v3 offset:8192
v_cvt_pk_bf16_f32 v136,v136,v137
v_mfma_f32_16x16x32_bf16 a[116:119],a[232:235],v[232:235],a[116:119]
ds_read_b64_tr_b16 a[206:207],v3 offset:12288
v_cvt_pk_bf16_f32 v172,v184,v185
v_mfma_f32_16x16x32_bf16 a[120:123],a[240:243],v[232:235],a[120:123]
ds_read_b64_tr_b16 a[200:201],v3
v_mfma_f32_16x16x32_bf16 a[120:123],a[244:247],v[236:239],a[120:123]
ds_read_b64_tr_b16 a[202:203],v3 offset:4096
v_cvt_pk_bf16_f32 v137,v138,v139
v_mfma_f32_16x16x32_bf16 a[124:127],a[252:255],v[236:239],a[124:127]
ds_read_b64_tr_b16 a[208:209],v2 offset:256
v_mfma_f32_16x16x32_bf16 a[124:127],a[248:251],v[232:235],a[124:127]
ds_read_b64_tr_b16 a[210:211],v2 offset:4352
v_smfmac_f32_16x16x64_bf16 v[60:63],v[28:31],v[232:239],v27
ds_read_b64_tr_b16 a[212:213],v2 offset:8448
v_mfma_f32_16x16x32_bf16 v[200:203],a[128:131],v[144:147],v[40:43]
ds_read_b64_tr_b16 a[214:215],v2 offset:12544
v_cvt_pk_bf16_f32 v138,v140,v141
v_mfma_f32_16x16x32_bf16 v[200:203],a[132:135],v[176:179],v[200:203]
ds_read_b64_tr_b16 a[220:221],v3 offset:8448
v_mfma_f32_16x16x32_bf16 v[200:203],a[136:139],v[208:211],v[200:203]
ds_read_b64_tr_b16 a[222:223],v3 offset:12544
v_cvt_pk_bf16_f32 v139,v142,v143
v_mfma_f32_16x16x32_bf16 v[200:203],a[140:143],v[240:243],v[200:203]
buffer_load_dwordx4 v5,s[24:27],s47 offen offset:3072 nt lds
v_mfma_f32_16x16x32_bf16 v[204:207],a[156:159],v[240:243],v[40:43]
s_add_u32 s47,s47,16384
s_and_b32 s47,s47,0x3ffff
ds_read_b64_tr_b16 a[216:217],v3 offset:256
v_mfma_f32_16x16x32_bf16 v[204:207],a[152:155],v[208:211],v[204:207]
s_or_b32 s47,s47,s46
ds_read_b64_tr_b16 a[218:219],v3 offset:4352
v_cvt_pk_bf16_f32 v140,v152,v153
v_mfma_f32_16x16x32_bf16 v[204:207],a[148:151],v[176:179],v[204:207]
ds_read_b64_tr_b16 a[224:225],v2 offset:512
v_mfma_f32_16x16x32_bf16 v[204:207],a[144:147],v[144:147],v[204:207]
ds_read_b64_tr_b16 a[226:227],v2 offset:4608
v_mfma_f32_16x16x32_bf16 v[216:219],a[160:163],v[144:147],v[40:43]
ds_read_b64_tr_b16 a[228:229],v2 offset:8704
v_cvt_pk_bf16_f32 v141,v154,v155
v_mfma_f32_16x16x32_bf16 v[216:219],a[164:167],v[176:179],v[216:219]
ds_read_b64_tr_b16 a[230:231],v2 offset:12800
v_mfma_f32_16x16x32_bf16 v[216:219],a[168:171],v[208:211],v[216:219]
ds_read_b64_tr_b16 a[236:237],v3 offset:8704
v_mfma_f32_16x16x32_bf16 v[216:219],a[172:175],v[240:243],v[216:219]
ds_read_b64_tr_b16 a[238:239],v3 offset:12800
v_cvt_pk_bf16_f32 v142,v156,v157
v_mfma_f32_16x16x32_bf16 v[220:223],a[188:191],v[240:243],v[40:43]
ds_read_b64_tr_b16 a[232:233],v3 offset:512
v_mfma_f32_16x16x32_bf16 v[220:223],a[184:187],v[208:211],v[220:223]
ds_read_b64_tr_b16 a[234:235],v3 offset:4608
v_mfma_f32_16x16x32_bf16 v[220:223],a[180:183],v[176:179],v[220:223]
ds_read_b64_tr_b16 a[240:241],v2 offset:768
v_cvt_pk_bf16_f32 v143,v158,v159
v_mfma_f32_16x16x32_bf16 v[220:223],a[176:179],v[144:147],v[220:223]
s_waitcnt lgkmcnt(7)
buffer_load_dword v26,v24,s[88:91],s93 offen
v_mfma_f32_16x16x32_bf16 a[0:3],a[192:195],v[136:139],a[0:3]
ds_read_b64_tr_b16 a[242:243],v2 offset:4864
v_mfma_f32_16x16x32_bf16 a[0:3],a[196:199],v[140:143],a[0:3]
ds_read_b64_tr_b16 a[244:245],v2 offset:8960
v_mfma_f32_16x16x32_bf16 a[4:7],a[204:207],v[140:143],a[4:7]
ds_read_b64_tr_b16 a[246:247],v2 offset:13056
v_mfma_f32_16x16x32_bf16 a[4:7],a[200:203],v[136:139],a[4:7]
s_memrealtime s[76:77]
ds_read_b64_tr_b16 a[252:253],v3 offset:8960
v_mfma_f32_16x16x32_bf16 a[8:11],a[208:211],v[136:139],a[8:11]
ds_read_b64_tr_b16 a[254:255],v3 offset:13056
v_mfma_f32_16x16x32_bf16 a[8:11],a[212:215],v[140:143],a[8:11]
ds_read_b64_tr_b16 a[248:249],v3 offset:768
v_mfma_f32_16x16x32_bf16 a[12:15],a[220:223],v[140:143],a[12:15]
ds_read_b64_tr_b16 a[250:251],v3 offset:4864
v_mfma_f32_16x16x32_bf16 a[12:15],a[216:219],v[136:139],a[12:15]
s_bitcmp1_b32 s45,0
s_cbranch_scc0 L_t311_%=
s_mov_b32 m0,s80
s_add_u32 s80,s80,4096
buffer_load_dwordx4 v25,s[24:27],s83 offen nt lds
s_add_u32 s83,s83,4096
L_t311_%=:
v_mfma_f32_16x16x32_bf16 a[16:19],a[224:227],v[136:139],a[16:19]
s_waitcnt lgkmcnt(6)
v_exp_f32 v186,v186
v_mfma_f32_16x16x32_bf16 a[16:19],a[228:231],v[140:143],a[16:19]
v_exp_f32 v187,v187
v_mfma_f32_16x16x32_bf16 a[20:23],a[236:239],v[140:143],a[20:23]
s_waitcnt lgkmcnt(0)
v_exp_f32 v188,v188
v_mfma_f32_16x16x32_bf16 a[20:23],a[232:235],v[136:139],a[20:23]
s_sub_u32 s79,s78,s76
v_exp_f32 v189,v189
v_mfma_f32_16x16x32_bf16 a[24:27],a[240:243],v[136:139],a[24:27]
s_add_u32 s78,s76,s60
v_exp_f32 v190,v190
v_mfma_f32_16x16x32_bf16 a[24:27],a[244:247],v[140:143],a[24:27]
s_cmp_gt_i32 s79,s95
s_cbranch_scc1 L_ps7_%=
L_pr7_%=:
v_cvt_pk_bf16_f32 v173,v186,v187
v_mfma_f32_16x16x32_bf16 a[28:31],a[252:255],v[140:143],a[28:31]
v_exp_f32 v191,v191
v_mfma_f32_16x16x32_bf16 a[28:31],a[248:251],v[136:139],a[28:31]
v_cvt_pk_bf16_f32 v174,v188,v189
v_cvt_pk_bf16_f32 v175,v190,v191
s_cmp_lg_u32 s53,0
v_smfmac_f32_16x16x64_bf16 v[48:51],v[28:31],v[136:143],v27
s_cbranch_scc1 L_att1_%=
L_att1x_%=:
L_endseg_%=:
v_mfma_f32_16x16x32_bf16 v[232:235],a[128:131],v[148:151],v[44:47]
s_waitcnt vmcnt(10)
s_lshl_b32 s43,s41,2
v_exp_f32 v200,v200
v_mfma_f32_16x16x32_bf16 v[232:235],a[132:135],v[180:183],v[232:235]
s_barrier
ds_read_b128 a[128:131],v1
v_add_u32 v2,s48,v2
v_mfma_f32_16x16x32_bf16 v[232:235],a[136:139],v[212:215],v[232:235]
s_add_u32 s43,s43,s70
v_exp_f32 v201,v201
v_mfma_f32_16x16x32_bf16 v[232:235],a[140:143],v[244:247],v[232:235]
s_lshl_b32 s43,s43,19
ds_read_b128 a[132:135],v1 offset:256
v_cvt_pk_bf16_f32 v200,v200,v201
v_mfma_f32_16x16x32_bf16 v[236:239],a[156:159],v[244:247],v[44:47]
s_add_u32 s43,s43,s55
v_exp_f32 v202,v202
v_mfma_f32_16x16x32_bf16 v[236:239],a[152:155],v[212:215],v[236:239]
s_mov_b64 s[28:29],s[12:13]
ds_read_b128 a[136:139],v1 offset:512
v_add_u32 v3,s48,v3
v_mfma_f32_16x16x32_bf16 v[236:239],a[148:151],v[180:183],v[236:239]
s_mov_b32 s48,32768
v_exp_f32 v203,v203
v_mfma_f32_16x16x32_bf16 v[236:239],a[144:147],v[148:151],v[236:239]
s_mov_b32 s30,475136
ds_read_b128 a[140:143],v1 offset:768
v_cvt_pk_bf16_f32 v201,v202,v203
v_mfma_f32_16x16x32_bf16 v[248:251],a[160:163],v[148:151],v[44:47]
s_mov_b32 s31,0x20000
v_exp_f32 v204,v204
v_mfma_f32_16x16x32_bf16 v[248:251],a[164:167],v[180:183],v[248:251]
ds_read_b128 a[156:159],v1 offset:4864
v_lshrrev_b32 v4,6,v6
v_mfma_f32_16x16x32_bf16 v[248:251],a[168:171],v[212:215],v[248:251]
v_exp_f32 v205,v205
v_mfma_f32_16x16x32_bf16 v[248:251],a[172:175],v[244:247],v[248:251]
ds_read_b128 a[152:155],v1 offset:4608
v_cvt_pk_bf16_f32 v202,v204,v205
v_mfma_f32_16x16x32_bf16 v[252:255],a[188:191],v[244:247],v[44:47]
v_exp_f32 v206,v206
v_mfma_f32_16x16x32_bf16 v[252:255],a[184:187],v[212:215],v[252:255]
ds_read_b128 a[148:151],v1 offset:4352
v_mul_u32_u24 v4,320,v4
v_mfma_f32_16x16x32_bf16 v[252:255],a[180:183],v[180:183],v[252:255]
v_exp_f32 v207,v207
v_mfma_f32_16x16x32_bf16 v[252:255],a[176:179],v[148:151],v[252:255]
ds_read_b128 a[144:147],v1 offset:4096
v_cvt_pk_bf16_f32 v203,v206,v207
v_mfma_f32_16x16x32_bf16 a[32:35],a[192:195],v[168:171],a[32:35]
v_exp_f32 v216,v216
v_mfma_f32_16x16x32_bf16 a[32:35],a[196:199],v[172:175],a[32:35]
ds_read_b128 a[160:163],v1 offset:8192
v_bfe_u32 v5,v6,4,2
v_mfma_f32_16x16x32_bf16 a[36:39],a[204:207],v[172:175],a[36:39]
v_exp_f32 v217,v217
v_mfma_f32_16x16x32_bf16 a[36:39],a[200:203],v[168:171],a[36:39]
ds_read_b128 a[164:167],v1 offset:8448
v_cvt_pk_bf16_f32 v204,v216,v217
v_mfma_f32_16x16x32_bf16 a[40:43],a[208:211],v[168:171],a[40:43]
v_exp_f32 v218,v218
v_mfma_f32_16x16x32_bf16 a[40:43],a[212:215],v[172:175],a[40:43]
ds_read_b128 a[168:171],v1 offset:8704
v_lshl_add_u32 v4,v5,3,v4
v_mfma_f32_16x16x32_bf16 a[44:47],a[220:223],v[172:175],a[44:47]
v_exp_f32 v219,v219
v_mfma_f32_16x16x32_bf16 a[44:47],a[216:219],v[168:171],a[44:47]
ds_read_b128 a[172:175],v1 offset:8960
v_cvt_pk_bf16_f32 v205,v218,v219
v_mfma_f32_16x16x32_bf16 a[48:51],a[224:227],v[168:171],a[48:51]
v_exp_f32 v220,v220
v_mfma_f32_16x16x32_bf16 a[48:51],a[228:231],v[172:175],a[48:51]
ds_read_b128 a[188:191],v1 offset:13056
v_bfe_u32 v5,v6,8,1
v_mfma_f32_16x16x32_bf16 a[52:55],a[236:239],v[172:175],a[52:55]
v_exp_f32 v221,v221
v_mfma_f32_16x16x32_bf16 a[52:55],a[232:235],v[168:171],a[52:55]
ds_read_b128 a[184:187],v1 offset:12800
v_cvt_pk_bf16_f32 v206,v220,v221
v_mfma_f32_16x16x32_bf16 a[56:59],a[240:243],v[168:171],a[56:59]
v_exp_f32 v222,v222
v_mfma_f32_16x16x32_bf16 a[56:59],a[244:247],v[172:175],a[56:59]
ds_read_b128 a[180:183],v1 offset:12544
v_lshlrev_b32 v5,5,v5
v_mfma_f32_16x16x32_bf16 a[60:63],a[252:255],v[172:175],a[60:63]
v_exp_f32 v223,v223
v_mfma_f32_16x16x32_bf16 a[60:63],a[248:251],v[168:171],a[60:63]
ds_read_b128 a[176:179],v1 offset:12288
v_cvt_pk_bf16_f32 v207,v222,v223
v_smfmac_f32_16x16x64_bf16 v[52:55],v[28:31],v[168:175],v27
s_waitcnt lgkmcnt(4)
v_add_u32 v1,s48,v1
v_mfma_f32_16x16x32_bf16 v[136:139],a[128:131],v[128:131],v[32:35]
v_exp_f32 v232,v232
v_mfma_f32_16x16x32_bf16 v[136:139],a[132:135],v[160:163],v[136:139]
v_exp_f32 v233,v233
v_mfma_f32_16x16x32_bf16 v[136:139],a[136:139],v[192:195],v[136:139]
v_exp_f32 v234,v234
v_mfma_f32_16x16x32_bf16 v[136:139],a[140:143],v[224:227],v[136:139]
v_exp_f32 v235,v235
v_mfma_f32_16x16x32_bf16 v[140:143],a[156:159],v[224:227],v[32:35]
v_cvt_pk_bf16_f32 v232,v232,v233
v_cvt_pk_bf16_f32 v233,v234,v235
v_mfma_f32_16x16x32_bf16 v[140:143],a[152:155],v[192:195],v[140:143]
s_add_u32 s80,s71,0
)A"
R"A(v_exp_f32 v236,v236
v_mfma_f32_16x16x32_bf16 v[140:143],a[148:151],v[160:163],v[140:143]
s_and_b32 s80,s80,3
v_exp_f32 v237,v237
v_mfma_f32_16x16x32_bf16 v[140:143],a[144:147],v[128:131],v[140:143]
s_lshl_b32 s80,s80,1
s_mul_i32 s80,s80,163840
s_add_u32 s80,s80,s81
v_mfma_f32_16x16x32_bf16 v[152:155],a[160:163],v[128:131],v[32:35]
s_add_u32 s83,s80,20480
v_exp_f32 v238,v238
v_mfma_f32_16x16x32_bf16 v[152:155],a[164:167],v[160:163],v[152:155]
buffer_store_dwordx4 v[64:67],v6,s[36:39],s84 offen sc1
v_mfma_f32_16x16x32_bf16 v[152:155],a[168:171],v[192:195],v[152:155]
v_exp_f32 v239,v239
v_mfma_f32_16x16x32_bf16 v[152:155],a[172:175],v[224:227],v[152:155]
s_waitcnt lgkmcnt(0)
v_cvt_pk_bf16_f32 v234,v236,v237
v_cvt_pk_bf16_f32 v235,v238,v239
v_mfma_f32_16x16x32_bf16 v[156:159],a[188:191],v[224:227],v[32:35]
v_exp_f32 v248,v248
v_mfma_f32_16x16x32_bf16 v[156:159],a[184:187],v[192:195],v[156:159]
v_exp_f32 v249,v249
v_mfma_f32_16x16x32_bf16 v[156:159],a[180:183],v[160:163],v[156:159]
v_exp_f32 v250,v250
v_mfma_f32_16x16x32_bf16 v[156:159],a[176:179],v[128:131],v[156:159]
v_exp_f32 v251,v251
v_mfma_f32_16x16x32_bf16 a[64:67],a[192:195],v[200:203],a[64:67]
v_exp_f32 v252,v252
v_mfma_f32_16x16x32_bf16 a[64:67],a[196:199],v[204:207],a[64:67]
v_exp_f32 v253,v253
v_mfma_f32_16x16x32_bf16 a[68:71],a[204:207],v[204:207],a[68:71]
v_exp_f32 v254,v254
v_mfma_f32_16x16x32_bf16 a[68:71],a[200:203],v[200:203],a[68:71]
v_exp_f32 v255,v255
v_mfma_f32_16x16x32_bf16 a[72:75],a[208:211],v[200:203],a[72:75]
v_cvt_pk_bf16_f32 v236,v248,v249
v_cvt_pk_bf16_f32 v237,v250,v251
v_mfma_f32_16x16x32_bf16 a[72:75],a[212:215],v[204:207],a[72:75]
v_cvt_pk_bf16_f32 v238,v252,v253
v_cvt_pk_bf16_f32 v239,v254,v255
v_mfma_f32_16x16x32_bf16 a[76:79],a[220:223],v[204:207],a[76:79]
v_exp_f32 v136,v136
v_mfma_f32_16x16x32_bf16 a[76:79],a[216:219],v[200:203],a[76:79]
v_exp_f32 v137,v137
v_mfma_f32_16x16x32_bf16 a[80:83],a[224:227],v[200:203],a[80:83]
v_exp_f32 v138,v138
v_mfma_f32_16x16x32_bf16 a[80:83],a[228:231],v[204:207],a[80:83]
v_exp_f32 v139,v139
v_mfma_f32_16x16x32_bf16 a[84:87],a[236:239],v[204:207],a[84:87]
s_add_u32 m0,s82,81920
v_exp_f32 v140,v140
v_mfma_f32_16x16x32_bf16 a[84:87],a[232:235],v[200:203],a[84:87]
v_cvt_pk_bf16_f32 v136,v136,v137
v_cvt_pk_bf16_f32 v137,v138,v139
v_mfma_f32_16x16x32_bf16 a[88:91],a[240:243],v[200:203],a[88:91]
v_exp_f32 v141,v141
v_mfma_f32_16x16x32_bf16 a[88:91],a[244:247],v[204:207],a[88:91]
buffer_load_dwordx4 v7,s[72:75],s80 offen lds
v_mfma_f32_16x16x32_bf16 a[92:95],a[252:255],v[204:207],a[92:95]
v_exp_f32 v142,v142
v_mfma_f32_16x16x32_bf16 a[92:95],a[248:251],v[200:203],a[92:95]
v_exp_f32 v143,v143
v_smfmac_f32_16x16x64_bf16 v[56:59],v[28:31],v[200:207],v27
v_cvt_pk_bf16_f32 v138,v140,v141
v_mfma_f32_16x16x32_bf16 v[168:171],a[128:131],v[132:135],v[36:39]
buffer_store_dwordx4 v[68:71],v6,s[36:39],s84 offen offset:1024 sc1
v_mfma_f32_16x16x32_bf16 v[168:171],a[132:135],v[164:167],v[168:171]
s_add_u32 s28,s28,s43
s_addc_u32 s29,s29,0
v_cvt_pk_bf16_f32 v139,v142,v143
v_mfma_f32_16x16x32_bf16 v[168:171],a[136:139],v[196:199],v[168:171]
s_and_b32 s29,s29,0xffff
v_exp_f32 v152,v152
v_mfma_f32_16x16x32_bf16 v[168:171],a[140:143],v[228:231],v[168:171]
v_exp_f32 v153,v153
v_mfma_f32_16x16x32_bf16 v[172:175],a[156:159],v[228:231],v[36:39]
v_exp_f32 v154,v154
v_mfma_f32_16x16x32_bf16 v[172:175],a[152:155],v[196:199],v[172:175]
v_exp_f32 v155,v155
v_mfma_f32_16x16x32_bf16 v[172:175],a[148:151],v[164:167],v[172:175]
v_exp_f32 v156,v156
v_mfma_f32_16x16x32_bf16 v[172:175],a[144:147],v[132:135],v[172:175]
v_exp_f32 v157,v157
v_mfma_f32_16x16x32_bf16 v[184:187],a[160:163],v[132:135],v[36:39]
v_exp_f32 v158,v158
v_mfma_f32_16x16x32_bf16 v[184:187],a[164:167],v[164:167],v[184:187]
v_exp_f32 v159,v159
v_mfma_f32_16x16x32_bf16 v[184:187],a[168:171],v[196:199],v[184:187]
v_cvt_pk_bf16_f32 v140,v152,v153
v_cvt_pk_bf16_f32 v141,v154,v155
v_mfma_f32_16x16x32_bf16 v[184:187],a[172:175],v[228:231],v[184:187]
v_cvt_pk_bf16_f32 v142,v156,v157
v_cvt_pk_bf16_f32 v143,v158,v159
v_mfma_f32_16x16x32_bf16 v[188:191],a[188:191],v[228:231],v[36:39]
v_exp_f32 v168,v168
v_mfma_f32_16x16x32_bf16 v[188:191],a[184:187],v[196:199],v[188:191]
v_exp_f32 v169,v169
v_mfma_f32_16x16x32_bf16 v[188:191],a[180:183],v[164:167],v[188:191]
v_exp_f32 v170,v170
v_mfma_f32_16x16x32_bf16 v[188:191],a[176:179],v[132:135],v[188:191]
v_exp_f32 v171,v171
v_mfma_f32_16x16x32_bf16 a[96:99],a[192:195],v[232:235],a[96:99]
buffer_load_dwordx4 v8,s[72:75],s80 offen offset:1024 lds
v_mfma_f32_16x16x32_bf16 a[96:99],a[196:199],v[236:239],a[96:99]
ds_read_b64_tr_b16 a[192:193],v2
ds_read_b64_tr_b16 a[194:195],v2 offset:4096
v_mfma_f32_16x16x32_bf16 a[100:103],a[204:207],v[236:239],a[100:103]
ds_read_b64_tr_b16 a[196:197],v2 offset:8192
v_cvt_pk_bf16_f32 v168,v168,v169
v_mfma_f32_16x16x32_bf16 a[100:103],a[200:203],v[232:235],a[100:103]
ds_read_b64_tr_b16 a[198:199],v2 offset:12288
v_cvt_pk_bf16_f32 v169,v170,v171
v_mfma_f32_16x16x32_bf16 a[104:107],a[208:211],v[232:235],a[104:107]
buffer_store_dwordx4 v[72:75],v6,s[36:39],s84 offen offset:2048 sc1
v_mfma_f32_16x16x32_bf16 a[104:107],a[212:215],v[236:239],a[104:107]
ds_read_b64_tr_b16 a[204:205],v3 offset:8192
ds_read_b64_tr_b16 a[206:207],v3 offset:12288
v_mfma_f32_16x16x32_bf16 a[108:111],a[220:223],v[236:239],a[108:111]
v_exp_f32 v172,v172
v_mfma_f32_16x16x32_bf16 a[108:111],a[216:219],v[232:235],a[108:111]
ds_read_b64_tr_b16 a[200:201],v3
ds_read_b64_tr_b16 a[202:203],v3 offset:4096
v_mfma_f32_16x16x32_bf16 a[112:115],a[224:227],v[232:235],a[112:115]
v_exp_f32 v173,v173
v_mfma_f32_16x16x32_bf16 a[112:115],a[228:231],v[236:239],a[112:115]
ds_read_b64_tr_b16 a[208:209],v2 offset:256
ds_read_b64_tr_b16 a[210:211],v2 offset:4352
v_mfma_f32_16x16x32_bf16 a[116:119],a[236:239],v[236:239],a[116:119]
ds_read_b64_tr_b16 a[212:213],v2 offset:8448
v_cvt_pk_bf16_f32 v170,v172,v173
v_mfma_f32_16x16x32_bf16 a[116:119],a[232:235],v[232:235],a[116:119]
v_exp_f32 v174,v174
v_mfma_f32_16x16x32_bf16 a[120:123],a[240:243],v[232:235],a[120:123]
ds_read_b64_tr_b16 a[214:215],v2 offset:12544
ds_read_b64_tr_b16 a[220:221],v3 offset:8448
v_mfma_f32_16x16x32_bf16 a[120:123],a[244:247],v[236:239],a[120:123]
ds_read_b64_tr_b16 a[222:223],v3 offset:12544
v_mfma_f32_16x16x32_bf16 a[124:127],a[252:255],v[236:239],a[124:127]
ds_read_b64_tr_b16 a[216:217],v3 offset:256
v_mfma_f32_16x16x32_bf16 a[124:127],a[248:251],v[232:235],a[124:127]
ds_read_b64_tr_b16 a[218:219],v3 offset:4352
v_smfmac_f32_16x16x64_bf16 v[60:63],v[28:31],v[232:239],v27
ds_read_b64_tr_b16 a[224:225],v2 offset:512
v_mfma_f32_16x16x32_bf16 v[200:203],a[128:131],v[144:147],v[40:43]
ds_read_b64_tr_b16 a[226:227],v2 offset:4608
ds_read_b64_tr_b16 a[228:229],v2 offset:8704
v_mfma_f32_16x16x32_bf16 v[200:203],a[132:135],v[176:179],v[200:203]
ds_read_b64_tr_b16 a[230:231],v2 offset:12800
v_mfma_f32_16x16x32_bf16 v[200:203],a[136:139],v[208:211],v[200:203]
ds_read_b64_tr_b16 a[236:237],v3 offset:8704
v_mfma_f32_16x16x32_bf16 v[200:203],a[140:143],v[240:243],v[200:203]
buffer_load_dwordx4 v9,s[72:75],s80 offen offset:2048 lds
v_mfma_f32_16x16x32_bf16 v[204:207],a[156:159],v[240:243],v[40:43]
ds_read_b64_tr_b16 a[238:239],v3 offset:12800
ds_read_b64_tr_b16 a[232:233],v3 offset:512
v_mfma_f32_16x16x32_bf16 v[204:207],a[152:155],v[208:211],v[204:207]
ds_read_b64_tr_b16 a[234:235],v3 offset:4608
v_mfma_f32_16x16x32_bf16 v[204:207],a[148:151],v[176:179],v[204:207]
ds_read_b64_tr_b16 a[240:241],v2 offset:768
v_mfma_f32_16x16x32_bf16 v[204:207],a[144:147],v[144:147],v[204:207]
ds_read_b64_tr_b16 a[242:243],v2 offset:4864
v_mfma_f32_16x16x32_bf16 v[216:219],a[160:163],v[144:147],v[40:43]
ds_read_b64_tr_b16 a[244:245],v2 offset:8960
ds_read_b64_tr_b16 a[246:247],v2 offset:13056
v_mfma_f32_16x16x32_bf16 v[216:219],a[164:167],v[176:179],v[216:219]
buffer_store_dwordx4 v[76:79],v6,s[36:39],s84 offen offset:3072 sc1
v_mfma_f32_16x16x32_bf16 v[216:219],a[168:171],v[208:211],v[216:219]
s_add_u32 s84,s55,65536
ds_read_b64_tr_b16 a[252:253],v3 offset:8960
ds_read_b64_tr_b16 a[254:255],v3 offset:13056
v_mfma_f32_16x16x32_bf16 v[216:219],a[172:175],v[240:243],v[216:219]
s_max_i32 s84,s84,s18
ds_read_b64_tr_b16 a[248:249],v3 offset:768
v_add_u32 v2,s48,v2
v_mfma_f32_16x16x32_bf16 v[220:223],a[188:191],v[240:243],v[40:43]
ds_read_b64_tr_b16 a[250:251],v3 offset:4864
v_add_u32 v3,s48,v3
v_mfma_f32_16x16x32_bf16 v[220:223],a[184:187],v[208:211],v[220:223]
s_mov_b32 s48,98304
v_exp_f32 v175,v175
v_mfma_f32_16x16x32_bf16 v[220:223],a[180:183],v[176:179],v[220:223]
v_exp_f32 v184,v184
v_mfma_f32_16x16x32_bf16 v[220:223],a[176:179],v[144:147],v[220:223]
s_waitcnt lgkmcnt(6)
v_exp_f32 v185,v185
v_mfma_f32_16x16x32_bf16 a[0:3],a[192:195],v[136:139],a[0:3]
v_cvt_pk_bf16_f32 v171,v174,v175
v_cvt_pk_bf16_f32 v172,v184,v185
v_mfma_f32_16x16x32_bf16 a[0:3],a[196:199],v[140:143],a[0:3]
v_exp_f32 v186,v186
v_mfma_f32_16x16x32_bf16 a[4:7],a[204:207],v[140:143],a[4:7]
v_exp_f32 v187,v187
v_mfma_f32_16x16x32_bf16 a[4:7],a[200:203],v[136:139],a[4:7]
v_exp_f32 v188,v188
v_mfma_f32_16x16x32_bf16 a[8:11],a[208:211],v[136:139],a[8:11]
v_exp_f32 v189,v189
v_mfma_f32_16x16x32_bf16 a[8:11],a[212:215],v[140:143],a[8:11]
v_exp_f32 v190,v190
v_mfma_f32_16x16x32_bf16 a[12:15],a[220:223],v[140:143],a[12:15]
v_exp_f32 v191,v191
v_mfma_f32_16x16x32_bf16 a[12:15],a[216:219],v[136:139],a[12:15]
buffer_load_dwordx4 v10,s[72:75],s80 offen offset:3072 lds
v_mfma_f32_16x16x32_bf16 a[16:19],a[224:227],v[136:139],a[16:19]
v_cvt_pk_bf16_f32 v173,v186,v187
v_cvt_pk_bf16_f32 v174,v188,v189
v_mfma_f32_16x16x32_bf16 a[16:19],a[228:231],v[140:143],a[16:19]
v_exp_f32 v200,v200
v_mfma_f32_16x16x32_bf16 a[20:23],a[236:239],v[140:143],a[20:23]
v_exp_f32 v201,v201
v_mfma_f32_16x16x32_bf16 a[20:23],a[232:235],v[136:139],a[20:23]
v_cvt_pk_bf16_f32 v175,v190,v191
v_cvt_pk_bf16_f32 v200,v200,v201
v_mfma_f32_16x16x32_bf16 a[24:27],a[240:243],v[136:139],a[24:27]
s_waitcnt lgkmcnt(0)
v_exp_f32 v202,v202
v_mfma_f32_16x16x32_bf16 a[24:27],a[244:247],v[140:143],a[24:27]
v_exp_f32 v203,v203
v_mfma_f32_16x16x32_bf16 a[28:31],a[252:255],v[140:143],a[28:31]
v_exp_f32 v204,v204
v_mfma_f32_16x16x32_bf16 a[28:31],a[248:251],v[136:139],a[28:31]
buffer_store_dwordx4 v[80:83],v6,s[36:39],s85 offen sc1
v_smfmac_f32_16x16x64_bf16 v[48:51],v[28:31],v[136:143],v27
v_cvt_pk_bf16_f32 v201,v202,v203
v_mfma_f32_16x16x32_bf16 v[232:235],a[128:131],v[148:151],v[44:47]
s_waitcnt vmcnt(10)
v_exp_f32 v205,v205
v_mfma_f32_16x16x32_bf16 v[232:235],a[132:135],v[180:183],v[232:235]
s_barrier
ds_read_b128 a[128:131],v1
v_cvt_pk_bf16_f32 v202,v204,v205
v_mfma_f32_16x16x32_bf16 v[232:235],a[136:139],v[212:215],v[232:235]
v_exp_f32 v206,v206
v_mfma_f32_16x16x32_bf16 v[232:235],a[140:143],v[244:247],v[232:235]
v_exp_f32 v207,v207
v_mfma_f32_16x16x32_bf16 v[236:239],a[156:159],v[244:247],v[44:47]
ds_read_b128 a[132:135],v1 offset:256
v_cvt_pk_bf16_f32 v203,v206,v207
v_mfma_f32_16x16x32_bf16 v[236:239],a[152:155],v[212:215],v[236:239]
v_exp_f32 v216,v216
v_mfma_f32_16x16x32_bf16 v[236:239],a[148:151],v[180:183],v[236:239]
s_add_u32 m0,s82,86016
v_exp_f32 v217,v217
v_mfma_f32_16x16x32_bf16 v[236:239],a[144:147],v[148:151],v[236:239]
ds_read_b128 a[136:139],v1 offset:512
v_cvt_pk_bf16_f32 v204,v216,v217
v_mfma_f32_16x16x32_bf16 v[248:251],a[160:163],v[148:151],v[44:47]
v_exp_f32 v218,v218
v_mfma_f32_16x16x32_bf16 v[248:251],a[164:167],v[180:183],v[248:251]
buffer_load_dwordx4 v11,s[72:75],s80 offen lds
v_mfma_f32_16x16x32_bf16 v[248:251],a[168:171],v[212:215],v[248:251]
ds_read_b128 a[140:143],v1 offset:768
v_mfma_f32_16x16x32_bf16 v[248:251],a[172:175],v[244:247],v[248:251]
v_exp_f32 v219,v219
v_mfma_f32_16x16x32_bf16 v[252:255],a[188:191],v[244:247],v[44:47]
ds_read_b128 a[156:159],v1 offset:4864
v_cvt_pk_bf16_f32 v205,v218,v219
v_mfma_f32_16x16x32_bf16 v[252:255],a[184:187],v[212:215],v[252:255]
v_exp_f32 v220,v220
v_mfma_f32_16x16x32_bf16 v[252:255],a[180:183],v[180:183],v[252:255]
ds_read_b128 a[152:155],v1 offset:4608
v_mfma_f32_16x16x32_bf16 v[252:255],a[176:179],v[148:151],v[252:255]
)A"
R"A(v_exp_f32 v221,v221
v_mfma_f32_16x16x32_bf16 a[32:35],a[192:195],v[168:171],a[32:35]
ds_read_b128 a[148:151],v1 offset:4352
v_cvt_pk_bf16_f32 v206,v220,v221
v_mfma_f32_16x16x32_bf16 a[32:35],a[196:199],v[172:175],a[32:35]
v_exp_f32 v222,v222
v_mfma_f32_16x16x32_bf16 a[36:39],a[204:207],v[172:175],a[36:39]
ds_read_b128 a[144:147],v1 offset:4096
v_mfma_f32_16x16x32_bf16 a[36:39],a[200:203],v[168:171],a[36:39]
buffer_store_dwordx4 v[84:87],v6,s[36:39],s85 offen offset:1024 sc1
v_mfma_f32_16x16x32_bf16 a[40:43],a[208:211],v[168:171],a[40:43]
ds_read_b128 a[160:163],v1 offset:8192
v_mfma_f32_16x16x32_bf16 a[40:43],a[212:215],v[172:175],a[40:43]
v_exp_f32 v223,v223
v_mfma_f32_16x16x32_bf16 a[44:47],a[220:223],v[172:175],a[44:47]
ds_read_b128 a[164:167],v1 offset:8448
v_cvt_pk_bf16_f32 v207,v222,v223
v_mfma_f32_16x16x32_bf16 a[44:47],a[216:219],v[168:171],a[44:47]
v_exp_f32 v232,v232
v_mfma_f32_16x16x32_bf16 a[48:51],a[224:227],v[168:171],a[48:51]
ds_read_b128 a[168:171],v1 offset:8704
v_mfma_f32_16x16x32_bf16 a[48:51],a[228:231],v[172:175],a[48:51]
v_exp_f32 v233,v233
v_mfma_f32_16x16x32_bf16 a[52:55],a[236:239],v[172:175],a[52:55]
s_add_u32 m0,s82,87040
ds_read_b128 a[172:175],v1 offset:8960
v_cvt_pk_bf16_f32 v232,v232,v233
v_mfma_f32_16x16x32_bf16 a[52:55],a[232:235],v[168:171],a[52:55]
v_exp_f32 v234,v234
v_mfma_f32_16x16x32_bf16 a[56:59],a[240:243],v[168:171],a[56:59]
ds_read_b128 a[188:191],v1 offset:13056
v_mfma_f32_16x16x32_bf16 a[56:59],a[244:247],v[172:175],a[56:59]
buffer_load_dwordx4 v7,s[72:75],s83 offen lds
v_mfma_f32_16x16x32_bf16 a[60:63],a[252:255],v[172:175],a[60:63]
ds_read_b128 a[184:187],v1 offset:12800
v_mfma_f32_16x16x32_bf16 a[60:63],a[248:251],v[168:171],a[60:63]
v_exp_f32 v235,v235
v_smfmac_f32_16x16x64_bf16 v[52:55],v[28:31],v[168:175],v27
s_waitcnt lgkmcnt(3)
ds_read_b128 a[180:183],v1 offset:12544
v_mfma_f32_16x16x32_bf16 v[136:139],a[128:131],v[128:131],v[32:35]
v_exp_f32 v236,v236
v_mfma_f32_16x16x32_bf16 v[136:139],a[132:135],v[160:163],v[136:139]
ds_read_b128 a[176:179],v1 offset:12288
v_cvt_pk_bf16_f32 v233,v234,v235
v_mfma_f32_16x16x32_bf16 v[136:139],a[136:139],v[192:195],v[136:139]
v_exp_f32 v237,v237
v_mfma_f32_16x16x32_bf16 v[136:139],a[140:143],v[224:227],v[136:139]
v_exp_f32 v238,v238
v_mfma_f32_16x16x32_bf16 v[140:143],a[156:159],v[224:227],v[32:35]
v_exp_f32 v239,v239
v_mfma_f32_16x16x32_bf16 v[140:143],a[152:155],v[192:195],v[140:143]
v_cvt_pk_bf16_f32 v234,v236,v237
v_cvt_pk_bf16_f32 v235,v238,v239
v_mfma_f32_16x16x32_bf16 v[140:143],a[148:151],v[160:163],v[140:143]
v_exp_f32 v248,v248
v_mfma_f32_16x16x32_bf16 v[140:143],a[144:147],v[128:131],v[140:143]
v_exp_f32 v249,v249
v_mfma_f32_16x16x32_bf16 v[152:155],a[160:163],v[128:131],v[32:35]
buffer_store_dwordx4 v[88:91],v6,s[36:39],s85 offen offset:2048 sc1
v_mfma_f32_16x16x32_bf16 v[152:155],a[164:167],v[160:163],v[152:155]
v_exp_f32 v250,v250
v_mfma_f32_16x16x32_bf16 v[152:155],a[168:171],v[192:195],v[152:155]
s_waitcnt lgkmcnt(0)
v_exp_f32 v251,v251
v_mfma_f32_16x16x32_bf16 v[152:155],a[172:175],v[224:227],v[152:155]
v_exp_f32 v252,v252
v_mfma_f32_16x16x32_bf16 v[156:159],a[188:191],v[224:227],v[32:35]
v_exp_f32 v253,v253
v_mfma_f32_16x16x32_bf16 v[156:159],a[184:187],v[192:195],v[156:159]
v_exp_f32 v254,v254
v_mfma_f32_16x16x32_bf16 v[156:159],a[180:183],v[160:163],v[156:159]
v_exp_f32 v255,v255
v_mfma_f32_16x16x32_bf16 v[156:159],a[176:179],v[128:131],v[156:159]
v_cvt_pk_bf16_f32 v236,v248,v249
v_cvt_pk_bf16_f32 v237,v250,v251
v_mfma_f32_16x16x32_bf16 a[64:67],a[192:195],v[200:203],a[64:67]
buffer_load_dwordx4 v8,s[72:75],s83 offen offset:1024 lds
v_mfma_f32_16x16x32_bf16 a[64:67],a[196:199],v[204:207],a[64:67]
v_cvt_pk_bf16_f32 v238,v252,v253
v_cvt_pk_bf16_f32 v239,v254,v255
v_mfma_f32_16x16x32_bf16 a[68:71],a[204:207],v[204:207],a[68:71]
v_exp_f32 v136,v136
v_mfma_f32_16x16x32_bf16 a[68:71],a[200:203],v[200:203],a[68:71]
v_exp_f32 v137,v137
v_mfma_f32_16x16x32_bf16 a[72:75],a[208:211],v[200:203],a[72:75]
v_exp_f32 v138,v138
v_mfma_f32_16x16x32_bf16 a[72:75],a[212:215],v[204:207],a[72:75]
v_exp_f32 v139,v139
v_mfma_f32_16x16x32_bf16 a[76:79],a[220:223],v[204:207],a[76:79]
v_cvt_pk_bf16_f32 v136,v136,v137
v_cvt_pk_bf16_f32 v137,v138,v139
v_mfma_f32_16x16x32_bf16 a[76:79],a[216:219],v[200:203],a[76:79]
v_exp_f32 v140,v140
v_mfma_f32_16x16x32_bf16 a[80:83],a[224:227],v[200:203],a[80:83]
v_exp_f32 v141,v141
v_mfma_f32_16x16x32_bf16 a[80:83],a[228:231],v[204:207],a[80:83]
v_exp_f32 v142,v142
v_mfma_f32_16x16x32_bf16 a[84:87],a[236:239],v[204:207],a[84:87]
v_exp_f32 v143,v143
v_mfma_f32_16x16x32_bf16 a[84:87],a[232:235],v[200:203],a[84:87]
v_cvt_pk_bf16_f32 v138,v140,v141
v_cvt_pk_bf16_f32 v139,v142,v143
v_mfma_f32_16x16x32_bf16 a[88:91],a[240:243],v[200:203],a[88:91]
v_exp_f32 v152,v152
v_mfma_f32_16x16x32_bf16 a[88:91],a[244:247],v[204:207],a[88:91]
v_exp_f32 v153,v153
v_mfma_f32_16x16x32_bf16 a[92:95],a[252:255],v[204:207],a[92:95]
buffer_store_dwordx4 v[92:95],v6,s[36:39],s85 offen offset:3072 sc1
v_mfma_f32_16x16x32_bf16 a[92:95],a[248:251],v[200:203],a[92:95]
s_add_u32 s85,s55,69632
v_exp_f32 v154,v154
v_smfmac_f32_16x16x64_bf16 v[56:59],v[28:31],v[200:207],v27
s_max_i32 s85,s85,s19
v_cvt_pk_bf16_f32 v140,v152,v153
v_mfma_f32_16x16x32_bf16 v[168:171],a[128:131],v[132:135],v[36:39]
v_exp_f32 v155,v155
v_mfma_f32_16x16x32_bf16 v[168:171],a[132:135],v[164:167],v[168:171]
v_exp_f32 v156,v156
v_mfma_f32_16x16x32_bf16 v[168:171],a[136:139],v[196:199],v[168:171]
v_exp_f32 v157,v157
v_mfma_f32_16x16x32_bf16 v[168:171],a[140:143],v[228:231],v[168:171]
buffer_load_dwordx4 v9,s[72:75],s83 offen offset:2048 lds
v_mfma_f32_16x16x32_bf16 v[172:175],a[156:159],v[228:231],v[36:39]
v_exp_f32 v158,v158
v_mfma_f32_16x16x32_bf16 v[172:175],a[152:155],v[196:199],v[172:175]
v_exp_f32 v159,v159
v_mfma_f32_16x16x32_bf16 v[172:175],a[148:151],v[164:167],v[172:175]
v_cvt_pk_bf16_f32 v141,v154,v155
v_cvt_pk_bf16_f32 v142,v156,v157
v_mfma_f32_16x16x32_bf16 v[172:175],a[144:147],v[132:135],v[172:175]
v_cvt_pk_bf16_f32 v143,v158,v159
v_add_u32 v1,s48,v1
v_mfma_f32_16x16x32_bf16 v[184:187],a[160:163],v[132:135],v[36:39]
v_mfma_f32_16x16x32_bf16 v[184:187],a[164:167],v[164:167],v[184:187]
v_exp_f32 v168,v168
v_mfma_f32_16x16x32_bf16 v[184:187],a[168:171],v[196:199],v[184:187]
v_exp_f32 v169,v169
v_mfma_f32_16x16x32_bf16 v[184:187],a[172:175],v[228:231],v[184:187]
v_exp_f32 v170,v170
v_mfma_f32_16x16x32_bf16 v[188:191],a[188:191],v[228:231],v[36:39]
v_exp_f32 v171,v171
v_mfma_f32_16x16x32_bf16 v[188:191],a[184:187],v[196:199],v[188:191]
v_exp_f32 v172,v172
v_mfma_f32_16x16x32_bf16 v[188:191],a[180:183],v[164:167],v[188:191]
v_exp_f32 v173,v173
v_mfma_f32_16x16x32_bf16 v[188:191],a[176:179],v[132:135],v[188:191]
v_exp_f32 v174,v174
v_mfma_f32_16x16x32_bf16 a[96:99],a[192:195],v[232:235],a[96:99]
ds_read_b64_tr_b16 a[192:193],v2
ds_read_b64_tr_b16 a[194:195],v2 offset:4096
v_mfma_f32_16x16x32_bf16 a[96:99],a[196:199],v[236:239],a[96:99]
ds_read_b64_tr_b16 a[196:197],v2 offset:8192
v_cvt_pk_bf16_f32 v168,v168,v169
v_mfma_f32_16x16x32_bf16 a[100:103],a[204:207],v[236:239],a[100:103]
ds_read_b64_tr_b16 a[198:199],v2 offset:12288
v_cvt_pk_bf16_f32 v169,v170,v171
v_mfma_f32_16x16x32_bf16 a[100:103],a[200:203],v[232:235],a[100:103]
buffer_store_dwordx4 v[96:99],v6,s[36:39],s86 offen sc1
v_mfma_f32_16x16x32_bf16 a[104:107],a[208:211],v[232:235],a[104:107]
ds_read_b64_tr_b16 a[204:205],v3 offset:8192
ds_read_b64_tr_b16 a[206:207],v3 offset:12288
v_mfma_f32_16x16x32_bf16 a[104:107],a[212:215],v[236:239],a[104:107]
ds_read_b64_tr_b16 a[200:201],v3
v_cvt_pk_bf16_f32 v170,v172,v173
v_mfma_f32_16x16x32_bf16 a[108:111],a[220:223],v[236:239],a[108:111]
v_exp_f32 v175,v175
v_mfma_f32_16x16x32_bf16 a[108:111],a[216:219],v[232:235],a[108:111]
buffer_load_dwordx4 v10,s[72:75],s83 offen offset:3072 lds
v_mfma_f32_16x16x32_bf16 a[112:115],a[224:227],v[232:235],a[112:115]
ds_read_b64_tr_b16 a[202:203],v3 offset:4096
ds_read_b64_tr_b16 a[208:209],v2 offset:256
v_mfma_f32_16x16x32_bf16 a[112:115],a[228:231],v[236:239],a[112:115]
ds_read_b64_tr_b16 a[210:211],v2 offset:4352
v_cvt_pk_bf16_f32 v171,v174,v175
v_mfma_f32_16x16x32_bf16 a[116:119],a[236:239],v[236:239],a[116:119]
v_exp_f32 v184,v184
v_mfma_f32_16x16x32_bf16 a[116:119],a[232:235],v[232:235],a[116:119]
ds_read_b64_tr_b16 a[212:213],v2 offset:8448
ds_read_b64_tr_b16 a[214:215],v2 offset:12544
v_mfma_f32_16x16x32_bf16 a[120:123],a[240:243],v[232:235],a[120:123]
v_exp_f32 v185,v185
v_mfma_f32_16x16x32_bf16 a[120:123],a[244:247],v[236:239],a[120:123]
ds_read_b64_tr_b16 a[220:221],v3 offset:8448
ds_read_b64_tr_b16 a[222:223],v3 offset:12544
v_mfma_f32_16x16x32_bf16 a[124:127],a[252:255],v[236:239],a[124:127]
ds_read_b64_tr_b16 a[216:217],v3 offset:256
v_cvt_pk_bf16_f32 v172,v184,v185
v_mfma_f32_16x16x32_bf16 a[124:127],a[248:251],v[232:235],a[124:127]
ds_read_b64_tr_b16 a[218:219],v3 offset:4352
v_smfmac_f32_16x16x64_bf16 v[60:63],v[28:31],v[232:239],v27
ds_read_b64_tr_b16 a[224:225],v2 offset:512
v_mfma_f32_16x16x32_bf16 v[200:203],a[128:131],v[144:147],v[40:43]
ds_read_b64_tr_b16 a[226:227],v2 offset:4608
ds_read_b64_tr_b16 a[228:229],v2 offset:8704
v_mfma_f32_16x16x32_bf16 v[200:203],a[132:135],v[176:179],v[200:203]
ds_read_b64_tr_b16 a[230:231],v2 offset:12800
v_mfma_f32_16x16x32_bf16 v[200:203],a[136:139],v[208:211],v[200:203]
ds_read_b64_tr_b16 a[236:237],v3 offset:8704
v_mfma_f32_16x16x32_bf16 v[200:203],a[140:143],v[240:243],v[200:203]
ds_read_b64_tr_b16 a[238:239],v3 offset:12800
v_mfma_f32_16x16x32_bf16 v[204:207],a[156:159],v[240:243],v[40:43]
ds_read_b64_tr_b16 a[232:233],v3 offset:512
ds_read_b64_tr_b16 a[234:235],v3 offset:4608
v_mfma_f32_16x16x32_bf16 v[204:207],a[152:155],v[208:211],v[204:207]
ds_read_b64_tr_b16 a[240:241],v2 offset:768
v_mfma_f32_16x16x32_bf16 v[204:207],a[148:151],v[176:179],v[204:207]
ds_read_b64_tr_b16 a[242:243],v2 offset:4864
v_mfma_f32_16x16x32_bf16 v[204:207],a[144:147],v[144:147],v[204:207]
s_add_u32 m0,s82,91136
ds_read_b64_tr_b16 a[244:245],v2 offset:8960
v_mfma_f32_16x16x32_bf16 v[216:219],a[160:163],v[144:147],v[40:43]
buffer_store_dwordx4 v[100:103],v6,s[36:39],s86 offen offset:1024 sc1
v_mfma_f32_16x16x32_bf16 v[216:219],a[164:167],v[176:179],v[216:219]
ds_read_b64_tr_b16 a[246:247],v2 offset:13056
ds_read_b64_tr_b16 a[252:253],v3 offset:8960
v_mfma_f32_16x16x32_bf16 v[216:219],a[168:171],v[208:211],v[216:219]
ds_read_b64_tr_b16 a[254:255],v3 offset:13056
v_add_u32 v2,s48,v2
v_mfma_f32_16x16x32_bf16 v[216:219],a[172:175],v[240:243],v[216:219]
ds_read_b64_tr_b16 a[248:249],v3 offset:768
v_mfma_f32_16x16x32_bf16 v[220:223],a[188:191],v[240:243],v[40:43]
buffer_load_dwordx4 v11,s[72:75],s83 offen lds
v_mfma_f32_16x16x32_bf16 v[220:223],a[184:187],v[208:211],v[220:223]
ds_read_b64_tr_b16 a[250:251],v3 offset:4864
v_add_u32 v3,s48,v3
v_mfma_f32_16x16x32_bf16 v[220:223],a[180:183],v[176:179],v[220:223]
v_exp_f32 v186,v186
v_mfma_f32_16x16x32_bf16 v[220:223],a[176:179],v[144:147],v[220:223]
s_waitcnt lgkmcnt(5)
v_exp_f32 v187,v187
v_mfma_f32_16x16x32_bf16 a[0:3],a[192:195],v[136:139],a[0:3]
v_exp_f32 v188,v188
v_mfma_f32_16x16x32_bf16 a[0:3],a[196:199],v[140:143],a[0:3]
s_add_u32 s80,s71,0
s_and_b32 s80,s80,3
s_lshl_b32 s80,s80,1
v_mfma_f32_16x16x32_bf16 a[4:7],a[204:207],v[140:143],a[4:7]
s_or_b32 s80,s80,1
s_mul_i32 s80,s80,163840
s_add_u32 s80,s80,s81
v_mfma_f32_16x16x32_bf16 a[4:7],a[200:203],v[136:139],a[4:7]
s_add_u32 s83,s80,20480
v_exp_f32 v189,v189
v_mfma_f32_16x16x32_bf16 a[8:11],a[208:211],v[136:139],a[8:11]
v_exp_f32 v190,v190
v_mfma_f32_16x16x32_bf16 a[8:11],a[212:215],v[140:143],a[8:11]
v_exp_f32 v191,v191
v_mfma_f32_16x16x32_bf16 a[12:15],a[220:223],v[140:143],a[12:15]
v_cvt_pk_bf16_f32 v173,v186,v187
v_cvt_pk_bf16_f32 v174,v188,v189
v_mfma_f32_16x16x32_bf16 a[12:15],a[216:219],v[136:139],a[12:15]
v_exp_f32 v200,v200
v_mfma_f32_16x16x32_bf16 a[16:19],a[224:227],v[136:139],a[16:19]
v_exp_f32 v201,v201
v_mfma_f32_16x16x32_bf16 a[16:19],a[228:231],v[140:143],a[16:19]
v_cvt_pk_bf16_f32 v175,v190,v191
v_cvt_pk_bf16_f32 v200,v200,v201
)A"
R"A(v_mfma_f32_16x16x32_bf16 a[20:23],a[236:239],v[140:143],a[20:23]
v_exp_f32 v202,v202
v_mfma_f32_16x16x32_bf16 a[20:23],a[232:235],v[136:139],a[20:23]
v_exp_f32 v203,v203
v_mfma_f32_16x16x32_bf16 a[24:27],a[240:243],v[136:139],a[24:27]
s_waitcnt lgkmcnt(0)
v_exp_f32 v204,v204
v_mfma_f32_16x16x32_bf16 a[24:27],a[244:247],v[140:143],a[24:27]
v_exp_f32 v205,v205
v_mfma_f32_16x16x32_bf16 a[28:31],a[252:255],v[140:143],a[28:31]
buffer_store_dwordx4 v[104:107],v6,s[36:39],s86 offen offset:2048 sc1
v_mfma_f32_16x16x32_bf16 a[28:31],a[248:251],v[136:139],a[28:31]
v_exp_f32 v206,v206
v_smfmac_f32_16x16x64_bf16 v[48:51],v[28:31],v[136:143],v27
v_cvt_pk_bf16_f32 v201,v202,v203
v_mfma_f32_16x16x32_bf16 v[232:235],a[128:131],v[148:151],v[44:47]
v_exp_f32 v207,v207
v_mfma_f32_16x16x32_bf16 v[232:235],a[132:135],v[180:183],v[232:235]
s_barrier
ds_read_b128 a[128:131],v1
v_cvt_pk_bf16_f32 v202,v204,v205
v_mfma_f32_16x16x32_bf16 v[232:235],a[136:139],v[212:215],v[232:235]
v_exp_f32 v216,v216
v_mfma_f32_16x16x32_bf16 v[232:235],a[140:143],v[244:247],v[232:235]
ds_read_b128 a[132:135],v1 offset:256
v_cvt_pk_bf16_f32 v203,v206,v207
v_mfma_f32_16x16x32_bf16 v[236:239],a[156:159],v[244:247],v[44:47]
s_add_u32 m0,s82,0
v_exp_f32 v217,v217
v_mfma_f32_16x16x32_bf16 v[236:239],a[152:155],v[212:215],v[236:239]
ds_read_b128 a[136:139],v1 offset:512
v_cvt_pk_bf16_f32 v204,v216,v217
v_mfma_f32_16x16x32_bf16 v[236:239],a[148:151],v[180:183],v[236:239]
v_exp_f32 v218,v218
v_mfma_f32_16x16x32_bf16 v[236:239],a[144:147],v[148:151],v[236:239]
buffer_load_dwordx4 v7,s[72:75],s80 offen lds
v_mfma_f32_16x16x32_bf16 v[248:251],a[160:163],v[148:151],v[44:47]
v_exp_f32 v219,v219
v_mfma_f32_16x16x32_bf16 v[248:251],a[164:167],v[180:183],v[248:251]
ds_read_b128 a[140:143],v1 offset:768
v_cvt_pk_bf16_f32 v205,v218,v219
v_mfma_f32_16x16x32_bf16 v[248:251],a[168:171],v[212:215],v[248:251]
v_exp_f32 v220,v220
v_mfma_f32_16x16x32_bf16 v[248:251],a[172:175],v[244:247],v[248:251]
ds_read_b128 a[156:159],v1 offset:4864
v_mfma_f32_16x16x32_bf16 v[252:255],a[188:191],v[244:247],v[44:47]
v_exp_f32 v221,v221
v_mfma_f32_16x16x32_bf16 v[252:255],a[184:187],v[212:215],v[252:255]
ds_read_b128 a[152:155],v1 offset:4608
v_cvt_pk_bf16_f32 v206,v220,v221
v_mfma_f32_16x16x32_bf16 v[252:255],a[180:183],v[180:183],v[252:255]
v_exp_f32 v222,v222
v_mfma_f32_16x16x32_bf16 v[252:255],a[176:179],v[148:151],v[252:255]
ds_read_b128 a[148:151],v1 offset:4352
v_mfma_f32_16x16x32_bf16 a[32:35],a[192:195],v[168:171],a[32:35]
v_exp_f32 v223,v223
v_mfma_f32_16x16x32_bf16 a[32:35],a[196:199],v[172:175],a[32:35]
ds_read_b128 a[144:147],v1 offset:4096
v_cvt_pk_bf16_f32 v207,v222,v223
v_mfma_f32_16x16x32_bf16 a[36:39],a[204:207],v[172:175],a[36:39]
buffer_store_dwordx4 v[108:111],v6,s[36:39],s86 offen offset:3072 sc1
v_mfma_f32_16x16x32_bf16 a[36:39],a[200:203],v[168:171],a[36:39]
s_add_u32 s86,s55,73728
s_max_i32 s86,s86,s100
ds_read_b128 a[160:163],v1 offset:8192
v_mfma_f32_16x16x32_bf16 a[40:43],a[208:211],v[168:171],a[40:43]
v_exp_f32 v232,v232
v_mfma_f32_16x16x32_bf16 a[40:43],a[212:215],v[172:175],a[40:43]
ds_read_b128 a[164:167],v1 offset:8448
v_mfma_f32_16x16x32_bf16 a[44:47],a[220:223],v[172:175],a[44:47]
buffer_load_dwordx4 v8,s[72:75],s80 offen offset:1024 lds
v_mfma_f32_16x16x32_bf16 a[44:47],a[216:219],v[168:171],a[44:47]
ds_read_b128 a[168:171],v1 offset:8704
v_mfma_f32_16x16x32_bf16 a[48:51],a[224:227],v[168:171],a[48:51]
v_exp_f32 v233,v233
v_mfma_f32_16x16x32_bf16 a[48:51],a[228:231],v[172:175],a[48:51]
ds_read_b128 a[172:175],v1 offset:8960
v_cvt_pk_bf16_f32 v232,v232,v233
v_mfma_f32_16x16x32_bf16 a[52:55],a[236:239],v[172:175],a[52:55]
v_exp_f32 v234,v234
v_mfma_f32_16x16x32_bf16 a[52:55],a[232:235],v[168:171],a[52:55]
ds_read_b128 a[188:191],v1 offset:13056
v_mfma_f32_16x16x32_bf16 a[56:59],a[240:243],v[168:171],a[56:59]
v_exp_f32 v235,v235
v_mfma_f32_16x16x32_bf16 a[56:59],a[244:247],v[172:175],a[56:59]
buffer_load_dwordx4 v9,s[72:75],s80 offen offset:2048 lds
v_mfma_f32_16x16x32_bf16 a[60:63],a[252:255],v[172:175],a[60:63]
ds_read_b128 a[184:187],v1 offset:12800
v_cvt_pk_bf16_f32 v233,v234,v235
v_mfma_f32_16x16x32_bf16 a[60:63],a[248:251],v[168:171],a[60:63]
v_exp_f32 v236,v236
v_smfmac_f32_16x16x64_bf16 v[52:55],v[28:31],v[168:175],v27
s_waitcnt lgkmcnt(3)
ds_read_b128 a[180:183],v1 offset:12544
v_mfma_f32_16x16x32_bf16 v[136:139],a[128:131],v[128:131],v[32:35]
v_exp_f32 v237,v237
v_mfma_f32_16x16x32_bf16 v[136:139],a[132:135],v[160:163],v[136:139]
ds_read_b128 a[176:179],v1 offset:12288
v_cvt_pk_bf16_f32 v234,v236,v237
v_mfma_f32_16x16x32_bf16 v[136:139],a[136:139],v[192:195],v[136:139]
v_exp_f32 v238,v238
v_mfma_f32_16x16x32_bf16 v[136:139],a[140:143],v[224:227],v[136:139]
v_exp_f32 v239,v239
v_mfma_f32_16x16x32_bf16 v[140:143],a[156:159],v[224:227],v[32:35]
v_cvt_pk_bf16_f32 v235,v238,v239
v_add_u32 v1,v4,v5
v_mfma_f32_16x16x32_bf16 v[140:143],a[152:155],v[192:195],v[140:143]
v_exp_f32 v248,v248
v_mfma_f32_16x16x32_bf16 v[140:143],a[148:151],v[160:163],v[140:143]
v_exp_f32 v249,v249
v_mfma_f32_16x16x32_bf16 v[140:143],a[144:147],v[128:131],v[140:143]
buffer_store_dwordx4 v[112:115],v6,s[36:39],s87 offen sc1
v_mfma_f32_16x16x32_bf16 v[152:155],a[160:163],v[128:131],v[32:35]
v_exp_f32 v250,v250
v_mfma_f32_16x16x32_bf16 v[152:155],a[164:167],v[160:163],v[152:155]
v_exp_f32 v251,v251
v_mfma_f32_16x16x32_bf16 v[152:155],a[168:171],v[192:195],v[152:155]
s_waitcnt lgkmcnt(0)
v_exp_f32 v252,v252
v_mfma_f32_16x16x32_bf16 v[152:155],a[172:175],v[224:227],v[152:155]
buffer_load_dwordx4 v10,s[72:75],s80 offen offset:3072 lds
v_mfma_f32_16x16x32_bf16 v[156:159],a[188:191],v[224:227],v[32:35]
v_exp_f32 v253,v253
v_mfma_f32_16x16x32_bf16 v[156:159],a[184:187],v[192:195],v[156:159]
v_exp_f32 v254,v254
v_mfma_f32_16x16x32_bf16 v[156:159],a[180:183],v[160:163],v[156:159]
v_exp_f32 v255,v255
v_mfma_f32_16x16x32_bf16 v[156:159],a[176:179],v[128:131],v[156:159]
s_add_u32 m0,s82,4096
v_cvt_pk_bf16_f32 v236,v248,v249
v_cvt_pk_bf16_f32 v237,v250,v251
v_mfma_f32_16x16x32_bf16 a[64:67],a[192:195],v[200:203],a[64:67]
v_cvt_pk_bf16_f32 v238,v252,v253
v_cvt_pk_bf16_f32 v239,v254,v255
v_mfma_f32_16x16x32_bf16 a[64:67],a[196:199],v[204:207],a[64:67]
v_exp_f32 v136,v136
v_mfma_f32_16x16x32_bf16 a[68:71],a[204:207],v[204:207],a[68:71]
buffer_load_dwordx4 v11,s[72:75],s80 offen lds
v_mfma_f32_16x16x32_bf16 a[68:71],a[200:203],v[200:203],a[68:71]
v_exp_f32 v137,v137
v_mfma_f32_16x16x32_bf16 a[72:75],a[208:211],v[200:203],a[72:75]
v_exp_f32 v138,v138
v_mfma_f32_16x16x32_bf16 a[72:75],a[212:215],v[204:207],a[72:75]
v_exp_f32 v139,v139
v_mfma_f32_16x16x32_bf16 a[76:79],a[220:223],v[204:207],a[76:79]
v_cvt_pk_bf16_f32 v136,v136,v137
v_cvt_pk_bf16_f32 v137,v138,v139
v_mfma_f32_16x16x32_bf16 a[76:79],a[216:219],v[200:203],a[76:79]
v_exp_f32 v140,v140
v_mfma_f32_16x16x32_bf16 a[80:83],a[224:227],v[200:203],a[80:83]
v_exp_f32 v141,v141
v_mfma_f32_16x16x32_bf16 a[80:83],a[228:231],v[204:207],a[80:83]
v_exp_f32 v142,v142
v_mfma_f32_16x16x32_bf16 a[84:87],a[236:239],v[204:207],a[84:87]
s_add_u32 m0,s82,5120
v_exp_f32 v143,v143
v_mfma_f32_16x16x32_bf16 a[84:87],a[232:235],v[200:203],a[84:87]
v_cvt_pk_bf16_f32 v138,v140,v141
v_cvt_pk_bf16_f32 v139,v142,v143
v_mfma_f32_16x16x32_bf16 a[88:91],a[240:243],v[200:203],a[88:91]
v_exp_f32 v152,v152
v_mfma_f32_16x16x32_bf16 a[88:91],a[244:247],v[204:207],a[88:91]
buffer_store_dwordx4 v[116:119],v6,s[36:39],s87 offen offset:1024 sc1
v_mfma_f32_16x16x32_bf16 a[92:95],a[252:255],v[204:207],a[92:95]
v_exp_f32 v153,v153
v_mfma_f32_16x16x32_bf16 a[92:95],a[248:251],v[200:203],a[92:95]
v_exp_f32 v154,v154
v_smfmac_f32_16x16x64_bf16 v[56:59],v[28:31],v[200:207],v27
v_cvt_pk_bf16_f32 v140,v152,v153
v_mfma_f32_16x16x32_bf16 v[168:171],a[128:131],v[132:135],v[36:39]
buffer_load_dwordx4 v7,s[72:75],s83 offen lds
v_mfma_f32_16x16x32_bf16 v[168:171],a[132:135],v[164:167],v[168:171]
v_exp_f32 v155,v155
v_mfma_f32_16x16x32_bf16 v[168:171],a[136:139],v[196:199],v[168:171]
v_exp_f32 v156,v156
v_mfma_f32_16x16x32_bf16 v[168:171],a[140:143],v[228:231],v[168:171]
v_exp_f32 v157,v157
v_mfma_f32_16x16x32_bf16 v[172:175],a[156:159],v[228:231],v[36:39]
v_exp_f32 v158,v158
v_mfma_f32_16x16x32_bf16 v[172:175],a[152:155],v[196:199],v[172:175]
v_exp_f32 v159,v159
v_mfma_f32_16x16x32_bf16 v[172:175],a[148:151],v[164:167],v[172:175]
v_cvt_pk_bf16_f32 v141,v154,v155
v_cvt_pk_bf16_f32 v142,v156,v157
v_mfma_f32_16x16x32_bf16 v[172:175],a[144:147],v[132:135],v[172:175]
buffer_load_dwordx4 v8,s[72:75],s83 offen offset:1024 lds
v_mfma_f32_16x16x32_bf16 v[184:187],a[160:163],v[132:135],v[36:39]
v_cvt_pk_bf16_f32 v143,v158,v159
v_add_u32 v1,81920,v1
v_mfma_f32_16x16x32_bf16 v[184:187],a[164:167],v[164:167],v[184:187]
v_exp_f32 v168,v168
v_mfma_f32_16x16x32_bf16 v[184:187],a[168:171],v[196:199],v[184:187]
v_exp_f32 v169,v169
v_mfma_f32_16x16x32_bf16 v[184:187],a[172:175],v[228:231],v[184:187]
v_exp_f32 v170,v170
v_mfma_f32_16x16x32_bf16 v[188:191],a[188:191],v[228:231],v[36:39]
v_exp_f32 v171,v171
v_mfma_f32_16x16x32_bf16 v[188:191],a[184:187],v[196:199],v[188:191]
v_exp_f32 v172,v172
v_mfma_f32_16x16x32_bf16 v[188:191],a[180:183],v[164:167],v[188:191]
v_exp_f32 v173,v173
v_mfma_f32_16x16x32_bf16 v[188:191],a[176:179],v[132:135],v[188:191]
v_exp_f32 v174,v174
v_mfma_f32_16x16x32_bf16 a[96:99],a[192:195],v[232:235],a[96:99]
ds_read_b64_tr_b16 a[192:193],v2
ds_read_b64_tr_b16 a[194:195],v2 offset:4096
v_mfma_f32_16x16x32_bf16 a[96:99],a[196:199],v[236:239],a[96:99]
ds_read_b64_tr_b16 a[196:197],v2 offset:8192
v_cvt_pk_bf16_f32 v168,v168,v169
v_mfma_f32_16x16x32_bf16 a[100:103],a[204:207],v[236:239],a[100:103]
buffer_store_dwordx4 v[120:123],v6,s[36:39],s87 offen offset:2048 sc1
v_mfma_f32_16x16x32_bf16 a[100:103],a[200:203],v[232:235],a[100:103]
ds_read_b64_tr_b16 a[198:199],v2 offset:12288
ds_read_b64_tr_b16 a[204:205],v3 offset:8192
v_mfma_f32_16x16x32_bf16 a[104:107],a[208:211],v[232:235],a[104:107]
ds_read_b64_tr_b16 a[206:207],v3 offset:12288
v_cvt_pk_bf16_f32 v169,v170,v171
v_mfma_f32_16x16x32_bf16 a[104:107],a[212:215],v[236:239],a[104:107]
ds_read_b64_tr_b16 a[200:201],v3
v_cvt_pk_bf16_f32 v170,v172,v173
v_mfma_f32_16x16x32_bf16 a[108:111],a[220:223],v[236:239],a[108:111]
buffer_load_dwordx4 v9,s[72:75],s83 offen offset:2048 lds
v_mfma_f32_16x16x32_bf16 a[108:111],a[216:219],v[232:235],a[108:111]
ds_read_b64_tr_b16 a[202:203],v3 offset:4096
ds_read_b64_tr_b16 a[208:209],v2 offset:256
v_mfma_f32_16x16x32_bf16 a[112:115],a[224:227],v[232:235],a[112:115]
v_exp_f32 v175,v175
v_mfma_f32_16x16x32_bf16 a[112:115],a[228:231],v[236:239],a[112:115]
ds_read_b64_tr_b16 a[210:211],v2 offset:4352
ds_read_b64_tr_b16 a[212:213],v2 offset:8448
v_mfma_f32_16x16x32_bf16 a[116:119],a[236:239],v[236:239],a[116:119]
ds_read_b64_tr_b16 a[214:215],v2 offset:12544
v_cvt_pk_bf16_f32 v171,v174,v175
v_mfma_f32_16x16x32_bf16 a[116:119],a[232:235],v[232:235],a[116:119]
s_cmp_ge_u32 0,s71
s_cselect_b32 s80,2,0
ds_read_b64_tr_b16 a[220:221],v3 offset:8448
v_mfma_f32_16x16x32_bf16 a[120:123],a[240:243],v[232:235],a[120:123]
s_mul_i32 s80,s80,163840
v_exp_f32 v184,v184
v_mfma_f32_16x16x32_bf16 a[120:123],a[244:247],v[236:239],a[120:123]
s_add_u32 s80,s80,s81
buffer_load_dwordx4 v10,s[72:75],s83 offen offset:3072 lds
v_mfma_f32_16x16x32_bf16 a[124:127],a[252:255],v[236:239],a[124:127]
ds_read_b64_tr_b16 a[222:223],v3 offset:12544
ds_read_b64_tr_b16 a[216:217],v3 offset:256
v_mfma_f32_16x16x32_bf16 a[124:127],a[248:251],v[232:235],a[124:127]
ds_read_b64_tr_b16 a[218:219],v3 offset:4352
v_smfmac_f32_16x16x64_bf16 v[60:63],v[28:31],v[232:239],v27
ds_read_b64_tr_b16 a[224:225],v2 offset:512
v_mfma_f32_16x16x32_bf16 v[200:203],a[128:131],v[144:147],v[40:43]
ds_read_b64_tr_b16 a[226:227],v2 offset:4608
v_mfma_f32_16x16x32_bf16 v[200:203],a[132:135],v[176:179],v[200:203]
ds_read_b64_tr_b16 a[228:229],v2 offset:8704
ds_read_b64_tr_b16 a[230:231],v2 offset:12800
v_mfma_f32_16x16x32_bf16 v[200:203],a[136:139],v[208:211],v[200:203]
ds_read_b64_tr_b16 a[236:237],v3 offset:8704
v_mfma_f32_16x16x32_bf16 v[200:203],a[140:143],v[240:243],v[200:203]
)A"
R"A(ds_read_b64_tr_b16 a[238:239],v3 offset:12800
v_mfma_f32_16x16x32_bf16 v[204:207],a[156:159],v[240:243],v[40:43]
s_add_u32 m0,s82,9216
ds_read_b64_tr_b16 a[232:233],v3 offset:512
v_mfma_f32_16x16x32_bf16 v[204:207],a[152:155],v[208:211],v[204:207]
ds_read_b64_tr_b16 a[234:235],v3 offset:4608
ds_read_b64_tr_b16 a[240:241],v2 offset:768
v_mfma_f32_16x16x32_bf16 v[204:207],a[148:151],v[176:179],v[204:207]
ds_read_b64_tr_b16 a[242:243],v2 offset:4864
v_mfma_f32_16x16x32_bf16 v[204:207],a[144:147],v[144:147],v[204:207]
buffer_store_dwordx4 v[124:127],v6,s[36:39],s87 offen offset:3072 sc1
v_mfma_f32_16x16x32_bf16 v[216:219],a[160:163],v[144:147],v[40:43]
s_add_u32 s87,s55,77824
ds_read_b64_tr_b16 a[244:245],v2 offset:8960
ds_read_b64_tr_b16 a[246:247],v2 offset:13056
v_mfma_f32_16x16x32_bf16 v[216:219],a[164:167],v[176:179],v[216:219]
s_max_i32 s87,s87,s101
ds_read_b64_tr_b16 a[252:253],v3 offset:8960
v_sub_u32 v2,v4,v5
v_mfma_f32_16x16x32_bf16 v[216:219],a[168:171],v[208:211],v[216:219]
ds_read_b64_tr_b16 a[254:255],v3 offset:13056
v_add_u32 v2,81920,v2
v_mfma_f32_16x16x32_bf16 v[216:219],a[172:175],v[240:243],v[216:219]
buffer_load_dwordx4 v11,s[72:75],s83 offen lds
v_mfma_f32_16x16x32_bf16 v[220:223],a[188:191],v[240:243],v[40:43]
s_add_u32 s83,s80,20480
ds_read_b64_tr_b16 a[248:249],v3 offset:768
ds_read_b64_tr_b16 a[250:251],v3 offset:4864
v_mfma_f32_16x16x32_bf16 v[220:223],a[184:187],v[208:211],v[220:223]
v_exp_f32 v185,v185
v_mfma_f32_16x16x32_bf16 v[220:223],a[180:183],v[176:179],v[220:223]
v_exp_f32 v186,v186
v_mfma_f32_16x16x32_bf16 v[220:223],a[176:179],v[144:147],v[220:223]
s_add_u32 m0,s82,40960
v_exp_f32 v187,v187
s_waitcnt lgkmcnt(6)
v_mfma_f32_16x16x32_bf16 a[0:3],a[192:195],v[136:139],a[0:3]
v_exp_f32 v188,v188
v_mfma_f32_16x16x32_bf16 a[0:3],a[196:199],v[140:143],a[0:3]
v_exp_f32 v189,v189
v_mfma_f32_16x16x32_bf16 a[4:7],a[204:207],v[140:143],a[4:7]
buffer_load_dwordx4 v7,s[72:75],s80 offen lds
v_mfma_f32_16x16x32_bf16 a[4:7],a[200:203],v[136:139],a[4:7]
v_exp_f32 v190,v190
v_mfma_f32_16x16x32_bf16 a[8:11],a[208:211],v[136:139],a[8:11]
v_exp_f32 v191,v191
v_mfma_f32_16x16x32_bf16 a[8:11],a[212:215],v[140:143],a[8:11]
v_cvt_pk_bf16_f32 v172,v184,v185
v_cvt_pk_bf16_f32 v173,v186,v187
v_mfma_f32_16x16x32_bf16 a[12:15],a[220:223],v[140:143],a[12:15]
v_cvt_pk_bf16_f32 v174,v188,v189
v_cvt_pk_bf16_f32 v175,v190,v191
v_mfma_f32_16x16x32_bf16 a[12:15],a[216:219],v[136:139],a[12:15]
v_exp_f32 v200,v200
v_mfma_f32_16x16x32_bf16 a[16:19],a[224:227],v[136:139],a[16:19]
v_exp_f32 v201,v201
v_mfma_f32_16x16x32_bf16 a[16:19],a[228:231],v[140:143],a[16:19]
v_exp_f32 v202,v202
v_mfma_f32_16x16x32_bf16 a[20:23],a[236:239],v[140:143],a[20:23]
v_exp_f32 v203,v203
v_mfma_f32_16x16x32_bf16 a[20:23],a[232:235],v[136:139],a[20:23]
v_exp_f32 v204,v204
v_mfma_f32_16x16x32_bf16 a[24:27],a[240:243],v[136:139],a[24:27]
s_waitcnt lgkmcnt(0)
v_exp_f32 v205,v205
v_mfma_f32_16x16x32_bf16 a[24:27],a[244:247],v[140:143],a[24:27]
buffer_load_dwordx4 v8,s[72:75],s80 offen offset:1024 lds
v_mfma_f32_16x16x32_bf16 a[28:31],a[252:255],v[140:143],a[28:31]
v_exp_f32 v206,v206
v_mfma_f32_16x16x32_bf16 a[28:31],a[248:251],v[136:139],a[28:31]
s_waitcnt vmcnt(18)
v_cvt_pk_bf16_f32 v200,v200,v201
v_cvt_pk_bf16_f32 v201,v202,v203
v_smfmac_f32_16x16x64_bf16 v[48:51],v[28:31],v[136:143],v27
s_barrier
v_exp_f32 v207,v207
v_mfma_f32_16x16x32_bf16 v[232:235],a[128:131],v[148:151],v[44:47]
v_cvt_pk_bf16_f32 v202,v204,v205
v_cvt_pk_bf16_f32 v203,v206,v207
v_mfma_f32_16x16x32_bf16 v[232:235],a[132:135],v[180:183],v[232:235]
v_exp_f32 v216,v216
v_mfma_f32_16x16x32_bf16 v[232:235],a[136:139],v[212:215],v[232:235]
v_exp_f32 v217,v217
v_mfma_f32_16x16x32_bf16 v[232:235],a[140:143],v[244:247],v[232:235]
v_exp_f32 v218,v218
v_mfma_f32_16x16x32_bf16 v[236:239],a[156:159],v[244:247],v[44:47]
v_exp_f32 v219,v219
v_mfma_f32_16x16x32_bf16 v[236:239],a[152:155],v[212:215],v[236:239]
v_exp_f32 v220,v220
v_mfma_f32_16x16x32_bf16 v[236:239],a[148:151],v[180:183],v[236:239]
buffer_load_dwordx4 v9,s[72:75],s80 offen offset:2048 lds
v_mfma_f32_16x16x32_bf16 v[236:239],a[144:147],v[148:151],v[236:239]
v_exp_f32 v221,v221
v_mfma_f32_16x16x32_bf16 v[248:251],a[160:163],v[148:151],v[44:47]
v_exp_f32 v222,v222
v_mfma_f32_16x16x32_bf16 v[248:251],a[164:167],v[180:183],v[248:251]
v_exp_f32 v223,v223
v_mfma_f32_16x16x32_bf16 v[248:251],a[168:171],v[212:215],v[248:251]
v_cvt_pk_bf16_f32 v204,v216,v217
v_cvt_pk_bf16_f32 v205,v218,v219
v_mfma_f32_16x16x32_bf16 v[248:251],a[172:175],v[244:247],v[248:251]
v_cvt_pk_bf16_f32 v206,v220,v221
v_cvt_pk_bf16_f32 v207,v222,v223
v_mfma_f32_16x16x32_bf16 v[252:255],a[188:191],v[244:247],v[44:47]
v_exp_f32 v232,v232
v_mfma_f32_16x16x32_bf16 v[252:255],a[184:187],v[212:215],v[252:255]
v_exp_f32 v233,v233
v_mfma_f32_16x16x32_bf16 v[252:255],a[180:183],v[180:183],v[252:255]
v_exp_f32 v234,v234
v_mfma_f32_16x16x32_bf16 v[252:255],a[176:179],v[148:151],v[252:255]
v_exp_f32 v235,v235
v_mfma_f32_16x16x32_bf16 a[32:35],a[192:195],v[168:171],a[32:35]
v_cvt_pk_bf16_f32 v232,v232,v233
v_add_f32 v48,v49,v48
v_mfma_f32_16x16x32_bf16 a[32:35],a[196:199],v[172:175],a[32:35]
buffer_load_dwordx4 v10,s[72:75],s80 offen offset:3072 lds
v_mfma_f32_16x16x32_bf16 a[36:39],a[204:207],v[172:175],a[36:39]
v_exp_f32 v236,v236
v_mfma_f32_16x16x32_bf16 a[36:39],a[200:203],v[168:171],a[36:39]
v_exp_f32 v237,v237
v_mfma_f32_16x16x32_bf16 a[40:43],a[208:211],v[168:171],a[40:43]
v_exp_f32 v238,v238
v_mfma_f32_16x16x32_bf16 a[40:43],a[212:215],v[172:175],a[40:43]
v_exp_f32 v239,v239
v_mfma_f32_16x16x32_bf16 a[44:47],a[220:223],v[172:175],a[44:47]
v_cvt_pk_bf16_f32 v233,v234,v235
v_max_f32 v20,0x00800000,v48
v_mfma_f32_16x16x32_bf16 a[44:47],a[216:219],v[168:171],a[44:47]
s_lshl_b32 s95,s42,2
s_add_u32 s95,s95,s40
s_lshl_b32 s95,s95,4
v_mfma_f32_16x16x32_bf16 a[48:51],a[224:227],v[168:171],a[48:51]
v_mov_b32 v18,s68
v_mov_b32 v19,s69
v_mfma_f32_16x16x32_bf16 a[48:51],a[228:231],v[172:175],a[48:51]
s_add_u32 m0,s82,45056
v_cvt_pk_bf16_f32 v234,v236,v237
v_cvt_pk_bf16_f32 v235,v238,v239
v_mfma_f32_16x16x32_bf16 a[52:55],a[236:239],v[172:175],a[52:55]
v_exp_f32 v248,v248
v_mfma_f32_16x16x32_bf16 a[52:55],a[232:235],v[168:171],a[52:55]
v_exp_f32 v249,v249
v_mfma_f32_16x16x32_bf16 a[56:59],a[240:243],v[168:171],a[56:59]
buffer_load_dwordx4 v11,s[72:75],s80 offen lds
v_mfma_f32_16x16x32_bf16 a[56:59],a[244:247],v[172:175],a[56:59]
v_exp_f32 v250,v250
v_mfma_f32_16x16x32_bf16 a[60:63],a[252:255],v[172:175],a[60:63]
v_exp_f32 v251,v251
v_mfma_f32_16x16x32_bf16 a[60:63],a[248:251],v[168:171],a[60:63]
v_exp_f32 v252,v252
v_smfmac_f32_16x16x64_bf16 v[52:55],v[28:31],v[168:175],v27
s_waitcnt vmcnt(6)
s_mov_b64 exec,1
buffer_store_dwordx2 v[18:19],v6,s[64:67],s95 offen sc1
s_mov_b64 exec,-1
v_mfma_f32_16x16x32_bf16 a[64:67],a[192:195],v[200:203],a[64:67]
v_exp_f32 v253,v253
v_mfma_f32_16x16x32_bf16 a[64:67],a[196:199],v[204:207],a[64:67]
v_exp_f32 v254,v254
v_mfma_f32_16x16x32_bf16 a[68:71],a[204:207],v[204:207],a[68:71]
v_exp_f32 v255,v255
v_mfma_f32_16x16x32_bf16 a[68:71],a[200:203],v[200:203],a[68:71]
s_add_u32 m0,s82,46080
v_rcp_f32 v20,v20
v_mfma_f32_16x16x32_bf16 a[72:75],a[208:211],v[200:203],a[72:75]
v_cvt_pk_bf16_f32 v236,v248,v249
v_cvt_pk_bf16_f32 v237,v250,v251
v_mfma_f32_16x16x32_bf16 a[72:75],a[212:215],v[204:207],a[72:75]
v_cvt_pk_bf16_f32 v238,v252,v253
v_cvt_pk_bf16_f32 v239,v254,v255
v_mfma_f32_16x16x32_bf16 a[76:79],a[220:223],v[204:207],a[76:79]
buffer_load_dwordx4 v7,s[72:75],s83 offen lds
v_mfma_f32_16x16x32_bf16 a[76:79],a[216:219],v[200:203],a[76:79]
v_accvgpr_read_b32 v136,a0
v_accvgpr_read_b32 v137,a1
v_accvgpr_read_b32 v138,a2
v_accvgpr_read_b32 v139,a3
v_accvgpr_read_b32 v12,a4
v_accvgpr_read_b32 v13,a5
v_accvgpr_read_b32 v14,a6
v_accvgpr_read_b32 v15,a7
v_pk_mul_f32 v[136:137],v[20:21],v[136:137] op_sel:[0,0] op_sel_hi:[0,1]
v_pk_mul_f32 v[138:139],v[20:21],v[138:139] op_sel:[0,0] op_sel_hi:[0,1]
v_pk_mul_f32 v[12:13],v[20:21],v[12:13] op_sel:[0,0] op_sel_hi:[0,1]
v_pk_mul_f32 v[14:15],v[20:21],v[14:15] op_sel:[0,0] op_sel_hi:[0,1]
v_cvt_pk_bf16_f32 v136,v136,v137
v_cvt_pk_bf16_f32 v137,v138,v139
v_cvt_pk_bf16_f32 v138,v12,v13
v_cvt_pk_bf16_f32 v139,v14,v15
v_mfma_f32_16x16x32_bf16 a[80:83],a[224:227],v[200:203],a[80:83]
ds_read_b64_tr_b16 a[128:129],v1
ds_read_b64_tr_b16 a[130:131],v1 offset:5120
v_mfma_f32_16x16x32_bf16 a[80:83],a[228:231],v[204:207],a[80:83]
ds_read_b64_tr_b16 a[132:133],v1 offset:10240
v_accvgpr_read_b32 v152,a16
v_mfma_f32_16x16x32_bf16 a[84:87],a[236:239],v[204:207],a[84:87]
ds_read_b64_tr_b16 a[134:135],v1 offset:15360
v_accvgpr_read_b32 v153,a17
v_mfma_f32_16x16x32_bf16 a[84:87],a[232:235],v[200:203],a[84:87]
ds_read_b64_tr_b16 a[136:137],v1 offset:20480
v_mul_f32 v152,v20,v152
v_mfma_f32_16x16x32_bf16 a[88:91],a[240:243],v[200:203],a[88:91]
ds_read_b64_tr_b16 a[138:139],v1 offset:25600
ds_read_b64_tr_b16 a[140:141],v1 offset:30720
v_mfma_f32_16x16x32_bf16 a[88:91],a[244:247],v[204:207],a[88:91]
v_accvgpr_read_b32 v140,a8
v_accvgpr_read_b32 v141,a9
v_accvgpr_read_b32 v142,a10
v_accvgpr_read_b32 v143,a11
v_accvgpr_read_b32 v12,a12
v_accvgpr_read_b32 v13,a13
v_accvgpr_read_b32 v14,a14
v_accvgpr_read_b32 v15,a15
v_pk_mul_f32 v[140:141],v[20:21],v[140:141] op_sel:[0,0] op_sel_hi:[0,1]
v_pk_mul_f32 v[142:143],v[20:21],v[142:143] op_sel:[0,0] op_sel_hi:[0,1]
v_pk_mul_f32 v[12:13],v[20:21],v[12:13] op_sel:[0,0] op_sel_hi:[0,1]
v_pk_mul_f32 v[14:15],v[20:21],v[14:15] op_sel:[0,0] op_sel_hi:[0,1]
v_cvt_pk_bf16_f32 v140,v140,v141
v_cvt_pk_bf16_f32 v141,v142,v143
v_cvt_pk_bf16_f32 v142,v12,v13
v_cvt_pk_bf16_f32 v143,v14,v15
v_mfma_f32_16x16x32_bf16 a[92:95],a[252:255],v[204:207],a[92:95]
ds_read_b64_tr_b16 a[142:143],v1 offset:35840
v_mul_f32 v153,v20,v153
v_mfma_f32_16x16x32_bf16 a[92:95],a[248:251],v[200:203],a[92:95]
v_accvgpr_read_b32 v154,a18
v_accvgpr_read_b32 v155,a19
v_smfmac_f32_16x16x64_bf16 v[56:59],v[28:31],v[200:207],v27
v_cvt_pk_bf16_f32 v152,v152,v153
v_mfma_f32_16x16x32_bf16 a[96:99],a[192:195],v[232:235],a[96:99]
buffer_store_dwordx4 v[136:139],v6,s[36:39],s84 offen sc1
v_mfma_f32_16x16x32_bf16 a[96:99],a[196:199],v[236:239],a[96:99]
v_mul_f32 v154,v20,v154
v_mul_f32 v155,v20,v155
v_mfma_f32_16x16x32_bf16 a[100:103],a[204:207],v[236:239],a[100:103]
v_cvt_pk_bf16_f32 v153,v154,v155
v_accvgpr_read_b32 v154,a20
v_mfma_f32_16x16x32_bf16 a[100:103],a[200:203],v[232:235],a[100:103]
v_accvgpr_read_b32 v155,a21
v_accvgpr_read_b32 v12,a22
v_mfma_f32_16x16x32_bf16 a[104:107],a[208:211],v[232:235],a[104:107]
buffer_load_dwordx4 v8,s[72:75],s83 offen offset:1024 lds
v_mfma_f32_16x16x32_bf16 a[104:107],a[212:215],v[236:239],a[104:107]
v_accvgpr_read_b32 v13,a23
v_mul_f32 v154,v20,v154
v_mfma_f32_16x16x32_bf16 a[108:111],a[220:223],v[236:239],a[108:111]
v_mul_f32 v155,v20,v155
v_mul_f32 v12,v20,v12
v_mfma_f32_16x16x32_bf16 a[108:111],a[216:219],v[232:235],a[108:111]
v_mul_f32 v13,v20,v13
v_cvt_pk_bf16_f32 v154,v154,v155
v_mfma_f32_16x16x32_bf16 a[112:115],a[224:227],v[232:235],a[112:115]
ds_read_b64_tr_b16 a[144:145],v2 offset:32
v_cvt_pk_bf16_f32 v155,v12,v13
v_mfma_f32_16x16x32_bf16 a[112:115],a[228:231],v[236:239],a[112:115]
ds_read_b64_tr_b16 a[146:147],v2 offset:5152
ds_read_b64_tr_b16 a[148:149],v2 offset:10272
v_mfma_f32_16x16x32_bf16 a[116:119],a[236:239],v[236:239],a[116:119]
ds_read_b64_tr_b16 a[150:151],v2 offset:15392
v_accvgpr_read_b32 v156,a24
v_mfma_f32_16x16x32_bf16 a[116:119],a[232:235],v[232:235],a[116:119]
buffer_load_dwordx4 v9,s[72:75],s83 offen offset:2048 lds
v_mfma_f32_16x16x32_bf16 a[120:123],a[240:243],v[232:235],a[120:123]
ds_read_b64_tr_b16 a[152:153],v2 offset:20512
ds_read_b64_tr_b16 a[154:155],v2 offset:25632
v_mfma_f32_16x16x32_bf16 a[120:123],a[244:247],v[236:239],a[120:123]
ds_read_b64_tr_b16 a[156:157],v2 offset:30752
v_accvgpr_read_b32 v157,a25
v_mfma_f32_16x16x32_bf16 a[124:127],a[252:255],v[236:239],a[124:127]
ds_read_b64_tr_b16 a[158:159],v2 offset:35872
v_mul_f32 v156,v20,v156
v_mfma_f32_16x16x32_bf16 a[124:127],a[248:251],v[232:235],a[124:127]
v_mul_f32 v157,v20,v157
)A"
R"A(v_accvgpr_read_b32 v158,a26
v_smfmac_f32_16x16x64_bf16 v[60:63],v[28:31],v[232:239],v27
s_waitcnt lgkmcnt(7)
v_accvgpr_read_b32 v159,a27
v_mfma_f32_16x16x32_bf16 a[192:195],a[128:131],v[64:67],0
v_cvt_pk_bf16_f32 v156,v156,v157
v_mul_f32 v158,v20,v158
v_mfma_f32_16x16x32_bf16 a[192:195],a[132:135],v[68:71],a[192:195]
v_mul_f32 v159,v20,v159
v_cvt_pk_bf16_f32 v157,v158,v159
v_mfma_f32_16x16x32_bf16 a[192:195],a[136:139],v[72:75],a[192:195]
buffer_store_dwordx4 v[140:143],v6,s[36:39],s84 offen offset:1024 sc1
v_mfma_f32_16x16x32_bf16 a[192:195],a[140:143],v[76:79],a[192:195]
v_accvgpr_read_b32 v158,a28
v_accvgpr_read_b32 v159,a29
v_mfma_f32_16x16x32_bf16 a[196:199],a[140:143],v[92:95],0
v_accvgpr_read_b32 v14,a30
v_accvgpr_read_b32 v15,a31
v_mfma_f32_16x16x32_bf16 a[196:199],a[136:139],v[88:91],a[196:199]
v_mul_f32 v158,v20,v158
v_mul_f32 v159,v20,v159
v_mfma_f32_16x16x32_bf16 a[196:199],a[132:135],v[84:87],a[196:199]
buffer_load_dwordx4 v10,s[72:75],s83 offen offset:3072 lds
v_mfma_f32_16x16x32_bf16 a[196:199],a[128:131],v[80:83],a[196:199]
v_mul_f32 v14,v20,v14
v_mul_f32 v15,v20,v15
v_mfma_f32_16x16x32_bf16 a[200:203],a[128:131],v[96:99],0
v_cvt_pk_bf16_f32 v158,v158,v159
v_cvt_pk_bf16_f32 v159,v14,v15
v_mfma_f32_16x16x32_bf16 a[200:203],a[132:135],v[100:103],a[200:203]
ds_read_b64_tr_b16 a[160:161],v1 offset:64
ds_read_b64_tr_b16 a[162:163],v1 offset:5184
v_mfma_f32_16x16x32_bf16 a[200:203],a[136:139],v[104:107],a[200:203]
ds_read_b64_tr_b16 a[164:165],v1 offset:10304
v_add_f32 v52,v53,v52
v_mfma_f32_16x16x32_bf16 a[200:203],a[140:143],v[108:111],a[200:203]
ds_read_b64_tr_b16 a[166:167],v1 offset:15424
v_max_f32 v21,0x00800000,v52
v_mfma_f32_16x16x32_bf16 a[204:207],a[140:143],v[124:127],0
s_cmp_ge_u32 1,s71
s_cselect_b32 s80,4,2
ds_read_b64_tr_b16 a[168:169],v1 offset:20544
v_mfma_f32_16x16x32_bf16 a[204:207],a[136:139],v[120:123],a[204:207]
s_add_u32 m0,s82,50176
ds_read_b64_tr_b16 a[170:171],v1 offset:25664
ds_read_b64_tr_b16 a[172:173],v1 offset:30784
v_mfma_f32_16x16x32_bf16 a[204:207],a[132:135],v[116:119],a[204:207]
s_mul_i32 s80,s80,163840
s_add_u32 s80,s80,s81
ds_read_b64_tr_b16 a[174:175],v1 offset:35904
v_mfma_f32_16x16x32_bf16 a[204:207],a[128:131],v[112:115],a[204:207]
s_waitcnt lgkmcnt(8)
v_rcp_f32 v21,v21
v_mfma_f32_16x16x32_bf16 a[208:211],a[144:147],v[64:67],0
buffer_load_dwordx4 v11,s[72:75],s83 offen lds
v_mfma_f32_16x16x32_bf16 a[208:211],a[148:151],v[68:71],a[208:211]
v_accvgpr_read_b32 v168,a32
v_accvgpr_read_b32 v169,a33
v_accvgpr_read_b32 v170,a34
v_accvgpr_read_b32 v171,a35
v_accvgpr_read_b32 v12,a36
v_accvgpr_read_b32 v13,a37
v_accvgpr_read_b32 v14,a38
v_accvgpr_read_b32 v15,a39
v_pk_mul_f32 v[168:169],v[20:21],v[168:169] op_sel:[1,0] op_sel_hi:[1,1]
v_pk_mul_f32 v[170:171],v[20:21],v[170:171] op_sel:[1,0] op_sel_hi:[1,1]
v_pk_mul_f32 v[12:13],v[20:21],v[12:13] op_sel:[1,0] op_sel_hi:[1,1]
v_pk_mul_f32 v[14:15],v[20:21],v[14:15] op_sel:[1,0] op_sel_hi:[1,1]
v_cvt_pk_bf16_f32 v168,v168,v169
v_cvt_pk_bf16_f32 v169,v170,v171
v_cvt_pk_bf16_f32 v170,v12,v13
v_cvt_pk_bf16_f32 v171,v14,v15
v_mfma_f32_16x16x32_bf16 a[208:211],a[152:155],v[72:75],a[208:211]
s_add_u32 s83,s80,20480
ds_read_b64_tr_b16 a[176:177],v2 offset:96
ds_read_b64_tr_b16 a[178:179],v2 offset:5216
v_mfma_f32_16x16x32_bf16 a[208:211],a[156:159],v[76:79],a[208:211]
ds_read_b64_tr_b16 a[180:181],v2 offset:10336
v_accvgpr_read_b32 v184,a48
v_mfma_f32_16x16x32_bf16 a[212:215],a[156:159],v[92:95],0
ds_read_b64_tr_b16 a[182:183],v2 offset:15456
v_accvgpr_read_b32 v185,a49
v_mfma_f32_16x16x32_bf16 a[212:215],a[152:155],v[88:91],a[212:215]
ds_read_b64_tr_b16 a[184:185],v2 offset:20576
v_mul_f32 v184,v21,v184
v_mfma_f32_16x16x32_bf16 a[212:215],a[148:151],v[84:87],a[212:215]
buffer_store_dwordx4 v[152:155],v6,s[36:39],s84 offen offset:2048 sc1
v_mfma_f32_16x16x32_bf16 a[212:215],a[144:147],v[80:83],a[212:215]
ds_read_b64_tr_b16 a[186:187],v2 offset:25696
ds_read_b64_tr_b16 a[188:189],v2 offset:30816
v_mfma_f32_16x16x32_bf16 a[216:219],a[144:147],v[96:99],0
ds_read_b64_tr_b16 a[190:191],v2 offset:35936
v_mul_f32 v185,v21,v185
v_mfma_f32_16x16x32_bf16 a[216:219],a[148:151],v[100:103],a[216:219]
v_accvgpr_read_b32 v172,a40
v_accvgpr_read_b32 v173,a41
v_accvgpr_read_b32 v174,a42
v_accvgpr_read_b32 v175,a43
v_accvgpr_read_b32 v12,a44
v_accvgpr_read_b32 v13,a45
v_accvgpr_read_b32 v14,a46
v_accvgpr_read_b32 v15,a47
v_pk_mul_f32 v[172:173],v[20:21],v[172:173] op_sel:[1,0] op_sel_hi:[1,1]
v_pk_mul_f32 v[174:175],v[20:21],v[174:175] op_sel:[1,0] op_sel_hi:[1,1]
v_pk_mul_f32 v[12:13],v[20:21],v[12:13] op_sel:[1,0] op_sel_hi:[1,1]
v_pk_mul_f32 v[14:15],v[20:21],v[14:15] op_sel:[1,0] op_sel_hi:[1,1]
v_cvt_pk_bf16_f32 v172,v172,v173
v_cvt_pk_bf16_f32 v173,v174,v175
v_cvt_pk_bf16_f32 v174,v12,v13
v_cvt_pk_bf16_f32 v175,v14,v15
v_mfma_f32_16x16x32_bf16 a[216:219],a[152:155],v[104:107],a[216:219]
v_accvgpr_read_b32 v186,a50
v_accvgpr_read_b32 v187,a51
v_mfma_f32_16x16x32_bf16 a[216:219],a[156:159],v[108:111],a[216:219]
v_cvt_pk_bf16_f32 v184,v184,v185
v_mul_f32 v186,v21,v186
v_mfma_f32_16x16x32_bf16 a[220:223],a[156:159],v[124:127],0
v_mul_f32 v187,v21,v187
v_cvt_pk_bf16_f32 v185,v186,v187
v_mfma_f32_16x16x32_bf16 a[220:223],a[152:155],v[120:123],a[220:223]
v_accvgpr_read_b32 v186,a52
v_accvgpr_read_b32 v187,a53
v_mfma_f32_16x16x32_bf16 a[220:223],a[148:151],v[116:119],a[220:223]
s_add_u32 m0,s82,122880
v_accvgpr_read_b32 v16,a54
v_accvgpr_read_b32 v17,a55
v_mfma_f32_16x16x32_bf16 a[220:223],a[144:147],v[112:115],a[220:223]
s_waitcnt lgkmcnt(1)
v_mul_f32 v186,v21,v186
v_mul_f32 v187,v21,v187
v_mfma_f32_16x16x32_bf16 a[224:227],a[160:163],v[64:67],0
v_mul_f32 v16,v21,v16
v_mul_f32 v17,v21,v17
v_mfma_f32_16x16x32_bf16 a[224:227],a[164:167],v[68:71],a[224:227]
buffer_load_dwordx4 v7,s[72:75],s80 offen lds
v_mfma_f32_16x16x32_bf16 a[224:227],a[168:171],v[72:75],a[224:227]
v_cvt_pk_bf16_f32 v186,v186,v187
v_cvt_pk_bf16_f32 v187,v16,v17
v_mfma_f32_16x16x32_bf16 a[224:227],a[172:175],v[76:79],a[224:227]
ds_read_b64_tr_b16 a[128:129],v1 offset:128
ds_read_b64_tr_b16 a[130:131],v1 offset:5248
v_mfma_f32_16x16x32_bf16 a[228:231],a[172:175],v[92:95],0
ds_read_b64_tr_b16 a[132:133],v1 offset:10368
v_accvgpr_read_b32 v188,a56
v_mfma_f32_16x16x32_bf16 a[228:231],a[168:171],v[88:91],a[228:231]
ds_read_b64_tr_b16 a[134:135],v1 offset:15488
v_accvgpr_read_b32 v189,a57
v_mfma_f32_16x16x32_bf16 a[228:231],a[164:167],v[84:87],a[228:231]
ds_read_b64_tr_b16 a[136:137],v1 offset:20608
v_mul_f32 v188,v21,v188
v_mfma_f32_16x16x32_bf16 a[228:231],a[160:163],v[80:83],a[228:231]
ds_read_b64_tr_b16 a[138:139],v1 offset:25728
ds_read_b64_tr_b16 a[140:141],v1 offset:30848
v_mfma_f32_16x16x32_bf16 a[232:235],a[160:163],v[96:99],0
ds_read_b64_tr_b16 a[142:143],v1 offset:35968
v_mul_f32 v189,v21,v189
v_mfma_f32_16x16x32_bf16 a[232:235],a[164:167],v[100:103],a[232:235]
v_accvgpr_read_b32 v190,a58
v_accvgpr_read_b32 v191,a59
v_mfma_f32_16x16x32_bf16 a[232:235],a[168:171],v[104:107],a[232:235]
buffer_store_dwordx4 v[156:159],v6,s[36:39],s84 offen offset:3072 sc1
v_mfma_f32_16x16x32_bf16 a[232:235],a[172:175],v[108:111],a[232:235]
v_cvt_pk_bf16_f32 v188,v188,v189
v_mul_f32 v190,v21,v190
v_mfma_f32_16x16x32_bf16 a[236:239],a[172:175],v[124:127],0
v_mul_f32 v191,v21,v191
v_cvt_pk_bf16_f32 v189,v190,v191
v_mfma_f32_16x16x32_bf16 a[236:239],a[168:171],v[120:123],a[236:239]
v_accvgpr_read_b32 v190,a60
v_accvgpr_read_b32 v191,a61
v_mfma_f32_16x16x32_bf16 a[236:239],a[164:167],v[116:119],a[236:239]
buffer_load_dwordx4 v8,s[72:75],s80 offen offset:1024 lds
v_mfma_f32_16x16x32_bf16 a[236:239],a[160:163],v[112:115],a[236:239]
v_accvgpr_read_b32 v12,a62
v_accvgpr_read_b32 v13,a63
v_mfma_f32_16x16x32_bf16 a[240:243],a[176:179],v[64:67],0
v_mul_f32 v190,v21,v190
v_mul_f32 v191,v21,v191
v_mfma_f32_16x16x32_bf16 a[240:243],a[180:183],v[68:71],a[240:243]
v_mul_f32 v12,v21,v12
v_mul_f32 v13,v21,v13
v_mfma_f32_16x16x32_bf16 a[240:243],a[184:187],v[72:75],a[240:243]
s_waitcnt lgkmcnt(0)
v_cvt_pk_bf16_f32 v190,v190,v191
v_cvt_pk_bf16_f32 v191,v12,v13
v_mfma_f32_16x16x32_bf16 a[240:243],a[188:191],v[76:79],a[240:243]
ds_read_b64_tr_b16 a[144:145],v2 offset:160
ds_read_b64_tr_b16 a[146:147],v2 offset:5280
v_mfma_f32_16x16x32_bf16 a[244:247],a[188:191],v[92:95],0
ds_read_b64_tr_b16 a[148:149],v2 offset:10400
v_add_f32 v56,v57,v56
v_mfma_f32_16x16x32_bf16 a[244:247],a[184:187],v[88:91],a[244:247]
ds_read_b64_tr_b16 a[150:151],v2 offset:15520
v_max_f32 v22,0x00800000,v56
v_mfma_f32_16x16x32_bf16 a[244:247],a[180:183],v[84:87],a[244:247]
ds_read_b64_tr_b16 a[152:153],v2 offset:20640
v_accvgpr_read_b32 v200,a64
v_mfma_f32_16x16x32_bf16 a[244:247],a[176:179],v[80:83],a[244:247]
ds_read_b64_tr_b16 a[154:155],v2 offset:25760
ds_read_b64_tr_b16 a[156:157],v2 offset:30880
v_mfma_f32_16x16x32_bf16 a[248:251],a[176:179],v[96:99],0
v_rcp_f32 v22,v22
v_mfma_f32_16x16x32_bf16 a[248:251],a[180:183],v[100:103],a[248:251]
buffer_load_dwordx4 v9,s[72:75],s80 offen offset:2048 lds
v_mfma_f32_16x16x32_bf16 a[248:251],a[184:187],v[104:107],a[248:251]
ds_read_b64_tr_b16 a[158:159],v2 offset:36000
v_accvgpr_read_b32 v201,a65
v_mfma_f32_16x16x32_bf16 a[248:251],a[188:191],v[108:111],a[248:251]
v_mul_f32 v200,v22,v200
v_mul_f32 v201,v22,v201
v_mfma_f32_16x16x32_bf16 a[252:255],a[188:191],v[124:127],0
v_accvgpr_read_b32 v202,a66
v_accvgpr_read_b32 v203,a67
v_mfma_f32_16x16x32_bf16 a[252:255],a[184:187],v[120:123],a[252:255]
v_cvt_pk_bf16_f32 v200,v200,v201
v_mul_f32 v202,v22,v202
v_mfma_f32_16x16x32_bf16 a[252:255],a[180:183],v[116:119],a[252:255]
s_waitcnt vmcnt(18)
v_mul_f32 v203,v22,v203
v_cvt_pk_bf16_f32 v201,v202,v203
v_mfma_f32_16x16x32_bf16 a[252:255],a[176:179],v[112:115],a[252:255]
s_barrier
buffer_store_dwordx4 v[168:171],v6,s[36:39],s85 offen sc1
v_mfma_f32_16x16x32_bf16 a[0:3],a[128:131],v[64:67],0
v_accvgpr_read_b32 v202,a68
v_accvgpr_read_b32 v203,a69
v_mfma_f32_16x16x32_bf16 a[0:3],a[132:135],v[68:71],a[0:3]
v_accvgpr_read_b32 v14,a70
v_accvgpr_read_b32 v15,a71
v_mfma_f32_16x16x32_bf16 a[0:3],a[136:139],v[72:75],a[0:3]
v_mul_f32 v202,v22,v202
v_mul_f32 v203,v22,v203
v_mfma_f32_16x16x32_bf16 a[0:3],a[140:143],v[76:79],a[0:3]
v_mul_f32 v14,v22,v14
v_mul_f32 v15,v22,v15
v_mfma_f32_16x16x32_bf16 a[4:7],a[140:143],v[92:95],0
buffer_load_dwordx4 v10,s[72:75],s80 offen offset:3072 lds
v_mfma_f32_16x16x32_bf16 a[4:7],a[136:139],v[88:91],a[4:7]
s_lshl_b32 s94,s70,4
v_cvt_pk_bf16_f32 v202,v202,v203
v_cvt_pk_bf16_f32 v203,v14,v15
v_mfma_f32_16x16x32_bf16 a[4:7],a[132:135],v[84:87],a[4:7]
s_add_u32 s94,s94,s40
v_accvgpr_read_b32 v204,a72
v_accvgpr_read_b32 v205,a73
v_mfma_f32_16x16x32_bf16 a[4:7],a[128:131],v[80:83],a[4:7]
s_lshl_b32 s94,s94,4
v_mul_f32 v204,v22,v204
v_mul_f32 v205,v22,v205
v_mfma_f32_16x16x32_bf16 a[8:11],a[128:131],v[96:99],0
v_accvgpr_read_b32 v206,a74
v_accvgpr_read_b32 v207,a75
v_mfma_f32_16x16x32_bf16 a[8:11],a[132:135],v[100:103],a[8:11]
ds_read_b64_tr_b16 a[160:161],v1 offset:192
ds_read_b64_tr_b16 a[162:163],v1 offset:5312
v_mfma_f32_16x16x32_bf16 a[8:11],a[136:139],v[104:107],a[8:11]
v_cvt_pk_bf16_f32 v204,v204,v205
v_mul_f32 v206,v22,v206
v_mfma_f32_16x16x32_bf16 a[8:11],a[140:143],v[108:111],a[8:11]
v_mul_f32 v207,v22,v207
v_cvt_pk_bf16_f32 v205,v206,v207
v_mfma_f32_16x16x32_bf16 a[12:15],a[140:143],v[124:127],0
ds_read_b64_tr_b16 a[164:165],v1 offset:10432
ds_read_b64_tr_b16 a[166:167],v1 offset:15552
v_mfma_f32_16x16x32_bf16 a[12:15],a[136:139],v[120:123],a[12:15]
s_add_u32 m0,s82,126976
v_accvgpr_read_b32 v206,a76
v_accvgpr_read_b32 v207,a77
v_mfma_f32_16x16x32_bf16 a[12:15],a[132:135],v[116:119],a[12:15]
v_accvgpr_read_b32 v16,a78
v_accvgpr_read_b32 v17,a79
v_mfma_f32_16x16x32_bf16 a[12:15],a[128:131],v[112:115],a[12:15]
s_waitcnt lgkmcnt(4)
v_mul_f32 v206,v22,v206
v_mul_f32 v207,v22,v207
v_mfma_f32_16x16x32_bf16 a[16:19],a[144:147],v[64:67],0
buffer_load_dwordx4 v11,s[72:75],s80 offen lds
v_mfma_f32_16x16x32_bf16 a[16:19],a[148:151],v[68:71],a[16:19]
ds_read_b64_tr_b16 a[168:169],v1 offset:20672
ds_read_b64_tr_b16 a[170:171],v1 offset:25792
v_mfma_f32_16x16x32_bf16 a[16:19],a[152:155],v[72:75],a[16:19]
)A"
R"A(v_mul_f32 v16,v22,v16
v_mul_f32 v17,v22,v17
v_mfma_f32_16x16x32_bf16 a[16:19],a[156:159],v[76:79],a[16:19]
v_cvt_pk_bf16_f32 v206,v206,v207
v_cvt_pk_bf16_f32 v207,v16,v17
v_mfma_f32_16x16x32_bf16 a[20:23],a[156:159],v[92:95],0
buffer_store_dwordx4 v[172:175],v6,s[36:39],s85 offen offset:1024 sc1
v_mfma_f32_16x16x32_bf16 a[20:23],a[152:155],v[88:91],a[20:23]
ds_read_b64_tr_b16 a[172:173],v1 offset:30912
ds_read_b64_tr_b16 a[174:175],v1 offset:36032
v_mfma_f32_16x16x32_bf16 a[20:23],a[148:151],v[84:87],a[20:23]
v_accvgpr_read_b32 v216,a80
v_accvgpr_read_b32 v217,a81
v_mfma_f32_16x16x32_bf16 a[20:23],a[144:147],v[80:83],a[20:23]
v_mul_f32 v216,v22,v216
v_mul_f32 v217,v22,v217
v_mfma_f32_16x16x32_bf16 a[24:27],a[144:147],v[96:99],0
v_accvgpr_read_b32 v218,a82
v_accvgpr_read_b32 v219,a83
v_mfma_f32_16x16x32_bf16 a[24:27],a[148:151],v[100:103],a[24:27]
s_add_u32 m0,s82,128000
v_cvt_pk_bf16_f32 v216,v216,v217
v_mul_f32 v218,v22,v218
v_mfma_f32_16x16x32_bf16 a[24:27],a[152:155],v[104:107],a[24:27]
v_mul_f32 v219,v22,v219
v_cvt_pk_bf16_f32 v217,v218,v219
v_mfma_f32_16x16x32_bf16 a[24:27],a[156:159],v[108:111],a[24:27]
v_accvgpr_read_b32 v218,a84
v_accvgpr_read_b32 v219,a85
v_mfma_f32_16x16x32_bf16 a[28:31],a[156:159],v[124:127],0
buffer_load_dwordx4 v7,s[72:75],s83 offen lds
v_mfma_f32_16x16x32_bf16 a[28:31],a[152:155],v[120:123],a[28:31]
v_accvgpr_read_b32 v12,a86
v_accvgpr_read_b32 v13,a87
v_mfma_f32_16x16x32_bf16 a[28:31],a[148:151],v[116:119],a[28:31]
v_mul_f32 v218,v22,v218
v_mul_f32 v219,v22,v219
v_mfma_f32_16x16x32_bf16 a[28:31],a[144:147],v[112:115],a[28:31]
s_waitcnt lgkmcnt(0)
v_mul_f32 v12,v22,v12
v_mul_f32 v13,v22,v13
v_mfma_f32_16x16x32_bf16 a[32:35],a[160:163],v[64:67],0
v_cvt_pk_bf16_f32 v218,v218,v219
v_cvt_pk_bf16_f32 v219,v12,v13
v_mfma_f32_16x16x32_bf16 a[32:35],a[164:167],v[68:71],a[32:35]
ds_read_b64_tr_b16 a[176:177],v2 offset:224
ds_read_b64_tr_b16 a[178:179],v2 offset:5344
v_mfma_f32_16x16x32_bf16 a[32:35],a[168:171],v[72:75],a[32:35]
ds_read_b64_tr_b16 a[180:181],v2 offset:10464
v_bfe_u32 v24,v6,4,2
v_mfma_f32_16x16x32_bf16 a[32:35],a[172:175],v[76:79],a[32:35]
ds_read_b64_tr_b16 a[182:183],v2 offset:15584
v_min_u32 v24,2,v24
v_mfma_f32_16x16x32_bf16 a[36:39],a[172:175],v[92:95],0
ds_read_b64_tr_b16 a[184:185],v2 offset:20704
v_add_u32 v24,s71,v24
v_mfma_f32_16x16x32_bf16 a[36:39],a[168:171],v[88:91],a[36:39]
ds_read_b64_tr_b16 a[186:187],v2 offset:25824
ds_read_b64_tr_b16 a[188:189],v2 offset:30944
v_mfma_f32_16x16x32_bf16 a[36:39],a[164:167],v[84:87],a[36:39]
buffer_store_dwordx4 v[184:187],v6,s[36:39],s85 offen offset:2048 sc1
v_mfma_f32_16x16x32_bf16 a[36:39],a[160:163],v[80:83],a[36:39]
ds_read_b64_tr_b16 a[190:191],v2 offset:36064
v_add_u32 v24,1,v24
v_mfma_f32_16x16x32_bf16 a[40:43],a[160:163],v[96:99],0
v_and_b32 v24,3,v24
v_lshlrev_b32 v24,6,v24
v_mfma_f32_16x16x32_bf16 a[40:43],a[164:167],v[100:103],a[40:43]
v_accvgpr_read_b32 v220,a88
v_accvgpr_read_b32 v221,a89
v_mfma_f32_16x16x32_bf16 a[40:43],a[168:171],v[104:107],a[40:43]
buffer_load_dwordx4 v8,s[72:75],s83 offen offset:1024 lds
v_mfma_f32_16x16x32_bf16 a[40:43],a[172:175],v[108:111],a[40:43]
v_mul_f32 v220,v22,v220
v_mul_f32 v221,v22,v221
v_mfma_f32_16x16x32_bf16 a[44:47],a[172:175],v[124:127],0
v_accvgpr_read_b32 v222,a90
v_accvgpr_read_b32 v223,a91
v_mfma_f32_16x16x32_bf16 a[44:47],a[168:171],v[120:123],a[44:47]
v_cvt_pk_bf16_f32 v220,v220,v221
v_mul_f32 v222,v22,v222
v_mfma_f32_16x16x32_bf16 a[44:47],a[164:167],v[116:119],a[44:47]
buffer_load_dwordx2 v[26:27],v24,s[64:67],s94 offen sc1
v_mfma_f32_16x16x32_bf16 a[44:47],a[160:163],v[112:115],a[44:47]
s_waitcnt lgkmcnt(0)
v_mul_f32 v223,v22,v223
v_cvt_pk_bf16_f32 v221,v222,v223
v_mfma_f32_16x16x32_bf16 a[48:51],a[176:179],v[64:67],0
v_accvgpr_read_b32 v222,a92
v_accvgpr_read_b32 v223,a93
v_mfma_f32_16x16x32_bf16 a[48:51],a[180:183],v[68:71],a[48:51]
v_accvgpr_read_b32 v14,a94
v_accvgpr_read_b32 v15,a95
v_mfma_f32_16x16x32_bf16 a[48:51],a[184:187],v[72:75],a[48:51]
v_mul_f32 v222,v22,v222
v_mul_f32 v223,v22,v223
v_mfma_f32_16x16x32_bf16 a[48:51],a[188:191],v[76:79],a[48:51]
v_mul_f32 v14,v22,v14
v_mul_f32 v15,v22,v15
v_mfma_f32_16x16x32_bf16 a[52:55],a[188:191],v[92:95],0
v_cvt_pk_bf16_f32 v222,v222,v223
v_cvt_pk_bf16_f32 v223,v14,v15
v_mfma_f32_16x16x32_bf16 a[52:55],a[184:187],v[88:91],a[52:55]
buffer_load_dwordx4 v9,s[72:75],s83 offen offset:2048 lds
v_mfma_f32_16x16x32_bf16 a[52:55],a[180:183],v[84:87],a[52:55]
ds_read_b64_tr_b16 a[128:129],v1 offset:256
ds_read_b64_tr_b16 a[130:131],v1 offset:5376
v_mfma_f32_16x16x32_bf16 a[52:55],a[176:179],v[80:83],a[52:55]
ds_read_b64_tr_b16 a[132:133],v1 offset:10496
v_add_f32 v60,v61,v60
v_mfma_f32_16x16x32_bf16 a[56:59],a[176:179],v[96:99],0
ds_read_b64_tr_b16 a[134:135],v1 offset:15616
v_max_f32 v23,0x00800000,v60
v_mfma_f32_16x16x32_bf16 a[56:59],a[180:183],v[100:103],a[56:59]
ds_read_b64_tr_b16 a[136:137],v1 offset:20736
ds_read_b64_tr_b16 a[138:139],v1 offset:25856
v_mfma_f32_16x16x32_bf16 a[56:59],a[184:187],v[104:107],a[56:59]
ds_read_b64_tr_b16 a[140:141],v1 offset:30976
ds_read_b64_tr_b16 a[142:143],v1 offset:36096
v_mfma_f32_16x16x32_bf16 a[56:59],a[188:191],v[108:111],a[56:59]
buffer_store_dwordx4 v[188:191],v6,s[36:39],s85 offen offset:3072 sc1
v_mfma_f32_16x16x32_bf16 a[60:63],a[188:191],v[124:127],0
v_rcp_f32 v23,v23
v_mfma_f32_16x16x32_bf16 a[60:63],a[184:187],v[120:123],a[60:63]
v_accvgpr_read_b32 v232,a96
v_accvgpr_read_b32 v233,a97
v_mfma_f32_16x16x32_bf16 a[60:63],a[180:183],v[116:119],a[60:63]
v_mul_f32 v232,v23,v232
v_mul_f32 v233,v23,v233
v_mfma_f32_16x16x32_bf16 a[60:63],a[176:179],v[112:115],a[60:63]
s_waitcnt lgkmcnt(5)
v_accvgpr_read_b32 v234,a98
v_accvgpr_read_b32 v235,a99
v_mfma_f32_16x16x32_bf16 a[64:67],a[128:131],v[64:67],0
s_waitcnt lgkmcnt(4)
v_cvt_pk_bf16_f32 v232,v232,v233
v_mul_f32 v234,v23,v234
v_mfma_f32_16x16x32_bf16 a[64:67],a[132:135],v[68:71],a[64:67]
s_waitcnt lgkmcnt(2)
v_mul_f32 v235,v23,v235
v_cvt_pk_bf16_f32 v233,v234,v235
v_mfma_f32_16x16x32_bf16 a[64:67],a[136:139],v[72:75],a[64:67]
s_waitcnt lgkmcnt(0)
v_accvgpr_read_b32 v234,a100
v_accvgpr_read_b32 v235,a101
v_mfma_f32_16x16x32_bf16 a[64:67],a[140:143],v[76:79],a[64:67]
buffer_load_dwordx4 v10,s[72:75],s83 offen offset:3072 lds
v_mfma_f32_16x16x32_bf16 a[68:71],a[140:143],v[92:95],0
v_accvgpr_read_b32 v16,a102
v_accvgpr_read_b32 v17,a103
v_mfma_f32_16x16x32_bf16 a[68:71],a[136:139],v[88:91],a[68:71]
v_mul_f32 v234,v23,v234
v_mul_f32 v235,v23,v235
v_mfma_f32_16x16x32_bf16 a[68:71],a[132:135],v[84:87],a[68:71]
v_mul_f32 v16,v23,v16
v_mul_f32 v17,v23,v17
v_mfma_f32_16x16x32_bf16 a[68:71],a[128:131],v[80:83],a[68:71]
ds_read_b64_tr_b16 a[144:145],v2 offset:288
ds_read_b64_tr_b16 a[146:147],v2 offset:5408
v_mfma_f32_16x16x32_bf16 a[72:75],a[128:131],v[96:99],0
ds_read_b64_tr_b16 a[148:149],v2 offset:10528
ds_read_b64_tr_b16 a[150:151],v2 offset:15648
v_mfma_f32_16x16x32_bf16 a[72:75],a[132:135],v[100:103],a[72:75]
ds_read_b64_tr_b16 a[152:153],v2 offset:20768
ds_read_b64_tr_b16 a[154:155],v2 offset:25888
v_mfma_f32_16x16x32_bf16 a[72:75],a[136:139],v[104:107],a[72:75]
ds_read_b64_tr_b16 a[156:157],v2 offset:31008
ds_read_b64_tr_b16 a[158:159],v2 offset:36128
v_mfma_f32_16x16x32_bf16 a[72:75],a[140:143],v[108:111],a[72:75]
s_add_u32 m0,s82,132096
v_mfma_f32_16x16x32_bf16 a[76:79],a[140:143],v[124:127],0
v_cvt_pk_bf16_f32 v234,v234,v235
v_cvt_pk_bf16_f32 v235,v16,v17
v_mfma_f32_16x16x32_bf16 a[76:79],a[136:139],v[120:123],a[76:79]
v_accvgpr_read_b32 v236,a104
v_accvgpr_read_b32 v237,a105
v_mfma_f32_16x16x32_bf16 a[76:79],a[132:135],v[116:119],a[76:79]
v_mul_f32 v236,v23,v236
v_mul_f32 v237,v23,v237
v_mfma_f32_16x16x32_bf16 a[76:79],a[128:131],v[112:115],a[76:79]
s_waitcnt lgkmcnt(6)
buffer_load_dwordx4 v11,s[72:75],s83 offen lds
v_mfma_f32_16x16x32_bf16 a[80:83],a[144:147],v[64:67],0
s_waitcnt lgkmcnt(4)
v_accvgpr_read_b32 v238,a106
v_accvgpr_read_b32 v239,a107
v_mfma_f32_16x16x32_bf16 a[80:83],a[148:151],v[68:71],a[80:83]
s_waitcnt lgkmcnt(2)
v_cvt_pk_bf16_f32 v236,v236,v237
v_mul_f32 v238,v23,v238
v_mfma_f32_16x16x32_bf16 a[80:83],a[152:155],v[72:75],a[80:83]
s_waitcnt lgkmcnt(0)
v_mul_f32 v239,v23,v239
v_cvt_pk_bf16_f32 v237,v238,v239
v_mfma_f32_16x16x32_bf16 a[80:83],a[156:159],v[76:79],a[80:83]
v_accvgpr_read_b32 v238,a108
v_accvgpr_read_b32 v239,a109
v_mfma_f32_16x16x32_bf16 a[84:87],a[156:159],v[92:95],0
v_accvgpr_read_b32 v12,a110
v_accvgpr_read_b32 v13,a111
v_mfma_f32_16x16x32_bf16 a[84:87],a[152:155],v[88:91],a[84:87]
v_mul_f32 v238,v23,v238
v_mul_f32 v239,v23,v239
v_mfma_f32_16x16x32_bf16 a[84:87],a[148:151],v[84:87],a[84:87]
v_subrev_u32 v1,81920,v1
v_mul_f32 v12,v23,v12
v_mfma_f32_16x16x32_bf16 a[84:87],a[144:147],v[80:83],a[84:87]
ds_read_b64_tr_b16 a[160:161],v1
ds_read_b64_tr_b16 a[162:163],v1 offset:5120
v_mfma_f32_16x16x32_bf16 a[88:91],a[144:147],v[96:99],0
ds_read_b64_tr_b16 a[164:165],v1 offset:10240
ds_read_b64_tr_b16 a[166:167],v1 offset:15360
v_mfma_f32_16x16x32_bf16 a[88:91],a[148:151],v[100:103],a[88:91]
ds_read_b64_tr_b16 a[168:169],v1 offset:20480
ds_read_b64_tr_b16 a[170:171],v1 offset:25600
v_mfma_f32_16x16x32_bf16 a[88:91],a[152:155],v[104:107],a[88:91]
ds_read_b64_tr_b16 a[172:173],v1 offset:30720
ds_read_b64_tr_b16 a[174:175],v1 offset:35840
v_mfma_f32_16x16x32_bf16 a[88:91],a[156:159],v[108:111],a[88:91]
v_accvgpr_read_b32 v248,a112
v_accvgpr_read_b32 v249,a113
v_mul_f32 v248,v23,v248
v_mul_f32 v249,v23,v249
v_accvgpr_read_b32 v250,a114
v_accvgpr_read_b32 v251,a115
v_accvgpr_read_b32 v252,a120
v_accvgpr_read_b32 v253,a121
v_cmp_nlt_f32 s[56:57],v48,s63
v_mul_f32 v252,v23,v252
v_mul_f32 v253,v23,v253
v_accvgpr_read_b32 v254,a122
v_accvgpr_read_b32 v255,a123
v_mfma_f32_16x16x32_bf16 a[92:95],a[156:159],v[124:127],0
v_cvt_pk_bf16_f32 v248,v248,v249
v_mul_f32 v250,v23,v250
v_mul_f32 v251,v23,v251
v_cvt_pk_bf16_f32 v249,v250,v251
v_cvt_pk_bf16_f32 v252,v252,v253
v_mul_f32 v254,v23,v254
v_mul_f32 v255,v23,v255
v_cmp_nlt_f32 s[58:59],v52,s63
s_nop 1
s_or_b64 s[56:57],s[56:57],s[58:59]
v_cvt_pk_bf16_f32 v253,v254,v255
v_mfma_f32_16x16x32_bf16 a[92:95],a[152:155],v[120:123],a[92:95]
v_accvgpr_read_b32 v250,a116
v_accvgpr_read_b32 v251,a117
v_accvgpr_read_b32 v14,a118
v_accvgpr_read_b32 v15,a119
v_mul_f32 v250,v23,v250
v_mul_f32 v251,v23,v251
v_accvgpr_read_b32 v254,a124
v_accvgpr_read_b32 v255,a125
v_cmp_nlt_f32 s[58:59],v56,s63
s_nop 1
s_or_b64 s[56:57],s[56:57],s[58:59]
v_accvgpr_read_b32 v16,a126
v_accvgpr_read_b32 v17,a127
v_mul_f32 v254,v23,v254
v_mul_f32 v255,v23,v255
s_waitcnt vmcnt(4)
v_cmp_ne_u32 s[90:91],s68,v26
v_cmp_ne_u32 s[92:93],s69,v27
s_nop 0
s_or_b64 s[90:91],s[90:91],s[92:93]
s_cbranch_scc1 L_fr8_%=
L_fo8_%=:
v_mfma_f32_16x16x32_bf16 a[92:95],a[148:151],v[116:119],a[92:95]
v_mul_f32 v13,v23,v13
v_cvt_pk_bf16_f32 v238,v238,v239
v_cvt_pk_bf16_f32 v239,v12,v13
v_mul_f32 v14,v23,v14
v_mul_f32 v15,v23,v15
v_cvt_pk_bf16_f32 v250,v250,v251
v_cvt_pk_bf16_f32 v251,v14,v15
v_mul_f32 v16,v23,v16
v_mul_f32 v17,v23,v17
v_cvt_pk_bf16_f32 v254,v254,v255
v_cmp_nlt_f32 s[58:59],v60,s63
s_nop 1
s_or_b64 s[56:57],s[56:57],s[58:59]
v_cvt_pk_bf16_f32 v255,v16,v17
v_mfma_f32_16x16x32_bf16 a[92:95],a[144:147],v[112:115],a[92:95]
s_cmp_lg_u64 s[56:57],0
s_cbranch_scc1 L_slow1_%=
L_sret1_%=:
v_subrev_u32 v2,81920,v2
s_waitcnt lgkmcnt(6)
v_mfma_f32_16x16x32_bf16 a[192:195],a[160:163],v[136:139],a[192:195]
s_cmp_ge_u32 0,s71
s_cselect_b32 s76,2,0
s_lshl_b32 s76,s76,16
s_waitcnt lgkmcnt(4)
v_mfma_f32_16x16x32_bf16 a[192:195],a[164:167],v[140:143],a[192:195]
s_add_u32 s77,s76,4096
s_add_u32 s78,s76,8192
s_add_u32 s79,s76,12288
s_max_i32 s76,s76,s18
s_waitcnt lgkmcnt(2)
v_mfma_f32_16x16x32_bf16 a[192:195],a[168:171],v[152:155],a[192:195]
s_max_i32 s77,s77,s19
buffer_load_dwordx4 v[128:131],v6,s[28:31],s76 offen sc1
s_waitcnt lgkmcnt(0)
v_mfma_f32_16x16x32_bf16 a[192:195],a[172:175],v[156:159],a[192:195]
s_max_i32 s78,s78,s100
ds_read_b64_tr_b16 a[176:177],v2 offset:32
ds_read_b64_tr_b16 a[178:179],v2 offset:5152
v_mfma_f32_16x16x32_bf16 a[196:199],a[172:175],v[188:191],a[196:199]
s_cmp_ge_u32 2,s71
)A"
R"A(s_cselect_b32 s80,6,4
ds_read_b64_tr_b16 a[180:181],v2 offset:10272
v_mfma_f32_16x16x32_bf16 a[196:199],a[168:171],v[184:187],a[196:199]
s_mul_i32 s80,s80,163840
s_add_u32 s80,s80,s81
ds_read_b64_tr_b16 a[182:183],v2 offset:15392
v_mfma_f32_16x16x32_bf16 a[196:199],a[164:167],v[172:175],a[196:199]
s_max_i32 s79,s79,s101
buffer_store_dwordx4 v[200:203],v6,s[36:39],s86 offen sc1
v_mfma_f32_16x16x32_bf16 a[196:199],a[160:163],v[168:171],a[196:199]
s_add_u32 s83,s80,20480
ds_read_b64_tr_b16 a[184:185],v2 offset:20512
ds_read_b64_tr_b16 a[186:187],v2 offset:25632
v_mfma_f32_16x16x32_bf16 a[200:203],a[160:163],v[200:203],a[200:203]
ds_read_b64_tr_b16 a[188:189],v2 offset:30752
v_mfma_f32_16x16x32_bf16 a[200:203],a[164:167],v[204:207],a[200:203]
ds_read_b64_tr_b16 a[190:191],v2 offset:35872
v_mfma_f32_16x16x32_bf16 a[200:203],a[168:171],v[216:219],a[200:203]
buffer_load_dwordx4 v[160:163],v6,s[28:31],s76 offen offset:1024 sc1
v_mfma_f32_16x16x32_bf16 a[200:203],a[172:175],v[220:223],a[200:203]
ds_read_b64_tr_b16 a[128:129],v1 offset:64
ds_read_b64_tr_b16 a[130:131],v1 offset:5184
v_mfma_f32_16x16x32_bf16 a[204:207],a[172:175],v[252:255],a[204:207]
ds_read_b64_tr_b16 a[132:133],v1 offset:10304
v_mfma_f32_16x16x32_bf16 a[204:207],a[168:171],v[248:251],a[204:207]
ds_read_b64_tr_b16 a[134:135],v1 offset:15424
v_mfma_f32_16x16x32_bf16 a[204:207],a[164:167],v[236:239],a[204:207]
buffer_store_dwordx4 v[204:207],v6,s[36:39],s86 offen offset:1024 sc1
v_mfma_f32_16x16x32_bf16 a[204:207],a[160:163],v[232:235],a[204:207]
s_barrier
ds_read_b64_tr_b16 a[136:137],v1 offset:20544
ds_read_b64_tr_b16 a[138:139],v1 offset:25664
s_waitcnt lgkmcnt(8)
v_mfma_f32_16x16x32_bf16 a[208:211],a[176:179],v[136:139],a[208:211]
ds_read_b64_tr_b16 a[140:141],v1 offset:30784
v_mfma_f32_16x16x32_bf16 a[208:211],a[180:183],v[140:143],a[208:211]
buffer_load_dwordx4 v[192:195],v6,s[28:31],s76 offen offset:2048 sc1
v_mfma_f32_16x16x32_bf16 a[208:211],a[184:187],v[152:155],a[208:211]
s_add_u32 m0,s82,81920
ds_read_b64_tr_b16 a[142:143],v1 offset:35904
ds_read_b64_tr_b16 a[144:145],v2 offset:96
s_waitcnt lgkmcnt(9)
v_mfma_f32_16x16x32_bf16 a[208:211],a[188:191],v[156:159],a[208:211]
ds_read_b64_tr_b16 a[146:147],v2 offset:5216
v_mfma_f32_16x16x32_bf16 a[212:215],a[188:191],v[188:191],a[212:215]
ds_read_b64_tr_b16 a[148:149],v2 offset:10336
v_mfma_f32_16x16x32_bf16 a[212:215],a[184:187],v[184:187],a[212:215]
buffer_store_dwordx4 v[216:219],v6,s[36:39],s86 offen offset:2048 sc1
v_mfma_f32_16x16x32_bf16 a[212:215],a[180:183],v[172:175],a[212:215]
ds_read_b64_tr_b16 a[150:151],v2 offset:15456
ds_read_b64_tr_b16 a[152:153],v2 offset:20576
v_mfma_f32_16x16x32_bf16 a[212:215],a[176:179],v[168:171],a[212:215]
ds_read_b64_tr_b16 a[154:155],v2 offset:25696
v_mfma_f32_16x16x32_bf16 a[216:219],a[176:179],v[200:203],a[216:219]
ds_read_b64_tr_b16 a[156:157],v2 offset:30816
v_mfma_f32_16x16x32_bf16 a[216:219],a[180:183],v[204:207],a[216:219]
buffer_load_dwordx4 v[224:227],v6,s[28:31],s76 offen offset:3072 sc1
v_mfma_f32_16x16x32_bf16 a[216:219],a[184:187],v[216:219],a[216:219]
ds_read_b64_tr_b16 a[158:159],v2 offset:35936
ds_read_b64_tr_b16 a[160:161],v1 offset:128
v_mfma_f32_16x16x32_bf16 a[216:219],a[188:191],v[220:223],a[216:219]
ds_read_b64_tr_b16 a[162:163],v1 offset:5248
v_mfma_f32_16x16x32_bf16 a[220:223],a[188:191],v[252:255],a[220:223]
ds_read_b64_tr_b16 a[164:165],v1 offset:10368
v_mfma_f32_16x16x32_bf16 a[220:223],a[184:187],v[248:251],a[220:223]
buffer_load_dwordx4 v7,s[72:75],s80 offen lds
v_mfma_f32_16x16x32_bf16 a[220:223],a[180:183],v[236:239],a[220:223]
ds_read_b64_tr_b16 a[166:167],v1 offset:15488
ds_read_b64_tr_b16 a[168:169],v1 offset:20608
v_mfma_f32_16x16x32_bf16 a[220:223],a[176:179],v[232:235],a[220:223]
s_waitcnt lgkmcnt(7)
ds_read_b64_tr_b16 a[170:171],v1 offset:25728
v_mfma_f32_16x16x32_bf16 a[224:227],a[128:131],v[136:139],a[224:227]
ds_read_b64_tr_b16 a[172:173],v1 offset:30848
v_mfma_f32_16x16x32_bf16 a[224:227],a[132:135],v[140:143],a[224:227]
buffer_load_dwordx4 v[132:135],v6,s[28:31],s77 offen sc1
v_mfma_f32_16x16x32_bf16 a[224:227],a[136:139],v[152:155],a[224:227]
ds_read_b64_tr_b16 a[174:175],v1 offset:35968
ds_read_b64_tr_b16 a[176:177],v2 offset:160
v_mfma_f32_16x16x32_bf16 a[224:227],a[140:143],v[156:159],a[224:227]
ds_read_b64_tr_b16 a[178:179],v2 offset:5280
v_mfma_f32_16x16x32_bf16 a[228:231],a[140:143],v[188:191],a[228:231]
ds_read_b64_tr_b16 a[180:181],v2 offset:10400
v_mfma_f32_16x16x32_bf16 a[228:231],a[136:139],v[184:187],a[228:231]
buffer_load_dwordx4 v[164:167],v6,s[28:31],s77 offen offset:1024 sc1
v_mfma_f32_16x16x32_bf16 a[228:231],a[132:135],v[172:175],a[228:231]
ds_read_b64_tr_b16 a[182:183],v2 offset:15520
ds_read_b64_tr_b16 a[184:185],v2 offset:20640
v_mfma_f32_16x16x32_bf16 a[228:231],a[128:131],v[168:171],a[228:231]
ds_read_b64_tr_b16 a[186:187],v2 offset:25760
v_mfma_f32_16x16x32_bf16 a[232:235],a[128:131],v[200:203],a[232:235]
ds_read_b64_tr_b16 a[188:189],v2 offset:30880
v_mfma_f32_16x16x32_bf16 a[232:235],a[132:135],v[204:207],a[232:235]
buffer_load_dwordx4 v8,s[72:75],s80 offen offset:1024 lds
v_mfma_f32_16x16x32_bf16 a[232:235],a[136:139],v[216:219],a[232:235]
ds_read_b64_tr_b16 a[190:191],v2 offset:36000
v_mfma_f32_16x16x32_bf16 a[232:235],a[140:143],v[220:223],a[232:235]
v_mfma_f32_16x16x32_bf16 a[236:239],a[140:143],v[252:255],a[236:239]
ds_read_b64_tr_b16 a[140:141],v1 offset:30912
ds_read_b64_tr_b16 a[142:143],v1 offset:36032
v_mfma_f32_16x16x32_bf16 a[236:239],a[136:139],v[248:251],a[236:239]
buffer_load_dwordx4 v[196:199],v6,s[28:31],s77 offen offset:2048 sc1
v_mfma_f32_16x16x32_bf16 a[236:239],a[132:135],v[236:239],a[236:239]
ds_read_b64_tr_b16 a[132:133],v1 offset:10432
ds_read_b64_tr_b16 a[134:135],v1 offset:15552
v_mfma_f32_16x16x32_bf16 a[236:239],a[128:131],v[232:235],a[236:239]
ds_read_b64_tr_b16 a[128:129],v1 offset:192
v_mfma_f32_16x16x32_bf16 a[240:243],a[144:147],v[136:139],a[240:243]
ds_read_b64_tr_b16 a[130:131],v1 offset:5312
v_mfma_f32_16x16x32_bf16 a[240:243],a[148:151],v[140:143],a[240:243]
buffer_store_dwordx4 v[220:223],v6,s[36:39],s86 offen offset:3072 sc1
v_mfma_f32_16x16x32_bf16 a[240:243],a[152:155],v[152:155],a[240:243]
s_waitcnt lgkmcnt(6)
ds_read_b64_tr_b16 a[136:137],v1 offset:20672
ds_read_b64_tr_b16 a[138:139],v1 offset:25792
v_mfma_f32_16x16x32_bf16 a[240:243],a[156:159],v[156:159],a[240:243]
v_mfma_f32_16x16x32_bf16 a[244:247],a[156:159],v[188:191],a[244:247]
v_mfma_f32_16x16x32_bf16 a[244:247],a[152:155],v[184:187],a[244:247]
buffer_load_dwordx4 v[228:231],v6,s[28:31],s77 offen offset:3072 sc1
v_mfma_f32_16x16x32_bf16 a[244:247],a[148:151],v[172:175],a[244:247]
v_mfma_f32_16x16x32_bf16 a[244:247],a[144:147],v[168:171],a[244:247]
v_mfma_f32_16x16x32_bf16 a[248:251],a[144:147],v[200:203],a[248:251]
v_mfma_f32_16x16x32_bf16 a[248:251],a[148:151],v[204:207],a[248:251]
buffer_load_dwordx4 v9,s[72:75],s80 offen offset:2048 lds
v_mfma_f32_16x16x32_bf16 a[248:251],a[152:155],v[216:219],a[248:251]
v_mfma_f32_16x16x32_bf16 a[248:251],a[156:159],v[220:223],a[248:251]
v_mfma_f32_16x16x32_bf16 a[252:255],a[156:159],v[252:255],a[252:255]
ds_read_b64_tr_b16 a[156:157],v2 offset:30944
ds_read_b64_tr_b16 a[158:159],v2 offset:36064
v_mfma_f32_16x16x32_bf16 a[252:255],a[152:155],v[248:251],a[252:255]
buffer_load_dwordx4 v[144:147],v6,s[28:31],s78 offen sc1
v_mfma_f32_16x16x32_bf16 a[252:255],a[148:151],v[236:239],a[252:255]
ds_read_b64_tr_b16 a[148:149],v2 offset:10464
ds_read_b64_tr_b16 a[150:151],v2 offset:15584
v_mfma_f32_16x16x32_bf16 a[252:255],a[144:147],v[232:235],a[252:255]
ds_read_b64_tr_b16 a[144:145],v2 offset:224
v_mfma_f32_16x16x32_bf16 a[0:3],a[160:163],v[136:139],a[0:3]
ds_read_b64_tr_b16 a[146:147],v2 offset:5344
v_mfma_f32_16x16x32_bf16 a[0:3],a[164:167],v[140:143],a[0:3]
buffer_load_dwordx4 v[176:179],v6,s[28:31],s78 offen offset:1024 sc1
v_mfma_f32_16x16x32_bf16 a[0:3],a[168:171],v[152:155],a[0:3]
ds_read_b64_tr_b16 a[152:153],v2 offset:20704
ds_read_b64_tr_b16 a[154:155],v2 offset:25824
v_mfma_f32_16x16x32_bf16 a[0:3],a[172:175],v[156:159],a[0:3]
v_mfma_f32_16x16x32_bf16 a[4:7],a[172:175],v[188:191],a[4:7]
v_mfma_f32_16x16x32_bf16 a[4:7],a[168:171],v[184:187],a[4:7]
buffer_load_dwordx4 v10,s[72:75],s80 offen offset:3072 lds
v_mfma_f32_16x16x32_bf16 a[4:7],a[164:167],v[172:175],a[4:7]
v_mfma_f32_16x16x32_bf16 a[4:7],a[160:163],v[168:171],a[4:7]
v_mfma_f32_16x16x32_bf16 a[8:11],a[160:163],v[200:203],a[8:11]
v_mfma_f32_16x16x32_bf16 a[8:11],a[164:167],v[204:207],a[8:11]
buffer_load_dwordx4 v[208:211],v6,s[28:31],s78 offen offset:2048 sc1
v_mfma_f32_16x16x32_bf16 a[8:11],a[168:171],v[216:219],a[8:11]
v_mfma_f32_16x16x32_bf16 a[8:11],a[172:175],v[220:223],a[8:11]
v_mfma_f32_16x16x32_bf16 a[12:15],a[172:175],v[252:255],a[12:15]
ds_read_b64_tr_b16 a[172:173],v1 offset:30976
ds_read_b64_tr_b16 a[174:175],v1 offset:36096
v_mfma_f32_16x16x32_bf16 a[12:15],a[168:171],v[248:251],a[12:15]
buffer_store_dwordx4 v[232:235],v6,s[36:39],s87 offen sc1
v_mfma_f32_16x16x32_bf16 a[12:15],a[164:167],v[236:239],a[12:15]
s_add_u32 m0,s82,86016
ds_read_b64_tr_b16 a[164:165],v1 offset:10496
ds_read_b64_tr_b16 a[166:167],v1 offset:15616
v_mfma_f32_16x16x32_bf16 a[12:15],a[160:163],v[232:235],a[12:15]
ds_read_b64_tr_b16 a[160:161],v1 offset:256
v_mfma_f32_16x16x32_bf16 a[16:19],a[176:179],v[136:139],a[16:19]
ds_read_b64_tr_b16 a[162:163],v1 offset:5376
v_mfma_f32_16x16x32_bf16 a[16:19],a[180:183],v[140:143],a[16:19]
buffer_load_dwordx4 v[240:243],v6,s[28:31],s78 offen offset:3072 sc1
v_mfma_f32_16x16x32_bf16 a[16:19],a[184:187],v[152:155],a[16:19]
ds_read_b64_tr_b16 a[168:169],v1 offset:20736
ds_read_b64_tr_b16 a[170:171],v1 offset:25856
v_mfma_f32_16x16x32_bf16 a[16:19],a[188:191],v[156:159],a[16:19]
v_add_u32 v1,40960,v1
v_mfma_f32_16x16x32_bf16 a[20:23],a[188:191],v[188:191],a[20:23]
ds_read_b64_tr_b16 a[96:97],v1 offset:128
ds_read_b64_tr_b16 a[98:99],v1 offset:5248
v_mfma_f32_16x16x32_bf16 a[20:23],a[184:187],v[184:187],a[20:23]
buffer_load_dwordx4 v11,s[72:75],s80 offen lds
v_mfma_f32_16x16x32_bf16 a[20:23],a[180:183],v[172:175],a[20:23]
ds_read_b64_tr_b16 a[100:101],v1 offset:10368
ds_read_b64_tr_b16 a[102:103],v1 offset:15488
v_mfma_f32_16x16x32_bf16 a[20:23],a[176:179],v[168:171],a[20:23]
ds_read_b64_tr_b16 a[104:105],v1 offset:20608
v_mfma_f32_16x16x32_bf16 a[24:27],a[176:179],v[200:203],a[24:27]
ds_read_b64_tr_b16 a[106:107],v1 offset:25728
v_mfma_f32_16x16x32_bf16 a[24:27],a[180:183],v[204:207],a[24:27]
buffer_load_dwordx4 v[148:151],v6,s[28:31],s79 offen sc1
v_mfma_f32_16x16x32_bf16 a[24:27],a[184:187],v[216:219],a[24:27]
ds_read_b64_tr_b16 a[108:109],v1 offset:30848
ds_read_b64_tr_b16 a[110:111],v1 offset:35968
v_mfma_f32_16x16x32_bf16 a[24:27],a[188:191],v[220:223],a[24:27]
v_mfma_f32_16x16x32_bf16 a[28:31],a[188:191],v[252:255],a[28:31]
ds_read_b64_tr_b16 a[188:189],v2 offset:31008
ds_read_b64_tr_b16 a[190:191],v2 offset:36128
v_mfma_f32_16x16x32_bf16 a[28:31],a[184:187],v[248:251],a[28:31]
s_add_u32 m0,s82,87040
buffer_load_dwordx4 v[180:183],v6,s[28:31],s79 offen offset:1024 sc1
v_mfma_f32_16x16x32_bf16 a[28:31],a[180:183],v[236:239],a[28:31]
ds_read_b64_tr_b16 a[180:181],v2 offset:10528
ds_read_b64_tr_b16 a[182:183],v2 offset:15648
v_mfma_f32_16x16x32_bf16 a[28:31],a[176:179],v[232:235],a[28:31]
s_waitcnt lgkmcnt(7)
ds_read_b64_tr_b16 a[176:177],v2 offset:288
v_mfma_f32_16x16x32_bf16 a[32:35],a[128:131],v[136:139],a[32:35]
ds_read_b64_tr_b16 a[178:179],v2 offset:5408
v_mfma_f32_16x16x32_bf16 a[32:35],a[132:135],v[140:143],a[32:35]
buffer_store_dwordx4 v[236:239],v6,s[36:39],s87 offen offset:1024 sc1
v_mfma_f32_16x16x32_bf16 a[32:35],a[136:139],v[152:155],a[32:35]
ds_read_b64_tr_b16 a[184:185],v2 offset:20768
ds_read_b64_tr_b16 a[186:187],v2 offset:25888
v_mfma_f32_16x16x32_bf16 a[32:35],a[140:143],v[156:159],a[32:35]
v_add_u32 v2,40960,v2
v_mfma_f32_16x16x32_bf16 a[36:39],a[140:143],v[188:191],a[36:39]
ds_read_b64_tr_b16 a[112:113],v2 offset:160
ds_read_b64_tr_b16 a[114:115],v2 offset:5280
v_mfma_f32_16x16x32_bf16 a[36:39],a[136:139],v[184:187],a[36:39]
)A"
R"A(buffer_load_dwordx4 v[212:215],v6,s[28:31],s79 offen offset:2048 sc1
v_mfma_f32_16x16x32_bf16 a[36:39],a[132:135],v[172:175],a[36:39]
ds_read_b64_tr_b16 a[116:117],v2 offset:10400
ds_read_b64_tr_b16 a[118:119],v2 offset:15520
v_mfma_f32_16x16x32_bf16 a[36:39],a[128:131],v[168:171],a[36:39]
ds_read_b64_tr_b16 a[120:121],v2 offset:20640
v_mfma_f32_16x16x32_bf16 a[40:43],a[128:131],v[200:203],a[40:43]
ds_read_b64_tr_b16 a[122:123],v2 offset:25760
v_mfma_f32_16x16x32_bf16 a[40:43],a[132:135],v[204:207],a[40:43]
buffer_load_dwordx4 v7,s[72:75],s83 offen lds
v_mfma_f32_16x16x32_bf16 a[40:43],a[136:139],v[216:219],a[40:43]
ds_read_b64_tr_b16 a[124:125],v2 offset:30880
ds_read_b64_tr_b16 a[126:127],v2 offset:36000
v_mfma_f32_16x16x32_bf16 a[40:43],a[140:143],v[220:223],a[40:43]
v_mfma_f32_16x16x32_bf16 a[44:47],a[140:143],v[252:255],a[44:47]
ds_read_b64_tr_b16 a[140:141],v1 offset:30720
ds_read_b64_tr_b16 a[142:143],v1 offset:35840
v_mfma_f32_16x16x32_bf16 a[44:47],a[136:139],v[248:251],a[44:47]
buffer_load_dwordx4 v[244:247],v6,s[28:31],s79 offen offset:3072 sc1
v_mfma_f32_16x16x32_bf16 a[44:47],a[132:135],v[236:239],a[44:47]
ds_read_b64_tr_b16 a[132:133],v1 offset:10240
ds_read_b64_tr_b16 a[134:135],v1 offset:15360
v_mfma_f32_16x16x32_bf16 a[44:47],a[128:131],v[232:235],a[44:47]
ds_read_b64_tr_b16 a[128:129],v1
v_mfma_f32_16x16x32_bf16 a[48:51],a[144:147],v[136:139],a[48:51]
ds_read_b64_tr_b16 a[130:131],v1 offset:5120
v_mfma_f32_16x16x32_bf16 a[48:51],a[148:151],v[140:143],a[48:51]
buffer_store_dwordx4 v[248:251],v6,s[36:39],s87 offen offset:2048 sc1
v_mfma_f32_16x16x32_bf16 a[48:51],a[152:155],v[152:155],a[48:51]
ds_read_b64_tr_b16 a[136:137],v1 offset:20480
ds_read_b64_tr_b16 a[138:139],v1 offset:25600
v_mfma_f32_16x16x32_bf16 a[48:51],a[156:159],v[156:159],a[48:51]
s_cmp_ge_u32 1,s71
s_cselect_b32 s76,4,2
s_lshl_b32 s76,s76,16
v_mfma_f32_16x16x32_bf16 a[52:55],a[156:159],v[188:191],a[52:55]
s_add_u32 s77,s76,4096
s_add_u32 s78,s76,8192
s_add_u32 s79,s76,12288
v_mfma_f32_16x16x32_bf16 a[52:55],a[152:155],v[184:187],a[52:55]
s_max_i32 s76,s76,s18
buffer_load_dwordx4 v8,s[72:75],s83 offen offset:1024 lds
v_mfma_f32_16x16x32_bf16 a[52:55],a[148:151],v[172:175],a[52:55]
s_max_i32 s77,s77,s19
s_max_i32 s78,s78,s100
s_max_i32 s79,s79,s101
v_mfma_f32_16x16x32_bf16 a[52:55],a[144:147],v[168:171],a[52:55]
v_mfma_f32_16x16x32_bf16 a[56:59],a[144:147],v[200:203],a[56:59]
v_mfma_f32_16x16x32_bf16 a[56:59],a[148:151],v[204:207],a[56:59]
buffer_store_dwordx4 v[252:255],v6,s[36:39],s87 offen offset:3072 sc1
v_mfma_f32_16x16x32_bf16 a[56:59],a[152:155],v[216:219],a[56:59]
v_mfma_f32_16x16x32_bf16 a[56:59],a[156:159],v[220:223],a[56:59]
v_mfma_f32_16x16x32_bf16 a[60:63],a[156:159],v[252:255],a[60:63]
ds_read_b64_tr_b16 a[156:157],v2 offset:30752
ds_read_b64_tr_b16 a[158:159],v2 offset:35872
v_mfma_f32_16x16x32_bf16 a[60:63],a[152:155],v[248:251],a[60:63]
ds_read_b64_tr_b16 a[152:153],v2 offset:20512
v_mfma_f32_16x16x32_bf16 a[60:63],a[148:151],v[236:239],a[60:63]
buffer_load_dwordx4 v9,s[72:75],s83 offen offset:2048 lds
v_mfma_f32_16x16x32_bf16 a[60:63],a[144:147],v[232:235],a[60:63]
ds_read_b64_tr_b16 a[144:145],v2 offset:32
ds_read_b64_tr_b16 a[146:147],v2 offset:5152
v_mfma_f32_16x16x32_bf16 a[64:67],a[160:163],v[136:139],a[64:67]
ds_read_b64_tr_b16 a[148:149],v2 offset:10272
v_mfma_f32_16x16x32_bf16 a[64:67],a[164:167],v[140:143],a[64:67]
ds_read_b64_tr_b16 a[150:151],v2 offset:15392
v_mfma_f32_16x16x32_bf16 a[64:67],a[168:171],v[152:155],a[64:67]
ds_read_b64_tr_b16 a[154:155],v2 offset:25632
v_mfma_f32_16x16x32_bf16 a[64:67],a[172:175],v[156:159],a[64:67]
v_mfma_f32_16x16x32_bf16 a[68:71],a[172:175],v[188:191],a[68:71]
v_mfma_f32_16x16x32_bf16 a[68:71],a[168:171],v[184:187],a[68:71]
v_mfma_f32_16x16x32_bf16 a[68:71],a[164:167],v[172:175],a[68:71]
v_mfma_f32_16x16x32_bf16 a[68:71],a[160:163],v[168:171],a[68:71]
v_mfma_f32_16x16x32_bf16 a[72:75],a[160:163],v[200:203],a[72:75]
v_mfma_f32_16x16x32_bf16 a[72:75],a[164:167],v[204:207],a[72:75]
v_mfma_f32_16x16x32_bf16 a[72:75],a[168:171],v[216:219],a[72:75]
v_mfma_f32_16x16x32_bf16 a[72:75],a[172:175],v[220:223],a[72:75]
buffer_load_dwordx4 v[64:67],v6,s[28:31],s76 offen sc1
v_mfma_f32_16x16x32_bf16 a[76:79],a[172:175],v[252:255],a[76:79]
ds_read_b64_tr_b16 a[172:173],v1 offset:30784
ds_read_b64_tr_b16 a[174:175],v1 offset:35904
v_mfma_f32_16x16x32_bf16 a[76:79],a[168:171],v[248:251],a[76:79]
ds_read_b64_tr_b16 a[168:169],v1 offset:20544
v_mfma_f32_16x16x32_bf16 a[76:79],a[164:167],v[236:239],a[76:79]
ds_read_b64_tr_b16 a[164:165],v1 offset:10304
v_mfma_f32_16x16x32_bf16 a[76:79],a[160:163],v[232:235],a[76:79]
s_waitcnt lgkmcnt(4)
buffer_load_dwordx4 v10,s[72:75],s83 offen offset:3072 lds
v_mfma_f32_16x16x32_bf16 a[80:83],a[176:179],v[136:139],a[80:83]
ds_read_b64_tr_b16 a[160:161],v1 offset:64
ds_read_b64_tr_b16 a[162:163],v1 offset:5184
v_mfma_f32_16x16x32_bf16 a[80:83],a[180:183],v[140:143],a[80:83]
ds_read_b64_tr_b16 a[166:167],v1 offset:15424
v_mfma_f32_16x16x32_bf16 a[80:83],a[184:187],v[152:155],a[80:83]
ds_read_b64_tr_b16 a[170:171],v1 offset:25664
v_mfma_f32_16x16x32_bf16 a[80:83],a[188:191],v[156:159],a[80:83]
buffer_load_dwordx4 v[68:71],v6,s[28:31],s76 offen offset:1024 sc1
v_mfma_f32_16x16x32_bf16 a[84:87],a[188:191],v[188:191],a[84:87]
v_mfma_f32_16x16x32_bf16 a[84:87],a[184:187],v[184:187],a[84:87]
v_mfma_f32_16x16x32_bf16 a[84:87],a[180:183],v[172:175],a[84:87]
v_mfma_f32_16x16x32_bf16 a[84:87],a[176:179],v[168:171],a[84:87]
v_mfma_f32_16x16x32_bf16 a[88:91],a[176:179],v[200:203],a[88:91]
v_mfma_f32_16x16x32_bf16 a[88:91],a[180:183],v[204:207],a[88:91]
s_add_u32 m0,s82,91136
v_mfma_f32_16x16x32_bf16 a[88:91],a[184:187],v[216:219],a[88:91]
v_mfma_f32_16x16x32_bf16 a[88:91],a[188:191],v[220:223],a[88:91]
buffer_load_dwordx4 v[72:75],v6,s[28:31],s76 offen offset:2048 sc1
v_mfma_f32_16x16x32_bf16 a[92:95],a[188:191],v[252:255],a[92:95]
ds_read_b64_tr_b16 a[188:189],v2 offset:30816
ds_read_b64_tr_b16 a[190:191],v2 offset:35936
v_mfma_f32_16x16x32_bf16 a[92:95],a[184:187],v[248:251],a[92:95]
ds_read_b64_tr_b16 a[184:185],v2 offset:20576
v_mfma_f32_16x16x32_bf16 a[92:95],a[180:183],v[236:239],a[92:95]
s_cmp_ge_u32 0,s71
s_cselect_b32 s80,3,1
s_mul_i32 s80,s80,163840
v_mfma_f32_16x16x32_bf16 a[92:95],a[176:179],v[232:235],a[92:95]
s_add_u32 s80,s80,s81
buffer_load_dwordx4 v11,s[72:75],s83 offen lds
s_waitcnt vmcnt(14)
v_mfma_f32_16x16x32_bf16 a[192:195],a[128:131],v[128:131],a[192:195]
s_add_u32 s83,s80,20480
ds_read_b64_tr_b16 a[176:177],v2 offset:96
ds_read_b64_tr_b16 a[178:179],v2 offset:5216
v_mfma_f32_16x16x32_bf16 a[192:195],a[132:135],v[160:163],a[192:195]
ds_read_b64_tr_b16 a[180:181],v2 offset:10336
v_mfma_f32_16x16x32_bf16 a[192:195],a[136:139],v[192:195],a[192:195]
ds_read_b64_tr_b16 a[182:183],v2 offset:15456
v_mfma_f32_16x16x32_bf16 a[192:195],a[140:143],v[224:227],a[192:195]
buffer_load_dwordx4 v[76:79],v6,s[28:31],s76 offen offset:3072 sc1
v_mfma_f32_16x16x32_bf16 a[196:199],a[140:143],v[228:231],a[196:199]
ds_read_b64_tr_b16 a[186:187],v2 offset:25696
v_mfma_f32_16x16x32_bf16 a[196:199],a[136:139],v[196:199],a[196:199]
v_mfma_f32_16x16x32_bf16 a[196:199],a[132:135],v[164:167],a[196:199]
v_mfma_f32_16x16x32_bf16 a[196:199],a[128:131],v[132:135],a[196:199]
v_mfma_f32_16x16x32_bf16 a[200:203],a[128:131],v[144:147],a[200:203]
v_mfma_f32_16x16x32_bf16 a[200:203],a[132:135],v[176:179],a[200:203]
v_mfma_f32_16x16x32_bf16 a[200:203],a[136:139],v[208:211],a[200:203]
v_mfma_f32_16x16x32_bf16 a[200:203],a[140:143],v[240:243],a[200:203]
s_lshl_b32 s95,s42,2
buffer_load_dwordx4 v[80:83],v6,s[28:31],s77 offen sc1
s_waitcnt vmcnt(11)
v_mfma_f32_16x16x32_bf16 a[204:207],a[140:143],v[244:247],a[204:207]
s_add_u32 s95,s95,s40
s_lshl_b32 s95,s95,4
v_mov_b32 v18,s68
v_mfma_f32_16x16x32_bf16 a[204:207],a[136:139],v[212:215],a[204:207]
ds_read_b64_tr_b16 a[136:137],v1 offset:20672
v_mov_b32 v19,s69
v_mfma_f32_16x16x32_bf16 a[204:207],a[132:135],v[180:183],a[204:207]
ds_read_b64_tr_b16 a[132:133],v1 offset:10432
ds_read_b64_tr_b16 a[134:135],v1 offset:15552
v_mfma_f32_16x16x32_bf16 a[204:207],a[128:131],v[148:151],a[204:207]
s_barrier
ds_read_b64_tr_b16 a[128:129],v1 offset:192
ds_read_b64_tr_b16 a[130:131],v1 offset:5312
v_mfma_f32_16x16x32_bf16 a[208:211],a[144:147],v[128:131],a[208:211]
ds_read_b64_tr_b16 a[138:139],v1 offset:25792
v_mfma_f32_16x16x32_bf16 a[208:211],a[148:151],v[160:163],a[208:211]
ds_read_b64_tr_b16 a[140:141],v1 offset:30912
v_mfma_f32_16x16x32_bf16 a[208:211],a[152:155],v[192:195],a[208:211]
s_add_u32 m0,s82,0
s_waitcnt vmcnt(8)
s_mov_b64 exec,1
buffer_store_dwordx2 v[18:19],v6,s[64:67],s95 offen offset:1024 sc1
s_mov_b64 exec,-1
v_mfma_f32_16x16x32_bf16 a[208:211],a[156:159],v[224:227],a[208:211]
ds_read_b64_tr_b16 a[142:143],v1 offset:36032
v_mfma_f32_16x16x32_bf16 a[212:215],a[156:159],v[228:231],a[212:215]
v_mfma_f32_16x16x32_bf16 a[212:215],a[152:155],v[196:199],a[212:215]
v_mfma_f32_16x16x32_bf16 a[212:215],a[148:151],v[164:167],a[212:215]
buffer_load_dwordx4 v[84:87],v6,s[28:31],s77 offen offset:1024 sc1
v_mfma_f32_16x16x32_bf16 a[212:215],a[144:147],v[132:135],a[212:215]
v_mfma_f32_16x16x32_bf16 a[216:219],a[144:147],v[144:147],a[216:219]
v_mfma_f32_16x16x32_bf16 a[216:219],a[148:151],v[176:179],a[216:219]
v_mfma_f32_16x16x32_bf16 a[216:219],a[152:155],v[208:211],a[216:219]
buffer_load_dwordx4 v7,s[72:75],s80 offen lds
v_mfma_f32_16x16x32_bf16 a[216:219],a[156:159],v[240:243],a[216:219]
v_mfma_f32_16x16x32_bf16 a[220:223],a[156:159],v[244:247],a[220:223]
ds_read_b64_tr_b16 a[156:157],v2 offset:30944
ds_read_b64_tr_b16 a[158:159],v2 offset:36064
v_mfma_f32_16x16x32_bf16 a[220:223],a[152:155],v[212:215],a[220:223]
ds_read_b64_tr_b16 a[152:153],v2 offset:20704
v_mfma_f32_16x16x32_bf16 a[220:223],a[148:151],v[180:183],a[220:223]
buffer_load_dwordx4 v[88:91],v6,s[28:31],s77 offen offset:2048 sc1
v_mfma_f32_16x16x32_bf16 a[220:223],a[144:147],v[148:151],a[220:223]
s_waitcnt lgkmcnt(3)
ds_read_b64_tr_b16 a[144:145],v2 offset:224
ds_read_b64_tr_b16 a[146:147],v2 offset:5344
v_mfma_f32_16x16x32_bf16 a[224:227],a[160:163],v[128:131],a[224:227]
ds_read_b64_tr_b16 a[148:149],v2 offset:10464
v_mfma_f32_16x16x32_bf16 a[224:227],a[164:167],v[160:163],a[224:227]
ds_read_b64_tr_b16 a[150:151],v2 offset:15584
v_mfma_f32_16x16x32_bf16 a[224:227],a[168:171],v[192:195],a[224:227]
buffer_load_dwordx4 v[92:95],v6,s[28:31],s77 offen offset:3072 sc1
v_mfma_f32_16x16x32_bf16 a[224:227],a[172:175],v[224:227],a[224:227]
ds_read_b64_tr_b16 a[154:155],v2 offset:25824
v_mfma_f32_16x16x32_bf16 a[228:231],a[172:175],v[228:231],a[228:231]
v_mfma_f32_16x16x32_bf16 a[228:231],a[168:171],v[196:199],a[228:231]
v_mfma_f32_16x16x32_bf16 a[228:231],a[164:167],v[164:167],a[228:231]
buffer_load_dwordx4 v8,s[72:75],s80 offen offset:1024 lds
v_mfma_f32_16x16x32_bf16 a[228:231],a[160:163],v[132:135],a[228:231]
v_mfma_f32_16x16x32_bf16 a[232:235],a[160:163],v[144:147],a[232:235]
v_mfma_f32_16x16x32_bf16 a[232:235],a[164:167],v[176:179],a[232:235]
v_mfma_f32_16x16x32_bf16 a[232:235],a[168:171],v[208:211],a[232:235]
buffer_load_dwordx4 v[96:99],v6,s[28:31],s78 offen sc1
v_mfma_f32_16x16x32_bf16 a[232:235],a[172:175],v[240:243],a[232:235]
v_mfma_f32_16x16x32_bf16 a[236:239],a[172:175],v[244:247],a[236:239]
ds_read_b64_tr_b16 a[172:173],v1 offset:30976
ds_read_b64_tr_b16 a[174:175],v1 offset:36096
v_mfma_f32_16x16x32_bf16 a[236:239],a[168:171],v[212:215],a[236:239]
ds_read_b64_tr_b16 a[168:169],v1 offset:20736
v_mfma_f32_16x16x32_bf16 a[236:239],a[164:167],v[180:183],a[236:239]
ds_read_b64_tr_b16 a[164:165],v1 offset:10496
v_mfma_f32_16x16x32_bf16 a[236:239],a[160:163],v[148:151],a[236:239]
ds_read_b64_tr_b16 a[160:161],v1 offset:256
v_mfma_f32_16x16x32_bf16 a[240:243],a[176:179],v[128:131],a[240:243]
ds_read_b64_tr_b16 a[162:163],v1 offset:5376
ds_read_b64_tr_b16 a[166:167],v1 offset:15616
v_mfma_f32_16x16x32_bf16 a[240:243],a[180:183],v[160:163],a[240:243]
ds_read_b64_tr_b16 a[170:171],v1 offset:25856
v_add_u32 v1,81920,v1
v_mfma_f32_16x16x32_bf16 a[240:243],a[184:187],v[192:195],a[240:243]
)A"
R"A(buffer_load_dwordx4 v[100:103],v6,s[28:31],s78 offen offset:1024 sc1
v_mfma_f32_16x16x32_bf16 a[240:243],a[188:191],v[224:227],a[240:243]
v_mfma_f32_16x16x32_bf16 a[244:247],a[188:191],v[228:231],a[244:247]
v_mfma_f32_16x16x32_bf16 a[244:247],a[184:187],v[196:199],a[244:247]
v_mfma_f32_16x16x32_bf16 a[244:247],a[180:183],v[164:167],a[244:247]
buffer_load_dwordx4 v9,s[72:75],s80 offen offset:2048 lds
v_mfma_f32_16x16x32_bf16 a[244:247],a[176:179],v[132:135],a[244:247]
v_mfma_f32_16x16x32_bf16 a[248:251],a[176:179],v[144:147],a[248:251]
v_mfma_f32_16x16x32_bf16 a[248:251],a[180:183],v[176:179],a[248:251]
v_mfma_f32_16x16x32_bf16 a[248:251],a[184:187],v[208:211],a[248:251]
buffer_load_dwordx4 v[104:107],v6,s[28:31],s78 offen offset:2048 sc1
v_mfma_f32_16x16x32_bf16 a[248:251],a[188:191],v[240:243],a[248:251]
v_mfma_f32_16x16x32_bf16 a[252:255],a[188:191],v[244:247],a[252:255]
ds_read_b64_tr_b16 a[188:189],v2 offset:31008
ds_read_b64_tr_b16 a[190:191],v2 offset:36128
v_mfma_f32_16x16x32_bf16 a[252:255],a[184:187],v[212:215],a[252:255]
ds_read_b64_tr_b16 a[184:185],v2 offset:20768
v_mfma_f32_16x16x32_bf16 a[252:255],a[180:183],v[180:183],a[252:255]
ds_read_b64_tr_b16 a[180:181],v2 offset:10528
v_mfma_f32_16x16x32_bf16 a[252:255],a[176:179],v[148:151],a[252:255]
ds_read_b64_tr_b16 a[176:177],v2 offset:288
v_mfma_f32_16x16x32_bf16 a[0:3],a[96:99],v[128:131],a[0:3]
ds_read_b64_tr_b16 a[178:179],v2 offset:5408
ds_read_b64_tr_b16 a[182:183],v2 offset:15648
v_mfma_f32_16x16x32_bf16 a[0:3],a[100:103],v[160:163],a[0:3]
ds_read_b64_tr_b16 a[186:187],v2 offset:25888
v_add_u32 v2,81920,v2
v_mfma_f32_16x16x32_bf16 a[0:3],a[104:107],v[192:195],a[0:3]
buffer_load_dwordx4 v[108:111],v6,s[28:31],s78 offen offset:3072 sc1
v_mfma_f32_16x16x32_bf16 a[0:3],a[108:111],v[224:227],a[0:3]
v_mfma_f32_16x16x32_bf16 a[4:7],a[108:111],v[228:231],a[4:7]
v_mfma_f32_16x16x32_bf16 a[4:7],a[104:107],v[196:199],a[4:7]
v_mfma_f32_16x16x32_bf16 a[4:7],a[100:103],v[164:167],a[4:7]
buffer_load_dwordx4 v10,s[72:75],s80 offen offset:3072 lds
v_mfma_f32_16x16x32_bf16 a[4:7],a[96:99],v[132:135],a[4:7]
v_mfma_f32_16x16x32_bf16 a[8:11],a[96:99],v[144:147],a[8:11]
v_mfma_f32_16x16x32_bf16 a[8:11],a[100:103],v[176:179],a[8:11]
v_mfma_f32_16x16x32_bf16 a[8:11],a[104:107],v[208:211],a[8:11]
buffer_load_dwordx4 v[112:115],v6,s[28:31],s79 offen sc1
v_mfma_f32_16x16x32_bf16 a[8:11],a[108:111],v[240:243],a[8:11]
v_mfma_f32_16x16x32_bf16 a[12:15],a[108:111],v[244:247],a[12:15]
ds_read_b64_tr_b16 a[108:109],v1 offset:30720
ds_read_b64_tr_b16 a[110:111],v1 offset:35840
v_mfma_f32_16x16x32_bf16 a[12:15],a[104:107],v[212:215],a[12:15]
ds_read_b64_tr_b16 a[104:105],v1 offset:20480
v_mfma_f32_16x16x32_bf16 a[12:15],a[100:103],v[180:183],a[12:15]
s_add_u32 m0,s82,4096
ds_read_b64_tr_b16 a[100:101],v1 offset:10240
v_mfma_f32_16x16x32_bf16 a[12:15],a[96:99],v[148:151],a[12:15]
ds_read_b64_tr_b16 a[96:97],v1
v_mfma_f32_16x16x32_bf16 a[16:19],a[112:115],v[128:131],a[16:19]
ds_read_b64_tr_b16 a[98:99],v1 offset:5120
ds_read_b64_tr_b16 a[102:103],v1 offset:15360
v_mfma_f32_16x16x32_bf16 a[16:19],a[116:119],v[160:163],a[16:19]
buffer_load_dwordx4 v11,s[72:75],s80 offen lds
v_mfma_f32_16x16x32_bf16 a[16:19],a[120:123],v[192:195],a[16:19]
ds_read_b64_tr_b16 a[106:107],v1 offset:25600
v_mfma_f32_16x16x32_bf16 a[16:19],a[124:127],v[224:227],a[16:19]
v_mfma_f32_16x16x32_bf16 a[20:23],a[124:127],v[228:231],a[20:23]
v_mfma_f32_16x16x32_bf16 a[20:23],a[120:123],v[196:199],a[20:23]
buffer_load_dwordx4 v[116:119],v6,s[28:31],s79 offen offset:1024 sc1
v_mfma_f32_16x16x32_bf16 a[20:23],a[116:119],v[164:167],a[20:23]
v_mfma_f32_16x16x32_bf16 a[20:23],a[112:115],v[132:135],a[20:23]
v_mfma_f32_16x16x32_bf16 a[24:27],a[112:115],v[144:147],a[24:27]
v_mfma_f32_16x16x32_bf16 a[24:27],a[116:119],v[176:179],a[24:27]
v_mfma_f32_16x16x32_bf16 a[24:27],a[120:123],v[208:211],a[24:27]
buffer_load_dwordx4 v[120:123],v6,s[28:31],s79 offen offset:2048 sc1
v_mfma_f32_16x16x32_bf16 a[24:27],a[124:127],v[240:243],a[24:27]
v_mfma_f32_16x16x32_bf16 a[28:31],a[124:127],v[244:247],a[28:31]
ds_read_b64_tr_b16 a[124:125],v2 offset:30752
ds_read_b64_tr_b16 a[126:127],v2 offset:35872
v_mfma_f32_16x16x32_bf16 a[28:31],a[120:123],v[212:215],a[28:31]
s_add_u32 m0,s82,5120
ds_read_b64_tr_b16 a[120:121],v2 offset:20512
v_mfma_f32_16x16x32_bf16 a[28:31],a[116:119],v[180:183],a[28:31]
ds_read_b64_tr_b16 a[116:117],v2 offset:10272
v_mfma_f32_16x16x32_bf16 a[28:31],a[112:115],v[148:151],a[28:31]
s_cmp_ge_u32 2,s71
s_cselect_b32 s76,6,4
s_lshl_b32 s76,s76,16
v_mfma_f32_16x16x32_bf16 a[32:35],a[128:131],v[128:131],a[32:35]
s_add_u32 s77,s76,4096
buffer_load_dwordx4 v7,s[72:75],s83 offen lds
v_mfma_f32_16x16x32_bf16 a[32:35],a[132:135],v[160:163],a[32:35]
s_add_u32 s78,s76,8192
s_max_i32 s77,s77,s19
ds_read_b64_tr_b16 a[112:113],v2 offset:32
v_mfma_f32_16x16x32_bf16 a[32:35],a[136:139],v[192:195],a[32:35]
s_max_i32 s78,s78,s100
ds_read_b64_tr_b16 a[114:115],v2 offset:5152
ds_read_b64_tr_b16 a[118:119],v2 offset:15392
v_mfma_f32_16x16x32_bf16 a[32:35],a[140:143],v[224:227],a[32:35]
ds_read_b64_tr_b16 a[122:123],v2 offset:25632
v_mfma_f32_16x16x32_bf16 a[36:39],a[140:143],v[228:231],a[36:39]
buffer_load_dwordx4 v[124:127],v6,s[28:31],s79 offen offset:3072 sc1
v_mfma_f32_16x16x32_bf16 a[36:39],a[136:139],v[196:199],a[36:39]
s_add_u32 s79,s76,12288
s_max_i32 s76,s76,s18
s_max_i32 s79,s79,s101
v_mfma_f32_16x16x32_bf16 a[36:39],a[132:135],v[164:167],a[36:39]
v_mfma_f32_16x16x32_bf16 a[36:39],a[128:131],v[132:135],a[36:39]
v_mfma_f32_16x16x32_bf16 a[40:43],a[128:131],v[144:147],a[40:43]
v_mfma_f32_16x16x32_bf16 a[40:43],a[132:135],v[176:179],a[40:43]
v_mfma_f32_16x16x32_bf16 a[40:43],a[136:139],v[208:211],a[40:43]
v_mfma_f32_16x16x32_bf16 a[40:43],a[140:143],v[240:243],a[40:43]
v_mfma_f32_16x16x32_bf16 a[44:47],a[140:143],v[244:247],a[44:47]
ds_read_b64_tr_b16 a[140:141],v1 offset:30784
ds_read_b64_tr_b16 a[142:143],v1 offset:35904
v_mfma_f32_16x16x32_bf16 a[44:47],a[136:139],v[212:215],a[44:47]
ds_read_b64_tr_b16 a[136:137],v1 offset:20544
v_mfma_f32_16x16x32_bf16 a[44:47],a[132:135],v[180:183],a[44:47]
ds_read_b64_tr_b16 a[132:133],v1 offset:10304
v_mfma_f32_16x16x32_bf16 a[44:47],a[128:131],v[148:151],a[44:47]
s_waitcnt lgkmcnt(4)
buffer_load_dwordx4 v8,s[72:75],s83 offen offset:1024 lds
v_mfma_f32_16x16x32_bf16 a[48:51],a[144:147],v[128:131],a[48:51]
ds_read_b64_tr_b16 a[128:129],v1 offset:64
ds_read_b64_tr_b16 a[130:131],v1 offset:5184
v_mfma_f32_16x16x32_bf16 a[48:51],a[148:151],v[160:163],a[48:51]
ds_read_b64_tr_b16 a[134:135],v1 offset:15424
v_mfma_f32_16x16x32_bf16 a[48:51],a[152:155],v[192:195],a[48:51]
ds_read_b64_tr_b16 a[138:139],v1 offset:25664
v_mfma_f32_16x16x32_bf16 a[48:51],a[156:159],v[224:227],a[48:51]
s_lshl_b32 s94,s70,4
v_bfe_u32 v24,v6,4,2
v_min_u32 v24,2,v24
v_mfma_f32_16x16x32_bf16 a[52:55],a[156:159],v[228:231],a[52:55]
s_add_u32 s94,s94,s40
v_add_u32 v24,s71,v24
v_add_u32 v24,1,v24
v_mfma_f32_16x16x32_bf16 a[52:55],a[152:155],v[196:199],a[52:55]
s_lshl_b32 s94,s94,4
v_and_b32 v24,3,v24
v_lshlrev_b32 v24,6,v24
v_mfma_f32_16x16x32_bf16 a[52:55],a[148:151],v[164:167],a[52:55]
v_mfma_f32_16x16x32_bf16 a[52:55],a[144:147],v[132:135],a[52:55]
buffer_load_dwordx4 v[136:139],v6,s[28:31],s76 offen sc1
v_mfma_f32_16x16x32_bf16 a[56:59],a[144:147],v[144:147],a[56:59]
v_mfma_f32_16x16x32_bf16 a[56:59],a[148:151],v[176:179],a[56:59]
v_mfma_f32_16x16x32_bf16 a[56:59],a[152:155],v[208:211],a[56:59]
v_mfma_f32_16x16x32_bf16 a[56:59],a[156:159],v[240:243],a[56:59]
v_mfma_f32_16x16x32_bf16 a[60:63],a[156:159],v[244:247],a[60:63]
ds_read_b64_tr_b16 a[156:157],v2 offset:30816
ds_read_b64_tr_b16 a[158:159],v2 offset:35936
v_mfma_f32_16x16x32_bf16 a[60:63],a[152:155],v[212:215],a[60:63]
buffer_load_dwordx4 v[140:143],v6,s[28:31],s76 offen offset:1024 sc1
v_mfma_f32_16x16x32_bf16 a[60:63],a[148:151],v[180:183],a[60:63]
ds_read_b64_tr_b16 a[148:149],v2 offset:10336
ds_read_b64_tr_b16 a[150:151],v2 offset:15456
v_mfma_f32_16x16x32_bf16 a[60:63],a[144:147],v[148:151],a[60:63]
ds_read_b64_tr_b16 a[144:145],v2 offset:96
v_mfma_f32_16x16x32_bf16 a[64:67],a[160:163],v[128:131],a[64:67]
ds_read_b64_tr_b16 a[146:147],v2 offset:5216
v_mfma_f32_16x16x32_bf16 a[64:67],a[164:167],v[160:163],a[64:67]
buffer_load_dwordx4 v9,s[72:75],s83 offen offset:2048 lds
v_mfma_f32_16x16x32_bf16 a[64:67],a[168:171],v[192:195],a[64:67]
ds_read_b64_tr_b16 a[152:153],v2 offset:20576
ds_read_b64_tr_b16 a[154:155],v2 offset:25696
v_mfma_f32_16x16x32_bf16 a[64:67],a[172:175],v[224:227],a[64:67]
v_mfma_f32_16x16x32_bf16 a[68:71],a[172:175],v[228:231],a[68:71]
v_mfma_f32_16x16x32_bf16 a[68:71],a[168:171],v[196:199],a[68:71]
buffer_load_dwordx4 v[152:155],v6,s[28:31],s76 offen offset:2048 sc1
v_mfma_f32_16x16x32_bf16 a[68:71],a[164:167],v[164:167],a[68:71]
v_mfma_f32_16x16x32_bf16 a[68:71],a[160:163],v[132:135],a[68:71]
v_mfma_f32_16x16x32_bf16 a[72:75],a[160:163],v[144:147],a[72:75]
v_mfma_f32_16x16x32_bf16 a[72:75],a[164:167],v[176:179],a[72:75]
buffer_load_dwordx4 v[156:159],v6,s[28:31],s76 offen offset:3072 sc1
v_mfma_f32_16x16x32_bf16 a[72:75],a[168:171],v[208:211],a[72:75]
v_mfma_f32_16x16x32_bf16 a[72:75],a[172:175],v[240:243],a[72:75]
v_mfma_f32_16x16x32_bf16 a[76:79],a[172:175],v[244:247],a[76:79]
ds_read_b64_tr_b16 a[172:173],v1 offset:30848
ds_read_b64_tr_b16 a[174:175],v1 offset:35968
v_mfma_f32_16x16x32_bf16 a[76:79],a[168:171],v[212:215],a[76:79]
buffer_load_dwordx4 v10,s[72:75],s83 offen offset:3072 lds
v_mfma_f32_16x16x32_bf16 a[76:79],a[164:167],v[180:183],a[76:79]
ds_read_b64_tr_b16 a[164:165],v1 offset:10368
ds_read_b64_tr_b16 a[166:167],v1 offset:15488
v_mfma_f32_16x16x32_bf16 a[76:79],a[160:163],v[148:151],a[76:79]
ds_read_b64_tr_b16 a[160:161],v1 offset:128
v_mfma_f32_16x16x32_bf16 a[80:83],a[176:179],v[128:131],a[80:83]
ds_read_b64_tr_b16 a[162:163],v1 offset:5248
v_mfma_f32_16x16x32_bf16 a[80:83],a[180:183],v[160:163],a[80:83]
buffer_load_dwordx4 v[168:171],v6,s[28:31],s77 offen sc1
v_mfma_f32_16x16x32_bf16 a[80:83],a[184:187],v[192:195],a[80:83]
ds_read_b64_tr_b16 a[168:169],v1 offset:20608
ds_read_b64_tr_b16 a[170:171],v1 offset:25728
v_mfma_f32_16x16x32_bf16 a[80:83],a[188:191],v[224:227],a[80:83]
v_mfma_f32_16x16x32_bf16 a[84:87],a[188:191],v[228:231],a[84:87]
v_mfma_f32_16x16x32_bf16 a[84:87],a[184:187],v[196:199],a[84:87]
buffer_load_dwordx4 v[172:175],v6,s[28:31],s77 offen offset:1024 sc1
v_mfma_f32_16x16x32_bf16 a[84:87],a[180:183],v[164:167],a[84:87]
v_mfma_f32_16x16x32_bf16 a[84:87],a[176:179],v[132:135],a[84:87]
v_mfma_f32_16x16x32_bf16 a[88:91],a[176:179],v[144:147],a[88:91]
v_mfma_f32_16x16x32_bf16 a[88:91],a[180:183],v[176:179],a[88:91]
s_add_u32 m0,s82,9216
v_mfma_f32_16x16x32_bf16 a[88:91],a[184:187],v[208:211],a[88:91]
v_mfma_f32_16x16x32_bf16 a[88:91],a[188:191],v[240:243],a[88:91]
buffer_load_dwordx2 v[26:27],v24,s[64:67],s94 offen offset:1024 sc1
v_mfma_f32_16x16x32_bf16 a[92:95],a[188:191],v[244:247],a[92:95]
ds_read_b64_tr_b16 a[188:189],v2 offset:30880
ds_read_b64_tr_b16 a[190:191],v2 offset:36000
v_mfma_f32_16x16x32_bf16 a[92:95],a[184:187],v[212:215],a[92:95]
ds_read_b64_tr_b16 a[184:185],v2 offset:20640
v_mfma_f32_16x16x32_bf16 a[92:95],a[180:183],v[180:183],a[92:95]
ds_read_b64_tr_b16 a[180:181],v2 offset:10400
v_mfma_f32_16x16x32_bf16 a[92:95],a[176:179],v[148:151],a[92:95]
s_waitcnt vmcnt(13)
buffer_load_dwordx4 v[184:187],v6,s[28:31],s77 offen offset:2048 sc1
v_mfma_f32_16x16x32_bf16 a[192:195],a[96:99],v[64:67],a[192:195]
s_lshl_b32 s43,s41,2
ds_read_b64_tr_b16 a[176:177],v2 offset:160
ds_read_b64_tr_b16 a[178:179],v2 offset:5280
v_mfma_f32_16x16x32_bf16 a[192:195],a[100:103],v[68:71],a[192:195]
s_add_u32 s84,s70,1
s_add_u32 s85,s70,2
ds_read_b64_tr_b16 a[182:183],v2 offset:15520
v_mfma_f32_16x16x32_bf16 a[192:195],a[104:107],v[72:75],a[192:195]
s_add_u32 s86,s70,3
s_add_u32 s43,s43,s71
ds_read_b64_tr_b16 a[186:187],v2 offset:25760
v_mfma_f32_16x16x32_bf16 a[192:195],a[108:111],v[76:79],a[192:195]
s_and_b32 s84,s84,3
buffer_load_dwordx4 v11,s[72:75],s83 offen lds
v_mfma_f32_16x16x32_bf16 a[196:199],a[108:111],v[92:95],a[196:199]
)A"
R"A(s_and_b32 s85,s85,3
s_and_b32 s86,s86,3
s_mul_i32 s43,s43,245760
v_mfma_f32_16x16x32_bf16 a[196:199],a[104:107],v[88:91],a[196:199]
s_mul_i32 s84,s84,12
s_mul_i32 s85,s85,12
s_mul_i32 s86,s86,12
v_mfma_f32_16x16x32_bf16 a[196:199],a[100:103],v[84:87],a[196:199]
s_add_u32 s43,s43,134217728
s_mov_b64 s[32:33],s[12:13]
s_add_u32 s84,s84,0
v_mfma_f32_16x16x32_bf16 a[196:199],a[96:99],v[80:83],a[196:199]
s_add_u32 s85,s85,4
buffer_load_dwordx4 v[188:191],v6,s[28:31],s77 offen offset:3072 sc1
v_mfma_f32_16x16x32_bf16 a[200:203],a[96:99],v[96:99],a[200:203]
s_add_u32 s86,s86,8
s_add_u32 s84,s84,s40
s_add_u32 s85,s85,s40
v_mfma_f32_16x16x32_bf16 a[200:203],a[100:103],v[100:103],a[200:203]
s_add_u32 s86,s86,s40
s_add_u32 s32,s32,s43
s_addc_u32 s33,s33,0
v_mfma_f32_16x16x32_bf16 a[200:203],a[104:107],v[104:107],a[200:203]
s_mul_i32 s84,s84,5120
s_mul_i32 s85,s85,5120
s_mul_i32 s86,s86,5120
v_mfma_f32_16x16x32_bf16 a[200:203],a[108:111],v[108:111],a[200:203]
s_and_b32 s33,s33,0xffff
buffer_load_dwordx4 v[200:203],v6,s[28:31],s78 offen sc1
s_waitcnt vmcnt(14)
v_mfma_f32_16x16x32_bf16 a[204:207],a[108:111],v[124:127],a[204:207]
s_mov_b32 s34,245760
ds_read_b64_tr_b16 a[108:109],v1 offset:30912
ds_read_b64_tr_b16 a[110:111],v1 offset:36032
v_mfma_f32_16x16x32_bf16 a[204:207],a[104:107],v[120:123],a[204:207]
s_mov_b32 s35,0x20000
s_max_i32 s84,s84,s18
ds_read_b64_tr_b16 a[104:105],v1 offset:20672
v_mfma_f32_16x16x32_bf16 a[204:207],a[100:103],v[116:119],a[204:207]
s_max_i32 s85,s85,s19
s_max_i32 s86,s86,s100
ds_read_b64_tr_b16 a[100:101],v1 offset:10432
v_mfma_f32_16x16x32_bf16 a[204:207],a[96:99],v[112:115],a[204:207]
s_barrier
buffer_load_dwordx4 v[204:207],v6,s[28:31],s78 offen offset:1024 sc1
v_mfma_f32_16x16x32_bf16 a[208:211],a[112:115],v[64:67],a[208:211]
s_mul_i32 s44,s41,307200
ds_read_b64_tr_b16 a[96:97],v1 offset:192
ds_read_b64_tr_b16 a[98:99],v1 offset:5312
v_mfma_f32_16x16x32_bf16 a[208:211],a[116:119],v[68:71],a[208:211]
s_mov_b64 s[20:21],s[16:17]
ds_read_b64_tr_b16 a[102:103],v1 offset:15552
v_bfe_u32 v18,v6,6,2
v_mfma_f32_16x16x32_bf16 a[208:211],a[120:123],v[72:75],a[208:211]
s_lshl_b32 s43,s40,2
ds_read_b64_tr_b16 a[106:107],v1 offset:25792
v_lshrrev_b32 v19,8,v6
v_mfma_f32_16x16x32_bf16 a[208:211],a[124:127],v[76:79],a[208:211]
s_add_u32 s43,s43,s70
buffer_load_dwordx4 v[216:219],v6,s[28:31],s78 offen offset:2048 sc1
v_mfma_f32_16x16x32_bf16 a[212:215],a[124:127],v[92:95],a[212:215]
s_mul_i32 s43,s43,20480
v_xor_b32 v18,v18,v19
v_lshlrev_b32 v18,2,v18
v_mfma_f32_16x16x32_bf16 a[212:215],a[120:123],v[88:91],a[212:215]
s_mov_b32 s22,307200
v_bfe_u32 v19,v6,4,2
v_or_b32 v18,v18,v19
v_mfma_f32_16x16x32_bf16 a[212:215],a[116:119],v[84:87],a[212:215]
s_mov_b32 s23,0x20000
v_and_b32 v19,16,v6
v_mov_b32 v34,0x3f80
v_mfma_f32_16x16x32_bf16 a[212:215],a[112:115],v[80:83],a[212:215]
s_cmp_ge_u32 0,s71
s_cselect_b32 s76,3,1
s_lshl_b32 s76,s76,16
v_mfma_f32_16x16x32_bf16 a[216:219],a[112:115],v[96:99],a[216:219]
s_add_u32 s77,s76,4096
buffer_load_dwordx4 v[220:223],v6,s[28:31],s78 offen offset:3072 sc1
v_mfma_f32_16x16x32_bf16 a[216:219],a[116:119],v[100:103],a[216:219]
s_add_u32 s78,s76,8192
s_max_i32 s77,s77,s19
s_max_i32 s78,s78,s100
v_mfma_f32_16x16x32_bf16 a[216:219],a[120:123],v[104:107],a[216:219]
v_lshlrev_b32 v19,v19,v34
v_lshrrev_b32 v18,1,v18
v_mfma_f32_16x16x32_bf16 a[216:219],a[124:127],v[108:111],a[216:219]
v_cmp_eq_u32 vcc,0,v18
s_nop 1
v_cndmask_b32 v34,0,v19,vcc
v_mfma_f32_16x16x32_bf16 a[220:223],a[124:127],v[124:127],a[220:223]
ds_read_b64_tr_b16 a[124:125],v2 offset:30944
ds_read_b64_tr_b16 a[126:127],v2 offset:36064
v_mfma_f32_16x16x32_bf16 a[220:223],a[120:123],v[120:123],a[220:223]
ds_read_b64_tr_b16 a[120:121],v2 offset:20704
v_add_u32 v25,4096,v6
v_mfma_f32_16x16x32_bf16 a[220:223],a[116:119],v[116:119],a[220:223]
buffer_load_dwordx4 v[232:235],v6,s[28:31],s79 offen sc1
v_mfma_f32_16x16x32_bf16 a[220:223],a[112:115],v[112:115],a[220:223]
s_waitcnt lgkmcnt(3)
ds_read_b64_tr_b16 a[112:113],v2 offset:224
ds_read_b64_tr_b16 a[114:115],v2 offset:5344
v_mfma_f32_16x16x32_bf16 a[224:227],a[128:131],v[64:67],a[224:227]
ds_read_b64_tr_b16 a[116:117],v2 offset:10464
v_mov_b32 v36,0
v_mfma_f32_16x16x32_bf16 a[224:227],a[132:135],v[68:71],a[224:227]
ds_read_b64_tr_b16 a[118:119],v2 offset:15584
v_mov_b32 v37,0
v_mfma_f32_16x16x32_bf16 a[224:227],a[136:139],v[72:75],a[224:227]
ds_read_b64_tr_b16 a[122:123],v2 offset:25824
v_mov_b32 v32,0
v_mfma_f32_16x16x32_bf16 a[224:227],a[140:143],v[76:79],a[224:227]
v_cmp_eq_u32 vcc,1,v18
s_nop 1
v_cndmask_b32 v35,0,v19,vcc
v_mfma_f32_16x16x32_bf16 a[228:231],a[140:143],v[92:95],a[228:231]
buffer_load_dwordx4 v[236:239],v6,s[28:31],s79 offen offset:1024 sc1
v_mfma_f32_16x16x32_bf16 a[228:231],a[136:139],v[88:91],a[228:231]
v_mov_b32 v33,0
v_bfe_u32 v40,v6,4,4
v_mfma_f32_16x16x32_bf16 a[228:231],a[132:135],v[84:87],a[228:231]
v_mul_u32_u24 v40,1280,v40
v_bfe_u32 v41,v6,8,1
v_mfma_f32_16x16x32_bf16 a[228:231],a[128:131],v[80:83],a[228:231]
s_waitcnt vmcnt(9)
v_cmp_ne_u32 s[90:91],s68,v26
v_cmp_ne_u32 s[92:93],s69,v27
s_nop 0
s_or_b64 s[90:91],s[90:91],s[92:93]
s_cbranch_scc1 L_fr9_%=
L_fo9_%=:
v_mfma_f32_16x16x32_bf16 a[232:235],a[128:131],v[96:99],a[232:235]
s_add_u32 s20,s20,s44
s_addc_u32 s21,s21,0
v_lshl_add_u32 v40,v41,5,v40
v_mfma_f32_16x16x32_bf16 a[232:235],a[132:135],v[100:103],a[232:235]
s_mul_i32 s44,s71,320
s_and_b32 s21,s21,0xffff
v_lshrrev_b32 v41,9,v6
v_mfma_f32_16x16x32_bf16 a[232:235],a[136:139],v[104:107],a[232:235]
s_add_u32 s43,s43,s44
buffer_load_dwordx4 v[248:251],v6,s[28:31],s79 offen offset:2048 sc1
v_mfma_f32_16x16x32_bf16 a[232:235],a[140:143],v[108:111],a[232:235]
v_lshl_add_u32 v40,v41,4,v40
v_mfma_f32_16x16x32_bf16 a[236:239],a[140:143],v[124:127],a[236:239]
ds_read_b64_tr_b16 a[140:141],v1 offset:30976
ds_read_b64_tr_b16 a[142:143],v1 offset:36096
v_mfma_f32_16x16x32_bf16 a[236:239],a[136:139],v[120:123],a[236:239]
ds_read_b64_tr_b16 a[136:137],v1 offset:20736
v_mfma_f32_16x16x32_bf16 a[236:239],a[132:135],v[116:119],a[236:239]
ds_read_b64_tr_b16 a[132:133],v1 offset:10496
v_mfma_f32_16x16x32_bf16 a[236:239],a[128:131],v[112:115],a[236:239]
ds_read_b64_tr_b16 a[128:129],v1 offset:256
v_mfma_f32_16x16x32_bf16 a[240:243],a[144:147],v[64:67],a[240:243]
buffer_load_dwordx4 v[252:255],v6,s[28:31],s79 offen offset:3072 sc1
v_mfma_f32_16x16x32_bf16 a[240:243],a[148:151],v[68:71],a[240:243]
s_add_u32 s79,s76,12288
s_max_i32 s76,s76,s18
ds_read_b64_tr_b16 a[130:131],v1 offset:5376
v_mfma_f32_16x16x32_bf16 a[240:243],a[152:155],v[72:75],a[240:243]
s_max_i32 s79,s79,s101
ds_read_b64_tr_b16 a[134:135],v1 offset:15616
ds_read_b64_tr_b16 a[138:139],v1 offset:25856
v_mfma_f32_16x16x32_bf16 a[240:243],a[156:159],v[76:79],a[240:243]
v_subrev_u32 v1,40960,v1
v_mfma_f32_16x16x32_bf16 a[244:247],a[156:159],v[92:95],a[244:247]
buffer_load_dwordx4 v[128:131],v6,s[28:31],s76 offen sc1
v_mfma_f32_16x16x32_bf16 a[244:247],a[152:155],v[88:91],a[244:247]
v_mfma_f32_16x16x32_bf16 a[244:247],a[148:151],v[84:87],a[244:247]
v_mfma_f32_16x16x32_bf16 a[244:247],a[144:147],v[80:83],a[244:247]
v_mfma_f32_16x16x32_bf16 a[248:251],a[144:147],v[96:99],a[248:251]
buffer_load_dwordx4 v[160:163],v6,s[28:31],s76 offen offset:1024 sc1
v_mfma_f32_16x16x32_bf16 a[248:251],a[148:151],v[100:103],a[248:251]
v_mfma_f32_16x16x32_bf16 a[248:251],a[152:155],v[104:107],a[248:251]
v_mfma_f32_16x16x32_bf16 a[248:251],a[156:159],v[108:111],a[248:251]
v_mfma_f32_16x16x32_bf16 a[252:255],a[156:159],v[124:127],a[252:255]
buffer_load_dwordx4 v[192:195],v6,s[28:31],s76 offen offset:2048 sc1
v_mfma_f32_16x16x32_bf16 a[252:255],a[152:155],v[120:123],a[252:255]
ds_read_b64_tr_b16 a[152:153],v2 offset:20768
ds_read_b64_tr_b16 a[154:155],v2 offset:25888
v_mfma_f32_16x16x32_bf16 a[252:255],a[148:151],v[116:119],a[252:255]
ds_read_b64_tr_b16 a[148:149],v2 offset:10528
v_mfma_f32_16x16x32_bf16 a[252:255],a[144:147],v[112:115],a[252:255]
ds_read_b64_tr_b16 a[144:145],v2 offset:288
v_mfma_f32_16x16x32_bf16 a[0:3],a[160:163],v[64:67],a[0:3]
ds_read_b64_tr_b16 a[146:147],v2 offset:5408
v_mfma_f32_16x16x32_bf16 a[0:3],a[164:167],v[68:71],a[0:3]
buffer_load_dwordx4 v[224:227],v6,s[28:31],s76 offen offset:3072 sc1
v_mfma_f32_16x16x32_bf16 a[0:3],a[168:171],v[72:75],a[0:3]
ds_read_b64_tr_b16 a[150:151],v2 offset:15648
ds_read_b64_tr_b16 a[156:157],v2 offset:31008
v_mfma_f32_16x16x32_bf16 a[0:3],a[172:175],v[76:79],a[0:3]
ds_read_b64_tr_b16 a[158:159],v2 offset:36128
v_subrev_u32 v2,40960,v2
v_mfma_f32_16x16x32_bf16 a[4:7],a[172:175],v[92:95],a[4:7]
v_mfma_f32_16x16x32_bf16 a[4:7],a[168:171],v[88:91],a[4:7]
v_mfma_f32_16x16x32_bf16 a[4:7],a[164:167],v[84:87],a[4:7]
v_mfma_f32_16x16x32_bf16 a[4:7],a[160:163],v[80:83],a[4:7]
buffer_load_dwordx4 v[132:135],v6,s[28:31],s77 offen sc1
v_mfma_f32_16x16x32_bf16 a[8:11],a[160:163],v[96:99],a[8:11]
v_mfma_f32_16x16x32_bf16 a[8:11],a[164:167],v[100:103],a[8:11]
v_mfma_f32_16x16x32_bf16 a[8:11],a[168:171],v[104:107],a[8:11]
v_mfma_f32_16x16x32_bf16 a[8:11],a[172:175],v[108:111],a[8:11]
v_mfma_f32_16x16x32_bf16 a[12:15],a[172:175],v[124:127],a[12:15]
ds_read_b64_tr_b16 a[172:173],v1 offset:30720
ds_read_b64_tr_b16 a[174:175],v1 offset:35840
v_mfma_f32_16x16x32_bf16 a[12:15],a[168:171],v[120:123],a[12:15]
buffer_load_dwordx4 v[164:167],v6,s[28:31],s77 offen offset:1024 sc1
v_mfma_f32_16x16x32_bf16 a[12:15],a[164:167],v[116:119],a[12:15]
ds_read_b64_tr_b16 a[164:165],v1 offset:10240
ds_read_b64_tr_b16 a[166:167],v1 offset:15360
v_mfma_f32_16x16x32_bf16 a[12:15],a[160:163],v[112:115],a[12:15]
ds_read_b64_tr_b16 a[160:161],v1
v_mfma_f32_16x16x32_bf16 a[16:19],a[176:179],v[64:67],a[16:19]
ds_read_b64_tr_b16 a[162:163],v1 offset:5120
v_mfma_f32_16x16x32_bf16 a[16:19],a[180:183],v[68:71],a[16:19]
ds_read_b64_tr_b16 a[168:169],v1 offset:20480
v_mfma_f32_16x16x32_bf16 a[16:19],a[184:187],v[72:75],a[16:19]
ds_read_b64_tr_b16 a[170:171],v1 offset:25600
v_mfma_f32_16x16x32_bf16 a[16:19],a[188:191],v[76:79],a[16:19]
buffer_load_dwordx4 v[196:199],v6,s[28:31],s77 offen offset:2048 sc1
v_mfma_f32_16x16x32_bf16 a[20:23],a[188:191],v[92:95],a[20:23]
v_mfma_f32_16x16x32_bf16 a[20:23],a[184:187],v[88:91],a[20:23]
v_mfma_f32_16x16x32_bf16 a[20:23],a[180:183],v[84:87],a[20:23]
v_mfma_f32_16x16x32_bf16 a[20:23],a[176:179],v[80:83],a[20:23]
v_mfma_f32_16x16x32_bf16 a[24:27],a[176:179],v[96:99],a[24:27]
v_mfma_f32_16x16x32_bf16 a[24:27],a[180:183],v[100:103],a[24:27]
buffer_load_dwordx4 v[228:231],v6,s[28:31],s77 offen offset:3072 sc1
v_mfma_f32_16x16x32_bf16 a[24:27],a[184:187],v[104:107],a[24:27]
v_mfma_f32_16x16x32_bf16 a[24:27],a[188:191],v[108:111],a[24:27]
v_mfma_f32_16x16x32_bf16 a[28:31],a[188:191],v[124:127],a[28:31]
ds_read_b64_tr_b16 a[188:189],v2 offset:30752
ds_read_b64_tr_b16 a[190:191],v2 offset:35872
v_mfma_f32_16x16x32_bf16 a[28:31],a[184:187],v[120:123],a[28:31]
ds_read_b64_tr_b16 a[184:185],v2 offset:20512
v_mfma_f32_16x16x32_bf16 a[28:31],a[180:183],v[116:119],a[28:31]
ds_read_b64_tr_b16 a[180:181],v2 offset:10272
v_mfma_f32_16x16x32_bf16 a[28:31],a[176:179],v[112:115],a[28:31]
buffer_load_dwordx4 v[144:147],v6,s[28:31],s78 offen sc1
v_mfma_f32_16x16x32_bf16 a[32:35],a[96:99],v[64:67],a[32:35]
ds_read_b64_tr_b16 a[176:177],v2 offset:32
ds_read_b64_tr_b16 a[178:179],v2 offset:5152
v_mfma_f32_16x16x32_bf16 a[32:35],a[100:103],v[68:71],a[32:35]
ds_read_b64_tr_b16 a[182:183],v2 offset:15392
v_mfma_f32_16x16x32_bf16 a[32:35],a[104:107],v[72:75],a[32:35]
ds_read_b64_tr_b16 a[186:187],v2 offset:25632
v_mfma_f32_16x16x32_bf16 a[32:35],a[108:111],v[76:79],a[32:35]
v_mfma_f32_16x16x32_bf16 a[36:39],a[108:111],v[92:95],a[36:39]
v_mfma_f32_16x16x32_bf16 a[36:39],a[104:107],v[88:91],a[36:39]
buffer_load_dwordx4 v[176:179],v6,s[28:31],s78 offen offset:1024 sc1
v_mfma_f32_16x16x32_bf16 a[36:39],a[100:103],v[84:87],a[36:39]
v_mfma_f32_16x16x32_bf16 a[36:39],a[96:99],v[80:83],a[36:39]
v_mfma_f32_16x16x32_bf16 a[40:43],a[96:99],v[96:99],a[40:43]
v_mfma_f32_16x16x32_bf16 a[40:43],a[100:103],v[100:103],a[40:43]
v_mfma_f32_16x16x32_bf16 a[40:43],a[104:107],v[104:107],a[40:43]
)A"
R"A(v_mfma_f32_16x16x32_bf16 a[40:43],a[108:111],v[108:111],a[40:43]
buffer_load_dwordx4 v[208:211],v6,s[28:31],s78 offen offset:2048 sc1
v_mfma_f32_16x16x32_bf16 a[44:47],a[108:111],v[124:127],a[44:47]
ds_read_b64_tr_b16 a[108:109],v1 offset:30784
ds_read_b64_tr_b16 a[110:111],v1 offset:35904
v_mfma_f32_16x16x32_bf16 a[44:47],a[104:107],v[120:123],a[44:47]
ds_read_b64_tr_b16 a[104:105],v1 offset:20544
v_mfma_f32_16x16x32_bf16 a[44:47],a[100:103],v[116:119],a[44:47]
ds_read_b64_tr_b16 a[100:101],v1 offset:10304
v_mfma_f32_16x16x32_bf16 a[44:47],a[96:99],v[112:115],a[44:47]
s_waitcnt lgkmcnt(4)
ds_read_b64_tr_b16 a[96:97],v1 offset:64
v_mfma_f32_16x16x32_bf16 a[48:51],a[112:115],v[64:67],a[48:51]
ds_read_b64_tr_b16 a[98:99],v1 offset:5184
ds_read_b64_tr_b16 a[102:103],v1 offset:15424
v_mfma_f32_16x16x32_bf16 a[48:51],a[116:119],v[68:71],a[48:51]
buffer_load_dwordx4 v[240:243],v6,s[28:31],s78 offen offset:3072 sc1
v_mfma_f32_16x16x32_bf16 a[48:51],a[120:123],v[72:75],a[48:51]
ds_read_b64_tr_b16 a[106:107],v1 offset:25664
v_mfma_f32_16x16x32_bf16 a[48:51],a[124:127],v[76:79],a[48:51]
v_mfma_f32_16x16x32_bf16 a[52:55],a[124:127],v[92:95],a[52:55]
v_mfma_f32_16x16x32_bf16 a[52:55],a[120:123],v[88:91],a[52:55]
v_mfma_f32_16x16x32_bf16 a[52:55],a[116:119],v[84:87],a[52:55]
v_mfma_f32_16x16x32_bf16 a[52:55],a[112:115],v[80:83],a[52:55]
buffer_load_dwordx4 v[148:151],v6,s[28:31],s79 offen sc1
v_mfma_f32_16x16x32_bf16 a[56:59],a[112:115],v[96:99],a[56:59]
v_mfma_f32_16x16x32_bf16 a[56:59],a[116:119],v[100:103],a[56:59]
s_cmp_ge_u32 1,s71
s_cselect_b32 s80,5,3
s_mul_i32 s80,s80,163840
v_mfma_f32_16x16x32_bf16 a[56:59],a[120:123],v[104:107],a[56:59]
s_add_u32 s80,s80,s81
s_add_u32 s83,s80,20480
v_mfma_f32_16x16x32_bf16 a[56:59],a[124:127],v[108:111],a[56:59]
v_mfma_f32_16x16x32_bf16 a[60:63],a[124:127],v[124:127],a[60:63]
ds_read_b64_tr_b16 a[124:125],v2 offset:30816
ds_read_b64_tr_b16 a[126:127],v2 offset:35936
v_mfma_f32_16x16x32_bf16 a[60:63],a[120:123],v[120:123],a[60:63]
buffer_load_dwordx4 v[180:183],v6,s[28:31],s79 offen offset:1024 sc1
v_mfma_f32_16x16x32_bf16 a[60:63],a[116:119],v[116:119],a[60:63]
ds_read_b64_tr_b16 a[116:117],v2 offset:10336
ds_read_b64_tr_b16 a[118:119],v2 offset:15456
v_mfma_f32_16x16x32_bf16 a[60:63],a[112:115],v[112:115],a[60:63]
ds_read_b64_tr_b16 a[112:113],v2 offset:96
v_mfma_f32_16x16x32_bf16 a[64:67],a[128:131],v[64:67],a[64:67]
ds_read_b64_tr_b16 a[114:115],v2 offset:5216
v_mfma_f32_16x16x32_bf16 a[64:67],a[132:135],v[68:71],a[64:67]
ds_read_b64_tr_b16 a[120:121],v2 offset:20576
v_mfma_f32_16x16x32_bf16 a[64:67],a[136:139],v[72:75],a[64:67]
ds_read_b64_tr_b16 a[122:123],v2 offset:25696
v_mfma_f32_16x16x32_bf16 a[64:67],a[140:143],v[76:79],a[64:67]
buffer_load_dwordx4 v[212:215],v6,s[28:31],s79 offen offset:2048 sc1
v_mfma_f32_16x16x32_bf16 a[68:71],a[140:143],v[92:95],a[68:71]
v_mfma_f32_16x16x32_bf16 a[68:71],a[136:139],v[88:91],a[68:71]
v_mfma_f32_16x16x32_bf16 a[68:71],a[132:135],v[84:87],a[68:71]
v_mfma_f32_16x16x32_bf16 a[68:71],a[128:131],v[80:83],a[68:71]
v_mfma_f32_16x16x32_bf16 a[72:75],a[128:131],v[96:99],a[72:75]
v_mfma_f32_16x16x32_bf16 a[72:75],a[132:135],v[100:103],a[72:75]
buffer_load_dwordx4 v[244:247],v6,s[28:31],s79 offen offset:3072 sc1
v_mfma_f32_16x16x32_bf16 a[72:75],a[136:139],v[104:107],a[72:75]
v_mfma_f32_16x16x32_bf16 a[72:75],a[140:143],v[108:111],a[72:75]
v_mfma_f32_16x16x32_bf16 a[76:79],a[140:143],v[124:127],a[76:79]
ds_read_b64_tr_b16 a[140:141],v1 offset:30848
ds_read_b64_tr_b16 a[142:143],v1 offset:35968
v_mfma_f32_16x16x32_bf16 a[76:79],a[136:139],v[120:123],a[76:79]
ds_read_b64_tr_b16 a[136:137],v1 offset:20608
v_mfma_f32_16x16x32_bf16 a[76:79],a[132:135],v[116:119],a[76:79]
s_add_u32 m0,s82,40960
ds_read_b64_tr_b16 a[132:133],v1 offset:10368
v_mfma_f32_16x16x32_bf16 a[76:79],a[128:131],v[112:115],a[76:79]
ds_read_b64_tr_b16 a[128:129],v1 offset:128
v_mfma_f32_16x16x32_bf16 a[80:83],a[144:147],v[64:67],a[80:83]
ds_read_b64_tr_b16 a[130:131],v1 offset:5248
ds_read_b64_tr_b16 a[134:135],v1 offset:15488
v_mfma_f32_16x16x32_bf16 a[80:83],a[148:151],v[68:71],a[80:83]
buffer_load_dwordx4 v7,s[72:75],s80 offen lds
v_mfma_f32_16x16x32_bf16 a[80:83],a[152:155],v[72:75],a[80:83]
ds_read_b64_tr_b16 a[138:139],v1 offset:25728
v_mfma_f32_16x16x32_bf16 a[80:83],a[156:159],v[76:79],a[80:83]
v_mfma_f32_16x16x32_bf16 a[84:87],a[156:159],v[92:95],a[84:87]
v_mfma_f32_16x16x32_bf16 a[84:87],a[152:155],v[88:91],a[84:87]
v_mfma_f32_16x16x32_bf16 a[84:87],a[148:151],v[84:87],a[84:87]
buffer_load_dwordx4 v8,s[72:75],s80 offen offset:1024 lds
v_mfma_f32_16x16x32_bf16 a[84:87],a[144:147],v[80:83],a[84:87]
v_mfma_f32_16x16x32_bf16 a[88:91],a[144:147],v[96:99],a[88:91]
v_mfma_f32_16x16x32_bf16 a[88:91],a[148:151],v[100:103],a[88:91]
v_mfma_f32_16x16x32_bf16 a[88:91],a[152:155],v[104:107],a[88:91]
v_mfma_f32_16x16x32_bf16 a[88:91],a[156:159],v[108:111],a[88:91]
buffer_load_dwordx4 v9,s[72:75],s80 offen offset:2048 lds
v_mfma_f32_16x16x32_bf16 a[92:95],a[156:159],v[124:127],a[92:95]
ds_read_b64_tr_b16 a[156:157],v2 offset:30880
ds_read_b64_tr_b16 a[158:159],v2 offset:36000
v_mfma_f32_16x16x32_bf16 a[92:95],a[152:155],v[120:123],a[92:95]
ds_read_b64_tr_b16 a[152:153],v2 offset:20640
v_mfma_f32_16x16x32_bf16 a[92:95],a[148:151],v[116:119],a[92:95]
ds_read_b64_tr_b16 a[148:149],v2 offset:10400
v_mfma_f32_16x16x32_bf16 a[92:95],a[144:147],v[112:115],a[92:95]
ds_read_b64_tr_b16 a[144:145],v2 offset:160
v_mfma_f32_16x16x32_bf16 a[192:195],a[160:163],v[136:139],a[192:195]
buffer_load_dwordx4 v10,s[72:75],s80 offen offset:3072 lds
v_mfma_f32_16x16x32_bf16 a[192:195],a[164:167],v[140:143],a[192:195]
ds_read_b64_tr_b16 a[146:147],v2 offset:5280
ds_read_b64_tr_b16 a[150:151],v2 offset:15520
v_mfma_f32_16x16x32_bf16 a[192:195],a[168:171],v[152:155],a[192:195]
s_add_u32 m0,s82,45056
ds_read_b64_tr_b16 a[154:155],v2 offset:25760
v_mfma_f32_16x16x32_bf16 a[192:195],a[172:175],v[156:159],a[192:195]
s_waitcnt vmcnt(12)
v_mfma_f32_16x16x32_bf16 a[196:199],a[172:175],v[188:191],a[196:199]
v_mfma_f32_16x16x32_bf16 a[196:199],a[168:171],v[184:187],a[196:199]
buffer_load_dwordx4 v11,s[72:75],s80 offen lds
v_mfma_f32_16x16x32_bf16 a[196:199],a[164:167],v[172:175],a[196:199]
v_mfma_f32_16x16x32_bf16 a[196:199],a[160:163],v[168:171],a[196:199]
s_add_u32 m0,s82,46080
v_mfma_f32_16x16x32_bf16 a[200:203],a[160:163],v[200:203],a[200:203]
v_mfma_f32_16x16x32_bf16 a[200:203],a[164:167],v[204:207],a[200:203]
v_mfma_f32_16x16x32_bf16 a[200:203],a[168:171],v[216:219],a[200:203]
buffer_load_dwordx4 v7,s[72:75],s83 offen lds
v_mfma_f32_16x16x32_bf16 a[200:203],a[172:175],v[220:223],a[200:203]
v_mfma_f32_16x16x32_bf16 a[204:207],a[172:175],v[252:255],a[204:207]
ds_read_b64_tr_b16 a[172:173],v1 offset:30912
ds_read_b64_tr_b16 a[174:175],v1 offset:36032
v_mfma_f32_16x16x32_bf16 a[204:207],a[168:171],v[248:251],a[204:207]
ds_read_b64_tr_b16 a[168:169],v1 offset:20672
v_mfma_f32_16x16x32_bf16 a[204:207],a[164:167],v[236:239],a[204:207]
ds_read_b64_tr_b16 a[164:165],v1 offset:10432
v_mfma_f32_16x16x32_bf16 a[204:207],a[160:163],v[232:235],a[204:207]
s_barrier
buffer_load_dwordx4 v8,s[72:75],s83 offen offset:1024 lds
v_mfma_f32_16x16x32_bf16 a[208:211],a[176:179],v[136:139],a[208:211]
s_cmp_ge_u32 1,s71
s_cselect_b32 s76,5,3
s_lshl_b32 s76,s76,16
v_mfma_f32_16x16x32_bf16 a[208:211],a[180:183],v[140:143],a[208:211]
s_add_u32 s77,s76,4096
s_add_u32 s78,s76,8192
s_add_u32 s79,s76,12288
v_mfma_f32_16x16x32_bf16 a[208:211],a[184:187],v[152:155],a[208:211]
s_max_i32 s76,s76,s18
s_max_i32 s77,s77,s19
s_max_i32 s78,s78,s100
v_mfma_f32_16x16x32_bf16 a[208:211],a[188:191],v[156:159],a[208:211]
s_max_i32 s79,s79,s101
buffer_load_dwordx4 v9,s[72:75],s83 offen offset:2048 lds
v_mfma_f32_16x16x32_bf16 a[212:215],a[188:191],v[188:191],a[212:215]
ds_read_b64_tr_b16 a[160:161],v1 offset:192
ds_read_b64_tr_b16 a[162:163],v1 offset:5312
v_mfma_f32_16x16x32_bf16 a[212:215],a[184:187],v[184:187],a[212:215]
ds_read_b64_tr_b16 a[166:167],v1 offset:15552
v_mfma_f32_16x16x32_bf16 a[212:215],a[180:183],v[172:175],a[212:215]
ds_read_b64_tr_b16 a[170:171],v1 offset:25792
v_mfma_f32_16x16x32_bf16 a[212:215],a[176:179],v[168:171],a[212:215]
v_mfma_f32_16x16x32_bf16 a[216:219],a[176:179],v[200:203],a[216:219]
buffer_load_dwordx4 v10,s[72:75],s83 offen offset:3072 lds
v_mfma_f32_16x16x32_bf16 a[216:219],a[180:183],v[204:207],a[216:219]
v_mfma_f32_16x16x32_bf16 a[216:219],a[184:187],v[216:219],a[216:219]
s_add_u32 m0,s82,50176
v_mfma_f32_16x16x32_bf16 a[216:219],a[188:191],v[220:223],a[216:219]
v_mfma_f32_16x16x32_bf16 a[220:223],a[188:191],v[252:255],a[220:223]
ds_read_b64_tr_b16 a[188:189],v2 offset:30944
ds_read_b64_tr_b16 a[190:191],v2 offset:36064
v_mfma_f32_16x16x32_bf16 a[220:223],a[184:187],v[248:251],a[220:223]
buffer_load_dwordx4 v11,s[72:75],s83 offen lds
v_mfma_f32_16x16x32_bf16 a[220:223],a[180:183],v[236:239],a[220:223]
ds_read_b64_tr_b16 a[180:181],v2 offset:10464
ds_read_b64_tr_b16 a[182:183],v2 offset:15584
v_mfma_f32_16x16x32_bf16 a[220:223],a[176:179],v[232:235],a[220:223]
s_waitcnt lgkmcnt(4)
ds_read_b64_tr_b16 a[176:177],v2 offset:224
v_mfma_f32_16x16x32_bf16 a[224:227],a[96:99],v[136:139],a[224:227]
ds_read_b64_tr_b16 a[178:179],v2 offset:5344
v_mfma_f32_16x16x32_bf16 a[224:227],a[100:103],v[140:143],a[224:227]
ds_read_b64_tr_b16 a[184:185],v2 offset:20704
v_mfma_f32_16x16x32_bf16 a[224:227],a[104:107],v[152:155],a[224:227]
ds_read_b64_tr_b16 a[186:187],v2 offset:25824
v_mfma_f32_16x16x32_bf16 a[224:227],a[108:111],v[156:159],a[224:227]
v_mfma_f32_16x16x32_bf16 a[228:231],a[108:111],v[188:191],a[228:231]
v_mfma_f32_16x16x32_bf16 a[228:231],a[104:107],v[184:187],a[228:231]
v_mfma_f32_16x16x32_bf16 a[228:231],a[100:103],v[172:175],a[228:231]
v_mfma_f32_16x16x32_bf16 a[228:231],a[96:99],v[168:171],a[228:231]
buffer_load_dwordx4 v[64:67],v6,s[28:31],s76 offen sc1
v_mfma_f32_16x16x32_bf16 a[232:235],a[96:99],v[200:203],a[232:235]
v_mfma_f32_16x16x32_bf16 a[232:235],a[100:103],v[204:207],a[232:235]
v_mfma_f32_16x16x32_bf16 a[232:235],a[104:107],v[216:219],a[232:235]
v_mfma_f32_16x16x32_bf16 a[232:235],a[108:111],v[220:223],a[232:235]
v_mfma_f32_16x16x32_bf16 a[236:239],a[108:111],v[252:255],a[236:239]
buffer_load_dwordx4 v[68:71],v6,s[28:31],s76 offen offset:1024 sc1
v_mfma_f32_16x16x32_bf16 a[236:239],a[104:107],v[248:251],a[236:239]
ds_read_b64_tr_b16 a[104:105],v1 offset:20736
ds_read_b64_tr_b16 a[106:107],v1 offset:25856
v_mfma_f32_16x16x32_bf16 a[236:239],a[100:103],v[236:239],a[236:239]
ds_read_b64_tr_b16 a[100:101],v1 offset:10496
v_mfma_f32_16x16x32_bf16 a[236:239],a[96:99],v[232:235],a[236:239]
ds_read_b64_tr_b16 a[96:97],v1 offset:256
v_mfma_f32_16x16x32_bf16 a[240:243],a[112:115],v[136:139],a[240:243]
ds_read_b64_tr_b16 a[98:99],v1 offset:5376
v_mfma_f32_16x16x32_bf16 a[240:243],a[116:119],v[140:143],a[240:243]
buffer_load_dwordx4 v[72:75],v6,s[28:31],s76 offen offset:2048 sc1
v_mfma_f32_16x16x32_bf16 a[240:243],a[120:123],v[152:155],a[240:243]
ds_read_b64_tr_b16 a[102:103],v1 offset:15616
ds_read_b64_tr_b16 a[108:109],v1 offset:30976
v_mfma_f32_16x16x32_bf16 a[240:243],a[124:127],v[156:159],a[240:243]
ds_read_b64_tr_b16 a[110:111],v1 offset:36096
v_subrev_u32 v1,81920,v1
v_mfma_f32_16x16x32_bf16 a[244:247],a[124:127],v[188:191],a[244:247]
v_add_u32 v42,40960,v1
v_add_u32 v44,122880,v1
v_mfma_f32_16x16x32_bf16 a[244:247],a[120:123],v[184:187],a[244:247]
v_mfma_f32_16x16x32_bf16 a[244:247],a[116:119],v[172:175],a[244:247]
buffer_load_dwordx4 v[76:79],v6,s[28:31],s76 offen offset:3072 sc1
v_mfma_f32_16x16x32_bf16 a[244:247],a[112:115],v[168:171],a[244:247]
v_mfma_f32_16x16x32_bf16 a[248:251],a[112:115],v[200:203],a[248:251]
v_mfma_f32_16x16x32_bf16 a[248:251],a[116:119],v[204:207],a[248:251]
v_mfma_f32_16x16x32_bf16 a[248:251],a[120:123],v[216:219],a[248:251]
v_mfma_f32_16x16x32_bf16 a[248:251],a[124:127],v[220:223],a[248:251]
buffer_load_dwordx4 v[80:83],v6,s[28:31],s77 offen sc1
v_mfma_f32_16x16x32_bf16 a[252:255],a[124:127],v[252:255],a[252:255]
ds_read_b64_tr_b16 a[124:125],v2 offset:31008
)A"
R"A(ds_read_b64_tr_b16 a[126:127],v2 offset:36128
v_mfma_f32_16x16x32_bf16 a[252:255],a[120:123],v[248:251],a[252:255]
ds_read_b64_tr_b16 a[120:121],v2 offset:20768
v_mfma_f32_16x16x32_bf16 a[252:255],a[116:119],v[236:239],a[252:255]
ds_read_b64_tr_b16 a[116:117],v2 offset:10528
v_mfma_f32_16x16x32_bf16 a[252:255],a[112:115],v[232:235],a[252:255]
ds_read_b64_tr_b16 a[112:113],v2 offset:288
v_mfma_f32_16x16x32_bf16 a[0:3],a[128:131],v[136:139],a[0:3]
buffer_load_dwordx4 v[84:87],v6,s[28:31],s77 offen offset:1024 sc1
v_mfma_f32_16x16x32_bf16 a[0:3],a[132:135],v[140:143],a[0:3]
ds_read_b64_tr_b16 a[114:115],v2 offset:5408
ds_read_b64_tr_b16 a[118:119],v2 offset:15648
v_mfma_f32_16x16x32_bf16 a[0:3],a[136:139],v[152:155],a[0:3]
ds_read_b64_tr_b16 a[122:123],v2 offset:25888
v_subrev_u32 v2,81920,v2
v_mfma_f32_16x16x32_bf16 a[0:3],a[140:143],v[156:159],a[0:3]
v_add_u32 v43,40960,v2
v_add_u32 v45,122880,v2
v_mfma_f32_16x16x32_bf16 a[4:7],a[140:143],v[188:191],a[4:7]
v_mfma_f32_16x16x32_bf16 a[4:7],a[136:139],v[184:187],a[4:7]
buffer_load_dwordx4 v[88:91],v6,s[28:31],s77 offen offset:2048 sc1
v_mfma_f32_16x16x32_bf16 a[4:7],a[132:135],v[172:175],a[4:7]
v_mfma_f32_16x16x32_bf16 a[4:7],a[128:131],v[168:171],a[4:7]
v_mfma_f32_16x16x32_bf16 a[8:11],a[128:131],v[200:203],a[8:11]
v_mfma_f32_16x16x32_bf16 a[8:11],a[132:135],v[204:207],a[8:11]
v_mfma_f32_16x16x32_bf16 a[8:11],a[136:139],v[216:219],a[8:11]
buffer_load_dwordx4 v[92:95],v6,s[28:31],s77 offen offset:3072 sc1
v_mfma_f32_16x16x32_bf16 a[8:11],a[140:143],v[220:223],a[8:11]
v_mfma_f32_16x16x32_bf16 a[12:15],a[140:143],v[252:255],a[12:15]
ds_read_b64_tr_b16 a[140:141],v1 offset:30720
ds_read_b64_tr_b16 a[142:143],v1 offset:35840
v_mfma_f32_16x16x32_bf16 a[12:15],a[136:139],v[248:251],a[12:15]
ds_read_b64_tr_b16 a[136:137],v1 offset:20480
v_mfma_f32_16x16x32_bf16 a[12:15],a[132:135],v[236:239],a[12:15]
ds_read_b64_tr_b16 a[132:133],v1 offset:10240
v_mfma_f32_16x16x32_bf16 a[12:15],a[128:131],v[232:235],a[12:15]
buffer_load_dwordx4 v[96:99],v6,s[28:31],s78 offen sc1
v_mfma_f32_16x16x32_bf16 a[16:19],a[144:147],v[136:139],a[16:19]
ds_read_b64_tr_b16 a[128:129],v1
ds_read_b64_tr_b16 a[130:131],v1 offset:5120
v_mfma_f32_16x16x32_bf16 a[16:19],a[148:151],v[140:143],a[16:19]
ds_read_b64_tr_b16 a[134:135],v1 offset:15360
v_mfma_f32_16x16x32_bf16 a[16:19],a[152:155],v[152:155],a[16:19]
ds_read_b64_tr_b16 a[138:139],v1 offset:25600
v_mfma_f32_16x16x32_bf16 a[16:19],a[156:159],v[156:159],a[16:19]
v_mfma_f32_16x16x32_bf16 a[20:23],a[156:159],v[188:191],a[20:23]
buffer_load_dwordx4 v[100:103],v6,s[28:31],s78 offen offset:1024 sc1
v_mfma_f32_16x16x32_bf16 a[20:23],a[152:155],v[184:187],a[20:23]
v_mfma_f32_16x16x32_bf16 a[20:23],a[148:151],v[172:175],a[20:23]
v_mfma_f32_16x16x32_bf16 a[20:23],a[144:147],v[168:171],a[20:23]
v_mfma_f32_16x16x32_bf16 a[24:27],a[144:147],v[200:203],a[24:27]
v_mfma_f32_16x16x32_bf16 a[24:27],a[148:151],v[204:207],a[24:27]
buffer_load_dwordx4 v[104:107],v6,s[28:31],s78 offen offset:2048 sc1
v_mfma_f32_16x16x32_bf16 a[24:27],a[152:155],v[216:219],a[24:27]
v_mfma_f32_16x16x32_bf16 a[24:27],a[156:159],v[220:223],a[24:27]
v_mfma_f32_16x16x32_bf16 a[28:31],a[156:159],v[252:255],a[28:31]
ds_read_b64_tr_b16 a[156:157],v2 offset:30752
ds_read_b64_tr_b16 a[158:159],v2 offset:35872
v_mfma_f32_16x16x32_bf16 a[28:31],a[152:155],v[248:251],a[28:31]
ds_read_b64_tr_b16 a[152:153],v2 offset:20512
v_mfma_f32_16x16x32_bf16 a[28:31],a[148:151],v[236:239],a[28:31]
buffer_load_dwordx4 v[108:111],v6,s[28:31],s78 offen offset:3072 sc1
v_mfma_f32_16x16x32_bf16 a[28:31],a[144:147],v[232:235],a[28:31]
s_cmp_ge_u32 2,s71
s_cselect_b32 s80,7,5
s_mul_i32 s80,s80,163840
v_mfma_f32_16x16x32_bf16 a[32:35],a[160:163],v[136:139],a[32:35]
s_add_u32 s80,s80,s81
s_add_u32 s83,s80,20480
ds_read_b64_tr_b16 a[144:145],v2 offset:32
v_mfma_f32_16x16x32_bf16 a[32:35],a[164:167],v[140:143],a[32:35]
ds_read_b64_tr_b16 a[146:147],v2 offset:5152
ds_read_b64_tr_b16 a[148:149],v2 offset:10272
v_mfma_f32_16x16x32_bf16 a[32:35],a[168:171],v[152:155],a[32:35]
ds_read_b64_tr_b16 a[150:151],v2 offset:15392
v_mfma_f32_16x16x32_bf16 a[32:35],a[172:175],v[156:159],a[32:35]
buffer_load_dwordx4 v[112:115],v6,s[28:31],s79 offen sc1
v_mfma_f32_16x16x32_bf16 a[36:39],a[172:175],v[188:191],a[36:39]
ds_read_b64_tr_b16 a[154:155],v2 offset:25632
v_mfma_f32_16x16x32_bf16 a[36:39],a[168:171],v[184:187],a[36:39]
v_mfma_f32_16x16x32_bf16 a[36:39],a[164:167],v[172:175],a[36:39]
v_mfma_f32_16x16x32_bf16 a[36:39],a[160:163],v[168:171],a[36:39]
v_mfma_f32_16x16x32_bf16 a[40:43],a[160:163],v[200:203],a[40:43]
buffer_load_dwordx4 v[116:119],v6,s[28:31],s79 offen offset:1024 sc1
v_mfma_f32_16x16x32_bf16 a[40:43],a[164:167],v[204:207],a[40:43]
v_mfma_f32_16x16x32_bf16 a[40:43],a[168:171],v[216:219],a[40:43]
v_mfma_f32_16x16x32_bf16 a[40:43],a[172:175],v[220:223],a[40:43]
v_mfma_f32_16x16x32_bf16 a[44:47],a[172:175],v[252:255],a[44:47]
ds_read_b64_tr_b16 a[172:173],v1 offset:30784
ds_read_b64_tr_b16 a[174:175],v1 offset:35904
v_mfma_f32_16x16x32_bf16 a[44:47],a[168:171],v[248:251],a[44:47]
buffer_load_dwordx4 v[120:123],v6,s[28:31],s79 offen offset:2048 sc1
v_mfma_f32_16x16x32_bf16 a[44:47],a[164:167],v[236:239],a[44:47]
ds_read_b64_tr_b16 a[164:165],v1 offset:10304
ds_read_b64_tr_b16 a[166:167],v1 offset:15424
v_mfma_f32_16x16x32_bf16 a[44:47],a[160:163],v[232:235],a[44:47]
s_waitcnt lgkmcnt(4)
ds_read_b64_tr_b16 a[160:161],v1 offset:64
v_mfma_f32_16x16x32_bf16 a[48:51],a[176:179],v[136:139],a[48:51]
ds_read_b64_tr_b16 a[162:163],v1 offset:5184
v_mfma_f32_16x16x32_bf16 a[48:51],a[180:183],v[140:143],a[48:51]
ds_read_b64_tr_b16 a[168:169],v1 offset:20544
v_mfma_f32_16x16x32_bf16 a[48:51],a[184:187],v[152:155],a[48:51]
buffer_load_dwordx4 v[124:127],v6,s[28:31],s79 offen offset:3072 sc1
v_mfma_f32_16x16x32_bf16 a[48:51],a[188:191],v[156:159],a[48:51]
ds_read_b64_tr_b16 a[170:171],v1 offset:25664
v_mfma_f32_16x16x32_bf16 a[52:55],a[188:191],v[188:191],a[52:55]
s_add_u32 m0,s82,122880
v_mfma_f32_16x16x32_bf16 a[52:55],a[184:187],v[184:187],a[52:55]
v_mfma_f32_16x16x32_bf16 a[52:55],a[180:183],v[172:175],a[52:55]
v_mfma_f32_16x16x32_bf16 a[52:55],a[176:179],v[168:171],a[52:55]
buffer_load_dwordx4 v7,s[72:75],s80 offen lds
v_mfma_f32_16x16x32_bf16 a[56:59],a[176:179],v[200:203],a[56:59]
v_mfma_f32_16x16x32_bf16 a[56:59],a[180:183],v[204:207],a[56:59]
v_mfma_f32_16x16x32_bf16 a[56:59],a[184:187],v[216:219],a[56:59]
v_mfma_f32_16x16x32_bf16 a[56:59],a[188:191],v[220:223],a[56:59]
v_mfma_f32_16x16x32_bf16 a[60:63],a[188:191],v[252:255],a[60:63]
buffer_load_dwordx4 v8,s[72:75],s80 offen offset:1024 lds
v_mfma_f32_16x16x32_bf16 a[60:63],a[184:187],v[248:251],a[60:63]
ds_read_b64_tr_b16 a[184:185],v2 offset:20576
ds_read_b64_tr_b16 a[186:187],v2 offset:25696
v_mfma_f32_16x16x32_bf16 a[60:63],a[180:183],v[236:239],a[60:63]
ds_read_b64_tr_b16 a[180:181],v2 offset:10336
v_mfma_f32_16x16x32_bf16 a[60:63],a[176:179],v[232:235],a[60:63]
ds_read_b64_tr_b16 a[176:177],v2 offset:96
v_mfma_f32_16x16x32_bf16 a[64:67],a[96:99],v[136:139],a[64:67]
ds_read_b64_tr_b16 a[178:179],v2 offset:5216
v_mfma_f32_16x16x32_bf16 a[64:67],a[100:103],v[140:143],a[64:67]
buffer_load_dwordx4 v9,s[72:75],s80 offen offset:2048 lds
v_mfma_f32_16x16x32_bf16 a[64:67],a[104:107],v[152:155],a[64:67]
ds_read_b64_tr_b16 a[182:183],v2 offset:15456
ds_read_b64_tr_b16 a[188:189],v2 offset:30816
v_mfma_f32_16x16x32_bf16 a[64:67],a[108:111],v[156:159],a[64:67]
ds_read_b64_tr_b16 a[190:191],v2 offset:35936
v_mfma_f32_16x16x32_bf16 a[68:71],a[108:111],v[188:191],a[68:71]
v_mfma_f32_16x16x32_bf16 a[68:71],a[104:107],v[184:187],a[68:71]
v_mfma_f32_16x16x32_bf16 a[68:71],a[100:103],v[172:175],a[68:71]
buffer_load_dwordx4 v10,s[72:75],s80 offen offset:3072 lds
v_mfma_f32_16x16x32_bf16 a[68:71],a[96:99],v[168:171],a[68:71]
v_mfma_f32_16x16x32_bf16 a[72:75],a[96:99],v[200:203],a[72:75]
s_add_u32 m0,s82,126976
v_mfma_f32_16x16x32_bf16 a[72:75],a[100:103],v[204:207],a[72:75]
v_mfma_f32_16x16x32_bf16 a[72:75],a[104:107],v[216:219],a[72:75]
s_cmp_ge_u32 2,s71
s_cselect_b32 s76,7,5
s_lshl_b32 s76,s76,16
v_mfma_f32_16x16x32_bf16 a[72:75],a[108:111],v[220:223],a[72:75]
s_add_u32 s77,s76,4096
buffer_load_dwordx4 v11,s[72:75],s80 offen lds
v_mfma_f32_16x16x32_bf16 a[76:79],a[108:111],v[252:255],a[76:79]
s_add_u32 s78,s76,8192
s_add_u32 s79,s76,12288
s_max_i32 s76,s76,s18
v_mfma_f32_16x16x32_bf16 a[76:79],a[104:107],v[248:251],a[76:79]
s_add_u32 m0,s82,128000
s_max_i32 s77,s77,s19
ds_read_b64_tr_b16 a[104:105],v1 offset:20608
v_mfma_f32_16x16x32_bf16 a[76:79],a[100:103],v[236:239],a[76:79]
s_max_i32 s78,s78,s100
ds_read_b64_tr_b16 a[100:101],v1 offset:10368
ds_read_b64_tr_b16 a[102:103],v1 offset:15488
v_mfma_f32_16x16x32_bf16 a[76:79],a[96:99],v[232:235],a[76:79]
s_max_i32 s79,s79,s101
ds_read_b64_tr_b16 a[96:97],v1 offset:128
v_mfma_f32_16x16x32_bf16 a[80:83],a[112:115],v[136:139],a[80:83]
buffer_load_dwordx4 v7,s[72:75],s83 offen lds
v_mfma_f32_16x16x32_bf16 a[80:83],a[116:119],v[140:143],a[80:83]
ds_read_b64_tr_b16 a[98:99],v1 offset:5248
ds_read_b64_tr_b16 a[106:107],v1 offset:25728
v_mfma_f32_16x16x32_bf16 a[80:83],a[120:123],v[152:155],a[80:83]
ds_read_b64_tr_b16 a[108:109],v1 offset:30848
v_mfma_f32_16x16x32_bf16 a[80:83],a[124:127],v[156:159],a[80:83]
ds_read_b64_tr_b16 a[110:111],v1 offset:35968
v_mfma_f32_16x16x32_bf16 a[84:87],a[124:127],v[188:191],a[84:87]
v_mfma_f32_16x16x32_bf16 a[84:87],a[120:123],v[184:187],a[84:87]
buffer_load_dwordx4 v8,s[72:75],s83 offen offset:1024 lds
v_mfma_f32_16x16x32_bf16 a[84:87],a[116:119],v[172:175],a[84:87]
v_mfma_f32_16x16x32_bf16 a[84:87],a[112:115],v[168:171],a[84:87]
v_mfma_f32_16x16x32_bf16 a[88:91],a[112:115],v[200:203],a[88:91]
v_mfma_f32_16x16x32_bf16 a[88:91],a[116:119],v[204:207],a[88:91]
v_mfma_f32_16x16x32_bf16 a[88:91],a[120:123],v[216:219],a[88:91]
buffer_load_dwordx4 v9,s[72:75],s83 offen offset:2048 lds
v_mfma_f32_16x16x32_bf16 a[88:91],a[124:127],v[220:223],a[88:91]
v_mfma_f32_16x16x32_bf16 a[92:95],a[124:127],v[252:255],a[92:95]
ds_read_b64_tr_b16 a[124:125],v2 offset:30880
ds_read_b64_tr_b16 a[126:127],v2 offset:36000
v_mfma_f32_16x16x32_bf16 a[92:95],a[120:123],v[248:251],a[92:95]
ds_read_b64_tr_b16 a[120:121],v2 offset:20640
v_mfma_f32_16x16x32_bf16 a[92:95],a[116:119],v[236:239],a[92:95]
ds_read_b64_tr_b16 a[116:117],v2 offset:10400
v_mfma_f32_16x16x32_bf16 a[92:95],a[112:115],v[232:235],a[92:95]
buffer_load_dwordx4 v10,s[72:75],s83 offen offset:3072 lds
v_mfma_f32_16x16x32_bf16 a[192:195],a[128:131],v[128:131],a[192:195]
ds_read_b64_tr_b16 a[112:113],v2 offset:160
ds_read_b64_tr_b16 a[114:115],v2 offset:5280
v_mfma_f32_16x16x32_bf16 a[192:195],a[132:135],v[160:163],a[192:195]
s_add_u32 m0,s82,132096
ds_read_b64_tr_b16 a[118:119],v2 offset:15520
v_mfma_f32_16x16x32_bf16 a[192:195],a[136:139],v[192:195],a[192:195]
ds_read_b64_tr_b16 a[122:123],v2 offset:25760
v_mfma_f32_16x16x32_bf16 a[192:195],a[140:143],v[224:227],a[192:195]
buffer_load_dwordx4 v[136:139],v6,s[28:31],s76 offen sc1
v_mfma_f32_16x16x32_bf16 a[196:199],a[140:143],v[228:231],a[196:199]
v_mfma_f32_16x16x32_bf16 a[196:199],a[136:139],v[196:199],a[196:199]
v_mfma_f32_16x16x32_bf16 a[196:199],a[132:135],v[164:167],a[196:199]
v_mfma_f32_16x16x32_bf16 a[196:199],a[128:131],v[132:135],a[196:199]
s_waitcnt vmcnt(14)
buffer_load_dwordx4 v11,s[72:75],s83 offen lds
v_mfma_f32_16x16x32_bf16 a[200:203],a[128:131],v[144:147],a[200:203]
ds_read_b64_tr_b16 a[128:129],v1 offset:192
ds_read_b64_tr_b16 a[130:131],v1 offset:5312
v_mfma_f32_16x16x32_bf16 a[200:203],a[132:135],v[176:179],a[200:203]
ds_read_b64_tr_b16 a[132:133],v1 offset:10432
v_mfma_f32_16x16x32_bf16 a[200:203],a[136:139],v[208:211],a[200:203]
ds_read_b64_tr_b16 a[134:135],v1 offset:15552
v_mfma_f32_16x16x32_bf16 a[200:203],a[140:143],v[240:243],a[200:203]
buffer_load_dwordx4 v[140:143],v6,s[28:31],s76 offen offset:1024 sc1
v_mfma_f32_16x16x32_bf16 a[208:211],a[144:147],v[128:131],a[208:211]
ds_read_b64_tr_b16 a[136:137],v1 offset:20672
ds_read_b64_tr_b16 a[138:139],v1 offset:25792
v_mfma_f32_16x16x32_bf16 a[208:211],a[148:151],v[160:163],a[208:211]
)A"
R"A(ds_read_b64_tr_b16 a[140:141],v1 offset:30912
v_mfma_f32_16x16x32_bf16 a[208:211],a[152:155],v[192:195],a[208:211]
ds_read_b64_tr_b16 a[142:143],v1 offset:36032
v_mfma_f32_16x16x32_bf16 a[208:211],a[156:159],v[224:227],a[208:211]
buffer_load_dwordx4 v[152:155],v6,s[28:31],s76 offen offset:2048 sc1
v_mfma_f32_16x16x32_bf16 a[212:215],a[156:159],v[228:231],a[212:215]
v_mfma_f32_16x16x32_bf16 a[212:215],a[152:155],v[196:199],a[212:215]
v_mfma_f32_16x16x32_bf16 a[212:215],a[148:151],v[164:167],a[212:215]
v_mfma_f32_16x16x32_bf16 a[212:215],a[144:147],v[132:135],a[212:215]
buffer_load_dwordx4 v[156:159],v6,s[28:31],s76 offen offset:3072 sc1
v_mfma_f32_16x16x32_bf16 a[216:219],a[144:147],v[144:147],a[216:219]
s_mul_i32 s76,s70,12
ds_read_b64_tr_b16 a[144:145],v2 offset:224
ds_read_b64_tr_b16 a[146:147],v2 offset:5344
v_mfma_f32_16x16x32_bf16 a[216:219],a[148:151],v[176:179],a[216:219]
s_add_u32 s76,s76,0
s_add_u32 s76,s76,s40
ds_read_b64_tr_b16 a[148:149],v2 offset:10464
v_mfma_f32_16x16x32_bf16 a[216:219],a[152:155],v[208:211],a[216:219]
s_mul_i32 s76,s76,5120
s_max_i32 s76,s76,s101
ds_read_b64_tr_b16 a[150:151],v2 offset:15584
v_mfma_f32_16x16x32_bf16 a[216:219],a[156:159],v[240:243],a[216:219]
s_waitcnt lgkmcnt(4)
buffer_load_dwordx4 v[168:171],v6,s[28:31],s77 offen sc1
v_mfma_f32_16x16x32_bf16 a[224:227],a[160:163],v[128:131],a[224:227]
ds_read_b64_tr_b16 a[152:153],v2 offset:20704
ds_read_b64_tr_b16 a[154:155],v2 offset:25824
v_mfma_f32_16x16x32_bf16 a[224:227],a[164:167],v[160:163],a[224:227]
ds_read_b64_tr_b16 a[156:157],v2 offset:30944
v_mfma_f32_16x16x32_bf16 a[224:227],a[168:171],v[192:195],a[224:227]
ds_read_b64_tr_b16 a[158:159],v2 offset:36064
v_mfma_f32_16x16x32_bf16 a[224:227],a[172:175],v[224:227],a[224:227]
buffer_load_dwordx4 v[172:175],v6,s[28:31],s77 offen offset:1024 sc1
v_mfma_f32_16x16x32_bf16 a[228:231],a[172:175],v[228:231],a[228:231]
v_mfma_f32_16x16x32_bf16 a[228:231],a[168:171],v[196:199],a[228:231]
v_mfma_f32_16x16x32_bf16 a[228:231],a[164:167],v[164:167],a[228:231]
v_mfma_f32_16x16x32_bf16 a[228:231],a[160:163],v[132:135],a[228:231]
v_mfma_f32_16x16x32_bf16 a[232:235],a[160:163],v[144:147],a[232:235]
buffer_load_dwordx4 v[184:187],v6,s[28:31],s77 offen offset:2048 sc1
v_mfma_f32_16x16x32_bf16 a[232:235],a[164:167],v[176:179],a[232:235]
ds_read_b64_tr_b16 a[160:161],v1 offset:256
ds_read_b64_tr_b16 a[162:163],v1 offset:5376
v_mfma_f32_16x16x32_bf16 a[232:235],a[168:171],v[208:211],a[232:235]
ds_read_b64_tr_b16 a[164:165],v1 offset:10496
v_mfma_f32_16x16x32_bf16 a[232:235],a[172:175],v[240:243],a[232:235]
ds_read_b64_tr_b16 a[166:167],v1 offset:15616
v_mfma_f32_16x16x32_bf16 a[240:243],a[176:179],v[128:131],a[240:243]
ds_read_b64_tr_b16 a[168:169],v1 offset:20736
v_mfma_f32_16x16x32_bf16 a[240:243],a[180:183],v[160:163],a[240:243]
buffer_load_dwordx4 v[188:191],v6,s[28:31],s77 offen offset:3072 sc1
v_mfma_f32_16x16x32_bf16 a[240:243],a[184:187],v[192:195],a[240:243]
s_mul_i32 s77,s70,12
ds_read_b64_tr_b16 a[170:171],v1 offset:25856
ds_read_b64_tr_b16 a[172:173],v1 offset:30976
v_mfma_f32_16x16x32_bf16 a[240:243],a[188:191],v[224:227],a[240:243]
s_add_u32 s77,s77,4
s_add_u32 s77,s77,s40
ds_read_b64_tr_b16 a[174:175],v1 offset:36096
v_mfma_f32_16x16x32_bf16 a[244:247],a[188:191],v[228:231],a[244:247]
s_mul_i32 s77,s77,5120
s_max_i32 s77,s77,s101
v_mfma_f32_16x16x32_bf16 a[244:247],a[184:187],v[196:199],a[244:247]
v_mfma_f32_16x16x32_bf16 a[244:247],a[180:183],v[164:167],a[244:247]
buffer_load_dwordx4 v[200:203],v6,s[28:31],s78 offen sc1
v_mfma_f32_16x16x32_bf16 a[244:247],a[176:179],v[132:135],a[244:247]
v_mfma_f32_16x16x32_bf16 a[248:251],a[176:179],v[144:147],a[248:251]
ds_read_b64_tr_b16 a[176:177],v2 offset:288
ds_read_b64_tr_b16 a[178:179],v2 offset:5408
v_mfma_f32_16x16x32_bf16 a[248:251],a[180:183],v[176:179],a[248:251]
ds_read_b64_tr_b16 a[180:181],v2 offset:10528
v_mfma_f32_16x16x32_bf16 a[248:251],a[184:187],v[208:211],a[248:251]
ds_read_b64_tr_b16 a[182:183],v2 offset:15648
v_mfma_f32_16x16x32_bf16 a[248:251],a[188:191],v[240:243],a[248:251]
buffer_load_dwordx4 v[204:207],v6,s[28:31],s78 offen offset:1024 sc1
v_mfma_f32_16x16x32_bf16 a[0:3],a[96:99],v[128:131],a[0:3]
ds_read_b64_tr_b16 a[184:185],v2 offset:20768
ds_read_b64_tr_b16 a[186:187],v2 offset:25888
v_mfma_f32_16x16x32_bf16 a[0:3],a[100:103],v[160:163],a[0:3]
ds_read_b64_tr_b16 a[188:189],v2 offset:31008
v_mfma_f32_16x16x32_bf16 a[0:3],a[104:107],v[192:195],a[0:3]
ds_read_b64_tr_b16 a[190:191],v2 offset:36128
v_mfma_f32_16x16x32_bf16 a[0:3],a[108:111],v[224:227],a[0:3]
v_mfma_f32_16x16x32_bf16 a[4:7],a[108:111],v[228:231],a[4:7]
buffer_load_dwordx4 v[216:219],v6,s[28:31],s78 offen offset:2048 sc1
v_mfma_f32_16x16x32_bf16 a[4:7],a[104:107],v[196:199],a[4:7]
v_mfma_f32_16x16x32_bf16 a[4:7],a[100:103],v[164:167],a[4:7]
v_mfma_f32_16x16x32_bf16 a[4:7],a[96:99],v[132:135],a[4:7]
v_mfma_f32_16x16x32_bf16 a[8:11],a[96:99],v[144:147],a[8:11]
v_mfma_f32_16x16x32_bf16 a[8:11],a[100:103],v[176:179],a[8:11]
buffer_load_dwordx4 v[220:223],v6,s[28:31],s78 offen offset:3072 sc1
v_mfma_f32_16x16x32_bf16 a[8:11],a[104:107],v[208:211],a[8:11]
s_mul_i32 s78,s70,12
s_add_u32 s78,s78,8
s_add_u32 s78,s78,s40
v_mfma_f32_16x16x32_bf16 a[8:11],a[108:111],v[240:243],a[8:11]
s_mul_i32 s78,s78,5120
s_max_i32 s78,s78,s101
v_mfma_f32_16x16x32_bf16 a[16:19],a[112:115],v[128:131],a[16:19]
v_mfma_f32_16x16x32_bf16 a[16:19],a[116:119],v[160:163],a[16:19]
v_mfma_f32_16x16x32_bf16 a[16:19],a[120:123],v[192:195],a[16:19]
buffer_load_dwordx4 v[232:235],v6,s[28:31],s79 offen sc1
v_mfma_f32_16x16x32_bf16 a[16:19],a[124:127],v[224:227],a[16:19]
v_mfma_f32_16x16x32_bf16 a[20:23],a[124:127],v[228:231],a[20:23]
v_mfma_f32_16x16x32_bf16 a[20:23],a[120:123],v[196:199],a[20:23]
v_mfma_f32_16x16x32_bf16 a[20:23],a[116:119],v[164:167],a[20:23]
v_mfma_f32_16x16x32_bf16 a[20:23],a[112:115],v[132:135],a[20:23]
buffer_load_dwordx4 v[236:239],v6,s[28:31],s79 offen offset:1024 sc1
v_mfma_f32_16x16x32_bf16 a[24:27],a[112:115],v[144:147],a[24:27]
v_mfma_f32_16x16x32_bf16 a[24:27],a[116:119],v[176:179],a[24:27]
v_mfma_f32_16x16x32_bf16 a[24:27],a[120:123],v[208:211],a[24:27]
v_mfma_f32_16x16x32_bf16 a[24:27],a[124:127],v[240:243],a[24:27]
v_mfma_f32_16x16x32_bf16 a[32:35],a[128:131],v[128:131],a[32:35]
buffer_load_dwordx4 v[248:251],v6,s[28:31],s79 offen offset:2048 sc1
v_mfma_f32_16x16x32_bf16 a[32:35],a[132:135],v[160:163],a[32:35]
v_mfma_f32_16x16x32_bf16 a[32:35],a[136:139],v[192:195],a[32:35]
v_mfma_f32_16x16x32_bf16 a[32:35],a[140:143],v[224:227],a[32:35]
v_mfma_f32_16x16x32_bf16 a[36:39],a[140:143],v[228:231],a[36:39]
v_mfma_f32_16x16x32_bf16 a[36:39],a[136:139],v[196:199],a[36:39]
buffer_load_dwordx4 v[252:255],v6,s[28:31],s79 offen offset:3072 sc1
v_mfma_f32_16x16x32_bf16 a[36:39],a[132:135],v[164:167],a[36:39]
v_mfma_f32_16x16x32_bf16 a[36:39],a[128:131],v[132:135],a[36:39]
v_mfma_f32_16x16x32_bf16 a[40:43],a[128:131],v[144:147],a[40:43]
v_mfma_f32_16x16x32_bf16 a[40:43],a[132:135],v[176:179],a[40:43]
v_mfma_f32_16x16x32_bf16 a[40:43],a[136:139],v[208:211],a[40:43]
s_waitcnt vmcnt(15)
v_mfma_f32_16x16x32_bf16 a[40:43],a[140:143],v[240:243],a[40:43]
s_barrier
ds_read_b64_tr_b16 a[96:97],v42
ds_read_b64_tr_b16 a[98:99],v42 offset:5120
s_waitcnt lgkmcnt(2)
v_mfma_f32_16x16x32_bf16 a[48:51],a[144:147],v[128:131],a[48:51]
ds_read_b64_tr_b16 a[100:101],v42 offset:10240
v_mfma_f32_16x16x32_bf16 a[48:51],a[148:151],v[160:163],a[48:51]
ds_read_b64_tr_b16 a[102:103],v42 offset:15360
v_mfma_f32_16x16x32_bf16 a[48:51],a[152:155],v[192:195],a[48:51]
ds_read_b64_tr_b16 a[104:105],v42 offset:20480
v_mfma_f32_16x16x32_bf16 a[48:51],a[156:159],v[224:227],a[48:51]
ds_read_b64_tr_b16 a[106:107],v42 offset:25600
ds_read_b64_tr_b16 a[108:109],v42 offset:30720
v_mfma_f32_16x16x32_bf16 a[52:55],a[156:159],v[228:231],a[52:55]
ds_read_b64_tr_b16 a[110:111],v42 offset:35840
v_mfma_f32_16x16x32_bf16 a[52:55],a[152:155],v[196:199],a[52:55]
ds_read_b64_tr_b16 a[112:113],v44
v_mfma_f32_16x16x32_bf16 a[52:55],a[148:151],v[164:167],a[52:55]
ds_read_b64_tr_b16 a[114:115],v44 offset:5120
v_mfma_f32_16x16x32_bf16 a[52:55],a[144:147],v[132:135],a[52:55]
ds_read_b64_tr_b16 a[116:117],v44 offset:10240
ds_read_b64_tr_b16 a[118:119],v44 offset:15360
v_mfma_f32_16x16x32_bf16 a[56:59],a[144:147],v[144:147],a[56:59]
ds_read_b64_tr_b16 a[120:121],v44 offset:20480
v_mfma_f32_16x16x32_bf16 a[56:59],a[148:151],v[176:179],a[56:59]
ds_read_b64_tr_b16 a[122:123],v44 offset:25600
v_mfma_f32_16x16x32_bf16 a[56:59],a[152:155],v[208:211],a[56:59]
ds_read_b64_tr_b16 a[124:125],v44 offset:30720
v_mfma_f32_16x16x32_bf16 a[56:59],a[156:159],v[240:243],a[56:59]
ds_read_b64_tr_b16 a[126:127],v44 offset:35840
ds_read_b64_tr_b16 a[128:129],v43 offset:32
v_mfma_f32_16x16x32_bf16 a[64:67],a[160:163],v[128:131],a[64:67]
ds_read_b64_tr_b16 a[130:131],v43 offset:5152
v_mfma_f32_16x16x32_bf16 a[64:67],a[164:167],v[160:163],a[64:67]
ds_read_b64_tr_b16 a[132:133],v43 offset:10272
v_mfma_f32_16x16x32_bf16 a[64:67],a[168:171],v[192:195],a[64:67]
ds_read_b64_tr_b16 a[134:135],v43 offset:15392
v_mfma_f32_16x16x32_bf16 a[64:67],a[172:175],v[224:227],a[64:67]
ds_read_b64_tr_b16 a[136:137],v43 offset:20512
ds_read_b64_tr_b16 a[138:139],v43 offset:25632
v_mfma_f32_16x16x32_bf16 a[68:71],a[172:175],v[228:231],a[68:71]
ds_read_b64_tr_b16 a[140:141],v43 offset:30752
v_mfma_f32_16x16x32_bf16 a[68:71],a[168:171],v[196:199],a[68:71]
ds_read_b64_tr_b16 a[142:143],v43 offset:35872
v_mfma_f32_16x16x32_bf16 a[68:71],a[164:167],v[164:167],a[68:71]
ds_read_b64_tr_b16 a[144:145],v45 offset:32
v_mfma_f32_16x16x32_bf16 a[68:71],a[160:163],v[132:135],a[68:71]
ds_read_b64_tr_b16 a[146:147],v45 offset:5152
ds_read_b64_tr_b16 a[148:149],v45 offset:10272
v_mfma_f32_16x16x32_bf16 a[72:75],a[160:163],v[144:147],a[72:75]
ds_read_b64_tr_b16 a[150:151],v45 offset:15392
v_mfma_f32_16x16x32_bf16 a[72:75],a[164:167],v[176:179],a[72:75]
ds_read_b64_tr_b16 a[152:153],v45 offset:20512
v_mfma_f32_16x16x32_bf16 a[72:75],a[168:171],v[208:211],a[72:75]
ds_read_b64_tr_b16 a[154:155],v45 offset:25632
v_mfma_f32_16x16x32_bf16 a[72:75],a[172:175],v[240:243],a[72:75]
ds_read_b64_tr_b16 a[156:157],v45 offset:30752
ds_read_b64_tr_b16 a[158:159],v45 offset:35872
v_mfma_f32_16x16x32_bf16 a[80:83],a[176:179],v[128:131],a[80:83]
ds_read_b64_tr_b16 a[160:161],v42 offset:64
v_mfma_f32_16x16x32_bf16 a[80:83],a[180:183],v[160:163],a[80:83]
ds_read_b64_tr_b16 a[162:163],v42 offset:5184
v_mfma_f32_16x16x32_bf16 a[80:83],a[184:187],v[192:195],a[80:83]
ds_read_b64_tr_b16 a[164:165],v42 offset:10304
v_mfma_f32_16x16x32_bf16 a[80:83],a[188:191],v[224:227],a[80:83]
ds_read_b64_tr_b16 a[166:167],v42 offset:15424
ds_read_b64_tr_b16 a[168:169],v42 offset:20544
v_mfma_f32_16x16x32_bf16 a[84:87],a[188:191],v[228:231],a[84:87]
ds_read_b64_tr_b16 a[170:171],v42 offset:25664
v_mfma_f32_16x16x32_bf16 a[84:87],a[184:187],v[196:199],a[84:87]
ds_read_b64_tr_b16 a[172:173],v42 offset:30784
v_mfma_f32_16x16x32_bf16 a[84:87],a[180:183],v[164:167],a[84:87]
ds_read_b64_tr_b16 a[174:175],v42 offset:35904
v_mfma_f32_16x16x32_bf16 a[84:87],a[176:179],v[132:135],a[84:87]
v_mfma_f32_16x16x32_bf16 a[88:91],a[176:179],v[144:147],a[88:91]
ds_read_b64_tr_b16 a[176:177],v44 offset:64
ds_read_b64_tr_b16 a[178:179],v44 offset:5184
v_mfma_f32_16x16x32_bf16 a[88:91],a[180:183],v[176:179],a[88:91]
ds_read_b64_tr_b16 a[180:181],v44 offset:10304
v_mfma_f32_16x16x32_bf16 a[88:91],a[184:187],v[208:211],a[88:91]
ds_read_b64_tr_b16 a[182:183],v44 offset:15424
v_mfma_f32_16x16x32_bf16 a[88:91],a[188:191],v[240:243],a[88:91]
s_waitcnt lgkmcnt(7)
ds_read_b64_tr_b16 a[184:185],v44 offset:20544
v_mfma_f32_16x16x32_bf16 a[192:195],a[96:99],v[64:67],a[192:195]
ds_read_b64_tr_b16 a[186:187],v44 offset:25664
ds_read_b64_tr_b16 a[188:189],v44 offset:30784
v_mfma_f32_16x16x32_bf16 a[192:195],a[100:103],v[68:71],a[192:195]
ds_read_b64_tr_b16 a[190:191],v44 offset:35904
v_mfma_f32_16x16x32_bf16 a[192:195],a[104:107],v[72:75],a[192:195]
v_mfma_f32_16x16x32_bf16 a[192:195],a[108:111],v[76:79],a[192:195]
v_mfma_f32_16x16x32_bf16 a[196:199],a[108:111],v[92:95],a[196:199]
)A"
R"A(v_mfma_f32_16x16x32_bf16 a[196:199],a[104:107],v[88:91],a[196:199]
v_mfma_f32_16x16x32_bf16 a[196:199],a[100:103],v[84:87],a[196:199]
v_mfma_f32_16x16x32_bf16 a[196:199],a[96:99],v[80:83],a[196:199]
v_mfma_f32_16x16x32_bf16 a[200:203],a[96:99],v[96:99],a[200:203]
ds_read_b64_tr_b16 a[96:97],v43 offset:96
ds_read_b64_tr_b16 a[98:99],v43 offset:5216
v_mfma_f32_16x16x32_bf16 a[200:203],a[100:103],v[100:103],a[200:203]
ds_read_b64_tr_b16 a[100:101],v43 offset:10336
v_mfma_f32_16x16x32_bf16 a[200:203],a[104:107],v[104:107],a[200:203]
ds_read_b64_tr_b16 a[102:103],v43 offset:15456
v_mfma_f32_16x16x32_bf16 a[200:203],a[108:111],v[108:111],a[200:203]
ds_read_b64_tr_b16 a[104:105],v43 offset:20576
v_mfma_f32_16x16x32_bf16 a[192:195],a[112:115],v[136:139],a[192:195]
s_waitcnt vmcnt(3)
ds_read_b64_tr_b16 a[106:107],v43 offset:25696
ds_read_b64_tr_b16 a[108:109],v43 offset:30816
v_mfma_f32_16x16x32_bf16 a[192:195],a[116:119],v[140:143],a[192:195]
ds_read_b64_tr_b16 a[110:111],v43 offset:35936
v_mfma_f32_16x16x32_bf16 a[192:195],a[120:123],v[152:155],a[192:195]
v_mfma_f32_16x16x32_bf16 a[192:195],a[124:127],v[156:159],a[192:195]
v_mfma_f32_16x16x32_bf16 a[196:199],a[124:127],v[188:191],a[196:199]
v_mfma_f32_16x16x32_bf16 a[196:199],a[120:123],v[184:187],a[196:199]
v_mfma_f32_16x16x32_bf16 a[196:199],a[116:119],v[172:175],a[196:199]
v_mfma_f32_16x16x32_bf16 a[196:199],a[112:115],v[168:171],a[196:199]
v_mfma_f32_16x16x32_bf16 a[200:203],a[112:115],v[200:203],a[200:203]
ds_read_b64_tr_b16 a[112:113],v45 offset:96
ds_read_b64_tr_b16 a[114:115],v45 offset:5216
v_mfma_f32_16x16x32_bf16 a[200:203],a[116:119],v[204:207],a[200:203]
v_accvgpr_read_b32 v128,a192
v_accvgpr_read_b32 v129,a193
v_mfma_f32_16x16x32_bf16 a[200:203],a[120:123],v[216:219],a[200:203]
v_cvt_pk_bf16_f32 v128,v128,v129
v_accvgpr_read_b32 v130,a194
v_mfma_f32_16x16x32_bf16 a[200:203],a[124:127],v[220:223],a[200:203]
v_accvgpr_read_b32 v131,a195
v_cvt_pk_bf16_f32 v129,v130,v131
v_mfma_f32_16x16x32_bf16 a[208:211],a[128:131],v[64:67],a[208:211]
ds_read_b64_tr_b16 a[116:117],v45 offset:10336
ds_read_b64_tr_b16 a[118:119],v45 offset:15456
v_mfma_f32_16x16x32_bf16 a[208:211],a[132:135],v[68:71],a[208:211]
v_accvgpr_read_b32 v132,a196
v_accvgpr_read_b32 v133,a197
v_mfma_f32_16x16x32_bf16 a[208:211],a[136:139],v[72:75],a[208:211]
v_cvt_pk_bf16_f32 v132,v132,v133
v_accvgpr_read_b32 v134,a198
v_mfma_f32_16x16x32_bf16 a[208:211],a[140:143],v[76:79],a[208:211]
v_accvgpr_read_b32 v135,a199
v_cvt_pk_bf16_f32 v133,v134,v135
v_mfma_f32_16x16x32_bf16 a[212:215],a[140:143],v[92:95],a[212:215]
ds_read_b64_tr_b16 a[120:121],v45 offset:20576
ds_read_b64_tr_b16 a[122:123],v45 offset:25696
v_mfma_f32_16x16x32_bf16 a[212:215],a[136:139],v[88:91],a[212:215]
ds_read_b64_tr_b16 a[124:125],v45 offset:30816
v_accvgpr_read_b32 v144,a200
v_mfma_f32_16x16x32_bf16 a[212:215],a[132:135],v[84:87],a[212:215]
ds_read_b64_tr_b16 a[126:127],v45 offset:35936
v_accvgpr_read_b32 v145,a201
v_mfma_f32_16x16x32_bf16 a[212:215],a[128:131],v[80:83],a[212:215]
v_cvt_pk_bf16_f32 v144,v144,v145
v_accvgpr_read_b32 v146,a202
v_mfma_f32_16x16x32_bf16 a[216:219],a[128:131],v[96:99],a[216:219]
v_accvgpr_read_b32 v147,a203
v_cvt_pk_bf16_f32 v145,v146,v147
v_mfma_f32_16x16x32_bf16 a[216:219],a[132:135],v[100:103],a[216:219]
ds_read_b64_tr_b16 a[128:129],v42 offset:128
ds_read_b64_tr_b16 a[130:131],v42 offset:5248
v_mfma_f32_16x16x32_bf16 a[216:219],a[136:139],v[104:107],a[216:219]
ds_read_b64_tr_b16 a[132:133],v42 offset:10368
v_mfma_f32_16x16x32_bf16 a[216:219],a[140:143],v[108:111],a[216:219]
ds_read_b64_tr_b16 a[134:135],v42 offset:15488
v_mfma_f32_16x16x32_bf16 a[208:211],a[144:147],v[136:139],a[208:211]
ds_read_b64_tr_b16 a[136:137],v42 offset:20608
v_mfma_f32_16x16x32_bf16 a[208:211],a[148:151],v[140:143],a[208:211]
ds_read_b64_tr_b16 a[138:139],v42 offset:25728
ds_read_b64_tr_b16 a[140:141],v42 offset:30848
v_mfma_f32_16x16x32_bf16 a[208:211],a[152:155],v[152:155],a[208:211]
ds_read_b64_tr_b16 a[142:143],v42 offset:35968
v_mfma_f32_16x16x32_bf16 a[208:211],a[156:159],v[156:159],a[208:211]
v_mfma_f32_16x16x32_bf16 a[212:215],a[156:159],v[188:191],a[212:215]
v_mfma_f32_16x16x32_bf16 a[212:215],a[152:155],v[184:187],a[212:215]
v_mfma_f32_16x16x32_bf16 a[212:215],a[148:151],v[172:175],a[212:215]
v_mfma_f32_16x16x32_bf16 a[212:215],a[144:147],v[168:171],a[212:215]
v_mfma_f32_16x16x32_bf16 a[216:219],a[144:147],v[200:203],a[216:219]
ds_read_b64_tr_b16 a[144:145],v44 offset:128
ds_read_b64_tr_b16 a[146:147],v44 offset:5248
v_mfma_f32_16x16x32_bf16 a[216:219],a[148:151],v[204:207],a[216:219]
v_accvgpr_read_b32 v130,a208
v_accvgpr_read_b32 v131,a209
v_mfma_f32_16x16x32_bf16 a[216:219],a[152:155],v[216:219],a[216:219]
v_accvgpr_read_b32 v12,a210
v_accvgpr_read_b32 v13,a211
v_mfma_f32_16x16x32_bf16 a[216:219],a[156:159],v[220:223],a[216:219]
v_cvt_pk_bf16_f32 v130,v130,v131
v_cvt_pk_bf16_f32 v131,v12,v13
v_mfma_f32_16x16x32_bf16 a[224:227],a[160:163],v[64:67],a[224:227]
buffer_store_dwordx4 v[128:131],v6,s[32:35],s84 offen sc1
v_mfma_f32_16x16x32_bf16 a[224:227],a[164:167],v[68:71],a[224:227]
s_waitcnt lgkmcnt(2)
v_accvgpr_read_b32 v134,a212
v_accvgpr_read_b32 v135,a213
v_mfma_f32_16x16x32_bf16 a[224:227],a[168:171],v[72:75],a[224:227]
v_accvgpr_read_b32 v14,a214
v_accvgpr_read_b32 v15,a215
v_mfma_f32_16x16x32_bf16 a[224:227],a[172:175],v[76:79],a[224:227]
v_cvt_pk_bf16_f32 v134,v134,v135
v_cvt_pk_bf16_f32 v135,v14,v15
v_mfma_f32_16x16x32_bf16 a[228:231],a[172:175],v[92:95],a[228:231]
buffer_store_dwordx4 v[132:135],v6,s[32:35],s85 offen sc1
v_mfma_f32_16x16x32_bf16 a[228:231],a[168:171],v[88:91],a[228:231]
v_accvgpr_read_b32 v146,a216
v_accvgpr_read_b32 v147,a217
v_mfma_f32_16x16x32_bf16 a[228:231],a[164:167],v[84:87],a[228:231]
v_accvgpr_read_b32 v16,a218
v_accvgpr_read_b32 v17,a219
v_mfma_f32_16x16x32_bf16 a[228:231],a[160:163],v[80:83],a[228:231]
v_cvt_pk_bf16_f32 v146,v146,v147
v_cvt_pk_bf16_f32 v147,v16,v17
v_mfma_f32_16x16x32_bf16 a[232:235],a[160:163],v[96:99],a[232:235]
buffer_store_dwordx4 v[144:147],v6,s[32:35],s86 offen sc1
v_mfma_f32_16x16x32_bf16 a[232:235],a[164:167],v[100:103],a[232:235]
ds_read_b64_tr_b16 a[148:149],v44 offset:10368
ds_read_b64_tr_b16 a[150:151],v44 offset:15488
v_mfma_f32_16x16x32_bf16 a[232:235],a[168:171],v[104:107],a[232:235]
ds_read_b64_tr_b16 a[152:153],v44 offset:20608
v_mfma_f32_16x16x32_bf16 a[232:235],a[172:175],v[108:111],a[232:235]
ds_read_b64_tr_b16 a[154:155],v44 offset:25728
v_mfma_f32_16x16x32_bf16 a[224:227],a[176:179],v[136:139],a[224:227]
ds_read_b64_tr_b16 a[156:157],v44 offset:30848
v_mfma_f32_16x16x32_bf16 a[224:227],a[180:183],v[140:143],a[224:227]
ds_read_b64_tr_b16 a[158:159],v44 offset:35968
ds_read_b64_tr_b16 a[160:161],v43 offset:160
v_mfma_f32_16x16x32_bf16 a[224:227],a[184:187],v[152:155],a[224:227]
ds_read_b64_tr_b16 a[162:163],v43 offset:5280
v_mfma_f32_16x16x32_bf16 a[224:227],a[188:191],v[156:159],a[224:227]
ds_read_b64_tr_b16 a[164:165],v43 offset:10400
v_mfma_f32_16x16x32_bf16 a[228:231],a[188:191],v[188:191],a[228:231]
ds_read_b64_tr_b16 a[166:167],v43 offset:15520
v_mfma_f32_16x16x32_bf16 a[228:231],a[184:187],v[184:187],a[228:231]
ds_read_b64_tr_b16 a[168:169],v43 offset:20640
ds_read_b64_tr_b16 a[170:171],v43 offset:25760
v_mfma_f32_16x16x32_bf16 a[228:231],a[180:183],v[172:175],a[228:231]
ds_read_b64_tr_b16 a[172:173],v43 offset:30880
v_mfma_f32_16x16x32_bf16 a[228:231],a[176:179],v[168:171],a[228:231]
ds_read_b64_tr_b16 a[174:175],v43 offset:36000
v_mfma_f32_16x16x32_bf16 a[232:235],a[176:179],v[200:203],a[232:235]
ds_read_b64_tr_b16 a[176:177],v45 offset:160
v_mfma_f32_16x16x32_bf16 a[232:235],a[180:183],v[204:207],a[232:235]
v_accvgpr_read_b32 v160,a224
v_accvgpr_read_b32 v161,a225
v_mfma_f32_16x16x32_bf16 a[232:235],a[184:187],v[216:219],a[232:235]
v_cvt_pk_bf16_f32 v160,v160,v161
v_accvgpr_read_b32 v162,a226
v_mfma_f32_16x16x32_bf16 a[232:235],a[188:191],v[220:223],a[232:235]
v_accvgpr_read_b32 v163,a227
v_cvt_pk_bf16_f32 v161,v162,v163
v_mfma_f32_16x16x32_bf16 a[240:243],a[96:99],v[64:67],a[240:243]
ds_read_b64_tr_b16 a[178:179],v45 offset:5280
ds_read_b64_tr_b16 a[180:181],v45 offset:10400
v_mfma_f32_16x16x32_bf16 a[240:243],a[100:103],v[68:71],a[240:243]
v_accvgpr_read_b32 v164,a228
v_accvgpr_read_b32 v165,a229
v_mfma_f32_16x16x32_bf16 a[240:243],a[104:107],v[72:75],a[240:243]
v_cvt_pk_bf16_f32 v164,v164,v165
v_accvgpr_read_b32 v166,a230
v_mfma_f32_16x16x32_bf16 a[240:243],a[108:111],v[76:79],a[240:243]
v_accvgpr_read_b32 v167,a231
v_cvt_pk_bf16_f32 v165,v166,v167
v_mfma_f32_16x16x32_bf16 a[244:247],a[108:111],v[92:95],a[244:247]
ds_read_b64_tr_b16 a[182:183],v45 offset:15520
ds_read_b64_tr_b16 a[184:185],v45 offset:20640
v_mfma_f32_16x16x32_bf16 a[244:247],a[104:107],v[88:91],a[244:247]
v_accvgpr_read_b32 v176,a232
v_accvgpr_read_b32 v177,a233
v_mfma_f32_16x16x32_bf16 a[244:247],a[100:103],v[84:87],a[244:247]
v_cvt_pk_bf16_f32 v176,v176,v177
v_accvgpr_read_b32 v178,a234
v_mfma_f32_16x16x32_bf16 a[244:247],a[96:99],v[80:83],a[244:247]
v_accvgpr_read_b32 v179,a235
v_cvt_pk_bf16_f32 v177,v178,v179
v_mfma_f32_16x16x32_bf16 a[248:251],a[96:99],v[96:99],a[248:251]
ds_read_b64_tr_b16 a[186:187],v45 offset:25760
ds_read_b64_tr_b16 a[188:189],v45 offset:30880
v_mfma_f32_16x16x32_bf16 a[248:251],a[100:103],v[100:103],a[248:251]
ds_read_b64_tr_b16 a[190:191],v45 offset:36000
v_mfma_f32_16x16x32_bf16 a[248:251],a[104:107],v[104:107],a[248:251]
ds_read_b64_tr_b16 a[96:97],v42 offset:192
v_mfma_f32_16x16x32_bf16 a[248:251],a[108:111],v[108:111],a[248:251]
ds_read_b64_tr_b16 a[98:99],v42 offset:5312
v_mfma_f32_16x16x32_bf16 a[240:243],a[112:115],v[136:139],a[240:243]
ds_read_b64_tr_b16 a[100:101],v42 offset:10432
ds_read_b64_tr_b16 a[102:103],v42 offset:15552
v_mfma_f32_16x16x32_bf16 a[240:243],a[116:119],v[140:143],a[240:243]
ds_read_b64_tr_b16 a[104:105],v42 offset:20672
v_mfma_f32_16x16x32_bf16 a[240:243],a[120:123],v[152:155],a[240:243]
ds_read_b64_tr_b16 a[106:107],v42 offset:25792
v_mfma_f32_16x16x32_bf16 a[240:243],a[124:127],v[156:159],a[240:243]
ds_read_b64_tr_b16 a[108:109],v42 offset:30912
v_mfma_f32_16x16x32_bf16 a[244:247],a[124:127],v[188:191],a[244:247]
ds_read_b64_tr_b16 a[110:111],v42 offset:36032
v_mfma_f32_16x16x32_bf16 a[244:247],a[120:123],v[184:187],a[244:247]
v_mfma_f32_16x16x32_bf16 a[244:247],a[116:119],v[172:175],a[244:247]
v_mfma_f32_16x16x32_bf16 a[244:247],a[112:115],v[168:171],a[244:247]
v_mfma_f32_16x16x32_bf16 a[248:251],a[112:115],v[200:203],a[248:251]
ds_read_b64_tr_b16 a[112:113],v44 offset:192
ds_read_b64_tr_b16 a[114:115],v44 offset:5312
v_mfma_f32_16x16x32_bf16 a[248:251],a[116:119],v[204:207],a[248:251]
v_accvgpr_read_b32 v162,a240
v_accvgpr_read_b32 v163,a241
v_mfma_f32_16x16x32_bf16 a[248:251],a[120:123],v[216:219],a[248:251]
v_accvgpr_read_b32 v12,a242
v_accvgpr_read_b32 v13,a243
v_mfma_f32_16x16x32_bf16 a[248:251],a[124:127],v[220:223],a[248:251]
v_cvt_pk_bf16_f32 v162,v162,v163
v_cvt_pk_bf16_f32 v163,v12,v13
v_mfma_f32_16x16x32_bf16 a[0:3],a[128:131],v[64:67],a[0:3]
buffer_store_dwordx4 v[160:163],v6,s[32:35],s84 offen offset:1024 sc1
v_mfma_f32_16x16x32_bf16 a[0:3],a[132:135],v[68:71],a[0:3]
v_accvgpr_read_b32 v166,a244
v_accvgpr_read_b32 v167,a245
v_mfma_f32_16x16x32_bf16 a[0:3],a[136:139],v[72:75],a[0:3]
v_accvgpr_read_b32 v14,a246
v_accvgpr_read_b32 v15,a247
v_mfma_f32_16x16x32_bf16 a[0:3],a[140:143],v[76:79],a[0:3]
v_cvt_pk_bf16_f32 v166,v166,v167
v_cvt_pk_bf16_f32 v167,v14,v15
v_mfma_f32_16x16x32_bf16 a[4:7],a[140:143],v[92:95],a[4:7]
buffer_store_dwordx4 v[164:167],v6,s[32:35],s85 offen offset:1024 sc1
v_mfma_f32_16x16x32_bf16 a[4:7],a[136:139],v[88:91],a[4:7]
v_accvgpr_read_b32 v178,a248
v_accvgpr_read_b32 v179,a249
v_mfma_f32_16x16x32_bf16 a[4:7],a[132:135],v[84:87],a[4:7]
v_accvgpr_read_b32 v16,a250
v_accvgpr_read_b32 v17,a251
v_cvt_pk_bf16_f32 v178,v178,v179
v_cvt_pk_bf16_f32 v179,v16,v17
v_mfma_f32_16x16x32_bf16 a[4:7],a[128:131],v[80:83],a[4:7]
ds_read_b64_tr_b16 a[116:117],v44 offset:10432
ds_read_b64_tr_b16 a[118:119],v44 offset:15552
v_mfma_f32_16x16x32_bf16 a[8:11],a[128:131],v[96:99],a[8:11]
)A"
R"A(buffer_store_dwordx4 v[176:179],v6,s[32:35],s86 offen offset:1024 sc1
v_mfma_f32_16x16x32_bf16 a[8:11],a[132:135],v[100:103],a[8:11]
ds_read_b64_tr_b16 a[120:121],v44 offset:20672
ds_read_b64_tr_b16 a[122:123],v44 offset:25792
v_mfma_f32_16x16x32_bf16 a[8:11],a[136:139],v[104:107],a[8:11]
ds_read_b64_tr_b16 a[124:125],v44 offset:30912
v_mfma_f32_16x16x32_bf16 a[8:11],a[140:143],v[108:111],a[8:11]
s_waitcnt lgkmcnt(5)
ds_read_b64_tr_b16 a[126:127],v44 offset:36032
v_mfma_f32_16x16x32_bf16 a[0:3],a[144:147],v[136:139],a[0:3]
ds_read_b64_tr_b16 a[128:129],v43 offset:224
v_mfma_f32_16x16x32_bf16 a[0:3],a[148:151],v[140:143],a[0:3]
ds_read_b64_tr_b16 a[130:131],v43 offset:5344
ds_read_b64_tr_b16 a[132:133],v43 offset:10464
v_mfma_f32_16x16x32_bf16 a[0:3],a[152:155],v[152:155],a[0:3]
ds_read_b64_tr_b16 a[134:135],v43 offset:15584
v_mfma_f32_16x16x32_bf16 a[0:3],a[156:159],v[156:159],a[0:3]
ds_read_b64_tr_b16 a[136:137],v43 offset:20704
v_mfma_f32_16x16x32_bf16 a[4:7],a[156:159],v[188:191],a[4:7]
ds_read_b64_tr_b16 a[138:139],v43 offset:25824
v_mfma_f32_16x16x32_bf16 a[4:7],a[152:155],v[184:187],a[4:7]
ds_read_b64_tr_b16 a[140:141],v43 offset:30944
ds_read_b64_tr_b16 a[142:143],v43 offset:36064
v_mfma_f32_16x16x32_bf16 a[4:7],a[148:151],v[172:175],a[4:7]
v_mfma_f32_16x16x32_bf16 a[4:7],a[144:147],v[168:171],a[4:7]
v_mfma_f32_16x16x32_bf16 a[8:11],a[144:147],v[200:203],a[8:11]
ds_read_b64_tr_b16 a[144:145],v45 offset:224
ds_read_b64_tr_b16 a[146:147],v45 offset:5344
v_mfma_f32_16x16x32_bf16 a[8:11],a[148:151],v[204:207],a[8:11]
v_accvgpr_read_b32 v192,a0
v_accvgpr_read_b32 v193,a1
v_mfma_f32_16x16x32_bf16 a[8:11],a[152:155],v[216:219],a[8:11]
v_cvt_pk_bf16_f32 v192,v192,v193
v_accvgpr_read_b32 v194,a2
v_mfma_f32_16x16x32_bf16 a[8:11],a[156:159],v[220:223],a[8:11]
v_accvgpr_read_b32 v195,a3
v_cvt_pk_bf16_f32 v193,v194,v195
v_mfma_f32_16x16x32_bf16 a[16:19],a[160:163],v[64:67],a[16:19]
ds_read_b64_tr_b16 a[148:149],v45 offset:10464
ds_read_b64_tr_b16 a[150:151],v45 offset:15584
v_mfma_f32_16x16x32_bf16 a[16:19],a[164:167],v[68:71],a[16:19]
v_accvgpr_read_b32 v196,a4
v_accvgpr_read_b32 v197,a5
v_mfma_f32_16x16x32_bf16 a[16:19],a[168:171],v[72:75],a[16:19]
v_cvt_pk_bf16_f32 v196,v196,v197
v_accvgpr_read_b32 v198,a6
v_mfma_f32_16x16x32_bf16 a[16:19],a[172:175],v[76:79],a[16:19]
v_accvgpr_read_b32 v199,a7
v_cvt_pk_bf16_f32 v197,v198,v199
v_mfma_f32_16x16x32_bf16 a[20:23],a[172:175],v[92:95],a[20:23]
ds_read_b64_tr_b16 a[152:153],v45 offset:20704
ds_read_b64_tr_b16 a[154:155],v45 offset:25824
v_mfma_f32_16x16x32_bf16 a[20:23],a[168:171],v[88:91],a[20:23]
ds_read_b64_tr_b16 a[156:157],v45 offset:30944
v_accvgpr_read_b32 v208,a8
v_mfma_f32_16x16x32_bf16 a[20:23],a[164:167],v[84:87],a[20:23]
ds_read_b64_tr_b16 a[158:159],v45 offset:36064
v_accvgpr_read_b32 v209,a9
v_mfma_f32_16x16x32_bf16 a[20:23],a[160:163],v[80:83],a[20:23]
v_cvt_pk_bf16_f32 v208,v208,v209
v_accvgpr_read_b32 v210,a10
v_mfma_f32_16x16x32_bf16 a[24:27],a[160:163],v[96:99],a[24:27]
v_accvgpr_read_b32 v211,a11
v_cvt_pk_bf16_f32 v209,v210,v211
v_mfma_f32_16x16x32_bf16 a[24:27],a[164:167],v[100:103],a[24:27]
ds_read_b64_tr_b16 a[160:161],v42 offset:256
ds_read_b64_tr_b16 a[162:163],v42 offset:5376
v_mfma_f32_16x16x32_bf16 a[24:27],a[168:171],v[104:107],a[24:27]
ds_read_b64_tr_b16 a[164:165],v42 offset:10496
v_mfma_f32_16x16x32_bf16 a[24:27],a[172:175],v[108:111],a[24:27]
ds_read_b64_tr_b16 a[166:167],v42 offset:15616
v_mfma_f32_16x16x32_bf16 a[16:19],a[176:179],v[136:139],a[16:19]
ds_read_b64_tr_b16 a[168:169],v42 offset:20736
v_mfma_f32_16x16x32_bf16 a[16:19],a[180:183],v[140:143],a[16:19]
ds_read_b64_tr_b16 a[170:171],v42 offset:25856
ds_read_b64_tr_b16 a[172:173],v42 offset:30976
v_mfma_f32_16x16x32_bf16 a[16:19],a[184:187],v[152:155],a[16:19]
ds_read_b64_tr_b16 a[174:175],v42 offset:36096
v_mfma_f32_16x16x32_bf16 a[16:19],a[188:191],v[156:159],a[16:19]
v_mfma_f32_16x16x32_bf16 a[20:23],a[188:191],v[188:191],a[20:23]
v_mfma_f32_16x16x32_bf16 a[20:23],a[184:187],v[184:187],a[20:23]
v_mfma_f32_16x16x32_bf16 a[20:23],a[180:183],v[172:175],a[20:23]
v_mfma_f32_16x16x32_bf16 a[20:23],a[176:179],v[168:171],a[20:23]
v_mfma_f32_16x16x32_bf16 a[24:27],a[176:179],v[200:203],a[24:27]
ds_read_b64_tr_b16 a[176:177],v44 offset:256
ds_read_b64_tr_b16 a[178:179],v44 offset:5376
v_mfma_f32_16x16x32_bf16 a[24:27],a[180:183],v[204:207],a[24:27]
s_lshl_b32 s95,s42,2
s_add_u32 s95,s95,s40
s_lshl_b32 s95,s95,4
v_mfma_f32_16x16x32_bf16 a[24:27],a[184:187],v[216:219],a[24:27]
s_add_u32 s95,s95,5120
v_mov_b32 v18,s68
v_mov_b32 v19,s69
v_mfma_f32_16x16x32_bf16 a[24:27],a[188:191],v[220:223],a[24:27]
s_waitcnt vmcnt(0)
s_mov_b64 exec,1
buffer_store_dwordx2 v[18:19],v6,s[64:67],s95 offen sc1
s_mov_b64 exec,-1
v_mfma_f32_16x16x32_bf16 a[32:35],a[96:99],v[64:67],a[32:35]
v_accvgpr_read_b32 v194,a16
v_accvgpr_read_b32 v195,a17
v_mfma_f32_16x16x32_bf16 a[32:35],a[100:103],v[68:71],a[32:35]
v_accvgpr_read_b32 v12,a18
v_accvgpr_read_b32 v13,a19
v_mfma_f32_16x16x32_bf16 a[32:35],a[104:107],v[72:75],a[32:35]
v_cvt_pk_bf16_f32 v194,v194,v195
v_cvt_pk_bf16_f32 v195,v12,v13
v_mfma_f32_16x16x32_bf16 a[32:35],a[108:111],v[76:79],a[32:35]
buffer_store_dwordx4 v[192:195],v6,s[32:35],s84 offen offset:2048 sc1
v_mfma_f32_16x16x32_bf16 a[36:39],a[108:111],v[92:95],a[36:39]
v_accvgpr_read_b32 v198,a20
v_accvgpr_read_b32 v199,a21
v_mfma_f32_16x16x32_bf16 a[36:39],a[104:107],v[88:91],a[36:39]
v_accvgpr_read_b32 v14,a22
v_accvgpr_read_b32 v15,a23
v_mfma_f32_16x16x32_bf16 a[36:39],a[100:103],v[84:87],a[36:39]
v_cvt_pk_bf16_f32 v198,v198,v199
v_cvt_pk_bf16_f32 v199,v14,v15
v_mfma_f32_16x16x32_bf16 a[36:39],a[96:99],v[80:83],a[36:39]
buffer_store_dwordx4 v[196:199],v6,s[32:35],s85 offen offset:2048 sc1
v_mfma_f32_16x16x32_bf16 a[40:43],a[96:99],v[96:99],a[40:43]
v_accvgpr_read_b32 v210,a24
v_accvgpr_read_b32 v211,a25
v_mfma_f32_16x16x32_bf16 a[40:43],a[100:103],v[100:103],a[40:43]
v_accvgpr_read_b32 v16,a26
v_accvgpr_read_b32 v17,a27
v_mfma_f32_16x16x32_bf16 a[40:43],a[104:107],v[104:107],a[40:43]
v_cvt_pk_bf16_f32 v210,v210,v211
v_cvt_pk_bf16_f32 v211,v16,v17
v_mfma_f32_16x16x32_bf16 a[40:43],a[108:111],v[108:111],a[40:43]
buffer_store_dwordx4 v[208:211],v6,s[32:35],s86 offen offset:2048 sc1
v_mfma_f32_16x16x32_bf16 a[32:35],a[112:115],v[136:139],a[32:35]
s_waitcnt lgkmcnt(0)
ds_read_b64_tr_b16 a[180:181],v44 offset:10496
ds_read_b64_tr_b16 a[182:183],v44 offset:15616
v_mfma_f32_16x16x32_bf16 a[32:35],a[116:119],v[140:143],a[32:35]
ds_read_b64_tr_b16 a[184:185],v44 offset:20736
v_mfma_f32_16x16x32_bf16 a[32:35],a[120:123],v[152:155],a[32:35]
ds_read_b64_tr_b16 a[186:187],v44 offset:25856
v_mfma_f32_16x16x32_bf16 a[32:35],a[124:127],v[156:159],a[32:35]
ds_read_b64_tr_b16 a[188:189],v44 offset:30976
v_mfma_f32_16x16x32_bf16 a[36:39],a[124:127],v[188:191],a[36:39]
ds_read_b64_tr_b16 a[190:191],v44 offset:36096
ds_read_b64_tr_b16 a[96:97],v43 offset:288
v_mfma_f32_16x16x32_bf16 a[36:39],a[120:123],v[184:187],a[36:39]
ds_read_b64_tr_b16 a[98:99],v43 offset:5408
v_mfma_f32_16x16x32_bf16 a[36:39],a[116:119],v[172:175],a[36:39]
ds_read_b64_tr_b16 a[100:101],v43 offset:10528
v_mfma_f32_16x16x32_bf16 a[36:39],a[112:115],v[168:171],a[36:39]
ds_read_b64_tr_b16 a[102:103],v43 offset:15648
v_mfma_f32_16x16x32_bf16 a[40:43],a[112:115],v[200:203],a[40:43]
ds_read_b64_tr_b16 a[104:105],v43 offset:20768
ds_read_b64_tr_b16 a[106:107],v43 offset:25888
v_mfma_f32_16x16x32_bf16 a[40:43],a[116:119],v[204:207],a[40:43]
v_accvgpr_read_b32 v224,a32
v_accvgpr_read_b32 v225,a33
v_mfma_f32_16x16x32_bf16 a[40:43],a[120:123],v[216:219],a[40:43]
s_lshl_b32 s94,s71,2
v_cvt_pk_bf16_f32 v224,v224,v225
v_accvgpr_read_b32 v226,a34
v_mfma_f32_16x16x32_bf16 a[40:43],a[124:127],v[220:223],a[40:43]
s_add_u32 s94,s94,s40
v_accvgpr_read_b32 v227,a35
v_cvt_pk_bf16_f32 v225,v226,v227
v_mfma_f32_16x16x32_bf16 a[48:51],a[128:131],v[64:67],a[48:51]
s_lshl_b32 s94,s94,4
v_bfe_u32 v24,v6,4,2
v_min_u32 v24,2,v24
v_mfma_f32_16x16x32_bf16 a[48:51],a[132:135],v[68:71],a[48:51]
s_add_u32 s94,s94,5120
v_accvgpr_read_b32 v228,a36
v_accvgpr_read_b32 v229,a37
v_mfma_f32_16x16x32_bf16 a[48:51],a[136:139],v[72:75],a[48:51]
v_cvt_pk_bf16_f32 v228,v228,v229
v_accvgpr_read_b32 v230,a38
v_mfma_f32_16x16x32_bf16 a[48:51],a[140:143],v[76:79],a[48:51]
v_accvgpr_read_b32 v231,a39
v_cvt_pk_bf16_f32 v229,v230,v231
v_mfma_f32_16x16x32_bf16 a[52:55],a[140:143],v[92:95],a[52:55]
v_add_u32 v24,s70,v24
v_add_u32 v24,1,v24
v_mfma_f32_16x16x32_bf16 a[52:55],a[136:139],v[88:91],a[52:55]
v_accvgpr_read_b32 v240,a40
v_accvgpr_read_b32 v241,a41
v_mfma_f32_16x16x32_bf16 a[52:55],a[132:135],v[84:87],a[52:55]
v_cvt_pk_bf16_f32 v240,v240,v241
v_accvgpr_read_b32 v242,a42
v_mfma_f32_16x16x32_bf16 a[52:55],a[128:131],v[80:83],a[52:55]
v_accvgpr_read_b32 v243,a43
v_cvt_pk_bf16_f32 v241,v242,v243
v_mfma_f32_16x16x32_bf16 a[56:59],a[128:131],v[96:99],a[56:59]
v_and_b32 v24,3,v24
v_lshlrev_b32 v24,8,v24
v_mfma_f32_16x16x32_bf16 a[56:59],a[132:135],v[100:103],a[56:59]
ds_read_b64_tr_b16 a[108:109],v43 offset:31008
ds_read_b64_tr_b16 a[110:111],v43 offset:36128
v_mfma_f32_16x16x32_bf16 a[56:59],a[136:139],v[104:107],a[56:59]
ds_read_b64_tr_b16 a[112:113],v45 offset:288
v_mfma_f32_16x16x32_bf16 a[56:59],a[140:143],v[108:111],a[56:59]
ds_read_b64_tr_b16 a[114:115],v45 offset:5408
v_mfma_f32_16x16x32_bf16 a[48:51],a[144:147],v[136:139],a[48:51]
ds_read_b64_tr_b16 a[116:117],v45 offset:10528
v_mfma_f32_16x16x32_bf16 a[48:51],a[148:151],v[140:143],a[48:51]
ds_read_b64_tr_b16 a[118:119],v45 offset:15648
ds_read_b64_tr_b16 a[120:121],v45 offset:20768
v_mfma_f32_16x16x32_bf16 a[48:51],a[152:155],v[152:155],a[48:51]
ds_read_b64_tr_b16 a[122:123],v45 offset:25888
v_mfma_f32_16x16x32_bf16 a[48:51],a[156:159],v[156:159],a[48:51]
ds_read_b64_tr_b16 a[124:125],v45 offset:31008
v_mfma_f32_16x16x32_bf16 a[52:55],a[156:159],v[188:191],a[52:55]
ds_read_b64_tr_b16 a[126:127],v45 offset:36128
v_mfma_f32_16x16x32_bf16 a[52:55],a[152:155],v[184:187],a[52:55]
ds_read_b64_tr_b16 a[128:129],v1
ds_read_b64_tr_b16 a[130:131],v1 offset:5120
v_mfma_f32_16x16x32_bf16 a[52:55],a[148:151],v[172:175],a[52:55]
ds_read_b64_tr_b16 a[132:133],v1 offset:10240
v_mfma_f32_16x16x32_bf16 a[52:55],a[144:147],v[168:171],a[52:55]
ds_read_b64_tr_b16 a[134:135],v1 offset:15360
v_mfma_f32_16x16x32_bf16 a[56:59],a[144:147],v[200:203],a[56:59]
ds_read_b64_tr_b16 a[136:137],v1 offset:20480
v_mfma_f32_16x16x32_bf16 a[56:59],a[148:151],v[204:207],a[56:59]
v_accvgpr_read_b32 v226,a48
v_accvgpr_read_b32 v227,a49
v_mfma_f32_16x16x32_bf16 a[56:59],a[152:155],v[216:219],a[56:59]
v_accvgpr_read_b32 v12,a50
v_accvgpr_read_b32 v13,a51
v_mfma_f32_16x16x32_bf16 a[56:59],a[156:159],v[220:223],a[56:59]
v_cvt_pk_bf16_f32 v226,v226,v227
v_cvt_pk_bf16_f32 v227,v12,v13
v_mfma_f32_16x16x32_bf16 a[64:67],a[160:163],v[64:67],a[64:67]
buffer_store_dwordx4 v[224:227],v6,s[32:35],s84 offen offset:3072 sc1
v_mfma_f32_16x16x32_bf16 a[64:67],a[164:167],v[68:71],a[64:67]
v_accvgpr_read_b32 v230,a52
v_accvgpr_read_b32 v231,a53
v_mfma_f32_16x16x32_bf16 a[64:67],a[168:171],v[72:75],a[64:67]
v_accvgpr_read_b32 v14,a54
v_accvgpr_read_b32 v15,a55
v_mfma_f32_16x16x32_bf16 a[64:67],a[172:175],v[76:79],a[64:67]
v_cvt_pk_bf16_f32 v230,v230,v231
v_cvt_pk_bf16_f32 v231,v14,v15
v_mfma_f32_16x16x32_bf16 a[68:71],a[172:175],v[92:95],a[68:71]
buffer_store_dwordx4 v[228:231],v6,s[32:35],s85 offen offset:3072 sc1
v_mfma_f32_16x16x32_bf16 a[68:71],a[168:171],v[88:91],a[68:71]
v_accvgpr_read_b32 v242,a56
v_accvgpr_read_b32 v243,a57
v_mfma_f32_16x16x32_bf16 a[68:71],a[164:167],v[84:87],a[68:71]
v_accvgpr_read_b32 v16,a58
v_accvgpr_read_b32 v17,a59
v_mfma_f32_16x16x32_bf16 a[68:71],a[160:163],v[80:83],a[68:71]
v_cvt_pk_bf16_f32 v242,v242,v243
v_cvt_pk_bf16_f32 v243,v16,v17
v_mfma_f32_16x16x32_bf16 a[72:75],a[160:163],v[96:99],a[72:75]
buffer_store_dwordx4 v[240:243],v6,s[32:35],s86 offen offset:3072 sc1
v_mfma_f32_16x16x32_bf16 a[72:75],a[164:167],v[100:103],a[72:75]
ds_read_b64_tr_b16 a[138:139],v1 offset:25600
ds_read_b64_tr_b16 a[140:141],v1 offset:30720
v_mfma_f32_16x16x32_bf16 a[72:75],a[168:171],v[104:107],a[72:75]
)A"
R"A(ds_read_b64_tr_b16 a[142:143],v1 offset:35840
v_mfma_f32_16x16x32_bf16 a[72:75],a[172:175],v[108:111],a[72:75]
ds_read_b64_tr_b16 a[144:145],v42
v_mfma_f32_16x16x32_bf16 a[64:67],a[176:179],v[136:139],a[64:67]
s_waitcnt lgkmcnt(4)
ds_read_b64_tr_b16 a[146:147],v42 offset:5120
v_mfma_f32_16x16x32_bf16 a[64:67],a[180:183],v[140:143],a[64:67]
ds_read_b64_tr_b16 a[148:149],v42 offset:10240
ds_read_b64_tr_b16 a[150:151],v42 offset:15360
v_mfma_f32_16x16x32_bf16 a[64:67],a[184:187],v[152:155],a[64:67]
buffer_load_dwordx2 v[26:27],v24,s[64:67],s94 offen sc1
v_mfma_f32_16x16x32_bf16 a[64:67],a[188:191],v[156:159],a[64:67]
ds_read_b64_tr_b16 a[152:153],v42 offset:20480
ds_read_b64_tr_b16 a[154:155],v42 offset:25600
v_mfma_f32_16x16x32_bf16 a[68:71],a[188:191],v[188:191],a[68:71]
ds_read_b64_tr_b16 a[156:157],v42 offset:30720
v_mfma_f32_16x16x32_bf16 a[68:71],a[184:187],v[184:187],a[68:71]
ds_read_b64_tr_b16 a[158:159],v42 offset:35840
v_mfma_f32_16x16x32_bf16 a[68:71],a[180:183],v[172:175],a[68:71]
ds_read_b64_tr_b16 a[160:161],v44
v_mfma_f32_16x16x32_bf16 a[68:71],a[176:179],v[168:171],a[68:71]
ds_read_b64_tr_b16 a[162:163],v44 offset:5120
ds_read_b64_tr_b16 a[164:165],v44 offset:10240
v_mfma_f32_16x16x32_bf16 a[72:75],a[176:179],v[200:203],a[72:75]
ds_read_b64_tr_b16 a[166:167],v44 offset:15360
v_mfma_f32_16x16x32_bf16 a[72:75],a[180:183],v[204:207],a[72:75]
v_accvgpr_read_b32 v128,a64
v_accvgpr_read_b32 v129,a65
v_mfma_f32_16x16x32_bf16 a[72:75],a[184:187],v[216:219],a[72:75]
v_cvt_pk_bf16_f32 v128,v128,v129
v_accvgpr_read_b32 v130,a66
v_mfma_f32_16x16x32_bf16 a[72:75],a[188:191],v[220:223],a[72:75]
v_accvgpr_read_b32 v131,a67
v_cvt_pk_bf16_f32 v129,v130,v131
v_mfma_f32_16x16x32_bf16 a[80:83],a[96:99],v[64:67],a[80:83]
ds_read_b64_tr_b16 a[168:169],v44 offset:20480
ds_read_b64_tr_b16 a[170:171],v44 offset:25600
v_mfma_f32_16x16x32_bf16 a[80:83],a[100:103],v[68:71],a[80:83]
ds_read_b64_tr_b16 a[172:173],v44 offset:30720
v_accvgpr_read_b32 v132,a68
v_mfma_f32_16x16x32_bf16 a[80:83],a[104:107],v[72:75],a[80:83]
ds_read_b64_tr_b16 a[174:175],v44 offset:35840
v_accvgpr_read_b32 v133,a69
v_mfma_f32_16x16x32_bf16 a[80:83],a[108:111],v[76:79],a[80:83]
ds_read_b64_tr_b16 a[176:177],v2 offset:32
v_cvt_pk_bf16_f32 v132,v132,v133
v_mfma_f32_16x16x32_bf16 a[84:87],a[108:111],v[92:95],a[84:87]
ds_read_b64_tr_b16 a[178:179],v2 offset:5152
v_accvgpr_read_b32 v134,a70
v_mfma_f32_16x16x32_bf16 a[84:87],a[104:107],v[88:91],a[84:87]
v_accvgpr_read_b32 v144,a72
v_accvgpr_read_b32 v145,a73
v_mfma_f32_16x16x32_bf16 a[84:87],a[100:103],v[84:87],a[84:87]
v_accvgpr_read_b32 v135,a71
v_cvt_pk_bf16_f32 v144,v144,v145
v_mfma_f32_16x16x32_bf16 a[84:87],a[96:99],v[80:83],a[84:87]
v_accvgpr_read_b32 v146,a74
v_accvgpr_read_b32 v147,a75
v_mfma_f32_16x16x32_bf16 a[88:91],a[96:99],v[96:99],a[88:91]
ds_read_b64_tr_b16 a[180:181],v2 offset:10272
ds_read_b64_tr_b16 a[182:183],v2 offset:15392
v_mfma_f32_16x16x32_bf16 a[88:91],a[100:103],v[100:103],a[88:91]
v_cvt_pk_bf16_f32 v133,v134,v135
v_cvt_pk_bf16_f32 v145,v146,v147
v_mfma_f32_16x16x32_bf16 a[88:91],a[104:107],v[104:107],a[88:91]
ds_read_b64_tr_b16 a[184:185],v2 offset:20512
ds_read_b64_tr_b16 a[186:187],v2 offset:25632
v_mfma_f32_16x16x32_bf16 a[88:91],a[108:111],v[108:111],a[88:91]
ds_read_b64_tr_b16 a[188:189],v2 offset:30752
v_mfma_f32_16x16x32_bf16 a[80:83],a[112:115],v[136:139],a[80:83]
ds_read_b64_tr_b16 a[190:191],v2 offset:35872
v_mfma_f32_16x16x32_bf16 a[80:83],a[116:119],v[140:143],a[80:83]
ds_read_b64_tr_b16 a[96:97],v43 offset:32
v_mfma_f32_16x16x32_bf16 a[80:83],a[120:123],v[152:155],a[80:83]
ds_read_b64_tr_b16 a[98:99],v43 offset:5152
ds_read_b64_tr_b16 a[100:101],v43 offset:10272
v_mfma_f32_16x16x32_bf16 a[80:83],a[124:127],v[156:159],a[80:83]
ds_read_b64_tr_b16 a[102:103],v43 offset:15392
v_mfma_f32_16x16x32_bf16 a[84:87],a[124:127],v[188:191],a[84:87]
ds_read_b64_tr_b16 a[104:105],v43 offset:20512
v_mfma_f32_16x16x32_bf16 a[84:87],a[120:123],v[184:187],a[84:87]
ds_read_b64_tr_b16 a[106:107],v43 offset:25632
v_mfma_f32_16x16x32_bf16 a[84:87],a[116:119],v[172:175],a[84:87]
ds_read_b64_tr_b16 a[108:109],v43 offset:30752
ds_read_b64_tr_b16 a[110:111],v43 offset:35872
v_mfma_f32_16x16x32_bf16 a[84:87],a[112:115],v[168:171],a[84:87]
v_mfma_f32_16x16x32_bf16 a[88:91],a[112:115],v[200:203],a[88:91]
ds_read_b64_tr_b16 a[112:113],v45 offset:32
ds_read_b64_tr_b16 a[114:115],v45 offset:5152
v_mfma_f32_16x16x32_bf16 a[88:91],a[116:119],v[204:207],a[88:91]
v_accvgpr_read_b32 v130,a80
v_accvgpr_read_b32 v131,a81
v_mfma_f32_16x16x32_bf16 a[88:91],a[120:123],v[216:219],a[88:91]
v_accvgpr_read_b32 v12,a82
v_accvgpr_read_b32 v13,a83
v_mfma_f32_16x16x32_bf16 a[88:91],a[124:127],v[220:223],a[88:91]
v_cvt_pk_bf16_f32 v130,v130,v131
v_cvt_pk_bf16_f32 v131,v12,v13
v_mfma_f32_16x16x32_bf16 a[204:207],a[128:131],v[148:151],a[204:207]
buffer_store_dwordx4 v[128:131],v25,s[32:35],s84 offen sc1
v_mfma_f32_16x16x32_bf16 a[204:207],a[132:135],v[180:183],a[204:207]
s_waitcnt lgkmcnt(4)
v_accvgpr_read_b32 v134,a84
v_accvgpr_read_b32 v135,a85
v_mfma_f32_16x16x32_bf16 a[204:207],a[136:139],v[212:215],a[204:207]
s_lshl_b32 s95,s42,2
s_add_u32 s95,s95,s40
s_lshl_b32 s95,s95,4
v_mfma_f32_16x16x32_bf16 a[204:207],a[140:143],v[244:247],a[204:207]
v_mov_b32 v18,s68
v_mov_b32 v19,s69
v_mfma_f32_16x16x32_bf16 a[204:207],a[144:147],v[112:115],a[204:207]
s_waitcnt vmcnt(2)
s_mov_b64 exec,1
buffer_store_dwordx2 v[18:19],v6,s[64:67],s95 offen offset:2048 sc1
s_mov_b64 exec,-1
s_waitcnt vmcnt(2)
v_cmp_ne_u32 s[90:91],s68,v26
v_cmp_ne_u32 s[92:93],s69,v27
s_nop 0
s_or_b64 s[90:91],s[90:91],s[92:93]
s_cbranch_scc1 L_fr10_%=
L_fo10_%=:
v_mfma_f32_16x16x32_bf16 a[204:207],a[148:151],v[116:119],a[204:207]
s_lshl_b32 s94,s71,2
v_accvgpr_read_b32 v146,a88
v_accvgpr_read_b32 v147,a89
v_mfma_f32_16x16x32_bf16 a[204:207],a[152:155],v[120:123],a[204:207]
v_accvgpr_read_b32 v14,a86
v_accvgpr_read_b32 v15,a87
v_cvt_pk_bf16_f32 v134,v134,v135
v_accvgpr_read_b32 v16,a90
v_accvgpr_read_b32 v17,a91
v_cvt_pk_bf16_f32 v146,v146,v147
v_cvt_pk_bf16_f32 v135,v14,v15
v_cvt_pk_bf16_f32 v147,v16,v17
v_mfma_f32_16x16x32_bf16 a[204:207],a[156:159],v[124:127],a[204:207]
s_add_u32 s94,s94,s40
ds_read_b64_tr_b16 a[116:117],v45 offset:10272
ds_read_b64_tr_b16 a[118:119],v45 offset:15392
v_mfma_f32_16x16x32_bf16 a[204:207],a[160:163],v[232:235],a[204:207]
s_lshl_b32 s94,s94,4
buffer_store_dwordx4 v[132:135],v25,s[32:35],s85 offen sc1
v_mfma_f32_16x16x32_bf16 a[204:207],a[164:167],v[236:239],a[204:207]
ds_read_b64_tr_b16 a[120:121],v45 offset:20512
ds_read_b64_tr_b16 a[122:123],v45 offset:25632
v_mfma_f32_16x16x32_bf16 a[204:207],a[168:171],v[248:251],a[204:207]
ds_read_b64_tr_b16 a[124:125],v45 offset:30752
ds_read_b64_tr_b16 a[126:127],v45 offset:35872
v_mfma_f32_16x16x32_bf16 a[204:207],a[172:175],v[252:255],a[204:207]
ds_read_b64_tr_b16 a[128:129],v1 offset:64
ds_read_b64_tr_b16 a[130:131],v1 offset:5184
v_mfma_f32_16x16x32_bf16 a[220:223],a[176:179],v[148:151],a[220:223]
buffer_store_dwordx4 v[144:147],v25,s[32:35],s86 offen sc1
v_mfma_f32_16x16x32_bf16 a[220:223],a[180:183],v[180:183],a[220:223]
ds_read_b64_tr_b16 a[132:133],v1 offset:10304
ds_read_b64_tr_b16 a[134:135],v1 offset:15424
v_mfma_f32_16x16x32_bf16 a[220:223],a[184:187],v[212:215],a[220:223]
ds_read_b64_tr_b16 a[136:137],v1 offset:20544
ds_read_b64_tr_b16 a[138:139],v1 offset:25664
v_mfma_f32_16x16x32_bf16 a[220:223],a[188:191],v[244:247],a[220:223]
ds_read_b64_tr_b16 a[140:141],v1 offset:30784
ds_read_b64_tr_b16 a[142:143],v1 offset:35904
v_mfma_f32_16x16x32_bf16 a[220:223],a[96:99],v[112:115],a[220:223]
buffer_load_dwordx4 a[192:195],v6,s[32:35],s76 offen sc1
v_mfma_f32_16x16x32_bf16 a[220:223],a[100:103],v[116:119],a[220:223]
ds_read_b64_tr_b16 a[144:145],v42 offset:64
ds_read_b64_tr_b16 a[146:147],v42 offset:5184
v_mfma_f32_16x16x32_bf16 a[220:223],a[104:107],v[120:123],a[220:223]
s_waitcnt lgkmcnt(10)
ds_read_b64_tr_b16 a[148:149],v42 offset:10304
ds_read_b64_tr_b16 a[150:151],v42 offset:15424
v_mfma_f32_16x16x32_bf16 a[220:223],a[108:111],v[124:127],a[220:223]
ds_read_b64_tr_b16 a[152:153],v42 offset:20544
ds_read_b64_tr_b16 a[154:155],v42 offset:25664
v_mfma_f32_16x16x32_bf16 a[220:223],a[112:115],v[232:235],a[220:223]
buffer_load_dwordx4 a[196:199],v6,s[32:35],s77 offen sc1
v_mfma_f32_16x16x32_bf16 a[220:223],a[116:119],v[236:239],a[220:223]
ds_read_b64_tr_b16 a[156:157],v42 offset:30784
ds_read_b64_tr_b16 a[158:159],v42 offset:35904
v_mfma_f32_16x16x32_bf16 a[220:223],a[120:123],v[248:251],a[220:223]
ds_read_b64_tr_b16 a[160:161],v44 offset:64
ds_read_b64_tr_b16 a[162:163],v44 offset:5184
v_mfma_f32_16x16x32_bf16 a[220:223],a[124:127],v[252:255],a[220:223]
s_waitcnt lgkmcnt(10)
ds_read_b64_tr_b16 a[164:165],v44 offset:10304
ds_read_b64_tr_b16 a[166:167],v44 offset:15424
v_mfma_f32_16x16x32_bf16 a[236:239],a[128:131],v[148:151],a[236:239]
buffer_load_dwordx4 a[200:203],v6,s[32:35],s78 offen sc1
v_mfma_f32_16x16x32_bf16 a[236:239],a[132:135],v[180:183],a[236:239]
ds_read_b64_tr_b16 a[168:169],v44 offset:20544
ds_read_b64_tr_b16 a[170:171],v44 offset:25664
v_mfma_f32_16x16x32_bf16 a[236:239],a[136:139],v[212:215],a[236:239]
ds_read_b64_tr_b16 a[172:173],v44 offset:30784
ds_read_b64_tr_b16 a[174:175],v44 offset:35904
v_mfma_f32_16x16x32_bf16 a[236:239],a[140:143],v[244:247],a[236:239]
s_waitcnt lgkmcnt(10)
ds_read_b64_tr_b16 a[176:177],v2 offset:96
ds_read_b64_tr_b16 a[178:179],v2 offset:5216
v_mfma_f32_16x16x32_bf16 a[236:239],a[144:147],v[112:115],a[236:239]
buffer_load_dwordx4 a[224:227],v6,s[32:35],s76 offen offset:1024 sc1
ds_read_b64_tr_b16 a[180:181],v2 offset:10336
ds_read_b64_tr_b16 a[182:183],v2 offset:15456
v_mfma_f32_16x16x32_bf16 a[236:239],a[148:151],v[116:119],a[236:239]
ds_read_b64_tr_b16 a[184:185],v2 offset:20576
ds_read_b64_tr_b16 a[186:187],v2 offset:25696
v_mfma_f32_16x16x32_bf16 a[236:239],a[152:155],v[120:123],a[236:239]
s_waitcnt lgkmcnt(12)
ds_read_b64_tr_b16 a[188:189],v2 offset:30816
ds_read_b64_tr_b16 a[190:191],v2 offset:35936
v_mfma_f32_16x16x32_bf16 a[236:239],a[156:159],v[124:127],a[236:239]
ds_read_b64_tr_b16 a[96:97],v43 offset:96
ds_read_b64_tr_b16 a[98:99],v43 offset:5216
v_mfma_f32_16x16x32_bf16 a[236:239],a[160:163],v[232:235],a[236:239]
s_waitcnt lgkmcnt(14)
buffer_load_dwordx4 a[228:231],v6,s[32:35],s77 offen offset:1024 sc1
ds_read_b64_tr_b16 a[100:101],v43 offset:10336
ds_read_b64_tr_b16 a[102:103],v43 offset:15456
v_mfma_f32_16x16x32_bf16 a[236:239],a[164:167],v[236:239],a[236:239]
s_waitcnt lgkmcnt(14)
ds_read_b64_tr_b16 a[104:105],v43 offset:20576
ds_read_b64_tr_b16 a[106:107],v43 offset:25696
v_mfma_f32_16x16x32_bf16 a[236:239],a[168:171],v[248:251],a[236:239]
s_waitcnt lgkmcnt(14)
ds_read_b64_tr_b16 a[108:109],v43 offset:30816
ds_read_b64_tr_b16 a[110:111],v43 offset:35936
v_mfma_f32_16x16x32_bf16 a[236:239],a[172:175],v[252:255],a[236:239]
s_waitcnt lgkmcnt(14)
ds_read_b64_tr_b16 a[112:113],v45 offset:96
ds_read_b64_tr_b16 a[114:115],v45 offset:5216
v_mfma_f32_16x16x32_bf16 a[252:255],a[176:179],v[148:151],a[252:255]
s_waitcnt lgkmcnt(14)
buffer_load_dwordx4 a[232:235],v6,s[32:35],s78 offen offset:1024 sc1
ds_read_b64_tr_b16 a[116:117],v45 offset:10336
ds_read_b64_tr_b16 a[118:119],v45 offset:15456
v_mfma_f32_16x16x32_bf16 a[252:255],a[180:183],v[180:183],a[252:255]
s_waitcnt lgkmcnt(14)
ds_read_b64_tr_b16 a[120:121],v45 offset:20576
ds_read_b64_tr_b16 a[122:123],v45 offset:25696
v_mfma_f32_16x16x32_bf16 a[252:255],a[184:187],v[212:215],a[252:255]
s_waitcnt lgkmcnt(14)
ds_read_b64_tr_b16 a[124:125],v45 offset:30816
ds_read_b64_tr_b16 a[126:127],v45 offset:35936
v_mfma_f32_16x16x32_bf16 a[252:255],a[188:191],v[244:247],a[252:255]
s_waitcnt lgkmcnt(14)
ds_read_b64_tr_b16 a[128:129],v1 offset:128
ds_read_b64_tr_b16 a[130:131],v1 offset:5248
v_bfe_u32 v24,v6,4,2
v_min_u32 v24,2,v24
v_mfma_f32_16x16x32_bf16 a[252:255],a[96:99],v[112:115],a[252:255]
s_waitcnt lgkmcnt(14)
ds_read_b64_tr_b16 a[132:133],v1 offset:10368
ds_read_b64_tr_b16 a[134:135],v1 offset:15488
v_add_u32 v24,s70,v24
v_add_u32 v24,1,v24
v_mfma_f32_16x16x32_bf16 a[252:255],a[100:103],v[116:119],a[252:255]
)A"
R"A(s_waitcnt lgkmcnt(14)
ds_read_b64_tr_b16 a[136:137],v1 offset:20608
ds_read_b64_tr_b16 a[138:139],v1 offset:25728
v_and_b32 v24,3,v24
v_lshlrev_b32 v24,8,v24
v_mfma_f32_16x16x32_bf16 a[252:255],a[104:107],v[120:123],a[252:255]
s_waitcnt lgkmcnt(14)
buffer_load_dwordx2 v[26:27],v24,s[64:67],s94 offen offset:2048 sc1
ds_read_b64_tr_b16 a[140:141],v1 offset:30848
ds_read_b64_tr_b16 a[142:143],v1 offset:35968
v_mfma_f32_16x16x32_bf16 a[252:255],a[108:111],v[124:127],a[252:255]
s_waitcnt lgkmcnt(14)
ds_read_b64_tr_b16 a[144:145],v42 offset:128
ds_read_b64_tr_b16 a[146:147],v42 offset:5248
v_mfma_f32_16x16x32_bf16 a[252:255],a[112:115],v[232:235],a[252:255]
s_waitcnt lgkmcnt(14)
ds_read_b64_tr_b16 a[148:149],v42 offset:10368
ds_read_b64_tr_b16 a[150:151],v42 offset:15488
v_mfma_f32_16x16x32_bf16 a[252:255],a[116:119],v[236:239],a[252:255]
s_lshl_b32 s95,s42,2
ds_read_b64_tr_b16 a[152:153],v42 offset:20608
ds_read_b64_tr_b16 a[154:155],v42 offset:25728
s_waitcnt lgkmcnt(15)
v_mfma_f32_16x16x32_bf16 a[252:255],a[120:123],v[248:251],a[252:255]
s_add_u32 s95,s95,s40
s_lshl_b32 s95,s95,4
ds_read_b64_tr_b16 a[156:157],v42 offset:30848
ds_read_b64_tr_b16 a[158:159],v42 offset:35968
v_mov_b32 v18,s68
v_mov_b32 v19,s69
s_waitcnt lgkmcnt(15)
v_mfma_f32_16x16x32_bf16 a[252:255],a[124:127],v[252:255],a[252:255]
s_waitcnt vmcnt(7)
s_mov_b64 exec,1
buffer_store_dwordx2 v[18:19],v6,s[64:67],s95 offen offset:3072 sc1
s_mov_b64 exec,-1
ds_read_b64_tr_b16 a[160:161],v44 offset:128
ds_read_b64_tr_b16 a[162:163],v44 offset:5248
s_waitcnt lgkmcnt(15)
v_mfma_f32_16x16x32_bf16 a[12:15],a[128:131],v[148:151],a[12:15]
s_waitcnt lgkmcnt(14)
ds_read_b64_tr_b16 a[164:165],v44 offset:10368
ds_read_b64_tr_b16 a[166:167],v44 offset:15488
v_mfma_f32_16x16x32_bf16 a[12:15],a[132:135],v[180:183],a[12:15]
s_waitcnt lgkmcnt(14)
ds_read_b64_tr_b16 a[168:169],v44 offset:20608
ds_read_b64_tr_b16 a[170:171],v44 offset:25728
v_mfma_f32_16x16x32_bf16 a[12:15],a[136:139],v[212:215],a[12:15]
s_waitcnt lgkmcnt(14)
ds_read_b64_tr_b16 a[172:173],v44 offset:30848
ds_read_b64_tr_b16 a[174:175],v44 offset:35968
v_mfma_f32_16x16x32_bf16 a[12:15],a[140:143],v[244:247],a[12:15]
s_waitcnt lgkmcnt(14)
ds_read_b64_tr_b16 a[176:177],v2 offset:160
ds_read_b64_tr_b16 a[178:179],v2 offset:5280
v_mfma_f32_16x16x32_bf16 a[12:15],a[144:147],v[112:115],a[12:15]
s_waitcnt lgkmcnt(14)
ds_read_b64_tr_b16 a[180:181],v2 offset:10400
ds_read_b64_tr_b16 a[182:183],v2 offset:15520
v_mfma_f32_16x16x32_bf16 a[12:15],a[148:151],v[116:119],a[12:15]
s_waitcnt lgkmcnt(14)
ds_read_b64_tr_b16 a[184:185],v2 offset:20640
ds_read_b64_tr_b16 a[186:187],v2 offset:25760
v_mfma_f32_16x16x32_bf16 a[12:15],a[152:155],v[120:123],a[12:15]
s_waitcnt lgkmcnt(14)
ds_read_b64_tr_b16 a[188:189],v2 offset:30880
ds_read_b64_tr_b16 a[190:191],v2 offset:36000
v_mfma_f32_16x16x32_bf16 a[12:15],a[156:159],v[124:127],a[12:15]
s_waitcnt lgkmcnt(14)
ds_read_b64_tr_b16 a[96:97],v43 offset:160
ds_read_b64_tr_b16 a[98:99],v43 offset:5280
v_mfma_f32_16x16x32_bf16 a[12:15],a[160:163],v[232:235],a[12:15]
s_waitcnt lgkmcnt(14)
ds_read_b64_tr_b16 a[100:101],v43 offset:10400
ds_read_b64_tr_b16 a[102:103],v43 offset:15520
v_mfma_f32_16x16x32_bf16 a[12:15],a[164:167],v[236:239],a[12:15]
s_waitcnt lgkmcnt(14)
ds_read_b64_tr_b16 a[104:105],v43 offset:20640
ds_read_b64_tr_b16 a[106:107],v43 offset:25760
v_mfma_f32_16x16x32_bf16 a[12:15],a[168:171],v[248:251],a[12:15]
s_waitcnt lgkmcnt(14)
ds_read_b64_tr_b16 a[108:109],v43 offset:30880
ds_read_b64_tr_b16 a[110:111],v43 offset:36000
v_mfma_f32_16x16x32_bf16 a[12:15],a[172:175],v[252:255],a[12:15]
s_waitcnt lgkmcnt(14)
ds_read_b64_tr_b16 a[112:113],v45 offset:160
ds_read_b64_tr_b16 a[114:115],v45 offset:5280
v_mfma_f32_16x16x32_bf16 a[28:31],a[176:179],v[148:151],a[28:31]
s_waitcnt lgkmcnt(14)
ds_read_b64_tr_b16 a[116:117],v45 offset:10400
ds_read_b64_tr_b16 a[118:119],v45 offset:15520
v_mfma_f32_16x16x32_bf16 a[28:31],a[180:183],v[180:183],a[28:31]
s_waitcnt lgkmcnt(14)
ds_read_b64_tr_b16 a[120:121],v45 offset:20640
ds_read_b64_tr_b16 a[122:123],v45 offset:25760
v_mfma_f32_16x16x32_bf16 a[28:31],a[184:187],v[212:215],a[28:31]
s_waitcnt lgkmcnt(14)
ds_read_b64_tr_b16 a[124:125],v45 offset:30880
ds_read_b64_tr_b16 a[126:127],v45 offset:36000
v_mfma_f32_16x16x32_bf16 a[28:31],a[188:191],v[244:247],a[28:31]
s_waitcnt lgkmcnt(14)
ds_read_b64_tr_b16 a[128:129],v1 offset:192
ds_read_b64_tr_b16 a[130:131],v1 offset:5312
v_mfma_f32_16x16x32_bf16 a[28:31],a[96:99],v[112:115],a[28:31]
s_waitcnt lgkmcnt(14)
ds_read_b64_tr_b16 a[132:133],v1 offset:10432
ds_read_b64_tr_b16 a[134:135],v1 offset:15552
v_mfma_f32_16x16x32_bf16 a[28:31],a[100:103],v[116:119],a[28:31]
s_waitcnt lgkmcnt(14)
ds_read_b64_tr_b16 a[136:137],v1 offset:20672
ds_read_b64_tr_b16 a[138:139],v1 offset:25792
v_mfma_f32_16x16x32_bf16 a[28:31],a[104:107],v[120:123],a[28:31]
s_waitcnt lgkmcnt(14)
ds_read_b64_tr_b16 a[140:141],v1 offset:30912
ds_read_b64_tr_b16 a[142:143],v1 offset:36032
v_mfma_f32_16x16x32_bf16 a[28:31],a[108:111],v[124:127],a[28:31]
s_waitcnt lgkmcnt(14)
ds_read_b64_tr_b16 a[144:145],v42 offset:192
ds_read_b64_tr_b16 a[146:147],v42 offset:5312
v_mfma_f32_16x16x32_bf16 a[28:31],a[112:115],v[232:235],a[28:31]
s_waitcnt lgkmcnt(14)
ds_read_b64_tr_b16 a[148:149],v42 offset:10432
ds_read_b64_tr_b16 a[150:151],v42 offset:15552
v_mfma_f32_16x16x32_bf16 a[28:31],a[116:119],v[236:239],a[28:31]
s_waitcnt lgkmcnt(14)
ds_read_b64_tr_b16 a[152:153],v42 offset:20672
ds_read_b64_tr_b16 a[154:155],v42 offset:25792
v_mfma_f32_16x16x32_bf16 a[28:31],a[120:123],v[248:251],a[28:31]
s_waitcnt lgkmcnt(14)
ds_read_b64_tr_b16 a[156:157],v42 offset:30912
ds_read_b64_tr_b16 a[158:159],v42 offset:36032
v_mfma_f32_16x16x32_bf16 a[28:31],a[124:127],v[252:255],a[28:31]
s_waitcnt lgkmcnt(14)
ds_read_b64_tr_b16 a[160:161],v44 offset:192
ds_read_b64_tr_b16 a[162:163],v44 offset:5312
v_mfma_f32_16x16x32_bf16 a[44:47],a[128:131],v[148:151],a[44:47]
s_waitcnt lgkmcnt(14)
ds_read_b64_tr_b16 a[164:165],v44 offset:10432
ds_read_b64_tr_b16 a[166:167],v44 offset:15552
v_mfma_f32_16x16x32_bf16 a[44:47],a[132:135],v[180:183],a[44:47]
s_waitcnt lgkmcnt(14)
ds_read_b64_tr_b16 a[168:169],v44 offset:20672
ds_read_b64_tr_b16 a[170:171],v44 offset:25792
v_mfma_f32_16x16x32_bf16 a[44:47],a[136:139],v[212:215],a[44:47]
ds_read_b64_tr_b16 a[172:173],v44 offset:30912
ds_read_b64_tr_b16 a[174:175],v44 offset:36032
s_waitcnt vmcnt(1)
v_cmp_ne_u32 s[90:91],s68,v26
v_cmp_ne_u32 s[92:93],s69,v27
s_nop 0
s_or_b64 s[90:91],s[90:91],s[92:93]
s_cbranch_scc1 L_fr11_%=
L_fo11_%=:
s_waitcnt lgkmcnt(15)
v_mfma_f32_16x16x32_bf16 a[44:47],a[140:143],v[244:247],a[44:47]
s_waitcnt lgkmcnt(14)
buffer_load_dwordx4 v[72:75],v6,s[32:35],s76 offen offset:2048 sc1
ds_read_b64_tr_b16 a[176:177],v2 offset:224
ds_read_b64_tr_b16 a[178:179],v2 offset:5344
v_mfma_f32_16x16x32_bf16 a[44:47],a[144:147],v[112:115],a[44:47]
s_lshl_b32 s94,s71,2
ds_read_b64_tr_b16 a[180:181],v2 offset:10464
ds_read_b64_tr_b16 a[182:183],v2 offset:15584
s_waitcnt lgkmcnt(15)
v_mfma_f32_16x16x32_bf16 a[44:47],a[148:151],v[116:119],a[44:47]
s_add_u32 s94,s94,s40
ds_read_b64_tr_b16 a[184:185],v2 offset:20704
ds_read_b64_tr_b16 a[186:187],v2 offset:25824
s_waitcnt lgkmcnt(15)
v_mfma_f32_16x16x32_bf16 a[44:47],a[152:155],v[120:123],a[44:47]
s_lshl_b32 s94,s94,4
ds_read_b64_tr_b16 a[188:189],v2 offset:30944
ds_read_b64_tr_b16 a[190:191],v2 offset:36064
s_waitcnt lgkmcnt(15)
v_mfma_f32_16x16x32_bf16 a[44:47],a[156:159],v[124:127],a[44:47]
s_waitcnt lgkmcnt(14)
buffer_load_dwordx4 v[88:91],v6,s[32:35],s77 offen offset:2048 sc1
ds_read_b64_tr_b16 a[96:97],v43 offset:224
ds_read_b64_tr_b16 a[98:99],v43 offset:5344
v_mfma_f32_16x16x32_bf16 a[44:47],a[160:163],v[232:235],a[44:47]
s_waitcnt lgkmcnt(14)
ds_read_b64_tr_b16 a[100:101],v43 offset:10464
ds_read_b64_tr_b16 a[102:103],v43 offset:15584
v_mfma_f32_16x16x32_bf16 a[44:47],a[164:167],v[236:239],a[44:47]
s_waitcnt lgkmcnt(14)
ds_read_b64_tr_b16 a[104:105],v43 offset:20704
ds_read_b64_tr_b16 a[106:107],v43 offset:25824
v_mfma_f32_16x16x32_bf16 a[44:47],a[168:171],v[248:251],a[44:47]
s_waitcnt lgkmcnt(14)
ds_read_b64_tr_b16 a[108:109],v43 offset:30944
ds_read_b64_tr_b16 a[110:111],v43 offset:36064
v_mfma_f32_16x16x32_bf16 a[44:47],a[172:175],v[252:255],a[44:47]
s_waitcnt lgkmcnt(14)
buffer_load_dwordx4 v[104:107],v6,s[32:35],s78 offen offset:2048 sc1
ds_read_b64_tr_b16 a[112:113],v45 offset:224
ds_read_b64_tr_b16 a[114:115],v45 offset:5344
v_mfma_f32_16x16x32_bf16 a[60:63],a[176:179],v[148:151],a[60:63]
s_waitcnt lgkmcnt(14)
ds_read_b64_tr_b16 a[116:117],v45 offset:10464
ds_read_b64_tr_b16 a[118:119],v45 offset:15584
v_mfma_f32_16x16x32_bf16 a[60:63],a[180:183],v[180:183],a[60:63]
s_waitcnt lgkmcnt(14)
ds_read_b64_tr_b16 a[120:121],v45 offset:20704
ds_read_b64_tr_b16 a[122:123],v45 offset:25824
v_mfma_f32_16x16x32_bf16 a[60:63],a[184:187],v[212:215],a[60:63]
s_waitcnt lgkmcnt(14)
ds_read_b64_tr_b16 a[124:125],v45 offset:30944
ds_read_b64_tr_b16 a[126:127],v45 offset:36064
v_mfma_f32_16x16x32_bf16 a[60:63],a[188:191],v[244:247],a[60:63]
s_waitcnt lgkmcnt(14)
buffer_load_dwordx4 v[76:79],v6,s[32:35],s76 offen offset:3072 sc1
v_mfma_f32_16x16x32_bf16 a[60:63],a[96:99],v[112:115],a[60:63]
s_waitcnt lgkmcnt(12)
ds_read_b64_tr_b16 a[128:129],v1 offset:256
ds_read_b64_tr_b16 a[130:131],v1 offset:5376
v_mfma_f32_16x16x32_bf16 a[60:63],a[100:103],v[116:119],a[60:63]
s_waitcnt lgkmcnt(12)
ds_read_b64_tr_b16 a[132:133],v1 offset:10496
ds_read_b64_tr_b16 a[134:135],v1 offset:15616
v_mfma_f32_16x16x32_bf16 a[60:63],a[104:107],v[120:123],a[60:63]
s_waitcnt lgkmcnt(12)
ds_read_b64_tr_b16 a[136:137],v1 offset:20736
ds_read_b64_tr_b16 a[138:139],v1 offset:25856
v_mfma_f32_16x16x32_bf16 a[60:63],a[108:111],v[124:127],a[60:63]
s_waitcnt lgkmcnt(12)
buffer_load_dwordx4 v[92:95],v6,s[32:35],s77 offen offset:3072 sc1
v_mfma_f32_16x16x32_bf16 a[60:63],a[112:115],v[232:235],a[60:63]
s_waitcnt lgkmcnt(10)
ds_read_b64_tr_b16 a[140:141],v1 offset:30976
ds_read_b64_tr_b16 a[142:143],v1 offset:36096
v_mfma_f32_16x16x32_bf16 a[60:63],a[116:119],v[236:239],a[60:63]
s_waitcnt lgkmcnt(10)
ds_read_b64_tr_b16 a[144:145],v42 offset:256
ds_read_b64_tr_b16 a[146:147],v42 offset:5376
v_mfma_f32_16x16x32_bf16 a[60:63],a[120:123],v[248:251],a[60:63]
s_waitcnt lgkmcnt(10)
ds_read_b64_tr_b16 a[148:149],v42 offset:10496
ds_read_b64_tr_b16 a[150:151],v42 offset:15616
v_mfma_f32_16x16x32_bf16 a[60:63],a[124:127],v[252:255],a[60:63]
buffer_load_dwordx4 v[108:111],v6,s[32:35],s78 offen offset:3072 sc1
v_mfma_f32_16x16x32_bf16 a[204:207],v[34:37],a[192:195],a[204:207]
ds_read_b64_tr_b16 a[152:153],v42 offset:20736
ds_read_b64_tr_b16 a[154:155],v42 offset:25856
v_mfma_f32_16x16x32_bf16 a[204:207],v[34:37],a[196:199],a[204:207]
ds_read_b64_tr_b16 a[156:157],v42 offset:30976
ds_read_b64_tr_b16 a[158:159],v42 offset:36096
v_mfma_f32_16x16x32_bf16 a[204:207],v[34:37],a[200:203],a[204:207]
ds_read_b64_tr_b16 a[160:161],v44 offset:256
ds_read_b64_tr_b16 a[162:163],v44 offset:5376
v_mfma_f32_16x16x32_bf16 a[220:223],v[32:35],a[192:195],a[220:223]
ds_read_b64_tr_b16 a[164:165],v44 offset:10496
ds_read_b64_tr_b16 a[166:167],v44 offset:15616
v_mfma_f32_16x16x32_bf16 a[220:223],v[32:35],a[196:199],a[220:223]
ds_read_b64_tr_b16 a[168:169],v44 offset:20736
ds_read_b64_tr_b16 a[170:171],v44 offset:25856
v_mfma_f32_16x16x32_bf16 a[220:223],v[32:35],a[200:203],a[220:223]
s_waitcnt lgkmcnt(12)
ds_read_b64_tr_b16 a[172:173],v44 offset:30976
ds_read_b64_tr_b16 a[174:175],v44 offset:36096
v_mfma_f32_16x16x32_bf16 a[76:79],a[128:131],v[148:151],a[76:79]
v_bfe_u32 v24,v6,4,2
v_min_u32 v24,2,v24
v_mfma_f32_16x16x32_bf16 a[76:79],a[132:135],v[180:183],a[76:79]
v_add_u32 v24,s70,v24
v_add_u32 v24,1,v24
v_mfma_f32_16x16x32_bf16 a[76:79],a[136:139],v[212:215],a[76:79]
v_accvgpr_read_b32 v160,a204
v_accvgpr_read_b32 v161,a205
v_mfma_f32_16x16x32_bf16 a[76:79],a[140:143],v[244:247],a[76:79]
v_cvt_pk_bf16_f32 v160,v160,v161
v_accvgpr_read_b32 v162,a206
v_mfma_f32_16x16x32_bf16 a[76:79],a[144:147],v[112:115],a[76:79]
s_waitcnt lgkmcnt(6)
)A"
R"A(v_accvgpr_read_b32 v163,a207
v_cvt_pk_bf16_f32 v161,v162,v163
v_mfma_f32_16x16x32_bf16 a[76:79],a[148:151],v[116:119],a[76:79]
v_accvgpr_read_b32 v162,a220
v_accvgpr_read_b32 v163,a221
v_mfma_f32_16x16x32_bf16 a[76:79],a[152:155],v[120:123],a[76:79]
v_accvgpr_read_b32 v12,a222
v_accvgpr_read_b32 v13,a223
v_mfma_f32_16x16x32_bf16 a[76:79],a[156:159],v[124:127],a[76:79]
v_cvt_pk_bf16_f32 v162,v162,v163
v_cvt_pk_bf16_f32 v163,v12,v13
v_mfma_f32_16x16x32_bf16 a[76:79],a[160:163],v[232:235],a[76:79]
s_waitcnt lgkmcnt(0)
ds_read_b64_tr_b16 a[176:177],v2 offset:288
ds_read_b64_tr_b16 a[178:179],v2 offset:5408
v_mfma_f32_16x16x32_bf16 a[76:79],a[164:167],v[236:239],a[76:79]
v_permlane16_swap_b32 v160,v162
v_mfma_f32_16x16x32_bf16 a[76:79],a[168:171],v[248:251],a[76:79]
ds_read_b64_tr_b16 a[180:181],v2 offset:10528
ds_read_b64_tr_b16 a[182:183],v2 offset:15648
v_and_b32 v24,3,v24
v_lshlrev_b32 v24,8,v24
v_mfma_f32_16x16x32_bf16 a[76:79],a[172:175],v[252:255],a[76:79]
buffer_load_dwordx2 v[26:27],v24,s[64:67],s94 offen offset:3072 sc1
ds_read_b64_tr_b16 a[184:185],v2 offset:20768
ds_read_b64_tr_b16 a[186:187],v2 offset:25888
v_mfma_f32_16x16x32_bf16 a[236:239],v[34:37],a[224:227],a[236:239]
ds_read_b64_tr_b16 a[188:189],v2 offset:31008
ds_read_b64_tr_b16 a[190:191],v2 offset:36128
v_mfma_f32_16x16x32_bf16 a[236:239],v[34:37],a[228:231],a[236:239]
ds_read_b64_tr_b16 a[96:97],v43 offset:288
ds_read_b64_tr_b16 a[98:99],v43 offset:5408
v_mfma_f32_16x16x32_bf16 a[236:239],v[34:37],a[232:235],a[236:239]
ds_read_b64_tr_b16 a[100:101],v43 offset:10528
ds_read_b64_tr_b16 a[102:103],v43 offset:15648
v_mfma_f32_16x16x32_bf16 a[252:255],v[32:35],a[224:227],a[252:255]
ds_read_b64_tr_b16 a[104:105],v43 offset:20768
ds_read_b64_tr_b16 a[106:107],v43 offset:25888
v_mfma_f32_16x16x32_bf16 a[252:255],v[32:35],a[228:231],a[252:255]
ds_read_b64_tr_b16 a[108:109],v43 offset:31008
ds_read_b64_tr_b16 a[110:111],v43 offset:36128
v_mfma_f32_16x16x32_bf16 a[252:255],v[32:35],a[232:235],a[252:255]
s_waitcnt lgkmcnt(14)
ds_read_b64_tr_b16 a[112:113],v45 offset:288
ds_read_b64_tr_b16 a[114:115],v45 offset:5408
v_mfma_f32_16x16x32_bf16 a[92:95],a[176:179],v[148:151],a[92:95]
s_waitcnt lgkmcnt(14)
ds_read_b64_tr_b16 a[116:117],v45 offset:10528
ds_read_b64_tr_b16 a[118:119],v45 offset:15648
v_mfma_f32_16x16x32_bf16 a[92:95],a[180:183],v[180:183],a[92:95]
s_waitcnt lgkmcnt(14)
ds_read_b64_tr_b16 a[120:121],v45 offset:20768
ds_read_b64_tr_b16 a[122:123],v45 offset:25888
v_mfma_f32_16x16x32_bf16 a[92:95],a[184:187],v[212:215],a[92:95]
s_waitcnt lgkmcnt(14)
ds_read_b64_tr_b16 a[124:125],v45 offset:31008
ds_read_b64_tr_b16 a[126:127],v45 offset:36128
v_mfma_f32_16x16x32_bf16 a[92:95],a[188:191],v[244:247],a[92:95]
s_waitcnt lgkmcnt(14)
v_permlane16_swap_b32 v161,v163
v_mfma_f32_16x16x32_bf16 a[92:95],a[96:99],v[112:115],a[92:95]
s_cmp_lg_u32 %2,0
s_cbranch_scc1 L_ea0_%=
buffer_store_dwordx4 v[160:163],v40,s[20:23],s43 offen
s_branch L_eb0_%=
L_ea0_%=:
buffer_store_dwordx4 v[160:163],v40,s[20:23],s43 offen sc1
L_eb0_%=:
s_waitcnt lgkmcnt(12)
v_mfma_f32_16x16x32_bf16 a[92:95],a[100:103],v[116:119],a[92:95]
s_waitcnt lgkmcnt(10)
v_accvgpr_read_b32 v164,a236
v_accvgpr_read_b32 v165,a237
v_mfma_f32_16x16x32_bf16 a[92:95],a[104:107],v[120:123],a[92:95]
s_waitcnt lgkmcnt(8)
v_cvt_pk_bf16_f32 v164,v164,v165
v_accvgpr_read_b32 v166,a238
v_mfma_f32_16x16x32_bf16 a[92:95],a[108:111],v[124:127],a[92:95]
s_waitcnt lgkmcnt(6)
v_accvgpr_read_b32 v167,a239
v_cvt_pk_bf16_f32 v165,v166,v167
v_mfma_f32_16x16x32_bf16 a[92:95],a[112:115],v[232:235],a[92:95]
s_waitcnt lgkmcnt(4)
v_accvgpr_read_b32 v166,a252
v_accvgpr_read_b32 v167,a253
v_mfma_f32_16x16x32_bf16 a[92:95],a[116:119],v[236:239],a[92:95]
s_waitcnt lgkmcnt(2)
v_accvgpr_read_b32 v14,a254
v_accvgpr_read_b32 v15,a255
v_cvt_pk_bf16_f32 v166,v166,v167
v_cvt_pk_bf16_f32 v167,v14,v15
v_mfma_f32_16x16x32_bf16 a[92:95],a[120:123],v[248:251],a[92:95]
s_waitcnt lgkmcnt(0)
v_mfma_f32_16x16x32_bf16 a[92:95],a[124:127],v[252:255],a[92:95]
v_permlane16_swap_b32 v164,v166
v_permlane16_swap_b32 v165,v167
s_nop 3
s_cmp_lg_u32 %2,0
s_cbranch_scc1 L_ea1_%=
buffer_store_dwordx4 v[164:167],v40,s[20:23],s43 offen offset:64
s_branch L_eb1_%=
L_ea1_%=:
buffer_store_dwordx4 v[164:167],v40,s[20:23],s43 offen offset:64 sc1
L_eb1_%=:
s_waitcnt vmcnt(2)
v_cmp_ne_u32 s[90:91],s68,v26
v_cmp_ne_u32 s[92:93],s69,v27
s_nop 0
s_or_b64 s[90:91],s[90:91],s[92:93]
s_cbranch_scc1 L_fr12_%=
L_fo12_%=:
buffer_load_dwordx4 v[136:139],v25,s[32:35],s76 offen sc1
buffer_load_dwordx4 v[168:171],v25,s[32:35],s77 offen sc1
buffer_load_dwordx4 v[200:203],v25,s[32:35],s78 offen sc1
s_cmp_lg_u32 %2,0
s_cbranch_scc1 L_dual_%=
v_mfma_f32_16x16x32_bf16 a[12:15],v[34:37],v[72:75],a[12:15]
v_mfma_f32_16x16x32_bf16 a[12:15],v[34:37],v[88:91],a[12:15]
v_mfma_f32_16x16x32_bf16 a[12:15],v[34:37],v[104:107],a[12:15]
v_mfma_f32_16x16x32_bf16 a[28:31],v[32:35],v[72:75],a[28:31]
v_mfma_f32_16x16x32_bf16 a[28:31],v[32:35],v[88:91],a[28:31]
v_mfma_f32_16x16x32_bf16 a[28:31],v[32:35],v[104:107],a[28:31]
v_mfma_f32_16x16x32_bf16 a[44:47],v[34:37],v[76:79],a[44:47]
v_mfma_f32_16x16x32_bf16 a[44:47],v[34:37],v[92:95],a[44:47]
v_mfma_f32_16x16x32_bf16 a[44:47],v[34:37],v[108:111],a[44:47]
s_nop 1
v_accvgpr_read_b32 v176,a12
v_accvgpr_read_b32 v177,a13
v_cvt_pk_bf16_f32 v176,v176,v177
v_accvgpr_read_b32 v178,a14
v_accvgpr_read_b32 v179,a15
v_cvt_pk_bf16_f32 v177,v178,v179
v_mfma_f32_16x16x32_bf16 a[60:63],v[32:35],v[76:79],a[60:63]
v_mfma_f32_16x16x32_bf16 a[60:63],v[32:35],v[92:95],a[60:63]
v_mfma_f32_16x16x32_bf16 a[60:63],v[32:35],v[108:111],a[60:63]
v_accvgpr_read_b32 v178,a28
v_accvgpr_read_b32 v179,a29
v_accvgpr_read_b32 v16,a30
v_accvgpr_read_b32 v17,a31
v_cvt_pk_bf16_f32 v178,v178,v179
v_cvt_pk_bf16_f32 v179,v16,v17
s_nop 3
v_permlane16_swap_b32 v176,v178
v_permlane16_swap_b32 v177,v179
buffer_store_dwordx4 v[176:179],v40,s[20:23],s43 offen offset:128
v_accvgpr_read_b32 v192,a44
v_accvgpr_read_b32 v193,a45
v_cvt_pk_bf16_f32 v192,v192,v193
v_accvgpr_read_b32 v194,a46
v_accvgpr_read_b32 v195,a47
v_cvt_pk_bf16_f32 v193,v194,v195
v_accvgpr_read_b32 v194,a60
v_accvgpr_read_b32 v195,a61
v_accvgpr_read_b32 v12,a62
v_accvgpr_read_b32 v13,a63
v_cvt_pk_bf16_f32 v194,v194,v195
s_nop 1
v_permlane16_swap_b32 v192,v194
v_cvt_pk_bf16_f32 v195,v12,v13
s_nop 1
v_permlane16_swap_b32 v193,v195
buffer_store_dwordx4 v[192:195],v40,s[20:23],s43 offen offset:192
s_waitcnt vmcnt(4)
v_mfma_f32_16x16x32_bf16 a[76:79],v[34:37],v[136:139],a[76:79]
s_waitcnt vmcnt(3)
v_mfma_f32_16x16x32_bf16 a[76:79],v[34:37],v[168:171],a[76:79]
s_waitcnt vmcnt(2)
v_mfma_f32_16x16x32_bf16 a[76:79],v[34:37],v[200:203],a[76:79]
v_mfma_f32_16x16x32_bf16 a[92:95],v[32:35],v[136:139],a[92:95]
v_mfma_f32_16x16x32_bf16 a[92:95],v[32:35],v[168:171],a[92:95]
v_mfma_f32_16x16x32_bf16 a[92:95],v[32:35],v[200:203],a[92:95]
s_nop 3
s_nop 0
v_accvgpr_read_b32 v196,a76
v_accvgpr_read_b32 v197,a77
v_cvt_pk_bf16_f32 v196,v196,v197
v_accvgpr_read_b32 v198,a78
v_accvgpr_read_b32 v199,a79
v_cvt_pk_bf16_f32 v197,v198,v199
v_accvgpr_read_b32 v198,a92
v_accvgpr_read_b32 v199,a93
v_accvgpr_read_b32 v14,a94
v_accvgpr_read_b32 v15,a95
v_cvt_pk_bf16_f32 v198,v198,v199
s_nop 1
v_permlane16_swap_b32 v196,v198
v_cvt_pk_bf16_f32 v199,v14,v15
s_nop 1
v_permlane16_swap_b32 v197,v199
buffer_store_dwordx4 v[196:199],v40,s[20:23],s43 offen offset:256
s_or_b32 s62,s42,s40
s_cmp_lg_u32 s62,0
s_cbranch_scc1 L_noep_%=
v_mov_b32 v12,s68
s_mov_b64 exec,1
buffer_store_dword v12,v25,s[64:67],0 offen sc1
s_mov_b64 exec,-1
L_noep_%=:
s_endpgm
L_dual_%=:
v_mfma_f32_16x16x32_bf16 a[12:15],v[34:37],v[72:75],a[12:15]
v_mfma_f32_16x16x32_bf16 a[12:15],v[34:37],v[88:91],a[12:15]
v_mfma_f32_16x16x32_bf16 a[12:15],v[34:37],v[104:107],a[12:15]
v_mfma_f32_16x16x32_bf16 a[28:31],v[32:35],v[72:75],a[28:31]
v_mfma_f32_16x16x32_bf16 a[28:31],v[32:35],v[88:91],a[28:31]
v_mfma_f32_16x16x32_bf16 a[28:31],v[32:35],v[104:107],a[28:31]
v_mfma_f32_16x16x32_bf16 a[44:47],v[34:37],v[76:79],a[44:47]
v_mfma_f32_16x16x32_bf16 a[44:47],v[34:37],v[92:95],a[44:47]
v_mfma_f32_16x16x32_bf16 a[44:47],v[34:37],v[108:111],a[44:47]
s_nop 1
v_accvgpr_read_b32 v176,a12
v_accvgpr_read_b32 v177,a13
v_cvt_pk_bf16_f32 v176,v176,v177
v_accvgpr_read_b32 v178,a14
v_accvgpr_read_b32 v179,a15
v_cvt_pk_bf16_f32 v177,v178,v179
v_mfma_f32_16x16x32_bf16 a[60:63],v[32:35],v[76:79],a[60:63]
v_mfma_f32_16x16x32_bf16 a[60:63],v[32:35],v[92:95],a[60:63]
v_mfma_f32_16x16x32_bf16 a[60:63],v[32:35],v[108:111],a[60:63]
v_accvgpr_read_b32 v178,a28
v_accvgpr_read_b32 v179,a29
v_accvgpr_read_b32 v16,a30
v_accvgpr_read_b32 v17,a31
v_cvt_pk_bf16_f32 v178,v178,v179
v_cvt_pk_bf16_f32 v179,v16,v17
s_nop 3
v_permlane16_swap_b32 v176,v178
v_permlane16_swap_b32 v177,v179
buffer_store_dwordx4 v[176:179],v40,s[20:23],s43 offen offset:128 sc1
v_accvgpr_read_b32 v192,a44
v_accvgpr_read_b32 v193,a45
v_cvt_pk_bf16_f32 v192,v192,v193
v_accvgpr_read_b32 v194,a46
v_accvgpr_read_b32 v195,a47
v_cvt_pk_bf16_f32 v193,v194,v195
v_accvgpr_read_b32 v194,a60
v_accvgpr_read_b32 v195,a61
v_accvgpr_read_b32 v12,a62
v_accvgpr_read_b32 v13,a63
v_cvt_pk_bf16_f32 v194,v194,v195
s_nop 1
v_permlane16_swap_b32 v192,v194
v_cvt_pk_bf16_f32 v195,v12,v13
s_nop 1
v_permlane16_swap_b32 v193,v195
buffer_store_dwordx4 v[192:195],v40,s[20:23],s43 offen offset:192 sc1
s_waitcnt vmcnt(4)
v_mfma_f32_16x16x32_bf16 a[76:79],v[34:37],v[136:139],a[76:79]
s_waitcnt vmcnt(3)
v_mfma_f32_16x16x32_bf16 a[76:79],v[34:37],v[168:171],a[76:79]
s_waitcnt vmcnt(2)
v_mfma_f32_16x16x32_bf16 a[76:79],v[34:37],v[200:203],a[76:79]
v_mfma_f32_16x16x32_bf16 a[92:95],v[32:35],v[136:139],a[92:95]
v_mfma_f32_16x16x32_bf16 a[92:95],v[32:35],v[168:171],a[92:95]
v_mfma_f32_16x16x32_bf16 a[92:95],v[32:35],v[200:203],a[92:95]
s_nop 3
s_nop 0
v_accvgpr_read_b32 v196,a76
v_accvgpr_read_b32 v197,a77
v_cvt_pk_bf16_f32 v196,v196,v197
v_accvgpr_read_b32 v198,a78
v_accvgpr_read_b32 v199,a79
v_cvt_pk_bf16_f32 v197,v198,v199
v_accvgpr_read_b32 v198,a92
v_accvgpr_read_b32 v199,a93
v_accvgpr_read_b32 v14,a94
v_accvgpr_read_b32 v15,a95
v_cvt_pk_bf16_f32 v198,v198,v199
s_nop 1
v_permlane16_swap_b32 v196,v198
v_cvt_pk_bf16_f32 v199,v14,v15
s_nop 1
v_permlane16_swap_b32 v197,v199
buffer_store_dwordx4 v[196:199],v40,s[20:23],s43 offen offset:256 sc1
s_or_b32 s62,s42,s40
s_cmp_lg_u32 s62,0
s_cbranch_scc1 L_noep2_%=
v_mov_b32 v12,s68
s_mov_b64 exec,1
buffer_store_dword v12,v25,s[64:67],0 offen sc1
s_mov_b64 exec,-1
L_noep2_%=:
s_endpgm
L_ps2_%=:
s_min_i32 s79,s79,100
s_add_u32 s78,s78,s79
L_pl2_%=:
s_sleep 1
s_sub_u32 s79,s79,4
s_cmp_gt_i32 s79,1
s_cbranch_scc1 L_pl2_%=
s_branch L_pr2_%=
L_ps3_%=:
s_min_i32 s79,s79,100
s_add_u32 s78,s78,s79
L_pl3_%=:
s_sleep 1
s_sub_u32 s79,s79,4
s_cmp_gt_i32 s79,1
s_cbranch_scc1 L_pl3_%=
s_branch L_pr3_%=
L_ps4_%=:
s_min_i32 s79,s79,100
s_add_u32 s78,s78,s79
L_pl4_%=:
s_sleep 1
s_sub_u32 s79,s79,4
s_cmp_gt_i32 s79,1
s_cbranch_scc1 L_pl4_%=
s_branch L_pr4_%=
L_ps5_%=:
s_min_i32 s79,s79,100
s_add_u32 s78,s78,s79
L_pl5_%=:
s_sleep 1
s_sub_u32 s79,s79,4
s_cmp_gt_i32 s79,1
s_cbranch_scc1 L_pl5_%=
s_branch L_pr5_%=
L_ps6_%=:
s_min_i32 s79,s79,100
s_add_u32 s78,s78,s79
L_pl6_%=:
s_sleep 1
s_sub_u32 s79,s79,4
s_cmp_gt_i32 s79,1
s_cbranch_scc1 L_pl6_%=
s_branch L_pr6_%=
L_ps7_%=:
s_min_i32 s79,s79,100
s_add_u32 s78,s78,s79
L_pl7_%=:
s_sleep 1
s_sub_u32 s79,s79,4
s_cmp_gt_i32 s79,1
s_cbranch_scc1 L_pl7_%=
s_branch L_pr7_%=
L_fr8_%=:
s_mov_b32 s88,262144
L_fl8_%=:
buffer_load_dwordx2 v[26:27],v24,s[64:67],s94 offen sc1
s_waitcnt vmcnt(0)
s_sub_u32 s88,s88,1
s_cmp_eq_u32 s88,0
s_cbranch_scc1 L_fo8_%=
v_cmp_ne_u32 s[90:91],s68,v26
v_cmp_ne_u32 s[92:93],s69,v27
s_nop 0
s_or_b64 s[90:91],s[90:91],s[92:93]
s_cbranch_scc1 L_fl8_%=
s_branch L_fo8_%=
L_fr9_%=:
s_mov_b32 s88,262144
L_fl9_%=:
buffer_load_dwordx2 v[26:27],v24,s[64:67],s94 offen offset:1024 sc1
s_waitcnt vmcnt(0)
s_sub_u32 s88,s88,1
s_cmp_eq_u32 s88,0
s_cbranch_scc1 L_fo9_%=
v_cmp_ne_u32 s[90:91],s68,v26
v_cmp_ne_u32 s[92:93],s69,v27
s_nop 0
s_or_b64 s[90:91],s[90:91],s[92:93]
s_cbranch_scc1 L_fl9_%=
s_branch L_fo9_%=
)A"
R"A(L_fr10_%=:
s_mov_b32 s88,262144
L_fl10_%=:
buffer_load_dwordx2 v[26:27],v24,s[64:67],s94 offen sc1
s_waitcnt vmcnt(0)
s_sub_u32 s88,s88,1
s_cmp_eq_u32 s88,0
s_cbranch_scc1 L_fo10_%=
v_cmp_ne_u32 s[90:91],s68,v26
v_cmp_ne_u32 s[92:93],s69,v27
s_nop 0
s_or_b64 s[90:91],s[90:91],s[92:93]
s_cbranch_scc1 L_fl10_%=
s_branch L_fo10_%=
L_fr11_%=:
s_mov_b32 s88,262144
L_fl11_%=:
buffer_load_dwordx2 v[26:27],v24,s[64:67],s94 offen offset:2048 sc1
s_waitcnt vmcnt(0)
s_sub_u32 s88,s88,1
s_cmp_eq_u32 s88,0
s_cbranch_scc1 L_fo11_%=
v_cmp_ne_u32 s[90:91],s68,v26
v_cmp_ne_u32 s[92:93],s69,v27
s_nop 0
s_or_b64 s[90:91],s[90:91],s[92:93]
s_cbranch_scc1 L_fl11_%=
s_branch L_fo11_%=
L_fr12_%=:
s_mov_b32 s88,262144
L_fl12_%=:
buffer_load_dwordx2 v[26:27],v24,s[64:67],s94 offen offset:3072 sc1
s_waitcnt vmcnt(0)
s_sub_u32 s88,s88,1
s_cmp_eq_u32 s88,0
s_cbranch_scc1 L_fo12_%=
v_cmp_ne_u32 s[90:91],s68,v26
v_cmp_ne_u32 s[92:93],s69,v27
s_nop 0
s_or_b64 s[90:91],s[90:91],s[92:93]
s_cbranch_scc1 L_fl12_%=
s_branch L_fo12_%=
L_out_end_%=:
L_fast_end_%=:
L_slow0_%=:
s_mov_b32 s98,0
s_branch L_slowc_%=
L_slow1_%=:
s_mov_b32 s98,1
L_slowc_%=:
s_nop 7
s_nop 7
s_waitcnt vmcnt(0) lgkmcnt(0)
s_lshl_b32 s96,%1,2
s_add_u32 s96,s96,s40
s_lshl_b32 s96,s96,15
s_add_u32 s96,s96,167772160
s_mov_b64 s[56:57],s[12:13]
s_add_u32 s56,s56,s96
s_addc_u32 s57,s57,0
s_and_b32 s57,s57,0xffff
s_mov_b32 s58,32768
s_mov_b32 s59,0x20000
s_mov_b32 s96,0
buffer_store_dwordx4 v[64:67],v6,s[56:59],s96 offen
buffer_store_dwordx4 v[68:71],v6,s[56:59],s96 offen offset:1024
buffer_store_dwordx4 v[72:75],v6,s[56:59],s96 offen offset:2048
buffer_store_dwordx4 v[76:79],v6,s[56:59],s96 offen offset:3072
s_mov_b32 s96,4096
buffer_store_dwordx4 v[80:83],v6,s[56:59],s96 offen
buffer_store_dwordx4 v[84:87],v6,s[56:59],s96 offen offset:1024
buffer_store_dwordx4 v[88:91],v6,s[56:59],s96 offen offset:2048
buffer_store_dwordx4 v[92:95],v6,s[56:59],s96 offen offset:3072
s_mov_b32 s96,8192
buffer_store_dwordx4 v[96:99],v6,s[56:59],s96 offen
buffer_store_dwordx4 v[100:103],v6,s[56:59],s96 offen offset:1024
buffer_store_dwordx4 v[104:107],v6,s[56:59],s96 offen offset:2048
buffer_store_dwordx4 v[108:111],v6,s[56:59],s96 offen offset:3072
s_mov_b32 s96,12288
buffer_store_dwordx4 v[112:115],v6,s[56:59],s96 offen
buffer_store_dwordx4 v[116:119],v6,s[56:59],s96 offen offset:1024
buffer_store_dwordx4 v[120:123],v6,s[56:59],s96 offen offset:2048
buffer_store_dwordx4 v[124:127],v6,s[56:59],s96 offen offset:3072
s_mov_b32 s96,16384
buffer_store_dwordx4 v[128:131],v6,s[56:59],s96 offen
buffer_store_dwordx4 v[132:135],v6,s[56:59],s96 offen offset:1024
buffer_store_dwordx4 v[136:139],v6,s[56:59],s96 offen offset:2048
buffer_store_dwordx4 v[140:143],v6,s[56:59],s96 offen offset:3072
s_mov_b32 s96,20480
buffer_store_dwordx4 v[144:147],v6,s[56:59],s96 offen
buffer_store_dwordx4 v[148:151],v6,s[56:59],s96 offen offset:1024
buffer_store_dwordx4 v[152:155],v6,s[56:59],s96 offen offset:2048
buffer_store_dwordx4 v[156:159],v6,s[56:59],s96 offen offset:3072
s_mov_b32 s96,24576
buffer_store_dwordx4 v[160:163],v6,s[56:59],s96 offen
buffer_store_dwordx4 v[164:167],v6,s[56:59],s96 offen offset:1024
buffer_store_dwordx4 v[168:171],v6,s[56:59],s96 offen offset:2048
buffer_store_dwordx4 v[172:175],v6,s[56:59],s96 offen offset:3072
s_mov_b32 s96,28672
buffer_store_dwordx4 v[176:179],v6,s[56:59],s96 offen
buffer_store_dwordx4 v[180:183],v6,s[56:59],s96 offen offset:1024
buffer_store_dwordx4 v[184:187],v6,s[56:59],s96 offen offset:2048
buffer_store_dwordx4 v[188:191],v6,s[56:59],s96 offen offset:3072
s_waitcnt vmcnt(0)
v_writelane_b32 v173,s88,0
v_writelane_b32 v173,s89,1
v_writelane_b32 v173,s90,2
v_writelane_b32 v173,s91,3
v_writelane_b32 v173,s92,4
v_writelane_b32 v173,s93,5
v_writelane_b32 v173,s94,6
v_writelane_b32 v173,s95,7
s_mov_b32 s88,s98
s_mul_i32 s90,s41,307200
s_mov_b64 s[92:93],s[4:5]
s_add_u32 s92,s92,s90
s_addc_u32 s93,s93,0
s_and_b32 s93,s93,0xffff
s_mov_b32 s94,307200
s_mov_b32 s95,0x20000
s_mov_b64 s[96:97],s[10:11]
s_and_b32 s97,s97,0xffff
s_mov_b32 s98,5242880
s_mov_b32 s99,0x20000
v_bfe_u32 v171,v6,4,4
v_lshrrev_b32 v172,8,v6
v_lshlrev_b32 v164,8,v171
v_lshl_add_u32 v164,v172,4,v164
v_lshlrev_b32 v165,10,v172
v_lshl_add_u32 v165,v171,1,v165
v_add_u32 v166,4096,v165
v_lshlrev_b32 v168,16,v172
v_lshrrev_b32 v170,2,v171
v_lshl_add_u32 v168,v170,4,v168
v_and_b32 v170,3,v171
v_lshl_add_u32 v168,v170,1,v168
s_lshl_b32 s90,s42,1
s_add_u32 s90,s90,s88
s_lshl_b32 s90,s90,8
v_add_u32 v168,s90,v168
s_mov_b32 s89,0
L_sjb_%=:
s_add_u32 s90,s89,s70
s_add_u32 s90,s90,1
s_and_b32 s90,s90,3
s_lshl_b32 s91,s40,2
s_add_u32 s90,s90,s91
s_lshl_b32 s90,s90,4
v_bfe_u32 v171,v6,4,4
v_lshrrev_b32 v172,8,v6
v_add_u32 v167,s90,v171
v_mul_u32_u24 v167,1280,v167
v_lshl_add_u32 v167,v172,4,v167
s_lshl_b32 s90,s88,4
s_add_u32 s90,s90,s89
s_lshl_b32 s90,s90,12
v_add_u32 v169,s90,v6
v_mov_b32 v64,0
v_mov_b32 v65,0
v_mov_b32 v66,0
v_mov_b32 v67,0
v_mov_b32 v68,0
v_mov_b32 v69,0
v_mov_b32 v70,0
v_mov_b32 v71,0
v_mov_b32 v72,0
v_mov_b32 v73,0
v_mov_b32 v74,0
v_mov_b32 v75,0
v_mov_b32 v76,0
v_mov_b32 v77,0
v_mov_b32 v78,0
v_mov_b32 v79,0
v_mov_b32 v80,0
v_mov_b32 v81,0
v_mov_b32 v82,0
v_mov_b32 v83,0
v_mov_b32 v84,0
v_mov_b32 v85,0
v_mov_b32 v86,0
v_mov_b32 v87,0
v_mov_b32 v88,0
v_mov_b32 v89,0
v_mov_b32 v90,0
v_mov_b32 v91,0
v_mov_b32 v92,0
v_mov_b32 v93,0
v_mov_b32 v94,0
v_mov_b32 v95,0
s_mov_b32 s90,0
L_sq_%=:
s_lshl_b32 s91,s90,6
buffer_load_dwordx4 v[160:163],v167,s[92:95],s91 offen
s_lshl_b32 s91,s90,18
buffer_load_ushort v112,v168,s[96:99],s91 offen
buffer_load_ushort v120,v168,s[96:99],s91 offen offset:8
buffer_load_ushort v128,v168,s[96:99],s91 offen offset:64
buffer_load_ushort v136,v168,s[96:99],s91 offen offset:72
s_add_u32 s91,s91,8192
buffer_load_ushort v113,v168,s[96:99],s91 offen
buffer_load_ushort v121,v168,s[96:99],s91 offen offset:8
buffer_load_ushort v129,v168,s[96:99],s91 offen offset:64
buffer_load_ushort v137,v168,s[96:99],s91 offen offset:72
s_add_u32 s91,s91,8192
buffer_load_ushort v114,v168,s[96:99],s91 offen
buffer_load_ushort v122,v168,s[96:99],s91 offen offset:8
buffer_load_ushort v130,v168,s[96:99],s91 offen offset:64
buffer_load_ushort v138,v168,s[96:99],s91 offen offset:72
s_add_u32 s91,s91,8192
buffer_load_ushort v115,v168,s[96:99],s91 offen
buffer_load_ushort v123,v168,s[96:99],s91 offen offset:8
buffer_load_ushort v131,v168,s[96:99],s91 offen offset:64
buffer_load_ushort v139,v168,s[96:99],s91 offen offset:72
s_add_u32 s91,s91,8192
buffer_load_ushort v116,v168,s[96:99],s91 offen
buffer_load_ushort v124,v168,s[96:99],s91 offen offset:8
buffer_load_ushort v132,v168,s[96:99],s91 offen offset:64
buffer_load_ushort v140,v168,s[96:99],s91 offen offset:72
s_add_u32 s91,s91,8192
buffer_load_ushort v117,v168,s[96:99],s91 offen
buffer_load_ushort v125,v168,s[96:99],s91 offen offset:8
buffer_load_ushort v133,v168,s[96:99],s91 offen offset:64
buffer_load_ushort v141,v168,s[96:99],s91 offen offset:72
s_add_u32 s91,s91,8192
buffer_load_ushort v118,v168,s[96:99],s91 offen
buffer_load_ushort v126,v168,s[96:99],s91 offen offset:8
buffer_load_ushort v134,v168,s[96:99],s91 offen offset:64
buffer_load_ushort v142,v168,s[96:99],s91 offen offset:72
s_add_u32 s91,s91,8192
buffer_load_ushort v119,v168,s[96:99],s91 offen
buffer_load_ushort v127,v168,s[96:99],s91 offen offset:8
buffer_load_ushort v135,v168,s[96:99],s91 offen offset:64
buffer_load_ushort v143,v168,s[96:99],s91 offen offset:72
s_add_u32 s91,s91,8192
s_waitcnt vmcnt(0)
v_lshl_or_b32 v112,v113,16,v112
v_lshl_or_b32 v113,v115,16,v114
v_lshl_or_b32 v114,v117,16,v116
v_lshl_or_b32 v115,v119,16,v118
v_lshl_or_b32 v120,v121,16,v120
v_lshl_or_b32 v121,v123,16,v122
v_lshl_or_b32 v122,v125,16,v124
v_lshl_or_b32 v123,v127,16,v126
v_lshl_or_b32 v128,v129,16,v128
v_lshl_or_b32 v129,v131,16,v130
v_lshl_or_b32 v130,v133,16,v132
v_lshl_or_b32 v131,v135,16,v134
v_lshl_or_b32 v136,v137,16,v136
v_lshl_or_b32 v137,v139,16,v138
v_lshl_or_b32 v138,v141,16,v140
v_lshl_or_b32 v139,v143,16,v142
s_nop 1
v_mfma_f32_16x16x32_bf16 v[64:67],v[112:115],v[160:163],v[64:67]
v_mfma_f32_16x16x32_bf16 v[68:71],v[120:123],v[160:163],v[68:71]
v_mfma_f32_16x16x32_bf16 v[72:75],v[128:131],v[160:163],v[72:75]
v_mfma_f32_16x16x32_bf16 v[76:79],v[136:139],v[160:163],v[76:79]
s_nop 7
s_lshl_b32 s91,s90,18
buffer_load_ushort v112,v168,s[96:99],s91 offen offset:128
buffer_load_ushort v120,v168,s[96:99],s91 offen offset:136
buffer_load_ushort v128,v168,s[96:99],s91 offen offset:192
buffer_load_ushort v136,v168,s[96:99],s91 offen offset:200
s_add_u32 s91,s91,8192
buffer_load_ushort v113,v168,s[96:99],s91 offen offset:128
buffer_load_ushort v121,v168,s[96:99],s91 offen offset:136
buffer_load_ushort v129,v168,s[96:99],s91 offen offset:192
buffer_load_ushort v137,v168,s[96:99],s91 offen offset:200
s_add_u32 s91,s91,8192
buffer_load_ushort v114,v168,s[96:99],s91 offen offset:128
buffer_load_ushort v122,v168,s[96:99],s91 offen offset:136
buffer_load_ushort v130,v168,s[96:99],s91 offen offset:192
buffer_load_ushort v138,v168,s[96:99],s91 offen offset:200
s_add_u32 s91,s91,8192
buffer_load_ushort v115,v168,s[96:99],s91 offen offset:128
buffer_load_ushort v123,v168,s[96:99],s91 offen offset:136
buffer_load_ushort v131,v168,s[96:99],s91 offen offset:192
buffer_load_ushort v139,v168,s[96:99],s91 offen offset:200
s_add_u32 s91,s91,8192
buffer_load_ushort v116,v168,s[96:99],s91 offen offset:128
buffer_load_ushort v124,v168,s[96:99],s91 offen offset:136
buffer_load_ushort v132,v168,s[96:99],s91 offen offset:192
buffer_load_ushort v140,v168,s[96:99],s91 offen offset:200
s_add_u32 s91,s91,8192
buffer_load_ushort v117,v168,s[96:99],s91 offen offset:128
buffer_load_ushort v125,v168,s[96:99],s91 offen offset:136
buffer_load_ushort v133,v168,s[96:99],s91 offen offset:192
buffer_load_ushort v141,v168,s[96:99],s91 offen offset:200
s_add_u32 s91,s91,8192
buffer_load_ushort v118,v168,s[96:99],s91 offen offset:128
buffer_load_ushort v126,v168,s[96:99],s91 offen offset:136
buffer_load_ushort v134,v168,s[96:99],s91 offen offset:192
buffer_load_ushort v142,v168,s[96:99],s91 offen offset:200
s_add_u32 s91,s91,8192
buffer_load_ushort v119,v168,s[96:99],s91 offen offset:128
buffer_load_ushort v127,v168,s[96:99],s91 offen offset:136
buffer_load_ushort v135,v168,s[96:99],s91 offen offset:192
buffer_load_ushort v143,v168,s[96:99],s91 offen offset:200
s_add_u32 s91,s91,8192
s_waitcnt vmcnt(0)
v_lshl_or_b32 v112,v113,16,v112
v_lshl_or_b32 v113,v115,16,v114
v_lshl_or_b32 v114,v117,16,v116
v_lshl_or_b32 v115,v119,16,v118
v_lshl_or_b32 v120,v121,16,v120
v_lshl_or_b32 v121,v123,16,v122
v_lshl_or_b32 v122,v125,16,v124
v_lshl_or_b32 v123,v127,16,v126
v_lshl_or_b32 v128,v129,16,v128
v_lshl_or_b32 v129,v131,16,v130
v_lshl_or_b32 v130,v133,16,v132
v_lshl_or_b32 v131,v135,16,v134
v_lshl_or_b32 v136,v137,16,v136
v_lshl_or_b32 v137,v139,16,v138
v_lshl_or_b32 v138,v141,16,v140
v_lshl_or_b32 v139,v143,16,v142
s_nop 1
v_mfma_f32_16x16x32_bf16 v[80:83],v[112:115],v[160:163],v[80:83]
v_mfma_f32_16x16x32_bf16 v[84:87],v[120:123],v[160:163],v[84:87]
v_mfma_f32_16x16x32_bf16 v[88:91],v[128:131],v[160:163],v[88:91]
v_mfma_f32_16x16x32_bf16 v[92:95],v[136:139],v[160:163],v[92:95]
s_nop 7
s_add_u32 s90,s90,1
s_cmp_lg_u32 s90,20
s_cbranch_scc1 L_sq_%=
s_nop 7
v_mul_f32 v64,s61,v64
v_mul_f32 v65,s61,v65
v_mul_f32 v66,s61,v66
v_mul_f32 v67,s61,v67
v_mul_f32 v68,s61,v68
v_mul_f32 v69,s61,v69
v_mul_f32 v70,s61,v70
v_mul_f32 v71,s61,v71
v_mul_f32 v72,s61,v72
v_mul_f32 v73,s61,v73
v_mul_f32 v74,s61,v74
v_mul_f32 v75,s61,v75
v_mul_f32 v76,s61,v76
v_mul_f32 v77,s61,v77
v_mul_f32 v78,s61,v78
v_mul_f32 v79,s61,v79
v_mul_f32 v80,s61,v80
v_mul_f32 v81,s61,v81
v_mul_f32 v82,s61,v82
v_mul_f32 v83,s61,v83
v_mul_f32 v84,s61,v84
v_mul_f32 v85,s61,v85
v_mul_f32 v86,s61,v86
v_mul_f32 v87,s61,v87
v_mul_f32 v88,s61,v88
v_mul_f32 v89,s61,v89
v_mul_f32 v90,s61,v90
v_mul_f32 v91,s61,v91
v_mul_f32 v92,s61,v92
v_mul_f32 v93,s61,v93
v_mul_f32 v94,s61,v94
v_mul_f32 v95,s61,v95
v_cvt_pk_bf16_f32 v96,v64,v65
v_cvt_pk_bf16_f32 v97,v66,v67
v_cvt_pk_bf16_f32 v98,v68,v69
)A"
R"A(v_cvt_pk_bf16_f32 v99,v70,v71
v_cvt_pk_bf16_f32 v100,v72,v73
v_cvt_pk_bf16_f32 v101,v74,v75
v_cvt_pk_bf16_f32 v102,v76,v77
v_cvt_pk_bf16_f32 v103,v78,v79
v_cvt_pk_bf16_f32 v104,v80,v81
v_cvt_pk_bf16_f32 v105,v82,v83
v_cvt_pk_bf16_f32 v106,v84,v85
v_cvt_pk_bf16_f32 v107,v86,v87
v_cvt_pk_bf16_f32 v108,v88,v89
v_cvt_pk_bf16_f32 v109,v90,v91
v_cvt_pk_bf16_f32 v110,v92,v93
v_cvt_pk_bf16_f32 v111,v94,v95
v_mov_b32 v170,0xff800000
s_lshl_b32 s91,s88,18
s_mov_b32 s90,64
L_s1_%=:
buffer_load_dwordx4 v[112:115],v164,s[20:23],s91 offen
buffer_load_dwordx4 v[116:119],v164,s[20:23],s91 offen offset:64
buffer_load_dwordx4 v[120:123],v164,s[20:23],s91 offen offset:128
buffer_load_dwordx4 v[124:127],v164,s[20:23],s91 offen offset:192
s_waitcnt vmcnt(0)
v_mfma_f32_16x16x32_bf16 v[144:147],v[112:115],v[96:99],0
v_mfma_f32_16x16x32_bf16 v[144:147],v[116:119],v[100:103],v[144:147]
v_mfma_f32_16x16x32_bf16 v[144:147],v[120:123],v[104:107],v[144:147]
v_mfma_f32_16x16x32_bf16 v[144:147],v[124:127],v[108:111],v[144:147]
s_nop 7
v_max3_f32 v170,v170,v144,v145
v_max3_f32 v170,v170,v146,v147
s_add_u32 s91,s91,4096
s_sub_u32 s90,s90,1
s_cmp_lg_u32 s90,0
s_cbranch_scc1 L_s1_%=
v_mov_b32 v171,v170
s_nop 1
v_permlane32_swap_b32 v170,v171
v_max_f32 v170,v170,v171
v_mov_b32 v171,v170
s_nop 1
v_permlane16_swap_b32 v170,v171
v_max_f32 v170,v170,v171
v_sub_f32 v152,0,v170
v_mov_b32 v156,0
v_sub_f32 v153,0,v170
v_mov_b32 v157,0
v_sub_f32 v154,0,v170
v_mov_b32 v158,0
v_sub_f32 v155,0,v170
v_mov_b32 v159,0
v_mov_b32 v64,0
v_mov_b32 v65,0
v_mov_b32 v66,0
v_mov_b32 v67,0
v_mov_b32 v68,0
v_mov_b32 v69,0
v_mov_b32 v70,0
v_mov_b32 v71,0
v_mov_b32 v72,0
v_mov_b32 v73,0
v_mov_b32 v74,0
v_mov_b32 v75,0
v_mov_b32 v76,0
v_mov_b32 v77,0
v_mov_b32 v78,0
v_mov_b32 v79,0
v_mov_b32 v80,0
v_mov_b32 v81,0
v_mov_b32 v82,0
v_mov_b32 v83,0
v_mov_b32 v84,0
v_mov_b32 v85,0
v_mov_b32 v86,0
v_mov_b32 v87,0
v_mov_b32 v88,0
v_mov_b32 v89,0
v_mov_b32 v90,0
v_mov_b32 v91,0
v_mov_b32 v92,0
v_mov_b32 v93,0
v_mov_b32 v94,0
v_mov_b32 v95,0
s_lshl_b32 s91,s88,18
s_mov_b32 s90,32
v_add_u32 v172,4096,v164
L_s2_%=:
buffer_load_dwordx4 v[112:115],v164,s[20:23],s91 offen
buffer_load_dwordx4 v[116:119],v164,s[20:23],s91 offen offset:64
buffer_load_dwordx4 v[120:123],v164,s[20:23],s91 offen offset:128
buffer_load_dwordx4 v[124:127],v164,s[20:23],s91 offen offset:192
buffer_load_dwordx4 v[128:131],v172,s[20:23],s91 offen
buffer_load_dwordx4 v[132:135],v172,s[20:23],s91 offen offset:64
buffer_load_dwordx4 v[136:139],v172,s[20:23],s91 offen offset:128
buffer_load_dwordx4 v[140:143],v172,s[20:23],s91 offen offset:192
s_waitcnt vmcnt(0)
v_mfma_f32_16x16x32_bf16 v[144:147],v[112:115],v[96:99],v[152:155]
v_mfma_f32_16x16x32_bf16 v[144:147],v[116:119],v[100:103],v[144:147]
v_mfma_f32_16x16x32_bf16 v[144:147],v[120:123],v[104:107],v[144:147]
v_mfma_f32_16x16x32_bf16 v[144:147],v[124:127],v[108:111],v[144:147]
v_mfma_f32_16x16x32_bf16 v[148:151],v[128:131],v[96:99],v[152:155]
v_mfma_f32_16x16x32_bf16 v[148:151],v[132:135],v[100:103],v[148:151]
v_mfma_f32_16x16x32_bf16 v[148:151],v[136:139],v[104:107],v[148:151]
v_mfma_f32_16x16x32_bf16 v[148:151],v[140:143],v[108:111],v[148:151]
s_nop 7
v_exp_f32 v144,v144
v_exp_f32 v145,v145
v_exp_f32 v146,v146
v_exp_f32 v147,v147
v_exp_f32 v148,v148
v_exp_f32 v149,v149
v_exp_f32 v150,v150
v_exp_f32 v151,v151
s_nop 0
v_cvt_pk_bf16_f32 v144,v144,v145
v_cvt_pk_bf16_f32 v145,v146,v147
v_cvt_pk_bf16_f32 v146,v148,v149
v_cvt_pk_bf16_f32 v147,v150,v151
buffer_load_ushort v112,v165,s[24:27],s91 offen
buffer_load_ushort v113,v165,s[24:27],s91 offen offset:256
buffer_load_ushort v114,v165,s[24:27],s91 offen offset:512
buffer_load_ushort v115,v165,s[24:27],s91 offen offset:768
buffer_load_ushort v116,v166,s[24:27],s91 offen
buffer_load_ushort v117,v166,s[24:27],s91 offen offset:256
buffer_load_ushort v118,v166,s[24:27],s91 offen offset:512
buffer_load_ushort v119,v166,s[24:27],s91 offen offset:768
buffer_load_ushort v120,v165,s[24:27],s91 offen offset:32
buffer_load_ushort v121,v165,s[24:27],s91 offen offset:288
buffer_load_ushort v122,v165,s[24:27],s91 offen offset:544
buffer_load_ushort v123,v165,s[24:27],s91 offen offset:800
buffer_load_ushort v124,v166,s[24:27],s91 offen offset:32
buffer_load_ushort v125,v166,s[24:27],s91 offen offset:288
buffer_load_ushort v126,v166,s[24:27],s91 offen offset:544
buffer_load_ushort v127,v166,s[24:27],s91 offen offset:800
buffer_load_ushort v128,v165,s[24:27],s91 offen offset:64
buffer_load_ushort v129,v165,s[24:27],s91 offen offset:320
buffer_load_ushort v130,v165,s[24:27],s91 offen offset:576
buffer_load_ushort v131,v165,s[24:27],s91 offen offset:832
buffer_load_ushort v132,v166,s[24:27],s91 offen offset:64
buffer_load_ushort v133,v166,s[24:27],s91 offen offset:320
buffer_load_ushort v134,v166,s[24:27],s91 offen offset:576
buffer_load_ushort v135,v166,s[24:27],s91 offen offset:832
buffer_load_ushort v136,v165,s[24:27],s91 offen offset:96
buffer_load_ushort v137,v165,s[24:27],s91 offen offset:352
buffer_load_ushort v138,v165,s[24:27],s91 offen offset:608
buffer_load_ushort v139,v165,s[24:27],s91 offen offset:864
buffer_load_ushort v140,v166,s[24:27],s91 offen offset:96
buffer_load_ushort v141,v166,s[24:27],s91 offen offset:352
buffer_load_ushort v142,v166,s[24:27],s91 offen offset:608
buffer_load_ushort v143,v166,s[24:27],s91 offen offset:864
s_waitcnt vmcnt(0)
v_lshl_or_b32 v112,v113,16,v112
v_lshl_or_b32 v113,v115,16,v114
v_lshl_or_b32 v114,v117,16,v116
v_lshl_or_b32 v115,v119,16,v118
v_lshl_or_b32 v120,v121,16,v120
v_lshl_or_b32 v121,v123,16,v122
v_lshl_or_b32 v122,v125,16,v124
v_lshl_or_b32 v123,v127,16,v126
v_lshl_or_b32 v128,v129,16,v128
v_lshl_or_b32 v129,v131,16,v130
v_lshl_or_b32 v130,v133,16,v132
v_lshl_or_b32 v131,v135,16,v134
v_lshl_or_b32 v136,v137,16,v136
v_lshl_or_b32 v137,v139,16,v138
v_lshl_or_b32 v138,v141,16,v140
v_lshl_or_b32 v139,v143,16,v142
s_nop 1
v_mfma_f32_16x16x32_bf16 v[64:67],v[112:115],v[144:147],v[64:67]
v_mfma_f32_16x16x32_bf16 v[68:71],v[120:123],v[144:147],v[68:71]
v_mfma_f32_16x16x32_bf16 v[72:75],v[128:131],v[144:147],v[72:75]
v_mfma_f32_16x16x32_bf16 v[76:79],v[136:139],v[144:147],v[76:79]
s_nop 7
buffer_load_ushort v112,v165,s[24:27],s91 offen offset:128
buffer_load_ushort v113,v165,s[24:27],s91 offen offset:384
buffer_load_ushort v114,v165,s[24:27],s91 offen offset:640
buffer_load_ushort v115,v165,s[24:27],s91 offen offset:896
buffer_load_ushort v116,v166,s[24:27],s91 offen offset:128
buffer_load_ushort v117,v166,s[24:27],s91 offen offset:384
buffer_load_ushort v118,v166,s[24:27],s91 offen offset:640
buffer_load_ushort v119,v166,s[24:27],s91 offen offset:896
buffer_load_ushort v120,v165,s[24:27],s91 offen offset:160
buffer_load_ushort v121,v165,s[24:27],s91 offen offset:416
buffer_load_ushort v122,v165,s[24:27],s91 offen offset:672
buffer_load_ushort v123,v165,s[24:27],s91 offen offset:928
buffer_load_ushort v124,v166,s[24:27],s91 offen offset:160
buffer_load_ushort v125,v166,s[24:27],s91 offen offset:416
buffer_load_ushort v126,v166,s[24:27],s91 offen offset:672
buffer_load_ushort v127,v166,s[24:27],s91 offen offset:928
buffer_load_ushort v128,v165,s[24:27],s91 offen offset:192
buffer_load_ushort v129,v165,s[24:27],s91 offen offset:448
buffer_load_ushort v130,v165,s[24:27],s91 offen offset:704
buffer_load_ushort v131,v165,s[24:27],s91 offen offset:960
buffer_load_ushort v132,v166,s[24:27],s91 offen offset:192
buffer_load_ushort v133,v166,s[24:27],s91 offen offset:448
buffer_load_ushort v134,v166,s[24:27],s91 offen offset:704
buffer_load_ushort v135,v166,s[24:27],s91 offen offset:960
buffer_load_ushort v136,v165,s[24:27],s91 offen offset:224
buffer_load_ushort v137,v165,s[24:27],s91 offen offset:480
buffer_load_ushort v138,v165,s[24:27],s91 offen offset:736
buffer_load_ushort v139,v165,s[24:27],s91 offen offset:992
buffer_load_ushort v140,v166,s[24:27],s91 offen offset:224
buffer_load_ushort v141,v166,s[24:27],s91 offen offset:480
buffer_load_ushort v142,v166,s[24:27],s91 offen offset:736
buffer_load_ushort v143,v166,s[24:27],s91 offen offset:992
s_waitcnt vmcnt(0)
v_lshl_or_b32 v112,v113,16,v112
v_lshl_or_b32 v113,v115,16,v114
v_lshl_or_b32 v114,v117,16,v116
v_lshl_or_b32 v115,v119,16,v118
v_lshl_or_b32 v120,v121,16,v120
v_lshl_or_b32 v121,v123,16,v122
v_lshl_or_b32 v122,v125,16,v124
v_lshl_or_b32 v123,v127,16,v126
v_lshl_or_b32 v128,v129,16,v128
v_lshl_or_b32 v129,v131,16,v130
v_lshl_or_b32 v130,v133,16,v132
v_lshl_or_b32 v131,v135,16,v134
v_lshl_or_b32 v136,v137,16,v136
v_lshl_or_b32 v137,v139,16,v138
v_lshl_or_b32 v138,v141,16,v140
v_lshl_or_b32 v139,v143,16,v142
s_nop 1
v_mfma_f32_16x16x32_bf16 v[80:83],v[112:115],v[144:147],v[80:83]
v_mfma_f32_16x16x32_bf16 v[84:87],v[120:123],v[144:147],v[84:87]
v_mfma_f32_16x16x32_bf16 v[88:91],v[128:131],v[144:147],v[88:91]
v_mfma_f32_16x16x32_bf16 v[92:95],v[136:139],v[144:147],v[92:95]
s_nop 7
v_mfma_f32_16x16x32_bf16 v[156:159],v[28:31],v[144:147],v[156:159]
s_nop 7
s_add_u32 s91,s91,8192
s_sub_u32 s90,s90,1
s_cmp_lg_u32 s90,0
s_cbranch_scc1 L_s2_%=
v_rcp_f32 v170,v156
s_nop 0
v_mul_f32 v64,v170,v64
v_mul_f32 v65,v170,v65
v_mul_f32 v66,v170,v66
v_mul_f32 v67,v170,v67
v_mul_f32 v68,v170,v68
v_mul_f32 v69,v170,v69
v_mul_f32 v70,v170,v70
v_mul_f32 v71,v170,v71
v_mul_f32 v72,v170,v72
v_mul_f32 v73,v170,v73
v_mul_f32 v74,v170,v74
v_mul_f32 v75,v170,v75
v_mul_f32 v76,v170,v76
v_mul_f32 v77,v170,v77
v_mul_f32 v78,v170,v78
v_mul_f32 v79,v170,v79
v_mul_f32 v80,v170,v80
v_mul_f32 v81,v170,v81
v_mul_f32 v82,v170,v82
v_mul_f32 v83,v170,v83
v_mul_f32 v84,v170,v84
v_mul_f32 v85,v170,v85
v_mul_f32 v86,v170,v86
v_mul_f32 v87,v170,v87
v_mul_f32 v88,v170,v88
v_mul_f32 v89,v170,v89
v_mul_f32 v90,v170,v90
v_mul_f32 v91,v170,v91
v_mul_f32 v92,v170,v92
v_mul_f32 v93,v170,v93
v_mul_f32 v94,v170,v94
v_mul_f32 v95,v170,v95
v_cvt_pk_bf16_f32 v96,v64,v65
v_cvt_pk_bf16_f32 v97,v66,v67
v_cvt_pk_bf16_f32 v98,v68,v69
v_cvt_pk_bf16_f32 v99,v70,v71
v_cvt_pk_bf16_f32 v100,v72,v73
v_cvt_pk_bf16_f32 v101,v74,v75
v_cvt_pk_bf16_f32 v102,v76,v77
v_cvt_pk_bf16_f32 v103,v78,v79
v_cvt_pk_bf16_f32 v104,v80,v81
v_cvt_pk_bf16_f32 v105,v82,v83
v_cvt_pk_bf16_f32 v106,v84,v85
v_cvt_pk_bf16_f32 v107,v86,v87
v_cvt_pk_bf16_f32 v108,v88,v89
v_cvt_pk_bf16_f32 v109,v90,v91
v_cvt_pk_bf16_f32 v110,v92,v93
v_cvt_pk_bf16_f32 v111,v94,v95
buffer_store_dwordx4 v[96:99],v169,s[36:39],s55 offen sc1
buffer_store_dwordx4 v[100:103],v169,s[36:39],s55 offen offset:1024 sc1
buffer_store_dwordx4 v[104:107],v169,s[36:39],s55 offen offset:2048 sc1
buffer_store_dwordx4 v[108:111],v169,s[36:39],s55 offen offset:3072 sc1
s_waitcnt vmcnt(0)
s_add_u32 s89,s89,1
s_cmp_lg_u32 s89,4
s_cbranch_scc1 L_sjb_%=
s_mov_b32 s98,s88
v_readlane_b32 s88,v173,0
v_readlane_b32 s89,v173,1
v_readlane_b32 s90,v173,2
v_readlane_b32 s91,v173,3
v_readlane_b32 s92,v173,4
v_readlane_b32 s93,v173,5
v_readlane_b32 s94,v173,6
v_readlane_b32 s95,v173,7
s_nop 4
s_mov_b32 s96,0
buffer_load_dwordx4 v[64:67],v6,s[56:59],s96 offen sc1
buffer_load_dwordx4 v[68:71],v6,s[56:59],s96 offen offset:1024 sc1
buffer_load_dwordx4 v[72:75],v6,s[56:59],s96 offen offset:2048 sc1
buffer_load_dwordx4 v[76:79],v6,s[56:59],s96 offen offset:3072 sc1
s_mov_b32 s96,4096
buffer_load_dwordx4 v[80:83],v6,s[56:59],s96 offen sc1
buffer_load_dwordx4 v[84:87],v6,s[56:59],s96 offen offset:1024 sc1
buffer_load_dwordx4 v[88:91],v6,s[56:59],s96 offen offset:2048 sc1
buffer_load_dwordx4 v[92:95],v6,s[56:59],s96 offen offset:3072 sc1
s_mov_b32 s96,8192
buffer_load_dwordx4 v[96:99],v6,s[56:59],s96 offen sc1
buffer_load_dwordx4 v[100:103],v6,s[56:59],s96 offen offset:1024 sc1
buffer_load_dwordx4 v[104:107],v6,s[56:59],s96 offen offset:2048 sc1
buffer_load_dwordx4 v[108:111],v6,s[56:59],s96 offen offset:3072 sc1
s_mov_b32 s96,12288
buffer_load_dwordx4 v[112:115],v6,s[56:59],s96 offen sc1
buffer_load_dwordx4 v[116:119],v6,s[56:59],s96 offen offset:1024 sc1
buffer_load_dwordx4 v[120:123],v6,s[56:59],s96 offen offset:2048 sc1
buffer_load_dwordx4 v[124:127],v6,s[56:59],s96 offen offset:3072 sc1
s_mov_b32 s96,16384
buffer_load_dwordx4 v[128:131],v6,s[56:59],s96 offen sc1
)A"
R"A(buffer_load_dwordx4 v[132:135],v6,s[56:59],s96 offen offset:1024 sc1
buffer_load_dwordx4 v[136:139],v6,s[56:59],s96 offen offset:2048 sc1
buffer_load_dwordx4 v[140:143],v6,s[56:59],s96 offen offset:3072 sc1
s_mov_b32 s96,20480
buffer_load_dwordx4 v[144:147],v6,s[56:59],s96 offen sc1
buffer_load_dwordx4 v[148:151],v6,s[56:59],s96 offen offset:1024 sc1
buffer_load_dwordx4 v[152:155],v6,s[56:59],s96 offen offset:2048 sc1
buffer_load_dwordx4 v[156:159],v6,s[56:59],s96 offen offset:3072 sc1
s_mov_b32 s96,24576
buffer_load_dwordx4 v[160:163],v6,s[56:59],s96 offen sc1
buffer_load_dwordx4 v[164:167],v6,s[56:59],s96 offen offset:1024 sc1
buffer_load_dwordx4 v[168:171],v6,s[56:59],s96 offen offset:2048 sc1
buffer_load_dwordx4 v[172:175],v6,s[56:59],s96 offen offset:3072 sc1
s_mov_b32 s96,28672
buffer_load_dwordx4 v[176:179],v6,s[56:59],s96 offen sc1
buffer_load_dwordx4 v[180:183],v6,s[56:59],s96 offen offset:1024 sc1
buffer_load_dwordx4 v[184:187],v6,s[56:59],s96 offen offset:2048 sc1
buffer_load_dwordx4 v[188:191],v6,s[56:59],s96 offen offset:3072 sc1
s_waitcnt vmcnt(0)
s_cmp_lg_u32 s98,0
s_cbranch_scc1 L_sr1_%=
buffer_load_dwordx4 v[64:67],v6,s[36:39],s84 offen sc1
buffer_load_dwordx4 v[68:71],v6,s[36:39],s84 offen offset:1024 sc1
buffer_load_dwordx4 v[72:75],v6,s[36:39],s84 offen offset:2048 sc1
buffer_load_dwordx4 v[76:79],v6,s[36:39],s84 offen offset:3072 sc1
buffer_load_dwordx4 v[80:83],v6,s[36:39],s85 offen sc1
buffer_load_dwordx4 v[84:87],v6,s[36:39],s85 offen offset:1024 sc1
buffer_load_dwordx4 v[88:91],v6,s[36:39],s85 offen offset:2048 sc1
buffer_load_dwordx4 v[92:95],v6,s[36:39],s85 offen offset:3072 sc1
buffer_load_dwordx4 v[96:99],v6,s[36:39],s86 offen sc1
buffer_load_dwordx4 v[100:103],v6,s[36:39],s86 offen offset:1024 sc1
buffer_load_dwordx4 v[104:107],v6,s[36:39],s86 offen offset:2048 sc1
buffer_load_dwordx4 v[108:111],v6,s[36:39],s86 offen offset:3072 sc1
buffer_load_dwordx4 v[112:115],v6,s[36:39],s87 offen sc1
buffer_load_dwordx4 v[116:119],v6,s[36:39],s87 offen offset:1024 sc1
buffer_load_dwordx4 v[120:123],v6,s[36:39],s87 offen offset:2048 sc1
buffer_load_dwordx4 v[124:127],v6,s[36:39],s87 offen offset:3072 sc1
s_waitcnt vmcnt(0)
s_nop 7
s_branch L_sret0_%=
L_sr1_%=:
buffer_load_dwordx4 v[136:139],v6,s[36:39],s84 offen sc1
buffer_load_dwordx4 v[140:143],v6,s[36:39],s84 offen offset:1024 sc1
buffer_load_dwordx4 v[152:155],v6,s[36:39],s84 offen offset:2048 sc1
buffer_load_dwordx4 v[156:159],v6,s[36:39],s84 offen offset:3072 sc1
buffer_load_dwordx4 v[168:171],v6,s[36:39],s85 offen sc1
buffer_load_dwordx4 v[172:175],v6,s[36:39],s85 offen offset:1024 sc1
buffer_load_dwordx4 v[184:187],v6,s[36:39],s85 offen offset:2048 sc1
buffer_load_dwordx4 v[188:191],v6,s[36:39],s85 offen offset:3072 sc1
buffer_load_dwordx4 v[200:203],v6,s[36:39],s86 offen sc1
buffer_load_dwordx4 v[204:207],v6,s[36:39],s86 offen offset:1024 sc1
buffer_load_dwordx4 v[216:219],v6,s[36:39],s86 offen offset:2048 sc1
buffer_load_dwordx4 v[220:223],v6,s[36:39],s86 offen offset:3072 sc1
buffer_load_dwordx4 v[232:235],v6,s[36:39],s87 offen sc1
buffer_load_dwordx4 v[236:239],v6,s[36:39],s87 offen offset:1024 sc1
buffer_load_dwordx4 v[248:251],v6,s[36:39],s87 offen offset:2048 sc1
buffer_load_dwordx4 v[252:255],v6,s[36:39],s87 offen offset:3072 sc1
s_waitcnt vmcnt(0)
s_nop 7
s_branch L_sret1_%=
L_end_%=:
)A"
    :: "s"(karg), "s"(blk), "s"(wid)
    : "v0", "v1", "v2", "v3", "v4", "v5", "v6", "v7", "v8", "v9", "v10", "v11", "v12", "v13", "v14", "v15", "v16", "v17", "v18", "v19", "v20", "v21", "v22", "v23", "v24", "v25", "v26", "v27", "v28", "v29", "v30", "v31", "v32", "v33", "v34", "v35", "v36", "v37", "v38", "v39", "v40", "v41", "v42", "v43", "v44", "v45", "v46", "v47", "v48", "v49", "v50", "v51", "v52", "v53", "v54", "v55", "v56", "v57", "v58", "v59", "v60", "v61", "v62", "v63", "v64", "v65", "v66", "v67", "v68", "v69", "v70", "v71", "v72", "v73", "v74", "v75", "v76", "v77", "v78", "v79", "v80", "v81", "v82", "v83", "v84", "v85", "v86", "v87", "v88", "v89", "v90", "v91", "v92", "v93", "v94", "v95", "v96", "v97", "v98", "v99", "v100", "v101", "v102", "v103", "v104", "v105", "v106", "v107", "v108", "v109", "v110", "v111", "v112", "v113", "v114", "v115", "v116", "v117", "v118", "v119", "v120", "v121", "v122", "v123", "v124", "v125", "v126", "v127", "v128", "v129", "v130", "v131", "v132", "v133", "v134", "v135", "v136", "v137", "v138", "v139", "v140", "v141", "v142", "v143", "v144", "v145", "v146", "v147", "v148", "v149", "v150", "v151", "v152", "v153", "v154", "v155", "v156", "v157", "v158", "v159", "v160", "v161", "v162", "v163", "v164", "v165", "v166", "v167", "v168", "v169", "v170", "v171", "v172", "v173", "v174", "v175", "v176", "v177", "v178", "v179", "v180", "v181", "v182", "v183", "v184", "v185", "v186", "v187", "v188", "v189", "v190", "v191", "v192", "v193", "v194", "v195", "v196", "v197", "v198", "v199", "v200", "v201", "v202", "v203", "v204", "v205", "v206", "v207", "v208", "v209", "v210", "v211", "v212", "v213", "v214", "v215", "v216", "v217", "v218", "v219", "v220", "v221", "v222", "v223", "v224", "v225", "v226", "v227", "v228", "v229", "v230", "v231", "v232", "v233", "v234", "v235", "v236", "v237", "v238", "v239", "v240", "v241", "v242", "v243", "v244", "v245", "v246", "v247", "v248", "v249", "v250", "v251", "v252", "v253", "v254", "v255", "a0", "a1", "a2", "a3", "a4", "a5", "a6", "a7", "a8", "a9", "a10", "a11", "a12", "a13", "a14", "a15", "a16", "a17", "a18", "a19", "a20", "a21", "a22", "a23", "a24", "a25", "a26", "a27", "a28", "a29", "a30", "a31", "a32", "a33", "a34", "a35", "a36", "a37", "a38", "a39", "a40", "a41", "a42", "a43", "a44", "a45", "a46", "a47", "a48", "a49", "a50", "a51", "a52", "a53", "a54", "a55", "a56", "a57", "a58", "a59", "a60", "a61", "a62", "a63", "a64", "a65", "a66", "a67", "a68", "a69", "a70", "a71", "a72", "a73", "a74", "a75", "a76", "a77", "a78", "a79", "a80", "a81", "a82", "a83", "a84", "a85", "a86", "a87", "a88", "a89", "a90", "a91", "a92", "a93", "a94", "a95", "a96", "a97", "a98", "a99", "a100", "a101", "a102", "a103", "a104", "a105", "a106", "a107", "a108", "a109", "a110", "a111", "a112", "a113", "a114", "a115", "a116", "a117", "a118", "a119", "a120", "a121", "a122", "a123", "a124", "a125", "a126", "a127", "a128", "a129", "a130", "a131", "a132", "a133", "a134", "a135", "a136", "a137", "a138", "a139", "a140", "a141", "a142", "a143", "a144", "a145", "a146", "a147", "a148", "a149", "a150", "a151", "a152", "a153", "a154", "a155", "a156", "a157", "a158", "a159", "a160", "a161", "a162", "a163", "a164", "a165", "a166", "a167", "a168", "a169", "a170", "a171", "a172", "a173", "a174", "a175", "a176", "a177", "a178", "a179", "a180", "a181", "a182", "a183", "a184", "a185", "a186", "a187", "a188", "a189", "a190", "a191", "a192", "a193", "a194", "a195", "a196", "a197", "a198", "a199", "a200", "a201", "a202", "a203", "a204", "a205", "a206", "a207", "a208", "a209", "a210", "a211", "a212", "a213", "a214", "a215", "a216", "a217", "a218", "a219", "a220", "a221", "a222", "a223", "a224", "a225", "a226", "a227", "a228", "a229", "a230", "a231", "a232", "a233", "a234", "a235", "a236", "a237", "a238", "a239", "a240", "a241", "a242", "a243", "a244", "a245", "a246", "a247", "a248", "a249", "a250", "a251", "a252", "a253", "a254", "a255", "s4", "s5", "s6", "s7", "s8", "s9", "s10", "s11", "s12", "s13", "s14", "s15", "s16", "s17", "s18", "s19", "s20", "s21", "s22", "s23", "s24", "s25", "s26", "s27", "s28", "s29", "s30", "s31", "s32", "s33", "s34", "s35", "s36", "s37", "s38", "s39", "s40", "s41", "s42", "s43", "s44", "s45", "s46", "s47", "s48", "s49", "s50", "s51", "s52", "s53", "s54", "s55", "s56", "s57", "s58", "s59", "s60", "s61", "s62", "s63", "s64", "s65", "s66", "s67", "s68", "s69", "s70", "s71", "s72", "s73", "s74", "s75", "s76", "s77", "s78", "s79", "s80", "s81", "s82", "s83", "s84", "s85", "s86", "s87", "s88", "s89", "s90", "s91", "s92", "s93", "s94", "s95", "s96", "s97", "s98", "s99", "s100", "s101", "vcc", "scc", "memory");
#endif
}

extern "C" void kernel_launch(void* const* d_in, const int* in_sizes, int n_in,
                              void* d_out, int out_size, void* d_ws, size_t ws_size,
                              hipStream_t stream) {
  (void)hipFuncSetAttribute((const void*)qattn_kernel, hipFuncAttributeMaxDynamicSharedMemorySize, LDS_ATT);
  qattn_kernel<<<256, 256, LDS_ATT, stream>>>(d_in[0], d_in[1], d_in[2], d_in[3], d_ws, d_in[4], d_out);
}
```

```cpp
#include <hip/hip_runtime.h>
#include <hip/hip_bf16.h>

constexpr int LDS_ATT = 163840;

__global__ __launch_bounds__(256, 1) void qattn_kernel(const void* __restrict__ x, const void* __restrict__ kc, const void* __restrict__ vc,
                                                       const void* __restrict__ wq, void* __restrict__ ws, const void* __restrict__ wo,
                                                       void* __restrict__ out) {
#if defined(__HIP_DEVICE_COMPILE__)
  const auto karg = __builtin_amdgcn_kernarg_segment_ptr();
  const unsigned blk = blockIdx.x;
  const unsigned wid = __builtin_amdgcn_readfirstlane(threadIdx.x >> 6);
  asm volatile(
R"A(s_memtime s[72:73]
s_memrealtime s[74:75]
s_load_dwordx8 s[4:11],%0,0x0
s_load_dwordx4 s[12:15],%0,0x20
s_load_dwordx2 s[16:17],%0,0x30
s_mov_b32 s40,%2
s_mov_b32 s43,%1
s_lshr_b32 s45,s43,3
v_mbcnt_lo_u32_b32 v12,-1,0
v_mbcnt_hi_u32_b32 v12,-1,v12
s_getpc_b64 s[88:89]
L_pc_%=:
s_and_b32 s88,s88,0xffffff80
s_and_b32 s89,s89,0xffff
s_mov_b32 s90,L_fast_end_%=-L_pc_%=
s_mov_b32 s91,0x20000
s_lshl_b32 s44,s45,2
s_add_u32 s44,s44,s40
s_mul_i32 s44,s44,896
v_lshlrev_b32 v67,7,v12
s_mov_b64 exec,127
buffer_load_dword v19,v67,s[88:91],s44 offen
s_mov_b64 exec,-1
s_and_b32 s70,s43,3
s_and_b32 s71,s45,3
s_lshl_b32 s42,s70,2
s_add_u32 s42,s42,s71
s_and_b32 s41,s43,4
s_lshl_b32 s41,s41,1
s_lshr_b32 s46,s45,2
s_add_u32 s41,s41,s46
s_lshl_b32 s56,s42,9
s_mov_b32 s57,0
s_lshl_b32 s59,s40,13
s_add_u32 s58,s59,65536
v_and_b32 v64,3,v12
v_bfe_u32 v65,v12,2,2
v_lshrrev_b32 v66,4,v12
s_lshl_b32 s44,s40,17
v_lshlrev_b32 v75,13,v65
v_lshl_add_u32 v75,v66,6,v75
v_lshl_add_u32 v75,v64,4,v75
v_add_u32 v75,s44,v75
v_add_u32 v56,0,v75
v_add_u32 v57,256,v75
v_add_u32 v58,32768,v75
v_add_u32 v59,33024,v75
v_add_u32 v60,65536,v75
v_add_u32 v61,65792,v75
v_add_u32 v62,98304,v75
v_add_u32 v63,98560,v75
s_waitcnt lgkmcnt(0)
s_mov_b64 s[32:33],s[10:11]
s_and_b32 s33,s33,0xffff
s_mov_b32 s34,5242880
s_mov_b32 s35,0x20000
s_add_u32 m0,s58,1024
s_nop 0
buffer_load_dwordx4 v57,s[32:35],s56 offen lds
s_add_u32 m0,s58,3072
s_nop 0
buffer_load_dwordx4 v59,s[32:35],s56 offen lds
s_add_u32 m0,s58,5120
s_nop 0
buffer_load_dwordx4 v61,s[32:35],s56 offen lds
s_add_u32 m0,s58,7168
s_nop 0
buffer_load_dwordx4 v63,s[32:35],s56 offen lds
v_lshrrev_b32 v75,3,v12
s_lshl_b32 s44,s40,6
v_add_u32 v75,s44,v75
v_mul_u32_u24 v75,1280,v75
v_and_b32 v76,7,v12
s_add_u32 s84,s70,1
s_and_b32 s84,s84,3
s_mul_i32 s84,s84,20480
s_add_u32 s84,s84,4096
s_add_u32 s85,s70,2
s_and_b32 s85,s85,3
s_mul_i32 s85,s85,20480
s_add_u32 s85,s85,2048
s_add_u32 s86,s70,3
s_and_b32 s86,s86,3
s_mul_i32 s86,s86,20480
s_add_u32 s86,s86,4096
s_add_u32 s87,s70,4
s_and_b32 s87,s87,3
s_mul_i32 s87,s87,20480
s_add_u32 s87,s87,2048
v_or_b32 v77,0,v66
v_xor_b32 v77,v77,v76
v_lshl_add_u32 v77,v77,4,v75
v_add_u32 v48,s84,v77
v_add_u32 v50,s85,v77
v_add_u32 v52,s86,v77
v_add_u32 v54,s87,v77
v_or_b32 v77,4,v66
v_xor_b32 v77,v77,v76
v_lshl_add_u32 v77,v77,4,v75
v_add_u32 v77,9216,v77
v_add_u32 v49,s84,v77
v_add_u32 v51,s85,v77
v_add_u32 v53,s86,v77
v_add_u32 v55,s87,v77
s_mul_i32 s44,s41,307200
s_mov_b64 s[28:29],s[4:5]
s_add_u32 s28,s28,s44
s_addc_u32 s29,s29,0
s_sub_u32 s28,s28,4096
s_subb_u32 s29,s29,0
s_and_b32 s29,s29,0xffff
s_mov_b32 s30,311296
s_mov_b32 s31,0x20000
s_add_u32 m0,s59,0
s_nop 0
buffer_load_dwordx4 v48,s[28:31],s57 offen lds
buffer_load_dwordx4 v49,s[28:31],s57 offen offset:1024 lds
buffer_load_dwordx4 v50,s[28:31],s57 offen offset:2048 lds
buffer_load_dwordx4 v51,s[28:31],s57 offen offset:3072 lds
s_add_u32 m0,s59,4096
s_nop 0
buffer_load_dwordx4 v52,s[28:31],s57 offen lds
buffer_load_dwordx4 v53,s[28:31],s57 offen offset:1024 lds
buffer_load_dwordx4 v54,s[28:31],s57 offen offset:2048 lds
buffer_load_dwordx4 v55,s[28:31],s57 offen offset:3072 lds
s_add_u32 s57,s57,128
s_xor_b32 s59,s59,131072
s_add_u32 m0,s58,0
s_nop 0
buffer_load_dwordx4 v56,s[32:35],s56 offen lds
s_add_u32 m0,s58,2048
s_nop 0
buffer_load_dwordx4 v58,s[32:35],s56 offen lds
s_add_u32 m0,s58,4096
s_nop 0
buffer_load_dwordx4 v60,s[32:35],s56 offen lds
s_add_u32 m0,s58,6144
s_nop 0
buffer_load_dwordx4 v62,s[32:35],s56 offen lds
s_add_u32 s56,s56,524288
s_add_u32 s58,s58,32768
s_cmp_ge_u32 s58,131072
s_cselect_b32 s62,98304,0
s_sub_u32 s58,s58,s62
s_add_u32 m0,s58,0
s_nop 0
buffer_load_dwordx4 v56,s[32:35],s56 offen lds
s_add_u32 m0,s58,1024
s_nop 0
buffer_load_dwordx4 v57,s[32:35],s56 offen lds
s_add_u32 m0,s58,2048
s_nop 0
buffer_load_dwordx4 v58,s[32:35],s56 offen lds
s_add_u32 m0,s58,3072
s_nop 0
buffer_load_dwordx4 v59,s[32:35],s56 offen lds
s_add_u32 m0,s58,4096
s_nop 0
buffer_load_dwordx4 v60,s[32:35],s56 offen lds
s_add_u32 m0,s58,5120
s_nop 0
buffer_load_dwordx4 v61,s[32:35],s56 offen lds
s_add_u32 m0,s58,6144
s_nop 0
buffer_load_dwordx4 v62,s[32:35],s56 offen lds
s_add_u32 m0,s58,7168
s_nop 0
buffer_load_dwordx4 v63,s[32:35],s56 offen lds
s_add_u32 s56,s56,524288
s_add_u32 s58,s58,32768
s_cmp_ge_u32 s58,131072
s_cselect_b32 s62,98304,0
s_sub_u32 s58,s58,s62
s_mov_b32 s60,65536
v_lshlrev_b32 v6,4,v12
v_lshlrev_b32 v72,12,v66
v_lshl_add_u32 v72,v65,6,v72
v_lshl_add_u32 v23,v64,4,v72
v_add_u32 v22,65536,v23
v_and_b32 v74,15,v12
v_lshrrev_b32 v76,1,v74
v_lshlrev_b32 v75,7,v74
s_lshl_b32 s44,s40,13
v_add_u32 v75,s44,v75
v_or_b32 v77,0,v66
v_xor_b32 v77,v77,v76
v_lshl_add_u32 v20,v77,4,v75
v_or_b32 v77,4,v66
v_xor_b32 v77,v77,v76
v_lshl_add_u32 v21,v77,4,v75
s_waitcnt vmcnt(12)
s_nop 0
s_memtime s[64:65]
s_memrealtime s[66:67]
s_bfe_u32 s68,%1,327683
s_waitcnt lgkmcnt(0)
s_sub_u32 s64,s64,s72
s_sub_u32 s66,s66,s74
s_mul_i32 s64,s64,10
s_mul_i32 s66,s66,215
s_cmp_lt_u32 s64,s66
s_cselect_b32 s95,64,0
s_mov_b32 %2,s95
s_cbranch_scc1 L_se1_%=
L_sg1_%=:
s_cmp_eq_u32 s68,0
s_cbranch_scc1 L_se1_%=
s_sleep 4
s_sub_u32 s68,s68,1
s_branch L_sg1_%=
L_se1_%=:
s_barrier
ds_read_b128 a[192:195],v20
ds_read_b64_tr_b16 a[128:129],v22 offset:1024
ds_read_b64_tr_b16 a[130:131],v22 offset:3072
ds_read_b128 a[196:199],v21
ds_read_b64_tr_b16 a[132:133],v22 offset:17408
ds_read_b64_tr_b16 a[134:135],v22 offset:19456
ds_read_b128 a[204:207],v21 offset:2048
ds_read_b128 a[200:203],v20 offset:2048
ds_read_b128 a[208:211],v20 offset:4096
ds_read_b128 a[212:215],v21 offset:4096
ds_read_b128 a[220:223],v21 offset:6144
ds_read_b128 a[216:219],v20 offset:6144
ds_read_b64_tr_b16 a[136:137],v22 offset:1032
ds_read_b64_tr_b16 a[138:139],v22 offset:3080
s_waitcnt lgkmcnt(11)
v_mfma_f32_16x16x32_bf16 v[128:131],a[128:131],a[192:195],0
s_add_u32 m0,s59,0
ds_read_b64_tr_b16 a[140:141],v22 offset:17416
ds_read_b64_tr_b16 a[142:143],v22 offset:19464
s_waitcnt lgkmcnt(10)
v_mfma_f32_16x16x32_bf16 v[128:131],a[132:135],a[196:199],v[128:131]
s_waitcnt lgkmcnt(9)
v_mfma_f32_16x16x32_bf16 v[136:139],a[132:135],a[204:207],0
s_waitcnt lgkmcnt(8)
v_mfma_f32_16x16x32_bf16 v[136:139],a[128:131],a[200:203],v[136:139]
s_waitcnt lgkmcnt(7)
v_mfma_f32_16x16x32_bf16 v[144:147],a[128:131],a[208:211],0
s_waitcnt lgkmcnt(6)
buffer_load_dwordx4 v48,s[28:31],s57 offen lds
v_mfma_f32_16x16x32_bf16 v[144:147],a[132:135],a[212:215],v[144:147]
s_waitcnt lgkmcnt(5)
v_mfma_f32_16x16x32_bf16 v[152:155],a[132:135],a[220:223],0
s_waitcnt lgkmcnt(4)
v_mfma_f32_16x16x32_bf16 v[152:155],a[128:131],a[216:219],v[152:155]
s_waitcnt lgkmcnt(2)
ds_read_b64_tr_b16 a[144:145],v22 offset:1280
ds_read_b64_tr_b16 a[146:147],v22 offset:3328
v_mfma_f32_16x16x32_bf16 v[132:135],a[136:139],a[192:195],0
s_waitcnt lgkmcnt(2)
ds_read_b64_tr_b16 a[148:149],v22 offset:17664
ds_read_b64_tr_b16 a[150:151],v22 offset:19712
v_mfma_f32_16x16x32_bf16 v[132:135],a[140:143],a[196:199],v[132:135]
buffer_load_dwordx4 v49,s[28:31],s57 offen offset:1024 lds
v_mfma_f32_16x16x32_bf16 v[140:143],a[140:143],a[204:207],0
v_mfma_f32_16x16x32_bf16 v[140:143],a[136:139],a[200:203],v[140:143]
v_mfma_f32_16x16x32_bf16 v[148:151],a[136:139],a[208:211],0
v_mfma_f32_16x16x32_bf16 v[148:151],a[140:143],a[212:215],v[148:151]
v_mfma_f32_16x16x32_bf16 v[156:159],a[140:143],a[220:223],0
buffer_load_dwordx4 v50,s[28:31],s57 offen offset:2048 lds
v_mfma_f32_16x16x32_bf16 v[156:159],a[136:139],a[216:219],v[156:159]
s_waitcnt lgkmcnt(2)
ds_read_b64_tr_b16 a[152:153],v22 offset:1288
ds_read_b64_tr_b16 a[154:155],v22 offset:3336
v_mfma_f32_16x16x32_bf16 v[160:163],a[144:147],a[192:195],0
s_waitcnt lgkmcnt(2)
ds_read_b64_tr_b16 a[156:157],v22 offset:17672
ds_read_b64_tr_b16 a[158:159],v22 offset:19720
v_mfma_f32_16x16x32_bf16 v[160:163],a[148:151],a[196:199],v[160:163]
v_mfma_f32_16x16x32_bf16 v[168:171],a[148:151],a[204:207],0
v_mfma_f32_16x16x32_bf16 v[168:171],a[144:147],a[200:203],v[168:171]
buffer_load_dwordx4 v51,s[28:31],s57 offen offset:3072 lds
v_mfma_f32_16x16x32_bf16 v[176:179],a[144:147],a[208:211],0
s_add_u32 m0,s59,4096
v_mfma_f32_16x16x32_bf16 v[176:179],a[148:151],a[212:215],v[176:179]
v_mfma_f32_16x16x32_bf16 v[184:187],a[148:151],a[220:223],0
v_mfma_f32_16x16x32_bf16 v[184:187],a[144:147],a[216:219],v[184:187]
s_waitcnt lgkmcnt(2)
v_mfma_f32_16x16x32_bf16 v[164:167],a[152:155],a[192:195],0
s_waitcnt lgkmcnt(0)
buffer_load_dwordx4 v52,s[28:31],s57 offen lds
ds_read_b64_tr_b16 a[160:161],v22 offset:1536
ds_read_b64_tr_b16 a[162:163],v22 offset:3584
v_mfma_f32_16x16x32_bf16 v[164:167],a[156:159],a[196:199],v[164:167]
ds_read_b64_tr_b16 a[164:165],v22 offset:17920
ds_read_b64_tr_b16 a[166:167],v22 offset:19968
v_mfma_f32_16x16x32_bf16 v[172:175],a[156:159],a[204:207],0
v_mfma_f32_16x16x32_bf16 v[172:175],a[152:155],a[200:203],v[172:175]
v_mfma_f32_16x16x32_bf16 v[180:183],a[152:155],a[208:211],0
v_mfma_f32_16x16x32_bf16 v[180:183],a[156:159],a[212:215],v[180:183]
buffer_load_dwordx4 v53,s[28:31],s57 offen offset:1024 lds
v_mfma_f32_16x16x32_bf16 v[188:191],a[156:159],a[220:223],0
s_waitcnt vmcnt(14)
v_mfma_f32_16x16x32_bf16 v[188:191],a[152:155],a[216:219],v[188:191]
s_barrier
ds_read_b64_tr_b16 a[168:169],v22 offset:1544
ds_read_b64_tr_b16 a[170:171],v22 offset:3592
s_waitcnt lgkmcnt(4)
v_mfma_f32_16x16x32_bf16 v[192:195],a[160:163],a[192:195],0
s_waitcnt lgkmcnt(2)
ds_read_b64_tr_b16 a[172:173],v22 offset:17928
ds_read_b64_tr_b16 a[174:175],v22 offset:19976
v_mfma_f32_16x16x32_bf16 v[192:195],a[164:167],a[196:199],v[192:195]
buffer_load_dwordx4 v54,s[28:31],s57 offen offset:2048 lds
v_mfma_f32_16x16x32_bf16 v[200:203],a[164:167],a[204:207],0
s_lshl_b32 s44,s41,13
s_add_u32 s44,s44,104857600
s_mov_b64 s[64:65],s[12:13]
v_mfma_f32_16x16x32_bf16 v[200:203],a[160:163],a[200:203],v[200:203]
s_add_u32 s64,s64,s44
s_addc_u32 s65,s65,0
s_and_b32 s65,s65,0xffff
v_mfma_f32_16x16x32_bf16 v[208:211],a[160:163],a[208:211],0
s_mov_b32 s66,8192
s_mov_b32 s67,0x20000
v_mov_b32 v14,4096
v_mfma_f32_16x16x32_bf16 v[208:211],a[164:167],a[212:215],v[208:211]
s_mov_b64 s[72:73],s[14:15]
buffer_load_dwordx4 v55,s[28:31],s57 offen offset:3072 lds
v_mfma_f32_16x16x32_bf16 v[216:219],a[164:167],a[220:223],0
s_add_u32 m0,s58,0
s_and_b32 s73,s73,0xffff
ds_read_b64_tr_b16 a[176:177],v22 offset:1792
v_mfma_f32_16x16x32_bf16 v[216:219],a[160:163],a[216:219],v[216:219]
s_mov_b32 s74,5242880
s_mov_b32 s75,0x20000
ds_read_b64_tr_b16 a[178:179],v22 offset:3840
s_waitcnt lgkmcnt(4)
v_mfma_f32_16x16x32_bf16 v[196:199],a[168:171],a[192:195],0
s_waitcnt lgkmcnt(2)
ds_read_b64_tr_b16 a[180:181],v22 offset:18176
ds_read_b64_tr_b16 a[182:183],v22 offset:20224
v_mfma_f32_16x16x32_bf16 v[196:199],a[172:175],a[196:199],v[196:199]
s_lshl_b32 s83,%1,2
buffer_load_dwordx4 v56,s[32:35],s56 offen lds
v_mfma_f32_16x16x32_bf16 v[204:207],a[172:175],a[204:207],0
s_add_u32 m0,s58,1024
v_lshlrev_b32 v15,3,v6
v_add_u32 v16,128,v15
v_mfma_f32_16x16x32_bf16 v[204:207],a[168:171],a[200:203],v[204:207]
s_add_u32 s83,s83,s40
ds_read_b64_tr_b16 a[184:185],v22 offset:1800
v_lshrrev_b32 v16,10,v16
v_mfma_f32_16x16x32_bf16 v[212:215],a[168:171],a[208:211],0
s_mul_i32 s83,s83,5120
ds_read_b64_tr_b16 a[186:187],v22 offset:3848
v_lshl_add_u32 v15,v16,24,v15
v_mfma_f32_16x16x32_bf16 v[212:215],a[172:175],a[212:215],v[212:215]
s_lshl_b32 s43,s41,5
buffer_load_dwordx4 v57,s[32:35],s56 offen lds
v_mfma_f32_16x16x32_bf16 v[220:223],a[172:175],a[220:223],0
s_lshl_b32 s44,s42,1
s_add_u32 s43,s43,s44
ds_read_b64_tr_b16 a[188:189],v22 offset:18184
v_mfma_f32_16x16x32_bf16 v[220:223],a[168:171],a[216:219],v[220:223]
s_add_u32 m0,s58,2048
s_lshl_b32 s44,s43,18
ds_read_b64_tr_b16 a[190:191],v22 offset:20232
s_waitcnt lgkmcnt(6)
v_mfma_f32_16x16x32_bf16 v[224:227],a[176:179],a[192:195],0
s_mov_b64 s[20:21],s[6:7]
s_mov_b32 s22,524288
ds_read_b64_tr_b16 a[128:129],v22
s_waitcnt lgkmcnt(5)
v_mfma_f32_16x16x32_bf16 v[224:227],a[180:183],a[196:199],v[224:227]
s_add_u32 s20,s20,s44
s_addc_u32 s21,s21,0
ds_read_b64_tr_b16 a[130:131],v22 offset:2048
v_mfma_f32_16x16x32_bf16 v[232:235],a[180:183],a[204:207],0
)A"
R"A(s_and_b32 s21,s21,0xffff
buffer_load_dwordx4 v58,s[32:35],s56 offen lds
v_mfma_f32_16x16x32_bf16 v[232:235],a[176:179],a[200:203],v[232:235]
s_mov_b32 s23,0x20000
s_mov_b64 s[24:25],s[8:9]
ds_read_b64_tr_b16 a[132:133],v22 offset:16384
v_mfma_f32_16x16x32_bf16 v[240:243],a[176:179],a[208:211],0
s_add_u32 m0,s58,3072
s_mov_b32 s26,524288
ds_read_b64_tr_b16 a[134:135],v22 offset:18432
v_mfma_f32_16x16x32_bf16 v[240:243],a[180:183],a[212:215],v[240:243]
s_add_u32 s24,s24,s44
s_addc_u32 s25,s25,0
ds_read_b64_tr_b16 a[136:137],v22 offset:8
v_mfma_f32_16x16x32_bf16 v[248:251],a[180:183],a[220:223],0
s_and_b32 s25,s25,0xffff
s_mov_b32 s27,0x20000
ds_read_b64_tr_b16 a[138:139],v22 offset:2056
v_mfma_f32_16x16x32_bf16 v[248:251],a[176:179],a[216:219],v[248:251]
s_lshl_b32 s44,s43,16
buffer_load_dwordx4 v59,s[32:35],s56 offen lds
s_waitcnt lgkmcnt(6)
v_mfma_f32_16x16x32_bf16 v[228:231],a[184:187],a[192:195],0
s_mov_b64 s[36:37],s[12:13]
s_mov_b32 s38,131072
ds_read_b64_tr_b16 a[140:141],v22 offset:16392
v_mfma_f32_16x16x32_bf16 v[228:231],a[188:191],a[196:199],v[228:231]
s_add_u32 m0,s58,4096
s_mov_b32 s39,0x20000
ds_read_b64_tr_b16 a[142:143],v22 offset:18440
v_mfma_f32_16x16x32_bf16 v[236:239],a[188:191],a[204:207],0
s_add_u32 s36,s36,s44
s_addc_u32 s37,s37,0
ds_read_b64_tr_b16 a[144:145],v22 offset:256
v_mfma_f32_16x16x32_bf16 v[236:239],a[184:187],a[200:203],v[236:239]
s_and_b32 s37,s37,0xffff
s_lshl_b32 s51,s40,12
ds_read_b64_tr_b16 a[146:147],v22 offset:2304
v_mfma_f32_16x16x32_bf16 v[244:247],a[184:187],a[208:211],0
s_add_u32 s52,s51,16384
buffer_load_dwordx4 v60,s[32:35],s56 offen lds
v_mfma_f32_16x16x32_bf16 v[244:247],a[188:191],a[212:215],v[244:247]
s_xor_b32 s59,s59,131072
s_mul_i32 s47,s41,5
ds_read_b64_tr_b16 a[148:149],v22 offset:16640
v_mfma_f32_16x16x32_bf16 v[252:255],a[188:191],a[220:223],0
s_add_u32 m0,s58,5120
s_add_u32 s47,s47,s42
ds_read_b64_tr_b16 a[150:151],v22 offset:18688
v_mfma_f32_16x16x32_bf16 v[252:255],a[184:187],a[216:219],v[252:255]
s_and_b32 s47,s47,15
s_add_u32 s57,s57,128
ds_read_b64_tr_b16 a[152:153],v22 offset:264
s_waitcnt lgkmcnt(7)
v_mfma_f32_16x16x32_bf16 a[0:3],a[128:131],a[192:195],0
s_mul_i32 s44,s41,5
s_add_u32 s44,s44,s42
ds_read_b64_tr_b16 a[154:155],v22 offset:2312
v_mfma_f32_16x16x32_bf16 a[0:3],a[132:135],a[196:199],a[0:3]
s_lshl_b32 s47,s44,12
buffer_load_dwordx4 v61,s[32:35],s56 offen lds
v_mfma_f32_16x16x32_bf16 a[32:35],a[132:135],a[204:207],0
s_and_b32 s47,s47,0x7000
s_lshl_b32 s44,s44,14
ds_read_b64_tr_b16 a[156:157],v22 offset:16648
v_mfma_f32_16x16x32_bf16 a[32:35],a[128:131],a[200:203],a[32:35]
s_add_u32 m0,s58,6144
s_and_b32 s44,s44,0x20000
ds_read_b64_tr_b16 a[158:159],v22 offset:18696
v_mfma_f32_16x16x32_bf16 a[64:67],a[128:131],a[208:211],0
s_or_b32 s47,s47,s44
s_mov_b32 s54,0
ds_read_b64_tr_b16 a[160:161],v22 offset:512
v_mfma_f32_16x16x32_bf16 a[64:67],a[132:135],a[212:215],a[64:67]
s_lshl_b32 s55,s40,14
s_mov_b32 s61,0x3e0293ee
ds_read_b64_tr_b16 a[162:163],v22 offset:2560
v_mfma_f32_16x16x32_bf16 a[96:99],a[132:135],a[220:223],0
s_mov_b32 s63,0x5d800000
buffer_load_dwordx4 v62,s[32:35],s56 offen lds
v_mfma_f32_16x16x32_bf16 a[96:99],a[128:131],a[216:219],a[96:99]
s_lshl_b32 s44,s40,2
s_add_u32 s44,s44,s70
ds_read_b64_tr_b16 a[164:165],v22 offset:16896
v_mfma_f32_16x16x32_bf16 a[4:7],a[136:139],a[192:195],0
s_add_u32 m0,s58,7168
s_mov_b32 s46,0xff800000
ds_read_b64_tr_b16 a[166:167],v22 offset:18944
s_waitcnt lgkmcnt(6)
v_mfma_f32_16x16x32_bf16 a[4:7],a[140:143],a[196:199],a[4:7]
s_cmp_eq_u32 s44,14
s_cselect_b32 s18,0x7149f2ca,s46
ds_read_b64_tr_b16 a[168:169],v22 offset:520
v_mfma_f32_16x16x32_bf16 a[36:39],a[140:143],a[204:207],0
s_cmp_eq_u32 s44,13
s_cselect_b32 s19,0x7149f2ca,s46
ds_read_b64_tr_b16 a[170:171],v22 offset:2568
v_mfma_f32_16x16x32_bf16 a[36:39],a[136:139],a[200:203],a[36:39]
s_add_u32 s60,s60,32768
buffer_load_dwordx4 v63,s[32:35],s56 offen lds
v_mfma_f32_16x16x32_bf16 a[68:71],a[136:139],a[208:211],0
s_cmp_eq_u32 s44,12
s_cselect_b32 s100,0x7149f2ca,s46
ds_read_b64_tr_b16 a[172:173],v22 offset:16904
v_mfma_f32_16x16x32_bf16 a[68:71],a[140:143],a[212:215],a[68:71]
s_add_u32 m0,s59,0
ds_read_b64_tr_b16 a[174:175],v22 offset:18952
v_lshlrev_b32 v70,15,v65
v_mfma_f32_16x16x32_bf16 a[100:103],a[140:143],a[220:223],0
s_cmp_eq_u32 s44,15
s_cselect_b32 s101,0x7149f2ca,s46
ds_read_b64_tr_b16 a[176:177],v22 offset:768
v_mfma_f32_16x16x32_bf16 a[100:103],a[136:139],a[216:219],a[100:103]
s_lshl_b32 s44,s40,8
ds_read_b64_tr_b16 a[178:179],v22 offset:2816
v_lshl_add_u32 v70,v66,6,v70
v_mfma_f32_16x16x32_bf16 a[8:11],a[144:147],a[192:195],0
s_add_u32 s58,s58,32768
buffer_load_dwordx4 v48,s[28:31],s57 offen lds
v_mfma_f32_16x16x32_bf16 a[8:11],a[148:151],a[196:199],a[8:11]
s_add_u32 s56,s56,524288
ds_read_b64_tr_b16 a[180:181],v22 offset:17152
v_add_u32 v70,s44,v70
v_mfma_f32_16x16x32_bf16 a[40:43],a[148:151],a[204:207],0
s_mul_i32 s81,s70,1310720
ds_read_b64_tr_b16 a[182:183],v22 offset:19200
v_lshl_add_u32 v4,v64,4,v70
v_mfma_f32_16x16x32_bf16 a[40:43],a[144:147],a[200:203],a[40:43]
s_mul_i32 s44,s71,320
ds_read_b64_tr_b16 a[184:185],v22 offset:776
v_xor_b32 v71,2,v64
v_mfma_f32_16x16x32_bf16 a[72:75],a[144:147],a[208:211],0
s_add_u32 s81,s81,s44
ds_read_b64_tr_b16 a[186:187],v22 offset:2824
v_lshl_add_u32 v5,v71,4,v70
v_mfma_f32_16x16x32_bf16 a[72:75],a[148:151],a[212:215],a[72:75]
s_mul_i32 s82,s40,10240
buffer_load_dwordx4 v49,s[28:31],s57 offen offset:1024 lds
v_mfma_f32_16x16x32_bf16 a[104:107],a[148:151],a[220:223],0
s_mul_i32 s44,s40,40960
ds_read_b64_tr_b16 a[188:189],v22 offset:17160
v_lshlrev_b32 v72,10,v65
v_mfma_f32_16x16x32_bf16 a[104:107],a[144:147],a[216:219],a[104:107]
s_lshr_b32 s92,%1,3
ds_read_b64_tr_b16 a[190:191],v22 offset:19208
v_lshl_add_u32 v72,v64,6,v72
v_mfma_f32_16x16x32_bf16 a[12:15],a[152:155],a[192:195],0
s_lshl_b32 s92,s92,2
v_and_b32 v73,2,v65
v_xor_b32 v73,v73,v66
s_waitcnt lgkmcnt(6)
v_mfma_f32_16x16x32_bf16 a[12:15],a[156:159],a[196:199],a[12:15]
s_cmp_eq_u32 s60,131072
s_cselect_b32 s60,32768,s60
v_lshl_add_u32 v72,v73,4,v72
v_mfma_f32_16x16x32_bf16 a[44:47],a[156:159],a[204:207],0
s_add_u32 s92,s92,s40
buffer_load_dwordx4 v50,s[28:31],s57 offen offset:2048 lds
v_mfma_f32_16x16x32_bf16 a[44:47],a[152:155],a[200:203],a[44:47]
s_lshl_b32 s92,s92,7
v_add_u32 v1,98304,v72
v_add_u32 v22,s60,v23
v_mfma_f32_16x16x32_bf16 a[76:79],a[152:155],a[208:211],0
s_add_u32 s93,s92,L_tailseg_%=-L_pc_%=
v_xor_b32 v20,131072,v20
v_xor_b32 v21,131072,v21
v_mfma_f32_16x16x32_bf16 a[76:79],a[156:159],a[212:215],a[76:79]
s_add_u32 s60,s60,32768
v_lshlrev_b32 v72,10,v66
v_lshl_add_u32 v72,v65,6,v72
v_mfma_f32_16x16x32_bf16 a[108:111],a[156:159],a[220:223],0
v_lshl_add_u32 v72,v64,3,v72
v_add_u32 v72,114688,v72
v_mfma_f32_16x16x32_bf16 a[108:111],a[152:155],a[216:219],a[108:111]
s_waitcnt vmcnt(11) lgkmcnt(0)
buffer_load_dwordx4 v51,s[28:31],s57 offen offset:3072 lds
v_mfma_f32_16x16x32_bf16 a[16:19],a[160:163],a[192:195],0
s_barrier
s_add_u32 m0,s59,4096
ds_read_b128 a[224:227],v20
v_mfma_f32_16x16x32_bf16 a[16:19],a[164:167],a[196:199],a[16:19]
s_xor_b32 s59,s59,131072
ds_read_b64_tr_b16 a[128:129],v22 offset:1024
v_and_b32 v73,1,v66
v_mfma_f32_16x16x32_bf16 a[48:51],a[164:167],a[204:207],0
ds_read_b64_tr_b16 a[130:131],v22 offset:3072
v_lshl_add_u32 v2,v73,5,v72
v_mfma_f32_16x16x32_bf16 a[48:51],a[160:163],a[200:203],a[48:51]
buffer_load_dwordx4 v52,s[28:31],s57 offen lds
v_mfma_f32_16x16x32_bf16 a[80:83],a[160:163],a[208:211],0
ds_read_b128 a[228:231],v21
v_xor_b32 v73,1,v73
v_mfma_f32_16x16x32_bf16 a[80:83],a[164:167],a[212:215],a[80:83]
s_cmp_ge_u32 s58,131072
s_cselect_b32 s62,98304,0
ds_read_b64_tr_b16 a[132:133],v22 offset:17408
v_mfma_f32_16x16x32_bf16 a[112:115],a[164:167],a[220:223],0
s_sub_u32 s58,s58,s62
ds_read_b64_tr_b16 a[134:135],v22 offset:19456
v_lshl_add_u32 v3,v73,5,v72
v_mfma_f32_16x16x32_bf16 a[112:115],a[160:163],a[216:219],a[112:115]
ds_read_b128 a[232:235],v20 offset:2048
v_mov_b32 v73,0x3f803f80
v_mfma_f32_16x16x32_bf16 a[20:23],a[168:171],a[192:195],0
buffer_load_dwordx4 v53,s[28:31],s57 offen offset:1024 lds
v_mfma_f32_16x16x32_bf16 a[20:23],a[172:175],a[196:199],a[20:23]
ds_read_b128 a[236:239],v21 offset:2048
v_add_u32 v70,0,v12
v_mfma_f32_16x16x32_bf16 a[52:55],a[172:175],a[204:207],0
ds_read_b128 a[240:243],v20 offset:4096
v_mul_u32_u24 v71,3277,v70
v_mfma_f32_16x16x32_bf16 a[52:55],a[168:171],a[200:203],a[52:55]
ds_read_b128 a[244:247],v21 offset:4096
v_lshrrev_b32 v71,16,v71
v_mfma_f32_16x16x32_bf16 a[84:87],a[168:171],a[208:211],0
buffer_load_dword v13,v14,s[64:67],0 offen sc1
v_mfma_f32_16x16x32_bf16 a[84:87],a[172:175],a[212:215],a[84:87]
ds_read_b128 a[248:251],v20 offset:6144
v_mul_u32_u24 v72,20,v71
v_mfma_f32_16x16x32_bf16 a[116:119],a[172:175],a[220:223],0
ds_read_b128 a[252:255],v21 offset:6144
v_sub_u32 v72,v70,v72
v_mfma_f32_16x16x32_bf16 a[116:119],a[168:171],a[216:219],a[116:119]
ds_read_b64_tr_b16 a[136:137],v22 offset:1032
v_add_u32 v70,64,v12
v_mfma_f32_16x16x32_bf16 a[24:27],a[176:179],a[192:195],0
buffer_load_dwordx4 v54,s[28:31],s57 offen offset:2048 lds
v_mfma_f32_16x16x32_bf16 a[24:27],a[180:183],a[196:199],a[24:27]
ds_read_b64_tr_b16 a[138:139],v22 offset:3080
v_lshlrev_b32 v24,10,v6
v_mfma_f32_16x16x32_bf16 a[56:59],a[180:183],a[204:207],0
s_cmp_eq_u32 s60,131072
s_cselect_b32 s60,32768,s60
ds_read_b64_tr_b16 a[140:141],v22 offset:17416
v_mfma_f32_16x16x32_bf16 a[56:59],a[176:179],a[200:203],a[56:59]
ds_read_b64_tr_b16 a[142:143],v22 offset:19464
v_xor_b32 v20,131072,v20
v_mfma_f32_16x16x32_bf16 a[88:91],a[176:179],a[208:211],0
buffer_load_dwordx4 v55,s[28:31],s57 offen offset:3072 lds
v_mfma_f32_16x16x32_bf16 a[88:91],a[180:183],a[212:215],a[88:91]
s_add_u32 s57,s57,128
ds_read_b64_tr_b16 a[144:145],v22 offset:1280
v_xor_b32 v21,131072,v21
v_mfma_f32_16x16x32_bf16 a[120:123],a[180:183],a[220:223],0
ds_read_b64_tr_b16 a[146:147],v22 offset:3328
v_mfma_f32_16x16x32_bf16 a[120:123],a[176:179],a[216:219],a[120:123]
ds_read_b64_tr_b16 a[148:149],v22 offset:17664
v_mfma_f32_16x16x32_bf16 a[28:31],a[184:187],a[192:195],0
ds_read_b64_tr_b16 a[150:151],v22 offset:19712
v_mfma_f32_16x16x32_bf16 a[28:31],a[188:191],a[196:199],a[28:31]
ds_read_b64_tr_b16 a[152:153],v22 offset:1288
ds_read_b64_tr_b16 a[154:155],v22 offset:3336
ds_read_b64_tr_b16 a[156:157],v22 offset:17672
ds_read_b64_tr_b16 a[158:159],v22 offset:19720
v_mfma_f32_16x16x32_bf16 a[60:63],a[188:191],a[204:207],0
v_mfma_f32_16x16x32_bf16 a[60:63],a[184:187],a[200:203],a[60:63]
v_cmp_gt_u32 vcc,2,v64
s_nop 1
v_cndmask_b32 v28,0,v73,vcc
v_mfma_f32_16x16x32_bf16 a[92:95],a[184:187],a[208:211],0
v_and_b32 v73,1,v64
v_mul_u32_u24 v73,0xaaaa,v73
v_mfma_f32_16x16x32_bf16 a[92:95],a[188:191],a[212:215],a[92:95]
v_add_u32 v73,0x4444,v73
v_lshl_or_b32 v27,v73,16,v73
v_mfma_f32_16x16x32_bf16 a[124:127],a[188:191],a[220:223],0
s_add_u32 m0,s58,0
v_mov_b32 v29,v28
v_mov_b32 v30,v28
v_mfma_f32_16x16x32_bf16 a[124:127],a[184:187],a[216:219],a[124:127]
s_waitcnt lgkmcnt(5)
ds_read_b64_tr_b16 a[160:161],v22 offset:1536
v_mov_b32 v31,v28
v_mfma_f32_16x16x32_bf16 v[128:131],a[128:131],a[224:227],v[128:131]
ds_read_b64_tr_b16 a[162:163],v22 offset:3584
v_bfe_u32 v73,v71,2,1
v_mfma_f32_16x16x32_bf16 v[128:131],a[132:135],a[228:231],v[128:131]
buffer_load_dwordx4 v56,s[32:35],s56 offen lds
v_mfma_f32_16x16x32_bf16 v[136:139],a[132:135],a[236:239],v[136:139]
ds_read_b64_tr_b16 a[164:165],v22 offset:17920
v_lshlrev_b32 v73,1,v73
v_mfma_f32_16x16x32_bf16 v[136:139],a[128:131],a[232:235],v[136:139]
s_add_u32 m0,s58,1024
ds_read_b64_tr_b16 a[166:167],v22 offset:19968
v_xor_b32 v72,v72,v73
v_mfma_f32_16x16x32_bf16 v[144:147],a[128:131],a[240:243],v[144:147]
v_mul_u32_u24 v71,1280,v71
v_lshl_add_u32 v71,v72,4,v71
v_mfma_f32_16x16x32_bf16 v[144:147],a[132:135],a[244:247],v[144:147]
v_add_u32 v71,s44,v71
v_subrev_u32 v7,0,v71
v_mfma_f32_16x16x32_bf16 v[152:155],a[132:135],a[252:255],v[152:155]
buffer_load_dwordx4 v57,s[32:35],s56 offen lds
v_mfma_f32_16x16x32_bf16 v[152:155],a[128:131],a[248:251],v[152:155]
ds_read_b64_tr_b16 a[168:169],v22 offset:1544
)A"
R"A(v_mul_u32_u24 v71,3277,v70
v_mfma_f32_16x16x32_bf16 v[132:135],a[136:139],a[224:227],v[132:135]
ds_read_b64_tr_b16 a[170:171],v22 offset:3592
v_lshrrev_b32 v71,16,v71
v_mfma_f32_16x16x32_bf16 v[132:135],a[140:143],a[228:231],v[132:135]
s_add_u32 m0,s58,2048
ds_read_b64_tr_b16 a[172:173],v22 offset:17928
v_mul_u32_u24 v72,20,v71
v_mfma_f32_16x16x32_bf16 v[140:143],a[140:143],a[236:239],v[140:143]
ds_read_b64_tr_b16 a[174:175],v22 offset:19976
v_sub_u32 v72,v70,v72
v_mfma_f32_16x16x32_bf16 v[140:143],a[136:139],a[232:235],v[140:143]
ds_read_b64_tr_b16 a[176:177],v22 offset:1792
v_bfe_u32 v73,v71,2,1
v_mfma_f32_16x16x32_bf16 v[148:151],a[136:139],a[240:243],v[148:151]
buffer_load_dwordx4 v58,s[32:35],s56 offen lds
v_mfma_f32_16x16x32_bf16 v[148:151],a[140:143],a[244:247],v[148:151]
ds_read_b64_tr_b16 a[178:179],v22 offset:3840
v_lshlrev_b32 v73,1,v73
v_mfma_f32_16x16x32_bf16 v[156:159],a[140:143],a[252:255],v[156:159]
ds_read_b64_tr_b16 a[180:181],v22 offset:18176
v_xor_b32 v72,v72,v73
v_mfma_f32_16x16x32_bf16 v[156:159],a[136:139],a[248:251],v[156:159]
s_add_u32 m0,s58,3072
ds_read_b64_tr_b16 a[182:183],v22 offset:20224
v_mul_u32_u24 v71,1280,v71
v_mfma_f32_16x16x32_bf16 v[160:163],a[144:147],a[224:227],v[160:163]
s_waitcnt lgkmcnt(6)
v_lshl_add_u32 v71,v72,4,v71
v_add_u32 v71,s44,v71
v_mfma_f32_16x16x32_bf16 v[160:163],a[148:151],a[228:231],v[160:163]
v_subrev_u32 v8,1024,v71
v_add_u32 v70,128,v12
v_mfma_f32_16x16x32_bf16 v[168:171],a[148:151],a[236:239],v[168:171]
buffer_load_dwordx4 v59,s[32:35],s56 offen lds
v_mfma_f32_16x16x32_bf16 v[168:171],a[144:147],a[232:235],v[168:171]
ds_read_b64_tr_b16 a[184:185],v22 offset:1800
v_mul_u32_u24 v71,3277,v70
v_mfma_f32_16x16x32_bf16 v[176:179],a[144:147],a[240:243],v[176:179]
s_add_u32 m0,s58,4096
ds_read_b64_tr_b16 a[186:187],v22 offset:3848
v_lshrrev_b32 v71,16,v71
v_mfma_f32_16x16x32_bf16 v[176:179],a[148:151],a[244:247],v[176:179]
ds_read_b64_tr_b16 a[188:189],v22 offset:18184
v_mul_u32_u24 v72,20,v71
v_mfma_f32_16x16x32_bf16 v[184:187],a[148:151],a[252:255],v[184:187]
ds_read_b64_tr_b16 a[190:191],v22 offset:20232
v_sub_u32 v72,v70,v72
v_mfma_f32_16x16x32_bf16 v[184:187],a[144:147],a[248:251],v[184:187]
buffer_load_dwordx4 v60,s[32:35],s56 offen lds
v_mfma_f32_16x16x32_bf16 v[164:167],a[152:155],a[224:227],v[164:167]
v_bfe_u32 v73,v71,2,1
v_lshlrev_b32 v73,1,v73
v_mfma_f32_16x16x32_bf16 v[164:167],a[156:159],a[228:231],v[164:167]
ds_read_b64_tr_b16 a[128:129],v22
v_xor_b32 v72,v72,v73
v_mfma_f32_16x16x32_bf16 v[172:175],a[156:159],a[236:239],v[172:175]
s_add_u32 m0,s58,5120
ds_read_b64_tr_b16 a[130:131],v22 offset:2048
v_mul_u32_u24 v71,1280,v71
v_mfma_f32_16x16x32_bf16 v[172:175],a[152:155],a[232:235],v[172:175]
ds_read_b64_tr_b16 a[132:133],v22 offset:16384
v_lshl_add_u32 v71,v72,4,v71
v_mfma_f32_16x16x32_bf16 v[180:183],a[152:155],a[240:243],v[180:183]
ds_read_b64_tr_b16 a[134:135],v22 offset:18432
v_add_u32 v71,s44,v71
v_mfma_f32_16x16x32_bf16 v[180:183],a[156:159],a[244:247],v[180:183]
buffer_load_dwordx4 v61,s[32:35],s56 offen lds
v_mfma_f32_16x16x32_bf16 v[188:191],a[156:159],a[252:255],v[188:191]
v_subrev_u32 v9,2048,v71
v_add_u32 v70,192,v12
v_mfma_f32_16x16x32_bf16 v[188:191],a[152:155],a[248:251],v[188:191]
ds_read_b64_tr_b16 a[136:137],v22 offset:8
v_mul_u32_u24 v71,3277,v70
v_mfma_f32_16x16x32_bf16 v[192:195],a[160:163],a[224:227],v[192:195]
s_add_u32 m0,s58,6144
ds_read_b64_tr_b16 a[138:139],v22 offset:2056
v_lshrrev_b32 v71,16,v71
v_mfma_f32_16x16x32_bf16 v[192:195],a[164:167],a[228:231],v[192:195]
ds_read_b64_tr_b16 a[140:141],v22 offset:16392
v_mul_u32_u24 v72,20,v71
v_mfma_f32_16x16x32_bf16 v[200:203],a[164:167],a[236:239],v[200:203]
ds_read_b64_tr_b16 a[142:143],v22 offset:18440
v_sub_u32 v72,v70,v72
v_mfma_f32_16x16x32_bf16 v[200:203],a[160:163],a[232:235],v[200:203]
buffer_load_dwordx4 v62,s[32:35],s56 offen lds
v_mfma_f32_16x16x32_bf16 v[208:211],a[160:163],a[240:243],v[208:211]
v_bfe_u32 v73,v71,2,1
v_lshlrev_b32 v73,1,v73
v_mfma_f32_16x16x32_bf16 v[208:211],a[164:167],a[244:247],v[208:211]
s_add_u32 m0,s58,7168
v_xor_b32 v72,v72,v73
v_mul_u32_u24 v71,1280,v71
v_mfma_f32_16x16x32_bf16 v[216:219],a[164:167],a[252:255],v[216:219]
s_add_u32 s58,s58,32768
ds_read_b64_tr_b16 a[144:145],v22 offset:256
v_lshl_add_u32 v71,v72,4,v71
v_mfma_f32_16x16x32_bf16 v[216:219],a[160:163],a[248:251],v[216:219]
ds_read_b64_tr_b16 a[146:147],v22 offset:2304
v_add_u32 v71,s44,v71
v_mfma_f32_16x16x32_bf16 v[196:199],a[168:171],a[224:227],v[196:199]
s_waitcnt lgkmcnt(4)
buffer_load_dwordx4 v63,s[32:35],s56 offen lds
v_mfma_f32_16x16x32_bf16 v[196:199],a[172:175],a[228:231],v[196:199]
s_add_u32 s56,s56,524288
ds_read_b64_tr_b16 a[148:149],v22 offset:16640
v_subrev_u32 v10,3072,v71
v_mfma_f32_16x16x32_bf16 v[204:207],a[172:175],a[236:239],v[204:207]
ds_read_b64_tr_b16 a[150:151],v22 offset:18688
v_add_u32 v70,256,v12
v_mfma_f32_16x16x32_bf16 v[204:207],a[168:171],a[232:235],v[204:207]
s_add_u32 m0,s59,0
v_mul_u32_u24 v71,3277,v70
v_lshrrev_b32 v71,16,v71
v_mfma_f32_16x16x32_bf16 v[212:215],a[168:171],a[240:243],v[212:215]
ds_read_b64_tr_b16 a[152:153],v22 offset:264
v_mul_u32_u24 v72,20,v71
v_mfma_f32_16x16x32_bf16 v[212:215],a[172:175],a[244:247],v[212:215]
ds_read_b64_tr_b16 a[154:155],v22 offset:2312
v_sub_u32 v72,v70,v72
v_mfma_f32_16x16x32_bf16 v[220:223],a[172:175],a[252:255],v[220:223]
buffer_load_dwordx4 v48,s[28:31],s57 offen lds
v_mfma_f32_16x16x32_bf16 v[220:223],a[168:171],a[248:251],v[220:223]
ds_read_b64_tr_b16 a[156:157],v22 offset:16648
v_bfe_u32 v73,v71,2,1
v_mfma_f32_16x16x32_bf16 v[224:227],a[176:179],a[224:227],v[224:227]
ds_read_b64_tr_b16 a[158:159],v22 offset:18696
v_lshlrev_b32 v73,1,v73
v_mfma_f32_16x16x32_bf16 v[224:227],a[180:183],a[228:231],v[224:227]
v_xor_b32 v72,v72,v73
v_mul_u32_u24 v71,1280,v71
v_mfma_f32_16x16x32_bf16 v[232:235],a[180:183],a[236:239],v[232:235]
ds_read_b64_tr_b16 a[160:161],v22 offset:512
v_lshl_add_u32 v71,v72,4,v71
v_mfma_f32_16x16x32_bf16 v[232:235],a[176:179],a[232:235],v[232:235]
ds_read_b64_tr_b16 a[162:163],v22 offset:2560
v_add_u32 v71,s44,v71
v_mfma_f32_16x16x32_bf16 v[240:243],a[176:179],a[240:243],v[240:243]
buffer_load_dwordx4 v49,s[28:31],s57 offen offset:1024 lds
v_mfma_f32_16x16x32_bf16 v[240:243],a[180:183],a[244:247],v[240:243]
ds_read_b64_tr_b16 a[164:165],v22 offset:16896
v_subrev_u32 v11,0,v71
v_mfma_f32_16x16x32_bf16 v[248:251],a[180:183],a[252:255],v[248:251]
s_cmp_ge_u32 s58,131072
s_cselect_b32 s62,98304,0
ds_read_b64_tr_b16 a[166:167],v22 offset:18944
v_mfma_f32_16x16x32_bf16 v[248:251],a[176:179],a[248:251],v[248:251]
s_sub_u32 s58,s58,s62
ds_read_b64_tr_b16 a[168:169],v22 offset:520
v_mfma_f32_16x16x32_bf16 v[228:231],a[184:187],a[224:227],v[228:231]
ds_read_b64_tr_b16 a[170:171],v22 offset:2568
v_mfma_f32_16x16x32_bf16 v[228:231],a[188:191],a[228:231],v[228:231]
buffer_load_dwordx4 v50,s[28:31],s57 offen offset:2048 lds
v_mfma_f32_16x16x32_bf16 v[236:239],a[188:191],a[236:239],v[236:239]
ds_read_b64_tr_b16 a[172:173],v22 offset:16904
v_mfma_f32_16x16x32_bf16 v[236:239],a[184:187],a[232:235],v[236:239]
ds_read_b64_tr_b16 a[174:175],v22 offset:18952
v_mfma_f32_16x16x32_bf16 v[244:247],a[184:187],a[240:243],v[244:247]
ds_read_b64_tr_b16 a[176:177],v22 offset:768
v_mfma_f32_16x16x32_bf16 v[244:247],a[188:191],a[244:247],v[244:247]
ds_read_b64_tr_b16 a[178:179],v22 offset:2816
v_mfma_f32_16x16x32_bf16 v[252:255],a[188:191],a[252:255],v[252:255]
ds_read_b64_tr_b16 a[180:181],v22 offset:17152
v_mfma_f32_16x16x32_bf16 v[252:255],a[184:187],a[248:251],v[252:255]
buffer_load_dwordx4 v51,s[28:31],s57 offen offset:3072 lds
v_mfma_f32_16x16x32_bf16 a[0:3],a[128:131],a[224:227],a[0:3]
ds_read_b64_tr_b16 a[182:183],v22 offset:19200
v_mfma_f32_16x16x32_bf16 a[0:3],a[132:135],a[228:231],a[0:3]
ds_read_b64_tr_b16 a[184:185],v22 offset:776
v_mfma_f32_16x16x32_bf16 a[32:35],a[132:135],a[236:239],a[32:35]
s_add_u32 m0,s59,4096
s_xor_b32 s59,s59,131072
ds_read_b64_tr_b16 a[186:187],v22 offset:2824
v_mfma_f32_16x16x32_bf16 a[32:35],a[128:131],a[232:235],a[32:35]
ds_read_b64_tr_b16 a[188:189],v22 offset:17160
v_mfma_f32_16x16x32_bf16 a[64:67],a[128:131],a[240:243],a[64:67]
ds_read_b64_tr_b16 a[190:191],v22 offset:19208
v_add_u32 v22,s60,v23
v_mfma_f32_16x16x32_bf16 a[64:67],a[132:135],a[244:247],a[64:67]
buffer_load_dwordx4 v52,s[28:31],s57 offen lds
v_mfma_f32_16x16x32_bf16 a[96:99],a[132:135],a[252:255],a[96:99]
v_mfma_f32_16x16x32_bf16 a[96:99],a[128:131],a[248:251],a[96:99]
v_mfma_f32_16x16x32_bf16 a[4:7],a[136:139],a[224:227],a[4:7]
s_waitcnt lgkmcnt(4)
v_mfma_f32_16x16x32_bf16 a[4:7],a[140:143],a[228:231],a[4:7]
v_mfma_f32_16x16x32_bf16 a[36:39],a[140:143],a[236:239],a[36:39]
buffer_load_dwordx4 v53,s[28:31],s57 offen offset:1024 lds
v_mfma_f32_16x16x32_bf16 a[36:39],a[136:139],a[232:235],a[36:39]
v_mfma_f32_16x16x32_bf16 a[68:71],a[136:139],a[240:243],a[68:71]
v_mfma_f32_16x16x32_bf16 a[68:71],a[140:143],a[244:247],a[68:71]
v_mfma_f32_16x16x32_bf16 a[100:103],a[140:143],a[252:255],a[100:103]
v_mfma_f32_16x16x32_bf16 a[100:103],a[136:139],a[248:251],a[100:103]
v_mfma_f32_16x16x32_bf16 a[8:11],a[144:147],a[224:227],a[8:11]
buffer_load_dwordx4 v54,s[28:31],s57 offen offset:2048 lds
v_mfma_f32_16x16x32_bf16 a[8:11],a[148:151],a[228:231],a[8:11]
v_mfma_f32_16x16x32_bf16 a[40:43],a[148:151],a[236:239],a[40:43]
v_mfma_f32_16x16x32_bf16 a[40:43],a[144:147],a[232:235],a[40:43]
v_mfma_f32_16x16x32_bf16 a[72:75],a[144:147],a[240:243],a[72:75]
v_mfma_f32_16x16x32_bf16 a[72:75],a[148:151],a[244:247],a[72:75]
v_mfma_f32_16x16x32_bf16 a[104:107],a[148:151],a[252:255],a[104:107]
buffer_load_dwordx4 v55,s[28:31],s57 offen offset:3072 lds
v_mfma_f32_16x16x32_bf16 a[104:107],a[144:147],a[248:251],a[104:107]
s_add_u32 s57,s57,128
v_mfma_f32_16x16x32_bf16 a[12:15],a[152:155],a[224:227],a[12:15]
v_mfma_f32_16x16x32_bf16 a[12:15],a[156:159],a[228:231],a[12:15]
v_mfma_f32_16x16x32_bf16 a[44:47],a[156:159],a[236:239],a[44:47]
v_mfma_f32_16x16x32_bf16 a[44:47],a[152:155],a[232:235],a[44:47]
v_mfma_f32_16x16x32_bf16 a[76:79],a[152:155],a[240:243],a[76:79]
v_mfma_f32_16x16x32_bf16 a[76:79],a[156:159],a[244:247],a[76:79]
v_mfma_f32_16x16x32_bf16 a[108:111],a[156:159],a[252:255],a[108:111]
v_mfma_f32_16x16x32_bf16 a[108:111],a[152:155],a[248:251],a[108:111]
s_waitcnt vmcnt(16) lgkmcnt(0)
v_mfma_f32_16x16x32_bf16 a[16:19],a[160:163],a[224:227],a[16:19]
s_barrier
ds_read_b128 a[192:195],v20
v_mfma_f32_16x16x32_bf16 a[16:19],a[164:167],a[228:231],a[16:19]
ds_read_b128 a[196:199],v21
v_mfma_f32_16x16x32_bf16 a[48:51],a[164:167],a[236:239],a[48:51]
ds_read_b128 a[200:203],v20 offset:2048
v_mfma_f32_16x16x32_bf16 a[48:51],a[160:163],a[232:235],a[48:51]
ds_read_b128 a[204:207],v21 offset:2048
v_mfma_f32_16x16x32_bf16 a[80:83],a[160:163],a[240:243],a[80:83]
ds_read_b128 a[208:211],v20 offset:4096
v_mfma_f32_16x16x32_bf16 a[80:83],a[164:167],a[244:247],a[80:83]
ds_read_b128 a[212:215],v21 offset:4096
v_mfma_f32_16x16x32_bf16 a[112:115],a[164:167],a[252:255],a[112:115]
ds_read_b128 a[216:219],v20 offset:6144
v_mfma_f32_16x16x32_bf16 a[112:115],a[160:163],a[248:251],a[112:115]
ds_read_b128 a[220:223],v21 offset:6144
v_mfma_f32_16x16x32_bf16 a[20:23],a[168:171],a[224:227],a[20:23]
ds_read_b64_tr_b16 a[128:129],v22 offset:1024
v_mfma_f32_16x16x32_bf16 a[20:23],a[172:175],a[228:231],a[20:23]
ds_read_b64_tr_b16 a[130:131],v22 offset:3072
v_mfma_f32_16x16x32_bf16 a[52:55],a[172:175],a[236:239],a[52:55]
ds_read_b64_tr_b16 a[132:133],v22 offset:17408
v_mfma_f32_16x16x32_bf16 a[52:55],a[168:171],a[232:235],a[52:55]
ds_read_b64_tr_b16 a[134:135],v22 offset:19456
v_mfma_f32_16x16x32_bf16 a[84:87],a[168:171],a[240:243],a[84:87]
ds_read_b64_tr_b16 a[136:137],v22 offset:1032
v_mfma_f32_16x16x32_bf16 a[84:87],a[172:175],a[244:247],a[84:87]
ds_read_b64_tr_b16 a[138:139],v22 offset:3080
v_mfma_f32_16x16x32_bf16 a[116:119],a[172:175],a[252:255],a[116:119]
ds_read_b64_tr_b16 a[140:141],v22 offset:17416
v_mfma_f32_16x16x32_bf16 a[116:119],a[168:171],a[248:251],a[116:119]
ds_read_b64_tr_b16 a[142:143],v22 offset:19464
v_mfma_f32_16x16x32_bf16 a[24:27],a[176:179],a[224:227],a[24:27]
)A"
R"A(ds_read_b64_tr_b16 a[144:145],v22 offset:1280
v_mfma_f32_16x16x32_bf16 a[24:27],a[180:183],a[228:231],a[24:27]
ds_read_b64_tr_b16 a[146:147],v22 offset:3328
v_mfma_f32_16x16x32_bf16 a[56:59],a[180:183],a[236:239],a[56:59]
ds_read_b64_tr_b16 a[148:149],v22 offset:17664
v_mfma_f32_16x16x32_bf16 a[56:59],a[176:179],a[232:235],a[56:59]
ds_read_b64_tr_b16 a[150:151],v22 offset:19712
v_mfma_f32_16x16x32_bf16 a[88:91],a[176:179],a[240:243],a[88:91]
ds_read_b64_tr_b16 a[152:153],v22 offset:1288
v_mfma_f32_16x16x32_bf16 a[88:91],a[180:183],a[244:247],a[88:91]
ds_read_b64_tr_b16 a[154:155],v22 offset:3336
v_mfma_f32_16x16x32_bf16 a[120:123],a[180:183],a[252:255],a[120:123]
ds_read_b64_tr_b16 a[156:157],v22 offset:17672
v_mfma_f32_16x16x32_bf16 a[120:123],a[176:179],a[248:251],a[120:123]
ds_read_b64_tr_b16 a[158:159],v22 offset:19720
v_mfma_f32_16x16x32_bf16 a[28:31],a[184:187],a[224:227],a[28:31]
v_mfma_f32_16x16x32_bf16 a[28:31],a[188:191],a[228:231],a[28:31]
v_mfma_f32_16x16x32_bf16 a[60:63],a[188:191],a[236:239],a[60:63]
v_mfma_f32_16x16x32_bf16 a[60:63],a[184:187],a[232:235],a[60:63]
v_mfma_f32_16x16x32_bf16 a[92:95],a[184:187],a[240:243],a[92:95]
v_mfma_f32_16x16x32_bf16 a[92:95],a[188:191],a[244:247],a[92:95]
v_mfma_f32_16x16x32_bf16 a[124:127],a[188:191],a[252:255],a[124:127]
v_mfma_f32_16x16x32_bf16 a[124:127],a[184:187],a[248:251],a[124:127]
s_mov_b32 s53,3
.p2align 5
L_qp_%=:
s_waitcnt lgkmcnt(1)
v_mfma_f32_16x16x32_bf16 v[128:131],a[128:131],a[192:195],v[128:131]
s_add_u32 m0,s58,0
s_add_u32 s60,s60,32768
ds_read_b64_tr_b16 a[160:161],v22 offset:1536
v_mfma_f32_16x16x32_bf16 v[128:131],a[132:135],a[196:199],v[128:131]
s_mov_b32 s90,L_att0x_%=-L_pc_%=+128
buffer_load_dwordx4 v56,s[32:35],s56 offen lds
v_mfma_f32_16x16x32_bf16 v[136:139],a[132:135],a[204:207],v[136:139]
s_cmp_eq_u32 s60,131072
s_cselect_b32 s60,32768,s60
ds_read_b64_tr_b16 a[162:163],v22 offset:3584
v_mfma_f32_16x16x32_bf16 v[136:139],a[128:131],a[200:203],v[136:139]
s_add_u32 m0,s58,1024
ds_read_b64_tr_b16 a[164:165],v22 offset:17920
v_xor_b32 v20,131072,v20
v_mfma_f32_16x16x32_bf16 v[144:147],a[128:131],a[208:211],v[144:147]
s_sub_u32 s53,s53,1
ds_read_b64_tr_b16 a[166:167],v22 offset:19968
v_xor_b32 v21,131072,v21
v_mfma_f32_16x16x32_bf16 v[144:147],a[132:135],a[212:215],v[144:147]
ds_read_b64_tr_b16 a[168:169],v22 offset:1544
v_mfma_f32_16x16x32_bf16 v[152:155],a[132:135],a[220:223],v[152:155]
buffer_load_dwordx4 v57,s[32:35],s56 offen lds
v_mfma_f32_16x16x32_bf16 v[152:155],a[128:131],a[216:219],v[152:155]
ds_read_b64_tr_b16 a[170:171],v22 offset:3592
v_mfma_f32_16x16x32_bf16 v[132:135],a[136:139],a[192:195],v[132:135]
ds_read_b64_tr_b16 a[172:173],v22 offset:17928
v_mfma_f32_16x16x32_bf16 v[132:135],a[140:143],a[196:199],v[132:135]
s_add_u32 m0,s58,2048
ds_read_b64_tr_b16 a[174:175],v22 offset:19976
v_mfma_f32_16x16x32_bf16 v[140:143],a[140:143],a[204:207],v[140:143]
ds_read_b64_tr_b16 a[176:177],v22 offset:1792
v_mfma_f32_16x16x32_bf16 v[140:143],a[136:139],a[200:203],v[140:143]
ds_read_b64_tr_b16 a[178:179],v22 offset:3840
v_mfma_f32_16x16x32_bf16 v[148:151],a[136:139],a[208:211],v[148:151]
buffer_load_dwordx4 v58,s[32:35],s56 offen lds
v_mfma_f32_16x16x32_bf16 v[148:151],a[140:143],a[212:215],v[148:151]
ds_read_b64_tr_b16 a[180:181],v22 offset:18176
v_mfma_f32_16x16x32_bf16 v[156:159],a[140:143],a[220:223],v[156:159]
s_add_u32 m0,s58,3072
ds_read_b64_tr_b16 a[182:183],v22 offset:20224
v_mfma_f32_16x16x32_bf16 v[156:159],a[136:139],a[216:219],v[156:159]
ds_read_b64_tr_b16 a[184:185],v22 offset:1800
v_mfma_f32_16x16x32_bf16 v[160:163],a[144:147],a[192:195],v[160:163]
ds_read_b64_tr_b16 a[186:187],v22 offset:3848
v_mfma_f32_16x16x32_bf16 v[160:163],a[148:151],a[196:199],v[160:163]
buffer_load_dwordx4 v59,s[32:35],s56 offen lds
v_mfma_f32_16x16x32_bf16 v[168:171],a[148:151],a[204:207],v[168:171]
ds_read_b64_tr_b16 a[188:189],v22 offset:18184
v_mfma_f32_16x16x32_bf16 v[168:171],a[144:147],a[200:203],v[168:171]
ds_read_b64_tr_b16 a[190:191],v22 offset:20232
v_mfma_f32_16x16x32_bf16 v[176:179],a[144:147],a[208:211],v[176:179]
s_add_u32 m0,s58,4096
ds_read_b64_tr_b16 a[128:129],v22
v_mfma_f32_16x16x32_bf16 v[176:179],a[148:151],a[212:215],v[176:179]
ds_read_b64_tr_b16 a[130:131],v22 offset:2048
v_mfma_f32_16x16x32_bf16 v[184:187],a[148:151],a[220:223],v[184:187]
ds_read_b64_tr_b16 a[132:133],v22 offset:16384
v_mfma_f32_16x16x32_bf16 v[184:187],a[144:147],a[216:219],v[184:187]
buffer_load_dwordx4 v60,s[32:35],s56 offen lds
v_mfma_f32_16x16x32_bf16 v[164:167],a[152:155],a[192:195],v[164:167]
s_waitcnt lgkmcnt(5)
ds_read_b64_tr_b16 a[134:135],v22 offset:18432
v_mfma_f32_16x16x32_bf16 v[164:167],a[156:159],a[196:199],v[164:167]
s_add_u32 m0,s58,5120
ds_read_b64_tr_b16 a[136:137],v22 offset:8
v_mfma_f32_16x16x32_bf16 v[172:175],a[156:159],a[204:207],v[172:175]
ds_read_b64_tr_b16 a[138:139],v22 offset:2056
v_mfma_f32_16x16x32_bf16 v[172:175],a[152:155],a[200:203],v[172:175]
ds_read_b64_tr_b16 a[140:141],v22 offset:16392
v_mfma_f32_16x16x32_bf16 v[180:183],a[152:155],a[208:211],v[180:183]
buffer_load_dwordx4 v61,s[32:35],s56 offen lds
v_mfma_f32_16x16x32_bf16 v[180:183],a[156:159],a[212:215],v[180:183]
ds_read_b64_tr_b16 a[142:143],v22 offset:18440
v_mfma_f32_16x16x32_bf16 v[188:191],a[156:159],a[220:223],v[188:191]
ds_read_b64_tr_b16 a[144:145],v22 offset:256
v_mfma_f32_16x16x32_bf16 v[188:191],a[152:155],a[216:219],v[188:191]
s_add_u32 m0,s58,6144
ds_read_b64_tr_b16 a[146:147],v22 offset:2304
v_mfma_f32_16x16x32_bf16 v[192:195],a[160:163],a[192:195],v[192:195]
ds_read_b64_tr_b16 a[148:149],v22 offset:16640
v_mfma_f32_16x16x32_bf16 v[192:195],a[164:167],a[196:199],v[192:195]
ds_read_b64_tr_b16 a[150:151],v22 offset:18688
v_mfma_f32_16x16x32_bf16 v[200:203],a[164:167],a[204:207],v[200:203]
buffer_load_dwordx4 v62,s[32:35],s56 offen lds
v_mfma_f32_16x16x32_bf16 v[200:203],a[160:163],a[200:203],v[200:203]
ds_read_b64_tr_b16 a[152:153],v22 offset:264
v_mfma_f32_16x16x32_bf16 v[208:211],a[160:163],a[208:211],v[208:211]
s_add_u32 m0,s58,7168
s_add_u32 s58,s58,32768
ds_read_b64_tr_b16 a[154:155],v22 offset:2312
v_mfma_f32_16x16x32_bf16 v[208:211],a[164:167],a[212:215],v[208:211]
s_cmp_ge_u32 s58,131072
s_cselect_b32 s62,98304,0
ds_read_b64_tr_b16 a[156:157],v22 offset:16648
v_mfma_f32_16x16x32_bf16 v[216:219],a[164:167],a[220:223],v[216:219]
s_sub_u32 s58,s58,s62
ds_read_b64_tr_b16 a[158:159],v22 offset:18696
v_mfma_f32_16x16x32_bf16 v[216:219],a[160:163],a[216:219],v[216:219]
buffer_load_dwordx4 v63,s[32:35],s56 offen lds
v_mfma_f32_16x16x32_bf16 v[196:199],a[168:171],a[192:195],v[196:199]
s_add_u32 s56,s56,524288
ds_read_b64_tr_b16 a[160:161],v22 offset:512
v_mfma_f32_16x16x32_bf16 v[196:199],a[172:175],a[196:199],v[196:199]
ds_read_b64_tr_b16 a[162:163],v22 offset:2560
v_mfma_f32_16x16x32_bf16 v[204:207],a[172:175],a[204:207],v[204:207]
s_add_u32 m0,s59,0
ds_read_b64_tr_b16 a[164:165],v22 offset:16896
v_mfma_f32_16x16x32_bf16 v[204:207],a[168:171],a[200:203],v[204:207]
ds_read_b64_tr_b16 a[166:167],v22 offset:18944
v_mfma_f32_16x16x32_bf16 v[212:215],a[168:171],a[208:211],v[212:215]
v_mfma_f32_16x16x32_bf16 v[212:215],a[172:175],a[212:215],v[212:215]
buffer_load_dwordx4 v48,s[28:31],s57 offen lds
v_mfma_f32_16x16x32_bf16 v[220:223],a[172:175],a[220:223],v[220:223]
ds_read_b64_tr_b16 a[172:173],v22 offset:16904
v_mfma_f32_16x16x32_bf16 v[220:223],a[168:171],a[216:219],v[220:223]
ds_read_b64_tr_b16 a[168:169],v22 offset:520
v_mfma_f32_16x16x32_bf16 v[224:227],a[176:179],a[192:195],v[224:227]
ds_read_b64_tr_b16 a[170:171],v22 offset:2568
v_mfma_f32_16x16x32_bf16 v[224:227],a[180:183],a[196:199],v[224:227]
ds_read_b64_tr_b16 a[174:175],v22 offset:18952
v_mfma_f32_16x16x32_bf16 v[232:235],a[180:183],a[204:207],v[232:235]
buffer_load_dwordx4 v49,s[28:31],s57 offen offset:1024 lds
v_mfma_f32_16x16x32_bf16 v[232:235],a[176:179],a[200:203],v[232:235]
v_mfma_f32_16x16x32_bf16 v[240:243],a[176:179],a[208:211],v[240:243]
v_mfma_f32_16x16x32_bf16 v[240:243],a[180:183],a[212:215],v[240:243]
v_mfma_f32_16x16x32_bf16 v[248:251],a[180:183],a[220:223],v[248:251]
ds_read_b64_tr_b16 a[180:181],v22 offset:17152
v_mfma_f32_16x16x32_bf16 v[248:251],a[176:179],a[216:219],v[248:251]
ds_read_b64_tr_b16 a[176:177],v22 offset:768
v_mfma_f32_16x16x32_bf16 v[228:231],a[184:187],a[192:195],v[228:231]
s_waitcnt lgkmcnt(3)
buffer_load_dwordx4 v50,s[28:31],s57 offen offset:2048 lds
v_mfma_f32_16x16x32_bf16 v[228:231],a[188:191],a[196:199],v[228:231]
ds_read_b64_tr_b16 a[178:179],v22 offset:2816
v_mfma_f32_16x16x32_bf16 v[236:239],a[188:191],a[204:207],v[236:239]
ds_read_b64_tr_b16 a[182:183],v22 offset:19200
v_mfma_f32_16x16x32_bf16 v[236:239],a[184:187],a[200:203],v[236:239]
v_mfma_f32_16x16x32_bf16 v[244:247],a[184:187],a[208:211],v[244:247]
v_mfma_f32_16x16x32_bf16 v[244:247],a[188:191],a[212:215],v[244:247]
buffer_load_dwordx4 v51,s[28:31],s57 offen offset:3072 lds
v_mfma_f32_16x16x32_bf16 v[252:255],a[188:191],a[220:223],v[252:255]
ds_read_b64_tr_b16 a[188:189],v22 offset:17160
v_mfma_f32_16x16x32_bf16 v[252:255],a[184:187],a[216:219],v[252:255]
ds_read_b64_tr_b16 a[184:185],v22 offset:776
v_mfma_f32_16x16x32_bf16 a[0:3],a[128:131],a[192:195],a[0:3]
s_add_u32 m0,s59,4096
s_xor_b32 s59,s59,131072
ds_read_b64_tr_b16 a[186:187],v22 offset:2824
v_mfma_f32_16x16x32_bf16 a[0:3],a[132:135],a[196:199],a[0:3]
ds_read_b64_tr_b16 a[190:191],v22 offset:19208
v_add_u32 v22,s60,v23
v_mfma_f32_16x16x32_bf16 a[32:35],a[132:135],a[204:207],a[32:35]
s_add_u32 s60,s60,32768
s_cmp_eq_u32 s60,131072
s_cselect_b32 s60,32768,s60
v_mfma_f32_16x16x32_bf16 a[32:35],a[128:131],a[200:203],a[32:35]
buffer_load_dwordx4 v52,s[28:31],s57 offen lds
v_mfma_f32_16x16x32_bf16 a[64:67],a[128:131],a[208:211],a[64:67]
v_mfma_f32_16x16x32_bf16 a[64:67],a[132:135],a[212:215],a[64:67]
v_mfma_f32_16x16x32_bf16 a[96:99],a[132:135],a[220:223],a[96:99]
v_mfma_f32_16x16x32_bf16 a[96:99],a[128:131],a[216:219],a[96:99]
v_mfma_f32_16x16x32_bf16 a[4:7],a[136:139],a[192:195],a[4:7]
buffer_load_dwordx4 v53,s[28:31],s57 offen offset:1024 lds
v_mfma_f32_16x16x32_bf16 a[4:7],a[140:143],a[196:199],a[4:7]
v_mfma_f32_16x16x32_bf16 a[36:39],a[140:143],a[204:207],a[36:39]
v_mfma_f32_16x16x32_bf16 a[36:39],a[136:139],a[200:203],a[36:39]
v_mfma_f32_16x16x32_bf16 a[68:71],a[136:139],a[208:211],a[68:71]
v_mfma_f32_16x16x32_bf16 a[68:71],a[140:143],a[212:215],a[68:71]
v_mfma_f32_16x16x32_bf16 a[100:103],a[140:143],a[220:223],a[100:103]
buffer_load_dwordx4 v54,s[28:31],s57 offen offset:2048 lds
v_mfma_f32_16x16x32_bf16 a[100:103],a[136:139],a[216:219],a[100:103]
v_mfma_f32_16x16x32_bf16 a[8:11],a[144:147],a[192:195],a[8:11]
v_mfma_f32_16x16x32_bf16 a[8:11],a[148:151],a[196:199],a[8:11]
v_mfma_f32_16x16x32_bf16 a[40:43],a[148:151],a[204:207],a[40:43]
v_mfma_f32_16x16x32_bf16 a[40:43],a[144:147],a[200:203],a[40:43]
buffer_load_dwordx4 v55,s[28:31],s57 offen offset:3072 lds
v_mfma_f32_16x16x32_bf16 a[72:75],a[144:147],a[208:211],a[72:75]
s_add_u32 s57,s57,128
v_mfma_f32_16x16x32_bf16 a[72:75],a[148:151],a[212:215],a[72:75]
v_mfma_f32_16x16x32_bf16 a[104:107],a[148:151],a[220:223],a[104:107]
v_mfma_f32_16x16x32_bf16 a[104:107],a[144:147],a[216:219],a[104:107]
v_mfma_f32_16x16x32_bf16 a[12:15],a[152:155],a[192:195],a[12:15]
v_mfma_f32_16x16x32_bf16 a[12:15],a[156:159],a[196:199],a[12:15]
v_mfma_f32_16x16x32_bf16 a[44:47],a[156:159],a[204:207],a[44:47]
buffer_load_dword v16,v15,s[72:75],s83 offen
v_mfma_f32_16x16x32_bf16 a[44:47],a[152:155],a[200:203],a[44:47]
s_add_u32 s83,s83,896
v_mfma_f32_16x16x32_bf16 a[76:79],a[152:155],a[208:211],a[76:79]
v_mfma_f32_16x16x32_bf16 a[76:79],a[156:159],a[212:215],a[76:79]
v_mfma_f32_16x16x32_bf16 a[108:111],a[156:159],a[220:223],a[108:111]
v_mfma_f32_16x16x32_bf16 a[108:111],a[152:155],a[216:219],a[108:111]
s_waitcnt vmcnt(17) lgkmcnt(0)
v_mfma_f32_16x16x32_bf16 a[16:19],a[160:163],a[192:195],a[16:19]
s_barrier
ds_read_b128 a[224:227],v20
v_mfma_f32_16x16x32_bf16 a[16:19],a[164:167],a[196:199],a[16:19]
ds_read_b64_tr_b16 a[128:129],v22 offset:1024
v_mfma_f32_16x16x32_bf16 a[48:51],a[164:167],a[204:207],a[48:51]
)A"
R"A(ds_read_b64_tr_b16 a[130:131],v22 offset:3072
v_mfma_f32_16x16x32_bf16 a[48:51],a[160:163],a[200:203],a[48:51]
ds_read_b128 a[228:231],v21
v_mfma_f32_16x16x32_bf16 a[80:83],a[160:163],a[208:211],a[80:83]
ds_read_b64_tr_b16 a[132:133],v22 offset:17408
v_mfma_f32_16x16x32_bf16 a[80:83],a[164:167],a[212:215],a[80:83]
ds_read_b64_tr_b16 a[134:135],v22 offset:19456
v_mfma_f32_16x16x32_bf16 a[112:115],a[164:167],a[220:223],a[112:115]
ds_read_b128 a[232:235],v20 offset:2048
v_mfma_f32_16x16x32_bf16 a[112:115],a[160:163],a[216:219],a[112:115]
ds_read_b128 a[236:239],v21 offset:2048
v_mfma_f32_16x16x32_bf16 a[20:23],a[168:171],a[192:195],a[20:23]
ds_read_b128 a[240:243],v20 offset:4096
v_mfma_f32_16x16x32_bf16 a[20:23],a[172:175],a[196:199],a[20:23]
ds_read_b128 a[244:247],v21 offset:4096
v_mfma_f32_16x16x32_bf16 a[52:55],a[172:175],a[204:207],a[52:55]
ds_read_b128 a[248:251],v20 offset:6144
v_xor_b32 v20,131072,v20
v_mfma_f32_16x16x32_bf16 a[52:55],a[168:171],a[200:203],a[52:55]
ds_read_b128 a[252:255],v21 offset:6144
v_xor_b32 v21,131072,v21
v_mfma_f32_16x16x32_bf16 a[84:87],a[168:171],a[208:211],a[84:87]
ds_read_b64_tr_b16 a[136:137],v22 offset:1032
v_mfma_f32_16x16x32_bf16 a[84:87],a[172:175],a[212:215],a[84:87]
ds_read_b64_tr_b16 a[138:139],v22 offset:3080
v_mfma_f32_16x16x32_bf16 a[116:119],a[172:175],a[220:223],a[116:119]
ds_read_b64_tr_b16 a[140:141],v22 offset:17416
v_mfma_f32_16x16x32_bf16 a[116:119],a[168:171],a[216:219],a[116:119]
ds_read_b64_tr_b16 a[142:143],v22 offset:19464
v_mfma_f32_16x16x32_bf16 a[24:27],a[176:179],a[192:195],a[24:27]
ds_read_b64_tr_b16 a[144:145],v22 offset:1280
v_mfma_f32_16x16x32_bf16 a[24:27],a[180:183],a[196:199],a[24:27]
ds_read_b64_tr_b16 a[146:147],v22 offset:3328
v_mfma_f32_16x16x32_bf16 a[56:59],a[180:183],a[204:207],a[56:59]
ds_read_b64_tr_b16 a[148:149],v22 offset:17664
v_mfma_f32_16x16x32_bf16 a[56:59],a[176:179],a[200:203],a[56:59]
ds_read_b64_tr_b16 a[150:151],v22 offset:19712
v_mfma_f32_16x16x32_bf16 a[88:91],a[176:179],a[208:211],a[88:91]
ds_read_b64_tr_b16 a[152:153],v22 offset:1288
v_mfma_f32_16x16x32_bf16 a[88:91],a[180:183],a[212:215],a[88:91]
ds_read_b64_tr_b16 a[154:155],v22 offset:3336
v_mfma_f32_16x16x32_bf16 a[120:123],a[180:183],a[220:223],a[120:123]
ds_read_b64_tr_b16 a[156:157],v22 offset:17672
v_mfma_f32_16x16x32_bf16 a[120:123],a[176:179],a[216:219],a[120:123]
ds_read_b64_tr_b16 a[158:159],v22 offset:19720
v_mfma_f32_16x16x32_bf16 a[28:31],a[184:187],a[192:195],a[28:31]
ds_read_b64_tr_b16 a[160:161],v22 offset:1536
v_mfma_f32_16x16x32_bf16 a[28:31],a[188:191],a[196:199],a[28:31]
ds_read_b64_tr_b16 a[162:163],v22 offset:3584
v_mfma_f32_16x16x32_bf16 a[60:63],a[188:191],a[204:207],a[60:63]
ds_read_b64_tr_b16 a[164:165],v22 offset:17920
v_mfma_f32_16x16x32_bf16 a[60:63],a[184:187],a[200:203],a[60:63]
ds_read_b64_tr_b16 a[166:167],v22 offset:19968
v_mfma_f32_16x16x32_bf16 a[92:95],a[184:187],a[208:211],a[92:95]
ds_read_b64_tr_b16 a[168:169],v22 offset:1544
v_mfma_f32_16x16x32_bf16 a[92:95],a[188:191],a[212:215],a[92:95]
ds_read_b64_tr_b16 a[170:171],v22 offset:3592
v_mfma_f32_16x16x32_bf16 a[124:127],a[188:191],a[220:223],a[124:127]
s_add_u32 m0,s58,0
ds_read_b64_tr_b16 a[172:173],v22 offset:17928
v_mfma_f32_16x16x32_bf16 a[124:127],a[184:187],a[216:219],a[124:127]
s_waitcnt lgkmcnt(7)
ds_read_b64_tr_b16 a[174:175],v22 offset:19976
v_mfma_f32_16x16x32_bf16 v[128:131],a[128:131],a[224:227],v[128:131]
ds_read_b64_tr_b16 a[176:177],v22 offset:1792
v_mfma_f32_16x16x32_bf16 v[128:131],a[132:135],a[228:231],v[128:131]
buffer_load_dwordx4 v56,s[32:35],s56 offen lds
v_mfma_f32_16x16x32_bf16 v[136:139],a[132:135],a[236:239],v[136:139]
ds_read_b64_tr_b16 a[178:179],v22 offset:3840
v_mfma_f32_16x16x32_bf16 v[136:139],a[128:131],a[232:235],v[136:139]
s_add_u32 m0,s58,1024
ds_read_b64_tr_b16 a[180:181],v22 offset:18176
v_mfma_f32_16x16x32_bf16 v[144:147],a[128:131],a[240:243],v[144:147]
ds_read_b64_tr_b16 a[182:183],v22 offset:20224
v_mfma_f32_16x16x32_bf16 v[144:147],a[132:135],a[244:247],v[144:147]
ds_read_b64_tr_b16 a[184:185],v22 offset:1800
v_mfma_f32_16x16x32_bf16 v[152:155],a[132:135],a[252:255],v[152:155]
buffer_load_dwordx4 v57,s[32:35],s56 offen lds
v_mfma_f32_16x16x32_bf16 v[152:155],a[128:131],a[248:251],v[152:155]
ds_read_b64_tr_b16 a[186:187],v22 offset:3848
v_mfma_f32_16x16x32_bf16 v[132:135],a[136:139],a[224:227],v[132:135]
ds_read_b64_tr_b16 a[188:189],v22 offset:18184
v_mfma_f32_16x16x32_bf16 v[132:135],a[140:143],a[228:231],v[132:135]
s_add_u32 m0,s58,2048
ds_read_b64_tr_b16 a[190:191],v22 offset:20232
v_mfma_f32_16x16x32_bf16 v[140:143],a[140:143],a[236:239],v[140:143]
ds_read_b64_tr_b16 a[128:129],v22
v_mfma_f32_16x16x32_bf16 v[140:143],a[136:139],a[232:235],v[140:143]
ds_read_b64_tr_b16 a[130:131],v22 offset:2048
v_mfma_f32_16x16x32_bf16 v[148:151],a[136:139],a[240:243],v[148:151]
buffer_load_dwordx4 v58,s[32:35],s56 offen lds
v_mfma_f32_16x16x32_bf16 v[148:151],a[140:143],a[244:247],v[148:151]
ds_read_b64_tr_b16 a[132:133],v22 offset:16384
v_mfma_f32_16x16x32_bf16 v[156:159],a[140:143],a[252:255],v[156:159]
s_add_u32 m0,s58,3072
ds_read_b64_tr_b16 a[134:135],v22 offset:18432
v_mfma_f32_16x16x32_bf16 v[156:159],a[136:139],a[248:251],v[156:159]
ds_read_b64_tr_b16 a[136:137],v22 offset:8
v_mfma_f32_16x16x32_bf16 v[160:163],a[144:147],a[224:227],v[160:163]
ds_read_b64_tr_b16 a[138:139],v22 offset:2056
v_mfma_f32_16x16x32_bf16 v[160:163],a[148:151],a[228:231],v[160:163]
buffer_load_dwordx4 v59,s[32:35],s56 offen lds
v_mfma_f32_16x16x32_bf16 v[168:171],a[148:151],a[236:239],v[168:171]
ds_read_b64_tr_b16 a[140:141],v22 offset:16392
v_mfma_f32_16x16x32_bf16 v[168:171],a[144:147],a[232:235],v[168:171]
ds_read_b64_tr_b16 a[142:143],v22 offset:18440
v_mfma_f32_16x16x32_bf16 v[176:179],a[144:147],a[240:243],v[176:179]
s_add_u32 m0,s58,4096
v_mfma_f32_16x16x32_bf16 v[176:179],a[148:151],a[244:247],v[176:179]
v_mfma_f32_16x16x32_bf16 v[184:187],a[148:151],a[252:255],v[184:187]
ds_read_b64_tr_b16 a[148:149],v22 offset:16640
v_mfma_f32_16x16x32_bf16 v[184:187],a[144:147],a[248:251],v[184:187]
buffer_load_dwordx4 v60,s[32:35],s56 offen lds
v_mfma_f32_16x16x32_bf16 v[164:167],a[152:155],a[224:227],v[164:167]
ds_read_b64_tr_b16 a[144:145],v22 offset:256
v_mfma_f32_16x16x32_bf16 v[164:167],a[156:159],a[228:231],v[164:167]
s_add_u32 m0,s58,5120
ds_read_b64_tr_b16 a[146:147],v22 offset:2304
v_mfma_f32_16x16x32_bf16 v[172:175],a[156:159],a[236:239],v[172:175]
ds_read_b64_tr_b16 a[150:151],v22 offset:18688
v_mfma_f32_16x16x32_bf16 v[172:175],a[152:155],a[232:235],v[172:175]
v_mfma_f32_16x16x32_bf16 v[180:183],a[152:155],a[240:243],v[180:183]
buffer_load_dwordx4 v61,s[32:35],s56 offen lds
v_mfma_f32_16x16x32_bf16 v[180:183],a[156:159],a[244:247],v[180:183]
v_mfma_f32_16x16x32_bf16 v[188:191],a[156:159],a[252:255],v[188:191]
ds_read_b64_tr_b16 a[156:157],v22 offset:16648
v_mfma_f32_16x16x32_bf16 v[188:191],a[152:155],a[248:251],v[188:191]
s_add_u32 m0,s58,6144
ds_read_b64_tr_b16 a[152:153],v22 offset:264
s_waitcnt lgkmcnt(5)
v_mfma_f32_16x16x32_bf16 v[192:195],a[160:163],a[224:227],v[192:195]
ds_read_b64_tr_b16 a[154:155],v22 offset:2312
v_mfma_f32_16x16x32_bf16 v[192:195],a[164:167],a[228:231],v[192:195]
ds_read_b64_tr_b16 a[158:159],v22 offset:18696
v_mfma_f32_16x16x32_bf16 v[200:203],a[164:167],a[236:239],v[200:203]
buffer_load_dwordx4 v62,s[32:35],s56 offen lds
v_mfma_f32_16x16x32_bf16 v[200:203],a[160:163],a[232:235],v[200:203]
v_mfma_f32_16x16x32_bf16 v[208:211],a[160:163],a[240:243],v[208:211]
s_add_u32 m0,s58,7168
s_add_u32 s58,s58,32768
v_mfma_f32_16x16x32_bf16 v[208:211],a[164:167],a[244:247],v[208:211]
s_cmp_ge_u32 s58,131072
s_cselect_b32 s62,98304,0
s_sub_u32 s58,s58,s62
v_mfma_f32_16x16x32_bf16 v[216:219],a[164:167],a[252:255],v[216:219]
ds_read_b64_tr_b16 a[164:165],v22 offset:16896
v_mfma_f32_16x16x32_bf16 v[216:219],a[160:163],a[248:251],v[216:219]
buffer_load_dwordx4 v63,s[32:35],s56 offen lds
v_mfma_f32_16x16x32_bf16 v[196:199],a[168:171],a[224:227],v[196:199]
s_add_u32 s56,s56,524288
ds_read_b64_tr_b16 a[160:161],v22 offset:512
v_mfma_f32_16x16x32_bf16 v[196:199],a[172:175],a[228:231],v[196:199]
ds_read_b64_tr_b16 a[162:163],v22 offset:2560
v_mfma_f32_16x16x32_bf16 v[204:207],a[172:175],a[236:239],v[204:207]
s_add_u32 m0,s59,0
ds_read_b64_tr_b16 a[166:167],v22 offset:18944
v_mfma_f32_16x16x32_bf16 v[204:207],a[168:171],a[232:235],v[204:207]
v_mfma_f32_16x16x32_bf16 v[212:215],a[168:171],a[240:243],v[212:215]
v_mfma_f32_16x16x32_bf16 v[212:215],a[172:175],a[244:247],v[212:215]
buffer_load_dwordx4 v48,s[28:31],s57 offen lds
v_mfma_f32_16x16x32_bf16 v[220:223],a[172:175],a[252:255],v[220:223]
ds_read_b64_tr_b16 a[172:173],v22 offset:16904
v_mfma_f32_16x16x32_bf16 v[220:223],a[168:171],a[248:251],v[220:223]
ds_read_b64_tr_b16 a[168:169],v22 offset:520
v_mfma_f32_16x16x32_bf16 v[224:227],a[176:179],a[224:227],v[224:227]
ds_read_b64_tr_b16 a[170:171],v22 offset:2568
v_mfma_f32_16x16x32_bf16 v[224:227],a[180:183],a[228:231],v[224:227]
ds_read_b64_tr_b16 a[174:175],v22 offset:18952
v_mfma_f32_16x16x32_bf16 v[232:235],a[180:183],a[236:239],v[232:235]
buffer_load_dwordx4 v49,s[28:31],s57 offen offset:1024 lds
v_mfma_f32_16x16x32_bf16 v[232:235],a[176:179],a[232:235],v[232:235]
v_mfma_f32_16x16x32_bf16 v[240:243],a[176:179],a[240:243],v[240:243]
v_mfma_f32_16x16x32_bf16 v[240:243],a[180:183],a[244:247],v[240:243]
v_mfma_f32_16x16x32_bf16 v[248:251],a[180:183],a[252:255],v[248:251]
ds_read_b64_tr_b16 a[180:181],v22 offset:17152
v_mfma_f32_16x16x32_bf16 v[248:251],a[176:179],a[248:251],v[248:251]
ds_read_b64_tr_b16 a[176:177],v22 offset:768
v_mfma_f32_16x16x32_bf16 v[228:231],a[184:187],a[224:227],v[228:231]
buffer_load_dwordx4 v50,s[28:31],s57 offen offset:2048 lds
v_mfma_f32_16x16x32_bf16 v[228:231],a[188:191],a[228:231],v[228:231]
ds_read_b64_tr_b16 a[178:179],v22 offset:2816
v_mfma_f32_16x16x32_bf16 v[236:239],a[188:191],a[236:239],v[236:239]
ds_read_b64_tr_b16 a[182:183],v22 offset:19200
v_mfma_f32_16x16x32_bf16 v[236:239],a[184:187],a[232:235],v[236:239]
v_mfma_f32_16x16x32_bf16 v[244:247],a[184:187],a[240:243],v[244:247]
v_mfma_f32_16x16x32_bf16 v[244:247],a[188:191],a[244:247],v[244:247]
buffer_load_dwordx4 v51,s[28:31],s57 offen offset:3072 lds
v_mfma_f32_16x16x32_bf16 v[252:255],a[188:191],a[252:255],v[252:255]
ds_read_b64_tr_b16 a[188:189],v22 offset:17160
v_mfma_f32_16x16x32_bf16 v[252:255],a[184:187],a[248:251],v[252:255]
ds_read_b64_tr_b16 a[184:185],v22 offset:776
v_mfma_f32_16x16x32_bf16 a[0:3],a[128:131],a[224:227],a[0:3]
s_add_u32 m0,s59,4096
s_xor_b32 s59,s59,131072
ds_read_b64_tr_b16 a[186:187],v22 offset:2824
v_mfma_f32_16x16x32_bf16 a[0:3],a[132:135],a[228:231],a[0:3]
ds_read_b64_tr_b16 a[190:191],v22 offset:19208
v_add_u32 v22,s60,v23
v_mfma_f32_16x16x32_bf16 a[32:35],a[132:135],a[236:239],a[32:35]
v_mfma_f32_16x16x32_bf16 a[32:35],a[128:131],a[232:235],a[32:35]
buffer_load_dwordx4 v52,s[28:31],s57 offen lds
v_mfma_f32_16x16x32_bf16 a[64:67],a[128:131],a[240:243],a[64:67]
v_mfma_f32_16x16x32_bf16 a[64:67],a[132:135],a[244:247],a[64:67]
v_mfma_f32_16x16x32_bf16 a[96:99],a[132:135],a[252:255],a[96:99]
v_mfma_f32_16x16x32_bf16 a[96:99],a[128:131],a[248:251],a[96:99]
v_mfma_f32_16x16x32_bf16 a[4:7],a[136:139],a[224:227],a[4:7]
buffer_load_dwordx4 v53,s[28:31],s57 offen offset:1024 lds
v_mfma_f32_16x16x32_bf16 a[4:7],a[140:143],a[228:231],a[4:7]
v_mfma_f32_16x16x32_bf16 a[36:39],a[140:143],a[236:239],a[36:39]
v_mfma_f32_16x16x32_bf16 a[36:39],a[136:139],a[232:235],a[36:39]
v_mfma_f32_16x16x32_bf16 a[68:71],a[136:139],a[240:243],a[68:71]
v_mfma_f32_16x16x32_bf16 a[68:71],a[140:143],a[244:247],a[68:71]
v_mfma_f32_16x16x32_bf16 a[100:103],a[140:143],a[252:255],a[100:103]
buffer_load_dwordx4 v54,s[28:31],s57 offen offset:2048 lds
v_mfma_f32_16x16x32_bf16 a[100:103],a[136:139],a[248:251],a[100:103]
s_waitcnt lgkmcnt(0)
v_mfma_f32_16x16x32_bf16 a[8:11],a[144:147],a[224:227],a[8:11]
v_mfma_f32_16x16x32_bf16 a[8:11],a[148:151],a[228:231],a[8:11]
v_mfma_f32_16x16x32_bf16 a[40:43],a[148:151],a[236:239],a[40:43]
v_mfma_f32_16x16x32_bf16 a[40:43],a[144:147],a[232:235],a[40:43]
)A"
R"A(buffer_load_dwordx4 v55,s[28:31],s57 offen offset:3072 lds
v_mfma_f32_16x16x32_bf16 a[72:75],a[144:147],a[240:243],a[72:75]
s_add_u32 s57,s57,128
v_mfma_f32_16x16x32_bf16 a[72:75],a[148:151],a[244:247],a[72:75]
v_mfma_f32_16x16x32_bf16 a[104:107],a[148:151],a[252:255],a[104:107]
v_mfma_f32_16x16x32_bf16 a[104:107],a[144:147],a[248:251],a[104:107]
v_mfma_f32_16x16x32_bf16 a[12:15],a[152:155],a[224:227],a[12:15]
v_mfma_f32_16x16x32_bf16 a[12:15],a[156:159],a[228:231],a[12:15]
v_mfma_f32_16x16x32_bf16 a[44:47],a[156:159],a[236:239],a[44:47]
buffer_load_dword v16,v15,s[72:75],s83 offen
v_mfma_f32_16x16x32_bf16 a[44:47],a[152:155],a[232:235],a[44:47]
s_add_u32 s83,s83,896
v_mfma_f32_16x16x32_bf16 a[76:79],a[152:155],a[240:243],a[76:79]
v_mfma_f32_16x16x32_bf16 a[76:79],a[156:159],a[244:247],a[76:79]
v_mfma_f32_16x16x32_bf16 a[108:111],a[156:159],a[252:255],a[108:111]
v_mfma_f32_16x16x32_bf16 a[108:111],a[152:155],a[248:251],a[108:111]
s_waitcnt vmcnt(17) lgkmcnt(0)
v_mfma_f32_16x16x32_bf16 a[16:19],a[160:163],a[224:227],a[16:19]
s_barrier
ds_read_b128 a[192:195],v20
v_mfma_f32_16x16x32_bf16 a[16:19],a[164:167],a[228:231],a[16:19]
ds_read_b128 a[196:199],v21
v_mfma_f32_16x16x32_bf16 a[48:51],a[164:167],a[236:239],a[48:51]
ds_read_b128 a[200:203],v20 offset:2048
v_mfma_f32_16x16x32_bf16 a[48:51],a[160:163],a[232:235],a[48:51]
ds_read_b128 a[204:207],v21 offset:2048
v_mfma_f32_16x16x32_bf16 a[80:83],a[160:163],a[240:243],a[80:83]
ds_read_b128 a[208:211],v20 offset:4096
v_mfma_f32_16x16x32_bf16 a[80:83],a[164:167],a[244:247],a[80:83]
ds_read_b128 a[212:215],v21 offset:4096
v_mfma_f32_16x16x32_bf16 a[112:115],a[164:167],a[252:255],a[112:115]
buffer_load_dword v26,v24,s[88:91],s93 offen
v_mfma_f32_16x16x32_bf16 a[112:115],a[160:163],a[248:251],a[112:115]
ds_read_b128 a[216:219],v20 offset:6144
v_mfma_f32_16x16x32_bf16 a[20:23],a[168:171],a[224:227],a[20:23]
ds_read_b128 a[220:223],v21 offset:6144
v_mfma_f32_16x16x32_bf16 a[20:23],a[172:175],a[228:231],a[20:23]
ds_read_b64_tr_b16 a[128:129],v22 offset:1024
v_mfma_f32_16x16x32_bf16 a[52:55],a[172:175],a[236:239],a[52:55]
ds_read_b64_tr_b16 a[130:131],v22 offset:3072
v_mfma_f32_16x16x32_bf16 a[52:55],a[168:171],a[232:235],a[52:55]
ds_read_b64_tr_b16 a[132:133],v22 offset:17408
v_mfma_f32_16x16x32_bf16 a[84:87],a[168:171],a[240:243],a[84:87]
ds_read_b64_tr_b16 a[134:135],v22 offset:19456
v_mfma_f32_16x16x32_bf16 a[84:87],a[172:175],a[244:247],a[84:87]
ds_read_b64_tr_b16 a[136:137],v22 offset:1032
v_mfma_f32_16x16x32_bf16 a[116:119],a[172:175],a[252:255],a[116:119]
ds_read_b64_tr_b16 a[138:139],v22 offset:3080
v_mfma_f32_16x16x32_bf16 a[116:119],a[168:171],a[248:251],a[116:119]
ds_read_b64_tr_b16 a[140:141],v22 offset:17416
v_mfma_f32_16x16x32_bf16 a[24:27],a[176:179],a[224:227],a[24:27]
ds_read_b64_tr_b16 a[142:143],v22 offset:19464
v_mfma_f32_16x16x32_bf16 a[24:27],a[180:183],a[228:231],a[24:27]
ds_read_b64_tr_b16 a[144:145],v22 offset:1280
v_mfma_f32_16x16x32_bf16 a[56:59],a[180:183],a[236:239],a[56:59]
ds_read_b64_tr_b16 a[146:147],v22 offset:3328
v_mfma_f32_16x16x32_bf16 a[56:59],a[176:179],a[232:235],a[56:59]
ds_read_b64_tr_b16 a[148:149],v22 offset:17664
v_mfma_f32_16x16x32_bf16 a[88:91],a[176:179],a[240:243],a[88:91]
ds_read_b64_tr_b16 a[150:151],v22 offset:19712
v_mfma_f32_16x16x32_bf16 a[88:91],a[180:183],a[244:247],a[88:91]
ds_read_b64_tr_b16 a[152:153],v22 offset:1288
v_mfma_f32_16x16x32_bf16 a[120:123],a[180:183],a[252:255],a[120:123]
ds_read_b64_tr_b16 a[154:155],v22 offset:3336
v_mfma_f32_16x16x32_bf16 a[120:123],a[176:179],a[248:251],a[120:123]
ds_read_b64_tr_b16 a[156:157],v22 offset:17672
v_mfma_f32_16x16x32_bf16 a[28:31],a[184:187],a[224:227],a[28:31]
ds_read_b64_tr_b16 a[158:159],v22 offset:19720
v_mfma_f32_16x16x32_bf16 a[28:31],a[188:191],a[228:231],a[28:31]
v_mfma_f32_16x16x32_bf16 a[60:63],a[188:191],a[236:239],a[60:63]
v_mfma_f32_16x16x32_bf16 a[60:63],a[184:187],a[232:235],a[60:63]
v_mfma_f32_16x16x32_bf16 a[92:95],a[184:187],a[240:243],a[92:95]
v_mfma_f32_16x16x32_bf16 a[92:95],a[188:191],a[244:247],a[92:95]
v_mfma_f32_16x16x32_bf16 a[124:127],a[188:191],a[252:255],a[124:127]
s_cmp_lg_u32 s53,0
v_mfma_f32_16x16x32_bf16 a[124:127],a[184:187],a[248:251],a[124:127]
s_cbranch_scc1 L_qp_%=
L_qpx_%=:
L_tailseg_%=:
s_waitcnt lgkmcnt(1)
v_mfma_f32_16x16x32_bf16 v[128:131],a[128:131],a[192:195],v[128:131]
s_add_u32 m0,98304,s51
ds_read_b64_tr_b16 a[160:161],v22 offset:1536
v_xor_b32 v20,131072,v20
v_mfma_f32_16x16x32_bf16 v[128:131],a[132:135],a[196:199],v[128:131]
s_add_u32 s54,s54,16384
buffer_load_dwordx4 v4,s[20:23],s47 offen nt lds
v_mfma_f32_16x16x32_bf16 v[136:139],a[132:135],a[204:207],v[136:139]
s_and_b32 s46,s54,0xfffc0000
ds_read_b64_tr_b16 a[162:163],v22 offset:3584
v_xor_b32 v21,131072,v21
v_mfma_f32_16x16x32_bf16 v[136:139],a[128:131],a[200:203],v[136:139]
s_add_u32 s54,s54,16384
ds_read_b64_tr_b16 a[164:165],v22 offset:17920
v_and_b32 v18,48,v6
v_mfma_f32_16x16x32_bf16 v[144:147],a[128:131],a[208:211],v[144:147]
s_mov_b32 s48,-98304
ds_read_b64_tr_b16 a[166:167],v22 offset:19968
v_lshrrev_b32 v19,4,v6
v_mfma_f32_16x16x32_bf16 v[144:147],a[132:135],a[212:215],v[144:147]
s_lshr_b32 s79,%1,3
ds_read_b64_tr_b16 a[168:169],v22 offset:1544
v_lshlrev_b32 v18,2,v18
v_mfma_f32_16x16x32_bf16 v[152:155],a[132:135],a[220:223],v[152:155]
s_mul_i32 s79,s79,7
ds_read_b64_tr_b16 a[170:171],v22 offset:3592
v_and_b32 v19,48,v19
v_mfma_f32_16x16x32_bf16 v[152:155],a[128:131],a[216:219],v[152:155]
s_mov_b32 s60,148
ds_read_b64_tr_b16 a[172:173],v22 offset:17928
v_add_u32 v18,v18,v19
v_mfma_f32_16x16x32_bf16 v[132:135],a[136:139],a[192:195],v[132:135]
s_lshr_b32 s79,s79,5
ds_read_b64_tr_b16 a[174:175],v22 offset:19976
v_add_u32 v18,98304,v18
v_mfma_f32_16x16x32_bf16 v[132:135],a[140:143],a[196:199],v[132:135]
s_sub_u32 s60,s60,s79
v_readfirstlane_b32 s68,v13
s_nop 1
s_add_u32 s68,s68,1
v_mfma_f32_16x16x32_bf16 v[140:143],a[140:143],a[204:207],v[140:143]
s_lshl_b32 s94,s60,2
s_sub_u32 s94,s94,3
ds_read_b64_tr_b16 a[176:177],v22 offset:1792
v_mfma_f32_16x16x32_bf16 v[140:143],a[136:139],a[200:203],v[140:143]
s_mul_i32 s83,s41,5
s_lshl_b32 s43,s40,8
ds_read_b64_tr_b16 a[178:179],v22 offset:3840
v_mfma_f32_16x16x32_bf16 v[148:151],a[136:139],a[208:211],v[148:151]
s_add_u32 s83,s83,s42
ds_read_b64_tr_b16 a[180:181],v22 offset:18176
v_subrev_u32 v25,s43,v4
v_mfma_f32_16x16x32_bf16 v[148:151],a[140:143],a[212:215],v[148:151]
s_add_u32 s83,s83,15
buffer_load_dwordx4 v4,s[20:23],s47 offen offset:1024 nt lds
v_mfma_f32_16x16x32_bf16 v[156:159],a[140:143],a[220:223],v[156:159]
s_lshl_b32 s44,s40,4
s_lshl_b32 s43,s83,14
ds_read_b64_tr_b16 a[182:183],v22 offset:20224
v_mfma_f32_16x16x32_bf16 v[156:159],a[136:139],a[216:219],v[156:159]
s_and_b32 s44,s44,32
s_lshl_b32 s83,s83,12
ds_read_b64_tr_b16 a[184:185],v22 offset:1800
v_mfma_f32_16x16x32_bf16 v[160:163],a[144:147],a[192:195],v[160:163]
s_lshl_b32 s80,s40,10
ds_read_b64_tr_b16 a[186:187],v22 offset:3848
v_xor_b32 v25,s44,v25
v_mfma_f32_16x16x32_bf16 v[160:163],a[148:151],a[196:199],v[160:163]
s_and_b32 s43,s43,0x20000
s_and_b32 s83,s83,0x7000
ds_read_b64_tr_b16 a[188:189],v22 offset:18184
v_mfma_f32_16x16x32_bf16 v[168:171],a[148:151],a[204:207],v[168:171]
s_or_b32 s83,s83,s43
ds_read_b64_tr_b16 a[190:191],v22 offset:20232
v_add_u32 v25,s80,v25
v_mfma_f32_16x16x32_bf16 v[168:171],a[144:147],a[200:203],v[168:171]
s_xor_b32 s69,s68,0x9e3779b9
s_add_u32 s93,s92,L_att0x_%=-L_pc_%=
ds_read_b64_tr_b16 a[128:129],v22
v_mfma_f32_16x16x32_bf16 v[176:179],a[144:147],a[208:211],v[176:179]
s_mov_b32 s50,32768
s_mov_b32 s49,98304
ds_read_b64_tr_b16 a[130:131],v22 offset:2048
v_mfma_f32_16x16x32_bf16 v[176:179],a[148:151],a[212:215],v[176:179]
s_mov_b32 s45,0x1249
s_add_u32 s80,s80,131072
ds_read_b64_tr_b16 a[132:133],v22 offset:16384
v_mfma_f32_16x16x32_bf16 v[184:187],a[148:151],a[220:223],v[184:187]
s_or_b32 s83,s83,0x40000
ds_read_b64_tr_b16 a[134:135],v22 offset:18432
v_mfma_f32_16x16x32_bf16 v[184:187],a[144:147],a[216:219],v[184:187]
ds_read_b64_tr_b16 a[144:145],v22 offset:8
v_mfma_f32_16x16x32_bf16 v[164:167],a[152:155],a[192:195],v[164:167]
s_waitcnt lgkmcnt(7)
ds_read_b64_tr_b16 a[146:147],v22 offset:2056
v_mfma_f32_16x16x32_bf16 v[164:167],a[156:159],a[196:199],v[164:167]
buffer_load_dwordx4 v5,s[20:23],s47 offen offset:2048 nt lds
v_mfma_f32_16x16x32_bf16 v[172:175],a[156:159],a[204:207],v[172:175]
s_waitcnt vmcnt(5)
ds_read_b128 a[224:227],v20
v_mfma_f32_16x16x32_bf16 v[172:175],a[152:155],a[200:203],v[172:175]
ds_read_b64_tr_b16 a[148:149],v22 offset:16392
v_mfma_f32_16x16x32_bf16 v[180:183],a[152:155],a[208:211],v[180:183]
ds_read_b64_tr_b16 a[150:151],v22 offset:18440
v_mfma_f32_16x16x32_bf16 v[180:183],a[156:159],a[212:215],v[180:183]
v_mfma_f32_16x16x32_bf16 v[188:191],a[156:159],a[220:223],v[188:191]
ds_read_b128 a[228:231],v21
v_mfma_f32_16x16x32_bf16 v[188:191],a[152:155],a[216:219],v[188:191]
v_mfma_f32_16x16x32_bf16 v[192:195],a[160:163],a[192:195],v[192:195]
ds_read_b128 a[232:235],v20 offset:2048
v_mfma_f32_16x16x32_bf16 v[192:195],a[164:167],a[196:199],v[192:195]
v_mfma_f32_16x16x32_bf16 v[200:203],a[164:167],a[204:207],v[200:203]
ds_read_b128 a[236:239],v21 offset:2048
v_mfma_f32_16x16x32_bf16 v[200:203],a[160:163],a[200:203],v[200:203]
v_mfma_f32_16x16x32_bf16 v[208:211],a[160:163],a[208:211],v[208:211]
ds_read_b128 a[240:243],v20 offset:4096
v_mfma_f32_16x16x32_bf16 v[208:211],a[164:167],a[212:215],v[208:211]
buffer_load_dwordx4 v5,s[20:23],s47 offen offset:3072 nt lds
v_mfma_f32_16x16x32_bf16 v[216:219],a[164:167],a[220:223],v[216:219]
ds_read_b128 a[244:247],v21 offset:4096
v_mfma_f32_16x16x32_bf16 v[216:219],a[160:163],a[216:219],v[216:219]
ds_read_b64_tr_b16 a[160:161],v22 offset:256
v_mfma_f32_16x16x32_bf16 v[196:199],a[168:171],a[192:195],v[196:199]
ds_read_b128 a[248:251],v20 offset:6144
ds_read_b128 a[252:255],v21 offset:6144
v_mfma_f32_16x16x32_bf16 v[196:199],a[172:175],a[196:199],v[196:199]
v_mfma_f32_16x16x32_bf16 v[204:207],a[172:175],a[204:207],v[204:207]
v_mfma_f32_16x16x32_bf16 v[204:207],a[168:171],a[200:203],v[204:207]
v_mfma_f32_16x16x32_bf16 v[212:215],a[168:171],a[208:211],v[212:215]
ds_read_b64_tr_b16 a[162:163],v22 offset:2304
v_mfma_f32_16x16x32_bf16 v[212:215],a[172:175],a[212:215],v[212:215]
ds_read_b64_tr_b16 a[164:165],v22 offset:16640
v_mfma_f32_16x16x32_bf16 v[220:223],a[172:175],a[220:223],v[220:223]
s_waitcnt vmcnt(5) lgkmcnt(2)
s_add_u32 m0,98304,s52
ds_read_b64_tr_b16 a[166:167],v22 offset:18688
v_mfma_f32_16x16x32_bf16 v[220:223],a[168:171],a[216:219],v[220:223]
s_barrier
ds_read_b64_tr_b16 a[136:137],v22 offset:32768
v_mfma_f32_16x16x32_bf16 v[224:227],a[176:179],a[192:195],v[224:227]
buffer_load_dwordx4 v4,s[24:27],s47 offen nt lds
v_mfma_f32_16x16x32_bf16 v[224:227],a[180:183],a[196:199],v[224:227]
ds_read_b64_tr_b16 a[138:139],v22 offset:34816
v_mfma_f32_16x16x32_bf16 v[232:235],a[180:183],a[204:207],v[232:235]
ds_read_b64_tr_b16 a[140:141],v22 offset:49152
v_mfma_f32_16x16x32_bf16 v[232:235],a[176:179],a[200:203],v[232:235]
ds_read_b64_tr_b16 a[142:143],v22 offset:51200
v_mfma_f32_16x16x32_bf16 v[240:243],a[176:179],a[208:211],v[240:243]
ds_read_b64_tr_b16 a[152:153],v22 offset:32776
v_mfma_f32_16x16x32_bf16 v[240:243],a[180:183],a[212:215],v[240:243]
ds_read_b64_tr_b16 a[154:155],v22 offset:34824
v_mfma_f32_16x16x32_bf16 v[248:251],a[180:183],a[220:223],v[248:251]
ds_read_b64_tr_b16 a[156:157],v22 offset:49160
v_mfma_f32_16x16x32_bf16 v[248:251],a[176:179],a[216:219],v[248:251]
ds_read_b64_tr_b16 a[158:159],v22 offset:51208
v_mfma_f32_16x16x32_bf16 v[228:231],a[184:187],a[192:195],v[228:231]
ds_read_b64_tr_b16 a[168:169],v22 offset:33024
v_mfma_f32_16x16x32_bf16 v[228:231],a[188:191],a[196:199],v[228:231]
ds_read_b64_tr_b16 a[170:171],v22 offset:35072
v_mfma_f32_16x16x32_bf16 v[236:239],a[188:191],a[204:207],v[236:239]
ds_read_b64_tr_b16 a[172:173],v22 offset:49408
v_mfma_f32_16x16x32_bf16 v[236:239],a[184:187],a[200:203],v[236:239]
ds_read_b64_tr_b16 a[174:175],v22 offset:51456
v_mfma_f32_16x16x32_bf16 v[244:247],a[184:187],a[208:211],v[244:247]
)A"
R"A(buffer_load_dwordx4 v5,s[24:27],s47 offen offset:1024 nt lds
v_mfma_f32_16x16x32_bf16 v[244:247],a[188:191],a[212:215],v[244:247]
ds_read_b64_tr_b16 a[176:177],v22 offset:264
v_mfma_f32_16x16x32_bf16 v[252:255],a[188:191],a[220:223],v[252:255]
ds_read_b64_tr_b16 a[178:179],v22 offset:2312
v_mfma_f32_16x16x32_bf16 v[252:255],a[184:187],a[216:219],v[252:255]
ds_read_b64_tr_b16 a[180:181],v22 offset:16648
v_mfma_f32_16x16x32_bf16 a[0:3],a[128:131],a[192:195],a[0:3]
ds_read_b64_tr_b16 a[182:183],v22 offset:18696
v_mfma_f32_16x16x32_bf16 a[0:3],a[132:135],a[196:199],a[0:3]
s_waitcnt lgkmcnt(6)
ds_read_b64_tr_b16 a[184:185],v22 offset:33032
v_mfma_f32_16x16x32_bf16 a[0:3],a[136:139],a[224:227],a[0:3]
ds_read_b64_tr_b16 a[186:187],v22 offset:35080
v_mfma_f32_16x16x32_bf16 a[0:3],a[140:143],a[228:231],a[0:3]
ds_read_b64_tr_b16 a[188:189],v22 offset:49416
v_mfma_f32_16x16x32_bf16 a[32:35],a[140:143],a[236:239],a[32:35]
ds_read_b64_tr_b16 a[190:191],v22 offset:51464
v_mfma_f32_16x16x32_bf16 a[32:35],a[136:139],a[232:235],a[32:35]
v_mfma_f32_16x16x32_bf16 a[32:35],a[132:135],a[204:207],a[32:35]
v_mfma_f32_16x16x32_bf16 a[32:35],a[128:131],a[200:203],a[32:35]
v_mfma_f32_16x16x32_bf16 a[64:67],a[128:131],a[208:211],a[64:67]
buffer_load_dwordx4 v4,s[24:27],s47 offen offset:2048 nt lds
v_mfma_f32_16x16x32_bf16 a[64:67],a[132:135],a[212:215],a[64:67]
v_accvgpr_read_b32 v64,a0
v_accvgpr_read_b32 v65,a1
v_mfma_f32_16x16x32_bf16 a[64:67],a[136:139],a[240:243],a[64:67]
v_mul_f32 v64,s61,v64
v_mul_f32 v65,s61,v65
v_mfma_f32_16x16x32_bf16 a[64:67],a[140:143],a[244:247],a[64:67]
v_accvgpr_read_b32 v66,a2
v_accvgpr_read_b32 v67,a3
v_mfma_f32_16x16x32_bf16 a[96:99],a[140:143],a[252:255],a[96:99]
ds_read_b64_tr_b16 a[140:141],v22 offset:49664
v_cvt_pk_bf16_f32 v64,v64,v65
v_mfma_f32_16x16x32_bf16 a[96:99],a[136:139],a[248:251],a[96:99]
ds_read_b64_tr_b16 a[136:137],v22 offset:33280
v_accvgpr_read_b32 v80,a32
v_mfma_f32_16x16x32_bf16 a[96:99],a[132:135],a[220:223],a[96:99]
ds_read_b64_tr_b16 a[132:133],v22 offset:16896
v_accvgpr_read_b32 v81,a33
v_mfma_f32_16x16x32_bf16 a[96:99],a[128:131],a[216:219],a[96:99]
ds_read_b64_tr_b16 a[128:129],v22 offset:512
v_mul_f32 v80,s61,v80
v_mfma_f32_16x16x32_bf16 a[4:7],a[144:147],a[192:195],a[4:7]
ds_read_b64_tr_b16 a[130:131],v22 offset:2560
v_mul_f32 v81,s61,v81
v_mfma_f32_16x16x32_bf16 a[4:7],a[148:151],a[196:199],a[4:7]
ds_read_b64_tr_b16 a[134:135],v22 offset:18944
v_accvgpr_read_b32 v82,a34
v_mfma_f32_16x16x32_bf16 a[4:7],a[152:155],a[224:227],a[4:7]
ds_read_b64_tr_b16 a[138:139],v22 offset:35328
v_accvgpr_read_b32 v83,a35
v_mfma_f32_16x16x32_bf16 a[4:7],a[156:159],a[228:231],a[4:7]
ds_read_b64_tr_b16 a[142:143],v22 offset:51712
v_mul_f32 v66,s61,v66
v_mfma_f32_16x16x32_bf16 a[36:39],a[156:159],a[236:239],a[36:39]
buffer_load_dwordx4 v5,s[24:27],s47 offen offset:3072 nt lds
v_mfma_f32_16x16x32_bf16 a[36:39],a[152:155],a[232:235],a[36:39]
s_or_b32 s47,s47,0x18000
s_add_u32 s47,s47,4096
s_and_b32 s47,s47,0x27000
v_mfma_f32_16x16x32_bf16 a[36:39],a[148:151],a[204:207],a[36:39]
s_or_b32 s47,s47,s46
s_and_b32 s46,s54,0xfffc0000
v_mul_f32 v67,s61,v67
v_mfma_f32_16x16x32_bf16 a[36:39],a[144:147],a[200:203],a[36:39]
s_add_u32 s54,s54,16384
v_cvt_pk_bf16_f32 v80,v80,v81
v_mul_f32 v82,s61,v82
v_mfma_f32_16x16x32_bf16 a[68:71],a[144:147],a[208:211],a[68:71]
v_mul_f32 v83,s61,v83
v_cvt_pk_bf16_f32 v65,v66,v67
v_mfma_f32_16x16x32_bf16 a[68:71],a[148:151],a[212:215],a[68:71]
v_cvt_pk_bf16_f32 v81,v82,v83
v_accvgpr_read_b32 v112,a96
v_mfma_f32_16x16x32_bf16 a[68:71],a[152:155],a[240:243],a[68:71]
v_accvgpr_read_b32 v113,a97
v_accvgpr_read_b32 v66,a4
v_mfma_f32_16x16x32_bf16 a[68:71],a[156:159],a[244:247],a[68:71]
v_accvgpr_read_b32 v67,a5
v_mul_f32 v112,s61,v112
v_mfma_f32_16x16x32_bf16 a[100:103],a[156:159],a[252:255],a[100:103]
ds_read_b64_tr_b16 a[156:157],v22 offset:49672
v_mul_f32 v113,s61,v113
v_mfma_f32_16x16x32_bf16 a[100:103],a[152:155],a[248:251],a[100:103]
ds_read_b64_tr_b16 a[152:153],v22 offset:33288
v_accvgpr_read_b32 v82,a36
v_mfma_f32_16x16x32_bf16 a[100:103],a[148:151],a[220:223],a[100:103]
ds_read_b64_tr_b16 a[148:149],v22 offset:16904
v_accvgpr_read_b32 v83,a37
v_mfma_f32_16x16x32_bf16 a[100:103],a[144:147],a[216:219],a[100:103]
ds_read_b64_tr_b16 a[144:145],v22 offset:520
v_accvgpr_read_b32 v114,a98
v_mfma_f32_16x16x32_bf16 a[8:11],a[160:163],a[192:195],a[8:11]
s_add_u32 m0,0,s51
ds_read_b64_tr_b16 a[146:147],v22 offset:2568
v_accvgpr_read_b32 v115,a99
v_mfma_f32_16x16x32_bf16 a[8:11],a[164:167],a[196:199],a[8:11]
ds_read_b64_tr_b16 a[150:151],v22 offset:18952
v_accvgpr_read_b32 v12,a6
v_mfma_f32_16x16x32_bf16 a[8:11],a[168:171],a[224:227],a[8:11]
s_waitcnt lgkmcnt(6)
ds_read_b64_tr_b16 a[154:155],v22 offset:35336
v_accvgpr_read_b32 v13,a7
v_mfma_f32_16x16x32_bf16 a[8:11],a[172:175],a[228:231],a[8:11]
buffer_load_dwordx4 v4,s[20:23],s47 offen nt lds
v_mfma_f32_16x16x32_bf16 a[40:43],a[172:175],a[236:239],a[40:43]
ds_read_b64_tr_b16 a[158:159],v22 offset:51720
v_mul_f32 v66,s61,v66
v_mfma_f32_16x16x32_bf16 a[40:43],a[168:171],a[232:235],a[40:43]
v_mul_f32 v67,s61,v67
v_accvgpr_read_b32 v14,a38
v_mfma_f32_16x16x32_bf16 a[40:43],a[164:167],a[204:207],a[40:43]
v_accvgpr_read_b32 v15,a39
v_mul_f32 v82,s61,v82
v_mfma_f32_16x16x32_bf16 a[40:43],a[160:163],a[200:203],a[40:43]
v_mul_f32 v83,s61,v83
v_cvt_pk_bf16_f32 v112,v112,v113
v_mfma_f32_16x16x32_bf16 a[72:75],a[160:163],a[208:211],a[72:75]
v_mul_f32 v114,s61,v114
v_mul_f32 v115,s61,v115
v_mfma_f32_16x16x32_bf16 a[72:75],a[164:167],a[212:215],a[72:75]
v_accvgpr_read_b32 v68,a8
v_accvgpr_read_b32 v69,a9
v_mfma_f32_16x16x32_bf16 a[72:75],a[168:171],a[240:243],a[72:75]
v_mul_f32 v68,s61,v68
v_mul_f32 v69,s61,v69
v_mfma_f32_16x16x32_bf16 a[72:75],a[172:175],a[244:247],a[72:75]
v_accvgpr_read_b32 v70,a10
v_accvgpr_read_b32 v71,a11
v_mfma_f32_16x16x32_bf16 a[104:107],a[172:175],a[252:255],a[104:107]
ds_read_b64_tr_b16 a[172:173],v22 offset:49920
v_cvt_pk_bf16_f32 v68,v68,v69
v_mfma_f32_16x16x32_bf16 a[104:107],a[168:171],a[248:251],a[104:107]
ds_read_b64_tr_b16 a[168:169],v22 offset:33536
v_mul_f32 v70,s61,v70
v_mfma_f32_16x16x32_bf16 a[104:107],a[164:167],a[220:223],a[104:107]
ds_read_b64_tr_b16 a[164:165],v22 offset:17152
v_mul_f32 v71,s61,v71
v_mfma_f32_16x16x32_bf16 a[104:107],a[160:163],a[216:219],a[104:107]
ds_read_b64_tr_b16 a[160:161],v22 offset:768
v_mul_f32 v12,s61,v12
v_mfma_f32_16x16x32_bf16 a[12:15],a[176:179],a[192:195],a[12:15]
ds_read_b64_tr_b16 a[162:163],v22 offset:2816
v_mul_f32 v13,s61,v13
v_mfma_f32_16x16x32_bf16 a[12:15],a[180:183],a[196:199],a[12:15]
buffer_load_dwordx4 v4,s[20:23],s47 offen offset:1024 nt lds
v_mfma_f32_16x16x32_bf16 a[12:15],a[184:187],a[224:227],a[12:15]
ds_read_b64_tr_b16 a[166:167],v22 offset:19200
v_cvt_pk_bf16_f32 v66,v66,v67
v_mfma_f32_16x16x32_bf16 a[12:15],a[188:191],a[228:231],a[12:15]
ds_read_b64_tr_b16 a[170:171],v22 offset:35584
v_mul_f32 v14,s61,v14
v_mfma_f32_16x16x32_bf16 a[44:47],a[188:191],a[236:239],a[44:47]
ds_read_b64_tr_b16 a[174:175],v22 offset:51968
v_mul_f32 v15,s61,v15
v_mfma_f32_16x16x32_bf16 a[44:47],a[184:187],a[232:235],a[44:47]
v_cvt_pk_bf16_f32 v82,v82,v83
v_cvt_pk_bf16_f32 v113,v114,v115
v_mfma_f32_16x16x32_bf16 a[44:47],a[180:183],a[204:207],a[44:47]
v_cvt_pk_bf16_f32 v69,v70,v71
v_accvgpr_read_b32 v100,a72
v_mfma_f32_16x16x32_bf16 a[44:47],a[176:179],a[200:203],a[44:47]
v_accvgpr_read_b32 v101,a73
v_accvgpr_read_b32 v116,a104
v_mfma_f32_16x16x32_bf16 a[76:79],a[176:179],a[208:211],a[76:79]
v_accvgpr_read_b32 v117,a105
v_cvt_pk_bf16_f32 v67,v12,v13
v_mfma_f32_16x16x32_bf16 a[76:79],a[180:183],a[212:215],a[76:79]
v_cvt_pk_bf16_f32 v83,v14,v15
v_accvgpr_read_b32 v114,a100
v_mfma_f32_16x16x32_bf16 a[76:79],a[184:187],a[240:243],a[76:79]
v_accvgpr_read_b32 v115,a101
v_accvgpr_read_b32 v70,a12
v_mfma_f32_16x16x32_bf16 a[76:79],a[188:191],a[244:247],a[76:79]
v_accvgpr_read_b32 v71,a13
v_mul_f32 v100,s61,v100
v_mfma_f32_16x16x32_bf16 a[108:111],a[188:191],a[252:255],a[108:111]
ds_read_b64_tr_b16 a[188:189],v22 offset:49928
v_mul_f32 v101,s61,v101
v_mfma_f32_16x16x32_bf16 a[108:111],a[184:187],a[248:251],a[108:111]
ds_read_b64_tr_b16 a[184:185],v22 offset:33544
v_accvgpr_read_b32 v102,a74
v_mfma_f32_16x16x32_bf16 a[108:111],a[180:183],a[220:223],a[108:111]
ds_read_b64_tr_b16 a[180:181],v22 offset:17160
v_accvgpr_read_b32 v103,a75
v_mfma_f32_16x16x32_bf16 a[108:111],a[176:179],a[216:219],a[108:111]
buffer_load_dwordx4 v5,s[20:23],s47 offen offset:2048 nt lds
v_mfma_f32_16x16x32_bf16 a[16:19],a[128:131],a[192:195],a[16:19]
ds_read_b64_tr_b16 a[176:177],v22 offset:776
v_mul_f32 v116,s61,v116
v_mfma_f32_16x16x32_bf16 a[16:19],a[132:135],a[196:199],a[16:19]
ds_read_b64_tr_b16 a[178:179],v22 offset:2824
v_mul_f32 v117,s61,v117
v_mfma_f32_16x16x32_bf16 a[16:19],a[136:139],a[224:227],a[16:19]
ds_read_b64_tr_b16 a[182:183],v22 offset:19208
v_accvgpr_read_b32 v118,a106
v_mfma_f32_16x16x32_bf16 a[16:19],a[140:143],a[228:231],a[16:19]
ds_read_b64_tr_b16 a[186:187],v22 offset:35592
v_accvgpr_read_b32 v119,a107
v_mfma_f32_16x16x32_bf16 a[48:51],a[140:143],a[236:239],a[48:51]
ds_read_b64_tr_b16 a[190:191],v22 offset:51976
v_accvgpr_read_b32 v12,a102
v_mfma_f32_16x16x32_bf16 a[48:51],a[136:139],a[232:235],a[48:51]
v_accvgpr_read_b32 v13,a103
v_mul_f32 v114,s61,v114
v_mfma_f32_16x16x32_bf16 a[48:51],a[132:135],a[204:207],a[48:51]
v_mul_f32 v115,s61,v115
v_accvgpr_read_b32 v14,a14
v_mfma_f32_16x16x32_bf16 a[48:51],a[128:131],a[200:203],a[48:51]
v_accvgpr_read_b32 v15,a15
v_mul_f32 v70,s61,v70
v_mfma_f32_16x16x32_bf16 a[80:83],a[128:131],a[208:211],a[80:83]
v_mul_f32 v71,s61,v71
v_cvt_pk_bf16_f32 v100,v100,v101
v_mfma_f32_16x16x32_bf16 a[80:83],a[132:135],a[212:215],a[80:83]
v_mul_f32 v102,s61,v102
v_mul_f32 v103,s61,v103
v_mfma_f32_16x16x32_bf16 a[80:83],a[136:139],a[240:243],a[80:83]
v_cvt_pk_bf16_f32 v116,v116,v117
v_mul_f32 v118,s61,v118
v_mfma_f32_16x16x32_bf16 a[80:83],a[140:143],a[244:247],a[80:83]
v_mul_f32 v119,s61,v119
v_mul_f32 v12,s61,v12
v_mfma_f32_16x16x32_bf16 a[112:115],a[140:143],a[252:255],a[112:115]
ds_read_b64_tr_b16 a[140:141],v22 offset:50184
v_mul_f32 v13,s61,v13
v_mfma_f32_16x16x32_bf16 a[112:115],a[136:139],a[248:251],a[112:115]
buffer_load_dwordx4 v5,s[20:23],s47 offen offset:3072 nt lds
v_mfma_f32_16x16x32_bf16 a[112:115],a[132:135],a[220:223],a[112:115]
ds_read_b64_tr_b16 a[132:133],v22 offset:50176
v_cvt_pk_bf16_f32 v114,v114,v115
v_mfma_f32_16x16x32_bf16 a[112:115],a[128:131],a[216:219],a[112:115]
s_waitcnt lgkmcnt(2)
ds_read_b64_tr_b16 a[128:129],v22 offset:33792
v_mul_f32 v14,s61,v14
v_mfma_f32_16x16x32_bf16 a[20:23],a[144:147],a[192:195],a[20:23]
ds_read_b64_tr_b16 a[130:131],v22 offset:35840
v_mul_f32 v15,s61,v15
v_mfma_f32_16x16x32_bf16 a[20:23],a[148:151],a[196:199],a[20:23]
ds_read_b64_tr_b16 a[134:135],v22 offset:52224
v_cvt_pk_bf16_f32 v70,v70,v71
v_mfma_f32_16x16x32_bf16 a[20:23],a[152:155],a[224:227],a[20:23]
ds_read_b64_tr_b16 a[136:137],v22 offset:33800
v_cvt_pk_bf16_f32 v101,v102,v103
v_mfma_f32_16x16x32_bf16 a[20:23],a[156:159],a[228:231],a[20:23]
ds_read_b64_tr_b16 a[138:139],v22 offset:35848
v_cvt_pk_bf16_f32 v117,v118,v119
v_mfma_f32_16x16x32_bf16 a[52:55],a[156:159],a[236:239],a[52:55]
ds_read_b64_tr_b16 a[142:143],v22 offset:52232
v_cvt_pk_bf16_f32 v115,v12,v13
v_mfma_f32_16x16x32_bf16 a[52:55],a[152:155],a[232:235],a[52:55]
v_cvt_pk_bf16_f32 v71,v14,v15
v_accvgpr_read_b32 v102,a76
v_mfma_f32_16x16x32_bf16 a[52:55],a[148:151],a[204:207],a[52:55]
v_accvgpr_read_b32 v103,a77
v_accvgpr_read_b32 v118,a108
v_mfma_f32_16x16x32_bf16 a[52:55],a[144:147],a[200:203],a[52:55]
v_accvgpr_read_b32 v119,a109
v_accvgpr_read_b32 v12,a78
v_mfma_f32_16x16x32_bf16 a[84:87],a[144:147],a[208:211],a[84:87]
s_add_u32 m0,0,s52
v_accvgpr_read_b32 v13,a79
v_mul_f32 v102,s61,v102
v_mfma_f32_16x16x32_bf16 a[84:87],a[148:151],a[212:215],a[84:87]
v_mul_f32 v103,s61,v103
v_accvgpr_read_b32 v14,a110
v_mfma_f32_16x16x32_bf16 a[84:87],a[152:155],a[240:243],a[84:87]
v_accvgpr_read_b32 v15,a111
v_mul_f32 v118,s61,v118
v_mfma_f32_16x16x32_bf16 a[84:87],a[156:159],a[244:247],a[84:87]
)A"
R"A(buffer_load_dwordx4 v4,s[24:27],s47 offen nt lds
v_mfma_f32_16x16x32_bf16 a[116:119],a[156:159],a[252:255],a[116:119]
ds_read_b64_tr_b16 a[156:157],v22 offset:50440
v_mul_f32 v119,s61,v119
v_mfma_f32_16x16x32_bf16 a[116:119],a[152:155],a[248:251],a[116:119]
ds_read_b64_tr_b16 a[152:153],v22 offset:34056
v_mul_f32 v12,s61,v12
v_mfma_f32_16x16x32_bf16 a[116:119],a[148:151],a[220:223],a[116:119]
ds_read_b64_tr_b16 a[148:149],v22 offset:50432
v_mul_f32 v13,s61,v13
v_mfma_f32_16x16x32_bf16 a[116:119],a[144:147],a[216:219],a[116:119]
ds_read_b64_tr_b16 a[144:145],v22 offset:34048
v_cvt_pk_bf16_f32 v102,v102,v103
v_mfma_f32_16x16x32_bf16 a[24:27],a[160:163],a[192:195],a[24:27]
ds_read_b64_tr_b16 a[146:147],v22 offset:36096
v_mul_f32 v14,s61,v14
v_mfma_f32_16x16x32_bf16 a[24:27],a[164:167],a[196:199],a[24:27]
ds_read_b64_tr_b16 a[150:151],v22 offset:52480
v_mul_f32 v15,s61,v15
v_mfma_f32_16x16x32_bf16 a[24:27],a[168:171],a[224:227],a[24:27]
ds_read_b64_tr_b16 a[154:155],v22 offset:36104
v_cvt_pk_bf16_f32 v118,v118,v119
v_mfma_f32_16x16x32_bf16 a[24:27],a[172:175],a[228:231],a[24:27]
ds_read_b64_tr_b16 a[158:159],v22 offset:52488
v_cvt_pk_bf16_f32 v103,v12,v13
v_mfma_f32_16x16x32_bf16 a[56:59],a[172:175],a[236:239],a[56:59]
v_cvt_pk_bf16_f32 v119,v14,v15
v_accvgpr_read_b32 v96,a64
v_mfma_f32_16x16x32_bf16 a[56:59],a[168:171],a[232:235],a[56:59]
v_accvgpr_read_b32 v72,a16
v_accvgpr_read_b32 v73,a17
v_accvgpr_read_b32 v74,a18
v_accvgpr_read_b32 v75,a19
v_accvgpr_read_b32 v12,a20
v_accvgpr_read_b32 v13,a21
v_accvgpr_read_b32 v14,a22
v_accvgpr_read_b32 v15,a23
v_pk_mul_f32 v[72:73],s[60:61],v[72:73] op_sel:[1,0] op_sel_hi:[1,1]
v_pk_mul_f32 v[74:75],s[60:61],v[74:75] op_sel:[1,0] op_sel_hi:[1,1]
v_pk_mul_f32 v[12:13],s[60:61],v[12:13] op_sel:[1,0] op_sel_hi:[1,1]
v_pk_mul_f32 v[14:15],s[60:61],v[14:15] op_sel:[1,0] op_sel_hi:[1,1]
v_cvt_pk_bf16_f32 v72,v72,v73
v_cvt_pk_bf16_f32 v73,v74,v75
v_cvt_pk_bf16_f32 v74,v12,v13
v_cvt_pk_bf16_f32 v75,v14,v15
v_mfma_f32_16x16x32_bf16 a[56:59],a[164:167],a[204:207],a[56:59]
v_accvgpr_read_b32 v97,a65
v_mul_f32 v96,s61,v96
v_mfma_f32_16x16x32_bf16 a[56:59],a[160:163],a[200:203],a[56:59]
v_accvgpr_read_b32 v88,a48
v_accvgpr_read_b32 v89,a49
v_accvgpr_read_b32 v90,a50
v_accvgpr_read_b32 v91,a51
v_accvgpr_read_b32 v12,a52
v_accvgpr_read_b32 v13,a53
v_accvgpr_read_b32 v14,a54
v_accvgpr_read_b32 v15,a55
v_pk_mul_f32 v[88:89],s[60:61],v[88:89] op_sel:[1,0] op_sel_hi:[1,1]
v_pk_mul_f32 v[90:91],s[60:61],v[90:91] op_sel:[1,0] op_sel_hi:[1,1]
v_pk_mul_f32 v[12:13],s[60:61],v[12:13] op_sel:[1,0] op_sel_hi:[1,1]
v_pk_mul_f32 v[14:15],s[60:61],v[14:15] op_sel:[1,0] op_sel_hi:[1,1]
v_cvt_pk_bf16_f32 v88,v88,v89
v_cvt_pk_bf16_f32 v89,v90,v91
v_cvt_pk_bf16_f32 v90,v12,v13
v_cvt_pk_bf16_f32 v91,v14,v15
v_mfma_f32_16x16x32_bf16 a[88:91],a[160:163],a[208:211],a[88:91]
v_mul_f32 v97,s61,v97
v_accvgpr_read_b32 v98,a66
v_mfma_f32_16x16x32_bf16 a[88:91],a[164:167],a[212:215],a[88:91]
buffer_load_dwordx4 v5,s[24:27],s47 offen offset:1024 nt lds
v_mfma_f32_16x16x32_bf16 a[88:91],a[168:171],a[240:243],a[88:91]
v_accvgpr_read_b32 v99,a67
v_cvt_pk_bf16_f32 v96,v96,v97
v_mfma_f32_16x16x32_bf16 a[88:91],a[172:175],a[244:247],a[88:91]
v_mul_f32 v98,s61,v98
v_mul_f32 v99,s61,v99
v_mfma_f32_16x16x32_bf16 a[120:123],a[172:175],a[252:255],a[120:123]
ds_read_b64_tr_b16 a[172:173],v22 offset:50696
v_cvt_pk_bf16_f32 v97,v98,v99
v_mfma_f32_16x16x32_bf16 a[120:123],a[168:171],a[248:251],a[120:123]
ds_read_b64_tr_b16 a[168:169],v22 offset:34312
v_accvgpr_read_b32 v84,a40
v_mfma_f32_16x16x32_bf16 a[120:123],a[164:167],a[220:223],a[120:123]
ds_read_b64_tr_b16 a[164:165],v22 offset:50688
v_accvgpr_read_b32 v85,a41
v_mfma_f32_16x16x32_bf16 a[120:123],a[160:163],a[216:219],a[120:123]
ds_read_b64_tr_b16 a[160:161],v22 offset:34304
v_accvgpr_read_b32 v98,a68
v_mfma_f32_16x16x32_bf16 a[28:31],a[176:179],a[192:195],a[28:31]
ds_read_b64_tr_b16 a[162:163],v22 offset:36352
v_accvgpr_read_b32 v99,a69
v_mfma_f32_16x16x32_bf16 a[28:31],a[180:183],a[196:199],a[28:31]
ds_read_b64_tr_b16 a[166:167],v22 offset:52736
v_mul_f32 v84,s61,v84
v_mfma_f32_16x16x32_bf16 a[28:31],a[184:187],a[224:227],a[28:31]
ds_read_b64_tr_b16 a[170:171],v22 offset:36360
v_mul_f32 v85,s61,v85
v_mfma_f32_16x16x32_bf16 a[28:31],a[188:191],a[228:231],a[28:31]
ds_read_b64_tr_b16 a[174:175],v22 offset:52744
v_accvgpr_read_b32 v86,a42
v_mfma_f32_16x16x32_bf16 a[60:63],a[188:191],a[236:239],a[60:63]
v_accvgpr_read_b32 v104,a80
v_accvgpr_read_b32 v105,a81
v_accvgpr_read_b32 v106,a82
v_accvgpr_read_b32 v107,a83
v_accvgpr_read_b32 v12,a84
v_accvgpr_read_b32 v13,a85
v_accvgpr_read_b32 v14,a86
v_accvgpr_read_b32 v15,a87
v_pk_mul_f32 v[104:105],s[60:61],v[104:105] op_sel:[1,0] op_sel_hi:[1,1]
v_pk_mul_f32 v[106:107],s[60:61],v[106:107] op_sel:[1,0] op_sel_hi:[1,1]
v_pk_mul_f32 v[12:13],s[60:61],v[12:13] op_sel:[1,0] op_sel_hi:[1,1]
v_pk_mul_f32 v[14:15],s[60:61],v[14:15] op_sel:[1,0] op_sel_hi:[1,1]
v_cvt_pk_bf16_f32 v104,v104,v105
v_cvt_pk_bf16_f32 v105,v106,v107
v_cvt_pk_bf16_f32 v106,v12,v13
v_cvt_pk_bf16_f32 v107,v14,v15
v_mfma_f32_16x16x32_bf16 a[60:63],a[184:187],a[232:235],a[60:63]
v_accvgpr_read_b32 v87,a43
v_accvgpr_read_b32 v16,a70
v_mfma_f32_16x16x32_bf16 a[60:63],a[180:183],a[204:207],a[60:63]
v_accvgpr_read_b32 v120,a112
v_accvgpr_read_b32 v121,a113
v_accvgpr_read_b32 v122,a114
v_accvgpr_read_b32 v123,a115
v_accvgpr_read_b32 v12,a116
v_accvgpr_read_b32 v13,a117
v_accvgpr_read_b32 v14,a118
v_accvgpr_read_b32 v15,a119
v_pk_mul_f32 v[120:121],s[60:61],v[120:121] op_sel:[1,0] op_sel_hi:[1,1]
v_pk_mul_f32 v[122:123],s[60:61],v[122:123] op_sel:[1,0] op_sel_hi:[1,1]
v_pk_mul_f32 v[12:13],s[60:61],v[12:13] op_sel:[1,0] op_sel_hi:[1,1]
v_pk_mul_f32 v[14:15],s[60:61],v[14:15] op_sel:[1,0] op_sel_hi:[1,1]
v_cvt_pk_bf16_f32 v120,v120,v121
v_cvt_pk_bf16_f32 v121,v122,v123
v_cvt_pk_bf16_f32 v122,v12,v13
v_cvt_pk_bf16_f32 v123,v14,v15
v_mfma_f32_16x16x32_bf16 a[60:63],a[176:179],a[200:203],a[60:63]
buffer_load_dwordx4 v4,s[24:27],s47 offen offset:2048 nt lds
v_mfma_f32_16x16x32_bf16 a[92:95],a[176:179],a[208:211],a[92:95]
v_accvgpr_read_b32 v17,a71
v_mul_f32 v98,s61,v98
v_mfma_f32_16x16x32_bf16 a[92:95],a[180:183],a[212:215],a[92:95]
v_mul_f32 v99,s61,v99
v_cvt_pk_bf16_f32 v84,v84,v85
v_mfma_f32_16x16x32_bf16 a[92:95],a[184:187],a[240:243],a[92:95]
v_mul_f32 v86,s61,v86
v_mul_f32 v87,s61,v87
v_mfma_f32_16x16x32_bf16 a[92:95],a[188:191],a[244:247],a[92:95]
v_mul_f32 v16,s61,v16
v_mul_f32 v17,s61,v17
v_mfma_f32_16x16x32_bf16 a[124:127],a[188:191],a[252:255],a[124:127]
ds_read_b64_tr_b16 a[188:189],v22 offset:50952
v_cvt_pk_bf16_f32 v98,v98,v99
v_mfma_f32_16x16x32_bf16 a[124:127],a[184:187],a[248:251],a[124:127]
ds_read_b64_tr_b16 a[184:185],v22 offset:34568
v_cvt_pk_bf16_f32 v85,v86,v87
v_mfma_f32_16x16x32_bf16 a[124:127],a[180:183],a[220:223],a[124:127]
ds_read_b64_tr_b16 a[180:181],v22 offset:50944
v_cvt_pk_bf16_f32 v99,v16,v17
v_mfma_f32_16x16x32_bf16 a[124:127],a[176:179],a[216:219],a[124:127]
s_waitcnt lgkmcnt(3)
ds_read_b64_tr_b16 a[176:177],v22 offset:34560
v_accvgpr_read_b32 v86,a44
v_mfma_f32_16x16x32_bf16 v[128:131],a[128:131],a[224:227],v[128:131]
ds_read_b64_tr_b16 a[178:179],v22 offset:36608
v_accvgpr_read_b32 v87,a45
v_mfma_f32_16x16x32_bf16 v[128:131],a[132:135],a[228:231],v[128:131]
ds_read_b64_tr_b16 a[182:183],v22 offset:52992
v_accvgpr_read_b32 v16,a46
v_mfma_f32_16x16x32_bf16 v[136:139],a[132:135],a[236:239],v[136:139]
ds_read_b64_tr_b16 a[186:187],v22 offset:36616
v_accvgpr_read_b32 v17,a47
v_mfma_f32_16x16x32_bf16 v[136:139],a[128:131],a[232:235],v[136:139]
ds_read_b64_tr_b16 a[190:191],v22 offset:53000
v_mul_f32 v86,s61,v86
v_mfma_f32_16x16x32_bf16 v[144:147],a[128:131],a[240:243],v[144:147]
v_accvgpr_read_b32 v76,a24
v_accvgpr_read_b32 v77,a25
v_accvgpr_read_b32 v78,a26
v_accvgpr_read_b32 v79,a27
v_accvgpr_read_b32 v12,a28
v_accvgpr_read_b32 v13,a29
v_accvgpr_read_b32 v14,a30
v_accvgpr_read_b32 v15,a31
v_pk_mul_f32 v[76:77],s[60:61],v[76:77] op_sel:[1,0] op_sel_hi:[1,1]
v_pk_mul_f32 v[78:79],s[60:61],v[78:79] op_sel:[1,0] op_sel_hi:[1,1]
v_pk_mul_f32 v[12:13],s[60:61],v[12:13] op_sel:[1,0] op_sel_hi:[1,1]
v_pk_mul_f32 v[14:15],s[60:61],v[14:15] op_sel:[1,0] op_sel_hi:[1,1]
v_cvt_pk_bf16_f32 v76,v76,v77
v_cvt_pk_bf16_f32 v77,v78,v79
v_cvt_pk_bf16_f32 v78,v12,v13
v_cvt_pk_bf16_f32 v79,v14,v15
v_mfma_f32_16x16x32_bf16 v[144:147],a[132:135],a[244:247],v[144:147]
buffer_load_dwordx4 v5,s[24:27],s47 offen offset:3072 nt lds
v_mfma_f32_16x16x32_bf16 v[152:155],a[132:135],a[252:255],v[152:155]
s_or_b32 s47,s47,0x18000
s_add_u32 s47,s47,4096
s_and_b32 s47,s47,0x27000
v_mfma_f32_16x16x32_bf16 v[152:155],a[128:131],a[248:251],v[152:155]
s_or_b32 s47,s47,s46
v_mul_f32 v87,s61,v87
v_mul_f32 v16,s61,v16
v_mfma_f32_16x16x32_bf16 v[132:135],a[136:139],a[224:227],v[132:135]
s_and_b32 s46,s54,0xfffc0000
v_mul_f32 v17,s61,v17
v_cvt_pk_bf16_f32 v86,v86,v87
v_mfma_f32_16x16x32_bf16 v[132:135],a[140:143],a[228:231],v[132:135]
s_add_u32 s54,s54,16384
v_cvt_pk_bf16_f32 v87,v16,v17
v_mfma_f32_16x16x32_bf16 v[140:143],a[140:143],a[236:239],v[140:143]
v_accvgpr_read_b32 v92,a56
v_accvgpr_read_b32 v93,a57
v_accvgpr_read_b32 v94,a58
v_accvgpr_read_b32 v95,a59
v_accvgpr_read_b32 v12,a60
v_accvgpr_read_b32 v13,a61
v_accvgpr_read_b32 v14,a62
v_accvgpr_read_b32 v15,a63
v_pk_mul_f32 v[92:93],s[60:61],v[92:93] op_sel:[1,0] op_sel_hi:[1,1]
v_pk_mul_f32 v[94:95],s[60:61],v[94:95] op_sel:[1,0] op_sel_hi:[1,1]
v_pk_mul_f32 v[12:13],s[60:61],v[12:13] op_sel:[1,0] op_sel_hi:[1,1]
v_pk_mul_f32 v[14:15],s[60:61],v[14:15] op_sel:[1,0] op_sel_hi:[1,1]
v_cvt_pk_bf16_f32 v92,v92,v93
v_cvt_pk_bf16_f32 v93,v94,v95
v_cvt_pk_bf16_f32 v94,v12,v13
v_cvt_pk_bf16_f32 v95,v14,v15
v_mfma_f32_16x16x32_bf16 v[140:143],a[136:139],a[232:235],v[140:143]
v_accvgpr_read_b32 v108,a88
v_accvgpr_read_b32 v109,a89
v_accvgpr_read_b32 v110,a90
v_accvgpr_read_b32 v111,a91
v_accvgpr_read_b32 v12,a92
v_accvgpr_read_b32 v13,a93
v_accvgpr_read_b32 v14,a94
v_accvgpr_read_b32 v15,a95
v_pk_mul_f32 v[108:109],s[60:61],v[108:109] op_sel:[1,0] op_sel_hi:[1,1]
v_pk_mul_f32 v[110:111],s[60:61],v[110:111] op_sel:[1,0] op_sel_hi:[1,1]
v_pk_mul_f32 v[12:13],s[60:61],v[12:13] op_sel:[1,0] op_sel_hi:[1,1]
v_pk_mul_f32 v[14:15],s[60:61],v[14:15] op_sel:[1,0] op_sel_hi:[1,1]
v_cvt_pk_bf16_f32 v108,v108,v109
v_cvt_pk_bf16_f32 v109,v110,v111
v_cvt_pk_bf16_f32 v110,v12,v13
v_cvt_pk_bf16_f32 v111,v14,v15
v_mfma_f32_16x16x32_bf16 v[148:151],a[136:139],a[240:243],v[148:151]
v_accvgpr_read_b32 v124,a120
v_accvgpr_read_b32 v125,a121
v_accvgpr_read_b32 v126,a122
v_accvgpr_read_b32 v127,a123
v_accvgpr_read_b32 v12,a124
v_accvgpr_read_b32 v13,a125
v_accvgpr_read_b32 v14,a126
v_accvgpr_read_b32 v15,a127
v_pk_mul_f32 v[124:125],s[60:61],v[124:125] op_sel:[1,0] op_sel_hi:[1,1]
v_pk_mul_f32 v[126:127],s[60:61],v[126:127] op_sel:[1,0] op_sel_hi:[1,1]
v_pk_mul_f32 v[12:13],s[60:61],v[12:13] op_sel:[1,0] op_sel_hi:[1,1]
v_pk_mul_f32 v[14:15],s[60:61],v[14:15] op_sel:[1,0] op_sel_hi:[1,1]
v_cvt_pk_bf16_f32 v124,v124,v125
v_cvt_pk_bf16_f32 v125,v126,v127
v_cvt_pk_bf16_f32 v126,v12,v13
v_cvt_pk_bf16_f32 v127,v14,v15
v_mfma_f32_16x16x32_bf16 v[148:151],a[140:143],a[244:247],v[148:151]
v_mfma_f32_16x16x32_bf16 v[156:159],a[140:143],a[252:255],v[156:159]
s_waitcnt vmcnt(8) lgkmcnt(0)
v_mfma_f32_16x16x32_bf16 v[156:159],a[136:139],a[248:251],v[156:159]
s_barrier
ds_read_b128 v[48:51],v18
v_mfma_f32_16x16x32_bf16 v[160:163],a[144:147],a[224:227],v[160:163]
ds_read_b64_tr_b16 a[192:193],v2
v_mfma_f32_16x16x32_bf16 v[160:163],a[148:151],a[228:231],v[160:163]
ds_read_b64_tr_b16 a[194:195],v2 offset:4096
v_mfma_f32_16x16x32_bf16 v[168:171],a[148:151],a[236:239],v[168:171]
ds_read_b64_tr_b16 a[196:197],v2 offset:8192
v_mfma_f32_16x16x32_bf16 v[168:171],a[144:147],a[232:235],v[168:171]
ds_read_b128 v[52:55],v18 offset:256
v_mfma_f32_16x16x32_bf16 v[176:179],a[144:147],a[240:243],v[176:179]
v_mul_f32 v128,s61,v128
v_mul_f32 v129,s61,v129
v_mul_f32 v130,s61,v130
v_mul_f32 v131,s61,v131
v_cvt_pk_bf16_f32 v128,v128,v129
v_cvt_pk_bf16_f32 v129,v130,v131
)A"
R"A(v_pk_mul_f32 v[132:133],s[60:61],v[132:133] op_sel:[1,0] op_sel_hi:[1,1]
v_pk_mul_f32 v[134:135],s[60:61],v[134:135] op_sel:[1,0] op_sel_hi:[1,1]
v_pk_mul_f32 v[136:137],s[60:61],v[136:137] op_sel:[1,0] op_sel_hi:[1,1]
v_pk_mul_f32 v[138:139],s[60:61],v[138:139] op_sel:[1,0] op_sel_hi:[1,1]
v_pk_mul_f32 v[140:141],s[60:61],v[140:141] op_sel:[1,0] op_sel_hi:[1,1]
v_pk_mul_f32 v[142:143],s[60:61],v[142:143] op_sel:[1,0] op_sel_hi:[1,1]
v_pk_mul_f32 v[144:145],s[60:61],v[144:145] op_sel:[1,0] op_sel_hi:[1,1]
v_pk_mul_f32 v[146:147],s[60:61],v[146:147] op_sel:[1,0] op_sel_hi:[1,1]
v_pk_mul_f32 v[148:149],s[60:61],v[148:149] op_sel:[1,0] op_sel_hi:[1,1]
v_pk_mul_f32 v[150:151],s[60:61],v[150:151] op_sel:[1,0] op_sel_hi:[1,1]
v_pk_mul_f32 v[152:153],s[60:61],v[152:153] op_sel:[1,0] op_sel_hi:[1,1]
v_pk_mul_f32 v[154:155],s[60:61],v[154:155] op_sel:[1,0] op_sel_hi:[1,1]
v_pk_mul_f32 v[156:157],s[60:61],v[156:157] op_sel:[1,0] op_sel_hi:[1,1]
v_pk_mul_f32 v[158:159],s[60:61],v[158:159] op_sel:[1,0] op_sel_hi:[1,1]
v_mfma_f32_16x16x32_bf16 v[176:179],a[148:151],a[244:247],v[176:179]
ds_read_b128 v[56:59],v18 offset:512
v_cvt_pk_bf16_f32 v130,v132,v133
v_mfma_f32_16x16x32_bf16 v[184:187],a[148:151],a[252:255],v[184:187]
v_cvt_pk_bf16_f32 v132,v136,v137
v_cvt_pk_bf16_f32 v133,v138,v139
v_mfma_f32_16x16x32_bf16 v[184:187],a[144:147],a[248:251],v[184:187]
ds_read_b128 v[60:63],v18 offset:768
v_cvt_pk_bf16_f32 v131,v134,v135
v_mfma_f32_16x16x32_bf16 v[164:167],a[152:155],a[224:227],v[164:167]
v_cvt_pk_bf16_f32 v134,v140,v141
v_cvt_pk_bf16_f32 v135,v142,v143
v_mfma_f32_16x16x32_bf16 v[164:167],a[156:159],a[228:231],v[164:167]
ds_read_b128 a[128:131],v1
v_cvt_pk_bf16_f32 v144,v144,v145
v_mfma_f32_16x16x32_bf16 v[172:175],a[156:159],a[236:239],v[172:175]
v_cvt_pk_bf16_f32 v145,v146,v147
v_cvt_pk_bf16_f32 v146,v148,v149
v_mfma_f32_16x16x32_bf16 v[172:175],a[152:155],a[232:235],v[172:175]
ds_read_b128 a[132:135],v1 offset:256
v_cvt_pk_bf16_f32 v148,v152,v153
v_mfma_f32_16x16x32_bf16 v[180:183],a[152:155],a[240:243],v[180:183]
v_cvt_pk_bf16_f32 v149,v154,v155
v_cvt_pk_bf16_f32 v147,v150,v151
v_mfma_f32_16x16x32_bf16 v[180:183],a[156:159],a[244:247],v[180:183]
ds_read_b128 a[136:139],v1 offset:512
v_cvt_pk_bf16_f32 v150,v156,v157
v_mfma_f32_16x16x32_bf16 v[188:191],a[156:159],a[252:255],v[188:191]
ds_read_b64_tr_b16 a[198:199],v2 offset:12288
v_cvt_pk_bf16_f32 v151,v158,v159
v_mfma_f32_16x16x32_bf16 v[188:191],a[152:155],a[248:251],v[188:191]
ds_read_b64_tr_b16 a[204:205],v3 offset:8192
v_mfma_f32_16x16x32_bf16 v[192:195],a[160:163],a[224:227],v[192:195]
ds_read_b64_tr_b16 a[206:207],v3 offset:12288
v_mfma_f32_16x16x32_bf16 v[192:195],a[164:167],a[228:231],v[192:195]
s_add_u32 m0,32768,s51
ds_read_b128 a[140:143],v1 offset:768
v_mfma_f32_16x16x32_bf16 v[200:203],a[164:167],a[236:239],v[200:203]
ds_read_b64_tr_b16 a[200:201],v3
v_mfma_f32_16x16x32_bf16 v[200:203],a[160:163],a[232:235],v[200:203]
ds_read_b64_tr_b16 a[202:203],v3 offset:4096
v_mfma_f32_16x16x32_bf16 v[208:211],a[160:163],a[240:243],v[208:211]
buffer_load_dwordx4 v4,s[20:23],s47 offen nt lds
v_mfma_f32_16x16x32_bf16 v[208:211],a[164:167],a[244:247],v[208:211]
ds_read_b128 a[156:159],v1 offset:4864
v_mfma_f32_16x16x32_bf16 v[216:219],a[164:167],a[252:255],v[216:219]
v_mul_f32 v160,s61,v160
v_mul_f32 v161,s61,v161
v_mul_f32 v162,s61,v162
v_mul_f32 v163,s61,v163
v_cvt_pk_bf16_f32 v160,v160,v161
v_cvt_pk_bf16_f32 v161,v162,v163
v_pk_mul_f32 v[164:165],s[60:61],v[164:165] op_sel:[1,0] op_sel_hi:[1,1]
v_pk_mul_f32 v[166:167],s[60:61],v[166:167] op_sel:[1,0] op_sel_hi:[1,1]
v_pk_mul_f32 v[168:169],s[60:61],v[168:169] op_sel:[1,0] op_sel_hi:[1,1]
v_pk_mul_f32 v[170:171],s[60:61],v[170:171] op_sel:[1,0] op_sel_hi:[1,1]
v_pk_mul_f32 v[172:173],s[60:61],v[172:173] op_sel:[1,0] op_sel_hi:[1,1]
v_pk_mul_f32 v[174:175],s[60:61],v[174:175] op_sel:[1,0] op_sel_hi:[1,1]
v_pk_mul_f32 v[176:177],s[60:61],v[176:177] op_sel:[1,0] op_sel_hi:[1,1]
v_pk_mul_f32 v[178:179],s[60:61],v[178:179] op_sel:[1,0] op_sel_hi:[1,1]
v_pk_mul_f32 v[180:181],s[60:61],v[180:181] op_sel:[1,0] op_sel_hi:[1,1]
v_pk_mul_f32 v[182:183],s[60:61],v[182:183] op_sel:[1,0] op_sel_hi:[1,1]
v_pk_mul_f32 v[184:185],s[60:61],v[184:185] op_sel:[1,0] op_sel_hi:[1,1]
v_pk_mul_f32 v[186:187],s[60:61],v[186:187] op_sel:[1,0] op_sel_hi:[1,1]
v_pk_mul_f32 v[188:189],s[60:61],v[188:189] op_sel:[1,0] op_sel_hi:[1,1]
v_pk_mul_f32 v[190:191],s[60:61],v[190:191] op_sel:[1,0] op_sel_hi:[1,1]
v_mfma_f32_16x16x32_bf16 v[216:219],a[160:163],a[248:251],v[216:219]
ds_read_b128 a[152:155],v1 offset:4608
v_cvt_pk_bf16_f32 v162,v164,v165
v_mfma_f32_16x16x32_bf16 v[196:199],a[168:171],a[224:227],v[196:199]
v_cvt_pk_bf16_f32 v164,v168,v169
v_cvt_pk_bf16_f32 v165,v170,v171
v_mfma_f32_16x16x32_bf16 v[196:199],a[172:175],a[228:231],v[196:199]
ds_read_b128 a[148:151],v1 offset:4352
v_cvt_pk_bf16_f32 v163,v166,v167
v_mfma_f32_16x16x32_bf16 v[204:207],a[172:175],a[236:239],v[204:207]
v_cvt_pk_bf16_f32 v166,v172,v173
v_cvt_pk_bf16_f32 v167,v174,v175
v_mfma_f32_16x16x32_bf16 v[204:207],a[168:171],a[232:235],v[204:207]
ds_read_b128 a[144:147],v1 offset:4096
v_cvt_pk_bf16_f32 v176,v176,v177
v_mfma_f32_16x16x32_bf16 v[212:215],a[168:171],a[240:243],v[212:215]
v_cvt_pk_bf16_f32 v177,v178,v179
v_cvt_pk_bf16_f32 v178,v180,v181
v_mfma_f32_16x16x32_bf16 v[212:215],a[172:175],a[244:247],v[212:215]
ds_read_b128 a[160:163],v1 offset:8192
v_cvt_pk_bf16_f32 v180,v184,v185
v_mfma_f32_16x16x32_bf16 v[220:223],a[172:175],a[252:255],v[220:223]
v_cvt_pk_bf16_f32 v181,v186,v187
v_cvt_pk_bf16_f32 v179,v182,v183
v_mfma_f32_16x16x32_bf16 v[220:223],a[168:171],a[248:251],v[220:223]
ds_read_b128 a[164:167],v1 offset:8448
v_cvt_pk_bf16_f32 v182,v188,v189
v_mfma_f32_16x16x32_bf16 v[224:227],a[176:179],a[224:227],v[224:227]
ds_read_b64_tr_b16 a[208:209],v2 offset:256
v_cvt_pk_bf16_f32 v183,v190,v191
v_mfma_f32_16x16x32_bf16 v[224:227],a[180:183],a[228:231],v[224:227]
buffer_load_dwordx4 v4,s[20:23],s47 offen offset:1024 nt lds
v_mfma_f32_16x16x32_bf16 v[232:235],a[180:183],a[236:239],v[232:235]
ds_read_b128 a[168:171],v1 offset:8704
v_mfma_f32_16x16x32_bf16 v[232:235],a[176:179],a[232:235],v[232:235]
ds_read_b64_tr_b16 a[210:211],v2 offset:4352
v_mfma_f32_16x16x32_bf16 v[240:243],a[176:179],a[240:243],v[240:243]
ds_read_b64_tr_b16 a[212:213],v2 offset:8448
v_mfma_f32_16x16x32_bf16 v[240:243],a[180:183],a[244:247],v[240:243]
v_mul_f32 v192,s61,v192
v_mul_f32 v193,s61,v193
v_mul_f32 v194,s61,v194
v_mul_f32 v195,s61,v195
v_cvt_pk_bf16_f32 v192,v192,v193
v_cvt_pk_bf16_f32 v193,v194,v195
v_pk_mul_f32 v[196:197],s[60:61],v[196:197] op_sel:[1,0] op_sel_hi:[1,1]
v_pk_mul_f32 v[198:199],s[60:61],v[198:199] op_sel:[1,0] op_sel_hi:[1,1]
v_pk_mul_f32 v[200:201],s[60:61],v[200:201] op_sel:[1,0] op_sel_hi:[1,1]
v_pk_mul_f32 v[202:203],s[60:61],v[202:203] op_sel:[1,0] op_sel_hi:[1,1]
v_pk_mul_f32 v[204:205],s[60:61],v[204:205] op_sel:[1,0] op_sel_hi:[1,1]
v_pk_mul_f32 v[206:207],s[60:61],v[206:207] op_sel:[1,0] op_sel_hi:[1,1]
v_pk_mul_f32 v[208:209],s[60:61],v[208:209] op_sel:[1,0] op_sel_hi:[1,1]
v_pk_mul_f32 v[210:211],s[60:61],v[210:211] op_sel:[1,0] op_sel_hi:[1,1]
v_pk_mul_f32 v[212:213],s[60:61],v[212:213] op_sel:[1,0] op_sel_hi:[1,1]
v_pk_mul_f32 v[214:215],s[60:61],v[214:215] op_sel:[1,0] op_sel_hi:[1,1]
v_pk_mul_f32 v[216:217],s[60:61],v[216:217] op_sel:[1,0] op_sel_hi:[1,1]
v_pk_mul_f32 v[218:219],s[60:61],v[218:219] op_sel:[1,0] op_sel_hi:[1,1]
v_pk_mul_f32 v[220:221],s[60:61],v[220:221] op_sel:[1,0] op_sel_hi:[1,1]
v_pk_mul_f32 v[222:223],s[60:61],v[222:223] op_sel:[1,0] op_sel_hi:[1,1]
v_mfma_f32_16x16x32_bf16 v[248:251],a[180:183],a[252:255],v[248:251]
ds_read_b128 a[172:175],v1 offset:8960
v_cvt_pk_bf16_f32 v194,v196,v197
v_mfma_f32_16x16x32_bf16 v[248:251],a[176:179],a[248:251],v[248:251]
v_cvt_pk_bf16_f32 v196,v200,v201
v_cvt_pk_bf16_f32 v197,v202,v203
v_mfma_f32_16x16x32_bf16 v[228:231],a[184:187],a[224:227],v[228:231]
ds_read_b128 a[180:183],v1 offset:12544
v_cvt_pk_bf16_f32 v195,v198,v199
v_mfma_f32_16x16x32_bf16 v[228:231],a[188:191],a[228:231],v[228:231]
v_cvt_pk_bf16_f32 v198,v204,v205
v_cvt_pk_bf16_f32 v199,v206,v207
v_mfma_f32_16x16x32_bf16 v[236:239],a[188:191],a[236:239],v[236:239]
ds_read_b128 a[176:179],v1 offset:12288
v_cvt_pk_bf16_f32 v208,v208,v209
v_mfma_f32_16x16x32_bf16 v[236:239],a[184:187],a[232:235],v[236:239]
v_cvt_pk_bf16_f32 v209,v210,v211
v_cvt_pk_bf16_f32 v210,v212,v213
v_mfma_f32_16x16x32_bf16 v[244:247],a[184:187],a[240:243],v[244:247]
v_cvt_pk_bf16_f32 v212,v216,v217
v_cvt_pk_bf16_f32 v213,v218,v219
v_mfma_f32_16x16x32_bf16 v[244:247],a[188:191],a[244:247],v[244:247]
v_cvt_pk_bf16_f32 v211,v214,v215
v_cvt_pk_bf16_f32 v214,v220,v221
v_mfma_f32_16x16x32_bf16 v[252:255],a[188:191],a[252:255],v[252:255]
buffer_load_dwordx4 v5,s[20:23],s47 offen offset:2048 nt lds
v_mfma_f32_16x16x32_bf16 v[252:255],a[184:187],a[248:251],v[252:255]
s_waitcnt lgkmcnt(2)
ds_read_b128 a[188:191],v1 offset:13056
v_cvt_pk_bf16_f32 v215,v222,v223
v_mfma_f32_16x16x32_bf16 v[32:35],v[48:51],v[64:67],0
ds_read_b64_tr_b16 a[214:215],v2 offset:12544
v_mfma_f32_16x16x32_bf16 v[32:35],v[52:55],v[68:71],v[32:35]
ds_read_b64_tr_b16 a[220:221],v3 offset:8448
v_mfma_f32_16x16x32_bf16 v[32:35],v[56:59],v[72:75],v[32:35]
ds_read_b64_tr_b16 a[222:223],v3 offset:12544
v_mfma_f32_16x16x32_bf16 v[32:35],v[60:63],v[76:79],v[32:35]
ds_read_b128 a[184:187],v1 offset:12800
v_add_u32 v1,s48,v1
v_mfma_f32_16x16x32_bf16 v[36:39],v[48:51],v[80:83],0
ds_read_b64_tr_b16 a[216:217],v3 offset:256
v_mfma_f32_16x16x32_bf16 v[36:39],v[52:55],v[84:87],v[36:39]
ds_read_b64_tr_b16 a[218:219],v3 offset:4352
v_mfma_f32_16x16x32_bf16 v[36:39],v[56:59],v[88:91],v[36:39]
ds_read_b64_tr_b16 a[224:225],v2 offset:512
v_mfma_f32_16x16x32_bf16 v[36:39],v[60:63],v[92:95],v[36:39]
ds_read_b64_tr_b16 a[226:227],v2 offset:4608
ds_read_b64_tr_b16 a[228:229],v2 offset:8704
v_mfma_f32_16x16x32_bf16 v[40:43],v[48:51],v[96:99],0
ds_read_b64_tr_b16 a[230:231],v2 offset:12800
v_mfma_f32_16x16x32_bf16 v[40:43],v[52:55],v[100:103],v[40:43]
v_max3_f32 v32,v32,v33,v34
v_max3_f32 v32,v32,v35,s18
v_mfma_f32_16x16x32_bf16 v[40:43],v[56:59],v[104:107],v[40:43]
v_sub_f32 v35,0,v32
v_sub_f32 v34,0,v32
v_mfma_f32_16x16x32_bf16 v[40:43],v[60:63],v[108:111],v[40:43]
v_sub_f32 v33,0,v32
v_sub_f32 v32,0,v32
v_mfma_f32_16x16x32_bf16 v[44:47],v[48:51],v[112:115],0
buffer_load_dwordx4 v5,s[20:23],s47 offen offset:3072 nt lds
v_mfma_f32_16x16x32_bf16 v[44:47],v[52:55],v[116:119],v[44:47]
v_max3_f32 v36,v36,v37,v38
v_max3_f32 v36,v36,v39,s19
v_mfma_f32_16x16x32_bf16 v[44:47],v[56:59],v[120:123],v[44:47]
v_sub_f32 v39,0,v36
v_sub_f32 v38,0,v36
v_mfma_f32_16x16x32_bf16 v[44:47],v[60:63],v[124:127],v[44:47]
v_sub_f32 v37,0,v36
v_sub_f32 v36,0,v36
v_mfma_f32_16x16x32_bf16 v[136:139],a[128:131],v[64:67],v[32:35]
ds_read_b64_tr_b16 a[236:237],v3 offset:8704
ds_read_b64_tr_b16 a[238:239],v3 offset:12800
v_mfma_f32_16x16x32_bf16 v[136:139],a[132:135],v[68:71],v[136:139]
v_max3_f32 v40,v40,v41,v42
v_max3_f32 v40,v40,v43,s100
v_mfma_f32_16x16x32_bf16 v[136:139],a[136:139],v[72:75],v[136:139]
v_sub_f32 v43,0,v40
v_sub_f32 v42,0,v40
v_mfma_f32_16x16x32_bf16 v[136:139],a[140:143],v[76:79],v[136:139]
v_sub_f32 v41,0,v40
v_sub_f32 v40,0,v40
v_mfma_f32_16x16x32_bf16 v[140:143],a[156:159],v[76:79],v[32:35]
ds_read_b64_tr_b16 a[232:233],v3 offset:512
ds_read_b64_tr_b16 a[234:235],v3 offset:4608
v_mfma_f32_16x16x32_bf16 v[140:143],a[152:155],v[72:75],v[140:143]
s_add_u32 m0,32768,s52
ds_read_b64_tr_b16 a[240:241],v2 offset:768
v_max3_f32 v44,v44,v45,v46
v_mfma_f32_16x16x32_bf16 v[140:143],a[148:151],v[68:71],v[140:143]
ds_read_b64_tr_b16 a[242:243],v2 offset:4864
v_max3_f32 v44,v44,v47,s101
v_mfma_f32_16x16x32_bf16 v[140:143],a[144:147],v[64:67],v[140:143]
ds_read_b64_tr_b16 a[244:245],v2 offset:8960
v_mov_b32 v48,0
v_mfma_f32_16x16x32_bf16 v[152:155],a[160:163],v[64:67],v[32:35]
buffer_load_dwordx4 v4,s[24:27],s47 offen nt lds
v_mfma_f32_16x16x32_bf16 v[152:155],a[164:167],v[68:71],v[152:155]
v_exp_f32 v136,v136
v_mfma_f32_16x16x32_bf16 v[152:155],a[168:171],v[72:75],v[152:155]
)A"
R"A(v_exp_f32 v137,v137
v_mfma_f32_16x16x32_bf16 v[152:155],a[172:175],v[76:79],v[152:155]
s_waitcnt lgkmcnt(5)
v_exp_f32 v138,v138
v_mfma_f32_16x16x32_bf16 v[156:159],a[188:191],v[76:79],v[32:35]
v_exp_f32 v139,v139
v_mfma_f32_16x16x32_bf16 v[156:159],a[184:187],v[72:75],v[156:159]
v_cvt_pk_bf16_f32 v136,v136,v137
v_cvt_pk_bf16_f32 v137,v138,v139
v_mfma_f32_16x16x32_bf16 v[156:159],a[180:183],v[68:71],v[156:159]
v_exp_f32 v140,v140
v_mfma_f32_16x16x32_bf16 v[156:159],a[176:179],v[64:67],v[156:159]
v_exp_f32 v141,v141
v_mfma_f32_16x16x32_bf16 v[168:171],a[128:131],v[80:83],v[36:39]
v_exp_f32 v142,v142
v_mfma_f32_16x16x32_bf16 v[168:171],a[132:135],v[84:87],v[168:171]
v_exp_f32 v143,v143
v_mfma_f32_16x16x32_bf16 v[168:171],a[136:139],v[88:91],v[168:171]
v_cvt_pk_bf16_f32 v138,v140,v141
v_cvt_pk_bf16_f32 v139,v142,v143
v_mfma_f32_16x16x32_bf16 v[168:171],a[140:143],v[92:95],v[168:171]
v_exp_f32 v152,v152
v_mfma_f32_16x16x32_bf16 v[172:175],a[156:159],v[92:95],v[36:39]
v_exp_f32 v153,v153
v_mfma_f32_16x16x32_bf16 v[172:175],a[152:155],v[88:91],v[172:175]
buffer_load_dwordx4 v5,s[24:27],s47 offen offset:1024 nt lds
v_mfma_f32_16x16x32_bf16 v[172:175],a[148:151],v[84:87],v[172:175]
v_exp_f32 v154,v154
v_mfma_f32_16x16x32_bf16 v[172:175],a[144:147],v[80:83],v[172:175]
v_exp_f32 v155,v155
v_mfma_f32_16x16x32_bf16 v[184:187],a[160:163],v[80:83],v[36:39]
v_exp_f32 v156,v156
v_mfma_f32_16x16x32_bf16 v[184:187],a[164:167],v[84:87],v[184:187]
v_exp_f32 v157,v157
v_mfma_f32_16x16x32_bf16 v[184:187],a[168:171],v[88:91],v[184:187]
v_exp_f32 v158,v158
v_mfma_f32_16x16x32_bf16 v[184:187],a[172:175],v[92:95],v[184:187]
v_exp_f32 v159,v159
v_mfma_f32_16x16x32_bf16 v[188:191],a[188:191],v[92:95],v[36:39]
v_cvt_pk_bf16_f32 v140,v152,v153
v_cvt_pk_bf16_f32 v141,v154,v155
v_mfma_f32_16x16x32_bf16 v[188:191],a[184:187],v[88:91],v[188:191]
v_cvt_pk_bf16_f32 v142,v156,v157
v_cvt_pk_bf16_f32 v143,v158,v159
v_mfma_f32_16x16x32_bf16 v[188:191],a[180:183],v[84:87],v[188:191]
ds_read_b64_tr_b16 a[246:247],v2 offset:13056
ds_read_b64_tr_b16 a[252:253],v3 offset:8960
v_mfma_f32_16x16x32_bf16 v[188:191],a[176:179],v[80:83],v[188:191]
ds_read_b64_tr_b16 a[254:255],v3 offset:13056
v_mov_b32 v49,0
v_mfma_f32_16x16x32_bf16 v[200:203],a[128:131],v[96:99],v[40:43]
ds_read_b64_tr_b16 a[248:249],v3 offset:768
v_mov_b32 v50,0
v_mfma_f32_16x16x32_bf16 v[200:203],a[132:135],v[100:103],v[200:203]
ds_read_b64_tr_b16 a[250:251],v3 offset:4864
v_mov_b32 v51,0
v_mfma_f32_16x16x32_bf16 v[200:203],a[136:139],v[104:107],v[200:203]
buffer_load_dwordx4 v4,s[24:27],s47 offen offset:2048 nt lds
v_mfma_f32_16x16x32_bf16 v[200:203],a[140:143],v[108:111],v[200:203]
v_sub_f32 v47,0,v44
v_sub_f32 v46,0,v44
v_mfma_f32_16x16x32_bf16 v[204:207],a[156:159],v[108:111],v[40:43]
v_sub_f32 v45,0,v44
v_sub_f32 v44,0,v44
v_mfma_f32_16x16x32_bf16 v[204:207],a[152:155],v[104:107],v[204:207]
v_mul_f32 v224,s61,v224
v_mul_f32 v225,s61,v225
v_mul_f32 v226,s61,v226
v_mul_f32 v227,s61,v227
v_cvt_pk_bf16_f32 v224,v224,v225
v_cvt_pk_bf16_f32 v225,v226,v227
v_pk_mul_f32 v[228:229],s[60:61],v[228:229] op_sel:[1,0] op_sel_hi:[1,1]
v_pk_mul_f32 v[230:231],s[60:61],v[230:231] op_sel:[1,0] op_sel_hi:[1,1]
v_pk_mul_f32 v[232:233],s[60:61],v[232:233] op_sel:[1,0] op_sel_hi:[1,1]
v_pk_mul_f32 v[234:235],s[60:61],v[234:235] op_sel:[1,0] op_sel_hi:[1,1]
v_pk_mul_f32 v[236:237],s[60:61],v[236:237] op_sel:[1,0] op_sel_hi:[1,1]
v_pk_mul_f32 v[238:239],s[60:61],v[238:239] op_sel:[1,0] op_sel_hi:[1,1]
v_pk_mul_f32 v[240:241],s[60:61],v[240:241] op_sel:[1,0] op_sel_hi:[1,1]
v_pk_mul_f32 v[242:243],s[60:61],v[242:243] op_sel:[1,0] op_sel_hi:[1,1]
v_pk_mul_f32 v[244:245],s[60:61],v[244:245] op_sel:[1,0] op_sel_hi:[1,1]
v_pk_mul_f32 v[246:247],s[60:61],v[246:247] op_sel:[1,0] op_sel_hi:[1,1]
v_pk_mul_f32 v[248:249],s[60:61],v[248:249] op_sel:[1,0] op_sel_hi:[1,1]
v_pk_mul_f32 v[250:251],s[60:61],v[250:251] op_sel:[1,0] op_sel_hi:[1,1]
v_pk_mul_f32 v[252:253],s[60:61],v[252:253] op_sel:[1,0] op_sel_hi:[1,1]
v_pk_mul_f32 v[254:255],s[60:61],v[254:255] op_sel:[1,0] op_sel_hi:[1,1]
v_mfma_f32_16x16x32_bf16 v[204:207],a[148:151],v[100:103],v[204:207]
v_cvt_pk_bf16_f32 v226,v228,v229
v_cvt_pk_bf16_f32 v228,v232,v233
v_mfma_f32_16x16x32_bf16 v[204:207],a[144:147],v[96:99],v[204:207]
v_cvt_pk_bf16_f32 v229,v234,v235
v_cvt_pk_bf16_f32 v227,v230,v231
v_mfma_f32_16x16x32_bf16 v[216:219],a[160:163],v[96:99],v[40:43]
v_cvt_pk_bf16_f32 v230,v236,v237
v_cvt_pk_bf16_f32 v231,v238,v239
v_mfma_f32_16x16x32_bf16 v[216:219],a[164:167],v[100:103],v[216:219]
v_cvt_pk_bf16_f32 v240,v240,v241
v_cvt_pk_bf16_f32 v241,v242,v243
v_mfma_f32_16x16x32_bf16 v[216:219],a[168:171],v[104:107],v[216:219]
v_cvt_pk_bf16_f32 v242,v244,v245
v_cvt_pk_bf16_f32 v244,v248,v249
v_mfma_f32_16x16x32_bf16 v[216:219],a[172:175],v[108:111],v[216:219]
v_cvt_pk_bf16_f32 v245,v250,v251
v_cvt_pk_bf16_f32 v243,v246,v247
v_mfma_f32_16x16x32_bf16 v[220:223],a[188:191],v[108:111],v[40:43]
v_cvt_pk_bf16_f32 v246,v252,v253
v_cvt_pk_bf16_f32 v247,v254,v255
v_mfma_f32_16x16x32_bf16 v[220:223],a[184:187],v[104:107],v[220:223]
v_exp_f32 v168,v168
v_mfma_f32_16x16x32_bf16 v[220:223],a[180:183],v[100:103],v[220:223]
v_exp_f32 v169,v169
v_mfma_f32_16x16x32_bf16 v[220:223],a[176:179],v[96:99],v[220:223]
buffer_load_dwordx4 v5,s[24:27],s47 offen offset:3072 nt lds
v_mfma_f32_16x16x32_bf16 a[0:3],a[192:195],v[136:139],0
s_or_b32 s47,s47,0x18000
s_add_u32 s47,s47,4096
s_and_b32 s47,s47,0x27000
v_mfma_f32_16x16x32_bf16 a[0:3],a[196:199],v[140:143],a[0:3]
s_or_b32 s47,s47,s46
v_exp_f32 v170,v170
v_mfma_f32_16x16x32_bf16 a[4:7],a[204:207],v[140:143],0
s_and_b32 s46,s54,0xfffc0000
v_exp_f32 v171,v171
v_mfma_f32_16x16x32_bf16 a[4:7],a[200:203],v[136:139],a[4:7]
s_memrealtime s[76:77]
v_cvt_pk_bf16_f32 v168,v168,v169
v_cvt_pk_bf16_f32 v169,v170,v171
v_mfma_f32_16x16x32_bf16 a[8:11],a[208:211],v[136:139],0
v_exp_f32 v172,v172
v_mfma_f32_16x16x32_bf16 a[8:11],a[212:215],v[140:143],a[8:11]
v_exp_f32 v173,v173
v_mfma_f32_16x16x32_bf16 a[12:15],a[220:223],v[140:143],0
v_exp_f32 v174,v174
v_mfma_f32_16x16x32_bf16 a[12:15],a[216:219],v[136:139],a[12:15]
v_exp_f32 v175,v175
v_mfma_f32_16x16x32_bf16 a[16:19],a[224:227],v[136:139],0
v_cvt_pk_bf16_f32 v170,v172,v173
v_cvt_pk_bf16_f32 v171,v174,v175
v_mfma_f32_16x16x32_bf16 a[16:19],a[228:231],v[140:143],a[16:19]
v_exp_f32 v184,v184
v_mfma_f32_16x16x32_bf16 a[20:23],a[236:239],v[140:143],0
s_waitcnt lgkmcnt(0)
v_exp_f32 v185,v185
v_mfma_f32_16x16x32_bf16 a[20:23],a[232:235],v[136:139],a[20:23]
s_add_u32 s78,s76,s60
v_exp_f32 v186,v186
v_mfma_f32_16x16x32_bf16 a[24:27],a[240:243],v[136:139],0
s_lshr_b32 s60,s94,2
v_exp_f32 v187,v187
v_mfma_f32_16x16x32_bf16 a[24:27],a[244:247],v[140:143],a[24:27]
s_max_i32 s60,s60,128
v_exp_f32 v188,v188
v_mfma_f32_16x16x32_bf16 a[28:31],a[252:255],v[140:143],0
v_exp_f32 v189,v189
v_mfma_f32_16x16x32_bf16 a[28:31],a[248:251],v[136:139],a[28:31]
v_exp_f32 v190,v190
v_smfmac_f32_16x16x64_bf16 v[48:51],v[28:31],v[136:143],v27
v_cvt_pk_bf16_f32 v172,v184,v185
v_mfma_f32_16x16x32_bf16 v[232:235],a[128:131],v[112:115],v[44:47]
s_waitcnt vmcnt(8)
v_exp_f32 v191,v191
v_mfma_f32_16x16x32_bf16 v[232:235],a[132:135],v[116:119],v[232:235]
s_barrier
s_add_u32 m0,65536,s51
v_cvt_pk_bf16_f32 v173,v186,v187
v_mfma_f32_16x16x32_bf16 v[232:235],a[136:139],v[120:123],v[232:235]
v_cvt_pk_bf16_f32 v174,v188,v189
v_cvt_pk_bf16_f32 v175,v190,v191
v_mfma_f32_16x16x32_bf16 v[232:235],a[140:143],v[124:127],v[232:235]
ds_read_b128 a[128:131],v1
v_mov_b32 v52,0
v_mfma_f32_16x16x32_bf16 v[236:239],a[156:159],v[124:127],v[44:47]
buffer_load_dwordx4 v4,s[20:23],s47 offen nt lds
v_mfma_f32_16x16x32_bf16 v[236:239],a[152:155],v[120:123],v[236:239]
ds_read_b128 a[132:135],v1 offset:256
v_mov_b32 v53,0
v_mfma_f32_16x16x32_bf16 v[236:239],a[148:151],v[116:119],v[236:239]
v_mov_b32 v54,0
v_mov_b32 v55,0
v_mfma_f32_16x16x32_bf16 v[236:239],a[144:147],v[112:115],v[236:239]
ds_read_b128 a[136:139],v1 offset:512
v_mov_b32 v56,0
v_mfma_f32_16x16x32_bf16 v[248:251],a[160:163],v[112:115],v[44:47]
v_exp_f32 v200,v200
v_mfma_f32_16x16x32_bf16 v[248:251],a[164:167],v[116:119],v[248:251]
ds_read_b128 a[140:143],v1 offset:768
v_mov_b32 v57,0
v_mfma_f32_16x16x32_bf16 v[248:251],a[168:171],v[120:123],v[248:251]
v_exp_f32 v201,v201
v_mfma_f32_16x16x32_bf16 v[248:251],a[172:175],v[124:127],v[248:251]
ds_read_b128 a[156:159],v1 offset:4864
v_cvt_pk_bf16_f32 v200,v200,v201
v_mfma_f32_16x16x32_bf16 v[252:255],a[188:191],v[124:127],v[44:47]
v_exp_f32 v202,v202
v_mfma_f32_16x16x32_bf16 v[252:255],a[184:187],v[120:123],v[252:255]
ds_read_b128 a[152:155],v1 offset:4608
v_mov_b32 v58,0
v_mfma_f32_16x16x32_bf16 v[252:255],a[180:183],v[116:119],v[252:255]
v_exp_f32 v203,v203
v_mfma_f32_16x16x32_bf16 v[252:255],a[176:179],v[112:115],v[252:255]
ds_read_b128 a[148:151],v1 offset:4352
v_cvt_pk_bf16_f32 v201,v202,v203
v_mfma_f32_16x16x32_bf16 a[32:35],a[192:195],v[168:171],0
v_exp_f32 v204,v204
v_mfma_f32_16x16x32_bf16 a[32:35],a[196:199],v[172:175],a[32:35]
ds_read_b128 a[144:147],v1 offset:4096
v_mov_b32 v59,0
v_mfma_f32_16x16x32_bf16 a[36:39],a[204:207],v[172:175],0
buffer_load_dwordx4 v4,s[20:23],s47 offen offset:1024 nt lds
v_mfma_f32_16x16x32_bf16 a[36:39],a[200:203],v[168:171],a[36:39]
ds_read_b128 a[160:163],v1 offset:8192
v_mov_b32 v60,0
v_mfma_f32_16x16x32_bf16 a[40:43],a[208:211],v[168:171],0
v_exp_f32 v205,v205
v_mfma_f32_16x16x32_bf16 a[40:43],a[212:215],v[172:175],a[40:43]
ds_read_b128 a[164:167],v1 offset:8448
v_cvt_pk_bf16_f32 v202,v204,v205
v_mfma_f32_16x16x32_bf16 a[44:47],a[220:223],v[172:175],0
v_exp_f32 v206,v206
v_mfma_f32_16x16x32_bf16 a[44:47],a[216:219],v[168:171],a[44:47]
ds_read_b128 a[168:171],v1 offset:8704
v_mov_b32 v61,0
v_mfma_f32_16x16x32_bf16 a[48:51],a[224:227],v[168:171],0
v_exp_f32 v207,v207
v_mfma_f32_16x16x32_bf16 a[48:51],a[228:231],v[172:175],a[48:51]
ds_read_b128 a[172:175],v1 offset:8960
v_cvt_pk_bf16_f32 v203,v206,v207
v_mfma_f32_16x16x32_bf16 a[52:55],a[236:239],v[172:175],0
v_exp_f32 v216,v216
v_mfma_f32_16x16x32_bf16 a[52:55],a[232:235],v[168:171],a[52:55]
ds_read_b128 a[188:191],v1 offset:13056
v_mov_b32 v62,0
v_mfma_f32_16x16x32_bf16 a[56:59],a[240:243],v[168:171],0
v_exp_f32 v217,v217
v_mfma_f32_16x16x32_bf16 a[56:59],a[244:247],v[172:175],a[56:59]
ds_read_b128 a[184:187],v1 offset:12800
v_cvt_pk_bf16_f32 v204,v216,v217
v_mfma_f32_16x16x32_bf16 a[60:63],a[252:255],v[172:175],0
v_exp_f32 v218,v218
v_mfma_f32_16x16x32_bf16 a[60:63],a[248:251],v[168:171],a[60:63]
ds_read_b128 a[180:183],v1 offset:12544
v_mov_b32 v63,0
v_smfmac_f32_16x16x64_bf16 v[52:55],v[28:31],v[168:175],v27
s_waitcnt lgkmcnt(4)
v_add_u32 v2,s48,v2
v_mfma_f32_16x16x32_bf16 v[136:139],a[128:131],v[64:67],v[32:35]
buffer_load_dwordx4 v5,s[20:23],s47 offen offset:2048 nt lds
v_mfma_f32_16x16x32_bf16 v[136:139],a[132:135],v[68:71],v[136:139]
ds_read_b128 a[176:179],v1 offset:12288
v_add_u32 v3,s48,v3
v_mfma_f32_16x16x32_bf16 v[136:139],a[136:139],v[72:75],v[136:139]
s_mov_b32 s48,32768
v_exp_f32 v219,v219
v_mfma_f32_16x16x32_bf16 v[136:139],a[140:143],v[76:79],v[136:139]
v_exp_f32 v220,v220
v_mfma_f32_16x16x32_bf16 v[140:143],a[156:159],v[76:79],v[32:35]
v_exp_f32 v221,v221
v_mfma_f32_16x16x32_bf16 v[140:143],a[152:155],v[72:75],v[140:143]
v_exp_f32 v222,v222
v_mfma_f32_16x16x32_bf16 v[140:143],a[148:151],v[68:71],v[140:143]
v_exp_f32 v223,v223
v_mfma_f32_16x16x32_bf16 v[140:143],a[144:147],v[64:67],v[140:143]
v_cvt_pk_bf16_f32 v205,v218,v219
v_cvt_pk_bf16_f32 v206,v220,v221
v_mfma_f32_16x16x32_bf16 v[152:155],a[160:163],v[64:67],v[32:35]
v_cvt_pk_bf16_f32 v207,v222,v223
v_add_u32 v1,s48,v1
v_mfma_f32_16x16x32_bf16 v[152:155],a[164:167],v[68:71],v[152:155]
v_exp_f32 v232,v232
v_mfma_f32_16x16x32_bf16 v[152:155],a[168:171],v[72:75],v[152:155]
s_waitcnt lgkmcnt(0)
v_exp_f32 v233,v233
v_mfma_f32_16x16x32_bf16 v[152:155],a[172:175],v[76:79],v[152:155]
v_exp_f32 v234,v234
v_mfma_f32_16x16x32_bf16 v[156:159],a[188:191],v[76:79],v[32:35]
v_exp_f32 v235,v235
v_mfma_f32_16x16x32_bf16 v[156:159],a[184:187],v[72:75],v[156:159]
buffer_load_dwordx4 v5,s[20:23],s47 offen offset:3072 nt lds
)A"
R"A(v_mfma_f32_16x16x32_bf16 v[156:159],a[180:183],v[68:71],v[156:159]
v_exp_f32 v236,v236
v_mfma_f32_16x16x32_bf16 v[156:159],a[176:179],v[64:67],v[156:159]
v_exp_f32 v237,v237
v_mfma_f32_16x16x32_bf16 a[64:67],a[192:195],v[200:203],0
v_exp_f32 v238,v238
v_mfma_f32_16x16x32_bf16 a[64:67],a[196:199],v[204:207],a[64:67]
v_exp_f32 v239,v239
v_mfma_f32_16x16x32_bf16 a[68:71],a[204:207],v[204:207],0
v_exp_f32 v248,v248
v_mfma_f32_16x16x32_bf16 a[68:71],a[200:203],v[200:203],a[68:71]
v_exp_f32 v249,v249
v_mfma_f32_16x16x32_bf16 a[72:75],a[208:211],v[200:203],0
v_exp_f32 v250,v250
v_mfma_f32_16x16x32_bf16 a[72:75],a[212:215],v[204:207],a[72:75]
v_exp_f32 v251,v251
v_mfma_f32_16x16x32_bf16 a[76:79],a[220:223],v[204:207],0
v_exp_f32 v252,v252
v_mfma_f32_16x16x32_bf16 a[76:79],a[216:219],v[200:203],a[76:79]
v_exp_f32 v253,v253
v_mfma_f32_16x16x32_bf16 a[80:83],a[224:227],v[200:203],0
s_add_u32 m0,65536,s52
v_exp_f32 v254,v254
v_mfma_f32_16x16x32_bf16 a[80:83],a[228:231],v[204:207],a[80:83]
v_exp_f32 v255,v255
v_mfma_f32_16x16x32_bf16 a[84:87],a[236:239],v[204:207],0
v_exp_f32 v136,v136
v_mfma_f32_16x16x32_bf16 a[84:87],a[232:235],v[200:203],a[84:87]
buffer_load_dwordx4 v4,s[24:27],s47 offen nt lds
v_mfma_f32_16x16x32_bf16 a[88:91],a[240:243],v[200:203],0
v_cvt_pk_bf16_f32 v232,v232,v233
v_cvt_pk_bf16_f32 v233,v234,v235
v_mfma_f32_16x16x32_bf16 a[88:91],a[244:247],v[204:207],a[88:91]
v_cvt_pk_bf16_f32 v234,v236,v237
v_cvt_pk_bf16_f32 v235,v238,v239
v_mfma_f32_16x16x32_bf16 a[92:95],a[252:255],v[204:207],0
v_cvt_pk_bf16_f32 v236,v248,v249
v_cvt_pk_bf16_f32 v237,v250,v251
v_mfma_f32_16x16x32_bf16 a[92:95],a[248:251],v[200:203],a[92:95]
v_cvt_pk_bf16_f32 v238,v252,v253
v_cvt_pk_bf16_f32 v239,v254,v255
v_smfmac_f32_16x16x64_bf16 v[56:59],v[28:31],v[200:207],v27
v_mfma_f32_16x16x32_bf16 v[168:171],a[128:131],v[80:83],v[36:39]
v_exp_f32 v137,v137
v_mfma_f32_16x16x32_bf16 v[168:171],a[132:135],v[84:87],v[168:171]
v_exp_f32 v138,v138
v_mfma_f32_16x16x32_bf16 v[168:171],a[136:139],v[88:91],v[168:171]
v_exp_f32 v139,v139
v_mfma_f32_16x16x32_bf16 v[168:171],a[140:143],v[92:95],v[168:171]
v_exp_f32 v140,v140
v_mfma_f32_16x16x32_bf16 v[172:175],a[156:159],v[92:95],v[36:39]
v_exp_f32 v141,v141
v_mfma_f32_16x16x32_bf16 v[172:175],a[152:155],v[88:91],v[172:175]
v_exp_f32 v142,v142
v_mfma_f32_16x16x32_bf16 v[172:175],a[148:151],v[84:87],v[172:175]
v_exp_f32 v143,v143
v_mfma_f32_16x16x32_bf16 v[172:175],a[144:147],v[80:83],v[172:175]
v_exp_f32 v152,v152
v_mfma_f32_16x16x32_bf16 v[184:187],a[160:163],v[80:83],v[36:39]
buffer_load_dwordx4 v5,s[24:27],s47 offen offset:1024 nt lds
v_mfma_f32_16x16x32_bf16 v[184:187],a[164:167],v[84:87],v[184:187]
v_exp_f32 v153,v153
v_mfma_f32_16x16x32_bf16 v[184:187],a[168:171],v[88:91],v[184:187]
v_exp_f32 v154,v154
v_mfma_f32_16x16x32_bf16 v[184:187],a[172:175],v[92:95],v[184:187]
v_exp_f32 v155,v155
v_mfma_f32_16x16x32_bf16 v[188:191],a[188:191],v[92:95],v[36:39]
v_exp_f32 v156,v156
v_mfma_f32_16x16x32_bf16 v[188:191],a[184:187],v[88:91],v[188:191]
v_exp_f32 v157,v157
v_mfma_f32_16x16x32_bf16 v[188:191],a[180:183],v[84:87],v[188:191]
v_exp_f32 v158,v158
v_mfma_f32_16x16x32_bf16 v[188:191],a[176:179],v[80:83],v[188:191]
v_exp_f32 v159,v159
v_mfma_f32_16x16x32_bf16 a[96:99],a[192:195],v[232:235],0
ds_read_b64_tr_b16 a[192:193],v2
ds_read_b64_tr_b16 a[194:195],v2 offset:4096
v_mfma_f32_16x16x32_bf16 a[96:99],a[196:199],v[236:239],a[96:99]
ds_read_b64_tr_b16 a[196:197],v2 offset:8192
v_cvt_pk_bf16_f32 v136,v136,v137
v_mfma_f32_16x16x32_bf16 a[100:103],a[204:207],v[236:239],0
ds_read_b64_tr_b16 a[198:199],v2 offset:12288
v_cvt_pk_bf16_f32 v137,v138,v139
v_mfma_f32_16x16x32_bf16 a[100:103],a[200:203],v[232:235],a[100:103]
ds_read_b64_tr_b16 a[204:205],v3 offset:8192
v_cvt_pk_bf16_f32 v138,v140,v141
v_mfma_f32_16x16x32_bf16 a[104:107],a[208:211],v[232:235],0
ds_read_b64_tr_b16 a[206:207],v3 offset:12288
ds_read_b64_tr_b16 a[200:201],v3
v_mfma_f32_16x16x32_bf16 a[104:107],a[212:215],v[236:239],a[104:107]
ds_read_b64_tr_b16 a[202:203],v3 offset:4096
v_cvt_pk_bf16_f32 v139,v142,v143
v_mfma_f32_16x16x32_bf16 a[108:111],a[220:223],v[236:239],0
buffer_load_dwordx4 v4,s[24:27],s47 offen offset:2048 nt lds
v_mfma_f32_16x16x32_bf16 a[108:111],a[216:219],v[232:235],a[108:111]
ds_read_b64_tr_b16 a[208:209],v2 offset:256
ds_read_b64_tr_b16 a[210:211],v2 offset:4352
v_mfma_f32_16x16x32_bf16 a[112:115],a[224:227],v[232:235],0
ds_read_b64_tr_b16 a[212:213],v2 offset:8448
v_cvt_pk_bf16_f32 v140,v152,v153
v_mfma_f32_16x16x32_bf16 a[112:115],a[228:231],v[236:239],a[112:115]
ds_read_b64_tr_b16 a[214:215],v2 offset:12544
v_cvt_pk_bf16_f32 v141,v154,v155
v_mfma_f32_16x16x32_bf16 a[116:119],a[236:239],v[236:239],0
ds_read_b64_tr_b16 a[220:221],v3 offset:8448
v_cvt_pk_bf16_f32 v142,v156,v157
v_mfma_f32_16x16x32_bf16 a[116:119],a[232:235],v[232:235],a[116:119]
ds_read_b64_tr_b16 a[222:223],v3 offset:12544
v_cvt_pk_bf16_f32 v143,v158,v159
v_mfma_f32_16x16x32_bf16 a[120:123],a[240:243],v[232:235],0
ds_read_b64_tr_b16 a[216:217],v3 offset:256
ds_read_b64_tr_b16 a[218:219],v3 offset:4352
v_mfma_f32_16x16x32_bf16 a[120:123],a[244:247],v[236:239],a[120:123]
v_exp_f32 v168,v168
v_mfma_f32_16x16x32_bf16 a[124:127],a[252:255],v[236:239],0
ds_read_b64_tr_b16 a[224:225],v2 offset:512
ds_read_b64_tr_b16 a[226:227],v2 offset:4608
v_mfma_f32_16x16x32_bf16 a[124:127],a[248:251],v[232:235],a[124:127]
v_exp_f32 v169,v169
v_smfmac_f32_16x16x64_bf16 v[60:63],v[28:31],v[232:239],v27
ds_read_b64_tr_b16 a[228:229],v2 offset:8704
v_mfma_f32_16x16x32_bf16 v[200:203],a[128:131],v[96:99],v[40:43]
ds_read_b64_tr_b16 a[230:231],v2 offset:12800
ds_read_b64_tr_b16 a[236:237],v3 offset:8704
v_mfma_f32_16x16x32_bf16 v[200:203],a[132:135],v[100:103],v[200:203]
ds_read_b64_tr_b16 a[238:239],v3 offset:12800
v_cvt_pk_bf16_f32 v168,v168,v169
v_mfma_f32_16x16x32_bf16 v[200:203],a[136:139],v[104:107],v[200:203]
ds_read_b64_tr_b16 a[232:233],v3 offset:512
v_mfma_f32_16x16x32_bf16 v[200:203],a[140:143],v[108:111],v[200:203]
buffer_load_dwordx4 v5,s[24:27],s47 offen offset:3072 nt lds
v_mfma_f32_16x16x32_bf16 v[204:207],a[156:159],v[108:111],v[40:43]
s_or_b32 s47,s47,0x18000
ds_read_b64_tr_b16 a[234:235],v3 offset:4608
ds_read_b64_tr_b16 a[240:241],v2 offset:768
v_mfma_f32_16x16x32_bf16 v[204:207],a[152:155],v[104:107],v[204:207]
s_add_u32 s47,s47,4096
s_and_b32 s47,s47,0x27000
ds_read_b64_tr_b16 a[242:243],v2 offset:4864
v_mfma_f32_16x16x32_bf16 v[204:207],a[148:151],v[100:103],v[204:207]
s_or_b32 s47,s47,s46
ds_read_b64_tr_b16 a[244:245],v2 offset:8960
v_mfma_f32_16x16x32_bf16 v[204:207],a[144:147],v[96:99],v[204:207]
ds_read_b64_tr_b16 a[246:247],v2 offset:13056
v_mfma_f32_16x16x32_bf16 v[216:219],a[160:163],v[96:99],v[40:43]
ds_read_b64_tr_b16 a[252:253],v3 offset:8960
ds_read_b64_tr_b16 a[254:255],v3 offset:13056
v_mfma_f32_16x16x32_bf16 v[216:219],a[164:167],v[100:103],v[216:219]
ds_read_b64_tr_b16 a[248:249],v3 offset:768
v_mfma_f32_16x16x32_bf16 v[216:219],a[168:171],v[104:107],v[216:219]
ds_read_b64_tr_b16 a[250:251],v3 offset:4864
v_mfma_f32_16x16x32_bf16 v[216:219],a[172:175],v[108:111],v[216:219]
v_exp_f32 v170,v170
v_mfma_f32_16x16x32_bf16 v[220:223],a[188:191],v[108:111],v[40:43]
v_exp_f32 v171,v171
v_mfma_f32_16x16x32_bf16 v[220:223],a[184:187],v[104:107],v[220:223]
v_exp_f32 v172,v172
v_mfma_f32_16x16x32_bf16 v[220:223],a[180:183],v[100:103],v[220:223]
v_exp_f32 v173,v173
v_mfma_f32_16x16x32_bf16 v[220:223],a[176:179],v[96:99],v[220:223]
s_waitcnt lgkmcnt(4)
v_exp_f32 v174,v174
v_mfma_f32_16x16x32_bf16 a[0:3],a[192:195],v[136:139],a[0:3]
v_exp_f32 v175,v175
v_mfma_f32_16x16x32_bf16 a[0:3],a[196:199],v[140:143],a[0:3]
v_cvt_pk_bf16_f32 v169,v170,v171
v_cvt_pk_bf16_f32 v170,v172,v173
v_mfma_f32_16x16x32_bf16 a[4:7],a[204:207],v[140:143],a[4:7]
v_exp_f32 v184,v184
v_mfma_f32_16x16x32_bf16 a[4:7],a[200:203],v[136:139],a[4:7]
s_memrealtime s[76:77]
v_exp_f32 v185,v185
v_mfma_f32_16x16x32_bf16 a[8:11],a[208:211],v[136:139],a[8:11]
v_exp_f32 v186,v186
v_mfma_f32_16x16x32_bf16 a[8:11],a[212:215],v[140:143],a[8:11]
v_exp_f32 v187,v187
v_mfma_f32_16x16x32_bf16 a[12:15],a[220:223],v[140:143],a[12:15]
v_exp_f32 v188,v188
v_mfma_f32_16x16x32_bf16 a[12:15],a[216:219],v[136:139],a[12:15]
v_exp_f32 v189,v189
v_mfma_f32_16x16x32_bf16 a[16:19],a[224:227],v[136:139],a[16:19]
v_exp_f32 v190,v190
v_mfma_f32_16x16x32_bf16 a[16:19],a[228:231],v[140:143],a[16:19]
v_exp_f32 v191,v191
v_mfma_f32_16x16x32_bf16 a[20:23],a[236:239],v[140:143],a[20:23]
s_waitcnt lgkmcnt(0)
v_cvt_pk_bf16_f32 v171,v174,v175
v_cvt_pk_bf16_f32 v172,v184,v185
v_mfma_f32_16x16x32_bf16 a[20:23],a[232:235],v[136:139],a[20:23]
s_sub_u32 s79,s78,s76
v_cvt_pk_bf16_f32 v173,v186,v187
v_cvt_pk_bf16_f32 v174,v188,v189
v_mfma_f32_16x16x32_bf16 a[24:27],a[240:243],v[136:139],a[24:27]
s_add_u32 s78,s76,s60
v_cvt_pk_bf16_f32 v175,v190,v191
v_mfma_f32_16x16x32_bf16 a[24:27],a[244:247],v[140:143],a[24:27]
s_cmp_gt_i32 s79,s95
s_cbranch_scc1 L_ps2_%=
L_pr2_%=:
v_mfma_f32_16x16x32_bf16 a[28:31],a[252:255],v[140:143],a[28:31]
v_mfma_f32_16x16x32_bf16 a[28:31],a[248:251],v[136:139],a[28:31]
v_smfmac_f32_16x16x64_bf16 v[48:51],v[28:31],v[136:143],v27
s_mov_b32 s53,13
s_cmp_eq_u32 s40,3
s_cbranch_scc0 L_att0_%=
s_cmp_gt_i32 s18,0
s_cbranch_scc1 L_att0v0_%=
s_cmp_gt_i32 s19,0
s_cbranch_scc1 L_att0v1_%=
s_cmp_gt_i32 s100,0
s_cbranch_scc1 L_att0v2_%=
.p2align 5
L_att0v3_%=:
s_waitcnt vmcnt(8) lgkmcnt(0)
v_exp_f32 v200,v200
s_barrier
s_add_u32 m0,s49,s51
ds_read_b128 a[128:131],v1
s_mov_b32 s90,L_endseg_%=-L_pc_%=+128
v_exp_f32 v201,v201
s_lshr_b32 s45,s45,1
ds_read_b128 a[132:135],v1 offset:256
v_cvt_pk_bf16_f32 v200,v200,v201
buffer_load_dwordx4 v4,s[20:23],s47 offen nt lds
ds_read_b128 a[136:139],v1 offset:512
v_add_u32 v2,s48,v2
v_exp_f32 v202,v202
s_add_u32 s62,s50,32768
ds_read_b128 a[140:143],v1 offset:768
v_add_u32 v3,s48,v3
s_and_b32 s62,s62,131071
v_exp_f32 v203,v203
s_add_u32 s54,s54,16384
ds_read_b128 a[156:159],v1 offset:4864
v_cvt_pk_bf16_f32 v201,v202,v203
s_sub_u32 s48,s62,s50
v_exp_f32 v204,v204
s_sub_u32 s53,s53,1
v_exp_f32 v205,v205
s_and_b32 s46,s54,0xfffc0000
ds_read_b128 a[152:155],v1 offset:4608
v_cvt_pk_bf16_f32 v202,v204,v205
s_mov_b32 s50,s62
v_exp_f32 v206,v206
v_exp_f32 v207,v207
ds_read_b128 a[148:151],v1 offset:4352
v_cvt_pk_bf16_f32 v203,v206,v207
v_mfma_f32_16x16x32_bf16 a[32:35],a[192:195],v[168:171],a[32:35]
v_exp_f32 v216,v216
v_mfma_f32_16x16x32_bf16 a[32:35],a[196:199],v[172:175],a[32:35]
ds_read_b128 a[144:147],v1 offset:4096
v_mfma_f32_16x16x32_bf16 a[36:39],a[204:207],v[172:175],a[36:39]
buffer_load_dwordx4 v4,s[20:23],s47 offen offset:1024 nt lds
v_mfma_f32_16x16x32_bf16 a[36:39],a[200:203],v[168:171],a[36:39]
ds_read_b128 a[160:163],v1 offset:8192
v_mfma_f32_16x16x32_bf16 a[40:43],a[208:211],v[168:171],a[40:43]
v_exp_f32 v217,v217
v_mfma_f32_16x16x32_bf16 a[40:43],a[212:215],v[172:175],a[40:43]
ds_read_b128 a[164:167],v1 offset:8448
v_cvt_pk_bf16_f32 v204,v216,v217
v_mfma_f32_16x16x32_bf16 a[44:47],a[220:223],v[172:175],a[44:47]
v_exp_f32 v218,v218
v_mfma_f32_16x16x32_bf16 a[44:47],a[216:219],v[168:171],a[44:47]
ds_read_b128 a[168:171],v1 offset:8704
v_mfma_f32_16x16x32_bf16 a[48:51],a[224:227],v[168:171],a[48:51]
v_exp_f32 v219,v219
v_mfma_f32_16x16x32_bf16 a[48:51],a[228:231],v[172:175],a[48:51]
ds_read_b128 a[172:175],v1 offset:8960
v_cvt_pk_bf16_f32 v205,v218,v219
v_mfma_f32_16x16x32_bf16 a[52:55],a[236:239],v[172:175],a[52:55]
v_exp_f32 v220,v220
v_mfma_f32_16x16x32_bf16 a[52:55],a[232:235],v[168:171],a[52:55]
ds_read_b128 a[188:191],v1 offset:13056
v_mfma_f32_16x16x32_bf16 a[56:59],a[240:243],v[168:171],a[56:59]
v_exp_f32 v221,v221
v_mfma_f32_16x16x32_bf16 a[56:59],a[244:247],v[172:175],a[56:59]
ds_read_b128 a[184:187],v1 offset:12800
v_cvt_pk_bf16_f32 v206,v220,v221
v_mfma_f32_16x16x32_bf16 a[60:63],a[252:255],v[172:175],a[60:63]
v_exp_f32 v222,v222
v_mfma_f32_16x16x32_bf16 a[60:63],a[248:251],v[168:171],a[60:63]
ds_read_b128 a[180:183],v1 offset:12544
v_smfmac_f32_16x16x64_bf16 v[52:55],v[28:31],v[168:175],v27
s_waitcnt lgkmcnt(4)
v_mfma_f32_16x16x32_bf16 v[136:139],a[128:131],v[64:67],v[32:35]
)A"
R"A(buffer_load_dwordx4 v5,s[20:23],s47 offen offset:2048 nt lds
v_mfma_f32_16x16x32_bf16 v[136:139],a[132:135],v[68:71],v[136:139]
ds_read_b128 a[176:179],v1 offset:12288
v_add_u32 v1,s48,v1
v_mfma_f32_16x16x32_bf16 v[136:139],a[136:139],v[72:75],v[136:139]
v_exp_f32 v223,v223
v_mfma_f32_16x16x32_bf16 v[136:139],a[140:143],v[76:79],v[136:139]
v_cvt_pk_bf16_f32 v207,v222,v223
v_mfma_f32_16x16x32_bf16 v[140:143],a[156:159],v[76:79],v[32:35]
v_mfma_f32_16x16x32_bf16 v[140:143],a[152:155],v[72:75],v[140:143]
v_mfma_f32_16x16x32_bf16 v[140:143],a[148:151],v[68:71],v[140:143]
v_mfma_f32_16x16x32_bf16 v[140:143],a[144:147],v[64:67],v[140:143]
v_mfma_f32_16x16x32_bf16 v[152:155],a[160:163],v[64:67],v[32:35]
v_mfma_f32_16x16x32_bf16 v[152:155],a[164:167],v[68:71],v[152:155]
v_mfma_f32_16x16x32_bf16 v[152:155],a[168:171],v[72:75],v[152:155]
s_waitcnt lgkmcnt(0)
v_mfma_f32_16x16x32_bf16 v[152:155],a[172:175],v[76:79],v[152:155]
v_mfma_f32_16x16x32_bf16 v[156:159],a[188:191],v[76:79],v[32:35]
v_mfma_f32_16x16x32_bf16 v[156:159],a[184:187],v[72:75],v[156:159]
buffer_load_dwordx4 v5,s[20:23],s47 offen offset:3072 nt lds
v_mfma_f32_16x16x32_bf16 v[156:159],a[180:183],v[68:71],v[156:159]
v_mfma_f32_16x16x32_bf16 v[156:159],a[176:179],v[64:67],v[156:159]
v_mfma_f32_16x16x32_bf16 a[64:67],a[192:195],v[200:203],a[64:67]
v_mfma_f32_16x16x32_bf16 a[64:67],a[196:199],v[204:207],a[64:67]
v_mfma_f32_16x16x32_bf16 a[68:71],a[204:207],v[204:207],a[68:71]
v_mfma_f32_16x16x32_bf16 a[68:71],a[200:203],v[200:203],a[68:71]
v_mfma_f32_16x16x32_bf16 a[72:75],a[208:211],v[200:203],a[72:75]
v_mfma_f32_16x16x32_bf16 a[72:75],a[212:215],v[204:207],a[72:75]
v_exp_f32 v136,v136
v_mfma_f32_16x16x32_bf16 a[76:79],a[220:223],v[204:207],a[76:79]
v_exp_f32 v137,v137
v_mfma_f32_16x16x32_bf16 a[76:79],a[216:219],v[200:203],a[76:79]
v_exp_f32 v138,v138
v_mfma_f32_16x16x32_bf16 a[80:83],a[224:227],v[200:203],a[80:83]
s_add_u32 m0,s49,s52
v_mfma_f32_16x16x32_bf16 a[80:83],a[228:231],v[204:207],a[80:83]
s_xor_b32 s49,s50,65536
v_mfma_f32_16x16x32_bf16 a[84:87],a[236:239],v[204:207],a[84:87]
v_mfma_f32_16x16x32_bf16 a[84:87],a[232:235],v[200:203],a[84:87]
buffer_load_dwordx4 v4,s[24:27],s47 offen nt lds
v_mfma_f32_16x16x32_bf16 a[88:91],a[240:243],v[200:203],a[88:91]
v_mfma_f32_16x16x32_bf16 a[88:91],a[244:247],v[204:207],a[88:91]
v_exp_f32 v139,v139
v_mfma_f32_16x16x32_bf16 a[92:95],a[252:255],v[204:207],a[92:95]
v_exp_f32 v140,v140
v_mfma_f32_16x16x32_bf16 a[92:95],a[248:251],v[200:203],a[92:95]
v_exp_f32 v141,v141
v_smfmac_f32_16x16x64_bf16 v[56:59],v[28:31],v[200:207],v27
v_mfma_f32_16x16x32_bf16 v[168:171],a[128:131],v[80:83],v[36:39]
v_exp_f32 v142,v142
v_mfma_f32_16x16x32_bf16 v[168:171],a[132:135],v[84:87],v[168:171]
v_exp_f32 v143,v143
v_mfma_f32_16x16x32_bf16 v[168:171],a[136:139],v[88:91],v[168:171]
v_exp_f32 v152,v152
v_mfma_f32_16x16x32_bf16 v[168:171],a[140:143],v[92:95],v[168:171]
v_exp_f32 v153,v153
v_mfma_f32_16x16x32_bf16 v[172:175],a[156:159],v[92:95],v[36:39]
v_exp_f32 v154,v154
v_mfma_f32_16x16x32_bf16 v[172:175],a[152:155],v[88:91],v[172:175]
v_exp_f32 v155,v155
v_mfma_f32_16x16x32_bf16 v[172:175],a[148:151],v[84:87],v[172:175]
v_exp_f32 v156,v156
v_mfma_f32_16x16x32_bf16 v[172:175],a[144:147],v[80:83],v[172:175]
v_exp_f32 v157,v157
v_mfma_f32_16x16x32_bf16 v[184:187],a[160:163],v[80:83],v[36:39]
buffer_load_dwordx4 v5,s[24:27],s47 offen offset:1024 nt lds
v_mfma_f32_16x16x32_bf16 v[184:187],a[164:167],v[84:87],v[184:187]
v_exp_f32 v158,v158
v_mfma_f32_16x16x32_bf16 v[184:187],a[168:171],v[88:91],v[184:187]
v_exp_f32 v159,v159
v_mfma_f32_16x16x32_bf16 v[184:187],a[172:175],v[92:95],v[184:187]
v_exp_f32 v168,v168
v_mfma_f32_16x16x32_bf16 v[188:191],a[188:191],v[92:95],v[36:39]
v_exp_f32 v169,v169
v_mfma_f32_16x16x32_bf16 v[188:191],a[184:187],v[88:91],v[188:191]
v_exp_f32 v170,v170
v_mfma_f32_16x16x32_bf16 v[188:191],a[180:183],v[84:87],v[188:191]
v_exp_f32 v171,v171
v_mfma_f32_16x16x32_bf16 v[188:191],a[176:179],v[80:83],v[188:191]
v_exp_f32 v172,v172
ds_read_b64_tr_b16 a[192:193],v2
v_cvt_pk_bf16_f32 v168,v168,v169
ds_read_b64_tr_b16 a[194:195],v2 offset:4096
v_cvt_pk_bf16_f32 v169,v170,v171
v_exp_f32 v173,v173
ds_read_b64_tr_b16 a[196:197],v2 offset:8192
v_cvt_pk_bf16_f32 v170,v172,v173
v_exp_f32 v174,v174
v_exp_f32 v175,v175
buffer_load_dwordx4 v4,s[24:27],s47 offen offset:2048 nt lds
ds_read_b64_tr_b16 a[198:199],v2 offset:12288
v_cvt_pk_bf16_f32 v171,v174,v175
v_exp_f32 v184,v184
v_exp_f32 v185,v185
ds_read_b64_tr_b16 a[204:205],v3 offset:8192
v_cvt_pk_bf16_f32 v136,v136,v137
ds_read_b64_tr_b16 a[206:207],v3 offset:12288
v_cvt_pk_bf16_f32 v172,v184,v185
ds_read_b64_tr_b16 a[200:201],v3
ds_read_b64_tr_b16 a[202:203],v3 offset:4096
v_cvt_pk_bf16_f32 v137,v138,v139
ds_read_b64_tr_b16 a[208:209],v2 offset:256
ds_read_b64_tr_b16 a[210:211],v2 offset:4352
ds_read_b64_tr_b16 a[212:213],v2 offset:8448
v_mfma_f32_16x16x32_bf16 v[200:203],a[128:131],v[96:99],v[40:43]
ds_read_b64_tr_b16 a[214:215],v2 offset:12544
v_cvt_pk_bf16_f32 v138,v140,v141
v_mfma_f32_16x16x32_bf16 v[200:203],a[132:135],v[100:103],v[200:203]
ds_read_b64_tr_b16 a[220:221],v3 offset:8448
v_mfma_f32_16x16x32_bf16 v[200:203],a[136:139],v[104:107],v[200:203]
ds_read_b64_tr_b16 a[222:223],v3 offset:12544
v_cvt_pk_bf16_f32 v139,v142,v143
v_mfma_f32_16x16x32_bf16 v[200:203],a[140:143],v[108:111],v[200:203]
buffer_load_dwordx4 v5,s[24:27],s47 offen offset:3072 nt lds
v_mfma_f32_16x16x32_bf16 v[204:207],a[156:159],v[108:111],v[40:43]
s_or_b32 s47,s47,0x18000
s_add_u32 s47,s47,4096
ds_read_b64_tr_b16 a[216:217],v3 offset:256
v_mfma_f32_16x16x32_bf16 v[204:207],a[152:155],v[104:107],v[204:207]
s_and_b32 s47,s47,0x27000
ds_read_b64_tr_b16 a[218:219],v3 offset:4352
v_cvt_pk_bf16_f32 v140,v152,v153
v_mfma_f32_16x16x32_bf16 v[204:207],a[148:151],v[100:103],v[204:207]
s_or_b32 s47,s47,s46
ds_read_b64_tr_b16 a[224:225],v2 offset:512
v_mfma_f32_16x16x32_bf16 v[204:207],a[144:147],v[96:99],v[204:207]
ds_read_b64_tr_b16 a[226:227],v2 offset:4608
v_mfma_f32_16x16x32_bf16 v[216:219],a[160:163],v[96:99],v[40:43]
ds_read_b64_tr_b16 a[228:229],v2 offset:8704
v_cvt_pk_bf16_f32 v141,v154,v155
v_mfma_f32_16x16x32_bf16 v[216:219],a[164:167],v[100:103],v[216:219]
ds_read_b64_tr_b16 a[230:231],v2 offset:12800
v_mfma_f32_16x16x32_bf16 v[216:219],a[168:171],v[104:107],v[216:219]
ds_read_b64_tr_b16 a[236:237],v3 offset:8704
v_mfma_f32_16x16x32_bf16 v[216:219],a[172:175],v[108:111],v[216:219]
ds_read_b64_tr_b16 a[238:239],v3 offset:12800
v_cvt_pk_bf16_f32 v142,v156,v157
v_mfma_f32_16x16x32_bf16 v[220:223],a[188:191],v[108:111],v[40:43]
ds_read_b64_tr_b16 a[232:233],v3 offset:512
v_mfma_f32_16x16x32_bf16 v[220:223],a[184:187],v[104:107],v[220:223]
ds_read_b64_tr_b16 a[234:235],v3 offset:4608
v_mfma_f32_16x16x32_bf16 v[220:223],a[180:183],v[100:103],v[220:223]
ds_read_b64_tr_b16 a[240:241],v2 offset:768
v_cvt_pk_bf16_f32 v143,v158,v159
v_mfma_f32_16x16x32_bf16 v[220:223],a[176:179],v[96:99],v[220:223]
s_waitcnt lgkmcnt(7)
buffer_load_dword v26,v24,s[88:91],s93 offen
v_mfma_f32_16x16x32_bf16 a[0:3],a[192:195],v[136:139],a[0:3]
ds_read_b64_tr_b16 a[242:243],v2 offset:4864
v_mfma_f32_16x16x32_bf16 a[0:3],a[196:199],v[140:143],a[0:3]
ds_read_b64_tr_b16 a[244:245],v2 offset:8960
v_mfma_f32_16x16x32_bf16 a[4:7],a[204:207],v[140:143],a[4:7]
ds_read_b64_tr_b16 a[246:247],v2 offset:13056
v_mfma_f32_16x16x32_bf16 a[4:7],a[200:203],v[136:139],a[4:7]
ds_read_b64_tr_b16 a[252:253],v3 offset:8960
v_mfma_f32_16x16x32_bf16 a[8:11],a[208:211],v[136:139],a[8:11]
ds_read_b64_tr_b16 a[254:255],v3 offset:13056
v_mfma_f32_16x16x32_bf16 a[8:11],a[212:215],v[140:143],a[8:11]
ds_read_b64_tr_b16 a[248:249],v3 offset:768
v_mfma_f32_16x16x32_bf16 a[12:15],a[220:223],v[140:143],a[12:15]
ds_read_b64_tr_b16 a[250:251],v3 offset:4864
v_mfma_f32_16x16x32_bf16 a[12:15],a[216:219],v[136:139],a[12:15]
s_bitcmp1_b32 s45,0
s_cbranch_scc0 L_t31v30_%=
s_mov_b32 m0,s80
s_add_u32 s80,s80,4096
buffer_load_dwordx4 v25,s[20:23],s83 offen nt lds
s_add_u32 s83,s83,256
L_t31v30_%=:
v_mfma_f32_16x16x32_bf16 a[16:19],a[224:227],v[136:139],a[16:19]
s_waitcnt lgkmcnt(6)
v_exp_f32 v186,v186
v_mfma_f32_16x16x32_bf16 a[16:19],a[228:231],v[140:143],a[16:19]
v_exp_f32 v187,v187
v_mfma_f32_16x16x32_bf16 a[20:23],a[236:239],v[140:143],a[20:23]
v_exp_f32 v188,v188
v_mfma_f32_16x16x32_bf16 a[20:23],a[232:235],v[136:139],a[20:23]
v_exp_f32 v189,v189
v_mfma_f32_16x16x32_bf16 a[24:27],a[240:243],v[136:139],a[24:27]
s_waitcnt lgkmcnt(2)
v_exp_f32 v190,v190
v_mfma_f32_16x16x32_bf16 a[24:27],a[244:247],v[140:143],a[24:27]
v_cvt_pk_bf16_f32 v173,v186,v187
v_mfma_f32_16x16x32_bf16 a[28:31],a[252:255],v[140:143],a[28:31]
s_waitcnt lgkmcnt(0)
v_exp_f32 v191,v191
v_mfma_f32_16x16x32_bf16 a[28:31],a[248:251],v[136:139],a[28:31]
v_cvt_pk_bf16_f32 v174,v188,v189
v_cvt_pk_bf16_f32 v175,v190,v191
s_cmp_lg_u32 s53,0
v_smfmac_f32_16x16x64_bf16 v[48:51],v[28:31],v[136:143],v27
s_cbranch_scc1 L_att0v3_%=
s_waitcnt lgkmcnt(0)
s_nop 7
s_branch L_att0x_%=
.p2align 5
L_att0v0_%=:
v_mfma_f32_16x16x32_bf16 v[232:235],a[128:131],v[112:115],v[44:47]
s_waitcnt vmcnt(8) lgkmcnt(0)
v_exp_f32 v200,v200
v_mfma_f32_16x16x32_bf16 v[232:235],a[132:135],v[116:119],v[232:235]
s_barrier
s_add_u32 m0,s49,s51
ds_read_b128 a[128:131],v1
v_mfma_f32_16x16x32_bf16 v[232:235],a[136:139],v[120:123],v[232:235]
s_mov_b32 s90,L_endseg_%=-L_pc_%=+128
v_exp_f32 v201,v201
v_mfma_f32_16x16x32_bf16 v[232:235],a[140:143],v[124:127],v[232:235]
s_lshr_b32 s45,s45,1
ds_read_b128 a[132:135],v1 offset:256
v_cvt_pk_bf16_f32 v200,v200,v201
v_mfma_f32_16x16x32_bf16 v[236:239],a[156:159],v[124:127],v[44:47]
buffer_load_dwordx4 v4,s[20:23],s47 offen nt lds
v_mfma_f32_16x16x32_bf16 v[236:239],a[152:155],v[120:123],v[236:239]
ds_read_b128 a[136:139],v1 offset:512
v_add_u32 v2,s48,v2
v_mfma_f32_16x16x32_bf16 v[236:239],a[148:151],v[116:119],v[236:239]
v_exp_f32 v202,v202
v_mfma_f32_16x16x32_bf16 v[236:239],a[144:147],v[112:115],v[236:239]
s_add_u32 s62,s50,32768
ds_read_b128 a[140:143],v1 offset:768
v_add_u32 v3,s48,v3
v_mfma_f32_16x16x32_bf16 v[248:251],a[160:163],v[112:115],v[44:47]
s_and_b32 s62,s62,131071
v_exp_f32 v203,v203
v_mfma_f32_16x16x32_bf16 v[248:251],a[164:167],v[116:119],v[248:251]
s_add_u32 s54,s54,16384
ds_read_b128 a[156:159],v1 offset:4864
v_cvt_pk_bf16_f32 v201,v202,v203
v_mfma_f32_16x16x32_bf16 v[248:251],a[168:171],v[120:123],v[248:251]
s_sub_u32 s48,s62,s50
v_exp_f32 v204,v204
v_mfma_f32_16x16x32_bf16 v[248:251],a[172:175],v[124:127],v[248:251]
s_sub_u32 s53,s53,1
v_exp_f32 v205,v205
v_mfma_f32_16x16x32_bf16 v[252:255],a[188:191],v[124:127],v[44:47]
s_and_b32 s46,s54,0xfffc0000
ds_read_b128 a[152:155],v1 offset:4608
v_cvt_pk_bf16_f32 v202,v204,v205
v_mfma_f32_16x16x32_bf16 v[252:255],a[184:187],v[120:123],v[252:255]
s_mov_b32 s50,s62
v_exp_f32 v206,v206
v_mfma_f32_16x16x32_bf16 v[252:255],a[180:183],v[116:119],v[252:255]
v_exp_f32 v207,v207
v_mfma_f32_16x16x32_bf16 v[252:255],a[176:179],v[112:115],v[252:255]
ds_read_b128 a[148:151],v1 offset:4352
v_cvt_pk_bf16_f32 v203,v206,v207
v_mfma_f32_16x16x32_bf16 a[32:35],a[192:195],v[168:171],a[32:35]
v_exp_f32 v216,v216
v_mfma_f32_16x16x32_bf16 a[32:35],a[196:199],v[172:175],a[32:35]
ds_read_b128 a[144:147],v1 offset:4096
v_mfma_f32_16x16x32_bf16 a[36:39],a[204:207],v[172:175],a[36:39]
buffer_load_dwordx4 v4,s[20:23],s47 offen offset:1024 nt lds
v_mfma_f32_16x16x32_bf16 a[36:39],a[200:203],v[168:171],a[36:39]
ds_read_b128 a[160:163],v1 offset:8192
v_mfma_f32_16x16x32_bf16 a[40:43],a[208:211],v[168:171],a[40:43]
v_exp_f32 v217,v217
v_mfma_f32_16x16x32_bf16 a[40:43],a[212:215],v[172:175],a[40:43]
ds_read_b128 a[164:167],v1 offset:8448
v_cvt_pk_bf16_f32 v204,v216,v217
v_mfma_f32_16x16x32_bf16 a[44:47],a[220:223],v[172:175],a[44:47]
v_exp_f32 v218,v218
v_mfma_f32_16x16x32_bf16 a[44:47],a[216:219],v[168:171],a[44:47]
ds_read_b128 a[168:171],v1 offset:8704
v_mfma_f32_16x16x32_bf16 a[48:51],a[224:227],v[168:171],a[48:51]
v_exp_f32 v219,v219
v_mfma_f32_16x16x32_bf16 a[48:51],a[228:231],v[172:175],a[48:51]
ds_read_b128 a[172:175],v1 offset:8960
)A"
R"A(v_cvt_pk_bf16_f32 v205,v218,v219
v_mfma_f32_16x16x32_bf16 a[52:55],a[236:239],v[172:175],a[52:55]
v_exp_f32 v220,v220
v_mfma_f32_16x16x32_bf16 a[52:55],a[232:235],v[168:171],a[52:55]
ds_read_b128 a[188:191],v1 offset:13056
v_mfma_f32_16x16x32_bf16 a[56:59],a[240:243],v[168:171],a[56:59]
v_exp_f32 v221,v221
v_mfma_f32_16x16x32_bf16 a[56:59],a[244:247],v[172:175],a[56:59]
ds_read_b128 a[184:187],v1 offset:12800
v_cvt_pk_bf16_f32 v206,v220,v221
v_mfma_f32_16x16x32_bf16 a[60:63],a[252:255],v[172:175],a[60:63]
v_exp_f32 v222,v222
v_mfma_f32_16x16x32_bf16 a[60:63],a[248:251],v[168:171],a[60:63]
ds_read_b128 a[180:183],v1 offset:12544
v_smfmac_f32_16x16x64_bf16 v[52:55],v[28:31],v[168:175],v27
buffer_load_dwordx4 v5,s[20:23],s47 offen offset:2048 nt lds
ds_read_b128 a[176:179],v1 offset:12288
v_add_u32 v1,s48,v1
v_exp_f32 v223,v223
s_nop 0
v_cvt_pk_bf16_f32 v207,v222,v223
v_exp_f32 v232,v232
v_exp_f32 v233,v233
v_exp_f32 v234,v234
v_exp_f32 v235,v235
v_exp_f32 v236,v236
v_exp_f32 v237,v237
v_exp_f32 v238,v238
v_exp_f32 v239,v239
v_exp_f32 v248,v248
buffer_load_dwordx4 v5,s[20:23],s47 offen offset:3072 nt lds
v_exp_f32 v249,v249
v_exp_f32 v250,v250
v_mfma_f32_16x16x32_bf16 a[64:67],a[192:195],v[200:203],a[64:67]
v_exp_f32 v251,v251
v_mfma_f32_16x16x32_bf16 a[64:67],a[196:199],v[204:207],a[64:67]
v_exp_f32 v252,v252
v_mfma_f32_16x16x32_bf16 a[68:71],a[204:207],v[204:207],a[68:71]
v_exp_f32 v253,v253
v_mfma_f32_16x16x32_bf16 a[68:71],a[200:203],v[200:203],a[68:71]
v_exp_f32 v254,v254
v_mfma_f32_16x16x32_bf16 a[72:75],a[208:211],v[200:203],a[72:75]
v_exp_f32 v255,v255
v_mfma_f32_16x16x32_bf16 a[72:75],a[212:215],v[204:207],a[72:75]
v_mfma_f32_16x16x32_bf16 a[76:79],a[220:223],v[204:207],a[76:79]
v_mfma_f32_16x16x32_bf16 a[76:79],a[216:219],v[200:203],a[76:79]
v_mfma_f32_16x16x32_bf16 a[80:83],a[224:227],v[200:203],a[80:83]
s_add_u32 m0,s49,s52
v_cvt_pk_bf16_f32 v232,v232,v233
v_cvt_pk_bf16_f32 v233,v234,v235
v_mfma_f32_16x16x32_bf16 a[80:83],a[228:231],v[204:207],a[80:83]
s_xor_b32 s49,s50,65536
v_cvt_pk_bf16_f32 v234,v236,v237
v_cvt_pk_bf16_f32 v235,v238,v239
v_mfma_f32_16x16x32_bf16 a[84:87],a[236:239],v[204:207],a[84:87]
v_cvt_pk_bf16_f32 v236,v248,v249
v_cvt_pk_bf16_f32 v237,v250,v251
v_mfma_f32_16x16x32_bf16 a[84:87],a[232:235],v[200:203],a[84:87]
buffer_load_dwordx4 v4,s[24:27],s47 offen nt lds
v_mfma_f32_16x16x32_bf16 a[88:91],a[240:243],v[200:203],a[88:91]
v_cvt_pk_bf16_f32 v238,v252,v253
v_cvt_pk_bf16_f32 v239,v254,v255
v_mfma_f32_16x16x32_bf16 a[88:91],a[244:247],v[204:207],a[88:91]
v_mfma_f32_16x16x32_bf16 a[92:95],a[252:255],v[204:207],a[92:95]
v_mfma_f32_16x16x32_bf16 a[92:95],a[248:251],v[200:203],a[92:95]
v_smfmac_f32_16x16x64_bf16 v[56:59],v[28:31],v[200:207],v27
s_waitcnt lgkmcnt(0)
v_mfma_f32_16x16x32_bf16 v[168:171],a[128:131],v[80:83],v[36:39]
v_mfma_f32_16x16x32_bf16 v[168:171],a[132:135],v[84:87],v[168:171]
v_mfma_f32_16x16x32_bf16 v[168:171],a[136:139],v[88:91],v[168:171]
v_mfma_f32_16x16x32_bf16 v[168:171],a[140:143],v[92:95],v[168:171]
v_mfma_f32_16x16x32_bf16 v[172:175],a[156:159],v[92:95],v[36:39]
v_mfma_f32_16x16x32_bf16 v[172:175],a[152:155],v[88:91],v[172:175]
v_mfma_f32_16x16x32_bf16 v[172:175],a[148:151],v[84:87],v[172:175]
v_mfma_f32_16x16x32_bf16 v[172:175],a[144:147],v[80:83],v[172:175]
v_mfma_f32_16x16x32_bf16 v[184:187],a[160:163],v[80:83],v[36:39]
buffer_load_dwordx4 v5,s[24:27],s47 offen offset:1024 nt lds
v_mfma_f32_16x16x32_bf16 v[184:187],a[164:167],v[84:87],v[184:187]
v_mfma_f32_16x16x32_bf16 v[184:187],a[168:171],v[88:91],v[184:187]
v_mfma_f32_16x16x32_bf16 v[184:187],a[172:175],v[92:95],v[184:187]
v_exp_f32 v168,v168
v_mfma_f32_16x16x32_bf16 v[188:191],a[188:191],v[92:95],v[36:39]
v_exp_f32 v169,v169
v_mfma_f32_16x16x32_bf16 v[188:191],a[184:187],v[88:91],v[188:191]
v_exp_f32 v170,v170
v_mfma_f32_16x16x32_bf16 v[188:191],a[180:183],v[84:87],v[188:191]
v_exp_f32 v171,v171
v_mfma_f32_16x16x32_bf16 v[188:191],a[176:179],v[80:83],v[188:191]
v_exp_f32 v172,v172
v_mfma_f32_16x16x32_bf16 a[96:99],a[192:195],v[232:235],a[96:99]
ds_read_b64_tr_b16 a[192:193],v2
v_cvt_pk_bf16_f32 v168,v168,v169
v_mfma_f32_16x16x32_bf16 a[96:99],a[196:199],v[236:239],a[96:99]
ds_read_b64_tr_b16 a[194:195],v2 offset:4096
v_cvt_pk_bf16_f32 v169,v170,v171
v_mfma_f32_16x16x32_bf16 a[100:103],a[204:207],v[236:239],a[100:103]
v_exp_f32 v173,v173
v_mfma_f32_16x16x32_bf16 a[100:103],a[200:203],v[232:235],a[100:103]
ds_read_b64_tr_b16 a[196:197],v2 offset:8192
v_cvt_pk_bf16_f32 v170,v172,v173
v_mfma_f32_16x16x32_bf16 a[104:107],a[208:211],v[232:235],a[104:107]
v_exp_f32 v174,v174
v_mfma_f32_16x16x32_bf16 a[104:107],a[212:215],v[236:239],a[104:107]
v_exp_f32 v175,v175
v_mfma_f32_16x16x32_bf16 a[108:111],a[220:223],v[236:239],a[108:111]
buffer_load_dwordx4 v4,s[24:27],s47 offen offset:2048 nt lds
v_mfma_f32_16x16x32_bf16 a[108:111],a[216:219],v[232:235],a[108:111]
ds_read_b64_tr_b16 a[198:199],v2 offset:12288
v_cvt_pk_bf16_f32 v171,v174,v175
v_mfma_f32_16x16x32_bf16 a[112:115],a[224:227],v[232:235],a[112:115]
v_exp_f32 v184,v184
v_mfma_f32_16x16x32_bf16 a[112:115],a[228:231],v[236:239],a[112:115]
v_exp_f32 v185,v185
v_mfma_f32_16x16x32_bf16 a[116:119],a[236:239],v[236:239],a[116:119]
ds_read_b64_tr_b16 a[204:205],v3 offset:8192
v_mfma_f32_16x16x32_bf16 a[116:119],a[232:235],v[232:235],a[116:119]
ds_read_b64_tr_b16 a[206:207],v3 offset:12288
v_cvt_pk_bf16_f32 v172,v184,v185
v_mfma_f32_16x16x32_bf16 a[120:123],a[240:243],v[232:235],a[120:123]
ds_read_b64_tr_b16 a[200:201],v3
v_mfma_f32_16x16x32_bf16 a[120:123],a[244:247],v[236:239],a[120:123]
ds_read_b64_tr_b16 a[202:203],v3 offset:4096
v_mfma_f32_16x16x32_bf16 a[124:127],a[252:255],v[236:239],a[124:127]
ds_read_b64_tr_b16 a[208:209],v2 offset:256
v_mfma_f32_16x16x32_bf16 a[124:127],a[248:251],v[232:235],a[124:127]
ds_read_b64_tr_b16 a[210:211],v2 offset:4352
v_smfmac_f32_16x16x64_bf16 v[60:63],v[28:31],v[232:239],v27
ds_read_b64_tr_b16 a[212:213],v2 offset:8448
v_mfma_f32_16x16x32_bf16 v[200:203],a[128:131],v[96:99],v[40:43]
ds_read_b64_tr_b16 a[214:215],v2 offset:12544
v_mfma_f32_16x16x32_bf16 v[200:203],a[132:135],v[100:103],v[200:203]
ds_read_b64_tr_b16 a[220:221],v3 offset:8448
v_mfma_f32_16x16x32_bf16 v[200:203],a[136:139],v[104:107],v[200:203]
ds_read_b64_tr_b16 a[222:223],v3 offset:12544
v_mfma_f32_16x16x32_bf16 v[200:203],a[140:143],v[108:111],v[200:203]
buffer_load_dwordx4 v5,s[24:27],s47 offen offset:3072 nt lds
v_mfma_f32_16x16x32_bf16 v[204:207],a[156:159],v[108:111],v[40:43]
s_or_b32 s47,s47,0x18000
s_add_u32 s47,s47,4096
ds_read_b64_tr_b16 a[216:217],v3 offset:256
v_mfma_f32_16x16x32_bf16 v[204:207],a[152:155],v[104:107],v[204:207]
s_and_b32 s47,s47,0x27000
ds_read_b64_tr_b16 a[218:219],v3 offset:4352
v_mfma_f32_16x16x32_bf16 v[204:207],a[148:151],v[100:103],v[204:207]
s_or_b32 s47,s47,s46
ds_read_b64_tr_b16 a[224:225],v2 offset:512
v_mfma_f32_16x16x32_bf16 v[204:207],a[144:147],v[96:99],v[204:207]
ds_read_b64_tr_b16 a[226:227],v2 offset:4608
v_mfma_f32_16x16x32_bf16 v[216:219],a[160:163],v[96:99],v[40:43]
ds_read_b64_tr_b16 a[228:229],v2 offset:8704
v_mfma_f32_16x16x32_bf16 v[216:219],a[164:167],v[100:103],v[216:219]
ds_read_b64_tr_b16 a[230:231],v2 offset:12800
v_mfma_f32_16x16x32_bf16 v[216:219],a[168:171],v[104:107],v[216:219]
ds_read_b64_tr_b16 a[236:237],v3 offset:8704
v_mfma_f32_16x16x32_bf16 v[216:219],a[172:175],v[108:111],v[216:219]
ds_read_b64_tr_b16 a[238:239],v3 offset:12800
v_mfma_f32_16x16x32_bf16 v[220:223],a[188:191],v[108:111],v[40:43]
ds_read_b64_tr_b16 a[232:233],v3 offset:512
v_mfma_f32_16x16x32_bf16 v[220:223],a[184:187],v[104:107],v[220:223]
ds_read_b64_tr_b16 a[234:235],v3 offset:4608
v_mfma_f32_16x16x32_bf16 v[220:223],a[180:183],v[100:103],v[220:223]
ds_read_b64_tr_b16 a[240:241],v2 offset:768
v_mfma_f32_16x16x32_bf16 v[220:223],a[176:179],v[96:99],v[220:223]
buffer_load_dword v26,v24,s[88:91],s93 offen
ds_read_b64_tr_b16 a[242:243],v2 offset:4864
ds_read_b64_tr_b16 a[244:245],v2 offset:8960
ds_read_b64_tr_b16 a[246:247],v2 offset:13056
ds_read_b64_tr_b16 a[252:253],v3 offset:8960
ds_read_b64_tr_b16 a[254:255],v3 offset:13056
ds_read_b64_tr_b16 a[248:249],v3 offset:768
ds_read_b64_tr_b16 a[250:251],v3 offset:4864
s_bitcmp1_b32 s45,0
s_cbranch_scc0 L_t31v00_%=
s_mov_b32 m0,s80
s_add_u32 s80,s80,4096
buffer_load_dwordx4 v25,s[20:23],s83 offen nt lds
s_add_u32 s83,s83,256
L_t31v00_%=:
v_exp_f32 v186,v186
v_exp_f32 v187,v187
v_exp_f32 v188,v188
v_exp_f32 v189,v189
v_exp_f32 v190,v190
v_cvt_pk_bf16_f32 v173,v186,v187
v_exp_f32 v191,v191
v_cvt_pk_bf16_f32 v174,v188,v189
v_cvt_pk_bf16_f32 v175,v190,v191
s_cmp_lg_u32 s53,0
s_cbranch_scc1 L_att0v0_%=
s_waitcnt lgkmcnt(0)
s_nop 7
s_branch L_att0x_%=
.p2align 5
L_att0v1_%=:
v_mfma_f32_16x16x32_bf16 v[232:235],a[128:131],v[112:115],v[44:47]
s_waitcnt vmcnt(8) lgkmcnt(0)
v_exp_f32 v200,v200
v_mfma_f32_16x16x32_bf16 v[232:235],a[132:135],v[116:119],v[232:235]
s_barrier
s_add_u32 m0,s49,s51
ds_read_b128 a[128:131],v1
v_mfma_f32_16x16x32_bf16 v[232:235],a[136:139],v[120:123],v[232:235]
s_mov_b32 s90,L_endseg_%=-L_pc_%=+128
v_exp_f32 v201,v201
v_mfma_f32_16x16x32_bf16 v[232:235],a[140:143],v[124:127],v[232:235]
s_lshr_b32 s45,s45,1
ds_read_b128 a[132:135],v1 offset:256
v_cvt_pk_bf16_f32 v200,v200,v201
v_mfma_f32_16x16x32_bf16 v[236:239],a[156:159],v[124:127],v[44:47]
buffer_load_dwordx4 v4,s[20:23],s47 offen nt lds
v_mfma_f32_16x16x32_bf16 v[236:239],a[152:155],v[120:123],v[236:239]
ds_read_b128 a[136:139],v1 offset:512
v_add_u32 v2,s48,v2
v_mfma_f32_16x16x32_bf16 v[236:239],a[148:151],v[116:119],v[236:239]
v_exp_f32 v202,v202
v_mfma_f32_16x16x32_bf16 v[236:239],a[144:147],v[112:115],v[236:239]
s_add_u32 s62,s50,32768
ds_read_b128 a[140:143],v1 offset:768
v_add_u32 v3,s48,v3
v_mfma_f32_16x16x32_bf16 v[248:251],a[160:163],v[112:115],v[44:47]
s_and_b32 s62,s62,131071
v_exp_f32 v203,v203
v_mfma_f32_16x16x32_bf16 v[248:251],a[164:167],v[116:119],v[248:251]
s_add_u32 s54,s54,16384
ds_read_b128 a[156:159],v1 offset:4864
v_cvt_pk_bf16_f32 v201,v202,v203
v_mfma_f32_16x16x32_bf16 v[248:251],a[168:171],v[120:123],v[248:251]
s_sub_u32 s48,s62,s50
v_exp_f32 v204,v204
v_mfma_f32_16x16x32_bf16 v[248:251],a[172:175],v[124:127],v[248:251]
s_sub_u32 s53,s53,1
v_exp_f32 v205,v205
v_mfma_f32_16x16x32_bf16 v[252:255],a[188:191],v[124:127],v[44:47]
s_and_b32 s46,s54,0xfffc0000
ds_read_b128 a[152:155],v1 offset:4608
v_cvt_pk_bf16_f32 v202,v204,v205
v_mfma_f32_16x16x32_bf16 v[252:255],a[184:187],v[120:123],v[252:255]
s_mov_b32 s50,s62
v_exp_f32 v206,v206
v_mfma_f32_16x16x32_bf16 v[252:255],a[180:183],v[116:119],v[252:255]
v_exp_f32 v207,v207
v_mfma_f32_16x16x32_bf16 v[252:255],a[176:179],v[112:115],v[252:255]
ds_read_b128 a[148:151],v1 offset:4352
v_cvt_pk_bf16_f32 v203,v206,v207
v_exp_f32 v216,v216
ds_read_b128 a[144:147],v1 offset:4096
buffer_load_dwordx4 v4,s[20:23],s47 offen offset:1024 nt lds
ds_read_b128 a[160:163],v1 offset:8192
v_exp_f32 v217,v217
ds_read_b128 a[164:167],v1 offset:8448
v_cvt_pk_bf16_f32 v204,v216,v217
v_exp_f32 v218,v218
ds_read_b128 a[168:171],v1 offset:8704
v_exp_f32 v219,v219
ds_read_b128 a[172:175],v1 offset:8960
v_cvt_pk_bf16_f32 v205,v218,v219
v_exp_f32 v220,v220
ds_read_b128 a[188:191],v1 offset:13056
v_exp_f32 v221,v221
ds_read_b128 a[184:187],v1 offset:12800
v_cvt_pk_bf16_f32 v206,v220,v221
v_exp_f32 v222,v222
ds_read_b128 a[180:183],v1 offset:12544
s_waitcnt lgkmcnt(11)
v_mfma_f32_16x16x32_bf16 v[136:139],a[128:131],v[64:67],v[32:35]
buffer_load_dwordx4 v5,s[20:23],s47 offen offset:2048 nt lds
v_mfma_f32_16x16x32_bf16 v[136:139],a[132:135],v[68:71],v[136:139]
ds_read_b128 a[176:179],v1 offset:12288
v_add_u32 v1,s48,v1
v_mfma_f32_16x16x32_bf16 v[136:139],a[136:139],v[72:75],v[136:139]
v_exp_f32 v223,v223
v_mfma_f32_16x16x32_bf16 v[136:139],a[140:143],v[76:79],v[136:139]
s_waitcnt lgkmcnt(11)
v_cvt_pk_bf16_f32 v207,v222,v223
v_mfma_f32_16x16x32_bf16 v[140:143],a[156:159],v[76:79],v[32:35]
s_waitcnt lgkmcnt(10)
v_exp_f32 v232,v232
v_mfma_f32_16x16x32_bf16 v[140:143],a[152:155],v[72:75],v[140:143]
s_waitcnt lgkmcnt(9)
v_exp_f32 v233,v233
v_mfma_f32_16x16x32_bf16 v[140:143],a[148:151],v[68:71],v[140:143]
)A"
R"A(s_waitcnt lgkmcnt(8)
v_exp_f32 v234,v234
v_mfma_f32_16x16x32_bf16 v[140:143],a[144:147],v[64:67],v[140:143]
s_waitcnt lgkmcnt(1)
v_exp_f32 v235,v235
v_mfma_f32_16x16x32_bf16 v[152:155],a[160:163],v[64:67],v[32:35]
v_exp_f32 v236,v236
v_mfma_f32_16x16x32_bf16 v[152:155],a[164:167],v[68:71],v[152:155]
v_exp_f32 v237,v237
v_mfma_f32_16x16x32_bf16 v[152:155],a[168:171],v[72:75],v[152:155]
v_exp_f32 v238,v238
v_mfma_f32_16x16x32_bf16 v[152:155],a[172:175],v[76:79],v[152:155]
v_exp_f32 v239,v239
v_mfma_f32_16x16x32_bf16 v[156:159],a[188:191],v[76:79],v[32:35]
v_exp_f32 v248,v248
v_mfma_f32_16x16x32_bf16 v[156:159],a[184:187],v[72:75],v[156:159]
buffer_load_dwordx4 v5,s[20:23],s47 offen offset:3072 nt lds
v_mfma_f32_16x16x32_bf16 v[156:159],a[180:183],v[68:71],v[156:159]
s_waitcnt lgkmcnt(0)
v_exp_f32 v249,v249
v_mfma_f32_16x16x32_bf16 v[156:159],a[176:179],v[64:67],v[156:159]
v_exp_f32 v250,v250
v_mfma_f32_16x16x32_bf16 a[64:67],a[192:195],v[200:203],a[64:67]
v_exp_f32 v251,v251
v_mfma_f32_16x16x32_bf16 a[64:67],a[196:199],v[204:207],a[64:67]
v_exp_f32 v252,v252
v_mfma_f32_16x16x32_bf16 a[68:71],a[204:207],v[204:207],a[68:71]
v_exp_f32 v253,v253
v_mfma_f32_16x16x32_bf16 a[68:71],a[200:203],v[200:203],a[68:71]
v_exp_f32 v254,v254
v_mfma_f32_16x16x32_bf16 a[72:75],a[208:211],v[200:203],a[72:75]
v_exp_f32 v255,v255
v_mfma_f32_16x16x32_bf16 a[72:75],a[212:215],v[204:207],a[72:75]
v_exp_f32 v136,v136
v_mfma_f32_16x16x32_bf16 a[76:79],a[220:223],v[204:207],a[76:79]
v_exp_f32 v137,v137
v_mfma_f32_16x16x32_bf16 a[76:79],a[216:219],v[200:203],a[76:79]
v_exp_f32 v138,v138
v_mfma_f32_16x16x32_bf16 a[80:83],a[224:227],v[200:203],a[80:83]
s_add_u32 m0,s49,s52
v_cvt_pk_bf16_f32 v232,v232,v233
v_cvt_pk_bf16_f32 v233,v234,v235
v_mfma_f32_16x16x32_bf16 a[80:83],a[228:231],v[204:207],a[80:83]
s_xor_b32 s49,s50,65536
v_cvt_pk_bf16_f32 v234,v236,v237
v_cvt_pk_bf16_f32 v235,v238,v239
v_mfma_f32_16x16x32_bf16 a[84:87],a[236:239],v[204:207],a[84:87]
v_cvt_pk_bf16_f32 v236,v248,v249
v_cvt_pk_bf16_f32 v237,v250,v251
v_mfma_f32_16x16x32_bf16 a[84:87],a[232:235],v[200:203],a[84:87]
buffer_load_dwordx4 v4,s[24:27],s47 offen nt lds
v_mfma_f32_16x16x32_bf16 a[88:91],a[240:243],v[200:203],a[88:91]
v_cvt_pk_bf16_f32 v238,v252,v253
v_cvt_pk_bf16_f32 v239,v254,v255
v_mfma_f32_16x16x32_bf16 a[88:91],a[244:247],v[204:207],a[88:91]
v_exp_f32 v139,v139
v_mfma_f32_16x16x32_bf16 a[92:95],a[252:255],v[204:207],a[92:95]
v_exp_f32 v140,v140
v_mfma_f32_16x16x32_bf16 a[92:95],a[248:251],v[200:203],a[92:95]
v_exp_f32 v141,v141
v_smfmac_f32_16x16x64_bf16 v[56:59],v[28:31],v[200:207],v27
v_exp_f32 v142,v142
v_exp_f32 v143,v143
v_exp_f32 v152,v152
v_exp_f32 v153,v153
v_exp_f32 v154,v154
v_exp_f32 v155,v155
v_exp_f32 v156,v156
v_exp_f32 v157,v157
buffer_load_dwordx4 v5,s[24:27],s47 offen offset:1024 nt lds
v_exp_f32 v158,v158
v_exp_f32 v159,v159
v_mfma_f32_16x16x32_bf16 a[96:99],a[192:195],v[232:235],a[96:99]
ds_read_b64_tr_b16 a[192:193],v2
v_mfma_f32_16x16x32_bf16 a[96:99],a[196:199],v[236:239],a[96:99]
ds_read_b64_tr_b16 a[194:195],v2 offset:4096
v_mfma_f32_16x16x32_bf16 a[100:103],a[204:207],v[236:239],a[100:103]
v_mfma_f32_16x16x32_bf16 a[100:103],a[200:203],v[232:235],a[100:103]
ds_read_b64_tr_b16 a[196:197],v2 offset:8192
v_mfma_f32_16x16x32_bf16 a[104:107],a[208:211],v[232:235],a[104:107]
v_mfma_f32_16x16x32_bf16 a[104:107],a[212:215],v[236:239],a[104:107]
v_mfma_f32_16x16x32_bf16 a[108:111],a[220:223],v[236:239],a[108:111]
buffer_load_dwordx4 v4,s[24:27],s47 offen offset:2048 nt lds
v_mfma_f32_16x16x32_bf16 a[108:111],a[216:219],v[232:235],a[108:111]
ds_read_b64_tr_b16 a[198:199],v2 offset:12288
v_mfma_f32_16x16x32_bf16 a[112:115],a[224:227],v[232:235],a[112:115]
v_mfma_f32_16x16x32_bf16 a[112:115],a[228:231],v[236:239],a[112:115]
v_mfma_f32_16x16x32_bf16 a[116:119],a[236:239],v[236:239],a[116:119]
ds_read_b64_tr_b16 a[204:205],v3 offset:8192
v_cvt_pk_bf16_f32 v136,v136,v137
v_mfma_f32_16x16x32_bf16 a[116:119],a[232:235],v[232:235],a[116:119]
ds_read_b64_tr_b16 a[206:207],v3 offset:12288
v_mfma_f32_16x16x32_bf16 a[120:123],a[240:243],v[232:235],a[120:123]
ds_read_b64_tr_b16 a[200:201],v3
v_mfma_f32_16x16x32_bf16 a[120:123],a[244:247],v[236:239],a[120:123]
ds_read_b64_tr_b16 a[202:203],v3 offset:4096
v_cvt_pk_bf16_f32 v137,v138,v139
v_mfma_f32_16x16x32_bf16 a[124:127],a[252:255],v[236:239],a[124:127]
ds_read_b64_tr_b16 a[208:209],v2 offset:256
v_mfma_f32_16x16x32_bf16 a[124:127],a[248:251],v[232:235],a[124:127]
ds_read_b64_tr_b16 a[210:211],v2 offset:4352
v_smfmac_f32_16x16x64_bf16 v[60:63],v[28:31],v[232:239],v27
ds_read_b64_tr_b16 a[212:213],v2 offset:8448
v_mfma_f32_16x16x32_bf16 v[200:203],a[128:131],v[96:99],v[40:43]
ds_read_b64_tr_b16 a[214:215],v2 offset:12544
v_cvt_pk_bf16_f32 v138,v140,v141
v_mfma_f32_16x16x32_bf16 v[200:203],a[132:135],v[100:103],v[200:203]
ds_read_b64_tr_b16 a[220:221],v3 offset:8448
v_mfma_f32_16x16x32_bf16 v[200:203],a[136:139],v[104:107],v[200:203]
ds_read_b64_tr_b16 a[222:223],v3 offset:12544
v_cvt_pk_bf16_f32 v139,v142,v143
v_mfma_f32_16x16x32_bf16 v[200:203],a[140:143],v[108:111],v[200:203]
buffer_load_dwordx4 v5,s[24:27],s47 offen offset:3072 nt lds
v_mfma_f32_16x16x32_bf16 v[204:207],a[156:159],v[108:111],v[40:43]
s_or_b32 s47,s47,0x18000
s_add_u32 s47,s47,4096
ds_read_b64_tr_b16 a[216:217],v3 offset:256
v_mfma_f32_16x16x32_bf16 v[204:207],a[152:155],v[104:107],v[204:207]
s_and_b32 s47,s47,0x27000
ds_read_b64_tr_b16 a[218:219],v3 offset:4352
v_cvt_pk_bf16_f32 v140,v152,v153
v_mfma_f32_16x16x32_bf16 v[204:207],a[148:151],v[100:103],v[204:207]
s_or_b32 s47,s47,s46
ds_read_b64_tr_b16 a[224:225],v2 offset:512
v_mfma_f32_16x16x32_bf16 v[204:207],a[144:147],v[96:99],v[204:207]
ds_read_b64_tr_b16 a[226:227],v2 offset:4608
v_mfma_f32_16x16x32_bf16 v[216:219],a[160:163],v[96:99],v[40:43]
ds_read_b64_tr_b16 a[228:229],v2 offset:8704
v_cvt_pk_bf16_f32 v141,v154,v155
v_mfma_f32_16x16x32_bf16 v[216:219],a[164:167],v[100:103],v[216:219]
ds_read_b64_tr_b16 a[230:231],v2 offset:12800
v_mfma_f32_16x16x32_bf16 v[216:219],a[168:171],v[104:107],v[216:219]
ds_read_b64_tr_b16 a[236:237],v3 offset:8704
v_mfma_f32_16x16x32_bf16 v[216:219],a[172:175],v[108:111],v[216:219]
ds_read_b64_tr_b16 a[238:239],v3 offset:12800
v_cvt_pk_bf16_f32 v142,v156,v157
v_mfma_f32_16x16x32_bf16 v[220:223],a[188:191],v[108:111],v[40:43]
ds_read_b64_tr_b16 a[232:233],v3 offset:512
v_mfma_f32_16x16x32_bf16 v[220:223],a[184:187],v[104:107],v[220:223]
ds_read_b64_tr_b16 a[234:235],v3 offset:4608
v_mfma_f32_16x16x32_bf16 v[220:223],a[180:183],v[100:103],v[220:223]
ds_read_b64_tr_b16 a[240:241],v2 offset:768
v_cvt_pk_bf16_f32 v143,v158,v159
v_mfma_f32_16x16x32_bf16 v[220:223],a[176:179],v[96:99],v[220:223]
s_waitcnt lgkmcnt(7)
buffer_load_dword v26,v24,s[88:91],s93 offen
v_mfma_f32_16x16x32_bf16 a[0:3],a[192:195],v[136:139],a[0:3]
ds_read_b64_tr_b16 a[242:243],v2 offset:4864
v_mfma_f32_16x16x32_bf16 a[0:3],a[196:199],v[140:143],a[0:3]
ds_read_b64_tr_b16 a[244:245],v2 offset:8960
v_mfma_f32_16x16x32_bf16 a[4:7],a[204:207],v[140:143],a[4:7]
ds_read_b64_tr_b16 a[246:247],v2 offset:13056
v_mfma_f32_16x16x32_bf16 a[4:7],a[200:203],v[136:139],a[4:7]
ds_read_b64_tr_b16 a[252:253],v3 offset:8960
v_mfma_f32_16x16x32_bf16 a[8:11],a[208:211],v[136:139],a[8:11]
ds_read_b64_tr_b16 a[254:255],v3 offset:13056
v_mfma_f32_16x16x32_bf16 a[8:11],a[212:215],v[140:143],a[8:11]
ds_read_b64_tr_b16 a[248:249],v3 offset:768
v_mfma_f32_16x16x32_bf16 a[12:15],a[220:223],v[140:143],a[12:15]
ds_read_b64_tr_b16 a[250:251],v3 offset:4864
v_mfma_f32_16x16x32_bf16 a[12:15],a[216:219],v[136:139],a[12:15]
s_bitcmp1_b32 s45,0
s_cbranch_scc0 L_t31v10_%=
s_mov_b32 m0,s80
s_add_u32 s80,s80,4096
buffer_load_dwordx4 v25,s[20:23],s83 offen nt lds
s_add_u32 s83,s83,256
L_t31v10_%=:
v_mfma_f32_16x16x32_bf16 a[16:19],a[224:227],v[136:139],a[16:19]
s_waitcnt lgkmcnt(6)
v_mfma_f32_16x16x32_bf16 a[16:19],a[228:231],v[140:143],a[16:19]
v_mfma_f32_16x16x32_bf16 a[20:23],a[236:239],v[140:143],a[20:23]
v_mfma_f32_16x16x32_bf16 a[20:23],a[232:235],v[136:139],a[20:23]
v_mfma_f32_16x16x32_bf16 a[24:27],a[240:243],v[136:139],a[24:27]
s_waitcnt lgkmcnt(2)
v_mfma_f32_16x16x32_bf16 a[24:27],a[244:247],v[140:143],a[24:27]
v_mfma_f32_16x16x32_bf16 a[28:31],a[252:255],v[140:143],a[28:31]
s_waitcnt lgkmcnt(0)
v_mfma_f32_16x16x32_bf16 a[28:31],a[248:251],v[136:139],a[28:31]
s_cmp_lg_u32 s53,0
v_smfmac_f32_16x16x64_bf16 v[48:51],v[28:31],v[136:143],v27
s_cbranch_scc1 L_att0v1_%=
s_waitcnt lgkmcnt(0)
s_nop 7
s_branch L_att0x_%=
.p2align 5
L_att0v2_%=:
v_mfma_f32_16x16x32_bf16 v[232:235],a[128:131],v[112:115],v[44:47]
s_waitcnt vmcnt(8) lgkmcnt(0)
v_mfma_f32_16x16x32_bf16 v[232:235],a[132:135],v[116:119],v[232:235]
s_barrier
s_add_u32 m0,s49,s51
ds_read_b128 a[128:131],v1
v_mfma_f32_16x16x32_bf16 v[232:235],a[136:139],v[120:123],v[232:235]
s_mov_b32 s90,L_endseg_%=-L_pc_%=+128
v_mfma_f32_16x16x32_bf16 v[232:235],a[140:143],v[124:127],v[232:235]
s_lshr_b32 s45,s45,1
ds_read_b128 a[132:135],v1 offset:256
v_mfma_f32_16x16x32_bf16 v[236:239],a[156:159],v[124:127],v[44:47]
buffer_load_dwordx4 v4,s[20:23],s47 offen nt lds
v_mfma_f32_16x16x32_bf16 v[236:239],a[152:155],v[120:123],v[236:239]
ds_read_b128 a[136:139],v1 offset:512
v_add_u32 v2,s48,v2
v_mfma_f32_16x16x32_bf16 v[236:239],a[148:151],v[116:119],v[236:239]
v_mfma_f32_16x16x32_bf16 v[236:239],a[144:147],v[112:115],v[236:239]
s_add_u32 s62,s50,32768
ds_read_b128 a[140:143],v1 offset:768
v_add_u32 v3,s48,v3
v_mfma_f32_16x16x32_bf16 v[248:251],a[160:163],v[112:115],v[44:47]
s_and_b32 s62,s62,131071
v_mfma_f32_16x16x32_bf16 v[248:251],a[164:167],v[116:119],v[248:251]
s_add_u32 s54,s54,16384
ds_read_b128 a[156:159],v1 offset:4864
v_mfma_f32_16x16x32_bf16 v[248:251],a[168:171],v[120:123],v[248:251]
s_sub_u32 s48,s62,s50
v_mfma_f32_16x16x32_bf16 v[248:251],a[172:175],v[124:127],v[248:251]
s_sub_u32 s53,s53,1
v_mfma_f32_16x16x32_bf16 v[252:255],a[188:191],v[124:127],v[44:47]
s_and_b32 s46,s54,0xfffc0000
ds_read_b128 a[152:155],v1 offset:4608
v_mfma_f32_16x16x32_bf16 v[252:255],a[184:187],v[120:123],v[252:255]
s_mov_b32 s50,s62
v_mfma_f32_16x16x32_bf16 v[252:255],a[180:183],v[116:119],v[252:255]
v_mfma_f32_16x16x32_bf16 v[252:255],a[176:179],v[112:115],v[252:255]
ds_read_b128 a[148:151],v1 offset:4352
v_mfma_f32_16x16x32_bf16 a[32:35],a[192:195],v[168:171],a[32:35]
v_mfma_f32_16x16x32_bf16 a[32:35],a[196:199],v[172:175],a[32:35]
ds_read_b128 a[144:147],v1 offset:4096
v_mfma_f32_16x16x32_bf16 a[36:39],a[204:207],v[172:175],a[36:39]
buffer_load_dwordx4 v4,s[20:23],s47 offen offset:1024 nt lds
v_mfma_f32_16x16x32_bf16 a[36:39],a[200:203],v[168:171],a[36:39]
ds_read_b128 a[160:163],v1 offset:8192
v_mfma_f32_16x16x32_bf16 a[40:43],a[208:211],v[168:171],a[40:43]
v_mfma_f32_16x16x32_bf16 a[40:43],a[212:215],v[172:175],a[40:43]
ds_read_b128 a[164:167],v1 offset:8448
v_mfma_f32_16x16x32_bf16 a[44:47],a[220:223],v[172:175],a[44:47]
v_mfma_f32_16x16x32_bf16 a[44:47],a[216:219],v[168:171],a[44:47]
ds_read_b128 a[168:171],v1 offset:8704
v_mfma_f32_16x16x32_bf16 a[48:51],a[224:227],v[168:171],a[48:51]
v_mfma_f32_16x16x32_bf16 a[48:51],a[228:231],v[172:175],a[48:51]
ds_read_b128 a[172:175],v1 offset:8960
v_mfma_f32_16x16x32_bf16 a[52:55],a[236:239],v[172:175],a[52:55]
v_mfma_f32_16x16x32_bf16 a[52:55],a[232:235],v[168:171],a[52:55]
ds_read_b128 a[188:191],v1 offset:13056
v_mfma_f32_16x16x32_bf16 a[56:59],a[240:243],v[168:171],a[56:59]
v_mfma_f32_16x16x32_bf16 a[56:59],a[244:247],v[172:175],a[56:59]
ds_read_b128 a[184:187],v1 offset:12800
v_mfma_f32_16x16x32_bf16 a[60:63],a[252:255],v[172:175],a[60:63]
v_mfma_f32_16x16x32_bf16 a[60:63],a[248:251],v[168:171],a[60:63]
ds_read_b128 a[180:183],v1 offset:12544
v_smfmac_f32_16x16x64_bf16 v[52:55],v[28:31],v[168:175],v27
s_waitcnt lgkmcnt(4)
v_mfma_f32_16x16x32_bf16 v[136:139],a[128:131],v[64:67],v[32:35]
buffer_load_dwordx4 v5,s[20:23],s47 offen offset:2048 nt lds
v_mfma_f32_16x16x32_bf16 v[136:139],a[132:135],v[68:71],v[136:139]
ds_read_b128 a[176:179],v1 offset:12288
v_add_u32 v1,s48,v1
v_mfma_f32_16x16x32_bf16 v[136:139],a[136:139],v[72:75],v[136:139]
)A"
R"A(v_mfma_f32_16x16x32_bf16 v[136:139],a[140:143],v[76:79],v[136:139]
v_mfma_f32_16x16x32_bf16 v[140:143],a[156:159],v[76:79],v[32:35]
v_exp_f32 v232,v232
v_mfma_f32_16x16x32_bf16 v[140:143],a[152:155],v[72:75],v[140:143]
v_exp_f32 v233,v233
v_mfma_f32_16x16x32_bf16 v[140:143],a[148:151],v[68:71],v[140:143]
v_exp_f32 v234,v234
v_mfma_f32_16x16x32_bf16 v[140:143],a[144:147],v[64:67],v[140:143]
v_exp_f32 v235,v235
v_mfma_f32_16x16x32_bf16 v[152:155],a[160:163],v[64:67],v[32:35]
v_exp_f32 v236,v236
v_mfma_f32_16x16x32_bf16 v[152:155],a[164:167],v[68:71],v[152:155]
v_exp_f32 v237,v237
v_mfma_f32_16x16x32_bf16 v[152:155],a[168:171],v[72:75],v[152:155]
s_waitcnt lgkmcnt(0)
v_exp_f32 v238,v238
v_mfma_f32_16x16x32_bf16 v[152:155],a[172:175],v[76:79],v[152:155]
v_exp_f32 v239,v239
v_mfma_f32_16x16x32_bf16 v[156:159],a[188:191],v[76:79],v[32:35]
v_exp_f32 v248,v248
v_mfma_f32_16x16x32_bf16 v[156:159],a[184:187],v[72:75],v[156:159]
buffer_load_dwordx4 v5,s[20:23],s47 offen offset:3072 nt lds
v_mfma_f32_16x16x32_bf16 v[156:159],a[180:183],v[68:71],v[156:159]
v_exp_f32 v249,v249
v_mfma_f32_16x16x32_bf16 v[156:159],a[176:179],v[64:67],v[156:159]
v_exp_f32 v250,v250
v_exp_f32 v251,v251
v_exp_f32 v252,v252
v_exp_f32 v253,v253
v_exp_f32 v254,v254
v_exp_f32 v255,v255
v_exp_f32 v136,v136
v_exp_f32 v137,v137
v_exp_f32 v138,v138
s_add_u32 m0,s49,s52
v_cvt_pk_bf16_f32 v232,v232,v233
v_cvt_pk_bf16_f32 v233,v234,v235
s_xor_b32 s49,s50,65536
v_cvt_pk_bf16_f32 v234,v236,v237
v_cvt_pk_bf16_f32 v235,v238,v239
v_cvt_pk_bf16_f32 v236,v248,v249
v_cvt_pk_bf16_f32 v237,v250,v251
buffer_load_dwordx4 v4,s[24:27],s47 offen nt lds
v_cvt_pk_bf16_f32 v238,v252,v253
v_cvt_pk_bf16_f32 v239,v254,v255
v_exp_f32 v139,v139
v_exp_f32 v140,v140
v_exp_f32 v141,v141
v_mfma_f32_16x16x32_bf16 v[168:171],a[128:131],v[80:83],v[36:39]
v_exp_f32 v142,v142
v_mfma_f32_16x16x32_bf16 v[168:171],a[132:135],v[84:87],v[168:171]
v_exp_f32 v143,v143
v_mfma_f32_16x16x32_bf16 v[168:171],a[136:139],v[88:91],v[168:171]
v_exp_f32 v152,v152
v_mfma_f32_16x16x32_bf16 v[168:171],a[140:143],v[92:95],v[168:171]
v_exp_f32 v153,v153
v_mfma_f32_16x16x32_bf16 v[172:175],a[156:159],v[92:95],v[36:39]
v_exp_f32 v154,v154
v_mfma_f32_16x16x32_bf16 v[172:175],a[152:155],v[88:91],v[172:175]
v_exp_f32 v155,v155
v_mfma_f32_16x16x32_bf16 v[172:175],a[148:151],v[84:87],v[172:175]
v_exp_f32 v156,v156
v_mfma_f32_16x16x32_bf16 v[172:175],a[144:147],v[80:83],v[172:175]
v_exp_f32 v157,v157
v_mfma_f32_16x16x32_bf16 v[184:187],a[160:163],v[80:83],v[36:39]
buffer_load_dwordx4 v5,s[24:27],s47 offen offset:1024 nt lds
v_mfma_f32_16x16x32_bf16 v[184:187],a[164:167],v[84:87],v[184:187]
v_exp_f32 v158,v158
v_mfma_f32_16x16x32_bf16 v[184:187],a[168:171],v[88:91],v[184:187]
v_exp_f32 v159,v159
v_mfma_f32_16x16x32_bf16 v[184:187],a[172:175],v[92:95],v[184:187]
v_exp_f32 v168,v168
v_mfma_f32_16x16x32_bf16 v[188:191],a[188:191],v[92:95],v[36:39]
v_exp_f32 v169,v169
v_mfma_f32_16x16x32_bf16 v[188:191],a[184:187],v[88:91],v[188:191]
v_exp_f32 v170,v170
v_mfma_f32_16x16x32_bf16 v[188:191],a[180:183],v[84:87],v[188:191]
v_exp_f32 v171,v171
v_mfma_f32_16x16x32_bf16 v[188:191],a[176:179],v[80:83],v[188:191]
v_exp_f32 v172,v172
v_mfma_f32_16x16x32_bf16 a[96:99],a[192:195],v[232:235],a[96:99]
ds_read_b64_tr_b16 a[192:193],v2
v_cvt_pk_bf16_f32 v168,v168,v169
v_mfma_f32_16x16x32_bf16 a[96:99],a[196:199],v[236:239],a[96:99]
ds_read_b64_tr_b16 a[194:195],v2 offset:4096
v_cvt_pk_bf16_f32 v169,v170,v171
v_mfma_f32_16x16x32_bf16 a[100:103],a[204:207],v[236:239],a[100:103]
v_exp_f32 v173,v173
v_mfma_f32_16x16x32_bf16 a[100:103],a[200:203],v[232:235],a[100:103]
ds_read_b64_tr_b16 a[196:197],v2 offset:8192
v_cvt_pk_bf16_f32 v170,v172,v173
v_mfma_f32_16x16x32_bf16 a[104:107],a[208:211],v[232:235],a[104:107]
v_exp_f32 v174,v174
v_mfma_f32_16x16x32_bf16 a[104:107],a[212:215],v[236:239],a[104:107]
v_exp_f32 v175,v175
v_mfma_f32_16x16x32_bf16 a[108:111],a[220:223],v[236:239],a[108:111]
buffer_load_dwordx4 v4,s[24:27],s47 offen offset:2048 nt lds
v_mfma_f32_16x16x32_bf16 a[108:111],a[216:219],v[232:235],a[108:111]
ds_read_b64_tr_b16 a[198:199],v2 offset:12288
v_cvt_pk_bf16_f32 v171,v174,v175
v_mfma_f32_16x16x32_bf16 a[112:115],a[224:227],v[232:235],a[112:115]
v_exp_f32 v184,v184
v_mfma_f32_16x16x32_bf16 a[112:115],a[228:231],v[236:239],a[112:115]
v_exp_f32 v185,v185
v_mfma_f32_16x16x32_bf16 a[116:119],a[236:239],v[236:239],a[116:119]
ds_read_b64_tr_b16 a[204:205],v3 offset:8192
v_cvt_pk_bf16_f32 v136,v136,v137
v_mfma_f32_16x16x32_bf16 a[116:119],a[232:235],v[232:235],a[116:119]
ds_read_b64_tr_b16 a[206:207],v3 offset:12288
v_cvt_pk_bf16_f32 v172,v184,v185
v_mfma_f32_16x16x32_bf16 a[120:123],a[240:243],v[232:235],a[120:123]
ds_read_b64_tr_b16 a[200:201],v3
v_mfma_f32_16x16x32_bf16 a[120:123],a[244:247],v[236:239],a[120:123]
ds_read_b64_tr_b16 a[202:203],v3 offset:4096
v_cvt_pk_bf16_f32 v137,v138,v139
v_mfma_f32_16x16x32_bf16 a[124:127],a[252:255],v[236:239],a[124:127]
ds_read_b64_tr_b16 a[208:209],v2 offset:256
v_mfma_f32_16x16x32_bf16 a[124:127],a[248:251],v[232:235],a[124:127]
ds_read_b64_tr_b16 a[210:211],v2 offset:4352
v_smfmac_f32_16x16x64_bf16 v[60:63],v[28:31],v[232:239],v27
ds_read_b64_tr_b16 a[212:213],v2 offset:8448
ds_read_b64_tr_b16 a[214:215],v2 offset:12544
v_cvt_pk_bf16_f32 v138,v140,v141
ds_read_b64_tr_b16 a[220:221],v3 offset:8448
ds_read_b64_tr_b16 a[222:223],v3 offset:12544
v_cvt_pk_bf16_f32 v139,v142,v143
buffer_load_dwordx4 v5,s[24:27],s47 offen offset:3072 nt lds
s_or_b32 s47,s47,0x18000
s_add_u32 s47,s47,4096
ds_read_b64_tr_b16 a[216:217],v3 offset:256
s_and_b32 s47,s47,0x27000
ds_read_b64_tr_b16 a[218:219],v3 offset:4352
v_cvt_pk_bf16_f32 v140,v152,v153
s_or_b32 s47,s47,s46
ds_read_b64_tr_b16 a[224:225],v2 offset:512
ds_read_b64_tr_b16 a[226:227],v2 offset:4608
ds_read_b64_tr_b16 a[228:229],v2 offset:8704
v_cvt_pk_bf16_f32 v141,v154,v155
ds_read_b64_tr_b16 a[230:231],v2 offset:12800
ds_read_b64_tr_b16 a[236:237],v3 offset:8704
ds_read_b64_tr_b16 a[238:239],v3 offset:12800
v_cvt_pk_bf16_f32 v142,v156,v157
ds_read_b64_tr_b16 a[232:233],v3 offset:512
ds_read_b64_tr_b16 a[234:235],v3 offset:4608
ds_read_b64_tr_b16 a[240:241],v2 offset:768
v_cvt_pk_bf16_f32 v143,v158,v159
buffer_load_dword v26,v24,s[88:91],s93 offen
s_waitcnt lgkmcnt(15)
v_mfma_f32_16x16x32_bf16 a[0:3],a[192:195],v[136:139],a[0:3]
ds_read_b64_tr_b16 a[242:243],v2 offset:4864
v_mfma_f32_16x16x32_bf16 a[0:3],a[196:199],v[140:143],a[0:3]
ds_read_b64_tr_b16 a[244:245],v2 offset:8960
v_mfma_f32_16x16x32_bf16 a[4:7],a[204:207],v[140:143],a[4:7]
ds_read_b64_tr_b16 a[246:247],v2 offset:13056
v_mfma_f32_16x16x32_bf16 a[4:7],a[200:203],v[136:139],a[4:7]
ds_read_b64_tr_b16 a[252:253],v3 offset:8960
v_mfma_f32_16x16x32_bf16 a[8:11],a[208:211],v[136:139],a[8:11]
s_waitcnt lgkmcnt(15)
ds_read_b64_tr_b16 a[254:255],v3 offset:13056
v_mfma_f32_16x16x32_bf16 a[8:11],a[212:215],v[140:143],a[8:11]
ds_read_b64_tr_b16 a[248:249],v3 offset:768
v_mfma_f32_16x16x32_bf16 a[12:15],a[220:223],v[140:143],a[12:15]
s_waitcnt lgkmcnt(15)
ds_read_b64_tr_b16 a[250:251],v3 offset:4864
v_mfma_f32_16x16x32_bf16 a[12:15],a[216:219],v[136:139],a[12:15]
s_bitcmp1_b32 s45,0
s_cbranch_scc0 L_t31v20_%=
s_mov_b32 m0,s80
s_add_u32 s80,s80,4096
buffer_load_dwordx4 v25,s[20:23],s83 offen nt lds
s_add_u32 s83,s83,256
L_t31v20_%=:
s_waitcnt lgkmcnt(7)
v_mfma_f32_16x16x32_bf16 a[16:19],a[224:227],v[136:139],a[16:19]
v_exp_f32 v186,v186
v_mfma_f32_16x16x32_bf16 a[16:19],a[228:231],v[140:143],a[16:19]
v_exp_f32 v187,v187
v_mfma_f32_16x16x32_bf16 a[20:23],a[236:239],v[140:143],a[20:23]
v_exp_f32 v188,v188
v_mfma_f32_16x16x32_bf16 a[20:23],a[232:235],v[136:139],a[20:23]
s_waitcnt lgkmcnt(3)
v_exp_f32 v189,v189
v_mfma_f32_16x16x32_bf16 a[24:27],a[240:243],v[136:139],a[24:27]
v_exp_f32 v190,v190
v_mfma_f32_16x16x32_bf16 a[24:27],a[244:247],v[140:143],a[24:27]
s_waitcnt lgkmcnt(1)
v_cvt_pk_bf16_f32 v173,v186,v187
v_mfma_f32_16x16x32_bf16 a[28:31],a[252:255],v[140:143],a[28:31]
s_waitcnt lgkmcnt(0)
v_exp_f32 v191,v191
v_mfma_f32_16x16x32_bf16 a[28:31],a[248:251],v[136:139],a[28:31]
v_cvt_pk_bf16_f32 v174,v188,v189
v_cvt_pk_bf16_f32 v175,v190,v191
s_cmp_lg_u32 s53,0
v_smfmac_f32_16x16x64_bf16 v[48:51],v[28:31],v[136:143],v27
s_cbranch_scc1 L_att0v2_%=
s_waitcnt lgkmcnt(0)
s_nop 7
s_branch L_att0x_%=
.p2align 5
L_att0_%=:
v_mfma_f32_16x16x32_bf16 v[232:235],a[128:131],v[112:115],v[44:47]
s_waitcnt vmcnt(8) lgkmcnt(0)
v_exp_f32 v200,v200
v_mfma_f32_16x16x32_bf16 v[232:235],a[132:135],v[116:119],v[232:235]
s_barrier
s_add_u32 m0,s49,s51
ds_read_b128 a[128:131],v1
v_mfma_f32_16x16x32_bf16 v[232:235],a[136:139],v[120:123],v[232:235]
s_mov_b32 s90,L_endseg_%=-L_pc_%=+128
v_exp_f32 v201,v201
v_mfma_f32_16x16x32_bf16 v[232:235],a[140:143],v[124:127],v[232:235]
s_lshr_b32 s45,s45,1
ds_read_b128 a[132:135],v1 offset:256
v_cvt_pk_bf16_f32 v200,v200,v201
v_mfma_f32_16x16x32_bf16 v[236:239],a[156:159],v[124:127],v[44:47]
s_sub_u32 s94,s94,3
buffer_load_dwordx4 v4,s[20:23],s47 offen nt lds
v_mfma_f32_16x16x32_bf16 v[236:239],a[152:155],v[120:123],v[236:239]
s_lshr_b32 s60,s94,2
ds_read_b128 a[136:139],v1 offset:512
v_add_u32 v2,s48,v2
v_mfma_f32_16x16x32_bf16 v[236:239],a[148:151],v[116:119],v[236:239]
s_max_i32 s60,s60,128
v_exp_f32 v202,v202
v_mfma_f32_16x16x32_bf16 v[236:239],a[144:147],v[112:115],v[236:239]
s_add_u32 s62,s50,32768
ds_read_b128 a[140:143],v1 offset:768
v_add_u32 v3,s48,v3
v_mfma_f32_16x16x32_bf16 v[248:251],a[160:163],v[112:115],v[44:47]
s_and_b32 s62,s62,131071
v_exp_f32 v203,v203
v_mfma_f32_16x16x32_bf16 v[248:251],a[164:167],v[116:119],v[248:251]
s_add_u32 s54,s54,16384
ds_read_b128 a[156:159],v1 offset:4864
v_cvt_pk_bf16_f32 v201,v202,v203
v_mfma_f32_16x16x32_bf16 v[248:251],a[168:171],v[120:123],v[248:251]
s_sub_u32 s48,s62,s50
v_exp_f32 v204,v204
v_mfma_f32_16x16x32_bf16 v[248:251],a[172:175],v[124:127],v[248:251]
s_sub_u32 s53,s53,1
v_exp_f32 v205,v205
v_mfma_f32_16x16x32_bf16 v[252:255],a[188:191],v[124:127],v[44:47]
s_and_b32 s46,s54,0xfffc0000
ds_read_b128 a[152:155],v1 offset:4608
v_cvt_pk_bf16_f32 v202,v204,v205
v_mfma_f32_16x16x32_bf16 v[252:255],a[184:187],v[120:123],v[252:255]
s_mov_b32 s50,s62
v_exp_f32 v206,v206
v_mfma_f32_16x16x32_bf16 v[252:255],a[180:183],v[116:119],v[252:255]
v_exp_f32 v207,v207
v_mfma_f32_16x16x32_bf16 v[252:255],a[176:179],v[112:115],v[252:255]
ds_read_b128 a[148:151],v1 offset:4352
v_cvt_pk_bf16_f32 v203,v206,v207
v_mfma_f32_16x16x32_bf16 a[32:35],a[192:195],v[168:171],a[32:35]
v_exp_f32 v216,v216
v_mfma_f32_16x16x32_bf16 a[32:35],a[196:199],v[172:175],a[32:35]
ds_read_b128 a[144:147],v1 offset:4096
v_mfma_f32_16x16x32_bf16 a[36:39],a[204:207],v[172:175],a[36:39]
buffer_load_dwordx4 v4,s[20:23],s47 offen offset:1024 nt lds
v_mfma_f32_16x16x32_bf16 a[36:39],a[200:203],v[168:171],a[36:39]
ds_read_b128 a[160:163],v1 offset:8192
v_mfma_f32_16x16x32_bf16 a[40:43],a[208:211],v[168:171],a[40:43]
v_exp_f32 v217,v217
v_mfma_f32_16x16x32_bf16 a[40:43],a[212:215],v[172:175],a[40:43]
ds_read_b128 a[164:167],v1 offset:8448
v_cvt_pk_bf16_f32 v204,v216,v217
v_mfma_f32_16x16x32_bf16 a[44:47],a[220:223],v[172:175],a[44:47]
v_exp_f32 v218,v218
v_mfma_f32_16x16x32_bf16 a[44:47],a[216:219],v[168:171],a[44:47]
ds_read_b128 a[168:171],v1 offset:8704
v_mfma_f32_16x16x32_bf16 a[48:51],a[224:227],v[168:171],a[48:51]
v_exp_f32 v219,v219
v_mfma_f32_16x16x32_bf16 a[48:51],a[228:231],v[172:175],a[48:51]
ds_read_b128 a[172:175],v1 offset:8960
v_cvt_pk_bf16_f32 v205,v218,v219
v_mfma_f32_16x16x32_bf16 a[52:55],a[236:239],v[172:175],a[52:55]
v_exp_f32 v220,v220
v_mfma_f32_16x16x32_bf16 a[52:55],a[232:235],v[168:171],a[52:55]
ds_read_b128 a[188:191],v1 offset:13056
v_mfma_f32_16x16x32_bf16 a[56:59],a[240:243],v[168:171],a[56:59]
v_exp_f32 v221,v221
v_mfma_f32_16x16x32_bf16 a[56:59],a[244:247],v[172:175],a[56:59]
ds_read_b128 a[184:187],v1 offset:12800
v_cvt_pk_bf16_f32 v206,v220,v221
v_mfma_f32_16x16x32_bf16 a[60:63],a[252:255],v[172:175],a[60:63]
v_exp_f32 v222,v222
v_mfma_f32_16x16x32_bf16 a[60:63],a[248:251],v[168:171],a[60:63]
ds_read_b128 a[180:183],v1 offset:12544
)A"
R"A(v_smfmac_f32_16x16x64_bf16 v[52:55],v[28:31],v[168:175],v27
s_waitcnt lgkmcnt(4)
v_mfma_f32_16x16x32_bf16 v[136:139],a[128:131],v[64:67],v[32:35]
buffer_load_dwordx4 v5,s[20:23],s47 offen offset:2048 nt lds
v_mfma_f32_16x16x32_bf16 v[136:139],a[132:135],v[68:71],v[136:139]
ds_read_b128 a[176:179],v1 offset:12288
v_add_u32 v1,s48,v1
v_mfma_f32_16x16x32_bf16 v[136:139],a[136:139],v[72:75],v[136:139]
v_exp_f32 v223,v223
v_mfma_f32_16x16x32_bf16 v[136:139],a[140:143],v[76:79],v[136:139]
v_cvt_pk_bf16_f32 v207,v222,v223
v_mfma_f32_16x16x32_bf16 v[140:143],a[156:159],v[76:79],v[32:35]
v_exp_f32 v232,v232
v_mfma_f32_16x16x32_bf16 v[140:143],a[152:155],v[72:75],v[140:143]
v_exp_f32 v233,v233
v_mfma_f32_16x16x32_bf16 v[140:143],a[148:151],v[68:71],v[140:143]
v_exp_f32 v234,v234
v_mfma_f32_16x16x32_bf16 v[140:143],a[144:147],v[64:67],v[140:143]
v_exp_f32 v235,v235
v_mfma_f32_16x16x32_bf16 v[152:155],a[160:163],v[64:67],v[32:35]
v_exp_f32 v236,v236
v_mfma_f32_16x16x32_bf16 v[152:155],a[164:167],v[68:71],v[152:155]
v_exp_f32 v237,v237
v_mfma_f32_16x16x32_bf16 v[152:155],a[168:171],v[72:75],v[152:155]
s_waitcnt lgkmcnt(0)
v_exp_f32 v238,v238
v_mfma_f32_16x16x32_bf16 v[152:155],a[172:175],v[76:79],v[152:155]
v_exp_f32 v239,v239
v_mfma_f32_16x16x32_bf16 v[156:159],a[188:191],v[76:79],v[32:35]
v_exp_f32 v248,v248
v_mfma_f32_16x16x32_bf16 v[156:159],a[184:187],v[72:75],v[156:159]
buffer_load_dwordx4 v5,s[20:23],s47 offen offset:3072 nt lds
v_mfma_f32_16x16x32_bf16 v[156:159],a[180:183],v[68:71],v[156:159]
v_exp_f32 v249,v249
v_mfma_f32_16x16x32_bf16 v[156:159],a[176:179],v[64:67],v[156:159]
v_exp_f32 v250,v250
v_mfma_f32_16x16x32_bf16 a[64:67],a[192:195],v[200:203],a[64:67]
v_exp_f32 v251,v251
v_mfma_f32_16x16x32_bf16 a[64:67],a[196:199],v[204:207],a[64:67]
v_exp_f32 v252,v252
v_mfma_f32_16x16x32_bf16 a[68:71],a[204:207],v[204:207],a[68:71]
v_exp_f32 v253,v253
v_mfma_f32_16x16x32_bf16 a[68:71],a[200:203],v[200:203],a[68:71]
v_exp_f32 v254,v254
v_mfma_f32_16x16x32_bf16 a[72:75],a[208:211],v[200:203],a[72:75]
v_exp_f32 v255,v255
v_mfma_f32_16x16x32_bf16 a[72:75],a[212:215],v[204:207],a[72:75]
v_exp_f32 v136,v136
v_mfma_f32_16x16x32_bf16 a[76:79],a[220:223],v[204:207],a[76:79]
v_exp_f32 v137,v137
v_mfma_f32_16x16x32_bf16 a[76:79],a[216:219],v[200:203],a[76:79]
v_exp_f32 v138,v138
v_mfma_f32_16x16x32_bf16 a[80:83],a[224:227],v[200:203],a[80:83]
s_add_u32 m0,s49,s52
v_cvt_pk_bf16_f32 v232,v232,v233
v_cvt_pk_bf16_f32 v233,v234,v235
v_mfma_f32_16x16x32_bf16 a[80:83],a[228:231],v[204:207],a[80:83]
s_xor_b32 s49,s50,65536
v_cvt_pk_bf16_f32 v234,v236,v237
v_cvt_pk_bf16_f32 v235,v238,v239
v_mfma_f32_16x16x32_bf16 a[84:87],a[236:239],v[204:207],a[84:87]
v_cvt_pk_bf16_f32 v236,v248,v249
v_cvt_pk_bf16_f32 v237,v250,v251
v_mfma_f32_16x16x32_bf16 a[84:87],a[232:235],v[200:203],a[84:87]
buffer_load_dwordx4 v4,s[24:27],s47 offen nt lds
v_mfma_f32_16x16x32_bf16 a[88:91],a[240:243],v[200:203],a[88:91]
v_cvt_pk_bf16_f32 v238,v252,v253
v_cvt_pk_bf16_f32 v239,v254,v255
v_mfma_f32_16x16x32_bf16 a[88:91],a[244:247],v[204:207],a[88:91]
v_exp_f32 v139,v139
v_mfma_f32_16x16x32_bf16 a[92:95],a[252:255],v[204:207],a[92:95]
v_exp_f32 v140,v140
v_mfma_f32_16x16x32_bf16 a[92:95],a[248:251],v[200:203],a[92:95]
v_exp_f32 v141,v141
v_smfmac_f32_16x16x64_bf16 v[56:59],v[28:31],v[200:207],v27
v_mfma_f32_16x16x32_bf16 v[168:171],a[128:131],v[80:83],v[36:39]
v_exp_f32 v142,v142
v_mfma_f32_16x16x32_bf16 v[168:171],a[132:135],v[84:87],v[168:171]
v_exp_f32 v143,v143
v_mfma_f32_16x16x32_bf16 v[168:171],a[136:139],v[88:91],v[168:171]
v_exp_f32 v152,v152
v_mfma_f32_16x16x32_bf16 v[168:171],a[140:143],v[92:95],v[168:171]
v_exp_f32 v153,v153
v_mfma_f32_16x16x32_bf16 v[172:175],a[156:159],v[92:95],v[36:39]
v_exp_f32 v154,v154
v_mfma_f32_16x16x32_bf16 v[172:175],a[152:155],v[88:91],v[172:175]
v_exp_f32 v155,v155
v_mfma_f32_16x16x32_bf16 v[172:175],a[148:151],v[84:87],v[172:175]
v_exp_f32 v156,v156
v_mfma_f32_16x16x32_bf16 v[172:175],a[144:147],v[80:83],v[172:175]
v_exp_f32 v157,v157
v_mfma_f32_16x16x32_bf16 v[184:187],a[160:163],v[80:83],v[36:39]
buffer_load_dwordx4 v5,s[24:27],s47 offen offset:1024 nt lds
v_mfma_f32_16x16x32_bf16 v[184:187],a[164:167],v[84:87],v[184:187]
v_exp_f32 v158,v158
v_mfma_f32_16x16x32_bf16 v[184:187],a[168:171],v[88:91],v[184:187]
v_exp_f32 v159,v159
v_mfma_f32_16x16x32_bf16 v[184:187],a[172:175],v[92:95],v[184:187]
v_exp_f32 v168,v168
v_mfma_f32_16x16x32_bf16 v[188:191],a[188:191],v[92:95],v[36:39]
v_exp_f32 v169,v169
v_mfma_f32_16x16x32_bf16 v[188:191],a[184:187],v[88:91],v[188:191]
v_exp_f32 v170,v170
v_mfma_f32_16x16x32_bf16 v[188:191],a[180:183],v[84:87],v[188:191]
v_exp_f32 v171,v171
v_mfma_f32_16x16x32_bf16 v[188:191],a[176:179],v[80:83],v[188:191]
v_exp_f32 v172,v172
v_mfma_f32_16x16x32_bf16 a[96:99],a[192:195],v[232:235],a[96:99]
ds_read_b64_tr_b16 a[192:193],v2
v_cvt_pk_bf16_f32 v168,v168,v169
v_mfma_f32_16x16x32_bf16 a[96:99],a[196:199],v[236:239],a[96:99]
ds_read_b64_tr_b16 a[194:195],v2 offset:4096
v_cvt_pk_bf16_f32 v169,v170,v171
v_mfma_f32_16x16x32_bf16 a[100:103],a[204:207],v[236:239],a[100:103]
v_exp_f32 v173,v173
v_mfma_f32_16x16x32_bf16 a[100:103],a[200:203],v[232:235],a[100:103]
ds_read_b64_tr_b16 a[196:197],v2 offset:8192
v_cvt_pk_bf16_f32 v170,v172,v173
v_mfma_f32_16x16x32_bf16 a[104:107],a[208:211],v[232:235],a[104:107]
v_exp_f32 v174,v174
v_mfma_f32_16x16x32_bf16 a[104:107],a[212:215],v[236:239],a[104:107]
v_exp_f32 v175,v175
v_mfma_f32_16x16x32_bf16 a[108:111],a[220:223],v[236:239],a[108:111]
buffer_load_dwordx4 v4,s[24:27],s47 offen offset:2048 nt lds
v_mfma_f32_16x16x32_bf16 a[108:111],a[216:219],v[232:235],a[108:111]
ds_read_b64_tr_b16 a[198:199],v2 offset:12288
v_cvt_pk_bf16_f32 v171,v174,v175
v_mfma_f32_16x16x32_bf16 a[112:115],a[224:227],v[232:235],a[112:115]
v_exp_f32 v184,v184
v_mfma_f32_16x16x32_bf16 a[112:115],a[228:231],v[236:239],a[112:115]
v_exp_f32 v185,v185
v_mfma_f32_16x16x32_bf16 a[116:119],a[236:239],v[236:239],a[116:119]
ds_read_b64_tr_b16 a[204:205],v3 offset:8192
v_cvt_pk_bf16_f32 v136,v136,v137
v_mfma_f32_16x16x32_bf16 a[116:119],a[232:235],v[232:235],a[116:119]
ds_read_b64_tr_b16 a[206:207],v3 offset:12288
v_cvt_pk_bf16_f32 v172,v184,v185
v_mfma_f32_16x16x32_bf16 a[120:123],a[240:243],v[232:235],a[120:123]
ds_read_b64_tr_b16 a[200:201],v3
v_mfma_f32_16x16x32_bf16 a[120:123],a[244:247],v[236:239],a[120:123]
ds_read_b64_tr_b16 a[202:203],v3 offset:4096
v_cvt_pk_bf16_f32 v137,v138,v139
v_mfma_f32_16x16x32_bf16 a[124:127],a[252:255],v[236:239],a[124:127]
ds_read_b64_tr_b16 a[208:209],v2 offset:256
v_mfma_f32_16x16x32_bf16 a[124:127],a[248:251],v[232:235],a[124:127]
ds_read_b64_tr_b16 a[210:211],v2 offset:4352
v_smfmac_f32_16x16x64_bf16 v[60:63],v[28:31],v[232:239],v27
ds_read_b64_tr_b16 a[212:213],v2 offset:8448
v_mfma_f32_16x16x32_bf16 v[200:203],a[128:131],v[96:99],v[40:43]
ds_read_b64_tr_b16 a[214:215],v2 offset:12544
v_cvt_pk_bf16_f32 v138,v140,v141
v_mfma_f32_16x16x32_bf16 v[200:203],a[132:135],v[100:103],v[200:203]
ds_read_b64_tr_b16 a[220:221],v3 offset:8448
v_mfma_f32_16x16x32_bf16 v[200:203],a[136:139],v[104:107],v[200:203]
ds_read_b64_tr_b16 a[222:223],v3 offset:12544
v_cvt_pk_bf16_f32 v139,v142,v143
v_mfma_f32_16x16x32_bf16 v[200:203],a[140:143],v[108:111],v[200:203]
buffer_load_dwordx4 v5,s[24:27],s47 offen offset:3072 nt lds
v_mfma_f32_16x16x32_bf16 v[204:207],a[156:159],v[108:111],v[40:43]
s_or_b32 s47,s47,0x18000
s_add_u32 s47,s47,4096
ds_read_b64_tr_b16 a[216:217],v3 offset:256
v_mfma_f32_16x16x32_bf16 v[204:207],a[152:155],v[104:107],v[204:207]
s_and_b32 s47,s47,0x27000
ds_read_b64_tr_b16 a[218:219],v3 offset:4352
v_cvt_pk_bf16_f32 v140,v152,v153
v_mfma_f32_16x16x32_bf16 v[204:207],a[148:151],v[100:103],v[204:207]
s_or_b32 s47,s47,s46
ds_read_b64_tr_b16 a[224:225],v2 offset:512
v_mfma_f32_16x16x32_bf16 v[204:207],a[144:147],v[96:99],v[204:207]
ds_read_b64_tr_b16 a[226:227],v2 offset:4608
v_mfma_f32_16x16x32_bf16 v[216:219],a[160:163],v[96:99],v[40:43]
ds_read_b64_tr_b16 a[228:229],v2 offset:8704
v_cvt_pk_bf16_f32 v141,v154,v155
v_mfma_f32_16x16x32_bf16 v[216:219],a[164:167],v[100:103],v[216:219]
ds_read_b64_tr_b16 a[230:231],v2 offset:12800
v_mfma_f32_16x16x32_bf16 v[216:219],a[168:171],v[104:107],v[216:219]
ds_read_b64_tr_b16 a[236:237],v3 offset:8704
v_mfma_f32_16x16x32_bf16 v[216:219],a[172:175],v[108:111],v[216:219]
ds_read_b64_tr_b16 a[238:239],v3 offset:12800
v_cvt_pk_bf16_f32 v142,v156,v157
v_mfma_f32_16x16x32_bf16 v[220:223],a[188:191],v[108:111],v[40:43]
ds_read_b64_tr_b16 a[232:233],v3 offset:512
v_mfma_f32_16x16x32_bf16 v[220:223],a[184:187],v[104:107],v[220:223]
ds_read_b64_tr_b16 a[234:235],v3 offset:4608
v_mfma_f32_16x16x32_bf16 v[220:223],a[180:183],v[100:103],v[220:223]
ds_read_b64_tr_b16 a[240:241],v2 offset:768
v_cvt_pk_bf16_f32 v143,v158,v159
v_mfma_f32_16x16x32_bf16 v[220:223],a[176:179],v[96:99],v[220:223]
s_waitcnt lgkmcnt(7)
buffer_load_dword v26,v24,s[88:91],s93 offen
v_mfma_f32_16x16x32_bf16 a[0:3],a[192:195],v[136:139],a[0:3]
ds_read_b64_tr_b16 a[242:243],v2 offset:4864
v_mfma_f32_16x16x32_bf16 a[0:3],a[196:199],v[140:143],a[0:3]
ds_read_b64_tr_b16 a[244:245],v2 offset:8960
v_mfma_f32_16x16x32_bf16 a[4:7],a[204:207],v[140:143],a[4:7]
ds_read_b64_tr_b16 a[246:247],v2 offset:13056
v_mfma_f32_16x16x32_bf16 a[4:7],a[200:203],v[136:139],a[4:7]
s_memrealtime s[76:77]
ds_read_b64_tr_b16 a[252:253],v3 offset:8960
v_mfma_f32_16x16x32_bf16 a[8:11],a[208:211],v[136:139],a[8:11]
ds_read_b64_tr_b16 a[254:255],v3 offset:13056
v_mfma_f32_16x16x32_bf16 a[8:11],a[212:215],v[140:143],a[8:11]
ds_read_b64_tr_b16 a[248:249],v3 offset:768
v_mfma_f32_16x16x32_bf16 a[12:15],a[220:223],v[140:143],a[12:15]
ds_read_b64_tr_b16 a[250:251],v3 offset:4864
v_mfma_f32_16x16x32_bf16 a[12:15],a[216:219],v[136:139],a[12:15]
s_bitcmp1_b32 s45,0
s_cbranch_scc0 L_t310_%=
s_mov_b32 m0,s80
s_add_u32 s80,s80,4096
buffer_load_dwordx4 v25,s[20:23],s83 offen nt lds
s_add_u32 s83,s83,256
L_t310_%=:
v_mfma_f32_16x16x32_bf16 a[16:19],a[224:227],v[136:139],a[16:19]
s_waitcnt lgkmcnt(6)
v_exp_f32 v186,v186
v_mfma_f32_16x16x32_bf16 a[16:19],a[228:231],v[140:143],a[16:19]
v_exp_f32 v187,v187
v_mfma_f32_16x16x32_bf16 a[20:23],a[236:239],v[140:143],a[20:23]
s_waitcnt lgkmcnt(0)
v_exp_f32 v188,v188
v_mfma_f32_16x16x32_bf16 a[20:23],a[232:235],v[136:139],a[20:23]
s_sub_u32 s79,s78,s76
v_exp_f32 v189,v189
v_mfma_f32_16x16x32_bf16 a[24:27],a[240:243],v[136:139],a[24:27]
s_add_u32 s78,s76,s60
v_exp_f32 v190,v190
v_mfma_f32_16x16x32_bf16 a[24:27],a[244:247],v[140:143],a[24:27]
s_cmp_gt_i32 s79,s95
s_cbranch_scc1 L_ps3_%=
L_pr3_%=:
v_cvt_pk_bf16_f32 v173,v186,v187
v_mfma_f32_16x16x32_bf16 a[28:31],a[252:255],v[140:143],a[28:31]
v_exp_f32 v191,v191
v_mfma_f32_16x16x32_bf16 a[28:31],a[248:251],v[136:139],a[28:31]
v_cvt_pk_bf16_f32 v174,v188,v189
v_cvt_pk_bf16_f32 v175,v190,v191
s_cmp_lg_u32 s53,0
v_smfmac_f32_16x16x64_bf16 v[48:51],v[28:31],v[136:143],v27
s_cbranch_scc1 L_att0_%=
L_att0x_%=:
v_mfma_f32_16x16x32_bf16 v[232:235],a[128:131],v[112:115],v[44:47]
s_waitcnt vmcnt(10)
s_sub_u32 s94,s94,3
v_exp_f32 v200,v200
v_mfma_f32_16x16x32_bf16 v[232:235],a[132:135],v[116:119],v[232:235]
s_barrier
s_add_u32 m0,0,s51
ds_read_b128 a[128:131],v1
v_mfma_f32_16x16x32_bf16 v[232:235],a[136:139],v[120:123],v[232:235]
s_lshr_b32 s60,s94,2
v_exp_f32 v201,v201
v_mfma_f32_16x16x32_bf16 v[232:235],a[140:143],v[124:127],v[232:235]
s_max_i32 s60,s60,128
ds_read_b128 a[132:135],v1 offset:256
v_cvt_pk_bf16_f32 v200,v200,v201
v_mfma_f32_16x16x32_bf16 v[236:239],a[156:159],v[124:127],v[44:47]
s_add_u32 s54,s54,16384
buffer_load_dwordx4 v4,s[20:23],s47 offen nt lds
v_mfma_f32_16x16x32_bf16 v[236:239],a[152:155],v[120:123],v[236:239]
s_and_b32 s46,s54,0xfffc0000
ds_read_b128 a[136:139],v1 offset:512
v_and_b32 v18,48,v6
v_mfma_f32_16x16x32_bf16 v[236:239],a[148:151],v[116:119],v[236:239]
s_sub_u32 s94,s94,3
v_exp_f32 v202,v202
v_mfma_f32_16x16x32_bf16 v[236:239],a[144:147],v[112:115],v[236:239]
s_add_u32 s54,s54,16384
ds_read_b128 a[140:143],v1 offset:768
v_lshrrev_b32 v19,4,v6
v_mfma_f32_16x16x32_bf16 v[248:251],a[160:163],v[112:115],v[44:47]
)A"
R"A(s_mul_i32 s83,s41,5
v_exp_f32 v203,v203
v_mfma_f32_16x16x32_bf16 v[248:251],a[164:167],v[116:119],v[248:251]
s_lshl_b32 s43,s40,8
ds_read_b128 a[156:159],v1 offset:4864
v_cvt_pk_bf16_f32 v201,v202,v203
v_mfma_f32_16x16x32_bf16 v[248:251],a[168:171],v[120:123],v[248:251]
s_add_u32 s83,s83,s42
v_exp_f32 v204,v204
v_mfma_f32_16x16x32_bf16 v[248:251],a[172:175],v[124:127],v[248:251]
s_add_u32 s83,s83,15
ds_read_b128 a[152:155],v1 offset:4608
v_lshlrev_b32 v18,2,v18
v_mfma_f32_16x16x32_bf16 v[252:255],a[188:191],v[124:127],v[44:47]
s_lshl_b32 s44,s40,5
v_exp_f32 v205,v205
v_mfma_f32_16x16x32_bf16 v[252:255],a[184:187],v[120:123],v[252:255]
s_add_u32 s84,s55,0
ds_read_b128 a[148:151],v1 offset:4352
v_cvt_pk_bf16_f32 v202,v204,v205
v_mfma_f32_16x16x32_bf16 v[252:255],a[180:183],v[116:119],v[252:255]
s_add_u32 s85,s55,4096
v_exp_f32 v206,v206
v_mfma_f32_16x16x32_bf16 v[252:255],a[176:179],v[112:115],v[252:255]
s_add_u32 s86,s55,8192
ds_read_b128 a[144:147],v1 offset:4096
v_and_b32 v19,48,v19
v_mfma_f32_16x16x32_bf16 a[32:35],a[192:195],v[168:171],a[32:35]
s_add_u32 s87,s55,12288
v_exp_f32 v207,v207
v_mfma_f32_16x16x32_bf16 a[32:35],a[196:199],v[172:175],a[32:35]
s_and_b32 s44,s44,32
ds_read_b128 a[160:163],v1 offset:8192
v_cvt_pk_bf16_f32 v203,v206,v207
v_mfma_f32_16x16x32_bf16 a[36:39],a[204:207],v[172:175],a[36:39]
s_max_i32 s84,s84,s18
buffer_load_dwordx4 v4,s[20:23],s47 offen offset:1024 nt lds
v_mfma_f32_16x16x32_bf16 a[36:39],a[200:203],v[168:171],a[36:39]
s_max_i32 s85,s85,s19
ds_read_b128 a[164:167],v1 offset:8448
v_add_u32 v18,v18,v19
v_mfma_f32_16x16x32_bf16 a[40:43],a[208:211],v[168:171],a[40:43]
s_max_i32 s86,s86,s100
v_exp_f32 v216,v216
v_mfma_f32_16x16x32_bf16 a[40:43],a[212:215],v[172:175],a[40:43]
s_max_i32 s87,s87,s101
ds_read_b128 a[168:171],v1 offset:8704
v_add_u32 v18,98304,v18
v_mfma_f32_16x16x32_bf16 a[44:47],a[220:223],v[172:175],a[44:47]
s_lshl_b32 s80,s40,10
v_exp_f32 v217,v217
v_mfma_f32_16x16x32_bf16 a[44:47],a[216:219],v[168:171],a[44:47]
s_mov_b32 s50,32768
ds_read_b128 a[172:175],v1 offset:8960
v_cvt_pk_bf16_f32 v204,v216,v217
v_mfma_f32_16x16x32_bf16 a[48:51],a[224:227],v[168:171],a[48:51]
s_mov_b32 s49,98304
v_exp_f32 v218,v218
v_mfma_f32_16x16x32_bf16 a[48:51],a[228:231],v[172:175],a[48:51]
s_mov_b32 s45,0x2a8
ds_read_b128 a[188:191],v1 offset:13056
v_add_u32 v2,s48,v2
v_mfma_f32_16x16x32_bf16 a[52:55],a[236:239],v[172:175],a[52:55]
s_add_u32 s93,s92,L_endseg_%=-L_pc_%=
v_exp_f32 v219,v219
v_mfma_f32_16x16x32_bf16 a[52:55],a[232:235],v[168:171],a[52:55]
ds_read_b128 a[184:187],v1 offset:12800
v_cvt_pk_bf16_f32 v205,v218,v219
v_mfma_f32_16x16x32_bf16 a[56:59],a[240:243],v[168:171],a[56:59]
v_exp_f32 v220,v220
v_mfma_f32_16x16x32_bf16 a[56:59],a[244:247],v[172:175],a[56:59]
ds_read_b128 a[180:183],v1 offset:12544
v_add_u32 v3,s48,v3
v_mfma_f32_16x16x32_bf16 a[60:63],a[252:255],v[172:175],a[60:63]
s_mov_b32 s48,32768
v_exp_f32 v221,v221
v_mfma_f32_16x16x32_bf16 a[60:63],a[248:251],v[168:171],a[60:63]
ds_read_b128 a[176:179],v1 offset:12288
v_cvt_pk_bf16_f32 v206,v220,v221
v_smfmac_f32_16x16x64_bf16 v[52:55],v[28:31],v[168:175],v27
s_waitcnt lgkmcnt(4)
v_add_u32 v1,s48,v1
v_mfma_f32_16x16x32_bf16 v[136:139],a[128:131],v[64:67],v[32:35]
buffer_load_dwordx4 v5,s[20:23],s47 offen offset:2048 nt lds
v_mfma_f32_16x16x32_bf16 v[136:139],a[132:135],v[68:71],v[136:139]
v_exp_f32 v222,v222
v_mfma_f32_16x16x32_bf16 v[136:139],a[136:139],v[72:75],v[136:139]
v_exp_f32 v223,v223
v_mfma_f32_16x16x32_bf16 v[136:139],a[140:143],v[76:79],v[136:139]
v_cvt_pk_bf16_f32 v207,v222,v223
v_subrev_u32 v25,s43,v4
v_mfma_f32_16x16x32_bf16 v[140:143],a[156:159],v[76:79],v[32:35]
s_lshl_b32 s43,s83,14
v_exp_f32 v232,v232
v_mfma_f32_16x16x32_bf16 v[140:143],a[152:155],v[72:75],v[140:143]
s_lshl_b32 s83,s83,12
v_exp_f32 v233,v233
v_mfma_f32_16x16x32_bf16 v[140:143],a[148:151],v[68:71],v[140:143]
s_and_b32 s43,s43,0x20000
v_exp_f32 v234,v234
v_mfma_f32_16x16x32_bf16 v[140:143],a[144:147],v[64:67],v[140:143]
s_and_b32 s83,s83,0x7000
v_exp_f32 v235,v235
v_mfma_f32_16x16x32_bf16 v[152:155],a[160:163],v[64:67],v[32:35]
s_or_b32 s83,s83,s43
v_cvt_pk_bf16_f32 v232,v232,v233
v_cvt_pk_bf16_f32 v233,v234,v235
v_mfma_f32_16x16x32_bf16 v[152:155],a[164:167],v[68:71],v[152:155]
s_or_b32 s83,s83,0x40000
v_exp_f32 v236,v236
v_mfma_f32_16x16x32_bf16 v[152:155],a[168:171],v[72:75],v[152:155]
v_exp_f32 v237,v237
v_mfma_f32_16x16x32_bf16 v[152:155],a[172:175],v[76:79],v[152:155]
s_waitcnt lgkmcnt(0)
v_exp_f32 v238,v238
v_mfma_f32_16x16x32_bf16 v[156:159],a[188:191],v[76:79],v[32:35]
v_exp_f32 v239,v239
v_mfma_f32_16x16x32_bf16 v[156:159],a[184:187],v[72:75],v[156:159]
buffer_load_dwordx4 v5,s[20:23],s47 offen offset:3072 nt lds
v_mfma_f32_16x16x32_bf16 v[156:159],a[180:183],v[68:71],v[156:159]
v_cvt_pk_bf16_f32 v234,v236,v237
v_cvt_pk_bf16_f32 v235,v238,v239
v_mfma_f32_16x16x32_bf16 v[156:159],a[176:179],v[64:67],v[156:159]
v_exp_f32 v248,v248
v_mfma_f32_16x16x32_bf16 a[64:67],a[192:195],v[200:203],a[64:67]
v_exp_f32 v249,v249
v_mfma_f32_16x16x32_bf16 a[64:67],a[196:199],v[204:207],a[64:67]
v_exp_f32 v250,v250
v_mfma_f32_16x16x32_bf16 a[68:71],a[204:207],v[204:207],a[68:71]
v_exp_f32 v251,v251
v_mfma_f32_16x16x32_bf16 a[68:71],a[200:203],v[200:203],a[68:71]
v_exp_f32 v252,v252
v_mfma_f32_16x16x32_bf16 a[72:75],a[208:211],v[200:203],a[72:75]
v_exp_f32 v253,v253
v_mfma_f32_16x16x32_bf16 a[72:75],a[212:215],v[204:207],a[72:75]
v_exp_f32 v254,v254
v_mfma_f32_16x16x32_bf16 a[76:79],a[220:223],v[204:207],a[76:79]
v_exp_f32 v255,v255
v_mfma_f32_16x16x32_bf16 a[76:79],a[216:219],v[200:203],a[76:79]
v_cvt_pk_bf16_f32 v236,v248,v249
v_cvt_pk_bf16_f32 v237,v250,v251
v_mfma_f32_16x16x32_bf16 a[80:83],a[224:227],v[200:203],a[80:83]
s_add_u32 m0,0,s52
v_cvt_pk_bf16_f32 v238,v252,v253
v_cvt_pk_bf16_f32 v239,v254,v255
v_mfma_f32_16x16x32_bf16 a[80:83],a[228:231],v[204:207],a[80:83]
v_exp_f32 v136,v136
v_mfma_f32_16x16x32_bf16 a[84:87],a[236:239],v[204:207],a[84:87]
v_exp_f32 v137,v137
v_mfma_f32_16x16x32_bf16 a[84:87],a[232:235],v[200:203],a[84:87]
buffer_load_dwordx4 v4,s[24:27],s47 offen nt lds
v_mfma_f32_16x16x32_bf16 a[88:91],a[240:243],v[200:203],a[88:91]
v_exp_f32 v138,v138
v_mfma_f32_16x16x32_bf16 a[88:91],a[244:247],v[204:207],a[88:91]
v_exp_f32 v139,v139
v_mfma_f32_16x16x32_bf16 a[92:95],a[252:255],v[204:207],a[92:95]
v_cvt_pk_bf16_f32 v136,v136,v137
v_cvt_pk_bf16_f32 v137,v138,v139
v_mfma_f32_16x16x32_bf16 a[92:95],a[248:251],v[200:203],a[92:95]
v_exp_f32 v140,v140
v_smfmac_f32_16x16x64_bf16 v[56:59],v[28:31],v[200:207],v27
v_xor_b32 v25,s44,v25
v_mfma_f32_16x16x32_bf16 v[168:171],a[128:131],v[80:83],v[36:39]
v_exp_f32 v141,v141
v_mfma_f32_16x16x32_bf16 v[168:171],a[132:135],v[84:87],v[168:171]
v_exp_f32 v142,v142
v_mfma_f32_16x16x32_bf16 v[168:171],a[136:139],v[88:91],v[168:171]
v_exp_f32 v143,v143
v_mfma_f32_16x16x32_bf16 v[168:171],a[140:143],v[92:95],v[168:171]
v_cvt_pk_bf16_f32 v138,v140,v141
v_cvt_pk_bf16_f32 v139,v142,v143
v_mfma_f32_16x16x32_bf16 v[172:175],a[156:159],v[92:95],v[36:39]
v_exp_f32 v152,v152
v_mfma_f32_16x16x32_bf16 v[172:175],a[152:155],v[88:91],v[172:175]
v_exp_f32 v153,v153
v_mfma_f32_16x16x32_bf16 v[172:175],a[148:151],v[84:87],v[172:175]
v_exp_f32 v154,v154
v_mfma_f32_16x16x32_bf16 v[172:175],a[144:147],v[80:83],v[172:175]
v_exp_f32 v155,v155
v_mfma_f32_16x16x32_bf16 v[184:187],a[160:163],v[80:83],v[36:39]
buffer_load_dwordx4 v5,s[24:27],s47 offen offset:1024 nt lds
v_mfma_f32_16x16x32_bf16 v[184:187],a[164:167],v[84:87],v[184:187]
v_exp_f32 v156,v156
v_mfma_f32_16x16x32_bf16 v[184:187],a[168:171],v[88:91],v[184:187]
v_exp_f32 v157,v157
v_mfma_f32_16x16x32_bf16 v[184:187],a[172:175],v[92:95],v[184:187]
v_exp_f32 v158,v158
v_mfma_f32_16x16x32_bf16 v[188:191],a[188:191],v[92:95],v[36:39]
v_exp_f32 v159,v159
v_mfma_f32_16x16x32_bf16 v[188:191],a[184:187],v[88:91],v[188:191]
v_cvt_pk_bf16_f32 v140,v152,v153
v_cvt_pk_bf16_f32 v141,v154,v155
v_mfma_f32_16x16x32_bf16 v[188:191],a[180:183],v[84:87],v[188:191]
s_waitcnt vmcnt(14)
v_cvt_pk_bf16_f32 v142,v156,v157
v_cvt_pk_bf16_f32 v143,v158,v159
v_mfma_f32_16x16x32_bf16 v[188:191],a[176:179],v[80:83],v[188:191]
s_barrier
ds_read_b128 v[80:83],v18
v_add_u32 v25,s80,v25
v_mfma_f32_16x16x32_bf16 a[96:99],a[192:195],v[232:235],a[96:99]
s_add_u32 s80,s80,147456
v_exp_f32 v168,v168
v_mfma_f32_16x16x32_bf16 a[96:99],a[196:199],v[236:239],a[96:99]
v_exp_f32 v169,v169
v_mfma_f32_16x16x32_bf16 a[100:103],a[204:207],v[236:239],a[100:103]
ds_read_b128 v[84:87],v18 offset:256
v_cvt_pk_bf16_f32 v168,v168,v169
v_mfma_f32_16x16x32_bf16 a[100:103],a[200:203],v[232:235],a[100:103]
v_exp_f32 v170,v170
v_mfma_f32_16x16x32_bf16 a[104:107],a[208:211],v[232:235],a[104:107]
v_exp_f32 v171,v171
v_mfma_f32_16x16x32_bf16 a[104:107],a[212:215],v[236:239],a[104:107]
buffer_load_dwordx4 v4,s[24:27],s47 offen offset:2048 nt lds
v_mfma_f32_16x16x32_bf16 a[108:111],a[220:223],v[236:239],a[108:111]
ds_read_b128 v[88:91],v18 offset:512
v_cvt_pk_bf16_f32 v169,v170,v171
v_mfma_f32_16x16x32_bf16 a[108:111],a[216:219],v[232:235],a[108:111]
v_exp_f32 v172,v172
v_mfma_f32_16x16x32_bf16 a[112:115],a[224:227],v[232:235],a[112:115]
v_exp_f32 v173,v173
v_mfma_f32_16x16x32_bf16 a[112:115],a[228:231],v[236:239],a[112:115]
ds_read_b128 v[92:95],v18 offset:768
v_cvt_pk_bf16_f32 v170,v172,v173
v_mfma_f32_16x16x32_bf16 a[116:119],a[236:239],v[236:239],a[116:119]
v_exp_f32 v174,v174
v_mfma_f32_16x16x32_bf16 a[116:119],a[232:235],v[232:235],a[116:119]
ds_read_b64_tr_b16 a[192:193],v2
ds_read_b64_tr_b16 a[194:195],v2 offset:4096
v_mfma_f32_16x16x32_bf16 a[120:123],a[240:243],v[232:235],a[120:123]
v_exp_f32 v175,v175
v_mfma_f32_16x16x32_bf16 a[120:123],a[244:247],v[236:239],a[120:123]
ds_read_b64_tr_b16 a[196:197],v2 offset:8192
ds_read_b64_tr_b16 a[198:199],v2 offset:12288
v_mfma_f32_16x16x32_bf16 a[124:127],a[252:255],v[236:239],a[124:127]
ds_read_b64_tr_b16 a[204:205],v3 offset:8192
v_cvt_pk_bf16_f32 v171,v174,v175
v_mfma_f32_16x16x32_bf16 a[124:127],a[248:251],v[232:235],a[124:127]
v_exp_f32 v184,v184
v_smfmac_f32_16x16x64_bf16 v[60:63],v[28:31],v[232:239],v27
ds_read_b64_tr_b16 a[206:207],v3 offset:12288
v_mfma_f32_16x16x32_bf16 v[200:203],a[128:131],v[96:99],v[40:43]
ds_read_b64_tr_b16 a[200:201],v3
ds_read_b64_tr_b16 a[202:203],v3 offset:4096
v_mfma_f32_16x16x32_bf16 v[200:203],a[132:135],v[100:103],v[200:203]
v_exp_f32 v185,v185
v_mfma_f32_16x16x32_bf16 v[200:203],a[136:139],v[104:107],v[200:203]
buffer_load_dwordx4 v5,s[24:27],s47 offen offset:3072 nt lds
v_mfma_f32_16x16x32_bf16 v[200:203],a[140:143],v[108:111],v[200:203]
s_or_b32 s47,s47,0x18000
ds_read_b64_tr_b16 a[208:209],v2 offset:256
ds_read_b64_tr_b16 a[210:211],v2 offset:4352
v_mfma_f32_16x16x32_bf16 v[204:207],a[156:159],v[108:111],v[40:43]
s_add_u32 s47,s47,4096
s_and_b32 s47,s47,0x27000
ds_read_b64_tr_b16 a[212:213],v2 offset:8448
v_mfma_f32_16x16x32_bf16 v[204:207],a[152:155],v[104:107],v[204:207]
s_or_b32 s47,s47,s46
ds_read_b64_tr_b16 a[214:215],v2 offset:12544
v_cvt_pk_bf16_f32 v172,v184,v185
v_mfma_f32_16x16x32_bf16 v[204:207],a[148:151],v[100:103],v[204:207]
s_and_b32 s46,s54,0xfffc0000
v_exp_f32 v186,v186
v_mfma_f32_16x16x32_bf16 v[204:207],a[144:147],v[96:99],v[204:207]
s_add_u32 s54,s54,16384
ds_read_b64_tr_b16 a[220:221],v3 offset:8448
ds_read_b64_tr_b16 a[222:223],v3 offset:12544
v_mfma_f32_16x16x32_bf16 v[216:219],a[160:163],v[96:99],v[40:43]
v_exp_f32 v187,v187
v_mfma_f32_16x16x32_bf16 v[216:219],a[164:167],v[100:103],v[216:219]
ds_read_b64_tr_b16 a[216:217],v3 offset:256
ds_read_b64_tr_b16 a[218:219],v3 offset:4352
v_mfma_f32_16x16x32_bf16 v[216:219],a[168:171],v[104:107],v[216:219]
ds_read_b64_tr_b16 a[224:225],v2 offset:512
v_cvt_pk_bf16_f32 v173,v186,v187
v_mfma_f32_16x16x32_bf16 v[216:219],a[172:175],v[108:111],v[216:219]
ds_read_b64_tr_b16 a[226:227],v2 offset:4608
v_mfma_f32_16x16x32_bf16 v[220:223],a[188:191],v[108:111],v[40:43]
ds_read_b64_tr_b16 a[228:229],v2 offset:8704
v_mfma_f32_16x16x32_bf16 v[220:223],a[184:187],v[104:107],v[220:223]
ds_read_b64_tr_b16 a[230:231],v2 offset:12800
ds_read_b64_tr_b16 a[236:237],v3 offset:8704
)A"
R"A(v_mfma_f32_16x16x32_bf16 v[220:223],a[180:183],v[100:103],v[220:223]
ds_read_b64_tr_b16 a[238:239],v3 offset:12800
v_mfma_f32_16x16x32_bf16 v[220:223],a[176:179],v[96:99],v[220:223]
s_waitcnt lgkmcnt(8)
ds_read_b64_tr_b16 a[232:233],v3 offset:512
v_mfma_f32_16x16x32_bf16 v[32:35],v[80:83],v[128:131],0
ds_read_b64_tr_b16 a[234:235],v3 offset:4608
v_mfma_f32_16x16x32_bf16 v[32:35],v[84:87],v[160:163],v[32:35]
ds_read_b64_tr_b16 a[240:241],v2 offset:768
ds_read_b64_tr_b16 a[242:243],v2 offset:4864
v_mfma_f32_16x16x32_bf16 v[32:35],v[88:91],v[192:195],v[32:35]
ds_read_b64_tr_b16 a[244:245],v2 offset:8960
v_mfma_f32_16x16x32_bf16 v[32:35],v[92:95],v[224:227],v[32:35]
ds_read_b64_tr_b16 a[246:247],v2 offset:13056
v_add_u32 v2,s48,v2
v_mfma_f32_16x16x32_bf16 v[36:39],v[80:83],v[132:135],0
ds_read_b64_tr_b16 a[252:253],v3 offset:8960
v_mfma_f32_16x16x32_bf16 v[36:39],v[84:87],v[164:167],v[36:39]
ds_read_b64_tr_b16 a[254:255],v3 offset:13056
ds_read_b64_tr_b16 a[248:249],v3 offset:768
v_mfma_f32_16x16x32_bf16 v[36:39],v[88:91],v[196:199],v[36:39]
ds_read_b64_tr_b16 a[250:251],v3 offset:4864
v_add_u32 v3,s48,v3
v_mfma_f32_16x16x32_bf16 v[36:39],v[92:95],v[228:231],v[36:39]
s_mov_b32 s48,-98304
v_exp_f32 v188,v188
v_mfma_f32_16x16x32_bf16 v[40:43],v[80:83],v[144:147],0
v_exp_f32 v189,v189
v_mfma_f32_16x16x32_bf16 v[40:43],v[84:87],v[176:179],v[40:43]
v_exp_f32 v190,v190
v_mfma_f32_16x16x32_bf16 v[40:43],v[88:91],v[208:211],v[40:43]
v_exp_f32 v191,v191
v_mfma_f32_16x16x32_bf16 v[40:43],v[92:95],v[240:243],v[40:43]
v_cvt_pk_bf16_f32 v174,v188,v189
v_cvt_pk_bf16_f32 v175,v190,v191
v_mfma_f32_16x16x32_bf16 a[0:3],a[192:195],v[136:139],a[0:3]
v_exp_f32 v200,v200
v_mfma_f32_16x16x32_bf16 a[0:3],a[196:199],v[140:143],a[0:3]
v_max3_f32 v32,v32,v33,v34
v_max3_f32 v32,v32,v35,s18
v_mfma_f32_16x16x32_bf16 a[4:7],a[204:207],v[140:143],a[4:7]
v_sub_f32 v35,0,v32
v_sub_f32 v34,0,v32
v_mfma_f32_16x16x32_bf16 a[4:7],a[200:203],v[136:139],a[4:7]
s_memrealtime s[76:77]
v_sub_f32 v33,0,v32
v_sub_f32 v32,0,v32
v_mfma_f32_16x16x32_bf16 a[8:11],a[208:211],v[136:139],a[8:11]
v_exp_f32 v201,v201
v_mfma_f32_16x16x32_bf16 a[8:11],a[212:215],v[140:143],a[8:11]
v_exp_f32 v202,v202
v_mfma_f32_16x16x32_bf16 a[12:15],a[220:223],v[140:143],a[12:15]
s_waitcnt lgkmcnt(0)
v_exp_f32 v203,v203
v_mfma_f32_16x16x32_bf16 a[12:15],a[216:219],v[136:139],a[12:15]
v_cvt_pk_bf16_f32 v200,v200,v201
v_cvt_pk_bf16_f32 v201,v202,v203
v_mfma_f32_16x16x32_bf16 a[16:19],a[224:227],v[136:139],a[16:19]
v_exp_f32 v204,v204
v_mfma_f32_16x16x32_bf16 a[16:19],a[228:231],v[140:143],a[16:19]
v_exp_f32 v205,v205
v_mfma_f32_16x16x32_bf16 a[20:23],a[236:239],v[140:143],a[20:23]
s_waitcnt lgkmcnt(0)
v_exp_f32 v206,v206
v_mfma_f32_16x16x32_bf16 a[20:23],a[232:235],v[136:139],a[20:23]
s_sub_u32 s79,s78,s76
v_exp_f32 v207,v207
v_mfma_f32_16x16x32_bf16 a[24:27],a[240:243],v[136:139],a[24:27]
s_add_u32 s78,s76,s60
v_cvt_pk_bf16_f32 v202,v204,v205
v_cvt_pk_bf16_f32 v203,v206,v207
v_mfma_f32_16x16x32_bf16 a[24:27],a[244:247],v[140:143],a[24:27]
s_cmp_gt_i32 s79,s95
s_cbranch_scc1 L_ps4_%=
L_pr4_%=:
v_max3_f32 v36,v36,v37,v38
v_mfma_f32_16x16x32_bf16 a[28:31],a[252:255],v[140:143],a[28:31]
s_lshr_b32 s60,s94,2
v_exp_f32 v216,v216
v_mfma_f32_16x16x32_bf16 a[28:31],a[248:251],v[136:139],a[28:31]
s_max_i32 s60,s60,128
v_exp_f32 v217,v217
v_smfmac_f32_16x16x64_bf16 v[48:51],v[28:31],v[136:143],v27
s_sub_u32 s94,s94,3
v_cvt_pk_bf16_f32 v204,v216,v217
v_mfma_f32_16x16x32_bf16 v[232:235],a[128:131],v[112:115],v[44:47]
s_waitcnt vmcnt(9)
v_exp_f32 v218,v218
v_mfma_f32_16x16x32_bf16 v[232:235],a[132:135],v[116:119],v[232:235]
s_barrier
s_add_u32 m0,32768,s51
ds_read_b128 a[128:131],v1
v_mfma_f32_16x16x32_bf16 v[232:235],a[136:139],v[120:123],v[232:235]
v_exp_f32 v219,v219
v_mfma_f32_16x16x32_bf16 v[232:235],a[140:143],v[124:127],v[232:235]
ds_read_b128 a[132:135],v1 offset:256
v_cvt_pk_bf16_f32 v205,v218,v219
v_mfma_f32_16x16x32_bf16 v[236:239],a[156:159],v[124:127],v[44:47]
buffer_load_dwordx4 v4,s[20:23],s47 offen nt lds
v_mfma_f32_16x16x32_bf16 v[236:239],a[152:155],v[120:123],v[236:239]
ds_read_b128 a[136:139],v1 offset:512
v_max3_f32 v36,v36,v39,s19
v_mfma_f32_16x16x32_bf16 v[236:239],a[148:151],v[116:119],v[236:239]
v_exp_f32 v220,v220
v_mfma_f32_16x16x32_bf16 v[236:239],a[144:147],v[112:115],v[236:239]
ds_read_b128 a[140:143],v1 offset:768
v_sub_f32 v39,0,v36
v_mfma_f32_16x16x32_bf16 v[248:251],a[160:163],v[112:115],v[44:47]
v_exp_f32 v221,v221
v_mfma_f32_16x16x32_bf16 v[248:251],a[164:167],v[116:119],v[248:251]
ds_read_b128 a[156:159],v1 offset:4864
v_cvt_pk_bf16_f32 v206,v220,v221
v_mfma_f32_16x16x32_bf16 v[248:251],a[168:171],v[120:123],v[248:251]
v_exp_f32 v222,v222
v_mfma_f32_16x16x32_bf16 v[248:251],a[172:175],v[124:127],v[248:251]
ds_read_b128 a[152:155],v1 offset:4608
v_sub_f32 v38,0,v36
v_mfma_f32_16x16x32_bf16 v[252:255],a[188:191],v[124:127],v[44:47]
v_exp_f32 v223,v223
v_mfma_f32_16x16x32_bf16 v[252:255],a[184:187],v[120:123],v[252:255]
ds_read_b128 a[148:151],v1 offset:4352
v_cvt_pk_bf16_f32 v207,v222,v223
v_mfma_f32_16x16x32_bf16 v[252:255],a[180:183],v[116:119],v[252:255]
v_sub_f32 v37,0,v36
v_sub_f32 v36,0,v36
v_mfma_f32_16x16x32_bf16 v[252:255],a[176:179],v[112:115],v[252:255]
ds_read_b128 a[144:147],v1 offset:4096
v_max3_f32 v40,v40,v41,v42
v_mfma_f32_16x16x32_bf16 v[44:47],v[80:83],v[148:151],0
v_exp_f32 v232,v232
v_mfma_f32_16x16x32_bf16 v[44:47],v[84:87],v[180:183],v[44:47]
ds_read_b128 a[160:163],v1 offset:8192
v_max3_f32 v40,v40,v43,s100
v_mfma_f32_16x16x32_bf16 v[44:47],v[88:91],v[212:215],v[44:47]
buffer_load_dwordx4 v4,s[20:23],s47 offen offset:1024 nt lds
v_mfma_f32_16x16x32_bf16 v[44:47],v[92:95],v[244:247],v[44:47]
ds_read_b128 a[164:167],v1 offset:8448
v_sub_f32 v43,0,v40
v_mfma_f32_16x16x32_bf16 a[32:35],a[192:195],v[168:171],a[32:35]
v_exp_f32 v233,v233
v_mfma_f32_16x16x32_bf16 a[32:35],a[196:199],v[172:175],a[32:35]
ds_read_b128 a[168:171],v1 offset:8704
v_cvt_pk_bf16_f32 v232,v232,v233
v_mfma_f32_16x16x32_bf16 a[36:39],a[204:207],v[172:175],a[36:39]
v_exp_f32 v234,v234
v_mfma_f32_16x16x32_bf16 a[36:39],a[200:203],v[168:171],a[36:39]
ds_read_b128 a[172:175],v1 offset:8960
v_sub_f32 v42,0,v40
v_mfma_f32_16x16x32_bf16 a[40:43],a[208:211],v[168:171],a[40:43]
v_exp_f32 v235,v235
v_mfma_f32_16x16x32_bf16 a[40:43],a[212:215],v[172:175],a[40:43]
ds_read_b128 a[188:191],v1 offset:13056
v_cvt_pk_bf16_f32 v233,v234,v235
v_mfma_f32_16x16x32_bf16 a[44:47],a[220:223],v[172:175],a[44:47]
v_exp_f32 v236,v236
v_mfma_f32_16x16x32_bf16 a[44:47],a[216:219],v[168:171],a[44:47]
ds_read_b128 a[184:187],v1 offset:12800
v_sub_f32 v41,0,v40
v_mfma_f32_16x16x32_bf16 a[48:51],a[224:227],v[168:171],a[48:51]
v_exp_f32 v237,v237
v_mfma_f32_16x16x32_bf16 a[48:51],a[228:231],v[172:175],a[48:51]
ds_read_b128 a[180:183],v1 offset:12544
v_cvt_pk_bf16_f32 v234,v236,v237
v_mfma_f32_16x16x32_bf16 a[52:55],a[236:239],v[172:175],a[52:55]
v_exp_f32 v238,v238
v_mfma_f32_16x16x32_bf16 a[52:55],a[232:235],v[168:171],a[52:55]
ds_read_b128 a[176:179],v1 offset:12288
v_sub_f32 v40,0,v40
v_mfma_f32_16x16x32_bf16 a[56:59],a[240:243],v[168:171],a[56:59]
buffer_load_dwordx4 v5,s[20:23],s47 offen offset:2048 nt lds
v_mfma_f32_16x16x32_bf16 a[56:59],a[244:247],v[172:175],a[56:59]
v_exp_f32 v239,v239
v_mfma_f32_16x16x32_bf16 a[60:63],a[252:255],v[172:175],a[60:63]
v_cvt_pk_bf16_f32 v235,v238,v239
v_add_f32 v48,v49,v48
v_mfma_f32_16x16x32_bf16 a[60:63],a[248:251],v[168:171],a[60:63]
v_exp_f32 v248,v248
v_smfmac_f32_16x16x64_bf16 v[52:55],v[28:31],v[168:175],v27
s_waitcnt lgkmcnt(2)
v_max_f32 v20,0x00800000,v48
v_mfma_f32_16x16x32_bf16 v[136:139],a[128:131],v[128:131],v[32:35]
v_exp_f32 v249,v249
v_mfma_f32_16x16x32_bf16 v[136:139],a[132:135],v[160:163],v[136:139]
v_exp_f32 v250,v250
v_mfma_f32_16x16x32_bf16 v[136:139],a[136:139],v[192:195],v[136:139]
v_exp_f32 v251,v251
v_mfma_f32_16x16x32_bf16 v[136:139],a[140:143],v[224:227],v[136:139]
v_exp_f32 v252,v252
v_mfma_f32_16x16x32_bf16 v[140:143],a[156:159],v[224:227],v[32:35]
v_exp_f32 v253,v253
v_mfma_f32_16x16x32_bf16 v[140:143],a[152:155],v[192:195],v[140:143]
v_exp_f32 v254,v254
v_mfma_f32_16x16x32_bf16 v[140:143],a[148:151],v[160:163],v[140:143]
v_exp_f32 v255,v255
v_mfma_f32_16x16x32_bf16 v[140:143],a[144:147],v[128:131],v[140:143]
v_cvt_pk_bf16_f32 v236,v248,v249
v_cvt_pk_bf16_f32 v237,v250,v251
v_mfma_f32_16x16x32_bf16 v[152:155],a[160:163],v[128:131],v[32:35]
v_cvt_pk_bf16_f32 v238,v252,v253
v_cvt_pk_bf16_f32 v239,v254,v255
v_mfma_f32_16x16x32_bf16 v[152:155],a[164:167],v[160:163],v[152:155]
buffer_load_dwordx4 v5,s[20:23],s47 offen offset:3072 nt lds
v_mfma_f32_16x16x32_bf16 v[152:155],a[168:171],v[192:195],v[152:155]
v_exp_f32 v136,v136
v_mfma_f32_16x16x32_bf16 v[152:155],a[172:175],v[224:227],v[152:155]
v_exp_f32 v137,v137
v_mfma_f32_16x16x32_bf16 v[156:159],a[188:191],v[224:227],v[32:35]
v_exp_f32 v138,v138
v_mfma_f32_16x16x32_bf16 v[156:159],a[184:187],v[192:195],v[156:159]
s_waitcnt lgkmcnt(0)
v_exp_f32 v139,v139
v_mfma_f32_16x16x32_bf16 v[156:159],a[180:183],v[160:163],v[156:159]
v_cvt_pk_bf16_f32 v136,v136,v137
v_cvt_pk_bf16_f32 v137,v138,v139
v_mfma_f32_16x16x32_bf16 v[156:159],a[176:179],v[128:131],v[156:159]
v_exp_f32 v140,v140
v_mfma_f32_16x16x32_bf16 a[64:67],a[192:195],v[200:203],a[64:67]
v_exp_f32 v141,v141
v_mfma_f32_16x16x32_bf16 a[64:67],a[196:199],v[204:207],a[64:67]
v_exp_f32 v142,v142
v_mfma_f32_16x16x32_bf16 a[68:71],a[204:207],v[204:207],a[68:71]
v_exp_f32 v143,v143
v_mfma_f32_16x16x32_bf16 a[68:71],a[200:203],v[200:203],a[68:71]
v_cvt_pk_bf16_f32 v138,v140,v141
v_cvt_pk_bf16_f32 v139,v142,v143
v_mfma_f32_16x16x32_bf16 a[72:75],a[208:211],v[200:203],a[72:75]
s_add_u32 m0,32768,s52
v_exp_f32 v152,v152
v_mfma_f32_16x16x32_bf16 a[72:75],a[212:215],v[204:207],a[72:75]
v_exp_f32 v153,v153
v_mfma_f32_16x16x32_bf16 a[76:79],a[220:223],v[204:207],a[76:79]
v_exp_f32 v154,v154
v_mfma_f32_16x16x32_bf16 a[76:79],a[216:219],v[200:203],a[76:79]
buffer_load_dwordx4 v4,s[24:27],s47 offen nt lds
v_mfma_f32_16x16x32_bf16 a[80:83],a[224:227],v[200:203],a[80:83]
v_exp_f32 v155,v155
v_mfma_f32_16x16x32_bf16 a[80:83],a[228:231],v[204:207],a[80:83]
v_exp_f32 v156,v156
v_mfma_f32_16x16x32_bf16 a[84:87],a[236:239],v[204:207],a[84:87]
v_exp_f32 v157,v157
v_mfma_f32_16x16x32_bf16 a[84:87],a[232:235],v[200:203],a[84:87]
v_exp_f32 v158,v158
v_mfma_f32_16x16x32_bf16 a[88:91],a[240:243],v[200:203],a[88:91]
v_exp_f32 v159,v159
v_mfma_f32_16x16x32_bf16 a[88:91],a[244:247],v[204:207],a[88:91]
v_cvt_pk_bf16_f32 v140,v152,v153
v_cvt_pk_bf16_f32 v141,v154,v155
v_mfma_f32_16x16x32_bf16 a[92:95],a[252:255],v[204:207],a[92:95]
v_cvt_pk_bf16_f32 v142,v156,v157
v_cvt_pk_bf16_f32 v143,v158,v159
v_mfma_f32_16x16x32_bf16 a[92:95],a[248:251],v[200:203],a[92:95]
v_rcp_f32 v20,v20
v_smfmac_f32_16x16x64_bf16 v[56:59],v[28:31],v[200:207],v27
v_accvgpr_read_b32 v64,a0
v_mfma_f32_16x16x32_bf16 v[168:171],a[128:131],v[132:135],v[36:39]
v_accvgpr_read_b32 v65,a1
v_accvgpr_read_b32 v66,a2
v_mfma_f32_16x16x32_bf16 v[168:171],a[132:135],v[164:167],v[168:171]
v_accvgpr_read_b32 v67,a3
v_mul_f32 v64,v20,v64
v_mfma_f32_16x16x32_bf16 v[168:171],a[136:139],v[196:199],v[168:171]
v_mul_f32 v65,v20,v65
v_cvt_pk_bf16_f32 v64,v64,v65
v_mfma_f32_16x16x32_bf16 v[168:171],a[140:143],v[228:231],v[168:171]
v_mul_f32 v66,v20,v66
v_mul_f32 v67,v20,v67
v_mfma_f32_16x16x32_bf16 v[172:175],a[156:159],v[228:231],v[36:39]
buffer_load_dwordx4 v5,s[24:27],s47 offen offset:1024 nt lds
v_mfma_f32_16x16x32_bf16 v[172:175],a[152:155],v[196:199],v[172:175]
v_cvt_pk_bf16_f32 v65,v66,v67
v_accvgpr_read_b32 v16,a6
v_mfma_f32_16x16x32_bf16 v[172:175],a[148:151],v[164:167],v[172:175]
v_accvgpr_read_b32 v17,a7
v_accvgpr_read_b32 v66,a4
v_mfma_f32_16x16x32_bf16 v[172:175],a[144:147],v[132:135],v[172:175]
v_accvgpr_read_b32 v67,a5
v_cmp_nlt_f32 s[56:57],v48,s63
v_mfma_f32_16x16x32_bf16 v[184:187],a[160:163],v[132:135],v[36:39]
v_accvgpr_read_b32 v68,a8
v_accvgpr_read_b32 v69,a9
v_accvgpr_read_b32 v70,a10
v_accvgpr_read_b32 v71,a11
v_accvgpr_read_b32 v12,a12
v_accvgpr_read_b32 v13,a13
v_accvgpr_read_b32 v14,a14
)A"
R"A(v_accvgpr_read_b32 v15,a15
v_pk_mul_f32 v[68:69],v[20:21],v[68:69] op_sel:[0,0] op_sel_hi:[0,1]
v_pk_mul_f32 v[70:71],v[20:21],v[70:71] op_sel:[0,0] op_sel_hi:[0,1]
v_pk_mul_f32 v[12:13],v[20:21],v[12:13] op_sel:[0,0] op_sel_hi:[0,1]
v_pk_mul_f32 v[14:15],v[20:21],v[14:15] op_sel:[0,0] op_sel_hi:[0,1]
v_cvt_pk_bf16_f32 v68,v68,v69
v_cvt_pk_bf16_f32 v69,v70,v71
v_cvt_pk_bf16_f32 v70,v12,v13
v_cvt_pk_bf16_f32 v71,v14,v15
v_mfma_f32_16x16x32_bf16 v[184:187],a[164:167],v[164:167],v[184:187]
v_accvgpr_read_b32 v72,a16
v_accvgpr_read_b32 v73,a17
v_accvgpr_read_b32 v74,a18
v_accvgpr_read_b32 v75,a19
v_accvgpr_read_b32 v12,a20
v_accvgpr_read_b32 v13,a21
v_accvgpr_read_b32 v14,a22
v_accvgpr_read_b32 v15,a23
v_pk_mul_f32 v[72:73],v[20:21],v[72:73] op_sel:[0,0] op_sel_hi:[0,1]
v_pk_mul_f32 v[74:75],v[20:21],v[74:75] op_sel:[0,0] op_sel_hi:[0,1]
v_pk_mul_f32 v[12:13],v[20:21],v[12:13] op_sel:[0,0] op_sel_hi:[0,1]
v_pk_mul_f32 v[14:15],v[20:21],v[14:15] op_sel:[0,0] op_sel_hi:[0,1]
v_cvt_pk_bf16_f32 v72,v72,v73
v_cvt_pk_bf16_f32 v73,v74,v75
v_cvt_pk_bf16_f32 v74,v12,v13
v_cvt_pk_bf16_f32 v75,v14,v15
v_mfma_f32_16x16x32_bf16 v[184:187],a[168:171],v[196:199],v[184:187]
v_accvgpr_read_b32 v76,a24
v_accvgpr_read_b32 v77,a25
v_accvgpr_read_b32 v78,a26
v_accvgpr_read_b32 v79,a27
v_accvgpr_read_b32 v12,a28
v_accvgpr_read_b32 v13,a29
v_accvgpr_read_b32 v14,a30
v_accvgpr_read_b32 v15,a31
v_pk_mul_f32 v[76:77],v[20:21],v[76:77] op_sel:[0,0] op_sel_hi:[0,1]
v_pk_mul_f32 v[78:79],v[20:21],v[78:79] op_sel:[0,0] op_sel_hi:[0,1]
v_pk_mul_f32 v[12:13],v[20:21],v[12:13] op_sel:[0,0] op_sel_hi:[0,1]
v_pk_mul_f32 v[14:15],v[20:21],v[14:15] op_sel:[0,0] op_sel_hi:[0,1]
v_cvt_pk_bf16_f32 v76,v76,v77
v_cvt_pk_bf16_f32 v77,v78,v79
v_cvt_pk_bf16_f32 v78,v12,v13
v_cvt_pk_bf16_f32 v79,v14,v15
v_mfma_f32_16x16x32_bf16 v[184:187],a[172:175],v[228:231],v[184:187]
v_max3_f32 v44,v44,v45,v46
v_max3_f32 v44,v44,v47,s101
v_mfma_f32_16x16x32_bf16 v[188:191],a[188:191],v[228:231],v[36:39]
v_mov_b32 v48,0
v_mov_b32 v49,0
v_mfma_f32_16x16x32_bf16 v[188:191],a[184:187],v[196:199],v[188:191]
v_mov_b32 v50,0
v_mov_b32 v51,0
v_mfma_f32_16x16x32_bf16 v[188:191],a[180:183],v[164:167],v[188:191]
v_sub_f32 v47,0,v44
v_sub_f32 v46,0,v44
v_mfma_f32_16x16x32_bf16 v[188:191],a[176:179],v[132:135],v[188:191]
v_sub_f32 v45,0,v44
v_sub_f32 v44,0,v44
v_mfma_f32_16x16x32_bf16 a[96:99],a[192:195],v[232:235],a[96:99]
ds_read_b64_tr_b16 a[192:193],v2
ds_read_b64_tr_b16 a[194:195],v2 offset:4096
v_mfma_f32_16x16x32_bf16 a[96:99],a[196:199],v[236:239],a[96:99]
ds_read_b64_tr_b16 a[196:197],v2 offset:8192
v_add_f32 v52,v53,v52
v_mfma_f32_16x16x32_bf16 a[100:103],a[204:207],v[236:239],a[100:103]
buffer_load_dwordx4 v4,s[24:27],s47 offen offset:2048 nt lds
v_mfma_f32_16x16x32_bf16 a[100:103],a[200:203],v[232:235],a[100:103]
ds_read_b64_tr_b16 a[198:199],v2 offset:12288
ds_read_b64_tr_b16 a[204:205],v3 offset:8192
v_mfma_f32_16x16x32_bf16 a[104:107],a[208:211],v[232:235],a[104:107]
ds_read_b64_tr_b16 a[206:207],v3 offset:12288
v_max_f32 v21,0x00800000,v52
v_mfma_f32_16x16x32_bf16 a[104:107],a[212:215],v[236:239],a[104:107]
ds_read_b64_tr_b16 a[200:201],v3
v_accvgpr_read_b32 v80,a32
v_mfma_f32_16x16x32_bf16 a[108:111],a[220:223],v[236:239],a[108:111]
ds_read_b64_tr_b16 a[202:203],v3 offset:4096
v_accvgpr_read_b32 v81,a33
v_mfma_f32_16x16x32_bf16 a[108:111],a[216:219],v[232:235],a[108:111]
ds_read_b64_tr_b16 a[208:209],v2 offset:256
ds_read_b64_tr_b16 a[210:211],v2 offset:4352
v_mfma_f32_16x16x32_bf16 a[112:115],a[224:227],v[232:235],a[112:115]
ds_read_b64_tr_b16 a[212:213],v2 offset:8448
v_accvgpr_read_b32 v82,a34
v_mfma_f32_16x16x32_bf16 a[112:115],a[228:231],v[236:239],a[112:115]
ds_read_b64_tr_b16 a[214:215],v2 offset:12544
v_accvgpr_read_b32 v83,a35
v_mfma_f32_16x16x32_bf16 a[116:119],a[236:239],v[236:239],a[116:119]
ds_read_b64_tr_b16 a[220:221],v3 offset:8448
v_accvgpr_read_b32 v12,a38
v_mfma_f32_16x16x32_bf16 a[116:119],a[232:235],v[232:235],a[116:119]
ds_read_b64_tr_b16 a[222:223],v3 offset:12544
ds_read_b64_tr_b16 a[216:217],v3 offset:256
v_mfma_f32_16x16x32_bf16 a[120:123],a[240:243],v[232:235],a[120:123]
ds_read_b64_tr_b16 a[218:219],v3 offset:4352
v_accvgpr_read_b32 v13,a39
v_mfma_f32_16x16x32_bf16 a[120:123],a[244:247],v[236:239],a[120:123]
ds_read_b64_tr_b16 a[224:225],v2 offset:512
v_add_u32 v1,s48,v1
v_mfma_f32_16x16x32_bf16 a[124:127],a[252:255],v[236:239],a[124:127]
ds_read_b64_tr_b16 a[226:227],v2 offset:4608
v_mov_b32 v53,0
v_mfma_f32_16x16x32_bf16 a[124:127],a[248:251],v[232:235],a[124:127]
ds_read_b64_tr_b16 a[228:229],v2 offset:8704
ds_read_b64_tr_b16 a[230:231],v2 offset:12800
v_smfmac_f32_16x16x64_bf16 v[60:63],v[28:31],v[232:239],v27
ds_read_b64_tr_b16 a[236:237],v3 offset:8704
v_mfma_f32_16x16x32_bf16 v[200:203],a[128:131],v[144:147],v[40:43]
buffer_load_dwordx4 v5,s[24:27],s47 offen offset:3072 nt lds
v_mfma_f32_16x16x32_bf16 v[200:203],a[132:135],v[176:179],v[200:203]
s_or_b32 s47,s47,0x18000
ds_read_b64_tr_b16 a[238:239],v3 offset:12800
ds_read_b64_tr_b16 a[232:233],v3 offset:512
v_mfma_f32_16x16x32_bf16 v[200:203],a[136:139],v[208:211],v[200:203]
s_add_u32 s47,s47,4096
s_and_b32 s47,s47,0x27000
ds_read_b64_tr_b16 a[234:235],v3 offset:4608
v_mfma_f32_16x16x32_bf16 v[200:203],a[140:143],v[240:243],v[200:203]
s_or_b32 s47,s47,s46
ds_read_b64_tr_b16 a[240:241],v2 offset:768
v_mov_b32 v54,0
v_mfma_f32_16x16x32_bf16 v[204:207],a[156:159],v[240:243],v[40:43]
s_and_b32 s46,s54,0xfffc0000
ds_read_b64_tr_b16 a[242:243],v2 offset:4864
v_mov_b32 v55,0
v_mfma_f32_16x16x32_bf16 v[204:207],a[152:155],v[208:211],v[204:207]
ds_read_b64_tr_b16 a[244:245],v2 offset:8960
ds_read_b64_tr_b16 a[246:247],v2 offset:13056
v_mfma_f32_16x16x32_bf16 v[204:207],a[148:151],v[176:179],v[204:207]
ds_read_b64_tr_b16 a[252:253],v3 offset:8960
v_add_f32 v56,v57,v56
v_mfma_f32_16x16x32_bf16 v[204:207],a[144:147],v[144:147],v[204:207]
ds_read_b64_tr_b16 a[254:255],v3 offset:13056
v_max_f32 v22,0x00800000,v56
v_mfma_f32_16x16x32_bf16 v[216:219],a[160:163],v[144:147],v[40:43]
ds_read_b64_tr_b16 a[248:249],v3 offset:768
v_accvgpr_read_b32 v96,a64
v_mfma_f32_16x16x32_bf16 v[216:219],a[164:167],v[176:179],v[216:219]
ds_read_b64_tr_b16 a[250:251],v3 offset:4864
v_accvgpr_read_b32 v97,a65
v_mfma_f32_16x16x32_bf16 v[216:219],a[168:171],v[208:211],v[216:219]
v_exp_f32 v168,v168
v_mfma_f32_16x16x32_bf16 v[216:219],a[172:175],v[240:243],v[216:219]
v_exp_f32 v169,v169
v_mfma_f32_16x16x32_bf16 v[220:223],a[188:191],v[240:243],v[40:43]
v_exp_f32 v170,v170
v_mfma_f32_16x16x32_bf16 v[220:223],a[184:187],v[208:211],v[220:223]
v_exp_f32 v171,v171
v_mfma_f32_16x16x32_bf16 v[220:223],a[180:183],v[176:179],v[220:223]
v_cvt_pk_bf16_f32 v168,v168,v169
v_cvt_pk_bf16_f32 v169,v170,v171
v_mfma_f32_16x16x32_bf16 v[220:223],a[176:179],v[144:147],v[220:223]
s_waitcnt lgkmcnt(2)
v_exp_f32 v172,v172
v_mfma_f32_16x16x32_bf16 a[0:3],a[192:195],v[136:139],0
v_exp_f32 v173,v173
v_mfma_f32_16x16x32_bf16 a[0:3],a[196:199],v[140:143],a[0:3]
v_exp_f32 v174,v174
v_mfma_f32_16x16x32_bf16 a[4:7],a[204:207],v[140:143],0
v_exp_f32 v175,v175
v_mfma_f32_16x16x32_bf16 a[4:7],a[200:203],v[136:139],a[4:7]
s_memrealtime s[76:77]
v_cvt_pk_bf16_f32 v170,v172,v173
v_cvt_pk_bf16_f32 v171,v174,v175
v_mfma_f32_16x16x32_bf16 a[8:11],a[208:211],v[136:139],0
v_exp_f32 v184,v184
v_mfma_f32_16x16x32_bf16 a[8:11],a[212:215],v[140:143],a[8:11]
v_exp_f32 v185,v185
v_mfma_f32_16x16x32_bf16 a[12:15],a[220:223],v[140:143],0
v_exp_f32 v186,v186
v_mfma_f32_16x16x32_bf16 a[12:15],a[216:219],v[136:139],a[12:15]
v_exp_f32 v187,v187
v_mfma_f32_16x16x32_bf16 a[16:19],a[224:227],v[136:139],0
v_exp_f32 v188,v188
v_mfma_f32_16x16x32_bf16 a[16:19],a[228:231],v[140:143],a[16:19]
v_exp_f32 v189,v189
v_mfma_f32_16x16x32_bf16 a[20:23],a[236:239],v[140:143],0
s_waitcnt lgkmcnt(0)
v_exp_f32 v190,v190
v_mfma_f32_16x16x32_bf16 a[20:23],a[232:235],v[136:139],a[20:23]
s_sub_u32 s79,s78,s76
v_exp_f32 v191,v191
v_mfma_f32_16x16x32_bf16 a[24:27],a[240:243],v[136:139],0
s_add_u32 s78,s76,s60
v_rcp_f32 v21,v21
v_mfma_f32_16x16x32_bf16 a[24:27],a[244:247],v[140:143],a[24:27]
s_cmp_gt_i32 s79,s95
s_cbranch_scc1 L_ps5_%=
L_pr5_%=:
v_cvt_pk_bf16_f32 v172,v184,v185
v_mfma_f32_16x16x32_bf16 a[28:31],a[252:255],v[140:143],0
s_lshr_b32 s60,s94,2
v_cvt_pk_bf16_f32 v173,v186,v187
v_cvt_pk_bf16_f32 v174,v188,v189
v_mfma_f32_16x16x32_bf16 a[28:31],a[248:251],v[136:139],a[28:31]
s_max_i32 s60,s60,128
v_cvt_pk_bf16_f32 v175,v190,v191
v_mul_f32 v80,v21,v80
v_smfmac_f32_16x16x64_bf16 v[48:51],v[28:31],v[136:143],v27
v_mul_f32 v81,v21,v81
v_mfma_f32_16x16x32_bf16 v[232:235],a[128:131],v[148:151],v[44:47]
s_waitcnt vmcnt(8)
v_cvt_pk_bf16_f32 v80,v80,v81
v_mul_f32 v82,v21,v82
v_mfma_f32_16x16x32_bf16 v[232:235],a[132:135],v[180:183],v[232:235]
s_barrier
s_add_u32 m0,65536,s51
v_mul_f32 v83,v21,v83
v_mfma_f32_16x16x32_bf16 v[232:235],a[136:139],v[212:215],v[232:235]
v_cvt_pk_bf16_f32 v81,v82,v83
v_accvgpr_read_b32 v82,a36
v_mfma_f32_16x16x32_bf16 v[232:235],a[140:143],v[244:247],v[232:235]
v_accvgpr_read_b32 v83,a37
v_mul_f32 v82,v21,v82
v_mfma_f32_16x16x32_bf16 v[236:239],a[156:159],v[244:247],v[44:47]
buffer_load_dwordx4 v4,s[20:23],s47 offen nt lds
v_mfma_f32_16x16x32_bf16 v[236:239],a[152:155],v[212:215],v[236:239]
v_mul_f32 v83,v21,v83
v_mul_f32 v12,v21,v12
v_mfma_f32_16x16x32_bf16 v[236:239],a[148:151],v[180:183],v[236:239]
v_mul_f32 v13,v21,v13
v_cvt_pk_bf16_f32 v82,v82,v83
v_mfma_f32_16x16x32_bf16 v[236:239],a[144:147],v[148:151],v[236:239]
ds_read_b128 a[128:131],v1
v_cvt_pk_bf16_f32 v83,v12,v13
v_mfma_f32_16x16x32_bf16 v[248:251],a[160:163],v[148:151],v[44:47]
v_accvgpr_read_b32 v84,a40
v_accvgpr_read_b32 v85,a41
v_accvgpr_read_b32 v86,a42
v_accvgpr_read_b32 v87,a43
v_accvgpr_read_b32 v12,a44
v_accvgpr_read_b32 v13,a45
v_accvgpr_read_b32 v14,a46
v_accvgpr_read_b32 v15,a47
v_pk_mul_f32 v[84:85],v[20:21],v[84:85] op_sel:[1,0] op_sel_hi:[1,1]
v_pk_mul_f32 v[86:87],v[20:21],v[86:87] op_sel:[1,0] op_sel_hi:[1,1]
v_pk_mul_f32 v[12:13],v[20:21],v[12:13] op_sel:[1,0] op_sel_hi:[1,1]
v_pk_mul_f32 v[14:15],v[20:21],v[14:15] op_sel:[1,0] op_sel_hi:[1,1]
v_cvt_pk_bf16_f32 v84,v84,v85
v_cvt_pk_bf16_f32 v85,v86,v87
v_cvt_pk_bf16_f32 v86,v12,v13
v_cvt_pk_bf16_f32 v87,v14,v15
v_mfma_f32_16x16x32_bf16 v[248:251],a[164:167],v[180:183],v[248:251]
v_accvgpr_read_b32 v88,a48
v_accvgpr_read_b32 v89,a49
v_accvgpr_read_b32 v90,a50
v_accvgpr_read_b32 v91,a51
v_accvgpr_read_b32 v12,a52
v_accvgpr_read_b32 v13,a53
v_accvgpr_read_b32 v14,a54
v_accvgpr_read_b32 v15,a55
v_pk_mul_f32 v[88:89],v[20:21],v[88:89] op_sel:[1,0] op_sel_hi:[1,1]
v_pk_mul_f32 v[90:91],v[20:21],v[90:91] op_sel:[1,0] op_sel_hi:[1,1]
v_pk_mul_f32 v[12:13],v[20:21],v[12:13] op_sel:[1,0] op_sel_hi:[1,1]
v_pk_mul_f32 v[14:15],v[20:21],v[14:15] op_sel:[1,0] op_sel_hi:[1,1]
v_cvt_pk_bf16_f32 v88,v88,v89
v_cvt_pk_bf16_f32 v89,v90,v91
v_cvt_pk_bf16_f32 v90,v12,v13
v_cvt_pk_bf16_f32 v91,v14,v15
v_mfma_f32_16x16x32_bf16 v[248:251],a[168:171],v[212:215],v[248:251]
ds_read_b128 a[132:135],v1 offset:256
v_accvgpr_read_b32 v98,a66
v_mfma_f32_16x16x32_bf16 v[248:251],a[172:175],v[244:247],v[248:251]
v_accvgpr_read_b32 v92,a56
v_accvgpr_read_b32 v93,a57
v_accvgpr_read_b32 v94,a58
v_accvgpr_read_b32 v95,a59
v_accvgpr_read_b32 v12,a60
v_accvgpr_read_b32 v13,a61
v_accvgpr_read_b32 v14,a62
v_accvgpr_read_b32 v15,a63
v_pk_mul_f32 v[92:93],v[20:21],v[92:93] op_sel:[1,0] op_sel_hi:[1,1]
v_pk_mul_f32 v[94:95],v[20:21],v[94:95] op_sel:[1,0] op_sel_hi:[1,1]
v_pk_mul_f32 v[12:13],v[20:21],v[12:13] op_sel:[1,0] op_sel_hi:[1,1]
v_pk_mul_f32 v[14:15],v[20:21],v[14:15] op_sel:[1,0] op_sel_hi:[1,1]
v_cvt_pk_bf16_f32 v92,v92,v93
v_cvt_pk_bf16_f32 v93,v94,v95
v_cvt_pk_bf16_f32 v94,v12,v13
v_cvt_pk_bf16_f32 v95,v14,v15
v_mfma_f32_16x16x32_bf16 v[252:255],a[188:191],v[244:247],v[44:47]
ds_read_b128 a[136:139],v1 offset:512
v_accvgpr_read_b32 v99,a67
v_mfma_f32_16x16x32_bf16 v[252:255],a[184:187],v[212:215],v[252:255]
v_cmp_nlt_f32 s[58:59],v52,s63
s_nop 1
s_or_b64 s[56:57],s[56:57],s[58:59]
v_mfma_f32_16x16x32_bf16 v[252:255],a[180:183],v[180:183],v[252:255]
)A"
R"A(ds_read_b128 a[140:143],v1 offset:768
v_mov_b32 v52,0
v_mfma_f32_16x16x32_bf16 v[252:255],a[176:179],v[148:151],v[252:255]
v_exp_f32 v200,v200
v_mfma_f32_16x16x32_bf16 a[32:35],a[192:195],v[168:171],0
ds_read_b128 a[156:159],v1 offset:4864
v_accvgpr_read_b32 v14,a70
v_mfma_f32_16x16x32_bf16 a[32:35],a[196:199],v[172:175],a[32:35]
v_exp_f32 v201,v201
v_mfma_f32_16x16x32_bf16 a[36:39],a[204:207],v[172:175],0
buffer_load_dwordx4 v4,s[20:23],s47 offen offset:1024 nt lds
v_mfma_f32_16x16x32_bf16 a[36:39],a[200:203],v[168:171],a[36:39]
ds_read_b128 a[152:155],v1 offset:4608
v_cvt_pk_bf16_f32 v200,v200,v201
v_mfma_f32_16x16x32_bf16 a[40:43],a[208:211],v[168:171],0
v_exp_f32 v202,v202
v_mfma_f32_16x16x32_bf16 a[40:43],a[212:215],v[172:175],a[40:43]
ds_read_b128 a[148:151],v1 offset:4352
v_accvgpr_read_b32 v15,a71
v_mfma_f32_16x16x32_bf16 a[44:47],a[220:223],v[172:175],0
v_exp_f32 v203,v203
v_mfma_f32_16x16x32_bf16 a[44:47],a[216:219],v[168:171],a[44:47]
ds_read_b128 a[144:147],v1 offset:4096
v_cvt_pk_bf16_f32 v201,v202,v203
v_mfma_f32_16x16x32_bf16 a[48:51],a[224:227],v[168:171],0
v_exp_f32 v204,v204
v_mfma_f32_16x16x32_bf16 a[48:51],a[228:231],v[172:175],a[48:51]
ds_read_b128 a[160:163],v1 offset:8192
v_mov_b32 v57,0
v_mfma_f32_16x16x32_bf16 a[52:55],a[236:239],v[172:175],0
v_exp_f32 v205,v205
v_mfma_f32_16x16x32_bf16 a[52:55],a[232:235],v[168:171],a[52:55]
ds_read_b128 a[164:167],v1 offset:8448
v_cvt_pk_bf16_f32 v202,v204,v205
v_mfma_f32_16x16x32_bf16 a[56:59],a[240:243],v[168:171],0
v_exp_f32 v206,v206
v_mfma_f32_16x16x32_bf16 a[56:59],a[244:247],v[172:175],a[56:59]
ds_read_b128 a[168:171],v1 offset:8704
v_mov_b32 v58,0
v_mfma_f32_16x16x32_bf16 a[60:63],a[252:255],v[172:175],0
v_exp_f32 v207,v207
v_mfma_f32_16x16x32_bf16 a[60:63],a[248:251],v[168:171],a[60:63]
ds_read_b128 a[172:175],v1 offset:8960
v_cvt_pk_bf16_f32 v203,v206,v207
v_smfmac_f32_16x16x64_bf16 v[52:55],v[28:31],v[168:175],v27
s_waitcnt lgkmcnt(4)
v_mov_b32 v59,0
v_mfma_f32_16x16x32_bf16 v[136:139],a[128:131],v[128:131],v[32:35]
buffer_load_dwordx4 v5,s[20:23],s47 offen offset:2048 nt lds
v_mfma_f32_16x16x32_bf16 v[136:139],a[132:135],v[160:163],v[136:139]
ds_read_b128 a[188:191],v1 offset:13056
v_add_f32 v60,v61,v60
v_mfma_f32_16x16x32_bf16 v[136:139],a[136:139],v[192:195],v[136:139]
v_exp_f32 v216,v216
v_mfma_f32_16x16x32_bf16 v[136:139],a[140:143],v[224:227],v[136:139]
ds_read_b128 a[184:187],v1 offset:12800
v_max_f32 v23,0x00800000,v60
v_mfma_f32_16x16x32_bf16 v[140:143],a[156:159],v[224:227],v[32:35]
v_exp_f32 v217,v217
v_mfma_f32_16x16x32_bf16 v[140:143],a[152:155],v[192:195],v[140:143]
ds_read_b128 a[180:183],v1 offset:12544
v_cvt_pk_bf16_f32 v204,v216,v217
v_mfma_f32_16x16x32_bf16 v[140:143],a[148:151],v[160:163],v[140:143]
ds_read_b128 a[176:179],v1 offset:12288
v_mfma_f32_16x16x32_bf16 v[140:143],a[144:147],v[128:131],v[140:143]
s_waitcnt lgkmcnt(4)
v_exp_f32 v218,v218
v_mfma_f32_16x16x32_bf16 v[152:155],a[160:163],v[128:131],v[32:35]
v_exp_f32 v219,v219
v_mfma_f32_16x16x32_bf16 v[152:155],a[164:167],v[160:163],v[152:155]
v_exp_f32 v220,v220
v_mfma_f32_16x16x32_bf16 v[152:155],a[168:171],v[192:195],v[152:155]
v_exp_f32 v221,v221
v_mfma_f32_16x16x32_bf16 v[152:155],a[172:175],v[224:227],v[152:155]
s_waitcnt lgkmcnt(2)
v_exp_f32 v222,v222
v_mfma_f32_16x16x32_bf16 v[156:159],a[188:191],v[224:227],v[32:35]
v_exp_f32 v223,v223
v_mfma_f32_16x16x32_bf16 v[156:159],a[184:187],v[192:195],v[156:159]
s_waitcnt lgkmcnt(1)
v_rcp_f32 v22,v22
v_mfma_f32_16x16x32_bf16 v[156:159],a[180:183],v[160:163],v[156:159]
s_waitcnt lgkmcnt(0)
v_cvt_pk_bf16_f32 v205,v218,v219
v_cvt_pk_bf16_f32 v206,v220,v221
v_cvt_pk_bf16_f32 v207,v222,v223
v_mfma_f32_16x16x32_bf16 v[156:159],a[176:179],v[128:131],v[156:159]
v_mul_f32 v96,v22,v96
v_mul_f32 v97,v22,v97
v_mfma_f32_16x16x32_bf16 a[64:67],a[192:195],v[200:203],0
buffer_load_dwordx4 v5,s[20:23],s47 offen offset:3072 nt lds
v_cvt_pk_bf16_f32 v96,v96,v97
v_mul_f32 v98,v22,v98
v_mul_f32 v99,v22,v99
v_cvt_pk_bf16_f32 v97,v98,v99
v_mfma_f32_16x16x32_bf16 a[64:67],a[196:199],v[204:207],a[64:67]
v_accvgpr_read_b32 v98,a68
v_accvgpr_read_b32 v99,a69
v_mul_f32 v98,v22,v98
v_mul_f32 v99,v22,v99
v_mfma_f32_16x16x32_bf16 a[68:71],a[204:207],v[204:207],0
v_mul_f32 v14,v22,v14
v_mul_f32 v15,v22,v15
v_cvt_pk_bf16_f32 v98,v98,v99
v_cvt_pk_bf16_f32 v99,v14,v15
v_mfma_f32_16x16x32_bf16 a[68:71],a[200:203],v[200:203],a[68:71]
v_accvgpr_read_b32 v100,a72
v_accvgpr_read_b32 v101,a73
v_accvgpr_read_b32 v102,a74
v_accvgpr_read_b32 v103,a75
v_accvgpr_read_b32 v12,a76
v_accvgpr_read_b32 v13,a77
v_accvgpr_read_b32 v14,a78
v_accvgpr_read_b32 v15,a79
v_pk_mul_f32 v[100:101],v[22:23],v[100:101] op_sel:[0,0] op_sel_hi:[0,1]
v_pk_mul_f32 v[102:103],v[22:23],v[102:103] op_sel:[0,0] op_sel_hi:[0,1]
v_pk_mul_f32 v[12:13],v[22:23],v[12:13] op_sel:[0,0] op_sel_hi:[0,1]
v_pk_mul_f32 v[14:15],v[22:23],v[14:15] op_sel:[0,0] op_sel_hi:[0,1]
v_cvt_pk_bf16_f32 v100,v100,v101
v_cvt_pk_bf16_f32 v101,v102,v103
v_cvt_pk_bf16_f32 v102,v12,v13
v_cvt_pk_bf16_f32 v103,v14,v15
v_mfma_f32_16x16x32_bf16 a[72:75],a[208:211],v[200:203],0
v_accvgpr_read_b32 v104,a80
v_accvgpr_read_b32 v105,a81
v_accvgpr_read_b32 v106,a82
v_accvgpr_read_b32 v107,a83
v_accvgpr_read_b32 v12,a84
v_accvgpr_read_b32 v13,a85
v_accvgpr_read_b32 v14,a86
v_accvgpr_read_b32 v15,a87
v_pk_mul_f32 v[104:105],v[22:23],v[104:105] op_sel:[0,0] op_sel_hi:[0,1]
v_pk_mul_f32 v[106:107],v[22:23],v[106:107] op_sel:[0,0] op_sel_hi:[0,1]
v_pk_mul_f32 v[12:13],v[22:23],v[12:13] op_sel:[0,0] op_sel_hi:[0,1]
v_pk_mul_f32 v[14:15],v[22:23],v[14:15] op_sel:[0,0] op_sel_hi:[0,1]
v_cvt_pk_bf16_f32 v104,v104,v105
v_cvt_pk_bf16_f32 v105,v106,v107
v_cvt_pk_bf16_f32 v106,v12,v13
v_cvt_pk_bf16_f32 v107,v14,v15
v_mfma_f32_16x16x32_bf16 a[72:75],a[212:215],v[204:207],a[72:75]
v_accvgpr_read_b32 v108,a88
v_accvgpr_read_b32 v109,a89
v_accvgpr_read_b32 v110,a90
v_accvgpr_read_b32 v111,a91
v_accvgpr_read_b32 v12,a92
v_accvgpr_read_b32 v13,a93
v_accvgpr_read_b32 v14,a94
v_accvgpr_read_b32 v15,a95
v_pk_mul_f32 v[108:109],v[22:23],v[108:109] op_sel:[0,0] op_sel_hi:[0,1]
v_pk_mul_f32 v[110:111],v[22:23],v[110:111] op_sel:[0,0] op_sel_hi:[0,1]
v_pk_mul_f32 v[12:13],v[22:23],v[12:13] op_sel:[0,0] op_sel_hi:[0,1]
v_pk_mul_f32 v[14:15],v[22:23],v[14:15] op_sel:[0,0] op_sel_hi:[0,1]
v_cvt_pk_bf16_f32 v108,v108,v109
v_cvt_pk_bf16_f32 v109,v110,v111
v_cvt_pk_bf16_f32 v110,v12,v13
v_cvt_pk_bf16_f32 v111,v14,v15
v_mfma_f32_16x16x32_bf16 a[76:79],a[220:223],v[204:207],0
v_cmp_nlt_f32 s[58:59],v56,s63
s_nop 1
s_or_b64 s[56:57],s[56:57],s[58:59]
v_mfma_f32_16x16x32_bf16 a[76:79],a[216:219],v[200:203],a[76:79]
v_mov_b32 v56,0
v_accvgpr_read_b32 v112,a96
v_mfma_f32_16x16x32_bf16 a[80:83],a[224:227],v[200:203],0
s_add_u32 m0,65536,s52
v_exp_f32 v232,v232
v_mfma_f32_16x16x32_bf16 a[80:83],a[228:231],v[204:207],a[80:83]
v_exp_f32 v233,v233
v_mfma_f32_16x16x32_bf16 a[84:87],a[236:239],v[204:207],0
v_exp_f32 v234,v234
v_mfma_f32_16x16x32_bf16 a[84:87],a[232:235],v[200:203],a[84:87]
buffer_load_dwordx4 v4,s[24:27],s47 offen nt lds
v_mfma_f32_16x16x32_bf16 a[88:91],a[240:243],v[200:203],0
v_exp_f32 v235,v235
v_mfma_f32_16x16x32_bf16 a[88:91],a[244:247],v[204:207],a[88:91]
v_cvt_pk_bf16_f32 v232,v232,v233
v_cvt_pk_bf16_f32 v233,v234,v235
v_mfma_f32_16x16x32_bf16 a[92:95],a[252:255],v[204:207],0
v_exp_f32 v236,v236
v_mfma_f32_16x16x32_bf16 a[92:95],a[248:251],v[200:203],a[92:95]
v_exp_f32 v237,v237
v_smfmac_f32_16x16x64_bf16 v[56:59],v[28:31],v[200:207],v27
v_cvt_pk_bf16_f32 v234,v236,v237
v_mfma_f32_16x16x32_bf16 v[168:171],a[128:131],v[132:135],v[36:39]
v_exp_f32 v238,v238
v_mfma_f32_16x16x32_bf16 v[168:171],a[132:135],v[164:167],v[168:171]
v_exp_f32 v239,v239
v_mfma_f32_16x16x32_bf16 v[168:171],a[136:139],v[196:199],v[168:171]
v_cvt_pk_bf16_f32 v235,v238,v239
v_accvgpr_read_b32 v113,a97
v_mfma_f32_16x16x32_bf16 v[168:171],a[140:143],v[228:231],v[168:171]
v_exp_f32 v248,v248
v_mfma_f32_16x16x32_bf16 v[172:175],a[156:159],v[228:231],v[36:39]
v_exp_f32 v249,v249
v_mfma_f32_16x16x32_bf16 v[172:175],a[152:155],v[196:199],v[172:175]
v_exp_f32 v250,v250
v_mfma_f32_16x16x32_bf16 v[172:175],a[148:151],v[164:167],v[172:175]
v_exp_f32 v251,v251
v_mfma_f32_16x16x32_bf16 v[172:175],a[144:147],v[132:135],v[172:175]
v_exp_f32 v252,v252
v_mfma_f32_16x16x32_bf16 v[184:187],a[160:163],v[132:135],v[36:39]
buffer_load_dwordx4 v5,s[24:27],s47 offen offset:1024 nt lds
v_mfma_f32_16x16x32_bf16 v[184:187],a[164:167],v[164:167],v[184:187]
v_exp_f32 v253,v253
v_mfma_f32_16x16x32_bf16 v[184:187],a[168:171],v[196:199],v[184:187]
v_exp_f32 v254,v254
v_mfma_f32_16x16x32_bf16 v[184:187],a[172:175],v[228:231],v[184:187]
v_exp_f32 v255,v255
v_mfma_f32_16x16x32_bf16 v[188:191],a[188:191],v[228:231],v[36:39]
v_cvt_pk_bf16_f32 v236,v248,v249
v_cvt_pk_bf16_f32 v237,v250,v251
v_mfma_f32_16x16x32_bf16 v[188:191],a[184:187],v[196:199],v[188:191]
v_cvt_pk_bf16_f32 v238,v252,v253
v_cvt_pk_bf16_f32 v239,v254,v255
v_mfma_f32_16x16x32_bf16 v[188:191],a[180:183],v[164:167],v[188:191]
v_rcp_f32 v23,v23
v_mfma_f32_16x16x32_bf16 v[188:191],a[176:179],v[132:135],v[188:191]
v_accvgpr_read_b32 v114,a98
v_accvgpr_read_b32 v115,a99
v_mfma_f32_16x16x32_bf16 a[96:99],a[192:195],v[232:235],0
v_mul_f32 v66,v20,v66
v_mul_f32 v67,v20,v67
v_mul_f32 v112,v23,v112
v_mul_f32 v113,v23,v113
v_mul_f32 v16,v20,v16
v_mul_f32 v17,v20,v17
v_cvt_pk_bf16_f32 v66,v66,v67
v_cvt_pk_bf16_f32 v112,v112,v113
v_mul_f32 v114,v23,v114
v_mul_f32 v115,v23,v115
v_mfma_f32_16x16x32_bf16 a[96:99],a[196:199],v[236:239],a[96:99]
v_cvt_pk_bf16_f32 v67,v16,v17
v_cvt_pk_bf16_f32 v113,v114,v115
v_accvgpr_read_b32 v16,a102
v_accvgpr_read_b32 v17,a103
v_accvgpr_read_b32 v114,a100
v_accvgpr_read_b32 v115,a101
v_mfma_f32_16x16x32_bf16 a[100:103],a[204:207],v[236:239],0
v_accvgpr_read_b32 v116,a104
v_accvgpr_read_b32 v117,a105
v_accvgpr_read_b32 v118,a106
v_accvgpr_read_b32 v119,a107
v_accvgpr_read_b32 v12,a108
v_accvgpr_read_b32 v13,a109
v_accvgpr_read_b32 v14,a110
v_accvgpr_read_b32 v15,a111
v_pk_mul_f32 v[116:117],v[22:23],v[116:117] op_sel:[1,0] op_sel_hi:[1,1]
v_pk_mul_f32 v[118:119],v[22:23],v[118:119] op_sel:[1,0] op_sel_hi:[1,1]
v_pk_mul_f32 v[12:13],v[22:23],v[12:13] op_sel:[1,0] op_sel_hi:[1,1]
v_pk_mul_f32 v[14:15],v[22:23],v[14:15] op_sel:[1,0] op_sel_hi:[1,1]
v_cvt_pk_bf16_f32 v116,v116,v117
v_cvt_pk_bf16_f32 v117,v118,v119
v_cvt_pk_bf16_f32 v118,v12,v13
v_cvt_pk_bf16_f32 v119,v14,v15
v_mfma_f32_16x16x32_bf16 a[100:103],a[200:203],v[232:235],a[100:103]
v_accvgpr_read_b32 v120,a112
v_accvgpr_read_b32 v121,a113
v_accvgpr_read_b32 v122,a114
v_accvgpr_read_b32 v123,a115
v_accvgpr_read_b32 v12,a116
v_accvgpr_read_b32 v13,a117
v_accvgpr_read_b32 v14,a118
v_accvgpr_read_b32 v15,a119
v_pk_mul_f32 v[120:121],v[22:23],v[120:121] op_sel:[1,0] op_sel_hi:[1,1]
v_pk_mul_f32 v[122:123],v[22:23],v[122:123] op_sel:[1,0] op_sel_hi:[1,1]
v_pk_mul_f32 v[12:13],v[22:23],v[12:13] op_sel:[1,0] op_sel_hi:[1,1]
v_pk_mul_f32 v[14:15],v[22:23],v[14:15] op_sel:[1,0] op_sel_hi:[1,1]
v_cvt_pk_bf16_f32 v120,v120,v121
v_cvt_pk_bf16_f32 v121,v122,v123
v_cvt_pk_bf16_f32 v122,v12,v13
v_cvt_pk_bf16_f32 v123,v14,v15
v_mfma_f32_16x16x32_bf16 a[104:107],a[208:211],v[232:235],0
v_accvgpr_read_b32 v124,a120
v_accvgpr_read_b32 v125,a121
v_accvgpr_read_b32 v126,a122
v_accvgpr_read_b32 v127,a123
v_accvgpr_read_b32 v12,a124
v_accvgpr_read_b32 v13,a125
v_accvgpr_read_b32 v14,a126
v_accvgpr_read_b32 v15,a127
v_pk_mul_f32 v[124:125],v[22:23],v[124:125] op_sel:[1,0] op_sel_hi:[1,1]
v_pk_mul_f32 v[126:127],v[22:23],v[126:127] op_sel:[1,0] op_sel_hi:[1,1]
v_pk_mul_f32 v[12:13],v[22:23],v[12:13] op_sel:[1,0] op_sel_hi:[1,1]
v_pk_mul_f32 v[14:15],v[22:23],v[14:15] op_sel:[1,0] op_sel_hi:[1,1]
v_cvt_pk_bf16_f32 v124,v124,v125
v_cvt_pk_bf16_f32 v125,v126,v127
v_cvt_pk_bf16_f32 v126,v12,v13
v_cvt_pk_bf16_f32 v127,v14,v15
v_mfma_f32_16x16x32_bf16 a[104:107],a[212:215],v[236:239],a[104:107]
v_cmp_nlt_f32 s[58:59],v60,s63
s_nop 1
s_or_b64 s[56:57],s[56:57],s[58:59]
v_mfma_f32_16x16x32_bf16 a[108:111],a[220:223],v[236:239],0
buffer_load_dwordx4 v4,s[24:27],s47 offen offset:2048 nt lds
)A"
R"A(v_mfma_f32_16x16x32_bf16 a[108:111],a[216:219],v[232:235],a[108:111]
v_mov_b32 v60,0
v_mov_b32 v61,0
v_mfma_f32_16x16x32_bf16 a[112:115],a[224:227],v[232:235],0
v_mov_b32 v62,0
v_mov_b32 v63,0
v_mfma_f32_16x16x32_bf16 a[112:115],a[228:231],v[236:239],a[112:115]
v_add_u32 v2,s48,v2
v_add_u32 v3,s48,v3
v_mfma_f32_16x16x32_bf16 a[116:119],a[236:239],v[236:239],0
s_mov_b32 s48,32768
ds_read_b64_tr_b16 a[192:193],v2
ds_read_b64_tr_b16 a[194:195],v2 offset:4096
v_mfma_f32_16x16x32_bf16 a[116:119],a[232:235],v[232:235],a[116:119]
ds_read_b64_tr_b16 a[196:197],v2 offset:8192
v_mul_f32 v114,v23,v114
v_mfma_f32_16x16x32_bf16 a[120:123],a[240:243],v[232:235],0
ds_read_b64_tr_b16 a[198:199],v2 offset:12288
v_mul_f32 v115,v23,v115
v_mfma_f32_16x16x32_bf16 a[120:123],a[244:247],v[236:239],a[120:123]
ds_read_b64_tr_b16 a[204:205],v3 offset:8192
v_mul_f32 v16,v23,v16
v_mfma_f32_16x16x32_bf16 a[124:127],a[252:255],v[236:239],0
ds_read_b64_tr_b16 a[206:207],v3 offset:12288
ds_read_b64_tr_b16 a[200:201],v3
v_mfma_f32_16x16x32_bf16 a[124:127],a[248:251],v[232:235],a[124:127]
ds_read_b64_tr_b16 a[202:203],v3 offset:4096
v_mul_f32 v17,v23,v17
v_smfmac_f32_16x16x64_bf16 v[60:63],v[28:31],v[232:239],v27
ds_read_b64_tr_b16 a[208:209],v2 offset:256
v_mfma_f32_16x16x32_bf16 v[200:203],a[128:131],v[144:147],v[40:43]
v_exp_f32 v136,v136
v_mfma_f32_16x16x32_bf16 v[200:203],a[132:135],v[176:179],v[200:203]
v_exp_f32 v137,v137
v_mfma_f32_16x16x32_bf16 v[200:203],a[136:139],v[208:211],v[200:203]
ds_read_b64_tr_b16 a[210:211],v2 offset:4352
v_cvt_pk_bf16_f32 v136,v136,v137
v_mfma_f32_16x16x32_bf16 v[200:203],a[140:143],v[240:243],v[200:203]
buffer_load_dwordx4 v5,s[24:27],s47 offen offset:3072 nt lds
v_mfma_f32_16x16x32_bf16 v[204:207],a[156:159],v[240:243],v[40:43]
s_or_b32 s47,s47,0x18000
v_exp_f32 v138,v138
v_mfma_f32_16x16x32_bf16 v[204:207],a[152:155],v[208:211],v[204:207]
s_add_u32 s47,s47,4096
v_exp_f32 v139,v139
v_mfma_f32_16x16x32_bf16 v[204:207],a[148:151],v[176:179],v[204:207]
s_and_b32 s47,s47,0x27000
v_exp_f32 v140,v140
v_mfma_f32_16x16x32_bf16 v[204:207],a[144:147],v[144:147],v[204:207]
s_or_b32 s47,s47,s46
v_exp_f32 v141,v141
v_mfma_f32_16x16x32_bf16 v[216:219],a[160:163],v[144:147],v[40:43]
v_exp_f32 v142,v142
v_mfma_f32_16x16x32_bf16 v[216:219],a[164:167],v[176:179],v[216:219]
v_exp_f32 v143,v143
v_mfma_f32_16x16x32_bf16 v[216:219],a[168:171],v[208:211],v[216:219]
ds_read_b64_tr_b16 a[212:213],v2 offset:8448
ds_read_b64_tr_b16 a[214:215],v2 offset:12544
v_mfma_f32_16x16x32_bf16 v[216:219],a[172:175],v[240:243],v[216:219]
v_cvt_pk_bf16_f32 v137,v138,v139
v_cvt_pk_bf16_f32 v138,v140,v141
v_mfma_f32_16x16x32_bf16 v[220:223],a[188:191],v[240:243],v[40:43]
ds_read_b64_tr_b16 a[220:221],v3 offset:8448
v_cvt_pk_bf16_f32 v139,v142,v143
v_mfma_f32_16x16x32_bf16 v[220:223],a[184:187],v[208:211],v[220:223]
ds_read_b64_tr_b16 a[222:223],v3 offset:12544
v_exp_f32 v152,v152
v_exp_f32 v153,v153
v_exp_f32 v154,v154
v_exp_f32 v155,v155
v_exp_f32 v156,v156
v_exp_f32 v157,v157
v_exp_f32 v158,v158
v_exp_f32 v159,v159
v_mfma_f32_16x16x32_bf16 v[220:223],a[180:183],v[176:179],v[220:223]
ds_read_b64_tr_b16 a[216:217],v3 offset:256
ds_read_b64_tr_b16 a[218:219],v3 offset:4352
v_cvt_pk_bf16_f32 v140,v152,v153
v_cvt_pk_bf16_f32 v141,v154,v155
v_cvt_pk_bf16_f32 v142,v156,v157
v_cvt_pk_bf16_f32 v143,v158,v159
v_mfma_f32_16x16x32_bf16 v[220:223],a[176:179],v[144:147],v[220:223]
s_waitcnt lgkmcnt(6)
ds_read_b64_tr_b16 a[224:225],v2 offset:512
ds_read_b64_tr_b16 a[226:227],v2 offset:4608
v_mfma_f32_16x16x32_bf16 a[0:3],a[192:195],v[136:139],a[0:3]
ds_read_b64_tr_b16 a[228:229],v2 offset:8704
ds_read_b64_tr_b16 a[230:231],v2 offset:12800
v_mfma_f32_16x16x32_bf16 a[0:3],a[196:199],v[140:143],a[0:3]
ds_read_b64_tr_b16 a[236:237],v3 offset:8704
ds_read_b64_tr_b16 a[238:239],v3 offset:12800
v_mfma_f32_16x16x32_bf16 a[4:7],a[204:207],v[140:143],a[4:7]
ds_read_b64_tr_b16 a[232:233],v3 offset:512
ds_read_b64_tr_b16 a[234:235],v3 offset:4608
v_mfma_f32_16x16x32_bf16 a[4:7],a[200:203],v[136:139],a[4:7]
ds_read_b64_tr_b16 a[240:241],v2 offset:768
ds_read_b64_tr_b16 a[242:243],v2 offset:4864
v_mfma_f32_16x16x32_bf16 a[8:11],a[208:211],v[136:139],a[8:11]
s_memrealtime s[76:77]
ds_read_b64_tr_b16 a[244:245],v2 offset:8960
ds_read_b64_tr_b16 a[246:247],v2 offset:13056
s_waitcnt lgkmcnt(15)
v_mfma_f32_16x16x32_bf16 a[8:11],a[212:215],v[140:143],a[8:11]
s_waitcnt lgkmcnt(14)
ds_read_b64_tr_b16 a[252:253],v3 offset:8960
ds_read_b64_tr_b16 a[254:255],v3 offset:13056
v_mfma_f32_16x16x32_bf16 a[12:15],a[220:223],v[140:143],a[12:15]
s_waitcnt lgkmcnt(14)
ds_read_b64_tr_b16 a[248:249],v3 offset:768
ds_read_b64_tr_b16 a[250:251],v3 offset:4864
v_mfma_f32_16x16x32_bf16 a[12:15],a[216:219],v[136:139],a[12:15]
s_waitcnt lgkmcnt(14)
v_mfma_f32_16x16x32_bf16 a[16:19],a[224:227],v[136:139],a[16:19]
s_waitcnt lgkmcnt(12)
v_mfma_f32_16x16x32_bf16 a[16:19],a[228:231],v[140:143],a[16:19]
s_waitcnt lgkmcnt(10)
v_mfma_f32_16x16x32_bf16 a[20:23],a[236:239],v[140:143],a[20:23]
s_waitcnt lgkmcnt(0)
v_mfma_f32_16x16x32_bf16 a[20:23],a[232:235],v[136:139],a[20:23]
s_sub_u32 s79,s78,s76
v_exp_f32 v168,v168
v_mfma_f32_16x16x32_bf16 a[24:27],a[240:243],v[136:139],a[24:27]
s_add_u32 s78,s76,s60
v_exp_f32 v169,v169
v_mfma_f32_16x16x32_bf16 a[24:27],a[244:247],v[140:143],a[24:27]
s_cmp_gt_i32 s79,s95
s_cbranch_scc1 L_ps6_%=
L_pr6_%=:
v_exp_f32 v170,v170
v_exp_f32 v171,v171
v_mfma_f32_16x16x32_bf16 a[28:31],a[252:255],v[140:143],a[28:31]
v_cvt_pk_bf16_f32 v114,v114,v115
v_cvt_pk_bf16_f32 v168,v168,v169
v_exp_f32 v172,v172
v_exp_f32 v173,v173
v_exp_f32 v174,v174
v_exp_f32 v175,v175
v_cvt_pk_bf16_f32 v115,v16,v17
v_cvt_pk_bf16_f32 v169,v170,v171
v_exp_f32 v184,v184
v_exp_f32 v185,v185
v_exp_f32 v186,v186
v_exp_f32 v187,v187
v_exp_f32 v188,v188
v_exp_f32 v189,v189
v_exp_f32 v190,v190
v_exp_f32 v191,v191
v_mfma_f32_16x16x32_bf16 a[28:31],a[248:251],v[136:139],a[28:31]
s_cmp_lg_u64 s[56:57],0
s_cbranch_scc1 L_slow0_%=
L_sret0_%=:
v_cvt_pk_bf16_f32 v170,v172,v173
v_cvt_pk_bf16_f32 v171,v174,v175
v_add_u32 v1,s48,v1
v_cvt_pk_bf16_f32 v172,v184,v185
v_cvt_pk_bf16_f32 v173,v186,v187
v_cvt_pk_bf16_f32 v174,v188,v189
v_cvt_pk_bf16_f32 v175,v190,v191
v_smfmac_f32_16x16x64_bf16 v[48:51],v[28:31],v[136:143],v27
s_mov_b32 s53,11
s_cmp_eq_u32 s40,3
s_cbranch_scc0 L_att1_%=
s_cmp_gt_i32 s18,0
s_cbranch_scc1 L_att1v0_%=
s_cmp_gt_i32 s19,0
s_cbranch_scc1 L_att1v1_%=
s_cmp_gt_i32 s100,0
s_cbranch_scc1 L_att1v2_%=
.p2align 5
L_att1v3_%=:
s_waitcnt vmcnt(8) lgkmcnt(0)
v_exp_f32 v200,v200
s_barrier
s_add_u32 m0,s49,s51
ds_read_b128 a[128:131],v1
s_mov_b32 s90,L_out_end_%=-L_pc_%=+128
v_exp_f32 v201,v201
s_lshr_b32 s45,s45,1
ds_read_b128 a[132:135],v1 offset:256
v_cvt_pk_bf16_f32 v200,v200,v201
buffer_load_dwordx4 v4,s[20:23],s47 offen nt lds
ds_read_b128 a[136:139],v1 offset:512
v_add_u32 v2,s48,v2
v_exp_f32 v202,v202
s_add_u32 s62,s50,32768
ds_read_b128 a[140:143],v1 offset:768
v_add_u32 v3,s48,v3
s_and_b32 s62,s62,131071
v_exp_f32 v203,v203
s_add_u32 s54,s54,16384
ds_read_b128 a[156:159],v1 offset:4864
v_cvt_pk_bf16_f32 v201,v202,v203
s_sub_u32 s48,s62,s50
v_exp_f32 v204,v204
s_sub_u32 s53,s53,1
v_exp_f32 v205,v205
s_and_b32 s46,s54,0xfffc0000
ds_read_b128 a[152:155],v1 offset:4608
v_cvt_pk_bf16_f32 v202,v204,v205
s_mov_b32 s50,s62
v_exp_f32 v206,v206
v_exp_f32 v207,v207
ds_read_b128 a[148:151],v1 offset:4352
v_cvt_pk_bf16_f32 v203,v206,v207
v_mfma_f32_16x16x32_bf16 a[32:35],a[192:195],v[168:171],a[32:35]
v_exp_f32 v216,v216
v_mfma_f32_16x16x32_bf16 a[32:35],a[196:199],v[172:175],a[32:35]
ds_read_b128 a[144:147],v1 offset:4096
v_mfma_f32_16x16x32_bf16 a[36:39],a[204:207],v[172:175],a[36:39]
buffer_load_dwordx4 v4,s[20:23],s47 offen offset:1024 nt lds
v_mfma_f32_16x16x32_bf16 a[36:39],a[200:203],v[168:171],a[36:39]
ds_read_b128 a[160:163],v1 offset:8192
v_mfma_f32_16x16x32_bf16 a[40:43],a[208:211],v[168:171],a[40:43]
v_exp_f32 v217,v217
v_mfma_f32_16x16x32_bf16 a[40:43],a[212:215],v[172:175],a[40:43]
ds_read_b128 a[164:167],v1 offset:8448
v_cvt_pk_bf16_f32 v204,v216,v217
v_mfma_f32_16x16x32_bf16 a[44:47],a[220:223],v[172:175],a[44:47]
v_exp_f32 v218,v218
v_mfma_f32_16x16x32_bf16 a[44:47],a[216:219],v[168:171],a[44:47]
ds_read_b128 a[168:171],v1 offset:8704
v_mfma_f32_16x16x32_bf16 a[48:51],a[224:227],v[168:171],a[48:51]
v_exp_f32 v219,v219
v_mfma_f32_16x16x32_bf16 a[48:51],a[228:231],v[172:175],a[48:51]
ds_read_b128 a[172:175],v1 offset:8960
v_cvt_pk_bf16_f32 v205,v218,v219
v_mfma_f32_16x16x32_bf16 a[52:55],a[236:239],v[172:175],a[52:55]
v_exp_f32 v220,v220
v_mfma_f32_16x16x32_bf16 a[52:55],a[232:235],v[168:171],a[52:55]
ds_read_b128 a[188:191],v1 offset:13056
v_mfma_f32_16x16x32_bf16 a[56:59],a[240:243],v[168:171],a[56:59]
v_exp_f32 v221,v221
v_mfma_f32_16x16x32_bf16 a[56:59],a[244:247],v[172:175],a[56:59]
ds_read_b128 a[184:187],v1 offset:12800
v_cvt_pk_bf16_f32 v206,v220,v221
v_mfma_f32_16x16x32_bf16 a[60:63],a[252:255],v[172:175],a[60:63]
v_exp_f32 v222,v222
v_mfma_f32_16x16x32_bf16 a[60:63],a[248:251],v[168:171],a[60:63]
ds_read_b128 a[180:183],v1 offset:12544
v_smfmac_f32_16x16x64_bf16 v[52:55],v[28:31],v[168:175],v27
s_waitcnt lgkmcnt(4)
v_mfma_f32_16x16x32_bf16 v[136:139],a[128:131],v[128:131],v[32:35]
buffer_load_dwordx4 v5,s[20:23],s47 offen offset:2048 nt lds
v_mfma_f32_16x16x32_bf16 v[136:139],a[132:135],v[160:163],v[136:139]
ds_read_b128 a[176:179],v1 offset:12288
v_add_u32 v1,s48,v1
v_mfma_f32_16x16x32_bf16 v[136:139],a[136:139],v[192:195],v[136:139]
v_exp_f32 v223,v223
v_mfma_f32_16x16x32_bf16 v[136:139],a[140:143],v[224:227],v[136:139]
v_cvt_pk_bf16_f32 v207,v222,v223
v_mfma_f32_16x16x32_bf16 v[140:143],a[156:159],v[224:227],v[32:35]
v_mfma_f32_16x16x32_bf16 v[140:143],a[152:155],v[192:195],v[140:143]
v_mfma_f32_16x16x32_bf16 v[140:143],a[148:151],v[160:163],v[140:143]
v_mfma_f32_16x16x32_bf16 v[140:143],a[144:147],v[128:131],v[140:143]
v_mfma_f32_16x16x32_bf16 v[152:155],a[160:163],v[128:131],v[32:35]
v_mfma_f32_16x16x32_bf16 v[152:155],a[164:167],v[160:163],v[152:155]
v_mfma_f32_16x16x32_bf16 v[152:155],a[168:171],v[192:195],v[152:155]
s_waitcnt lgkmcnt(0)
v_mfma_f32_16x16x32_bf16 v[152:155],a[172:175],v[224:227],v[152:155]
v_mfma_f32_16x16x32_bf16 v[156:159],a[188:191],v[224:227],v[32:35]
v_mfma_f32_16x16x32_bf16 v[156:159],a[184:187],v[192:195],v[156:159]
buffer_load_dwordx4 v5,s[20:23],s47 offen offset:3072 nt lds
v_mfma_f32_16x16x32_bf16 v[156:159],a[180:183],v[160:163],v[156:159]
v_mfma_f32_16x16x32_bf16 v[156:159],a[176:179],v[128:131],v[156:159]
v_mfma_f32_16x16x32_bf16 a[64:67],a[192:195],v[200:203],a[64:67]
v_mfma_f32_16x16x32_bf16 a[64:67],a[196:199],v[204:207],a[64:67]
v_mfma_f32_16x16x32_bf16 a[68:71],a[204:207],v[204:207],a[68:71]
v_mfma_f32_16x16x32_bf16 a[68:71],a[200:203],v[200:203],a[68:71]
v_mfma_f32_16x16x32_bf16 a[72:75],a[208:211],v[200:203],a[72:75]
v_mfma_f32_16x16x32_bf16 a[72:75],a[212:215],v[204:207],a[72:75]
v_exp_f32 v136,v136
v_mfma_f32_16x16x32_bf16 a[76:79],a[220:223],v[204:207],a[76:79]
v_exp_f32 v137,v137
v_mfma_f32_16x16x32_bf16 a[76:79],a[216:219],v[200:203],a[76:79]
v_exp_f32 v138,v138
v_mfma_f32_16x16x32_bf16 a[80:83],a[224:227],v[200:203],a[80:83]
s_add_u32 m0,s49,s52
v_mfma_f32_16x16x32_bf16 a[80:83],a[228:231],v[204:207],a[80:83]
s_xor_b32 s49,s50,65536
v_mfma_f32_16x16x32_bf16 a[84:87],a[236:239],v[204:207],a[84:87]
v_mfma_f32_16x16x32_bf16 a[84:87],a[232:235],v[200:203],a[84:87]
buffer_load_dwordx4 v4,s[24:27],s47 offen nt lds
v_mfma_f32_16x16x32_bf16 a[88:91],a[240:243],v[200:203],a[88:91]
v_mfma_f32_16x16x32_bf16 a[88:91],a[244:247],v[204:207],a[88:91]
v_exp_f32 v139,v139
v_mfma_f32_16x16x32_bf16 a[92:95],a[252:255],v[204:207],a[92:95]
v_exp_f32 v140,v140
v_mfma_f32_16x16x32_bf16 a[92:95],a[248:251],v[200:203],a[92:95]
v_exp_f32 v141,v141
v_smfmac_f32_16x16x64_bf16 v[56:59],v[28:31],v[200:207],v27
v_mfma_f32_16x16x32_bf16 v[168:171],a[128:131],v[132:135],v[36:39]
v_exp_f32 v142,v142
v_mfma_f32_16x16x32_bf16 v[168:171],a[132:135],v[164:167],v[168:171]
v_exp_f32 v143,v143
v_mfma_f32_16x16x32_bf16 v[168:171],a[136:139],v[196:199],v[168:171]
)A"
R"A(v_exp_f32 v152,v152
v_mfma_f32_16x16x32_bf16 v[168:171],a[140:143],v[228:231],v[168:171]
v_exp_f32 v153,v153
v_mfma_f32_16x16x32_bf16 v[172:175],a[156:159],v[228:231],v[36:39]
v_exp_f32 v154,v154
v_mfma_f32_16x16x32_bf16 v[172:175],a[152:155],v[196:199],v[172:175]
v_exp_f32 v155,v155
v_mfma_f32_16x16x32_bf16 v[172:175],a[148:151],v[164:167],v[172:175]
v_exp_f32 v156,v156
v_mfma_f32_16x16x32_bf16 v[172:175],a[144:147],v[132:135],v[172:175]
v_exp_f32 v157,v157
v_mfma_f32_16x16x32_bf16 v[184:187],a[160:163],v[132:135],v[36:39]
buffer_load_dwordx4 v5,s[24:27],s47 offen offset:1024 nt lds
v_mfma_f32_16x16x32_bf16 v[184:187],a[164:167],v[164:167],v[184:187]
v_exp_f32 v158,v158
v_mfma_f32_16x16x32_bf16 v[184:187],a[168:171],v[196:199],v[184:187]
v_exp_f32 v159,v159
v_mfma_f32_16x16x32_bf16 v[184:187],a[172:175],v[228:231],v[184:187]
v_exp_f32 v168,v168
v_mfma_f32_16x16x32_bf16 v[188:191],a[188:191],v[228:231],v[36:39]
v_exp_f32 v169,v169
v_mfma_f32_16x16x32_bf16 v[188:191],a[184:187],v[196:199],v[188:191]
v_exp_f32 v170,v170
v_mfma_f32_16x16x32_bf16 v[188:191],a[180:183],v[164:167],v[188:191]
v_exp_f32 v171,v171
v_mfma_f32_16x16x32_bf16 v[188:191],a[176:179],v[132:135],v[188:191]
v_exp_f32 v172,v172
ds_read_b64_tr_b16 a[192:193],v2
v_cvt_pk_bf16_f32 v168,v168,v169
ds_read_b64_tr_b16 a[194:195],v2 offset:4096
v_cvt_pk_bf16_f32 v169,v170,v171
v_exp_f32 v173,v173
ds_read_b64_tr_b16 a[196:197],v2 offset:8192
v_cvt_pk_bf16_f32 v170,v172,v173
v_exp_f32 v174,v174
v_exp_f32 v175,v175
buffer_load_dwordx4 v4,s[24:27],s47 offen offset:2048 nt lds
ds_read_b64_tr_b16 a[198:199],v2 offset:12288
v_cvt_pk_bf16_f32 v171,v174,v175
v_exp_f32 v184,v184
v_exp_f32 v185,v185
ds_read_b64_tr_b16 a[204:205],v3 offset:8192
v_cvt_pk_bf16_f32 v136,v136,v137
ds_read_b64_tr_b16 a[206:207],v3 offset:12288
v_cvt_pk_bf16_f32 v172,v184,v185
ds_read_b64_tr_b16 a[200:201],v3
ds_read_b64_tr_b16 a[202:203],v3 offset:4096
v_cvt_pk_bf16_f32 v137,v138,v139
ds_read_b64_tr_b16 a[208:209],v2 offset:256
ds_read_b64_tr_b16 a[210:211],v2 offset:4352
ds_read_b64_tr_b16 a[212:213],v2 offset:8448
v_mfma_f32_16x16x32_bf16 v[200:203],a[128:131],v[144:147],v[40:43]
ds_read_b64_tr_b16 a[214:215],v2 offset:12544
v_cvt_pk_bf16_f32 v138,v140,v141
v_mfma_f32_16x16x32_bf16 v[200:203],a[132:135],v[176:179],v[200:203]
ds_read_b64_tr_b16 a[220:221],v3 offset:8448
v_mfma_f32_16x16x32_bf16 v[200:203],a[136:139],v[208:211],v[200:203]
ds_read_b64_tr_b16 a[222:223],v3 offset:12544
v_cvt_pk_bf16_f32 v139,v142,v143
v_mfma_f32_16x16x32_bf16 v[200:203],a[140:143],v[240:243],v[200:203]
buffer_load_dwordx4 v5,s[24:27],s47 offen offset:3072 nt lds
v_mfma_f32_16x16x32_bf16 v[204:207],a[156:159],v[240:243],v[40:43]
s_or_b32 s47,s47,0x18000
s_add_u32 s47,s47,4096
ds_read_b64_tr_b16 a[216:217],v3 offset:256
v_mfma_f32_16x16x32_bf16 v[204:207],a[152:155],v[208:211],v[204:207]
s_and_b32 s47,s47,0x27000
ds_read_b64_tr_b16 a[218:219],v3 offset:4352
v_cvt_pk_bf16_f32 v140,v152,v153
v_mfma_f32_16x16x32_bf16 v[204:207],a[148:151],v[176:179],v[204:207]
s_or_b32 s47,s47,s46
ds_read_b64_tr_b16 a[224:225],v2 offset:512
v_mfma_f32_16x16x32_bf16 v[204:207],a[144:147],v[144:147],v[204:207]
ds_read_b64_tr_b16 a[226:227],v2 offset:4608
v_mfma_f32_16x16x32_bf16 v[216:219],a[160:163],v[144:147],v[40:43]
ds_read_b64_tr_b16 a[228:229],v2 offset:8704
v_cvt_pk_bf16_f32 v141,v154,v155
v_mfma_f32_16x16x32_bf16 v[216:219],a[164:167],v[176:179],v[216:219]
ds_read_b64_tr_b16 a[230:231],v2 offset:12800
v_mfma_f32_16x16x32_bf16 v[216:219],a[168:171],v[208:211],v[216:219]
ds_read_b64_tr_b16 a[236:237],v3 offset:8704
v_mfma_f32_16x16x32_bf16 v[216:219],a[172:175],v[240:243],v[216:219]
ds_read_b64_tr_b16 a[238:239],v3 offset:12800
v_cvt_pk_bf16_f32 v142,v156,v157
v_mfma_f32_16x16x32_bf16 v[220:223],a[188:191],v[240:243],v[40:43]
ds_read_b64_tr_b16 a[232:233],v3 offset:512
v_mfma_f32_16x16x32_bf16 v[220:223],a[184:187],v[208:211],v[220:223]
ds_read_b64_tr_b16 a[234:235],v3 offset:4608
v_mfma_f32_16x16x32_bf16 v[220:223],a[180:183],v[176:179],v[220:223]
ds_read_b64_tr_b16 a[240:241],v2 offset:768
v_cvt_pk_bf16_f32 v143,v158,v159
v_mfma_f32_16x16x32_bf16 v[220:223],a[176:179],v[144:147],v[220:223]
s_waitcnt lgkmcnt(7)
buffer_load_dword v26,v24,s[88:91],s93 offen
v_mfma_f32_16x16x32_bf16 a[0:3],a[192:195],v[136:139],a[0:3]
ds_read_b64_tr_b16 a[242:243],v2 offset:4864
v_mfma_f32_16x16x32_bf16 a[0:3],a[196:199],v[140:143],a[0:3]
ds_read_b64_tr_b16 a[244:245],v2 offset:8960
v_mfma_f32_16x16x32_bf16 a[4:7],a[204:207],v[140:143],a[4:7]
ds_read_b64_tr_b16 a[246:247],v2 offset:13056
v_mfma_f32_16x16x32_bf16 a[4:7],a[200:203],v[136:139],a[4:7]
ds_read_b64_tr_b16 a[252:253],v3 offset:8960
v_mfma_f32_16x16x32_bf16 a[8:11],a[208:211],v[136:139],a[8:11]
ds_read_b64_tr_b16 a[254:255],v3 offset:13056
v_mfma_f32_16x16x32_bf16 a[8:11],a[212:215],v[140:143],a[8:11]
ds_read_b64_tr_b16 a[248:249],v3 offset:768
v_mfma_f32_16x16x32_bf16 a[12:15],a[220:223],v[140:143],a[12:15]
ds_read_b64_tr_b16 a[250:251],v3 offset:4864
v_mfma_f32_16x16x32_bf16 a[12:15],a[216:219],v[136:139],a[12:15]
s_bitcmp1_b32 s45,0
s_cbranch_scc0 L_t31v31_%=
s_mov_b32 m0,s80
s_add_u32 s80,s80,4096
buffer_load_dwordx4 v25,s[24:27],s83 offen nt lds
s_add_u32 s83,s83,256
L_t31v31_%=:
v_mfma_f32_16x16x32_bf16 a[16:19],a[224:227],v[136:139],a[16:19]
s_waitcnt lgkmcnt(6)
v_exp_f32 v186,v186
v_mfma_f32_16x16x32_bf16 a[16:19],a[228:231],v[140:143],a[16:19]
v_exp_f32 v187,v187
v_mfma_f32_16x16x32_bf16 a[20:23],a[236:239],v[140:143],a[20:23]
v_exp_f32 v188,v188
v_mfma_f32_16x16x32_bf16 a[20:23],a[232:235],v[136:139],a[20:23]
v_exp_f32 v189,v189
v_mfma_f32_16x16x32_bf16 a[24:27],a[240:243],v[136:139],a[24:27]
s_waitcnt lgkmcnt(2)
v_exp_f32 v190,v190
v_mfma_f32_16x16x32_bf16 a[24:27],a[244:247],v[140:143],a[24:27]
v_cvt_pk_bf16_f32 v173,v186,v187
v_mfma_f32_16x16x32_bf16 a[28:31],a[252:255],v[140:143],a[28:31]
s_waitcnt lgkmcnt(0)
v_exp_f32 v191,v191
v_mfma_f32_16x16x32_bf16 a[28:31],a[248:251],v[136:139],a[28:31]
v_cvt_pk_bf16_f32 v174,v188,v189
v_cvt_pk_bf16_f32 v175,v190,v191
s_cmp_lg_u32 s53,0
v_smfmac_f32_16x16x64_bf16 v[48:51],v[28:31],v[136:143],v27
s_cbranch_scc1 L_att1v3_%=
s_waitcnt lgkmcnt(0)
s_nop 7
s_branch L_att1x_%=
.p2align 5
L_att1v0_%=:
v_mfma_f32_16x16x32_bf16 v[232:235],a[128:131],v[148:151],v[44:47]
s_waitcnt vmcnt(8) lgkmcnt(0)
v_exp_f32 v200,v200
v_mfma_f32_16x16x32_bf16 v[232:235],a[132:135],v[180:183],v[232:235]
s_barrier
s_add_u32 m0,s49,s51
ds_read_b128 a[128:131],v1
v_mfma_f32_16x16x32_bf16 v[232:235],a[136:139],v[212:215],v[232:235]
s_mov_b32 s90,L_out_end_%=-L_pc_%=+128
v_exp_f32 v201,v201
v_mfma_f32_16x16x32_bf16 v[232:235],a[140:143],v[244:247],v[232:235]
s_lshr_b32 s45,s45,1
ds_read_b128 a[132:135],v1 offset:256
v_cvt_pk_bf16_f32 v200,v200,v201
v_mfma_f32_16x16x32_bf16 v[236:239],a[156:159],v[244:247],v[44:47]
buffer_load_dwordx4 v4,s[20:23],s47 offen nt lds
v_mfma_f32_16x16x32_bf16 v[236:239],a[152:155],v[212:215],v[236:239]
ds_read_b128 a[136:139],v1 offset:512
v_add_u32 v2,s48,v2
v_mfma_f32_16x16x32_bf16 v[236:239],a[148:151],v[180:183],v[236:239]
v_exp_f32 v202,v202
v_mfma_f32_16x16x32_bf16 v[236:239],a[144:147],v[148:151],v[236:239]
s_add_u32 s62,s50,32768
ds_read_b128 a[140:143],v1 offset:768
v_add_u32 v3,s48,v3
v_mfma_f32_16x16x32_bf16 v[248:251],a[160:163],v[148:151],v[44:47]
s_and_b32 s62,s62,131071
v_exp_f32 v203,v203
v_mfma_f32_16x16x32_bf16 v[248:251],a[164:167],v[180:183],v[248:251]
s_add_u32 s54,s54,16384
ds_read_b128 a[156:159],v1 offset:4864
v_cvt_pk_bf16_f32 v201,v202,v203
v_mfma_f32_16x16x32_bf16 v[248:251],a[168:171],v[212:215],v[248:251]
s_sub_u32 s48,s62,s50
v_exp_f32 v204,v204
v_mfma_f32_16x16x32_bf16 v[248:251],a[172:175],v[244:247],v[248:251]
s_sub_u32 s53,s53,1
v_exp_f32 v205,v205
v_mfma_f32_16x16x32_bf16 v[252:255],a[188:191],v[244:247],v[44:47]
s_and_b32 s46,s54,0xfffc0000
ds_read_b128 a[152:155],v1 offset:4608
v_cvt_pk_bf16_f32 v202,v204,v205
v_mfma_f32_16x16x32_bf16 v[252:255],a[184:187],v[212:215],v[252:255]
s_mov_b32 s50,s62
v_exp_f32 v206,v206
v_mfma_f32_16x16x32_bf16 v[252:255],a[180:183],v[180:183],v[252:255]
v_exp_f32 v207,v207
v_mfma_f32_16x16x32_bf16 v[252:255],a[176:179],v[148:151],v[252:255]
ds_read_b128 a[148:151],v1 offset:4352
v_cvt_pk_bf16_f32 v203,v206,v207
v_mfma_f32_16x16x32_bf16 a[32:35],a[192:195],v[168:171],a[32:35]
v_exp_f32 v216,v216
v_mfma_f32_16x16x32_bf16 a[32:35],a[196:199],v[172:175],a[32:35]
ds_read_b128 a[144:147],v1 offset:4096
v_mfma_f32_16x16x32_bf16 a[36:39],a[204:207],v[172:175],a[36:39]
buffer_load_dwordx4 v4,s[20:23],s47 offen offset:1024 nt lds
v_mfma_f32_16x16x32_bf16 a[36:39],a[200:203],v[168:171],a[36:39]
ds_read_b128 a[160:163],v1 offset:8192
v_mfma_f32_16x16x32_bf16 a[40:43],a[208:211],v[168:171],a[40:43]
v_exp_f32 v217,v217
v_mfma_f32_16x16x32_bf16 a[40:43],a[212:215],v[172:175],a[40:43]
ds_read_b128 a[164:167],v1 offset:8448
v_cvt_pk_bf16_f32 v204,v216,v217
v_mfma_f32_16x16x32_bf16 a[44:47],a[220:223],v[172:175],a[44:47]
v_exp_f32 v218,v218
v_mfma_f32_16x16x32_bf16 a[44:47],a[216:219],v[168:171],a[44:47]
ds_read_b128 a[168:171],v1 offset:8704
v_mfma_f32_16x16x32_bf16 a[48:51],a[224:227],v[168:171],a[48:51]
v_exp_f32 v219,v219
v_mfma_f32_16x16x32_bf16 a[48:51],a[228:231],v[172:175],a[48:51]
ds_read_b128 a[172:175],v1 offset:8960
v_cvt_pk_bf16_f32 v205,v218,v219
v_mfma_f32_16x16x32_bf16 a[52:55],a[236:239],v[172:175],a[52:55]
v_exp_f32 v220,v220
v_mfma_f32_16x16x32_bf16 a[52:55],a[232:235],v[168:171],a[52:55]
ds_read_b128 a[188:191],v1 offset:13056
v_mfma_f32_16x16x32_bf16 a[56:59],a[240:243],v[168:171],a[56:59]
v_exp_f32 v221,v221
v_mfma_f32_16x16x32_bf16 a[56:59],a[244:247],v[172:175],a[56:59]
ds_read_b128 a[184:187],v1 offset:12800
v_cvt_pk_bf16_f32 v206,v220,v221
v_mfma_f32_16x16x32_bf16 a[60:63],a[252:255],v[172:175],a[60:63]
v_exp_f32 v222,v222
v_mfma_f32_16x16x32_bf16 a[60:63],a[248:251],v[168:171],a[60:63]
ds_read_b128 a[180:183],v1 offset:12544
v_smfmac_f32_16x16x64_bf16 v[52:55],v[28:31],v[168:175],v27
buffer_load_dwordx4 v5,s[20:23],s47 offen offset:2048 nt lds
ds_read_b128 a[176:179],v1 offset:12288
v_add_u32 v1,s48,v1
v_exp_f32 v223,v223
s_nop 0
v_cvt_pk_bf16_f32 v207,v222,v223
v_exp_f32 v232,v232
v_exp_f32 v233,v233
v_exp_f32 v234,v234
v_exp_f32 v235,v235
v_exp_f32 v236,v236
v_exp_f32 v237,v237
v_exp_f32 v238,v238
v_exp_f32 v239,v239
v_exp_f32 v248,v248
buffer_load_dwordx4 v5,s[20:23],s47 offen offset:3072 nt lds
v_exp_f32 v249,v249
v_exp_f32 v250,v250
v_mfma_f32_16x16x32_bf16 a[64:67],a[192:195],v[200:203],a[64:67]
v_exp_f32 v251,v251
v_mfma_f32_16x16x32_bf16 a[64:67],a[196:199],v[204:207],a[64:67]
v_exp_f32 v252,v252
v_mfma_f32_16x16x32_bf16 a[68:71],a[204:207],v[204:207],a[68:71]
v_exp_f32 v253,v253
v_mfma_f32_16x16x32_bf16 a[68:71],a[200:203],v[200:203],a[68:71]
v_exp_f32 v254,v254
v_mfma_f32_16x16x32_bf16 a[72:75],a[208:211],v[200:203],a[72:75]
v_exp_f32 v255,v255
v_mfma_f32_16x16x32_bf16 a[72:75],a[212:215],v[204:207],a[72:75]
v_mfma_f32_16x16x32_bf16 a[76:79],a[220:223],v[204:207],a[76:79]
v_mfma_f32_16x16x32_bf16 a[76:79],a[216:219],v[200:203],a[76:79]
v_mfma_f32_16x16x32_bf16 a[80:83],a[224:227],v[200:203],a[80:83]
s_add_u32 m0,s49,s52
v_cvt_pk_bf16_f32 v232,v232,v233
v_cvt_pk_bf16_f32 v233,v234,v235
v_mfma_f32_16x16x32_bf16 a[80:83],a[228:231],v[204:207],a[80:83]
s_xor_b32 s49,s50,65536
v_cvt_pk_bf16_f32 v234,v236,v237
v_cvt_pk_bf16_f32 v235,v238,v239
v_mfma_f32_16x16x32_bf16 a[84:87],a[236:239],v[204:207],a[84:87]
v_cvt_pk_bf16_f32 v236,v248,v249
v_cvt_pk_bf16_f32 v237,v250,v251
v_mfma_f32_16x16x32_bf16 a[84:87],a[232:235],v[200:203],a[84:87]
buffer_load_dwordx4 v4,s[24:27],s47 offen nt lds
v_mfma_f32_16x16x32_bf16 a[88:91],a[240:243],v[200:203],a[88:91]
v_cvt_pk_bf16_f32 v238,v252,v253
v_cvt_pk_bf16_f32 v239,v254,v255
v_mfma_f32_16x16x32_bf16 a[88:91],a[244:247],v[204:207],a[88:91]
v_mfma_f32_16x16x32_bf16 a[92:95],a[252:255],v[204:207],a[92:95]
v_mfma_f32_16x16x32_bf16 a[92:95],a[248:251],v[200:203],a[92:95]
v_smfmac_f32_16x16x64_bf16 v[56:59],v[28:31],v[200:207],v27
s_waitcnt lgkmcnt(0)
v_mfma_f32_16x16x32_bf16 v[168:171],a[128:131],v[132:135],v[36:39]
)A"
R"A(v_mfma_f32_16x16x32_bf16 v[168:171],a[132:135],v[164:167],v[168:171]
v_mfma_f32_16x16x32_bf16 v[168:171],a[136:139],v[196:199],v[168:171]
v_mfma_f32_16x16x32_bf16 v[168:171],a[140:143],v[228:231],v[168:171]
v_mfma_f32_16x16x32_bf16 v[172:175],a[156:159],v[228:231],v[36:39]
v_mfma_f32_16x16x32_bf16 v[172:175],a[152:155],v[196:199],v[172:175]
v_mfma_f32_16x16x32_bf16 v[172:175],a[148:151],v[164:167],v[172:175]
v_mfma_f32_16x16x32_bf16 v[172:175],a[144:147],v[132:135],v[172:175]
v_mfma_f32_16x16x32_bf16 v[184:187],a[160:163],v[132:135],v[36:39]
buffer_load_dwordx4 v5,s[24:27],s47 offen offset:1024 nt lds
v_mfma_f32_16x16x32_bf16 v[184:187],a[164:167],v[164:167],v[184:187]
v_mfma_f32_16x16x32_bf16 v[184:187],a[168:171],v[196:199],v[184:187]
v_mfma_f32_16x16x32_bf16 v[184:187],a[172:175],v[228:231],v[184:187]
v_exp_f32 v168,v168
v_mfma_f32_16x16x32_bf16 v[188:191],a[188:191],v[228:231],v[36:39]
v_exp_f32 v169,v169
v_mfma_f32_16x16x32_bf16 v[188:191],a[184:187],v[196:199],v[188:191]
v_exp_f32 v170,v170
v_mfma_f32_16x16x32_bf16 v[188:191],a[180:183],v[164:167],v[188:191]
v_exp_f32 v171,v171
v_mfma_f32_16x16x32_bf16 v[188:191],a[176:179],v[132:135],v[188:191]
v_exp_f32 v172,v172
v_mfma_f32_16x16x32_bf16 a[96:99],a[192:195],v[232:235],a[96:99]
ds_read_b64_tr_b16 a[192:193],v2
v_cvt_pk_bf16_f32 v168,v168,v169
v_mfma_f32_16x16x32_bf16 a[96:99],a[196:199],v[236:239],a[96:99]
ds_read_b64_tr_b16 a[194:195],v2 offset:4096
v_cvt_pk_bf16_f32 v169,v170,v171
v_mfma_f32_16x16x32_bf16 a[100:103],a[204:207],v[236:239],a[100:103]
v_exp_f32 v173,v173
v_mfma_f32_16x16x32_bf16 a[100:103],a[200:203],v[232:235],a[100:103]
ds_read_b64_tr_b16 a[196:197],v2 offset:8192
v_cvt_pk_bf16_f32 v170,v172,v173
v_mfma_f32_16x16x32_bf16 a[104:107],a[208:211],v[232:235],a[104:107]
v_exp_f32 v174,v174
v_mfma_f32_16x16x32_bf16 a[104:107],a[212:215],v[236:239],a[104:107]
v_exp_f32 v175,v175
v_mfma_f32_16x16x32_bf16 a[108:111],a[220:223],v[236:239],a[108:111]
buffer_load_dwordx4 v4,s[24:27],s47 offen offset:2048 nt lds
v_mfma_f32_16x16x32_bf16 a[108:111],a[216:219],v[232:235],a[108:111]
ds_read_b64_tr_b16 a[198:199],v2 offset:12288
v_cvt_pk_bf16_f32 v171,v174,v175
v_mfma_f32_16x16x32_bf16 a[112:115],a[224:227],v[232:235],a[112:115]
v_exp_f32 v184,v184
v_mfma_f32_16x16x32_bf16 a[112:115],a[228:231],v[236:239],a[112:115]
v_exp_f32 v185,v185
v_mfma_f32_16x16x32_bf16 a[116:119],a[236:239],v[236:239],a[116:119]
ds_read_b64_tr_b16 a[204:205],v3 offset:8192
v_mfma_f32_16x16x32_bf16 a[116:119],a[232:235],v[232:235],a[116:119]
ds_read_b64_tr_b16 a[206:207],v3 offset:12288
v_cvt_pk_bf16_f32 v172,v184,v185
v_mfma_f32_16x16x32_bf16 a[120:123],a[240:243],v[232:235],a[120:123]
ds_read_b64_tr_b16 a[200:201],v3
v_mfma_f32_16x16x32_bf16 a[120:123],a[244:247],v[236:239],a[120:123]
ds_read_b64_tr_b16 a[202:203],v3 offset:4096
v_mfma_f32_16x16x32_bf16 a[124:127],a[252:255],v[236:239],a[124:127]
ds_read_b64_tr_b16 a[208:209],v2 offset:256
v_mfma_f32_16x16x32_bf16 a[124:127],a[248:251],v[232:235],a[124:127]
ds_read_b64_tr_b16 a[210:211],v2 offset:4352
v_smfmac_f32_16x16x64_bf16 v[60:63],v[28:31],v[232:239],v27
ds_read_b64_tr_b16 a[212:213],v2 offset:8448
v_mfma_f32_16x16x32_bf16 v[200:203],a[128:131],v[144:147],v[40:43]
ds_read_b64_tr_b16 a[214:215],v2 offset:12544
v_mfma_f32_16x16x32_bf16 v[200:203],a[132:135],v[176:179],v[200:203]
ds_read_b64_tr_b16 a[220:221],v3 offset:8448
v_mfma_f32_16x16x32_bf16 v[200:203],a[136:139],v[208:211],v[200:203]
ds_read_b64_tr_b16 a[222:223],v3 offset:12544
v_mfma_f32_16x16x32_bf16 v[200:203],a[140:143],v[240:243],v[200:203]
buffer_load_dwordx4 v5,s[24:27],s47 offen offset:3072 nt lds
v_mfma_f32_16x16x32_bf16 v[204:207],a[156:159],v[240:243],v[40:43]
s_or_b32 s47,s47,0x18000
s_add_u32 s47,s47,4096
ds_read_b64_tr_b16 a[216:217],v3 offset:256
v_mfma_f32_16x16x32_bf16 v[204:207],a[152:155],v[208:211],v[204:207]
s_and_b32 s47,s47,0x27000
ds_read_b64_tr_b16 a[218:219],v3 offset:4352
v_mfma_f32_16x16x32_bf16 v[204:207],a[148:151],v[176:179],v[204:207]
s_or_b32 s47,s47,s46
ds_read_b64_tr_b16 a[224:225],v2 offset:512
v_mfma_f32_16x16x32_bf16 v[204:207],a[144:147],v[144:147],v[204:207]
ds_read_b64_tr_b16 a[226:227],v2 offset:4608
v_mfma_f32_16x16x32_bf16 v[216:219],a[160:163],v[144:147],v[40:43]
ds_read_b64_tr_b16 a[228:229],v2 offset:8704
v_mfma_f32_16x16x32_bf16 v[216:219],a[164:167],v[176:179],v[216:219]
ds_read_b64_tr_b16 a[230:231],v2 offset:12800
v_mfma_f32_16x16x32_bf16 v[216:219],a[168:171],v[208:211],v[216:219]
ds_read_b64_tr_b16 a[236:237],v3 offset:8704
v_mfma_f32_16x16x32_bf16 v[216:219],a[172:175],v[240:243],v[216:219]
ds_read_b64_tr_b16 a[238:239],v3 offset:12800
v_mfma_f32_16x16x32_bf16 v[220:223],a[188:191],v[240:243],v[40:43]
ds_read_b64_tr_b16 a[232:233],v3 offset:512
v_mfma_f32_16x16x32_bf16 v[220:223],a[184:187],v[208:211],v[220:223]
ds_read_b64_tr_b16 a[234:235],v3 offset:4608
v_mfma_f32_16x16x32_bf16 v[220:223],a[180:183],v[176:179],v[220:223]
ds_read_b64_tr_b16 a[240:241],v2 offset:768
v_mfma_f32_16x16x32_bf16 v[220:223],a[176:179],v[144:147],v[220:223]
buffer_load_dword v26,v24,s[88:91],s93 offen
ds_read_b64_tr_b16 a[242:243],v2 offset:4864
ds_read_b64_tr_b16 a[244:245],v2 offset:8960
ds_read_b64_tr_b16 a[246:247],v2 offset:13056
ds_read_b64_tr_b16 a[252:253],v3 offset:8960
ds_read_b64_tr_b16 a[254:255],v3 offset:13056
ds_read_b64_tr_b16 a[248:249],v3 offset:768
ds_read_b64_tr_b16 a[250:251],v3 offset:4864
s_bitcmp1_b32 s45,0
s_cbranch_scc0 L_t31v01_%=
s_mov_b32 m0,s80
s_add_u32 s80,s80,4096
buffer_load_dwordx4 v25,s[24:27],s83 offen nt lds
s_add_u32 s83,s83,256
L_t31v01_%=:
v_exp_f32 v186,v186
v_exp_f32 v187,v187
v_exp_f32 v188,v188
v_exp_f32 v189,v189
v_exp_f32 v190,v190
v_cvt_pk_bf16_f32 v173,v186,v187
v_exp_f32 v191,v191
v_cvt_pk_bf16_f32 v174,v188,v189
v_cvt_pk_bf16_f32 v175,v190,v191
s_cmp_lg_u32 s53,0
s_cbranch_scc1 L_att1v0_%=
s_waitcnt lgkmcnt(0)
s_nop 7
s_branch L_att1x_%=
.p2align 5
L_att1v1_%=:
v_mfma_f32_16x16x32_bf16 v[232:235],a[128:131],v[148:151],v[44:47]
s_waitcnt vmcnt(8) lgkmcnt(0)
v_exp_f32 v200,v200
v_mfma_f32_16x16x32_bf16 v[232:235],a[132:135],v[180:183],v[232:235]
s_barrier
s_add_u32 m0,s49,s51
ds_read_b128 a[128:131],v1
v_mfma_f32_16x16x32_bf16 v[232:235],a[136:139],v[212:215],v[232:235]
s_mov_b32 s90,L_out_end_%=-L_pc_%=+128
v_exp_f32 v201,v201
v_mfma_f32_16x16x32_bf16 v[232:235],a[140:143],v[244:247],v[232:235]
s_lshr_b32 s45,s45,1
ds_read_b128 a[132:135],v1 offset:256
v_cvt_pk_bf16_f32 v200,v200,v201
v_mfma_f32_16x16x32_bf16 v[236:239],a[156:159],v[244:247],v[44:47]
buffer_load_dwordx4 v4,s[20:23],s47 offen nt lds
v_mfma_f32_16x16x32_bf16 v[236:239],a[152:155],v[212:215],v[236:239]
ds_read_b128 a[136:139],v1 offset:512
v_add_u32 v2,s48,v2
v_mfma_f32_16x16x32_bf16 v[236:239],a[148:151],v[180:183],v[236:239]
v_exp_f32 v202,v202
v_mfma_f32_16x16x32_bf16 v[236:239],a[144:147],v[148:151],v[236:239]
s_add_u32 s62,s50,32768
ds_read_b128 a[140:143],v1 offset:768
v_add_u32 v3,s48,v3
v_mfma_f32_16x16x32_bf16 v[248:251],a[160:163],v[148:151],v[44:47]
s_and_b32 s62,s62,131071
v_exp_f32 v203,v203
v_mfma_f32_16x16x32_bf16 v[248:251],a[164:167],v[180:183],v[248:251]
s_add_u32 s54,s54,16384
ds_read_b128 a[156:159],v1 offset:4864
v_cvt_pk_bf16_f32 v201,v202,v203
v_mfma_f32_16x16x32_bf16 v[248:251],a[168:171],v[212:215],v[248:251]
s_sub_u32 s48,s62,s50
v_exp_f32 v204,v204
v_mfma_f32_16x16x32_bf16 v[248:251],a[172:175],v[244:247],v[248:251]
s_sub_u32 s53,s53,1
v_exp_f32 v205,v205
v_mfma_f32_16x16x32_bf16 v[252:255],a[188:191],v[244:247],v[44:47]
s_and_b32 s46,s54,0xfffc0000
ds_read_b128 a[152:155],v1 offset:4608
v_cvt_pk_bf16_f32 v202,v204,v205
v_mfma_f32_16x16x32_bf16 v[252:255],a[184:187],v[212:215],v[252:255]
s_mov_b32 s50,s62
v_exp_f32 v206,v206
v_mfma_f32_16x16x32_bf16 v[252:255],a[180:183],v[180:183],v[252:255]
v_exp_f32 v207,v207
v_mfma_f32_16x16x32_bf16 v[252:255],a[176:179],v[148:151],v[252:255]
ds_read_b128 a[148:151],v1 offset:4352
v_cvt_pk_bf16_f32 v203,v206,v207
v_exp_f32 v216,v216
ds_read_b128 a[144:147],v1 offset:4096
buffer_load_dwordx4 v4,s[20:23],s47 offen offset:1024 nt lds
ds_read_b128 a[160:163],v1 offset:8192
v_exp_f32 v217,v217
ds_read_b128 a[164:167],v1 offset:8448
v_cvt_pk_bf16_f32 v204,v216,v217
v_exp_f32 v218,v218
ds_read_b128 a[168:171],v1 offset:8704
v_exp_f32 v219,v219
ds_read_b128 a[172:175],v1 offset:8960
v_cvt_pk_bf16_f32 v205,v218,v219
v_exp_f32 v220,v220
ds_read_b128 a[188:191],v1 offset:13056
v_exp_f32 v221,v221
ds_read_b128 a[184:187],v1 offset:12800
v_cvt_pk_bf16_f32 v206,v220,v221
v_exp_f32 v222,v222
ds_read_b128 a[180:183],v1 offset:12544
s_waitcnt lgkmcnt(11)
v_mfma_f32_16x16x32_bf16 v[136:139],a[128:131],v[128:131],v[32:35]
buffer_load_dwordx4 v5,s[20:23],s47 offen offset:2048 nt lds
v_mfma_f32_16x16x32_bf16 v[136:139],a[132:135],v[160:163],v[136:139]
ds_read_b128 a[176:179],v1 offset:12288
v_add_u32 v1,s48,v1
v_mfma_f32_16x16x32_bf16 v[136:139],a[136:139],v[192:195],v[136:139]
v_exp_f32 v223,v223
v_mfma_f32_16x16x32_bf16 v[136:139],a[140:143],v[224:227],v[136:139]
s_waitcnt lgkmcnt(11)
v_cvt_pk_bf16_f32 v207,v222,v223
v_mfma_f32_16x16x32_bf16 v[140:143],a[156:159],v[224:227],v[32:35]
s_waitcnt lgkmcnt(10)
v_exp_f32 v232,v232
v_mfma_f32_16x16x32_bf16 v[140:143],a[152:155],v[192:195],v[140:143]
s_waitcnt lgkmcnt(9)
v_exp_f32 v233,v233
v_mfma_f32_16x16x32_bf16 v[140:143],a[148:151],v[160:163],v[140:143]
s_waitcnt lgkmcnt(8)
v_exp_f32 v234,v234
v_mfma_f32_16x16x32_bf16 v[140:143],a[144:147],v[128:131],v[140:143]
s_waitcnt lgkmcnt(1)
v_exp_f32 v235,v235
v_mfma_f32_16x16x32_bf16 v[152:155],a[160:163],v[128:131],v[32:35]
v_exp_f32 v236,v236
v_mfma_f32_16x16x32_bf16 v[152:155],a[164:167],v[160:163],v[152:155]
v_exp_f32 v237,v237
v_mfma_f32_16x16x32_bf16 v[152:155],a[168:171],v[192:195],v[152:155]
v_exp_f32 v238,v238
v_mfma_f32_16x16x32_bf16 v[152:155],a[172:175],v[224:227],v[152:155]
v_exp_f32 v239,v239
v_mfma_f32_16x16x32_bf16 v[156:159],a[188:191],v[224:227],v[32:35]
v_exp_f32 v248,v248
v_mfma_f32_16x16x32_bf16 v[156:159],a[184:187],v[192:195],v[156:159]
buffer_load_dwordx4 v5,s[20:23],s47 offen offset:3072 nt lds
v_mfma_f32_16x16x32_bf16 v[156:159],a[180:183],v[160:163],v[156:159]
s_waitcnt lgkmcnt(0)
v_exp_f32 v249,v249
v_mfma_f32_16x16x32_bf16 v[156:159],a[176:179],v[128:131],v[156:159]
v_exp_f32 v250,v250
v_mfma_f32_16x16x32_bf16 a[64:67],a[192:195],v[200:203],a[64:67]
v_exp_f32 v251,v251
v_mfma_f32_16x16x32_bf16 a[64:67],a[196:199],v[204:207],a[64:67]
v_exp_f32 v252,v252
v_mfma_f32_16x16x32_bf16 a[68:71],a[204:207],v[204:207],a[68:71]
v_exp_f32 v253,v253
v_mfma_f32_16x16x32_bf16 a[68:71],a[200:203],v[200:203],a[68:71]
v_exp_f32 v254,v254
v_mfma_f32_16x16x32_bf16 a[72:75],a[208:211],v[200:203],a[72:75]
v_exp_f32 v255,v255
v_mfma_f32_16x16x32_bf16 a[72:75],a[212:215],v[204:207],a[72:75]
v_exp_f32 v136,v136
v_mfma_f32_16x16x32_bf16 a[76:79],a[220:223],v[204:207],a[76:79]
v_exp_f32 v137,v137
v_mfma_f32_16x16x32_bf16 a[76:79],a[216:219],v[200:203],a[76:79]
v_exp_f32 v138,v138
v_mfma_f32_16x16x32_bf16 a[80:83],a[224:227],v[200:203],a[80:83]
s_add_u32 m0,s49,s52
v_cvt_pk_bf16_f32 v232,v232,v233
v_cvt_pk_bf16_f32 v233,v234,v235
v_mfma_f32_16x16x32_bf16 a[80:83],a[228:231],v[204:207],a[80:83]
s_xor_b32 s49,s50,65536
v_cvt_pk_bf16_f32 v234,v236,v237
v_cvt_pk_bf16_f32 v235,v238,v239
v_mfma_f32_16x16x32_bf16 a[84:87],a[236:239],v[204:207],a[84:87]
v_cvt_pk_bf16_f32 v236,v248,v249
v_cvt_pk_bf16_f32 v237,v250,v251
v_mfma_f32_16x16x32_bf16 a[84:87],a[232:235],v[200:203],a[84:87]
buffer_load_dwordx4 v4,s[24:27],s47 offen nt lds
v_mfma_f32_16x16x32_bf16 a[88:91],a[240:243],v[200:203],a[88:91]
v_cvt_pk_bf16_f32 v238,v252,v253
v_cvt_pk_bf16_f32 v239,v254,v255
v_mfma_f32_16x16x32_bf16 a[88:91],a[244:247],v[204:207],a[88:91]
v_exp_f32 v139,v139
v_mfma_f32_16x16x32_bf16 a[92:95],a[252:255],v[204:207],a[92:95]
v_exp_f32 v140,v140
v_mfma_f32_16x16x32_bf16 a[92:95],a[248:251],v[200:203],a[92:95]
v_exp_f32 v141,v141
v_smfmac_f32_16x16x64_bf16 v[56:59],v[28:31],v[200:207],v27
v_exp_f32 v142,v142
v_exp_f32 v143,v143
v_exp_f32 v152,v152
v_exp_f32 v153,v153
v_exp_f32 v154,v154
)A"
R"A(v_exp_f32 v155,v155
v_exp_f32 v156,v156
v_exp_f32 v157,v157
buffer_load_dwordx4 v5,s[24:27],s47 offen offset:1024 nt lds
v_exp_f32 v158,v158
v_exp_f32 v159,v159
v_mfma_f32_16x16x32_bf16 a[96:99],a[192:195],v[232:235],a[96:99]
ds_read_b64_tr_b16 a[192:193],v2
v_mfma_f32_16x16x32_bf16 a[96:99],a[196:199],v[236:239],a[96:99]
ds_read_b64_tr_b16 a[194:195],v2 offset:4096
v_mfma_f32_16x16x32_bf16 a[100:103],a[204:207],v[236:239],a[100:103]
v_mfma_f32_16x16x32_bf16 a[100:103],a[200:203],v[232:235],a[100:103]
ds_read_b64_tr_b16 a[196:197],v2 offset:8192
v_mfma_f32_16x16x32_bf16 a[104:107],a[208:211],v[232:235],a[104:107]
v_mfma_f32_16x16x32_bf16 a[104:107],a[212:215],v[236:239],a[104:107]
v_mfma_f32_16x16x32_bf16 a[108:111],a[220:223],v[236:239],a[108:111]
buffer_load_dwordx4 v4,s[24:27],s47 offen offset:2048 nt lds
v_mfma_f32_16x16x32_bf16 a[108:111],a[216:219],v[232:235],a[108:111]
ds_read_b64_tr_b16 a[198:199],v2 offset:12288
v_mfma_f32_16x16x32_bf16 a[112:115],a[224:227],v[232:235],a[112:115]
v_mfma_f32_16x16x32_bf16 a[112:115],a[228:231],v[236:239],a[112:115]
v_mfma_f32_16x16x32_bf16 a[116:119],a[236:239],v[236:239],a[116:119]
ds_read_b64_tr_b16 a[204:205],v3 offset:8192
v_cvt_pk_bf16_f32 v136,v136,v137
v_mfma_f32_16x16x32_bf16 a[116:119],a[232:235],v[232:235],a[116:119]
ds_read_b64_tr_b16 a[206:207],v3 offset:12288
v_mfma_f32_16x16x32_bf16 a[120:123],a[240:243],v[232:235],a[120:123]
ds_read_b64_tr_b16 a[200:201],v3
v_mfma_f32_16x16x32_bf16 a[120:123],a[244:247],v[236:239],a[120:123]
ds_read_b64_tr_b16 a[202:203],v3 offset:4096
v_cvt_pk_bf16_f32 v137,v138,v139
v_mfma_f32_16x16x32_bf16 a[124:127],a[252:255],v[236:239],a[124:127]
ds_read_b64_tr_b16 a[208:209],v2 offset:256
v_mfma_f32_16x16x32_bf16 a[124:127],a[248:251],v[232:235],a[124:127]
ds_read_b64_tr_b16 a[210:211],v2 offset:4352
v_smfmac_f32_16x16x64_bf16 v[60:63],v[28:31],v[232:239],v27
ds_read_b64_tr_b16 a[212:213],v2 offset:8448
v_mfma_f32_16x16x32_bf16 v[200:203],a[128:131],v[144:147],v[40:43]
ds_read_b64_tr_b16 a[214:215],v2 offset:12544
v_cvt_pk_bf16_f32 v138,v140,v141
v_mfma_f32_16x16x32_bf16 v[200:203],a[132:135],v[176:179],v[200:203]
ds_read_b64_tr_b16 a[220:221],v3 offset:8448
v_mfma_f32_16x16x32_bf16 v[200:203],a[136:139],v[208:211],v[200:203]
ds_read_b64_tr_b16 a[222:223],v3 offset:12544
v_cvt_pk_bf16_f32 v139,v142,v143
v_mfma_f32_16x16x32_bf16 v[200:203],a[140:143],v[240:243],v[200:203]
buffer_load_dwordx4 v5,s[24:27],s47 offen offset:3072 nt lds
v_mfma_f32_16x16x32_bf16 v[204:207],a[156:159],v[240:243],v[40:43]
s_or_b32 s47,s47,0x18000
s_add_u32 s47,s47,4096
ds_read_b64_tr_b16 a[216:217],v3 offset:256
v_mfma_f32_16x16x32_bf16 v[204:207],a[152:155],v[208:211],v[204:207]
s_and_b32 s47,s47,0x27000
ds_read_b64_tr_b16 a[218:219],v3 offset:4352
v_cvt_pk_bf16_f32 v140,v152,v153
v_mfma_f32_16x16x32_bf16 v[204:207],a[148:151],v[176:179],v[204:207]
s_or_b32 s47,s47,s46
ds_read_b64_tr_b16 a[224:225],v2 offset:512
v_mfma_f32_16x16x32_bf16 v[204:207],a[144:147],v[144:147],v[204:207]
ds_read_b64_tr_b16 a[226:227],v2 offset:4608
v_mfma_f32_16x16x32_bf16 v[216:219],a[160:163],v[144:147],v[40:43]
ds_read_b64_tr_b16 a[228:229],v2 offset:8704
v_cvt_pk_bf16_f32 v141,v154,v155
v_mfma_f32_16x16x32_bf16 v[216:219],a[164:167],v[176:179],v[216:219]
ds_read_b64_tr_b16 a[230:231],v2 offset:12800
v_mfma_f32_16x16x32_bf16 v[216:219],a[168:171],v[208:211],v[216:219]
ds_read_b64_tr_b16 a[236:237],v3 offset:8704
v_mfma_f32_16x16x32_bf16 v[216:219],a[172:175],v[240:243],v[216:219]
ds_read_b64_tr_b16 a[238:239],v3 offset:12800
v_cvt_pk_bf16_f32 v142,v156,v157
v_mfma_f32_16x16x32_bf16 v[220:223],a[188:191],v[240:243],v[40:43]
ds_read_b64_tr_b16 a[232:233],v3 offset:512
v_mfma_f32_16x16x32_bf16 v[220:223],a[184:187],v[208:211],v[220:223]
ds_read_b64_tr_b16 a[234:235],v3 offset:4608
v_mfma_f32_16x16x32_bf16 v[220:223],a[180:183],v[176:179],v[220:223]
ds_read_b64_tr_b16 a[240:241],v2 offset:768
v_cvt_pk_bf16_f32 v143,v158,v159
v_mfma_f32_16x16x32_bf16 v[220:223],a[176:179],v[144:147],v[220:223]
s_waitcnt lgkmcnt(7)
buffer_load_dword v26,v24,s[88:91],s93 offen
v_mfma_f32_16x16x32_bf16 a[0:3],a[192:195],v[136:139],a[0:3]
ds_read_b64_tr_b16 a[242:243],v2 offset:4864
v_mfma_f32_16x16x32_bf16 a[0:3],a[196:199],v[140:143],a[0:3]
ds_read_b64_tr_b16 a[244:245],v2 offset:8960
v_mfma_f32_16x16x32_bf16 a[4:7],a[204:207],v[140:143],a[4:7]
ds_read_b64_tr_b16 a[246:247],v2 offset:13056
v_mfma_f32_16x16x32_bf16 a[4:7],a[200:203],v[136:139],a[4:7]
ds_read_b64_tr_b16 a[252:253],v3 offset:8960
v_mfma_f32_16x16x32_bf16 a[8:11],a[208:211],v[136:139],a[8:11]
ds_read_b64_tr_b16 a[254:255],v3 offset:13056
v_mfma_f32_16x16x32_bf16 a[8:11],a[212:215],v[140:143],a[8:11]
ds_read_b64_tr_b16 a[248:249],v3 offset:768
v_mfma_f32_16x16x32_bf16 a[12:15],a[220:223],v[140:143],a[12:15]
ds_read_b64_tr_b16 a[250:251],v3 offset:4864
v_mfma_f32_16x16x32_bf16 a[12:15],a[216:219],v[136:139],a[12:15]
s_bitcmp1_b32 s45,0
s_cbranch_scc0 L_t31v11_%=
s_mov_b32 m0,s80
s_add_u32 s80,s80,4096
buffer_load_dwordx4 v25,s[24:27],s83 offen nt lds
s_add_u32 s83,s83,256
L_t31v11_%=:
v_mfma_f32_16x16x32_bf16 a[16:19],a[224:227],v[136:139],a[16:19]
s_waitcnt lgkmcnt(6)
v_mfma_f32_16x16x32_bf16 a[16:19],a[228:231],v[140:143],a[16:19]
v_mfma_f32_16x16x32_bf16 a[20:23],a[236:239],v[140:143],a[20:23]
v_mfma_f32_16x16x32_bf16 a[20:23],a[232:235],v[136:139],a[20:23]
v_mfma_f32_16x16x32_bf16 a[24:27],a[240:243],v[136:139],a[24:27]
s_waitcnt lgkmcnt(2)
v_mfma_f32_16x16x32_bf16 a[24:27],a[244:247],v[140:143],a[24:27]
v_mfma_f32_16x16x32_bf16 a[28:31],a[252:255],v[140:143],a[28:31]
s_waitcnt lgkmcnt(0)
v_mfma_f32_16x16x32_bf16 a[28:31],a[248:251],v[136:139],a[28:31]
s_cmp_lg_u32 s53,0
v_smfmac_f32_16x16x64_bf16 v[48:51],v[28:31],v[136:143],v27
s_cbranch_scc1 L_att1v1_%=
s_waitcnt lgkmcnt(0)
s_nop 7
s_branch L_att1x_%=
.p2align 5
L_att1v2_%=:
v_mfma_f32_16x16x32_bf16 v[232:235],a[128:131],v[148:151],v[44:47]
s_waitcnt vmcnt(8) lgkmcnt(0)
v_mfma_f32_16x16x32_bf16 v[232:235],a[132:135],v[180:183],v[232:235]
s_barrier
s_add_u32 m0,s49,s51
ds_read_b128 a[128:131],v1
v_mfma_f32_16x16x32_bf16 v[232:235],a[136:139],v[212:215],v[232:235]
s_mov_b32 s90,L_out_end_%=-L_pc_%=+128
v_mfma_f32_16x16x32_bf16 v[232:235],a[140:143],v[244:247],v[232:235]
s_lshr_b32 s45,s45,1
ds_read_b128 a[132:135],v1 offset:256
v_mfma_f32_16x16x32_bf16 v[236:239],a[156:159],v[244:247],v[44:47]
buffer_load_dwordx4 v4,s[20:23],s47 offen nt lds
v_mfma_f32_16x16x32_bf16 v[236:239],a[152:155],v[212:215],v[236:239]
ds_read_b128 a[136:139],v1 offset:512
v_add_u32 v2,s48,v2
v_mfma_f32_16x16x32_bf16 v[236:239],a[148:151],v[180:183],v[236:239]
v_mfma_f32_16x16x32_bf16 v[236:239],a[144:147],v[148:151],v[236:239]
s_add_u32 s62,s50,32768
ds_read_b128 a[140:143],v1 offset:768
v_add_u32 v3,s48,v3
v_mfma_f32_16x16x32_bf16 v[248:251],a[160:163],v[148:151],v[44:47]
s_and_b32 s62,s62,131071
v_mfma_f32_16x16x32_bf16 v[248:251],a[164:167],v[180:183],v[248:251]
s_add_u32 s54,s54,16384
ds_read_b128 a[156:159],v1 offset:4864
v_mfma_f32_16x16x32_bf16 v[248:251],a[168:171],v[212:215],v[248:251]
s_sub_u32 s48,s62,s50
v_mfma_f32_16x16x32_bf16 v[248:251],a[172:175],v[244:247],v[248:251]
s_sub_u32 s53,s53,1
v_mfma_f32_16x16x32_bf16 v[252:255],a[188:191],v[244:247],v[44:47]
s_and_b32 s46,s54,0xfffc0000
ds_read_b128 a[152:155],v1 offset:4608
v_mfma_f32_16x16x32_bf16 v[252:255],a[184:187],v[212:215],v[252:255]
s_mov_b32 s50,s62
v_mfma_f32_16x16x32_bf16 v[252:255],a[180:183],v[180:183],v[252:255]
v_mfma_f32_16x16x32_bf16 v[252:255],a[176:179],v[148:151],v[252:255]
ds_read_b128 a[148:151],v1 offset:4352
v_mfma_f32_16x16x32_bf16 a[32:35],a[192:195],v[168:171],a[32:35]
v_mfma_f32_16x16x32_bf16 a[32:35],a[196:199],v[172:175],a[32:35]
ds_read_b128 a[144:147],v1 offset:4096
v_mfma_f32_16x16x32_bf16 a[36:39],a[204:207],v[172:175],a[36:39]
buffer_load_dwordx4 v4,s[20:23],s47 offen offset:1024 nt lds
v_mfma_f32_16x16x32_bf16 a[36:39],a[200:203],v[168:171],a[36:39]
ds_read_b128 a[160:163],v1 offset:8192
v_mfma_f32_16x16x32_bf16 a[40:43],a[208:211],v[168:171],a[40:43]
v_mfma_f32_16x16x32_bf16 a[40:43],a[212:215],v[172:175],a[40:43]
ds_read_b128 a[164:167],v1 offset:8448
v_mfma_f32_16x16x32_bf16 a[44:47],a[220:223],v[172:175],a[44:47]
v_mfma_f32_16x16x32_bf16 a[44:47],a[216:219],v[168:171],a[44:47]
ds_read_b128 a[168:171],v1 offset:8704
v_mfma_f32_16x16x32_bf16 a[48:51],a[224:227],v[168:171],a[48:51]
v_mfma_f32_16x16x32_bf16 a[48:51],a[228:231],v[172:175],a[48:51]
ds_read_b128 a[172:175],v1 offset:8960
v_mfma_f32_16x16x32_bf16 a[52:55],a[236:239],v[172:175],a[52:55]
v_mfma_f32_16x16x32_bf16 a[52:55],a[232:235],v[168:171],a[52:55]
ds_read_b128 a[188:191],v1 offset:13056
v_mfma_f32_16x16x32_bf16 a[56:59],a[240:243],v[168:171],a[56:59]
v_mfma_f32_16x16x32_bf16 a[56:59],a[244:247],v[172:175],a[56:59]
ds_read_b128 a[184:187],v1 offset:12800
v_mfma_f32_16x16x32_bf16 a[60:63],a[252:255],v[172:175],a[60:63]
v_mfma_f32_16x16x32_bf16 a[60:63],a[248:251],v[168:171],a[60:63]
ds_read_b128 a[180:183],v1 offset:12544
v_smfmac_f32_16x16x64_bf16 v[52:55],v[28:31],v[168:175],v27
s_waitcnt lgkmcnt(4)
v_mfma_f32_16x16x32_bf16 v[136:139],a[128:131],v[128:131],v[32:35]
buffer_load_dwordx4 v5,s[20:23],s47 offen offset:2048 nt lds
v_mfma_f32_16x16x32_bf16 v[136:139],a[132:135],v[160:163],v[136:139]
ds_read_b128 a[176:179],v1 offset:12288
v_add_u32 v1,s48,v1
v_mfma_f32_16x16x32_bf16 v[136:139],a[136:139],v[192:195],v[136:139]
v_mfma_f32_16x16x32_bf16 v[136:139],a[140:143],v[224:227],v[136:139]
v_mfma_f32_16x16x32_bf16 v[140:143],a[156:159],v[224:227],v[32:35]
v_exp_f32 v232,v232
v_mfma_f32_16x16x32_bf16 v[140:143],a[152:155],v[192:195],v[140:143]
v_exp_f32 v233,v233
v_mfma_f32_16x16x32_bf16 v[140:143],a[148:151],v[160:163],v[140:143]
v_exp_f32 v234,v234
v_mfma_f32_16x16x32_bf16 v[140:143],a[144:147],v[128:131],v[140:143]
v_exp_f32 v235,v235
v_mfma_f32_16x16x32_bf16 v[152:155],a[160:163],v[128:131],v[32:35]
v_exp_f32 v236,v236
v_mfma_f32_16x16x32_bf16 v[152:155],a[164:167],v[160:163],v[152:155]
v_exp_f32 v237,v237
v_mfma_f32_16x16x32_bf16 v[152:155],a[168:171],v[192:195],v[152:155]
s_waitcnt lgkmcnt(0)
v_exp_f32 v238,v238
v_mfma_f32_16x16x32_bf16 v[152:155],a[172:175],v[224:227],v[152:155]
v_exp_f32 v239,v239
v_mfma_f32_16x16x32_bf16 v[156:159],a[188:191],v[224:227],v[32:35]
v_exp_f32 v248,v248
v_mfma_f32_16x16x32_bf16 v[156:159],a[184:187],v[192:195],v[156:159]
buffer_load_dwordx4 v5,s[20:23],s47 offen offset:3072 nt lds
v_mfma_f32_16x16x32_bf16 v[156:159],a[180:183],v[160:163],v[156:159]
v_exp_f32 v249,v249
v_mfma_f32_16x16x32_bf16 v[156:159],a[176:179],v[128:131],v[156:159]
v_exp_f32 v250,v250
v_exp_f32 v251,v251
v_exp_f32 v252,v252
v_exp_f32 v253,v253
v_exp_f32 v254,v254
v_exp_f32 v255,v255
v_exp_f32 v136,v136
v_exp_f32 v137,v137
v_exp_f32 v138,v138
s_add_u32 m0,s49,s52
v_cvt_pk_bf16_f32 v232,v232,v233
v_cvt_pk_bf16_f32 v233,v234,v235
s_xor_b32 s49,s50,65536
v_cvt_pk_bf16_f32 v234,v236,v237
v_cvt_pk_bf16_f32 v235,v238,v239
v_cvt_pk_bf16_f32 v236,v248,v249
v_cvt_pk_bf16_f32 v237,v250,v251
buffer_load_dwordx4 v4,s[24:27],s47 offen nt lds
v_cvt_pk_bf16_f32 v238,v252,v253
v_cvt_pk_bf16_f32 v239,v254,v255
v_exp_f32 v139,v139
v_exp_f32 v140,v140
v_exp_f32 v141,v141
v_mfma_f32_16x16x32_bf16 v[168:171],a[128:131],v[132:135],v[36:39]
v_exp_f32 v142,v142
v_mfma_f32_16x16x32_bf16 v[168:171],a[132:135],v[164:167],v[168:171]
v_exp_f32 v143,v143
v_mfma_f32_16x16x32_bf16 v[168:171],a[136:139],v[196:199],v[168:171]
v_exp_f32 v152,v152
v_mfma_f32_16x16x32_bf16 v[168:171],a[140:143],v[228:231],v[168:171]
v_exp_f32 v153,v153
v_mfma_f32_16x16x32_bf16 v[172:175],a[156:159],v[228:231],v[36:39]
v_exp_f32 v154,v154
v_mfma_f32_16x16x32_bf16 v[172:175],a[152:155],v[196:199],v[172:175]
v_exp_f32 v155,v155
v_mfma_f32_16x16x32_bf16 v[172:175],a[148:151],v[164:167],v[172:175]
v_exp_f32 v156,v156
v_mfma_f32_16x16x32_bf16 v[172:175],a[144:147],v[132:135],v[172:175]
v_exp_f32 v157,v157
v_mfma_f32_16x16x32_bf16 v[184:187],a[160:163],v[132:135],v[36:39]
buffer_load_dwordx4 v5,s[24:27],s47 offen offset:1024 nt lds
v_mfma_f32_16x16x32_bf16 v[184:187],a[164:167],v[164:167],v[184:187]
)A"
R"A(v_exp_f32 v158,v158
v_mfma_f32_16x16x32_bf16 v[184:187],a[168:171],v[196:199],v[184:187]
v_exp_f32 v159,v159
v_mfma_f32_16x16x32_bf16 v[184:187],a[172:175],v[228:231],v[184:187]
v_exp_f32 v168,v168
v_mfma_f32_16x16x32_bf16 v[188:191],a[188:191],v[228:231],v[36:39]
v_exp_f32 v169,v169
v_mfma_f32_16x16x32_bf16 v[188:191],a[184:187],v[196:199],v[188:191]
v_exp_f32 v170,v170
v_mfma_f32_16x16x32_bf16 v[188:191],a[180:183],v[164:167],v[188:191]
v_exp_f32 v171,v171
v_mfma_f32_16x16x32_bf16 v[188:191],a[176:179],v[132:135],v[188:191]
v_exp_f32 v172,v172
v_mfma_f32_16x16x32_bf16 a[96:99],a[192:195],v[232:235],a[96:99]
ds_read_b64_tr_b16 a[192:193],v2
v_cvt_pk_bf16_f32 v168,v168,v169
v_mfma_f32_16x16x32_bf16 a[96:99],a[196:199],v[236:239],a[96:99]
ds_read_b64_tr_b16 a[194:195],v2 offset:4096
v_cvt_pk_bf16_f32 v169,v170,v171
v_mfma_f32_16x16x32_bf16 a[100:103],a[204:207],v[236:239],a[100:103]
v_exp_f32 v173,v173
v_mfma_f32_16x16x32_bf16 a[100:103],a[200:203],v[232:235],a[100:103]
ds_read_b64_tr_b16 a[196:197],v2 offset:8192
v_cvt_pk_bf16_f32 v170,v172,v173
v_mfma_f32_16x16x32_bf16 a[104:107],a[208:211],v[232:235],a[104:107]
v_exp_f32 v174,v174
v_mfma_f32_16x16x32_bf16 a[104:107],a[212:215],v[236:239],a[104:107]
v_exp_f32 v175,v175
v_mfma_f32_16x16x32_bf16 a[108:111],a[220:223],v[236:239],a[108:111]
buffer_load_dwordx4 v4,s[24:27],s47 offen offset:2048 nt lds
v_mfma_f32_16x16x32_bf16 a[108:111],a[216:219],v[232:235],a[108:111]
ds_read_b64_tr_b16 a[198:199],v2 offset:12288
v_cvt_pk_bf16_f32 v171,v174,v175
v_mfma_f32_16x16x32_bf16 a[112:115],a[224:227],v[232:235],a[112:115]
v_exp_f32 v184,v184
v_mfma_f32_16x16x32_bf16 a[112:115],a[228:231],v[236:239],a[112:115]
v_exp_f32 v185,v185
v_mfma_f32_16x16x32_bf16 a[116:119],a[236:239],v[236:239],a[116:119]
ds_read_b64_tr_b16 a[204:205],v3 offset:8192
v_cvt_pk_bf16_f32 v136,v136,v137
v_mfma_f32_16x16x32_bf16 a[116:119],a[232:235],v[232:235],a[116:119]
ds_read_b64_tr_b16 a[206:207],v3 offset:12288
v_cvt_pk_bf16_f32 v172,v184,v185
v_mfma_f32_16x16x32_bf16 a[120:123],a[240:243],v[232:235],a[120:123]
ds_read_b64_tr_b16 a[200:201],v3
v_mfma_f32_16x16x32_bf16 a[120:123],a[244:247],v[236:239],a[120:123]
ds_read_b64_tr_b16 a[202:203],v3 offset:4096
v_cvt_pk_bf16_f32 v137,v138,v139
v_mfma_f32_16x16x32_bf16 a[124:127],a[252:255],v[236:239],a[124:127]
ds_read_b64_tr_b16 a[208:209],v2 offset:256
v_mfma_f32_16x16x32_bf16 a[124:127],a[248:251],v[232:235],a[124:127]
ds_read_b64_tr_b16 a[210:211],v2 offset:4352
v_smfmac_f32_16x16x64_bf16 v[60:63],v[28:31],v[232:239],v27
ds_read_b64_tr_b16 a[212:213],v2 offset:8448
ds_read_b64_tr_b16 a[214:215],v2 offset:12544
v_cvt_pk_bf16_f32 v138,v140,v141
ds_read_b64_tr_b16 a[220:221],v3 offset:8448
ds_read_b64_tr_b16 a[222:223],v3 offset:12544
v_cvt_pk_bf16_f32 v139,v142,v143
buffer_load_dwordx4 v5,s[24:27],s47 offen offset:3072 nt lds
s_or_b32 s47,s47,0x18000
s_add_u32 s47,s47,4096
ds_read_b64_tr_b16 a[216:217],v3 offset:256
s_and_b32 s47,s47,0x27000
ds_read_b64_tr_b16 a[218:219],v3 offset:4352
v_cvt_pk_bf16_f32 v140,v152,v153
s_or_b32 s47,s47,s46
ds_read_b64_tr_b16 a[224:225],v2 offset:512
ds_read_b64_tr_b16 a[226:227],v2 offset:4608
ds_read_b64_tr_b16 a[228:229],v2 offset:8704
v_cvt_pk_bf16_f32 v141,v154,v155
ds_read_b64_tr_b16 a[230:231],v2 offset:12800
ds_read_b64_tr_b16 a[236:237],v3 offset:8704
ds_read_b64_tr_b16 a[238:239],v3 offset:12800
v_cvt_pk_bf16_f32 v142,v156,v157
ds_read_b64_tr_b16 a[232:233],v3 offset:512
ds_read_b64_tr_b16 a[234:235],v3 offset:4608
ds_read_b64_tr_b16 a[240:241],v2 offset:768
v_cvt_pk_bf16_f32 v143,v158,v159
buffer_load_dword v26,v24,s[88:91],s93 offen
s_waitcnt lgkmcnt(15)
v_mfma_f32_16x16x32_bf16 a[0:3],a[192:195],v[136:139],a[0:3]
ds_read_b64_tr_b16 a[242:243],v2 offset:4864
v_mfma_f32_16x16x32_bf16 a[0:3],a[196:199],v[140:143],a[0:3]
ds_read_b64_tr_b16 a[244:245],v2 offset:8960
v_mfma_f32_16x16x32_bf16 a[4:7],a[204:207],v[140:143],a[4:7]
ds_read_b64_tr_b16 a[246:247],v2 offset:13056
v_mfma_f32_16x16x32_bf16 a[4:7],a[200:203],v[136:139],a[4:7]
ds_read_b64_tr_b16 a[252:253],v3 offset:8960
v_mfma_f32_16x16x32_bf16 a[8:11],a[208:211],v[136:139],a[8:11]
s_waitcnt lgkmcnt(15)
ds_read_b64_tr_b16 a[254:255],v3 offset:13056
v_mfma_f32_16x16x32_bf16 a[8:11],a[212:215],v[140:143],a[8:11]
ds_read_b64_tr_b16 a[248:249],v3 offset:768
v_mfma_f32_16x16x32_bf16 a[12:15],a[220:223],v[140:143],a[12:15]
s_waitcnt lgkmcnt(15)
ds_read_b64_tr_b16 a[250:251],v3 offset:4864
v_mfma_f32_16x16x32_bf16 a[12:15],a[216:219],v[136:139],a[12:15]
s_bitcmp1_b32 s45,0
s_cbranch_scc0 L_t31v21_%=
s_mov_b32 m0,s80
s_add_u32 s80,s80,4096
buffer_load_dwordx4 v25,s[24:27],s83 offen nt lds
s_add_u32 s83,s83,256
L_t31v21_%=:
s_waitcnt lgkmcnt(7)
v_mfma_f32_16x16x32_bf16 a[16:19],a[224:227],v[136:139],a[16:19]
v_exp_f32 v186,v186
v_mfma_f32_16x16x32_bf16 a[16:19],a[228:231],v[140:143],a[16:19]
v_exp_f32 v187,v187
v_mfma_f32_16x16x32_bf16 a[20:23],a[236:239],v[140:143],a[20:23]
v_exp_f32 v188,v188
v_mfma_f32_16x16x32_bf16 a[20:23],a[232:235],v[136:139],a[20:23]
s_waitcnt lgkmcnt(3)
v_exp_f32 v189,v189
v_mfma_f32_16x16x32_bf16 a[24:27],a[240:243],v[136:139],a[24:27]
v_exp_f32 v190,v190
v_mfma_f32_16x16x32_bf16 a[24:27],a[244:247],v[140:143],a[24:27]
s_waitcnt lgkmcnt(1)
v_cvt_pk_bf16_f32 v173,v186,v187
v_mfma_f32_16x16x32_bf16 a[28:31],a[252:255],v[140:143],a[28:31]
s_waitcnt lgkmcnt(0)
v_exp_f32 v191,v191
v_mfma_f32_16x16x32_bf16 a[28:31],a[248:251],v[136:139],a[28:31]
v_cvt_pk_bf16_f32 v174,v188,v189
v_cvt_pk_bf16_f32 v175,v190,v191
s_cmp_lg_u32 s53,0
v_smfmac_f32_16x16x64_bf16 v[48:51],v[28:31],v[136:143],v27
s_cbranch_scc1 L_att1v2_%=
s_waitcnt lgkmcnt(0)
s_nop 7
s_branch L_att1x_%=
.p2align 5
L_att1_%=:
v_mfma_f32_16x16x32_bf16 v[232:235],a[128:131],v[148:151],v[44:47]
s_waitcnt vmcnt(8) lgkmcnt(0)
v_exp_f32 v200,v200
v_mfma_f32_16x16x32_bf16 v[232:235],a[132:135],v[180:183],v[232:235]
s_barrier
s_add_u32 m0,s49,s51
ds_read_b128 a[128:131],v1
v_mfma_f32_16x16x32_bf16 v[232:235],a[136:139],v[212:215],v[232:235]
s_mov_b32 s90,L_out_end_%=-L_pc_%=+128
v_exp_f32 v201,v201
v_mfma_f32_16x16x32_bf16 v[232:235],a[140:143],v[244:247],v[232:235]
s_lshr_b32 s45,s45,1
ds_read_b128 a[132:135],v1 offset:256
v_cvt_pk_bf16_f32 v200,v200,v201
v_mfma_f32_16x16x32_bf16 v[236:239],a[156:159],v[244:247],v[44:47]
s_sub_u32 s94,s94,3
buffer_load_dwordx4 v4,s[20:23],s47 offen nt lds
v_mfma_f32_16x16x32_bf16 v[236:239],a[152:155],v[212:215],v[236:239]
s_lshr_b32 s60,s94,2
ds_read_b128 a[136:139],v1 offset:512
v_add_u32 v2,s48,v2
v_mfma_f32_16x16x32_bf16 v[236:239],a[148:151],v[180:183],v[236:239]
s_max_i32 s60,s60,128
v_exp_f32 v202,v202
v_mfma_f32_16x16x32_bf16 v[236:239],a[144:147],v[148:151],v[236:239]
s_add_u32 s62,s50,32768
ds_read_b128 a[140:143],v1 offset:768
v_add_u32 v3,s48,v3
v_mfma_f32_16x16x32_bf16 v[248:251],a[160:163],v[148:151],v[44:47]
s_and_b32 s62,s62,131071
v_exp_f32 v203,v203
v_mfma_f32_16x16x32_bf16 v[248:251],a[164:167],v[180:183],v[248:251]
s_add_u32 s54,s54,16384
ds_read_b128 a[156:159],v1 offset:4864
v_cvt_pk_bf16_f32 v201,v202,v203
v_mfma_f32_16x16x32_bf16 v[248:251],a[168:171],v[212:215],v[248:251]
s_sub_u32 s48,s62,s50
v_exp_f32 v204,v204
v_mfma_f32_16x16x32_bf16 v[248:251],a[172:175],v[244:247],v[248:251]
s_sub_u32 s53,s53,1
v_exp_f32 v205,v205
v_mfma_f32_16x16x32_bf16 v[252:255],a[188:191],v[244:247],v[44:47]
s_and_b32 s46,s54,0xfffc0000
ds_read_b128 a[152:155],v1 offset:4608
v_cvt_pk_bf16_f32 v202,v204,v205
v_mfma_f32_16x16x32_bf16 v[252:255],a[184:187],v[212:215],v[252:255]
s_mov_b32 s50,s62
v_exp_f32 v206,v206
v_mfma_f32_16x16x32_bf16 v[252:255],a[180:183],v[180:183],v[252:255]
v_exp_f32 v207,v207
v_mfma_f32_16x16x32_bf16 v[252:255],a[176:179],v[148:151],v[252:255]
ds_read_b128 a[148:151],v1 offset:4352
v_cvt_pk_bf16_f32 v203,v206,v207
v_mfma_f32_16x16x32_bf16 a[32:35],a[192:195],v[168:171],a[32:35]
v_exp_f32 v216,v216
v_mfma_f32_16x16x32_bf16 a[32:35],a[196:199],v[172:175],a[32:35]
ds_read_b128 a[144:147],v1 offset:4096
v_mfma_f32_16x16x32_bf16 a[36:39],a[204:207],v[172:175],a[36:39]
buffer_load_dwordx4 v4,s[20:23],s47 offen offset:1024 nt lds
v_mfma_f32_16x16x32_bf16 a[36:39],a[200:203],v[168:171],a[36:39]
ds_read_b128 a[160:163],v1 offset:8192
v_mfma_f32_16x16x32_bf16 a[40:43],a[208:211],v[168:171],a[40:43]
v_exp_f32 v217,v217
v_mfma_f32_16x16x32_bf16 a[40:43],a[212:215],v[172:175],a[40:43]
ds_read_b128 a[164:167],v1 offset:8448
v_cvt_pk_bf16_f32 v204,v216,v217
v_mfma_f32_16x16x32_bf16 a[44:47],a[220:223],v[172:175],a[44:47]
v_exp_f32 v218,v218
v_mfma_f32_16x16x32_bf16 a[44:47],a[216:219],v[168:171],a[44:47]
ds_read_b128 a[168:171],v1 offset:8704
v_mfma_f32_16x16x32_bf16 a[48:51],a[224:227],v[168:171],a[48:51]
v_exp_f32 v219,v219
v_mfma_f32_16x16x32_bf16 a[48:51],a[228:231],v[172:175],a[48:51]
ds_read_b128 a[172:175],v1 offset:8960
v_cvt_pk_bf16_f32 v205,v218,v219
v_mfma_f32_16x16x32_bf16 a[52:55],a[236:239],v[172:175],a[52:55]
v_exp_f32 v220,v220
v_mfma_f32_16x16x32_bf16 a[52:55],a[232:235],v[168:171],a[52:55]
ds_read_b128 a[188:191],v1 offset:13056
v_mfma_f32_16x16x32_bf16 a[56:59],a[240:243],v[168:171],a[56:59]
v_exp_f32 v221,v221
v_mfma_f32_16x16x32_bf16 a[56:59],a[244:247],v[172:175],a[56:59]
ds_read_b128 a[184:187],v1 offset:12800
v_cvt_pk_bf16_f32 v206,v220,v221
v_mfma_f32_16x16x32_bf16 a[60:63],a[252:255],v[172:175],a[60:63]
v_exp_f32 v222,v222
v_mfma_f32_16x16x32_bf16 a[60:63],a[248:251],v[168:171],a[60:63]
ds_read_b128 a[180:183],v1 offset:12544
v_smfmac_f32_16x16x64_bf16 v[52:55],v[28:31],v[168:175],v27
s_waitcnt lgkmcnt(4)
v_mfma_f32_16x16x32_bf16 v[136:139],a[128:131],v[128:131],v[32:35]
buffer_load_dwordx4 v5,s[20:23],s47 offen offset:2048 nt lds
v_mfma_f32_16x16x32_bf16 v[136:139],a[132:135],v[160:163],v[136:139]
ds_read_b128 a[176:179],v1 offset:12288
v_add_u32 v1,s48,v1
v_mfma_f32_16x16x32_bf16 v[136:139],a[136:139],v[192:195],v[136:139]
v_exp_f32 v223,v223
v_mfma_f32_16x16x32_bf16 v[136:139],a[140:143],v[224:227],v[136:139]
v_cvt_pk_bf16_f32 v207,v222,v223
v_mfma_f32_16x16x32_bf16 v[140:143],a[156:159],v[224:227],v[32:35]
v_exp_f32 v232,v232
v_mfma_f32_16x16x32_bf16 v[140:143],a[152:155],v[192:195],v[140:143]
v_exp_f32 v233,v233
v_mfma_f32_16x16x32_bf16 v[140:143],a[148:151],v[160:163],v[140:143]
v_exp_f32 v234,v234
v_mfma_f32_16x16x32_bf16 v[140:143],a[144:147],v[128:131],v[140:143]
v_exp_f32 v235,v235
v_mfma_f32_16x16x32_bf16 v[152:155],a[160:163],v[128:131],v[32:35]
v_exp_f32 v236,v236
v_mfma_f32_16x16x32_bf16 v[152:155],a[164:167],v[160:163],v[152:155]
v_exp_f32 v237,v237
v_mfma_f32_16x16x32_bf16 v[152:155],a[168:171],v[192:195],v[152:155]
s_waitcnt lgkmcnt(0)
v_exp_f32 v238,v238
v_mfma_f32_16x16x32_bf16 v[152:155],a[172:175],v[224:227],v[152:155]
v_exp_f32 v239,v239
v_mfma_f32_16x16x32_bf16 v[156:159],a[188:191],v[224:227],v[32:35]
v_exp_f32 v248,v248
v_mfma_f32_16x16x32_bf16 v[156:159],a[184:187],v[192:195],v[156:159]
buffer_load_dwordx4 v5,s[20:23],s47 offen offset:3072 nt lds
v_mfma_f32_16x16x32_bf16 v[156:159],a[180:183],v[160:163],v[156:159]
v_exp_f32 v249,v249
v_mfma_f32_16x16x32_bf16 v[156:159],a[176:179],v[128:131],v[156:159]
v_exp_f32 v250,v250
v_mfma_f32_16x16x32_bf16 a[64:67],a[192:195],v[200:203],a[64:67]
v_exp_f32 v251,v251
v_mfma_f32_16x16x32_bf16 a[64:67],a[196:199],v[204:207],a[64:67]
v_exp_f32 v252,v252
v_mfma_f32_16x16x32_bf16 a[68:71],a[204:207],v[204:207],a[68:71]
v_exp_f32 v253,v253
v_mfma_f32_16x16x32_bf16 a[68:71],a[200:203],v[200:203],a[68:71]
v_exp_f32 v254,v254
v_mfma_f32_16x16x32_bf16 a[72:75],a[208:211],v[200:203],a[72:75]
v_exp_f32 v255,v255
v_mfma_f32_16x16x32_bf16 a[72:75],a[212:215],v[204:207],a[72:75]
v_exp_f32 v136,v136
v_mfma_f32_16x16x32_bf16 a[76:79],a[220:223],v[204:207],a[76:79]
v_exp_f32 v137,v137
v_mfma_f32_16x16x32_bf16 a[76:79],a[216:219],v[200:203],a[76:79]
v_exp_f32 v138,v138
v_mfma_f32_16x16x32_bf16 a[80:83],a[224:227],v[200:203],a[80:83]
s_add_u32 m0,s49,s52
v_cvt_pk_bf16_f32 v232,v232,v233
v_cvt_pk_bf16_f32 v233,v234,v235
v_mfma_f32_16x16x32_bf16 a[80:83],a[228:231],v[204:207],a[80:83]
s_xor_b32 s49,s50,65536
v_cvt_pk_bf16_f32 v234,v236,v237
v_cvt_pk_bf16_f32 v235,v238,v239
)A"
R"A(v_mfma_f32_16x16x32_bf16 a[84:87],a[236:239],v[204:207],a[84:87]
v_cvt_pk_bf16_f32 v236,v248,v249
v_cvt_pk_bf16_f32 v237,v250,v251
v_mfma_f32_16x16x32_bf16 a[84:87],a[232:235],v[200:203],a[84:87]
buffer_load_dwordx4 v4,s[24:27],s47 offen nt lds
v_mfma_f32_16x16x32_bf16 a[88:91],a[240:243],v[200:203],a[88:91]
v_cvt_pk_bf16_f32 v238,v252,v253
v_cvt_pk_bf16_f32 v239,v254,v255
v_mfma_f32_16x16x32_bf16 a[88:91],a[244:247],v[204:207],a[88:91]
v_exp_f32 v139,v139
v_mfma_f32_16x16x32_bf16 a[92:95],a[252:255],v[204:207],a[92:95]
v_exp_f32 v140,v140
v_mfma_f32_16x16x32_bf16 a[92:95],a[248:251],v[200:203],a[92:95]
v_exp_f32 v141,v141
v_smfmac_f32_16x16x64_bf16 v[56:59],v[28:31],v[200:207],v27
v_mfma_f32_16x16x32_bf16 v[168:171],a[128:131],v[132:135],v[36:39]
v_exp_f32 v142,v142
v_mfma_f32_16x16x32_bf16 v[168:171],a[132:135],v[164:167],v[168:171]
v_exp_f32 v143,v143
v_mfma_f32_16x16x32_bf16 v[168:171],a[136:139],v[196:199],v[168:171]
v_exp_f32 v152,v152
v_mfma_f32_16x16x32_bf16 v[168:171],a[140:143],v[228:231],v[168:171]
v_exp_f32 v153,v153
v_mfma_f32_16x16x32_bf16 v[172:175],a[156:159],v[228:231],v[36:39]
v_exp_f32 v154,v154
v_mfma_f32_16x16x32_bf16 v[172:175],a[152:155],v[196:199],v[172:175]
v_exp_f32 v155,v155
v_mfma_f32_16x16x32_bf16 v[172:175],a[148:151],v[164:167],v[172:175]
v_exp_f32 v156,v156
v_mfma_f32_16x16x32_bf16 v[172:175],a[144:147],v[132:135],v[172:175]
v_exp_f32 v157,v157
v_mfma_f32_16x16x32_bf16 v[184:187],a[160:163],v[132:135],v[36:39]
buffer_load_dwordx4 v5,s[24:27],s47 offen offset:1024 nt lds
v_mfma_f32_16x16x32_bf16 v[184:187],a[164:167],v[164:167],v[184:187]
v_exp_f32 v158,v158
v_mfma_f32_16x16x32_bf16 v[184:187],a[168:171],v[196:199],v[184:187]
v_exp_f32 v159,v159
v_mfma_f32_16x16x32_bf16 v[184:187],a[172:175],v[228:231],v[184:187]
v_exp_f32 v168,v168
v_mfma_f32_16x16x32_bf16 v[188:191],a[188:191],v[228:231],v[36:39]
v_exp_f32 v169,v169
v_mfma_f32_16x16x32_bf16 v[188:191],a[184:187],v[196:199],v[188:191]
v_exp_f32 v170,v170
v_mfma_f32_16x16x32_bf16 v[188:191],a[180:183],v[164:167],v[188:191]
v_exp_f32 v171,v171
v_mfma_f32_16x16x32_bf16 v[188:191],a[176:179],v[132:135],v[188:191]
v_exp_f32 v172,v172
v_mfma_f32_16x16x32_bf16 a[96:99],a[192:195],v[232:235],a[96:99]
ds_read_b64_tr_b16 a[192:193],v2
v_cvt_pk_bf16_f32 v168,v168,v169
v_mfma_f32_16x16x32_bf16 a[96:99],a[196:199],v[236:239],a[96:99]
ds_read_b64_tr_b16 a[194:195],v2 offset:4096
v_cvt_pk_bf16_f32 v169,v170,v171
v_mfma_f32_16x16x32_bf16 a[100:103],a[204:207],v[236:239],a[100:103]
v_exp_f32 v173,v173
v_mfma_f32_16x16x32_bf16 a[100:103],a[200:203],v[232:235],a[100:103]
ds_read_b64_tr_b16 a[196:197],v2 offset:8192
v_cvt_pk_bf16_f32 v170,v172,v173
v_mfma_f32_16x16x32_bf16 a[104:107],a[208:211],v[232:235],a[104:107]
v_exp_f32 v174,v174
v_mfma_f32_16x16x32_bf16 a[104:107],a[212:215],v[236:239],a[104:107]
v_exp_f32 v175,v175
v_mfma_f32_16x16x32_bf16 a[108:111],a[220:223],v[236:239],a[108:111]
buffer_load_dwordx4 v4,s[24:27],s47 offen offset:2048 nt lds
v_mfma_f32_16x16x32_bf16 a[108:111],a[216:219],v[232:235],a[108:111]
ds_read_b64_tr_b16 a[198:199],v2 offset:12288
v_cvt_pk_bf16_f32 v171,v174,v175
v_mfma_f32_16x16x32_bf16 a[112:115],a[224:227],v[232:235],a[112:115]
v_exp_f32 v184,v184
v_mfma_f32_16x16x32_bf16 a[112:115],a[228:231],v[236:239],a[112:115]
v_exp_f32 v185,v185
v_mfma_f32_16x16x32_bf16 a[116:119],a[236:239],v[236:239],a[116:119]
ds_read_b64_tr_b16 a[204:205],v3 offset:8192
v_cvt_pk_bf16_f32 v136,v136,v137
v_mfma_f32_16x16x32_bf16 a[116:119],a[232:235],v[232:235],a[116:119]
ds_read_b64_tr_b16 a[206:207],v3 offset:12288
v_cvt_pk_bf16_f32 v172,v184,v185
v_mfma_f32_16x16x32_bf16 a[120:123],a[240:243],v[232:235],a[120:123]
ds_read_b64_tr_b16 a[200:201],v3
v_mfma_f32_16x16x32_bf16 a[120:123],a[244:247],v[236:239],a[120:123]
ds_read_b64_tr_b16 a[202:203],v3 offset:4096
v_cvt_pk_bf16_f32 v137,v138,v139
v_mfma_f32_16x16x32_bf16 a[124:127],a[252:255],v[236:239],a[124:127]
ds_read_b64_tr_b16 a[208:209],v2 offset:256
v_mfma_f32_16x16x32_bf16 a[124:127],a[248:251],v[232:235],a[124:127]
ds_read_b64_tr_b16 a[210:211],v2 offset:4352
v_smfmac_f32_16x16x64_bf16 v[60:63],v[28:31],v[232:239],v27
ds_read_b64_tr_b16 a[212:213],v2 offset:8448
v_mfma_f32_16x16x32_bf16 v[200:203],a[128:131],v[144:147],v[40:43]
ds_read_b64_tr_b16 a[214:215],v2 offset:12544
v_cvt_pk_bf16_f32 v138,v140,v141
v_mfma_f32_16x16x32_bf16 v[200:203],a[132:135],v[176:179],v[200:203]
ds_read_b64_tr_b16 a[220:221],v3 offset:8448
v_mfma_f32_16x16x32_bf16 v[200:203],a[136:139],v[208:211],v[200:203]
ds_read_b64_tr_b16 a[222:223],v3 offset:12544
v_cvt_pk_bf16_f32 v139,v142,v143
v_mfma_f32_16x16x32_bf16 v[200:203],a[140:143],v[240:243],v[200:203]
buffer_load_dwordx4 v5,s[24:27],s47 offen offset:3072 nt lds
v_mfma_f32_16x16x32_bf16 v[204:207],a[156:159],v[240:243],v[40:43]
s_or_b32 s47,s47,0x18000
s_add_u32 s47,s47,4096
ds_read_b64_tr_b16 a[216:217],v3 offset:256
v_mfma_f32_16x16x32_bf16 v[204:207],a[152:155],v[208:211],v[204:207]
s_and_b32 s47,s47,0x27000
ds_read_b64_tr_b16 a[218:219],v3 offset:4352
v_cvt_pk_bf16_f32 v140,v152,v153
v_mfma_f32_16x16x32_bf16 v[204:207],a[148:151],v[176:179],v[204:207]
s_or_b32 s47,s47,s46
ds_read_b64_tr_b16 a[224:225],v2 offset:512
v_mfma_f32_16x16x32_bf16 v[204:207],a[144:147],v[144:147],v[204:207]
ds_read_b64_tr_b16 a[226:227],v2 offset:4608
v_mfma_f32_16x16x32_bf16 v[216:219],a[160:163],v[144:147],v[40:43]
ds_read_b64_tr_b16 a[228:229],v2 offset:8704
v_cvt_pk_bf16_f32 v141,v154,v155
v_mfma_f32_16x16x32_bf16 v[216:219],a[164:167],v[176:179],v[216:219]
ds_read_b64_tr_b16 a[230:231],v2 offset:12800
v_mfma_f32_16x16x32_bf16 v[216:219],a[168:171],v[208:211],v[216:219]
ds_read_b64_tr_b16 a[236:237],v3 offset:8704
v_mfma_f32_16x16x32_bf16 v[216:219],a[172:175],v[240:243],v[216:219]
ds_read_b64_tr_b16 a[238:239],v3 offset:12800
v_cvt_pk_bf16_f32 v142,v156,v157
v_mfma_f32_16x16x32_bf16 v[220:223],a[188:191],v[240:243],v[40:43]
ds_read_b64_tr_b16 a[232:233],v3 offset:512
v_mfma_f32_16x16x32_bf16 v[220:223],a[184:187],v[208:211],v[220:223]
ds_read_b64_tr_b16 a[234:235],v3 offset:4608
v_mfma_f32_16x16x32_bf16 v[220:223],a[180:183],v[176:179],v[220:223]
ds_read_b64_tr_b16 a[240:241],v2 offset:768
v_cvt_pk_bf16_f32 v143,v158,v159
v_mfma_f32_16x16x32_bf16 v[220:223],a[176:179],v[144:147],v[220:223]
s_waitcnt lgkmcnt(7)
buffer_load_dword v26,v24,s[88:91],s93 offen
v_mfma_f32_16x16x32_bf16 a[0:3],a[192:195],v[136:139],a[0:3]
ds_read_b64_tr_b16 a[242:243],v2 offset:4864
v_mfma_f32_16x16x32_bf16 a[0:3],a[196:199],v[140:143],a[0:3]
ds_read_b64_tr_b16 a[244:245],v2 offset:8960
v_mfma_f32_16x16x32_bf16 a[4:7],a[204:207],v[140:143],a[4:7]
ds_read_b64_tr_b16 a[246:247],v2 offset:13056
v_mfma_f32_16x16x32_bf16 a[4:7],a[200:203],v[136:139],a[4:7]
s_memrealtime s[76:77]
ds_read_b64_tr_b16 a[252:253],v3 offset:8960
v_mfma_f32_16x16x32_bf16 a[8:11],a[208:211],v[136:139],a[8:11]
ds_read_b64_tr_b16 a[254:255],v3 offset:13056
v_mfma_f32_16x16x32_bf16 a[8:11],a[212:215],v[140:143],a[8:11]
ds_read_b64_tr_b16 a[248:249],v3 offset:768
v_mfma_f32_16x16x32_bf16 a[12:15],a[220:223],v[140:143],a[12:15]
ds_read_b64_tr_b16 a[250:251],v3 offset:4864
v_mfma_f32_16x16x32_bf16 a[12:15],a[216:219],v[136:139],a[12:15]
s_bitcmp1_b32 s45,0
s_cbranch_scc0 L_t311_%=
s_mov_b32 m0,s80
s_add_u32 s80,s80,4096
buffer_load_dwordx4 v25,s[24:27],s83 offen nt lds
s_add_u32 s83,s83,256
L_t311_%=:
v_mfma_f32_16x16x32_bf16 a[16:19],a[224:227],v[136:139],a[16:19]
s_waitcnt lgkmcnt(6)
v_exp_f32 v186,v186
v_mfma_f32_16x16x32_bf16 a[16:19],a[228:231],v[140:143],a[16:19]
v_exp_f32 v187,v187
v_mfma_f32_16x16x32_bf16 a[20:23],a[236:239],v[140:143],a[20:23]
s_waitcnt lgkmcnt(0)
v_exp_f32 v188,v188
v_mfma_f32_16x16x32_bf16 a[20:23],a[232:235],v[136:139],a[20:23]
s_sub_u32 s79,s78,s76
v_exp_f32 v189,v189
v_mfma_f32_16x16x32_bf16 a[24:27],a[240:243],v[136:139],a[24:27]
s_add_u32 s78,s76,s60
v_exp_f32 v190,v190
v_mfma_f32_16x16x32_bf16 a[24:27],a[244:247],v[140:143],a[24:27]
s_cmp_gt_i32 s79,s95
s_cbranch_scc1 L_ps7_%=
L_pr7_%=:
v_cvt_pk_bf16_f32 v173,v186,v187
v_mfma_f32_16x16x32_bf16 a[28:31],a[252:255],v[140:143],a[28:31]
v_exp_f32 v191,v191
v_mfma_f32_16x16x32_bf16 a[28:31],a[248:251],v[136:139],a[28:31]
v_cvt_pk_bf16_f32 v174,v188,v189
v_cvt_pk_bf16_f32 v175,v190,v191
s_cmp_lg_u32 s53,0
v_smfmac_f32_16x16x64_bf16 v[48:51],v[28:31],v[136:143],v27
s_cbranch_scc1 L_att1_%=
L_att1x_%=:
L_endseg_%=:
v_mfma_f32_16x16x32_bf16 v[232:235],a[128:131],v[148:151],v[44:47]
s_waitcnt vmcnt(10)
s_lshl_b32 s43,s41,2
v_exp_f32 v200,v200
v_mfma_f32_16x16x32_bf16 v[232:235],a[132:135],v[180:183],v[232:235]
s_barrier
ds_read_b128 a[128:131],v1
v_add_u32 v2,s48,v2
v_mfma_f32_16x16x32_bf16 v[232:235],a[136:139],v[212:215],v[232:235]
s_add_u32 s43,s43,s70
v_exp_f32 v201,v201
v_mfma_f32_16x16x32_bf16 v[232:235],a[140:143],v[244:247],v[232:235]
s_lshl_b32 s43,s43,19
ds_read_b128 a[132:135],v1 offset:256
v_cvt_pk_bf16_f32 v200,v200,v201
v_mfma_f32_16x16x32_bf16 v[236:239],a[156:159],v[244:247],v[44:47]
s_add_u32 s43,s43,s55
v_exp_f32 v202,v202
v_mfma_f32_16x16x32_bf16 v[236:239],a[152:155],v[212:215],v[236:239]
s_mov_b64 s[28:29],s[12:13]
ds_read_b128 a[136:139],v1 offset:512
v_add_u32 v3,s48,v3
v_mfma_f32_16x16x32_bf16 v[236:239],a[148:151],v[180:183],v[236:239]
s_mov_b32 s48,32768
v_exp_f32 v203,v203
v_mfma_f32_16x16x32_bf16 v[236:239],a[144:147],v[148:151],v[236:239]
s_mov_b32 s30,475136
ds_read_b128 a[140:143],v1 offset:768
v_cvt_pk_bf16_f32 v201,v202,v203
v_mfma_f32_16x16x32_bf16 v[248:251],a[160:163],v[148:151],v[44:47]
s_mov_b32 s31,0x20000
v_exp_f32 v204,v204
v_mfma_f32_16x16x32_bf16 v[248:251],a[164:167],v[180:183],v[248:251]
ds_read_b128 a[156:159],v1 offset:4864
v_lshrrev_b32 v4,6,v6
v_mfma_f32_16x16x32_bf16 v[248:251],a[168:171],v[212:215],v[248:251]
v_exp_f32 v205,v205
v_mfma_f32_16x16x32_bf16 v[248:251],a[172:175],v[244:247],v[248:251]
ds_read_b128 a[152:155],v1 offset:4608
v_cvt_pk_bf16_f32 v202,v204,v205
v_mfma_f32_16x16x32_bf16 v[252:255],a[188:191],v[244:247],v[44:47]
v_exp_f32 v206,v206
v_mfma_f32_16x16x32_bf16 v[252:255],a[184:187],v[212:215],v[252:255]
ds_read_b128 a[148:151],v1 offset:4352
v_mul_u32_u24 v4,320,v4
v_mfma_f32_16x16x32_bf16 v[252:255],a[180:183],v[180:183],v[252:255]
v_exp_f32 v207,v207
v_mfma_f32_16x16x32_bf16 v[252:255],a[176:179],v[148:151],v[252:255]
ds_read_b128 a[144:147],v1 offset:4096
v_cvt_pk_bf16_f32 v203,v206,v207
v_mfma_f32_16x16x32_bf16 a[32:35],a[192:195],v[168:171],a[32:35]
v_exp_f32 v216,v216
v_mfma_f32_16x16x32_bf16 a[32:35],a[196:199],v[172:175],a[32:35]
ds_read_b128 a[160:163],v1 offset:8192
v_bfe_u32 v5,v6,4,2
v_mfma_f32_16x16x32_bf16 a[36:39],a[204:207],v[172:175],a[36:39]
v_exp_f32 v217,v217
v_mfma_f32_16x16x32_bf16 a[36:39],a[200:203],v[168:171],a[36:39]
ds_read_b128 a[164:167],v1 offset:8448
v_cvt_pk_bf16_f32 v204,v216,v217
v_mfma_f32_16x16x32_bf16 a[40:43],a[208:211],v[168:171],a[40:43]
v_exp_f32 v218,v218
v_mfma_f32_16x16x32_bf16 a[40:43],a[212:215],v[172:175],a[40:43]
ds_read_b128 a[168:171],v1 offset:8704
v_lshl_add_u32 v4,v5,3,v4
v_mfma_f32_16x16x32_bf16 a[44:47],a[220:223],v[172:175],a[44:47]
v_exp_f32 v219,v219
v_mfma_f32_16x16x32_bf16 a[44:47],a[216:219],v[168:171],a[44:47]
ds_read_b128 a[172:175],v1 offset:8960
v_cvt_pk_bf16_f32 v205,v218,v219
v_mfma_f32_16x16x32_bf16 a[48:51],a[224:227],v[168:171],a[48:51]
v_exp_f32 v220,v220
v_mfma_f32_16x16x32_bf16 a[48:51],a[228:231],v[172:175],a[48:51]
ds_read_b128 a[188:191],v1 offset:13056
v_bfe_u32 v5,v6,8,1
v_mfma_f32_16x16x32_bf16 a[52:55],a[236:239],v[172:175],a[52:55]
v_exp_f32 v221,v221
v_mfma_f32_16x16x32_bf16 a[52:55],a[232:235],v[168:171],a[52:55]
ds_read_b128 a[184:187],v1 offset:12800
v_cvt_pk_bf16_f32 v206,v220,v221
v_mfma_f32_16x16x32_bf16 a[56:59],a[240:243],v[168:171],a[56:59]
v_exp_f32 v222,v222
v_mfma_f32_16x16x32_bf16 a[56:59],a[244:247],v[172:175],a[56:59]
ds_read_b128 a[180:183],v1 offset:12544
v_lshlrev_b32 v5,5,v5
v_mfma_f32_16x16x32_bf16 a[60:63],a[252:255],v[172:175],a[60:63]
v_exp_f32 v223,v223
v_mfma_f32_16x16x32_bf16 a[60:63],a[248:251],v[168:171],a[60:63]
ds_read_b128 a[176:179],v1 offset:12288
)A"
R"A(v_cvt_pk_bf16_f32 v207,v222,v223
v_smfmac_f32_16x16x64_bf16 v[52:55],v[28:31],v[168:175],v27
s_waitcnt lgkmcnt(4)
v_add_u32 v1,s48,v1
v_mfma_f32_16x16x32_bf16 v[136:139],a[128:131],v[128:131],v[32:35]
v_exp_f32 v232,v232
v_mfma_f32_16x16x32_bf16 v[136:139],a[132:135],v[160:163],v[136:139]
v_exp_f32 v233,v233
v_mfma_f32_16x16x32_bf16 v[136:139],a[136:139],v[192:195],v[136:139]
v_exp_f32 v234,v234
v_mfma_f32_16x16x32_bf16 v[136:139],a[140:143],v[224:227],v[136:139]
v_exp_f32 v235,v235
v_mfma_f32_16x16x32_bf16 v[140:143],a[156:159],v[224:227],v[32:35]
v_cvt_pk_bf16_f32 v232,v232,v233
v_cvt_pk_bf16_f32 v233,v234,v235
v_mfma_f32_16x16x32_bf16 v[140:143],a[152:155],v[192:195],v[140:143]
s_add_u32 s80,s71,0
v_exp_f32 v236,v236
v_mfma_f32_16x16x32_bf16 v[140:143],a[148:151],v[160:163],v[140:143]
s_and_b32 s80,s80,3
v_exp_f32 v237,v237
v_mfma_f32_16x16x32_bf16 v[140:143],a[144:147],v[128:131],v[140:143]
s_lshl_b32 s80,s80,1
s_mul_i32 s80,s80,163840
s_add_u32 s80,s80,s81
v_mfma_f32_16x16x32_bf16 v[152:155],a[160:163],v[128:131],v[32:35]
s_add_u32 s83,s80,20480
v_exp_f32 v238,v238
v_mfma_f32_16x16x32_bf16 v[152:155],a[164:167],v[160:163],v[152:155]
buffer_store_dwordx4 v[64:67],v6,s[36:39],s84 offen sc1
v_mfma_f32_16x16x32_bf16 v[152:155],a[168:171],v[192:195],v[152:155]
v_exp_f32 v239,v239
v_mfma_f32_16x16x32_bf16 v[152:155],a[172:175],v[224:227],v[152:155]
s_waitcnt lgkmcnt(0)
v_cvt_pk_bf16_f32 v234,v236,v237
v_cvt_pk_bf16_f32 v235,v238,v239
v_mfma_f32_16x16x32_bf16 v[156:159],a[188:191],v[224:227],v[32:35]
v_exp_f32 v248,v248
v_mfma_f32_16x16x32_bf16 v[156:159],a[184:187],v[192:195],v[156:159]
v_exp_f32 v249,v249
v_mfma_f32_16x16x32_bf16 v[156:159],a[180:183],v[160:163],v[156:159]
v_exp_f32 v250,v250
v_mfma_f32_16x16x32_bf16 v[156:159],a[176:179],v[128:131],v[156:159]
v_exp_f32 v251,v251
v_mfma_f32_16x16x32_bf16 a[64:67],a[192:195],v[200:203],a[64:67]
v_exp_f32 v252,v252
v_mfma_f32_16x16x32_bf16 a[64:67],a[196:199],v[204:207],a[64:67]
v_exp_f32 v253,v253
v_mfma_f32_16x16x32_bf16 a[68:71],a[204:207],v[204:207],a[68:71]
v_exp_f32 v254,v254
v_mfma_f32_16x16x32_bf16 a[68:71],a[200:203],v[200:203],a[68:71]
v_exp_f32 v255,v255
v_mfma_f32_16x16x32_bf16 a[72:75],a[208:211],v[200:203],a[72:75]
v_cvt_pk_bf16_f32 v236,v248,v249
v_cvt_pk_bf16_f32 v237,v250,v251
v_mfma_f32_16x16x32_bf16 a[72:75],a[212:215],v[204:207],a[72:75]
v_cvt_pk_bf16_f32 v238,v252,v253
v_cvt_pk_bf16_f32 v239,v254,v255
v_mfma_f32_16x16x32_bf16 a[76:79],a[220:223],v[204:207],a[76:79]
v_exp_f32 v136,v136
v_mfma_f32_16x16x32_bf16 a[76:79],a[216:219],v[200:203],a[76:79]
v_exp_f32 v137,v137
v_mfma_f32_16x16x32_bf16 a[80:83],a[224:227],v[200:203],a[80:83]
v_exp_f32 v138,v138
v_mfma_f32_16x16x32_bf16 a[80:83],a[228:231],v[204:207],a[80:83]
v_exp_f32 v139,v139
v_mfma_f32_16x16x32_bf16 a[84:87],a[236:239],v[204:207],a[84:87]
s_add_u32 m0,s82,81920
v_exp_f32 v140,v140
v_mfma_f32_16x16x32_bf16 a[84:87],a[232:235],v[200:203],a[84:87]
v_cvt_pk_bf16_f32 v136,v136,v137
v_cvt_pk_bf16_f32 v137,v138,v139
v_mfma_f32_16x16x32_bf16 a[88:91],a[240:243],v[200:203],a[88:91]
v_exp_f32 v141,v141
v_mfma_f32_16x16x32_bf16 a[88:91],a[244:247],v[204:207],a[88:91]
buffer_load_dwordx4 v7,s[72:75],s80 offen lds
v_mfma_f32_16x16x32_bf16 a[92:95],a[252:255],v[204:207],a[92:95]
v_exp_f32 v142,v142
v_mfma_f32_16x16x32_bf16 a[92:95],a[248:251],v[200:203],a[92:95]
v_exp_f32 v143,v143
v_smfmac_f32_16x16x64_bf16 v[56:59],v[28:31],v[200:207],v27
v_cvt_pk_bf16_f32 v138,v140,v141
v_mfma_f32_16x16x32_bf16 v[168:171],a[128:131],v[132:135],v[36:39]
buffer_store_dwordx4 v[68:71],v6,s[36:39],s84 offen offset:1024 sc1
v_mfma_f32_16x16x32_bf16 v[168:171],a[132:135],v[164:167],v[168:171]
s_add_u32 s28,s28,s43
s_addc_u32 s29,s29,0
v_cvt_pk_bf16_f32 v139,v142,v143
v_mfma_f32_16x16x32_bf16 v[168:171],a[136:139],v[196:199],v[168:171]
s_and_b32 s29,s29,0xffff
v_exp_f32 v152,v152
v_mfma_f32_16x16x32_bf16 v[168:171],a[140:143],v[228:231],v[168:171]
v_exp_f32 v153,v153
v_mfma_f32_16x16x32_bf16 v[172:175],a[156:159],v[228:231],v[36:39]
v_exp_f32 v154,v154
v_mfma_f32_16x16x32_bf16 v[172:175],a[152:155],v[196:199],v[172:175]
v_exp_f32 v155,v155
v_mfma_f32_16x16x32_bf16 v[172:175],a[148:151],v[164:167],v[172:175]
v_exp_f32 v156,v156
v_mfma_f32_16x16x32_bf16 v[172:175],a[144:147],v[132:135],v[172:175]
v_exp_f32 v157,v157
v_mfma_f32_16x16x32_bf16 v[184:187],a[160:163],v[132:135],v[36:39]
v_exp_f32 v158,v158
v_mfma_f32_16x16x32_bf16 v[184:187],a[164:167],v[164:167],v[184:187]
v_exp_f32 v159,v159
v_mfma_f32_16x16x32_bf16 v[184:187],a[168:171],v[196:199],v[184:187]
v_cvt_pk_bf16_f32 v140,v152,v153
v_cvt_pk_bf16_f32 v141,v154,v155
v_mfma_f32_16x16x32_bf16 v[184:187],a[172:175],v[228:231],v[184:187]
v_cvt_pk_bf16_f32 v142,v156,v157
v_cvt_pk_bf16_f32 v143,v158,v159
v_mfma_f32_16x16x32_bf16 v[188:191],a[188:191],v[228:231],v[36:39]
v_exp_f32 v168,v168
v_mfma_f32_16x16x32_bf16 v[188:191],a[184:187],v[196:199],v[188:191]
v_exp_f32 v169,v169
v_mfma_f32_16x16x32_bf16 v[188:191],a[180:183],v[164:167],v[188:191]
v_exp_f32 v170,v170
v_mfma_f32_16x16x32_bf16 v[188:191],a[176:179],v[132:135],v[188:191]
v_exp_f32 v171,v171
v_mfma_f32_16x16x32_bf16 a[96:99],a[192:195],v[232:235],a[96:99]
buffer_load_dwordx4 v8,s[72:75],s80 offen offset:1024 lds
v_mfma_f32_16x16x32_bf16 a[96:99],a[196:199],v[236:239],a[96:99]
ds_read_b64_tr_b16 a[192:193],v2
ds_read_b64_tr_b16 a[194:195],v2 offset:4096
v_mfma_f32_16x16x32_bf16 a[100:103],a[204:207],v[236:239],a[100:103]
ds_read_b64_tr_b16 a[196:197],v2 offset:8192
v_cvt_pk_bf16_f32 v168,v168,v169
v_mfma_f32_16x16x32_bf16 a[100:103],a[200:203],v[232:235],a[100:103]
ds_read_b64_tr_b16 a[198:199],v2 offset:12288
v_cvt_pk_bf16_f32 v169,v170,v171
v_mfma_f32_16x16x32_bf16 a[104:107],a[208:211],v[232:235],a[104:107]
buffer_store_dwordx4 v[72:75],v6,s[36:39],s84 offen offset:2048 sc1
v_mfma_f32_16x16x32_bf16 a[104:107],a[212:215],v[236:239],a[104:107]
ds_read_b64_tr_b16 a[204:205],v3 offset:8192
ds_read_b64_tr_b16 a[206:207],v3 offset:12288
v_mfma_f32_16x16x32_bf16 a[108:111],a[220:223],v[236:239],a[108:111]
v_exp_f32 v172,v172
v_mfma_f32_16x16x32_bf16 a[108:111],a[216:219],v[232:235],a[108:111]
ds_read_b64_tr_b16 a[200:201],v3
ds_read_b64_tr_b16 a[202:203],v3 offset:4096
v_mfma_f32_16x16x32_bf16 a[112:115],a[224:227],v[232:235],a[112:115]
v_exp_f32 v173,v173
v_mfma_f32_16x16x32_bf16 a[112:115],a[228:231],v[236:239],a[112:115]
ds_read_b64_tr_b16 a[208:209],v2 offset:256
ds_read_b64_tr_b16 a[210:211],v2 offset:4352
v_mfma_f32_16x16x32_bf16 a[116:119],a[236:239],v[236:239],a[116:119]
ds_read_b64_tr_b16 a[212:213],v2 offset:8448
v_cvt_pk_bf16_f32 v170,v172,v173
v_mfma_f32_16x16x32_bf16 a[116:119],a[232:235],v[232:235],a[116:119]
v_exp_f32 v174,v174
v_mfma_f32_16x16x32_bf16 a[120:123],a[240:243],v[232:235],a[120:123]
ds_read_b64_tr_b16 a[214:215],v2 offset:12544
ds_read_b64_tr_b16 a[220:221],v3 offset:8448
v_mfma_f32_16x16x32_bf16 a[120:123],a[244:247],v[236:239],a[120:123]
ds_read_b64_tr_b16 a[222:223],v3 offset:12544
v_mfma_f32_16x16x32_bf16 a[124:127],a[252:255],v[236:239],a[124:127]
ds_read_b64_tr_b16 a[216:217],v3 offset:256
v_mfma_f32_16x16x32_bf16 a[124:127],a[248:251],v[232:235],a[124:127]
ds_read_b64_tr_b16 a[218:219],v3 offset:4352
v_smfmac_f32_16x16x64_bf16 v[60:63],v[28:31],v[232:239],v27
ds_read_b64_tr_b16 a[224:225],v2 offset:512
v_mfma_f32_16x16x32_bf16 v[200:203],a[128:131],v[144:147],v[40:43]
ds_read_b64_tr_b16 a[226:227],v2 offset:4608
ds_read_b64_tr_b16 a[228:229],v2 offset:8704
v_mfma_f32_16x16x32_bf16 v[200:203],a[132:135],v[176:179],v[200:203]
ds_read_b64_tr_b16 a[230:231],v2 offset:12800
v_mfma_f32_16x16x32_bf16 v[200:203],a[136:139],v[208:211],v[200:203]
ds_read_b64_tr_b16 a[236:237],v3 offset:8704
v_mfma_f32_16x16x32_bf16 v[200:203],a[140:143],v[240:243],v[200:203]
buffer_load_dwordx4 v9,s[72:75],s80 offen offset:2048 lds
v_mfma_f32_16x16x32_bf16 v[204:207],a[156:159],v[240:243],v[40:43]
ds_read_b64_tr_b16 a[238:239],v3 offset:12800
ds_read_b64_tr_b16 a[232:233],v3 offset:512
v_mfma_f32_16x16x32_bf16 v[204:207],a[152:155],v[208:211],v[204:207]
ds_read_b64_tr_b16 a[234:235],v3 offset:4608
v_mfma_f32_16x16x32_bf16 v[204:207],a[148:151],v[176:179],v[204:207]
ds_read_b64_tr_b16 a[240:241],v2 offset:768
v_mfma_f32_16x16x32_bf16 v[204:207],a[144:147],v[144:147],v[204:207]
ds_read_b64_tr_b16 a[242:243],v2 offset:4864
v_mfma_f32_16x16x32_bf16 v[216:219],a[160:163],v[144:147],v[40:43]
ds_read_b64_tr_b16 a[244:245],v2 offset:8960
ds_read_b64_tr_b16 a[246:247],v2 offset:13056
v_mfma_f32_16x16x32_bf16 v[216:219],a[164:167],v[176:179],v[216:219]
buffer_store_dwordx4 v[76:79],v6,s[36:39],s84 offen offset:3072 sc1
v_mfma_f32_16x16x32_bf16 v[216:219],a[168:171],v[208:211],v[216:219]
s_add_u32 s84,s55,65536
ds_read_b64_tr_b16 a[252:253],v3 offset:8960
ds_read_b64_tr_b16 a[254:255],v3 offset:13056
v_mfma_f32_16x16x32_bf16 v[216:219],a[172:175],v[240:243],v[216:219]
s_max_i32 s84,s84,s18
ds_read_b64_tr_b16 a[248:249],v3 offset:768
v_add_u32 v2,s48,v2
v_mfma_f32_16x16x32_bf16 v[220:223],a[188:191],v[240:243],v[40:43]
ds_read_b64_tr_b16 a[250:251],v3 offset:4864
v_add_u32 v3,s48,v3
v_mfma_f32_16x16x32_bf16 v[220:223],a[184:187],v[208:211],v[220:223]
s_mov_b32 s48,98304
v_exp_f32 v175,v175
v_mfma_f32_16x16x32_bf16 v[220:223],a[180:183],v[176:179],v[220:223]
v_exp_f32 v184,v184
v_mfma_f32_16x16x32_bf16 v[220:223],a[176:179],v[144:147],v[220:223]
s_waitcnt lgkmcnt(6)
v_exp_f32 v185,v185
v_mfma_f32_16x16x32_bf16 a[0:3],a[192:195],v[136:139],a[0:3]
v_cvt_pk_bf16_f32 v171,v174,v175
v_cvt_pk_bf16_f32 v172,v184,v185
v_mfma_f32_16x16x32_bf16 a[0:3],a[196:199],v[140:143],a[0:3]
v_exp_f32 v186,v186
v_mfma_f32_16x16x32_bf16 a[4:7],a[204:207],v[140:143],a[4:7]
v_exp_f32 v187,v187
v_mfma_f32_16x16x32_bf16 a[4:7],a[200:203],v[136:139],a[4:7]
v_exp_f32 v188,v188
v_mfma_f32_16x16x32_bf16 a[8:11],a[208:211],v[136:139],a[8:11]
v_exp_f32 v189,v189
v_mfma_f32_16x16x32_bf16 a[8:11],a[212:215],v[140:143],a[8:11]
v_exp_f32 v190,v190
v_mfma_f32_16x16x32_bf16 a[12:15],a[220:223],v[140:143],a[12:15]
v_exp_f32 v191,v191
v_mfma_f32_16x16x32_bf16 a[12:15],a[216:219],v[136:139],a[12:15]
buffer_load_dwordx4 v10,s[72:75],s80 offen offset:3072 lds
v_mfma_f32_16x16x32_bf16 a[16:19],a[224:227],v[136:139],a[16:19]
v_cvt_pk_bf16_f32 v173,v186,v187
v_cvt_pk_bf16_f32 v174,v188,v189
v_mfma_f32_16x16x32_bf16 a[16:19],a[228:231],v[140:143],a[16:19]
v_exp_f32 v200,v200
v_mfma_f32_16x16x32_bf16 a[20:23],a[236:239],v[140:143],a[20:23]
v_exp_f32 v201,v201
v_mfma_f32_16x16x32_bf16 a[20:23],a[232:235],v[136:139],a[20:23]
v_cvt_pk_bf16_f32 v175,v190,v191
v_cvt_pk_bf16_f32 v200,v200,v201
v_mfma_f32_16x16x32_bf16 a[24:27],a[240:243],v[136:139],a[24:27]
s_waitcnt lgkmcnt(0)
v_exp_f32 v202,v202
v_mfma_f32_16x16x32_bf16 a[24:27],a[244:247],v[140:143],a[24:27]
v_exp_f32 v203,v203
v_mfma_f32_16x16x32_bf16 a[28:31],a[252:255],v[140:143],a[28:31]
v_exp_f32 v204,v204
v_mfma_f32_16x16x32_bf16 a[28:31],a[248:251],v[136:139],a[28:31]
buffer_store_dwordx4 v[80:83],v6,s[36:39],s85 offen sc1
v_smfmac_f32_16x16x64_bf16 v[48:51],v[28:31],v[136:143],v27
v_cvt_pk_bf16_f32 v201,v202,v203
v_mfma_f32_16x16x32_bf16 v[232:235],a[128:131],v[148:151],v[44:47]
s_waitcnt vmcnt(10)
v_exp_f32 v205,v205
v_mfma_f32_16x16x32_bf16 v[232:235],a[132:135],v[180:183],v[232:235]
s_barrier
ds_read_b128 a[128:131],v1
v_cvt_pk_bf16_f32 v202,v204,v205
v_mfma_f32_16x16x32_bf16 v[232:235],a[136:139],v[212:215],v[232:235]
v_exp_f32 v206,v206
v_mfma_f32_16x16x32_bf16 v[232:235],a[140:143],v[244:247],v[232:235]
v_exp_f32 v207,v207
v_mfma_f32_16x16x32_bf16 v[236:239],a[156:159],v[244:247],v[44:47]
ds_read_b128 a[132:135],v1 offset:256
v_cvt_pk_bf16_f32 v203,v206,v207
v_mfma_f32_16x16x32_bf16 v[236:239],a[152:155],v[212:215],v[236:239]
v_exp_f32 v216,v216
v_mfma_f32_16x16x32_bf16 v[236:239],a[148:151],v[180:183],v[236:239]
s_add_u32 m0,s82,86016
v_exp_f32 v217,v217
v_mfma_f32_16x16x32_bf16 v[236:239],a[144:147],v[148:151],v[236:239]
ds_read_b128 a[136:139],v1 offset:512
v_cvt_pk_bf16_f32 v204,v216,v217
v_mfma_f32_16x16x32_bf16 v[248:251],a[160:163],v[148:151],v[44:47]
v_exp_f32 v218,v218
)A"
R"A(v_mfma_f32_16x16x32_bf16 v[248:251],a[164:167],v[180:183],v[248:251]
buffer_load_dwordx4 v11,s[72:75],s80 offen lds
v_mfma_f32_16x16x32_bf16 v[248:251],a[168:171],v[212:215],v[248:251]
ds_read_b128 a[140:143],v1 offset:768
v_mfma_f32_16x16x32_bf16 v[248:251],a[172:175],v[244:247],v[248:251]
v_exp_f32 v219,v219
v_mfma_f32_16x16x32_bf16 v[252:255],a[188:191],v[244:247],v[44:47]
ds_read_b128 a[156:159],v1 offset:4864
v_cvt_pk_bf16_f32 v205,v218,v219
v_mfma_f32_16x16x32_bf16 v[252:255],a[184:187],v[212:215],v[252:255]
v_exp_f32 v220,v220
v_mfma_f32_16x16x32_bf16 v[252:255],a[180:183],v[180:183],v[252:255]
ds_read_b128 a[152:155],v1 offset:4608
v_mfma_f32_16x16x32_bf16 v[252:255],a[176:179],v[148:151],v[252:255]
v_exp_f32 v221,v221
v_mfma_f32_16x16x32_bf16 a[32:35],a[192:195],v[168:171],a[32:35]
ds_read_b128 a[148:151],v1 offset:4352
v_cvt_pk_bf16_f32 v206,v220,v221
v_mfma_f32_16x16x32_bf16 a[32:35],a[196:199],v[172:175],a[32:35]
v_exp_f32 v222,v222
v_mfma_f32_16x16x32_bf16 a[36:39],a[204:207],v[172:175],a[36:39]
ds_read_b128 a[144:147],v1 offset:4096
v_mfma_f32_16x16x32_bf16 a[36:39],a[200:203],v[168:171],a[36:39]
buffer_store_dwordx4 v[84:87],v6,s[36:39],s85 offen offset:1024 sc1
v_mfma_f32_16x16x32_bf16 a[40:43],a[208:211],v[168:171],a[40:43]
ds_read_b128 a[160:163],v1 offset:8192
v_mfma_f32_16x16x32_bf16 a[40:43],a[212:215],v[172:175],a[40:43]
v_exp_f32 v223,v223
v_mfma_f32_16x16x32_bf16 a[44:47],a[220:223],v[172:175],a[44:47]
ds_read_b128 a[164:167],v1 offset:8448
v_cvt_pk_bf16_f32 v207,v222,v223
v_mfma_f32_16x16x32_bf16 a[44:47],a[216:219],v[168:171],a[44:47]
v_exp_f32 v232,v232
v_mfma_f32_16x16x32_bf16 a[48:51],a[224:227],v[168:171],a[48:51]
ds_read_b128 a[168:171],v1 offset:8704
v_mfma_f32_16x16x32_bf16 a[48:51],a[228:231],v[172:175],a[48:51]
v_exp_f32 v233,v233
v_mfma_f32_16x16x32_bf16 a[52:55],a[236:239],v[172:175],a[52:55]
s_add_u32 m0,s82,87040
ds_read_b128 a[172:175],v1 offset:8960
v_cvt_pk_bf16_f32 v232,v232,v233
v_mfma_f32_16x16x32_bf16 a[52:55],a[232:235],v[168:171],a[52:55]
v_exp_f32 v234,v234
v_mfma_f32_16x16x32_bf16 a[56:59],a[240:243],v[168:171],a[56:59]
ds_read_b128 a[188:191],v1 offset:13056
v_mfma_f32_16x16x32_bf16 a[56:59],a[244:247],v[172:175],a[56:59]
buffer_load_dwordx4 v7,s[72:75],s83 offen lds
v_mfma_f32_16x16x32_bf16 a[60:63],a[252:255],v[172:175],a[60:63]
ds_read_b128 a[184:187],v1 offset:12800
v_mfma_f32_16x16x32_bf16 a[60:63],a[248:251],v[168:171],a[60:63]
v_exp_f32 v235,v235
v_smfmac_f32_16x16x64_bf16 v[52:55],v[28:31],v[168:175],v27
s_waitcnt lgkmcnt(3)
ds_read_b128 a[180:183],v1 offset:12544
v_mfma_f32_16x16x32_bf16 v[136:139],a[128:131],v[128:131],v[32:35]
v_exp_f32 v236,v236
v_mfma_f32_16x16x32_bf16 v[136:139],a[132:135],v[160:163],v[136:139]
ds_read_b128 a[176:179],v1 offset:12288
v_cvt_pk_bf16_f32 v233,v234,v235
v_mfma_f32_16x16x32_bf16 v[136:139],a[136:139],v[192:195],v[136:139]
v_exp_f32 v237,v237
v_mfma_f32_16x16x32_bf16 v[136:139],a[140:143],v[224:227],v[136:139]
v_exp_f32 v238,v238
v_mfma_f32_16x16x32_bf16 v[140:143],a[156:159],v[224:227],v[32:35]
v_exp_f32 v239,v239
v_mfma_f32_16x16x32_bf16 v[140:143],a[152:155],v[192:195],v[140:143]
v_cvt_pk_bf16_f32 v234,v236,v237
v_cvt_pk_bf16_f32 v235,v238,v239
v_mfma_f32_16x16x32_bf16 v[140:143],a[148:151],v[160:163],v[140:143]
v_exp_f32 v248,v248
v_mfma_f32_16x16x32_bf16 v[140:143],a[144:147],v[128:131],v[140:143]
v_exp_f32 v249,v249
v_mfma_f32_16x16x32_bf16 v[152:155],a[160:163],v[128:131],v[32:35]
buffer_store_dwordx4 v[88:91],v6,s[36:39],s85 offen offset:2048 sc1
v_mfma_f32_16x16x32_bf16 v[152:155],a[164:167],v[160:163],v[152:155]
v_exp_f32 v250,v250
v_mfma_f32_16x16x32_bf16 v[152:155],a[168:171],v[192:195],v[152:155]
s_waitcnt lgkmcnt(0)
v_exp_f32 v251,v251
v_mfma_f32_16x16x32_bf16 v[152:155],a[172:175],v[224:227],v[152:155]
v_exp_f32 v252,v252
v_mfma_f32_16x16x32_bf16 v[156:159],a[188:191],v[224:227],v[32:35]
v_exp_f32 v253,v253
v_mfma_f32_16x16x32_bf16 v[156:159],a[184:187],v[192:195],v[156:159]
v_exp_f32 v254,v254
v_mfma_f32_16x16x32_bf16 v[156:159],a[180:183],v[160:163],v[156:159]
v_exp_f32 v255,v255
v_mfma_f32_16x16x32_bf16 v[156:159],a[176:179],v[128:131],v[156:159]
v_cvt_pk_bf16_f32 v236,v248,v249
v_cvt_pk_bf16_f32 v237,v250,v251
v_mfma_f32_16x16x32_bf16 a[64:67],a[192:195],v[200:203],a[64:67]
buffer_load_dwordx4 v8,s[72:75],s83 offen offset:1024 lds
v_mfma_f32_16x16x32_bf16 a[64:67],a[196:199],v[204:207],a[64:67]
v_cvt_pk_bf16_f32 v238,v252,v253
v_cvt_pk_bf16_f32 v239,v254,v255
v_mfma_f32_16x16x32_bf16 a[68:71],a[204:207],v[204:207],a[68:71]
v_exp_f32 v136,v136
v_mfma_f32_16x16x32_bf16 a[68:71],a[200:203],v[200:203],a[68:71]
v_exp_f32 v137,v137
v_mfma_f32_16x16x32_bf16 a[72:75],a[208:211],v[200:203],a[72:75]
v_exp_f32 v138,v138
v_mfma_f32_16x16x32_bf16 a[72:75],a[212:215],v[204:207],a[72:75]
v_exp_f32 v139,v139
v_mfma_f32_16x16x32_bf16 a[76:79],a[220:223],v[204:207],a[76:79]
v_cvt_pk_bf16_f32 v136,v136,v137
v_cvt_pk_bf16_f32 v137,v138,v139
v_mfma_f32_16x16x32_bf16 a[76:79],a[216:219],v[200:203],a[76:79]
v_exp_f32 v140,v140
v_mfma_f32_16x16x32_bf16 a[80:83],a[224:227],v[200:203],a[80:83]
v_exp_f32 v141,v141
v_mfma_f32_16x16x32_bf16 a[80:83],a[228:231],v[204:207],a[80:83]
v_exp_f32 v142,v142
v_mfma_f32_16x16x32_bf16 a[84:87],a[236:239],v[204:207],a[84:87]
v_exp_f32 v143,v143
v_mfma_f32_16x16x32_bf16 a[84:87],a[232:235],v[200:203],a[84:87]
v_cvt_pk_bf16_f32 v138,v140,v141
v_cvt_pk_bf16_f32 v139,v142,v143
v_mfma_f32_16x16x32_bf16 a[88:91],a[240:243],v[200:203],a[88:91]
v_exp_f32 v152,v152
v_mfma_f32_16x16x32_bf16 a[88:91],a[244:247],v[204:207],a[88:91]
v_exp_f32 v153,v153
v_mfma_f32_16x16x32_bf16 a[92:95],a[252:255],v[204:207],a[92:95]
buffer_store_dwordx4 v[92:95],v6,s[36:39],s85 offen offset:3072 sc1
v_mfma_f32_16x16x32_bf16 a[92:95],a[248:251],v[200:203],a[92:95]
s_add_u32 s85,s55,69632
v_exp_f32 v154,v154
v_smfmac_f32_16x16x64_bf16 v[56:59],v[28:31],v[200:207],v27
s_max_i32 s85,s85,s19
v_cvt_pk_bf16_f32 v140,v152,v153
v_mfma_f32_16x16x32_bf16 v[168:171],a[128:131],v[132:135],v[36:39]
v_exp_f32 v155,v155
v_mfma_f32_16x16x32_bf16 v[168:171],a[132:135],v[164:167],v[168:171]
v_exp_f32 v156,v156
v_mfma_f32_16x16x32_bf16 v[168:171],a[136:139],v[196:199],v[168:171]
v_exp_f32 v157,v157
v_mfma_f32_16x16x32_bf16 v[168:171],a[140:143],v[228:231],v[168:171]
buffer_load_dwordx4 v9,s[72:75],s83 offen offset:2048 lds
v_mfma_f32_16x16x32_bf16 v[172:175],a[156:159],v[228:231],v[36:39]
v_exp_f32 v158,v158
v_mfma_f32_16x16x32_bf16 v[172:175],a[152:155],v[196:199],v[172:175]
v_exp_f32 v159,v159
v_mfma_f32_16x16x32_bf16 v[172:175],a[148:151],v[164:167],v[172:175]
v_cvt_pk_bf16_f32 v141,v154,v155
v_cvt_pk_bf16_f32 v142,v156,v157
v_mfma_f32_16x16x32_bf16 v[172:175],a[144:147],v[132:135],v[172:175]
v_cvt_pk_bf16_f32 v143,v158,v159
v_add_u32 v1,s48,v1
v_mfma_f32_16x16x32_bf16 v[184:187],a[160:163],v[132:135],v[36:39]
v_mfma_f32_16x16x32_bf16 v[184:187],a[164:167],v[164:167],v[184:187]
v_exp_f32 v168,v168
v_mfma_f32_16x16x32_bf16 v[184:187],a[168:171],v[196:199],v[184:187]
v_exp_f32 v169,v169
v_mfma_f32_16x16x32_bf16 v[184:187],a[172:175],v[228:231],v[184:187]
v_exp_f32 v170,v170
v_mfma_f32_16x16x32_bf16 v[188:191],a[188:191],v[228:231],v[36:39]
v_exp_f32 v171,v171
v_mfma_f32_16x16x32_bf16 v[188:191],a[184:187],v[196:199],v[188:191]
v_exp_f32 v172,v172
v_mfma_f32_16x16x32_bf16 v[188:191],a[180:183],v[164:167],v[188:191]
v_exp_f32 v173,v173
v_mfma_f32_16x16x32_bf16 v[188:191],a[176:179],v[132:135],v[188:191]
v_exp_f32 v174,v174
v_mfma_f32_16x16x32_bf16 a[96:99],a[192:195],v[232:235],a[96:99]
ds_read_b64_tr_b16 a[192:193],v2
ds_read_b64_tr_b16 a[194:195],v2 offset:4096
v_mfma_f32_16x16x32_bf16 a[96:99],a[196:199],v[236:239],a[96:99]
ds_read_b64_tr_b16 a[196:197],v2 offset:8192
v_cvt_pk_bf16_f32 v168,v168,v169
v_mfma_f32_16x16x32_bf16 a[100:103],a[204:207],v[236:239],a[100:103]
ds_read_b64_tr_b16 a[198:199],v2 offset:12288
v_cvt_pk_bf16_f32 v169,v170,v171
v_mfma_f32_16x16x32_bf16 a[100:103],a[200:203],v[232:235],a[100:103]
buffer_store_dwordx4 v[96:99],v6,s[36:39],s86 offen sc1
v_mfma_f32_16x16x32_bf16 a[104:107],a[208:211],v[232:235],a[104:107]
ds_read_b64_tr_b16 a[204:205],v3 offset:8192
ds_read_b64_tr_b16 a[206:207],v3 offset:12288
v_mfma_f32_16x16x32_bf16 a[104:107],a[212:215],v[236:239],a[104:107]
ds_read_b64_tr_b16 a[200:201],v3
v_cvt_pk_bf16_f32 v170,v172,v173
v_mfma_f32_16x16x32_bf16 a[108:111],a[220:223],v[236:239],a[108:111]
v_exp_f32 v175,v175
v_mfma_f32_16x16x32_bf16 a[108:111],a[216:219],v[232:235],a[108:111]
buffer_load_dwordx4 v10,s[72:75],s83 offen offset:3072 lds
v_mfma_f32_16x16x32_bf16 a[112:115],a[224:227],v[232:235],a[112:115]
ds_read_b64_tr_b16 a[202:203],v3 offset:4096
ds_read_b64_tr_b16 a[208:209],v2 offset:256
v_mfma_f32_16x16x32_bf16 a[112:115],a[228:231],v[236:239],a[112:115]
ds_read_b64_tr_b16 a[210:211],v2 offset:4352
v_cvt_pk_bf16_f32 v171,v174,v175
v_mfma_f32_16x16x32_bf16 a[116:119],a[236:239],v[236:239],a[116:119]
v_exp_f32 v184,v184
v_mfma_f32_16x16x32_bf16 a[116:119],a[232:235],v[232:235],a[116:119]
ds_read_b64_tr_b16 a[212:213],v2 offset:8448
ds_read_b64_tr_b16 a[214:215],v2 offset:12544
v_mfma_f32_16x16x32_bf16 a[120:123],a[240:243],v[232:235],a[120:123]
v_exp_f32 v185,v185
v_mfma_f32_16x16x32_bf16 a[120:123],a[244:247],v[236:239],a[120:123]
ds_read_b64_tr_b16 a[220:221],v3 offset:8448
ds_read_b64_tr_b16 a[222:223],v3 offset:12544
v_mfma_f32_16x16x32_bf16 a[124:127],a[252:255],v[236:239],a[124:127]
ds_read_b64_tr_b16 a[216:217],v3 offset:256
v_cvt_pk_bf16_f32 v172,v184,v185
v_mfma_f32_16x16x32_bf16 a[124:127],a[248:251],v[232:235],a[124:127]
ds_read_b64_tr_b16 a[218:219],v3 offset:4352
v_smfmac_f32_16x16x64_bf16 v[60:63],v[28:31],v[232:239],v27
ds_read_b64_tr_b16 a[224:225],v2 offset:512
v_mfma_f32_16x16x32_bf16 v[200:203],a[128:131],v[144:147],v[40:43]
ds_read_b64_tr_b16 a[226:227],v2 offset:4608
ds_read_b64_tr_b16 a[228:229],v2 offset:8704
v_mfma_f32_16x16x32_bf16 v[200:203],a[132:135],v[176:179],v[200:203]
ds_read_b64_tr_b16 a[230:231],v2 offset:12800
v_mfma_f32_16x16x32_bf16 v[200:203],a[136:139],v[208:211],v[200:203]
ds_read_b64_tr_b16 a[236:237],v3 offset:8704
v_mfma_f32_16x16x32_bf16 v[200:203],a[140:143],v[240:243],v[200:203]
ds_read_b64_tr_b16 a[238:239],v3 offset:12800
v_mfma_f32_16x16x32_bf16 v[204:207],a[156:159],v[240:243],v[40:43]
ds_read_b64_tr_b16 a[232:233],v3 offset:512
ds_read_b64_tr_b16 a[234:235],v3 offset:4608
v_mfma_f32_16x16x32_bf16 v[204:207],a[152:155],v[208:211],v[204:207]
ds_read_b64_tr_b16 a[240:241],v2 offset:768
v_mfma_f32_16x16x32_bf16 v[204:207],a[148:151],v[176:179],v[204:207]
ds_read_b64_tr_b16 a[242:243],v2 offset:4864
v_mfma_f32_16x16x32_bf16 v[204:207],a[144:147],v[144:147],v[204:207]
s_add_u32 m0,s82,91136
ds_read_b64_tr_b16 a[244:245],v2 offset:8960
v_mfma_f32_16x16x32_bf16 v[216:219],a[160:163],v[144:147],v[40:43]
buffer_store_dwordx4 v[100:103],v6,s[36:39],s86 offen offset:1024 sc1
v_mfma_f32_16x16x32_bf16 v[216:219],a[164:167],v[176:179],v[216:219]
ds_read_b64_tr_b16 a[246:247],v2 offset:13056
ds_read_b64_tr_b16 a[252:253],v3 offset:8960
v_mfma_f32_16x16x32_bf16 v[216:219],a[168:171],v[208:211],v[216:219]
ds_read_b64_tr_b16 a[254:255],v3 offset:13056
v_add_u32 v2,s48,v2
v_mfma_f32_16x16x32_bf16 v[216:219],a[172:175],v[240:243],v[216:219]
ds_read_b64_tr_b16 a[248:249],v3 offset:768
v_mfma_f32_16x16x32_bf16 v[220:223],a[188:191],v[240:243],v[40:43]
buffer_load_dwordx4 v11,s[72:75],s83 offen lds
v_mfma_f32_16x16x32_bf16 v[220:223],a[184:187],v[208:211],v[220:223]
ds_read_b64_tr_b16 a[250:251],v3 offset:4864
v_add_u32 v3,s48,v3
v_mfma_f32_16x16x32_bf16 v[220:223],a[180:183],v[176:179],v[220:223]
v_exp_f32 v186,v186
v_mfma_f32_16x16x32_bf16 v[220:223],a[176:179],v[144:147],v[220:223]
s_waitcnt lgkmcnt(5)
v_exp_f32 v187,v187
v_mfma_f32_16x16x32_bf16 a[0:3],a[192:195],v[136:139],a[0:3]
v_exp_f32 v188,v188
v_mfma_f32_16x16x32_bf16 a[0:3],a[196:199],v[140:143],a[0:3]
s_add_u32 s80,s71,0
s_and_b32 s80,s80,3
s_lshl_b32 s80,s80,1
v_mfma_f32_16x16x32_bf16 a[4:7],a[204:207],v[140:143],a[4:7]
s_or_b32 s80,s80,1
s_mul_i32 s80,s80,163840
s_add_u32 s80,s80,s81
)A"
R"A(v_mfma_f32_16x16x32_bf16 a[4:7],a[200:203],v[136:139],a[4:7]
s_add_u32 s83,s80,20480
v_exp_f32 v189,v189
v_mfma_f32_16x16x32_bf16 a[8:11],a[208:211],v[136:139],a[8:11]
v_exp_f32 v190,v190
v_mfma_f32_16x16x32_bf16 a[8:11],a[212:215],v[140:143],a[8:11]
v_exp_f32 v191,v191
v_mfma_f32_16x16x32_bf16 a[12:15],a[220:223],v[140:143],a[12:15]
v_cvt_pk_bf16_f32 v173,v186,v187
v_cvt_pk_bf16_f32 v174,v188,v189
v_mfma_f32_16x16x32_bf16 a[12:15],a[216:219],v[136:139],a[12:15]
v_exp_f32 v200,v200
v_mfma_f32_16x16x32_bf16 a[16:19],a[224:227],v[136:139],a[16:19]
v_exp_f32 v201,v201
v_mfma_f32_16x16x32_bf16 a[16:19],a[228:231],v[140:143],a[16:19]
v_cvt_pk_bf16_f32 v175,v190,v191
v_cvt_pk_bf16_f32 v200,v200,v201
v_mfma_f32_16x16x32_bf16 a[20:23],a[236:239],v[140:143],a[20:23]
v_exp_f32 v202,v202
v_mfma_f32_16x16x32_bf16 a[20:23],a[232:235],v[136:139],a[20:23]
v_exp_f32 v203,v203
v_mfma_f32_16x16x32_bf16 a[24:27],a[240:243],v[136:139],a[24:27]
s_waitcnt lgkmcnt(0)
v_exp_f32 v204,v204
v_mfma_f32_16x16x32_bf16 a[24:27],a[244:247],v[140:143],a[24:27]
v_exp_f32 v205,v205
v_mfma_f32_16x16x32_bf16 a[28:31],a[252:255],v[140:143],a[28:31]
buffer_store_dwordx4 v[104:107],v6,s[36:39],s86 offen offset:2048 sc1
v_mfma_f32_16x16x32_bf16 a[28:31],a[248:251],v[136:139],a[28:31]
v_exp_f32 v206,v206
v_smfmac_f32_16x16x64_bf16 v[48:51],v[28:31],v[136:143],v27
v_cvt_pk_bf16_f32 v201,v202,v203
v_mfma_f32_16x16x32_bf16 v[232:235],a[128:131],v[148:151],v[44:47]
v_exp_f32 v207,v207
v_mfma_f32_16x16x32_bf16 v[232:235],a[132:135],v[180:183],v[232:235]
s_barrier
ds_read_b128 a[128:131],v1
v_cvt_pk_bf16_f32 v202,v204,v205
v_mfma_f32_16x16x32_bf16 v[232:235],a[136:139],v[212:215],v[232:235]
v_exp_f32 v216,v216
v_mfma_f32_16x16x32_bf16 v[232:235],a[140:143],v[244:247],v[232:235]
ds_read_b128 a[132:135],v1 offset:256
v_cvt_pk_bf16_f32 v203,v206,v207
v_mfma_f32_16x16x32_bf16 v[236:239],a[156:159],v[244:247],v[44:47]
s_add_u32 m0,s82,0
v_exp_f32 v217,v217
v_mfma_f32_16x16x32_bf16 v[236:239],a[152:155],v[212:215],v[236:239]
ds_read_b128 a[136:139],v1 offset:512
v_cvt_pk_bf16_f32 v204,v216,v217
v_mfma_f32_16x16x32_bf16 v[236:239],a[148:151],v[180:183],v[236:239]
v_exp_f32 v218,v218
v_mfma_f32_16x16x32_bf16 v[236:239],a[144:147],v[148:151],v[236:239]
buffer_load_dwordx4 v7,s[72:75],s80 offen lds
v_mfma_f32_16x16x32_bf16 v[248:251],a[160:163],v[148:151],v[44:47]
v_exp_f32 v219,v219
v_mfma_f32_16x16x32_bf16 v[248:251],a[164:167],v[180:183],v[248:251]
ds_read_b128 a[140:143],v1 offset:768
v_cvt_pk_bf16_f32 v205,v218,v219
v_mfma_f32_16x16x32_bf16 v[248:251],a[168:171],v[212:215],v[248:251]
v_exp_f32 v220,v220
v_mfma_f32_16x16x32_bf16 v[248:251],a[172:175],v[244:247],v[248:251]
ds_read_b128 a[156:159],v1 offset:4864
v_mfma_f32_16x16x32_bf16 v[252:255],a[188:191],v[244:247],v[44:47]
v_exp_f32 v221,v221
v_mfma_f32_16x16x32_bf16 v[252:255],a[184:187],v[212:215],v[252:255]
ds_read_b128 a[152:155],v1 offset:4608
v_cvt_pk_bf16_f32 v206,v220,v221
v_mfma_f32_16x16x32_bf16 v[252:255],a[180:183],v[180:183],v[252:255]
v_exp_f32 v222,v222
v_mfma_f32_16x16x32_bf16 v[252:255],a[176:179],v[148:151],v[252:255]
ds_read_b128 a[148:151],v1 offset:4352
v_mfma_f32_16x16x32_bf16 a[32:35],a[192:195],v[168:171],a[32:35]
v_exp_f32 v223,v223
v_mfma_f32_16x16x32_bf16 a[32:35],a[196:199],v[172:175],a[32:35]
ds_read_b128 a[144:147],v1 offset:4096
v_cvt_pk_bf16_f32 v207,v222,v223
v_mfma_f32_16x16x32_bf16 a[36:39],a[204:207],v[172:175],a[36:39]
buffer_store_dwordx4 v[108:111],v6,s[36:39],s86 offen offset:3072 sc1
v_mfma_f32_16x16x32_bf16 a[36:39],a[200:203],v[168:171],a[36:39]
s_add_u32 s86,s55,73728
s_max_i32 s86,s86,s100
ds_read_b128 a[160:163],v1 offset:8192
v_mfma_f32_16x16x32_bf16 a[40:43],a[208:211],v[168:171],a[40:43]
v_exp_f32 v232,v232
v_mfma_f32_16x16x32_bf16 a[40:43],a[212:215],v[172:175],a[40:43]
ds_read_b128 a[164:167],v1 offset:8448
v_mfma_f32_16x16x32_bf16 a[44:47],a[220:223],v[172:175],a[44:47]
buffer_load_dwordx4 v8,s[72:75],s80 offen offset:1024 lds
v_mfma_f32_16x16x32_bf16 a[44:47],a[216:219],v[168:171],a[44:47]
ds_read_b128 a[168:171],v1 offset:8704
v_mfma_f32_16x16x32_bf16 a[48:51],a[224:227],v[168:171],a[48:51]
v_exp_f32 v233,v233
v_mfma_f32_16x16x32_bf16 a[48:51],a[228:231],v[172:175],a[48:51]
ds_read_b128 a[172:175],v1 offset:8960
v_cvt_pk_bf16_f32 v232,v232,v233
v_mfma_f32_16x16x32_bf16 a[52:55],a[236:239],v[172:175],a[52:55]
v_exp_f32 v234,v234
v_mfma_f32_16x16x32_bf16 a[52:55],a[232:235],v[168:171],a[52:55]
ds_read_b128 a[188:191],v1 offset:13056
v_mfma_f32_16x16x32_bf16 a[56:59],a[240:243],v[168:171],a[56:59]
v_exp_f32 v235,v235
v_mfma_f32_16x16x32_bf16 a[56:59],a[244:247],v[172:175],a[56:59]
buffer_load_dwordx4 v9,s[72:75],s80 offen offset:2048 lds
v_mfma_f32_16x16x32_bf16 a[60:63],a[252:255],v[172:175],a[60:63]
ds_read_b128 a[184:187],v1 offset:12800
v_cvt_pk_bf16_f32 v233,v234,v235
v_mfma_f32_16x16x32_bf16 a[60:63],a[248:251],v[168:171],a[60:63]
v_exp_f32 v236,v236
v_smfmac_f32_16x16x64_bf16 v[52:55],v[28:31],v[168:175],v27
s_waitcnt lgkmcnt(3)
ds_read_b128 a[180:183],v1 offset:12544
v_mfma_f32_16x16x32_bf16 v[136:139],a[128:131],v[128:131],v[32:35]
v_exp_f32 v237,v237
v_mfma_f32_16x16x32_bf16 v[136:139],a[132:135],v[160:163],v[136:139]
ds_read_b128 a[176:179],v1 offset:12288
v_cvt_pk_bf16_f32 v234,v236,v237
v_mfma_f32_16x16x32_bf16 v[136:139],a[136:139],v[192:195],v[136:139]
v_exp_f32 v238,v238
v_mfma_f32_16x16x32_bf16 v[136:139],a[140:143],v[224:227],v[136:139]
v_exp_f32 v239,v239
v_mfma_f32_16x16x32_bf16 v[140:143],a[156:159],v[224:227],v[32:35]
v_cvt_pk_bf16_f32 v235,v238,v239
v_add_u32 v1,v4,v5
v_mfma_f32_16x16x32_bf16 v[140:143],a[152:155],v[192:195],v[140:143]
v_exp_f32 v248,v248
v_mfma_f32_16x16x32_bf16 v[140:143],a[148:151],v[160:163],v[140:143]
v_exp_f32 v249,v249
v_mfma_f32_16x16x32_bf16 v[140:143],a[144:147],v[128:131],v[140:143]
buffer_store_dwordx4 v[112:115],v6,s[36:39],s87 offen sc1
v_mfma_f32_16x16x32_bf16 v[152:155],a[160:163],v[128:131],v[32:35]
v_exp_f32 v250,v250
v_mfma_f32_16x16x32_bf16 v[152:155],a[164:167],v[160:163],v[152:155]
v_exp_f32 v251,v251
v_mfma_f32_16x16x32_bf16 v[152:155],a[168:171],v[192:195],v[152:155]
s_waitcnt lgkmcnt(0)
v_exp_f32 v252,v252
v_mfma_f32_16x16x32_bf16 v[152:155],a[172:175],v[224:227],v[152:155]
buffer_load_dwordx4 v10,s[72:75],s80 offen offset:3072 lds
v_mfma_f32_16x16x32_bf16 v[156:159],a[188:191],v[224:227],v[32:35]
v_exp_f32 v253,v253
v_mfma_f32_16x16x32_bf16 v[156:159],a[184:187],v[192:195],v[156:159]
v_exp_f32 v254,v254
v_mfma_f32_16x16x32_bf16 v[156:159],a[180:183],v[160:163],v[156:159]
v_exp_f32 v255,v255
v_mfma_f32_16x16x32_bf16 v[156:159],a[176:179],v[128:131],v[156:159]
s_add_u32 m0,s82,4096
v_cvt_pk_bf16_f32 v236,v248,v249
v_cvt_pk_bf16_f32 v237,v250,v251
v_mfma_f32_16x16x32_bf16 a[64:67],a[192:195],v[200:203],a[64:67]
v_cvt_pk_bf16_f32 v238,v252,v253
v_cvt_pk_bf16_f32 v239,v254,v255
v_mfma_f32_16x16x32_bf16 a[64:67],a[196:199],v[204:207],a[64:67]
v_exp_f32 v136,v136
v_mfma_f32_16x16x32_bf16 a[68:71],a[204:207],v[204:207],a[68:71]
buffer_load_dwordx4 v11,s[72:75],s80 offen lds
v_mfma_f32_16x16x32_bf16 a[68:71],a[200:203],v[200:203],a[68:71]
v_exp_f32 v137,v137
v_mfma_f32_16x16x32_bf16 a[72:75],a[208:211],v[200:203],a[72:75]
v_exp_f32 v138,v138
v_mfma_f32_16x16x32_bf16 a[72:75],a[212:215],v[204:207],a[72:75]
v_exp_f32 v139,v139
v_mfma_f32_16x16x32_bf16 a[76:79],a[220:223],v[204:207],a[76:79]
v_cvt_pk_bf16_f32 v136,v136,v137
v_cvt_pk_bf16_f32 v137,v138,v139
v_mfma_f32_16x16x32_bf16 a[76:79],a[216:219],v[200:203],a[76:79]
v_exp_f32 v140,v140
v_mfma_f32_16x16x32_bf16 a[80:83],a[224:227],v[200:203],a[80:83]
v_exp_f32 v141,v141
v_mfma_f32_16x16x32_bf16 a[80:83],a[228:231],v[204:207],a[80:83]
v_exp_f32 v142,v142
v_mfma_f32_16x16x32_bf16 a[84:87],a[236:239],v[204:207],a[84:87]
s_add_u32 m0,s82,5120
v_exp_f32 v143,v143
v_mfma_f32_16x16x32_bf16 a[84:87],a[232:235],v[200:203],a[84:87]
v_cvt_pk_bf16_f32 v138,v140,v141
v_cvt_pk_bf16_f32 v139,v142,v143
v_mfma_f32_16x16x32_bf16 a[88:91],a[240:243],v[200:203],a[88:91]
v_exp_f32 v152,v152
v_mfma_f32_16x16x32_bf16 a[88:91],a[244:247],v[204:207],a[88:91]
buffer_store_dwordx4 v[116:119],v6,s[36:39],s87 offen offset:1024 sc1
v_mfma_f32_16x16x32_bf16 a[92:95],a[252:255],v[204:207],a[92:95]
v_exp_f32 v153,v153
v_mfma_f32_16x16x32_bf16 a[92:95],a[248:251],v[200:203],a[92:95]
v_exp_f32 v154,v154
v_smfmac_f32_16x16x64_bf16 v[56:59],v[28:31],v[200:207],v27
v_cvt_pk_bf16_f32 v140,v152,v153
v_mfma_f32_16x16x32_bf16 v[168:171],a[128:131],v[132:135],v[36:39]
buffer_load_dwordx4 v7,s[72:75],s83 offen lds
v_mfma_f32_16x16x32_bf16 v[168:171],a[132:135],v[164:167],v[168:171]
v_exp_f32 v155,v155
v_mfma_f32_16x16x32_bf16 v[168:171],a[136:139],v[196:199],v[168:171]
v_exp_f32 v156,v156
v_mfma_f32_16x16x32_bf16 v[168:171],a[140:143],v[228:231],v[168:171]
v_exp_f32 v157,v157
v_mfma_f32_16x16x32_bf16 v[172:175],a[156:159],v[228:231],v[36:39]
v_exp_f32 v158,v158
v_mfma_f32_16x16x32_bf16 v[172:175],a[152:155],v[196:199],v[172:175]
v_exp_f32 v159,v159
v_mfma_f32_16x16x32_bf16 v[172:175],a[148:151],v[164:167],v[172:175]
v_cvt_pk_bf16_f32 v141,v154,v155
v_cvt_pk_bf16_f32 v142,v156,v157
v_mfma_f32_16x16x32_bf16 v[172:175],a[144:147],v[132:135],v[172:175]
buffer_load_dwordx4 v8,s[72:75],s83 offen offset:1024 lds
v_mfma_f32_16x16x32_bf16 v[184:187],a[160:163],v[132:135],v[36:39]
v_cvt_pk_bf16_f32 v143,v158,v159
v_add_u32 v1,81920,v1
v_mfma_f32_16x16x32_bf16 v[184:187],a[164:167],v[164:167],v[184:187]
v_exp_f32 v168,v168
v_mfma_f32_16x16x32_bf16 v[184:187],a[168:171],v[196:199],v[184:187]
v_exp_f32 v169,v169
v_mfma_f32_16x16x32_bf16 v[184:187],a[172:175],v[228:231],v[184:187]
v_exp_f32 v170,v170
v_mfma_f32_16x16x32_bf16 v[188:191],a[188:191],v[228:231],v[36:39]
v_exp_f32 v171,v171
v_mfma_f32_16x16x32_bf16 v[188:191],a[184:187],v[196:199],v[188:191]
v_exp_f32 v172,v172
v_mfma_f32_16x16x32_bf16 v[188:191],a[180:183],v[164:167],v[188:191]
v_exp_f32 v173,v173
v_mfma_f32_16x16x32_bf16 v[188:191],a[176:179],v[132:135],v[188:191]
v_exp_f32 v174,v174
v_mfma_f32_16x16x32_bf16 a[96:99],a[192:195],v[232:235],a[96:99]
ds_read_b64_tr_b16 a[192:193],v2
ds_read_b64_tr_b16 a[194:195],v2 offset:4096
v_mfma_f32_16x16x32_bf16 a[96:99],a[196:199],v[236:239],a[96:99]
ds_read_b64_tr_b16 a[196:197],v2 offset:8192
v_cvt_pk_bf16_f32 v168,v168,v169
v_mfma_f32_16x16x32_bf16 a[100:103],a[204:207],v[236:239],a[100:103]
buffer_store_dwordx4 v[120:123],v6,s[36:39],s87 offen offset:2048 sc1
v_mfma_f32_16x16x32_bf16 a[100:103],a[200:203],v[232:235],a[100:103]
ds_read_b64_tr_b16 a[198:199],v2 offset:12288
ds_read_b64_tr_b16 a[204:205],v3 offset:8192
v_mfma_f32_16x16x32_bf16 a[104:107],a[208:211],v[232:235],a[104:107]
ds_read_b64_tr_b16 a[206:207],v3 offset:12288
v_cvt_pk_bf16_f32 v169,v170,v171
v_mfma_f32_16x16x32_bf16 a[104:107],a[212:215],v[236:239],a[104:107]
ds_read_b64_tr_b16 a[200:201],v3
v_cvt_pk_bf16_f32 v170,v172,v173
v_mfma_f32_16x16x32_bf16 a[108:111],a[220:223],v[236:239],a[108:111]
buffer_load_dwordx4 v9,s[72:75],s83 offen offset:2048 lds
v_mfma_f32_16x16x32_bf16 a[108:111],a[216:219],v[232:235],a[108:111]
ds_read_b64_tr_b16 a[202:203],v3 offset:4096
ds_read_b64_tr_b16 a[208:209],v2 offset:256
v_mfma_f32_16x16x32_bf16 a[112:115],a[224:227],v[232:235],a[112:115]
v_exp_f32 v175,v175
v_mfma_f32_16x16x32_bf16 a[112:115],a[228:231],v[236:239],a[112:115]
ds_read_b64_tr_b16 a[210:211],v2 offset:4352
ds_read_b64_tr_b16 a[212:213],v2 offset:8448
v_mfma_f32_16x16x32_bf16 a[116:119],a[236:239],v[236:239],a[116:119]
ds_read_b64_tr_b16 a[214:215],v2 offset:12544
v_cvt_pk_bf16_f32 v171,v174,v175
v_mfma_f32_16x16x32_bf16 a[116:119],a[232:235],v[232:235],a[116:119]
s_cmp_ge_u32 0,s71
s_cselect_b32 s80,2,0
ds_read_b64_tr_b16 a[220:221],v3 offset:8448
v_mfma_f32_16x16x32_bf16 a[120:123],a[240:243],v[232:235],a[120:123]
s_mul_i32 s80,s80,163840
v_exp_f32 v184,v184
v_mfma_f32_16x16x32_bf16 a[120:123],a[244:247],v[236:239],a[120:123]
s_add_u32 s80,s80,s81
buffer_load_dwordx4 v10,s[72:75],s83 offen offset:3072 lds
v_mfma_f32_16x16x32_bf16 a[124:127],a[252:255],v[236:239],a[124:127]
ds_read_b64_tr_b16 a[222:223],v3 offset:12544
)A"
R"A(ds_read_b64_tr_b16 a[216:217],v3 offset:256
v_mfma_f32_16x16x32_bf16 a[124:127],a[248:251],v[232:235],a[124:127]
ds_read_b64_tr_b16 a[218:219],v3 offset:4352
v_smfmac_f32_16x16x64_bf16 v[60:63],v[28:31],v[232:239],v27
ds_read_b64_tr_b16 a[224:225],v2 offset:512
v_mfma_f32_16x16x32_bf16 v[200:203],a[128:131],v[144:147],v[40:43]
ds_read_b64_tr_b16 a[226:227],v2 offset:4608
v_mfma_f32_16x16x32_bf16 v[200:203],a[132:135],v[176:179],v[200:203]
ds_read_b64_tr_b16 a[228:229],v2 offset:8704
ds_read_b64_tr_b16 a[230:231],v2 offset:12800
v_mfma_f32_16x16x32_bf16 v[200:203],a[136:139],v[208:211],v[200:203]
ds_read_b64_tr_b16 a[236:237],v3 offset:8704
v_mfma_f32_16x16x32_bf16 v[200:203],a[140:143],v[240:243],v[200:203]
ds_read_b64_tr_b16 a[238:239],v3 offset:12800
v_mfma_f32_16x16x32_bf16 v[204:207],a[156:159],v[240:243],v[40:43]
s_add_u32 m0,s82,9216
ds_read_b64_tr_b16 a[232:233],v3 offset:512
v_mfma_f32_16x16x32_bf16 v[204:207],a[152:155],v[208:211],v[204:207]
ds_read_b64_tr_b16 a[234:235],v3 offset:4608
ds_read_b64_tr_b16 a[240:241],v2 offset:768
v_mfma_f32_16x16x32_bf16 v[204:207],a[148:151],v[176:179],v[204:207]
ds_read_b64_tr_b16 a[242:243],v2 offset:4864
v_mfma_f32_16x16x32_bf16 v[204:207],a[144:147],v[144:147],v[204:207]
buffer_store_dwordx4 v[124:127],v6,s[36:39],s87 offen offset:3072 sc1
v_mfma_f32_16x16x32_bf16 v[216:219],a[160:163],v[144:147],v[40:43]
s_add_u32 s87,s55,77824
ds_read_b64_tr_b16 a[244:245],v2 offset:8960
ds_read_b64_tr_b16 a[246:247],v2 offset:13056
v_mfma_f32_16x16x32_bf16 v[216:219],a[164:167],v[176:179],v[216:219]
s_max_i32 s87,s87,s101
ds_read_b64_tr_b16 a[252:253],v3 offset:8960
v_sub_u32 v2,v4,v5
v_mfma_f32_16x16x32_bf16 v[216:219],a[168:171],v[208:211],v[216:219]
ds_read_b64_tr_b16 a[254:255],v3 offset:13056
v_add_u32 v2,81920,v2
v_mfma_f32_16x16x32_bf16 v[216:219],a[172:175],v[240:243],v[216:219]
buffer_load_dwordx4 v11,s[72:75],s83 offen lds
v_mfma_f32_16x16x32_bf16 v[220:223],a[188:191],v[240:243],v[40:43]
s_add_u32 s83,s80,20480
ds_read_b64_tr_b16 a[248:249],v3 offset:768
ds_read_b64_tr_b16 a[250:251],v3 offset:4864
v_mfma_f32_16x16x32_bf16 v[220:223],a[184:187],v[208:211],v[220:223]
v_exp_f32 v185,v185
v_mfma_f32_16x16x32_bf16 v[220:223],a[180:183],v[176:179],v[220:223]
v_exp_f32 v186,v186
v_mfma_f32_16x16x32_bf16 v[220:223],a[176:179],v[144:147],v[220:223]
s_add_u32 m0,s82,40960
v_exp_f32 v187,v187
s_waitcnt lgkmcnt(6)
v_mfma_f32_16x16x32_bf16 a[0:3],a[192:195],v[136:139],a[0:3]
v_exp_f32 v188,v188
v_mfma_f32_16x16x32_bf16 a[0:3],a[196:199],v[140:143],a[0:3]
v_exp_f32 v189,v189
v_mfma_f32_16x16x32_bf16 a[4:7],a[204:207],v[140:143],a[4:7]
buffer_load_dwordx4 v7,s[72:75],s80 offen lds
v_mfma_f32_16x16x32_bf16 a[4:7],a[200:203],v[136:139],a[4:7]
v_exp_f32 v190,v190
v_mfma_f32_16x16x32_bf16 a[8:11],a[208:211],v[136:139],a[8:11]
v_exp_f32 v191,v191
v_mfma_f32_16x16x32_bf16 a[8:11],a[212:215],v[140:143],a[8:11]
v_cvt_pk_bf16_f32 v172,v184,v185
v_cvt_pk_bf16_f32 v173,v186,v187
v_mfma_f32_16x16x32_bf16 a[12:15],a[220:223],v[140:143],a[12:15]
v_cvt_pk_bf16_f32 v174,v188,v189
v_cvt_pk_bf16_f32 v175,v190,v191
v_mfma_f32_16x16x32_bf16 a[12:15],a[216:219],v[136:139],a[12:15]
v_exp_f32 v200,v200
v_mfma_f32_16x16x32_bf16 a[16:19],a[224:227],v[136:139],a[16:19]
v_exp_f32 v201,v201
v_mfma_f32_16x16x32_bf16 a[16:19],a[228:231],v[140:143],a[16:19]
v_exp_f32 v202,v202
v_mfma_f32_16x16x32_bf16 a[20:23],a[236:239],v[140:143],a[20:23]
v_exp_f32 v203,v203
v_mfma_f32_16x16x32_bf16 a[20:23],a[232:235],v[136:139],a[20:23]
v_exp_f32 v204,v204
v_mfma_f32_16x16x32_bf16 a[24:27],a[240:243],v[136:139],a[24:27]
s_waitcnt lgkmcnt(0)
v_exp_f32 v205,v205
v_mfma_f32_16x16x32_bf16 a[24:27],a[244:247],v[140:143],a[24:27]
buffer_load_dwordx4 v8,s[72:75],s80 offen offset:1024 lds
v_mfma_f32_16x16x32_bf16 a[28:31],a[252:255],v[140:143],a[28:31]
v_exp_f32 v206,v206
v_mfma_f32_16x16x32_bf16 a[28:31],a[248:251],v[136:139],a[28:31]
s_waitcnt vmcnt(18)
v_cvt_pk_bf16_f32 v200,v200,v201
v_cvt_pk_bf16_f32 v201,v202,v203
v_smfmac_f32_16x16x64_bf16 v[48:51],v[28:31],v[136:143],v27
s_barrier
v_exp_f32 v207,v207
v_mfma_f32_16x16x32_bf16 v[232:235],a[128:131],v[148:151],v[44:47]
v_cvt_pk_bf16_f32 v202,v204,v205
v_cvt_pk_bf16_f32 v203,v206,v207
v_mfma_f32_16x16x32_bf16 v[232:235],a[132:135],v[180:183],v[232:235]
v_exp_f32 v216,v216
v_mfma_f32_16x16x32_bf16 v[232:235],a[136:139],v[212:215],v[232:235]
v_exp_f32 v217,v217
v_mfma_f32_16x16x32_bf16 v[232:235],a[140:143],v[244:247],v[232:235]
v_exp_f32 v218,v218
v_mfma_f32_16x16x32_bf16 v[236:239],a[156:159],v[244:247],v[44:47]
v_exp_f32 v219,v219
v_mfma_f32_16x16x32_bf16 v[236:239],a[152:155],v[212:215],v[236:239]
v_exp_f32 v220,v220
v_mfma_f32_16x16x32_bf16 v[236:239],a[148:151],v[180:183],v[236:239]
buffer_load_dwordx4 v9,s[72:75],s80 offen offset:2048 lds
v_mfma_f32_16x16x32_bf16 v[236:239],a[144:147],v[148:151],v[236:239]
v_exp_f32 v221,v221
v_mfma_f32_16x16x32_bf16 v[248:251],a[160:163],v[148:151],v[44:47]
v_exp_f32 v222,v222
v_mfma_f32_16x16x32_bf16 v[248:251],a[164:167],v[180:183],v[248:251]
v_exp_f32 v223,v223
v_mfma_f32_16x16x32_bf16 v[248:251],a[168:171],v[212:215],v[248:251]
v_cvt_pk_bf16_f32 v204,v216,v217
v_cvt_pk_bf16_f32 v205,v218,v219
v_mfma_f32_16x16x32_bf16 v[248:251],a[172:175],v[244:247],v[248:251]
v_cvt_pk_bf16_f32 v206,v220,v221
v_cvt_pk_bf16_f32 v207,v222,v223
v_mfma_f32_16x16x32_bf16 v[252:255],a[188:191],v[244:247],v[44:47]
v_exp_f32 v232,v232
v_mfma_f32_16x16x32_bf16 v[252:255],a[184:187],v[212:215],v[252:255]
v_exp_f32 v233,v233
v_mfma_f32_16x16x32_bf16 v[252:255],a[180:183],v[180:183],v[252:255]
v_exp_f32 v234,v234
v_mfma_f32_16x16x32_bf16 v[252:255],a[176:179],v[148:151],v[252:255]
v_exp_f32 v235,v235
v_mfma_f32_16x16x32_bf16 a[32:35],a[192:195],v[168:171],a[32:35]
v_cvt_pk_bf16_f32 v232,v232,v233
v_add_f32 v48,v49,v48
v_mfma_f32_16x16x32_bf16 a[32:35],a[196:199],v[172:175],a[32:35]
buffer_load_dwordx4 v10,s[72:75],s80 offen offset:3072 lds
v_mfma_f32_16x16x32_bf16 a[36:39],a[204:207],v[172:175],a[36:39]
v_exp_f32 v236,v236
v_mfma_f32_16x16x32_bf16 a[36:39],a[200:203],v[168:171],a[36:39]
v_exp_f32 v237,v237
v_mfma_f32_16x16x32_bf16 a[40:43],a[208:211],v[168:171],a[40:43]
v_exp_f32 v238,v238
v_mfma_f32_16x16x32_bf16 a[40:43],a[212:215],v[172:175],a[40:43]
v_exp_f32 v239,v239
v_mfma_f32_16x16x32_bf16 a[44:47],a[220:223],v[172:175],a[44:47]
v_cvt_pk_bf16_f32 v233,v234,v235
v_max_f32 v20,0x00800000,v48
v_mfma_f32_16x16x32_bf16 a[44:47],a[216:219],v[168:171],a[44:47]
s_lshl_b32 s95,s42,2
s_add_u32 s95,s95,s40
s_lshl_b32 s95,s95,4
v_mfma_f32_16x16x32_bf16 a[48:51],a[224:227],v[168:171],a[48:51]
v_mov_b32 v18,s68
v_mov_b32 v19,s69
v_mfma_f32_16x16x32_bf16 a[48:51],a[228:231],v[172:175],a[48:51]
s_add_u32 m0,s82,45056
v_cvt_pk_bf16_f32 v234,v236,v237
v_cvt_pk_bf16_f32 v235,v238,v239
v_mfma_f32_16x16x32_bf16 a[52:55],a[236:239],v[172:175],a[52:55]
v_exp_f32 v248,v248
v_mfma_f32_16x16x32_bf16 a[52:55],a[232:235],v[168:171],a[52:55]
v_exp_f32 v249,v249
v_mfma_f32_16x16x32_bf16 a[56:59],a[240:243],v[168:171],a[56:59]
buffer_load_dwordx4 v11,s[72:75],s80 offen lds
v_mfma_f32_16x16x32_bf16 a[56:59],a[244:247],v[172:175],a[56:59]
v_exp_f32 v250,v250
v_mfma_f32_16x16x32_bf16 a[60:63],a[252:255],v[172:175],a[60:63]
v_exp_f32 v251,v251
v_mfma_f32_16x16x32_bf16 a[60:63],a[248:251],v[168:171],a[60:63]
v_exp_f32 v252,v252
v_smfmac_f32_16x16x64_bf16 v[52:55],v[28:31],v[168:175],v27
s_waitcnt vmcnt(6)
s_mov_b64 exec,1
buffer_store_dwordx2 v[18:19],v6,s[64:67],s95 offen sc1
s_mov_b64 exec,-1
v_mfma_f32_16x16x32_bf16 a[64:67],a[192:195],v[200:203],a[64:67]
v_exp_f32 v253,v253
v_mfma_f32_16x16x32_bf16 a[64:67],a[196:199],v[204:207],a[64:67]
v_exp_f32 v254,v254
v_mfma_f32_16x16x32_bf16 a[68:71],a[204:207],v[204:207],a[68:71]
v_exp_f32 v255,v255
v_mfma_f32_16x16x32_bf16 a[68:71],a[200:203],v[200:203],a[68:71]
s_add_u32 m0,s82,46080
v_rcp_f32 v20,v20
v_mfma_f32_16x16x32_bf16 a[72:75],a[208:211],v[200:203],a[72:75]
v_cvt_pk_bf16_f32 v236,v248,v249
v_cvt_pk_bf16_f32 v237,v250,v251
v_mfma_f32_16x16x32_bf16 a[72:75],a[212:215],v[204:207],a[72:75]
v_cvt_pk_bf16_f32 v238,v252,v253
v_cvt_pk_bf16_f32 v239,v254,v255
v_mfma_f32_16x16x32_bf16 a[76:79],a[220:223],v[204:207],a[76:79]
buffer_load_dwordx4 v7,s[72:75],s83 offen lds
v_mfma_f32_16x16x32_bf16 a[76:79],a[216:219],v[200:203],a[76:79]
v_accvgpr_read_b32 v136,a0
v_accvgpr_read_b32 v137,a1
v_accvgpr_read_b32 v138,a2
v_accvgpr_read_b32 v139,a3
v_accvgpr_read_b32 v12,a4
v_accvgpr_read_b32 v13,a5
v_accvgpr_read_b32 v14,a6
v_accvgpr_read_b32 v15,a7
v_pk_mul_f32 v[136:137],v[20:21],v[136:137] op_sel:[0,0] op_sel_hi:[0,1]
v_pk_mul_f32 v[138:139],v[20:21],v[138:139] op_sel:[0,0] op_sel_hi:[0,1]
v_pk_mul_f32 v[12:13],v[20:21],v[12:13] op_sel:[0,0] op_sel_hi:[0,1]
v_pk_mul_f32 v[14:15],v[20:21],v[14:15] op_sel:[0,0] op_sel_hi:[0,1]
v_cvt_pk_bf16_f32 v136,v136,v137
v_cvt_pk_bf16_f32 v137,v138,v139
v_cvt_pk_bf16_f32 v138,v12,v13
v_cvt_pk_bf16_f32 v139,v14,v15
v_mfma_f32_16x16x32_bf16 a[80:83],a[224:227],v[200:203],a[80:83]
ds_read_b64_tr_b16 a[128:129],v1
ds_read_b64_tr_b16 a[130:131],v1 offset:5120
v_mfma_f32_16x16x32_bf16 a[80:83],a[228:231],v[204:207],a[80:83]
ds_read_b64_tr_b16 a[132:133],v1 offset:10240
v_accvgpr_read_b32 v152,a16
v_mfma_f32_16x16x32_bf16 a[84:87],a[236:239],v[204:207],a[84:87]
ds_read_b64_tr_b16 a[134:135],v1 offset:15360
v_accvgpr_read_b32 v153,a17
v_mfma_f32_16x16x32_bf16 a[84:87],a[232:235],v[200:203],a[84:87]
ds_read_b64_tr_b16 a[136:137],v1 offset:20480
v_mul_f32 v152,v20,v152
v_mfma_f32_16x16x32_bf16 a[88:91],a[240:243],v[200:203],a[88:91]
ds_read_b64_tr_b16 a[138:139],v1 offset:25600
ds_read_b64_tr_b16 a[140:141],v1 offset:30720
v_mfma_f32_16x16x32_bf16 a[88:91],a[244:247],v[204:207],a[88:91]
v_accvgpr_read_b32 v140,a8
v_accvgpr_read_b32 v141,a9
v_accvgpr_read_b32 v142,a10
v_accvgpr_read_b32 v143,a11
v_accvgpr_read_b32 v12,a12
v_accvgpr_read_b32 v13,a13
v_accvgpr_read_b32 v14,a14
v_accvgpr_read_b32 v15,a15
v_pk_mul_f32 v[140:141],v[20:21],v[140:141] op_sel:[0,0] op_sel_hi:[0,1]
v_pk_mul_f32 v[142:143],v[20:21],v[142:143] op_sel:[0,0] op_sel_hi:[0,1]
v_pk_mul_f32 v[12:13],v[20:21],v[12:13] op_sel:[0,0] op_sel_hi:[0,1]
v_pk_mul_f32 v[14:15],v[20:21],v[14:15] op_sel:[0,0] op_sel_hi:[0,1]
v_cvt_pk_bf16_f32 v140,v140,v141
v_cvt_pk_bf16_f32 v141,v142,v143
v_cvt_pk_bf16_f32 v142,v12,v13
v_cvt_pk_bf16_f32 v143,v14,v15
v_mfma_f32_16x16x32_bf16 a[92:95],a[252:255],v[204:207],a[92:95]
ds_read_b64_tr_b16 a[142:143],v1 offset:35840
v_mul_f32 v153,v20,v153
v_mfma_f32_16x16x32_bf16 a[92:95],a[248:251],v[200:203],a[92:95]
v_accvgpr_read_b32 v154,a18
v_accvgpr_read_b32 v155,a19
v_smfmac_f32_16x16x64_bf16 v[56:59],v[28:31],v[200:207],v27
v_cvt_pk_bf16_f32 v152,v152,v153
v_mfma_f32_16x16x32_bf16 a[96:99],a[192:195],v[232:235],a[96:99]
buffer_store_dwordx4 v[136:139],v6,s[36:39],s84 offen sc1
v_mfma_f32_16x16x32_bf16 a[96:99],a[196:199],v[236:239],a[96:99]
v_mul_f32 v154,v20,v154
v_mul_f32 v155,v20,v155
v_mfma_f32_16x16x32_bf16 a[100:103],a[204:207],v[236:239],a[100:103]
v_cvt_pk_bf16_f32 v153,v154,v155
v_accvgpr_read_b32 v154,a20
v_mfma_f32_16x16x32_bf16 a[100:103],a[200:203],v[232:235],a[100:103]
v_accvgpr_read_b32 v155,a21
v_accvgpr_read_b32 v12,a22
v_mfma_f32_16x16x32_bf16 a[104:107],a[208:211],v[232:235],a[104:107]
buffer_load_dwordx4 v8,s[72:75],s83 offen offset:1024 lds
v_mfma_f32_16x16x32_bf16 a[104:107],a[212:215],v[236:239],a[104:107]
v_accvgpr_read_b32 v13,a23
v_mul_f32 v154,v20,v154
v_mfma_f32_16x16x32_bf16 a[108:111],a[220:223],v[236:239],a[108:111]
v_mul_f32 v155,v20,v155
v_mul_f32 v12,v20,v12
v_mfma_f32_16x16x32_bf16 a[108:111],a[216:219],v[232:235],a[108:111]
v_mul_f32 v13,v20,v13
v_cvt_pk_bf16_f32 v154,v154,v155
v_mfma_f32_16x16x32_bf16 a[112:115],a[224:227],v[232:235],a[112:115]
ds_read_b64_tr_b16 a[144:145],v2 offset:32
v_cvt_pk_bf16_f32 v155,v12,v13
v_mfma_f32_16x16x32_bf16 a[112:115],a[228:231],v[236:239],a[112:115]
ds_read_b64_tr_b16 a[146:147],v2 offset:5152
ds_read_b64_tr_b16 a[148:149],v2 offset:10272
v_mfma_f32_16x16x32_bf16 a[116:119],a[236:239],v[236:239],a[116:119]
ds_read_b64_tr_b16 a[150:151],v2 offset:15392
)A"
R"A(v_accvgpr_read_b32 v156,a24
v_mfma_f32_16x16x32_bf16 a[116:119],a[232:235],v[232:235],a[116:119]
buffer_load_dwordx4 v9,s[72:75],s83 offen offset:2048 lds
v_mfma_f32_16x16x32_bf16 a[120:123],a[240:243],v[232:235],a[120:123]
ds_read_b64_tr_b16 a[152:153],v2 offset:20512
ds_read_b64_tr_b16 a[154:155],v2 offset:25632
v_mfma_f32_16x16x32_bf16 a[120:123],a[244:247],v[236:239],a[120:123]
ds_read_b64_tr_b16 a[156:157],v2 offset:30752
v_accvgpr_read_b32 v157,a25
v_mfma_f32_16x16x32_bf16 a[124:127],a[252:255],v[236:239],a[124:127]
ds_read_b64_tr_b16 a[158:159],v2 offset:35872
v_mul_f32 v156,v20,v156
v_mfma_f32_16x16x32_bf16 a[124:127],a[248:251],v[232:235],a[124:127]
v_mul_f32 v157,v20,v157
v_accvgpr_read_b32 v158,a26
v_smfmac_f32_16x16x64_bf16 v[60:63],v[28:31],v[232:239],v27
s_waitcnt lgkmcnt(7)
v_accvgpr_read_b32 v159,a27
v_mfma_f32_16x16x32_bf16 a[192:195],a[128:131],v[64:67],0
v_cvt_pk_bf16_f32 v156,v156,v157
v_mul_f32 v158,v20,v158
v_mfma_f32_16x16x32_bf16 a[192:195],a[132:135],v[68:71],a[192:195]
v_mul_f32 v159,v20,v159
v_cvt_pk_bf16_f32 v157,v158,v159
v_mfma_f32_16x16x32_bf16 a[192:195],a[136:139],v[72:75],a[192:195]
buffer_store_dwordx4 v[140:143],v6,s[36:39],s84 offen offset:1024 sc1
v_mfma_f32_16x16x32_bf16 a[192:195],a[140:143],v[76:79],a[192:195]
v_accvgpr_read_b32 v158,a28
v_accvgpr_read_b32 v159,a29
v_mfma_f32_16x16x32_bf16 a[196:199],a[140:143],v[92:95],0
v_accvgpr_read_b32 v14,a30
v_accvgpr_read_b32 v15,a31
v_mfma_f32_16x16x32_bf16 a[196:199],a[136:139],v[88:91],a[196:199]
v_mul_f32 v158,v20,v158
v_mul_f32 v159,v20,v159
v_mfma_f32_16x16x32_bf16 a[196:199],a[132:135],v[84:87],a[196:199]
buffer_load_dwordx4 v10,s[72:75],s83 offen offset:3072 lds
v_mfma_f32_16x16x32_bf16 a[196:199],a[128:131],v[80:83],a[196:199]
v_mul_f32 v14,v20,v14
v_mul_f32 v15,v20,v15
v_mfma_f32_16x16x32_bf16 a[200:203],a[128:131],v[96:99],0
v_cvt_pk_bf16_f32 v158,v158,v159
v_cvt_pk_bf16_f32 v159,v14,v15
v_mfma_f32_16x16x32_bf16 a[200:203],a[132:135],v[100:103],a[200:203]
ds_read_b64_tr_b16 a[160:161],v1 offset:64
ds_read_b64_tr_b16 a[162:163],v1 offset:5184
v_mfma_f32_16x16x32_bf16 a[200:203],a[136:139],v[104:107],a[200:203]
ds_read_b64_tr_b16 a[164:165],v1 offset:10304
v_add_f32 v52,v53,v52
v_mfma_f32_16x16x32_bf16 a[200:203],a[140:143],v[108:111],a[200:203]
ds_read_b64_tr_b16 a[166:167],v1 offset:15424
v_max_f32 v21,0x00800000,v52
v_mfma_f32_16x16x32_bf16 a[204:207],a[140:143],v[124:127],0
s_cmp_ge_u32 1,s71
s_cselect_b32 s80,4,2
ds_read_b64_tr_b16 a[168:169],v1 offset:20544
v_mfma_f32_16x16x32_bf16 a[204:207],a[136:139],v[120:123],a[204:207]
s_add_u32 m0,s82,50176
ds_read_b64_tr_b16 a[170:171],v1 offset:25664
ds_read_b64_tr_b16 a[172:173],v1 offset:30784
v_mfma_f32_16x16x32_bf16 a[204:207],a[132:135],v[116:119],a[204:207]
s_mul_i32 s80,s80,163840
s_add_u32 s80,s80,s81
ds_read_b64_tr_b16 a[174:175],v1 offset:35904
v_mfma_f32_16x16x32_bf16 a[204:207],a[128:131],v[112:115],a[204:207]
s_waitcnt lgkmcnt(8)
v_rcp_f32 v21,v21
v_mfma_f32_16x16x32_bf16 a[208:211],a[144:147],v[64:67],0
buffer_load_dwordx4 v11,s[72:75],s83 offen lds
v_mfma_f32_16x16x32_bf16 a[208:211],a[148:151],v[68:71],a[208:211]
v_accvgpr_read_b32 v168,a32
v_accvgpr_read_b32 v169,a33
v_accvgpr_read_b32 v170,a34
v_accvgpr_read_b32 v171,a35
v_accvgpr_read_b32 v12,a36
v_accvgpr_read_b32 v13,a37
v_accvgpr_read_b32 v14,a38
v_accvgpr_read_b32 v15,a39
v_pk_mul_f32 v[168:169],v[20:21],v[168:169] op_sel:[1,0] op_sel_hi:[1,1]
v_pk_mul_f32 v[170:171],v[20:21],v[170:171] op_sel:[1,0] op_sel_hi:[1,1]
v_pk_mul_f32 v[12:13],v[20:21],v[12:13] op_sel:[1,0] op_sel_hi:[1,1]
v_pk_mul_f32 v[14:15],v[20:21],v[14:15] op_sel:[1,0] op_sel_hi:[1,1]
v_cvt_pk_bf16_f32 v168,v168,v169
v_cvt_pk_bf16_f32 v169,v170,v171
v_cvt_pk_bf16_f32 v170,v12,v13
v_cvt_pk_bf16_f32 v171,v14,v15
v_mfma_f32_16x16x32_bf16 a[208:211],a[152:155],v[72:75],a[208:211]
s_add_u32 s83,s80,20480
ds_read_b64_tr_b16 a[176:177],v2 offset:96
ds_read_b64_tr_b16 a[178:179],v2 offset:5216
v_mfma_f32_16x16x32_bf16 a[208:211],a[156:159],v[76:79],a[208:211]
ds_read_b64_tr_b16 a[180:181],v2 offset:10336
v_accvgpr_read_b32 v184,a48
v_mfma_f32_16x16x32_bf16 a[212:215],a[156:159],v[92:95],0
ds_read_b64_tr_b16 a[182:183],v2 offset:15456
v_accvgpr_read_b32 v185,a49
v_mfma_f32_16x16x32_bf16 a[212:215],a[152:155],v[88:91],a[212:215]
ds_read_b64_tr_b16 a[184:185],v2 offset:20576
v_mul_f32 v184,v21,v184
v_mfma_f32_16x16x32_bf16 a[212:215],a[148:151],v[84:87],a[212:215]
buffer_store_dwordx4 v[152:155],v6,s[36:39],s84 offen offset:2048 sc1
v_mfma_f32_16x16x32_bf16 a[212:215],a[144:147],v[80:83],a[212:215]
ds_read_b64_tr_b16 a[186:187],v2 offset:25696
ds_read_b64_tr_b16 a[188:189],v2 offset:30816
v_mfma_f32_16x16x32_bf16 a[216:219],a[144:147],v[96:99],0
ds_read_b64_tr_b16 a[190:191],v2 offset:35936
v_mul_f32 v185,v21,v185
v_mfma_f32_16x16x32_bf16 a[216:219],a[148:151],v[100:103],a[216:219]
v_accvgpr_read_b32 v172,a40
v_accvgpr_read_b32 v173,a41
v_accvgpr_read_b32 v174,a42
v_accvgpr_read_b32 v175,a43
v_accvgpr_read_b32 v12,a44
v_accvgpr_read_b32 v13,a45
v_accvgpr_read_b32 v14,a46
v_accvgpr_read_b32 v15,a47
v_pk_mul_f32 v[172:173],v[20:21],v[172:173] op_sel:[1,0] op_sel_hi:[1,1]
v_pk_mul_f32 v[174:175],v[20:21],v[174:175] op_sel:[1,0] op_sel_hi:[1,1]
v_pk_mul_f32 v[12:13],v[20:21],v[12:13] op_sel:[1,0] op_sel_hi:[1,1]
v_pk_mul_f32 v[14:15],v[20:21],v[14:15] op_sel:[1,0] op_sel_hi:[1,1]
v_cvt_pk_bf16_f32 v172,v172,v173
v_cvt_pk_bf16_f32 v173,v174,v175
v_cvt_pk_bf16_f32 v174,v12,v13
v_cvt_pk_bf16_f32 v175,v14,v15
v_mfma_f32_16x16x32_bf16 a[216:219],a[152:155],v[104:107],a[216:219]
v_accvgpr_read_b32 v186,a50
v_accvgpr_read_b32 v187,a51
v_mfma_f32_16x16x32_bf16 a[216:219],a[156:159],v[108:111],a[216:219]
v_cvt_pk_bf16_f32 v184,v184,v185
v_mul_f32 v186,v21,v186
v_mfma_f32_16x16x32_bf16 a[220:223],a[156:159],v[124:127],0
v_mul_f32 v187,v21,v187
v_cvt_pk_bf16_f32 v185,v186,v187
v_mfma_f32_16x16x32_bf16 a[220:223],a[152:155],v[120:123],a[220:223]
v_accvgpr_read_b32 v186,a52
v_accvgpr_read_b32 v187,a53
v_mfma_f32_16x16x32_bf16 a[220:223],a[148:151],v[116:119],a[220:223]
s_add_u32 m0,s82,122880
v_accvgpr_read_b32 v16,a54
v_accvgpr_read_b32 v17,a55
v_mfma_f32_16x16x32_bf16 a[220:223],a[144:147],v[112:115],a[220:223]
s_waitcnt lgkmcnt(1)
v_mul_f32 v186,v21,v186
v_mul_f32 v187,v21,v187
v_mfma_f32_16x16x32_bf16 a[224:227],a[160:163],v[64:67],0
v_mul_f32 v16,v21,v16
v_mul_f32 v17,v21,v17
v_mfma_f32_16x16x32_bf16 a[224:227],a[164:167],v[68:71],a[224:227]
buffer_load_dwordx4 v7,s[72:75],s80 offen lds
v_mfma_f32_16x16x32_bf16 a[224:227],a[168:171],v[72:75],a[224:227]
v_cvt_pk_bf16_f32 v186,v186,v187
v_cvt_pk_bf16_f32 v187,v16,v17
v_mfma_f32_16x16x32_bf16 a[224:227],a[172:175],v[76:79],a[224:227]
ds_read_b64_tr_b16 a[128:129],v1 offset:128
ds_read_b64_tr_b16 a[130:131],v1 offset:5248
v_mfma_f32_16x16x32_bf16 a[228:231],a[172:175],v[92:95],0
ds_read_b64_tr_b16 a[132:133],v1 offset:10368
v_accvgpr_read_b32 v188,a56
v_mfma_f32_16x16x32_bf16 a[228:231],a[168:171],v[88:91],a[228:231]
ds_read_b64_tr_b16 a[134:135],v1 offset:15488
v_accvgpr_read_b32 v189,a57
v_mfma_f32_16x16x32_bf16 a[228:231],a[164:167],v[84:87],a[228:231]
ds_read_b64_tr_b16 a[136:137],v1 offset:20608
v_mul_f32 v188,v21,v188
v_mfma_f32_16x16x32_bf16 a[228:231],a[160:163],v[80:83],a[228:231]
ds_read_b64_tr_b16 a[138:139],v1 offset:25728
ds_read_b64_tr_b16 a[140:141],v1 offset:30848
v_mfma_f32_16x16x32_bf16 a[232:235],a[160:163],v[96:99],0
ds_read_b64_tr_b16 a[142:143],v1 offset:35968
v_mul_f32 v189,v21,v189
v_mfma_f32_16x16x32_bf16 a[232:235],a[164:167],v[100:103],a[232:235]
v_accvgpr_read_b32 v190,a58
v_accvgpr_read_b32 v191,a59
v_mfma_f32_16x16x32_bf16 a[232:235],a[168:171],v[104:107],a[232:235]
buffer_store_dwordx4 v[156:159],v6,s[36:39],s84 offen offset:3072 sc1
v_mfma_f32_16x16x32_bf16 a[232:235],a[172:175],v[108:111],a[232:235]
v_cvt_pk_bf16_f32 v188,v188,v189
v_mul_f32 v190,v21,v190
v_mfma_f32_16x16x32_bf16 a[236:239],a[172:175],v[124:127],0
v_mul_f32 v191,v21,v191
v_cvt_pk_bf16_f32 v189,v190,v191
v_mfma_f32_16x16x32_bf16 a[236:239],a[168:171],v[120:123],a[236:239]
v_accvgpr_read_b32 v190,a60
v_accvgpr_read_b32 v191,a61
v_mfma_f32_16x16x32_bf16 a[236:239],a[164:167],v[116:119],a[236:239]
buffer_load_dwordx4 v8,s[72:75],s80 offen offset:1024 lds
v_mfma_f32_16x16x32_bf16 a[236:239],a[160:163],v[112:115],a[236:239]
v_accvgpr_read_b32 v12,a62
v_accvgpr_read_b32 v13,a63
v_mfma_f32_16x16x32_bf16 a[240:243],a[176:179],v[64:67],0
v_mul_f32 v190,v21,v190
v_mul_f32 v191,v21,v191
v_mfma_f32_16x16x32_bf16 a[240:243],a[180:183],v[68:71],a[240:243]
v_mul_f32 v12,v21,v12
v_mul_f32 v13,v21,v13
v_mfma_f32_16x16x32_bf16 a[240:243],a[184:187],v[72:75],a[240:243]
s_waitcnt lgkmcnt(0)
v_cvt_pk_bf16_f32 v190,v190,v191
v_cvt_pk_bf16_f32 v191,v12,v13
v_mfma_f32_16x16x32_bf16 a[240:243],a[188:191],v[76:79],a[240:243]
ds_read_b64_tr_b16 a[144:145],v2 offset:160
ds_read_b64_tr_b16 a[146:147],v2 offset:5280
v_mfma_f32_16x16x32_bf16 a[244:247],a[188:191],v[92:95],0
ds_read_b64_tr_b16 a[148:149],v2 offset:10400
v_add_f32 v56,v57,v56
v_mfma_f32_16x16x32_bf16 a[244:247],a[184:187],v[88:91],a[244:247]
ds_read_b64_tr_b16 a[150:151],v2 offset:15520
v_max_f32 v22,0x00800000,v56
v_mfma_f32_16x16x32_bf16 a[244:247],a[180:183],v[84:87],a[244:247]
ds_read_b64_tr_b16 a[152:153],v2 offset:20640
v_accvgpr_read_b32 v200,a64
v_mfma_f32_16x16x32_bf16 a[244:247],a[176:179],v[80:83],a[244:247]
ds_read_b64_tr_b16 a[154:155],v2 offset:25760
ds_read_b64_tr_b16 a[156:157],v2 offset:30880
v_mfma_f32_16x16x32_bf16 a[248:251],a[176:179],v[96:99],0
v_rcp_f32 v22,v22
v_mfma_f32_16x16x32_bf16 a[248:251],a[180:183],v[100:103],a[248:251]
buffer_load_dwordx4 v9,s[72:75],s80 offen offset:2048 lds
v_mfma_f32_16x16x32_bf16 a[248:251],a[184:187],v[104:107],a[248:251]
ds_read_b64_tr_b16 a[158:159],v2 offset:36000
v_accvgpr_read_b32 v201,a65
v_mfma_f32_16x16x32_bf16 a[248:251],a[188:191],v[108:111],a[248:251]
v_mul_f32 v200,v22,v200
v_mul_f32 v201,v22,v201
v_mfma_f32_16x16x32_bf16 a[252:255],a[188:191],v[124:127],0
v_accvgpr_read_b32 v202,a66
v_accvgpr_read_b32 v203,a67
v_mfma_f32_16x16x32_bf16 a[252:255],a[184:187],v[120:123],a[252:255]
v_cvt_pk_bf16_f32 v200,v200,v201
v_mul_f32 v202,v22,v202
v_mfma_f32_16x16x32_bf16 a[252:255],a[180:183],v[116:119],a[252:255]
s_waitcnt vmcnt(18)
v_mul_f32 v203,v22,v203
v_cvt_pk_bf16_f32 v201,v202,v203
v_mfma_f32_16x16x32_bf16 a[252:255],a[176:179],v[112:115],a[252:255]
s_barrier
buffer_store_dwordx4 v[168:171],v6,s[36:39],s85 offen sc1
v_mfma_f32_16x16x32_bf16 a[0:3],a[128:131],v[64:67],0
v_accvgpr_read_b32 v202,a68
v_accvgpr_read_b32 v203,a69
v_mfma_f32_16x16x32_bf16 a[0:3],a[132:135],v[68:71],a[0:3]
v_accvgpr_read_b32 v14,a70
v_accvgpr_read_b32 v15,a71
v_mfma_f32_16x16x32_bf16 a[0:3],a[136:139],v[72:75],a[0:3]
v_mul_f32 v202,v22,v202
v_mul_f32 v203,v22,v203
v_mfma_f32_16x16x32_bf16 a[0:3],a[140:143],v[76:79],a[0:3]
v_mul_f32 v14,v22,v14
v_mul_f32 v15,v22,v15
v_mfma_f32_16x16x32_bf16 a[4:7],a[140:143],v[92:95],0
buffer_load_dwordx4 v10,s[72:75],s80 offen offset:3072 lds
v_mfma_f32_16x16x32_bf16 a[4:7],a[136:139],v[88:91],a[4:7]
s_lshl_b32 s94,s70,4
v_cvt_pk_bf16_f32 v202,v202,v203
v_cvt_pk_bf16_f32 v203,v14,v15
v_mfma_f32_16x16x32_bf16 a[4:7],a[132:135],v[84:87],a[4:7]
s_add_u32 s94,s94,s40
v_accvgpr_read_b32 v204,a72
v_accvgpr_read_b32 v205,a73
v_mfma_f32_16x16x32_bf16 a[4:7],a[128:131],v[80:83],a[4:7]
s_lshl_b32 s94,s94,4
v_mul_f32 v204,v22,v204
v_mul_f32 v205,v22,v205
v_mfma_f32_16x16x32_bf16 a[8:11],a[128:131],v[96:99],0
v_accvgpr_read_b32 v206,a74
v_accvgpr_read_b32 v207,a75
v_mfma_f32_16x16x32_bf16 a[8:11],a[132:135],v[100:103],a[8:11]
ds_read_b64_tr_b16 a[160:161],v1 offset:192
ds_read_b64_tr_b16 a[162:163],v1 offset:5312
v_mfma_f32_16x16x32_bf16 a[8:11],a[136:139],v[104:107],a[8:11]
v_cvt_pk_bf16_f32 v204,v204,v205
v_mul_f32 v206,v22,v206
v_mfma_f32_16x16x32_bf16 a[8:11],a[140:143],v[108:111],a[8:11]
v_mul_f32 v207,v22,v207
v_cvt_pk_bf16_f32 v205,v206,v207
v_mfma_f32_16x16x32_bf16 a[12:15],a[140:143],v[124:127],0
ds_read_b64_tr_b16 a[164:165],v1 offset:10432
ds_read_b64_tr_b16 a[166:167],v1 offset:15552
)A"
R"A(v_mfma_f32_16x16x32_bf16 a[12:15],a[136:139],v[120:123],a[12:15]
s_add_u32 m0,s82,126976
v_accvgpr_read_b32 v206,a76
v_accvgpr_read_b32 v207,a77
v_mfma_f32_16x16x32_bf16 a[12:15],a[132:135],v[116:119],a[12:15]
v_accvgpr_read_b32 v16,a78
v_accvgpr_read_b32 v17,a79
v_mfma_f32_16x16x32_bf16 a[12:15],a[128:131],v[112:115],a[12:15]
s_waitcnt lgkmcnt(4)
v_mul_f32 v206,v22,v206
v_mul_f32 v207,v22,v207
v_mfma_f32_16x16x32_bf16 a[16:19],a[144:147],v[64:67],0
buffer_load_dwordx4 v11,s[72:75],s80 offen lds
v_mfma_f32_16x16x32_bf16 a[16:19],a[148:151],v[68:71],a[16:19]
ds_read_b64_tr_b16 a[168:169],v1 offset:20672
ds_read_b64_tr_b16 a[170:171],v1 offset:25792
v_mfma_f32_16x16x32_bf16 a[16:19],a[152:155],v[72:75],a[16:19]
v_mul_f32 v16,v22,v16
v_mul_f32 v17,v22,v17
v_mfma_f32_16x16x32_bf16 a[16:19],a[156:159],v[76:79],a[16:19]
v_cvt_pk_bf16_f32 v206,v206,v207
v_cvt_pk_bf16_f32 v207,v16,v17
v_mfma_f32_16x16x32_bf16 a[20:23],a[156:159],v[92:95],0
buffer_store_dwordx4 v[172:175],v6,s[36:39],s85 offen offset:1024 sc1
v_mfma_f32_16x16x32_bf16 a[20:23],a[152:155],v[88:91],a[20:23]
ds_read_b64_tr_b16 a[172:173],v1 offset:30912
ds_read_b64_tr_b16 a[174:175],v1 offset:36032
v_mfma_f32_16x16x32_bf16 a[20:23],a[148:151],v[84:87],a[20:23]
v_accvgpr_read_b32 v216,a80
v_accvgpr_read_b32 v217,a81
v_mfma_f32_16x16x32_bf16 a[20:23],a[144:147],v[80:83],a[20:23]
v_mul_f32 v216,v22,v216
v_mul_f32 v217,v22,v217
v_mfma_f32_16x16x32_bf16 a[24:27],a[144:147],v[96:99],0
v_accvgpr_read_b32 v218,a82
v_accvgpr_read_b32 v219,a83
v_mfma_f32_16x16x32_bf16 a[24:27],a[148:151],v[100:103],a[24:27]
s_add_u32 m0,s82,128000
v_cvt_pk_bf16_f32 v216,v216,v217
v_mul_f32 v218,v22,v218
v_mfma_f32_16x16x32_bf16 a[24:27],a[152:155],v[104:107],a[24:27]
v_mul_f32 v219,v22,v219
v_cvt_pk_bf16_f32 v217,v218,v219
v_mfma_f32_16x16x32_bf16 a[24:27],a[156:159],v[108:111],a[24:27]
v_accvgpr_read_b32 v218,a84
v_accvgpr_read_b32 v219,a85
v_mfma_f32_16x16x32_bf16 a[28:31],a[156:159],v[124:127],0
buffer_load_dwordx4 v7,s[72:75],s83 offen lds
v_mfma_f32_16x16x32_bf16 a[28:31],a[152:155],v[120:123],a[28:31]
v_accvgpr_read_b32 v12,a86
v_accvgpr_read_b32 v13,a87
v_mfma_f32_16x16x32_bf16 a[28:31],a[148:151],v[116:119],a[28:31]
v_mul_f32 v218,v22,v218
v_mul_f32 v219,v22,v219
v_mfma_f32_16x16x32_bf16 a[28:31],a[144:147],v[112:115],a[28:31]
s_waitcnt lgkmcnt(0)
v_mul_f32 v12,v22,v12
v_mul_f32 v13,v22,v13
v_mfma_f32_16x16x32_bf16 a[32:35],a[160:163],v[64:67],0
v_cvt_pk_bf16_f32 v218,v218,v219
v_cvt_pk_bf16_f32 v219,v12,v13
v_mfma_f32_16x16x32_bf16 a[32:35],a[164:167],v[68:71],a[32:35]
ds_read_b64_tr_b16 a[176:177],v2 offset:224
ds_read_b64_tr_b16 a[178:179],v2 offset:5344
v_mfma_f32_16x16x32_bf16 a[32:35],a[168:171],v[72:75],a[32:35]
ds_read_b64_tr_b16 a[180:181],v2 offset:10464
v_bfe_u32 v24,v6,4,2
v_mfma_f32_16x16x32_bf16 a[32:35],a[172:175],v[76:79],a[32:35]
ds_read_b64_tr_b16 a[182:183],v2 offset:15584
v_min_u32 v24,2,v24
v_mfma_f32_16x16x32_bf16 a[36:39],a[172:175],v[92:95],0
ds_read_b64_tr_b16 a[184:185],v2 offset:20704
v_add_u32 v24,s71,v24
v_mfma_f32_16x16x32_bf16 a[36:39],a[168:171],v[88:91],a[36:39]
ds_read_b64_tr_b16 a[186:187],v2 offset:25824
ds_read_b64_tr_b16 a[188:189],v2 offset:30944
v_mfma_f32_16x16x32_bf16 a[36:39],a[164:167],v[84:87],a[36:39]
buffer_store_dwordx4 v[184:187],v6,s[36:39],s85 offen offset:2048 sc1
v_mfma_f32_16x16x32_bf16 a[36:39],a[160:163],v[80:83],a[36:39]
ds_read_b64_tr_b16 a[190:191],v2 offset:36064
v_add_u32 v24,1,v24
v_mfma_f32_16x16x32_bf16 a[40:43],a[160:163],v[96:99],0
v_and_b32 v24,3,v24
v_lshlrev_b32 v24,6,v24
v_mfma_f32_16x16x32_bf16 a[40:43],a[164:167],v[100:103],a[40:43]
v_accvgpr_read_b32 v220,a88
v_accvgpr_read_b32 v221,a89
v_mfma_f32_16x16x32_bf16 a[40:43],a[168:171],v[104:107],a[40:43]
buffer_load_dwordx4 v8,s[72:75],s83 offen offset:1024 lds
v_mfma_f32_16x16x32_bf16 a[40:43],a[172:175],v[108:111],a[40:43]
v_mul_f32 v220,v22,v220
v_mul_f32 v221,v22,v221
v_mfma_f32_16x16x32_bf16 a[44:47],a[172:175],v[124:127],0
v_accvgpr_read_b32 v222,a90
v_accvgpr_read_b32 v223,a91
v_mfma_f32_16x16x32_bf16 a[44:47],a[168:171],v[120:123],a[44:47]
v_cvt_pk_bf16_f32 v220,v220,v221
v_mul_f32 v222,v22,v222
v_mfma_f32_16x16x32_bf16 a[44:47],a[164:167],v[116:119],a[44:47]
buffer_load_dwordx2 v[26:27],v24,s[64:67],s94 offen sc1
v_mfma_f32_16x16x32_bf16 a[44:47],a[160:163],v[112:115],a[44:47]
s_waitcnt lgkmcnt(0)
v_mul_f32 v223,v22,v223
v_cvt_pk_bf16_f32 v221,v222,v223
v_mfma_f32_16x16x32_bf16 a[48:51],a[176:179],v[64:67],0
v_accvgpr_read_b32 v222,a92
v_accvgpr_read_b32 v223,a93
v_mfma_f32_16x16x32_bf16 a[48:51],a[180:183],v[68:71],a[48:51]
v_accvgpr_read_b32 v14,a94
v_accvgpr_read_b32 v15,a95
v_mfma_f32_16x16x32_bf16 a[48:51],a[184:187],v[72:75],a[48:51]
v_mul_f32 v222,v22,v222
v_mul_f32 v223,v22,v223
v_mfma_f32_16x16x32_bf16 a[48:51],a[188:191],v[76:79],a[48:51]
v_mul_f32 v14,v22,v14
v_mul_f32 v15,v22,v15
v_mfma_f32_16x16x32_bf16 a[52:55],a[188:191],v[92:95],0
v_cvt_pk_bf16_f32 v222,v222,v223
v_cvt_pk_bf16_f32 v223,v14,v15
v_mfma_f32_16x16x32_bf16 a[52:55],a[184:187],v[88:91],a[52:55]
buffer_load_dwordx4 v9,s[72:75],s83 offen offset:2048 lds
v_mfma_f32_16x16x32_bf16 a[52:55],a[180:183],v[84:87],a[52:55]
ds_read_b64_tr_b16 a[128:129],v1 offset:256
ds_read_b64_tr_b16 a[130:131],v1 offset:5376
v_mfma_f32_16x16x32_bf16 a[52:55],a[176:179],v[80:83],a[52:55]
ds_read_b64_tr_b16 a[132:133],v1 offset:10496
v_add_f32 v60,v61,v60
v_mfma_f32_16x16x32_bf16 a[56:59],a[176:179],v[96:99],0
ds_read_b64_tr_b16 a[134:135],v1 offset:15616
v_max_f32 v23,0x00800000,v60
v_mfma_f32_16x16x32_bf16 a[56:59],a[180:183],v[100:103],a[56:59]
ds_read_b64_tr_b16 a[136:137],v1 offset:20736
ds_read_b64_tr_b16 a[138:139],v1 offset:25856
v_mfma_f32_16x16x32_bf16 a[56:59],a[184:187],v[104:107],a[56:59]
ds_read_b64_tr_b16 a[140:141],v1 offset:30976
ds_read_b64_tr_b16 a[142:143],v1 offset:36096
v_mfma_f32_16x16x32_bf16 a[56:59],a[188:191],v[108:111],a[56:59]
buffer_store_dwordx4 v[188:191],v6,s[36:39],s85 offen offset:3072 sc1
v_mfma_f32_16x16x32_bf16 a[60:63],a[188:191],v[124:127],0
v_rcp_f32 v23,v23
v_mfma_f32_16x16x32_bf16 a[60:63],a[184:187],v[120:123],a[60:63]
v_accvgpr_read_b32 v232,a96
v_accvgpr_read_b32 v233,a97
v_mfma_f32_16x16x32_bf16 a[60:63],a[180:183],v[116:119],a[60:63]
v_mul_f32 v232,v23,v232
v_mul_f32 v233,v23,v233
v_mfma_f32_16x16x32_bf16 a[60:63],a[176:179],v[112:115],a[60:63]
s_waitcnt lgkmcnt(5)
v_accvgpr_read_b32 v234,a98
v_accvgpr_read_b32 v235,a99
v_mfma_f32_16x16x32_bf16 a[64:67],a[128:131],v[64:67],0
s_waitcnt lgkmcnt(4)
v_cvt_pk_bf16_f32 v232,v232,v233
v_mul_f32 v234,v23,v234
v_mfma_f32_16x16x32_bf16 a[64:67],a[132:135],v[68:71],a[64:67]
s_waitcnt lgkmcnt(2)
v_mul_f32 v235,v23,v235
v_cvt_pk_bf16_f32 v233,v234,v235
v_mfma_f32_16x16x32_bf16 a[64:67],a[136:139],v[72:75],a[64:67]
s_waitcnt lgkmcnt(0)
v_accvgpr_read_b32 v234,a100
v_accvgpr_read_b32 v235,a101
v_mfma_f32_16x16x32_bf16 a[64:67],a[140:143],v[76:79],a[64:67]
buffer_load_dwordx4 v10,s[72:75],s83 offen offset:3072 lds
v_mfma_f32_16x16x32_bf16 a[68:71],a[140:143],v[92:95],0
v_accvgpr_read_b32 v16,a102
v_accvgpr_read_b32 v17,a103
v_mfma_f32_16x16x32_bf16 a[68:71],a[136:139],v[88:91],a[68:71]
v_mul_f32 v234,v23,v234
v_mul_f32 v235,v23,v235
v_mfma_f32_16x16x32_bf16 a[68:71],a[132:135],v[84:87],a[68:71]
v_mul_f32 v16,v23,v16
v_mul_f32 v17,v23,v17
v_mfma_f32_16x16x32_bf16 a[68:71],a[128:131],v[80:83],a[68:71]
ds_read_b64_tr_b16 a[144:145],v2 offset:288
ds_read_b64_tr_b16 a[146:147],v2 offset:5408
v_mfma_f32_16x16x32_bf16 a[72:75],a[128:131],v[96:99],0
ds_read_b64_tr_b16 a[148:149],v2 offset:10528
ds_read_b64_tr_b16 a[150:151],v2 offset:15648
v_mfma_f32_16x16x32_bf16 a[72:75],a[132:135],v[100:103],a[72:75]
ds_read_b64_tr_b16 a[152:153],v2 offset:20768
ds_read_b64_tr_b16 a[154:155],v2 offset:25888
v_mfma_f32_16x16x32_bf16 a[72:75],a[136:139],v[104:107],a[72:75]
ds_read_b64_tr_b16 a[156:157],v2 offset:31008
ds_read_b64_tr_b16 a[158:159],v2 offset:36128
v_mfma_f32_16x16x32_bf16 a[72:75],a[140:143],v[108:111],a[72:75]
s_add_u32 m0,s82,132096
v_mfma_f32_16x16x32_bf16 a[76:79],a[140:143],v[124:127],0
v_cvt_pk_bf16_f32 v234,v234,v235
v_cvt_pk_bf16_f32 v235,v16,v17
v_mfma_f32_16x16x32_bf16 a[76:79],a[136:139],v[120:123],a[76:79]
v_accvgpr_read_b32 v236,a104
v_accvgpr_read_b32 v237,a105
v_mfma_f32_16x16x32_bf16 a[76:79],a[132:135],v[116:119],a[76:79]
v_mul_f32 v236,v23,v236
v_mul_f32 v237,v23,v237
v_mfma_f32_16x16x32_bf16 a[76:79],a[128:131],v[112:115],a[76:79]
s_waitcnt lgkmcnt(6)
buffer_load_dwordx4 v11,s[72:75],s83 offen lds
v_mfma_f32_16x16x32_bf16 a[80:83],a[144:147],v[64:67],0
s_waitcnt lgkmcnt(4)
v_accvgpr_read_b32 v238,a106
v_accvgpr_read_b32 v239,a107
v_mfma_f32_16x16x32_bf16 a[80:83],a[148:151],v[68:71],a[80:83]
s_waitcnt lgkmcnt(2)
v_cvt_pk_bf16_f32 v236,v236,v237
v_mul_f32 v238,v23,v238
v_mfma_f32_16x16x32_bf16 a[80:83],a[152:155],v[72:75],a[80:83]
s_waitcnt lgkmcnt(0)
v_mul_f32 v239,v23,v239
v_cvt_pk_bf16_f32 v237,v238,v239
v_mfma_f32_16x16x32_bf16 a[80:83],a[156:159],v[76:79],a[80:83]
v_accvgpr_read_b32 v238,a108
v_accvgpr_read_b32 v239,a109
v_mfma_f32_16x16x32_bf16 a[84:87],a[156:159],v[92:95],0
v_accvgpr_read_b32 v12,a110
v_accvgpr_read_b32 v13,a111
v_mfma_f32_16x16x32_bf16 a[84:87],a[152:155],v[88:91],a[84:87]
v_mul_f32 v238,v23,v238
v_mul_f32 v239,v23,v239
v_mfma_f32_16x16x32_bf16 a[84:87],a[148:151],v[84:87],a[84:87]
v_subrev_u32 v1,81920,v1
v_mul_f32 v12,v23,v12
v_mfma_f32_16x16x32_bf16 a[84:87],a[144:147],v[80:83],a[84:87]
ds_read_b64_tr_b16 a[160:161],v1
ds_read_b64_tr_b16 a[162:163],v1 offset:5120
v_mfma_f32_16x16x32_bf16 a[88:91],a[144:147],v[96:99],0
ds_read_b64_tr_b16 a[164:165],v1 offset:10240
ds_read_b64_tr_b16 a[166:167],v1 offset:15360
v_mfma_f32_16x16x32_bf16 a[88:91],a[148:151],v[100:103],a[88:91]
ds_read_b64_tr_b16 a[168:169],v1 offset:20480
ds_read_b64_tr_b16 a[170:171],v1 offset:25600
v_mfma_f32_16x16x32_bf16 a[88:91],a[152:155],v[104:107],a[88:91]
ds_read_b64_tr_b16 a[172:173],v1 offset:30720
ds_read_b64_tr_b16 a[174:175],v1 offset:35840
v_mfma_f32_16x16x32_bf16 a[88:91],a[156:159],v[108:111],a[88:91]
v_accvgpr_read_b32 v248,a112
v_accvgpr_read_b32 v249,a113
v_mul_f32 v248,v23,v248
v_mul_f32 v249,v23,v249
v_accvgpr_read_b32 v250,a114
v_accvgpr_read_b32 v251,a115
v_accvgpr_read_b32 v252,a120
v_accvgpr_read_b32 v253,a121
v_cmp_nlt_f32 s[56:57],v48,s63
v_mul_f32 v252,v23,v252
v_mul_f32 v253,v23,v253
v_accvgpr_read_b32 v254,a122
v_accvgpr_read_b32 v255,a123
v_mfma_f32_16x16x32_bf16 a[92:95],a[156:159],v[124:127],0
v_cvt_pk_bf16_f32 v248,v248,v249
v_mul_f32 v250,v23,v250
v_mul_f32 v251,v23,v251
v_cvt_pk_bf16_f32 v249,v250,v251
v_cvt_pk_bf16_f32 v252,v252,v253
v_mul_f32 v254,v23,v254
v_mul_f32 v255,v23,v255
v_cmp_nlt_f32 s[58:59],v52,s63
s_nop 1
s_or_b64 s[56:57],s[56:57],s[58:59]
v_cvt_pk_bf16_f32 v253,v254,v255
v_mfma_f32_16x16x32_bf16 a[92:95],a[152:155],v[120:123],a[92:95]
v_accvgpr_read_b32 v250,a116
v_accvgpr_read_b32 v251,a117
v_accvgpr_read_b32 v14,a118
v_accvgpr_read_b32 v15,a119
v_mul_f32 v250,v23,v250
v_mul_f32 v251,v23,v251
v_accvgpr_read_b32 v254,a124
v_accvgpr_read_b32 v255,a125
v_cmp_nlt_f32 s[58:59],v56,s63
s_nop 1
s_or_b64 s[56:57],s[56:57],s[58:59]
v_accvgpr_read_b32 v16,a126
v_accvgpr_read_b32 v17,a127
v_mul_f32 v254,v23,v254
v_mul_f32 v255,v23,v255
s_waitcnt vmcnt(4)
v_cmp_ne_u32 s[90:91],s68,v26
v_cmp_ne_u32 s[92:93],s69,v27
s_nop 0
s_or_b64 s[90:91],s[90:91],s[92:93]
s_cbranch_scc1 L_fr8_%=
L_fo8_%=:
v_mfma_f32_16x16x32_bf16 a[92:95],a[148:151],v[116:119],a[92:95]
v_mul_f32 v13,v23,v13
v_cvt_pk_bf16_f32 v238,v238,v239
v_cvt_pk_bf16_f32 v239,v12,v13
v_mul_f32 v14,v23,v14
v_mul_f32 v15,v23,v15
v_cvt_pk_bf16_f32 v250,v250,v251
v_cvt_pk_bf16_f32 v251,v14,v15
v_mul_f32 v16,v23,v16
v_mul_f32 v17,v23,v17
v_cvt_pk_bf16_f32 v254,v254,v255
v_cmp_nlt_f32 s[58:59],v60,s63
s_nop 1
s_or_b64 s[56:57],s[56:57],s[58:59]
v_cvt_pk_bf16_f32 v255,v16,v17
v_mfma_f32_16x16x32_bf16 a[92:95],a[144:147],v[112:115],a[92:95]
s_cmp_lg_u64 s[56:57],0
s_cbranch_scc1 L_slow1_%=
L_sret1_%=:
v_subrev_u32 v2,81920,v2
s_waitcnt lgkmcnt(6)
v_mfma_f32_16x16x32_bf16 a[192:195],a[160:163],v[136:139],a[192:195]
)A"
R"A(s_cmp_ge_u32 0,s71
s_cselect_b32 s76,2,0
s_lshl_b32 s76,s76,16
s_waitcnt lgkmcnt(4)
v_mfma_f32_16x16x32_bf16 a[192:195],a[164:167],v[140:143],a[192:195]
s_add_u32 s77,s76,4096
s_add_u32 s78,s76,8192
s_add_u32 s79,s76,12288
s_max_i32 s76,s76,s18
s_waitcnt lgkmcnt(2)
v_mfma_f32_16x16x32_bf16 a[192:195],a[168:171],v[152:155],a[192:195]
s_max_i32 s77,s77,s19
buffer_load_dwordx4 v[128:131],v6,s[28:31],s76 offen sc1
s_waitcnt lgkmcnt(0)
v_mfma_f32_16x16x32_bf16 a[192:195],a[172:175],v[156:159],a[192:195]
s_max_i32 s78,s78,s100
ds_read_b64_tr_b16 a[176:177],v2 offset:32
ds_read_b64_tr_b16 a[178:179],v2 offset:5152
v_mfma_f32_16x16x32_bf16 a[196:199],a[172:175],v[188:191],a[196:199]
s_cmp_ge_u32 2,s71
s_cselect_b32 s80,6,4
ds_read_b64_tr_b16 a[180:181],v2 offset:10272
v_mfma_f32_16x16x32_bf16 a[196:199],a[168:171],v[184:187],a[196:199]
s_mul_i32 s80,s80,163840
s_add_u32 s80,s80,s81
ds_read_b64_tr_b16 a[182:183],v2 offset:15392
v_mfma_f32_16x16x32_bf16 a[196:199],a[164:167],v[172:175],a[196:199]
s_max_i32 s79,s79,s101
buffer_store_dwordx4 v[200:203],v6,s[36:39],s86 offen sc1
v_mfma_f32_16x16x32_bf16 a[196:199],a[160:163],v[168:171],a[196:199]
s_add_u32 s83,s80,20480
ds_read_b64_tr_b16 a[184:185],v2 offset:20512
ds_read_b64_tr_b16 a[186:187],v2 offset:25632
v_mfma_f32_16x16x32_bf16 a[200:203],a[160:163],v[200:203],a[200:203]
ds_read_b64_tr_b16 a[188:189],v2 offset:30752
v_mfma_f32_16x16x32_bf16 a[200:203],a[164:167],v[204:207],a[200:203]
ds_read_b64_tr_b16 a[190:191],v2 offset:35872
v_mfma_f32_16x16x32_bf16 a[200:203],a[168:171],v[216:219],a[200:203]
buffer_load_dwordx4 v[160:163],v6,s[28:31],s76 offen offset:1024 sc1
v_mfma_f32_16x16x32_bf16 a[200:203],a[172:175],v[220:223],a[200:203]
ds_read_b64_tr_b16 a[128:129],v1 offset:64
ds_read_b64_tr_b16 a[130:131],v1 offset:5184
v_mfma_f32_16x16x32_bf16 a[204:207],a[172:175],v[252:255],a[204:207]
ds_read_b64_tr_b16 a[132:133],v1 offset:10304
v_mfma_f32_16x16x32_bf16 a[204:207],a[168:171],v[248:251],a[204:207]
ds_read_b64_tr_b16 a[134:135],v1 offset:15424
v_mfma_f32_16x16x32_bf16 a[204:207],a[164:167],v[236:239],a[204:207]
buffer_store_dwordx4 v[204:207],v6,s[36:39],s86 offen offset:1024 sc1
v_mfma_f32_16x16x32_bf16 a[204:207],a[160:163],v[232:235],a[204:207]
s_barrier
ds_read_b64_tr_b16 a[136:137],v1 offset:20544
ds_read_b64_tr_b16 a[138:139],v1 offset:25664
s_waitcnt lgkmcnt(8)
v_mfma_f32_16x16x32_bf16 a[208:211],a[176:179],v[136:139],a[208:211]
ds_read_b64_tr_b16 a[140:141],v1 offset:30784
v_mfma_f32_16x16x32_bf16 a[208:211],a[180:183],v[140:143],a[208:211]
buffer_load_dwordx4 v[192:195],v6,s[28:31],s76 offen offset:2048 sc1
v_mfma_f32_16x16x32_bf16 a[208:211],a[184:187],v[152:155],a[208:211]
s_add_u32 m0,s82,81920
ds_read_b64_tr_b16 a[142:143],v1 offset:35904
ds_read_b64_tr_b16 a[144:145],v2 offset:96
s_waitcnt lgkmcnt(9)
v_mfma_f32_16x16x32_bf16 a[208:211],a[188:191],v[156:159],a[208:211]
ds_read_b64_tr_b16 a[146:147],v2 offset:5216
v_mfma_f32_16x16x32_bf16 a[212:215],a[188:191],v[188:191],a[212:215]
ds_read_b64_tr_b16 a[148:149],v2 offset:10336
v_mfma_f32_16x16x32_bf16 a[212:215],a[184:187],v[184:187],a[212:215]
buffer_store_dwordx4 v[216:219],v6,s[36:39],s86 offen offset:2048 sc1
v_mfma_f32_16x16x32_bf16 a[212:215],a[180:183],v[172:175],a[212:215]
ds_read_b64_tr_b16 a[150:151],v2 offset:15456
ds_read_b64_tr_b16 a[152:153],v2 offset:20576
v_mfma_f32_16x16x32_bf16 a[212:215],a[176:179],v[168:171],a[212:215]
ds_read_b64_tr_b16 a[154:155],v2 offset:25696
v_mfma_f32_16x16x32_bf16 a[216:219],a[176:179],v[200:203],a[216:219]
ds_read_b64_tr_b16 a[156:157],v2 offset:30816
v_mfma_f32_16x16x32_bf16 a[216:219],a[180:183],v[204:207],a[216:219]
buffer_load_dwordx4 v[224:227],v6,s[28:31],s76 offen offset:3072 sc1
v_mfma_f32_16x16x32_bf16 a[216:219],a[184:187],v[216:219],a[216:219]
ds_read_b64_tr_b16 a[158:159],v2 offset:35936
ds_read_b64_tr_b16 a[160:161],v1 offset:128
v_mfma_f32_16x16x32_bf16 a[216:219],a[188:191],v[220:223],a[216:219]
ds_read_b64_tr_b16 a[162:163],v1 offset:5248
v_mfma_f32_16x16x32_bf16 a[220:223],a[188:191],v[252:255],a[220:223]
ds_read_b64_tr_b16 a[164:165],v1 offset:10368
v_mfma_f32_16x16x32_bf16 a[220:223],a[184:187],v[248:251],a[220:223]
buffer_load_dwordx4 v7,s[72:75],s80 offen lds
v_mfma_f32_16x16x32_bf16 a[220:223],a[180:183],v[236:239],a[220:223]
ds_read_b64_tr_b16 a[166:167],v1 offset:15488
ds_read_b64_tr_b16 a[168:169],v1 offset:20608
v_mfma_f32_16x16x32_bf16 a[220:223],a[176:179],v[232:235],a[220:223]
s_waitcnt lgkmcnt(7)
ds_read_b64_tr_b16 a[170:171],v1 offset:25728
v_mfma_f32_16x16x32_bf16 a[224:227],a[128:131],v[136:139],a[224:227]
ds_read_b64_tr_b16 a[172:173],v1 offset:30848
v_mfma_f32_16x16x32_bf16 a[224:227],a[132:135],v[140:143],a[224:227]
buffer_load_dwordx4 v[132:135],v6,s[28:31],s77 offen sc1
v_mfma_f32_16x16x32_bf16 a[224:227],a[136:139],v[152:155],a[224:227]
ds_read_b64_tr_b16 a[174:175],v1 offset:35968
ds_read_b64_tr_b16 a[176:177],v2 offset:160
v_mfma_f32_16x16x32_bf16 a[224:227],a[140:143],v[156:159],a[224:227]
ds_read_b64_tr_b16 a[178:179],v2 offset:5280
v_mfma_f32_16x16x32_bf16 a[228:231],a[140:143],v[188:191],a[228:231]
ds_read_b64_tr_b16 a[180:181],v2 offset:10400
v_mfma_f32_16x16x32_bf16 a[228:231],a[136:139],v[184:187],a[228:231]
buffer_load_dwordx4 v[164:167],v6,s[28:31],s77 offen offset:1024 sc1
v_mfma_f32_16x16x32_bf16 a[228:231],a[132:135],v[172:175],a[228:231]
ds_read_b64_tr_b16 a[182:183],v2 offset:15520
ds_read_b64_tr_b16 a[184:185],v2 offset:20640
v_mfma_f32_16x16x32_bf16 a[228:231],a[128:131],v[168:171],a[228:231]
ds_read_b64_tr_b16 a[186:187],v2 offset:25760
v_mfma_f32_16x16x32_bf16 a[232:235],a[128:131],v[200:203],a[232:235]
ds_read_b64_tr_b16 a[188:189],v2 offset:30880
v_mfma_f32_16x16x32_bf16 a[232:235],a[132:135],v[204:207],a[232:235]
buffer_load_dwordx4 v8,s[72:75],s80 offen offset:1024 lds
v_mfma_f32_16x16x32_bf16 a[232:235],a[136:139],v[216:219],a[232:235]
ds_read_b64_tr_b16 a[190:191],v2 offset:36000
v_mfma_f32_16x16x32_bf16 a[232:235],a[140:143],v[220:223],a[232:235]
v_mfma_f32_16x16x32_bf16 a[236:239],a[140:143],v[252:255],a[236:239]
ds_read_b64_tr_b16 a[140:141],v1 offset:30912
ds_read_b64_tr_b16 a[142:143],v1 offset:36032
v_mfma_f32_16x16x32_bf16 a[236:239],a[136:139],v[248:251],a[236:239]
buffer_load_dwordx4 v[196:199],v6,s[28:31],s77 offen offset:2048 sc1
v_mfma_f32_16x16x32_bf16 a[236:239],a[132:135],v[236:239],a[236:239]
ds_read_b64_tr_b16 a[132:133],v1 offset:10432
ds_read_b64_tr_b16 a[134:135],v1 offset:15552
v_mfma_f32_16x16x32_bf16 a[236:239],a[128:131],v[232:235],a[236:239]
ds_read_b64_tr_b16 a[128:129],v1 offset:192
v_mfma_f32_16x16x32_bf16 a[240:243],a[144:147],v[136:139],a[240:243]
ds_read_b64_tr_b16 a[130:131],v1 offset:5312
v_mfma_f32_16x16x32_bf16 a[240:243],a[148:151],v[140:143],a[240:243]
buffer_store_dwordx4 v[220:223],v6,s[36:39],s86 offen offset:3072 sc1
v_mfma_f32_16x16x32_bf16 a[240:243],a[152:155],v[152:155],a[240:243]
s_waitcnt lgkmcnt(6)
ds_read_b64_tr_b16 a[136:137],v1 offset:20672
ds_read_b64_tr_b16 a[138:139],v1 offset:25792
v_mfma_f32_16x16x32_bf16 a[240:243],a[156:159],v[156:159],a[240:243]
v_mfma_f32_16x16x32_bf16 a[244:247],a[156:159],v[188:191],a[244:247]
v_mfma_f32_16x16x32_bf16 a[244:247],a[152:155],v[184:187],a[244:247]
buffer_load_dwordx4 v[228:231],v6,s[28:31],s77 offen offset:3072 sc1
v_mfma_f32_16x16x32_bf16 a[244:247],a[148:151],v[172:175],a[244:247]
v_mfma_f32_16x16x32_bf16 a[244:247],a[144:147],v[168:171],a[244:247]
v_mfma_f32_16x16x32_bf16 a[248:251],a[144:147],v[200:203],a[248:251]
v_mfma_f32_16x16x32_bf16 a[248:251],a[148:151],v[204:207],a[248:251]
buffer_load_dwordx4 v9,s[72:75],s80 offen offset:2048 lds
v_mfma_f32_16x16x32_bf16 a[248:251],a[152:155],v[216:219],a[248:251]
v_mfma_f32_16x16x32_bf16 a[248:251],a[156:159],v[220:223],a[248:251]
v_mfma_f32_16x16x32_bf16 a[252:255],a[156:159],v[252:255],a[252:255]
ds_read_b64_tr_b16 a[156:157],v2 offset:30944
ds_read_b64_tr_b16 a[158:159],v2 offset:36064
v_mfma_f32_16x16x32_bf16 a[252:255],a[152:155],v[248:251],a[252:255]
buffer_load_dwordx4 v[144:147],v6,s[28:31],s78 offen sc1
v_mfma_f32_16x16x32_bf16 a[252:255],a[148:151],v[236:239],a[252:255]
ds_read_b64_tr_b16 a[148:149],v2 offset:10464
ds_read_b64_tr_b16 a[150:151],v2 offset:15584
v_mfma_f32_16x16x32_bf16 a[252:255],a[144:147],v[232:235],a[252:255]
ds_read_b64_tr_b16 a[144:145],v2 offset:224
v_mfma_f32_16x16x32_bf16 a[0:3],a[160:163],v[136:139],a[0:3]
ds_read_b64_tr_b16 a[146:147],v2 offset:5344
v_mfma_f32_16x16x32_bf16 a[0:3],a[164:167],v[140:143],a[0:3]
buffer_load_dwordx4 v[176:179],v6,s[28:31],s78 offen offset:1024 sc1
v_mfma_f32_16x16x32_bf16 a[0:3],a[168:171],v[152:155],a[0:3]
ds_read_b64_tr_b16 a[152:153],v2 offset:20704
ds_read_b64_tr_b16 a[154:155],v2 offset:25824
v_mfma_f32_16x16x32_bf16 a[0:3],a[172:175],v[156:159],a[0:3]
v_mfma_f32_16x16x32_bf16 a[4:7],a[172:175],v[188:191],a[4:7]
v_mfma_f32_16x16x32_bf16 a[4:7],a[168:171],v[184:187],a[4:7]
buffer_load_dwordx4 v10,s[72:75],s80 offen offset:3072 lds
v_mfma_f32_16x16x32_bf16 a[4:7],a[164:167],v[172:175],a[4:7]
v_mfma_f32_16x16x32_bf16 a[4:7],a[160:163],v[168:171],a[4:7]
v_mfma_f32_16x16x32_bf16 a[8:11],a[160:163],v[200:203],a[8:11]
v_mfma_f32_16x16x32_bf16 a[8:11],a[164:167],v[204:207],a[8:11]
buffer_load_dwordx4 v[208:211],v6,s[28:31],s78 offen offset:2048 sc1
v_mfma_f32_16x16x32_bf16 a[8:11],a[168:171],v[216:219],a[8:11]
v_mfma_f32_16x16x32_bf16 a[8:11],a[172:175],v[220:223],a[8:11]
v_mfma_f32_16x16x32_bf16 a[12:15],a[172:175],v[252:255],a[12:15]
ds_read_b64_tr_b16 a[172:173],v1 offset:30976
ds_read_b64_tr_b16 a[174:175],v1 offset:36096
v_mfma_f32_16x16x32_bf16 a[12:15],a[168:171],v[248:251],a[12:15]
buffer_store_dwordx4 v[232:235],v6,s[36:39],s87 offen sc1
v_mfma_f32_16x16x32_bf16 a[12:15],a[164:167],v[236:239],a[12:15]
s_add_u32 m0,s82,86016
ds_read_b64_tr_b16 a[164:165],v1 offset:10496
ds_read_b64_tr_b16 a[166:167],v1 offset:15616
v_mfma_f32_16x16x32_bf16 a[12:15],a[160:163],v[232:235],a[12:15]
ds_read_b64_tr_b16 a[160:161],v1 offset:256
v_mfma_f32_16x16x32_bf16 a[16:19],a[176:179],v[136:139],a[16:19]
ds_read_b64_tr_b16 a[162:163],v1 offset:5376
v_mfma_f32_16x16x32_bf16 a[16:19],a[180:183],v[140:143],a[16:19]
buffer_load_dwordx4 v[240:243],v6,s[28:31],s78 offen offset:3072 sc1
v_mfma_f32_16x16x32_bf16 a[16:19],a[184:187],v[152:155],a[16:19]
ds_read_b64_tr_b16 a[168:169],v1 offset:20736
ds_read_b64_tr_b16 a[170:171],v1 offset:25856
v_mfma_f32_16x16x32_bf16 a[16:19],a[188:191],v[156:159],a[16:19]
v_add_u32 v1,40960,v1
v_mfma_f32_16x16x32_bf16 a[20:23],a[188:191],v[188:191],a[20:23]
ds_read_b64_tr_b16 a[96:97],v1 offset:128
ds_read_b64_tr_b16 a[98:99],v1 offset:5248
v_mfma_f32_16x16x32_bf16 a[20:23],a[184:187],v[184:187],a[20:23]
buffer_load_dwordx4 v11,s[72:75],s80 offen lds
v_mfma_f32_16x16x32_bf16 a[20:23],a[180:183],v[172:175],a[20:23]
ds_read_b64_tr_b16 a[100:101],v1 offset:10368
ds_read_b64_tr_b16 a[102:103],v1 offset:15488
v_mfma_f32_16x16x32_bf16 a[20:23],a[176:179],v[168:171],a[20:23]
ds_read_b64_tr_b16 a[104:105],v1 offset:20608
v_mfma_f32_16x16x32_bf16 a[24:27],a[176:179],v[200:203],a[24:27]
ds_read_b64_tr_b16 a[106:107],v1 offset:25728
v_mfma_f32_16x16x32_bf16 a[24:27],a[180:183],v[204:207],a[24:27]
buffer_load_dwordx4 v[148:151],v6,s[28:31],s79 offen sc1
v_mfma_f32_16x16x32_bf16 a[24:27],a[184:187],v[216:219],a[24:27]
ds_read_b64_tr_b16 a[108:109],v1 offset:30848
ds_read_b64_tr_b16 a[110:111],v1 offset:35968
v_mfma_f32_16x16x32_bf16 a[24:27],a[188:191],v[220:223],a[24:27]
v_mfma_f32_16x16x32_bf16 a[28:31],a[188:191],v[252:255],a[28:31]
ds_read_b64_tr_b16 a[188:189],v2 offset:31008
ds_read_b64_tr_b16 a[190:191],v2 offset:36128
v_mfma_f32_16x16x32_bf16 a[28:31],a[184:187],v[248:251],a[28:31]
s_add_u32 m0,s82,87040
buffer_load_dwordx4 v[180:183],v6,s[28:31],s79 offen offset:1024 sc1
v_mfma_f32_16x16x32_bf16 a[28:31],a[180:183],v[236:239],a[28:31]
ds_read_b64_tr_b16 a[180:181],v2 offset:10528
ds_read_b64_tr_b16 a[182:183],v2 offset:15648
v_mfma_f32_16x16x32_bf16 a[28:31],a[176:179],v[232:235],a[28:31]
s_waitcnt lgkmcnt(7)
ds_read_b64_tr_b16 a[176:177],v2 offset:288
)A"
R"A(v_mfma_f32_16x16x32_bf16 a[32:35],a[128:131],v[136:139],a[32:35]
ds_read_b64_tr_b16 a[178:179],v2 offset:5408
v_mfma_f32_16x16x32_bf16 a[32:35],a[132:135],v[140:143],a[32:35]
buffer_store_dwordx4 v[236:239],v6,s[36:39],s87 offen offset:1024 sc1
v_mfma_f32_16x16x32_bf16 a[32:35],a[136:139],v[152:155],a[32:35]
ds_read_b64_tr_b16 a[184:185],v2 offset:20768
ds_read_b64_tr_b16 a[186:187],v2 offset:25888
v_mfma_f32_16x16x32_bf16 a[32:35],a[140:143],v[156:159],a[32:35]
v_add_u32 v2,40960,v2
v_mfma_f32_16x16x32_bf16 a[36:39],a[140:143],v[188:191],a[36:39]
ds_read_b64_tr_b16 a[112:113],v2 offset:160
ds_read_b64_tr_b16 a[114:115],v2 offset:5280
v_mfma_f32_16x16x32_bf16 a[36:39],a[136:139],v[184:187],a[36:39]
buffer_load_dwordx4 v[212:215],v6,s[28:31],s79 offen offset:2048 sc1
v_mfma_f32_16x16x32_bf16 a[36:39],a[132:135],v[172:175],a[36:39]
ds_read_b64_tr_b16 a[116:117],v2 offset:10400
ds_read_b64_tr_b16 a[118:119],v2 offset:15520
v_mfma_f32_16x16x32_bf16 a[36:39],a[128:131],v[168:171],a[36:39]
ds_read_b64_tr_b16 a[120:121],v2 offset:20640
v_mfma_f32_16x16x32_bf16 a[40:43],a[128:131],v[200:203],a[40:43]
ds_read_b64_tr_b16 a[122:123],v2 offset:25760
v_mfma_f32_16x16x32_bf16 a[40:43],a[132:135],v[204:207],a[40:43]
buffer_load_dwordx4 v7,s[72:75],s83 offen lds
v_mfma_f32_16x16x32_bf16 a[40:43],a[136:139],v[216:219],a[40:43]
ds_read_b64_tr_b16 a[124:125],v2 offset:30880
ds_read_b64_tr_b16 a[126:127],v2 offset:36000
v_mfma_f32_16x16x32_bf16 a[40:43],a[140:143],v[220:223],a[40:43]
v_mfma_f32_16x16x32_bf16 a[44:47],a[140:143],v[252:255],a[44:47]
ds_read_b64_tr_b16 a[140:141],v1 offset:30720
ds_read_b64_tr_b16 a[142:143],v1 offset:35840
v_mfma_f32_16x16x32_bf16 a[44:47],a[136:139],v[248:251],a[44:47]
buffer_load_dwordx4 v[244:247],v6,s[28:31],s79 offen offset:3072 sc1
v_mfma_f32_16x16x32_bf16 a[44:47],a[132:135],v[236:239],a[44:47]
ds_read_b64_tr_b16 a[132:133],v1 offset:10240
ds_read_b64_tr_b16 a[134:135],v1 offset:15360
v_mfma_f32_16x16x32_bf16 a[44:47],a[128:131],v[232:235],a[44:47]
ds_read_b64_tr_b16 a[128:129],v1
v_mfma_f32_16x16x32_bf16 a[48:51],a[144:147],v[136:139],a[48:51]
ds_read_b64_tr_b16 a[130:131],v1 offset:5120
v_mfma_f32_16x16x32_bf16 a[48:51],a[148:151],v[140:143],a[48:51]
buffer_store_dwordx4 v[248:251],v6,s[36:39],s87 offen offset:2048 sc1
v_mfma_f32_16x16x32_bf16 a[48:51],a[152:155],v[152:155],a[48:51]
ds_read_b64_tr_b16 a[136:137],v1 offset:20480
ds_read_b64_tr_b16 a[138:139],v1 offset:25600
v_mfma_f32_16x16x32_bf16 a[48:51],a[156:159],v[156:159],a[48:51]
s_cmp_ge_u32 1,s71
s_cselect_b32 s76,4,2
s_lshl_b32 s76,s76,16
v_mfma_f32_16x16x32_bf16 a[52:55],a[156:159],v[188:191],a[52:55]
s_add_u32 s77,s76,4096
s_add_u32 s78,s76,8192
s_add_u32 s79,s76,12288
v_mfma_f32_16x16x32_bf16 a[52:55],a[152:155],v[184:187],a[52:55]
s_max_i32 s76,s76,s18
buffer_load_dwordx4 v8,s[72:75],s83 offen offset:1024 lds
v_mfma_f32_16x16x32_bf16 a[52:55],a[148:151],v[172:175],a[52:55]
s_max_i32 s77,s77,s19
s_max_i32 s78,s78,s100
s_max_i32 s79,s79,s101
v_mfma_f32_16x16x32_bf16 a[52:55],a[144:147],v[168:171],a[52:55]
v_mfma_f32_16x16x32_bf16 a[56:59],a[144:147],v[200:203],a[56:59]
v_mfma_f32_16x16x32_bf16 a[56:59],a[148:151],v[204:207],a[56:59]
buffer_store_dwordx4 v[252:255],v6,s[36:39],s87 offen offset:3072 sc1
v_mfma_f32_16x16x32_bf16 a[56:59],a[152:155],v[216:219],a[56:59]
v_mfma_f32_16x16x32_bf16 a[56:59],a[156:159],v[220:223],a[56:59]
v_mfma_f32_16x16x32_bf16 a[60:63],a[156:159],v[252:255],a[60:63]
ds_read_b64_tr_b16 a[156:157],v2 offset:30752
ds_read_b64_tr_b16 a[158:159],v2 offset:35872
v_mfma_f32_16x16x32_bf16 a[60:63],a[152:155],v[248:251],a[60:63]
ds_read_b64_tr_b16 a[152:153],v2 offset:20512
v_mfma_f32_16x16x32_bf16 a[60:63],a[148:151],v[236:239],a[60:63]
buffer_load_dwordx4 v9,s[72:75],s83 offen offset:2048 lds
v_mfma_f32_16x16x32_bf16 a[60:63],a[144:147],v[232:235],a[60:63]
ds_read_b64_tr_b16 a[144:145],v2 offset:32
ds_read_b64_tr_b16 a[146:147],v2 offset:5152
v_mfma_f32_16x16x32_bf16 a[64:67],a[160:163],v[136:139],a[64:67]
ds_read_b64_tr_b16 a[148:149],v2 offset:10272
v_mfma_f32_16x16x32_bf16 a[64:67],a[164:167],v[140:143],a[64:67]
ds_read_b64_tr_b16 a[150:151],v2 offset:15392
v_mfma_f32_16x16x32_bf16 a[64:67],a[168:171],v[152:155],a[64:67]
ds_read_b64_tr_b16 a[154:155],v2 offset:25632
v_mfma_f32_16x16x32_bf16 a[64:67],a[172:175],v[156:159],a[64:67]
v_mfma_f32_16x16x32_bf16 a[68:71],a[172:175],v[188:191],a[68:71]
v_mfma_f32_16x16x32_bf16 a[68:71],a[168:171],v[184:187],a[68:71]
v_mfma_f32_16x16x32_bf16 a[68:71],a[164:167],v[172:175],a[68:71]
v_mfma_f32_16x16x32_bf16 a[68:71],a[160:163],v[168:171],a[68:71]
v_mfma_f32_16x16x32_bf16 a[72:75],a[160:163],v[200:203],a[72:75]
v_mfma_f32_16x16x32_bf16 a[72:75],a[164:167],v[204:207],a[72:75]
v_mfma_f32_16x16x32_bf16 a[72:75],a[168:171],v[216:219],a[72:75]
v_mfma_f32_16x16x32_bf16 a[72:75],a[172:175],v[220:223],a[72:75]
buffer_load_dwordx4 v[64:67],v6,s[28:31],s76 offen sc1
v_mfma_f32_16x16x32_bf16 a[76:79],a[172:175],v[252:255],a[76:79]
ds_read_b64_tr_b16 a[172:173],v1 offset:30784
ds_read_b64_tr_b16 a[174:175],v1 offset:35904
v_mfma_f32_16x16x32_bf16 a[76:79],a[168:171],v[248:251],a[76:79]
ds_read_b64_tr_b16 a[168:169],v1 offset:20544
v_mfma_f32_16x16x32_bf16 a[76:79],a[164:167],v[236:239],a[76:79]
ds_read_b64_tr_b16 a[164:165],v1 offset:10304
v_mfma_f32_16x16x32_bf16 a[76:79],a[160:163],v[232:235],a[76:79]
s_waitcnt lgkmcnt(4)
buffer_load_dwordx4 v10,s[72:75],s83 offen offset:3072 lds
v_mfma_f32_16x16x32_bf16 a[80:83],a[176:179],v[136:139],a[80:83]
ds_read_b64_tr_b16 a[160:161],v1 offset:64
ds_read_b64_tr_b16 a[162:163],v1 offset:5184
v_mfma_f32_16x16x32_bf16 a[80:83],a[180:183],v[140:143],a[80:83]
ds_read_b64_tr_b16 a[166:167],v1 offset:15424
v_mfma_f32_16x16x32_bf16 a[80:83],a[184:187],v[152:155],a[80:83]
ds_read_b64_tr_b16 a[170:171],v1 offset:25664
v_mfma_f32_16x16x32_bf16 a[80:83],a[188:191],v[156:159],a[80:83]
buffer_load_dwordx4 v[68:71],v6,s[28:31],s76 offen offset:1024 sc1
v_mfma_f32_16x16x32_bf16 a[84:87],a[188:191],v[188:191],a[84:87]
v_mfma_f32_16x16x32_bf16 a[84:87],a[184:187],v[184:187],a[84:87]
v_mfma_f32_16x16x32_bf16 a[84:87],a[180:183],v[172:175],a[84:87]
v_mfma_f32_16x16x32_bf16 a[84:87],a[176:179],v[168:171],a[84:87]
v_mfma_f32_16x16x32_bf16 a[88:91],a[176:179],v[200:203],a[88:91]
v_mfma_f32_16x16x32_bf16 a[88:91],a[180:183],v[204:207],a[88:91]
s_add_u32 m0,s82,91136
v_mfma_f32_16x16x32_bf16 a[88:91],a[184:187],v[216:219],a[88:91]
v_mfma_f32_16x16x32_bf16 a[88:91],a[188:191],v[220:223],a[88:91]
buffer_load_dwordx4 v[72:75],v6,s[28:31],s76 offen offset:2048 sc1
v_mfma_f32_16x16x32_bf16 a[92:95],a[188:191],v[252:255],a[92:95]
ds_read_b64_tr_b16 a[188:189],v2 offset:30816
ds_read_b64_tr_b16 a[190:191],v2 offset:35936
v_mfma_f32_16x16x32_bf16 a[92:95],a[184:187],v[248:251],a[92:95]
ds_read_b64_tr_b16 a[184:185],v2 offset:20576
v_mfma_f32_16x16x32_bf16 a[92:95],a[180:183],v[236:239],a[92:95]
s_cmp_ge_u32 0,s71
s_cselect_b32 s80,3,1
s_mul_i32 s80,s80,163840
v_mfma_f32_16x16x32_bf16 a[92:95],a[176:179],v[232:235],a[92:95]
s_add_u32 s80,s80,s81
buffer_load_dwordx4 v11,s[72:75],s83 offen lds
s_waitcnt vmcnt(14)
v_mfma_f32_16x16x32_bf16 a[192:195],a[128:131],v[128:131],a[192:195]
s_add_u32 s83,s80,20480
ds_read_b64_tr_b16 a[176:177],v2 offset:96
ds_read_b64_tr_b16 a[178:179],v2 offset:5216
v_mfma_f32_16x16x32_bf16 a[192:195],a[132:135],v[160:163],a[192:195]
ds_read_b64_tr_b16 a[180:181],v2 offset:10336
v_mfma_f32_16x16x32_bf16 a[192:195],a[136:139],v[192:195],a[192:195]
ds_read_b64_tr_b16 a[182:183],v2 offset:15456
v_mfma_f32_16x16x32_bf16 a[192:195],a[140:143],v[224:227],a[192:195]
buffer_load_dwordx4 v[76:79],v6,s[28:31],s76 offen offset:3072 sc1
v_mfma_f32_16x16x32_bf16 a[196:199],a[140:143],v[228:231],a[196:199]
ds_read_b64_tr_b16 a[186:187],v2 offset:25696
v_mfma_f32_16x16x32_bf16 a[196:199],a[136:139],v[196:199],a[196:199]
v_mfma_f32_16x16x32_bf16 a[196:199],a[132:135],v[164:167],a[196:199]
v_mfma_f32_16x16x32_bf16 a[196:199],a[128:131],v[132:135],a[196:199]
v_mfma_f32_16x16x32_bf16 a[200:203],a[128:131],v[144:147],a[200:203]
v_mfma_f32_16x16x32_bf16 a[200:203],a[132:135],v[176:179],a[200:203]
v_mfma_f32_16x16x32_bf16 a[200:203],a[136:139],v[208:211],a[200:203]
v_mfma_f32_16x16x32_bf16 a[200:203],a[140:143],v[240:243],a[200:203]
s_lshl_b32 s95,s42,2
buffer_load_dwordx4 v[80:83],v6,s[28:31],s77 offen sc1
s_waitcnt vmcnt(11)
v_mfma_f32_16x16x32_bf16 a[204:207],a[140:143],v[244:247],a[204:207]
s_add_u32 s95,s95,s40
s_lshl_b32 s95,s95,4
v_mov_b32 v18,s68
v_mfma_f32_16x16x32_bf16 a[204:207],a[136:139],v[212:215],a[204:207]
ds_read_b64_tr_b16 a[136:137],v1 offset:20672
v_mov_b32 v19,s69
v_mfma_f32_16x16x32_bf16 a[204:207],a[132:135],v[180:183],a[204:207]
ds_read_b64_tr_b16 a[132:133],v1 offset:10432
ds_read_b64_tr_b16 a[134:135],v1 offset:15552
v_mfma_f32_16x16x32_bf16 a[204:207],a[128:131],v[148:151],a[204:207]
s_barrier
ds_read_b64_tr_b16 a[128:129],v1 offset:192
ds_read_b64_tr_b16 a[130:131],v1 offset:5312
v_mfma_f32_16x16x32_bf16 a[208:211],a[144:147],v[128:131],a[208:211]
ds_read_b64_tr_b16 a[138:139],v1 offset:25792
v_mfma_f32_16x16x32_bf16 a[208:211],a[148:151],v[160:163],a[208:211]
ds_read_b64_tr_b16 a[140:141],v1 offset:30912
v_mfma_f32_16x16x32_bf16 a[208:211],a[152:155],v[192:195],a[208:211]
s_add_u32 m0,s82,0
s_waitcnt vmcnt(8)
s_mov_b64 exec,1
buffer_store_dwordx2 v[18:19],v6,s[64:67],s95 offen offset:1024 sc1
s_mov_b64 exec,-1
v_mfma_f32_16x16x32_bf16 a[208:211],a[156:159],v[224:227],a[208:211]
ds_read_b64_tr_b16 a[142:143],v1 offset:36032
v_mfma_f32_16x16x32_bf16 a[212:215],a[156:159],v[228:231],a[212:215]
v_mfma_f32_16x16x32_bf16 a[212:215],a[152:155],v[196:199],a[212:215]
v_mfma_f32_16x16x32_bf16 a[212:215],a[148:151],v[164:167],a[212:215]
buffer_load_dwordx4 v[84:87],v6,s[28:31],s77 offen offset:1024 sc1
v_mfma_f32_16x16x32_bf16 a[212:215],a[144:147],v[132:135],a[212:215]
v_mfma_f32_16x16x32_bf16 a[216:219],a[144:147],v[144:147],a[216:219]
v_mfma_f32_16x16x32_bf16 a[216:219],a[148:151],v[176:179],a[216:219]
v_mfma_f32_16x16x32_bf16 a[216:219],a[152:155],v[208:211],a[216:219]
buffer_load_dwordx4 v7,s[72:75],s80 offen lds
v_mfma_f32_16x16x32_bf16 a[216:219],a[156:159],v[240:243],a[216:219]
v_mfma_f32_16x16x32_bf16 a[220:223],a[156:159],v[244:247],a[220:223]
ds_read_b64_tr_b16 a[156:157],v2 offset:30944
ds_read_b64_tr_b16 a[158:159],v2 offset:36064
v_mfma_f32_16x16x32_bf16 a[220:223],a[152:155],v[212:215],a[220:223]
ds_read_b64_tr_b16 a[152:153],v2 offset:20704
v_mfma_f32_16x16x32_bf16 a[220:223],a[148:151],v[180:183],a[220:223]
buffer_load_dwordx4 v[88:91],v6,s[28:31],s77 offen offset:2048 sc1
v_mfma_f32_16x16x32_bf16 a[220:223],a[144:147],v[148:151],a[220:223]
s_waitcnt lgkmcnt(3)
ds_read_b64_tr_b16 a[144:145],v2 offset:224
ds_read_b64_tr_b16 a[146:147],v2 offset:5344
v_mfma_f32_16x16x32_bf16 a[224:227],a[160:163],v[128:131],a[224:227]
ds_read_b64_tr_b16 a[148:149],v2 offset:10464
v_mfma_f32_16x16x32_bf16 a[224:227],a[164:167],v[160:163],a[224:227]
ds_read_b64_tr_b16 a[150:151],v2 offset:15584
v_mfma_f32_16x16x32_bf16 a[224:227],a[168:171],v[192:195],a[224:227]
buffer_load_dwordx4 v[92:95],v6,s[28:31],s77 offen offset:3072 sc1
v_mfma_f32_16x16x32_bf16 a[224:227],a[172:175],v[224:227],a[224:227]
ds_read_b64_tr_b16 a[154:155],v2 offset:25824
v_mfma_f32_16x16x32_bf16 a[228:231],a[172:175],v[228:231],a[228:231]
v_mfma_f32_16x16x32_bf16 a[228:231],a[168:171],v[196:199],a[228:231]
v_mfma_f32_16x16x32_bf16 a[228:231],a[164:167],v[164:167],a[228:231]
buffer_load_dwordx4 v8,s[72:75],s80 offen offset:1024 lds
v_mfma_f32_16x16x32_bf16 a[228:231],a[160:163],v[132:135],a[228:231]
v_mfma_f32_16x16x32_bf16 a[232:235],a[160:163],v[144:147],a[232:235]
v_mfma_f32_16x16x32_bf16 a[232:235],a[164:167],v[176:179],a[232:235]
v_mfma_f32_16x16x32_bf16 a[232:235],a[168:171],v[208:211],a[232:235]
buffer_load_dwordx4 v[96:99],v6,s[28:31],s78 offen sc1
v_mfma_f32_16x16x32_bf16 a[232:235],a[172:175],v[240:243],a[232:235]
v_mfma_f32_16x16x32_bf16 a[236:239],a[172:175],v[244:247],a[236:239]
ds_read_b64_tr_b16 a[172:173],v1 offset:30976
ds_read_b64_tr_b16 a[174:175],v1 offset:36096
)A"
R"A(v_mfma_f32_16x16x32_bf16 a[236:239],a[168:171],v[212:215],a[236:239]
ds_read_b64_tr_b16 a[168:169],v1 offset:20736
v_mfma_f32_16x16x32_bf16 a[236:239],a[164:167],v[180:183],a[236:239]
ds_read_b64_tr_b16 a[164:165],v1 offset:10496
v_mfma_f32_16x16x32_bf16 a[236:239],a[160:163],v[148:151],a[236:239]
ds_read_b64_tr_b16 a[160:161],v1 offset:256
v_mfma_f32_16x16x32_bf16 a[240:243],a[176:179],v[128:131],a[240:243]
ds_read_b64_tr_b16 a[162:163],v1 offset:5376
ds_read_b64_tr_b16 a[166:167],v1 offset:15616
v_mfma_f32_16x16x32_bf16 a[240:243],a[180:183],v[160:163],a[240:243]
ds_read_b64_tr_b16 a[170:171],v1 offset:25856
v_add_u32 v1,81920,v1
v_mfma_f32_16x16x32_bf16 a[240:243],a[184:187],v[192:195],a[240:243]
buffer_load_dwordx4 v[100:103],v6,s[28:31],s78 offen offset:1024 sc1
v_mfma_f32_16x16x32_bf16 a[240:243],a[188:191],v[224:227],a[240:243]
v_mfma_f32_16x16x32_bf16 a[244:247],a[188:191],v[228:231],a[244:247]
v_mfma_f32_16x16x32_bf16 a[244:247],a[184:187],v[196:199],a[244:247]
v_mfma_f32_16x16x32_bf16 a[244:247],a[180:183],v[164:167],a[244:247]
buffer_load_dwordx4 v9,s[72:75],s80 offen offset:2048 lds
v_mfma_f32_16x16x32_bf16 a[244:247],a[176:179],v[132:135],a[244:247]
v_mfma_f32_16x16x32_bf16 a[248:251],a[176:179],v[144:147],a[248:251]
v_mfma_f32_16x16x32_bf16 a[248:251],a[180:183],v[176:179],a[248:251]
v_mfma_f32_16x16x32_bf16 a[248:251],a[184:187],v[208:211],a[248:251]
buffer_load_dwordx4 v[104:107],v6,s[28:31],s78 offen offset:2048 sc1
v_mfma_f32_16x16x32_bf16 a[248:251],a[188:191],v[240:243],a[248:251]
v_mfma_f32_16x16x32_bf16 a[252:255],a[188:191],v[244:247],a[252:255]
ds_read_b64_tr_b16 a[188:189],v2 offset:31008
ds_read_b64_tr_b16 a[190:191],v2 offset:36128
v_mfma_f32_16x16x32_bf16 a[252:255],a[184:187],v[212:215],a[252:255]
ds_read_b64_tr_b16 a[184:185],v2 offset:20768
v_mfma_f32_16x16x32_bf16 a[252:255],a[180:183],v[180:183],a[252:255]
ds_read_b64_tr_b16 a[180:181],v2 offset:10528
v_mfma_f32_16x16x32_bf16 a[252:255],a[176:179],v[148:151],a[252:255]
ds_read_b64_tr_b16 a[176:177],v2 offset:288
v_mfma_f32_16x16x32_bf16 a[0:3],a[96:99],v[128:131],a[0:3]
ds_read_b64_tr_b16 a[178:179],v2 offset:5408
ds_read_b64_tr_b16 a[182:183],v2 offset:15648
v_mfma_f32_16x16x32_bf16 a[0:3],a[100:103],v[160:163],a[0:3]
ds_read_b64_tr_b16 a[186:187],v2 offset:25888
v_add_u32 v2,81920,v2
v_mfma_f32_16x16x32_bf16 a[0:3],a[104:107],v[192:195],a[0:3]
buffer_load_dwordx4 v[108:111],v6,s[28:31],s78 offen offset:3072 sc1
v_mfma_f32_16x16x32_bf16 a[0:3],a[108:111],v[224:227],a[0:3]
v_mfma_f32_16x16x32_bf16 a[4:7],a[108:111],v[228:231],a[4:7]
v_mfma_f32_16x16x32_bf16 a[4:7],a[104:107],v[196:199],a[4:7]
v_mfma_f32_16x16x32_bf16 a[4:7],a[100:103],v[164:167],a[4:7]
buffer_load_dwordx4 v10,s[72:75],s80 offen offset:3072 lds
v_mfma_f32_16x16x32_bf16 a[4:7],a[96:99],v[132:135],a[4:7]
v_mfma_f32_16x16x32_bf16 a[8:11],a[96:99],v[144:147],a[8:11]
v_mfma_f32_16x16x32_bf16 a[8:11],a[100:103],v[176:179],a[8:11]
v_mfma_f32_16x16x32_bf16 a[8:11],a[104:107],v[208:211],a[8:11]
buffer_load_dwordx4 v[112:115],v6,s[28:31],s79 offen sc1
v_mfma_f32_16x16x32_bf16 a[8:11],a[108:111],v[240:243],a[8:11]
v_mfma_f32_16x16x32_bf16 a[12:15],a[108:111],v[244:247],a[12:15]
ds_read_b64_tr_b16 a[108:109],v1 offset:30720
ds_read_b64_tr_b16 a[110:111],v1 offset:35840
v_mfma_f32_16x16x32_bf16 a[12:15],a[104:107],v[212:215],a[12:15]
ds_read_b64_tr_b16 a[104:105],v1 offset:20480
v_mfma_f32_16x16x32_bf16 a[12:15],a[100:103],v[180:183],a[12:15]
s_add_u32 m0,s82,4096
ds_read_b64_tr_b16 a[100:101],v1 offset:10240
v_mfma_f32_16x16x32_bf16 a[12:15],a[96:99],v[148:151],a[12:15]
ds_read_b64_tr_b16 a[96:97],v1
v_mfma_f32_16x16x32_bf16 a[16:19],a[112:115],v[128:131],a[16:19]
ds_read_b64_tr_b16 a[98:99],v1 offset:5120
ds_read_b64_tr_b16 a[102:103],v1 offset:15360
v_mfma_f32_16x16x32_bf16 a[16:19],a[116:119],v[160:163],a[16:19]
buffer_load_dwordx4 v11,s[72:75],s80 offen lds
v_mfma_f32_16x16x32_bf16 a[16:19],a[120:123],v[192:195],a[16:19]
ds_read_b64_tr_b16 a[106:107],v1 offset:25600
v_mfma_f32_16x16x32_bf16 a[16:19],a[124:127],v[224:227],a[16:19]
v_mfma_f32_16x16x32_bf16 a[20:23],a[124:127],v[228:231],a[20:23]
v_mfma_f32_16x16x32_bf16 a[20:23],a[120:123],v[196:199],a[20:23]
buffer_load_dwordx4 v[116:119],v6,s[28:31],s79 offen offset:1024 sc1
v_mfma_f32_16x16x32_bf16 a[20:23],a[116:119],v[164:167],a[20:23]
v_mfma_f32_16x16x32_bf16 a[20:23],a[112:115],v[132:135],a[20:23]
v_mfma_f32_16x16x32_bf16 a[24:27],a[112:115],v[144:147],a[24:27]
v_mfma_f32_16x16x32_bf16 a[24:27],a[116:119],v[176:179],a[24:27]
v_mfma_f32_16x16x32_bf16 a[24:27],a[120:123],v[208:211],a[24:27]
buffer_load_dwordx4 v[120:123],v6,s[28:31],s79 offen offset:2048 sc1
v_mfma_f32_16x16x32_bf16 a[24:27],a[124:127],v[240:243],a[24:27]
v_mfma_f32_16x16x32_bf16 a[28:31],a[124:127],v[244:247],a[28:31]
ds_read_b64_tr_b16 a[124:125],v2 offset:30752
ds_read_b64_tr_b16 a[126:127],v2 offset:35872
v_mfma_f32_16x16x32_bf16 a[28:31],a[120:123],v[212:215],a[28:31]
s_add_u32 m0,s82,5120
ds_read_b64_tr_b16 a[120:121],v2 offset:20512
v_mfma_f32_16x16x32_bf16 a[28:31],a[116:119],v[180:183],a[28:31]
ds_read_b64_tr_b16 a[116:117],v2 offset:10272
v_mfma_f32_16x16x32_bf16 a[28:31],a[112:115],v[148:151],a[28:31]
s_cmp_ge_u32 2,s71
s_cselect_b32 s76,6,4
s_lshl_b32 s76,s76,16
v_mfma_f32_16x16x32_bf16 a[32:35],a[128:131],v[128:131],a[32:35]
s_add_u32 s77,s76,4096
buffer_load_dwordx4 v7,s[72:75],s83 offen lds
v_mfma_f32_16x16x32_bf16 a[32:35],a[132:135],v[160:163],a[32:35]
s_add_u32 s78,s76,8192
s_max_i32 s77,s77,s19
ds_read_b64_tr_b16 a[112:113],v2 offset:32
v_mfma_f32_16x16x32_bf16 a[32:35],a[136:139],v[192:195],a[32:35]
s_max_i32 s78,s78,s100
ds_read_b64_tr_b16 a[114:115],v2 offset:5152
ds_read_b64_tr_b16 a[118:119],v2 offset:15392
v_mfma_f32_16x16x32_bf16 a[32:35],a[140:143],v[224:227],a[32:35]
ds_read_b64_tr_b16 a[122:123],v2 offset:25632
v_mfma_f32_16x16x32_bf16 a[36:39],a[140:143],v[228:231],a[36:39]
buffer_load_dwordx4 v[124:127],v6,s[28:31],s79 offen offset:3072 sc1
v_mfma_f32_16x16x32_bf16 a[36:39],a[136:139],v[196:199],a[36:39]
s_add_u32 s79,s76,12288
s_max_i32 s76,s76,s18
s_max_i32 s79,s79,s101
v_mfma_f32_16x16x32_bf16 a[36:39],a[132:135],v[164:167],a[36:39]
v_mfma_f32_16x16x32_bf16 a[36:39],a[128:131],v[132:135],a[36:39]
v_mfma_f32_16x16x32_bf16 a[40:43],a[128:131],v[144:147],a[40:43]
v_mfma_f32_16x16x32_bf16 a[40:43],a[132:135],v[176:179],a[40:43]
v_mfma_f32_16x16x32_bf16 a[40:43],a[136:139],v[208:211],a[40:43]
v_mfma_f32_16x16x32_bf16 a[40:43],a[140:143],v[240:243],a[40:43]
v_mfma_f32_16x16x32_bf16 a[44:47],a[140:143],v[244:247],a[44:47]
ds_read_b64_tr_b16 a[140:141],v1 offset:30784
ds_read_b64_tr_b16 a[142:143],v1 offset:35904
v_mfma_f32_16x16x32_bf16 a[44:47],a[136:139],v[212:215],a[44:47]
ds_read_b64_tr_b16 a[136:137],v1 offset:20544
v_mfma_f32_16x16x32_bf16 a[44:47],a[132:135],v[180:183],a[44:47]
ds_read_b64_tr_b16 a[132:133],v1 offset:10304
v_mfma_f32_16x16x32_bf16 a[44:47],a[128:131],v[148:151],a[44:47]
s_waitcnt lgkmcnt(4)
buffer_load_dwordx4 v8,s[72:75],s83 offen offset:1024 lds
v_mfma_f32_16x16x32_bf16 a[48:51],a[144:147],v[128:131],a[48:51]
ds_read_b64_tr_b16 a[128:129],v1 offset:64
ds_read_b64_tr_b16 a[130:131],v1 offset:5184
v_mfma_f32_16x16x32_bf16 a[48:51],a[148:151],v[160:163],a[48:51]
ds_read_b64_tr_b16 a[134:135],v1 offset:15424
v_mfma_f32_16x16x32_bf16 a[48:51],a[152:155],v[192:195],a[48:51]
ds_read_b64_tr_b16 a[138:139],v1 offset:25664
v_mfma_f32_16x16x32_bf16 a[48:51],a[156:159],v[224:227],a[48:51]
s_lshl_b32 s94,s70,4
v_bfe_u32 v24,v6,4,2
v_min_u32 v24,2,v24
v_mfma_f32_16x16x32_bf16 a[52:55],a[156:159],v[228:231],a[52:55]
s_add_u32 s94,s94,s40
v_add_u32 v24,s71,v24
v_add_u32 v24,1,v24
v_mfma_f32_16x16x32_bf16 a[52:55],a[152:155],v[196:199],a[52:55]
s_lshl_b32 s94,s94,4
v_and_b32 v24,3,v24
v_lshlrev_b32 v24,6,v24
v_mfma_f32_16x16x32_bf16 a[52:55],a[148:151],v[164:167],a[52:55]
v_mfma_f32_16x16x32_bf16 a[52:55],a[144:147],v[132:135],a[52:55]
buffer_load_dwordx4 v[136:139],v6,s[28:31],s76 offen sc1
v_mfma_f32_16x16x32_bf16 a[56:59],a[144:147],v[144:147],a[56:59]
v_mfma_f32_16x16x32_bf16 a[56:59],a[148:151],v[176:179],a[56:59]
v_mfma_f32_16x16x32_bf16 a[56:59],a[152:155],v[208:211],a[56:59]
v_mfma_f32_16x16x32_bf16 a[56:59],a[156:159],v[240:243],a[56:59]
v_mfma_f32_16x16x32_bf16 a[60:63],a[156:159],v[244:247],a[60:63]
ds_read_b64_tr_b16 a[156:157],v2 offset:30816
ds_read_b64_tr_b16 a[158:159],v2 offset:35936
v_mfma_f32_16x16x32_bf16 a[60:63],a[152:155],v[212:215],a[60:63]
buffer_load_dwordx4 v[140:143],v6,s[28:31],s76 offen offset:1024 sc1
v_mfma_f32_16x16x32_bf16 a[60:63],a[148:151],v[180:183],a[60:63]
ds_read_b64_tr_b16 a[148:149],v2 offset:10336
ds_read_b64_tr_b16 a[150:151],v2 offset:15456
v_mfma_f32_16x16x32_bf16 a[60:63],a[144:147],v[148:151],a[60:63]
ds_read_b64_tr_b16 a[144:145],v2 offset:96
v_mfma_f32_16x16x32_bf16 a[64:67],a[160:163],v[128:131],a[64:67]
ds_read_b64_tr_b16 a[146:147],v2 offset:5216
v_mfma_f32_16x16x32_bf16 a[64:67],a[164:167],v[160:163],a[64:67]
buffer_load_dwordx4 v9,s[72:75],s83 offen offset:2048 lds
v_mfma_f32_16x16x32_bf16 a[64:67],a[168:171],v[192:195],a[64:67]
ds_read_b64_tr_b16 a[152:153],v2 offset:20576
ds_read_b64_tr_b16 a[154:155],v2 offset:25696
v_mfma_f32_16x16x32_bf16 a[64:67],a[172:175],v[224:227],a[64:67]
v_mfma_f32_16x16x32_bf16 a[68:71],a[172:175],v[228:231],a[68:71]
v_mfma_f32_16x16x32_bf16 a[68:71],a[168:171],v[196:199],a[68:71]
buffer_load_dwordx4 v[152:155],v6,s[28:31],s76 offen offset:2048 sc1
v_mfma_f32_16x16x32_bf16 a[68:71],a[164:167],v[164:167],a[68:71]
v_mfma_f32_16x16x32_bf16 a[68:71],a[160:163],v[132:135],a[68:71]
v_mfma_f32_16x16x32_bf16 a[72:75],a[160:163],v[144:147],a[72:75]
v_mfma_f32_16x16x32_bf16 a[72:75],a[164:167],v[176:179],a[72:75]
buffer_load_dwordx4 v[156:159],v6,s[28:31],s76 offen offset:3072 sc1
v_mfma_f32_16x16x32_bf16 a[72:75],a[168:171],v[208:211],a[72:75]
v_mfma_f32_16x16x32_bf16 a[72:75],a[172:175],v[240:243],a[72:75]
v_mfma_f32_16x16x32_bf16 a[76:79],a[172:175],v[244:247],a[76:79]
ds_read_b64_tr_b16 a[172:173],v1 offset:30848
ds_read_b64_tr_b16 a[174:175],v1 offset:35968
v_mfma_f32_16x16x32_bf16 a[76:79],a[168:171],v[212:215],a[76:79]
buffer_load_dwordx4 v10,s[72:75],s83 offen offset:3072 lds
v_mfma_f32_16x16x32_bf16 a[76:79],a[164:167],v[180:183],a[76:79]
ds_read_b64_tr_b16 a[164:165],v1 offset:10368
ds_read_b64_tr_b16 a[166:167],v1 offset:15488
v_mfma_f32_16x16x32_bf16 a[76:79],a[160:163],v[148:151],a[76:79]
ds_read_b64_tr_b16 a[160:161],v1 offset:128
v_mfma_f32_16x16x32_bf16 a[80:83],a[176:179],v[128:131],a[80:83]
ds_read_b64_tr_b16 a[162:163],v1 offset:5248
v_mfma_f32_16x16x32_bf16 a[80:83],a[180:183],v[160:163],a[80:83]
buffer_load_dwordx4 v[168:171],v6,s[28:31],s77 offen sc1
v_mfma_f32_16x16x32_bf16 a[80:83],a[184:187],v[192:195],a[80:83]
ds_read_b64_tr_b16 a[168:169],v1 offset:20608
ds_read_b64_tr_b16 a[170:171],v1 offset:25728
v_mfma_f32_16x16x32_bf16 a[80:83],a[188:191],v[224:227],a[80:83]
v_mfma_f32_16x16x32_bf16 a[84:87],a[188:191],v[228:231],a[84:87]
v_mfma_f32_16x16x32_bf16 a[84:87],a[184:187],v[196:199],a[84:87]
buffer_load_dwordx4 v[172:175],v6,s[28:31],s77 offen offset:1024 sc1
v_mfma_f32_16x16x32_bf16 a[84:87],a[180:183],v[164:167],a[84:87]
v_mfma_f32_16x16x32_bf16 a[84:87],a[176:179],v[132:135],a[84:87]
v_mfma_f32_16x16x32_bf16 a[88:91],a[176:179],v[144:147],a[88:91]
v_mfma_f32_16x16x32_bf16 a[88:91],a[180:183],v[176:179],a[88:91]
s_add_u32 m0,s82,9216
v_mfma_f32_16x16x32_bf16 a[88:91],a[184:187],v[208:211],a[88:91]
v_mfma_f32_16x16x32_bf16 a[88:91],a[188:191],v[240:243],a[88:91]
buffer_load_dwordx2 v[26:27],v24,s[64:67],s94 offen offset:1024 sc1
v_mfma_f32_16x16x32_bf16 a[92:95],a[188:191],v[244:247],a[92:95]
ds_read_b64_tr_b16 a[188:189],v2 offset:30880
ds_read_b64_tr_b16 a[190:191],v2 offset:36000
v_mfma_f32_16x16x32_bf16 a[92:95],a[184:187],v[212:215],a[92:95]
ds_read_b64_tr_b16 a[184:185],v2 offset:20640
v_mfma_f32_16x16x32_bf16 a[92:95],a[180:183],v[180:183],a[92:95]
ds_read_b64_tr_b16 a[180:181],v2 offset:10400
v_mfma_f32_16x16x32_bf16 a[92:95],a[176:179],v[148:151],a[92:95]
s_waitcnt vmcnt(13)
)A"
R"A(buffer_load_dwordx4 v[184:187],v6,s[28:31],s77 offen offset:2048 sc1
v_mfma_f32_16x16x32_bf16 a[192:195],a[96:99],v[64:67],a[192:195]
s_lshl_b32 s43,s41,2
ds_read_b64_tr_b16 a[176:177],v2 offset:160
ds_read_b64_tr_b16 a[178:179],v2 offset:5280
v_mfma_f32_16x16x32_bf16 a[192:195],a[100:103],v[68:71],a[192:195]
s_add_u32 s84,s70,1
s_add_u32 s85,s70,2
ds_read_b64_tr_b16 a[182:183],v2 offset:15520
v_mfma_f32_16x16x32_bf16 a[192:195],a[104:107],v[72:75],a[192:195]
s_add_u32 s86,s70,3
s_add_u32 s43,s43,s71
ds_read_b64_tr_b16 a[186:187],v2 offset:25760
v_mfma_f32_16x16x32_bf16 a[192:195],a[108:111],v[76:79],a[192:195]
s_and_b32 s84,s84,3
buffer_load_dwordx4 v11,s[72:75],s83 offen lds
v_mfma_f32_16x16x32_bf16 a[196:199],a[108:111],v[92:95],a[196:199]
s_and_b32 s85,s85,3
s_and_b32 s86,s86,3
s_mul_i32 s43,s43,245760
v_mfma_f32_16x16x32_bf16 a[196:199],a[104:107],v[88:91],a[196:199]
s_mul_i32 s84,s84,12
s_mul_i32 s85,s85,12
s_mul_i32 s86,s86,12
v_mfma_f32_16x16x32_bf16 a[196:199],a[100:103],v[84:87],a[196:199]
s_add_u32 s43,s43,134217728
s_mov_b64 s[32:33],s[12:13]
s_add_u32 s84,s84,0
v_mfma_f32_16x16x32_bf16 a[196:199],a[96:99],v[80:83],a[196:199]
s_add_u32 s85,s85,4
buffer_load_dwordx4 v[188:191],v6,s[28:31],s77 offen offset:3072 sc1
v_mfma_f32_16x16x32_bf16 a[200:203],a[96:99],v[96:99],a[200:203]
s_add_u32 s86,s86,8
s_add_u32 s84,s84,s40
s_add_u32 s85,s85,s40
v_mfma_f32_16x16x32_bf16 a[200:203],a[100:103],v[100:103],a[200:203]
s_add_u32 s86,s86,s40
s_add_u32 s32,s32,s43
s_addc_u32 s33,s33,0
v_mfma_f32_16x16x32_bf16 a[200:203],a[104:107],v[104:107],a[200:203]
s_mul_i32 s84,s84,5120
s_mul_i32 s85,s85,5120
s_mul_i32 s86,s86,5120
v_mfma_f32_16x16x32_bf16 a[200:203],a[108:111],v[108:111],a[200:203]
s_and_b32 s33,s33,0xffff
buffer_load_dwordx4 v[200:203],v6,s[28:31],s78 offen sc1
s_waitcnt vmcnt(14)
v_mfma_f32_16x16x32_bf16 a[204:207],a[108:111],v[124:127],a[204:207]
s_mov_b32 s34,245760
ds_read_b64_tr_b16 a[108:109],v1 offset:30912
ds_read_b64_tr_b16 a[110:111],v1 offset:36032
v_mfma_f32_16x16x32_bf16 a[204:207],a[104:107],v[120:123],a[204:207]
s_mov_b32 s35,0x20000
s_max_i32 s84,s84,s18
ds_read_b64_tr_b16 a[104:105],v1 offset:20672
v_mfma_f32_16x16x32_bf16 a[204:207],a[100:103],v[116:119],a[204:207]
s_max_i32 s85,s85,s19
s_max_i32 s86,s86,s100
ds_read_b64_tr_b16 a[100:101],v1 offset:10432
v_mfma_f32_16x16x32_bf16 a[204:207],a[96:99],v[112:115],a[204:207]
s_barrier
buffer_load_dwordx4 v[204:207],v6,s[28:31],s78 offen offset:1024 sc1
v_mfma_f32_16x16x32_bf16 a[208:211],a[112:115],v[64:67],a[208:211]
s_mul_i32 s44,s41,307200
ds_read_b64_tr_b16 a[96:97],v1 offset:192
ds_read_b64_tr_b16 a[98:99],v1 offset:5312
v_mfma_f32_16x16x32_bf16 a[208:211],a[116:119],v[68:71],a[208:211]
s_mov_b64 s[20:21],s[16:17]
ds_read_b64_tr_b16 a[102:103],v1 offset:15552
v_bfe_u32 v18,v6,6,2
v_mfma_f32_16x16x32_bf16 a[208:211],a[120:123],v[72:75],a[208:211]
s_lshl_b32 s43,s40,2
ds_read_b64_tr_b16 a[106:107],v1 offset:25792
v_lshrrev_b32 v19,8,v6
v_mfma_f32_16x16x32_bf16 a[208:211],a[124:127],v[76:79],a[208:211]
s_add_u32 s43,s43,s70
buffer_load_dwordx4 v[216:219],v6,s[28:31],s78 offen offset:2048 sc1
v_mfma_f32_16x16x32_bf16 a[212:215],a[124:127],v[92:95],a[212:215]
s_mul_i32 s43,s43,20480
v_xor_b32 v18,v18,v19
v_lshlrev_b32 v18,2,v18
v_mfma_f32_16x16x32_bf16 a[212:215],a[120:123],v[88:91],a[212:215]
s_mov_b32 s22,307200
v_bfe_u32 v19,v6,4,2
v_or_b32 v18,v18,v19
v_mfma_f32_16x16x32_bf16 a[212:215],a[116:119],v[84:87],a[212:215]
s_mov_b32 s23,0x20000
v_and_b32 v19,16,v6
v_mov_b32 v34,0x3f80
v_mfma_f32_16x16x32_bf16 a[212:215],a[112:115],v[80:83],a[212:215]
s_cmp_ge_u32 0,s71
s_cselect_b32 s76,3,1
s_lshl_b32 s76,s76,16
v_mfma_f32_16x16x32_bf16 a[216:219],a[112:115],v[96:99],a[216:219]
s_add_u32 s77,s76,4096
buffer_load_dwordx4 v[220:223],v6,s[28:31],s78 offen offset:3072 sc1
v_mfma_f32_16x16x32_bf16 a[216:219],a[116:119],v[100:103],a[216:219]
s_add_u32 s78,s76,8192
s_max_i32 s77,s77,s19
s_max_i32 s78,s78,s100
v_mfma_f32_16x16x32_bf16 a[216:219],a[120:123],v[104:107],a[216:219]
v_lshlrev_b32 v19,v19,v34
v_lshrrev_b32 v18,1,v18
v_mfma_f32_16x16x32_bf16 a[216:219],a[124:127],v[108:111],a[216:219]
v_cmp_eq_u32 vcc,0,v18
s_nop 1
v_cndmask_b32 v34,0,v19,vcc
v_mfma_f32_16x16x32_bf16 a[220:223],a[124:127],v[124:127],a[220:223]
ds_read_b64_tr_b16 a[124:125],v2 offset:30944
ds_read_b64_tr_b16 a[126:127],v2 offset:36064
v_mfma_f32_16x16x32_bf16 a[220:223],a[120:123],v[120:123],a[220:223]
ds_read_b64_tr_b16 a[120:121],v2 offset:20704
v_add_u32 v25,4096,v6
v_mfma_f32_16x16x32_bf16 a[220:223],a[116:119],v[116:119],a[220:223]
buffer_load_dwordx4 v[232:235],v6,s[28:31],s79 offen sc1
v_mfma_f32_16x16x32_bf16 a[220:223],a[112:115],v[112:115],a[220:223]
s_waitcnt lgkmcnt(3)
ds_read_b64_tr_b16 a[112:113],v2 offset:224
ds_read_b64_tr_b16 a[114:115],v2 offset:5344
v_mfma_f32_16x16x32_bf16 a[224:227],a[128:131],v[64:67],a[224:227]
ds_read_b64_tr_b16 a[116:117],v2 offset:10464
v_mov_b32 v36,0
v_mfma_f32_16x16x32_bf16 a[224:227],a[132:135],v[68:71],a[224:227]
ds_read_b64_tr_b16 a[118:119],v2 offset:15584
v_mov_b32 v37,0
v_mfma_f32_16x16x32_bf16 a[224:227],a[136:139],v[72:75],a[224:227]
ds_read_b64_tr_b16 a[122:123],v2 offset:25824
v_mov_b32 v32,0
v_mfma_f32_16x16x32_bf16 a[224:227],a[140:143],v[76:79],a[224:227]
v_cmp_eq_u32 vcc,1,v18
s_nop 1
v_cndmask_b32 v35,0,v19,vcc
v_mfma_f32_16x16x32_bf16 a[228:231],a[140:143],v[92:95],a[228:231]
buffer_load_dwordx4 v[236:239],v6,s[28:31],s79 offen offset:1024 sc1
v_mfma_f32_16x16x32_bf16 a[228:231],a[136:139],v[88:91],a[228:231]
v_mov_b32 v33,0
v_bfe_u32 v40,v6,4,4
v_mfma_f32_16x16x32_bf16 a[228:231],a[132:135],v[84:87],a[228:231]
v_mul_u32_u24 v40,1280,v40
v_bfe_u32 v41,v6,8,1
v_mfma_f32_16x16x32_bf16 a[228:231],a[128:131],v[80:83],a[228:231]
s_waitcnt vmcnt(9)
v_cmp_ne_u32 s[90:91],s68,v26
v_cmp_ne_u32 s[92:93],s69,v27
s_nop 0
s_or_b64 s[90:91],s[90:91],s[92:93]
s_cbranch_scc1 L_fr9_%=
L_fo9_%=:
v_mfma_f32_16x16x32_bf16 a[232:235],a[128:131],v[96:99],a[232:235]
s_add_u32 s20,s20,s44
s_addc_u32 s21,s21,0
v_lshl_add_u32 v40,v41,5,v40
v_mfma_f32_16x16x32_bf16 a[232:235],a[132:135],v[100:103],a[232:235]
s_mul_i32 s44,s71,320
s_and_b32 s21,s21,0xffff
v_lshrrev_b32 v41,9,v6
v_mfma_f32_16x16x32_bf16 a[232:235],a[136:139],v[104:107],a[232:235]
s_add_u32 s43,s43,s44
buffer_load_dwordx4 v[248:251],v6,s[28:31],s79 offen offset:2048 sc1
v_mfma_f32_16x16x32_bf16 a[232:235],a[140:143],v[108:111],a[232:235]
v_lshl_add_u32 v40,v41,4,v40
v_mfma_f32_16x16x32_bf16 a[236:239],a[140:143],v[124:127],a[236:239]
ds_read_b64_tr_b16 a[140:141],v1 offset:30976
ds_read_b64_tr_b16 a[142:143],v1 offset:36096
v_mfma_f32_16x16x32_bf16 a[236:239],a[136:139],v[120:123],a[236:239]
ds_read_b64_tr_b16 a[136:137],v1 offset:20736
v_mfma_f32_16x16x32_bf16 a[236:239],a[132:135],v[116:119],a[236:239]
ds_read_b64_tr_b16 a[132:133],v1 offset:10496
v_mfma_f32_16x16x32_bf16 a[236:239],a[128:131],v[112:115],a[236:239]
ds_read_b64_tr_b16 a[128:129],v1 offset:256
v_mfma_f32_16x16x32_bf16 a[240:243],a[144:147],v[64:67],a[240:243]
buffer_load_dwordx4 v[252:255],v6,s[28:31],s79 offen offset:3072 sc1
v_mfma_f32_16x16x32_bf16 a[240:243],a[148:151],v[68:71],a[240:243]
s_add_u32 s79,s76,12288
s_max_i32 s76,s76,s18
ds_read_b64_tr_b16 a[130:131],v1 offset:5376
v_mfma_f32_16x16x32_bf16 a[240:243],a[152:155],v[72:75],a[240:243]
s_max_i32 s79,s79,s101
ds_read_b64_tr_b16 a[134:135],v1 offset:15616
ds_read_b64_tr_b16 a[138:139],v1 offset:25856
v_mfma_f32_16x16x32_bf16 a[240:243],a[156:159],v[76:79],a[240:243]
v_subrev_u32 v1,40960,v1
v_mfma_f32_16x16x32_bf16 a[244:247],a[156:159],v[92:95],a[244:247]
buffer_load_dwordx4 v[128:131],v6,s[28:31],s76 offen sc1
v_mfma_f32_16x16x32_bf16 a[244:247],a[152:155],v[88:91],a[244:247]
v_mfma_f32_16x16x32_bf16 a[244:247],a[148:151],v[84:87],a[244:247]
v_mfma_f32_16x16x32_bf16 a[244:247],a[144:147],v[80:83],a[244:247]
v_mfma_f32_16x16x32_bf16 a[248:251],a[144:147],v[96:99],a[248:251]
buffer_load_dwordx4 v[160:163],v6,s[28:31],s76 offen offset:1024 sc1
v_mfma_f32_16x16x32_bf16 a[248:251],a[148:151],v[100:103],a[248:251]
v_mfma_f32_16x16x32_bf16 a[248:251],a[152:155],v[104:107],a[248:251]
v_mfma_f32_16x16x32_bf16 a[248:251],a[156:159],v[108:111],a[248:251]
v_mfma_f32_16x16x32_bf16 a[252:255],a[156:159],v[124:127],a[252:255]
buffer_load_dwordx4 v[192:195],v6,s[28:31],s76 offen offset:2048 sc1
v_mfma_f32_16x16x32_bf16 a[252:255],a[152:155],v[120:123],a[252:255]
ds_read_b64_tr_b16 a[152:153],v2 offset:20768
ds_read_b64_tr_b16 a[154:155],v2 offset:25888
v_mfma_f32_16x16x32_bf16 a[252:255],a[148:151],v[116:119],a[252:255]
ds_read_b64_tr_b16 a[148:149],v2 offset:10528
v_mfma_f32_16x16x32_bf16 a[252:255],a[144:147],v[112:115],a[252:255]
ds_read_b64_tr_b16 a[144:145],v2 offset:288
v_mfma_f32_16x16x32_bf16 a[0:3],a[160:163],v[64:67],a[0:3]
ds_read_b64_tr_b16 a[146:147],v2 offset:5408
v_mfma_f32_16x16x32_bf16 a[0:3],a[164:167],v[68:71],a[0:3]
buffer_load_dwordx4 v[224:227],v6,s[28:31],s76 offen offset:3072 sc1
v_mfma_f32_16x16x32_bf16 a[0:3],a[168:171],v[72:75],a[0:3]
ds_read_b64_tr_b16 a[150:151],v2 offset:15648
ds_read_b64_tr_b16 a[156:157],v2 offset:31008
v_mfma_f32_16x16x32_bf16 a[0:3],a[172:175],v[76:79],a[0:3]
ds_read_b64_tr_b16 a[158:159],v2 offset:36128
v_subrev_u32 v2,40960,v2
v_mfma_f32_16x16x32_bf16 a[4:7],a[172:175],v[92:95],a[4:7]
v_mfma_f32_16x16x32_bf16 a[4:7],a[168:171],v[88:91],a[4:7]
v_mfma_f32_16x16x32_bf16 a[4:7],a[164:167],v[84:87],a[4:7]
v_mfma_f32_16x16x32_bf16 a[4:7],a[160:163],v[80:83],a[4:7]
buffer_load_dwordx4 v[132:135],v6,s[28:31],s77 offen sc1
v_mfma_f32_16x16x32_bf16 a[8:11],a[160:163],v[96:99],a[8:11]
v_mfma_f32_16x16x32_bf16 a[8:11],a[164:167],v[100:103],a[8:11]
v_mfma_f32_16x16x32_bf16 a[8:11],a[168:171],v[104:107],a[8:11]
v_mfma_f32_16x16x32_bf16 a[8:11],a[172:175],v[108:111],a[8:11]
v_mfma_f32_16x16x32_bf16 a[12:15],a[172:175],v[124:127],a[12:15]
ds_read_b64_tr_b16 a[172:173],v1 offset:30720
ds_read_b64_tr_b16 a[174:175],v1 offset:35840
v_mfma_f32_16x16x32_bf16 a[12:15],a[168:171],v[120:123],a[12:15]
buffer_load_dwordx4 v[164:167],v6,s[28:31],s77 offen offset:1024 sc1
v_mfma_f32_16x16x32_bf16 a[12:15],a[164:167],v[116:119],a[12:15]
ds_read_b64_tr_b16 a[164:165],v1 offset:10240
ds_read_b64_tr_b16 a[166:167],v1 offset:15360
v_mfma_f32_16x16x32_bf16 a[12:15],a[160:163],v[112:115],a[12:15]
ds_read_b64_tr_b16 a[160:161],v1
v_mfma_f32_16x16x32_bf16 a[16:19],a[176:179],v[64:67],a[16:19]
ds_read_b64_tr_b16 a[162:163],v1 offset:5120
v_mfma_f32_16x16x32_bf16 a[16:19],a[180:183],v[68:71],a[16:19]
ds_read_b64_tr_b16 a[168:169],v1 offset:20480
v_mfma_f32_16x16x32_bf16 a[16:19],a[184:187],v[72:75],a[16:19]
ds_read_b64_tr_b16 a[170:171],v1 offset:25600
v_mfma_f32_16x16x32_bf16 a[16:19],a[188:191],v[76:79],a[16:19]
buffer_load_dwordx4 v[196:199],v6,s[28:31],s77 offen offset:2048 sc1
v_mfma_f32_16x16x32_bf16 a[20:23],a[188:191],v[92:95],a[20:23]
v_mfma_f32_16x16x32_bf16 a[20:23],a[184:187],v[88:91],a[20:23]
v_mfma_f32_16x16x32_bf16 a[20:23],a[180:183],v[84:87],a[20:23]
v_mfma_f32_16x16x32_bf16 a[20:23],a[176:179],v[80:83],a[20:23]
v_mfma_f32_16x16x32_bf16 a[24:27],a[176:179],v[96:99],a[24:27]
v_mfma_f32_16x16x32_bf16 a[24:27],a[180:183],v[100:103],a[24:27]
buffer_load_dwordx4 v[228:231],v6,s[28:31],s77 offen offset:3072 sc1
v_mfma_f32_16x16x32_bf16 a[24:27],a[184:187],v[104:107],a[24:27]
v_mfma_f32_16x16x32_bf16 a[24:27],a[188:191],v[108:111],a[24:27]
v_mfma_f32_16x16x32_bf16 a[28:31],a[188:191],v[124:127],a[28:31]
ds_read_b64_tr_b16 a[188:189],v2 offset:30752
ds_read_b64_tr_b16 a[190:191],v2 offset:35872
v_mfma_f32_16x16x32_bf16 a[28:31],a[184:187],v[120:123],a[28:31]
ds_read_b64_tr_b16 a[184:185],v2 offset:20512
v_mfma_f32_16x16x32_bf16 a[28:31],a[180:183],v[116:119],a[28:31]
ds_read_b64_tr_b16 a[180:181],v2 offset:10272
v_mfma_f32_16x16x32_bf16 a[28:31],a[176:179],v[112:115],a[28:31]
buffer_load_dwordx4 v[144:147],v6,s[28:31],s78 offen sc1
v_mfma_f32_16x16x32_bf16 a[32:35],a[96:99],v[64:67],a[32:35]
ds_read_b64_tr_b16 a[176:177],v2 offset:32
ds_read_b64_tr_b16 a[178:179],v2 offset:5152
v_mfma_f32_16x16x32_bf16 a[32:35],a[100:103],v[68:71],a[32:35]
)A"
R"A(ds_read_b64_tr_b16 a[182:183],v2 offset:15392
v_mfma_f32_16x16x32_bf16 a[32:35],a[104:107],v[72:75],a[32:35]
ds_read_b64_tr_b16 a[186:187],v2 offset:25632
v_mfma_f32_16x16x32_bf16 a[32:35],a[108:111],v[76:79],a[32:35]
v_mfma_f32_16x16x32_bf16 a[36:39],a[108:111],v[92:95],a[36:39]
v_mfma_f32_16x16x32_bf16 a[36:39],a[104:107],v[88:91],a[36:39]
buffer_load_dwordx4 v[176:179],v6,s[28:31],s78 offen offset:1024 sc1
v_mfma_f32_16x16x32_bf16 a[36:39],a[100:103],v[84:87],a[36:39]
v_mfma_f32_16x16x32_bf16 a[36:39],a[96:99],v[80:83],a[36:39]
v_mfma_f32_16x16x32_bf16 a[40:43],a[96:99],v[96:99],a[40:43]
v_mfma_f32_16x16x32_bf16 a[40:43],a[100:103],v[100:103],a[40:43]
v_mfma_f32_16x16x32_bf16 a[40:43],a[104:107],v[104:107],a[40:43]
v_mfma_f32_16x16x32_bf16 a[40:43],a[108:111],v[108:111],a[40:43]
buffer_load_dwordx4 v[208:211],v6,s[28:31],s78 offen offset:2048 sc1
v_mfma_f32_16x16x32_bf16 a[44:47],a[108:111],v[124:127],a[44:47]
ds_read_b64_tr_b16 a[108:109],v1 offset:30784
ds_read_b64_tr_b16 a[110:111],v1 offset:35904
v_mfma_f32_16x16x32_bf16 a[44:47],a[104:107],v[120:123],a[44:47]
ds_read_b64_tr_b16 a[104:105],v1 offset:20544
v_mfma_f32_16x16x32_bf16 a[44:47],a[100:103],v[116:119],a[44:47]
ds_read_b64_tr_b16 a[100:101],v1 offset:10304
v_mfma_f32_16x16x32_bf16 a[44:47],a[96:99],v[112:115],a[44:47]
s_waitcnt lgkmcnt(4)
ds_read_b64_tr_b16 a[96:97],v1 offset:64
v_mfma_f32_16x16x32_bf16 a[48:51],a[112:115],v[64:67],a[48:51]
ds_read_b64_tr_b16 a[98:99],v1 offset:5184
ds_read_b64_tr_b16 a[102:103],v1 offset:15424
v_mfma_f32_16x16x32_bf16 a[48:51],a[116:119],v[68:71],a[48:51]
buffer_load_dwordx4 v[240:243],v6,s[28:31],s78 offen offset:3072 sc1
v_mfma_f32_16x16x32_bf16 a[48:51],a[120:123],v[72:75],a[48:51]
ds_read_b64_tr_b16 a[106:107],v1 offset:25664
v_mfma_f32_16x16x32_bf16 a[48:51],a[124:127],v[76:79],a[48:51]
v_mfma_f32_16x16x32_bf16 a[52:55],a[124:127],v[92:95],a[52:55]
v_mfma_f32_16x16x32_bf16 a[52:55],a[120:123],v[88:91],a[52:55]
v_mfma_f32_16x16x32_bf16 a[52:55],a[116:119],v[84:87],a[52:55]
v_mfma_f32_16x16x32_bf16 a[52:55],a[112:115],v[80:83],a[52:55]
buffer_load_dwordx4 v[148:151],v6,s[28:31],s79 offen sc1
v_mfma_f32_16x16x32_bf16 a[56:59],a[112:115],v[96:99],a[56:59]
v_mfma_f32_16x16x32_bf16 a[56:59],a[116:119],v[100:103],a[56:59]
s_cmp_ge_u32 1,s71
s_cselect_b32 s80,5,3
s_mul_i32 s80,s80,163840
v_mfma_f32_16x16x32_bf16 a[56:59],a[120:123],v[104:107],a[56:59]
s_add_u32 s80,s80,s81
s_add_u32 s83,s80,20480
v_mfma_f32_16x16x32_bf16 a[56:59],a[124:127],v[108:111],a[56:59]
v_mfma_f32_16x16x32_bf16 a[60:63],a[124:127],v[124:127],a[60:63]
ds_read_b64_tr_b16 a[124:125],v2 offset:30816
ds_read_b64_tr_b16 a[126:127],v2 offset:35936
v_mfma_f32_16x16x32_bf16 a[60:63],a[120:123],v[120:123],a[60:63]
buffer_load_dwordx4 v[180:183],v6,s[28:31],s79 offen offset:1024 sc1
v_mfma_f32_16x16x32_bf16 a[60:63],a[116:119],v[116:119],a[60:63]
ds_read_b64_tr_b16 a[116:117],v2 offset:10336
ds_read_b64_tr_b16 a[118:119],v2 offset:15456
v_mfma_f32_16x16x32_bf16 a[60:63],a[112:115],v[112:115],a[60:63]
ds_read_b64_tr_b16 a[112:113],v2 offset:96
v_mfma_f32_16x16x32_bf16 a[64:67],a[128:131],v[64:67],a[64:67]
ds_read_b64_tr_b16 a[114:115],v2 offset:5216
v_mfma_f32_16x16x32_bf16 a[64:67],a[132:135],v[68:71],a[64:67]
ds_read_b64_tr_b16 a[120:121],v2 offset:20576
v_mfma_f32_16x16x32_bf16 a[64:67],a[136:139],v[72:75],a[64:67]
ds_read_b64_tr_b16 a[122:123],v2 offset:25696
v_mfma_f32_16x16x32_bf16 a[64:67],a[140:143],v[76:79],a[64:67]
buffer_load_dwordx4 v[212:215],v6,s[28:31],s79 offen offset:2048 sc1
v_mfma_f32_16x16x32_bf16 a[68:71],a[140:143],v[92:95],a[68:71]
v_mfma_f32_16x16x32_bf16 a[68:71],a[136:139],v[88:91],a[68:71]
v_mfma_f32_16x16x32_bf16 a[68:71],a[132:135],v[84:87],a[68:71]
v_mfma_f32_16x16x32_bf16 a[68:71],a[128:131],v[80:83],a[68:71]
v_mfma_f32_16x16x32_bf16 a[72:75],a[128:131],v[96:99],a[72:75]
v_mfma_f32_16x16x32_bf16 a[72:75],a[132:135],v[100:103],a[72:75]
buffer_load_dwordx4 v[244:247],v6,s[28:31],s79 offen offset:3072 sc1
v_mfma_f32_16x16x32_bf16 a[72:75],a[136:139],v[104:107],a[72:75]
v_mfma_f32_16x16x32_bf16 a[72:75],a[140:143],v[108:111],a[72:75]
v_mfma_f32_16x16x32_bf16 a[76:79],a[140:143],v[124:127],a[76:79]
ds_read_b64_tr_b16 a[140:141],v1 offset:30848
ds_read_b64_tr_b16 a[142:143],v1 offset:35968
v_mfma_f32_16x16x32_bf16 a[76:79],a[136:139],v[120:123],a[76:79]
ds_read_b64_tr_b16 a[136:137],v1 offset:20608
v_mfma_f32_16x16x32_bf16 a[76:79],a[132:135],v[116:119],a[76:79]
s_add_u32 m0,s82,40960
ds_read_b64_tr_b16 a[132:133],v1 offset:10368
v_mfma_f32_16x16x32_bf16 a[76:79],a[128:131],v[112:115],a[76:79]
ds_read_b64_tr_b16 a[128:129],v1 offset:128
v_mfma_f32_16x16x32_bf16 a[80:83],a[144:147],v[64:67],a[80:83]
ds_read_b64_tr_b16 a[130:131],v1 offset:5248
ds_read_b64_tr_b16 a[134:135],v1 offset:15488
v_mfma_f32_16x16x32_bf16 a[80:83],a[148:151],v[68:71],a[80:83]
buffer_load_dwordx4 v7,s[72:75],s80 offen lds
v_mfma_f32_16x16x32_bf16 a[80:83],a[152:155],v[72:75],a[80:83]
ds_read_b64_tr_b16 a[138:139],v1 offset:25728
v_mfma_f32_16x16x32_bf16 a[80:83],a[156:159],v[76:79],a[80:83]
v_mfma_f32_16x16x32_bf16 a[84:87],a[156:159],v[92:95],a[84:87]
v_mfma_f32_16x16x32_bf16 a[84:87],a[152:155],v[88:91],a[84:87]
v_mfma_f32_16x16x32_bf16 a[84:87],a[148:151],v[84:87],a[84:87]
buffer_load_dwordx4 v8,s[72:75],s80 offen offset:1024 lds
v_mfma_f32_16x16x32_bf16 a[84:87],a[144:147],v[80:83],a[84:87]
v_mfma_f32_16x16x32_bf16 a[88:91],a[144:147],v[96:99],a[88:91]
v_mfma_f32_16x16x32_bf16 a[88:91],a[148:151],v[100:103],a[88:91]
v_mfma_f32_16x16x32_bf16 a[88:91],a[152:155],v[104:107],a[88:91]
v_mfma_f32_16x16x32_bf16 a[88:91],a[156:159],v[108:111],a[88:91]
buffer_load_dwordx4 v9,s[72:75],s80 offen offset:2048 lds
v_mfma_f32_16x16x32_bf16 a[92:95],a[156:159],v[124:127],a[92:95]
ds_read_b64_tr_b16 a[156:157],v2 offset:30880
ds_read_b64_tr_b16 a[158:159],v2 offset:36000
v_mfma_f32_16x16x32_bf16 a[92:95],a[152:155],v[120:123],a[92:95]
ds_read_b64_tr_b16 a[152:153],v2 offset:20640
v_mfma_f32_16x16x32_bf16 a[92:95],a[148:151],v[116:119],a[92:95]
ds_read_b64_tr_b16 a[148:149],v2 offset:10400
v_mfma_f32_16x16x32_bf16 a[92:95],a[144:147],v[112:115],a[92:95]
ds_read_b64_tr_b16 a[144:145],v2 offset:160
v_mfma_f32_16x16x32_bf16 a[192:195],a[160:163],v[136:139],a[192:195]
buffer_load_dwordx4 v10,s[72:75],s80 offen offset:3072 lds
v_mfma_f32_16x16x32_bf16 a[192:195],a[164:167],v[140:143],a[192:195]
ds_read_b64_tr_b16 a[146:147],v2 offset:5280
ds_read_b64_tr_b16 a[150:151],v2 offset:15520
v_mfma_f32_16x16x32_bf16 a[192:195],a[168:171],v[152:155],a[192:195]
s_add_u32 m0,s82,45056
ds_read_b64_tr_b16 a[154:155],v2 offset:25760
v_mfma_f32_16x16x32_bf16 a[192:195],a[172:175],v[156:159],a[192:195]
s_waitcnt vmcnt(12)
v_mfma_f32_16x16x32_bf16 a[196:199],a[172:175],v[188:191],a[196:199]
v_mfma_f32_16x16x32_bf16 a[196:199],a[168:171],v[184:187],a[196:199]
buffer_load_dwordx4 v11,s[72:75],s80 offen lds
v_mfma_f32_16x16x32_bf16 a[196:199],a[164:167],v[172:175],a[196:199]
v_mfma_f32_16x16x32_bf16 a[196:199],a[160:163],v[168:171],a[196:199]
s_add_u32 m0,s82,46080
v_mfma_f32_16x16x32_bf16 a[200:203],a[160:163],v[200:203],a[200:203]
v_mfma_f32_16x16x32_bf16 a[200:203],a[164:167],v[204:207],a[200:203]
v_mfma_f32_16x16x32_bf16 a[200:203],a[168:171],v[216:219],a[200:203]
buffer_load_dwordx4 v7,s[72:75],s83 offen lds
v_mfma_f32_16x16x32_bf16 a[200:203],a[172:175],v[220:223],a[200:203]
v_mfma_f32_16x16x32_bf16 a[204:207],a[172:175],v[252:255],a[204:207]
ds_read_b64_tr_b16 a[172:173],v1 offset:30912
ds_read_b64_tr_b16 a[174:175],v1 offset:36032
v_mfma_f32_16x16x32_bf16 a[204:207],a[168:171],v[248:251],a[204:207]
ds_read_b64_tr_b16 a[168:169],v1 offset:20672
v_mfma_f32_16x16x32_bf16 a[204:207],a[164:167],v[236:239],a[204:207]
ds_read_b64_tr_b16 a[164:165],v1 offset:10432
v_mfma_f32_16x16x32_bf16 a[204:207],a[160:163],v[232:235],a[204:207]
s_barrier
buffer_load_dwordx4 v8,s[72:75],s83 offen offset:1024 lds
v_mfma_f32_16x16x32_bf16 a[208:211],a[176:179],v[136:139],a[208:211]
s_cmp_ge_u32 1,s71
s_cselect_b32 s76,5,3
s_lshl_b32 s76,s76,16
v_mfma_f32_16x16x32_bf16 a[208:211],a[180:183],v[140:143],a[208:211]
s_add_u32 s77,s76,4096
s_add_u32 s78,s76,8192
s_add_u32 s79,s76,12288
v_mfma_f32_16x16x32_bf16 a[208:211],a[184:187],v[152:155],a[208:211]
s_max_i32 s76,s76,s18
s_max_i32 s77,s77,s19
s_max_i32 s78,s78,s100
v_mfma_f32_16x16x32_bf16 a[208:211],a[188:191],v[156:159],a[208:211]
s_max_i32 s79,s79,s101
buffer_load_dwordx4 v9,s[72:75],s83 offen offset:2048 lds
v_mfma_f32_16x16x32_bf16 a[212:215],a[188:191],v[188:191],a[212:215]
ds_read_b64_tr_b16 a[160:161],v1 offset:192
ds_read_b64_tr_b16 a[162:163],v1 offset:5312
v_mfma_f32_16x16x32_bf16 a[212:215],a[184:187],v[184:187],a[212:215]
ds_read_b64_tr_b16 a[166:167],v1 offset:15552
v_mfma_f32_16x16x32_bf16 a[212:215],a[180:183],v[172:175],a[212:215]
ds_read_b64_tr_b16 a[170:171],v1 offset:25792
v_mfma_f32_16x16x32_bf16 a[212:215],a[176:179],v[168:171],a[212:215]
v_mfma_f32_16x16x32_bf16 a[216:219],a[176:179],v[200:203],a[216:219]
buffer_load_dwordx4 v10,s[72:75],s83 offen offset:3072 lds
v_mfma_f32_16x16x32_bf16 a[216:219],a[180:183],v[204:207],a[216:219]
v_mfma_f32_16x16x32_bf16 a[216:219],a[184:187],v[216:219],a[216:219]
s_add_u32 m0,s82,50176
v_mfma_f32_16x16x32_bf16 a[216:219],a[188:191],v[220:223],a[216:219]
v_mfma_f32_16x16x32_bf16 a[220:223],a[188:191],v[252:255],a[220:223]
ds_read_b64_tr_b16 a[188:189],v2 offset:30944
ds_read_b64_tr_b16 a[190:191],v2 offset:36064
v_mfma_f32_16x16x32_bf16 a[220:223],a[184:187],v[248:251],a[220:223]
buffer_load_dwordx4 v11,s[72:75],s83 offen lds
v_mfma_f32_16x16x32_bf16 a[220:223],a[180:183],v[236:239],a[220:223]
ds_read_b64_tr_b16 a[180:181],v2 offset:10464
ds_read_b64_tr_b16 a[182:183],v2 offset:15584
v_mfma_f32_16x16x32_bf16 a[220:223],a[176:179],v[232:235],a[220:223]
s_waitcnt lgkmcnt(4)
ds_read_b64_tr_b16 a[176:177],v2 offset:224
v_mfma_f32_16x16x32_bf16 a[224:227],a[96:99],v[136:139],a[224:227]
ds_read_b64_tr_b16 a[178:179],v2 offset:5344
v_mfma_f32_16x16x32_bf16 a[224:227],a[100:103],v[140:143],a[224:227]
ds_read_b64_tr_b16 a[184:185],v2 offset:20704
v_mfma_f32_16x16x32_bf16 a[224:227],a[104:107],v[152:155],a[224:227]
ds_read_b64_tr_b16 a[186:187],v2 offset:25824
v_mfma_f32_16x16x32_bf16 a[224:227],a[108:111],v[156:159],a[224:227]
v_mfma_f32_16x16x32_bf16 a[228:231],a[108:111],v[188:191],a[228:231]
v_mfma_f32_16x16x32_bf16 a[228:231],a[104:107],v[184:187],a[228:231]
v_mfma_f32_16x16x32_bf16 a[228:231],a[100:103],v[172:175],a[228:231]
v_mfma_f32_16x16x32_bf16 a[228:231],a[96:99],v[168:171],a[228:231]
buffer_load_dwordx4 v[64:67],v6,s[28:31],s76 offen sc1
v_mfma_f32_16x16x32_bf16 a[232:235],a[96:99],v[200:203],a[232:235]
v_mfma_f32_16x16x32_bf16 a[232:235],a[100:103],v[204:207],a[232:235]
v_mfma_f32_16x16x32_bf16 a[232:235],a[104:107],v[216:219],a[232:235]
v_mfma_f32_16x16x32_bf16 a[232:235],a[108:111],v[220:223],a[232:235]
v_mfma_f32_16x16x32_bf16 a[236:239],a[108:111],v[252:255],a[236:239]
buffer_load_dwordx4 v[68:71],v6,s[28:31],s76 offen offset:1024 sc1
v_mfma_f32_16x16x32_bf16 a[236:239],a[104:107],v[248:251],a[236:239]
ds_read_b64_tr_b16 a[104:105],v1 offset:20736
ds_read_b64_tr_b16 a[106:107],v1 offset:25856
v_mfma_f32_16x16x32_bf16 a[236:239],a[100:103],v[236:239],a[236:239]
ds_read_b64_tr_b16 a[100:101],v1 offset:10496
v_mfma_f32_16x16x32_bf16 a[236:239],a[96:99],v[232:235],a[236:239]
ds_read_b64_tr_b16 a[96:97],v1 offset:256
v_mfma_f32_16x16x32_bf16 a[240:243],a[112:115],v[136:139],a[240:243]
ds_read_b64_tr_b16 a[98:99],v1 offset:5376
v_mfma_f32_16x16x32_bf16 a[240:243],a[116:119],v[140:143],a[240:243]
buffer_load_dwordx4 v[72:75],v6,s[28:31],s76 offen offset:2048 sc1
v_mfma_f32_16x16x32_bf16 a[240:243],a[120:123],v[152:155],a[240:243]
ds_read_b64_tr_b16 a[102:103],v1 offset:15616
ds_read_b64_tr_b16 a[108:109],v1 offset:30976
v_mfma_f32_16x16x32_bf16 a[240:243],a[124:127],v[156:159],a[240:243]
ds_read_b64_tr_b16 a[110:111],v1 offset:36096
v_subrev_u32 v1,81920,v1
v_mfma_f32_16x16x32_bf16 a[244:247],a[124:127],v[188:191],a[244:247]
v_add_u32 v42,40960,v1
v_add_u32 v44,122880,v1
)A"
R"A(v_mfma_f32_16x16x32_bf16 a[244:247],a[120:123],v[184:187],a[244:247]
v_mfma_f32_16x16x32_bf16 a[244:247],a[116:119],v[172:175],a[244:247]
buffer_load_dwordx4 v[76:79],v6,s[28:31],s76 offen offset:3072 sc1
v_mfma_f32_16x16x32_bf16 a[244:247],a[112:115],v[168:171],a[244:247]
v_mfma_f32_16x16x32_bf16 a[248:251],a[112:115],v[200:203],a[248:251]
v_mfma_f32_16x16x32_bf16 a[248:251],a[116:119],v[204:207],a[248:251]
v_mfma_f32_16x16x32_bf16 a[248:251],a[120:123],v[216:219],a[248:251]
v_mfma_f32_16x16x32_bf16 a[248:251],a[124:127],v[220:223],a[248:251]
buffer_load_dwordx4 v[80:83],v6,s[28:31],s77 offen sc1
v_mfma_f32_16x16x32_bf16 a[252:255],a[124:127],v[252:255],a[252:255]
ds_read_b64_tr_b16 a[124:125],v2 offset:31008
ds_read_b64_tr_b16 a[126:127],v2 offset:36128
v_mfma_f32_16x16x32_bf16 a[252:255],a[120:123],v[248:251],a[252:255]
ds_read_b64_tr_b16 a[120:121],v2 offset:20768
v_mfma_f32_16x16x32_bf16 a[252:255],a[116:119],v[236:239],a[252:255]
ds_read_b64_tr_b16 a[116:117],v2 offset:10528
v_mfma_f32_16x16x32_bf16 a[252:255],a[112:115],v[232:235],a[252:255]
ds_read_b64_tr_b16 a[112:113],v2 offset:288
v_mfma_f32_16x16x32_bf16 a[0:3],a[128:131],v[136:139],a[0:3]
buffer_load_dwordx4 v[84:87],v6,s[28:31],s77 offen offset:1024 sc1
v_mfma_f32_16x16x32_bf16 a[0:3],a[132:135],v[140:143],a[0:3]
ds_read_b64_tr_b16 a[114:115],v2 offset:5408
ds_read_b64_tr_b16 a[118:119],v2 offset:15648
v_mfma_f32_16x16x32_bf16 a[0:3],a[136:139],v[152:155],a[0:3]
ds_read_b64_tr_b16 a[122:123],v2 offset:25888
v_subrev_u32 v2,81920,v2
v_mfma_f32_16x16x32_bf16 a[0:3],a[140:143],v[156:159],a[0:3]
v_add_u32 v43,40960,v2
v_add_u32 v45,122880,v2
v_mfma_f32_16x16x32_bf16 a[4:7],a[140:143],v[188:191],a[4:7]
v_mfma_f32_16x16x32_bf16 a[4:7],a[136:139],v[184:187],a[4:7]
buffer_load_dwordx4 v[88:91],v6,s[28:31],s77 offen offset:2048 sc1
v_mfma_f32_16x16x32_bf16 a[4:7],a[132:135],v[172:175],a[4:7]
v_mfma_f32_16x16x32_bf16 a[4:7],a[128:131],v[168:171],a[4:7]
v_mfma_f32_16x16x32_bf16 a[8:11],a[128:131],v[200:203],a[8:11]
v_mfma_f32_16x16x32_bf16 a[8:11],a[132:135],v[204:207],a[8:11]
v_mfma_f32_16x16x32_bf16 a[8:11],a[136:139],v[216:219],a[8:11]
buffer_load_dwordx4 v[92:95],v6,s[28:31],s77 offen offset:3072 sc1
v_mfma_f32_16x16x32_bf16 a[8:11],a[140:143],v[220:223],a[8:11]
v_mfma_f32_16x16x32_bf16 a[12:15],a[140:143],v[252:255],a[12:15]
ds_read_b64_tr_b16 a[140:141],v1 offset:30720
ds_read_b64_tr_b16 a[142:143],v1 offset:35840
v_mfma_f32_16x16x32_bf16 a[12:15],a[136:139],v[248:251],a[12:15]
ds_read_b64_tr_b16 a[136:137],v1 offset:20480
v_mfma_f32_16x16x32_bf16 a[12:15],a[132:135],v[236:239],a[12:15]
ds_read_b64_tr_b16 a[132:133],v1 offset:10240
v_mfma_f32_16x16x32_bf16 a[12:15],a[128:131],v[232:235],a[12:15]
buffer_load_dwordx4 v[96:99],v6,s[28:31],s78 offen sc1
v_mfma_f32_16x16x32_bf16 a[16:19],a[144:147],v[136:139],a[16:19]
ds_read_b64_tr_b16 a[128:129],v1
ds_read_b64_tr_b16 a[130:131],v1 offset:5120
v_mfma_f32_16x16x32_bf16 a[16:19],a[148:151],v[140:143],a[16:19]
ds_read_b64_tr_b16 a[134:135],v1 offset:15360
v_mfma_f32_16x16x32_bf16 a[16:19],a[152:155],v[152:155],a[16:19]
ds_read_b64_tr_b16 a[138:139],v1 offset:25600
v_mfma_f32_16x16x32_bf16 a[16:19],a[156:159],v[156:159],a[16:19]
v_mfma_f32_16x16x32_bf16 a[20:23],a[156:159],v[188:191],a[20:23]
buffer_load_dwordx4 v[100:103],v6,s[28:31],s78 offen offset:1024 sc1
v_mfma_f32_16x16x32_bf16 a[20:23],a[152:155],v[184:187],a[20:23]
v_mfma_f32_16x16x32_bf16 a[20:23],a[148:151],v[172:175],a[20:23]
v_mfma_f32_16x16x32_bf16 a[20:23],a[144:147],v[168:171],a[20:23]
v_mfma_f32_16x16x32_bf16 a[24:27],a[144:147],v[200:203],a[24:27]
v_mfma_f32_16x16x32_bf16 a[24:27],a[148:151],v[204:207],a[24:27]
buffer_load_dwordx4 v[104:107],v6,s[28:31],s78 offen offset:2048 sc1
v_mfma_f32_16x16x32_bf16 a[24:27],a[152:155],v[216:219],a[24:27]
v_mfma_f32_16x16x32_bf16 a[24:27],a[156:159],v[220:223],a[24:27]
v_mfma_f32_16x16x32_bf16 a[28:31],a[156:159],v[252:255],a[28:31]
ds_read_b64_tr_b16 a[156:157],v2 offset:30752
ds_read_b64_tr_b16 a[158:159],v2 offset:35872
v_mfma_f32_16x16x32_bf16 a[28:31],a[152:155],v[248:251],a[28:31]
ds_read_b64_tr_b16 a[152:153],v2 offset:20512
v_mfma_f32_16x16x32_bf16 a[28:31],a[148:151],v[236:239],a[28:31]
buffer_load_dwordx4 v[108:111],v6,s[28:31],s78 offen offset:3072 sc1
v_mfma_f32_16x16x32_bf16 a[28:31],a[144:147],v[232:235],a[28:31]
s_cmp_ge_u32 2,s71
s_cselect_b32 s80,7,5
s_mul_i32 s80,s80,163840
v_mfma_f32_16x16x32_bf16 a[32:35],a[160:163],v[136:139],a[32:35]
s_add_u32 s80,s80,s81
s_add_u32 s83,s80,20480
ds_read_b64_tr_b16 a[144:145],v2 offset:32
v_mfma_f32_16x16x32_bf16 a[32:35],a[164:167],v[140:143],a[32:35]
ds_read_b64_tr_b16 a[146:147],v2 offset:5152
ds_read_b64_tr_b16 a[148:149],v2 offset:10272
v_mfma_f32_16x16x32_bf16 a[32:35],a[168:171],v[152:155],a[32:35]
ds_read_b64_tr_b16 a[150:151],v2 offset:15392
v_mfma_f32_16x16x32_bf16 a[32:35],a[172:175],v[156:159],a[32:35]
buffer_load_dwordx4 v[112:115],v6,s[28:31],s79 offen sc1
v_mfma_f32_16x16x32_bf16 a[36:39],a[172:175],v[188:191],a[36:39]
ds_read_b64_tr_b16 a[154:155],v2 offset:25632
v_mfma_f32_16x16x32_bf16 a[36:39],a[168:171],v[184:187],a[36:39]
v_mfma_f32_16x16x32_bf16 a[36:39],a[164:167],v[172:175],a[36:39]
v_mfma_f32_16x16x32_bf16 a[36:39],a[160:163],v[168:171],a[36:39]
v_mfma_f32_16x16x32_bf16 a[40:43],a[160:163],v[200:203],a[40:43]
buffer_load_dwordx4 v[116:119],v6,s[28:31],s79 offen offset:1024 sc1
v_mfma_f32_16x16x32_bf16 a[40:43],a[164:167],v[204:207],a[40:43]
v_mfma_f32_16x16x32_bf16 a[40:43],a[168:171],v[216:219],a[40:43]
v_mfma_f32_16x16x32_bf16 a[40:43],a[172:175],v[220:223],a[40:43]
v_mfma_f32_16x16x32_bf16 a[44:47],a[172:175],v[252:255],a[44:47]
ds_read_b64_tr_b16 a[172:173],v1 offset:30784
ds_read_b64_tr_b16 a[174:175],v1 offset:35904
v_mfma_f32_16x16x32_bf16 a[44:47],a[168:171],v[248:251],a[44:47]
buffer_load_dwordx4 v[120:123],v6,s[28:31],s79 offen offset:2048 sc1
v_mfma_f32_16x16x32_bf16 a[44:47],a[164:167],v[236:239],a[44:47]
ds_read_b64_tr_b16 a[164:165],v1 offset:10304
ds_read_b64_tr_b16 a[166:167],v1 offset:15424
v_mfma_f32_16x16x32_bf16 a[44:47],a[160:163],v[232:235],a[44:47]
s_waitcnt lgkmcnt(4)
ds_read_b64_tr_b16 a[160:161],v1 offset:64
v_mfma_f32_16x16x32_bf16 a[48:51],a[176:179],v[136:139],a[48:51]
ds_read_b64_tr_b16 a[162:163],v1 offset:5184
v_mfma_f32_16x16x32_bf16 a[48:51],a[180:183],v[140:143],a[48:51]
ds_read_b64_tr_b16 a[168:169],v1 offset:20544
v_mfma_f32_16x16x32_bf16 a[48:51],a[184:187],v[152:155],a[48:51]
buffer_load_dwordx4 v[124:127],v6,s[28:31],s79 offen offset:3072 sc1
v_mfma_f32_16x16x32_bf16 a[48:51],a[188:191],v[156:159],a[48:51]
ds_read_b64_tr_b16 a[170:171],v1 offset:25664
v_mfma_f32_16x16x32_bf16 a[52:55],a[188:191],v[188:191],a[52:55]
s_add_u32 m0,s82,122880
v_mfma_f32_16x16x32_bf16 a[52:55],a[184:187],v[184:187],a[52:55]
v_mfma_f32_16x16x32_bf16 a[52:55],a[180:183],v[172:175],a[52:55]
v_mfma_f32_16x16x32_bf16 a[52:55],a[176:179],v[168:171],a[52:55]
buffer_load_dwordx4 v7,s[72:75],s80 offen lds
v_mfma_f32_16x16x32_bf16 a[56:59],a[176:179],v[200:203],a[56:59]
v_mfma_f32_16x16x32_bf16 a[56:59],a[180:183],v[204:207],a[56:59]
v_mfma_f32_16x16x32_bf16 a[56:59],a[184:187],v[216:219],a[56:59]
v_mfma_f32_16x16x32_bf16 a[56:59],a[188:191],v[220:223],a[56:59]
v_mfma_f32_16x16x32_bf16 a[60:63],a[188:191],v[252:255],a[60:63]
buffer_load_dwordx4 v8,s[72:75],s80 offen offset:1024 lds
v_mfma_f32_16x16x32_bf16 a[60:63],a[184:187],v[248:251],a[60:63]
ds_read_b64_tr_b16 a[184:185],v2 offset:20576
ds_read_b64_tr_b16 a[186:187],v2 offset:25696
v_mfma_f32_16x16x32_bf16 a[60:63],a[180:183],v[236:239],a[60:63]
ds_read_b64_tr_b16 a[180:181],v2 offset:10336
v_mfma_f32_16x16x32_bf16 a[60:63],a[176:179],v[232:235],a[60:63]
ds_read_b64_tr_b16 a[176:177],v2 offset:96
v_mfma_f32_16x16x32_bf16 a[64:67],a[96:99],v[136:139],a[64:67]
ds_read_b64_tr_b16 a[178:179],v2 offset:5216
v_mfma_f32_16x16x32_bf16 a[64:67],a[100:103],v[140:143],a[64:67]
buffer_load_dwordx4 v9,s[72:75],s80 offen offset:2048 lds
v_mfma_f32_16x16x32_bf16 a[64:67],a[104:107],v[152:155],a[64:67]
ds_read_b64_tr_b16 a[182:183],v2 offset:15456
ds_read_b64_tr_b16 a[188:189],v2 offset:30816
v_mfma_f32_16x16x32_bf16 a[64:67],a[108:111],v[156:159],a[64:67]
ds_read_b64_tr_b16 a[190:191],v2 offset:35936
v_mfma_f32_16x16x32_bf16 a[68:71],a[108:111],v[188:191],a[68:71]
v_mfma_f32_16x16x32_bf16 a[68:71],a[104:107],v[184:187],a[68:71]
v_mfma_f32_16x16x32_bf16 a[68:71],a[100:103],v[172:175],a[68:71]
buffer_load_dwordx4 v10,s[72:75],s80 offen offset:3072 lds
v_mfma_f32_16x16x32_bf16 a[68:71],a[96:99],v[168:171],a[68:71]
v_mfma_f32_16x16x32_bf16 a[72:75],a[96:99],v[200:203],a[72:75]
s_add_u32 m0,s82,126976
v_mfma_f32_16x16x32_bf16 a[72:75],a[100:103],v[204:207],a[72:75]
v_mfma_f32_16x16x32_bf16 a[72:75],a[104:107],v[216:219],a[72:75]
s_cmp_ge_u32 2,s71
s_cselect_b32 s76,7,5
s_lshl_b32 s76,s76,16
v_mfma_f32_16x16x32_bf16 a[72:75],a[108:111],v[220:223],a[72:75]
s_add_u32 s77,s76,4096
buffer_load_dwordx4 v11,s[72:75],s80 offen lds
v_mfma_f32_16x16x32_bf16 a[76:79],a[108:111],v[252:255],a[76:79]
s_add_u32 s78,s76,8192
s_add_u32 s79,s76,12288
s_max_i32 s76,s76,s18
v_mfma_f32_16x16x32_bf16 a[76:79],a[104:107],v[248:251],a[76:79]
s_add_u32 m0,s82,128000
s_max_i32 s77,s77,s19
ds_read_b64_tr_b16 a[104:105],v1 offset:20608
v_mfma_f32_16x16x32_bf16 a[76:79],a[100:103],v[236:239],a[76:79]
s_max_i32 s78,s78,s100
ds_read_b64_tr_b16 a[100:101],v1 offset:10368
ds_read_b64_tr_b16 a[102:103],v1 offset:15488
v_mfma_f32_16x16x32_bf16 a[76:79],a[96:99],v[232:235],a[76:79]
s_max_i32 s79,s79,s101
ds_read_b64_tr_b16 a[96:97],v1 offset:128
v_mfma_f32_16x16x32_bf16 a[80:83],a[112:115],v[136:139],a[80:83]
buffer_load_dwordx4 v7,s[72:75],s83 offen lds
v_mfma_f32_16x16x32_bf16 a[80:83],a[116:119],v[140:143],a[80:83]
ds_read_b64_tr_b16 a[98:99],v1 offset:5248
ds_read_b64_tr_b16 a[106:107],v1 offset:25728
v_mfma_f32_16x16x32_bf16 a[80:83],a[120:123],v[152:155],a[80:83]
ds_read_b64_tr_b16 a[108:109],v1 offset:30848
v_mfma_f32_16x16x32_bf16 a[80:83],a[124:127],v[156:159],a[80:83]
ds_read_b64_tr_b16 a[110:111],v1 offset:35968
v_mfma_f32_16x16x32_bf16 a[84:87],a[124:127],v[188:191],a[84:87]
v_mfma_f32_16x16x32_bf16 a[84:87],a[120:123],v[184:187],a[84:87]
buffer_load_dwordx4 v8,s[72:75],s83 offen offset:1024 lds
v_mfma_f32_16x16x32_bf16 a[84:87],a[116:119],v[172:175],a[84:87]
v_mfma_f32_16x16x32_bf16 a[84:87],a[112:115],v[168:171],a[84:87]
v_mfma_f32_16x16x32_bf16 a[88:91],a[112:115],v[200:203],a[88:91]
v_mfma_f32_16x16x32_bf16 a[88:91],a[116:119],v[204:207],a[88:91]
v_mfma_f32_16x16x32_bf16 a[88:91],a[120:123],v[216:219],a[88:91]
buffer_load_dwordx4 v9,s[72:75],s83 offen offset:2048 lds
v_mfma_f32_16x16x32_bf16 a[88:91],a[124:127],v[220:223],a[88:91]
v_mfma_f32_16x16x32_bf16 a[92:95],a[124:127],v[252:255],a[92:95]
ds_read_b64_tr_b16 a[124:125],v2 offset:30880
ds_read_b64_tr_b16 a[126:127],v2 offset:36000
v_mfma_f32_16x16x32_bf16 a[92:95],a[120:123],v[248:251],a[92:95]
ds_read_b64_tr_b16 a[120:121],v2 offset:20640
v_mfma_f32_16x16x32_bf16 a[92:95],a[116:119],v[236:239],a[92:95]
ds_read_b64_tr_b16 a[116:117],v2 offset:10400
v_mfma_f32_16x16x32_bf16 a[92:95],a[112:115],v[232:235],a[92:95]
buffer_load_dwordx4 v10,s[72:75],s83 offen offset:3072 lds
v_mfma_f32_16x16x32_bf16 a[192:195],a[128:131],v[128:131],a[192:195]
ds_read_b64_tr_b16 a[112:113],v2 offset:160
ds_read_b64_tr_b16 a[114:115],v2 offset:5280
v_mfma_f32_16x16x32_bf16 a[192:195],a[132:135],v[160:163],a[192:195]
s_add_u32 m0,s82,132096
ds_read_b64_tr_b16 a[118:119],v2 offset:15520
v_mfma_f32_16x16x32_bf16 a[192:195],a[136:139],v[192:195],a[192:195]
ds_read_b64_tr_b16 a[122:123],v2 offset:25760
v_mfma_f32_16x16x32_bf16 a[192:195],a[140:143],v[224:227],a[192:195]
buffer_load_dwordx4 v[136:139],v6,s[28:31],s76 offen sc1
v_mfma_f32_16x16x32_bf16 a[196:199],a[140:143],v[228:231],a[196:199]
v_mfma_f32_16x16x32_bf16 a[196:199],a[136:139],v[196:199],a[196:199]
v_mfma_f32_16x16x32_bf16 a[196:199],a[132:135],v[164:167],a[196:199]
v_mfma_f32_16x16x32_bf16 a[196:199],a[128:131],v[132:135],a[196:199]
s_waitcnt vmcnt(14)
buffer_load_dwordx4 v11,s[72:75],s83 offen lds
)A"
R"A(v_mfma_f32_16x16x32_bf16 a[200:203],a[128:131],v[144:147],a[200:203]
ds_read_b64_tr_b16 a[128:129],v1 offset:192
ds_read_b64_tr_b16 a[130:131],v1 offset:5312
v_mfma_f32_16x16x32_bf16 a[200:203],a[132:135],v[176:179],a[200:203]
ds_read_b64_tr_b16 a[132:133],v1 offset:10432
v_mfma_f32_16x16x32_bf16 a[200:203],a[136:139],v[208:211],a[200:203]
ds_read_b64_tr_b16 a[134:135],v1 offset:15552
v_mfma_f32_16x16x32_bf16 a[200:203],a[140:143],v[240:243],a[200:203]
buffer_load_dwordx4 v[140:143],v6,s[28:31],s76 offen offset:1024 sc1
v_mfma_f32_16x16x32_bf16 a[208:211],a[144:147],v[128:131],a[208:211]
ds_read_b64_tr_b16 a[136:137],v1 offset:20672
ds_read_b64_tr_b16 a[138:139],v1 offset:25792
v_mfma_f32_16x16x32_bf16 a[208:211],a[148:151],v[160:163],a[208:211]
ds_read_b64_tr_b16 a[140:141],v1 offset:30912
v_mfma_f32_16x16x32_bf16 a[208:211],a[152:155],v[192:195],a[208:211]
ds_read_b64_tr_b16 a[142:143],v1 offset:36032
v_mfma_f32_16x16x32_bf16 a[208:211],a[156:159],v[224:227],a[208:211]
buffer_load_dwordx4 v[152:155],v6,s[28:31],s76 offen offset:2048 sc1
v_mfma_f32_16x16x32_bf16 a[212:215],a[156:159],v[228:231],a[212:215]
v_mfma_f32_16x16x32_bf16 a[212:215],a[152:155],v[196:199],a[212:215]
v_mfma_f32_16x16x32_bf16 a[212:215],a[148:151],v[164:167],a[212:215]
v_mfma_f32_16x16x32_bf16 a[212:215],a[144:147],v[132:135],a[212:215]
buffer_load_dwordx4 v[156:159],v6,s[28:31],s76 offen offset:3072 sc1
v_mfma_f32_16x16x32_bf16 a[216:219],a[144:147],v[144:147],a[216:219]
s_mul_i32 s76,s70,12
ds_read_b64_tr_b16 a[144:145],v2 offset:224
ds_read_b64_tr_b16 a[146:147],v2 offset:5344
v_mfma_f32_16x16x32_bf16 a[216:219],a[148:151],v[176:179],a[216:219]
s_add_u32 s76,s76,0
s_add_u32 s76,s76,s40
ds_read_b64_tr_b16 a[148:149],v2 offset:10464
v_mfma_f32_16x16x32_bf16 a[216:219],a[152:155],v[208:211],a[216:219]
s_mul_i32 s76,s76,5120
s_max_i32 s76,s76,s101
ds_read_b64_tr_b16 a[150:151],v2 offset:15584
v_mfma_f32_16x16x32_bf16 a[216:219],a[156:159],v[240:243],a[216:219]
s_waitcnt lgkmcnt(4)
buffer_load_dwordx4 v[168:171],v6,s[28:31],s77 offen sc1
v_mfma_f32_16x16x32_bf16 a[224:227],a[160:163],v[128:131],a[224:227]
ds_read_b64_tr_b16 a[152:153],v2 offset:20704
ds_read_b64_tr_b16 a[154:155],v2 offset:25824
v_mfma_f32_16x16x32_bf16 a[224:227],a[164:167],v[160:163],a[224:227]
ds_read_b64_tr_b16 a[156:157],v2 offset:30944
v_mfma_f32_16x16x32_bf16 a[224:227],a[168:171],v[192:195],a[224:227]
ds_read_b64_tr_b16 a[158:159],v2 offset:36064
v_mfma_f32_16x16x32_bf16 a[224:227],a[172:175],v[224:227],a[224:227]
buffer_load_dwordx4 v[172:175],v6,s[28:31],s77 offen offset:1024 sc1
v_mfma_f32_16x16x32_bf16 a[228:231],a[172:175],v[228:231],a[228:231]
v_mfma_f32_16x16x32_bf16 a[228:231],a[168:171],v[196:199],a[228:231]
v_mfma_f32_16x16x32_bf16 a[228:231],a[164:167],v[164:167],a[228:231]
v_mfma_f32_16x16x32_bf16 a[228:231],a[160:163],v[132:135],a[228:231]
v_mfma_f32_16x16x32_bf16 a[232:235],a[160:163],v[144:147],a[232:235]
buffer_load_dwordx4 v[184:187],v6,s[28:31],s77 offen offset:2048 sc1
v_mfma_f32_16x16x32_bf16 a[232:235],a[164:167],v[176:179],a[232:235]
ds_read_b64_tr_b16 a[160:161],v1 offset:256
ds_read_b64_tr_b16 a[162:163],v1 offset:5376
v_mfma_f32_16x16x32_bf16 a[232:235],a[168:171],v[208:211],a[232:235]
ds_read_b64_tr_b16 a[164:165],v1 offset:10496
v_mfma_f32_16x16x32_bf16 a[232:235],a[172:175],v[240:243],a[232:235]
ds_read_b64_tr_b16 a[166:167],v1 offset:15616
v_mfma_f32_16x16x32_bf16 a[240:243],a[176:179],v[128:131],a[240:243]
ds_read_b64_tr_b16 a[168:169],v1 offset:20736
v_mfma_f32_16x16x32_bf16 a[240:243],a[180:183],v[160:163],a[240:243]
buffer_load_dwordx4 v[188:191],v6,s[28:31],s77 offen offset:3072 sc1
v_mfma_f32_16x16x32_bf16 a[240:243],a[184:187],v[192:195],a[240:243]
s_mul_i32 s77,s70,12
ds_read_b64_tr_b16 a[170:171],v1 offset:25856
ds_read_b64_tr_b16 a[172:173],v1 offset:30976
v_mfma_f32_16x16x32_bf16 a[240:243],a[188:191],v[224:227],a[240:243]
s_add_u32 s77,s77,4
s_add_u32 s77,s77,s40
ds_read_b64_tr_b16 a[174:175],v1 offset:36096
v_mfma_f32_16x16x32_bf16 a[244:247],a[188:191],v[228:231],a[244:247]
s_mul_i32 s77,s77,5120
s_max_i32 s77,s77,s101
v_mfma_f32_16x16x32_bf16 a[244:247],a[184:187],v[196:199],a[244:247]
v_mfma_f32_16x16x32_bf16 a[244:247],a[180:183],v[164:167],a[244:247]
buffer_load_dwordx4 v[200:203],v6,s[28:31],s78 offen sc1
v_mfma_f32_16x16x32_bf16 a[244:247],a[176:179],v[132:135],a[244:247]
v_mfma_f32_16x16x32_bf16 a[248:251],a[176:179],v[144:147],a[248:251]
ds_read_b64_tr_b16 a[176:177],v2 offset:288
ds_read_b64_tr_b16 a[178:179],v2 offset:5408
v_mfma_f32_16x16x32_bf16 a[248:251],a[180:183],v[176:179],a[248:251]
ds_read_b64_tr_b16 a[180:181],v2 offset:10528
v_mfma_f32_16x16x32_bf16 a[248:251],a[184:187],v[208:211],a[248:251]
ds_read_b64_tr_b16 a[182:183],v2 offset:15648
v_mfma_f32_16x16x32_bf16 a[248:251],a[188:191],v[240:243],a[248:251]
buffer_load_dwordx4 v[204:207],v6,s[28:31],s78 offen offset:1024 sc1
v_mfma_f32_16x16x32_bf16 a[0:3],a[96:99],v[128:131],a[0:3]
ds_read_b64_tr_b16 a[184:185],v2 offset:20768
ds_read_b64_tr_b16 a[186:187],v2 offset:25888
v_mfma_f32_16x16x32_bf16 a[0:3],a[100:103],v[160:163],a[0:3]
ds_read_b64_tr_b16 a[188:189],v2 offset:31008
v_mfma_f32_16x16x32_bf16 a[0:3],a[104:107],v[192:195],a[0:3]
ds_read_b64_tr_b16 a[190:191],v2 offset:36128
v_mfma_f32_16x16x32_bf16 a[0:3],a[108:111],v[224:227],a[0:3]
v_mfma_f32_16x16x32_bf16 a[4:7],a[108:111],v[228:231],a[4:7]
buffer_load_dwordx4 v[216:219],v6,s[28:31],s78 offen offset:2048 sc1
v_mfma_f32_16x16x32_bf16 a[4:7],a[104:107],v[196:199],a[4:7]
v_mfma_f32_16x16x32_bf16 a[4:7],a[100:103],v[164:167],a[4:7]
v_mfma_f32_16x16x32_bf16 a[4:7],a[96:99],v[132:135],a[4:7]
v_mfma_f32_16x16x32_bf16 a[8:11],a[96:99],v[144:147],a[8:11]
v_mfma_f32_16x16x32_bf16 a[8:11],a[100:103],v[176:179],a[8:11]
buffer_load_dwordx4 v[220:223],v6,s[28:31],s78 offen offset:3072 sc1
v_mfma_f32_16x16x32_bf16 a[8:11],a[104:107],v[208:211],a[8:11]
s_mul_i32 s78,s70,12
s_add_u32 s78,s78,8
s_add_u32 s78,s78,s40
v_mfma_f32_16x16x32_bf16 a[8:11],a[108:111],v[240:243],a[8:11]
s_mul_i32 s78,s78,5120
s_max_i32 s78,s78,s101
v_mfma_f32_16x16x32_bf16 a[16:19],a[112:115],v[128:131],a[16:19]
v_mfma_f32_16x16x32_bf16 a[16:19],a[116:119],v[160:163],a[16:19]
v_mfma_f32_16x16x32_bf16 a[16:19],a[120:123],v[192:195],a[16:19]
buffer_load_dwordx4 v[232:235],v6,s[28:31],s79 offen sc1
v_mfma_f32_16x16x32_bf16 a[16:19],a[124:127],v[224:227],a[16:19]
v_mfma_f32_16x16x32_bf16 a[20:23],a[124:127],v[228:231],a[20:23]
v_mfma_f32_16x16x32_bf16 a[20:23],a[120:123],v[196:199],a[20:23]
v_mfma_f32_16x16x32_bf16 a[20:23],a[116:119],v[164:167],a[20:23]
v_mfma_f32_16x16x32_bf16 a[20:23],a[112:115],v[132:135],a[20:23]
buffer_load_dwordx4 v[236:239],v6,s[28:31],s79 offen offset:1024 sc1
v_mfma_f32_16x16x32_bf16 a[24:27],a[112:115],v[144:147],a[24:27]
v_mfma_f32_16x16x32_bf16 a[24:27],a[116:119],v[176:179],a[24:27]
v_mfma_f32_16x16x32_bf16 a[24:27],a[120:123],v[208:211],a[24:27]
v_mfma_f32_16x16x32_bf16 a[24:27],a[124:127],v[240:243],a[24:27]
v_mfma_f32_16x16x32_bf16 a[32:35],a[128:131],v[128:131],a[32:35]
buffer_load_dwordx4 v[248:251],v6,s[28:31],s79 offen offset:2048 sc1
v_mfma_f32_16x16x32_bf16 a[32:35],a[132:135],v[160:163],a[32:35]
v_mfma_f32_16x16x32_bf16 a[32:35],a[136:139],v[192:195],a[32:35]
v_mfma_f32_16x16x32_bf16 a[32:35],a[140:143],v[224:227],a[32:35]
v_mfma_f32_16x16x32_bf16 a[36:39],a[140:143],v[228:231],a[36:39]
v_mfma_f32_16x16x32_bf16 a[36:39],a[136:139],v[196:199],a[36:39]
buffer_load_dwordx4 v[252:255],v6,s[28:31],s79 offen offset:3072 sc1
v_mfma_f32_16x16x32_bf16 a[36:39],a[132:135],v[164:167],a[36:39]
v_mfma_f32_16x16x32_bf16 a[36:39],a[128:131],v[132:135],a[36:39]
v_mfma_f32_16x16x32_bf16 a[40:43],a[128:131],v[144:147],a[40:43]
v_mfma_f32_16x16x32_bf16 a[40:43],a[132:135],v[176:179],a[40:43]
v_mfma_f32_16x16x32_bf16 a[40:43],a[136:139],v[208:211],a[40:43]
s_waitcnt vmcnt(15)
v_mfma_f32_16x16x32_bf16 a[40:43],a[140:143],v[240:243],a[40:43]
s_barrier
ds_read_b64_tr_b16 a[96:97],v42
ds_read_b64_tr_b16 a[98:99],v42 offset:5120
s_waitcnt lgkmcnt(2)
v_mfma_f32_16x16x32_bf16 a[48:51],a[144:147],v[128:131],a[48:51]
ds_read_b64_tr_b16 a[100:101],v42 offset:10240
v_mfma_f32_16x16x32_bf16 a[48:51],a[148:151],v[160:163],a[48:51]
ds_read_b64_tr_b16 a[102:103],v42 offset:15360
v_mfma_f32_16x16x32_bf16 a[48:51],a[152:155],v[192:195],a[48:51]
ds_read_b64_tr_b16 a[104:105],v42 offset:20480
v_mfma_f32_16x16x32_bf16 a[48:51],a[156:159],v[224:227],a[48:51]
ds_read_b64_tr_b16 a[106:107],v42 offset:25600
ds_read_b64_tr_b16 a[108:109],v42 offset:30720
v_mfma_f32_16x16x32_bf16 a[52:55],a[156:159],v[228:231],a[52:55]
ds_read_b64_tr_b16 a[110:111],v42 offset:35840
v_mfma_f32_16x16x32_bf16 a[52:55],a[152:155],v[196:199],a[52:55]
ds_read_b64_tr_b16 a[112:113],v44
v_mfma_f32_16x16x32_bf16 a[52:55],a[148:151],v[164:167],a[52:55]
ds_read_b64_tr_b16 a[114:115],v44 offset:5120
v_mfma_f32_16x16x32_bf16 a[52:55],a[144:147],v[132:135],a[52:55]
ds_read_b64_tr_b16 a[116:117],v44 offset:10240
ds_read_b64_tr_b16 a[118:119],v44 offset:15360
v_mfma_f32_16x16x32_bf16 a[56:59],a[144:147],v[144:147],a[56:59]
ds_read_b64_tr_b16 a[120:121],v44 offset:20480
v_mfma_f32_16x16x32_bf16 a[56:59],a[148:151],v[176:179],a[56:59]
ds_read_b64_tr_b16 a[122:123],v44 offset:25600
v_mfma_f32_16x16x32_bf16 a[56:59],a[152:155],v[208:211],a[56:59]
ds_read_b64_tr_b16 a[124:125],v44 offset:30720
v_mfma_f32_16x16x32_bf16 a[56:59],a[156:159],v[240:243],a[56:59]
ds_read_b64_tr_b16 a[126:127],v44 offset:35840
ds_read_b64_tr_b16 a[128:129],v43 offset:32
v_mfma_f32_16x16x32_bf16 a[64:67],a[160:163],v[128:131],a[64:67]
ds_read_b64_tr_b16 a[130:131],v43 offset:5152
v_mfma_f32_16x16x32_bf16 a[64:67],a[164:167],v[160:163],a[64:67]
ds_read_b64_tr_b16 a[132:133],v43 offset:10272
v_mfma_f32_16x16x32_bf16 a[64:67],a[168:171],v[192:195],a[64:67]
ds_read_b64_tr_b16 a[134:135],v43 offset:15392
v_mfma_f32_16x16x32_bf16 a[64:67],a[172:175],v[224:227],a[64:67]
ds_read_b64_tr_b16 a[136:137],v43 offset:20512
ds_read_b64_tr_b16 a[138:139],v43 offset:25632
v_mfma_f32_16x16x32_bf16 a[68:71],a[172:175],v[228:231],a[68:71]
ds_read_b64_tr_b16 a[140:141],v43 offset:30752
v_mfma_f32_16x16x32_bf16 a[68:71],a[168:171],v[196:199],a[68:71]
ds_read_b64_tr_b16 a[142:143],v43 offset:35872
v_mfma_f32_16x16x32_bf16 a[68:71],a[164:167],v[164:167],a[68:71]
ds_read_b64_tr_b16 a[144:145],v45 offset:32
v_mfma_f32_16x16x32_bf16 a[68:71],a[160:163],v[132:135],a[68:71]
ds_read_b64_tr_b16 a[146:147],v45 offset:5152
ds_read_b64_tr_b16 a[148:149],v45 offset:10272
v_mfma_f32_16x16x32_bf16 a[72:75],a[160:163],v[144:147],a[72:75]
ds_read_b64_tr_b16 a[150:151],v45 offset:15392
v_mfma_f32_16x16x32_bf16 a[72:75],a[164:167],v[176:179],a[72:75]
ds_read_b64_tr_b16 a[152:153],v45 offset:20512
v_mfma_f32_16x16x32_bf16 a[72:75],a[168:171],v[208:211],a[72:75]
ds_read_b64_tr_b16 a[154:155],v45 offset:25632
v_mfma_f32_16x16x32_bf16 a[72:75],a[172:175],v[240:243],a[72:75]
ds_read_b64_tr_b16 a[156:157],v45 offset:30752
ds_read_b64_tr_b16 a[158:159],v45 offset:35872
v_mfma_f32_16x16x32_bf16 a[80:83],a[176:179],v[128:131],a[80:83]
ds_read_b64_tr_b16 a[160:161],v42 offset:64
v_mfma_f32_16x16x32_bf16 a[80:83],a[180:183],v[160:163],a[80:83]
ds_read_b64_tr_b16 a[162:163],v42 offset:5184
v_mfma_f32_16x16x32_bf16 a[80:83],a[184:187],v[192:195],a[80:83]
ds_read_b64_tr_b16 a[164:165],v42 offset:10304
v_mfma_f32_16x16x32_bf16 a[80:83],a[188:191],v[224:227],a[80:83]
ds_read_b64_tr_b16 a[166:167],v42 offset:15424
ds_read_b64_tr_b16 a[168:169],v42 offset:20544
v_mfma_f32_16x16x32_bf16 a[84:87],a[188:191],v[228:231],a[84:87]
ds_read_b64_tr_b16 a[170:171],v42 offset:25664
v_mfma_f32_16x16x32_bf16 a[84:87],a[184:187],v[196:199],a[84:87]
ds_read_b64_tr_b16 a[172:173],v42 offset:30784
v_mfma_f32_16x16x32_bf16 a[84:87],a[180:183],v[164:167],a[84:87]
ds_read_b64_tr_b16 a[174:175],v42 offset:35904
v_mfma_f32_16x16x32_bf16 a[84:87],a[176:179],v[132:135],a[84:87]
v_mfma_f32_16x16x32_bf16 a[88:91],a[176:179],v[144:147],a[88:91]
ds_read_b64_tr_b16 a[176:177],v44 offset:64
ds_read_b64_tr_b16 a[178:179],v44 offset:5184
v_mfma_f32_16x16x32_bf16 a[88:91],a[180:183],v[176:179],a[88:91]
)A"
R"A(ds_read_b64_tr_b16 a[180:181],v44 offset:10304
v_mfma_f32_16x16x32_bf16 a[88:91],a[184:187],v[208:211],a[88:91]
ds_read_b64_tr_b16 a[182:183],v44 offset:15424
v_mfma_f32_16x16x32_bf16 a[88:91],a[188:191],v[240:243],a[88:91]
s_waitcnt lgkmcnt(7)
ds_read_b64_tr_b16 a[184:185],v44 offset:20544
v_mfma_f32_16x16x32_bf16 a[192:195],a[96:99],v[64:67],a[192:195]
ds_read_b64_tr_b16 a[186:187],v44 offset:25664
ds_read_b64_tr_b16 a[188:189],v44 offset:30784
v_mfma_f32_16x16x32_bf16 a[192:195],a[100:103],v[68:71],a[192:195]
ds_read_b64_tr_b16 a[190:191],v44 offset:35904
v_mfma_f32_16x16x32_bf16 a[192:195],a[104:107],v[72:75],a[192:195]
v_mfma_f32_16x16x32_bf16 a[192:195],a[108:111],v[76:79],a[192:195]
v_mfma_f32_16x16x32_bf16 a[196:199],a[108:111],v[92:95],a[196:199]
v_mfma_f32_16x16x32_bf16 a[196:199],a[104:107],v[88:91],a[196:199]
v_mfma_f32_16x16x32_bf16 a[196:199],a[100:103],v[84:87],a[196:199]
v_mfma_f32_16x16x32_bf16 a[196:199],a[96:99],v[80:83],a[196:199]
v_mfma_f32_16x16x32_bf16 a[200:203],a[96:99],v[96:99],a[200:203]
ds_read_b64_tr_b16 a[96:97],v43 offset:96
ds_read_b64_tr_b16 a[98:99],v43 offset:5216
v_mfma_f32_16x16x32_bf16 a[200:203],a[100:103],v[100:103],a[200:203]
ds_read_b64_tr_b16 a[100:101],v43 offset:10336
v_mfma_f32_16x16x32_bf16 a[200:203],a[104:107],v[104:107],a[200:203]
ds_read_b64_tr_b16 a[102:103],v43 offset:15456
v_mfma_f32_16x16x32_bf16 a[200:203],a[108:111],v[108:111],a[200:203]
ds_read_b64_tr_b16 a[104:105],v43 offset:20576
v_mfma_f32_16x16x32_bf16 a[192:195],a[112:115],v[136:139],a[192:195]
s_waitcnt vmcnt(3)
ds_read_b64_tr_b16 a[106:107],v43 offset:25696
ds_read_b64_tr_b16 a[108:109],v43 offset:30816
v_mfma_f32_16x16x32_bf16 a[192:195],a[116:119],v[140:143],a[192:195]
ds_read_b64_tr_b16 a[110:111],v43 offset:35936
v_mfma_f32_16x16x32_bf16 a[192:195],a[120:123],v[152:155],a[192:195]
v_mfma_f32_16x16x32_bf16 a[192:195],a[124:127],v[156:159],a[192:195]
v_mfma_f32_16x16x32_bf16 a[196:199],a[124:127],v[188:191],a[196:199]
v_mfma_f32_16x16x32_bf16 a[196:199],a[120:123],v[184:187],a[196:199]
v_mfma_f32_16x16x32_bf16 a[196:199],a[116:119],v[172:175],a[196:199]
v_mfma_f32_16x16x32_bf16 a[196:199],a[112:115],v[168:171],a[196:199]
v_mfma_f32_16x16x32_bf16 a[200:203],a[112:115],v[200:203],a[200:203]
ds_read_b64_tr_b16 a[112:113],v45 offset:96
ds_read_b64_tr_b16 a[114:115],v45 offset:5216
v_mfma_f32_16x16x32_bf16 a[200:203],a[116:119],v[204:207],a[200:203]
v_accvgpr_read_b32 v128,a192
v_accvgpr_read_b32 v129,a193
v_mfma_f32_16x16x32_bf16 a[200:203],a[120:123],v[216:219],a[200:203]
v_cvt_pk_bf16_f32 v128,v128,v129
v_accvgpr_read_b32 v130,a194
v_mfma_f32_16x16x32_bf16 a[200:203],a[124:127],v[220:223],a[200:203]
v_accvgpr_read_b32 v131,a195
v_cvt_pk_bf16_f32 v129,v130,v131
v_mfma_f32_16x16x32_bf16 a[208:211],a[128:131],v[64:67],a[208:211]
ds_read_b64_tr_b16 a[116:117],v45 offset:10336
ds_read_b64_tr_b16 a[118:119],v45 offset:15456
v_mfma_f32_16x16x32_bf16 a[208:211],a[132:135],v[68:71],a[208:211]
v_accvgpr_read_b32 v132,a196
v_accvgpr_read_b32 v133,a197
v_mfma_f32_16x16x32_bf16 a[208:211],a[136:139],v[72:75],a[208:211]
v_cvt_pk_bf16_f32 v132,v132,v133
v_accvgpr_read_b32 v134,a198
v_mfma_f32_16x16x32_bf16 a[208:211],a[140:143],v[76:79],a[208:211]
v_accvgpr_read_b32 v135,a199
v_cvt_pk_bf16_f32 v133,v134,v135
v_mfma_f32_16x16x32_bf16 a[212:215],a[140:143],v[92:95],a[212:215]
ds_read_b64_tr_b16 a[120:121],v45 offset:20576
ds_read_b64_tr_b16 a[122:123],v45 offset:25696
v_mfma_f32_16x16x32_bf16 a[212:215],a[136:139],v[88:91],a[212:215]
ds_read_b64_tr_b16 a[124:125],v45 offset:30816
v_accvgpr_read_b32 v144,a200
v_mfma_f32_16x16x32_bf16 a[212:215],a[132:135],v[84:87],a[212:215]
ds_read_b64_tr_b16 a[126:127],v45 offset:35936
v_accvgpr_read_b32 v145,a201
v_mfma_f32_16x16x32_bf16 a[212:215],a[128:131],v[80:83],a[212:215]
v_cvt_pk_bf16_f32 v144,v144,v145
v_accvgpr_read_b32 v146,a202
v_mfma_f32_16x16x32_bf16 a[216:219],a[128:131],v[96:99],a[216:219]
v_accvgpr_read_b32 v147,a203
v_cvt_pk_bf16_f32 v145,v146,v147
v_mfma_f32_16x16x32_bf16 a[216:219],a[132:135],v[100:103],a[216:219]
ds_read_b64_tr_b16 a[128:129],v42 offset:128
ds_read_b64_tr_b16 a[130:131],v42 offset:5248
v_mfma_f32_16x16x32_bf16 a[216:219],a[136:139],v[104:107],a[216:219]
ds_read_b64_tr_b16 a[132:133],v42 offset:10368
v_mfma_f32_16x16x32_bf16 a[216:219],a[140:143],v[108:111],a[216:219]
ds_read_b64_tr_b16 a[134:135],v42 offset:15488
v_mfma_f32_16x16x32_bf16 a[208:211],a[144:147],v[136:139],a[208:211]
ds_read_b64_tr_b16 a[136:137],v42 offset:20608
v_mfma_f32_16x16x32_bf16 a[208:211],a[148:151],v[140:143],a[208:211]
ds_read_b64_tr_b16 a[138:139],v42 offset:25728
ds_read_b64_tr_b16 a[140:141],v42 offset:30848
v_mfma_f32_16x16x32_bf16 a[208:211],a[152:155],v[152:155],a[208:211]
ds_read_b64_tr_b16 a[142:143],v42 offset:35968
v_mfma_f32_16x16x32_bf16 a[208:211],a[156:159],v[156:159],a[208:211]
v_mfma_f32_16x16x32_bf16 a[212:215],a[156:159],v[188:191],a[212:215]
v_mfma_f32_16x16x32_bf16 a[212:215],a[152:155],v[184:187],a[212:215]
v_mfma_f32_16x16x32_bf16 a[212:215],a[148:151],v[172:175],a[212:215]
v_mfma_f32_16x16x32_bf16 a[212:215],a[144:147],v[168:171],a[212:215]
v_mfma_f32_16x16x32_bf16 a[216:219],a[144:147],v[200:203],a[216:219]
ds_read_b64_tr_b16 a[144:145],v44 offset:128
ds_read_b64_tr_b16 a[146:147],v44 offset:5248
v_mfma_f32_16x16x32_bf16 a[216:219],a[148:151],v[204:207],a[216:219]
v_accvgpr_read_b32 v130,a208
v_accvgpr_read_b32 v131,a209
v_mfma_f32_16x16x32_bf16 a[216:219],a[152:155],v[216:219],a[216:219]
v_accvgpr_read_b32 v12,a210
v_accvgpr_read_b32 v13,a211
v_mfma_f32_16x16x32_bf16 a[216:219],a[156:159],v[220:223],a[216:219]
v_cvt_pk_bf16_f32 v130,v130,v131
v_cvt_pk_bf16_f32 v131,v12,v13
v_mfma_f32_16x16x32_bf16 a[224:227],a[160:163],v[64:67],a[224:227]
buffer_store_dwordx4 v[128:131],v6,s[32:35],s84 offen sc1
v_mfma_f32_16x16x32_bf16 a[224:227],a[164:167],v[68:71],a[224:227]
s_waitcnt lgkmcnt(2)
v_accvgpr_read_b32 v134,a212
v_accvgpr_read_b32 v135,a213
v_mfma_f32_16x16x32_bf16 a[224:227],a[168:171],v[72:75],a[224:227]
v_accvgpr_read_b32 v14,a214
v_accvgpr_read_b32 v15,a215
v_mfma_f32_16x16x32_bf16 a[224:227],a[172:175],v[76:79],a[224:227]
v_cvt_pk_bf16_f32 v134,v134,v135
v_cvt_pk_bf16_f32 v135,v14,v15
v_mfma_f32_16x16x32_bf16 a[228:231],a[172:175],v[92:95],a[228:231]
buffer_store_dwordx4 v[132:135],v6,s[32:35],s85 offen sc1
v_mfma_f32_16x16x32_bf16 a[228:231],a[168:171],v[88:91],a[228:231]
v_accvgpr_read_b32 v146,a216
v_accvgpr_read_b32 v147,a217
v_mfma_f32_16x16x32_bf16 a[228:231],a[164:167],v[84:87],a[228:231]
v_accvgpr_read_b32 v16,a218
v_accvgpr_read_b32 v17,a219
v_mfma_f32_16x16x32_bf16 a[228:231],a[160:163],v[80:83],a[228:231]
v_cvt_pk_bf16_f32 v146,v146,v147
v_cvt_pk_bf16_f32 v147,v16,v17
v_mfma_f32_16x16x32_bf16 a[232:235],a[160:163],v[96:99],a[232:235]
buffer_store_dwordx4 v[144:147],v6,s[32:35],s86 offen sc1
v_mfma_f32_16x16x32_bf16 a[232:235],a[164:167],v[100:103],a[232:235]
ds_read_b64_tr_b16 a[148:149],v44 offset:10368
ds_read_b64_tr_b16 a[150:151],v44 offset:15488
v_mfma_f32_16x16x32_bf16 a[232:235],a[168:171],v[104:107],a[232:235]
ds_read_b64_tr_b16 a[152:153],v44 offset:20608
v_mfma_f32_16x16x32_bf16 a[232:235],a[172:175],v[108:111],a[232:235]
ds_read_b64_tr_b16 a[154:155],v44 offset:25728
v_mfma_f32_16x16x32_bf16 a[224:227],a[176:179],v[136:139],a[224:227]
ds_read_b64_tr_b16 a[156:157],v44 offset:30848
v_mfma_f32_16x16x32_bf16 a[224:227],a[180:183],v[140:143],a[224:227]
ds_read_b64_tr_b16 a[158:159],v44 offset:35968
ds_read_b64_tr_b16 a[160:161],v43 offset:160
v_mfma_f32_16x16x32_bf16 a[224:227],a[184:187],v[152:155],a[224:227]
ds_read_b64_tr_b16 a[162:163],v43 offset:5280
v_mfma_f32_16x16x32_bf16 a[224:227],a[188:191],v[156:159],a[224:227]
ds_read_b64_tr_b16 a[164:165],v43 offset:10400
v_mfma_f32_16x16x32_bf16 a[228:231],a[188:191],v[188:191],a[228:231]
ds_read_b64_tr_b16 a[166:167],v43 offset:15520
v_mfma_f32_16x16x32_bf16 a[228:231],a[184:187],v[184:187],a[228:231]
ds_read_b64_tr_b16 a[168:169],v43 offset:20640
ds_read_b64_tr_b16 a[170:171],v43 offset:25760
v_mfma_f32_16x16x32_bf16 a[228:231],a[180:183],v[172:175],a[228:231]
ds_read_b64_tr_b16 a[172:173],v43 offset:30880
v_mfma_f32_16x16x32_bf16 a[228:231],a[176:179],v[168:171],a[228:231]
ds_read_b64_tr_b16 a[174:175],v43 offset:36000
v_mfma_f32_16x16x32_bf16 a[232:235],a[176:179],v[200:203],a[232:235]
ds_read_b64_tr_b16 a[176:177],v45 offset:160
v_mfma_f32_16x16x32_bf16 a[232:235],a[180:183],v[204:207],a[232:235]
v_accvgpr_read_b32 v160,a224
v_accvgpr_read_b32 v161,a225
v_mfma_f32_16x16x32_bf16 a[232:235],a[184:187],v[216:219],a[232:235]
v_cvt_pk_bf16_f32 v160,v160,v161
v_accvgpr_read_b32 v162,a226
v_mfma_f32_16x16x32_bf16 a[232:235],a[188:191],v[220:223],a[232:235]
v_accvgpr_read_b32 v163,a227
v_cvt_pk_bf16_f32 v161,v162,v163
v_mfma_f32_16x16x32_bf16 a[240:243],a[96:99],v[64:67],a[240:243]
ds_read_b64_tr_b16 a[178:179],v45 offset:5280
ds_read_b64_tr_b16 a[180:181],v45 offset:10400
v_mfma_f32_16x16x32_bf16 a[240:243],a[100:103],v[68:71],a[240:243]
v_accvgpr_read_b32 v164,a228
v_accvgpr_read_b32 v165,a229
v_mfma_f32_16x16x32_bf16 a[240:243],a[104:107],v[72:75],a[240:243]
v_cvt_pk_bf16_f32 v164,v164,v165
v_accvgpr_read_b32 v166,a230
v_mfma_f32_16x16x32_bf16 a[240:243],a[108:111],v[76:79],a[240:243]
v_accvgpr_read_b32 v167,a231
v_cvt_pk_bf16_f32 v165,v166,v167
v_mfma_f32_16x16x32_bf16 a[244:247],a[108:111],v[92:95],a[244:247]
ds_read_b64_tr_b16 a[182:183],v45 offset:15520
ds_read_b64_tr_b16 a[184:185],v45 offset:20640
v_mfma_f32_16x16x32_bf16 a[244:247],a[104:107],v[88:91],a[244:247]
v_accvgpr_read_b32 v176,a232
v_accvgpr_read_b32 v177,a233
v_mfma_f32_16x16x32_bf16 a[244:247],a[100:103],v[84:87],a[244:247]
v_cvt_pk_bf16_f32 v176,v176,v177
v_accvgpr_read_b32 v178,a234
v_mfma_f32_16x16x32_bf16 a[244:247],a[96:99],v[80:83],a[244:247]
v_accvgpr_read_b32 v179,a235
v_cvt_pk_bf16_f32 v177,v178,v179
v_mfma_f32_16x16x32_bf16 a[248:251],a[96:99],v[96:99],a[248:251]
ds_read_b64_tr_b16 a[186:187],v45 offset:25760
ds_read_b64_tr_b16 a[188:189],v45 offset:30880
v_mfma_f32_16x16x32_bf16 a[248:251],a[100:103],v[100:103],a[248:251]
ds_read_b64_tr_b16 a[190:191],v45 offset:36000
v_mfma_f32_16x16x32_bf16 a[248:251],a[104:107],v[104:107],a[248:251]
ds_read_b64_tr_b16 a[96:97],v42 offset:192
v_mfma_f32_16x16x32_bf16 a[248:251],a[108:111],v[108:111],a[248:251]
ds_read_b64_tr_b16 a[98:99],v42 offset:5312
v_mfma_f32_16x16x32_bf16 a[240:243],a[112:115],v[136:139],a[240:243]
ds_read_b64_tr_b16 a[100:101],v42 offset:10432
ds_read_b64_tr_b16 a[102:103],v42 offset:15552
v_mfma_f32_16x16x32_bf16 a[240:243],a[116:119],v[140:143],a[240:243]
ds_read_b64_tr_b16 a[104:105],v42 offset:20672
v_mfma_f32_16x16x32_bf16 a[240:243],a[120:123],v[152:155],a[240:243]
ds_read_b64_tr_b16 a[106:107],v42 offset:25792
v_mfma_f32_16x16x32_bf16 a[240:243],a[124:127],v[156:159],a[240:243]
ds_read_b64_tr_b16 a[108:109],v42 offset:30912
v_mfma_f32_16x16x32_bf16 a[244:247],a[124:127],v[188:191],a[244:247]
ds_read_b64_tr_b16 a[110:111],v42 offset:36032
v_mfma_f32_16x16x32_bf16 a[244:247],a[120:123],v[184:187],a[244:247]
v_mfma_f32_16x16x32_bf16 a[244:247],a[116:119],v[172:175],a[244:247]
v_mfma_f32_16x16x32_bf16 a[244:247],a[112:115],v[168:171],a[244:247]
v_mfma_f32_16x16x32_bf16 a[248:251],a[112:115],v[200:203],a[248:251]
ds_read_b64_tr_b16 a[112:113],v44 offset:192
ds_read_b64_tr_b16 a[114:115],v44 offset:5312
v_mfma_f32_16x16x32_bf16 a[248:251],a[116:119],v[204:207],a[248:251]
v_accvgpr_read_b32 v162,a240
v_accvgpr_read_b32 v163,a241
v_mfma_f32_16x16x32_bf16 a[248:251],a[120:123],v[216:219],a[248:251]
v_accvgpr_read_b32 v12,a242
v_accvgpr_read_b32 v13,a243
v_mfma_f32_16x16x32_bf16 a[248:251],a[124:127],v[220:223],a[248:251]
v_cvt_pk_bf16_f32 v162,v162,v163
v_cvt_pk_bf16_f32 v163,v12,v13
v_mfma_f32_16x16x32_bf16 a[0:3],a[128:131],v[64:67],a[0:3]
buffer_store_dwordx4 v[160:163],v6,s[32:35],s84 offen offset:1024 sc1
v_mfma_f32_16x16x32_bf16 a[0:3],a[132:135],v[68:71],a[0:3]
v_accvgpr_read_b32 v166,a244
v_accvgpr_read_b32 v167,a245
v_mfma_f32_16x16x32_bf16 a[0:3],a[136:139],v[72:75],a[0:3]
v_accvgpr_read_b32 v14,a246
)A"
R"A(v_accvgpr_read_b32 v15,a247
v_mfma_f32_16x16x32_bf16 a[0:3],a[140:143],v[76:79],a[0:3]
v_cvt_pk_bf16_f32 v166,v166,v167
v_cvt_pk_bf16_f32 v167,v14,v15
v_mfma_f32_16x16x32_bf16 a[4:7],a[140:143],v[92:95],a[4:7]
buffer_store_dwordx4 v[164:167],v6,s[32:35],s85 offen offset:1024 sc1
v_mfma_f32_16x16x32_bf16 a[4:7],a[136:139],v[88:91],a[4:7]
v_accvgpr_read_b32 v178,a248
v_accvgpr_read_b32 v179,a249
v_mfma_f32_16x16x32_bf16 a[4:7],a[132:135],v[84:87],a[4:7]
v_accvgpr_read_b32 v16,a250
v_accvgpr_read_b32 v17,a251
v_cvt_pk_bf16_f32 v178,v178,v179
v_cvt_pk_bf16_f32 v179,v16,v17
v_mfma_f32_16x16x32_bf16 a[4:7],a[128:131],v[80:83],a[4:7]
ds_read_b64_tr_b16 a[116:117],v44 offset:10432
ds_read_b64_tr_b16 a[118:119],v44 offset:15552
v_mfma_f32_16x16x32_bf16 a[8:11],a[128:131],v[96:99],a[8:11]
buffer_store_dwordx4 v[176:179],v6,s[32:35],s86 offen offset:1024 sc1
v_mfma_f32_16x16x32_bf16 a[8:11],a[132:135],v[100:103],a[8:11]
ds_read_b64_tr_b16 a[120:121],v44 offset:20672
ds_read_b64_tr_b16 a[122:123],v44 offset:25792
v_mfma_f32_16x16x32_bf16 a[8:11],a[136:139],v[104:107],a[8:11]
ds_read_b64_tr_b16 a[124:125],v44 offset:30912
v_mfma_f32_16x16x32_bf16 a[8:11],a[140:143],v[108:111],a[8:11]
s_waitcnt lgkmcnt(5)
ds_read_b64_tr_b16 a[126:127],v44 offset:36032
v_mfma_f32_16x16x32_bf16 a[0:3],a[144:147],v[136:139],a[0:3]
ds_read_b64_tr_b16 a[128:129],v43 offset:224
v_mfma_f32_16x16x32_bf16 a[0:3],a[148:151],v[140:143],a[0:3]
ds_read_b64_tr_b16 a[130:131],v43 offset:5344
ds_read_b64_tr_b16 a[132:133],v43 offset:10464
v_mfma_f32_16x16x32_bf16 a[0:3],a[152:155],v[152:155],a[0:3]
ds_read_b64_tr_b16 a[134:135],v43 offset:15584
v_mfma_f32_16x16x32_bf16 a[0:3],a[156:159],v[156:159],a[0:3]
ds_read_b64_tr_b16 a[136:137],v43 offset:20704
v_mfma_f32_16x16x32_bf16 a[4:7],a[156:159],v[188:191],a[4:7]
ds_read_b64_tr_b16 a[138:139],v43 offset:25824
v_mfma_f32_16x16x32_bf16 a[4:7],a[152:155],v[184:187],a[4:7]
ds_read_b64_tr_b16 a[140:141],v43 offset:30944
ds_read_b64_tr_b16 a[142:143],v43 offset:36064
v_mfma_f32_16x16x32_bf16 a[4:7],a[148:151],v[172:175],a[4:7]
v_mfma_f32_16x16x32_bf16 a[4:7],a[144:147],v[168:171],a[4:7]
v_mfma_f32_16x16x32_bf16 a[8:11],a[144:147],v[200:203],a[8:11]
ds_read_b64_tr_b16 a[144:145],v45 offset:224
ds_read_b64_tr_b16 a[146:147],v45 offset:5344
v_mfma_f32_16x16x32_bf16 a[8:11],a[148:151],v[204:207],a[8:11]
v_accvgpr_read_b32 v192,a0
v_accvgpr_read_b32 v193,a1
v_mfma_f32_16x16x32_bf16 a[8:11],a[152:155],v[216:219],a[8:11]
v_cvt_pk_bf16_f32 v192,v192,v193
v_accvgpr_read_b32 v194,a2
v_mfma_f32_16x16x32_bf16 a[8:11],a[156:159],v[220:223],a[8:11]
v_accvgpr_read_b32 v195,a3
v_cvt_pk_bf16_f32 v193,v194,v195
v_mfma_f32_16x16x32_bf16 a[16:19],a[160:163],v[64:67],a[16:19]
ds_read_b64_tr_b16 a[148:149],v45 offset:10464
ds_read_b64_tr_b16 a[150:151],v45 offset:15584
v_mfma_f32_16x16x32_bf16 a[16:19],a[164:167],v[68:71],a[16:19]
v_accvgpr_read_b32 v196,a4
v_accvgpr_read_b32 v197,a5
v_mfma_f32_16x16x32_bf16 a[16:19],a[168:171],v[72:75],a[16:19]
v_cvt_pk_bf16_f32 v196,v196,v197
v_accvgpr_read_b32 v198,a6
v_mfma_f32_16x16x32_bf16 a[16:19],a[172:175],v[76:79],a[16:19]
v_accvgpr_read_b32 v199,a7
v_cvt_pk_bf16_f32 v197,v198,v199
v_mfma_f32_16x16x32_bf16 a[20:23],a[172:175],v[92:95],a[20:23]
ds_read_b64_tr_b16 a[152:153],v45 offset:20704
ds_read_b64_tr_b16 a[154:155],v45 offset:25824
v_mfma_f32_16x16x32_bf16 a[20:23],a[168:171],v[88:91],a[20:23]
ds_read_b64_tr_b16 a[156:157],v45 offset:30944
v_accvgpr_read_b32 v208,a8
v_mfma_f32_16x16x32_bf16 a[20:23],a[164:167],v[84:87],a[20:23]
ds_read_b64_tr_b16 a[158:159],v45 offset:36064
v_accvgpr_read_b32 v209,a9
v_mfma_f32_16x16x32_bf16 a[20:23],a[160:163],v[80:83],a[20:23]
v_cvt_pk_bf16_f32 v208,v208,v209
v_accvgpr_read_b32 v210,a10
v_mfma_f32_16x16x32_bf16 a[24:27],a[160:163],v[96:99],a[24:27]
v_accvgpr_read_b32 v211,a11
v_cvt_pk_bf16_f32 v209,v210,v211
v_mfma_f32_16x16x32_bf16 a[24:27],a[164:167],v[100:103],a[24:27]
ds_read_b64_tr_b16 a[160:161],v42 offset:256
ds_read_b64_tr_b16 a[162:163],v42 offset:5376
v_mfma_f32_16x16x32_bf16 a[24:27],a[168:171],v[104:107],a[24:27]
ds_read_b64_tr_b16 a[164:165],v42 offset:10496
v_mfma_f32_16x16x32_bf16 a[24:27],a[172:175],v[108:111],a[24:27]
ds_read_b64_tr_b16 a[166:167],v42 offset:15616
v_mfma_f32_16x16x32_bf16 a[16:19],a[176:179],v[136:139],a[16:19]
ds_read_b64_tr_b16 a[168:169],v42 offset:20736
v_mfma_f32_16x16x32_bf16 a[16:19],a[180:183],v[140:143],a[16:19]
ds_read_b64_tr_b16 a[170:171],v42 offset:25856
ds_read_b64_tr_b16 a[172:173],v42 offset:30976
v_mfma_f32_16x16x32_bf16 a[16:19],a[184:187],v[152:155],a[16:19]
ds_read_b64_tr_b16 a[174:175],v42 offset:36096
v_mfma_f32_16x16x32_bf16 a[16:19],a[188:191],v[156:159],a[16:19]
v_mfma_f32_16x16x32_bf16 a[20:23],a[188:191],v[188:191],a[20:23]
v_mfma_f32_16x16x32_bf16 a[20:23],a[184:187],v[184:187],a[20:23]
v_mfma_f32_16x16x32_bf16 a[20:23],a[180:183],v[172:175],a[20:23]
v_mfma_f32_16x16x32_bf16 a[20:23],a[176:179],v[168:171],a[20:23]
v_mfma_f32_16x16x32_bf16 a[24:27],a[176:179],v[200:203],a[24:27]
ds_read_b64_tr_b16 a[176:177],v44 offset:256
ds_read_b64_tr_b16 a[178:179],v44 offset:5376
v_mfma_f32_16x16x32_bf16 a[24:27],a[180:183],v[204:207],a[24:27]
s_lshl_b32 s95,s42,2
s_add_u32 s95,s95,s40
s_lshl_b32 s95,s95,4
v_mfma_f32_16x16x32_bf16 a[24:27],a[184:187],v[216:219],a[24:27]
s_add_u32 s95,s95,5120
v_mov_b32 v18,s68
v_mov_b32 v19,s69
v_mfma_f32_16x16x32_bf16 a[24:27],a[188:191],v[220:223],a[24:27]
s_waitcnt vmcnt(0)
s_mov_b64 exec,1
buffer_store_dwordx2 v[18:19],v6,s[64:67],s95 offen sc1
s_mov_b64 exec,-1
v_mfma_f32_16x16x32_bf16 a[32:35],a[96:99],v[64:67],a[32:35]
v_accvgpr_read_b32 v194,a16
v_accvgpr_read_b32 v195,a17
v_mfma_f32_16x16x32_bf16 a[32:35],a[100:103],v[68:71],a[32:35]
v_accvgpr_read_b32 v12,a18
v_accvgpr_read_b32 v13,a19
v_mfma_f32_16x16x32_bf16 a[32:35],a[104:107],v[72:75],a[32:35]
v_cvt_pk_bf16_f32 v194,v194,v195
v_cvt_pk_bf16_f32 v195,v12,v13
v_mfma_f32_16x16x32_bf16 a[32:35],a[108:111],v[76:79],a[32:35]
buffer_store_dwordx4 v[192:195],v6,s[32:35],s84 offen offset:2048 sc1
v_mfma_f32_16x16x32_bf16 a[36:39],a[108:111],v[92:95],a[36:39]
v_accvgpr_read_b32 v198,a20
v_accvgpr_read_b32 v199,a21
v_mfma_f32_16x16x32_bf16 a[36:39],a[104:107],v[88:91],a[36:39]
v_accvgpr_read_b32 v14,a22
v_accvgpr_read_b32 v15,a23
v_mfma_f32_16x16x32_bf16 a[36:39],a[100:103],v[84:87],a[36:39]
v_cvt_pk_bf16_f32 v198,v198,v199
v_cvt_pk_bf16_f32 v199,v14,v15
v_mfma_f32_16x16x32_bf16 a[36:39],a[96:99],v[80:83],a[36:39]
buffer_store_dwordx4 v[196:199],v6,s[32:35],s85 offen offset:2048 sc1
v_mfma_f32_16x16x32_bf16 a[40:43],a[96:99],v[96:99],a[40:43]
v_accvgpr_read_b32 v210,a24
v_accvgpr_read_b32 v211,a25
v_mfma_f32_16x16x32_bf16 a[40:43],a[100:103],v[100:103],a[40:43]
v_accvgpr_read_b32 v16,a26
v_accvgpr_read_b32 v17,a27
v_mfma_f32_16x16x32_bf16 a[40:43],a[104:107],v[104:107],a[40:43]
v_cvt_pk_bf16_f32 v210,v210,v211
v_cvt_pk_bf16_f32 v211,v16,v17
v_mfma_f32_16x16x32_bf16 a[40:43],a[108:111],v[108:111],a[40:43]
buffer_store_dwordx4 v[208:211],v6,s[32:35],s86 offen offset:2048 sc1
v_mfma_f32_16x16x32_bf16 a[32:35],a[112:115],v[136:139],a[32:35]
s_waitcnt lgkmcnt(0)
ds_read_b64_tr_b16 a[180:181],v44 offset:10496
ds_read_b64_tr_b16 a[182:183],v44 offset:15616
v_mfma_f32_16x16x32_bf16 a[32:35],a[116:119],v[140:143],a[32:35]
ds_read_b64_tr_b16 a[184:185],v44 offset:20736
v_mfma_f32_16x16x32_bf16 a[32:35],a[120:123],v[152:155],a[32:35]
ds_read_b64_tr_b16 a[186:187],v44 offset:25856
v_mfma_f32_16x16x32_bf16 a[32:35],a[124:127],v[156:159],a[32:35]
ds_read_b64_tr_b16 a[188:189],v44 offset:30976
v_mfma_f32_16x16x32_bf16 a[36:39],a[124:127],v[188:191],a[36:39]
ds_read_b64_tr_b16 a[190:191],v44 offset:36096
ds_read_b64_tr_b16 a[96:97],v43 offset:288
v_mfma_f32_16x16x32_bf16 a[36:39],a[120:123],v[184:187],a[36:39]
ds_read_b64_tr_b16 a[98:99],v43 offset:5408
v_mfma_f32_16x16x32_bf16 a[36:39],a[116:119],v[172:175],a[36:39]
ds_read_b64_tr_b16 a[100:101],v43 offset:10528
v_mfma_f32_16x16x32_bf16 a[36:39],a[112:115],v[168:171],a[36:39]
ds_read_b64_tr_b16 a[102:103],v43 offset:15648
v_mfma_f32_16x16x32_bf16 a[40:43],a[112:115],v[200:203],a[40:43]
ds_read_b64_tr_b16 a[104:105],v43 offset:20768
ds_read_b64_tr_b16 a[106:107],v43 offset:25888
v_mfma_f32_16x16x32_bf16 a[40:43],a[116:119],v[204:207],a[40:43]
v_accvgpr_read_b32 v224,a32
v_accvgpr_read_b32 v225,a33
v_mfma_f32_16x16x32_bf16 a[40:43],a[120:123],v[216:219],a[40:43]
s_lshl_b32 s94,s71,2
v_cvt_pk_bf16_f32 v224,v224,v225
v_accvgpr_read_b32 v226,a34
v_mfma_f32_16x16x32_bf16 a[40:43],a[124:127],v[220:223],a[40:43]
s_add_u32 s94,s94,s40
v_accvgpr_read_b32 v227,a35
v_cvt_pk_bf16_f32 v225,v226,v227
v_mfma_f32_16x16x32_bf16 a[48:51],a[128:131],v[64:67],a[48:51]
s_lshl_b32 s94,s94,4
v_bfe_u32 v24,v6,4,2
v_min_u32 v24,2,v24
v_mfma_f32_16x16x32_bf16 a[48:51],a[132:135],v[68:71],a[48:51]
s_add_u32 s94,s94,5120
v_accvgpr_read_b32 v228,a36
v_accvgpr_read_b32 v229,a37
v_mfma_f32_16x16x32_bf16 a[48:51],a[136:139],v[72:75],a[48:51]
v_cvt_pk_bf16_f32 v228,v228,v229
v_accvgpr_read_b32 v230,a38
v_mfma_f32_16x16x32_bf16 a[48:51],a[140:143],v[76:79],a[48:51]
v_accvgpr_read_b32 v231,a39
v_cvt_pk_bf16_f32 v229,v230,v231
v_mfma_f32_16x16x32_bf16 a[52:55],a[140:143],v[92:95],a[52:55]
v_add_u32 v24,s70,v24
v_add_u32 v24,1,v24
v_mfma_f32_16x16x32_bf16 a[52:55],a[136:139],v[88:91],a[52:55]
v_accvgpr_read_b32 v240,a40
v_accvgpr_read_b32 v241,a41
v_mfma_f32_16x16x32_bf16 a[52:55],a[132:135],v[84:87],a[52:55]
v_cvt_pk_bf16_f32 v240,v240,v241
v_accvgpr_read_b32 v242,a42
v_mfma_f32_16x16x32_bf16 a[52:55],a[128:131],v[80:83],a[52:55]
v_accvgpr_read_b32 v243,a43
v_cvt_pk_bf16_f32 v241,v242,v243
v_mfma_f32_16x16x32_bf16 a[56:59],a[128:131],v[96:99],a[56:59]
v_and_b32 v24,3,v24
v_lshlrev_b32 v24,8,v24
v_mfma_f32_16x16x32_bf16 a[56:59],a[132:135],v[100:103],a[56:59]
ds_read_b64_tr_b16 a[108:109],v43 offset:31008
ds_read_b64_tr_b16 a[110:111],v43 offset:36128
v_mfma_f32_16x16x32_bf16 a[56:59],a[136:139],v[104:107],a[56:59]
ds_read_b64_tr_b16 a[112:113],v45 offset:288
v_mfma_f32_16x16x32_bf16 a[56:59],a[140:143],v[108:111],a[56:59]
ds_read_b64_tr_b16 a[114:115],v45 offset:5408
v_mfma_f32_16x16x32_bf16 a[48:51],a[144:147],v[136:139],a[48:51]
ds_read_b64_tr_b16 a[116:117],v45 offset:10528
v_mfma_f32_16x16x32_bf16 a[48:51],a[148:151],v[140:143],a[48:51]
ds_read_b64_tr_b16 a[118:119],v45 offset:15648
ds_read_b64_tr_b16 a[120:121],v45 offset:20768
v_mfma_f32_16x16x32_bf16 a[48:51],a[152:155],v[152:155],a[48:51]
ds_read_b64_tr_b16 a[122:123],v45 offset:25888
v_mfma_f32_16x16x32_bf16 a[48:51],a[156:159],v[156:159],a[48:51]
ds_read_b64_tr_b16 a[124:125],v45 offset:31008
v_mfma_f32_16x16x32_bf16 a[52:55],a[156:159],v[188:191],a[52:55]
ds_read_b64_tr_b16 a[126:127],v45 offset:36128
v_mfma_f32_16x16x32_bf16 a[52:55],a[152:155],v[184:187],a[52:55]
ds_read_b64_tr_b16 a[128:129],v1
ds_read_b64_tr_b16 a[130:131],v1 offset:5120
v_mfma_f32_16x16x32_bf16 a[52:55],a[148:151],v[172:175],a[52:55]
ds_read_b64_tr_b16 a[132:133],v1 offset:10240
v_mfma_f32_16x16x32_bf16 a[52:55],a[144:147],v[168:171],a[52:55]
ds_read_b64_tr_b16 a[134:135],v1 offset:15360
v_mfma_f32_16x16x32_bf16 a[56:59],a[144:147],v[200:203],a[56:59]
ds_read_b64_tr_b16 a[136:137],v1 offset:20480
v_mfma_f32_16x16x32_bf16 a[56:59],a[148:151],v[204:207],a[56:59]
v_accvgpr_read_b32 v226,a48
v_accvgpr_read_b32 v227,a49
v_mfma_f32_16x16x32_bf16 a[56:59],a[152:155],v[216:219],a[56:59]
v_accvgpr_read_b32 v12,a50
v_accvgpr_read_b32 v13,a51
v_mfma_f32_16x16x32_bf16 a[56:59],a[156:159],v[220:223],a[56:59]
v_cvt_pk_bf16_f32 v226,v226,v227
v_cvt_pk_bf16_f32 v227,v12,v13
v_mfma_f32_16x16x32_bf16 a[64:67],a[160:163],v[64:67],a[64:67]
buffer_store_dwordx4 v[224:227],v6,s[32:35],s84 offen offset:3072 sc1
v_mfma_f32_16x16x32_bf16 a[64:67],a[164:167],v[68:71],a[64:67]
v_accvgpr_read_b32 v230,a52
v_accvgpr_read_b32 v231,a53
v_mfma_f32_16x16x32_bf16 a[64:67],a[168:171],v[72:75],a[64:67]
v_accvgpr_read_b32 v14,a54
v_accvgpr_read_b32 v15,a55
v_mfma_f32_16x16x32_bf16 a[64:67],a[172:175],v[76:79],a[64:67]
v_cvt_pk_bf16_f32 v230,v230,v231
v_cvt_pk_bf16_f32 v231,v14,v15
v_mfma_f32_16x16x32_bf16 a[68:71],a[172:175],v[92:95],a[68:71]
)A"
R"A(buffer_store_dwordx4 v[228:231],v6,s[32:35],s85 offen offset:3072 sc1
v_mfma_f32_16x16x32_bf16 a[68:71],a[168:171],v[88:91],a[68:71]
v_accvgpr_read_b32 v242,a56
v_accvgpr_read_b32 v243,a57
v_mfma_f32_16x16x32_bf16 a[68:71],a[164:167],v[84:87],a[68:71]
v_accvgpr_read_b32 v16,a58
v_accvgpr_read_b32 v17,a59
v_mfma_f32_16x16x32_bf16 a[68:71],a[160:163],v[80:83],a[68:71]
v_cvt_pk_bf16_f32 v242,v242,v243
v_cvt_pk_bf16_f32 v243,v16,v17
v_mfma_f32_16x16x32_bf16 a[72:75],a[160:163],v[96:99],a[72:75]
buffer_store_dwordx4 v[240:243],v6,s[32:35],s86 offen offset:3072 sc1
v_mfma_f32_16x16x32_bf16 a[72:75],a[164:167],v[100:103],a[72:75]
ds_read_b64_tr_b16 a[138:139],v1 offset:25600
ds_read_b64_tr_b16 a[140:141],v1 offset:30720
v_mfma_f32_16x16x32_bf16 a[72:75],a[168:171],v[104:107],a[72:75]
ds_read_b64_tr_b16 a[142:143],v1 offset:35840
v_mfma_f32_16x16x32_bf16 a[72:75],a[172:175],v[108:111],a[72:75]
ds_read_b64_tr_b16 a[144:145],v42
v_mfma_f32_16x16x32_bf16 a[64:67],a[176:179],v[136:139],a[64:67]
s_waitcnt lgkmcnt(4)
ds_read_b64_tr_b16 a[146:147],v42 offset:5120
v_mfma_f32_16x16x32_bf16 a[64:67],a[180:183],v[140:143],a[64:67]
ds_read_b64_tr_b16 a[148:149],v42 offset:10240
ds_read_b64_tr_b16 a[150:151],v42 offset:15360
v_mfma_f32_16x16x32_bf16 a[64:67],a[184:187],v[152:155],a[64:67]
buffer_load_dwordx2 v[26:27],v24,s[64:67],s94 offen sc1
v_mfma_f32_16x16x32_bf16 a[64:67],a[188:191],v[156:159],a[64:67]
ds_read_b64_tr_b16 a[152:153],v42 offset:20480
ds_read_b64_tr_b16 a[154:155],v42 offset:25600
v_mfma_f32_16x16x32_bf16 a[68:71],a[188:191],v[188:191],a[68:71]
ds_read_b64_tr_b16 a[156:157],v42 offset:30720
v_mfma_f32_16x16x32_bf16 a[68:71],a[184:187],v[184:187],a[68:71]
ds_read_b64_tr_b16 a[158:159],v42 offset:35840
v_mfma_f32_16x16x32_bf16 a[68:71],a[180:183],v[172:175],a[68:71]
ds_read_b64_tr_b16 a[160:161],v44
v_mfma_f32_16x16x32_bf16 a[68:71],a[176:179],v[168:171],a[68:71]
ds_read_b64_tr_b16 a[162:163],v44 offset:5120
ds_read_b64_tr_b16 a[164:165],v44 offset:10240
v_mfma_f32_16x16x32_bf16 a[72:75],a[176:179],v[200:203],a[72:75]
ds_read_b64_tr_b16 a[166:167],v44 offset:15360
v_mfma_f32_16x16x32_bf16 a[72:75],a[180:183],v[204:207],a[72:75]
v_accvgpr_read_b32 v128,a64
v_accvgpr_read_b32 v129,a65
v_mfma_f32_16x16x32_bf16 a[72:75],a[184:187],v[216:219],a[72:75]
v_cvt_pk_bf16_f32 v128,v128,v129
v_accvgpr_read_b32 v130,a66
v_mfma_f32_16x16x32_bf16 a[72:75],a[188:191],v[220:223],a[72:75]
v_accvgpr_read_b32 v131,a67
v_cvt_pk_bf16_f32 v129,v130,v131
v_mfma_f32_16x16x32_bf16 a[80:83],a[96:99],v[64:67],a[80:83]
ds_read_b64_tr_b16 a[168:169],v44 offset:20480
ds_read_b64_tr_b16 a[170:171],v44 offset:25600
v_mfma_f32_16x16x32_bf16 a[80:83],a[100:103],v[68:71],a[80:83]
ds_read_b64_tr_b16 a[172:173],v44 offset:30720
v_accvgpr_read_b32 v132,a68
v_mfma_f32_16x16x32_bf16 a[80:83],a[104:107],v[72:75],a[80:83]
ds_read_b64_tr_b16 a[174:175],v44 offset:35840
v_accvgpr_read_b32 v133,a69
v_mfma_f32_16x16x32_bf16 a[80:83],a[108:111],v[76:79],a[80:83]
ds_read_b64_tr_b16 a[176:177],v2 offset:32
v_cvt_pk_bf16_f32 v132,v132,v133
v_mfma_f32_16x16x32_bf16 a[84:87],a[108:111],v[92:95],a[84:87]
ds_read_b64_tr_b16 a[178:179],v2 offset:5152
v_accvgpr_read_b32 v134,a70
v_mfma_f32_16x16x32_bf16 a[84:87],a[104:107],v[88:91],a[84:87]
v_accvgpr_read_b32 v144,a72
v_accvgpr_read_b32 v145,a73
v_mfma_f32_16x16x32_bf16 a[84:87],a[100:103],v[84:87],a[84:87]
v_accvgpr_read_b32 v135,a71
v_cvt_pk_bf16_f32 v144,v144,v145
v_mfma_f32_16x16x32_bf16 a[84:87],a[96:99],v[80:83],a[84:87]
v_accvgpr_read_b32 v146,a74
v_accvgpr_read_b32 v147,a75
v_mfma_f32_16x16x32_bf16 a[88:91],a[96:99],v[96:99],a[88:91]
ds_read_b64_tr_b16 a[180:181],v2 offset:10272
ds_read_b64_tr_b16 a[182:183],v2 offset:15392
v_mfma_f32_16x16x32_bf16 a[88:91],a[100:103],v[100:103],a[88:91]
v_cvt_pk_bf16_f32 v133,v134,v135
v_cvt_pk_bf16_f32 v145,v146,v147
v_mfma_f32_16x16x32_bf16 a[88:91],a[104:107],v[104:107],a[88:91]
ds_read_b64_tr_b16 a[184:185],v2 offset:20512
ds_read_b64_tr_b16 a[186:187],v2 offset:25632
v_mfma_f32_16x16x32_bf16 a[88:91],a[108:111],v[108:111],a[88:91]
ds_read_b64_tr_b16 a[188:189],v2 offset:30752
v_mfma_f32_16x16x32_bf16 a[80:83],a[112:115],v[136:139],a[80:83]
ds_read_b64_tr_b16 a[190:191],v2 offset:35872
v_mfma_f32_16x16x32_bf16 a[80:83],a[116:119],v[140:143],a[80:83]
ds_read_b64_tr_b16 a[96:97],v43 offset:32
v_mfma_f32_16x16x32_bf16 a[80:83],a[120:123],v[152:155],a[80:83]
ds_read_b64_tr_b16 a[98:99],v43 offset:5152
ds_read_b64_tr_b16 a[100:101],v43 offset:10272
v_mfma_f32_16x16x32_bf16 a[80:83],a[124:127],v[156:159],a[80:83]
ds_read_b64_tr_b16 a[102:103],v43 offset:15392
v_mfma_f32_16x16x32_bf16 a[84:87],a[124:127],v[188:191],a[84:87]
ds_read_b64_tr_b16 a[104:105],v43 offset:20512
v_mfma_f32_16x16x32_bf16 a[84:87],a[120:123],v[184:187],a[84:87]
ds_read_b64_tr_b16 a[106:107],v43 offset:25632
v_mfma_f32_16x16x32_bf16 a[84:87],a[116:119],v[172:175],a[84:87]
ds_read_b64_tr_b16 a[108:109],v43 offset:30752
ds_read_b64_tr_b16 a[110:111],v43 offset:35872
v_mfma_f32_16x16x32_bf16 a[84:87],a[112:115],v[168:171],a[84:87]
v_mfma_f32_16x16x32_bf16 a[88:91],a[112:115],v[200:203],a[88:91]
ds_read_b64_tr_b16 a[112:113],v45 offset:32
ds_read_b64_tr_b16 a[114:115],v45 offset:5152
v_mfma_f32_16x16x32_bf16 a[88:91],a[116:119],v[204:207],a[88:91]
v_accvgpr_read_b32 v130,a80
v_accvgpr_read_b32 v131,a81
v_mfma_f32_16x16x32_bf16 a[88:91],a[120:123],v[216:219],a[88:91]
v_accvgpr_read_b32 v12,a82
v_accvgpr_read_b32 v13,a83
v_mfma_f32_16x16x32_bf16 a[88:91],a[124:127],v[220:223],a[88:91]
v_cvt_pk_bf16_f32 v130,v130,v131
v_cvt_pk_bf16_f32 v131,v12,v13
v_mfma_f32_16x16x32_bf16 a[204:207],a[128:131],v[148:151],a[204:207]
buffer_store_dwordx4 v[128:131],v25,s[32:35],s84 offen sc1
v_mfma_f32_16x16x32_bf16 a[204:207],a[132:135],v[180:183],a[204:207]
s_waitcnt lgkmcnt(4)
v_accvgpr_read_b32 v134,a84
v_accvgpr_read_b32 v135,a85
v_mfma_f32_16x16x32_bf16 a[204:207],a[136:139],v[212:215],a[204:207]
s_lshl_b32 s95,s42,2
s_add_u32 s95,s95,s40
s_lshl_b32 s95,s95,4
v_mfma_f32_16x16x32_bf16 a[204:207],a[140:143],v[244:247],a[204:207]
v_mov_b32 v18,s68
v_mov_b32 v19,s69
v_mfma_f32_16x16x32_bf16 a[204:207],a[144:147],v[112:115],a[204:207]
s_waitcnt vmcnt(2)
s_mov_b64 exec,1
buffer_store_dwordx2 v[18:19],v6,s[64:67],s95 offen offset:2048 sc1
s_mov_b64 exec,-1
s_waitcnt vmcnt(2)
v_cmp_ne_u32 s[90:91],s68,v26
v_cmp_ne_u32 s[92:93],s69,v27
s_nop 0
s_or_b64 s[90:91],s[90:91],s[92:93]
s_cbranch_scc1 L_fr10_%=
L_fo10_%=:
v_mfma_f32_16x16x32_bf16 a[204:207],a[148:151],v[116:119],a[204:207]
s_lshl_b32 s94,s71,2
v_accvgpr_read_b32 v146,a88
v_accvgpr_read_b32 v147,a89
v_mfma_f32_16x16x32_bf16 a[204:207],a[152:155],v[120:123],a[204:207]
v_accvgpr_read_b32 v14,a86
v_accvgpr_read_b32 v15,a87
v_cvt_pk_bf16_f32 v134,v134,v135
v_accvgpr_read_b32 v16,a90
v_accvgpr_read_b32 v17,a91
v_cvt_pk_bf16_f32 v146,v146,v147
v_cvt_pk_bf16_f32 v135,v14,v15
v_cvt_pk_bf16_f32 v147,v16,v17
v_mfma_f32_16x16x32_bf16 a[204:207],a[156:159],v[124:127],a[204:207]
s_add_u32 s94,s94,s40
ds_read_b64_tr_b16 a[116:117],v45 offset:10272
ds_read_b64_tr_b16 a[118:119],v45 offset:15392
v_mfma_f32_16x16x32_bf16 a[204:207],a[160:163],v[232:235],a[204:207]
s_lshl_b32 s94,s94,4
buffer_store_dwordx4 v[132:135],v25,s[32:35],s85 offen sc1
v_mfma_f32_16x16x32_bf16 a[204:207],a[164:167],v[236:239],a[204:207]
ds_read_b64_tr_b16 a[120:121],v45 offset:20512
ds_read_b64_tr_b16 a[122:123],v45 offset:25632
v_mfma_f32_16x16x32_bf16 a[204:207],a[168:171],v[248:251],a[204:207]
ds_read_b64_tr_b16 a[124:125],v45 offset:30752
ds_read_b64_tr_b16 a[126:127],v45 offset:35872
v_mfma_f32_16x16x32_bf16 a[204:207],a[172:175],v[252:255],a[204:207]
ds_read_b64_tr_b16 a[128:129],v1 offset:64
ds_read_b64_tr_b16 a[130:131],v1 offset:5184
v_mfma_f32_16x16x32_bf16 a[220:223],a[176:179],v[148:151],a[220:223]
buffer_store_dwordx4 v[144:147],v25,s[32:35],s86 offen sc1
v_mfma_f32_16x16x32_bf16 a[220:223],a[180:183],v[180:183],a[220:223]
ds_read_b64_tr_b16 a[132:133],v1 offset:10304
ds_read_b64_tr_b16 a[134:135],v1 offset:15424
v_mfma_f32_16x16x32_bf16 a[220:223],a[184:187],v[212:215],a[220:223]
ds_read_b64_tr_b16 a[136:137],v1 offset:20544
ds_read_b64_tr_b16 a[138:139],v1 offset:25664
v_mfma_f32_16x16x32_bf16 a[220:223],a[188:191],v[244:247],a[220:223]
ds_read_b64_tr_b16 a[140:141],v1 offset:30784
ds_read_b64_tr_b16 a[142:143],v1 offset:35904
v_mfma_f32_16x16x32_bf16 a[220:223],a[96:99],v[112:115],a[220:223]
buffer_load_dwordx4 a[192:195],v6,s[32:35],s76 offen sc1
v_mfma_f32_16x16x32_bf16 a[220:223],a[100:103],v[116:119],a[220:223]
ds_read_b64_tr_b16 a[144:145],v42 offset:64
ds_read_b64_tr_b16 a[146:147],v42 offset:5184
v_mfma_f32_16x16x32_bf16 a[220:223],a[104:107],v[120:123],a[220:223]
s_waitcnt lgkmcnt(10)
ds_read_b64_tr_b16 a[148:149],v42 offset:10304
ds_read_b64_tr_b16 a[150:151],v42 offset:15424
v_mfma_f32_16x16x32_bf16 a[220:223],a[108:111],v[124:127],a[220:223]
ds_read_b64_tr_b16 a[152:153],v42 offset:20544
ds_read_b64_tr_b16 a[154:155],v42 offset:25664
v_mfma_f32_16x16x32_bf16 a[220:223],a[112:115],v[232:235],a[220:223]
buffer_load_dwordx4 a[196:199],v6,s[32:35],s77 offen sc1
v_mfma_f32_16x16x32_bf16 a[220:223],a[116:119],v[236:239],a[220:223]
ds_read_b64_tr_b16 a[156:157],v42 offset:30784
ds_read_b64_tr_b16 a[158:159],v42 offset:35904
v_mfma_f32_16x16x32_bf16 a[220:223],a[120:123],v[248:251],a[220:223]
ds_read_b64_tr_b16 a[160:161],v44 offset:64
ds_read_b64_tr_b16 a[162:163],v44 offset:5184
v_mfma_f32_16x16x32_bf16 a[220:223],a[124:127],v[252:255],a[220:223]
s_waitcnt lgkmcnt(10)
ds_read_b64_tr_b16 a[164:165],v44 offset:10304
ds_read_b64_tr_b16 a[166:167],v44 offset:15424
v_mfma_f32_16x16x32_bf16 a[236:239],a[128:131],v[148:151],a[236:239]
buffer_load_dwordx4 a[200:203],v6,s[32:35],s78 offen sc1
v_mfma_f32_16x16x32_bf16 a[236:239],a[132:135],v[180:183],a[236:239]
ds_read_b64_tr_b16 a[168:169],v44 offset:20544
ds_read_b64_tr_b16 a[170:171],v44 offset:25664
v_mfma_f32_16x16x32_bf16 a[236:239],a[136:139],v[212:215],a[236:239]
ds_read_b64_tr_b16 a[172:173],v44 offset:30784
ds_read_b64_tr_b16 a[174:175],v44 offset:35904
v_mfma_f32_16x16x32_bf16 a[236:239],a[140:143],v[244:247],a[236:239]
s_waitcnt lgkmcnt(10)
ds_read_b64_tr_b16 a[176:177],v2 offset:96
ds_read_b64_tr_b16 a[178:179],v2 offset:5216
v_mfma_f32_16x16x32_bf16 a[236:239],a[144:147],v[112:115],a[236:239]
buffer_load_dwordx4 a[224:227],v6,s[32:35],s76 offen offset:1024 sc1
ds_read_b64_tr_b16 a[180:181],v2 offset:10336
ds_read_b64_tr_b16 a[182:183],v2 offset:15456
v_mfma_f32_16x16x32_bf16 a[236:239],a[148:151],v[116:119],a[236:239]
ds_read_b64_tr_b16 a[184:185],v2 offset:20576
ds_read_b64_tr_b16 a[186:187],v2 offset:25696
v_mfma_f32_16x16x32_bf16 a[236:239],a[152:155],v[120:123],a[236:239]
s_waitcnt lgkmcnt(12)
ds_read_b64_tr_b16 a[188:189],v2 offset:30816
ds_read_b64_tr_b16 a[190:191],v2 offset:35936
v_mfma_f32_16x16x32_bf16 a[236:239],a[156:159],v[124:127],a[236:239]
ds_read_b64_tr_b16 a[96:97],v43 offset:96
ds_read_b64_tr_b16 a[98:99],v43 offset:5216
v_mfma_f32_16x16x32_bf16 a[236:239],a[160:163],v[232:235],a[236:239]
s_waitcnt lgkmcnt(14)
buffer_load_dwordx4 a[228:231],v6,s[32:35],s77 offen offset:1024 sc1
ds_read_b64_tr_b16 a[100:101],v43 offset:10336
ds_read_b64_tr_b16 a[102:103],v43 offset:15456
v_mfma_f32_16x16x32_bf16 a[236:239],a[164:167],v[236:239],a[236:239]
s_waitcnt lgkmcnt(14)
ds_read_b64_tr_b16 a[104:105],v43 offset:20576
ds_read_b64_tr_b16 a[106:107],v43 offset:25696
v_mfma_f32_16x16x32_bf16 a[236:239],a[168:171],v[248:251],a[236:239]
s_waitcnt lgkmcnt(14)
ds_read_b64_tr_b16 a[108:109],v43 offset:30816
ds_read_b64_tr_b16 a[110:111],v43 offset:35936
v_mfma_f32_16x16x32_bf16 a[236:239],a[172:175],v[252:255],a[236:239]
s_waitcnt lgkmcnt(14)
ds_read_b64_tr_b16 a[112:113],v45 offset:96
ds_read_b64_tr_b16 a[114:115],v45 offset:5216
v_mfma_f32_16x16x32_bf16 a[252:255],a[176:179],v[148:151],a[252:255]
s_waitcnt lgkmcnt(14)
buffer_load_dwordx4 a[232:235],v6,s[32:35],s78 offen offset:1024 sc1
ds_read_b64_tr_b16 a[116:117],v45 offset:10336
ds_read_b64_tr_b16 a[118:119],v45 offset:15456
v_mfma_f32_16x16x32_bf16 a[252:255],a[180:183],v[180:183],a[252:255]
)A"
R"A(s_waitcnt lgkmcnt(14)
ds_read_b64_tr_b16 a[120:121],v45 offset:20576
ds_read_b64_tr_b16 a[122:123],v45 offset:25696
v_mfma_f32_16x16x32_bf16 a[252:255],a[184:187],v[212:215],a[252:255]
s_waitcnt lgkmcnt(14)
ds_read_b64_tr_b16 a[124:125],v45 offset:30816
ds_read_b64_tr_b16 a[126:127],v45 offset:35936
v_mfma_f32_16x16x32_bf16 a[252:255],a[188:191],v[244:247],a[252:255]
s_waitcnt lgkmcnt(14)
ds_read_b64_tr_b16 a[128:129],v1 offset:128
ds_read_b64_tr_b16 a[130:131],v1 offset:5248
v_bfe_u32 v24,v6,4,2
v_min_u32 v24,2,v24
v_mfma_f32_16x16x32_bf16 a[252:255],a[96:99],v[112:115],a[252:255]
s_waitcnt lgkmcnt(14)
ds_read_b64_tr_b16 a[132:133],v1 offset:10368
ds_read_b64_tr_b16 a[134:135],v1 offset:15488
v_add_u32 v24,s70,v24
v_add_u32 v24,1,v24
v_mfma_f32_16x16x32_bf16 a[252:255],a[100:103],v[116:119],a[252:255]
s_waitcnt lgkmcnt(14)
ds_read_b64_tr_b16 a[136:137],v1 offset:20608
ds_read_b64_tr_b16 a[138:139],v1 offset:25728
v_and_b32 v24,3,v24
v_lshlrev_b32 v24,8,v24
v_mfma_f32_16x16x32_bf16 a[252:255],a[104:107],v[120:123],a[252:255]
s_waitcnt lgkmcnt(14)
buffer_load_dwordx2 v[26:27],v24,s[64:67],s94 offen offset:2048 sc1
ds_read_b64_tr_b16 a[140:141],v1 offset:30848
ds_read_b64_tr_b16 a[142:143],v1 offset:35968
v_mfma_f32_16x16x32_bf16 a[252:255],a[108:111],v[124:127],a[252:255]
s_waitcnt lgkmcnt(14)
ds_read_b64_tr_b16 a[144:145],v42 offset:128
ds_read_b64_tr_b16 a[146:147],v42 offset:5248
v_mfma_f32_16x16x32_bf16 a[252:255],a[112:115],v[232:235],a[252:255]
s_waitcnt lgkmcnt(14)
ds_read_b64_tr_b16 a[148:149],v42 offset:10368
ds_read_b64_tr_b16 a[150:151],v42 offset:15488
v_mfma_f32_16x16x32_bf16 a[252:255],a[116:119],v[236:239],a[252:255]
s_lshl_b32 s95,s42,2
ds_read_b64_tr_b16 a[152:153],v42 offset:20608
ds_read_b64_tr_b16 a[154:155],v42 offset:25728
s_waitcnt lgkmcnt(15)
v_mfma_f32_16x16x32_bf16 a[252:255],a[120:123],v[248:251],a[252:255]
s_add_u32 s95,s95,s40
s_lshl_b32 s95,s95,4
ds_read_b64_tr_b16 a[156:157],v42 offset:30848
ds_read_b64_tr_b16 a[158:159],v42 offset:35968
v_mov_b32 v18,s68
v_mov_b32 v19,s69
s_waitcnt lgkmcnt(15)
v_mfma_f32_16x16x32_bf16 a[252:255],a[124:127],v[252:255],a[252:255]
s_waitcnt vmcnt(7)
s_mov_b64 exec,1
buffer_store_dwordx2 v[18:19],v6,s[64:67],s95 offen offset:3072 sc1
s_mov_b64 exec,-1
ds_read_b64_tr_b16 a[160:161],v44 offset:128
ds_read_b64_tr_b16 a[162:163],v44 offset:5248
s_waitcnt lgkmcnt(15)
v_mfma_f32_16x16x32_bf16 a[12:15],a[128:131],v[148:151],a[12:15]
s_waitcnt lgkmcnt(14)
ds_read_b64_tr_b16 a[164:165],v44 offset:10368
ds_read_b64_tr_b16 a[166:167],v44 offset:15488
v_mfma_f32_16x16x32_bf16 a[12:15],a[132:135],v[180:183],a[12:15]
s_waitcnt lgkmcnt(14)
ds_read_b64_tr_b16 a[168:169],v44 offset:20608
ds_read_b64_tr_b16 a[170:171],v44 offset:25728
v_mfma_f32_16x16x32_bf16 a[12:15],a[136:139],v[212:215],a[12:15]
s_waitcnt lgkmcnt(14)
ds_read_b64_tr_b16 a[172:173],v44 offset:30848
ds_read_b64_tr_b16 a[174:175],v44 offset:35968
v_mfma_f32_16x16x32_bf16 a[12:15],a[140:143],v[244:247],a[12:15]
s_waitcnt lgkmcnt(14)
ds_read_b64_tr_b16 a[176:177],v2 offset:160
ds_read_b64_tr_b16 a[178:179],v2 offset:5280
v_mfma_f32_16x16x32_bf16 a[12:15],a[144:147],v[112:115],a[12:15]
s_waitcnt lgkmcnt(14)
ds_read_b64_tr_b16 a[180:181],v2 offset:10400
ds_read_b64_tr_b16 a[182:183],v2 offset:15520
v_mfma_f32_16x16x32_bf16 a[12:15],a[148:151],v[116:119],a[12:15]
s_waitcnt lgkmcnt(14)
ds_read_b64_tr_b16 a[184:185],v2 offset:20640
ds_read_b64_tr_b16 a[186:187],v2 offset:25760
v_mfma_f32_16x16x32_bf16 a[12:15],a[152:155],v[120:123],a[12:15]
s_waitcnt lgkmcnt(14)
ds_read_b64_tr_b16 a[188:189],v2 offset:30880
ds_read_b64_tr_b16 a[190:191],v2 offset:36000
v_mfma_f32_16x16x32_bf16 a[12:15],a[156:159],v[124:127],a[12:15]
s_waitcnt lgkmcnt(14)
ds_read_b64_tr_b16 a[96:97],v43 offset:160
ds_read_b64_tr_b16 a[98:99],v43 offset:5280
v_mfma_f32_16x16x32_bf16 a[12:15],a[160:163],v[232:235],a[12:15]
s_waitcnt lgkmcnt(14)
ds_read_b64_tr_b16 a[100:101],v43 offset:10400
ds_read_b64_tr_b16 a[102:103],v43 offset:15520
v_mfma_f32_16x16x32_bf16 a[12:15],a[164:167],v[236:239],a[12:15]
s_waitcnt lgkmcnt(14)
ds_read_b64_tr_b16 a[104:105],v43 offset:20640
ds_read_b64_tr_b16 a[106:107],v43 offset:25760
v_mfma_f32_16x16x32_bf16 a[12:15],a[168:171],v[248:251],a[12:15]
s_waitcnt lgkmcnt(14)
ds_read_b64_tr_b16 a[108:109],v43 offset:30880
ds_read_b64_tr_b16 a[110:111],v43 offset:36000
v_mfma_f32_16x16x32_bf16 a[12:15],a[172:175],v[252:255],a[12:15]
s_waitcnt lgkmcnt(14)
ds_read_b64_tr_b16 a[112:113],v45 offset:160
ds_read_b64_tr_b16 a[114:115],v45 offset:5280
v_mfma_f32_16x16x32_bf16 a[28:31],a[176:179],v[148:151],a[28:31]
s_waitcnt lgkmcnt(14)
ds_read_b64_tr_b16 a[116:117],v45 offset:10400
ds_read_b64_tr_b16 a[118:119],v45 offset:15520
v_mfma_f32_16x16x32_bf16 a[28:31],a[180:183],v[180:183],a[28:31]
s_waitcnt lgkmcnt(14)
ds_read_b64_tr_b16 a[120:121],v45 offset:20640
ds_read_b64_tr_b16 a[122:123],v45 offset:25760
v_mfma_f32_16x16x32_bf16 a[28:31],a[184:187],v[212:215],a[28:31]
s_waitcnt lgkmcnt(14)
ds_read_b64_tr_b16 a[124:125],v45 offset:30880
ds_read_b64_tr_b16 a[126:127],v45 offset:36000
v_mfma_f32_16x16x32_bf16 a[28:31],a[188:191],v[244:247],a[28:31]
s_waitcnt lgkmcnt(14)
ds_read_b64_tr_b16 a[128:129],v1 offset:192
ds_read_b64_tr_b16 a[130:131],v1 offset:5312
v_mfma_f32_16x16x32_bf16 a[28:31],a[96:99],v[112:115],a[28:31]
s_waitcnt lgkmcnt(14)
ds_read_b64_tr_b16 a[132:133],v1 offset:10432
ds_read_b64_tr_b16 a[134:135],v1 offset:15552
v_mfma_f32_16x16x32_bf16 a[28:31],a[100:103],v[116:119],a[28:31]
s_waitcnt lgkmcnt(14)
ds_read_b64_tr_b16 a[136:137],v1 offset:20672
ds_read_b64_tr_b16 a[138:139],v1 offset:25792
v_mfma_f32_16x16x32_bf16 a[28:31],a[104:107],v[120:123],a[28:31]
s_waitcnt lgkmcnt(14)
ds_read_b64_tr_b16 a[140:141],v1 offset:30912
ds_read_b64_tr_b16 a[142:143],v1 offset:36032
v_mfma_f32_16x16x32_bf16 a[28:31],a[108:111],v[124:127],a[28:31]
s_waitcnt lgkmcnt(14)
ds_read_b64_tr_b16 a[144:145],v42 offset:192
ds_read_b64_tr_b16 a[146:147],v42 offset:5312
v_mfma_f32_16x16x32_bf16 a[28:31],a[112:115],v[232:235],a[28:31]
s_waitcnt lgkmcnt(14)
ds_read_b64_tr_b16 a[148:149],v42 offset:10432
ds_read_b64_tr_b16 a[150:151],v42 offset:15552
v_mfma_f32_16x16x32_bf16 a[28:31],a[116:119],v[236:239],a[28:31]
s_waitcnt lgkmcnt(14)
ds_read_b64_tr_b16 a[152:153],v42 offset:20672
ds_read_b64_tr_b16 a[154:155],v42 offset:25792
v_mfma_f32_16x16x32_bf16 a[28:31],a[120:123],v[248:251],a[28:31]
s_waitcnt lgkmcnt(14)
ds_read_b64_tr_b16 a[156:157],v42 offset:30912
ds_read_b64_tr_b16 a[158:159],v42 offset:36032
v_mfma_f32_16x16x32_bf16 a[28:31],a[124:127],v[252:255],a[28:31]
s_waitcnt lgkmcnt(14)
ds_read_b64_tr_b16 a[160:161],v44 offset:192
ds_read_b64_tr_b16 a[162:163],v44 offset:5312
v_mfma_f32_16x16x32_bf16 a[44:47],a[128:131],v[148:151],a[44:47]
s_waitcnt lgkmcnt(14)
ds_read_b64_tr_b16 a[164:165],v44 offset:10432
ds_read_b64_tr_b16 a[166:167],v44 offset:15552
v_mfma_f32_16x16x32_bf16 a[44:47],a[132:135],v[180:183],a[44:47]
s_waitcnt lgkmcnt(14)
ds_read_b64_tr_b16 a[168:169],v44 offset:20672
ds_read_b64_tr_b16 a[170:171],v44 offset:25792
v_mfma_f32_16x16x32_bf16 a[44:47],a[136:139],v[212:215],a[44:47]
ds_read_b64_tr_b16 a[172:173],v44 offset:30912
ds_read_b64_tr_b16 a[174:175],v44 offset:36032
s_waitcnt vmcnt(1)
v_cmp_ne_u32 s[90:91],s68,v26
v_cmp_ne_u32 s[92:93],s69,v27
s_nop 0
s_or_b64 s[90:91],s[90:91],s[92:93]
s_cbranch_scc1 L_fr11_%=
L_fo11_%=:
s_waitcnt lgkmcnt(15)
v_mfma_f32_16x16x32_bf16 a[44:47],a[140:143],v[244:247],a[44:47]
s_waitcnt lgkmcnt(14)
buffer_load_dwordx4 v[72:75],v6,s[32:35],s76 offen offset:2048 sc1
ds_read_b64_tr_b16 a[176:177],v2 offset:224
ds_read_b64_tr_b16 a[178:179],v2 offset:5344
v_mfma_f32_16x16x32_bf16 a[44:47],a[144:147],v[112:115],a[44:47]
s_lshl_b32 s94,s71,2
ds_read_b64_tr_b16 a[180:181],v2 offset:10464
ds_read_b64_tr_b16 a[182:183],v2 offset:15584
s_waitcnt lgkmcnt(15)
v_mfma_f32_16x16x32_bf16 a[44:47],a[148:151],v[116:119],a[44:47]
s_add_u32 s94,s94,s40
ds_read_b64_tr_b16 a[184:185],v2 offset:20704
ds_read_b64_tr_b16 a[186:187],v2 offset:25824
s_waitcnt lgkmcnt(15)
v_mfma_f32_16x16x32_bf16 a[44:47],a[152:155],v[120:123],a[44:47]
s_lshl_b32 s94,s94,4
ds_read_b64_tr_b16 a[188:189],v2 offset:30944
ds_read_b64_tr_b16 a[190:191],v2 offset:36064
s_waitcnt lgkmcnt(15)
v_mfma_f32_16x16x32_bf16 a[44:47],a[156:159],v[124:127],a[44:47]
s_waitcnt lgkmcnt(14)
buffer_load_dwordx4 v[88:91],v6,s[32:35],s77 offen offset:2048 sc1
ds_read_b64_tr_b16 a[96:97],v43 offset:224
ds_read_b64_tr_b16 a[98:99],v43 offset:5344
v_mfma_f32_16x16x32_bf16 a[44:47],a[160:163],v[232:235],a[44:47]
s_waitcnt lgkmcnt(14)
ds_read_b64_tr_b16 a[100:101],v43 offset:10464
ds_read_b64_tr_b16 a[102:103],v43 offset:15584
v_mfma_f32_16x16x32_bf16 a[44:47],a[164:167],v[236:239],a[44:47]
s_waitcnt lgkmcnt(14)
ds_read_b64_tr_b16 a[104:105],v43 offset:20704
ds_read_b64_tr_b16 a[106:107],v43 offset:25824
v_mfma_f32_16x16x32_bf16 a[44:47],a[168:171],v[248:251],a[44:47]
s_waitcnt lgkmcnt(14)
ds_read_b64_tr_b16 a[108:109],v43 offset:30944
ds_read_b64_tr_b16 a[110:111],v43 offset:36064
v_mfma_f32_16x16x32_bf16 a[44:47],a[172:175],v[252:255],a[44:47]
s_waitcnt lgkmcnt(14)
buffer_load_dwordx4 v[104:107],v6,s[32:35],s78 offen offset:2048 sc1
ds_read_b64_tr_b16 a[112:113],v45 offset:224
ds_read_b64_tr_b16 a[114:115],v45 offset:5344
v_mfma_f32_16x16x32_bf16 a[60:63],a[176:179],v[148:151],a[60:63]
s_waitcnt lgkmcnt(14)
ds_read_b64_tr_b16 a[116:117],v45 offset:10464
ds_read_b64_tr_b16 a[118:119],v45 offset:15584
v_mfma_f32_16x16x32_bf16 a[60:63],a[180:183],v[180:183],a[60:63]
s_waitcnt lgkmcnt(14)
ds_read_b64_tr_b16 a[120:121],v45 offset:20704
ds_read_b64_tr_b16 a[122:123],v45 offset:25824
v_mfma_f32_16x16x32_bf16 a[60:63],a[184:187],v[212:215],a[60:63]
s_waitcnt lgkmcnt(14)
ds_read_b64_tr_b16 a[124:125],v45 offset:30944
ds_read_b64_tr_b16 a[126:127],v45 offset:36064
v_mfma_f32_16x16x32_bf16 a[60:63],a[188:191],v[244:247],a[60:63]
s_waitcnt lgkmcnt(14)
buffer_load_dwordx4 v[76:79],v6,s[32:35],s76 offen offset:3072 sc1
v_mfma_f32_16x16x32_bf16 a[60:63],a[96:99],v[112:115],a[60:63]
s_waitcnt lgkmcnt(12)
ds_read_b64_tr_b16 a[128:129],v1 offset:256
ds_read_b64_tr_b16 a[130:131],v1 offset:5376
v_mfma_f32_16x16x32_bf16 a[60:63],a[100:103],v[116:119],a[60:63]
s_waitcnt lgkmcnt(12)
ds_read_b64_tr_b16 a[132:133],v1 offset:10496
ds_read_b64_tr_b16 a[134:135],v1 offset:15616
v_mfma_f32_16x16x32_bf16 a[60:63],a[104:107],v[120:123],a[60:63]
s_waitcnt lgkmcnt(12)
ds_read_b64_tr_b16 a[136:137],v1 offset:20736
ds_read_b64_tr_b16 a[138:139],v1 offset:25856
v_mfma_f32_16x16x32_bf16 a[60:63],a[108:111],v[124:127],a[60:63]
s_waitcnt lgkmcnt(12)
buffer_load_dwordx4 v[92:95],v6,s[32:35],s77 offen offset:3072 sc1
v_mfma_f32_16x16x32_bf16 a[60:63],a[112:115],v[232:235],a[60:63]
s_waitcnt lgkmcnt(10)
ds_read_b64_tr_b16 a[140:141],v1 offset:30976
ds_read_b64_tr_b16 a[142:143],v1 offset:36096
v_mfma_f32_16x16x32_bf16 a[60:63],a[116:119],v[236:239],a[60:63]
s_waitcnt lgkmcnt(10)
ds_read_b64_tr_b16 a[144:145],v42 offset:256
ds_read_b64_tr_b16 a[146:147],v42 offset:5376
v_mfma_f32_16x16x32_bf16 a[60:63],a[120:123],v[248:251],a[60:63]
s_waitcnt lgkmcnt(10)
ds_read_b64_tr_b16 a[148:149],v42 offset:10496
ds_read_b64_tr_b16 a[150:151],v42 offset:15616
v_mfma_f32_16x16x32_bf16 a[60:63],a[124:127],v[252:255],a[60:63]
buffer_load_dwordx4 v[108:111],v6,s[32:35],s78 offen offset:3072 sc1
v_mfma_f32_16x16x32_bf16 a[204:207],v[34:37],a[192:195],a[204:207]
ds_read_b64_tr_b16 a[152:153],v42 offset:20736
ds_read_b64_tr_b16 a[154:155],v42 offset:25856
v_mfma_f32_16x16x32_bf16 a[204:207],v[34:37],a[196:199],a[204:207]
ds_read_b64_tr_b16 a[156:157],v42 offset:30976
ds_read_b64_tr_b16 a[158:159],v42 offset:36096
v_mfma_f32_16x16x32_bf16 a[204:207],v[34:37],a[200:203],a[204:207]
ds_read_b64_tr_b16 a[160:161],v44 offset:256
ds_read_b64_tr_b16 a[162:163],v44 offset:5376
v_mfma_f32_16x16x32_bf16 a[220:223],v[32:35],a[192:195],a[220:223]
ds_read_b64_tr_b16 a[164:165],v44 offset:10496
ds_read_b64_tr_b16 a[166:167],v44 offset:15616
v_mfma_f32_16x16x32_bf16 a[220:223],v[32:35],a[196:199],a[220:223]
)A"
R"A(ds_read_b64_tr_b16 a[168:169],v44 offset:20736
ds_read_b64_tr_b16 a[170:171],v44 offset:25856
v_mfma_f32_16x16x32_bf16 a[220:223],v[32:35],a[200:203],a[220:223]
s_waitcnt lgkmcnt(12)
ds_read_b64_tr_b16 a[172:173],v44 offset:30976
ds_read_b64_tr_b16 a[174:175],v44 offset:36096
v_mfma_f32_16x16x32_bf16 a[76:79],a[128:131],v[148:151],a[76:79]
v_bfe_u32 v24,v6,4,2
v_min_u32 v24,2,v24
v_mfma_f32_16x16x32_bf16 a[76:79],a[132:135],v[180:183],a[76:79]
v_add_u32 v24,s70,v24
v_add_u32 v24,1,v24
v_mfma_f32_16x16x32_bf16 a[76:79],a[136:139],v[212:215],a[76:79]
v_accvgpr_read_b32 v160,a204
v_accvgpr_read_b32 v161,a205
v_mfma_f32_16x16x32_bf16 a[76:79],a[140:143],v[244:247],a[76:79]
v_cvt_pk_bf16_f32 v160,v160,v161
v_accvgpr_read_b32 v162,a206
v_mfma_f32_16x16x32_bf16 a[76:79],a[144:147],v[112:115],a[76:79]
s_waitcnt lgkmcnt(6)
v_accvgpr_read_b32 v163,a207
v_cvt_pk_bf16_f32 v161,v162,v163
v_mfma_f32_16x16x32_bf16 a[76:79],a[148:151],v[116:119],a[76:79]
v_accvgpr_read_b32 v162,a220
v_accvgpr_read_b32 v163,a221
v_mfma_f32_16x16x32_bf16 a[76:79],a[152:155],v[120:123],a[76:79]
v_accvgpr_read_b32 v12,a222
v_accvgpr_read_b32 v13,a223
v_mfma_f32_16x16x32_bf16 a[76:79],a[156:159],v[124:127],a[76:79]
v_cvt_pk_bf16_f32 v162,v162,v163
v_cvt_pk_bf16_f32 v163,v12,v13
v_mfma_f32_16x16x32_bf16 a[76:79],a[160:163],v[232:235],a[76:79]
s_waitcnt lgkmcnt(0)
ds_read_b64_tr_b16 a[176:177],v2 offset:288
ds_read_b64_tr_b16 a[178:179],v2 offset:5408
v_mfma_f32_16x16x32_bf16 a[76:79],a[164:167],v[236:239],a[76:79]
v_permlane16_swap_b32 v160,v162
v_mfma_f32_16x16x32_bf16 a[76:79],a[168:171],v[248:251],a[76:79]
ds_read_b64_tr_b16 a[180:181],v2 offset:10528
ds_read_b64_tr_b16 a[182:183],v2 offset:15648
v_and_b32 v24,3,v24
v_lshlrev_b32 v24,8,v24
v_mfma_f32_16x16x32_bf16 a[76:79],a[172:175],v[252:255],a[76:79]
buffer_load_dwordx2 v[26:27],v24,s[64:67],s94 offen offset:3072 sc1
ds_read_b64_tr_b16 a[184:185],v2 offset:20768
ds_read_b64_tr_b16 a[186:187],v2 offset:25888
v_mfma_f32_16x16x32_bf16 a[236:239],v[34:37],a[224:227],a[236:239]
ds_read_b64_tr_b16 a[188:189],v2 offset:31008
ds_read_b64_tr_b16 a[190:191],v2 offset:36128
v_mfma_f32_16x16x32_bf16 a[236:239],v[34:37],a[228:231],a[236:239]
ds_read_b64_tr_b16 a[96:97],v43 offset:288
ds_read_b64_tr_b16 a[98:99],v43 offset:5408
v_mfma_f32_16x16x32_bf16 a[236:239],v[34:37],a[232:235],a[236:239]
ds_read_b64_tr_b16 a[100:101],v43 offset:10528
ds_read_b64_tr_b16 a[102:103],v43 offset:15648
v_mfma_f32_16x16x32_bf16 a[252:255],v[32:35],a[224:227],a[252:255]
ds_read_b64_tr_b16 a[104:105],v43 offset:20768
ds_read_b64_tr_b16 a[106:107],v43 offset:25888
v_mfma_f32_16x16x32_bf16 a[252:255],v[32:35],a[228:231],a[252:255]
ds_read_b64_tr_b16 a[108:109],v43 offset:31008
ds_read_b64_tr_b16 a[110:111],v43 offset:36128
v_mfma_f32_16x16x32_bf16 a[252:255],v[32:35],a[232:235],a[252:255]
s_waitcnt lgkmcnt(14)
ds_read_b64_tr_b16 a[112:113],v45 offset:288
ds_read_b64_tr_b16 a[114:115],v45 offset:5408
v_mfma_f32_16x16x32_bf16 a[92:95],a[176:179],v[148:151],a[92:95]
s_waitcnt lgkmcnt(14)
ds_read_b64_tr_b16 a[116:117],v45 offset:10528
ds_read_b64_tr_b16 a[118:119],v45 offset:15648
v_mfma_f32_16x16x32_bf16 a[92:95],a[180:183],v[180:183],a[92:95]
s_waitcnt lgkmcnt(14)
ds_read_b64_tr_b16 a[120:121],v45 offset:20768
ds_read_b64_tr_b16 a[122:123],v45 offset:25888
v_mfma_f32_16x16x32_bf16 a[92:95],a[184:187],v[212:215],a[92:95]
s_waitcnt lgkmcnt(14)
ds_read_b64_tr_b16 a[124:125],v45 offset:31008
ds_read_b64_tr_b16 a[126:127],v45 offset:36128
v_mfma_f32_16x16x32_bf16 a[92:95],a[188:191],v[244:247],a[92:95]
s_waitcnt lgkmcnt(14)
v_permlane16_swap_b32 v161,v163
v_mfma_f32_16x16x32_bf16 a[92:95],a[96:99],v[112:115],a[92:95]
s_cmp_lg_u32 %2,0
s_cbranch_scc1 L_ea0_%=
buffer_store_dwordx4 v[160:163],v40,s[20:23],s43 offen
s_branch L_eb0_%=
L_ea0_%=:
buffer_store_dwordx4 v[160:163],v40,s[20:23],s43 offen sc1
L_eb0_%=:
s_waitcnt lgkmcnt(12)
v_mfma_f32_16x16x32_bf16 a[92:95],a[100:103],v[116:119],a[92:95]
s_waitcnt lgkmcnt(10)
v_accvgpr_read_b32 v164,a236
v_accvgpr_read_b32 v165,a237
v_mfma_f32_16x16x32_bf16 a[92:95],a[104:107],v[120:123],a[92:95]
s_waitcnt lgkmcnt(8)
v_cvt_pk_bf16_f32 v164,v164,v165
v_accvgpr_read_b32 v166,a238
v_mfma_f32_16x16x32_bf16 a[92:95],a[108:111],v[124:127],a[92:95]
s_waitcnt lgkmcnt(6)
v_accvgpr_read_b32 v167,a239
v_cvt_pk_bf16_f32 v165,v166,v167
v_mfma_f32_16x16x32_bf16 a[92:95],a[112:115],v[232:235],a[92:95]
s_waitcnt lgkmcnt(4)
v_accvgpr_read_b32 v166,a252
v_accvgpr_read_b32 v167,a253
v_mfma_f32_16x16x32_bf16 a[92:95],a[116:119],v[236:239],a[92:95]
s_waitcnt lgkmcnt(2)
v_accvgpr_read_b32 v14,a254
v_accvgpr_read_b32 v15,a255
v_cvt_pk_bf16_f32 v166,v166,v167
v_cvt_pk_bf16_f32 v167,v14,v15
v_mfma_f32_16x16x32_bf16 a[92:95],a[120:123],v[248:251],a[92:95]
s_waitcnt lgkmcnt(0)
v_mfma_f32_16x16x32_bf16 a[92:95],a[124:127],v[252:255],a[92:95]
v_permlane16_swap_b32 v164,v166
v_permlane16_swap_b32 v165,v167
s_nop 3
s_cmp_lg_u32 %2,0
s_cbranch_scc1 L_ea1_%=
buffer_store_dwordx4 v[164:167],v40,s[20:23],s43 offen offset:64
s_branch L_eb1_%=
L_ea1_%=:
buffer_store_dwordx4 v[164:167],v40,s[20:23],s43 offen offset:64 sc1
L_eb1_%=:
s_waitcnt vmcnt(2)
v_cmp_ne_u32 s[90:91],s68,v26
v_cmp_ne_u32 s[92:93],s69,v27
s_nop 0
s_or_b64 s[90:91],s[90:91],s[92:93]
s_cbranch_scc1 L_fr12_%=
L_fo12_%=:
buffer_load_dwordx4 v[136:139],v25,s[32:35],s76 offen sc1
buffer_load_dwordx4 v[168:171],v25,s[32:35],s77 offen sc1
buffer_load_dwordx4 v[200:203],v25,s[32:35],s78 offen sc1
s_cmp_lg_u32 %2,0
s_cbranch_scc1 L_dual_%=
v_mfma_f32_16x16x32_bf16 a[12:15],v[34:37],v[72:75],a[12:15]
v_mfma_f32_16x16x32_bf16 a[12:15],v[34:37],v[88:91],a[12:15]
v_mfma_f32_16x16x32_bf16 a[12:15],v[34:37],v[104:107],a[12:15]
v_mfma_f32_16x16x32_bf16 a[28:31],v[32:35],v[72:75],a[28:31]
v_mfma_f32_16x16x32_bf16 a[28:31],v[32:35],v[88:91],a[28:31]
v_mfma_f32_16x16x32_bf16 a[28:31],v[32:35],v[104:107],a[28:31]
v_mfma_f32_16x16x32_bf16 a[44:47],v[34:37],v[76:79],a[44:47]
v_mfma_f32_16x16x32_bf16 a[44:47],v[34:37],v[92:95],a[44:47]
v_mfma_f32_16x16x32_bf16 a[44:47],v[34:37],v[108:111],a[44:47]
s_nop 1
v_accvgpr_read_b32 v176,a12
v_accvgpr_read_b32 v177,a13
v_cvt_pk_bf16_f32 v176,v176,v177
v_accvgpr_read_b32 v178,a14
v_accvgpr_read_b32 v179,a15
v_cvt_pk_bf16_f32 v177,v178,v179
v_mfma_f32_16x16x32_bf16 a[60:63],v[32:35],v[76:79],a[60:63]
v_mfma_f32_16x16x32_bf16 a[60:63],v[32:35],v[92:95],a[60:63]
v_mfma_f32_16x16x32_bf16 a[60:63],v[32:35],v[108:111],a[60:63]
v_accvgpr_read_b32 v178,a28
v_accvgpr_read_b32 v179,a29
v_accvgpr_read_b32 v16,a30
v_accvgpr_read_b32 v17,a31
v_cvt_pk_bf16_f32 v178,v178,v179
v_cvt_pk_bf16_f32 v179,v16,v17
s_nop 3
v_permlane16_swap_b32 v176,v178
v_permlane16_swap_b32 v177,v179
buffer_store_dwordx4 v[176:179],v40,s[20:23],s43 offen offset:128
v_accvgpr_read_b32 v192,a44
v_accvgpr_read_b32 v193,a45
v_cvt_pk_bf16_f32 v192,v192,v193
v_accvgpr_read_b32 v194,a46
v_accvgpr_read_b32 v195,a47
v_cvt_pk_bf16_f32 v193,v194,v195
v_accvgpr_read_b32 v194,a60
v_accvgpr_read_b32 v195,a61
v_accvgpr_read_b32 v12,a62
v_accvgpr_read_b32 v13,a63
v_cvt_pk_bf16_f32 v194,v194,v195
s_nop 1
v_permlane16_swap_b32 v192,v194
v_cvt_pk_bf16_f32 v195,v12,v13
s_nop 1
v_permlane16_swap_b32 v193,v195
buffer_store_dwordx4 v[192:195],v40,s[20:23],s43 offen offset:192
s_waitcnt vmcnt(4)
v_mfma_f32_16x16x32_bf16 a[76:79],v[34:37],v[136:139],a[76:79]
s_waitcnt vmcnt(3)
v_mfma_f32_16x16x32_bf16 a[76:79],v[34:37],v[168:171],a[76:79]
s_waitcnt vmcnt(2)
v_mfma_f32_16x16x32_bf16 a[76:79],v[34:37],v[200:203],a[76:79]
v_mfma_f32_16x16x32_bf16 a[92:95],v[32:35],v[136:139],a[92:95]
v_mfma_f32_16x16x32_bf16 a[92:95],v[32:35],v[168:171],a[92:95]
v_mfma_f32_16x16x32_bf16 a[92:95],v[32:35],v[200:203],a[92:95]
s_nop 3
s_nop 0
v_accvgpr_read_b32 v196,a76
v_accvgpr_read_b32 v197,a77
v_cvt_pk_bf16_f32 v196,v196,v197
v_accvgpr_read_b32 v198,a78
v_accvgpr_read_b32 v199,a79
v_cvt_pk_bf16_f32 v197,v198,v199
v_accvgpr_read_b32 v198,a92
v_accvgpr_read_b32 v199,a93
v_accvgpr_read_b32 v14,a94
v_accvgpr_read_b32 v15,a95
v_cvt_pk_bf16_f32 v198,v198,v199
s_nop 1
v_permlane16_swap_b32 v196,v198
v_cvt_pk_bf16_f32 v199,v14,v15
s_nop 1
v_permlane16_swap_b32 v197,v199
buffer_store_dwordx4 v[196:199],v40,s[20:23],s43 offen offset:256
s_or_b32 s62,s42,s40
s_cmp_lg_u32 s62,0
s_cbranch_scc1 L_noep_%=
v_mov_b32 v12,s68
s_mov_b64 exec,1
buffer_store_dword v12,v25,s[64:67],0 offen sc1
s_mov_b64 exec,-1
L_noep_%=:
s_endpgm
L_dual_%=:
v_mfma_f32_16x16x32_bf16 a[12:15],v[34:37],v[72:75],a[12:15]
v_mfma_f32_16x16x32_bf16 a[12:15],v[34:37],v[88:91],a[12:15]
v_mfma_f32_16x16x32_bf16 a[12:15],v[34:37],v[104:107],a[12:15]
v_mfma_f32_16x16x32_bf16 a[28:31],v[32:35],v[72:75],a[28:31]
v_mfma_f32_16x16x32_bf16 a[28:31],v[32:35],v[88:91],a[28:31]
v_mfma_f32_16x16x32_bf16 a[28:31],v[32:35],v[104:107],a[28:31]
v_mfma_f32_16x16x32_bf16 a[44:47],v[34:37],v[76:79],a[44:47]
v_mfma_f32_16x16x32_bf16 a[44:47],v[34:37],v[92:95],a[44:47]
v_mfma_f32_16x16x32_bf16 a[44:47],v[34:37],v[108:111],a[44:47]
s_nop 1
v_accvgpr_read_b32 v176,a12
v_accvgpr_read_b32 v177,a13
v_cvt_pk_bf16_f32 v176,v176,v177
v_accvgpr_read_b32 v178,a14
v_accvgpr_read_b32 v179,a15
v_cvt_pk_bf16_f32 v177,v178,v179
v_mfma_f32_16x16x32_bf16 a[60:63],v[32:35],v[76:79],a[60:63]
v_mfma_f32_16x16x32_bf16 a[60:63],v[32:35],v[92:95],a[60:63]
v_mfma_f32_16x16x32_bf16 a[60:63],v[32:35],v[108:111],a[60:63]
v_accvgpr_read_b32 v178,a28
v_accvgpr_read_b32 v179,a29
v_accvgpr_read_b32 v16,a30
v_accvgpr_read_b32 v17,a31
v_cvt_pk_bf16_f32 v178,v178,v179
v_cvt_pk_bf16_f32 v179,v16,v17
s_nop 3
v_permlane16_swap_b32 v176,v178
v_permlane16_swap_b32 v177,v179
buffer_store_dwordx4 v[176:179],v40,s[20:23],s43 offen offset:128 sc1
v_accvgpr_read_b32 v192,a44
v_accvgpr_read_b32 v193,a45
v_cvt_pk_bf16_f32 v192,v192,v193
v_accvgpr_read_b32 v194,a46
v_accvgpr_read_b32 v195,a47
v_cvt_pk_bf16_f32 v193,v194,v195
v_accvgpr_read_b32 v194,a60
v_accvgpr_read_b32 v195,a61
v_accvgpr_read_b32 v12,a62
v_accvgpr_read_b32 v13,a63
v_cvt_pk_bf16_f32 v194,v194,v195
s_nop 1
v_permlane16_swap_b32 v192,v194
v_cvt_pk_bf16_f32 v195,v12,v13
s_nop 1
v_permlane16_swap_b32 v193,v195
buffer_store_dwordx4 v[192:195],v40,s[20:23],s43 offen offset:192 sc1
s_waitcnt vmcnt(4)
v_mfma_f32_16x16x32_bf16 a[76:79],v[34:37],v[136:139],a[76:79]
s_waitcnt vmcnt(3)
v_mfma_f32_16x16x32_bf16 a[76:79],v[34:37],v[168:171],a[76:79]
s_waitcnt vmcnt(2)
v_mfma_f32_16x16x32_bf16 a[76:79],v[34:37],v[200:203],a[76:79]
v_mfma_f32_16x16x32_bf16 a[92:95],v[32:35],v[136:139],a[92:95]
v_mfma_f32_16x16x32_bf16 a[92:95],v[32:35],v[168:171],a[92:95]
v_mfma_f32_16x16x32_bf16 a[92:95],v[32:35],v[200:203],a[92:95]
s_nop 3
s_nop 0
v_accvgpr_read_b32 v196,a76
v_accvgpr_read_b32 v197,a77
v_cvt_pk_bf16_f32 v196,v196,v197
v_accvgpr_read_b32 v198,a78
v_accvgpr_read_b32 v199,a79
v_cvt_pk_bf16_f32 v197,v198,v199
v_accvgpr_read_b32 v198,a92
v_accvgpr_read_b32 v199,a93
v_accvgpr_read_b32 v14,a94
v_accvgpr_read_b32 v15,a95
v_cvt_pk_bf16_f32 v198,v198,v199
s_nop 1
v_permlane16_swap_b32 v196,v198
v_cvt_pk_bf16_f32 v199,v14,v15
s_nop 1
v_permlane16_swap_b32 v197,v199
buffer_store_dwordx4 v[196:199],v40,s[20:23],s43 offen offset:256 sc1
s_or_b32 s62,s42,s40
s_cmp_lg_u32 s62,0
s_cbranch_scc1 L_noep2_%=
v_mov_b32 v12,s68
s_mov_b64 exec,1
buffer_store_dword v12,v25,s[64:67],0 offen sc1
s_mov_b64 exec,-1
L_noep2_%=:
s_endpgm
L_ps2_%=:
s_min_i32 s79,s79,100
s_add_u32 s78,s78,s79
L_pl2_%=:
s_sleep 1
s_sub_u32 s79,s79,4
s_cmp_gt_i32 s79,1
s_cbranch_scc1 L_pl2_%=
s_branch L_pr2_%=
L_ps3_%=:
s_min_i32 s79,s79,100
s_add_u32 s78,s78,s79
L_pl3_%=:
s_sleep 1
s_sub_u32 s79,s79,4
s_cmp_gt_i32 s79,1
s_cbranch_scc1 L_pl3_%=
s_branch L_pr3_%=
L_ps4_%=:
s_min_i32 s79,s79,100
s_add_u32 s78,s78,s79
L_pl4_%=:
s_sleep 1
s_sub_u32 s79,s79,4
s_cmp_gt_i32 s79,1
s_cbranch_scc1 L_pl4_%=
s_branch L_pr4_%=
L_ps5_%=:
s_min_i32 s79,s79,100
s_add_u32 s78,s78,s79
L_pl5_%=:
s_sleep 1
s_sub_u32 s79,s79,4
s_cmp_gt_i32 s79,1
s_cbranch_scc1 L_pl5_%=
s_branch L_pr5_%=
L_ps6_%=:
s_min_i32 s79,s79,100
s_add_u32 s78,s78,s79
L_pl6_%=:
s_sleep 1
s_sub_u32 s79,s79,4
s_cmp_gt_i32 s79,1
s_cbranch_scc1 L_pl6_%=
)A"
R"A(s_branch L_pr6_%=
L_ps7_%=:
s_min_i32 s79,s79,100
s_add_u32 s78,s78,s79
L_pl7_%=:
s_sleep 1
s_sub_u32 s79,s79,4
s_cmp_gt_i32 s79,1
s_cbranch_scc1 L_pl7_%=
s_branch L_pr7_%=
L_fr8_%=:
s_mov_b32 s88,262144
L_fl8_%=:
buffer_load_dwordx2 v[26:27],v24,s[64:67],s94 offen sc1
s_waitcnt vmcnt(0)
s_sub_u32 s88,s88,1
s_cmp_eq_u32 s88,0
s_cbranch_scc1 L_fo8_%=
v_cmp_ne_u32 s[90:91],s68,v26
v_cmp_ne_u32 s[92:93],s69,v27
s_nop 0
s_or_b64 s[90:91],s[90:91],s[92:93]
s_cbranch_scc1 L_fl8_%=
s_branch L_fo8_%=
L_fr9_%=:
s_mov_b32 s88,262144
L_fl9_%=:
buffer_load_dwordx2 v[26:27],v24,s[64:67],s94 offen offset:1024 sc1
s_waitcnt vmcnt(0)
s_sub_u32 s88,s88,1
s_cmp_eq_u32 s88,0
s_cbranch_scc1 L_fo9_%=
v_cmp_ne_u32 s[90:91],s68,v26
v_cmp_ne_u32 s[92:93],s69,v27
s_nop 0
s_or_b64 s[90:91],s[90:91],s[92:93]
s_cbranch_scc1 L_fl9_%=
s_branch L_fo9_%=
L_fr10_%=:
s_mov_b32 s88,262144
L_fl10_%=:
buffer_load_dwordx2 v[26:27],v24,s[64:67],s94 offen sc1
s_waitcnt vmcnt(0)
s_sub_u32 s88,s88,1
s_cmp_eq_u32 s88,0
s_cbranch_scc1 L_fo10_%=
v_cmp_ne_u32 s[90:91],s68,v26
v_cmp_ne_u32 s[92:93],s69,v27
s_nop 0
s_or_b64 s[90:91],s[90:91],s[92:93]
s_cbranch_scc1 L_fl10_%=
s_branch L_fo10_%=
L_fr11_%=:
s_mov_b32 s88,262144
L_fl11_%=:
buffer_load_dwordx2 v[26:27],v24,s[64:67],s94 offen offset:2048 sc1
s_waitcnt vmcnt(0)
s_sub_u32 s88,s88,1
s_cmp_eq_u32 s88,0
s_cbranch_scc1 L_fo11_%=
v_cmp_ne_u32 s[90:91],s68,v26
v_cmp_ne_u32 s[92:93],s69,v27
s_nop 0
s_or_b64 s[90:91],s[90:91],s[92:93]
s_cbranch_scc1 L_fl11_%=
s_branch L_fo11_%=
L_fr12_%=:
s_mov_b32 s88,262144
L_fl12_%=:
buffer_load_dwordx2 v[26:27],v24,s[64:67],s94 offen offset:3072 sc1
s_waitcnt vmcnt(0)
s_sub_u32 s88,s88,1
s_cmp_eq_u32 s88,0
s_cbranch_scc1 L_fo12_%=
v_cmp_ne_u32 s[90:91],s68,v26
v_cmp_ne_u32 s[92:93],s69,v27
s_nop 0
s_or_b64 s[90:91],s[90:91],s[92:93]
s_cbranch_scc1 L_fl12_%=
s_branch L_fo12_%=
L_out_end_%=:
L_fast_end_%=:
L_slow0_%=:
s_mov_b32 s98,0
s_branch L_slowc_%=
L_slow1_%=:
s_mov_b32 s98,1
L_slowc_%=:
s_nop 7
s_nop 7
s_waitcnt vmcnt(0) lgkmcnt(0)
s_lshl_b32 s96,%1,2
s_add_u32 s96,s96,s40
s_lshl_b32 s96,s96,15
s_add_u32 s96,s96,167772160
s_mov_b64 s[56:57],s[12:13]
s_add_u32 s56,s56,s96
s_addc_u32 s57,s57,0
s_and_b32 s57,s57,0xffff
s_mov_b32 s58,32768
s_mov_b32 s59,0x20000
s_mov_b32 s96,0
buffer_store_dwordx4 v[64:67],v6,s[56:59],s96 offen
buffer_store_dwordx4 v[68:71],v6,s[56:59],s96 offen offset:1024
buffer_store_dwordx4 v[72:75],v6,s[56:59],s96 offen offset:2048
buffer_store_dwordx4 v[76:79],v6,s[56:59],s96 offen offset:3072
s_mov_b32 s96,4096
buffer_store_dwordx4 v[80:83],v6,s[56:59],s96 offen
buffer_store_dwordx4 v[84:87],v6,s[56:59],s96 offen offset:1024
buffer_store_dwordx4 v[88:91],v6,s[56:59],s96 offen offset:2048
buffer_store_dwordx4 v[92:95],v6,s[56:59],s96 offen offset:3072
s_mov_b32 s96,8192
buffer_store_dwordx4 v[96:99],v6,s[56:59],s96 offen
buffer_store_dwordx4 v[100:103],v6,s[56:59],s96 offen offset:1024
buffer_store_dwordx4 v[104:107],v6,s[56:59],s96 offen offset:2048
buffer_store_dwordx4 v[108:111],v6,s[56:59],s96 offen offset:3072
s_mov_b32 s96,12288
buffer_store_dwordx4 v[112:115],v6,s[56:59],s96 offen
buffer_store_dwordx4 v[116:119],v6,s[56:59],s96 offen offset:1024
buffer_store_dwordx4 v[120:123],v6,s[56:59],s96 offen offset:2048
buffer_store_dwordx4 v[124:127],v6,s[56:59],s96 offen offset:3072
s_mov_b32 s96,16384
buffer_store_dwordx4 v[128:131],v6,s[56:59],s96 offen
buffer_store_dwordx4 v[132:135],v6,s[56:59],s96 offen offset:1024
buffer_store_dwordx4 v[136:139],v6,s[56:59],s96 offen offset:2048
buffer_store_dwordx4 v[140:143],v6,s[56:59],s96 offen offset:3072
s_mov_b32 s96,20480
buffer_store_dwordx4 v[144:147],v6,s[56:59],s96 offen
buffer_store_dwordx4 v[148:151],v6,s[56:59],s96 offen offset:1024
buffer_store_dwordx4 v[152:155],v6,s[56:59],s96 offen offset:2048
buffer_store_dwordx4 v[156:159],v6,s[56:59],s96 offen offset:3072
s_mov_b32 s96,24576
buffer_store_dwordx4 v[160:163],v6,s[56:59],s96 offen
buffer_store_dwordx4 v[164:167],v6,s[56:59],s96 offen offset:1024
buffer_store_dwordx4 v[168:171],v6,s[56:59],s96 offen offset:2048
buffer_store_dwordx4 v[172:175],v6,s[56:59],s96 offen offset:3072
s_mov_b32 s96,28672
buffer_store_dwordx4 v[176:179],v6,s[56:59],s96 offen
buffer_store_dwordx4 v[180:183],v6,s[56:59],s96 offen offset:1024
buffer_store_dwordx4 v[184:187],v6,s[56:59],s96 offen offset:2048
buffer_store_dwordx4 v[188:191],v6,s[56:59],s96 offen offset:3072
s_waitcnt vmcnt(0)
v_writelane_b32 v173,s88,0
v_writelane_b32 v173,s89,1
v_writelane_b32 v173,s90,2
v_writelane_b32 v173,s91,3
v_writelane_b32 v173,s92,4
v_writelane_b32 v173,s93,5
v_writelane_b32 v173,s94,6
v_writelane_b32 v173,s95,7
s_mov_b32 s88,s98
s_mul_i32 s90,s41,307200
s_mov_b64 s[92:93],s[4:5]
s_add_u32 s92,s92,s90
s_addc_u32 s93,s93,0
s_and_b32 s93,s93,0xffff
s_mov_b32 s94,307200
s_mov_b32 s95,0x20000
s_mov_b64 s[96:97],s[10:11]
s_and_b32 s97,s97,0xffff
s_mov_b32 s98,5242880
s_mov_b32 s99,0x20000
v_bfe_u32 v171,v6,4,4
v_lshrrev_b32 v172,8,v6
v_lshlrev_b32 v164,8,v171
v_lshl_add_u32 v164,v172,4,v164
v_lshlrev_b32 v165,10,v172
v_lshl_add_u32 v165,v171,1,v165
v_add_u32 v166,4096,v165
v_lshlrev_b32 v168,16,v172
v_lshrrev_b32 v170,2,v171
v_lshl_add_u32 v168,v170,4,v168
v_and_b32 v170,3,v171
v_lshl_add_u32 v168,v170,1,v168
s_lshl_b32 s90,s42,1
s_add_u32 s90,s90,s88
s_lshl_b32 s90,s90,8
v_add_u32 v168,s90,v168
s_mov_b32 s89,0
L_sjb_%=:
s_add_u32 s90,s89,s70
s_add_u32 s90,s90,1
s_and_b32 s90,s90,3
s_lshl_b32 s91,s40,2
s_add_u32 s90,s90,s91
s_lshl_b32 s90,s90,4
v_bfe_u32 v171,v6,4,4
v_lshrrev_b32 v172,8,v6
v_add_u32 v167,s90,v171
v_mul_u32_u24 v167,1280,v167
v_lshl_add_u32 v167,v172,4,v167
s_lshl_b32 s90,s88,4
s_add_u32 s90,s90,s89
s_lshl_b32 s90,s90,12
v_add_u32 v169,s90,v6
v_mov_b32 v64,0
v_mov_b32 v65,0
v_mov_b32 v66,0
v_mov_b32 v67,0
v_mov_b32 v68,0
v_mov_b32 v69,0
v_mov_b32 v70,0
v_mov_b32 v71,0
v_mov_b32 v72,0
v_mov_b32 v73,0
v_mov_b32 v74,0
v_mov_b32 v75,0
v_mov_b32 v76,0
v_mov_b32 v77,0
v_mov_b32 v78,0
v_mov_b32 v79,0
v_mov_b32 v80,0
v_mov_b32 v81,0
v_mov_b32 v82,0
v_mov_b32 v83,0
v_mov_b32 v84,0
v_mov_b32 v85,0
v_mov_b32 v86,0
v_mov_b32 v87,0
v_mov_b32 v88,0
v_mov_b32 v89,0
v_mov_b32 v90,0
v_mov_b32 v91,0
v_mov_b32 v92,0
v_mov_b32 v93,0
v_mov_b32 v94,0
v_mov_b32 v95,0
s_mov_b32 s90,0
L_sq_%=:
s_lshl_b32 s91,s90,6
buffer_load_dwordx4 v[160:163],v167,s[92:95],s91 offen
s_lshl_b32 s91,s90,18
buffer_load_ushort v112,v168,s[96:99],s91 offen
buffer_load_ushort v120,v168,s[96:99],s91 offen offset:8
buffer_load_ushort v128,v168,s[96:99],s91 offen offset:64
buffer_load_ushort v136,v168,s[96:99],s91 offen offset:72
s_add_u32 s91,s91,8192
buffer_load_ushort v113,v168,s[96:99],s91 offen
buffer_load_ushort v121,v168,s[96:99],s91 offen offset:8
buffer_load_ushort v129,v168,s[96:99],s91 offen offset:64
buffer_load_ushort v137,v168,s[96:99],s91 offen offset:72
s_add_u32 s91,s91,8192
buffer_load_ushort v114,v168,s[96:99],s91 offen
buffer_load_ushort v122,v168,s[96:99],s91 offen offset:8
buffer_load_ushort v130,v168,s[96:99],s91 offen offset:64
buffer_load_ushort v138,v168,s[96:99],s91 offen offset:72
s_add_u32 s91,s91,8192
buffer_load_ushort v115,v168,s[96:99],s91 offen
buffer_load_ushort v123,v168,s[96:99],s91 offen offset:8
buffer_load_ushort v131,v168,s[96:99],s91 offen offset:64
buffer_load_ushort v139,v168,s[96:99],s91 offen offset:72
s_add_u32 s91,s91,8192
buffer_load_ushort v116,v168,s[96:99],s91 offen
buffer_load_ushort v124,v168,s[96:99],s91 offen offset:8
buffer_load_ushort v132,v168,s[96:99],s91 offen offset:64
buffer_load_ushort v140,v168,s[96:99],s91 offen offset:72
s_add_u32 s91,s91,8192
buffer_load_ushort v117,v168,s[96:99],s91 offen
buffer_load_ushort v125,v168,s[96:99],s91 offen offset:8
buffer_load_ushort v133,v168,s[96:99],s91 offen offset:64
buffer_load_ushort v141,v168,s[96:99],s91 offen offset:72
s_add_u32 s91,s91,8192
buffer_load_ushort v118,v168,s[96:99],s91 offen
buffer_load_ushort v126,v168,s[96:99],s91 offen offset:8
buffer_load_ushort v134,v168,s[96:99],s91 offen offset:64
buffer_load_ushort v142,v168,s[96:99],s91 offen offset:72
s_add_u32 s91,s91,8192
buffer_load_ushort v119,v168,s[96:99],s91 offen
buffer_load_ushort v127,v168,s[96:99],s91 offen offset:8
buffer_load_ushort v135,v168,s[96:99],s91 offen offset:64
buffer_load_ushort v143,v168,s[96:99],s91 offen offset:72
s_add_u32 s91,s91,8192
s_waitcnt vmcnt(0)
v_lshl_or_b32 v112,v113,16,v112
v_lshl_or_b32 v113,v115,16,v114
v_lshl_or_b32 v114,v117,16,v116
v_lshl_or_b32 v115,v119,16,v118
v_lshl_or_b32 v120,v121,16,v120
v_lshl_or_b32 v121,v123,16,v122
v_lshl_or_b32 v122,v125,16,v124
v_lshl_or_b32 v123,v127,16,v126
v_lshl_or_b32 v128,v129,16,v128
v_lshl_or_b32 v129,v131,16,v130
v_lshl_or_b32 v130,v133,16,v132
v_lshl_or_b32 v131,v135,16,v134
v_lshl_or_b32 v136,v137,16,v136
v_lshl_or_b32 v137,v139,16,v138
v_lshl_or_b32 v138,v141,16,v140
v_lshl_or_b32 v139,v143,16,v142
s_nop 1
v_mfma_f32_16x16x32_bf16 v[64:67],v[112:115],v[160:163],v[64:67]
v_mfma_f32_16x16x32_bf16 v[68:71],v[120:123],v[160:163],v[68:71]
v_mfma_f32_16x16x32_bf16 v[72:75],v[128:131],v[160:163],v[72:75]
v_mfma_f32_16x16x32_bf16 v[76:79],v[136:139],v[160:163],v[76:79]
s_nop 7
s_lshl_b32 s91,s90,18
buffer_load_ushort v112,v168,s[96:99],s91 offen offset:128
buffer_load_ushort v120,v168,s[96:99],s91 offen offset:136
buffer_load_ushort v128,v168,s[96:99],s91 offen offset:192
buffer_load_ushort v136,v168,s[96:99],s91 offen offset:200
s_add_u32 s91,s91,8192
buffer_load_ushort v113,v168,s[96:99],s91 offen offset:128
buffer_load_ushort v121,v168,s[96:99],s91 offen offset:136
buffer_load_ushort v129,v168,s[96:99],s91 offen offset:192
buffer_load_ushort v137,v168,s[96:99],s91 offen offset:200
s_add_u32 s91,s91,8192
buffer_load_ushort v114,v168,s[96:99],s91 offen offset:128
buffer_load_ushort v122,v168,s[96:99],s91 offen offset:136
buffer_load_ushort v130,v168,s[96:99],s91 offen offset:192
buffer_load_ushort v138,v168,s[96:99],s91 offen offset:200
s_add_u32 s91,s91,8192
buffer_load_ushort v115,v168,s[96:99],s91 offen offset:128
buffer_load_ushort v123,v168,s[96:99],s91 offen offset:136
buffer_load_ushort v131,v168,s[96:99],s91 offen offset:192
buffer_load_ushort v139,v168,s[96:99],s91 offen offset:200
s_add_u32 s91,s91,8192
buffer_load_ushort v116,v168,s[96:99],s91 offen offset:128
buffer_load_ushort v124,v168,s[96:99],s91 offen offset:136
buffer_load_ushort v132,v168,s[96:99],s91 offen offset:192
buffer_load_ushort v140,v168,s[96:99],s91 offen offset:200
s_add_u32 s91,s91,8192
buffer_load_ushort v117,v168,s[96:99],s91 offen offset:128
buffer_load_ushort v125,v168,s[96:99],s91 offen offset:136
buffer_load_ushort v133,v168,s[96:99],s91 offen offset:192
buffer_load_ushort v141,v168,s[96:99],s91 offen offset:200
s_add_u32 s91,s91,8192
buffer_load_ushort v118,v168,s[96:99],s91 offen offset:128
buffer_load_ushort v126,v168,s[96:99],s91 offen offset:136
buffer_load_ushort v134,v168,s[96:99],s91 offen offset:192
buffer_load_ushort v142,v168,s[96:99],s91 offen offset:200
s_add_u32 s91,s91,8192
buffer_load_ushort v119,v168,s[96:99],s91 offen offset:128
buffer_load_ushort v127,v168,s[96:99],s91 offen offset:136
buffer_load_ushort v135,v168,s[96:99],s91 offen offset:192
buffer_load_ushort v143,v168,s[96:99],s91 offen offset:200
s_add_u32 s91,s91,8192
s_waitcnt vmcnt(0)
v_lshl_or_b32 v112,v113,16,v112
v_lshl_or_b32 v113,v115,16,v114
v_lshl_or_b32 v114,v117,16,v116
v_lshl_or_b32 v115,v119,16,v118
v_lshl_or_b32 v120,v121,16,v120
v_lshl_or_b32 v121,v123,16,v122
v_lshl_or_b32 v122,v125,16,v124
v_lshl_or_b32 v123,v127,16,v126
v_lshl_or_b32 v128,v129,16,v128
v_lshl_or_b32 v129,v131,16,v130
v_lshl_or_b32 v130,v133,16,v132
v_lshl_or_b32 v131,v135,16,v134
v_lshl_or_b32 v136,v137,16,v136
v_lshl_or_b32 v137,v139,16,v138
v_lshl_or_b32 v138,v141,16,v140
v_lshl_or_b32 v139,v143,16,v142
s_nop 1
v_mfma_f32_16x16x32_bf16 v[80:83],v[112:115],v[160:163],v[80:83]
v_mfma_f32_16x16x32_bf16 v[84:87],v[120:123],v[160:163],v[84:87]
v_mfma_f32_16x16x32_bf16 v[88:91],v[128:131],v[160:163],v[88:91]
v_mfma_f32_16x16x32_bf16 v[92:95],v[136:139],v[160:163],v[92:95]
s_nop 7
s_add_u32 s90,s90,1
)A"
R"A(s_cmp_lg_u32 s90,20
s_cbranch_scc1 L_sq_%=
s_nop 7
v_mul_f32 v64,s61,v64
v_mul_f32 v65,s61,v65
v_mul_f32 v66,s61,v66
v_mul_f32 v67,s61,v67
v_mul_f32 v68,s61,v68
v_mul_f32 v69,s61,v69
v_mul_f32 v70,s61,v70
v_mul_f32 v71,s61,v71
v_mul_f32 v72,s61,v72
v_mul_f32 v73,s61,v73
v_mul_f32 v74,s61,v74
v_mul_f32 v75,s61,v75
v_mul_f32 v76,s61,v76
v_mul_f32 v77,s61,v77
v_mul_f32 v78,s61,v78
v_mul_f32 v79,s61,v79
v_mul_f32 v80,s61,v80
v_mul_f32 v81,s61,v81
v_mul_f32 v82,s61,v82
v_mul_f32 v83,s61,v83
v_mul_f32 v84,s61,v84
v_mul_f32 v85,s61,v85
v_mul_f32 v86,s61,v86
v_mul_f32 v87,s61,v87
v_mul_f32 v88,s61,v88
v_mul_f32 v89,s61,v89
v_mul_f32 v90,s61,v90
v_mul_f32 v91,s61,v91
v_mul_f32 v92,s61,v92
v_mul_f32 v93,s61,v93
v_mul_f32 v94,s61,v94
v_mul_f32 v95,s61,v95
v_cvt_pk_bf16_f32 v96,v64,v65
v_cvt_pk_bf16_f32 v97,v66,v67
v_cvt_pk_bf16_f32 v98,v68,v69
v_cvt_pk_bf16_f32 v99,v70,v71
v_cvt_pk_bf16_f32 v100,v72,v73
v_cvt_pk_bf16_f32 v101,v74,v75
v_cvt_pk_bf16_f32 v102,v76,v77
v_cvt_pk_bf16_f32 v103,v78,v79
v_cvt_pk_bf16_f32 v104,v80,v81
v_cvt_pk_bf16_f32 v105,v82,v83
v_cvt_pk_bf16_f32 v106,v84,v85
v_cvt_pk_bf16_f32 v107,v86,v87
v_cvt_pk_bf16_f32 v108,v88,v89
v_cvt_pk_bf16_f32 v109,v90,v91
v_cvt_pk_bf16_f32 v110,v92,v93
v_cvt_pk_bf16_f32 v111,v94,v95
v_mov_b32 v170,0xff800000
s_lshl_b32 s91,s88,18
s_mov_b32 s90,64
L_s1_%=:
buffer_load_dwordx4 v[112:115],v164,s[20:23],s91 offen
buffer_load_dwordx4 v[116:119],v164,s[20:23],s91 offen offset:64
buffer_load_dwordx4 v[120:123],v164,s[20:23],s91 offen offset:128
buffer_load_dwordx4 v[124:127],v164,s[20:23],s91 offen offset:192
s_waitcnt vmcnt(0)
v_mfma_f32_16x16x32_bf16 v[144:147],v[112:115],v[96:99],0
v_mfma_f32_16x16x32_bf16 v[144:147],v[116:119],v[100:103],v[144:147]
v_mfma_f32_16x16x32_bf16 v[144:147],v[120:123],v[104:107],v[144:147]
v_mfma_f32_16x16x32_bf16 v[144:147],v[124:127],v[108:111],v[144:147]
s_nop 7
v_max3_f32 v170,v170,v144,v145
v_max3_f32 v170,v170,v146,v147
s_add_u32 s91,s91,4096
s_sub_u32 s90,s90,1
s_cmp_lg_u32 s90,0
s_cbranch_scc1 L_s1_%=
v_mov_b32 v171,v170
s_nop 1
v_permlane32_swap_b32 v170,v171
v_max_f32 v170,v170,v171
v_mov_b32 v171,v170
s_nop 1
v_permlane16_swap_b32 v170,v171
v_max_f32 v170,v170,v171
v_sub_f32 v152,0,v170
v_mov_b32 v156,0
v_sub_f32 v153,0,v170
v_mov_b32 v157,0
v_sub_f32 v154,0,v170
v_mov_b32 v158,0
v_sub_f32 v155,0,v170
v_mov_b32 v159,0
v_mov_b32 v64,0
v_mov_b32 v65,0
v_mov_b32 v66,0
v_mov_b32 v67,0
v_mov_b32 v68,0
v_mov_b32 v69,0
v_mov_b32 v70,0
v_mov_b32 v71,0
v_mov_b32 v72,0
v_mov_b32 v73,0
v_mov_b32 v74,0
v_mov_b32 v75,0
v_mov_b32 v76,0
v_mov_b32 v77,0
v_mov_b32 v78,0
v_mov_b32 v79,0
v_mov_b32 v80,0
v_mov_b32 v81,0
v_mov_b32 v82,0
v_mov_b32 v83,0
v_mov_b32 v84,0
v_mov_b32 v85,0
v_mov_b32 v86,0
v_mov_b32 v87,0
v_mov_b32 v88,0
v_mov_b32 v89,0
v_mov_b32 v90,0
v_mov_b32 v91,0
v_mov_b32 v92,0
v_mov_b32 v93,0
v_mov_b32 v94,0
v_mov_b32 v95,0
s_lshl_b32 s91,s88,18
s_mov_b32 s90,32
v_add_u32 v172,4096,v164
L_s2_%=:
buffer_load_dwordx4 v[112:115],v164,s[20:23],s91 offen
buffer_load_dwordx4 v[116:119],v164,s[20:23],s91 offen offset:64
buffer_load_dwordx4 v[120:123],v164,s[20:23],s91 offen offset:128
buffer_load_dwordx4 v[124:127],v164,s[20:23],s91 offen offset:192
buffer_load_dwordx4 v[128:131],v172,s[20:23],s91 offen
buffer_load_dwordx4 v[132:135],v172,s[20:23],s91 offen offset:64
buffer_load_dwordx4 v[136:139],v172,s[20:23],s91 offen offset:128
buffer_load_dwordx4 v[140:143],v172,s[20:23],s91 offen offset:192
s_waitcnt vmcnt(0)
v_mfma_f32_16x16x32_bf16 v[144:147],v[112:115],v[96:99],v[152:155]
v_mfma_f32_16x16x32_bf16 v[144:147],v[116:119],v[100:103],v[144:147]
v_mfma_f32_16x16x32_bf16 v[144:147],v[120:123],v[104:107],v[144:147]
v_mfma_f32_16x16x32_bf16 v[144:147],v[124:127],v[108:111],v[144:147]
v_mfma_f32_16x16x32_bf16 v[148:151],v[128:131],v[96:99],v[152:155]
v_mfma_f32_16x16x32_bf16 v[148:151],v[132:135],v[100:103],v[148:151]
v_mfma_f32_16x16x32_bf16 v[148:151],v[136:139],v[104:107],v[148:151]
v_mfma_f32_16x16x32_bf16 v[148:151],v[140:143],v[108:111],v[148:151]
s_nop 7
v_exp_f32 v144,v144
v_exp_f32 v145,v145
v_exp_f32 v146,v146
v_exp_f32 v147,v147
v_exp_f32 v148,v148
v_exp_f32 v149,v149
v_exp_f32 v150,v150
v_exp_f32 v151,v151
s_nop 0
v_cvt_pk_bf16_f32 v144,v144,v145
v_cvt_pk_bf16_f32 v145,v146,v147
v_cvt_pk_bf16_f32 v146,v148,v149
v_cvt_pk_bf16_f32 v147,v150,v151
buffer_load_ushort v112,v165,s[24:27],s91 offen
buffer_load_ushort v113,v165,s[24:27],s91 offen offset:256
buffer_load_ushort v114,v165,s[24:27],s91 offen offset:512
buffer_load_ushort v115,v165,s[24:27],s91 offen offset:768
buffer_load_ushort v116,v166,s[24:27],s91 offen
buffer_load_ushort v117,v166,s[24:27],s91 offen offset:256
buffer_load_ushort v118,v166,s[24:27],s91 offen offset:512
buffer_load_ushort v119,v166,s[24:27],s91 offen offset:768
buffer_load_ushort v120,v165,s[24:27],s91 offen offset:32
buffer_load_ushort v121,v165,s[24:27],s91 offen offset:288
buffer_load_ushort v122,v165,s[24:27],s91 offen offset:544
buffer_load_ushort v123,v165,s[24:27],s91 offen offset:800
buffer_load_ushort v124,v166,s[24:27],s91 offen offset:32
buffer_load_ushort v125,v166,s[24:27],s91 offen offset:288
buffer_load_ushort v126,v166,s[24:27],s91 offen offset:544
buffer_load_ushort v127,v166,s[24:27],s91 offen offset:800
buffer_load_ushort v128,v165,s[24:27],s91 offen offset:64
buffer_load_ushort v129,v165,s[24:27],s91 offen offset:320
buffer_load_ushort v130,v165,s[24:27],s91 offen offset:576
buffer_load_ushort v131,v165,s[24:27],s91 offen offset:832
buffer_load_ushort v132,v166,s[24:27],s91 offen offset:64
buffer_load_ushort v133,v166,s[24:27],s91 offen offset:320
buffer_load_ushort v134,v166,s[24:27],s91 offen offset:576
buffer_load_ushort v135,v166,s[24:27],s91 offen offset:832
buffer_load_ushort v136,v165,s[24:27],s91 offen offset:96
buffer_load_ushort v137,v165,s[24:27],s91 offen offset:352
buffer_load_ushort v138,v165,s[24:27],s91 offen offset:608
buffer_load_ushort v139,v165,s[24:27],s91 offen offset:864
buffer_load_ushort v140,v166,s[24:27],s91 offen offset:96
buffer_load_ushort v141,v166,s[24:27],s91 offen offset:352
buffer_load_ushort v142,v166,s[24:27],s91 offen offset:608
buffer_load_ushort v143,v166,s[24:27],s91 offen offset:864
s_waitcnt vmcnt(0)
v_lshl_or_b32 v112,v113,16,v112
v_lshl_or_b32 v113,v115,16,v114
v_lshl_or_b32 v114,v117,16,v116
v_lshl_or_b32 v115,v119,16,v118
v_lshl_or_b32 v120,v121,16,v120
v_lshl_or_b32 v121,v123,16,v122
v_lshl_or_b32 v122,v125,16,v124
v_lshl_or_b32 v123,v127,16,v126
v_lshl_or_b32 v128,v129,16,v128
v_lshl_or_b32 v129,v131,16,v130
v_lshl_or_b32 v130,v133,16,v132
v_lshl_or_b32 v131,v135,16,v134
v_lshl_or_b32 v136,v137,16,v136
v_lshl_or_b32 v137,v139,16,v138
v_lshl_or_b32 v138,v141,16,v140
v_lshl_or_b32 v139,v143,16,v142
s_nop 1
v_mfma_f32_16x16x32_bf16 v[64:67],v[112:115],v[144:147],v[64:67]
v_mfma_f32_16x16x32_bf16 v[68:71],v[120:123],v[144:147],v[68:71]
v_mfma_f32_16x16x32_bf16 v[72:75],v[128:131],v[144:147],v[72:75]
v_mfma_f32_16x16x32_bf16 v[76:79],v[136:139],v[144:147],v[76:79]
s_nop 7
buffer_load_ushort v112,v165,s[24:27],s91 offen offset:128
buffer_load_ushort v113,v165,s[24:27],s91 offen offset:384
buffer_load_ushort v114,v165,s[24:27],s91 offen offset:640
buffer_load_ushort v115,v165,s[24:27],s91 offen offset:896
buffer_load_ushort v116,v166,s[24:27],s91 offen offset:128
buffer_load_ushort v117,v166,s[24:27],s91 offen offset:384
buffer_load_ushort v118,v166,s[24:27],s91 offen offset:640
buffer_load_ushort v119,v166,s[24:27],s91 offen offset:896
buffer_load_ushort v120,v165,s[24:27],s91 offen offset:160
buffer_load_ushort v121,v165,s[24:27],s91 offen offset:416
buffer_load_ushort v122,v165,s[24:27],s91 offen offset:672
buffer_load_ushort v123,v165,s[24:27],s91 offen offset:928
buffer_load_ushort v124,v166,s[24:27],s91 offen offset:160
buffer_load_ushort v125,v166,s[24:27],s91 offen offset:416
buffer_load_ushort v126,v166,s[24:27],s91 offen offset:672
buffer_load_ushort v127,v166,s[24:27],s91 offen offset:928
buffer_load_ushort v128,v165,s[24:27],s91 offen offset:192
buffer_load_ushort v129,v165,s[24:27],s91 offen offset:448
buffer_load_ushort v130,v165,s[24:27],s91 offen offset:704
buffer_load_ushort v131,v165,s[24:27],s91 offen offset:960
buffer_load_ushort v132,v166,s[24:27],s91 offen offset:192
buffer_load_ushort v133,v166,s[24:27],s91 offen offset:448
buffer_load_ushort v134,v166,s[24:27],s91 offen offset:704
buffer_load_ushort v135,v166,s[24:27],s91 offen offset:960
buffer_load_ushort v136,v165,s[24:27],s91 offen offset:224
buffer_load_ushort v137,v165,s[24:27],s91 offen offset:480
buffer_load_ushort v138,v165,s[24:27],s91 offen offset:736
buffer_load_ushort v139,v165,s[24:27],s91 offen offset:992
buffer_load_ushort v140,v166,s[24:27],s91 offen offset:224
buffer_load_ushort v141,v166,s[24:27],s91 offen offset:480
buffer_load_ushort v142,v166,s[24:27],s91 offen offset:736
buffer_load_ushort v143,v166,s[24:27],s91 offen offset:992
s_waitcnt vmcnt(0)
v_lshl_or_b32 v112,v113,16,v112
v_lshl_or_b32 v113,v115,16,v114
v_lshl_or_b32 v114,v117,16,v116
v_lshl_or_b32 v115,v119,16,v118
v_lshl_or_b32 v120,v121,16,v120
v_lshl_or_b32 v121,v123,16,v122
v_lshl_or_b32 v122,v125,16,v124
v_lshl_or_b32 v123,v127,16,v126
v_lshl_or_b32 v128,v129,16,v128
v_lshl_or_b32 v129,v131,16,v130
v_lshl_or_b32 v130,v133,16,v132
v_lshl_or_b32 v131,v135,16,v134
v_lshl_or_b32 v136,v137,16,v136
v_lshl_or_b32 v137,v139,16,v138
v_lshl_or_b32 v138,v141,16,v140
v_lshl_or_b32 v139,v143,16,v142
s_nop 1
v_mfma_f32_16x16x32_bf16 v[80:83],v[112:115],v[144:147],v[80:83]
v_mfma_f32_16x16x32_bf16 v[84:87],v[120:123],v[144:147],v[84:87]
v_mfma_f32_16x16x32_bf16 v[88:91],v[128:131],v[144:147],v[88:91]
v_mfma_f32_16x16x32_bf16 v[92:95],v[136:139],v[144:147],v[92:95]
s_nop 7
v_mfma_f32_16x16x32_bf16 v[156:159],v[28:31],v[144:147],v[156:159]
s_nop 7
s_add_u32 s91,s91,8192
s_sub_u32 s90,s90,1
s_cmp_lg_u32 s90,0
s_cbranch_scc1 L_s2_%=
v_rcp_f32 v170,v156
s_nop 0
v_mul_f32 v64,v170,v64
v_mul_f32 v65,v170,v65
v_mul_f32 v66,v170,v66
v_mul_f32 v67,v170,v67
v_mul_f32 v68,v170,v68
v_mul_f32 v69,v170,v69
v_mul_f32 v70,v170,v70
v_mul_f32 v71,v170,v71
v_mul_f32 v72,v170,v72
v_mul_f32 v73,v170,v73
v_mul_f32 v74,v170,v74
v_mul_f32 v75,v170,v75
v_mul_f32 v76,v170,v76
v_mul_f32 v77,v170,v77
v_mul_f32 v78,v170,v78
v_mul_f32 v79,v170,v79
v_mul_f32 v80,v170,v80
v_mul_f32 v81,v170,v81
v_mul_f32 v82,v170,v82
v_mul_f32 v83,v170,v83
v_mul_f32 v84,v170,v84
v_mul_f32 v85,v170,v85
v_mul_f32 v86,v170,v86
v_mul_f32 v87,v170,v87
v_mul_f32 v88,v170,v88
v_mul_f32 v89,v170,v89
v_mul_f32 v90,v170,v90
v_mul_f32 v91,v170,v91
v_mul_f32 v92,v170,v92
v_mul_f32 v93,v170,v93
v_mul_f32 v94,v170,v94
v_mul_f32 v95,v170,v95
v_cvt_pk_bf16_f32 v96,v64,v65
v_cvt_pk_bf16_f32 v97,v66,v67
v_cvt_pk_bf16_f32 v98,v68,v69
v_cvt_pk_bf16_f32 v99,v70,v71
v_cvt_pk_bf16_f32 v100,v72,v73
v_cvt_pk_bf16_f32 v101,v74,v75
v_cvt_pk_bf16_f32 v102,v76,v77
v_cvt_pk_bf16_f32 v103,v78,v79
v_cvt_pk_bf16_f32 v104,v80,v81
v_cvt_pk_bf16_f32 v105,v82,v83
v_cvt_pk_bf16_f32 v106,v84,v85
v_cvt_pk_bf16_f32 v107,v86,v87
v_cvt_pk_bf16_f32 v108,v88,v89
v_cvt_pk_bf16_f32 v109,v90,v91
v_cvt_pk_bf16_f32 v110,v92,v93
v_cvt_pk_bf16_f32 v111,v94,v95
buffer_store_dwordx4 v[96:99],v169,s[36:39],s55 offen sc1
buffer_store_dwordx4 v[100:103],v169,s[36:39],s55 offen offset:1024 sc1
buffer_store_dwordx4 v[104:107],v169,s[36:39],s55 offen offset:2048 sc1
buffer_store_dwordx4 v[108:111],v169,s[36:39],s55 offen offset:3072 sc1
s_waitcnt vmcnt(0)
s_add_u32 s89,s89,1
s_cmp_lg_u32 s89,4
s_cbranch_scc1 L_sjb_%=
s_mov_b32 s98,s88
v_readlane_b32 s88,v173,0
v_readlane_b32 s89,v173,1
v_readlane_b32 s90,v173,2
v_readlane_b32 s91,v173,3
v_readlane_b32 s92,v173,4
v_readlane_b32 s93,v173,5
v_readlane_b32 s94,v173,6
v_readlane_b32 s95,v173,7
s_nop 4
s_mov_b32 s96,0
buffer_load_dwordx4 v[64:67],v6,s[56:59],s96 offen sc1
buffer_load_dwordx4 v[68:71],v6,s[56:59],s96 offen offset:1024 sc1
buffer_load_dwordx4 v[72:75],v6,s[56:59],s96 offen offset:2048 sc1
buffer_load_dwordx4 v[76:79],v6,s[56:59],s96 offen offset:3072 sc1
s_mov_b32 s96,4096
buffer_load_dwordx4 v[80:83],v6,s[56:59],s96 offen sc1
)A"
R"A(buffer_load_dwordx4 v[84:87],v6,s[56:59],s96 offen offset:1024 sc1
buffer_load_dwordx4 v[88:91],v6,s[56:59],s96 offen offset:2048 sc1
buffer_load_dwordx4 v[92:95],v6,s[56:59],s96 offen offset:3072 sc1
s_mov_b32 s96,8192
buffer_load_dwordx4 v[96:99],v6,s[56:59],s96 offen sc1
buffer_load_dwordx4 v[100:103],v6,s[56:59],s96 offen offset:1024 sc1
buffer_load_dwordx4 v[104:107],v6,s[56:59],s96 offen offset:2048 sc1
buffer_load_dwordx4 v[108:111],v6,s[56:59],s96 offen offset:3072 sc1
s_mov_b32 s96,12288
buffer_load_dwordx4 v[112:115],v6,s[56:59],s96 offen sc1
buffer_load_dwordx4 v[116:119],v6,s[56:59],s96 offen offset:1024 sc1
buffer_load_dwordx4 v[120:123],v6,s[56:59],s96 offen offset:2048 sc1
buffer_load_dwordx4 v[124:127],v6,s[56:59],s96 offen offset:3072 sc1
s_mov_b32 s96,16384
buffer_load_dwordx4 v[128:131],v6,s[56:59],s96 offen sc1
buffer_load_dwordx4 v[132:135],v6,s[56:59],s96 offen offset:1024 sc1
buffer_load_dwordx4 v[136:139],v6,s[56:59],s96 offen offset:2048 sc1
buffer_load_dwordx4 v[140:143],v6,s[56:59],s96 offen offset:3072 sc1
s_mov_b32 s96,20480
buffer_load_dwordx4 v[144:147],v6,s[56:59],s96 offen sc1
buffer_load_dwordx4 v[148:151],v6,s[56:59],s96 offen offset:1024 sc1
buffer_load_dwordx4 v[152:155],v6,s[56:59],s96 offen offset:2048 sc1
buffer_load_dwordx4 v[156:159],v6,s[56:59],s96 offen offset:3072 sc1
s_mov_b32 s96,24576
buffer_load_dwordx4 v[160:163],v6,s[56:59],s96 offen sc1
buffer_load_dwordx4 v[164:167],v6,s[56:59],s96 offen offset:1024 sc1
buffer_load_dwordx4 v[168:171],v6,s[56:59],s96 offen offset:2048 sc1
buffer_load_dwordx4 v[172:175],v6,s[56:59],s96 offen offset:3072 sc1
s_mov_b32 s96,28672
buffer_load_dwordx4 v[176:179],v6,s[56:59],s96 offen sc1
buffer_load_dwordx4 v[180:183],v6,s[56:59],s96 offen offset:1024 sc1
buffer_load_dwordx4 v[184:187],v6,s[56:59],s96 offen offset:2048 sc1
buffer_load_dwordx4 v[188:191],v6,s[56:59],s96 offen offset:3072 sc1
s_waitcnt vmcnt(0)
s_cmp_lg_u32 s98,0
s_cbranch_scc1 L_sr1_%=
buffer_load_dwordx4 v[64:67],v6,s[36:39],s84 offen sc1
buffer_load_dwordx4 v[68:71],v6,s[36:39],s84 offen offset:1024 sc1
buffer_load_dwordx4 v[72:75],v6,s[36:39],s84 offen offset:2048 sc1
buffer_load_dwordx4 v[76:79],v6,s[36:39],s84 offen offset:3072 sc1
buffer_load_dwordx4 v[80:83],v6,s[36:39],s85 offen sc1
buffer_load_dwordx4 v[84:87],v6,s[36:39],s85 offen offset:1024 sc1
buffer_load_dwordx4 v[88:91],v6,s[36:39],s85 offen offset:2048 sc1
buffer_load_dwordx4 v[92:95],v6,s[36:39],s85 offen offset:3072 sc1
buffer_load_dwordx4 v[96:99],v6,s[36:39],s86 offen sc1
buffer_load_dwordx4 v[100:103],v6,s[36:39],s86 offen offset:1024 sc1
buffer_load_dwordx4 v[104:107],v6,s[36:39],s86 offen offset:2048 sc1
buffer_load_dwordx4 v[108:111],v6,s[36:39],s86 offen offset:3072 sc1
buffer_load_dwordx4 v[112:115],v6,s[36:39],s87 offen sc1
buffer_load_dwordx4 v[116:119],v6,s[36:39],s87 offen offset:1024 sc1
buffer_load_dwordx4 v[120:123],v6,s[36:39],s87 offen offset:2048 sc1
buffer_load_dwordx4 v[124:127],v6,s[36:39],s87 offen offset:3072 sc1
s_waitcnt vmcnt(0)
s_nop 7
s_branch L_sret0_%=
L_sr1_%=:
buffer_load_dwordx4 v[136:139],v6,s[36:39],s84 offen sc1
buffer_load_dwordx4 v[140:143],v6,s[36:39],s84 offen offset:1024 sc1
buffer_load_dwordx4 v[152:155],v6,s[36:39],s84 offen offset:2048 sc1
buffer_load_dwordx4 v[156:159],v6,s[36:39],s84 offen offset:3072 sc1
buffer_load_dwordx4 v[168:171],v6,s[36:39],s85 offen sc1
buffer_load_dwordx4 v[172:175],v6,s[36:39],s85 offen offset:1024 sc1
buffer_load_dwordx4 v[184:187],v6,s[36:39],s85 offen offset:2048 sc1
buffer_load_dwordx4 v[188:191],v6,s[36:39],s85 offen offset:3072 sc1
buffer_load_dwordx4 v[200:203],v6,s[36:39],s86 offen sc1
buffer_load_dwordx4 v[204:207],v6,s[36:39],s86 offen offset:1024 sc1
buffer_load_dwordx4 v[216:219],v6,s[36:39],s86 offen offset:2048 sc1
buffer_load_dwordx4 v[220:223],v6,s[36:39],s86 offen offset:3072 sc1
buffer_load_dwordx4 v[232:235],v6,s[36:39],s87 offen sc1
buffer_load_dwordx4 v[236:239],v6,s[36:39],s87 offen offset:1024 sc1
buffer_load_dwordx4 v[248:251],v6,s[36:39],s87 offen offset:2048 sc1
buffer_load_dwordx4 v[252:255],v6,s[36:39],s87 offen offset:3072 sc1
s_waitcnt vmcnt(0)
s_nop 7
s_branch L_sret1_%=
L_end_%=:
)A"
    :: "s"(karg), "s"(blk), "s"(wid)
    : "v0", "v1", "v2", "v3", "v4", "v5", "v6", "v7", "v8", "v9", "v10", "v11", "v12", "v13", "v14", "v15", "v16", "v17", "v18", "v19", "v20", "v21", "v22", "v23", "v24", "v25", "v26", "v27", "v28", "v29", "v30", "v31", "v32", "v33", "v34", "v35", "v36", "v37", "v38", "v39", "v40", "v41", "v42", "v43", "v44", "v45", "v46", "v47", "v48", "v49", "v50", "v51", "v52", "v53", "v54", "v55", "v56", "v57", "v58", "v59", "v60", "v61", "v62", "v63", "v64", "v65", "v66", "v67", "v68", "v69", "v70", "v71", "v72", "v73", "v74", "v75", "v76", "v77", "v78", "v79", "v80", "v81", "v82", "v83", "v84", "v85", "v86", "v87", "v88", "v89", "v90", "v91", "v92", "v93", "v94", "v95", "v96", "v97", "v98", "v99", "v100", "v101", "v102", "v103", "v104", "v105", "v106", "v107", "v108", "v109", "v110", "v111", "v112", "v113", "v114", "v115", "v116", "v117", "v118", "v119", "v120", "v121", "v122", "v123", "v124", "v125", "v126", "v127", "v128", "v129", "v130", "v131", "v132", "v133", "v134", "v135", "v136", "v137", "v138", "v139", "v140", "v141", "v142", "v143", "v144", "v145", "v146", "v147", "v148", "v149", "v150", "v151", "v152", "v153", "v154", "v155", "v156", "v157", "v158", "v159", "v160", "v161", "v162", "v163", "v164", "v165", "v166", "v167", "v168", "v169", "v170", "v171", "v172", "v173", "v174", "v175", "v176", "v177", "v178", "v179", "v180", "v181", "v182", "v183", "v184", "v185", "v186", "v187", "v188", "v189", "v190", "v191", "v192", "v193", "v194", "v195", "v196", "v197", "v198", "v199", "v200", "v201", "v202", "v203", "v204", "v205", "v206", "v207", "v208", "v209", "v210", "v211", "v212", "v213", "v214", "v215", "v216", "v217", "v218", "v219", "v220", "v221", "v222", "v223", "v224", "v225", "v226", "v227", "v228", "v229", "v230", "v231", "v232", "v233", "v234", "v235", "v236", "v237", "v238", "v239", "v240", "v241", "v242", "v243", "v244", "v245", "v246", "v247", "v248", "v249", "v250", "v251", "v252", "v253", "v254", "v255", "a0", "a1", "a2", "a3", "a4", "a5", "a6", "a7", "a8", "a9", "a10", "a11", "a12", "a13", "a14", "a15", "a16", "a17", "a18", "a19", "a20", "a21", "a22", "a23", "a24", "a25", "a26", "a27", "a28", "a29", "a30", "a31", "a32", "a33", "a34", "a35", "a36", "a37", "a38", "a39", "a40", "a41", "a42", "a43", "a44", "a45", "a46", "a47", "a48", "a49", "a50", "a51", "a52", "a53", "a54", "a55", "a56", "a57", "a58", "a59", "a60", "a61", "a62", "a63", "a64", "a65", "a66", "a67", "a68", "a69", "a70", "a71", "a72", "a73", "a74", "a75", "a76", "a77", "a78", "a79", "a80", "a81", "a82", "a83", "a84", "a85", "a86", "a87", "a88", "a89", "a90", "a91", "a92", "a93", "a94", "a95", "a96", "a97", "a98", "a99", "a100", "a101", "a102", "a103", "a104", "a105", "a106", "a107", "a108", "a109", "a110", "a111", "a112", "a113", "a114", "a115", "a116", "a117", "a118", "a119", "a120", "a121", "a122", "a123", "a124", "a125", "a126", "a127", "a128", "a129", "a130", "a131", "a132", "a133", "a134", "a135", "a136", "a137", "a138", "a139", "a140", "a141", "a142", "a143", "a144", "a145", "a146", "a147", "a148", "a149", "a150", "a151", "a152", "a153", "a154", "a155", "a156", "a157", "a158", "a159", "a160", "a161", "a162", "a163", "a164", "a165", "a166", "a167", "a168", "a169", "a170", "a171", "a172", "a173", "a174", "a175", "a176", "a177", "a178", "a179", "a180", "a181", "a182", "a183", "a184", "a185", "a186", "a187", "a188", "a189", "a190", "a191", "a192", "a193", "a194", "a195", "a196", "a197", "a198", "a199", "a200", "a201", "a202", "a203", "a204", "a205", "a206", "a207", "a208", "a209", "a210", "a211", "a212", "a213", "a214", "a215", "a216", "a217", "a218", "a219", "a220", "a221", "a222", "a223", "a224", "a225", "a226", "a227", "a228", "a229", "a230", "a231", "a232", "a233", "a234", "a235", "a236", "a237", "a238", "a239", "a240", "a241", "a242", "a243", "a244", "a245", "a246", "a247", "a248", "a249", "a250", "a251", "a252", "a253", "a254", "a255", "s4", "s5", "s6", "s7", "s8", "s9", "s10", "s11", "s12", "s13", "s14", "s15", "s16", "s17", "s18", "s19", "s20", "s21", "s22", "s23", "s24", "s25", "s26", "s27", "s28", "s29", "s30", "s31", "s32", "s33", "s34", "s35", "s36", "s37", "s38", "s39", "s40", "s41", "s42", "s43", "s44", "s45", "s46", "s47", "s48", "s49", "s50", "s51", "s52", "s53", "s54", "s55", "s56", "s57", "s58", "s59", "s60", "s61", "s62", "s63", "s64", "s65", "s66", "s67", "s68", "s69", "s70", "s71", "s72", "s73", "s74", "s75", "s76", "s77", "s78", "s79", "s80", "s81", "s82", "s83", "s84", "s85", "s86", "s87", "s88", "s89", "s90", "s91", "s92", "s93", "s94", "s95", "s96", "s97", "s98", "s99", "s100", "s101", "vcc", "scc", "memory");
#endif
}

extern "C" void kernel_launch(void* const* d_in, const int* in_sizes, int n_in,
                              void* d_out, int out_size, void* d_ws, size_t ws_size,
                              hipStream_t stream) {
  (void)hipFuncSetAttribute((const void*)qattn_kernel, hipFuncAttributeMaxDynamicSharedMemorySize, LDS_ATT);
  qattn_kernel<<<256, 256, LDS_ATT, stream>>>(d_in[0], d_in[1], d_in[2], d_in[3], d_ws, d_in[4], d_out);
}
```

```cpp
#include <hip/hip_runtime.h>
#include <hip/hip_bf16.h>

constexpr int LDS_ATT = 163840;

__global__ __launch_bounds__(256, 1) void qattn_kernel(const void* __restrict__ x, const void* __restrict__ kc, const void* __restrict__ vc,
                                                       const void* __restrict__ wq, void* __restrict__ ws, const void* __restrict__ wo,
                                                       void* __restrict__ out) {
#if defined(__HIP_DEVICE_COMPILE__)
  const auto karg = __builtin_amdgcn_kernarg_segment_ptr();
  const unsigned blk = blockIdx.x;
  const unsigned wid = __builtin_amdgcn_readfirstlane(threadIdx.x >> 6);
  asm volatile(
R"A(s_memtime s[72:73]
s_memrealtime s[74:75]
s_load_dwordx8 s[4:11],%0,0x0
s_load_dwordx4 s[12:15],%0,0x20
s_load_dwordx2 s[16:17],%0,0x30
s_mov_b32 s40,%2
s_mov_b32 s43,%1
s_lshr_b32 s45,s43,3
v_mbcnt_lo_u32_b32 v12,-1,0
v_mbcnt_hi_u32_b32 v12,-1,v12
s_getpc_b64 s[88:89]
L_pc_%=:
s_and_b32 s88,s88,0xffffff80
s_and_b32 s89,s89,0xffff
s_mov_b32 s90,L_fast_end_%=-L_pc_%=
s_mov_b32 s91,0x20000
s_lshl_b32 s44,s45,2
s_add_u32 s44,s44,s40
s_mul_i32 s44,s44,896
v_lshlrev_b32 v67,7,v12
s_mov_b64 exec,127
buffer_load_dword v19,v67,s[88:91],s44 offen
s_mov_b64 exec,-1
s_and_b32 s70,s43,3
s_and_b32 s71,s45,3
s_lshl_b32 s42,s70,2
s_add_u32 s42,s42,s71
s_and_b32 s41,s43,4
s_lshl_b32 s41,s41,1
s_lshr_b32 s46,s45,2
s_add_u32 s41,s41,s46
s_lshl_b32 s56,s42,9
s_mov_b32 s57,0
s_lshl_b32 s59,s40,13
s_add_u32 s58,s59,65536
v_and_b32 v64,3,v12
v_bfe_u32 v65,v12,2,2
v_lshrrev_b32 v66,4,v12
s_lshl_b32 s44,s40,17
v_lshlrev_b32 v75,13,v65
v_lshl_add_u32 v75,v66,6,v75
v_lshl_add_u32 v75,v64,4,v75
v_add_u32 v75,s44,v75
v_add_u32 v56,0,v75
v_add_u32 v57,256,v75
v_add_u32 v58,32768,v75
v_add_u32 v59,33024,v75
v_add_u32 v60,65536,v75
v_add_u32 v61,65792,v75
v_add_u32 v62,98304,v75
v_add_u32 v63,98560,v75
s_waitcnt lgkmcnt(0)
s_mov_b64 s[32:33],s[10:11]
s_and_b32 s33,s33,0xffff
s_mov_b32 s34,5242880
s_mov_b32 s35,0x20000
s_add_u32 m0,s58,1024
s_nop 0
buffer_load_dwordx4 v57,s[32:35],s56 offen lds
s_add_u32 m0,s58,3072
s_nop 0
buffer_load_dwordx4 v59,s[32:35],s56 offen lds
s_add_u32 m0,s58,5120
s_nop 0
buffer_load_dwordx4 v61,s[32:35],s56 offen lds
s_add_u32 m0,s58,7168
s_nop 0
buffer_load_dwordx4 v63,s[32:35],s56 offen lds
v_lshrrev_b32 v75,3,v12
s_lshl_b32 s44,s40,6
v_add_u32 v75,s44,v75
v_mul_u32_u24 v75,1280,v75
v_and_b32 v76,7,v12
s_add_u32 s84,s70,1
s_and_b32 s84,s84,3
s_mul_i32 s84,s84,20480
s_add_u32 s84,s84,4096
s_add_u32 s85,s70,2
s_and_b32 s85,s85,3
s_mul_i32 s85,s85,20480
s_add_u32 s85,s85,2048
s_add_u32 s86,s70,3
s_and_b32 s86,s86,3
s_mul_i32 s86,s86,20480
s_add_u32 s86,s86,4096
s_add_u32 s87,s70,4
s_and_b32 s87,s87,3
s_mul_i32 s87,s87,20480
s_add_u32 s87,s87,2048
v_or_b32 v77,0,v66
v_xor_b32 v77,v77,v76
v_lshl_add_u32 v77,v77,4,v75
v_add_u32 v48,s84,v77
v_add_u32 v50,s85,v77
v_add_u32 v52,s86,v77
v_add_u32 v54,s87,v77
v_or_b32 v77,4,v66
v_xor_b32 v77,v77,v76
v_lshl_add_u32 v77,v77,4,v75
v_add_u32 v77,9216,v77
v_add_u32 v49,s84,v77
v_add_u32 v51,s85,v77
v_add_u32 v53,s86,v77
v_add_u32 v55,s87,v77
s_mul_i32 s44,s41,307200
s_mov_b64 s[28:29],s[4:5]
s_add_u32 s28,s28,s44
s_addc_u32 s29,s29,0
s_sub_u32 s28,s28,4096
s_subb_u32 s29,s29,0
s_and_b32 s29,s29,0xffff
s_mov_b32 s30,311296
s_mov_b32 s31,0x20000
s_add_u32 m0,s59,0
s_nop 0
buffer_load_dwordx4 v48,s[28:31],s57 offen lds
buffer_load_dwordx4 v49,s[28:31],s57 offen offset:1024 lds
buffer_load_dwordx4 v50,s[28:31],s57 offen offset:2048 lds
buffer_load_dwordx4 v51,s[28:31],s57 offen offset:3072 lds
s_add_u32 m0,s59,4096
s_nop 0
buffer_load_dwordx4 v52,s[28:31],s57 offen lds
buffer_load_dwordx4 v53,s[28:31],s57 offen offset:1024 lds
buffer_load_dwordx4 v54,s[28:31],s57 offen offset:2048 lds
buffer_load_dwordx4 v55,s[28:31],s57 offen offset:3072 lds
s_add_u32 s57,s57,128
s_xor_b32 s59,s59,131072
s_add_u32 m0,s58,0
s_nop 0
buffer_load_dwordx4 v56,s[32:35],s56 offen lds
s_add_u32 m0,s58,2048
s_nop 0
buffer_load_dwordx4 v58,s[32:35],s56 offen lds
s_add_u32 m0,s58,4096
s_nop 0
buffer_load_dwordx4 v60,s[32:35],s56 offen lds
s_add_u32 m0,s58,6144
s_nop 0
buffer_load_dwordx4 v62,s[32:35],s56 offen lds
s_add_u32 s56,s56,524288
s_add_u32 s58,s58,32768
s_cmp_ge_u32 s58,131072
s_cselect_b32 s62,98304,0
s_sub_u32 s58,s58,s62
s_add_u32 s12,s12,4194303
s_addc_u32 s13,s13,0
s_and_b32 s12,s12,0xffc00000
s_lshl_b32 s55,s40,15
s_lshl_b32 s84,s41,2
s_add_u32 s84,s84,s70
s_lshl_b32 s84,s84,7
s_lshl_b32 s85,s71,2
s_add_u32 s84,s84,s85
s_add_u32 s84,s84,s40
s_add_u32 s84,s84,112
s_lshl_b32 s84,s84,14
s_add_u32 s84,s84,s12
s_addc_u32 s85,s13,0
s_mov_b32 s87,0x13572468
s_mov_b32 s79,0
s_load_dword s78,s[84:85],0x1000 glc
s_xor_b32 s76,s84,2097152
s_mov_b32 s77,s85
s_load_dword s78,s[76:77],0x0 glc
s_load_dword s78,s[76:77],0x2000 glc
s_load_dword s78,s[76:77],0x100 glc
s_load_dword s78,s[76:77],0x2100 glc
s_load_dword s78,s[76:77],0x200 glc
s_load_dword s78,s[76:77],0x2200 glc
s_add_u32 m0,s58,0
s_nop 0
buffer_load_dwordx4 v56,s[32:35],s56 offen lds
s_add_u32 m0,s58,1024
s_nop 0
buffer_load_dwordx4 v57,s[32:35],s56 offen lds
s_add_u32 m0,s58,2048
s_nop 0
buffer_load_dwordx4 v58,s[32:35],s56 offen lds
s_add_u32 m0,s58,3072
s_nop 0
buffer_load_dwordx4 v59,s[32:35],s56 offen lds
s_add_u32 m0,s58,4096
s_nop 0
buffer_load_dwordx4 v60,s[32:35],s56 offen lds
s_add_u32 m0,s58,5120
s_nop 0
buffer_load_dwordx4 v61,s[32:35],s56 offen lds
s_add_u32 m0,s58,6144
s_nop 0
buffer_load_dwordx4 v62,s[32:35],s56 offen lds
s_add_u32 m0,s58,7168
s_nop 0
buffer_load_dwordx4 v63,s[32:35],s56 offen lds
s_add_u32 s56,s56,524288
s_add_u32 s58,s58,32768
s_cmp_ge_u32 s58,131072
s_cselect_b32 s62,98304,0
s_sub_u32 s58,s58,s62
s_mov_b32 s60,65536
v_lshlrev_b32 v6,4,v12
v_lshlrev_b32 v72,12,v66
v_lshl_add_u32 v72,v65,6,v72
v_lshl_add_u32 v23,v64,4,v72
v_add_u32 v22,65536,v23
v_and_b32 v74,15,v12
v_lshrrev_b32 v76,1,v74
v_lshlrev_b32 v75,7,v74
s_lshl_b32 s44,s40,13
v_add_u32 v75,s44,v75
v_or_b32 v77,0,v66
v_xor_b32 v77,v77,v76
v_lshl_add_u32 v20,v77,4,v75
v_or_b32 v77,4,v66
v_xor_b32 v77,v77,v76
v_lshl_add_u32 v21,v77,4,v75
s_waitcnt vmcnt(12)
s_nop 0
s_memtime s[64:65]
s_memrealtime s[66:67]
s_bfe_u32 s68,%1,327683
s_waitcnt lgkmcnt(0)
s_sub_u32 s64,s64,s72
s_sub_u32 s66,s66,s74
s_mul_i32 s64,s64,10
s_mul_i32 s66,s66,215
s_cmp_lt_u32 s64,s66
s_cselect_b32 s95,64,0
s_mov_b32 %2,s95
s_cbranch_scc1 L_se1_%=
L_sg1_%=:
s_cmp_eq_u32 s68,0
s_cbranch_scc1 L_se1_%=
s_sleep 4
s_sub_u32 s68,s68,1
s_branch L_sg1_%=
L_se1_%=:
s_barrier
ds_read_b128 a[192:195],v20
ds_read_b64_tr_b16 a[128:129],v22 offset:1024
ds_read_b64_tr_b16 a[130:131],v22 offset:3072
ds_read_b128 a[196:199],v21
ds_read_b64_tr_b16 a[132:133],v22 offset:17408
ds_read_b64_tr_b16 a[134:135],v22 offset:19456
ds_read_b128 a[204:207],v21 offset:2048
ds_read_b128 a[200:203],v20 offset:2048
ds_read_b128 a[208:211],v20 offset:4096
ds_read_b128 a[212:215],v21 offset:4096
ds_read_b128 a[220:223],v21 offset:6144
ds_read_b128 a[216:219],v20 offset:6144
ds_read_b64_tr_b16 a[136:137],v22 offset:1032
ds_read_b64_tr_b16 a[138:139],v22 offset:3080
s_waitcnt lgkmcnt(11)
v_mfma_f32_16x16x32_bf16 v[128:131],a[128:131],a[192:195],0
s_add_u32 m0,s59,0
ds_read_b64_tr_b16 a[140:141],v22 offset:17416
ds_read_b64_tr_b16 a[142:143],v22 offset:19464
s_waitcnt lgkmcnt(10)
v_mfma_f32_16x16x32_bf16 v[128:131],a[132:135],a[196:199],v[128:131]
s_waitcnt lgkmcnt(9)
v_mfma_f32_16x16x32_bf16 v[136:139],a[132:135],a[204:207],0
s_waitcnt lgkmcnt(8)
v_mfma_f32_16x16x32_bf16 v[136:139],a[128:131],a[200:203],v[136:139]
s_waitcnt lgkmcnt(7)
v_mfma_f32_16x16x32_bf16 v[144:147],a[128:131],a[208:211],0
s_waitcnt lgkmcnt(6)
buffer_load_dwordx4 v48,s[28:31],s57 offen lds
v_mfma_f32_16x16x32_bf16 v[144:147],a[132:135],a[212:215],v[144:147]
s_waitcnt lgkmcnt(5)
v_mfma_f32_16x16x32_bf16 v[152:155],a[132:135],a[220:223],0
s_waitcnt lgkmcnt(4)
v_mfma_f32_16x16x32_bf16 v[152:155],a[128:131],a[216:219],v[152:155]
s_waitcnt lgkmcnt(2)
ds_read_b64_tr_b16 a[144:145],v22 offset:1280
ds_read_b64_tr_b16 a[146:147],v22 offset:3328
v_mfma_f32_16x16x32_bf16 v[132:135],a[136:139],a[192:195],0
s_waitcnt lgkmcnt(2)
ds_read_b64_tr_b16 a[148:149],v22 offset:17664
ds_read_b64_tr_b16 a[150:151],v22 offset:19712
v_mfma_f32_16x16x32_bf16 v[132:135],a[140:143],a[196:199],v[132:135]
buffer_load_dwordx4 v49,s[28:31],s57 offen offset:1024 lds
v_mfma_f32_16x16x32_bf16 v[140:143],a[140:143],a[204:207],0
v_mfma_f32_16x16x32_bf16 v[140:143],a[136:139],a[200:203],v[140:143]
v_mfma_f32_16x16x32_bf16 v[148:151],a[136:139],a[208:211],0
v_mfma_f32_16x16x32_bf16 v[148:151],a[140:143],a[212:215],v[148:151]
v_mfma_f32_16x16x32_bf16 v[156:159],a[140:143],a[220:223],0
buffer_load_dwordx4 v50,s[28:31],s57 offen offset:2048 lds
v_mfma_f32_16x16x32_bf16 v[156:159],a[136:139],a[216:219],v[156:159]
s_waitcnt lgkmcnt(2)
ds_read_b64_tr_b16 a[152:153],v22 offset:1288
ds_read_b64_tr_b16 a[154:155],v22 offset:3336
v_mfma_f32_16x16x32_bf16 v[160:163],a[144:147],a[192:195],0
s_waitcnt lgkmcnt(2)
ds_read_b64_tr_b16 a[156:157],v22 offset:17672
ds_read_b64_tr_b16 a[158:159],v22 offset:19720
v_mfma_f32_16x16x32_bf16 v[160:163],a[148:151],a[196:199],v[160:163]
v_mfma_f32_16x16x32_bf16 v[168:171],a[148:151],a[204:207],0
v_mfma_f32_16x16x32_bf16 v[168:171],a[144:147],a[200:203],v[168:171]
buffer_load_dwordx4 v51,s[28:31],s57 offen offset:3072 lds
v_mfma_f32_16x16x32_bf16 v[176:179],a[144:147],a[208:211],0
s_add_u32 m0,s59,4096
v_mfma_f32_16x16x32_bf16 v[176:179],a[148:151],a[212:215],v[176:179]
v_mfma_f32_16x16x32_bf16 v[184:187],a[148:151],a[220:223],0
v_mfma_f32_16x16x32_bf16 v[184:187],a[144:147],a[216:219],v[184:187]
s_waitcnt lgkmcnt(2)
v_mfma_f32_16x16x32_bf16 v[164:167],a[152:155],a[192:195],0
s_waitcnt lgkmcnt(0)
buffer_load_dwordx4 v52,s[28:31],s57 offen lds
ds_read_b64_tr_b16 a[160:161],v22 offset:1536
ds_read_b64_tr_b16 a[162:163],v22 offset:3584
v_mfma_f32_16x16x32_bf16 v[164:167],a[156:159],a[196:199],v[164:167]
ds_read_b64_tr_b16 a[164:165],v22 offset:17920
ds_read_b64_tr_b16 a[166:167],v22 offset:19968
v_mfma_f32_16x16x32_bf16 v[172:175],a[156:159],a[204:207],0
v_mfma_f32_16x16x32_bf16 v[172:175],a[152:155],a[200:203],v[172:175]
v_mfma_f32_16x16x32_bf16 v[180:183],a[152:155],a[208:211],0
v_mfma_f32_16x16x32_bf16 v[180:183],a[156:159],a[212:215],v[180:183]
buffer_load_dwordx4 v53,s[28:31],s57 offen offset:1024 lds
v_mfma_f32_16x16x32_bf16 v[188:191],a[156:159],a[220:223],0
s_waitcnt vmcnt(14)
v_mfma_f32_16x16x32_bf16 v[188:191],a[152:155],a[216:219],v[188:191]
s_barrier
ds_read_b64_tr_b16 a[168:169],v22 offset:1544
ds_read_b64_tr_b16 a[170:171],v22 offset:3592
s_waitcnt lgkmcnt(4)
v_mfma_f32_16x16x32_bf16 v[192:195],a[160:163],a[192:195],0
s_waitcnt lgkmcnt(2)
ds_read_b64_tr_b16 a[172:173],v22 offset:17928
ds_read_b64_tr_b16 a[174:175],v22 offset:19976
v_mfma_f32_16x16x32_bf16 v[192:195],a[164:167],a[196:199],v[192:195]
buffer_load_dwordx4 v54,s[28:31],s57 offen offset:2048 lds
v_mfma_f32_16x16x32_bf16 v[200:203],a[164:167],a[204:207],0
s_lshl_b32 s44,s41,13
s_add_u32 s44,s44,134217728
s_mov_b64 s[64:65],s[12:13]
v_mfma_f32_16x16x32_bf16 v[200:203],a[160:163],a[200:203],v[200:203]
s_add_u32 s64,s64,s44
s_addc_u32 s65,s65,0
s_and_b32 s65,s65,0xffff
v_mfma_f32_16x16x32_bf16 v[208:211],a[160:163],a[208:211],0
s_mov_b32 s66,8192
s_mov_b32 s67,0x20000
v_mov_b32 v14,4096
v_mfma_f32_16x16x32_bf16 v[208:211],a[164:167],a[212:215],v[208:211]
s_mov_b64 s[72:73],s[14:15]
buffer_load_dwordx4 v55,s[28:31],s57 offen offset:3072 lds
v_mfma_f32_16x16x32_bf16 v[216:219],a[164:167],a[220:223],0
s_add_u32 m0,s58,0
s_and_b32 s73,s73,0xffff
ds_read_b64_tr_b16 a[176:177],v22 offset:1792
v_mfma_f32_16x16x32_bf16 v[216:219],a[160:163],a[216:219],v[216:219]
s_mov_b32 s74,5242880
s_mov_b32 s75,0x20000
ds_read_b64_tr_b16 a[178:179],v22 offset:3840
s_waitcnt lgkmcnt(4)
v_mfma_f32_16x16x32_bf16 v[196:199],a[168:171],a[192:195],0
s_waitcnt lgkmcnt(2)
ds_read_b64_tr_b16 a[180:181],v22 offset:18176
ds_read_b64_tr_b16 a[182:183],v22 offset:20224
v_mfma_f32_16x16x32_bf16 v[196:199],a[172:175],a[196:199],v[196:199]
s_lshl_b32 s83,%1,2
buffer_load_dwordx4 v56,s[32:35],s56 offen lds
v_mfma_f32_16x16x32_bf16 v[204:207],a[172:175],a[204:207],0
s_add_u32 m0,s58,1024
v_lshlrev_b32 v15,3,v6
v_add_u32 v16,128,v15
v_mfma_f32_16x16x32_bf16 v[204:207],a[168:171],a[200:203],v[204:207]
s_add_u32 s83,s83,s40
ds_read_b64_tr_b16 a[184:185],v22 offset:1800
v_lshrrev_b32 v16,10,v16
v_mfma_f32_16x16x32_bf16 v[212:215],a[168:171],a[208:211],0
s_mul_i32 s83,s83,5120
ds_read_b64_tr_b16 a[186:187],v22 offset:3848
v_lshl_add_u32 v15,v16,24,v15
v_mfma_f32_16x16x32_bf16 v[212:215],a[172:175],a[212:215],v[212:215]
s_lshl_b32 s43,s41,5
buffer_load_dwordx4 v57,s[32:35],s56 offen lds
v_mfma_f32_16x16x32_bf16 v[220:223],a[172:175],a[220:223],0
)A"
R"A(s_lshl_b32 s44,s42,1
s_add_u32 s43,s43,s44
ds_read_b64_tr_b16 a[188:189],v22 offset:18184
v_mfma_f32_16x16x32_bf16 v[220:223],a[168:171],a[216:219],v[220:223]
s_add_u32 m0,s58,2048
s_lshl_b32 s44,s43,18
ds_read_b64_tr_b16 a[190:191],v22 offset:20232
s_waitcnt lgkmcnt(6)
v_mfma_f32_16x16x32_bf16 v[224:227],a[176:179],a[192:195],0
s_mov_b64 s[20:21],s[6:7]
s_mov_b32 s22,524288
ds_read_b64_tr_b16 a[128:129],v22
s_waitcnt lgkmcnt(5)
v_mfma_f32_16x16x32_bf16 v[224:227],a[180:183],a[196:199],v[224:227]
s_add_u32 s20,s20,s44
s_addc_u32 s21,s21,0
ds_read_b64_tr_b16 a[130:131],v22 offset:2048
v_mfma_f32_16x16x32_bf16 v[232:235],a[180:183],a[204:207],0
s_and_b32 s21,s21,0xffff
buffer_load_dwordx4 v58,s[32:35],s56 offen lds
v_mfma_f32_16x16x32_bf16 v[232:235],a[176:179],a[200:203],v[232:235]
s_mov_b32 s23,0x20000
s_mov_b64 s[24:25],s[8:9]
ds_read_b64_tr_b16 a[132:133],v22 offset:16384
v_mfma_f32_16x16x32_bf16 v[240:243],a[176:179],a[208:211],0
s_add_u32 m0,s58,3072
s_mov_b32 s26,524288
ds_read_b64_tr_b16 a[134:135],v22 offset:18432
v_mfma_f32_16x16x32_bf16 v[240:243],a[180:183],a[212:215],v[240:243]
s_add_u32 s24,s24,s44
s_addc_u32 s25,s25,0
ds_read_b64_tr_b16 a[136:137],v22 offset:8
v_mfma_f32_16x16x32_bf16 v[248:251],a[180:183],a[220:223],0
s_and_b32 s25,s25,0xffff
s_mov_b32 s27,0x20000
ds_read_b64_tr_b16 a[138:139],v22 offset:2056
v_mfma_f32_16x16x32_bf16 v[248:251],a[176:179],a[216:219],v[248:251]
s_lshl_b32 s44,s41,2
buffer_load_dwordx4 v59,s[32:35],s56 offen lds
s_waitcnt lgkmcnt(6)
v_mfma_f32_16x16x32_bf16 v[228:231],a[184:187],a[192:195],0
s_add_u32 s44,s44,s70
s_lshl_b32 s44,s44,3
ds_read_b64_tr_b16 a[140:141],v22 offset:16392
v_mfma_f32_16x16x32_bf16 v[228:231],a[188:191],a[196:199],v[228:231]
s_add_u32 m0,s58,4096
s_add_u32 s44,s44,s71
ds_read_b64_tr_b16 a[142:143],v22 offset:18440
v_mfma_f32_16x16x32_bf16 v[236:239],a[188:191],a[204:207],0
s_lshl_b32 s44,s44,18
s_mov_b64 s[36:37],s[12:13]
ds_read_b64_tr_b16 a[144:145],v22 offset:256
v_mfma_f32_16x16x32_bf16 v[236:239],a[184:187],a[200:203],v[236:239]
s_add_u32 s36,s36,s44
s_addc_u32 s37,s37,0
ds_read_b64_tr_b16 a[146:147],v22 offset:2304
v_mfma_f32_16x16x32_bf16 v[244:247],a[184:187],a[208:211],0
s_and_b32 s37,s37,0xffff
buffer_load_dwordx4 v60,s[32:35],s56 offen lds
v_mfma_f32_16x16x32_bf16 v[244:247],a[188:191],a[212:215],v[244:247]
s_xor_b32 s59,s59,131072
s_mov_b32 s38,262144
ds_read_b64_tr_b16 a[148:149],v22 offset:16640
v_mfma_f32_16x16x32_bf16 v[252:255],a[188:191],a[220:223],0
s_add_u32 m0,s58,5120
s_mov_b32 s39,0x20000
ds_read_b64_tr_b16 a[150:151],v22 offset:18688
v_mfma_f32_16x16x32_bf16 v[252:255],a[184:187],a[216:219],v[252:255]
s_lshl_b32 s51,s40,12
s_add_u32 s57,s57,128
ds_read_b64_tr_b16 a[152:153],v22 offset:264
s_waitcnt lgkmcnt(7)
v_mfma_f32_16x16x32_bf16 a[0:3],a[128:131],a[192:195],0
s_add_u32 s52,s51,16384
s_mul_i32 s47,s41,5
ds_read_b64_tr_b16 a[154:155],v22 offset:2312
v_mfma_f32_16x16x32_bf16 a[0:3],a[132:135],a[196:199],a[0:3]
s_add_u32 s47,s47,s42
buffer_load_dwordx4 v61,s[32:35],s56 offen lds
v_mfma_f32_16x16x32_bf16 a[32:35],a[132:135],a[204:207],0
s_and_b32 s47,s47,15
s_mul_i32 s44,s41,5
ds_read_b64_tr_b16 a[156:157],v22 offset:16648
v_mfma_f32_16x16x32_bf16 a[32:35],a[128:131],a[200:203],a[32:35]
s_add_u32 m0,s58,6144
s_add_u32 s44,s44,s42
ds_read_b64_tr_b16 a[158:159],v22 offset:18696
v_mfma_f32_16x16x32_bf16 a[64:67],a[128:131],a[208:211],0
s_lshl_b32 s47,s44,12
s_and_b32 s47,s47,0x7000
ds_read_b64_tr_b16 a[160:161],v22 offset:512
v_mfma_f32_16x16x32_bf16 a[64:67],a[132:135],a[212:215],a[64:67]
s_lshl_b32 s44,s44,14
s_and_b32 s44,s44,0x20000
ds_read_b64_tr_b16 a[162:163],v22 offset:2560
v_mfma_f32_16x16x32_bf16 a[96:99],a[132:135],a[220:223],0
s_or_b32 s47,s47,s44
buffer_load_dwordx4 v62,s[32:35],s56 offen lds
v_mfma_f32_16x16x32_bf16 a[96:99],a[128:131],a[216:219],a[96:99]
s_mov_b32 s54,0
s_mov_b32 s61,0x3e0293ee
ds_read_b64_tr_b16 a[164:165],v22 offset:16896
v_mfma_f32_16x16x32_bf16 a[4:7],a[136:139],a[192:195],0
s_add_u32 m0,s58,7168
s_mov_b32 s63,0x5d800000
ds_read_b64_tr_b16 a[166:167],v22 offset:18944
s_waitcnt lgkmcnt(6)
v_mfma_f32_16x16x32_bf16 a[4:7],a[140:143],a[196:199],a[4:7]
s_lshl_b32 s44,s40,2
s_add_u32 s44,s44,s70
ds_read_b64_tr_b16 a[168:169],v22 offset:520
v_mfma_f32_16x16x32_bf16 a[36:39],a[140:143],a[204:207],0
s_mov_b32 s46,0xff800000
ds_read_b64_tr_b16 a[170:171],v22 offset:2568
v_lshlrev_b32 v70,15,v65
v_mfma_f32_16x16x32_bf16 a[36:39],a[136:139],a[200:203],a[36:39]
s_add_u32 s60,s60,32768
buffer_load_dwordx4 v63,s[32:35],s56 offen lds
v_mfma_f32_16x16x32_bf16 a[68:71],a[136:139],a[208:211],0
s_cmp_eq_u32 s44,14
s_cselect_b32 s18,0x7149f2ca,s46
ds_read_b64_tr_b16 a[172:173],v22 offset:16904
v_mfma_f32_16x16x32_bf16 a[68:71],a[140:143],a[212:215],a[68:71]
s_add_u32 m0,s59,0
ds_read_b64_tr_b16 a[174:175],v22 offset:18952
v_lshl_add_u32 v70,v66,6,v70
v_mfma_f32_16x16x32_bf16 a[100:103],a[140:143],a[220:223],0
s_cmp_eq_u32 s44,13
s_cselect_b32 s19,0x7149f2ca,s46
ds_read_b64_tr_b16 a[176:177],v22 offset:768
v_mfma_f32_16x16x32_bf16 a[100:103],a[136:139],a[216:219],a[100:103]
s_cmp_eq_u32 s44,12
s_cselect_b32 s100,0x7149f2ca,s46
ds_read_b64_tr_b16 a[178:179],v22 offset:2816
v_mfma_f32_16x16x32_bf16 a[8:11],a[144:147],a[192:195],0
s_add_u32 s58,s58,32768
buffer_load_dwordx4 v48,s[28:31],s57 offen lds
v_mfma_f32_16x16x32_bf16 a[8:11],a[148:151],a[196:199],a[8:11]
s_cmp_eq_u32 s44,15
s_cselect_b32 s101,0x7149f2ca,s46
ds_read_b64_tr_b16 a[180:181],v22 offset:17152
v_mfma_f32_16x16x32_bf16 a[40:43],a[148:151],a[204:207],0
s_lshl_b32 s44,s40,8
ds_read_b64_tr_b16 a[182:183],v22 offset:19200
v_add_u32 v70,s44,v70
v_mfma_f32_16x16x32_bf16 a[40:43],a[144:147],a[200:203],a[40:43]
s_add_u32 s56,s56,524288
ds_read_b64_tr_b16 a[184:185],v22 offset:776
v_lshl_add_u32 v4,v64,4,v70
v_mfma_f32_16x16x32_bf16 a[72:75],a[144:147],a[208:211],0
s_mul_i32 s81,s70,1310720
ds_read_b64_tr_b16 a[186:187],v22 offset:2824
v_xor_b32 v71,2,v64
v_mfma_f32_16x16x32_bf16 a[72:75],a[148:151],a[212:215],a[72:75]
s_mul_i32 s44,s71,320
buffer_load_dwordx4 v49,s[28:31],s57 offen offset:1024 lds
v_mfma_f32_16x16x32_bf16 a[104:107],a[148:151],a[220:223],0
s_add_u32 s81,s81,s44
ds_read_b64_tr_b16 a[188:189],v22 offset:17160
v_lshl_add_u32 v5,v71,4,v70
v_mfma_f32_16x16x32_bf16 a[104:107],a[144:147],a[216:219],a[104:107]
s_mul_i32 s82,s40,10240
ds_read_b64_tr_b16 a[190:191],v22 offset:19208
v_lshlrev_b32 v72,10,v65
v_mfma_f32_16x16x32_bf16 a[12:15],a[152:155],a[192:195],0
s_cmp_eq_u32 s60,131072
s_cselect_b32 s60,32768,s60
v_lshl_add_u32 v72,v64,6,v72
s_waitcnt lgkmcnt(6)
v_mfma_f32_16x16x32_bf16 a[12:15],a[156:159],a[196:199],a[12:15]
s_mul_i32 s44,s40,40960
v_and_b32 v73,2,v65
v_add_u32 v22,s60,v23
v_mfma_f32_16x16x32_bf16 a[44:47],a[156:159],a[204:207],0
s_lshr_b32 s92,%1,3
buffer_load_dwordx4 v50,s[28:31],s57 offen offset:2048 lds
v_mfma_f32_16x16x32_bf16 a[44:47],a[152:155],a[200:203],a[44:47]
s_lshl_b32 s92,s92,2
v_xor_b32 v20,131072,v20
v_xor_b32 v21,131072,v21
v_mfma_f32_16x16x32_bf16 a[76:79],a[152:155],a[208:211],0
s_add_u32 s92,s92,s40
v_xor_b32 v73,v73,v66
v_lshl_add_u32 v72,v73,4,v72
v_mfma_f32_16x16x32_bf16 a[76:79],a[156:159],a[212:215],a[76:79]
s_lshl_b32 s92,s92,7
v_add_u32 v1,98304,v72
v_lshlrev_b32 v72,10,v66
v_mfma_f32_16x16x32_bf16 a[108:111],a[156:159],a[220:223],0
s_add_u32 s93,s92,L_tailseg_%=-L_pc_%=
v_lshl_add_u32 v72,v65,6,v72
v_lshl_add_u32 v72,v64,3,v72
v_mfma_f32_16x16x32_bf16 a[108:111],a[152:155],a[216:219],a[108:111]
s_waitcnt vmcnt(11) lgkmcnt(0)
s_add_u32 s60,s60,32768
buffer_load_dwordx4 v51,s[28:31],s57 offen offset:3072 lds
v_mfma_f32_16x16x32_bf16 a[16:19],a[160:163],a[192:195],0
s_barrier
s_add_u32 m0,s59,4096
ds_read_b128 a[224:227],v20
v_mfma_f32_16x16x32_bf16 a[16:19],a[164:167],a[196:199],a[16:19]
s_xor_b32 s59,s59,131072
ds_read_b64_tr_b16 a[128:129],v22 offset:1024
v_add_u32 v72,114688,v72
v_mfma_f32_16x16x32_bf16 a[48:51],a[164:167],a[204:207],0
s_cmp_ge_u32 s58,131072
s_cselect_b32 s62,98304,0
ds_read_b64_tr_b16 a[130:131],v22 offset:3072
v_mfma_f32_16x16x32_bf16 a[48:51],a[160:163],a[200:203],a[48:51]
s_sub_u32 s58,s58,s62
buffer_load_dwordx4 v52,s[28:31],s57 offen lds
v_mfma_f32_16x16x32_bf16 a[80:83],a[160:163],a[208:211],0
ds_read_b128 a[228:231],v21
v_and_b32 v73,1,v66
v_mfma_f32_16x16x32_bf16 a[80:83],a[164:167],a[212:215],a[80:83]
ds_read_b64_tr_b16 a[132:133],v22 offset:17408
v_lshl_add_u32 v2,v73,5,v72
v_mfma_f32_16x16x32_bf16 a[112:115],a[164:167],a[220:223],0
ds_read_b64_tr_b16 a[134:135],v22 offset:19456
v_xor_b32 v73,1,v73
v_mfma_f32_16x16x32_bf16 a[112:115],a[160:163],a[216:219],a[112:115]
ds_read_b128 a[232:235],v20 offset:2048
v_lshl_add_u32 v3,v73,5,v72
v_mfma_f32_16x16x32_bf16 a[20:23],a[168:171],a[192:195],0
buffer_load_dwordx4 v53,s[28:31],s57 offen offset:1024 lds
v_mfma_f32_16x16x32_bf16 a[20:23],a[172:175],a[196:199],a[20:23]
ds_read_b128 a[236:239],v21 offset:2048
v_mov_b32 v73,0x3f803f80
v_mfma_f32_16x16x32_bf16 a[52:55],a[172:175],a[204:207],0
ds_read_b128 a[240:243],v20 offset:4096
v_add_u32 v70,0,v12
v_mfma_f32_16x16x32_bf16 a[52:55],a[168:171],a[200:203],a[52:55]
ds_read_b128 a[244:247],v21 offset:4096
v_mul_u32_u24 v71,3277,v70
v_mfma_f32_16x16x32_bf16 a[84:87],a[168:171],a[208:211],0
buffer_load_dword v13,v14,s[64:67],0 offen sc1
v_mfma_f32_16x16x32_bf16 a[84:87],a[172:175],a[212:215],a[84:87]
ds_read_b128 a[248:251],v20 offset:6144
v_lshrrev_b32 v71,16,v71
v_mfma_f32_16x16x32_bf16 a[116:119],a[172:175],a[220:223],0
ds_read_b128 a[252:255],v21 offset:6144
v_mul_u32_u24 v72,20,v71
v_mfma_f32_16x16x32_bf16 a[116:119],a[168:171],a[216:219],a[116:119]
ds_read_b64_tr_b16 a[136:137],v22 offset:1032
v_sub_u32 v72,v70,v72
v_mfma_f32_16x16x32_bf16 a[24:27],a[176:179],a[192:195],0
buffer_load_dwordx4 v54,s[28:31],s57 offen offset:2048 lds
v_mfma_f32_16x16x32_bf16 a[24:27],a[180:183],a[196:199],a[24:27]
ds_read_b64_tr_b16 a[138:139],v22 offset:3080
v_add_u32 v70,64,v12
v_mfma_f32_16x16x32_bf16 a[56:59],a[180:183],a[204:207],0
ds_read_b64_tr_b16 a[140:141],v22 offset:17416
v_lshlrev_b32 v24,10,v6
v_mfma_f32_16x16x32_bf16 a[56:59],a[176:179],a[200:203],a[56:59]
s_cmp_eq_u32 s60,131072
s_cselect_b32 s60,32768,s60
ds_read_b64_tr_b16 a[142:143],v22 offset:19464
v_mfma_f32_16x16x32_bf16 a[88:91],a[176:179],a[208:211],0
buffer_load_dwordx4 v55,s[28:31],s57 offen offset:3072 lds
v_mfma_f32_16x16x32_bf16 a[88:91],a[180:183],a[212:215],a[88:91]
s_add_u32 s57,s57,128
ds_read_b64_tr_b16 a[144:145],v22 offset:1280
v_xor_b32 v20,131072,v20
v_mfma_f32_16x16x32_bf16 a[120:123],a[180:183],a[220:223],0
ds_read_b64_tr_b16 a[146:147],v22 offset:3328
v_xor_b32 v21,131072,v21
v_mfma_f32_16x16x32_bf16 a[120:123],a[176:179],a[216:219],a[120:123]
ds_read_b64_tr_b16 a[148:149],v22 offset:17664
v_mfma_f32_16x16x32_bf16 a[28:31],a[184:187],a[192:195],0
ds_read_b64_tr_b16 a[150:151],v22 offset:19712
v_mfma_f32_16x16x32_bf16 a[28:31],a[188:191],a[196:199],a[28:31]
ds_read_b64_tr_b16 a[152:153],v22 offset:1288
ds_read_b64_tr_b16 a[154:155],v22 offset:3336
ds_read_b64_tr_b16 a[156:157],v22 offset:17672
ds_read_b64_tr_b16 a[158:159],v22 offset:19720
v_mfma_f32_16x16x32_bf16 a[60:63],a[188:191],a[204:207],0
v_mfma_f32_16x16x32_bf16 a[60:63],a[184:187],a[200:203],a[60:63]
v_cmp_gt_u32 vcc,2,v64
s_nop 1
v_cndmask_b32 v28,0,v73,vcc
v_mfma_f32_16x16x32_bf16 a[92:95],a[184:187],a[208:211],0
v_and_b32 v73,1,v64
v_mul_u32_u24 v73,0xaaaa,v73
v_mfma_f32_16x16x32_bf16 a[92:95],a[188:191],a[212:215],a[92:95]
v_add_u32 v73,0x4444,v73
v_lshl_or_b32 v27,v73,16,v73
v_mfma_f32_16x16x32_bf16 a[124:127],a[188:191],a[220:223],0
s_add_u32 m0,s58,0
v_mov_b32 v29,v28
v_mov_b32 v30,v28
v_mfma_f32_16x16x32_bf16 a[124:127],a[184:187],a[216:219],a[124:127]
s_waitcnt lgkmcnt(5)
ds_read_b64_tr_b16 a[160:161],v22 offset:1536
v_mov_b32 v31,v28
v_mfma_f32_16x16x32_bf16 v[128:131],a[128:131],a[224:227],v[128:131]
ds_read_b64_tr_b16 a[162:163],v22 offset:3584
v_bfe_u32 v73,v71,2,1
v_mfma_f32_16x16x32_bf16 v[128:131],a[132:135],a[228:231],v[128:131]
buffer_load_dwordx4 v56,s[32:35],s56 offen lds
v_mfma_f32_16x16x32_bf16 v[136:139],a[132:135],a[236:239],v[136:139]
)A"
R"A(ds_read_b64_tr_b16 a[164:165],v22 offset:17920
v_lshlrev_b32 v73,1,v73
v_mfma_f32_16x16x32_bf16 v[136:139],a[128:131],a[232:235],v[136:139]
s_add_u32 m0,s58,1024
ds_read_b64_tr_b16 a[166:167],v22 offset:19968
v_xor_b32 v72,v72,v73
v_mfma_f32_16x16x32_bf16 v[144:147],a[128:131],a[240:243],v[144:147]
ds_read_b64_tr_b16 a[168:169],v22 offset:1544
v_mul_u32_u24 v71,1280,v71
v_mfma_f32_16x16x32_bf16 v[144:147],a[132:135],a[244:247],v[144:147]
ds_read_b64_tr_b16 a[170:171],v22 offset:3592
v_lshl_add_u32 v71,v72,4,v71
v_mfma_f32_16x16x32_bf16 v[152:155],a[132:135],a[252:255],v[152:155]
buffer_load_dwordx4 v57,s[32:35],s56 offen lds
v_mfma_f32_16x16x32_bf16 v[152:155],a[128:131],a[248:251],v[152:155]
ds_read_b64_tr_b16 a[172:173],v22 offset:17928
v_add_u32 v71,s44,v71
v_mfma_f32_16x16x32_bf16 v[132:135],a[136:139],a[224:227],v[132:135]
ds_read_b64_tr_b16 a[174:175],v22 offset:19976
v_subrev_u32 v7,0,v71
v_mfma_f32_16x16x32_bf16 v[132:135],a[140:143],a[228:231],v[132:135]
s_add_u32 m0,s58,2048
v_mul_u32_u24 v71,3277,v70
v_lshrrev_b32 v71,16,v71
v_mfma_f32_16x16x32_bf16 v[140:143],a[140:143],a[236:239],v[140:143]
ds_read_b64_tr_b16 a[176:177],v22 offset:1792
v_mul_u32_u24 v72,20,v71
v_mfma_f32_16x16x32_bf16 v[140:143],a[136:139],a[232:235],v[140:143]
ds_read_b64_tr_b16 a[178:179],v22 offset:3840
v_sub_u32 v72,v70,v72
v_mfma_f32_16x16x32_bf16 v[148:151],a[136:139],a[240:243],v[148:151]
buffer_load_dwordx4 v58,s[32:35],s56 offen lds
v_mfma_f32_16x16x32_bf16 v[148:151],a[140:143],a[244:247],v[148:151]
ds_read_b64_tr_b16 a[180:181],v22 offset:18176
v_bfe_u32 v73,v71,2,1
v_mfma_f32_16x16x32_bf16 v[156:159],a[140:143],a[252:255],v[156:159]
ds_read_b64_tr_b16 a[182:183],v22 offset:20224
v_lshlrev_b32 v73,1,v73
v_mfma_f32_16x16x32_bf16 v[156:159],a[136:139],a[248:251],v[156:159]
s_add_u32 m0,s58,3072
v_xor_b32 v72,v72,v73
v_mul_u32_u24 v71,1280,v71
v_mfma_f32_16x16x32_bf16 v[160:163],a[144:147],a[224:227],v[160:163]
s_waitcnt lgkmcnt(4)
ds_read_b64_tr_b16 a[184:185],v22 offset:1800
v_lshl_add_u32 v71,v72,4,v71
v_mfma_f32_16x16x32_bf16 v[160:163],a[148:151],a[228:231],v[160:163]
ds_read_b64_tr_b16 a[186:187],v22 offset:3848
v_add_u32 v71,s44,v71
v_mfma_f32_16x16x32_bf16 v[168:171],a[148:151],a[236:239],v[168:171]
buffer_load_dwordx4 v59,s[32:35],s56 offen lds
v_mfma_f32_16x16x32_bf16 v[168:171],a[144:147],a[232:235],v[168:171]
ds_read_b64_tr_b16 a[188:189],v22 offset:18184
v_subrev_u32 v8,1024,v71
v_mfma_f32_16x16x32_bf16 v[176:179],a[144:147],a[240:243],v[176:179]
s_add_u32 m0,s58,4096
ds_read_b64_tr_b16 a[190:191],v22 offset:20232
v_add_u32 v70,128,v12
v_mfma_f32_16x16x32_bf16 v[176:179],a[148:151],a[244:247],v[176:179]
v_mul_u32_u24 v71,3277,v70
v_lshrrev_b32 v71,16,v71
v_mfma_f32_16x16x32_bf16 v[184:187],a[148:151],a[252:255],v[184:187]
v_mul_u32_u24 v72,20,v71
v_sub_u32 v72,v70,v72
v_mfma_f32_16x16x32_bf16 v[184:187],a[144:147],a[248:251],v[184:187]
buffer_load_dwordx4 v60,s[32:35],s56 offen lds
v_mfma_f32_16x16x32_bf16 v[164:167],a[152:155],a[224:227],v[164:167]
ds_read_b64_tr_b16 a[128:129],v22
v_bfe_u32 v73,v71,2,1
v_mfma_f32_16x16x32_bf16 v[164:167],a[156:159],a[228:231],v[164:167]
ds_read_b64_tr_b16 a[130:131],v22 offset:2048
v_lshlrev_b32 v73,1,v73
v_mfma_f32_16x16x32_bf16 v[172:175],a[156:159],a[236:239],v[172:175]
s_add_u32 m0,s58,5120
ds_read_b64_tr_b16 a[132:133],v22 offset:16384
v_xor_b32 v72,v72,v73
v_mfma_f32_16x16x32_bf16 v[172:175],a[152:155],a[232:235],v[172:175]
ds_read_b64_tr_b16 a[134:135],v22 offset:18432
v_mul_u32_u24 v71,1280,v71
v_mfma_f32_16x16x32_bf16 v[180:183],a[152:155],a[240:243],v[180:183]
v_lshl_add_u32 v71,v72,4,v71
v_add_u32 v71,s44,v71
v_mfma_f32_16x16x32_bf16 v[180:183],a[156:159],a[244:247],v[180:183]
buffer_load_dwordx4 v61,s[32:35],s56 offen lds
v_mfma_f32_16x16x32_bf16 v[188:191],a[156:159],a[252:255],v[188:191]
ds_read_b64_tr_b16 a[136:137],v22 offset:8
v_subrev_u32 v9,2048,v71
v_mfma_f32_16x16x32_bf16 v[188:191],a[152:155],a[248:251],v[188:191]
ds_read_b64_tr_b16 a[138:139],v22 offset:2056
v_add_u32 v70,192,v12
v_mfma_f32_16x16x32_bf16 v[192:195],a[160:163],a[224:227],v[192:195]
s_add_u32 m0,s58,6144
ds_read_b64_tr_b16 a[140:141],v22 offset:16392
v_mul_u32_u24 v71,3277,v70
v_mfma_f32_16x16x32_bf16 v[192:195],a[164:167],a[228:231],v[192:195]
ds_read_b64_tr_b16 a[142:143],v22 offset:18440
v_lshrrev_b32 v71,16,v71
v_mfma_f32_16x16x32_bf16 v[200:203],a[164:167],a[236:239],v[200:203]
v_mul_u32_u24 v72,20,v71
v_sub_u32 v72,v70,v72
v_mfma_f32_16x16x32_bf16 v[200:203],a[160:163],a[232:235],v[200:203]
buffer_load_dwordx4 v62,s[32:35],s56 offen lds
v_mfma_f32_16x16x32_bf16 v[208:211],a[160:163],a[240:243],v[208:211]
ds_read_b64_tr_b16 a[144:145],v22 offset:256
v_bfe_u32 v73,v71,2,1
v_mfma_f32_16x16x32_bf16 v[208:211],a[164:167],a[244:247],v[208:211]
s_add_u32 m0,s58,7168
ds_read_b64_tr_b16 a[146:147],v22 offset:2304
v_lshlrev_b32 v73,1,v73
v_mfma_f32_16x16x32_bf16 v[216:219],a[164:167],a[252:255],v[216:219]
s_add_u32 s58,s58,32768
ds_read_b64_tr_b16 a[148:149],v22 offset:16640
v_xor_b32 v72,v72,v73
v_mfma_f32_16x16x32_bf16 v[216:219],a[160:163],a[248:251],v[216:219]
ds_read_b64_tr_b16 a[150:151],v22 offset:18688
v_mul_u32_u24 v71,1280,v71
v_mfma_f32_16x16x32_bf16 v[196:199],a[168:171],a[224:227],v[196:199]
buffer_load_dwordx4 v63,s[32:35],s56 offen lds
v_mfma_f32_16x16x32_bf16 v[196:199],a[172:175],a[228:231],v[196:199]
s_add_u32 s56,s56,524288
v_lshl_add_u32 v71,v72,4,v71
v_add_u32 v71,s44,v71
v_mfma_f32_16x16x32_bf16 v[204:207],a[172:175],a[236:239],v[204:207]
v_subrev_u32 v10,3072,v71
v_add_u32 v70,256,v12
v_mfma_f32_16x16x32_bf16 v[204:207],a[168:171],a[232:235],v[204:207]
s_add_u32 m0,s59,0
ds_read_b64_tr_b16 a[152:153],v22 offset:264
v_mul_u32_u24 v71,3277,v70
v_mfma_f32_16x16x32_bf16 v[212:215],a[168:171],a[240:243],v[212:215]
ds_read_b64_tr_b16 a[154:155],v22 offset:2312
v_lshrrev_b32 v71,16,v71
v_mfma_f32_16x16x32_bf16 v[212:215],a[172:175],a[244:247],v[212:215]
ds_read_b64_tr_b16 a[156:157],v22 offset:16648
v_mul_u32_u24 v72,20,v71
v_mfma_f32_16x16x32_bf16 v[220:223],a[172:175],a[252:255],v[220:223]
buffer_load_dwordx4 v48,s[28:31],s57 offen lds
v_mfma_f32_16x16x32_bf16 v[220:223],a[168:171],a[248:251],v[220:223]
s_waitcnt lgkmcnt(3)
ds_read_b64_tr_b16 a[158:159],v22 offset:18696
v_sub_u32 v72,v70,v72
v_mfma_f32_16x16x32_bf16 v[224:227],a[176:179],a[224:227],v[224:227]
v_bfe_u32 v73,v71,2,1
v_lshlrev_b32 v73,1,v73
v_mfma_f32_16x16x32_bf16 v[224:227],a[180:183],a[228:231],v[224:227]
ds_read_b64_tr_b16 a[160:161],v22 offset:512
v_xor_b32 v72,v72,v73
v_mfma_f32_16x16x32_bf16 v[232:235],a[180:183],a[236:239],v[232:235]
ds_read_b64_tr_b16 a[162:163],v22 offset:2560
v_mul_u32_u24 v71,1280,v71
v_mfma_f32_16x16x32_bf16 v[232:235],a[176:179],a[232:235],v[232:235]
ds_read_b64_tr_b16 a[164:165],v22 offset:16896
v_lshl_add_u32 v71,v72,4,v71
v_mfma_f32_16x16x32_bf16 v[240:243],a[176:179],a[240:243],v[240:243]
buffer_load_dwordx4 v49,s[28:31],s57 offen offset:1024 lds
v_mfma_f32_16x16x32_bf16 v[240:243],a[180:183],a[244:247],v[240:243]
ds_read_b64_tr_b16 a[166:167],v22 offset:18944
v_add_u32 v71,s44,v71
v_mfma_f32_16x16x32_bf16 v[248:251],a[180:183],a[252:255],v[248:251]
ds_read_b64_tr_b16 a[168:169],v22 offset:520
v_subrev_u32 v11,0,v71
v_mfma_f32_16x16x32_bf16 v[248:251],a[176:179],a[248:251],v[248:251]
s_cmp_ge_u32 s58,131072
s_cselect_b32 s62,98304,0
ds_read_b64_tr_b16 a[170:171],v22 offset:2568
v_mfma_f32_16x16x32_bf16 v[228:231],a[184:187],a[224:227],v[228:231]
s_sub_u32 s58,s58,s62
ds_read_b64_tr_b16 a[172:173],v22 offset:16904
v_mfma_f32_16x16x32_bf16 v[228:231],a[188:191],a[228:231],v[228:231]
buffer_load_dwordx4 v50,s[28:31],s57 offen offset:2048 lds
v_mfma_f32_16x16x32_bf16 v[236:239],a[188:191],a[236:239],v[236:239]
ds_read_b64_tr_b16 a[174:175],v22 offset:18952
v_mfma_f32_16x16x32_bf16 v[236:239],a[184:187],a[232:235],v[236:239]
ds_read_b64_tr_b16 a[176:177],v22 offset:768
v_mfma_f32_16x16x32_bf16 v[244:247],a[184:187],a[240:243],v[244:247]
ds_read_b64_tr_b16 a[178:179],v22 offset:2816
v_mfma_f32_16x16x32_bf16 v[244:247],a[188:191],a[244:247],v[244:247]
ds_read_b64_tr_b16 a[180:181],v22 offset:17152
v_mfma_f32_16x16x32_bf16 v[252:255],a[188:191],a[252:255],v[252:255]
ds_read_b64_tr_b16 a[182:183],v22 offset:19200
v_mfma_f32_16x16x32_bf16 v[252:255],a[184:187],a[248:251],v[252:255]
buffer_load_dwordx4 v51,s[28:31],s57 offen offset:3072 lds
v_mfma_f32_16x16x32_bf16 a[0:3],a[128:131],a[224:227],a[0:3]
ds_read_b64_tr_b16 a[184:185],v22 offset:776
v_mfma_f32_16x16x32_bf16 a[0:3],a[132:135],a[228:231],a[0:3]
ds_read_b64_tr_b16 a[186:187],v22 offset:2824
v_mfma_f32_16x16x32_bf16 a[32:35],a[132:135],a[236:239],a[32:35]
s_add_u32 m0,s59,4096
s_xor_b32 s59,s59,131072
ds_read_b64_tr_b16 a[188:189],v22 offset:17160
v_mfma_f32_16x16x32_bf16 a[32:35],a[128:131],a[232:235],a[32:35]
ds_read_b64_tr_b16 a[190:191],v22 offset:19208
v_add_u32 v22,s60,v23
v_mfma_f32_16x16x32_bf16 a[64:67],a[128:131],a[240:243],a[64:67]
v_mfma_f32_16x16x32_bf16 a[64:67],a[132:135],a[244:247],a[64:67]
buffer_load_dwordx4 v52,s[28:31],s57 offen lds
v_mfma_f32_16x16x32_bf16 a[96:99],a[132:135],a[252:255],a[96:99]
v_mfma_f32_16x16x32_bf16 a[96:99],a[128:131],a[248:251],a[96:99]
v_mfma_f32_16x16x32_bf16 a[4:7],a[136:139],a[224:227],a[4:7]
v_mfma_f32_16x16x32_bf16 a[4:7],a[140:143],a[228:231],a[4:7]
v_mfma_f32_16x16x32_bf16 a[36:39],a[140:143],a[236:239],a[36:39]
buffer_load_dwordx4 v53,s[28:31],s57 offen offset:1024 lds
v_mfma_f32_16x16x32_bf16 a[36:39],a[136:139],a[232:235],a[36:39]
v_mfma_f32_16x16x32_bf16 a[68:71],a[136:139],a[240:243],a[68:71]
v_mfma_f32_16x16x32_bf16 a[68:71],a[140:143],a[244:247],a[68:71]
v_mfma_f32_16x16x32_bf16 a[100:103],a[140:143],a[252:255],a[100:103]
v_mfma_f32_16x16x32_bf16 a[100:103],a[136:139],a[248:251],a[100:103]
v_mfma_f32_16x16x32_bf16 a[8:11],a[144:147],a[224:227],a[8:11]
buffer_load_dwordx4 v54,s[28:31],s57 offen offset:2048 lds
v_mfma_f32_16x16x32_bf16 a[8:11],a[148:151],a[228:231],a[8:11]
v_mfma_f32_16x16x32_bf16 a[40:43],a[148:151],a[236:239],a[40:43]
v_mfma_f32_16x16x32_bf16 a[40:43],a[144:147],a[232:235],a[40:43]
v_mfma_f32_16x16x32_bf16 a[72:75],a[144:147],a[240:243],a[72:75]
v_mfma_f32_16x16x32_bf16 a[72:75],a[148:151],a[244:247],a[72:75]
v_mfma_f32_16x16x32_bf16 a[104:107],a[148:151],a[252:255],a[104:107]
buffer_load_dwordx4 v55,s[28:31],s57 offen offset:3072 lds
v_mfma_f32_16x16x32_bf16 a[104:107],a[144:147],a[248:251],a[104:107]
s_add_u32 s57,s57,128
s_waitcnt lgkmcnt(0)
v_mfma_f32_16x16x32_bf16 a[12:15],a[152:155],a[224:227],a[12:15]
v_mfma_f32_16x16x32_bf16 a[12:15],a[156:159],a[228:231],a[12:15]
v_mfma_f32_16x16x32_bf16 a[44:47],a[156:159],a[236:239],a[44:47]
v_mfma_f32_16x16x32_bf16 a[44:47],a[152:155],a[232:235],a[44:47]
v_mfma_f32_16x16x32_bf16 a[76:79],a[152:155],a[240:243],a[76:79]
v_mfma_f32_16x16x32_bf16 a[76:79],a[156:159],a[244:247],a[76:79]
v_mfma_f32_16x16x32_bf16 a[108:111],a[156:159],a[252:255],a[108:111]
v_mfma_f32_16x16x32_bf16 a[108:111],a[152:155],a[248:251],a[108:111]
s_waitcnt vmcnt(16)
v_mfma_f32_16x16x32_bf16 a[16:19],a[160:163],a[224:227],a[16:19]
s_barrier
ds_read_b128 a[192:195],v20
v_mfma_f32_16x16x32_bf16 a[16:19],a[164:167],a[228:231],a[16:19]
ds_read_b128 a[196:199],v21
v_mfma_f32_16x16x32_bf16 a[48:51],a[164:167],a[236:239],a[48:51]
ds_read_b128 a[200:203],v20 offset:2048
v_mfma_f32_16x16x32_bf16 a[48:51],a[160:163],a[232:235],a[48:51]
ds_read_b128 a[204:207],v21 offset:2048
v_mfma_f32_16x16x32_bf16 a[80:83],a[160:163],a[240:243],a[80:83]
ds_read_b128 a[208:211],v20 offset:4096
v_mfma_f32_16x16x32_bf16 a[80:83],a[164:167],a[244:247],a[80:83]
ds_read_b128 a[212:215],v21 offset:4096
v_mfma_f32_16x16x32_bf16 a[112:115],a[164:167],a[252:255],a[112:115]
ds_read_b128 a[216:219],v20 offset:6144
v_mfma_f32_16x16x32_bf16 a[112:115],a[160:163],a[248:251],a[112:115]
ds_read_b128 a[220:223],v21 offset:6144
v_mfma_f32_16x16x32_bf16 a[20:23],a[168:171],a[224:227],a[20:23]
ds_read_b64_tr_b16 a[128:129],v22 offset:1024
v_mfma_f32_16x16x32_bf16 a[20:23],a[172:175],a[228:231],a[20:23]
ds_read_b64_tr_b16 a[130:131],v22 offset:3072
)A"
R"A(v_mfma_f32_16x16x32_bf16 a[52:55],a[172:175],a[236:239],a[52:55]
ds_read_b64_tr_b16 a[132:133],v22 offset:17408
v_mfma_f32_16x16x32_bf16 a[52:55],a[168:171],a[232:235],a[52:55]
ds_read_b64_tr_b16 a[134:135],v22 offset:19456
v_mfma_f32_16x16x32_bf16 a[84:87],a[168:171],a[240:243],a[84:87]
ds_read_b64_tr_b16 a[136:137],v22 offset:1032
v_mfma_f32_16x16x32_bf16 a[84:87],a[172:175],a[244:247],a[84:87]
ds_read_b64_tr_b16 a[138:139],v22 offset:3080
v_mfma_f32_16x16x32_bf16 a[116:119],a[172:175],a[252:255],a[116:119]
ds_read_b64_tr_b16 a[140:141],v22 offset:17416
v_mfma_f32_16x16x32_bf16 a[116:119],a[168:171],a[248:251],a[116:119]
ds_read_b64_tr_b16 a[142:143],v22 offset:19464
v_mfma_f32_16x16x32_bf16 a[24:27],a[176:179],a[224:227],a[24:27]
ds_read_b64_tr_b16 a[144:145],v22 offset:1280
v_mfma_f32_16x16x32_bf16 a[24:27],a[180:183],a[228:231],a[24:27]
ds_read_b64_tr_b16 a[146:147],v22 offset:3328
v_mfma_f32_16x16x32_bf16 a[56:59],a[180:183],a[236:239],a[56:59]
ds_read_b64_tr_b16 a[148:149],v22 offset:17664
v_mfma_f32_16x16x32_bf16 a[56:59],a[176:179],a[232:235],a[56:59]
ds_read_b64_tr_b16 a[150:151],v22 offset:19712
v_mfma_f32_16x16x32_bf16 a[88:91],a[176:179],a[240:243],a[88:91]
ds_read_b64_tr_b16 a[152:153],v22 offset:1288
v_mfma_f32_16x16x32_bf16 a[88:91],a[180:183],a[244:247],a[88:91]
ds_read_b64_tr_b16 a[154:155],v22 offset:3336
v_mfma_f32_16x16x32_bf16 a[120:123],a[180:183],a[252:255],a[120:123]
ds_read_b64_tr_b16 a[156:157],v22 offset:17672
v_mfma_f32_16x16x32_bf16 a[120:123],a[176:179],a[248:251],a[120:123]
ds_read_b64_tr_b16 a[158:159],v22 offset:19720
v_mfma_f32_16x16x32_bf16 a[28:31],a[184:187],a[224:227],a[28:31]
v_mfma_f32_16x16x32_bf16 a[28:31],a[188:191],a[228:231],a[28:31]
v_mfma_f32_16x16x32_bf16 a[60:63],a[188:191],a[236:239],a[60:63]
v_mfma_f32_16x16x32_bf16 a[60:63],a[184:187],a[232:235],a[60:63]
v_mfma_f32_16x16x32_bf16 a[92:95],a[184:187],a[240:243],a[92:95]
v_mfma_f32_16x16x32_bf16 a[92:95],a[188:191],a[244:247],a[92:95]
v_mfma_f32_16x16x32_bf16 a[124:127],a[188:191],a[252:255],a[124:127]
v_mfma_f32_16x16x32_bf16 a[124:127],a[184:187],a[248:251],a[124:127]
s_mov_b32 s53,3
.p2align 5
L_qp_%=:
s_waitcnt lgkmcnt(1)
v_mfma_f32_16x16x32_bf16 v[128:131],a[128:131],a[192:195],v[128:131]
s_add_u32 m0,s58,0
s_mov_b32 s86,s87
ds_read_b64_tr_b16 a[160:161],v22 offset:1536
v_mfma_f32_16x16x32_bf16 v[128:131],a[132:135],a[196:199],v[128:131]
s_add_u32 s60,s60,32768
buffer_load_dwordx4 v56,s[32:35],s56 offen lds
v_mfma_f32_16x16x32_bf16 v[136:139],a[132:135],a[204:207],v[136:139]
s_cmp_eq_u32 s60,131072
s_cselect_b32 s60,32768,s60
ds_read_b64_tr_b16 a[162:163],v22 offset:3584
v_mfma_f32_16x16x32_bf16 v[136:139],a[128:131],a[200:203],v[136:139]
s_load_dword s86,s[84:85],0x0 glc
s_add_u32 m0,s58,1024
ds_read_b64_tr_b16 a[164:165],v22 offset:17920
v_mfma_f32_16x16x32_bf16 v[144:147],a[128:131],a[208:211],v[144:147]
s_mov_b32 s90,L_att0x_%=-L_pc_%=+128
ds_read_b64_tr_b16 a[166:167],v22 offset:19968
v_xor_b32 v20,131072,v20
v_mfma_f32_16x16x32_bf16 v[144:147],a[132:135],a[212:215],v[144:147]
s_sub_u32 s53,s53,1
ds_read_b64_tr_b16 a[168:169],v22 offset:1544
v_xor_b32 v21,131072,v21
v_mfma_f32_16x16x32_bf16 v[152:155],a[132:135],a[220:223],v[152:155]
buffer_load_dwordx4 v57,s[32:35],s56 offen lds
v_mfma_f32_16x16x32_bf16 v[152:155],a[128:131],a[216:219],v[152:155]
ds_read_b64_tr_b16 a[170:171],v22 offset:3592
v_mfma_f32_16x16x32_bf16 v[132:135],a[136:139],a[192:195],v[132:135]
ds_read_b64_tr_b16 a[172:173],v22 offset:17928
v_mfma_f32_16x16x32_bf16 v[132:135],a[140:143],a[196:199],v[132:135]
s_add_u32 m0,s58,2048
ds_read_b64_tr_b16 a[174:175],v22 offset:19976
v_mfma_f32_16x16x32_bf16 v[140:143],a[140:143],a[204:207],v[140:143]
ds_read_b64_tr_b16 a[176:177],v22 offset:1792
v_mfma_f32_16x16x32_bf16 v[140:143],a[136:139],a[200:203],v[140:143]
ds_read_b64_tr_b16 a[178:179],v22 offset:3840
v_mfma_f32_16x16x32_bf16 v[148:151],a[136:139],a[208:211],v[148:151]
buffer_load_dwordx4 v58,s[32:35],s56 offen lds
v_mfma_f32_16x16x32_bf16 v[148:151],a[140:143],a[212:215],v[148:151]
ds_read_b64_tr_b16 a[180:181],v22 offset:18176
v_mfma_f32_16x16x32_bf16 v[156:159],a[140:143],a[220:223],v[156:159]
s_cmp_eq_u32 s86,s87
s_addc_u32 s79,s79,0
s_add_u32 m0,s58,3072
v_mfma_f32_16x16x32_bf16 v[156:159],a[136:139],a[216:219],v[156:159]
ds_read_b64_tr_b16 a[182:183],v22 offset:20224
v_mfma_f32_16x16x32_bf16 v[160:163],a[144:147],a[192:195],v[160:163]
ds_read_b64_tr_b16 a[184:185],v22 offset:1800
v_mfma_f32_16x16x32_bf16 v[160:163],a[148:151],a[196:199],v[160:163]
s_cmp_eq_u32 s86,s87
s_addc_u32 s79,s79,0
ds_read_b64_tr_b16 a[186:187],v22 offset:3848
v_mfma_f32_16x16x32_bf16 v[168:171],a[148:151],a[204:207],v[168:171]
buffer_load_dwordx4 v59,s[32:35],s56 offen lds
v_mfma_f32_16x16x32_bf16 v[168:171],a[144:147],a[200:203],v[168:171]
ds_read_b64_tr_b16 a[188:189],v22 offset:18184
v_mfma_f32_16x16x32_bf16 v[176:179],a[144:147],a[208:211],v[176:179]
s_cmp_eq_u32 s86,s87
s_addc_u32 s79,s79,0
s_add_u32 m0,s58,4096
v_mfma_f32_16x16x32_bf16 v[176:179],a[148:151],a[212:215],v[176:179]
ds_read_b64_tr_b16 a[190:191],v22 offset:20232
v_mfma_f32_16x16x32_bf16 v[184:187],a[148:151],a[220:223],v[184:187]
ds_read_b64_tr_b16 a[128:129],v22
v_mfma_f32_16x16x32_bf16 v[184:187],a[144:147],a[216:219],v[184:187]
s_cmp_eq_u32 s86,s87
s_addc_u32 s79,s79,0
ds_read_b64_tr_b16 a[130:131],v22 offset:2048
v_mfma_f32_16x16x32_bf16 v[164:167],a[152:155],a[192:195],v[164:167]
s_waitcnt lgkmcnt(5)
buffer_load_dwordx4 v60,s[32:35],s56 offen lds
v_mfma_f32_16x16x32_bf16 v[164:167],a[156:159],a[196:199],v[164:167]
s_add_u32 m0,s58,5120
ds_read_b64_tr_b16 a[132:133],v22 offset:16384
v_mfma_f32_16x16x32_bf16 v[172:175],a[156:159],a[204:207],v[172:175]
s_cmp_eq_u32 s86,s87
s_addc_u32 s79,s79,0
ds_read_b64_tr_b16 a[134:135],v22 offset:18432
v_mfma_f32_16x16x32_bf16 v[172:175],a[152:155],a[200:203],v[172:175]
ds_read_b64_tr_b16 a[136:137],v22 offset:8
v_mfma_f32_16x16x32_bf16 v[180:183],a[152:155],a[208:211],v[180:183]
buffer_load_dwordx4 v61,s[32:35],s56 offen lds
v_mfma_f32_16x16x32_bf16 v[180:183],a[156:159],a[212:215],v[180:183]
s_cmp_eq_u32 s86,s87
s_addc_u32 s79,s79,0
ds_read_b64_tr_b16 a[138:139],v22 offset:2056
v_mfma_f32_16x16x32_bf16 v[188:191],a[156:159],a[220:223],v[188:191]
ds_read_b64_tr_b16 a[140:141],v22 offset:16392
v_mfma_f32_16x16x32_bf16 v[188:191],a[152:155],a[216:219],v[188:191]
s_add_u32 m0,s58,6144
ds_read_b64_tr_b16 a[142:143],v22 offset:18440
v_mfma_f32_16x16x32_bf16 v[192:195],a[160:163],a[192:195],v[192:195]
s_cmp_eq_u32 s86,s87
s_addc_u32 s79,s79,0
ds_read_b64_tr_b16 a[144:145],v22 offset:256
v_mfma_f32_16x16x32_bf16 v[192:195],a[164:167],a[196:199],v[192:195]
ds_read_b64_tr_b16 a[146:147],v22 offset:2304
v_mfma_f32_16x16x32_bf16 v[200:203],a[164:167],a[204:207],v[200:203]
buffer_load_dwordx4 v62,s[32:35],s56 offen lds
v_mfma_f32_16x16x32_bf16 v[200:203],a[160:163],a[200:203],v[200:203]
s_cmp_eq_u32 s86,s87
s_addc_u32 s79,s79,0
ds_read_b64_tr_b16 a[148:149],v22 offset:16640
v_mfma_f32_16x16x32_bf16 v[208:211],a[160:163],a[208:211],v[208:211]
s_add_u32 m0,s58,7168
s_add_u32 s58,s58,32768
ds_read_b64_tr_b16 a[150:151],v22 offset:18688
v_mfma_f32_16x16x32_bf16 v[208:211],a[164:167],a[212:215],v[208:211]
s_cmp_ge_u32 s58,131072
s_cselect_b32 s62,98304,0
ds_read_b64_tr_b16 a[152:153],v22 offset:264
v_mfma_f32_16x16x32_bf16 v[216:219],a[164:167],a[220:223],v[216:219]
s_cmp_eq_u32 s86,s87
s_addc_u32 s79,s79,0
ds_read_b64_tr_b16 a[154:155],v22 offset:2312
v_mfma_f32_16x16x32_bf16 v[216:219],a[160:163],a[216:219],v[216:219]
s_sub_u32 s58,s58,s62
buffer_load_dwordx4 v63,s[32:35],s56 offen lds
v_mfma_f32_16x16x32_bf16 v[196:199],a[168:171],a[192:195],v[196:199]
s_add_u32 s56,s56,524288
ds_read_b64_tr_b16 a[156:157],v22 offset:16648
v_mfma_f32_16x16x32_bf16 v[196:199],a[172:175],a[196:199],v[196:199]
s_cmp_eq_u32 s86,s87
s_addc_u32 s79,s79,0
ds_read_b64_tr_b16 a[158:159],v22 offset:18696
v_mfma_f32_16x16x32_bf16 v[204:207],a[172:175],a[204:207],v[204:207]
s_add_u32 m0,s59,0
ds_read_b64_tr_b16 a[160:161],v22 offset:512
v_mfma_f32_16x16x32_bf16 v[204:207],a[168:171],a[200:203],v[204:207]
ds_read_b64_tr_b16 a[162:163],v22 offset:2560
v_mfma_f32_16x16x32_bf16 v[212:215],a[168:171],a[208:211],v[212:215]
s_cmp_eq_u32 s86,s87
s_addc_u32 s79,s79,0
ds_read_b64_tr_b16 a[164:165],v22 offset:16896
v_mfma_f32_16x16x32_bf16 v[212:215],a[172:175],a[212:215],v[212:215]
buffer_load_dwordx4 v48,s[28:31],s57 offen lds
v_mfma_f32_16x16x32_bf16 v[220:223],a[172:175],a[220:223],v[220:223]
ds_read_b64_tr_b16 a[166:167],v22 offset:18944
v_mfma_f32_16x16x32_bf16 v[220:223],a[168:171],a[216:219],v[220:223]
s_cmp_eq_u32 s86,s87
s_addc_u32 s79,s79,0
ds_read_b64_tr_b16 a[168:169],v22 offset:520
v_mfma_f32_16x16x32_bf16 v[224:227],a[176:179],a[192:195],v[224:227]
ds_read_b64_tr_b16 a[170:171],v22 offset:2568
v_mfma_f32_16x16x32_bf16 v[224:227],a[180:183],a[196:199],v[224:227]
ds_read_b64_tr_b16 a[172:173],v22 offset:16904
v_mfma_f32_16x16x32_bf16 v[232:235],a[180:183],a[204:207],v[232:235]
s_cmp_eq_u32 s86,s87
s_addc_u32 s79,s79,0
ds_read_b64_tr_b16 a[174:175],v22 offset:18952
v_mfma_f32_16x16x32_bf16 v[232:235],a[176:179],a[200:203],v[232:235]
buffer_load_dwordx4 v49,s[28:31],s57 offen offset:1024 lds
v_mfma_f32_16x16x32_bf16 v[240:243],a[176:179],a[208:211],v[240:243]
v_mfma_f32_16x16x32_bf16 v[240:243],a[180:183],a[212:215],v[240:243]
s_cmp_eq_u32 s86,s87
s_addc_u32 s79,s79,0
v_mfma_f32_16x16x32_bf16 v[248:251],a[180:183],a[220:223],v[248:251]
ds_read_b64_tr_b16 a[180:181],v22 offset:17152
v_mfma_f32_16x16x32_bf16 v[248:251],a[176:179],a[216:219],v[248:251]
s_waitcnt lgkmcnt(4)
ds_read_b64_tr_b16 a[176:177],v22 offset:768
v_mfma_f32_16x16x32_bf16 v[228:231],a[184:187],a[192:195],v[228:231]
s_cmp_eq_u32 s86,s87
s_addc_u32 s79,s79,0
ds_read_b64_tr_b16 a[178:179],v22 offset:2816
v_mfma_f32_16x16x32_bf16 v[228:231],a[188:191],a[196:199],v[228:231]
buffer_load_dwordx4 v50,s[28:31],s57 offen offset:2048 lds
v_mfma_f32_16x16x32_bf16 v[236:239],a[188:191],a[204:207],v[236:239]
ds_read_b64_tr_b16 a[182:183],v22 offset:19200
v_mfma_f32_16x16x32_bf16 v[236:239],a[184:187],a[200:203],v[236:239]
s_cmp_eq_u32 s86,s87
s_addc_u32 s79,s79,0
v_mfma_f32_16x16x32_bf16 v[244:247],a[184:187],a[208:211],v[244:247]
v_mfma_f32_16x16x32_bf16 v[244:247],a[188:191],a[212:215],v[244:247]
buffer_load_dwordx4 v51,s[28:31],s57 offen offset:3072 lds
v_mfma_f32_16x16x32_bf16 v[252:255],a[188:191],a[220:223],v[252:255]
ds_read_b64_tr_b16 a[188:189],v22 offset:17160
v_mfma_f32_16x16x32_bf16 v[252:255],a[184:187],a[216:219],v[252:255]
ds_read_b64_tr_b16 a[184:185],v22 offset:776
v_mfma_f32_16x16x32_bf16 a[0:3],a[128:131],a[192:195],a[0:3]
s_add_u32 m0,s59,4096
s_xor_b32 s59,s59,131072
ds_read_b64_tr_b16 a[186:187],v22 offset:2824
v_mfma_f32_16x16x32_bf16 a[0:3],a[132:135],a[196:199],a[0:3]
ds_read_b64_tr_b16 a[190:191],v22 offset:19208
v_add_u32 v22,s60,v23
v_mfma_f32_16x16x32_bf16 a[32:35],a[132:135],a[204:207],a[32:35]
s_add_u32 s60,s60,32768
s_cmp_eq_u32 s60,131072
s_cselect_b32 s60,32768,s60
v_mfma_f32_16x16x32_bf16 a[32:35],a[128:131],a[200:203],a[32:35]
buffer_load_dwordx4 v52,s[28:31],s57 offen lds
v_mfma_f32_16x16x32_bf16 a[64:67],a[128:131],a[208:211],a[64:67]
v_mfma_f32_16x16x32_bf16 a[64:67],a[132:135],a[212:215],a[64:67]
v_mfma_f32_16x16x32_bf16 a[96:99],a[132:135],a[220:223],a[96:99]
v_mfma_f32_16x16x32_bf16 a[96:99],a[128:131],a[216:219],a[96:99]
v_mfma_f32_16x16x32_bf16 a[4:7],a[136:139],a[192:195],a[4:7]
buffer_load_dwordx4 v53,s[28:31],s57 offen offset:1024 lds
v_mfma_f32_16x16x32_bf16 a[4:7],a[140:143],a[196:199],a[4:7]
v_mfma_f32_16x16x32_bf16 a[36:39],a[140:143],a[204:207],a[36:39]
v_mfma_f32_16x16x32_bf16 a[36:39],a[136:139],a[200:203],a[36:39]
v_mfma_f32_16x16x32_bf16 a[68:71],a[136:139],a[208:211],a[68:71]
v_mfma_f32_16x16x32_bf16 a[68:71],a[140:143],a[212:215],a[68:71]
v_mfma_f32_16x16x32_bf16 a[100:103],a[140:143],a[220:223],a[100:103]
buffer_load_dwordx4 v54,s[28:31],s57 offen offset:2048 lds
v_mfma_f32_16x16x32_bf16 a[100:103],a[136:139],a[216:219],a[100:103]
v_mfma_f32_16x16x32_bf16 a[8:11],a[144:147],a[192:195],a[8:11]
)A"
R"A(v_mfma_f32_16x16x32_bf16 a[8:11],a[148:151],a[196:199],a[8:11]
v_mfma_f32_16x16x32_bf16 a[40:43],a[148:151],a[204:207],a[40:43]
v_mfma_f32_16x16x32_bf16 a[40:43],a[144:147],a[200:203],a[40:43]
buffer_load_dwordx4 v55,s[28:31],s57 offen offset:3072 lds
v_mfma_f32_16x16x32_bf16 a[72:75],a[144:147],a[208:211],a[72:75]
s_add_u32 s57,s57,128
v_mfma_f32_16x16x32_bf16 a[72:75],a[148:151],a[212:215],a[72:75]
v_mfma_f32_16x16x32_bf16 a[104:107],a[148:151],a[220:223],a[104:107]
v_mfma_f32_16x16x32_bf16 a[104:107],a[144:147],a[216:219],a[104:107]
v_mfma_f32_16x16x32_bf16 a[12:15],a[152:155],a[192:195],a[12:15]
v_mfma_f32_16x16x32_bf16 a[12:15],a[156:159],a[196:199],a[12:15]
v_mfma_f32_16x16x32_bf16 a[44:47],a[156:159],a[204:207],a[44:47]
buffer_load_dword v16,v15,s[72:75],s83 offen
v_mfma_f32_16x16x32_bf16 a[44:47],a[152:155],a[200:203],a[44:47]
s_add_u32 s83,s83,896
v_mfma_f32_16x16x32_bf16 a[76:79],a[152:155],a[208:211],a[76:79]
v_mfma_f32_16x16x32_bf16 a[76:79],a[156:159],a[212:215],a[76:79]
v_mfma_f32_16x16x32_bf16 a[108:111],a[156:159],a[220:223],a[108:111]
v_mfma_f32_16x16x32_bf16 a[108:111],a[152:155],a[216:219],a[108:111]
s_waitcnt vmcnt(17) lgkmcnt(0)
v_mfma_f32_16x16x32_bf16 a[16:19],a[160:163],a[192:195],a[16:19]
s_barrier
ds_read_b128 a[224:227],v20
v_mfma_f32_16x16x32_bf16 a[16:19],a[164:167],a[196:199],a[16:19]
ds_read_b64_tr_b16 a[128:129],v22 offset:1024
v_mfma_f32_16x16x32_bf16 a[48:51],a[164:167],a[204:207],a[48:51]
ds_read_b64_tr_b16 a[130:131],v22 offset:3072
v_mfma_f32_16x16x32_bf16 a[48:51],a[160:163],a[200:203],a[48:51]
ds_read_b128 a[228:231],v21
v_mfma_f32_16x16x32_bf16 a[80:83],a[160:163],a[208:211],a[80:83]
ds_read_b64_tr_b16 a[132:133],v22 offset:17408
v_mfma_f32_16x16x32_bf16 a[80:83],a[164:167],a[212:215],a[80:83]
ds_read_b64_tr_b16 a[134:135],v22 offset:19456
v_mfma_f32_16x16x32_bf16 a[112:115],a[164:167],a[220:223],a[112:115]
ds_read_b128 a[232:235],v20 offset:2048
v_mfma_f32_16x16x32_bf16 a[112:115],a[160:163],a[216:219],a[112:115]
ds_read_b128 a[236:239],v21 offset:2048
v_mfma_f32_16x16x32_bf16 a[20:23],a[168:171],a[192:195],a[20:23]
ds_read_b128 a[240:243],v20 offset:4096
v_mfma_f32_16x16x32_bf16 a[20:23],a[172:175],a[196:199],a[20:23]
ds_read_b128 a[244:247],v21 offset:4096
v_mfma_f32_16x16x32_bf16 a[52:55],a[172:175],a[204:207],a[52:55]
ds_read_b128 a[248:251],v20 offset:6144
v_xor_b32 v20,131072,v20
v_mfma_f32_16x16x32_bf16 a[52:55],a[168:171],a[200:203],a[52:55]
ds_read_b128 a[252:255],v21 offset:6144
v_xor_b32 v21,131072,v21
v_mfma_f32_16x16x32_bf16 a[84:87],a[168:171],a[208:211],a[84:87]
ds_read_b64_tr_b16 a[136:137],v22 offset:1032
v_mfma_f32_16x16x32_bf16 a[84:87],a[172:175],a[212:215],a[84:87]
ds_read_b64_tr_b16 a[138:139],v22 offset:3080
v_mfma_f32_16x16x32_bf16 a[116:119],a[172:175],a[220:223],a[116:119]
ds_read_b64_tr_b16 a[140:141],v22 offset:17416
v_mfma_f32_16x16x32_bf16 a[116:119],a[168:171],a[216:219],a[116:119]
ds_read_b64_tr_b16 a[142:143],v22 offset:19464
v_mfma_f32_16x16x32_bf16 a[24:27],a[176:179],a[192:195],a[24:27]
ds_read_b64_tr_b16 a[144:145],v22 offset:1280
v_mfma_f32_16x16x32_bf16 a[24:27],a[180:183],a[196:199],a[24:27]
ds_read_b64_tr_b16 a[146:147],v22 offset:3328
v_mfma_f32_16x16x32_bf16 a[56:59],a[180:183],a[204:207],a[56:59]
ds_read_b64_tr_b16 a[148:149],v22 offset:17664
v_mfma_f32_16x16x32_bf16 a[56:59],a[176:179],a[200:203],a[56:59]
ds_read_b64_tr_b16 a[150:151],v22 offset:19712
v_mfma_f32_16x16x32_bf16 a[88:91],a[176:179],a[208:211],a[88:91]
ds_read_b64_tr_b16 a[152:153],v22 offset:1288
v_mfma_f32_16x16x32_bf16 a[88:91],a[180:183],a[212:215],a[88:91]
ds_read_b64_tr_b16 a[154:155],v22 offset:3336
v_mfma_f32_16x16x32_bf16 a[120:123],a[180:183],a[220:223],a[120:123]
ds_read_b64_tr_b16 a[156:157],v22 offset:17672
v_mfma_f32_16x16x32_bf16 a[120:123],a[176:179],a[216:219],a[120:123]
ds_read_b64_tr_b16 a[158:159],v22 offset:19720
v_mfma_f32_16x16x32_bf16 a[28:31],a[184:187],a[192:195],a[28:31]
ds_read_b64_tr_b16 a[160:161],v22 offset:1536
v_mfma_f32_16x16x32_bf16 a[28:31],a[188:191],a[196:199],a[28:31]
ds_read_b64_tr_b16 a[162:163],v22 offset:3584
v_mfma_f32_16x16x32_bf16 a[60:63],a[188:191],a[204:207],a[60:63]
ds_read_b64_tr_b16 a[164:165],v22 offset:17920
v_mfma_f32_16x16x32_bf16 a[60:63],a[184:187],a[200:203],a[60:63]
ds_read_b64_tr_b16 a[166:167],v22 offset:19968
v_mfma_f32_16x16x32_bf16 a[92:95],a[184:187],a[208:211],a[92:95]
ds_read_b64_tr_b16 a[168:169],v22 offset:1544
v_mfma_f32_16x16x32_bf16 a[92:95],a[188:191],a[212:215],a[92:95]
ds_read_b64_tr_b16 a[170:171],v22 offset:3592
v_mfma_f32_16x16x32_bf16 a[124:127],a[188:191],a[220:223],a[124:127]
s_add_u32 m0,s58,0
ds_read_b64_tr_b16 a[172:173],v22 offset:17928
v_mfma_f32_16x16x32_bf16 a[124:127],a[184:187],a[216:219],a[124:127]
s_waitcnt lgkmcnt(7)
ds_read_b64_tr_b16 a[174:175],v22 offset:19976
v_mfma_f32_16x16x32_bf16 v[128:131],a[128:131],a[224:227],v[128:131]
s_mov_b32 s86,s87
ds_read_b64_tr_b16 a[176:177],v22 offset:1792
v_mfma_f32_16x16x32_bf16 v[128:131],a[132:135],a[228:231],v[128:131]
buffer_load_dwordx4 v56,s[32:35],s56 offen lds
v_mfma_f32_16x16x32_bf16 v[136:139],a[132:135],a[236:239],v[136:139]
ds_read_b64_tr_b16 a[178:179],v22 offset:3840
v_mfma_f32_16x16x32_bf16 v[136:139],a[128:131],a[232:235],v[136:139]
s_load_dword s86,s[84:85],0x2000 glc
s_add_u32 m0,s58,1024
ds_read_b64_tr_b16 a[180:181],v22 offset:18176
v_mfma_f32_16x16x32_bf16 v[144:147],a[128:131],a[240:243],v[144:147]
s_add_u32 s84,s84,256
ds_read_b64_tr_b16 a[182:183],v22 offset:20224
v_mfma_f32_16x16x32_bf16 v[144:147],a[132:135],a[244:247],v[144:147]
ds_read_b64_tr_b16 a[184:185],v22 offset:1800
v_mfma_f32_16x16x32_bf16 v[152:155],a[132:135],a[252:255],v[152:155]
buffer_load_dwordx4 v57,s[32:35],s56 offen lds
v_mfma_f32_16x16x32_bf16 v[152:155],a[128:131],a[248:251],v[152:155]
ds_read_b64_tr_b16 a[186:187],v22 offset:3848
v_mfma_f32_16x16x32_bf16 v[132:135],a[136:139],a[224:227],v[132:135]
ds_read_b64_tr_b16 a[188:189],v22 offset:18184
v_mfma_f32_16x16x32_bf16 v[132:135],a[140:143],a[228:231],v[132:135]
s_add_u32 m0,s58,2048
ds_read_b64_tr_b16 a[190:191],v22 offset:20232
v_mfma_f32_16x16x32_bf16 v[140:143],a[140:143],a[236:239],v[140:143]
ds_read_b64_tr_b16 a[128:129],v22
v_mfma_f32_16x16x32_bf16 v[140:143],a[136:139],a[232:235],v[140:143]
ds_read_b64_tr_b16 a[130:131],v22 offset:2048
v_mfma_f32_16x16x32_bf16 v[148:151],a[136:139],a[240:243],v[148:151]
buffer_load_dwordx4 v58,s[32:35],s56 offen lds
v_mfma_f32_16x16x32_bf16 v[148:151],a[140:143],a[244:247],v[148:151]
ds_read_b64_tr_b16 a[132:133],v22 offset:16384
v_mfma_f32_16x16x32_bf16 v[156:159],a[140:143],a[252:255],v[156:159]
s_cmp_eq_u32 s86,s87
s_subb_u32 s79,s79,0
s_add_u32 m0,s58,3072
v_mfma_f32_16x16x32_bf16 v[156:159],a[136:139],a[248:251],v[156:159]
ds_read_b64_tr_b16 a[134:135],v22 offset:18432
v_mfma_f32_16x16x32_bf16 v[160:163],a[144:147],a[224:227],v[160:163]
ds_read_b64_tr_b16 a[136:137],v22 offset:8
v_mfma_f32_16x16x32_bf16 v[160:163],a[148:151],a[228:231],v[160:163]
s_cmp_eq_u32 s86,s87
s_subb_u32 s79,s79,0
ds_read_b64_tr_b16 a[138:139],v22 offset:2056
v_mfma_f32_16x16x32_bf16 v[168:171],a[148:151],a[236:239],v[168:171]
buffer_load_dwordx4 v59,s[32:35],s56 offen lds
v_mfma_f32_16x16x32_bf16 v[168:171],a[144:147],a[232:235],v[168:171]
ds_read_b64_tr_b16 a[140:141],v22 offset:16392
v_mfma_f32_16x16x32_bf16 v[176:179],a[144:147],a[240:243],v[176:179]
s_cmp_eq_u32 s86,s87
s_subb_u32 s79,s79,0
s_add_u32 m0,s58,4096
v_mfma_f32_16x16x32_bf16 v[176:179],a[148:151],a[244:247],v[176:179]
ds_read_b64_tr_b16 a[142:143],v22 offset:18440
v_mfma_f32_16x16x32_bf16 v[184:187],a[148:151],a[252:255],v[184:187]
ds_read_b64_tr_b16 a[148:149],v22 offset:16640
v_mfma_f32_16x16x32_bf16 v[184:187],a[144:147],a[248:251],v[184:187]
s_cmp_eq_u32 s86,s87
s_subb_u32 s79,s79,0
ds_read_b64_tr_b16 a[144:145],v22 offset:256
v_mfma_f32_16x16x32_bf16 v[164:167],a[152:155],a[224:227],v[164:167]
buffer_load_dwordx4 v60,s[32:35],s56 offen lds
v_mfma_f32_16x16x32_bf16 v[164:167],a[156:159],a[228:231],v[164:167]
s_add_u32 m0,s58,5120
ds_read_b64_tr_b16 a[146:147],v22 offset:2304
v_mfma_f32_16x16x32_bf16 v[172:175],a[156:159],a[236:239],v[172:175]
s_cmp_eq_u32 s86,s87
s_subb_u32 s79,s79,0
ds_read_b64_tr_b16 a[150:151],v22 offset:18688
v_mfma_f32_16x16x32_bf16 v[172:175],a[152:155],a[232:235],v[172:175]
v_mfma_f32_16x16x32_bf16 v[180:183],a[152:155],a[240:243],v[180:183]
buffer_load_dwordx4 v61,s[32:35],s56 offen lds
v_mfma_f32_16x16x32_bf16 v[180:183],a[156:159],a[244:247],v[180:183]
s_cmp_eq_u32 s86,s87
s_subb_u32 s79,s79,0
v_mfma_f32_16x16x32_bf16 v[188:191],a[156:159],a[252:255],v[188:191]
ds_read_b64_tr_b16 a[156:157],v22 offset:16648
v_mfma_f32_16x16x32_bf16 v[188:191],a[152:155],a[248:251],v[188:191]
s_add_u32 m0,s58,6144
ds_read_b64_tr_b16 a[152:153],v22 offset:264
s_waitcnt lgkmcnt(4)
v_mfma_f32_16x16x32_bf16 v[192:195],a[160:163],a[224:227],v[192:195]
s_cmp_eq_u32 s86,s87
s_subb_u32 s79,s79,0
ds_read_b64_tr_b16 a[154:155],v22 offset:2312
v_mfma_f32_16x16x32_bf16 v[192:195],a[164:167],a[228:231],v[192:195]
ds_read_b64_tr_b16 a[158:159],v22 offset:18696
v_mfma_f32_16x16x32_bf16 v[200:203],a[164:167],a[236:239],v[200:203]
buffer_load_dwordx4 v62,s[32:35],s56 offen lds
v_mfma_f32_16x16x32_bf16 v[200:203],a[160:163],a[232:235],v[200:203]
s_cmp_eq_u32 s86,s87
s_subb_u32 s79,s79,0
v_mfma_f32_16x16x32_bf16 v[208:211],a[160:163],a[240:243],v[208:211]
s_add_u32 m0,s58,7168
s_add_u32 s58,s58,32768
v_mfma_f32_16x16x32_bf16 v[208:211],a[164:167],a[244:247],v[208:211]
s_cmp_ge_u32 s58,131072
s_cselect_b32 s62,98304,0
s_sub_u32 s58,s58,s62
v_mfma_f32_16x16x32_bf16 v[216:219],a[164:167],a[252:255],v[216:219]
s_cmp_eq_u32 s86,s87
s_subb_u32 s79,s79,0
ds_read_b64_tr_b16 a[164:165],v22 offset:16896
v_mfma_f32_16x16x32_bf16 v[216:219],a[160:163],a[248:251],v[216:219]
buffer_load_dwordx4 v63,s[32:35],s56 offen lds
v_mfma_f32_16x16x32_bf16 v[196:199],a[168:171],a[224:227],v[196:199]
s_add_u32 s56,s56,524288
ds_read_b64_tr_b16 a[160:161],v22 offset:512
v_mfma_f32_16x16x32_bf16 v[196:199],a[172:175],a[228:231],v[196:199]
s_cmp_eq_u32 s86,s87
s_subb_u32 s79,s79,0
ds_read_b64_tr_b16 a[162:163],v22 offset:2560
v_mfma_f32_16x16x32_bf16 v[204:207],a[172:175],a[236:239],v[204:207]
s_add_u32 m0,s59,0
ds_read_b64_tr_b16 a[166:167],v22 offset:18944
v_mfma_f32_16x16x32_bf16 v[204:207],a[168:171],a[232:235],v[204:207]
v_mfma_f32_16x16x32_bf16 v[212:215],a[168:171],a[240:243],v[212:215]
s_cmp_eq_u32 s86,s87
s_subb_u32 s79,s79,0
v_mfma_f32_16x16x32_bf16 v[212:215],a[172:175],a[244:247],v[212:215]
buffer_load_dwordx4 v48,s[28:31],s57 offen lds
v_mfma_f32_16x16x32_bf16 v[220:223],a[172:175],a[252:255],v[220:223]
ds_read_b64_tr_b16 a[172:173],v22 offset:16904
v_mfma_f32_16x16x32_bf16 v[220:223],a[168:171],a[248:251],v[220:223]
s_cmp_eq_u32 s86,s87
s_subb_u32 s79,s79,0
ds_read_b64_tr_b16 a[168:169],v22 offset:520
v_mfma_f32_16x16x32_bf16 v[224:227],a[176:179],a[224:227],v[224:227]
ds_read_b64_tr_b16 a[170:171],v22 offset:2568
v_mfma_f32_16x16x32_bf16 v[224:227],a[180:183],a[228:231],v[224:227]
ds_read_b64_tr_b16 a[174:175],v22 offset:18952
v_mfma_f32_16x16x32_bf16 v[232:235],a[180:183],a[236:239],v[232:235]
s_cmp_eq_u32 s86,s87
s_subb_u32 s79,s79,0
v_mfma_f32_16x16x32_bf16 v[232:235],a[176:179],a[232:235],v[232:235]
buffer_load_dwordx4 v49,s[28:31],s57 offen offset:1024 lds
v_mfma_f32_16x16x32_bf16 v[240:243],a[176:179],a[240:243],v[240:243]
v_mfma_f32_16x16x32_bf16 v[240:243],a[180:183],a[244:247],v[240:243]
s_cmp_eq_u32 s86,s87
s_subb_u32 s79,s79,0
v_mfma_f32_16x16x32_bf16 v[248:251],a[180:183],a[252:255],v[248:251]
ds_read_b64_tr_b16 a[180:181],v22 offset:17152
v_mfma_f32_16x16x32_bf16 v[248:251],a[176:179],a[248:251],v[248:251]
ds_read_b64_tr_b16 a[176:177],v22 offset:768
v_mfma_f32_16x16x32_bf16 v[228:231],a[184:187],a[224:227],v[228:231]
s_cmp_eq_u32 s86,s87
s_subb_u32 s79,s79,0
ds_read_b64_tr_b16 a[178:179],v22 offset:2816
v_mfma_f32_16x16x32_bf16 v[228:231],a[188:191],a[228:231],v[228:231]
buffer_load_dwordx4 v50,s[28:31],s57 offen offset:2048 lds
v_mfma_f32_16x16x32_bf16 v[236:239],a[188:191],a[236:239],v[236:239]
)A"
R"A(ds_read_b64_tr_b16 a[182:183],v22 offset:19200
v_mfma_f32_16x16x32_bf16 v[236:239],a[184:187],a[232:235],v[236:239]
s_cmp_eq_u32 s86,s87
s_subb_u32 s79,s79,0
v_mfma_f32_16x16x32_bf16 v[244:247],a[184:187],a[240:243],v[244:247]
v_mfma_f32_16x16x32_bf16 v[244:247],a[188:191],a[244:247],v[244:247]
buffer_load_dwordx4 v51,s[28:31],s57 offen offset:3072 lds
v_mfma_f32_16x16x32_bf16 v[252:255],a[188:191],a[252:255],v[252:255]
ds_read_b64_tr_b16 a[188:189],v22 offset:17160
v_mfma_f32_16x16x32_bf16 v[252:255],a[184:187],a[248:251],v[252:255]
ds_read_b64_tr_b16 a[184:185],v22 offset:776
v_mfma_f32_16x16x32_bf16 a[0:3],a[128:131],a[224:227],a[0:3]
s_add_u32 m0,s59,4096
s_xor_b32 s59,s59,131072
ds_read_b64_tr_b16 a[186:187],v22 offset:2824
v_mfma_f32_16x16x32_bf16 a[0:3],a[132:135],a[228:231],a[0:3]
ds_read_b64_tr_b16 a[190:191],v22 offset:19208
v_add_u32 v22,s60,v23
v_mfma_f32_16x16x32_bf16 a[32:35],a[132:135],a[236:239],a[32:35]
v_mfma_f32_16x16x32_bf16 a[32:35],a[128:131],a[232:235],a[32:35]
buffer_load_dwordx4 v52,s[28:31],s57 offen lds
v_mfma_f32_16x16x32_bf16 a[64:67],a[128:131],a[240:243],a[64:67]
v_mfma_f32_16x16x32_bf16 a[64:67],a[132:135],a[244:247],a[64:67]
v_mfma_f32_16x16x32_bf16 a[96:99],a[132:135],a[252:255],a[96:99]
v_mfma_f32_16x16x32_bf16 a[96:99],a[128:131],a[248:251],a[96:99]
v_mfma_f32_16x16x32_bf16 a[4:7],a[136:139],a[224:227],a[4:7]
buffer_load_dwordx4 v53,s[28:31],s57 offen offset:1024 lds
v_mfma_f32_16x16x32_bf16 a[4:7],a[140:143],a[228:231],a[4:7]
v_mfma_f32_16x16x32_bf16 a[36:39],a[140:143],a[236:239],a[36:39]
v_mfma_f32_16x16x32_bf16 a[36:39],a[136:139],a[232:235],a[36:39]
v_mfma_f32_16x16x32_bf16 a[68:71],a[136:139],a[240:243],a[68:71]
v_mfma_f32_16x16x32_bf16 a[68:71],a[140:143],a[244:247],a[68:71]
v_mfma_f32_16x16x32_bf16 a[100:103],a[140:143],a[252:255],a[100:103]
buffer_load_dwordx4 v54,s[28:31],s57 offen offset:2048 lds
v_mfma_f32_16x16x32_bf16 a[100:103],a[136:139],a[248:251],a[100:103]
s_waitcnt lgkmcnt(0)
v_mfma_f32_16x16x32_bf16 a[8:11],a[144:147],a[224:227],a[8:11]
v_mfma_f32_16x16x32_bf16 a[8:11],a[148:151],a[228:231],a[8:11]
v_mfma_f32_16x16x32_bf16 a[40:43],a[148:151],a[236:239],a[40:43]
v_mfma_f32_16x16x32_bf16 a[40:43],a[144:147],a[232:235],a[40:43]
buffer_load_dwordx4 v55,s[28:31],s57 offen offset:3072 lds
v_mfma_f32_16x16x32_bf16 a[72:75],a[144:147],a[240:243],a[72:75]
s_add_u32 s57,s57,128
v_mfma_f32_16x16x32_bf16 a[72:75],a[148:151],a[244:247],a[72:75]
v_mfma_f32_16x16x32_bf16 a[104:107],a[148:151],a[252:255],a[104:107]
v_mfma_f32_16x16x32_bf16 a[104:107],a[144:147],a[248:251],a[104:107]
v_mfma_f32_16x16x32_bf16 a[12:15],a[152:155],a[224:227],a[12:15]
v_mfma_f32_16x16x32_bf16 a[12:15],a[156:159],a[228:231],a[12:15]
v_mfma_f32_16x16x32_bf16 a[44:47],a[156:159],a[236:239],a[44:47]
buffer_load_dword v16,v15,s[72:75],s83 offen
v_mfma_f32_16x16x32_bf16 a[44:47],a[152:155],a[232:235],a[44:47]
s_add_u32 s83,s83,896
v_mfma_f32_16x16x32_bf16 a[76:79],a[152:155],a[240:243],a[76:79]
v_mfma_f32_16x16x32_bf16 a[76:79],a[156:159],a[244:247],a[76:79]
v_mfma_f32_16x16x32_bf16 a[108:111],a[156:159],a[252:255],a[108:111]
v_mfma_f32_16x16x32_bf16 a[108:111],a[152:155],a[248:251],a[108:111]
s_waitcnt vmcnt(17) lgkmcnt(0)
v_mfma_f32_16x16x32_bf16 a[16:19],a[160:163],a[224:227],a[16:19]
s_barrier
ds_read_b128 a[192:195],v20
v_mfma_f32_16x16x32_bf16 a[16:19],a[164:167],a[228:231],a[16:19]
ds_read_b128 a[196:199],v21
v_mfma_f32_16x16x32_bf16 a[48:51],a[164:167],a[236:239],a[48:51]
ds_read_b128 a[200:203],v20 offset:2048
v_mfma_f32_16x16x32_bf16 a[48:51],a[160:163],a[232:235],a[48:51]
ds_read_b128 a[204:207],v21 offset:2048
v_mfma_f32_16x16x32_bf16 a[80:83],a[160:163],a[240:243],a[80:83]
ds_read_b128 a[208:211],v20 offset:4096
v_mfma_f32_16x16x32_bf16 a[80:83],a[164:167],a[244:247],a[80:83]
ds_read_b128 a[212:215],v21 offset:4096
v_mfma_f32_16x16x32_bf16 a[112:115],a[164:167],a[252:255],a[112:115]
buffer_load_dword v26,v24,s[88:91],s93 offen
v_mfma_f32_16x16x32_bf16 a[112:115],a[160:163],a[248:251],a[112:115]
ds_read_b128 a[216:219],v20 offset:6144
v_mfma_f32_16x16x32_bf16 a[20:23],a[168:171],a[224:227],a[20:23]
ds_read_b128 a[220:223],v21 offset:6144
v_mfma_f32_16x16x32_bf16 a[20:23],a[172:175],a[228:231],a[20:23]
ds_read_b64_tr_b16 a[128:129],v22 offset:1024
v_mfma_f32_16x16x32_bf16 a[52:55],a[172:175],a[236:239],a[52:55]
ds_read_b64_tr_b16 a[130:131],v22 offset:3072
v_mfma_f32_16x16x32_bf16 a[52:55],a[168:171],a[232:235],a[52:55]
ds_read_b64_tr_b16 a[132:133],v22 offset:17408
v_mfma_f32_16x16x32_bf16 a[84:87],a[168:171],a[240:243],a[84:87]
ds_read_b64_tr_b16 a[134:135],v22 offset:19456
v_mfma_f32_16x16x32_bf16 a[84:87],a[172:175],a[244:247],a[84:87]
ds_read_b64_tr_b16 a[136:137],v22 offset:1032
v_mfma_f32_16x16x32_bf16 a[116:119],a[172:175],a[252:255],a[116:119]
ds_read_b64_tr_b16 a[138:139],v22 offset:3080
v_mfma_f32_16x16x32_bf16 a[116:119],a[168:171],a[248:251],a[116:119]
ds_read_b64_tr_b16 a[140:141],v22 offset:17416
v_mfma_f32_16x16x32_bf16 a[24:27],a[176:179],a[224:227],a[24:27]
ds_read_b64_tr_b16 a[142:143],v22 offset:19464
v_mfma_f32_16x16x32_bf16 a[24:27],a[180:183],a[228:231],a[24:27]
ds_read_b64_tr_b16 a[144:145],v22 offset:1280
v_mfma_f32_16x16x32_bf16 a[56:59],a[180:183],a[236:239],a[56:59]
ds_read_b64_tr_b16 a[146:147],v22 offset:3328
v_mfma_f32_16x16x32_bf16 a[56:59],a[176:179],a[232:235],a[56:59]
ds_read_b64_tr_b16 a[148:149],v22 offset:17664
v_mfma_f32_16x16x32_bf16 a[88:91],a[176:179],a[240:243],a[88:91]
ds_read_b64_tr_b16 a[150:151],v22 offset:19712
v_mfma_f32_16x16x32_bf16 a[88:91],a[180:183],a[244:247],a[88:91]
ds_read_b64_tr_b16 a[152:153],v22 offset:1288
v_mfma_f32_16x16x32_bf16 a[120:123],a[180:183],a[252:255],a[120:123]
ds_read_b64_tr_b16 a[154:155],v22 offset:3336
v_mfma_f32_16x16x32_bf16 a[120:123],a[176:179],a[248:251],a[120:123]
ds_read_b64_tr_b16 a[156:157],v22 offset:17672
v_mfma_f32_16x16x32_bf16 a[28:31],a[184:187],a[224:227],a[28:31]
ds_read_b64_tr_b16 a[158:159],v22 offset:19720
v_mfma_f32_16x16x32_bf16 a[28:31],a[188:191],a[228:231],a[28:31]
v_mfma_f32_16x16x32_bf16 a[60:63],a[188:191],a[236:239],a[60:63]
v_mfma_f32_16x16x32_bf16 a[60:63],a[184:187],a[232:235],a[60:63]
v_mfma_f32_16x16x32_bf16 a[92:95],a[184:187],a[240:243],a[92:95]
v_mfma_f32_16x16x32_bf16 a[92:95],a[188:191],a[244:247],a[92:95]
v_mfma_f32_16x16x32_bf16 a[124:127],a[188:191],a[252:255],a[124:127]
s_cmp_lg_u32 s53,0
v_mfma_f32_16x16x32_bf16 a[124:127],a[184:187],a[248:251],a[124:127]
s_cbranch_scc1 L_qp_%=
L_qpx_%=:
L_tailseg_%=:
s_waitcnt lgkmcnt(1)
v_mfma_f32_16x16x32_bf16 v[128:131],a[128:131],a[192:195],v[128:131]
s_add_u32 m0,98304,s51
ds_read_b64_tr_b16 a[160:161],v22 offset:1536
v_xor_b32 v20,131072,v20
v_mfma_f32_16x16x32_bf16 v[128:131],a[132:135],a[196:199],v[128:131]
s_add_u32 s54,s54,16384
buffer_load_dwordx4 v4,s[20:23],s47 offen nt lds
v_mfma_f32_16x16x32_bf16 v[136:139],a[132:135],a[204:207],v[136:139]
s_and_b32 s46,s54,0xfffc0000
ds_read_b64_tr_b16 a[162:163],v22 offset:3584
v_xor_b32 v21,131072,v21
v_mfma_f32_16x16x32_bf16 v[136:139],a[128:131],a[200:203],v[136:139]
s_add_u32 s54,s54,16384
ds_read_b64_tr_b16 a[164:165],v22 offset:17920
v_and_b32 v18,48,v6
v_mfma_f32_16x16x32_bf16 v[144:147],a[128:131],a[208:211],v[144:147]
s_mov_b32 s48,-98304
ds_read_b64_tr_b16 a[166:167],v22 offset:19968
v_lshrrev_b32 v19,4,v6
v_mfma_f32_16x16x32_bf16 v[144:147],a[132:135],a[212:215],v[144:147]
s_mov_b32 s60,148
ds_read_b64_tr_b16 a[168:169],v22 offset:1544
v_lshlrev_b32 v18,2,v18
v_mfma_f32_16x16x32_bf16 v[152:155],a[132:135],a[220:223],v[152:155]
s_mul_i32 s83,s41,5
ds_read_b64_tr_b16 a[170:171],v22 offset:3592
v_and_b32 v19,48,v19
v_mfma_f32_16x16x32_bf16 v[152:155],a[128:131],a[216:219],v[152:155]
s_lshl_b32 s43,s40,8
ds_read_b64_tr_b16 a[172:173],v22 offset:17928
v_add_u32 v18,v18,v19
v_mfma_f32_16x16x32_bf16 v[132:135],a[136:139],a[192:195],v[132:135]
s_add_u32 s83,s83,s42
ds_read_b64_tr_b16 a[174:175],v22 offset:19976
v_add_u32 v18,98304,v18
v_mfma_f32_16x16x32_bf16 v[132:135],a[140:143],a[196:199],v[132:135]
s_cmp_gt_i32 s79,0
s_cselect_b32 s86,8192,0
ds_read_b64_tr_b16 a[176:177],v22 offset:1792
v_mfma_f32_16x16x32_bf16 v[140:143],a[140:143],a[204:207],v[140:143]
s_lshr_b32 s79,%1,3
s_mul_i32 s79,s79,7
ds_read_b64_tr_b16 a[178:179],v22 offset:3840
v_mfma_f32_16x16x32_bf16 v[140:143],a[136:139],a[200:203],v[140:143]
s_lshr_b32 s79,s79,5
s_sub_u32 s60,s60,s79
ds_read_b64_tr_b16 a[180:181],v22 offset:18176
v_mfma_f32_16x16x32_bf16 v[148:151],a[136:139],a[208:211],v[148:151]
s_lshl_b32 s94,s60,2
s_sub_u32 s94,s94,3
ds_read_b64_tr_b16 a[182:183],v22 offset:20224
v_mfma_f32_16x16x32_bf16 v[148:151],a[140:143],a[212:215],v[148:151]
s_add_u32 s83,s83,15
buffer_load_dwordx4 v4,s[20:23],s47 offen offset:1024 nt lds
v_mfma_f32_16x16x32_bf16 v[156:159],a[140:143],a[220:223],v[156:159]
s_lshl_b32 s44,s40,4
ds_read_b64_tr_b16 a[184:185],v22 offset:1800
v_subrev_u32 v25,s43,v4
v_mfma_f32_16x16x32_bf16 v[156:159],a[136:139],a[216:219],v[156:159]
s_lshl_b32 s43,s83,14
v_readfirstlane_b32 s68,v13
s_nop 1
s_add_u32 s68,s68,1
v_mfma_f32_16x16x32_bf16 v[160:163],a[144:147],a[192:195],v[160:163]
s_and_b32 s44,s44,32
s_lshl_b32 s83,s83,12
ds_read_b64_tr_b16 a[186:187],v22 offset:3848
v_mfma_f32_16x16x32_bf16 v[160:163],a[148:151],a[196:199],v[160:163]
s_lshl_b32 s80,s40,10
ds_read_b64_tr_b16 a[188:189],v22 offset:18184
v_xor_b32 v25,s44,v25
v_mfma_f32_16x16x32_bf16 v[168:171],a[148:151],a[204:207],v[168:171]
s_and_b32 s43,s43,0x20000
s_and_b32 s83,s83,0x7000
ds_read_b64_tr_b16 a[190:191],v22 offset:20232
v_mfma_f32_16x16x32_bf16 v[168:171],a[144:147],a[200:203],v[168:171]
s_xor_b32 s69,s68,0x9e3779b9
ds_read_b64_tr_b16 a[128:129],v22
v_add_u32 v25,s80,v25
v_mfma_f32_16x16x32_bf16 v[176:179],a[144:147],a[208:211],v[176:179]
s_or_b32 s83,s83,s43
s_and_b32 s69,s69,-2
ds_read_b64_tr_b16 a[130:131],v22 offset:2048
v_mfma_f32_16x16x32_bf16 v[176:179],a[148:151],a[212:215],v[176:179]
s_add_u32 s55,s55,s86
s_add_u32 s93,s92,L_att0x_%=-L_pc_%=
ds_read_b64_tr_b16 a[132:133],v22 offset:16384
v_mfma_f32_16x16x32_bf16 v[184:187],a[148:151],a[220:223],v[184:187]
s_mov_b32 s50,32768
s_mov_b32 s49,98304
ds_read_b64_tr_b16 a[134:135],v22 offset:18432
v_mfma_f32_16x16x32_bf16 v[184:187],a[144:147],a[216:219],v[184:187]
s_mov_b32 s45,0x1249
s_add_u32 s80,s80,131072
ds_read_b64_tr_b16 a[144:145],v22 offset:8
v_mfma_f32_16x16x32_bf16 v[164:167],a[152:155],a[192:195],v[164:167]
s_or_b32 s83,s83,0x40000
ds_read_b64_tr_b16 a[146:147],v22 offset:2056
s_waitcnt lgkmcnt(8)
v_mfma_f32_16x16x32_bf16 v[164:167],a[156:159],a[196:199],v[164:167]
buffer_load_dwordx4 v5,s[20:23],s47 offen offset:2048 nt lds
v_mfma_f32_16x16x32_bf16 v[172:175],a[156:159],a[204:207],v[172:175]
s_waitcnt vmcnt(5)
ds_read_b128 a[224:227],v20
v_mfma_f32_16x16x32_bf16 v[172:175],a[152:155],a[200:203],v[172:175]
ds_read_b64_tr_b16 a[148:149],v22 offset:16392
v_mfma_f32_16x16x32_bf16 v[180:183],a[152:155],a[208:211],v[180:183]
ds_read_b64_tr_b16 a[150:151],v22 offset:18440
v_mfma_f32_16x16x32_bf16 v[180:183],a[156:159],a[212:215],v[180:183]
v_mfma_f32_16x16x32_bf16 v[188:191],a[156:159],a[220:223],v[188:191]
ds_read_b128 a[228:231],v21
v_mfma_f32_16x16x32_bf16 v[188:191],a[152:155],a[216:219],v[188:191]
v_mfma_f32_16x16x32_bf16 v[192:195],a[160:163],a[192:195],v[192:195]
ds_read_b128 a[232:235],v20 offset:2048
v_mfma_f32_16x16x32_bf16 v[192:195],a[164:167],a[196:199],v[192:195]
v_mfma_f32_16x16x32_bf16 v[200:203],a[164:167],a[204:207],v[200:203]
ds_read_b128 a[236:239],v21 offset:2048
v_mfma_f32_16x16x32_bf16 v[200:203],a[160:163],a[200:203],v[200:203]
v_mfma_f32_16x16x32_bf16 v[208:211],a[160:163],a[208:211],v[208:211]
ds_read_b128 a[240:243],v20 offset:4096
v_mfma_f32_16x16x32_bf16 v[208:211],a[164:167],a[212:215],v[208:211]
buffer_load_dwordx4 v5,s[20:23],s47 offen offset:3072 nt lds
v_mfma_f32_16x16x32_bf16 v[216:219],a[164:167],a[220:223],v[216:219]
ds_read_b128 a[244:247],v21 offset:4096
v_mfma_f32_16x16x32_bf16 v[216:219],a[160:163],a[216:219],v[216:219]
ds_read_b64_tr_b16 a[160:161],v22 offset:256
v_mfma_f32_16x16x32_bf16 v[196:199],a[168:171],a[192:195],v[196:199]
)A"
R"A(ds_read_b128 a[248:251],v20 offset:6144
ds_read_b128 a[252:255],v21 offset:6144
v_mfma_f32_16x16x32_bf16 v[196:199],a[172:175],a[196:199],v[196:199]
v_mfma_f32_16x16x32_bf16 v[204:207],a[172:175],a[204:207],v[204:207]
v_mfma_f32_16x16x32_bf16 v[204:207],a[168:171],a[200:203],v[204:207]
v_mfma_f32_16x16x32_bf16 v[212:215],a[168:171],a[208:211],v[212:215]
ds_read_b64_tr_b16 a[162:163],v22 offset:2304
v_mfma_f32_16x16x32_bf16 v[212:215],a[172:175],a[212:215],v[212:215]
ds_read_b64_tr_b16 a[164:165],v22 offset:16640
v_mfma_f32_16x16x32_bf16 v[220:223],a[172:175],a[220:223],v[220:223]
s_waitcnt vmcnt(5) lgkmcnt(2)
s_add_u32 m0,98304,s52
ds_read_b64_tr_b16 a[166:167],v22 offset:18688
v_mfma_f32_16x16x32_bf16 v[220:223],a[168:171],a[216:219],v[220:223]
s_barrier
ds_read_b64_tr_b16 a[136:137],v22 offset:32768
v_mfma_f32_16x16x32_bf16 v[224:227],a[176:179],a[192:195],v[224:227]
buffer_load_dwordx4 v4,s[24:27],s47 offen nt lds
v_mfma_f32_16x16x32_bf16 v[224:227],a[180:183],a[196:199],v[224:227]
ds_read_b64_tr_b16 a[138:139],v22 offset:34816
v_mfma_f32_16x16x32_bf16 v[232:235],a[180:183],a[204:207],v[232:235]
ds_read_b64_tr_b16 a[140:141],v22 offset:49152
v_mfma_f32_16x16x32_bf16 v[232:235],a[176:179],a[200:203],v[232:235]
ds_read_b64_tr_b16 a[142:143],v22 offset:51200
v_mfma_f32_16x16x32_bf16 v[240:243],a[176:179],a[208:211],v[240:243]
ds_read_b64_tr_b16 a[152:153],v22 offset:32776
v_mfma_f32_16x16x32_bf16 v[240:243],a[180:183],a[212:215],v[240:243]
ds_read_b64_tr_b16 a[154:155],v22 offset:34824
v_mfma_f32_16x16x32_bf16 v[248:251],a[180:183],a[220:223],v[248:251]
ds_read_b64_tr_b16 a[156:157],v22 offset:49160
v_mfma_f32_16x16x32_bf16 v[248:251],a[176:179],a[216:219],v[248:251]
ds_read_b64_tr_b16 a[158:159],v22 offset:51208
v_mfma_f32_16x16x32_bf16 v[228:231],a[184:187],a[192:195],v[228:231]
ds_read_b64_tr_b16 a[168:169],v22 offset:33024
v_mfma_f32_16x16x32_bf16 v[228:231],a[188:191],a[196:199],v[228:231]
ds_read_b64_tr_b16 a[170:171],v22 offset:35072
v_mfma_f32_16x16x32_bf16 v[236:239],a[188:191],a[204:207],v[236:239]
ds_read_b64_tr_b16 a[172:173],v22 offset:49408
v_mfma_f32_16x16x32_bf16 v[236:239],a[184:187],a[200:203],v[236:239]
ds_read_b64_tr_b16 a[174:175],v22 offset:51456
v_mfma_f32_16x16x32_bf16 v[244:247],a[184:187],a[208:211],v[244:247]
buffer_load_dwordx4 v5,s[24:27],s47 offen offset:1024 nt lds
v_mfma_f32_16x16x32_bf16 v[244:247],a[188:191],a[212:215],v[244:247]
ds_read_b64_tr_b16 a[176:177],v22 offset:264
v_mfma_f32_16x16x32_bf16 v[252:255],a[188:191],a[220:223],v[252:255]
ds_read_b64_tr_b16 a[178:179],v22 offset:2312
v_mfma_f32_16x16x32_bf16 v[252:255],a[184:187],a[216:219],v[252:255]
ds_read_b64_tr_b16 a[180:181],v22 offset:16648
v_mfma_f32_16x16x32_bf16 a[0:3],a[128:131],a[192:195],a[0:3]
ds_read_b64_tr_b16 a[182:183],v22 offset:18696
v_mfma_f32_16x16x32_bf16 a[0:3],a[132:135],a[196:199],a[0:3]
s_waitcnt lgkmcnt(6)
ds_read_b64_tr_b16 a[184:185],v22 offset:33032
v_mfma_f32_16x16x32_bf16 a[0:3],a[136:139],a[224:227],a[0:3]
ds_read_b64_tr_b16 a[186:187],v22 offset:35080
v_mfma_f32_16x16x32_bf16 a[0:3],a[140:143],a[228:231],a[0:3]
ds_read_b64_tr_b16 a[188:189],v22 offset:49416
v_mfma_f32_16x16x32_bf16 a[32:35],a[140:143],a[236:239],a[32:35]
ds_read_b64_tr_b16 a[190:191],v22 offset:51464
v_mfma_f32_16x16x32_bf16 a[32:35],a[136:139],a[232:235],a[32:35]
v_mfma_f32_16x16x32_bf16 a[32:35],a[132:135],a[204:207],a[32:35]
v_mfma_f32_16x16x32_bf16 a[32:35],a[128:131],a[200:203],a[32:35]
v_mfma_f32_16x16x32_bf16 a[64:67],a[128:131],a[208:211],a[64:67]
buffer_load_dwordx4 v4,s[24:27],s47 offen offset:2048 nt lds
v_mfma_f32_16x16x32_bf16 a[64:67],a[132:135],a[212:215],a[64:67]
v_accvgpr_read_b32 v64,a0
v_accvgpr_read_b32 v65,a1
v_mfma_f32_16x16x32_bf16 a[64:67],a[136:139],a[240:243],a[64:67]
v_mul_f32 v64,s61,v64
v_mul_f32 v65,s61,v65
v_mfma_f32_16x16x32_bf16 a[64:67],a[140:143],a[244:247],a[64:67]
v_accvgpr_read_b32 v66,a2
v_accvgpr_read_b32 v67,a3
v_mfma_f32_16x16x32_bf16 a[96:99],a[140:143],a[252:255],a[96:99]
ds_read_b64_tr_b16 a[140:141],v22 offset:49664
v_cvt_pk_bf16_f32 v64,v64,v65
v_mfma_f32_16x16x32_bf16 a[96:99],a[136:139],a[248:251],a[96:99]
ds_read_b64_tr_b16 a[136:137],v22 offset:33280
v_accvgpr_read_b32 v80,a32
v_mfma_f32_16x16x32_bf16 a[96:99],a[132:135],a[220:223],a[96:99]
ds_read_b64_tr_b16 a[132:133],v22 offset:16896
v_accvgpr_read_b32 v81,a33
v_mfma_f32_16x16x32_bf16 a[96:99],a[128:131],a[216:219],a[96:99]
ds_read_b64_tr_b16 a[128:129],v22 offset:512
v_mul_f32 v80,s61,v80
v_mfma_f32_16x16x32_bf16 a[4:7],a[144:147],a[192:195],a[4:7]
ds_read_b64_tr_b16 a[130:131],v22 offset:2560
v_mul_f32 v81,s61,v81
v_mfma_f32_16x16x32_bf16 a[4:7],a[148:151],a[196:199],a[4:7]
ds_read_b64_tr_b16 a[134:135],v22 offset:18944
v_accvgpr_read_b32 v82,a34
v_mfma_f32_16x16x32_bf16 a[4:7],a[152:155],a[224:227],a[4:7]
ds_read_b64_tr_b16 a[138:139],v22 offset:35328
v_accvgpr_read_b32 v83,a35
v_mfma_f32_16x16x32_bf16 a[4:7],a[156:159],a[228:231],a[4:7]
ds_read_b64_tr_b16 a[142:143],v22 offset:51712
v_mul_f32 v66,s61,v66
v_mfma_f32_16x16x32_bf16 a[36:39],a[156:159],a[236:239],a[36:39]
buffer_load_dwordx4 v5,s[24:27],s47 offen offset:3072 nt lds
v_mfma_f32_16x16x32_bf16 a[36:39],a[152:155],a[232:235],a[36:39]
s_or_b32 s47,s47,0x18000
s_add_u32 s47,s47,4096
s_and_b32 s47,s47,0x27000
v_mfma_f32_16x16x32_bf16 a[36:39],a[148:151],a[204:207],a[36:39]
s_or_b32 s47,s47,s46
s_and_b32 s46,s54,0xfffc0000
v_mul_f32 v67,s61,v67
v_mfma_f32_16x16x32_bf16 a[36:39],a[144:147],a[200:203],a[36:39]
s_add_u32 s54,s54,16384
v_cvt_pk_bf16_f32 v80,v80,v81
v_mul_f32 v82,s61,v82
v_mfma_f32_16x16x32_bf16 a[68:71],a[144:147],a[208:211],a[68:71]
v_mul_f32 v83,s61,v83
v_cvt_pk_bf16_f32 v65,v66,v67
v_mfma_f32_16x16x32_bf16 a[68:71],a[148:151],a[212:215],a[68:71]
v_cvt_pk_bf16_f32 v81,v82,v83
v_accvgpr_read_b32 v112,a96
v_mfma_f32_16x16x32_bf16 a[68:71],a[152:155],a[240:243],a[68:71]
v_accvgpr_read_b32 v113,a97
v_accvgpr_read_b32 v66,a4
v_mfma_f32_16x16x32_bf16 a[68:71],a[156:159],a[244:247],a[68:71]
v_accvgpr_read_b32 v67,a5
v_mul_f32 v112,s61,v112
v_mfma_f32_16x16x32_bf16 a[100:103],a[156:159],a[252:255],a[100:103]
ds_read_b64_tr_b16 a[156:157],v22 offset:49672
v_mul_f32 v113,s61,v113
v_mfma_f32_16x16x32_bf16 a[100:103],a[152:155],a[248:251],a[100:103]
ds_read_b64_tr_b16 a[152:153],v22 offset:33288
v_accvgpr_read_b32 v82,a36
v_mfma_f32_16x16x32_bf16 a[100:103],a[148:151],a[220:223],a[100:103]
ds_read_b64_tr_b16 a[148:149],v22 offset:16904
v_accvgpr_read_b32 v83,a37
v_mfma_f32_16x16x32_bf16 a[100:103],a[144:147],a[216:219],a[100:103]
ds_read_b64_tr_b16 a[144:145],v22 offset:520
v_accvgpr_read_b32 v114,a98
v_mfma_f32_16x16x32_bf16 a[8:11],a[160:163],a[192:195],a[8:11]
s_add_u32 m0,0,s51
ds_read_b64_tr_b16 a[146:147],v22 offset:2568
v_accvgpr_read_b32 v115,a99
v_mfma_f32_16x16x32_bf16 a[8:11],a[164:167],a[196:199],a[8:11]
ds_read_b64_tr_b16 a[150:151],v22 offset:18952
v_accvgpr_read_b32 v12,a6
v_mfma_f32_16x16x32_bf16 a[8:11],a[168:171],a[224:227],a[8:11]
s_waitcnt lgkmcnt(6)
ds_read_b64_tr_b16 a[154:155],v22 offset:35336
v_accvgpr_read_b32 v13,a7
v_mfma_f32_16x16x32_bf16 a[8:11],a[172:175],a[228:231],a[8:11]
buffer_load_dwordx4 v4,s[20:23],s47 offen nt lds
v_mfma_f32_16x16x32_bf16 a[40:43],a[172:175],a[236:239],a[40:43]
ds_read_b64_tr_b16 a[158:159],v22 offset:51720
v_mul_f32 v66,s61,v66
v_mfma_f32_16x16x32_bf16 a[40:43],a[168:171],a[232:235],a[40:43]
v_mul_f32 v67,s61,v67
v_accvgpr_read_b32 v14,a38
v_mfma_f32_16x16x32_bf16 a[40:43],a[164:167],a[204:207],a[40:43]
v_accvgpr_read_b32 v15,a39
v_mul_f32 v82,s61,v82
v_mfma_f32_16x16x32_bf16 a[40:43],a[160:163],a[200:203],a[40:43]
v_mul_f32 v83,s61,v83
v_cvt_pk_bf16_f32 v112,v112,v113
v_mfma_f32_16x16x32_bf16 a[72:75],a[160:163],a[208:211],a[72:75]
v_mul_f32 v114,s61,v114
v_mul_f32 v115,s61,v115
v_mfma_f32_16x16x32_bf16 a[72:75],a[164:167],a[212:215],a[72:75]
v_accvgpr_read_b32 v68,a8
v_accvgpr_read_b32 v69,a9
v_mfma_f32_16x16x32_bf16 a[72:75],a[168:171],a[240:243],a[72:75]
v_mul_f32 v68,s61,v68
v_mul_f32 v69,s61,v69
v_mfma_f32_16x16x32_bf16 a[72:75],a[172:175],a[244:247],a[72:75]
v_accvgpr_read_b32 v70,a10
v_accvgpr_read_b32 v71,a11
v_mfma_f32_16x16x32_bf16 a[104:107],a[172:175],a[252:255],a[104:107]
ds_read_b64_tr_b16 a[172:173],v22 offset:49920
v_cvt_pk_bf16_f32 v68,v68,v69
v_mfma_f32_16x16x32_bf16 a[104:107],a[168:171],a[248:251],a[104:107]
ds_read_b64_tr_b16 a[168:169],v22 offset:33536
v_mul_f32 v70,s61,v70
v_mfma_f32_16x16x32_bf16 a[104:107],a[164:167],a[220:223],a[104:107]
ds_read_b64_tr_b16 a[164:165],v22 offset:17152
v_mul_f32 v71,s61,v71
v_mfma_f32_16x16x32_bf16 a[104:107],a[160:163],a[216:219],a[104:107]
ds_read_b64_tr_b16 a[160:161],v22 offset:768
v_mul_f32 v12,s61,v12
v_mfma_f32_16x16x32_bf16 a[12:15],a[176:179],a[192:195],a[12:15]
ds_read_b64_tr_b16 a[162:163],v22 offset:2816
v_mul_f32 v13,s61,v13
v_mfma_f32_16x16x32_bf16 a[12:15],a[180:183],a[196:199],a[12:15]
buffer_load_dwordx4 v4,s[20:23],s47 offen offset:1024 nt lds
v_mfma_f32_16x16x32_bf16 a[12:15],a[184:187],a[224:227],a[12:15]
ds_read_b64_tr_b16 a[166:167],v22 offset:19200
v_cvt_pk_bf16_f32 v66,v66,v67
v_mfma_f32_16x16x32_bf16 a[12:15],a[188:191],a[228:231],a[12:15]
ds_read_b64_tr_b16 a[170:171],v22 offset:35584
v_mul_f32 v14,s61,v14
v_mfma_f32_16x16x32_bf16 a[44:47],a[188:191],a[236:239],a[44:47]
ds_read_b64_tr_b16 a[174:175],v22 offset:51968
v_mul_f32 v15,s61,v15
v_mfma_f32_16x16x32_bf16 a[44:47],a[184:187],a[232:235],a[44:47]
v_cvt_pk_bf16_f32 v82,v82,v83
v_cvt_pk_bf16_f32 v113,v114,v115
v_mfma_f32_16x16x32_bf16 a[44:47],a[180:183],a[204:207],a[44:47]
v_cvt_pk_bf16_f32 v69,v70,v71
v_accvgpr_read_b32 v100,a72
v_mfma_f32_16x16x32_bf16 a[44:47],a[176:179],a[200:203],a[44:47]
v_accvgpr_read_b32 v101,a73
v_accvgpr_read_b32 v116,a104
v_mfma_f32_16x16x32_bf16 a[76:79],a[176:179],a[208:211],a[76:79]
v_accvgpr_read_b32 v117,a105
v_cvt_pk_bf16_f32 v67,v12,v13
v_mfma_f32_16x16x32_bf16 a[76:79],a[180:183],a[212:215],a[76:79]
v_cvt_pk_bf16_f32 v83,v14,v15
v_accvgpr_read_b32 v114,a100
v_mfma_f32_16x16x32_bf16 a[76:79],a[184:187],a[240:243],a[76:79]
v_accvgpr_read_b32 v115,a101
v_accvgpr_read_b32 v70,a12
v_mfma_f32_16x16x32_bf16 a[76:79],a[188:191],a[244:247],a[76:79]
v_accvgpr_read_b32 v71,a13
v_mul_f32 v100,s61,v100
v_mfma_f32_16x16x32_bf16 a[108:111],a[188:191],a[252:255],a[108:111]
ds_read_b64_tr_b16 a[188:189],v22 offset:49928
v_mul_f32 v101,s61,v101
v_mfma_f32_16x16x32_bf16 a[108:111],a[184:187],a[248:251],a[108:111]
ds_read_b64_tr_b16 a[184:185],v22 offset:33544
v_accvgpr_read_b32 v102,a74
v_mfma_f32_16x16x32_bf16 a[108:111],a[180:183],a[220:223],a[108:111]
ds_read_b64_tr_b16 a[180:181],v22 offset:17160
v_accvgpr_read_b32 v103,a75
v_mfma_f32_16x16x32_bf16 a[108:111],a[176:179],a[216:219],a[108:111]
buffer_load_dwordx4 v5,s[20:23],s47 offen offset:2048 nt lds
v_mfma_f32_16x16x32_bf16 a[16:19],a[128:131],a[192:195],a[16:19]
ds_read_b64_tr_b16 a[176:177],v22 offset:776
v_mul_f32 v116,s61,v116
v_mfma_f32_16x16x32_bf16 a[16:19],a[132:135],a[196:199],a[16:19]
ds_read_b64_tr_b16 a[178:179],v22 offset:2824
v_mul_f32 v117,s61,v117
v_mfma_f32_16x16x32_bf16 a[16:19],a[136:139],a[224:227],a[16:19]
ds_read_b64_tr_b16 a[182:183],v22 offset:19208
v_accvgpr_read_b32 v118,a106
v_mfma_f32_16x16x32_bf16 a[16:19],a[140:143],a[228:231],a[16:19]
ds_read_b64_tr_b16 a[186:187],v22 offset:35592
v_accvgpr_read_b32 v119,a107
v_mfma_f32_16x16x32_bf16 a[48:51],a[140:143],a[236:239],a[48:51]
ds_read_b64_tr_b16 a[190:191],v22 offset:51976
v_accvgpr_read_b32 v12,a102
v_mfma_f32_16x16x32_bf16 a[48:51],a[136:139],a[232:235],a[48:51]
v_accvgpr_read_b32 v13,a103
v_mul_f32 v114,s61,v114
v_mfma_f32_16x16x32_bf16 a[48:51],a[132:135],a[204:207],a[48:51]
v_mul_f32 v115,s61,v115
v_accvgpr_read_b32 v14,a14
v_mfma_f32_16x16x32_bf16 a[48:51],a[128:131],a[200:203],a[48:51]
v_accvgpr_read_b32 v15,a15
v_mul_f32 v70,s61,v70
v_mfma_f32_16x16x32_bf16 a[80:83],a[128:131],a[208:211],a[80:83]
v_mul_f32 v71,s61,v71
v_cvt_pk_bf16_f32 v100,v100,v101
v_mfma_f32_16x16x32_bf16 a[80:83],a[132:135],a[212:215],a[80:83]
v_mul_f32 v102,s61,v102
v_mul_f32 v103,s61,v103
v_mfma_f32_16x16x32_bf16 a[80:83],a[136:139],a[240:243],a[80:83]
)A"
R"A(v_cvt_pk_bf16_f32 v116,v116,v117
v_mul_f32 v118,s61,v118
v_mfma_f32_16x16x32_bf16 a[80:83],a[140:143],a[244:247],a[80:83]
v_mul_f32 v119,s61,v119
v_mul_f32 v12,s61,v12
v_mfma_f32_16x16x32_bf16 a[112:115],a[140:143],a[252:255],a[112:115]
ds_read_b64_tr_b16 a[140:141],v22 offset:50184
v_mul_f32 v13,s61,v13
v_mfma_f32_16x16x32_bf16 a[112:115],a[136:139],a[248:251],a[112:115]
buffer_load_dwordx4 v5,s[20:23],s47 offen offset:3072 nt lds
v_mfma_f32_16x16x32_bf16 a[112:115],a[132:135],a[220:223],a[112:115]
ds_read_b64_tr_b16 a[132:133],v22 offset:50176
v_cvt_pk_bf16_f32 v114,v114,v115
v_mfma_f32_16x16x32_bf16 a[112:115],a[128:131],a[216:219],a[112:115]
s_waitcnt lgkmcnt(2)
ds_read_b64_tr_b16 a[128:129],v22 offset:33792
v_mul_f32 v14,s61,v14
v_mfma_f32_16x16x32_bf16 a[20:23],a[144:147],a[192:195],a[20:23]
ds_read_b64_tr_b16 a[130:131],v22 offset:35840
v_mul_f32 v15,s61,v15
v_mfma_f32_16x16x32_bf16 a[20:23],a[148:151],a[196:199],a[20:23]
ds_read_b64_tr_b16 a[134:135],v22 offset:52224
v_cvt_pk_bf16_f32 v70,v70,v71
v_mfma_f32_16x16x32_bf16 a[20:23],a[152:155],a[224:227],a[20:23]
ds_read_b64_tr_b16 a[136:137],v22 offset:33800
v_cvt_pk_bf16_f32 v101,v102,v103
v_mfma_f32_16x16x32_bf16 a[20:23],a[156:159],a[228:231],a[20:23]
ds_read_b64_tr_b16 a[138:139],v22 offset:35848
v_cvt_pk_bf16_f32 v117,v118,v119
v_mfma_f32_16x16x32_bf16 a[52:55],a[156:159],a[236:239],a[52:55]
ds_read_b64_tr_b16 a[142:143],v22 offset:52232
v_cvt_pk_bf16_f32 v115,v12,v13
v_mfma_f32_16x16x32_bf16 a[52:55],a[152:155],a[232:235],a[52:55]
v_cvt_pk_bf16_f32 v71,v14,v15
v_accvgpr_read_b32 v102,a76
v_mfma_f32_16x16x32_bf16 a[52:55],a[148:151],a[204:207],a[52:55]
v_accvgpr_read_b32 v103,a77
v_accvgpr_read_b32 v118,a108
v_mfma_f32_16x16x32_bf16 a[52:55],a[144:147],a[200:203],a[52:55]
v_accvgpr_read_b32 v119,a109
v_accvgpr_read_b32 v12,a78
v_mfma_f32_16x16x32_bf16 a[84:87],a[144:147],a[208:211],a[84:87]
s_add_u32 m0,0,s52
v_accvgpr_read_b32 v13,a79
v_mul_f32 v102,s61,v102
v_mfma_f32_16x16x32_bf16 a[84:87],a[148:151],a[212:215],a[84:87]
v_mul_f32 v103,s61,v103
v_accvgpr_read_b32 v14,a110
v_mfma_f32_16x16x32_bf16 a[84:87],a[152:155],a[240:243],a[84:87]
v_accvgpr_read_b32 v15,a111
v_mul_f32 v118,s61,v118
v_mfma_f32_16x16x32_bf16 a[84:87],a[156:159],a[244:247],a[84:87]
buffer_load_dwordx4 v4,s[24:27],s47 offen nt lds
v_mfma_f32_16x16x32_bf16 a[116:119],a[156:159],a[252:255],a[116:119]
ds_read_b64_tr_b16 a[156:157],v22 offset:50440
v_mul_f32 v119,s61,v119
v_mfma_f32_16x16x32_bf16 a[116:119],a[152:155],a[248:251],a[116:119]
ds_read_b64_tr_b16 a[152:153],v22 offset:34056
v_mul_f32 v12,s61,v12
v_mfma_f32_16x16x32_bf16 a[116:119],a[148:151],a[220:223],a[116:119]
ds_read_b64_tr_b16 a[148:149],v22 offset:50432
v_mul_f32 v13,s61,v13
v_mfma_f32_16x16x32_bf16 a[116:119],a[144:147],a[216:219],a[116:119]
ds_read_b64_tr_b16 a[144:145],v22 offset:34048
v_cvt_pk_bf16_f32 v102,v102,v103
v_mfma_f32_16x16x32_bf16 a[24:27],a[160:163],a[192:195],a[24:27]
ds_read_b64_tr_b16 a[146:147],v22 offset:36096
v_mul_f32 v14,s61,v14
v_mfma_f32_16x16x32_bf16 a[24:27],a[164:167],a[196:199],a[24:27]
ds_read_b64_tr_b16 a[150:151],v22 offset:52480
v_mul_f32 v15,s61,v15
v_mfma_f32_16x16x32_bf16 a[24:27],a[168:171],a[224:227],a[24:27]
ds_read_b64_tr_b16 a[154:155],v22 offset:36104
v_cvt_pk_bf16_f32 v118,v118,v119
v_mfma_f32_16x16x32_bf16 a[24:27],a[172:175],a[228:231],a[24:27]
ds_read_b64_tr_b16 a[158:159],v22 offset:52488
v_cvt_pk_bf16_f32 v103,v12,v13
v_mfma_f32_16x16x32_bf16 a[56:59],a[172:175],a[236:239],a[56:59]
v_cvt_pk_bf16_f32 v119,v14,v15
v_accvgpr_read_b32 v96,a64
v_mfma_f32_16x16x32_bf16 a[56:59],a[168:171],a[232:235],a[56:59]
v_accvgpr_read_b32 v72,a16
v_accvgpr_read_b32 v73,a17
v_accvgpr_read_b32 v74,a18
v_accvgpr_read_b32 v75,a19
v_accvgpr_read_b32 v12,a20
v_accvgpr_read_b32 v13,a21
v_accvgpr_read_b32 v14,a22
v_accvgpr_read_b32 v15,a23
v_pk_mul_f32 v[72:73],s[60:61],v[72:73] op_sel:[1,0] op_sel_hi:[1,1]
v_pk_mul_f32 v[74:75],s[60:61],v[74:75] op_sel:[1,0] op_sel_hi:[1,1]
v_pk_mul_f32 v[12:13],s[60:61],v[12:13] op_sel:[1,0] op_sel_hi:[1,1]
v_pk_mul_f32 v[14:15],s[60:61],v[14:15] op_sel:[1,0] op_sel_hi:[1,1]
v_cvt_pk_bf16_f32 v72,v72,v73
v_cvt_pk_bf16_f32 v73,v74,v75
v_cvt_pk_bf16_f32 v74,v12,v13
v_cvt_pk_bf16_f32 v75,v14,v15
v_mfma_f32_16x16x32_bf16 a[56:59],a[164:167],a[204:207],a[56:59]
v_accvgpr_read_b32 v97,a65
v_mul_f32 v96,s61,v96
v_mfma_f32_16x16x32_bf16 a[56:59],a[160:163],a[200:203],a[56:59]
v_accvgpr_read_b32 v88,a48
v_accvgpr_read_b32 v89,a49
v_accvgpr_read_b32 v90,a50
v_accvgpr_read_b32 v91,a51
v_accvgpr_read_b32 v12,a52
v_accvgpr_read_b32 v13,a53
v_accvgpr_read_b32 v14,a54
v_accvgpr_read_b32 v15,a55
v_pk_mul_f32 v[88:89],s[60:61],v[88:89] op_sel:[1,0] op_sel_hi:[1,1]
v_pk_mul_f32 v[90:91],s[60:61],v[90:91] op_sel:[1,0] op_sel_hi:[1,1]
v_pk_mul_f32 v[12:13],s[60:61],v[12:13] op_sel:[1,0] op_sel_hi:[1,1]
v_pk_mul_f32 v[14:15],s[60:61],v[14:15] op_sel:[1,0] op_sel_hi:[1,1]
v_cvt_pk_bf16_f32 v88,v88,v89
v_cvt_pk_bf16_f32 v89,v90,v91
v_cvt_pk_bf16_f32 v90,v12,v13
v_cvt_pk_bf16_f32 v91,v14,v15
v_mfma_f32_16x16x32_bf16 a[88:91],a[160:163],a[208:211],a[88:91]
v_mul_f32 v97,s61,v97
v_accvgpr_read_b32 v98,a66
v_mfma_f32_16x16x32_bf16 a[88:91],a[164:167],a[212:215],a[88:91]
buffer_load_dwordx4 v5,s[24:27],s47 offen offset:1024 nt lds
v_mfma_f32_16x16x32_bf16 a[88:91],a[168:171],a[240:243],a[88:91]
v_accvgpr_read_b32 v99,a67
v_cvt_pk_bf16_f32 v96,v96,v97
v_mfma_f32_16x16x32_bf16 a[88:91],a[172:175],a[244:247],a[88:91]
v_mul_f32 v98,s61,v98
v_mul_f32 v99,s61,v99
v_mfma_f32_16x16x32_bf16 a[120:123],a[172:175],a[252:255],a[120:123]
ds_read_b64_tr_b16 a[172:173],v22 offset:50696
v_cvt_pk_bf16_f32 v97,v98,v99
v_mfma_f32_16x16x32_bf16 a[120:123],a[168:171],a[248:251],a[120:123]
ds_read_b64_tr_b16 a[168:169],v22 offset:34312
v_accvgpr_read_b32 v84,a40
v_mfma_f32_16x16x32_bf16 a[120:123],a[164:167],a[220:223],a[120:123]
ds_read_b64_tr_b16 a[164:165],v22 offset:50688
v_accvgpr_read_b32 v85,a41
v_mfma_f32_16x16x32_bf16 a[120:123],a[160:163],a[216:219],a[120:123]
ds_read_b64_tr_b16 a[160:161],v22 offset:34304
v_accvgpr_read_b32 v98,a68
v_mfma_f32_16x16x32_bf16 a[28:31],a[176:179],a[192:195],a[28:31]
ds_read_b64_tr_b16 a[162:163],v22 offset:36352
v_accvgpr_read_b32 v99,a69
v_mfma_f32_16x16x32_bf16 a[28:31],a[180:183],a[196:199],a[28:31]
ds_read_b64_tr_b16 a[166:167],v22 offset:52736
v_mul_f32 v84,s61,v84
v_mfma_f32_16x16x32_bf16 a[28:31],a[184:187],a[224:227],a[28:31]
ds_read_b64_tr_b16 a[170:171],v22 offset:36360
v_mul_f32 v85,s61,v85
v_mfma_f32_16x16x32_bf16 a[28:31],a[188:191],a[228:231],a[28:31]
ds_read_b64_tr_b16 a[174:175],v22 offset:52744
v_accvgpr_read_b32 v86,a42
v_mfma_f32_16x16x32_bf16 a[60:63],a[188:191],a[236:239],a[60:63]
v_accvgpr_read_b32 v104,a80
v_accvgpr_read_b32 v105,a81
v_accvgpr_read_b32 v106,a82
v_accvgpr_read_b32 v107,a83
v_accvgpr_read_b32 v12,a84
v_accvgpr_read_b32 v13,a85
v_accvgpr_read_b32 v14,a86
v_accvgpr_read_b32 v15,a87
v_pk_mul_f32 v[104:105],s[60:61],v[104:105] op_sel:[1,0] op_sel_hi:[1,1]
v_pk_mul_f32 v[106:107],s[60:61],v[106:107] op_sel:[1,0] op_sel_hi:[1,1]
v_pk_mul_f32 v[12:13],s[60:61],v[12:13] op_sel:[1,0] op_sel_hi:[1,1]
v_pk_mul_f32 v[14:15],s[60:61],v[14:15] op_sel:[1,0] op_sel_hi:[1,1]
v_cvt_pk_bf16_f32 v104,v104,v105
v_cvt_pk_bf16_f32 v105,v106,v107
v_cvt_pk_bf16_f32 v106,v12,v13
v_cvt_pk_bf16_f32 v107,v14,v15
v_mfma_f32_16x16x32_bf16 a[60:63],a[184:187],a[232:235],a[60:63]
v_accvgpr_read_b32 v87,a43
v_accvgpr_read_b32 v16,a70
v_mfma_f32_16x16x32_bf16 a[60:63],a[180:183],a[204:207],a[60:63]
v_accvgpr_read_b32 v120,a112
v_accvgpr_read_b32 v121,a113
v_accvgpr_read_b32 v122,a114
v_accvgpr_read_b32 v123,a115
v_accvgpr_read_b32 v12,a116
v_accvgpr_read_b32 v13,a117
v_accvgpr_read_b32 v14,a118
v_accvgpr_read_b32 v15,a119
v_pk_mul_f32 v[120:121],s[60:61],v[120:121] op_sel:[1,0] op_sel_hi:[1,1]
v_pk_mul_f32 v[122:123],s[60:61],v[122:123] op_sel:[1,0] op_sel_hi:[1,1]
v_pk_mul_f32 v[12:13],s[60:61],v[12:13] op_sel:[1,0] op_sel_hi:[1,1]
v_pk_mul_f32 v[14:15],s[60:61],v[14:15] op_sel:[1,0] op_sel_hi:[1,1]
v_cvt_pk_bf16_f32 v120,v120,v121
v_cvt_pk_bf16_f32 v121,v122,v123
v_cvt_pk_bf16_f32 v122,v12,v13
v_cvt_pk_bf16_f32 v123,v14,v15
v_mfma_f32_16x16x32_bf16 a[60:63],a[176:179],a[200:203],a[60:63]
buffer_load_dwordx4 v4,s[24:27],s47 offen offset:2048 nt lds
v_mfma_f32_16x16x32_bf16 a[92:95],a[176:179],a[208:211],a[92:95]
v_accvgpr_read_b32 v17,a71
v_mul_f32 v98,s61,v98
v_mfma_f32_16x16x32_bf16 a[92:95],a[180:183],a[212:215],a[92:95]
v_mul_f32 v99,s61,v99
v_cvt_pk_bf16_f32 v84,v84,v85
v_mfma_f32_16x16x32_bf16 a[92:95],a[184:187],a[240:243],a[92:95]
v_mul_f32 v86,s61,v86
v_mul_f32 v87,s61,v87
v_mfma_f32_16x16x32_bf16 a[92:95],a[188:191],a[244:247],a[92:95]
v_mul_f32 v16,s61,v16
v_mul_f32 v17,s61,v17
v_mfma_f32_16x16x32_bf16 a[124:127],a[188:191],a[252:255],a[124:127]
ds_read_b64_tr_b16 a[188:189],v22 offset:50952
v_cvt_pk_bf16_f32 v98,v98,v99
v_mfma_f32_16x16x32_bf16 a[124:127],a[184:187],a[248:251],a[124:127]
ds_read_b64_tr_b16 a[184:185],v22 offset:34568
v_cvt_pk_bf16_f32 v85,v86,v87
v_mfma_f32_16x16x32_bf16 a[124:127],a[180:183],a[220:223],a[124:127]
ds_read_b64_tr_b16 a[180:181],v22 offset:50944
v_cvt_pk_bf16_f32 v99,v16,v17
v_mfma_f32_16x16x32_bf16 a[124:127],a[176:179],a[216:219],a[124:127]
s_waitcnt lgkmcnt(3)
ds_read_b64_tr_b16 a[176:177],v22 offset:34560
v_accvgpr_read_b32 v86,a44
v_mfma_f32_16x16x32_bf16 v[128:131],a[128:131],a[224:227],v[128:131]
ds_read_b64_tr_b16 a[178:179],v22 offset:36608
v_accvgpr_read_b32 v87,a45
v_mfma_f32_16x16x32_bf16 v[128:131],a[132:135],a[228:231],v[128:131]
ds_read_b64_tr_b16 a[182:183],v22 offset:52992
v_accvgpr_read_b32 v16,a46
v_mfma_f32_16x16x32_bf16 v[136:139],a[132:135],a[236:239],v[136:139]
ds_read_b64_tr_b16 a[186:187],v22 offset:36616
v_accvgpr_read_b32 v17,a47
v_mfma_f32_16x16x32_bf16 v[136:139],a[128:131],a[232:235],v[136:139]
ds_read_b64_tr_b16 a[190:191],v22 offset:53000
v_mul_f32 v86,s61,v86
v_mfma_f32_16x16x32_bf16 v[144:147],a[128:131],a[240:243],v[144:147]
v_accvgpr_read_b32 v76,a24
v_accvgpr_read_b32 v77,a25
v_accvgpr_read_b32 v78,a26
v_accvgpr_read_b32 v79,a27
v_accvgpr_read_b32 v12,a28
v_accvgpr_read_b32 v13,a29
v_accvgpr_read_b32 v14,a30
v_accvgpr_read_b32 v15,a31
v_pk_mul_f32 v[76:77],s[60:61],v[76:77] op_sel:[1,0] op_sel_hi:[1,1]
v_pk_mul_f32 v[78:79],s[60:61],v[78:79] op_sel:[1,0] op_sel_hi:[1,1]
v_pk_mul_f32 v[12:13],s[60:61],v[12:13] op_sel:[1,0] op_sel_hi:[1,1]
v_pk_mul_f32 v[14:15],s[60:61],v[14:15] op_sel:[1,0] op_sel_hi:[1,1]
v_cvt_pk_bf16_f32 v76,v76,v77
v_cvt_pk_bf16_f32 v77,v78,v79
v_cvt_pk_bf16_f32 v78,v12,v13
v_cvt_pk_bf16_f32 v79,v14,v15
v_mfma_f32_16x16x32_bf16 v[144:147],a[132:135],a[244:247],v[144:147]
buffer_load_dwordx4 v5,s[24:27],s47 offen offset:3072 nt lds
v_mfma_f32_16x16x32_bf16 v[152:155],a[132:135],a[252:255],v[152:155]
s_or_b32 s47,s47,0x18000
s_add_u32 s47,s47,4096
s_and_b32 s47,s47,0x27000
v_mfma_f32_16x16x32_bf16 v[152:155],a[128:131],a[248:251],v[152:155]
s_or_b32 s47,s47,s46
v_mul_f32 v87,s61,v87
v_mul_f32 v16,s61,v16
v_mfma_f32_16x16x32_bf16 v[132:135],a[136:139],a[224:227],v[132:135]
s_and_b32 s46,s54,0xfffc0000
v_mul_f32 v17,s61,v17
v_cvt_pk_bf16_f32 v86,v86,v87
v_mfma_f32_16x16x32_bf16 v[132:135],a[140:143],a[228:231],v[132:135]
s_add_u32 s54,s54,16384
v_cvt_pk_bf16_f32 v87,v16,v17
v_mfma_f32_16x16x32_bf16 v[140:143],a[140:143],a[236:239],v[140:143]
v_accvgpr_read_b32 v92,a56
v_accvgpr_read_b32 v93,a57
v_accvgpr_read_b32 v94,a58
v_accvgpr_read_b32 v95,a59
v_accvgpr_read_b32 v12,a60
v_accvgpr_read_b32 v13,a61
v_accvgpr_read_b32 v14,a62
v_accvgpr_read_b32 v15,a63
v_pk_mul_f32 v[92:93],s[60:61],v[92:93] op_sel:[1,0] op_sel_hi:[1,1]
v_pk_mul_f32 v[94:95],s[60:61],v[94:95] op_sel:[1,0] op_sel_hi:[1,1]
v_pk_mul_f32 v[12:13],s[60:61],v[12:13] op_sel:[1,0] op_sel_hi:[1,1]
v_pk_mul_f32 v[14:15],s[60:61],v[14:15] op_sel:[1,0] op_sel_hi:[1,1]
v_cvt_pk_bf16_f32 v92,v92,v93
v_cvt_pk_bf16_f32 v93,v94,v95
v_cvt_pk_bf16_f32 v94,v12,v13
v_cvt_pk_bf16_f32 v95,v14,v15
v_mfma_f32_16x16x32_bf16 v[140:143],a[136:139],a[232:235],v[140:143]
v_accvgpr_read_b32 v108,a88
)A"
R"A(v_accvgpr_read_b32 v109,a89
v_accvgpr_read_b32 v110,a90
v_accvgpr_read_b32 v111,a91
v_accvgpr_read_b32 v12,a92
v_accvgpr_read_b32 v13,a93
v_accvgpr_read_b32 v14,a94
v_accvgpr_read_b32 v15,a95
v_pk_mul_f32 v[108:109],s[60:61],v[108:109] op_sel:[1,0] op_sel_hi:[1,1]
v_pk_mul_f32 v[110:111],s[60:61],v[110:111] op_sel:[1,0] op_sel_hi:[1,1]
v_pk_mul_f32 v[12:13],s[60:61],v[12:13] op_sel:[1,0] op_sel_hi:[1,1]
v_pk_mul_f32 v[14:15],s[60:61],v[14:15] op_sel:[1,0] op_sel_hi:[1,1]
v_cvt_pk_bf16_f32 v108,v108,v109
v_cvt_pk_bf16_f32 v109,v110,v111
v_cvt_pk_bf16_f32 v110,v12,v13
v_cvt_pk_bf16_f32 v111,v14,v15
v_mfma_f32_16x16x32_bf16 v[148:151],a[136:139],a[240:243],v[148:151]
v_accvgpr_read_b32 v124,a120
v_accvgpr_read_b32 v125,a121
v_accvgpr_read_b32 v126,a122
v_accvgpr_read_b32 v127,a123
v_accvgpr_read_b32 v12,a124
v_accvgpr_read_b32 v13,a125
v_accvgpr_read_b32 v14,a126
v_accvgpr_read_b32 v15,a127
v_pk_mul_f32 v[124:125],s[60:61],v[124:125] op_sel:[1,0] op_sel_hi:[1,1]
v_pk_mul_f32 v[126:127],s[60:61],v[126:127] op_sel:[1,0] op_sel_hi:[1,1]
v_pk_mul_f32 v[12:13],s[60:61],v[12:13] op_sel:[1,0] op_sel_hi:[1,1]
v_pk_mul_f32 v[14:15],s[60:61],v[14:15] op_sel:[1,0] op_sel_hi:[1,1]
v_cvt_pk_bf16_f32 v124,v124,v125
v_cvt_pk_bf16_f32 v125,v126,v127
v_cvt_pk_bf16_f32 v126,v12,v13
v_cvt_pk_bf16_f32 v127,v14,v15
v_mfma_f32_16x16x32_bf16 v[148:151],a[140:143],a[244:247],v[148:151]
v_mfma_f32_16x16x32_bf16 v[156:159],a[140:143],a[252:255],v[156:159]
s_waitcnt vmcnt(8) lgkmcnt(0)
v_mfma_f32_16x16x32_bf16 v[156:159],a[136:139],a[248:251],v[156:159]
s_barrier
ds_read_b128 v[48:51],v18
v_mfma_f32_16x16x32_bf16 v[160:163],a[144:147],a[224:227],v[160:163]
ds_read_b64_tr_b16 a[192:193],v2
v_mfma_f32_16x16x32_bf16 v[160:163],a[148:151],a[228:231],v[160:163]
ds_read_b64_tr_b16 a[194:195],v2 offset:4096
v_mfma_f32_16x16x32_bf16 v[168:171],a[148:151],a[236:239],v[168:171]
ds_read_b64_tr_b16 a[196:197],v2 offset:8192
v_mfma_f32_16x16x32_bf16 v[168:171],a[144:147],a[232:235],v[168:171]
ds_read_b128 v[52:55],v18 offset:256
v_mfma_f32_16x16x32_bf16 v[176:179],a[144:147],a[240:243],v[176:179]
v_mul_f32 v128,s61,v128
v_mul_f32 v129,s61,v129
v_mul_f32 v130,s61,v130
v_mul_f32 v131,s61,v131
v_cvt_pk_bf16_f32 v128,v128,v129
v_cvt_pk_bf16_f32 v129,v130,v131
v_pk_mul_f32 v[132:133],s[60:61],v[132:133] op_sel:[1,0] op_sel_hi:[1,1]
v_pk_mul_f32 v[134:135],s[60:61],v[134:135] op_sel:[1,0] op_sel_hi:[1,1]
v_pk_mul_f32 v[136:137],s[60:61],v[136:137] op_sel:[1,0] op_sel_hi:[1,1]
v_pk_mul_f32 v[138:139],s[60:61],v[138:139] op_sel:[1,0] op_sel_hi:[1,1]
v_pk_mul_f32 v[140:141],s[60:61],v[140:141] op_sel:[1,0] op_sel_hi:[1,1]
v_pk_mul_f32 v[142:143],s[60:61],v[142:143] op_sel:[1,0] op_sel_hi:[1,1]
v_pk_mul_f32 v[144:145],s[60:61],v[144:145] op_sel:[1,0] op_sel_hi:[1,1]
v_pk_mul_f32 v[146:147],s[60:61],v[146:147] op_sel:[1,0] op_sel_hi:[1,1]
v_pk_mul_f32 v[148:149],s[60:61],v[148:149] op_sel:[1,0] op_sel_hi:[1,1]
v_pk_mul_f32 v[150:151],s[60:61],v[150:151] op_sel:[1,0] op_sel_hi:[1,1]
v_pk_mul_f32 v[152:153],s[60:61],v[152:153] op_sel:[1,0] op_sel_hi:[1,1]
v_pk_mul_f32 v[154:155],s[60:61],v[154:155] op_sel:[1,0] op_sel_hi:[1,1]
v_pk_mul_f32 v[156:157],s[60:61],v[156:157] op_sel:[1,0] op_sel_hi:[1,1]
v_pk_mul_f32 v[158:159],s[60:61],v[158:159] op_sel:[1,0] op_sel_hi:[1,1]
v_mfma_f32_16x16x32_bf16 v[176:179],a[148:151],a[244:247],v[176:179]
ds_read_b128 v[56:59],v18 offset:512
v_cvt_pk_bf16_f32 v130,v132,v133
v_mfma_f32_16x16x32_bf16 v[184:187],a[148:151],a[252:255],v[184:187]
v_cvt_pk_bf16_f32 v132,v136,v137
v_cvt_pk_bf16_f32 v133,v138,v139
v_mfma_f32_16x16x32_bf16 v[184:187],a[144:147],a[248:251],v[184:187]
ds_read_b128 v[60:63],v18 offset:768
v_cvt_pk_bf16_f32 v131,v134,v135
v_mfma_f32_16x16x32_bf16 v[164:167],a[152:155],a[224:227],v[164:167]
v_cvt_pk_bf16_f32 v134,v140,v141
v_cvt_pk_bf16_f32 v135,v142,v143
v_mfma_f32_16x16x32_bf16 v[164:167],a[156:159],a[228:231],v[164:167]
ds_read_b128 a[128:131],v1
v_cvt_pk_bf16_f32 v144,v144,v145
v_mfma_f32_16x16x32_bf16 v[172:175],a[156:159],a[236:239],v[172:175]
v_cvt_pk_bf16_f32 v145,v146,v147
v_cvt_pk_bf16_f32 v146,v148,v149
v_mfma_f32_16x16x32_bf16 v[172:175],a[152:155],a[232:235],v[172:175]
ds_read_b128 a[132:135],v1 offset:256
v_cvt_pk_bf16_f32 v148,v152,v153
v_mfma_f32_16x16x32_bf16 v[180:183],a[152:155],a[240:243],v[180:183]
v_cvt_pk_bf16_f32 v149,v154,v155
v_cvt_pk_bf16_f32 v147,v150,v151
v_mfma_f32_16x16x32_bf16 v[180:183],a[156:159],a[244:247],v[180:183]
ds_read_b128 a[136:139],v1 offset:512
v_cvt_pk_bf16_f32 v150,v156,v157
v_mfma_f32_16x16x32_bf16 v[188:191],a[156:159],a[252:255],v[188:191]
ds_read_b64_tr_b16 a[198:199],v2 offset:12288
v_cvt_pk_bf16_f32 v151,v158,v159
v_mfma_f32_16x16x32_bf16 v[188:191],a[152:155],a[248:251],v[188:191]
ds_read_b64_tr_b16 a[204:205],v3 offset:8192
v_mfma_f32_16x16x32_bf16 v[192:195],a[160:163],a[224:227],v[192:195]
ds_read_b64_tr_b16 a[206:207],v3 offset:12288
v_mfma_f32_16x16x32_bf16 v[192:195],a[164:167],a[228:231],v[192:195]
s_add_u32 m0,32768,s51
ds_read_b128 a[140:143],v1 offset:768
v_mfma_f32_16x16x32_bf16 v[200:203],a[164:167],a[236:239],v[200:203]
ds_read_b64_tr_b16 a[200:201],v3
v_mfma_f32_16x16x32_bf16 v[200:203],a[160:163],a[232:235],v[200:203]
ds_read_b64_tr_b16 a[202:203],v3 offset:4096
v_mfma_f32_16x16x32_bf16 v[208:211],a[160:163],a[240:243],v[208:211]
buffer_load_dwordx4 v4,s[20:23],s47 offen nt lds
v_mfma_f32_16x16x32_bf16 v[208:211],a[164:167],a[244:247],v[208:211]
ds_read_b128 a[156:159],v1 offset:4864
v_mfma_f32_16x16x32_bf16 v[216:219],a[164:167],a[252:255],v[216:219]
v_mul_f32 v160,s61,v160
v_mul_f32 v161,s61,v161
v_mul_f32 v162,s61,v162
v_mul_f32 v163,s61,v163
v_cvt_pk_bf16_f32 v160,v160,v161
v_cvt_pk_bf16_f32 v161,v162,v163
v_pk_mul_f32 v[164:165],s[60:61],v[164:165] op_sel:[1,0] op_sel_hi:[1,1]
v_pk_mul_f32 v[166:167],s[60:61],v[166:167] op_sel:[1,0] op_sel_hi:[1,1]
v_pk_mul_f32 v[168:169],s[60:61],v[168:169] op_sel:[1,0] op_sel_hi:[1,1]
v_pk_mul_f32 v[170:171],s[60:61],v[170:171] op_sel:[1,0] op_sel_hi:[1,1]
v_pk_mul_f32 v[172:173],s[60:61],v[172:173] op_sel:[1,0] op_sel_hi:[1,1]
v_pk_mul_f32 v[174:175],s[60:61],v[174:175] op_sel:[1,0] op_sel_hi:[1,1]
v_pk_mul_f32 v[176:177],s[60:61],v[176:177] op_sel:[1,0] op_sel_hi:[1,1]
v_pk_mul_f32 v[178:179],s[60:61],v[178:179] op_sel:[1,0] op_sel_hi:[1,1]
v_pk_mul_f32 v[180:181],s[60:61],v[180:181] op_sel:[1,0] op_sel_hi:[1,1]
v_pk_mul_f32 v[182:183],s[60:61],v[182:183] op_sel:[1,0] op_sel_hi:[1,1]
v_pk_mul_f32 v[184:185],s[60:61],v[184:185] op_sel:[1,0] op_sel_hi:[1,1]
v_pk_mul_f32 v[186:187],s[60:61],v[186:187] op_sel:[1,0] op_sel_hi:[1,1]
v_pk_mul_f32 v[188:189],s[60:61],v[188:189] op_sel:[1,0] op_sel_hi:[1,1]
v_pk_mul_f32 v[190:191],s[60:61],v[190:191] op_sel:[1,0] op_sel_hi:[1,1]
v_mfma_f32_16x16x32_bf16 v[216:219],a[160:163],a[248:251],v[216:219]
ds_read_b128 a[152:155],v1 offset:4608
v_cvt_pk_bf16_f32 v162,v164,v165
v_mfma_f32_16x16x32_bf16 v[196:199],a[168:171],a[224:227],v[196:199]
v_cvt_pk_bf16_f32 v164,v168,v169
v_cvt_pk_bf16_f32 v165,v170,v171
v_mfma_f32_16x16x32_bf16 v[196:199],a[172:175],a[228:231],v[196:199]
ds_read_b128 a[148:151],v1 offset:4352
v_cvt_pk_bf16_f32 v163,v166,v167
v_mfma_f32_16x16x32_bf16 v[204:207],a[172:175],a[236:239],v[204:207]
v_cvt_pk_bf16_f32 v166,v172,v173
v_cvt_pk_bf16_f32 v167,v174,v175
v_mfma_f32_16x16x32_bf16 v[204:207],a[168:171],a[232:235],v[204:207]
ds_read_b128 a[144:147],v1 offset:4096
v_cvt_pk_bf16_f32 v176,v176,v177
v_mfma_f32_16x16x32_bf16 v[212:215],a[168:171],a[240:243],v[212:215]
v_cvt_pk_bf16_f32 v177,v178,v179
v_cvt_pk_bf16_f32 v178,v180,v181
v_mfma_f32_16x16x32_bf16 v[212:215],a[172:175],a[244:247],v[212:215]
ds_read_b128 a[160:163],v1 offset:8192
v_cvt_pk_bf16_f32 v180,v184,v185
v_mfma_f32_16x16x32_bf16 v[220:223],a[172:175],a[252:255],v[220:223]
v_cvt_pk_bf16_f32 v181,v186,v187
v_cvt_pk_bf16_f32 v179,v182,v183
v_mfma_f32_16x16x32_bf16 v[220:223],a[168:171],a[248:251],v[220:223]
ds_read_b128 a[164:167],v1 offset:8448
v_cvt_pk_bf16_f32 v182,v188,v189
v_mfma_f32_16x16x32_bf16 v[224:227],a[176:179],a[224:227],v[224:227]
ds_read_b64_tr_b16 a[208:209],v2 offset:256
v_cvt_pk_bf16_f32 v183,v190,v191
v_mfma_f32_16x16x32_bf16 v[224:227],a[180:183],a[228:231],v[224:227]
buffer_load_dwordx4 v4,s[20:23],s47 offen offset:1024 nt lds
v_mfma_f32_16x16x32_bf16 v[232:235],a[180:183],a[236:239],v[232:235]
ds_read_b128 a[168:171],v1 offset:8704
v_mfma_f32_16x16x32_bf16 v[232:235],a[176:179],a[232:235],v[232:235]
ds_read_b64_tr_b16 a[210:211],v2 offset:4352
v_mfma_f32_16x16x32_bf16 v[240:243],a[176:179],a[240:243],v[240:243]
ds_read_b64_tr_b16 a[212:213],v2 offset:8448
v_mfma_f32_16x16x32_bf16 v[240:243],a[180:183],a[244:247],v[240:243]
v_mul_f32 v192,s61,v192
v_mul_f32 v193,s61,v193
v_mul_f32 v194,s61,v194
v_mul_f32 v195,s61,v195
v_cvt_pk_bf16_f32 v192,v192,v193
v_cvt_pk_bf16_f32 v193,v194,v195
v_pk_mul_f32 v[196:197],s[60:61],v[196:197] op_sel:[1,0] op_sel_hi:[1,1]
v_pk_mul_f32 v[198:199],s[60:61],v[198:199] op_sel:[1,0] op_sel_hi:[1,1]
v_pk_mul_f32 v[200:201],s[60:61],v[200:201] op_sel:[1,0] op_sel_hi:[1,1]
v_pk_mul_f32 v[202:203],s[60:61],v[202:203] op_sel:[1,0] op_sel_hi:[1,1]
v_pk_mul_f32 v[204:205],s[60:61],v[204:205] op_sel:[1,0] op_sel_hi:[1,1]
v_pk_mul_f32 v[206:207],s[60:61],v[206:207] op_sel:[1,0] op_sel_hi:[1,1]
v_pk_mul_f32 v[208:209],s[60:61],v[208:209] op_sel:[1,0] op_sel_hi:[1,1]
v_pk_mul_f32 v[210:211],s[60:61],v[210:211] op_sel:[1,0] op_sel_hi:[1,1]
v_pk_mul_f32 v[212:213],s[60:61],v[212:213] op_sel:[1,0] op_sel_hi:[1,1]
v_pk_mul_f32 v[214:215],s[60:61],v[214:215] op_sel:[1,0] op_sel_hi:[1,1]
v_pk_mul_f32 v[216:217],s[60:61],v[216:217] op_sel:[1,0] op_sel_hi:[1,1]
v_pk_mul_f32 v[218:219],s[60:61],v[218:219] op_sel:[1,0] op_sel_hi:[1,1]
v_pk_mul_f32 v[220:221],s[60:61],v[220:221] op_sel:[1,0] op_sel_hi:[1,1]
v_pk_mul_f32 v[222:223],s[60:61],v[222:223] op_sel:[1,0] op_sel_hi:[1,1]
v_mfma_f32_16x16x32_bf16 v[248:251],a[180:183],a[252:255],v[248:251]
ds_read_b128 a[172:175],v1 offset:8960
v_cvt_pk_bf16_f32 v194,v196,v197
v_mfma_f32_16x16x32_bf16 v[248:251],a[176:179],a[248:251],v[248:251]
v_cvt_pk_bf16_f32 v196,v200,v201
v_cvt_pk_bf16_f32 v197,v202,v203
v_mfma_f32_16x16x32_bf16 v[228:231],a[184:187],a[224:227],v[228:231]
ds_read_b128 a[180:183],v1 offset:12544
v_cvt_pk_bf16_f32 v195,v198,v199
v_mfma_f32_16x16x32_bf16 v[228:231],a[188:191],a[228:231],v[228:231]
v_cvt_pk_bf16_f32 v198,v204,v205
v_cvt_pk_bf16_f32 v199,v206,v207
v_mfma_f32_16x16x32_bf16 v[236:239],a[188:191],a[236:239],v[236:239]
ds_read_b128 a[176:179],v1 offset:12288
v_cvt_pk_bf16_f32 v208,v208,v209
v_mfma_f32_16x16x32_bf16 v[236:239],a[184:187],a[232:235],v[236:239]
v_cvt_pk_bf16_f32 v209,v210,v211
v_cvt_pk_bf16_f32 v210,v212,v213
v_mfma_f32_16x16x32_bf16 v[244:247],a[184:187],a[240:243],v[244:247]
v_cvt_pk_bf16_f32 v212,v216,v217
v_cvt_pk_bf16_f32 v213,v218,v219
v_mfma_f32_16x16x32_bf16 v[244:247],a[188:191],a[244:247],v[244:247]
v_cvt_pk_bf16_f32 v211,v214,v215
v_cvt_pk_bf16_f32 v214,v220,v221
v_mfma_f32_16x16x32_bf16 v[252:255],a[188:191],a[252:255],v[252:255]
buffer_load_dwordx4 v5,s[20:23],s47 offen offset:2048 nt lds
v_mfma_f32_16x16x32_bf16 v[252:255],a[184:187],a[248:251],v[252:255]
s_waitcnt lgkmcnt(2)
ds_read_b128 a[188:191],v1 offset:13056
v_cvt_pk_bf16_f32 v215,v222,v223
v_mfma_f32_16x16x32_bf16 v[32:35],v[48:51],v[64:67],0
ds_read_b64_tr_b16 a[214:215],v2 offset:12544
v_mfma_f32_16x16x32_bf16 v[32:35],v[52:55],v[68:71],v[32:35]
ds_read_b64_tr_b16 a[220:221],v3 offset:8448
v_mfma_f32_16x16x32_bf16 v[32:35],v[56:59],v[72:75],v[32:35]
ds_read_b64_tr_b16 a[222:223],v3 offset:12544
v_mfma_f32_16x16x32_bf16 v[32:35],v[60:63],v[76:79],v[32:35]
ds_read_b128 a[184:187],v1 offset:12800
v_add_u32 v1,s48,v1
v_mfma_f32_16x16x32_bf16 v[36:39],v[48:51],v[80:83],0
ds_read_b64_tr_b16 a[216:217],v3 offset:256
v_mfma_f32_16x16x32_bf16 v[36:39],v[52:55],v[84:87],v[36:39]
ds_read_b64_tr_b16 a[218:219],v3 offset:4352
v_mfma_f32_16x16x32_bf16 v[36:39],v[56:59],v[88:91],v[36:39]
ds_read_b64_tr_b16 a[224:225],v2 offset:512
v_mfma_f32_16x16x32_bf16 v[36:39],v[60:63],v[92:95],v[36:39]
)A"
R"A(ds_read_b64_tr_b16 a[226:227],v2 offset:4608
ds_read_b64_tr_b16 a[228:229],v2 offset:8704
v_mfma_f32_16x16x32_bf16 v[40:43],v[48:51],v[96:99],0
ds_read_b64_tr_b16 a[230:231],v2 offset:12800
v_mfma_f32_16x16x32_bf16 v[40:43],v[52:55],v[100:103],v[40:43]
v_max3_f32 v32,v32,v33,v34
v_max3_f32 v32,v32,v35,s18
v_mfma_f32_16x16x32_bf16 v[40:43],v[56:59],v[104:107],v[40:43]
v_sub_f32 v35,0,v32
v_sub_f32 v34,0,v32
v_mfma_f32_16x16x32_bf16 v[40:43],v[60:63],v[108:111],v[40:43]
v_sub_f32 v33,0,v32
v_sub_f32 v32,0,v32
v_mfma_f32_16x16x32_bf16 v[44:47],v[48:51],v[112:115],0
buffer_load_dwordx4 v5,s[20:23],s47 offen offset:3072 nt lds
v_mfma_f32_16x16x32_bf16 v[44:47],v[52:55],v[116:119],v[44:47]
v_max3_f32 v36,v36,v37,v38
v_max3_f32 v36,v36,v39,s19
v_mfma_f32_16x16x32_bf16 v[44:47],v[56:59],v[120:123],v[44:47]
v_sub_f32 v39,0,v36
v_sub_f32 v38,0,v36
v_mfma_f32_16x16x32_bf16 v[44:47],v[60:63],v[124:127],v[44:47]
v_sub_f32 v37,0,v36
v_sub_f32 v36,0,v36
v_mfma_f32_16x16x32_bf16 v[136:139],a[128:131],v[64:67],v[32:35]
ds_read_b64_tr_b16 a[236:237],v3 offset:8704
ds_read_b64_tr_b16 a[238:239],v3 offset:12800
v_mfma_f32_16x16x32_bf16 v[136:139],a[132:135],v[68:71],v[136:139]
v_max3_f32 v40,v40,v41,v42
v_max3_f32 v40,v40,v43,s100
v_mfma_f32_16x16x32_bf16 v[136:139],a[136:139],v[72:75],v[136:139]
v_sub_f32 v43,0,v40
v_sub_f32 v42,0,v40
v_mfma_f32_16x16x32_bf16 v[136:139],a[140:143],v[76:79],v[136:139]
v_sub_f32 v41,0,v40
v_sub_f32 v40,0,v40
v_mfma_f32_16x16x32_bf16 v[140:143],a[156:159],v[76:79],v[32:35]
ds_read_b64_tr_b16 a[232:233],v3 offset:512
ds_read_b64_tr_b16 a[234:235],v3 offset:4608
v_mfma_f32_16x16x32_bf16 v[140:143],a[152:155],v[72:75],v[140:143]
s_add_u32 m0,32768,s52
ds_read_b64_tr_b16 a[240:241],v2 offset:768
v_max3_f32 v44,v44,v45,v46
v_mfma_f32_16x16x32_bf16 v[140:143],a[148:151],v[68:71],v[140:143]
ds_read_b64_tr_b16 a[242:243],v2 offset:4864
v_max3_f32 v44,v44,v47,s101
v_mfma_f32_16x16x32_bf16 v[140:143],a[144:147],v[64:67],v[140:143]
ds_read_b64_tr_b16 a[244:245],v2 offset:8960
v_mov_b32 v48,0
v_mfma_f32_16x16x32_bf16 v[152:155],a[160:163],v[64:67],v[32:35]
buffer_load_dwordx4 v4,s[24:27],s47 offen nt lds
v_mfma_f32_16x16x32_bf16 v[152:155],a[164:167],v[68:71],v[152:155]
v_exp_f32 v136,v136
v_mfma_f32_16x16x32_bf16 v[152:155],a[168:171],v[72:75],v[152:155]
v_exp_f32 v137,v137
v_mfma_f32_16x16x32_bf16 v[152:155],a[172:175],v[76:79],v[152:155]
s_waitcnt lgkmcnt(5)
v_exp_f32 v138,v138
v_mfma_f32_16x16x32_bf16 v[156:159],a[188:191],v[76:79],v[32:35]
v_exp_f32 v139,v139
v_mfma_f32_16x16x32_bf16 v[156:159],a[184:187],v[72:75],v[156:159]
v_cvt_pk_bf16_f32 v136,v136,v137
v_cvt_pk_bf16_f32 v137,v138,v139
v_mfma_f32_16x16x32_bf16 v[156:159],a[180:183],v[68:71],v[156:159]
v_exp_f32 v140,v140
v_mfma_f32_16x16x32_bf16 v[156:159],a[176:179],v[64:67],v[156:159]
v_exp_f32 v141,v141
v_mfma_f32_16x16x32_bf16 v[168:171],a[128:131],v[80:83],v[36:39]
v_exp_f32 v142,v142
v_mfma_f32_16x16x32_bf16 v[168:171],a[132:135],v[84:87],v[168:171]
v_exp_f32 v143,v143
v_mfma_f32_16x16x32_bf16 v[168:171],a[136:139],v[88:91],v[168:171]
v_cvt_pk_bf16_f32 v138,v140,v141
v_cvt_pk_bf16_f32 v139,v142,v143
v_mfma_f32_16x16x32_bf16 v[168:171],a[140:143],v[92:95],v[168:171]
v_exp_f32 v152,v152
v_mfma_f32_16x16x32_bf16 v[172:175],a[156:159],v[92:95],v[36:39]
v_exp_f32 v153,v153
v_mfma_f32_16x16x32_bf16 v[172:175],a[152:155],v[88:91],v[172:175]
buffer_load_dwordx4 v5,s[24:27],s47 offen offset:1024 nt lds
v_mfma_f32_16x16x32_bf16 v[172:175],a[148:151],v[84:87],v[172:175]
v_exp_f32 v154,v154
v_mfma_f32_16x16x32_bf16 v[172:175],a[144:147],v[80:83],v[172:175]
v_exp_f32 v155,v155
v_mfma_f32_16x16x32_bf16 v[184:187],a[160:163],v[80:83],v[36:39]
v_exp_f32 v156,v156
v_mfma_f32_16x16x32_bf16 v[184:187],a[164:167],v[84:87],v[184:187]
v_exp_f32 v157,v157
v_mfma_f32_16x16x32_bf16 v[184:187],a[168:171],v[88:91],v[184:187]
v_exp_f32 v158,v158
v_mfma_f32_16x16x32_bf16 v[184:187],a[172:175],v[92:95],v[184:187]
v_exp_f32 v159,v159
v_mfma_f32_16x16x32_bf16 v[188:191],a[188:191],v[92:95],v[36:39]
v_cvt_pk_bf16_f32 v140,v152,v153
v_cvt_pk_bf16_f32 v141,v154,v155
v_mfma_f32_16x16x32_bf16 v[188:191],a[184:187],v[88:91],v[188:191]
v_cvt_pk_bf16_f32 v142,v156,v157
v_cvt_pk_bf16_f32 v143,v158,v159
v_mfma_f32_16x16x32_bf16 v[188:191],a[180:183],v[84:87],v[188:191]
ds_read_b64_tr_b16 a[246:247],v2 offset:13056
ds_read_b64_tr_b16 a[252:253],v3 offset:8960
v_mfma_f32_16x16x32_bf16 v[188:191],a[176:179],v[80:83],v[188:191]
ds_read_b64_tr_b16 a[254:255],v3 offset:13056
v_mov_b32 v49,0
v_mfma_f32_16x16x32_bf16 v[200:203],a[128:131],v[96:99],v[40:43]
ds_read_b64_tr_b16 a[248:249],v3 offset:768
v_mov_b32 v50,0
v_mfma_f32_16x16x32_bf16 v[200:203],a[132:135],v[100:103],v[200:203]
ds_read_b64_tr_b16 a[250:251],v3 offset:4864
v_mov_b32 v51,0
v_mfma_f32_16x16x32_bf16 v[200:203],a[136:139],v[104:107],v[200:203]
buffer_load_dwordx4 v4,s[24:27],s47 offen offset:2048 nt lds
v_mfma_f32_16x16x32_bf16 v[200:203],a[140:143],v[108:111],v[200:203]
v_sub_f32 v47,0,v44
v_sub_f32 v46,0,v44
v_mfma_f32_16x16x32_bf16 v[204:207],a[156:159],v[108:111],v[40:43]
v_sub_f32 v45,0,v44
v_sub_f32 v44,0,v44
v_mfma_f32_16x16x32_bf16 v[204:207],a[152:155],v[104:107],v[204:207]
v_mul_f32 v224,s61,v224
v_mul_f32 v225,s61,v225
v_mul_f32 v226,s61,v226
v_mul_f32 v227,s61,v227
v_cvt_pk_bf16_f32 v224,v224,v225
v_cvt_pk_bf16_f32 v225,v226,v227
v_pk_mul_f32 v[228:229],s[60:61],v[228:229] op_sel:[1,0] op_sel_hi:[1,1]
v_pk_mul_f32 v[230:231],s[60:61],v[230:231] op_sel:[1,0] op_sel_hi:[1,1]
v_pk_mul_f32 v[232:233],s[60:61],v[232:233] op_sel:[1,0] op_sel_hi:[1,1]
v_pk_mul_f32 v[234:235],s[60:61],v[234:235] op_sel:[1,0] op_sel_hi:[1,1]
v_pk_mul_f32 v[236:237],s[60:61],v[236:237] op_sel:[1,0] op_sel_hi:[1,1]
v_pk_mul_f32 v[238:239],s[60:61],v[238:239] op_sel:[1,0] op_sel_hi:[1,1]
v_pk_mul_f32 v[240:241],s[60:61],v[240:241] op_sel:[1,0] op_sel_hi:[1,1]
v_pk_mul_f32 v[242:243],s[60:61],v[242:243] op_sel:[1,0] op_sel_hi:[1,1]
v_pk_mul_f32 v[244:245],s[60:61],v[244:245] op_sel:[1,0] op_sel_hi:[1,1]
v_pk_mul_f32 v[246:247],s[60:61],v[246:247] op_sel:[1,0] op_sel_hi:[1,1]
v_pk_mul_f32 v[248:249],s[60:61],v[248:249] op_sel:[1,0] op_sel_hi:[1,1]
v_pk_mul_f32 v[250:251],s[60:61],v[250:251] op_sel:[1,0] op_sel_hi:[1,1]
v_pk_mul_f32 v[252:253],s[60:61],v[252:253] op_sel:[1,0] op_sel_hi:[1,1]
v_pk_mul_f32 v[254:255],s[60:61],v[254:255] op_sel:[1,0] op_sel_hi:[1,1]
v_mfma_f32_16x16x32_bf16 v[204:207],a[148:151],v[100:103],v[204:207]
v_cvt_pk_bf16_f32 v226,v228,v229
v_cvt_pk_bf16_f32 v228,v232,v233
v_mfma_f32_16x16x32_bf16 v[204:207],a[144:147],v[96:99],v[204:207]
v_cvt_pk_bf16_f32 v229,v234,v235
v_cvt_pk_bf16_f32 v227,v230,v231
v_mfma_f32_16x16x32_bf16 v[216:219],a[160:163],v[96:99],v[40:43]
v_cvt_pk_bf16_f32 v230,v236,v237
v_cvt_pk_bf16_f32 v231,v238,v239
v_mfma_f32_16x16x32_bf16 v[216:219],a[164:167],v[100:103],v[216:219]
v_cvt_pk_bf16_f32 v240,v240,v241
v_cvt_pk_bf16_f32 v241,v242,v243
v_mfma_f32_16x16x32_bf16 v[216:219],a[168:171],v[104:107],v[216:219]
v_cvt_pk_bf16_f32 v242,v244,v245
v_cvt_pk_bf16_f32 v244,v248,v249
v_mfma_f32_16x16x32_bf16 v[216:219],a[172:175],v[108:111],v[216:219]
v_cvt_pk_bf16_f32 v245,v250,v251
v_cvt_pk_bf16_f32 v243,v246,v247
v_mfma_f32_16x16x32_bf16 v[220:223],a[188:191],v[108:111],v[40:43]
v_cvt_pk_bf16_f32 v246,v252,v253
v_cvt_pk_bf16_f32 v247,v254,v255
v_mfma_f32_16x16x32_bf16 v[220:223],a[184:187],v[104:107],v[220:223]
v_exp_f32 v168,v168
v_mfma_f32_16x16x32_bf16 v[220:223],a[180:183],v[100:103],v[220:223]
v_exp_f32 v169,v169
v_mfma_f32_16x16x32_bf16 v[220:223],a[176:179],v[96:99],v[220:223]
buffer_load_dwordx4 v5,s[24:27],s47 offen offset:3072 nt lds
v_mfma_f32_16x16x32_bf16 a[0:3],a[192:195],v[136:139],0
s_or_b32 s47,s47,0x18000
s_add_u32 s47,s47,4096
s_and_b32 s47,s47,0x27000
v_mfma_f32_16x16x32_bf16 a[0:3],a[196:199],v[140:143],a[0:3]
s_or_b32 s47,s47,s46
v_exp_f32 v170,v170
v_mfma_f32_16x16x32_bf16 a[4:7],a[204:207],v[140:143],0
s_and_b32 s46,s54,0xfffc0000
v_exp_f32 v171,v171
v_mfma_f32_16x16x32_bf16 a[4:7],a[200:203],v[136:139],a[4:7]
s_memrealtime s[76:77]
v_cvt_pk_bf16_f32 v168,v168,v169
v_cvt_pk_bf16_f32 v169,v170,v171
v_mfma_f32_16x16x32_bf16 a[8:11],a[208:211],v[136:139],0
v_exp_f32 v172,v172
v_mfma_f32_16x16x32_bf16 a[8:11],a[212:215],v[140:143],a[8:11]
v_exp_f32 v173,v173
v_mfma_f32_16x16x32_bf16 a[12:15],a[220:223],v[140:143],0
v_exp_f32 v174,v174
v_mfma_f32_16x16x32_bf16 a[12:15],a[216:219],v[136:139],a[12:15]
v_exp_f32 v175,v175
v_mfma_f32_16x16x32_bf16 a[16:19],a[224:227],v[136:139],0
v_cvt_pk_bf16_f32 v170,v172,v173
v_cvt_pk_bf16_f32 v171,v174,v175
v_mfma_f32_16x16x32_bf16 a[16:19],a[228:231],v[140:143],a[16:19]
v_exp_f32 v184,v184
v_mfma_f32_16x16x32_bf16 a[20:23],a[236:239],v[140:143],0
s_waitcnt lgkmcnt(0)
v_exp_f32 v185,v185
v_mfma_f32_16x16x32_bf16 a[20:23],a[232:235],v[136:139],a[20:23]
s_add_u32 s78,s76,s60
v_exp_f32 v186,v186
v_mfma_f32_16x16x32_bf16 a[24:27],a[240:243],v[136:139],0
s_lshr_b32 s60,s94,2
v_exp_f32 v187,v187
v_mfma_f32_16x16x32_bf16 a[24:27],a[244:247],v[140:143],a[24:27]
s_max_i32 s60,s60,128
v_exp_f32 v188,v188
v_mfma_f32_16x16x32_bf16 a[28:31],a[252:255],v[140:143],0
v_exp_f32 v189,v189
v_mfma_f32_16x16x32_bf16 a[28:31],a[248:251],v[136:139],a[28:31]
v_exp_f32 v190,v190
v_smfmac_f32_16x16x64_bf16 v[48:51],v[28:31],v[136:143],v27
v_cvt_pk_bf16_f32 v172,v184,v185
v_mfma_f32_16x16x32_bf16 v[232:235],a[128:131],v[112:115],v[44:47]
s_waitcnt vmcnt(8)
v_exp_f32 v191,v191
v_mfma_f32_16x16x32_bf16 v[232:235],a[132:135],v[116:119],v[232:235]
s_barrier
s_add_u32 m0,65536,s51
v_cvt_pk_bf16_f32 v173,v186,v187
v_mfma_f32_16x16x32_bf16 v[232:235],a[136:139],v[120:123],v[232:235]
v_cvt_pk_bf16_f32 v174,v188,v189
v_cvt_pk_bf16_f32 v175,v190,v191
v_mfma_f32_16x16x32_bf16 v[232:235],a[140:143],v[124:127],v[232:235]
ds_read_b128 a[128:131],v1
v_mov_b32 v52,0
v_mfma_f32_16x16x32_bf16 v[236:239],a[156:159],v[124:127],v[44:47]
buffer_load_dwordx4 v4,s[20:23],s47 offen nt lds
v_mfma_f32_16x16x32_bf16 v[236:239],a[152:155],v[120:123],v[236:239]
ds_read_b128 a[132:135],v1 offset:256
v_mov_b32 v53,0
v_mfma_f32_16x16x32_bf16 v[236:239],a[148:151],v[116:119],v[236:239]
v_mov_b32 v54,0
v_mov_b32 v55,0
v_mfma_f32_16x16x32_bf16 v[236:239],a[144:147],v[112:115],v[236:239]
ds_read_b128 a[136:139],v1 offset:512
v_mov_b32 v56,0
v_mfma_f32_16x16x32_bf16 v[248:251],a[160:163],v[112:115],v[44:47]
v_exp_f32 v200,v200
v_mfma_f32_16x16x32_bf16 v[248:251],a[164:167],v[116:119],v[248:251]
ds_read_b128 a[140:143],v1 offset:768
v_mov_b32 v57,0
v_mfma_f32_16x16x32_bf16 v[248:251],a[168:171],v[120:123],v[248:251]
v_exp_f32 v201,v201
v_mfma_f32_16x16x32_bf16 v[248:251],a[172:175],v[124:127],v[248:251]
ds_read_b128 a[156:159],v1 offset:4864
v_cvt_pk_bf16_f32 v200,v200,v201
v_mfma_f32_16x16x32_bf16 v[252:255],a[188:191],v[124:127],v[44:47]
v_exp_f32 v202,v202
v_mfma_f32_16x16x32_bf16 v[252:255],a[184:187],v[120:123],v[252:255]
ds_read_b128 a[152:155],v1 offset:4608
v_mov_b32 v58,0
v_mfma_f32_16x16x32_bf16 v[252:255],a[180:183],v[116:119],v[252:255]
v_exp_f32 v203,v203
v_mfma_f32_16x16x32_bf16 v[252:255],a[176:179],v[112:115],v[252:255]
ds_read_b128 a[148:151],v1 offset:4352
v_cvt_pk_bf16_f32 v201,v202,v203
v_mfma_f32_16x16x32_bf16 a[32:35],a[192:195],v[168:171],0
v_exp_f32 v204,v204
v_mfma_f32_16x16x32_bf16 a[32:35],a[196:199],v[172:175],a[32:35]
ds_read_b128 a[144:147],v1 offset:4096
v_mov_b32 v59,0
v_mfma_f32_16x16x32_bf16 a[36:39],a[204:207],v[172:175],0
buffer_load_dwordx4 v4,s[20:23],s47 offen offset:1024 nt lds
v_mfma_f32_16x16x32_bf16 a[36:39],a[200:203],v[168:171],a[36:39]
ds_read_b128 a[160:163],v1 offset:8192
v_mov_b32 v60,0
v_mfma_f32_16x16x32_bf16 a[40:43],a[208:211],v[168:171],0
v_exp_f32 v205,v205
v_mfma_f32_16x16x32_bf16 a[40:43],a[212:215],v[172:175],a[40:43]
ds_read_b128 a[164:167],v1 offset:8448
v_cvt_pk_bf16_f32 v202,v204,v205
v_mfma_f32_16x16x32_bf16 a[44:47],a[220:223],v[172:175],0
v_exp_f32 v206,v206
v_mfma_f32_16x16x32_bf16 a[44:47],a[216:219],v[168:171],a[44:47]
ds_read_b128 a[168:171],v1 offset:8704
v_mov_b32 v61,0
v_mfma_f32_16x16x32_bf16 a[48:51],a[224:227],v[168:171],0
)A"
R"A(v_exp_f32 v207,v207
v_mfma_f32_16x16x32_bf16 a[48:51],a[228:231],v[172:175],a[48:51]
ds_read_b128 a[172:175],v1 offset:8960
v_cvt_pk_bf16_f32 v203,v206,v207
v_mfma_f32_16x16x32_bf16 a[52:55],a[236:239],v[172:175],0
v_exp_f32 v216,v216
v_mfma_f32_16x16x32_bf16 a[52:55],a[232:235],v[168:171],a[52:55]
ds_read_b128 a[188:191],v1 offset:13056
v_mov_b32 v62,0
v_mfma_f32_16x16x32_bf16 a[56:59],a[240:243],v[168:171],0
v_exp_f32 v217,v217
v_mfma_f32_16x16x32_bf16 a[56:59],a[244:247],v[172:175],a[56:59]
ds_read_b128 a[184:187],v1 offset:12800
v_cvt_pk_bf16_f32 v204,v216,v217
v_mfma_f32_16x16x32_bf16 a[60:63],a[252:255],v[172:175],0
v_exp_f32 v218,v218
v_mfma_f32_16x16x32_bf16 a[60:63],a[248:251],v[168:171],a[60:63]
ds_read_b128 a[180:183],v1 offset:12544
v_mov_b32 v63,0
v_smfmac_f32_16x16x64_bf16 v[52:55],v[28:31],v[168:175],v27
s_waitcnt lgkmcnt(4)
v_add_u32 v2,s48,v2
v_mfma_f32_16x16x32_bf16 v[136:139],a[128:131],v[64:67],v[32:35]
buffer_load_dwordx4 v5,s[20:23],s47 offen offset:2048 nt lds
v_mfma_f32_16x16x32_bf16 v[136:139],a[132:135],v[68:71],v[136:139]
ds_read_b128 a[176:179],v1 offset:12288
v_add_u32 v3,s48,v3
v_mfma_f32_16x16x32_bf16 v[136:139],a[136:139],v[72:75],v[136:139]
s_mov_b32 s48,32768
v_exp_f32 v219,v219
v_mfma_f32_16x16x32_bf16 v[136:139],a[140:143],v[76:79],v[136:139]
v_exp_f32 v220,v220
v_mfma_f32_16x16x32_bf16 v[140:143],a[156:159],v[76:79],v[32:35]
v_exp_f32 v221,v221
v_mfma_f32_16x16x32_bf16 v[140:143],a[152:155],v[72:75],v[140:143]
v_exp_f32 v222,v222
v_mfma_f32_16x16x32_bf16 v[140:143],a[148:151],v[68:71],v[140:143]
v_exp_f32 v223,v223
v_mfma_f32_16x16x32_bf16 v[140:143],a[144:147],v[64:67],v[140:143]
v_cvt_pk_bf16_f32 v205,v218,v219
v_cvt_pk_bf16_f32 v206,v220,v221
v_mfma_f32_16x16x32_bf16 v[152:155],a[160:163],v[64:67],v[32:35]
v_cvt_pk_bf16_f32 v207,v222,v223
v_add_u32 v1,s48,v1
v_mfma_f32_16x16x32_bf16 v[152:155],a[164:167],v[68:71],v[152:155]
v_exp_f32 v232,v232
v_mfma_f32_16x16x32_bf16 v[152:155],a[168:171],v[72:75],v[152:155]
s_waitcnt lgkmcnt(0)
v_exp_f32 v233,v233
v_mfma_f32_16x16x32_bf16 v[152:155],a[172:175],v[76:79],v[152:155]
v_exp_f32 v234,v234
v_mfma_f32_16x16x32_bf16 v[156:159],a[188:191],v[76:79],v[32:35]
v_exp_f32 v235,v235
v_mfma_f32_16x16x32_bf16 v[156:159],a[184:187],v[72:75],v[156:159]
buffer_load_dwordx4 v5,s[20:23],s47 offen offset:3072 nt lds
v_mfma_f32_16x16x32_bf16 v[156:159],a[180:183],v[68:71],v[156:159]
v_exp_f32 v236,v236
v_mfma_f32_16x16x32_bf16 v[156:159],a[176:179],v[64:67],v[156:159]
v_exp_f32 v237,v237
v_mfma_f32_16x16x32_bf16 a[64:67],a[192:195],v[200:203],0
v_exp_f32 v238,v238
v_mfma_f32_16x16x32_bf16 a[64:67],a[196:199],v[204:207],a[64:67]
v_exp_f32 v239,v239
v_mfma_f32_16x16x32_bf16 a[68:71],a[204:207],v[204:207],0
v_exp_f32 v248,v248
v_mfma_f32_16x16x32_bf16 a[68:71],a[200:203],v[200:203],a[68:71]
v_exp_f32 v249,v249
v_mfma_f32_16x16x32_bf16 a[72:75],a[208:211],v[200:203],0
v_exp_f32 v250,v250
v_mfma_f32_16x16x32_bf16 a[72:75],a[212:215],v[204:207],a[72:75]
v_exp_f32 v251,v251
v_mfma_f32_16x16x32_bf16 a[76:79],a[220:223],v[204:207],0
v_exp_f32 v252,v252
v_mfma_f32_16x16x32_bf16 a[76:79],a[216:219],v[200:203],a[76:79]
v_exp_f32 v253,v253
v_mfma_f32_16x16x32_bf16 a[80:83],a[224:227],v[200:203],0
s_add_u32 m0,65536,s52
v_exp_f32 v254,v254
v_mfma_f32_16x16x32_bf16 a[80:83],a[228:231],v[204:207],a[80:83]
v_exp_f32 v255,v255
v_mfma_f32_16x16x32_bf16 a[84:87],a[236:239],v[204:207],0
v_exp_f32 v136,v136
v_mfma_f32_16x16x32_bf16 a[84:87],a[232:235],v[200:203],a[84:87]
buffer_load_dwordx4 v4,s[24:27],s47 offen nt lds
v_mfma_f32_16x16x32_bf16 a[88:91],a[240:243],v[200:203],0
v_cvt_pk_bf16_f32 v232,v232,v233
v_cvt_pk_bf16_f32 v233,v234,v235
v_mfma_f32_16x16x32_bf16 a[88:91],a[244:247],v[204:207],a[88:91]
v_cvt_pk_bf16_f32 v234,v236,v237
v_cvt_pk_bf16_f32 v235,v238,v239
v_mfma_f32_16x16x32_bf16 a[92:95],a[252:255],v[204:207],0
v_cvt_pk_bf16_f32 v236,v248,v249
v_cvt_pk_bf16_f32 v237,v250,v251
v_mfma_f32_16x16x32_bf16 a[92:95],a[248:251],v[200:203],a[92:95]
v_cvt_pk_bf16_f32 v238,v252,v253
v_cvt_pk_bf16_f32 v239,v254,v255
v_smfmac_f32_16x16x64_bf16 v[56:59],v[28:31],v[200:207],v27
v_mfma_f32_16x16x32_bf16 v[168:171],a[128:131],v[80:83],v[36:39]
v_exp_f32 v137,v137
v_mfma_f32_16x16x32_bf16 v[168:171],a[132:135],v[84:87],v[168:171]
v_exp_f32 v138,v138
v_mfma_f32_16x16x32_bf16 v[168:171],a[136:139],v[88:91],v[168:171]
v_exp_f32 v139,v139
v_mfma_f32_16x16x32_bf16 v[168:171],a[140:143],v[92:95],v[168:171]
v_exp_f32 v140,v140
v_mfma_f32_16x16x32_bf16 v[172:175],a[156:159],v[92:95],v[36:39]
v_exp_f32 v141,v141
v_mfma_f32_16x16x32_bf16 v[172:175],a[152:155],v[88:91],v[172:175]
v_exp_f32 v142,v142
v_mfma_f32_16x16x32_bf16 v[172:175],a[148:151],v[84:87],v[172:175]
v_exp_f32 v143,v143
v_mfma_f32_16x16x32_bf16 v[172:175],a[144:147],v[80:83],v[172:175]
v_exp_f32 v152,v152
v_mfma_f32_16x16x32_bf16 v[184:187],a[160:163],v[80:83],v[36:39]
buffer_load_dwordx4 v5,s[24:27],s47 offen offset:1024 nt lds
v_mfma_f32_16x16x32_bf16 v[184:187],a[164:167],v[84:87],v[184:187]
v_exp_f32 v153,v153
v_mfma_f32_16x16x32_bf16 v[184:187],a[168:171],v[88:91],v[184:187]
v_exp_f32 v154,v154
v_mfma_f32_16x16x32_bf16 v[184:187],a[172:175],v[92:95],v[184:187]
v_exp_f32 v155,v155
v_mfma_f32_16x16x32_bf16 v[188:191],a[188:191],v[92:95],v[36:39]
v_exp_f32 v156,v156
v_mfma_f32_16x16x32_bf16 v[188:191],a[184:187],v[88:91],v[188:191]
v_exp_f32 v157,v157
v_mfma_f32_16x16x32_bf16 v[188:191],a[180:183],v[84:87],v[188:191]
v_exp_f32 v158,v158
v_mfma_f32_16x16x32_bf16 v[188:191],a[176:179],v[80:83],v[188:191]
v_exp_f32 v159,v159
v_mfma_f32_16x16x32_bf16 a[96:99],a[192:195],v[232:235],0
ds_read_b64_tr_b16 a[192:193],v2
ds_read_b64_tr_b16 a[194:195],v2 offset:4096
v_mfma_f32_16x16x32_bf16 a[96:99],a[196:199],v[236:239],a[96:99]
ds_read_b64_tr_b16 a[196:197],v2 offset:8192
v_cvt_pk_bf16_f32 v136,v136,v137
v_mfma_f32_16x16x32_bf16 a[100:103],a[204:207],v[236:239],0
ds_read_b64_tr_b16 a[198:199],v2 offset:12288
v_cvt_pk_bf16_f32 v137,v138,v139
v_mfma_f32_16x16x32_bf16 a[100:103],a[200:203],v[232:235],a[100:103]
ds_read_b64_tr_b16 a[204:205],v3 offset:8192
v_cvt_pk_bf16_f32 v138,v140,v141
v_mfma_f32_16x16x32_bf16 a[104:107],a[208:211],v[232:235],0
ds_read_b64_tr_b16 a[206:207],v3 offset:12288
ds_read_b64_tr_b16 a[200:201],v3
v_mfma_f32_16x16x32_bf16 a[104:107],a[212:215],v[236:239],a[104:107]
ds_read_b64_tr_b16 a[202:203],v3 offset:4096
v_cvt_pk_bf16_f32 v139,v142,v143
v_mfma_f32_16x16x32_bf16 a[108:111],a[220:223],v[236:239],0
buffer_load_dwordx4 v4,s[24:27],s47 offen offset:2048 nt lds
v_mfma_f32_16x16x32_bf16 a[108:111],a[216:219],v[232:235],a[108:111]
ds_read_b64_tr_b16 a[208:209],v2 offset:256
ds_read_b64_tr_b16 a[210:211],v2 offset:4352
v_mfma_f32_16x16x32_bf16 a[112:115],a[224:227],v[232:235],0
ds_read_b64_tr_b16 a[212:213],v2 offset:8448
v_cvt_pk_bf16_f32 v140,v152,v153
v_mfma_f32_16x16x32_bf16 a[112:115],a[228:231],v[236:239],a[112:115]
ds_read_b64_tr_b16 a[214:215],v2 offset:12544
v_cvt_pk_bf16_f32 v141,v154,v155
v_mfma_f32_16x16x32_bf16 a[116:119],a[236:239],v[236:239],0
ds_read_b64_tr_b16 a[220:221],v3 offset:8448
v_cvt_pk_bf16_f32 v142,v156,v157
v_mfma_f32_16x16x32_bf16 a[116:119],a[232:235],v[232:235],a[116:119]
ds_read_b64_tr_b16 a[222:223],v3 offset:12544
v_cvt_pk_bf16_f32 v143,v158,v159
v_mfma_f32_16x16x32_bf16 a[120:123],a[240:243],v[232:235],0
ds_read_b64_tr_b16 a[216:217],v3 offset:256
ds_read_b64_tr_b16 a[218:219],v3 offset:4352
v_mfma_f32_16x16x32_bf16 a[120:123],a[244:247],v[236:239],a[120:123]
v_exp_f32 v168,v168
v_mfma_f32_16x16x32_bf16 a[124:127],a[252:255],v[236:239],0
ds_read_b64_tr_b16 a[224:225],v2 offset:512
ds_read_b64_tr_b16 a[226:227],v2 offset:4608
v_mfma_f32_16x16x32_bf16 a[124:127],a[248:251],v[232:235],a[124:127]
v_exp_f32 v169,v169
v_smfmac_f32_16x16x64_bf16 v[60:63],v[28:31],v[232:239],v27
ds_read_b64_tr_b16 a[228:229],v2 offset:8704
v_mfma_f32_16x16x32_bf16 v[200:203],a[128:131],v[96:99],v[40:43]
ds_read_b64_tr_b16 a[230:231],v2 offset:12800
ds_read_b64_tr_b16 a[236:237],v3 offset:8704
v_mfma_f32_16x16x32_bf16 v[200:203],a[132:135],v[100:103],v[200:203]
ds_read_b64_tr_b16 a[238:239],v3 offset:12800
v_cvt_pk_bf16_f32 v168,v168,v169
v_mfma_f32_16x16x32_bf16 v[200:203],a[136:139],v[104:107],v[200:203]
ds_read_b64_tr_b16 a[232:233],v3 offset:512
v_mfma_f32_16x16x32_bf16 v[200:203],a[140:143],v[108:111],v[200:203]
buffer_load_dwordx4 v5,s[24:27],s47 offen offset:3072 nt lds
v_mfma_f32_16x16x32_bf16 v[204:207],a[156:159],v[108:111],v[40:43]
s_or_b32 s47,s47,0x18000
ds_read_b64_tr_b16 a[234:235],v3 offset:4608
ds_read_b64_tr_b16 a[240:241],v2 offset:768
v_mfma_f32_16x16x32_bf16 v[204:207],a[152:155],v[104:107],v[204:207]
s_add_u32 s47,s47,4096
s_and_b32 s47,s47,0x27000
ds_read_b64_tr_b16 a[242:243],v2 offset:4864
v_mfma_f32_16x16x32_bf16 v[204:207],a[148:151],v[100:103],v[204:207]
s_or_b32 s47,s47,s46
ds_read_b64_tr_b16 a[244:245],v2 offset:8960
v_mfma_f32_16x16x32_bf16 v[204:207],a[144:147],v[96:99],v[204:207]
ds_read_b64_tr_b16 a[246:247],v2 offset:13056
v_mfma_f32_16x16x32_bf16 v[216:219],a[160:163],v[96:99],v[40:43]
ds_read_b64_tr_b16 a[252:253],v3 offset:8960
ds_read_b64_tr_b16 a[254:255],v3 offset:13056
v_mfma_f32_16x16x32_bf16 v[216:219],a[164:167],v[100:103],v[216:219]
ds_read_b64_tr_b16 a[248:249],v3 offset:768
v_mfma_f32_16x16x32_bf16 v[216:219],a[168:171],v[104:107],v[216:219]
ds_read_b64_tr_b16 a[250:251],v3 offset:4864
v_mfma_f32_16x16x32_bf16 v[216:219],a[172:175],v[108:111],v[216:219]
v_exp_f32 v170,v170
v_mfma_f32_16x16x32_bf16 v[220:223],a[188:191],v[108:111],v[40:43]
v_exp_f32 v171,v171
v_mfma_f32_16x16x32_bf16 v[220:223],a[184:187],v[104:107],v[220:223]
v_exp_f32 v172,v172
v_mfma_f32_16x16x32_bf16 v[220:223],a[180:183],v[100:103],v[220:223]
v_exp_f32 v173,v173
v_mfma_f32_16x16x32_bf16 v[220:223],a[176:179],v[96:99],v[220:223]
s_waitcnt lgkmcnt(4)
v_exp_f32 v174,v174
v_mfma_f32_16x16x32_bf16 a[0:3],a[192:195],v[136:139],a[0:3]
v_exp_f32 v175,v175
v_mfma_f32_16x16x32_bf16 a[0:3],a[196:199],v[140:143],a[0:3]
v_cvt_pk_bf16_f32 v169,v170,v171
v_cvt_pk_bf16_f32 v170,v172,v173
v_mfma_f32_16x16x32_bf16 a[4:7],a[204:207],v[140:143],a[4:7]
v_exp_f32 v184,v184
v_mfma_f32_16x16x32_bf16 a[4:7],a[200:203],v[136:139],a[4:7]
s_memrealtime s[76:77]
v_exp_f32 v185,v185
v_mfma_f32_16x16x32_bf16 a[8:11],a[208:211],v[136:139],a[8:11]
v_exp_f32 v186,v186
v_mfma_f32_16x16x32_bf16 a[8:11],a[212:215],v[140:143],a[8:11]
v_exp_f32 v187,v187
v_mfma_f32_16x16x32_bf16 a[12:15],a[220:223],v[140:143],a[12:15]
v_exp_f32 v188,v188
v_mfma_f32_16x16x32_bf16 a[12:15],a[216:219],v[136:139],a[12:15]
v_exp_f32 v189,v189
v_mfma_f32_16x16x32_bf16 a[16:19],a[224:227],v[136:139],a[16:19]
v_exp_f32 v190,v190
v_mfma_f32_16x16x32_bf16 a[16:19],a[228:231],v[140:143],a[16:19]
v_exp_f32 v191,v191
v_mfma_f32_16x16x32_bf16 a[20:23],a[236:239],v[140:143],a[20:23]
s_waitcnt lgkmcnt(0)
v_cvt_pk_bf16_f32 v171,v174,v175
v_cvt_pk_bf16_f32 v172,v184,v185
v_mfma_f32_16x16x32_bf16 a[20:23],a[232:235],v[136:139],a[20:23]
s_sub_u32 s79,s78,s76
v_cvt_pk_bf16_f32 v173,v186,v187
v_cvt_pk_bf16_f32 v174,v188,v189
v_mfma_f32_16x16x32_bf16 a[24:27],a[240:243],v[136:139],a[24:27]
s_add_u32 s78,s76,s60
v_cvt_pk_bf16_f32 v175,v190,v191
v_mfma_f32_16x16x32_bf16 a[24:27],a[244:247],v[140:143],a[24:27]
s_cmp_gt_i32 s79,s95
s_cbranch_scc1 L_ps2_%=
L_pr2_%=:
v_mfma_f32_16x16x32_bf16 a[28:31],a[252:255],v[140:143],a[28:31]
v_mfma_f32_16x16x32_bf16 a[28:31],a[248:251],v[136:139],a[28:31]
v_smfmac_f32_16x16x64_bf16 v[48:51],v[28:31],v[136:143],v27
s_mov_b32 s53,13
s_cmp_eq_u32 s40,3
s_cbranch_scc0 L_att0_%=
s_cmp_gt_i32 s18,0
s_cbranch_scc1 L_att0v0_%=
s_cmp_gt_i32 s19,0
s_cbranch_scc1 L_att0v1_%=
s_cmp_gt_i32 s100,0
s_cbranch_scc1 L_att0v2_%=
.p2align 5
L_att0v3_%=:
s_waitcnt vmcnt(8) lgkmcnt(0)
v_exp_f32 v200,v200
s_barrier
s_add_u32 m0,s49,s51
ds_read_b128 a[128:131],v1
s_mov_b32 s90,L_endseg_%=-L_pc_%=+128
v_exp_f32 v201,v201
s_lshr_b32 s45,s45,1
ds_read_b128 a[132:135],v1 offset:256
v_cvt_pk_bf16_f32 v200,v200,v201
buffer_load_dwordx4 v4,s[20:23],s47 offen nt lds
ds_read_b128 a[136:139],v1 offset:512
v_add_u32 v2,s48,v2
)A"
R"A(v_exp_f32 v202,v202
s_add_u32 s62,s50,32768
ds_read_b128 a[140:143],v1 offset:768
v_add_u32 v3,s48,v3
s_and_b32 s62,s62,131071
v_exp_f32 v203,v203
s_add_u32 s54,s54,16384
ds_read_b128 a[156:159],v1 offset:4864
v_cvt_pk_bf16_f32 v201,v202,v203
s_sub_u32 s48,s62,s50
v_exp_f32 v204,v204
s_sub_u32 s53,s53,1
v_exp_f32 v205,v205
s_and_b32 s46,s54,0xfffc0000
ds_read_b128 a[152:155],v1 offset:4608
v_cvt_pk_bf16_f32 v202,v204,v205
s_mov_b32 s50,s62
v_exp_f32 v206,v206
v_exp_f32 v207,v207
ds_read_b128 a[148:151],v1 offset:4352
v_cvt_pk_bf16_f32 v203,v206,v207
v_mfma_f32_16x16x32_bf16 a[32:35],a[192:195],v[168:171],a[32:35]
v_exp_f32 v216,v216
v_mfma_f32_16x16x32_bf16 a[32:35],a[196:199],v[172:175],a[32:35]
ds_read_b128 a[144:147],v1 offset:4096
v_mfma_f32_16x16x32_bf16 a[36:39],a[204:207],v[172:175],a[36:39]
buffer_load_dwordx4 v4,s[20:23],s47 offen offset:1024 nt lds
v_mfma_f32_16x16x32_bf16 a[36:39],a[200:203],v[168:171],a[36:39]
ds_read_b128 a[160:163],v1 offset:8192
v_mfma_f32_16x16x32_bf16 a[40:43],a[208:211],v[168:171],a[40:43]
v_exp_f32 v217,v217
v_mfma_f32_16x16x32_bf16 a[40:43],a[212:215],v[172:175],a[40:43]
ds_read_b128 a[164:167],v1 offset:8448
v_cvt_pk_bf16_f32 v204,v216,v217
v_mfma_f32_16x16x32_bf16 a[44:47],a[220:223],v[172:175],a[44:47]
v_exp_f32 v218,v218
v_mfma_f32_16x16x32_bf16 a[44:47],a[216:219],v[168:171],a[44:47]
ds_read_b128 a[168:171],v1 offset:8704
v_mfma_f32_16x16x32_bf16 a[48:51],a[224:227],v[168:171],a[48:51]
v_exp_f32 v219,v219
v_mfma_f32_16x16x32_bf16 a[48:51],a[228:231],v[172:175],a[48:51]
ds_read_b128 a[172:175],v1 offset:8960
v_cvt_pk_bf16_f32 v205,v218,v219
v_mfma_f32_16x16x32_bf16 a[52:55],a[236:239],v[172:175],a[52:55]
v_exp_f32 v220,v220
v_mfma_f32_16x16x32_bf16 a[52:55],a[232:235],v[168:171],a[52:55]
ds_read_b128 a[188:191],v1 offset:13056
v_mfma_f32_16x16x32_bf16 a[56:59],a[240:243],v[168:171],a[56:59]
v_exp_f32 v221,v221
v_mfma_f32_16x16x32_bf16 a[56:59],a[244:247],v[172:175],a[56:59]
ds_read_b128 a[184:187],v1 offset:12800
v_cvt_pk_bf16_f32 v206,v220,v221
v_mfma_f32_16x16x32_bf16 a[60:63],a[252:255],v[172:175],a[60:63]
v_exp_f32 v222,v222
v_mfma_f32_16x16x32_bf16 a[60:63],a[248:251],v[168:171],a[60:63]
ds_read_b128 a[180:183],v1 offset:12544
v_smfmac_f32_16x16x64_bf16 v[52:55],v[28:31],v[168:175],v27
s_waitcnt lgkmcnt(4)
v_mfma_f32_16x16x32_bf16 v[136:139],a[128:131],v[64:67],v[32:35]
buffer_load_dwordx4 v5,s[20:23],s47 offen offset:2048 nt lds
v_mfma_f32_16x16x32_bf16 v[136:139],a[132:135],v[68:71],v[136:139]
ds_read_b128 a[176:179],v1 offset:12288
v_add_u32 v1,s48,v1
v_mfma_f32_16x16x32_bf16 v[136:139],a[136:139],v[72:75],v[136:139]
v_exp_f32 v223,v223
v_mfma_f32_16x16x32_bf16 v[136:139],a[140:143],v[76:79],v[136:139]
v_cvt_pk_bf16_f32 v207,v222,v223
v_mfma_f32_16x16x32_bf16 v[140:143],a[156:159],v[76:79],v[32:35]
v_mfma_f32_16x16x32_bf16 v[140:143],a[152:155],v[72:75],v[140:143]
v_mfma_f32_16x16x32_bf16 v[140:143],a[148:151],v[68:71],v[140:143]
v_mfma_f32_16x16x32_bf16 v[140:143],a[144:147],v[64:67],v[140:143]
v_mfma_f32_16x16x32_bf16 v[152:155],a[160:163],v[64:67],v[32:35]
v_mfma_f32_16x16x32_bf16 v[152:155],a[164:167],v[68:71],v[152:155]
v_mfma_f32_16x16x32_bf16 v[152:155],a[168:171],v[72:75],v[152:155]
s_waitcnt lgkmcnt(0)
v_mfma_f32_16x16x32_bf16 v[152:155],a[172:175],v[76:79],v[152:155]
v_mfma_f32_16x16x32_bf16 v[156:159],a[188:191],v[76:79],v[32:35]
v_mfma_f32_16x16x32_bf16 v[156:159],a[184:187],v[72:75],v[156:159]
buffer_load_dwordx4 v5,s[20:23],s47 offen offset:3072 nt lds
v_mfma_f32_16x16x32_bf16 v[156:159],a[180:183],v[68:71],v[156:159]
v_mfma_f32_16x16x32_bf16 v[156:159],a[176:179],v[64:67],v[156:159]
v_mfma_f32_16x16x32_bf16 a[64:67],a[192:195],v[200:203],a[64:67]
v_mfma_f32_16x16x32_bf16 a[64:67],a[196:199],v[204:207],a[64:67]
v_mfma_f32_16x16x32_bf16 a[68:71],a[204:207],v[204:207],a[68:71]
v_mfma_f32_16x16x32_bf16 a[68:71],a[200:203],v[200:203],a[68:71]
v_mfma_f32_16x16x32_bf16 a[72:75],a[208:211],v[200:203],a[72:75]
v_mfma_f32_16x16x32_bf16 a[72:75],a[212:215],v[204:207],a[72:75]
v_exp_f32 v136,v136
v_mfma_f32_16x16x32_bf16 a[76:79],a[220:223],v[204:207],a[76:79]
v_exp_f32 v137,v137
v_mfma_f32_16x16x32_bf16 a[76:79],a[216:219],v[200:203],a[76:79]
v_exp_f32 v138,v138
v_mfma_f32_16x16x32_bf16 a[80:83],a[224:227],v[200:203],a[80:83]
s_add_u32 m0,s49,s52
v_mfma_f32_16x16x32_bf16 a[80:83],a[228:231],v[204:207],a[80:83]
s_xor_b32 s49,s50,65536
v_mfma_f32_16x16x32_bf16 a[84:87],a[236:239],v[204:207],a[84:87]
v_mfma_f32_16x16x32_bf16 a[84:87],a[232:235],v[200:203],a[84:87]
buffer_load_dwordx4 v4,s[24:27],s47 offen nt lds
v_mfma_f32_16x16x32_bf16 a[88:91],a[240:243],v[200:203],a[88:91]
v_mfma_f32_16x16x32_bf16 a[88:91],a[244:247],v[204:207],a[88:91]
v_exp_f32 v139,v139
v_mfma_f32_16x16x32_bf16 a[92:95],a[252:255],v[204:207],a[92:95]
v_exp_f32 v140,v140
v_mfma_f32_16x16x32_bf16 a[92:95],a[248:251],v[200:203],a[92:95]
v_exp_f32 v141,v141
v_smfmac_f32_16x16x64_bf16 v[56:59],v[28:31],v[200:207],v27
v_mfma_f32_16x16x32_bf16 v[168:171],a[128:131],v[80:83],v[36:39]
v_exp_f32 v142,v142
v_mfma_f32_16x16x32_bf16 v[168:171],a[132:135],v[84:87],v[168:171]
v_exp_f32 v143,v143
v_mfma_f32_16x16x32_bf16 v[168:171],a[136:139],v[88:91],v[168:171]
v_exp_f32 v152,v152
v_mfma_f32_16x16x32_bf16 v[168:171],a[140:143],v[92:95],v[168:171]
v_exp_f32 v153,v153
v_mfma_f32_16x16x32_bf16 v[172:175],a[156:159],v[92:95],v[36:39]
v_exp_f32 v154,v154
v_mfma_f32_16x16x32_bf16 v[172:175],a[152:155],v[88:91],v[172:175]
v_exp_f32 v155,v155
v_mfma_f32_16x16x32_bf16 v[172:175],a[148:151],v[84:87],v[172:175]
v_exp_f32 v156,v156
v_mfma_f32_16x16x32_bf16 v[172:175],a[144:147],v[80:83],v[172:175]
v_exp_f32 v157,v157
v_mfma_f32_16x16x32_bf16 v[184:187],a[160:163],v[80:83],v[36:39]
buffer_load_dwordx4 v5,s[24:27],s47 offen offset:1024 nt lds
v_mfma_f32_16x16x32_bf16 v[184:187],a[164:167],v[84:87],v[184:187]
v_exp_f32 v158,v158
v_mfma_f32_16x16x32_bf16 v[184:187],a[168:171],v[88:91],v[184:187]
v_exp_f32 v159,v159
v_mfma_f32_16x16x32_bf16 v[184:187],a[172:175],v[92:95],v[184:187]
v_exp_f32 v168,v168
v_mfma_f32_16x16x32_bf16 v[188:191],a[188:191],v[92:95],v[36:39]
v_exp_f32 v169,v169
v_mfma_f32_16x16x32_bf16 v[188:191],a[184:187],v[88:91],v[188:191]
v_exp_f32 v170,v170
v_mfma_f32_16x16x32_bf16 v[188:191],a[180:183],v[84:87],v[188:191]
v_exp_f32 v171,v171
v_mfma_f32_16x16x32_bf16 v[188:191],a[176:179],v[80:83],v[188:191]
v_exp_f32 v172,v172
ds_read_b64_tr_b16 a[192:193],v2
v_cvt_pk_bf16_f32 v168,v168,v169
ds_read_b64_tr_b16 a[194:195],v2 offset:4096
v_cvt_pk_bf16_f32 v169,v170,v171
v_exp_f32 v173,v173
ds_read_b64_tr_b16 a[196:197],v2 offset:8192
v_cvt_pk_bf16_f32 v170,v172,v173
v_exp_f32 v174,v174
v_exp_f32 v175,v175
buffer_load_dwordx4 v4,s[24:27],s47 offen offset:2048 nt lds
ds_read_b64_tr_b16 a[198:199],v2 offset:12288
v_cvt_pk_bf16_f32 v171,v174,v175
v_exp_f32 v184,v184
v_exp_f32 v185,v185
ds_read_b64_tr_b16 a[204:205],v3 offset:8192
v_cvt_pk_bf16_f32 v136,v136,v137
ds_read_b64_tr_b16 a[206:207],v3 offset:12288
v_cvt_pk_bf16_f32 v172,v184,v185
ds_read_b64_tr_b16 a[200:201],v3
ds_read_b64_tr_b16 a[202:203],v3 offset:4096
v_cvt_pk_bf16_f32 v137,v138,v139
ds_read_b64_tr_b16 a[208:209],v2 offset:256
ds_read_b64_tr_b16 a[210:211],v2 offset:4352
ds_read_b64_tr_b16 a[212:213],v2 offset:8448
v_mfma_f32_16x16x32_bf16 v[200:203],a[128:131],v[96:99],v[40:43]
ds_read_b64_tr_b16 a[214:215],v2 offset:12544
v_cvt_pk_bf16_f32 v138,v140,v141
v_mfma_f32_16x16x32_bf16 v[200:203],a[132:135],v[100:103],v[200:203]
ds_read_b64_tr_b16 a[220:221],v3 offset:8448
v_mfma_f32_16x16x32_bf16 v[200:203],a[136:139],v[104:107],v[200:203]
ds_read_b64_tr_b16 a[222:223],v3 offset:12544
v_cvt_pk_bf16_f32 v139,v142,v143
v_mfma_f32_16x16x32_bf16 v[200:203],a[140:143],v[108:111],v[200:203]
buffer_load_dwordx4 v5,s[24:27],s47 offen offset:3072 nt lds
v_mfma_f32_16x16x32_bf16 v[204:207],a[156:159],v[108:111],v[40:43]
s_or_b32 s47,s47,0x18000
s_add_u32 s47,s47,4096
ds_read_b64_tr_b16 a[216:217],v3 offset:256
v_mfma_f32_16x16x32_bf16 v[204:207],a[152:155],v[104:107],v[204:207]
s_and_b32 s47,s47,0x27000
ds_read_b64_tr_b16 a[218:219],v3 offset:4352
v_cvt_pk_bf16_f32 v140,v152,v153
v_mfma_f32_16x16x32_bf16 v[204:207],a[148:151],v[100:103],v[204:207]
s_or_b32 s47,s47,s46
ds_read_b64_tr_b16 a[224:225],v2 offset:512
v_mfma_f32_16x16x32_bf16 v[204:207],a[144:147],v[96:99],v[204:207]
ds_read_b64_tr_b16 a[226:227],v2 offset:4608
v_mfma_f32_16x16x32_bf16 v[216:219],a[160:163],v[96:99],v[40:43]
ds_read_b64_tr_b16 a[228:229],v2 offset:8704
v_cvt_pk_bf16_f32 v141,v154,v155
v_mfma_f32_16x16x32_bf16 v[216:219],a[164:167],v[100:103],v[216:219]
ds_read_b64_tr_b16 a[230:231],v2 offset:12800
v_mfma_f32_16x16x32_bf16 v[216:219],a[168:171],v[104:107],v[216:219]
ds_read_b64_tr_b16 a[236:237],v3 offset:8704
v_mfma_f32_16x16x32_bf16 v[216:219],a[172:175],v[108:111],v[216:219]
ds_read_b64_tr_b16 a[238:239],v3 offset:12800
v_cvt_pk_bf16_f32 v142,v156,v157
v_mfma_f32_16x16x32_bf16 v[220:223],a[188:191],v[108:111],v[40:43]
ds_read_b64_tr_b16 a[232:233],v3 offset:512
v_mfma_f32_16x16x32_bf16 v[220:223],a[184:187],v[104:107],v[220:223]
ds_read_b64_tr_b16 a[234:235],v3 offset:4608
v_mfma_f32_16x16x32_bf16 v[220:223],a[180:183],v[100:103],v[220:223]
ds_read_b64_tr_b16 a[240:241],v2 offset:768
v_cvt_pk_bf16_f32 v143,v158,v159
v_mfma_f32_16x16x32_bf16 v[220:223],a[176:179],v[96:99],v[220:223]
s_waitcnt lgkmcnt(7)
buffer_load_dword v26,v24,s[88:91],s93 offen
v_mfma_f32_16x16x32_bf16 a[0:3],a[192:195],v[136:139],a[0:3]
ds_read_b64_tr_b16 a[242:243],v2 offset:4864
v_mfma_f32_16x16x32_bf16 a[0:3],a[196:199],v[140:143],a[0:3]
ds_read_b64_tr_b16 a[244:245],v2 offset:8960
v_mfma_f32_16x16x32_bf16 a[4:7],a[204:207],v[140:143],a[4:7]
ds_read_b64_tr_b16 a[246:247],v2 offset:13056
v_mfma_f32_16x16x32_bf16 a[4:7],a[200:203],v[136:139],a[4:7]
ds_read_b64_tr_b16 a[252:253],v3 offset:8960
v_mfma_f32_16x16x32_bf16 a[8:11],a[208:211],v[136:139],a[8:11]
ds_read_b64_tr_b16 a[254:255],v3 offset:13056
v_mfma_f32_16x16x32_bf16 a[8:11],a[212:215],v[140:143],a[8:11]
ds_read_b64_tr_b16 a[248:249],v3 offset:768
v_mfma_f32_16x16x32_bf16 a[12:15],a[220:223],v[140:143],a[12:15]
ds_read_b64_tr_b16 a[250:251],v3 offset:4864
v_mfma_f32_16x16x32_bf16 a[12:15],a[216:219],v[136:139],a[12:15]
s_bitcmp1_b32 s45,0
s_cbranch_scc0 L_t31v30_%=
s_mov_b32 m0,s80
s_add_u32 s80,s80,4096
buffer_load_dwordx4 v25,s[20:23],s83 offen nt lds
s_add_u32 s83,s83,256
L_t31v30_%=:
v_mfma_f32_16x16x32_bf16 a[16:19],a[224:227],v[136:139],a[16:19]
s_waitcnt lgkmcnt(6)
v_exp_f32 v186,v186
v_mfma_f32_16x16x32_bf16 a[16:19],a[228:231],v[140:143],a[16:19]
v_exp_f32 v187,v187
v_mfma_f32_16x16x32_bf16 a[20:23],a[236:239],v[140:143],a[20:23]
v_exp_f32 v188,v188
v_mfma_f32_16x16x32_bf16 a[20:23],a[232:235],v[136:139],a[20:23]
v_exp_f32 v189,v189
v_mfma_f32_16x16x32_bf16 a[24:27],a[240:243],v[136:139],a[24:27]
s_waitcnt lgkmcnt(2)
v_exp_f32 v190,v190
v_mfma_f32_16x16x32_bf16 a[24:27],a[244:247],v[140:143],a[24:27]
v_cvt_pk_bf16_f32 v173,v186,v187
v_mfma_f32_16x16x32_bf16 a[28:31],a[252:255],v[140:143],a[28:31]
s_waitcnt lgkmcnt(0)
v_exp_f32 v191,v191
v_mfma_f32_16x16x32_bf16 a[28:31],a[248:251],v[136:139],a[28:31]
v_cvt_pk_bf16_f32 v174,v188,v189
v_cvt_pk_bf16_f32 v175,v190,v191
s_cmp_lg_u32 s53,0
v_smfmac_f32_16x16x64_bf16 v[48:51],v[28:31],v[136:143],v27
s_cbranch_scc1 L_att0v3_%=
s_waitcnt lgkmcnt(0)
s_nop 7
s_branch L_att0x_%=
.p2align 5
L_att0v0_%=:
v_mfma_f32_16x16x32_bf16 v[232:235],a[128:131],v[112:115],v[44:47]
s_waitcnt vmcnt(8) lgkmcnt(0)
v_exp_f32 v200,v200
v_mfma_f32_16x16x32_bf16 v[232:235],a[132:135],v[116:119],v[232:235]
s_barrier
s_add_u32 m0,s49,s51
ds_read_b128 a[128:131],v1
v_mfma_f32_16x16x32_bf16 v[232:235],a[136:139],v[120:123],v[232:235]
s_mov_b32 s90,L_endseg_%=-L_pc_%=+128
v_exp_f32 v201,v201
v_mfma_f32_16x16x32_bf16 v[232:235],a[140:143],v[124:127],v[232:235]
s_lshr_b32 s45,s45,1
ds_read_b128 a[132:135],v1 offset:256
v_cvt_pk_bf16_f32 v200,v200,v201
v_mfma_f32_16x16x32_bf16 v[236:239],a[156:159],v[124:127],v[44:47]
buffer_load_dwordx4 v4,s[20:23],s47 offen nt lds
v_mfma_f32_16x16x32_bf16 v[236:239],a[152:155],v[120:123],v[236:239]
)A"
R"A(ds_read_b128 a[136:139],v1 offset:512
v_add_u32 v2,s48,v2
v_mfma_f32_16x16x32_bf16 v[236:239],a[148:151],v[116:119],v[236:239]
v_exp_f32 v202,v202
v_mfma_f32_16x16x32_bf16 v[236:239],a[144:147],v[112:115],v[236:239]
s_add_u32 s62,s50,32768
ds_read_b128 a[140:143],v1 offset:768
v_add_u32 v3,s48,v3
v_mfma_f32_16x16x32_bf16 v[248:251],a[160:163],v[112:115],v[44:47]
s_and_b32 s62,s62,131071
v_exp_f32 v203,v203
v_mfma_f32_16x16x32_bf16 v[248:251],a[164:167],v[116:119],v[248:251]
s_add_u32 s54,s54,16384
ds_read_b128 a[156:159],v1 offset:4864
v_cvt_pk_bf16_f32 v201,v202,v203
v_mfma_f32_16x16x32_bf16 v[248:251],a[168:171],v[120:123],v[248:251]
s_sub_u32 s48,s62,s50
v_exp_f32 v204,v204
v_mfma_f32_16x16x32_bf16 v[248:251],a[172:175],v[124:127],v[248:251]
s_sub_u32 s53,s53,1
v_exp_f32 v205,v205
v_mfma_f32_16x16x32_bf16 v[252:255],a[188:191],v[124:127],v[44:47]
s_and_b32 s46,s54,0xfffc0000
ds_read_b128 a[152:155],v1 offset:4608
v_cvt_pk_bf16_f32 v202,v204,v205
v_mfma_f32_16x16x32_bf16 v[252:255],a[184:187],v[120:123],v[252:255]
s_mov_b32 s50,s62
v_exp_f32 v206,v206
v_mfma_f32_16x16x32_bf16 v[252:255],a[180:183],v[116:119],v[252:255]
v_exp_f32 v207,v207
v_mfma_f32_16x16x32_bf16 v[252:255],a[176:179],v[112:115],v[252:255]
ds_read_b128 a[148:151],v1 offset:4352
v_cvt_pk_bf16_f32 v203,v206,v207
v_mfma_f32_16x16x32_bf16 a[32:35],a[192:195],v[168:171],a[32:35]
v_exp_f32 v216,v216
v_mfma_f32_16x16x32_bf16 a[32:35],a[196:199],v[172:175],a[32:35]
ds_read_b128 a[144:147],v1 offset:4096
v_mfma_f32_16x16x32_bf16 a[36:39],a[204:207],v[172:175],a[36:39]
buffer_load_dwordx4 v4,s[20:23],s47 offen offset:1024 nt lds
v_mfma_f32_16x16x32_bf16 a[36:39],a[200:203],v[168:171],a[36:39]
ds_read_b128 a[160:163],v1 offset:8192
v_mfma_f32_16x16x32_bf16 a[40:43],a[208:211],v[168:171],a[40:43]
v_exp_f32 v217,v217
v_mfma_f32_16x16x32_bf16 a[40:43],a[212:215],v[172:175],a[40:43]
ds_read_b128 a[164:167],v1 offset:8448
v_cvt_pk_bf16_f32 v204,v216,v217
v_mfma_f32_16x16x32_bf16 a[44:47],a[220:223],v[172:175],a[44:47]
v_exp_f32 v218,v218
v_mfma_f32_16x16x32_bf16 a[44:47],a[216:219],v[168:171],a[44:47]
ds_read_b128 a[168:171],v1 offset:8704
v_mfma_f32_16x16x32_bf16 a[48:51],a[224:227],v[168:171],a[48:51]
v_exp_f32 v219,v219
v_mfma_f32_16x16x32_bf16 a[48:51],a[228:231],v[172:175],a[48:51]
ds_read_b128 a[172:175],v1 offset:8960
v_cvt_pk_bf16_f32 v205,v218,v219
v_mfma_f32_16x16x32_bf16 a[52:55],a[236:239],v[172:175],a[52:55]
v_exp_f32 v220,v220
v_mfma_f32_16x16x32_bf16 a[52:55],a[232:235],v[168:171],a[52:55]
ds_read_b128 a[188:191],v1 offset:13056
v_mfma_f32_16x16x32_bf16 a[56:59],a[240:243],v[168:171],a[56:59]
v_exp_f32 v221,v221
v_mfma_f32_16x16x32_bf16 a[56:59],a[244:247],v[172:175],a[56:59]
ds_read_b128 a[184:187],v1 offset:12800
v_cvt_pk_bf16_f32 v206,v220,v221
v_mfma_f32_16x16x32_bf16 a[60:63],a[252:255],v[172:175],a[60:63]
v_exp_f32 v222,v222
v_mfma_f32_16x16x32_bf16 a[60:63],a[248:251],v[168:171],a[60:63]
ds_read_b128 a[180:183],v1 offset:12544
v_smfmac_f32_16x16x64_bf16 v[52:55],v[28:31],v[168:175],v27
buffer_load_dwordx4 v5,s[20:23],s47 offen offset:2048 nt lds
ds_read_b128 a[176:179],v1 offset:12288
v_add_u32 v1,s48,v1
v_exp_f32 v223,v223
s_nop 0
v_cvt_pk_bf16_f32 v207,v222,v223
v_exp_f32 v232,v232
v_exp_f32 v233,v233
v_exp_f32 v234,v234
v_exp_f32 v235,v235
v_exp_f32 v236,v236
v_exp_f32 v237,v237
v_exp_f32 v238,v238
v_exp_f32 v239,v239
v_exp_f32 v248,v248
buffer_load_dwordx4 v5,s[20:23],s47 offen offset:3072 nt lds
v_exp_f32 v249,v249
v_exp_f32 v250,v250
v_mfma_f32_16x16x32_bf16 a[64:67],a[192:195],v[200:203],a[64:67]
v_exp_f32 v251,v251
v_mfma_f32_16x16x32_bf16 a[64:67],a[196:199],v[204:207],a[64:67]
v_exp_f32 v252,v252
v_mfma_f32_16x16x32_bf16 a[68:71],a[204:207],v[204:207],a[68:71]
v_exp_f32 v253,v253
v_mfma_f32_16x16x32_bf16 a[68:71],a[200:203],v[200:203],a[68:71]
v_exp_f32 v254,v254
v_mfma_f32_16x16x32_bf16 a[72:75],a[208:211],v[200:203],a[72:75]
v_exp_f32 v255,v255
v_mfma_f32_16x16x32_bf16 a[72:75],a[212:215],v[204:207],a[72:75]
v_mfma_f32_16x16x32_bf16 a[76:79],a[220:223],v[204:207],a[76:79]
v_mfma_f32_16x16x32_bf16 a[76:79],a[216:219],v[200:203],a[76:79]
v_mfma_f32_16x16x32_bf16 a[80:83],a[224:227],v[200:203],a[80:83]
s_add_u32 m0,s49,s52
v_cvt_pk_bf16_f32 v232,v232,v233
v_cvt_pk_bf16_f32 v233,v234,v235
v_mfma_f32_16x16x32_bf16 a[80:83],a[228:231],v[204:207],a[80:83]
s_xor_b32 s49,s50,65536
v_cvt_pk_bf16_f32 v234,v236,v237
v_cvt_pk_bf16_f32 v235,v238,v239
v_mfma_f32_16x16x32_bf16 a[84:87],a[236:239],v[204:207],a[84:87]
v_cvt_pk_bf16_f32 v236,v248,v249
v_cvt_pk_bf16_f32 v237,v250,v251
v_mfma_f32_16x16x32_bf16 a[84:87],a[232:235],v[200:203],a[84:87]
buffer_load_dwordx4 v4,s[24:27],s47 offen nt lds
v_mfma_f32_16x16x32_bf16 a[88:91],a[240:243],v[200:203],a[88:91]
v_cvt_pk_bf16_f32 v238,v252,v253
v_cvt_pk_bf16_f32 v239,v254,v255
v_mfma_f32_16x16x32_bf16 a[88:91],a[244:247],v[204:207],a[88:91]
v_mfma_f32_16x16x32_bf16 a[92:95],a[252:255],v[204:207],a[92:95]
v_mfma_f32_16x16x32_bf16 a[92:95],a[248:251],v[200:203],a[92:95]
v_smfmac_f32_16x16x64_bf16 v[56:59],v[28:31],v[200:207],v27
s_waitcnt lgkmcnt(0)
v_mfma_f32_16x16x32_bf16 v[168:171],a[128:131],v[80:83],v[36:39]
v_mfma_f32_16x16x32_bf16 v[168:171],a[132:135],v[84:87],v[168:171]
v_mfma_f32_16x16x32_bf16 v[168:171],a[136:139],v[88:91],v[168:171]
v_mfma_f32_16x16x32_bf16 v[168:171],a[140:143],v[92:95],v[168:171]
v_mfma_f32_16x16x32_bf16 v[172:175],a[156:159],v[92:95],v[36:39]
v_mfma_f32_16x16x32_bf16 v[172:175],a[152:155],v[88:91],v[172:175]
v_mfma_f32_16x16x32_bf16 v[172:175],a[148:151],v[84:87],v[172:175]
v_mfma_f32_16x16x32_bf16 v[172:175],a[144:147],v[80:83],v[172:175]
v_mfma_f32_16x16x32_bf16 v[184:187],a[160:163],v[80:83],v[36:39]
buffer_load_dwordx4 v5,s[24:27],s47 offen offset:1024 nt lds
v_mfma_f32_16x16x32_bf16 v[184:187],a[164:167],v[84:87],v[184:187]
v_mfma_f32_16x16x32_bf16 v[184:187],a[168:171],v[88:91],v[184:187]
v_mfma_f32_16x16x32_bf16 v[184:187],a[172:175],v[92:95],v[184:187]
v_exp_f32 v168,v168
v_mfma_f32_16x16x32_bf16 v[188:191],a[188:191],v[92:95],v[36:39]
v_exp_f32 v169,v169
v_mfma_f32_16x16x32_bf16 v[188:191],a[184:187],v[88:91],v[188:191]
v_exp_f32 v170,v170
v_mfma_f32_16x16x32_bf16 v[188:191],a[180:183],v[84:87],v[188:191]
v_exp_f32 v171,v171
v_mfma_f32_16x16x32_bf16 v[188:191],a[176:179],v[80:83],v[188:191]
v_exp_f32 v172,v172
v_mfma_f32_16x16x32_bf16 a[96:99],a[192:195],v[232:235],a[96:99]
ds_read_b64_tr_b16 a[192:193],v2
v_cvt_pk_bf16_f32 v168,v168,v169
v_mfma_f32_16x16x32_bf16 a[96:99],a[196:199],v[236:239],a[96:99]
ds_read_b64_tr_b16 a[194:195],v2 offset:4096
v_cvt_pk_bf16_f32 v169,v170,v171
v_mfma_f32_16x16x32_bf16 a[100:103],a[204:207],v[236:239],a[100:103]
v_exp_f32 v173,v173
v_mfma_f32_16x16x32_bf16 a[100:103],a[200:203],v[232:235],a[100:103]
ds_read_b64_tr_b16 a[196:197],v2 offset:8192
v_cvt_pk_bf16_f32 v170,v172,v173
v_mfma_f32_16x16x32_bf16 a[104:107],a[208:211],v[232:235],a[104:107]
v_exp_f32 v174,v174
v_mfma_f32_16x16x32_bf16 a[104:107],a[212:215],v[236:239],a[104:107]
v_exp_f32 v175,v175
v_mfma_f32_16x16x32_bf16 a[108:111],a[220:223],v[236:239],a[108:111]
buffer_load_dwordx4 v4,s[24:27],s47 offen offset:2048 nt lds
v_mfma_f32_16x16x32_bf16 a[108:111],a[216:219],v[232:235],a[108:111]
ds_read_b64_tr_b16 a[198:199],v2 offset:12288
v_cvt_pk_bf16_f32 v171,v174,v175
v_mfma_f32_16x16x32_bf16 a[112:115],a[224:227],v[232:235],a[112:115]
v_exp_f32 v184,v184
v_mfma_f32_16x16x32_bf16 a[112:115],a[228:231],v[236:239],a[112:115]
v_exp_f32 v185,v185
v_mfma_f32_16x16x32_bf16 a[116:119],a[236:239],v[236:239],a[116:119]
ds_read_b64_tr_b16 a[204:205],v3 offset:8192
v_mfma_f32_16x16x32_bf16 a[116:119],a[232:235],v[232:235],a[116:119]
ds_read_b64_tr_b16 a[206:207],v3 offset:12288
v_cvt_pk_bf16_f32 v172,v184,v185
v_mfma_f32_16x16x32_bf16 a[120:123],a[240:243],v[232:235],a[120:123]
ds_read_b64_tr_b16 a[200:201],v3
v_mfma_f32_16x16x32_bf16 a[120:123],a[244:247],v[236:239],a[120:123]
ds_read_b64_tr_b16 a[202:203],v3 offset:4096
v_mfma_f32_16x16x32_bf16 a[124:127],a[252:255],v[236:239],a[124:127]
ds_read_b64_tr_b16 a[208:209],v2 offset:256
v_mfma_f32_16x16x32_bf16 a[124:127],a[248:251],v[232:235],a[124:127]
ds_read_b64_tr_b16 a[210:211],v2 offset:4352
v_smfmac_f32_16x16x64_bf16 v[60:63],v[28:31],v[232:239],v27
ds_read_b64_tr_b16 a[212:213],v2 offset:8448
v_mfma_f32_16x16x32_bf16 v[200:203],a[128:131],v[96:99],v[40:43]
ds_read_b64_tr_b16 a[214:215],v2 offset:12544
v_mfma_f32_16x16x32_bf16 v[200:203],a[132:135],v[100:103],v[200:203]
ds_read_b64_tr_b16 a[220:221],v3 offset:8448
v_mfma_f32_16x16x32_bf16 v[200:203],a[136:139],v[104:107],v[200:203]
ds_read_b64_tr_b16 a[222:223],v3 offset:12544
v_mfma_f32_16x16x32_bf16 v[200:203],a[140:143],v[108:111],v[200:203]
buffer_load_dwordx4 v5,s[24:27],s47 offen offset:3072 nt lds
v_mfma_f32_16x16x32_bf16 v[204:207],a[156:159],v[108:111],v[40:43]
s_or_b32 s47,s47,0x18000
s_add_u32 s47,s47,4096
ds_read_b64_tr_b16 a[216:217],v3 offset:256
v_mfma_f32_16x16x32_bf16 v[204:207],a[152:155],v[104:107],v[204:207]
s_and_b32 s47,s47,0x27000
ds_read_b64_tr_b16 a[218:219],v3 offset:4352
v_mfma_f32_16x16x32_bf16 v[204:207],a[148:151],v[100:103],v[204:207]
s_or_b32 s47,s47,s46
ds_read_b64_tr_b16 a[224:225],v2 offset:512
v_mfma_f32_16x16x32_bf16 v[204:207],a[144:147],v[96:99],v[204:207]
ds_read_b64_tr_b16 a[226:227],v2 offset:4608
v_mfma_f32_16x16x32_bf16 v[216:219],a[160:163],v[96:99],v[40:43]
ds_read_b64_tr_b16 a[228:229],v2 offset:8704
v_mfma_f32_16x16x32_bf16 v[216:219],a[164:167],v[100:103],v[216:219]
ds_read_b64_tr_b16 a[230:231],v2 offset:12800
v_mfma_f32_16x16x32_bf16 v[216:219],a[168:171],v[104:107],v[216:219]
ds_read_b64_tr_b16 a[236:237],v3 offset:8704
v_mfma_f32_16x16x32_bf16 v[216:219],a[172:175],v[108:111],v[216:219]
ds_read_b64_tr_b16 a[238:239],v3 offset:12800
v_mfma_f32_16x16x32_bf16 v[220:223],a[188:191],v[108:111],v[40:43]
ds_read_b64_tr_b16 a[232:233],v3 offset:512
v_mfma_f32_16x16x32_bf16 v[220:223],a[184:187],v[104:107],v[220:223]
ds_read_b64_tr_b16 a[234:235],v3 offset:4608
v_mfma_f32_16x16x32_bf16 v[220:223],a[180:183],v[100:103],v[220:223]
ds_read_b64_tr_b16 a[240:241],v2 offset:768
v_mfma_f32_16x16x32_bf16 v[220:223],a[176:179],v[96:99],v[220:223]
buffer_load_dword v26,v24,s[88:91],s93 offen
ds_read_b64_tr_b16 a[242:243],v2 offset:4864
ds_read_b64_tr_b16 a[244:245],v2 offset:8960
ds_read_b64_tr_b16 a[246:247],v2 offset:13056
ds_read_b64_tr_b16 a[252:253],v3 offset:8960
ds_read_b64_tr_b16 a[254:255],v3 offset:13056
ds_read_b64_tr_b16 a[248:249],v3 offset:768
ds_read_b64_tr_b16 a[250:251],v3 offset:4864
s_bitcmp1_b32 s45,0
s_cbranch_scc0 L_t31v00_%=
s_mov_b32 m0,s80
s_add_u32 s80,s80,4096
buffer_load_dwordx4 v25,s[20:23],s83 offen nt lds
s_add_u32 s83,s83,256
L_t31v00_%=:
v_exp_f32 v186,v186
v_exp_f32 v187,v187
v_exp_f32 v188,v188
v_exp_f32 v189,v189
v_exp_f32 v190,v190
v_cvt_pk_bf16_f32 v173,v186,v187
v_exp_f32 v191,v191
v_cvt_pk_bf16_f32 v174,v188,v189
v_cvt_pk_bf16_f32 v175,v190,v191
s_cmp_lg_u32 s53,0
s_cbranch_scc1 L_att0v0_%=
s_waitcnt lgkmcnt(0)
s_nop 7
s_branch L_att0x_%=
.p2align 5
L_att0v1_%=:
v_mfma_f32_16x16x32_bf16 v[232:235],a[128:131],v[112:115],v[44:47]
s_waitcnt vmcnt(8) lgkmcnt(0)
v_exp_f32 v200,v200
v_mfma_f32_16x16x32_bf16 v[232:235],a[132:135],v[116:119],v[232:235]
s_barrier
s_add_u32 m0,s49,s51
ds_read_b128 a[128:131],v1
v_mfma_f32_16x16x32_bf16 v[232:235],a[136:139],v[120:123],v[232:235]
s_mov_b32 s90,L_endseg_%=-L_pc_%=+128
v_exp_f32 v201,v201
v_mfma_f32_16x16x32_bf16 v[232:235],a[140:143],v[124:127],v[232:235]
s_lshr_b32 s45,s45,1
ds_read_b128 a[132:135],v1 offset:256
v_cvt_pk_bf16_f32 v200,v200,v201
v_mfma_f32_16x16x32_bf16 v[236:239],a[156:159],v[124:127],v[44:47]
buffer_load_dwordx4 v4,s[20:23],s47 offen nt lds
v_mfma_f32_16x16x32_bf16 v[236:239],a[152:155],v[120:123],v[236:239]
ds_read_b128 a[136:139],v1 offset:512
v_add_u32 v2,s48,v2
v_mfma_f32_16x16x32_bf16 v[236:239],a[148:151],v[116:119],v[236:239]
v_exp_f32 v202,v202
v_mfma_f32_16x16x32_bf16 v[236:239],a[144:147],v[112:115],v[236:239]
s_add_u32 s62,s50,32768
ds_read_b128 a[140:143],v1 offset:768
v_add_u32 v3,s48,v3
v_mfma_f32_16x16x32_bf16 v[248:251],a[160:163],v[112:115],v[44:47]
)A"
R"A(s_and_b32 s62,s62,131071
v_exp_f32 v203,v203
v_mfma_f32_16x16x32_bf16 v[248:251],a[164:167],v[116:119],v[248:251]
s_add_u32 s54,s54,16384
ds_read_b128 a[156:159],v1 offset:4864
v_cvt_pk_bf16_f32 v201,v202,v203
v_mfma_f32_16x16x32_bf16 v[248:251],a[168:171],v[120:123],v[248:251]
s_sub_u32 s48,s62,s50
v_exp_f32 v204,v204
v_mfma_f32_16x16x32_bf16 v[248:251],a[172:175],v[124:127],v[248:251]
s_sub_u32 s53,s53,1
v_exp_f32 v205,v205
v_mfma_f32_16x16x32_bf16 v[252:255],a[188:191],v[124:127],v[44:47]
s_and_b32 s46,s54,0xfffc0000
ds_read_b128 a[152:155],v1 offset:4608
v_cvt_pk_bf16_f32 v202,v204,v205
v_mfma_f32_16x16x32_bf16 v[252:255],a[184:187],v[120:123],v[252:255]
s_mov_b32 s50,s62
v_exp_f32 v206,v206
v_mfma_f32_16x16x32_bf16 v[252:255],a[180:183],v[116:119],v[252:255]
v_exp_f32 v207,v207
v_mfma_f32_16x16x32_bf16 v[252:255],a[176:179],v[112:115],v[252:255]
ds_read_b128 a[148:151],v1 offset:4352
v_cvt_pk_bf16_f32 v203,v206,v207
v_exp_f32 v216,v216
ds_read_b128 a[144:147],v1 offset:4096
buffer_load_dwordx4 v4,s[20:23],s47 offen offset:1024 nt lds
ds_read_b128 a[160:163],v1 offset:8192
v_exp_f32 v217,v217
ds_read_b128 a[164:167],v1 offset:8448
v_cvt_pk_bf16_f32 v204,v216,v217
v_exp_f32 v218,v218
ds_read_b128 a[168:171],v1 offset:8704
v_exp_f32 v219,v219
ds_read_b128 a[172:175],v1 offset:8960
v_cvt_pk_bf16_f32 v205,v218,v219
v_exp_f32 v220,v220
ds_read_b128 a[188:191],v1 offset:13056
v_exp_f32 v221,v221
ds_read_b128 a[184:187],v1 offset:12800
v_cvt_pk_bf16_f32 v206,v220,v221
v_exp_f32 v222,v222
ds_read_b128 a[180:183],v1 offset:12544
s_waitcnt lgkmcnt(11)
v_mfma_f32_16x16x32_bf16 v[136:139],a[128:131],v[64:67],v[32:35]
buffer_load_dwordx4 v5,s[20:23],s47 offen offset:2048 nt lds
v_mfma_f32_16x16x32_bf16 v[136:139],a[132:135],v[68:71],v[136:139]
ds_read_b128 a[176:179],v1 offset:12288
v_add_u32 v1,s48,v1
v_mfma_f32_16x16x32_bf16 v[136:139],a[136:139],v[72:75],v[136:139]
v_exp_f32 v223,v223
v_mfma_f32_16x16x32_bf16 v[136:139],a[140:143],v[76:79],v[136:139]
s_waitcnt lgkmcnt(11)
v_cvt_pk_bf16_f32 v207,v222,v223
v_mfma_f32_16x16x32_bf16 v[140:143],a[156:159],v[76:79],v[32:35]
s_waitcnt lgkmcnt(10)
v_exp_f32 v232,v232
v_mfma_f32_16x16x32_bf16 v[140:143],a[152:155],v[72:75],v[140:143]
s_waitcnt lgkmcnt(9)
v_exp_f32 v233,v233
v_mfma_f32_16x16x32_bf16 v[140:143],a[148:151],v[68:71],v[140:143]
s_waitcnt lgkmcnt(8)
v_exp_f32 v234,v234
v_mfma_f32_16x16x32_bf16 v[140:143],a[144:147],v[64:67],v[140:143]
s_waitcnt lgkmcnt(1)
v_exp_f32 v235,v235
v_mfma_f32_16x16x32_bf16 v[152:155],a[160:163],v[64:67],v[32:35]
v_exp_f32 v236,v236
v_mfma_f32_16x16x32_bf16 v[152:155],a[164:167],v[68:71],v[152:155]
v_exp_f32 v237,v237
v_mfma_f32_16x16x32_bf16 v[152:155],a[168:171],v[72:75],v[152:155]
v_exp_f32 v238,v238
v_mfma_f32_16x16x32_bf16 v[152:155],a[172:175],v[76:79],v[152:155]
v_exp_f32 v239,v239
v_mfma_f32_16x16x32_bf16 v[156:159],a[188:191],v[76:79],v[32:35]
v_exp_f32 v248,v248
v_mfma_f32_16x16x32_bf16 v[156:159],a[184:187],v[72:75],v[156:159]
buffer_load_dwordx4 v5,s[20:23],s47 offen offset:3072 nt lds
v_mfma_f32_16x16x32_bf16 v[156:159],a[180:183],v[68:71],v[156:159]
s_waitcnt lgkmcnt(0)
v_exp_f32 v249,v249
v_mfma_f32_16x16x32_bf16 v[156:159],a[176:179],v[64:67],v[156:159]
v_exp_f32 v250,v250
v_mfma_f32_16x16x32_bf16 a[64:67],a[192:195],v[200:203],a[64:67]
v_exp_f32 v251,v251
v_mfma_f32_16x16x32_bf16 a[64:67],a[196:199],v[204:207],a[64:67]
v_exp_f32 v252,v252
v_mfma_f32_16x16x32_bf16 a[68:71],a[204:207],v[204:207],a[68:71]
v_exp_f32 v253,v253
v_mfma_f32_16x16x32_bf16 a[68:71],a[200:203],v[200:203],a[68:71]
v_exp_f32 v254,v254
v_mfma_f32_16x16x32_bf16 a[72:75],a[208:211],v[200:203],a[72:75]
v_exp_f32 v255,v255
v_mfma_f32_16x16x32_bf16 a[72:75],a[212:215],v[204:207],a[72:75]
v_exp_f32 v136,v136
v_mfma_f32_16x16x32_bf16 a[76:79],a[220:223],v[204:207],a[76:79]
v_exp_f32 v137,v137
v_mfma_f32_16x16x32_bf16 a[76:79],a[216:219],v[200:203],a[76:79]
v_exp_f32 v138,v138
v_mfma_f32_16x16x32_bf16 a[80:83],a[224:227],v[200:203],a[80:83]
s_add_u32 m0,s49,s52
v_cvt_pk_bf16_f32 v232,v232,v233
v_cvt_pk_bf16_f32 v233,v234,v235
v_mfma_f32_16x16x32_bf16 a[80:83],a[228:231],v[204:207],a[80:83]
s_xor_b32 s49,s50,65536
v_cvt_pk_bf16_f32 v234,v236,v237
v_cvt_pk_bf16_f32 v235,v238,v239
v_mfma_f32_16x16x32_bf16 a[84:87],a[236:239],v[204:207],a[84:87]
v_cvt_pk_bf16_f32 v236,v248,v249
v_cvt_pk_bf16_f32 v237,v250,v251
v_mfma_f32_16x16x32_bf16 a[84:87],a[232:235],v[200:203],a[84:87]
buffer_load_dwordx4 v4,s[24:27],s47 offen nt lds
v_mfma_f32_16x16x32_bf16 a[88:91],a[240:243],v[200:203],a[88:91]
v_cvt_pk_bf16_f32 v238,v252,v253
v_cvt_pk_bf16_f32 v239,v254,v255
v_mfma_f32_16x16x32_bf16 a[88:91],a[244:247],v[204:207],a[88:91]
v_exp_f32 v139,v139
v_mfma_f32_16x16x32_bf16 a[92:95],a[252:255],v[204:207],a[92:95]
v_exp_f32 v140,v140
v_mfma_f32_16x16x32_bf16 a[92:95],a[248:251],v[200:203],a[92:95]
v_exp_f32 v141,v141
v_smfmac_f32_16x16x64_bf16 v[56:59],v[28:31],v[200:207],v27
v_exp_f32 v142,v142
v_exp_f32 v143,v143
v_exp_f32 v152,v152
v_exp_f32 v153,v153
v_exp_f32 v154,v154
v_exp_f32 v155,v155
v_exp_f32 v156,v156
v_exp_f32 v157,v157
buffer_load_dwordx4 v5,s[24:27],s47 offen offset:1024 nt lds
v_exp_f32 v158,v158
v_exp_f32 v159,v159
v_mfma_f32_16x16x32_bf16 a[96:99],a[192:195],v[232:235],a[96:99]
ds_read_b64_tr_b16 a[192:193],v2
v_mfma_f32_16x16x32_bf16 a[96:99],a[196:199],v[236:239],a[96:99]
ds_read_b64_tr_b16 a[194:195],v2 offset:4096
v_mfma_f32_16x16x32_bf16 a[100:103],a[204:207],v[236:239],a[100:103]
v_mfma_f32_16x16x32_bf16 a[100:103],a[200:203],v[232:235],a[100:103]
ds_read_b64_tr_b16 a[196:197],v2 offset:8192
v_mfma_f32_16x16x32_bf16 a[104:107],a[208:211],v[232:235],a[104:107]
v_mfma_f32_16x16x32_bf16 a[104:107],a[212:215],v[236:239],a[104:107]
v_mfma_f32_16x16x32_bf16 a[108:111],a[220:223],v[236:239],a[108:111]
buffer_load_dwordx4 v4,s[24:27],s47 offen offset:2048 nt lds
v_mfma_f32_16x16x32_bf16 a[108:111],a[216:219],v[232:235],a[108:111]
ds_read_b64_tr_b16 a[198:199],v2 offset:12288
v_mfma_f32_16x16x32_bf16 a[112:115],a[224:227],v[232:235],a[112:115]
v_mfma_f32_16x16x32_bf16 a[112:115],a[228:231],v[236:239],a[112:115]
v_mfma_f32_16x16x32_bf16 a[116:119],a[236:239],v[236:239],a[116:119]
ds_read_b64_tr_b16 a[204:205],v3 offset:8192
v_cvt_pk_bf16_f32 v136,v136,v137
v_mfma_f32_16x16x32_bf16 a[116:119],a[232:235],v[232:235],a[116:119]
ds_read_b64_tr_b16 a[206:207],v3 offset:12288
v_mfma_f32_16x16x32_bf16 a[120:123],a[240:243],v[232:235],a[120:123]
ds_read_b64_tr_b16 a[200:201],v3
v_mfma_f32_16x16x32_bf16 a[120:123],a[244:247],v[236:239],a[120:123]
ds_read_b64_tr_b16 a[202:203],v3 offset:4096
v_cvt_pk_bf16_f32 v137,v138,v139
v_mfma_f32_16x16x32_bf16 a[124:127],a[252:255],v[236:239],a[124:127]
ds_read_b64_tr_b16 a[208:209],v2 offset:256
v_mfma_f32_16x16x32_bf16 a[124:127],a[248:251],v[232:235],a[124:127]
ds_read_b64_tr_b16 a[210:211],v2 offset:4352
v_smfmac_f32_16x16x64_bf16 v[60:63],v[28:31],v[232:239],v27
ds_read_b64_tr_b16 a[212:213],v2 offset:8448
v_mfma_f32_16x16x32_bf16 v[200:203],a[128:131],v[96:99],v[40:43]
ds_read_b64_tr_b16 a[214:215],v2 offset:12544
v_cvt_pk_bf16_f32 v138,v140,v141
v_mfma_f32_16x16x32_bf16 v[200:203],a[132:135],v[100:103],v[200:203]
ds_read_b64_tr_b16 a[220:221],v3 offset:8448
v_mfma_f32_16x16x32_bf16 v[200:203],a[136:139],v[104:107],v[200:203]
ds_read_b64_tr_b16 a[222:223],v3 offset:12544
v_cvt_pk_bf16_f32 v139,v142,v143
v_mfma_f32_16x16x32_bf16 v[200:203],a[140:143],v[108:111],v[200:203]
buffer_load_dwordx4 v5,s[24:27],s47 offen offset:3072 nt lds
v_mfma_f32_16x16x32_bf16 v[204:207],a[156:159],v[108:111],v[40:43]
s_or_b32 s47,s47,0x18000
s_add_u32 s47,s47,4096
ds_read_b64_tr_b16 a[216:217],v3 offset:256
v_mfma_f32_16x16x32_bf16 v[204:207],a[152:155],v[104:107],v[204:207]
s_and_b32 s47,s47,0x27000
ds_read_b64_tr_b16 a[218:219],v3 offset:4352
v_cvt_pk_bf16_f32 v140,v152,v153
v_mfma_f32_16x16x32_bf16 v[204:207],a[148:151],v[100:103],v[204:207]
s_or_b32 s47,s47,s46
ds_read_b64_tr_b16 a[224:225],v2 offset:512
v_mfma_f32_16x16x32_bf16 v[204:207],a[144:147],v[96:99],v[204:207]
ds_read_b64_tr_b16 a[226:227],v2 offset:4608
v_mfma_f32_16x16x32_bf16 v[216:219],a[160:163],v[96:99],v[40:43]
ds_read_b64_tr_b16 a[228:229],v2 offset:8704
v_cvt_pk_bf16_f32 v141,v154,v155
v_mfma_f32_16x16x32_bf16 v[216:219],a[164:167],v[100:103],v[216:219]
ds_read_b64_tr_b16 a[230:231],v2 offset:12800
v_mfma_f32_16x16x32_bf16 v[216:219],a[168:171],v[104:107],v[216:219]
ds_read_b64_tr_b16 a[236:237],v3 offset:8704
v_mfma_f32_16x16x32_bf16 v[216:219],a[172:175],v[108:111],v[216:219]
ds_read_b64_tr_b16 a[238:239],v3 offset:12800
v_cvt_pk_bf16_f32 v142,v156,v157
v_mfma_f32_16x16x32_bf16 v[220:223],a[188:191],v[108:111],v[40:43]
ds_read_b64_tr_b16 a[232:233],v3 offset:512
v_mfma_f32_16x16x32_bf16 v[220:223],a[184:187],v[104:107],v[220:223]
ds_read_b64_tr_b16 a[234:235],v3 offset:4608
v_mfma_f32_16x16x32_bf16 v[220:223],a[180:183],v[100:103],v[220:223]
ds_read_b64_tr_b16 a[240:241],v2 offset:768
v_cvt_pk_bf16_f32 v143,v158,v159
v_mfma_f32_16x16x32_bf16 v[220:223],a[176:179],v[96:99],v[220:223]
s_waitcnt lgkmcnt(7)
buffer_load_dword v26,v24,s[88:91],s93 offen
v_mfma_f32_16x16x32_bf16 a[0:3],a[192:195],v[136:139],a[0:3]
ds_read_b64_tr_b16 a[242:243],v2 offset:4864
v_mfma_f32_16x16x32_bf16 a[0:3],a[196:199],v[140:143],a[0:3]
ds_read_b64_tr_b16 a[244:245],v2 offset:8960
v_mfma_f32_16x16x32_bf16 a[4:7],a[204:207],v[140:143],a[4:7]
ds_read_b64_tr_b16 a[246:247],v2 offset:13056
v_mfma_f32_16x16x32_bf16 a[4:7],a[200:203],v[136:139],a[4:7]
ds_read_b64_tr_b16 a[252:253],v3 offset:8960
v_mfma_f32_16x16x32_bf16 a[8:11],a[208:211],v[136:139],a[8:11]
ds_read_b64_tr_b16 a[254:255],v3 offset:13056
v_mfma_f32_16x16x32_bf16 a[8:11],a[212:215],v[140:143],a[8:11]
ds_read_b64_tr_b16 a[248:249],v3 offset:768
v_mfma_f32_16x16x32_bf16 a[12:15],a[220:223],v[140:143],a[12:15]
ds_read_b64_tr_b16 a[250:251],v3 offset:4864
v_mfma_f32_16x16x32_bf16 a[12:15],a[216:219],v[136:139],a[12:15]
s_bitcmp1_b32 s45,0
s_cbranch_scc0 L_t31v10_%=
s_mov_b32 m0,s80
s_add_u32 s80,s80,4096
buffer_load_dwordx4 v25,s[20:23],s83 offen nt lds
s_add_u32 s83,s83,256
L_t31v10_%=:
v_mfma_f32_16x16x32_bf16 a[16:19],a[224:227],v[136:139],a[16:19]
s_waitcnt lgkmcnt(6)
v_mfma_f32_16x16x32_bf16 a[16:19],a[228:231],v[140:143],a[16:19]
v_mfma_f32_16x16x32_bf16 a[20:23],a[236:239],v[140:143],a[20:23]
v_mfma_f32_16x16x32_bf16 a[20:23],a[232:235],v[136:139],a[20:23]
v_mfma_f32_16x16x32_bf16 a[24:27],a[240:243],v[136:139],a[24:27]
s_waitcnt lgkmcnt(2)
v_mfma_f32_16x16x32_bf16 a[24:27],a[244:247],v[140:143],a[24:27]
v_mfma_f32_16x16x32_bf16 a[28:31],a[252:255],v[140:143],a[28:31]
s_waitcnt lgkmcnt(0)
v_mfma_f32_16x16x32_bf16 a[28:31],a[248:251],v[136:139],a[28:31]
s_cmp_lg_u32 s53,0
v_smfmac_f32_16x16x64_bf16 v[48:51],v[28:31],v[136:143],v27
s_cbranch_scc1 L_att0v1_%=
s_waitcnt lgkmcnt(0)
s_nop 7
s_branch L_att0x_%=
.p2align 5
L_att0v2_%=:
v_mfma_f32_16x16x32_bf16 v[232:235],a[128:131],v[112:115],v[44:47]
s_waitcnt vmcnt(8) lgkmcnt(0)
v_mfma_f32_16x16x32_bf16 v[232:235],a[132:135],v[116:119],v[232:235]
s_barrier
s_add_u32 m0,s49,s51
ds_read_b128 a[128:131],v1
v_mfma_f32_16x16x32_bf16 v[232:235],a[136:139],v[120:123],v[232:235]
s_mov_b32 s90,L_endseg_%=-L_pc_%=+128
v_mfma_f32_16x16x32_bf16 v[232:235],a[140:143],v[124:127],v[232:235]
s_lshr_b32 s45,s45,1
ds_read_b128 a[132:135],v1 offset:256
v_mfma_f32_16x16x32_bf16 v[236:239],a[156:159],v[124:127],v[44:47]
buffer_load_dwordx4 v4,s[20:23],s47 offen nt lds
v_mfma_f32_16x16x32_bf16 v[236:239],a[152:155],v[120:123],v[236:239]
ds_read_b128 a[136:139],v1 offset:512
v_add_u32 v2,s48,v2
v_mfma_f32_16x16x32_bf16 v[236:239],a[148:151],v[116:119],v[236:239]
v_mfma_f32_16x16x32_bf16 v[236:239],a[144:147],v[112:115],v[236:239]
s_add_u32 s62,s50,32768
ds_read_b128 a[140:143],v1 offset:768
v_add_u32 v3,s48,v3
v_mfma_f32_16x16x32_bf16 v[248:251],a[160:163],v[112:115],v[44:47]
s_and_b32 s62,s62,131071
v_mfma_f32_16x16x32_bf16 v[248:251],a[164:167],v[116:119],v[248:251]
s_add_u32 s54,s54,16384
ds_read_b128 a[156:159],v1 offset:4864
v_mfma_f32_16x16x32_bf16 v[248:251],a[168:171],v[120:123],v[248:251]
)A"
R"A(s_sub_u32 s48,s62,s50
v_mfma_f32_16x16x32_bf16 v[248:251],a[172:175],v[124:127],v[248:251]
s_sub_u32 s53,s53,1
v_mfma_f32_16x16x32_bf16 v[252:255],a[188:191],v[124:127],v[44:47]
s_and_b32 s46,s54,0xfffc0000
ds_read_b128 a[152:155],v1 offset:4608
v_mfma_f32_16x16x32_bf16 v[252:255],a[184:187],v[120:123],v[252:255]
s_mov_b32 s50,s62
v_mfma_f32_16x16x32_bf16 v[252:255],a[180:183],v[116:119],v[252:255]
v_mfma_f32_16x16x32_bf16 v[252:255],a[176:179],v[112:115],v[252:255]
ds_read_b128 a[148:151],v1 offset:4352
v_mfma_f32_16x16x32_bf16 a[32:35],a[192:195],v[168:171],a[32:35]
v_mfma_f32_16x16x32_bf16 a[32:35],a[196:199],v[172:175],a[32:35]
ds_read_b128 a[144:147],v1 offset:4096
v_mfma_f32_16x16x32_bf16 a[36:39],a[204:207],v[172:175],a[36:39]
buffer_load_dwordx4 v4,s[20:23],s47 offen offset:1024 nt lds
v_mfma_f32_16x16x32_bf16 a[36:39],a[200:203],v[168:171],a[36:39]
ds_read_b128 a[160:163],v1 offset:8192
v_mfma_f32_16x16x32_bf16 a[40:43],a[208:211],v[168:171],a[40:43]
v_mfma_f32_16x16x32_bf16 a[40:43],a[212:215],v[172:175],a[40:43]
ds_read_b128 a[164:167],v1 offset:8448
v_mfma_f32_16x16x32_bf16 a[44:47],a[220:223],v[172:175],a[44:47]
v_mfma_f32_16x16x32_bf16 a[44:47],a[216:219],v[168:171],a[44:47]
ds_read_b128 a[168:171],v1 offset:8704
v_mfma_f32_16x16x32_bf16 a[48:51],a[224:227],v[168:171],a[48:51]
v_mfma_f32_16x16x32_bf16 a[48:51],a[228:231],v[172:175],a[48:51]
ds_read_b128 a[172:175],v1 offset:8960
v_mfma_f32_16x16x32_bf16 a[52:55],a[236:239],v[172:175],a[52:55]
v_mfma_f32_16x16x32_bf16 a[52:55],a[232:235],v[168:171],a[52:55]
ds_read_b128 a[188:191],v1 offset:13056
v_mfma_f32_16x16x32_bf16 a[56:59],a[240:243],v[168:171],a[56:59]
v_mfma_f32_16x16x32_bf16 a[56:59],a[244:247],v[172:175],a[56:59]
ds_read_b128 a[184:187],v1 offset:12800
v_mfma_f32_16x16x32_bf16 a[60:63],a[252:255],v[172:175],a[60:63]
v_mfma_f32_16x16x32_bf16 a[60:63],a[248:251],v[168:171],a[60:63]
ds_read_b128 a[180:183],v1 offset:12544
v_smfmac_f32_16x16x64_bf16 v[52:55],v[28:31],v[168:175],v27
s_waitcnt lgkmcnt(4)
v_mfma_f32_16x16x32_bf16 v[136:139],a[128:131],v[64:67],v[32:35]
buffer_load_dwordx4 v5,s[20:23],s47 offen offset:2048 nt lds
v_mfma_f32_16x16x32_bf16 v[136:139],a[132:135],v[68:71],v[136:139]
ds_read_b128 a[176:179],v1 offset:12288
v_add_u32 v1,s48,v1
v_mfma_f32_16x16x32_bf16 v[136:139],a[136:139],v[72:75],v[136:139]
v_mfma_f32_16x16x32_bf16 v[136:139],a[140:143],v[76:79],v[136:139]
v_mfma_f32_16x16x32_bf16 v[140:143],a[156:159],v[76:79],v[32:35]
v_exp_f32 v232,v232
v_mfma_f32_16x16x32_bf16 v[140:143],a[152:155],v[72:75],v[140:143]
v_exp_f32 v233,v233
v_mfma_f32_16x16x32_bf16 v[140:143],a[148:151],v[68:71],v[140:143]
v_exp_f32 v234,v234
v_mfma_f32_16x16x32_bf16 v[140:143],a[144:147],v[64:67],v[140:143]
v_exp_f32 v235,v235
v_mfma_f32_16x16x32_bf16 v[152:155],a[160:163],v[64:67],v[32:35]
v_exp_f32 v236,v236
v_mfma_f32_16x16x32_bf16 v[152:155],a[164:167],v[68:71],v[152:155]
v_exp_f32 v237,v237
v_mfma_f32_16x16x32_bf16 v[152:155],a[168:171],v[72:75],v[152:155]
s_waitcnt lgkmcnt(0)
v_exp_f32 v238,v238
v_mfma_f32_16x16x32_bf16 v[152:155],a[172:175],v[76:79],v[152:155]
v_exp_f32 v239,v239
v_mfma_f32_16x16x32_bf16 v[156:159],a[188:191],v[76:79],v[32:35]
v_exp_f32 v248,v248
v_mfma_f32_16x16x32_bf16 v[156:159],a[184:187],v[72:75],v[156:159]
buffer_load_dwordx4 v5,s[20:23],s47 offen offset:3072 nt lds
v_mfma_f32_16x16x32_bf16 v[156:159],a[180:183],v[68:71],v[156:159]
v_exp_f32 v249,v249
v_mfma_f32_16x16x32_bf16 v[156:159],a[176:179],v[64:67],v[156:159]
v_exp_f32 v250,v250
v_exp_f32 v251,v251
v_exp_f32 v252,v252
v_exp_f32 v253,v253
v_exp_f32 v254,v254
v_exp_f32 v255,v255
v_exp_f32 v136,v136
v_exp_f32 v137,v137
v_exp_f32 v138,v138
s_add_u32 m0,s49,s52
v_cvt_pk_bf16_f32 v232,v232,v233
v_cvt_pk_bf16_f32 v233,v234,v235
s_xor_b32 s49,s50,65536
v_cvt_pk_bf16_f32 v234,v236,v237
v_cvt_pk_bf16_f32 v235,v238,v239
v_cvt_pk_bf16_f32 v236,v248,v249
v_cvt_pk_bf16_f32 v237,v250,v251
buffer_load_dwordx4 v4,s[24:27],s47 offen nt lds
v_cvt_pk_bf16_f32 v238,v252,v253
v_cvt_pk_bf16_f32 v239,v254,v255
v_exp_f32 v139,v139
v_exp_f32 v140,v140
v_exp_f32 v141,v141
v_mfma_f32_16x16x32_bf16 v[168:171],a[128:131],v[80:83],v[36:39]
v_exp_f32 v142,v142
v_mfma_f32_16x16x32_bf16 v[168:171],a[132:135],v[84:87],v[168:171]
v_exp_f32 v143,v143
v_mfma_f32_16x16x32_bf16 v[168:171],a[136:139],v[88:91],v[168:171]
v_exp_f32 v152,v152
v_mfma_f32_16x16x32_bf16 v[168:171],a[140:143],v[92:95],v[168:171]
v_exp_f32 v153,v153
v_mfma_f32_16x16x32_bf16 v[172:175],a[156:159],v[92:95],v[36:39]
v_exp_f32 v154,v154
v_mfma_f32_16x16x32_bf16 v[172:175],a[152:155],v[88:91],v[172:175]
v_exp_f32 v155,v155
v_mfma_f32_16x16x32_bf16 v[172:175],a[148:151],v[84:87],v[172:175]
v_exp_f32 v156,v156
v_mfma_f32_16x16x32_bf16 v[172:175],a[144:147],v[80:83],v[172:175]
v_exp_f32 v157,v157
v_mfma_f32_16x16x32_bf16 v[184:187],a[160:163],v[80:83],v[36:39]
buffer_load_dwordx4 v5,s[24:27],s47 offen offset:1024 nt lds
v_mfma_f32_16x16x32_bf16 v[184:187],a[164:167],v[84:87],v[184:187]
v_exp_f32 v158,v158
v_mfma_f32_16x16x32_bf16 v[184:187],a[168:171],v[88:91],v[184:187]
v_exp_f32 v159,v159
v_mfma_f32_16x16x32_bf16 v[184:187],a[172:175],v[92:95],v[184:187]
v_exp_f32 v168,v168
v_mfma_f32_16x16x32_bf16 v[188:191],a[188:191],v[92:95],v[36:39]
v_exp_f32 v169,v169
v_mfma_f32_16x16x32_bf16 v[188:191],a[184:187],v[88:91],v[188:191]
v_exp_f32 v170,v170
v_mfma_f32_16x16x32_bf16 v[188:191],a[180:183],v[84:87],v[188:191]
v_exp_f32 v171,v171
v_mfma_f32_16x16x32_bf16 v[188:191],a[176:179],v[80:83],v[188:191]
v_exp_f32 v172,v172
v_mfma_f32_16x16x32_bf16 a[96:99],a[192:195],v[232:235],a[96:99]
ds_read_b64_tr_b16 a[192:193],v2
v_cvt_pk_bf16_f32 v168,v168,v169
v_mfma_f32_16x16x32_bf16 a[96:99],a[196:199],v[236:239],a[96:99]
ds_read_b64_tr_b16 a[194:195],v2 offset:4096
v_cvt_pk_bf16_f32 v169,v170,v171
v_mfma_f32_16x16x32_bf16 a[100:103],a[204:207],v[236:239],a[100:103]
v_exp_f32 v173,v173
v_mfma_f32_16x16x32_bf16 a[100:103],a[200:203],v[232:235],a[100:103]
ds_read_b64_tr_b16 a[196:197],v2 offset:8192
v_cvt_pk_bf16_f32 v170,v172,v173
v_mfma_f32_16x16x32_bf16 a[104:107],a[208:211],v[232:235],a[104:107]
v_exp_f32 v174,v174
v_mfma_f32_16x16x32_bf16 a[104:107],a[212:215],v[236:239],a[104:107]
v_exp_f32 v175,v175
v_mfma_f32_16x16x32_bf16 a[108:111],a[220:223],v[236:239],a[108:111]
buffer_load_dwordx4 v4,s[24:27],s47 offen offset:2048 nt lds
v_mfma_f32_16x16x32_bf16 a[108:111],a[216:219],v[232:235],a[108:111]
ds_read_b64_tr_b16 a[198:199],v2 offset:12288
v_cvt_pk_bf16_f32 v171,v174,v175
v_mfma_f32_16x16x32_bf16 a[112:115],a[224:227],v[232:235],a[112:115]
v_exp_f32 v184,v184
v_mfma_f32_16x16x32_bf16 a[112:115],a[228:231],v[236:239],a[112:115]
v_exp_f32 v185,v185
v_mfma_f32_16x16x32_bf16 a[116:119],a[236:239],v[236:239],a[116:119]
ds_read_b64_tr_b16 a[204:205],v3 offset:8192
v_cvt_pk_bf16_f32 v136,v136,v137
v_mfma_f32_16x16x32_bf16 a[116:119],a[232:235],v[232:235],a[116:119]
ds_read_b64_tr_b16 a[206:207],v3 offset:12288
v_cvt_pk_bf16_f32 v172,v184,v185
v_mfma_f32_16x16x32_bf16 a[120:123],a[240:243],v[232:235],a[120:123]
ds_read_b64_tr_b16 a[200:201],v3
v_mfma_f32_16x16x32_bf16 a[120:123],a[244:247],v[236:239],a[120:123]
ds_read_b64_tr_b16 a[202:203],v3 offset:4096
v_cvt_pk_bf16_f32 v137,v138,v139
v_mfma_f32_16x16x32_bf16 a[124:127],a[252:255],v[236:239],a[124:127]
ds_read_b64_tr_b16 a[208:209],v2 offset:256
v_mfma_f32_16x16x32_bf16 a[124:127],a[248:251],v[232:235],a[124:127]
ds_read_b64_tr_b16 a[210:211],v2 offset:4352
v_smfmac_f32_16x16x64_bf16 v[60:63],v[28:31],v[232:239],v27
ds_read_b64_tr_b16 a[212:213],v2 offset:8448
ds_read_b64_tr_b16 a[214:215],v2 offset:12544
v_cvt_pk_bf16_f32 v138,v140,v141
ds_read_b64_tr_b16 a[220:221],v3 offset:8448
ds_read_b64_tr_b16 a[222:223],v3 offset:12544
v_cvt_pk_bf16_f32 v139,v142,v143
buffer_load_dwordx4 v5,s[24:27],s47 offen offset:3072 nt lds
s_or_b32 s47,s47,0x18000
s_add_u32 s47,s47,4096
ds_read_b64_tr_b16 a[216:217],v3 offset:256
s_and_b32 s47,s47,0x27000
ds_read_b64_tr_b16 a[218:219],v3 offset:4352
v_cvt_pk_bf16_f32 v140,v152,v153
s_or_b32 s47,s47,s46
ds_read_b64_tr_b16 a[224:225],v2 offset:512
ds_read_b64_tr_b16 a[226:227],v2 offset:4608
ds_read_b64_tr_b16 a[228:229],v2 offset:8704
v_cvt_pk_bf16_f32 v141,v154,v155
ds_read_b64_tr_b16 a[230:231],v2 offset:12800
ds_read_b64_tr_b16 a[236:237],v3 offset:8704
ds_read_b64_tr_b16 a[238:239],v3 offset:12800
v_cvt_pk_bf16_f32 v142,v156,v157
ds_read_b64_tr_b16 a[232:233],v3 offset:512
ds_read_b64_tr_b16 a[234:235],v3 offset:4608
ds_read_b64_tr_b16 a[240:241],v2 offset:768
v_cvt_pk_bf16_f32 v143,v158,v159
buffer_load_dword v26,v24,s[88:91],s93 offen
s_waitcnt lgkmcnt(15)
v_mfma_f32_16x16x32_bf16 a[0:3],a[192:195],v[136:139],a[0:3]
ds_read_b64_tr_b16 a[242:243],v2 offset:4864
v_mfma_f32_16x16x32_bf16 a[0:3],a[196:199],v[140:143],a[0:3]
ds_read_b64_tr_b16 a[244:245],v2 offset:8960
v_mfma_f32_16x16x32_bf16 a[4:7],a[204:207],v[140:143],a[4:7]
ds_read_b64_tr_b16 a[246:247],v2 offset:13056
v_mfma_f32_16x16x32_bf16 a[4:7],a[200:203],v[136:139],a[4:7]
ds_read_b64_tr_b16 a[252:253],v3 offset:8960
v_mfma_f32_16x16x32_bf16 a[8:11],a[208:211],v[136:139],a[8:11]
s_waitcnt lgkmcnt(15)
ds_read_b64_tr_b16 a[254:255],v3 offset:13056
v_mfma_f32_16x16x32_bf16 a[8:11],a[212:215],v[140:143],a[8:11]
ds_read_b64_tr_b16 a[248:249],v3 offset:768
v_mfma_f32_16x16x32_bf16 a[12:15],a[220:223],v[140:143],a[12:15]
s_waitcnt lgkmcnt(15)
ds_read_b64_tr_b16 a[250:251],v3 offset:4864
v_mfma_f32_16x16x32_bf16 a[12:15],a[216:219],v[136:139],a[12:15]
s_bitcmp1_b32 s45,0
s_cbranch_scc0 L_t31v20_%=
s_mov_b32 m0,s80
s_add_u32 s80,s80,4096
buffer_load_dwordx4 v25,s[20:23],s83 offen nt lds
s_add_u32 s83,s83,256
L_t31v20_%=:
s_waitcnt lgkmcnt(7)
v_mfma_f32_16x16x32_bf16 a[16:19],a[224:227],v[136:139],a[16:19]
v_exp_f32 v186,v186
v_mfma_f32_16x16x32_bf16 a[16:19],a[228:231],v[140:143],a[16:19]
v_exp_f32 v187,v187
v_mfma_f32_16x16x32_bf16 a[20:23],a[236:239],v[140:143],a[20:23]
v_exp_f32 v188,v188
v_mfma_f32_16x16x32_bf16 a[20:23],a[232:235],v[136:139],a[20:23]
s_waitcnt lgkmcnt(3)
v_exp_f32 v189,v189
v_mfma_f32_16x16x32_bf16 a[24:27],a[240:243],v[136:139],a[24:27]
v_exp_f32 v190,v190
v_mfma_f32_16x16x32_bf16 a[24:27],a[244:247],v[140:143],a[24:27]
s_waitcnt lgkmcnt(1)
v_cvt_pk_bf16_f32 v173,v186,v187
v_mfma_f32_16x16x32_bf16 a[28:31],a[252:255],v[140:143],a[28:31]
s_waitcnt lgkmcnt(0)
v_exp_f32 v191,v191
v_mfma_f32_16x16x32_bf16 a[28:31],a[248:251],v[136:139],a[28:31]
v_cvt_pk_bf16_f32 v174,v188,v189
v_cvt_pk_bf16_f32 v175,v190,v191
s_cmp_lg_u32 s53,0
v_smfmac_f32_16x16x64_bf16 v[48:51],v[28:31],v[136:143],v27
s_cbranch_scc1 L_att0v2_%=
s_waitcnt lgkmcnt(0)
s_nop 7
s_branch L_att0x_%=
.p2align 5
L_att0_%=:
v_mfma_f32_16x16x32_bf16 v[232:235],a[128:131],v[112:115],v[44:47]
s_waitcnt vmcnt(8) lgkmcnt(0)
v_exp_f32 v200,v200
v_mfma_f32_16x16x32_bf16 v[232:235],a[132:135],v[116:119],v[232:235]
s_barrier
s_add_u32 m0,s49,s51
ds_read_b128 a[128:131],v1
v_mfma_f32_16x16x32_bf16 v[232:235],a[136:139],v[120:123],v[232:235]
s_mov_b32 s90,L_endseg_%=-L_pc_%=+128
v_exp_f32 v201,v201
v_mfma_f32_16x16x32_bf16 v[232:235],a[140:143],v[124:127],v[232:235]
s_lshr_b32 s45,s45,1
ds_read_b128 a[132:135],v1 offset:256
v_cvt_pk_bf16_f32 v200,v200,v201
v_mfma_f32_16x16x32_bf16 v[236:239],a[156:159],v[124:127],v[44:47]
s_sub_u32 s94,s94,3
buffer_load_dwordx4 v4,s[20:23],s47 offen nt lds
v_mfma_f32_16x16x32_bf16 v[236:239],a[152:155],v[120:123],v[236:239]
s_lshr_b32 s60,s94,2
ds_read_b128 a[136:139],v1 offset:512
v_add_u32 v2,s48,v2
v_mfma_f32_16x16x32_bf16 v[236:239],a[148:151],v[116:119],v[236:239]
s_max_i32 s60,s60,128
v_exp_f32 v202,v202
v_mfma_f32_16x16x32_bf16 v[236:239],a[144:147],v[112:115],v[236:239]
s_add_u32 s62,s50,32768
ds_read_b128 a[140:143],v1 offset:768
v_add_u32 v3,s48,v3
v_mfma_f32_16x16x32_bf16 v[248:251],a[160:163],v[112:115],v[44:47]
s_and_b32 s62,s62,131071
v_exp_f32 v203,v203
v_mfma_f32_16x16x32_bf16 v[248:251],a[164:167],v[116:119],v[248:251]
s_add_u32 s54,s54,16384
ds_read_b128 a[156:159],v1 offset:4864
v_cvt_pk_bf16_f32 v201,v202,v203
v_mfma_f32_16x16x32_bf16 v[248:251],a[168:171],v[120:123],v[248:251]
)A"
R"A(s_sub_u32 s48,s62,s50
v_exp_f32 v204,v204
v_mfma_f32_16x16x32_bf16 v[248:251],a[172:175],v[124:127],v[248:251]
s_sub_u32 s53,s53,1
v_exp_f32 v205,v205
v_mfma_f32_16x16x32_bf16 v[252:255],a[188:191],v[124:127],v[44:47]
s_and_b32 s46,s54,0xfffc0000
ds_read_b128 a[152:155],v1 offset:4608
v_cvt_pk_bf16_f32 v202,v204,v205
v_mfma_f32_16x16x32_bf16 v[252:255],a[184:187],v[120:123],v[252:255]
s_mov_b32 s50,s62
v_exp_f32 v206,v206
v_mfma_f32_16x16x32_bf16 v[252:255],a[180:183],v[116:119],v[252:255]
v_exp_f32 v207,v207
v_mfma_f32_16x16x32_bf16 v[252:255],a[176:179],v[112:115],v[252:255]
ds_read_b128 a[148:151],v1 offset:4352
v_cvt_pk_bf16_f32 v203,v206,v207
v_mfma_f32_16x16x32_bf16 a[32:35],a[192:195],v[168:171],a[32:35]
v_exp_f32 v216,v216
v_mfma_f32_16x16x32_bf16 a[32:35],a[196:199],v[172:175],a[32:35]
ds_read_b128 a[144:147],v1 offset:4096
v_mfma_f32_16x16x32_bf16 a[36:39],a[204:207],v[172:175],a[36:39]
buffer_load_dwordx4 v4,s[20:23],s47 offen offset:1024 nt lds
v_mfma_f32_16x16x32_bf16 a[36:39],a[200:203],v[168:171],a[36:39]
ds_read_b128 a[160:163],v1 offset:8192
v_mfma_f32_16x16x32_bf16 a[40:43],a[208:211],v[168:171],a[40:43]
v_exp_f32 v217,v217
v_mfma_f32_16x16x32_bf16 a[40:43],a[212:215],v[172:175],a[40:43]
ds_read_b128 a[164:167],v1 offset:8448
v_cvt_pk_bf16_f32 v204,v216,v217
v_mfma_f32_16x16x32_bf16 a[44:47],a[220:223],v[172:175],a[44:47]
v_exp_f32 v218,v218
v_mfma_f32_16x16x32_bf16 a[44:47],a[216:219],v[168:171],a[44:47]
ds_read_b128 a[168:171],v1 offset:8704
v_mfma_f32_16x16x32_bf16 a[48:51],a[224:227],v[168:171],a[48:51]
v_exp_f32 v219,v219
v_mfma_f32_16x16x32_bf16 a[48:51],a[228:231],v[172:175],a[48:51]
ds_read_b128 a[172:175],v1 offset:8960
v_cvt_pk_bf16_f32 v205,v218,v219
v_mfma_f32_16x16x32_bf16 a[52:55],a[236:239],v[172:175],a[52:55]
v_exp_f32 v220,v220
v_mfma_f32_16x16x32_bf16 a[52:55],a[232:235],v[168:171],a[52:55]
ds_read_b128 a[188:191],v1 offset:13056
v_mfma_f32_16x16x32_bf16 a[56:59],a[240:243],v[168:171],a[56:59]
v_exp_f32 v221,v221
v_mfma_f32_16x16x32_bf16 a[56:59],a[244:247],v[172:175],a[56:59]
ds_read_b128 a[184:187],v1 offset:12800
v_cvt_pk_bf16_f32 v206,v220,v221
v_mfma_f32_16x16x32_bf16 a[60:63],a[252:255],v[172:175],a[60:63]
v_exp_f32 v222,v222
v_mfma_f32_16x16x32_bf16 a[60:63],a[248:251],v[168:171],a[60:63]
ds_read_b128 a[180:183],v1 offset:12544
v_smfmac_f32_16x16x64_bf16 v[52:55],v[28:31],v[168:175],v27
s_waitcnt lgkmcnt(4)
v_mfma_f32_16x16x32_bf16 v[136:139],a[128:131],v[64:67],v[32:35]
buffer_load_dwordx4 v5,s[20:23],s47 offen offset:2048 nt lds
v_mfma_f32_16x16x32_bf16 v[136:139],a[132:135],v[68:71],v[136:139]
ds_read_b128 a[176:179],v1 offset:12288
v_add_u32 v1,s48,v1
v_mfma_f32_16x16x32_bf16 v[136:139],a[136:139],v[72:75],v[136:139]
v_exp_f32 v223,v223
v_mfma_f32_16x16x32_bf16 v[136:139],a[140:143],v[76:79],v[136:139]
v_cvt_pk_bf16_f32 v207,v222,v223
v_mfma_f32_16x16x32_bf16 v[140:143],a[156:159],v[76:79],v[32:35]
v_exp_f32 v232,v232
v_mfma_f32_16x16x32_bf16 v[140:143],a[152:155],v[72:75],v[140:143]
v_exp_f32 v233,v233
v_mfma_f32_16x16x32_bf16 v[140:143],a[148:151],v[68:71],v[140:143]
v_exp_f32 v234,v234
v_mfma_f32_16x16x32_bf16 v[140:143],a[144:147],v[64:67],v[140:143]
v_exp_f32 v235,v235
v_mfma_f32_16x16x32_bf16 v[152:155],a[160:163],v[64:67],v[32:35]
v_exp_f32 v236,v236
v_mfma_f32_16x16x32_bf16 v[152:155],a[164:167],v[68:71],v[152:155]
v_exp_f32 v237,v237
v_mfma_f32_16x16x32_bf16 v[152:155],a[168:171],v[72:75],v[152:155]
s_waitcnt lgkmcnt(0)
v_exp_f32 v238,v238
v_mfma_f32_16x16x32_bf16 v[152:155],a[172:175],v[76:79],v[152:155]
v_exp_f32 v239,v239
v_mfma_f32_16x16x32_bf16 v[156:159],a[188:191],v[76:79],v[32:35]
v_exp_f32 v248,v248
v_mfma_f32_16x16x32_bf16 v[156:159],a[184:187],v[72:75],v[156:159]
buffer_load_dwordx4 v5,s[20:23],s47 offen offset:3072 nt lds
v_mfma_f32_16x16x32_bf16 v[156:159],a[180:183],v[68:71],v[156:159]
v_exp_f32 v249,v249
v_mfma_f32_16x16x32_bf16 v[156:159],a[176:179],v[64:67],v[156:159]
v_exp_f32 v250,v250
v_mfma_f32_16x16x32_bf16 a[64:67],a[192:195],v[200:203],a[64:67]
v_exp_f32 v251,v251
v_mfma_f32_16x16x32_bf16 a[64:67],a[196:199],v[204:207],a[64:67]
v_exp_f32 v252,v252
v_mfma_f32_16x16x32_bf16 a[68:71],a[204:207],v[204:207],a[68:71]
v_exp_f32 v253,v253
v_mfma_f32_16x16x32_bf16 a[68:71],a[200:203],v[200:203],a[68:71]
v_exp_f32 v254,v254
v_mfma_f32_16x16x32_bf16 a[72:75],a[208:211],v[200:203],a[72:75]
v_exp_f32 v255,v255
v_mfma_f32_16x16x32_bf16 a[72:75],a[212:215],v[204:207],a[72:75]
v_exp_f32 v136,v136
v_mfma_f32_16x16x32_bf16 a[76:79],a[220:223],v[204:207],a[76:79]
v_exp_f32 v137,v137
v_mfma_f32_16x16x32_bf16 a[76:79],a[216:219],v[200:203],a[76:79]
v_exp_f32 v138,v138
v_mfma_f32_16x16x32_bf16 a[80:83],a[224:227],v[200:203],a[80:83]
s_add_u32 m0,s49,s52
v_cvt_pk_bf16_f32 v232,v232,v233
v_cvt_pk_bf16_f32 v233,v234,v235
v_mfma_f32_16x16x32_bf16 a[80:83],a[228:231],v[204:207],a[80:83]
s_xor_b32 s49,s50,65536
v_cvt_pk_bf16_f32 v234,v236,v237
v_cvt_pk_bf16_f32 v235,v238,v239
v_mfma_f32_16x16x32_bf16 a[84:87],a[236:239],v[204:207],a[84:87]
v_cvt_pk_bf16_f32 v236,v248,v249
v_cvt_pk_bf16_f32 v237,v250,v251
v_mfma_f32_16x16x32_bf16 a[84:87],a[232:235],v[200:203],a[84:87]
buffer_load_dwordx4 v4,s[24:27],s47 offen nt lds
v_mfma_f32_16x16x32_bf16 a[88:91],a[240:243],v[200:203],a[88:91]
v_cvt_pk_bf16_f32 v238,v252,v253
v_cvt_pk_bf16_f32 v239,v254,v255
v_mfma_f32_16x16x32_bf16 a[88:91],a[244:247],v[204:207],a[88:91]
v_exp_f32 v139,v139
v_mfma_f32_16x16x32_bf16 a[92:95],a[252:255],v[204:207],a[92:95]
v_exp_f32 v140,v140
v_mfma_f32_16x16x32_bf16 a[92:95],a[248:251],v[200:203],a[92:95]
v_exp_f32 v141,v141
v_smfmac_f32_16x16x64_bf16 v[56:59],v[28:31],v[200:207],v27
v_mfma_f32_16x16x32_bf16 v[168:171],a[128:131],v[80:83],v[36:39]
v_exp_f32 v142,v142
v_mfma_f32_16x16x32_bf16 v[168:171],a[132:135],v[84:87],v[168:171]
v_exp_f32 v143,v143
v_mfma_f32_16x16x32_bf16 v[168:171],a[136:139],v[88:91],v[168:171]
v_exp_f32 v152,v152
v_mfma_f32_16x16x32_bf16 v[168:171],a[140:143],v[92:95],v[168:171]
v_exp_f32 v153,v153
v_mfma_f32_16x16x32_bf16 v[172:175],a[156:159],v[92:95],v[36:39]
v_exp_f32 v154,v154
v_mfma_f32_16x16x32_bf16 v[172:175],a[152:155],v[88:91],v[172:175]
v_exp_f32 v155,v155
v_mfma_f32_16x16x32_bf16 v[172:175],a[148:151],v[84:87],v[172:175]
v_exp_f32 v156,v156
v_mfma_f32_16x16x32_bf16 v[172:175],a[144:147],v[80:83],v[172:175]
v_exp_f32 v157,v157
v_mfma_f32_16x16x32_bf16 v[184:187],a[160:163],v[80:83],v[36:39]
buffer_load_dwordx4 v5,s[24:27],s47 offen offset:1024 nt lds
v_mfma_f32_16x16x32_bf16 v[184:187],a[164:167],v[84:87],v[184:187]
v_exp_f32 v158,v158
v_mfma_f32_16x16x32_bf16 v[184:187],a[168:171],v[88:91],v[184:187]
v_exp_f32 v159,v159
v_mfma_f32_16x16x32_bf16 v[184:187],a[172:175],v[92:95],v[184:187]
v_exp_f32 v168,v168
v_mfma_f32_16x16x32_bf16 v[188:191],a[188:191],v[92:95],v[36:39]
v_exp_f32 v169,v169
v_mfma_f32_16x16x32_bf16 v[188:191],a[184:187],v[88:91],v[188:191]
v_exp_f32 v170,v170
v_mfma_f32_16x16x32_bf16 v[188:191],a[180:183],v[84:87],v[188:191]
v_exp_f32 v171,v171
v_mfma_f32_16x16x32_bf16 v[188:191],a[176:179],v[80:83],v[188:191]
v_exp_f32 v172,v172
v_mfma_f32_16x16x32_bf16 a[96:99],a[192:195],v[232:235],a[96:99]
ds_read_b64_tr_b16 a[192:193],v2
v_cvt_pk_bf16_f32 v168,v168,v169
v_mfma_f32_16x16x32_bf16 a[96:99],a[196:199],v[236:239],a[96:99]
ds_read_b64_tr_b16 a[194:195],v2 offset:4096
v_cvt_pk_bf16_f32 v169,v170,v171
v_mfma_f32_16x16x32_bf16 a[100:103],a[204:207],v[236:239],a[100:103]
v_exp_f32 v173,v173
v_mfma_f32_16x16x32_bf16 a[100:103],a[200:203],v[232:235],a[100:103]
ds_read_b64_tr_b16 a[196:197],v2 offset:8192
v_cvt_pk_bf16_f32 v170,v172,v173
v_mfma_f32_16x16x32_bf16 a[104:107],a[208:211],v[232:235],a[104:107]
v_exp_f32 v174,v174
v_mfma_f32_16x16x32_bf16 a[104:107],a[212:215],v[236:239],a[104:107]
v_exp_f32 v175,v175
v_mfma_f32_16x16x32_bf16 a[108:111],a[220:223],v[236:239],a[108:111]
buffer_load_dwordx4 v4,s[24:27],s47 offen offset:2048 nt lds
v_mfma_f32_16x16x32_bf16 a[108:111],a[216:219],v[232:235],a[108:111]
ds_read_b64_tr_b16 a[198:199],v2 offset:12288
v_cvt_pk_bf16_f32 v171,v174,v175
v_mfma_f32_16x16x32_bf16 a[112:115],a[224:227],v[232:235],a[112:115]
v_exp_f32 v184,v184
v_mfma_f32_16x16x32_bf16 a[112:115],a[228:231],v[236:239],a[112:115]
v_exp_f32 v185,v185
v_mfma_f32_16x16x32_bf16 a[116:119],a[236:239],v[236:239],a[116:119]
ds_read_b64_tr_b16 a[204:205],v3 offset:8192
v_cvt_pk_bf16_f32 v136,v136,v137
v_mfma_f32_16x16x32_bf16 a[116:119],a[232:235],v[232:235],a[116:119]
ds_read_b64_tr_b16 a[206:207],v3 offset:12288
v_cvt_pk_bf16_f32 v172,v184,v185
v_mfma_f32_16x16x32_bf16 a[120:123],a[240:243],v[232:235],a[120:123]
ds_read_b64_tr_b16 a[200:201],v3
v_mfma_f32_16x16x32_bf16 a[120:123],a[244:247],v[236:239],a[120:123]
ds_read_b64_tr_b16 a[202:203],v3 offset:4096
v_cvt_pk_bf16_f32 v137,v138,v139
v_mfma_f32_16x16x32_bf16 a[124:127],a[252:255],v[236:239],a[124:127]
ds_read_b64_tr_b16 a[208:209],v2 offset:256
v_mfma_f32_16x16x32_bf16 a[124:127],a[248:251],v[232:235],a[124:127]
ds_read_b64_tr_b16 a[210:211],v2 offset:4352
v_smfmac_f32_16x16x64_bf16 v[60:63],v[28:31],v[232:239],v27
ds_read_b64_tr_b16 a[212:213],v2 offset:8448
v_mfma_f32_16x16x32_bf16 v[200:203],a[128:131],v[96:99],v[40:43]
ds_read_b64_tr_b16 a[214:215],v2 offset:12544
v_cvt_pk_bf16_f32 v138,v140,v141
v_mfma_f32_16x16x32_bf16 v[200:203],a[132:135],v[100:103],v[200:203]
ds_read_b64_tr_b16 a[220:221],v3 offset:8448
v_mfma_f32_16x16x32_bf16 v[200:203],a[136:139],v[104:107],v[200:203]
ds_read_b64_tr_b16 a[222:223],v3 offset:12544
v_cvt_pk_bf16_f32 v139,v142,v143
v_mfma_f32_16x16x32_bf16 v[200:203],a[140:143],v[108:111],v[200:203]
buffer_load_dwordx4 v5,s[24:27],s47 offen offset:3072 nt lds
v_mfma_f32_16x16x32_bf16 v[204:207],a[156:159],v[108:111],v[40:43]
s_or_b32 s47,s47,0x18000
s_add_u32 s47,s47,4096
ds_read_b64_tr_b16 a[216:217],v3 offset:256
v_mfma_f32_16x16x32_bf16 v[204:207],a[152:155],v[104:107],v[204:207]
s_and_b32 s47,s47,0x27000
ds_read_b64_tr_b16 a[218:219],v3 offset:4352
v_cvt_pk_bf16_f32 v140,v152,v153
v_mfma_f32_16x16x32_bf16 v[204:207],a[148:151],v[100:103],v[204:207]
s_or_b32 s47,s47,s46
ds_read_b64_tr_b16 a[224:225],v2 offset:512
v_mfma_f32_16x16x32_bf16 v[204:207],a[144:147],v[96:99],v[204:207]
ds_read_b64_tr_b16 a[226:227],v2 offset:4608
v_mfma_f32_16x16x32_bf16 v[216:219],a[160:163],v[96:99],v[40:43]
ds_read_b64_tr_b16 a[228:229],v2 offset:8704
v_cvt_pk_bf16_f32 v141,v154,v155
v_mfma_f32_16x16x32_bf16 v[216:219],a[164:167],v[100:103],v[216:219]
ds_read_b64_tr_b16 a[230:231],v2 offset:12800
v_mfma_f32_16x16x32_bf16 v[216:219],a[168:171],v[104:107],v[216:219]
ds_read_b64_tr_b16 a[236:237],v3 offset:8704
v_mfma_f32_16x16x32_bf16 v[216:219],a[172:175],v[108:111],v[216:219]
ds_read_b64_tr_b16 a[238:239],v3 offset:12800
v_cvt_pk_bf16_f32 v142,v156,v157
v_mfma_f32_16x16x32_bf16 v[220:223],a[188:191],v[108:111],v[40:43]
ds_read_b64_tr_b16 a[232:233],v3 offset:512
v_mfma_f32_16x16x32_bf16 v[220:223],a[184:187],v[104:107],v[220:223]
ds_read_b64_tr_b16 a[234:235],v3 offset:4608
v_mfma_f32_16x16x32_bf16 v[220:223],a[180:183],v[100:103],v[220:223]
ds_read_b64_tr_b16 a[240:241],v2 offset:768
v_cvt_pk_bf16_f32 v143,v158,v159
v_mfma_f32_16x16x32_bf16 v[220:223],a[176:179],v[96:99],v[220:223]
s_waitcnt lgkmcnt(7)
buffer_load_dword v26,v24,s[88:91],s93 offen
v_mfma_f32_16x16x32_bf16 a[0:3],a[192:195],v[136:139],a[0:3]
ds_read_b64_tr_b16 a[242:243],v2 offset:4864
v_mfma_f32_16x16x32_bf16 a[0:3],a[196:199],v[140:143],a[0:3]
ds_read_b64_tr_b16 a[244:245],v2 offset:8960
v_mfma_f32_16x16x32_bf16 a[4:7],a[204:207],v[140:143],a[4:7]
ds_read_b64_tr_b16 a[246:247],v2 offset:13056
v_mfma_f32_16x16x32_bf16 a[4:7],a[200:203],v[136:139],a[4:7]
s_memrealtime s[76:77]
ds_read_b64_tr_b16 a[252:253],v3 offset:8960
v_mfma_f32_16x16x32_bf16 a[8:11],a[208:211],v[136:139],a[8:11]
ds_read_b64_tr_b16 a[254:255],v3 offset:13056
v_mfma_f32_16x16x32_bf16 a[8:11],a[212:215],v[140:143],a[8:11]
ds_read_b64_tr_b16 a[248:249],v3 offset:768
v_mfma_f32_16x16x32_bf16 a[12:15],a[220:223],v[140:143],a[12:15]
ds_read_b64_tr_b16 a[250:251],v3 offset:4864
v_mfma_f32_16x16x32_bf16 a[12:15],a[216:219],v[136:139],a[12:15]
)A"
R"A(s_bitcmp1_b32 s45,0
s_cbranch_scc0 L_t310_%=
s_mov_b32 m0,s80
s_add_u32 s80,s80,4096
buffer_load_dwordx4 v25,s[20:23],s83 offen nt lds
s_add_u32 s83,s83,256
L_t310_%=:
v_mfma_f32_16x16x32_bf16 a[16:19],a[224:227],v[136:139],a[16:19]
s_waitcnt lgkmcnt(6)
v_exp_f32 v186,v186
v_mfma_f32_16x16x32_bf16 a[16:19],a[228:231],v[140:143],a[16:19]
v_exp_f32 v187,v187
v_mfma_f32_16x16x32_bf16 a[20:23],a[236:239],v[140:143],a[20:23]
s_waitcnt lgkmcnt(0)
v_exp_f32 v188,v188
v_mfma_f32_16x16x32_bf16 a[20:23],a[232:235],v[136:139],a[20:23]
s_sub_u32 s79,s78,s76
v_exp_f32 v189,v189
v_mfma_f32_16x16x32_bf16 a[24:27],a[240:243],v[136:139],a[24:27]
s_add_u32 s78,s76,s60
v_exp_f32 v190,v190
v_mfma_f32_16x16x32_bf16 a[24:27],a[244:247],v[140:143],a[24:27]
s_cmp_gt_i32 s79,s95
s_cbranch_scc1 L_ps3_%=
L_pr3_%=:
v_cvt_pk_bf16_f32 v173,v186,v187
v_mfma_f32_16x16x32_bf16 a[28:31],a[252:255],v[140:143],a[28:31]
v_exp_f32 v191,v191
v_mfma_f32_16x16x32_bf16 a[28:31],a[248:251],v[136:139],a[28:31]
v_cvt_pk_bf16_f32 v174,v188,v189
v_cvt_pk_bf16_f32 v175,v190,v191
s_cmp_lg_u32 s53,0
v_smfmac_f32_16x16x64_bf16 v[48:51],v[28:31],v[136:143],v27
s_cbranch_scc1 L_att0_%=
L_att0x_%=:
v_mfma_f32_16x16x32_bf16 v[232:235],a[128:131],v[112:115],v[44:47]
s_waitcnt vmcnt(10)
s_sub_u32 s94,s94,3
v_exp_f32 v200,v200
v_mfma_f32_16x16x32_bf16 v[232:235],a[132:135],v[116:119],v[232:235]
s_barrier
s_add_u32 m0,0,s51
ds_read_b128 a[128:131],v1
v_mfma_f32_16x16x32_bf16 v[232:235],a[136:139],v[120:123],v[232:235]
s_lshr_b32 s60,s94,2
v_exp_f32 v201,v201
v_mfma_f32_16x16x32_bf16 v[232:235],a[140:143],v[124:127],v[232:235]
s_max_i32 s60,s60,128
ds_read_b128 a[132:135],v1 offset:256
v_cvt_pk_bf16_f32 v200,v200,v201
v_mfma_f32_16x16x32_bf16 v[236:239],a[156:159],v[124:127],v[44:47]
s_add_u32 s54,s54,16384
buffer_load_dwordx4 v4,s[20:23],s47 offen nt lds
v_mfma_f32_16x16x32_bf16 v[236:239],a[152:155],v[120:123],v[236:239]
s_and_b32 s46,s54,0xfffc0000
ds_read_b128 a[136:139],v1 offset:512
v_and_b32 v18,48,v6
v_mfma_f32_16x16x32_bf16 v[236:239],a[148:151],v[116:119],v[236:239]
s_sub_u32 s94,s94,3
v_exp_f32 v202,v202
v_mfma_f32_16x16x32_bf16 v[236:239],a[144:147],v[112:115],v[236:239]
s_add_u32 s54,s54,16384
ds_read_b128 a[140:143],v1 offset:768
v_lshrrev_b32 v19,4,v6
v_mfma_f32_16x16x32_bf16 v[248:251],a[160:163],v[112:115],v[44:47]
s_mul_i32 s83,s41,5
v_exp_f32 v203,v203
v_mfma_f32_16x16x32_bf16 v[248:251],a[164:167],v[116:119],v[248:251]
s_lshl_b32 s43,s40,8
ds_read_b128 a[156:159],v1 offset:4864
v_cvt_pk_bf16_f32 v201,v202,v203
v_mfma_f32_16x16x32_bf16 v[248:251],a[168:171],v[120:123],v[248:251]
s_add_u32 s83,s83,s42
v_exp_f32 v204,v204
v_mfma_f32_16x16x32_bf16 v[248:251],a[172:175],v[124:127],v[248:251]
s_add_u32 s83,s83,15
ds_read_b128 a[152:155],v1 offset:4608
v_lshlrev_b32 v18,2,v18
v_mfma_f32_16x16x32_bf16 v[252:255],a[188:191],v[124:127],v[44:47]
s_lshl_b32 s44,s40,5
v_exp_f32 v205,v205
v_mfma_f32_16x16x32_bf16 v[252:255],a[184:187],v[120:123],v[252:255]
s_add_u32 s84,s55,0
ds_read_b128 a[148:151],v1 offset:4352
v_cvt_pk_bf16_f32 v202,v204,v205
v_mfma_f32_16x16x32_bf16 v[252:255],a[180:183],v[116:119],v[252:255]
s_add_u32 s85,s55,4096
v_exp_f32 v206,v206
v_mfma_f32_16x16x32_bf16 v[252:255],a[176:179],v[112:115],v[252:255]
s_add_u32 s86,s55,16384
ds_read_b128 a[144:147],v1 offset:4096
v_and_b32 v19,48,v19
v_mfma_f32_16x16x32_bf16 a[32:35],a[192:195],v[168:171],a[32:35]
s_add_u32 s87,s55,20480
v_exp_f32 v207,v207
v_mfma_f32_16x16x32_bf16 a[32:35],a[196:199],v[172:175],a[32:35]
s_and_b32 s44,s44,32
ds_read_b128 a[160:163],v1 offset:8192
v_cvt_pk_bf16_f32 v203,v206,v207
v_mfma_f32_16x16x32_bf16 a[36:39],a[204:207],v[172:175],a[36:39]
s_max_i32 s84,s84,s18
buffer_load_dwordx4 v4,s[20:23],s47 offen offset:1024 nt lds
v_mfma_f32_16x16x32_bf16 a[36:39],a[200:203],v[168:171],a[36:39]
s_max_i32 s85,s85,s19
ds_read_b128 a[164:167],v1 offset:8448
v_add_u32 v18,v18,v19
v_mfma_f32_16x16x32_bf16 a[40:43],a[208:211],v[168:171],a[40:43]
s_max_i32 s86,s86,s100
v_exp_f32 v216,v216
v_mfma_f32_16x16x32_bf16 a[40:43],a[212:215],v[172:175],a[40:43]
s_max_i32 s87,s87,s101
ds_read_b128 a[168:171],v1 offset:8704
v_add_u32 v18,98304,v18
v_mfma_f32_16x16x32_bf16 a[44:47],a[220:223],v[172:175],a[44:47]
s_lshl_b32 s80,s40,10
v_exp_f32 v217,v217
v_mfma_f32_16x16x32_bf16 a[44:47],a[216:219],v[168:171],a[44:47]
s_mov_b32 s50,32768
ds_read_b128 a[172:175],v1 offset:8960
v_cvt_pk_bf16_f32 v204,v216,v217
v_mfma_f32_16x16x32_bf16 a[48:51],a[224:227],v[168:171],a[48:51]
s_mov_b32 s49,98304
v_exp_f32 v218,v218
v_mfma_f32_16x16x32_bf16 a[48:51],a[228:231],v[172:175],a[48:51]
s_mov_b32 s45,0x2a8
ds_read_b128 a[188:191],v1 offset:13056
v_add_u32 v2,s48,v2
v_mfma_f32_16x16x32_bf16 a[52:55],a[236:239],v[172:175],a[52:55]
s_add_u32 s93,s92,L_endseg_%=-L_pc_%=
v_exp_f32 v219,v219
v_mfma_f32_16x16x32_bf16 a[52:55],a[232:235],v[168:171],a[52:55]
ds_read_b128 a[184:187],v1 offset:12800
v_cvt_pk_bf16_f32 v205,v218,v219
v_mfma_f32_16x16x32_bf16 a[56:59],a[240:243],v[168:171],a[56:59]
v_exp_f32 v220,v220
v_mfma_f32_16x16x32_bf16 a[56:59],a[244:247],v[172:175],a[56:59]
ds_read_b128 a[180:183],v1 offset:12544
v_add_u32 v3,s48,v3
v_mfma_f32_16x16x32_bf16 a[60:63],a[252:255],v[172:175],a[60:63]
s_mov_b32 s48,32768
v_exp_f32 v221,v221
v_mfma_f32_16x16x32_bf16 a[60:63],a[248:251],v[168:171],a[60:63]
ds_read_b128 a[176:179],v1 offset:12288
v_cvt_pk_bf16_f32 v206,v220,v221
v_smfmac_f32_16x16x64_bf16 v[52:55],v[28:31],v[168:175],v27
s_waitcnt lgkmcnt(4)
v_add_u32 v1,s48,v1
v_mfma_f32_16x16x32_bf16 v[136:139],a[128:131],v[64:67],v[32:35]
buffer_load_dwordx4 v5,s[20:23],s47 offen offset:2048 nt lds
v_mfma_f32_16x16x32_bf16 v[136:139],a[132:135],v[68:71],v[136:139]
v_exp_f32 v222,v222
v_mfma_f32_16x16x32_bf16 v[136:139],a[136:139],v[72:75],v[136:139]
v_exp_f32 v223,v223
v_mfma_f32_16x16x32_bf16 v[136:139],a[140:143],v[76:79],v[136:139]
v_cvt_pk_bf16_f32 v207,v222,v223
v_subrev_u32 v25,s43,v4
v_mfma_f32_16x16x32_bf16 v[140:143],a[156:159],v[76:79],v[32:35]
s_lshl_b32 s43,s83,14
v_exp_f32 v232,v232
v_mfma_f32_16x16x32_bf16 v[140:143],a[152:155],v[72:75],v[140:143]
s_lshl_b32 s83,s83,12
v_exp_f32 v233,v233
v_mfma_f32_16x16x32_bf16 v[140:143],a[148:151],v[68:71],v[140:143]
s_and_b32 s43,s43,0x20000
v_exp_f32 v234,v234
v_mfma_f32_16x16x32_bf16 v[140:143],a[144:147],v[64:67],v[140:143]
s_and_b32 s83,s83,0x7000
v_exp_f32 v235,v235
v_mfma_f32_16x16x32_bf16 v[152:155],a[160:163],v[64:67],v[32:35]
s_or_b32 s83,s83,s43
v_cvt_pk_bf16_f32 v232,v232,v233
v_cvt_pk_bf16_f32 v233,v234,v235
v_mfma_f32_16x16x32_bf16 v[152:155],a[164:167],v[68:71],v[152:155]
s_or_b32 s83,s83,0x40000
v_exp_f32 v236,v236
v_mfma_f32_16x16x32_bf16 v[152:155],a[168:171],v[72:75],v[152:155]
v_exp_f32 v237,v237
v_mfma_f32_16x16x32_bf16 v[152:155],a[172:175],v[76:79],v[152:155]
s_waitcnt lgkmcnt(0)
v_exp_f32 v238,v238
v_mfma_f32_16x16x32_bf16 v[156:159],a[188:191],v[76:79],v[32:35]
v_exp_f32 v239,v239
v_mfma_f32_16x16x32_bf16 v[156:159],a[184:187],v[72:75],v[156:159]
buffer_load_dwordx4 v5,s[20:23],s47 offen offset:3072 nt lds
v_mfma_f32_16x16x32_bf16 v[156:159],a[180:183],v[68:71],v[156:159]
v_cvt_pk_bf16_f32 v234,v236,v237
v_cvt_pk_bf16_f32 v235,v238,v239
v_mfma_f32_16x16x32_bf16 v[156:159],a[176:179],v[64:67],v[156:159]
v_exp_f32 v248,v248
v_mfma_f32_16x16x32_bf16 a[64:67],a[192:195],v[200:203],a[64:67]
v_exp_f32 v249,v249
v_mfma_f32_16x16x32_bf16 a[64:67],a[196:199],v[204:207],a[64:67]
v_exp_f32 v250,v250
v_mfma_f32_16x16x32_bf16 a[68:71],a[204:207],v[204:207],a[68:71]
v_exp_f32 v251,v251
v_mfma_f32_16x16x32_bf16 a[68:71],a[200:203],v[200:203],a[68:71]
v_exp_f32 v252,v252
v_mfma_f32_16x16x32_bf16 a[72:75],a[208:211],v[200:203],a[72:75]
v_exp_f32 v253,v253
v_mfma_f32_16x16x32_bf16 a[72:75],a[212:215],v[204:207],a[72:75]
v_exp_f32 v254,v254
v_mfma_f32_16x16x32_bf16 a[76:79],a[220:223],v[204:207],a[76:79]
v_exp_f32 v255,v255
v_mfma_f32_16x16x32_bf16 a[76:79],a[216:219],v[200:203],a[76:79]
v_cvt_pk_bf16_f32 v236,v248,v249
v_cvt_pk_bf16_f32 v237,v250,v251
v_mfma_f32_16x16x32_bf16 a[80:83],a[224:227],v[200:203],a[80:83]
s_add_u32 m0,0,s52
v_cvt_pk_bf16_f32 v238,v252,v253
v_cvt_pk_bf16_f32 v239,v254,v255
v_mfma_f32_16x16x32_bf16 a[80:83],a[228:231],v[204:207],a[80:83]
v_exp_f32 v136,v136
v_mfma_f32_16x16x32_bf16 a[84:87],a[236:239],v[204:207],a[84:87]
v_exp_f32 v137,v137
v_mfma_f32_16x16x32_bf16 a[84:87],a[232:235],v[200:203],a[84:87]
buffer_load_dwordx4 v4,s[24:27],s47 offen nt lds
v_mfma_f32_16x16x32_bf16 a[88:91],a[240:243],v[200:203],a[88:91]
v_exp_f32 v138,v138
v_mfma_f32_16x16x32_bf16 a[88:91],a[244:247],v[204:207],a[88:91]
v_exp_f32 v139,v139
v_mfma_f32_16x16x32_bf16 a[92:95],a[252:255],v[204:207],a[92:95]
v_cvt_pk_bf16_f32 v136,v136,v137
v_cvt_pk_bf16_f32 v137,v138,v139
v_mfma_f32_16x16x32_bf16 a[92:95],a[248:251],v[200:203],a[92:95]
v_exp_f32 v140,v140
v_smfmac_f32_16x16x64_bf16 v[56:59],v[28:31],v[200:207],v27
v_xor_b32 v25,s44,v25
v_mfma_f32_16x16x32_bf16 v[168:171],a[128:131],v[80:83],v[36:39]
v_exp_f32 v141,v141
v_mfma_f32_16x16x32_bf16 v[168:171],a[132:135],v[84:87],v[168:171]
v_exp_f32 v142,v142
v_mfma_f32_16x16x32_bf16 v[168:171],a[136:139],v[88:91],v[168:171]
v_exp_f32 v143,v143
v_mfma_f32_16x16x32_bf16 v[168:171],a[140:143],v[92:95],v[168:171]
v_cvt_pk_bf16_f32 v138,v140,v141
v_cvt_pk_bf16_f32 v139,v142,v143
v_mfma_f32_16x16x32_bf16 v[172:175],a[156:159],v[92:95],v[36:39]
v_exp_f32 v152,v152
v_mfma_f32_16x16x32_bf16 v[172:175],a[152:155],v[88:91],v[172:175]
v_exp_f32 v153,v153
v_mfma_f32_16x16x32_bf16 v[172:175],a[148:151],v[84:87],v[172:175]
v_exp_f32 v154,v154
v_mfma_f32_16x16x32_bf16 v[172:175],a[144:147],v[80:83],v[172:175]
v_exp_f32 v155,v155
v_mfma_f32_16x16x32_bf16 v[184:187],a[160:163],v[80:83],v[36:39]
buffer_load_dwordx4 v5,s[24:27],s47 offen offset:1024 nt lds
v_mfma_f32_16x16x32_bf16 v[184:187],a[164:167],v[84:87],v[184:187]
v_exp_f32 v156,v156
v_mfma_f32_16x16x32_bf16 v[184:187],a[168:171],v[88:91],v[184:187]
v_exp_f32 v157,v157
v_mfma_f32_16x16x32_bf16 v[184:187],a[172:175],v[92:95],v[184:187]
v_exp_f32 v158,v158
v_mfma_f32_16x16x32_bf16 v[188:191],a[188:191],v[92:95],v[36:39]
v_exp_f32 v159,v159
v_mfma_f32_16x16x32_bf16 v[188:191],a[184:187],v[88:91],v[188:191]
v_cvt_pk_bf16_f32 v140,v152,v153
v_cvt_pk_bf16_f32 v141,v154,v155
v_mfma_f32_16x16x32_bf16 v[188:191],a[180:183],v[84:87],v[188:191]
s_waitcnt vmcnt(14)
v_cvt_pk_bf16_f32 v142,v156,v157
v_cvt_pk_bf16_f32 v143,v158,v159
v_mfma_f32_16x16x32_bf16 v[188:191],a[176:179],v[80:83],v[188:191]
s_barrier
ds_read_b128 v[80:83],v18
v_add_u32 v25,s80,v25
v_mfma_f32_16x16x32_bf16 a[96:99],a[192:195],v[232:235],a[96:99]
s_add_u32 s80,s80,147456
v_exp_f32 v168,v168
v_mfma_f32_16x16x32_bf16 a[96:99],a[196:199],v[236:239],a[96:99]
v_exp_f32 v169,v169
v_mfma_f32_16x16x32_bf16 a[100:103],a[204:207],v[236:239],a[100:103]
ds_read_b128 v[84:87],v18 offset:256
v_cvt_pk_bf16_f32 v168,v168,v169
v_mfma_f32_16x16x32_bf16 a[100:103],a[200:203],v[232:235],a[100:103]
v_exp_f32 v170,v170
v_mfma_f32_16x16x32_bf16 a[104:107],a[208:211],v[232:235],a[104:107]
v_exp_f32 v171,v171
v_mfma_f32_16x16x32_bf16 a[104:107],a[212:215],v[236:239],a[104:107]
buffer_load_dwordx4 v4,s[24:27],s47 offen offset:2048 nt lds
v_mfma_f32_16x16x32_bf16 a[108:111],a[220:223],v[236:239],a[108:111]
ds_read_b128 v[88:91],v18 offset:512
v_cvt_pk_bf16_f32 v169,v170,v171
v_mfma_f32_16x16x32_bf16 a[108:111],a[216:219],v[232:235],a[108:111]
v_exp_f32 v172,v172
v_mfma_f32_16x16x32_bf16 a[112:115],a[224:227],v[232:235],a[112:115]
v_exp_f32 v173,v173
v_mfma_f32_16x16x32_bf16 a[112:115],a[228:231],v[236:239],a[112:115]
ds_read_b128 v[92:95],v18 offset:768
v_cvt_pk_bf16_f32 v170,v172,v173
v_mfma_f32_16x16x32_bf16 a[116:119],a[236:239],v[236:239],a[116:119]
v_exp_f32 v174,v174
v_mfma_f32_16x16x32_bf16 a[116:119],a[232:235],v[232:235],a[116:119]
ds_read_b64_tr_b16 a[192:193],v2
ds_read_b64_tr_b16 a[194:195],v2 offset:4096
v_mfma_f32_16x16x32_bf16 a[120:123],a[240:243],v[232:235],a[120:123]
v_exp_f32 v175,v175
v_mfma_f32_16x16x32_bf16 a[120:123],a[244:247],v[236:239],a[120:123]
ds_read_b64_tr_b16 a[196:197],v2 offset:8192
)A"
R"A(ds_read_b64_tr_b16 a[198:199],v2 offset:12288
v_mfma_f32_16x16x32_bf16 a[124:127],a[252:255],v[236:239],a[124:127]
ds_read_b64_tr_b16 a[204:205],v3 offset:8192
v_cvt_pk_bf16_f32 v171,v174,v175
v_mfma_f32_16x16x32_bf16 a[124:127],a[248:251],v[232:235],a[124:127]
v_exp_f32 v184,v184
v_smfmac_f32_16x16x64_bf16 v[60:63],v[28:31],v[232:239],v27
ds_read_b64_tr_b16 a[206:207],v3 offset:12288
v_mfma_f32_16x16x32_bf16 v[200:203],a[128:131],v[96:99],v[40:43]
ds_read_b64_tr_b16 a[200:201],v3
ds_read_b64_tr_b16 a[202:203],v3 offset:4096
v_mfma_f32_16x16x32_bf16 v[200:203],a[132:135],v[100:103],v[200:203]
v_exp_f32 v185,v185
v_mfma_f32_16x16x32_bf16 v[200:203],a[136:139],v[104:107],v[200:203]
buffer_load_dwordx4 v5,s[24:27],s47 offen offset:3072 nt lds
v_mfma_f32_16x16x32_bf16 v[200:203],a[140:143],v[108:111],v[200:203]
s_or_b32 s47,s47,0x18000
ds_read_b64_tr_b16 a[208:209],v2 offset:256
ds_read_b64_tr_b16 a[210:211],v2 offset:4352
v_mfma_f32_16x16x32_bf16 v[204:207],a[156:159],v[108:111],v[40:43]
s_add_u32 s47,s47,4096
s_and_b32 s47,s47,0x27000
ds_read_b64_tr_b16 a[212:213],v2 offset:8448
v_mfma_f32_16x16x32_bf16 v[204:207],a[152:155],v[104:107],v[204:207]
s_or_b32 s47,s47,s46
ds_read_b64_tr_b16 a[214:215],v2 offset:12544
v_cvt_pk_bf16_f32 v172,v184,v185
v_mfma_f32_16x16x32_bf16 v[204:207],a[148:151],v[100:103],v[204:207]
s_and_b32 s46,s54,0xfffc0000
v_exp_f32 v186,v186
v_mfma_f32_16x16x32_bf16 v[204:207],a[144:147],v[96:99],v[204:207]
s_add_u32 s54,s54,16384
ds_read_b64_tr_b16 a[220:221],v3 offset:8448
ds_read_b64_tr_b16 a[222:223],v3 offset:12544
v_mfma_f32_16x16x32_bf16 v[216:219],a[160:163],v[96:99],v[40:43]
v_exp_f32 v187,v187
v_mfma_f32_16x16x32_bf16 v[216:219],a[164:167],v[100:103],v[216:219]
ds_read_b64_tr_b16 a[216:217],v3 offset:256
ds_read_b64_tr_b16 a[218:219],v3 offset:4352
v_mfma_f32_16x16x32_bf16 v[216:219],a[168:171],v[104:107],v[216:219]
ds_read_b64_tr_b16 a[224:225],v2 offset:512
v_cvt_pk_bf16_f32 v173,v186,v187
v_mfma_f32_16x16x32_bf16 v[216:219],a[172:175],v[108:111],v[216:219]
ds_read_b64_tr_b16 a[226:227],v2 offset:4608
v_mfma_f32_16x16x32_bf16 v[220:223],a[188:191],v[108:111],v[40:43]
ds_read_b64_tr_b16 a[228:229],v2 offset:8704
v_mfma_f32_16x16x32_bf16 v[220:223],a[184:187],v[104:107],v[220:223]
ds_read_b64_tr_b16 a[230:231],v2 offset:12800
ds_read_b64_tr_b16 a[236:237],v3 offset:8704
v_mfma_f32_16x16x32_bf16 v[220:223],a[180:183],v[100:103],v[220:223]
ds_read_b64_tr_b16 a[238:239],v3 offset:12800
v_mfma_f32_16x16x32_bf16 v[220:223],a[176:179],v[96:99],v[220:223]
s_waitcnt lgkmcnt(8)
ds_read_b64_tr_b16 a[232:233],v3 offset:512
v_mfma_f32_16x16x32_bf16 v[32:35],v[80:83],v[128:131],0
ds_read_b64_tr_b16 a[234:235],v3 offset:4608
v_mfma_f32_16x16x32_bf16 v[32:35],v[84:87],v[160:163],v[32:35]
ds_read_b64_tr_b16 a[240:241],v2 offset:768
ds_read_b64_tr_b16 a[242:243],v2 offset:4864
v_mfma_f32_16x16x32_bf16 v[32:35],v[88:91],v[192:195],v[32:35]
ds_read_b64_tr_b16 a[244:245],v2 offset:8960
v_mfma_f32_16x16x32_bf16 v[32:35],v[92:95],v[224:227],v[32:35]
ds_read_b64_tr_b16 a[246:247],v2 offset:13056
v_add_u32 v2,s48,v2
v_mfma_f32_16x16x32_bf16 v[36:39],v[80:83],v[132:135],0
ds_read_b64_tr_b16 a[252:253],v3 offset:8960
v_mfma_f32_16x16x32_bf16 v[36:39],v[84:87],v[164:167],v[36:39]
ds_read_b64_tr_b16 a[254:255],v3 offset:13056
ds_read_b64_tr_b16 a[248:249],v3 offset:768
v_mfma_f32_16x16x32_bf16 v[36:39],v[88:91],v[196:199],v[36:39]
ds_read_b64_tr_b16 a[250:251],v3 offset:4864
v_add_u32 v3,s48,v3
v_mfma_f32_16x16x32_bf16 v[36:39],v[92:95],v[228:231],v[36:39]
s_mov_b32 s48,-98304
v_exp_f32 v188,v188
v_mfma_f32_16x16x32_bf16 v[40:43],v[80:83],v[144:147],0
v_exp_f32 v189,v189
v_mfma_f32_16x16x32_bf16 v[40:43],v[84:87],v[176:179],v[40:43]
v_exp_f32 v190,v190
v_mfma_f32_16x16x32_bf16 v[40:43],v[88:91],v[208:211],v[40:43]
v_exp_f32 v191,v191
v_mfma_f32_16x16x32_bf16 v[40:43],v[92:95],v[240:243],v[40:43]
v_cvt_pk_bf16_f32 v174,v188,v189
v_cvt_pk_bf16_f32 v175,v190,v191
v_mfma_f32_16x16x32_bf16 a[0:3],a[192:195],v[136:139],a[0:3]
v_exp_f32 v200,v200
v_mfma_f32_16x16x32_bf16 a[0:3],a[196:199],v[140:143],a[0:3]
v_max3_f32 v32,v32,v33,v34
v_max3_f32 v32,v32,v35,s18
v_mfma_f32_16x16x32_bf16 a[4:7],a[204:207],v[140:143],a[4:7]
v_sub_f32 v35,0,v32
v_sub_f32 v34,0,v32
v_mfma_f32_16x16x32_bf16 a[4:7],a[200:203],v[136:139],a[4:7]
s_memrealtime s[76:77]
v_sub_f32 v33,0,v32
v_sub_f32 v32,0,v32
v_mfma_f32_16x16x32_bf16 a[8:11],a[208:211],v[136:139],a[8:11]
v_exp_f32 v201,v201
v_mfma_f32_16x16x32_bf16 a[8:11],a[212:215],v[140:143],a[8:11]
v_exp_f32 v202,v202
v_mfma_f32_16x16x32_bf16 a[12:15],a[220:223],v[140:143],a[12:15]
s_waitcnt lgkmcnt(0)
v_exp_f32 v203,v203
v_mfma_f32_16x16x32_bf16 a[12:15],a[216:219],v[136:139],a[12:15]
v_cvt_pk_bf16_f32 v200,v200,v201
v_cvt_pk_bf16_f32 v201,v202,v203
v_mfma_f32_16x16x32_bf16 a[16:19],a[224:227],v[136:139],a[16:19]
v_exp_f32 v204,v204
v_mfma_f32_16x16x32_bf16 a[16:19],a[228:231],v[140:143],a[16:19]
v_exp_f32 v205,v205
v_mfma_f32_16x16x32_bf16 a[20:23],a[236:239],v[140:143],a[20:23]
s_waitcnt lgkmcnt(0)
v_exp_f32 v206,v206
v_mfma_f32_16x16x32_bf16 a[20:23],a[232:235],v[136:139],a[20:23]
s_sub_u32 s79,s78,s76
v_exp_f32 v207,v207
v_mfma_f32_16x16x32_bf16 a[24:27],a[240:243],v[136:139],a[24:27]
s_add_u32 s78,s76,s60
v_cvt_pk_bf16_f32 v202,v204,v205
v_cvt_pk_bf16_f32 v203,v206,v207
v_mfma_f32_16x16x32_bf16 a[24:27],a[244:247],v[140:143],a[24:27]
s_cmp_gt_i32 s79,s95
s_cbranch_scc1 L_ps4_%=
L_pr4_%=:
v_max3_f32 v36,v36,v37,v38
v_mfma_f32_16x16x32_bf16 a[28:31],a[252:255],v[140:143],a[28:31]
s_lshr_b32 s60,s94,2
v_exp_f32 v216,v216
v_mfma_f32_16x16x32_bf16 a[28:31],a[248:251],v[136:139],a[28:31]
s_max_i32 s60,s60,128
v_exp_f32 v217,v217
v_smfmac_f32_16x16x64_bf16 v[48:51],v[28:31],v[136:143],v27
s_sub_u32 s94,s94,3
v_cvt_pk_bf16_f32 v204,v216,v217
v_mfma_f32_16x16x32_bf16 v[232:235],a[128:131],v[112:115],v[44:47]
s_waitcnt vmcnt(9)
v_exp_f32 v218,v218
v_mfma_f32_16x16x32_bf16 v[232:235],a[132:135],v[116:119],v[232:235]
s_barrier
s_add_u32 m0,32768,s51
ds_read_b128 a[128:131],v1
v_mfma_f32_16x16x32_bf16 v[232:235],a[136:139],v[120:123],v[232:235]
v_exp_f32 v219,v219
v_mfma_f32_16x16x32_bf16 v[232:235],a[140:143],v[124:127],v[232:235]
ds_read_b128 a[132:135],v1 offset:256
v_cvt_pk_bf16_f32 v205,v218,v219
v_mfma_f32_16x16x32_bf16 v[236:239],a[156:159],v[124:127],v[44:47]
buffer_load_dwordx4 v4,s[20:23],s47 offen nt lds
v_mfma_f32_16x16x32_bf16 v[236:239],a[152:155],v[120:123],v[236:239]
ds_read_b128 a[136:139],v1 offset:512
v_max3_f32 v36,v36,v39,s19
v_mfma_f32_16x16x32_bf16 v[236:239],a[148:151],v[116:119],v[236:239]
v_exp_f32 v220,v220
v_mfma_f32_16x16x32_bf16 v[236:239],a[144:147],v[112:115],v[236:239]
ds_read_b128 a[140:143],v1 offset:768
v_sub_f32 v39,0,v36
v_mfma_f32_16x16x32_bf16 v[248:251],a[160:163],v[112:115],v[44:47]
v_exp_f32 v221,v221
v_mfma_f32_16x16x32_bf16 v[248:251],a[164:167],v[116:119],v[248:251]
ds_read_b128 a[156:159],v1 offset:4864
v_cvt_pk_bf16_f32 v206,v220,v221
v_mfma_f32_16x16x32_bf16 v[248:251],a[168:171],v[120:123],v[248:251]
v_exp_f32 v222,v222
v_mfma_f32_16x16x32_bf16 v[248:251],a[172:175],v[124:127],v[248:251]
ds_read_b128 a[152:155],v1 offset:4608
v_sub_f32 v38,0,v36
v_mfma_f32_16x16x32_bf16 v[252:255],a[188:191],v[124:127],v[44:47]
v_exp_f32 v223,v223
v_mfma_f32_16x16x32_bf16 v[252:255],a[184:187],v[120:123],v[252:255]
ds_read_b128 a[148:151],v1 offset:4352
v_cvt_pk_bf16_f32 v207,v222,v223
v_mfma_f32_16x16x32_bf16 v[252:255],a[180:183],v[116:119],v[252:255]
v_sub_f32 v37,0,v36
v_sub_f32 v36,0,v36
v_mfma_f32_16x16x32_bf16 v[252:255],a[176:179],v[112:115],v[252:255]
ds_read_b128 a[144:147],v1 offset:4096
v_max3_f32 v40,v40,v41,v42
v_mfma_f32_16x16x32_bf16 v[44:47],v[80:83],v[148:151],0
v_exp_f32 v232,v232
v_mfma_f32_16x16x32_bf16 v[44:47],v[84:87],v[180:183],v[44:47]
ds_read_b128 a[160:163],v1 offset:8192
v_max3_f32 v40,v40,v43,s100
v_mfma_f32_16x16x32_bf16 v[44:47],v[88:91],v[212:215],v[44:47]
buffer_load_dwordx4 v4,s[20:23],s47 offen offset:1024 nt lds
v_mfma_f32_16x16x32_bf16 v[44:47],v[92:95],v[244:247],v[44:47]
ds_read_b128 a[164:167],v1 offset:8448
v_sub_f32 v43,0,v40
v_mfma_f32_16x16x32_bf16 a[32:35],a[192:195],v[168:171],a[32:35]
v_exp_f32 v233,v233
v_mfma_f32_16x16x32_bf16 a[32:35],a[196:199],v[172:175],a[32:35]
ds_read_b128 a[168:171],v1 offset:8704
v_cvt_pk_bf16_f32 v232,v232,v233
v_mfma_f32_16x16x32_bf16 a[36:39],a[204:207],v[172:175],a[36:39]
v_exp_f32 v234,v234
v_mfma_f32_16x16x32_bf16 a[36:39],a[200:203],v[168:171],a[36:39]
ds_read_b128 a[172:175],v1 offset:8960
v_sub_f32 v42,0,v40
v_mfma_f32_16x16x32_bf16 a[40:43],a[208:211],v[168:171],a[40:43]
v_exp_f32 v235,v235
v_mfma_f32_16x16x32_bf16 a[40:43],a[212:215],v[172:175],a[40:43]
ds_read_b128 a[188:191],v1 offset:13056
v_cvt_pk_bf16_f32 v233,v234,v235
v_mfma_f32_16x16x32_bf16 a[44:47],a[220:223],v[172:175],a[44:47]
v_exp_f32 v236,v236
v_mfma_f32_16x16x32_bf16 a[44:47],a[216:219],v[168:171],a[44:47]
ds_read_b128 a[184:187],v1 offset:12800
v_sub_f32 v41,0,v40
v_mfma_f32_16x16x32_bf16 a[48:51],a[224:227],v[168:171],a[48:51]
v_exp_f32 v237,v237
v_mfma_f32_16x16x32_bf16 a[48:51],a[228:231],v[172:175],a[48:51]
ds_read_b128 a[180:183],v1 offset:12544
v_cvt_pk_bf16_f32 v234,v236,v237
v_mfma_f32_16x16x32_bf16 a[52:55],a[236:239],v[172:175],a[52:55]
v_exp_f32 v238,v238
v_mfma_f32_16x16x32_bf16 a[52:55],a[232:235],v[168:171],a[52:55]
ds_read_b128 a[176:179],v1 offset:12288
v_sub_f32 v40,0,v40
v_mfma_f32_16x16x32_bf16 a[56:59],a[240:243],v[168:171],a[56:59]
buffer_load_dwordx4 v5,s[20:23],s47 offen offset:2048 nt lds
v_mfma_f32_16x16x32_bf16 a[56:59],a[244:247],v[172:175],a[56:59]
v_exp_f32 v239,v239
v_mfma_f32_16x16x32_bf16 a[60:63],a[252:255],v[172:175],a[60:63]
v_cvt_pk_bf16_f32 v235,v238,v239
v_add_f32 v48,v49,v48
v_mfma_f32_16x16x32_bf16 a[60:63],a[248:251],v[168:171],a[60:63]
v_exp_f32 v248,v248
v_smfmac_f32_16x16x64_bf16 v[52:55],v[28:31],v[168:175],v27
s_waitcnt lgkmcnt(2)
v_max_f32 v20,0x00800000,v48
v_mfma_f32_16x16x32_bf16 v[136:139],a[128:131],v[128:131],v[32:35]
v_exp_f32 v249,v249
v_mfma_f32_16x16x32_bf16 v[136:139],a[132:135],v[160:163],v[136:139]
v_exp_f32 v250,v250
v_mfma_f32_16x16x32_bf16 v[136:139],a[136:139],v[192:195],v[136:139]
v_exp_f32 v251,v251
v_mfma_f32_16x16x32_bf16 v[136:139],a[140:143],v[224:227],v[136:139]
v_exp_f32 v252,v252
v_mfma_f32_16x16x32_bf16 v[140:143],a[156:159],v[224:227],v[32:35]
v_exp_f32 v253,v253
v_mfma_f32_16x16x32_bf16 v[140:143],a[152:155],v[192:195],v[140:143]
v_exp_f32 v254,v254
v_mfma_f32_16x16x32_bf16 v[140:143],a[148:151],v[160:163],v[140:143]
v_exp_f32 v255,v255
v_mfma_f32_16x16x32_bf16 v[140:143],a[144:147],v[128:131],v[140:143]
v_cvt_pk_bf16_f32 v236,v248,v249
v_cvt_pk_bf16_f32 v237,v250,v251
v_mfma_f32_16x16x32_bf16 v[152:155],a[160:163],v[128:131],v[32:35]
v_cvt_pk_bf16_f32 v238,v252,v253
v_cvt_pk_bf16_f32 v239,v254,v255
v_mfma_f32_16x16x32_bf16 v[152:155],a[164:167],v[160:163],v[152:155]
buffer_load_dwordx4 v5,s[20:23],s47 offen offset:3072 nt lds
v_mfma_f32_16x16x32_bf16 v[152:155],a[168:171],v[192:195],v[152:155]
v_exp_f32 v136,v136
v_mfma_f32_16x16x32_bf16 v[152:155],a[172:175],v[224:227],v[152:155]
v_exp_f32 v137,v137
v_mfma_f32_16x16x32_bf16 v[156:159],a[188:191],v[224:227],v[32:35]
v_exp_f32 v138,v138
v_mfma_f32_16x16x32_bf16 v[156:159],a[184:187],v[192:195],v[156:159]
s_waitcnt lgkmcnt(0)
v_exp_f32 v139,v139
v_mfma_f32_16x16x32_bf16 v[156:159],a[180:183],v[160:163],v[156:159]
v_cvt_pk_bf16_f32 v136,v136,v137
v_cvt_pk_bf16_f32 v137,v138,v139
v_mfma_f32_16x16x32_bf16 v[156:159],a[176:179],v[128:131],v[156:159]
v_exp_f32 v140,v140
v_mfma_f32_16x16x32_bf16 a[64:67],a[192:195],v[200:203],a[64:67]
v_exp_f32 v141,v141
v_mfma_f32_16x16x32_bf16 a[64:67],a[196:199],v[204:207],a[64:67]
v_exp_f32 v142,v142
v_mfma_f32_16x16x32_bf16 a[68:71],a[204:207],v[204:207],a[68:71]
v_exp_f32 v143,v143
v_mfma_f32_16x16x32_bf16 a[68:71],a[200:203],v[200:203],a[68:71]
v_cvt_pk_bf16_f32 v138,v140,v141
v_cvt_pk_bf16_f32 v139,v142,v143
v_mfma_f32_16x16x32_bf16 a[72:75],a[208:211],v[200:203],a[72:75]
s_add_u32 m0,32768,s52
v_exp_f32 v152,v152
v_mfma_f32_16x16x32_bf16 a[72:75],a[212:215],v[204:207],a[72:75]
)A"
R"A(v_exp_f32 v153,v153
v_mfma_f32_16x16x32_bf16 a[76:79],a[220:223],v[204:207],a[76:79]
v_exp_f32 v154,v154
v_mfma_f32_16x16x32_bf16 a[76:79],a[216:219],v[200:203],a[76:79]
buffer_load_dwordx4 v4,s[24:27],s47 offen nt lds
v_mfma_f32_16x16x32_bf16 a[80:83],a[224:227],v[200:203],a[80:83]
v_exp_f32 v155,v155
v_mfma_f32_16x16x32_bf16 a[80:83],a[228:231],v[204:207],a[80:83]
v_exp_f32 v156,v156
v_mfma_f32_16x16x32_bf16 a[84:87],a[236:239],v[204:207],a[84:87]
v_exp_f32 v157,v157
v_mfma_f32_16x16x32_bf16 a[84:87],a[232:235],v[200:203],a[84:87]
v_exp_f32 v158,v158
v_mfma_f32_16x16x32_bf16 a[88:91],a[240:243],v[200:203],a[88:91]
v_exp_f32 v159,v159
v_mfma_f32_16x16x32_bf16 a[88:91],a[244:247],v[204:207],a[88:91]
v_cvt_pk_bf16_f32 v140,v152,v153
v_cvt_pk_bf16_f32 v141,v154,v155
v_mfma_f32_16x16x32_bf16 a[92:95],a[252:255],v[204:207],a[92:95]
v_cvt_pk_bf16_f32 v142,v156,v157
v_cvt_pk_bf16_f32 v143,v158,v159
v_mfma_f32_16x16x32_bf16 a[92:95],a[248:251],v[200:203],a[92:95]
v_rcp_f32 v20,v20
v_smfmac_f32_16x16x64_bf16 v[56:59],v[28:31],v[200:207],v27
v_accvgpr_read_b32 v64,a0
v_mfma_f32_16x16x32_bf16 v[168:171],a[128:131],v[132:135],v[36:39]
v_accvgpr_read_b32 v65,a1
v_accvgpr_read_b32 v66,a2
v_mfma_f32_16x16x32_bf16 v[168:171],a[132:135],v[164:167],v[168:171]
v_accvgpr_read_b32 v67,a3
v_mul_f32 v64,v20,v64
v_mfma_f32_16x16x32_bf16 v[168:171],a[136:139],v[196:199],v[168:171]
v_mul_f32 v65,v20,v65
v_cvt_pk_bf16_f32 v64,v64,v65
v_mfma_f32_16x16x32_bf16 v[168:171],a[140:143],v[228:231],v[168:171]
v_mul_f32 v66,v20,v66
v_mul_f32 v67,v20,v67
v_mfma_f32_16x16x32_bf16 v[172:175],a[156:159],v[228:231],v[36:39]
buffer_load_dwordx4 v5,s[24:27],s47 offen offset:1024 nt lds
v_mfma_f32_16x16x32_bf16 v[172:175],a[152:155],v[196:199],v[172:175]
v_cvt_pk_bf16_f32 v65,v66,v67
v_accvgpr_read_b32 v16,a6
v_mfma_f32_16x16x32_bf16 v[172:175],a[148:151],v[164:167],v[172:175]
v_accvgpr_read_b32 v17,a7
v_accvgpr_read_b32 v66,a4
v_mfma_f32_16x16x32_bf16 v[172:175],a[144:147],v[132:135],v[172:175]
v_accvgpr_read_b32 v67,a5
v_cmp_nlt_f32 s[56:57],v48,s63
v_mfma_f32_16x16x32_bf16 v[184:187],a[160:163],v[132:135],v[36:39]
v_accvgpr_read_b32 v68,a8
v_accvgpr_read_b32 v69,a9
v_accvgpr_read_b32 v70,a10
v_accvgpr_read_b32 v71,a11
v_accvgpr_read_b32 v12,a12
v_accvgpr_read_b32 v13,a13
v_accvgpr_read_b32 v14,a14
v_accvgpr_read_b32 v15,a15
v_pk_mul_f32 v[68:69],v[20:21],v[68:69] op_sel:[0,0] op_sel_hi:[0,1]
v_pk_mul_f32 v[70:71],v[20:21],v[70:71] op_sel:[0,0] op_sel_hi:[0,1]
v_pk_mul_f32 v[12:13],v[20:21],v[12:13] op_sel:[0,0] op_sel_hi:[0,1]
v_pk_mul_f32 v[14:15],v[20:21],v[14:15] op_sel:[0,0] op_sel_hi:[0,1]
v_cvt_pk_bf16_f32 v68,v68,v69
v_cvt_pk_bf16_f32 v69,v70,v71
v_cvt_pk_bf16_f32 v70,v12,v13
v_cvt_pk_bf16_f32 v71,v14,v15
v_mfma_f32_16x16x32_bf16 v[184:187],a[164:167],v[164:167],v[184:187]
v_accvgpr_read_b32 v72,a16
v_accvgpr_read_b32 v73,a17
v_accvgpr_read_b32 v74,a18
v_accvgpr_read_b32 v75,a19
v_accvgpr_read_b32 v12,a20
v_accvgpr_read_b32 v13,a21
v_accvgpr_read_b32 v14,a22
v_accvgpr_read_b32 v15,a23
v_pk_mul_f32 v[72:73],v[20:21],v[72:73] op_sel:[0,0] op_sel_hi:[0,1]
v_pk_mul_f32 v[74:75],v[20:21],v[74:75] op_sel:[0,0] op_sel_hi:[0,1]
v_pk_mul_f32 v[12:13],v[20:21],v[12:13] op_sel:[0,0] op_sel_hi:[0,1]
v_pk_mul_f32 v[14:15],v[20:21],v[14:15] op_sel:[0,0] op_sel_hi:[0,1]
v_cvt_pk_bf16_f32 v72,v72,v73
v_cvt_pk_bf16_f32 v73,v74,v75
v_cvt_pk_bf16_f32 v74,v12,v13
v_cvt_pk_bf16_f32 v75,v14,v15
v_mfma_f32_16x16x32_bf16 v[184:187],a[168:171],v[196:199],v[184:187]
v_accvgpr_read_b32 v76,a24
v_accvgpr_read_b32 v77,a25
v_accvgpr_read_b32 v78,a26
v_accvgpr_read_b32 v79,a27
v_accvgpr_read_b32 v12,a28
v_accvgpr_read_b32 v13,a29
v_accvgpr_read_b32 v14,a30
v_accvgpr_read_b32 v15,a31
v_pk_mul_f32 v[76:77],v[20:21],v[76:77] op_sel:[0,0] op_sel_hi:[0,1]
v_pk_mul_f32 v[78:79],v[20:21],v[78:79] op_sel:[0,0] op_sel_hi:[0,1]
v_pk_mul_f32 v[12:13],v[20:21],v[12:13] op_sel:[0,0] op_sel_hi:[0,1]
v_pk_mul_f32 v[14:15],v[20:21],v[14:15] op_sel:[0,0] op_sel_hi:[0,1]
v_cvt_pk_bf16_f32 v76,v76,v77
v_cvt_pk_bf16_f32 v77,v78,v79
v_cvt_pk_bf16_f32 v78,v12,v13
v_cvt_pk_bf16_f32 v79,v14,v15
v_mfma_f32_16x16x32_bf16 v[184:187],a[172:175],v[228:231],v[184:187]
v_max3_f32 v44,v44,v45,v46
v_max3_f32 v44,v44,v47,s101
v_mfma_f32_16x16x32_bf16 v[188:191],a[188:191],v[228:231],v[36:39]
v_mov_b32 v48,0
v_mov_b32 v49,0
v_mfma_f32_16x16x32_bf16 v[188:191],a[184:187],v[196:199],v[188:191]
v_mov_b32 v50,0
v_mov_b32 v51,0
v_mfma_f32_16x16x32_bf16 v[188:191],a[180:183],v[164:167],v[188:191]
v_sub_f32 v47,0,v44
v_sub_f32 v46,0,v44
v_mfma_f32_16x16x32_bf16 v[188:191],a[176:179],v[132:135],v[188:191]
v_sub_f32 v45,0,v44
v_sub_f32 v44,0,v44
v_mfma_f32_16x16x32_bf16 a[96:99],a[192:195],v[232:235],a[96:99]
ds_read_b64_tr_b16 a[192:193],v2
ds_read_b64_tr_b16 a[194:195],v2 offset:4096
v_mfma_f32_16x16x32_bf16 a[96:99],a[196:199],v[236:239],a[96:99]
ds_read_b64_tr_b16 a[196:197],v2 offset:8192
v_add_f32 v52,v53,v52
v_mfma_f32_16x16x32_bf16 a[100:103],a[204:207],v[236:239],a[100:103]
buffer_load_dwordx4 v4,s[24:27],s47 offen offset:2048 nt lds
v_mfma_f32_16x16x32_bf16 a[100:103],a[200:203],v[232:235],a[100:103]
ds_read_b64_tr_b16 a[198:199],v2 offset:12288
ds_read_b64_tr_b16 a[204:205],v3 offset:8192
v_mfma_f32_16x16x32_bf16 a[104:107],a[208:211],v[232:235],a[104:107]
ds_read_b64_tr_b16 a[206:207],v3 offset:12288
v_max_f32 v21,0x00800000,v52
v_mfma_f32_16x16x32_bf16 a[104:107],a[212:215],v[236:239],a[104:107]
ds_read_b64_tr_b16 a[200:201],v3
v_accvgpr_read_b32 v80,a32
v_mfma_f32_16x16x32_bf16 a[108:111],a[220:223],v[236:239],a[108:111]
ds_read_b64_tr_b16 a[202:203],v3 offset:4096
v_accvgpr_read_b32 v81,a33
v_mfma_f32_16x16x32_bf16 a[108:111],a[216:219],v[232:235],a[108:111]
ds_read_b64_tr_b16 a[208:209],v2 offset:256
ds_read_b64_tr_b16 a[210:211],v2 offset:4352
v_mfma_f32_16x16x32_bf16 a[112:115],a[224:227],v[232:235],a[112:115]
ds_read_b64_tr_b16 a[212:213],v2 offset:8448
v_accvgpr_read_b32 v82,a34
v_mfma_f32_16x16x32_bf16 a[112:115],a[228:231],v[236:239],a[112:115]
ds_read_b64_tr_b16 a[214:215],v2 offset:12544
v_accvgpr_read_b32 v83,a35
v_mfma_f32_16x16x32_bf16 a[116:119],a[236:239],v[236:239],a[116:119]
ds_read_b64_tr_b16 a[220:221],v3 offset:8448
v_accvgpr_read_b32 v12,a38
v_mfma_f32_16x16x32_bf16 a[116:119],a[232:235],v[232:235],a[116:119]
ds_read_b64_tr_b16 a[222:223],v3 offset:12544
ds_read_b64_tr_b16 a[216:217],v3 offset:256
v_mfma_f32_16x16x32_bf16 a[120:123],a[240:243],v[232:235],a[120:123]
ds_read_b64_tr_b16 a[218:219],v3 offset:4352
v_accvgpr_read_b32 v13,a39
v_mfma_f32_16x16x32_bf16 a[120:123],a[244:247],v[236:239],a[120:123]
ds_read_b64_tr_b16 a[224:225],v2 offset:512
v_add_u32 v1,s48,v1
v_mfma_f32_16x16x32_bf16 a[124:127],a[252:255],v[236:239],a[124:127]
ds_read_b64_tr_b16 a[226:227],v2 offset:4608
v_mov_b32 v53,0
v_mfma_f32_16x16x32_bf16 a[124:127],a[248:251],v[232:235],a[124:127]
ds_read_b64_tr_b16 a[228:229],v2 offset:8704
ds_read_b64_tr_b16 a[230:231],v2 offset:12800
v_smfmac_f32_16x16x64_bf16 v[60:63],v[28:31],v[232:239],v27
ds_read_b64_tr_b16 a[236:237],v3 offset:8704
v_mfma_f32_16x16x32_bf16 v[200:203],a[128:131],v[144:147],v[40:43]
buffer_load_dwordx4 v5,s[24:27],s47 offen offset:3072 nt lds
v_mfma_f32_16x16x32_bf16 v[200:203],a[132:135],v[176:179],v[200:203]
s_or_b32 s47,s47,0x18000
ds_read_b64_tr_b16 a[238:239],v3 offset:12800
ds_read_b64_tr_b16 a[232:233],v3 offset:512
v_mfma_f32_16x16x32_bf16 v[200:203],a[136:139],v[208:211],v[200:203]
s_add_u32 s47,s47,4096
s_and_b32 s47,s47,0x27000
ds_read_b64_tr_b16 a[234:235],v3 offset:4608
v_mfma_f32_16x16x32_bf16 v[200:203],a[140:143],v[240:243],v[200:203]
s_or_b32 s47,s47,s46
ds_read_b64_tr_b16 a[240:241],v2 offset:768
v_mov_b32 v54,0
v_mfma_f32_16x16x32_bf16 v[204:207],a[156:159],v[240:243],v[40:43]
s_and_b32 s46,s54,0xfffc0000
ds_read_b64_tr_b16 a[242:243],v2 offset:4864
v_mov_b32 v55,0
v_mfma_f32_16x16x32_bf16 v[204:207],a[152:155],v[208:211],v[204:207]
ds_read_b64_tr_b16 a[244:245],v2 offset:8960
ds_read_b64_tr_b16 a[246:247],v2 offset:13056
v_mfma_f32_16x16x32_bf16 v[204:207],a[148:151],v[176:179],v[204:207]
ds_read_b64_tr_b16 a[252:253],v3 offset:8960
v_add_f32 v56,v57,v56
v_mfma_f32_16x16x32_bf16 v[204:207],a[144:147],v[144:147],v[204:207]
ds_read_b64_tr_b16 a[254:255],v3 offset:13056
v_max_f32 v22,0x00800000,v56
v_mfma_f32_16x16x32_bf16 v[216:219],a[160:163],v[144:147],v[40:43]
ds_read_b64_tr_b16 a[248:249],v3 offset:768
v_accvgpr_read_b32 v96,a64
v_mfma_f32_16x16x32_bf16 v[216:219],a[164:167],v[176:179],v[216:219]
ds_read_b64_tr_b16 a[250:251],v3 offset:4864
v_accvgpr_read_b32 v97,a65
v_mfma_f32_16x16x32_bf16 v[216:219],a[168:171],v[208:211],v[216:219]
v_exp_f32 v168,v168
v_mfma_f32_16x16x32_bf16 v[216:219],a[172:175],v[240:243],v[216:219]
v_exp_f32 v169,v169
v_mfma_f32_16x16x32_bf16 v[220:223],a[188:191],v[240:243],v[40:43]
v_exp_f32 v170,v170
v_mfma_f32_16x16x32_bf16 v[220:223],a[184:187],v[208:211],v[220:223]
v_exp_f32 v171,v171
v_mfma_f32_16x16x32_bf16 v[220:223],a[180:183],v[176:179],v[220:223]
v_cvt_pk_bf16_f32 v168,v168,v169
v_cvt_pk_bf16_f32 v169,v170,v171
v_mfma_f32_16x16x32_bf16 v[220:223],a[176:179],v[144:147],v[220:223]
s_waitcnt lgkmcnt(2)
v_exp_f32 v172,v172
v_mfma_f32_16x16x32_bf16 a[0:3],a[192:195],v[136:139],0
v_exp_f32 v173,v173
v_mfma_f32_16x16x32_bf16 a[0:3],a[196:199],v[140:143],a[0:3]
v_exp_f32 v174,v174
v_mfma_f32_16x16x32_bf16 a[4:7],a[204:207],v[140:143],0
v_exp_f32 v175,v175
v_mfma_f32_16x16x32_bf16 a[4:7],a[200:203],v[136:139],a[4:7]
s_memrealtime s[76:77]
v_cvt_pk_bf16_f32 v170,v172,v173
v_cvt_pk_bf16_f32 v171,v174,v175
v_mfma_f32_16x16x32_bf16 a[8:11],a[208:211],v[136:139],0
v_exp_f32 v184,v184
v_mfma_f32_16x16x32_bf16 a[8:11],a[212:215],v[140:143],a[8:11]
v_exp_f32 v185,v185
v_mfma_f32_16x16x32_bf16 a[12:15],a[220:223],v[140:143],0
v_exp_f32 v186,v186
v_mfma_f32_16x16x32_bf16 a[12:15],a[216:219],v[136:139],a[12:15]
v_exp_f32 v187,v187
v_mfma_f32_16x16x32_bf16 a[16:19],a[224:227],v[136:139],0
v_exp_f32 v188,v188
v_mfma_f32_16x16x32_bf16 a[16:19],a[228:231],v[140:143],a[16:19]
v_exp_f32 v189,v189
v_mfma_f32_16x16x32_bf16 a[20:23],a[236:239],v[140:143],0
s_waitcnt lgkmcnt(0)
v_exp_f32 v190,v190
v_mfma_f32_16x16x32_bf16 a[20:23],a[232:235],v[136:139],a[20:23]
s_sub_u32 s79,s78,s76
v_exp_f32 v191,v191
v_mfma_f32_16x16x32_bf16 a[24:27],a[240:243],v[136:139],0
s_add_u32 s78,s76,s60
v_rcp_f32 v21,v21
v_mfma_f32_16x16x32_bf16 a[24:27],a[244:247],v[140:143],a[24:27]
s_cmp_gt_i32 s79,s95
s_cbranch_scc1 L_ps5_%=
L_pr5_%=:
v_cvt_pk_bf16_f32 v172,v184,v185
v_mfma_f32_16x16x32_bf16 a[28:31],a[252:255],v[140:143],0
s_lshr_b32 s60,s94,2
v_cvt_pk_bf16_f32 v173,v186,v187
v_cvt_pk_bf16_f32 v174,v188,v189
v_mfma_f32_16x16x32_bf16 a[28:31],a[248:251],v[136:139],a[28:31]
s_max_i32 s60,s60,128
v_cvt_pk_bf16_f32 v175,v190,v191
v_mul_f32 v80,v21,v80
v_smfmac_f32_16x16x64_bf16 v[48:51],v[28:31],v[136:143],v27
v_mul_f32 v81,v21,v81
v_mfma_f32_16x16x32_bf16 v[232:235],a[128:131],v[148:151],v[44:47]
s_waitcnt vmcnt(8)
v_cvt_pk_bf16_f32 v80,v80,v81
v_mul_f32 v82,v21,v82
v_mfma_f32_16x16x32_bf16 v[232:235],a[132:135],v[180:183],v[232:235]
s_barrier
s_add_u32 m0,65536,s51
v_mul_f32 v83,v21,v83
v_mfma_f32_16x16x32_bf16 v[232:235],a[136:139],v[212:215],v[232:235]
v_cvt_pk_bf16_f32 v81,v82,v83
v_accvgpr_read_b32 v82,a36
v_mfma_f32_16x16x32_bf16 v[232:235],a[140:143],v[244:247],v[232:235]
v_accvgpr_read_b32 v83,a37
v_mul_f32 v82,v21,v82
v_mfma_f32_16x16x32_bf16 v[236:239],a[156:159],v[244:247],v[44:47]
buffer_load_dwordx4 v4,s[20:23],s47 offen nt lds
v_mfma_f32_16x16x32_bf16 v[236:239],a[152:155],v[212:215],v[236:239]
v_mul_f32 v83,v21,v83
v_mul_f32 v12,v21,v12
v_mfma_f32_16x16x32_bf16 v[236:239],a[148:151],v[180:183],v[236:239]
v_mul_f32 v13,v21,v13
v_cvt_pk_bf16_f32 v82,v82,v83
v_mfma_f32_16x16x32_bf16 v[236:239],a[144:147],v[148:151],v[236:239]
ds_read_b128 a[128:131],v1
v_cvt_pk_bf16_f32 v83,v12,v13
v_mfma_f32_16x16x32_bf16 v[248:251],a[160:163],v[148:151],v[44:47]
v_accvgpr_read_b32 v84,a40
v_accvgpr_read_b32 v85,a41
v_accvgpr_read_b32 v86,a42
v_accvgpr_read_b32 v87,a43
v_accvgpr_read_b32 v12,a44
v_accvgpr_read_b32 v13,a45
)A"
R"A(v_accvgpr_read_b32 v14,a46
v_accvgpr_read_b32 v15,a47
v_pk_mul_f32 v[84:85],v[20:21],v[84:85] op_sel:[1,0] op_sel_hi:[1,1]
v_pk_mul_f32 v[86:87],v[20:21],v[86:87] op_sel:[1,0] op_sel_hi:[1,1]
v_pk_mul_f32 v[12:13],v[20:21],v[12:13] op_sel:[1,0] op_sel_hi:[1,1]
v_pk_mul_f32 v[14:15],v[20:21],v[14:15] op_sel:[1,0] op_sel_hi:[1,1]
v_cvt_pk_bf16_f32 v84,v84,v85
v_cvt_pk_bf16_f32 v85,v86,v87
v_cvt_pk_bf16_f32 v86,v12,v13
v_cvt_pk_bf16_f32 v87,v14,v15
v_mfma_f32_16x16x32_bf16 v[248:251],a[164:167],v[180:183],v[248:251]
v_accvgpr_read_b32 v88,a48
v_accvgpr_read_b32 v89,a49
v_accvgpr_read_b32 v90,a50
v_accvgpr_read_b32 v91,a51
v_accvgpr_read_b32 v12,a52
v_accvgpr_read_b32 v13,a53
v_accvgpr_read_b32 v14,a54
v_accvgpr_read_b32 v15,a55
v_pk_mul_f32 v[88:89],v[20:21],v[88:89] op_sel:[1,0] op_sel_hi:[1,1]
v_pk_mul_f32 v[90:91],v[20:21],v[90:91] op_sel:[1,0] op_sel_hi:[1,1]
v_pk_mul_f32 v[12:13],v[20:21],v[12:13] op_sel:[1,0] op_sel_hi:[1,1]
v_pk_mul_f32 v[14:15],v[20:21],v[14:15] op_sel:[1,0] op_sel_hi:[1,1]
v_cvt_pk_bf16_f32 v88,v88,v89
v_cvt_pk_bf16_f32 v89,v90,v91
v_cvt_pk_bf16_f32 v90,v12,v13
v_cvt_pk_bf16_f32 v91,v14,v15
v_mfma_f32_16x16x32_bf16 v[248:251],a[168:171],v[212:215],v[248:251]
ds_read_b128 a[132:135],v1 offset:256
v_accvgpr_read_b32 v98,a66
v_mfma_f32_16x16x32_bf16 v[248:251],a[172:175],v[244:247],v[248:251]
v_accvgpr_read_b32 v92,a56
v_accvgpr_read_b32 v93,a57
v_accvgpr_read_b32 v94,a58
v_accvgpr_read_b32 v95,a59
v_accvgpr_read_b32 v12,a60
v_accvgpr_read_b32 v13,a61
v_accvgpr_read_b32 v14,a62
v_accvgpr_read_b32 v15,a63
v_pk_mul_f32 v[92:93],v[20:21],v[92:93] op_sel:[1,0] op_sel_hi:[1,1]
v_pk_mul_f32 v[94:95],v[20:21],v[94:95] op_sel:[1,0] op_sel_hi:[1,1]
v_pk_mul_f32 v[12:13],v[20:21],v[12:13] op_sel:[1,0] op_sel_hi:[1,1]
v_pk_mul_f32 v[14:15],v[20:21],v[14:15] op_sel:[1,0] op_sel_hi:[1,1]
v_cvt_pk_bf16_f32 v92,v92,v93
v_cvt_pk_bf16_f32 v93,v94,v95
v_cvt_pk_bf16_f32 v94,v12,v13
v_cvt_pk_bf16_f32 v95,v14,v15
v_mfma_f32_16x16x32_bf16 v[252:255],a[188:191],v[244:247],v[44:47]
ds_read_b128 a[136:139],v1 offset:512
v_accvgpr_read_b32 v99,a67
v_mfma_f32_16x16x32_bf16 v[252:255],a[184:187],v[212:215],v[252:255]
v_cmp_nlt_f32 s[58:59],v52,s63
s_nop 1
s_or_b64 s[56:57],s[56:57],s[58:59]
v_mfma_f32_16x16x32_bf16 v[252:255],a[180:183],v[180:183],v[252:255]
ds_read_b128 a[140:143],v1 offset:768
v_mov_b32 v52,0
v_mfma_f32_16x16x32_bf16 v[252:255],a[176:179],v[148:151],v[252:255]
v_exp_f32 v200,v200
v_mfma_f32_16x16x32_bf16 a[32:35],a[192:195],v[168:171],0
ds_read_b128 a[156:159],v1 offset:4864
v_accvgpr_read_b32 v14,a70
v_mfma_f32_16x16x32_bf16 a[32:35],a[196:199],v[172:175],a[32:35]
v_exp_f32 v201,v201
v_mfma_f32_16x16x32_bf16 a[36:39],a[204:207],v[172:175],0
buffer_load_dwordx4 v4,s[20:23],s47 offen offset:1024 nt lds
v_mfma_f32_16x16x32_bf16 a[36:39],a[200:203],v[168:171],a[36:39]
ds_read_b128 a[152:155],v1 offset:4608
v_cvt_pk_bf16_f32 v200,v200,v201
v_mfma_f32_16x16x32_bf16 a[40:43],a[208:211],v[168:171],0
v_exp_f32 v202,v202
v_mfma_f32_16x16x32_bf16 a[40:43],a[212:215],v[172:175],a[40:43]
ds_read_b128 a[148:151],v1 offset:4352
v_accvgpr_read_b32 v15,a71
v_mfma_f32_16x16x32_bf16 a[44:47],a[220:223],v[172:175],0
v_exp_f32 v203,v203
v_mfma_f32_16x16x32_bf16 a[44:47],a[216:219],v[168:171],a[44:47]
ds_read_b128 a[144:147],v1 offset:4096
v_cvt_pk_bf16_f32 v201,v202,v203
v_mfma_f32_16x16x32_bf16 a[48:51],a[224:227],v[168:171],0
v_exp_f32 v204,v204
v_mfma_f32_16x16x32_bf16 a[48:51],a[228:231],v[172:175],a[48:51]
ds_read_b128 a[160:163],v1 offset:8192
v_mov_b32 v57,0
v_mfma_f32_16x16x32_bf16 a[52:55],a[236:239],v[172:175],0
v_exp_f32 v205,v205
v_mfma_f32_16x16x32_bf16 a[52:55],a[232:235],v[168:171],a[52:55]
ds_read_b128 a[164:167],v1 offset:8448
v_cvt_pk_bf16_f32 v202,v204,v205
v_mfma_f32_16x16x32_bf16 a[56:59],a[240:243],v[168:171],0
v_exp_f32 v206,v206
v_mfma_f32_16x16x32_bf16 a[56:59],a[244:247],v[172:175],a[56:59]
ds_read_b128 a[168:171],v1 offset:8704
v_mov_b32 v58,0
v_mfma_f32_16x16x32_bf16 a[60:63],a[252:255],v[172:175],0
v_exp_f32 v207,v207
v_mfma_f32_16x16x32_bf16 a[60:63],a[248:251],v[168:171],a[60:63]
ds_read_b128 a[172:175],v1 offset:8960
v_cvt_pk_bf16_f32 v203,v206,v207
v_smfmac_f32_16x16x64_bf16 v[52:55],v[28:31],v[168:175],v27
s_waitcnt lgkmcnt(4)
v_mov_b32 v59,0
v_mfma_f32_16x16x32_bf16 v[136:139],a[128:131],v[128:131],v[32:35]
buffer_load_dwordx4 v5,s[20:23],s47 offen offset:2048 nt lds
v_mfma_f32_16x16x32_bf16 v[136:139],a[132:135],v[160:163],v[136:139]
ds_read_b128 a[188:191],v1 offset:13056
v_add_f32 v60,v61,v60
v_mfma_f32_16x16x32_bf16 v[136:139],a[136:139],v[192:195],v[136:139]
v_exp_f32 v216,v216
v_mfma_f32_16x16x32_bf16 v[136:139],a[140:143],v[224:227],v[136:139]
ds_read_b128 a[184:187],v1 offset:12800
v_max_f32 v23,0x00800000,v60
v_mfma_f32_16x16x32_bf16 v[140:143],a[156:159],v[224:227],v[32:35]
v_exp_f32 v217,v217
v_mfma_f32_16x16x32_bf16 v[140:143],a[152:155],v[192:195],v[140:143]
ds_read_b128 a[180:183],v1 offset:12544
v_cvt_pk_bf16_f32 v204,v216,v217
v_mfma_f32_16x16x32_bf16 v[140:143],a[148:151],v[160:163],v[140:143]
ds_read_b128 a[176:179],v1 offset:12288
v_mfma_f32_16x16x32_bf16 v[140:143],a[144:147],v[128:131],v[140:143]
s_waitcnt lgkmcnt(4)
v_exp_f32 v218,v218
v_mfma_f32_16x16x32_bf16 v[152:155],a[160:163],v[128:131],v[32:35]
v_exp_f32 v219,v219
v_mfma_f32_16x16x32_bf16 v[152:155],a[164:167],v[160:163],v[152:155]
v_exp_f32 v220,v220
v_mfma_f32_16x16x32_bf16 v[152:155],a[168:171],v[192:195],v[152:155]
v_exp_f32 v221,v221
v_mfma_f32_16x16x32_bf16 v[152:155],a[172:175],v[224:227],v[152:155]
s_waitcnt lgkmcnt(2)
v_exp_f32 v222,v222
v_mfma_f32_16x16x32_bf16 v[156:159],a[188:191],v[224:227],v[32:35]
v_exp_f32 v223,v223
v_mfma_f32_16x16x32_bf16 v[156:159],a[184:187],v[192:195],v[156:159]
s_waitcnt lgkmcnt(1)
v_rcp_f32 v22,v22
v_mfma_f32_16x16x32_bf16 v[156:159],a[180:183],v[160:163],v[156:159]
s_waitcnt lgkmcnt(0)
v_cvt_pk_bf16_f32 v205,v218,v219
v_cvt_pk_bf16_f32 v206,v220,v221
v_cvt_pk_bf16_f32 v207,v222,v223
v_mfma_f32_16x16x32_bf16 v[156:159],a[176:179],v[128:131],v[156:159]
v_mul_f32 v96,v22,v96
v_mul_f32 v97,v22,v97
v_mfma_f32_16x16x32_bf16 a[64:67],a[192:195],v[200:203],0
buffer_load_dwordx4 v5,s[20:23],s47 offen offset:3072 nt lds
v_cvt_pk_bf16_f32 v96,v96,v97
v_mul_f32 v98,v22,v98
v_mul_f32 v99,v22,v99
v_cvt_pk_bf16_f32 v97,v98,v99
v_mfma_f32_16x16x32_bf16 a[64:67],a[196:199],v[204:207],a[64:67]
v_accvgpr_read_b32 v98,a68
v_accvgpr_read_b32 v99,a69
v_mul_f32 v98,v22,v98
v_mul_f32 v99,v22,v99
v_mfma_f32_16x16x32_bf16 a[68:71],a[204:207],v[204:207],0
v_mul_f32 v14,v22,v14
v_mul_f32 v15,v22,v15
v_cvt_pk_bf16_f32 v98,v98,v99
v_cvt_pk_bf16_f32 v99,v14,v15
v_mfma_f32_16x16x32_bf16 a[68:71],a[200:203],v[200:203],a[68:71]
v_accvgpr_read_b32 v100,a72
v_accvgpr_read_b32 v101,a73
v_accvgpr_read_b32 v102,a74
v_accvgpr_read_b32 v103,a75
v_accvgpr_read_b32 v12,a76
v_accvgpr_read_b32 v13,a77
v_accvgpr_read_b32 v14,a78
v_accvgpr_read_b32 v15,a79
v_pk_mul_f32 v[100:101],v[22:23],v[100:101] op_sel:[0,0] op_sel_hi:[0,1]
v_pk_mul_f32 v[102:103],v[22:23],v[102:103] op_sel:[0,0] op_sel_hi:[0,1]
v_pk_mul_f32 v[12:13],v[22:23],v[12:13] op_sel:[0,0] op_sel_hi:[0,1]
v_pk_mul_f32 v[14:15],v[22:23],v[14:15] op_sel:[0,0] op_sel_hi:[0,1]
v_cvt_pk_bf16_f32 v100,v100,v101
v_cvt_pk_bf16_f32 v101,v102,v103
v_cvt_pk_bf16_f32 v102,v12,v13
v_cvt_pk_bf16_f32 v103,v14,v15
v_mfma_f32_16x16x32_bf16 a[72:75],a[208:211],v[200:203],0
v_accvgpr_read_b32 v104,a80
v_accvgpr_read_b32 v105,a81
v_accvgpr_read_b32 v106,a82
v_accvgpr_read_b32 v107,a83
v_accvgpr_read_b32 v12,a84
v_accvgpr_read_b32 v13,a85
v_accvgpr_read_b32 v14,a86
v_accvgpr_read_b32 v15,a87
v_pk_mul_f32 v[104:105],v[22:23],v[104:105] op_sel:[0,0] op_sel_hi:[0,1]
v_pk_mul_f32 v[106:107],v[22:23],v[106:107] op_sel:[0,0] op_sel_hi:[0,1]
v_pk_mul_f32 v[12:13],v[22:23],v[12:13] op_sel:[0,0] op_sel_hi:[0,1]
v_pk_mul_f32 v[14:15],v[22:23],v[14:15] op_sel:[0,0] op_sel_hi:[0,1]
v_cvt_pk_bf16_f32 v104,v104,v105
v_cvt_pk_bf16_f32 v105,v106,v107
v_cvt_pk_bf16_f32 v106,v12,v13
v_cvt_pk_bf16_f32 v107,v14,v15
v_mfma_f32_16x16x32_bf16 a[72:75],a[212:215],v[204:207],a[72:75]
v_accvgpr_read_b32 v108,a88
v_accvgpr_read_b32 v109,a89
v_accvgpr_read_b32 v110,a90
v_accvgpr_read_b32 v111,a91
v_accvgpr_read_b32 v12,a92
v_accvgpr_read_b32 v13,a93
v_accvgpr_read_b32 v14,a94
v_accvgpr_read_b32 v15,a95
v_pk_mul_f32 v[108:109],v[22:23],v[108:109] op_sel:[0,0] op_sel_hi:[0,1]
v_pk_mul_f32 v[110:111],v[22:23],v[110:111] op_sel:[0,0] op_sel_hi:[0,1]
v_pk_mul_f32 v[12:13],v[22:23],v[12:13] op_sel:[0,0] op_sel_hi:[0,1]
v_pk_mul_f32 v[14:15],v[22:23],v[14:15] op_sel:[0,0] op_sel_hi:[0,1]
v_cvt_pk_bf16_f32 v108,v108,v109
v_cvt_pk_bf16_f32 v109,v110,v111
v_cvt_pk_bf16_f32 v110,v12,v13
v_cvt_pk_bf16_f32 v111,v14,v15
v_mfma_f32_16x16x32_bf16 a[76:79],a[220:223],v[204:207],0
v_cmp_nlt_f32 s[58:59],v56,s63
s_nop 1
s_or_b64 s[56:57],s[56:57],s[58:59]
v_mfma_f32_16x16x32_bf16 a[76:79],a[216:219],v[200:203],a[76:79]
v_mov_b32 v56,0
v_accvgpr_read_b32 v112,a96
v_mfma_f32_16x16x32_bf16 a[80:83],a[224:227],v[200:203],0
s_add_u32 m0,65536,s52
v_exp_f32 v232,v232
v_mfma_f32_16x16x32_bf16 a[80:83],a[228:231],v[204:207],a[80:83]
v_exp_f32 v233,v233
v_mfma_f32_16x16x32_bf16 a[84:87],a[236:239],v[204:207],0
v_exp_f32 v234,v234
v_mfma_f32_16x16x32_bf16 a[84:87],a[232:235],v[200:203],a[84:87]
buffer_load_dwordx4 v4,s[24:27],s47 offen nt lds
v_mfma_f32_16x16x32_bf16 a[88:91],a[240:243],v[200:203],0
v_exp_f32 v235,v235
v_mfma_f32_16x16x32_bf16 a[88:91],a[244:247],v[204:207],a[88:91]
v_cvt_pk_bf16_f32 v232,v232,v233
v_cvt_pk_bf16_f32 v233,v234,v235
v_mfma_f32_16x16x32_bf16 a[92:95],a[252:255],v[204:207],0
v_exp_f32 v236,v236
v_mfma_f32_16x16x32_bf16 a[92:95],a[248:251],v[200:203],a[92:95]
v_exp_f32 v237,v237
v_smfmac_f32_16x16x64_bf16 v[56:59],v[28:31],v[200:207],v27
v_cvt_pk_bf16_f32 v234,v236,v237
v_mfma_f32_16x16x32_bf16 v[168:171],a[128:131],v[132:135],v[36:39]
v_exp_f32 v238,v238
v_mfma_f32_16x16x32_bf16 v[168:171],a[132:135],v[164:167],v[168:171]
v_exp_f32 v239,v239
v_mfma_f32_16x16x32_bf16 v[168:171],a[136:139],v[196:199],v[168:171]
v_cvt_pk_bf16_f32 v235,v238,v239
v_accvgpr_read_b32 v113,a97
v_mfma_f32_16x16x32_bf16 v[168:171],a[140:143],v[228:231],v[168:171]
v_exp_f32 v248,v248
v_mfma_f32_16x16x32_bf16 v[172:175],a[156:159],v[228:231],v[36:39]
v_exp_f32 v249,v249
v_mfma_f32_16x16x32_bf16 v[172:175],a[152:155],v[196:199],v[172:175]
v_exp_f32 v250,v250
v_mfma_f32_16x16x32_bf16 v[172:175],a[148:151],v[164:167],v[172:175]
v_exp_f32 v251,v251
v_mfma_f32_16x16x32_bf16 v[172:175],a[144:147],v[132:135],v[172:175]
v_exp_f32 v252,v252
v_mfma_f32_16x16x32_bf16 v[184:187],a[160:163],v[132:135],v[36:39]
buffer_load_dwordx4 v5,s[24:27],s47 offen offset:1024 nt lds
v_mfma_f32_16x16x32_bf16 v[184:187],a[164:167],v[164:167],v[184:187]
v_exp_f32 v253,v253
v_mfma_f32_16x16x32_bf16 v[184:187],a[168:171],v[196:199],v[184:187]
v_exp_f32 v254,v254
v_mfma_f32_16x16x32_bf16 v[184:187],a[172:175],v[228:231],v[184:187]
v_exp_f32 v255,v255
v_mfma_f32_16x16x32_bf16 v[188:191],a[188:191],v[228:231],v[36:39]
v_cvt_pk_bf16_f32 v236,v248,v249
v_cvt_pk_bf16_f32 v237,v250,v251
v_mfma_f32_16x16x32_bf16 v[188:191],a[184:187],v[196:199],v[188:191]
v_cvt_pk_bf16_f32 v238,v252,v253
v_cvt_pk_bf16_f32 v239,v254,v255
v_mfma_f32_16x16x32_bf16 v[188:191],a[180:183],v[164:167],v[188:191]
v_rcp_f32 v23,v23
v_mfma_f32_16x16x32_bf16 v[188:191],a[176:179],v[132:135],v[188:191]
v_accvgpr_read_b32 v114,a98
v_accvgpr_read_b32 v115,a99
v_mfma_f32_16x16x32_bf16 a[96:99],a[192:195],v[232:235],0
v_mul_f32 v66,v20,v66
v_mul_f32 v67,v20,v67
v_mul_f32 v112,v23,v112
v_mul_f32 v113,v23,v113
v_mul_f32 v16,v20,v16
v_mul_f32 v17,v20,v17
v_cvt_pk_bf16_f32 v66,v66,v67
v_cvt_pk_bf16_f32 v112,v112,v113
v_mul_f32 v114,v23,v114
v_mul_f32 v115,v23,v115
v_mfma_f32_16x16x32_bf16 a[96:99],a[196:199],v[236:239],a[96:99]
v_cvt_pk_bf16_f32 v67,v16,v17
v_cvt_pk_bf16_f32 v113,v114,v115
v_accvgpr_read_b32 v16,a102
v_accvgpr_read_b32 v17,a103
v_accvgpr_read_b32 v114,a100
v_accvgpr_read_b32 v115,a101
v_mfma_f32_16x16x32_bf16 a[100:103],a[204:207],v[236:239],0
v_accvgpr_read_b32 v116,a104
)A"
R"A(v_accvgpr_read_b32 v117,a105
v_accvgpr_read_b32 v118,a106
v_accvgpr_read_b32 v119,a107
v_accvgpr_read_b32 v12,a108
v_accvgpr_read_b32 v13,a109
v_accvgpr_read_b32 v14,a110
v_accvgpr_read_b32 v15,a111
v_pk_mul_f32 v[116:117],v[22:23],v[116:117] op_sel:[1,0] op_sel_hi:[1,1]
v_pk_mul_f32 v[118:119],v[22:23],v[118:119] op_sel:[1,0] op_sel_hi:[1,1]
v_pk_mul_f32 v[12:13],v[22:23],v[12:13] op_sel:[1,0] op_sel_hi:[1,1]
v_pk_mul_f32 v[14:15],v[22:23],v[14:15] op_sel:[1,0] op_sel_hi:[1,1]
v_cvt_pk_bf16_f32 v116,v116,v117
v_cvt_pk_bf16_f32 v117,v118,v119
v_cvt_pk_bf16_f32 v118,v12,v13
v_cvt_pk_bf16_f32 v119,v14,v15
v_mfma_f32_16x16x32_bf16 a[100:103],a[200:203],v[232:235],a[100:103]
v_accvgpr_read_b32 v120,a112
v_accvgpr_read_b32 v121,a113
v_accvgpr_read_b32 v122,a114
v_accvgpr_read_b32 v123,a115
v_accvgpr_read_b32 v12,a116
v_accvgpr_read_b32 v13,a117
v_accvgpr_read_b32 v14,a118
v_accvgpr_read_b32 v15,a119
v_pk_mul_f32 v[120:121],v[22:23],v[120:121] op_sel:[1,0] op_sel_hi:[1,1]
v_pk_mul_f32 v[122:123],v[22:23],v[122:123] op_sel:[1,0] op_sel_hi:[1,1]
v_pk_mul_f32 v[12:13],v[22:23],v[12:13] op_sel:[1,0] op_sel_hi:[1,1]
v_pk_mul_f32 v[14:15],v[22:23],v[14:15] op_sel:[1,0] op_sel_hi:[1,1]
v_cvt_pk_bf16_f32 v120,v120,v121
v_cvt_pk_bf16_f32 v121,v122,v123
v_cvt_pk_bf16_f32 v122,v12,v13
v_cvt_pk_bf16_f32 v123,v14,v15
v_mfma_f32_16x16x32_bf16 a[104:107],a[208:211],v[232:235],0
v_accvgpr_read_b32 v124,a120
v_accvgpr_read_b32 v125,a121
v_accvgpr_read_b32 v126,a122
v_accvgpr_read_b32 v127,a123
v_accvgpr_read_b32 v12,a124
v_accvgpr_read_b32 v13,a125
v_accvgpr_read_b32 v14,a126
v_accvgpr_read_b32 v15,a127
v_pk_mul_f32 v[124:125],v[22:23],v[124:125] op_sel:[1,0] op_sel_hi:[1,1]
v_pk_mul_f32 v[126:127],v[22:23],v[126:127] op_sel:[1,0] op_sel_hi:[1,1]
v_pk_mul_f32 v[12:13],v[22:23],v[12:13] op_sel:[1,0] op_sel_hi:[1,1]
v_pk_mul_f32 v[14:15],v[22:23],v[14:15] op_sel:[1,0] op_sel_hi:[1,1]
v_cvt_pk_bf16_f32 v124,v124,v125
v_cvt_pk_bf16_f32 v125,v126,v127
v_cvt_pk_bf16_f32 v126,v12,v13
v_cvt_pk_bf16_f32 v127,v14,v15
v_mfma_f32_16x16x32_bf16 a[104:107],a[212:215],v[236:239],a[104:107]
v_cmp_nlt_f32 s[58:59],v60,s63
s_nop 1
s_or_b64 s[56:57],s[56:57],s[58:59]
v_mfma_f32_16x16x32_bf16 a[108:111],a[220:223],v[236:239],0
buffer_load_dwordx4 v4,s[24:27],s47 offen offset:2048 nt lds
v_mfma_f32_16x16x32_bf16 a[108:111],a[216:219],v[232:235],a[108:111]
v_mov_b32 v60,0
v_mov_b32 v61,0
v_mfma_f32_16x16x32_bf16 a[112:115],a[224:227],v[232:235],0
v_mov_b32 v62,0
v_mov_b32 v63,0
v_mfma_f32_16x16x32_bf16 a[112:115],a[228:231],v[236:239],a[112:115]
v_add_u32 v2,s48,v2
v_add_u32 v3,s48,v3
v_mfma_f32_16x16x32_bf16 a[116:119],a[236:239],v[236:239],0
s_mov_b32 s48,32768
ds_read_b64_tr_b16 a[192:193],v2
ds_read_b64_tr_b16 a[194:195],v2 offset:4096
v_mfma_f32_16x16x32_bf16 a[116:119],a[232:235],v[232:235],a[116:119]
ds_read_b64_tr_b16 a[196:197],v2 offset:8192
v_mul_f32 v114,v23,v114
v_mfma_f32_16x16x32_bf16 a[120:123],a[240:243],v[232:235],0
ds_read_b64_tr_b16 a[198:199],v2 offset:12288
v_mul_f32 v115,v23,v115
v_mfma_f32_16x16x32_bf16 a[120:123],a[244:247],v[236:239],a[120:123]
ds_read_b64_tr_b16 a[204:205],v3 offset:8192
v_mul_f32 v16,v23,v16
v_mfma_f32_16x16x32_bf16 a[124:127],a[252:255],v[236:239],0
ds_read_b64_tr_b16 a[206:207],v3 offset:12288
ds_read_b64_tr_b16 a[200:201],v3
v_mfma_f32_16x16x32_bf16 a[124:127],a[248:251],v[232:235],a[124:127]
ds_read_b64_tr_b16 a[202:203],v3 offset:4096
v_mul_f32 v17,v23,v17
v_smfmac_f32_16x16x64_bf16 v[60:63],v[28:31],v[232:239],v27
ds_read_b64_tr_b16 a[208:209],v2 offset:256
v_mfma_f32_16x16x32_bf16 v[200:203],a[128:131],v[144:147],v[40:43]
v_exp_f32 v136,v136
v_mfma_f32_16x16x32_bf16 v[200:203],a[132:135],v[176:179],v[200:203]
v_exp_f32 v137,v137
v_mfma_f32_16x16x32_bf16 v[200:203],a[136:139],v[208:211],v[200:203]
ds_read_b64_tr_b16 a[210:211],v2 offset:4352
v_cvt_pk_bf16_f32 v136,v136,v137
v_mfma_f32_16x16x32_bf16 v[200:203],a[140:143],v[240:243],v[200:203]
buffer_load_dwordx4 v5,s[24:27],s47 offen offset:3072 nt lds
v_mfma_f32_16x16x32_bf16 v[204:207],a[156:159],v[240:243],v[40:43]
s_or_b32 s47,s47,0x18000
v_exp_f32 v138,v138
v_mfma_f32_16x16x32_bf16 v[204:207],a[152:155],v[208:211],v[204:207]
s_add_u32 s47,s47,4096
v_exp_f32 v139,v139
v_mfma_f32_16x16x32_bf16 v[204:207],a[148:151],v[176:179],v[204:207]
s_and_b32 s47,s47,0x27000
v_exp_f32 v140,v140
v_mfma_f32_16x16x32_bf16 v[204:207],a[144:147],v[144:147],v[204:207]
s_or_b32 s47,s47,s46
v_exp_f32 v141,v141
v_mfma_f32_16x16x32_bf16 v[216:219],a[160:163],v[144:147],v[40:43]
v_exp_f32 v142,v142
v_mfma_f32_16x16x32_bf16 v[216:219],a[164:167],v[176:179],v[216:219]
v_exp_f32 v143,v143
v_mfma_f32_16x16x32_bf16 v[216:219],a[168:171],v[208:211],v[216:219]
ds_read_b64_tr_b16 a[212:213],v2 offset:8448
ds_read_b64_tr_b16 a[214:215],v2 offset:12544
v_mfma_f32_16x16x32_bf16 v[216:219],a[172:175],v[240:243],v[216:219]
v_cvt_pk_bf16_f32 v137,v138,v139
v_cvt_pk_bf16_f32 v138,v140,v141
v_mfma_f32_16x16x32_bf16 v[220:223],a[188:191],v[240:243],v[40:43]
ds_read_b64_tr_b16 a[220:221],v3 offset:8448
v_cvt_pk_bf16_f32 v139,v142,v143
v_mfma_f32_16x16x32_bf16 v[220:223],a[184:187],v[208:211],v[220:223]
ds_read_b64_tr_b16 a[222:223],v3 offset:12544
v_exp_f32 v152,v152
v_exp_f32 v153,v153
v_exp_f32 v154,v154
v_exp_f32 v155,v155
v_exp_f32 v156,v156
v_exp_f32 v157,v157
v_exp_f32 v158,v158
v_exp_f32 v159,v159
v_mfma_f32_16x16x32_bf16 v[220:223],a[180:183],v[176:179],v[220:223]
ds_read_b64_tr_b16 a[216:217],v3 offset:256
ds_read_b64_tr_b16 a[218:219],v3 offset:4352
v_cvt_pk_bf16_f32 v140,v152,v153
v_cvt_pk_bf16_f32 v141,v154,v155
v_cvt_pk_bf16_f32 v142,v156,v157
v_cvt_pk_bf16_f32 v143,v158,v159
v_mfma_f32_16x16x32_bf16 v[220:223],a[176:179],v[144:147],v[220:223]
s_waitcnt lgkmcnt(6)
ds_read_b64_tr_b16 a[224:225],v2 offset:512
ds_read_b64_tr_b16 a[226:227],v2 offset:4608
v_mfma_f32_16x16x32_bf16 a[0:3],a[192:195],v[136:139],a[0:3]
ds_read_b64_tr_b16 a[228:229],v2 offset:8704
ds_read_b64_tr_b16 a[230:231],v2 offset:12800
v_mfma_f32_16x16x32_bf16 a[0:3],a[196:199],v[140:143],a[0:3]
ds_read_b64_tr_b16 a[236:237],v3 offset:8704
ds_read_b64_tr_b16 a[238:239],v3 offset:12800
v_mfma_f32_16x16x32_bf16 a[4:7],a[204:207],v[140:143],a[4:7]
ds_read_b64_tr_b16 a[232:233],v3 offset:512
ds_read_b64_tr_b16 a[234:235],v3 offset:4608
v_mfma_f32_16x16x32_bf16 a[4:7],a[200:203],v[136:139],a[4:7]
ds_read_b64_tr_b16 a[240:241],v2 offset:768
ds_read_b64_tr_b16 a[242:243],v2 offset:4864
v_mfma_f32_16x16x32_bf16 a[8:11],a[208:211],v[136:139],a[8:11]
s_memrealtime s[76:77]
ds_read_b64_tr_b16 a[244:245],v2 offset:8960
ds_read_b64_tr_b16 a[246:247],v2 offset:13056
s_waitcnt lgkmcnt(15)
v_mfma_f32_16x16x32_bf16 a[8:11],a[212:215],v[140:143],a[8:11]
s_waitcnt lgkmcnt(14)
ds_read_b64_tr_b16 a[252:253],v3 offset:8960
ds_read_b64_tr_b16 a[254:255],v3 offset:13056
v_mfma_f32_16x16x32_bf16 a[12:15],a[220:223],v[140:143],a[12:15]
s_waitcnt lgkmcnt(14)
ds_read_b64_tr_b16 a[248:249],v3 offset:768
ds_read_b64_tr_b16 a[250:251],v3 offset:4864
v_mfma_f32_16x16x32_bf16 a[12:15],a[216:219],v[136:139],a[12:15]
s_waitcnt lgkmcnt(14)
v_mfma_f32_16x16x32_bf16 a[16:19],a[224:227],v[136:139],a[16:19]
s_waitcnt lgkmcnt(12)
v_mfma_f32_16x16x32_bf16 a[16:19],a[228:231],v[140:143],a[16:19]
s_waitcnt lgkmcnt(10)
v_mfma_f32_16x16x32_bf16 a[20:23],a[236:239],v[140:143],a[20:23]
s_waitcnt lgkmcnt(0)
v_mfma_f32_16x16x32_bf16 a[20:23],a[232:235],v[136:139],a[20:23]
s_sub_u32 s79,s78,s76
v_exp_f32 v168,v168
v_mfma_f32_16x16x32_bf16 a[24:27],a[240:243],v[136:139],a[24:27]
s_add_u32 s78,s76,s60
v_exp_f32 v169,v169
v_mfma_f32_16x16x32_bf16 a[24:27],a[244:247],v[140:143],a[24:27]
s_cmp_gt_i32 s79,s95
s_cbranch_scc1 L_ps6_%=
L_pr6_%=:
v_exp_f32 v170,v170
v_exp_f32 v171,v171
v_mfma_f32_16x16x32_bf16 a[28:31],a[252:255],v[140:143],a[28:31]
v_cvt_pk_bf16_f32 v114,v114,v115
v_cvt_pk_bf16_f32 v168,v168,v169
v_exp_f32 v172,v172
v_exp_f32 v173,v173
v_exp_f32 v174,v174
v_exp_f32 v175,v175
v_cvt_pk_bf16_f32 v115,v16,v17
v_cvt_pk_bf16_f32 v169,v170,v171
v_exp_f32 v184,v184
v_exp_f32 v185,v185
v_exp_f32 v186,v186
v_exp_f32 v187,v187
v_exp_f32 v188,v188
v_exp_f32 v189,v189
v_exp_f32 v190,v190
v_exp_f32 v191,v191
v_mfma_f32_16x16x32_bf16 a[28:31],a[248:251],v[136:139],a[28:31]
s_cmp_lg_u64 s[56:57],0
s_cbranch_scc1 L_slow0_%=
L_sret0_%=:
v_cvt_pk_bf16_f32 v170,v172,v173
v_cvt_pk_bf16_f32 v171,v174,v175
v_add_u32 v1,s48,v1
v_cvt_pk_bf16_f32 v172,v184,v185
v_cvt_pk_bf16_f32 v173,v186,v187
v_cvt_pk_bf16_f32 v174,v188,v189
v_cvt_pk_bf16_f32 v175,v190,v191
v_smfmac_f32_16x16x64_bf16 v[48:51],v[28:31],v[136:143],v27
s_mov_b32 s53,11
s_cmp_eq_u32 s40,3
s_cbranch_scc0 L_att1_%=
s_cmp_gt_i32 s18,0
s_cbranch_scc1 L_att1v0_%=
s_cmp_gt_i32 s19,0
s_cbranch_scc1 L_att1v1_%=
s_cmp_gt_i32 s100,0
s_cbranch_scc1 L_att1v2_%=
.p2align 5
L_att1v3_%=:
s_waitcnt vmcnt(8) lgkmcnt(0)
v_exp_f32 v200,v200
s_barrier
s_add_u32 m0,s49,s51
ds_read_b128 a[128:131],v1
s_mov_b32 s90,L_out_end_%=-L_pc_%=+128
v_exp_f32 v201,v201
s_lshr_b32 s45,s45,1
ds_read_b128 a[132:135],v1 offset:256
v_cvt_pk_bf16_f32 v200,v200,v201
buffer_load_dwordx4 v4,s[20:23],s47 offen nt lds
ds_read_b128 a[136:139],v1 offset:512
v_add_u32 v2,s48,v2
v_exp_f32 v202,v202
s_add_u32 s62,s50,32768
ds_read_b128 a[140:143],v1 offset:768
v_add_u32 v3,s48,v3
s_and_b32 s62,s62,131071
v_exp_f32 v203,v203
s_add_u32 s54,s54,16384
ds_read_b128 a[156:159],v1 offset:4864
v_cvt_pk_bf16_f32 v201,v202,v203
s_sub_u32 s48,s62,s50
v_exp_f32 v204,v204
s_sub_u32 s53,s53,1
v_exp_f32 v205,v205
s_and_b32 s46,s54,0xfffc0000
ds_read_b128 a[152:155],v1 offset:4608
v_cvt_pk_bf16_f32 v202,v204,v205
s_mov_b32 s50,s62
v_exp_f32 v206,v206
v_exp_f32 v207,v207
ds_read_b128 a[148:151],v1 offset:4352
v_cvt_pk_bf16_f32 v203,v206,v207
v_mfma_f32_16x16x32_bf16 a[32:35],a[192:195],v[168:171],a[32:35]
v_exp_f32 v216,v216
v_mfma_f32_16x16x32_bf16 a[32:35],a[196:199],v[172:175],a[32:35]
ds_read_b128 a[144:147],v1 offset:4096
v_mfma_f32_16x16x32_bf16 a[36:39],a[204:207],v[172:175],a[36:39]
buffer_load_dwordx4 v4,s[20:23],s47 offen offset:1024 nt lds
v_mfma_f32_16x16x32_bf16 a[36:39],a[200:203],v[168:171],a[36:39]
ds_read_b128 a[160:163],v1 offset:8192
v_mfma_f32_16x16x32_bf16 a[40:43],a[208:211],v[168:171],a[40:43]
v_exp_f32 v217,v217
v_mfma_f32_16x16x32_bf16 a[40:43],a[212:215],v[172:175],a[40:43]
ds_read_b128 a[164:167],v1 offset:8448
v_cvt_pk_bf16_f32 v204,v216,v217
v_mfma_f32_16x16x32_bf16 a[44:47],a[220:223],v[172:175],a[44:47]
v_exp_f32 v218,v218
v_mfma_f32_16x16x32_bf16 a[44:47],a[216:219],v[168:171],a[44:47]
ds_read_b128 a[168:171],v1 offset:8704
v_mfma_f32_16x16x32_bf16 a[48:51],a[224:227],v[168:171],a[48:51]
v_exp_f32 v219,v219
v_mfma_f32_16x16x32_bf16 a[48:51],a[228:231],v[172:175],a[48:51]
ds_read_b128 a[172:175],v1 offset:8960
v_cvt_pk_bf16_f32 v205,v218,v219
v_mfma_f32_16x16x32_bf16 a[52:55],a[236:239],v[172:175],a[52:55]
v_exp_f32 v220,v220
v_mfma_f32_16x16x32_bf16 a[52:55],a[232:235],v[168:171],a[52:55]
ds_read_b128 a[188:191],v1 offset:13056
v_mfma_f32_16x16x32_bf16 a[56:59],a[240:243],v[168:171],a[56:59]
v_exp_f32 v221,v221
v_mfma_f32_16x16x32_bf16 a[56:59],a[244:247],v[172:175],a[56:59]
ds_read_b128 a[184:187],v1 offset:12800
v_cvt_pk_bf16_f32 v206,v220,v221
v_mfma_f32_16x16x32_bf16 a[60:63],a[252:255],v[172:175],a[60:63]
v_exp_f32 v222,v222
v_mfma_f32_16x16x32_bf16 a[60:63],a[248:251],v[168:171],a[60:63]
ds_read_b128 a[180:183],v1 offset:12544
v_smfmac_f32_16x16x64_bf16 v[52:55],v[28:31],v[168:175],v27
s_waitcnt lgkmcnt(4)
v_mfma_f32_16x16x32_bf16 v[136:139],a[128:131],v[128:131],v[32:35]
buffer_load_dwordx4 v5,s[20:23],s47 offen offset:2048 nt lds
v_mfma_f32_16x16x32_bf16 v[136:139],a[132:135],v[160:163],v[136:139]
ds_read_b128 a[176:179],v1 offset:12288
v_add_u32 v1,s48,v1
v_mfma_f32_16x16x32_bf16 v[136:139],a[136:139],v[192:195],v[136:139]
v_exp_f32 v223,v223
v_mfma_f32_16x16x32_bf16 v[136:139],a[140:143],v[224:227],v[136:139]
v_cvt_pk_bf16_f32 v207,v222,v223
v_mfma_f32_16x16x32_bf16 v[140:143],a[156:159],v[224:227],v[32:35]
v_mfma_f32_16x16x32_bf16 v[140:143],a[152:155],v[192:195],v[140:143]
v_mfma_f32_16x16x32_bf16 v[140:143],a[148:151],v[160:163],v[140:143]
)A"
R"A(v_mfma_f32_16x16x32_bf16 v[140:143],a[144:147],v[128:131],v[140:143]
v_mfma_f32_16x16x32_bf16 v[152:155],a[160:163],v[128:131],v[32:35]
v_mfma_f32_16x16x32_bf16 v[152:155],a[164:167],v[160:163],v[152:155]
v_mfma_f32_16x16x32_bf16 v[152:155],a[168:171],v[192:195],v[152:155]
s_waitcnt lgkmcnt(0)
v_mfma_f32_16x16x32_bf16 v[152:155],a[172:175],v[224:227],v[152:155]
v_mfma_f32_16x16x32_bf16 v[156:159],a[188:191],v[224:227],v[32:35]
v_mfma_f32_16x16x32_bf16 v[156:159],a[184:187],v[192:195],v[156:159]
buffer_load_dwordx4 v5,s[20:23],s47 offen offset:3072 nt lds
v_mfma_f32_16x16x32_bf16 v[156:159],a[180:183],v[160:163],v[156:159]
v_mfma_f32_16x16x32_bf16 v[156:159],a[176:179],v[128:131],v[156:159]
v_mfma_f32_16x16x32_bf16 a[64:67],a[192:195],v[200:203],a[64:67]
v_mfma_f32_16x16x32_bf16 a[64:67],a[196:199],v[204:207],a[64:67]
v_mfma_f32_16x16x32_bf16 a[68:71],a[204:207],v[204:207],a[68:71]
v_mfma_f32_16x16x32_bf16 a[68:71],a[200:203],v[200:203],a[68:71]
v_mfma_f32_16x16x32_bf16 a[72:75],a[208:211],v[200:203],a[72:75]
v_mfma_f32_16x16x32_bf16 a[72:75],a[212:215],v[204:207],a[72:75]
v_exp_f32 v136,v136
v_mfma_f32_16x16x32_bf16 a[76:79],a[220:223],v[204:207],a[76:79]
v_exp_f32 v137,v137
v_mfma_f32_16x16x32_bf16 a[76:79],a[216:219],v[200:203],a[76:79]
v_exp_f32 v138,v138
v_mfma_f32_16x16x32_bf16 a[80:83],a[224:227],v[200:203],a[80:83]
s_add_u32 m0,s49,s52
v_mfma_f32_16x16x32_bf16 a[80:83],a[228:231],v[204:207],a[80:83]
s_xor_b32 s49,s50,65536
v_mfma_f32_16x16x32_bf16 a[84:87],a[236:239],v[204:207],a[84:87]
v_mfma_f32_16x16x32_bf16 a[84:87],a[232:235],v[200:203],a[84:87]
buffer_load_dwordx4 v4,s[24:27],s47 offen nt lds
v_mfma_f32_16x16x32_bf16 a[88:91],a[240:243],v[200:203],a[88:91]
v_mfma_f32_16x16x32_bf16 a[88:91],a[244:247],v[204:207],a[88:91]
v_exp_f32 v139,v139
v_mfma_f32_16x16x32_bf16 a[92:95],a[252:255],v[204:207],a[92:95]
v_exp_f32 v140,v140
v_mfma_f32_16x16x32_bf16 a[92:95],a[248:251],v[200:203],a[92:95]
v_exp_f32 v141,v141
v_smfmac_f32_16x16x64_bf16 v[56:59],v[28:31],v[200:207],v27
v_mfma_f32_16x16x32_bf16 v[168:171],a[128:131],v[132:135],v[36:39]
v_exp_f32 v142,v142
v_mfma_f32_16x16x32_bf16 v[168:171],a[132:135],v[164:167],v[168:171]
v_exp_f32 v143,v143
v_mfma_f32_16x16x32_bf16 v[168:171],a[136:139],v[196:199],v[168:171]
v_exp_f32 v152,v152
v_mfma_f32_16x16x32_bf16 v[168:171],a[140:143],v[228:231],v[168:171]
v_exp_f32 v153,v153
v_mfma_f32_16x16x32_bf16 v[172:175],a[156:159],v[228:231],v[36:39]
v_exp_f32 v154,v154
v_mfma_f32_16x16x32_bf16 v[172:175],a[152:155],v[196:199],v[172:175]
v_exp_f32 v155,v155
v_mfma_f32_16x16x32_bf16 v[172:175],a[148:151],v[164:167],v[172:175]
v_exp_f32 v156,v156
v_mfma_f32_16x16x32_bf16 v[172:175],a[144:147],v[132:135],v[172:175]
v_exp_f32 v157,v157
v_mfma_f32_16x16x32_bf16 v[184:187],a[160:163],v[132:135],v[36:39]
buffer_load_dwordx4 v5,s[24:27],s47 offen offset:1024 nt lds
v_mfma_f32_16x16x32_bf16 v[184:187],a[164:167],v[164:167],v[184:187]
v_exp_f32 v158,v158
v_mfma_f32_16x16x32_bf16 v[184:187],a[168:171],v[196:199],v[184:187]
v_exp_f32 v159,v159
v_mfma_f32_16x16x32_bf16 v[184:187],a[172:175],v[228:231],v[184:187]
v_exp_f32 v168,v168
v_mfma_f32_16x16x32_bf16 v[188:191],a[188:191],v[228:231],v[36:39]
v_exp_f32 v169,v169
v_mfma_f32_16x16x32_bf16 v[188:191],a[184:187],v[196:199],v[188:191]
v_exp_f32 v170,v170
v_mfma_f32_16x16x32_bf16 v[188:191],a[180:183],v[164:167],v[188:191]
v_exp_f32 v171,v171
v_mfma_f32_16x16x32_bf16 v[188:191],a[176:179],v[132:135],v[188:191]
v_exp_f32 v172,v172
ds_read_b64_tr_b16 a[192:193],v2
v_cvt_pk_bf16_f32 v168,v168,v169
ds_read_b64_tr_b16 a[194:195],v2 offset:4096
v_cvt_pk_bf16_f32 v169,v170,v171
v_exp_f32 v173,v173
ds_read_b64_tr_b16 a[196:197],v2 offset:8192
v_cvt_pk_bf16_f32 v170,v172,v173
v_exp_f32 v174,v174
v_exp_f32 v175,v175
buffer_load_dwordx4 v4,s[24:27],s47 offen offset:2048 nt lds
ds_read_b64_tr_b16 a[198:199],v2 offset:12288
v_cvt_pk_bf16_f32 v171,v174,v175
v_exp_f32 v184,v184
v_exp_f32 v185,v185
ds_read_b64_tr_b16 a[204:205],v3 offset:8192
v_cvt_pk_bf16_f32 v136,v136,v137
ds_read_b64_tr_b16 a[206:207],v3 offset:12288
v_cvt_pk_bf16_f32 v172,v184,v185
ds_read_b64_tr_b16 a[200:201],v3
ds_read_b64_tr_b16 a[202:203],v3 offset:4096
v_cvt_pk_bf16_f32 v137,v138,v139
ds_read_b64_tr_b16 a[208:209],v2 offset:256
ds_read_b64_tr_b16 a[210:211],v2 offset:4352
ds_read_b64_tr_b16 a[212:213],v2 offset:8448
v_mfma_f32_16x16x32_bf16 v[200:203],a[128:131],v[144:147],v[40:43]
ds_read_b64_tr_b16 a[214:215],v2 offset:12544
v_cvt_pk_bf16_f32 v138,v140,v141
v_mfma_f32_16x16x32_bf16 v[200:203],a[132:135],v[176:179],v[200:203]
ds_read_b64_tr_b16 a[220:221],v3 offset:8448
v_mfma_f32_16x16x32_bf16 v[200:203],a[136:139],v[208:211],v[200:203]
ds_read_b64_tr_b16 a[222:223],v3 offset:12544
v_cvt_pk_bf16_f32 v139,v142,v143
v_mfma_f32_16x16x32_bf16 v[200:203],a[140:143],v[240:243],v[200:203]
buffer_load_dwordx4 v5,s[24:27],s47 offen offset:3072 nt lds
v_mfma_f32_16x16x32_bf16 v[204:207],a[156:159],v[240:243],v[40:43]
s_or_b32 s47,s47,0x18000
s_add_u32 s47,s47,4096
ds_read_b64_tr_b16 a[216:217],v3 offset:256
v_mfma_f32_16x16x32_bf16 v[204:207],a[152:155],v[208:211],v[204:207]
s_and_b32 s47,s47,0x27000
ds_read_b64_tr_b16 a[218:219],v3 offset:4352
v_cvt_pk_bf16_f32 v140,v152,v153
v_mfma_f32_16x16x32_bf16 v[204:207],a[148:151],v[176:179],v[204:207]
s_or_b32 s47,s47,s46
ds_read_b64_tr_b16 a[224:225],v2 offset:512
v_mfma_f32_16x16x32_bf16 v[204:207],a[144:147],v[144:147],v[204:207]
ds_read_b64_tr_b16 a[226:227],v2 offset:4608
v_mfma_f32_16x16x32_bf16 v[216:219],a[160:163],v[144:147],v[40:43]
ds_read_b64_tr_b16 a[228:229],v2 offset:8704
v_cvt_pk_bf16_f32 v141,v154,v155
v_mfma_f32_16x16x32_bf16 v[216:219],a[164:167],v[176:179],v[216:219]
ds_read_b64_tr_b16 a[230:231],v2 offset:12800
v_mfma_f32_16x16x32_bf16 v[216:219],a[168:171],v[208:211],v[216:219]
ds_read_b64_tr_b16 a[236:237],v3 offset:8704
v_mfma_f32_16x16x32_bf16 v[216:219],a[172:175],v[240:243],v[216:219]
ds_read_b64_tr_b16 a[238:239],v3 offset:12800
v_cvt_pk_bf16_f32 v142,v156,v157
v_mfma_f32_16x16x32_bf16 v[220:223],a[188:191],v[240:243],v[40:43]
ds_read_b64_tr_b16 a[232:233],v3 offset:512
v_mfma_f32_16x16x32_bf16 v[220:223],a[184:187],v[208:211],v[220:223]
ds_read_b64_tr_b16 a[234:235],v3 offset:4608
v_mfma_f32_16x16x32_bf16 v[220:223],a[180:183],v[176:179],v[220:223]
ds_read_b64_tr_b16 a[240:241],v2 offset:768
v_cvt_pk_bf16_f32 v143,v158,v159
v_mfma_f32_16x16x32_bf16 v[220:223],a[176:179],v[144:147],v[220:223]
s_waitcnt lgkmcnt(7)
buffer_load_dword v26,v24,s[88:91],s93 offen
v_mfma_f32_16x16x32_bf16 a[0:3],a[192:195],v[136:139],a[0:3]
ds_read_b64_tr_b16 a[242:243],v2 offset:4864
v_mfma_f32_16x16x32_bf16 a[0:3],a[196:199],v[140:143],a[0:3]
ds_read_b64_tr_b16 a[244:245],v2 offset:8960
v_mfma_f32_16x16x32_bf16 a[4:7],a[204:207],v[140:143],a[4:7]
ds_read_b64_tr_b16 a[246:247],v2 offset:13056
v_mfma_f32_16x16x32_bf16 a[4:7],a[200:203],v[136:139],a[4:7]
ds_read_b64_tr_b16 a[252:253],v3 offset:8960
v_mfma_f32_16x16x32_bf16 a[8:11],a[208:211],v[136:139],a[8:11]
ds_read_b64_tr_b16 a[254:255],v3 offset:13056
v_mfma_f32_16x16x32_bf16 a[8:11],a[212:215],v[140:143],a[8:11]
ds_read_b64_tr_b16 a[248:249],v3 offset:768
v_mfma_f32_16x16x32_bf16 a[12:15],a[220:223],v[140:143],a[12:15]
ds_read_b64_tr_b16 a[250:251],v3 offset:4864
v_mfma_f32_16x16x32_bf16 a[12:15],a[216:219],v[136:139],a[12:15]
s_bitcmp1_b32 s45,0
s_cbranch_scc0 L_t31v31_%=
s_mov_b32 m0,s80
s_add_u32 s80,s80,4096
buffer_load_dwordx4 v25,s[24:27],s83 offen nt lds
s_add_u32 s83,s83,256
L_t31v31_%=:
v_mfma_f32_16x16x32_bf16 a[16:19],a[224:227],v[136:139],a[16:19]
s_waitcnt lgkmcnt(6)
v_exp_f32 v186,v186
v_mfma_f32_16x16x32_bf16 a[16:19],a[228:231],v[140:143],a[16:19]
v_exp_f32 v187,v187
v_mfma_f32_16x16x32_bf16 a[20:23],a[236:239],v[140:143],a[20:23]
v_exp_f32 v188,v188
v_mfma_f32_16x16x32_bf16 a[20:23],a[232:235],v[136:139],a[20:23]
v_exp_f32 v189,v189
v_mfma_f32_16x16x32_bf16 a[24:27],a[240:243],v[136:139],a[24:27]
s_waitcnt lgkmcnt(2)
v_exp_f32 v190,v190
v_mfma_f32_16x16x32_bf16 a[24:27],a[244:247],v[140:143],a[24:27]
v_cvt_pk_bf16_f32 v173,v186,v187
v_mfma_f32_16x16x32_bf16 a[28:31],a[252:255],v[140:143],a[28:31]
s_waitcnt lgkmcnt(0)
v_exp_f32 v191,v191
v_mfma_f32_16x16x32_bf16 a[28:31],a[248:251],v[136:139],a[28:31]
v_cvt_pk_bf16_f32 v174,v188,v189
v_cvt_pk_bf16_f32 v175,v190,v191
s_cmp_lg_u32 s53,0
v_smfmac_f32_16x16x64_bf16 v[48:51],v[28:31],v[136:143],v27
s_cbranch_scc1 L_att1v3_%=
s_waitcnt lgkmcnt(0)
s_nop 7
s_branch L_att1x_%=
.p2align 5
L_att1v0_%=:
v_mfma_f32_16x16x32_bf16 v[232:235],a[128:131],v[148:151],v[44:47]
s_waitcnt vmcnt(8) lgkmcnt(0)
v_exp_f32 v200,v200
v_mfma_f32_16x16x32_bf16 v[232:235],a[132:135],v[180:183],v[232:235]
s_barrier
s_add_u32 m0,s49,s51
ds_read_b128 a[128:131],v1
v_mfma_f32_16x16x32_bf16 v[232:235],a[136:139],v[212:215],v[232:235]
s_mov_b32 s90,L_out_end_%=-L_pc_%=+128
v_exp_f32 v201,v201
v_mfma_f32_16x16x32_bf16 v[232:235],a[140:143],v[244:247],v[232:235]
s_lshr_b32 s45,s45,1
ds_read_b128 a[132:135],v1 offset:256
v_cvt_pk_bf16_f32 v200,v200,v201
v_mfma_f32_16x16x32_bf16 v[236:239],a[156:159],v[244:247],v[44:47]
buffer_load_dwordx4 v4,s[20:23],s47 offen nt lds
v_mfma_f32_16x16x32_bf16 v[236:239],a[152:155],v[212:215],v[236:239]
ds_read_b128 a[136:139],v1 offset:512
v_add_u32 v2,s48,v2
v_mfma_f32_16x16x32_bf16 v[236:239],a[148:151],v[180:183],v[236:239]
v_exp_f32 v202,v202
v_mfma_f32_16x16x32_bf16 v[236:239],a[144:147],v[148:151],v[236:239]
s_add_u32 s62,s50,32768
ds_read_b128 a[140:143],v1 offset:768
v_add_u32 v3,s48,v3
v_mfma_f32_16x16x32_bf16 v[248:251],a[160:163],v[148:151],v[44:47]
s_and_b32 s62,s62,131071
v_exp_f32 v203,v203
v_mfma_f32_16x16x32_bf16 v[248:251],a[164:167],v[180:183],v[248:251]
s_add_u32 s54,s54,16384
ds_read_b128 a[156:159],v1 offset:4864
v_cvt_pk_bf16_f32 v201,v202,v203
v_mfma_f32_16x16x32_bf16 v[248:251],a[168:171],v[212:215],v[248:251]
s_sub_u32 s48,s62,s50
v_exp_f32 v204,v204
v_mfma_f32_16x16x32_bf16 v[248:251],a[172:175],v[244:247],v[248:251]
s_sub_u32 s53,s53,1
v_exp_f32 v205,v205
v_mfma_f32_16x16x32_bf16 v[252:255],a[188:191],v[244:247],v[44:47]
s_and_b32 s46,s54,0xfffc0000
ds_read_b128 a[152:155],v1 offset:4608
v_cvt_pk_bf16_f32 v202,v204,v205
v_mfma_f32_16x16x32_bf16 v[252:255],a[184:187],v[212:215],v[252:255]
s_mov_b32 s50,s62
v_exp_f32 v206,v206
v_mfma_f32_16x16x32_bf16 v[252:255],a[180:183],v[180:183],v[252:255]
v_exp_f32 v207,v207
v_mfma_f32_16x16x32_bf16 v[252:255],a[176:179],v[148:151],v[252:255]
ds_read_b128 a[148:151],v1 offset:4352
v_cvt_pk_bf16_f32 v203,v206,v207
v_mfma_f32_16x16x32_bf16 a[32:35],a[192:195],v[168:171],a[32:35]
v_exp_f32 v216,v216
v_mfma_f32_16x16x32_bf16 a[32:35],a[196:199],v[172:175],a[32:35]
ds_read_b128 a[144:147],v1 offset:4096
v_mfma_f32_16x16x32_bf16 a[36:39],a[204:207],v[172:175],a[36:39]
buffer_load_dwordx4 v4,s[20:23],s47 offen offset:1024 nt lds
v_mfma_f32_16x16x32_bf16 a[36:39],a[200:203],v[168:171],a[36:39]
ds_read_b128 a[160:163],v1 offset:8192
v_mfma_f32_16x16x32_bf16 a[40:43],a[208:211],v[168:171],a[40:43]
v_exp_f32 v217,v217
v_mfma_f32_16x16x32_bf16 a[40:43],a[212:215],v[172:175],a[40:43]
ds_read_b128 a[164:167],v1 offset:8448
v_cvt_pk_bf16_f32 v204,v216,v217
v_mfma_f32_16x16x32_bf16 a[44:47],a[220:223],v[172:175],a[44:47]
v_exp_f32 v218,v218
v_mfma_f32_16x16x32_bf16 a[44:47],a[216:219],v[168:171],a[44:47]
ds_read_b128 a[168:171],v1 offset:8704
v_mfma_f32_16x16x32_bf16 a[48:51],a[224:227],v[168:171],a[48:51]
v_exp_f32 v219,v219
v_mfma_f32_16x16x32_bf16 a[48:51],a[228:231],v[172:175],a[48:51]
ds_read_b128 a[172:175],v1 offset:8960
v_cvt_pk_bf16_f32 v205,v218,v219
v_mfma_f32_16x16x32_bf16 a[52:55],a[236:239],v[172:175],a[52:55]
v_exp_f32 v220,v220
v_mfma_f32_16x16x32_bf16 a[52:55],a[232:235],v[168:171],a[52:55]
ds_read_b128 a[188:191],v1 offset:13056
v_mfma_f32_16x16x32_bf16 a[56:59],a[240:243],v[168:171],a[56:59]
v_exp_f32 v221,v221
v_mfma_f32_16x16x32_bf16 a[56:59],a[244:247],v[172:175],a[56:59]
ds_read_b128 a[184:187],v1 offset:12800
v_cvt_pk_bf16_f32 v206,v220,v221
v_mfma_f32_16x16x32_bf16 a[60:63],a[252:255],v[172:175],a[60:63]
v_exp_f32 v222,v222
)A"
R"A(v_mfma_f32_16x16x32_bf16 a[60:63],a[248:251],v[168:171],a[60:63]
ds_read_b128 a[180:183],v1 offset:12544
v_smfmac_f32_16x16x64_bf16 v[52:55],v[28:31],v[168:175],v27
buffer_load_dwordx4 v5,s[20:23],s47 offen offset:2048 nt lds
ds_read_b128 a[176:179],v1 offset:12288
v_add_u32 v1,s48,v1
v_exp_f32 v223,v223
s_nop 0
v_cvt_pk_bf16_f32 v207,v222,v223
v_exp_f32 v232,v232
v_exp_f32 v233,v233
v_exp_f32 v234,v234
v_exp_f32 v235,v235
v_exp_f32 v236,v236
v_exp_f32 v237,v237
v_exp_f32 v238,v238
v_exp_f32 v239,v239
v_exp_f32 v248,v248
buffer_load_dwordx4 v5,s[20:23],s47 offen offset:3072 nt lds
v_exp_f32 v249,v249
v_exp_f32 v250,v250
v_mfma_f32_16x16x32_bf16 a[64:67],a[192:195],v[200:203],a[64:67]
v_exp_f32 v251,v251
v_mfma_f32_16x16x32_bf16 a[64:67],a[196:199],v[204:207],a[64:67]
v_exp_f32 v252,v252
v_mfma_f32_16x16x32_bf16 a[68:71],a[204:207],v[204:207],a[68:71]
v_exp_f32 v253,v253
v_mfma_f32_16x16x32_bf16 a[68:71],a[200:203],v[200:203],a[68:71]
v_exp_f32 v254,v254
v_mfma_f32_16x16x32_bf16 a[72:75],a[208:211],v[200:203],a[72:75]
v_exp_f32 v255,v255
v_mfma_f32_16x16x32_bf16 a[72:75],a[212:215],v[204:207],a[72:75]
v_mfma_f32_16x16x32_bf16 a[76:79],a[220:223],v[204:207],a[76:79]
v_mfma_f32_16x16x32_bf16 a[76:79],a[216:219],v[200:203],a[76:79]
v_mfma_f32_16x16x32_bf16 a[80:83],a[224:227],v[200:203],a[80:83]
s_add_u32 m0,s49,s52
v_cvt_pk_bf16_f32 v232,v232,v233
v_cvt_pk_bf16_f32 v233,v234,v235
v_mfma_f32_16x16x32_bf16 a[80:83],a[228:231],v[204:207],a[80:83]
s_xor_b32 s49,s50,65536
v_cvt_pk_bf16_f32 v234,v236,v237
v_cvt_pk_bf16_f32 v235,v238,v239
v_mfma_f32_16x16x32_bf16 a[84:87],a[236:239],v[204:207],a[84:87]
v_cvt_pk_bf16_f32 v236,v248,v249
v_cvt_pk_bf16_f32 v237,v250,v251
v_mfma_f32_16x16x32_bf16 a[84:87],a[232:235],v[200:203],a[84:87]
buffer_load_dwordx4 v4,s[24:27],s47 offen nt lds
v_mfma_f32_16x16x32_bf16 a[88:91],a[240:243],v[200:203],a[88:91]
v_cvt_pk_bf16_f32 v238,v252,v253
v_cvt_pk_bf16_f32 v239,v254,v255
v_mfma_f32_16x16x32_bf16 a[88:91],a[244:247],v[204:207],a[88:91]
v_mfma_f32_16x16x32_bf16 a[92:95],a[252:255],v[204:207],a[92:95]
v_mfma_f32_16x16x32_bf16 a[92:95],a[248:251],v[200:203],a[92:95]
v_smfmac_f32_16x16x64_bf16 v[56:59],v[28:31],v[200:207],v27
s_waitcnt lgkmcnt(0)
v_mfma_f32_16x16x32_bf16 v[168:171],a[128:131],v[132:135],v[36:39]
v_mfma_f32_16x16x32_bf16 v[168:171],a[132:135],v[164:167],v[168:171]
v_mfma_f32_16x16x32_bf16 v[168:171],a[136:139],v[196:199],v[168:171]
v_mfma_f32_16x16x32_bf16 v[168:171],a[140:143],v[228:231],v[168:171]
v_mfma_f32_16x16x32_bf16 v[172:175],a[156:159],v[228:231],v[36:39]
v_mfma_f32_16x16x32_bf16 v[172:175],a[152:155],v[196:199],v[172:175]
v_mfma_f32_16x16x32_bf16 v[172:175],a[148:151],v[164:167],v[172:175]
v_mfma_f32_16x16x32_bf16 v[172:175],a[144:147],v[132:135],v[172:175]
v_mfma_f32_16x16x32_bf16 v[184:187],a[160:163],v[132:135],v[36:39]
buffer_load_dwordx4 v5,s[24:27],s47 offen offset:1024 nt lds
v_mfma_f32_16x16x32_bf16 v[184:187],a[164:167],v[164:167],v[184:187]
v_mfma_f32_16x16x32_bf16 v[184:187],a[168:171],v[196:199],v[184:187]
v_mfma_f32_16x16x32_bf16 v[184:187],a[172:175],v[228:231],v[184:187]
v_exp_f32 v168,v168
v_mfma_f32_16x16x32_bf16 v[188:191],a[188:191],v[228:231],v[36:39]
v_exp_f32 v169,v169
v_mfma_f32_16x16x32_bf16 v[188:191],a[184:187],v[196:199],v[188:191]
v_exp_f32 v170,v170
v_mfma_f32_16x16x32_bf16 v[188:191],a[180:183],v[164:167],v[188:191]
v_exp_f32 v171,v171
v_mfma_f32_16x16x32_bf16 v[188:191],a[176:179],v[132:135],v[188:191]
v_exp_f32 v172,v172
v_mfma_f32_16x16x32_bf16 a[96:99],a[192:195],v[232:235],a[96:99]
ds_read_b64_tr_b16 a[192:193],v2
v_cvt_pk_bf16_f32 v168,v168,v169
v_mfma_f32_16x16x32_bf16 a[96:99],a[196:199],v[236:239],a[96:99]
ds_read_b64_tr_b16 a[194:195],v2 offset:4096
v_cvt_pk_bf16_f32 v169,v170,v171
v_mfma_f32_16x16x32_bf16 a[100:103],a[204:207],v[236:239],a[100:103]
v_exp_f32 v173,v173
v_mfma_f32_16x16x32_bf16 a[100:103],a[200:203],v[232:235],a[100:103]
ds_read_b64_tr_b16 a[196:197],v2 offset:8192
v_cvt_pk_bf16_f32 v170,v172,v173
v_mfma_f32_16x16x32_bf16 a[104:107],a[208:211],v[232:235],a[104:107]
v_exp_f32 v174,v174
v_mfma_f32_16x16x32_bf16 a[104:107],a[212:215],v[236:239],a[104:107]
v_exp_f32 v175,v175
v_mfma_f32_16x16x32_bf16 a[108:111],a[220:223],v[236:239],a[108:111]
buffer_load_dwordx4 v4,s[24:27],s47 offen offset:2048 nt lds
v_mfma_f32_16x16x32_bf16 a[108:111],a[216:219],v[232:235],a[108:111]
ds_read_b64_tr_b16 a[198:199],v2 offset:12288
v_cvt_pk_bf16_f32 v171,v174,v175
v_mfma_f32_16x16x32_bf16 a[112:115],a[224:227],v[232:235],a[112:115]
v_exp_f32 v184,v184
v_mfma_f32_16x16x32_bf16 a[112:115],a[228:231],v[236:239],a[112:115]
v_exp_f32 v185,v185
v_mfma_f32_16x16x32_bf16 a[116:119],a[236:239],v[236:239],a[116:119]
ds_read_b64_tr_b16 a[204:205],v3 offset:8192
v_mfma_f32_16x16x32_bf16 a[116:119],a[232:235],v[232:235],a[116:119]
ds_read_b64_tr_b16 a[206:207],v3 offset:12288
v_cvt_pk_bf16_f32 v172,v184,v185
v_mfma_f32_16x16x32_bf16 a[120:123],a[240:243],v[232:235],a[120:123]
ds_read_b64_tr_b16 a[200:201],v3
v_mfma_f32_16x16x32_bf16 a[120:123],a[244:247],v[236:239],a[120:123]
ds_read_b64_tr_b16 a[202:203],v3 offset:4096
v_mfma_f32_16x16x32_bf16 a[124:127],a[252:255],v[236:239],a[124:127]
ds_read_b64_tr_b16 a[208:209],v2 offset:256
v_mfma_f32_16x16x32_bf16 a[124:127],a[248:251],v[232:235],a[124:127]
ds_read_b64_tr_b16 a[210:211],v2 offset:4352
v_smfmac_f32_16x16x64_bf16 v[60:63],v[28:31],v[232:239],v27
ds_read_b64_tr_b16 a[212:213],v2 offset:8448
v_mfma_f32_16x16x32_bf16 v[200:203],a[128:131],v[144:147],v[40:43]
ds_read_b64_tr_b16 a[214:215],v2 offset:12544
v_mfma_f32_16x16x32_bf16 v[200:203],a[132:135],v[176:179],v[200:203]
ds_read_b64_tr_b16 a[220:221],v3 offset:8448
v_mfma_f32_16x16x32_bf16 v[200:203],a[136:139],v[208:211],v[200:203]
ds_read_b64_tr_b16 a[222:223],v3 offset:12544
v_mfma_f32_16x16x32_bf16 v[200:203],a[140:143],v[240:243],v[200:203]
buffer_load_dwordx4 v5,s[24:27],s47 offen offset:3072 nt lds
v_mfma_f32_16x16x32_bf16 v[204:207],a[156:159],v[240:243],v[40:43]
s_or_b32 s47,s47,0x18000
s_add_u32 s47,s47,4096
ds_read_b64_tr_b16 a[216:217],v3 offset:256
v_mfma_f32_16x16x32_bf16 v[204:207],a[152:155],v[208:211],v[204:207]
s_and_b32 s47,s47,0x27000
ds_read_b64_tr_b16 a[218:219],v3 offset:4352
v_mfma_f32_16x16x32_bf16 v[204:207],a[148:151],v[176:179],v[204:207]
s_or_b32 s47,s47,s46
ds_read_b64_tr_b16 a[224:225],v2 offset:512
v_mfma_f32_16x16x32_bf16 v[204:207],a[144:147],v[144:147],v[204:207]
ds_read_b64_tr_b16 a[226:227],v2 offset:4608
v_mfma_f32_16x16x32_bf16 v[216:219],a[160:163],v[144:147],v[40:43]
ds_read_b64_tr_b16 a[228:229],v2 offset:8704
v_mfma_f32_16x16x32_bf16 v[216:219],a[164:167],v[176:179],v[216:219]
ds_read_b64_tr_b16 a[230:231],v2 offset:12800
v_mfma_f32_16x16x32_bf16 v[216:219],a[168:171],v[208:211],v[216:219]
ds_read_b64_tr_b16 a[236:237],v3 offset:8704
v_mfma_f32_16x16x32_bf16 v[216:219],a[172:175],v[240:243],v[216:219]
ds_read_b64_tr_b16 a[238:239],v3 offset:12800
v_mfma_f32_16x16x32_bf16 v[220:223],a[188:191],v[240:243],v[40:43]
ds_read_b64_tr_b16 a[232:233],v3 offset:512
v_mfma_f32_16x16x32_bf16 v[220:223],a[184:187],v[208:211],v[220:223]
ds_read_b64_tr_b16 a[234:235],v3 offset:4608
v_mfma_f32_16x16x32_bf16 v[220:223],a[180:183],v[176:179],v[220:223]
ds_read_b64_tr_b16 a[240:241],v2 offset:768
v_mfma_f32_16x16x32_bf16 v[220:223],a[176:179],v[144:147],v[220:223]
buffer_load_dword v26,v24,s[88:91],s93 offen
ds_read_b64_tr_b16 a[242:243],v2 offset:4864
ds_read_b64_tr_b16 a[244:245],v2 offset:8960
ds_read_b64_tr_b16 a[246:247],v2 offset:13056
ds_read_b64_tr_b16 a[252:253],v3 offset:8960
ds_read_b64_tr_b16 a[254:255],v3 offset:13056
ds_read_b64_tr_b16 a[248:249],v3 offset:768
ds_read_b64_tr_b16 a[250:251],v3 offset:4864
s_bitcmp1_b32 s45,0
s_cbranch_scc0 L_t31v01_%=
s_mov_b32 m0,s80
s_add_u32 s80,s80,4096
buffer_load_dwordx4 v25,s[24:27],s83 offen nt lds
s_add_u32 s83,s83,256
L_t31v01_%=:
v_exp_f32 v186,v186
v_exp_f32 v187,v187
v_exp_f32 v188,v188
v_exp_f32 v189,v189
v_exp_f32 v190,v190
v_cvt_pk_bf16_f32 v173,v186,v187
v_exp_f32 v191,v191
v_cvt_pk_bf16_f32 v174,v188,v189
v_cvt_pk_bf16_f32 v175,v190,v191
s_cmp_lg_u32 s53,0
s_cbranch_scc1 L_att1v0_%=
s_waitcnt lgkmcnt(0)
s_nop 7
s_branch L_att1x_%=
.p2align 5
L_att1v1_%=:
v_mfma_f32_16x16x32_bf16 v[232:235],a[128:131],v[148:151],v[44:47]
s_waitcnt vmcnt(8) lgkmcnt(0)
v_exp_f32 v200,v200
v_mfma_f32_16x16x32_bf16 v[232:235],a[132:135],v[180:183],v[232:235]
s_barrier
s_add_u32 m0,s49,s51
ds_read_b128 a[128:131],v1
v_mfma_f32_16x16x32_bf16 v[232:235],a[136:139],v[212:215],v[232:235]
s_mov_b32 s90,L_out_end_%=-L_pc_%=+128
v_exp_f32 v201,v201
v_mfma_f32_16x16x32_bf16 v[232:235],a[140:143],v[244:247],v[232:235]
s_lshr_b32 s45,s45,1
ds_read_b128 a[132:135],v1 offset:256
v_cvt_pk_bf16_f32 v200,v200,v201
v_mfma_f32_16x16x32_bf16 v[236:239],a[156:159],v[244:247],v[44:47]
buffer_load_dwordx4 v4,s[20:23],s47 offen nt lds
v_mfma_f32_16x16x32_bf16 v[236:239],a[152:155],v[212:215],v[236:239]
ds_read_b128 a[136:139],v1 offset:512
v_add_u32 v2,s48,v2
v_mfma_f32_16x16x32_bf16 v[236:239],a[148:151],v[180:183],v[236:239]
v_exp_f32 v202,v202
v_mfma_f32_16x16x32_bf16 v[236:239],a[144:147],v[148:151],v[236:239]
s_add_u32 s62,s50,32768
ds_read_b128 a[140:143],v1 offset:768
v_add_u32 v3,s48,v3
v_mfma_f32_16x16x32_bf16 v[248:251],a[160:163],v[148:151],v[44:47]
s_and_b32 s62,s62,131071
v_exp_f32 v203,v203
v_mfma_f32_16x16x32_bf16 v[248:251],a[164:167],v[180:183],v[248:251]
s_add_u32 s54,s54,16384
ds_read_b128 a[156:159],v1 offset:4864
v_cvt_pk_bf16_f32 v201,v202,v203
v_mfma_f32_16x16x32_bf16 v[248:251],a[168:171],v[212:215],v[248:251]
s_sub_u32 s48,s62,s50
v_exp_f32 v204,v204
v_mfma_f32_16x16x32_bf16 v[248:251],a[172:175],v[244:247],v[248:251]
s_sub_u32 s53,s53,1
v_exp_f32 v205,v205
v_mfma_f32_16x16x32_bf16 v[252:255],a[188:191],v[244:247],v[44:47]
s_and_b32 s46,s54,0xfffc0000
ds_read_b128 a[152:155],v1 offset:4608
v_cvt_pk_bf16_f32 v202,v204,v205
v_mfma_f32_16x16x32_bf16 v[252:255],a[184:187],v[212:215],v[252:255]
s_mov_b32 s50,s62
v_exp_f32 v206,v206
v_mfma_f32_16x16x32_bf16 v[252:255],a[180:183],v[180:183],v[252:255]
v_exp_f32 v207,v207
v_mfma_f32_16x16x32_bf16 v[252:255],a[176:179],v[148:151],v[252:255]
ds_read_b128 a[148:151],v1 offset:4352
v_cvt_pk_bf16_f32 v203,v206,v207
v_exp_f32 v216,v216
ds_read_b128 a[144:147],v1 offset:4096
buffer_load_dwordx4 v4,s[20:23],s47 offen offset:1024 nt lds
ds_read_b128 a[160:163],v1 offset:8192
v_exp_f32 v217,v217
ds_read_b128 a[164:167],v1 offset:8448
v_cvt_pk_bf16_f32 v204,v216,v217
v_exp_f32 v218,v218
ds_read_b128 a[168:171],v1 offset:8704
v_exp_f32 v219,v219
ds_read_b128 a[172:175],v1 offset:8960
v_cvt_pk_bf16_f32 v205,v218,v219
v_exp_f32 v220,v220
ds_read_b128 a[188:191],v1 offset:13056
v_exp_f32 v221,v221
ds_read_b128 a[184:187],v1 offset:12800
v_cvt_pk_bf16_f32 v206,v220,v221
v_exp_f32 v222,v222
ds_read_b128 a[180:183],v1 offset:12544
s_waitcnt lgkmcnt(11)
v_mfma_f32_16x16x32_bf16 v[136:139],a[128:131],v[128:131],v[32:35]
buffer_load_dwordx4 v5,s[20:23],s47 offen offset:2048 nt lds
v_mfma_f32_16x16x32_bf16 v[136:139],a[132:135],v[160:163],v[136:139]
ds_read_b128 a[176:179],v1 offset:12288
v_add_u32 v1,s48,v1
v_mfma_f32_16x16x32_bf16 v[136:139],a[136:139],v[192:195],v[136:139]
v_exp_f32 v223,v223
v_mfma_f32_16x16x32_bf16 v[136:139],a[140:143],v[224:227],v[136:139]
s_waitcnt lgkmcnt(11)
v_cvt_pk_bf16_f32 v207,v222,v223
v_mfma_f32_16x16x32_bf16 v[140:143],a[156:159],v[224:227],v[32:35]
s_waitcnt lgkmcnt(10)
v_exp_f32 v232,v232
v_mfma_f32_16x16x32_bf16 v[140:143],a[152:155],v[192:195],v[140:143]
s_waitcnt lgkmcnt(9)
v_exp_f32 v233,v233
v_mfma_f32_16x16x32_bf16 v[140:143],a[148:151],v[160:163],v[140:143]
s_waitcnt lgkmcnt(8)
v_exp_f32 v234,v234
v_mfma_f32_16x16x32_bf16 v[140:143],a[144:147],v[128:131],v[140:143]
s_waitcnt lgkmcnt(1)
v_exp_f32 v235,v235
v_mfma_f32_16x16x32_bf16 v[152:155],a[160:163],v[128:131],v[32:35]
v_exp_f32 v236,v236
v_mfma_f32_16x16x32_bf16 v[152:155],a[164:167],v[160:163],v[152:155]
v_exp_f32 v237,v237
v_mfma_f32_16x16x32_bf16 v[152:155],a[168:171],v[192:195],v[152:155]
v_exp_f32 v238,v238
v_mfma_f32_16x16x32_bf16 v[152:155],a[172:175],v[224:227],v[152:155]
)A"
R"A(v_exp_f32 v239,v239
v_mfma_f32_16x16x32_bf16 v[156:159],a[188:191],v[224:227],v[32:35]
v_exp_f32 v248,v248
v_mfma_f32_16x16x32_bf16 v[156:159],a[184:187],v[192:195],v[156:159]
buffer_load_dwordx4 v5,s[20:23],s47 offen offset:3072 nt lds
v_mfma_f32_16x16x32_bf16 v[156:159],a[180:183],v[160:163],v[156:159]
s_waitcnt lgkmcnt(0)
v_exp_f32 v249,v249
v_mfma_f32_16x16x32_bf16 v[156:159],a[176:179],v[128:131],v[156:159]
v_exp_f32 v250,v250
v_mfma_f32_16x16x32_bf16 a[64:67],a[192:195],v[200:203],a[64:67]
v_exp_f32 v251,v251
v_mfma_f32_16x16x32_bf16 a[64:67],a[196:199],v[204:207],a[64:67]
v_exp_f32 v252,v252
v_mfma_f32_16x16x32_bf16 a[68:71],a[204:207],v[204:207],a[68:71]
v_exp_f32 v253,v253
v_mfma_f32_16x16x32_bf16 a[68:71],a[200:203],v[200:203],a[68:71]
v_exp_f32 v254,v254
v_mfma_f32_16x16x32_bf16 a[72:75],a[208:211],v[200:203],a[72:75]
v_exp_f32 v255,v255
v_mfma_f32_16x16x32_bf16 a[72:75],a[212:215],v[204:207],a[72:75]
v_exp_f32 v136,v136
v_mfma_f32_16x16x32_bf16 a[76:79],a[220:223],v[204:207],a[76:79]
v_exp_f32 v137,v137
v_mfma_f32_16x16x32_bf16 a[76:79],a[216:219],v[200:203],a[76:79]
v_exp_f32 v138,v138
v_mfma_f32_16x16x32_bf16 a[80:83],a[224:227],v[200:203],a[80:83]
s_add_u32 m0,s49,s52
v_cvt_pk_bf16_f32 v232,v232,v233
v_cvt_pk_bf16_f32 v233,v234,v235
v_mfma_f32_16x16x32_bf16 a[80:83],a[228:231],v[204:207],a[80:83]
s_xor_b32 s49,s50,65536
v_cvt_pk_bf16_f32 v234,v236,v237
v_cvt_pk_bf16_f32 v235,v238,v239
v_mfma_f32_16x16x32_bf16 a[84:87],a[236:239],v[204:207],a[84:87]
v_cvt_pk_bf16_f32 v236,v248,v249
v_cvt_pk_bf16_f32 v237,v250,v251
v_mfma_f32_16x16x32_bf16 a[84:87],a[232:235],v[200:203],a[84:87]
buffer_load_dwordx4 v4,s[24:27],s47 offen nt lds
v_mfma_f32_16x16x32_bf16 a[88:91],a[240:243],v[200:203],a[88:91]
v_cvt_pk_bf16_f32 v238,v252,v253
v_cvt_pk_bf16_f32 v239,v254,v255
v_mfma_f32_16x16x32_bf16 a[88:91],a[244:247],v[204:207],a[88:91]
v_exp_f32 v139,v139
v_mfma_f32_16x16x32_bf16 a[92:95],a[252:255],v[204:207],a[92:95]
v_exp_f32 v140,v140
v_mfma_f32_16x16x32_bf16 a[92:95],a[248:251],v[200:203],a[92:95]
v_exp_f32 v141,v141
v_smfmac_f32_16x16x64_bf16 v[56:59],v[28:31],v[200:207],v27
v_exp_f32 v142,v142
v_exp_f32 v143,v143
v_exp_f32 v152,v152
v_exp_f32 v153,v153
v_exp_f32 v154,v154
v_exp_f32 v155,v155
v_exp_f32 v156,v156
v_exp_f32 v157,v157
buffer_load_dwordx4 v5,s[24:27],s47 offen offset:1024 nt lds
v_exp_f32 v158,v158
v_exp_f32 v159,v159
v_mfma_f32_16x16x32_bf16 a[96:99],a[192:195],v[232:235],a[96:99]
ds_read_b64_tr_b16 a[192:193],v2
v_mfma_f32_16x16x32_bf16 a[96:99],a[196:199],v[236:239],a[96:99]
ds_read_b64_tr_b16 a[194:195],v2 offset:4096
v_mfma_f32_16x16x32_bf16 a[100:103],a[204:207],v[236:239],a[100:103]
v_mfma_f32_16x16x32_bf16 a[100:103],a[200:203],v[232:235],a[100:103]
ds_read_b64_tr_b16 a[196:197],v2 offset:8192
v_mfma_f32_16x16x32_bf16 a[104:107],a[208:211],v[232:235],a[104:107]
v_mfma_f32_16x16x32_bf16 a[104:107],a[212:215],v[236:239],a[104:107]
v_mfma_f32_16x16x32_bf16 a[108:111],a[220:223],v[236:239],a[108:111]
buffer_load_dwordx4 v4,s[24:27],s47 offen offset:2048 nt lds
v_mfma_f32_16x16x32_bf16 a[108:111],a[216:219],v[232:235],a[108:111]
ds_read_b64_tr_b16 a[198:199],v2 offset:12288
v_mfma_f32_16x16x32_bf16 a[112:115],a[224:227],v[232:235],a[112:115]
v_mfma_f32_16x16x32_bf16 a[112:115],a[228:231],v[236:239],a[112:115]
v_mfma_f32_16x16x32_bf16 a[116:119],a[236:239],v[236:239],a[116:119]
ds_read_b64_tr_b16 a[204:205],v3 offset:8192
v_cvt_pk_bf16_f32 v136,v136,v137
v_mfma_f32_16x16x32_bf16 a[116:119],a[232:235],v[232:235],a[116:119]
ds_read_b64_tr_b16 a[206:207],v3 offset:12288
v_mfma_f32_16x16x32_bf16 a[120:123],a[240:243],v[232:235],a[120:123]
ds_read_b64_tr_b16 a[200:201],v3
v_mfma_f32_16x16x32_bf16 a[120:123],a[244:247],v[236:239],a[120:123]
ds_read_b64_tr_b16 a[202:203],v3 offset:4096
v_cvt_pk_bf16_f32 v137,v138,v139
v_mfma_f32_16x16x32_bf16 a[124:127],a[252:255],v[236:239],a[124:127]
ds_read_b64_tr_b16 a[208:209],v2 offset:256
v_mfma_f32_16x16x32_bf16 a[124:127],a[248:251],v[232:235],a[124:127]
ds_read_b64_tr_b16 a[210:211],v2 offset:4352
v_smfmac_f32_16x16x64_bf16 v[60:63],v[28:31],v[232:239],v27
ds_read_b64_tr_b16 a[212:213],v2 offset:8448
v_mfma_f32_16x16x32_bf16 v[200:203],a[128:131],v[144:147],v[40:43]
ds_read_b64_tr_b16 a[214:215],v2 offset:12544
v_cvt_pk_bf16_f32 v138,v140,v141
v_mfma_f32_16x16x32_bf16 v[200:203],a[132:135],v[176:179],v[200:203]
ds_read_b64_tr_b16 a[220:221],v3 offset:8448
v_mfma_f32_16x16x32_bf16 v[200:203],a[136:139],v[208:211],v[200:203]
ds_read_b64_tr_b16 a[222:223],v3 offset:12544
v_cvt_pk_bf16_f32 v139,v142,v143
v_mfma_f32_16x16x32_bf16 v[200:203],a[140:143],v[240:243],v[200:203]
buffer_load_dwordx4 v5,s[24:27],s47 offen offset:3072 nt lds
v_mfma_f32_16x16x32_bf16 v[204:207],a[156:159],v[240:243],v[40:43]
s_or_b32 s47,s47,0x18000
s_add_u32 s47,s47,4096
ds_read_b64_tr_b16 a[216:217],v3 offset:256
v_mfma_f32_16x16x32_bf16 v[204:207],a[152:155],v[208:211],v[204:207]
s_and_b32 s47,s47,0x27000
ds_read_b64_tr_b16 a[218:219],v3 offset:4352
v_cvt_pk_bf16_f32 v140,v152,v153
v_mfma_f32_16x16x32_bf16 v[204:207],a[148:151],v[176:179],v[204:207]
s_or_b32 s47,s47,s46
ds_read_b64_tr_b16 a[224:225],v2 offset:512
v_mfma_f32_16x16x32_bf16 v[204:207],a[144:147],v[144:147],v[204:207]
ds_read_b64_tr_b16 a[226:227],v2 offset:4608
v_mfma_f32_16x16x32_bf16 v[216:219],a[160:163],v[144:147],v[40:43]
ds_read_b64_tr_b16 a[228:229],v2 offset:8704
v_cvt_pk_bf16_f32 v141,v154,v155
v_mfma_f32_16x16x32_bf16 v[216:219],a[164:167],v[176:179],v[216:219]
ds_read_b64_tr_b16 a[230:231],v2 offset:12800
v_mfma_f32_16x16x32_bf16 v[216:219],a[168:171],v[208:211],v[216:219]
ds_read_b64_tr_b16 a[236:237],v3 offset:8704
v_mfma_f32_16x16x32_bf16 v[216:219],a[172:175],v[240:243],v[216:219]
ds_read_b64_tr_b16 a[238:239],v3 offset:12800
v_cvt_pk_bf16_f32 v142,v156,v157
v_mfma_f32_16x16x32_bf16 v[220:223],a[188:191],v[240:243],v[40:43]
ds_read_b64_tr_b16 a[232:233],v3 offset:512
v_mfma_f32_16x16x32_bf16 v[220:223],a[184:187],v[208:211],v[220:223]
ds_read_b64_tr_b16 a[234:235],v3 offset:4608
v_mfma_f32_16x16x32_bf16 v[220:223],a[180:183],v[176:179],v[220:223]
ds_read_b64_tr_b16 a[240:241],v2 offset:768
v_cvt_pk_bf16_f32 v143,v158,v159
v_mfma_f32_16x16x32_bf16 v[220:223],a[176:179],v[144:147],v[220:223]
s_waitcnt lgkmcnt(7)
buffer_load_dword v26,v24,s[88:91],s93 offen
v_mfma_f32_16x16x32_bf16 a[0:3],a[192:195],v[136:139],a[0:3]
ds_read_b64_tr_b16 a[242:243],v2 offset:4864
v_mfma_f32_16x16x32_bf16 a[0:3],a[196:199],v[140:143],a[0:3]
ds_read_b64_tr_b16 a[244:245],v2 offset:8960
v_mfma_f32_16x16x32_bf16 a[4:7],a[204:207],v[140:143],a[4:7]
ds_read_b64_tr_b16 a[246:247],v2 offset:13056
v_mfma_f32_16x16x32_bf16 a[4:7],a[200:203],v[136:139],a[4:7]
ds_read_b64_tr_b16 a[252:253],v3 offset:8960
v_mfma_f32_16x16x32_bf16 a[8:11],a[208:211],v[136:139],a[8:11]
ds_read_b64_tr_b16 a[254:255],v3 offset:13056
v_mfma_f32_16x16x32_bf16 a[8:11],a[212:215],v[140:143],a[8:11]
ds_read_b64_tr_b16 a[248:249],v3 offset:768
v_mfma_f32_16x16x32_bf16 a[12:15],a[220:223],v[140:143],a[12:15]
ds_read_b64_tr_b16 a[250:251],v3 offset:4864
v_mfma_f32_16x16x32_bf16 a[12:15],a[216:219],v[136:139],a[12:15]
s_bitcmp1_b32 s45,0
s_cbranch_scc0 L_t31v11_%=
s_mov_b32 m0,s80
s_add_u32 s80,s80,4096
buffer_load_dwordx4 v25,s[24:27],s83 offen nt lds
s_add_u32 s83,s83,256
L_t31v11_%=:
v_mfma_f32_16x16x32_bf16 a[16:19],a[224:227],v[136:139],a[16:19]
s_waitcnt lgkmcnt(6)
v_mfma_f32_16x16x32_bf16 a[16:19],a[228:231],v[140:143],a[16:19]
v_mfma_f32_16x16x32_bf16 a[20:23],a[236:239],v[140:143],a[20:23]
v_mfma_f32_16x16x32_bf16 a[20:23],a[232:235],v[136:139],a[20:23]
v_mfma_f32_16x16x32_bf16 a[24:27],a[240:243],v[136:139],a[24:27]
s_waitcnt lgkmcnt(2)
v_mfma_f32_16x16x32_bf16 a[24:27],a[244:247],v[140:143],a[24:27]
v_mfma_f32_16x16x32_bf16 a[28:31],a[252:255],v[140:143],a[28:31]
s_waitcnt lgkmcnt(0)
v_mfma_f32_16x16x32_bf16 a[28:31],a[248:251],v[136:139],a[28:31]
s_cmp_lg_u32 s53,0
v_smfmac_f32_16x16x64_bf16 v[48:51],v[28:31],v[136:143],v27
s_cbranch_scc1 L_att1v1_%=
s_waitcnt lgkmcnt(0)
s_nop 7
s_branch L_att1x_%=
.p2align 5
L_att1v2_%=:
v_mfma_f32_16x16x32_bf16 v[232:235],a[128:131],v[148:151],v[44:47]
s_waitcnt vmcnt(8) lgkmcnt(0)
v_mfma_f32_16x16x32_bf16 v[232:235],a[132:135],v[180:183],v[232:235]
s_barrier
s_add_u32 m0,s49,s51
ds_read_b128 a[128:131],v1
v_mfma_f32_16x16x32_bf16 v[232:235],a[136:139],v[212:215],v[232:235]
s_mov_b32 s90,L_out_end_%=-L_pc_%=+128
v_mfma_f32_16x16x32_bf16 v[232:235],a[140:143],v[244:247],v[232:235]
s_lshr_b32 s45,s45,1
ds_read_b128 a[132:135],v1 offset:256
v_mfma_f32_16x16x32_bf16 v[236:239],a[156:159],v[244:247],v[44:47]
buffer_load_dwordx4 v4,s[20:23],s47 offen nt lds
v_mfma_f32_16x16x32_bf16 v[236:239],a[152:155],v[212:215],v[236:239]
ds_read_b128 a[136:139],v1 offset:512
v_add_u32 v2,s48,v2
v_mfma_f32_16x16x32_bf16 v[236:239],a[148:151],v[180:183],v[236:239]
v_mfma_f32_16x16x32_bf16 v[236:239],a[144:147],v[148:151],v[236:239]
s_add_u32 s62,s50,32768
ds_read_b128 a[140:143],v1 offset:768
v_add_u32 v3,s48,v3
v_mfma_f32_16x16x32_bf16 v[248:251],a[160:163],v[148:151],v[44:47]
s_and_b32 s62,s62,131071
v_mfma_f32_16x16x32_bf16 v[248:251],a[164:167],v[180:183],v[248:251]
s_add_u32 s54,s54,16384
ds_read_b128 a[156:159],v1 offset:4864
v_mfma_f32_16x16x32_bf16 v[248:251],a[168:171],v[212:215],v[248:251]
s_sub_u32 s48,s62,s50
v_mfma_f32_16x16x32_bf16 v[248:251],a[172:175],v[244:247],v[248:251]
s_sub_u32 s53,s53,1
v_mfma_f32_16x16x32_bf16 v[252:255],a[188:191],v[244:247],v[44:47]
s_and_b32 s46,s54,0xfffc0000
ds_read_b128 a[152:155],v1 offset:4608
v_mfma_f32_16x16x32_bf16 v[252:255],a[184:187],v[212:215],v[252:255]
s_mov_b32 s50,s62
v_mfma_f32_16x16x32_bf16 v[252:255],a[180:183],v[180:183],v[252:255]
v_mfma_f32_16x16x32_bf16 v[252:255],a[176:179],v[148:151],v[252:255]
ds_read_b128 a[148:151],v1 offset:4352
v_mfma_f32_16x16x32_bf16 a[32:35],a[192:195],v[168:171],a[32:35]
v_mfma_f32_16x16x32_bf16 a[32:35],a[196:199],v[172:175],a[32:35]
ds_read_b128 a[144:147],v1 offset:4096
v_mfma_f32_16x16x32_bf16 a[36:39],a[204:207],v[172:175],a[36:39]
buffer_load_dwordx4 v4,s[20:23],s47 offen offset:1024 nt lds
v_mfma_f32_16x16x32_bf16 a[36:39],a[200:203],v[168:171],a[36:39]
ds_read_b128 a[160:163],v1 offset:8192
v_mfma_f32_16x16x32_bf16 a[40:43],a[208:211],v[168:171],a[40:43]
v_mfma_f32_16x16x32_bf16 a[40:43],a[212:215],v[172:175],a[40:43]
ds_read_b128 a[164:167],v1 offset:8448
v_mfma_f32_16x16x32_bf16 a[44:47],a[220:223],v[172:175],a[44:47]
v_mfma_f32_16x16x32_bf16 a[44:47],a[216:219],v[168:171],a[44:47]
ds_read_b128 a[168:171],v1 offset:8704
v_mfma_f32_16x16x32_bf16 a[48:51],a[224:227],v[168:171],a[48:51]
v_mfma_f32_16x16x32_bf16 a[48:51],a[228:231],v[172:175],a[48:51]
ds_read_b128 a[172:175],v1 offset:8960
v_mfma_f32_16x16x32_bf16 a[52:55],a[236:239],v[172:175],a[52:55]
v_mfma_f32_16x16x32_bf16 a[52:55],a[232:235],v[168:171],a[52:55]
ds_read_b128 a[188:191],v1 offset:13056
v_mfma_f32_16x16x32_bf16 a[56:59],a[240:243],v[168:171],a[56:59]
v_mfma_f32_16x16x32_bf16 a[56:59],a[244:247],v[172:175],a[56:59]
ds_read_b128 a[184:187],v1 offset:12800
v_mfma_f32_16x16x32_bf16 a[60:63],a[252:255],v[172:175],a[60:63]
v_mfma_f32_16x16x32_bf16 a[60:63],a[248:251],v[168:171],a[60:63]
ds_read_b128 a[180:183],v1 offset:12544
v_smfmac_f32_16x16x64_bf16 v[52:55],v[28:31],v[168:175],v27
s_waitcnt lgkmcnt(4)
v_mfma_f32_16x16x32_bf16 v[136:139],a[128:131],v[128:131],v[32:35]
buffer_load_dwordx4 v5,s[20:23],s47 offen offset:2048 nt lds
v_mfma_f32_16x16x32_bf16 v[136:139],a[132:135],v[160:163],v[136:139]
ds_read_b128 a[176:179],v1 offset:12288
v_add_u32 v1,s48,v1
v_mfma_f32_16x16x32_bf16 v[136:139],a[136:139],v[192:195],v[136:139]
v_mfma_f32_16x16x32_bf16 v[136:139],a[140:143],v[224:227],v[136:139]
v_mfma_f32_16x16x32_bf16 v[140:143],a[156:159],v[224:227],v[32:35]
v_exp_f32 v232,v232
v_mfma_f32_16x16x32_bf16 v[140:143],a[152:155],v[192:195],v[140:143]
v_exp_f32 v233,v233
v_mfma_f32_16x16x32_bf16 v[140:143],a[148:151],v[160:163],v[140:143]
v_exp_f32 v234,v234
v_mfma_f32_16x16x32_bf16 v[140:143],a[144:147],v[128:131],v[140:143]
v_exp_f32 v235,v235
v_mfma_f32_16x16x32_bf16 v[152:155],a[160:163],v[128:131],v[32:35]
)A"
R"A(v_exp_f32 v236,v236
v_mfma_f32_16x16x32_bf16 v[152:155],a[164:167],v[160:163],v[152:155]
v_exp_f32 v237,v237
v_mfma_f32_16x16x32_bf16 v[152:155],a[168:171],v[192:195],v[152:155]
s_waitcnt lgkmcnt(0)
v_exp_f32 v238,v238
v_mfma_f32_16x16x32_bf16 v[152:155],a[172:175],v[224:227],v[152:155]
v_exp_f32 v239,v239
v_mfma_f32_16x16x32_bf16 v[156:159],a[188:191],v[224:227],v[32:35]
v_exp_f32 v248,v248
v_mfma_f32_16x16x32_bf16 v[156:159],a[184:187],v[192:195],v[156:159]
buffer_load_dwordx4 v5,s[20:23],s47 offen offset:3072 nt lds
v_mfma_f32_16x16x32_bf16 v[156:159],a[180:183],v[160:163],v[156:159]
v_exp_f32 v249,v249
v_mfma_f32_16x16x32_bf16 v[156:159],a[176:179],v[128:131],v[156:159]
v_exp_f32 v250,v250
v_exp_f32 v251,v251
v_exp_f32 v252,v252
v_exp_f32 v253,v253
v_exp_f32 v254,v254
v_exp_f32 v255,v255
v_exp_f32 v136,v136
v_exp_f32 v137,v137
v_exp_f32 v138,v138
s_add_u32 m0,s49,s52
v_cvt_pk_bf16_f32 v232,v232,v233
v_cvt_pk_bf16_f32 v233,v234,v235
s_xor_b32 s49,s50,65536
v_cvt_pk_bf16_f32 v234,v236,v237
v_cvt_pk_bf16_f32 v235,v238,v239
v_cvt_pk_bf16_f32 v236,v248,v249
v_cvt_pk_bf16_f32 v237,v250,v251
buffer_load_dwordx4 v4,s[24:27],s47 offen nt lds
v_cvt_pk_bf16_f32 v238,v252,v253
v_cvt_pk_bf16_f32 v239,v254,v255
v_exp_f32 v139,v139
v_exp_f32 v140,v140
v_exp_f32 v141,v141
v_mfma_f32_16x16x32_bf16 v[168:171],a[128:131],v[132:135],v[36:39]
v_exp_f32 v142,v142
v_mfma_f32_16x16x32_bf16 v[168:171],a[132:135],v[164:167],v[168:171]
v_exp_f32 v143,v143
v_mfma_f32_16x16x32_bf16 v[168:171],a[136:139],v[196:199],v[168:171]
v_exp_f32 v152,v152
v_mfma_f32_16x16x32_bf16 v[168:171],a[140:143],v[228:231],v[168:171]
v_exp_f32 v153,v153
v_mfma_f32_16x16x32_bf16 v[172:175],a[156:159],v[228:231],v[36:39]
v_exp_f32 v154,v154
v_mfma_f32_16x16x32_bf16 v[172:175],a[152:155],v[196:199],v[172:175]
v_exp_f32 v155,v155
v_mfma_f32_16x16x32_bf16 v[172:175],a[148:151],v[164:167],v[172:175]
v_exp_f32 v156,v156
v_mfma_f32_16x16x32_bf16 v[172:175],a[144:147],v[132:135],v[172:175]
v_exp_f32 v157,v157
v_mfma_f32_16x16x32_bf16 v[184:187],a[160:163],v[132:135],v[36:39]
buffer_load_dwordx4 v5,s[24:27],s47 offen offset:1024 nt lds
v_mfma_f32_16x16x32_bf16 v[184:187],a[164:167],v[164:167],v[184:187]
v_exp_f32 v158,v158
v_mfma_f32_16x16x32_bf16 v[184:187],a[168:171],v[196:199],v[184:187]
v_exp_f32 v159,v159
v_mfma_f32_16x16x32_bf16 v[184:187],a[172:175],v[228:231],v[184:187]
v_exp_f32 v168,v168
v_mfma_f32_16x16x32_bf16 v[188:191],a[188:191],v[228:231],v[36:39]
v_exp_f32 v169,v169
v_mfma_f32_16x16x32_bf16 v[188:191],a[184:187],v[196:199],v[188:191]
v_exp_f32 v170,v170
v_mfma_f32_16x16x32_bf16 v[188:191],a[180:183],v[164:167],v[188:191]
v_exp_f32 v171,v171
v_mfma_f32_16x16x32_bf16 v[188:191],a[176:179],v[132:135],v[188:191]
v_exp_f32 v172,v172
v_mfma_f32_16x16x32_bf16 a[96:99],a[192:195],v[232:235],a[96:99]
ds_read_b64_tr_b16 a[192:193],v2
v_cvt_pk_bf16_f32 v168,v168,v169
v_mfma_f32_16x16x32_bf16 a[96:99],a[196:199],v[236:239],a[96:99]
ds_read_b64_tr_b16 a[194:195],v2 offset:4096
v_cvt_pk_bf16_f32 v169,v170,v171
v_mfma_f32_16x16x32_bf16 a[100:103],a[204:207],v[236:239],a[100:103]
v_exp_f32 v173,v173
v_mfma_f32_16x16x32_bf16 a[100:103],a[200:203],v[232:235],a[100:103]
ds_read_b64_tr_b16 a[196:197],v2 offset:8192
v_cvt_pk_bf16_f32 v170,v172,v173
v_mfma_f32_16x16x32_bf16 a[104:107],a[208:211],v[232:235],a[104:107]
v_exp_f32 v174,v174
v_mfma_f32_16x16x32_bf16 a[104:107],a[212:215],v[236:239],a[104:107]
v_exp_f32 v175,v175
v_mfma_f32_16x16x32_bf16 a[108:111],a[220:223],v[236:239],a[108:111]
buffer_load_dwordx4 v4,s[24:27],s47 offen offset:2048 nt lds
v_mfma_f32_16x16x32_bf16 a[108:111],a[216:219],v[232:235],a[108:111]
ds_read_b64_tr_b16 a[198:199],v2 offset:12288
v_cvt_pk_bf16_f32 v171,v174,v175
v_mfma_f32_16x16x32_bf16 a[112:115],a[224:227],v[232:235],a[112:115]
v_exp_f32 v184,v184
v_mfma_f32_16x16x32_bf16 a[112:115],a[228:231],v[236:239],a[112:115]
v_exp_f32 v185,v185
v_mfma_f32_16x16x32_bf16 a[116:119],a[236:239],v[236:239],a[116:119]
ds_read_b64_tr_b16 a[204:205],v3 offset:8192
v_cvt_pk_bf16_f32 v136,v136,v137
v_mfma_f32_16x16x32_bf16 a[116:119],a[232:235],v[232:235],a[116:119]
ds_read_b64_tr_b16 a[206:207],v3 offset:12288
v_cvt_pk_bf16_f32 v172,v184,v185
v_mfma_f32_16x16x32_bf16 a[120:123],a[240:243],v[232:235],a[120:123]
ds_read_b64_tr_b16 a[200:201],v3
v_mfma_f32_16x16x32_bf16 a[120:123],a[244:247],v[236:239],a[120:123]
ds_read_b64_tr_b16 a[202:203],v3 offset:4096
v_cvt_pk_bf16_f32 v137,v138,v139
v_mfma_f32_16x16x32_bf16 a[124:127],a[252:255],v[236:239],a[124:127]
ds_read_b64_tr_b16 a[208:209],v2 offset:256
v_mfma_f32_16x16x32_bf16 a[124:127],a[248:251],v[232:235],a[124:127]
ds_read_b64_tr_b16 a[210:211],v2 offset:4352
v_smfmac_f32_16x16x64_bf16 v[60:63],v[28:31],v[232:239],v27
ds_read_b64_tr_b16 a[212:213],v2 offset:8448
ds_read_b64_tr_b16 a[214:215],v2 offset:12544
v_cvt_pk_bf16_f32 v138,v140,v141
ds_read_b64_tr_b16 a[220:221],v3 offset:8448
ds_read_b64_tr_b16 a[222:223],v3 offset:12544
v_cvt_pk_bf16_f32 v139,v142,v143
buffer_load_dwordx4 v5,s[24:27],s47 offen offset:3072 nt lds
s_or_b32 s47,s47,0x18000
s_add_u32 s47,s47,4096
ds_read_b64_tr_b16 a[216:217],v3 offset:256
s_and_b32 s47,s47,0x27000
ds_read_b64_tr_b16 a[218:219],v3 offset:4352
v_cvt_pk_bf16_f32 v140,v152,v153
s_or_b32 s47,s47,s46
ds_read_b64_tr_b16 a[224:225],v2 offset:512
ds_read_b64_tr_b16 a[226:227],v2 offset:4608
ds_read_b64_tr_b16 a[228:229],v2 offset:8704
v_cvt_pk_bf16_f32 v141,v154,v155
ds_read_b64_tr_b16 a[230:231],v2 offset:12800
ds_read_b64_tr_b16 a[236:237],v3 offset:8704
ds_read_b64_tr_b16 a[238:239],v3 offset:12800
v_cvt_pk_bf16_f32 v142,v156,v157
ds_read_b64_tr_b16 a[232:233],v3 offset:512
ds_read_b64_tr_b16 a[234:235],v3 offset:4608
ds_read_b64_tr_b16 a[240:241],v2 offset:768
v_cvt_pk_bf16_f32 v143,v158,v159
buffer_load_dword v26,v24,s[88:91],s93 offen
s_waitcnt lgkmcnt(15)
v_mfma_f32_16x16x32_bf16 a[0:3],a[192:195],v[136:139],a[0:3]
ds_read_b64_tr_b16 a[242:243],v2 offset:4864
v_mfma_f32_16x16x32_bf16 a[0:3],a[196:199],v[140:143],a[0:3]
ds_read_b64_tr_b16 a[244:245],v2 offset:8960
v_mfma_f32_16x16x32_bf16 a[4:7],a[204:207],v[140:143],a[4:7]
ds_read_b64_tr_b16 a[246:247],v2 offset:13056
v_mfma_f32_16x16x32_bf16 a[4:7],a[200:203],v[136:139],a[4:7]
ds_read_b64_tr_b16 a[252:253],v3 offset:8960
v_mfma_f32_16x16x32_bf16 a[8:11],a[208:211],v[136:139],a[8:11]
s_waitcnt lgkmcnt(15)
ds_read_b64_tr_b16 a[254:255],v3 offset:13056
v_mfma_f32_16x16x32_bf16 a[8:11],a[212:215],v[140:143],a[8:11]
ds_read_b64_tr_b16 a[248:249],v3 offset:768
v_mfma_f32_16x16x32_bf16 a[12:15],a[220:223],v[140:143],a[12:15]
s_waitcnt lgkmcnt(15)
ds_read_b64_tr_b16 a[250:251],v3 offset:4864
v_mfma_f32_16x16x32_bf16 a[12:15],a[216:219],v[136:139],a[12:15]
s_bitcmp1_b32 s45,0
s_cbranch_scc0 L_t31v21_%=
s_mov_b32 m0,s80
s_add_u32 s80,s80,4096
buffer_load_dwordx4 v25,s[24:27],s83 offen nt lds
s_add_u32 s83,s83,256
L_t31v21_%=:
s_waitcnt lgkmcnt(7)
v_mfma_f32_16x16x32_bf16 a[16:19],a[224:227],v[136:139],a[16:19]
v_exp_f32 v186,v186
v_mfma_f32_16x16x32_bf16 a[16:19],a[228:231],v[140:143],a[16:19]
v_exp_f32 v187,v187
v_mfma_f32_16x16x32_bf16 a[20:23],a[236:239],v[140:143],a[20:23]
v_exp_f32 v188,v188
v_mfma_f32_16x16x32_bf16 a[20:23],a[232:235],v[136:139],a[20:23]
s_waitcnt lgkmcnt(3)
v_exp_f32 v189,v189
v_mfma_f32_16x16x32_bf16 a[24:27],a[240:243],v[136:139],a[24:27]
v_exp_f32 v190,v190
v_mfma_f32_16x16x32_bf16 a[24:27],a[244:247],v[140:143],a[24:27]
s_waitcnt lgkmcnt(1)
v_cvt_pk_bf16_f32 v173,v186,v187
v_mfma_f32_16x16x32_bf16 a[28:31],a[252:255],v[140:143],a[28:31]
s_waitcnt lgkmcnt(0)
v_exp_f32 v191,v191
v_mfma_f32_16x16x32_bf16 a[28:31],a[248:251],v[136:139],a[28:31]
v_cvt_pk_bf16_f32 v174,v188,v189
v_cvt_pk_bf16_f32 v175,v190,v191
s_cmp_lg_u32 s53,0
v_smfmac_f32_16x16x64_bf16 v[48:51],v[28:31],v[136:143],v27
s_cbranch_scc1 L_att1v2_%=
s_waitcnt lgkmcnt(0)
s_nop 7
s_branch L_att1x_%=
.p2align 5
L_att1_%=:
v_mfma_f32_16x16x32_bf16 v[232:235],a[128:131],v[148:151],v[44:47]
s_waitcnt vmcnt(8) lgkmcnt(0)
v_exp_f32 v200,v200
v_mfma_f32_16x16x32_bf16 v[232:235],a[132:135],v[180:183],v[232:235]
s_barrier
s_add_u32 m0,s49,s51
ds_read_b128 a[128:131],v1
v_mfma_f32_16x16x32_bf16 v[232:235],a[136:139],v[212:215],v[232:235]
s_mov_b32 s90,L_out_end_%=-L_pc_%=+128
v_exp_f32 v201,v201
v_mfma_f32_16x16x32_bf16 v[232:235],a[140:143],v[244:247],v[232:235]
s_lshr_b32 s45,s45,1
ds_read_b128 a[132:135],v1 offset:256
v_cvt_pk_bf16_f32 v200,v200,v201
v_mfma_f32_16x16x32_bf16 v[236:239],a[156:159],v[244:247],v[44:47]
s_sub_u32 s94,s94,3
buffer_load_dwordx4 v4,s[20:23],s47 offen nt lds
v_mfma_f32_16x16x32_bf16 v[236:239],a[152:155],v[212:215],v[236:239]
s_lshr_b32 s60,s94,2
ds_read_b128 a[136:139],v1 offset:512
v_add_u32 v2,s48,v2
v_mfma_f32_16x16x32_bf16 v[236:239],a[148:151],v[180:183],v[236:239]
s_max_i32 s60,s60,128
v_exp_f32 v202,v202
v_mfma_f32_16x16x32_bf16 v[236:239],a[144:147],v[148:151],v[236:239]
s_add_u32 s62,s50,32768
ds_read_b128 a[140:143],v1 offset:768
v_add_u32 v3,s48,v3
v_mfma_f32_16x16x32_bf16 v[248:251],a[160:163],v[148:151],v[44:47]
s_and_b32 s62,s62,131071
v_exp_f32 v203,v203
v_mfma_f32_16x16x32_bf16 v[248:251],a[164:167],v[180:183],v[248:251]
s_add_u32 s54,s54,16384
ds_read_b128 a[156:159],v1 offset:4864
v_cvt_pk_bf16_f32 v201,v202,v203
v_mfma_f32_16x16x32_bf16 v[248:251],a[168:171],v[212:215],v[248:251]
s_sub_u32 s48,s62,s50
v_exp_f32 v204,v204
v_mfma_f32_16x16x32_bf16 v[248:251],a[172:175],v[244:247],v[248:251]
s_sub_u32 s53,s53,1
v_exp_f32 v205,v205
v_mfma_f32_16x16x32_bf16 v[252:255],a[188:191],v[244:247],v[44:47]
s_and_b32 s46,s54,0xfffc0000
ds_read_b128 a[152:155],v1 offset:4608
v_cvt_pk_bf16_f32 v202,v204,v205
v_mfma_f32_16x16x32_bf16 v[252:255],a[184:187],v[212:215],v[252:255]
s_mov_b32 s50,s62
v_exp_f32 v206,v206
v_mfma_f32_16x16x32_bf16 v[252:255],a[180:183],v[180:183],v[252:255]
v_exp_f32 v207,v207
v_mfma_f32_16x16x32_bf16 v[252:255],a[176:179],v[148:151],v[252:255]
ds_read_b128 a[148:151],v1 offset:4352
v_cvt_pk_bf16_f32 v203,v206,v207
v_mfma_f32_16x16x32_bf16 a[32:35],a[192:195],v[168:171],a[32:35]
v_exp_f32 v216,v216
v_mfma_f32_16x16x32_bf16 a[32:35],a[196:199],v[172:175],a[32:35]
ds_read_b128 a[144:147],v1 offset:4096
v_mfma_f32_16x16x32_bf16 a[36:39],a[204:207],v[172:175],a[36:39]
buffer_load_dwordx4 v4,s[20:23],s47 offen offset:1024 nt lds
v_mfma_f32_16x16x32_bf16 a[36:39],a[200:203],v[168:171],a[36:39]
ds_read_b128 a[160:163],v1 offset:8192
v_mfma_f32_16x16x32_bf16 a[40:43],a[208:211],v[168:171],a[40:43]
v_exp_f32 v217,v217
v_mfma_f32_16x16x32_bf16 a[40:43],a[212:215],v[172:175],a[40:43]
ds_read_b128 a[164:167],v1 offset:8448
v_cvt_pk_bf16_f32 v204,v216,v217
v_mfma_f32_16x16x32_bf16 a[44:47],a[220:223],v[172:175],a[44:47]
v_exp_f32 v218,v218
v_mfma_f32_16x16x32_bf16 a[44:47],a[216:219],v[168:171],a[44:47]
ds_read_b128 a[168:171],v1 offset:8704
v_mfma_f32_16x16x32_bf16 a[48:51],a[224:227],v[168:171],a[48:51]
v_exp_f32 v219,v219
v_mfma_f32_16x16x32_bf16 a[48:51],a[228:231],v[172:175],a[48:51]
ds_read_b128 a[172:175],v1 offset:8960
v_cvt_pk_bf16_f32 v205,v218,v219
v_mfma_f32_16x16x32_bf16 a[52:55],a[236:239],v[172:175],a[52:55]
v_exp_f32 v220,v220
v_mfma_f32_16x16x32_bf16 a[52:55],a[232:235],v[168:171],a[52:55]
ds_read_b128 a[188:191],v1 offset:13056
v_mfma_f32_16x16x32_bf16 a[56:59],a[240:243],v[168:171],a[56:59]
v_exp_f32 v221,v221
v_mfma_f32_16x16x32_bf16 a[56:59],a[244:247],v[172:175],a[56:59]
ds_read_b128 a[184:187],v1 offset:12800
v_cvt_pk_bf16_f32 v206,v220,v221
v_mfma_f32_16x16x32_bf16 a[60:63],a[252:255],v[172:175],a[60:63]
v_exp_f32 v222,v222
v_mfma_f32_16x16x32_bf16 a[60:63],a[248:251],v[168:171],a[60:63]
ds_read_b128 a[180:183],v1 offset:12544
v_smfmac_f32_16x16x64_bf16 v[52:55],v[28:31],v[168:175],v27
s_waitcnt lgkmcnt(4)
v_mfma_f32_16x16x32_bf16 v[136:139],a[128:131],v[128:131],v[32:35]
buffer_load_dwordx4 v5,s[20:23],s47 offen offset:2048 nt lds
v_mfma_f32_16x16x32_bf16 v[136:139],a[132:135],v[160:163],v[136:139]
ds_read_b128 a[176:179],v1 offset:12288
v_add_u32 v1,s48,v1
v_mfma_f32_16x16x32_bf16 v[136:139],a[136:139],v[192:195],v[136:139]
v_exp_f32 v223,v223
)A"
R"A(v_mfma_f32_16x16x32_bf16 v[136:139],a[140:143],v[224:227],v[136:139]
v_cvt_pk_bf16_f32 v207,v222,v223
v_mfma_f32_16x16x32_bf16 v[140:143],a[156:159],v[224:227],v[32:35]
v_exp_f32 v232,v232
v_mfma_f32_16x16x32_bf16 v[140:143],a[152:155],v[192:195],v[140:143]
v_exp_f32 v233,v233
v_mfma_f32_16x16x32_bf16 v[140:143],a[148:151],v[160:163],v[140:143]
v_exp_f32 v234,v234
v_mfma_f32_16x16x32_bf16 v[140:143],a[144:147],v[128:131],v[140:143]
v_exp_f32 v235,v235
v_mfma_f32_16x16x32_bf16 v[152:155],a[160:163],v[128:131],v[32:35]
v_exp_f32 v236,v236
v_mfma_f32_16x16x32_bf16 v[152:155],a[164:167],v[160:163],v[152:155]
v_exp_f32 v237,v237
v_mfma_f32_16x16x32_bf16 v[152:155],a[168:171],v[192:195],v[152:155]
s_waitcnt lgkmcnt(0)
v_exp_f32 v238,v238
v_mfma_f32_16x16x32_bf16 v[152:155],a[172:175],v[224:227],v[152:155]
v_exp_f32 v239,v239
v_mfma_f32_16x16x32_bf16 v[156:159],a[188:191],v[224:227],v[32:35]
v_exp_f32 v248,v248
v_mfma_f32_16x16x32_bf16 v[156:159],a[184:187],v[192:195],v[156:159]
buffer_load_dwordx4 v5,s[20:23],s47 offen offset:3072 nt lds
v_mfma_f32_16x16x32_bf16 v[156:159],a[180:183],v[160:163],v[156:159]
v_exp_f32 v249,v249
v_mfma_f32_16x16x32_bf16 v[156:159],a[176:179],v[128:131],v[156:159]
v_exp_f32 v250,v250
v_mfma_f32_16x16x32_bf16 a[64:67],a[192:195],v[200:203],a[64:67]
v_exp_f32 v251,v251
v_mfma_f32_16x16x32_bf16 a[64:67],a[196:199],v[204:207],a[64:67]
v_exp_f32 v252,v252
v_mfma_f32_16x16x32_bf16 a[68:71],a[204:207],v[204:207],a[68:71]
v_exp_f32 v253,v253
v_mfma_f32_16x16x32_bf16 a[68:71],a[200:203],v[200:203],a[68:71]
v_exp_f32 v254,v254
v_mfma_f32_16x16x32_bf16 a[72:75],a[208:211],v[200:203],a[72:75]
v_exp_f32 v255,v255
v_mfma_f32_16x16x32_bf16 a[72:75],a[212:215],v[204:207],a[72:75]
v_exp_f32 v136,v136
v_mfma_f32_16x16x32_bf16 a[76:79],a[220:223],v[204:207],a[76:79]
v_exp_f32 v137,v137
v_mfma_f32_16x16x32_bf16 a[76:79],a[216:219],v[200:203],a[76:79]
v_exp_f32 v138,v138
v_mfma_f32_16x16x32_bf16 a[80:83],a[224:227],v[200:203],a[80:83]
s_add_u32 m0,s49,s52
v_cvt_pk_bf16_f32 v232,v232,v233
v_cvt_pk_bf16_f32 v233,v234,v235
v_mfma_f32_16x16x32_bf16 a[80:83],a[228:231],v[204:207],a[80:83]
s_xor_b32 s49,s50,65536
v_cvt_pk_bf16_f32 v234,v236,v237
v_cvt_pk_bf16_f32 v235,v238,v239
v_mfma_f32_16x16x32_bf16 a[84:87],a[236:239],v[204:207],a[84:87]
v_cvt_pk_bf16_f32 v236,v248,v249
v_cvt_pk_bf16_f32 v237,v250,v251
v_mfma_f32_16x16x32_bf16 a[84:87],a[232:235],v[200:203],a[84:87]
buffer_load_dwordx4 v4,s[24:27],s47 offen nt lds
v_mfma_f32_16x16x32_bf16 a[88:91],a[240:243],v[200:203],a[88:91]
v_cvt_pk_bf16_f32 v238,v252,v253
v_cvt_pk_bf16_f32 v239,v254,v255
v_mfma_f32_16x16x32_bf16 a[88:91],a[244:247],v[204:207],a[88:91]
v_exp_f32 v139,v139
v_mfma_f32_16x16x32_bf16 a[92:95],a[252:255],v[204:207],a[92:95]
v_exp_f32 v140,v140
v_mfma_f32_16x16x32_bf16 a[92:95],a[248:251],v[200:203],a[92:95]
v_exp_f32 v141,v141
v_smfmac_f32_16x16x64_bf16 v[56:59],v[28:31],v[200:207],v27
v_mfma_f32_16x16x32_bf16 v[168:171],a[128:131],v[132:135],v[36:39]
v_exp_f32 v142,v142
v_mfma_f32_16x16x32_bf16 v[168:171],a[132:135],v[164:167],v[168:171]
v_exp_f32 v143,v143
v_mfma_f32_16x16x32_bf16 v[168:171],a[136:139],v[196:199],v[168:171]
v_exp_f32 v152,v152
v_mfma_f32_16x16x32_bf16 v[168:171],a[140:143],v[228:231],v[168:171]
v_exp_f32 v153,v153
v_mfma_f32_16x16x32_bf16 v[172:175],a[156:159],v[228:231],v[36:39]
v_exp_f32 v154,v154
v_mfma_f32_16x16x32_bf16 v[172:175],a[152:155],v[196:199],v[172:175]
v_exp_f32 v155,v155
v_mfma_f32_16x16x32_bf16 v[172:175],a[148:151],v[164:167],v[172:175]
v_exp_f32 v156,v156
v_mfma_f32_16x16x32_bf16 v[172:175],a[144:147],v[132:135],v[172:175]
v_exp_f32 v157,v157
v_mfma_f32_16x16x32_bf16 v[184:187],a[160:163],v[132:135],v[36:39]
buffer_load_dwordx4 v5,s[24:27],s47 offen offset:1024 nt lds
v_mfma_f32_16x16x32_bf16 v[184:187],a[164:167],v[164:167],v[184:187]
v_exp_f32 v158,v158
v_mfma_f32_16x16x32_bf16 v[184:187],a[168:171],v[196:199],v[184:187]
v_exp_f32 v159,v159
v_mfma_f32_16x16x32_bf16 v[184:187],a[172:175],v[228:231],v[184:187]
v_exp_f32 v168,v168
v_mfma_f32_16x16x32_bf16 v[188:191],a[188:191],v[228:231],v[36:39]
v_exp_f32 v169,v169
v_mfma_f32_16x16x32_bf16 v[188:191],a[184:187],v[196:199],v[188:191]
v_exp_f32 v170,v170
v_mfma_f32_16x16x32_bf16 v[188:191],a[180:183],v[164:167],v[188:191]
v_exp_f32 v171,v171
v_mfma_f32_16x16x32_bf16 v[188:191],a[176:179],v[132:135],v[188:191]
v_exp_f32 v172,v172
v_mfma_f32_16x16x32_bf16 a[96:99],a[192:195],v[232:235],a[96:99]
ds_read_b64_tr_b16 a[192:193],v2
v_cvt_pk_bf16_f32 v168,v168,v169
v_mfma_f32_16x16x32_bf16 a[96:99],a[196:199],v[236:239],a[96:99]
ds_read_b64_tr_b16 a[194:195],v2 offset:4096
v_cvt_pk_bf16_f32 v169,v170,v171
v_mfma_f32_16x16x32_bf16 a[100:103],a[204:207],v[236:239],a[100:103]
v_exp_f32 v173,v173
v_mfma_f32_16x16x32_bf16 a[100:103],a[200:203],v[232:235],a[100:103]
ds_read_b64_tr_b16 a[196:197],v2 offset:8192
v_cvt_pk_bf16_f32 v170,v172,v173
v_mfma_f32_16x16x32_bf16 a[104:107],a[208:211],v[232:235],a[104:107]
v_exp_f32 v174,v174
v_mfma_f32_16x16x32_bf16 a[104:107],a[212:215],v[236:239],a[104:107]
v_exp_f32 v175,v175
v_mfma_f32_16x16x32_bf16 a[108:111],a[220:223],v[236:239],a[108:111]
buffer_load_dwordx4 v4,s[24:27],s47 offen offset:2048 nt lds
v_mfma_f32_16x16x32_bf16 a[108:111],a[216:219],v[232:235],a[108:111]
ds_read_b64_tr_b16 a[198:199],v2 offset:12288
v_cvt_pk_bf16_f32 v171,v174,v175
v_mfma_f32_16x16x32_bf16 a[112:115],a[224:227],v[232:235],a[112:115]
v_exp_f32 v184,v184
v_mfma_f32_16x16x32_bf16 a[112:115],a[228:231],v[236:239],a[112:115]
v_exp_f32 v185,v185
v_mfma_f32_16x16x32_bf16 a[116:119],a[236:239],v[236:239],a[116:119]
ds_read_b64_tr_b16 a[204:205],v3 offset:8192
v_cvt_pk_bf16_f32 v136,v136,v137
v_mfma_f32_16x16x32_bf16 a[116:119],a[232:235],v[232:235],a[116:119]
ds_read_b64_tr_b16 a[206:207],v3 offset:12288
v_cvt_pk_bf16_f32 v172,v184,v185
v_mfma_f32_16x16x32_bf16 a[120:123],a[240:243],v[232:235],a[120:123]
ds_read_b64_tr_b16 a[200:201],v3
v_mfma_f32_16x16x32_bf16 a[120:123],a[244:247],v[236:239],a[120:123]
ds_read_b64_tr_b16 a[202:203],v3 offset:4096
v_cvt_pk_bf16_f32 v137,v138,v139
v_mfma_f32_16x16x32_bf16 a[124:127],a[252:255],v[236:239],a[124:127]
ds_read_b64_tr_b16 a[208:209],v2 offset:256
v_mfma_f32_16x16x32_bf16 a[124:127],a[248:251],v[232:235],a[124:127]
ds_read_b64_tr_b16 a[210:211],v2 offset:4352
v_smfmac_f32_16x16x64_bf16 v[60:63],v[28:31],v[232:239],v27
ds_read_b64_tr_b16 a[212:213],v2 offset:8448
v_mfma_f32_16x16x32_bf16 v[200:203],a[128:131],v[144:147],v[40:43]
ds_read_b64_tr_b16 a[214:215],v2 offset:12544
v_cvt_pk_bf16_f32 v138,v140,v141
v_mfma_f32_16x16x32_bf16 v[200:203],a[132:135],v[176:179],v[200:203]
ds_read_b64_tr_b16 a[220:221],v3 offset:8448
v_mfma_f32_16x16x32_bf16 v[200:203],a[136:139],v[208:211],v[200:203]
ds_read_b64_tr_b16 a[222:223],v3 offset:12544
v_cvt_pk_bf16_f32 v139,v142,v143
v_mfma_f32_16x16x32_bf16 v[200:203],a[140:143],v[240:243],v[200:203]
buffer_load_dwordx4 v5,s[24:27],s47 offen offset:3072 nt lds
v_mfma_f32_16x16x32_bf16 v[204:207],a[156:159],v[240:243],v[40:43]
s_or_b32 s47,s47,0x18000
s_add_u32 s47,s47,4096
ds_read_b64_tr_b16 a[216:217],v3 offset:256
v_mfma_f32_16x16x32_bf16 v[204:207],a[152:155],v[208:211],v[204:207]
s_and_b32 s47,s47,0x27000
ds_read_b64_tr_b16 a[218:219],v3 offset:4352
v_cvt_pk_bf16_f32 v140,v152,v153
v_mfma_f32_16x16x32_bf16 v[204:207],a[148:151],v[176:179],v[204:207]
s_or_b32 s47,s47,s46
ds_read_b64_tr_b16 a[224:225],v2 offset:512
v_mfma_f32_16x16x32_bf16 v[204:207],a[144:147],v[144:147],v[204:207]
ds_read_b64_tr_b16 a[226:227],v2 offset:4608
v_mfma_f32_16x16x32_bf16 v[216:219],a[160:163],v[144:147],v[40:43]
ds_read_b64_tr_b16 a[228:229],v2 offset:8704
v_cvt_pk_bf16_f32 v141,v154,v155
v_mfma_f32_16x16x32_bf16 v[216:219],a[164:167],v[176:179],v[216:219]
ds_read_b64_tr_b16 a[230:231],v2 offset:12800
v_mfma_f32_16x16x32_bf16 v[216:219],a[168:171],v[208:211],v[216:219]
ds_read_b64_tr_b16 a[236:237],v3 offset:8704
v_mfma_f32_16x16x32_bf16 v[216:219],a[172:175],v[240:243],v[216:219]
ds_read_b64_tr_b16 a[238:239],v3 offset:12800
v_cvt_pk_bf16_f32 v142,v156,v157
v_mfma_f32_16x16x32_bf16 v[220:223],a[188:191],v[240:243],v[40:43]
ds_read_b64_tr_b16 a[232:233],v3 offset:512
v_mfma_f32_16x16x32_bf16 v[220:223],a[184:187],v[208:211],v[220:223]
ds_read_b64_tr_b16 a[234:235],v3 offset:4608
v_mfma_f32_16x16x32_bf16 v[220:223],a[180:183],v[176:179],v[220:223]
ds_read_b64_tr_b16 a[240:241],v2 offset:768
v_cvt_pk_bf16_f32 v143,v158,v159
v_mfma_f32_16x16x32_bf16 v[220:223],a[176:179],v[144:147],v[220:223]
s_waitcnt lgkmcnt(7)
buffer_load_dword v26,v24,s[88:91],s93 offen
v_mfma_f32_16x16x32_bf16 a[0:3],a[192:195],v[136:139],a[0:3]
ds_read_b64_tr_b16 a[242:243],v2 offset:4864
v_mfma_f32_16x16x32_bf16 a[0:3],a[196:199],v[140:143],a[0:3]
ds_read_b64_tr_b16 a[244:245],v2 offset:8960
v_mfma_f32_16x16x32_bf16 a[4:7],a[204:207],v[140:143],a[4:7]
ds_read_b64_tr_b16 a[246:247],v2 offset:13056
v_mfma_f32_16x16x32_bf16 a[4:7],a[200:203],v[136:139],a[4:7]
s_memrealtime s[76:77]
ds_read_b64_tr_b16 a[252:253],v3 offset:8960
v_mfma_f32_16x16x32_bf16 a[8:11],a[208:211],v[136:139],a[8:11]
ds_read_b64_tr_b16 a[254:255],v3 offset:13056
v_mfma_f32_16x16x32_bf16 a[8:11],a[212:215],v[140:143],a[8:11]
ds_read_b64_tr_b16 a[248:249],v3 offset:768
v_mfma_f32_16x16x32_bf16 a[12:15],a[220:223],v[140:143],a[12:15]
ds_read_b64_tr_b16 a[250:251],v3 offset:4864
v_mfma_f32_16x16x32_bf16 a[12:15],a[216:219],v[136:139],a[12:15]
s_bitcmp1_b32 s45,0
s_cbranch_scc0 L_t311_%=
s_mov_b32 m0,s80
s_add_u32 s80,s80,4096
buffer_load_dwordx4 v25,s[24:27],s83 offen nt lds
s_add_u32 s83,s83,256
L_t311_%=:
v_mfma_f32_16x16x32_bf16 a[16:19],a[224:227],v[136:139],a[16:19]
s_waitcnt lgkmcnt(6)
v_exp_f32 v186,v186
v_mfma_f32_16x16x32_bf16 a[16:19],a[228:231],v[140:143],a[16:19]
v_exp_f32 v187,v187
v_mfma_f32_16x16x32_bf16 a[20:23],a[236:239],v[140:143],a[20:23]
s_waitcnt lgkmcnt(0)
v_exp_f32 v188,v188
v_mfma_f32_16x16x32_bf16 a[20:23],a[232:235],v[136:139],a[20:23]
s_sub_u32 s79,s78,s76
v_exp_f32 v189,v189
v_mfma_f32_16x16x32_bf16 a[24:27],a[240:243],v[136:139],a[24:27]
s_add_u32 s78,s76,s60
v_exp_f32 v190,v190
v_mfma_f32_16x16x32_bf16 a[24:27],a[244:247],v[140:143],a[24:27]
s_cmp_gt_i32 s79,s95
s_cbranch_scc1 L_ps7_%=
L_pr7_%=:
v_cvt_pk_bf16_f32 v173,v186,v187
v_mfma_f32_16x16x32_bf16 a[28:31],a[252:255],v[140:143],a[28:31]
v_exp_f32 v191,v191
v_mfma_f32_16x16x32_bf16 a[28:31],a[248:251],v[136:139],a[28:31]
v_cvt_pk_bf16_f32 v174,v188,v189
v_cvt_pk_bf16_f32 v175,v190,v191
s_cmp_lg_u32 s53,0
v_smfmac_f32_16x16x64_bf16 v[48:51],v[28:31],v[136:143],v27
s_cbranch_scc1 L_att1_%=
L_att1x_%=:
L_endseg_%=:
v_mfma_f32_16x16x32_bf16 v[232:235],a[128:131],v[148:151],v[44:47]
s_waitcnt vmcnt(10)
s_lshl_b32 s43,s41,2
v_exp_f32 v200,v200
v_mfma_f32_16x16x32_bf16 v[232:235],a[132:135],v[180:183],v[232:235]
s_barrier
ds_read_b128 a[128:131],v1
v_add_u32 v2,s48,v2
v_mfma_f32_16x16x32_bf16 v[232:235],a[136:139],v[212:215],v[232:235]
s_add_u32 s43,s43,s70
v_exp_f32 v201,v201
v_mfma_f32_16x16x32_bf16 v[232:235],a[140:143],v[244:247],v[232:235]
s_lshl_b32 s43,s43,6
ds_read_b128 a[132:135],v1 offset:256
v_cvt_pk_bf16_f32 v200,v200,v201
v_mfma_f32_16x16x32_bf16 v[236:239],a[156:159],v[244:247],v[44:47]
s_add_u32 s43,s43,s40
v_exp_f32 v202,v202
v_mfma_f32_16x16x32_bf16 v[236:239],a[152:155],v[212:215],v[236:239]
s_lshl_b32 s43,s43,15
ds_read_b128 a[136:139],v1 offset:512
v_add_u32 v3,s48,v3
v_mfma_f32_16x16x32_bf16 v[236:239],a[148:151],v[180:183],v[236:239]
s_mov_b32 s48,32768
v_exp_f32 v203,v203
v_mfma_f32_16x16x32_bf16 v[236:239],a[144:147],v[148:151],v[236:239]
s_mov_b64 s[28:29],s[12:13]
ds_read_b128 a[140:143],v1 offset:768
v_cvt_pk_bf16_f32 v201,v202,v203
v_mfma_f32_16x16x32_bf16 v[248:251],a[160:163],v[148:151],v[44:47]
s_mov_b32 s30,950272
v_exp_f32 v204,v204
v_mfma_f32_16x16x32_bf16 v[248:251],a[164:167],v[180:183],v[248:251]
s_mov_b32 s31,0x20000
ds_read_b128 a[156:159],v1 offset:4864
v_lshrrev_b32 v4,6,v6
v_mfma_f32_16x16x32_bf16 v[248:251],a[168:171],v[212:215],v[248:251]
v_exp_f32 v205,v205
v_mfma_f32_16x16x32_bf16 v[248:251],a[172:175],v[244:247],v[248:251]
)A"
R"A(ds_read_b128 a[152:155],v1 offset:4608
v_cvt_pk_bf16_f32 v202,v204,v205
v_mfma_f32_16x16x32_bf16 v[252:255],a[188:191],v[244:247],v[44:47]
v_exp_f32 v206,v206
v_mfma_f32_16x16x32_bf16 v[252:255],a[184:187],v[212:215],v[252:255]
ds_read_b128 a[148:151],v1 offset:4352
v_mul_u32_u24 v4,320,v4
v_mfma_f32_16x16x32_bf16 v[252:255],a[180:183],v[180:183],v[252:255]
v_exp_f32 v207,v207
v_mfma_f32_16x16x32_bf16 v[252:255],a[176:179],v[148:151],v[252:255]
ds_read_b128 a[144:147],v1 offset:4096
v_cvt_pk_bf16_f32 v203,v206,v207
v_mfma_f32_16x16x32_bf16 a[32:35],a[192:195],v[168:171],a[32:35]
v_exp_f32 v216,v216
v_mfma_f32_16x16x32_bf16 a[32:35],a[196:199],v[172:175],a[32:35]
ds_read_b128 a[160:163],v1 offset:8192
v_bfe_u32 v5,v6,4,2
v_mfma_f32_16x16x32_bf16 a[36:39],a[204:207],v[172:175],a[36:39]
v_exp_f32 v217,v217
v_mfma_f32_16x16x32_bf16 a[36:39],a[200:203],v[168:171],a[36:39]
ds_read_b128 a[164:167],v1 offset:8448
v_cvt_pk_bf16_f32 v204,v216,v217
v_mfma_f32_16x16x32_bf16 a[40:43],a[208:211],v[168:171],a[40:43]
v_exp_f32 v218,v218
v_mfma_f32_16x16x32_bf16 a[40:43],a[212:215],v[172:175],a[40:43]
ds_read_b128 a[168:171],v1 offset:8704
v_lshl_add_u32 v4,v5,3,v4
v_mfma_f32_16x16x32_bf16 a[44:47],a[220:223],v[172:175],a[44:47]
v_exp_f32 v219,v219
v_mfma_f32_16x16x32_bf16 a[44:47],a[216:219],v[168:171],a[44:47]
ds_read_b128 a[172:175],v1 offset:8960
v_cvt_pk_bf16_f32 v205,v218,v219
v_mfma_f32_16x16x32_bf16 a[48:51],a[224:227],v[168:171],a[48:51]
v_exp_f32 v220,v220
v_mfma_f32_16x16x32_bf16 a[48:51],a[228:231],v[172:175],a[48:51]
ds_read_b128 a[188:191],v1 offset:13056
v_bfe_u32 v5,v6,8,1
v_mfma_f32_16x16x32_bf16 a[52:55],a[236:239],v[172:175],a[52:55]
v_exp_f32 v221,v221
v_mfma_f32_16x16x32_bf16 a[52:55],a[232:235],v[168:171],a[52:55]
ds_read_b128 a[184:187],v1 offset:12800
v_cvt_pk_bf16_f32 v206,v220,v221
v_mfma_f32_16x16x32_bf16 a[56:59],a[240:243],v[168:171],a[56:59]
v_exp_f32 v222,v222
v_mfma_f32_16x16x32_bf16 a[56:59],a[244:247],v[172:175],a[56:59]
ds_read_b128 a[180:183],v1 offset:12544
v_lshlrev_b32 v5,5,v5
v_mfma_f32_16x16x32_bf16 a[60:63],a[252:255],v[172:175],a[60:63]
v_exp_f32 v223,v223
v_mfma_f32_16x16x32_bf16 a[60:63],a[248:251],v[168:171],a[60:63]
ds_read_b128 a[176:179],v1 offset:12288
v_cvt_pk_bf16_f32 v207,v222,v223
v_smfmac_f32_16x16x64_bf16 v[52:55],v[28:31],v[168:175],v27
s_waitcnt lgkmcnt(4)
v_add_u32 v1,s48,v1
v_mfma_f32_16x16x32_bf16 v[136:139],a[128:131],v[128:131],v[32:35]
v_exp_f32 v232,v232
v_mfma_f32_16x16x32_bf16 v[136:139],a[132:135],v[160:163],v[136:139]
v_exp_f32 v233,v233
v_mfma_f32_16x16x32_bf16 v[136:139],a[136:139],v[192:195],v[136:139]
v_exp_f32 v234,v234
v_mfma_f32_16x16x32_bf16 v[136:139],a[140:143],v[224:227],v[136:139]
v_exp_f32 v235,v235
v_mfma_f32_16x16x32_bf16 v[140:143],a[156:159],v[224:227],v[32:35]
v_cvt_pk_bf16_f32 v232,v232,v233
v_cvt_pk_bf16_f32 v233,v234,v235
v_mfma_f32_16x16x32_bf16 v[140:143],a[152:155],v[192:195],v[140:143]
s_add_u32 s80,s71,0
v_exp_f32 v236,v236
v_mfma_f32_16x16x32_bf16 v[140:143],a[148:151],v[160:163],v[140:143]
s_and_b32 s80,s80,3
v_exp_f32 v237,v237
v_mfma_f32_16x16x32_bf16 v[140:143],a[144:147],v[128:131],v[140:143]
s_lshl_b32 s80,s80,1
s_mul_i32 s80,s80,163840
s_add_u32 s80,s80,s81
v_mfma_f32_16x16x32_bf16 v[152:155],a[160:163],v[128:131],v[32:35]
s_add_u32 s83,s80,20480
v_exp_f32 v238,v238
v_mfma_f32_16x16x32_bf16 v[152:155],a[164:167],v[160:163],v[152:155]
buffer_store_dwordx4 v[64:67],v6,s[36:39],s84 offen sc1
v_mfma_f32_16x16x32_bf16 v[152:155],a[168:171],v[192:195],v[152:155]
v_exp_f32 v239,v239
v_mfma_f32_16x16x32_bf16 v[152:155],a[172:175],v[224:227],v[152:155]
s_waitcnt lgkmcnt(0)
v_cvt_pk_bf16_f32 v234,v236,v237
v_cvt_pk_bf16_f32 v235,v238,v239
v_mfma_f32_16x16x32_bf16 v[156:159],a[188:191],v[224:227],v[32:35]
v_exp_f32 v248,v248
v_mfma_f32_16x16x32_bf16 v[156:159],a[184:187],v[192:195],v[156:159]
v_exp_f32 v249,v249
v_mfma_f32_16x16x32_bf16 v[156:159],a[180:183],v[160:163],v[156:159]
v_exp_f32 v250,v250
v_mfma_f32_16x16x32_bf16 v[156:159],a[176:179],v[128:131],v[156:159]
v_exp_f32 v251,v251
v_mfma_f32_16x16x32_bf16 a[64:67],a[192:195],v[200:203],a[64:67]
v_exp_f32 v252,v252
v_mfma_f32_16x16x32_bf16 a[64:67],a[196:199],v[204:207],a[64:67]
v_exp_f32 v253,v253
v_mfma_f32_16x16x32_bf16 a[68:71],a[204:207],v[204:207],a[68:71]
v_exp_f32 v254,v254
v_mfma_f32_16x16x32_bf16 a[68:71],a[200:203],v[200:203],a[68:71]
v_exp_f32 v255,v255
v_mfma_f32_16x16x32_bf16 a[72:75],a[208:211],v[200:203],a[72:75]
v_cvt_pk_bf16_f32 v236,v248,v249
v_cvt_pk_bf16_f32 v237,v250,v251
v_mfma_f32_16x16x32_bf16 a[72:75],a[212:215],v[204:207],a[72:75]
v_cvt_pk_bf16_f32 v238,v252,v253
v_cvt_pk_bf16_f32 v239,v254,v255
v_mfma_f32_16x16x32_bf16 a[76:79],a[220:223],v[204:207],a[76:79]
v_exp_f32 v136,v136
v_mfma_f32_16x16x32_bf16 a[76:79],a[216:219],v[200:203],a[76:79]
v_exp_f32 v137,v137
v_mfma_f32_16x16x32_bf16 a[80:83],a[224:227],v[200:203],a[80:83]
v_exp_f32 v138,v138
v_mfma_f32_16x16x32_bf16 a[80:83],a[228:231],v[204:207],a[80:83]
v_exp_f32 v139,v139
v_mfma_f32_16x16x32_bf16 a[84:87],a[236:239],v[204:207],a[84:87]
s_add_u32 m0,s82,81920
v_exp_f32 v140,v140
v_mfma_f32_16x16x32_bf16 a[84:87],a[232:235],v[200:203],a[84:87]
v_cvt_pk_bf16_f32 v136,v136,v137
v_cvt_pk_bf16_f32 v137,v138,v139
v_mfma_f32_16x16x32_bf16 a[88:91],a[240:243],v[200:203],a[88:91]
v_exp_f32 v141,v141
v_mfma_f32_16x16x32_bf16 a[88:91],a[244:247],v[204:207],a[88:91]
buffer_load_dwordx4 v7,s[72:75],s80 offen lds
v_mfma_f32_16x16x32_bf16 a[92:95],a[252:255],v[204:207],a[92:95]
v_exp_f32 v142,v142
v_mfma_f32_16x16x32_bf16 a[92:95],a[248:251],v[200:203],a[92:95]
v_exp_f32 v143,v143
v_smfmac_f32_16x16x64_bf16 v[56:59],v[28:31],v[200:207],v27
v_cvt_pk_bf16_f32 v138,v140,v141
v_mfma_f32_16x16x32_bf16 v[168:171],a[128:131],v[132:135],v[36:39]
buffer_store_dwordx4 v[68:71],v6,s[36:39],s84 offen offset:1024 sc1
v_mfma_f32_16x16x32_bf16 v[168:171],a[132:135],v[164:167],v[168:171]
s_add_u32 s28,s28,s43
s_addc_u32 s29,s29,0
v_cvt_pk_bf16_f32 v139,v142,v143
v_mfma_f32_16x16x32_bf16 v[168:171],a[136:139],v[196:199],v[168:171]
s_and_b32 s29,s29,0xffff
v_exp_f32 v152,v152
v_mfma_f32_16x16x32_bf16 v[168:171],a[140:143],v[228:231],v[168:171]
v_exp_f32 v153,v153
v_mfma_f32_16x16x32_bf16 v[172:175],a[156:159],v[228:231],v[36:39]
v_exp_f32 v154,v154
v_mfma_f32_16x16x32_bf16 v[172:175],a[152:155],v[196:199],v[172:175]
v_exp_f32 v155,v155
v_mfma_f32_16x16x32_bf16 v[172:175],a[148:151],v[164:167],v[172:175]
v_exp_f32 v156,v156
v_mfma_f32_16x16x32_bf16 v[172:175],a[144:147],v[132:135],v[172:175]
v_exp_f32 v157,v157
v_mfma_f32_16x16x32_bf16 v[184:187],a[160:163],v[132:135],v[36:39]
v_exp_f32 v158,v158
v_mfma_f32_16x16x32_bf16 v[184:187],a[164:167],v[164:167],v[184:187]
v_exp_f32 v159,v159
v_mfma_f32_16x16x32_bf16 v[184:187],a[168:171],v[196:199],v[184:187]
v_cvt_pk_bf16_f32 v140,v152,v153
v_cvt_pk_bf16_f32 v141,v154,v155
v_mfma_f32_16x16x32_bf16 v[184:187],a[172:175],v[228:231],v[184:187]
v_cvt_pk_bf16_f32 v142,v156,v157
v_cvt_pk_bf16_f32 v143,v158,v159
v_mfma_f32_16x16x32_bf16 v[188:191],a[188:191],v[228:231],v[36:39]
v_exp_f32 v168,v168
v_mfma_f32_16x16x32_bf16 v[188:191],a[184:187],v[196:199],v[188:191]
v_exp_f32 v169,v169
v_mfma_f32_16x16x32_bf16 v[188:191],a[180:183],v[164:167],v[188:191]
v_exp_f32 v170,v170
v_mfma_f32_16x16x32_bf16 v[188:191],a[176:179],v[132:135],v[188:191]
v_exp_f32 v171,v171
v_mfma_f32_16x16x32_bf16 a[96:99],a[192:195],v[232:235],a[96:99]
buffer_load_dwordx4 v8,s[72:75],s80 offen offset:1024 lds
v_mfma_f32_16x16x32_bf16 a[96:99],a[196:199],v[236:239],a[96:99]
ds_read_b64_tr_b16 a[192:193],v2
ds_read_b64_tr_b16 a[194:195],v2 offset:4096
v_mfma_f32_16x16x32_bf16 a[100:103],a[204:207],v[236:239],a[100:103]
ds_read_b64_tr_b16 a[196:197],v2 offset:8192
v_cvt_pk_bf16_f32 v168,v168,v169
v_mfma_f32_16x16x32_bf16 a[100:103],a[200:203],v[232:235],a[100:103]
ds_read_b64_tr_b16 a[198:199],v2 offset:12288
v_cvt_pk_bf16_f32 v169,v170,v171
v_mfma_f32_16x16x32_bf16 a[104:107],a[208:211],v[232:235],a[104:107]
buffer_store_dwordx4 v[72:75],v6,s[36:39],s84 offen offset:2048 sc1
v_mfma_f32_16x16x32_bf16 a[104:107],a[212:215],v[236:239],a[104:107]
ds_read_b64_tr_b16 a[204:205],v3 offset:8192
ds_read_b64_tr_b16 a[206:207],v3 offset:12288
v_mfma_f32_16x16x32_bf16 a[108:111],a[220:223],v[236:239],a[108:111]
v_exp_f32 v172,v172
v_mfma_f32_16x16x32_bf16 a[108:111],a[216:219],v[232:235],a[108:111]
ds_read_b64_tr_b16 a[200:201],v3
ds_read_b64_tr_b16 a[202:203],v3 offset:4096
v_mfma_f32_16x16x32_bf16 a[112:115],a[224:227],v[232:235],a[112:115]
v_exp_f32 v173,v173
v_mfma_f32_16x16x32_bf16 a[112:115],a[228:231],v[236:239],a[112:115]
ds_read_b64_tr_b16 a[208:209],v2 offset:256
ds_read_b64_tr_b16 a[210:211],v2 offset:4352
v_mfma_f32_16x16x32_bf16 a[116:119],a[236:239],v[236:239],a[116:119]
ds_read_b64_tr_b16 a[212:213],v2 offset:8448
v_cvt_pk_bf16_f32 v170,v172,v173
v_mfma_f32_16x16x32_bf16 a[116:119],a[232:235],v[232:235],a[116:119]
v_exp_f32 v174,v174
v_mfma_f32_16x16x32_bf16 a[120:123],a[240:243],v[232:235],a[120:123]
ds_read_b64_tr_b16 a[214:215],v2 offset:12544
ds_read_b64_tr_b16 a[220:221],v3 offset:8448
v_mfma_f32_16x16x32_bf16 a[120:123],a[244:247],v[236:239],a[120:123]
ds_read_b64_tr_b16 a[222:223],v3 offset:12544
v_mfma_f32_16x16x32_bf16 a[124:127],a[252:255],v[236:239],a[124:127]
ds_read_b64_tr_b16 a[216:217],v3 offset:256
v_mfma_f32_16x16x32_bf16 a[124:127],a[248:251],v[232:235],a[124:127]
ds_read_b64_tr_b16 a[218:219],v3 offset:4352
v_smfmac_f32_16x16x64_bf16 v[60:63],v[28:31],v[232:239],v27
ds_read_b64_tr_b16 a[224:225],v2 offset:512
v_mfma_f32_16x16x32_bf16 v[200:203],a[128:131],v[144:147],v[40:43]
ds_read_b64_tr_b16 a[226:227],v2 offset:4608
ds_read_b64_tr_b16 a[228:229],v2 offset:8704
v_mfma_f32_16x16x32_bf16 v[200:203],a[132:135],v[176:179],v[200:203]
ds_read_b64_tr_b16 a[230:231],v2 offset:12800
v_mfma_f32_16x16x32_bf16 v[200:203],a[136:139],v[208:211],v[200:203]
ds_read_b64_tr_b16 a[236:237],v3 offset:8704
v_mfma_f32_16x16x32_bf16 v[200:203],a[140:143],v[240:243],v[200:203]
buffer_load_dwordx4 v9,s[72:75],s80 offen offset:2048 lds
v_mfma_f32_16x16x32_bf16 v[204:207],a[156:159],v[240:243],v[40:43]
ds_read_b64_tr_b16 a[238:239],v3 offset:12800
ds_read_b64_tr_b16 a[232:233],v3 offset:512
v_mfma_f32_16x16x32_bf16 v[204:207],a[152:155],v[208:211],v[204:207]
ds_read_b64_tr_b16 a[234:235],v3 offset:4608
v_mfma_f32_16x16x32_bf16 v[204:207],a[148:151],v[176:179],v[204:207]
ds_read_b64_tr_b16 a[240:241],v2 offset:768
v_mfma_f32_16x16x32_bf16 v[204:207],a[144:147],v[144:147],v[204:207]
ds_read_b64_tr_b16 a[242:243],v2 offset:4864
v_mfma_f32_16x16x32_bf16 v[216:219],a[160:163],v[144:147],v[40:43]
ds_read_b64_tr_b16 a[244:245],v2 offset:8960
ds_read_b64_tr_b16 a[246:247],v2 offset:13056
v_mfma_f32_16x16x32_bf16 v[216:219],a[164:167],v[176:179],v[216:219]
buffer_store_dwordx4 v[76:79],v6,s[36:39],s84 offen offset:3072 sc1
v_mfma_f32_16x16x32_bf16 v[216:219],a[168:171],v[208:211],v[216:219]
s_add_u32 s84,s55,131072
ds_read_b64_tr_b16 a[252:253],v3 offset:8960
ds_read_b64_tr_b16 a[254:255],v3 offset:13056
v_mfma_f32_16x16x32_bf16 v[216:219],a[172:175],v[240:243],v[216:219]
s_max_i32 s84,s84,s18
ds_read_b64_tr_b16 a[248:249],v3 offset:768
v_add_u32 v2,s48,v2
v_mfma_f32_16x16x32_bf16 v[220:223],a[188:191],v[240:243],v[40:43]
ds_read_b64_tr_b16 a[250:251],v3 offset:4864
v_add_u32 v3,s48,v3
v_mfma_f32_16x16x32_bf16 v[220:223],a[184:187],v[208:211],v[220:223]
s_mov_b32 s48,98304
v_exp_f32 v175,v175
v_mfma_f32_16x16x32_bf16 v[220:223],a[180:183],v[176:179],v[220:223]
v_exp_f32 v184,v184
v_mfma_f32_16x16x32_bf16 v[220:223],a[176:179],v[144:147],v[220:223]
s_waitcnt lgkmcnt(6)
v_exp_f32 v185,v185
v_mfma_f32_16x16x32_bf16 a[0:3],a[192:195],v[136:139],a[0:3]
v_cvt_pk_bf16_f32 v171,v174,v175
v_cvt_pk_bf16_f32 v172,v184,v185
v_mfma_f32_16x16x32_bf16 a[0:3],a[196:199],v[140:143],a[0:3]
v_exp_f32 v186,v186
v_mfma_f32_16x16x32_bf16 a[4:7],a[204:207],v[140:143],a[4:7]
v_exp_f32 v187,v187
v_mfma_f32_16x16x32_bf16 a[4:7],a[200:203],v[136:139],a[4:7]
v_exp_f32 v188,v188
v_mfma_f32_16x16x32_bf16 a[8:11],a[208:211],v[136:139],a[8:11]
)A"
R"A(v_exp_f32 v189,v189
v_mfma_f32_16x16x32_bf16 a[8:11],a[212:215],v[140:143],a[8:11]
v_exp_f32 v190,v190
v_mfma_f32_16x16x32_bf16 a[12:15],a[220:223],v[140:143],a[12:15]
v_exp_f32 v191,v191
v_mfma_f32_16x16x32_bf16 a[12:15],a[216:219],v[136:139],a[12:15]
buffer_load_dwordx4 v10,s[72:75],s80 offen offset:3072 lds
v_mfma_f32_16x16x32_bf16 a[16:19],a[224:227],v[136:139],a[16:19]
v_cvt_pk_bf16_f32 v173,v186,v187
v_cvt_pk_bf16_f32 v174,v188,v189
v_mfma_f32_16x16x32_bf16 a[16:19],a[228:231],v[140:143],a[16:19]
v_exp_f32 v200,v200
v_mfma_f32_16x16x32_bf16 a[20:23],a[236:239],v[140:143],a[20:23]
v_exp_f32 v201,v201
v_mfma_f32_16x16x32_bf16 a[20:23],a[232:235],v[136:139],a[20:23]
v_cvt_pk_bf16_f32 v175,v190,v191
v_cvt_pk_bf16_f32 v200,v200,v201
v_mfma_f32_16x16x32_bf16 a[24:27],a[240:243],v[136:139],a[24:27]
s_waitcnt lgkmcnt(0)
v_exp_f32 v202,v202
v_mfma_f32_16x16x32_bf16 a[24:27],a[244:247],v[140:143],a[24:27]
v_exp_f32 v203,v203
v_mfma_f32_16x16x32_bf16 a[28:31],a[252:255],v[140:143],a[28:31]
v_exp_f32 v204,v204
v_mfma_f32_16x16x32_bf16 a[28:31],a[248:251],v[136:139],a[28:31]
buffer_store_dwordx4 v[80:83],v6,s[36:39],s85 offen sc1
v_smfmac_f32_16x16x64_bf16 v[48:51],v[28:31],v[136:143],v27
v_cvt_pk_bf16_f32 v201,v202,v203
v_mfma_f32_16x16x32_bf16 v[232:235],a[128:131],v[148:151],v[44:47]
s_waitcnt vmcnt(10)
v_exp_f32 v205,v205
v_mfma_f32_16x16x32_bf16 v[232:235],a[132:135],v[180:183],v[232:235]
s_barrier
ds_read_b128 a[128:131],v1
v_cvt_pk_bf16_f32 v202,v204,v205
v_mfma_f32_16x16x32_bf16 v[232:235],a[136:139],v[212:215],v[232:235]
v_exp_f32 v206,v206
v_mfma_f32_16x16x32_bf16 v[232:235],a[140:143],v[244:247],v[232:235]
v_exp_f32 v207,v207
v_mfma_f32_16x16x32_bf16 v[236:239],a[156:159],v[244:247],v[44:47]
ds_read_b128 a[132:135],v1 offset:256
v_cvt_pk_bf16_f32 v203,v206,v207
v_mfma_f32_16x16x32_bf16 v[236:239],a[152:155],v[212:215],v[236:239]
v_exp_f32 v216,v216
v_mfma_f32_16x16x32_bf16 v[236:239],a[148:151],v[180:183],v[236:239]
s_add_u32 m0,s82,86016
v_exp_f32 v217,v217
v_mfma_f32_16x16x32_bf16 v[236:239],a[144:147],v[148:151],v[236:239]
ds_read_b128 a[136:139],v1 offset:512
v_cvt_pk_bf16_f32 v204,v216,v217
v_mfma_f32_16x16x32_bf16 v[248:251],a[160:163],v[148:151],v[44:47]
v_exp_f32 v218,v218
v_mfma_f32_16x16x32_bf16 v[248:251],a[164:167],v[180:183],v[248:251]
buffer_load_dwordx4 v11,s[72:75],s80 offen lds
v_mfma_f32_16x16x32_bf16 v[248:251],a[168:171],v[212:215],v[248:251]
ds_read_b128 a[140:143],v1 offset:768
v_mfma_f32_16x16x32_bf16 v[248:251],a[172:175],v[244:247],v[248:251]
v_exp_f32 v219,v219
v_mfma_f32_16x16x32_bf16 v[252:255],a[188:191],v[244:247],v[44:47]
ds_read_b128 a[156:159],v1 offset:4864
v_cvt_pk_bf16_f32 v205,v218,v219
v_mfma_f32_16x16x32_bf16 v[252:255],a[184:187],v[212:215],v[252:255]
v_exp_f32 v220,v220
v_mfma_f32_16x16x32_bf16 v[252:255],a[180:183],v[180:183],v[252:255]
ds_read_b128 a[152:155],v1 offset:4608
v_mfma_f32_16x16x32_bf16 v[252:255],a[176:179],v[148:151],v[252:255]
v_exp_f32 v221,v221
v_mfma_f32_16x16x32_bf16 a[32:35],a[192:195],v[168:171],a[32:35]
ds_read_b128 a[148:151],v1 offset:4352
v_cvt_pk_bf16_f32 v206,v220,v221
v_mfma_f32_16x16x32_bf16 a[32:35],a[196:199],v[172:175],a[32:35]
v_exp_f32 v222,v222
v_mfma_f32_16x16x32_bf16 a[36:39],a[204:207],v[172:175],a[36:39]
ds_read_b128 a[144:147],v1 offset:4096
v_mfma_f32_16x16x32_bf16 a[36:39],a[200:203],v[168:171],a[36:39]
buffer_store_dwordx4 v[84:87],v6,s[36:39],s85 offen offset:1024 sc1
v_mfma_f32_16x16x32_bf16 a[40:43],a[208:211],v[168:171],a[40:43]
ds_read_b128 a[160:163],v1 offset:8192
v_mfma_f32_16x16x32_bf16 a[40:43],a[212:215],v[172:175],a[40:43]
v_exp_f32 v223,v223
v_mfma_f32_16x16x32_bf16 a[44:47],a[220:223],v[172:175],a[44:47]
ds_read_b128 a[164:167],v1 offset:8448
v_cvt_pk_bf16_f32 v207,v222,v223
v_mfma_f32_16x16x32_bf16 a[44:47],a[216:219],v[168:171],a[44:47]
v_exp_f32 v232,v232
v_mfma_f32_16x16x32_bf16 a[48:51],a[224:227],v[168:171],a[48:51]
ds_read_b128 a[168:171],v1 offset:8704
v_mfma_f32_16x16x32_bf16 a[48:51],a[228:231],v[172:175],a[48:51]
v_exp_f32 v233,v233
v_mfma_f32_16x16x32_bf16 a[52:55],a[236:239],v[172:175],a[52:55]
s_add_u32 m0,s82,87040
ds_read_b128 a[172:175],v1 offset:8960
v_cvt_pk_bf16_f32 v232,v232,v233
v_mfma_f32_16x16x32_bf16 a[52:55],a[232:235],v[168:171],a[52:55]
v_exp_f32 v234,v234
v_mfma_f32_16x16x32_bf16 a[56:59],a[240:243],v[168:171],a[56:59]
ds_read_b128 a[188:191],v1 offset:13056
v_mfma_f32_16x16x32_bf16 a[56:59],a[244:247],v[172:175],a[56:59]
buffer_load_dwordx4 v7,s[72:75],s83 offen lds
v_mfma_f32_16x16x32_bf16 a[60:63],a[252:255],v[172:175],a[60:63]
ds_read_b128 a[184:187],v1 offset:12800
v_mfma_f32_16x16x32_bf16 a[60:63],a[248:251],v[168:171],a[60:63]
v_exp_f32 v235,v235
v_smfmac_f32_16x16x64_bf16 v[52:55],v[28:31],v[168:175],v27
s_waitcnt lgkmcnt(3)
ds_read_b128 a[180:183],v1 offset:12544
v_mfma_f32_16x16x32_bf16 v[136:139],a[128:131],v[128:131],v[32:35]
v_exp_f32 v236,v236
v_mfma_f32_16x16x32_bf16 v[136:139],a[132:135],v[160:163],v[136:139]
ds_read_b128 a[176:179],v1 offset:12288
v_cvt_pk_bf16_f32 v233,v234,v235
v_mfma_f32_16x16x32_bf16 v[136:139],a[136:139],v[192:195],v[136:139]
v_exp_f32 v237,v237
v_mfma_f32_16x16x32_bf16 v[136:139],a[140:143],v[224:227],v[136:139]
v_exp_f32 v238,v238
v_mfma_f32_16x16x32_bf16 v[140:143],a[156:159],v[224:227],v[32:35]
v_exp_f32 v239,v239
v_mfma_f32_16x16x32_bf16 v[140:143],a[152:155],v[192:195],v[140:143]
v_cvt_pk_bf16_f32 v234,v236,v237
v_cvt_pk_bf16_f32 v235,v238,v239
v_mfma_f32_16x16x32_bf16 v[140:143],a[148:151],v[160:163],v[140:143]
v_exp_f32 v248,v248
v_mfma_f32_16x16x32_bf16 v[140:143],a[144:147],v[128:131],v[140:143]
v_exp_f32 v249,v249
v_mfma_f32_16x16x32_bf16 v[152:155],a[160:163],v[128:131],v[32:35]
buffer_store_dwordx4 v[88:91],v6,s[36:39],s85 offen offset:2048 sc1
v_mfma_f32_16x16x32_bf16 v[152:155],a[164:167],v[160:163],v[152:155]
v_exp_f32 v250,v250
v_mfma_f32_16x16x32_bf16 v[152:155],a[168:171],v[192:195],v[152:155]
s_waitcnt lgkmcnt(0)
v_exp_f32 v251,v251
v_mfma_f32_16x16x32_bf16 v[152:155],a[172:175],v[224:227],v[152:155]
v_exp_f32 v252,v252
v_mfma_f32_16x16x32_bf16 v[156:159],a[188:191],v[224:227],v[32:35]
v_exp_f32 v253,v253
v_mfma_f32_16x16x32_bf16 v[156:159],a[184:187],v[192:195],v[156:159]
v_exp_f32 v254,v254
v_mfma_f32_16x16x32_bf16 v[156:159],a[180:183],v[160:163],v[156:159]
v_exp_f32 v255,v255
v_mfma_f32_16x16x32_bf16 v[156:159],a[176:179],v[128:131],v[156:159]
v_cvt_pk_bf16_f32 v236,v248,v249
v_cvt_pk_bf16_f32 v237,v250,v251
v_mfma_f32_16x16x32_bf16 a[64:67],a[192:195],v[200:203],a[64:67]
buffer_load_dwordx4 v8,s[72:75],s83 offen offset:1024 lds
v_mfma_f32_16x16x32_bf16 a[64:67],a[196:199],v[204:207],a[64:67]
v_cvt_pk_bf16_f32 v238,v252,v253
v_cvt_pk_bf16_f32 v239,v254,v255
v_mfma_f32_16x16x32_bf16 a[68:71],a[204:207],v[204:207],a[68:71]
v_exp_f32 v136,v136
v_mfma_f32_16x16x32_bf16 a[68:71],a[200:203],v[200:203],a[68:71]
v_exp_f32 v137,v137
v_mfma_f32_16x16x32_bf16 a[72:75],a[208:211],v[200:203],a[72:75]
v_exp_f32 v138,v138
v_mfma_f32_16x16x32_bf16 a[72:75],a[212:215],v[204:207],a[72:75]
v_exp_f32 v139,v139
v_mfma_f32_16x16x32_bf16 a[76:79],a[220:223],v[204:207],a[76:79]
v_cvt_pk_bf16_f32 v136,v136,v137
v_cvt_pk_bf16_f32 v137,v138,v139
v_mfma_f32_16x16x32_bf16 a[76:79],a[216:219],v[200:203],a[76:79]
v_exp_f32 v140,v140
v_mfma_f32_16x16x32_bf16 a[80:83],a[224:227],v[200:203],a[80:83]
v_exp_f32 v141,v141
v_mfma_f32_16x16x32_bf16 a[80:83],a[228:231],v[204:207],a[80:83]
v_exp_f32 v142,v142
v_mfma_f32_16x16x32_bf16 a[84:87],a[236:239],v[204:207],a[84:87]
v_exp_f32 v143,v143
v_mfma_f32_16x16x32_bf16 a[84:87],a[232:235],v[200:203],a[84:87]
v_cvt_pk_bf16_f32 v138,v140,v141
v_cvt_pk_bf16_f32 v139,v142,v143
v_mfma_f32_16x16x32_bf16 a[88:91],a[240:243],v[200:203],a[88:91]
v_exp_f32 v152,v152
v_mfma_f32_16x16x32_bf16 a[88:91],a[244:247],v[204:207],a[88:91]
v_exp_f32 v153,v153
v_mfma_f32_16x16x32_bf16 a[92:95],a[252:255],v[204:207],a[92:95]
buffer_store_dwordx4 v[92:95],v6,s[36:39],s85 offen offset:3072 sc1
v_mfma_f32_16x16x32_bf16 a[92:95],a[248:251],v[200:203],a[92:95]
s_add_u32 s85,s55,135168
v_exp_f32 v154,v154
v_smfmac_f32_16x16x64_bf16 v[56:59],v[28:31],v[200:207],v27
s_max_i32 s85,s85,s19
v_cvt_pk_bf16_f32 v140,v152,v153
v_mfma_f32_16x16x32_bf16 v[168:171],a[128:131],v[132:135],v[36:39]
v_exp_f32 v155,v155
v_mfma_f32_16x16x32_bf16 v[168:171],a[132:135],v[164:167],v[168:171]
v_exp_f32 v156,v156
v_mfma_f32_16x16x32_bf16 v[168:171],a[136:139],v[196:199],v[168:171]
v_exp_f32 v157,v157
v_mfma_f32_16x16x32_bf16 v[168:171],a[140:143],v[228:231],v[168:171]
buffer_load_dwordx4 v9,s[72:75],s83 offen offset:2048 lds
v_mfma_f32_16x16x32_bf16 v[172:175],a[156:159],v[228:231],v[36:39]
v_exp_f32 v158,v158
v_mfma_f32_16x16x32_bf16 v[172:175],a[152:155],v[196:199],v[172:175]
v_exp_f32 v159,v159
v_mfma_f32_16x16x32_bf16 v[172:175],a[148:151],v[164:167],v[172:175]
v_cvt_pk_bf16_f32 v141,v154,v155
v_cvt_pk_bf16_f32 v142,v156,v157
v_mfma_f32_16x16x32_bf16 v[172:175],a[144:147],v[132:135],v[172:175]
v_cvt_pk_bf16_f32 v143,v158,v159
v_add_u32 v1,s48,v1
v_mfma_f32_16x16x32_bf16 v[184:187],a[160:163],v[132:135],v[36:39]
v_mfma_f32_16x16x32_bf16 v[184:187],a[164:167],v[164:167],v[184:187]
v_exp_f32 v168,v168
v_mfma_f32_16x16x32_bf16 v[184:187],a[168:171],v[196:199],v[184:187]
v_exp_f32 v169,v169
v_mfma_f32_16x16x32_bf16 v[184:187],a[172:175],v[228:231],v[184:187]
v_exp_f32 v170,v170
v_mfma_f32_16x16x32_bf16 v[188:191],a[188:191],v[228:231],v[36:39]
v_exp_f32 v171,v171
v_mfma_f32_16x16x32_bf16 v[188:191],a[184:187],v[196:199],v[188:191]
v_exp_f32 v172,v172
v_mfma_f32_16x16x32_bf16 v[188:191],a[180:183],v[164:167],v[188:191]
v_exp_f32 v173,v173
v_mfma_f32_16x16x32_bf16 v[188:191],a[176:179],v[132:135],v[188:191]
v_exp_f32 v174,v174
v_mfma_f32_16x16x32_bf16 a[96:99],a[192:195],v[232:235],a[96:99]
ds_read_b64_tr_b16 a[192:193],v2
ds_read_b64_tr_b16 a[194:195],v2 offset:4096
v_mfma_f32_16x16x32_bf16 a[96:99],a[196:199],v[236:239],a[96:99]
ds_read_b64_tr_b16 a[196:197],v2 offset:8192
v_cvt_pk_bf16_f32 v168,v168,v169
v_mfma_f32_16x16x32_bf16 a[100:103],a[204:207],v[236:239],a[100:103]
ds_read_b64_tr_b16 a[198:199],v2 offset:12288
v_cvt_pk_bf16_f32 v169,v170,v171
v_mfma_f32_16x16x32_bf16 a[100:103],a[200:203],v[232:235],a[100:103]
buffer_store_dwordx4 v[96:99],v6,s[36:39],s86 offen sc1
v_mfma_f32_16x16x32_bf16 a[104:107],a[208:211],v[232:235],a[104:107]
ds_read_b64_tr_b16 a[204:205],v3 offset:8192
ds_read_b64_tr_b16 a[206:207],v3 offset:12288
v_mfma_f32_16x16x32_bf16 a[104:107],a[212:215],v[236:239],a[104:107]
ds_read_b64_tr_b16 a[200:201],v3
v_cvt_pk_bf16_f32 v170,v172,v173
v_mfma_f32_16x16x32_bf16 a[108:111],a[220:223],v[236:239],a[108:111]
v_exp_f32 v175,v175
v_mfma_f32_16x16x32_bf16 a[108:111],a[216:219],v[232:235],a[108:111]
buffer_load_dwordx4 v10,s[72:75],s83 offen offset:3072 lds
v_mfma_f32_16x16x32_bf16 a[112:115],a[224:227],v[232:235],a[112:115]
ds_read_b64_tr_b16 a[202:203],v3 offset:4096
ds_read_b64_tr_b16 a[208:209],v2 offset:256
v_mfma_f32_16x16x32_bf16 a[112:115],a[228:231],v[236:239],a[112:115]
ds_read_b64_tr_b16 a[210:211],v2 offset:4352
v_cvt_pk_bf16_f32 v171,v174,v175
v_mfma_f32_16x16x32_bf16 a[116:119],a[236:239],v[236:239],a[116:119]
v_exp_f32 v184,v184
v_mfma_f32_16x16x32_bf16 a[116:119],a[232:235],v[232:235],a[116:119]
ds_read_b64_tr_b16 a[212:213],v2 offset:8448
ds_read_b64_tr_b16 a[214:215],v2 offset:12544
v_mfma_f32_16x16x32_bf16 a[120:123],a[240:243],v[232:235],a[120:123]
v_exp_f32 v185,v185
v_mfma_f32_16x16x32_bf16 a[120:123],a[244:247],v[236:239],a[120:123]
ds_read_b64_tr_b16 a[220:221],v3 offset:8448
ds_read_b64_tr_b16 a[222:223],v3 offset:12544
v_mfma_f32_16x16x32_bf16 a[124:127],a[252:255],v[236:239],a[124:127]
ds_read_b64_tr_b16 a[216:217],v3 offset:256
v_cvt_pk_bf16_f32 v172,v184,v185
v_mfma_f32_16x16x32_bf16 a[124:127],a[248:251],v[232:235],a[124:127]
ds_read_b64_tr_b16 a[218:219],v3 offset:4352
v_smfmac_f32_16x16x64_bf16 v[60:63],v[28:31],v[232:239],v27
ds_read_b64_tr_b16 a[224:225],v2 offset:512
v_mfma_f32_16x16x32_bf16 v[200:203],a[128:131],v[144:147],v[40:43]
ds_read_b64_tr_b16 a[226:227],v2 offset:4608
)A"
R"A(ds_read_b64_tr_b16 a[228:229],v2 offset:8704
v_mfma_f32_16x16x32_bf16 v[200:203],a[132:135],v[176:179],v[200:203]
ds_read_b64_tr_b16 a[230:231],v2 offset:12800
v_mfma_f32_16x16x32_bf16 v[200:203],a[136:139],v[208:211],v[200:203]
ds_read_b64_tr_b16 a[236:237],v3 offset:8704
v_mfma_f32_16x16x32_bf16 v[200:203],a[140:143],v[240:243],v[200:203]
ds_read_b64_tr_b16 a[238:239],v3 offset:12800
v_mfma_f32_16x16x32_bf16 v[204:207],a[156:159],v[240:243],v[40:43]
ds_read_b64_tr_b16 a[232:233],v3 offset:512
ds_read_b64_tr_b16 a[234:235],v3 offset:4608
v_mfma_f32_16x16x32_bf16 v[204:207],a[152:155],v[208:211],v[204:207]
ds_read_b64_tr_b16 a[240:241],v2 offset:768
v_mfma_f32_16x16x32_bf16 v[204:207],a[148:151],v[176:179],v[204:207]
ds_read_b64_tr_b16 a[242:243],v2 offset:4864
v_mfma_f32_16x16x32_bf16 v[204:207],a[144:147],v[144:147],v[204:207]
s_add_u32 m0,s82,91136
ds_read_b64_tr_b16 a[244:245],v2 offset:8960
v_mfma_f32_16x16x32_bf16 v[216:219],a[160:163],v[144:147],v[40:43]
buffer_store_dwordx4 v[100:103],v6,s[36:39],s86 offen offset:1024 sc1
v_mfma_f32_16x16x32_bf16 v[216:219],a[164:167],v[176:179],v[216:219]
ds_read_b64_tr_b16 a[246:247],v2 offset:13056
ds_read_b64_tr_b16 a[252:253],v3 offset:8960
v_mfma_f32_16x16x32_bf16 v[216:219],a[168:171],v[208:211],v[216:219]
ds_read_b64_tr_b16 a[254:255],v3 offset:13056
v_add_u32 v2,s48,v2
v_mfma_f32_16x16x32_bf16 v[216:219],a[172:175],v[240:243],v[216:219]
ds_read_b64_tr_b16 a[248:249],v3 offset:768
v_mfma_f32_16x16x32_bf16 v[220:223],a[188:191],v[240:243],v[40:43]
buffer_load_dwordx4 v11,s[72:75],s83 offen lds
v_mfma_f32_16x16x32_bf16 v[220:223],a[184:187],v[208:211],v[220:223]
ds_read_b64_tr_b16 a[250:251],v3 offset:4864
v_add_u32 v3,s48,v3
v_mfma_f32_16x16x32_bf16 v[220:223],a[180:183],v[176:179],v[220:223]
v_exp_f32 v186,v186
v_mfma_f32_16x16x32_bf16 v[220:223],a[176:179],v[144:147],v[220:223]
s_waitcnt lgkmcnt(5)
v_exp_f32 v187,v187
v_mfma_f32_16x16x32_bf16 a[0:3],a[192:195],v[136:139],a[0:3]
v_exp_f32 v188,v188
v_mfma_f32_16x16x32_bf16 a[0:3],a[196:199],v[140:143],a[0:3]
s_add_u32 s80,s71,0
s_and_b32 s80,s80,3
s_lshl_b32 s80,s80,1
v_mfma_f32_16x16x32_bf16 a[4:7],a[204:207],v[140:143],a[4:7]
s_or_b32 s80,s80,1
s_mul_i32 s80,s80,163840
s_add_u32 s80,s80,s81
v_mfma_f32_16x16x32_bf16 a[4:7],a[200:203],v[136:139],a[4:7]
s_add_u32 s83,s80,20480
v_exp_f32 v189,v189
v_mfma_f32_16x16x32_bf16 a[8:11],a[208:211],v[136:139],a[8:11]
v_exp_f32 v190,v190
v_mfma_f32_16x16x32_bf16 a[8:11],a[212:215],v[140:143],a[8:11]
v_exp_f32 v191,v191
v_mfma_f32_16x16x32_bf16 a[12:15],a[220:223],v[140:143],a[12:15]
v_cvt_pk_bf16_f32 v173,v186,v187
v_cvt_pk_bf16_f32 v174,v188,v189
v_mfma_f32_16x16x32_bf16 a[12:15],a[216:219],v[136:139],a[12:15]
v_exp_f32 v200,v200
v_mfma_f32_16x16x32_bf16 a[16:19],a[224:227],v[136:139],a[16:19]
v_exp_f32 v201,v201
v_mfma_f32_16x16x32_bf16 a[16:19],a[228:231],v[140:143],a[16:19]
v_cvt_pk_bf16_f32 v175,v190,v191
v_cvt_pk_bf16_f32 v200,v200,v201
v_mfma_f32_16x16x32_bf16 a[20:23],a[236:239],v[140:143],a[20:23]
v_exp_f32 v202,v202
v_mfma_f32_16x16x32_bf16 a[20:23],a[232:235],v[136:139],a[20:23]
v_exp_f32 v203,v203
v_mfma_f32_16x16x32_bf16 a[24:27],a[240:243],v[136:139],a[24:27]
s_waitcnt lgkmcnt(0)
v_exp_f32 v204,v204
v_mfma_f32_16x16x32_bf16 a[24:27],a[244:247],v[140:143],a[24:27]
v_exp_f32 v205,v205
v_mfma_f32_16x16x32_bf16 a[28:31],a[252:255],v[140:143],a[28:31]
buffer_store_dwordx4 v[104:107],v6,s[36:39],s86 offen offset:2048 sc1
v_mfma_f32_16x16x32_bf16 a[28:31],a[248:251],v[136:139],a[28:31]
v_exp_f32 v206,v206
v_smfmac_f32_16x16x64_bf16 v[48:51],v[28:31],v[136:143],v27
v_cvt_pk_bf16_f32 v201,v202,v203
v_mfma_f32_16x16x32_bf16 v[232:235],a[128:131],v[148:151],v[44:47]
v_exp_f32 v207,v207
v_mfma_f32_16x16x32_bf16 v[232:235],a[132:135],v[180:183],v[232:235]
s_barrier
ds_read_b128 a[128:131],v1
v_cvt_pk_bf16_f32 v202,v204,v205
v_mfma_f32_16x16x32_bf16 v[232:235],a[136:139],v[212:215],v[232:235]
v_exp_f32 v216,v216
v_mfma_f32_16x16x32_bf16 v[232:235],a[140:143],v[244:247],v[232:235]
ds_read_b128 a[132:135],v1 offset:256
v_cvt_pk_bf16_f32 v203,v206,v207
v_mfma_f32_16x16x32_bf16 v[236:239],a[156:159],v[244:247],v[44:47]
s_add_u32 m0,s82,0
v_exp_f32 v217,v217
v_mfma_f32_16x16x32_bf16 v[236:239],a[152:155],v[212:215],v[236:239]
ds_read_b128 a[136:139],v1 offset:512
v_cvt_pk_bf16_f32 v204,v216,v217
v_mfma_f32_16x16x32_bf16 v[236:239],a[148:151],v[180:183],v[236:239]
v_exp_f32 v218,v218
v_mfma_f32_16x16x32_bf16 v[236:239],a[144:147],v[148:151],v[236:239]
buffer_load_dwordx4 v7,s[72:75],s80 offen lds
v_mfma_f32_16x16x32_bf16 v[248:251],a[160:163],v[148:151],v[44:47]
v_exp_f32 v219,v219
v_mfma_f32_16x16x32_bf16 v[248:251],a[164:167],v[180:183],v[248:251]
ds_read_b128 a[140:143],v1 offset:768
v_cvt_pk_bf16_f32 v205,v218,v219
v_mfma_f32_16x16x32_bf16 v[248:251],a[168:171],v[212:215],v[248:251]
v_exp_f32 v220,v220
v_mfma_f32_16x16x32_bf16 v[248:251],a[172:175],v[244:247],v[248:251]
ds_read_b128 a[156:159],v1 offset:4864
v_mfma_f32_16x16x32_bf16 v[252:255],a[188:191],v[244:247],v[44:47]
v_exp_f32 v221,v221
v_mfma_f32_16x16x32_bf16 v[252:255],a[184:187],v[212:215],v[252:255]
ds_read_b128 a[152:155],v1 offset:4608
v_cvt_pk_bf16_f32 v206,v220,v221
v_mfma_f32_16x16x32_bf16 v[252:255],a[180:183],v[180:183],v[252:255]
v_exp_f32 v222,v222
v_mfma_f32_16x16x32_bf16 v[252:255],a[176:179],v[148:151],v[252:255]
ds_read_b128 a[148:151],v1 offset:4352
v_mfma_f32_16x16x32_bf16 a[32:35],a[192:195],v[168:171],a[32:35]
v_exp_f32 v223,v223
v_mfma_f32_16x16x32_bf16 a[32:35],a[196:199],v[172:175],a[32:35]
ds_read_b128 a[144:147],v1 offset:4096
v_cvt_pk_bf16_f32 v207,v222,v223
v_mfma_f32_16x16x32_bf16 a[36:39],a[204:207],v[172:175],a[36:39]
buffer_store_dwordx4 v[108:111],v6,s[36:39],s86 offen offset:3072 sc1
v_mfma_f32_16x16x32_bf16 a[36:39],a[200:203],v[168:171],a[36:39]
s_add_u32 s86,s55,147456
s_max_i32 s86,s86,s100
ds_read_b128 a[160:163],v1 offset:8192
v_mfma_f32_16x16x32_bf16 a[40:43],a[208:211],v[168:171],a[40:43]
v_exp_f32 v232,v232
v_mfma_f32_16x16x32_bf16 a[40:43],a[212:215],v[172:175],a[40:43]
ds_read_b128 a[164:167],v1 offset:8448
v_mfma_f32_16x16x32_bf16 a[44:47],a[220:223],v[172:175],a[44:47]
buffer_load_dwordx4 v8,s[72:75],s80 offen offset:1024 lds
v_mfma_f32_16x16x32_bf16 a[44:47],a[216:219],v[168:171],a[44:47]
ds_read_b128 a[168:171],v1 offset:8704
v_mfma_f32_16x16x32_bf16 a[48:51],a[224:227],v[168:171],a[48:51]
v_exp_f32 v233,v233
v_mfma_f32_16x16x32_bf16 a[48:51],a[228:231],v[172:175],a[48:51]
ds_read_b128 a[172:175],v1 offset:8960
v_cvt_pk_bf16_f32 v232,v232,v233
v_mfma_f32_16x16x32_bf16 a[52:55],a[236:239],v[172:175],a[52:55]
v_exp_f32 v234,v234
v_mfma_f32_16x16x32_bf16 a[52:55],a[232:235],v[168:171],a[52:55]
ds_read_b128 a[188:191],v1 offset:13056
v_mfma_f32_16x16x32_bf16 a[56:59],a[240:243],v[168:171],a[56:59]
v_exp_f32 v235,v235
v_mfma_f32_16x16x32_bf16 a[56:59],a[244:247],v[172:175],a[56:59]
buffer_load_dwordx4 v9,s[72:75],s80 offen offset:2048 lds
v_mfma_f32_16x16x32_bf16 a[60:63],a[252:255],v[172:175],a[60:63]
ds_read_b128 a[184:187],v1 offset:12800
v_cvt_pk_bf16_f32 v233,v234,v235
v_mfma_f32_16x16x32_bf16 a[60:63],a[248:251],v[168:171],a[60:63]
v_exp_f32 v236,v236
v_smfmac_f32_16x16x64_bf16 v[52:55],v[28:31],v[168:175],v27
s_waitcnt lgkmcnt(3)
ds_read_b128 a[180:183],v1 offset:12544
v_mfma_f32_16x16x32_bf16 v[136:139],a[128:131],v[128:131],v[32:35]
v_exp_f32 v237,v237
v_mfma_f32_16x16x32_bf16 v[136:139],a[132:135],v[160:163],v[136:139]
ds_read_b128 a[176:179],v1 offset:12288
v_cvt_pk_bf16_f32 v234,v236,v237
v_mfma_f32_16x16x32_bf16 v[136:139],a[136:139],v[192:195],v[136:139]
v_exp_f32 v238,v238
v_mfma_f32_16x16x32_bf16 v[136:139],a[140:143],v[224:227],v[136:139]
v_exp_f32 v239,v239
v_mfma_f32_16x16x32_bf16 v[140:143],a[156:159],v[224:227],v[32:35]
v_cvt_pk_bf16_f32 v235,v238,v239
v_add_u32 v1,v4,v5
v_mfma_f32_16x16x32_bf16 v[140:143],a[152:155],v[192:195],v[140:143]
v_exp_f32 v248,v248
v_mfma_f32_16x16x32_bf16 v[140:143],a[148:151],v[160:163],v[140:143]
v_exp_f32 v249,v249
v_mfma_f32_16x16x32_bf16 v[140:143],a[144:147],v[128:131],v[140:143]
buffer_store_dwordx4 v[112:115],v6,s[36:39],s87 offen sc1
v_mfma_f32_16x16x32_bf16 v[152:155],a[160:163],v[128:131],v[32:35]
v_exp_f32 v250,v250
v_mfma_f32_16x16x32_bf16 v[152:155],a[164:167],v[160:163],v[152:155]
v_exp_f32 v251,v251
v_mfma_f32_16x16x32_bf16 v[152:155],a[168:171],v[192:195],v[152:155]
s_waitcnt lgkmcnt(0)
v_exp_f32 v252,v252
v_mfma_f32_16x16x32_bf16 v[152:155],a[172:175],v[224:227],v[152:155]
buffer_load_dwordx4 v10,s[72:75],s80 offen offset:3072 lds
v_mfma_f32_16x16x32_bf16 v[156:159],a[188:191],v[224:227],v[32:35]
v_exp_f32 v253,v253
v_mfma_f32_16x16x32_bf16 v[156:159],a[184:187],v[192:195],v[156:159]
v_exp_f32 v254,v254
v_mfma_f32_16x16x32_bf16 v[156:159],a[180:183],v[160:163],v[156:159]
v_exp_f32 v255,v255
v_mfma_f32_16x16x32_bf16 v[156:159],a[176:179],v[128:131],v[156:159]
s_add_u32 m0,s82,4096
v_cvt_pk_bf16_f32 v236,v248,v249
v_cvt_pk_bf16_f32 v237,v250,v251
v_mfma_f32_16x16x32_bf16 a[64:67],a[192:195],v[200:203],a[64:67]
v_cvt_pk_bf16_f32 v238,v252,v253
v_cvt_pk_bf16_f32 v239,v254,v255
v_mfma_f32_16x16x32_bf16 a[64:67],a[196:199],v[204:207],a[64:67]
v_exp_f32 v136,v136
v_mfma_f32_16x16x32_bf16 a[68:71],a[204:207],v[204:207],a[68:71]
buffer_load_dwordx4 v11,s[72:75],s80 offen lds
v_mfma_f32_16x16x32_bf16 a[68:71],a[200:203],v[200:203],a[68:71]
v_exp_f32 v137,v137
v_mfma_f32_16x16x32_bf16 a[72:75],a[208:211],v[200:203],a[72:75]
v_exp_f32 v138,v138
v_mfma_f32_16x16x32_bf16 a[72:75],a[212:215],v[204:207],a[72:75]
v_exp_f32 v139,v139
v_mfma_f32_16x16x32_bf16 a[76:79],a[220:223],v[204:207],a[76:79]
v_cvt_pk_bf16_f32 v136,v136,v137
v_cvt_pk_bf16_f32 v137,v138,v139
v_mfma_f32_16x16x32_bf16 a[76:79],a[216:219],v[200:203],a[76:79]
v_exp_f32 v140,v140
v_mfma_f32_16x16x32_bf16 a[80:83],a[224:227],v[200:203],a[80:83]
v_exp_f32 v141,v141
v_mfma_f32_16x16x32_bf16 a[80:83],a[228:231],v[204:207],a[80:83]
v_exp_f32 v142,v142
v_mfma_f32_16x16x32_bf16 a[84:87],a[236:239],v[204:207],a[84:87]
s_add_u32 m0,s82,5120
v_exp_f32 v143,v143
v_mfma_f32_16x16x32_bf16 a[84:87],a[232:235],v[200:203],a[84:87]
v_cvt_pk_bf16_f32 v138,v140,v141
v_cvt_pk_bf16_f32 v139,v142,v143
v_mfma_f32_16x16x32_bf16 a[88:91],a[240:243],v[200:203],a[88:91]
v_exp_f32 v152,v152
v_mfma_f32_16x16x32_bf16 a[88:91],a[244:247],v[204:207],a[88:91]
buffer_store_dwordx4 v[116:119],v6,s[36:39],s87 offen offset:1024 sc1
v_mfma_f32_16x16x32_bf16 a[92:95],a[252:255],v[204:207],a[92:95]
v_exp_f32 v153,v153
v_mfma_f32_16x16x32_bf16 a[92:95],a[248:251],v[200:203],a[92:95]
v_exp_f32 v154,v154
v_smfmac_f32_16x16x64_bf16 v[56:59],v[28:31],v[200:207],v27
v_cvt_pk_bf16_f32 v140,v152,v153
v_mfma_f32_16x16x32_bf16 v[168:171],a[128:131],v[132:135],v[36:39]
buffer_load_dwordx4 v7,s[72:75],s83 offen lds
v_mfma_f32_16x16x32_bf16 v[168:171],a[132:135],v[164:167],v[168:171]
v_exp_f32 v155,v155
v_mfma_f32_16x16x32_bf16 v[168:171],a[136:139],v[196:199],v[168:171]
v_exp_f32 v156,v156
v_mfma_f32_16x16x32_bf16 v[168:171],a[140:143],v[228:231],v[168:171]
v_exp_f32 v157,v157
v_mfma_f32_16x16x32_bf16 v[172:175],a[156:159],v[228:231],v[36:39]
v_exp_f32 v158,v158
v_mfma_f32_16x16x32_bf16 v[172:175],a[152:155],v[196:199],v[172:175]
v_exp_f32 v159,v159
v_mfma_f32_16x16x32_bf16 v[172:175],a[148:151],v[164:167],v[172:175]
v_cvt_pk_bf16_f32 v141,v154,v155
v_cvt_pk_bf16_f32 v142,v156,v157
v_mfma_f32_16x16x32_bf16 v[172:175],a[144:147],v[132:135],v[172:175]
buffer_load_dwordx4 v8,s[72:75],s83 offen offset:1024 lds
v_mfma_f32_16x16x32_bf16 v[184:187],a[160:163],v[132:135],v[36:39]
v_cvt_pk_bf16_f32 v143,v158,v159
v_add_u32 v1,81920,v1
v_mfma_f32_16x16x32_bf16 v[184:187],a[164:167],v[164:167],v[184:187]
v_exp_f32 v168,v168
v_mfma_f32_16x16x32_bf16 v[184:187],a[168:171],v[196:199],v[184:187]
v_exp_f32 v169,v169
v_mfma_f32_16x16x32_bf16 v[184:187],a[172:175],v[228:231],v[184:187]
v_exp_f32 v170,v170
v_mfma_f32_16x16x32_bf16 v[188:191],a[188:191],v[228:231],v[36:39]
v_exp_f32 v171,v171
v_mfma_f32_16x16x32_bf16 v[188:191],a[184:187],v[196:199],v[188:191]
v_exp_f32 v172,v172
v_mfma_f32_16x16x32_bf16 v[188:191],a[180:183],v[164:167],v[188:191]
)A"
R"A(v_exp_f32 v173,v173
v_mfma_f32_16x16x32_bf16 v[188:191],a[176:179],v[132:135],v[188:191]
v_exp_f32 v174,v174
v_mfma_f32_16x16x32_bf16 a[96:99],a[192:195],v[232:235],a[96:99]
ds_read_b64_tr_b16 a[192:193],v2
ds_read_b64_tr_b16 a[194:195],v2 offset:4096
v_mfma_f32_16x16x32_bf16 a[96:99],a[196:199],v[236:239],a[96:99]
ds_read_b64_tr_b16 a[196:197],v2 offset:8192
v_cvt_pk_bf16_f32 v168,v168,v169
v_mfma_f32_16x16x32_bf16 a[100:103],a[204:207],v[236:239],a[100:103]
buffer_store_dwordx4 v[120:123],v6,s[36:39],s87 offen offset:2048 sc1
v_mfma_f32_16x16x32_bf16 a[100:103],a[200:203],v[232:235],a[100:103]
ds_read_b64_tr_b16 a[198:199],v2 offset:12288
ds_read_b64_tr_b16 a[204:205],v3 offset:8192
v_mfma_f32_16x16x32_bf16 a[104:107],a[208:211],v[232:235],a[104:107]
ds_read_b64_tr_b16 a[206:207],v3 offset:12288
v_cvt_pk_bf16_f32 v169,v170,v171
v_mfma_f32_16x16x32_bf16 a[104:107],a[212:215],v[236:239],a[104:107]
ds_read_b64_tr_b16 a[200:201],v3
v_cvt_pk_bf16_f32 v170,v172,v173
v_mfma_f32_16x16x32_bf16 a[108:111],a[220:223],v[236:239],a[108:111]
buffer_load_dwordx4 v9,s[72:75],s83 offen offset:2048 lds
v_mfma_f32_16x16x32_bf16 a[108:111],a[216:219],v[232:235],a[108:111]
ds_read_b64_tr_b16 a[202:203],v3 offset:4096
ds_read_b64_tr_b16 a[208:209],v2 offset:256
v_mfma_f32_16x16x32_bf16 a[112:115],a[224:227],v[232:235],a[112:115]
v_exp_f32 v175,v175
v_mfma_f32_16x16x32_bf16 a[112:115],a[228:231],v[236:239],a[112:115]
ds_read_b64_tr_b16 a[210:211],v2 offset:4352
ds_read_b64_tr_b16 a[212:213],v2 offset:8448
v_mfma_f32_16x16x32_bf16 a[116:119],a[236:239],v[236:239],a[116:119]
ds_read_b64_tr_b16 a[214:215],v2 offset:12544
v_cvt_pk_bf16_f32 v171,v174,v175
v_mfma_f32_16x16x32_bf16 a[116:119],a[232:235],v[232:235],a[116:119]
s_cmp_ge_u32 0,s71
s_cselect_b32 s80,2,0
ds_read_b64_tr_b16 a[220:221],v3 offset:8448
v_mfma_f32_16x16x32_bf16 a[120:123],a[240:243],v[232:235],a[120:123]
s_mul_i32 s80,s80,163840
v_exp_f32 v184,v184
v_mfma_f32_16x16x32_bf16 a[120:123],a[244:247],v[236:239],a[120:123]
s_add_u32 s80,s80,s81
buffer_load_dwordx4 v10,s[72:75],s83 offen offset:3072 lds
v_mfma_f32_16x16x32_bf16 a[124:127],a[252:255],v[236:239],a[124:127]
ds_read_b64_tr_b16 a[222:223],v3 offset:12544
ds_read_b64_tr_b16 a[216:217],v3 offset:256
v_mfma_f32_16x16x32_bf16 a[124:127],a[248:251],v[232:235],a[124:127]
ds_read_b64_tr_b16 a[218:219],v3 offset:4352
v_smfmac_f32_16x16x64_bf16 v[60:63],v[28:31],v[232:239],v27
ds_read_b64_tr_b16 a[224:225],v2 offset:512
v_mfma_f32_16x16x32_bf16 v[200:203],a[128:131],v[144:147],v[40:43]
ds_read_b64_tr_b16 a[226:227],v2 offset:4608
v_mfma_f32_16x16x32_bf16 v[200:203],a[132:135],v[176:179],v[200:203]
ds_read_b64_tr_b16 a[228:229],v2 offset:8704
ds_read_b64_tr_b16 a[230:231],v2 offset:12800
v_mfma_f32_16x16x32_bf16 v[200:203],a[136:139],v[208:211],v[200:203]
ds_read_b64_tr_b16 a[236:237],v3 offset:8704
v_mfma_f32_16x16x32_bf16 v[200:203],a[140:143],v[240:243],v[200:203]
ds_read_b64_tr_b16 a[238:239],v3 offset:12800
v_mfma_f32_16x16x32_bf16 v[204:207],a[156:159],v[240:243],v[40:43]
s_add_u32 m0,s82,9216
ds_read_b64_tr_b16 a[232:233],v3 offset:512
v_mfma_f32_16x16x32_bf16 v[204:207],a[152:155],v[208:211],v[204:207]
ds_read_b64_tr_b16 a[234:235],v3 offset:4608
ds_read_b64_tr_b16 a[240:241],v2 offset:768
v_mfma_f32_16x16x32_bf16 v[204:207],a[148:151],v[176:179],v[204:207]
ds_read_b64_tr_b16 a[242:243],v2 offset:4864
v_mfma_f32_16x16x32_bf16 v[204:207],a[144:147],v[144:147],v[204:207]
buffer_store_dwordx4 v[124:127],v6,s[36:39],s87 offen offset:3072 sc1
v_mfma_f32_16x16x32_bf16 v[216:219],a[160:163],v[144:147],v[40:43]
s_add_u32 s87,s55,151552
ds_read_b64_tr_b16 a[244:245],v2 offset:8960
ds_read_b64_tr_b16 a[246:247],v2 offset:13056
v_mfma_f32_16x16x32_bf16 v[216:219],a[164:167],v[176:179],v[216:219]
s_max_i32 s87,s87,s101
ds_read_b64_tr_b16 a[252:253],v3 offset:8960
v_sub_u32 v2,v4,v5
v_mfma_f32_16x16x32_bf16 v[216:219],a[168:171],v[208:211],v[216:219]
ds_read_b64_tr_b16 a[254:255],v3 offset:13056
v_add_u32 v2,81920,v2
v_mfma_f32_16x16x32_bf16 v[216:219],a[172:175],v[240:243],v[216:219]
buffer_load_dwordx4 v11,s[72:75],s83 offen lds
v_mfma_f32_16x16x32_bf16 v[220:223],a[188:191],v[240:243],v[40:43]
s_add_u32 s83,s80,20480
ds_read_b64_tr_b16 a[248:249],v3 offset:768
ds_read_b64_tr_b16 a[250:251],v3 offset:4864
v_mfma_f32_16x16x32_bf16 v[220:223],a[184:187],v[208:211],v[220:223]
v_exp_f32 v185,v185
v_mfma_f32_16x16x32_bf16 v[220:223],a[180:183],v[176:179],v[220:223]
v_exp_f32 v186,v186
v_mfma_f32_16x16x32_bf16 v[220:223],a[176:179],v[144:147],v[220:223]
s_add_u32 m0,s82,40960
v_exp_f32 v187,v187
s_waitcnt lgkmcnt(6)
v_mfma_f32_16x16x32_bf16 a[0:3],a[192:195],v[136:139],a[0:3]
v_exp_f32 v188,v188
v_mfma_f32_16x16x32_bf16 a[0:3],a[196:199],v[140:143],a[0:3]
v_exp_f32 v189,v189
v_mfma_f32_16x16x32_bf16 a[4:7],a[204:207],v[140:143],a[4:7]
buffer_load_dwordx4 v7,s[72:75],s80 offen lds
v_mfma_f32_16x16x32_bf16 a[4:7],a[200:203],v[136:139],a[4:7]
v_exp_f32 v190,v190
v_mfma_f32_16x16x32_bf16 a[8:11],a[208:211],v[136:139],a[8:11]
v_exp_f32 v191,v191
v_mfma_f32_16x16x32_bf16 a[8:11],a[212:215],v[140:143],a[8:11]
v_cvt_pk_bf16_f32 v172,v184,v185
v_cvt_pk_bf16_f32 v173,v186,v187
v_mfma_f32_16x16x32_bf16 a[12:15],a[220:223],v[140:143],a[12:15]
v_cvt_pk_bf16_f32 v174,v188,v189
v_cvt_pk_bf16_f32 v175,v190,v191
v_mfma_f32_16x16x32_bf16 a[12:15],a[216:219],v[136:139],a[12:15]
v_exp_f32 v200,v200
v_mfma_f32_16x16x32_bf16 a[16:19],a[224:227],v[136:139],a[16:19]
v_exp_f32 v201,v201
v_mfma_f32_16x16x32_bf16 a[16:19],a[228:231],v[140:143],a[16:19]
v_exp_f32 v202,v202
v_mfma_f32_16x16x32_bf16 a[20:23],a[236:239],v[140:143],a[20:23]
v_exp_f32 v203,v203
v_mfma_f32_16x16x32_bf16 a[20:23],a[232:235],v[136:139],a[20:23]
v_exp_f32 v204,v204
v_mfma_f32_16x16x32_bf16 a[24:27],a[240:243],v[136:139],a[24:27]
s_waitcnt lgkmcnt(0)
v_exp_f32 v205,v205
v_mfma_f32_16x16x32_bf16 a[24:27],a[244:247],v[140:143],a[24:27]
buffer_load_dwordx4 v8,s[72:75],s80 offen offset:1024 lds
v_mfma_f32_16x16x32_bf16 a[28:31],a[252:255],v[140:143],a[28:31]
v_exp_f32 v206,v206
v_mfma_f32_16x16x32_bf16 a[28:31],a[248:251],v[136:139],a[28:31]
s_waitcnt vmcnt(18)
v_cvt_pk_bf16_f32 v200,v200,v201
v_cvt_pk_bf16_f32 v201,v202,v203
v_smfmac_f32_16x16x64_bf16 v[48:51],v[28:31],v[136:143],v27
s_barrier
v_exp_f32 v207,v207
v_mfma_f32_16x16x32_bf16 v[232:235],a[128:131],v[148:151],v[44:47]
v_cvt_pk_bf16_f32 v202,v204,v205
v_cvt_pk_bf16_f32 v203,v206,v207
v_mfma_f32_16x16x32_bf16 v[232:235],a[132:135],v[180:183],v[232:235]
v_exp_f32 v216,v216
v_mfma_f32_16x16x32_bf16 v[232:235],a[136:139],v[212:215],v[232:235]
v_exp_f32 v217,v217
v_mfma_f32_16x16x32_bf16 v[232:235],a[140:143],v[244:247],v[232:235]
v_exp_f32 v218,v218
v_mfma_f32_16x16x32_bf16 v[236:239],a[156:159],v[244:247],v[44:47]
v_exp_f32 v219,v219
v_mfma_f32_16x16x32_bf16 v[236:239],a[152:155],v[212:215],v[236:239]
v_exp_f32 v220,v220
v_mfma_f32_16x16x32_bf16 v[236:239],a[148:151],v[180:183],v[236:239]
buffer_load_dwordx4 v9,s[72:75],s80 offen offset:2048 lds
v_mfma_f32_16x16x32_bf16 v[236:239],a[144:147],v[148:151],v[236:239]
v_exp_f32 v221,v221
v_mfma_f32_16x16x32_bf16 v[248:251],a[160:163],v[148:151],v[44:47]
v_exp_f32 v222,v222
v_mfma_f32_16x16x32_bf16 v[248:251],a[164:167],v[180:183],v[248:251]
v_exp_f32 v223,v223
v_mfma_f32_16x16x32_bf16 v[248:251],a[168:171],v[212:215],v[248:251]
v_cvt_pk_bf16_f32 v204,v216,v217
v_cvt_pk_bf16_f32 v205,v218,v219
v_mfma_f32_16x16x32_bf16 v[248:251],a[172:175],v[244:247],v[248:251]
v_cvt_pk_bf16_f32 v206,v220,v221
v_cvt_pk_bf16_f32 v207,v222,v223
v_mfma_f32_16x16x32_bf16 v[252:255],a[188:191],v[244:247],v[44:47]
v_exp_f32 v232,v232
v_mfma_f32_16x16x32_bf16 v[252:255],a[184:187],v[212:215],v[252:255]
v_exp_f32 v233,v233
v_mfma_f32_16x16x32_bf16 v[252:255],a[180:183],v[180:183],v[252:255]
v_exp_f32 v234,v234
v_mfma_f32_16x16x32_bf16 v[252:255],a[176:179],v[148:151],v[252:255]
v_exp_f32 v235,v235
v_mfma_f32_16x16x32_bf16 a[32:35],a[192:195],v[168:171],a[32:35]
v_cvt_pk_bf16_f32 v232,v232,v233
v_add_f32 v48,v49,v48
v_mfma_f32_16x16x32_bf16 a[32:35],a[196:199],v[172:175],a[32:35]
buffer_load_dwordx4 v10,s[72:75],s80 offen offset:3072 lds
v_mfma_f32_16x16x32_bf16 a[36:39],a[204:207],v[172:175],a[36:39]
v_exp_f32 v236,v236
v_mfma_f32_16x16x32_bf16 a[36:39],a[200:203],v[168:171],a[36:39]
v_exp_f32 v237,v237
v_mfma_f32_16x16x32_bf16 a[40:43],a[208:211],v[168:171],a[40:43]
v_exp_f32 v238,v238
v_mfma_f32_16x16x32_bf16 a[40:43],a[212:215],v[172:175],a[40:43]
v_exp_f32 v239,v239
v_mfma_f32_16x16x32_bf16 a[44:47],a[220:223],v[172:175],a[44:47]
v_cvt_pk_bf16_f32 v233,v234,v235
v_max_f32 v20,0x00800000,v48
v_mfma_f32_16x16x32_bf16 a[44:47],a[216:219],v[168:171],a[44:47]
s_lshl_b32 s95,s42,2
s_add_u32 s95,s95,s40
v_bfe_u32 v19,s55,13,1
v_mfma_f32_16x16x32_bf16 a[48:51],a[224:227],v[168:171],a[48:51]
s_lshl_b32 s95,s95,4
v_mov_b32 v18,s68
v_or_b32 v19,s69,v19
v_mfma_f32_16x16x32_bf16 a[48:51],a[228:231],v[172:175],a[48:51]
s_add_u32 m0,s82,45056
v_cvt_pk_bf16_f32 v234,v236,v237
v_cvt_pk_bf16_f32 v235,v238,v239
v_mfma_f32_16x16x32_bf16 a[52:55],a[236:239],v[172:175],a[52:55]
v_exp_f32 v248,v248
v_mfma_f32_16x16x32_bf16 a[52:55],a[232:235],v[168:171],a[52:55]
v_exp_f32 v249,v249
v_mfma_f32_16x16x32_bf16 a[56:59],a[240:243],v[168:171],a[56:59]
buffer_load_dwordx4 v11,s[72:75],s80 offen lds
v_mfma_f32_16x16x32_bf16 a[56:59],a[244:247],v[172:175],a[56:59]
v_exp_f32 v250,v250
v_mfma_f32_16x16x32_bf16 a[60:63],a[252:255],v[172:175],a[60:63]
v_exp_f32 v251,v251
v_mfma_f32_16x16x32_bf16 a[60:63],a[248:251],v[168:171],a[60:63]
v_exp_f32 v252,v252
v_smfmac_f32_16x16x64_bf16 v[52:55],v[28:31],v[168:175],v27
s_waitcnt vmcnt(6)
s_mov_b64 exec,1
buffer_store_dwordx2 v[18:19],v6,s[64:67],s95 offen sc1
s_mov_b64 exec,-1
v_mfma_f32_16x16x32_bf16 a[64:67],a[192:195],v[200:203],a[64:67]
v_exp_f32 v253,v253
v_mfma_f32_16x16x32_bf16 a[64:67],a[196:199],v[204:207],a[64:67]
v_exp_f32 v254,v254
v_mfma_f32_16x16x32_bf16 a[68:71],a[204:207],v[204:207],a[68:71]
v_exp_f32 v255,v255
v_mfma_f32_16x16x32_bf16 a[68:71],a[200:203],v[200:203],a[68:71]
s_add_u32 m0,s82,46080
v_rcp_f32 v20,v20
v_mfma_f32_16x16x32_bf16 a[72:75],a[208:211],v[200:203],a[72:75]
v_cvt_pk_bf16_f32 v236,v248,v249
v_cvt_pk_bf16_f32 v237,v250,v251
v_mfma_f32_16x16x32_bf16 a[72:75],a[212:215],v[204:207],a[72:75]
v_cvt_pk_bf16_f32 v238,v252,v253
v_cvt_pk_bf16_f32 v239,v254,v255
v_mfma_f32_16x16x32_bf16 a[76:79],a[220:223],v[204:207],a[76:79]
buffer_load_dwordx4 v7,s[72:75],s83 offen lds
v_mfma_f32_16x16x32_bf16 a[76:79],a[216:219],v[200:203],a[76:79]
v_accvgpr_read_b32 v136,a0
v_accvgpr_read_b32 v137,a1
v_accvgpr_read_b32 v138,a2
v_accvgpr_read_b32 v139,a3
v_accvgpr_read_b32 v12,a4
v_accvgpr_read_b32 v13,a5
v_accvgpr_read_b32 v14,a6
v_accvgpr_read_b32 v15,a7
v_pk_mul_f32 v[136:137],v[20:21],v[136:137] op_sel:[0,0] op_sel_hi:[0,1]
v_pk_mul_f32 v[138:139],v[20:21],v[138:139] op_sel:[0,0] op_sel_hi:[0,1]
v_pk_mul_f32 v[12:13],v[20:21],v[12:13] op_sel:[0,0] op_sel_hi:[0,1]
v_pk_mul_f32 v[14:15],v[20:21],v[14:15] op_sel:[0,0] op_sel_hi:[0,1]
v_cvt_pk_bf16_f32 v136,v136,v137
v_cvt_pk_bf16_f32 v137,v138,v139
v_cvt_pk_bf16_f32 v138,v12,v13
v_cvt_pk_bf16_f32 v139,v14,v15
v_mfma_f32_16x16x32_bf16 a[80:83],a[224:227],v[200:203],a[80:83]
ds_read_b64_tr_b16 a[128:129],v1
ds_read_b64_tr_b16 a[130:131],v1 offset:5120
v_mfma_f32_16x16x32_bf16 a[80:83],a[228:231],v[204:207],a[80:83]
ds_read_b64_tr_b16 a[132:133],v1 offset:10240
v_accvgpr_read_b32 v152,a16
v_mfma_f32_16x16x32_bf16 a[84:87],a[236:239],v[204:207],a[84:87]
ds_read_b64_tr_b16 a[134:135],v1 offset:15360
v_accvgpr_read_b32 v153,a17
v_mfma_f32_16x16x32_bf16 a[84:87],a[232:235],v[200:203],a[84:87]
ds_read_b64_tr_b16 a[136:137],v1 offset:20480
v_mul_f32 v152,v20,v152
v_mfma_f32_16x16x32_bf16 a[88:91],a[240:243],v[200:203],a[88:91]
ds_read_b64_tr_b16 a[138:139],v1 offset:25600
ds_read_b64_tr_b16 a[140:141],v1 offset:30720
v_mfma_f32_16x16x32_bf16 a[88:91],a[244:247],v[204:207],a[88:91]
v_accvgpr_read_b32 v140,a8
v_accvgpr_read_b32 v141,a9
v_accvgpr_read_b32 v142,a10
v_accvgpr_read_b32 v143,a11
v_accvgpr_read_b32 v12,a12
)A"
R"A(v_accvgpr_read_b32 v13,a13
v_accvgpr_read_b32 v14,a14
v_accvgpr_read_b32 v15,a15
v_pk_mul_f32 v[140:141],v[20:21],v[140:141] op_sel:[0,0] op_sel_hi:[0,1]
v_pk_mul_f32 v[142:143],v[20:21],v[142:143] op_sel:[0,0] op_sel_hi:[0,1]
v_pk_mul_f32 v[12:13],v[20:21],v[12:13] op_sel:[0,0] op_sel_hi:[0,1]
v_pk_mul_f32 v[14:15],v[20:21],v[14:15] op_sel:[0,0] op_sel_hi:[0,1]
v_cvt_pk_bf16_f32 v140,v140,v141
v_cvt_pk_bf16_f32 v141,v142,v143
v_cvt_pk_bf16_f32 v142,v12,v13
v_cvt_pk_bf16_f32 v143,v14,v15
v_mfma_f32_16x16x32_bf16 a[92:95],a[252:255],v[204:207],a[92:95]
ds_read_b64_tr_b16 a[142:143],v1 offset:35840
v_mul_f32 v153,v20,v153
v_mfma_f32_16x16x32_bf16 a[92:95],a[248:251],v[200:203],a[92:95]
v_accvgpr_read_b32 v154,a18
v_accvgpr_read_b32 v155,a19
v_smfmac_f32_16x16x64_bf16 v[56:59],v[28:31],v[200:207],v27
v_cvt_pk_bf16_f32 v152,v152,v153
v_mfma_f32_16x16x32_bf16 a[96:99],a[192:195],v[232:235],a[96:99]
buffer_store_dwordx4 v[136:139],v6,s[36:39],s84 offen sc1
v_mfma_f32_16x16x32_bf16 a[96:99],a[196:199],v[236:239],a[96:99]
v_mul_f32 v154,v20,v154
v_mul_f32 v155,v20,v155
v_mfma_f32_16x16x32_bf16 a[100:103],a[204:207],v[236:239],a[100:103]
v_cvt_pk_bf16_f32 v153,v154,v155
v_accvgpr_read_b32 v154,a20
v_mfma_f32_16x16x32_bf16 a[100:103],a[200:203],v[232:235],a[100:103]
v_accvgpr_read_b32 v155,a21
v_accvgpr_read_b32 v12,a22
v_mfma_f32_16x16x32_bf16 a[104:107],a[208:211],v[232:235],a[104:107]
buffer_load_dwordx4 v8,s[72:75],s83 offen offset:1024 lds
v_mfma_f32_16x16x32_bf16 a[104:107],a[212:215],v[236:239],a[104:107]
v_accvgpr_read_b32 v13,a23
v_mul_f32 v154,v20,v154
v_mfma_f32_16x16x32_bf16 a[108:111],a[220:223],v[236:239],a[108:111]
v_mul_f32 v155,v20,v155
v_mul_f32 v12,v20,v12
v_mfma_f32_16x16x32_bf16 a[108:111],a[216:219],v[232:235],a[108:111]
v_mul_f32 v13,v20,v13
v_cvt_pk_bf16_f32 v154,v154,v155
v_mfma_f32_16x16x32_bf16 a[112:115],a[224:227],v[232:235],a[112:115]
ds_read_b64_tr_b16 a[144:145],v2 offset:32
v_cvt_pk_bf16_f32 v155,v12,v13
v_mfma_f32_16x16x32_bf16 a[112:115],a[228:231],v[236:239],a[112:115]
ds_read_b64_tr_b16 a[146:147],v2 offset:5152
ds_read_b64_tr_b16 a[148:149],v2 offset:10272
v_mfma_f32_16x16x32_bf16 a[116:119],a[236:239],v[236:239],a[116:119]
ds_read_b64_tr_b16 a[150:151],v2 offset:15392
v_accvgpr_read_b32 v156,a24
v_mfma_f32_16x16x32_bf16 a[116:119],a[232:235],v[232:235],a[116:119]
buffer_load_dwordx4 v9,s[72:75],s83 offen offset:2048 lds
v_mfma_f32_16x16x32_bf16 a[120:123],a[240:243],v[232:235],a[120:123]
ds_read_b64_tr_b16 a[152:153],v2 offset:20512
ds_read_b64_tr_b16 a[154:155],v2 offset:25632
v_mfma_f32_16x16x32_bf16 a[120:123],a[244:247],v[236:239],a[120:123]
ds_read_b64_tr_b16 a[156:157],v2 offset:30752
v_accvgpr_read_b32 v157,a25
v_mfma_f32_16x16x32_bf16 a[124:127],a[252:255],v[236:239],a[124:127]
ds_read_b64_tr_b16 a[158:159],v2 offset:35872
v_mul_f32 v156,v20,v156
v_mfma_f32_16x16x32_bf16 a[124:127],a[248:251],v[232:235],a[124:127]
v_mul_f32 v157,v20,v157
v_accvgpr_read_b32 v158,a26
v_smfmac_f32_16x16x64_bf16 v[60:63],v[28:31],v[232:239],v27
s_waitcnt lgkmcnt(7)
v_accvgpr_read_b32 v159,a27
v_mfma_f32_16x16x32_bf16 a[192:195],a[128:131],v[64:67],0
v_cvt_pk_bf16_f32 v156,v156,v157
v_mul_f32 v158,v20,v158
v_mfma_f32_16x16x32_bf16 a[192:195],a[132:135],v[68:71],a[192:195]
v_mul_f32 v159,v20,v159
v_cvt_pk_bf16_f32 v157,v158,v159
v_mfma_f32_16x16x32_bf16 a[192:195],a[136:139],v[72:75],a[192:195]
buffer_store_dwordx4 v[140:143],v6,s[36:39],s84 offen offset:1024 sc1
v_mfma_f32_16x16x32_bf16 a[192:195],a[140:143],v[76:79],a[192:195]
v_accvgpr_read_b32 v158,a28
v_accvgpr_read_b32 v159,a29
v_mfma_f32_16x16x32_bf16 a[196:199],a[140:143],v[92:95],0
v_accvgpr_read_b32 v14,a30
v_accvgpr_read_b32 v15,a31
v_mfma_f32_16x16x32_bf16 a[196:199],a[136:139],v[88:91],a[196:199]
v_mul_f32 v158,v20,v158
v_mul_f32 v159,v20,v159
v_mfma_f32_16x16x32_bf16 a[196:199],a[132:135],v[84:87],a[196:199]
buffer_load_dwordx4 v10,s[72:75],s83 offen offset:3072 lds
v_mfma_f32_16x16x32_bf16 a[196:199],a[128:131],v[80:83],a[196:199]
v_mul_f32 v14,v20,v14
v_mul_f32 v15,v20,v15
v_mfma_f32_16x16x32_bf16 a[200:203],a[128:131],v[96:99],0
v_cvt_pk_bf16_f32 v158,v158,v159
v_cvt_pk_bf16_f32 v159,v14,v15
v_mfma_f32_16x16x32_bf16 a[200:203],a[132:135],v[100:103],a[200:203]
ds_read_b64_tr_b16 a[160:161],v1 offset:64
ds_read_b64_tr_b16 a[162:163],v1 offset:5184
v_mfma_f32_16x16x32_bf16 a[200:203],a[136:139],v[104:107],a[200:203]
ds_read_b64_tr_b16 a[164:165],v1 offset:10304
v_add_f32 v52,v53,v52
v_mfma_f32_16x16x32_bf16 a[200:203],a[140:143],v[108:111],a[200:203]
ds_read_b64_tr_b16 a[166:167],v1 offset:15424
v_max_f32 v21,0x00800000,v52
v_mfma_f32_16x16x32_bf16 a[204:207],a[140:143],v[124:127],0
s_cmp_ge_u32 1,s71
s_cselect_b32 s80,4,2
ds_read_b64_tr_b16 a[168:169],v1 offset:20544
v_mfma_f32_16x16x32_bf16 a[204:207],a[136:139],v[120:123],a[204:207]
s_add_u32 m0,s82,50176
ds_read_b64_tr_b16 a[170:171],v1 offset:25664
ds_read_b64_tr_b16 a[172:173],v1 offset:30784
v_mfma_f32_16x16x32_bf16 a[204:207],a[132:135],v[116:119],a[204:207]
s_mul_i32 s80,s80,163840
s_add_u32 s80,s80,s81
ds_read_b64_tr_b16 a[174:175],v1 offset:35904
v_mfma_f32_16x16x32_bf16 a[204:207],a[128:131],v[112:115],a[204:207]
s_waitcnt lgkmcnt(8)
v_rcp_f32 v21,v21
v_mfma_f32_16x16x32_bf16 a[208:211],a[144:147],v[64:67],0
buffer_load_dwordx4 v11,s[72:75],s83 offen lds
v_mfma_f32_16x16x32_bf16 a[208:211],a[148:151],v[68:71],a[208:211]
v_accvgpr_read_b32 v168,a32
v_accvgpr_read_b32 v169,a33
v_accvgpr_read_b32 v170,a34
v_accvgpr_read_b32 v171,a35
v_accvgpr_read_b32 v12,a36
v_accvgpr_read_b32 v13,a37
v_accvgpr_read_b32 v14,a38
v_accvgpr_read_b32 v15,a39
v_pk_mul_f32 v[168:169],v[20:21],v[168:169] op_sel:[1,0] op_sel_hi:[1,1]
v_pk_mul_f32 v[170:171],v[20:21],v[170:171] op_sel:[1,0] op_sel_hi:[1,1]
v_pk_mul_f32 v[12:13],v[20:21],v[12:13] op_sel:[1,0] op_sel_hi:[1,1]
v_pk_mul_f32 v[14:15],v[20:21],v[14:15] op_sel:[1,0] op_sel_hi:[1,1]
v_cvt_pk_bf16_f32 v168,v168,v169
v_cvt_pk_bf16_f32 v169,v170,v171
v_cvt_pk_bf16_f32 v170,v12,v13
v_cvt_pk_bf16_f32 v171,v14,v15
v_mfma_f32_16x16x32_bf16 a[208:211],a[152:155],v[72:75],a[208:211]
s_add_u32 s83,s80,20480
ds_read_b64_tr_b16 a[176:177],v2 offset:96
ds_read_b64_tr_b16 a[178:179],v2 offset:5216
v_mfma_f32_16x16x32_bf16 a[208:211],a[156:159],v[76:79],a[208:211]
ds_read_b64_tr_b16 a[180:181],v2 offset:10336
v_accvgpr_read_b32 v184,a48
v_mfma_f32_16x16x32_bf16 a[212:215],a[156:159],v[92:95],0
ds_read_b64_tr_b16 a[182:183],v2 offset:15456
v_accvgpr_read_b32 v185,a49
v_mfma_f32_16x16x32_bf16 a[212:215],a[152:155],v[88:91],a[212:215]
ds_read_b64_tr_b16 a[184:185],v2 offset:20576
v_mul_f32 v184,v21,v184
v_mfma_f32_16x16x32_bf16 a[212:215],a[148:151],v[84:87],a[212:215]
buffer_store_dwordx4 v[152:155],v6,s[36:39],s84 offen offset:2048 sc1
v_mfma_f32_16x16x32_bf16 a[212:215],a[144:147],v[80:83],a[212:215]
ds_read_b64_tr_b16 a[186:187],v2 offset:25696
ds_read_b64_tr_b16 a[188:189],v2 offset:30816
v_mfma_f32_16x16x32_bf16 a[216:219],a[144:147],v[96:99],0
ds_read_b64_tr_b16 a[190:191],v2 offset:35936
v_mul_f32 v185,v21,v185
v_mfma_f32_16x16x32_bf16 a[216:219],a[148:151],v[100:103],a[216:219]
v_accvgpr_read_b32 v172,a40
v_accvgpr_read_b32 v173,a41
v_accvgpr_read_b32 v174,a42
v_accvgpr_read_b32 v175,a43
v_accvgpr_read_b32 v12,a44
v_accvgpr_read_b32 v13,a45
v_accvgpr_read_b32 v14,a46
v_accvgpr_read_b32 v15,a47
v_pk_mul_f32 v[172:173],v[20:21],v[172:173] op_sel:[1,0] op_sel_hi:[1,1]
v_pk_mul_f32 v[174:175],v[20:21],v[174:175] op_sel:[1,0] op_sel_hi:[1,1]
v_pk_mul_f32 v[12:13],v[20:21],v[12:13] op_sel:[1,0] op_sel_hi:[1,1]
v_pk_mul_f32 v[14:15],v[20:21],v[14:15] op_sel:[1,0] op_sel_hi:[1,1]
v_cvt_pk_bf16_f32 v172,v172,v173
v_cvt_pk_bf16_f32 v173,v174,v175
v_cvt_pk_bf16_f32 v174,v12,v13
v_cvt_pk_bf16_f32 v175,v14,v15
v_mfma_f32_16x16x32_bf16 a[216:219],a[152:155],v[104:107],a[216:219]
v_accvgpr_read_b32 v186,a50
v_accvgpr_read_b32 v187,a51
v_mfma_f32_16x16x32_bf16 a[216:219],a[156:159],v[108:111],a[216:219]
v_cvt_pk_bf16_f32 v184,v184,v185
v_mul_f32 v186,v21,v186
v_mfma_f32_16x16x32_bf16 a[220:223],a[156:159],v[124:127],0
v_mul_f32 v187,v21,v187
v_cvt_pk_bf16_f32 v185,v186,v187
v_mfma_f32_16x16x32_bf16 a[220:223],a[152:155],v[120:123],a[220:223]
v_accvgpr_read_b32 v186,a52
v_accvgpr_read_b32 v187,a53
v_mfma_f32_16x16x32_bf16 a[220:223],a[148:151],v[116:119],a[220:223]
s_add_u32 m0,s82,122880
v_accvgpr_read_b32 v16,a54
v_accvgpr_read_b32 v17,a55
v_mfma_f32_16x16x32_bf16 a[220:223],a[144:147],v[112:115],a[220:223]
s_waitcnt lgkmcnt(1)
v_mul_f32 v186,v21,v186
v_mul_f32 v187,v21,v187
v_mfma_f32_16x16x32_bf16 a[224:227],a[160:163],v[64:67],0
v_mul_f32 v16,v21,v16
v_mul_f32 v17,v21,v17
v_mfma_f32_16x16x32_bf16 a[224:227],a[164:167],v[68:71],a[224:227]
buffer_load_dwordx4 v7,s[72:75],s80 offen lds
v_mfma_f32_16x16x32_bf16 a[224:227],a[168:171],v[72:75],a[224:227]
v_cvt_pk_bf16_f32 v186,v186,v187
v_cvt_pk_bf16_f32 v187,v16,v17
v_mfma_f32_16x16x32_bf16 a[224:227],a[172:175],v[76:79],a[224:227]
ds_read_b64_tr_b16 a[128:129],v1 offset:128
ds_read_b64_tr_b16 a[130:131],v1 offset:5248
v_mfma_f32_16x16x32_bf16 a[228:231],a[172:175],v[92:95],0
ds_read_b64_tr_b16 a[132:133],v1 offset:10368
v_accvgpr_read_b32 v188,a56
v_mfma_f32_16x16x32_bf16 a[228:231],a[168:171],v[88:91],a[228:231]
ds_read_b64_tr_b16 a[134:135],v1 offset:15488
v_accvgpr_read_b32 v189,a57
v_mfma_f32_16x16x32_bf16 a[228:231],a[164:167],v[84:87],a[228:231]
ds_read_b64_tr_b16 a[136:137],v1 offset:20608
v_mul_f32 v188,v21,v188
v_mfma_f32_16x16x32_bf16 a[228:231],a[160:163],v[80:83],a[228:231]
ds_read_b64_tr_b16 a[138:139],v1 offset:25728
ds_read_b64_tr_b16 a[140:141],v1 offset:30848
v_mfma_f32_16x16x32_bf16 a[232:235],a[160:163],v[96:99],0
ds_read_b64_tr_b16 a[142:143],v1 offset:35968
v_mul_f32 v189,v21,v189
v_mfma_f32_16x16x32_bf16 a[232:235],a[164:167],v[100:103],a[232:235]
v_accvgpr_read_b32 v190,a58
v_accvgpr_read_b32 v191,a59
v_mfma_f32_16x16x32_bf16 a[232:235],a[168:171],v[104:107],a[232:235]
buffer_store_dwordx4 v[156:159],v6,s[36:39],s84 offen offset:3072 sc1
v_mfma_f32_16x16x32_bf16 a[232:235],a[172:175],v[108:111],a[232:235]
v_cvt_pk_bf16_f32 v188,v188,v189
v_mul_f32 v190,v21,v190
v_mfma_f32_16x16x32_bf16 a[236:239],a[172:175],v[124:127],0
v_mul_f32 v191,v21,v191
v_cvt_pk_bf16_f32 v189,v190,v191
v_mfma_f32_16x16x32_bf16 a[236:239],a[168:171],v[120:123],a[236:239]
v_accvgpr_read_b32 v190,a60
v_accvgpr_read_b32 v191,a61
v_mfma_f32_16x16x32_bf16 a[236:239],a[164:167],v[116:119],a[236:239]
buffer_load_dwordx4 v8,s[72:75],s80 offen offset:1024 lds
v_mfma_f32_16x16x32_bf16 a[236:239],a[160:163],v[112:115],a[236:239]
v_accvgpr_read_b32 v12,a62
v_accvgpr_read_b32 v13,a63
v_mfma_f32_16x16x32_bf16 a[240:243],a[176:179],v[64:67],0
v_mul_f32 v190,v21,v190
v_mul_f32 v191,v21,v191
v_mfma_f32_16x16x32_bf16 a[240:243],a[180:183],v[68:71],a[240:243]
v_mul_f32 v12,v21,v12
v_mul_f32 v13,v21,v13
v_mfma_f32_16x16x32_bf16 a[240:243],a[184:187],v[72:75],a[240:243]
s_waitcnt lgkmcnt(0)
v_cvt_pk_bf16_f32 v190,v190,v191
v_cvt_pk_bf16_f32 v191,v12,v13
v_mfma_f32_16x16x32_bf16 a[240:243],a[188:191],v[76:79],a[240:243]
ds_read_b64_tr_b16 a[144:145],v2 offset:160
ds_read_b64_tr_b16 a[146:147],v2 offset:5280
v_mfma_f32_16x16x32_bf16 a[244:247],a[188:191],v[92:95],0
ds_read_b64_tr_b16 a[148:149],v2 offset:10400
v_add_f32 v56,v57,v56
v_mfma_f32_16x16x32_bf16 a[244:247],a[184:187],v[88:91],a[244:247]
ds_read_b64_tr_b16 a[150:151],v2 offset:15520
v_max_f32 v22,0x00800000,v56
v_mfma_f32_16x16x32_bf16 a[244:247],a[180:183],v[84:87],a[244:247]
ds_read_b64_tr_b16 a[152:153],v2 offset:20640
v_accvgpr_read_b32 v200,a64
v_mfma_f32_16x16x32_bf16 a[244:247],a[176:179],v[80:83],a[244:247]
ds_read_b64_tr_b16 a[154:155],v2 offset:25760
ds_read_b64_tr_b16 a[156:157],v2 offset:30880
v_mfma_f32_16x16x32_bf16 a[248:251],a[176:179],v[96:99],0
v_rcp_f32 v22,v22
v_mfma_f32_16x16x32_bf16 a[248:251],a[180:183],v[100:103],a[248:251]
buffer_load_dwordx4 v9,s[72:75],s80 offen offset:2048 lds
v_mfma_f32_16x16x32_bf16 a[248:251],a[184:187],v[104:107],a[248:251]
ds_read_b64_tr_b16 a[158:159],v2 offset:36000
v_accvgpr_read_b32 v201,a65
)A"
R"A(v_mfma_f32_16x16x32_bf16 a[248:251],a[188:191],v[108:111],a[248:251]
v_mul_f32 v200,v22,v200
v_mul_f32 v201,v22,v201
v_mfma_f32_16x16x32_bf16 a[252:255],a[188:191],v[124:127],0
v_accvgpr_read_b32 v202,a66
v_accvgpr_read_b32 v203,a67
v_mfma_f32_16x16x32_bf16 a[252:255],a[184:187],v[120:123],a[252:255]
v_cvt_pk_bf16_f32 v200,v200,v201
v_mul_f32 v202,v22,v202
v_mfma_f32_16x16x32_bf16 a[252:255],a[180:183],v[116:119],a[252:255]
s_waitcnt vmcnt(18)
v_mul_f32 v203,v22,v203
v_cvt_pk_bf16_f32 v201,v202,v203
v_mfma_f32_16x16x32_bf16 a[252:255],a[176:179],v[112:115],a[252:255]
s_barrier
buffer_store_dwordx4 v[168:171],v6,s[36:39],s85 offen sc1
v_mfma_f32_16x16x32_bf16 a[0:3],a[128:131],v[64:67],0
v_accvgpr_read_b32 v202,a68
v_accvgpr_read_b32 v203,a69
v_mfma_f32_16x16x32_bf16 a[0:3],a[132:135],v[68:71],a[0:3]
v_accvgpr_read_b32 v14,a70
v_accvgpr_read_b32 v15,a71
v_mfma_f32_16x16x32_bf16 a[0:3],a[136:139],v[72:75],a[0:3]
v_mul_f32 v202,v22,v202
v_mul_f32 v203,v22,v203
v_mfma_f32_16x16x32_bf16 a[0:3],a[140:143],v[76:79],a[0:3]
v_mul_f32 v14,v22,v14
v_mul_f32 v15,v22,v15
v_mfma_f32_16x16x32_bf16 a[4:7],a[140:143],v[92:95],0
buffer_load_dwordx4 v10,s[72:75],s80 offen offset:3072 lds
v_mfma_f32_16x16x32_bf16 a[4:7],a[136:139],v[88:91],a[4:7]
s_lshl_b32 s94,s70,4
v_cvt_pk_bf16_f32 v202,v202,v203
v_cvt_pk_bf16_f32 v203,v14,v15
v_mfma_f32_16x16x32_bf16 a[4:7],a[132:135],v[84:87],a[4:7]
s_add_u32 s94,s94,s40
v_accvgpr_read_b32 v204,a72
v_accvgpr_read_b32 v205,a73
v_mfma_f32_16x16x32_bf16 a[4:7],a[128:131],v[80:83],a[4:7]
s_lshl_b32 s94,s94,4
v_mul_f32 v204,v22,v204
v_mul_f32 v205,v22,v205
v_mfma_f32_16x16x32_bf16 a[8:11],a[128:131],v[96:99],0
v_accvgpr_read_b32 v206,a74
v_accvgpr_read_b32 v207,a75
v_mfma_f32_16x16x32_bf16 a[8:11],a[132:135],v[100:103],a[8:11]
ds_read_b64_tr_b16 a[160:161],v1 offset:192
ds_read_b64_tr_b16 a[162:163],v1 offset:5312
v_mfma_f32_16x16x32_bf16 a[8:11],a[136:139],v[104:107],a[8:11]
v_cvt_pk_bf16_f32 v204,v204,v205
v_mul_f32 v206,v22,v206
v_mfma_f32_16x16x32_bf16 a[8:11],a[140:143],v[108:111],a[8:11]
v_mul_f32 v207,v22,v207
v_cvt_pk_bf16_f32 v205,v206,v207
v_mfma_f32_16x16x32_bf16 a[12:15],a[140:143],v[124:127],0
ds_read_b64_tr_b16 a[164:165],v1 offset:10432
ds_read_b64_tr_b16 a[166:167],v1 offset:15552
v_mfma_f32_16x16x32_bf16 a[12:15],a[136:139],v[120:123],a[12:15]
s_add_u32 m0,s82,126976
v_accvgpr_read_b32 v206,a76
v_accvgpr_read_b32 v207,a77
v_mfma_f32_16x16x32_bf16 a[12:15],a[132:135],v[116:119],a[12:15]
v_accvgpr_read_b32 v16,a78
v_accvgpr_read_b32 v17,a79
v_mfma_f32_16x16x32_bf16 a[12:15],a[128:131],v[112:115],a[12:15]
s_waitcnt lgkmcnt(4)
v_mul_f32 v206,v22,v206
v_mul_f32 v207,v22,v207
v_mfma_f32_16x16x32_bf16 a[16:19],a[144:147],v[64:67],0
buffer_load_dwordx4 v11,s[72:75],s80 offen lds
v_mfma_f32_16x16x32_bf16 a[16:19],a[148:151],v[68:71],a[16:19]
ds_read_b64_tr_b16 a[168:169],v1 offset:20672
ds_read_b64_tr_b16 a[170:171],v1 offset:25792
v_mfma_f32_16x16x32_bf16 a[16:19],a[152:155],v[72:75],a[16:19]
v_mul_f32 v16,v22,v16
v_mul_f32 v17,v22,v17
v_mfma_f32_16x16x32_bf16 a[16:19],a[156:159],v[76:79],a[16:19]
v_cvt_pk_bf16_f32 v206,v206,v207
v_cvt_pk_bf16_f32 v207,v16,v17
v_mfma_f32_16x16x32_bf16 a[20:23],a[156:159],v[92:95],0
buffer_store_dwordx4 v[172:175],v6,s[36:39],s85 offen offset:1024 sc1
v_mfma_f32_16x16x32_bf16 a[20:23],a[152:155],v[88:91],a[20:23]
ds_read_b64_tr_b16 a[172:173],v1 offset:30912
ds_read_b64_tr_b16 a[174:175],v1 offset:36032
v_mfma_f32_16x16x32_bf16 a[20:23],a[148:151],v[84:87],a[20:23]
v_accvgpr_read_b32 v216,a80
v_accvgpr_read_b32 v217,a81
v_mfma_f32_16x16x32_bf16 a[20:23],a[144:147],v[80:83],a[20:23]
v_mul_f32 v216,v22,v216
v_mul_f32 v217,v22,v217
v_mfma_f32_16x16x32_bf16 a[24:27],a[144:147],v[96:99],0
v_accvgpr_read_b32 v218,a82
v_accvgpr_read_b32 v219,a83
v_mfma_f32_16x16x32_bf16 a[24:27],a[148:151],v[100:103],a[24:27]
s_add_u32 m0,s82,128000
v_cvt_pk_bf16_f32 v216,v216,v217
v_mul_f32 v218,v22,v218
v_mfma_f32_16x16x32_bf16 a[24:27],a[152:155],v[104:107],a[24:27]
v_mul_f32 v219,v22,v219
v_cvt_pk_bf16_f32 v217,v218,v219
v_mfma_f32_16x16x32_bf16 a[24:27],a[156:159],v[108:111],a[24:27]
v_accvgpr_read_b32 v218,a84
v_accvgpr_read_b32 v219,a85
v_mfma_f32_16x16x32_bf16 a[28:31],a[156:159],v[124:127],0
buffer_load_dwordx4 v7,s[72:75],s83 offen lds
v_mfma_f32_16x16x32_bf16 a[28:31],a[152:155],v[120:123],a[28:31]
v_accvgpr_read_b32 v12,a86
v_accvgpr_read_b32 v13,a87
v_mfma_f32_16x16x32_bf16 a[28:31],a[148:151],v[116:119],a[28:31]
v_mul_f32 v218,v22,v218
v_mul_f32 v219,v22,v219
v_mfma_f32_16x16x32_bf16 a[28:31],a[144:147],v[112:115],a[28:31]
s_waitcnt lgkmcnt(0)
v_mul_f32 v12,v22,v12
v_mul_f32 v13,v22,v13
v_mfma_f32_16x16x32_bf16 a[32:35],a[160:163],v[64:67],0
v_cvt_pk_bf16_f32 v218,v218,v219
v_bfe_u32 v24,v6,4,2
v_mfma_f32_16x16x32_bf16 a[32:35],a[164:167],v[68:71],a[32:35]
v_cvt_pk_bf16_f32 v219,v12,v13
v_min_u32 v24,2,v24
v_mfma_f32_16x16x32_bf16 a[32:35],a[168:171],v[72:75],a[32:35]
v_add_u32 v24,s71,v24
v_add_u32 v24,1,v24
v_mfma_f32_16x16x32_bf16 a[32:35],a[172:175],v[76:79],a[32:35]
ds_read_b64_tr_b16 a[176:177],v2 offset:224
ds_read_b64_tr_b16 a[178:179],v2 offset:5344
v_mfma_f32_16x16x32_bf16 a[36:39],a[172:175],v[92:95],0
v_and_b32 v24,3,v24
v_lshlrev_b32 v24,6,v24
v_mfma_f32_16x16x32_bf16 a[36:39],a[168:171],v[88:91],a[36:39]
ds_read_b64_tr_b16 a[180:181],v2 offset:10464
ds_read_b64_tr_b16 a[182:183],v2 offset:15584
v_mfma_f32_16x16x32_bf16 a[36:39],a[164:167],v[84:87],a[36:39]
buffer_store_dwordx4 v[184:187],v6,s[36:39],s85 offen offset:2048 sc1
v_mfma_f32_16x16x32_bf16 a[36:39],a[160:163],v[80:83],a[36:39]
ds_read_b64_tr_b16 a[184:185],v2 offset:20704
ds_read_b64_tr_b16 a[186:187],v2 offset:25824
v_mfma_f32_16x16x32_bf16 a[40:43],a[160:163],v[96:99],0
ds_read_b64_tr_b16 a[188:189],v2 offset:30944
v_accvgpr_read_b32 v220,a88
v_mfma_f32_16x16x32_bf16 a[40:43],a[164:167],v[100:103],a[40:43]
ds_read_b64_tr_b16 a[190:191],v2 offset:36064
v_accvgpr_read_b32 v221,a89
v_mfma_f32_16x16x32_bf16 a[40:43],a[168:171],v[104:107],a[40:43]
buffer_load_dwordx4 v8,s[72:75],s83 offen offset:1024 lds
v_mfma_f32_16x16x32_bf16 a[40:43],a[172:175],v[108:111],a[40:43]
v_mul_f32 v220,v22,v220
v_mul_f32 v221,v22,v221
v_mfma_f32_16x16x32_bf16 a[44:47],a[172:175],v[124:127],0
v_accvgpr_read_b32 v222,a90
v_accvgpr_read_b32 v223,a91
v_mfma_f32_16x16x32_bf16 a[44:47],a[168:171],v[120:123],a[44:47]
v_cvt_pk_bf16_f32 v220,v220,v221
v_mul_f32 v222,v22,v222
v_mfma_f32_16x16x32_bf16 a[44:47],a[164:167],v[116:119],a[44:47]
buffer_load_dwordx2 v[26:27],v24,s[64:67],s94 offen sc1
v_mfma_f32_16x16x32_bf16 a[44:47],a[160:163],v[112:115],a[44:47]
s_waitcnt lgkmcnt(2)
v_mul_f32 v223,v22,v223
v_cvt_pk_bf16_f32 v221,v222,v223
v_mfma_f32_16x16x32_bf16 a[48:51],a[176:179],v[64:67],0
v_accvgpr_read_b32 v222,a92
v_accvgpr_read_b32 v223,a93
v_mfma_f32_16x16x32_bf16 a[48:51],a[180:183],v[68:71],a[48:51]
v_accvgpr_read_b32 v14,a94
v_accvgpr_read_b32 v15,a95
v_mfma_f32_16x16x32_bf16 a[48:51],a[184:187],v[72:75],a[48:51]
s_waitcnt lgkmcnt(0)
v_mul_f32 v222,v22,v222
v_mul_f32 v223,v22,v223
v_mfma_f32_16x16x32_bf16 a[48:51],a[188:191],v[76:79],a[48:51]
v_mul_f32 v14,v22,v14
v_mul_f32 v15,v22,v15
v_mfma_f32_16x16x32_bf16 a[52:55],a[188:191],v[92:95],0
v_cvt_pk_bf16_f32 v222,v222,v223
v_cvt_pk_bf16_f32 v223,v14,v15
v_mfma_f32_16x16x32_bf16 a[52:55],a[184:187],v[88:91],a[52:55]
buffer_load_dwordx4 v9,s[72:75],s83 offen offset:2048 lds
v_mfma_f32_16x16x32_bf16 a[52:55],a[180:183],v[84:87],a[52:55]
ds_read_b64_tr_b16 a[128:129],v1 offset:256
ds_read_b64_tr_b16 a[130:131],v1 offset:5376
v_mfma_f32_16x16x32_bf16 a[52:55],a[176:179],v[80:83],a[52:55]
ds_read_b64_tr_b16 a[132:133],v1 offset:10496
v_add_f32 v60,v61,v60
v_mfma_f32_16x16x32_bf16 a[56:59],a[176:179],v[96:99],0
ds_read_b64_tr_b16 a[134:135],v1 offset:15616
v_max_f32 v23,0x00800000,v60
v_mfma_f32_16x16x32_bf16 a[56:59],a[180:183],v[100:103],a[56:59]
ds_read_b64_tr_b16 a[136:137],v1 offset:20736
ds_read_b64_tr_b16 a[138:139],v1 offset:25856
v_mfma_f32_16x16x32_bf16 a[56:59],a[184:187],v[104:107],a[56:59]
ds_read_b64_tr_b16 a[140:141],v1 offset:30976
ds_read_b64_tr_b16 a[142:143],v1 offset:36096
v_mfma_f32_16x16x32_bf16 a[56:59],a[188:191],v[108:111],a[56:59]
buffer_store_dwordx4 v[188:191],v6,s[36:39],s85 offen offset:3072 sc1
v_mfma_f32_16x16x32_bf16 a[60:63],a[188:191],v[124:127],0
v_rcp_f32 v23,v23
v_mfma_f32_16x16x32_bf16 a[60:63],a[184:187],v[120:123],a[60:63]
v_accvgpr_read_b32 v232,a96
v_accvgpr_read_b32 v233,a97
v_mfma_f32_16x16x32_bf16 a[60:63],a[180:183],v[116:119],a[60:63]
v_mul_f32 v232,v23,v232
v_mul_f32 v233,v23,v233
v_mfma_f32_16x16x32_bf16 a[60:63],a[176:179],v[112:115],a[60:63]
s_waitcnt lgkmcnt(5)
v_accvgpr_read_b32 v234,a98
v_accvgpr_read_b32 v235,a99
v_mfma_f32_16x16x32_bf16 a[64:67],a[128:131],v[64:67],0
s_waitcnt lgkmcnt(4)
v_cvt_pk_bf16_f32 v232,v232,v233
v_mul_f32 v234,v23,v234
v_mfma_f32_16x16x32_bf16 a[64:67],a[132:135],v[68:71],a[64:67]
s_waitcnt lgkmcnt(2)
v_mul_f32 v235,v23,v235
v_cvt_pk_bf16_f32 v233,v234,v235
v_mfma_f32_16x16x32_bf16 a[64:67],a[136:139],v[72:75],a[64:67]
s_waitcnt lgkmcnt(0)
v_accvgpr_read_b32 v234,a100
v_accvgpr_read_b32 v235,a101
v_mfma_f32_16x16x32_bf16 a[64:67],a[140:143],v[76:79],a[64:67]
buffer_load_dwordx4 v10,s[72:75],s83 offen offset:3072 lds
v_mfma_f32_16x16x32_bf16 a[68:71],a[140:143],v[92:95],0
v_accvgpr_read_b32 v16,a102
v_accvgpr_read_b32 v17,a103
v_mfma_f32_16x16x32_bf16 a[68:71],a[136:139],v[88:91],a[68:71]
v_mul_f32 v234,v23,v234
v_mul_f32 v235,v23,v235
v_mfma_f32_16x16x32_bf16 a[68:71],a[132:135],v[84:87],a[68:71]
v_mul_f32 v16,v23,v16
v_mul_f32 v17,v23,v17
v_mfma_f32_16x16x32_bf16 a[68:71],a[128:131],v[80:83],a[68:71]
ds_read_b64_tr_b16 a[144:145],v2 offset:288
ds_read_b64_tr_b16 a[146:147],v2 offset:5408
v_mfma_f32_16x16x32_bf16 a[72:75],a[128:131],v[96:99],0
ds_read_b64_tr_b16 a[148:149],v2 offset:10528
ds_read_b64_tr_b16 a[150:151],v2 offset:15648
v_mfma_f32_16x16x32_bf16 a[72:75],a[132:135],v[100:103],a[72:75]
ds_read_b64_tr_b16 a[152:153],v2 offset:20768
ds_read_b64_tr_b16 a[154:155],v2 offset:25888
v_mfma_f32_16x16x32_bf16 a[72:75],a[136:139],v[104:107],a[72:75]
ds_read_b64_tr_b16 a[156:157],v2 offset:31008
ds_read_b64_tr_b16 a[158:159],v2 offset:36128
v_mfma_f32_16x16x32_bf16 a[72:75],a[140:143],v[108:111],a[72:75]
s_add_u32 m0,s82,132096
v_mfma_f32_16x16x32_bf16 a[76:79],a[140:143],v[124:127],0
v_cvt_pk_bf16_f32 v234,v234,v235
v_cvt_pk_bf16_f32 v235,v16,v17
v_mfma_f32_16x16x32_bf16 a[76:79],a[136:139],v[120:123],a[76:79]
v_accvgpr_read_b32 v236,a104
v_accvgpr_read_b32 v237,a105
v_mfma_f32_16x16x32_bf16 a[76:79],a[132:135],v[116:119],a[76:79]
v_mul_f32 v236,v23,v236
v_mul_f32 v237,v23,v237
v_mfma_f32_16x16x32_bf16 a[76:79],a[128:131],v[112:115],a[76:79]
s_waitcnt lgkmcnt(6)
buffer_load_dwordx4 v11,s[72:75],s83 offen lds
v_mfma_f32_16x16x32_bf16 a[80:83],a[144:147],v[64:67],0
s_waitcnt lgkmcnt(4)
v_accvgpr_read_b32 v238,a106
v_accvgpr_read_b32 v239,a107
v_mfma_f32_16x16x32_bf16 a[80:83],a[148:151],v[68:71],a[80:83]
s_waitcnt vmcnt(4)
v_cmp_ne_u32 s[90:91],s68,v26
v_and_b32 v26,-2,v27
v_cmp_ne_u32 s[92:93],s69,v26
s_nop 0
s_or_b64 s[90:91],s[90:91],s[92:93]
s_cbranch_scc1 L_fr8_%=
L_fo8_%=:
s_waitcnt lgkmcnt(2)
v_mfma_f32_16x16x32_bf16 a[80:83],a[152:155],v[72:75],a[80:83]
s_waitcnt lgkmcnt(0)
v_cvt_pk_bf16_f32 v236,v236,v237
v_mul_f32 v238,v23,v238
v_mfma_f32_16x16x32_bf16 a[80:83],a[156:159],v[76:79],a[80:83]
v_mul_f32 v239,v23,v239
v_cvt_pk_bf16_f32 v237,v238,v239
v_mfma_f32_16x16x32_bf16 a[84:87],a[156:159],v[92:95],0
v_accvgpr_read_b32 v238,a108
v_accvgpr_read_b32 v239,a109
v_mfma_f32_16x16x32_bf16 a[84:87],a[152:155],v[88:91],a[84:87]
v_accvgpr_read_b32 v12,a110
v_accvgpr_read_b32 v13,a111
v_mfma_f32_16x16x32_bf16 a[84:87],a[148:151],v[84:87],a[84:87]
v_subrev_u32 v1,81920,v1
v_mul_f32 v238,v23,v238
v_mfma_f32_16x16x32_bf16 a[84:87],a[144:147],v[80:83],a[84:87]
ds_read_b64_tr_b16 a[160:161],v1
ds_read_b64_tr_b16 a[162:163],v1 offset:5120
v_mfma_f32_16x16x32_bf16 a[88:91],a[144:147],v[96:99],0
ds_read_b64_tr_b16 a[164:165],v1 offset:10240
ds_read_b64_tr_b16 a[166:167],v1 offset:15360
)A"
R"A(v_mfma_f32_16x16x32_bf16 a[88:91],a[148:151],v[100:103],a[88:91]
ds_read_b64_tr_b16 a[168:169],v1 offset:20480
ds_read_b64_tr_b16 a[170:171],v1 offset:25600
v_mfma_f32_16x16x32_bf16 a[88:91],a[152:155],v[104:107],a[88:91]
ds_read_b64_tr_b16 a[172:173],v1 offset:30720
ds_read_b64_tr_b16 a[174:175],v1 offset:35840
v_mfma_f32_16x16x32_bf16 a[88:91],a[156:159],v[108:111],a[88:91]
v_accvgpr_read_b32 v248,a112
v_accvgpr_read_b32 v249,a113
v_mul_f32 v248,v23,v248
v_mul_f32 v249,v23,v249
v_accvgpr_read_b32 v250,a114
v_accvgpr_read_b32 v251,a115
v_accvgpr_read_b32 v252,a120
v_accvgpr_read_b32 v253,a121
v_cmp_nlt_f32 s[56:57],v48,s63
v_mul_f32 v252,v23,v252
v_mul_f32 v253,v23,v253
v_accvgpr_read_b32 v254,a122
v_accvgpr_read_b32 v255,a123
v_mfma_f32_16x16x32_bf16 a[92:95],a[156:159],v[124:127],0
v_cvt_pk_bf16_f32 v248,v248,v249
v_mul_f32 v250,v23,v250
v_mul_f32 v251,v23,v251
v_cvt_pk_bf16_f32 v249,v250,v251
v_cvt_pk_bf16_f32 v252,v252,v253
v_mul_f32 v254,v23,v254
v_mul_f32 v255,v23,v255
v_cmp_nlt_f32 s[58:59],v52,s63
s_nop 1
s_or_b64 s[56:57],s[56:57],s[58:59]
v_cvt_pk_bf16_f32 v253,v254,v255
v_mfma_f32_16x16x32_bf16 a[92:95],a[152:155],v[120:123],a[92:95]
v_mul_f32 v239,v23,v239
v_accvgpr_read_b32 v250,a116
v_accvgpr_read_b32 v251,a117
v_accvgpr_read_b32 v14,a118
v_accvgpr_read_b32 v15,a119
v_mul_f32 v250,v23,v250
v_mul_f32 v251,v23,v251
v_accvgpr_read_b32 v254,a124
v_accvgpr_read_b32 v255,a125
v_cmp_nlt_f32 s[58:59],v56,s63
s_nop 1
s_or_b64 s[56:57],s[56:57],s[58:59]
v_accvgpr_read_b32 v16,a126
v_accvgpr_read_b32 v17,a127
v_mul_f32 v254,v23,v254
v_mul_f32 v255,v23,v255
v_mfma_f32_16x16x32_bf16 a[92:95],a[148:151],v[116:119],a[92:95]
v_mul_f32 v12,v23,v12
v_mul_f32 v13,v23,v13
v_cvt_pk_bf16_f32 v238,v238,v239
v_cvt_pk_bf16_f32 v239,v12,v13
v_mul_f32 v14,v23,v14
v_mul_f32 v15,v23,v15
v_cvt_pk_bf16_f32 v250,v250,v251
v_cvt_pk_bf16_f32 v251,v14,v15
v_mul_f32 v16,v23,v16
v_mul_f32 v17,v23,v17
v_cvt_pk_bf16_f32 v254,v254,v255
v_cmp_nlt_f32 s[58:59],v60,s63
s_nop 1
s_or_b64 s[56:57],s[56:57],s[58:59]
v_cvt_pk_bf16_f32 v255,v16,v17
v_mfma_f32_16x16x32_bf16 a[92:95],a[144:147],v[112:115],a[92:95]
s_cmp_lg_u64 s[56:57],0
s_cbranch_scc1 L_slow1_%=
L_sret1_%=:
v_subrev_u32 v2,81920,v2
s_waitcnt lgkmcnt(6)
v_mfma_f32_16x16x32_bf16 a[192:195],a[160:163],v[136:139],a[192:195]
s_nop 0
v_readlane_b32 s76,v27,0
v_readlane_b32 s77,v27,1
v_readlane_b32 s78,v27,2
s_waitcnt lgkmcnt(4)
v_mfma_f32_16x16x32_bf16 a[192:195],a[164:167],v[140:143],a[192:195]
s_and_b32 s77,s77,1
s_nop 1
s_and_b32 s54,s76,1
s_lshl_b32 s77,s77,2
s_and_b32 s78,s78,1
s_waitcnt lgkmcnt(2)
v_mfma_f32_16x16x32_bf16 a[192:195],a[168:171],v[152:155],a[192:195]
s_or_b32 s54,s54,s77
s_lshl_b32 s78,s78,4
s_or_b32 s54,s54,s78
s_waitcnt lgkmcnt(0)
v_mfma_f32_16x16x32_bf16 a[192:195],a[172:175],v[156:159],a[192:195]
s_mul_i32 s54,s54,3084
s_lshl_b32 s76,s71,1
s_lshl_b32 s54,s54,s76
v_mfma_f32_16x16x32_bf16 a[196:199],a[172:175],v[188:191],a[196:199]
s_lshr_b32 s54,s54,8
s_cmp_ge_u32 0,s71
s_cselect_b32 s76,2,0
v_mfma_f32_16x16x32_bf16 a[196:199],a[168:171],v[184:187],a[196:199]
s_bitcmp1_b32 s54,s76
s_cselect_b32 s77,8192,0
s_lshl_b32 s76,s76,17
v_mfma_f32_16x16x32_bf16 a[196:199],a[164:167],v[172:175],a[196:199]
s_add_u32 s76,s76,s77
buffer_store_dwordx4 v[200:203],v6,s[36:39],s86 offen sc1
v_mfma_f32_16x16x32_bf16 a[196:199],a[160:163],v[168:171],a[196:199]
s_add_u32 s77,s76,4096
s_add_u32 s78,s76,16384
s_add_u32 s79,s76,20480
s_max_i32 s76,s76,s18
v_mfma_f32_16x16x32_bf16 a[200:203],a[160:163],v[200:203],a[200:203]
s_max_i32 s77,s77,s19
ds_read_b64_tr_b16 a[176:177],v2 offset:32
ds_read_b64_tr_b16 a[178:179],v2 offset:5152
v_mfma_f32_16x16x32_bf16 a[200:203],a[164:167],v[204:207],a[200:203]
ds_read_b64_tr_b16 a[180:181],v2 offset:10272
ds_read_b64_tr_b16 a[182:183],v2 offset:15392
v_mfma_f32_16x16x32_bf16 a[200:203],a[168:171],v[216:219],a[200:203]
buffer_load_dwordx4 v[128:131],v6,s[28:31],s76 offen sc1
ds_read_b64_tr_b16 a[184:185],v2 offset:20512
ds_read_b64_tr_b16 a[186:187],v2 offset:25632
v_mfma_f32_16x16x32_bf16 a[200:203],a[172:175],v[220:223],a[200:203]
ds_read_b64_tr_b16 a[188:189],v2 offset:30752
ds_read_b64_tr_b16 a[190:191],v2 offset:35872
v_mfma_f32_16x16x32_bf16 a[204:207],a[172:175],v[252:255],a[204:207]
v_mfma_f32_16x16x32_bf16 a[204:207],a[168:171],v[248:251],a[204:207]
s_cmp_ge_u32 2,s71
s_cselect_b32 s80,6,4
s_mul_i32 s80,s80,163840
v_mfma_f32_16x16x32_bf16 a[204:207],a[164:167],v[236:239],a[204:207]
s_add_u32 s80,s80,s81
buffer_store_dwordx4 v[204:207],v6,s[36:39],s86 offen offset:1024 sc1
v_mfma_f32_16x16x32_bf16 a[204:207],a[160:163],v[232:235],a[204:207]
s_barrier
ds_read_b64_tr_b16 a[128:129],v1 offset:64
ds_read_b64_tr_b16 a[130:131],v1 offset:5184
s_waitcnt lgkmcnt(8)
v_mfma_f32_16x16x32_bf16 a[208:211],a[176:179],v[136:139],a[208:211]
s_max_i32 s78,s78,s100
s_max_i32 s79,s79,s101
ds_read_b64_tr_b16 a[132:133],v1 offset:10304
s_waitcnt lgkmcnt(7)
v_mfma_f32_16x16x32_bf16 a[208:211],a[180:183],v[140:143],a[208:211]
s_add_u32 s83,s80,20480
buffer_load_dwordx4 v[160:163],v6,s[28:31],s76 offen offset:1024 sc1
s_waitcnt lgkmcnt(5)
v_mfma_f32_16x16x32_bf16 a[208:211],a[184:187],v[152:155],a[208:211]
s_add_u32 m0,s82,81920
ds_read_b64_tr_b16 a[134:135],v1 offset:15424
ds_read_b64_tr_b16 a[136:137],v1 offset:20544
s_waitcnt lgkmcnt(5)
v_mfma_f32_16x16x32_bf16 a[208:211],a[188:191],v[156:159],a[208:211]
ds_read_b64_tr_b16 a[138:139],v1 offset:25664
v_mfma_f32_16x16x32_bf16 a[212:215],a[188:191],v[188:191],a[212:215]
ds_read_b64_tr_b16 a[140:141],v1 offset:30784
v_mfma_f32_16x16x32_bf16 a[212:215],a[184:187],v[184:187],a[212:215]
buffer_load_dwordx4 v[192:195],v6,s[28:31],s76 offen offset:2048 sc1
v_mfma_f32_16x16x32_bf16 a[212:215],a[180:183],v[172:175],a[212:215]
ds_read_b64_tr_b16 a[142:143],v1 offset:35904
ds_read_b64_tr_b16 a[144:145],v2 offset:96
v_mfma_f32_16x16x32_bf16 a[212:215],a[176:179],v[168:171],a[212:215]
ds_read_b64_tr_b16 a[146:147],v2 offset:5216
v_mfma_f32_16x16x32_bf16 a[216:219],a[176:179],v[200:203],a[216:219]
ds_read_b64_tr_b16 a[148:149],v2 offset:10336
v_mfma_f32_16x16x32_bf16 a[216:219],a[180:183],v[204:207],a[216:219]
buffer_load_dwordx4 v7,s[72:75],s80 offen lds
v_mfma_f32_16x16x32_bf16 a[216:219],a[184:187],v[216:219],a[216:219]
ds_read_b64_tr_b16 a[150:151],v2 offset:15456
ds_read_b64_tr_b16 a[152:153],v2 offset:20576
v_mfma_f32_16x16x32_bf16 a[216:219],a[188:191],v[220:223],a[216:219]
ds_read_b64_tr_b16 a[154:155],v2 offset:25696
v_mfma_f32_16x16x32_bf16 a[220:223],a[188:191],v[252:255],a[220:223]
ds_read_b64_tr_b16 a[156:157],v2 offset:30816
v_mfma_f32_16x16x32_bf16 a[220:223],a[184:187],v[248:251],a[220:223]
buffer_load_dwordx4 v[224:227],v6,s[28:31],s76 offen offset:3072 sc1
v_mfma_f32_16x16x32_bf16 a[220:223],a[180:183],v[236:239],a[220:223]
ds_read_b64_tr_b16 a[158:159],v2 offset:35936
ds_read_b64_tr_b16 a[160:161],v1 offset:128
v_mfma_f32_16x16x32_bf16 a[220:223],a[176:179],v[232:235],a[220:223]
s_waitcnt lgkmcnt(7)
ds_read_b64_tr_b16 a[162:163],v1 offset:5248
v_mfma_f32_16x16x32_bf16 a[224:227],a[128:131],v[136:139],a[224:227]
ds_read_b64_tr_b16 a[164:165],v1 offset:10368
v_mfma_f32_16x16x32_bf16 a[224:227],a[132:135],v[140:143],a[224:227]
buffer_store_dwordx4 v[216:219],v6,s[36:39],s86 offen offset:2048 sc1
v_mfma_f32_16x16x32_bf16 a[224:227],a[136:139],v[152:155],a[224:227]
ds_read_b64_tr_b16 a[166:167],v1 offset:15488
ds_read_b64_tr_b16 a[168:169],v1 offset:20608
v_mfma_f32_16x16x32_bf16 a[224:227],a[140:143],v[156:159],a[224:227]
ds_read_b64_tr_b16 a[170:171],v1 offset:25728
v_mfma_f32_16x16x32_bf16 a[228:231],a[140:143],v[188:191],a[228:231]
ds_read_b64_tr_b16 a[172:173],v1 offset:30848
v_mfma_f32_16x16x32_bf16 a[228:231],a[136:139],v[184:187],a[228:231]
buffer_load_dwordx4 v[132:135],v6,s[28:31],s77 offen sc1
v_mfma_f32_16x16x32_bf16 a[228:231],a[132:135],v[172:175],a[228:231]
ds_read_b64_tr_b16 a[174:175],v1 offset:35968
ds_read_b64_tr_b16 a[176:177],v2 offset:160
v_mfma_f32_16x16x32_bf16 a[228:231],a[128:131],v[168:171],a[228:231]
ds_read_b64_tr_b16 a[178:179],v2 offset:5280
v_mfma_f32_16x16x32_bf16 a[232:235],a[128:131],v[200:203],a[232:235]
ds_read_b64_tr_b16 a[180:181],v2 offset:10400
v_mfma_f32_16x16x32_bf16 a[232:235],a[132:135],v[204:207],a[232:235]
buffer_load_dwordx4 v8,s[72:75],s80 offen offset:1024 lds
v_mfma_f32_16x16x32_bf16 a[232:235],a[136:139],v[216:219],a[232:235]
ds_read_b64_tr_b16 a[182:183],v2 offset:15520
ds_read_b64_tr_b16 a[184:185],v2 offset:20640
v_mfma_f32_16x16x32_bf16 a[232:235],a[140:143],v[220:223],a[232:235]
ds_read_b64_tr_b16 a[186:187],v2 offset:25760
v_mfma_f32_16x16x32_bf16 a[236:239],a[140:143],v[252:255],a[236:239]
ds_read_b64_tr_b16 a[188:189],v2 offset:30880
v_mfma_f32_16x16x32_bf16 a[236:239],a[136:139],v[248:251],a[236:239]
buffer_load_dwordx4 v[164:167],v6,s[28:31],s77 offen offset:1024 sc1
v_mfma_f32_16x16x32_bf16 a[236:239],a[132:135],v[236:239],a[236:239]
ds_read_b64_tr_b16 a[190:191],v2 offset:36000
ds_read_b64_tr_b16 a[132:133],v1 offset:10432
v_mfma_f32_16x16x32_bf16 a[236:239],a[128:131],v[232:235],a[236:239]
ds_read_b64_tr_b16 a[128:129],v1 offset:192
v_mfma_f32_16x16x32_bf16 a[240:243],a[144:147],v[136:139],a[240:243]
s_waitcnt lgkmcnt(7)
ds_read_b64_tr_b16 a[130:131],v1 offset:5312
v_mfma_f32_16x16x32_bf16 a[240:243],a[148:151],v[140:143],a[240:243]
buffer_load_dwordx4 v[196:199],v6,s[28:31],s77 offen offset:2048 sc1
v_mfma_f32_16x16x32_bf16 a[240:243],a[152:155],v[152:155],a[240:243]
ds_read_b64_tr_b16 a[134:135],v1 offset:15552
ds_read_b64_tr_b16 a[136:137],v1 offset:20672
v_mfma_f32_16x16x32_bf16 a[240:243],a[156:159],v[156:159],a[240:243]
ds_read_b64_tr_b16 a[138:139],v1 offset:25792
v_mfma_f32_16x16x32_bf16 a[244:247],a[156:159],v[188:191],a[244:247]
ds_read_b64_tr_b16 a[140:141],v1 offset:30912
v_mfma_f32_16x16x32_bf16 a[244:247],a[152:155],v[184:187],a[244:247]
buffer_store_dwordx4 v[220:223],v6,s[36:39],s86 offen offset:3072 sc1
v_mfma_f32_16x16x32_bf16 a[244:247],a[148:151],v[172:175],a[244:247]
ds_read_b64_tr_b16 a[142:143],v1 offset:36032
v_mfma_f32_16x16x32_bf16 a[244:247],a[144:147],v[168:171],a[244:247]
v_mfma_f32_16x16x32_bf16 a[248:251],a[144:147],v[200:203],a[248:251]
v_mfma_f32_16x16x32_bf16 a[248:251],a[148:151],v[204:207],a[248:251]
buffer_load_dwordx4 v9,s[72:75],s80 offen offset:2048 lds
v_mfma_f32_16x16x32_bf16 a[248:251],a[152:155],v[216:219],a[248:251]
v_mfma_f32_16x16x32_bf16 a[248:251],a[156:159],v[220:223],a[248:251]
v_mfma_f32_16x16x32_bf16 a[252:255],a[156:159],v[252:255],a[252:255]
ds_read_b64_tr_b16 a[156:157],v2 offset:30944
ds_read_b64_tr_b16 a[158:159],v2 offset:36064
v_mfma_f32_16x16x32_bf16 a[252:255],a[152:155],v[248:251],a[252:255]
buffer_load_dwordx4 v[228:231],v6,s[28:31],s77 offen offset:3072 sc1
v_mfma_f32_16x16x32_bf16 a[252:255],a[148:151],v[236:239],a[252:255]
ds_read_b64_tr_b16 a[148:149],v2 offset:10464
ds_read_b64_tr_b16 a[150:151],v2 offset:15584
v_mfma_f32_16x16x32_bf16 a[252:255],a[144:147],v[232:235],a[252:255]
ds_read_b64_tr_b16 a[144:145],v2 offset:224
v_mfma_f32_16x16x32_bf16 a[0:3],a[160:163],v[136:139],a[0:3]
ds_read_b64_tr_b16 a[146:147],v2 offset:5344
v_mfma_f32_16x16x32_bf16 a[0:3],a[164:167],v[140:143],a[0:3]
buffer_load_dwordx4 v[144:147],v6,s[28:31],s78 offen sc1
v_mfma_f32_16x16x32_bf16 a[0:3],a[168:171],v[152:155],a[0:3]
ds_read_b64_tr_b16 a[152:153],v2 offset:20704
ds_read_b64_tr_b16 a[154:155],v2 offset:25824
v_mfma_f32_16x16x32_bf16 a[0:3],a[172:175],v[156:159],a[0:3]
v_mfma_f32_16x16x32_bf16 a[4:7],a[172:175],v[188:191],a[4:7]
v_mfma_f32_16x16x32_bf16 a[4:7],a[168:171],v[184:187],a[4:7]
buffer_load_dwordx4 v10,s[72:75],s80 offen offset:3072 lds
v_mfma_f32_16x16x32_bf16 a[4:7],a[164:167],v[172:175],a[4:7]
v_mfma_f32_16x16x32_bf16 a[4:7],a[160:163],v[168:171],a[4:7]
v_mfma_f32_16x16x32_bf16 a[8:11],a[160:163],v[200:203],a[8:11]
v_mfma_f32_16x16x32_bf16 a[8:11],a[164:167],v[204:207],a[8:11]
buffer_load_dwordx4 v[176:179],v6,s[28:31],s78 offen offset:1024 sc1
v_mfma_f32_16x16x32_bf16 a[8:11],a[168:171],v[216:219],a[8:11]
v_mfma_f32_16x16x32_bf16 a[8:11],a[172:175],v[220:223],a[8:11]
)A"
R"A(v_mfma_f32_16x16x32_bf16 a[12:15],a[172:175],v[252:255],a[12:15]
ds_read_b64_tr_b16 a[172:173],v1 offset:30976
ds_read_b64_tr_b16 a[174:175],v1 offset:36096
v_mfma_f32_16x16x32_bf16 a[12:15],a[168:171],v[248:251],a[12:15]
buffer_load_dwordx4 v[208:211],v6,s[28:31],s78 offen offset:2048 sc1
v_mfma_f32_16x16x32_bf16 a[12:15],a[164:167],v[236:239],a[12:15]
s_add_u32 m0,s82,86016
ds_read_b64_tr_b16 a[164:165],v1 offset:10496
ds_read_b64_tr_b16 a[166:167],v1 offset:15616
v_mfma_f32_16x16x32_bf16 a[12:15],a[160:163],v[232:235],a[12:15]
ds_read_b64_tr_b16 a[160:161],v1 offset:256
v_mfma_f32_16x16x32_bf16 a[16:19],a[176:179],v[136:139],a[16:19]
s_waitcnt lgkmcnt(5)
ds_read_b64_tr_b16 a[162:163],v1 offset:5376
v_mfma_f32_16x16x32_bf16 a[16:19],a[180:183],v[140:143],a[16:19]
buffer_store_dwordx4 v[232:235],v6,s[36:39],s87 offen sc1
v_mfma_f32_16x16x32_bf16 a[16:19],a[184:187],v[152:155],a[16:19]
ds_read_b64_tr_b16 a[168:169],v1 offset:20736
ds_read_b64_tr_b16 a[170:171],v1 offset:25856
v_mfma_f32_16x16x32_bf16 a[16:19],a[188:191],v[156:159],a[16:19]
v_add_u32 v1,40960,v1
v_mfma_f32_16x16x32_bf16 a[20:23],a[188:191],v[188:191],a[20:23]
ds_read_b64_tr_b16 a[96:97],v1 offset:128
ds_read_b64_tr_b16 a[98:99],v1 offset:5248
v_mfma_f32_16x16x32_bf16 a[20:23],a[184:187],v[184:187],a[20:23]
buffer_load_dwordx4 v[240:243],v6,s[28:31],s78 offen offset:3072 sc1
v_mfma_f32_16x16x32_bf16 a[20:23],a[180:183],v[172:175],a[20:23]
ds_read_b64_tr_b16 a[100:101],v1 offset:10368
ds_read_b64_tr_b16 a[102:103],v1 offset:15488
v_mfma_f32_16x16x32_bf16 a[20:23],a[176:179],v[168:171],a[20:23]
ds_read_b64_tr_b16 a[104:105],v1 offset:20608
v_mfma_f32_16x16x32_bf16 a[24:27],a[176:179],v[200:203],a[24:27]
ds_read_b64_tr_b16 a[106:107],v1 offset:25728
v_mfma_f32_16x16x32_bf16 a[24:27],a[180:183],v[204:207],a[24:27]
buffer_load_dwordx4 v11,s[72:75],s80 offen lds
v_mfma_f32_16x16x32_bf16 a[24:27],a[184:187],v[216:219],a[24:27]
ds_read_b64_tr_b16 a[108:109],v1 offset:30848
ds_read_b64_tr_b16 a[110:111],v1 offset:35968
v_mfma_f32_16x16x32_bf16 a[24:27],a[188:191],v[220:223],a[24:27]
v_mfma_f32_16x16x32_bf16 a[28:31],a[188:191],v[252:255],a[28:31]
ds_read_b64_tr_b16 a[188:189],v2 offset:31008
ds_read_b64_tr_b16 a[190:191],v2 offset:36128
v_mfma_f32_16x16x32_bf16 a[28:31],a[184:187],v[248:251],a[28:31]
s_add_u32 m0,s82,87040
buffer_load_dwordx4 v[148:151],v6,s[28:31],s79 offen sc1
v_mfma_f32_16x16x32_bf16 a[28:31],a[180:183],v[236:239],a[28:31]
ds_read_b64_tr_b16 a[180:181],v2 offset:10528
ds_read_b64_tr_b16 a[182:183],v2 offset:15648
v_mfma_f32_16x16x32_bf16 a[28:31],a[176:179],v[232:235],a[28:31]
ds_read_b64_tr_b16 a[176:177],v2 offset:288
v_mfma_f32_16x16x32_bf16 a[32:35],a[128:131],v[136:139],a[32:35]
ds_read_b64_tr_b16 a[178:179],v2 offset:5408
v_mfma_f32_16x16x32_bf16 a[32:35],a[132:135],v[140:143],a[32:35]
buffer_store_dwordx4 v[236:239],v6,s[36:39],s87 offen offset:1024 sc1
v_mfma_f32_16x16x32_bf16 a[32:35],a[136:139],v[152:155],a[32:35]
ds_read_b64_tr_b16 a[184:185],v2 offset:20768
ds_read_b64_tr_b16 a[186:187],v2 offset:25888
v_mfma_f32_16x16x32_bf16 a[32:35],a[140:143],v[156:159],a[32:35]
v_add_u32 v2,40960,v2
v_mfma_f32_16x16x32_bf16 a[36:39],a[140:143],v[188:191],a[36:39]
ds_read_b64_tr_b16 a[112:113],v2 offset:160
ds_read_b64_tr_b16 a[114:115],v2 offset:5280
v_mfma_f32_16x16x32_bf16 a[36:39],a[136:139],v[184:187],a[36:39]
buffer_load_dwordx4 v[180:183],v6,s[28:31],s79 offen offset:1024 sc1
v_mfma_f32_16x16x32_bf16 a[36:39],a[132:135],v[172:175],a[36:39]
ds_read_b64_tr_b16 a[116:117],v2 offset:10400
ds_read_b64_tr_b16 a[118:119],v2 offset:15520
v_mfma_f32_16x16x32_bf16 a[36:39],a[128:131],v[168:171],a[36:39]
ds_read_b64_tr_b16 a[120:121],v2 offset:20640
v_mfma_f32_16x16x32_bf16 a[40:43],a[128:131],v[200:203],a[40:43]
ds_read_b64_tr_b16 a[122:123],v2 offset:25760
v_mfma_f32_16x16x32_bf16 a[40:43],a[132:135],v[204:207],a[40:43]
buffer_load_dwordx4 v7,s[72:75],s83 offen lds
v_mfma_f32_16x16x32_bf16 a[40:43],a[136:139],v[216:219],a[40:43]
ds_read_b64_tr_b16 a[124:125],v2 offset:30880
ds_read_b64_tr_b16 a[126:127],v2 offset:36000
v_mfma_f32_16x16x32_bf16 a[40:43],a[140:143],v[220:223],a[40:43]
v_mfma_f32_16x16x32_bf16 a[44:47],a[140:143],v[252:255],a[44:47]
ds_read_b64_tr_b16 a[140:141],v1 offset:30720
ds_read_b64_tr_b16 a[142:143],v1 offset:35840
v_mfma_f32_16x16x32_bf16 a[44:47],a[136:139],v[248:251],a[44:47]
buffer_load_dwordx4 v[212:215],v6,s[28:31],s79 offen offset:2048 sc1
v_mfma_f32_16x16x32_bf16 a[44:47],a[132:135],v[236:239],a[44:47]
ds_read_b64_tr_b16 a[132:133],v1 offset:10240
ds_read_b64_tr_b16 a[134:135],v1 offset:15360
v_mfma_f32_16x16x32_bf16 a[44:47],a[128:131],v[232:235],a[44:47]
ds_read_b64_tr_b16 a[128:129],v1
v_mfma_f32_16x16x32_bf16 a[48:51],a[144:147],v[136:139],a[48:51]
ds_read_b64_tr_b16 a[130:131],v1 offset:5120
v_mfma_f32_16x16x32_bf16 a[48:51],a[148:151],v[140:143],a[48:51]
buffer_load_dwordx4 v[244:247],v6,s[28:31],s79 offen offset:3072 sc1
v_mfma_f32_16x16x32_bf16 a[48:51],a[152:155],v[152:155],a[48:51]
ds_read_b64_tr_b16 a[136:137],v1 offset:20480
ds_read_b64_tr_b16 a[138:139],v1 offset:25600
v_mfma_f32_16x16x32_bf16 a[48:51],a[156:159],v[156:159],a[48:51]
s_cmp_ge_u32 1,s71
s_cselect_b32 s76,4,2
v_mfma_f32_16x16x32_bf16 a[52:55],a[156:159],v[188:191],a[52:55]
s_bitcmp1_b32 s54,s76
s_cselect_b32 s77,8192,0
s_lshl_b32 s76,s76,17
v_mfma_f32_16x16x32_bf16 a[52:55],a[152:155],v[184:187],a[52:55]
s_add_u32 s76,s76,s77
buffer_load_dwordx4 v8,s[72:75],s83 offen offset:1024 lds
v_mfma_f32_16x16x32_bf16 a[52:55],a[148:151],v[172:175],a[52:55]
s_add_u32 s77,s76,4096
s_add_u32 s78,s76,16384
s_add_u32 s79,s76,20480
v_mfma_f32_16x16x32_bf16 a[52:55],a[144:147],v[168:171],a[52:55]
s_max_i32 s76,s76,s18
s_max_i32 s77,s77,s19
s_max_i32 s78,s78,s100
v_mfma_f32_16x16x32_bf16 a[56:59],a[144:147],v[200:203],a[56:59]
s_max_i32 s79,s79,s101
v_mfma_f32_16x16x32_bf16 a[56:59],a[148:151],v[204:207],a[56:59]
buffer_store_dwordx4 v[248:251],v6,s[36:39],s87 offen offset:2048 sc1
v_mfma_f32_16x16x32_bf16 a[56:59],a[152:155],v[216:219],a[56:59]
v_mfma_f32_16x16x32_bf16 a[56:59],a[156:159],v[220:223],a[56:59]
v_mfma_f32_16x16x32_bf16 a[60:63],a[156:159],v[252:255],a[60:63]
ds_read_b64_tr_b16 a[156:157],v2 offset:30752
ds_read_b64_tr_b16 a[158:159],v2 offset:35872
v_mfma_f32_16x16x32_bf16 a[60:63],a[152:155],v[248:251],a[60:63]
buffer_store_dwordx4 v[252:255],v6,s[36:39],s87 offen offset:3072 sc1
v_mfma_f32_16x16x32_bf16 a[60:63],a[148:151],v[236:239],a[60:63]
ds_read_b64_tr_b16 a[148:149],v2 offset:10272
ds_read_b64_tr_b16 a[150:151],v2 offset:15392
v_mfma_f32_16x16x32_bf16 a[60:63],a[144:147],v[232:235],a[60:63]
s_waitcnt lgkmcnt(4)
ds_read_b64_tr_b16 a[144:145],v2 offset:32
v_mfma_f32_16x16x32_bf16 a[64:67],a[160:163],v[136:139],a[64:67]
ds_read_b64_tr_b16 a[146:147],v2 offset:5152
v_mfma_f32_16x16x32_bf16 a[64:67],a[164:167],v[140:143],a[64:67]
buffer_load_dwordx4 v9,s[72:75],s83 offen offset:2048 lds
v_mfma_f32_16x16x32_bf16 a[64:67],a[168:171],v[152:155],a[64:67]
ds_read_b64_tr_b16 a[152:153],v2 offset:20512
ds_read_b64_tr_b16 a[154:155],v2 offset:25632
v_mfma_f32_16x16x32_bf16 a[64:67],a[172:175],v[156:159],a[64:67]
v_mfma_f32_16x16x32_bf16 a[68:71],a[172:175],v[188:191],a[68:71]
v_mfma_f32_16x16x32_bf16 a[68:71],a[168:171],v[184:187],a[68:71]
v_mfma_f32_16x16x32_bf16 a[68:71],a[164:167],v[172:175],a[68:71]
v_mfma_f32_16x16x32_bf16 a[68:71],a[160:163],v[168:171],a[68:71]
v_mfma_f32_16x16x32_bf16 a[72:75],a[160:163],v[200:203],a[72:75]
v_mfma_f32_16x16x32_bf16 a[72:75],a[164:167],v[204:207],a[72:75]
v_mfma_f32_16x16x32_bf16 a[72:75],a[168:171],v[216:219],a[72:75]
v_mfma_f32_16x16x32_bf16 a[72:75],a[172:175],v[220:223],a[72:75]
buffer_load_dwordx4 v[64:67],v6,s[28:31],s76 offen sc1
v_mfma_f32_16x16x32_bf16 a[76:79],a[172:175],v[252:255],a[76:79]
ds_read_b64_tr_b16 a[172:173],v1 offset:30784
ds_read_b64_tr_b16 a[174:175],v1 offset:35904
v_mfma_f32_16x16x32_bf16 a[76:79],a[168:171],v[248:251],a[76:79]
ds_read_b64_tr_b16 a[168:169],v1 offset:20544
v_mfma_f32_16x16x32_bf16 a[76:79],a[164:167],v[236:239],a[76:79]
ds_read_b64_tr_b16 a[164:165],v1 offset:10304
v_mfma_f32_16x16x32_bf16 a[76:79],a[160:163],v[232:235],a[76:79]
buffer_load_dwordx4 v10,s[72:75],s83 offen offset:3072 lds
v_mfma_f32_16x16x32_bf16 a[80:83],a[176:179],v[136:139],a[80:83]
ds_read_b64_tr_b16 a[160:161],v1 offset:64
ds_read_b64_tr_b16 a[162:163],v1 offset:5184
v_mfma_f32_16x16x32_bf16 a[80:83],a[180:183],v[140:143],a[80:83]
ds_read_b64_tr_b16 a[166:167],v1 offset:15424
v_mfma_f32_16x16x32_bf16 a[80:83],a[184:187],v[152:155],a[80:83]
ds_read_b64_tr_b16 a[170:171],v1 offset:25664
v_mfma_f32_16x16x32_bf16 a[80:83],a[188:191],v[156:159],a[80:83]
buffer_load_dwordx4 v[68:71],v6,s[28:31],s76 offen offset:1024 sc1
v_mfma_f32_16x16x32_bf16 a[84:87],a[188:191],v[188:191],a[84:87]
v_mfma_f32_16x16x32_bf16 a[84:87],a[184:187],v[184:187],a[84:87]
v_mfma_f32_16x16x32_bf16 a[84:87],a[180:183],v[172:175],a[84:87]
v_mfma_f32_16x16x32_bf16 a[84:87],a[176:179],v[168:171],a[84:87]
v_mfma_f32_16x16x32_bf16 a[88:91],a[176:179],v[200:203],a[88:91]
v_mfma_f32_16x16x32_bf16 a[88:91],a[180:183],v[204:207],a[88:91]
s_add_u32 m0,s82,91136
v_mfma_f32_16x16x32_bf16 a[88:91],a[184:187],v[216:219],a[88:91]
v_mfma_f32_16x16x32_bf16 a[88:91],a[188:191],v[220:223],a[88:91]
buffer_load_dwordx4 v[72:75],v6,s[28:31],s76 offen offset:2048 sc1
v_mfma_f32_16x16x32_bf16 a[92:95],a[188:191],v[252:255],a[92:95]
ds_read_b64_tr_b16 a[188:189],v2 offset:30816
ds_read_b64_tr_b16 a[190:191],v2 offset:35936
v_mfma_f32_16x16x32_bf16 a[92:95],a[184:187],v[248:251],a[92:95]
ds_read_b64_tr_b16 a[184:185],v2 offset:20576
v_mfma_f32_16x16x32_bf16 a[92:95],a[180:183],v[236:239],a[92:95]
s_cmp_ge_u32 0,s71
s_cselect_b32 s80,3,1
s_mul_i32 s80,s80,163840
v_mfma_f32_16x16x32_bf16 a[92:95],a[176:179],v[232:235],a[92:95]
s_add_u32 s80,s80,s81
buffer_load_dwordx4 v11,s[72:75],s83 offen lds
s_waitcnt vmcnt(15)
v_mfma_f32_16x16x32_bf16 a[192:195],a[128:131],v[128:131],a[192:195]
s_add_u32 s83,s80,20480
ds_read_b64_tr_b16 a[176:177],v2 offset:96
ds_read_b64_tr_b16 a[178:179],v2 offset:5216
v_mfma_f32_16x16x32_bf16 a[192:195],a[132:135],v[160:163],a[192:195]
ds_read_b64_tr_b16 a[180:181],v2 offset:10336
v_mfma_f32_16x16x32_bf16 a[192:195],a[136:139],v[192:195],a[192:195]
ds_read_b64_tr_b16 a[182:183],v2 offset:15456
v_mfma_f32_16x16x32_bf16 a[192:195],a[140:143],v[224:227],a[192:195]
buffer_load_dwordx4 v[76:79],v6,s[28:31],s76 offen offset:3072 sc1
v_mfma_f32_16x16x32_bf16 a[196:199],a[140:143],v[228:231],a[196:199]
ds_read_b64_tr_b16 a[186:187],v2 offset:25696
v_mfma_f32_16x16x32_bf16 a[196:199],a[136:139],v[196:199],a[196:199]
v_mfma_f32_16x16x32_bf16 a[196:199],a[132:135],v[164:167],a[196:199]
v_mfma_f32_16x16x32_bf16 a[196:199],a[128:131],v[132:135],a[196:199]
v_mfma_f32_16x16x32_bf16 a[200:203],a[128:131],v[144:147],a[200:203]
v_mfma_f32_16x16x32_bf16 a[200:203],a[132:135],v[176:179],a[200:203]
v_mfma_f32_16x16x32_bf16 a[200:203],a[136:139],v[208:211],a[200:203]
v_mfma_f32_16x16x32_bf16 a[200:203],a[140:143],v[240:243],a[200:203]
s_lshl_b32 s95,s42,2
buffer_load_dwordx4 v[80:83],v6,s[28:31],s77 offen sc1
s_waitcnt vmcnt(11)
v_mfma_f32_16x16x32_bf16 a[204:207],a[140:143],v[244:247],a[204:207]
s_add_u32 s95,s95,s40
s_lshl_b32 s95,s95,4
v_bfe_u32 v19,s55,13,1
v_mfma_f32_16x16x32_bf16 a[204:207],a[136:139],v[212:215],a[204:207]
v_mov_b32 v18,s68
v_or_b32 v19,s69,v19
v_mfma_f32_16x16x32_bf16 a[204:207],a[132:135],v[180:183],a[204:207]
ds_read_b64_tr_b16 a[132:133],v1 offset:10432
ds_read_b64_tr_b16 a[134:135],v1 offset:15552
v_mfma_f32_16x16x32_bf16 a[204:207],a[128:131],v[148:151],a[204:207]
s_barrier
ds_read_b64_tr_b16 a[128:129],v1 offset:192
ds_read_b64_tr_b16 a[130:131],v1 offset:5312
s_waitcnt lgkmcnt(4)
v_mfma_f32_16x16x32_bf16 a[208:211],a[144:147],v[128:131],a[208:211]
ds_read_b64_tr_b16 a[136:137],v1 offset:20672
v_mfma_f32_16x16x32_bf16 a[208:211],a[148:151],v[160:163],a[208:211]
ds_read_b64_tr_b16 a[138:139],v1 offset:25792
v_mfma_f32_16x16x32_bf16 a[208:211],a[152:155],v[192:195],a[208:211]
s_add_u32 m0,s82,0
)A"
R"A(s_waitcnt vmcnt(8)
s_mov_b64 exec,1
buffer_store_dwordx2 v[18:19],v6,s[64:67],s95 offen offset:1024 sc1
s_mov_b64 exec,-1
v_mfma_f32_16x16x32_bf16 a[208:211],a[156:159],v[224:227],a[208:211]
ds_read_b64_tr_b16 a[140:141],v1 offset:30912
ds_read_b64_tr_b16 a[142:143],v1 offset:36032
v_mfma_f32_16x16x32_bf16 a[212:215],a[156:159],v[228:231],a[212:215]
v_mfma_f32_16x16x32_bf16 a[212:215],a[152:155],v[196:199],a[212:215]
v_mfma_f32_16x16x32_bf16 a[212:215],a[148:151],v[164:167],a[212:215]
buffer_load_dwordx4 v[84:87],v6,s[28:31],s77 offen offset:1024 sc1
v_mfma_f32_16x16x32_bf16 a[212:215],a[144:147],v[132:135],a[212:215]
v_mfma_f32_16x16x32_bf16 a[216:219],a[144:147],v[144:147],a[216:219]
v_mfma_f32_16x16x32_bf16 a[216:219],a[148:151],v[176:179],a[216:219]
v_mfma_f32_16x16x32_bf16 a[216:219],a[152:155],v[208:211],a[216:219]
buffer_load_dwordx4 v7,s[72:75],s80 offen lds
v_mfma_f32_16x16x32_bf16 a[216:219],a[156:159],v[240:243],a[216:219]
v_mfma_f32_16x16x32_bf16 a[220:223],a[156:159],v[244:247],a[220:223]
ds_read_b64_tr_b16 a[156:157],v2 offset:30944
ds_read_b64_tr_b16 a[158:159],v2 offset:36064
v_mfma_f32_16x16x32_bf16 a[220:223],a[152:155],v[212:215],a[220:223]
ds_read_b64_tr_b16 a[152:153],v2 offset:20704
v_mfma_f32_16x16x32_bf16 a[220:223],a[148:151],v[180:183],a[220:223]
buffer_load_dwordx4 v[88:91],v6,s[28:31],s77 offen offset:2048 sc1
v_mfma_f32_16x16x32_bf16 a[220:223],a[144:147],v[148:151],a[220:223]
ds_read_b64_tr_b16 a[144:145],v2 offset:224
ds_read_b64_tr_b16 a[146:147],v2 offset:5344
v_mfma_f32_16x16x32_bf16 a[224:227],a[160:163],v[128:131],a[224:227]
ds_read_b64_tr_b16 a[148:149],v2 offset:10464
v_mfma_f32_16x16x32_bf16 a[224:227],a[164:167],v[160:163],a[224:227]
ds_read_b64_tr_b16 a[150:151],v2 offset:15584
v_mfma_f32_16x16x32_bf16 a[224:227],a[168:171],v[192:195],a[224:227]
buffer_load_dwordx4 v[92:95],v6,s[28:31],s77 offen offset:3072 sc1
v_mfma_f32_16x16x32_bf16 a[224:227],a[172:175],v[224:227],a[224:227]
ds_read_b64_tr_b16 a[154:155],v2 offset:25824
v_mfma_f32_16x16x32_bf16 a[228:231],a[172:175],v[228:231],a[228:231]
v_mfma_f32_16x16x32_bf16 a[228:231],a[168:171],v[196:199],a[228:231]
v_mfma_f32_16x16x32_bf16 a[228:231],a[164:167],v[164:167],a[228:231]
buffer_load_dwordx4 v8,s[72:75],s80 offen offset:1024 lds
v_mfma_f32_16x16x32_bf16 a[228:231],a[160:163],v[132:135],a[228:231]
v_mfma_f32_16x16x32_bf16 a[232:235],a[160:163],v[144:147],a[232:235]
v_mfma_f32_16x16x32_bf16 a[232:235],a[164:167],v[176:179],a[232:235]
v_mfma_f32_16x16x32_bf16 a[232:235],a[168:171],v[208:211],a[232:235]
buffer_load_dwordx4 v[96:99],v6,s[28:31],s78 offen sc1
v_mfma_f32_16x16x32_bf16 a[232:235],a[172:175],v[240:243],a[232:235]
v_mfma_f32_16x16x32_bf16 a[236:239],a[172:175],v[244:247],a[236:239]
ds_read_b64_tr_b16 a[172:173],v1 offset:30976
ds_read_b64_tr_b16 a[174:175],v1 offset:36096
v_mfma_f32_16x16x32_bf16 a[236:239],a[168:171],v[212:215],a[236:239]
ds_read_b64_tr_b16 a[168:169],v1 offset:20736
v_mfma_f32_16x16x32_bf16 a[236:239],a[164:167],v[180:183],a[236:239]
ds_read_b64_tr_b16 a[164:165],v1 offset:10496
v_mfma_f32_16x16x32_bf16 a[236:239],a[160:163],v[148:151],a[236:239]
ds_read_b64_tr_b16 a[160:161],v1 offset:256
v_mfma_f32_16x16x32_bf16 a[240:243],a[176:179],v[128:131],a[240:243]
ds_read_b64_tr_b16 a[162:163],v1 offset:5376
ds_read_b64_tr_b16 a[166:167],v1 offset:15616
v_mfma_f32_16x16x32_bf16 a[240:243],a[180:183],v[160:163],a[240:243]
ds_read_b64_tr_b16 a[170:171],v1 offset:25856
v_add_u32 v1,81920,v1
v_mfma_f32_16x16x32_bf16 a[240:243],a[184:187],v[192:195],a[240:243]
buffer_load_dwordx4 v[100:103],v6,s[28:31],s78 offen offset:1024 sc1
v_mfma_f32_16x16x32_bf16 a[240:243],a[188:191],v[224:227],a[240:243]
v_mfma_f32_16x16x32_bf16 a[244:247],a[188:191],v[228:231],a[244:247]
v_mfma_f32_16x16x32_bf16 a[244:247],a[184:187],v[196:199],a[244:247]
v_mfma_f32_16x16x32_bf16 a[244:247],a[180:183],v[164:167],a[244:247]
buffer_load_dwordx4 v9,s[72:75],s80 offen offset:2048 lds
v_mfma_f32_16x16x32_bf16 a[244:247],a[176:179],v[132:135],a[244:247]
v_mfma_f32_16x16x32_bf16 a[248:251],a[176:179],v[144:147],a[248:251]
v_mfma_f32_16x16x32_bf16 a[248:251],a[180:183],v[176:179],a[248:251]
v_mfma_f32_16x16x32_bf16 a[248:251],a[184:187],v[208:211],a[248:251]
buffer_load_dwordx4 v[104:107],v6,s[28:31],s78 offen offset:2048 sc1
v_mfma_f32_16x16x32_bf16 a[248:251],a[188:191],v[240:243],a[248:251]
v_mfma_f32_16x16x32_bf16 a[252:255],a[188:191],v[244:247],a[252:255]
ds_read_b64_tr_b16 a[188:189],v2 offset:31008
ds_read_b64_tr_b16 a[190:191],v2 offset:36128
v_mfma_f32_16x16x32_bf16 a[252:255],a[184:187],v[212:215],a[252:255]
ds_read_b64_tr_b16 a[184:185],v2 offset:20768
v_mfma_f32_16x16x32_bf16 a[252:255],a[180:183],v[180:183],a[252:255]
ds_read_b64_tr_b16 a[180:181],v2 offset:10528
v_mfma_f32_16x16x32_bf16 a[252:255],a[176:179],v[148:151],a[252:255]
ds_read_b64_tr_b16 a[176:177],v2 offset:288
v_mfma_f32_16x16x32_bf16 a[0:3],a[96:99],v[128:131],a[0:3]
ds_read_b64_tr_b16 a[178:179],v2 offset:5408
ds_read_b64_tr_b16 a[182:183],v2 offset:15648
v_mfma_f32_16x16x32_bf16 a[0:3],a[100:103],v[160:163],a[0:3]
ds_read_b64_tr_b16 a[186:187],v2 offset:25888
v_add_u32 v2,81920,v2
v_mfma_f32_16x16x32_bf16 a[0:3],a[104:107],v[192:195],a[0:3]
buffer_load_dwordx4 v[108:111],v6,s[28:31],s78 offen offset:3072 sc1
v_mfma_f32_16x16x32_bf16 a[0:3],a[108:111],v[224:227],a[0:3]
v_mfma_f32_16x16x32_bf16 a[4:7],a[108:111],v[228:231],a[4:7]
v_mfma_f32_16x16x32_bf16 a[4:7],a[104:107],v[196:199],a[4:7]
v_mfma_f32_16x16x32_bf16 a[4:7],a[100:103],v[164:167],a[4:7]
buffer_load_dwordx4 v10,s[72:75],s80 offen offset:3072 lds
v_mfma_f32_16x16x32_bf16 a[4:7],a[96:99],v[132:135],a[4:7]
v_mfma_f32_16x16x32_bf16 a[8:11],a[96:99],v[144:147],a[8:11]
v_mfma_f32_16x16x32_bf16 a[8:11],a[100:103],v[176:179],a[8:11]
v_mfma_f32_16x16x32_bf16 a[8:11],a[104:107],v[208:211],a[8:11]
buffer_load_dwordx4 v[112:115],v6,s[28:31],s79 offen sc1
v_mfma_f32_16x16x32_bf16 a[8:11],a[108:111],v[240:243],a[8:11]
v_mfma_f32_16x16x32_bf16 a[12:15],a[108:111],v[244:247],a[12:15]
ds_read_b64_tr_b16 a[108:109],v1 offset:30720
ds_read_b64_tr_b16 a[110:111],v1 offset:35840
v_mfma_f32_16x16x32_bf16 a[12:15],a[104:107],v[212:215],a[12:15]
ds_read_b64_tr_b16 a[104:105],v1 offset:20480
v_mfma_f32_16x16x32_bf16 a[12:15],a[100:103],v[180:183],a[12:15]
s_add_u32 m0,s82,4096
ds_read_b64_tr_b16 a[100:101],v1 offset:10240
v_mfma_f32_16x16x32_bf16 a[12:15],a[96:99],v[148:151],a[12:15]
ds_read_b64_tr_b16 a[96:97],v1
v_mfma_f32_16x16x32_bf16 a[16:19],a[112:115],v[128:131],a[16:19]
ds_read_b64_tr_b16 a[98:99],v1 offset:5120
ds_read_b64_tr_b16 a[102:103],v1 offset:15360
v_mfma_f32_16x16x32_bf16 a[16:19],a[116:119],v[160:163],a[16:19]
buffer_load_dwordx4 v11,s[72:75],s80 offen lds
v_mfma_f32_16x16x32_bf16 a[16:19],a[120:123],v[192:195],a[16:19]
ds_read_b64_tr_b16 a[106:107],v1 offset:25600
v_mfma_f32_16x16x32_bf16 a[16:19],a[124:127],v[224:227],a[16:19]
v_mfma_f32_16x16x32_bf16 a[20:23],a[124:127],v[228:231],a[20:23]
v_mfma_f32_16x16x32_bf16 a[20:23],a[120:123],v[196:199],a[20:23]
buffer_load_dwordx4 v[116:119],v6,s[28:31],s79 offen offset:1024 sc1
v_mfma_f32_16x16x32_bf16 a[20:23],a[116:119],v[164:167],a[20:23]
v_mfma_f32_16x16x32_bf16 a[20:23],a[112:115],v[132:135],a[20:23]
v_mfma_f32_16x16x32_bf16 a[24:27],a[112:115],v[144:147],a[24:27]
v_mfma_f32_16x16x32_bf16 a[24:27],a[116:119],v[176:179],a[24:27]
v_mfma_f32_16x16x32_bf16 a[24:27],a[120:123],v[208:211],a[24:27]
buffer_load_dwordx4 v[120:123],v6,s[28:31],s79 offen offset:2048 sc1
v_mfma_f32_16x16x32_bf16 a[24:27],a[124:127],v[240:243],a[24:27]
v_mfma_f32_16x16x32_bf16 a[28:31],a[124:127],v[244:247],a[28:31]
ds_read_b64_tr_b16 a[124:125],v2 offset:30752
ds_read_b64_tr_b16 a[126:127],v2 offset:35872
v_mfma_f32_16x16x32_bf16 a[28:31],a[120:123],v[212:215],a[28:31]
s_add_u32 m0,s82,5120
ds_read_b64_tr_b16 a[120:121],v2 offset:20512
v_mfma_f32_16x16x32_bf16 a[28:31],a[116:119],v[180:183],a[28:31]
ds_read_b64_tr_b16 a[116:117],v2 offset:10272
v_mfma_f32_16x16x32_bf16 a[28:31],a[112:115],v[148:151],a[28:31]
s_cmp_ge_u32 2,s71
s_cselect_b32 s76,6,4
ds_read_b64_tr_b16 a[112:113],v2 offset:32
s_waitcnt lgkmcnt(5)
v_mfma_f32_16x16x32_bf16 a[32:35],a[128:131],v[128:131],a[32:35]
buffer_load_dwordx4 v7,s[72:75],s83 offen lds
v_mfma_f32_16x16x32_bf16 a[32:35],a[132:135],v[160:163],a[32:35]
s_bitcmp1_b32 s54,s76
s_cselect_b32 s77,8192,0
s_lshl_b32 s76,s76,17
v_mfma_f32_16x16x32_bf16 a[32:35],a[136:139],v[192:195],a[32:35]
s_add_u32 s76,s76,s77
s_add_u32 s77,s76,4096
s_add_u32 s78,s76,16384
v_mfma_f32_16x16x32_bf16 a[32:35],a[140:143],v[224:227],a[32:35]
s_max_i32 s77,s77,s19
s_max_i32 s78,s78,s100
ds_read_b64_tr_b16 a[114:115],v2 offset:5152
v_mfma_f32_16x16x32_bf16 a[36:39],a[140:143],v[228:231],a[36:39]
buffer_load_dwordx4 v[124:127],v6,s[28:31],s79 offen offset:3072 sc1
v_mfma_f32_16x16x32_bf16 a[36:39],a[136:139],v[196:199],a[36:39]
s_add_u32 s79,s76,20480
s_max_i32 s76,s76,s18
ds_read_b64_tr_b16 a[118:119],v2 offset:15392
v_mfma_f32_16x16x32_bf16 a[36:39],a[132:135],v[164:167],a[36:39]
s_max_i32 s79,s79,s101
ds_read_b64_tr_b16 a[122:123],v2 offset:25632
v_mfma_f32_16x16x32_bf16 a[36:39],a[128:131],v[132:135],a[36:39]
v_mfma_f32_16x16x32_bf16 a[40:43],a[128:131],v[144:147],a[40:43]
v_mfma_f32_16x16x32_bf16 a[40:43],a[132:135],v[176:179],a[40:43]
v_mfma_f32_16x16x32_bf16 a[40:43],a[136:139],v[208:211],a[40:43]
v_mfma_f32_16x16x32_bf16 a[40:43],a[140:143],v[240:243],a[40:43]
v_mfma_f32_16x16x32_bf16 a[44:47],a[140:143],v[244:247],a[44:47]
ds_read_b64_tr_b16 a[140:141],v1 offset:30784
ds_read_b64_tr_b16 a[142:143],v1 offset:35904
v_mfma_f32_16x16x32_bf16 a[44:47],a[136:139],v[212:215],a[44:47]
ds_read_b64_tr_b16 a[136:137],v1 offset:20544
v_mfma_f32_16x16x32_bf16 a[44:47],a[132:135],v[180:183],a[44:47]
ds_read_b64_tr_b16 a[132:133],v1 offset:10304
v_mfma_f32_16x16x32_bf16 a[44:47],a[128:131],v[148:151],a[44:47]
buffer_load_dwordx4 v8,s[72:75],s83 offen offset:1024 lds
v_mfma_f32_16x16x32_bf16 a[48:51],a[144:147],v[128:131],a[48:51]
ds_read_b64_tr_b16 a[128:129],v1 offset:64
ds_read_b64_tr_b16 a[130:131],v1 offset:5184
v_mfma_f32_16x16x32_bf16 a[48:51],a[148:151],v[160:163],a[48:51]
ds_read_b64_tr_b16 a[134:135],v1 offset:15424
v_mfma_f32_16x16x32_bf16 a[48:51],a[152:155],v[192:195],a[48:51]
ds_read_b64_tr_b16 a[138:139],v1 offset:25664
v_mfma_f32_16x16x32_bf16 a[48:51],a[156:159],v[224:227],a[48:51]
s_lshl_b32 s94,s70,4
v_bfe_u32 v24,v6,4,2
v_min_u32 v24,2,v24
v_mfma_f32_16x16x32_bf16 a[52:55],a[156:159],v[228:231],a[52:55]
s_add_u32 s94,s94,s40
v_add_u32 v24,s71,v24
v_add_u32 v24,1,v24
v_mfma_f32_16x16x32_bf16 a[52:55],a[152:155],v[196:199],a[52:55]
s_lshl_b32 s94,s94,4
v_and_b32 v24,3,v24
v_lshlrev_b32 v24,6,v24
v_mfma_f32_16x16x32_bf16 a[52:55],a[148:151],v[164:167],a[52:55]
v_mfma_f32_16x16x32_bf16 a[52:55],a[144:147],v[132:135],a[52:55]
buffer_load_dwordx4 v[136:139],v6,s[28:31],s76 offen sc1
v_mfma_f32_16x16x32_bf16 a[56:59],a[144:147],v[144:147],a[56:59]
v_mfma_f32_16x16x32_bf16 a[56:59],a[148:151],v[176:179],a[56:59]
v_mfma_f32_16x16x32_bf16 a[56:59],a[152:155],v[208:211],a[56:59]
v_mfma_f32_16x16x32_bf16 a[56:59],a[156:159],v[240:243],a[56:59]
v_mfma_f32_16x16x32_bf16 a[60:63],a[156:159],v[244:247],a[60:63]
ds_read_b64_tr_b16 a[156:157],v2 offset:30816
ds_read_b64_tr_b16 a[158:159],v2 offset:35936
v_mfma_f32_16x16x32_bf16 a[60:63],a[152:155],v[212:215],a[60:63]
buffer_load_dwordx4 v[140:143],v6,s[28:31],s76 offen offset:1024 sc1
v_mfma_f32_16x16x32_bf16 a[60:63],a[148:151],v[180:183],a[60:63]
ds_read_b64_tr_b16 a[148:149],v2 offset:10336
ds_read_b64_tr_b16 a[150:151],v2 offset:15456
v_mfma_f32_16x16x32_bf16 a[60:63],a[144:147],v[148:151],a[60:63]
ds_read_b64_tr_b16 a[144:145],v2 offset:96
v_mfma_f32_16x16x32_bf16 a[64:67],a[160:163],v[128:131],a[64:67]
ds_read_b64_tr_b16 a[146:147],v2 offset:5216
v_mfma_f32_16x16x32_bf16 a[64:67],a[164:167],v[160:163],a[64:67]
buffer_load_dwordx4 v9,s[72:75],s83 offen offset:2048 lds
v_mfma_f32_16x16x32_bf16 a[64:67],a[168:171],v[192:195],a[64:67]
ds_read_b64_tr_b16 a[152:153],v2 offset:20576
ds_read_b64_tr_b16 a[154:155],v2 offset:25696
)A"
R"A(v_mfma_f32_16x16x32_bf16 a[64:67],a[172:175],v[224:227],a[64:67]
v_mfma_f32_16x16x32_bf16 a[68:71],a[172:175],v[228:231],a[68:71]
v_mfma_f32_16x16x32_bf16 a[68:71],a[168:171],v[196:199],a[68:71]
buffer_load_dwordx4 v[152:155],v6,s[28:31],s76 offen offset:2048 sc1
v_mfma_f32_16x16x32_bf16 a[68:71],a[164:167],v[164:167],a[68:71]
v_mfma_f32_16x16x32_bf16 a[68:71],a[160:163],v[132:135],a[68:71]
v_mfma_f32_16x16x32_bf16 a[72:75],a[160:163],v[144:147],a[72:75]
v_mfma_f32_16x16x32_bf16 a[72:75],a[164:167],v[176:179],a[72:75]
buffer_load_dwordx4 v[156:159],v6,s[28:31],s76 offen offset:3072 sc1
v_mfma_f32_16x16x32_bf16 a[72:75],a[168:171],v[208:211],a[72:75]
v_mfma_f32_16x16x32_bf16 a[72:75],a[172:175],v[240:243],a[72:75]
v_mfma_f32_16x16x32_bf16 a[76:79],a[172:175],v[244:247],a[76:79]
ds_read_b64_tr_b16 a[172:173],v1 offset:30848
ds_read_b64_tr_b16 a[174:175],v1 offset:35968
v_mfma_f32_16x16x32_bf16 a[76:79],a[168:171],v[212:215],a[76:79]
buffer_load_dwordx4 v10,s[72:75],s83 offen offset:3072 lds
v_mfma_f32_16x16x32_bf16 a[76:79],a[164:167],v[180:183],a[76:79]
ds_read_b64_tr_b16 a[164:165],v1 offset:10368
ds_read_b64_tr_b16 a[166:167],v1 offset:15488
v_mfma_f32_16x16x32_bf16 a[76:79],a[160:163],v[148:151],a[76:79]
ds_read_b64_tr_b16 a[160:161],v1 offset:128
v_mfma_f32_16x16x32_bf16 a[80:83],a[176:179],v[128:131],a[80:83]
ds_read_b64_tr_b16 a[162:163],v1 offset:5248
v_mfma_f32_16x16x32_bf16 a[80:83],a[180:183],v[160:163],a[80:83]
buffer_load_dwordx4 v[168:171],v6,s[28:31],s77 offen sc1
v_mfma_f32_16x16x32_bf16 a[80:83],a[184:187],v[192:195],a[80:83]
ds_read_b64_tr_b16 a[168:169],v1 offset:20608
ds_read_b64_tr_b16 a[170:171],v1 offset:25728
v_mfma_f32_16x16x32_bf16 a[80:83],a[188:191],v[224:227],a[80:83]
v_mfma_f32_16x16x32_bf16 a[84:87],a[188:191],v[228:231],a[84:87]
v_mfma_f32_16x16x32_bf16 a[84:87],a[184:187],v[196:199],a[84:87]
buffer_load_dwordx4 v[172:175],v6,s[28:31],s77 offen offset:1024 sc1
v_mfma_f32_16x16x32_bf16 a[84:87],a[180:183],v[164:167],a[84:87]
v_mfma_f32_16x16x32_bf16 a[84:87],a[176:179],v[132:135],a[84:87]
v_mfma_f32_16x16x32_bf16 a[88:91],a[176:179],v[144:147],a[88:91]
v_mfma_f32_16x16x32_bf16 a[88:91],a[180:183],v[176:179],a[88:91]
s_add_u32 m0,s82,9216
v_mfma_f32_16x16x32_bf16 a[88:91],a[184:187],v[208:211],a[88:91]
v_mfma_f32_16x16x32_bf16 a[88:91],a[188:191],v[240:243],a[88:91]
buffer_load_dwordx2 v[26:27],v24,s[64:67],s94 offen offset:1024 sc1
v_mfma_f32_16x16x32_bf16 a[92:95],a[188:191],v[244:247],a[92:95]
ds_read_b64_tr_b16 a[188:189],v2 offset:30880
ds_read_b64_tr_b16 a[190:191],v2 offset:36000
v_mfma_f32_16x16x32_bf16 a[92:95],a[184:187],v[212:215],a[92:95]
ds_read_b64_tr_b16 a[184:185],v2 offset:20640
v_mfma_f32_16x16x32_bf16 a[92:95],a[180:183],v[180:183],a[92:95]
ds_read_b64_tr_b16 a[180:181],v2 offset:10400
v_mfma_f32_16x16x32_bf16 a[92:95],a[176:179],v[148:151],a[92:95]
s_waitcnt vmcnt(13)
buffer_load_dwordx4 v[184:187],v6,s[28:31],s77 offen offset:2048 sc1
v_mfma_f32_16x16x32_bf16 a[192:195],a[96:99],v[64:67],a[192:195]
s_lshl_b32 s43,s41,9
ds_read_b64_tr_b16 a[176:177],v2 offset:160
ds_read_b64_tr_b16 a[178:179],v2 offset:5280
v_mfma_f32_16x16x32_bf16 a[192:195],a[100:103],v[68:71],a[192:195]
s_mul_i32 s44,s71,12
s_add_u32 s43,s43,s44
ds_read_b64_tr_b16 a[182:183],v2 offset:15520
v_mfma_f32_16x16x32_bf16 a[192:195],a[104:107],v[72:75],a[192:195]
s_add_u32 s43,s43,s40
s_add_u32 s43,s43,64
ds_read_b64_tr_b16 a[186:187],v2 offset:25760
v_mfma_f32_16x16x32_bf16 a[192:195],a[108:111],v[76:79],a[192:195]
s_lshl_b32 s43,s43,14
buffer_load_dwordx4 v11,s[72:75],s83 offen lds
v_mfma_f32_16x16x32_bf16 a[196:199],a[108:111],v[92:95],a[196:199]
s_mov_b64 s[32:33],s[12:13]
s_and_b32 s87,s55,8192
s_lshl_b32 s84,s70,21
v_mfma_f32_16x16x32_bf16 a[196:199],a[104:107],v[88:91],a[196:199]
s_add_u32 s87,s87,s84
s_add_u32 s32,s32,s43
s_addc_u32 s33,s33,0
v_mfma_f32_16x16x32_bf16 a[196:199],a[100:103],v[84:87],a[196:199]
s_add_u32 s84,s87,0
s_add_u32 s85,s87,65536
s_add_u32 s86,s87,131072
v_mfma_f32_16x16x32_bf16 a[196:199],a[96:99],v[80:83],a[196:199]
s_and_b32 s33,s33,0xffff
buffer_load_dwordx4 v[188:191],v6,s[28:31],s77 offen offset:3072 sc1
v_mfma_f32_16x16x32_bf16 a[200:203],a[96:99],v[96:99],a[200:203]
s_mov_b32 s34,6438912
s_mov_b32 s35,0x20000
s_max_i32 s84,s84,s18
v_mfma_f32_16x16x32_bf16 a[200:203],a[100:103],v[100:103],a[200:203]
s_max_i32 s85,s85,s19
s_max_i32 s86,s86,s100
v_bfe_u32 v18,v6,6,2
v_mfma_f32_16x16x32_bf16 a[200:203],a[104:107],v[104:107],a[200:203]
s_mul_i32 s44,s41,307200
v_lshrrev_b32 v19,8,v6
v_xor_b32 v18,v18,v19
v_mfma_f32_16x16x32_bf16 a[200:203],a[108:111],v[108:111],a[200:203]
s_mov_b64 s[20:21],s[16:17]
buffer_load_dwordx4 v[200:203],v6,s[28:31],s78 offen sc1
s_waitcnt vmcnt(14)
v_mfma_f32_16x16x32_bf16 a[204:207],a[108:111],v[124:127],a[204:207]
s_lshl_b32 s43,s40,2
ds_read_b64_tr_b16 a[108:109],v1 offset:30912
ds_read_b64_tr_b16 a[110:111],v1 offset:36032
v_mfma_f32_16x16x32_bf16 a[204:207],a[104:107],v[120:123],a[204:207]
s_add_u32 s43,s43,s70
ds_read_b64_tr_b16 a[104:105],v1 offset:20672
v_lshlrev_b32 v18,2,v18
v_mfma_f32_16x16x32_bf16 a[204:207],a[100:103],v[116:119],a[204:207]
s_mul_i32 s43,s43,20480
ds_read_b64_tr_b16 a[100:101],v1 offset:10432
v_bfe_u32 v19,v6,4,2
v_mfma_f32_16x16x32_bf16 a[204:207],a[96:99],v[112:115],a[204:207]
s_barrier
buffer_load_dwordx4 v[204:207],v6,s[28:31],s78 offen offset:1024 sc1
s_waitcnt lgkmcnt(4)
v_mfma_f32_16x16x32_bf16 a[208:211],a[112:115],v[64:67],a[208:211]
s_mov_b32 s22,307200
ds_read_b64_tr_b16 a[96:97],v1 offset:192
ds_read_b64_tr_b16 a[98:99],v1 offset:5312
v_mfma_f32_16x16x32_bf16 a[208:211],a[116:119],v[68:71],a[208:211]
s_mov_b32 s23,0x20000
ds_read_b64_tr_b16 a[102:103],v1 offset:15552
v_or_b32 v18,v18,v19
v_mfma_f32_16x16x32_bf16 a[208:211],a[120:123],v[72:75],a[208:211]
ds_read_b64_tr_b16 a[106:107],v1 offset:25792
v_and_b32 v19,16,v6
v_mfma_f32_16x16x32_bf16 a[208:211],a[124:127],v[76:79],a[208:211]
buffer_load_dwordx4 v[216:219],v6,s[28:31],s78 offen offset:2048 sc1
v_mfma_f32_16x16x32_bf16 a[212:215],a[124:127],v[92:95],a[212:215]
v_mov_b32 v34,0x3f80
v_lshlrev_b32 v19,v19,v34
v_mfma_f32_16x16x32_bf16 a[212:215],a[120:123],v[88:91],a[212:215]
v_lshrrev_b32 v18,1,v18
v_add_u32 v25,4096,v6
v_mfma_f32_16x16x32_bf16 a[212:215],a[116:119],v[84:87],a[212:215]
v_cmp_eq_u32 vcc,0,v18
s_nop 1
v_cndmask_b32 v34,0,v19,vcc
v_mfma_f32_16x16x32_bf16 a[212:215],a[112:115],v[80:83],a[212:215]
s_cmp_ge_u32 0,s71
s_cselect_b32 s76,3,1
v_mov_b32 v36,0
v_mfma_f32_16x16x32_bf16 a[216:219],a[112:115],v[96:99],a[216:219]
buffer_load_dwordx4 v[220:223],v6,s[28:31],s78 offen offset:3072 sc1
v_mfma_f32_16x16x32_bf16 a[216:219],a[116:119],v[100:103],a[216:219]
s_bitcmp1_b32 s54,s76
s_cselect_b32 s77,8192,0
s_lshl_b32 s76,s76,17
v_mfma_f32_16x16x32_bf16 a[216:219],a[120:123],v[104:107],a[216:219]
s_add_u32 s76,s76,s77
s_add_u32 s77,s76,4096
s_add_u32 s78,s76,16384
v_mfma_f32_16x16x32_bf16 a[216:219],a[124:127],v[108:111],a[216:219]
s_max_i32 s77,s77,s19
s_max_i32 s78,s78,s100
v_mov_b32 v37,0
v_mfma_f32_16x16x32_bf16 a[220:223],a[124:127],v[124:127],a[220:223]
ds_read_b64_tr_b16 a[124:125],v2 offset:30944
ds_read_b64_tr_b16 a[126:127],v2 offset:36064
v_mfma_f32_16x16x32_bf16 a[220:223],a[120:123],v[120:123],a[220:223]
ds_read_b64_tr_b16 a[120:121],v2 offset:20704
v_mov_b32 v32,0
v_mfma_f32_16x16x32_bf16 a[220:223],a[116:119],v[116:119],a[220:223]
buffer_load_dwordx4 v[232:235],v6,s[28:31],s79 offen sc1
v_mfma_f32_16x16x32_bf16 a[220:223],a[112:115],v[112:115],a[220:223]
ds_read_b64_tr_b16 a[112:113],v2 offset:224
ds_read_b64_tr_b16 a[114:115],v2 offset:5344
v_mfma_f32_16x16x32_bf16 a[224:227],a[128:131],v[64:67],a[224:227]
ds_read_b64_tr_b16 a[116:117],v2 offset:10464
v_mov_b32 v33,0
v_mfma_f32_16x16x32_bf16 a[224:227],a[132:135],v[68:71],a[224:227]
ds_read_b64_tr_b16 a[118:119],v2 offset:15584
v_bfe_u32 v40,v6,4,4
v_mfma_f32_16x16x32_bf16 a[224:227],a[136:139],v[72:75],a[224:227]
ds_read_b64_tr_b16 a[122:123],v2 offset:25824
v_mul_u32_u24 v40,1280,v40
v_mfma_f32_16x16x32_bf16 a[224:227],a[140:143],v[76:79],a[224:227]
v_cmp_eq_u32 vcc,1,v18
s_nop 1
v_cndmask_b32 v35,0,v19,vcc
v_mfma_f32_16x16x32_bf16 a[228:231],a[140:143],v[92:95],a[228:231]
buffer_load_dwordx4 v[236:239],v6,s[28:31],s79 offen offset:1024 sc1
v_mfma_f32_16x16x32_bf16 a[228:231],a[136:139],v[88:91],a[228:231]
s_add_u32 s20,s20,s44
s_addc_u32 s21,s21,0
v_bfe_u32 v41,v6,8,1
v_mfma_f32_16x16x32_bf16 a[228:231],a[132:135],v[84:87],a[228:231]
s_mul_i32 s44,s71,320
v_lshl_add_u32 v40,v41,5,v40
v_lshrrev_b32 v41,9,v6
v_mfma_f32_16x16x32_bf16 a[228:231],a[128:131],v[80:83],a[228:231]
s_waitcnt vmcnt(9)
v_cmp_ne_u32 s[90:91],s68,v26
v_and_b32 v26,-2,v27
v_cmp_ne_u32 s[92:93],s69,v26
s_nop 0
s_or_b64 s[90:91],s[90:91],s[92:93]
s_cbranch_scc1 L_fr9_%=
L_fo9_%=:
v_mfma_f32_16x16x32_bf16 a[232:235],a[128:131],v[96:99],a[232:235]
s_and_b32 s21,s21,0xffff
s_add_u32 s43,s43,s44
v_lshl_add_u32 v40,v41,4,v40
v_mfma_f32_16x16x32_bf16 a[232:235],a[132:135],v[100:103],a[232:235]
v_mfma_f32_16x16x32_bf16 a[232:235],a[136:139],v[104:107],a[232:235]
buffer_load_dwordx4 v[248:251],v6,s[28:31],s79 offen offset:2048 sc1
v_mfma_f32_16x16x32_bf16 a[232:235],a[140:143],v[108:111],a[232:235]
v_mfma_f32_16x16x32_bf16 a[236:239],a[140:143],v[124:127],a[236:239]
ds_read_b64_tr_b16 a[140:141],v1 offset:30976
ds_read_b64_tr_b16 a[142:143],v1 offset:36096
v_mfma_f32_16x16x32_bf16 a[236:239],a[136:139],v[120:123],a[236:239]
ds_read_b64_tr_b16 a[136:137],v1 offset:20736
v_mfma_f32_16x16x32_bf16 a[236:239],a[132:135],v[116:119],a[236:239]
ds_read_b64_tr_b16 a[132:133],v1 offset:10496
v_mfma_f32_16x16x32_bf16 a[236:239],a[128:131],v[112:115],a[236:239]
ds_read_b64_tr_b16 a[128:129],v1 offset:256
v_mfma_f32_16x16x32_bf16 a[240:243],a[144:147],v[64:67],a[240:243]
buffer_load_dwordx4 v[252:255],v6,s[28:31],s79 offen offset:3072 sc1
v_mfma_f32_16x16x32_bf16 a[240:243],a[148:151],v[68:71],a[240:243]
s_add_u32 s79,s76,20480
s_max_i32 s76,s76,s18
ds_read_b64_tr_b16 a[130:131],v1 offset:5376
v_mfma_f32_16x16x32_bf16 a[240:243],a[152:155],v[72:75],a[240:243]
s_max_i32 s79,s79,s101
ds_read_b64_tr_b16 a[134:135],v1 offset:15616
ds_read_b64_tr_b16 a[138:139],v1 offset:25856
v_mfma_f32_16x16x32_bf16 a[240:243],a[156:159],v[76:79],a[240:243]
v_subrev_u32 v1,40960,v1
v_mfma_f32_16x16x32_bf16 a[244:247],a[156:159],v[92:95],a[244:247]
buffer_load_dwordx4 v[128:131],v6,s[28:31],s76 offen sc1
v_mfma_f32_16x16x32_bf16 a[244:247],a[152:155],v[88:91],a[244:247]
v_mfma_f32_16x16x32_bf16 a[244:247],a[148:151],v[84:87],a[244:247]
v_mfma_f32_16x16x32_bf16 a[244:247],a[144:147],v[80:83],a[244:247]
v_mfma_f32_16x16x32_bf16 a[248:251],a[144:147],v[96:99],a[248:251]
buffer_load_dwordx4 v[160:163],v6,s[28:31],s76 offen offset:1024 sc1
v_mfma_f32_16x16x32_bf16 a[248:251],a[148:151],v[100:103],a[248:251]
v_mfma_f32_16x16x32_bf16 a[248:251],a[152:155],v[104:107],a[248:251]
v_mfma_f32_16x16x32_bf16 a[248:251],a[156:159],v[108:111],a[248:251]
v_mfma_f32_16x16x32_bf16 a[252:255],a[156:159],v[124:127],a[252:255]
buffer_load_dwordx4 v[192:195],v6,s[28:31],s76 offen offset:2048 sc1
v_mfma_f32_16x16x32_bf16 a[252:255],a[152:155],v[120:123],a[252:255]
ds_read_b64_tr_b16 a[152:153],v2 offset:20768
ds_read_b64_tr_b16 a[154:155],v2 offset:25888
v_mfma_f32_16x16x32_bf16 a[252:255],a[148:151],v[116:119],a[252:255]
ds_read_b64_tr_b16 a[148:149],v2 offset:10528
v_mfma_f32_16x16x32_bf16 a[252:255],a[144:147],v[112:115],a[252:255]
ds_read_b64_tr_b16 a[144:145],v2 offset:288
v_mfma_f32_16x16x32_bf16 a[0:3],a[160:163],v[64:67],a[0:3]
ds_read_b64_tr_b16 a[146:147],v2 offset:5408
v_mfma_f32_16x16x32_bf16 a[0:3],a[164:167],v[68:71],a[0:3]
buffer_load_dwordx4 v[224:227],v6,s[28:31],s76 offen offset:3072 sc1
v_mfma_f32_16x16x32_bf16 a[0:3],a[168:171],v[72:75],a[0:3]
ds_read_b64_tr_b16 a[150:151],v2 offset:15648
ds_read_b64_tr_b16 a[156:157],v2 offset:31008
v_mfma_f32_16x16x32_bf16 a[0:3],a[172:175],v[76:79],a[0:3]
ds_read_b64_tr_b16 a[158:159],v2 offset:36128
v_subrev_u32 v2,40960,v2
v_mfma_f32_16x16x32_bf16 a[4:7],a[172:175],v[92:95],a[4:7]
)A"
R"A(v_mfma_f32_16x16x32_bf16 a[4:7],a[168:171],v[88:91],a[4:7]
v_mfma_f32_16x16x32_bf16 a[4:7],a[164:167],v[84:87],a[4:7]
v_mfma_f32_16x16x32_bf16 a[4:7],a[160:163],v[80:83],a[4:7]
buffer_load_dwordx4 v[132:135],v6,s[28:31],s77 offen sc1
v_mfma_f32_16x16x32_bf16 a[8:11],a[160:163],v[96:99],a[8:11]
v_mfma_f32_16x16x32_bf16 a[8:11],a[164:167],v[100:103],a[8:11]
v_mfma_f32_16x16x32_bf16 a[8:11],a[168:171],v[104:107],a[8:11]
v_mfma_f32_16x16x32_bf16 a[8:11],a[172:175],v[108:111],a[8:11]
v_mfma_f32_16x16x32_bf16 a[12:15],a[172:175],v[124:127],a[12:15]
ds_read_b64_tr_b16 a[172:173],v1 offset:30720
ds_read_b64_tr_b16 a[174:175],v1 offset:35840
v_mfma_f32_16x16x32_bf16 a[12:15],a[168:171],v[120:123],a[12:15]
buffer_load_dwordx4 v[164:167],v6,s[28:31],s77 offen offset:1024 sc1
v_mfma_f32_16x16x32_bf16 a[12:15],a[164:167],v[116:119],a[12:15]
ds_read_b64_tr_b16 a[164:165],v1 offset:10240
ds_read_b64_tr_b16 a[166:167],v1 offset:15360
v_mfma_f32_16x16x32_bf16 a[12:15],a[160:163],v[112:115],a[12:15]
ds_read_b64_tr_b16 a[160:161],v1
v_mfma_f32_16x16x32_bf16 a[16:19],a[176:179],v[64:67],a[16:19]
ds_read_b64_tr_b16 a[162:163],v1 offset:5120
v_mfma_f32_16x16x32_bf16 a[16:19],a[180:183],v[68:71],a[16:19]
ds_read_b64_tr_b16 a[168:169],v1 offset:20480
v_mfma_f32_16x16x32_bf16 a[16:19],a[184:187],v[72:75],a[16:19]
ds_read_b64_tr_b16 a[170:171],v1 offset:25600
v_mfma_f32_16x16x32_bf16 a[16:19],a[188:191],v[76:79],a[16:19]
buffer_load_dwordx4 v[196:199],v6,s[28:31],s77 offen offset:2048 sc1
v_mfma_f32_16x16x32_bf16 a[20:23],a[188:191],v[92:95],a[20:23]
v_mfma_f32_16x16x32_bf16 a[20:23],a[184:187],v[88:91],a[20:23]
v_mfma_f32_16x16x32_bf16 a[20:23],a[180:183],v[84:87],a[20:23]
v_mfma_f32_16x16x32_bf16 a[20:23],a[176:179],v[80:83],a[20:23]
v_mfma_f32_16x16x32_bf16 a[24:27],a[176:179],v[96:99],a[24:27]
v_mfma_f32_16x16x32_bf16 a[24:27],a[180:183],v[100:103],a[24:27]
buffer_load_dwordx4 v[228:231],v6,s[28:31],s77 offen offset:3072 sc1
v_mfma_f32_16x16x32_bf16 a[24:27],a[184:187],v[104:107],a[24:27]
v_mfma_f32_16x16x32_bf16 a[24:27],a[188:191],v[108:111],a[24:27]
v_mfma_f32_16x16x32_bf16 a[28:31],a[188:191],v[124:127],a[28:31]
ds_read_b64_tr_b16 a[188:189],v2 offset:30752
ds_read_b64_tr_b16 a[190:191],v2 offset:35872
v_mfma_f32_16x16x32_bf16 a[28:31],a[184:187],v[120:123],a[28:31]
ds_read_b64_tr_b16 a[184:185],v2 offset:20512
v_mfma_f32_16x16x32_bf16 a[28:31],a[180:183],v[116:119],a[28:31]
ds_read_b64_tr_b16 a[180:181],v2 offset:10272
v_mfma_f32_16x16x32_bf16 a[28:31],a[176:179],v[112:115],a[28:31]
s_waitcnt lgkmcnt(4)
buffer_load_dwordx4 v[144:147],v6,s[28:31],s78 offen sc1
v_mfma_f32_16x16x32_bf16 a[32:35],a[96:99],v[64:67],a[32:35]
ds_read_b64_tr_b16 a[176:177],v2 offset:32
ds_read_b64_tr_b16 a[178:179],v2 offset:5152
v_mfma_f32_16x16x32_bf16 a[32:35],a[100:103],v[68:71],a[32:35]
ds_read_b64_tr_b16 a[182:183],v2 offset:15392
v_mfma_f32_16x16x32_bf16 a[32:35],a[104:107],v[72:75],a[32:35]
ds_read_b64_tr_b16 a[186:187],v2 offset:25632
v_mfma_f32_16x16x32_bf16 a[32:35],a[108:111],v[76:79],a[32:35]
v_mfma_f32_16x16x32_bf16 a[36:39],a[108:111],v[92:95],a[36:39]
v_mfma_f32_16x16x32_bf16 a[36:39],a[104:107],v[88:91],a[36:39]
buffer_load_dwordx4 v[176:179],v6,s[28:31],s78 offen offset:1024 sc1
v_mfma_f32_16x16x32_bf16 a[36:39],a[100:103],v[84:87],a[36:39]
v_mfma_f32_16x16x32_bf16 a[36:39],a[96:99],v[80:83],a[36:39]
v_mfma_f32_16x16x32_bf16 a[40:43],a[96:99],v[96:99],a[40:43]
v_mfma_f32_16x16x32_bf16 a[40:43],a[100:103],v[100:103],a[40:43]
v_mfma_f32_16x16x32_bf16 a[40:43],a[104:107],v[104:107],a[40:43]
v_mfma_f32_16x16x32_bf16 a[40:43],a[108:111],v[108:111],a[40:43]
buffer_load_dwordx4 v[208:211],v6,s[28:31],s78 offen offset:2048 sc1
v_mfma_f32_16x16x32_bf16 a[44:47],a[108:111],v[124:127],a[44:47]
ds_read_b64_tr_b16 a[108:109],v1 offset:30784
ds_read_b64_tr_b16 a[110:111],v1 offset:35904
v_mfma_f32_16x16x32_bf16 a[44:47],a[104:107],v[120:123],a[44:47]
ds_read_b64_tr_b16 a[104:105],v1 offset:20544
v_mfma_f32_16x16x32_bf16 a[44:47],a[100:103],v[116:119],a[44:47]
ds_read_b64_tr_b16 a[100:101],v1 offset:10304
v_mfma_f32_16x16x32_bf16 a[44:47],a[96:99],v[112:115],a[44:47]
ds_read_b64_tr_b16 a[96:97],v1 offset:64
v_mfma_f32_16x16x32_bf16 a[48:51],a[112:115],v[64:67],a[48:51]
ds_read_b64_tr_b16 a[98:99],v1 offset:5184
ds_read_b64_tr_b16 a[102:103],v1 offset:15424
v_mfma_f32_16x16x32_bf16 a[48:51],a[116:119],v[68:71],a[48:51]
buffer_load_dwordx4 v[240:243],v6,s[28:31],s78 offen offset:3072 sc1
v_mfma_f32_16x16x32_bf16 a[48:51],a[120:123],v[72:75],a[48:51]
ds_read_b64_tr_b16 a[106:107],v1 offset:25664
v_mfma_f32_16x16x32_bf16 a[48:51],a[124:127],v[76:79],a[48:51]
v_mfma_f32_16x16x32_bf16 a[52:55],a[124:127],v[92:95],a[52:55]
v_mfma_f32_16x16x32_bf16 a[52:55],a[120:123],v[88:91],a[52:55]
v_mfma_f32_16x16x32_bf16 a[52:55],a[116:119],v[84:87],a[52:55]
v_mfma_f32_16x16x32_bf16 a[52:55],a[112:115],v[80:83],a[52:55]
buffer_load_dwordx4 v[148:151],v6,s[28:31],s79 offen sc1
v_mfma_f32_16x16x32_bf16 a[56:59],a[112:115],v[96:99],a[56:59]
v_mfma_f32_16x16x32_bf16 a[56:59],a[116:119],v[100:103],a[56:59]
s_cmp_ge_u32 1,s71
s_cselect_b32 s80,5,3
s_mul_i32 s80,s80,163840
v_mfma_f32_16x16x32_bf16 a[56:59],a[120:123],v[104:107],a[56:59]
s_add_u32 s80,s80,s81
s_add_u32 s83,s80,20480
v_mfma_f32_16x16x32_bf16 a[56:59],a[124:127],v[108:111],a[56:59]
v_mfma_f32_16x16x32_bf16 a[60:63],a[124:127],v[124:127],a[60:63]
ds_read_b64_tr_b16 a[124:125],v2 offset:30816
ds_read_b64_tr_b16 a[126:127],v2 offset:35936
v_mfma_f32_16x16x32_bf16 a[60:63],a[120:123],v[120:123],a[60:63]
buffer_load_dwordx4 v[180:183],v6,s[28:31],s79 offen offset:1024 sc1
v_mfma_f32_16x16x32_bf16 a[60:63],a[116:119],v[116:119],a[60:63]
ds_read_b64_tr_b16 a[116:117],v2 offset:10336
ds_read_b64_tr_b16 a[118:119],v2 offset:15456
v_mfma_f32_16x16x32_bf16 a[60:63],a[112:115],v[112:115],a[60:63]
ds_read_b64_tr_b16 a[112:113],v2 offset:96
v_mfma_f32_16x16x32_bf16 a[64:67],a[128:131],v[64:67],a[64:67]
ds_read_b64_tr_b16 a[114:115],v2 offset:5216
v_mfma_f32_16x16x32_bf16 a[64:67],a[132:135],v[68:71],a[64:67]
ds_read_b64_tr_b16 a[120:121],v2 offset:20576
v_mfma_f32_16x16x32_bf16 a[64:67],a[136:139],v[72:75],a[64:67]
ds_read_b64_tr_b16 a[122:123],v2 offset:25696
v_mfma_f32_16x16x32_bf16 a[64:67],a[140:143],v[76:79],a[64:67]
buffer_load_dwordx4 v[212:215],v6,s[28:31],s79 offen offset:2048 sc1
v_mfma_f32_16x16x32_bf16 a[68:71],a[140:143],v[92:95],a[68:71]
v_mfma_f32_16x16x32_bf16 a[68:71],a[136:139],v[88:91],a[68:71]
v_mfma_f32_16x16x32_bf16 a[68:71],a[132:135],v[84:87],a[68:71]
v_mfma_f32_16x16x32_bf16 a[68:71],a[128:131],v[80:83],a[68:71]
v_mfma_f32_16x16x32_bf16 a[72:75],a[128:131],v[96:99],a[72:75]
v_mfma_f32_16x16x32_bf16 a[72:75],a[132:135],v[100:103],a[72:75]
buffer_load_dwordx4 v[244:247],v6,s[28:31],s79 offen offset:3072 sc1
v_mfma_f32_16x16x32_bf16 a[72:75],a[136:139],v[104:107],a[72:75]
v_mfma_f32_16x16x32_bf16 a[72:75],a[140:143],v[108:111],a[72:75]
v_mfma_f32_16x16x32_bf16 a[76:79],a[140:143],v[124:127],a[76:79]
ds_read_b64_tr_b16 a[140:141],v1 offset:30848
ds_read_b64_tr_b16 a[142:143],v1 offset:35968
v_mfma_f32_16x16x32_bf16 a[76:79],a[136:139],v[120:123],a[76:79]
ds_read_b64_tr_b16 a[136:137],v1 offset:20608
v_mfma_f32_16x16x32_bf16 a[76:79],a[132:135],v[116:119],a[76:79]
s_add_u32 m0,s82,40960
ds_read_b64_tr_b16 a[132:133],v1 offset:10368
v_mfma_f32_16x16x32_bf16 a[76:79],a[128:131],v[112:115],a[76:79]
ds_read_b64_tr_b16 a[128:129],v1 offset:128
v_mfma_f32_16x16x32_bf16 a[80:83],a[144:147],v[64:67],a[80:83]
ds_read_b64_tr_b16 a[130:131],v1 offset:5248
ds_read_b64_tr_b16 a[134:135],v1 offset:15488
v_mfma_f32_16x16x32_bf16 a[80:83],a[148:151],v[68:71],a[80:83]
buffer_load_dwordx4 v7,s[72:75],s80 offen lds
v_mfma_f32_16x16x32_bf16 a[80:83],a[152:155],v[72:75],a[80:83]
ds_read_b64_tr_b16 a[138:139],v1 offset:25728
v_mfma_f32_16x16x32_bf16 a[80:83],a[156:159],v[76:79],a[80:83]
v_mfma_f32_16x16x32_bf16 a[84:87],a[156:159],v[92:95],a[84:87]
v_mfma_f32_16x16x32_bf16 a[84:87],a[152:155],v[88:91],a[84:87]
v_mfma_f32_16x16x32_bf16 a[84:87],a[148:151],v[84:87],a[84:87]
buffer_load_dwordx4 v8,s[72:75],s80 offen offset:1024 lds
v_mfma_f32_16x16x32_bf16 a[84:87],a[144:147],v[80:83],a[84:87]
v_mfma_f32_16x16x32_bf16 a[88:91],a[144:147],v[96:99],a[88:91]
v_mfma_f32_16x16x32_bf16 a[88:91],a[148:151],v[100:103],a[88:91]
v_mfma_f32_16x16x32_bf16 a[88:91],a[152:155],v[104:107],a[88:91]
v_mfma_f32_16x16x32_bf16 a[88:91],a[156:159],v[108:111],a[88:91]
buffer_load_dwordx4 v9,s[72:75],s80 offen offset:2048 lds
v_mfma_f32_16x16x32_bf16 a[92:95],a[156:159],v[124:127],a[92:95]
ds_read_b64_tr_b16 a[156:157],v2 offset:30880
ds_read_b64_tr_b16 a[158:159],v2 offset:36000
v_mfma_f32_16x16x32_bf16 a[92:95],a[152:155],v[120:123],a[92:95]
ds_read_b64_tr_b16 a[152:153],v2 offset:20640
v_mfma_f32_16x16x32_bf16 a[92:95],a[148:151],v[116:119],a[92:95]
ds_read_b64_tr_b16 a[148:149],v2 offset:10400
v_mfma_f32_16x16x32_bf16 a[92:95],a[144:147],v[112:115],a[92:95]
ds_read_b64_tr_b16 a[144:145],v2 offset:160
v_mfma_f32_16x16x32_bf16 a[192:195],a[160:163],v[136:139],a[192:195]
buffer_load_dwordx4 v10,s[72:75],s80 offen offset:3072 lds
v_mfma_f32_16x16x32_bf16 a[192:195],a[164:167],v[140:143],a[192:195]
ds_read_b64_tr_b16 a[146:147],v2 offset:5280
ds_read_b64_tr_b16 a[150:151],v2 offset:15520
v_mfma_f32_16x16x32_bf16 a[192:195],a[168:171],v[152:155],a[192:195]
s_add_u32 m0,s82,45056
ds_read_b64_tr_b16 a[154:155],v2 offset:25760
v_mfma_f32_16x16x32_bf16 a[192:195],a[172:175],v[156:159],a[192:195]
s_waitcnt vmcnt(12)
v_mfma_f32_16x16x32_bf16 a[196:199],a[172:175],v[188:191],a[196:199]
v_mfma_f32_16x16x32_bf16 a[196:199],a[168:171],v[184:187],a[196:199]
buffer_load_dwordx4 v11,s[72:75],s80 offen lds
v_mfma_f32_16x16x32_bf16 a[196:199],a[164:167],v[172:175],a[196:199]
v_mfma_f32_16x16x32_bf16 a[196:199],a[160:163],v[168:171],a[196:199]
s_add_u32 m0,s82,46080
v_mfma_f32_16x16x32_bf16 a[200:203],a[160:163],v[200:203],a[200:203]
v_mfma_f32_16x16x32_bf16 a[200:203],a[164:167],v[204:207],a[200:203]
v_mfma_f32_16x16x32_bf16 a[200:203],a[168:171],v[216:219],a[200:203]
buffer_load_dwordx4 v7,s[72:75],s83 offen lds
v_mfma_f32_16x16x32_bf16 a[200:203],a[172:175],v[220:223],a[200:203]
v_mfma_f32_16x16x32_bf16 a[204:207],a[172:175],v[252:255],a[204:207]
ds_read_b64_tr_b16 a[172:173],v1 offset:30912
ds_read_b64_tr_b16 a[174:175],v1 offset:36032
v_mfma_f32_16x16x32_bf16 a[204:207],a[168:171],v[248:251],a[204:207]
ds_read_b64_tr_b16 a[168:169],v1 offset:20672
v_mfma_f32_16x16x32_bf16 a[204:207],a[164:167],v[236:239],a[204:207]
ds_read_b64_tr_b16 a[164:165],v1 offset:10432
v_mfma_f32_16x16x32_bf16 a[204:207],a[160:163],v[232:235],a[204:207]
s_barrier
buffer_load_dwordx4 v8,s[72:75],s83 offen offset:1024 lds
s_waitcnt lgkmcnt(4)
v_mfma_f32_16x16x32_bf16 a[208:211],a[176:179],v[136:139],a[208:211]
s_cmp_ge_u32 1,s71
s_cselect_b32 s76,5,3
ds_read_b64_tr_b16 a[160:161],v1 offset:192
v_mfma_f32_16x16x32_bf16 a[208:211],a[180:183],v[140:143],a[208:211]
s_bitcmp1_b32 s54,s76
s_cselect_b32 s77,8192,0
s_lshl_b32 s76,s76,17
v_mfma_f32_16x16x32_bf16 a[208:211],a[184:187],v[152:155],a[208:211]
s_add_u32 s76,s76,s77
s_add_u32 s77,s76,4096
s_add_u32 s78,s76,16384
v_mfma_f32_16x16x32_bf16 a[208:211],a[188:191],v[156:159],a[208:211]
s_add_u32 s79,s76,20480
buffer_load_dwordx4 v9,s[72:75],s83 offen offset:2048 lds
v_mfma_f32_16x16x32_bf16 a[212:215],a[188:191],v[188:191],a[212:215]
s_max_i32 s76,s76,s18
s_max_i32 s77,s77,s19
s_max_i32 s78,s78,s100
v_mfma_f32_16x16x32_bf16 a[212:215],a[184:187],v[184:187],a[212:215]
s_max_i32 s79,s79,s101
ds_read_b64_tr_b16 a[162:163],v1 offset:5312
ds_read_b64_tr_b16 a[166:167],v1 offset:15552
v_mfma_f32_16x16x32_bf16 a[212:215],a[180:183],v[172:175],a[212:215]
ds_read_b64_tr_b16 a[170:171],v1 offset:25792
v_mfma_f32_16x16x32_bf16 a[212:215],a[176:179],v[168:171],a[212:215]
v_mfma_f32_16x16x32_bf16 a[216:219],a[176:179],v[200:203],a[216:219]
buffer_load_dwordx4 v10,s[72:75],s83 offen offset:3072 lds
v_mfma_f32_16x16x32_bf16 a[216:219],a[180:183],v[204:207],a[216:219]
v_mfma_f32_16x16x32_bf16 a[216:219],a[184:187],v[216:219],a[216:219]
)A"
R"A(s_add_u32 m0,s82,50176
v_mfma_f32_16x16x32_bf16 a[216:219],a[188:191],v[220:223],a[216:219]
v_mfma_f32_16x16x32_bf16 a[220:223],a[188:191],v[252:255],a[220:223]
ds_read_b64_tr_b16 a[188:189],v2 offset:30944
ds_read_b64_tr_b16 a[190:191],v2 offset:36064
v_mfma_f32_16x16x32_bf16 a[220:223],a[184:187],v[248:251],a[220:223]
buffer_load_dwordx4 v11,s[72:75],s83 offen lds
v_mfma_f32_16x16x32_bf16 a[220:223],a[180:183],v[236:239],a[220:223]
ds_read_b64_tr_b16 a[180:181],v2 offset:10464
ds_read_b64_tr_b16 a[182:183],v2 offset:15584
v_mfma_f32_16x16x32_bf16 a[220:223],a[176:179],v[232:235],a[220:223]
ds_read_b64_tr_b16 a[176:177],v2 offset:224
v_mfma_f32_16x16x32_bf16 a[224:227],a[96:99],v[136:139],a[224:227]
ds_read_b64_tr_b16 a[178:179],v2 offset:5344
v_mfma_f32_16x16x32_bf16 a[224:227],a[100:103],v[140:143],a[224:227]
ds_read_b64_tr_b16 a[184:185],v2 offset:20704
v_mfma_f32_16x16x32_bf16 a[224:227],a[104:107],v[152:155],a[224:227]
ds_read_b64_tr_b16 a[186:187],v2 offset:25824
v_mfma_f32_16x16x32_bf16 a[224:227],a[108:111],v[156:159],a[224:227]
v_mfma_f32_16x16x32_bf16 a[228:231],a[108:111],v[188:191],a[228:231]
v_mfma_f32_16x16x32_bf16 a[228:231],a[104:107],v[184:187],a[228:231]
v_mfma_f32_16x16x32_bf16 a[228:231],a[100:103],v[172:175],a[228:231]
v_mfma_f32_16x16x32_bf16 a[228:231],a[96:99],v[168:171],a[228:231]
buffer_load_dwordx4 v[64:67],v6,s[28:31],s76 offen sc1
v_mfma_f32_16x16x32_bf16 a[232:235],a[96:99],v[200:203],a[232:235]
v_mfma_f32_16x16x32_bf16 a[232:235],a[100:103],v[204:207],a[232:235]
v_mfma_f32_16x16x32_bf16 a[232:235],a[104:107],v[216:219],a[232:235]
v_mfma_f32_16x16x32_bf16 a[232:235],a[108:111],v[220:223],a[232:235]
v_mfma_f32_16x16x32_bf16 a[236:239],a[108:111],v[252:255],a[236:239]
buffer_load_dwordx4 v[68:71],v6,s[28:31],s76 offen offset:1024 sc1
v_mfma_f32_16x16x32_bf16 a[236:239],a[104:107],v[248:251],a[236:239]
ds_read_b64_tr_b16 a[104:105],v1 offset:20736
ds_read_b64_tr_b16 a[106:107],v1 offset:25856
v_mfma_f32_16x16x32_bf16 a[236:239],a[100:103],v[236:239],a[236:239]
ds_read_b64_tr_b16 a[100:101],v1 offset:10496
v_mfma_f32_16x16x32_bf16 a[236:239],a[96:99],v[232:235],a[236:239]
ds_read_b64_tr_b16 a[96:97],v1 offset:256
v_mfma_f32_16x16x32_bf16 a[240:243],a[112:115],v[136:139],a[240:243]
ds_read_b64_tr_b16 a[98:99],v1 offset:5376
v_mfma_f32_16x16x32_bf16 a[240:243],a[116:119],v[140:143],a[240:243]
buffer_load_dwordx4 v[72:75],v6,s[28:31],s76 offen offset:2048 sc1
v_mfma_f32_16x16x32_bf16 a[240:243],a[120:123],v[152:155],a[240:243]
ds_read_b64_tr_b16 a[102:103],v1 offset:15616
ds_read_b64_tr_b16 a[108:109],v1 offset:30976
v_mfma_f32_16x16x32_bf16 a[240:243],a[124:127],v[156:159],a[240:243]
ds_read_b64_tr_b16 a[110:111],v1 offset:36096
v_subrev_u32 v1,81920,v1
v_mfma_f32_16x16x32_bf16 a[244:247],a[124:127],v[188:191],a[244:247]
v_add_u32 v42,40960,v1
v_add_u32 v44,122880,v1
v_mfma_f32_16x16x32_bf16 a[244:247],a[120:123],v[184:187],a[244:247]
v_mfma_f32_16x16x32_bf16 a[244:247],a[116:119],v[172:175],a[244:247]
buffer_load_dwordx4 v[76:79],v6,s[28:31],s76 offen offset:3072 sc1
v_mfma_f32_16x16x32_bf16 a[244:247],a[112:115],v[168:171],a[244:247]
v_mfma_f32_16x16x32_bf16 a[248:251],a[112:115],v[200:203],a[248:251]
v_mfma_f32_16x16x32_bf16 a[248:251],a[116:119],v[204:207],a[248:251]
v_mfma_f32_16x16x32_bf16 a[248:251],a[120:123],v[216:219],a[248:251]
v_mfma_f32_16x16x32_bf16 a[248:251],a[124:127],v[220:223],a[248:251]
buffer_load_dwordx4 v[80:83],v6,s[28:31],s77 offen sc1
v_mfma_f32_16x16x32_bf16 a[252:255],a[124:127],v[252:255],a[252:255]
ds_read_b64_tr_b16 a[124:125],v2 offset:31008
ds_read_b64_tr_b16 a[126:127],v2 offset:36128
v_mfma_f32_16x16x32_bf16 a[252:255],a[120:123],v[248:251],a[252:255]
ds_read_b64_tr_b16 a[120:121],v2 offset:20768
v_mfma_f32_16x16x32_bf16 a[252:255],a[116:119],v[236:239],a[252:255]
ds_read_b64_tr_b16 a[116:117],v2 offset:10528
v_mfma_f32_16x16x32_bf16 a[252:255],a[112:115],v[232:235],a[252:255]
ds_read_b64_tr_b16 a[112:113],v2 offset:288
v_mfma_f32_16x16x32_bf16 a[0:3],a[128:131],v[136:139],a[0:3]
buffer_load_dwordx4 v[84:87],v6,s[28:31],s77 offen offset:1024 sc1
v_mfma_f32_16x16x32_bf16 a[0:3],a[132:135],v[140:143],a[0:3]
ds_read_b64_tr_b16 a[114:115],v2 offset:5408
ds_read_b64_tr_b16 a[118:119],v2 offset:15648
v_mfma_f32_16x16x32_bf16 a[0:3],a[136:139],v[152:155],a[0:3]
ds_read_b64_tr_b16 a[122:123],v2 offset:25888
v_subrev_u32 v2,81920,v2
v_mfma_f32_16x16x32_bf16 a[0:3],a[140:143],v[156:159],a[0:3]
v_add_u32 v43,40960,v2
v_add_u32 v45,122880,v2
v_mfma_f32_16x16x32_bf16 a[4:7],a[140:143],v[188:191],a[4:7]
v_mfma_f32_16x16x32_bf16 a[4:7],a[136:139],v[184:187],a[4:7]
buffer_load_dwordx4 v[88:91],v6,s[28:31],s77 offen offset:2048 sc1
v_mfma_f32_16x16x32_bf16 a[4:7],a[132:135],v[172:175],a[4:7]
v_mfma_f32_16x16x32_bf16 a[4:7],a[128:131],v[168:171],a[4:7]
v_mfma_f32_16x16x32_bf16 a[8:11],a[128:131],v[200:203],a[8:11]
v_mfma_f32_16x16x32_bf16 a[8:11],a[132:135],v[204:207],a[8:11]
v_mfma_f32_16x16x32_bf16 a[8:11],a[136:139],v[216:219],a[8:11]
buffer_load_dwordx4 v[92:95],v6,s[28:31],s77 offen offset:3072 sc1
v_mfma_f32_16x16x32_bf16 a[8:11],a[140:143],v[220:223],a[8:11]
v_mfma_f32_16x16x32_bf16 a[12:15],a[140:143],v[252:255],a[12:15]
ds_read_b64_tr_b16 a[140:141],v1 offset:30720
ds_read_b64_tr_b16 a[142:143],v1 offset:35840
v_mfma_f32_16x16x32_bf16 a[12:15],a[136:139],v[248:251],a[12:15]
ds_read_b64_tr_b16 a[136:137],v1 offset:20480
v_mfma_f32_16x16x32_bf16 a[12:15],a[132:135],v[236:239],a[12:15]
ds_read_b64_tr_b16 a[132:133],v1 offset:10240
v_mfma_f32_16x16x32_bf16 a[12:15],a[128:131],v[232:235],a[12:15]
buffer_load_dwordx4 v[96:99],v6,s[28:31],s78 offen sc1
v_mfma_f32_16x16x32_bf16 a[16:19],a[144:147],v[136:139],a[16:19]
ds_read_b64_tr_b16 a[128:129],v1
ds_read_b64_tr_b16 a[130:131],v1 offset:5120
v_mfma_f32_16x16x32_bf16 a[16:19],a[148:151],v[140:143],a[16:19]
ds_read_b64_tr_b16 a[134:135],v1 offset:15360
v_mfma_f32_16x16x32_bf16 a[16:19],a[152:155],v[152:155],a[16:19]
ds_read_b64_tr_b16 a[138:139],v1 offset:25600
v_mfma_f32_16x16x32_bf16 a[16:19],a[156:159],v[156:159],a[16:19]
v_mfma_f32_16x16x32_bf16 a[20:23],a[156:159],v[188:191],a[20:23]
buffer_load_dwordx4 v[100:103],v6,s[28:31],s78 offen offset:1024 sc1
v_mfma_f32_16x16x32_bf16 a[20:23],a[152:155],v[184:187],a[20:23]
v_mfma_f32_16x16x32_bf16 a[20:23],a[148:151],v[172:175],a[20:23]
v_mfma_f32_16x16x32_bf16 a[20:23],a[144:147],v[168:171],a[20:23]
v_mfma_f32_16x16x32_bf16 a[24:27],a[144:147],v[200:203],a[24:27]
v_mfma_f32_16x16x32_bf16 a[24:27],a[148:151],v[204:207],a[24:27]
buffer_load_dwordx4 v[104:107],v6,s[28:31],s78 offen offset:2048 sc1
v_mfma_f32_16x16x32_bf16 a[24:27],a[152:155],v[216:219],a[24:27]
v_mfma_f32_16x16x32_bf16 a[24:27],a[156:159],v[220:223],a[24:27]
v_mfma_f32_16x16x32_bf16 a[28:31],a[156:159],v[252:255],a[28:31]
ds_read_b64_tr_b16 a[156:157],v2 offset:30752
ds_read_b64_tr_b16 a[158:159],v2 offset:35872
v_mfma_f32_16x16x32_bf16 a[28:31],a[152:155],v[248:251],a[28:31]
ds_read_b64_tr_b16 a[152:153],v2 offset:20512
v_mfma_f32_16x16x32_bf16 a[28:31],a[148:151],v[236:239],a[28:31]
buffer_load_dwordx4 v[108:111],v6,s[28:31],s78 offen offset:3072 sc1
v_mfma_f32_16x16x32_bf16 a[28:31],a[144:147],v[232:235],a[28:31]
s_cmp_ge_u32 2,s71
s_cselect_b32 s80,7,5
s_mul_i32 s80,s80,163840
s_waitcnt lgkmcnt(3)
v_mfma_f32_16x16x32_bf16 a[32:35],a[160:163],v[136:139],a[32:35]
s_add_u32 s80,s80,s81
s_add_u32 s83,s80,20480
ds_read_b64_tr_b16 a[144:145],v2 offset:32
v_mfma_f32_16x16x32_bf16 a[32:35],a[164:167],v[140:143],a[32:35]
ds_read_b64_tr_b16 a[146:147],v2 offset:5152
ds_read_b64_tr_b16 a[148:149],v2 offset:10272
v_mfma_f32_16x16x32_bf16 a[32:35],a[168:171],v[152:155],a[32:35]
ds_read_b64_tr_b16 a[150:151],v2 offset:15392
v_mfma_f32_16x16x32_bf16 a[32:35],a[172:175],v[156:159],a[32:35]
buffer_load_dwordx4 v[112:115],v6,s[28:31],s79 offen sc1
v_mfma_f32_16x16x32_bf16 a[36:39],a[172:175],v[188:191],a[36:39]
ds_read_b64_tr_b16 a[154:155],v2 offset:25632
v_mfma_f32_16x16x32_bf16 a[36:39],a[168:171],v[184:187],a[36:39]
v_mfma_f32_16x16x32_bf16 a[36:39],a[164:167],v[172:175],a[36:39]
v_mfma_f32_16x16x32_bf16 a[36:39],a[160:163],v[168:171],a[36:39]
v_mfma_f32_16x16x32_bf16 a[40:43],a[160:163],v[200:203],a[40:43]
buffer_load_dwordx4 v[116:119],v6,s[28:31],s79 offen offset:1024 sc1
v_mfma_f32_16x16x32_bf16 a[40:43],a[164:167],v[204:207],a[40:43]
v_mfma_f32_16x16x32_bf16 a[40:43],a[168:171],v[216:219],a[40:43]
v_mfma_f32_16x16x32_bf16 a[40:43],a[172:175],v[220:223],a[40:43]
v_mfma_f32_16x16x32_bf16 a[44:47],a[172:175],v[252:255],a[44:47]
ds_read_b64_tr_b16 a[172:173],v1 offset:30784
ds_read_b64_tr_b16 a[174:175],v1 offset:35904
v_mfma_f32_16x16x32_bf16 a[44:47],a[168:171],v[248:251],a[44:47]
buffer_load_dwordx4 v[120:123],v6,s[28:31],s79 offen offset:2048 sc1
v_mfma_f32_16x16x32_bf16 a[44:47],a[164:167],v[236:239],a[44:47]
ds_read_b64_tr_b16 a[164:165],v1 offset:10304
ds_read_b64_tr_b16 a[166:167],v1 offset:15424
v_mfma_f32_16x16x32_bf16 a[44:47],a[160:163],v[232:235],a[44:47]
ds_read_b64_tr_b16 a[160:161],v1 offset:64
v_mfma_f32_16x16x32_bf16 a[48:51],a[176:179],v[136:139],a[48:51]
ds_read_b64_tr_b16 a[162:163],v1 offset:5184
v_mfma_f32_16x16x32_bf16 a[48:51],a[180:183],v[140:143],a[48:51]
ds_read_b64_tr_b16 a[168:169],v1 offset:20544
v_mfma_f32_16x16x32_bf16 a[48:51],a[184:187],v[152:155],a[48:51]
buffer_load_dwordx4 v[124:127],v6,s[28:31],s79 offen offset:3072 sc1
v_mfma_f32_16x16x32_bf16 a[48:51],a[188:191],v[156:159],a[48:51]
ds_read_b64_tr_b16 a[170:171],v1 offset:25664
v_mfma_f32_16x16x32_bf16 a[52:55],a[188:191],v[188:191],a[52:55]
s_add_u32 m0,s82,122880
v_mfma_f32_16x16x32_bf16 a[52:55],a[184:187],v[184:187],a[52:55]
v_mfma_f32_16x16x32_bf16 a[52:55],a[180:183],v[172:175],a[52:55]
v_mfma_f32_16x16x32_bf16 a[52:55],a[176:179],v[168:171],a[52:55]
buffer_load_dwordx4 v7,s[72:75],s80 offen lds
v_mfma_f32_16x16x32_bf16 a[56:59],a[176:179],v[200:203],a[56:59]
v_mfma_f32_16x16x32_bf16 a[56:59],a[180:183],v[204:207],a[56:59]
v_mfma_f32_16x16x32_bf16 a[56:59],a[184:187],v[216:219],a[56:59]
v_mfma_f32_16x16x32_bf16 a[56:59],a[188:191],v[220:223],a[56:59]
v_mfma_f32_16x16x32_bf16 a[60:63],a[188:191],v[252:255],a[60:63]
buffer_load_dwordx4 v8,s[72:75],s80 offen offset:1024 lds
v_mfma_f32_16x16x32_bf16 a[60:63],a[184:187],v[248:251],a[60:63]
ds_read_b64_tr_b16 a[184:185],v2 offset:20576
ds_read_b64_tr_b16 a[186:187],v2 offset:25696
v_mfma_f32_16x16x32_bf16 a[60:63],a[180:183],v[236:239],a[60:63]
ds_read_b64_tr_b16 a[180:181],v2 offset:10336
v_mfma_f32_16x16x32_bf16 a[60:63],a[176:179],v[232:235],a[60:63]
ds_read_b64_tr_b16 a[176:177],v2 offset:96
v_mfma_f32_16x16x32_bf16 a[64:67],a[96:99],v[136:139],a[64:67]
ds_read_b64_tr_b16 a[178:179],v2 offset:5216
v_mfma_f32_16x16x32_bf16 a[64:67],a[100:103],v[140:143],a[64:67]
buffer_load_dwordx4 v9,s[72:75],s80 offen offset:2048 lds
v_mfma_f32_16x16x32_bf16 a[64:67],a[104:107],v[152:155],a[64:67]
ds_read_b64_tr_b16 a[182:183],v2 offset:15456
ds_read_b64_tr_b16 a[188:189],v2 offset:30816
v_mfma_f32_16x16x32_bf16 a[64:67],a[108:111],v[156:159],a[64:67]
ds_read_b64_tr_b16 a[190:191],v2 offset:35936
v_mfma_f32_16x16x32_bf16 a[68:71],a[108:111],v[188:191],a[68:71]
v_mfma_f32_16x16x32_bf16 a[68:71],a[104:107],v[184:187],a[68:71]
v_mfma_f32_16x16x32_bf16 a[68:71],a[100:103],v[172:175],a[68:71]
buffer_load_dwordx4 v10,s[72:75],s80 offen offset:3072 lds
v_mfma_f32_16x16x32_bf16 a[68:71],a[96:99],v[168:171],a[68:71]
v_mfma_f32_16x16x32_bf16 a[72:75],a[96:99],v[200:203],a[72:75]
s_add_u32 m0,s82,126976
v_mfma_f32_16x16x32_bf16 a[72:75],a[100:103],v[204:207],a[72:75]
v_mfma_f32_16x16x32_bf16 a[72:75],a[104:107],v[216:219],a[72:75]
s_cmp_ge_u32 2,s71
s_cselect_b32 s76,7,5
v_mfma_f32_16x16x32_bf16 a[72:75],a[108:111],v[220:223],a[72:75]
buffer_load_dwordx4 v11,s[72:75],s80 offen lds
v_mfma_f32_16x16x32_bf16 a[76:79],a[108:111],v[252:255],a[76:79]
s_bitcmp1_b32 s54,s76
s_cselect_b32 s77,8192,0
s_lshl_b32 s76,s76,17
v_mfma_f32_16x16x32_bf16 a[76:79],a[104:107],v[248:251],a[76:79]
s_add_u32 m0,s82,128000
s_add_u32 s76,s76,s77
s_add_u32 s77,s76,4096
v_mfma_f32_16x16x32_bf16 a[76:79],a[100:103],v[236:239],a[76:79]
)A"
R"A(s_add_u32 s78,s76,16384
s_add_u32 s79,s76,20480
s_max_i32 s76,s76,s18
v_mfma_f32_16x16x32_bf16 a[76:79],a[96:99],v[232:235],a[76:79]
s_max_i32 s77,s77,s19
ds_read_b64_tr_b16 a[96:97],v1 offset:128
ds_read_b64_tr_b16 a[98:99],v1 offset:5248
v_mfma_f32_16x16x32_bf16 a[80:83],a[112:115],v[136:139],a[80:83]
s_max_i32 s78,s78,s100
buffer_load_dwordx4 v7,s[72:75],s83 offen lds
v_mfma_f32_16x16x32_bf16 a[80:83],a[116:119],v[140:143],a[80:83]
s_max_i32 s79,s79,s101
ds_read_b64_tr_b16 a[100:101],v1 offset:10368
ds_read_b64_tr_b16 a[102:103],v1 offset:15488
v_mfma_f32_16x16x32_bf16 a[80:83],a[120:123],v[152:155],a[80:83]
ds_read_b64_tr_b16 a[104:105],v1 offset:20608
v_mfma_f32_16x16x32_bf16 a[80:83],a[124:127],v[156:159],a[80:83]
ds_read_b64_tr_b16 a[106:107],v1 offset:25728
v_mfma_f32_16x16x32_bf16 a[84:87],a[124:127],v[188:191],a[84:87]
ds_read_b64_tr_b16 a[108:109],v1 offset:30848
v_mfma_f32_16x16x32_bf16 a[84:87],a[120:123],v[184:187],a[84:87]
buffer_load_dwordx4 v8,s[72:75],s83 offen offset:1024 lds
v_mfma_f32_16x16x32_bf16 a[84:87],a[116:119],v[172:175],a[84:87]
ds_read_b64_tr_b16 a[110:111],v1 offset:35968
v_mfma_f32_16x16x32_bf16 a[84:87],a[112:115],v[168:171],a[84:87]
v_mfma_f32_16x16x32_bf16 a[88:91],a[112:115],v[200:203],a[88:91]
v_mfma_f32_16x16x32_bf16 a[88:91],a[116:119],v[204:207],a[88:91]
v_mfma_f32_16x16x32_bf16 a[88:91],a[120:123],v[216:219],a[88:91]
buffer_load_dwordx4 v9,s[72:75],s83 offen offset:2048 lds
v_mfma_f32_16x16x32_bf16 a[88:91],a[124:127],v[220:223],a[88:91]
v_mfma_f32_16x16x32_bf16 a[92:95],a[124:127],v[252:255],a[92:95]
ds_read_b64_tr_b16 a[124:125],v2 offset:30880
ds_read_b64_tr_b16 a[126:127],v2 offset:36000
v_mfma_f32_16x16x32_bf16 a[92:95],a[120:123],v[248:251],a[92:95]
ds_read_b64_tr_b16 a[120:121],v2 offset:20640
v_mfma_f32_16x16x32_bf16 a[92:95],a[116:119],v[236:239],a[92:95]
ds_read_b64_tr_b16 a[116:117],v2 offset:10400
v_mfma_f32_16x16x32_bf16 a[92:95],a[112:115],v[232:235],a[92:95]
buffer_load_dwordx4 v10,s[72:75],s83 offen offset:3072 lds
v_mfma_f32_16x16x32_bf16 a[192:195],a[128:131],v[128:131],a[192:195]
ds_read_b64_tr_b16 a[112:113],v2 offset:160
ds_read_b64_tr_b16 a[114:115],v2 offset:5280
v_mfma_f32_16x16x32_bf16 a[192:195],a[132:135],v[160:163],a[192:195]
s_add_u32 m0,s82,132096
ds_read_b64_tr_b16 a[118:119],v2 offset:15520
v_mfma_f32_16x16x32_bf16 a[192:195],a[136:139],v[192:195],a[192:195]
ds_read_b64_tr_b16 a[122:123],v2 offset:25760
v_mfma_f32_16x16x32_bf16 a[192:195],a[140:143],v[224:227],a[192:195]
buffer_load_dwordx4 v[136:139],v6,s[28:31],s76 offen sc1
v_mfma_f32_16x16x32_bf16 a[196:199],a[140:143],v[228:231],a[196:199]
v_mfma_f32_16x16x32_bf16 a[196:199],a[136:139],v[196:199],a[196:199]
v_mfma_f32_16x16x32_bf16 a[196:199],a[132:135],v[164:167],a[196:199]
v_mfma_f32_16x16x32_bf16 a[196:199],a[128:131],v[132:135],a[196:199]
s_waitcnt vmcnt(14)
buffer_load_dwordx4 v11,s[72:75],s83 offen lds
v_mfma_f32_16x16x32_bf16 a[200:203],a[128:131],v[144:147],a[200:203]
ds_read_b64_tr_b16 a[128:129],v1 offset:192
ds_read_b64_tr_b16 a[130:131],v1 offset:5312
v_mfma_f32_16x16x32_bf16 a[200:203],a[132:135],v[176:179],a[200:203]
ds_read_b64_tr_b16 a[132:133],v1 offset:10432
v_mfma_f32_16x16x32_bf16 a[200:203],a[136:139],v[208:211],a[200:203]
ds_read_b64_tr_b16 a[134:135],v1 offset:15552
v_mfma_f32_16x16x32_bf16 a[200:203],a[140:143],v[240:243],a[200:203]
s_waitcnt lgkmcnt(4)
buffer_load_dwordx4 v[140:143],v6,s[28:31],s76 offen offset:1024 sc1
v_mfma_f32_16x16x32_bf16 a[208:211],a[144:147],v[128:131],a[208:211]
ds_read_b64_tr_b16 a[136:137],v1 offset:20672
ds_read_b64_tr_b16 a[138:139],v1 offset:25792
v_mfma_f32_16x16x32_bf16 a[208:211],a[148:151],v[160:163],a[208:211]
ds_read_b64_tr_b16 a[140:141],v1 offset:30912
v_mfma_f32_16x16x32_bf16 a[208:211],a[152:155],v[192:195],a[208:211]
ds_read_b64_tr_b16 a[142:143],v1 offset:36032
v_mfma_f32_16x16x32_bf16 a[208:211],a[156:159],v[224:227],a[208:211]
buffer_load_dwordx4 v[152:155],v6,s[28:31],s76 offen offset:2048 sc1
v_mfma_f32_16x16x32_bf16 a[212:215],a[156:159],v[228:231],a[212:215]
v_mfma_f32_16x16x32_bf16 a[212:215],a[152:155],v[196:199],a[212:215]
v_mfma_f32_16x16x32_bf16 a[212:215],a[148:151],v[164:167],a[212:215]
v_mfma_f32_16x16x32_bf16 a[212:215],a[144:147],v[132:135],a[212:215]
buffer_load_dwordx4 v[156:159],v6,s[28:31],s76 offen offset:3072 sc1
v_mfma_f32_16x16x32_bf16 a[216:219],a[144:147],v[144:147],a[216:219]
s_add_u32 s76,s70,3
ds_read_b64_tr_b16 a[144:145],v2 offset:224
ds_read_b64_tr_b16 a[146:147],v2 offset:5344
v_mfma_f32_16x16x32_bf16 a[216:219],a[148:151],v[176:179],a[216:219]
s_and_b32 s76,s76,3
s_lshl_b32 s76,s76,21
ds_read_b64_tr_b16 a[148:149],v2 offset:10464
v_mfma_f32_16x16x32_bf16 a[216:219],a[152:155],v[208:211],a[216:219]
s_add_u32 s76,s76,0
s_max_i32 s76,s76,s101
ds_read_b64_tr_b16 a[150:151],v2 offset:15584
v_mfma_f32_16x16x32_bf16 a[216:219],a[156:159],v[240:243],a[216:219]
buffer_load_dwordx4 v[168:171],v6,s[28:31],s77 offen sc1
v_mfma_f32_16x16x32_bf16 a[224:227],a[160:163],v[128:131],a[224:227]
ds_read_b64_tr_b16 a[152:153],v2 offset:20704
ds_read_b64_tr_b16 a[154:155],v2 offset:25824
v_mfma_f32_16x16x32_bf16 a[224:227],a[164:167],v[160:163],a[224:227]
ds_read_b64_tr_b16 a[156:157],v2 offset:30944
v_mfma_f32_16x16x32_bf16 a[224:227],a[168:171],v[192:195],a[224:227]
ds_read_b64_tr_b16 a[158:159],v2 offset:36064
v_mfma_f32_16x16x32_bf16 a[224:227],a[172:175],v[224:227],a[224:227]
buffer_load_dwordx4 v[172:175],v6,s[28:31],s77 offen offset:1024 sc1
v_mfma_f32_16x16x32_bf16 a[228:231],a[172:175],v[228:231],a[228:231]
v_mfma_f32_16x16x32_bf16 a[228:231],a[168:171],v[196:199],a[228:231]
v_mfma_f32_16x16x32_bf16 a[228:231],a[164:167],v[164:167],a[228:231]
v_mfma_f32_16x16x32_bf16 a[228:231],a[160:163],v[132:135],a[228:231]
v_mfma_f32_16x16x32_bf16 a[232:235],a[160:163],v[144:147],a[232:235]
buffer_load_dwordx4 v[184:187],v6,s[28:31],s77 offen offset:2048 sc1
v_mfma_f32_16x16x32_bf16 a[232:235],a[164:167],v[176:179],a[232:235]
ds_read_b64_tr_b16 a[160:161],v1 offset:256
ds_read_b64_tr_b16 a[162:163],v1 offset:5376
v_mfma_f32_16x16x32_bf16 a[232:235],a[168:171],v[208:211],a[232:235]
ds_read_b64_tr_b16 a[164:165],v1 offset:10496
v_mfma_f32_16x16x32_bf16 a[232:235],a[172:175],v[240:243],a[232:235]
ds_read_b64_tr_b16 a[166:167],v1 offset:15616
v_mfma_f32_16x16x32_bf16 a[240:243],a[176:179],v[128:131],a[240:243]
ds_read_b64_tr_b16 a[168:169],v1 offset:20736
v_mfma_f32_16x16x32_bf16 a[240:243],a[180:183],v[160:163],a[240:243]
buffer_load_dwordx4 v[188:191],v6,s[28:31],s77 offen offset:3072 sc1
v_mfma_f32_16x16x32_bf16 a[240:243],a[184:187],v[192:195],a[240:243]
s_add_u32 s77,s70,2
ds_read_b64_tr_b16 a[170:171],v1 offset:25856
ds_read_b64_tr_b16 a[172:173],v1 offset:30976
v_mfma_f32_16x16x32_bf16 a[240:243],a[188:191],v[224:227],a[240:243]
s_and_b32 s77,s77,3
s_lshl_b32 s77,s77,21
ds_read_b64_tr_b16 a[174:175],v1 offset:36096
v_mfma_f32_16x16x32_bf16 a[244:247],a[188:191],v[228:231],a[244:247]
s_add_u32 s77,s77,65536
s_max_i32 s77,s77,s101
v_mfma_f32_16x16x32_bf16 a[244:247],a[184:187],v[196:199],a[244:247]
v_mfma_f32_16x16x32_bf16 a[244:247],a[180:183],v[164:167],a[244:247]
buffer_load_dwordx4 v[200:203],v6,s[28:31],s78 offen sc1
v_mfma_f32_16x16x32_bf16 a[244:247],a[176:179],v[132:135],a[244:247]
v_mfma_f32_16x16x32_bf16 a[248:251],a[176:179],v[144:147],a[248:251]
ds_read_b64_tr_b16 a[176:177],v2 offset:288
ds_read_b64_tr_b16 a[178:179],v2 offset:5408
v_mfma_f32_16x16x32_bf16 a[248:251],a[180:183],v[176:179],a[248:251]
ds_read_b64_tr_b16 a[180:181],v2 offset:10528
v_mfma_f32_16x16x32_bf16 a[248:251],a[184:187],v[208:211],a[248:251]
ds_read_b64_tr_b16 a[182:183],v2 offset:15648
v_mfma_f32_16x16x32_bf16 a[248:251],a[188:191],v[240:243],a[248:251]
buffer_load_dwordx4 v[204:207],v6,s[28:31],s78 offen offset:1024 sc1
v_mfma_f32_16x16x32_bf16 a[0:3],a[96:99],v[128:131],a[0:3]
ds_read_b64_tr_b16 a[184:185],v2 offset:20768
ds_read_b64_tr_b16 a[186:187],v2 offset:25888
v_mfma_f32_16x16x32_bf16 a[0:3],a[100:103],v[160:163],a[0:3]
ds_read_b64_tr_b16 a[188:189],v2 offset:31008
v_mfma_f32_16x16x32_bf16 a[0:3],a[104:107],v[192:195],a[0:3]
ds_read_b64_tr_b16 a[190:191],v2 offset:36128
v_mfma_f32_16x16x32_bf16 a[0:3],a[108:111],v[224:227],a[0:3]
v_mfma_f32_16x16x32_bf16 a[4:7],a[108:111],v[228:231],a[4:7]
buffer_load_dwordx4 v[216:219],v6,s[28:31],s78 offen offset:2048 sc1
v_mfma_f32_16x16x32_bf16 a[4:7],a[104:107],v[196:199],a[4:7]
v_mfma_f32_16x16x32_bf16 a[4:7],a[100:103],v[164:167],a[4:7]
v_mfma_f32_16x16x32_bf16 a[4:7],a[96:99],v[132:135],a[4:7]
v_mfma_f32_16x16x32_bf16 a[8:11],a[96:99],v[144:147],a[8:11]
v_mfma_f32_16x16x32_bf16 a[8:11],a[100:103],v[176:179],a[8:11]
buffer_load_dwordx4 v[220:223],v6,s[28:31],s78 offen offset:3072 sc1
v_mfma_f32_16x16x32_bf16 a[8:11],a[104:107],v[208:211],a[8:11]
s_add_u32 s78,s70,1
s_and_b32 s78,s78,3
s_lshl_b32 s78,s78,21
v_mfma_f32_16x16x32_bf16 a[8:11],a[108:111],v[240:243],a[8:11]
s_add_u32 s78,s78,131072
s_max_i32 s78,s78,s101
v_mfma_f32_16x16x32_bf16 a[16:19],a[112:115],v[128:131],a[16:19]
v_mfma_f32_16x16x32_bf16 a[16:19],a[116:119],v[160:163],a[16:19]
v_mfma_f32_16x16x32_bf16 a[16:19],a[120:123],v[192:195],a[16:19]
buffer_load_dwordx4 v[232:235],v6,s[28:31],s79 offen sc1
v_mfma_f32_16x16x32_bf16 a[16:19],a[124:127],v[224:227],a[16:19]
v_mfma_f32_16x16x32_bf16 a[20:23],a[124:127],v[228:231],a[20:23]
v_mfma_f32_16x16x32_bf16 a[20:23],a[120:123],v[196:199],a[20:23]
v_mfma_f32_16x16x32_bf16 a[20:23],a[116:119],v[164:167],a[20:23]
v_mfma_f32_16x16x32_bf16 a[20:23],a[112:115],v[132:135],a[20:23]
buffer_load_dwordx4 v[236:239],v6,s[28:31],s79 offen offset:1024 sc1
v_mfma_f32_16x16x32_bf16 a[24:27],a[112:115],v[144:147],a[24:27]
v_mfma_f32_16x16x32_bf16 a[24:27],a[116:119],v[176:179],a[24:27]
v_mfma_f32_16x16x32_bf16 a[24:27],a[120:123],v[208:211],a[24:27]
v_mfma_f32_16x16x32_bf16 a[24:27],a[124:127],v[240:243],a[24:27]
s_waitcnt lgkmcnt(0)
v_mfma_f32_16x16x32_bf16 a[32:35],a[128:131],v[128:131],a[32:35]
buffer_load_dwordx4 v[248:251],v6,s[28:31],s79 offen offset:2048 sc1
v_mfma_f32_16x16x32_bf16 a[32:35],a[132:135],v[160:163],a[32:35]
v_mfma_f32_16x16x32_bf16 a[32:35],a[136:139],v[192:195],a[32:35]
v_mfma_f32_16x16x32_bf16 a[32:35],a[140:143],v[224:227],a[32:35]
v_mfma_f32_16x16x32_bf16 a[36:39],a[140:143],v[228:231],a[36:39]
v_mfma_f32_16x16x32_bf16 a[36:39],a[136:139],v[196:199],a[36:39]
buffer_load_dwordx4 v[252:255],v6,s[28:31],s79 offen offset:3072 sc1
v_mfma_f32_16x16x32_bf16 a[36:39],a[132:135],v[164:167],a[36:39]
v_mfma_f32_16x16x32_bf16 a[36:39],a[128:131],v[132:135],a[36:39]
v_mfma_f32_16x16x32_bf16 a[40:43],a[128:131],v[144:147],a[40:43]
v_mfma_f32_16x16x32_bf16 a[40:43],a[132:135],v[176:179],a[40:43]
v_mfma_f32_16x16x32_bf16 a[40:43],a[136:139],v[208:211],a[40:43]
s_waitcnt vmcnt(15)
v_mfma_f32_16x16x32_bf16 a[40:43],a[140:143],v[240:243],a[40:43]
s_barrier
ds_read_b64_tr_b16 a[96:97],v42
ds_read_b64_tr_b16 a[98:99],v42 offset:5120
v_mfma_f32_16x16x32_bf16 a[48:51],a[144:147],v[128:131],a[48:51]
ds_read_b64_tr_b16 a[100:101],v42 offset:10240
v_mfma_f32_16x16x32_bf16 a[48:51],a[148:151],v[160:163],a[48:51]
ds_read_b64_tr_b16 a[102:103],v42 offset:15360
v_mfma_f32_16x16x32_bf16 a[48:51],a[152:155],v[192:195],a[48:51]
ds_read_b64_tr_b16 a[104:105],v42 offset:20480
v_mfma_f32_16x16x32_bf16 a[48:51],a[156:159],v[224:227],a[48:51]
ds_read_b64_tr_b16 a[106:107],v42 offset:25600
ds_read_b64_tr_b16 a[108:109],v42 offset:30720
v_mfma_f32_16x16x32_bf16 a[52:55],a[156:159],v[228:231],a[52:55]
ds_read_b64_tr_b16 a[110:111],v42 offset:35840
v_mfma_f32_16x16x32_bf16 a[52:55],a[152:155],v[196:199],a[52:55]
ds_read_b64_tr_b16 a[112:113],v44
v_mfma_f32_16x16x32_bf16 a[52:55],a[148:151],v[164:167],a[52:55]
ds_read_b64_tr_b16 a[114:115],v44 offset:5120
v_mfma_f32_16x16x32_bf16 a[52:55],a[144:147],v[132:135],a[52:55]
ds_read_b64_tr_b16 a[116:117],v44 offset:10240
ds_read_b64_tr_b16 a[118:119],v44 offset:15360
v_mfma_f32_16x16x32_bf16 a[56:59],a[144:147],v[144:147],a[56:59]
ds_read_b64_tr_b16 a[120:121],v44 offset:20480
v_mfma_f32_16x16x32_bf16 a[56:59],a[148:151],v[176:179],a[56:59]
)A"
R"A(ds_read_b64_tr_b16 a[122:123],v44 offset:25600
v_mfma_f32_16x16x32_bf16 a[56:59],a[152:155],v[208:211],a[56:59]
ds_read_b64_tr_b16 a[124:125],v44 offset:30720
v_mfma_f32_16x16x32_bf16 a[56:59],a[156:159],v[240:243],a[56:59]
ds_read_b64_tr_b16 a[126:127],v44 offset:35840
ds_read_b64_tr_b16 a[128:129],v43 offset:32
v_mfma_f32_16x16x32_bf16 a[64:67],a[160:163],v[128:131],a[64:67]
ds_read_b64_tr_b16 a[130:131],v43 offset:5152
v_mfma_f32_16x16x32_bf16 a[64:67],a[164:167],v[160:163],a[64:67]
ds_read_b64_tr_b16 a[132:133],v43 offset:10272
v_mfma_f32_16x16x32_bf16 a[64:67],a[168:171],v[192:195],a[64:67]
ds_read_b64_tr_b16 a[134:135],v43 offset:15392
v_mfma_f32_16x16x32_bf16 a[64:67],a[172:175],v[224:227],a[64:67]
ds_read_b64_tr_b16 a[136:137],v43 offset:20512
ds_read_b64_tr_b16 a[138:139],v43 offset:25632
v_mfma_f32_16x16x32_bf16 a[68:71],a[172:175],v[228:231],a[68:71]
ds_read_b64_tr_b16 a[140:141],v43 offset:30752
v_mfma_f32_16x16x32_bf16 a[68:71],a[168:171],v[196:199],a[68:71]
ds_read_b64_tr_b16 a[142:143],v43 offset:35872
v_mfma_f32_16x16x32_bf16 a[68:71],a[164:167],v[164:167],a[68:71]
ds_read_b64_tr_b16 a[144:145],v45 offset:32
v_mfma_f32_16x16x32_bf16 a[68:71],a[160:163],v[132:135],a[68:71]
ds_read_b64_tr_b16 a[146:147],v45 offset:5152
ds_read_b64_tr_b16 a[148:149],v45 offset:10272
v_mfma_f32_16x16x32_bf16 a[72:75],a[160:163],v[144:147],a[72:75]
ds_read_b64_tr_b16 a[150:151],v45 offset:15392
v_mfma_f32_16x16x32_bf16 a[72:75],a[164:167],v[176:179],a[72:75]
ds_read_b64_tr_b16 a[152:153],v45 offset:20512
v_mfma_f32_16x16x32_bf16 a[72:75],a[168:171],v[208:211],a[72:75]
ds_read_b64_tr_b16 a[154:155],v45 offset:25632
v_mfma_f32_16x16x32_bf16 a[72:75],a[172:175],v[240:243],a[72:75]
ds_read_b64_tr_b16 a[156:157],v45 offset:30752
ds_read_b64_tr_b16 a[158:159],v45 offset:35872
v_mfma_f32_16x16x32_bf16 a[80:83],a[176:179],v[128:131],a[80:83]
ds_read_b64_tr_b16 a[160:161],v42 offset:64
v_mfma_f32_16x16x32_bf16 a[80:83],a[180:183],v[160:163],a[80:83]
ds_read_b64_tr_b16 a[162:163],v42 offset:5184
v_mfma_f32_16x16x32_bf16 a[80:83],a[184:187],v[192:195],a[80:83]
ds_read_b64_tr_b16 a[164:165],v42 offset:10304
v_mfma_f32_16x16x32_bf16 a[80:83],a[188:191],v[224:227],a[80:83]
ds_read_b64_tr_b16 a[166:167],v42 offset:15424
ds_read_b64_tr_b16 a[168:169],v42 offset:20544
v_mfma_f32_16x16x32_bf16 a[84:87],a[188:191],v[228:231],a[84:87]
ds_read_b64_tr_b16 a[170:171],v42 offset:25664
v_mfma_f32_16x16x32_bf16 a[84:87],a[184:187],v[196:199],a[84:87]
ds_read_b64_tr_b16 a[172:173],v42 offset:30784
v_mfma_f32_16x16x32_bf16 a[84:87],a[180:183],v[164:167],a[84:87]
ds_read_b64_tr_b16 a[174:175],v42 offset:35904
v_mfma_f32_16x16x32_bf16 a[84:87],a[176:179],v[132:135],a[84:87]
v_mfma_f32_16x16x32_bf16 a[88:91],a[176:179],v[144:147],a[88:91]
ds_read_b64_tr_b16 a[176:177],v44 offset:64
ds_read_b64_tr_b16 a[178:179],v44 offset:5184
v_mfma_f32_16x16x32_bf16 a[88:91],a[180:183],v[176:179],a[88:91]
ds_read_b64_tr_b16 a[180:181],v44 offset:10304
v_mfma_f32_16x16x32_bf16 a[88:91],a[184:187],v[208:211],a[88:91]
ds_read_b64_tr_b16 a[182:183],v44 offset:15424
v_mfma_f32_16x16x32_bf16 a[88:91],a[188:191],v[240:243],a[88:91]
s_waitcnt lgkmcnt(7)
ds_read_b64_tr_b16 a[184:185],v44 offset:20544
v_mfma_f32_16x16x32_bf16 a[192:195],a[96:99],v[64:67],a[192:195]
ds_read_b64_tr_b16 a[186:187],v44 offset:25664
ds_read_b64_tr_b16 a[188:189],v44 offset:30784
v_mfma_f32_16x16x32_bf16 a[192:195],a[100:103],v[68:71],a[192:195]
ds_read_b64_tr_b16 a[190:191],v44 offset:35904
v_mfma_f32_16x16x32_bf16 a[192:195],a[104:107],v[72:75],a[192:195]
v_mfma_f32_16x16x32_bf16 a[192:195],a[108:111],v[76:79],a[192:195]
v_mfma_f32_16x16x32_bf16 a[196:199],a[108:111],v[92:95],a[196:199]
v_mfma_f32_16x16x32_bf16 a[196:199],a[104:107],v[88:91],a[196:199]
v_mfma_f32_16x16x32_bf16 a[196:199],a[100:103],v[84:87],a[196:199]
v_mfma_f32_16x16x32_bf16 a[196:199],a[96:99],v[80:83],a[196:199]
v_mfma_f32_16x16x32_bf16 a[200:203],a[96:99],v[96:99],a[200:203]
ds_read_b64_tr_b16 a[96:97],v43 offset:96
ds_read_b64_tr_b16 a[98:99],v43 offset:5216
v_mfma_f32_16x16x32_bf16 a[200:203],a[100:103],v[100:103],a[200:203]
ds_read_b64_tr_b16 a[100:101],v43 offset:10336
v_mfma_f32_16x16x32_bf16 a[200:203],a[104:107],v[104:107],a[200:203]
ds_read_b64_tr_b16 a[102:103],v43 offset:15456
v_mfma_f32_16x16x32_bf16 a[200:203],a[108:111],v[108:111],a[200:203]
ds_read_b64_tr_b16 a[104:105],v43 offset:20576
v_mfma_f32_16x16x32_bf16 a[192:195],a[112:115],v[136:139],a[192:195]
s_waitcnt vmcnt(3)
ds_read_b64_tr_b16 a[106:107],v43 offset:25696
ds_read_b64_tr_b16 a[108:109],v43 offset:30816
v_mfma_f32_16x16x32_bf16 a[192:195],a[116:119],v[140:143],a[192:195]
ds_read_b64_tr_b16 a[110:111],v43 offset:35936
v_mfma_f32_16x16x32_bf16 a[192:195],a[120:123],v[152:155],a[192:195]
v_mfma_f32_16x16x32_bf16 a[192:195],a[124:127],v[156:159],a[192:195]
v_mfma_f32_16x16x32_bf16 a[196:199],a[124:127],v[188:191],a[196:199]
v_mfma_f32_16x16x32_bf16 a[196:199],a[120:123],v[184:187],a[196:199]
v_mfma_f32_16x16x32_bf16 a[196:199],a[116:119],v[172:175],a[196:199]
v_mfma_f32_16x16x32_bf16 a[196:199],a[112:115],v[168:171],a[196:199]
v_mfma_f32_16x16x32_bf16 a[200:203],a[112:115],v[200:203],a[200:203]
ds_read_b64_tr_b16 a[112:113],v45 offset:96
ds_read_b64_tr_b16 a[114:115],v45 offset:5216
v_mfma_f32_16x16x32_bf16 a[200:203],a[116:119],v[204:207],a[200:203]
v_accvgpr_read_b32 v128,a192
v_accvgpr_read_b32 v129,a193
v_mfma_f32_16x16x32_bf16 a[200:203],a[120:123],v[216:219],a[200:203]
v_cvt_pk_bf16_f32 v128,v128,v129
v_accvgpr_read_b32 v130,a194
v_mfma_f32_16x16x32_bf16 a[200:203],a[124:127],v[220:223],a[200:203]
v_accvgpr_read_b32 v131,a195
v_cvt_pk_bf16_f32 v129,v130,v131
v_mfma_f32_16x16x32_bf16 a[208:211],a[128:131],v[64:67],a[208:211]
ds_read_b64_tr_b16 a[116:117],v45 offset:10336
ds_read_b64_tr_b16 a[118:119],v45 offset:15456
v_mfma_f32_16x16x32_bf16 a[208:211],a[132:135],v[68:71],a[208:211]
v_accvgpr_read_b32 v132,a196
v_accvgpr_read_b32 v133,a197
v_mfma_f32_16x16x32_bf16 a[208:211],a[136:139],v[72:75],a[208:211]
v_cvt_pk_bf16_f32 v132,v132,v133
v_accvgpr_read_b32 v134,a198
v_mfma_f32_16x16x32_bf16 a[208:211],a[140:143],v[76:79],a[208:211]
v_accvgpr_read_b32 v135,a199
v_cvt_pk_bf16_f32 v133,v134,v135
v_mfma_f32_16x16x32_bf16 a[212:215],a[140:143],v[92:95],a[212:215]
ds_read_b64_tr_b16 a[120:121],v45 offset:20576
ds_read_b64_tr_b16 a[122:123],v45 offset:25696
v_mfma_f32_16x16x32_bf16 a[212:215],a[136:139],v[88:91],a[212:215]
ds_read_b64_tr_b16 a[124:125],v45 offset:30816
v_accvgpr_read_b32 v144,a200
v_mfma_f32_16x16x32_bf16 a[212:215],a[132:135],v[84:87],a[212:215]
ds_read_b64_tr_b16 a[126:127],v45 offset:35936
v_accvgpr_read_b32 v145,a201
v_mfma_f32_16x16x32_bf16 a[212:215],a[128:131],v[80:83],a[212:215]
v_cvt_pk_bf16_f32 v144,v144,v145
v_accvgpr_read_b32 v146,a202
v_mfma_f32_16x16x32_bf16 a[216:219],a[128:131],v[96:99],a[216:219]
v_accvgpr_read_b32 v147,a203
v_cvt_pk_bf16_f32 v145,v146,v147
v_mfma_f32_16x16x32_bf16 a[216:219],a[132:135],v[100:103],a[216:219]
ds_read_b64_tr_b16 a[128:129],v42 offset:128
ds_read_b64_tr_b16 a[130:131],v42 offset:5248
v_mfma_f32_16x16x32_bf16 a[216:219],a[136:139],v[104:107],a[216:219]
ds_read_b64_tr_b16 a[132:133],v42 offset:10368
v_mfma_f32_16x16x32_bf16 a[216:219],a[140:143],v[108:111],a[216:219]
ds_read_b64_tr_b16 a[134:135],v42 offset:15488
v_mfma_f32_16x16x32_bf16 a[208:211],a[144:147],v[136:139],a[208:211]
ds_read_b64_tr_b16 a[136:137],v42 offset:20608
v_mfma_f32_16x16x32_bf16 a[208:211],a[148:151],v[140:143],a[208:211]
ds_read_b64_tr_b16 a[138:139],v42 offset:25728
ds_read_b64_tr_b16 a[140:141],v42 offset:30848
v_mfma_f32_16x16x32_bf16 a[208:211],a[152:155],v[152:155],a[208:211]
ds_read_b64_tr_b16 a[142:143],v42 offset:35968
v_mfma_f32_16x16x32_bf16 a[208:211],a[156:159],v[156:159],a[208:211]
v_mfma_f32_16x16x32_bf16 a[212:215],a[156:159],v[188:191],a[212:215]
v_mfma_f32_16x16x32_bf16 a[212:215],a[152:155],v[184:187],a[212:215]
v_mfma_f32_16x16x32_bf16 a[212:215],a[148:151],v[172:175],a[212:215]
v_mfma_f32_16x16x32_bf16 a[212:215],a[144:147],v[168:171],a[212:215]
v_mfma_f32_16x16x32_bf16 a[216:219],a[144:147],v[200:203],a[216:219]
ds_read_b64_tr_b16 a[144:145],v44 offset:128
ds_read_b64_tr_b16 a[146:147],v44 offset:5248
v_mfma_f32_16x16x32_bf16 a[216:219],a[148:151],v[204:207],a[216:219]
v_accvgpr_read_b32 v130,a208
v_accvgpr_read_b32 v131,a209
v_mfma_f32_16x16x32_bf16 a[216:219],a[152:155],v[216:219],a[216:219]
v_accvgpr_read_b32 v12,a210
v_accvgpr_read_b32 v13,a211
v_mfma_f32_16x16x32_bf16 a[216:219],a[156:159],v[220:223],a[216:219]
v_cvt_pk_bf16_f32 v130,v130,v131
v_cvt_pk_bf16_f32 v131,v12,v13
v_mfma_f32_16x16x32_bf16 a[224:227],a[160:163],v[64:67],a[224:227]
buffer_store_dwordx4 v[128:131],v6,s[32:35],s84 offen sc1
v_mfma_f32_16x16x32_bf16 a[224:227],a[164:167],v[68:71],a[224:227]
s_waitcnt lgkmcnt(2)
v_accvgpr_read_b32 v134,a212
v_accvgpr_read_b32 v135,a213
v_mfma_f32_16x16x32_bf16 a[224:227],a[168:171],v[72:75],a[224:227]
v_accvgpr_read_b32 v14,a214
v_accvgpr_read_b32 v15,a215
v_mfma_f32_16x16x32_bf16 a[224:227],a[172:175],v[76:79],a[224:227]
v_cvt_pk_bf16_f32 v134,v134,v135
v_cvt_pk_bf16_f32 v135,v14,v15
v_mfma_f32_16x16x32_bf16 a[228:231],a[172:175],v[92:95],a[228:231]
buffer_store_dwordx4 v[132:135],v6,s[32:35],s85 offen sc1
v_mfma_f32_16x16x32_bf16 a[228:231],a[168:171],v[88:91],a[228:231]
v_accvgpr_read_b32 v146,a216
v_accvgpr_read_b32 v147,a217
v_mfma_f32_16x16x32_bf16 a[228:231],a[164:167],v[84:87],a[228:231]
v_accvgpr_read_b32 v16,a218
v_accvgpr_read_b32 v17,a219
v_mfma_f32_16x16x32_bf16 a[228:231],a[160:163],v[80:83],a[228:231]
v_cvt_pk_bf16_f32 v146,v146,v147
v_cvt_pk_bf16_f32 v147,v16,v17
v_mfma_f32_16x16x32_bf16 a[232:235],a[160:163],v[96:99],a[232:235]
buffer_store_dwordx4 v[144:147],v6,s[32:35],s86 offen sc1
v_mfma_f32_16x16x32_bf16 a[232:235],a[164:167],v[100:103],a[232:235]
ds_read_b64_tr_b16 a[148:149],v44 offset:10368
ds_read_b64_tr_b16 a[150:151],v44 offset:15488
v_mfma_f32_16x16x32_bf16 a[232:235],a[168:171],v[104:107],a[232:235]
ds_read_b64_tr_b16 a[152:153],v44 offset:20608
v_mfma_f32_16x16x32_bf16 a[232:235],a[172:175],v[108:111],a[232:235]
ds_read_b64_tr_b16 a[154:155],v44 offset:25728
v_mfma_f32_16x16x32_bf16 a[224:227],a[176:179],v[136:139],a[224:227]
ds_read_b64_tr_b16 a[156:157],v44 offset:30848
v_mfma_f32_16x16x32_bf16 a[224:227],a[180:183],v[140:143],a[224:227]
ds_read_b64_tr_b16 a[158:159],v44 offset:35968
ds_read_b64_tr_b16 a[160:161],v43 offset:160
v_mfma_f32_16x16x32_bf16 a[224:227],a[184:187],v[152:155],a[224:227]
ds_read_b64_tr_b16 a[162:163],v43 offset:5280
v_mfma_f32_16x16x32_bf16 a[224:227],a[188:191],v[156:159],a[224:227]
ds_read_b64_tr_b16 a[164:165],v43 offset:10400
v_mfma_f32_16x16x32_bf16 a[228:231],a[188:191],v[188:191],a[228:231]
ds_read_b64_tr_b16 a[166:167],v43 offset:15520
v_mfma_f32_16x16x32_bf16 a[228:231],a[184:187],v[184:187],a[228:231]
ds_read_b64_tr_b16 a[168:169],v43 offset:20640
ds_read_b64_tr_b16 a[170:171],v43 offset:25760
v_mfma_f32_16x16x32_bf16 a[228:231],a[180:183],v[172:175],a[228:231]
ds_read_b64_tr_b16 a[172:173],v43 offset:30880
v_mfma_f32_16x16x32_bf16 a[228:231],a[176:179],v[168:171],a[228:231]
ds_read_b64_tr_b16 a[174:175],v43 offset:36000
v_mfma_f32_16x16x32_bf16 a[232:235],a[176:179],v[200:203],a[232:235]
ds_read_b64_tr_b16 a[176:177],v45 offset:160
v_mfma_f32_16x16x32_bf16 a[232:235],a[180:183],v[204:207],a[232:235]
v_accvgpr_read_b32 v160,a224
v_accvgpr_read_b32 v161,a225
v_mfma_f32_16x16x32_bf16 a[232:235],a[184:187],v[216:219],a[232:235]
v_cvt_pk_bf16_f32 v160,v160,v161
v_accvgpr_read_b32 v162,a226
v_mfma_f32_16x16x32_bf16 a[232:235],a[188:191],v[220:223],a[232:235]
v_accvgpr_read_b32 v163,a227
v_cvt_pk_bf16_f32 v161,v162,v163
v_mfma_f32_16x16x32_bf16 a[240:243],a[96:99],v[64:67],a[240:243]
ds_read_b64_tr_b16 a[178:179],v45 offset:5280
ds_read_b64_tr_b16 a[180:181],v45 offset:10400
v_mfma_f32_16x16x32_bf16 a[240:243],a[100:103],v[68:71],a[240:243]
v_accvgpr_read_b32 v164,a228
v_accvgpr_read_b32 v165,a229
v_mfma_f32_16x16x32_bf16 a[240:243],a[104:107],v[72:75],a[240:243]
v_cvt_pk_bf16_f32 v164,v164,v165
)A"
R"A(v_accvgpr_read_b32 v166,a230
v_mfma_f32_16x16x32_bf16 a[240:243],a[108:111],v[76:79],a[240:243]
v_accvgpr_read_b32 v167,a231
v_cvt_pk_bf16_f32 v165,v166,v167
v_mfma_f32_16x16x32_bf16 a[244:247],a[108:111],v[92:95],a[244:247]
ds_read_b64_tr_b16 a[182:183],v45 offset:15520
ds_read_b64_tr_b16 a[184:185],v45 offset:20640
v_mfma_f32_16x16x32_bf16 a[244:247],a[104:107],v[88:91],a[244:247]
v_accvgpr_read_b32 v176,a232
v_accvgpr_read_b32 v177,a233
v_mfma_f32_16x16x32_bf16 a[244:247],a[100:103],v[84:87],a[244:247]
v_cvt_pk_bf16_f32 v176,v176,v177
v_accvgpr_read_b32 v178,a234
v_mfma_f32_16x16x32_bf16 a[244:247],a[96:99],v[80:83],a[244:247]
v_accvgpr_read_b32 v179,a235
v_cvt_pk_bf16_f32 v177,v178,v179
v_mfma_f32_16x16x32_bf16 a[248:251],a[96:99],v[96:99],a[248:251]
ds_read_b64_tr_b16 a[186:187],v45 offset:25760
ds_read_b64_tr_b16 a[188:189],v45 offset:30880
v_mfma_f32_16x16x32_bf16 a[248:251],a[100:103],v[100:103],a[248:251]
ds_read_b64_tr_b16 a[190:191],v45 offset:36000
v_mfma_f32_16x16x32_bf16 a[248:251],a[104:107],v[104:107],a[248:251]
ds_read_b64_tr_b16 a[96:97],v42 offset:192
v_mfma_f32_16x16x32_bf16 a[248:251],a[108:111],v[108:111],a[248:251]
ds_read_b64_tr_b16 a[98:99],v42 offset:5312
v_mfma_f32_16x16x32_bf16 a[240:243],a[112:115],v[136:139],a[240:243]
ds_read_b64_tr_b16 a[100:101],v42 offset:10432
ds_read_b64_tr_b16 a[102:103],v42 offset:15552
v_mfma_f32_16x16x32_bf16 a[240:243],a[116:119],v[140:143],a[240:243]
ds_read_b64_tr_b16 a[104:105],v42 offset:20672
v_mfma_f32_16x16x32_bf16 a[240:243],a[120:123],v[152:155],a[240:243]
ds_read_b64_tr_b16 a[106:107],v42 offset:25792
v_mfma_f32_16x16x32_bf16 a[240:243],a[124:127],v[156:159],a[240:243]
ds_read_b64_tr_b16 a[108:109],v42 offset:30912
v_mfma_f32_16x16x32_bf16 a[244:247],a[124:127],v[188:191],a[244:247]
ds_read_b64_tr_b16 a[110:111],v42 offset:36032
v_mfma_f32_16x16x32_bf16 a[244:247],a[120:123],v[184:187],a[244:247]
v_mfma_f32_16x16x32_bf16 a[244:247],a[116:119],v[172:175],a[244:247]
v_mfma_f32_16x16x32_bf16 a[244:247],a[112:115],v[168:171],a[244:247]
v_mfma_f32_16x16x32_bf16 a[248:251],a[112:115],v[200:203],a[248:251]
ds_read_b64_tr_b16 a[112:113],v44 offset:192
ds_read_b64_tr_b16 a[114:115],v44 offset:5312
v_mfma_f32_16x16x32_bf16 a[248:251],a[116:119],v[204:207],a[248:251]
v_accvgpr_read_b32 v162,a240
v_accvgpr_read_b32 v163,a241
v_mfma_f32_16x16x32_bf16 a[248:251],a[120:123],v[216:219],a[248:251]
v_accvgpr_read_b32 v12,a242
v_accvgpr_read_b32 v13,a243
v_mfma_f32_16x16x32_bf16 a[248:251],a[124:127],v[220:223],a[248:251]
v_cvt_pk_bf16_f32 v162,v162,v163
v_cvt_pk_bf16_f32 v163,v12,v13
v_mfma_f32_16x16x32_bf16 a[0:3],a[128:131],v[64:67],a[0:3]
buffer_store_dwordx4 v[160:163],v6,s[32:35],s84 offen offset:1024 sc1
v_mfma_f32_16x16x32_bf16 a[0:3],a[132:135],v[68:71],a[0:3]
v_accvgpr_read_b32 v166,a244
v_accvgpr_read_b32 v167,a245
v_mfma_f32_16x16x32_bf16 a[0:3],a[136:139],v[72:75],a[0:3]
v_accvgpr_read_b32 v14,a246
v_accvgpr_read_b32 v15,a247
v_mfma_f32_16x16x32_bf16 a[0:3],a[140:143],v[76:79],a[0:3]
v_cvt_pk_bf16_f32 v166,v166,v167
v_cvt_pk_bf16_f32 v167,v14,v15
v_mfma_f32_16x16x32_bf16 a[4:7],a[140:143],v[92:95],a[4:7]
buffer_store_dwordx4 v[164:167],v6,s[32:35],s85 offen offset:1024 sc1
v_mfma_f32_16x16x32_bf16 a[4:7],a[136:139],v[88:91],a[4:7]
v_accvgpr_read_b32 v178,a248
v_accvgpr_read_b32 v179,a249
v_mfma_f32_16x16x32_bf16 a[4:7],a[132:135],v[84:87],a[4:7]
v_accvgpr_read_b32 v16,a250
v_accvgpr_read_b32 v17,a251
v_cvt_pk_bf16_f32 v178,v178,v179
v_cvt_pk_bf16_f32 v179,v16,v17
v_mfma_f32_16x16x32_bf16 a[4:7],a[128:131],v[80:83],a[4:7]
ds_read_b64_tr_b16 a[116:117],v44 offset:10432
ds_read_b64_tr_b16 a[118:119],v44 offset:15552
v_mfma_f32_16x16x32_bf16 a[8:11],a[128:131],v[96:99],a[8:11]
buffer_store_dwordx4 v[176:179],v6,s[32:35],s86 offen offset:1024 sc1
v_mfma_f32_16x16x32_bf16 a[8:11],a[132:135],v[100:103],a[8:11]
ds_read_b64_tr_b16 a[120:121],v44 offset:20672
ds_read_b64_tr_b16 a[122:123],v44 offset:25792
v_mfma_f32_16x16x32_bf16 a[8:11],a[136:139],v[104:107],a[8:11]
ds_read_b64_tr_b16 a[124:125],v44 offset:30912
v_mfma_f32_16x16x32_bf16 a[8:11],a[140:143],v[108:111],a[8:11]
s_waitcnt lgkmcnt(5)
ds_read_b64_tr_b16 a[126:127],v44 offset:36032
v_mfma_f32_16x16x32_bf16 a[0:3],a[144:147],v[136:139],a[0:3]
ds_read_b64_tr_b16 a[128:129],v43 offset:224
v_mfma_f32_16x16x32_bf16 a[0:3],a[148:151],v[140:143],a[0:3]
ds_read_b64_tr_b16 a[130:131],v43 offset:5344
ds_read_b64_tr_b16 a[132:133],v43 offset:10464
v_mfma_f32_16x16x32_bf16 a[0:3],a[152:155],v[152:155],a[0:3]
ds_read_b64_tr_b16 a[134:135],v43 offset:15584
v_mfma_f32_16x16x32_bf16 a[0:3],a[156:159],v[156:159],a[0:3]
ds_read_b64_tr_b16 a[136:137],v43 offset:20704
v_mfma_f32_16x16x32_bf16 a[4:7],a[156:159],v[188:191],a[4:7]
ds_read_b64_tr_b16 a[138:139],v43 offset:25824
v_mfma_f32_16x16x32_bf16 a[4:7],a[152:155],v[184:187],a[4:7]
ds_read_b64_tr_b16 a[140:141],v43 offset:30944
ds_read_b64_tr_b16 a[142:143],v43 offset:36064
v_mfma_f32_16x16x32_bf16 a[4:7],a[148:151],v[172:175],a[4:7]
v_mfma_f32_16x16x32_bf16 a[4:7],a[144:147],v[168:171],a[4:7]
v_mfma_f32_16x16x32_bf16 a[8:11],a[144:147],v[200:203],a[8:11]
ds_read_b64_tr_b16 a[144:145],v45 offset:224
ds_read_b64_tr_b16 a[146:147],v45 offset:5344
v_mfma_f32_16x16x32_bf16 a[8:11],a[148:151],v[204:207],a[8:11]
v_accvgpr_read_b32 v192,a0
v_accvgpr_read_b32 v193,a1
v_mfma_f32_16x16x32_bf16 a[8:11],a[152:155],v[216:219],a[8:11]
v_cvt_pk_bf16_f32 v192,v192,v193
v_accvgpr_read_b32 v194,a2
v_mfma_f32_16x16x32_bf16 a[8:11],a[156:159],v[220:223],a[8:11]
v_accvgpr_read_b32 v195,a3
v_cvt_pk_bf16_f32 v193,v194,v195
v_mfma_f32_16x16x32_bf16 a[16:19],a[160:163],v[64:67],a[16:19]
ds_read_b64_tr_b16 a[148:149],v45 offset:10464
ds_read_b64_tr_b16 a[150:151],v45 offset:15584
v_mfma_f32_16x16x32_bf16 a[16:19],a[164:167],v[68:71],a[16:19]
v_accvgpr_read_b32 v196,a4
v_accvgpr_read_b32 v197,a5
v_mfma_f32_16x16x32_bf16 a[16:19],a[168:171],v[72:75],a[16:19]
v_cvt_pk_bf16_f32 v196,v196,v197
v_accvgpr_read_b32 v198,a6
v_mfma_f32_16x16x32_bf16 a[16:19],a[172:175],v[76:79],a[16:19]
v_accvgpr_read_b32 v199,a7
v_cvt_pk_bf16_f32 v197,v198,v199
v_mfma_f32_16x16x32_bf16 a[20:23],a[172:175],v[92:95],a[20:23]
ds_read_b64_tr_b16 a[152:153],v45 offset:20704
ds_read_b64_tr_b16 a[154:155],v45 offset:25824
v_mfma_f32_16x16x32_bf16 a[20:23],a[168:171],v[88:91],a[20:23]
ds_read_b64_tr_b16 a[156:157],v45 offset:30944
v_accvgpr_read_b32 v208,a8
v_mfma_f32_16x16x32_bf16 a[20:23],a[164:167],v[84:87],a[20:23]
ds_read_b64_tr_b16 a[158:159],v45 offset:36064
v_accvgpr_read_b32 v209,a9
v_mfma_f32_16x16x32_bf16 a[20:23],a[160:163],v[80:83],a[20:23]
v_cvt_pk_bf16_f32 v208,v208,v209
v_accvgpr_read_b32 v210,a10
v_mfma_f32_16x16x32_bf16 a[24:27],a[160:163],v[96:99],a[24:27]
v_accvgpr_read_b32 v211,a11
v_cvt_pk_bf16_f32 v209,v210,v211
v_mfma_f32_16x16x32_bf16 a[24:27],a[164:167],v[100:103],a[24:27]
ds_read_b64_tr_b16 a[160:161],v42 offset:256
ds_read_b64_tr_b16 a[162:163],v42 offset:5376
v_mfma_f32_16x16x32_bf16 a[24:27],a[168:171],v[104:107],a[24:27]
ds_read_b64_tr_b16 a[164:165],v42 offset:10496
v_mfma_f32_16x16x32_bf16 a[24:27],a[172:175],v[108:111],a[24:27]
ds_read_b64_tr_b16 a[166:167],v42 offset:15616
v_mfma_f32_16x16x32_bf16 a[16:19],a[176:179],v[136:139],a[16:19]
ds_read_b64_tr_b16 a[168:169],v42 offset:20736
v_mfma_f32_16x16x32_bf16 a[16:19],a[180:183],v[140:143],a[16:19]
ds_read_b64_tr_b16 a[170:171],v42 offset:25856
ds_read_b64_tr_b16 a[172:173],v42 offset:30976
v_mfma_f32_16x16x32_bf16 a[16:19],a[184:187],v[152:155],a[16:19]
ds_read_b64_tr_b16 a[174:175],v42 offset:36096
v_mfma_f32_16x16x32_bf16 a[16:19],a[188:191],v[156:159],a[16:19]
v_mfma_f32_16x16x32_bf16 a[20:23],a[188:191],v[188:191],a[20:23]
v_mfma_f32_16x16x32_bf16 a[20:23],a[184:187],v[184:187],a[20:23]
v_mfma_f32_16x16x32_bf16 a[20:23],a[180:183],v[172:175],a[20:23]
v_mfma_f32_16x16x32_bf16 a[20:23],a[176:179],v[168:171],a[20:23]
v_mfma_f32_16x16x32_bf16 a[24:27],a[176:179],v[200:203],a[24:27]
ds_read_b64_tr_b16 a[176:177],v44 offset:256
ds_read_b64_tr_b16 a[178:179],v44 offset:5376
v_mfma_f32_16x16x32_bf16 a[24:27],a[180:183],v[204:207],a[24:27]
s_lshl_b32 s95,s42,2
s_add_u32 s95,s95,s40
s_lshl_b32 s95,s95,4
v_mfma_f32_16x16x32_bf16 a[24:27],a[184:187],v[216:219],a[24:27]
s_add_u32 s95,s95,5120
v_bfe_u32 v19,s55,13,1
v_mov_b32 v18,s68
v_or_b32 v19,s69,v19
v_mfma_f32_16x16x32_bf16 a[24:27],a[188:191],v[220:223],a[24:27]
s_waitcnt vmcnt(0)
s_mov_b64 exec,1
buffer_store_dwordx2 v[18:19],v6,s[64:67],s95 offen sc1
s_mov_b64 exec,-1
v_mfma_f32_16x16x32_bf16 a[32:35],a[96:99],v[64:67],a[32:35]
v_accvgpr_read_b32 v194,a16
v_accvgpr_read_b32 v195,a17
v_mfma_f32_16x16x32_bf16 a[32:35],a[100:103],v[68:71],a[32:35]
v_accvgpr_read_b32 v12,a18
v_accvgpr_read_b32 v13,a19
v_mfma_f32_16x16x32_bf16 a[32:35],a[104:107],v[72:75],a[32:35]
v_cvt_pk_bf16_f32 v194,v194,v195
v_cvt_pk_bf16_f32 v195,v12,v13
v_mfma_f32_16x16x32_bf16 a[32:35],a[108:111],v[76:79],a[32:35]
buffer_store_dwordx4 v[192:195],v6,s[32:35],s84 offen offset:2048 sc1
v_mfma_f32_16x16x32_bf16 a[36:39],a[108:111],v[92:95],a[36:39]
v_accvgpr_read_b32 v198,a20
v_accvgpr_read_b32 v199,a21
v_mfma_f32_16x16x32_bf16 a[36:39],a[104:107],v[88:91],a[36:39]
v_accvgpr_read_b32 v14,a22
v_accvgpr_read_b32 v15,a23
v_mfma_f32_16x16x32_bf16 a[36:39],a[100:103],v[84:87],a[36:39]
v_cvt_pk_bf16_f32 v198,v198,v199
v_cvt_pk_bf16_f32 v199,v14,v15
v_mfma_f32_16x16x32_bf16 a[36:39],a[96:99],v[80:83],a[36:39]
buffer_store_dwordx4 v[196:199],v6,s[32:35],s85 offen offset:2048 sc1
v_mfma_f32_16x16x32_bf16 a[40:43],a[96:99],v[96:99],a[40:43]
v_accvgpr_read_b32 v210,a24
v_accvgpr_read_b32 v211,a25
v_mfma_f32_16x16x32_bf16 a[40:43],a[100:103],v[100:103],a[40:43]
v_accvgpr_read_b32 v16,a26
v_accvgpr_read_b32 v17,a27
v_mfma_f32_16x16x32_bf16 a[40:43],a[104:107],v[104:107],a[40:43]
v_cvt_pk_bf16_f32 v210,v210,v211
v_cvt_pk_bf16_f32 v211,v16,v17
v_mfma_f32_16x16x32_bf16 a[40:43],a[108:111],v[108:111],a[40:43]
buffer_store_dwordx4 v[208:211],v6,s[32:35],s86 offen offset:2048 sc1
v_mfma_f32_16x16x32_bf16 a[32:35],a[112:115],v[136:139],a[32:35]
s_waitcnt lgkmcnt(0)
ds_read_b64_tr_b16 a[180:181],v44 offset:10496
ds_read_b64_tr_b16 a[182:183],v44 offset:15616
v_mfma_f32_16x16x32_bf16 a[32:35],a[116:119],v[140:143],a[32:35]
ds_read_b64_tr_b16 a[184:185],v44 offset:20736
v_mfma_f32_16x16x32_bf16 a[32:35],a[120:123],v[152:155],a[32:35]
ds_read_b64_tr_b16 a[186:187],v44 offset:25856
v_mfma_f32_16x16x32_bf16 a[32:35],a[124:127],v[156:159],a[32:35]
ds_read_b64_tr_b16 a[188:189],v44 offset:30976
v_mfma_f32_16x16x32_bf16 a[36:39],a[124:127],v[188:191],a[36:39]
ds_read_b64_tr_b16 a[190:191],v44 offset:36096
ds_read_b64_tr_b16 a[96:97],v43 offset:288
v_mfma_f32_16x16x32_bf16 a[36:39],a[120:123],v[184:187],a[36:39]
ds_read_b64_tr_b16 a[98:99],v43 offset:5408
v_mfma_f32_16x16x32_bf16 a[36:39],a[116:119],v[172:175],a[36:39]
ds_read_b64_tr_b16 a[100:101],v43 offset:10528
v_mfma_f32_16x16x32_bf16 a[36:39],a[112:115],v[168:171],a[36:39]
ds_read_b64_tr_b16 a[102:103],v43 offset:15648
v_mfma_f32_16x16x32_bf16 a[40:43],a[112:115],v[200:203],a[40:43]
ds_read_b64_tr_b16 a[104:105],v43 offset:20768
ds_read_b64_tr_b16 a[106:107],v43 offset:25888
v_mfma_f32_16x16x32_bf16 a[40:43],a[116:119],v[204:207],a[40:43]
v_accvgpr_read_b32 v224,a32
v_accvgpr_read_b32 v225,a33
v_mfma_f32_16x16x32_bf16 a[40:43],a[120:123],v[216:219],a[40:43]
s_lshl_b32 s94,s71,2
v_cvt_pk_bf16_f32 v224,v224,v225
v_accvgpr_read_b32 v226,a34
v_mfma_f32_16x16x32_bf16 a[40:43],a[124:127],v[220:223],a[40:43]
s_add_u32 s94,s94,s40
v_accvgpr_read_b32 v227,a35
v_cvt_pk_bf16_f32 v225,v226,v227
v_mfma_f32_16x16x32_bf16 a[48:51],a[128:131],v[64:67],a[48:51]
s_lshl_b32 s94,s94,4
v_bfe_u32 v24,v6,4,2
v_min_u32 v24,2,v24
v_mfma_f32_16x16x32_bf16 a[48:51],a[132:135],v[68:71],a[48:51]
s_add_u32 s94,s94,5120
v_accvgpr_read_b32 v228,a36
v_accvgpr_read_b32 v229,a37
v_mfma_f32_16x16x32_bf16 a[48:51],a[136:139],v[72:75],a[48:51]
v_cvt_pk_bf16_f32 v228,v228,v229
v_accvgpr_read_b32 v230,a38
v_mfma_f32_16x16x32_bf16 a[48:51],a[140:143],v[76:79],a[48:51]
v_accvgpr_read_b32 v231,a39
v_cvt_pk_bf16_f32 v229,v230,v231
)A"
R"A(v_mfma_f32_16x16x32_bf16 a[52:55],a[140:143],v[92:95],a[52:55]
v_add_u32 v24,s70,v24
v_add_u32 v24,1,v24
v_mfma_f32_16x16x32_bf16 a[52:55],a[136:139],v[88:91],a[52:55]
v_accvgpr_read_b32 v240,a40
v_accvgpr_read_b32 v241,a41
v_mfma_f32_16x16x32_bf16 a[52:55],a[132:135],v[84:87],a[52:55]
v_cvt_pk_bf16_f32 v240,v240,v241
v_accvgpr_read_b32 v242,a42
v_mfma_f32_16x16x32_bf16 a[52:55],a[128:131],v[80:83],a[52:55]
v_accvgpr_read_b32 v243,a43
v_cvt_pk_bf16_f32 v241,v242,v243
v_mfma_f32_16x16x32_bf16 a[56:59],a[128:131],v[96:99],a[56:59]
v_and_b32 v24,3,v24
v_lshlrev_b32 v24,8,v24
v_mfma_f32_16x16x32_bf16 a[56:59],a[132:135],v[100:103],a[56:59]
ds_read_b64_tr_b16 a[108:109],v43 offset:31008
ds_read_b64_tr_b16 a[110:111],v43 offset:36128
v_mfma_f32_16x16x32_bf16 a[56:59],a[136:139],v[104:107],a[56:59]
ds_read_b64_tr_b16 a[112:113],v45 offset:288
v_mfma_f32_16x16x32_bf16 a[56:59],a[140:143],v[108:111],a[56:59]
ds_read_b64_tr_b16 a[114:115],v45 offset:5408
v_mfma_f32_16x16x32_bf16 a[48:51],a[144:147],v[136:139],a[48:51]
ds_read_b64_tr_b16 a[116:117],v45 offset:10528
v_mfma_f32_16x16x32_bf16 a[48:51],a[148:151],v[140:143],a[48:51]
ds_read_b64_tr_b16 a[118:119],v45 offset:15648
ds_read_b64_tr_b16 a[120:121],v45 offset:20768
v_mfma_f32_16x16x32_bf16 a[48:51],a[152:155],v[152:155],a[48:51]
ds_read_b64_tr_b16 a[122:123],v45 offset:25888
v_mfma_f32_16x16x32_bf16 a[48:51],a[156:159],v[156:159],a[48:51]
ds_read_b64_tr_b16 a[124:125],v45 offset:31008
v_mfma_f32_16x16x32_bf16 a[52:55],a[156:159],v[188:191],a[52:55]
ds_read_b64_tr_b16 a[126:127],v45 offset:36128
v_mfma_f32_16x16x32_bf16 a[52:55],a[152:155],v[184:187],a[52:55]
ds_read_b64_tr_b16 a[128:129],v1
ds_read_b64_tr_b16 a[130:131],v1 offset:5120
v_mfma_f32_16x16x32_bf16 a[52:55],a[148:151],v[172:175],a[52:55]
ds_read_b64_tr_b16 a[132:133],v1 offset:10240
v_mfma_f32_16x16x32_bf16 a[52:55],a[144:147],v[168:171],a[52:55]
ds_read_b64_tr_b16 a[134:135],v1 offset:15360
v_mfma_f32_16x16x32_bf16 a[56:59],a[144:147],v[200:203],a[56:59]
ds_read_b64_tr_b16 a[136:137],v1 offset:20480
v_mfma_f32_16x16x32_bf16 a[56:59],a[148:151],v[204:207],a[56:59]
v_accvgpr_read_b32 v226,a48
v_accvgpr_read_b32 v227,a49
v_mfma_f32_16x16x32_bf16 a[56:59],a[152:155],v[216:219],a[56:59]
v_accvgpr_read_b32 v12,a50
v_accvgpr_read_b32 v13,a51
v_mfma_f32_16x16x32_bf16 a[56:59],a[156:159],v[220:223],a[56:59]
v_cvt_pk_bf16_f32 v226,v226,v227
v_cvt_pk_bf16_f32 v227,v12,v13
v_mfma_f32_16x16x32_bf16 a[64:67],a[160:163],v[64:67],a[64:67]
buffer_store_dwordx4 v[224:227],v6,s[32:35],s84 offen offset:3072 sc1
v_mfma_f32_16x16x32_bf16 a[64:67],a[164:167],v[68:71],a[64:67]
v_accvgpr_read_b32 v230,a52
v_accvgpr_read_b32 v231,a53
v_mfma_f32_16x16x32_bf16 a[64:67],a[168:171],v[72:75],a[64:67]
v_accvgpr_read_b32 v14,a54
v_accvgpr_read_b32 v15,a55
v_mfma_f32_16x16x32_bf16 a[64:67],a[172:175],v[76:79],a[64:67]
v_cvt_pk_bf16_f32 v230,v230,v231
v_cvt_pk_bf16_f32 v231,v14,v15
v_mfma_f32_16x16x32_bf16 a[68:71],a[172:175],v[92:95],a[68:71]
buffer_store_dwordx4 v[228:231],v6,s[32:35],s85 offen offset:3072 sc1
v_mfma_f32_16x16x32_bf16 a[68:71],a[168:171],v[88:91],a[68:71]
v_accvgpr_read_b32 v242,a56
v_accvgpr_read_b32 v243,a57
v_mfma_f32_16x16x32_bf16 a[68:71],a[164:167],v[84:87],a[68:71]
v_accvgpr_read_b32 v16,a58
v_accvgpr_read_b32 v17,a59
v_mfma_f32_16x16x32_bf16 a[68:71],a[160:163],v[80:83],a[68:71]
v_cvt_pk_bf16_f32 v242,v242,v243
v_cvt_pk_bf16_f32 v243,v16,v17
v_mfma_f32_16x16x32_bf16 a[72:75],a[160:163],v[96:99],a[72:75]
buffer_store_dwordx4 v[240:243],v6,s[32:35],s86 offen offset:3072 sc1
v_mfma_f32_16x16x32_bf16 a[72:75],a[164:167],v[100:103],a[72:75]
ds_read_b64_tr_b16 a[138:139],v1 offset:25600
ds_read_b64_tr_b16 a[140:141],v1 offset:30720
v_mfma_f32_16x16x32_bf16 a[72:75],a[168:171],v[104:107],a[72:75]
ds_read_b64_tr_b16 a[142:143],v1 offset:35840
v_mfma_f32_16x16x32_bf16 a[72:75],a[172:175],v[108:111],a[72:75]
ds_read_b64_tr_b16 a[144:145],v42
v_mfma_f32_16x16x32_bf16 a[64:67],a[176:179],v[136:139],a[64:67]
s_waitcnt lgkmcnt(4)
ds_read_b64_tr_b16 a[146:147],v42 offset:5120
v_mfma_f32_16x16x32_bf16 a[64:67],a[180:183],v[140:143],a[64:67]
ds_read_b64_tr_b16 a[148:149],v42 offset:10240
ds_read_b64_tr_b16 a[150:151],v42 offset:15360
v_mfma_f32_16x16x32_bf16 a[64:67],a[184:187],v[152:155],a[64:67]
buffer_load_dwordx2 v[26:27],v24,s[64:67],s94 offen sc1
v_mfma_f32_16x16x32_bf16 a[64:67],a[188:191],v[156:159],a[64:67]
ds_read_b64_tr_b16 a[152:153],v42 offset:20480
ds_read_b64_tr_b16 a[154:155],v42 offset:25600
v_mfma_f32_16x16x32_bf16 a[68:71],a[188:191],v[188:191],a[68:71]
ds_read_b64_tr_b16 a[156:157],v42 offset:30720
v_mfma_f32_16x16x32_bf16 a[68:71],a[184:187],v[184:187],a[68:71]
ds_read_b64_tr_b16 a[158:159],v42 offset:35840
v_mfma_f32_16x16x32_bf16 a[68:71],a[180:183],v[172:175],a[68:71]
ds_read_b64_tr_b16 a[160:161],v44
v_mfma_f32_16x16x32_bf16 a[68:71],a[176:179],v[168:171],a[68:71]
ds_read_b64_tr_b16 a[162:163],v44 offset:5120
ds_read_b64_tr_b16 a[164:165],v44 offset:10240
v_mfma_f32_16x16x32_bf16 a[72:75],a[176:179],v[200:203],a[72:75]
ds_read_b64_tr_b16 a[166:167],v44 offset:15360
v_mfma_f32_16x16x32_bf16 a[72:75],a[180:183],v[204:207],a[72:75]
v_accvgpr_read_b32 v128,a64
v_accvgpr_read_b32 v129,a65
v_mfma_f32_16x16x32_bf16 a[72:75],a[184:187],v[216:219],a[72:75]
v_cvt_pk_bf16_f32 v128,v128,v129
v_accvgpr_read_b32 v130,a66
v_mfma_f32_16x16x32_bf16 a[72:75],a[188:191],v[220:223],a[72:75]
v_accvgpr_read_b32 v131,a67
v_cvt_pk_bf16_f32 v129,v130,v131
v_mfma_f32_16x16x32_bf16 a[80:83],a[96:99],v[64:67],a[80:83]
ds_read_b64_tr_b16 a[168:169],v44 offset:20480
ds_read_b64_tr_b16 a[170:171],v44 offset:25600
v_mfma_f32_16x16x32_bf16 a[80:83],a[100:103],v[68:71],a[80:83]
ds_read_b64_tr_b16 a[172:173],v44 offset:30720
v_accvgpr_read_b32 v132,a68
v_mfma_f32_16x16x32_bf16 a[80:83],a[104:107],v[72:75],a[80:83]
ds_read_b64_tr_b16 a[174:175],v44 offset:35840
v_accvgpr_read_b32 v133,a69
v_mfma_f32_16x16x32_bf16 a[80:83],a[108:111],v[76:79],a[80:83]
ds_read_b64_tr_b16 a[176:177],v2 offset:32
v_cvt_pk_bf16_f32 v132,v132,v133
v_mfma_f32_16x16x32_bf16 a[84:87],a[108:111],v[92:95],a[84:87]
ds_read_b64_tr_b16 a[178:179],v2 offset:5152
v_accvgpr_read_b32 v134,a70
v_mfma_f32_16x16x32_bf16 a[84:87],a[104:107],v[88:91],a[84:87]
v_accvgpr_read_b32 v144,a72
v_accvgpr_read_b32 v145,a73
v_mfma_f32_16x16x32_bf16 a[84:87],a[100:103],v[84:87],a[84:87]
v_accvgpr_read_b32 v135,a71
v_cvt_pk_bf16_f32 v144,v144,v145
v_mfma_f32_16x16x32_bf16 a[84:87],a[96:99],v[80:83],a[84:87]
v_accvgpr_read_b32 v146,a74
v_accvgpr_read_b32 v147,a75
v_mfma_f32_16x16x32_bf16 a[88:91],a[96:99],v[96:99],a[88:91]
ds_read_b64_tr_b16 a[180:181],v2 offset:10272
ds_read_b64_tr_b16 a[182:183],v2 offset:15392
v_mfma_f32_16x16x32_bf16 a[88:91],a[100:103],v[100:103],a[88:91]
v_cvt_pk_bf16_f32 v133,v134,v135
v_cvt_pk_bf16_f32 v145,v146,v147
v_mfma_f32_16x16x32_bf16 a[88:91],a[104:107],v[104:107],a[88:91]
ds_read_b64_tr_b16 a[184:185],v2 offset:20512
ds_read_b64_tr_b16 a[186:187],v2 offset:25632
v_mfma_f32_16x16x32_bf16 a[88:91],a[108:111],v[108:111],a[88:91]
ds_read_b64_tr_b16 a[188:189],v2 offset:30752
v_mfma_f32_16x16x32_bf16 a[80:83],a[112:115],v[136:139],a[80:83]
ds_read_b64_tr_b16 a[190:191],v2 offset:35872
v_mfma_f32_16x16x32_bf16 a[80:83],a[116:119],v[140:143],a[80:83]
ds_read_b64_tr_b16 a[96:97],v43 offset:32
v_mfma_f32_16x16x32_bf16 a[80:83],a[120:123],v[152:155],a[80:83]
ds_read_b64_tr_b16 a[98:99],v43 offset:5152
ds_read_b64_tr_b16 a[100:101],v43 offset:10272
v_mfma_f32_16x16x32_bf16 a[80:83],a[124:127],v[156:159],a[80:83]
ds_read_b64_tr_b16 a[102:103],v43 offset:15392
v_mfma_f32_16x16x32_bf16 a[84:87],a[124:127],v[188:191],a[84:87]
ds_read_b64_tr_b16 a[104:105],v43 offset:20512
v_mfma_f32_16x16x32_bf16 a[84:87],a[120:123],v[184:187],a[84:87]
ds_read_b64_tr_b16 a[106:107],v43 offset:25632
v_mfma_f32_16x16x32_bf16 a[84:87],a[116:119],v[172:175],a[84:87]
ds_read_b64_tr_b16 a[108:109],v43 offset:30752
ds_read_b64_tr_b16 a[110:111],v43 offset:35872
v_mfma_f32_16x16x32_bf16 a[84:87],a[112:115],v[168:171],a[84:87]
v_mfma_f32_16x16x32_bf16 a[88:91],a[112:115],v[200:203],a[88:91]
ds_read_b64_tr_b16 a[112:113],v45 offset:32
ds_read_b64_tr_b16 a[114:115],v45 offset:5152
v_mfma_f32_16x16x32_bf16 a[88:91],a[116:119],v[204:207],a[88:91]
v_accvgpr_read_b32 v130,a80
v_accvgpr_read_b32 v131,a81
v_mfma_f32_16x16x32_bf16 a[88:91],a[120:123],v[216:219],a[88:91]
v_accvgpr_read_b32 v12,a82
v_accvgpr_read_b32 v13,a83
v_mfma_f32_16x16x32_bf16 a[88:91],a[124:127],v[220:223],a[88:91]
v_cvt_pk_bf16_f32 v130,v130,v131
v_cvt_pk_bf16_f32 v131,v12,v13
v_mfma_f32_16x16x32_bf16 a[204:207],a[128:131],v[148:151],a[204:207]
buffer_store_dwordx4 v[128:131],v25,s[32:35],s84 offen sc1
v_mfma_f32_16x16x32_bf16 a[204:207],a[132:135],v[180:183],a[204:207]
s_waitcnt lgkmcnt(4)
v_accvgpr_read_b32 v134,a84
v_accvgpr_read_b32 v135,a85
v_mfma_f32_16x16x32_bf16 a[204:207],a[136:139],v[212:215],a[204:207]
s_lshl_b32 s95,s42,2
s_waitcnt vmcnt(1)
v_cmp_ne_u32 s[90:91],s68,v26
v_and_b32 v26,-2,v27
v_cmp_ne_u32 s[92:93],s69,v26
s_nop 0
s_or_b64 s[90:91],s[90:91],s[92:93]
s_cbranch_scc1 L_fr10_%=
L_fo10_%=:
v_lshlrev_b32 v19,13,v27
v_and_b32 v19,8192,v19
s_nop 0
v_readlane_b32 s90,v19,2
v_readlane_b32 s91,v19,1
v_readlane_b32 s92,v19,0
s_nop 1
s_or_b32 s76,s76,s90
s_or_b32 s77,s77,s91
s_or_b32 s78,s78,s92
v_mfma_f32_16x16x32_bf16 a[204:207],a[140:143],v[244:247],a[204:207]
s_add_u32 s95,s95,s40
s_lshl_b32 s95,s95,4
v_bfe_u32 v19,s55,13,1
v_mov_b32 v18,s68
v_or_b32 v19,s69,v19
v_mfma_f32_16x16x32_bf16 a[204:207],a[144:147],v[112:115],a[204:207]
s_lshl_b32 s94,s71,2
s_mov_b64 exec,1
buffer_store_dwordx2 v[18:19],v6,s[64:67],s95 offen offset:2048 sc1
s_mov_b64 exec,-1
v_mfma_f32_16x16x32_bf16 a[204:207],a[148:151],v[116:119],a[204:207]
s_add_u32 s94,s94,s40
v_accvgpr_read_b32 v146,a88
v_accvgpr_read_b32 v147,a89
v_mfma_f32_16x16x32_bf16 a[204:207],a[152:155],v[120:123],a[204:207]
v_accvgpr_read_b32 v14,a86
v_accvgpr_read_b32 v15,a87
v_cvt_pk_bf16_f32 v134,v134,v135
v_accvgpr_read_b32 v16,a90
v_accvgpr_read_b32 v17,a91
v_cvt_pk_bf16_f32 v146,v146,v147
v_cvt_pk_bf16_f32 v135,v14,v15
v_cvt_pk_bf16_f32 v147,v16,v17
v_mfma_f32_16x16x32_bf16 a[204:207],a[156:159],v[124:127],a[204:207]
s_lshl_b32 s94,s94,4
ds_read_b64_tr_b16 a[116:117],v45 offset:10272
ds_read_b64_tr_b16 a[118:119],v45 offset:15392
v_mfma_f32_16x16x32_bf16 a[204:207],a[160:163],v[232:235],a[204:207]
buffer_store_dwordx4 v[132:135],v25,s[32:35],s85 offen sc1
v_mfma_f32_16x16x32_bf16 a[204:207],a[164:167],v[236:239],a[204:207]
ds_read_b64_tr_b16 a[120:121],v45 offset:20512
ds_read_b64_tr_b16 a[122:123],v45 offset:25632
v_mfma_f32_16x16x32_bf16 a[204:207],a[168:171],v[248:251],a[204:207]
ds_read_b64_tr_b16 a[124:125],v45 offset:30752
ds_read_b64_tr_b16 a[126:127],v45 offset:35872
v_mfma_f32_16x16x32_bf16 a[204:207],a[172:175],v[252:255],a[204:207]
ds_read_b64_tr_b16 a[128:129],v1 offset:64
ds_read_b64_tr_b16 a[130:131],v1 offset:5184
v_mfma_f32_16x16x32_bf16 a[220:223],a[176:179],v[148:151],a[220:223]
buffer_store_dwordx4 v[144:147],v25,s[32:35],s86 offen sc1
v_mfma_f32_16x16x32_bf16 a[220:223],a[180:183],v[180:183],a[220:223]
ds_read_b64_tr_b16 a[132:133],v1 offset:10304
ds_read_b64_tr_b16 a[134:135],v1 offset:15424
v_mfma_f32_16x16x32_bf16 a[220:223],a[184:187],v[212:215],a[220:223]
ds_read_b64_tr_b16 a[136:137],v1 offset:20544
ds_read_b64_tr_b16 a[138:139],v1 offset:25664
v_mfma_f32_16x16x32_bf16 a[220:223],a[188:191],v[244:247],a[220:223]
ds_read_b64_tr_b16 a[140:141],v1 offset:30784
ds_read_b64_tr_b16 a[142:143],v1 offset:35904
v_mfma_f32_16x16x32_bf16 a[220:223],a[96:99],v[112:115],a[220:223]
buffer_load_dwordx4 a[192:195],v6,s[32:35],s76 offen sc1
v_mfma_f32_16x16x32_bf16 a[220:223],a[100:103],v[116:119],a[220:223]
ds_read_b64_tr_b16 a[144:145],v42 offset:64
ds_read_b64_tr_b16 a[146:147],v42 offset:5184
v_mfma_f32_16x16x32_bf16 a[220:223],a[104:107],v[120:123],a[220:223]
s_waitcnt lgkmcnt(10)
ds_read_b64_tr_b16 a[148:149],v42 offset:10304
ds_read_b64_tr_b16 a[150:151],v42 offset:15424
v_mfma_f32_16x16x32_bf16 a[220:223],a[108:111],v[124:127],a[220:223]
)A"
R"A(ds_read_b64_tr_b16 a[152:153],v42 offset:20544
ds_read_b64_tr_b16 a[154:155],v42 offset:25664
v_mfma_f32_16x16x32_bf16 a[220:223],a[112:115],v[232:235],a[220:223]
buffer_load_dwordx4 a[196:199],v6,s[32:35],s77 offen sc1
v_mfma_f32_16x16x32_bf16 a[220:223],a[116:119],v[236:239],a[220:223]
ds_read_b64_tr_b16 a[156:157],v42 offset:30784
ds_read_b64_tr_b16 a[158:159],v42 offset:35904
v_mfma_f32_16x16x32_bf16 a[220:223],a[120:123],v[248:251],a[220:223]
ds_read_b64_tr_b16 a[160:161],v44 offset:64
ds_read_b64_tr_b16 a[162:163],v44 offset:5184
v_mfma_f32_16x16x32_bf16 a[220:223],a[124:127],v[252:255],a[220:223]
s_waitcnt lgkmcnt(10)
ds_read_b64_tr_b16 a[164:165],v44 offset:10304
ds_read_b64_tr_b16 a[166:167],v44 offset:15424
v_mfma_f32_16x16x32_bf16 a[236:239],a[128:131],v[148:151],a[236:239]
buffer_load_dwordx4 a[200:203],v6,s[32:35],s78 offen sc1
v_mfma_f32_16x16x32_bf16 a[236:239],a[132:135],v[180:183],a[236:239]
ds_read_b64_tr_b16 a[168:169],v44 offset:20544
ds_read_b64_tr_b16 a[170:171],v44 offset:25664
v_mfma_f32_16x16x32_bf16 a[236:239],a[136:139],v[212:215],a[236:239]
ds_read_b64_tr_b16 a[172:173],v44 offset:30784
ds_read_b64_tr_b16 a[174:175],v44 offset:35904
v_mfma_f32_16x16x32_bf16 a[236:239],a[140:143],v[244:247],a[236:239]
s_waitcnt lgkmcnt(10)
ds_read_b64_tr_b16 a[176:177],v2 offset:96
ds_read_b64_tr_b16 a[178:179],v2 offset:5216
v_mfma_f32_16x16x32_bf16 a[236:239],a[144:147],v[112:115],a[236:239]
buffer_load_dwordx4 a[224:227],v6,s[32:35],s76 offen offset:1024 sc1
ds_read_b64_tr_b16 a[180:181],v2 offset:10336
ds_read_b64_tr_b16 a[182:183],v2 offset:15456
v_mfma_f32_16x16x32_bf16 a[236:239],a[148:151],v[116:119],a[236:239]
ds_read_b64_tr_b16 a[184:185],v2 offset:20576
ds_read_b64_tr_b16 a[186:187],v2 offset:25696
v_mfma_f32_16x16x32_bf16 a[236:239],a[152:155],v[120:123],a[236:239]
s_waitcnt lgkmcnt(12)
ds_read_b64_tr_b16 a[188:189],v2 offset:30816
ds_read_b64_tr_b16 a[190:191],v2 offset:35936
v_mfma_f32_16x16x32_bf16 a[236:239],a[156:159],v[124:127],a[236:239]
ds_read_b64_tr_b16 a[96:97],v43 offset:96
ds_read_b64_tr_b16 a[98:99],v43 offset:5216
v_mfma_f32_16x16x32_bf16 a[236:239],a[160:163],v[232:235],a[236:239]
s_waitcnt lgkmcnt(14)
buffer_load_dwordx4 a[228:231],v6,s[32:35],s77 offen offset:1024 sc1
ds_read_b64_tr_b16 a[100:101],v43 offset:10336
ds_read_b64_tr_b16 a[102:103],v43 offset:15456
v_mfma_f32_16x16x32_bf16 a[236:239],a[164:167],v[236:239],a[236:239]
s_waitcnt lgkmcnt(14)
ds_read_b64_tr_b16 a[104:105],v43 offset:20576
ds_read_b64_tr_b16 a[106:107],v43 offset:25696
v_mfma_f32_16x16x32_bf16 a[236:239],a[168:171],v[248:251],a[236:239]
s_waitcnt lgkmcnt(14)
ds_read_b64_tr_b16 a[108:109],v43 offset:30816
ds_read_b64_tr_b16 a[110:111],v43 offset:35936
v_mfma_f32_16x16x32_bf16 a[236:239],a[172:175],v[252:255],a[236:239]
s_waitcnt lgkmcnt(14)
ds_read_b64_tr_b16 a[112:113],v45 offset:96
ds_read_b64_tr_b16 a[114:115],v45 offset:5216
v_mfma_f32_16x16x32_bf16 a[252:255],a[176:179],v[148:151],a[252:255]
s_waitcnt lgkmcnt(14)
buffer_load_dwordx4 a[232:235],v6,s[32:35],s78 offen offset:1024 sc1
ds_read_b64_tr_b16 a[116:117],v45 offset:10336
ds_read_b64_tr_b16 a[118:119],v45 offset:15456
v_mfma_f32_16x16x32_bf16 a[252:255],a[180:183],v[180:183],a[252:255]
s_waitcnt lgkmcnt(14)
ds_read_b64_tr_b16 a[120:121],v45 offset:20576
ds_read_b64_tr_b16 a[122:123],v45 offset:25696
v_mfma_f32_16x16x32_bf16 a[252:255],a[184:187],v[212:215],a[252:255]
s_waitcnt lgkmcnt(14)
ds_read_b64_tr_b16 a[124:125],v45 offset:30816
ds_read_b64_tr_b16 a[126:127],v45 offset:35936
v_mfma_f32_16x16x32_bf16 a[252:255],a[188:191],v[244:247],a[252:255]
s_waitcnt lgkmcnt(14)
ds_read_b64_tr_b16 a[128:129],v1 offset:128
ds_read_b64_tr_b16 a[130:131],v1 offset:5248
v_bfe_u32 v24,v6,4,2
v_min_u32 v24,2,v24
v_mfma_f32_16x16x32_bf16 a[252:255],a[96:99],v[112:115],a[252:255]
s_waitcnt lgkmcnt(14)
ds_read_b64_tr_b16 a[132:133],v1 offset:10368
ds_read_b64_tr_b16 a[134:135],v1 offset:15488
v_add_u32 v24,s70,v24
v_add_u32 v24,1,v24
v_mfma_f32_16x16x32_bf16 a[252:255],a[100:103],v[116:119],a[252:255]
s_waitcnt lgkmcnt(14)
ds_read_b64_tr_b16 a[136:137],v1 offset:20608
ds_read_b64_tr_b16 a[138:139],v1 offset:25728
v_and_b32 v24,3,v24
v_lshlrev_b32 v24,8,v24
v_mfma_f32_16x16x32_bf16 a[252:255],a[104:107],v[120:123],a[252:255]
s_waitcnt lgkmcnt(14)
buffer_load_dwordx2 v[26:27],v24,s[64:67],s94 offen offset:2048 sc1
ds_read_b64_tr_b16 a[140:141],v1 offset:30848
ds_read_b64_tr_b16 a[142:143],v1 offset:35968
v_mfma_f32_16x16x32_bf16 a[252:255],a[108:111],v[124:127],a[252:255]
s_waitcnt lgkmcnt(14)
ds_read_b64_tr_b16 a[144:145],v42 offset:128
ds_read_b64_tr_b16 a[146:147],v42 offset:5248
v_mfma_f32_16x16x32_bf16 a[252:255],a[112:115],v[232:235],a[252:255]
s_waitcnt lgkmcnt(14)
ds_read_b64_tr_b16 a[148:149],v42 offset:10368
ds_read_b64_tr_b16 a[150:151],v42 offset:15488
v_mfma_f32_16x16x32_bf16 a[252:255],a[116:119],v[236:239],a[252:255]
s_lshl_b32 s95,s42,2
ds_read_b64_tr_b16 a[152:153],v42 offset:20608
ds_read_b64_tr_b16 a[154:155],v42 offset:25728
s_waitcnt lgkmcnt(15)
v_mfma_f32_16x16x32_bf16 a[252:255],a[120:123],v[248:251],a[252:255]
s_add_u32 s95,s95,s40
s_lshl_b32 s95,s95,4
ds_read_b64_tr_b16 a[156:157],v42 offset:30848
ds_read_b64_tr_b16 a[158:159],v42 offset:35968
v_bfe_u32 v19,s55,13,1
v_mov_b32 v18,s68
v_or_b32 v19,s69,v19
s_waitcnt lgkmcnt(15)
v_mfma_f32_16x16x32_bf16 a[252:255],a[124:127],v[252:255],a[252:255]
s_waitcnt vmcnt(7)
s_mov_b64 exec,1
buffer_store_dwordx2 v[18:19],v6,s[64:67],s95 offen offset:3072 sc1
s_mov_b64 exec,-1
ds_read_b64_tr_b16 a[160:161],v44 offset:128
ds_read_b64_tr_b16 a[162:163],v44 offset:5248
s_waitcnt lgkmcnt(15)
v_mfma_f32_16x16x32_bf16 a[12:15],a[128:131],v[148:151],a[12:15]
s_waitcnt lgkmcnt(14)
ds_read_b64_tr_b16 a[164:165],v44 offset:10368
ds_read_b64_tr_b16 a[166:167],v44 offset:15488
v_mfma_f32_16x16x32_bf16 a[12:15],a[132:135],v[180:183],a[12:15]
s_waitcnt lgkmcnt(14)
ds_read_b64_tr_b16 a[168:169],v44 offset:20608
ds_read_b64_tr_b16 a[170:171],v44 offset:25728
v_mfma_f32_16x16x32_bf16 a[12:15],a[136:139],v[212:215],a[12:15]
s_waitcnt lgkmcnt(14)
ds_read_b64_tr_b16 a[172:173],v44 offset:30848
ds_read_b64_tr_b16 a[174:175],v44 offset:35968
v_mfma_f32_16x16x32_bf16 a[12:15],a[140:143],v[244:247],a[12:15]
s_waitcnt lgkmcnt(14)
ds_read_b64_tr_b16 a[176:177],v2 offset:160
ds_read_b64_tr_b16 a[178:179],v2 offset:5280
v_mfma_f32_16x16x32_bf16 a[12:15],a[144:147],v[112:115],a[12:15]
s_waitcnt lgkmcnt(14)
ds_read_b64_tr_b16 a[180:181],v2 offset:10400
ds_read_b64_tr_b16 a[182:183],v2 offset:15520
v_mfma_f32_16x16x32_bf16 a[12:15],a[148:151],v[116:119],a[12:15]
s_waitcnt lgkmcnt(14)
ds_read_b64_tr_b16 a[184:185],v2 offset:20640
ds_read_b64_tr_b16 a[186:187],v2 offset:25760
v_mfma_f32_16x16x32_bf16 a[12:15],a[152:155],v[120:123],a[12:15]
s_waitcnt lgkmcnt(14)
ds_read_b64_tr_b16 a[188:189],v2 offset:30880
ds_read_b64_tr_b16 a[190:191],v2 offset:36000
v_mfma_f32_16x16x32_bf16 a[12:15],a[156:159],v[124:127],a[12:15]
s_waitcnt lgkmcnt(14)
ds_read_b64_tr_b16 a[96:97],v43 offset:160
ds_read_b64_tr_b16 a[98:99],v43 offset:5280
v_mfma_f32_16x16x32_bf16 a[12:15],a[160:163],v[232:235],a[12:15]
s_waitcnt lgkmcnt(14)
ds_read_b64_tr_b16 a[100:101],v43 offset:10400
ds_read_b64_tr_b16 a[102:103],v43 offset:15520
v_mfma_f32_16x16x32_bf16 a[12:15],a[164:167],v[236:239],a[12:15]
s_waitcnt lgkmcnt(14)
ds_read_b64_tr_b16 a[104:105],v43 offset:20640
ds_read_b64_tr_b16 a[106:107],v43 offset:25760
v_mfma_f32_16x16x32_bf16 a[12:15],a[168:171],v[248:251],a[12:15]
s_waitcnt lgkmcnt(14)
ds_read_b64_tr_b16 a[108:109],v43 offset:30880
ds_read_b64_tr_b16 a[110:111],v43 offset:36000
v_mfma_f32_16x16x32_bf16 a[12:15],a[172:175],v[252:255],a[12:15]
s_waitcnt lgkmcnt(14)
ds_read_b64_tr_b16 a[112:113],v45 offset:160
ds_read_b64_tr_b16 a[114:115],v45 offset:5280
v_mfma_f32_16x16x32_bf16 a[28:31],a[176:179],v[148:151],a[28:31]
s_waitcnt lgkmcnt(14)
ds_read_b64_tr_b16 a[116:117],v45 offset:10400
ds_read_b64_tr_b16 a[118:119],v45 offset:15520
v_mfma_f32_16x16x32_bf16 a[28:31],a[180:183],v[180:183],a[28:31]
s_waitcnt lgkmcnt(14)
ds_read_b64_tr_b16 a[120:121],v45 offset:20640
ds_read_b64_tr_b16 a[122:123],v45 offset:25760
v_mfma_f32_16x16x32_bf16 a[28:31],a[184:187],v[212:215],a[28:31]
s_waitcnt lgkmcnt(14)
ds_read_b64_tr_b16 a[124:125],v45 offset:30880
ds_read_b64_tr_b16 a[126:127],v45 offset:36000
v_mfma_f32_16x16x32_bf16 a[28:31],a[188:191],v[244:247],a[28:31]
s_waitcnt lgkmcnt(14)
ds_read_b64_tr_b16 a[128:129],v1 offset:192
ds_read_b64_tr_b16 a[130:131],v1 offset:5312
v_mfma_f32_16x16x32_bf16 a[28:31],a[96:99],v[112:115],a[28:31]
s_waitcnt lgkmcnt(14)
ds_read_b64_tr_b16 a[132:133],v1 offset:10432
ds_read_b64_tr_b16 a[134:135],v1 offset:15552
v_mfma_f32_16x16x32_bf16 a[28:31],a[100:103],v[116:119],a[28:31]
s_waitcnt lgkmcnt(14)
ds_read_b64_tr_b16 a[136:137],v1 offset:20672
ds_read_b64_tr_b16 a[138:139],v1 offset:25792
v_mfma_f32_16x16x32_bf16 a[28:31],a[104:107],v[120:123],a[28:31]
s_waitcnt lgkmcnt(14)
ds_read_b64_tr_b16 a[140:141],v1 offset:30912
ds_read_b64_tr_b16 a[142:143],v1 offset:36032
v_mfma_f32_16x16x32_bf16 a[28:31],a[108:111],v[124:127],a[28:31]
s_waitcnt lgkmcnt(14)
ds_read_b64_tr_b16 a[144:145],v42 offset:192
ds_read_b64_tr_b16 a[146:147],v42 offset:5312
v_mfma_f32_16x16x32_bf16 a[28:31],a[112:115],v[232:235],a[28:31]
s_waitcnt lgkmcnt(14)
ds_read_b64_tr_b16 a[148:149],v42 offset:10432
ds_read_b64_tr_b16 a[150:151],v42 offset:15552
v_mfma_f32_16x16x32_bf16 a[28:31],a[116:119],v[236:239],a[28:31]
s_waitcnt lgkmcnt(14)
ds_read_b64_tr_b16 a[152:153],v42 offset:20672
ds_read_b64_tr_b16 a[154:155],v42 offset:25792
v_mfma_f32_16x16x32_bf16 a[28:31],a[120:123],v[248:251],a[28:31]
s_waitcnt lgkmcnt(14)
ds_read_b64_tr_b16 a[156:157],v42 offset:30912
ds_read_b64_tr_b16 a[158:159],v42 offset:36032
v_mfma_f32_16x16x32_bf16 a[28:31],a[124:127],v[252:255],a[28:31]
s_waitcnt lgkmcnt(14)
ds_read_b64_tr_b16 a[160:161],v44 offset:192
ds_read_b64_tr_b16 a[162:163],v44 offset:5312
v_mfma_f32_16x16x32_bf16 a[44:47],a[128:131],v[148:151],a[44:47]
s_waitcnt lgkmcnt(14)
ds_read_b64_tr_b16 a[164:165],v44 offset:10432
ds_read_b64_tr_b16 a[166:167],v44 offset:15552
v_mfma_f32_16x16x32_bf16 a[44:47],a[132:135],v[180:183],a[44:47]
s_waitcnt lgkmcnt(14)
ds_read_b64_tr_b16 a[168:169],v44 offset:20672
ds_read_b64_tr_b16 a[170:171],v44 offset:25792
v_mfma_f32_16x16x32_bf16 a[44:47],a[136:139],v[212:215],a[44:47]
ds_read_b64_tr_b16 a[172:173],v44 offset:30912
ds_read_b64_tr_b16 a[174:175],v44 offset:36032
s_waitcnt vmcnt(1)
v_cmp_ne_u32 s[90:91],s68,v26
v_and_b32 v26,-2,v27
v_cmp_ne_u32 s[92:93],s69,v26
s_nop 0
s_or_b64 s[90:91],s[90:91],s[92:93]
s_cbranch_scc1 L_fr11_%=
L_fo11_%=:
s_waitcnt lgkmcnt(15)
v_mfma_f32_16x16x32_bf16 a[44:47],a[140:143],v[244:247],a[44:47]
s_waitcnt lgkmcnt(14)
buffer_load_dwordx4 v[72:75],v6,s[32:35],s76 offen offset:2048 sc1
ds_read_b64_tr_b16 a[176:177],v2 offset:224
ds_read_b64_tr_b16 a[178:179],v2 offset:5344
v_mfma_f32_16x16x32_bf16 a[44:47],a[144:147],v[112:115],a[44:47]
s_lshl_b32 s94,s71,2
ds_read_b64_tr_b16 a[180:181],v2 offset:10464
ds_read_b64_tr_b16 a[182:183],v2 offset:15584
s_waitcnt lgkmcnt(15)
v_mfma_f32_16x16x32_bf16 a[44:47],a[148:151],v[116:119],a[44:47]
s_add_u32 s94,s94,s40
ds_read_b64_tr_b16 a[184:185],v2 offset:20704
ds_read_b64_tr_b16 a[186:187],v2 offset:25824
s_waitcnt lgkmcnt(15)
v_mfma_f32_16x16x32_bf16 a[44:47],a[152:155],v[120:123],a[44:47]
s_lshl_b32 s94,s94,4
ds_read_b64_tr_b16 a[188:189],v2 offset:30944
ds_read_b64_tr_b16 a[190:191],v2 offset:36064
s_waitcnt lgkmcnt(15)
v_mfma_f32_16x16x32_bf16 a[44:47],a[156:159],v[124:127],a[44:47]
s_waitcnt lgkmcnt(14)
buffer_load_dwordx4 v[88:91],v6,s[32:35],s77 offen offset:2048 sc1
ds_read_b64_tr_b16 a[96:97],v43 offset:224
ds_read_b64_tr_b16 a[98:99],v43 offset:5344
v_mfma_f32_16x16x32_bf16 a[44:47],a[160:163],v[232:235],a[44:47]
s_waitcnt lgkmcnt(14)
ds_read_b64_tr_b16 a[100:101],v43 offset:10464
ds_read_b64_tr_b16 a[102:103],v43 offset:15584
v_mfma_f32_16x16x32_bf16 a[44:47],a[164:167],v[236:239],a[44:47]
s_waitcnt lgkmcnt(14)
ds_read_b64_tr_b16 a[104:105],v43 offset:20704
)A"
R"A(ds_read_b64_tr_b16 a[106:107],v43 offset:25824
v_mfma_f32_16x16x32_bf16 a[44:47],a[168:171],v[248:251],a[44:47]
s_waitcnt lgkmcnt(14)
ds_read_b64_tr_b16 a[108:109],v43 offset:30944
ds_read_b64_tr_b16 a[110:111],v43 offset:36064
v_mfma_f32_16x16x32_bf16 a[44:47],a[172:175],v[252:255],a[44:47]
s_waitcnt lgkmcnt(14)
buffer_load_dwordx4 v[104:107],v6,s[32:35],s78 offen offset:2048 sc1
ds_read_b64_tr_b16 a[112:113],v45 offset:224
ds_read_b64_tr_b16 a[114:115],v45 offset:5344
v_mfma_f32_16x16x32_bf16 a[60:63],a[176:179],v[148:151],a[60:63]
s_waitcnt lgkmcnt(14)
ds_read_b64_tr_b16 a[116:117],v45 offset:10464
ds_read_b64_tr_b16 a[118:119],v45 offset:15584
v_mfma_f32_16x16x32_bf16 a[60:63],a[180:183],v[180:183],a[60:63]
s_waitcnt lgkmcnt(14)
ds_read_b64_tr_b16 a[120:121],v45 offset:20704
ds_read_b64_tr_b16 a[122:123],v45 offset:25824
v_mfma_f32_16x16x32_bf16 a[60:63],a[184:187],v[212:215],a[60:63]
s_waitcnt lgkmcnt(14)
ds_read_b64_tr_b16 a[124:125],v45 offset:30944
ds_read_b64_tr_b16 a[126:127],v45 offset:36064
v_mfma_f32_16x16x32_bf16 a[60:63],a[188:191],v[244:247],a[60:63]
s_waitcnt lgkmcnt(14)
buffer_load_dwordx4 v[76:79],v6,s[32:35],s76 offen offset:3072 sc1
v_mfma_f32_16x16x32_bf16 a[60:63],a[96:99],v[112:115],a[60:63]
s_waitcnt lgkmcnt(12)
ds_read_b64_tr_b16 a[128:129],v1 offset:256
ds_read_b64_tr_b16 a[130:131],v1 offset:5376
v_mfma_f32_16x16x32_bf16 a[60:63],a[100:103],v[116:119],a[60:63]
s_waitcnt lgkmcnt(12)
ds_read_b64_tr_b16 a[132:133],v1 offset:10496
ds_read_b64_tr_b16 a[134:135],v1 offset:15616
v_mfma_f32_16x16x32_bf16 a[60:63],a[104:107],v[120:123],a[60:63]
s_waitcnt lgkmcnt(12)
ds_read_b64_tr_b16 a[136:137],v1 offset:20736
ds_read_b64_tr_b16 a[138:139],v1 offset:25856
v_mfma_f32_16x16x32_bf16 a[60:63],a[108:111],v[124:127],a[60:63]
s_waitcnt lgkmcnt(12)
buffer_load_dwordx4 v[92:95],v6,s[32:35],s77 offen offset:3072 sc1
v_mfma_f32_16x16x32_bf16 a[60:63],a[112:115],v[232:235],a[60:63]
s_waitcnt lgkmcnt(10)
ds_read_b64_tr_b16 a[140:141],v1 offset:30976
ds_read_b64_tr_b16 a[142:143],v1 offset:36096
v_mfma_f32_16x16x32_bf16 a[60:63],a[116:119],v[236:239],a[60:63]
s_waitcnt lgkmcnt(10)
ds_read_b64_tr_b16 a[144:145],v42 offset:256
ds_read_b64_tr_b16 a[146:147],v42 offset:5376
v_mfma_f32_16x16x32_bf16 a[60:63],a[120:123],v[248:251],a[60:63]
s_waitcnt lgkmcnt(10)
ds_read_b64_tr_b16 a[148:149],v42 offset:10496
ds_read_b64_tr_b16 a[150:151],v42 offset:15616
v_mfma_f32_16x16x32_bf16 a[60:63],a[124:127],v[252:255],a[60:63]
buffer_load_dwordx4 v[108:111],v6,s[32:35],s78 offen offset:3072 sc1
v_mfma_f32_16x16x32_bf16 a[204:207],v[34:37],a[192:195],a[204:207]
ds_read_b64_tr_b16 a[152:153],v42 offset:20736
ds_read_b64_tr_b16 a[154:155],v42 offset:25856
v_mfma_f32_16x16x32_bf16 a[204:207],v[34:37],a[196:199],a[204:207]
ds_read_b64_tr_b16 a[156:157],v42 offset:30976
ds_read_b64_tr_b16 a[158:159],v42 offset:36096
v_mfma_f32_16x16x32_bf16 a[204:207],v[34:37],a[200:203],a[204:207]
ds_read_b64_tr_b16 a[160:161],v44 offset:256
ds_read_b64_tr_b16 a[162:163],v44 offset:5376
v_mfma_f32_16x16x32_bf16 a[220:223],v[32:35],a[192:195],a[220:223]
ds_read_b64_tr_b16 a[164:165],v44 offset:10496
ds_read_b64_tr_b16 a[166:167],v44 offset:15616
v_mfma_f32_16x16x32_bf16 a[220:223],v[32:35],a[196:199],a[220:223]
ds_read_b64_tr_b16 a[168:169],v44 offset:20736
ds_read_b64_tr_b16 a[170:171],v44 offset:25856
v_mfma_f32_16x16x32_bf16 a[220:223],v[32:35],a[200:203],a[220:223]
s_waitcnt lgkmcnt(12)
ds_read_b64_tr_b16 a[172:173],v44 offset:30976
ds_read_b64_tr_b16 a[174:175],v44 offset:36096
v_mfma_f32_16x16x32_bf16 a[76:79],a[128:131],v[148:151],a[76:79]
v_bfe_u32 v24,v6,4,2
v_min_u32 v24,2,v24
v_mfma_f32_16x16x32_bf16 a[76:79],a[132:135],v[180:183],a[76:79]
v_add_u32 v24,s70,v24
v_add_u32 v24,1,v24
v_mfma_f32_16x16x32_bf16 a[76:79],a[136:139],v[212:215],a[76:79]
v_accvgpr_read_b32 v160,a204
v_accvgpr_read_b32 v161,a205
v_mfma_f32_16x16x32_bf16 a[76:79],a[140:143],v[244:247],a[76:79]
v_cvt_pk_bf16_f32 v160,v160,v161
v_accvgpr_read_b32 v162,a206
v_mfma_f32_16x16x32_bf16 a[76:79],a[144:147],v[112:115],a[76:79]
s_waitcnt lgkmcnt(6)
v_accvgpr_read_b32 v163,a207
v_cvt_pk_bf16_f32 v161,v162,v163
v_mfma_f32_16x16x32_bf16 a[76:79],a[148:151],v[116:119],a[76:79]
v_accvgpr_read_b32 v162,a220
v_accvgpr_read_b32 v163,a221
v_mfma_f32_16x16x32_bf16 a[76:79],a[152:155],v[120:123],a[76:79]
v_accvgpr_read_b32 v12,a222
v_accvgpr_read_b32 v13,a223
v_mfma_f32_16x16x32_bf16 a[76:79],a[156:159],v[124:127],a[76:79]
v_cvt_pk_bf16_f32 v162,v162,v163
v_cvt_pk_bf16_f32 v163,v12,v13
v_mfma_f32_16x16x32_bf16 a[76:79],a[160:163],v[232:235],a[76:79]
s_waitcnt lgkmcnt(0)
ds_read_b64_tr_b16 a[176:177],v2 offset:288
ds_read_b64_tr_b16 a[178:179],v2 offset:5408
v_mfma_f32_16x16x32_bf16 a[76:79],a[164:167],v[236:239],a[76:79]
v_permlane16_swap_b32 v160,v162
v_mfma_f32_16x16x32_bf16 a[76:79],a[168:171],v[248:251],a[76:79]
ds_read_b64_tr_b16 a[180:181],v2 offset:10528
ds_read_b64_tr_b16 a[182:183],v2 offset:15648
v_and_b32 v24,3,v24
v_lshlrev_b32 v24,8,v24
v_mfma_f32_16x16x32_bf16 a[76:79],a[172:175],v[252:255],a[76:79]
buffer_load_dwordx2 v[26:27],v24,s[64:67],s94 offen offset:3072 sc1
ds_read_b64_tr_b16 a[184:185],v2 offset:20768
ds_read_b64_tr_b16 a[186:187],v2 offset:25888
v_mfma_f32_16x16x32_bf16 a[236:239],v[34:37],a[224:227],a[236:239]
ds_read_b64_tr_b16 a[188:189],v2 offset:31008
ds_read_b64_tr_b16 a[190:191],v2 offset:36128
v_mfma_f32_16x16x32_bf16 a[236:239],v[34:37],a[228:231],a[236:239]
ds_read_b64_tr_b16 a[96:97],v43 offset:288
ds_read_b64_tr_b16 a[98:99],v43 offset:5408
v_mfma_f32_16x16x32_bf16 a[236:239],v[34:37],a[232:235],a[236:239]
ds_read_b64_tr_b16 a[100:101],v43 offset:10528
ds_read_b64_tr_b16 a[102:103],v43 offset:15648
v_mfma_f32_16x16x32_bf16 a[252:255],v[32:35],a[224:227],a[252:255]
ds_read_b64_tr_b16 a[104:105],v43 offset:20768
ds_read_b64_tr_b16 a[106:107],v43 offset:25888
v_mfma_f32_16x16x32_bf16 a[252:255],v[32:35],a[228:231],a[252:255]
ds_read_b64_tr_b16 a[108:109],v43 offset:31008
ds_read_b64_tr_b16 a[110:111],v43 offset:36128
v_mfma_f32_16x16x32_bf16 a[252:255],v[32:35],a[232:235],a[252:255]
s_waitcnt lgkmcnt(14)
ds_read_b64_tr_b16 a[112:113],v45 offset:288
ds_read_b64_tr_b16 a[114:115],v45 offset:5408
v_mfma_f32_16x16x32_bf16 a[92:95],a[176:179],v[148:151],a[92:95]
s_waitcnt lgkmcnt(14)
ds_read_b64_tr_b16 a[116:117],v45 offset:10528
ds_read_b64_tr_b16 a[118:119],v45 offset:15648
v_mfma_f32_16x16x32_bf16 a[92:95],a[180:183],v[180:183],a[92:95]
s_waitcnt lgkmcnt(14)
ds_read_b64_tr_b16 a[120:121],v45 offset:20768
ds_read_b64_tr_b16 a[122:123],v45 offset:25888
v_mfma_f32_16x16x32_bf16 a[92:95],a[184:187],v[212:215],a[92:95]
s_waitcnt lgkmcnt(14)
ds_read_b64_tr_b16 a[124:125],v45 offset:31008
ds_read_b64_tr_b16 a[126:127],v45 offset:36128
v_mfma_f32_16x16x32_bf16 a[92:95],a[188:191],v[244:247],a[92:95]
s_waitcnt lgkmcnt(14)
v_permlane16_swap_b32 v161,v163
v_mfma_f32_16x16x32_bf16 a[92:95],a[96:99],v[112:115],a[92:95]
s_cmp_lg_u32 %2,0
s_cbranch_scc1 L_ea0_%=
buffer_store_dwordx4 v[160:163],v40,s[20:23],s43 offen
s_branch L_eb0_%=
L_ea0_%=:
buffer_store_dwordx4 v[160:163],v40,s[20:23],s43 offen sc1
L_eb0_%=:
s_waitcnt lgkmcnt(12)
v_mfma_f32_16x16x32_bf16 a[92:95],a[100:103],v[116:119],a[92:95]
s_waitcnt lgkmcnt(10)
v_accvgpr_read_b32 v164,a236
v_accvgpr_read_b32 v165,a237
v_mfma_f32_16x16x32_bf16 a[92:95],a[104:107],v[120:123],a[92:95]
s_waitcnt lgkmcnt(8)
v_cvt_pk_bf16_f32 v164,v164,v165
v_accvgpr_read_b32 v166,a238
v_mfma_f32_16x16x32_bf16 a[92:95],a[108:111],v[124:127],a[92:95]
s_waitcnt lgkmcnt(6)
v_accvgpr_read_b32 v167,a239
v_cvt_pk_bf16_f32 v165,v166,v167
v_mfma_f32_16x16x32_bf16 a[92:95],a[112:115],v[232:235],a[92:95]
s_waitcnt lgkmcnt(4)
v_accvgpr_read_b32 v166,a252
v_accvgpr_read_b32 v167,a253
v_mfma_f32_16x16x32_bf16 a[92:95],a[116:119],v[236:239],a[92:95]
s_waitcnt lgkmcnt(2)
v_accvgpr_read_b32 v14,a254
v_accvgpr_read_b32 v15,a255
v_cvt_pk_bf16_f32 v166,v166,v167
v_cvt_pk_bf16_f32 v167,v14,v15
v_mfma_f32_16x16x32_bf16 a[92:95],a[120:123],v[248:251],a[92:95]
s_waitcnt lgkmcnt(0)
v_mfma_f32_16x16x32_bf16 a[92:95],a[124:127],v[252:255],a[92:95]
v_permlane16_swap_b32 v164,v166
v_permlane16_swap_b32 v165,v167
s_nop 3
s_cmp_lg_u32 %2,0
s_cbranch_scc1 L_ea1_%=
buffer_store_dwordx4 v[164:167],v40,s[20:23],s43 offen offset:64
s_branch L_eb1_%=
L_ea1_%=:
buffer_store_dwordx4 v[164:167],v40,s[20:23],s43 offen offset:64 sc1
L_eb1_%=:
s_waitcnt vmcnt(2)
v_cmp_ne_u32 s[90:91],s68,v26
v_and_b32 v26,-2,v27
v_cmp_ne_u32 s[92:93],s69,v26
s_nop 0
s_or_b64 s[90:91],s[90:91],s[92:93]
s_cbranch_scc1 L_fr12_%=
L_fo12_%=:
buffer_load_dwordx4 v[136:139],v25,s[32:35],s76 offen sc1
buffer_load_dwordx4 v[168:171],v25,s[32:35],s77 offen sc1
buffer_load_dwordx4 v[200:203],v25,s[32:35],s78 offen sc1
s_cmp_lg_u32 %2,0
s_cbranch_scc1 L_dual_%=
v_mfma_f32_16x16x32_bf16 a[12:15],v[34:37],v[72:75],a[12:15]
v_mfma_f32_16x16x32_bf16 a[12:15],v[34:37],v[88:91],a[12:15]
v_mfma_f32_16x16x32_bf16 a[12:15],v[34:37],v[104:107],a[12:15]
v_mfma_f32_16x16x32_bf16 a[28:31],v[32:35],v[72:75],a[28:31]
v_mfma_f32_16x16x32_bf16 a[28:31],v[32:35],v[88:91],a[28:31]
v_mfma_f32_16x16x32_bf16 a[28:31],v[32:35],v[104:107],a[28:31]
v_mfma_f32_16x16x32_bf16 a[44:47],v[34:37],v[76:79],a[44:47]
v_mfma_f32_16x16x32_bf16 a[44:47],v[34:37],v[92:95],a[44:47]
v_mfma_f32_16x16x32_bf16 a[44:47],v[34:37],v[108:111],a[44:47]
s_nop 1
v_accvgpr_read_b32 v176,a12
v_accvgpr_read_b32 v177,a13
v_cvt_pk_bf16_f32 v176,v176,v177
v_accvgpr_read_b32 v178,a14
v_accvgpr_read_b32 v179,a15
v_cvt_pk_bf16_f32 v177,v178,v179
v_mfma_f32_16x16x32_bf16 a[60:63],v[32:35],v[76:79],a[60:63]
v_mfma_f32_16x16x32_bf16 a[60:63],v[32:35],v[92:95],a[60:63]
v_mfma_f32_16x16x32_bf16 a[60:63],v[32:35],v[108:111],a[60:63]
v_accvgpr_read_b32 v178,a28
v_accvgpr_read_b32 v179,a29
v_accvgpr_read_b32 v16,a30
v_accvgpr_read_b32 v17,a31
v_cvt_pk_bf16_f32 v178,v178,v179
v_cvt_pk_bf16_f32 v179,v16,v17
s_nop 3
v_permlane16_swap_b32 v176,v178
v_permlane16_swap_b32 v177,v179
buffer_store_dwordx4 v[176:179],v40,s[20:23],s43 offen offset:128
v_accvgpr_read_b32 v192,a44
v_accvgpr_read_b32 v193,a45
v_cvt_pk_bf16_f32 v192,v192,v193
v_accvgpr_read_b32 v194,a46
v_accvgpr_read_b32 v195,a47
v_cvt_pk_bf16_f32 v193,v194,v195
v_accvgpr_read_b32 v194,a60
v_accvgpr_read_b32 v195,a61
v_accvgpr_read_b32 v12,a62
v_accvgpr_read_b32 v13,a63
v_cvt_pk_bf16_f32 v194,v194,v195
s_nop 1
v_permlane16_swap_b32 v192,v194
v_cvt_pk_bf16_f32 v195,v12,v13
s_nop 1
v_permlane16_swap_b32 v193,v195
buffer_store_dwordx4 v[192:195],v40,s[20:23],s43 offen offset:192
s_waitcnt vmcnt(4)
v_mfma_f32_16x16x32_bf16 a[76:79],v[34:37],v[136:139],a[76:79]
s_waitcnt vmcnt(3)
v_mfma_f32_16x16x32_bf16 a[76:79],v[34:37],v[168:171],a[76:79]
s_waitcnt vmcnt(2)
v_mfma_f32_16x16x32_bf16 a[76:79],v[34:37],v[200:203],a[76:79]
v_mfma_f32_16x16x32_bf16 a[92:95],v[32:35],v[136:139],a[92:95]
v_mfma_f32_16x16x32_bf16 a[92:95],v[32:35],v[168:171],a[92:95]
v_mfma_f32_16x16x32_bf16 a[92:95],v[32:35],v[200:203],a[92:95]
s_nop 3
s_nop 0
v_accvgpr_read_b32 v196,a76
v_accvgpr_read_b32 v197,a77
v_cvt_pk_bf16_f32 v196,v196,v197
v_accvgpr_read_b32 v198,a78
v_accvgpr_read_b32 v199,a79
v_cvt_pk_bf16_f32 v197,v198,v199
v_accvgpr_read_b32 v198,a92
v_accvgpr_read_b32 v199,a93
v_accvgpr_read_b32 v14,a94
v_accvgpr_read_b32 v15,a95
v_cvt_pk_bf16_f32 v198,v198,v199
s_nop 1
v_permlane16_swap_b32 v196,v198
v_cvt_pk_bf16_f32 v199,v14,v15
s_nop 1
v_permlane16_swap_b32 v197,v199
buffer_store_dwordx4 v[196:199],v40,s[20:23],s43 offen offset:256
s_or_b32 s62,s42,s40
s_cmp_lg_u32 s62,0
s_cbranch_scc1 L_noep_%=
v_mov_b32 v12,s68
s_mov_b64 exec,1
buffer_store_dword v12,v25,s[64:67],0 offen sc1
s_mov_b64 exec,-1
L_noep_%=:
s_endpgm
L_dual_%=:
v_mfma_f32_16x16x32_bf16 a[12:15],v[34:37],v[72:75],a[12:15]
v_mfma_f32_16x16x32_bf16 a[12:15],v[34:37],v[88:91],a[12:15]
v_mfma_f32_16x16x32_bf16 a[12:15],v[34:37],v[104:107],a[12:15]
v_mfma_f32_16x16x32_bf16 a[28:31],v[32:35],v[72:75],a[28:31]
v_mfma_f32_16x16x32_bf16 a[28:31],v[32:35],v[88:91],a[28:31]
v_mfma_f32_16x16x32_bf16 a[28:31],v[32:35],v[104:107],a[28:31]
v_mfma_f32_16x16x32_bf16 a[44:47],v[34:37],v[76:79],a[44:47]
)A"
R"A(v_mfma_f32_16x16x32_bf16 a[44:47],v[34:37],v[92:95],a[44:47]
v_mfma_f32_16x16x32_bf16 a[44:47],v[34:37],v[108:111],a[44:47]
s_nop 1
v_accvgpr_read_b32 v176,a12
v_accvgpr_read_b32 v177,a13
v_cvt_pk_bf16_f32 v176,v176,v177
v_accvgpr_read_b32 v178,a14
v_accvgpr_read_b32 v179,a15
v_cvt_pk_bf16_f32 v177,v178,v179
v_mfma_f32_16x16x32_bf16 a[60:63],v[32:35],v[76:79],a[60:63]
v_mfma_f32_16x16x32_bf16 a[60:63],v[32:35],v[92:95],a[60:63]
v_mfma_f32_16x16x32_bf16 a[60:63],v[32:35],v[108:111],a[60:63]
v_accvgpr_read_b32 v178,a28
v_accvgpr_read_b32 v179,a29
v_accvgpr_read_b32 v16,a30
v_accvgpr_read_b32 v17,a31
v_cvt_pk_bf16_f32 v178,v178,v179
v_cvt_pk_bf16_f32 v179,v16,v17
s_nop 3
v_permlane16_swap_b32 v176,v178
v_permlane16_swap_b32 v177,v179
buffer_store_dwordx4 v[176:179],v40,s[20:23],s43 offen offset:128 sc1
v_accvgpr_read_b32 v192,a44
v_accvgpr_read_b32 v193,a45
v_cvt_pk_bf16_f32 v192,v192,v193
v_accvgpr_read_b32 v194,a46
v_accvgpr_read_b32 v195,a47
v_cvt_pk_bf16_f32 v193,v194,v195
v_accvgpr_read_b32 v194,a60
v_accvgpr_read_b32 v195,a61
v_accvgpr_read_b32 v12,a62
v_accvgpr_read_b32 v13,a63
v_cvt_pk_bf16_f32 v194,v194,v195
s_nop 1
v_permlane16_swap_b32 v192,v194
v_cvt_pk_bf16_f32 v195,v12,v13
s_nop 1
v_permlane16_swap_b32 v193,v195
buffer_store_dwordx4 v[192:195],v40,s[20:23],s43 offen offset:192 sc1
s_waitcnt vmcnt(4)
v_mfma_f32_16x16x32_bf16 a[76:79],v[34:37],v[136:139],a[76:79]
s_waitcnt vmcnt(3)
v_mfma_f32_16x16x32_bf16 a[76:79],v[34:37],v[168:171],a[76:79]
s_waitcnt vmcnt(2)
v_mfma_f32_16x16x32_bf16 a[76:79],v[34:37],v[200:203],a[76:79]
v_mfma_f32_16x16x32_bf16 a[92:95],v[32:35],v[136:139],a[92:95]
v_mfma_f32_16x16x32_bf16 a[92:95],v[32:35],v[168:171],a[92:95]
v_mfma_f32_16x16x32_bf16 a[92:95],v[32:35],v[200:203],a[92:95]
s_nop 3
s_nop 0
v_accvgpr_read_b32 v196,a76
v_accvgpr_read_b32 v197,a77
v_cvt_pk_bf16_f32 v196,v196,v197
v_accvgpr_read_b32 v198,a78
v_accvgpr_read_b32 v199,a79
v_cvt_pk_bf16_f32 v197,v198,v199
v_accvgpr_read_b32 v198,a92
v_accvgpr_read_b32 v199,a93
v_accvgpr_read_b32 v14,a94
v_accvgpr_read_b32 v15,a95
v_cvt_pk_bf16_f32 v198,v198,v199
s_nop 1
v_permlane16_swap_b32 v196,v198
v_cvt_pk_bf16_f32 v199,v14,v15
s_nop 1
v_permlane16_swap_b32 v197,v199
buffer_store_dwordx4 v[196:199],v40,s[20:23],s43 offen offset:256 sc1
s_or_b32 s62,s42,s40
s_cmp_lg_u32 s62,0
s_cbranch_scc1 L_noep2_%=
v_mov_b32 v12,s68
s_mov_b64 exec,1
buffer_store_dword v12,v25,s[64:67],0 offen sc1
s_mov_b64 exec,-1
L_noep2_%=:
s_endpgm
L_ps2_%=:
s_min_i32 s79,s79,100
s_add_u32 s78,s78,s79
L_pl2_%=:
s_sleep 1
s_sub_u32 s79,s79,4
s_cmp_gt_i32 s79,1
s_cbranch_scc1 L_pl2_%=
s_branch L_pr2_%=
L_ps3_%=:
s_min_i32 s79,s79,100
s_add_u32 s78,s78,s79
L_pl3_%=:
s_sleep 1
s_sub_u32 s79,s79,4
s_cmp_gt_i32 s79,1
s_cbranch_scc1 L_pl3_%=
s_branch L_pr3_%=
L_ps4_%=:
s_min_i32 s79,s79,100
s_add_u32 s78,s78,s79
L_pl4_%=:
s_sleep 1
s_sub_u32 s79,s79,4
s_cmp_gt_i32 s79,1
s_cbranch_scc1 L_pl4_%=
s_branch L_pr4_%=
L_ps5_%=:
s_min_i32 s79,s79,100
s_add_u32 s78,s78,s79
L_pl5_%=:
s_sleep 1
s_sub_u32 s79,s79,4
s_cmp_gt_i32 s79,1
s_cbranch_scc1 L_pl5_%=
s_branch L_pr5_%=
L_ps6_%=:
s_min_i32 s79,s79,100
s_add_u32 s78,s78,s79
L_pl6_%=:
s_sleep 1
s_sub_u32 s79,s79,4
s_cmp_gt_i32 s79,1
s_cbranch_scc1 L_pl6_%=
s_branch L_pr6_%=
L_ps7_%=:
s_min_i32 s79,s79,100
s_add_u32 s78,s78,s79
L_pl7_%=:
s_sleep 1
s_sub_u32 s79,s79,4
s_cmp_gt_i32 s79,1
s_cbranch_scc1 L_pl7_%=
s_branch L_pr7_%=
L_fr8_%=:
s_mov_b32 s88,262144
L_fl8_%=:
buffer_load_dwordx2 v[26:27],v24,s[64:67],s94 offen sc1
s_waitcnt vmcnt(0)
s_sub_u32 s88,s88,1
s_cmp_eq_u32 s88,0
s_cbranch_scc1 L_fo8_%=
v_cmp_ne_u32 s[90:91],s68,v26
v_and_b32 v26,-2,v27
v_cmp_ne_u32 s[92:93],s69,v26
s_nop 0
s_or_b64 s[90:91],s[90:91],s[92:93]
s_cbranch_scc1 L_fl8_%=
s_branch L_fo8_%=
L_fr9_%=:
s_mov_b32 s88,262144
L_fl9_%=:
buffer_load_dwordx2 v[26:27],v24,s[64:67],s94 offen offset:1024 sc1
s_waitcnt vmcnt(0)
s_sub_u32 s88,s88,1
s_cmp_eq_u32 s88,0
s_cbranch_scc1 L_fo9_%=
v_cmp_ne_u32 s[90:91],s68,v26
v_and_b32 v26,-2,v27
v_cmp_ne_u32 s[92:93],s69,v26
s_nop 0
s_or_b64 s[90:91],s[90:91],s[92:93]
s_cbranch_scc1 L_fl9_%=
s_branch L_fo9_%=
L_fr10_%=:
s_mov_b32 s88,262144
L_fl10_%=:
buffer_load_dwordx2 v[26:27],v24,s[64:67],s94 offen sc1
s_waitcnt vmcnt(0)
s_sub_u32 s88,s88,1
s_cmp_eq_u32 s88,0
s_cbranch_scc1 L_fo10_%=
v_cmp_ne_u32 s[90:91],s68,v26
v_and_b32 v26,-2,v27
v_cmp_ne_u32 s[92:93],s69,v26
s_nop 0
s_or_b64 s[90:91],s[90:91],s[92:93]
s_cbranch_scc1 L_fl10_%=
s_branch L_fo10_%=
L_fr11_%=:
s_mov_b32 s88,262144
L_fl11_%=:
buffer_load_dwordx2 v[26:27],v24,s[64:67],s94 offen offset:2048 sc1
s_waitcnt vmcnt(0)
s_sub_u32 s88,s88,1
s_cmp_eq_u32 s88,0
s_cbranch_scc1 L_fo11_%=
v_cmp_ne_u32 s[90:91],s68,v26
v_and_b32 v26,-2,v27
v_cmp_ne_u32 s[92:93],s69,v26
s_nop 0
s_or_b64 s[90:91],s[90:91],s[92:93]
s_cbranch_scc1 L_fl11_%=
s_branch L_fo11_%=
L_fr12_%=:
s_mov_b32 s88,262144
L_fl12_%=:
buffer_load_dwordx2 v[26:27],v24,s[64:67],s94 offen offset:3072 sc1
s_waitcnt vmcnt(0)
s_sub_u32 s88,s88,1
s_cmp_eq_u32 s88,0
s_cbranch_scc1 L_fo12_%=
v_cmp_ne_u32 s[90:91],s68,v26
v_and_b32 v26,-2,v27
v_cmp_ne_u32 s[92:93],s69,v26
s_nop 0
s_or_b64 s[90:91],s[90:91],s[92:93]
s_cbranch_scc1 L_fl12_%=
s_branch L_fo12_%=
L_out_end_%=:
L_fast_end_%=:
L_slow0_%=:
s_mov_b32 s98,0
s_branch L_slowc_%=
L_slow1_%=:
s_mov_b32 s98,1
L_slowc_%=:
s_nop 7
s_nop 7
s_waitcnt vmcnt(0) lgkmcnt(0)
s_lshl_b32 s96,%1,2
s_add_u32 s96,s96,s40
s_lshl_b32 s96,s96,15
s_add_u32 s96,s96,167772160
s_mov_b64 s[56:57],s[12:13]
s_add_u32 s56,s56,s96
s_addc_u32 s57,s57,0
s_and_b32 s57,s57,0xffff
s_mov_b32 s58,32768
s_mov_b32 s59,0x20000
s_mov_b32 s96,0
buffer_store_dwordx4 v[64:67],v6,s[56:59],s96 offen
buffer_store_dwordx4 v[68:71],v6,s[56:59],s96 offen offset:1024
buffer_store_dwordx4 v[72:75],v6,s[56:59],s96 offen offset:2048
buffer_store_dwordx4 v[76:79],v6,s[56:59],s96 offen offset:3072
s_mov_b32 s96,4096
buffer_store_dwordx4 v[80:83],v6,s[56:59],s96 offen
buffer_store_dwordx4 v[84:87],v6,s[56:59],s96 offen offset:1024
buffer_store_dwordx4 v[88:91],v6,s[56:59],s96 offen offset:2048
buffer_store_dwordx4 v[92:95],v6,s[56:59],s96 offen offset:3072
s_mov_b32 s96,8192
buffer_store_dwordx4 v[96:99],v6,s[56:59],s96 offen
buffer_store_dwordx4 v[100:103],v6,s[56:59],s96 offen offset:1024
buffer_store_dwordx4 v[104:107],v6,s[56:59],s96 offen offset:2048
buffer_store_dwordx4 v[108:111],v6,s[56:59],s96 offen offset:3072
s_mov_b32 s96,12288
buffer_store_dwordx4 v[112:115],v6,s[56:59],s96 offen
buffer_store_dwordx4 v[116:119],v6,s[56:59],s96 offen offset:1024
buffer_store_dwordx4 v[120:123],v6,s[56:59],s96 offen offset:2048
buffer_store_dwordx4 v[124:127],v6,s[56:59],s96 offen offset:3072
s_mov_b32 s96,16384
buffer_store_dwordx4 v[128:131],v6,s[56:59],s96 offen
buffer_store_dwordx4 v[132:135],v6,s[56:59],s96 offen offset:1024
buffer_store_dwordx4 v[136:139],v6,s[56:59],s96 offen offset:2048
buffer_store_dwordx4 v[140:143],v6,s[56:59],s96 offen offset:3072
s_mov_b32 s96,20480
buffer_store_dwordx4 v[144:147],v6,s[56:59],s96 offen
buffer_store_dwordx4 v[148:151],v6,s[56:59],s96 offen offset:1024
buffer_store_dwordx4 v[152:155],v6,s[56:59],s96 offen offset:2048
buffer_store_dwordx4 v[156:159],v6,s[56:59],s96 offen offset:3072
s_mov_b32 s96,24576
buffer_store_dwordx4 v[160:163],v6,s[56:59],s96 offen
buffer_store_dwordx4 v[164:167],v6,s[56:59],s96 offen offset:1024
buffer_store_dwordx4 v[168:171],v6,s[56:59],s96 offen offset:2048
buffer_store_dwordx4 v[172:175],v6,s[56:59],s96 offen offset:3072
s_mov_b32 s96,28672
buffer_store_dwordx4 v[176:179],v6,s[56:59],s96 offen
buffer_store_dwordx4 v[180:183],v6,s[56:59],s96 offen offset:1024
buffer_store_dwordx4 v[184:187],v6,s[56:59],s96 offen offset:2048
buffer_store_dwordx4 v[188:191],v6,s[56:59],s96 offen offset:3072
s_waitcnt vmcnt(0)
v_writelane_b32 v173,s88,0
v_writelane_b32 v173,s89,1
v_writelane_b32 v173,s90,2
v_writelane_b32 v173,s91,3
v_writelane_b32 v173,s92,4
v_writelane_b32 v173,s93,5
v_writelane_b32 v173,s94,6
v_writelane_b32 v173,s95,7
s_mov_b32 s88,s98
s_mul_i32 s90,s41,307200
s_mov_b64 s[92:93],s[4:5]
s_add_u32 s92,s92,s90
s_addc_u32 s93,s93,0
s_and_b32 s93,s93,0xffff
s_mov_b32 s94,307200
s_mov_b32 s95,0x20000
s_mov_b64 s[96:97],s[10:11]
s_and_b32 s97,s97,0xffff
s_mov_b32 s98,5242880
s_mov_b32 s99,0x20000
v_bfe_u32 v171,v6,4,4
v_lshrrev_b32 v172,8,v6
v_lshlrev_b32 v164,8,v171
v_lshl_add_u32 v164,v172,4,v164
v_lshlrev_b32 v165,10,v172
v_lshl_add_u32 v165,v171,1,v165
v_add_u32 v166,4096,v165
v_lshlrev_b32 v168,16,v172
v_lshrrev_b32 v170,2,v171
v_lshl_add_u32 v168,v170,4,v168
v_and_b32 v170,3,v171
v_lshl_add_u32 v168,v170,1,v168
s_lshl_b32 s90,s42,1
s_add_u32 s90,s90,s88
s_lshl_b32 s90,s90,8
v_add_u32 v168,s90,v168
s_mov_b32 s89,0
L_sjb_%=:
s_add_u32 s90,s89,s70
s_add_u32 s90,s90,1
s_and_b32 s90,s90,3
s_lshl_b32 s91,s40,2
s_add_u32 s90,s90,s91
s_lshl_b32 s90,s90,4
v_bfe_u32 v171,v6,4,4
v_lshrrev_b32 v172,8,v6
v_add_u32 v167,s90,v171
v_mul_u32_u24 v167,1280,v167
v_lshl_add_u32 v167,v172,4,v167
s_lshl_b32 s90,s88,5
s_add_u32 s90,s90,s89
s_and_b32 s91,s89,2
s_add_u32 s90,s90,s91
s_lshl_b32 s90,s90,12
v_add_u32 v169,s90,v6
v_mov_b32 v64,0
v_mov_b32 v65,0
v_mov_b32 v66,0
v_mov_b32 v67,0
v_mov_b32 v68,0
v_mov_b32 v69,0
v_mov_b32 v70,0
v_mov_b32 v71,0
v_mov_b32 v72,0
v_mov_b32 v73,0
v_mov_b32 v74,0
v_mov_b32 v75,0
v_mov_b32 v76,0
v_mov_b32 v77,0
v_mov_b32 v78,0
v_mov_b32 v79,0
v_mov_b32 v80,0
v_mov_b32 v81,0
v_mov_b32 v82,0
v_mov_b32 v83,0
v_mov_b32 v84,0
v_mov_b32 v85,0
v_mov_b32 v86,0
v_mov_b32 v87,0
v_mov_b32 v88,0
v_mov_b32 v89,0
v_mov_b32 v90,0
v_mov_b32 v91,0
v_mov_b32 v92,0
v_mov_b32 v93,0
v_mov_b32 v94,0
v_mov_b32 v95,0
s_mov_b32 s90,0
L_sq_%=:
s_lshl_b32 s91,s90,6
buffer_load_dwordx4 v[160:163],v167,s[92:95],s91 offen
s_lshl_b32 s91,s90,18
buffer_load_ushort v112,v168,s[96:99],s91 offen
buffer_load_ushort v120,v168,s[96:99],s91 offen offset:8
buffer_load_ushort v128,v168,s[96:99],s91 offen offset:64
buffer_load_ushort v136,v168,s[96:99],s91 offen offset:72
s_add_u32 s91,s91,8192
buffer_load_ushort v113,v168,s[96:99],s91 offen
buffer_load_ushort v121,v168,s[96:99],s91 offen offset:8
buffer_load_ushort v129,v168,s[96:99],s91 offen offset:64
buffer_load_ushort v137,v168,s[96:99],s91 offen offset:72
s_add_u32 s91,s91,8192
buffer_load_ushort v114,v168,s[96:99],s91 offen
buffer_load_ushort v122,v168,s[96:99],s91 offen offset:8
buffer_load_ushort v130,v168,s[96:99],s91 offen offset:64
buffer_load_ushort v138,v168,s[96:99],s91 offen offset:72
s_add_u32 s91,s91,8192
buffer_load_ushort v115,v168,s[96:99],s91 offen
buffer_load_ushort v123,v168,s[96:99],s91 offen offset:8
buffer_load_ushort v131,v168,s[96:99],s91 offen offset:64
buffer_load_ushort v139,v168,s[96:99],s91 offen offset:72
s_add_u32 s91,s91,8192
buffer_load_ushort v116,v168,s[96:99],s91 offen
buffer_load_ushort v124,v168,s[96:99],s91 offen offset:8
buffer_load_ushort v132,v168,s[96:99],s91 offen offset:64
buffer_load_ushort v140,v168,s[96:99],s91 offen offset:72
s_add_u32 s91,s91,8192
buffer_load_ushort v117,v168,s[96:99],s91 offen
buffer_load_ushort v125,v168,s[96:99],s91 offen offset:8
buffer_load_ushort v133,v168,s[96:99],s91 offen offset:64
buffer_load_ushort v141,v168,s[96:99],s91 offen offset:72
s_add_u32 s91,s91,8192
buffer_load_ushort v118,v168,s[96:99],s91 offen
buffer_load_ushort v126,v168,s[96:99],s91 offen offset:8
buffer_load_ushort v134,v168,s[96:99],s91 offen offset:64
buffer_load_ushort v142,v168,s[96:99],s91 offen offset:72
s_add_u32 s91,s91,8192
buffer_load_ushort v119,v168,s[96:99],s91 offen
buffer_load_ushort v127,v168,s[96:99],s91 offen offset:8
buffer_load_ushort v135,v168,s[96:99],s91 offen offset:64
buffer_load_ushort v143,v168,s[96:99],s91 offen offset:72
s_add_u32 s91,s91,8192
s_waitcnt vmcnt(0)
v_lshl_or_b32 v112,v113,16,v112
v_lshl_or_b32 v113,v115,16,v114
v_lshl_or_b32 v114,v117,16,v116
v_lshl_or_b32 v115,v119,16,v118
v_lshl_or_b32 v120,v121,16,v120
v_lshl_or_b32 v121,v123,16,v122
v_lshl_or_b32 v122,v125,16,v124
v_lshl_or_b32 v123,v127,16,v126
v_lshl_or_b32 v128,v129,16,v128
v_lshl_or_b32 v129,v131,16,v130
v_lshl_or_b32 v130,v133,16,v132
)A"
R"A(v_lshl_or_b32 v131,v135,16,v134
v_lshl_or_b32 v136,v137,16,v136
v_lshl_or_b32 v137,v139,16,v138
v_lshl_or_b32 v138,v141,16,v140
v_lshl_or_b32 v139,v143,16,v142
s_nop 1
v_mfma_f32_16x16x32_bf16 v[64:67],v[112:115],v[160:163],v[64:67]
v_mfma_f32_16x16x32_bf16 v[68:71],v[120:123],v[160:163],v[68:71]
v_mfma_f32_16x16x32_bf16 v[72:75],v[128:131],v[160:163],v[72:75]
v_mfma_f32_16x16x32_bf16 v[76:79],v[136:139],v[160:163],v[76:79]
s_nop 7
s_lshl_b32 s91,s90,18
buffer_load_ushort v112,v168,s[96:99],s91 offen offset:128
buffer_load_ushort v120,v168,s[96:99],s91 offen offset:136
buffer_load_ushort v128,v168,s[96:99],s91 offen offset:192
buffer_load_ushort v136,v168,s[96:99],s91 offen offset:200
s_add_u32 s91,s91,8192
buffer_load_ushort v113,v168,s[96:99],s91 offen offset:128
buffer_load_ushort v121,v168,s[96:99],s91 offen offset:136
buffer_load_ushort v129,v168,s[96:99],s91 offen offset:192
buffer_load_ushort v137,v168,s[96:99],s91 offen offset:200
s_add_u32 s91,s91,8192
buffer_load_ushort v114,v168,s[96:99],s91 offen offset:128
buffer_load_ushort v122,v168,s[96:99],s91 offen offset:136
buffer_load_ushort v130,v168,s[96:99],s91 offen offset:192
buffer_load_ushort v138,v168,s[96:99],s91 offen offset:200
s_add_u32 s91,s91,8192
buffer_load_ushort v115,v168,s[96:99],s91 offen offset:128
buffer_load_ushort v123,v168,s[96:99],s91 offen offset:136
buffer_load_ushort v131,v168,s[96:99],s91 offen offset:192
buffer_load_ushort v139,v168,s[96:99],s91 offen offset:200
s_add_u32 s91,s91,8192
buffer_load_ushort v116,v168,s[96:99],s91 offen offset:128
buffer_load_ushort v124,v168,s[96:99],s91 offen offset:136
buffer_load_ushort v132,v168,s[96:99],s91 offen offset:192
buffer_load_ushort v140,v168,s[96:99],s91 offen offset:200
s_add_u32 s91,s91,8192
buffer_load_ushort v117,v168,s[96:99],s91 offen offset:128
buffer_load_ushort v125,v168,s[96:99],s91 offen offset:136
buffer_load_ushort v133,v168,s[96:99],s91 offen offset:192
buffer_load_ushort v141,v168,s[96:99],s91 offen offset:200
s_add_u32 s91,s91,8192
buffer_load_ushort v118,v168,s[96:99],s91 offen offset:128
buffer_load_ushort v126,v168,s[96:99],s91 offen offset:136
buffer_load_ushort v134,v168,s[96:99],s91 offen offset:192
buffer_load_ushort v142,v168,s[96:99],s91 offen offset:200
s_add_u32 s91,s91,8192
buffer_load_ushort v119,v168,s[96:99],s91 offen offset:128
buffer_load_ushort v127,v168,s[96:99],s91 offen offset:136
buffer_load_ushort v135,v168,s[96:99],s91 offen offset:192
buffer_load_ushort v143,v168,s[96:99],s91 offen offset:200
s_add_u32 s91,s91,8192
s_waitcnt vmcnt(0)
v_lshl_or_b32 v112,v113,16,v112
v_lshl_or_b32 v113,v115,16,v114
v_lshl_or_b32 v114,v117,16,v116
v_lshl_or_b32 v115,v119,16,v118
v_lshl_or_b32 v120,v121,16,v120
v_lshl_or_b32 v121,v123,16,v122
v_lshl_or_b32 v122,v125,16,v124
v_lshl_or_b32 v123,v127,16,v126
v_lshl_or_b32 v128,v129,16,v128
v_lshl_or_b32 v129,v131,16,v130
v_lshl_or_b32 v130,v133,16,v132
v_lshl_or_b32 v131,v135,16,v134
v_lshl_or_b32 v136,v137,16,v136
v_lshl_or_b32 v137,v139,16,v138
v_lshl_or_b32 v138,v141,16,v140
v_lshl_or_b32 v139,v143,16,v142
s_nop 1
v_mfma_f32_16x16x32_bf16 v[80:83],v[112:115],v[160:163],v[80:83]
v_mfma_f32_16x16x32_bf16 v[84:87],v[120:123],v[160:163],v[84:87]
v_mfma_f32_16x16x32_bf16 v[88:91],v[128:131],v[160:163],v[88:91]
v_mfma_f32_16x16x32_bf16 v[92:95],v[136:139],v[160:163],v[92:95]
s_nop 7
s_add_u32 s90,s90,1
s_cmp_lg_u32 s90,20
s_cbranch_scc1 L_sq_%=
s_nop 7
v_mul_f32 v64,s61,v64
v_mul_f32 v65,s61,v65
v_mul_f32 v66,s61,v66
v_mul_f32 v67,s61,v67
v_mul_f32 v68,s61,v68
v_mul_f32 v69,s61,v69
v_mul_f32 v70,s61,v70
v_mul_f32 v71,s61,v71
v_mul_f32 v72,s61,v72
v_mul_f32 v73,s61,v73
v_mul_f32 v74,s61,v74
v_mul_f32 v75,s61,v75
v_mul_f32 v76,s61,v76
v_mul_f32 v77,s61,v77
v_mul_f32 v78,s61,v78
v_mul_f32 v79,s61,v79
v_mul_f32 v80,s61,v80
v_mul_f32 v81,s61,v81
v_mul_f32 v82,s61,v82
v_mul_f32 v83,s61,v83
v_mul_f32 v84,s61,v84
v_mul_f32 v85,s61,v85
v_mul_f32 v86,s61,v86
v_mul_f32 v87,s61,v87
v_mul_f32 v88,s61,v88
v_mul_f32 v89,s61,v89
v_mul_f32 v90,s61,v90
v_mul_f32 v91,s61,v91
v_mul_f32 v92,s61,v92
v_mul_f32 v93,s61,v93
v_mul_f32 v94,s61,v94
v_mul_f32 v95,s61,v95
v_cvt_pk_bf16_f32 v96,v64,v65
v_cvt_pk_bf16_f32 v97,v66,v67
v_cvt_pk_bf16_f32 v98,v68,v69
v_cvt_pk_bf16_f32 v99,v70,v71
v_cvt_pk_bf16_f32 v100,v72,v73
v_cvt_pk_bf16_f32 v101,v74,v75
v_cvt_pk_bf16_f32 v102,v76,v77
v_cvt_pk_bf16_f32 v103,v78,v79
v_cvt_pk_bf16_f32 v104,v80,v81
v_cvt_pk_bf16_f32 v105,v82,v83
v_cvt_pk_bf16_f32 v106,v84,v85
v_cvt_pk_bf16_f32 v107,v86,v87
v_cvt_pk_bf16_f32 v108,v88,v89
v_cvt_pk_bf16_f32 v109,v90,v91
v_cvt_pk_bf16_f32 v110,v92,v93
v_cvt_pk_bf16_f32 v111,v94,v95
v_mov_b32 v170,0xff800000
s_lshl_b32 s91,s88,18
s_mov_b32 s90,64
L_s1_%=:
buffer_load_dwordx4 v[112:115],v164,s[20:23],s91 offen
buffer_load_dwordx4 v[116:119],v164,s[20:23],s91 offen offset:64
buffer_load_dwordx4 v[120:123],v164,s[20:23],s91 offen offset:128
buffer_load_dwordx4 v[124:127],v164,s[20:23],s91 offen offset:192
s_waitcnt vmcnt(0)
v_mfma_f32_16x16x32_bf16 v[144:147],v[112:115],v[96:99],0
v_mfma_f32_16x16x32_bf16 v[144:147],v[116:119],v[100:103],v[144:147]
v_mfma_f32_16x16x32_bf16 v[144:147],v[120:123],v[104:107],v[144:147]
v_mfma_f32_16x16x32_bf16 v[144:147],v[124:127],v[108:111],v[144:147]
s_nop 7
v_max3_f32 v170,v170,v144,v145
v_max3_f32 v170,v170,v146,v147
s_add_u32 s91,s91,4096
s_sub_u32 s90,s90,1
s_cmp_lg_u32 s90,0
s_cbranch_scc1 L_s1_%=
v_mov_b32 v171,v170
s_nop 1
v_permlane32_swap_b32 v170,v171
v_max_f32 v170,v170,v171
v_mov_b32 v171,v170
s_nop 1
v_permlane16_swap_b32 v170,v171
v_max_f32 v170,v170,v171
v_sub_f32 v152,0,v170
v_mov_b32 v156,0
v_sub_f32 v153,0,v170
v_mov_b32 v157,0
v_sub_f32 v154,0,v170
v_mov_b32 v158,0
v_sub_f32 v155,0,v170
v_mov_b32 v159,0
v_mov_b32 v64,0
v_mov_b32 v65,0
v_mov_b32 v66,0
v_mov_b32 v67,0
v_mov_b32 v68,0
v_mov_b32 v69,0
v_mov_b32 v70,0
v_mov_b32 v71,0
v_mov_b32 v72,0
v_mov_b32 v73,0
v_mov_b32 v74,0
v_mov_b32 v75,0
v_mov_b32 v76,0
v_mov_b32 v77,0
v_mov_b32 v78,0
v_mov_b32 v79,0
v_mov_b32 v80,0
v_mov_b32 v81,0
v_mov_b32 v82,0
v_mov_b32 v83,0
v_mov_b32 v84,0
v_mov_b32 v85,0
v_mov_b32 v86,0
v_mov_b32 v87,0
v_mov_b32 v88,0
v_mov_b32 v89,0
v_mov_b32 v90,0
v_mov_b32 v91,0
v_mov_b32 v92,0
v_mov_b32 v93,0
v_mov_b32 v94,0
v_mov_b32 v95,0
s_lshl_b32 s91,s88,18
s_mov_b32 s90,32
v_add_u32 v172,4096,v164
L_s2_%=:
buffer_load_dwordx4 v[112:115],v164,s[20:23],s91 offen
buffer_load_dwordx4 v[116:119],v164,s[20:23],s91 offen offset:64
buffer_load_dwordx4 v[120:123],v164,s[20:23],s91 offen offset:128
buffer_load_dwordx4 v[124:127],v164,s[20:23],s91 offen offset:192
buffer_load_dwordx4 v[128:131],v172,s[20:23],s91 offen
buffer_load_dwordx4 v[132:135],v172,s[20:23],s91 offen offset:64
buffer_load_dwordx4 v[136:139],v172,s[20:23],s91 offen offset:128
buffer_load_dwordx4 v[140:143],v172,s[20:23],s91 offen offset:192
s_waitcnt vmcnt(0)
v_mfma_f32_16x16x32_bf16 v[144:147],v[112:115],v[96:99],v[152:155]
v_mfma_f32_16x16x32_bf16 v[144:147],v[116:119],v[100:103],v[144:147]
v_mfma_f32_16x16x32_bf16 v[144:147],v[120:123],v[104:107],v[144:147]
v_mfma_f32_16x16x32_bf16 v[144:147],v[124:127],v[108:111],v[144:147]
v_mfma_f32_16x16x32_bf16 v[148:151],v[128:131],v[96:99],v[152:155]
v_mfma_f32_16x16x32_bf16 v[148:151],v[132:135],v[100:103],v[148:151]
v_mfma_f32_16x16x32_bf16 v[148:151],v[136:139],v[104:107],v[148:151]
v_mfma_f32_16x16x32_bf16 v[148:151],v[140:143],v[108:111],v[148:151]
s_nop 7
v_exp_f32 v144,v144
v_exp_f32 v145,v145
v_exp_f32 v146,v146
v_exp_f32 v147,v147
v_exp_f32 v148,v148
v_exp_f32 v149,v149
v_exp_f32 v150,v150
v_exp_f32 v151,v151
s_nop 0
v_cvt_pk_bf16_f32 v144,v144,v145
v_cvt_pk_bf16_f32 v145,v146,v147
v_cvt_pk_bf16_f32 v146,v148,v149
v_cvt_pk_bf16_f32 v147,v150,v151
buffer_load_ushort v112,v165,s[24:27],s91 offen
buffer_load_ushort v113,v165,s[24:27],s91 offen offset:256
buffer_load_ushort v114,v165,s[24:27],s91 offen offset:512
buffer_load_ushort v115,v165,s[24:27],s91 offen offset:768
buffer_load_ushort v116,v166,s[24:27],s91 offen
buffer_load_ushort v117,v166,s[24:27],s91 offen offset:256
buffer_load_ushort v118,v166,s[24:27],s91 offen offset:512
buffer_load_ushort v119,v166,s[24:27],s91 offen offset:768
buffer_load_ushort v120,v165,s[24:27],s91 offen offset:32
buffer_load_ushort v121,v165,s[24:27],s91 offen offset:288
buffer_load_ushort v122,v165,s[24:27],s91 offen offset:544
buffer_load_ushort v123,v165,s[24:27],s91 offen offset:800
buffer_load_ushort v124,v166,s[24:27],s91 offen offset:32
buffer_load_ushort v125,v166,s[24:27],s91 offen offset:288
buffer_load_ushort v126,v166,s[24:27],s91 offen offset:544
buffer_load_ushort v127,v166,s[24:27],s91 offen offset:800
buffer_load_ushort v128,v165,s[24:27],s91 offen offset:64
buffer_load_ushort v129,v165,s[24:27],s91 offen offset:320
buffer_load_ushort v130,v165,s[24:27],s91 offen offset:576
buffer_load_ushort v131,v165,s[24:27],s91 offen offset:832
buffer_load_ushort v132,v166,s[24:27],s91 offen offset:64
buffer_load_ushort v133,v166,s[24:27],s91 offen offset:320
buffer_load_ushort v134,v166,s[24:27],s91 offen offset:576
buffer_load_ushort v135,v166,s[24:27],s91 offen offset:832
buffer_load_ushort v136,v165,s[24:27],s91 offen offset:96
buffer_load_ushort v137,v165,s[24:27],s91 offen offset:352
buffer_load_ushort v138,v165,s[24:27],s91 offen offset:608
buffer_load_ushort v139,v165,s[24:27],s91 offen offset:864
buffer_load_ushort v140,v166,s[24:27],s91 offen offset:96
buffer_load_ushort v141,v166,s[24:27],s91 offen offset:352
buffer_load_ushort v142,v166,s[24:27],s91 offen offset:608
buffer_load_ushort v143,v166,s[24:27],s91 offen offset:864
s_waitcnt vmcnt(0)
v_lshl_or_b32 v112,v113,16,v112
v_lshl_or_b32 v113,v115,16,v114
v_lshl_or_b32 v114,v117,16,v116
v_lshl_or_b32 v115,v119,16,v118
v_lshl_or_b32 v120,v121,16,v120
v_lshl_or_b32 v121,v123,16,v122
v_lshl_or_b32 v122,v125,16,v124
v_lshl_or_b32 v123,v127,16,v126
v_lshl_or_b32 v128,v129,16,v128
v_lshl_or_b32 v129,v131,16,v130
v_lshl_or_b32 v130,v133,16,v132
v_lshl_or_b32 v131,v135,16,v134
v_lshl_or_b32 v136,v137,16,v136
v_lshl_or_b32 v137,v139,16,v138
v_lshl_or_b32 v138,v141,16,v140
v_lshl_or_b32 v139,v143,16,v142
s_nop 1
v_mfma_f32_16x16x32_bf16 v[64:67],v[112:115],v[144:147],v[64:67]
v_mfma_f32_16x16x32_bf16 v[68:71],v[120:123],v[144:147],v[68:71]
v_mfma_f32_16x16x32_bf16 v[72:75],v[128:131],v[144:147],v[72:75]
v_mfma_f32_16x16x32_bf16 v[76:79],v[136:139],v[144:147],v[76:79]
s_nop 7
buffer_load_ushort v112,v165,s[24:27],s91 offen offset:128
buffer_load_ushort v113,v165,s[24:27],s91 offen offset:384
buffer_load_ushort v114,v165,s[24:27],s91 offen offset:640
buffer_load_ushort v115,v165,s[24:27],s91 offen offset:896
buffer_load_ushort v116,v166,s[24:27],s91 offen offset:128
buffer_load_ushort v117,v166,s[24:27],s91 offen offset:384
buffer_load_ushort v118,v166,s[24:27],s91 offen offset:640
buffer_load_ushort v119,v166,s[24:27],s91 offen offset:896
buffer_load_ushort v120,v165,s[24:27],s91 offen offset:160
buffer_load_ushort v121,v165,s[24:27],s91 offen offset:416
buffer_load_ushort v122,v165,s[24:27],s91 offen offset:672
buffer_load_ushort v123,v165,s[24:27],s91 offen offset:928
buffer_load_ushort v124,v166,s[24:27],s91 offen offset:160
buffer_load_ushort v125,v166,s[24:27],s91 offen offset:416
buffer_load_ushort v126,v166,s[24:27],s91 offen offset:672
buffer_load_ushort v127,v166,s[24:27],s91 offen offset:928
buffer_load_ushort v128,v165,s[24:27],s91 offen offset:192
buffer_load_ushort v129,v165,s[24:27],s91 offen offset:448
buffer_load_ushort v130,v165,s[24:27],s91 offen offset:704
buffer_load_ushort v131,v165,s[24:27],s91 offen offset:960
buffer_load_ushort v132,v166,s[24:27],s91 offen offset:192
buffer_load_ushort v133,v166,s[24:27],s91 offen offset:448
buffer_load_ushort v134,v166,s[24:27],s91 offen offset:704
buffer_load_ushort v135,v166,s[24:27],s91 offen offset:960
buffer_load_ushort v136,v165,s[24:27],s91 offen offset:224
buffer_load_ushort v137,v165,s[24:27],s91 offen offset:480
buffer_load_ushort v138,v165,s[24:27],s91 offen offset:736
buffer_load_ushort v139,v165,s[24:27],s91 offen offset:992
buffer_load_ushort v140,v166,s[24:27],s91 offen offset:224
)A"
R"A(buffer_load_ushort v141,v166,s[24:27],s91 offen offset:480
buffer_load_ushort v142,v166,s[24:27],s91 offen offset:736
buffer_load_ushort v143,v166,s[24:27],s91 offen offset:992
s_waitcnt vmcnt(0)
v_lshl_or_b32 v112,v113,16,v112
v_lshl_or_b32 v113,v115,16,v114
v_lshl_or_b32 v114,v117,16,v116
v_lshl_or_b32 v115,v119,16,v118
v_lshl_or_b32 v120,v121,16,v120
v_lshl_or_b32 v121,v123,16,v122
v_lshl_or_b32 v122,v125,16,v124
v_lshl_or_b32 v123,v127,16,v126
v_lshl_or_b32 v128,v129,16,v128
v_lshl_or_b32 v129,v131,16,v130
v_lshl_or_b32 v130,v133,16,v132
v_lshl_or_b32 v131,v135,16,v134
v_lshl_or_b32 v136,v137,16,v136
v_lshl_or_b32 v137,v139,16,v138
v_lshl_or_b32 v138,v141,16,v140
v_lshl_or_b32 v139,v143,16,v142
s_nop 1
v_mfma_f32_16x16x32_bf16 v[80:83],v[112:115],v[144:147],v[80:83]
v_mfma_f32_16x16x32_bf16 v[84:87],v[120:123],v[144:147],v[84:87]
v_mfma_f32_16x16x32_bf16 v[88:91],v[128:131],v[144:147],v[88:91]
v_mfma_f32_16x16x32_bf16 v[92:95],v[136:139],v[144:147],v[92:95]
s_nop 7
v_mfma_f32_16x16x32_bf16 v[156:159],v[28:31],v[144:147],v[156:159]
s_nop 7
s_add_u32 s91,s91,8192
s_sub_u32 s90,s90,1
s_cmp_lg_u32 s90,0
s_cbranch_scc1 L_s2_%=
v_rcp_f32 v170,v156
s_nop 0
v_mul_f32 v64,v170,v64
v_mul_f32 v65,v170,v65
v_mul_f32 v66,v170,v66
v_mul_f32 v67,v170,v67
v_mul_f32 v68,v170,v68
v_mul_f32 v69,v170,v69
v_mul_f32 v70,v170,v70
v_mul_f32 v71,v170,v71
v_mul_f32 v72,v170,v72
v_mul_f32 v73,v170,v73
v_mul_f32 v74,v170,v74
v_mul_f32 v75,v170,v75
v_mul_f32 v76,v170,v76
v_mul_f32 v77,v170,v77
v_mul_f32 v78,v170,v78
v_mul_f32 v79,v170,v79
v_mul_f32 v80,v170,v80
v_mul_f32 v81,v170,v81
v_mul_f32 v82,v170,v82
v_mul_f32 v83,v170,v83
v_mul_f32 v84,v170,v84
v_mul_f32 v85,v170,v85
v_mul_f32 v86,v170,v86
v_mul_f32 v87,v170,v87
v_mul_f32 v88,v170,v88
v_mul_f32 v89,v170,v89
v_mul_f32 v90,v170,v90
v_mul_f32 v91,v170,v91
v_mul_f32 v92,v170,v92
v_mul_f32 v93,v170,v93
v_mul_f32 v94,v170,v94
v_mul_f32 v95,v170,v95
v_cvt_pk_bf16_f32 v96,v64,v65
v_cvt_pk_bf16_f32 v97,v66,v67
v_cvt_pk_bf16_f32 v98,v68,v69
v_cvt_pk_bf16_f32 v99,v70,v71
v_cvt_pk_bf16_f32 v100,v72,v73
v_cvt_pk_bf16_f32 v101,v74,v75
v_cvt_pk_bf16_f32 v102,v76,v77
v_cvt_pk_bf16_f32 v103,v78,v79
v_cvt_pk_bf16_f32 v104,v80,v81
v_cvt_pk_bf16_f32 v105,v82,v83
v_cvt_pk_bf16_f32 v106,v84,v85
v_cvt_pk_bf16_f32 v107,v86,v87
v_cvt_pk_bf16_f32 v108,v88,v89
v_cvt_pk_bf16_f32 v109,v90,v91
v_cvt_pk_bf16_f32 v110,v92,v93
v_cvt_pk_bf16_f32 v111,v94,v95
buffer_store_dwordx4 v[96:99],v169,s[36:39],s55 offen sc1
buffer_store_dwordx4 v[100:103],v169,s[36:39],s55 offen offset:1024 sc1
buffer_store_dwordx4 v[104:107],v169,s[36:39],s55 offen offset:2048 sc1
buffer_store_dwordx4 v[108:111],v169,s[36:39],s55 offen offset:3072 sc1
s_waitcnt vmcnt(0)
s_add_u32 s89,s89,1
s_cmp_lg_u32 s89,4
s_cbranch_scc1 L_sjb_%=
s_mov_b32 s98,s88
v_readlane_b32 s88,v173,0
v_readlane_b32 s89,v173,1
v_readlane_b32 s90,v173,2
v_readlane_b32 s91,v173,3
v_readlane_b32 s92,v173,4
v_readlane_b32 s93,v173,5
v_readlane_b32 s94,v173,6
v_readlane_b32 s95,v173,7
s_nop 4
s_mov_b32 s96,0
buffer_load_dwordx4 v[64:67],v6,s[56:59],s96 offen sc1
buffer_load_dwordx4 v[68:71],v6,s[56:59],s96 offen offset:1024 sc1
buffer_load_dwordx4 v[72:75],v6,s[56:59],s96 offen offset:2048 sc1
buffer_load_dwordx4 v[76:79],v6,s[56:59],s96 offen offset:3072 sc1
s_mov_b32 s96,4096
buffer_load_dwordx4 v[80:83],v6,s[56:59],s96 offen sc1
buffer_load_dwordx4 v[84:87],v6,s[56:59],s96 offen offset:1024 sc1
buffer_load_dwordx4 v[88:91],v6,s[56:59],s96 offen offset:2048 sc1
buffer_load_dwordx4 v[92:95],v6,s[56:59],s96 offen offset:3072 sc1
s_mov_b32 s96,8192
buffer_load_dwordx4 v[96:99],v6,s[56:59],s96 offen sc1
buffer_load_dwordx4 v[100:103],v6,s[56:59],s96 offen offset:1024 sc1
buffer_load_dwordx4 v[104:107],v6,s[56:59],s96 offen offset:2048 sc1
buffer_load_dwordx4 v[108:111],v6,s[56:59],s96 offen offset:3072 sc1
s_mov_b32 s96,12288
buffer_load_dwordx4 v[112:115],v6,s[56:59],s96 offen sc1
buffer_load_dwordx4 v[116:119],v6,s[56:59],s96 offen offset:1024 sc1
buffer_load_dwordx4 v[120:123],v6,s[56:59],s96 offen offset:2048 sc1
buffer_load_dwordx4 v[124:127],v6,s[56:59],s96 offen offset:3072 sc1
s_mov_b32 s96,16384
buffer_load_dwordx4 v[128:131],v6,s[56:59],s96 offen sc1
buffer_load_dwordx4 v[132:135],v6,s[56:59],s96 offen offset:1024 sc1
buffer_load_dwordx4 v[136:139],v6,s[56:59],s96 offen offset:2048 sc1
buffer_load_dwordx4 v[140:143],v6,s[56:59],s96 offen offset:3072 sc1
s_mov_b32 s96,20480
buffer_load_dwordx4 v[144:147],v6,s[56:59],s96 offen sc1
buffer_load_dwordx4 v[148:151],v6,s[56:59],s96 offen offset:1024 sc1
buffer_load_dwordx4 v[152:155],v6,s[56:59],s96 offen offset:2048 sc1
buffer_load_dwordx4 v[156:159],v6,s[56:59],s96 offen offset:3072 sc1
s_mov_b32 s96,24576
buffer_load_dwordx4 v[160:163],v6,s[56:59],s96 offen sc1
buffer_load_dwordx4 v[164:167],v6,s[56:59],s96 offen offset:1024 sc1
buffer_load_dwordx4 v[168:171],v6,s[56:59],s96 offen offset:2048 sc1
buffer_load_dwordx4 v[172:175],v6,s[56:59],s96 offen offset:3072 sc1
s_mov_b32 s96,28672
buffer_load_dwordx4 v[176:179],v6,s[56:59],s96 offen sc1
buffer_load_dwordx4 v[180:183],v6,s[56:59],s96 offen offset:1024 sc1
buffer_load_dwordx4 v[184:187],v6,s[56:59],s96 offen offset:2048 sc1
buffer_load_dwordx4 v[188:191],v6,s[56:59],s96 offen offset:3072 sc1
s_waitcnt vmcnt(0)
s_cmp_lg_u32 s98,0
s_cbranch_scc1 L_sr1_%=
buffer_load_dwordx4 v[64:67],v6,s[36:39],s84 offen sc1
buffer_load_dwordx4 v[68:71],v6,s[36:39],s84 offen offset:1024 sc1
buffer_load_dwordx4 v[72:75],v6,s[36:39],s84 offen offset:2048 sc1
buffer_load_dwordx4 v[76:79],v6,s[36:39],s84 offen offset:3072 sc1
buffer_load_dwordx4 v[80:83],v6,s[36:39],s85 offen sc1
buffer_load_dwordx4 v[84:87],v6,s[36:39],s85 offen offset:1024 sc1
buffer_load_dwordx4 v[88:91],v6,s[36:39],s85 offen offset:2048 sc1
buffer_load_dwordx4 v[92:95],v6,s[36:39],s85 offen offset:3072 sc1
buffer_load_dwordx4 v[96:99],v6,s[36:39],s86 offen sc1
buffer_load_dwordx4 v[100:103],v6,s[36:39],s86 offen offset:1024 sc1
buffer_load_dwordx4 v[104:107],v6,s[36:39],s86 offen offset:2048 sc1
buffer_load_dwordx4 v[108:111],v6,s[36:39],s86 offen offset:3072 sc1
buffer_load_dwordx4 v[112:115],v6,s[36:39],s87 offen sc1
buffer_load_dwordx4 v[116:119],v6,s[36:39],s87 offen offset:1024 sc1
buffer_load_dwordx4 v[120:123],v6,s[36:39],s87 offen offset:2048 sc1
buffer_load_dwordx4 v[124:127],v6,s[36:39],s87 offen offset:3072 sc1
s_waitcnt vmcnt(0)
s_nop 7
s_branch L_sret0_%=
L_sr1_%=:
buffer_load_dwordx4 v[136:139],v6,s[36:39],s84 offen sc1
buffer_load_dwordx4 v[140:143],v6,s[36:39],s84 offen offset:1024 sc1
buffer_load_dwordx4 v[152:155],v6,s[36:39],s84 offen offset:2048 sc1
buffer_load_dwordx4 v[156:159],v6,s[36:39],s84 offen offset:3072 sc1
buffer_load_dwordx4 v[168:171],v6,s[36:39],s85 offen sc1
buffer_load_dwordx4 v[172:175],v6,s[36:39],s85 offen offset:1024 sc1
buffer_load_dwordx4 v[184:187],v6,s[36:39],s85 offen offset:2048 sc1
buffer_load_dwordx4 v[188:191],v6,s[36:39],s85 offen offset:3072 sc1
buffer_load_dwordx4 v[200:203],v6,s[36:39],s86 offen sc1
buffer_load_dwordx4 v[204:207],v6,s[36:39],s86 offen offset:1024 sc1
buffer_load_dwordx4 v[216:219],v6,s[36:39],s86 offen offset:2048 sc1
buffer_load_dwordx4 v[220:223],v6,s[36:39],s86 offen offset:3072 sc1
buffer_load_dwordx4 v[232:235],v6,s[36:39],s87 offen sc1
buffer_load_dwordx4 v[236:239],v6,s[36:39],s87 offen offset:1024 sc1
buffer_load_dwordx4 v[248:251],v6,s[36:39],s87 offen offset:2048 sc1
buffer_load_dwordx4 v[252:255],v6,s[36:39],s87 offen offset:3072 sc1
s_waitcnt vmcnt(0)
s_nop 7
s_branch L_sret1_%=
L_end_%=:
)A"
    :: "s"(karg), "s"(blk), "s"(wid)
    : "v0", "v1", "v2", "v3", "v4", "v5", "v6", "v7", "v8", "v9", "v10", "v11", "v12", "v13", "v14", "v15", "v16", "v17", "v18", "v19", "v20", "v21", "v22", "v23", "v24", "v25", "v26", "v27", "v28", "v29", "v30", "v31", "v32", "v33", "v34", "v35", "v36", "v37", "v38", "v39", "v40", "v41", "v42", "v43", "v44", "v45", "v46", "v47", "v48", "v49", "v50", "v51", "v52", "v53", "v54", "v55", "v56", "v57", "v58", "v59", "v60", "v61", "v62", "v63", "v64", "v65", "v66", "v67", "v68", "v69", "v70", "v71", "v72", "v73", "v74", "v75", "v76", "v77", "v78", "v79", "v80", "v81", "v82", "v83", "v84", "v85", "v86", "v87", "v88", "v89", "v90", "v91", "v92", "v93", "v94", "v95", "v96", "v97", "v98", "v99", "v100", "v101", "v102", "v103", "v104", "v105", "v106", "v107", "v108", "v109", "v110", "v111", "v112", "v113", "v114", "v115", "v116", "v117", "v118", "v119", "v120", "v121", "v122", "v123", "v124", "v125", "v126", "v127", "v128", "v129", "v130", "v131", "v132", "v133", "v134", "v135", "v136", "v137", "v138", "v139", "v140", "v141", "v142", "v143", "v144", "v145", "v146", "v147", "v148", "v149", "v150", "v151", "v152", "v153", "v154", "v155", "v156", "v157", "v158", "v159", "v160", "v161", "v162", "v163", "v164", "v165", "v166", "v167", "v168", "v169", "v170", "v171", "v172", "v173", "v174", "v175", "v176", "v177", "v178", "v179", "v180", "v181", "v182", "v183", "v184", "v185", "v186", "v187", "v188", "v189", "v190", "v191", "v192", "v193", "v194", "v195", "v196", "v197", "v198", "v199", "v200", "v201", "v202", "v203", "v204", "v205", "v206", "v207", "v208", "v209", "v210", "v211", "v212", "v213", "v214", "v215", "v216", "v217", "v218", "v219", "v220", "v221", "v222", "v223", "v224", "v225", "v226", "v227", "v228", "v229", "v230", "v231", "v232", "v233", "v234", "v235", "v236", "v237", "v238", "v239", "v240", "v241", "v242", "v243", "v244", "v245", "v246", "v247", "v248", "v249", "v250", "v251", "v252", "v253", "v254", "v255", "a0", "a1", "a2", "a3", "a4", "a5", "a6", "a7", "a8", "a9", "a10", "a11", "a12", "a13", "a14", "a15", "a16", "a17", "a18", "a19", "a20", "a21", "a22", "a23", "a24", "a25", "a26", "a27", "a28", "a29", "a30", "a31", "a32", "a33", "a34", "a35", "a36", "a37", "a38", "a39", "a40", "a41", "a42", "a43", "a44", "a45", "a46", "a47", "a48", "a49", "a50", "a51", "a52", "a53", "a54", "a55", "a56", "a57", "a58", "a59", "a60", "a61", "a62", "a63", "a64", "a65", "a66", "a67", "a68", "a69", "a70", "a71", "a72", "a73", "a74", "a75", "a76", "a77", "a78", "a79", "a80", "a81", "a82", "a83", "a84", "a85", "a86", "a87", "a88", "a89", "a90", "a91", "a92", "a93", "a94", "a95", "a96", "a97", "a98", "a99", "a100", "a101", "a102", "a103", "a104", "a105", "a106", "a107", "a108", "a109", "a110", "a111", "a112", "a113", "a114", "a115", "a116", "a117", "a118", "a119", "a120", "a121", "a122", "a123", "a124", "a125", "a126", "a127", "a128", "a129", "a130", "a131", "a132", "a133", "a134", "a135", "a136", "a137", "a138", "a139", "a140", "a141", "a142", "a143", "a144", "a145", "a146", "a147", "a148", "a149", "a150", "a151", "a152", "a153", "a154", "a155", "a156", "a157", "a158", "a159", "a160", "a161", "a162", "a163", "a164", "a165", "a166", "a167", "a168", "a169", "a170", "a171", "a172", "a173", "a174", "a175", "a176", "a177", "a178", "a179", "a180", "a181", "a182", "a183", "a184", "a185", "a186", "a187", "a188", "a189", "a190", "a191", "a192", "a193", "a194", "a195", "a196", "a197", "a198", "a199", "a200", "a201", "a202", "a203", "a204", "a205", "a206", "a207", "a208", "a209", "a210", "a211", "a212", "a213", "a214", "a215", "a216", "a217", "a218", "a219", "a220", "a221", "a222", "a223", "a224", "a225", "a226", "a227", "a228", "a229", "a230", "a231", "a232", "a233", "a234", "a235", "a236", "a237", "a238", "a239", "a240", "a241", "a242", "a243", "a244", "a245", "a246", "a247", "a248", "a249", "a250", "a251", "a252", "a253", "a254", "a255", "s4", "s5", "s6", "s7", "s8", "s9", "s10", "s11", "s12", "s13", "s14", "s15", "s16", "s17", "s18", "s19", "s20", "s21", "s22", "s23", "s24", "s25", "s26", "s27", "s28", "s29", "s30", "s31", "s32", "s33", "s34", "s35", "s36", "s37", "s38", "s39", "s40", "s41", "s42", "s43", "s44", "s45", "s46", "s47", "s48", "s49", "s50", "s51", "s52", "s53", "s54", "s55", "s56", "s57", "s58", "s59", "s60", "s61", "s62", "s63", "s64", "s65", "s66", "s67", "s68", "s69", "s70", "s71", "s72", "s73", "s74", "s75", "s76", "s77", "s78", "s79", "s80", "s81", "s82", "s83", "s84", "s85", "s86", "s87", "s88", "s89", "s90", "s91", "s92", "s93", "s94", "s95", "s96", "s97", "s98", "s99", "s100", "s101", "vcc", "scc", "memory");
#endif
}

extern "C" void kernel_launch(void* const* d_in, const int* in_sizes, int n_in,
                              void* d_out, int out_size, void* d_ws, size_t ws_size,
                              hipStream_t stream) {
  (void)hipFuncSetAttribute((const void*)qattn_kernel, hipFuncAttributeMaxDynamicSharedMemorySize, LDS_ATT);
  qattn_kernel<<<256, 256, LDS_ATT, stream>>>(d_in[0], d_in[1], d_in[2], d_in[3], d_ws, d_in[4], d_out);
}
```

```cpp
#include <hip/hip_runtime.h>
#include <hip/hip_bf16.h>

constexpr int LDS_ATT = 163840;

__global__ __launch_bounds__(256, 1) void qattn_kernel(const void* __restrict__ x, const void* __restrict__ kc, const void* __restrict__ vc,
                                                       const void* __restrict__ wq, void* __restrict__ ws, const void* __restrict__ wo,
                                                       void* __restrict__ out) {
#if defined(__HIP_DEVICE_COMPILE__)
  const auto karg = __builtin_amdgcn_kernarg_segment_ptr();
  const unsigned blk = blockIdx.x;
  const unsigned wid = __builtin_amdgcn_readfirstlane(threadIdx.x >> 6);
  asm volatile(
R"A(s_memtime s[72:73]
s_memrealtime s[74:75]
s_load_dwordx8 s[4:11],%0,0x0
s_load_dwordx4 s[12:15],%0,0x20
s_load_dwordx2 s[16:17],%0,0x30
s_mov_b32 s40,%2
s_mov_b32 s43,%1
s_lshr_b32 s45,s43,3
v_mbcnt_lo_u32_b32 v12,-1,0
v_mbcnt_hi_u32_b32 v12,-1,v12
s_getpc_b64 s[88:89]
L_pc_%=:
s_and_b32 s88,s88,0xffffff80
s_and_b32 s89,s89,0xffff
s_mov_b32 s90,L_fast_end_%=-L_pc_%=
s_mov_b32 s91,0x20000
s_lshl_b32 s44,s45,2
s_add_u32 s44,s44,s40
s_mulk_i32 s44,896
v_lshlrev_b32 v67,7,v12
s_mov_b64 exec,127
buffer_load_dword v19,v67,s[88:91],s44 offen
s_mov_b64 exec,-1
s_and_b32 s70,s43,3
s_and_b32 s71,s45,3
s_lshl_b32 s42,s70,2
s_add_u32 s42,s42,s71
s_and_b32 s41,s43,4
s_lshl_b32 s41,s41,1
s_lshr_b32 s46,s45,2
s_add_u32 s41,s41,s46
s_lshl_b32 s56,s42,9
s_mov_b32 s57,0
s_lshl_b32 s59,s40,13
s_add_u32 s58,s59,65536
v_and_b32 v64,3,v12
v_bfe_u32 v65,v12,2,2
v_lshrrev_b32 v66,4,v12
s_lshl_b32 s44,s40,17
v_lshlrev_b32 v75,13,v65
v_lshl_add_u32 v75,v66,6,v75
v_lshl_add_u32 v75,v64,4,v75
v_add_u32 v75,s44,v75
v_add_u32 v56,0,v75
v_add_u32 v57,256,v75
v_add_u32 v58,32768,v75
v_add_u32 v59,33024,v75
v_add_u32 v60,65536,v75
v_add_u32 v61,65792,v75
v_add_u32 v62,98304,v75
v_add_u32 v63,98560,v75
s_waitcnt lgkmcnt(0)
s_mov_b64 s[32:33],s[10:11]
s_and_b32 s33,s33,0xffff
s_mov_b32 s34,5242880
s_mov_b32 s35,0x20000
s_add_u32 m0,s58,1024
s_nop 0
buffer_load_dwordx4 v57,s[32:35],s56 offen lds
s_add_u32 m0,s58,3072
s_nop 0
buffer_load_dwordx4 v59,s[32:35],s56 offen lds
s_add_u32 m0,s58,5120
s_nop 0
buffer_load_dwordx4 v61,s[32:35],s56 offen lds
s_add_u32 m0,s58,7168
s_nop 0
buffer_load_dwordx4 v63,s[32:35],s56 offen lds
v_lshrrev_b32 v75,3,v12
s_lshl_b32 s44,s40,6
v_add_u32 v75,s44,v75
v_mul_u32_u24 v75,1280,v75
v_and_b32 v76,7,v12
s_add_u32 s84,s70,1
s_and_b32 s84,s84,3
s_mulk_i32 s84,20480
s_addk_i32 s84,4096
s_add_u32 s85,s70,2
s_and_b32 s85,s85,3
s_mulk_i32 s85,20480
s_addk_i32 s85,2048
s_add_u32 s86,s70,3
s_and_b32 s86,s86,3
s_mulk_i32 s86,20480
s_addk_i32 s86,4096
s_add_u32 s87,s70,4
s_and_b32 s87,s87,3
s_mulk_i32 s87,20480
s_addk_i32 s87,2048
v_or_b32 v77,0,v66
v_xor_b32 v77,v77,v76
v_lshl_add_u32 v77,v77,4,v75
v_add_u32 v48,s84,v77
v_add_u32 v50,s85,v77
v_add_u32 v52,s86,v77
v_add_u32 v54,s87,v77
v_or_b32 v77,4,v66
v_xor_b32 v77,v77,v76
v_lshl_add_u32 v77,v77,4,v75
v_add_u32 v77,9216,v77
v_add_u32 v49,s84,v77
v_add_u32 v51,s85,v77
v_add_u32 v53,s86,v77
v_add_u32 v55,s87,v77
s_mul_i32 s44,s41,307200
s_mov_b64 s[28:29],s[4:5]
s_add_u32 s28,s28,s44
s_addc_u32 s29,s29,0
s_sub_u32 s28,s28,4096
s_subb_u32 s29,s29,0
s_and_b32 s29,s29,0xffff
s_mov_b32 s30,311296
s_mov_b32 s31,0x20000
s_add_u32 m0,s59,0
s_nop 0
buffer_load_dwordx4 v48,s[28:31],s57 offen lds
buffer_load_dwordx4 v49,s[28:31],s57 offen offset:1024 lds
buffer_load_dwordx4 v50,s[28:31],s57 offen offset:2048 lds
buffer_load_dwordx4 v51,s[28:31],s57 offen offset:3072 lds
s_addk_i32 m0,4096
s_nop 0
buffer_load_dwordx4 v52,s[28:31],s57 offen lds
buffer_load_dwordx4 v53,s[28:31],s57 offen offset:1024 lds
buffer_load_dwordx4 v54,s[28:31],s57 offen offset:2048 lds
buffer_load_dwordx4 v55,s[28:31],s57 offen offset:3072 lds
s_addk_i32 s57,128
s_xor_b32 s59,s59,131072
s_add_u32 m0,s58,0
s_nop 0
buffer_load_dwordx4 v56,s[32:35],s56 offen lds
s_add_u32 m0,s58,2048
s_nop 0
buffer_load_dwordx4 v58,s[32:35],s56 offen lds
s_add_u32 m0,s58,4096
s_nop 0
buffer_load_dwordx4 v60,s[32:35],s56 offen lds
s_add_u32 m0,s58,6144
s_nop 0
buffer_load_dwordx4 v62,s[32:35],s56 offen lds
s_add_u32 s56,s56,524288
s_add_u32 s58,s58,32768
s_cmp_ge_u32 s58,131072
s_cselect_b32 s62,98304,0
s_sub_u32 s58,s58,s62
s_add_u32 s12,s12,4194303
s_addc_u32 s13,s13,0
s_and_b32 s12,s12,0xffc00000
s_lshl_b32 s55,s40,15
s_lshl_b32 s84,s41,2
s_add_u32 s84,s84,s70
s_lshl_b32 s84,s84,7
s_lshl_b32 s85,s71,2
s_add_u32 s84,s84,s85
s_add_u32 s84,s84,s40
s_addk_i32 s84,112
s_lshl_b32 s84,s84,14
s_add_u32 s84,s84,s12
s_addc_u32 s85,s13,0
s_mov_b32 s87,0x13572468
s_mov_b32 s79,0
s_load_dword s78,s[84:85],0x1000 glc
s_xor_b32 s76,s84,2097152
s_mov_b32 s77,s85
s_load_dword s78,s[76:77],0x0 glc
s_load_dword s78,s[76:77],0x2000 glc
s_load_dword s78,s[76:77],0x100 glc
s_load_dword s78,s[76:77],0x2100 glc
s_load_dword s78,s[76:77],0x200 glc
s_load_dword s78,s[76:77],0x2200 glc
s_add_u32 m0,s58,0
s_nop 0
buffer_load_dwordx4 v56,s[32:35],s56 offen lds
s_addk_i32 m0,1024
s_nop 0
buffer_load_dwordx4 v57,s[32:35],s56 offen lds
s_addk_i32 m0,1024
s_nop 0
buffer_load_dwordx4 v58,s[32:35],s56 offen lds
s_addk_i32 m0,1024
s_nop 0
buffer_load_dwordx4 v59,s[32:35],s56 offen lds
s_addk_i32 m0,1024
s_nop 0
buffer_load_dwordx4 v60,s[32:35],s56 offen lds
s_addk_i32 m0,1024
s_nop 0
buffer_load_dwordx4 v61,s[32:35],s56 offen lds
s_addk_i32 m0,1024
s_nop 0
buffer_load_dwordx4 v62,s[32:35],s56 offen lds
s_addk_i32 m0,1024
s_nop 0
buffer_load_dwordx4 v63,s[32:35],s56 offen lds
s_add_u32 s56,s56,524288
s_add_u32 s58,s58,32768
s_cmp_ge_u32 s58,131072
s_cselect_b32 s62,98304,0
s_sub_u32 s58,s58,s62
s_mov_b32 s60,65536
v_lshlrev_b32 v6,4,v12
v_lshlrev_b32 v72,12,v66
v_lshl_add_u32 v72,v65,6,v72
v_lshl_add_u32 v23,v64,4,v72
v_add_u32 v22,65536,v23
v_and_b32 v74,15,v12
v_lshrrev_b32 v76,1,v74
v_lshlrev_b32 v75,7,v74
s_lshl_b32 s44,s40,13
v_add_u32 v75,s44,v75
v_or_b32 v77,0,v66
v_xor_b32 v77,v77,v76
v_lshl_add_u32 v20,v77,4,v75
v_or_b32 v77,4,v66
v_xor_b32 v77,v77,v76
v_lshl_add_u32 v21,v77,4,v75
s_waitcnt vmcnt(12)
s_nop 0
s_memtime s[64:65]
s_memrealtime s[66:67]
s_bfe_u32 s68,%1,327683
s_waitcnt lgkmcnt(0)
s_sub_u32 s64,s64,s72
s_sub_u32 s66,s66,s74
s_mul_i32 s64,s64,10
s_mulk_i32 s66,224
s_cmp_lt_u32 s64,s66
s_cselect_b32 s95,64,0
s_mov_b32 %2,s95
s_cbranch_scc1 L_se1_%=
L_sg1_%=:
s_cmp_eq_u32 s68,0
s_cbranch_scc1 L_se1_%=
s_sleep 4
s_sub_u32 s68,s68,1
s_branch L_sg1_%=
L_se1_%=:
s_barrier
ds_read_b128 a[192:195],v20
ds_read_b64_tr_b16 a[128:129],v22 offset:1024
ds_read_b64_tr_b16 a[130:131],v22 offset:3072
ds_read_b128 a[196:199],v21
ds_read_b64_tr_b16 a[132:133],v22 offset:17408
ds_read_b64_tr_b16 a[134:135],v22 offset:19456
ds_read_b128 a[204:207],v21 offset:2048
ds_read_b128 a[200:203],v20 offset:2048
ds_read_b128 a[208:211],v20 offset:4096
ds_read_b128 a[212:215],v21 offset:4096
ds_read_b128 a[220:223],v21 offset:6144
ds_read_b128 a[216:219],v20 offset:6144
ds_read_b64_tr_b16 a[136:137],v22 offset:1032
ds_read_b64_tr_b16 a[138:139],v22 offset:3080
s_waitcnt lgkmcnt(11)
v_mfma_f32_16x16x32_bf16 v[128:131],a[128:131],a[192:195],0
s_add_u32 m0,s59,0
ds_read_b64_tr_b16 a[140:141],v22 offset:17416
ds_read_b64_tr_b16 a[142:143],v22 offset:19464
s_waitcnt lgkmcnt(10)
v_mfma_f32_16x16x32_bf16 v[128:131],a[132:135],a[196:199],v[128:131]
s_waitcnt lgkmcnt(9)
v_mfma_f32_16x16x32_bf16 v[136:139],a[132:135],a[204:207],0
s_waitcnt lgkmcnt(8)
v_mfma_f32_16x16x32_bf16 v[136:139],a[128:131],a[200:203],v[136:139]
s_waitcnt lgkmcnt(7)
v_mfma_f32_16x16x32_bf16 v[144:147],a[128:131],a[208:211],0
s_waitcnt lgkmcnt(6)
buffer_load_dwordx4 v48,s[28:31],s57 offen lds
v_mfma_f32_16x16x32_bf16 v[144:147],a[132:135],a[212:215],v[144:147]
s_waitcnt lgkmcnt(5)
v_mfma_f32_16x16x32_bf16 v[152:155],a[132:135],a[220:223],0
s_waitcnt lgkmcnt(4)
v_mfma_f32_16x16x32_bf16 v[152:155],a[128:131],a[216:219],v[152:155]
s_waitcnt lgkmcnt(2)
ds_read_b64_tr_b16 a[144:145],v22 offset:1280
ds_read_b64_tr_b16 a[146:147],v22 offset:3328
v_mfma_f32_16x16x32_bf16 v[132:135],a[136:139],a[192:195],0
s_waitcnt lgkmcnt(2)
ds_read_b64_tr_b16 a[148:149],v22 offset:17664
ds_read_b64_tr_b16 a[150:151],v22 offset:19712
v_mfma_f32_16x16x32_bf16 v[132:135],a[140:143],a[196:199],v[132:135]
buffer_load_dwordx4 v49,s[28:31],s57 offen offset:1024 lds
v_mfma_f32_16x16x32_bf16 v[140:143],a[140:143],a[204:207],0
v_mfma_f32_16x16x32_bf16 v[140:143],a[136:139],a[200:203],v[140:143]
v_mfma_f32_16x16x32_bf16 v[148:151],a[136:139],a[208:211],0
v_mfma_f32_16x16x32_bf16 v[148:151],a[140:143],a[212:215],v[148:151]
v_mfma_f32_16x16x32_bf16 v[156:159],a[140:143],a[220:223],0
buffer_load_dwordx4 v50,s[28:31],s57 offen offset:2048 lds
v_mfma_f32_16x16x32_bf16 v[156:159],a[136:139],a[216:219],v[156:159]
s_waitcnt lgkmcnt(2)
ds_read_b64_tr_b16 a[152:153],v22 offset:1288
ds_read_b64_tr_b16 a[154:155],v22 offset:3336
v_mfma_f32_16x16x32_bf16 v[160:163],a[144:147],a[192:195],0
s_waitcnt lgkmcnt(2)
ds_read_b64_tr_b16 a[156:157],v22 offset:17672
ds_read_b64_tr_b16 a[158:159],v22 offset:19720
v_mfma_f32_16x16x32_bf16 v[160:163],a[148:151],a[196:199],v[160:163]
v_mfma_f32_16x16x32_bf16 v[168:171],a[148:151],a[204:207],0
v_mfma_f32_16x16x32_bf16 v[168:171],a[144:147],a[200:203],v[168:171]
buffer_load_dwordx4 v51,s[28:31],s57 offen offset:3072 lds
v_mfma_f32_16x16x32_bf16 v[176:179],a[144:147],a[208:211],0
s_addk_i32 m0,4096
v_mfma_f32_16x16x32_bf16 v[176:179],a[148:151],a[212:215],v[176:179]
v_mfma_f32_16x16x32_bf16 v[184:187],a[148:151],a[220:223],0
v_mfma_f32_16x16x32_bf16 v[184:187],a[144:147],a[216:219],v[184:187]
s_waitcnt lgkmcnt(2)
v_mfma_f32_16x16x32_bf16 v[164:167],a[152:155],a[192:195],0
s_waitcnt lgkmcnt(0)
buffer_load_dwordx4 v52,s[28:31],s57 offen lds
ds_read_b64_tr_b16 a[160:161],v22 offset:1536
ds_read_b64_tr_b16 a[162:163],v22 offset:3584
v_mfma_f32_16x16x32_bf16 v[164:167],a[156:159],a[196:199],v[164:167]
ds_read_b64_tr_b16 a[164:165],v22 offset:17920
ds_read_b64_tr_b16 a[166:167],v22 offset:19968
v_mfma_f32_16x16x32_bf16 v[172:175],a[156:159],a[204:207],0
v_mfma_f32_16x16x32_bf16 v[172:175],a[152:155],a[200:203],v[172:175]
v_mfma_f32_16x16x32_bf16 v[180:183],a[152:155],a[208:211],0
v_mfma_f32_16x16x32_bf16 v[180:183],a[156:159],a[212:215],v[180:183]
buffer_load_dwordx4 v53,s[28:31],s57 offen offset:1024 lds
v_mfma_f32_16x16x32_bf16 v[188:191],a[156:159],a[220:223],0
s_waitcnt vmcnt(14)
v_mfma_f32_16x16x32_bf16 v[188:191],a[152:155],a[216:219],v[188:191]
s_barrier
ds_read_b64_tr_b16 a[168:169],v22 offset:1544
ds_read_b64_tr_b16 a[170:171],v22 offset:3592
s_waitcnt lgkmcnt(4)
v_mfma_f32_16x16x32_bf16 v[192:195],a[160:163],a[192:195],0
s_waitcnt lgkmcnt(2)
ds_read_b64_tr_b16 a[172:173],v22 offset:17928
ds_read_b64_tr_b16 a[174:175],v22 offset:19976
v_mfma_f32_16x16x32_bf16 v[192:195],a[164:167],a[196:199],v[192:195]
buffer_load_dwordx4 v54,s[28:31],s57 offen offset:2048 lds
v_mfma_f32_16x16x32_bf16 v[200:203],a[164:167],a[204:207],0
s_lshl_b32 s44,s41,13
s_add_u32 s44,s44,134217728
s_mov_b64 s[64:65],s[12:13]
v_mfma_f32_16x16x32_bf16 v[200:203],a[160:163],a[200:203],v[200:203]
s_add_u32 s64,s64,s44
s_addc_u32 s65,s65,0
s_and_b32 s65,s65,0xffff
v_mfma_f32_16x16x32_bf16 v[208:211],a[160:163],a[208:211],0
s_movk_i32 s66,8192
s_mov_b32 s67,0x20000
v_mov_b32 v14,4096
v_mfma_f32_16x16x32_bf16 v[208:211],a[164:167],a[212:215],v[208:211]
s_mov_b64 s[72:73],s[14:15]
buffer_load_dwordx4 v55,s[28:31],s57 offen offset:3072 lds
v_mfma_f32_16x16x32_bf16 v[216:219],a[164:167],a[220:223],0
s_add_u32 m0,s58,0
s_and_b32 s73,s73,0xffff
ds_read_b64_tr_b16 a[176:177],v22 offset:1792
v_mfma_f32_16x16x32_bf16 v[216:219],a[160:163],a[216:219],v[216:219]
s_mov_b32 s74,5242880
s_mov_b32 s75,0x20000
ds_read_b64_tr_b16 a[178:179],v22 offset:3840
s_waitcnt lgkmcnt(4)
v_mfma_f32_16x16x32_bf16 v[196:199],a[168:171],a[192:195],0
s_waitcnt lgkmcnt(2)
ds_read_b64_tr_b16 a[180:181],v22 offset:18176
ds_read_b64_tr_b16 a[182:183],v22 offset:20224
v_mfma_f32_16x16x32_bf16 v[196:199],a[172:175],a[196:199],v[196:199]
s_lshl_b32 s83,%1,2
buffer_load_dwordx4 v56,s[32:35],s56 offen lds
v_mfma_f32_16x16x32_bf16 v[204:207],a[172:175],a[204:207],0
s_addk_i32 m0,1024
v_lshlrev_b32 v15,3,v6
v_add_u32 v16,128,v15
v_mfma_f32_16x16x32_bf16 v[204:207],a[168:171],a[200:203],v[204:207]
s_add_u32 s83,s83,s40
ds_read_b64_tr_b16 a[184:185],v22 offset:1800
v_lshrrev_b32 v16,10,v16
v_mfma_f32_16x16x32_bf16 v[212:215],a[168:171],a[208:211],0
s_mulk_i32 s83,5120
ds_read_b64_tr_b16 a[186:187],v22 offset:3848
v_lshl_add_u32 v15,v16,24,v15
v_mfma_f32_16x16x32_bf16 v[212:215],a[172:175],a[212:215],v[212:215]
s_lshl_b32 s43,s41,5
buffer_load_dwordx4 v57,s[32:35],s56 offen lds
v_mfma_f32_16x16x32_bf16 v[220:223],a[172:175],a[220:223],0
s_lshl_b32 s44,s42,1
s_add_u32 s43,s43,s44
)A"
R"A(ds_read_b64_tr_b16 a[188:189],v22 offset:18184
v_mfma_f32_16x16x32_bf16 v[220:223],a[168:171],a[216:219],v[220:223]
s_addk_i32 m0,1024
s_lshl_b32 s44,s43,18
ds_read_b64_tr_b16 a[190:191],v22 offset:20232
s_waitcnt lgkmcnt(6)
v_mfma_f32_16x16x32_bf16 v[224:227],a[176:179],a[192:195],0
s_mov_b64 s[20:21],s[6:7]
s_mov_b32 s22,524288
ds_read_b64_tr_b16 a[128:129],v22
s_waitcnt lgkmcnt(5)
v_mfma_f32_16x16x32_bf16 v[224:227],a[180:183],a[196:199],v[224:227]
s_add_u32 s20,s20,s44
s_addc_u32 s21,s21,0
ds_read_b64_tr_b16 a[130:131],v22 offset:2048
v_mfma_f32_16x16x32_bf16 v[232:235],a[180:183],a[204:207],0
s_and_b32 s21,s21,0xffff
buffer_load_dwordx4 v58,s[32:35],s56 offen lds
v_mfma_f32_16x16x32_bf16 v[232:235],a[176:179],a[200:203],v[232:235]
s_mov_b32 s23,0x20000
s_mov_b64 s[24:25],s[8:9]
ds_read_b64_tr_b16 a[132:133],v22 offset:16384
v_mfma_f32_16x16x32_bf16 v[240:243],a[176:179],a[208:211],0
s_addk_i32 m0,1024
s_mov_b32 s26,524288
ds_read_b64_tr_b16 a[134:135],v22 offset:18432
v_mfma_f32_16x16x32_bf16 v[240:243],a[180:183],a[212:215],v[240:243]
s_add_u32 s24,s24,s44
s_addc_u32 s25,s25,0
ds_read_b64_tr_b16 a[136:137],v22 offset:8
v_mfma_f32_16x16x32_bf16 v[248:251],a[180:183],a[220:223],0
s_and_b32 s25,s25,0xffff
s_mov_b32 s27,0x20000
ds_read_b64_tr_b16 a[138:139],v22 offset:2056
v_mfma_f32_16x16x32_bf16 v[248:251],a[176:179],a[216:219],v[248:251]
s_lshl_b32 s44,s41,2
buffer_load_dwordx4 v59,s[32:35],s56 offen lds
s_waitcnt lgkmcnt(6)
v_mfma_f32_16x16x32_bf16 v[228:231],a[184:187],a[192:195],0
s_add_u32 s44,s44,s70
s_lshl_b32 s44,s44,3
ds_read_b64_tr_b16 a[140:141],v22 offset:16392
v_mfma_f32_16x16x32_bf16 v[228:231],a[188:191],a[196:199],v[228:231]
s_addk_i32 m0,1024
s_add_u32 s44,s44,s71
ds_read_b64_tr_b16 a[142:143],v22 offset:18440
v_mfma_f32_16x16x32_bf16 v[236:239],a[188:191],a[204:207],0
s_lshl_b32 s44,s44,18
s_mov_b64 s[36:37],s[12:13]
ds_read_b64_tr_b16 a[144:145],v22 offset:256
v_mfma_f32_16x16x32_bf16 v[236:239],a[184:187],a[200:203],v[236:239]
s_add_u32 s36,s36,s44
s_addc_u32 s37,s37,0
ds_read_b64_tr_b16 a[146:147],v22 offset:2304
v_mfma_f32_16x16x32_bf16 v[244:247],a[184:187],a[208:211],0
s_and_b32 s37,s37,0xffff
buffer_load_dwordx4 v60,s[32:35],s56 offen lds
v_mfma_f32_16x16x32_bf16 v[244:247],a[188:191],a[212:215],v[244:247]
s_xor_b32 s59,s59,131072
s_mov_b32 s38,262144
ds_read_b64_tr_b16 a[148:149],v22 offset:16640
v_mfma_f32_16x16x32_bf16 v[252:255],a[188:191],a[220:223],0
s_addk_i32 m0,1024
s_mov_b32 s39,0x20000
ds_read_b64_tr_b16 a[150:151],v22 offset:18688
v_mfma_f32_16x16x32_bf16 v[252:255],a[184:187],a[216:219],v[252:255]
s_lshl_b32 s51,s40,12
s_addk_i32 s57,128
ds_read_b64_tr_b16 a[152:153],v22 offset:264
s_waitcnt lgkmcnt(7)
v_mfma_f32_16x16x32_bf16 a[0:3],a[128:131],a[192:195],0
s_add_u32 s52,s51,16384
s_mul_i32 s47,s41,5
ds_read_b64_tr_b16 a[154:155],v22 offset:2312
v_mfma_f32_16x16x32_bf16 a[0:3],a[132:135],a[196:199],a[0:3]
s_add_u32 s47,s47,s42
buffer_load_dwordx4 v61,s[32:35],s56 offen lds
v_mfma_f32_16x16x32_bf16 a[32:35],a[132:135],a[204:207],0
s_and_b32 s47,s47,15
s_mul_i32 s44,s41,5
ds_read_b64_tr_b16 a[156:157],v22 offset:16648
v_mfma_f32_16x16x32_bf16 a[32:35],a[128:131],a[200:203],a[32:35]
s_addk_i32 m0,1024
s_add_u32 s44,s44,s42
ds_read_b64_tr_b16 a[158:159],v22 offset:18696
v_mfma_f32_16x16x32_bf16 a[64:67],a[128:131],a[208:211],0
s_lshl_b32 s47,s44,12
s_and_b32 s47,s47,0x7000
ds_read_b64_tr_b16 a[160:161],v22 offset:512
v_mfma_f32_16x16x32_bf16 a[64:67],a[132:135],a[212:215],a[64:67]
s_lshl_b32 s44,s44,14
s_and_b32 s44,s44,0x20000
ds_read_b64_tr_b16 a[162:163],v22 offset:2560
v_mfma_f32_16x16x32_bf16 a[96:99],a[132:135],a[220:223],0
s_or_b32 s47,s47,s44
buffer_load_dwordx4 v62,s[32:35],s56 offen lds
v_mfma_f32_16x16x32_bf16 a[96:99],a[128:131],a[216:219],a[96:99]
s_mov_b32 s54,0
s_mov_b32 s61,0x3e0293ee
ds_read_b64_tr_b16 a[164:165],v22 offset:16896
v_mfma_f32_16x16x32_bf16 a[4:7],a[136:139],a[192:195],0
s_addk_i32 m0,1024
s_mov_b32 s63,0x5d800000
ds_read_b64_tr_b16 a[166:167],v22 offset:18944
s_waitcnt lgkmcnt(6)
v_mfma_f32_16x16x32_bf16 a[4:7],a[140:143],a[196:199],a[4:7]
s_lshl_b32 s44,s40,2
s_add_u32 s44,s44,s70
ds_read_b64_tr_b16 a[168:169],v22 offset:520
v_mfma_f32_16x16x32_bf16 a[36:39],a[140:143],a[204:207],0
s_mov_b32 s46,0xff800000
ds_read_b64_tr_b16 a[170:171],v22 offset:2568
v_lshlrev_b32 v70,15,v65
v_mfma_f32_16x16x32_bf16 a[36:39],a[136:139],a[200:203],a[36:39]
s_add_u32 s60,s60,32768
buffer_load_dwordx4 v63,s[32:35],s56 offen lds
v_mfma_f32_16x16x32_bf16 a[68:71],a[136:139],a[208:211],0
s_cmp_eq_u32 s44,14
s_cselect_b32 s18,0x7149f2ca,s46
ds_read_b64_tr_b16 a[172:173],v22 offset:16904
v_mfma_f32_16x16x32_bf16 a[68:71],a[140:143],a[212:215],a[68:71]
s_add_u32 m0,s59,0
ds_read_b64_tr_b16 a[174:175],v22 offset:18952
v_lshl_add_u32 v70,v66,6,v70
v_mfma_f32_16x16x32_bf16 a[100:103],a[140:143],a[220:223],0
s_cmp_eq_u32 s44,13
s_cselect_b32 s19,0x7149f2ca,s46
ds_read_b64_tr_b16 a[176:177],v22 offset:768
v_mfma_f32_16x16x32_bf16 a[100:103],a[136:139],a[216:219],a[100:103]
s_cmp_eq_u32 s44,12
s_cselect_b32 s100,0x7149f2ca,s46
ds_read_b64_tr_b16 a[178:179],v22 offset:2816
v_mfma_f32_16x16x32_bf16 a[8:11],a[144:147],a[192:195],0
s_add_u32 s58,s58,32768
buffer_load_dwordx4 v48,s[28:31],s57 offen lds
v_mfma_f32_16x16x32_bf16 a[8:11],a[148:151],a[196:199],a[8:11]
s_cmp_eq_u32 s44,15
s_cselect_b32 s101,0x7149f2ca,s46
ds_read_b64_tr_b16 a[180:181],v22 offset:17152
v_mfma_f32_16x16x32_bf16 a[40:43],a[148:151],a[204:207],0
s_lshl_b32 s44,s40,8
ds_read_b64_tr_b16 a[182:183],v22 offset:19200
v_add_u32 v70,s44,v70
v_mfma_f32_16x16x32_bf16 a[40:43],a[144:147],a[200:203],a[40:43]
s_add_u32 s56,s56,524288
ds_read_b64_tr_b16 a[184:185],v22 offset:776
v_lshl_add_u32 v4,v64,4,v70
v_mfma_f32_16x16x32_bf16 a[72:75],a[144:147],a[208:211],0
s_mul_i32 s81,s70,1310720
ds_read_b64_tr_b16 a[186:187],v22 offset:2824
v_xor_b32 v71,2,v64
v_mfma_f32_16x16x32_bf16 a[72:75],a[148:151],a[212:215],a[72:75]
s_mul_i32 s44,s71,320
buffer_load_dwordx4 v49,s[28:31],s57 offen offset:1024 lds
v_mfma_f32_16x16x32_bf16 a[104:107],a[148:151],a[220:223],0
s_add_u32 s81,s81,s44
ds_read_b64_tr_b16 a[188:189],v22 offset:17160
v_lshl_add_u32 v5,v71,4,v70
v_mfma_f32_16x16x32_bf16 a[104:107],a[144:147],a[216:219],a[104:107]
s_mul_i32 s82,s40,10240
ds_read_b64_tr_b16 a[190:191],v22 offset:19208
v_lshlrev_b32 v72,10,v65
v_mfma_f32_16x16x32_bf16 a[12:15],a[152:155],a[192:195],0
s_cmp_eq_u32 s60,131072
s_cselect_b32 s60,32768,s60
v_lshl_add_u32 v72,v64,6,v72
s_waitcnt lgkmcnt(6)
v_mfma_f32_16x16x32_bf16 a[12:15],a[156:159],a[196:199],a[12:15]
s_mul_i32 s44,s40,40960
v_and_b32 v73,2,v65
v_add_u32 v22,s60,v23
v_mfma_f32_16x16x32_bf16 a[44:47],a[156:159],a[204:207],0
s_xor_b32 s59,s59,131072
buffer_load_dwordx4 v50,s[28:31],s57 offen offset:2048 lds
v_mfma_f32_16x16x32_bf16 a[44:47],a[152:155],a[200:203],a[44:47]
s_lshr_b32 s92,%1,3
v_xor_b32 v20,131072,v20
v_xor_b32 v21,131072,v21
v_mfma_f32_16x16x32_bf16 a[76:79],a[152:155],a[208:211],0
s_lshl_b32 s92,s92,2
v_xor_b32 v73,v73,v66
v_lshl_add_u32 v72,v73,4,v72
v_mfma_f32_16x16x32_bf16 a[76:79],a[156:159],a[212:215],a[76:79]
s_add_u32 s92,s92,s40
v_add_u32 v1,98304,v72
v_lshlrev_b32 v72,10,v66
v_mfma_f32_16x16x32_bf16 a[108:111],a[156:159],a[220:223],0
s_lshl_b32 s92,s92,7
v_lshl_add_u32 v72,v65,6,v72
v_lshl_add_u32 v72,v64,3,v72
v_mfma_f32_16x16x32_bf16 a[108:111],a[152:155],a[216:219],a[108:111]
s_waitcnt vmcnt(11) lgkmcnt(0)
s_add_u32 s93,s92,L_tailseg_%=-L_pc_%=
buffer_load_dwordx4 v51,s[28:31],s57 offen offset:3072 lds
v_mfma_f32_16x16x32_bf16 a[16:19],a[160:163],a[192:195],0
s_barrier
s_addk_i32 m0,4096
ds_read_b128 a[224:227],v20
v_mfma_f32_16x16x32_bf16 a[16:19],a[164:167],a[196:199],a[16:19]
s_add_u32 s60,s60,32768
ds_read_b64_tr_b16 a[128:129],v22 offset:1024
v_add_u32 v72,114688,v72
v_mfma_f32_16x16x32_bf16 a[48:51],a[164:167],a[204:207],0
s_cmp_ge_u32 s58,131072
s_cselect_b32 s62,98304,0
ds_read_b64_tr_b16 a[130:131],v22 offset:3072
v_mfma_f32_16x16x32_bf16 a[48:51],a[160:163],a[200:203],a[48:51]
s_sub_u32 s58,s58,s62
buffer_load_dwordx4 v52,s[28:31],s57 offen lds
v_mfma_f32_16x16x32_bf16 a[80:83],a[160:163],a[208:211],0
ds_read_b128 a[228:231],v21
v_and_b32 v73,1,v66
v_mfma_f32_16x16x32_bf16 a[80:83],a[164:167],a[212:215],a[80:83]
ds_read_b64_tr_b16 a[132:133],v22 offset:17408
v_lshl_add_u32 v2,v73,5,v72
v_mfma_f32_16x16x32_bf16 a[112:115],a[164:167],a[220:223],0
ds_read_b64_tr_b16 a[134:135],v22 offset:19456
v_xor_b32 v73,1,v73
v_mfma_f32_16x16x32_bf16 a[112:115],a[160:163],a[216:219],a[112:115]
ds_read_b128 a[232:235],v20 offset:2048
v_lshl_add_u32 v3,v73,5,v72
v_mfma_f32_16x16x32_bf16 a[20:23],a[168:171],a[192:195],0
buffer_load_dwordx4 v53,s[28:31],s57 offen offset:1024 lds
v_mfma_f32_16x16x32_bf16 a[20:23],a[172:175],a[196:199],a[20:23]
ds_read_b128 a[236:239],v21 offset:2048
v_mov_b32 v73,0x3f803f80
v_mfma_f32_16x16x32_bf16 a[52:55],a[172:175],a[204:207],0
ds_read_b128 a[240:243],v20 offset:4096
v_add_u32 v70,0,v12
v_mfma_f32_16x16x32_bf16 a[52:55],a[168:171],a[200:203],a[52:55]
ds_read_b128 a[244:247],v21 offset:4096
v_mul_u32_u24 v71,3277,v70
v_mfma_f32_16x16x32_bf16 a[84:87],a[168:171],a[208:211],0
buffer_load_dword v13,v14,s[64:67],0 offen sc1
v_mfma_f32_16x16x32_bf16 a[84:87],a[172:175],a[212:215],a[84:87]
ds_read_b128 a[248:251],v20 offset:6144
v_lshrrev_b32 v71,16,v71
v_mfma_f32_16x16x32_bf16 a[116:119],a[172:175],a[220:223],0
ds_read_b128 a[252:255],v21 offset:6144
v_mul_u32_u24 v72,20,v71
v_mfma_f32_16x16x32_bf16 a[116:119],a[168:171],a[216:219],a[116:119]
ds_read_b64_tr_b16 a[136:137],v22 offset:1032
v_sub_u32 v72,v70,v72
v_mfma_f32_16x16x32_bf16 a[24:27],a[176:179],a[192:195],0
buffer_load_dwordx4 v54,s[28:31],s57 offen offset:2048 lds
v_mfma_f32_16x16x32_bf16 a[24:27],a[180:183],a[196:199],a[24:27]
ds_read_b64_tr_b16 a[138:139],v22 offset:3080
v_add_u32 v70,64,v12
v_mfma_f32_16x16x32_bf16 a[56:59],a[180:183],a[204:207],0
ds_read_b64_tr_b16 a[140:141],v22 offset:17416
v_lshlrev_b32 v24,10,v6
v_mfma_f32_16x16x32_bf16 a[56:59],a[176:179],a[200:203],a[56:59]
s_cmp_eq_u32 s60,131072
s_cselect_b32 s60,32768,s60
ds_read_b64_tr_b16 a[142:143],v22 offset:19464
v_mfma_f32_16x16x32_bf16 a[88:91],a[176:179],a[208:211],0
buffer_load_dwordx4 v55,s[28:31],s57 offen offset:3072 lds
v_mfma_f32_16x16x32_bf16 a[88:91],a[180:183],a[212:215],a[88:91]
s_addk_i32 s57,128
ds_read_b64_tr_b16 a[144:145],v22 offset:1280
v_xor_b32 v20,131072,v20
v_mfma_f32_16x16x32_bf16 a[120:123],a[180:183],a[220:223],0
ds_read_b64_tr_b16 a[146:147],v22 offset:3328
v_xor_b32 v21,131072,v21
v_mfma_f32_16x16x32_bf16 a[120:123],a[176:179],a[216:219],a[120:123]
ds_read_b64_tr_b16 a[148:149],v22 offset:17664
v_mfma_f32_16x16x32_bf16 a[28:31],a[184:187],a[192:195],0
ds_read_b64_tr_b16 a[150:151],v22 offset:19712
v_mfma_f32_16x16x32_bf16 a[28:31],a[188:191],a[196:199],a[28:31]
ds_read_b64_tr_b16 a[152:153],v22 offset:1288
ds_read_b64_tr_b16 a[154:155],v22 offset:3336
ds_read_b64_tr_b16 a[156:157],v22 offset:17672
ds_read_b64_tr_b16 a[158:159],v22 offset:19720
v_mfma_f32_16x16x32_bf16 a[60:63],a[188:191],a[204:207],0
v_mfma_f32_16x16x32_bf16 a[60:63],a[184:187],a[200:203],a[60:63]
v_cmp_gt_u32 vcc,2,v64
s_nop 1
v_cndmask_b32 v28,0,v73,vcc
v_mfma_f32_16x16x32_bf16 a[92:95],a[184:187],a[208:211],0
v_and_b32 v73,1,v64
v_mul_u32_u24 v73,0xaaaa,v73
v_mfma_f32_16x16x32_bf16 a[92:95],a[188:191],a[212:215],a[92:95]
v_add_u32 v73,0x4444,v73
v_lshl_or_b32 v27,v73,16,v73
v_mfma_f32_16x16x32_bf16 a[124:127],a[188:191],a[220:223],0
s_add_u32 m0,s58,0
v_mov_b32 v29,v28
v_mov_b32 v30,v28
v_mfma_f32_16x16x32_bf16 a[124:127],a[184:187],a[216:219],a[124:127]
s_add_u32 s58,s58,32768
ds_read_b64_tr_b16 a[160:161],v22 offset:1536
v_mov_b32 v31,v28
s_waitcnt lgkmcnt(6)
v_mfma_f32_16x16x32_bf16 v[128:131],a[128:131],a[224:227],v[128:131]
ds_read_b64_tr_b16 a[162:163],v22 offset:3584
v_bfe_u32 v73,v71,2,1
v_mfma_f32_16x16x32_bf16 v[128:131],a[132:135],a[228:231],v[128:131]
buffer_load_dwordx4 v56,s[32:35],s56 offen lds
v_mfma_f32_16x16x32_bf16 v[136:139],a[132:135],a[236:239],v[136:139]
)A"
R"A(ds_read_b64_tr_b16 a[164:165],v22 offset:17920
v_lshlrev_b32 v73,1,v73
v_mfma_f32_16x16x32_bf16 v[136:139],a[128:131],a[232:235],v[136:139]
s_addk_i32 m0,1024
ds_read_b64_tr_b16 a[166:167],v22 offset:19968
v_xor_b32 v72,v72,v73
v_mfma_f32_16x16x32_bf16 v[144:147],a[128:131],a[240:243],v[144:147]
ds_read_b64_tr_b16 a[168:169],v22 offset:1544
v_mul_u32_u24 v71,1280,v71
v_mfma_f32_16x16x32_bf16 v[144:147],a[132:135],a[244:247],v[144:147]
ds_read_b64_tr_b16 a[170:171],v22 offset:3592
v_lshl_add_u32 v71,v72,4,v71
v_mfma_f32_16x16x32_bf16 v[152:155],a[132:135],a[252:255],v[152:155]
buffer_load_dwordx4 v57,s[32:35],s56 offen lds
v_mfma_f32_16x16x32_bf16 v[152:155],a[128:131],a[248:251],v[152:155]
ds_read_b64_tr_b16 a[172:173],v22 offset:17928
v_add_u32 v71,s44,v71
v_mfma_f32_16x16x32_bf16 v[132:135],a[136:139],a[224:227],v[132:135]
ds_read_b64_tr_b16 a[174:175],v22 offset:19976
v_subrev_u32 v7,0,v71
v_mfma_f32_16x16x32_bf16 v[132:135],a[140:143],a[228:231],v[132:135]
s_addk_i32 m0,1024
v_mul_u32_u24 v71,3277,v70
v_lshrrev_b32 v71,16,v71
v_mfma_f32_16x16x32_bf16 v[140:143],a[140:143],a[236:239],v[140:143]
ds_read_b64_tr_b16 a[176:177],v22 offset:1792
v_mul_u32_u24 v72,20,v71
v_mfma_f32_16x16x32_bf16 v[140:143],a[136:139],a[232:235],v[140:143]
ds_read_b64_tr_b16 a[178:179],v22 offset:3840
v_sub_u32 v72,v70,v72
v_mfma_f32_16x16x32_bf16 v[148:151],a[136:139],a[240:243],v[148:151]
buffer_load_dwordx4 v58,s[32:35],s56 offen lds
v_mfma_f32_16x16x32_bf16 v[148:151],a[140:143],a[244:247],v[148:151]
ds_read_b64_tr_b16 a[180:181],v22 offset:18176
v_bfe_u32 v73,v71,2,1
v_mfma_f32_16x16x32_bf16 v[156:159],a[140:143],a[252:255],v[156:159]
ds_read_b64_tr_b16 a[182:183],v22 offset:20224
v_lshlrev_b32 v73,1,v73
v_mfma_f32_16x16x32_bf16 v[156:159],a[136:139],a[248:251],v[156:159]
s_addk_i32 m0,1024
v_xor_b32 v72,v72,v73
v_mul_u32_u24 v71,1280,v71
v_mfma_f32_16x16x32_bf16 v[160:163],a[144:147],a[224:227],v[160:163]
s_waitcnt lgkmcnt(4)
ds_read_b64_tr_b16 a[184:185],v22 offset:1800
v_lshl_add_u32 v71,v72,4,v71
v_mfma_f32_16x16x32_bf16 v[160:163],a[148:151],a[228:231],v[160:163]
ds_read_b64_tr_b16 a[186:187],v22 offset:3848
v_add_u32 v71,s44,v71
v_mfma_f32_16x16x32_bf16 v[168:171],a[148:151],a[236:239],v[168:171]
buffer_load_dwordx4 v59,s[32:35],s56 offen lds
v_mfma_f32_16x16x32_bf16 v[168:171],a[144:147],a[232:235],v[168:171]
ds_read_b64_tr_b16 a[188:189],v22 offset:18184
v_subrev_u32 v8,1024,v71
v_mfma_f32_16x16x32_bf16 v[176:179],a[144:147],a[240:243],v[176:179]
s_addk_i32 m0,1024
ds_read_b64_tr_b16 a[190:191],v22 offset:20232
v_add_u32 v70,128,v12
v_mfma_f32_16x16x32_bf16 v[176:179],a[148:151],a[244:247],v[176:179]
v_mul_u32_u24 v71,3277,v70
v_lshrrev_b32 v71,16,v71
v_mfma_f32_16x16x32_bf16 v[184:187],a[148:151],a[252:255],v[184:187]
v_mul_u32_u24 v72,20,v71
v_sub_u32 v72,v70,v72
v_mfma_f32_16x16x32_bf16 v[184:187],a[144:147],a[248:251],v[184:187]
buffer_load_dwordx4 v60,s[32:35],s56 offen lds
v_mfma_f32_16x16x32_bf16 v[164:167],a[152:155],a[224:227],v[164:167]
ds_read_b64_tr_b16 a[128:129],v22
v_bfe_u32 v73,v71,2,1
v_mfma_f32_16x16x32_bf16 v[164:167],a[156:159],a[228:231],v[164:167]
ds_read_b64_tr_b16 a[130:131],v22 offset:2048
v_lshlrev_b32 v73,1,v73
v_mfma_f32_16x16x32_bf16 v[172:175],a[156:159],a[236:239],v[172:175]
s_addk_i32 m0,1024
ds_read_b64_tr_b16 a[132:133],v22 offset:16384
v_xor_b32 v72,v72,v73
v_mfma_f32_16x16x32_bf16 v[172:175],a[152:155],a[232:235],v[172:175]
ds_read_b64_tr_b16 a[134:135],v22 offset:18432
v_mul_u32_u24 v71,1280,v71
v_mfma_f32_16x16x32_bf16 v[180:183],a[152:155],a[240:243],v[180:183]
v_lshl_add_u32 v71,v72,4,v71
v_add_u32 v71,s44,v71
v_mfma_f32_16x16x32_bf16 v[180:183],a[156:159],a[244:247],v[180:183]
buffer_load_dwordx4 v61,s[32:35],s56 offen lds
v_mfma_f32_16x16x32_bf16 v[188:191],a[156:159],a[252:255],v[188:191]
ds_read_b64_tr_b16 a[136:137],v22 offset:8
v_subrev_u32 v9,2048,v71
v_mfma_f32_16x16x32_bf16 v[188:191],a[152:155],a[248:251],v[188:191]
ds_read_b64_tr_b16 a[138:139],v22 offset:2056
v_add_u32 v70,192,v12
v_mfma_f32_16x16x32_bf16 v[192:195],a[160:163],a[224:227],v[192:195]
s_addk_i32 m0,1024
ds_read_b64_tr_b16 a[140:141],v22 offset:16392
v_mul_u32_u24 v71,3277,v70
v_mfma_f32_16x16x32_bf16 v[192:195],a[164:167],a[228:231],v[192:195]
ds_read_b64_tr_b16 a[142:143],v22 offset:18440
v_lshrrev_b32 v71,16,v71
v_mfma_f32_16x16x32_bf16 v[200:203],a[164:167],a[236:239],v[200:203]
v_mul_u32_u24 v72,20,v71
v_sub_u32 v72,v70,v72
v_mfma_f32_16x16x32_bf16 v[200:203],a[160:163],a[232:235],v[200:203]
buffer_load_dwordx4 v62,s[32:35],s56 offen lds
v_mfma_f32_16x16x32_bf16 v[208:211],a[160:163],a[240:243],v[208:211]
ds_read_b64_tr_b16 a[144:145],v22 offset:256
v_bfe_u32 v73,v71,2,1
v_mfma_f32_16x16x32_bf16 v[208:211],a[164:167],a[244:247],v[208:211]
s_addk_i32 m0,1024
ds_read_b64_tr_b16 a[146:147],v22 offset:2304
v_lshlrev_b32 v73,1,v73
v_mfma_f32_16x16x32_bf16 v[216:219],a[164:167],a[252:255],v[216:219]
ds_read_b64_tr_b16 a[148:149],v22 offset:16640
v_xor_b32 v72,v72,v73
v_mfma_f32_16x16x32_bf16 v[216:219],a[160:163],a[248:251],v[216:219]
ds_read_b64_tr_b16 a[150:151],v22 offset:18688
v_mul_u32_u24 v71,1280,v71
v_mfma_f32_16x16x32_bf16 v[196:199],a[168:171],a[224:227],v[196:199]
buffer_load_dwordx4 v63,s[32:35],s56 offen lds
v_mfma_f32_16x16x32_bf16 v[196:199],a[172:175],a[228:231],v[196:199]
s_add_u32 s56,s56,524288
v_lshl_add_u32 v71,v72,4,v71
v_add_u32 v71,s44,v71
v_mfma_f32_16x16x32_bf16 v[204:207],a[172:175],a[236:239],v[204:207]
v_subrev_u32 v10,3072,v71
v_add_u32 v70,256,v12
v_mfma_f32_16x16x32_bf16 v[204:207],a[168:171],a[232:235],v[204:207]
s_add_u32 m0,s59,0
ds_read_b64_tr_b16 a[152:153],v22 offset:264
v_mul_u32_u24 v71,3277,v70
v_mfma_f32_16x16x32_bf16 v[212:215],a[168:171],a[240:243],v[212:215]
s_xor_b32 s59,s59,131072
ds_read_b64_tr_b16 a[154:155],v22 offset:2312
v_lshrrev_b32 v71,16,v71
v_mfma_f32_16x16x32_bf16 v[212:215],a[172:175],a[244:247],v[212:215]
ds_read_b64_tr_b16 a[156:157],v22 offset:16648
v_mul_u32_u24 v72,20,v71
v_mfma_f32_16x16x32_bf16 v[220:223],a[172:175],a[252:255],v[220:223]
buffer_load_dwordx4 v48,s[28:31],s57 offen lds
v_mfma_f32_16x16x32_bf16 v[220:223],a[168:171],a[248:251],v[220:223]
s_waitcnt lgkmcnt(3)
ds_read_b64_tr_b16 a[158:159],v22 offset:18696
v_sub_u32 v72,v70,v72
v_mfma_f32_16x16x32_bf16 v[224:227],a[176:179],a[224:227],v[224:227]
v_bfe_u32 v73,v71,2,1
v_lshlrev_b32 v73,1,v73
v_mfma_f32_16x16x32_bf16 v[224:227],a[180:183],a[228:231],v[224:227]
ds_read_b64_tr_b16 a[160:161],v22 offset:512
v_xor_b32 v72,v72,v73
v_mfma_f32_16x16x32_bf16 v[232:235],a[180:183],a[236:239],v[232:235]
ds_read_b64_tr_b16 a[162:163],v22 offset:2560
v_mul_u32_u24 v71,1280,v71
v_mfma_f32_16x16x32_bf16 v[232:235],a[176:179],a[232:235],v[232:235]
ds_read_b64_tr_b16 a[164:165],v22 offset:16896
v_lshl_add_u32 v71,v72,4,v71
v_mfma_f32_16x16x32_bf16 v[240:243],a[176:179],a[240:243],v[240:243]
buffer_load_dwordx4 v49,s[28:31],s57 offen offset:1024 lds
v_mfma_f32_16x16x32_bf16 v[240:243],a[180:183],a[244:247],v[240:243]
ds_read_b64_tr_b16 a[166:167],v22 offset:18944
v_add_u32 v71,s44,v71
v_mfma_f32_16x16x32_bf16 v[248:251],a[180:183],a[252:255],v[248:251]
ds_read_b64_tr_b16 a[168:169],v22 offset:520
v_subrev_u32 v11,0,v71
v_mfma_f32_16x16x32_bf16 v[248:251],a[176:179],a[248:251],v[248:251]
s_cmp_ge_u32 s58,131072
s_cselect_b32 s62,98304,0
ds_read_b64_tr_b16 a[170:171],v22 offset:2568
v_mfma_f32_16x16x32_bf16 v[228:231],a[184:187],a[224:227],v[228:231]
s_sub_u32 s58,s58,s62
ds_read_b64_tr_b16 a[172:173],v22 offset:16904
v_mfma_f32_16x16x32_bf16 v[228:231],a[188:191],a[228:231],v[228:231]
buffer_load_dwordx4 v50,s[28:31],s57 offen offset:2048 lds
v_mfma_f32_16x16x32_bf16 v[236:239],a[188:191],a[236:239],v[236:239]
ds_read_b64_tr_b16 a[174:175],v22 offset:18952
v_mfma_f32_16x16x32_bf16 v[236:239],a[184:187],a[232:235],v[236:239]
ds_read_b64_tr_b16 a[176:177],v22 offset:768
v_mfma_f32_16x16x32_bf16 v[244:247],a[184:187],a[240:243],v[244:247]
ds_read_b64_tr_b16 a[178:179],v22 offset:2816
v_mfma_f32_16x16x32_bf16 v[244:247],a[188:191],a[244:247],v[244:247]
ds_read_b64_tr_b16 a[180:181],v22 offset:17152
v_mfma_f32_16x16x32_bf16 v[252:255],a[188:191],a[252:255],v[252:255]
ds_read_b64_tr_b16 a[182:183],v22 offset:19200
v_mfma_f32_16x16x32_bf16 v[252:255],a[184:187],a[248:251],v[252:255]
buffer_load_dwordx4 v51,s[28:31],s57 offen offset:3072 lds
v_mfma_f32_16x16x32_bf16 a[0:3],a[128:131],a[224:227],a[0:3]
ds_read_b64_tr_b16 a[184:185],v22 offset:776
v_mfma_f32_16x16x32_bf16 a[0:3],a[132:135],a[228:231],a[0:3]
ds_read_b64_tr_b16 a[186:187],v22 offset:2824
v_mfma_f32_16x16x32_bf16 a[32:35],a[132:135],a[236:239],a[32:35]
s_addk_i32 m0,4096
ds_read_b64_tr_b16 a[188:189],v22 offset:17160
v_mfma_f32_16x16x32_bf16 a[32:35],a[128:131],a[232:235],a[32:35]
ds_read_b64_tr_b16 a[190:191],v22 offset:19208
v_add_u32 v22,s60,v23
v_mfma_f32_16x16x32_bf16 a[64:67],a[128:131],a[240:243],a[64:67]
v_mfma_f32_16x16x32_bf16 a[64:67],a[132:135],a[244:247],a[64:67]
buffer_load_dwordx4 v52,s[28:31],s57 offen lds
v_mfma_f32_16x16x32_bf16 a[96:99],a[132:135],a[252:255],a[96:99]
v_mfma_f32_16x16x32_bf16 a[96:99],a[128:131],a[248:251],a[96:99]
v_mfma_f32_16x16x32_bf16 a[4:7],a[136:139],a[224:227],a[4:7]
v_mfma_f32_16x16x32_bf16 a[4:7],a[140:143],a[228:231],a[4:7]
v_mfma_f32_16x16x32_bf16 a[36:39],a[140:143],a[236:239],a[36:39]
buffer_load_dwordx4 v53,s[28:31],s57 offen offset:1024 lds
v_mfma_f32_16x16x32_bf16 a[36:39],a[136:139],a[232:235],a[36:39]
v_mfma_f32_16x16x32_bf16 a[68:71],a[136:139],a[240:243],a[68:71]
v_mfma_f32_16x16x32_bf16 a[68:71],a[140:143],a[244:247],a[68:71]
v_mfma_f32_16x16x32_bf16 a[100:103],a[140:143],a[252:255],a[100:103]
v_mfma_f32_16x16x32_bf16 a[100:103],a[136:139],a[248:251],a[100:103]
v_mfma_f32_16x16x32_bf16 a[8:11],a[144:147],a[224:227],a[8:11]
buffer_load_dwordx4 v54,s[28:31],s57 offen offset:2048 lds
v_mfma_f32_16x16x32_bf16 a[8:11],a[148:151],a[228:231],a[8:11]
v_mfma_f32_16x16x32_bf16 a[40:43],a[148:151],a[236:239],a[40:43]
v_mfma_f32_16x16x32_bf16 a[40:43],a[144:147],a[232:235],a[40:43]
v_mfma_f32_16x16x32_bf16 a[72:75],a[144:147],a[240:243],a[72:75]
v_mfma_f32_16x16x32_bf16 a[72:75],a[148:151],a[244:247],a[72:75]
v_mfma_f32_16x16x32_bf16 a[104:107],a[148:151],a[252:255],a[104:107]
buffer_load_dwordx4 v55,s[28:31],s57 offen offset:3072 lds
v_mfma_f32_16x16x32_bf16 a[104:107],a[144:147],a[248:251],a[104:107]
s_addk_i32 s57,128
s_waitcnt lgkmcnt(0)
v_mfma_f32_16x16x32_bf16 a[12:15],a[152:155],a[224:227],a[12:15]
v_mfma_f32_16x16x32_bf16 a[12:15],a[156:159],a[228:231],a[12:15]
v_mfma_f32_16x16x32_bf16 a[44:47],a[156:159],a[236:239],a[44:47]
v_mfma_f32_16x16x32_bf16 a[44:47],a[152:155],a[232:235],a[44:47]
v_mfma_f32_16x16x32_bf16 a[76:79],a[152:155],a[240:243],a[76:79]
v_mfma_f32_16x16x32_bf16 a[76:79],a[156:159],a[244:247],a[76:79]
v_mfma_f32_16x16x32_bf16 a[108:111],a[156:159],a[252:255],a[108:111]
v_mfma_f32_16x16x32_bf16 a[108:111],a[152:155],a[248:251],a[108:111]
s_waitcnt vmcnt(16)
v_mfma_f32_16x16x32_bf16 a[16:19],a[160:163],a[224:227],a[16:19]
s_barrier
ds_read_b128 a[192:195],v20
v_mfma_f32_16x16x32_bf16 a[16:19],a[164:167],a[228:231],a[16:19]
ds_read_b128 a[196:199],v21
v_mfma_f32_16x16x32_bf16 a[48:51],a[164:167],a[236:239],a[48:51]
ds_read_b128 a[200:203],v20 offset:2048
v_mfma_f32_16x16x32_bf16 a[48:51],a[160:163],a[232:235],a[48:51]
ds_read_b128 a[204:207],v21 offset:2048
v_mfma_f32_16x16x32_bf16 a[80:83],a[160:163],a[240:243],a[80:83]
ds_read_b128 a[208:211],v20 offset:4096
v_mfma_f32_16x16x32_bf16 a[80:83],a[164:167],a[244:247],a[80:83]
ds_read_b128 a[212:215],v21 offset:4096
v_mfma_f32_16x16x32_bf16 a[112:115],a[164:167],a[252:255],a[112:115]
ds_read_b128 a[216:219],v20 offset:6144
v_mfma_f32_16x16x32_bf16 a[112:115],a[160:163],a[248:251],a[112:115]
ds_read_b128 a[220:223],v21 offset:6144
v_mfma_f32_16x16x32_bf16 a[20:23],a[168:171],a[224:227],a[20:23]
ds_read_b64_tr_b16 a[128:129],v22 offset:1024
v_mfma_f32_16x16x32_bf16 a[20:23],a[172:175],a[228:231],a[20:23]
ds_read_b64_tr_b16 a[130:131],v22 offset:3072
v_mfma_f32_16x16x32_bf16 a[52:55],a[172:175],a[236:239],a[52:55]
)A"
R"A(ds_read_b64_tr_b16 a[132:133],v22 offset:17408
v_mfma_f32_16x16x32_bf16 a[52:55],a[168:171],a[232:235],a[52:55]
ds_read_b64_tr_b16 a[134:135],v22 offset:19456
v_mfma_f32_16x16x32_bf16 a[84:87],a[168:171],a[240:243],a[84:87]
ds_read_b64_tr_b16 a[136:137],v22 offset:1032
v_mfma_f32_16x16x32_bf16 a[84:87],a[172:175],a[244:247],a[84:87]
ds_read_b64_tr_b16 a[138:139],v22 offset:3080
v_mfma_f32_16x16x32_bf16 a[116:119],a[172:175],a[252:255],a[116:119]
ds_read_b64_tr_b16 a[140:141],v22 offset:17416
v_mfma_f32_16x16x32_bf16 a[116:119],a[168:171],a[248:251],a[116:119]
ds_read_b64_tr_b16 a[142:143],v22 offset:19464
v_mfma_f32_16x16x32_bf16 a[24:27],a[176:179],a[224:227],a[24:27]
ds_read_b64_tr_b16 a[144:145],v22 offset:1280
v_mfma_f32_16x16x32_bf16 a[24:27],a[180:183],a[228:231],a[24:27]
ds_read_b64_tr_b16 a[146:147],v22 offset:3328
v_mfma_f32_16x16x32_bf16 a[56:59],a[180:183],a[236:239],a[56:59]
ds_read_b64_tr_b16 a[148:149],v22 offset:17664
v_mfma_f32_16x16x32_bf16 a[56:59],a[176:179],a[232:235],a[56:59]
ds_read_b64_tr_b16 a[150:151],v22 offset:19712
v_mfma_f32_16x16x32_bf16 a[88:91],a[176:179],a[240:243],a[88:91]
ds_read_b64_tr_b16 a[152:153],v22 offset:1288
v_mfma_f32_16x16x32_bf16 a[88:91],a[180:183],a[244:247],a[88:91]
ds_read_b64_tr_b16 a[154:155],v22 offset:3336
v_mfma_f32_16x16x32_bf16 a[120:123],a[180:183],a[252:255],a[120:123]
ds_read_b64_tr_b16 a[156:157],v22 offset:17672
v_mfma_f32_16x16x32_bf16 a[120:123],a[176:179],a[248:251],a[120:123]
ds_read_b64_tr_b16 a[158:159],v22 offset:19720
v_mfma_f32_16x16x32_bf16 a[28:31],a[184:187],a[224:227],a[28:31]
v_mfma_f32_16x16x32_bf16 a[28:31],a[188:191],a[228:231],a[28:31]
v_mfma_f32_16x16x32_bf16 a[60:63],a[188:191],a[236:239],a[60:63]
v_mfma_f32_16x16x32_bf16 a[60:63],a[184:187],a[232:235],a[60:63]
v_mfma_f32_16x16x32_bf16 a[92:95],a[184:187],a[240:243],a[92:95]
v_mfma_f32_16x16x32_bf16 a[92:95],a[188:191],a[244:247],a[92:95]
v_mfma_f32_16x16x32_bf16 a[124:127],a[188:191],a[252:255],a[124:127]
v_mfma_f32_16x16x32_bf16 a[124:127],a[184:187],a[248:251],a[124:127]
s_mov_b32 s53,3
.p2align 5
L_qp_%=:
s_waitcnt lgkmcnt(1)
v_mfma_f32_16x16x32_bf16 v[128:131],a[128:131],a[192:195],v[128:131]
s_add_u32 m0,s58,0
s_mov_b32 s86,s87
ds_read_b64_tr_b16 a[160:161],v22 offset:1536
v_mfma_f32_16x16x32_bf16 v[128:131],a[132:135],a[196:199],v[128:131]
s_add_u32 s60,s60,32768
buffer_load_dwordx4 v56,s[32:35],s56 offen lds
v_mfma_f32_16x16x32_bf16 v[136:139],a[132:135],a[204:207],v[136:139]
s_cmp_eq_u32 s60,131072
s_cselect_b32 s60,32768,s60
ds_read_b64_tr_b16 a[162:163],v22 offset:3584
v_mfma_f32_16x16x32_bf16 v[136:139],a[128:131],a[200:203],v[136:139]
s_load_dword s86,s[84:85],0x0 glc
s_addk_i32 m0,1024
ds_read_b64_tr_b16 a[164:165],v22 offset:17920
v_mfma_f32_16x16x32_bf16 v[144:147],a[128:131],a[208:211],v[144:147]
s_add_u32 s58,s58,32768
ds_read_b64_tr_b16 a[166:167],v22 offset:19968
v_xor_b32 v20,131072,v20
v_mfma_f32_16x16x32_bf16 v[144:147],a[132:135],a[212:215],v[144:147]
s_mov_b32 s90,L_att0x_%=-L_pc_%=+128
ds_read_b64_tr_b16 a[168:169],v22 offset:1544
v_xor_b32 v21,131072,v21
v_mfma_f32_16x16x32_bf16 v[152:155],a[132:135],a[220:223],v[152:155]
s_sub_u32 s53,s53,1
buffer_load_dwordx4 v57,s[32:35],s56 offen lds
v_mfma_f32_16x16x32_bf16 v[152:155],a[128:131],a[216:219],v[152:155]
s_cmp_ge_u32 s58,131072
s_cselect_b32 s62,98304,0
ds_read_b64_tr_b16 a[170:171],v22 offset:3592
v_mfma_f32_16x16x32_bf16 v[132:135],a[136:139],a[192:195],v[132:135]
s_sub_u32 s58,s58,s62
ds_read_b64_tr_b16 a[172:173],v22 offset:17928
v_mfma_f32_16x16x32_bf16 v[132:135],a[140:143],a[196:199],v[132:135]
s_addk_i32 m0,1024
ds_read_b64_tr_b16 a[174:175],v22 offset:19976
v_mfma_f32_16x16x32_bf16 v[140:143],a[140:143],a[204:207],v[140:143]
ds_read_b64_tr_b16 a[176:177],v22 offset:1792
v_mfma_f32_16x16x32_bf16 v[140:143],a[136:139],a[200:203],v[140:143]
ds_read_b64_tr_b16 a[178:179],v22 offset:3840
v_mfma_f32_16x16x32_bf16 v[148:151],a[136:139],a[208:211],v[148:151]
buffer_load_dwordx4 v58,s[32:35],s56 offen lds
v_mfma_f32_16x16x32_bf16 v[148:151],a[140:143],a[212:215],v[148:151]
ds_read_b64_tr_b16 a[180:181],v22 offset:18176
v_mfma_f32_16x16x32_bf16 v[156:159],a[140:143],a[220:223],v[156:159]
s_cmp_eq_u32 s86,s87
s_addc_u32 s79,s79,0
s_addk_i32 m0,1024
v_mfma_f32_16x16x32_bf16 v[156:159],a[136:139],a[216:219],v[156:159]
ds_read_b64_tr_b16 a[182:183],v22 offset:20224
v_mfma_f32_16x16x32_bf16 v[160:163],a[144:147],a[192:195],v[160:163]
ds_read_b64_tr_b16 a[184:185],v22 offset:1800
v_mfma_f32_16x16x32_bf16 v[160:163],a[148:151],a[196:199],v[160:163]
s_cmp_eq_u32 s86,s87
s_addc_u32 s79,s79,0
ds_read_b64_tr_b16 a[186:187],v22 offset:3848
v_mfma_f32_16x16x32_bf16 v[168:171],a[148:151],a[204:207],v[168:171]
buffer_load_dwordx4 v59,s[32:35],s56 offen lds
v_mfma_f32_16x16x32_bf16 v[168:171],a[144:147],a[200:203],v[168:171]
ds_read_b64_tr_b16 a[188:189],v22 offset:18184
v_mfma_f32_16x16x32_bf16 v[176:179],a[144:147],a[208:211],v[176:179]
s_cmp_eq_u32 s86,s87
s_addc_u32 s79,s79,0
s_addk_i32 m0,1024
v_mfma_f32_16x16x32_bf16 v[176:179],a[148:151],a[212:215],v[176:179]
ds_read_b64_tr_b16 a[190:191],v22 offset:20232
v_mfma_f32_16x16x32_bf16 v[184:187],a[148:151],a[220:223],v[184:187]
ds_read_b64_tr_b16 a[128:129],v22
v_mfma_f32_16x16x32_bf16 v[184:187],a[144:147],a[216:219],v[184:187]
s_cmp_eq_u32 s86,s87
s_addc_u32 s79,s79,0
ds_read_b64_tr_b16 a[130:131],v22 offset:2048
v_mfma_f32_16x16x32_bf16 v[164:167],a[152:155],a[192:195],v[164:167]
s_waitcnt lgkmcnt(5)
buffer_load_dwordx4 v60,s[32:35],s56 offen lds
v_mfma_f32_16x16x32_bf16 v[164:167],a[156:159],a[196:199],v[164:167]
s_addk_i32 m0,1024
ds_read_b64_tr_b16 a[132:133],v22 offset:16384
v_mfma_f32_16x16x32_bf16 v[172:175],a[156:159],a[204:207],v[172:175]
s_cmp_eq_u32 s86,s87
s_addc_u32 s79,s79,0
ds_read_b64_tr_b16 a[134:135],v22 offset:18432
v_mfma_f32_16x16x32_bf16 v[172:175],a[152:155],a[200:203],v[172:175]
ds_read_b64_tr_b16 a[136:137],v22 offset:8
v_mfma_f32_16x16x32_bf16 v[180:183],a[152:155],a[208:211],v[180:183]
buffer_load_dwordx4 v61,s[32:35],s56 offen lds
v_mfma_f32_16x16x32_bf16 v[180:183],a[156:159],a[212:215],v[180:183]
s_cmp_eq_u32 s86,s87
s_addc_u32 s79,s79,0
ds_read_b64_tr_b16 a[138:139],v22 offset:2056
v_mfma_f32_16x16x32_bf16 v[188:191],a[156:159],a[220:223],v[188:191]
ds_read_b64_tr_b16 a[140:141],v22 offset:16392
v_mfma_f32_16x16x32_bf16 v[188:191],a[152:155],a[216:219],v[188:191]
s_addk_i32 m0,1024
ds_read_b64_tr_b16 a[142:143],v22 offset:18440
v_mfma_f32_16x16x32_bf16 v[192:195],a[160:163],a[192:195],v[192:195]
s_cmp_eq_u32 s86,s87
s_addc_u32 s79,s79,0
ds_read_b64_tr_b16 a[144:145],v22 offset:256
v_mfma_f32_16x16x32_bf16 v[192:195],a[164:167],a[196:199],v[192:195]
ds_read_b64_tr_b16 a[146:147],v22 offset:2304
v_mfma_f32_16x16x32_bf16 v[200:203],a[164:167],a[204:207],v[200:203]
buffer_load_dwordx4 v62,s[32:35],s56 offen lds
v_mfma_f32_16x16x32_bf16 v[200:203],a[160:163],a[200:203],v[200:203]
s_cmp_eq_u32 s86,s87
s_addc_u32 s79,s79,0
ds_read_b64_tr_b16 a[148:149],v22 offset:16640
v_mfma_f32_16x16x32_bf16 v[208:211],a[160:163],a[208:211],v[208:211]
s_addk_i32 m0,1024
ds_read_b64_tr_b16 a[150:151],v22 offset:18688
v_mfma_f32_16x16x32_bf16 v[208:211],a[164:167],a[212:215],v[208:211]
ds_read_b64_tr_b16 a[152:153],v22 offset:264
v_mfma_f32_16x16x32_bf16 v[216:219],a[164:167],a[220:223],v[216:219]
s_cmp_eq_u32 s86,s87
s_addc_u32 s79,s79,0
ds_read_b64_tr_b16 a[154:155],v22 offset:2312
v_mfma_f32_16x16x32_bf16 v[216:219],a[160:163],a[216:219],v[216:219]
buffer_load_dwordx4 v63,s[32:35],s56 offen lds
v_mfma_f32_16x16x32_bf16 v[196:199],a[168:171],a[192:195],v[196:199]
s_add_u32 s56,s56,524288
ds_read_b64_tr_b16 a[156:157],v22 offset:16648
v_mfma_f32_16x16x32_bf16 v[196:199],a[172:175],a[196:199],v[196:199]
s_cmp_eq_u32 s86,s87
s_addc_u32 s79,s79,0
ds_read_b64_tr_b16 a[158:159],v22 offset:18696
v_mfma_f32_16x16x32_bf16 v[204:207],a[172:175],a[204:207],v[204:207]
s_add_u32 m0,s59,0
s_xor_b32 s59,s59,131072
ds_read_b64_tr_b16 a[160:161],v22 offset:512
v_mfma_f32_16x16x32_bf16 v[204:207],a[168:171],a[200:203],v[204:207]
ds_read_b64_tr_b16 a[162:163],v22 offset:2560
v_mfma_f32_16x16x32_bf16 v[212:215],a[168:171],a[208:211],v[212:215]
s_cmp_eq_u32 s86,s87
s_addc_u32 s79,s79,0
ds_read_b64_tr_b16 a[164:165],v22 offset:16896
v_mfma_f32_16x16x32_bf16 v[212:215],a[172:175],a[212:215],v[212:215]
buffer_load_dwordx4 v48,s[28:31],s57 offen lds
v_mfma_f32_16x16x32_bf16 v[220:223],a[172:175],a[220:223],v[220:223]
ds_read_b64_tr_b16 a[166:167],v22 offset:18944
v_mfma_f32_16x16x32_bf16 v[220:223],a[168:171],a[216:219],v[220:223]
s_cmp_eq_u32 s86,s87
s_addc_u32 s79,s79,0
ds_read_b64_tr_b16 a[168:169],v22 offset:520
v_mfma_f32_16x16x32_bf16 v[224:227],a[176:179],a[192:195],v[224:227]
ds_read_b64_tr_b16 a[170:171],v22 offset:2568
v_mfma_f32_16x16x32_bf16 v[224:227],a[180:183],a[196:199],v[224:227]
ds_read_b64_tr_b16 a[172:173],v22 offset:16904
v_mfma_f32_16x16x32_bf16 v[232:235],a[180:183],a[204:207],v[232:235]
s_cmp_eq_u32 s86,s87
s_addc_u32 s79,s79,0
ds_read_b64_tr_b16 a[174:175],v22 offset:18952
v_mfma_f32_16x16x32_bf16 v[232:235],a[176:179],a[200:203],v[232:235]
buffer_load_dwordx4 v49,s[28:31],s57 offen offset:1024 lds
v_mfma_f32_16x16x32_bf16 v[240:243],a[176:179],a[208:211],v[240:243]
v_mfma_f32_16x16x32_bf16 v[240:243],a[180:183],a[212:215],v[240:243]
s_cmp_eq_u32 s86,s87
s_addc_u32 s79,s79,0
v_mfma_f32_16x16x32_bf16 v[248:251],a[180:183],a[220:223],v[248:251]
ds_read_b64_tr_b16 a[180:181],v22 offset:17152
v_mfma_f32_16x16x32_bf16 v[248:251],a[176:179],a[216:219],v[248:251]
s_waitcnt lgkmcnt(4)
ds_read_b64_tr_b16 a[176:177],v22 offset:768
v_mfma_f32_16x16x32_bf16 v[228:231],a[184:187],a[192:195],v[228:231]
s_cmp_eq_u32 s86,s87
s_addc_u32 s79,s79,0
ds_read_b64_tr_b16 a[178:179],v22 offset:2816
v_mfma_f32_16x16x32_bf16 v[228:231],a[188:191],a[196:199],v[228:231]
buffer_load_dwordx4 v50,s[28:31],s57 offen offset:2048 lds
v_mfma_f32_16x16x32_bf16 v[236:239],a[188:191],a[204:207],v[236:239]
ds_read_b64_tr_b16 a[182:183],v22 offset:19200
v_mfma_f32_16x16x32_bf16 v[236:239],a[184:187],a[200:203],v[236:239]
s_cmp_eq_u32 s86,s87
s_addc_u32 s79,s79,0
v_mfma_f32_16x16x32_bf16 v[244:247],a[184:187],a[208:211],v[244:247]
v_mfma_f32_16x16x32_bf16 v[244:247],a[188:191],a[212:215],v[244:247]
buffer_load_dwordx4 v51,s[28:31],s57 offen offset:3072 lds
v_mfma_f32_16x16x32_bf16 v[252:255],a[188:191],a[220:223],v[252:255]
ds_read_b64_tr_b16 a[188:189],v22 offset:17160
v_mfma_f32_16x16x32_bf16 v[252:255],a[184:187],a[216:219],v[252:255]
ds_read_b64_tr_b16 a[184:185],v22 offset:776
v_mfma_f32_16x16x32_bf16 a[0:3],a[128:131],a[192:195],a[0:3]
s_addk_i32 m0,4096
ds_read_b64_tr_b16 a[186:187],v22 offset:2824
v_mfma_f32_16x16x32_bf16 a[0:3],a[132:135],a[196:199],a[0:3]
ds_read_b64_tr_b16 a[190:191],v22 offset:19208
v_add_u32 v22,s60,v23
v_mfma_f32_16x16x32_bf16 a[32:35],a[132:135],a[204:207],a[32:35]
s_add_u32 s60,s60,32768
s_cmp_eq_u32 s60,131072
s_cselect_b32 s60,32768,s60
v_mfma_f32_16x16x32_bf16 a[32:35],a[128:131],a[200:203],a[32:35]
buffer_load_dwordx4 v52,s[28:31],s57 offen lds
v_mfma_f32_16x16x32_bf16 a[64:67],a[128:131],a[208:211],a[64:67]
v_mfma_f32_16x16x32_bf16 a[64:67],a[132:135],a[212:215],a[64:67]
v_mfma_f32_16x16x32_bf16 a[96:99],a[132:135],a[220:223],a[96:99]
v_mfma_f32_16x16x32_bf16 a[96:99],a[128:131],a[216:219],a[96:99]
v_mfma_f32_16x16x32_bf16 a[4:7],a[136:139],a[192:195],a[4:7]
buffer_load_dwordx4 v53,s[28:31],s57 offen offset:1024 lds
v_mfma_f32_16x16x32_bf16 a[4:7],a[140:143],a[196:199],a[4:7]
v_mfma_f32_16x16x32_bf16 a[36:39],a[140:143],a[204:207],a[36:39]
v_mfma_f32_16x16x32_bf16 a[36:39],a[136:139],a[200:203],a[36:39]
v_mfma_f32_16x16x32_bf16 a[68:71],a[136:139],a[208:211],a[68:71]
v_mfma_f32_16x16x32_bf16 a[68:71],a[140:143],a[212:215],a[68:71]
v_mfma_f32_16x16x32_bf16 a[100:103],a[140:143],a[220:223],a[100:103]
buffer_load_dwordx4 v54,s[28:31],s57 offen offset:2048 lds
v_mfma_f32_16x16x32_bf16 a[100:103],a[136:139],a[216:219],a[100:103]
v_mfma_f32_16x16x32_bf16 a[8:11],a[144:147],a[192:195],a[8:11]
v_mfma_f32_16x16x32_bf16 a[8:11],a[148:151],a[196:199],a[8:11]
)A"
R"A(v_mfma_f32_16x16x32_bf16 a[40:43],a[148:151],a[204:207],a[40:43]
v_mfma_f32_16x16x32_bf16 a[40:43],a[144:147],a[200:203],a[40:43]
buffer_load_dwordx4 v55,s[28:31],s57 offen offset:3072 lds
v_mfma_f32_16x16x32_bf16 a[72:75],a[144:147],a[208:211],a[72:75]
s_addk_i32 s57,128
v_mfma_f32_16x16x32_bf16 a[72:75],a[148:151],a[212:215],a[72:75]
v_mfma_f32_16x16x32_bf16 a[104:107],a[148:151],a[220:223],a[104:107]
v_mfma_f32_16x16x32_bf16 a[104:107],a[144:147],a[216:219],a[104:107]
v_mfma_f32_16x16x32_bf16 a[12:15],a[152:155],a[192:195],a[12:15]
v_mfma_f32_16x16x32_bf16 a[12:15],a[156:159],a[196:199],a[12:15]
v_mfma_f32_16x16x32_bf16 a[44:47],a[156:159],a[204:207],a[44:47]
buffer_load_dword v16,v15,s[72:75],s83 offen
v_mfma_f32_16x16x32_bf16 a[44:47],a[152:155],a[200:203],a[44:47]
s_addk_i32 s83,896
v_mfma_f32_16x16x32_bf16 a[76:79],a[152:155],a[208:211],a[76:79]
v_mfma_f32_16x16x32_bf16 a[76:79],a[156:159],a[212:215],a[76:79]
v_mfma_f32_16x16x32_bf16 a[108:111],a[156:159],a[220:223],a[108:111]
v_mfma_f32_16x16x32_bf16 a[108:111],a[152:155],a[216:219],a[108:111]
s_waitcnt vmcnt(17) lgkmcnt(0)
v_mfma_f32_16x16x32_bf16 a[16:19],a[160:163],a[192:195],a[16:19]
s_barrier
ds_read_b128 a[224:227],v20
v_mfma_f32_16x16x32_bf16 a[16:19],a[164:167],a[196:199],a[16:19]
ds_read_b64_tr_b16 a[128:129],v22 offset:1024
v_mfma_f32_16x16x32_bf16 a[48:51],a[164:167],a[204:207],a[48:51]
ds_read_b64_tr_b16 a[130:131],v22 offset:3072
v_mfma_f32_16x16x32_bf16 a[48:51],a[160:163],a[200:203],a[48:51]
ds_read_b128 a[228:231],v21
v_mfma_f32_16x16x32_bf16 a[80:83],a[160:163],a[208:211],a[80:83]
ds_read_b64_tr_b16 a[132:133],v22 offset:17408
v_mfma_f32_16x16x32_bf16 a[80:83],a[164:167],a[212:215],a[80:83]
ds_read_b64_tr_b16 a[134:135],v22 offset:19456
v_mfma_f32_16x16x32_bf16 a[112:115],a[164:167],a[220:223],a[112:115]
ds_read_b128 a[232:235],v20 offset:2048
v_mfma_f32_16x16x32_bf16 a[112:115],a[160:163],a[216:219],a[112:115]
ds_read_b128 a[236:239],v21 offset:2048
v_mfma_f32_16x16x32_bf16 a[20:23],a[168:171],a[192:195],a[20:23]
ds_read_b128 a[240:243],v20 offset:4096
v_mfma_f32_16x16x32_bf16 a[20:23],a[172:175],a[196:199],a[20:23]
ds_read_b128 a[244:247],v21 offset:4096
v_mfma_f32_16x16x32_bf16 a[52:55],a[172:175],a[204:207],a[52:55]
ds_read_b128 a[248:251],v20 offset:6144
v_xor_b32 v20,131072,v20
v_mfma_f32_16x16x32_bf16 a[52:55],a[168:171],a[200:203],a[52:55]
ds_read_b128 a[252:255],v21 offset:6144
v_xor_b32 v21,131072,v21
v_mfma_f32_16x16x32_bf16 a[84:87],a[168:171],a[208:211],a[84:87]
ds_read_b64_tr_b16 a[136:137],v22 offset:1032
v_mfma_f32_16x16x32_bf16 a[84:87],a[172:175],a[212:215],a[84:87]
ds_read_b64_tr_b16 a[138:139],v22 offset:3080
v_mfma_f32_16x16x32_bf16 a[116:119],a[172:175],a[220:223],a[116:119]
ds_read_b64_tr_b16 a[140:141],v22 offset:17416
v_mfma_f32_16x16x32_bf16 a[116:119],a[168:171],a[216:219],a[116:119]
ds_read_b64_tr_b16 a[142:143],v22 offset:19464
v_mfma_f32_16x16x32_bf16 a[24:27],a[176:179],a[192:195],a[24:27]
ds_read_b64_tr_b16 a[144:145],v22 offset:1280
v_mfma_f32_16x16x32_bf16 a[24:27],a[180:183],a[196:199],a[24:27]
ds_read_b64_tr_b16 a[146:147],v22 offset:3328
v_mfma_f32_16x16x32_bf16 a[56:59],a[180:183],a[204:207],a[56:59]
ds_read_b64_tr_b16 a[148:149],v22 offset:17664
v_mfma_f32_16x16x32_bf16 a[56:59],a[176:179],a[200:203],a[56:59]
ds_read_b64_tr_b16 a[150:151],v22 offset:19712
v_mfma_f32_16x16x32_bf16 a[88:91],a[176:179],a[208:211],a[88:91]
ds_read_b64_tr_b16 a[152:153],v22 offset:1288
v_mfma_f32_16x16x32_bf16 a[88:91],a[180:183],a[212:215],a[88:91]
ds_read_b64_tr_b16 a[154:155],v22 offset:3336
v_mfma_f32_16x16x32_bf16 a[120:123],a[180:183],a[220:223],a[120:123]
ds_read_b64_tr_b16 a[156:157],v22 offset:17672
v_mfma_f32_16x16x32_bf16 a[120:123],a[176:179],a[216:219],a[120:123]
ds_read_b64_tr_b16 a[158:159],v22 offset:19720
v_mfma_f32_16x16x32_bf16 a[28:31],a[184:187],a[192:195],a[28:31]
ds_read_b64_tr_b16 a[160:161],v22 offset:1536
v_mfma_f32_16x16x32_bf16 a[28:31],a[188:191],a[196:199],a[28:31]
ds_read_b64_tr_b16 a[162:163],v22 offset:3584
v_mfma_f32_16x16x32_bf16 a[60:63],a[188:191],a[204:207],a[60:63]
ds_read_b64_tr_b16 a[164:165],v22 offset:17920
v_mfma_f32_16x16x32_bf16 a[60:63],a[184:187],a[200:203],a[60:63]
ds_read_b64_tr_b16 a[166:167],v22 offset:19968
v_mfma_f32_16x16x32_bf16 a[92:95],a[184:187],a[208:211],a[92:95]
ds_read_b64_tr_b16 a[168:169],v22 offset:1544
v_mfma_f32_16x16x32_bf16 a[92:95],a[188:191],a[212:215],a[92:95]
ds_read_b64_tr_b16 a[170:171],v22 offset:3592
v_mfma_f32_16x16x32_bf16 a[124:127],a[188:191],a[220:223],a[124:127]
s_add_u32 m0,s58,0
s_add_u32 s58,s58,32768
ds_read_b64_tr_b16 a[172:173],v22 offset:17928
v_mfma_f32_16x16x32_bf16 a[124:127],a[184:187],a[216:219],a[124:127]
s_cmp_ge_u32 s58,131072
s_cselect_b32 s62,98304,0
ds_read_b64_tr_b16 a[174:175],v22 offset:19976
s_waitcnt lgkmcnt(8)
v_mfma_f32_16x16x32_bf16 v[128:131],a[128:131],a[224:227],v[128:131]
s_mov_b32 s86,s87
s_sub_u32 s58,s58,s62
ds_read_b64_tr_b16 a[176:177],v22 offset:1792
v_mfma_f32_16x16x32_bf16 v[128:131],a[132:135],a[228:231],v[128:131]
buffer_load_dwordx4 v56,s[32:35],s56 offen lds
v_mfma_f32_16x16x32_bf16 v[136:139],a[132:135],a[236:239],v[136:139]
ds_read_b64_tr_b16 a[178:179],v22 offset:3840
v_mfma_f32_16x16x32_bf16 v[136:139],a[128:131],a[232:235],v[136:139]
s_load_dword s86,s[84:85],0x2000 glc
s_addk_i32 m0,1024
ds_read_b64_tr_b16 a[180:181],v22 offset:18176
v_mfma_f32_16x16x32_bf16 v[144:147],a[128:131],a[240:243],v[144:147]
s_addk_i32 s84,256
ds_read_b64_tr_b16 a[182:183],v22 offset:20224
v_mfma_f32_16x16x32_bf16 v[144:147],a[132:135],a[244:247],v[144:147]
ds_read_b64_tr_b16 a[184:185],v22 offset:1800
v_mfma_f32_16x16x32_bf16 v[152:155],a[132:135],a[252:255],v[152:155]
buffer_load_dwordx4 v57,s[32:35],s56 offen lds
v_mfma_f32_16x16x32_bf16 v[152:155],a[128:131],a[248:251],v[152:155]
ds_read_b64_tr_b16 a[186:187],v22 offset:3848
v_mfma_f32_16x16x32_bf16 v[132:135],a[136:139],a[224:227],v[132:135]
ds_read_b64_tr_b16 a[188:189],v22 offset:18184
v_mfma_f32_16x16x32_bf16 v[132:135],a[140:143],a[228:231],v[132:135]
s_addk_i32 m0,1024
ds_read_b64_tr_b16 a[190:191],v22 offset:20232
v_mfma_f32_16x16x32_bf16 v[140:143],a[140:143],a[236:239],v[140:143]
ds_read_b64_tr_b16 a[128:129],v22
v_mfma_f32_16x16x32_bf16 v[140:143],a[136:139],a[232:235],v[140:143]
ds_read_b64_tr_b16 a[130:131],v22 offset:2048
v_mfma_f32_16x16x32_bf16 v[148:151],a[136:139],a[240:243],v[148:151]
buffer_load_dwordx4 v58,s[32:35],s56 offen lds
v_mfma_f32_16x16x32_bf16 v[148:151],a[140:143],a[244:247],v[148:151]
ds_read_b64_tr_b16 a[132:133],v22 offset:16384
v_mfma_f32_16x16x32_bf16 v[156:159],a[140:143],a[252:255],v[156:159]
s_cmp_eq_u32 s86,s87
s_subb_u32 s79,s79,0
s_addk_i32 m0,1024
v_mfma_f32_16x16x32_bf16 v[156:159],a[136:139],a[248:251],v[156:159]
ds_read_b64_tr_b16 a[134:135],v22 offset:18432
v_mfma_f32_16x16x32_bf16 v[160:163],a[144:147],a[224:227],v[160:163]
ds_read_b64_tr_b16 a[136:137],v22 offset:8
v_mfma_f32_16x16x32_bf16 v[160:163],a[148:151],a[228:231],v[160:163]
s_cmp_eq_u32 s86,s87
s_subb_u32 s79,s79,0
ds_read_b64_tr_b16 a[138:139],v22 offset:2056
v_mfma_f32_16x16x32_bf16 v[168:171],a[148:151],a[236:239],v[168:171]
buffer_load_dwordx4 v59,s[32:35],s56 offen lds
v_mfma_f32_16x16x32_bf16 v[168:171],a[144:147],a[232:235],v[168:171]
ds_read_b64_tr_b16 a[140:141],v22 offset:16392
v_mfma_f32_16x16x32_bf16 v[176:179],a[144:147],a[240:243],v[176:179]
s_cmp_eq_u32 s86,s87
s_subb_u32 s79,s79,0
s_addk_i32 m0,1024
v_mfma_f32_16x16x32_bf16 v[176:179],a[148:151],a[244:247],v[176:179]
ds_read_b64_tr_b16 a[142:143],v22 offset:18440
v_mfma_f32_16x16x32_bf16 v[184:187],a[148:151],a[252:255],v[184:187]
ds_read_b64_tr_b16 a[148:149],v22 offset:16640
v_mfma_f32_16x16x32_bf16 v[184:187],a[144:147],a[248:251],v[184:187]
s_cmp_eq_u32 s86,s87
s_subb_u32 s79,s79,0
ds_read_b64_tr_b16 a[144:145],v22 offset:256
v_mfma_f32_16x16x32_bf16 v[164:167],a[152:155],a[224:227],v[164:167]
buffer_load_dwordx4 v60,s[32:35],s56 offen lds
v_mfma_f32_16x16x32_bf16 v[164:167],a[156:159],a[228:231],v[164:167]
s_addk_i32 m0,1024
ds_read_b64_tr_b16 a[146:147],v22 offset:2304
v_mfma_f32_16x16x32_bf16 v[172:175],a[156:159],a[236:239],v[172:175]
s_cmp_eq_u32 s86,s87
s_subb_u32 s79,s79,0
ds_read_b64_tr_b16 a[150:151],v22 offset:18688
v_mfma_f32_16x16x32_bf16 v[172:175],a[152:155],a[232:235],v[172:175]
v_mfma_f32_16x16x32_bf16 v[180:183],a[152:155],a[240:243],v[180:183]
buffer_load_dwordx4 v61,s[32:35],s56 offen lds
v_mfma_f32_16x16x32_bf16 v[180:183],a[156:159],a[244:247],v[180:183]
s_cmp_eq_u32 s86,s87
s_subb_u32 s79,s79,0
v_mfma_f32_16x16x32_bf16 v[188:191],a[156:159],a[252:255],v[188:191]
ds_read_b64_tr_b16 a[156:157],v22 offset:16648
v_mfma_f32_16x16x32_bf16 v[188:191],a[152:155],a[248:251],v[188:191]
s_addk_i32 m0,1024
ds_read_b64_tr_b16 a[152:153],v22 offset:264
s_waitcnt lgkmcnt(4)
v_mfma_f32_16x16x32_bf16 v[192:195],a[160:163],a[224:227],v[192:195]
s_cmp_eq_u32 s86,s87
s_subb_u32 s79,s79,0
ds_read_b64_tr_b16 a[154:155],v22 offset:2312
v_mfma_f32_16x16x32_bf16 v[192:195],a[164:167],a[228:231],v[192:195]
ds_read_b64_tr_b16 a[158:159],v22 offset:18696
v_mfma_f32_16x16x32_bf16 v[200:203],a[164:167],a[236:239],v[200:203]
buffer_load_dwordx4 v62,s[32:35],s56 offen lds
v_mfma_f32_16x16x32_bf16 v[200:203],a[160:163],a[232:235],v[200:203]
s_cmp_eq_u32 s86,s87
s_subb_u32 s79,s79,0
v_mfma_f32_16x16x32_bf16 v[208:211],a[160:163],a[240:243],v[208:211]
s_addk_i32 m0,1024
v_mfma_f32_16x16x32_bf16 v[208:211],a[164:167],a[244:247],v[208:211]
v_mfma_f32_16x16x32_bf16 v[216:219],a[164:167],a[252:255],v[216:219]
s_cmp_eq_u32 s86,s87
s_subb_u32 s79,s79,0
ds_read_b64_tr_b16 a[164:165],v22 offset:16896
v_mfma_f32_16x16x32_bf16 v[216:219],a[160:163],a[248:251],v[216:219]
buffer_load_dwordx4 v63,s[32:35],s56 offen lds
v_mfma_f32_16x16x32_bf16 v[196:199],a[168:171],a[224:227],v[196:199]
s_add_u32 s56,s56,524288
ds_read_b64_tr_b16 a[160:161],v22 offset:512
v_mfma_f32_16x16x32_bf16 v[196:199],a[172:175],a[228:231],v[196:199]
s_cmp_eq_u32 s86,s87
s_subb_u32 s79,s79,0
ds_read_b64_tr_b16 a[162:163],v22 offset:2560
v_mfma_f32_16x16x32_bf16 v[204:207],a[172:175],a[236:239],v[204:207]
s_add_u32 m0,s59,0
s_xor_b32 s59,s59,131072
ds_read_b64_tr_b16 a[166:167],v22 offset:18944
v_mfma_f32_16x16x32_bf16 v[204:207],a[168:171],a[232:235],v[204:207]
v_mfma_f32_16x16x32_bf16 v[212:215],a[168:171],a[240:243],v[212:215]
s_cmp_eq_u32 s86,s87
s_subb_u32 s79,s79,0
v_mfma_f32_16x16x32_bf16 v[212:215],a[172:175],a[244:247],v[212:215]
buffer_load_dwordx4 v48,s[28:31],s57 offen lds
v_mfma_f32_16x16x32_bf16 v[220:223],a[172:175],a[252:255],v[220:223]
ds_read_b64_tr_b16 a[172:173],v22 offset:16904
v_mfma_f32_16x16x32_bf16 v[220:223],a[168:171],a[248:251],v[220:223]
s_cmp_eq_u32 s86,s87
s_subb_u32 s79,s79,0
ds_read_b64_tr_b16 a[168:169],v22 offset:520
v_mfma_f32_16x16x32_bf16 v[224:227],a[176:179],a[224:227],v[224:227]
ds_read_b64_tr_b16 a[170:171],v22 offset:2568
v_mfma_f32_16x16x32_bf16 v[224:227],a[180:183],a[228:231],v[224:227]
ds_read_b64_tr_b16 a[174:175],v22 offset:18952
v_mfma_f32_16x16x32_bf16 v[232:235],a[180:183],a[236:239],v[232:235]
s_cmp_eq_u32 s86,s87
s_subb_u32 s79,s79,0
v_mfma_f32_16x16x32_bf16 v[232:235],a[176:179],a[232:235],v[232:235]
buffer_load_dwordx4 v49,s[28:31],s57 offen offset:1024 lds
v_mfma_f32_16x16x32_bf16 v[240:243],a[176:179],a[240:243],v[240:243]
v_mfma_f32_16x16x32_bf16 v[240:243],a[180:183],a[244:247],v[240:243]
s_cmp_eq_u32 s86,s87
s_subb_u32 s79,s79,0
v_mfma_f32_16x16x32_bf16 v[248:251],a[180:183],a[252:255],v[248:251]
ds_read_b64_tr_b16 a[180:181],v22 offset:17152
v_mfma_f32_16x16x32_bf16 v[248:251],a[176:179],a[248:251],v[248:251]
ds_read_b64_tr_b16 a[176:177],v22 offset:768
v_mfma_f32_16x16x32_bf16 v[228:231],a[184:187],a[224:227],v[228:231]
s_cmp_eq_u32 s86,s87
s_subb_u32 s79,s79,0
ds_read_b64_tr_b16 a[178:179],v22 offset:2816
v_mfma_f32_16x16x32_bf16 v[228:231],a[188:191],a[228:231],v[228:231]
buffer_load_dwordx4 v50,s[28:31],s57 offen offset:2048 lds
v_mfma_f32_16x16x32_bf16 v[236:239],a[188:191],a[236:239],v[236:239]
ds_read_b64_tr_b16 a[182:183],v22 offset:19200
)A"
R"A(v_mfma_f32_16x16x32_bf16 v[236:239],a[184:187],a[232:235],v[236:239]
s_cmp_eq_u32 s86,s87
s_subb_u32 s79,s79,0
v_mfma_f32_16x16x32_bf16 v[244:247],a[184:187],a[240:243],v[244:247]
v_mfma_f32_16x16x32_bf16 v[244:247],a[188:191],a[244:247],v[244:247]
buffer_load_dwordx4 v51,s[28:31],s57 offen offset:3072 lds
v_mfma_f32_16x16x32_bf16 v[252:255],a[188:191],a[252:255],v[252:255]
ds_read_b64_tr_b16 a[188:189],v22 offset:17160
v_mfma_f32_16x16x32_bf16 v[252:255],a[184:187],a[248:251],v[252:255]
ds_read_b64_tr_b16 a[184:185],v22 offset:776
v_mfma_f32_16x16x32_bf16 a[0:3],a[128:131],a[224:227],a[0:3]
s_addk_i32 m0,4096
ds_read_b64_tr_b16 a[186:187],v22 offset:2824
v_mfma_f32_16x16x32_bf16 a[0:3],a[132:135],a[228:231],a[0:3]
ds_read_b64_tr_b16 a[190:191],v22 offset:19208
v_add_u32 v22,s60,v23
v_mfma_f32_16x16x32_bf16 a[32:35],a[132:135],a[236:239],a[32:35]
v_mfma_f32_16x16x32_bf16 a[32:35],a[128:131],a[232:235],a[32:35]
buffer_load_dwordx4 v52,s[28:31],s57 offen lds
v_mfma_f32_16x16x32_bf16 a[64:67],a[128:131],a[240:243],a[64:67]
v_mfma_f32_16x16x32_bf16 a[64:67],a[132:135],a[244:247],a[64:67]
v_mfma_f32_16x16x32_bf16 a[96:99],a[132:135],a[252:255],a[96:99]
v_mfma_f32_16x16x32_bf16 a[96:99],a[128:131],a[248:251],a[96:99]
v_mfma_f32_16x16x32_bf16 a[4:7],a[136:139],a[224:227],a[4:7]
buffer_load_dwordx4 v53,s[28:31],s57 offen offset:1024 lds
v_mfma_f32_16x16x32_bf16 a[4:7],a[140:143],a[228:231],a[4:7]
v_mfma_f32_16x16x32_bf16 a[36:39],a[140:143],a[236:239],a[36:39]
v_mfma_f32_16x16x32_bf16 a[36:39],a[136:139],a[232:235],a[36:39]
v_mfma_f32_16x16x32_bf16 a[68:71],a[136:139],a[240:243],a[68:71]
v_mfma_f32_16x16x32_bf16 a[68:71],a[140:143],a[244:247],a[68:71]
v_mfma_f32_16x16x32_bf16 a[100:103],a[140:143],a[252:255],a[100:103]
buffer_load_dwordx4 v54,s[28:31],s57 offen offset:2048 lds
v_mfma_f32_16x16x32_bf16 a[100:103],a[136:139],a[248:251],a[100:103]
s_waitcnt lgkmcnt(0)
v_mfma_f32_16x16x32_bf16 a[8:11],a[144:147],a[224:227],a[8:11]
v_mfma_f32_16x16x32_bf16 a[8:11],a[148:151],a[228:231],a[8:11]
v_mfma_f32_16x16x32_bf16 a[40:43],a[148:151],a[236:239],a[40:43]
v_mfma_f32_16x16x32_bf16 a[40:43],a[144:147],a[232:235],a[40:43]
buffer_load_dwordx4 v55,s[28:31],s57 offen offset:3072 lds
v_mfma_f32_16x16x32_bf16 a[72:75],a[144:147],a[240:243],a[72:75]
s_addk_i32 s57,128
v_mfma_f32_16x16x32_bf16 a[72:75],a[148:151],a[244:247],a[72:75]
v_mfma_f32_16x16x32_bf16 a[104:107],a[148:151],a[252:255],a[104:107]
v_mfma_f32_16x16x32_bf16 a[104:107],a[144:147],a[248:251],a[104:107]
v_mfma_f32_16x16x32_bf16 a[12:15],a[152:155],a[224:227],a[12:15]
v_mfma_f32_16x16x32_bf16 a[12:15],a[156:159],a[228:231],a[12:15]
v_mfma_f32_16x16x32_bf16 a[44:47],a[156:159],a[236:239],a[44:47]
buffer_load_dword v16,v15,s[72:75],s83 offen
v_mfma_f32_16x16x32_bf16 a[44:47],a[152:155],a[232:235],a[44:47]
s_addk_i32 s83,896
v_mfma_f32_16x16x32_bf16 a[76:79],a[152:155],a[240:243],a[76:79]
v_mfma_f32_16x16x32_bf16 a[76:79],a[156:159],a[244:247],a[76:79]
v_mfma_f32_16x16x32_bf16 a[108:111],a[156:159],a[252:255],a[108:111]
v_mfma_f32_16x16x32_bf16 a[108:111],a[152:155],a[248:251],a[108:111]
s_waitcnt vmcnt(17) lgkmcnt(0)
v_mfma_f32_16x16x32_bf16 a[16:19],a[160:163],a[224:227],a[16:19]
s_barrier
ds_read_b128 a[192:195],v20
v_mfma_f32_16x16x32_bf16 a[16:19],a[164:167],a[228:231],a[16:19]
ds_read_b128 a[196:199],v21
v_mfma_f32_16x16x32_bf16 a[48:51],a[164:167],a[236:239],a[48:51]
ds_read_b128 a[200:203],v20 offset:2048
v_mfma_f32_16x16x32_bf16 a[48:51],a[160:163],a[232:235],a[48:51]
ds_read_b128 a[204:207],v21 offset:2048
v_mfma_f32_16x16x32_bf16 a[80:83],a[160:163],a[240:243],a[80:83]
ds_read_b128 a[208:211],v20 offset:4096
v_mfma_f32_16x16x32_bf16 a[80:83],a[164:167],a[244:247],a[80:83]
ds_read_b128 a[212:215],v21 offset:4096
v_mfma_f32_16x16x32_bf16 a[112:115],a[164:167],a[252:255],a[112:115]
buffer_load_dword v26,v24,s[88:91],s93 offen
v_mfma_f32_16x16x32_bf16 a[112:115],a[160:163],a[248:251],a[112:115]
ds_read_b128 a[216:219],v20 offset:6144
v_mfma_f32_16x16x32_bf16 a[20:23],a[168:171],a[224:227],a[20:23]
ds_read_b128 a[220:223],v21 offset:6144
v_mfma_f32_16x16x32_bf16 a[20:23],a[172:175],a[228:231],a[20:23]
ds_read_b64_tr_b16 a[128:129],v22 offset:1024
v_mfma_f32_16x16x32_bf16 a[52:55],a[172:175],a[236:239],a[52:55]
ds_read_b64_tr_b16 a[130:131],v22 offset:3072
v_mfma_f32_16x16x32_bf16 a[52:55],a[168:171],a[232:235],a[52:55]
ds_read_b64_tr_b16 a[132:133],v22 offset:17408
v_mfma_f32_16x16x32_bf16 a[84:87],a[168:171],a[240:243],a[84:87]
ds_read_b64_tr_b16 a[134:135],v22 offset:19456
v_mfma_f32_16x16x32_bf16 a[84:87],a[172:175],a[244:247],a[84:87]
ds_read_b64_tr_b16 a[136:137],v22 offset:1032
v_mfma_f32_16x16x32_bf16 a[116:119],a[172:175],a[252:255],a[116:119]
ds_read_b64_tr_b16 a[138:139],v22 offset:3080
v_mfma_f32_16x16x32_bf16 a[116:119],a[168:171],a[248:251],a[116:119]
ds_read_b64_tr_b16 a[140:141],v22 offset:17416
v_mfma_f32_16x16x32_bf16 a[24:27],a[176:179],a[224:227],a[24:27]
ds_read_b64_tr_b16 a[142:143],v22 offset:19464
v_mfma_f32_16x16x32_bf16 a[24:27],a[180:183],a[228:231],a[24:27]
ds_read_b64_tr_b16 a[144:145],v22 offset:1280
v_mfma_f32_16x16x32_bf16 a[56:59],a[180:183],a[236:239],a[56:59]
ds_read_b64_tr_b16 a[146:147],v22 offset:3328
v_mfma_f32_16x16x32_bf16 a[56:59],a[176:179],a[232:235],a[56:59]
ds_read_b64_tr_b16 a[148:149],v22 offset:17664
v_mfma_f32_16x16x32_bf16 a[88:91],a[176:179],a[240:243],a[88:91]
ds_read_b64_tr_b16 a[150:151],v22 offset:19712
v_mfma_f32_16x16x32_bf16 a[88:91],a[180:183],a[244:247],a[88:91]
ds_read_b64_tr_b16 a[152:153],v22 offset:1288
v_mfma_f32_16x16x32_bf16 a[120:123],a[180:183],a[252:255],a[120:123]
ds_read_b64_tr_b16 a[154:155],v22 offset:3336
v_mfma_f32_16x16x32_bf16 a[120:123],a[176:179],a[248:251],a[120:123]
ds_read_b64_tr_b16 a[156:157],v22 offset:17672
v_mfma_f32_16x16x32_bf16 a[28:31],a[184:187],a[224:227],a[28:31]
ds_read_b64_tr_b16 a[158:159],v22 offset:19720
v_mfma_f32_16x16x32_bf16 a[28:31],a[188:191],a[228:231],a[28:31]
v_mfma_f32_16x16x32_bf16 a[60:63],a[188:191],a[236:239],a[60:63]
v_mfma_f32_16x16x32_bf16 a[60:63],a[184:187],a[232:235],a[60:63]
v_mfma_f32_16x16x32_bf16 a[92:95],a[184:187],a[240:243],a[92:95]
v_mfma_f32_16x16x32_bf16 a[92:95],a[188:191],a[244:247],a[92:95]
v_mfma_f32_16x16x32_bf16 a[124:127],a[188:191],a[252:255],a[124:127]
s_cmp_lg_u32 s53,0
v_mfma_f32_16x16x32_bf16 a[124:127],a[184:187],a[248:251],a[124:127]
s_cbranch_scc1 L_qp_%=
L_qpx_%=:
L_tailseg_%=:
s_waitcnt lgkmcnt(1)
v_mfma_f32_16x16x32_bf16 v[128:131],a[128:131],a[192:195],v[128:131]
s_add_u32 m0,98304,s51
ds_read_b64_tr_b16 a[160:161],v22 offset:1536
v_xor_b32 v20,131072,v20
v_mfma_f32_16x16x32_bf16 v[128:131],a[132:135],a[196:199],v[128:131]
s_addk_i32 s54,16384
buffer_load_dwordx4 v4,s[20:23],s47 offen nt lds
v_mfma_f32_16x16x32_bf16 v[136:139],a[132:135],a[204:207],v[136:139]
s_and_b32 s46,s54,0xfffc0000
ds_read_b64_tr_b16 a[162:163],v22 offset:3584
v_xor_b32 v21,131072,v21
v_mfma_f32_16x16x32_bf16 v[136:139],a[128:131],a[200:203],v[136:139]
s_addk_i32 s54,16384
ds_read_b64_tr_b16 a[164:165],v22 offset:17920
v_and_b32 v18,48,v6
v_mfma_f32_16x16x32_bf16 v[144:147],a[128:131],a[208:211],v[144:147]
s_mov_b32 s48,-98304
ds_read_b64_tr_b16 a[166:167],v22 offset:19968
v_lshrrev_b32 v19,4,v6
v_mfma_f32_16x16x32_bf16 v[144:147],a[132:135],a[212:215],v[144:147]
s_movk_i32 s60,142
ds_read_b64_tr_b16 a[168:169],v22 offset:1544
v_lshlrev_b32 v18,2,v18
v_mfma_f32_16x16x32_bf16 v[152:155],a[132:135],a[220:223],v[152:155]
s_mul_i32 s83,s41,5
ds_read_b64_tr_b16 a[170:171],v22 offset:3592
v_and_b32 v19,48,v19
v_mfma_f32_16x16x32_bf16 v[152:155],a[128:131],a[216:219],v[152:155]
s_lshl_b32 s43,s40,8
ds_read_b64_tr_b16 a[172:173],v22 offset:17928
v_add_u32 v18,v18,v19
v_mfma_f32_16x16x32_bf16 v[132:135],a[136:139],a[192:195],v[132:135]
s_add_u32 s83,s83,s42
ds_read_b64_tr_b16 a[174:175],v22 offset:19976
v_add_u32 v18,98304,v18
v_mfma_f32_16x16x32_bf16 v[132:135],a[140:143],a[196:199],v[132:135]
s_cmp_gt_i32 s79,0
s_cselect_b32 s86,8192,0
ds_read_b64_tr_b16 a[176:177],v22 offset:1792
v_mfma_f32_16x16x32_bf16 v[140:143],a[140:143],a[204:207],v[140:143]
s_lshr_b32 s79,%1,3
s_mul_i32 s79,s79,7
ds_read_b64_tr_b16 a[178:179],v22 offset:3840
v_mfma_f32_16x16x32_bf16 v[140:143],a[136:139],a[200:203],v[140:143]
s_lshr_b32 s79,s79,5
s_sub_u32 s60,s60,s79
ds_read_b64_tr_b16 a[180:181],v22 offset:18176
v_mfma_f32_16x16x32_bf16 v[148:151],a[136:139],a[208:211],v[148:151]
s_lshl_b32 s94,s60,2
s_sub_u32 s94,s94,3
ds_read_b64_tr_b16 a[182:183],v22 offset:20224
v_mfma_f32_16x16x32_bf16 v[148:151],a[140:143],a[212:215],v[148:151]
s_add_u32 s83,s83,15
buffer_load_dwordx4 v4,s[20:23],s47 offen offset:1024 nt lds
v_mfma_f32_16x16x32_bf16 v[156:159],a[140:143],a[220:223],v[156:159]
s_lshl_b32 s44,s40,4
ds_read_b64_tr_b16 a[184:185],v22 offset:1800
v_subrev_u32 v25,s43,v4
v_mfma_f32_16x16x32_bf16 v[156:159],a[136:139],a[216:219],v[156:159]
s_lshl_b32 s43,s83,14
v_readfirstlane_b32 s68,v13
s_nop 1
s_add_u32 s68,s68,1
v_mfma_f32_16x16x32_bf16 v[160:163],a[144:147],a[192:195],v[160:163]
s_and_b32 s44,s44,32
s_lshl_b32 s83,s83,12
ds_read_b64_tr_b16 a[186:187],v22 offset:3848
v_mfma_f32_16x16x32_bf16 v[160:163],a[148:151],a[196:199],v[160:163]
s_lshl_b32 s80,s40,10
ds_read_b64_tr_b16 a[188:189],v22 offset:18184
v_xor_b32 v25,s44,v25
v_mfma_f32_16x16x32_bf16 v[168:171],a[148:151],a[204:207],v[168:171]
s_and_b32 s43,s43,0x20000
s_and_b32 s83,s83,0x7000
ds_read_b64_tr_b16 a[190:191],v22 offset:20232
v_mfma_f32_16x16x32_bf16 v[168:171],a[144:147],a[200:203],v[168:171]
s_xor_b32 s69,s68,0x9e3779b9
ds_read_b64_tr_b16 a[128:129],v22
v_add_u32 v25,s80,v25
v_mfma_f32_16x16x32_bf16 v[176:179],a[144:147],a[208:211],v[176:179]
s_or_b32 s83,s83,s43
s_and_b32 s69,s69,-2
ds_read_b64_tr_b16 a[130:131],v22 offset:2048
v_mfma_f32_16x16x32_bf16 v[176:179],a[148:151],a[212:215],v[176:179]
s_add_u32 s55,s55,s86
s_add_u32 s93,s92,L_att0x_%=-L_pc_%=
ds_read_b64_tr_b16 a[132:133],v22 offset:16384
v_mfma_f32_16x16x32_bf16 v[184:187],a[148:151],a[220:223],v[184:187]
s_mov_b32 s50,32768
s_mov_b32 s49,98304
ds_read_b64_tr_b16 a[134:135],v22 offset:18432
v_mfma_f32_16x16x32_bf16 v[184:187],a[144:147],a[216:219],v[184:187]
s_mov_b32 s45,0x1249
s_add_u32 s80,s80,131072
ds_read_b64_tr_b16 a[144:145],v22 offset:8
v_mfma_f32_16x16x32_bf16 v[164:167],a[152:155],a[192:195],v[164:167]
s_or_b32 s83,s83,0x40000
ds_read_b64_tr_b16 a[146:147],v22 offset:2056
s_waitcnt lgkmcnt(8)
v_mfma_f32_16x16x32_bf16 v[164:167],a[156:159],a[196:199],v[164:167]
buffer_load_dwordx4 v5,s[20:23],s47 offen offset:2048 nt lds
v_mfma_f32_16x16x32_bf16 v[172:175],a[156:159],a[204:207],v[172:175]
s_waitcnt vmcnt(5)
ds_read_b128 a[224:227],v20
v_mfma_f32_16x16x32_bf16 v[172:175],a[152:155],a[200:203],v[172:175]
ds_read_b64_tr_b16 a[148:149],v22 offset:16392
v_mfma_f32_16x16x32_bf16 v[180:183],a[152:155],a[208:211],v[180:183]
ds_read_b64_tr_b16 a[150:151],v22 offset:18440
v_mfma_f32_16x16x32_bf16 v[180:183],a[156:159],a[212:215],v[180:183]
v_mfma_f32_16x16x32_bf16 v[188:191],a[156:159],a[220:223],v[188:191]
ds_read_b128 a[228:231],v21
v_mfma_f32_16x16x32_bf16 v[188:191],a[152:155],a[216:219],v[188:191]
v_mfma_f32_16x16x32_bf16 v[192:195],a[160:163],a[192:195],v[192:195]
ds_read_b128 a[232:235],v20 offset:2048
v_mfma_f32_16x16x32_bf16 v[192:195],a[164:167],a[196:199],v[192:195]
v_mfma_f32_16x16x32_bf16 v[200:203],a[164:167],a[204:207],v[200:203]
ds_read_b128 a[236:239],v21 offset:2048
v_mfma_f32_16x16x32_bf16 v[200:203],a[160:163],a[200:203],v[200:203]
v_mfma_f32_16x16x32_bf16 v[208:211],a[160:163],a[208:211],v[208:211]
ds_read_b128 a[240:243],v20 offset:4096
v_mfma_f32_16x16x32_bf16 v[208:211],a[164:167],a[212:215],v[208:211]
buffer_load_dwordx4 v5,s[20:23],s47 offen offset:3072 nt lds
v_mfma_f32_16x16x32_bf16 v[216:219],a[164:167],a[220:223],v[216:219]
ds_read_b128 a[244:247],v21 offset:4096
v_mfma_f32_16x16x32_bf16 v[216:219],a[160:163],a[216:219],v[216:219]
ds_read_b64_tr_b16 a[160:161],v22 offset:256
v_mfma_f32_16x16x32_bf16 v[196:199],a[168:171],a[192:195],v[196:199]
ds_read_b128 a[248:251],v20 offset:6144
)A"
R"A(ds_read_b128 a[252:255],v21 offset:6144
v_mfma_f32_16x16x32_bf16 v[196:199],a[172:175],a[196:199],v[196:199]
v_mfma_f32_16x16x32_bf16 v[204:207],a[172:175],a[204:207],v[204:207]
v_mfma_f32_16x16x32_bf16 v[204:207],a[168:171],a[200:203],v[204:207]
v_mfma_f32_16x16x32_bf16 v[212:215],a[168:171],a[208:211],v[212:215]
ds_read_b64_tr_b16 a[162:163],v22 offset:2304
v_mfma_f32_16x16x32_bf16 v[212:215],a[172:175],a[212:215],v[212:215]
ds_read_b64_tr_b16 a[164:165],v22 offset:16640
v_mfma_f32_16x16x32_bf16 v[220:223],a[172:175],a[220:223],v[220:223]
s_waitcnt vmcnt(5) lgkmcnt(2)
s_addk_i32 m0,16384
ds_read_b64_tr_b16 a[166:167],v22 offset:18688
v_mfma_f32_16x16x32_bf16 v[220:223],a[168:171],a[216:219],v[220:223]
s_barrier
ds_read_b64_tr_b16 a[136:137],v22 offset:32768
v_mfma_f32_16x16x32_bf16 v[224:227],a[176:179],a[192:195],v[224:227]
buffer_load_dwordx4 v4,s[24:27],s47 offen nt lds
v_mfma_f32_16x16x32_bf16 v[224:227],a[180:183],a[196:199],v[224:227]
ds_read_b64_tr_b16 a[138:139],v22 offset:34816
v_mfma_f32_16x16x32_bf16 v[232:235],a[180:183],a[204:207],v[232:235]
ds_read_b64_tr_b16 a[140:141],v22 offset:49152
v_mfma_f32_16x16x32_bf16 v[232:235],a[176:179],a[200:203],v[232:235]
ds_read_b64_tr_b16 a[142:143],v22 offset:51200
v_mfma_f32_16x16x32_bf16 v[240:243],a[176:179],a[208:211],v[240:243]
ds_read_b64_tr_b16 a[152:153],v22 offset:32776
v_mfma_f32_16x16x32_bf16 v[240:243],a[180:183],a[212:215],v[240:243]
ds_read_b64_tr_b16 a[154:155],v22 offset:34824
v_mfma_f32_16x16x32_bf16 v[248:251],a[180:183],a[220:223],v[248:251]
ds_read_b64_tr_b16 a[156:157],v22 offset:49160
v_mfma_f32_16x16x32_bf16 v[248:251],a[176:179],a[216:219],v[248:251]
ds_read_b64_tr_b16 a[158:159],v22 offset:51208
v_mfma_f32_16x16x32_bf16 v[228:231],a[184:187],a[192:195],v[228:231]
ds_read_b64_tr_b16 a[168:169],v22 offset:33024
v_mfma_f32_16x16x32_bf16 v[228:231],a[188:191],a[196:199],v[228:231]
ds_read_b64_tr_b16 a[170:171],v22 offset:35072
v_mfma_f32_16x16x32_bf16 v[236:239],a[188:191],a[204:207],v[236:239]
ds_read_b64_tr_b16 a[172:173],v22 offset:49408
v_mfma_f32_16x16x32_bf16 v[236:239],a[184:187],a[200:203],v[236:239]
ds_read_b64_tr_b16 a[174:175],v22 offset:51456
v_mfma_f32_16x16x32_bf16 v[244:247],a[184:187],a[208:211],v[244:247]
buffer_load_dwordx4 v5,s[24:27],s47 offen offset:1024 nt lds
v_mfma_f32_16x16x32_bf16 v[244:247],a[188:191],a[212:215],v[244:247]
ds_read_b64_tr_b16 a[176:177],v22 offset:264
v_mfma_f32_16x16x32_bf16 v[252:255],a[188:191],a[220:223],v[252:255]
ds_read_b64_tr_b16 a[178:179],v22 offset:2312
v_mfma_f32_16x16x32_bf16 v[252:255],a[184:187],a[216:219],v[252:255]
ds_read_b64_tr_b16 a[180:181],v22 offset:16648
v_mfma_f32_16x16x32_bf16 a[0:3],a[128:131],a[192:195],a[0:3]
ds_read_b64_tr_b16 a[182:183],v22 offset:18696
v_mfma_f32_16x16x32_bf16 a[0:3],a[132:135],a[196:199],a[0:3]
s_waitcnt lgkmcnt(6)
ds_read_b64_tr_b16 a[184:185],v22 offset:33032
v_mfma_f32_16x16x32_bf16 a[0:3],a[136:139],a[224:227],a[0:3]
ds_read_b64_tr_b16 a[186:187],v22 offset:35080
v_mfma_f32_16x16x32_bf16 a[0:3],a[140:143],a[228:231],a[0:3]
ds_read_b64_tr_b16 a[188:189],v22 offset:49416
v_mfma_f32_16x16x32_bf16 a[32:35],a[140:143],a[236:239],a[32:35]
ds_read_b64_tr_b16 a[190:191],v22 offset:51464
v_mfma_f32_16x16x32_bf16 a[32:35],a[136:139],a[232:235],a[32:35]
v_mfma_f32_16x16x32_bf16 a[32:35],a[132:135],a[204:207],a[32:35]
v_mfma_f32_16x16x32_bf16 a[32:35],a[128:131],a[200:203],a[32:35]
v_mfma_f32_16x16x32_bf16 a[64:67],a[128:131],a[208:211],a[64:67]
buffer_load_dwordx4 v4,s[24:27],s47 offen offset:2048 nt lds
v_mfma_f32_16x16x32_bf16 a[64:67],a[132:135],a[212:215],a[64:67]
v_accvgpr_read_b32 v64,a0
v_accvgpr_read_b32 v65,a1
v_mfma_f32_16x16x32_bf16 a[64:67],a[136:139],a[240:243],a[64:67]
v_mul_f32 v64,s61,v64
v_mul_f32 v65,s61,v65
v_mfma_f32_16x16x32_bf16 a[64:67],a[140:143],a[244:247],a[64:67]
v_accvgpr_read_b32 v66,a2
v_accvgpr_read_b32 v67,a3
v_mfma_f32_16x16x32_bf16 a[96:99],a[140:143],a[252:255],a[96:99]
ds_read_b64_tr_b16 a[140:141],v22 offset:49664
v_cvt_pk_bf16_f32 v64,v64,v65
v_mfma_f32_16x16x32_bf16 a[96:99],a[136:139],a[248:251],a[96:99]
ds_read_b64_tr_b16 a[136:137],v22 offset:33280
v_accvgpr_read_b32 v80,a32
v_mfma_f32_16x16x32_bf16 a[96:99],a[132:135],a[220:223],a[96:99]
ds_read_b64_tr_b16 a[132:133],v22 offset:16896
v_accvgpr_read_b32 v81,a33
v_mfma_f32_16x16x32_bf16 a[96:99],a[128:131],a[216:219],a[96:99]
ds_read_b64_tr_b16 a[128:129],v22 offset:512
v_mul_f32 v80,s61,v80
v_mfma_f32_16x16x32_bf16 a[4:7],a[144:147],a[192:195],a[4:7]
ds_read_b64_tr_b16 a[130:131],v22 offset:2560
v_mul_f32 v81,s61,v81
v_mfma_f32_16x16x32_bf16 a[4:7],a[148:151],a[196:199],a[4:7]
ds_read_b64_tr_b16 a[134:135],v22 offset:18944
v_accvgpr_read_b32 v82,a34
v_mfma_f32_16x16x32_bf16 a[4:7],a[152:155],a[224:227],a[4:7]
ds_read_b64_tr_b16 a[138:139],v22 offset:35328
v_accvgpr_read_b32 v83,a35
v_mfma_f32_16x16x32_bf16 a[4:7],a[156:159],a[228:231],a[4:7]
ds_read_b64_tr_b16 a[142:143],v22 offset:51712
v_mul_f32 v66,s61,v66
v_mfma_f32_16x16x32_bf16 a[36:39],a[156:159],a[236:239],a[36:39]
buffer_load_dwordx4 v5,s[24:27],s47 offen offset:3072 nt lds
v_mfma_f32_16x16x32_bf16 a[36:39],a[152:155],a[232:235],a[36:39]
s_or_b32 s47,s47,0x18000
s_addk_i32 s47,4096
s_and_b32 s47,s47,0x27000
v_mfma_f32_16x16x32_bf16 a[36:39],a[148:151],a[204:207],a[36:39]
s_or_b32 s47,s47,s46
s_and_b32 s46,s54,0xfffc0000
v_mul_f32 v67,s61,v67
v_mfma_f32_16x16x32_bf16 a[36:39],a[144:147],a[200:203],a[36:39]
s_addk_i32 s54,16384
v_cvt_pk_bf16_f32 v80,v80,v81
v_mul_f32 v82,s61,v82
v_mfma_f32_16x16x32_bf16 a[68:71],a[144:147],a[208:211],a[68:71]
v_mul_f32 v83,s61,v83
v_cvt_pk_bf16_f32 v65,v66,v67
v_mfma_f32_16x16x32_bf16 a[68:71],a[148:151],a[212:215],a[68:71]
v_cvt_pk_bf16_f32 v81,v82,v83
v_accvgpr_read_b32 v112,a96
v_mfma_f32_16x16x32_bf16 a[68:71],a[152:155],a[240:243],a[68:71]
v_accvgpr_read_b32 v113,a97
v_accvgpr_read_b32 v66,a4
v_mfma_f32_16x16x32_bf16 a[68:71],a[156:159],a[244:247],a[68:71]
v_accvgpr_read_b32 v67,a5
v_mul_f32 v112,s61,v112
v_mfma_f32_16x16x32_bf16 a[100:103],a[156:159],a[252:255],a[100:103]
ds_read_b64_tr_b16 a[156:157],v22 offset:49672
v_mul_f32 v113,s61,v113
v_mfma_f32_16x16x32_bf16 a[100:103],a[152:155],a[248:251],a[100:103]
ds_read_b64_tr_b16 a[152:153],v22 offset:33288
v_accvgpr_read_b32 v82,a36
v_mfma_f32_16x16x32_bf16 a[100:103],a[148:151],a[220:223],a[100:103]
ds_read_b64_tr_b16 a[148:149],v22 offset:16904
v_accvgpr_read_b32 v83,a37
v_mfma_f32_16x16x32_bf16 a[100:103],a[144:147],a[216:219],a[100:103]
ds_read_b64_tr_b16 a[144:145],v22 offset:520
v_accvgpr_read_b32 v114,a98
v_mfma_f32_16x16x32_bf16 a[8:11],a[160:163],a[192:195],a[8:11]
s_add_u32 m0,0,s51
ds_read_b64_tr_b16 a[146:147],v22 offset:2568
v_accvgpr_read_b32 v115,a99
v_mfma_f32_16x16x32_bf16 a[8:11],a[164:167],a[196:199],a[8:11]
ds_read_b64_tr_b16 a[150:151],v22 offset:18952
v_accvgpr_read_b32 v12,a6
v_mfma_f32_16x16x32_bf16 a[8:11],a[168:171],a[224:227],a[8:11]
s_waitcnt lgkmcnt(6)
ds_read_b64_tr_b16 a[154:155],v22 offset:35336
v_accvgpr_read_b32 v13,a7
v_mfma_f32_16x16x32_bf16 a[8:11],a[172:175],a[228:231],a[8:11]
buffer_load_dwordx4 v4,s[20:23],s47 offen nt lds
v_mfma_f32_16x16x32_bf16 a[40:43],a[172:175],a[236:239],a[40:43]
ds_read_b64_tr_b16 a[158:159],v22 offset:51720
v_mul_f32 v66,s61,v66
v_mfma_f32_16x16x32_bf16 a[40:43],a[168:171],a[232:235],a[40:43]
v_mul_f32 v67,s61,v67
v_accvgpr_read_b32 v14,a38
v_mfma_f32_16x16x32_bf16 a[40:43],a[164:167],a[204:207],a[40:43]
v_accvgpr_read_b32 v15,a39
v_mul_f32 v82,s61,v82
v_mfma_f32_16x16x32_bf16 a[40:43],a[160:163],a[200:203],a[40:43]
v_mul_f32 v83,s61,v83
v_cvt_pk_bf16_f32 v112,v112,v113
v_mfma_f32_16x16x32_bf16 a[72:75],a[160:163],a[208:211],a[72:75]
v_mul_f32 v114,s61,v114
v_mul_f32 v115,s61,v115
v_mfma_f32_16x16x32_bf16 a[72:75],a[164:167],a[212:215],a[72:75]
v_accvgpr_read_b32 v68,a8
v_accvgpr_read_b32 v69,a9
v_mfma_f32_16x16x32_bf16 a[72:75],a[168:171],a[240:243],a[72:75]
v_mul_f32 v68,s61,v68
v_mul_f32 v69,s61,v69
v_mfma_f32_16x16x32_bf16 a[72:75],a[172:175],a[244:247],a[72:75]
v_accvgpr_read_b32 v70,a10
v_accvgpr_read_b32 v71,a11
v_mfma_f32_16x16x32_bf16 a[104:107],a[172:175],a[252:255],a[104:107]
ds_read_b64_tr_b16 a[172:173],v22 offset:49920
v_cvt_pk_bf16_f32 v68,v68,v69
v_mfma_f32_16x16x32_bf16 a[104:107],a[168:171],a[248:251],a[104:107]
ds_read_b64_tr_b16 a[168:169],v22 offset:33536
v_mul_f32 v70,s61,v70
v_mfma_f32_16x16x32_bf16 a[104:107],a[164:167],a[220:223],a[104:107]
ds_read_b64_tr_b16 a[164:165],v22 offset:17152
v_mul_f32 v71,s61,v71
v_mfma_f32_16x16x32_bf16 a[104:107],a[160:163],a[216:219],a[104:107]
ds_read_b64_tr_b16 a[160:161],v22 offset:768
v_mul_f32 v12,s61,v12
v_mfma_f32_16x16x32_bf16 a[12:15],a[176:179],a[192:195],a[12:15]
ds_read_b64_tr_b16 a[162:163],v22 offset:2816
v_mul_f32 v13,s61,v13
v_mfma_f32_16x16x32_bf16 a[12:15],a[180:183],a[196:199],a[12:15]
buffer_load_dwordx4 v4,s[20:23],s47 offen offset:1024 nt lds
v_mfma_f32_16x16x32_bf16 a[12:15],a[184:187],a[224:227],a[12:15]
ds_read_b64_tr_b16 a[166:167],v22 offset:19200
v_cvt_pk_bf16_f32 v66,v66,v67
v_mfma_f32_16x16x32_bf16 a[12:15],a[188:191],a[228:231],a[12:15]
ds_read_b64_tr_b16 a[170:171],v22 offset:35584
v_mul_f32 v14,s61,v14
v_mfma_f32_16x16x32_bf16 a[44:47],a[188:191],a[236:239],a[44:47]
ds_read_b64_tr_b16 a[174:175],v22 offset:51968
v_mul_f32 v15,s61,v15
v_mfma_f32_16x16x32_bf16 a[44:47],a[184:187],a[232:235],a[44:47]
v_cvt_pk_bf16_f32 v82,v82,v83
v_cvt_pk_bf16_f32 v113,v114,v115
v_mfma_f32_16x16x32_bf16 a[44:47],a[180:183],a[204:207],a[44:47]
v_cvt_pk_bf16_f32 v69,v70,v71
v_accvgpr_read_b32 v100,a72
v_mfma_f32_16x16x32_bf16 a[44:47],a[176:179],a[200:203],a[44:47]
v_accvgpr_read_b32 v101,a73
v_accvgpr_read_b32 v116,a104
v_mfma_f32_16x16x32_bf16 a[76:79],a[176:179],a[208:211],a[76:79]
v_accvgpr_read_b32 v117,a105
v_cvt_pk_bf16_f32 v67,v12,v13
v_mfma_f32_16x16x32_bf16 a[76:79],a[180:183],a[212:215],a[76:79]
v_cvt_pk_bf16_f32 v83,v14,v15
v_accvgpr_read_b32 v114,a100
v_mfma_f32_16x16x32_bf16 a[76:79],a[184:187],a[240:243],a[76:79]
v_accvgpr_read_b32 v115,a101
v_accvgpr_read_b32 v70,a12
v_mfma_f32_16x16x32_bf16 a[76:79],a[188:191],a[244:247],a[76:79]
v_accvgpr_read_b32 v71,a13
v_mul_f32 v100,s61,v100
v_mfma_f32_16x16x32_bf16 a[108:111],a[188:191],a[252:255],a[108:111]
ds_read_b64_tr_b16 a[188:189],v22 offset:49928
v_mul_f32 v101,s61,v101
v_mfma_f32_16x16x32_bf16 a[108:111],a[184:187],a[248:251],a[108:111]
ds_read_b64_tr_b16 a[184:185],v22 offset:33544
v_accvgpr_read_b32 v102,a74
v_mfma_f32_16x16x32_bf16 a[108:111],a[180:183],a[220:223],a[108:111]
ds_read_b64_tr_b16 a[180:181],v22 offset:17160
v_accvgpr_read_b32 v103,a75
v_mfma_f32_16x16x32_bf16 a[108:111],a[176:179],a[216:219],a[108:111]
buffer_load_dwordx4 v5,s[20:23],s47 offen offset:2048 nt lds
v_mfma_f32_16x16x32_bf16 a[16:19],a[128:131],a[192:195],a[16:19]
ds_read_b64_tr_b16 a[176:177],v22 offset:776
v_mul_f32 v116,s61,v116
v_mfma_f32_16x16x32_bf16 a[16:19],a[132:135],a[196:199],a[16:19]
ds_read_b64_tr_b16 a[178:179],v22 offset:2824
v_mul_f32 v117,s61,v117
v_mfma_f32_16x16x32_bf16 a[16:19],a[136:139],a[224:227],a[16:19]
ds_read_b64_tr_b16 a[182:183],v22 offset:19208
v_accvgpr_read_b32 v118,a106
v_mfma_f32_16x16x32_bf16 a[16:19],a[140:143],a[228:231],a[16:19]
ds_read_b64_tr_b16 a[186:187],v22 offset:35592
v_accvgpr_read_b32 v119,a107
v_mfma_f32_16x16x32_bf16 a[48:51],a[140:143],a[236:239],a[48:51]
ds_read_b64_tr_b16 a[190:191],v22 offset:51976
v_accvgpr_read_b32 v12,a102
v_mfma_f32_16x16x32_bf16 a[48:51],a[136:139],a[232:235],a[48:51]
v_accvgpr_read_b32 v13,a103
v_mul_f32 v114,s61,v114
v_mfma_f32_16x16x32_bf16 a[48:51],a[132:135],a[204:207],a[48:51]
v_mul_f32 v115,s61,v115
v_accvgpr_read_b32 v14,a14
v_mfma_f32_16x16x32_bf16 a[48:51],a[128:131],a[200:203],a[48:51]
v_accvgpr_read_b32 v15,a15
v_mul_f32 v70,s61,v70
v_mfma_f32_16x16x32_bf16 a[80:83],a[128:131],a[208:211],a[80:83]
v_mul_f32 v71,s61,v71
v_cvt_pk_bf16_f32 v100,v100,v101
v_mfma_f32_16x16x32_bf16 a[80:83],a[132:135],a[212:215],a[80:83]
v_mul_f32 v102,s61,v102
v_mul_f32 v103,s61,v103
v_mfma_f32_16x16x32_bf16 a[80:83],a[136:139],a[240:243],a[80:83]
)A"
R"A(v_cvt_pk_bf16_f32 v116,v116,v117
v_mul_f32 v118,s61,v118
v_mfma_f32_16x16x32_bf16 a[80:83],a[140:143],a[244:247],a[80:83]
v_mul_f32 v119,s61,v119
v_mul_f32 v12,s61,v12
v_mfma_f32_16x16x32_bf16 a[112:115],a[140:143],a[252:255],a[112:115]
ds_read_b64_tr_b16 a[140:141],v22 offset:50184
v_mul_f32 v13,s61,v13
v_mfma_f32_16x16x32_bf16 a[112:115],a[136:139],a[248:251],a[112:115]
buffer_load_dwordx4 v5,s[20:23],s47 offen offset:3072 nt lds
v_mfma_f32_16x16x32_bf16 a[112:115],a[132:135],a[220:223],a[112:115]
ds_read_b64_tr_b16 a[132:133],v22 offset:50176
v_cvt_pk_bf16_f32 v114,v114,v115
v_mfma_f32_16x16x32_bf16 a[112:115],a[128:131],a[216:219],a[112:115]
s_waitcnt lgkmcnt(2)
ds_read_b64_tr_b16 a[128:129],v22 offset:33792
v_mul_f32 v14,s61,v14
v_mfma_f32_16x16x32_bf16 a[20:23],a[144:147],a[192:195],a[20:23]
ds_read_b64_tr_b16 a[130:131],v22 offset:35840
v_mul_f32 v15,s61,v15
v_mfma_f32_16x16x32_bf16 a[20:23],a[148:151],a[196:199],a[20:23]
ds_read_b64_tr_b16 a[134:135],v22 offset:52224
v_cvt_pk_bf16_f32 v70,v70,v71
v_mfma_f32_16x16x32_bf16 a[20:23],a[152:155],a[224:227],a[20:23]
ds_read_b64_tr_b16 a[136:137],v22 offset:33800
v_cvt_pk_bf16_f32 v101,v102,v103
v_mfma_f32_16x16x32_bf16 a[20:23],a[156:159],a[228:231],a[20:23]
ds_read_b64_tr_b16 a[138:139],v22 offset:35848
v_cvt_pk_bf16_f32 v117,v118,v119
v_mfma_f32_16x16x32_bf16 a[52:55],a[156:159],a[236:239],a[52:55]
ds_read_b64_tr_b16 a[142:143],v22 offset:52232
v_cvt_pk_bf16_f32 v115,v12,v13
v_mfma_f32_16x16x32_bf16 a[52:55],a[152:155],a[232:235],a[52:55]
v_cvt_pk_bf16_f32 v71,v14,v15
v_accvgpr_read_b32 v102,a76
v_mfma_f32_16x16x32_bf16 a[52:55],a[148:151],a[204:207],a[52:55]
v_accvgpr_read_b32 v103,a77
v_accvgpr_read_b32 v118,a108
v_mfma_f32_16x16x32_bf16 a[52:55],a[144:147],a[200:203],a[52:55]
v_accvgpr_read_b32 v119,a109
v_accvgpr_read_b32 v12,a78
v_mfma_f32_16x16x32_bf16 a[84:87],a[144:147],a[208:211],a[84:87]
s_addk_i32 m0,16384
v_accvgpr_read_b32 v13,a79
v_mul_f32 v102,s61,v102
v_mfma_f32_16x16x32_bf16 a[84:87],a[148:151],a[212:215],a[84:87]
v_mul_f32 v103,s61,v103
v_accvgpr_read_b32 v14,a110
v_mfma_f32_16x16x32_bf16 a[84:87],a[152:155],a[240:243],a[84:87]
v_accvgpr_read_b32 v15,a111
v_mul_f32 v118,s61,v118
v_mfma_f32_16x16x32_bf16 a[84:87],a[156:159],a[244:247],a[84:87]
buffer_load_dwordx4 v4,s[24:27],s47 offen nt lds
v_mfma_f32_16x16x32_bf16 a[116:119],a[156:159],a[252:255],a[116:119]
ds_read_b64_tr_b16 a[156:157],v22 offset:50440
v_mul_f32 v119,s61,v119
v_mfma_f32_16x16x32_bf16 a[116:119],a[152:155],a[248:251],a[116:119]
ds_read_b64_tr_b16 a[152:153],v22 offset:34056
v_mul_f32 v12,s61,v12
v_mfma_f32_16x16x32_bf16 a[116:119],a[148:151],a[220:223],a[116:119]
ds_read_b64_tr_b16 a[148:149],v22 offset:50432
v_mul_f32 v13,s61,v13
v_mfma_f32_16x16x32_bf16 a[116:119],a[144:147],a[216:219],a[116:119]
ds_read_b64_tr_b16 a[144:145],v22 offset:34048
v_cvt_pk_bf16_f32 v102,v102,v103
v_mfma_f32_16x16x32_bf16 a[24:27],a[160:163],a[192:195],a[24:27]
ds_read_b64_tr_b16 a[146:147],v22 offset:36096
v_mul_f32 v14,s61,v14
v_mfma_f32_16x16x32_bf16 a[24:27],a[164:167],a[196:199],a[24:27]
ds_read_b64_tr_b16 a[150:151],v22 offset:52480
v_mul_f32 v15,s61,v15
v_mfma_f32_16x16x32_bf16 a[24:27],a[168:171],a[224:227],a[24:27]
ds_read_b64_tr_b16 a[154:155],v22 offset:36104
v_cvt_pk_bf16_f32 v118,v118,v119
v_mfma_f32_16x16x32_bf16 a[24:27],a[172:175],a[228:231],a[24:27]
ds_read_b64_tr_b16 a[158:159],v22 offset:52488
v_cvt_pk_bf16_f32 v103,v12,v13
v_mfma_f32_16x16x32_bf16 a[56:59],a[172:175],a[236:239],a[56:59]
v_cvt_pk_bf16_f32 v119,v14,v15
v_accvgpr_read_b32 v96,a64
v_mfma_f32_16x16x32_bf16 a[56:59],a[168:171],a[232:235],a[56:59]
v_accvgpr_read_b32 v72,a16
v_accvgpr_read_b32 v73,a17
v_accvgpr_read_b32 v74,a18
v_accvgpr_read_b32 v75,a19
v_accvgpr_read_b32 v12,a20
v_accvgpr_read_b32 v13,a21
v_accvgpr_read_b32 v14,a22
v_accvgpr_read_b32 v15,a23
v_pk_mul_f32 v[72:73],s[60:61],v[72:73] op_sel:[1,0] op_sel_hi:[1,1]
v_pk_mul_f32 v[74:75],s[60:61],v[74:75] op_sel:[1,0] op_sel_hi:[1,1]
v_pk_mul_f32 v[12:13],s[60:61],v[12:13] op_sel:[1,0] op_sel_hi:[1,1]
v_pk_mul_f32 v[14:15],s[60:61],v[14:15] op_sel:[1,0] op_sel_hi:[1,1]
v_cvt_pk_bf16_f32 v72,v72,v73
v_cvt_pk_bf16_f32 v73,v74,v75
v_cvt_pk_bf16_f32 v74,v12,v13
v_cvt_pk_bf16_f32 v75,v14,v15
v_mfma_f32_16x16x32_bf16 a[56:59],a[164:167],a[204:207],a[56:59]
v_accvgpr_read_b32 v97,a65
v_mul_f32 v96,s61,v96
v_mfma_f32_16x16x32_bf16 a[56:59],a[160:163],a[200:203],a[56:59]
v_accvgpr_read_b32 v88,a48
v_accvgpr_read_b32 v89,a49
v_accvgpr_read_b32 v90,a50
v_accvgpr_read_b32 v91,a51
v_accvgpr_read_b32 v12,a52
v_accvgpr_read_b32 v13,a53
v_accvgpr_read_b32 v14,a54
v_accvgpr_read_b32 v15,a55
v_pk_mul_f32 v[88:89],s[60:61],v[88:89] op_sel:[1,0] op_sel_hi:[1,1]
v_pk_mul_f32 v[90:91],s[60:61],v[90:91] op_sel:[1,0] op_sel_hi:[1,1]
v_pk_mul_f32 v[12:13],s[60:61],v[12:13] op_sel:[1,0] op_sel_hi:[1,1]
v_pk_mul_f32 v[14:15],s[60:61],v[14:15] op_sel:[1,0] op_sel_hi:[1,1]
v_cvt_pk_bf16_f32 v88,v88,v89
v_cvt_pk_bf16_f32 v89,v90,v91
v_cvt_pk_bf16_f32 v90,v12,v13
v_cvt_pk_bf16_f32 v91,v14,v15
v_mfma_f32_16x16x32_bf16 a[88:91],a[160:163],a[208:211],a[88:91]
v_mul_f32 v97,s61,v97
v_accvgpr_read_b32 v98,a66
v_mfma_f32_16x16x32_bf16 a[88:91],a[164:167],a[212:215],a[88:91]
buffer_load_dwordx4 v5,s[24:27],s47 offen offset:1024 nt lds
v_mfma_f32_16x16x32_bf16 a[88:91],a[168:171],a[240:243],a[88:91]
v_accvgpr_read_b32 v99,a67
v_cvt_pk_bf16_f32 v96,v96,v97
v_mfma_f32_16x16x32_bf16 a[88:91],a[172:175],a[244:247],a[88:91]
v_mul_f32 v98,s61,v98
v_mul_f32 v99,s61,v99
v_mfma_f32_16x16x32_bf16 a[120:123],a[172:175],a[252:255],a[120:123]
ds_read_b64_tr_b16 a[172:173],v22 offset:50696
v_cvt_pk_bf16_f32 v97,v98,v99
v_mfma_f32_16x16x32_bf16 a[120:123],a[168:171],a[248:251],a[120:123]
ds_read_b64_tr_b16 a[168:169],v22 offset:34312
v_accvgpr_read_b32 v84,a40
v_mfma_f32_16x16x32_bf16 a[120:123],a[164:167],a[220:223],a[120:123]
ds_read_b64_tr_b16 a[164:165],v22 offset:50688
v_accvgpr_read_b32 v85,a41
v_mfma_f32_16x16x32_bf16 a[120:123],a[160:163],a[216:219],a[120:123]
ds_read_b64_tr_b16 a[160:161],v22 offset:34304
v_accvgpr_read_b32 v98,a68
v_mfma_f32_16x16x32_bf16 a[28:31],a[176:179],a[192:195],a[28:31]
ds_read_b64_tr_b16 a[162:163],v22 offset:36352
v_accvgpr_read_b32 v99,a69
v_mfma_f32_16x16x32_bf16 a[28:31],a[180:183],a[196:199],a[28:31]
ds_read_b64_tr_b16 a[166:167],v22 offset:52736
v_mul_f32 v84,s61,v84
v_mfma_f32_16x16x32_bf16 a[28:31],a[184:187],a[224:227],a[28:31]
ds_read_b64_tr_b16 a[170:171],v22 offset:36360
v_mul_f32 v85,s61,v85
v_mfma_f32_16x16x32_bf16 a[28:31],a[188:191],a[228:231],a[28:31]
ds_read_b64_tr_b16 a[174:175],v22 offset:52744
v_accvgpr_read_b32 v86,a42
v_mfma_f32_16x16x32_bf16 a[60:63],a[188:191],a[236:239],a[60:63]
v_accvgpr_read_b32 v104,a80
v_accvgpr_read_b32 v105,a81
v_accvgpr_read_b32 v106,a82
v_accvgpr_read_b32 v107,a83
v_accvgpr_read_b32 v12,a84
v_accvgpr_read_b32 v13,a85
v_accvgpr_read_b32 v14,a86
v_accvgpr_read_b32 v15,a87
v_pk_mul_f32 v[104:105],s[60:61],v[104:105] op_sel:[1,0] op_sel_hi:[1,1]
v_pk_mul_f32 v[106:107],s[60:61],v[106:107] op_sel:[1,0] op_sel_hi:[1,1]
v_pk_mul_f32 v[12:13],s[60:61],v[12:13] op_sel:[1,0] op_sel_hi:[1,1]
v_pk_mul_f32 v[14:15],s[60:61],v[14:15] op_sel:[1,0] op_sel_hi:[1,1]
v_cvt_pk_bf16_f32 v104,v104,v105
v_cvt_pk_bf16_f32 v105,v106,v107
v_cvt_pk_bf16_f32 v106,v12,v13
v_cvt_pk_bf16_f32 v107,v14,v15
v_mfma_f32_16x16x32_bf16 a[60:63],a[184:187],a[232:235],a[60:63]
v_accvgpr_read_b32 v87,a43
v_accvgpr_read_b32 v16,a70
v_mfma_f32_16x16x32_bf16 a[60:63],a[180:183],a[204:207],a[60:63]
v_accvgpr_read_b32 v120,a112
v_accvgpr_read_b32 v121,a113
v_accvgpr_read_b32 v122,a114
v_accvgpr_read_b32 v123,a115
v_accvgpr_read_b32 v12,a116
v_accvgpr_read_b32 v13,a117
v_accvgpr_read_b32 v14,a118
v_accvgpr_read_b32 v15,a119
v_pk_mul_f32 v[120:121],s[60:61],v[120:121] op_sel:[1,0] op_sel_hi:[1,1]
v_pk_mul_f32 v[122:123],s[60:61],v[122:123] op_sel:[1,0] op_sel_hi:[1,1]
v_pk_mul_f32 v[12:13],s[60:61],v[12:13] op_sel:[1,0] op_sel_hi:[1,1]
v_pk_mul_f32 v[14:15],s[60:61],v[14:15] op_sel:[1,0] op_sel_hi:[1,1]
v_cvt_pk_bf16_f32 v120,v120,v121
v_cvt_pk_bf16_f32 v121,v122,v123
v_cvt_pk_bf16_f32 v122,v12,v13
v_cvt_pk_bf16_f32 v123,v14,v15
v_mfma_f32_16x16x32_bf16 a[60:63],a[176:179],a[200:203],a[60:63]
buffer_load_dwordx4 v4,s[24:27],s47 offen offset:2048 nt lds
v_mfma_f32_16x16x32_bf16 a[92:95],a[176:179],a[208:211],a[92:95]
v_accvgpr_read_b32 v17,a71
v_mul_f32 v98,s61,v98
v_mfma_f32_16x16x32_bf16 a[92:95],a[180:183],a[212:215],a[92:95]
v_mul_f32 v99,s61,v99
v_cvt_pk_bf16_f32 v84,v84,v85
v_mfma_f32_16x16x32_bf16 a[92:95],a[184:187],a[240:243],a[92:95]
v_mul_f32 v86,s61,v86
v_mul_f32 v87,s61,v87
v_mfma_f32_16x16x32_bf16 a[92:95],a[188:191],a[244:247],a[92:95]
v_mul_f32 v16,s61,v16
v_mul_f32 v17,s61,v17
v_mfma_f32_16x16x32_bf16 a[124:127],a[188:191],a[252:255],a[124:127]
ds_read_b64_tr_b16 a[188:189],v22 offset:50952
v_cvt_pk_bf16_f32 v98,v98,v99
v_mfma_f32_16x16x32_bf16 a[124:127],a[184:187],a[248:251],a[124:127]
ds_read_b64_tr_b16 a[184:185],v22 offset:34568
v_cvt_pk_bf16_f32 v85,v86,v87
v_mfma_f32_16x16x32_bf16 a[124:127],a[180:183],a[220:223],a[124:127]
ds_read_b64_tr_b16 a[180:181],v22 offset:50944
v_cvt_pk_bf16_f32 v99,v16,v17
v_mfma_f32_16x16x32_bf16 a[124:127],a[176:179],a[216:219],a[124:127]
s_waitcnt lgkmcnt(3)
ds_read_b64_tr_b16 a[176:177],v22 offset:34560
v_accvgpr_read_b32 v86,a44
v_mfma_f32_16x16x32_bf16 v[128:131],a[128:131],a[224:227],v[128:131]
ds_read_b64_tr_b16 a[178:179],v22 offset:36608
v_accvgpr_read_b32 v87,a45
v_mfma_f32_16x16x32_bf16 v[128:131],a[132:135],a[228:231],v[128:131]
ds_read_b64_tr_b16 a[182:183],v22 offset:52992
v_accvgpr_read_b32 v16,a46
v_mfma_f32_16x16x32_bf16 v[136:139],a[132:135],a[236:239],v[136:139]
ds_read_b64_tr_b16 a[186:187],v22 offset:36616
v_accvgpr_read_b32 v17,a47
v_mfma_f32_16x16x32_bf16 v[136:139],a[128:131],a[232:235],v[136:139]
ds_read_b64_tr_b16 a[190:191],v22 offset:53000
v_mul_f32 v86,s61,v86
v_mfma_f32_16x16x32_bf16 v[144:147],a[128:131],a[240:243],v[144:147]
v_accvgpr_read_b32 v76,a24
v_accvgpr_read_b32 v77,a25
v_accvgpr_read_b32 v78,a26
v_accvgpr_read_b32 v79,a27
v_accvgpr_read_b32 v12,a28
v_accvgpr_read_b32 v13,a29
v_accvgpr_read_b32 v14,a30
v_accvgpr_read_b32 v15,a31
v_pk_mul_f32 v[76:77],s[60:61],v[76:77] op_sel:[1,0] op_sel_hi:[1,1]
v_pk_mul_f32 v[78:79],s[60:61],v[78:79] op_sel:[1,0] op_sel_hi:[1,1]
v_pk_mul_f32 v[12:13],s[60:61],v[12:13] op_sel:[1,0] op_sel_hi:[1,1]
v_pk_mul_f32 v[14:15],s[60:61],v[14:15] op_sel:[1,0] op_sel_hi:[1,1]
v_cvt_pk_bf16_f32 v76,v76,v77
v_cvt_pk_bf16_f32 v77,v78,v79
v_cvt_pk_bf16_f32 v78,v12,v13
v_cvt_pk_bf16_f32 v79,v14,v15
v_mfma_f32_16x16x32_bf16 v[144:147],a[132:135],a[244:247],v[144:147]
buffer_load_dwordx4 v5,s[24:27],s47 offen offset:3072 nt lds
v_mfma_f32_16x16x32_bf16 v[152:155],a[132:135],a[252:255],v[152:155]
s_or_b32 s47,s47,0x18000
s_addk_i32 s47,4096
s_and_b32 s47,s47,0x27000
v_mfma_f32_16x16x32_bf16 v[152:155],a[128:131],a[248:251],v[152:155]
s_or_b32 s47,s47,s46
v_mul_f32 v87,s61,v87
v_mul_f32 v16,s61,v16
v_mfma_f32_16x16x32_bf16 v[132:135],a[136:139],a[224:227],v[132:135]
s_and_b32 s46,s54,0xfffc0000
v_mul_f32 v17,s61,v17
v_cvt_pk_bf16_f32 v86,v86,v87
v_mfma_f32_16x16x32_bf16 v[132:135],a[140:143],a[228:231],v[132:135]
s_addk_i32 s54,16384
v_cvt_pk_bf16_f32 v87,v16,v17
v_mfma_f32_16x16x32_bf16 v[140:143],a[140:143],a[236:239],v[140:143]
v_accvgpr_read_b32 v92,a56
v_accvgpr_read_b32 v93,a57
v_accvgpr_read_b32 v94,a58
v_accvgpr_read_b32 v95,a59
v_accvgpr_read_b32 v12,a60
v_accvgpr_read_b32 v13,a61
v_accvgpr_read_b32 v14,a62
v_accvgpr_read_b32 v15,a63
v_pk_mul_f32 v[92:93],s[60:61],v[92:93] op_sel:[1,0] op_sel_hi:[1,1]
v_pk_mul_f32 v[94:95],s[60:61],v[94:95] op_sel:[1,0] op_sel_hi:[1,1]
v_pk_mul_f32 v[12:13],s[60:61],v[12:13] op_sel:[1,0] op_sel_hi:[1,1]
v_pk_mul_f32 v[14:15],s[60:61],v[14:15] op_sel:[1,0] op_sel_hi:[1,1]
v_cvt_pk_bf16_f32 v92,v92,v93
v_cvt_pk_bf16_f32 v93,v94,v95
v_cvt_pk_bf16_f32 v94,v12,v13
v_cvt_pk_bf16_f32 v95,v14,v15
v_mfma_f32_16x16x32_bf16 v[140:143],a[136:139],a[232:235],v[140:143]
v_accvgpr_read_b32 v108,a88
)A"
R"A(v_accvgpr_read_b32 v109,a89
v_accvgpr_read_b32 v110,a90
v_accvgpr_read_b32 v111,a91
v_accvgpr_read_b32 v12,a92
v_accvgpr_read_b32 v13,a93
v_accvgpr_read_b32 v14,a94
v_accvgpr_read_b32 v15,a95
v_pk_mul_f32 v[108:109],s[60:61],v[108:109] op_sel:[1,0] op_sel_hi:[1,1]
v_pk_mul_f32 v[110:111],s[60:61],v[110:111] op_sel:[1,0] op_sel_hi:[1,1]
v_pk_mul_f32 v[12:13],s[60:61],v[12:13] op_sel:[1,0] op_sel_hi:[1,1]
v_pk_mul_f32 v[14:15],s[60:61],v[14:15] op_sel:[1,0] op_sel_hi:[1,1]
v_cvt_pk_bf16_f32 v108,v108,v109
v_cvt_pk_bf16_f32 v109,v110,v111
v_cvt_pk_bf16_f32 v110,v12,v13
v_cvt_pk_bf16_f32 v111,v14,v15
v_mfma_f32_16x16x32_bf16 v[148:151],a[136:139],a[240:243],v[148:151]
v_accvgpr_read_b32 v124,a120
v_accvgpr_read_b32 v125,a121
v_accvgpr_read_b32 v126,a122
v_accvgpr_read_b32 v127,a123
v_accvgpr_read_b32 v12,a124
v_accvgpr_read_b32 v13,a125
v_accvgpr_read_b32 v14,a126
v_accvgpr_read_b32 v15,a127
v_pk_mul_f32 v[124:125],s[60:61],v[124:125] op_sel:[1,0] op_sel_hi:[1,1]
v_pk_mul_f32 v[126:127],s[60:61],v[126:127] op_sel:[1,0] op_sel_hi:[1,1]
v_pk_mul_f32 v[12:13],s[60:61],v[12:13] op_sel:[1,0] op_sel_hi:[1,1]
v_pk_mul_f32 v[14:15],s[60:61],v[14:15] op_sel:[1,0] op_sel_hi:[1,1]
v_cvt_pk_bf16_f32 v124,v124,v125
v_cvt_pk_bf16_f32 v125,v126,v127
v_cvt_pk_bf16_f32 v126,v12,v13
v_cvt_pk_bf16_f32 v127,v14,v15
v_mfma_f32_16x16x32_bf16 v[148:151],a[140:143],a[244:247],v[148:151]
v_mfma_f32_16x16x32_bf16 v[156:159],a[140:143],a[252:255],v[156:159]
s_waitcnt vmcnt(8) lgkmcnt(0)
v_mfma_f32_16x16x32_bf16 v[156:159],a[136:139],a[248:251],v[156:159]
s_barrier
ds_read_b128 v[48:51],v18
v_mfma_f32_16x16x32_bf16 v[160:163],a[144:147],a[224:227],v[160:163]
ds_read_b64_tr_b16 a[192:193],v2
v_mfma_f32_16x16x32_bf16 v[160:163],a[148:151],a[228:231],v[160:163]
ds_read_b64_tr_b16 a[194:195],v2 offset:4096
v_mfma_f32_16x16x32_bf16 v[168:171],a[148:151],a[236:239],v[168:171]
ds_read_b64_tr_b16 a[196:197],v2 offset:8192
v_mfma_f32_16x16x32_bf16 v[168:171],a[144:147],a[232:235],v[168:171]
ds_read_b128 v[52:55],v18 offset:256
v_mfma_f32_16x16x32_bf16 v[176:179],a[144:147],a[240:243],v[176:179]
v_mul_f32 v128,s61,v128
v_mul_f32 v129,s61,v129
v_mul_f32 v130,s61,v130
v_mul_f32 v131,s61,v131
v_cvt_pk_bf16_f32 v128,v128,v129
v_cvt_pk_bf16_f32 v129,v130,v131
v_pk_mul_f32 v[132:133],s[60:61],v[132:133] op_sel:[1,0] op_sel_hi:[1,1]
v_pk_mul_f32 v[134:135],s[60:61],v[134:135] op_sel:[1,0] op_sel_hi:[1,1]
v_pk_mul_f32 v[136:137],s[60:61],v[136:137] op_sel:[1,0] op_sel_hi:[1,1]
v_pk_mul_f32 v[138:139],s[60:61],v[138:139] op_sel:[1,0] op_sel_hi:[1,1]
v_pk_mul_f32 v[140:141],s[60:61],v[140:141] op_sel:[1,0] op_sel_hi:[1,1]
v_pk_mul_f32 v[142:143],s[60:61],v[142:143] op_sel:[1,0] op_sel_hi:[1,1]
v_pk_mul_f32 v[144:145],s[60:61],v[144:145] op_sel:[1,0] op_sel_hi:[1,1]
v_pk_mul_f32 v[146:147],s[60:61],v[146:147] op_sel:[1,0] op_sel_hi:[1,1]
v_pk_mul_f32 v[148:149],s[60:61],v[148:149] op_sel:[1,0] op_sel_hi:[1,1]
v_pk_mul_f32 v[150:151],s[60:61],v[150:151] op_sel:[1,0] op_sel_hi:[1,1]
v_pk_mul_f32 v[152:153],s[60:61],v[152:153] op_sel:[1,0] op_sel_hi:[1,1]
v_pk_mul_f32 v[154:155],s[60:61],v[154:155] op_sel:[1,0] op_sel_hi:[1,1]
v_pk_mul_f32 v[156:157],s[60:61],v[156:157] op_sel:[1,0] op_sel_hi:[1,1]
v_pk_mul_f32 v[158:159],s[60:61],v[158:159] op_sel:[1,0] op_sel_hi:[1,1]
v_mfma_f32_16x16x32_bf16 v[176:179],a[148:151],a[244:247],v[176:179]
ds_read_b128 v[56:59],v18 offset:512
v_cvt_pk_bf16_f32 v130,v132,v133
v_mfma_f32_16x16x32_bf16 v[184:187],a[148:151],a[252:255],v[184:187]
v_cvt_pk_bf16_f32 v132,v136,v137
v_cvt_pk_bf16_f32 v133,v138,v139
v_mfma_f32_16x16x32_bf16 v[184:187],a[144:147],a[248:251],v[184:187]
ds_read_b128 v[60:63],v18 offset:768
v_cvt_pk_bf16_f32 v131,v134,v135
v_mfma_f32_16x16x32_bf16 v[164:167],a[152:155],a[224:227],v[164:167]
v_cvt_pk_bf16_f32 v134,v140,v141
v_cvt_pk_bf16_f32 v135,v142,v143
v_mfma_f32_16x16x32_bf16 v[164:167],a[156:159],a[228:231],v[164:167]
ds_read_b128 a[128:131],v1
v_cvt_pk_bf16_f32 v144,v144,v145
v_mfma_f32_16x16x32_bf16 v[172:175],a[156:159],a[236:239],v[172:175]
v_cvt_pk_bf16_f32 v145,v146,v147
v_cvt_pk_bf16_f32 v146,v148,v149
v_mfma_f32_16x16x32_bf16 v[172:175],a[152:155],a[232:235],v[172:175]
ds_read_b128 a[132:135],v1 offset:256
v_cvt_pk_bf16_f32 v148,v152,v153
v_mfma_f32_16x16x32_bf16 v[180:183],a[152:155],a[240:243],v[180:183]
v_cvt_pk_bf16_f32 v149,v154,v155
v_cvt_pk_bf16_f32 v147,v150,v151
v_mfma_f32_16x16x32_bf16 v[180:183],a[156:159],a[244:247],v[180:183]
ds_read_b128 a[136:139],v1 offset:512
v_cvt_pk_bf16_f32 v150,v156,v157
v_mfma_f32_16x16x32_bf16 v[188:191],a[156:159],a[252:255],v[188:191]
ds_read_b64_tr_b16 a[198:199],v2 offset:12288
v_cvt_pk_bf16_f32 v151,v158,v159
v_mfma_f32_16x16x32_bf16 v[188:191],a[152:155],a[248:251],v[188:191]
ds_read_b64_tr_b16 a[204:205],v3 offset:8192
v_mfma_f32_16x16x32_bf16 v[192:195],a[160:163],a[224:227],v[192:195]
ds_read_b64_tr_b16 a[206:207],v3 offset:12288
v_mfma_f32_16x16x32_bf16 v[192:195],a[164:167],a[228:231],v[192:195]
s_add_u32 m0,32768,s51
ds_read_b128 a[140:143],v1 offset:768
v_mfma_f32_16x16x32_bf16 v[200:203],a[164:167],a[236:239],v[200:203]
ds_read_b64_tr_b16 a[200:201],v3
v_mfma_f32_16x16x32_bf16 v[200:203],a[160:163],a[232:235],v[200:203]
ds_read_b64_tr_b16 a[202:203],v3 offset:4096
v_mfma_f32_16x16x32_bf16 v[208:211],a[160:163],a[240:243],v[208:211]
buffer_load_dwordx4 v4,s[20:23],s47 offen nt lds
v_mfma_f32_16x16x32_bf16 v[208:211],a[164:167],a[244:247],v[208:211]
ds_read_b128 a[156:159],v1 offset:4864
v_mfma_f32_16x16x32_bf16 v[216:219],a[164:167],a[252:255],v[216:219]
v_mul_f32 v160,s61,v160
v_mul_f32 v161,s61,v161
v_mul_f32 v162,s61,v162
v_mul_f32 v163,s61,v163
v_cvt_pk_bf16_f32 v160,v160,v161
v_cvt_pk_bf16_f32 v161,v162,v163
v_pk_mul_f32 v[164:165],s[60:61],v[164:165] op_sel:[1,0] op_sel_hi:[1,1]
v_pk_mul_f32 v[166:167],s[60:61],v[166:167] op_sel:[1,0] op_sel_hi:[1,1]
v_pk_mul_f32 v[168:169],s[60:61],v[168:169] op_sel:[1,0] op_sel_hi:[1,1]
v_pk_mul_f32 v[170:171],s[60:61],v[170:171] op_sel:[1,0] op_sel_hi:[1,1]
v_pk_mul_f32 v[172:173],s[60:61],v[172:173] op_sel:[1,0] op_sel_hi:[1,1]
v_pk_mul_f32 v[174:175],s[60:61],v[174:175] op_sel:[1,0] op_sel_hi:[1,1]
v_pk_mul_f32 v[176:177],s[60:61],v[176:177] op_sel:[1,0] op_sel_hi:[1,1]
v_pk_mul_f32 v[178:179],s[60:61],v[178:179] op_sel:[1,0] op_sel_hi:[1,1]
v_pk_mul_f32 v[180:181],s[60:61],v[180:181] op_sel:[1,0] op_sel_hi:[1,1]
v_pk_mul_f32 v[182:183],s[60:61],v[182:183] op_sel:[1,0] op_sel_hi:[1,1]
v_pk_mul_f32 v[184:185],s[60:61],v[184:185] op_sel:[1,0] op_sel_hi:[1,1]
v_pk_mul_f32 v[186:187],s[60:61],v[186:187] op_sel:[1,0] op_sel_hi:[1,1]
v_pk_mul_f32 v[188:189],s[60:61],v[188:189] op_sel:[1,0] op_sel_hi:[1,1]
v_pk_mul_f32 v[190:191],s[60:61],v[190:191] op_sel:[1,0] op_sel_hi:[1,1]
v_mfma_f32_16x16x32_bf16 v[216:219],a[160:163],a[248:251],v[216:219]
ds_read_b128 a[152:155],v1 offset:4608
v_cvt_pk_bf16_f32 v162,v164,v165
v_mfma_f32_16x16x32_bf16 v[196:199],a[168:171],a[224:227],v[196:199]
v_cvt_pk_bf16_f32 v164,v168,v169
v_cvt_pk_bf16_f32 v165,v170,v171
v_mfma_f32_16x16x32_bf16 v[196:199],a[172:175],a[228:231],v[196:199]
ds_read_b128 a[148:151],v1 offset:4352
v_cvt_pk_bf16_f32 v163,v166,v167
v_mfma_f32_16x16x32_bf16 v[204:207],a[172:175],a[236:239],v[204:207]
v_cvt_pk_bf16_f32 v166,v172,v173
v_cvt_pk_bf16_f32 v167,v174,v175
v_mfma_f32_16x16x32_bf16 v[204:207],a[168:171],a[232:235],v[204:207]
ds_read_b128 a[144:147],v1 offset:4096
v_cvt_pk_bf16_f32 v176,v176,v177
v_mfma_f32_16x16x32_bf16 v[212:215],a[168:171],a[240:243],v[212:215]
v_cvt_pk_bf16_f32 v177,v178,v179
v_cvt_pk_bf16_f32 v178,v180,v181
v_mfma_f32_16x16x32_bf16 v[212:215],a[172:175],a[244:247],v[212:215]
ds_read_b128 a[160:163],v1 offset:8192
v_cvt_pk_bf16_f32 v180,v184,v185
v_mfma_f32_16x16x32_bf16 v[220:223],a[172:175],a[252:255],v[220:223]
v_cvt_pk_bf16_f32 v181,v186,v187
v_cvt_pk_bf16_f32 v179,v182,v183
v_mfma_f32_16x16x32_bf16 v[220:223],a[168:171],a[248:251],v[220:223]
ds_read_b128 a[164:167],v1 offset:8448
v_cvt_pk_bf16_f32 v182,v188,v189
v_mfma_f32_16x16x32_bf16 v[224:227],a[176:179],a[224:227],v[224:227]
ds_read_b64_tr_b16 a[208:209],v2 offset:256
v_cvt_pk_bf16_f32 v183,v190,v191
v_mfma_f32_16x16x32_bf16 v[224:227],a[180:183],a[228:231],v[224:227]
buffer_load_dwordx4 v4,s[20:23],s47 offen offset:1024 nt lds
v_mfma_f32_16x16x32_bf16 v[232:235],a[180:183],a[236:239],v[232:235]
ds_read_b128 a[168:171],v1 offset:8704
v_mfma_f32_16x16x32_bf16 v[232:235],a[176:179],a[232:235],v[232:235]
ds_read_b64_tr_b16 a[210:211],v2 offset:4352
v_mfma_f32_16x16x32_bf16 v[240:243],a[176:179],a[240:243],v[240:243]
ds_read_b64_tr_b16 a[212:213],v2 offset:8448
v_mfma_f32_16x16x32_bf16 v[240:243],a[180:183],a[244:247],v[240:243]
v_mul_f32 v192,s61,v192
v_mul_f32 v193,s61,v193
v_mul_f32 v194,s61,v194
v_mul_f32 v195,s61,v195
v_cvt_pk_bf16_f32 v192,v192,v193
v_cvt_pk_bf16_f32 v193,v194,v195
v_pk_mul_f32 v[196:197],s[60:61],v[196:197] op_sel:[1,0] op_sel_hi:[1,1]
v_pk_mul_f32 v[198:199],s[60:61],v[198:199] op_sel:[1,0] op_sel_hi:[1,1]
v_pk_mul_f32 v[200:201],s[60:61],v[200:201] op_sel:[1,0] op_sel_hi:[1,1]
v_pk_mul_f32 v[202:203],s[60:61],v[202:203] op_sel:[1,0] op_sel_hi:[1,1]
v_pk_mul_f32 v[204:205],s[60:61],v[204:205] op_sel:[1,0] op_sel_hi:[1,1]
v_pk_mul_f32 v[206:207],s[60:61],v[206:207] op_sel:[1,0] op_sel_hi:[1,1]
v_pk_mul_f32 v[208:209],s[60:61],v[208:209] op_sel:[1,0] op_sel_hi:[1,1]
v_pk_mul_f32 v[210:211],s[60:61],v[210:211] op_sel:[1,0] op_sel_hi:[1,1]
v_pk_mul_f32 v[212:213],s[60:61],v[212:213] op_sel:[1,0] op_sel_hi:[1,1]
v_pk_mul_f32 v[214:215],s[60:61],v[214:215] op_sel:[1,0] op_sel_hi:[1,1]
v_pk_mul_f32 v[216:217],s[60:61],v[216:217] op_sel:[1,0] op_sel_hi:[1,1]
v_pk_mul_f32 v[218:219],s[60:61],v[218:219] op_sel:[1,0] op_sel_hi:[1,1]
v_pk_mul_f32 v[220:221],s[60:61],v[220:221] op_sel:[1,0] op_sel_hi:[1,1]
v_pk_mul_f32 v[222:223],s[60:61],v[222:223] op_sel:[1,0] op_sel_hi:[1,1]
v_mfma_f32_16x16x32_bf16 v[248:251],a[180:183],a[252:255],v[248:251]
ds_read_b128 a[172:175],v1 offset:8960
v_cvt_pk_bf16_f32 v194,v196,v197
v_mfma_f32_16x16x32_bf16 v[248:251],a[176:179],a[248:251],v[248:251]
v_cvt_pk_bf16_f32 v196,v200,v201
v_cvt_pk_bf16_f32 v197,v202,v203
v_mfma_f32_16x16x32_bf16 v[228:231],a[184:187],a[224:227],v[228:231]
ds_read_b128 a[180:183],v1 offset:12544
v_cvt_pk_bf16_f32 v195,v198,v199
v_mfma_f32_16x16x32_bf16 v[228:231],a[188:191],a[228:231],v[228:231]
v_cvt_pk_bf16_f32 v198,v204,v205
v_cvt_pk_bf16_f32 v199,v206,v207
v_mfma_f32_16x16x32_bf16 v[236:239],a[188:191],a[236:239],v[236:239]
ds_read_b128 a[176:179],v1 offset:12288
v_cvt_pk_bf16_f32 v208,v208,v209
v_mfma_f32_16x16x32_bf16 v[236:239],a[184:187],a[232:235],v[236:239]
v_cvt_pk_bf16_f32 v209,v210,v211
v_cvt_pk_bf16_f32 v210,v212,v213
v_mfma_f32_16x16x32_bf16 v[244:247],a[184:187],a[240:243],v[244:247]
v_cvt_pk_bf16_f32 v212,v216,v217
v_cvt_pk_bf16_f32 v213,v218,v219
v_mfma_f32_16x16x32_bf16 v[244:247],a[188:191],a[244:247],v[244:247]
v_cvt_pk_bf16_f32 v211,v214,v215
v_cvt_pk_bf16_f32 v214,v220,v221
v_mfma_f32_16x16x32_bf16 v[252:255],a[188:191],a[252:255],v[252:255]
buffer_load_dwordx4 v5,s[20:23],s47 offen offset:2048 nt lds
v_mfma_f32_16x16x32_bf16 v[252:255],a[184:187],a[248:251],v[252:255]
s_waitcnt lgkmcnt(2)
ds_read_b128 a[188:191],v1 offset:13056
v_cvt_pk_bf16_f32 v215,v222,v223
v_mfma_f32_16x16x32_bf16 v[32:35],v[48:51],v[64:67],0
ds_read_b64_tr_b16 a[214:215],v2 offset:12544
v_mfma_f32_16x16x32_bf16 v[32:35],v[52:55],v[68:71],v[32:35]
ds_read_b64_tr_b16 a[220:221],v3 offset:8448
v_mfma_f32_16x16x32_bf16 v[32:35],v[56:59],v[72:75],v[32:35]
ds_read_b64_tr_b16 a[222:223],v3 offset:12544
v_mfma_f32_16x16x32_bf16 v[32:35],v[60:63],v[76:79],v[32:35]
ds_read_b128 a[184:187],v1 offset:12800
v_add_u32 v1,s48,v1
v_mfma_f32_16x16x32_bf16 v[36:39],v[48:51],v[80:83],0
ds_read_b64_tr_b16 a[216:217],v3 offset:256
v_mfma_f32_16x16x32_bf16 v[36:39],v[52:55],v[84:87],v[36:39]
ds_read_b64_tr_b16 a[218:219],v3 offset:4352
v_mfma_f32_16x16x32_bf16 v[36:39],v[56:59],v[88:91],v[36:39]
ds_read_b64_tr_b16 a[224:225],v2 offset:512
v_mfma_f32_16x16x32_bf16 v[36:39],v[60:63],v[92:95],v[36:39]
)A"
R"A(ds_read_b64_tr_b16 a[226:227],v2 offset:4608
ds_read_b64_tr_b16 a[228:229],v2 offset:8704
v_mfma_f32_16x16x32_bf16 v[40:43],v[48:51],v[96:99],0
ds_read_b64_tr_b16 a[230:231],v2 offset:12800
v_mfma_f32_16x16x32_bf16 v[40:43],v[52:55],v[100:103],v[40:43]
v_max3_f32 v32,v32,v33,v34
v_max3_f32 v32,v32,v35,s18
v_mfma_f32_16x16x32_bf16 v[40:43],v[56:59],v[104:107],v[40:43]
v_sub_f32 v35,0,v32
v_sub_f32 v34,0,v32
v_mfma_f32_16x16x32_bf16 v[40:43],v[60:63],v[108:111],v[40:43]
v_sub_f32 v33,0,v32
v_sub_f32 v32,0,v32
v_mfma_f32_16x16x32_bf16 v[44:47],v[48:51],v[112:115],0
buffer_load_dwordx4 v5,s[20:23],s47 offen offset:3072 nt lds
v_mfma_f32_16x16x32_bf16 v[44:47],v[52:55],v[116:119],v[44:47]
v_max3_f32 v36,v36,v37,v38
v_max3_f32 v36,v36,v39,s19
v_mfma_f32_16x16x32_bf16 v[44:47],v[56:59],v[120:123],v[44:47]
v_sub_f32 v39,0,v36
v_sub_f32 v38,0,v36
v_mfma_f32_16x16x32_bf16 v[44:47],v[60:63],v[124:127],v[44:47]
v_sub_f32 v37,0,v36
v_sub_f32 v36,0,v36
v_mfma_f32_16x16x32_bf16 v[136:139],a[128:131],v[64:67],v[32:35]
ds_read_b64_tr_b16 a[236:237],v3 offset:8704
ds_read_b64_tr_b16 a[238:239],v3 offset:12800
v_mfma_f32_16x16x32_bf16 v[136:139],a[132:135],v[68:71],v[136:139]
v_max3_f32 v40,v40,v41,v42
v_max3_f32 v40,v40,v43,s100
v_mfma_f32_16x16x32_bf16 v[136:139],a[136:139],v[72:75],v[136:139]
v_sub_f32 v43,0,v40
v_sub_f32 v42,0,v40
v_mfma_f32_16x16x32_bf16 v[136:139],a[140:143],v[76:79],v[136:139]
v_sub_f32 v41,0,v40
v_sub_f32 v40,0,v40
v_mfma_f32_16x16x32_bf16 v[140:143],a[156:159],v[76:79],v[32:35]
ds_read_b64_tr_b16 a[232:233],v3 offset:512
ds_read_b64_tr_b16 a[234:235],v3 offset:4608
v_mfma_f32_16x16x32_bf16 v[140:143],a[152:155],v[72:75],v[140:143]
s_addk_i32 m0,16384
ds_read_b64_tr_b16 a[240:241],v2 offset:768
v_max3_f32 v44,v44,v45,v46
v_mfma_f32_16x16x32_bf16 v[140:143],a[148:151],v[68:71],v[140:143]
ds_read_b64_tr_b16 a[242:243],v2 offset:4864
v_max3_f32 v44,v44,v47,s101
v_mfma_f32_16x16x32_bf16 v[140:143],a[144:147],v[64:67],v[140:143]
ds_read_b64_tr_b16 a[244:245],v2 offset:8960
v_mov_b32 v48,0
v_mfma_f32_16x16x32_bf16 v[152:155],a[160:163],v[64:67],v[32:35]
buffer_load_dwordx4 v4,s[24:27],s47 offen nt lds
v_mfma_f32_16x16x32_bf16 v[152:155],a[164:167],v[68:71],v[152:155]
v_exp_f32 v136,v136
v_mfma_f32_16x16x32_bf16 v[152:155],a[168:171],v[72:75],v[152:155]
v_exp_f32 v137,v137
v_mfma_f32_16x16x32_bf16 v[152:155],a[172:175],v[76:79],v[152:155]
s_waitcnt lgkmcnt(5)
v_exp_f32 v138,v138
v_mfma_f32_16x16x32_bf16 v[156:159],a[188:191],v[76:79],v[32:35]
v_exp_f32 v139,v139
v_mfma_f32_16x16x32_bf16 v[156:159],a[184:187],v[72:75],v[156:159]
v_cvt_pk_bf16_f32 v136,v136,v137
v_cvt_pk_bf16_f32 v137,v138,v139
v_mfma_f32_16x16x32_bf16 v[156:159],a[180:183],v[68:71],v[156:159]
v_exp_f32 v140,v140
v_mfma_f32_16x16x32_bf16 v[156:159],a[176:179],v[64:67],v[156:159]
v_exp_f32 v141,v141
v_mfma_f32_16x16x32_bf16 v[168:171],a[128:131],v[80:83],v[36:39]
v_exp_f32 v142,v142
v_mfma_f32_16x16x32_bf16 v[168:171],a[132:135],v[84:87],v[168:171]
v_exp_f32 v143,v143
v_mfma_f32_16x16x32_bf16 v[168:171],a[136:139],v[88:91],v[168:171]
v_cvt_pk_bf16_f32 v138,v140,v141
v_cvt_pk_bf16_f32 v139,v142,v143
v_mfma_f32_16x16x32_bf16 v[168:171],a[140:143],v[92:95],v[168:171]
v_exp_f32 v152,v152
v_mfma_f32_16x16x32_bf16 v[172:175],a[156:159],v[92:95],v[36:39]
v_exp_f32 v153,v153
v_mfma_f32_16x16x32_bf16 v[172:175],a[152:155],v[88:91],v[172:175]
buffer_load_dwordx4 v5,s[24:27],s47 offen offset:1024 nt lds
v_mfma_f32_16x16x32_bf16 v[172:175],a[148:151],v[84:87],v[172:175]
v_exp_f32 v154,v154
v_mfma_f32_16x16x32_bf16 v[172:175],a[144:147],v[80:83],v[172:175]
v_exp_f32 v155,v155
v_mfma_f32_16x16x32_bf16 v[184:187],a[160:163],v[80:83],v[36:39]
v_exp_f32 v156,v156
v_mfma_f32_16x16x32_bf16 v[184:187],a[164:167],v[84:87],v[184:187]
v_exp_f32 v157,v157
v_mfma_f32_16x16x32_bf16 v[184:187],a[168:171],v[88:91],v[184:187]
v_exp_f32 v158,v158
v_mfma_f32_16x16x32_bf16 v[184:187],a[172:175],v[92:95],v[184:187]
v_exp_f32 v159,v159
v_mfma_f32_16x16x32_bf16 v[188:191],a[188:191],v[92:95],v[36:39]
v_cvt_pk_bf16_f32 v140,v152,v153
v_cvt_pk_bf16_f32 v141,v154,v155
v_mfma_f32_16x16x32_bf16 v[188:191],a[184:187],v[88:91],v[188:191]
v_cvt_pk_bf16_f32 v142,v156,v157
v_cvt_pk_bf16_f32 v143,v158,v159
v_mfma_f32_16x16x32_bf16 v[188:191],a[180:183],v[84:87],v[188:191]
ds_read_b64_tr_b16 a[246:247],v2 offset:13056
ds_read_b64_tr_b16 a[252:253],v3 offset:8960
v_mfma_f32_16x16x32_bf16 v[188:191],a[176:179],v[80:83],v[188:191]
ds_read_b64_tr_b16 a[254:255],v3 offset:13056
v_mov_b32 v49,0
v_mfma_f32_16x16x32_bf16 v[200:203],a[128:131],v[96:99],v[40:43]
ds_read_b64_tr_b16 a[248:249],v3 offset:768
v_mov_b32 v50,0
v_mfma_f32_16x16x32_bf16 v[200:203],a[132:135],v[100:103],v[200:203]
ds_read_b64_tr_b16 a[250:251],v3 offset:4864
v_mov_b32 v51,0
v_mfma_f32_16x16x32_bf16 v[200:203],a[136:139],v[104:107],v[200:203]
buffer_load_dwordx4 v4,s[24:27],s47 offen offset:2048 nt lds
v_mfma_f32_16x16x32_bf16 v[200:203],a[140:143],v[108:111],v[200:203]
v_sub_f32 v47,0,v44
v_sub_f32 v46,0,v44
v_mfma_f32_16x16x32_bf16 v[204:207],a[156:159],v[108:111],v[40:43]
v_sub_f32 v45,0,v44
v_sub_f32 v44,0,v44
v_mfma_f32_16x16x32_bf16 v[204:207],a[152:155],v[104:107],v[204:207]
v_mul_f32 v224,s61,v224
v_mul_f32 v225,s61,v225
v_mul_f32 v226,s61,v226
v_mul_f32 v227,s61,v227
v_cvt_pk_bf16_f32 v224,v224,v225
v_cvt_pk_bf16_f32 v225,v226,v227
v_pk_mul_f32 v[228:229],s[60:61],v[228:229] op_sel:[1,0] op_sel_hi:[1,1]
v_pk_mul_f32 v[230:231],s[60:61],v[230:231] op_sel:[1,0] op_sel_hi:[1,1]
v_pk_mul_f32 v[232:233],s[60:61],v[232:233] op_sel:[1,0] op_sel_hi:[1,1]
v_pk_mul_f32 v[234:235],s[60:61],v[234:235] op_sel:[1,0] op_sel_hi:[1,1]
v_pk_mul_f32 v[236:237],s[60:61],v[236:237] op_sel:[1,0] op_sel_hi:[1,1]
v_pk_mul_f32 v[238:239],s[60:61],v[238:239] op_sel:[1,0] op_sel_hi:[1,1]
v_pk_mul_f32 v[240:241],s[60:61],v[240:241] op_sel:[1,0] op_sel_hi:[1,1]
v_pk_mul_f32 v[242:243],s[60:61],v[242:243] op_sel:[1,0] op_sel_hi:[1,1]
v_pk_mul_f32 v[244:245],s[60:61],v[244:245] op_sel:[1,0] op_sel_hi:[1,1]
v_pk_mul_f32 v[246:247],s[60:61],v[246:247] op_sel:[1,0] op_sel_hi:[1,1]
v_pk_mul_f32 v[248:249],s[60:61],v[248:249] op_sel:[1,0] op_sel_hi:[1,1]
v_pk_mul_f32 v[250:251],s[60:61],v[250:251] op_sel:[1,0] op_sel_hi:[1,1]
v_pk_mul_f32 v[252:253],s[60:61],v[252:253] op_sel:[1,0] op_sel_hi:[1,1]
v_pk_mul_f32 v[254:255],s[60:61],v[254:255] op_sel:[1,0] op_sel_hi:[1,1]
v_mfma_f32_16x16x32_bf16 v[204:207],a[148:151],v[100:103],v[204:207]
v_cvt_pk_bf16_f32 v226,v228,v229
v_cvt_pk_bf16_f32 v228,v232,v233
v_mfma_f32_16x16x32_bf16 v[204:207],a[144:147],v[96:99],v[204:207]
v_cvt_pk_bf16_f32 v229,v234,v235
v_cvt_pk_bf16_f32 v227,v230,v231
v_mfma_f32_16x16x32_bf16 v[216:219],a[160:163],v[96:99],v[40:43]
v_cvt_pk_bf16_f32 v230,v236,v237
v_cvt_pk_bf16_f32 v231,v238,v239
v_mfma_f32_16x16x32_bf16 v[216:219],a[164:167],v[100:103],v[216:219]
v_cvt_pk_bf16_f32 v240,v240,v241
v_cvt_pk_bf16_f32 v241,v242,v243
v_mfma_f32_16x16x32_bf16 v[216:219],a[168:171],v[104:107],v[216:219]
v_cvt_pk_bf16_f32 v242,v244,v245
v_cvt_pk_bf16_f32 v244,v248,v249
v_mfma_f32_16x16x32_bf16 v[216:219],a[172:175],v[108:111],v[216:219]
v_cvt_pk_bf16_f32 v245,v250,v251
v_cvt_pk_bf16_f32 v243,v246,v247
v_mfma_f32_16x16x32_bf16 v[220:223],a[188:191],v[108:111],v[40:43]
v_cvt_pk_bf16_f32 v246,v252,v253
v_cvt_pk_bf16_f32 v247,v254,v255
v_mfma_f32_16x16x32_bf16 v[220:223],a[184:187],v[104:107],v[220:223]
v_exp_f32 v168,v168
v_mfma_f32_16x16x32_bf16 v[220:223],a[180:183],v[100:103],v[220:223]
v_exp_f32 v169,v169
v_mfma_f32_16x16x32_bf16 v[220:223],a[176:179],v[96:99],v[220:223]
buffer_load_dwordx4 v5,s[24:27],s47 offen offset:3072 nt lds
v_mfma_f32_16x16x32_bf16 a[0:3],a[192:195],v[136:139],0
s_or_b32 s47,s47,0x18000
s_addk_i32 s47,4096
s_and_b32 s47,s47,0x27000
v_mfma_f32_16x16x32_bf16 a[0:3],a[196:199],v[140:143],a[0:3]
s_or_b32 s47,s47,s46
v_exp_f32 v170,v170
v_mfma_f32_16x16x32_bf16 a[4:7],a[204:207],v[140:143],0
s_and_b32 s46,s54,0xfffc0000
v_exp_f32 v171,v171
v_mfma_f32_16x16x32_bf16 a[4:7],a[200:203],v[136:139],a[4:7]
s_memrealtime s[76:77]
v_cvt_pk_bf16_f32 v168,v168,v169
v_cvt_pk_bf16_f32 v169,v170,v171
v_mfma_f32_16x16x32_bf16 a[8:11],a[208:211],v[136:139],0
v_exp_f32 v172,v172
v_mfma_f32_16x16x32_bf16 a[8:11],a[212:215],v[140:143],a[8:11]
v_exp_f32 v173,v173
v_mfma_f32_16x16x32_bf16 a[12:15],a[220:223],v[140:143],0
v_exp_f32 v174,v174
v_mfma_f32_16x16x32_bf16 a[12:15],a[216:219],v[136:139],a[12:15]
v_exp_f32 v175,v175
v_mfma_f32_16x16x32_bf16 a[16:19],a[224:227],v[136:139],0
v_cvt_pk_bf16_f32 v170,v172,v173
v_cvt_pk_bf16_f32 v171,v174,v175
v_mfma_f32_16x16x32_bf16 a[16:19],a[228:231],v[140:143],a[16:19]
v_exp_f32 v184,v184
v_mfma_f32_16x16x32_bf16 a[20:23],a[236:239],v[140:143],0
s_waitcnt lgkmcnt(0)
v_exp_f32 v185,v185
v_mfma_f32_16x16x32_bf16 a[20:23],a[232:235],v[136:139],a[20:23]
s_add_u32 s78,s76,s60
v_exp_f32 v186,v186
v_mfma_f32_16x16x32_bf16 a[24:27],a[240:243],v[136:139],0
s_lshr_b32 s60,s94,2
v_exp_f32 v187,v187
v_mfma_f32_16x16x32_bf16 a[24:27],a[244:247],v[140:143],a[24:27]
s_max_i32 s60,s60,124
v_exp_f32 v188,v188
v_mfma_f32_16x16x32_bf16 a[28:31],a[252:255],v[140:143],0
v_exp_f32 v189,v189
v_mfma_f32_16x16x32_bf16 a[28:31],a[248:251],v[136:139],a[28:31]
v_exp_f32 v190,v190
v_smfmac_f32_16x16x64_bf16 v[48:51],v[28:31],v[136:143],v27
v_cvt_pk_bf16_f32 v172,v184,v185
v_mfma_f32_16x16x32_bf16 v[232:235],a[128:131],v[112:115],v[44:47]
s_waitcnt vmcnt(8)
v_exp_f32 v191,v191
v_mfma_f32_16x16x32_bf16 v[232:235],a[132:135],v[116:119],v[232:235]
s_barrier
s_add_u32 m0,65536,s51
v_cvt_pk_bf16_f32 v173,v186,v187
v_mfma_f32_16x16x32_bf16 v[232:235],a[136:139],v[120:123],v[232:235]
v_cvt_pk_bf16_f32 v174,v188,v189
v_cvt_pk_bf16_f32 v175,v190,v191
v_mfma_f32_16x16x32_bf16 v[232:235],a[140:143],v[124:127],v[232:235]
ds_read_b128 a[128:131],v1
v_mov_b32 v52,0
v_mfma_f32_16x16x32_bf16 v[236:239],a[156:159],v[124:127],v[44:47]
buffer_load_dwordx4 v4,s[20:23],s47 offen nt lds
v_mfma_f32_16x16x32_bf16 v[236:239],a[152:155],v[120:123],v[236:239]
ds_read_b128 a[132:135],v1 offset:256
v_mov_b32 v53,0
v_mfma_f32_16x16x32_bf16 v[236:239],a[148:151],v[116:119],v[236:239]
v_mov_b32 v54,0
v_mov_b32 v55,0
v_mfma_f32_16x16x32_bf16 v[236:239],a[144:147],v[112:115],v[236:239]
ds_read_b128 a[136:139],v1 offset:512
v_mov_b32 v56,0
v_mfma_f32_16x16x32_bf16 v[248:251],a[160:163],v[112:115],v[44:47]
v_exp_f32 v200,v200
v_mfma_f32_16x16x32_bf16 v[248:251],a[164:167],v[116:119],v[248:251]
ds_read_b128 a[140:143],v1 offset:768
v_mov_b32 v57,0
v_mfma_f32_16x16x32_bf16 v[248:251],a[168:171],v[120:123],v[248:251]
v_exp_f32 v201,v201
v_mfma_f32_16x16x32_bf16 v[248:251],a[172:175],v[124:127],v[248:251]
ds_read_b128 a[156:159],v1 offset:4864
v_cvt_pk_bf16_f32 v200,v200,v201
v_mfma_f32_16x16x32_bf16 v[252:255],a[188:191],v[124:127],v[44:47]
v_exp_f32 v202,v202
v_mfma_f32_16x16x32_bf16 v[252:255],a[184:187],v[120:123],v[252:255]
ds_read_b128 a[152:155],v1 offset:4608
v_mov_b32 v58,0
v_mfma_f32_16x16x32_bf16 v[252:255],a[180:183],v[116:119],v[252:255]
v_exp_f32 v203,v203
v_mfma_f32_16x16x32_bf16 v[252:255],a[176:179],v[112:115],v[252:255]
ds_read_b128 a[148:151],v1 offset:4352
v_cvt_pk_bf16_f32 v201,v202,v203
v_mfma_f32_16x16x32_bf16 a[32:35],a[192:195],v[168:171],0
v_exp_f32 v204,v204
v_mfma_f32_16x16x32_bf16 a[32:35],a[196:199],v[172:175],a[32:35]
ds_read_b128 a[144:147],v1 offset:4096
v_mov_b32 v59,0
v_mfma_f32_16x16x32_bf16 a[36:39],a[204:207],v[172:175],0
buffer_load_dwordx4 v4,s[20:23],s47 offen offset:1024 nt lds
v_mfma_f32_16x16x32_bf16 a[36:39],a[200:203],v[168:171],a[36:39]
ds_read_b128 a[160:163],v1 offset:8192
v_mov_b32 v60,0
v_mfma_f32_16x16x32_bf16 a[40:43],a[208:211],v[168:171],0
v_exp_f32 v205,v205
v_mfma_f32_16x16x32_bf16 a[40:43],a[212:215],v[172:175],a[40:43]
ds_read_b128 a[164:167],v1 offset:8448
v_cvt_pk_bf16_f32 v202,v204,v205
v_mfma_f32_16x16x32_bf16 a[44:47],a[220:223],v[172:175],0
v_exp_f32 v206,v206
v_mfma_f32_16x16x32_bf16 a[44:47],a[216:219],v[168:171],a[44:47]
ds_read_b128 a[168:171],v1 offset:8704
v_mov_b32 v61,0
v_mfma_f32_16x16x32_bf16 a[48:51],a[224:227],v[168:171],0
)A"
R"A(v_exp_f32 v207,v207
v_mfma_f32_16x16x32_bf16 a[48:51],a[228:231],v[172:175],a[48:51]
ds_read_b128 a[172:175],v1 offset:8960
v_cvt_pk_bf16_f32 v203,v206,v207
v_mfma_f32_16x16x32_bf16 a[52:55],a[236:239],v[172:175],0
v_exp_f32 v216,v216
v_mfma_f32_16x16x32_bf16 a[52:55],a[232:235],v[168:171],a[52:55]
ds_read_b128 a[188:191],v1 offset:13056
v_mov_b32 v62,0
v_mfma_f32_16x16x32_bf16 a[56:59],a[240:243],v[168:171],0
v_exp_f32 v217,v217
v_mfma_f32_16x16x32_bf16 a[56:59],a[244:247],v[172:175],a[56:59]
ds_read_b128 a[184:187],v1 offset:12800
v_cvt_pk_bf16_f32 v204,v216,v217
v_mfma_f32_16x16x32_bf16 a[60:63],a[252:255],v[172:175],0
v_exp_f32 v218,v218
v_mfma_f32_16x16x32_bf16 a[60:63],a[248:251],v[168:171],a[60:63]
ds_read_b128 a[180:183],v1 offset:12544
v_mov_b32 v63,0
v_smfmac_f32_16x16x64_bf16 v[52:55],v[28:31],v[168:175],v27
s_waitcnt lgkmcnt(4)
v_add_u32 v2,s48,v2
v_mfma_f32_16x16x32_bf16 v[136:139],a[128:131],v[64:67],v[32:35]
buffer_load_dwordx4 v5,s[20:23],s47 offen offset:2048 nt lds
v_mfma_f32_16x16x32_bf16 v[136:139],a[132:135],v[68:71],v[136:139]
ds_read_b128 a[176:179],v1 offset:12288
v_add_u32 v3,s48,v3
v_mfma_f32_16x16x32_bf16 v[136:139],a[136:139],v[72:75],v[136:139]
s_mov_b32 s48,32768
v_exp_f32 v219,v219
v_mfma_f32_16x16x32_bf16 v[136:139],a[140:143],v[76:79],v[136:139]
v_exp_f32 v220,v220
v_mfma_f32_16x16x32_bf16 v[140:143],a[156:159],v[76:79],v[32:35]
v_exp_f32 v221,v221
v_mfma_f32_16x16x32_bf16 v[140:143],a[152:155],v[72:75],v[140:143]
v_exp_f32 v222,v222
v_mfma_f32_16x16x32_bf16 v[140:143],a[148:151],v[68:71],v[140:143]
v_exp_f32 v223,v223
v_mfma_f32_16x16x32_bf16 v[140:143],a[144:147],v[64:67],v[140:143]
v_cvt_pk_bf16_f32 v205,v218,v219
v_cvt_pk_bf16_f32 v206,v220,v221
v_mfma_f32_16x16x32_bf16 v[152:155],a[160:163],v[64:67],v[32:35]
v_cvt_pk_bf16_f32 v207,v222,v223
v_add_u32 v1,s48,v1
v_mfma_f32_16x16x32_bf16 v[152:155],a[164:167],v[68:71],v[152:155]
v_exp_f32 v232,v232
v_mfma_f32_16x16x32_bf16 v[152:155],a[168:171],v[72:75],v[152:155]
s_waitcnt lgkmcnt(0)
v_exp_f32 v233,v233
v_mfma_f32_16x16x32_bf16 v[152:155],a[172:175],v[76:79],v[152:155]
v_exp_f32 v234,v234
v_mfma_f32_16x16x32_bf16 v[156:159],a[188:191],v[76:79],v[32:35]
v_exp_f32 v235,v235
v_mfma_f32_16x16x32_bf16 v[156:159],a[184:187],v[72:75],v[156:159]
buffer_load_dwordx4 v5,s[20:23],s47 offen offset:3072 nt lds
v_mfma_f32_16x16x32_bf16 v[156:159],a[180:183],v[68:71],v[156:159]
v_exp_f32 v236,v236
v_mfma_f32_16x16x32_bf16 v[156:159],a[176:179],v[64:67],v[156:159]
v_exp_f32 v237,v237
v_mfma_f32_16x16x32_bf16 a[64:67],a[192:195],v[200:203],0
v_exp_f32 v238,v238
v_mfma_f32_16x16x32_bf16 a[64:67],a[196:199],v[204:207],a[64:67]
v_exp_f32 v239,v239
v_mfma_f32_16x16x32_bf16 a[68:71],a[204:207],v[204:207],0
v_exp_f32 v248,v248
v_mfma_f32_16x16x32_bf16 a[68:71],a[200:203],v[200:203],a[68:71]
v_exp_f32 v249,v249
v_mfma_f32_16x16x32_bf16 a[72:75],a[208:211],v[200:203],0
v_exp_f32 v250,v250
v_mfma_f32_16x16x32_bf16 a[72:75],a[212:215],v[204:207],a[72:75]
v_exp_f32 v251,v251
v_mfma_f32_16x16x32_bf16 a[76:79],a[220:223],v[204:207],0
v_exp_f32 v252,v252
v_mfma_f32_16x16x32_bf16 a[76:79],a[216:219],v[200:203],a[76:79]
v_exp_f32 v253,v253
v_mfma_f32_16x16x32_bf16 a[80:83],a[224:227],v[200:203],0
s_addk_i32 m0,16384
v_exp_f32 v254,v254
v_mfma_f32_16x16x32_bf16 a[80:83],a[228:231],v[204:207],a[80:83]
v_exp_f32 v255,v255
v_mfma_f32_16x16x32_bf16 a[84:87],a[236:239],v[204:207],0
v_exp_f32 v136,v136
v_mfma_f32_16x16x32_bf16 a[84:87],a[232:235],v[200:203],a[84:87]
buffer_load_dwordx4 v4,s[24:27],s47 offen nt lds
v_mfma_f32_16x16x32_bf16 a[88:91],a[240:243],v[200:203],0
v_cvt_pk_bf16_f32 v232,v232,v233
v_cvt_pk_bf16_f32 v233,v234,v235
v_mfma_f32_16x16x32_bf16 a[88:91],a[244:247],v[204:207],a[88:91]
v_cvt_pk_bf16_f32 v234,v236,v237
v_cvt_pk_bf16_f32 v235,v238,v239
v_mfma_f32_16x16x32_bf16 a[92:95],a[252:255],v[204:207],0
v_cvt_pk_bf16_f32 v236,v248,v249
v_cvt_pk_bf16_f32 v237,v250,v251
v_mfma_f32_16x16x32_bf16 a[92:95],a[248:251],v[200:203],a[92:95]
v_cvt_pk_bf16_f32 v238,v252,v253
v_cvt_pk_bf16_f32 v239,v254,v255
v_smfmac_f32_16x16x64_bf16 v[56:59],v[28:31],v[200:207],v27
v_mfma_f32_16x16x32_bf16 v[168:171],a[128:131],v[80:83],v[36:39]
v_exp_f32 v137,v137
v_mfma_f32_16x16x32_bf16 v[168:171],a[132:135],v[84:87],v[168:171]
v_exp_f32 v138,v138
v_mfma_f32_16x16x32_bf16 v[168:171],a[136:139],v[88:91],v[168:171]
v_exp_f32 v139,v139
v_mfma_f32_16x16x32_bf16 v[168:171],a[140:143],v[92:95],v[168:171]
v_exp_f32 v140,v140
v_mfma_f32_16x16x32_bf16 v[172:175],a[156:159],v[92:95],v[36:39]
v_exp_f32 v141,v141
v_mfma_f32_16x16x32_bf16 v[172:175],a[152:155],v[88:91],v[172:175]
v_exp_f32 v142,v142
v_mfma_f32_16x16x32_bf16 v[172:175],a[148:151],v[84:87],v[172:175]
v_exp_f32 v143,v143
v_mfma_f32_16x16x32_bf16 v[172:175],a[144:147],v[80:83],v[172:175]
v_exp_f32 v152,v152
v_mfma_f32_16x16x32_bf16 v[184:187],a[160:163],v[80:83],v[36:39]
buffer_load_dwordx4 v5,s[24:27],s47 offen offset:1024 nt lds
v_mfma_f32_16x16x32_bf16 v[184:187],a[164:167],v[84:87],v[184:187]
v_exp_f32 v153,v153
v_mfma_f32_16x16x32_bf16 v[184:187],a[168:171],v[88:91],v[184:187]
v_exp_f32 v154,v154
v_mfma_f32_16x16x32_bf16 v[184:187],a[172:175],v[92:95],v[184:187]
v_exp_f32 v155,v155
v_mfma_f32_16x16x32_bf16 v[188:191],a[188:191],v[92:95],v[36:39]
v_exp_f32 v156,v156
v_mfma_f32_16x16x32_bf16 v[188:191],a[184:187],v[88:91],v[188:191]
v_exp_f32 v157,v157
v_mfma_f32_16x16x32_bf16 v[188:191],a[180:183],v[84:87],v[188:191]
v_exp_f32 v158,v158
v_mfma_f32_16x16x32_bf16 v[188:191],a[176:179],v[80:83],v[188:191]
v_exp_f32 v159,v159
v_mfma_f32_16x16x32_bf16 a[96:99],a[192:195],v[232:235],0
ds_read_b64_tr_b16 a[192:193],v2
ds_read_b64_tr_b16 a[194:195],v2 offset:4096
v_mfma_f32_16x16x32_bf16 a[96:99],a[196:199],v[236:239],a[96:99]
ds_read_b64_tr_b16 a[196:197],v2 offset:8192
v_cvt_pk_bf16_f32 v136,v136,v137
v_mfma_f32_16x16x32_bf16 a[100:103],a[204:207],v[236:239],0
ds_read_b64_tr_b16 a[198:199],v2 offset:12288
v_cvt_pk_bf16_f32 v137,v138,v139
v_mfma_f32_16x16x32_bf16 a[100:103],a[200:203],v[232:235],a[100:103]
ds_read_b64_tr_b16 a[204:205],v3 offset:8192
v_cvt_pk_bf16_f32 v138,v140,v141
v_mfma_f32_16x16x32_bf16 a[104:107],a[208:211],v[232:235],0
ds_read_b64_tr_b16 a[206:207],v3 offset:12288
ds_read_b64_tr_b16 a[200:201],v3
v_mfma_f32_16x16x32_bf16 a[104:107],a[212:215],v[236:239],a[104:107]
ds_read_b64_tr_b16 a[202:203],v3 offset:4096
v_cvt_pk_bf16_f32 v139,v142,v143
v_mfma_f32_16x16x32_bf16 a[108:111],a[220:223],v[236:239],0
buffer_load_dwordx4 v4,s[24:27],s47 offen offset:2048 nt lds
v_mfma_f32_16x16x32_bf16 a[108:111],a[216:219],v[232:235],a[108:111]
ds_read_b64_tr_b16 a[208:209],v2 offset:256
ds_read_b64_tr_b16 a[210:211],v2 offset:4352
v_mfma_f32_16x16x32_bf16 a[112:115],a[224:227],v[232:235],0
ds_read_b64_tr_b16 a[212:213],v2 offset:8448
v_cvt_pk_bf16_f32 v140,v152,v153
v_mfma_f32_16x16x32_bf16 a[112:115],a[228:231],v[236:239],a[112:115]
ds_read_b64_tr_b16 a[214:215],v2 offset:12544
v_cvt_pk_bf16_f32 v141,v154,v155
v_mfma_f32_16x16x32_bf16 a[116:119],a[236:239],v[236:239],0
ds_read_b64_tr_b16 a[220:221],v3 offset:8448
v_cvt_pk_bf16_f32 v142,v156,v157
v_mfma_f32_16x16x32_bf16 a[116:119],a[232:235],v[232:235],a[116:119]
ds_read_b64_tr_b16 a[222:223],v3 offset:12544
v_cvt_pk_bf16_f32 v143,v158,v159
v_mfma_f32_16x16x32_bf16 a[120:123],a[240:243],v[232:235],0
ds_read_b64_tr_b16 a[216:217],v3 offset:256
ds_read_b64_tr_b16 a[218:219],v3 offset:4352
v_mfma_f32_16x16x32_bf16 a[120:123],a[244:247],v[236:239],a[120:123]
v_exp_f32 v168,v168
v_mfma_f32_16x16x32_bf16 a[124:127],a[252:255],v[236:239],0
ds_read_b64_tr_b16 a[224:225],v2 offset:512
ds_read_b64_tr_b16 a[226:227],v2 offset:4608
v_mfma_f32_16x16x32_bf16 a[124:127],a[248:251],v[232:235],a[124:127]
v_exp_f32 v169,v169
v_smfmac_f32_16x16x64_bf16 v[60:63],v[28:31],v[232:239],v27
ds_read_b64_tr_b16 a[228:229],v2 offset:8704
v_mfma_f32_16x16x32_bf16 v[200:203],a[128:131],v[96:99],v[40:43]
ds_read_b64_tr_b16 a[230:231],v2 offset:12800
ds_read_b64_tr_b16 a[236:237],v3 offset:8704
v_mfma_f32_16x16x32_bf16 v[200:203],a[132:135],v[100:103],v[200:203]
ds_read_b64_tr_b16 a[238:239],v3 offset:12800
v_cvt_pk_bf16_f32 v168,v168,v169
v_mfma_f32_16x16x32_bf16 v[200:203],a[136:139],v[104:107],v[200:203]
ds_read_b64_tr_b16 a[232:233],v3 offset:512
v_mfma_f32_16x16x32_bf16 v[200:203],a[140:143],v[108:111],v[200:203]
buffer_load_dwordx4 v5,s[24:27],s47 offen offset:3072 nt lds
v_mfma_f32_16x16x32_bf16 v[204:207],a[156:159],v[108:111],v[40:43]
s_or_b32 s47,s47,0x18000
ds_read_b64_tr_b16 a[234:235],v3 offset:4608
ds_read_b64_tr_b16 a[240:241],v2 offset:768
v_mfma_f32_16x16x32_bf16 v[204:207],a[152:155],v[104:107],v[204:207]
s_addk_i32 s47,4096
s_and_b32 s47,s47,0x27000
ds_read_b64_tr_b16 a[242:243],v2 offset:4864
v_mfma_f32_16x16x32_bf16 v[204:207],a[148:151],v[100:103],v[204:207]
s_or_b32 s47,s47,s46
ds_read_b64_tr_b16 a[244:245],v2 offset:8960
v_mfma_f32_16x16x32_bf16 v[204:207],a[144:147],v[96:99],v[204:207]
ds_read_b64_tr_b16 a[246:247],v2 offset:13056
v_mfma_f32_16x16x32_bf16 v[216:219],a[160:163],v[96:99],v[40:43]
ds_read_b64_tr_b16 a[252:253],v3 offset:8960
ds_read_b64_tr_b16 a[254:255],v3 offset:13056
v_mfma_f32_16x16x32_bf16 v[216:219],a[164:167],v[100:103],v[216:219]
ds_read_b64_tr_b16 a[248:249],v3 offset:768
v_mfma_f32_16x16x32_bf16 v[216:219],a[168:171],v[104:107],v[216:219]
ds_read_b64_tr_b16 a[250:251],v3 offset:4864
v_mfma_f32_16x16x32_bf16 v[216:219],a[172:175],v[108:111],v[216:219]
v_exp_f32 v170,v170
v_mfma_f32_16x16x32_bf16 v[220:223],a[188:191],v[108:111],v[40:43]
v_exp_f32 v171,v171
v_mfma_f32_16x16x32_bf16 v[220:223],a[184:187],v[104:107],v[220:223]
v_exp_f32 v172,v172
v_mfma_f32_16x16x32_bf16 v[220:223],a[180:183],v[100:103],v[220:223]
v_exp_f32 v173,v173
v_mfma_f32_16x16x32_bf16 v[220:223],a[176:179],v[96:99],v[220:223]
s_waitcnt lgkmcnt(4)
v_exp_f32 v174,v174
v_mfma_f32_16x16x32_bf16 a[0:3],a[192:195],v[136:139],a[0:3]
v_exp_f32 v175,v175
v_mfma_f32_16x16x32_bf16 a[0:3],a[196:199],v[140:143],a[0:3]
v_cvt_pk_bf16_f32 v169,v170,v171
v_cvt_pk_bf16_f32 v170,v172,v173
v_mfma_f32_16x16x32_bf16 a[4:7],a[204:207],v[140:143],a[4:7]
v_exp_f32 v184,v184
v_mfma_f32_16x16x32_bf16 a[4:7],a[200:203],v[136:139],a[4:7]
s_memrealtime s[76:77]
v_exp_f32 v185,v185
v_mfma_f32_16x16x32_bf16 a[8:11],a[208:211],v[136:139],a[8:11]
v_exp_f32 v186,v186
v_mfma_f32_16x16x32_bf16 a[8:11],a[212:215],v[140:143],a[8:11]
v_exp_f32 v187,v187
v_mfma_f32_16x16x32_bf16 a[12:15],a[220:223],v[140:143],a[12:15]
v_exp_f32 v188,v188
v_mfma_f32_16x16x32_bf16 a[12:15],a[216:219],v[136:139],a[12:15]
v_exp_f32 v189,v189
v_mfma_f32_16x16x32_bf16 a[16:19],a[224:227],v[136:139],a[16:19]
v_exp_f32 v190,v190
v_mfma_f32_16x16x32_bf16 a[16:19],a[228:231],v[140:143],a[16:19]
v_exp_f32 v191,v191
v_mfma_f32_16x16x32_bf16 a[20:23],a[236:239],v[140:143],a[20:23]
s_waitcnt lgkmcnt(0)
v_cvt_pk_bf16_f32 v171,v174,v175
v_cvt_pk_bf16_f32 v172,v184,v185
v_mfma_f32_16x16x32_bf16 a[20:23],a[232:235],v[136:139],a[20:23]
s_sub_u32 s79,s78,s76
v_cvt_pk_bf16_f32 v173,v186,v187
v_cvt_pk_bf16_f32 v174,v188,v189
v_mfma_f32_16x16x32_bf16 a[24:27],a[240:243],v[136:139],a[24:27]
s_add_u32 s78,s76,s60
v_cvt_pk_bf16_f32 v175,v190,v191
v_mfma_f32_16x16x32_bf16 a[24:27],a[244:247],v[140:143],a[24:27]
s_cmp_gt_i32 s79,s95
s_cbranch_scc1 L_ps2_%=
L_pr2_%=:
v_mfma_f32_16x16x32_bf16 a[28:31],a[252:255],v[140:143],a[28:31]
v_mfma_f32_16x16x32_bf16 a[28:31],a[248:251],v[136:139],a[28:31]
v_smfmac_f32_16x16x64_bf16 v[48:51],v[28:31],v[136:143],v27
s_mov_b32 s53,13
s_cmp_eq_u32 s40,3
s_cbranch_scc0 L_att0_%=
s_cmp_gt_i32 s18,0
s_cbranch_scc1 L_att0v0_%=
s_cmp_gt_i32 s19,0
s_cbranch_scc1 L_att0v1_%=
s_cmp_gt_i32 s100,0
s_cbranch_scc1 L_att0v2_%=
.p2align 5
L_att0v3_%=:
s_waitcnt vmcnt(8) lgkmcnt(0)
v_exp_f32 v200,v200
s_barrier
s_add_u32 m0,s49,s51
ds_read_b128 a[128:131],v1
s_mov_b32 s90,L_endseg_%=-L_pc_%=+128
v_exp_f32 v201,v201
s_lshr_b32 s45,s45,1
ds_read_b128 a[132:135],v1 offset:256
v_cvt_pk_bf16_f32 v200,v200,v201
buffer_load_dwordx4 v4,s[20:23],s47 offen nt lds
ds_read_b128 a[136:139],v1 offset:512
v_add_u32 v2,s48,v2
)A"
R"A(v_exp_f32 v202,v202
s_add_u32 s62,s50,32768
ds_read_b128 a[140:143],v1 offset:768
v_add_u32 v3,s48,v3
s_and_b32 s62,s62,131071
v_exp_f32 v203,v203
s_addk_i32 s54,16384
ds_read_b128 a[156:159],v1 offset:4864
v_cvt_pk_bf16_f32 v201,v202,v203
s_sub_u32 s48,s62,s50
v_exp_f32 v204,v204
s_sub_u32 s53,s53,1
v_exp_f32 v205,v205
s_and_b32 s46,s54,0xfffc0000
ds_read_b128 a[152:155],v1 offset:4608
v_cvt_pk_bf16_f32 v202,v204,v205
s_mov_b32 s50,s62
v_exp_f32 v206,v206
s_xor_b32 s49,s50,65536
v_exp_f32 v207,v207
ds_read_b128 a[148:151],v1 offset:4352
v_cvt_pk_bf16_f32 v203,v206,v207
v_mfma_f32_16x16x32_bf16 a[32:35],a[192:195],v[168:171],a[32:35]
v_exp_f32 v216,v216
v_mfma_f32_16x16x32_bf16 a[32:35],a[196:199],v[172:175],a[32:35]
ds_read_b128 a[144:147],v1 offset:4096
v_mfma_f32_16x16x32_bf16 a[36:39],a[204:207],v[172:175],a[36:39]
buffer_load_dwordx4 v4,s[20:23],s47 offen offset:1024 nt lds
v_mfma_f32_16x16x32_bf16 a[36:39],a[200:203],v[168:171],a[36:39]
ds_read_b128 a[160:163],v1 offset:8192
v_mfma_f32_16x16x32_bf16 a[40:43],a[208:211],v[168:171],a[40:43]
v_exp_f32 v217,v217
v_mfma_f32_16x16x32_bf16 a[40:43],a[212:215],v[172:175],a[40:43]
ds_read_b128 a[164:167],v1 offset:8448
v_cvt_pk_bf16_f32 v204,v216,v217
v_mfma_f32_16x16x32_bf16 a[44:47],a[220:223],v[172:175],a[44:47]
v_exp_f32 v218,v218
v_mfma_f32_16x16x32_bf16 a[44:47],a[216:219],v[168:171],a[44:47]
ds_read_b128 a[168:171],v1 offset:8704
v_mfma_f32_16x16x32_bf16 a[48:51],a[224:227],v[168:171],a[48:51]
v_exp_f32 v219,v219
v_mfma_f32_16x16x32_bf16 a[48:51],a[228:231],v[172:175],a[48:51]
ds_read_b128 a[172:175],v1 offset:8960
v_cvt_pk_bf16_f32 v205,v218,v219
v_mfma_f32_16x16x32_bf16 a[52:55],a[236:239],v[172:175],a[52:55]
v_exp_f32 v220,v220
v_mfma_f32_16x16x32_bf16 a[52:55],a[232:235],v[168:171],a[52:55]
ds_read_b128 a[188:191],v1 offset:13056
v_mfma_f32_16x16x32_bf16 a[56:59],a[240:243],v[168:171],a[56:59]
v_exp_f32 v221,v221
v_mfma_f32_16x16x32_bf16 a[56:59],a[244:247],v[172:175],a[56:59]
ds_read_b128 a[184:187],v1 offset:12800
v_cvt_pk_bf16_f32 v206,v220,v221
v_mfma_f32_16x16x32_bf16 a[60:63],a[252:255],v[172:175],a[60:63]
v_exp_f32 v222,v222
v_mfma_f32_16x16x32_bf16 a[60:63],a[248:251],v[168:171],a[60:63]
ds_read_b128 a[180:183],v1 offset:12544
v_smfmac_f32_16x16x64_bf16 v[52:55],v[28:31],v[168:175],v27
s_waitcnt lgkmcnt(4)
v_mfma_f32_16x16x32_bf16 v[136:139],a[128:131],v[64:67],v[32:35]
buffer_load_dwordx4 v5,s[20:23],s47 offen offset:2048 nt lds
v_mfma_f32_16x16x32_bf16 v[136:139],a[132:135],v[68:71],v[136:139]
ds_read_b128 a[176:179],v1 offset:12288
v_add_u32 v1,s48,v1
v_mfma_f32_16x16x32_bf16 v[136:139],a[136:139],v[72:75],v[136:139]
v_exp_f32 v223,v223
v_mfma_f32_16x16x32_bf16 v[136:139],a[140:143],v[76:79],v[136:139]
v_cvt_pk_bf16_f32 v207,v222,v223
v_mfma_f32_16x16x32_bf16 v[140:143],a[156:159],v[76:79],v[32:35]
v_mfma_f32_16x16x32_bf16 v[140:143],a[152:155],v[72:75],v[140:143]
v_mfma_f32_16x16x32_bf16 v[140:143],a[148:151],v[68:71],v[140:143]
v_mfma_f32_16x16x32_bf16 v[140:143],a[144:147],v[64:67],v[140:143]
v_mfma_f32_16x16x32_bf16 v[152:155],a[160:163],v[64:67],v[32:35]
v_mfma_f32_16x16x32_bf16 v[152:155],a[164:167],v[68:71],v[152:155]
v_mfma_f32_16x16x32_bf16 v[152:155],a[168:171],v[72:75],v[152:155]
s_waitcnt lgkmcnt(0)
v_mfma_f32_16x16x32_bf16 v[152:155],a[172:175],v[76:79],v[152:155]
v_mfma_f32_16x16x32_bf16 v[156:159],a[188:191],v[76:79],v[32:35]
v_mfma_f32_16x16x32_bf16 v[156:159],a[184:187],v[72:75],v[156:159]
buffer_load_dwordx4 v5,s[20:23],s47 offen offset:3072 nt lds
v_mfma_f32_16x16x32_bf16 v[156:159],a[180:183],v[68:71],v[156:159]
v_mfma_f32_16x16x32_bf16 v[156:159],a[176:179],v[64:67],v[156:159]
v_mfma_f32_16x16x32_bf16 a[64:67],a[192:195],v[200:203],a[64:67]
v_mfma_f32_16x16x32_bf16 a[64:67],a[196:199],v[204:207],a[64:67]
v_mfma_f32_16x16x32_bf16 a[68:71],a[204:207],v[204:207],a[68:71]
v_mfma_f32_16x16x32_bf16 a[68:71],a[200:203],v[200:203],a[68:71]
v_mfma_f32_16x16x32_bf16 a[72:75],a[208:211],v[200:203],a[72:75]
v_mfma_f32_16x16x32_bf16 a[72:75],a[212:215],v[204:207],a[72:75]
v_exp_f32 v136,v136
v_mfma_f32_16x16x32_bf16 a[76:79],a[220:223],v[204:207],a[76:79]
v_exp_f32 v137,v137
v_mfma_f32_16x16x32_bf16 a[76:79],a[216:219],v[200:203],a[76:79]
v_exp_f32 v138,v138
v_mfma_f32_16x16x32_bf16 a[80:83],a[224:227],v[200:203],a[80:83]
s_addk_i32 m0,16384
v_exp_f32 v139,v139
v_mfma_f32_16x16x32_bf16 a[80:83],a[228:231],v[204:207],a[80:83]
v_exp_f32 v140,v140
v_mfma_f32_16x16x32_bf16 a[84:87],a[236:239],v[204:207],a[84:87]
v_exp_f32 v141,v141
v_mfma_f32_16x16x32_bf16 a[84:87],a[232:235],v[200:203],a[84:87]
buffer_load_dwordx4 v4,s[24:27],s47 offen nt lds
v_mfma_f32_16x16x32_bf16 a[88:91],a[240:243],v[200:203],a[88:91]
v_exp_f32 v142,v142
v_mfma_f32_16x16x32_bf16 a[88:91],a[244:247],v[204:207],a[88:91]
v_exp_f32 v143,v143
v_mfma_f32_16x16x32_bf16 a[92:95],a[252:255],v[204:207],a[92:95]
v_mfma_f32_16x16x32_bf16 a[92:95],a[248:251],v[200:203],a[92:95]
v_smfmac_f32_16x16x64_bf16 v[56:59],v[28:31],v[200:207],v27
v_mfma_f32_16x16x32_bf16 v[168:171],a[128:131],v[80:83],v[36:39]
v_mfma_f32_16x16x32_bf16 v[168:171],a[132:135],v[84:87],v[168:171]
v_mfma_f32_16x16x32_bf16 v[168:171],a[136:139],v[88:91],v[168:171]
v_exp_f32 v152,v152
v_mfma_f32_16x16x32_bf16 v[168:171],a[140:143],v[92:95],v[168:171]
v_exp_f32 v153,v153
v_mfma_f32_16x16x32_bf16 v[172:175],a[156:159],v[92:95],v[36:39]
v_exp_f32 v154,v154
v_mfma_f32_16x16x32_bf16 v[172:175],a[152:155],v[88:91],v[172:175]
v_exp_f32 v155,v155
v_mfma_f32_16x16x32_bf16 v[172:175],a[148:151],v[84:87],v[172:175]
v_exp_f32 v156,v156
v_mfma_f32_16x16x32_bf16 v[172:175],a[144:147],v[80:83],v[172:175]
v_exp_f32 v157,v157
v_mfma_f32_16x16x32_bf16 v[184:187],a[160:163],v[80:83],v[36:39]
buffer_load_dwordx4 v5,s[24:27],s47 offen offset:1024 nt lds
v_mfma_f32_16x16x32_bf16 v[184:187],a[164:167],v[84:87],v[184:187]
v_exp_f32 v158,v158
v_mfma_f32_16x16x32_bf16 v[184:187],a[168:171],v[88:91],v[184:187]
v_exp_f32 v159,v159
v_mfma_f32_16x16x32_bf16 v[184:187],a[172:175],v[92:95],v[184:187]
v_exp_f32 v168,v168
v_mfma_f32_16x16x32_bf16 v[188:191],a[188:191],v[92:95],v[36:39]
v_exp_f32 v169,v169
v_mfma_f32_16x16x32_bf16 v[188:191],a[184:187],v[88:91],v[188:191]
v_exp_f32 v170,v170
v_mfma_f32_16x16x32_bf16 v[188:191],a[180:183],v[84:87],v[188:191]
v_exp_f32 v171,v171
v_mfma_f32_16x16x32_bf16 v[188:191],a[176:179],v[80:83],v[188:191]
v_exp_f32 v172,v172
ds_read_b64_tr_b16 a[192:193],v2
v_cvt_pk_bf16_f32 v168,v168,v169
ds_read_b64_tr_b16 a[194:195],v2 offset:4096
v_cvt_pk_bf16_f32 v169,v170,v171
v_exp_f32 v173,v173
ds_read_b64_tr_b16 a[196:197],v2 offset:8192
v_cvt_pk_bf16_f32 v170,v172,v173
v_exp_f32 v174,v174
v_exp_f32 v175,v175
buffer_load_dwordx4 v4,s[24:27],s47 offen offset:2048 nt lds
ds_read_b64_tr_b16 a[198:199],v2 offset:12288
v_cvt_pk_bf16_f32 v171,v174,v175
v_exp_f32 v184,v184
v_exp_f32 v185,v185
ds_read_b64_tr_b16 a[204:205],v3 offset:8192
v_cvt_pk_bf16_f32 v136,v136,v137
ds_read_b64_tr_b16 a[206:207],v3 offset:12288
v_cvt_pk_bf16_f32 v172,v184,v185
ds_read_b64_tr_b16 a[200:201],v3
ds_read_b64_tr_b16 a[202:203],v3 offset:4096
v_cvt_pk_bf16_f32 v137,v138,v139
ds_read_b64_tr_b16 a[208:209],v2 offset:256
ds_read_b64_tr_b16 a[210:211],v2 offset:4352
ds_read_b64_tr_b16 a[212:213],v2 offset:8448
v_mfma_f32_16x16x32_bf16 v[200:203],a[128:131],v[96:99],v[40:43]
ds_read_b64_tr_b16 a[214:215],v2 offset:12544
v_cvt_pk_bf16_f32 v138,v140,v141
v_mfma_f32_16x16x32_bf16 v[200:203],a[132:135],v[100:103],v[200:203]
ds_read_b64_tr_b16 a[220:221],v3 offset:8448
v_mfma_f32_16x16x32_bf16 v[200:203],a[136:139],v[104:107],v[200:203]
ds_read_b64_tr_b16 a[222:223],v3 offset:12544
v_cvt_pk_bf16_f32 v139,v142,v143
v_mfma_f32_16x16x32_bf16 v[200:203],a[140:143],v[108:111],v[200:203]
buffer_load_dwordx4 v5,s[24:27],s47 offen offset:3072 nt lds
v_mfma_f32_16x16x32_bf16 v[204:207],a[156:159],v[108:111],v[40:43]
s_or_b32 s47,s47,0x18000
s_addk_i32 s47,4096
ds_read_b64_tr_b16 a[216:217],v3 offset:256
v_mfma_f32_16x16x32_bf16 v[204:207],a[152:155],v[104:107],v[204:207]
s_and_b32 s47,s47,0x27000
ds_read_b64_tr_b16 a[218:219],v3 offset:4352
v_cvt_pk_bf16_f32 v140,v152,v153
v_mfma_f32_16x16x32_bf16 v[204:207],a[148:151],v[100:103],v[204:207]
s_or_b32 s47,s47,s46
ds_read_b64_tr_b16 a[224:225],v2 offset:512
v_mfma_f32_16x16x32_bf16 v[204:207],a[144:147],v[96:99],v[204:207]
ds_read_b64_tr_b16 a[226:227],v2 offset:4608
v_mfma_f32_16x16x32_bf16 v[216:219],a[160:163],v[96:99],v[40:43]
ds_read_b64_tr_b16 a[228:229],v2 offset:8704
v_cvt_pk_bf16_f32 v141,v154,v155
v_mfma_f32_16x16x32_bf16 v[216:219],a[164:167],v[100:103],v[216:219]
ds_read_b64_tr_b16 a[230:231],v2 offset:12800
v_mfma_f32_16x16x32_bf16 v[216:219],a[168:171],v[104:107],v[216:219]
ds_read_b64_tr_b16 a[236:237],v3 offset:8704
v_mfma_f32_16x16x32_bf16 v[216:219],a[172:175],v[108:111],v[216:219]
ds_read_b64_tr_b16 a[238:239],v3 offset:12800
v_cvt_pk_bf16_f32 v142,v156,v157
v_mfma_f32_16x16x32_bf16 v[220:223],a[188:191],v[108:111],v[40:43]
ds_read_b64_tr_b16 a[232:233],v3 offset:512
v_mfma_f32_16x16x32_bf16 v[220:223],a[184:187],v[104:107],v[220:223]
ds_read_b64_tr_b16 a[234:235],v3 offset:4608
v_mfma_f32_16x16x32_bf16 v[220:223],a[180:183],v[100:103],v[220:223]
ds_read_b64_tr_b16 a[240:241],v2 offset:768
v_cvt_pk_bf16_f32 v143,v158,v159
v_mfma_f32_16x16x32_bf16 v[220:223],a[176:179],v[96:99],v[220:223]
s_waitcnt lgkmcnt(7)
buffer_load_dword v26,v24,s[88:91],s93 offen
v_mfma_f32_16x16x32_bf16 a[0:3],a[192:195],v[136:139],a[0:3]
ds_read_b64_tr_b16 a[242:243],v2 offset:4864
v_mfma_f32_16x16x32_bf16 a[0:3],a[196:199],v[140:143],a[0:3]
ds_read_b64_tr_b16 a[244:245],v2 offset:8960
v_mfma_f32_16x16x32_bf16 a[4:7],a[204:207],v[140:143],a[4:7]
ds_read_b64_tr_b16 a[246:247],v2 offset:13056
v_mfma_f32_16x16x32_bf16 a[4:7],a[200:203],v[136:139],a[4:7]
ds_read_b64_tr_b16 a[252:253],v3 offset:8960
v_mfma_f32_16x16x32_bf16 a[8:11],a[208:211],v[136:139],a[8:11]
ds_read_b64_tr_b16 a[254:255],v3 offset:13056
v_mfma_f32_16x16x32_bf16 a[8:11],a[212:215],v[140:143],a[8:11]
ds_read_b64_tr_b16 a[248:249],v3 offset:768
v_mfma_f32_16x16x32_bf16 a[12:15],a[220:223],v[140:143],a[12:15]
ds_read_b64_tr_b16 a[250:251],v3 offset:4864
v_mfma_f32_16x16x32_bf16 a[12:15],a[216:219],v[136:139],a[12:15]
s_bitcmp1_b32 s45,0
s_cbranch_scc0 L_t31v30_%=
s_mov_b32 m0,s80
s_addk_i32 s80,4096
buffer_load_dwordx4 v25,s[20:23],s83 offen nt lds
s_addk_i32 s83,256
L_t31v30_%=:
v_mfma_f32_16x16x32_bf16 a[16:19],a[224:227],v[136:139],a[16:19]
s_waitcnt lgkmcnt(6)
v_exp_f32 v186,v186
v_mfma_f32_16x16x32_bf16 a[16:19],a[228:231],v[140:143],a[16:19]
v_exp_f32 v187,v187
v_mfma_f32_16x16x32_bf16 a[20:23],a[236:239],v[140:143],a[20:23]
v_exp_f32 v188,v188
v_mfma_f32_16x16x32_bf16 a[20:23],a[232:235],v[136:139],a[20:23]
v_exp_f32 v189,v189
v_mfma_f32_16x16x32_bf16 a[24:27],a[240:243],v[136:139],a[24:27]
s_waitcnt lgkmcnt(2)
v_exp_f32 v190,v190
v_mfma_f32_16x16x32_bf16 a[24:27],a[244:247],v[140:143],a[24:27]
v_cvt_pk_bf16_f32 v173,v186,v187
v_mfma_f32_16x16x32_bf16 a[28:31],a[252:255],v[140:143],a[28:31]
s_waitcnt lgkmcnt(0)
v_exp_f32 v191,v191
v_mfma_f32_16x16x32_bf16 a[28:31],a[248:251],v[136:139],a[28:31]
v_cvt_pk_bf16_f32 v174,v188,v189
v_cvt_pk_bf16_f32 v175,v190,v191
s_cmp_lg_u32 s53,0
v_smfmac_f32_16x16x64_bf16 v[48:51],v[28:31],v[136:143],v27
s_cbranch_scc1 L_att0v3_%=
s_waitcnt lgkmcnt(0)
s_nop 7
s_branch L_att0x_%=
.p2align 5
L_att0v0_%=:
v_mfma_f32_16x16x32_bf16 v[232:235],a[128:131],v[112:115],v[44:47]
s_waitcnt vmcnt(8) lgkmcnt(0)
v_exp_f32 v200,v200
v_mfma_f32_16x16x32_bf16 v[232:235],a[132:135],v[116:119],v[232:235]
s_barrier
s_add_u32 m0,s49,s51
ds_read_b128 a[128:131],v1
v_mfma_f32_16x16x32_bf16 v[232:235],a[136:139],v[120:123],v[232:235]
s_mov_b32 s90,L_endseg_%=-L_pc_%=+128
v_exp_f32 v201,v201
v_mfma_f32_16x16x32_bf16 v[232:235],a[140:143],v[124:127],v[232:235]
s_lshr_b32 s45,s45,1
ds_read_b128 a[132:135],v1 offset:256
v_cvt_pk_bf16_f32 v200,v200,v201
v_mfma_f32_16x16x32_bf16 v[236:239],a[156:159],v[124:127],v[44:47]
buffer_load_dwordx4 v4,s[20:23],s47 offen nt lds
v_mfma_f32_16x16x32_bf16 v[236:239],a[152:155],v[120:123],v[236:239]
)A"
R"A(ds_read_b128 a[136:139],v1 offset:512
v_add_u32 v2,s48,v2
v_mfma_f32_16x16x32_bf16 v[236:239],a[148:151],v[116:119],v[236:239]
v_exp_f32 v202,v202
v_mfma_f32_16x16x32_bf16 v[236:239],a[144:147],v[112:115],v[236:239]
s_add_u32 s62,s50,32768
ds_read_b128 a[140:143],v1 offset:768
v_add_u32 v3,s48,v3
v_mfma_f32_16x16x32_bf16 v[248:251],a[160:163],v[112:115],v[44:47]
s_and_b32 s62,s62,131071
v_exp_f32 v203,v203
v_mfma_f32_16x16x32_bf16 v[248:251],a[164:167],v[116:119],v[248:251]
s_addk_i32 s54,16384
ds_read_b128 a[156:159],v1 offset:4864
v_cvt_pk_bf16_f32 v201,v202,v203
v_mfma_f32_16x16x32_bf16 v[248:251],a[168:171],v[120:123],v[248:251]
s_sub_u32 s48,s62,s50
v_exp_f32 v204,v204
v_mfma_f32_16x16x32_bf16 v[248:251],a[172:175],v[124:127],v[248:251]
s_sub_u32 s53,s53,1
v_exp_f32 v205,v205
v_mfma_f32_16x16x32_bf16 v[252:255],a[188:191],v[124:127],v[44:47]
s_and_b32 s46,s54,0xfffc0000
ds_read_b128 a[152:155],v1 offset:4608
v_cvt_pk_bf16_f32 v202,v204,v205
v_mfma_f32_16x16x32_bf16 v[252:255],a[184:187],v[120:123],v[252:255]
s_mov_b32 s50,s62
v_exp_f32 v206,v206
v_mfma_f32_16x16x32_bf16 v[252:255],a[180:183],v[116:119],v[252:255]
s_xor_b32 s49,s50,65536
v_exp_f32 v207,v207
v_mfma_f32_16x16x32_bf16 v[252:255],a[176:179],v[112:115],v[252:255]
ds_read_b128 a[148:151],v1 offset:4352
v_cvt_pk_bf16_f32 v203,v206,v207
v_mfma_f32_16x16x32_bf16 a[32:35],a[192:195],v[168:171],a[32:35]
v_exp_f32 v216,v216
v_mfma_f32_16x16x32_bf16 a[32:35],a[196:199],v[172:175],a[32:35]
ds_read_b128 a[144:147],v1 offset:4096
v_mfma_f32_16x16x32_bf16 a[36:39],a[204:207],v[172:175],a[36:39]
buffer_load_dwordx4 v4,s[20:23],s47 offen offset:1024 nt lds
v_mfma_f32_16x16x32_bf16 a[36:39],a[200:203],v[168:171],a[36:39]
ds_read_b128 a[160:163],v1 offset:8192
v_mfma_f32_16x16x32_bf16 a[40:43],a[208:211],v[168:171],a[40:43]
v_exp_f32 v217,v217
v_mfma_f32_16x16x32_bf16 a[40:43],a[212:215],v[172:175],a[40:43]
ds_read_b128 a[164:167],v1 offset:8448
v_cvt_pk_bf16_f32 v204,v216,v217
v_mfma_f32_16x16x32_bf16 a[44:47],a[220:223],v[172:175],a[44:47]
v_exp_f32 v218,v218
v_mfma_f32_16x16x32_bf16 a[44:47],a[216:219],v[168:171],a[44:47]
ds_read_b128 a[168:171],v1 offset:8704
v_mfma_f32_16x16x32_bf16 a[48:51],a[224:227],v[168:171],a[48:51]
v_exp_f32 v219,v219
v_mfma_f32_16x16x32_bf16 a[48:51],a[228:231],v[172:175],a[48:51]
ds_read_b128 a[172:175],v1 offset:8960
v_cvt_pk_bf16_f32 v205,v218,v219
v_mfma_f32_16x16x32_bf16 a[52:55],a[236:239],v[172:175],a[52:55]
v_exp_f32 v220,v220
v_mfma_f32_16x16x32_bf16 a[52:55],a[232:235],v[168:171],a[52:55]
ds_read_b128 a[188:191],v1 offset:13056
v_mfma_f32_16x16x32_bf16 a[56:59],a[240:243],v[168:171],a[56:59]
v_exp_f32 v221,v221
v_mfma_f32_16x16x32_bf16 a[56:59],a[244:247],v[172:175],a[56:59]
ds_read_b128 a[184:187],v1 offset:12800
v_cvt_pk_bf16_f32 v206,v220,v221
v_mfma_f32_16x16x32_bf16 a[60:63],a[252:255],v[172:175],a[60:63]
v_exp_f32 v222,v222
v_mfma_f32_16x16x32_bf16 a[60:63],a[248:251],v[168:171],a[60:63]
ds_read_b128 a[180:183],v1 offset:12544
v_smfmac_f32_16x16x64_bf16 v[52:55],v[28:31],v[168:175],v27
buffer_load_dwordx4 v5,s[20:23],s47 offen offset:2048 nt lds
ds_read_b128 a[176:179],v1 offset:12288
v_add_u32 v1,s48,v1
v_exp_f32 v223,v223
s_nop 0
v_cvt_pk_bf16_f32 v207,v222,v223
v_exp_f32 v232,v232
v_exp_f32 v233,v233
v_exp_f32 v234,v234
v_exp_f32 v235,v235
v_exp_f32 v236,v236
v_exp_f32 v237,v237
v_exp_f32 v238,v238
v_exp_f32 v239,v239
v_exp_f32 v248,v248
buffer_load_dwordx4 v5,s[20:23],s47 offen offset:3072 nt lds
v_exp_f32 v249,v249
v_exp_f32 v250,v250
v_mfma_f32_16x16x32_bf16 a[64:67],a[192:195],v[200:203],a[64:67]
v_exp_f32 v251,v251
v_mfma_f32_16x16x32_bf16 a[64:67],a[196:199],v[204:207],a[64:67]
v_exp_f32 v252,v252
v_mfma_f32_16x16x32_bf16 a[68:71],a[204:207],v[204:207],a[68:71]
v_exp_f32 v253,v253
v_mfma_f32_16x16x32_bf16 a[68:71],a[200:203],v[200:203],a[68:71]
v_exp_f32 v254,v254
v_mfma_f32_16x16x32_bf16 a[72:75],a[208:211],v[200:203],a[72:75]
v_exp_f32 v255,v255
v_mfma_f32_16x16x32_bf16 a[72:75],a[212:215],v[204:207],a[72:75]
v_mfma_f32_16x16x32_bf16 a[76:79],a[220:223],v[204:207],a[76:79]
v_mfma_f32_16x16x32_bf16 a[76:79],a[216:219],v[200:203],a[76:79]
v_mfma_f32_16x16x32_bf16 a[80:83],a[224:227],v[200:203],a[80:83]
s_addk_i32 m0,16384
v_mfma_f32_16x16x32_bf16 a[80:83],a[228:231],v[204:207],a[80:83]
v_mfma_f32_16x16x32_bf16 a[84:87],a[236:239],v[204:207],a[84:87]
v_mfma_f32_16x16x32_bf16 a[84:87],a[232:235],v[200:203],a[84:87]
buffer_load_dwordx4 v4,s[24:27],s47 offen nt lds
v_mfma_f32_16x16x32_bf16 a[88:91],a[240:243],v[200:203],a[88:91]
v_mfma_f32_16x16x32_bf16 a[88:91],a[244:247],v[204:207],a[88:91]
v_mfma_f32_16x16x32_bf16 a[92:95],a[252:255],v[204:207],a[92:95]
v_cvt_pk_bf16_f32 v232,v232,v233
v_cvt_pk_bf16_f32 v233,v234,v235
v_mfma_f32_16x16x32_bf16 a[92:95],a[248:251],v[200:203],a[92:95]
v_cvt_pk_bf16_f32 v234,v236,v237
v_cvt_pk_bf16_f32 v235,v238,v239
v_smfmac_f32_16x16x64_bf16 v[56:59],v[28:31],v[200:207],v27
s_waitcnt lgkmcnt(0)
v_cvt_pk_bf16_f32 v236,v248,v249
v_mfma_f32_16x16x32_bf16 v[168:171],a[128:131],v[80:83],v[36:39]
v_cvt_pk_bf16_f32 v237,v250,v251
v_cvt_pk_bf16_f32 v238,v252,v253
v_mfma_f32_16x16x32_bf16 v[168:171],a[132:135],v[84:87],v[168:171]
v_cvt_pk_bf16_f32 v239,v254,v255
v_mfma_f32_16x16x32_bf16 v[168:171],a[136:139],v[88:91],v[168:171]
v_mfma_f32_16x16x32_bf16 v[168:171],a[140:143],v[92:95],v[168:171]
v_mfma_f32_16x16x32_bf16 v[172:175],a[156:159],v[92:95],v[36:39]
v_mfma_f32_16x16x32_bf16 v[172:175],a[152:155],v[88:91],v[172:175]
v_mfma_f32_16x16x32_bf16 v[172:175],a[148:151],v[84:87],v[172:175]
v_mfma_f32_16x16x32_bf16 v[172:175],a[144:147],v[80:83],v[172:175]
v_mfma_f32_16x16x32_bf16 v[184:187],a[160:163],v[80:83],v[36:39]
buffer_load_dwordx4 v5,s[24:27],s47 offen offset:1024 nt lds
v_mfma_f32_16x16x32_bf16 v[184:187],a[164:167],v[84:87],v[184:187]
v_mfma_f32_16x16x32_bf16 v[184:187],a[168:171],v[88:91],v[184:187]
v_mfma_f32_16x16x32_bf16 v[184:187],a[172:175],v[92:95],v[184:187]
v_exp_f32 v168,v168
v_mfma_f32_16x16x32_bf16 v[188:191],a[188:191],v[92:95],v[36:39]
v_exp_f32 v169,v169
v_mfma_f32_16x16x32_bf16 v[188:191],a[184:187],v[88:91],v[188:191]
v_exp_f32 v170,v170
v_mfma_f32_16x16x32_bf16 v[188:191],a[180:183],v[84:87],v[188:191]
v_exp_f32 v171,v171
v_mfma_f32_16x16x32_bf16 v[188:191],a[176:179],v[80:83],v[188:191]
v_exp_f32 v172,v172
v_mfma_f32_16x16x32_bf16 a[96:99],a[192:195],v[232:235],a[96:99]
ds_read_b64_tr_b16 a[192:193],v2
v_cvt_pk_bf16_f32 v168,v168,v169
v_mfma_f32_16x16x32_bf16 a[96:99],a[196:199],v[236:239],a[96:99]
ds_read_b64_tr_b16 a[194:195],v2 offset:4096
v_cvt_pk_bf16_f32 v169,v170,v171
v_mfma_f32_16x16x32_bf16 a[100:103],a[204:207],v[236:239],a[100:103]
v_exp_f32 v173,v173
v_mfma_f32_16x16x32_bf16 a[100:103],a[200:203],v[232:235],a[100:103]
ds_read_b64_tr_b16 a[196:197],v2 offset:8192
v_cvt_pk_bf16_f32 v170,v172,v173
v_mfma_f32_16x16x32_bf16 a[104:107],a[208:211],v[232:235],a[104:107]
v_exp_f32 v174,v174
v_mfma_f32_16x16x32_bf16 a[104:107],a[212:215],v[236:239],a[104:107]
v_exp_f32 v175,v175
v_mfma_f32_16x16x32_bf16 a[108:111],a[220:223],v[236:239],a[108:111]
buffer_load_dwordx4 v4,s[24:27],s47 offen offset:2048 nt lds
v_mfma_f32_16x16x32_bf16 a[108:111],a[216:219],v[232:235],a[108:111]
ds_read_b64_tr_b16 a[198:199],v2 offset:12288
v_cvt_pk_bf16_f32 v171,v174,v175
v_mfma_f32_16x16x32_bf16 a[112:115],a[224:227],v[232:235],a[112:115]
v_exp_f32 v184,v184
v_mfma_f32_16x16x32_bf16 a[112:115],a[228:231],v[236:239],a[112:115]
v_exp_f32 v185,v185
v_mfma_f32_16x16x32_bf16 a[116:119],a[236:239],v[236:239],a[116:119]
ds_read_b64_tr_b16 a[204:205],v3 offset:8192
v_mfma_f32_16x16x32_bf16 a[116:119],a[232:235],v[232:235],a[116:119]
ds_read_b64_tr_b16 a[206:207],v3 offset:12288
v_cvt_pk_bf16_f32 v172,v184,v185
v_mfma_f32_16x16x32_bf16 a[120:123],a[240:243],v[232:235],a[120:123]
ds_read_b64_tr_b16 a[200:201],v3
v_mfma_f32_16x16x32_bf16 a[120:123],a[244:247],v[236:239],a[120:123]
ds_read_b64_tr_b16 a[202:203],v3 offset:4096
v_mfma_f32_16x16x32_bf16 a[124:127],a[252:255],v[236:239],a[124:127]
ds_read_b64_tr_b16 a[208:209],v2 offset:256
v_mfma_f32_16x16x32_bf16 a[124:127],a[248:251],v[232:235],a[124:127]
ds_read_b64_tr_b16 a[210:211],v2 offset:4352
v_smfmac_f32_16x16x64_bf16 v[60:63],v[28:31],v[232:239],v27
ds_read_b64_tr_b16 a[212:213],v2 offset:8448
v_mfma_f32_16x16x32_bf16 v[200:203],a[128:131],v[96:99],v[40:43]
ds_read_b64_tr_b16 a[214:215],v2 offset:12544
v_mfma_f32_16x16x32_bf16 v[200:203],a[132:135],v[100:103],v[200:203]
ds_read_b64_tr_b16 a[220:221],v3 offset:8448
v_mfma_f32_16x16x32_bf16 v[200:203],a[136:139],v[104:107],v[200:203]
ds_read_b64_tr_b16 a[222:223],v3 offset:12544
v_mfma_f32_16x16x32_bf16 v[200:203],a[140:143],v[108:111],v[200:203]
buffer_load_dwordx4 v5,s[24:27],s47 offen offset:3072 nt lds
v_mfma_f32_16x16x32_bf16 v[204:207],a[156:159],v[108:111],v[40:43]
s_or_b32 s47,s47,0x18000
s_addk_i32 s47,4096
ds_read_b64_tr_b16 a[216:217],v3 offset:256
v_mfma_f32_16x16x32_bf16 v[204:207],a[152:155],v[104:107],v[204:207]
s_and_b32 s47,s47,0x27000
ds_read_b64_tr_b16 a[218:219],v3 offset:4352
v_mfma_f32_16x16x32_bf16 v[204:207],a[148:151],v[100:103],v[204:207]
s_or_b32 s47,s47,s46
ds_read_b64_tr_b16 a[224:225],v2 offset:512
v_mfma_f32_16x16x32_bf16 v[204:207],a[144:147],v[96:99],v[204:207]
ds_read_b64_tr_b16 a[226:227],v2 offset:4608
v_mfma_f32_16x16x32_bf16 v[216:219],a[160:163],v[96:99],v[40:43]
ds_read_b64_tr_b16 a[228:229],v2 offset:8704
v_mfma_f32_16x16x32_bf16 v[216:219],a[164:167],v[100:103],v[216:219]
ds_read_b64_tr_b16 a[230:231],v2 offset:12800
v_mfma_f32_16x16x32_bf16 v[216:219],a[168:171],v[104:107],v[216:219]
ds_read_b64_tr_b16 a[236:237],v3 offset:8704
v_mfma_f32_16x16x32_bf16 v[216:219],a[172:175],v[108:111],v[216:219]
ds_read_b64_tr_b16 a[238:239],v3 offset:12800
v_mfma_f32_16x16x32_bf16 v[220:223],a[188:191],v[108:111],v[40:43]
ds_read_b64_tr_b16 a[232:233],v3 offset:512
v_mfma_f32_16x16x32_bf16 v[220:223],a[184:187],v[104:107],v[220:223]
ds_read_b64_tr_b16 a[234:235],v3 offset:4608
v_mfma_f32_16x16x32_bf16 v[220:223],a[180:183],v[100:103],v[220:223]
ds_read_b64_tr_b16 a[240:241],v2 offset:768
v_mfma_f32_16x16x32_bf16 v[220:223],a[176:179],v[96:99],v[220:223]
buffer_load_dword v26,v24,s[88:91],s93 offen
ds_read_b64_tr_b16 a[242:243],v2 offset:4864
ds_read_b64_tr_b16 a[244:245],v2 offset:8960
ds_read_b64_tr_b16 a[246:247],v2 offset:13056
ds_read_b64_tr_b16 a[252:253],v3 offset:8960
ds_read_b64_tr_b16 a[254:255],v3 offset:13056
ds_read_b64_tr_b16 a[248:249],v3 offset:768
ds_read_b64_tr_b16 a[250:251],v3 offset:4864
s_bitcmp1_b32 s45,0
s_cbranch_scc0 L_t31v00_%=
s_mov_b32 m0,s80
s_addk_i32 s80,4096
buffer_load_dwordx4 v25,s[20:23],s83 offen nt lds
s_addk_i32 s83,256
L_t31v00_%=:
v_exp_f32 v186,v186
v_exp_f32 v187,v187
v_exp_f32 v188,v188
v_exp_f32 v189,v189
v_exp_f32 v190,v190
v_cvt_pk_bf16_f32 v173,v186,v187
v_exp_f32 v191,v191
v_cvt_pk_bf16_f32 v174,v188,v189
v_cvt_pk_bf16_f32 v175,v190,v191
s_cmp_lg_u32 s53,0
s_cbranch_scc1 L_att0v0_%=
s_waitcnt lgkmcnt(0)
s_nop 7
s_branch L_att0x_%=
.p2align 5
L_att0v1_%=:
v_mfma_f32_16x16x32_bf16 v[232:235],a[128:131],v[112:115],v[44:47]
s_waitcnt vmcnt(8) lgkmcnt(0)
v_exp_f32 v200,v200
v_mfma_f32_16x16x32_bf16 v[232:235],a[132:135],v[116:119],v[232:235]
s_barrier
s_add_u32 m0,s49,s51
ds_read_b128 a[128:131],v1
v_mfma_f32_16x16x32_bf16 v[232:235],a[136:139],v[120:123],v[232:235]
s_mov_b32 s90,L_endseg_%=-L_pc_%=+128
v_exp_f32 v201,v201
v_mfma_f32_16x16x32_bf16 v[232:235],a[140:143],v[124:127],v[232:235]
s_lshr_b32 s45,s45,1
ds_read_b128 a[132:135],v1 offset:256
v_cvt_pk_bf16_f32 v200,v200,v201
v_mfma_f32_16x16x32_bf16 v[236:239],a[156:159],v[124:127],v[44:47]
buffer_load_dwordx4 v4,s[20:23],s47 offen nt lds
v_mfma_f32_16x16x32_bf16 v[236:239],a[152:155],v[120:123],v[236:239]
ds_read_b128 a[136:139],v1 offset:512
v_add_u32 v2,s48,v2
v_mfma_f32_16x16x32_bf16 v[236:239],a[148:151],v[116:119],v[236:239]
v_exp_f32 v202,v202
v_mfma_f32_16x16x32_bf16 v[236:239],a[144:147],v[112:115],v[236:239]
s_add_u32 s62,s50,32768
ds_read_b128 a[140:143],v1 offset:768
v_add_u32 v3,s48,v3
v_mfma_f32_16x16x32_bf16 v[248:251],a[160:163],v[112:115],v[44:47]
)A"
R"A(s_and_b32 s62,s62,131071
v_exp_f32 v203,v203
v_mfma_f32_16x16x32_bf16 v[248:251],a[164:167],v[116:119],v[248:251]
s_addk_i32 s54,16384
ds_read_b128 a[156:159],v1 offset:4864
v_cvt_pk_bf16_f32 v201,v202,v203
v_mfma_f32_16x16x32_bf16 v[248:251],a[168:171],v[120:123],v[248:251]
s_sub_u32 s48,s62,s50
v_exp_f32 v204,v204
v_mfma_f32_16x16x32_bf16 v[248:251],a[172:175],v[124:127],v[248:251]
s_sub_u32 s53,s53,1
v_exp_f32 v205,v205
v_mfma_f32_16x16x32_bf16 v[252:255],a[188:191],v[124:127],v[44:47]
s_and_b32 s46,s54,0xfffc0000
ds_read_b128 a[152:155],v1 offset:4608
v_cvt_pk_bf16_f32 v202,v204,v205
v_mfma_f32_16x16x32_bf16 v[252:255],a[184:187],v[120:123],v[252:255]
s_mov_b32 s50,s62
v_exp_f32 v206,v206
v_mfma_f32_16x16x32_bf16 v[252:255],a[180:183],v[116:119],v[252:255]
s_xor_b32 s49,s50,65536
v_exp_f32 v207,v207
v_mfma_f32_16x16x32_bf16 v[252:255],a[176:179],v[112:115],v[252:255]
ds_read_b128 a[148:151],v1 offset:4352
v_cvt_pk_bf16_f32 v203,v206,v207
v_exp_f32 v216,v216
ds_read_b128 a[144:147],v1 offset:4096
buffer_load_dwordx4 v4,s[20:23],s47 offen offset:1024 nt lds
ds_read_b128 a[160:163],v1 offset:8192
v_exp_f32 v217,v217
ds_read_b128 a[164:167],v1 offset:8448
v_cvt_pk_bf16_f32 v204,v216,v217
v_exp_f32 v218,v218
ds_read_b128 a[168:171],v1 offset:8704
v_exp_f32 v219,v219
ds_read_b128 a[172:175],v1 offset:8960
v_cvt_pk_bf16_f32 v205,v218,v219
v_exp_f32 v220,v220
ds_read_b128 a[188:191],v1 offset:13056
v_exp_f32 v221,v221
ds_read_b128 a[184:187],v1 offset:12800
v_cvt_pk_bf16_f32 v206,v220,v221
v_exp_f32 v222,v222
ds_read_b128 a[180:183],v1 offset:12544
s_waitcnt lgkmcnt(11)
v_mfma_f32_16x16x32_bf16 v[136:139],a[128:131],v[64:67],v[32:35]
buffer_load_dwordx4 v5,s[20:23],s47 offen offset:2048 nt lds
v_mfma_f32_16x16x32_bf16 v[136:139],a[132:135],v[68:71],v[136:139]
ds_read_b128 a[176:179],v1 offset:12288
v_add_u32 v1,s48,v1
v_mfma_f32_16x16x32_bf16 v[136:139],a[136:139],v[72:75],v[136:139]
v_exp_f32 v223,v223
v_mfma_f32_16x16x32_bf16 v[136:139],a[140:143],v[76:79],v[136:139]
s_waitcnt lgkmcnt(11)
v_cvt_pk_bf16_f32 v207,v222,v223
v_mfma_f32_16x16x32_bf16 v[140:143],a[156:159],v[76:79],v[32:35]
s_waitcnt lgkmcnt(10)
v_exp_f32 v232,v232
v_mfma_f32_16x16x32_bf16 v[140:143],a[152:155],v[72:75],v[140:143]
s_waitcnt lgkmcnt(9)
v_exp_f32 v233,v233
v_mfma_f32_16x16x32_bf16 v[140:143],a[148:151],v[68:71],v[140:143]
s_waitcnt lgkmcnt(8)
v_exp_f32 v234,v234
v_mfma_f32_16x16x32_bf16 v[140:143],a[144:147],v[64:67],v[140:143]
s_waitcnt lgkmcnt(1)
v_exp_f32 v235,v235
v_mfma_f32_16x16x32_bf16 v[152:155],a[160:163],v[64:67],v[32:35]
v_exp_f32 v236,v236
v_mfma_f32_16x16x32_bf16 v[152:155],a[164:167],v[68:71],v[152:155]
v_exp_f32 v237,v237
v_mfma_f32_16x16x32_bf16 v[152:155],a[168:171],v[72:75],v[152:155]
v_exp_f32 v238,v238
v_mfma_f32_16x16x32_bf16 v[152:155],a[172:175],v[76:79],v[152:155]
v_exp_f32 v239,v239
v_mfma_f32_16x16x32_bf16 v[156:159],a[188:191],v[76:79],v[32:35]
v_exp_f32 v248,v248
v_mfma_f32_16x16x32_bf16 v[156:159],a[184:187],v[72:75],v[156:159]
buffer_load_dwordx4 v5,s[20:23],s47 offen offset:3072 nt lds
v_mfma_f32_16x16x32_bf16 v[156:159],a[180:183],v[68:71],v[156:159]
s_waitcnt lgkmcnt(0)
v_exp_f32 v249,v249
v_mfma_f32_16x16x32_bf16 v[156:159],a[176:179],v[64:67],v[156:159]
v_exp_f32 v250,v250
v_mfma_f32_16x16x32_bf16 a[64:67],a[192:195],v[200:203],a[64:67]
v_exp_f32 v251,v251
v_mfma_f32_16x16x32_bf16 a[64:67],a[196:199],v[204:207],a[64:67]
v_exp_f32 v252,v252
v_mfma_f32_16x16x32_bf16 a[68:71],a[204:207],v[204:207],a[68:71]
v_exp_f32 v253,v253
v_mfma_f32_16x16x32_bf16 a[68:71],a[200:203],v[200:203],a[68:71]
v_exp_f32 v254,v254
v_mfma_f32_16x16x32_bf16 a[72:75],a[208:211],v[200:203],a[72:75]
v_exp_f32 v255,v255
v_mfma_f32_16x16x32_bf16 a[72:75],a[212:215],v[204:207],a[72:75]
v_exp_f32 v136,v136
v_mfma_f32_16x16x32_bf16 a[76:79],a[220:223],v[204:207],a[76:79]
v_exp_f32 v137,v137
v_mfma_f32_16x16x32_bf16 a[76:79],a[216:219],v[200:203],a[76:79]
v_exp_f32 v138,v138
v_mfma_f32_16x16x32_bf16 a[80:83],a[224:227],v[200:203],a[80:83]
s_addk_i32 m0,16384
v_exp_f32 v139,v139
v_mfma_f32_16x16x32_bf16 a[80:83],a[228:231],v[204:207],a[80:83]
v_exp_f32 v140,v140
v_mfma_f32_16x16x32_bf16 a[84:87],a[236:239],v[204:207],a[84:87]
v_exp_f32 v141,v141
v_mfma_f32_16x16x32_bf16 a[84:87],a[232:235],v[200:203],a[84:87]
buffer_load_dwordx4 v4,s[24:27],s47 offen nt lds
v_mfma_f32_16x16x32_bf16 a[88:91],a[240:243],v[200:203],a[88:91]
v_exp_f32 v142,v142
v_mfma_f32_16x16x32_bf16 a[88:91],a[244:247],v[204:207],a[88:91]
v_exp_f32 v143,v143
v_mfma_f32_16x16x32_bf16 a[92:95],a[252:255],v[204:207],a[92:95]
v_cvt_pk_bf16_f32 v232,v232,v233
v_cvt_pk_bf16_f32 v233,v234,v235
v_mfma_f32_16x16x32_bf16 a[92:95],a[248:251],v[200:203],a[92:95]
v_cvt_pk_bf16_f32 v234,v236,v237
v_cvt_pk_bf16_f32 v235,v238,v239
v_smfmac_f32_16x16x64_bf16 v[56:59],v[28:31],v[200:207],v27
v_cvt_pk_bf16_f32 v236,v248,v249
v_cvt_pk_bf16_f32 v237,v250,v251
v_cvt_pk_bf16_f32 v238,v252,v253
v_cvt_pk_bf16_f32 v239,v254,v255
v_exp_f32 v152,v152
v_exp_f32 v153,v153
v_exp_f32 v154,v154
v_exp_f32 v155,v155
v_exp_f32 v156,v156
v_exp_f32 v157,v157
buffer_load_dwordx4 v5,s[24:27],s47 offen offset:1024 nt lds
v_exp_f32 v158,v158
v_exp_f32 v159,v159
v_mfma_f32_16x16x32_bf16 a[96:99],a[192:195],v[232:235],a[96:99]
ds_read_b64_tr_b16 a[192:193],v2
v_mfma_f32_16x16x32_bf16 a[96:99],a[196:199],v[236:239],a[96:99]
ds_read_b64_tr_b16 a[194:195],v2 offset:4096
v_mfma_f32_16x16x32_bf16 a[100:103],a[204:207],v[236:239],a[100:103]
v_mfma_f32_16x16x32_bf16 a[100:103],a[200:203],v[232:235],a[100:103]
ds_read_b64_tr_b16 a[196:197],v2 offset:8192
v_mfma_f32_16x16x32_bf16 a[104:107],a[208:211],v[232:235],a[104:107]
v_mfma_f32_16x16x32_bf16 a[104:107],a[212:215],v[236:239],a[104:107]
v_mfma_f32_16x16x32_bf16 a[108:111],a[220:223],v[236:239],a[108:111]
buffer_load_dwordx4 v4,s[24:27],s47 offen offset:2048 nt lds
v_mfma_f32_16x16x32_bf16 a[108:111],a[216:219],v[232:235],a[108:111]
ds_read_b64_tr_b16 a[198:199],v2 offset:12288
v_mfma_f32_16x16x32_bf16 a[112:115],a[224:227],v[232:235],a[112:115]
v_mfma_f32_16x16x32_bf16 a[112:115],a[228:231],v[236:239],a[112:115]
v_mfma_f32_16x16x32_bf16 a[116:119],a[236:239],v[236:239],a[116:119]
ds_read_b64_tr_b16 a[204:205],v3 offset:8192
v_cvt_pk_bf16_f32 v136,v136,v137
v_mfma_f32_16x16x32_bf16 a[116:119],a[232:235],v[232:235],a[116:119]
ds_read_b64_tr_b16 a[206:207],v3 offset:12288
v_mfma_f32_16x16x32_bf16 a[120:123],a[240:243],v[232:235],a[120:123]
ds_read_b64_tr_b16 a[200:201],v3
v_mfma_f32_16x16x32_bf16 a[120:123],a[244:247],v[236:239],a[120:123]
ds_read_b64_tr_b16 a[202:203],v3 offset:4096
v_cvt_pk_bf16_f32 v137,v138,v139
v_mfma_f32_16x16x32_bf16 a[124:127],a[252:255],v[236:239],a[124:127]
ds_read_b64_tr_b16 a[208:209],v2 offset:256
v_mfma_f32_16x16x32_bf16 a[124:127],a[248:251],v[232:235],a[124:127]
ds_read_b64_tr_b16 a[210:211],v2 offset:4352
v_smfmac_f32_16x16x64_bf16 v[60:63],v[28:31],v[232:239],v27
ds_read_b64_tr_b16 a[212:213],v2 offset:8448
v_mfma_f32_16x16x32_bf16 v[200:203],a[128:131],v[96:99],v[40:43]
ds_read_b64_tr_b16 a[214:215],v2 offset:12544
v_cvt_pk_bf16_f32 v138,v140,v141
v_mfma_f32_16x16x32_bf16 v[200:203],a[132:135],v[100:103],v[200:203]
ds_read_b64_tr_b16 a[220:221],v3 offset:8448
v_mfma_f32_16x16x32_bf16 v[200:203],a[136:139],v[104:107],v[200:203]
ds_read_b64_tr_b16 a[222:223],v3 offset:12544
v_cvt_pk_bf16_f32 v139,v142,v143
v_mfma_f32_16x16x32_bf16 v[200:203],a[140:143],v[108:111],v[200:203]
buffer_load_dwordx4 v5,s[24:27],s47 offen offset:3072 nt lds
v_mfma_f32_16x16x32_bf16 v[204:207],a[156:159],v[108:111],v[40:43]
s_or_b32 s47,s47,0x18000
s_addk_i32 s47,4096
ds_read_b64_tr_b16 a[216:217],v3 offset:256
v_mfma_f32_16x16x32_bf16 v[204:207],a[152:155],v[104:107],v[204:207]
s_and_b32 s47,s47,0x27000
ds_read_b64_tr_b16 a[218:219],v3 offset:4352
v_cvt_pk_bf16_f32 v140,v152,v153
v_mfma_f32_16x16x32_bf16 v[204:207],a[148:151],v[100:103],v[204:207]
s_or_b32 s47,s47,s46
ds_read_b64_tr_b16 a[224:225],v2 offset:512
v_mfma_f32_16x16x32_bf16 v[204:207],a[144:147],v[96:99],v[204:207]
ds_read_b64_tr_b16 a[226:227],v2 offset:4608
v_mfma_f32_16x16x32_bf16 v[216:219],a[160:163],v[96:99],v[40:43]
ds_read_b64_tr_b16 a[228:229],v2 offset:8704
v_cvt_pk_bf16_f32 v141,v154,v155
v_mfma_f32_16x16x32_bf16 v[216:219],a[164:167],v[100:103],v[216:219]
ds_read_b64_tr_b16 a[230:231],v2 offset:12800
v_mfma_f32_16x16x32_bf16 v[216:219],a[168:171],v[104:107],v[216:219]
ds_read_b64_tr_b16 a[236:237],v3 offset:8704
v_mfma_f32_16x16x32_bf16 v[216:219],a[172:175],v[108:111],v[216:219]
ds_read_b64_tr_b16 a[238:239],v3 offset:12800
v_cvt_pk_bf16_f32 v142,v156,v157
v_mfma_f32_16x16x32_bf16 v[220:223],a[188:191],v[108:111],v[40:43]
ds_read_b64_tr_b16 a[232:233],v3 offset:512
v_mfma_f32_16x16x32_bf16 v[220:223],a[184:187],v[104:107],v[220:223]
ds_read_b64_tr_b16 a[234:235],v3 offset:4608
v_mfma_f32_16x16x32_bf16 v[220:223],a[180:183],v[100:103],v[220:223]
ds_read_b64_tr_b16 a[240:241],v2 offset:768
v_cvt_pk_bf16_f32 v143,v158,v159
v_mfma_f32_16x16x32_bf16 v[220:223],a[176:179],v[96:99],v[220:223]
s_waitcnt lgkmcnt(7)
buffer_load_dword v26,v24,s[88:91],s93 offen
v_mfma_f32_16x16x32_bf16 a[0:3],a[192:195],v[136:139],a[0:3]
ds_read_b64_tr_b16 a[242:243],v2 offset:4864
v_mfma_f32_16x16x32_bf16 a[0:3],a[196:199],v[140:143],a[0:3]
ds_read_b64_tr_b16 a[244:245],v2 offset:8960
v_mfma_f32_16x16x32_bf16 a[4:7],a[204:207],v[140:143],a[4:7]
ds_read_b64_tr_b16 a[246:247],v2 offset:13056
v_mfma_f32_16x16x32_bf16 a[4:7],a[200:203],v[136:139],a[4:7]
ds_read_b64_tr_b16 a[252:253],v3 offset:8960
v_mfma_f32_16x16x32_bf16 a[8:11],a[208:211],v[136:139],a[8:11]
ds_read_b64_tr_b16 a[254:255],v3 offset:13056
v_mfma_f32_16x16x32_bf16 a[8:11],a[212:215],v[140:143],a[8:11]
ds_read_b64_tr_b16 a[248:249],v3 offset:768
v_mfma_f32_16x16x32_bf16 a[12:15],a[220:223],v[140:143],a[12:15]
ds_read_b64_tr_b16 a[250:251],v3 offset:4864
v_mfma_f32_16x16x32_bf16 a[12:15],a[216:219],v[136:139],a[12:15]
s_bitcmp1_b32 s45,0
s_cbranch_scc0 L_t31v10_%=
s_mov_b32 m0,s80
s_addk_i32 s80,4096
buffer_load_dwordx4 v25,s[20:23],s83 offen nt lds
s_addk_i32 s83,256
L_t31v10_%=:
v_mfma_f32_16x16x32_bf16 a[16:19],a[224:227],v[136:139],a[16:19]
s_waitcnt lgkmcnt(6)
v_mfma_f32_16x16x32_bf16 a[16:19],a[228:231],v[140:143],a[16:19]
v_mfma_f32_16x16x32_bf16 a[20:23],a[236:239],v[140:143],a[20:23]
v_mfma_f32_16x16x32_bf16 a[20:23],a[232:235],v[136:139],a[20:23]
v_mfma_f32_16x16x32_bf16 a[24:27],a[240:243],v[136:139],a[24:27]
s_waitcnt lgkmcnt(2)
v_mfma_f32_16x16x32_bf16 a[24:27],a[244:247],v[140:143],a[24:27]
v_mfma_f32_16x16x32_bf16 a[28:31],a[252:255],v[140:143],a[28:31]
s_waitcnt lgkmcnt(0)
v_mfma_f32_16x16x32_bf16 a[28:31],a[248:251],v[136:139],a[28:31]
s_cmp_lg_u32 s53,0
v_smfmac_f32_16x16x64_bf16 v[48:51],v[28:31],v[136:143],v27
s_cbranch_scc1 L_att0v1_%=
s_waitcnt lgkmcnt(0)
s_nop 7
s_branch L_att0x_%=
.p2align 5
L_att0v2_%=:
v_mfma_f32_16x16x32_bf16 v[232:235],a[128:131],v[112:115],v[44:47]
s_waitcnt vmcnt(8) lgkmcnt(0)
v_mfma_f32_16x16x32_bf16 v[232:235],a[132:135],v[116:119],v[232:235]
s_barrier
s_add_u32 m0,s49,s51
ds_read_b128 a[128:131],v1
v_mfma_f32_16x16x32_bf16 v[232:235],a[136:139],v[120:123],v[232:235]
s_mov_b32 s90,L_endseg_%=-L_pc_%=+128
v_mfma_f32_16x16x32_bf16 v[232:235],a[140:143],v[124:127],v[232:235]
s_lshr_b32 s45,s45,1
ds_read_b128 a[132:135],v1 offset:256
v_mfma_f32_16x16x32_bf16 v[236:239],a[156:159],v[124:127],v[44:47]
buffer_load_dwordx4 v4,s[20:23],s47 offen nt lds
v_mfma_f32_16x16x32_bf16 v[236:239],a[152:155],v[120:123],v[236:239]
ds_read_b128 a[136:139],v1 offset:512
v_add_u32 v2,s48,v2
v_mfma_f32_16x16x32_bf16 v[236:239],a[148:151],v[116:119],v[236:239]
v_mfma_f32_16x16x32_bf16 v[236:239],a[144:147],v[112:115],v[236:239]
s_add_u32 s62,s50,32768
ds_read_b128 a[140:143],v1 offset:768
v_add_u32 v3,s48,v3
v_mfma_f32_16x16x32_bf16 v[248:251],a[160:163],v[112:115],v[44:47]
s_and_b32 s62,s62,131071
v_mfma_f32_16x16x32_bf16 v[248:251],a[164:167],v[116:119],v[248:251]
s_addk_i32 s54,16384
ds_read_b128 a[156:159],v1 offset:4864
v_mfma_f32_16x16x32_bf16 v[248:251],a[168:171],v[120:123],v[248:251]
s_sub_u32 s48,s62,s50
)A"
R"A(v_mfma_f32_16x16x32_bf16 v[248:251],a[172:175],v[124:127],v[248:251]
s_sub_u32 s53,s53,1
v_mfma_f32_16x16x32_bf16 v[252:255],a[188:191],v[124:127],v[44:47]
s_and_b32 s46,s54,0xfffc0000
ds_read_b128 a[152:155],v1 offset:4608
v_mfma_f32_16x16x32_bf16 v[252:255],a[184:187],v[120:123],v[252:255]
s_mov_b32 s50,s62
v_mfma_f32_16x16x32_bf16 v[252:255],a[180:183],v[116:119],v[252:255]
s_xor_b32 s49,s50,65536
v_mfma_f32_16x16x32_bf16 v[252:255],a[176:179],v[112:115],v[252:255]
ds_read_b128 a[148:151],v1 offset:4352
v_mfma_f32_16x16x32_bf16 a[32:35],a[192:195],v[168:171],a[32:35]
v_mfma_f32_16x16x32_bf16 a[32:35],a[196:199],v[172:175],a[32:35]
ds_read_b128 a[144:147],v1 offset:4096
v_mfma_f32_16x16x32_bf16 a[36:39],a[204:207],v[172:175],a[36:39]
buffer_load_dwordx4 v4,s[20:23],s47 offen offset:1024 nt lds
v_mfma_f32_16x16x32_bf16 a[36:39],a[200:203],v[168:171],a[36:39]
ds_read_b128 a[160:163],v1 offset:8192
v_mfma_f32_16x16x32_bf16 a[40:43],a[208:211],v[168:171],a[40:43]
v_mfma_f32_16x16x32_bf16 a[40:43],a[212:215],v[172:175],a[40:43]
ds_read_b128 a[164:167],v1 offset:8448
v_mfma_f32_16x16x32_bf16 a[44:47],a[220:223],v[172:175],a[44:47]
v_mfma_f32_16x16x32_bf16 a[44:47],a[216:219],v[168:171],a[44:47]
ds_read_b128 a[168:171],v1 offset:8704
v_mfma_f32_16x16x32_bf16 a[48:51],a[224:227],v[168:171],a[48:51]
v_mfma_f32_16x16x32_bf16 a[48:51],a[228:231],v[172:175],a[48:51]
ds_read_b128 a[172:175],v1 offset:8960
v_mfma_f32_16x16x32_bf16 a[52:55],a[236:239],v[172:175],a[52:55]
v_mfma_f32_16x16x32_bf16 a[52:55],a[232:235],v[168:171],a[52:55]
ds_read_b128 a[188:191],v1 offset:13056
v_mfma_f32_16x16x32_bf16 a[56:59],a[240:243],v[168:171],a[56:59]
v_mfma_f32_16x16x32_bf16 a[56:59],a[244:247],v[172:175],a[56:59]
ds_read_b128 a[184:187],v1 offset:12800
v_mfma_f32_16x16x32_bf16 a[60:63],a[252:255],v[172:175],a[60:63]
v_mfma_f32_16x16x32_bf16 a[60:63],a[248:251],v[168:171],a[60:63]
ds_read_b128 a[180:183],v1 offset:12544
v_smfmac_f32_16x16x64_bf16 v[52:55],v[28:31],v[168:175],v27
s_waitcnt lgkmcnt(4)
v_mfma_f32_16x16x32_bf16 v[136:139],a[128:131],v[64:67],v[32:35]
buffer_load_dwordx4 v5,s[20:23],s47 offen offset:2048 nt lds
v_mfma_f32_16x16x32_bf16 v[136:139],a[132:135],v[68:71],v[136:139]
ds_read_b128 a[176:179],v1 offset:12288
v_add_u32 v1,s48,v1
v_mfma_f32_16x16x32_bf16 v[136:139],a[136:139],v[72:75],v[136:139]
v_mfma_f32_16x16x32_bf16 v[136:139],a[140:143],v[76:79],v[136:139]
v_mfma_f32_16x16x32_bf16 v[140:143],a[156:159],v[76:79],v[32:35]
v_exp_f32 v232,v232
v_mfma_f32_16x16x32_bf16 v[140:143],a[152:155],v[72:75],v[140:143]
v_exp_f32 v233,v233
v_mfma_f32_16x16x32_bf16 v[140:143],a[148:151],v[68:71],v[140:143]
v_exp_f32 v234,v234
v_mfma_f32_16x16x32_bf16 v[140:143],a[144:147],v[64:67],v[140:143]
v_exp_f32 v235,v235
v_mfma_f32_16x16x32_bf16 v[152:155],a[160:163],v[64:67],v[32:35]
v_exp_f32 v236,v236
v_mfma_f32_16x16x32_bf16 v[152:155],a[164:167],v[68:71],v[152:155]
v_exp_f32 v237,v237
v_mfma_f32_16x16x32_bf16 v[152:155],a[168:171],v[72:75],v[152:155]
s_waitcnt lgkmcnt(0)
v_exp_f32 v238,v238
v_mfma_f32_16x16x32_bf16 v[152:155],a[172:175],v[76:79],v[152:155]
v_exp_f32 v239,v239
v_mfma_f32_16x16x32_bf16 v[156:159],a[188:191],v[76:79],v[32:35]
v_exp_f32 v248,v248
v_mfma_f32_16x16x32_bf16 v[156:159],a[184:187],v[72:75],v[156:159]
buffer_load_dwordx4 v5,s[20:23],s47 offen offset:3072 nt lds
v_mfma_f32_16x16x32_bf16 v[156:159],a[180:183],v[68:71],v[156:159]
v_exp_f32 v249,v249
v_mfma_f32_16x16x32_bf16 v[156:159],a[176:179],v[64:67],v[156:159]
v_exp_f32 v250,v250
v_exp_f32 v251,v251
v_exp_f32 v252,v252
v_exp_f32 v253,v253
v_exp_f32 v254,v254
v_exp_f32 v255,v255
v_exp_f32 v136,v136
v_exp_f32 v137,v137
v_exp_f32 v138,v138
s_addk_i32 m0,16384
v_exp_f32 v139,v139
v_exp_f32 v140,v140
v_exp_f32 v141,v141
buffer_load_dwordx4 v4,s[24:27],s47 offen nt lds
v_exp_f32 v142,v142
v_exp_f32 v143,v143
v_cvt_pk_bf16_f32 v232,v232,v233
v_cvt_pk_bf16_f32 v233,v234,v235
v_cvt_pk_bf16_f32 v234,v236,v237
v_cvt_pk_bf16_f32 v235,v238,v239
v_cvt_pk_bf16_f32 v236,v248,v249
v_mfma_f32_16x16x32_bf16 v[168:171],a[128:131],v[80:83],v[36:39]
v_cvt_pk_bf16_f32 v237,v250,v251
v_cvt_pk_bf16_f32 v238,v252,v253
v_mfma_f32_16x16x32_bf16 v[168:171],a[132:135],v[84:87],v[168:171]
v_cvt_pk_bf16_f32 v239,v254,v255
v_mfma_f32_16x16x32_bf16 v[168:171],a[136:139],v[88:91],v[168:171]
v_exp_f32 v152,v152
v_mfma_f32_16x16x32_bf16 v[168:171],a[140:143],v[92:95],v[168:171]
v_exp_f32 v153,v153
v_mfma_f32_16x16x32_bf16 v[172:175],a[156:159],v[92:95],v[36:39]
v_exp_f32 v154,v154
v_mfma_f32_16x16x32_bf16 v[172:175],a[152:155],v[88:91],v[172:175]
v_exp_f32 v155,v155
v_mfma_f32_16x16x32_bf16 v[172:175],a[148:151],v[84:87],v[172:175]
v_exp_f32 v156,v156
v_mfma_f32_16x16x32_bf16 v[172:175],a[144:147],v[80:83],v[172:175]
v_exp_f32 v157,v157
v_mfma_f32_16x16x32_bf16 v[184:187],a[160:163],v[80:83],v[36:39]
buffer_load_dwordx4 v5,s[24:27],s47 offen offset:1024 nt lds
v_mfma_f32_16x16x32_bf16 v[184:187],a[164:167],v[84:87],v[184:187]
v_exp_f32 v158,v158
v_mfma_f32_16x16x32_bf16 v[184:187],a[168:171],v[88:91],v[184:187]
v_exp_f32 v159,v159
v_mfma_f32_16x16x32_bf16 v[184:187],a[172:175],v[92:95],v[184:187]
v_exp_f32 v168,v168
v_mfma_f32_16x16x32_bf16 v[188:191],a[188:191],v[92:95],v[36:39]
v_exp_f32 v169,v169
v_mfma_f32_16x16x32_bf16 v[188:191],a[184:187],v[88:91],v[188:191]
v_exp_f32 v170,v170
v_mfma_f32_16x16x32_bf16 v[188:191],a[180:183],v[84:87],v[188:191]
v_exp_f32 v171,v171
v_mfma_f32_16x16x32_bf16 v[188:191],a[176:179],v[80:83],v[188:191]
v_exp_f32 v172,v172
v_mfma_f32_16x16x32_bf16 a[96:99],a[192:195],v[232:235],a[96:99]
ds_read_b64_tr_b16 a[192:193],v2
v_cvt_pk_bf16_f32 v168,v168,v169
v_mfma_f32_16x16x32_bf16 a[96:99],a[196:199],v[236:239],a[96:99]
ds_read_b64_tr_b16 a[194:195],v2 offset:4096
v_cvt_pk_bf16_f32 v169,v170,v171
v_mfma_f32_16x16x32_bf16 a[100:103],a[204:207],v[236:239],a[100:103]
v_exp_f32 v173,v173
v_mfma_f32_16x16x32_bf16 a[100:103],a[200:203],v[232:235],a[100:103]
ds_read_b64_tr_b16 a[196:197],v2 offset:8192
v_cvt_pk_bf16_f32 v170,v172,v173
v_mfma_f32_16x16x32_bf16 a[104:107],a[208:211],v[232:235],a[104:107]
v_exp_f32 v174,v174
v_mfma_f32_16x16x32_bf16 a[104:107],a[212:215],v[236:239],a[104:107]
v_exp_f32 v175,v175
v_mfma_f32_16x16x32_bf16 a[108:111],a[220:223],v[236:239],a[108:111]
buffer_load_dwordx4 v4,s[24:27],s47 offen offset:2048 nt lds
v_mfma_f32_16x16x32_bf16 a[108:111],a[216:219],v[232:235],a[108:111]
ds_read_b64_tr_b16 a[198:199],v2 offset:12288
v_cvt_pk_bf16_f32 v171,v174,v175
v_mfma_f32_16x16x32_bf16 a[112:115],a[224:227],v[232:235],a[112:115]
v_exp_f32 v184,v184
v_mfma_f32_16x16x32_bf16 a[112:115],a[228:231],v[236:239],a[112:115]
v_exp_f32 v185,v185
v_mfma_f32_16x16x32_bf16 a[116:119],a[236:239],v[236:239],a[116:119]
ds_read_b64_tr_b16 a[204:205],v3 offset:8192
v_cvt_pk_bf16_f32 v136,v136,v137
v_mfma_f32_16x16x32_bf16 a[116:119],a[232:235],v[232:235],a[116:119]
ds_read_b64_tr_b16 a[206:207],v3 offset:12288
v_cvt_pk_bf16_f32 v172,v184,v185
v_mfma_f32_16x16x32_bf16 a[120:123],a[240:243],v[232:235],a[120:123]
ds_read_b64_tr_b16 a[200:201],v3
v_mfma_f32_16x16x32_bf16 a[120:123],a[244:247],v[236:239],a[120:123]
ds_read_b64_tr_b16 a[202:203],v3 offset:4096
v_cvt_pk_bf16_f32 v137,v138,v139
v_mfma_f32_16x16x32_bf16 a[124:127],a[252:255],v[236:239],a[124:127]
ds_read_b64_tr_b16 a[208:209],v2 offset:256
v_mfma_f32_16x16x32_bf16 a[124:127],a[248:251],v[232:235],a[124:127]
ds_read_b64_tr_b16 a[210:211],v2 offset:4352
v_smfmac_f32_16x16x64_bf16 v[60:63],v[28:31],v[232:239],v27
ds_read_b64_tr_b16 a[212:213],v2 offset:8448
ds_read_b64_tr_b16 a[214:215],v2 offset:12544
v_cvt_pk_bf16_f32 v138,v140,v141
ds_read_b64_tr_b16 a[220:221],v3 offset:8448
ds_read_b64_tr_b16 a[222:223],v3 offset:12544
v_cvt_pk_bf16_f32 v139,v142,v143
buffer_load_dwordx4 v5,s[24:27],s47 offen offset:3072 nt lds
s_or_b32 s47,s47,0x18000
s_addk_i32 s47,4096
ds_read_b64_tr_b16 a[216:217],v3 offset:256
s_and_b32 s47,s47,0x27000
ds_read_b64_tr_b16 a[218:219],v3 offset:4352
v_cvt_pk_bf16_f32 v140,v152,v153
s_or_b32 s47,s47,s46
ds_read_b64_tr_b16 a[224:225],v2 offset:512
ds_read_b64_tr_b16 a[226:227],v2 offset:4608
ds_read_b64_tr_b16 a[228:229],v2 offset:8704
v_cvt_pk_bf16_f32 v141,v154,v155
ds_read_b64_tr_b16 a[230:231],v2 offset:12800
ds_read_b64_tr_b16 a[236:237],v3 offset:8704
ds_read_b64_tr_b16 a[238:239],v3 offset:12800
v_cvt_pk_bf16_f32 v142,v156,v157
ds_read_b64_tr_b16 a[232:233],v3 offset:512
ds_read_b64_tr_b16 a[234:235],v3 offset:4608
ds_read_b64_tr_b16 a[240:241],v2 offset:768
v_cvt_pk_bf16_f32 v143,v158,v159
buffer_load_dword v26,v24,s[88:91],s93 offen
s_waitcnt lgkmcnt(15)
v_mfma_f32_16x16x32_bf16 a[0:3],a[192:195],v[136:139],a[0:3]
ds_read_b64_tr_b16 a[242:243],v2 offset:4864
v_mfma_f32_16x16x32_bf16 a[0:3],a[196:199],v[140:143],a[0:3]
ds_read_b64_tr_b16 a[244:245],v2 offset:8960
v_mfma_f32_16x16x32_bf16 a[4:7],a[204:207],v[140:143],a[4:7]
ds_read_b64_tr_b16 a[246:247],v2 offset:13056
v_mfma_f32_16x16x32_bf16 a[4:7],a[200:203],v[136:139],a[4:7]
ds_read_b64_tr_b16 a[252:253],v3 offset:8960
v_mfma_f32_16x16x32_bf16 a[8:11],a[208:211],v[136:139],a[8:11]
s_waitcnt lgkmcnt(15)
ds_read_b64_tr_b16 a[254:255],v3 offset:13056
v_mfma_f32_16x16x32_bf16 a[8:11],a[212:215],v[140:143],a[8:11]
ds_read_b64_tr_b16 a[248:249],v3 offset:768
v_mfma_f32_16x16x32_bf16 a[12:15],a[220:223],v[140:143],a[12:15]
s_waitcnt lgkmcnt(15)
ds_read_b64_tr_b16 a[250:251],v3 offset:4864
v_mfma_f32_16x16x32_bf16 a[12:15],a[216:219],v[136:139],a[12:15]
s_bitcmp1_b32 s45,0
s_cbranch_scc0 L_t31v20_%=
s_mov_b32 m0,s80
s_addk_i32 s80,4096
buffer_load_dwordx4 v25,s[20:23],s83 offen nt lds
s_addk_i32 s83,256
L_t31v20_%=:
s_waitcnt lgkmcnt(7)
v_mfma_f32_16x16x32_bf16 a[16:19],a[224:227],v[136:139],a[16:19]
v_exp_f32 v186,v186
v_mfma_f32_16x16x32_bf16 a[16:19],a[228:231],v[140:143],a[16:19]
v_exp_f32 v187,v187
v_mfma_f32_16x16x32_bf16 a[20:23],a[236:239],v[140:143],a[20:23]
v_exp_f32 v188,v188
v_mfma_f32_16x16x32_bf16 a[20:23],a[232:235],v[136:139],a[20:23]
s_waitcnt lgkmcnt(3)
v_exp_f32 v189,v189
v_mfma_f32_16x16x32_bf16 a[24:27],a[240:243],v[136:139],a[24:27]
v_exp_f32 v190,v190
v_mfma_f32_16x16x32_bf16 a[24:27],a[244:247],v[140:143],a[24:27]
s_waitcnt lgkmcnt(1)
v_cvt_pk_bf16_f32 v173,v186,v187
v_mfma_f32_16x16x32_bf16 a[28:31],a[252:255],v[140:143],a[28:31]
s_waitcnt lgkmcnt(0)
v_exp_f32 v191,v191
v_mfma_f32_16x16x32_bf16 a[28:31],a[248:251],v[136:139],a[28:31]
v_cvt_pk_bf16_f32 v174,v188,v189
v_cvt_pk_bf16_f32 v175,v190,v191
s_cmp_lg_u32 s53,0
v_smfmac_f32_16x16x64_bf16 v[48:51],v[28:31],v[136:143],v27
s_cbranch_scc1 L_att0v2_%=
s_waitcnt lgkmcnt(0)
s_nop 7
s_branch L_att0x_%=
.p2align 5
L_att0_%=:
v_mfma_f32_16x16x32_bf16 v[232:235],a[128:131],v[112:115],v[44:47]
s_waitcnt vmcnt(8) lgkmcnt(0)
v_exp_f32 v200,v200
v_mfma_f32_16x16x32_bf16 v[232:235],a[132:135],v[116:119],v[232:235]
s_barrier
s_add_u32 m0,s49,s51
ds_read_b128 a[128:131],v1
v_mfma_f32_16x16x32_bf16 v[232:235],a[136:139],v[120:123],v[232:235]
s_mov_b32 s90,L_endseg_%=-L_pc_%=+128
v_exp_f32 v201,v201
v_mfma_f32_16x16x32_bf16 v[232:235],a[140:143],v[124:127],v[232:235]
s_lshr_b32 s45,s45,1
ds_read_b128 a[132:135],v1 offset:256
v_cvt_pk_bf16_f32 v200,v200,v201
v_mfma_f32_16x16x32_bf16 v[236:239],a[156:159],v[124:127],v[44:47]
s_sub_u32 s94,s94,3
buffer_load_dwordx4 v4,s[20:23],s47 offen nt lds
v_mfma_f32_16x16x32_bf16 v[236:239],a[152:155],v[120:123],v[236:239]
s_lshr_b32 s60,s94,2
ds_read_b128 a[136:139],v1 offset:512
v_add_u32 v2,s48,v2
v_mfma_f32_16x16x32_bf16 v[236:239],a[148:151],v[116:119],v[236:239]
s_max_i32 s60,s60,124
v_exp_f32 v202,v202
v_mfma_f32_16x16x32_bf16 v[236:239],a[144:147],v[112:115],v[236:239]
s_add_u32 s62,s50,32768
ds_read_b128 a[140:143],v1 offset:768
v_add_u32 v3,s48,v3
v_mfma_f32_16x16x32_bf16 v[248:251],a[160:163],v[112:115],v[44:47]
s_and_b32 s62,s62,131071
v_exp_f32 v203,v203
v_mfma_f32_16x16x32_bf16 v[248:251],a[164:167],v[116:119],v[248:251]
s_addk_i32 s54,16384
ds_read_b128 a[156:159],v1 offset:4864
v_cvt_pk_bf16_f32 v201,v202,v203
v_mfma_f32_16x16x32_bf16 v[248:251],a[168:171],v[120:123],v[248:251]
s_sub_u32 s48,s62,s50
)A"
R"A(v_exp_f32 v204,v204
v_mfma_f32_16x16x32_bf16 v[248:251],a[172:175],v[124:127],v[248:251]
s_sub_u32 s53,s53,1
v_exp_f32 v205,v205
v_mfma_f32_16x16x32_bf16 v[252:255],a[188:191],v[124:127],v[44:47]
s_and_b32 s46,s54,0xfffc0000
ds_read_b128 a[152:155],v1 offset:4608
v_cvt_pk_bf16_f32 v202,v204,v205
v_mfma_f32_16x16x32_bf16 v[252:255],a[184:187],v[120:123],v[252:255]
s_mov_b32 s50,s62
v_exp_f32 v206,v206
v_mfma_f32_16x16x32_bf16 v[252:255],a[180:183],v[116:119],v[252:255]
s_xor_b32 s49,s50,65536
v_exp_f32 v207,v207
v_mfma_f32_16x16x32_bf16 v[252:255],a[176:179],v[112:115],v[252:255]
ds_read_b128 a[148:151],v1 offset:4352
v_cvt_pk_bf16_f32 v203,v206,v207
v_mfma_f32_16x16x32_bf16 a[32:35],a[192:195],v[168:171],a[32:35]
v_exp_f32 v216,v216
v_mfma_f32_16x16x32_bf16 a[32:35],a[196:199],v[172:175],a[32:35]
ds_read_b128 a[144:147],v1 offset:4096
v_mfma_f32_16x16x32_bf16 a[36:39],a[204:207],v[172:175],a[36:39]
buffer_load_dwordx4 v4,s[20:23],s47 offen offset:1024 nt lds
v_mfma_f32_16x16x32_bf16 a[36:39],a[200:203],v[168:171],a[36:39]
ds_read_b128 a[160:163],v1 offset:8192
v_mfma_f32_16x16x32_bf16 a[40:43],a[208:211],v[168:171],a[40:43]
v_exp_f32 v217,v217
v_mfma_f32_16x16x32_bf16 a[40:43],a[212:215],v[172:175],a[40:43]
ds_read_b128 a[164:167],v1 offset:8448
v_cvt_pk_bf16_f32 v204,v216,v217
v_mfma_f32_16x16x32_bf16 a[44:47],a[220:223],v[172:175],a[44:47]
v_exp_f32 v218,v218
v_mfma_f32_16x16x32_bf16 a[44:47],a[216:219],v[168:171],a[44:47]
ds_read_b128 a[168:171],v1 offset:8704
v_mfma_f32_16x16x32_bf16 a[48:51],a[224:227],v[168:171],a[48:51]
v_exp_f32 v219,v219
v_mfma_f32_16x16x32_bf16 a[48:51],a[228:231],v[172:175],a[48:51]
ds_read_b128 a[172:175],v1 offset:8960
v_cvt_pk_bf16_f32 v205,v218,v219
v_mfma_f32_16x16x32_bf16 a[52:55],a[236:239],v[172:175],a[52:55]
v_exp_f32 v220,v220
v_mfma_f32_16x16x32_bf16 a[52:55],a[232:235],v[168:171],a[52:55]
ds_read_b128 a[188:191],v1 offset:13056
v_mfma_f32_16x16x32_bf16 a[56:59],a[240:243],v[168:171],a[56:59]
v_exp_f32 v221,v221
v_mfma_f32_16x16x32_bf16 a[56:59],a[244:247],v[172:175],a[56:59]
ds_read_b128 a[184:187],v1 offset:12800
v_cvt_pk_bf16_f32 v206,v220,v221
v_mfma_f32_16x16x32_bf16 a[60:63],a[252:255],v[172:175],a[60:63]
v_exp_f32 v222,v222
v_mfma_f32_16x16x32_bf16 a[60:63],a[248:251],v[168:171],a[60:63]
ds_read_b128 a[180:183],v1 offset:12544
v_smfmac_f32_16x16x64_bf16 v[52:55],v[28:31],v[168:175],v27
s_waitcnt lgkmcnt(4)
v_mfma_f32_16x16x32_bf16 v[136:139],a[128:131],v[64:67],v[32:35]
buffer_load_dwordx4 v5,s[20:23],s47 offen offset:2048 nt lds
v_mfma_f32_16x16x32_bf16 v[136:139],a[132:135],v[68:71],v[136:139]
ds_read_b128 a[176:179],v1 offset:12288
v_add_u32 v1,s48,v1
v_mfma_f32_16x16x32_bf16 v[136:139],a[136:139],v[72:75],v[136:139]
v_exp_f32 v223,v223
v_mfma_f32_16x16x32_bf16 v[136:139],a[140:143],v[76:79],v[136:139]
v_cvt_pk_bf16_f32 v207,v222,v223
v_mfma_f32_16x16x32_bf16 v[140:143],a[156:159],v[76:79],v[32:35]
v_exp_f32 v232,v232
v_mfma_f32_16x16x32_bf16 v[140:143],a[152:155],v[72:75],v[140:143]
v_exp_f32 v233,v233
v_mfma_f32_16x16x32_bf16 v[140:143],a[148:151],v[68:71],v[140:143]
v_exp_f32 v234,v234
v_mfma_f32_16x16x32_bf16 v[140:143],a[144:147],v[64:67],v[140:143]
v_exp_f32 v235,v235
v_mfma_f32_16x16x32_bf16 v[152:155],a[160:163],v[64:67],v[32:35]
v_exp_f32 v236,v236
v_mfma_f32_16x16x32_bf16 v[152:155],a[164:167],v[68:71],v[152:155]
v_exp_f32 v237,v237
v_mfma_f32_16x16x32_bf16 v[152:155],a[168:171],v[72:75],v[152:155]
s_waitcnt lgkmcnt(0)
v_exp_f32 v238,v238
v_mfma_f32_16x16x32_bf16 v[152:155],a[172:175],v[76:79],v[152:155]
v_exp_f32 v239,v239
v_mfma_f32_16x16x32_bf16 v[156:159],a[188:191],v[76:79],v[32:35]
v_exp_f32 v248,v248
v_mfma_f32_16x16x32_bf16 v[156:159],a[184:187],v[72:75],v[156:159]
buffer_load_dwordx4 v5,s[20:23],s47 offen offset:3072 nt lds
v_mfma_f32_16x16x32_bf16 v[156:159],a[180:183],v[68:71],v[156:159]
v_exp_f32 v249,v249
v_mfma_f32_16x16x32_bf16 v[156:159],a[176:179],v[64:67],v[156:159]
v_exp_f32 v250,v250
v_mfma_f32_16x16x32_bf16 a[64:67],a[192:195],v[200:203],a[64:67]
v_exp_f32 v251,v251
v_mfma_f32_16x16x32_bf16 a[64:67],a[196:199],v[204:207],a[64:67]
v_exp_f32 v252,v252
v_mfma_f32_16x16x32_bf16 a[68:71],a[204:207],v[204:207],a[68:71]
v_exp_f32 v253,v253
v_mfma_f32_16x16x32_bf16 a[68:71],a[200:203],v[200:203],a[68:71]
v_exp_f32 v254,v254
v_mfma_f32_16x16x32_bf16 a[72:75],a[208:211],v[200:203],a[72:75]
v_exp_f32 v255,v255
v_mfma_f32_16x16x32_bf16 a[72:75],a[212:215],v[204:207],a[72:75]
v_exp_f32 v136,v136
v_mfma_f32_16x16x32_bf16 a[76:79],a[220:223],v[204:207],a[76:79]
v_exp_f32 v137,v137
v_mfma_f32_16x16x32_bf16 a[76:79],a[216:219],v[200:203],a[76:79]
v_exp_f32 v138,v138
v_mfma_f32_16x16x32_bf16 a[80:83],a[224:227],v[200:203],a[80:83]
s_addk_i32 m0,16384
v_exp_f32 v139,v139
v_mfma_f32_16x16x32_bf16 a[80:83],a[228:231],v[204:207],a[80:83]
v_exp_f32 v140,v140
v_mfma_f32_16x16x32_bf16 a[84:87],a[236:239],v[204:207],a[84:87]
v_exp_f32 v141,v141
v_mfma_f32_16x16x32_bf16 a[84:87],a[232:235],v[200:203],a[84:87]
buffer_load_dwordx4 v4,s[24:27],s47 offen nt lds
v_mfma_f32_16x16x32_bf16 a[88:91],a[240:243],v[200:203],a[88:91]
v_exp_f32 v142,v142
v_mfma_f32_16x16x32_bf16 a[88:91],a[244:247],v[204:207],a[88:91]
v_exp_f32 v143,v143
v_mfma_f32_16x16x32_bf16 a[92:95],a[252:255],v[204:207],a[92:95]
v_cvt_pk_bf16_f32 v232,v232,v233
v_cvt_pk_bf16_f32 v233,v234,v235
v_mfma_f32_16x16x32_bf16 a[92:95],a[248:251],v[200:203],a[92:95]
v_cvt_pk_bf16_f32 v234,v236,v237
v_cvt_pk_bf16_f32 v235,v238,v239
v_smfmac_f32_16x16x64_bf16 v[56:59],v[28:31],v[200:207],v27
v_cvt_pk_bf16_f32 v236,v248,v249
v_mfma_f32_16x16x32_bf16 v[168:171],a[128:131],v[80:83],v[36:39]
v_cvt_pk_bf16_f32 v237,v250,v251
v_cvt_pk_bf16_f32 v238,v252,v253
v_mfma_f32_16x16x32_bf16 v[168:171],a[132:135],v[84:87],v[168:171]
v_cvt_pk_bf16_f32 v239,v254,v255
v_mfma_f32_16x16x32_bf16 v[168:171],a[136:139],v[88:91],v[168:171]
v_exp_f32 v152,v152
v_mfma_f32_16x16x32_bf16 v[168:171],a[140:143],v[92:95],v[168:171]
v_exp_f32 v153,v153
v_mfma_f32_16x16x32_bf16 v[172:175],a[156:159],v[92:95],v[36:39]
v_exp_f32 v154,v154
v_mfma_f32_16x16x32_bf16 v[172:175],a[152:155],v[88:91],v[172:175]
v_exp_f32 v155,v155
v_mfma_f32_16x16x32_bf16 v[172:175],a[148:151],v[84:87],v[172:175]
v_exp_f32 v156,v156
v_mfma_f32_16x16x32_bf16 v[172:175],a[144:147],v[80:83],v[172:175]
v_exp_f32 v157,v157
v_mfma_f32_16x16x32_bf16 v[184:187],a[160:163],v[80:83],v[36:39]
buffer_load_dwordx4 v5,s[24:27],s47 offen offset:1024 nt lds
v_mfma_f32_16x16x32_bf16 v[184:187],a[164:167],v[84:87],v[184:187]
v_exp_f32 v158,v158
v_mfma_f32_16x16x32_bf16 v[184:187],a[168:171],v[88:91],v[184:187]
v_exp_f32 v159,v159
v_mfma_f32_16x16x32_bf16 v[184:187],a[172:175],v[92:95],v[184:187]
v_exp_f32 v168,v168
v_mfma_f32_16x16x32_bf16 v[188:191],a[188:191],v[92:95],v[36:39]
v_exp_f32 v169,v169
v_mfma_f32_16x16x32_bf16 v[188:191],a[184:187],v[88:91],v[188:191]
v_exp_f32 v170,v170
v_mfma_f32_16x16x32_bf16 v[188:191],a[180:183],v[84:87],v[188:191]
v_exp_f32 v171,v171
v_mfma_f32_16x16x32_bf16 v[188:191],a[176:179],v[80:83],v[188:191]
v_exp_f32 v172,v172
v_mfma_f32_16x16x32_bf16 a[96:99],a[192:195],v[232:235],a[96:99]
ds_read_b64_tr_b16 a[192:193],v2
v_cvt_pk_bf16_f32 v168,v168,v169
v_mfma_f32_16x16x32_bf16 a[96:99],a[196:199],v[236:239],a[96:99]
ds_read_b64_tr_b16 a[194:195],v2 offset:4096
v_cvt_pk_bf16_f32 v169,v170,v171
v_mfma_f32_16x16x32_bf16 a[100:103],a[204:207],v[236:239],a[100:103]
v_exp_f32 v173,v173
v_mfma_f32_16x16x32_bf16 a[100:103],a[200:203],v[232:235],a[100:103]
ds_read_b64_tr_b16 a[196:197],v2 offset:8192
v_cvt_pk_bf16_f32 v170,v172,v173
v_mfma_f32_16x16x32_bf16 a[104:107],a[208:211],v[232:235],a[104:107]
v_exp_f32 v174,v174
v_mfma_f32_16x16x32_bf16 a[104:107],a[212:215],v[236:239],a[104:107]
v_exp_f32 v175,v175
v_mfma_f32_16x16x32_bf16 a[108:111],a[220:223],v[236:239],a[108:111]
buffer_load_dwordx4 v4,s[24:27],s47 offen offset:2048 nt lds
v_mfma_f32_16x16x32_bf16 a[108:111],a[216:219],v[232:235],a[108:111]
ds_read_b64_tr_b16 a[198:199],v2 offset:12288
v_cvt_pk_bf16_f32 v171,v174,v175
v_mfma_f32_16x16x32_bf16 a[112:115],a[224:227],v[232:235],a[112:115]
v_exp_f32 v184,v184
v_mfma_f32_16x16x32_bf16 a[112:115],a[228:231],v[236:239],a[112:115]
v_exp_f32 v185,v185
v_mfma_f32_16x16x32_bf16 a[116:119],a[236:239],v[236:239],a[116:119]
ds_read_b64_tr_b16 a[204:205],v3 offset:8192
v_cvt_pk_bf16_f32 v136,v136,v137
v_mfma_f32_16x16x32_bf16 a[116:119],a[232:235],v[232:235],a[116:119]
ds_read_b64_tr_b16 a[206:207],v3 offset:12288
v_cvt_pk_bf16_f32 v172,v184,v185
v_mfma_f32_16x16x32_bf16 a[120:123],a[240:243],v[232:235],a[120:123]
ds_read_b64_tr_b16 a[200:201],v3
v_mfma_f32_16x16x32_bf16 a[120:123],a[244:247],v[236:239],a[120:123]
ds_read_b64_tr_b16 a[202:203],v3 offset:4096
v_cvt_pk_bf16_f32 v137,v138,v139
v_mfma_f32_16x16x32_bf16 a[124:127],a[252:255],v[236:239],a[124:127]
ds_read_b64_tr_b16 a[208:209],v2 offset:256
v_mfma_f32_16x16x32_bf16 a[124:127],a[248:251],v[232:235],a[124:127]
ds_read_b64_tr_b16 a[210:211],v2 offset:4352
v_smfmac_f32_16x16x64_bf16 v[60:63],v[28:31],v[232:239],v27
ds_read_b64_tr_b16 a[212:213],v2 offset:8448
v_mfma_f32_16x16x32_bf16 v[200:203],a[128:131],v[96:99],v[40:43]
ds_read_b64_tr_b16 a[214:215],v2 offset:12544
v_cvt_pk_bf16_f32 v138,v140,v141
v_mfma_f32_16x16x32_bf16 v[200:203],a[132:135],v[100:103],v[200:203]
ds_read_b64_tr_b16 a[220:221],v3 offset:8448
v_mfma_f32_16x16x32_bf16 v[200:203],a[136:139],v[104:107],v[200:203]
ds_read_b64_tr_b16 a[222:223],v3 offset:12544
v_cvt_pk_bf16_f32 v139,v142,v143
v_mfma_f32_16x16x32_bf16 v[200:203],a[140:143],v[108:111],v[200:203]
buffer_load_dwordx4 v5,s[24:27],s47 offen offset:3072 nt lds
v_mfma_f32_16x16x32_bf16 v[204:207],a[156:159],v[108:111],v[40:43]
s_or_b32 s47,s47,0x18000
s_addk_i32 s47,4096
ds_read_b64_tr_b16 a[216:217],v3 offset:256
v_mfma_f32_16x16x32_bf16 v[204:207],a[152:155],v[104:107],v[204:207]
s_and_b32 s47,s47,0x27000
ds_read_b64_tr_b16 a[218:219],v3 offset:4352
v_cvt_pk_bf16_f32 v140,v152,v153
v_mfma_f32_16x16x32_bf16 v[204:207],a[148:151],v[100:103],v[204:207]
s_or_b32 s47,s47,s46
ds_read_b64_tr_b16 a[224:225],v2 offset:512
v_mfma_f32_16x16x32_bf16 v[204:207],a[144:147],v[96:99],v[204:207]
ds_read_b64_tr_b16 a[226:227],v2 offset:4608
v_mfma_f32_16x16x32_bf16 v[216:219],a[160:163],v[96:99],v[40:43]
ds_read_b64_tr_b16 a[228:229],v2 offset:8704
v_cvt_pk_bf16_f32 v141,v154,v155
v_mfma_f32_16x16x32_bf16 v[216:219],a[164:167],v[100:103],v[216:219]
ds_read_b64_tr_b16 a[230:231],v2 offset:12800
v_mfma_f32_16x16x32_bf16 v[216:219],a[168:171],v[104:107],v[216:219]
ds_read_b64_tr_b16 a[236:237],v3 offset:8704
v_mfma_f32_16x16x32_bf16 v[216:219],a[172:175],v[108:111],v[216:219]
ds_read_b64_tr_b16 a[238:239],v3 offset:12800
v_cvt_pk_bf16_f32 v142,v156,v157
v_mfma_f32_16x16x32_bf16 v[220:223],a[188:191],v[108:111],v[40:43]
ds_read_b64_tr_b16 a[232:233],v3 offset:512
v_mfma_f32_16x16x32_bf16 v[220:223],a[184:187],v[104:107],v[220:223]
ds_read_b64_tr_b16 a[234:235],v3 offset:4608
v_mfma_f32_16x16x32_bf16 v[220:223],a[180:183],v[100:103],v[220:223]
ds_read_b64_tr_b16 a[240:241],v2 offset:768
v_cvt_pk_bf16_f32 v143,v158,v159
v_mfma_f32_16x16x32_bf16 v[220:223],a[176:179],v[96:99],v[220:223]
s_waitcnt lgkmcnt(7)
buffer_load_dword v26,v24,s[88:91],s93 offen
v_mfma_f32_16x16x32_bf16 a[0:3],a[192:195],v[136:139],a[0:3]
ds_read_b64_tr_b16 a[242:243],v2 offset:4864
v_mfma_f32_16x16x32_bf16 a[0:3],a[196:199],v[140:143],a[0:3]
ds_read_b64_tr_b16 a[244:245],v2 offset:8960
v_mfma_f32_16x16x32_bf16 a[4:7],a[204:207],v[140:143],a[4:7]
ds_read_b64_tr_b16 a[246:247],v2 offset:13056
v_mfma_f32_16x16x32_bf16 a[4:7],a[200:203],v[136:139],a[4:7]
s_memrealtime s[76:77]
ds_read_b64_tr_b16 a[252:253],v3 offset:8960
v_mfma_f32_16x16x32_bf16 a[8:11],a[208:211],v[136:139],a[8:11]
ds_read_b64_tr_b16 a[254:255],v3 offset:13056
v_mfma_f32_16x16x32_bf16 a[8:11],a[212:215],v[140:143],a[8:11]
ds_read_b64_tr_b16 a[248:249],v3 offset:768
v_mfma_f32_16x16x32_bf16 a[12:15],a[220:223],v[140:143],a[12:15]
ds_read_b64_tr_b16 a[250:251],v3 offset:4864
v_mfma_f32_16x16x32_bf16 a[12:15],a[216:219],v[136:139],a[12:15]
)A"
R"A(s_bitcmp1_b32 s45,0
s_cbranch_scc0 L_t310_%=
s_mov_b32 m0,s80
s_addk_i32 s80,4096
buffer_load_dwordx4 v25,s[20:23],s83 offen nt lds
s_addk_i32 s83,256
L_t310_%=:
v_mfma_f32_16x16x32_bf16 a[16:19],a[224:227],v[136:139],a[16:19]
s_waitcnt lgkmcnt(6)
v_exp_f32 v186,v186
v_mfma_f32_16x16x32_bf16 a[16:19],a[228:231],v[140:143],a[16:19]
v_exp_f32 v187,v187
v_mfma_f32_16x16x32_bf16 a[20:23],a[236:239],v[140:143],a[20:23]
s_waitcnt lgkmcnt(0)
v_exp_f32 v188,v188
v_mfma_f32_16x16x32_bf16 a[20:23],a[232:235],v[136:139],a[20:23]
s_sub_u32 s79,s78,s76
v_exp_f32 v189,v189
v_mfma_f32_16x16x32_bf16 a[24:27],a[240:243],v[136:139],a[24:27]
s_add_u32 s78,s76,s60
v_exp_f32 v190,v190
v_mfma_f32_16x16x32_bf16 a[24:27],a[244:247],v[140:143],a[24:27]
s_cmp_gt_i32 s79,s95
s_cbranch_scc1 L_ps3_%=
L_pr3_%=:
v_cvt_pk_bf16_f32 v173,v186,v187
v_mfma_f32_16x16x32_bf16 a[28:31],a[252:255],v[140:143],a[28:31]
v_exp_f32 v191,v191
v_mfma_f32_16x16x32_bf16 a[28:31],a[248:251],v[136:139],a[28:31]
v_cvt_pk_bf16_f32 v174,v188,v189
v_cvt_pk_bf16_f32 v175,v190,v191
s_cmp_lg_u32 s53,0
v_smfmac_f32_16x16x64_bf16 v[48:51],v[28:31],v[136:143],v27
s_cbranch_scc1 L_att0_%=
L_att0x_%=:
v_mfma_f32_16x16x32_bf16 v[232:235],a[128:131],v[112:115],v[44:47]
s_waitcnt vmcnt(10)
s_sub_u32 s94,s94,3
v_exp_f32 v200,v200
v_mfma_f32_16x16x32_bf16 v[232:235],a[132:135],v[116:119],v[232:235]
s_barrier
s_add_u32 m0,0,s51
ds_read_b128 a[128:131],v1
v_mfma_f32_16x16x32_bf16 v[232:235],a[136:139],v[120:123],v[232:235]
s_lshr_b32 s60,s94,2
v_exp_f32 v201,v201
v_mfma_f32_16x16x32_bf16 v[232:235],a[140:143],v[124:127],v[232:235]
s_max_i32 s60,s60,124
ds_read_b128 a[132:135],v1 offset:256
v_cvt_pk_bf16_f32 v200,v200,v201
v_mfma_f32_16x16x32_bf16 v[236:239],a[156:159],v[124:127],v[44:47]
s_addk_i32 s54,16384
buffer_load_dwordx4 v4,s[20:23],s47 offen nt lds
v_mfma_f32_16x16x32_bf16 v[236:239],a[152:155],v[120:123],v[236:239]
s_and_b32 s46,s54,0xfffc0000
ds_read_b128 a[136:139],v1 offset:512
v_and_b32 v18,48,v6
v_mfma_f32_16x16x32_bf16 v[236:239],a[148:151],v[116:119],v[236:239]
s_sub_u32 s94,s94,3
v_exp_f32 v202,v202
v_mfma_f32_16x16x32_bf16 v[236:239],a[144:147],v[112:115],v[236:239]
s_addk_i32 s54,16384
ds_read_b128 a[140:143],v1 offset:768
v_lshrrev_b32 v19,4,v6
v_mfma_f32_16x16x32_bf16 v[248:251],a[160:163],v[112:115],v[44:47]
s_mul_i32 s83,s41,5
v_exp_f32 v203,v203
v_mfma_f32_16x16x32_bf16 v[248:251],a[164:167],v[116:119],v[248:251]
s_lshl_b32 s43,s40,8
ds_read_b128 a[156:159],v1 offset:4864
v_cvt_pk_bf16_f32 v201,v202,v203
v_mfma_f32_16x16x32_bf16 v[248:251],a[168:171],v[120:123],v[248:251]
s_add_u32 s83,s83,s42
v_exp_f32 v204,v204
v_mfma_f32_16x16x32_bf16 v[248:251],a[172:175],v[124:127],v[248:251]
s_add_u32 s83,s83,15
ds_read_b128 a[152:155],v1 offset:4608
v_lshlrev_b32 v18,2,v18
v_mfma_f32_16x16x32_bf16 v[252:255],a[188:191],v[124:127],v[44:47]
s_lshl_b32 s44,s40,5
v_exp_f32 v205,v205
v_mfma_f32_16x16x32_bf16 v[252:255],a[184:187],v[120:123],v[252:255]
s_add_u32 s84,s55,0
ds_read_b128 a[148:151],v1 offset:4352
v_cvt_pk_bf16_f32 v202,v204,v205
v_mfma_f32_16x16x32_bf16 v[252:255],a[180:183],v[116:119],v[252:255]
s_add_u32 s85,s55,4096
v_exp_f32 v206,v206
v_mfma_f32_16x16x32_bf16 v[252:255],a[176:179],v[112:115],v[252:255]
s_add_u32 s86,s55,16384
ds_read_b128 a[144:147],v1 offset:4096
v_and_b32 v19,48,v19
v_mfma_f32_16x16x32_bf16 a[32:35],a[192:195],v[168:171],a[32:35]
s_add_u32 s87,s55,20480
v_exp_f32 v207,v207
v_mfma_f32_16x16x32_bf16 a[32:35],a[196:199],v[172:175],a[32:35]
s_and_b32 s44,s44,32
ds_read_b128 a[160:163],v1 offset:8192
v_cvt_pk_bf16_f32 v203,v206,v207
v_mfma_f32_16x16x32_bf16 a[36:39],a[204:207],v[172:175],a[36:39]
s_max_i32 s84,s84,s18
buffer_load_dwordx4 v4,s[20:23],s47 offen offset:1024 nt lds
v_mfma_f32_16x16x32_bf16 a[36:39],a[200:203],v[168:171],a[36:39]
s_max_i32 s85,s85,s19
ds_read_b128 a[164:167],v1 offset:8448
v_add_u32 v18,v18,v19
v_mfma_f32_16x16x32_bf16 a[40:43],a[208:211],v[168:171],a[40:43]
s_max_i32 s86,s86,s100
v_exp_f32 v216,v216
v_mfma_f32_16x16x32_bf16 a[40:43],a[212:215],v[172:175],a[40:43]
s_max_i32 s87,s87,s101
ds_read_b128 a[168:171],v1 offset:8704
v_add_u32 v18,98304,v18
v_mfma_f32_16x16x32_bf16 a[44:47],a[220:223],v[172:175],a[44:47]
s_lshl_b32 s80,s40,10
v_exp_f32 v217,v217
v_mfma_f32_16x16x32_bf16 a[44:47],a[216:219],v[168:171],a[44:47]
s_mov_b32 s50,32768
ds_read_b128 a[172:175],v1 offset:8960
v_cvt_pk_bf16_f32 v204,v216,v217
v_mfma_f32_16x16x32_bf16 a[48:51],a[224:227],v[168:171],a[48:51]
s_mov_b32 s49,98304
v_exp_f32 v218,v218
v_mfma_f32_16x16x32_bf16 a[48:51],a[228:231],v[172:175],a[48:51]
s_mov_b32 s45,0x2a8
ds_read_b128 a[188:191],v1 offset:13056
v_add_u32 v2,s48,v2
v_mfma_f32_16x16x32_bf16 a[52:55],a[236:239],v[172:175],a[52:55]
s_add_u32 s93,s92,L_endseg_%=-L_pc_%=
v_exp_f32 v219,v219
v_mfma_f32_16x16x32_bf16 a[52:55],a[232:235],v[168:171],a[52:55]
ds_read_b128 a[184:187],v1 offset:12800
v_cvt_pk_bf16_f32 v205,v218,v219
v_mfma_f32_16x16x32_bf16 a[56:59],a[240:243],v[168:171],a[56:59]
v_exp_f32 v220,v220
v_mfma_f32_16x16x32_bf16 a[56:59],a[244:247],v[172:175],a[56:59]
ds_read_b128 a[180:183],v1 offset:12544
v_add_u32 v3,s48,v3
v_mfma_f32_16x16x32_bf16 a[60:63],a[252:255],v[172:175],a[60:63]
s_mov_b32 s48,32768
v_exp_f32 v221,v221
v_mfma_f32_16x16x32_bf16 a[60:63],a[248:251],v[168:171],a[60:63]
ds_read_b128 a[176:179],v1 offset:12288
v_cvt_pk_bf16_f32 v206,v220,v221
v_smfmac_f32_16x16x64_bf16 v[52:55],v[28:31],v[168:175],v27
s_waitcnt lgkmcnt(4)
v_add_u32 v1,s48,v1
v_mfma_f32_16x16x32_bf16 v[136:139],a[128:131],v[64:67],v[32:35]
buffer_load_dwordx4 v5,s[20:23],s47 offen offset:2048 nt lds
v_mfma_f32_16x16x32_bf16 v[136:139],a[132:135],v[68:71],v[136:139]
v_exp_f32 v222,v222
v_mfma_f32_16x16x32_bf16 v[136:139],a[136:139],v[72:75],v[136:139]
v_exp_f32 v223,v223
v_mfma_f32_16x16x32_bf16 v[136:139],a[140:143],v[76:79],v[136:139]
v_cvt_pk_bf16_f32 v207,v222,v223
v_subrev_u32 v25,s43,v4
v_mfma_f32_16x16x32_bf16 v[140:143],a[156:159],v[76:79],v[32:35]
s_lshl_b32 s43,s83,14
v_exp_f32 v232,v232
v_mfma_f32_16x16x32_bf16 v[140:143],a[152:155],v[72:75],v[140:143]
s_lshl_b32 s83,s83,12
v_exp_f32 v233,v233
v_mfma_f32_16x16x32_bf16 v[140:143],a[148:151],v[68:71],v[140:143]
s_and_b32 s43,s43,0x20000
v_exp_f32 v234,v234
v_mfma_f32_16x16x32_bf16 v[140:143],a[144:147],v[64:67],v[140:143]
s_and_b32 s83,s83,0x7000
v_exp_f32 v235,v235
v_mfma_f32_16x16x32_bf16 v[152:155],a[160:163],v[64:67],v[32:35]
s_or_b32 s83,s83,s43
v_cvt_pk_bf16_f32 v232,v232,v233
v_cvt_pk_bf16_f32 v233,v234,v235
v_mfma_f32_16x16x32_bf16 v[152:155],a[164:167],v[68:71],v[152:155]
s_or_b32 s83,s83,0x40000
v_exp_f32 v236,v236
v_mfma_f32_16x16x32_bf16 v[152:155],a[168:171],v[72:75],v[152:155]
v_exp_f32 v237,v237
v_mfma_f32_16x16x32_bf16 v[152:155],a[172:175],v[76:79],v[152:155]
s_waitcnt lgkmcnt(0)
v_exp_f32 v238,v238
v_mfma_f32_16x16x32_bf16 v[156:159],a[188:191],v[76:79],v[32:35]
v_exp_f32 v239,v239
v_mfma_f32_16x16x32_bf16 v[156:159],a[184:187],v[72:75],v[156:159]
buffer_load_dwordx4 v5,s[20:23],s47 offen offset:3072 nt lds
v_mfma_f32_16x16x32_bf16 v[156:159],a[180:183],v[68:71],v[156:159]
v_cvt_pk_bf16_f32 v234,v236,v237
v_cvt_pk_bf16_f32 v235,v238,v239
v_mfma_f32_16x16x32_bf16 v[156:159],a[176:179],v[64:67],v[156:159]
v_exp_f32 v248,v248
v_mfma_f32_16x16x32_bf16 a[64:67],a[192:195],v[200:203],a[64:67]
v_exp_f32 v249,v249
v_mfma_f32_16x16x32_bf16 a[64:67],a[196:199],v[204:207],a[64:67]
v_exp_f32 v250,v250
v_mfma_f32_16x16x32_bf16 a[68:71],a[204:207],v[204:207],a[68:71]
v_exp_f32 v251,v251
v_mfma_f32_16x16x32_bf16 a[68:71],a[200:203],v[200:203],a[68:71]
v_exp_f32 v252,v252
v_mfma_f32_16x16x32_bf16 a[72:75],a[208:211],v[200:203],a[72:75]
v_exp_f32 v253,v253
v_mfma_f32_16x16x32_bf16 a[72:75],a[212:215],v[204:207],a[72:75]
v_exp_f32 v254,v254
v_mfma_f32_16x16x32_bf16 a[76:79],a[220:223],v[204:207],a[76:79]
v_exp_f32 v255,v255
v_mfma_f32_16x16x32_bf16 a[76:79],a[216:219],v[200:203],a[76:79]
v_cvt_pk_bf16_f32 v236,v248,v249
v_cvt_pk_bf16_f32 v237,v250,v251
v_mfma_f32_16x16x32_bf16 a[80:83],a[224:227],v[200:203],a[80:83]
s_addk_i32 m0,16384
v_cvt_pk_bf16_f32 v238,v252,v253
v_cvt_pk_bf16_f32 v239,v254,v255
v_mfma_f32_16x16x32_bf16 a[80:83],a[228:231],v[204:207],a[80:83]
v_exp_f32 v136,v136
v_mfma_f32_16x16x32_bf16 a[84:87],a[236:239],v[204:207],a[84:87]
v_exp_f32 v137,v137
v_mfma_f32_16x16x32_bf16 a[84:87],a[232:235],v[200:203],a[84:87]
buffer_load_dwordx4 v4,s[24:27],s47 offen nt lds
v_mfma_f32_16x16x32_bf16 a[88:91],a[240:243],v[200:203],a[88:91]
v_exp_f32 v138,v138
v_mfma_f32_16x16x32_bf16 a[88:91],a[244:247],v[204:207],a[88:91]
v_exp_f32 v139,v139
v_mfma_f32_16x16x32_bf16 a[92:95],a[252:255],v[204:207],a[92:95]
v_cvt_pk_bf16_f32 v136,v136,v137
v_cvt_pk_bf16_f32 v137,v138,v139
v_mfma_f32_16x16x32_bf16 a[92:95],a[248:251],v[200:203],a[92:95]
v_exp_f32 v140,v140
v_smfmac_f32_16x16x64_bf16 v[56:59],v[28:31],v[200:207],v27
v_xor_b32 v25,s44,v25
v_mfma_f32_16x16x32_bf16 v[168:171],a[128:131],v[80:83],v[36:39]
v_exp_f32 v141,v141
v_mfma_f32_16x16x32_bf16 v[168:171],a[132:135],v[84:87],v[168:171]
v_exp_f32 v142,v142
v_mfma_f32_16x16x32_bf16 v[168:171],a[136:139],v[88:91],v[168:171]
v_exp_f32 v143,v143
v_mfma_f32_16x16x32_bf16 v[168:171],a[140:143],v[92:95],v[168:171]
v_cvt_pk_bf16_f32 v138,v140,v141
v_cvt_pk_bf16_f32 v139,v142,v143
v_mfma_f32_16x16x32_bf16 v[172:175],a[156:159],v[92:95],v[36:39]
v_exp_f32 v152,v152
v_mfma_f32_16x16x32_bf16 v[172:175],a[152:155],v[88:91],v[172:175]
v_exp_f32 v153,v153
v_mfma_f32_16x16x32_bf16 v[172:175],a[148:151],v[84:87],v[172:175]
v_exp_f32 v154,v154
v_mfma_f32_16x16x32_bf16 v[172:175],a[144:147],v[80:83],v[172:175]
v_exp_f32 v155,v155
v_mfma_f32_16x16x32_bf16 v[184:187],a[160:163],v[80:83],v[36:39]
buffer_load_dwordx4 v5,s[24:27],s47 offen offset:1024 nt lds
v_mfma_f32_16x16x32_bf16 v[184:187],a[164:167],v[84:87],v[184:187]
v_exp_f32 v156,v156
v_mfma_f32_16x16x32_bf16 v[184:187],a[168:171],v[88:91],v[184:187]
v_exp_f32 v157,v157
v_mfma_f32_16x16x32_bf16 v[184:187],a[172:175],v[92:95],v[184:187]
v_exp_f32 v158,v158
v_mfma_f32_16x16x32_bf16 v[188:191],a[188:191],v[92:95],v[36:39]
v_exp_f32 v159,v159
v_mfma_f32_16x16x32_bf16 v[188:191],a[184:187],v[88:91],v[188:191]
v_cvt_pk_bf16_f32 v140,v152,v153
v_cvt_pk_bf16_f32 v141,v154,v155
v_mfma_f32_16x16x32_bf16 v[188:191],a[180:183],v[84:87],v[188:191]
s_waitcnt vmcnt(14)
v_cvt_pk_bf16_f32 v142,v156,v157
v_cvt_pk_bf16_f32 v143,v158,v159
v_mfma_f32_16x16x32_bf16 v[188:191],a[176:179],v[80:83],v[188:191]
s_barrier
ds_read_b128 v[80:83],v18
v_add_u32 v25,s80,v25
v_mfma_f32_16x16x32_bf16 a[96:99],a[192:195],v[232:235],a[96:99]
s_add_u32 s80,s80,147456
v_exp_f32 v168,v168
v_mfma_f32_16x16x32_bf16 a[96:99],a[196:199],v[236:239],a[96:99]
v_exp_f32 v169,v169
v_mfma_f32_16x16x32_bf16 a[100:103],a[204:207],v[236:239],a[100:103]
ds_read_b128 v[84:87],v18 offset:256
v_cvt_pk_bf16_f32 v168,v168,v169
v_mfma_f32_16x16x32_bf16 a[100:103],a[200:203],v[232:235],a[100:103]
v_exp_f32 v170,v170
v_mfma_f32_16x16x32_bf16 a[104:107],a[208:211],v[232:235],a[104:107]
v_exp_f32 v171,v171
v_mfma_f32_16x16x32_bf16 a[104:107],a[212:215],v[236:239],a[104:107]
buffer_load_dwordx4 v4,s[24:27],s47 offen offset:2048 nt lds
v_mfma_f32_16x16x32_bf16 a[108:111],a[220:223],v[236:239],a[108:111]
ds_read_b128 v[88:91],v18 offset:512
v_cvt_pk_bf16_f32 v169,v170,v171
v_mfma_f32_16x16x32_bf16 a[108:111],a[216:219],v[232:235],a[108:111]
v_exp_f32 v172,v172
v_mfma_f32_16x16x32_bf16 a[112:115],a[224:227],v[232:235],a[112:115]
v_exp_f32 v173,v173
v_mfma_f32_16x16x32_bf16 a[112:115],a[228:231],v[236:239],a[112:115]
ds_read_b128 v[92:95],v18 offset:768
v_cvt_pk_bf16_f32 v170,v172,v173
v_mfma_f32_16x16x32_bf16 a[116:119],a[236:239],v[236:239],a[116:119]
v_exp_f32 v174,v174
v_mfma_f32_16x16x32_bf16 a[116:119],a[232:235],v[232:235],a[116:119]
ds_read_b64_tr_b16 a[192:193],v2
ds_read_b64_tr_b16 a[194:195],v2 offset:4096
v_mfma_f32_16x16x32_bf16 a[120:123],a[240:243],v[232:235],a[120:123]
v_exp_f32 v175,v175
v_mfma_f32_16x16x32_bf16 a[120:123],a[244:247],v[236:239],a[120:123]
ds_read_b64_tr_b16 a[196:197],v2 offset:8192
ds_read_b64_tr_b16 a[198:199],v2 offset:12288
)A"
R"A(v_mfma_f32_16x16x32_bf16 a[124:127],a[252:255],v[236:239],a[124:127]
ds_read_b64_tr_b16 a[204:205],v3 offset:8192
v_cvt_pk_bf16_f32 v171,v174,v175
v_mfma_f32_16x16x32_bf16 a[124:127],a[248:251],v[232:235],a[124:127]
v_exp_f32 v184,v184
v_smfmac_f32_16x16x64_bf16 v[60:63],v[28:31],v[232:239],v27
ds_read_b64_tr_b16 a[206:207],v3 offset:12288
v_mfma_f32_16x16x32_bf16 v[200:203],a[128:131],v[96:99],v[40:43]
ds_read_b64_tr_b16 a[200:201],v3
ds_read_b64_tr_b16 a[202:203],v3 offset:4096
v_mfma_f32_16x16x32_bf16 v[200:203],a[132:135],v[100:103],v[200:203]
v_exp_f32 v185,v185
v_mfma_f32_16x16x32_bf16 v[200:203],a[136:139],v[104:107],v[200:203]
buffer_load_dwordx4 v5,s[24:27],s47 offen offset:3072 nt lds
v_mfma_f32_16x16x32_bf16 v[200:203],a[140:143],v[108:111],v[200:203]
s_or_b32 s47,s47,0x18000
ds_read_b64_tr_b16 a[208:209],v2 offset:256
ds_read_b64_tr_b16 a[210:211],v2 offset:4352
v_mfma_f32_16x16x32_bf16 v[204:207],a[156:159],v[108:111],v[40:43]
s_addk_i32 s47,4096
s_and_b32 s47,s47,0x27000
ds_read_b64_tr_b16 a[212:213],v2 offset:8448
v_mfma_f32_16x16x32_bf16 v[204:207],a[152:155],v[104:107],v[204:207]
s_or_b32 s47,s47,s46
ds_read_b64_tr_b16 a[214:215],v2 offset:12544
v_cvt_pk_bf16_f32 v172,v184,v185
v_mfma_f32_16x16x32_bf16 v[204:207],a[148:151],v[100:103],v[204:207]
s_and_b32 s46,s54,0xfffc0000
v_exp_f32 v186,v186
v_mfma_f32_16x16x32_bf16 v[204:207],a[144:147],v[96:99],v[204:207]
s_addk_i32 s54,16384
ds_read_b64_tr_b16 a[220:221],v3 offset:8448
ds_read_b64_tr_b16 a[222:223],v3 offset:12544
v_mfma_f32_16x16x32_bf16 v[216:219],a[160:163],v[96:99],v[40:43]
v_exp_f32 v187,v187
v_mfma_f32_16x16x32_bf16 v[216:219],a[164:167],v[100:103],v[216:219]
ds_read_b64_tr_b16 a[216:217],v3 offset:256
ds_read_b64_tr_b16 a[218:219],v3 offset:4352
v_mfma_f32_16x16x32_bf16 v[216:219],a[168:171],v[104:107],v[216:219]
ds_read_b64_tr_b16 a[224:225],v2 offset:512
v_cvt_pk_bf16_f32 v173,v186,v187
v_mfma_f32_16x16x32_bf16 v[216:219],a[172:175],v[108:111],v[216:219]
ds_read_b64_tr_b16 a[226:227],v2 offset:4608
v_mfma_f32_16x16x32_bf16 v[220:223],a[188:191],v[108:111],v[40:43]
ds_read_b64_tr_b16 a[228:229],v2 offset:8704
v_mfma_f32_16x16x32_bf16 v[220:223],a[184:187],v[104:107],v[220:223]
ds_read_b64_tr_b16 a[230:231],v2 offset:12800
ds_read_b64_tr_b16 a[236:237],v3 offset:8704
v_mfma_f32_16x16x32_bf16 v[220:223],a[180:183],v[100:103],v[220:223]
ds_read_b64_tr_b16 a[238:239],v3 offset:12800
v_mfma_f32_16x16x32_bf16 v[220:223],a[176:179],v[96:99],v[220:223]
s_waitcnt lgkmcnt(8)
ds_read_b64_tr_b16 a[232:233],v3 offset:512
v_mfma_f32_16x16x32_bf16 v[32:35],v[80:83],v[128:131],0
ds_read_b64_tr_b16 a[234:235],v3 offset:4608
v_mfma_f32_16x16x32_bf16 v[32:35],v[84:87],v[160:163],v[32:35]
ds_read_b64_tr_b16 a[240:241],v2 offset:768
ds_read_b64_tr_b16 a[242:243],v2 offset:4864
v_mfma_f32_16x16x32_bf16 v[32:35],v[88:91],v[192:195],v[32:35]
ds_read_b64_tr_b16 a[244:245],v2 offset:8960
v_mfma_f32_16x16x32_bf16 v[32:35],v[92:95],v[224:227],v[32:35]
ds_read_b64_tr_b16 a[246:247],v2 offset:13056
v_add_u32 v2,s48,v2
v_mfma_f32_16x16x32_bf16 v[36:39],v[80:83],v[132:135],0
ds_read_b64_tr_b16 a[252:253],v3 offset:8960
v_mfma_f32_16x16x32_bf16 v[36:39],v[84:87],v[164:167],v[36:39]
ds_read_b64_tr_b16 a[254:255],v3 offset:13056
ds_read_b64_tr_b16 a[248:249],v3 offset:768
v_mfma_f32_16x16x32_bf16 v[36:39],v[88:91],v[196:199],v[36:39]
ds_read_b64_tr_b16 a[250:251],v3 offset:4864
v_add_u32 v3,s48,v3
v_mfma_f32_16x16x32_bf16 v[36:39],v[92:95],v[228:231],v[36:39]
s_mov_b32 s48,-98304
v_exp_f32 v188,v188
v_mfma_f32_16x16x32_bf16 v[40:43],v[80:83],v[144:147],0
v_exp_f32 v189,v189
v_mfma_f32_16x16x32_bf16 v[40:43],v[84:87],v[176:179],v[40:43]
v_exp_f32 v190,v190
v_mfma_f32_16x16x32_bf16 v[40:43],v[88:91],v[208:211],v[40:43]
v_exp_f32 v191,v191
v_mfma_f32_16x16x32_bf16 v[40:43],v[92:95],v[240:243],v[40:43]
v_cvt_pk_bf16_f32 v174,v188,v189
v_cvt_pk_bf16_f32 v175,v190,v191
v_mfma_f32_16x16x32_bf16 a[0:3],a[192:195],v[136:139],a[0:3]
v_exp_f32 v200,v200
v_mfma_f32_16x16x32_bf16 a[0:3],a[196:199],v[140:143],a[0:3]
v_max3_f32 v32,v32,v33,v34
v_max3_f32 v32,v32,v35,s18
v_mfma_f32_16x16x32_bf16 a[4:7],a[204:207],v[140:143],a[4:7]
v_sub_f32 v35,0,v32
v_sub_f32 v34,0,v32
v_mfma_f32_16x16x32_bf16 a[4:7],a[200:203],v[136:139],a[4:7]
s_memrealtime s[76:77]
v_sub_f32 v33,0,v32
v_sub_f32 v32,0,v32
v_mfma_f32_16x16x32_bf16 a[8:11],a[208:211],v[136:139],a[8:11]
v_exp_f32 v201,v201
v_mfma_f32_16x16x32_bf16 a[8:11],a[212:215],v[140:143],a[8:11]
v_exp_f32 v202,v202
v_mfma_f32_16x16x32_bf16 a[12:15],a[220:223],v[140:143],a[12:15]
s_waitcnt lgkmcnt(0)
v_exp_f32 v203,v203
v_mfma_f32_16x16x32_bf16 a[12:15],a[216:219],v[136:139],a[12:15]
v_cvt_pk_bf16_f32 v200,v200,v201
v_cvt_pk_bf16_f32 v201,v202,v203
v_mfma_f32_16x16x32_bf16 a[16:19],a[224:227],v[136:139],a[16:19]
v_exp_f32 v204,v204
v_mfma_f32_16x16x32_bf16 a[16:19],a[228:231],v[140:143],a[16:19]
v_exp_f32 v205,v205
v_mfma_f32_16x16x32_bf16 a[20:23],a[236:239],v[140:143],a[20:23]
s_waitcnt lgkmcnt(0)
v_exp_f32 v206,v206
v_mfma_f32_16x16x32_bf16 a[20:23],a[232:235],v[136:139],a[20:23]
s_sub_u32 s79,s78,s76
v_exp_f32 v207,v207
v_mfma_f32_16x16x32_bf16 a[24:27],a[240:243],v[136:139],a[24:27]
s_add_u32 s78,s76,s60
v_cvt_pk_bf16_f32 v202,v204,v205
v_cvt_pk_bf16_f32 v203,v206,v207
v_mfma_f32_16x16x32_bf16 a[24:27],a[244:247],v[140:143],a[24:27]
s_cmp_gt_i32 s79,s95
s_cbranch_scc1 L_ps4_%=
L_pr4_%=:
v_max3_f32 v36,v36,v37,v38
v_mfma_f32_16x16x32_bf16 a[28:31],a[252:255],v[140:143],a[28:31]
s_lshr_b32 s60,s94,2
v_exp_f32 v216,v216
v_mfma_f32_16x16x32_bf16 a[28:31],a[248:251],v[136:139],a[28:31]
s_max_i32 s60,s60,124
v_exp_f32 v217,v217
v_smfmac_f32_16x16x64_bf16 v[48:51],v[28:31],v[136:143],v27
s_sub_u32 s94,s94,3
v_cvt_pk_bf16_f32 v204,v216,v217
v_mfma_f32_16x16x32_bf16 v[232:235],a[128:131],v[112:115],v[44:47]
s_waitcnt vmcnt(9)
v_exp_f32 v218,v218
v_mfma_f32_16x16x32_bf16 v[232:235],a[132:135],v[116:119],v[232:235]
s_barrier
s_add_u32 m0,32768,s51
ds_read_b128 a[128:131],v1
v_mfma_f32_16x16x32_bf16 v[232:235],a[136:139],v[120:123],v[232:235]
v_exp_f32 v219,v219
v_mfma_f32_16x16x32_bf16 v[232:235],a[140:143],v[124:127],v[232:235]
ds_read_b128 a[132:135],v1 offset:256
v_cvt_pk_bf16_f32 v205,v218,v219
v_mfma_f32_16x16x32_bf16 v[236:239],a[156:159],v[124:127],v[44:47]
buffer_load_dwordx4 v4,s[20:23],s47 offen nt lds
v_mfma_f32_16x16x32_bf16 v[236:239],a[152:155],v[120:123],v[236:239]
ds_read_b128 a[136:139],v1 offset:512
v_max3_f32 v36,v36,v39,s19
v_mfma_f32_16x16x32_bf16 v[236:239],a[148:151],v[116:119],v[236:239]
v_exp_f32 v220,v220
v_mfma_f32_16x16x32_bf16 v[236:239],a[144:147],v[112:115],v[236:239]
ds_read_b128 a[140:143],v1 offset:768
v_sub_f32 v39,0,v36
v_mfma_f32_16x16x32_bf16 v[248:251],a[160:163],v[112:115],v[44:47]
v_exp_f32 v221,v221
v_mfma_f32_16x16x32_bf16 v[248:251],a[164:167],v[116:119],v[248:251]
ds_read_b128 a[156:159],v1 offset:4864
v_cvt_pk_bf16_f32 v206,v220,v221
v_mfma_f32_16x16x32_bf16 v[248:251],a[168:171],v[120:123],v[248:251]
v_exp_f32 v222,v222
v_mfma_f32_16x16x32_bf16 v[248:251],a[172:175],v[124:127],v[248:251]
ds_read_b128 a[152:155],v1 offset:4608
v_sub_f32 v38,0,v36
v_mfma_f32_16x16x32_bf16 v[252:255],a[188:191],v[124:127],v[44:47]
v_exp_f32 v223,v223
v_mfma_f32_16x16x32_bf16 v[252:255],a[184:187],v[120:123],v[252:255]
ds_read_b128 a[148:151],v1 offset:4352
v_cvt_pk_bf16_f32 v207,v222,v223
v_mfma_f32_16x16x32_bf16 v[252:255],a[180:183],v[116:119],v[252:255]
v_sub_f32 v37,0,v36
v_sub_f32 v36,0,v36
v_mfma_f32_16x16x32_bf16 v[252:255],a[176:179],v[112:115],v[252:255]
ds_read_b128 a[144:147],v1 offset:4096
v_max3_f32 v40,v40,v41,v42
v_mfma_f32_16x16x32_bf16 v[44:47],v[80:83],v[148:151],0
v_exp_f32 v232,v232
v_mfma_f32_16x16x32_bf16 v[44:47],v[84:87],v[180:183],v[44:47]
ds_read_b128 a[160:163],v1 offset:8192
v_max3_f32 v40,v40,v43,s100
v_mfma_f32_16x16x32_bf16 v[44:47],v[88:91],v[212:215],v[44:47]
buffer_load_dwordx4 v4,s[20:23],s47 offen offset:1024 nt lds
v_mfma_f32_16x16x32_bf16 v[44:47],v[92:95],v[244:247],v[44:47]
ds_read_b128 a[164:167],v1 offset:8448
v_sub_f32 v43,0,v40
v_mfma_f32_16x16x32_bf16 a[32:35],a[192:195],v[168:171],a[32:35]
v_exp_f32 v233,v233
v_mfma_f32_16x16x32_bf16 a[32:35],a[196:199],v[172:175],a[32:35]
ds_read_b128 a[168:171],v1 offset:8704
v_cvt_pk_bf16_f32 v232,v232,v233
v_mfma_f32_16x16x32_bf16 a[36:39],a[204:207],v[172:175],a[36:39]
v_exp_f32 v234,v234
v_mfma_f32_16x16x32_bf16 a[36:39],a[200:203],v[168:171],a[36:39]
ds_read_b128 a[172:175],v1 offset:8960
v_sub_f32 v42,0,v40
v_mfma_f32_16x16x32_bf16 a[40:43],a[208:211],v[168:171],a[40:43]
v_exp_f32 v235,v235
v_mfma_f32_16x16x32_bf16 a[40:43],a[212:215],v[172:175],a[40:43]
ds_read_b128 a[188:191],v1 offset:13056
v_cvt_pk_bf16_f32 v233,v234,v235
v_mfma_f32_16x16x32_bf16 a[44:47],a[220:223],v[172:175],a[44:47]
v_exp_f32 v236,v236
v_mfma_f32_16x16x32_bf16 a[44:47],a[216:219],v[168:171],a[44:47]
ds_read_b128 a[184:187],v1 offset:12800
v_sub_f32 v41,0,v40
v_mfma_f32_16x16x32_bf16 a[48:51],a[224:227],v[168:171],a[48:51]
v_exp_f32 v237,v237
v_mfma_f32_16x16x32_bf16 a[48:51],a[228:231],v[172:175],a[48:51]
ds_read_b128 a[180:183],v1 offset:12544
v_cvt_pk_bf16_f32 v234,v236,v237
v_mfma_f32_16x16x32_bf16 a[52:55],a[236:239],v[172:175],a[52:55]
v_exp_f32 v238,v238
v_mfma_f32_16x16x32_bf16 a[52:55],a[232:235],v[168:171],a[52:55]
ds_read_b128 a[176:179],v1 offset:12288
v_sub_f32 v40,0,v40
v_mfma_f32_16x16x32_bf16 a[56:59],a[240:243],v[168:171],a[56:59]
buffer_load_dwordx4 v5,s[20:23],s47 offen offset:2048 nt lds
v_mfma_f32_16x16x32_bf16 a[56:59],a[244:247],v[172:175],a[56:59]
v_exp_f32 v239,v239
v_mfma_f32_16x16x32_bf16 a[60:63],a[252:255],v[172:175],a[60:63]
v_cvt_pk_bf16_f32 v235,v238,v239
v_add_f32 v48,v49,v48
v_mfma_f32_16x16x32_bf16 a[60:63],a[248:251],v[168:171],a[60:63]
v_exp_f32 v248,v248
v_smfmac_f32_16x16x64_bf16 v[52:55],v[28:31],v[168:175],v27
s_waitcnt lgkmcnt(2)
v_max_f32 v20,0x00800000,v48
v_mfma_f32_16x16x32_bf16 v[136:139],a[128:131],v[128:131],v[32:35]
v_exp_f32 v249,v249
v_mfma_f32_16x16x32_bf16 v[136:139],a[132:135],v[160:163],v[136:139]
v_exp_f32 v250,v250
v_mfma_f32_16x16x32_bf16 v[136:139],a[136:139],v[192:195],v[136:139]
v_exp_f32 v251,v251
v_mfma_f32_16x16x32_bf16 v[136:139],a[140:143],v[224:227],v[136:139]
v_exp_f32 v252,v252
v_mfma_f32_16x16x32_bf16 v[140:143],a[156:159],v[224:227],v[32:35]
v_exp_f32 v253,v253
v_mfma_f32_16x16x32_bf16 v[140:143],a[152:155],v[192:195],v[140:143]
v_exp_f32 v254,v254
v_mfma_f32_16x16x32_bf16 v[140:143],a[148:151],v[160:163],v[140:143]
v_exp_f32 v255,v255
v_mfma_f32_16x16x32_bf16 v[140:143],a[144:147],v[128:131],v[140:143]
v_cvt_pk_bf16_f32 v236,v248,v249
v_cvt_pk_bf16_f32 v237,v250,v251
v_mfma_f32_16x16x32_bf16 v[152:155],a[160:163],v[128:131],v[32:35]
v_cvt_pk_bf16_f32 v238,v252,v253
v_cvt_pk_bf16_f32 v239,v254,v255
v_mfma_f32_16x16x32_bf16 v[152:155],a[164:167],v[160:163],v[152:155]
buffer_load_dwordx4 v5,s[20:23],s47 offen offset:3072 nt lds
v_mfma_f32_16x16x32_bf16 v[152:155],a[168:171],v[192:195],v[152:155]
v_exp_f32 v136,v136
v_mfma_f32_16x16x32_bf16 v[152:155],a[172:175],v[224:227],v[152:155]
v_exp_f32 v137,v137
v_mfma_f32_16x16x32_bf16 v[156:159],a[188:191],v[224:227],v[32:35]
v_exp_f32 v138,v138
v_mfma_f32_16x16x32_bf16 v[156:159],a[184:187],v[192:195],v[156:159]
s_waitcnt lgkmcnt(0)
v_exp_f32 v139,v139
v_mfma_f32_16x16x32_bf16 v[156:159],a[180:183],v[160:163],v[156:159]
v_cvt_pk_bf16_f32 v136,v136,v137
v_cvt_pk_bf16_f32 v137,v138,v139
v_mfma_f32_16x16x32_bf16 v[156:159],a[176:179],v[128:131],v[156:159]
v_exp_f32 v140,v140
v_mfma_f32_16x16x32_bf16 a[64:67],a[192:195],v[200:203],a[64:67]
v_exp_f32 v141,v141
v_mfma_f32_16x16x32_bf16 a[64:67],a[196:199],v[204:207],a[64:67]
v_exp_f32 v142,v142
v_mfma_f32_16x16x32_bf16 a[68:71],a[204:207],v[204:207],a[68:71]
v_exp_f32 v143,v143
v_mfma_f32_16x16x32_bf16 a[68:71],a[200:203],v[200:203],a[68:71]
v_cvt_pk_bf16_f32 v138,v140,v141
v_cvt_pk_bf16_f32 v139,v142,v143
v_mfma_f32_16x16x32_bf16 a[72:75],a[208:211],v[200:203],a[72:75]
s_addk_i32 m0,16384
v_exp_f32 v152,v152
v_mfma_f32_16x16x32_bf16 a[72:75],a[212:215],v[204:207],a[72:75]
)A"
R"A(v_exp_f32 v153,v153
v_mfma_f32_16x16x32_bf16 a[76:79],a[220:223],v[204:207],a[76:79]
v_exp_f32 v154,v154
v_mfma_f32_16x16x32_bf16 a[76:79],a[216:219],v[200:203],a[76:79]
buffer_load_dwordx4 v4,s[24:27],s47 offen nt lds
v_mfma_f32_16x16x32_bf16 a[80:83],a[224:227],v[200:203],a[80:83]
v_exp_f32 v155,v155
v_mfma_f32_16x16x32_bf16 a[80:83],a[228:231],v[204:207],a[80:83]
v_exp_f32 v156,v156
v_mfma_f32_16x16x32_bf16 a[84:87],a[236:239],v[204:207],a[84:87]
v_exp_f32 v157,v157
v_mfma_f32_16x16x32_bf16 a[84:87],a[232:235],v[200:203],a[84:87]
v_exp_f32 v158,v158
v_mfma_f32_16x16x32_bf16 a[88:91],a[240:243],v[200:203],a[88:91]
v_exp_f32 v159,v159
v_mfma_f32_16x16x32_bf16 a[88:91],a[244:247],v[204:207],a[88:91]
v_cvt_pk_bf16_f32 v140,v152,v153
v_cvt_pk_bf16_f32 v141,v154,v155
v_mfma_f32_16x16x32_bf16 a[92:95],a[252:255],v[204:207],a[92:95]
v_cvt_pk_bf16_f32 v142,v156,v157
v_cvt_pk_bf16_f32 v143,v158,v159
v_mfma_f32_16x16x32_bf16 a[92:95],a[248:251],v[200:203],a[92:95]
v_rcp_f32 v20,v20
v_smfmac_f32_16x16x64_bf16 v[56:59],v[28:31],v[200:207],v27
v_accvgpr_read_b32 v64,a0
v_mfma_f32_16x16x32_bf16 v[168:171],a[128:131],v[132:135],v[36:39]
v_accvgpr_read_b32 v65,a1
v_accvgpr_read_b32 v66,a2
v_mfma_f32_16x16x32_bf16 v[168:171],a[132:135],v[164:167],v[168:171]
v_accvgpr_read_b32 v67,a3
v_mul_f32 v64,v20,v64
v_mfma_f32_16x16x32_bf16 v[168:171],a[136:139],v[196:199],v[168:171]
v_mul_f32 v65,v20,v65
v_cvt_pk_bf16_f32 v64,v64,v65
v_mfma_f32_16x16x32_bf16 v[168:171],a[140:143],v[228:231],v[168:171]
v_mul_f32 v66,v20,v66
v_mul_f32 v67,v20,v67
v_mfma_f32_16x16x32_bf16 v[172:175],a[156:159],v[228:231],v[36:39]
buffer_load_dwordx4 v5,s[24:27],s47 offen offset:1024 nt lds
v_mfma_f32_16x16x32_bf16 v[172:175],a[152:155],v[196:199],v[172:175]
v_cvt_pk_bf16_f32 v65,v66,v67
v_accvgpr_read_b32 v16,a6
v_mfma_f32_16x16x32_bf16 v[172:175],a[148:151],v[164:167],v[172:175]
v_accvgpr_read_b32 v17,a7
v_accvgpr_read_b32 v66,a4
v_mfma_f32_16x16x32_bf16 v[172:175],a[144:147],v[132:135],v[172:175]
v_accvgpr_read_b32 v67,a5
v_cmp_nlt_f32 s[56:57],v48,s63
v_mfma_f32_16x16x32_bf16 v[184:187],a[160:163],v[132:135],v[36:39]
v_accvgpr_read_b32 v68,a8
v_accvgpr_read_b32 v69,a9
v_accvgpr_read_b32 v70,a10
v_accvgpr_read_b32 v71,a11
v_accvgpr_read_b32 v12,a12
v_accvgpr_read_b32 v13,a13
v_accvgpr_read_b32 v14,a14
v_accvgpr_read_b32 v15,a15
v_pk_mul_f32 v[68:69],v[20:21],v[68:69] op_sel:[0,0] op_sel_hi:[0,1]
v_pk_mul_f32 v[70:71],v[20:21],v[70:71] op_sel:[0,0] op_sel_hi:[0,1]
v_pk_mul_f32 v[12:13],v[20:21],v[12:13] op_sel:[0,0] op_sel_hi:[0,1]
v_pk_mul_f32 v[14:15],v[20:21],v[14:15] op_sel:[0,0] op_sel_hi:[0,1]
v_cvt_pk_bf16_f32 v68,v68,v69
v_cvt_pk_bf16_f32 v69,v70,v71
v_cvt_pk_bf16_f32 v70,v12,v13
v_cvt_pk_bf16_f32 v71,v14,v15
v_mfma_f32_16x16x32_bf16 v[184:187],a[164:167],v[164:167],v[184:187]
v_accvgpr_read_b32 v72,a16
v_accvgpr_read_b32 v73,a17
v_accvgpr_read_b32 v74,a18
v_accvgpr_read_b32 v75,a19
v_accvgpr_read_b32 v12,a20
v_accvgpr_read_b32 v13,a21
v_accvgpr_read_b32 v14,a22
v_accvgpr_read_b32 v15,a23
v_pk_mul_f32 v[72:73],v[20:21],v[72:73] op_sel:[0,0] op_sel_hi:[0,1]
v_pk_mul_f32 v[74:75],v[20:21],v[74:75] op_sel:[0,0] op_sel_hi:[0,1]
v_pk_mul_f32 v[12:13],v[20:21],v[12:13] op_sel:[0,0] op_sel_hi:[0,1]
v_pk_mul_f32 v[14:15],v[20:21],v[14:15] op_sel:[0,0] op_sel_hi:[0,1]
v_cvt_pk_bf16_f32 v72,v72,v73
v_cvt_pk_bf16_f32 v73,v74,v75
v_cvt_pk_bf16_f32 v74,v12,v13
v_cvt_pk_bf16_f32 v75,v14,v15
v_mfma_f32_16x16x32_bf16 v[184:187],a[168:171],v[196:199],v[184:187]
v_accvgpr_read_b32 v76,a24
v_accvgpr_read_b32 v77,a25
v_accvgpr_read_b32 v78,a26
v_accvgpr_read_b32 v79,a27
v_accvgpr_read_b32 v12,a28
v_accvgpr_read_b32 v13,a29
v_accvgpr_read_b32 v14,a30
v_accvgpr_read_b32 v15,a31
v_pk_mul_f32 v[76:77],v[20:21],v[76:77] op_sel:[0,0] op_sel_hi:[0,1]
v_pk_mul_f32 v[78:79],v[20:21],v[78:79] op_sel:[0,0] op_sel_hi:[0,1]
v_pk_mul_f32 v[12:13],v[20:21],v[12:13] op_sel:[0,0] op_sel_hi:[0,1]
v_pk_mul_f32 v[14:15],v[20:21],v[14:15] op_sel:[0,0] op_sel_hi:[0,1]
v_cvt_pk_bf16_f32 v76,v76,v77
v_cvt_pk_bf16_f32 v77,v78,v79
v_cvt_pk_bf16_f32 v78,v12,v13
v_cvt_pk_bf16_f32 v79,v14,v15
v_mfma_f32_16x16x32_bf16 v[184:187],a[172:175],v[228:231],v[184:187]
v_max3_f32 v44,v44,v45,v46
v_max3_f32 v44,v44,v47,s101
v_mfma_f32_16x16x32_bf16 v[188:191],a[188:191],v[228:231],v[36:39]
v_mov_b32 v48,0
v_mov_b32 v49,0
v_mfma_f32_16x16x32_bf16 v[188:191],a[184:187],v[196:199],v[188:191]
v_mov_b32 v50,0
v_mov_b32 v51,0
v_mfma_f32_16x16x32_bf16 v[188:191],a[180:183],v[164:167],v[188:191]
v_sub_f32 v47,0,v44
v_sub_f32 v46,0,v44
v_mfma_f32_16x16x32_bf16 v[188:191],a[176:179],v[132:135],v[188:191]
v_sub_f32 v45,0,v44
v_sub_f32 v44,0,v44
v_mfma_f32_16x16x32_bf16 a[96:99],a[192:195],v[232:235],a[96:99]
ds_read_b64_tr_b16 a[192:193],v2
ds_read_b64_tr_b16 a[194:195],v2 offset:4096
v_mfma_f32_16x16x32_bf16 a[96:99],a[196:199],v[236:239],a[96:99]
ds_read_b64_tr_b16 a[196:197],v2 offset:8192
v_add_f32 v52,v53,v52
v_mfma_f32_16x16x32_bf16 a[100:103],a[204:207],v[236:239],a[100:103]
buffer_load_dwordx4 v4,s[24:27],s47 offen offset:2048 nt lds
v_mfma_f32_16x16x32_bf16 a[100:103],a[200:203],v[232:235],a[100:103]
ds_read_b64_tr_b16 a[198:199],v2 offset:12288
ds_read_b64_tr_b16 a[204:205],v3 offset:8192
v_mfma_f32_16x16x32_bf16 a[104:107],a[208:211],v[232:235],a[104:107]
ds_read_b64_tr_b16 a[206:207],v3 offset:12288
v_max_f32 v21,0x00800000,v52
v_mfma_f32_16x16x32_bf16 a[104:107],a[212:215],v[236:239],a[104:107]
ds_read_b64_tr_b16 a[200:201],v3
v_accvgpr_read_b32 v80,a32
v_mfma_f32_16x16x32_bf16 a[108:111],a[220:223],v[236:239],a[108:111]
ds_read_b64_tr_b16 a[202:203],v3 offset:4096
v_accvgpr_read_b32 v81,a33
v_mfma_f32_16x16x32_bf16 a[108:111],a[216:219],v[232:235],a[108:111]
ds_read_b64_tr_b16 a[208:209],v2 offset:256
ds_read_b64_tr_b16 a[210:211],v2 offset:4352
v_mfma_f32_16x16x32_bf16 a[112:115],a[224:227],v[232:235],a[112:115]
ds_read_b64_tr_b16 a[212:213],v2 offset:8448
v_accvgpr_read_b32 v82,a34
v_mfma_f32_16x16x32_bf16 a[112:115],a[228:231],v[236:239],a[112:115]
ds_read_b64_tr_b16 a[214:215],v2 offset:12544
v_accvgpr_read_b32 v83,a35
v_mfma_f32_16x16x32_bf16 a[116:119],a[236:239],v[236:239],a[116:119]
ds_read_b64_tr_b16 a[220:221],v3 offset:8448
v_accvgpr_read_b32 v12,a38
v_mfma_f32_16x16x32_bf16 a[116:119],a[232:235],v[232:235],a[116:119]
ds_read_b64_tr_b16 a[222:223],v3 offset:12544
ds_read_b64_tr_b16 a[216:217],v3 offset:256
v_mfma_f32_16x16x32_bf16 a[120:123],a[240:243],v[232:235],a[120:123]
ds_read_b64_tr_b16 a[218:219],v3 offset:4352
v_accvgpr_read_b32 v13,a39
v_mfma_f32_16x16x32_bf16 a[120:123],a[244:247],v[236:239],a[120:123]
ds_read_b64_tr_b16 a[224:225],v2 offset:512
v_add_u32 v1,s48,v1
v_mfma_f32_16x16x32_bf16 a[124:127],a[252:255],v[236:239],a[124:127]
ds_read_b64_tr_b16 a[226:227],v2 offset:4608
v_mov_b32 v53,0
v_mfma_f32_16x16x32_bf16 a[124:127],a[248:251],v[232:235],a[124:127]
ds_read_b64_tr_b16 a[228:229],v2 offset:8704
ds_read_b64_tr_b16 a[230:231],v2 offset:12800
v_smfmac_f32_16x16x64_bf16 v[60:63],v[28:31],v[232:239],v27
ds_read_b64_tr_b16 a[236:237],v3 offset:8704
v_mfma_f32_16x16x32_bf16 v[200:203],a[128:131],v[144:147],v[40:43]
buffer_load_dwordx4 v5,s[24:27],s47 offen offset:3072 nt lds
v_mfma_f32_16x16x32_bf16 v[200:203],a[132:135],v[176:179],v[200:203]
s_or_b32 s47,s47,0x18000
ds_read_b64_tr_b16 a[238:239],v3 offset:12800
ds_read_b64_tr_b16 a[232:233],v3 offset:512
v_mfma_f32_16x16x32_bf16 v[200:203],a[136:139],v[208:211],v[200:203]
s_addk_i32 s47,4096
s_and_b32 s47,s47,0x27000
ds_read_b64_tr_b16 a[234:235],v3 offset:4608
v_mfma_f32_16x16x32_bf16 v[200:203],a[140:143],v[240:243],v[200:203]
s_or_b32 s47,s47,s46
ds_read_b64_tr_b16 a[240:241],v2 offset:768
v_mov_b32 v54,0
v_mfma_f32_16x16x32_bf16 v[204:207],a[156:159],v[240:243],v[40:43]
s_and_b32 s46,s54,0xfffc0000
ds_read_b64_tr_b16 a[242:243],v2 offset:4864
v_mov_b32 v55,0
v_mfma_f32_16x16x32_bf16 v[204:207],a[152:155],v[208:211],v[204:207]
ds_read_b64_tr_b16 a[244:245],v2 offset:8960
ds_read_b64_tr_b16 a[246:247],v2 offset:13056
v_mfma_f32_16x16x32_bf16 v[204:207],a[148:151],v[176:179],v[204:207]
ds_read_b64_tr_b16 a[252:253],v3 offset:8960
v_add_f32 v56,v57,v56
v_mfma_f32_16x16x32_bf16 v[204:207],a[144:147],v[144:147],v[204:207]
ds_read_b64_tr_b16 a[254:255],v3 offset:13056
v_max_f32 v22,0x00800000,v56
v_mfma_f32_16x16x32_bf16 v[216:219],a[160:163],v[144:147],v[40:43]
ds_read_b64_tr_b16 a[248:249],v3 offset:768
v_accvgpr_read_b32 v96,a64
v_mfma_f32_16x16x32_bf16 v[216:219],a[164:167],v[176:179],v[216:219]
ds_read_b64_tr_b16 a[250:251],v3 offset:4864
v_accvgpr_read_b32 v97,a65
v_mfma_f32_16x16x32_bf16 v[216:219],a[168:171],v[208:211],v[216:219]
v_exp_f32 v168,v168
v_mfma_f32_16x16x32_bf16 v[216:219],a[172:175],v[240:243],v[216:219]
v_exp_f32 v169,v169
v_mfma_f32_16x16x32_bf16 v[220:223],a[188:191],v[240:243],v[40:43]
v_exp_f32 v170,v170
v_mfma_f32_16x16x32_bf16 v[220:223],a[184:187],v[208:211],v[220:223]
v_exp_f32 v171,v171
v_mfma_f32_16x16x32_bf16 v[220:223],a[180:183],v[176:179],v[220:223]
v_cvt_pk_bf16_f32 v168,v168,v169
v_cvt_pk_bf16_f32 v169,v170,v171
v_mfma_f32_16x16x32_bf16 v[220:223],a[176:179],v[144:147],v[220:223]
s_waitcnt lgkmcnt(2)
v_exp_f32 v172,v172
v_mfma_f32_16x16x32_bf16 a[0:3],a[192:195],v[136:139],0
v_exp_f32 v173,v173
v_mfma_f32_16x16x32_bf16 a[0:3],a[196:199],v[140:143],a[0:3]
v_exp_f32 v174,v174
v_mfma_f32_16x16x32_bf16 a[4:7],a[204:207],v[140:143],0
v_exp_f32 v175,v175
v_mfma_f32_16x16x32_bf16 a[4:7],a[200:203],v[136:139],a[4:7]
s_memrealtime s[76:77]
v_cvt_pk_bf16_f32 v170,v172,v173
v_cvt_pk_bf16_f32 v171,v174,v175
v_mfma_f32_16x16x32_bf16 a[8:11],a[208:211],v[136:139],0
v_exp_f32 v184,v184
v_mfma_f32_16x16x32_bf16 a[8:11],a[212:215],v[140:143],a[8:11]
v_exp_f32 v185,v185
v_mfma_f32_16x16x32_bf16 a[12:15],a[220:223],v[140:143],0
v_exp_f32 v186,v186
v_mfma_f32_16x16x32_bf16 a[12:15],a[216:219],v[136:139],a[12:15]
v_exp_f32 v187,v187
v_mfma_f32_16x16x32_bf16 a[16:19],a[224:227],v[136:139],0
v_exp_f32 v188,v188
v_mfma_f32_16x16x32_bf16 a[16:19],a[228:231],v[140:143],a[16:19]
v_exp_f32 v189,v189
v_mfma_f32_16x16x32_bf16 a[20:23],a[236:239],v[140:143],0
s_waitcnt lgkmcnt(0)
v_exp_f32 v190,v190
v_mfma_f32_16x16x32_bf16 a[20:23],a[232:235],v[136:139],a[20:23]
s_sub_u32 s79,s78,s76
v_exp_f32 v191,v191
v_mfma_f32_16x16x32_bf16 a[24:27],a[240:243],v[136:139],0
s_add_u32 s78,s76,s60
v_rcp_f32 v21,v21
v_mfma_f32_16x16x32_bf16 a[24:27],a[244:247],v[140:143],a[24:27]
s_cmp_gt_i32 s79,s95
s_cbranch_scc1 L_ps5_%=
L_pr5_%=:
v_cvt_pk_bf16_f32 v172,v184,v185
v_mfma_f32_16x16x32_bf16 a[28:31],a[252:255],v[140:143],0
s_lshr_b32 s60,s94,2
v_cvt_pk_bf16_f32 v173,v186,v187
v_cvt_pk_bf16_f32 v174,v188,v189
v_mfma_f32_16x16x32_bf16 a[28:31],a[248:251],v[136:139],a[28:31]
s_max_i32 s60,s60,124
v_cvt_pk_bf16_f32 v175,v190,v191
v_mul_f32 v80,v21,v80
v_smfmac_f32_16x16x64_bf16 v[48:51],v[28:31],v[136:143],v27
v_mul_f32 v81,v21,v81
v_mfma_f32_16x16x32_bf16 v[232:235],a[128:131],v[148:151],v[44:47]
s_waitcnt vmcnt(8)
v_cvt_pk_bf16_f32 v80,v80,v81
v_mul_f32 v82,v21,v82
v_mfma_f32_16x16x32_bf16 v[232:235],a[132:135],v[180:183],v[232:235]
s_barrier
s_add_u32 m0,65536,s51
v_mul_f32 v83,v21,v83
v_mfma_f32_16x16x32_bf16 v[232:235],a[136:139],v[212:215],v[232:235]
v_cvt_pk_bf16_f32 v81,v82,v83
v_accvgpr_read_b32 v82,a36
v_mfma_f32_16x16x32_bf16 v[232:235],a[140:143],v[244:247],v[232:235]
v_accvgpr_read_b32 v83,a37
v_mul_f32 v82,v21,v82
v_mfma_f32_16x16x32_bf16 v[236:239],a[156:159],v[244:247],v[44:47]
buffer_load_dwordx4 v4,s[20:23],s47 offen nt lds
v_mfma_f32_16x16x32_bf16 v[236:239],a[152:155],v[212:215],v[236:239]
v_mul_f32 v83,v21,v83
v_mul_f32 v12,v21,v12
v_mfma_f32_16x16x32_bf16 v[236:239],a[148:151],v[180:183],v[236:239]
v_mul_f32 v13,v21,v13
v_cvt_pk_bf16_f32 v82,v82,v83
v_mfma_f32_16x16x32_bf16 v[236:239],a[144:147],v[148:151],v[236:239]
ds_read_b128 a[128:131],v1
v_cvt_pk_bf16_f32 v83,v12,v13
v_mfma_f32_16x16x32_bf16 v[248:251],a[160:163],v[148:151],v[44:47]
v_accvgpr_read_b32 v84,a40
v_accvgpr_read_b32 v85,a41
v_accvgpr_read_b32 v86,a42
v_accvgpr_read_b32 v87,a43
v_accvgpr_read_b32 v12,a44
v_accvgpr_read_b32 v13,a45
)A"
R"A(v_accvgpr_read_b32 v14,a46
v_accvgpr_read_b32 v15,a47
v_pk_mul_f32 v[84:85],v[20:21],v[84:85] op_sel:[1,0] op_sel_hi:[1,1]
v_pk_mul_f32 v[86:87],v[20:21],v[86:87] op_sel:[1,0] op_sel_hi:[1,1]
v_pk_mul_f32 v[12:13],v[20:21],v[12:13] op_sel:[1,0] op_sel_hi:[1,1]
v_pk_mul_f32 v[14:15],v[20:21],v[14:15] op_sel:[1,0] op_sel_hi:[1,1]
v_cvt_pk_bf16_f32 v84,v84,v85
v_cvt_pk_bf16_f32 v85,v86,v87
v_cvt_pk_bf16_f32 v86,v12,v13
v_cvt_pk_bf16_f32 v87,v14,v15
v_mfma_f32_16x16x32_bf16 v[248:251],a[164:167],v[180:183],v[248:251]
v_accvgpr_read_b32 v88,a48
v_accvgpr_read_b32 v89,a49
v_accvgpr_read_b32 v90,a50
v_accvgpr_read_b32 v91,a51
v_accvgpr_read_b32 v12,a52
v_accvgpr_read_b32 v13,a53
v_accvgpr_read_b32 v14,a54
v_accvgpr_read_b32 v15,a55
v_pk_mul_f32 v[88:89],v[20:21],v[88:89] op_sel:[1,0] op_sel_hi:[1,1]
v_pk_mul_f32 v[90:91],v[20:21],v[90:91] op_sel:[1,0] op_sel_hi:[1,1]
v_pk_mul_f32 v[12:13],v[20:21],v[12:13] op_sel:[1,0] op_sel_hi:[1,1]
v_pk_mul_f32 v[14:15],v[20:21],v[14:15] op_sel:[1,0] op_sel_hi:[1,1]
v_cvt_pk_bf16_f32 v88,v88,v89
v_cvt_pk_bf16_f32 v89,v90,v91
v_cvt_pk_bf16_f32 v90,v12,v13
v_cvt_pk_bf16_f32 v91,v14,v15
v_mfma_f32_16x16x32_bf16 v[248:251],a[168:171],v[212:215],v[248:251]
ds_read_b128 a[132:135],v1 offset:256
v_accvgpr_read_b32 v98,a66
v_mfma_f32_16x16x32_bf16 v[248:251],a[172:175],v[244:247],v[248:251]
v_accvgpr_read_b32 v92,a56
v_accvgpr_read_b32 v93,a57
v_accvgpr_read_b32 v94,a58
v_accvgpr_read_b32 v95,a59
v_accvgpr_read_b32 v12,a60
v_accvgpr_read_b32 v13,a61
v_accvgpr_read_b32 v14,a62
v_accvgpr_read_b32 v15,a63
v_pk_mul_f32 v[92:93],v[20:21],v[92:93] op_sel:[1,0] op_sel_hi:[1,1]
v_pk_mul_f32 v[94:95],v[20:21],v[94:95] op_sel:[1,0] op_sel_hi:[1,1]
v_pk_mul_f32 v[12:13],v[20:21],v[12:13] op_sel:[1,0] op_sel_hi:[1,1]
v_pk_mul_f32 v[14:15],v[20:21],v[14:15] op_sel:[1,0] op_sel_hi:[1,1]
v_cvt_pk_bf16_f32 v92,v92,v93
v_cvt_pk_bf16_f32 v93,v94,v95
v_cvt_pk_bf16_f32 v94,v12,v13
v_cvt_pk_bf16_f32 v95,v14,v15
v_mfma_f32_16x16x32_bf16 v[252:255],a[188:191],v[244:247],v[44:47]
ds_read_b128 a[136:139],v1 offset:512
v_accvgpr_read_b32 v99,a67
v_mfma_f32_16x16x32_bf16 v[252:255],a[184:187],v[212:215],v[252:255]
v_cmp_nlt_f32 s[58:59],v52,s63
s_nop 1
s_or_b64 s[56:57],s[56:57],s[58:59]
v_mfma_f32_16x16x32_bf16 v[252:255],a[180:183],v[180:183],v[252:255]
ds_read_b128 a[140:143],v1 offset:768
v_mov_b32 v52,0
v_mfma_f32_16x16x32_bf16 v[252:255],a[176:179],v[148:151],v[252:255]
v_exp_f32 v200,v200
v_mfma_f32_16x16x32_bf16 a[32:35],a[192:195],v[168:171],0
ds_read_b128 a[156:159],v1 offset:4864
v_accvgpr_read_b32 v14,a70
v_mfma_f32_16x16x32_bf16 a[32:35],a[196:199],v[172:175],a[32:35]
v_exp_f32 v201,v201
v_mfma_f32_16x16x32_bf16 a[36:39],a[204:207],v[172:175],0
buffer_load_dwordx4 v4,s[20:23],s47 offen offset:1024 nt lds
v_mfma_f32_16x16x32_bf16 a[36:39],a[200:203],v[168:171],a[36:39]
ds_read_b128 a[152:155],v1 offset:4608
v_cvt_pk_bf16_f32 v200,v200,v201
v_mfma_f32_16x16x32_bf16 a[40:43],a[208:211],v[168:171],0
v_exp_f32 v202,v202
v_mfma_f32_16x16x32_bf16 a[40:43],a[212:215],v[172:175],a[40:43]
ds_read_b128 a[148:151],v1 offset:4352
v_accvgpr_read_b32 v15,a71
v_mfma_f32_16x16x32_bf16 a[44:47],a[220:223],v[172:175],0
v_exp_f32 v203,v203
v_mfma_f32_16x16x32_bf16 a[44:47],a[216:219],v[168:171],a[44:47]
ds_read_b128 a[144:147],v1 offset:4096
v_cvt_pk_bf16_f32 v201,v202,v203
v_mfma_f32_16x16x32_bf16 a[48:51],a[224:227],v[168:171],0
v_exp_f32 v204,v204
v_mfma_f32_16x16x32_bf16 a[48:51],a[228:231],v[172:175],a[48:51]
ds_read_b128 a[160:163],v1 offset:8192
v_mov_b32 v57,0
v_mfma_f32_16x16x32_bf16 a[52:55],a[236:239],v[172:175],0
v_exp_f32 v205,v205
v_mfma_f32_16x16x32_bf16 a[52:55],a[232:235],v[168:171],a[52:55]
ds_read_b128 a[164:167],v1 offset:8448
v_cvt_pk_bf16_f32 v202,v204,v205
v_mfma_f32_16x16x32_bf16 a[56:59],a[240:243],v[168:171],0
v_exp_f32 v206,v206
v_mfma_f32_16x16x32_bf16 a[56:59],a[244:247],v[172:175],a[56:59]
ds_read_b128 a[168:171],v1 offset:8704
v_mov_b32 v58,0
v_mfma_f32_16x16x32_bf16 a[60:63],a[252:255],v[172:175],0
v_exp_f32 v207,v207
v_mfma_f32_16x16x32_bf16 a[60:63],a[248:251],v[168:171],a[60:63]
ds_read_b128 a[172:175],v1 offset:8960
v_cvt_pk_bf16_f32 v203,v206,v207
v_smfmac_f32_16x16x64_bf16 v[52:55],v[28:31],v[168:175],v27
s_waitcnt lgkmcnt(4)
v_mov_b32 v59,0
v_mfma_f32_16x16x32_bf16 v[136:139],a[128:131],v[128:131],v[32:35]
buffer_load_dwordx4 v5,s[20:23],s47 offen offset:2048 nt lds
v_mfma_f32_16x16x32_bf16 v[136:139],a[132:135],v[160:163],v[136:139]
ds_read_b128 a[188:191],v1 offset:13056
v_add_f32 v60,v61,v60
v_mfma_f32_16x16x32_bf16 v[136:139],a[136:139],v[192:195],v[136:139]
v_exp_f32 v216,v216
v_mfma_f32_16x16x32_bf16 v[136:139],a[140:143],v[224:227],v[136:139]
ds_read_b128 a[184:187],v1 offset:12800
v_max_f32 v23,0x00800000,v60
v_mfma_f32_16x16x32_bf16 v[140:143],a[156:159],v[224:227],v[32:35]
v_exp_f32 v217,v217
v_mfma_f32_16x16x32_bf16 v[140:143],a[152:155],v[192:195],v[140:143]
ds_read_b128 a[180:183],v1 offset:12544
v_cvt_pk_bf16_f32 v204,v216,v217
v_mfma_f32_16x16x32_bf16 v[140:143],a[148:151],v[160:163],v[140:143]
ds_read_b128 a[176:179],v1 offset:12288
v_mfma_f32_16x16x32_bf16 v[140:143],a[144:147],v[128:131],v[140:143]
s_waitcnt lgkmcnt(4)
v_exp_f32 v218,v218
v_mfma_f32_16x16x32_bf16 v[152:155],a[160:163],v[128:131],v[32:35]
v_exp_f32 v219,v219
v_mfma_f32_16x16x32_bf16 v[152:155],a[164:167],v[160:163],v[152:155]
v_exp_f32 v220,v220
v_mfma_f32_16x16x32_bf16 v[152:155],a[168:171],v[192:195],v[152:155]
v_exp_f32 v221,v221
v_mfma_f32_16x16x32_bf16 v[152:155],a[172:175],v[224:227],v[152:155]
s_waitcnt lgkmcnt(2)
v_exp_f32 v222,v222
v_mfma_f32_16x16x32_bf16 v[156:159],a[188:191],v[224:227],v[32:35]
v_exp_f32 v223,v223
v_mfma_f32_16x16x32_bf16 v[156:159],a[184:187],v[192:195],v[156:159]
s_waitcnt lgkmcnt(1)
v_rcp_f32 v22,v22
v_mfma_f32_16x16x32_bf16 v[156:159],a[180:183],v[160:163],v[156:159]
s_waitcnt lgkmcnt(0)
v_cvt_pk_bf16_f32 v205,v218,v219
v_cvt_pk_bf16_f32 v206,v220,v221
v_cvt_pk_bf16_f32 v207,v222,v223
v_mfma_f32_16x16x32_bf16 v[156:159],a[176:179],v[128:131],v[156:159]
v_mul_f32 v96,v22,v96
v_mul_f32 v97,v22,v97
v_mfma_f32_16x16x32_bf16 a[64:67],a[192:195],v[200:203],0
buffer_load_dwordx4 v5,s[20:23],s47 offen offset:3072 nt lds
v_cvt_pk_bf16_f32 v96,v96,v97
v_mul_f32 v98,v22,v98
v_mul_f32 v99,v22,v99
v_cvt_pk_bf16_f32 v97,v98,v99
v_mfma_f32_16x16x32_bf16 a[64:67],a[196:199],v[204:207],a[64:67]
v_accvgpr_read_b32 v98,a68
v_accvgpr_read_b32 v99,a69
v_mul_f32 v98,v22,v98
v_mul_f32 v99,v22,v99
v_mfma_f32_16x16x32_bf16 a[68:71],a[204:207],v[204:207],0
v_mul_f32 v14,v22,v14
v_mul_f32 v15,v22,v15
v_cvt_pk_bf16_f32 v98,v98,v99
v_cvt_pk_bf16_f32 v99,v14,v15
v_mfma_f32_16x16x32_bf16 a[68:71],a[200:203],v[200:203],a[68:71]
v_accvgpr_read_b32 v100,a72
v_accvgpr_read_b32 v101,a73
v_accvgpr_read_b32 v102,a74
v_accvgpr_read_b32 v103,a75
v_accvgpr_read_b32 v12,a76
v_accvgpr_read_b32 v13,a77
v_accvgpr_read_b32 v14,a78
v_accvgpr_read_b32 v15,a79
v_pk_mul_f32 v[100:101],v[22:23],v[100:101] op_sel:[0,0] op_sel_hi:[0,1]
v_pk_mul_f32 v[102:103],v[22:23],v[102:103] op_sel:[0,0] op_sel_hi:[0,1]
v_pk_mul_f32 v[12:13],v[22:23],v[12:13] op_sel:[0,0] op_sel_hi:[0,1]
v_pk_mul_f32 v[14:15],v[22:23],v[14:15] op_sel:[0,0] op_sel_hi:[0,1]
v_cvt_pk_bf16_f32 v100,v100,v101
v_cvt_pk_bf16_f32 v101,v102,v103
v_cvt_pk_bf16_f32 v102,v12,v13
v_cvt_pk_bf16_f32 v103,v14,v15
v_mfma_f32_16x16x32_bf16 a[72:75],a[208:211],v[200:203],0
v_accvgpr_read_b32 v104,a80
v_accvgpr_read_b32 v105,a81
v_accvgpr_read_b32 v106,a82
v_accvgpr_read_b32 v107,a83
v_accvgpr_read_b32 v12,a84
v_accvgpr_read_b32 v13,a85
v_accvgpr_read_b32 v14,a86
v_accvgpr_read_b32 v15,a87
v_pk_mul_f32 v[104:105],v[22:23],v[104:105] op_sel:[0,0] op_sel_hi:[0,1]
v_pk_mul_f32 v[106:107],v[22:23],v[106:107] op_sel:[0,0] op_sel_hi:[0,1]
v_pk_mul_f32 v[12:13],v[22:23],v[12:13] op_sel:[0,0] op_sel_hi:[0,1]
v_pk_mul_f32 v[14:15],v[22:23],v[14:15] op_sel:[0,0] op_sel_hi:[0,1]
v_cvt_pk_bf16_f32 v104,v104,v105
v_cvt_pk_bf16_f32 v105,v106,v107
v_cvt_pk_bf16_f32 v106,v12,v13
v_cvt_pk_bf16_f32 v107,v14,v15
v_mfma_f32_16x16x32_bf16 a[72:75],a[212:215],v[204:207],a[72:75]
v_accvgpr_read_b32 v108,a88
v_accvgpr_read_b32 v109,a89
v_accvgpr_read_b32 v110,a90
v_accvgpr_read_b32 v111,a91
v_accvgpr_read_b32 v12,a92
v_accvgpr_read_b32 v13,a93
v_accvgpr_read_b32 v14,a94
v_accvgpr_read_b32 v15,a95
v_pk_mul_f32 v[108:109],v[22:23],v[108:109] op_sel:[0,0] op_sel_hi:[0,1]
v_pk_mul_f32 v[110:111],v[22:23],v[110:111] op_sel:[0,0] op_sel_hi:[0,1]
v_pk_mul_f32 v[12:13],v[22:23],v[12:13] op_sel:[0,0] op_sel_hi:[0,1]
v_pk_mul_f32 v[14:15],v[22:23],v[14:15] op_sel:[0,0] op_sel_hi:[0,1]
v_cvt_pk_bf16_f32 v108,v108,v109
v_cvt_pk_bf16_f32 v109,v110,v111
v_cvt_pk_bf16_f32 v110,v12,v13
v_cvt_pk_bf16_f32 v111,v14,v15
v_mfma_f32_16x16x32_bf16 a[76:79],a[220:223],v[204:207],0
v_cmp_nlt_f32 s[58:59],v56,s63
s_nop 1
s_or_b64 s[56:57],s[56:57],s[58:59]
v_mfma_f32_16x16x32_bf16 a[76:79],a[216:219],v[200:203],a[76:79]
v_mov_b32 v56,0
v_accvgpr_read_b32 v112,a96
v_mfma_f32_16x16x32_bf16 a[80:83],a[224:227],v[200:203],0
s_addk_i32 m0,16384
v_exp_f32 v232,v232
v_mfma_f32_16x16x32_bf16 a[80:83],a[228:231],v[204:207],a[80:83]
v_exp_f32 v233,v233
v_mfma_f32_16x16x32_bf16 a[84:87],a[236:239],v[204:207],0
v_exp_f32 v234,v234
v_mfma_f32_16x16x32_bf16 a[84:87],a[232:235],v[200:203],a[84:87]
buffer_load_dwordx4 v4,s[24:27],s47 offen nt lds
v_mfma_f32_16x16x32_bf16 a[88:91],a[240:243],v[200:203],0
v_exp_f32 v235,v235
v_mfma_f32_16x16x32_bf16 a[88:91],a[244:247],v[204:207],a[88:91]
v_cvt_pk_bf16_f32 v232,v232,v233
v_cvt_pk_bf16_f32 v233,v234,v235
v_mfma_f32_16x16x32_bf16 a[92:95],a[252:255],v[204:207],0
v_exp_f32 v236,v236
v_mfma_f32_16x16x32_bf16 a[92:95],a[248:251],v[200:203],a[92:95]
v_exp_f32 v237,v237
v_smfmac_f32_16x16x64_bf16 v[56:59],v[28:31],v[200:207],v27
v_cvt_pk_bf16_f32 v234,v236,v237
v_mfma_f32_16x16x32_bf16 v[168:171],a[128:131],v[132:135],v[36:39]
v_exp_f32 v238,v238
v_mfma_f32_16x16x32_bf16 v[168:171],a[132:135],v[164:167],v[168:171]
v_exp_f32 v239,v239
v_mfma_f32_16x16x32_bf16 v[168:171],a[136:139],v[196:199],v[168:171]
v_cvt_pk_bf16_f32 v235,v238,v239
v_accvgpr_read_b32 v113,a97
v_mfma_f32_16x16x32_bf16 v[168:171],a[140:143],v[228:231],v[168:171]
v_exp_f32 v248,v248
v_mfma_f32_16x16x32_bf16 v[172:175],a[156:159],v[228:231],v[36:39]
v_exp_f32 v249,v249
v_mfma_f32_16x16x32_bf16 v[172:175],a[152:155],v[196:199],v[172:175]
v_exp_f32 v250,v250
v_mfma_f32_16x16x32_bf16 v[172:175],a[148:151],v[164:167],v[172:175]
v_exp_f32 v251,v251
v_mfma_f32_16x16x32_bf16 v[172:175],a[144:147],v[132:135],v[172:175]
v_exp_f32 v252,v252
v_mfma_f32_16x16x32_bf16 v[184:187],a[160:163],v[132:135],v[36:39]
buffer_load_dwordx4 v5,s[24:27],s47 offen offset:1024 nt lds
v_mfma_f32_16x16x32_bf16 v[184:187],a[164:167],v[164:167],v[184:187]
v_exp_f32 v253,v253
v_mfma_f32_16x16x32_bf16 v[184:187],a[168:171],v[196:199],v[184:187]
v_exp_f32 v254,v254
v_mfma_f32_16x16x32_bf16 v[184:187],a[172:175],v[228:231],v[184:187]
v_exp_f32 v255,v255
v_mfma_f32_16x16x32_bf16 v[188:191],a[188:191],v[228:231],v[36:39]
v_cvt_pk_bf16_f32 v236,v248,v249
v_cvt_pk_bf16_f32 v237,v250,v251
v_mfma_f32_16x16x32_bf16 v[188:191],a[184:187],v[196:199],v[188:191]
v_cvt_pk_bf16_f32 v238,v252,v253
v_cvt_pk_bf16_f32 v239,v254,v255
v_mfma_f32_16x16x32_bf16 v[188:191],a[180:183],v[164:167],v[188:191]
v_rcp_f32 v23,v23
v_mfma_f32_16x16x32_bf16 v[188:191],a[176:179],v[132:135],v[188:191]
v_accvgpr_read_b32 v114,a98
v_accvgpr_read_b32 v115,a99
v_mfma_f32_16x16x32_bf16 a[96:99],a[192:195],v[232:235],0
v_mul_f32 v66,v20,v66
v_mul_f32 v67,v20,v67
v_mul_f32 v112,v23,v112
v_mul_f32 v113,v23,v113
v_mul_f32 v16,v20,v16
v_mul_f32 v17,v20,v17
v_cvt_pk_bf16_f32 v66,v66,v67
v_cvt_pk_bf16_f32 v112,v112,v113
v_mul_f32 v114,v23,v114
v_mul_f32 v115,v23,v115
v_mfma_f32_16x16x32_bf16 a[96:99],a[196:199],v[236:239],a[96:99]
v_cvt_pk_bf16_f32 v67,v16,v17
v_cvt_pk_bf16_f32 v113,v114,v115
v_accvgpr_read_b32 v16,a102
v_accvgpr_read_b32 v17,a103
v_accvgpr_read_b32 v114,a100
v_accvgpr_read_b32 v115,a101
v_mfma_f32_16x16x32_bf16 a[100:103],a[204:207],v[236:239],0
v_accvgpr_read_b32 v116,a104
)A"
R"A(v_accvgpr_read_b32 v117,a105
v_accvgpr_read_b32 v118,a106
v_accvgpr_read_b32 v119,a107
v_accvgpr_read_b32 v12,a108
v_accvgpr_read_b32 v13,a109
v_accvgpr_read_b32 v14,a110
v_accvgpr_read_b32 v15,a111
v_pk_mul_f32 v[116:117],v[22:23],v[116:117] op_sel:[1,0] op_sel_hi:[1,1]
v_pk_mul_f32 v[118:119],v[22:23],v[118:119] op_sel:[1,0] op_sel_hi:[1,1]
v_pk_mul_f32 v[12:13],v[22:23],v[12:13] op_sel:[1,0] op_sel_hi:[1,1]
v_pk_mul_f32 v[14:15],v[22:23],v[14:15] op_sel:[1,0] op_sel_hi:[1,1]
v_cvt_pk_bf16_f32 v116,v116,v117
v_cvt_pk_bf16_f32 v117,v118,v119
v_cvt_pk_bf16_f32 v118,v12,v13
v_cvt_pk_bf16_f32 v119,v14,v15
v_mfma_f32_16x16x32_bf16 a[100:103],a[200:203],v[232:235],a[100:103]
v_accvgpr_read_b32 v120,a112
v_accvgpr_read_b32 v121,a113
v_accvgpr_read_b32 v122,a114
v_accvgpr_read_b32 v123,a115
v_accvgpr_read_b32 v12,a116
v_accvgpr_read_b32 v13,a117
v_accvgpr_read_b32 v14,a118
v_accvgpr_read_b32 v15,a119
v_pk_mul_f32 v[120:121],v[22:23],v[120:121] op_sel:[1,0] op_sel_hi:[1,1]
v_pk_mul_f32 v[122:123],v[22:23],v[122:123] op_sel:[1,0] op_sel_hi:[1,1]
v_pk_mul_f32 v[12:13],v[22:23],v[12:13] op_sel:[1,0] op_sel_hi:[1,1]
v_pk_mul_f32 v[14:15],v[22:23],v[14:15] op_sel:[1,0] op_sel_hi:[1,1]
v_cvt_pk_bf16_f32 v120,v120,v121
v_cvt_pk_bf16_f32 v121,v122,v123
v_cvt_pk_bf16_f32 v122,v12,v13
v_cvt_pk_bf16_f32 v123,v14,v15
v_mfma_f32_16x16x32_bf16 a[104:107],a[208:211],v[232:235],0
v_accvgpr_read_b32 v124,a120
v_accvgpr_read_b32 v125,a121
v_accvgpr_read_b32 v126,a122
v_accvgpr_read_b32 v127,a123
v_accvgpr_read_b32 v12,a124
v_accvgpr_read_b32 v13,a125
v_accvgpr_read_b32 v14,a126
v_accvgpr_read_b32 v15,a127
v_pk_mul_f32 v[124:125],v[22:23],v[124:125] op_sel:[1,0] op_sel_hi:[1,1]
v_pk_mul_f32 v[126:127],v[22:23],v[126:127] op_sel:[1,0] op_sel_hi:[1,1]
v_pk_mul_f32 v[12:13],v[22:23],v[12:13] op_sel:[1,0] op_sel_hi:[1,1]
v_pk_mul_f32 v[14:15],v[22:23],v[14:15] op_sel:[1,0] op_sel_hi:[1,1]
v_cvt_pk_bf16_f32 v124,v124,v125
v_cvt_pk_bf16_f32 v125,v126,v127
v_cvt_pk_bf16_f32 v126,v12,v13
v_cvt_pk_bf16_f32 v127,v14,v15
v_mfma_f32_16x16x32_bf16 a[104:107],a[212:215],v[236:239],a[104:107]
v_cmp_nlt_f32 s[58:59],v60,s63
s_nop 1
s_or_b64 s[56:57],s[56:57],s[58:59]
v_mfma_f32_16x16x32_bf16 a[108:111],a[220:223],v[236:239],0
buffer_load_dwordx4 v4,s[24:27],s47 offen offset:2048 nt lds
v_mfma_f32_16x16x32_bf16 a[108:111],a[216:219],v[232:235],a[108:111]
v_mov_b32 v60,0
v_mov_b32 v61,0
v_mfma_f32_16x16x32_bf16 a[112:115],a[224:227],v[232:235],0
v_mov_b32 v62,0
v_mov_b32 v63,0
v_mfma_f32_16x16x32_bf16 a[112:115],a[228:231],v[236:239],a[112:115]
v_add_u32 v2,s48,v2
v_add_u32 v3,s48,v3
v_mfma_f32_16x16x32_bf16 a[116:119],a[236:239],v[236:239],0
s_mov_b32 s48,32768
ds_read_b64_tr_b16 a[192:193],v2
ds_read_b64_tr_b16 a[194:195],v2 offset:4096
v_mfma_f32_16x16x32_bf16 a[116:119],a[232:235],v[232:235],a[116:119]
ds_read_b64_tr_b16 a[196:197],v2 offset:8192
v_mul_f32 v114,v23,v114
v_mfma_f32_16x16x32_bf16 a[120:123],a[240:243],v[232:235],0
ds_read_b64_tr_b16 a[198:199],v2 offset:12288
v_mul_f32 v115,v23,v115
v_mfma_f32_16x16x32_bf16 a[120:123],a[244:247],v[236:239],a[120:123]
ds_read_b64_tr_b16 a[204:205],v3 offset:8192
v_mul_f32 v16,v23,v16
v_mfma_f32_16x16x32_bf16 a[124:127],a[252:255],v[236:239],0
ds_read_b64_tr_b16 a[206:207],v3 offset:12288
ds_read_b64_tr_b16 a[200:201],v3
v_mfma_f32_16x16x32_bf16 a[124:127],a[248:251],v[232:235],a[124:127]
ds_read_b64_tr_b16 a[202:203],v3 offset:4096
v_mul_f32 v17,v23,v17
v_smfmac_f32_16x16x64_bf16 v[60:63],v[28:31],v[232:239],v27
ds_read_b64_tr_b16 a[208:209],v2 offset:256
v_mfma_f32_16x16x32_bf16 v[200:203],a[128:131],v[144:147],v[40:43]
v_exp_f32 v136,v136
v_mfma_f32_16x16x32_bf16 v[200:203],a[132:135],v[176:179],v[200:203]
v_exp_f32 v137,v137
v_mfma_f32_16x16x32_bf16 v[200:203],a[136:139],v[208:211],v[200:203]
ds_read_b64_tr_b16 a[210:211],v2 offset:4352
v_cvt_pk_bf16_f32 v136,v136,v137
v_mfma_f32_16x16x32_bf16 v[200:203],a[140:143],v[240:243],v[200:203]
buffer_load_dwordx4 v5,s[24:27],s47 offen offset:3072 nt lds
v_mfma_f32_16x16x32_bf16 v[204:207],a[156:159],v[240:243],v[40:43]
s_or_b32 s47,s47,0x18000
v_exp_f32 v138,v138
v_mfma_f32_16x16x32_bf16 v[204:207],a[152:155],v[208:211],v[204:207]
s_addk_i32 s47,4096
v_exp_f32 v139,v139
v_mfma_f32_16x16x32_bf16 v[204:207],a[148:151],v[176:179],v[204:207]
s_and_b32 s47,s47,0x27000
v_exp_f32 v140,v140
v_mfma_f32_16x16x32_bf16 v[204:207],a[144:147],v[144:147],v[204:207]
s_or_b32 s47,s47,s46
v_exp_f32 v141,v141
v_mfma_f32_16x16x32_bf16 v[216:219],a[160:163],v[144:147],v[40:43]
v_exp_f32 v142,v142
v_mfma_f32_16x16x32_bf16 v[216:219],a[164:167],v[176:179],v[216:219]
v_exp_f32 v143,v143
v_mfma_f32_16x16x32_bf16 v[216:219],a[168:171],v[208:211],v[216:219]
ds_read_b64_tr_b16 a[212:213],v2 offset:8448
ds_read_b64_tr_b16 a[214:215],v2 offset:12544
v_mfma_f32_16x16x32_bf16 v[216:219],a[172:175],v[240:243],v[216:219]
v_cvt_pk_bf16_f32 v137,v138,v139
v_cvt_pk_bf16_f32 v138,v140,v141
v_mfma_f32_16x16x32_bf16 v[220:223],a[188:191],v[240:243],v[40:43]
ds_read_b64_tr_b16 a[220:221],v3 offset:8448
v_cvt_pk_bf16_f32 v139,v142,v143
v_mfma_f32_16x16x32_bf16 v[220:223],a[184:187],v[208:211],v[220:223]
ds_read_b64_tr_b16 a[222:223],v3 offset:12544
v_exp_f32 v152,v152
v_exp_f32 v153,v153
v_exp_f32 v154,v154
v_exp_f32 v155,v155
v_exp_f32 v156,v156
v_exp_f32 v157,v157
v_exp_f32 v158,v158
v_exp_f32 v159,v159
v_mfma_f32_16x16x32_bf16 v[220:223],a[180:183],v[176:179],v[220:223]
ds_read_b64_tr_b16 a[216:217],v3 offset:256
ds_read_b64_tr_b16 a[218:219],v3 offset:4352
v_cvt_pk_bf16_f32 v140,v152,v153
v_cvt_pk_bf16_f32 v141,v154,v155
v_cvt_pk_bf16_f32 v142,v156,v157
v_cvt_pk_bf16_f32 v143,v158,v159
v_mfma_f32_16x16x32_bf16 v[220:223],a[176:179],v[144:147],v[220:223]
s_waitcnt lgkmcnt(6)
ds_read_b64_tr_b16 a[224:225],v2 offset:512
ds_read_b64_tr_b16 a[226:227],v2 offset:4608
v_mfma_f32_16x16x32_bf16 a[0:3],a[192:195],v[136:139],a[0:3]
ds_read_b64_tr_b16 a[228:229],v2 offset:8704
ds_read_b64_tr_b16 a[230:231],v2 offset:12800
v_mfma_f32_16x16x32_bf16 a[0:3],a[196:199],v[140:143],a[0:3]
ds_read_b64_tr_b16 a[236:237],v3 offset:8704
ds_read_b64_tr_b16 a[238:239],v3 offset:12800
v_mfma_f32_16x16x32_bf16 a[4:7],a[204:207],v[140:143],a[4:7]
ds_read_b64_tr_b16 a[232:233],v3 offset:512
ds_read_b64_tr_b16 a[234:235],v3 offset:4608
v_mfma_f32_16x16x32_bf16 a[4:7],a[200:203],v[136:139],a[4:7]
ds_read_b64_tr_b16 a[240:241],v2 offset:768
ds_read_b64_tr_b16 a[242:243],v2 offset:4864
v_mfma_f32_16x16x32_bf16 a[8:11],a[208:211],v[136:139],a[8:11]
s_memrealtime s[76:77]
ds_read_b64_tr_b16 a[244:245],v2 offset:8960
ds_read_b64_tr_b16 a[246:247],v2 offset:13056
s_waitcnt lgkmcnt(15)
v_mfma_f32_16x16x32_bf16 a[8:11],a[212:215],v[140:143],a[8:11]
s_waitcnt lgkmcnt(14)
ds_read_b64_tr_b16 a[252:253],v3 offset:8960
ds_read_b64_tr_b16 a[254:255],v3 offset:13056
v_mfma_f32_16x16x32_bf16 a[12:15],a[220:223],v[140:143],a[12:15]
s_waitcnt lgkmcnt(14)
ds_read_b64_tr_b16 a[248:249],v3 offset:768
ds_read_b64_tr_b16 a[250:251],v3 offset:4864
v_mfma_f32_16x16x32_bf16 a[12:15],a[216:219],v[136:139],a[12:15]
s_waitcnt lgkmcnt(14)
v_mfma_f32_16x16x32_bf16 a[16:19],a[224:227],v[136:139],a[16:19]
s_waitcnt lgkmcnt(12)
v_mfma_f32_16x16x32_bf16 a[16:19],a[228:231],v[140:143],a[16:19]
s_waitcnt lgkmcnt(10)
v_mfma_f32_16x16x32_bf16 a[20:23],a[236:239],v[140:143],a[20:23]
s_waitcnt lgkmcnt(0)
v_mfma_f32_16x16x32_bf16 a[20:23],a[232:235],v[136:139],a[20:23]
s_sub_u32 s79,s78,s76
v_exp_f32 v168,v168
v_mfma_f32_16x16x32_bf16 a[24:27],a[240:243],v[136:139],a[24:27]
s_add_u32 s78,s76,s60
v_exp_f32 v169,v169
v_mfma_f32_16x16x32_bf16 a[24:27],a[244:247],v[140:143],a[24:27]
s_cmp_gt_i32 s79,s95
s_cbranch_scc1 L_ps6_%=
L_pr6_%=:
v_exp_f32 v170,v170
v_exp_f32 v171,v171
v_mfma_f32_16x16x32_bf16 a[28:31],a[252:255],v[140:143],a[28:31]
v_cvt_pk_bf16_f32 v114,v114,v115
v_cvt_pk_bf16_f32 v168,v168,v169
v_exp_f32 v172,v172
v_exp_f32 v173,v173
v_exp_f32 v174,v174
v_exp_f32 v175,v175
v_cvt_pk_bf16_f32 v115,v16,v17
v_cvt_pk_bf16_f32 v169,v170,v171
v_exp_f32 v184,v184
v_exp_f32 v185,v185
v_exp_f32 v186,v186
v_exp_f32 v187,v187
v_exp_f32 v188,v188
v_exp_f32 v189,v189
v_exp_f32 v190,v190
v_exp_f32 v191,v191
v_mfma_f32_16x16x32_bf16 a[28:31],a[248:251],v[136:139],a[28:31]
s_cmp_lg_u64 s[56:57],0
s_cbranch_scc1 L_slow0_%=
L_sret0_%=:
v_cvt_pk_bf16_f32 v170,v172,v173
v_cvt_pk_bf16_f32 v171,v174,v175
v_add_u32 v1,s48,v1
v_cvt_pk_bf16_f32 v172,v184,v185
v_cvt_pk_bf16_f32 v173,v186,v187
v_cvt_pk_bf16_f32 v174,v188,v189
v_cvt_pk_bf16_f32 v175,v190,v191
v_smfmac_f32_16x16x64_bf16 v[48:51],v[28:31],v[136:143],v27
s_mov_b32 s53,11
s_cmp_eq_u32 s40,3
s_cbranch_scc0 L_att1_%=
s_cmp_gt_i32 s18,0
s_cbranch_scc1 L_att1v0_%=
s_cmp_gt_i32 s19,0
s_cbranch_scc1 L_att1v1_%=
s_cmp_gt_i32 s100,0
s_cbranch_scc1 L_att1v2_%=
.p2align 5
L_att1v3_%=:
s_waitcnt vmcnt(8) lgkmcnt(0)
v_exp_f32 v200,v200
s_barrier
s_add_u32 m0,s49,s51
ds_read_b128 a[128:131],v1
s_mov_b32 s90,L_out_end_%=-L_pc_%=+128
v_exp_f32 v201,v201
s_lshr_b32 s45,s45,1
ds_read_b128 a[132:135],v1 offset:256
v_cvt_pk_bf16_f32 v200,v200,v201
buffer_load_dwordx4 v4,s[20:23],s47 offen nt lds
ds_read_b128 a[136:139],v1 offset:512
v_add_u32 v2,s48,v2
v_exp_f32 v202,v202
s_add_u32 s62,s50,32768
ds_read_b128 a[140:143],v1 offset:768
v_add_u32 v3,s48,v3
s_and_b32 s62,s62,131071
v_exp_f32 v203,v203
s_addk_i32 s54,16384
ds_read_b128 a[156:159],v1 offset:4864
v_cvt_pk_bf16_f32 v201,v202,v203
s_sub_u32 s48,s62,s50
v_exp_f32 v204,v204
s_sub_u32 s53,s53,1
v_exp_f32 v205,v205
s_and_b32 s46,s54,0xfffc0000
ds_read_b128 a[152:155],v1 offset:4608
v_cvt_pk_bf16_f32 v202,v204,v205
s_mov_b32 s50,s62
v_exp_f32 v206,v206
s_xor_b32 s49,s50,65536
v_exp_f32 v207,v207
ds_read_b128 a[148:151],v1 offset:4352
v_cvt_pk_bf16_f32 v203,v206,v207
v_mfma_f32_16x16x32_bf16 a[32:35],a[192:195],v[168:171],a[32:35]
v_exp_f32 v216,v216
v_mfma_f32_16x16x32_bf16 a[32:35],a[196:199],v[172:175],a[32:35]
ds_read_b128 a[144:147],v1 offset:4096
v_mfma_f32_16x16x32_bf16 a[36:39],a[204:207],v[172:175],a[36:39]
buffer_load_dwordx4 v4,s[20:23],s47 offen offset:1024 nt lds
v_mfma_f32_16x16x32_bf16 a[36:39],a[200:203],v[168:171],a[36:39]
ds_read_b128 a[160:163],v1 offset:8192
v_mfma_f32_16x16x32_bf16 a[40:43],a[208:211],v[168:171],a[40:43]
v_exp_f32 v217,v217
v_mfma_f32_16x16x32_bf16 a[40:43],a[212:215],v[172:175],a[40:43]
ds_read_b128 a[164:167],v1 offset:8448
v_cvt_pk_bf16_f32 v204,v216,v217
v_mfma_f32_16x16x32_bf16 a[44:47],a[220:223],v[172:175],a[44:47]
v_exp_f32 v218,v218
v_mfma_f32_16x16x32_bf16 a[44:47],a[216:219],v[168:171],a[44:47]
ds_read_b128 a[168:171],v1 offset:8704
v_mfma_f32_16x16x32_bf16 a[48:51],a[224:227],v[168:171],a[48:51]
v_exp_f32 v219,v219
v_mfma_f32_16x16x32_bf16 a[48:51],a[228:231],v[172:175],a[48:51]
ds_read_b128 a[172:175],v1 offset:8960
v_cvt_pk_bf16_f32 v205,v218,v219
v_mfma_f32_16x16x32_bf16 a[52:55],a[236:239],v[172:175],a[52:55]
v_exp_f32 v220,v220
v_mfma_f32_16x16x32_bf16 a[52:55],a[232:235],v[168:171],a[52:55]
ds_read_b128 a[188:191],v1 offset:13056
v_mfma_f32_16x16x32_bf16 a[56:59],a[240:243],v[168:171],a[56:59]
v_exp_f32 v221,v221
v_mfma_f32_16x16x32_bf16 a[56:59],a[244:247],v[172:175],a[56:59]
ds_read_b128 a[184:187],v1 offset:12800
v_cvt_pk_bf16_f32 v206,v220,v221
v_mfma_f32_16x16x32_bf16 a[60:63],a[252:255],v[172:175],a[60:63]
v_exp_f32 v222,v222
v_mfma_f32_16x16x32_bf16 a[60:63],a[248:251],v[168:171],a[60:63]
ds_read_b128 a[180:183],v1 offset:12544
v_smfmac_f32_16x16x64_bf16 v[52:55],v[28:31],v[168:175],v27
s_waitcnt lgkmcnt(4)
v_mfma_f32_16x16x32_bf16 v[136:139],a[128:131],v[128:131],v[32:35]
buffer_load_dwordx4 v5,s[20:23],s47 offen offset:2048 nt lds
v_mfma_f32_16x16x32_bf16 v[136:139],a[132:135],v[160:163],v[136:139]
ds_read_b128 a[176:179],v1 offset:12288
v_add_u32 v1,s48,v1
v_mfma_f32_16x16x32_bf16 v[136:139],a[136:139],v[192:195],v[136:139]
v_exp_f32 v223,v223
v_mfma_f32_16x16x32_bf16 v[136:139],a[140:143],v[224:227],v[136:139]
v_cvt_pk_bf16_f32 v207,v222,v223
v_mfma_f32_16x16x32_bf16 v[140:143],a[156:159],v[224:227],v[32:35]
v_mfma_f32_16x16x32_bf16 v[140:143],a[152:155],v[192:195],v[140:143]
v_mfma_f32_16x16x32_bf16 v[140:143],a[148:151],v[160:163],v[140:143]
)A"
R"A(v_mfma_f32_16x16x32_bf16 v[140:143],a[144:147],v[128:131],v[140:143]
v_mfma_f32_16x16x32_bf16 v[152:155],a[160:163],v[128:131],v[32:35]
v_mfma_f32_16x16x32_bf16 v[152:155],a[164:167],v[160:163],v[152:155]
v_mfma_f32_16x16x32_bf16 v[152:155],a[168:171],v[192:195],v[152:155]
s_waitcnt lgkmcnt(0)
v_mfma_f32_16x16x32_bf16 v[152:155],a[172:175],v[224:227],v[152:155]
v_mfma_f32_16x16x32_bf16 v[156:159],a[188:191],v[224:227],v[32:35]
v_mfma_f32_16x16x32_bf16 v[156:159],a[184:187],v[192:195],v[156:159]
buffer_load_dwordx4 v5,s[20:23],s47 offen offset:3072 nt lds
v_mfma_f32_16x16x32_bf16 v[156:159],a[180:183],v[160:163],v[156:159]
v_mfma_f32_16x16x32_bf16 v[156:159],a[176:179],v[128:131],v[156:159]
v_mfma_f32_16x16x32_bf16 a[64:67],a[192:195],v[200:203],a[64:67]
v_mfma_f32_16x16x32_bf16 a[64:67],a[196:199],v[204:207],a[64:67]
v_mfma_f32_16x16x32_bf16 a[68:71],a[204:207],v[204:207],a[68:71]
v_mfma_f32_16x16x32_bf16 a[68:71],a[200:203],v[200:203],a[68:71]
v_mfma_f32_16x16x32_bf16 a[72:75],a[208:211],v[200:203],a[72:75]
v_mfma_f32_16x16x32_bf16 a[72:75],a[212:215],v[204:207],a[72:75]
v_exp_f32 v136,v136
v_mfma_f32_16x16x32_bf16 a[76:79],a[220:223],v[204:207],a[76:79]
v_exp_f32 v137,v137
v_mfma_f32_16x16x32_bf16 a[76:79],a[216:219],v[200:203],a[76:79]
v_exp_f32 v138,v138
v_mfma_f32_16x16x32_bf16 a[80:83],a[224:227],v[200:203],a[80:83]
s_addk_i32 m0,16384
v_exp_f32 v139,v139
v_mfma_f32_16x16x32_bf16 a[80:83],a[228:231],v[204:207],a[80:83]
v_exp_f32 v140,v140
v_mfma_f32_16x16x32_bf16 a[84:87],a[236:239],v[204:207],a[84:87]
v_exp_f32 v141,v141
v_mfma_f32_16x16x32_bf16 a[84:87],a[232:235],v[200:203],a[84:87]
buffer_load_dwordx4 v4,s[24:27],s47 offen nt lds
v_mfma_f32_16x16x32_bf16 a[88:91],a[240:243],v[200:203],a[88:91]
v_exp_f32 v142,v142
v_mfma_f32_16x16x32_bf16 a[88:91],a[244:247],v[204:207],a[88:91]
v_exp_f32 v143,v143
v_mfma_f32_16x16x32_bf16 a[92:95],a[252:255],v[204:207],a[92:95]
v_mfma_f32_16x16x32_bf16 a[92:95],a[248:251],v[200:203],a[92:95]
v_smfmac_f32_16x16x64_bf16 v[56:59],v[28:31],v[200:207],v27
v_mfma_f32_16x16x32_bf16 v[168:171],a[128:131],v[132:135],v[36:39]
v_mfma_f32_16x16x32_bf16 v[168:171],a[132:135],v[164:167],v[168:171]
v_mfma_f32_16x16x32_bf16 v[168:171],a[136:139],v[196:199],v[168:171]
v_exp_f32 v152,v152
v_mfma_f32_16x16x32_bf16 v[168:171],a[140:143],v[228:231],v[168:171]
v_exp_f32 v153,v153
v_mfma_f32_16x16x32_bf16 v[172:175],a[156:159],v[228:231],v[36:39]
v_exp_f32 v154,v154
v_mfma_f32_16x16x32_bf16 v[172:175],a[152:155],v[196:199],v[172:175]
v_exp_f32 v155,v155
v_mfma_f32_16x16x32_bf16 v[172:175],a[148:151],v[164:167],v[172:175]
v_exp_f32 v156,v156
v_mfma_f32_16x16x32_bf16 v[172:175],a[144:147],v[132:135],v[172:175]
v_exp_f32 v157,v157
v_mfma_f32_16x16x32_bf16 v[184:187],a[160:163],v[132:135],v[36:39]
buffer_load_dwordx4 v5,s[24:27],s47 offen offset:1024 nt lds
v_mfma_f32_16x16x32_bf16 v[184:187],a[164:167],v[164:167],v[184:187]
v_exp_f32 v158,v158
v_mfma_f32_16x16x32_bf16 v[184:187],a[168:171],v[196:199],v[184:187]
v_exp_f32 v159,v159
v_mfma_f32_16x16x32_bf16 v[184:187],a[172:175],v[228:231],v[184:187]
v_exp_f32 v168,v168
v_mfma_f32_16x16x32_bf16 v[188:191],a[188:191],v[228:231],v[36:39]
v_exp_f32 v169,v169
v_mfma_f32_16x16x32_bf16 v[188:191],a[184:187],v[196:199],v[188:191]
v_exp_f32 v170,v170
v_mfma_f32_16x16x32_bf16 v[188:191],a[180:183],v[164:167],v[188:191]
v_exp_f32 v171,v171
v_mfma_f32_16x16x32_bf16 v[188:191],a[176:179],v[132:135],v[188:191]
v_exp_f32 v172,v172
ds_read_b64_tr_b16 a[192:193],v2
v_cvt_pk_bf16_f32 v168,v168,v169
ds_read_b64_tr_b16 a[194:195],v2 offset:4096
v_cvt_pk_bf16_f32 v169,v170,v171
v_exp_f32 v173,v173
ds_read_b64_tr_b16 a[196:197],v2 offset:8192
v_cvt_pk_bf16_f32 v170,v172,v173
v_exp_f32 v174,v174
v_exp_f32 v175,v175
buffer_load_dwordx4 v4,s[24:27],s47 offen offset:2048 nt lds
ds_read_b64_tr_b16 a[198:199],v2 offset:12288
v_cvt_pk_bf16_f32 v171,v174,v175
v_exp_f32 v184,v184
v_exp_f32 v185,v185
ds_read_b64_tr_b16 a[204:205],v3 offset:8192
v_cvt_pk_bf16_f32 v136,v136,v137
ds_read_b64_tr_b16 a[206:207],v3 offset:12288
v_cvt_pk_bf16_f32 v172,v184,v185
ds_read_b64_tr_b16 a[200:201],v3
ds_read_b64_tr_b16 a[202:203],v3 offset:4096
v_cvt_pk_bf16_f32 v137,v138,v139
ds_read_b64_tr_b16 a[208:209],v2 offset:256
ds_read_b64_tr_b16 a[210:211],v2 offset:4352
ds_read_b64_tr_b16 a[212:213],v2 offset:8448
v_mfma_f32_16x16x32_bf16 v[200:203],a[128:131],v[144:147],v[40:43]
ds_read_b64_tr_b16 a[214:215],v2 offset:12544
v_cvt_pk_bf16_f32 v138,v140,v141
v_mfma_f32_16x16x32_bf16 v[200:203],a[132:135],v[176:179],v[200:203]
ds_read_b64_tr_b16 a[220:221],v3 offset:8448
v_mfma_f32_16x16x32_bf16 v[200:203],a[136:139],v[208:211],v[200:203]
ds_read_b64_tr_b16 a[222:223],v3 offset:12544
v_cvt_pk_bf16_f32 v139,v142,v143
v_mfma_f32_16x16x32_bf16 v[200:203],a[140:143],v[240:243],v[200:203]
buffer_load_dwordx4 v5,s[24:27],s47 offen offset:3072 nt lds
v_mfma_f32_16x16x32_bf16 v[204:207],a[156:159],v[240:243],v[40:43]
s_or_b32 s47,s47,0x18000
s_addk_i32 s47,4096
ds_read_b64_tr_b16 a[216:217],v3 offset:256
v_mfma_f32_16x16x32_bf16 v[204:207],a[152:155],v[208:211],v[204:207]
s_and_b32 s47,s47,0x27000
ds_read_b64_tr_b16 a[218:219],v3 offset:4352
v_cvt_pk_bf16_f32 v140,v152,v153
v_mfma_f32_16x16x32_bf16 v[204:207],a[148:151],v[176:179],v[204:207]
s_or_b32 s47,s47,s46
ds_read_b64_tr_b16 a[224:225],v2 offset:512
v_mfma_f32_16x16x32_bf16 v[204:207],a[144:147],v[144:147],v[204:207]
ds_read_b64_tr_b16 a[226:227],v2 offset:4608
v_mfma_f32_16x16x32_bf16 v[216:219],a[160:163],v[144:147],v[40:43]
ds_read_b64_tr_b16 a[228:229],v2 offset:8704
v_cvt_pk_bf16_f32 v141,v154,v155
v_mfma_f32_16x16x32_bf16 v[216:219],a[164:167],v[176:179],v[216:219]
ds_read_b64_tr_b16 a[230:231],v2 offset:12800
v_mfma_f32_16x16x32_bf16 v[216:219],a[168:171],v[208:211],v[216:219]
ds_read_b64_tr_b16 a[236:237],v3 offset:8704
v_mfma_f32_16x16x32_bf16 v[216:219],a[172:175],v[240:243],v[216:219]
ds_read_b64_tr_b16 a[238:239],v3 offset:12800
v_cvt_pk_bf16_f32 v142,v156,v157
v_mfma_f32_16x16x32_bf16 v[220:223],a[188:191],v[240:243],v[40:43]
ds_read_b64_tr_b16 a[232:233],v3 offset:512
v_mfma_f32_16x16x32_bf16 v[220:223],a[184:187],v[208:211],v[220:223]
ds_read_b64_tr_b16 a[234:235],v3 offset:4608
v_mfma_f32_16x16x32_bf16 v[220:223],a[180:183],v[176:179],v[220:223]
ds_read_b64_tr_b16 a[240:241],v2 offset:768
v_cvt_pk_bf16_f32 v143,v158,v159
v_mfma_f32_16x16x32_bf16 v[220:223],a[176:179],v[144:147],v[220:223]
s_waitcnt lgkmcnt(7)
buffer_load_dword v26,v24,s[88:91],s93 offen
v_mfma_f32_16x16x32_bf16 a[0:3],a[192:195],v[136:139],a[0:3]
ds_read_b64_tr_b16 a[242:243],v2 offset:4864
v_mfma_f32_16x16x32_bf16 a[0:3],a[196:199],v[140:143],a[0:3]
ds_read_b64_tr_b16 a[244:245],v2 offset:8960
v_mfma_f32_16x16x32_bf16 a[4:7],a[204:207],v[140:143],a[4:7]
ds_read_b64_tr_b16 a[246:247],v2 offset:13056
v_mfma_f32_16x16x32_bf16 a[4:7],a[200:203],v[136:139],a[4:7]
ds_read_b64_tr_b16 a[252:253],v3 offset:8960
v_mfma_f32_16x16x32_bf16 a[8:11],a[208:211],v[136:139],a[8:11]
ds_read_b64_tr_b16 a[254:255],v3 offset:13056
v_mfma_f32_16x16x32_bf16 a[8:11],a[212:215],v[140:143],a[8:11]
ds_read_b64_tr_b16 a[248:249],v3 offset:768
v_mfma_f32_16x16x32_bf16 a[12:15],a[220:223],v[140:143],a[12:15]
ds_read_b64_tr_b16 a[250:251],v3 offset:4864
v_mfma_f32_16x16x32_bf16 a[12:15],a[216:219],v[136:139],a[12:15]
s_bitcmp1_b32 s45,0
s_cbranch_scc0 L_t31v31_%=
s_mov_b32 m0,s80
s_addk_i32 s80,4096
buffer_load_dwordx4 v25,s[24:27],s83 offen nt lds
s_addk_i32 s83,256
L_t31v31_%=:
v_mfma_f32_16x16x32_bf16 a[16:19],a[224:227],v[136:139],a[16:19]
s_waitcnt lgkmcnt(6)
v_exp_f32 v186,v186
v_mfma_f32_16x16x32_bf16 a[16:19],a[228:231],v[140:143],a[16:19]
v_exp_f32 v187,v187
v_mfma_f32_16x16x32_bf16 a[20:23],a[236:239],v[140:143],a[20:23]
v_exp_f32 v188,v188
v_mfma_f32_16x16x32_bf16 a[20:23],a[232:235],v[136:139],a[20:23]
v_exp_f32 v189,v189
v_mfma_f32_16x16x32_bf16 a[24:27],a[240:243],v[136:139],a[24:27]
s_waitcnt lgkmcnt(2)
v_exp_f32 v190,v190
v_mfma_f32_16x16x32_bf16 a[24:27],a[244:247],v[140:143],a[24:27]
v_cvt_pk_bf16_f32 v173,v186,v187
v_mfma_f32_16x16x32_bf16 a[28:31],a[252:255],v[140:143],a[28:31]
s_waitcnt lgkmcnt(0)
v_exp_f32 v191,v191
v_mfma_f32_16x16x32_bf16 a[28:31],a[248:251],v[136:139],a[28:31]
v_cvt_pk_bf16_f32 v174,v188,v189
v_cvt_pk_bf16_f32 v175,v190,v191
s_cmp_lg_u32 s53,0
v_smfmac_f32_16x16x64_bf16 v[48:51],v[28:31],v[136:143],v27
s_cbranch_scc1 L_att1v3_%=
s_waitcnt lgkmcnt(0)
s_nop 7
s_branch L_att1x_%=
.p2align 5
L_att1v0_%=:
v_mfma_f32_16x16x32_bf16 v[232:235],a[128:131],v[148:151],v[44:47]
s_waitcnt vmcnt(8) lgkmcnt(0)
v_exp_f32 v200,v200
v_mfma_f32_16x16x32_bf16 v[232:235],a[132:135],v[180:183],v[232:235]
s_barrier
s_add_u32 m0,s49,s51
ds_read_b128 a[128:131],v1
v_mfma_f32_16x16x32_bf16 v[232:235],a[136:139],v[212:215],v[232:235]
s_mov_b32 s90,L_out_end_%=-L_pc_%=+128
v_exp_f32 v201,v201
v_mfma_f32_16x16x32_bf16 v[232:235],a[140:143],v[244:247],v[232:235]
s_lshr_b32 s45,s45,1
ds_read_b128 a[132:135],v1 offset:256
v_cvt_pk_bf16_f32 v200,v200,v201
v_mfma_f32_16x16x32_bf16 v[236:239],a[156:159],v[244:247],v[44:47]
buffer_load_dwordx4 v4,s[20:23],s47 offen nt lds
v_mfma_f32_16x16x32_bf16 v[236:239],a[152:155],v[212:215],v[236:239]
ds_read_b128 a[136:139],v1 offset:512
v_add_u32 v2,s48,v2
v_mfma_f32_16x16x32_bf16 v[236:239],a[148:151],v[180:183],v[236:239]
v_exp_f32 v202,v202
v_mfma_f32_16x16x32_bf16 v[236:239],a[144:147],v[148:151],v[236:239]
s_add_u32 s62,s50,32768
ds_read_b128 a[140:143],v1 offset:768
v_add_u32 v3,s48,v3
v_mfma_f32_16x16x32_bf16 v[248:251],a[160:163],v[148:151],v[44:47]
s_and_b32 s62,s62,131071
v_exp_f32 v203,v203
v_mfma_f32_16x16x32_bf16 v[248:251],a[164:167],v[180:183],v[248:251]
s_addk_i32 s54,16384
ds_read_b128 a[156:159],v1 offset:4864
v_cvt_pk_bf16_f32 v201,v202,v203
v_mfma_f32_16x16x32_bf16 v[248:251],a[168:171],v[212:215],v[248:251]
s_sub_u32 s48,s62,s50
v_exp_f32 v204,v204
v_mfma_f32_16x16x32_bf16 v[248:251],a[172:175],v[244:247],v[248:251]
s_sub_u32 s53,s53,1
v_exp_f32 v205,v205
v_mfma_f32_16x16x32_bf16 v[252:255],a[188:191],v[244:247],v[44:47]
s_and_b32 s46,s54,0xfffc0000
ds_read_b128 a[152:155],v1 offset:4608
v_cvt_pk_bf16_f32 v202,v204,v205
v_mfma_f32_16x16x32_bf16 v[252:255],a[184:187],v[212:215],v[252:255]
s_mov_b32 s50,s62
v_exp_f32 v206,v206
v_mfma_f32_16x16x32_bf16 v[252:255],a[180:183],v[180:183],v[252:255]
s_xor_b32 s49,s50,65536
v_exp_f32 v207,v207
v_mfma_f32_16x16x32_bf16 v[252:255],a[176:179],v[148:151],v[252:255]
ds_read_b128 a[148:151],v1 offset:4352
v_cvt_pk_bf16_f32 v203,v206,v207
v_mfma_f32_16x16x32_bf16 a[32:35],a[192:195],v[168:171],a[32:35]
v_exp_f32 v216,v216
v_mfma_f32_16x16x32_bf16 a[32:35],a[196:199],v[172:175],a[32:35]
ds_read_b128 a[144:147],v1 offset:4096
v_mfma_f32_16x16x32_bf16 a[36:39],a[204:207],v[172:175],a[36:39]
buffer_load_dwordx4 v4,s[20:23],s47 offen offset:1024 nt lds
v_mfma_f32_16x16x32_bf16 a[36:39],a[200:203],v[168:171],a[36:39]
ds_read_b128 a[160:163],v1 offset:8192
v_mfma_f32_16x16x32_bf16 a[40:43],a[208:211],v[168:171],a[40:43]
v_exp_f32 v217,v217
v_mfma_f32_16x16x32_bf16 a[40:43],a[212:215],v[172:175],a[40:43]
ds_read_b128 a[164:167],v1 offset:8448
v_cvt_pk_bf16_f32 v204,v216,v217
v_mfma_f32_16x16x32_bf16 a[44:47],a[220:223],v[172:175],a[44:47]
v_exp_f32 v218,v218
v_mfma_f32_16x16x32_bf16 a[44:47],a[216:219],v[168:171],a[44:47]
ds_read_b128 a[168:171],v1 offset:8704
v_mfma_f32_16x16x32_bf16 a[48:51],a[224:227],v[168:171],a[48:51]
v_exp_f32 v219,v219
v_mfma_f32_16x16x32_bf16 a[48:51],a[228:231],v[172:175],a[48:51]
ds_read_b128 a[172:175],v1 offset:8960
v_cvt_pk_bf16_f32 v205,v218,v219
v_mfma_f32_16x16x32_bf16 a[52:55],a[236:239],v[172:175],a[52:55]
v_exp_f32 v220,v220
v_mfma_f32_16x16x32_bf16 a[52:55],a[232:235],v[168:171],a[52:55]
ds_read_b128 a[188:191],v1 offset:13056
v_mfma_f32_16x16x32_bf16 a[56:59],a[240:243],v[168:171],a[56:59]
v_exp_f32 v221,v221
v_mfma_f32_16x16x32_bf16 a[56:59],a[244:247],v[172:175],a[56:59]
ds_read_b128 a[184:187],v1 offset:12800
v_cvt_pk_bf16_f32 v206,v220,v221
v_mfma_f32_16x16x32_bf16 a[60:63],a[252:255],v[172:175],a[60:63]
v_exp_f32 v222,v222
)A"
R"A(v_mfma_f32_16x16x32_bf16 a[60:63],a[248:251],v[168:171],a[60:63]
ds_read_b128 a[180:183],v1 offset:12544
v_smfmac_f32_16x16x64_bf16 v[52:55],v[28:31],v[168:175],v27
buffer_load_dwordx4 v5,s[20:23],s47 offen offset:2048 nt lds
ds_read_b128 a[176:179],v1 offset:12288
v_add_u32 v1,s48,v1
v_exp_f32 v223,v223
s_nop 0
v_cvt_pk_bf16_f32 v207,v222,v223
v_exp_f32 v232,v232
v_exp_f32 v233,v233
v_exp_f32 v234,v234
v_exp_f32 v235,v235
v_exp_f32 v236,v236
v_exp_f32 v237,v237
v_exp_f32 v238,v238
v_exp_f32 v239,v239
v_exp_f32 v248,v248
buffer_load_dwordx4 v5,s[20:23],s47 offen offset:3072 nt lds
v_exp_f32 v249,v249
v_exp_f32 v250,v250
v_mfma_f32_16x16x32_bf16 a[64:67],a[192:195],v[200:203],a[64:67]
v_exp_f32 v251,v251
v_mfma_f32_16x16x32_bf16 a[64:67],a[196:199],v[204:207],a[64:67]
v_exp_f32 v252,v252
v_mfma_f32_16x16x32_bf16 a[68:71],a[204:207],v[204:207],a[68:71]
v_exp_f32 v253,v253
v_mfma_f32_16x16x32_bf16 a[68:71],a[200:203],v[200:203],a[68:71]
v_exp_f32 v254,v254
v_mfma_f32_16x16x32_bf16 a[72:75],a[208:211],v[200:203],a[72:75]
v_exp_f32 v255,v255
v_mfma_f32_16x16x32_bf16 a[72:75],a[212:215],v[204:207],a[72:75]
v_mfma_f32_16x16x32_bf16 a[76:79],a[220:223],v[204:207],a[76:79]
v_mfma_f32_16x16x32_bf16 a[76:79],a[216:219],v[200:203],a[76:79]
v_mfma_f32_16x16x32_bf16 a[80:83],a[224:227],v[200:203],a[80:83]
s_addk_i32 m0,16384
v_mfma_f32_16x16x32_bf16 a[80:83],a[228:231],v[204:207],a[80:83]
v_mfma_f32_16x16x32_bf16 a[84:87],a[236:239],v[204:207],a[84:87]
v_mfma_f32_16x16x32_bf16 a[84:87],a[232:235],v[200:203],a[84:87]
buffer_load_dwordx4 v4,s[24:27],s47 offen nt lds
v_mfma_f32_16x16x32_bf16 a[88:91],a[240:243],v[200:203],a[88:91]
v_mfma_f32_16x16x32_bf16 a[88:91],a[244:247],v[204:207],a[88:91]
v_mfma_f32_16x16x32_bf16 a[92:95],a[252:255],v[204:207],a[92:95]
v_cvt_pk_bf16_f32 v232,v232,v233
v_cvt_pk_bf16_f32 v233,v234,v235
v_mfma_f32_16x16x32_bf16 a[92:95],a[248:251],v[200:203],a[92:95]
v_cvt_pk_bf16_f32 v234,v236,v237
v_cvt_pk_bf16_f32 v235,v238,v239
v_smfmac_f32_16x16x64_bf16 v[56:59],v[28:31],v[200:207],v27
s_waitcnt lgkmcnt(0)
v_cvt_pk_bf16_f32 v236,v248,v249
v_mfma_f32_16x16x32_bf16 v[168:171],a[128:131],v[132:135],v[36:39]
v_cvt_pk_bf16_f32 v237,v250,v251
v_cvt_pk_bf16_f32 v238,v252,v253
v_mfma_f32_16x16x32_bf16 v[168:171],a[132:135],v[164:167],v[168:171]
v_cvt_pk_bf16_f32 v239,v254,v255
v_mfma_f32_16x16x32_bf16 v[168:171],a[136:139],v[196:199],v[168:171]
v_mfma_f32_16x16x32_bf16 v[168:171],a[140:143],v[228:231],v[168:171]
v_mfma_f32_16x16x32_bf16 v[172:175],a[156:159],v[228:231],v[36:39]
v_mfma_f32_16x16x32_bf16 v[172:175],a[152:155],v[196:199],v[172:175]
v_mfma_f32_16x16x32_bf16 v[172:175],a[148:151],v[164:167],v[172:175]
v_mfma_f32_16x16x32_bf16 v[172:175],a[144:147],v[132:135],v[172:175]
v_mfma_f32_16x16x32_bf16 v[184:187],a[160:163],v[132:135],v[36:39]
buffer_load_dwordx4 v5,s[24:27],s47 offen offset:1024 nt lds
v_mfma_f32_16x16x32_bf16 v[184:187],a[164:167],v[164:167],v[184:187]
v_mfma_f32_16x16x32_bf16 v[184:187],a[168:171],v[196:199],v[184:187]
v_mfma_f32_16x16x32_bf16 v[184:187],a[172:175],v[228:231],v[184:187]
v_exp_f32 v168,v168
v_mfma_f32_16x16x32_bf16 v[188:191],a[188:191],v[228:231],v[36:39]
v_exp_f32 v169,v169
v_mfma_f32_16x16x32_bf16 v[188:191],a[184:187],v[196:199],v[188:191]
v_exp_f32 v170,v170
v_mfma_f32_16x16x32_bf16 v[188:191],a[180:183],v[164:167],v[188:191]
v_exp_f32 v171,v171
v_mfma_f32_16x16x32_bf16 v[188:191],a[176:179],v[132:135],v[188:191]
v_exp_f32 v172,v172
v_mfma_f32_16x16x32_bf16 a[96:99],a[192:195],v[232:235],a[96:99]
ds_read_b64_tr_b16 a[192:193],v2
v_cvt_pk_bf16_f32 v168,v168,v169
v_mfma_f32_16x16x32_bf16 a[96:99],a[196:199],v[236:239],a[96:99]
ds_read_b64_tr_b16 a[194:195],v2 offset:4096
v_cvt_pk_bf16_f32 v169,v170,v171
v_mfma_f32_16x16x32_bf16 a[100:103],a[204:207],v[236:239],a[100:103]
v_exp_f32 v173,v173
v_mfma_f32_16x16x32_bf16 a[100:103],a[200:203],v[232:235],a[100:103]
ds_read_b64_tr_b16 a[196:197],v2 offset:8192
v_cvt_pk_bf16_f32 v170,v172,v173
v_mfma_f32_16x16x32_bf16 a[104:107],a[208:211],v[232:235],a[104:107]
v_exp_f32 v174,v174
v_mfma_f32_16x16x32_bf16 a[104:107],a[212:215],v[236:239],a[104:107]
v_exp_f32 v175,v175
v_mfma_f32_16x16x32_bf16 a[108:111],a[220:223],v[236:239],a[108:111]
buffer_load_dwordx4 v4,s[24:27],s47 offen offset:2048 nt lds
v_mfma_f32_16x16x32_bf16 a[108:111],a[216:219],v[232:235],a[108:111]
ds_read_b64_tr_b16 a[198:199],v2 offset:12288
v_cvt_pk_bf16_f32 v171,v174,v175
v_mfma_f32_16x16x32_bf16 a[112:115],a[224:227],v[232:235],a[112:115]
v_exp_f32 v184,v184
v_mfma_f32_16x16x32_bf16 a[112:115],a[228:231],v[236:239],a[112:115]
v_exp_f32 v185,v185
v_mfma_f32_16x16x32_bf16 a[116:119],a[236:239],v[236:239],a[116:119]
ds_read_b64_tr_b16 a[204:205],v3 offset:8192
v_mfma_f32_16x16x32_bf16 a[116:119],a[232:235],v[232:235],a[116:119]
ds_read_b64_tr_b16 a[206:207],v3 offset:12288
v_cvt_pk_bf16_f32 v172,v184,v185
v_mfma_f32_16x16x32_bf16 a[120:123],a[240:243],v[232:235],a[120:123]
ds_read_b64_tr_b16 a[200:201],v3
v_mfma_f32_16x16x32_bf16 a[120:123],a[244:247],v[236:239],a[120:123]
ds_read_b64_tr_b16 a[202:203],v3 offset:4096
v_mfma_f32_16x16x32_bf16 a[124:127],a[252:255],v[236:239],a[124:127]
ds_read_b64_tr_b16 a[208:209],v2 offset:256
v_mfma_f32_16x16x32_bf16 a[124:127],a[248:251],v[232:235],a[124:127]
ds_read_b64_tr_b16 a[210:211],v2 offset:4352
v_smfmac_f32_16x16x64_bf16 v[60:63],v[28:31],v[232:239],v27
ds_read_b64_tr_b16 a[212:213],v2 offset:8448
v_mfma_f32_16x16x32_bf16 v[200:203],a[128:131],v[144:147],v[40:43]
ds_read_b64_tr_b16 a[214:215],v2 offset:12544
v_mfma_f32_16x16x32_bf16 v[200:203],a[132:135],v[176:179],v[200:203]
ds_read_b64_tr_b16 a[220:221],v3 offset:8448
v_mfma_f32_16x16x32_bf16 v[200:203],a[136:139],v[208:211],v[200:203]
ds_read_b64_tr_b16 a[222:223],v3 offset:12544
v_mfma_f32_16x16x32_bf16 v[200:203],a[140:143],v[240:243],v[200:203]
buffer_load_dwordx4 v5,s[24:27],s47 offen offset:3072 nt lds
v_mfma_f32_16x16x32_bf16 v[204:207],a[156:159],v[240:243],v[40:43]
s_or_b32 s47,s47,0x18000
s_addk_i32 s47,4096
ds_read_b64_tr_b16 a[216:217],v3 offset:256
v_mfma_f32_16x16x32_bf16 v[204:207],a[152:155],v[208:211],v[204:207]
s_and_b32 s47,s47,0x27000
ds_read_b64_tr_b16 a[218:219],v3 offset:4352
v_mfma_f32_16x16x32_bf16 v[204:207],a[148:151],v[176:179],v[204:207]
s_or_b32 s47,s47,s46
ds_read_b64_tr_b16 a[224:225],v2 offset:512
v_mfma_f32_16x16x32_bf16 v[204:207],a[144:147],v[144:147],v[204:207]
ds_read_b64_tr_b16 a[226:227],v2 offset:4608
v_mfma_f32_16x16x32_bf16 v[216:219],a[160:163],v[144:147],v[40:43]
ds_read_b64_tr_b16 a[228:229],v2 offset:8704
v_mfma_f32_16x16x32_bf16 v[216:219],a[164:167],v[176:179],v[216:219]
ds_read_b64_tr_b16 a[230:231],v2 offset:12800
v_mfma_f32_16x16x32_bf16 v[216:219],a[168:171],v[208:211],v[216:219]
ds_read_b64_tr_b16 a[236:237],v3 offset:8704
v_mfma_f32_16x16x32_bf16 v[216:219],a[172:175],v[240:243],v[216:219]
ds_read_b64_tr_b16 a[238:239],v3 offset:12800
v_mfma_f32_16x16x32_bf16 v[220:223],a[188:191],v[240:243],v[40:43]
ds_read_b64_tr_b16 a[232:233],v3 offset:512
v_mfma_f32_16x16x32_bf16 v[220:223],a[184:187],v[208:211],v[220:223]
ds_read_b64_tr_b16 a[234:235],v3 offset:4608
v_mfma_f32_16x16x32_bf16 v[220:223],a[180:183],v[176:179],v[220:223]
ds_read_b64_tr_b16 a[240:241],v2 offset:768
v_mfma_f32_16x16x32_bf16 v[220:223],a[176:179],v[144:147],v[220:223]
buffer_load_dword v26,v24,s[88:91],s93 offen
ds_read_b64_tr_b16 a[242:243],v2 offset:4864
ds_read_b64_tr_b16 a[244:245],v2 offset:8960
ds_read_b64_tr_b16 a[246:247],v2 offset:13056
ds_read_b64_tr_b16 a[252:253],v3 offset:8960
ds_read_b64_tr_b16 a[254:255],v3 offset:13056
ds_read_b64_tr_b16 a[248:249],v3 offset:768
ds_read_b64_tr_b16 a[250:251],v3 offset:4864
s_bitcmp1_b32 s45,0
s_cbranch_scc0 L_t31v01_%=
s_mov_b32 m0,s80
s_addk_i32 s80,4096
buffer_load_dwordx4 v25,s[24:27],s83 offen nt lds
s_addk_i32 s83,256
L_t31v01_%=:
v_exp_f32 v186,v186
v_exp_f32 v187,v187
v_exp_f32 v188,v188
v_exp_f32 v189,v189
v_exp_f32 v190,v190
v_cvt_pk_bf16_f32 v173,v186,v187
v_exp_f32 v191,v191
v_cvt_pk_bf16_f32 v174,v188,v189
v_cvt_pk_bf16_f32 v175,v190,v191
s_cmp_lg_u32 s53,0
s_cbranch_scc1 L_att1v0_%=
s_waitcnt lgkmcnt(0)
s_nop 7
s_branch L_att1x_%=
.p2align 5
L_att1v1_%=:
v_mfma_f32_16x16x32_bf16 v[232:235],a[128:131],v[148:151],v[44:47]
s_waitcnt vmcnt(8) lgkmcnt(0)
v_exp_f32 v200,v200
v_mfma_f32_16x16x32_bf16 v[232:235],a[132:135],v[180:183],v[232:235]
s_barrier
s_add_u32 m0,s49,s51
ds_read_b128 a[128:131],v1
v_mfma_f32_16x16x32_bf16 v[232:235],a[136:139],v[212:215],v[232:235]
s_mov_b32 s90,L_out_end_%=-L_pc_%=+128
v_exp_f32 v201,v201
v_mfma_f32_16x16x32_bf16 v[232:235],a[140:143],v[244:247],v[232:235]
s_lshr_b32 s45,s45,1
ds_read_b128 a[132:135],v1 offset:256
v_cvt_pk_bf16_f32 v200,v200,v201
v_mfma_f32_16x16x32_bf16 v[236:239],a[156:159],v[244:247],v[44:47]
buffer_load_dwordx4 v4,s[20:23],s47 offen nt lds
v_mfma_f32_16x16x32_bf16 v[236:239],a[152:155],v[212:215],v[236:239]
ds_read_b128 a[136:139],v1 offset:512
v_add_u32 v2,s48,v2
v_mfma_f32_16x16x32_bf16 v[236:239],a[148:151],v[180:183],v[236:239]
v_exp_f32 v202,v202
v_mfma_f32_16x16x32_bf16 v[236:239],a[144:147],v[148:151],v[236:239]
s_add_u32 s62,s50,32768
ds_read_b128 a[140:143],v1 offset:768
v_add_u32 v3,s48,v3
v_mfma_f32_16x16x32_bf16 v[248:251],a[160:163],v[148:151],v[44:47]
s_and_b32 s62,s62,131071
v_exp_f32 v203,v203
v_mfma_f32_16x16x32_bf16 v[248:251],a[164:167],v[180:183],v[248:251]
s_addk_i32 s54,16384
ds_read_b128 a[156:159],v1 offset:4864
v_cvt_pk_bf16_f32 v201,v202,v203
v_mfma_f32_16x16x32_bf16 v[248:251],a[168:171],v[212:215],v[248:251]
s_sub_u32 s48,s62,s50
v_exp_f32 v204,v204
v_mfma_f32_16x16x32_bf16 v[248:251],a[172:175],v[244:247],v[248:251]
s_sub_u32 s53,s53,1
v_exp_f32 v205,v205
v_mfma_f32_16x16x32_bf16 v[252:255],a[188:191],v[244:247],v[44:47]
s_and_b32 s46,s54,0xfffc0000
ds_read_b128 a[152:155],v1 offset:4608
v_cvt_pk_bf16_f32 v202,v204,v205
v_mfma_f32_16x16x32_bf16 v[252:255],a[184:187],v[212:215],v[252:255]
s_mov_b32 s50,s62
v_exp_f32 v206,v206
v_mfma_f32_16x16x32_bf16 v[252:255],a[180:183],v[180:183],v[252:255]
s_xor_b32 s49,s50,65536
v_exp_f32 v207,v207
v_mfma_f32_16x16x32_bf16 v[252:255],a[176:179],v[148:151],v[252:255]
ds_read_b128 a[148:151],v1 offset:4352
v_cvt_pk_bf16_f32 v203,v206,v207
v_exp_f32 v216,v216
ds_read_b128 a[144:147],v1 offset:4096
buffer_load_dwordx4 v4,s[20:23],s47 offen offset:1024 nt lds
ds_read_b128 a[160:163],v1 offset:8192
v_exp_f32 v217,v217
ds_read_b128 a[164:167],v1 offset:8448
v_cvt_pk_bf16_f32 v204,v216,v217
v_exp_f32 v218,v218
ds_read_b128 a[168:171],v1 offset:8704
v_exp_f32 v219,v219
ds_read_b128 a[172:175],v1 offset:8960
v_cvt_pk_bf16_f32 v205,v218,v219
v_exp_f32 v220,v220
ds_read_b128 a[188:191],v1 offset:13056
v_exp_f32 v221,v221
ds_read_b128 a[184:187],v1 offset:12800
v_cvt_pk_bf16_f32 v206,v220,v221
v_exp_f32 v222,v222
ds_read_b128 a[180:183],v1 offset:12544
s_waitcnt lgkmcnt(11)
v_mfma_f32_16x16x32_bf16 v[136:139],a[128:131],v[128:131],v[32:35]
buffer_load_dwordx4 v5,s[20:23],s47 offen offset:2048 nt lds
v_mfma_f32_16x16x32_bf16 v[136:139],a[132:135],v[160:163],v[136:139]
ds_read_b128 a[176:179],v1 offset:12288
v_add_u32 v1,s48,v1
v_mfma_f32_16x16x32_bf16 v[136:139],a[136:139],v[192:195],v[136:139]
v_exp_f32 v223,v223
v_mfma_f32_16x16x32_bf16 v[136:139],a[140:143],v[224:227],v[136:139]
s_waitcnt lgkmcnt(11)
v_cvt_pk_bf16_f32 v207,v222,v223
v_mfma_f32_16x16x32_bf16 v[140:143],a[156:159],v[224:227],v[32:35]
s_waitcnt lgkmcnt(10)
v_exp_f32 v232,v232
v_mfma_f32_16x16x32_bf16 v[140:143],a[152:155],v[192:195],v[140:143]
s_waitcnt lgkmcnt(9)
v_exp_f32 v233,v233
v_mfma_f32_16x16x32_bf16 v[140:143],a[148:151],v[160:163],v[140:143]
s_waitcnt lgkmcnt(8)
v_exp_f32 v234,v234
v_mfma_f32_16x16x32_bf16 v[140:143],a[144:147],v[128:131],v[140:143]
s_waitcnt lgkmcnt(1)
v_exp_f32 v235,v235
v_mfma_f32_16x16x32_bf16 v[152:155],a[160:163],v[128:131],v[32:35]
v_exp_f32 v236,v236
v_mfma_f32_16x16x32_bf16 v[152:155],a[164:167],v[160:163],v[152:155]
v_exp_f32 v237,v237
v_mfma_f32_16x16x32_bf16 v[152:155],a[168:171],v[192:195],v[152:155]
v_exp_f32 v238,v238
v_mfma_f32_16x16x32_bf16 v[152:155],a[172:175],v[224:227],v[152:155]
)A"
R"A(v_exp_f32 v239,v239
v_mfma_f32_16x16x32_bf16 v[156:159],a[188:191],v[224:227],v[32:35]
v_exp_f32 v248,v248
v_mfma_f32_16x16x32_bf16 v[156:159],a[184:187],v[192:195],v[156:159]
buffer_load_dwordx4 v5,s[20:23],s47 offen offset:3072 nt lds
v_mfma_f32_16x16x32_bf16 v[156:159],a[180:183],v[160:163],v[156:159]
s_waitcnt lgkmcnt(0)
v_exp_f32 v249,v249
v_mfma_f32_16x16x32_bf16 v[156:159],a[176:179],v[128:131],v[156:159]
v_exp_f32 v250,v250
v_mfma_f32_16x16x32_bf16 a[64:67],a[192:195],v[200:203],a[64:67]
v_exp_f32 v251,v251
v_mfma_f32_16x16x32_bf16 a[64:67],a[196:199],v[204:207],a[64:67]
v_exp_f32 v252,v252
v_mfma_f32_16x16x32_bf16 a[68:71],a[204:207],v[204:207],a[68:71]
v_exp_f32 v253,v253
v_mfma_f32_16x16x32_bf16 a[68:71],a[200:203],v[200:203],a[68:71]
v_exp_f32 v254,v254
v_mfma_f32_16x16x32_bf16 a[72:75],a[208:211],v[200:203],a[72:75]
v_exp_f32 v255,v255
v_mfma_f32_16x16x32_bf16 a[72:75],a[212:215],v[204:207],a[72:75]
v_exp_f32 v136,v136
v_mfma_f32_16x16x32_bf16 a[76:79],a[220:223],v[204:207],a[76:79]
v_exp_f32 v137,v137
v_mfma_f32_16x16x32_bf16 a[76:79],a[216:219],v[200:203],a[76:79]
v_exp_f32 v138,v138
v_mfma_f32_16x16x32_bf16 a[80:83],a[224:227],v[200:203],a[80:83]
s_addk_i32 m0,16384
v_exp_f32 v139,v139
v_mfma_f32_16x16x32_bf16 a[80:83],a[228:231],v[204:207],a[80:83]
v_exp_f32 v140,v140
v_mfma_f32_16x16x32_bf16 a[84:87],a[236:239],v[204:207],a[84:87]
v_exp_f32 v141,v141
v_mfma_f32_16x16x32_bf16 a[84:87],a[232:235],v[200:203],a[84:87]
buffer_load_dwordx4 v4,s[24:27],s47 offen nt lds
v_mfma_f32_16x16x32_bf16 a[88:91],a[240:243],v[200:203],a[88:91]
v_exp_f32 v142,v142
v_mfma_f32_16x16x32_bf16 a[88:91],a[244:247],v[204:207],a[88:91]
v_exp_f32 v143,v143
v_mfma_f32_16x16x32_bf16 a[92:95],a[252:255],v[204:207],a[92:95]
v_cvt_pk_bf16_f32 v232,v232,v233
v_cvt_pk_bf16_f32 v233,v234,v235
v_mfma_f32_16x16x32_bf16 a[92:95],a[248:251],v[200:203],a[92:95]
v_cvt_pk_bf16_f32 v234,v236,v237
v_cvt_pk_bf16_f32 v235,v238,v239
v_smfmac_f32_16x16x64_bf16 v[56:59],v[28:31],v[200:207],v27
v_cvt_pk_bf16_f32 v236,v248,v249
v_cvt_pk_bf16_f32 v237,v250,v251
v_cvt_pk_bf16_f32 v238,v252,v253
v_cvt_pk_bf16_f32 v239,v254,v255
v_exp_f32 v152,v152
v_exp_f32 v153,v153
v_exp_f32 v154,v154
v_exp_f32 v155,v155
v_exp_f32 v156,v156
v_exp_f32 v157,v157
buffer_load_dwordx4 v5,s[24:27],s47 offen offset:1024 nt lds
v_exp_f32 v158,v158
v_exp_f32 v159,v159
v_mfma_f32_16x16x32_bf16 a[96:99],a[192:195],v[232:235],a[96:99]
ds_read_b64_tr_b16 a[192:193],v2
v_mfma_f32_16x16x32_bf16 a[96:99],a[196:199],v[236:239],a[96:99]
ds_read_b64_tr_b16 a[194:195],v2 offset:4096
v_mfma_f32_16x16x32_bf16 a[100:103],a[204:207],v[236:239],a[100:103]
v_mfma_f32_16x16x32_bf16 a[100:103],a[200:203],v[232:235],a[100:103]
ds_read_b64_tr_b16 a[196:197],v2 offset:8192
v_mfma_f32_16x16x32_bf16 a[104:107],a[208:211],v[232:235],a[104:107]
v_mfma_f32_16x16x32_bf16 a[104:107],a[212:215],v[236:239],a[104:107]
v_mfma_f32_16x16x32_bf16 a[108:111],a[220:223],v[236:239],a[108:111]
buffer_load_dwordx4 v4,s[24:27],s47 offen offset:2048 nt lds
v_mfma_f32_16x16x32_bf16 a[108:111],a[216:219],v[232:235],a[108:111]
ds_read_b64_tr_b16 a[198:199],v2 offset:12288
v_mfma_f32_16x16x32_bf16 a[112:115],a[224:227],v[232:235],a[112:115]
v_mfma_f32_16x16x32_bf16 a[112:115],a[228:231],v[236:239],a[112:115]
v_mfma_f32_16x16x32_bf16 a[116:119],a[236:239],v[236:239],a[116:119]
ds_read_b64_tr_b16 a[204:205],v3 offset:8192
v_cvt_pk_bf16_f32 v136,v136,v137
v_mfma_f32_16x16x32_bf16 a[116:119],a[232:235],v[232:235],a[116:119]
ds_read_b64_tr_b16 a[206:207],v3 offset:12288
v_mfma_f32_16x16x32_bf16 a[120:123],a[240:243],v[232:235],a[120:123]
ds_read_b64_tr_b16 a[200:201],v3
v_mfma_f32_16x16x32_bf16 a[120:123],a[244:247],v[236:239],a[120:123]
ds_read_b64_tr_b16 a[202:203],v3 offset:4096
v_cvt_pk_bf16_f32 v137,v138,v139
v_mfma_f32_16x16x32_bf16 a[124:127],a[252:255],v[236:239],a[124:127]
ds_read_b64_tr_b16 a[208:209],v2 offset:256
v_mfma_f32_16x16x32_bf16 a[124:127],a[248:251],v[232:235],a[124:127]
ds_read_b64_tr_b16 a[210:211],v2 offset:4352
v_smfmac_f32_16x16x64_bf16 v[60:63],v[28:31],v[232:239],v27
ds_read_b64_tr_b16 a[212:213],v2 offset:8448
v_mfma_f32_16x16x32_bf16 v[200:203],a[128:131],v[144:147],v[40:43]
ds_read_b64_tr_b16 a[214:215],v2 offset:12544
v_cvt_pk_bf16_f32 v138,v140,v141
v_mfma_f32_16x16x32_bf16 v[200:203],a[132:135],v[176:179],v[200:203]
ds_read_b64_tr_b16 a[220:221],v3 offset:8448
v_mfma_f32_16x16x32_bf16 v[200:203],a[136:139],v[208:211],v[200:203]
ds_read_b64_tr_b16 a[222:223],v3 offset:12544
v_cvt_pk_bf16_f32 v139,v142,v143
v_mfma_f32_16x16x32_bf16 v[200:203],a[140:143],v[240:243],v[200:203]
buffer_load_dwordx4 v5,s[24:27],s47 offen offset:3072 nt lds
v_mfma_f32_16x16x32_bf16 v[204:207],a[156:159],v[240:243],v[40:43]
s_or_b32 s47,s47,0x18000
s_addk_i32 s47,4096
ds_read_b64_tr_b16 a[216:217],v3 offset:256
v_mfma_f32_16x16x32_bf16 v[204:207],a[152:155],v[208:211],v[204:207]
s_and_b32 s47,s47,0x27000
ds_read_b64_tr_b16 a[218:219],v3 offset:4352
v_cvt_pk_bf16_f32 v140,v152,v153
v_mfma_f32_16x16x32_bf16 v[204:207],a[148:151],v[176:179],v[204:207]
s_or_b32 s47,s47,s46
ds_read_b64_tr_b16 a[224:225],v2 offset:512
v_mfma_f32_16x16x32_bf16 v[204:207],a[144:147],v[144:147],v[204:207]
ds_read_b64_tr_b16 a[226:227],v2 offset:4608
v_mfma_f32_16x16x32_bf16 v[216:219],a[160:163],v[144:147],v[40:43]
ds_read_b64_tr_b16 a[228:229],v2 offset:8704
v_cvt_pk_bf16_f32 v141,v154,v155
v_mfma_f32_16x16x32_bf16 v[216:219],a[164:167],v[176:179],v[216:219]
ds_read_b64_tr_b16 a[230:231],v2 offset:12800
v_mfma_f32_16x16x32_bf16 v[216:219],a[168:171],v[208:211],v[216:219]
ds_read_b64_tr_b16 a[236:237],v3 offset:8704
v_mfma_f32_16x16x32_bf16 v[216:219],a[172:175],v[240:243],v[216:219]
ds_read_b64_tr_b16 a[238:239],v3 offset:12800
v_cvt_pk_bf16_f32 v142,v156,v157
v_mfma_f32_16x16x32_bf16 v[220:223],a[188:191],v[240:243],v[40:43]
ds_read_b64_tr_b16 a[232:233],v3 offset:512
v_mfma_f32_16x16x32_bf16 v[220:223],a[184:187],v[208:211],v[220:223]
ds_read_b64_tr_b16 a[234:235],v3 offset:4608
v_mfma_f32_16x16x32_bf16 v[220:223],a[180:183],v[176:179],v[220:223]
ds_read_b64_tr_b16 a[240:241],v2 offset:768
v_cvt_pk_bf16_f32 v143,v158,v159
v_mfma_f32_16x16x32_bf16 v[220:223],a[176:179],v[144:147],v[220:223]
s_waitcnt lgkmcnt(7)
buffer_load_dword v26,v24,s[88:91],s93 offen
v_mfma_f32_16x16x32_bf16 a[0:3],a[192:195],v[136:139],a[0:3]
ds_read_b64_tr_b16 a[242:243],v2 offset:4864
v_mfma_f32_16x16x32_bf16 a[0:3],a[196:199],v[140:143],a[0:3]
ds_read_b64_tr_b16 a[244:245],v2 offset:8960
v_mfma_f32_16x16x32_bf16 a[4:7],a[204:207],v[140:143],a[4:7]
ds_read_b64_tr_b16 a[246:247],v2 offset:13056
v_mfma_f32_16x16x32_bf16 a[4:7],a[200:203],v[136:139],a[4:7]
ds_read_b64_tr_b16 a[252:253],v3 offset:8960
v_mfma_f32_16x16x32_bf16 a[8:11],a[208:211],v[136:139],a[8:11]
ds_read_b64_tr_b16 a[254:255],v3 offset:13056
v_mfma_f32_16x16x32_bf16 a[8:11],a[212:215],v[140:143],a[8:11]
ds_read_b64_tr_b16 a[248:249],v3 offset:768
v_mfma_f32_16x16x32_bf16 a[12:15],a[220:223],v[140:143],a[12:15]
ds_read_b64_tr_b16 a[250:251],v3 offset:4864
v_mfma_f32_16x16x32_bf16 a[12:15],a[216:219],v[136:139],a[12:15]
s_bitcmp1_b32 s45,0
s_cbranch_scc0 L_t31v11_%=
s_mov_b32 m0,s80
s_addk_i32 s80,4096
buffer_load_dwordx4 v25,s[24:27],s83 offen nt lds
s_addk_i32 s83,256
L_t31v11_%=:
v_mfma_f32_16x16x32_bf16 a[16:19],a[224:227],v[136:139],a[16:19]
s_waitcnt lgkmcnt(6)
v_mfma_f32_16x16x32_bf16 a[16:19],a[228:231],v[140:143],a[16:19]
v_mfma_f32_16x16x32_bf16 a[20:23],a[236:239],v[140:143],a[20:23]
v_mfma_f32_16x16x32_bf16 a[20:23],a[232:235],v[136:139],a[20:23]
v_mfma_f32_16x16x32_bf16 a[24:27],a[240:243],v[136:139],a[24:27]
s_waitcnt lgkmcnt(2)
v_mfma_f32_16x16x32_bf16 a[24:27],a[244:247],v[140:143],a[24:27]
v_mfma_f32_16x16x32_bf16 a[28:31],a[252:255],v[140:143],a[28:31]
s_waitcnt lgkmcnt(0)
v_mfma_f32_16x16x32_bf16 a[28:31],a[248:251],v[136:139],a[28:31]
s_cmp_lg_u32 s53,0
v_smfmac_f32_16x16x64_bf16 v[48:51],v[28:31],v[136:143],v27
s_cbranch_scc1 L_att1v1_%=
s_waitcnt lgkmcnt(0)
s_nop 7
s_branch L_att1x_%=
.p2align 5
L_att1v2_%=:
v_mfma_f32_16x16x32_bf16 v[232:235],a[128:131],v[148:151],v[44:47]
s_waitcnt vmcnt(8) lgkmcnt(0)
v_mfma_f32_16x16x32_bf16 v[232:235],a[132:135],v[180:183],v[232:235]
s_barrier
s_add_u32 m0,s49,s51
ds_read_b128 a[128:131],v1
v_mfma_f32_16x16x32_bf16 v[232:235],a[136:139],v[212:215],v[232:235]
s_mov_b32 s90,L_out_end_%=-L_pc_%=+128
v_mfma_f32_16x16x32_bf16 v[232:235],a[140:143],v[244:247],v[232:235]
s_lshr_b32 s45,s45,1
ds_read_b128 a[132:135],v1 offset:256
v_mfma_f32_16x16x32_bf16 v[236:239],a[156:159],v[244:247],v[44:47]
buffer_load_dwordx4 v4,s[20:23],s47 offen nt lds
v_mfma_f32_16x16x32_bf16 v[236:239],a[152:155],v[212:215],v[236:239]
ds_read_b128 a[136:139],v1 offset:512
v_add_u32 v2,s48,v2
v_mfma_f32_16x16x32_bf16 v[236:239],a[148:151],v[180:183],v[236:239]
v_mfma_f32_16x16x32_bf16 v[236:239],a[144:147],v[148:151],v[236:239]
s_add_u32 s62,s50,32768
ds_read_b128 a[140:143],v1 offset:768
v_add_u32 v3,s48,v3
v_mfma_f32_16x16x32_bf16 v[248:251],a[160:163],v[148:151],v[44:47]
s_and_b32 s62,s62,131071
v_mfma_f32_16x16x32_bf16 v[248:251],a[164:167],v[180:183],v[248:251]
s_addk_i32 s54,16384
ds_read_b128 a[156:159],v1 offset:4864
v_mfma_f32_16x16x32_bf16 v[248:251],a[168:171],v[212:215],v[248:251]
s_sub_u32 s48,s62,s50
v_mfma_f32_16x16x32_bf16 v[248:251],a[172:175],v[244:247],v[248:251]
s_sub_u32 s53,s53,1
v_mfma_f32_16x16x32_bf16 v[252:255],a[188:191],v[244:247],v[44:47]
s_and_b32 s46,s54,0xfffc0000
ds_read_b128 a[152:155],v1 offset:4608
v_mfma_f32_16x16x32_bf16 v[252:255],a[184:187],v[212:215],v[252:255]
s_mov_b32 s50,s62
v_mfma_f32_16x16x32_bf16 v[252:255],a[180:183],v[180:183],v[252:255]
s_xor_b32 s49,s50,65536
v_mfma_f32_16x16x32_bf16 v[252:255],a[176:179],v[148:151],v[252:255]
ds_read_b128 a[148:151],v1 offset:4352
v_mfma_f32_16x16x32_bf16 a[32:35],a[192:195],v[168:171],a[32:35]
v_mfma_f32_16x16x32_bf16 a[32:35],a[196:199],v[172:175],a[32:35]
ds_read_b128 a[144:147],v1 offset:4096
v_mfma_f32_16x16x32_bf16 a[36:39],a[204:207],v[172:175],a[36:39]
buffer_load_dwordx4 v4,s[20:23],s47 offen offset:1024 nt lds
v_mfma_f32_16x16x32_bf16 a[36:39],a[200:203],v[168:171],a[36:39]
ds_read_b128 a[160:163],v1 offset:8192
v_mfma_f32_16x16x32_bf16 a[40:43],a[208:211],v[168:171],a[40:43]
v_mfma_f32_16x16x32_bf16 a[40:43],a[212:215],v[172:175],a[40:43]
ds_read_b128 a[164:167],v1 offset:8448
v_mfma_f32_16x16x32_bf16 a[44:47],a[220:223],v[172:175],a[44:47]
v_mfma_f32_16x16x32_bf16 a[44:47],a[216:219],v[168:171],a[44:47]
ds_read_b128 a[168:171],v1 offset:8704
v_mfma_f32_16x16x32_bf16 a[48:51],a[224:227],v[168:171],a[48:51]
v_mfma_f32_16x16x32_bf16 a[48:51],a[228:231],v[172:175],a[48:51]
ds_read_b128 a[172:175],v1 offset:8960
v_mfma_f32_16x16x32_bf16 a[52:55],a[236:239],v[172:175],a[52:55]
v_mfma_f32_16x16x32_bf16 a[52:55],a[232:235],v[168:171],a[52:55]
ds_read_b128 a[188:191],v1 offset:13056
v_mfma_f32_16x16x32_bf16 a[56:59],a[240:243],v[168:171],a[56:59]
v_mfma_f32_16x16x32_bf16 a[56:59],a[244:247],v[172:175],a[56:59]
ds_read_b128 a[184:187],v1 offset:12800
v_mfma_f32_16x16x32_bf16 a[60:63],a[252:255],v[172:175],a[60:63]
v_mfma_f32_16x16x32_bf16 a[60:63],a[248:251],v[168:171],a[60:63]
ds_read_b128 a[180:183],v1 offset:12544
v_smfmac_f32_16x16x64_bf16 v[52:55],v[28:31],v[168:175],v27
s_waitcnt lgkmcnt(4)
v_mfma_f32_16x16x32_bf16 v[136:139],a[128:131],v[128:131],v[32:35]
buffer_load_dwordx4 v5,s[20:23],s47 offen offset:2048 nt lds
v_mfma_f32_16x16x32_bf16 v[136:139],a[132:135],v[160:163],v[136:139]
ds_read_b128 a[176:179],v1 offset:12288
v_add_u32 v1,s48,v1
v_mfma_f32_16x16x32_bf16 v[136:139],a[136:139],v[192:195],v[136:139]
v_mfma_f32_16x16x32_bf16 v[136:139],a[140:143],v[224:227],v[136:139]
v_mfma_f32_16x16x32_bf16 v[140:143],a[156:159],v[224:227],v[32:35]
v_exp_f32 v232,v232
v_mfma_f32_16x16x32_bf16 v[140:143],a[152:155],v[192:195],v[140:143]
v_exp_f32 v233,v233
v_mfma_f32_16x16x32_bf16 v[140:143],a[148:151],v[160:163],v[140:143]
v_exp_f32 v234,v234
v_mfma_f32_16x16x32_bf16 v[140:143],a[144:147],v[128:131],v[140:143]
v_exp_f32 v235,v235
v_mfma_f32_16x16x32_bf16 v[152:155],a[160:163],v[128:131],v[32:35]
)A"
R"A(v_exp_f32 v236,v236
v_mfma_f32_16x16x32_bf16 v[152:155],a[164:167],v[160:163],v[152:155]
v_exp_f32 v237,v237
v_mfma_f32_16x16x32_bf16 v[152:155],a[168:171],v[192:195],v[152:155]
s_waitcnt lgkmcnt(0)
v_exp_f32 v238,v238
v_mfma_f32_16x16x32_bf16 v[152:155],a[172:175],v[224:227],v[152:155]
v_exp_f32 v239,v239
v_mfma_f32_16x16x32_bf16 v[156:159],a[188:191],v[224:227],v[32:35]
v_exp_f32 v248,v248
v_mfma_f32_16x16x32_bf16 v[156:159],a[184:187],v[192:195],v[156:159]
buffer_load_dwordx4 v5,s[20:23],s47 offen offset:3072 nt lds
v_mfma_f32_16x16x32_bf16 v[156:159],a[180:183],v[160:163],v[156:159]
v_exp_f32 v249,v249
v_mfma_f32_16x16x32_bf16 v[156:159],a[176:179],v[128:131],v[156:159]
v_exp_f32 v250,v250
v_exp_f32 v251,v251
v_exp_f32 v252,v252
v_exp_f32 v253,v253
v_exp_f32 v254,v254
v_exp_f32 v255,v255
v_exp_f32 v136,v136
v_exp_f32 v137,v137
v_exp_f32 v138,v138
s_addk_i32 m0,16384
v_exp_f32 v139,v139
v_exp_f32 v140,v140
v_exp_f32 v141,v141
buffer_load_dwordx4 v4,s[24:27],s47 offen nt lds
v_exp_f32 v142,v142
v_exp_f32 v143,v143
v_cvt_pk_bf16_f32 v232,v232,v233
v_cvt_pk_bf16_f32 v233,v234,v235
v_cvt_pk_bf16_f32 v234,v236,v237
v_cvt_pk_bf16_f32 v235,v238,v239
v_cvt_pk_bf16_f32 v236,v248,v249
v_mfma_f32_16x16x32_bf16 v[168:171],a[128:131],v[132:135],v[36:39]
v_cvt_pk_bf16_f32 v237,v250,v251
v_cvt_pk_bf16_f32 v238,v252,v253
v_mfma_f32_16x16x32_bf16 v[168:171],a[132:135],v[164:167],v[168:171]
v_cvt_pk_bf16_f32 v239,v254,v255
v_mfma_f32_16x16x32_bf16 v[168:171],a[136:139],v[196:199],v[168:171]
v_exp_f32 v152,v152
v_mfma_f32_16x16x32_bf16 v[168:171],a[140:143],v[228:231],v[168:171]
v_exp_f32 v153,v153
v_mfma_f32_16x16x32_bf16 v[172:175],a[156:159],v[228:231],v[36:39]
v_exp_f32 v154,v154
v_mfma_f32_16x16x32_bf16 v[172:175],a[152:155],v[196:199],v[172:175]
v_exp_f32 v155,v155
v_mfma_f32_16x16x32_bf16 v[172:175],a[148:151],v[164:167],v[172:175]
v_exp_f32 v156,v156
v_mfma_f32_16x16x32_bf16 v[172:175],a[144:147],v[132:135],v[172:175]
v_exp_f32 v157,v157
v_mfma_f32_16x16x32_bf16 v[184:187],a[160:163],v[132:135],v[36:39]
buffer_load_dwordx4 v5,s[24:27],s47 offen offset:1024 nt lds
v_mfma_f32_16x16x32_bf16 v[184:187],a[164:167],v[164:167],v[184:187]
v_exp_f32 v158,v158
v_mfma_f32_16x16x32_bf16 v[184:187],a[168:171],v[196:199],v[184:187]
v_exp_f32 v159,v159
v_mfma_f32_16x16x32_bf16 v[184:187],a[172:175],v[228:231],v[184:187]
v_exp_f32 v168,v168
v_mfma_f32_16x16x32_bf16 v[188:191],a[188:191],v[228:231],v[36:39]
v_exp_f32 v169,v169
v_mfma_f32_16x16x32_bf16 v[188:191],a[184:187],v[196:199],v[188:191]
v_exp_f32 v170,v170
v_mfma_f32_16x16x32_bf16 v[188:191],a[180:183],v[164:167],v[188:191]
v_exp_f32 v171,v171
v_mfma_f32_16x16x32_bf16 v[188:191],a[176:179],v[132:135],v[188:191]
v_exp_f32 v172,v172
v_mfma_f32_16x16x32_bf16 a[96:99],a[192:195],v[232:235],a[96:99]
ds_read_b64_tr_b16 a[192:193],v2
v_cvt_pk_bf16_f32 v168,v168,v169
v_mfma_f32_16x16x32_bf16 a[96:99],a[196:199],v[236:239],a[96:99]
ds_read_b64_tr_b16 a[194:195],v2 offset:4096
v_cvt_pk_bf16_f32 v169,v170,v171
v_mfma_f32_16x16x32_bf16 a[100:103],a[204:207],v[236:239],a[100:103]
v_exp_f32 v173,v173
v_mfma_f32_16x16x32_bf16 a[100:103],a[200:203],v[232:235],a[100:103]
ds_read_b64_tr_b16 a[196:197],v2 offset:8192
v_cvt_pk_bf16_f32 v170,v172,v173
v_mfma_f32_16x16x32_bf16 a[104:107],a[208:211],v[232:235],a[104:107]
v_exp_f32 v174,v174
v_mfma_f32_16x16x32_bf16 a[104:107],a[212:215],v[236:239],a[104:107]
v_exp_f32 v175,v175
v_mfma_f32_16x16x32_bf16 a[108:111],a[220:223],v[236:239],a[108:111]
buffer_load_dwordx4 v4,s[24:27],s47 offen offset:2048 nt lds
v_mfma_f32_16x16x32_bf16 a[108:111],a[216:219],v[232:235],a[108:111]
ds_read_b64_tr_b16 a[198:199],v2 offset:12288
v_cvt_pk_bf16_f32 v171,v174,v175
v_mfma_f32_16x16x32_bf16 a[112:115],a[224:227],v[232:235],a[112:115]
v_exp_f32 v184,v184
v_mfma_f32_16x16x32_bf16 a[112:115],a[228:231],v[236:239],a[112:115]
v_exp_f32 v185,v185
v_mfma_f32_16x16x32_bf16 a[116:119],a[236:239],v[236:239],a[116:119]
ds_read_b64_tr_b16 a[204:205],v3 offset:8192
v_cvt_pk_bf16_f32 v136,v136,v137
v_mfma_f32_16x16x32_bf16 a[116:119],a[232:235],v[232:235],a[116:119]
ds_read_b64_tr_b16 a[206:207],v3 offset:12288
v_cvt_pk_bf16_f32 v172,v184,v185
v_mfma_f32_16x16x32_bf16 a[120:123],a[240:243],v[232:235],a[120:123]
ds_read_b64_tr_b16 a[200:201],v3
v_mfma_f32_16x16x32_bf16 a[120:123],a[244:247],v[236:239],a[120:123]
ds_read_b64_tr_b16 a[202:203],v3 offset:4096
v_cvt_pk_bf16_f32 v137,v138,v139
v_mfma_f32_16x16x32_bf16 a[124:127],a[252:255],v[236:239],a[124:127]
ds_read_b64_tr_b16 a[208:209],v2 offset:256
v_mfma_f32_16x16x32_bf16 a[124:127],a[248:251],v[232:235],a[124:127]
ds_read_b64_tr_b16 a[210:211],v2 offset:4352
v_smfmac_f32_16x16x64_bf16 v[60:63],v[28:31],v[232:239],v27
ds_read_b64_tr_b16 a[212:213],v2 offset:8448
ds_read_b64_tr_b16 a[214:215],v2 offset:12544
v_cvt_pk_bf16_f32 v138,v140,v141
ds_read_b64_tr_b16 a[220:221],v3 offset:8448
ds_read_b64_tr_b16 a[222:223],v3 offset:12544
v_cvt_pk_bf16_f32 v139,v142,v143
buffer_load_dwordx4 v5,s[24:27],s47 offen offset:3072 nt lds
s_or_b32 s47,s47,0x18000
s_addk_i32 s47,4096
ds_read_b64_tr_b16 a[216:217],v3 offset:256
s_and_b32 s47,s47,0x27000
ds_read_b64_tr_b16 a[218:219],v3 offset:4352
v_cvt_pk_bf16_f32 v140,v152,v153
s_or_b32 s47,s47,s46
ds_read_b64_tr_b16 a[224:225],v2 offset:512
ds_read_b64_tr_b16 a[226:227],v2 offset:4608
ds_read_b64_tr_b16 a[228:229],v2 offset:8704
v_cvt_pk_bf16_f32 v141,v154,v155
ds_read_b64_tr_b16 a[230:231],v2 offset:12800
ds_read_b64_tr_b16 a[236:237],v3 offset:8704
ds_read_b64_tr_b16 a[238:239],v3 offset:12800
v_cvt_pk_bf16_f32 v142,v156,v157
ds_read_b64_tr_b16 a[232:233],v3 offset:512
ds_read_b64_tr_b16 a[234:235],v3 offset:4608
ds_read_b64_tr_b16 a[240:241],v2 offset:768
v_cvt_pk_bf16_f32 v143,v158,v159
buffer_load_dword v26,v24,s[88:91],s93 offen
s_waitcnt lgkmcnt(15)
v_mfma_f32_16x16x32_bf16 a[0:3],a[192:195],v[136:139],a[0:3]
ds_read_b64_tr_b16 a[242:243],v2 offset:4864
v_mfma_f32_16x16x32_bf16 a[0:3],a[196:199],v[140:143],a[0:3]
ds_read_b64_tr_b16 a[244:245],v2 offset:8960
v_mfma_f32_16x16x32_bf16 a[4:7],a[204:207],v[140:143],a[4:7]
ds_read_b64_tr_b16 a[246:247],v2 offset:13056
v_mfma_f32_16x16x32_bf16 a[4:7],a[200:203],v[136:139],a[4:7]
ds_read_b64_tr_b16 a[252:253],v3 offset:8960
v_mfma_f32_16x16x32_bf16 a[8:11],a[208:211],v[136:139],a[8:11]
s_waitcnt lgkmcnt(15)
ds_read_b64_tr_b16 a[254:255],v3 offset:13056
v_mfma_f32_16x16x32_bf16 a[8:11],a[212:215],v[140:143],a[8:11]
ds_read_b64_tr_b16 a[248:249],v3 offset:768
v_mfma_f32_16x16x32_bf16 a[12:15],a[220:223],v[140:143],a[12:15]
s_waitcnt lgkmcnt(15)
ds_read_b64_tr_b16 a[250:251],v3 offset:4864
v_mfma_f32_16x16x32_bf16 a[12:15],a[216:219],v[136:139],a[12:15]
s_bitcmp1_b32 s45,0
s_cbranch_scc0 L_t31v21_%=
s_mov_b32 m0,s80
s_addk_i32 s80,4096
buffer_load_dwordx4 v25,s[24:27],s83 offen nt lds
s_addk_i32 s83,256
L_t31v21_%=:
s_waitcnt lgkmcnt(7)
v_mfma_f32_16x16x32_bf16 a[16:19],a[224:227],v[136:139],a[16:19]
v_exp_f32 v186,v186
v_mfma_f32_16x16x32_bf16 a[16:19],a[228:231],v[140:143],a[16:19]
v_exp_f32 v187,v187
v_mfma_f32_16x16x32_bf16 a[20:23],a[236:239],v[140:143],a[20:23]
v_exp_f32 v188,v188
v_mfma_f32_16x16x32_bf16 a[20:23],a[232:235],v[136:139],a[20:23]
s_waitcnt lgkmcnt(3)
v_exp_f32 v189,v189
v_mfma_f32_16x16x32_bf16 a[24:27],a[240:243],v[136:139],a[24:27]
v_exp_f32 v190,v190
v_mfma_f32_16x16x32_bf16 a[24:27],a[244:247],v[140:143],a[24:27]
s_waitcnt lgkmcnt(1)
v_cvt_pk_bf16_f32 v173,v186,v187
v_mfma_f32_16x16x32_bf16 a[28:31],a[252:255],v[140:143],a[28:31]
s_waitcnt lgkmcnt(0)
v_exp_f32 v191,v191
v_mfma_f32_16x16x32_bf16 a[28:31],a[248:251],v[136:139],a[28:31]
v_cvt_pk_bf16_f32 v174,v188,v189
v_cvt_pk_bf16_f32 v175,v190,v191
s_cmp_lg_u32 s53,0
v_smfmac_f32_16x16x64_bf16 v[48:51],v[28:31],v[136:143],v27
s_cbranch_scc1 L_att1v2_%=
s_waitcnt lgkmcnt(0)
s_nop 7
s_branch L_att1x_%=
.p2align 5
L_att1_%=:
v_mfma_f32_16x16x32_bf16 v[232:235],a[128:131],v[148:151],v[44:47]
s_waitcnt vmcnt(8) lgkmcnt(0)
v_exp_f32 v200,v200
v_mfma_f32_16x16x32_bf16 v[232:235],a[132:135],v[180:183],v[232:235]
s_barrier
s_add_u32 m0,s49,s51
ds_read_b128 a[128:131],v1
v_mfma_f32_16x16x32_bf16 v[232:235],a[136:139],v[212:215],v[232:235]
s_mov_b32 s90,L_out_end_%=-L_pc_%=+128
v_exp_f32 v201,v201
v_mfma_f32_16x16x32_bf16 v[232:235],a[140:143],v[244:247],v[232:235]
s_lshr_b32 s45,s45,1
ds_read_b128 a[132:135],v1 offset:256
v_cvt_pk_bf16_f32 v200,v200,v201
v_mfma_f32_16x16x32_bf16 v[236:239],a[156:159],v[244:247],v[44:47]
s_sub_u32 s94,s94,3
buffer_load_dwordx4 v4,s[20:23],s47 offen nt lds
v_mfma_f32_16x16x32_bf16 v[236:239],a[152:155],v[212:215],v[236:239]
s_lshr_b32 s60,s94,2
ds_read_b128 a[136:139],v1 offset:512
v_add_u32 v2,s48,v2
v_mfma_f32_16x16x32_bf16 v[236:239],a[148:151],v[180:183],v[236:239]
s_max_i32 s60,s60,124
v_exp_f32 v202,v202
v_mfma_f32_16x16x32_bf16 v[236:239],a[144:147],v[148:151],v[236:239]
s_add_u32 s62,s50,32768
ds_read_b128 a[140:143],v1 offset:768
v_add_u32 v3,s48,v3
v_mfma_f32_16x16x32_bf16 v[248:251],a[160:163],v[148:151],v[44:47]
s_and_b32 s62,s62,131071
v_exp_f32 v203,v203
v_mfma_f32_16x16x32_bf16 v[248:251],a[164:167],v[180:183],v[248:251]
s_addk_i32 s54,16384
ds_read_b128 a[156:159],v1 offset:4864
v_cvt_pk_bf16_f32 v201,v202,v203
v_mfma_f32_16x16x32_bf16 v[248:251],a[168:171],v[212:215],v[248:251]
s_sub_u32 s48,s62,s50
v_exp_f32 v204,v204
v_mfma_f32_16x16x32_bf16 v[248:251],a[172:175],v[244:247],v[248:251]
s_sub_u32 s53,s53,1
v_exp_f32 v205,v205
v_mfma_f32_16x16x32_bf16 v[252:255],a[188:191],v[244:247],v[44:47]
s_and_b32 s46,s54,0xfffc0000
ds_read_b128 a[152:155],v1 offset:4608
v_cvt_pk_bf16_f32 v202,v204,v205
v_mfma_f32_16x16x32_bf16 v[252:255],a[184:187],v[212:215],v[252:255]
s_mov_b32 s50,s62
v_exp_f32 v206,v206
v_mfma_f32_16x16x32_bf16 v[252:255],a[180:183],v[180:183],v[252:255]
s_xor_b32 s49,s50,65536
v_exp_f32 v207,v207
v_mfma_f32_16x16x32_bf16 v[252:255],a[176:179],v[148:151],v[252:255]
ds_read_b128 a[148:151],v1 offset:4352
v_cvt_pk_bf16_f32 v203,v206,v207
v_mfma_f32_16x16x32_bf16 a[32:35],a[192:195],v[168:171],a[32:35]
v_exp_f32 v216,v216
v_mfma_f32_16x16x32_bf16 a[32:35],a[196:199],v[172:175],a[32:35]
ds_read_b128 a[144:147],v1 offset:4096
v_mfma_f32_16x16x32_bf16 a[36:39],a[204:207],v[172:175],a[36:39]
buffer_load_dwordx4 v4,s[20:23],s47 offen offset:1024 nt lds
v_mfma_f32_16x16x32_bf16 a[36:39],a[200:203],v[168:171],a[36:39]
ds_read_b128 a[160:163],v1 offset:8192
v_mfma_f32_16x16x32_bf16 a[40:43],a[208:211],v[168:171],a[40:43]
v_exp_f32 v217,v217
v_mfma_f32_16x16x32_bf16 a[40:43],a[212:215],v[172:175],a[40:43]
ds_read_b128 a[164:167],v1 offset:8448
v_cvt_pk_bf16_f32 v204,v216,v217
v_mfma_f32_16x16x32_bf16 a[44:47],a[220:223],v[172:175],a[44:47]
v_exp_f32 v218,v218
v_mfma_f32_16x16x32_bf16 a[44:47],a[216:219],v[168:171],a[44:47]
ds_read_b128 a[168:171],v1 offset:8704
v_mfma_f32_16x16x32_bf16 a[48:51],a[224:227],v[168:171],a[48:51]
v_exp_f32 v219,v219
v_mfma_f32_16x16x32_bf16 a[48:51],a[228:231],v[172:175],a[48:51]
ds_read_b128 a[172:175],v1 offset:8960
v_cvt_pk_bf16_f32 v205,v218,v219
v_mfma_f32_16x16x32_bf16 a[52:55],a[236:239],v[172:175],a[52:55]
v_exp_f32 v220,v220
v_mfma_f32_16x16x32_bf16 a[52:55],a[232:235],v[168:171],a[52:55]
ds_read_b128 a[188:191],v1 offset:13056
v_mfma_f32_16x16x32_bf16 a[56:59],a[240:243],v[168:171],a[56:59]
v_exp_f32 v221,v221
v_mfma_f32_16x16x32_bf16 a[56:59],a[244:247],v[172:175],a[56:59]
ds_read_b128 a[184:187],v1 offset:12800
v_cvt_pk_bf16_f32 v206,v220,v221
v_mfma_f32_16x16x32_bf16 a[60:63],a[252:255],v[172:175],a[60:63]
v_exp_f32 v222,v222
v_mfma_f32_16x16x32_bf16 a[60:63],a[248:251],v[168:171],a[60:63]
ds_read_b128 a[180:183],v1 offset:12544
v_smfmac_f32_16x16x64_bf16 v[52:55],v[28:31],v[168:175],v27
s_waitcnt lgkmcnt(4)
v_mfma_f32_16x16x32_bf16 v[136:139],a[128:131],v[128:131],v[32:35]
buffer_load_dwordx4 v5,s[20:23],s47 offen offset:2048 nt lds
v_mfma_f32_16x16x32_bf16 v[136:139],a[132:135],v[160:163],v[136:139]
ds_read_b128 a[176:179],v1 offset:12288
v_add_u32 v1,s48,v1
v_mfma_f32_16x16x32_bf16 v[136:139],a[136:139],v[192:195],v[136:139]
v_exp_f32 v223,v223
v_mfma_f32_16x16x32_bf16 v[136:139],a[140:143],v[224:227],v[136:139]
)A"
R"A(v_cvt_pk_bf16_f32 v207,v222,v223
v_mfma_f32_16x16x32_bf16 v[140:143],a[156:159],v[224:227],v[32:35]
v_exp_f32 v232,v232
v_mfma_f32_16x16x32_bf16 v[140:143],a[152:155],v[192:195],v[140:143]
v_exp_f32 v233,v233
v_mfma_f32_16x16x32_bf16 v[140:143],a[148:151],v[160:163],v[140:143]
v_exp_f32 v234,v234
v_mfma_f32_16x16x32_bf16 v[140:143],a[144:147],v[128:131],v[140:143]
v_exp_f32 v235,v235
v_mfma_f32_16x16x32_bf16 v[152:155],a[160:163],v[128:131],v[32:35]
v_exp_f32 v236,v236
v_mfma_f32_16x16x32_bf16 v[152:155],a[164:167],v[160:163],v[152:155]
v_exp_f32 v237,v237
v_mfma_f32_16x16x32_bf16 v[152:155],a[168:171],v[192:195],v[152:155]
s_waitcnt lgkmcnt(0)
v_exp_f32 v238,v238
v_mfma_f32_16x16x32_bf16 v[152:155],a[172:175],v[224:227],v[152:155]
v_exp_f32 v239,v239
v_mfma_f32_16x16x32_bf16 v[156:159],a[188:191],v[224:227],v[32:35]
v_exp_f32 v248,v248
v_mfma_f32_16x16x32_bf16 v[156:159],a[184:187],v[192:195],v[156:159]
buffer_load_dwordx4 v5,s[20:23],s47 offen offset:3072 nt lds
v_mfma_f32_16x16x32_bf16 v[156:159],a[180:183],v[160:163],v[156:159]
v_exp_f32 v249,v249
v_mfma_f32_16x16x32_bf16 v[156:159],a[176:179],v[128:131],v[156:159]
v_exp_f32 v250,v250
v_mfma_f32_16x16x32_bf16 a[64:67],a[192:195],v[200:203],a[64:67]
v_exp_f32 v251,v251
v_mfma_f32_16x16x32_bf16 a[64:67],a[196:199],v[204:207],a[64:67]
v_exp_f32 v252,v252
v_mfma_f32_16x16x32_bf16 a[68:71],a[204:207],v[204:207],a[68:71]
v_exp_f32 v253,v253
v_mfma_f32_16x16x32_bf16 a[68:71],a[200:203],v[200:203],a[68:71]
v_exp_f32 v254,v254
v_mfma_f32_16x16x32_bf16 a[72:75],a[208:211],v[200:203],a[72:75]
v_exp_f32 v255,v255
v_mfma_f32_16x16x32_bf16 a[72:75],a[212:215],v[204:207],a[72:75]
v_exp_f32 v136,v136
v_mfma_f32_16x16x32_bf16 a[76:79],a[220:223],v[204:207],a[76:79]
v_exp_f32 v137,v137
v_mfma_f32_16x16x32_bf16 a[76:79],a[216:219],v[200:203],a[76:79]
v_exp_f32 v138,v138
v_mfma_f32_16x16x32_bf16 a[80:83],a[224:227],v[200:203],a[80:83]
s_addk_i32 m0,16384
v_exp_f32 v139,v139
v_mfma_f32_16x16x32_bf16 a[80:83],a[228:231],v[204:207],a[80:83]
v_exp_f32 v140,v140
v_mfma_f32_16x16x32_bf16 a[84:87],a[236:239],v[204:207],a[84:87]
v_exp_f32 v141,v141
v_mfma_f32_16x16x32_bf16 a[84:87],a[232:235],v[200:203],a[84:87]
buffer_load_dwordx4 v4,s[24:27],s47 offen nt lds
v_mfma_f32_16x16x32_bf16 a[88:91],a[240:243],v[200:203],a[88:91]
v_exp_f32 v142,v142
v_mfma_f32_16x16x32_bf16 a[88:91],a[244:247],v[204:207],a[88:91]
v_exp_f32 v143,v143
v_mfma_f32_16x16x32_bf16 a[92:95],a[252:255],v[204:207],a[92:95]
v_cvt_pk_bf16_f32 v232,v232,v233
v_cvt_pk_bf16_f32 v233,v234,v235
v_mfma_f32_16x16x32_bf16 a[92:95],a[248:251],v[200:203],a[92:95]
v_cvt_pk_bf16_f32 v234,v236,v237
v_cvt_pk_bf16_f32 v235,v238,v239
v_smfmac_f32_16x16x64_bf16 v[56:59],v[28:31],v[200:207],v27
v_cvt_pk_bf16_f32 v236,v248,v249
v_mfma_f32_16x16x32_bf16 v[168:171],a[128:131],v[132:135],v[36:39]
v_cvt_pk_bf16_f32 v237,v250,v251
v_cvt_pk_bf16_f32 v238,v252,v253
v_mfma_f32_16x16x32_bf16 v[168:171],a[132:135],v[164:167],v[168:171]
v_cvt_pk_bf16_f32 v239,v254,v255
v_mfma_f32_16x16x32_bf16 v[168:171],a[136:139],v[196:199],v[168:171]
v_exp_f32 v152,v152
v_mfma_f32_16x16x32_bf16 v[168:171],a[140:143],v[228:231],v[168:171]
v_exp_f32 v153,v153
v_mfma_f32_16x16x32_bf16 v[172:175],a[156:159],v[228:231],v[36:39]
v_exp_f32 v154,v154
v_mfma_f32_16x16x32_bf16 v[172:175],a[152:155],v[196:199],v[172:175]
v_exp_f32 v155,v155
v_mfma_f32_16x16x32_bf16 v[172:175],a[148:151],v[164:167],v[172:175]
v_exp_f32 v156,v156
v_mfma_f32_16x16x32_bf16 v[172:175],a[144:147],v[132:135],v[172:175]
v_exp_f32 v157,v157
v_mfma_f32_16x16x32_bf16 v[184:187],a[160:163],v[132:135],v[36:39]
buffer_load_dwordx4 v5,s[24:27],s47 offen offset:1024 nt lds
v_mfma_f32_16x16x32_bf16 v[184:187],a[164:167],v[164:167],v[184:187]
v_exp_f32 v158,v158
v_mfma_f32_16x16x32_bf16 v[184:187],a[168:171],v[196:199],v[184:187]
v_exp_f32 v159,v159
v_mfma_f32_16x16x32_bf16 v[184:187],a[172:175],v[228:231],v[184:187]
v_exp_f32 v168,v168
v_mfma_f32_16x16x32_bf16 v[188:191],a[188:191],v[228:231],v[36:39]
v_exp_f32 v169,v169
v_mfma_f32_16x16x32_bf16 v[188:191],a[184:187],v[196:199],v[188:191]
v_exp_f32 v170,v170
v_mfma_f32_16x16x32_bf16 v[188:191],a[180:183],v[164:167],v[188:191]
v_exp_f32 v171,v171
v_mfma_f32_16x16x32_bf16 v[188:191],a[176:179],v[132:135],v[188:191]
v_exp_f32 v172,v172
v_mfma_f32_16x16x32_bf16 a[96:99],a[192:195],v[232:235],a[96:99]
ds_read_b64_tr_b16 a[192:193],v2
v_cvt_pk_bf16_f32 v168,v168,v169
v_mfma_f32_16x16x32_bf16 a[96:99],a[196:199],v[236:239],a[96:99]
ds_read_b64_tr_b16 a[194:195],v2 offset:4096
v_cvt_pk_bf16_f32 v169,v170,v171
v_mfma_f32_16x16x32_bf16 a[100:103],a[204:207],v[236:239],a[100:103]
v_exp_f32 v173,v173
v_mfma_f32_16x16x32_bf16 a[100:103],a[200:203],v[232:235],a[100:103]
ds_read_b64_tr_b16 a[196:197],v2 offset:8192
v_cvt_pk_bf16_f32 v170,v172,v173
v_mfma_f32_16x16x32_bf16 a[104:107],a[208:211],v[232:235],a[104:107]
v_exp_f32 v174,v174
v_mfma_f32_16x16x32_bf16 a[104:107],a[212:215],v[236:239],a[104:107]
v_exp_f32 v175,v175
v_mfma_f32_16x16x32_bf16 a[108:111],a[220:223],v[236:239],a[108:111]
buffer_load_dwordx4 v4,s[24:27],s47 offen offset:2048 nt lds
v_mfma_f32_16x16x32_bf16 a[108:111],a[216:219],v[232:235],a[108:111]
ds_read_b64_tr_b16 a[198:199],v2 offset:12288
v_cvt_pk_bf16_f32 v171,v174,v175
v_mfma_f32_16x16x32_bf16 a[112:115],a[224:227],v[232:235],a[112:115]
v_exp_f32 v184,v184
v_mfma_f32_16x16x32_bf16 a[112:115],a[228:231],v[236:239],a[112:115]
v_exp_f32 v185,v185
v_mfma_f32_16x16x32_bf16 a[116:119],a[236:239],v[236:239],a[116:119]
ds_read_b64_tr_b16 a[204:205],v3 offset:8192
v_cvt_pk_bf16_f32 v136,v136,v137
v_mfma_f32_16x16x32_bf16 a[116:119],a[232:235],v[232:235],a[116:119]
ds_read_b64_tr_b16 a[206:207],v3 offset:12288
v_cvt_pk_bf16_f32 v172,v184,v185
v_mfma_f32_16x16x32_bf16 a[120:123],a[240:243],v[232:235],a[120:123]
ds_read_b64_tr_b16 a[200:201],v3
v_mfma_f32_16x16x32_bf16 a[120:123],a[244:247],v[236:239],a[120:123]
ds_read_b64_tr_b16 a[202:203],v3 offset:4096
v_cvt_pk_bf16_f32 v137,v138,v139
v_mfma_f32_16x16x32_bf16 a[124:127],a[252:255],v[236:239],a[124:127]
ds_read_b64_tr_b16 a[208:209],v2 offset:256
v_mfma_f32_16x16x32_bf16 a[124:127],a[248:251],v[232:235],a[124:127]
ds_read_b64_tr_b16 a[210:211],v2 offset:4352
v_smfmac_f32_16x16x64_bf16 v[60:63],v[28:31],v[232:239],v27
ds_read_b64_tr_b16 a[212:213],v2 offset:8448
v_mfma_f32_16x16x32_bf16 v[200:203],a[128:131],v[144:147],v[40:43]
ds_read_b64_tr_b16 a[214:215],v2 offset:12544
v_cvt_pk_bf16_f32 v138,v140,v141
v_mfma_f32_16x16x32_bf16 v[200:203],a[132:135],v[176:179],v[200:203]
ds_read_b64_tr_b16 a[220:221],v3 offset:8448
v_mfma_f32_16x16x32_bf16 v[200:203],a[136:139],v[208:211],v[200:203]
ds_read_b64_tr_b16 a[222:223],v3 offset:12544
v_cvt_pk_bf16_f32 v139,v142,v143
v_mfma_f32_16x16x32_bf16 v[200:203],a[140:143],v[240:243],v[200:203]
buffer_load_dwordx4 v5,s[24:27],s47 offen offset:3072 nt lds
v_mfma_f32_16x16x32_bf16 v[204:207],a[156:159],v[240:243],v[40:43]
s_or_b32 s47,s47,0x18000
s_addk_i32 s47,4096
ds_read_b64_tr_b16 a[216:217],v3 offset:256
v_mfma_f32_16x16x32_bf16 v[204:207],a[152:155],v[208:211],v[204:207]
s_and_b32 s47,s47,0x27000
ds_read_b64_tr_b16 a[218:219],v3 offset:4352
v_cvt_pk_bf16_f32 v140,v152,v153
v_mfma_f32_16x16x32_bf16 v[204:207],a[148:151],v[176:179],v[204:207]
s_or_b32 s47,s47,s46
ds_read_b64_tr_b16 a[224:225],v2 offset:512
v_mfma_f32_16x16x32_bf16 v[204:207],a[144:147],v[144:147],v[204:207]
ds_read_b64_tr_b16 a[226:227],v2 offset:4608
v_mfma_f32_16x16x32_bf16 v[216:219],a[160:163],v[144:147],v[40:43]
ds_read_b64_tr_b16 a[228:229],v2 offset:8704
v_cvt_pk_bf16_f32 v141,v154,v155
v_mfma_f32_16x16x32_bf16 v[216:219],a[164:167],v[176:179],v[216:219]
ds_read_b64_tr_b16 a[230:231],v2 offset:12800
v_mfma_f32_16x16x32_bf16 v[216:219],a[168:171],v[208:211],v[216:219]
ds_read_b64_tr_b16 a[236:237],v3 offset:8704
v_mfma_f32_16x16x32_bf16 v[216:219],a[172:175],v[240:243],v[216:219]
ds_read_b64_tr_b16 a[238:239],v3 offset:12800
v_cvt_pk_bf16_f32 v142,v156,v157
v_mfma_f32_16x16x32_bf16 v[220:223],a[188:191],v[240:243],v[40:43]
ds_read_b64_tr_b16 a[232:233],v3 offset:512
v_mfma_f32_16x16x32_bf16 v[220:223],a[184:187],v[208:211],v[220:223]
ds_read_b64_tr_b16 a[234:235],v3 offset:4608
v_mfma_f32_16x16x32_bf16 v[220:223],a[180:183],v[176:179],v[220:223]
ds_read_b64_tr_b16 a[240:241],v2 offset:768
v_cvt_pk_bf16_f32 v143,v158,v159
v_mfma_f32_16x16x32_bf16 v[220:223],a[176:179],v[144:147],v[220:223]
s_waitcnt lgkmcnt(7)
buffer_load_dword v26,v24,s[88:91],s93 offen
v_mfma_f32_16x16x32_bf16 a[0:3],a[192:195],v[136:139],a[0:3]
ds_read_b64_tr_b16 a[242:243],v2 offset:4864
v_mfma_f32_16x16x32_bf16 a[0:3],a[196:199],v[140:143],a[0:3]
ds_read_b64_tr_b16 a[244:245],v2 offset:8960
v_mfma_f32_16x16x32_bf16 a[4:7],a[204:207],v[140:143],a[4:7]
ds_read_b64_tr_b16 a[246:247],v2 offset:13056
v_mfma_f32_16x16x32_bf16 a[4:7],a[200:203],v[136:139],a[4:7]
s_memrealtime s[76:77]
ds_read_b64_tr_b16 a[252:253],v3 offset:8960
v_mfma_f32_16x16x32_bf16 a[8:11],a[208:211],v[136:139],a[8:11]
ds_read_b64_tr_b16 a[254:255],v3 offset:13056
v_mfma_f32_16x16x32_bf16 a[8:11],a[212:215],v[140:143],a[8:11]
ds_read_b64_tr_b16 a[248:249],v3 offset:768
v_mfma_f32_16x16x32_bf16 a[12:15],a[220:223],v[140:143],a[12:15]
ds_read_b64_tr_b16 a[250:251],v3 offset:4864
v_mfma_f32_16x16x32_bf16 a[12:15],a[216:219],v[136:139],a[12:15]
s_bitcmp1_b32 s45,0
s_cbranch_scc0 L_t311_%=
s_mov_b32 m0,s80
s_addk_i32 s80,4096
buffer_load_dwordx4 v25,s[24:27],s83 offen nt lds
s_addk_i32 s83,256
L_t311_%=:
v_mfma_f32_16x16x32_bf16 a[16:19],a[224:227],v[136:139],a[16:19]
s_waitcnt lgkmcnt(6)
v_exp_f32 v186,v186
v_mfma_f32_16x16x32_bf16 a[16:19],a[228:231],v[140:143],a[16:19]
v_exp_f32 v187,v187
v_mfma_f32_16x16x32_bf16 a[20:23],a[236:239],v[140:143],a[20:23]
s_waitcnt lgkmcnt(0)
v_exp_f32 v188,v188
v_mfma_f32_16x16x32_bf16 a[20:23],a[232:235],v[136:139],a[20:23]
s_sub_u32 s79,s78,s76
v_exp_f32 v189,v189
v_mfma_f32_16x16x32_bf16 a[24:27],a[240:243],v[136:139],a[24:27]
s_add_u32 s78,s76,s60
v_exp_f32 v190,v190
v_mfma_f32_16x16x32_bf16 a[24:27],a[244:247],v[140:143],a[24:27]
s_cmp_gt_i32 s79,s95
s_cbranch_scc1 L_ps7_%=
L_pr7_%=:
v_cvt_pk_bf16_f32 v173,v186,v187
v_mfma_f32_16x16x32_bf16 a[28:31],a[252:255],v[140:143],a[28:31]
v_exp_f32 v191,v191
v_mfma_f32_16x16x32_bf16 a[28:31],a[248:251],v[136:139],a[28:31]
v_cvt_pk_bf16_f32 v174,v188,v189
v_cvt_pk_bf16_f32 v175,v190,v191
s_cmp_lg_u32 s53,0
v_smfmac_f32_16x16x64_bf16 v[48:51],v[28:31],v[136:143],v27
s_cbranch_scc1 L_att1_%=
L_att1x_%=:
L_endseg_%=:
v_mfma_f32_16x16x32_bf16 v[232:235],a[128:131],v[148:151],v[44:47]
s_waitcnt vmcnt(10)
s_lshl_b32 s43,s41,2
v_exp_f32 v200,v200
v_mfma_f32_16x16x32_bf16 v[232:235],a[132:135],v[180:183],v[232:235]
s_barrier
ds_read_b128 a[128:131],v1
v_add_u32 v2,s48,v2
v_mfma_f32_16x16x32_bf16 v[232:235],a[136:139],v[212:215],v[232:235]
s_add_u32 s43,s43,s70
v_exp_f32 v201,v201
v_mfma_f32_16x16x32_bf16 v[232:235],a[140:143],v[244:247],v[232:235]
s_lshl_b32 s43,s43,6
ds_read_b128 a[132:135],v1 offset:256
v_cvt_pk_bf16_f32 v200,v200,v201
v_mfma_f32_16x16x32_bf16 v[236:239],a[156:159],v[244:247],v[44:47]
s_add_u32 s43,s43,s40
v_exp_f32 v202,v202
v_mfma_f32_16x16x32_bf16 v[236:239],a[152:155],v[212:215],v[236:239]
s_lshl_b32 s43,s43,15
ds_read_b128 a[136:139],v1 offset:512
v_add_u32 v3,s48,v3
v_mfma_f32_16x16x32_bf16 v[236:239],a[148:151],v[180:183],v[236:239]
s_mov_b32 s48,32768
v_exp_f32 v203,v203
v_mfma_f32_16x16x32_bf16 v[236:239],a[144:147],v[148:151],v[236:239]
s_mov_b64 s[28:29],s[12:13]
ds_read_b128 a[140:143],v1 offset:768
v_cvt_pk_bf16_f32 v201,v202,v203
v_mfma_f32_16x16x32_bf16 v[248:251],a[160:163],v[148:151],v[44:47]
s_mov_b32 s30,950272
v_exp_f32 v204,v204
v_mfma_f32_16x16x32_bf16 v[248:251],a[164:167],v[180:183],v[248:251]
s_mov_b32 s31,0x20000
ds_read_b128 a[156:159],v1 offset:4864
v_lshrrev_b32 v25,6,v6
v_mfma_f32_16x16x32_bf16 v[248:251],a[168:171],v[212:215],v[248:251]
v_exp_f32 v205,v205
v_mfma_f32_16x16x32_bf16 v[248:251],a[172:175],v[244:247],v[248:251]
ds_read_b128 a[152:155],v1 offset:4608
v_cvt_pk_bf16_f32 v202,v204,v205
v_mfma_f32_16x16x32_bf16 v[252:255],a[188:191],v[244:247],v[44:47]
)A"
R"A(v_exp_f32 v206,v206
v_mfma_f32_16x16x32_bf16 v[252:255],a[184:187],v[212:215],v[252:255]
ds_read_b128 a[148:151],v1 offset:4352
v_mul_u32_u24 v25,320,v25
v_mfma_f32_16x16x32_bf16 v[252:255],a[180:183],v[180:183],v[252:255]
v_exp_f32 v207,v207
v_mfma_f32_16x16x32_bf16 v[252:255],a[176:179],v[148:151],v[252:255]
ds_read_b128 a[144:147],v1 offset:4096
v_cvt_pk_bf16_f32 v203,v206,v207
v_mfma_f32_16x16x32_bf16 a[32:35],a[192:195],v[168:171],a[32:35]
v_exp_f32 v216,v216
v_mfma_f32_16x16x32_bf16 a[32:35],a[196:199],v[172:175],a[32:35]
s_add_u32 s28,s28,s43
s_addc_u32 s29,s29,0
ds_read_b128 a[160:163],v1 offset:8192
v_mfma_f32_16x16x32_bf16 a[36:39],a[204:207],v[172:175],a[36:39]
s_and_b32 s29,s29,0xffff
v_exp_f32 v217,v217
v_mfma_f32_16x16x32_bf16 a[36:39],a[200:203],v[168:171],a[36:39]
ds_read_b128 a[164:167],v1 offset:8448
v_cvt_pk_bf16_f32 v204,v216,v217
v_mfma_f32_16x16x32_bf16 a[40:43],a[208:211],v[168:171],a[40:43]
v_exp_f32 v218,v218
v_mfma_f32_16x16x32_bf16 a[40:43],a[212:215],v[172:175],a[40:43]
ds_read_b128 a[168:171],v1 offset:8704
v_mfma_f32_16x16x32_bf16 a[44:47],a[220:223],v[172:175],a[44:47]
v_exp_f32 v219,v219
v_mfma_f32_16x16x32_bf16 a[44:47],a[216:219],v[168:171],a[44:47]
ds_read_b128 a[172:175],v1 offset:8960
v_cvt_pk_bf16_f32 v205,v218,v219
v_mfma_f32_16x16x32_bf16 a[48:51],a[224:227],v[168:171],a[48:51]
v_exp_f32 v220,v220
v_mfma_f32_16x16x32_bf16 a[48:51],a[228:231],v[172:175],a[48:51]
ds_read_b128 a[188:191],v1 offset:13056
v_mfma_f32_16x16x32_bf16 a[52:55],a[236:239],v[172:175],a[52:55]
v_exp_f32 v221,v221
v_mfma_f32_16x16x32_bf16 a[52:55],a[232:235],v[168:171],a[52:55]
ds_read_b128 a[184:187],v1 offset:12800
v_cvt_pk_bf16_f32 v206,v220,v221
v_mfma_f32_16x16x32_bf16 a[56:59],a[240:243],v[168:171],a[56:59]
v_exp_f32 v222,v222
v_mfma_f32_16x16x32_bf16 a[56:59],a[244:247],v[172:175],a[56:59]
ds_read_b128 a[180:183],v1 offset:12544
v_mfma_f32_16x16x32_bf16 a[60:63],a[252:255],v[172:175],a[60:63]
v_exp_f32 v223,v223
v_mfma_f32_16x16x32_bf16 a[60:63],a[248:251],v[168:171],a[60:63]
ds_read_b128 a[176:179],v1 offset:12288
v_cvt_pk_bf16_f32 v207,v222,v223
v_smfmac_f32_16x16x64_bf16 v[52:55],v[28:31],v[168:175],v27
s_waitcnt lgkmcnt(4)
v_add_u32 v1,s48,v1
v_mfma_f32_16x16x32_bf16 v[136:139],a[128:131],v[128:131],v[32:35]
v_exp_f32 v232,v232
v_mfma_f32_16x16x32_bf16 v[136:139],a[132:135],v[160:163],v[136:139]
v_exp_f32 v233,v233
v_mfma_f32_16x16x32_bf16 v[136:139],a[136:139],v[192:195],v[136:139]
v_exp_f32 v234,v234
v_mfma_f32_16x16x32_bf16 v[136:139],a[140:143],v[224:227],v[136:139]
v_exp_f32 v235,v235
v_mfma_f32_16x16x32_bf16 v[140:143],a[156:159],v[224:227],v[32:35]
v_exp_f32 v236,v236
v_mfma_f32_16x16x32_bf16 v[140:143],a[152:155],v[192:195],v[140:143]
s_add_u32 s80,s71,0
s_and_b32 s80,s80,3
s_lshl_b32 s80,s80,1
v_mfma_f32_16x16x32_bf16 v[140:143],a[148:151],v[160:163],v[140:143]
s_mul_i32 s80,s80,163840
s_add_u32 s80,s80,s81
v_cvt_pk_bf16_f32 v232,v232,v233
v_mfma_f32_16x16x32_bf16 v[140:143],a[144:147],v[128:131],v[140:143]
s_add_u32 s83,s80,20480
v_exp_f32 v237,v237
v_mfma_f32_16x16x32_bf16 v[152:155],a[160:163],v[128:131],v[32:35]
v_exp_f32 v238,v238
v_mfma_f32_16x16x32_bf16 v[152:155],a[164:167],v[160:163],v[152:155]
buffer_store_dwordx4 v[64:67],v6,s[36:39],s84 offen sc1
v_mfma_f32_16x16x32_bf16 v[152:155],a[168:171],v[192:195],v[152:155]
v_exp_f32 v239,v239
v_mfma_f32_16x16x32_bf16 v[152:155],a[172:175],v[224:227],v[152:155]
s_waitcnt lgkmcnt(0)
v_exp_f32 v248,v248
v_mfma_f32_16x16x32_bf16 v[156:159],a[188:191],v[224:227],v[32:35]
v_exp_f32 v249,v249
v_mfma_f32_16x16x32_bf16 v[156:159],a[184:187],v[192:195],v[156:159]
v_exp_f32 v250,v250
v_mfma_f32_16x16x32_bf16 v[156:159],a[180:183],v[160:163],v[156:159]
v_exp_f32 v251,v251
v_mfma_f32_16x16x32_bf16 v[156:159],a[176:179],v[128:131],v[156:159]
v_exp_f32 v252,v252
v_mfma_f32_16x16x32_bf16 a[64:67],a[192:195],v[200:203],a[64:67]
v_exp_f32 v253,v253
v_mfma_f32_16x16x32_bf16 a[64:67],a[196:199],v[204:207],a[64:67]
v_exp_f32 v254,v254
v_mfma_f32_16x16x32_bf16 a[68:71],a[204:207],v[204:207],a[68:71]
v_exp_f32 v255,v255
v_mfma_f32_16x16x32_bf16 a[68:71],a[200:203],v[200:203],a[68:71]
v_exp_f32 v136,v136
v_mfma_f32_16x16x32_bf16 a[72:75],a[208:211],v[200:203],a[72:75]
v_exp_f32 v137,v137
v_mfma_f32_16x16x32_bf16 a[72:75],a[212:215],v[204:207],a[72:75]
v_cvt_pk_bf16_f32 v233,v234,v235
v_cvt_pk_bf16_f32 v234,v236,v237
v_mfma_f32_16x16x32_bf16 a[76:79],a[220:223],v[204:207],a[76:79]
v_cvt_pk_bf16_f32 v235,v238,v239
v_cvt_pk_bf16_f32 v236,v248,v249
v_mfma_f32_16x16x32_bf16 a[76:79],a[216:219],v[200:203],a[76:79]
v_cvt_pk_bf16_f32 v237,v250,v251
v_cvt_pk_bf16_f32 v238,v252,v253
v_mfma_f32_16x16x32_bf16 a[80:83],a[224:227],v[200:203],a[80:83]
v_cvt_pk_bf16_f32 v239,v254,v255
v_cvt_pk_bf16_f32 v136,v136,v137
v_mfma_f32_16x16x32_bf16 a[80:83],a[228:231],v[204:207],a[80:83]
buffer_store_dwordx4 v[68:71],v6,s[36:39],s84 offen offset:1024 sc1
v_mfma_f32_16x16x32_bf16 a[84:87],a[236:239],v[204:207],a[84:87]
s_add_u32 m0,s82,81920
v_exp_f32 v138,v138
v_mfma_f32_16x16x32_bf16 a[84:87],a[232:235],v[200:203],a[84:87]
v_exp_f32 v139,v139
v_mfma_f32_16x16x32_bf16 a[88:91],a[240:243],v[200:203],a[88:91]
v_exp_f32 v140,v140
v_mfma_f32_16x16x32_bf16 a[88:91],a[244:247],v[204:207],a[88:91]
buffer_load_dwordx4 v7,s[72:75],s80 offen lds
v_mfma_f32_16x16x32_bf16 a[92:95],a[252:255],v[204:207],a[92:95]
v_exp_f32 v141,v141
v_mfma_f32_16x16x32_bf16 a[92:95],a[248:251],v[200:203],a[92:95]
v_exp_f32 v142,v142
v_smfmac_f32_16x16x64_bf16 v[56:59],v[28:31],v[200:207],v27
v_cvt_pk_bf16_f32 v137,v138,v139
v_mfma_f32_16x16x32_bf16 v[168:171],a[128:131],v[132:135],v[36:39]
v_exp_f32 v143,v143
v_mfma_f32_16x16x32_bf16 v[168:171],a[132:135],v[164:167],v[168:171]
v_cvt_pk_bf16_f32 v138,v140,v141
v_cvt_pk_bf16_f32 v139,v142,v143
v_mfma_f32_16x16x32_bf16 v[168:171],a[136:139],v[196:199],v[168:171]
v_exp_f32 v152,v152
v_mfma_f32_16x16x32_bf16 v[168:171],a[140:143],v[228:231],v[168:171]
v_exp_f32 v153,v153
v_mfma_f32_16x16x32_bf16 v[172:175],a[156:159],v[228:231],v[36:39]
v_exp_f32 v154,v154
v_mfma_f32_16x16x32_bf16 v[172:175],a[152:155],v[196:199],v[172:175]
v_exp_f32 v155,v155
v_mfma_f32_16x16x32_bf16 v[172:175],a[148:151],v[164:167],v[172:175]
v_exp_f32 v156,v156
v_mfma_f32_16x16x32_bf16 v[172:175],a[144:147],v[132:135],v[172:175]
v_exp_f32 v157,v157
v_mfma_f32_16x16x32_bf16 v[184:187],a[160:163],v[132:135],v[36:39]
buffer_store_dwordx4 v[72:75],v6,s[36:39],s84 offen offset:2048 sc1
v_mfma_f32_16x16x32_bf16 v[184:187],a[164:167],v[164:167],v[184:187]
v_exp_f32 v158,v158
v_mfma_f32_16x16x32_bf16 v[184:187],a[168:171],v[196:199],v[184:187]
v_exp_f32 v159,v159
v_mfma_f32_16x16x32_bf16 v[184:187],a[172:175],v[228:231],v[184:187]
v_cvt_pk_bf16_f32 v140,v152,v153
v_cvt_pk_bf16_f32 v141,v154,v155
v_mfma_f32_16x16x32_bf16 v[188:191],a[188:191],v[228:231],v[36:39]
v_cvt_pk_bf16_f32 v142,v156,v157
v_cvt_pk_bf16_f32 v143,v158,v159
v_mfma_f32_16x16x32_bf16 v[188:191],a[184:187],v[196:199],v[188:191]
v_exp_f32 v168,v168
v_mfma_f32_16x16x32_bf16 v[188:191],a[180:183],v[164:167],v[188:191]
v_exp_f32 v169,v169
v_mfma_f32_16x16x32_bf16 v[188:191],a[176:179],v[132:135],v[188:191]
v_exp_f32 v170,v170
v_mfma_f32_16x16x32_bf16 a[96:99],a[192:195],v[232:235],a[96:99]
buffer_load_dwordx4 v8,s[72:75],s80 offen offset:1024 lds
v_mfma_f32_16x16x32_bf16 a[96:99],a[196:199],v[236:239],a[96:99]
ds_read_b64_tr_b16 a[192:193],v2
ds_read_b64_tr_b16 a[194:195],v2 offset:4096
v_mfma_f32_16x16x32_bf16 a[100:103],a[204:207],v[236:239],a[100:103]
ds_read_b64_tr_b16 a[196:197],v2 offset:8192
v_cvt_pk_bf16_f32 v168,v168,v169
v_mfma_f32_16x16x32_bf16 a[100:103],a[200:203],v[232:235],a[100:103]
v_exp_f32 v171,v171
v_mfma_f32_16x16x32_bf16 a[104:107],a[208:211],v[232:235],a[104:107]
ds_read_b64_tr_b16 a[198:199],v2 offset:12288
ds_read_b64_tr_b16 a[204:205],v3 offset:8192
v_mfma_f32_16x16x32_bf16 a[104:107],a[212:215],v[236:239],a[104:107]
ds_read_b64_tr_b16 a[206:207],v3 offset:12288
v_cvt_pk_bf16_f32 v169,v170,v171
v_mfma_f32_16x16x32_bf16 a[108:111],a[220:223],v[236:239],a[108:111]
v_exp_f32 v172,v172
v_mfma_f32_16x16x32_bf16 a[108:111],a[216:219],v[232:235],a[108:111]
ds_read_b64_tr_b16 a[200:201],v3
ds_read_b64_tr_b16 a[202:203],v3 offset:4096
v_mfma_f32_16x16x32_bf16 a[112:115],a[224:227],v[232:235],a[112:115]
buffer_store_dwordx4 v[76:79],v6,s[36:39],s84 offen offset:3072 sc1
v_mfma_f32_16x16x32_bf16 a[112:115],a[228:231],v[236:239],a[112:115]
s_add_u32 s84,s55,131072
ds_read_b64_tr_b16 a[208:209],v2 offset:256
ds_read_b64_tr_b16 a[210:211],v2 offset:4352
v_mfma_f32_16x16x32_bf16 a[116:119],a[236:239],v[236:239],a[116:119]
s_max_i32 s84,s84,s18
v_exp_f32 v173,v173
v_mfma_f32_16x16x32_bf16 a[116:119],a[232:235],v[232:235],a[116:119]
ds_read_b64_tr_b16 a[212:213],v2 offset:8448
ds_read_b64_tr_b16 a[214:215],v2 offset:12544
v_mfma_f32_16x16x32_bf16 a[120:123],a[240:243],v[232:235],a[120:123]
ds_read_b64_tr_b16 a[220:221],v3 offset:8448
v_cvt_pk_bf16_f32 v170,v172,v173
v_mfma_f32_16x16x32_bf16 a[120:123],a[244:247],v[236:239],a[120:123]
ds_read_b64_tr_b16 a[222:223],v3 offset:12544
v_mfma_f32_16x16x32_bf16 a[124:127],a[252:255],v[236:239],a[124:127]
ds_read_b64_tr_b16 a[216:217],v3 offset:256
v_mfma_f32_16x16x32_bf16 a[124:127],a[248:251],v[232:235],a[124:127]
ds_read_b64_tr_b16 a[218:219],v3 offset:4352
ds_read_b64_tr_b16 a[224:225],v2 offset:512
v_smfmac_f32_16x16x64_bf16 v[60:63],v[28:31],v[232:239],v27
ds_read_b64_tr_b16 a[226:227],v2 offset:4608
v_mfma_f32_16x16x32_bf16 v[200:203],a[128:131],v[144:147],v[40:43]
ds_read_b64_tr_b16 a[228:229],v2 offset:8704
v_mfma_f32_16x16x32_bf16 v[200:203],a[132:135],v[176:179],v[200:203]
ds_read_b64_tr_b16 a[230:231],v2 offset:12800
v_mfma_f32_16x16x32_bf16 v[200:203],a[136:139],v[208:211],v[200:203]
ds_read_b64_tr_b16 a[236:237],v3 offset:8704
ds_read_b64_tr_b16 a[238:239],v3 offset:12800
v_mfma_f32_16x16x32_bf16 v[200:203],a[140:143],v[240:243],v[200:203]
buffer_load_dwordx4 v9,s[72:75],s80 offen offset:2048 lds
v_mfma_f32_16x16x32_bf16 v[204:207],a[156:159],v[240:243],v[40:43]
ds_read_b64_tr_b16 a[232:233],v3 offset:512
ds_read_b64_tr_b16 a[234:235],v3 offset:4608
v_mfma_f32_16x16x32_bf16 v[204:207],a[152:155],v[208:211],v[204:207]
ds_read_b64_tr_b16 a[240:241],v2 offset:768
v_mfma_f32_16x16x32_bf16 v[204:207],a[148:151],v[176:179],v[204:207]
ds_read_b64_tr_b16 a[242:243],v2 offset:4864
v_mfma_f32_16x16x32_bf16 v[204:207],a[144:147],v[144:147],v[204:207]
buffer_store_dwordx4 v[80:83],v6,s[36:39],s85 offen sc1
v_mfma_f32_16x16x32_bf16 v[216:219],a[160:163],v[144:147],v[40:43]
ds_read_b64_tr_b16 a[244:245],v2 offset:8960
ds_read_b64_tr_b16 a[246:247],v2 offset:13056
v_mfma_f32_16x16x32_bf16 v[216:219],a[164:167],v[176:179],v[216:219]
ds_read_b64_tr_b16 a[252:253],v3 offset:8960
v_add_u32 v2,s48,v2
v_mfma_f32_16x16x32_bf16 v[216:219],a[168:171],v[208:211],v[216:219]
ds_read_b64_tr_b16 a[254:255],v3 offset:13056
v_mfma_f32_16x16x32_bf16 v[216:219],a[172:175],v[240:243],v[216:219]
ds_read_b64_tr_b16 a[248:249],v3 offset:768
v_mfma_f32_16x16x32_bf16 v[220:223],a[188:191],v[240:243],v[40:43]
ds_read_b64_tr_b16 a[250:251],v3 offset:4864
v_add_u32 v3,s48,v3
v_mfma_f32_16x16x32_bf16 v[220:223],a[184:187],v[208:211],v[220:223]
s_mov_b32 s48,98304
v_exp_f32 v174,v174
v_mfma_f32_16x16x32_bf16 v[220:223],a[180:183],v[176:179],v[220:223]
v_exp_f32 v175,v175
v_mfma_f32_16x16x32_bf16 v[220:223],a[176:179],v[144:147],v[220:223]
s_waitcnt lgkmcnt(6)
v_exp_f32 v184,v184
v_mfma_f32_16x16x32_bf16 a[0:3],a[192:195],v[136:139],a[0:3]
v_exp_f32 v185,v185
v_mfma_f32_16x16x32_bf16 a[0:3],a[196:199],v[140:143],a[0:3]
v_cvt_pk_bf16_f32 v171,v174,v175
v_cvt_pk_bf16_f32 v172,v184,v185
v_mfma_f32_16x16x32_bf16 a[4:7],a[204:207],v[140:143],a[4:7]
v_exp_f32 v186,v186
v_mfma_f32_16x16x32_bf16 a[4:7],a[200:203],v[136:139],a[4:7]
v_exp_f32 v187,v187
v_mfma_f32_16x16x32_bf16 a[8:11],a[208:211],v[136:139],a[8:11]
v_exp_f32 v188,v188
v_mfma_f32_16x16x32_bf16 a[8:11],a[212:215],v[140:143],a[8:11]
v_exp_f32 v189,v189
v_mfma_f32_16x16x32_bf16 a[12:15],a[220:223],v[140:143],a[12:15]
v_exp_f32 v190,v190
)A"
R"A(v_mfma_f32_16x16x32_bf16 a[12:15],a[216:219],v[136:139],a[12:15]
buffer_store_dwordx4 v[84:87],v6,s[36:39],s85 offen offset:1024 sc1
v_mfma_f32_16x16x32_bf16 a[16:19],a[224:227],v[136:139],a[16:19]
v_exp_f32 v191,v191
v_mfma_f32_16x16x32_bf16 a[16:19],a[228:231],v[140:143],a[16:19]
v_cvt_pk_bf16_f32 v173,v186,v187
v_cvt_pk_bf16_f32 v174,v188,v189
v_mfma_f32_16x16x32_bf16 a[20:23],a[236:239],v[140:143],a[20:23]
v_exp_f32 v200,v200
v_mfma_f32_16x16x32_bf16 a[20:23],a[232:235],v[136:139],a[20:23]
buffer_load_dwordx4 v10,s[72:75],s80 offen offset:3072 lds
v_mfma_f32_16x16x32_bf16 a[24:27],a[240:243],v[136:139],a[24:27]
s_waitcnt lgkmcnt(0)
v_exp_f32 v201,v201
v_mfma_f32_16x16x32_bf16 a[24:27],a[244:247],v[140:143],a[24:27]
v_cvt_pk_bf16_f32 v175,v190,v191
v_cvt_pk_bf16_f32 v200,v200,v201
v_mfma_f32_16x16x32_bf16 a[28:31],a[252:255],v[140:143],a[28:31]
v_exp_f32 v202,v202
v_mfma_f32_16x16x32_bf16 a[28:31],a[248:251],v[136:139],a[28:31]
v_exp_f32 v203,v203
v_smfmac_f32_16x16x64_bf16 v[48:51],v[28:31],v[136:143],v27
v_cvt_pk_bf16_f32 v201,v202,v203
v_mfma_f32_16x16x32_bf16 v[232:235],a[128:131],v[148:151],v[44:47]
s_waitcnt vmcnt(11)
v_exp_f32 v204,v204
v_mfma_f32_16x16x32_bf16 v[232:235],a[132:135],v[180:183],v[232:235]
s_barrier
v_exp_f32 v205,v205
v_mfma_f32_16x16x32_bf16 v[232:235],a[136:139],v[212:215],v[232:235]
ds_read_b128 a[128:131],v1
v_cvt_pk_bf16_f32 v202,v204,v205
v_mfma_f32_16x16x32_bf16 v[232:235],a[140:143],v[244:247],v[232:235]
v_exp_f32 v206,v206
v_mfma_f32_16x16x32_bf16 v[236:239],a[156:159],v[244:247],v[44:47]
v_exp_f32 v207,v207
v_mfma_f32_16x16x32_bf16 v[236:239],a[152:155],v[212:215],v[236:239]
buffer_store_dwordx4 v[88:91],v6,s[36:39],s85 offen offset:2048 sc1
v_mfma_f32_16x16x32_bf16 v[236:239],a[148:151],v[180:183],v[236:239]
s_addk_i32 m0,4096
ds_read_b128 a[132:135],v1 offset:256
v_cvt_pk_bf16_f32 v203,v206,v207
v_mfma_f32_16x16x32_bf16 v[236:239],a[144:147],v[148:151],v[236:239]
v_exp_f32 v216,v216
v_mfma_f32_16x16x32_bf16 v[248:251],a[160:163],v[148:151],v[44:47]
v_exp_f32 v217,v217
v_mfma_f32_16x16x32_bf16 v[248:251],a[164:167],v[180:183],v[248:251]
buffer_load_dwordx4 v11,s[72:75],s80 offen lds
v_mfma_f32_16x16x32_bf16 v[248:251],a[168:171],v[212:215],v[248:251]
ds_read_b128 a[136:139],v1 offset:512
v_cvt_pk_bf16_f32 v204,v216,v217
v_mfma_f32_16x16x32_bf16 v[248:251],a[172:175],v[244:247],v[248:251]
v_exp_f32 v218,v218
v_mfma_f32_16x16x32_bf16 v[252:255],a[188:191],v[244:247],v[44:47]
ds_read_b128 a[140:143],v1 offset:768
v_mfma_f32_16x16x32_bf16 v[252:255],a[184:187],v[212:215],v[252:255]
v_exp_f32 v219,v219
v_mfma_f32_16x16x32_bf16 v[252:255],a[180:183],v[180:183],v[252:255]
ds_read_b128 a[156:159],v1 offset:4864
v_cvt_pk_bf16_f32 v205,v218,v219
v_mfma_f32_16x16x32_bf16 v[252:255],a[176:179],v[148:151],v[252:255]
v_exp_f32 v220,v220
v_mfma_f32_16x16x32_bf16 a[32:35],a[192:195],v[168:171],a[32:35]
ds_read_b128 a[152:155],v1 offset:4608
v_mfma_f32_16x16x32_bf16 a[32:35],a[196:199],v[172:175],a[32:35]
v_exp_f32 v221,v221
v_mfma_f32_16x16x32_bf16 a[36:39],a[204:207],v[172:175],a[36:39]
ds_read_b128 a[148:151],v1 offset:4352
v_cvt_pk_bf16_f32 v206,v220,v221
v_mfma_f32_16x16x32_bf16 a[36:39],a[200:203],v[168:171],a[36:39]
v_exp_f32 v222,v222
v_mfma_f32_16x16x32_bf16 a[40:43],a[208:211],v[168:171],a[40:43]
ds_read_b128 a[144:147],v1 offset:4096
v_mfma_f32_16x16x32_bf16 a[40:43],a[212:215],v[172:175],a[40:43]
buffer_store_dwordx4 v[92:95],v6,s[36:39],s85 offen offset:3072 sc1
v_mfma_f32_16x16x32_bf16 a[44:47],a[220:223],v[172:175],a[44:47]
s_add_u32 s85,s55,135168
s_max_i32 s85,s85,s19
ds_read_b128 a[160:163],v1 offset:8192
v_mfma_f32_16x16x32_bf16 a[44:47],a[216:219],v[168:171],a[44:47]
v_exp_f32 v223,v223
v_mfma_f32_16x16x32_bf16 a[48:51],a[224:227],v[168:171],a[48:51]
ds_read_b128 a[164:167],v1 offset:8448
v_cvt_pk_bf16_f32 v207,v222,v223
v_mfma_f32_16x16x32_bf16 a[48:51],a[228:231],v[172:175],a[48:51]
v_exp_f32 v232,v232
v_mfma_f32_16x16x32_bf16 a[52:55],a[236:239],v[172:175],a[52:55]
s_addk_i32 m0,1024
ds_read_b128 a[168:171],v1 offset:8704
v_mfma_f32_16x16x32_bf16 a[52:55],a[232:235],v[168:171],a[52:55]
v_exp_f32 v233,v233
v_mfma_f32_16x16x32_bf16 a[56:59],a[240:243],v[168:171],a[56:59]
ds_read_b128 a[172:175],v1 offset:8960
v_cvt_pk_bf16_f32 v232,v232,v233
v_mfma_f32_16x16x32_bf16 a[56:59],a[244:247],v[172:175],a[56:59]
buffer_load_dwordx4 v7,s[72:75],s83 offen lds
v_mfma_f32_16x16x32_bf16 a[60:63],a[252:255],v[172:175],a[60:63]
ds_read_b128 a[188:191],v1 offset:13056
v_mfma_f32_16x16x32_bf16 a[60:63],a[248:251],v[168:171],a[60:63]
v_exp_f32 v234,v234
v_smfmac_f32_16x16x64_bf16 v[52:55],v[28:31],v[168:175],v27
s_waitcnt lgkmcnt(3)
ds_read_b128 a[184:187],v1 offset:12800
v_mfma_f32_16x16x32_bf16 v[136:139],a[128:131],v[128:131],v[32:35]
v_exp_f32 v235,v235
v_mfma_f32_16x16x32_bf16 v[136:139],a[132:135],v[160:163],v[136:139]
ds_read_b128 a[180:183],v1 offset:12544
v_cvt_pk_bf16_f32 v233,v234,v235
v_mfma_f32_16x16x32_bf16 v[136:139],a[136:139],v[192:195],v[136:139]
v_exp_f32 v236,v236
v_mfma_f32_16x16x32_bf16 v[136:139],a[140:143],v[224:227],v[136:139]
ds_read_b128 a[176:179],v1 offset:12288
v_add_u32 v1,s48,v1
v_mfma_f32_16x16x32_bf16 v[140:143],a[156:159],v[224:227],v[32:35]
buffer_store_dwordx4 v[96:99],v6,s[36:39],s86 offen sc1
v_mfma_f32_16x16x32_bf16 v[140:143],a[152:155],v[192:195],v[140:143]
v_exp_f32 v237,v237
v_mfma_f32_16x16x32_bf16 v[140:143],a[148:151],v[160:163],v[140:143]
v_exp_f32 v238,v238
v_mfma_f32_16x16x32_bf16 v[140:143],a[144:147],v[128:131],v[140:143]
v_exp_f32 v239,v239
v_mfma_f32_16x16x32_bf16 v[152:155],a[160:163],v[128:131],v[32:35]
v_cvt_pk_bf16_f32 v234,v236,v237
v_cvt_pk_bf16_f32 v235,v238,v239
v_mfma_f32_16x16x32_bf16 v[152:155],a[164:167],v[160:163],v[152:155]
s_waitcnt lgkmcnt(1)
v_exp_f32 v248,v248
v_mfma_f32_16x16x32_bf16 v[152:155],a[168:171],v[192:195],v[152:155]
v_exp_f32 v249,v249
v_mfma_f32_16x16x32_bf16 v[152:155],a[172:175],v[224:227],v[152:155]
v_exp_f32 v250,v250
v_mfma_f32_16x16x32_bf16 v[156:159],a[188:191],v[224:227],v[32:35]
v_exp_f32 v251,v251
v_mfma_f32_16x16x32_bf16 v[156:159],a[184:187],v[192:195],v[156:159]
v_exp_f32 v252,v252
v_mfma_f32_16x16x32_bf16 v[156:159],a[180:183],v[160:163],v[156:159]
s_waitcnt lgkmcnt(0)
v_exp_f32 v253,v253
v_mfma_f32_16x16x32_bf16 v[156:159],a[176:179],v[128:131],v[156:159]
v_exp_f32 v254,v254
v_mfma_f32_16x16x32_bf16 a[64:67],a[192:195],v[200:203],a[64:67]
buffer_load_dwordx4 v8,s[72:75],s83 offen offset:1024 lds
v_mfma_f32_16x16x32_bf16 a[64:67],a[196:199],v[204:207],a[64:67]
v_exp_f32 v255,v255
v_mfma_f32_16x16x32_bf16 a[68:71],a[204:207],v[204:207],a[68:71]
v_cvt_pk_bf16_f32 v236,v248,v249
v_cvt_pk_bf16_f32 v237,v250,v251
v_mfma_f32_16x16x32_bf16 a[68:71],a[200:203],v[200:203],a[68:71]
v_cvt_pk_bf16_f32 v238,v252,v253
v_cvt_pk_bf16_f32 v239,v254,v255
v_mfma_f32_16x16x32_bf16 a[72:75],a[208:211],v[200:203],a[72:75]
buffer_store_dwordx4 v[100:103],v6,s[36:39],s86 offen offset:1024 sc1
v_mfma_f32_16x16x32_bf16 a[72:75],a[212:215],v[204:207],a[72:75]
v_exp_f32 v136,v136
v_mfma_f32_16x16x32_bf16 a[76:79],a[220:223],v[204:207],a[76:79]
v_exp_f32 v137,v137
v_mfma_f32_16x16x32_bf16 a[76:79],a[216:219],v[200:203],a[76:79]
v_exp_f32 v138,v138
v_mfma_f32_16x16x32_bf16 a[80:83],a[224:227],v[200:203],a[80:83]
v_exp_f32 v139,v139
v_mfma_f32_16x16x32_bf16 a[80:83],a[228:231],v[204:207],a[80:83]
v_exp_f32 v140,v140
v_mfma_f32_16x16x32_bf16 a[84:87],a[236:239],v[204:207],a[84:87]
v_exp_f32 v141,v141
v_mfma_f32_16x16x32_bf16 a[84:87],a[232:235],v[200:203],a[84:87]
v_exp_f32 v142,v142
v_mfma_f32_16x16x32_bf16 a[88:91],a[240:243],v[200:203],a[88:91]
v_exp_f32 v143,v143
v_mfma_f32_16x16x32_bf16 a[88:91],a[244:247],v[204:207],a[88:91]
v_exp_f32 v152,v152
v_mfma_f32_16x16x32_bf16 a[92:95],a[252:255],v[204:207],a[92:95]
v_exp_f32 v153,v153
v_mfma_f32_16x16x32_bf16 a[92:95],a[248:251],v[200:203],a[92:95]
v_exp_f32 v154,v154
v_smfmac_f32_16x16x64_bf16 v[56:59],v[28:31],v[200:207],v27
v_cvt_pk_bf16_f32 v136,v136,v137
v_mfma_f32_16x16x32_bf16 v[168:171],a[128:131],v[132:135],v[36:39]
v_exp_f32 v155,v155
v_mfma_f32_16x16x32_bf16 v[168:171],a[132:135],v[164:167],v[168:171]
v_exp_f32 v156,v156
v_mfma_f32_16x16x32_bf16 v[168:171],a[136:139],v[196:199],v[168:171]
v_exp_f32 v157,v157
v_mfma_f32_16x16x32_bf16 v[168:171],a[140:143],v[228:231],v[168:171]
buffer_store_dwordx4 v[104:107],v6,s[36:39],s86 offen offset:2048 sc1
v_mfma_f32_16x16x32_bf16 v[172:175],a[156:159],v[228:231],v[36:39]
v_exp_f32 v158,v158
v_mfma_f32_16x16x32_bf16 v[172:175],a[152:155],v[196:199],v[172:175]
v_exp_f32 v159,v159
v_mfma_f32_16x16x32_bf16 v[172:175],a[148:151],v[164:167],v[172:175]
v_cvt_pk_bf16_f32 v137,v138,v139
v_cvt_pk_bf16_f32 v138,v140,v141
v_mfma_f32_16x16x32_bf16 v[172:175],a[144:147],v[132:135],v[172:175]
buffer_load_dwordx4 v9,s[72:75],s83 offen offset:2048 lds
v_mfma_f32_16x16x32_bf16 v[184:187],a[160:163],v[132:135],v[36:39]
v_cvt_pk_bf16_f32 v139,v142,v143
v_cvt_pk_bf16_f32 v140,v152,v153
v_mfma_f32_16x16x32_bf16 v[184:187],a[164:167],v[164:167],v[184:187]
v_cvt_pk_bf16_f32 v141,v154,v155
v_cvt_pk_bf16_f32 v142,v156,v157
v_mfma_f32_16x16x32_bf16 v[184:187],a[168:171],v[196:199],v[184:187]
v_exp_f32 v168,v168
v_mfma_f32_16x16x32_bf16 v[184:187],a[172:175],v[228:231],v[184:187]
v_exp_f32 v169,v169
v_mfma_f32_16x16x32_bf16 v[188:191],a[188:191],v[228:231],v[36:39]
v_cvt_pk_bf16_f32 v143,v158,v159
v_cvt_pk_bf16_f32 v168,v168,v169
v_mfma_f32_16x16x32_bf16 v[188:191],a[184:187],v[196:199],v[188:191]
v_exp_f32 v170,v170
v_mfma_f32_16x16x32_bf16 v[188:191],a[180:183],v[164:167],v[188:191]
v_exp_f32 v171,v171
v_mfma_f32_16x16x32_bf16 v[188:191],a[176:179],v[132:135],v[188:191]
v_exp_f32 v172,v172
v_mfma_f32_16x16x32_bf16 a[96:99],a[192:195],v[232:235],a[96:99]
ds_read_b64_tr_b16 a[192:193],v2
ds_read_b64_tr_b16 a[194:195],v2 offset:4096
v_mfma_f32_16x16x32_bf16 a[96:99],a[196:199],v[236:239],a[96:99]
ds_read_b64_tr_b16 a[196:197],v2 offset:8192
v_cvt_pk_bf16_f32 v169,v170,v171
v_mfma_f32_16x16x32_bf16 a[100:103],a[204:207],v[236:239],a[100:103]
v_exp_f32 v173,v173
v_mfma_f32_16x16x32_bf16 a[100:103],a[200:203],v[232:235],a[100:103]
buffer_store_dwordx4 v[108:111],v6,s[36:39],s86 offen offset:3072 sc1
v_mfma_f32_16x16x32_bf16 a[104:107],a[208:211],v[232:235],a[104:107]
s_add_u32 s86,s55,147456
ds_read_b64_tr_b16 a[198:199],v2 offset:12288
ds_read_b64_tr_b16 a[204:205],v3 offset:8192
v_mfma_f32_16x16x32_bf16 a[104:107],a[212:215],v[236:239],a[104:107]
s_max_i32 s86,s86,s100
ds_read_b64_tr_b16 a[206:207],v3 offset:12288
v_cvt_pk_bf16_f32 v170,v172,v173
v_mfma_f32_16x16x32_bf16 a[108:111],a[220:223],v[236:239],a[108:111]
v_exp_f32 v174,v174
v_mfma_f32_16x16x32_bf16 a[108:111],a[216:219],v[232:235],a[108:111]
buffer_load_dwordx4 v10,s[72:75],s83 offen offset:3072 lds
v_mfma_f32_16x16x32_bf16 a[112:115],a[224:227],v[232:235],a[112:115]
ds_read_b64_tr_b16 a[200:201],v3
ds_read_b64_tr_b16 a[202:203],v3 offset:4096
v_mfma_f32_16x16x32_bf16 a[112:115],a[228:231],v[236:239],a[112:115]
v_exp_f32 v175,v175
v_mfma_f32_16x16x32_bf16 a[116:119],a[236:239],v[236:239],a[116:119]
ds_read_b64_tr_b16 a[208:209],v2 offset:256
ds_read_b64_tr_b16 a[210:211],v2 offset:4352
v_mfma_f32_16x16x32_bf16 a[116:119],a[232:235],v[232:235],a[116:119]
ds_read_b64_tr_b16 a[212:213],v2 offset:8448
v_cvt_pk_bf16_f32 v171,v174,v175
v_mfma_f32_16x16x32_bf16 a[120:123],a[240:243],v[232:235],a[120:123]
ds_read_b64_tr_b16 a[214:215],v2 offset:12544
v_mfma_f32_16x16x32_bf16 a[120:123],a[244:247],v[236:239],a[120:123]
ds_read_b64_tr_b16 a[220:221],v3 offset:8448
v_mfma_f32_16x16x32_bf16 a[124:127],a[252:255],v[236:239],a[124:127]
ds_read_b64_tr_b16 a[222:223],v3 offset:12544
ds_read_b64_tr_b16 a[216:217],v3 offset:256
v_mfma_f32_16x16x32_bf16 a[124:127],a[248:251],v[232:235],a[124:127]
ds_read_b64_tr_b16 a[218:219],v3 offset:4352
v_smfmac_f32_16x16x64_bf16 v[60:63],v[28:31],v[232:239],v27
ds_read_b64_tr_b16 a[224:225],v2 offset:512
v_mfma_f32_16x16x32_bf16 v[200:203],a[128:131],v[144:147],v[40:43]
ds_read_b64_tr_b16 a[226:227],v2 offset:4608
v_mfma_f32_16x16x32_bf16 v[200:203],a[132:135],v[176:179],v[200:203]
)A"
R"A(ds_read_b64_tr_b16 a[228:229],v2 offset:8704
ds_read_b64_tr_b16 a[230:231],v2 offset:12800
v_mfma_f32_16x16x32_bf16 v[200:203],a[136:139],v[208:211],v[200:203]
buffer_store_dwordx4 v[112:115],v6,s[36:39],s87 offen sc1
v_mfma_f32_16x16x32_bf16 v[200:203],a[140:143],v[240:243],v[200:203]
ds_read_b64_tr_b16 a[236:237],v3 offset:8704
ds_read_b64_tr_b16 a[238:239],v3 offset:12800
v_mfma_f32_16x16x32_bf16 v[204:207],a[156:159],v[240:243],v[40:43]
ds_read_b64_tr_b16 a[232:233],v3 offset:512
v_mfma_f32_16x16x32_bf16 v[204:207],a[152:155],v[208:211],v[204:207]
ds_read_b64_tr_b16 a[234:235],v3 offset:4608
v_mfma_f32_16x16x32_bf16 v[204:207],a[148:151],v[176:179],v[204:207]
ds_read_b64_tr_b16 a[240:241],v2 offset:768
v_mfma_f32_16x16x32_bf16 v[204:207],a[144:147],v[144:147],v[204:207]
s_addk_i32 m0,4096
ds_read_b64_tr_b16 a[242:243],v2 offset:4864
ds_read_b64_tr_b16 a[244:245],v2 offset:8960
v_mfma_f32_16x16x32_bf16 v[216:219],a[160:163],v[144:147],v[40:43]
ds_read_b64_tr_b16 a[246:247],v2 offset:13056
v_add_u32 v2,s48,v2
v_mfma_f32_16x16x32_bf16 v[216:219],a[164:167],v[176:179],v[216:219]
ds_read_b64_tr_b16 a[252:253],v3 offset:8960
v_mfma_f32_16x16x32_bf16 v[216:219],a[168:171],v[208:211],v[216:219]
buffer_load_dwordx4 v11,s[72:75],s83 offen lds
v_mfma_f32_16x16x32_bf16 v[216:219],a[172:175],v[240:243],v[216:219]
ds_read_b64_tr_b16 a[254:255],v3 offset:13056
ds_read_b64_tr_b16 a[248:249],v3 offset:768
v_mfma_f32_16x16x32_bf16 v[220:223],a[188:191],v[240:243],v[40:43]
ds_read_b64_tr_b16 a[250:251],v3 offset:4864
v_add_u32 v3,s48,v3
v_mfma_f32_16x16x32_bf16 v[220:223],a[184:187],v[208:211],v[220:223]
v_exp_f32 v184,v184
v_mfma_f32_16x16x32_bf16 v[220:223],a[180:183],v[176:179],v[220:223]
v_exp_f32 v185,v185
v_mfma_f32_16x16x32_bf16 v[220:223],a[176:179],v[144:147],v[220:223]
s_waitcnt lgkmcnt(5)
v_exp_f32 v186,v186
v_mfma_f32_16x16x32_bf16 a[0:3],a[192:195],v[136:139],a[0:3]
v_exp_f32 v187,v187
v_mfma_f32_16x16x32_bf16 a[0:3],a[196:199],v[140:143],a[0:3]
s_add_u32 s80,s71,0
s_and_b32 s80,s80,3
s_lshl_b32 s80,s80,1
v_mfma_f32_16x16x32_bf16 a[4:7],a[204:207],v[140:143],a[4:7]
s_or_b32 s80,s80,1
buffer_store_dwordx4 v[116:119],v6,s[36:39],s87 offen offset:1024 sc1
v_mfma_f32_16x16x32_bf16 a[4:7],a[200:203],v[136:139],a[4:7]
s_mul_i32 s80,s80,163840
s_add_u32 s80,s80,s81
v_cvt_pk_bf16_f32 v172,v184,v185
v_mfma_f32_16x16x32_bf16 a[8:11],a[208:211],v[136:139],a[8:11]
s_add_u32 s83,s80,20480
v_exp_f32 v188,v188
v_mfma_f32_16x16x32_bf16 a[8:11],a[212:215],v[140:143],a[8:11]
v_exp_f32 v189,v189
v_mfma_f32_16x16x32_bf16 a[12:15],a[220:223],v[140:143],a[12:15]
v_exp_f32 v190,v190
v_mfma_f32_16x16x32_bf16 a[12:15],a[216:219],v[136:139],a[12:15]
v_exp_f32 v191,v191
v_mfma_f32_16x16x32_bf16 a[16:19],a[224:227],v[136:139],a[16:19]
v_cvt_pk_bf16_f32 v173,v186,v187
v_cvt_pk_bf16_f32 v174,v188,v189
v_mfma_f32_16x16x32_bf16 a[16:19],a[228:231],v[140:143],a[16:19]
v_exp_f32 v200,v200
v_mfma_f32_16x16x32_bf16 a[20:23],a[236:239],v[140:143],a[20:23]
v_exp_f32 v201,v201
v_mfma_f32_16x16x32_bf16 a[20:23],a[232:235],v[136:139],a[20:23]
v_cvt_pk_bf16_f32 v175,v190,v191
v_cvt_pk_bf16_f32 v200,v200,v201
v_mfma_f32_16x16x32_bf16 a[24:27],a[240:243],v[136:139],a[24:27]
s_waitcnt lgkmcnt(0)
v_exp_f32 v202,v202
v_mfma_f32_16x16x32_bf16 a[24:27],a[244:247],v[140:143],a[24:27]
v_exp_f32 v203,v203
v_mfma_f32_16x16x32_bf16 a[28:31],a[252:255],v[140:143],a[28:31]
v_exp_f32 v204,v204
v_mfma_f32_16x16x32_bf16 a[28:31],a[248:251],v[136:139],a[28:31]
v_exp_f32 v205,v205
v_smfmac_f32_16x16x64_bf16 v[48:51],v[28:31],v[136:143],v27
v_cvt_pk_bf16_f32 v201,v202,v203
v_mfma_f32_16x16x32_bf16 v[232:235],a[128:131],v[148:151],v[44:47]
v_exp_f32 v206,v206
v_mfma_f32_16x16x32_bf16 v[232:235],a[132:135],v[180:183],v[232:235]
s_barrier
buffer_store_dwordx4 v[120:123],v6,s[36:39],s87 offen offset:2048 sc1
v_mfma_f32_16x16x32_bf16 v[232:235],a[136:139],v[212:215],v[232:235]
ds_read_b128 a[128:131],v1
v_cvt_pk_bf16_f32 v202,v204,v205
v_mfma_f32_16x16x32_bf16 v[232:235],a[140:143],v[244:247],v[232:235]
v_exp_f32 v207,v207
v_mfma_f32_16x16x32_bf16 v[236:239],a[156:159],v[244:247],v[44:47]
s_add_u32 m0,s82,0
ds_read_b128 a[132:135],v1 offset:256
v_cvt_pk_bf16_f32 v203,v206,v207
v_mfma_f32_16x16x32_bf16 v[236:239],a[152:155],v[212:215],v[236:239]
v_exp_f32 v216,v216
v_mfma_f32_16x16x32_bf16 v[236:239],a[148:151],v[180:183],v[236:239]
v_exp_f32 v217,v217
v_mfma_f32_16x16x32_bf16 v[236:239],a[144:147],v[148:151],v[236:239]
buffer_load_dwordx4 v7,s[72:75],s80 offen lds
v_mfma_f32_16x16x32_bf16 v[248:251],a[160:163],v[148:151],v[44:47]
ds_read_b128 a[136:139],v1 offset:512
v_cvt_pk_bf16_f32 v204,v216,v217
v_mfma_f32_16x16x32_bf16 v[248:251],a[164:167],v[180:183],v[248:251]
v_exp_f32 v218,v218
v_mfma_f32_16x16x32_bf16 v[248:251],a[168:171],v[212:215],v[248:251]
ds_read_b128 a[140:143],v1 offset:768
v_mfma_f32_16x16x32_bf16 v[248:251],a[172:175],v[244:247],v[248:251]
v_exp_f32 v219,v219
v_mfma_f32_16x16x32_bf16 v[252:255],a[188:191],v[244:247],v[44:47]
ds_read_b128 a[156:159],v1 offset:4864
v_cvt_pk_bf16_f32 v205,v218,v219
v_mfma_f32_16x16x32_bf16 v[252:255],a[184:187],v[212:215],v[252:255]
v_exp_f32 v220,v220
v_mfma_f32_16x16x32_bf16 v[252:255],a[180:183],v[180:183],v[252:255]
ds_read_b128 a[152:155],v1 offset:4608
v_mfma_f32_16x16x32_bf16 v[252:255],a[176:179],v[148:151],v[252:255]
v_exp_f32 v221,v221
v_mfma_f32_16x16x32_bf16 a[32:35],a[192:195],v[168:171],a[32:35]
buffer_store_dwordx4 v[124:127],v6,s[36:39],s87 offen offset:3072 sc1
v_mfma_f32_16x16x32_bf16 a[32:35],a[196:199],v[172:175],a[32:35]
s_add_u32 s87,s55,151552
ds_read_b128 a[148:151],v1 offset:4352
v_cvt_pk_bf16_f32 v206,v220,v221
v_mfma_f32_16x16x32_bf16 a[36:39],a[204:207],v[172:175],a[36:39]
s_max_i32 s87,s87,s101
v_exp_f32 v222,v222
v_mfma_f32_16x16x32_bf16 a[36:39],a[200:203],v[168:171],a[36:39]
ds_read_b128 a[144:147],v1 offset:4096
v_mfma_f32_16x16x32_bf16 a[40:43],a[208:211],v[168:171],a[40:43]
buffer_load_dwordx4 v8,s[72:75],s80 offen offset:1024 lds
v_mfma_f32_16x16x32_bf16 a[40:43],a[212:215],v[172:175],a[40:43]
ds_read_b128 a[160:163],v1 offset:8192
v_mfma_f32_16x16x32_bf16 a[44:47],a[220:223],v[172:175],a[44:47]
v_exp_f32 v223,v223
v_mfma_f32_16x16x32_bf16 a[44:47],a[216:219],v[168:171],a[44:47]
ds_read_b128 a[164:167],v1 offset:8448
v_cvt_pk_bf16_f32 v207,v222,v223
v_mfma_f32_16x16x32_bf16 a[48:51],a[224:227],v[168:171],a[48:51]
v_exp_f32 v232,v232
v_mfma_f32_16x16x32_bf16 a[48:51],a[228:231],v[172:175],a[48:51]
ds_read_b128 a[168:171],v1 offset:8704
v_mfma_f32_16x16x32_bf16 a[52:55],a[236:239],v[172:175],a[52:55]
v_exp_f32 v233,v233
v_mfma_f32_16x16x32_bf16 a[52:55],a[232:235],v[168:171],a[52:55]
ds_read_b128 a[172:175],v1 offset:8960
v_cvt_pk_bf16_f32 v232,v232,v233
v_mfma_f32_16x16x32_bf16 a[56:59],a[240:243],v[168:171],a[56:59]
v_exp_f32 v234,v234
v_mfma_f32_16x16x32_bf16 a[56:59],a[244:247],v[172:175],a[56:59]
buffer_load_dwordx4 v9,s[72:75],s80 offen offset:2048 lds
v_mfma_f32_16x16x32_bf16 a[60:63],a[252:255],v[172:175],a[60:63]
ds_read_b128 a[188:191],v1 offset:13056
v_mfma_f32_16x16x32_bf16 a[60:63],a[248:251],v[168:171],a[60:63]
v_exp_f32 v235,v235
v_smfmac_f32_16x16x64_bf16 v[52:55],v[28:31],v[168:175],v27
s_waitcnt lgkmcnt(3)
ds_read_b128 a[184:187],v1 offset:12800
v_mfma_f32_16x16x32_bf16 v[136:139],a[128:131],v[128:131],v[32:35]
v_exp_f32 v236,v236
v_mfma_f32_16x16x32_bf16 v[136:139],a[132:135],v[160:163],v[136:139]
ds_read_b128 a[180:183],v1 offset:12544
v_cvt_pk_bf16_f32 v233,v234,v235
v_mfma_f32_16x16x32_bf16 v[136:139],a[136:139],v[192:195],v[136:139]
v_exp_f32 v237,v237
v_mfma_f32_16x16x32_bf16 v[136:139],a[140:143],v[224:227],v[136:139]
ds_read_b128 a[176:179],v1 offset:12288
v_cvt_pk_bf16_f32 v234,v236,v237
v_mfma_f32_16x16x32_bf16 v[140:143],a[156:159],v[224:227],v[32:35]
v_exp_f32 v238,v238
v_mfma_f32_16x16x32_bf16 v[140:143],a[152:155],v[192:195],v[140:143]
v_exp_f32 v239,v239
v_mfma_f32_16x16x32_bf16 v[140:143],a[148:151],v[160:163],v[140:143]
v_exp_f32 v248,v248
v_mfma_f32_16x16x32_bf16 v[140:143],a[144:147],v[128:131],v[140:143]
buffer_load_dwordx4 v10,s[72:75],s80 offen offset:3072 lds
v_mfma_f32_16x16x32_bf16 v[152:155],a[160:163],v[128:131],v[32:35]
v_exp_f32 v249,v249
v_mfma_f32_16x16x32_bf16 v[152:155],a[164:167],v[160:163],v[152:155]
s_waitcnt lgkmcnt(1)
v_cvt_pk_bf16_f32 v235,v238,v239
v_cvt_pk_bf16_f32 v236,v248,v249
v_mfma_f32_16x16x32_bf16 v[152:155],a[168:171],v[192:195],v[152:155]
v_exp_f32 v250,v250
v_mfma_f32_16x16x32_bf16 v[152:155],a[172:175],v[224:227],v[152:155]
v_exp_f32 v251,v251
v_mfma_f32_16x16x32_bf16 v[156:159],a[188:191],v[224:227],v[32:35]
v_exp_f32 v252,v252
v_mfma_f32_16x16x32_bf16 v[156:159],a[184:187],v[192:195],v[156:159]
v_exp_f32 v253,v253
v_mfma_f32_16x16x32_bf16 v[156:159],a[180:183],v[160:163],v[156:159]
s_waitcnt lgkmcnt(0)
v_exp_f32 v254,v254
v_mfma_f32_16x16x32_bf16 v[156:159],a[176:179],v[128:131],v[156:159]
s_addk_i32 m0,4096
v_exp_f32 v255,v255
v_mfma_f32_16x16x32_bf16 a[64:67],a[192:195],v[200:203],a[64:67]
v_cvt_pk_bf16_f32 v237,v250,v251
v_cvt_pk_bf16_f32 v238,v252,v253
v_mfma_f32_16x16x32_bf16 a[64:67],a[196:199],v[204:207],a[64:67]
v_cvt_pk_bf16_f32 v239,v254,v255
v_bfe_u32 v32,v6,4,2
v_mfma_f32_16x16x32_bf16 a[68:71],a[204:207],v[204:207],a[68:71]
buffer_load_dwordx4 v11,s[72:75],s80 offen lds
v_mfma_f32_16x16x32_bf16 a[68:71],a[200:203],v[200:203],a[68:71]
v_exp_f32 v136,v136
v_mfma_f32_16x16x32_bf16 a[72:75],a[208:211],v[200:203],a[72:75]
v_exp_f32 v137,v137
v_mfma_f32_16x16x32_bf16 a[72:75],a[212:215],v[204:207],a[72:75]
v_exp_f32 v138,v138
v_mfma_f32_16x16x32_bf16 a[76:79],a[220:223],v[204:207],a[76:79]
v_exp_f32 v139,v139
v_mfma_f32_16x16x32_bf16 a[76:79],a[216:219],v[200:203],a[76:79]
v_cvt_pk_bf16_f32 v136,v136,v137
v_cvt_pk_bf16_f32 v137,v138,v139
v_mfma_f32_16x16x32_bf16 a[80:83],a[224:227],v[200:203],a[80:83]
v_exp_f32 v140,v140
v_mfma_f32_16x16x32_bf16 a[80:83],a[228:231],v[204:207],a[80:83]
v_exp_f32 v141,v141
v_mfma_f32_16x16x32_bf16 a[84:87],a[236:239],v[204:207],a[84:87]
s_addk_i32 m0,1024
v_exp_f32 v142,v142
v_mfma_f32_16x16x32_bf16 a[84:87],a[232:235],v[200:203],a[84:87]
v_exp_f32 v143,v143
v_mfma_f32_16x16x32_bf16 a[88:91],a[240:243],v[200:203],a[88:91]
v_cvt_pk_bf16_f32 v138,v140,v141
v_cvt_pk_bf16_f32 v139,v142,v143
v_mfma_f32_16x16x32_bf16 a[88:91],a[244:247],v[204:207],a[88:91]
buffer_load_dwordx4 v7,s[72:75],s83 offen lds
v_mfma_f32_16x16x32_bf16 a[92:95],a[252:255],v[204:207],a[92:95]
v_exp_f32 v152,v152
v_mfma_f32_16x16x32_bf16 a[92:95],a[248:251],v[200:203],a[92:95]
v_exp_f32 v153,v153
v_smfmac_f32_16x16x64_bf16 v[56:59],v[28:31],v[200:207],v27
v_cvt_pk_bf16_f32 v140,v152,v153
v_mfma_f32_16x16x32_bf16 v[168:171],a[128:131],v[132:135],v[36:39]
s_lshl_b32 s95,s42,2
s_add_u32 s95,s95,s40
v_bfe_u32 v19,s55,13,1
v_mfma_f32_16x16x32_bf16 v[168:171],a[132:135],v[164:167],v[168:171]
s_lshl_b32 s95,s95,4
v_mov_b32 v18,s68
v_or_b32 v19,s69,v19
v_mfma_f32_16x16x32_bf16 v[168:171],a[136:139],v[196:199],v[168:171]
v_exp_f32 v154,v154
v_mfma_f32_16x16x32_bf16 v[168:171],a[140:143],v[228:231],v[168:171]
v_exp_f32 v155,v155
v_mfma_f32_16x16x32_bf16 v[172:175],a[156:159],v[228:231],v[36:39]
v_exp_f32 v156,v156
v_mfma_f32_16x16x32_bf16 v[172:175],a[152:155],v[196:199],v[172:175]
v_exp_f32 v157,v157
v_mfma_f32_16x16x32_bf16 v[172:175],a[148:151],v[164:167],v[172:175]
v_exp_f32 v158,v158
v_mfma_f32_16x16x32_bf16 v[172:175],a[144:147],v[132:135],v[172:175]
buffer_load_dwordx4 v8,s[72:75],s83 offen offset:1024 lds
v_mfma_f32_16x16x32_bf16 v[184:187],a[160:163],v[132:135],v[36:39]
v_exp_f32 v159,v159
v_mfma_f32_16x16x32_bf16 v[184:187],a[164:167],v[164:167],v[184:187]
v_cvt_pk_bf16_f32 v141,v154,v155
v_cvt_pk_bf16_f32 v142,v156,v157
v_mfma_f32_16x16x32_bf16 v[184:187],a[168:171],v[196:199],v[184:187]
v_cvt_pk_bf16_f32 v143,v158,v159
v_lshl_add_u32 v25,v32,3,v25
v_mfma_f32_16x16x32_bf16 v[184:187],a[172:175],v[228:231],v[184:187]
s_waitcnt vmcnt(6)
s_mov_b64 exec,1
buffer_store_dwordx2 v[18:19],v6,s[64:67],s95 offen sc1
s_mov_b64 exec,-1
v_mfma_f32_16x16x32_bf16 v[188:191],a[188:191],v[228:231],v[36:39]
)A"
R"A(v_exp_f32 v168,v168
v_mfma_f32_16x16x32_bf16 v[188:191],a[184:187],v[196:199],v[188:191]
v_exp_f32 v169,v169
v_mfma_f32_16x16x32_bf16 v[188:191],a[180:183],v[164:167],v[188:191]
v_exp_f32 v170,v170
v_mfma_f32_16x16x32_bf16 v[188:191],a[176:179],v[132:135],v[188:191]
v_exp_f32 v171,v171
v_mfma_f32_16x16x32_bf16 a[96:99],a[192:195],v[232:235],a[96:99]
ds_read_b64_tr_b16 a[192:193],v2
ds_read_b64_tr_b16 a[194:195],v2 offset:4096
v_mfma_f32_16x16x32_bf16 a[96:99],a[196:199],v[236:239],a[96:99]
ds_read_b64_tr_b16 a[196:197],v2 offset:8192
v_cvt_pk_bf16_f32 v168,v168,v169
v_mfma_f32_16x16x32_bf16 a[100:103],a[204:207],v[236:239],a[100:103]
buffer_load_dwordx4 v9,s[72:75],s83 offen offset:2048 lds
v_mfma_f32_16x16x32_bf16 a[100:103],a[200:203],v[232:235],a[100:103]
ds_read_b64_tr_b16 a[198:199],v2 offset:12288
ds_read_b64_tr_b16 a[204:205],v3 offset:8192
v_mfma_f32_16x16x32_bf16 a[104:107],a[208:211],v[232:235],a[104:107]
ds_read_b64_tr_b16 a[206:207],v3 offset:12288
v_cvt_pk_bf16_f32 v169,v170,v171
v_mfma_f32_16x16x32_bf16 a[104:107],a[212:215],v[236:239],a[104:107]
ds_read_b64_tr_b16 a[200:201],v3
v_bfe_u32 v32,v6,8,1
v_mfma_f32_16x16x32_bf16 a[108:111],a[220:223],v[236:239],a[108:111]
ds_read_b64_tr_b16 a[202:203],v3 offset:4096
v_lshlrev_b32 v32,5,v32
v_mfma_f32_16x16x32_bf16 a[108:111],a[216:219],v[232:235],a[108:111]
ds_read_b64_tr_b16 a[208:209],v2 offset:256
ds_read_b64_tr_b16 a[210:211],v2 offset:4352
v_mfma_f32_16x16x32_bf16 a[112:115],a[224:227],v[232:235],a[112:115]
ds_read_b64_tr_b16 a[212:213],v2 offset:8448
v_add_u32 v1,v25,v32
v_mfma_f32_16x16x32_bf16 a[112:115],a[228:231],v[236:239],a[112:115]
ds_read_b64_tr_b16 a[214:215],v2 offset:12544
v_add_u32 v1,81920,v1
v_mfma_f32_16x16x32_bf16 a[116:119],a[236:239],v[236:239],a[116:119]
v_exp_f32 v172,v172
v_mfma_f32_16x16x32_bf16 a[116:119],a[232:235],v[232:235],a[116:119]
ds_read_b64_tr_b16 a[220:221],v3 offset:8448
ds_read_b64_tr_b16 a[222:223],v3 offset:12544
v_mfma_f32_16x16x32_bf16 a[120:123],a[240:243],v[232:235],a[120:123]
s_cmp_ge_u32 0,s71
s_cselect_b32 s80,2,0
ds_read_b64_tr_b16 a[216:217],v3 offset:256
v_mfma_f32_16x16x32_bf16 a[120:123],a[244:247],v[236:239],a[120:123]
s_mul_i32 s80,s80,163840
buffer_load_dwordx4 v10,s[72:75],s83 offen offset:3072 lds
v_mfma_f32_16x16x32_bf16 a[124:127],a[252:255],v[236:239],a[124:127]
s_add_u32 s80,s80,s81
ds_read_b64_tr_b16 a[218:219],v3 offset:4352
ds_read_b64_tr_b16 a[224:225],v2 offset:512
v_mfma_f32_16x16x32_bf16 a[124:127],a[248:251],v[232:235],a[124:127]
ds_read_b64_tr_b16 a[226:227],v2 offset:4608
v_smfmac_f32_16x16x64_bf16 v[60:63],v[28:31],v[232:239],v27
ds_read_b64_tr_b16 a[228:229],v2 offset:8704
v_mfma_f32_16x16x32_bf16 v[200:203],a[128:131],v[144:147],v[40:43]
ds_read_b64_tr_b16 a[230:231],v2 offset:12800
v_mfma_f32_16x16x32_bf16 v[200:203],a[132:135],v[176:179],v[200:203]
ds_read_b64_tr_b16 a[236:237],v3 offset:8704
ds_read_b64_tr_b16 a[238:239],v3 offset:12800
v_mfma_f32_16x16x32_bf16 v[200:203],a[136:139],v[208:211],v[200:203]
ds_read_b64_tr_b16 a[232:233],v3 offset:512
v_mfma_f32_16x16x32_bf16 v[200:203],a[140:143],v[240:243],v[200:203]
ds_read_b64_tr_b16 a[234:235],v3 offset:4608
v_mfma_f32_16x16x32_bf16 v[204:207],a[156:159],v[240:243],v[40:43]
s_addk_i32 m0,4096
ds_read_b64_tr_b16 a[240:241],v2 offset:768
v_mfma_f32_16x16x32_bf16 v[204:207],a[152:155],v[208:211],v[204:207]
ds_read_b64_tr_b16 a[242:243],v2 offset:4864
ds_read_b64_tr_b16 a[244:245],v2 offset:8960
v_mfma_f32_16x16x32_bf16 v[204:207],a[148:151],v[176:179],v[204:207]
ds_read_b64_tr_b16 a[246:247],v2 offset:13056
v_sub_u32 v2,v25,v32
v_mfma_f32_16x16x32_bf16 v[204:207],a[144:147],v[144:147],v[204:207]
buffer_load_dwordx4 v11,s[72:75],s83 offen lds
v_mfma_f32_16x16x32_bf16 v[216:219],a[160:163],v[144:147],v[40:43]
s_add_u32 s83,s80,20480
ds_read_b64_tr_b16 a[252:253],v3 offset:8960
ds_read_b64_tr_b16 a[254:255],v3 offset:13056
v_mfma_f32_16x16x32_bf16 v[216:219],a[164:167],v[176:179],v[216:219]
ds_read_b64_tr_b16 a[248:249],v3 offset:768
v_add_u32 v2,81920,v2
v_mfma_f32_16x16x32_bf16 v[216:219],a[168:171],v[208:211],v[216:219]
ds_read_b64_tr_b16 a[250:251],v3 offset:4864
v_mfma_f32_16x16x32_bf16 v[216:219],a[172:175],v[240:243],v[216:219]
v_exp_f32 v173,v173
v_mfma_f32_16x16x32_bf16 v[220:223],a[188:191],v[240:243],v[40:43]
v_exp_f32 v174,v174
v_mfma_f32_16x16x32_bf16 v[220:223],a[184:187],v[208:211],v[220:223]
v_exp_f32 v175,v175
v_mfma_f32_16x16x32_bf16 v[220:223],a[180:183],v[176:179],v[220:223]
v_cvt_pk_bf16_f32 v170,v172,v173
v_cvt_pk_bf16_f32 v171,v174,v175
v_mfma_f32_16x16x32_bf16 v[220:223],a[176:179],v[144:147],v[220:223]
s_add_u32 m0,s82,40960
v_exp_f32 v184,v184
s_waitcnt lgkmcnt(4)
v_mfma_f32_16x16x32_bf16 a[0:3],a[192:195],v[136:139],a[0:3]
v_exp_f32 v185,v185
v_mfma_f32_16x16x32_bf16 a[0:3],a[196:199],v[140:143],a[0:3]
v_exp_f32 v186,v186
v_mfma_f32_16x16x32_bf16 a[4:7],a[204:207],v[140:143],a[4:7]
buffer_load_dwordx4 v7,s[72:75],s80 offen lds
v_mfma_f32_16x16x32_bf16 a[4:7],a[200:203],v[136:139],a[4:7]
v_exp_f32 v187,v187
v_mfma_f32_16x16x32_bf16 a[8:11],a[208:211],v[136:139],a[8:11]
v_exp_f32 v188,v188
v_mfma_f32_16x16x32_bf16 a[8:11],a[212:215],v[140:143],a[8:11]
v_exp_f32 v189,v189
v_mfma_f32_16x16x32_bf16 a[12:15],a[220:223],v[140:143],a[12:15]
v_exp_f32 v190,v190
v_mfma_f32_16x16x32_bf16 a[12:15],a[216:219],v[136:139],a[12:15]
v_exp_f32 v191,v191
v_mfma_f32_16x16x32_bf16 a[16:19],a[224:227],v[136:139],a[16:19]
v_cvt_pk_bf16_f32 v172,v184,v185
v_cvt_pk_bf16_f32 v173,v186,v187
v_mfma_f32_16x16x32_bf16 a[16:19],a[228:231],v[140:143],a[16:19]
v_cvt_pk_bf16_f32 v174,v188,v189
v_cvt_pk_bf16_f32 v175,v190,v191
v_mfma_f32_16x16x32_bf16 a[20:23],a[236:239],v[140:143],a[20:23]
v_exp_f32 v200,v200
v_mfma_f32_16x16x32_bf16 a[20:23],a[232:235],v[136:139],a[20:23]
v_exp_f32 v201,v201
v_mfma_f32_16x16x32_bf16 a[24:27],a[240:243],v[136:139],a[24:27]
v_exp_f32 v202,v202
v_mfma_f32_16x16x32_bf16 a[24:27],a[244:247],v[140:143],a[24:27]
s_waitcnt lgkmcnt(0)
buffer_load_dwordx4 v8,s[72:75],s80 offen offset:1024 lds
v_mfma_f32_16x16x32_bf16 a[28:31],a[252:255],v[140:143],a[28:31]
v_exp_f32 v203,v203
v_mfma_f32_16x16x32_bf16 a[28:31],a[248:251],v[136:139],a[28:31]
v_cvt_pk_bf16_f32 v200,v200,v201
v_cvt_pk_bf16_f32 v201,v202,v203
v_smfmac_f32_16x16x64_bf16 v[48:51],v[28:31],v[136:143],v27
s_barrier
v_exp_f32 v204,v204
v_mfma_f32_16x16x32_bf16 v[232:235],a[128:131],v[148:151],v[44:47]
v_exp_f32 v205,v205
v_mfma_f32_16x16x32_bf16 v[232:235],a[132:135],v[180:183],v[232:235]
v_exp_f32 v206,v206
v_mfma_f32_16x16x32_bf16 v[232:235],a[136:139],v[212:215],v[232:235]
v_exp_f32 v207,v207
v_mfma_f32_16x16x32_bf16 v[232:235],a[140:143],v[244:247],v[232:235]
v_cvt_pk_bf16_f32 v202,v204,v205
v_cvt_pk_bf16_f32 v203,v206,v207
v_mfma_f32_16x16x32_bf16 v[236:239],a[156:159],v[244:247],v[44:47]
v_exp_f32 v216,v216
v_mfma_f32_16x16x32_bf16 v[236:239],a[152:155],v[212:215],v[236:239]
v_exp_f32 v217,v217
v_mfma_f32_16x16x32_bf16 v[236:239],a[148:151],v[180:183],v[236:239]
buffer_load_dwordx4 v9,s[72:75],s80 offen offset:2048 lds
v_mfma_f32_16x16x32_bf16 v[236:239],a[144:147],v[148:151],v[236:239]
v_exp_f32 v218,v218
v_mfma_f32_16x16x32_bf16 v[248:251],a[160:163],v[148:151],v[44:47]
v_exp_f32 v219,v219
v_mfma_f32_16x16x32_bf16 v[248:251],a[164:167],v[180:183],v[248:251]
v_exp_f32 v220,v220
v_mfma_f32_16x16x32_bf16 v[248:251],a[168:171],v[212:215],v[248:251]
v_exp_f32 v221,v221
v_mfma_f32_16x16x32_bf16 v[248:251],a[172:175],v[244:247],v[248:251]
v_exp_f32 v222,v222
v_mfma_f32_16x16x32_bf16 v[252:255],a[188:191],v[244:247],v[44:47]
v_exp_f32 v223,v223
v_mfma_f32_16x16x32_bf16 v[252:255],a[184:187],v[212:215],v[252:255]
v_cvt_pk_bf16_f32 v204,v216,v217
v_cvt_pk_bf16_f32 v205,v218,v219
v_mfma_f32_16x16x32_bf16 v[252:255],a[180:183],v[180:183],v[252:255]
v_cvt_pk_bf16_f32 v206,v220,v221
v_cvt_pk_bf16_f32 v207,v222,v223
v_mfma_f32_16x16x32_bf16 v[252:255],a[176:179],v[148:151],v[252:255]
v_exp_f32 v232,v232
v_mfma_f32_16x16x32_bf16 a[32:35],a[192:195],v[168:171],a[32:35]
v_exp_f32 v233,v233
v_mfma_f32_16x16x32_bf16 a[32:35],a[196:199],v[172:175],a[32:35]
buffer_load_dwordx4 v10,s[72:75],s80 offen offset:3072 lds
v_mfma_f32_16x16x32_bf16 a[36:39],a[204:207],v[172:175],a[36:39]
v_exp_f32 v234,v234
v_mfma_f32_16x16x32_bf16 a[36:39],a[200:203],v[168:171],a[36:39]
v_exp_f32 v235,v235
v_mfma_f32_16x16x32_bf16 a[40:43],a[208:211],v[168:171],a[40:43]
v_cvt_pk_bf16_f32 v232,v232,v233
v_add_f32 v48,v49,v48
v_mfma_f32_16x16x32_bf16 a[40:43],a[212:215],v[172:175],a[40:43]
v_exp_f32 v236,v236
v_mfma_f32_16x16x32_bf16 a[44:47],a[220:223],v[172:175],a[44:47]
v_exp_f32 v237,v237
v_mfma_f32_16x16x32_bf16 a[44:47],a[216:219],v[168:171],a[44:47]
v_exp_f32 v238,v238
v_mfma_f32_16x16x32_bf16 a[48:51],a[224:227],v[168:171],a[48:51]
v_exp_f32 v239,v239
v_mfma_f32_16x16x32_bf16 a[48:51],a[228:231],v[172:175],a[48:51]
s_addk_i32 m0,4096
v_cvt_pk_bf16_f32 v233,v234,v235
v_max_f32 v20,0x00800000,v48
v_mfma_f32_16x16x32_bf16 a[52:55],a[236:239],v[172:175],a[52:55]
v_cvt_pk_bf16_f32 v234,v236,v237
v_cvt_pk_bf16_f32 v235,v238,v239
v_mfma_f32_16x16x32_bf16 a[52:55],a[232:235],v[168:171],a[52:55]
v_exp_f32 v248,v248
v_mfma_f32_16x16x32_bf16 a[56:59],a[240:243],v[168:171],a[56:59]
buffer_load_dwordx4 v11,s[72:75],s80 offen lds
v_mfma_f32_16x16x32_bf16 a[56:59],a[244:247],v[172:175],a[56:59]
v_exp_f32 v249,v249
v_mfma_f32_16x16x32_bf16 a[60:63],a[252:255],v[172:175],a[60:63]
v_exp_f32 v250,v250
v_mfma_f32_16x16x32_bf16 a[60:63],a[248:251],v[168:171],a[60:63]
v_exp_f32 v251,v251
v_smfmac_f32_16x16x64_bf16 v[52:55],v[28:31],v[168:175],v27
v_cvt_pk_bf16_f32 v236,v248,v249
v_mfma_f32_16x16x32_bf16 a[64:67],a[192:195],v[200:203],a[64:67]
v_exp_f32 v252,v252
v_mfma_f32_16x16x32_bf16 a[64:67],a[196:199],v[204:207],a[64:67]
v_exp_f32 v253,v253
v_mfma_f32_16x16x32_bf16 a[68:71],a[204:207],v[204:207],a[68:71]
v_exp_f32 v254,v254
v_mfma_f32_16x16x32_bf16 a[68:71],a[200:203],v[200:203],a[68:71]
s_addk_i32 m0,1024
v_exp_f32 v255,v255
v_mfma_f32_16x16x32_bf16 a[72:75],a[208:211],v[200:203],a[72:75]
v_rcp_f32 v20,v20
v_mfma_f32_16x16x32_bf16 a[72:75],a[212:215],v[204:207],a[72:75]
v_cvt_pk_bf16_f32 v237,v250,v251
v_cvt_pk_bf16_f32 v238,v252,v253
v_mfma_f32_16x16x32_bf16 a[76:79],a[220:223],v[204:207],a[76:79]
buffer_load_dwordx4 v7,s[72:75],s83 offen lds
v_mfma_f32_16x16x32_bf16 a[76:79],a[216:219],v[200:203],a[76:79]
ds_read_b64_tr_b16 a[128:129],v1
v_cvt_pk_bf16_f32 v239,v254,v255
v_mfma_f32_16x16x32_bf16 a[80:83],a[224:227],v[200:203],a[80:83]
v_accvgpr_read_b32 v136,a0
v_accvgpr_read_b32 v137,a1
v_accvgpr_read_b32 v138,a2
v_accvgpr_read_b32 v139,a3
v_accvgpr_read_b32 v12,a4
v_accvgpr_read_b32 v13,a5
v_accvgpr_read_b32 v14,a6
v_accvgpr_read_b32 v15,a7
v_pk_mul_f32 v[136:137],v[20:21],v[136:137] op_sel:[0,0] op_sel_hi:[0,1]
v_pk_mul_f32 v[138:139],v[20:21],v[138:139] op_sel:[0,0] op_sel_hi:[0,1]
v_pk_mul_f32 v[12:13],v[20:21],v[12:13] op_sel:[0,0] op_sel_hi:[0,1]
v_pk_mul_f32 v[14:15],v[20:21],v[14:15] op_sel:[0,0] op_sel_hi:[0,1]
v_cvt_pk_bf16_f32 v136,v136,v137
v_cvt_pk_bf16_f32 v137,v138,v139
v_cvt_pk_bf16_f32 v138,v12,v13
v_cvt_pk_bf16_f32 v139,v14,v15
v_mfma_f32_16x16x32_bf16 a[80:83],a[228:231],v[204:207],a[80:83]
ds_read_b64_tr_b16 a[130:131],v1 offset:5120
ds_read_b64_tr_b16 a[132:133],v1 offset:10240
v_mfma_f32_16x16x32_bf16 a[84:87],a[236:239],v[204:207],a[84:87]
ds_read_b64_tr_b16 a[134:135],v1 offset:15360
v_accvgpr_read_b32 v152,a16
v_mfma_f32_16x16x32_bf16 a[84:87],a[232:235],v[200:203],a[84:87]
ds_read_b64_tr_b16 a[136:137],v1 offset:20480
v_accvgpr_read_b32 v153,a17
v_mfma_f32_16x16x32_bf16 a[88:91],a[240:243],v[200:203],a[88:91]
ds_read_b64_tr_b16 a[138:139],v1 offset:25600
v_mul_f32 v152,v20,v152
v_mfma_f32_16x16x32_bf16 a[88:91],a[244:247],v[204:207],a[88:91]
v_accvgpr_read_b32 v140,a8
v_accvgpr_read_b32 v141,a9
v_accvgpr_read_b32 v142,a10
v_accvgpr_read_b32 v143,a11
v_accvgpr_read_b32 v12,a12
v_accvgpr_read_b32 v13,a13
v_accvgpr_read_b32 v14,a14
v_accvgpr_read_b32 v15,a15
v_pk_mul_f32 v[140:141],v[20:21],v[140:141] op_sel:[0,0] op_sel_hi:[0,1]
v_pk_mul_f32 v[142:143],v[20:21],v[142:143] op_sel:[0,0] op_sel_hi:[0,1]
)A"
R"A(v_pk_mul_f32 v[12:13],v[20:21],v[12:13] op_sel:[0,0] op_sel_hi:[0,1]
v_pk_mul_f32 v[14:15],v[20:21],v[14:15] op_sel:[0,0] op_sel_hi:[0,1]
v_cvt_pk_bf16_f32 v140,v140,v141
v_cvt_pk_bf16_f32 v141,v142,v143
v_cvt_pk_bf16_f32 v142,v12,v13
v_cvt_pk_bf16_f32 v143,v14,v15
v_mfma_f32_16x16x32_bf16 a[92:95],a[252:255],v[204:207],a[92:95]
ds_read_b64_tr_b16 a[140:141],v1 offset:30720
ds_read_b64_tr_b16 a[142:143],v1 offset:35840
v_mfma_f32_16x16x32_bf16 a[92:95],a[248:251],v[200:203],a[92:95]
v_mul_f32 v153,v20,v153
v_accvgpr_read_b32 v154,a18
v_smfmac_f32_16x16x64_bf16 v[56:59],v[28:31],v[200:207],v27
v_accvgpr_read_b32 v155,a19
v_mfma_f32_16x16x32_bf16 a[96:99],a[192:195],v[232:235],a[96:99]
buffer_store_dwordx4 v[136:139],v6,s[36:39],s84 offen sc1
v_mfma_f32_16x16x32_bf16 a[96:99],a[196:199],v[236:239],a[96:99]
v_cvt_pk_bf16_f32 v152,v152,v153
v_mul_f32 v154,v20,v154
v_mfma_f32_16x16x32_bf16 a[100:103],a[204:207],v[236:239],a[100:103]
v_mul_f32 v155,v20,v155
v_cvt_pk_bf16_f32 v153,v154,v155
v_mfma_f32_16x16x32_bf16 a[100:103],a[200:203],v[232:235],a[100:103]
v_accvgpr_read_b32 v154,a20
v_accvgpr_read_b32 v155,a21
v_mfma_f32_16x16x32_bf16 a[104:107],a[208:211],v[232:235],a[104:107]
buffer_load_dwordx4 v8,s[72:75],s83 offen offset:1024 lds
v_mfma_f32_16x16x32_bf16 a[104:107],a[212:215],v[236:239],a[104:107]
v_accvgpr_read_b32 v12,a22
v_accvgpr_read_b32 v13,a23
v_mfma_f32_16x16x32_bf16 a[108:111],a[220:223],v[236:239],a[108:111]
v_mul_f32 v154,v20,v154
v_mul_f32 v155,v20,v155
v_mfma_f32_16x16x32_bf16 a[108:111],a[216:219],v[232:235],a[108:111]
v_mul_f32 v12,v20,v12
v_mul_f32 v13,v20,v13
v_mfma_f32_16x16x32_bf16 a[112:115],a[224:227],v[232:235],a[112:115]
v_cvt_pk_bf16_f32 v154,v154,v155
v_cvt_pk_bf16_f32 v155,v12,v13
v_mfma_f32_16x16x32_bf16 a[112:115],a[228:231],v[236:239],a[112:115]
ds_read_b64_tr_b16 a[144:145],v2 offset:32
ds_read_b64_tr_b16 a[146:147],v2 offset:5152
v_mfma_f32_16x16x32_bf16 a[116:119],a[236:239],v[236:239],a[116:119]
ds_read_b64_tr_b16 a[148:149],v2 offset:10272
v_accvgpr_read_b32 v156,a24
v_mfma_f32_16x16x32_bf16 a[116:119],a[232:235],v[232:235],a[116:119]
buffer_load_dwordx4 v9,s[72:75],s83 offen offset:2048 lds
v_mfma_f32_16x16x32_bf16 a[120:123],a[240:243],v[232:235],a[120:123]
ds_read_b64_tr_b16 a[150:151],v2 offset:15392
ds_read_b64_tr_b16 a[152:153],v2 offset:20512
v_mfma_f32_16x16x32_bf16 a[120:123],a[244:247],v[236:239],a[120:123]
ds_read_b64_tr_b16 a[154:155],v2 offset:25632
v_accvgpr_read_b32 v157,a25
v_mfma_f32_16x16x32_bf16 a[124:127],a[252:255],v[236:239],a[124:127]
ds_read_b64_tr_b16 a[156:157],v2 offset:30752
v_mul_f32 v156,v20,v156
v_mfma_f32_16x16x32_bf16 a[124:127],a[248:251],v[232:235],a[124:127]
ds_read_b64_tr_b16 a[158:159],v2 offset:35872
v_mul_f32 v157,v20,v157
v_smfmac_f32_16x16x64_bf16 v[60:63],v[28:31],v[232:239],v27
s_waitcnt lgkmcnt(8)
v_accvgpr_read_b32 v158,a26
v_mfma_f32_16x16x32_bf16 a[192:195],a[128:131],v[64:67],0
s_lshl_b32 s94,s70,4
v_accvgpr_read_b32 v159,a27
v_cvt_pk_bf16_f32 v156,v156,v157
v_mfma_f32_16x16x32_bf16 a[192:195],a[132:135],v[68:71],a[192:195]
s_add_u32 s94,s94,s40
v_mul_f32 v158,v20,v158
v_mul_f32 v159,v20,v159
v_mfma_f32_16x16x32_bf16 a[192:195],a[136:139],v[72:75],a[192:195]
s_lshl_b32 s94,s94,4
buffer_store_dwordx4 v[140:143],v6,s[36:39],s84 offen offset:1024 sc1
v_mfma_f32_16x16x32_bf16 a[192:195],a[140:143],v[76:79],a[192:195]
v_cvt_pk_bf16_f32 v157,v158,v159
v_accvgpr_read_b32 v158,a28
v_mfma_f32_16x16x32_bf16 a[196:199],a[140:143],v[92:95],0
v_accvgpr_read_b32 v159,a29
v_accvgpr_read_b32 v14,a30
v_mfma_f32_16x16x32_bf16 a[196:199],a[136:139],v[88:91],a[196:199]
v_accvgpr_read_b32 v15,a31
v_mul_f32 v158,v20,v158
v_mfma_f32_16x16x32_bf16 a[196:199],a[132:135],v[84:87],a[196:199]
buffer_load_dwordx4 v10,s[72:75],s83 offen offset:3072 lds
v_mfma_f32_16x16x32_bf16 a[196:199],a[128:131],v[80:83],a[196:199]
v_mul_f32 v159,v20,v159
v_mul_f32 v14,v20,v14
v_mfma_f32_16x16x32_bf16 a[200:203],a[128:131],v[96:99],0
v_mul_f32 v15,v20,v15
v_cvt_pk_bf16_f32 v158,v158,v159
v_mfma_f32_16x16x32_bf16 a[200:203],a[132:135],v[100:103],a[200:203]
ds_read_b64_tr_b16 a[160:161],v1 offset:64
v_cvt_pk_bf16_f32 v159,v14,v15
v_mfma_f32_16x16x32_bf16 a[200:203],a[136:139],v[104:107],a[200:203]
ds_read_b64_tr_b16 a[162:163],v1 offset:5184
ds_read_b64_tr_b16 a[164:165],v1 offset:10304
v_mfma_f32_16x16x32_bf16 a[200:203],a[140:143],v[108:111],a[200:203]
ds_read_b64_tr_b16 a[166:167],v1 offset:15424
v_add_f32 v52,v53,v52
v_mfma_f32_16x16x32_bf16 a[204:207],a[140:143],v[124:127],0
ds_read_b64_tr_b16 a[168:169],v1 offset:20544
v_max_f32 v21,0x00800000,v52
v_mfma_f32_16x16x32_bf16 a[204:207],a[136:139],v[120:123],a[204:207]
s_addk_i32 m0,4096
ds_read_b64_tr_b16 a[170:171],v1 offset:25664
v_bfe_u32 v24,v6,4,2
v_mfma_f32_16x16x32_bf16 a[204:207],a[132:135],v[116:119],a[204:207]
ds_read_b64_tr_b16 a[172:173],v1 offset:30784
ds_read_b64_tr_b16 a[174:175],v1 offset:35904
v_mfma_f32_16x16x32_bf16 a[204:207],a[128:131],v[112:115],a[204:207]
s_waitcnt lgkmcnt(8)
v_rcp_f32 v21,v21
v_mfma_f32_16x16x32_bf16 a[208:211],a[144:147],v[64:67],0
buffer_load_dwordx4 v11,s[72:75],s83 offen lds
v_mfma_f32_16x16x32_bf16 a[208:211],a[148:151],v[68:71],a[208:211]
v_accvgpr_read_b32 v168,a32
v_accvgpr_read_b32 v169,a33
v_accvgpr_read_b32 v170,a34
v_accvgpr_read_b32 v171,a35
v_accvgpr_read_b32 v12,a36
v_accvgpr_read_b32 v13,a37
v_accvgpr_read_b32 v14,a38
v_accvgpr_read_b32 v15,a39
v_pk_mul_f32 v[168:169],v[20:21],v[168:169] op_sel:[1,0] op_sel_hi:[1,1]
v_pk_mul_f32 v[170:171],v[20:21],v[170:171] op_sel:[1,0] op_sel_hi:[1,1]
v_pk_mul_f32 v[12:13],v[20:21],v[12:13] op_sel:[1,0] op_sel_hi:[1,1]
v_pk_mul_f32 v[14:15],v[20:21],v[14:15] op_sel:[1,0] op_sel_hi:[1,1]
v_cvt_pk_bf16_f32 v168,v168,v169
v_cvt_pk_bf16_f32 v169,v170,v171
v_cvt_pk_bf16_f32 v170,v12,v13
v_cvt_pk_bf16_f32 v171,v14,v15
v_mfma_f32_16x16x32_bf16 a[208:211],a[152:155],v[72:75],a[208:211]
v_min_u32 v24,2,v24
v_add_u32 v24,s71,v24
v_mfma_f32_16x16x32_bf16 a[208:211],a[156:159],v[76:79],a[208:211]
s_cmp_ge_u32 1,s71
s_cselect_b32 s80,4,2
v_add_u32 v24,1,v24
v_mfma_f32_16x16x32_bf16 a[212:215],a[156:159],v[92:95],0
s_mul_i32 s80,s80,163840
s_add_u32 s80,s80,s81
v_and_b32 v24,3,v24
v_mfma_f32_16x16x32_bf16 a[212:215],a[152:155],v[88:91],a[212:215]
s_add_u32 s83,s80,20480
ds_read_b64_tr_b16 a[176:177],v2 offset:96
v_lshlrev_b32 v24,6,v24
v_mfma_f32_16x16x32_bf16 a[212:215],a[148:151],v[84:87],a[212:215]
buffer_store_dwordx4 v[152:155],v6,s[36:39],s84 offen offset:2048 sc1
v_mfma_f32_16x16x32_bf16 a[212:215],a[144:147],v[80:83],a[212:215]
ds_read_b64_tr_b16 a[178:179],v2 offset:5216
ds_read_b64_tr_b16 a[180:181],v2 offset:10336
v_mfma_f32_16x16x32_bf16 a[216:219],a[144:147],v[96:99],0
ds_read_b64_tr_b16 a[182:183],v2 offset:15456
v_accvgpr_read_b32 v184,a48
v_mfma_f32_16x16x32_bf16 a[216:219],a[148:151],v[100:103],a[216:219]
ds_read_b64_tr_b16 a[184:185],v2 offset:20576
v_accvgpr_read_b32 v185,a49
v_mfma_f32_16x16x32_bf16 a[216:219],a[152:155],v[104:107],a[216:219]
ds_read_b64_tr_b16 a[186:187],v2 offset:25696
v_mul_f32 v184,v21,v184
v_mfma_f32_16x16x32_bf16 a[216:219],a[156:159],v[108:111],a[216:219]
ds_read_b64_tr_b16 a[188:189],v2 offset:30816
ds_read_b64_tr_b16 a[190:191],v2 offset:35936
v_mfma_f32_16x16x32_bf16 a[220:223],a[156:159],v[124:127],0
v_accvgpr_read_b32 v172,a40
v_accvgpr_read_b32 v173,a41
v_accvgpr_read_b32 v174,a42
v_accvgpr_read_b32 v175,a43
v_accvgpr_read_b32 v12,a44
v_accvgpr_read_b32 v13,a45
v_accvgpr_read_b32 v14,a46
v_accvgpr_read_b32 v15,a47
v_pk_mul_f32 v[172:173],v[20:21],v[172:173] op_sel:[1,0] op_sel_hi:[1,1]
v_pk_mul_f32 v[174:175],v[20:21],v[174:175] op_sel:[1,0] op_sel_hi:[1,1]
v_pk_mul_f32 v[12:13],v[20:21],v[12:13] op_sel:[1,0] op_sel_hi:[1,1]
v_pk_mul_f32 v[14:15],v[20:21],v[14:15] op_sel:[1,0] op_sel_hi:[1,1]
v_cvt_pk_bf16_f32 v172,v172,v173
v_cvt_pk_bf16_f32 v173,v174,v175
v_cvt_pk_bf16_f32 v174,v12,v13
v_cvt_pk_bf16_f32 v175,v14,v15
v_mfma_f32_16x16x32_bf16 a[220:223],a[152:155],v[120:123],a[220:223]
v_mul_f32 v185,v21,v185
v_accvgpr_read_b32 v186,a50
v_mfma_f32_16x16x32_bf16 a[220:223],a[148:151],v[116:119],a[220:223]
s_add_u32 m0,s82,122880
v_accvgpr_read_b32 v187,a51
v_cvt_pk_bf16_f32 v184,v184,v185
v_mfma_f32_16x16x32_bf16 a[220:223],a[144:147],v[112:115],a[220:223]
s_waitcnt lgkmcnt(5)
v_mul_f32 v186,v21,v186
v_mul_f32 v187,v21,v187
v_mfma_f32_16x16x32_bf16 a[224:227],a[160:163],v[64:67],0
v_cvt_pk_bf16_f32 v185,v186,v187
v_accvgpr_read_b32 v186,a52
v_mfma_f32_16x16x32_bf16 a[224:227],a[164:167],v[68:71],a[224:227]
buffer_load_dwordx4 v7,s[72:75],s80 offen lds
v_mfma_f32_16x16x32_bf16 a[224:227],a[168:171],v[72:75],a[224:227]
v_accvgpr_read_b32 v187,a53
v_accvgpr_read_b32 v16,a54
v_mfma_f32_16x16x32_bf16 a[224:227],a[172:175],v[76:79],a[224:227]
v_accvgpr_read_b32 v17,a55
v_mul_f32 v186,v21,v186
v_mfma_f32_16x16x32_bf16 a[228:231],a[172:175],v[92:95],0
v_mul_f32 v187,v21,v187
v_mul_f32 v16,v21,v16
v_mfma_f32_16x16x32_bf16 a[228:231],a[168:171],v[88:91],a[228:231]
v_mul_f32 v17,v21,v17
v_cvt_pk_bf16_f32 v186,v186,v187
v_mfma_f32_16x16x32_bf16 a[228:231],a[164:167],v[84:87],a[228:231]
ds_read_b64_tr_b16 a[128:129],v1 offset:128
v_cvt_pk_bf16_f32 v187,v16,v17
v_mfma_f32_16x16x32_bf16 a[228:231],a[160:163],v[80:83],a[228:231]
ds_read_b64_tr_b16 a[130:131],v1 offset:5248
ds_read_b64_tr_b16 a[132:133],v1 offset:10368
v_mfma_f32_16x16x32_bf16 a[232:235],a[160:163],v[96:99],0
buffer_load_dwordx2 v[4:5],v24,s[64:67],s94 offen sc1
v_mfma_f32_16x16x32_bf16 a[232:235],a[164:167],v[100:103],a[232:235]
ds_read_b64_tr_b16 a[134:135],v1 offset:15488
ds_read_b64_tr_b16 a[136:137],v1 offset:20608
v_mfma_f32_16x16x32_bf16 a[232:235],a[168:171],v[104:107],a[232:235]
ds_read_b64_tr_b16 a[138:139],v1 offset:25728
v_accvgpr_read_b32 v188,a56
v_mfma_f32_16x16x32_bf16 a[232:235],a[172:175],v[108:111],a[232:235]
ds_read_b64_tr_b16 a[140:141],v1 offset:30848
v_accvgpr_read_b32 v189,a57
v_mfma_f32_16x16x32_bf16 a[236:239],a[172:175],v[124:127],0
buffer_store_dwordx4 v[156:159],v6,s[36:39],s84 offen offset:3072 sc1
v_mfma_f32_16x16x32_bf16 a[236:239],a[168:171],v[120:123],a[236:239]
ds_read_b64_tr_b16 a[142:143],v1 offset:35968
v_mul_f32 v188,v21,v188
v_mfma_f32_16x16x32_bf16 a[236:239],a[164:167],v[116:119],a[236:239]
v_mul_f32 v189,v21,v189
v_accvgpr_read_b32 v190,a58
v_mfma_f32_16x16x32_bf16 a[236:239],a[160:163],v[112:115],a[236:239]
v_accvgpr_read_b32 v191,a59
v_cvt_pk_bf16_f32 v188,v188,v189
v_mfma_f32_16x16x32_bf16 a[240:243],a[176:179],v[64:67],0
s_waitcnt lgkmcnt(5)
buffer_load_dwordx4 v8,s[72:75],s80 offen offset:1024 lds
v_mfma_f32_16x16x32_bf16 a[240:243],a[180:183],v[68:71],a[240:243]
v_mul_f32 v190,v21,v190
v_mul_f32 v191,v21,v191
v_mfma_f32_16x16x32_bf16 a[240:243],a[184:187],v[72:75],a[240:243]
v_cvt_pk_bf16_f32 v189,v190,v191
v_accvgpr_read_b32 v190,a60
v_mfma_f32_16x16x32_bf16 a[240:243],a[188:191],v[76:79],a[240:243]
v_accvgpr_read_b32 v191,a61
v_accvgpr_read_b32 v12,a62
v_mfma_f32_16x16x32_bf16 a[244:247],a[188:191],v[92:95],0
v_accvgpr_read_b32 v13,a63
v_mul_f32 v190,v21,v190
v_mfma_f32_16x16x32_bf16 a[244:247],a[184:187],v[88:91],a[244:247]
v_mul_f32 v191,v21,v191
v_mul_f32 v12,v21,v12
v_mfma_f32_16x16x32_bf16 a[244:247],a[180:183],v[84:87],a[244:247]
v_mul_f32 v13,v21,v13
v_cvt_pk_bf16_f32 v190,v190,v191
v_mfma_f32_16x16x32_bf16 a[244:247],a[176:179],v[80:83],a[244:247]
ds_read_b64_tr_b16 a[144:145],v2 offset:160
v_cvt_pk_bf16_f32 v191,v12,v13
v_mfma_f32_16x16x32_bf16 a[248:251],a[176:179],v[96:99],0
ds_read_b64_tr_b16 a[146:147],v2 offset:5280
ds_read_b64_tr_b16 a[148:149],v2 offset:10400
v_mfma_f32_16x16x32_bf16 a[248:251],a[180:183],v[100:103],a[248:251]
buffer_load_dwordx4 v9,s[72:75],s80 offen offset:2048 lds
v_mfma_f32_16x16x32_bf16 a[248:251],a[184:187],v[104:107],a[248:251]
ds_read_b64_tr_b16 a[150:151],v2 offset:15520
ds_read_b64_tr_b16 a[152:153],v2 offset:20640
v_mfma_f32_16x16x32_bf16 a[248:251],a[188:191],v[108:111],a[248:251]
ds_read_b64_tr_b16 a[154:155],v2 offset:25760
v_add_f32 v56,v57,v56
v_mfma_f32_16x16x32_bf16 a[252:255],a[188:191],v[124:127],0
ds_read_b64_tr_b16 a[156:157],v2 offset:30880
)A"
R"A(v_max_f32 v22,0x00800000,v56
v_mfma_f32_16x16x32_bf16 a[252:255],a[184:187],v[120:123],a[252:255]
ds_read_b64_tr_b16 a[158:159],v2 offset:36000
v_accvgpr_read_b32 v216,a80
v_mfma_f32_16x16x32_bf16 a[252:255],a[180:183],v[116:119],a[252:255]
s_waitcnt vmcnt(18)
v_rcp_f32 v22,v22
v_mfma_f32_16x16x32_bf16 a[252:255],a[176:179],v[112:115],a[252:255]
s_barrier
buffer_store_dwordx4 v[168:171],v6,s[36:39],s85 offen sc1
v_mfma_f32_16x16x32_bf16 a[0:3],a[128:131],v[64:67],0
v_accvgpr_read_b32 v200,a64
v_accvgpr_read_b32 v201,a65
v_accvgpr_read_b32 v202,a66
v_accvgpr_read_b32 v203,a67
v_accvgpr_read_b32 v12,a68
v_accvgpr_read_b32 v13,a69
v_accvgpr_read_b32 v14,a70
v_accvgpr_read_b32 v15,a71
v_pk_mul_f32 v[200:201],v[22:23],v[200:201] op_sel:[0,0] op_sel_hi:[0,1]
v_pk_mul_f32 v[202:203],v[22:23],v[202:203] op_sel:[0,0] op_sel_hi:[0,1]
v_pk_mul_f32 v[12:13],v[22:23],v[12:13] op_sel:[0,0] op_sel_hi:[0,1]
v_pk_mul_f32 v[14:15],v[22:23],v[14:15] op_sel:[0,0] op_sel_hi:[0,1]
v_cvt_pk_bf16_f32 v200,v200,v201
v_cvt_pk_bf16_f32 v201,v202,v203
v_cvt_pk_bf16_f32 v202,v12,v13
v_cvt_pk_bf16_f32 v203,v14,v15
s_waitcnt lgkmcnt(5)
v_mfma_f32_16x16x32_bf16 a[0:3],a[132:135],v[68:71],a[0:3]
ds_read_b64_tr_b16 a[160:161],v1 offset:192
ds_read_b64_tr_b16 a[162:163],v1 offset:5312
v_mfma_f32_16x16x32_bf16 a[0:3],a[136:139],v[72:75],a[0:3]
ds_read_b64_tr_b16 a[164:165],v1 offset:10432
v_accvgpr_read_b32 v217,a81
v_mfma_f32_16x16x32_bf16 a[0:3],a[140:143],v[76:79],a[0:3]
ds_read_b64_tr_b16 a[166:167],v1 offset:15552
v_mul_f32 v216,v22,v216
v_mfma_f32_16x16x32_bf16 a[4:7],a[140:143],v[92:95],0
buffer_load_dwordx4 v10,s[72:75],s80 offen offset:3072 lds
v_mfma_f32_16x16x32_bf16 a[4:7],a[136:139],v[88:91],a[4:7]
ds_read_b64_tr_b16 a[168:169],v1 offset:20672
ds_read_b64_tr_b16 a[170:171],v1 offset:25792
v_mfma_f32_16x16x32_bf16 a[4:7],a[132:135],v[84:87],a[4:7]
v_accvgpr_read_b32 v204,a72
v_accvgpr_read_b32 v205,a73
v_accvgpr_read_b32 v206,a74
v_accvgpr_read_b32 v207,a75
v_accvgpr_read_b32 v12,a76
v_accvgpr_read_b32 v13,a77
v_accvgpr_read_b32 v14,a78
v_accvgpr_read_b32 v15,a79
v_pk_mul_f32 v[204:205],v[22:23],v[204:205] op_sel:[0,0] op_sel_hi:[0,1]
v_pk_mul_f32 v[206:207],v[22:23],v[206:207] op_sel:[0,0] op_sel_hi:[0,1]
v_pk_mul_f32 v[12:13],v[22:23],v[12:13] op_sel:[0,0] op_sel_hi:[0,1]
v_pk_mul_f32 v[14:15],v[22:23],v[14:15] op_sel:[0,0] op_sel_hi:[0,1]
v_cvt_pk_bf16_f32 v204,v204,v205
v_cvt_pk_bf16_f32 v205,v206,v207
v_cvt_pk_bf16_f32 v206,v12,v13
v_cvt_pk_bf16_f32 v207,v14,v15
v_mfma_f32_16x16x32_bf16 a[4:7],a[128:131],v[80:83],a[4:7]
ds_read_b64_tr_b16 a[172:173],v1 offset:30912
ds_read_b64_tr_b16 a[174:175],v1 offset:36032
v_mfma_f32_16x16x32_bf16 a[8:11],a[128:131],v[96:99],0
v_mul_f32 v217,v22,v217
v_accvgpr_read_b32 v218,a82
v_mfma_f32_16x16x32_bf16 a[8:11],a[132:135],v[100:103],a[8:11]
v_accvgpr_read_b32 v219,a83
v_cvt_pk_bf16_f32 v216,v216,v217
v_mfma_f32_16x16x32_bf16 a[8:11],a[136:139],v[104:107],a[8:11]
v_mul_f32 v218,v22,v218
v_mul_f32 v219,v22,v219
v_mfma_f32_16x16x32_bf16 a[8:11],a[140:143],v[108:111],a[8:11]
s_waitcnt vmcnt(5)
v_cmp_ne_u32 s[90:91],s68,v4
v_and_b32 v4,-2,v5
v_cmp_ne_u32 s[92:93],s69,v4
s_nop 0
s_or_b64 s[90:91],s[90:91],s[92:93]
s_cbranch_scc1 L_fr8_%=
L_fo8_%=:
v_mfma_f32_16x16x32_bf16 a[12:15],a[140:143],v[124:127],0
s_nop 0
v_readlane_b32 s76,v5,0
v_readlane_b32 s77,v5,1
v_readlane_b32 s78,v5,2
v_mfma_f32_16x16x32_bf16 a[12:15],a[136:139],v[120:123],a[12:15]
s_addk_i32 m0,4096
s_and_b32 s77,s77,1
s_lshl_b32 s77,s77,2
v_mfma_f32_16x16x32_bf16 a[12:15],a[132:135],v[116:119],a[12:15]
s_nop 1
s_and_b32 s54,s76,1
v_mfma_f32_16x16x32_bf16 a[12:15],a[128:131],v[112:115],a[12:15]
s_and_b32 s78,s78,1
s_or_b32 s54,s54,s77
s_lshl_b32 s78,s78,4
v_mfma_f32_16x16x32_bf16 a[16:19],a[144:147],v[64:67],0
s_or_b32 s54,s54,s78
buffer_load_dwordx4 v11,s[72:75],s80 offen lds
s_waitcnt lgkmcnt(0)
v_mfma_f32_16x16x32_bf16 a[16:19],a[148:151],v[68:71],a[16:19]
s_mulk_i32 s54,3084
s_lshl_b32 s76,s71,1
s_lshl_b32 s54,s54,s76
v_mfma_f32_16x16x32_bf16 a[16:19],a[152:155],v[72:75],a[16:19]
s_lshr_b32 s54,s54,8
s_cmp_ge_u32 0,s71
s_cselect_b32 s76,2,0
v_mfma_f32_16x16x32_bf16 a[16:19],a[156:159],v[76:79],a[16:19]
s_bitcmp1_b32 s54,s76
s_cselect_b32 s77,8192,0
s_lshl_b32 s76,s76,17
v_mfma_f32_16x16x32_bf16 a[20:23],a[156:159],v[92:95],0
s_add_u32 s76,s76,s77
buffer_store_dwordx4 v[172:175],v6,s[36:39],s85 offen offset:1024 sc1
v_mfma_f32_16x16x32_bf16 a[20:23],a[152:155],v[88:91],a[20:23]
s_add_u32 s77,s76,4096
s_add_u32 s78,s76,16384
s_add_u32 s79,s76,20480
v_mfma_f32_16x16x32_bf16 a[20:23],a[148:151],v[84:87],a[20:23]
s_max_i32 s76,s76,s18
v_cvt_pk_bf16_f32 v217,v218,v219
v_accvgpr_read_b32 v218,a84
v_mfma_f32_16x16x32_bf16 a[20:23],a[144:147],v[80:83],a[20:23]
s_max_i32 s77,s77,s19
v_accvgpr_read_b32 v219,a85
v_accvgpr_read_b32 v14,a86
v_mfma_f32_16x16x32_bf16 a[24:27],a[144:147],v[96:99],0
s_max_i32 s78,s78,s100
v_accvgpr_read_b32 v15,a87
v_mul_f32 v218,v22,v218
v_mfma_f32_16x16x32_bf16 a[24:27],a[148:151],v[100:103],a[24:27]
s_addk_i32 m0,1024
v_mul_f32 v219,v22,v219
v_mul_f32 v14,v22,v14
v_mfma_f32_16x16x32_bf16 a[24:27],a[152:155],v[104:107],a[24:27]
s_max_i32 s79,s79,s101
v_mul_f32 v15,v22,v15
v_cvt_pk_bf16_f32 v218,v218,v219
v_mfma_f32_16x16x32_bf16 a[24:27],a[156:159],v[108:111],a[24:27]
ds_read_b64_tr_b16 a[176:177],v2 offset:224
v_cvt_pk_bf16_f32 v219,v14,v15
v_mfma_f32_16x16x32_bf16 a[28:31],a[156:159],v[124:127],0
buffer_load_dwordx4 v7,s[72:75],s83 offen lds
v_mfma_f32_16x16x32_bf16 a[28:31],a[152:155],v[120:123],a[28:31]
ds_read_b64_tr_b16 a[178:179],v2 offset:5344
ds_read_b64_tr_b16 a[180:181],v2 offset:10464
v_mfma_f32_16x16x32_bf16 a[28:31],a[148:151],v[116:119],a[28:31]
ds_read_b64_tr_b16 a[182:183],v2 offset:15584
v_accvgpr_read_b32 v220,a88
v_mfma_f32_16x16x32_bf16 a[28:31],a[144:147],v[112:115],a[28:31]
ds_read_b64_tr_b16 a[184:185],v2 offset:20704
v_accvgpr_read_b32 v221,a89
v_mfma_f32_16x16x32_bf16 a[32:35],a[160:163],v[64:67],0
buffer_load_dwordx4 v[128:131],v6,s[28:31],s76 offen sc1
v_mfma_f32_16x16x32_bf16 a[32:35],a[164:167],v[68:71],a[32:35]
ds_read_b64_tr_b16 a[186:187],v2 offset:25824
ds_read_b64_tr_b16 a[188:189],v2 offset:30944
v_mfma_f32_16x16x32_bf16 a[32:35],a[168:171],v[72:75],a[32:35]
ds_read_b64_tr_b16 a[190:191],v2 offset:36064
v_mul_f32 v220,v22,v220
v_mfma_f32_16x16x32_bf16 a[32:35],a[172:175],v[76:79],a[32:35]
v_mul_f32 v221,v22,v221
v_accvgpr_read_b32 v222,a90
v_mfma_f32_16x16x32_bf16 a[36:39],a[172:175],v[92:95],0
v_accvgpr_read_b32 v223,a91
v_cvt_pk_bf16_f32 v220,v220,v221
v_mfma_f32_16x16x32_bf16 a[36:39],a[168:171],v[88:91],a[36:39]
v_mul_f32 v222,v22,v222
v_mul_f32 v223,v22,v223
v_mfma_f32_16x16x32_bf16 a[36:39],a[164:167],v[84:87],a[36:39]
buffer_store_dwordx4 v[184:187],v6,s[36:39],s85 offen offset:2048 sc1
v_mfma_f32_16x16x32_bf16 a[36:39],a[160:163],v[80:83],a[36:39]
v_cvt_pk_bf16_f32 v221,v222,v223
v_accvgpr_read_b32 v222,a92
v_mfma_f32_16x16x32_bf16 a[40:43],a[160:163],v[96:99],0
v_accvgpr_read_b32 v223,a93
v_accvgpr_read_b32 v16,a94
v_mfma_f32_16x16x32_bf16 a[40:43],a[164:167],v[100:103],a[40:43]
v_accvgpr_read_b32 v17,a95
v_mul_f32 v222,v22,v222
v_mfma_f32_16x16x32_bf16 a[40:43],a[168:171],v[104:107],a[40:43]
buffer_load_dwordx4 v8,s[72:75],s83 offen offset:1024 lds
v_mfma_f32_16x16x32_bf16 a[40:43],a[172:175],v[108:111],a[40:43]
v_mul_f32 v223,v22,v223
v_mul_f32 v16,v22,v16
v_mfma_f32_16x16x32_bf16 a[44:47],a[172:175],v[124:127],0
v_mul_f32 v17,v22,v17
v_cvt_pk_bf16_f32 v222,v222,v223
v_mfma_f32_16x16x32_bf16 a[44:47],a[168:171],v[120:123],a[44:47]
ds_read_b64_tr_b16 a[128:129],v1 offset:256
v_cvt_pk_bf16_f32 v223,v16,v17
v_mfma_f32_16x16x32_bf16 a[44:47],a[164:167],v[116:119],a[44:47]
buffer_load_dwordx4 v[160:163],v6,s[28:31],s76 offen offset:1024 sc1
v_mfma_f32_16x16x32_bf16 a[44:47],a[160:163],v[112:115],a[44:47]
s_waitcnt lgkmcnt(1)
ds_read_b64_tr_b16 a[130:131],v1 offset:5376
ds_read_b64_tr_b16 a[132:133],v1 offset:10496
v_mfma_f32_16x16x32_bf16 a[48:51],a[176:179],v[64:67],0
ds_read_b64_tr_b16 a[134:135],v1 offset:15616
v_add_f32 v60,v61,v60
v_mfma_f32_16x16x32_bf16 a[48:51],a[180:183],v[68:71],a[48:51]
ds_read_b64_tr_b16 a[136:137],v1 offset:20736
v_max_f32 v23,0x00800000,v60
v_mfma_f32_16x16x32_bf16 a[48:51],a[184:187],v[72:75],a[48:51]
ds_read_b64_tr_b16 a[138:139],v1 offset:25856
v_accvgpr_read_b32 v248,a112
v_mfma_f32_16x16x32_bf16 a[48:51],a[188:191],v[76:79],a[48:51]
buffer_load_dwordx4 v[192:195],v6,s[28:31],s76 offen offset:2048 sc1
v_mfma_f32_16x16x32_bf16 a[52:55],a[188:191],v[92:95],0
ds_read_b64_tr_b16 a[140:141],v1 offset:30976
ds_read_b64_tr_b16 a[142:143],v1 offset:36096
v_mfma_f32_16x16x32_bf16 a[52:55],a[184:187],v[88:91],a[52:55]
v_rcp_f32 v23,v23
v_mfma_f32_16x16x32_bf16 a[52:55],a[180:183],v[84:87],a[52:55]
v_accvgpr_read_b32 v232,a96
v_accvgpr_read_b32 v233,a97
v_accvgpr_read_b32 v234,a98
v_accvgpr_read_b32 v235,a99
v_accvgpr_read_b32 v12,a100
v_accvgpr_read_b32 v13,a101
v_accvgpr_read_b32 v14,a102
v_accvgpr_read_b32 v15,a103
v_pk_mul_f32 v[232:233],v[22:23],v[232:233] op_sel:[1,0] op_sel_hi:[1,1]
v_pk_mul_f32 v[234:235],v[22:23],v[234:235] op_sel:[1,0] op_sel_hi:[1,1]
v_pk_mul_f32 v[12:13],v[22:23],v[12:13] op_sel:[1,0] op_sel_hi:[1,1]
v_pk_mul_f32 v[14:15],v[22:23],v[14:15] op_sel:[1,0] op_sel_hi:[1,1]
v_cvt_pk_bf16_f32 v232,v232,v233
v_cvt_pk_bf16_f32 v233,v234,v235
v_cvt_pk_bf16_f32 v234,v12,v13
v_cvt_pk_bf16_f32 v235,v14,v15
v_mfma_f32_16x16x32_bf16 a[52:55],a[176:179],v[80:83],a[52:55]
buffer_load_dwordx4 v9,s[72:75],s83 offen offset:2048 lds
v_mfma_f32_16x16x32_bf16 a[56:59],a[176:179],v[96:99],0
ds_read_b64_tr_b16 a[144:145],v2 offset:288
ds_read_b64_tr_b16 a[146:147],v2 offset:5408
v_mfma_f32_16x16x32_bf16 a[56:59],a[180:183],v[100:103],a[56:59]
ds_read_b64_tr_b16 a[148:149],v2 offset:10528
v_accvgpr_read_b32 v249,a113
v_mfma_f32_16x16x32_bf16 a[56:59],a[184:187],v[104:107],a[56:59]
ds_read_b64_tr_b16 a[150:151],v2 offset:15648
v_mul_f32 v248,v23,v248
v_mfma_f32_16x16x32_bf16 a[56:59],a[188:191],v[108:111],a[56:59]
buffer_store_dwordx4 v[188:191],v6,s[36:39],s85 offen offset:3072 sc1
v_mfma_f32_16x16x32_bf16 a[60:63],a[188:191],v[124:127],0
ds_read_b64_tr_b16 a[152:153],v2 offset:20768
ds_read_b64_tr_b16 a[154:155],v2 offset:25888
v_mfma_f32_16x16x32_bf16 a[60:63],a[184:187],v[120:123],a[60:63]
v_accvgpr_read_b32 v236,a104
v_accvgpr_read_b32 v237,a105
v_accvgpr_read_b32 v238,a106
v_accvgpr_read_b32 v239,a107
v_accvgpr_read_b32 v12,a108
v_accvgpr_read_b32 v13,a109
v_accvgpr_read_b32 v14,a110
v_accvgpr_read_b32 v15,a111
v_pk_mul_f32 v[236:237],v[22:23],v[236:237] op_sel:[1,0] op_sel_hi:[1,1]
v_pk_mul_f32 v[238:239],v[22:23],v[238:239] op_sel:[1,0] op_sel_hi:[1,1]
v_pk_mul_f32 v[12:13],v[22:23],v[12:13] op_sel:[1,0] op_sel_hi:[1,1]
v_pk_mul_f32 v[14:15],v[22:23],v[14:15] op_sel:[1,0] op_sel_hi:[1,1]
v_cvt_pk_bf16_f32 v236,v236,v237
v_cvt_pk_bf16_f32 v237,v238,v239
v_cvt_pk_bf16_f32 v238,v12,v13
v_cvt_pk_bf16_f32 v239,v14,v15
v_mfma_f32_16x16x32_bf16 a[60:63],a[180:183],v[116:119],a[60:63]
ds_read_b64_tr_b16 a[156:157],v2 offset:31008
ds_read_b64_tr_b16 a[158:159],v2 offset:36128
v_mfma_f32_16x16x32_bf16 a[60:63],a[176:179],v[112:115],a[60:63]
s_waitcnt lgkmcnt(8)
buffer_load_dwordx4 v[224:227],v6,s[28:31],s76 offen offset:3072 sc1
v_mfma_f32_16x16x32_bf16 a[64:67],a[128:131],v[64:67],0
v_mul_f32 v249,v23,v249
v_accvgpr_read_b32 v250,a114
v_mfma_f32_16x16x32_bf16 a[64:67],a[132:135],v[68:71],a[64:67]
v_accvgpr_read_b32 v251,a115
v_cvt_pk_bf16_f32 v248,v248,v249
v_mfma_f32_16x16x32_bf16 a[64:67],a[136:139],v[72:75],a[64:67]
v_mul_f32 v250,v23,v250
v_mul_f32 v251,v23,v251
v_mfma_f32_16x16x32_bf16 a[64:67],a[140:143],v[76:79],a[64:67]
buffer_load_dwordx4 v10,s[72:75],s83 offen offset:3072 lds
v_mfma_f32_16x16x32_bf16 a[68:71],a[140:143],v[92:95],0
v_cvt_pk_bf16_f32 v249,v250,v251
v_accvgpr_read_b32 v250,a116
v_mfma_f32_16x16x32_bf16 a[68:71],a[136:139],v[88:91],a[68:71]
v_accvgpr_read_b32 v251,a117
v_accvgpr_read_b32 v12,a118
v_mfma_f32_16x16x32_bf16 a[68:71],a[132:135],v[84:87],a[68:71]
v_accvgpr_read_b32 v13,a119
)A"
R"A(v_mul_f32 v250,v23,v250
v_mfma_f32_16x16x32_bf16 a[68:71],a[128:131],v[80:83],a[68:71]
buffer_load_dwordx4 v[132:135],v6,s[28:31],s77 offen sc1
v_mfma_f32_16x16x32_bf16 a[72:75],a[128:131],v[96:99],0
v_mul_f32 v251,v23,v251
v_mul_f32 v12,v23,v12
v_mfma_f32_16x16x32_bf16 a[72:75],a[132:135],v[100:103],a[72:75]
s_addk_i32 m0,4096
v_mul_f32 v13,v23,v13
v_cvt_pk_bf16_f32 v250,v250,v251
v_mfma_f32_16x16x32_bf16 a[72:75],a[136:139],v[104:107],a[72:75]
v_cvt_pk_bf16_f32 v251,v12,v13
v_subrev_u32 v1,81920,v1
v_mfma_f32_16x16x32_bf16 a[72:75],a[140:143],v[108:111],a[72:75]
ds_read_b64_tr_b16 a[160:161],v1
ds_read_b64_tr_b16 a[162:163],v1 offset:5120
v_mfma_f32_16x16x32_bf16 a[76:79],a[140:143],v[124:127],0
buffer_load_dwordx4 v11,s[72:75],s83 offen lds
v_mfma_f32_16x16x32_bf16 a[76:79],a[136:139],v[120:123],a[76:79]
ds_read_b64_tr_b16 a[164:165],v1 offset:10240
ds_read_b64_tr_b16 a[166:167],v1 offset:15360
v_mfma_f32_16x16x32_bf16 a[76:79],a[132:135],v[116:119],a[76:79]
ds_read_b64_tr_b16 a[168:169],v1 offset:20480
v_accvgpr_read_b32 v252,a120
v_mfma_f32_16x16x32_bf16 a[76:79],a[128:131],v[112:115],a[76:79]
s_waitcnt lgkmcnt(5)
ds_read_b64_tr_b16 a[170:171],v1 offset:25600
v_accvgpr_read_b32 v253,a121
v_mfma_f32_16x16x32_bf16 a[80:83],a[144:147],v[64:67],0
ds_read_b64_tr_b16 a[172:173],v1 offset:30720
v_cmp_nlt_f32 s[56:57],v48,s63
v_mfma_f32_16x16x32_bf16 a[80:83],a[148:151],v[68:71],a[80:83]
buffer_load_dwordx4 v[164:167],v6,s[28:31],s77 offen offset:1024 sc1
v_mfma_f32_16x16x32_bf16 a[80:83],a[152:155],v[72:75],a[80:83]
ds_read_b64_tr_b16 a[174:175],v1 offset:35840
v_mul_f32 v252,v23,v252
v_mfma_f32_16x16x32_bf16 a[80:83],a[156:159],v[76:79],a[80:83]
v_mul_f32 v253,v23,v253
v_accvgpr_read_b32 v254,a122
v_mfma_f32_16x16x32_bf16 a[84:87],a[156:159],v[92:95],0
v_accvgpr_read_b32 v255,a123
v_cvt_pk_bf16_f32 v252,v252,v253
v_mfma_f32_16x16x32_bf16 a[84:87],a[152:155],v[88:91],a[84:87]
v_mul_f32 v254,v23,v254
v_mul_f32 v255,v23,v255
v_mfma_f32_16x16x32_bf16 a[84:87],a[148:151],v[84:87],a[84:87]
v_cmp_nlt_f32 s[58:59],v52,s63
s_nop 1
s_or_b64 s[56:57],s[56:57],s[58:59]
v_mfma_f32_16x16x32_bf16 a[84:87],a[144:147],v[80:83],a[84:87]
v_cvt_pk_bf16_f32 v253,v254,v255
v_accvgpr_read_b32 v254,a124
v_mfma_f32_16x16x32_bf16 a[88:91],a[144:147],v[96:99],0
v_accvgpr_read_b32 v255,a125
v_accvgpr_read_b32 v14,a126
v_mfma_f32_16x16x32_bf16 a[88:91],a[148:151],v[100:103],a[88:91]
v_cmp_nlt_f32 s[58:59],v56,s63
s_nop 1
s_or_b64 s[56:57],s[56:57],s[58:59]
v_mfma_f32_16x16x32_bf16 a[88:91],a[152:155],v[104:107],a[88:91]
v_accvgpr_read_b32 v15,a127
v_mul_f32 v254,v23,v254
v_mfma_f32_16x16x32_bf16 a[88:91],a[156:159],v[108:111],a[88:91]
v_mul_f32 v255,v23,v255
v_mul_f32 v14,v23,v14
v_mfma_f32_16x16x32_bf16 a[92:95],a[156:159],v[124:127],0
v_mul_f32 v15,v23,v15
v_cvt_pk_bf16_f32 v254,v254,v255
v_mfma_f32_16x16x32_bf16 a[92:95],a[152:155],v[120:123],a[92:95]
v_cmp_nlt_f32 s[58:59],v60,s63
s_nop 1
s_or_b64 s[56:57],s[56:57],s[58:59]
v_mfma_f32_16x16x32_bf16 a[92:95],a[148:151],v[116:119],a[92:95]
buffer_load_dwordx4 v[196:199],v6,s[28:31],s77 offen offset:2048 sc1
v_cvt_pk_bf16_f32 v255,v14,v15
v_mfma_f32_16x16x32_bf16 a[92:95],a[144:147],v[112:115],a[92:95]
s_cmp_lg_u64 s[56:57],0
s_cbranch_scc1 L_slow1_%=
L_sret1_%=:
v_subrev_u32 v2,81920,v2
s_waitcnt lgkmcnt(0)
v_mfma_f32_16x16x32_bf16 a[192:195],a[160:163],v[136:139],a[192:195]
ds_read_b64_tr_b16 a[176:177],v2 offset:32
ds_read_b64_tr_b16 a[178:179],v2 offset:5152
v_mfma_f32_16x16x32_bf16 a[192:195],a[164:167],v[140:143],a[192:195]
s_cmp_ge_u32 2,s71
s_cselect_b32 s80,6,4
ds_read_b64_tr_b16 a[180:181],v2 offset:10272
v_mfma_f32_16x16x32_bf16 a[192:195],a[168:171],v[152:155],a[192:195]
s_mul_i32 s80,s80,163840
buffer_store_dwordx4 v[200:203],v6,s[36:39],s86 offen sc1
v_mfma_f32_16x16x32_bf16 a[192:195],a[172:175],v[156:159],a[192:195]
s_add_u32 s80,s80,s81
ds_read_b64_tr_b16 a[182:183],v2 offset:15392
ds_read_b64_tr_b16 a[184:185],v2 offset:20512
v_mfma_f32_16x16x32_bf16 a[196:199],a[172:175],v[188:191],a[196:199]
s_add_u32 s83,s80,20480
ds_read_b64_tr_b16 a[186:187],v2 offset:25632
v_mfma_f32_16x16x32_bf16 a[196:199],a[168:171],v[184:187],a[196:199]
ds_read_b64_tr_b16 a[188:189],v2 offset:30752
v_mfma_f32_16x16x32_bf16 a[196:199],a[164:167],v[172:175],a[196:199]
buffer_store_dwordx4 v[204:207],v6,s[36:39],s86 offen offset:1024 sc1
v_mfma_f32_16x16x32_bf16 a[196:199],a[160:163],v[168:171],a[196:199]
ds_read_b64_tr_b16 a[190:191],v2 offset:35872
ds_read_b64_tr_b16 a[128:129],v1 offset:64
v_mfma_f32_16x16x32_bf16 a[200:203],a[160:163],v[200:203],a[200:203]
ds_read_b64_tr_b16 a[130:131],v1 offset:5184
v_mfma_f32_16x16x32_bf16 a[200:203],a[164:167],v[204:207],a[200:203]
ds_read_b64_tr_b16 a[132:133],v1 offset:10304
v_mfma_f32_16x16x32_bf16 a[200:203],a[168:171],v[216:219],a[200:203]
buffer_load_dwordx4 v[228:231],v6,s[28:31],s77 offen offset:3072 sc1
v_mfma_f32_16x16x32_bf16 a[200:203],a[172:175],v[220:223],a[200:203]
ds_read_b64_tr_b16 a[134:135],v1 offset:15424
ds_read_b64_tr_b16 a[136:137],v1 offset:20544
v_mfma_f32_16x16x32_bf16 a[204:207],a[172:175],v[252:255],a[204:207]
ds_read_b64_tr_b16 a[138:139],v1 offset:25664
v_mfma_f32_16x16x32_bf16 a[204:207],a[168:171],v[248:251],a[204:207]
ds_read_b64_tr_b16 a[140:141],v1 offset:30784
v_mfma_f32_16x16x32_bf16 a[204:207],a[164:167],v[236:239],a[204:207]
ds_read_b64_tr_b16 a[142:143],v1 offset:35904
v_mfma_f32_16x16x32_bf16 a[204:207],a[160:163],v[232:235],a[204:207]
s_barrier
buffer_load_dwordx4 v[144:147],v6,s[28:31],s78 offen sc1
s_waitcnt lgkmcnt(7)
v_mfma_f32_16x16x32_bf16 a[208:211],a[176:179],v[136:139],a[208:211]
ds_read_b64_tr_b16 a[144:145],v2 offset:96
ds_read_b64_tr_b16 a[146:147],v2 offset:5216
v_mfma_f32_16x16x32_bf16 a[208:211],a[180:183],v[140:143],a[208:211]
ds_read_b64_tr_b16 a[148:149],v2 offset:10336
v_mfma_f32_16x16x32_bf16 a[208:211],a[184:187],v[152:155],a[208:211]
s_add_u32 m0,s82,81920
ds_read_b64_tr_b16 a[150:151],v2 offset:15456
v_mfma_f32_16x16x32_bf16 a[208:211],a[188:191],v[156:159],a[208:211]
ds_read_b64_tr_b16 a[152:153],v2 offset:20576
v_mfma_f32_16x16x32_bf16 a[212:215],a[188:191],v[188:191],a[212:215]
ds_read_b64_tr_b16 a[154:155],v2 offset:25696
ds_read_b64_tr_b16 a[156:157],v2 offset:30816
v_mfma_f32_16x16x32_bf16 a[212:215],a[184:187],v[184:187],a[212:215]
buffer_store_dwordx4 v[216:219],v6,s[36:39],s86 offen offset:2048 sc1
v_mfma_f32_16x16x32_bf16 a[212:215],a[180:183],v[172:175],a[212:215]
ds_read_b64_tr_b16 a[158:159],v2 offset:35936
ds_read_b64_tr_b16 a[160:161],v1 offset:128
v_mfma_f32_16x16x32_bf16 a[212:215],a[176:179],v[168:171],a[212:215]
ds_read_b64_tr_b16 a[162:163],v1 offset:5248
v_mfma_f32_16x16x32_bf16 a[216:219],a[176:179],v[200:203],a[216:219]
ds_read_b64_tr_b16 a[164:165],v1 offset:10368
v_mfma_f32_16x16x32_bf16 a[216:219],a[180:183],v[204:207],a[216:219]
buffer_load_dwordx4 v7,s[72:75],s80 offen lds
v_mfma_f32_16x16x32_bf16 a[216:219],a[184:187],v[216:219],a[216:219]
ds_read_b64_tr_b16 a[166:167],v1 offset:15488
ds_read_b64_tr_b16 a[168:169],v1 offset:20608
v_mfma_f32_16x16x32_bf16 a[216:219],a[188:191],v[220:223],a[216:219]
ds_read_b64_tr_b16 a[170:171],v1 offset:25728
v_mfma_f32_16x16x32_bf16 a[220:223],a[188:191],v[252:255],a[220:223]
ds_read_b64_tr_b16 a[172:173],v1 offset:30848
v_mfma_f32_16x16x32_bf16 a[220:223],a[184:187],v[248:251],a[220:223]
buffer_load_dwordx4 v[176:179],v6,s[28:31],s78 offen offset:1024 sc1
v_mfma_f32_16x16x32_bf16 a[220:223],a[180:183],v[236:239],a[220:223]
ds_read_b64_tr_b16 a[174:175],v1 offset:35968
ds_read_b64_tr_b16 a[180:181],v2 offset:10400
v_mfma_f32_16x16x32_bf16 a[220:223],a[176:179],v[232:235],a[220:223]
s_waitcnt lgkmcnt(7)
ds_read_b64_tr_b16 a[176:177],v2 offset:160
v_mfma_f32_16x16x32_bf16 a[224:227],a[128:131],v[136:139],a[224:227]
ds_read_b64_tr_b16 a[178:179],v2 offset:5280
v_mfma_f32_16x16x32_bf16 a[224:227],a[132:135],v[140:143],a[224:227]
ds_read_b64_tr_b16 a[182:183],v2 offset:15520
v_mfma_f32_16x16x32_bf16 a[224:227],a[136:139],v[152:155],a[224:227]
buffer_load_dwordx4 v[208:211],v6,s[28:31],s78 offen offset:2048 sc1
v_mfma_f32_16x16x32_bf16 a[224:227],a[140:143],v[156:159],a[224:227]
ds_read_b64_tr_b16 a[184:185],v2 offset:20640
ds_read_b64_tr_b16 a[186:187],v2 offset:25760
v_mfma_f32_16x16x32_bf16 a[228:231],a[140:143],v[188:191],a[228:231]
ds_read_b64_tr_b16 a[188:189],v2 offset:30880
v_mfma_f32_16x16x32_bf16 a[228:231],a[136:139],v[184:187],a[228:231]
ds_read_b64_tr_b16 a[190:191],v2 offset:36000
v_mfma_f32_16x16x32_bf16 a[228:231],a[132:135],v[172:175],a[228:231]
buffer_load_dwordx4 v8,s[72:75],s80 offen offset:1024 lds
v_mfma_f32_16x16x32_bf16 a[228:231],a[128:131],v[168:171],a[228:231]
v_mfma_f32_16x16x32_bf16 a[232:235],a[128:131],v[200:203],a[232:235]
v_mfma_f32_16x16x32_bf16 a[232:235],a[132:135],v[204:207],a[232:235]
v_mfma_f32_16x16x32_bf16 a[232:235],a[136:139],v[216:219],a[232:235]
buffer_store_dwordx4 v[220:223],v6,s[36:39],s86 offen offset:3072 sc1
v_mfma_f32_16x16x32_bf16 a[232:235],a[140:143],v[220:223],a[232:235]
v_mfma_f32_16x16x32_bf16 a[236:239],a[140:143],v[252:255],a[236:239]
ds_read_b64_tr_b16 a[140:141],v1 offset:30912
ds_read_b64_tr_b16 a[142:143],v1 offset:36032
v_mfma_f32_16x16x32_bf16 a[236:239],a[136:139],v[248:251],a[236:239]
ds_read_b64_tr_b16 a[136:137],v1 offset:20672
v_mfma_f32_16x16x32_bf16 a[236:239],a[132:135],v[236:239],a[236:239]
buffer_load_dwordx4 v[240:243],v6,s[28:31],s78 offen offset:3072 sc1
v_mfma_f32_16x16x32_bf16 a[236:239],a[128:131],v[232:235],a[236:239]
ds_read_b64_tr_b16 a[128:129],v1 offset:192
ds_read_b64_tr_b16 a[130:131],v1 offset:5312
v_mfma_f32_16x16x32_bf16 a[240:243],a[144:147],v[136:139],a[240:243]
ds_read_b64_tr_b16 a[132:133],v1 offset:10432
v_mfma_f32_16x16x32_bf16 a[240:243],a[148:151],v[140:143],a[240:243]
ds_read_b64_tr_b16 a[134:135],v1 offset:15552
v_mfma_f32_16x16x32_bf16 a[240:243],a[152:155],v[152:155],a[240:243]
ds_read_b64_tr_b16 a[138:139],v1 offset:25792
v_mfma_f32_16x16x32_bf16 a[240:243],a[156:159],v[156:159],a[240:243]
buffer_load_dwordx4 v9,s[72:75],s80 offen offset:2048 lds
v_mfma_f32_16x16x32_bf16 a[244:247],a[156:159],v[188:191],a[244:247]
v_mfma_f32_16x16x32_bf16 a[244:247],a[152:155],v[184:187],a[244:247]
v_mfma_f32_16x16x32_bf16 a[244:247],a[148:151],v[172:175],a[244:247]
v_mfma_f32_16x16x32_bf16 a[244:247],a[144:147],v[168:171],a[244:247]
buffer_load_dwordx4 v[148:151],v6,s[28:31],s79 offen sc1
v_mfma_f32_16x16x32_bf16 a[248:251],a[144:147],v[200:203],a[248:251]
v_mfma_f32_16x16x32_bf16 a[248:251],a[148:151],v[204:207],a[248:251]
v_mfma_f32_16x16x32_bf16 a[248:251],a[152:155],v[216:219],a[248:251]
v_mfma_f32_16x16x32_bf16 a[248:251],a[156:159],v[220:223],a[248:251]
v_mfma_f32_16x16x32_bf16 a[252:255],a[156:159],v[252:255],a[252:255]
ds_read_b64_tr_b16 a[156:157],v2 offset:30944
ds_read_b64_tr_b16 a[158:159],v2 offset:36064
v_mfma_f32_16x16x32_bf16 a[252:255],a[152:155],v[248:251],a[252:255]
buffer_store_dwordx4 v[232:235],v6,s[36:39],s87 offen sc1
v_mfma_f32_16x16x32_bf16 a[252:255],a[148:151],v[236:239],a[252:255]
ds_read_b64_tr_b16 a[148:149],v2 offset:10464
ds_read_b64_tr_b16 a[150:151],v2 offset:15584
v_mfma_f32_16x16x32_bf16 a[252:255],a[144:147],v[232:235],a[252:255]
ds_read_b64_tr_b16 a[144:145],v2 offset:224
v_mfma_f32_16x16x32_bf16 a[0:3],a[160:163],v[136:139],a[0:3]
s_waitcnt lgkmcnt(5)
ds_read_b64_tr_b16 a[146:147],v2 offset:5344
v_mfma_f32_16x16x32_bf16 a[0:3],a[164:167],v[140:143],a[0:3]
buffer_load_dwordx4 v[180:183],v6,s[28:31],s79 offen offset:1024 sc1
v_mfma_f32_16x16x32_bf16 a[0:3],a[168:171],v[152:155],a[0:3]
ds_read_b64_tr_b16 a[152:153],v2 offset:20704
ds_read_b64_tr_b16 a[154:155],v2 offset:25824
v_mfma_f32_16x16x32_bf16 a[0:3],a[172:175],v[156:159],a[0:3]
v_mfma_f32_16x16x32_bf16 a[4:7],a[172:175],v[188:191],a[4:7]
v_mfma_f32_16x16x32_bf16 a[4:7],a[168:171],v[184:187],a[4:7]
buffer_load_dwordx4 v10,s[72:75],s80 offen offset:3072 lds
v_mfma_f32_16x16x32_bf16 a[4:7],a[164:167],v[172:175],a[4:7]
v_mfma_f32_16x16x32_bf16 a[4:7],a[160:163],v[168:171],a[4:7]
v_mfma_f32_16x16x32_bf16 a[8:11],a[160:163],v[200:203],a[8:11]
)A"
R"A(v_mfma_f32_16x16x32_bf16 a[8:11],a[164:167],v[204:207],a[8:11]
v_mfma_f32_16x16x32_bf16 a[8:11],a[168:171],v[216:219],a[8:11]
buffer_load_dwordx4 v[212:215],v6,s[28:31],s79 offen offset:2048 sc1
v_mfma_f32_16x16x32_bf16 a[8:11],a[172:175],v[220:223],a[8:11]
v_mfma_f32_16x16x32_bf16 a[12:15],a[172:175],v[252:255],a[12:15]
ds_read_b64_tr_b16 a[172:173],v1 offset:30976
ds_read_b64_tr_b16 a[174:175],v1 offset:36096
v_mfma_f32_16x16x32_bf16 a[12:15],a[168:171],v[248:251],a[12:15]
ds_read_b64_tr_b16 a[168:169],v1 offset:20736
v_mfma_f32_16x16x32_bf16 a[12:15],a[164:167],v[236:239],a[12:15]
s_addk_i32 m0,4096
ds_read_b64_tr_b16 a[164:165],v1 offset:10496
v_mfma_f32_16x16x32_bf16 a[12:15],a[160:163],v[232:235],a[12:15]
ds_read_b64_tr_b16 a[160:161],v1 offset:256
v_mfma_f32_16x16x32_bf16 a[16:19],a[176:179],v[136:139],a[16:19]
ds_read_b64_tr_b16 a[162:163],v1 offset:5376
ds_read_b64_tr_b16 a[166:167],v1 offset:15616
v_mfma_f32_16x16x32_bf16 a[16:19],a[180:183],v[140:143],a[16:19]
buffer_store_dwordx4 v[236:239],v6,s[36:39],s87 offen offset:1024 sc1
v_mfma_f32_16x16x32_bf16 a[16:19],a[184:187],v[152:155],a[16:19]
ds_read_b64_tr_b16 a[170:171],v1 offset:25856
v_add_u32 v1,40960,v1
v_mfma_f32_16x16x32_bf16 a[16:19],a[188:191],v[156:159],a[16:19]
ds_read_b64_tr_b16 a[96:97],v1 offset:128
ds_read_b64_tr_b16 a[98:99],v1 offset:5248
v_mfma_f32_16x16x32_bf16 a[20:23],a[188:191],v[188:191],a[20:23]
ds_read_b64_tr_b16 a[100:101],v1 offset:10368
v_mfma_f32_16x16x32_bf16 a[20:23],a[184:187],v[184:187],a[20:23]
buffer_load_dwordx4 v11,s[72:75],s80 offen lds
v_mfma_f32_16x16x32_bf16 a[20:23],a[180:183],v[172:175],a[20:23]
ds_read_b64_tr_b16 a[102:103],v1 offset:15488
ds_read_b64_tr_b16 a[104:105],v1 offset:20608
v_mfma_f32_16x16x32_bf16 a[20:23],a[176:179],v[168:171],a[20:23]
ds_read_b64_tr_b16 a[106:107],v1 offset:25728
v_mfma_f32_16x16x32_bf16 a[24:27],a[176:179],v[200:203],a[24:27]
ds_read_b64_tr_b16 a[108:109],v1 offset:30848
v_mfma_f32_16x16x32_bf16 a[24:27],a[180:183],v[204:207],a[24:27]
buffer_load_dwordx4 v[244:247],v6,s[28:31],s79 offen offset:3072 sc1
v_mfma_f32_16x16x32_bf16 a[24:27],a[184:187],v[216:219],a[24:27]
ds_read_b64_tr_b16 a[110:111],v1 offset:35968
v_mfma_f32_16x16x32_bf16 a[24:27],a[188:191],v[220:223],a[24:27]
v_mfma_f32_16x16x32_bf16 a[28:31],a[188:191],v[252:255],a[28:31]
ds_read_b64_tr_b16 a[188:189],v2 offset:31008
ds_read_b64_tr_b16 a[190:191],v2 offset:36128
v_mfma_f32_16x16x32_bf16 a[28:31],a[184:187],v[248:251],a[28:31]
s_addk_i32 m0,1024
ds_read_b64_tr_b16 a[184:185],v2 offset:20768
v_mfma_f32_16x16x32_bf16 a[28:31],a[180:183],v[236:239],a[28:31]
ds_read_b64_tr_b16 a[180:181],v2 offset:10528
v_mfma_f32_16x16x32_bf16 a[28:31],a[176:179],v[232:235],a[28:31]
ds_read_b64_tr_b16 a[176:177],v2 offset:288
v_mfma_f32_16x16x32_bf16 a[32:35],a[128:131],v[136:139],a[32:35]
buffer_load_dwordx4 v7,s[72:75],s83 offen lds
v_mfma_f32_16x16x32_bf16 a[32:35],a[132:135],v[140:143],a[32:35]
ds_read_b64_tr_b16 a[178:179],v2 offset:5408
ds_read_b64_tr_b16 a[182:183],v2 offset:15648
v_mfma_f32_16x16x32_bf16 a[32:35],a[136:139],v[152:155],a[32:35]
ds_read_b64_tr_b16 a[186:187],v2 offset:25888
v_add_u32 v2,40960,v2
v_mfma_f32_16x16x32_bf16 a[32:35],a[140:143],v[156:159],a[32:35]
ds_read_b64_tr_b16 a[112:113],v2 offset:160
v_mfma_f32_16x16x32_bf16 a[36:39],a[140:143],v[188:191],a[36:39]
ds_read_b64_tr_b16 a[114:115],v2 offset:5280
v_mfma_f32_16x16x32_bf16 a[36:39],a[136:139],v[184:187],a[36:39]
buffer_store_dwordx4 v[248:251],v6,s[36:39],s87 offen offset:2048 sc1
v_mfma_f32_16x16x32_bf16 a[36:39],a[132:135],v[172:175],a[36:39]
ds_read_b64_tr_b16 a[116:117],v2 offset:10400
ds_read_b64_tr_b16 a[118:119],v2 offset:15520
v_mfma_f32_16x16x32_bf16 a[36:39],a[128:131],v[168:171],a[36:39]
ds_read_b64_tr_b16 a[120:121],v2 offset:20640
v_mfma_f32_16x16x32_bf16 a[40:43],a[128:131],v[200:203],a[40:43]
ds_read_b64_tr_b16 a[122:123],v2 offset:25760
v_mfma_f32_16x16x32_bf16 a[40:43],a[132:135],v[204:207],a[40:43]
ds_read_b64_tr_b16 a[124:125],v2 offset:30880
v_mfma_f32_16x16x32_bf16 a[40:43],a[136:139],v[216:219],a[40:43]
ds_read_b64_tr_b16 a[126:127],v2 offset:36000
v_mfma_f32_16x16x32_bf16 a[40:43],a[140:143],v[220:223],a[40:43]
v_mfma_f32_16x16x32_bf16 a[44:47],a[140:143],v[252:255],a[44:47]
ds_read_b64_tr_b16 a[140:141],v1 offset:30720
ds_read_b64_tr_b16 a[142:143],v1 offset:35840
v_mfma_f32_16x16x32_bf16 a[44:47],a[136:139],v[248:251],a[44:47]
ds_read_b64_tr_b16 a[136:137],v1 offset:20480
v_mfma_f32_16x16x32_bf16 a[44:47],a[132:135],v[236:239],a[44:47]
ds_read_b64_tr_b16 a[132:133],v1 offset:10240
v_mfma_f32_16x16x32_bf16 a[44:47],a[128:131],v[232:235],a[44:47]
s_waitcnt lgkmcnt(7)
buffer_load_dwordx4 v8,s[72:75],s83 offen offset:1024 lds
v_mfma_f32_16x16x32_bf16 a[48:51],a[144:147],v[136:139],a[48:51]
ds_read_b64_tr_b16 a[128:129],v1
ds_read_b64_tr_b16 a[130:131],v1 offset:5120
v_mfma_f32_16x16x32_bf16 a[48:51],a[148:151],v[140:143],a[48:51]
ds_read_b64_tr_b16 a[134:135],v1 offset:15360
v_mfma_f32_16x16x32_bf16 a[48:51],a[152:155],v[152:155],a[48:51]
ds_read_b64_tr_b16 a[138:139],v1 offset:25600
v_mfma_f32_16x16x32_bf16 a[48:51],a[156:159],v[156:159],a[48:51]
s_cmp_ge_u32 1,s71
s_cselect_b32 s76,4,2
v_mfma_f32_16x16x32_bf16 a[52:55],a[156:159],v[188:191],a[52:55]
s_bitcmp1_b32 s54,s76
s_cselect_b32 s77,8192,0
s_lshl_b32 s76,s76,17
v_mfma_f32_16x16x32_bf16 a[52:55],a[152:155],v[184:187],a[52:55]
s_add_u32 s76,s76,s77
s_add_u32 s77,s76,4096
s_add_u32 s78,s76,16384
v_mfma_f32_16x16x32_bf16 a[52:55],a[148:151],v[172:175],a[52:55]
s_add_u32 s79,s76,20480
s_max_i32 s76,s76,s18
s_max_i32 s77,s77,s19
v_mfma_f32_16x16x32_bf16 a[52:55],a[144:147],v[168:171],a[52:55]
s_max_i32 s78,s78,s100
s_max_i32 s79,s79,s101
v_mfma_f32_16x16x32_bf16 a[56:59],a[144:147],v[200:203],a[56:59]
v_mfma_f32_16x16x32_bf16 a[56:59],a[148:151],v[204:207],a[56:59]
buffer_store_dwordx4 v[252:255],v6,s[36:39],s87 offen offset:3072 sc1
v_mfma_f32_16x16x32_bf16 a[56:59],a[152:155],v[216:219],a[56:59]
v_mfma_f32_16x16x32_bf16 a[56:59],a[156:159],v[220:223],a[56:59]
v_mfma_f32_16x16x32_bf16 a[60:63],a[156:159],v[252:255],a[60:63]
ds_read_b64_tr_b16 a[156:157],v2 offset:30752
ds_read_b64_tr_b16 a[158:159],v2 offset:35872
v_mfma_f32_16x16x32_bf16 a[60:63],a[152:155],v[248:251],a[60:63]
ds_read_b64_tr_b16 a[152:153],v2 offset:20512
v_mfma_f32_16x16x32_bf16 a[60:63],a[148:151],v[236:239],a[60:63]
buffer_load_dwordx4 v9,s[72:75],s83 offen offset:2048 lds
v_mfma_f32_16x16x32_bf16 a[60:63],a[144:147],v[232:235],a[60:63]
ds_read_b64_tr_b16 a[144:145],v2 offset:32
ds_read_b64_tr_b16 a[146:147],v2 offset:5152
v_mfma_f32_16x16x32_bf16 a[64:67],a[160:163],v[136:139],a[64:67]
ds_read_b64_tr_b16 a[148:149],v2 offset:10272
v_mfma_f32_16x16x32_bf16 a[64:67],a[164:167],v[140:143],a[64:67]
ds_read_b64_tr_b16 a[150:151],v2 offset:15392
v_mfma_f32_16x16x32_bf16 a[64:67],a[168:171],v[152:155],a[64:67]
ds_read_b64_tr_b16 a[154:155],v2 offset:25632
v_mfma_f32_16x16x32_bf16 a[64:67],a[172:175],v[156:159],a[64:67]
v_mfma_f32_16x16x32_bf16 a[68:71],a[172:175],v[188:191],a[68:71]
v_mfma_f32_16x16x32_bf16 a[68:71],a[168:171],v[184:187],a[68:71]
v_mfma_f32_16x16x32_bf16 a[68:71],a[164:167],v[172:175],a[68:71]
v_mfma_f32_16x16x32_bf16 a[68:71],a[160:163],v[168:171],a[68:71]
v_mfma_f32_16x16x32_bf16 a[72:75],a[160:163],v[200:203],a[72:75]
v_mfma_f32_16x16x32_bf16 a[72:75],a[164:167],v[204:207],a[72:75]
v_mfma_f32_16x16x32_bf16 a[72:75],a[168:171],v[216:219],a[72:75]
v_mfma_f32_16x16x32_bf16 a[72:75],a[172:175],v[220:223],a[72:75]
buffer_load_dwordx4 v[64:67],v6,s[28:31],s76 offen sc1
v_mfma_f32_16x16x32_bf16 a[76:79],a[172:175],v[252:255],a[76:79]
ds_read_b64_tr_b16 a[172:173],v1 offset:30784
ds_read_b64_tr_b16 a[174:175],v1 offset:35904
v_mfma_f32_16x16x32_bf16 a[76:79],a[168:171],v[248:251],a[76:79]
ds_read_b64_tr_b16 a[168:169],v1 offset:20544
v_mfma_f32_16x16x32_bf16 a[76:79],a[164:167],v[236:239],a[76:79]
ds_read_b64_tr_b16 a[164:165],v1 offset:10304
v_mfma_f32_16x16x32_bf16 a[76:79],a[160:163],v[232:235],a[76:79]
buffer_load_dwordx4 v10,s[72:75],s83 offen offset:3072 lds
v_mfma_f32_16x16x32_bf16 a[80:83],a[176:179],v[136:139],a[80:83]
ds_read_b64_tr_b16 a[160:161],v1 offset:64
ds_read_b64_tr_b16 a[162:163],v1 offset:5184
v_mfma_f32_16x16x32_bf16 a[80:83],a[180:183],v[140:143],a[80:83]
ds_read_b64_tr_b16 a[166:167],v1 offset:15424
v_mfma_f32_16x16x32_bf16 a[80:83],a[184:187],v[152:155],a[80:83]
ds_read_b64_tr_b16 a[170:171],v1 offset:25664
v_mfma_f32_16x16x32_bf16 a[80:83],a[188:191],v[156:159],a[80:83]
buffer_load_dwordx4 v[68:71],v6,s[28:31],s76 offen offset:1024 sc1
v_mfma_f32_16x16x32_bf16 a[84:87],a[188:191],v[188:191],a[84:87]
v_mfma_f32_16x16x32_bf16 a[84:87],a[184:187],v[184:187],a[84:87]
v_mfma_f32_16x16x32_bf16 a[84:87],a[180:183],v[172:175],a[84:87]
v_mfma_f32_16x16x32_bf16 a[84:87],a[176:179],v[168:171],a[84:87]
v_mfma_f32_16x16x32_bf16 a[88:91],a[176:179],v[200:203],a[88:91]
v_mfma_f32_16x16x32_bf16 a[88:91],a[180:183],v[204:207],a[88:91]
s_addk_i32 m0,4096
v_mfma_f32_16x16x32_bf16 a[88:91],a[184:187],v[216:219],a[88:91]
v_mfma_f32_16x16x32_bf16 a[88:91],a[188:191],v[220:223],a[88:91]
buffer_load_dwordx4 v[72:75],v6,s[28:31],s76 offen offset:2048 sc1
v_mfma_f32_16x16x32_bf16 a[92:95],a[188:191],v[252:255],a[92:95]
ds_read_b64_tr_b16 a[188:189],v2 offset:30816
ds_read_b64_tr_b16 a[190:191],v2 offset:35936
v_mfma_f32_16x16x32_bf16 a[92:95],a[184:187],v[248:251],a[92:95]
ds_read_b64_tr_b16 a[184:185],v2 offset:20576
v_mfma_f32_16x16x32_bf16 a[92:95],a[180:183],v[236:239],a[92:95]
s_cmp_ge_u32 0,s71
s_cselect_b32 s80,3,1
s_mul_i32 s80,s80,163840
v_mfma_f32_16x16x32_bf16 a[92:95],a[176:179],v[232:235],a[92:95]
s_add_u32 s80,s80,s81
buffer_load_dwordx4 v11,s[72:75],s83 offen lds
s_waitcnt vmcnt(10) lgkmcnt(3)
v_mfma_f32_16x16x32_bf16 a[192:195],a[128:131],v[128:131],a[192:195]
s_add_u32 s83,s80,20480
ds_read_b64_tr_b16 a[176:177],v2 offset:96
ds_read_b64_tr_b16 a[178:179],v2 offset:5216
v_mfma_f32_16x16x32_bf16 a[192:195],a[132:135],v[160:163],a[192:195]
ds_read_b64_tr_b16 a[180:181],v2 offset:10336
v_mfma_f32_16x16x32_bf16 a[192:195],a[136:139],v[192:195],a[192:195]
ds_read_b64_tr_b16 a[182:183],v2 offset:15456
v_mfma_f32_16x16x32_bf16 a[192:195],a[140:143],v[224:227],a[192:195]
buffer_load_dwordx4 v[76:79],v6,s[28:31],s76 offen offset:3072 sc1
v_mfma_f32_16x16x32_bf16 a[196:199],a[140:143],v[228:231],a[196:199]
ds_read_b64_tr_b16 a[186:187],v2 offset:25696
v_mfma_f32_16x16x32_bf16 a[196:199],a[136:139],v[196:199],a[196:199]
v_mfma_f32_16x16x32_bf16 a[196:199],a[132:135],v[164:167],a[196:199]
v_mfma_f32_16x16x32_bf16 a[196:199],a[128:131],v[132:135],a[196:199]
v_mfma_f32_16x16x32_bf16 a[200:203],a[128:131],v[144:147],a[200:203]
v_mfma_f32_16x16x32_bf16 a[200:203],a[132:135],v[176:179],a[200:203]
v_mfma_f32_16x16x32_bf16 a[200:203],a[136:139],v[208:211],a[200:203]
v_mfma_f32_16x16x32_bf16 a[200:203],a[140:143],v[240:243],a[200:203]
s_lshl_b32 s95,s42,2
buffer_load_dwordx4 v[80:83],v6,s[28:31],s77 offen sc1
v_mfma_f32_16x16x32_bf16 a[204:207],a[140:143],v[244:247],a[204:207]
s_add_u32 s95,s95,s40
s_lshl_b32 s95,s95,4
v_bfe_u32 v19,s55,13,1
v_mfma_f32_16x16x32_bf16 a[204:207],a[136:139],v[212:215],a[204:207]
v_mov_b32 v18,s68
v_or_b32 v19,s69,v19
v_mfma_f32_16x16x32_bf16 a[204:207],a[132:135],v[180:183],a[204:207]
ds_read_b64_tr_b16 a[132:133],v1 offset:10432
ds_read_b64_tr_b16 a[134:135],v1 offset:15552
v_mfma_f32_16x16x32_bf16 a[204:207],a[128:131],v[148:151],a[204:207]
s_barrier
ds_read_b64_tr_b16 a[128:129],v1 offset:192
ds_read_b64_tr_b16 a[130:131],v1 offset:5312
v_mfma_f32_16x16x32_bf16 a[208:211],a[144:147],v[128:131],a[208:211]
ds_read_b64_tr_b16 a[136:137],v1 offset:20672
v_mfma_f32_16x16x32_bf16 a[208:211],a[148:151],v[160:163],a[208:211]
ds_read_b64_tr_b16 a[138:139],v1 offset:25792
v_mfma_f32_16x16x32_bf16 a[208:211],a[152:155],v[192:195],a[208:211]
s_add_u32 m0,s82,0
s_waitcnt vmcnt(8)
s_mov_b64 exec,1
buffer_store_dwordx2 v[18:19],v6,s[64:67],s95 offen offset:1024 sc1
s_mov_b64 exec,-1
v_mfma_f32_16x16x32_bf16 a[208:211],a[156:159],v[224:227],a[208:211]
)A"
R"A(ds_read_b64_tr_b16 a[140:141],v1 offset:30912
ds_read_b64_tr_b16 a[142:143],v1 offset:36032
v_mfma_f32_16x16x32_bf16 a[212:215],a[156:159],v[228:231],a[212:215]
v_mfma_f32_16x16x32_bf16 a[212:215],a[152:155],v[196:199],a[212:215]
v_mfma_f32_16x16x32_bf16 a[212:215],a[148:151],v[164:167],a[212:215]
buffer_load_dwordx4 v[84:87],v6,s[28:31],s77 offen offset:1024 sc1
v_mfma_f32_16x16x32_bf16 a[212:215],a[144:147],v[132:135],a[212:215]
v_mfma_f32_16x16x32_bf16 a[216:219],a[144:147],v[144:147],a[216:219]
v_mfma_f32_16x16x32_bf16 a[216:219],a[148:151],v[176:179],a[216:219]
v_mfma_f32_16x16x32_bf16 a[216:219],a[152:155],v[208:211],a[216:219]
buffer_load_dwordx4 v7,s[72:75],s80 offen lds
v_mfma_f32_16x16x32_bf16 a[216:219],a[156:159],v[240:243],a[216:219]
v_mfma_f32_16x16x32_bf16 a[220:223],a[156:159],v[244:247],a[220:223]
ds_read_b64_tr_b16 a[156:157],v2 offset:30944
ds_read_b64_tr_b16 a[158:159],v2 offset:36064
v_mfma_f32_16x16x32_bf16 a[220:223],a[152:155],v[212:215],a[220:223]
ds_read_b64_tr_b16 a[152:153],v2 offset:20704
v_mfma_f32_16x16x32_bf16 a[220:223],a[148:151],v[180:183],a[220:223]
buffer_load_dwordx4 v[88:91],v6,s[28:31],s77 offen offset:2048 sc1
v_mfma_f32_16x16x32_bf16 a[220:223],a[144:147],v[148:151],a[220:223]
ds_read_b64_tr_b16 a[144:145],v2 offset:224
ds_read_b64_tr_b16 a[146:147],v2 offset:5344
v_mfma_f32_16x16x32_bf16 a[224:227],a[160:163],v[128:131],a[224:227]
ds_read_b64_tr_b16 a[148:149],v2 offset:10464
v_mfma_f32_16x16x32_bf16 a[224:227],a[164:167],v[160:163],a[224:227]
ds_read_b64_tr_b16 a[150:151],v2 offset:15584
v_mfma_f32_16x16x32_bf16 a[224:227],a[168:171],v[192:195],a[224:227]
buffer_load_dwordx4 v[92:95],v6,s[28:31],s77 offen offset:3072 sc1
v_mfma_f32_16x16x32_bf16 a[224:227],a[172:175],v[224:227],a[224:227]
ds_read_b64_tr_b16 a[154:155],v2 offset:25824
v_mfma_f32_16x16x32_bf16 a[228:231],a[172:175],v[228:231],a[228:231]
v_mfma_f32_16x16x32_bf16 a[228:231],a[168:171],v[196:199],a[228:231]
v_mfma_f32_16x16x32_bf16 a[228:231],a[164:167],v[164:167],a[228:231]
buffer_load_dwordx4 v8,s[72:75],s80 offen offset:1024 lds
v_mfma_f32_16x16x32_bf16 a[228:231],a[160:163],v[132:135],a[228:231]
v_mfma_f32_16x16x32_bf16 a[232:235],a[160:163],v[144:147],a[232:235]
v_mfma_f32_16x16x32_bf16 a[232:235],a[164:167],v[176:179],a[232:235]
v_mfma_f32_16x16x32_bf16 a[232:235],a[168:171],v[208:211],a[232:235]
buffer_load_dwordx4 v[96:99],v6,s[28:31],s78 offen sc1
v_mfma_f32_16x16x32_bf16 a[232:235],a[172:175],v[240:243],a[232:235]
v_mfma_f32_16x16x32_bf16 a[236:239],a[172:175],v[244:247],a[236:239]
ds_read_b64_tr_b16 a[172:173],v1 offset:30976
ds_read_b64_tr_b16 a[174:175],v1 offset:36096
v_mfma_f32_16x16x32_bf16 a[236:239],a[168:171],v[212:215],a[236:239]
ds_read_b64_tr_b16 a[168:169],v1 offset:20736
v_mfma_f32_16x16x32_bf16 a[236:239],a[164:167],v[180:183],a[236:239]
ds_read_b64_tr_b16 a[164:165],v1 offset:10496
v_mfma_f32_16x16x32_bf16 a[236:239],a[160:163],v[148:151],a[236:239]
s_waitcnt lgkmcnt(4)
ds_read_b64_tr_b16 a[160:161],v1 offset:256
v_mfma_f32_16x16x32_bf16 a[240:243],a[176:179],v[128:131],a[240:243]
ds_read_b64_tr_b16 a[162:163],v1 offset:5376
ds_read_b64_tr_b16 a[166:167],v1 offset:15616
v_mfma_f32_16x16x32_bf16 a[240:243],a[180:183],v[160:163],a[240:243]
ds_read_b64_tr_b16 a[170:171],v1 offset:25856
v_add_u32 v1,81920,v1
v_mfma_f32_16x16x32_bf16 a[240:243],a[184:187],v[192:195],a[240:243]
buffer_load_dwordx4 v[100:103],v6,s[28:31],s78 offen offset:1024 sc1
v_mfma_f32_16x16x32_bf16 a[240:243],a[188:191],v[224:227],a[240:243]
v_mfma_f32_16x16x32_bf16 a[244:247],a[188:191],v[228:231],a[244:247]
v_mfma_f32_16x16x32_bf16 a[244:247],a[184:187],v[196:199],a[244:247]
v_mfma_f32_16x16x32_bf16 a[244:247],a[180:183],v[164:167],a[244:247]
buffer_load_dwordx4 v9,s[72:75],s80 offen offset:2048 lds
v_mfma_f32_16x16x32_bf16 a[244:247],a[176:179],v[132:135],a[244:247]
v_mfma_f32_16x16x32_bf16 a[248:251],a[176:179],v[144:147],a[248:251]
v_mfma_f32_16x16x32_bf16 a[248:251],a[180:183],v[176:179],a[248:251]
v_mfma_f32_16x16x32_bf16 a[248:251],a[184:187],v[208:211],a[248:251]
buffer_load_dwordx4 v[104:107],v6,s[28:31],s78 offen offset:2048 sc1
v_mfma_f32_16x16x32_bf16 a[248:251],a[188:191],v[240:243],a[248:251]
v_mfma_f32_16x16x32_bf16 a[252:255],a[188:191],v[244:247],a[252:255]
ds_read_b64_tr_b16 a[188:189],v2 offset:31008
ds_read_b64_tr_b16 a[190:191],v2 offset:36128
v_mfma_f32_16x16x32_bf16 a[252:255],a[184:187],v[212:215],a[252:255]
ds_read_b64_tr_b16 a[184:185],v2 offset:20768
v_mfma_f32_16x16x32_bf16 a[252:255],a[180:183],v[180:183],a[252:255]
ds_read_b64_tr_b16 a[180:181],v2 offset:10528
v_mfma_f32_16x16x32_bf16 a[252:255],a[176:179],v[148:151],a[252:255]
ds_read_b64_tr_b16 a[176:177],v2 offset:288
v_mfma_f32_16x16x32_bf16 a[0:3],a[96:99],v[128:131],a[0:3]
ds_read_b64_tr_b16 a[178:179],v2 offset:5408
ds_read_b64_tr_b16 a[182:183],v2 offset:15648
v_mfma_f32_16x16x32_bf16 a[0:3],a[100:103],v[160:163],a[0:3]
ds_read_b64_tr_b16 a[186:187],v2 offset:25888
v_add_u32 v2,81920,v2
v_mfma_f32_16x16x32_bf16 a[0:3],a[104:107],v[192:195],a[0:3]
buffer_load_dwordx4 v[108:111],v6,s[28:31],s78 offen offset:3072 sc1
v_mfma_f32_16x16x32_bf16 a[0:3],a[108:111],v[224:227],a[0:3]
v_mfma_f32_16x16x32_bf16 a[4:7],a[108:111],v[228:231],a[4:7]
v_mfma_f32_16x16x32_bf16 a[4:7],a[104:107],v[196:199],a[4:7]
v_mfma_f32_16x16x32_bf16 a[4:7],a[100:103],v[164:167],a[4:7]
buffer_load_dwordx4 v10,s[72:75],s80 offen offset:3072 lds
v_mfma_f32_16x16x32_bf16 a[4:7],a[96:99],v[132:135],a[4:7]
v_mfma_f32_16x16x32_bf16 a[8:11],a[96:99],v[144:147],a[8:11]
v_mfma_f32_16x16x32_bf16 a[8:11],a[100:103],v[176:179],a[8:11]
v_mfma_f32_16x16x32_bf16 a[8:11],a[104:107],v[208:211],a[8:11]
buffer_load_dwordx4 v[112:115],v6,s[28:31],s79 offen sc1
v_mfma_f32_16x16x32_bf16 a[8:11],a[108:111],v[240:243],a[8:11]
v_mfma_f32_16x16x32_bf16 a[12:15],a[108:111],v[244:247],a[12:15]
ds_read_b64_tr_b16 a[108:109],v1 offset:30720
ds_read_b64_tr_b16 a[110:111],v1 offset:35840
v_mfma_f32_16x16x32_bf16 a[12:15],a[104:107],v[212:215],a[12:15]
ds_read_b64_tr_b16 a[104:105],v1 offset:20480
v_mfma_f32_16x16x32_bf16 a[12:15],a[100:103],v[180:183],a[12:15]
s_addk_i32 m0,4096
ds_read_b64_tr_b16 a[100:101],v1 offset:10240
v_mfma_f32_16x16x32_bf16 a[12:15],a[96:99],v[148:151],a[12:15]
ds_read_b64_tr_b16 a[96:97],v1
v_mfma_f32_16x16x32_bf16 a[16:19],a[112:115],v[128:131],a[16:19]
ds_read_b64_tr_b16 a[98:99],v1 offset:5120
ds_read_b64_tr_b16 a[102:103],v1 offset:15360
v_mfma_f32_16x16x32_bf16 a[16:19],a[116:119],v[160:163],a[16:19]
buffer_load_dwordx4 v11,s[72:75],s80 offen lds
v_mfma_f32_16x16x32_bf16 a[16:19],a[120:123],v[192:195],a[16:19]
ds_read_b64_tr_b16 a[106:107],v1 offset:25600
v_mfma_f32_16x16x32_bf16 a[16:19],a[124:127],v[224:227],a[16:19]
v_mfma_f32_16x16x32_bf16 a[20:23],a[124:127],v[228:231],a[20:23]
v_mfma_f32_16x16x32_bf16 a[20:23],a[120:123],v[196:199],a[20:23]
buffer_load_dwordx4 v[116:119],v6,s[28:31],s79 offen offset:1024 sc1
v_mfma_f32_16x16x32_bf16 a[20:23],a[116:119],v[164:167],a[20:23]
v_mfma_f32_16x16x32_bf16 a[20:23],a[112:115],v[132:135],a[20:23]
v_mfma_f32_16x16x32_bf16 a[24:27],a[112:115],v[144:147],a[24:27]
v_mfma_f32_16x16x32_bf16 a[24:27],a[116:119],v[176:179],a[24:27]
v_mfma_f32_16x16x32_bf16 a[24:27],a[120:123],v[208:211],a[24:27]
buffer_load_dwordx4 v[120:123],v6,s[28:31],s79 offen offset:2048 sc1
v_mfma_f32_16x16x32_bf16 a[24:27],a[124:127],v[240:243],a[24:27]
v_mfma_f32_16x16x32_bf16 a[28:31],a[124:127],v[244:247],a[28:31]
ds_read_b64_tr_b16 a[124:125],v2 offset:30752
ds_read_b64_tr_b16 a[126:127],v2 offset:35872
v_mfma_f32_16x16x32_bf16 a[28:31],a[120:123],v[212:215],a[28:31]
s_addk_i32 m0,1024
ds_read_b64_tr_b16 a[120:121],v2 offset:20512
v_mfma_f32_16x16x32_bf16 a[28:31],a[116:119],v[180:183],a[28:31]
ds_read_b64_tr_b16 a[116:117],v2 offset:10272
v_mfma_f32_16x16x32_bf16 a[28:31],a[112:115],v[148:151],a[28:31]
s_cmp_ge_u32 2,s71
s_cselect_b32 s76,6,4
ds_read_b64_tr_b16 a[112:113],v2 offset:32
v_mfma_f32_16x16x32_bf16 a[32:35],a[128:131],v[128:131],a[32:35]
buffer_load_dwordx4 v7,s[72:75],s83 offen lds
v_mfma_f32_16x16x32_bf16 a[32:35],a[132:135],v[160:163],a[32:35]
s_bitcmp1_b32 s54,s76
s_cselect_b32 s77,8192,0
s_lshl_b32 s76,s76,17
v_mfma_f32_16x16x32_bf16 a[32:35],a[136:139],v[192:195],a[32:35]
s_add_u32 s76,s76,s77
s_add_u32 s77,s76,4096
s_add_u32 s78,s76,16384
v_mfma_f32_16x16x32_bf16 a[32:35],a[140:143],v[224:227],a[32:35]
s_max_i32 s77,s77,s19
s_max_i32 s78,s78,s100
ds_read_b64_tr_b16 a[114:115],v2 offset:5152
v_mfma_f32_16x16x32_bf16 a[36:39],a[140:143],v[228:231],a[36:39]
buffer_load_dwordx4 v[124:127],v6,s[28:31],s79 offen offset:3072 sc1
v_mfma_f32_16x16x32_bf16 a[36:39],a[136:139],v[196:199],a[36:39]
s_add_u32 s79,s76,20480
s_max_i32 s76,s76,s18
ds_read_b64_tr_b16 a[118:119],v2 offset:15392
v_mfma_f32_16x16x32_bf16 a[36:39],a[132:135],v[164:167],a[36:39]
s_max_i32 s79,s79,s101
ds_read_b64_tr_b16 a[122:123],v2 offset:25632
v_mfma_f32_16x16x32_bf16 a[36:39],a[128:131],v[132:135],a[36:39]
v_mfma_f32_16x16x32_bf16 a[40:43],a[128:131],v[144:147],a[40:43]
v_mfma_f32_16x16x32_bf16 a[40:43],a[132:135],v[176:179],a[40:43]
v_mfma_f32_16x16x32_bf16 a[40:43],a[136:139],v[208:211],a[40:43]
v_mfma_f32_16x16x32_bf16 a[40:43],a[140:143],v[240:243],a[40:43]
v_mfma_f32_16x16x32_bf16 a[44:47],a[140:143],v[244:247],a[44:47]
ds_read_b64_tr_b16 a[140:141],v1 offset:30784
ds_read_b64_tr_b16 a[142:143],v1 offset:35904
v_mfma_f32_16x16x32_bf16 a[44:47],a[136:139],v[212:215],a[44:47]
ds_read_b64_tr_b16 a[136:137],v1 offset:20544
v_mfma_f32_16x16x32_bf16 a[44:47],a[132:135],v[180:183],a[44:47]
ds_read_b64_tr_b16 a[132:133],v1 offset:10304
v_mfma_f32_16x16x32_bf16 a[44:47],a[128:131],v[148:151],a[44:47]
buffer_load_dwordx4 v8,s[72:75],s83 offen offset:1024 lds
v_mfma_f32_16x16x32_bf16 a[48:51],a[144:147],v[128:131],a[48:51]
ds_read_b64_tr_b16 a[128:129],v1 offset:64
ds_read_b64_tr_b16 a[130:131],v1 offset:5184
v_mfma_f32_16x16x32_bf16 a[48:51],a[148:151],v[160:163],a[48:51]
ds_read_b64_tr_b16 a[134:135],v1 offset:15424
v_mfma_f32_16x16x32_bf16 a[48:51],a[152:155],v[192:195],a[48:51]
ds_read_b64_tr_b16 a[138:139],v1 offset:25664
v_mfma_f32_16x16x32_bf16 a[48:51],a[156:159],v[224:227],a[48:51]
s_lshl_b32 s94,s70,4
v_bfe_u32 v24,v6,4,2
v_min_u32 v24,2,v24
v_mfma_f32_16x16x32_bf16 a[52:55],a[156:159],v[228:231],a[52:55]
s_add_u32 s94,s94,s40
v_add_u32 v24,s71,v24
v_add_u32 v24,1,v24
v_mfma_f32_16x16x32_bf16 a[52:55],a[152:155],v[196:199],a[52:55]
s_lshl_b32 s94,s94,4
v_and_b32 v24,3,v24
v_lshlrev_b32 v24,6,v24
v_mfma_f32_16x16x32_bf16 a[52:55],a[148:151],v[164:167],a[52:55]
v_mfma_f32_16x16x32_bf16 a[52:55],a[144:147],v[132:135],a[52:55]
buffer_load_dwordx4 v[136:139],v6,s[28:31],s76 offen sc1
v_mfma_f32_16x16x32_bf16 a[56:59],a[144:147],v[144:147],a[56:59]
v_mfma_f32_16x16x32_bf16 a[56:59],a[148:151],v[176:179],a[56:59]
v_mfma_f32_16x16x32_bf16 a[56:59],a[152:155],v[208:211],a[56:59]
v_mfma_f32_16x16x32_bf16 a[56:59],a[156:159],v[240:243],a[56:59]
v_mfma_f32_16x16x32_bf16 a[60:63],a[156:159],v[244:247],a[60:63]
ds_read_b64_tr_b16 a[156:157],v2 offset:30816
ds_read_b64_tr_b16 a[158:159],v2 offset:35936
v_mfma_f32_16x16x32_bf16 a[60:63],a[152:155],v[212:215],a[60:63]
buffer_load_dwordx4 v[140:143],v6,s[28:31],s76 offen offset:1024 sc1
v_mfma_f32_16x16x32_bf16 a[60:63],a[148:151],v[180:183],a[60:63]
ds_read_b64_tr_b16 a[148:149],v2 offset:10336
ds_read_b64_tr_b16 a[150:151],v2 offset:15456
v_mfma_f32_16x16x32_bf16 a[60:63],a[144:147],v[148:151],a[60:63]
s_waitcnt lgkmcnt(4)
ds_read_b64_tr_b16 a[144:145],v2 offset:96
v_mfma_f32_16x16x32_bf16 a[64:67],a[160:163],v[128:131],a[64:67]
ds_read_b64_tr_b16 a[146:147],v2 offset:5216
v_mfma_f32_16x16x32_bf16 a[64:67],a[164:167],v[160:163],a[64:67]
buffer_load_dwordx4 v9,s[72:75],s83 offen offset:2048 lds
v_mfma_f32_16x16x32_bf16 a[64:67],a[168:171],v[192:195],a[64:67]
ds_read_b64_tr_b16 a[152:153],v2 offset:20576
ds_read_b64_tr_b16 a[154:155],v2 offset:25696
v_mfma_f32_16x16x32_bf16 a[64:67],a[172:175],v[224:227],a[64:67]
v_mfma_f32_16x16x32_bf16 a[68:71],a[172:175],v[228:231],a[68:71]
v_mfma_f32_16x16x32_bf16 a[68:71],a[168:171],v[196:199],a[68:71]
)A"
R"A(buffer_load_dwordx4 v[152:155],v6,s[28:31],s76 offen offset:2048 sc1
v_mfma_f32_16x16x32_bf16 a[68:71],a[164:167],v[164:167],a[68:71]
v_mfma_f32_16x16x32_bf16 a[68:71],a[160:163],v[132:135],a[68:71]
v_mfma_f32_16x16x32_bf16 a[72:75],a[160:163],v[144:147],a[72:75]
v_mfma_f32_16x16x32_bf16 a[72:75],a[164:167],v[176:179],a[72:75]
buffer_load_dwordx4 v[156:159],v6,s[28:31],s76 offen offset:3072 sc1
v_mfma_f32_16x16x32_bf16 a[72:75],a[168:171],v[208:211],a[72:75]
v_mfma_f32_16x16x32_bf16 a[72:75],a[172:175],v[240:243],a[72:75]
v_mfma_f32_16x16x32_bf16 a[76:79],a[172:175],v[244:247],a[76:79]
ds_read_b64_tr_b16 a[172:173],v1 offset:30848
ds_read_b64_tr_b16 a[174:175],v1 offset:35968
v_mfma_f32_16x16x32_bf16 a[76:79],a[168:171],v[212:215],a[76:79]
buffer_load_dwordx4 v10,s[72:75],s83 offen offset:3072 lds
v_mfma_f32_16x16x32_bf16 a[76:79],a[164:167],v[180:183],a[76:79]
ds_read_b64_tr_b16 a[164:165],v1 offset:10368
ds_read_b64_tr_b16 a[166:167],v1 offset:15488
v_mfma_f32_16x16x32_bf16 a[76:79],a[160:163],v[148:151],a[76:79]
ds_read_b64_tr_b16 a[160:161],v1 offset:128
v_mfma_f32_16x16x32_bf16 a[80:83],a[176:179],v[128:131],a[80:83]
ds_read_b64_tr_b16 a[162:163],v1 offset:5248
v_mfma_f32_16x16x32_bf16 a[80:83],a[180:183],v[160:163],a[80:83]
buffer_load_dwordx4 v[168:171],v6,s[28:31],s77 offen sc1
v_mfma_f32_16x16x32_bf16 a[80:83],a[184:187],v[192:195],a[80:83]
ds_read_b64_tr_b16 a[168:169],v1 offset:20608
ds_read_b64_tr_b16 a[170:171],v1 offset:25728
v_mfma_f32_16x16x32_bf16 a[80:83],a[188:191],v[224:227],a[80:83]
v_mfma_f32_16x16x32_bf16 a[84:87],a[188:191],v[228:231],a[84:87]
v_mfma_f32_16x16x32_bf16 a[84:87],a[184:187],v[196:199],a[84:87]
buffer_load_dwordx4 v[172:175],v6,s[28:31],s77 offen offset:1024 sc1
v_mfma_f32_16x16x32_bf16 a[84:87],a[180:183],v[164:167],a[84:87]
v_mfma_f32_16x16x32_bf16 a[84:87],a[176:179],v[132:135],a[84:87]
v_mfma_f32_16x16x32_bf16 a[88:91],a[176:179],v[144:147],a[88:91]
v_mfma_f32_16x16x32_bf16 a[88:91],a[180:183],v[176:179],a[88:91]
s_addk_i32 m0,4096
v_mfma_f32_16x16x32_bf16 a[88:91],a[184:187],v[208:211],a[88:91]
v_mfma_f32_16x16x32_bf16 a[88:91],a[188:191],v[240:243],a[88:91]
buffer_load_dwordx2 v[4:5],v24,s[64:67],s94 offen offset:1024 sc1
v_mfma_f32_16x16x32_bf16 a[92:95],a[188:191],v[244:247],a[92:95]
ds_read_b64_tr_b16 a[188:189],v2 offset:30880
ds_read_b64_tr_b16 a[190:191],v2 offset:36000
v_mfma_f32_16x16x32_bf16 a[92:95],a[184:187],v[212:215],a[92:95]
ds_read_b64_tr_b16 a[184:185],v2 offset:20640
v_mfma_f32_16x16x32_bf16 a[92:95],a[180:183],v[180:183],a[92:95]
ds_read_b64_tr_b16 a[180:181],v2 offset:10400
v_mfma_f32_16x16x32_bf16 a[92:95],a[176:179],v[148:151],a[92:95]
s_waitcnt vmcnt(13)
buffer_load_dwordx4 v[184:187],v6,s[28:31],s77 offen offset:2048 sc1
v_mfma_f32_16x16x32_bf16 a[192:195],a[96:99],v[64:67],a[192:195]
s_lshl_b32 s43,s41,9
ds_read_b64_tr_b16 a[176:177],v2 offset:160
ds_read_b64_tr_b16 a[178:179],v2 offset:5280
v_mfma_f32_16x16x32_bf16 a[192:195],a[100:103],v[68:71],a[192:195]
s_mul_i32 s44,s71,12
s_add_u32 s43,s43,s44
ds_read_b64_tr_b16 a[182:183],v2 offset:15520
v_mfma_f32_16x16x32_bf16 a[192:195],a[104:107],v[72:75],a[192:195]
s_add_u32 s43,s43,s40
s_add_u32 s43,s43,64
ds_read_b64_tr_b16 a[186:187],v2 offset:25760
v_mfma_f32_16x16x32_bf16 a[192:195],a[108:111],v[76:79],a[192:195]
s_lshl_b32 s43,s43,14
buffer_load_dwordx4 v11,s[72:75],s83 offen lds
v_mfma_f32_16x16x32_bf16 a[196:199],a[108:111],v[92:95],a[196:199]
s_mov_b64 s[32:33],s[12:13]
s_and_b32 s87,s55,8192
s_lshl_b32 s84,s70,21
v_mfma_f32_16x16x32_bf16 a[196:199],a[104:107],v[88:91],a[196:199]
s_add_u32 s87,s87,s84
s_add_u32 s32,s32,s43
s_addc_u32 s33,s33,0
v_mfma_f32_16x16x32_bf16 a[196:199],a[100:103],v[84:87],a[196:199]
s_add_u32 s84,s87,0
s_add_u32 s85,s87,65536
s_add_u32 s86,s87,131072
v_mfma_f32_16x16x32_bf16 a[196:199],a[96:99],v[80:83],a[196:199]
s_and_b32 s33,s33,0xffff
buffer_load_dwordx4 v[188:191],v6,s[28:31],s77 offen offset:3072 sc1
v_mfma_f32_16x16x32_bf16 a[200:203],a[96:99],v[96:99],a[200:203]
s_mov_b32 s34,6438912
s_mov_b32 s35,0x20000
s_max_i32 s84,s84,s18
v_mfma_f32_16x16x32_bf16 a[200:203],a[100:103],v[100:103],a[200:203]
s_max_i32 s85,s85,s19
s_max_i32 s86,s86,s100
v_bfe_u32 v18,v6,6,2
v_mfma_f32_16x16x32_bf16 a[200:203],a[104:107],v[104:107],a[200:203]
s_mul_i32 s44,s41,307200
v_lshrrev_b32 v19,8,v6
v_xor_b32 v18,v18,v19
v_mfma_f32_16x16x32_bf16 a[200:203],a[108:111],v[108:111],a[200:203]
s_mov_b64 s[20:21],s[16:17]
buffer_load_dwordx4 v[200:203],v6,s[28:31],s78 offen sc1
s_waitcnt vmcnt(14)
v_mfma_f32_16x16x32_bf16 a[204:207],a[108:111],v[124:127],a[204:207]
s_lshl_b32 s43,s40,2
ds_read_b64_tr_b16 a[108:109],v1 offset:30912
ds_read_b64_tr_b16 a[110:111],v1 offset:36032
v_mfma_f32_16x16x32_bf16 a[204:207],a[104:107],v[120:123],a[204:207]
s_add_u32 s43,s43,s70
ds_read_b64_tr_b16 a[104:105],v1 offset:20672
v_lshlrev_b32 v18,2,v18
v_mfma_f32_16x16x32_bf16 a[204:207],a[100:103],v[116:119],a[204:207]
s_mulk_i32 s43,20480
ds_read_b64_tr_b16 a[100:101],v1 offset:10432
v_bfe_u32 v19,v6,4,2
v_mfma_f32_16x16x32_bf16 a[204:207],a[96:99],v[112:115],a[204:207]
s_barrier
buffer_load_dwordx4 v[204:207],v6,s[28:31],s78 offen offset:1024 sc1
v_mfma_f32_16x16x32_bf16 a[208:211],a[112:115],v[64:67],a[208:211]
s_mov_b32 s22,307200
ds_read_b64_tr_b16 a[96:97],v1 offset:192
ds_read_b64_tr_b16 a[98:99],v1 offset:5312
v_mfma_f32_16x16x32_bf16 a[208:211],a[116:119],v[68:71],a[208:211]
s_mov_b32 s23,0x20000
ds_read_b64_tr_b16 a[102:103],v1 offset:15552
v_or_b32 v18,v18,v19
v_mfma_f32_16x16x32_bf16 a[208:211],a[120:123],v[72:75],a[208:211]
ds_read_b64_tr_b16 a[106:107],v1 offset:25792
v_and_b32 v19,16,v6
v_mfma_f32_16x16x32_bf16 a[208:211],a[124:127],v[76:79],a[208:211]
buffer_load_dwordx4 v[216:219],v6,s[28:31],s78 offen offset:2048 sc1
v_mfma_f32_16x16x32_bf16 a[212:215],a[124:127],v[92:95],a[212:215]
v_mov_b32 v34,0x3f80
v_lshlrev_b32 v19,v19,v34
v_mfma_f32_16x16x32_bf16 a[212:215],a[120:123],v[88:91],a[212:215]
v_lshrrev_b32 v18,1,v18
v_add_u32 v25,4096,v6
v_mfma_f32_16x16x32_bf16 a[212:215],a[116:119],v[84:87],a[212:215]
v_cmp_eq_u32 vcc,0,v18
s_nop 1
v_cndmask_b32 v34,0,v19,vcc
v_mfma_f32_16x16x32_bf16 a[212:215],a[112:115],v[80:83],a[212:215]
s_cmp_ge_u32 0,s71
s_cselect_b32 s76,3,1
v_mov_b32 v36,0
v_mfma_f32_16x16x32_bf16 a[216:219],a[112:115],v[96:99],a[216:219]
buffer_load_dwordx4 v[220:223],v6,s[28:31],s78 offen offset:3072 sc1
v_mfma_f32_16x16x32_bf16 a[216:219],a[116:119],v[100:103],a[216:219]
s_bitcmp1_b32 s54,s76
s_cselect_b32 s77,8192,0
s_lshl_b32 s76,s76,17
v_mfma_f32_16x16x32_bf16 a[216:219],a[120:123],v[104:107],a[216:219]
s_add_u32 s76,s76,s77
s_add_u32 s77,s76,4096
s_add_u32 s78,s76,16384
v_mfma_f32_16x16x32_bf16 a[216:219],a[124:127],v[108:111],a[216:219]
s_max_i32 s77,s77,s19
s_max_i32 s78,s78,s100
v_mov_b32 v37,0
v_mfma_f32_16x16x32_bf16 a[220:223],a[124:127],v[124:127],a[220:223]
ds_read_b64_tr_b16 a[124:125],v2 offset:30944
ds_read_b64_tr_b16 a[126:127],v2 offset:36064
v_mfma_f32_16x16x32_bf16 a[220:223],a[120:123],v[120:123],a[220:223]
ds_read_b64_tr_b16 a[120:121],v2 offset:20704
v_mov_b32 v32,0
v_mfma_f32_16x16x32_bf16 a[220:223],a[116:119],v[116:119],a[220:223]
buffer_load_dwordx4 v[232:235],v6,s[28:31],s79 offen sc1
v_mfma_f32_16x16x32_bf16 a[220:223],a[112:115],v[112:115],a[220:223]
ds_read_b64_tr_b16 a[112:113],v2 offset:224
ds_read_b64_tr_b16 a[114:115],v2 offset:5344
v_mfma_f32_16x16x32_bf16 a[224:227],a[128:131],v[64:67],a[224:227]
ds_read_b64_tr_b16 a[116:117],v2 offset:10464
v_mov_b32 v33,0
v_mfma_f32_16x16x32_bf16 a[224:227],a[132:135],v[68:71],a[224:227]
ds_read_b64_tr_b16 a[118:119],v2 offset:15584
v_bfe_u32 v40,v6,4,4
v_mfma_f32_16x16x32_bf16 a[224:227],a[136:139],v[72:75],a[224:227]
ds_read_b64_tr_b16 a[122:123],v2 offset:25824
v_mul_u32_u24 v40,1280,v40
v_mfma_f32_16x16x32_bf16 a[224:227],a[140:143],v[76:79],a[224:227]
v_cmp_eq_u32 vcc,1,v18
s_nop 1
v_cndmask_b32 v35,0,v19,vcc
v_mfma_f32_16x16x32_bf16 a[228:231],a[140:143],v[92:95],a[228:231]
buffer_load_dwordx4 v[236:239],v6,s[28:31],s79 offen offset:1024 sc1
v_mfma_f32_16x16x32_bf16 a[228:231],a[136:139],v[88:91],a[228:231]
s_add_u32 s20,s20,s44
s_addc_u32 s21,s21,0
v_bfe_u32 v41,v6,8,1
v_mfma_f32_16x16x32_bf16 a[228:231],a[132:135],v[84:87],a[228:231]
s_mul_i32 s44,s71,320
v_lshl_add_u32 v40,v41,5,v40
v_lshrrev_b32 v41,9,v6
v_mfma_f32_16x16x32_bf16 a[228:231],a[128:131],v[80:83],a[228:231]
s_waitcnt vmcnt(9)
v_cmp_ne_u32 s[90:91],s68,v4
v_and_b32 v4,-2,v5
v_cmp_ne_u32 s[92:93],s69,v4
s_nop 0
s_or_b64 s[90:91],s[90:91],s[92:93]
s_cbranch_scc1 L_fr9_%=
L_fo9_%=:
v_mfma_f32_16x16x32_bf16 a[232:235],a[128:131],v[96:99],a[232:235]
s_and_b32 s21,s21,0xffff
s_add_u32 s43,s43,s44
v_lshl_add_u32 v40,v41,4,v40
v_mfma_f32_16x16x32_bf16 a[232:235],a[132:135],v[100:103],a[232:235]
v_mfma_f32_16x16x32_bf16 a[232:235],a[136:139],v[104:107],a[232:235]
buffer_load_dwordx4 v[248:251],v6,s[28:31],s79 offen offset:2048 sc1
v_mfma_f32_16x16x32_bf16 a[232:235],a[140:143],v[108:111],a[232:235]
v_mfma_f32_16x16x32_bf16 a[236:239],a[140:143],v[124:127],a[236:239]
ds_read_b64_tr_b16 a[140:141],v1 offset:30976
ds_read_b64_tr_b16 a[142:143],v1 offset:36096
v_mfma_f32_16x16x32_bf16 a[236:239],a[136:139],v[120:123],a[236:239]
ds_read_b64_tr_b16 a[136:137],v1 offset:20736
v_mfma_f32_16x16x32_bf16 a[236:239],a[132:135],v[116:119],a[236:239]
ds_read_b64_tr_b16 a[132:133],v1 offset:10496
v_mfma_f32_16x16x32_bf16 a[236:239],a[128:131],v[112:115],a[236:239]
s_waitcnt lgkmcnt(4)
ds_read_b64_tr_b16 a[128:129],v1 offset:256
v_mfma_f32_16x16x32_bf16 a[240:243],a[144:147],v[64:67],a[240:243]
buffer_load_dwordx4 v[252:255],v6,s[28:31],s79 offen offset:3072 sc1
v_mfma_f32_16x16x32_bf16 a[240:243],a[148:151],v[68:71],a[240:243]
s_add_u32 s79,s76,20480
s_max_i32 s76,s76,s18
ds_read_b64_tr_b16 a[130:131],v1 offset:5376
v_mfma_f32_16x16x32_bf16 a[240:243],a[152:155],v[72:75],a[240:243]
s_max_i32 s79,s79,s101
ds_read_b64_tr_b16 a[134:135],v1 offset:15616
ds_read_b64_tr_b16 a[138:139],v1 offset:25856
v_mfma_f32_16x16x32_bf16 a[240:243],a[156:159],v[76:79],a[240:243]
v_subrev_u32 v1,40960,v1
v_mfma_f32_16x16x32_bf16 a[244:247],a[156:159],v[92:95],a[244:247]
buffer_load_dwordx4 v[128:131],v6,s[28:31],s76 offen sc1
v_mfma_f32_16x16x32_bf16 a[244:247],a[152:155],v[88:91],a[244:247]
v_mfma_f32_16x16x32_bf16 a[244:247],a[148:151],v[84:87],a[244:247]
v_mfma_f32_16x16x32_bf16 a[244:247],a[144:147],v[80:83],a[244:247]
v_mfma_f32_16x16x32_bf16 a[248:251],a[144:147],v[96:99],a[248:251]
buffer_load_dwordx4 v[160:163],v6,s[28:31],s76 offen offset:1024 sc1
v_mfma_f32_16x16x32_bf16 a[248:251],a[148:151],v[100:103],a[248:251]
v_mfma_f32_16x16x32_bf16 a[248:251],a[152:155],v[104:107],a[248:251]
v_mfma_f32_16x16x32_bf16 a[248:251],a[156:159],v[108:111],a[248:251]
v_mfma_f32_16x16x32_bf16 a[252:255],a[156:159],v[124:127],a[252:255]
buffer_load_dwordx4 v[192:195],v6,s[28:31],s76 offen offset:2048 sc1
v_mfma_f32_16x16x32_bf16 a[252:255],a[152:155],v[120:123],a[252:255]
ds_read_b64_tr_b16 a[152:153],v2 offset:20768
ds_read_b64_tr_b16 a[154:155],v2 offset:25888
v_mfma_f32_16x16x32_bf16 a[252:255],a[148:151],v[116:119],a[252:255]
ds_read_b64_tr_b16 a[148:149],v2 offset:10528
v_mfma_f32_16x16x32_bf16 a[252:255],a[144:147],v[112:115],a[252:255]
ds_read_b64_tr_b16 a[144:145],v2 offset:288
v_mfma_f32_16x16x32_bf16 a[0:3],a[160:163],v[64:67],a[0:3]
ds_read_b64_tr_b16 a[146:147],v2 offset:5408
v_mfma_f32_16x16x32_bf16 a[0:3],a[164:167],v[68:71],a[0:3]
buffer_load_dwordx4 v[224:227],v6,s[28:31],s76 offen offset:3072 sc1
v_mfma_f32_16x16x32_bf16 a[0:3],a[168:171],v[72:75],a[0:3]
ds_read_b64_tr_b16 a[150:151],v2 offset:15648
ds_read_b64_tr_b16 a[156:157],v2 offset:31008
v_mfma_f32_16x16x32_bf16 a[0:3],a[172:175],v[76:79],a[0:3]
ds_read_b64_tr_b16 a[158:159],v2 offset:36128
v_subrev_u32 v2,40960,v2
v_mfma_f32_16x16x32_bf16 a[4:7],a[172:175],v[92:95],a[4:7]
v_mfma_f32_16x16x32_bf16 a[4:7],a[168:171],v[88:91],a[4:7]
v_mfma_f32_16x16x32_bf16 a[4:7],a[164:167],v[84:87],a[4:7]
v_mfma_f32_16x16x32_bf16 a[4:7],a[160:163],v[80:83],a[4:7]
buffer_load_dwordx4 v[132:135],v6,s[28:31],s77 offen sc1
)A"
R"A(v_mfma_f32_16x16x32_bf16 a[8:11],a[160:163],v[96:99],a[8:11]
v_mfma_f32_16x16x32_bf16 a[8:11],a[164:167],v[100:103],a[8:11]
v_mfma_f32_16x16x32_bf16 a[8:11],a[168:171],v[104:107],a[8:11]
v_mfma_f32_16x16x32_bf16 a[8:11],a[172:175],v[108:111],a[8:11]
v_mfma_f32_16x16x32_bf16 a[12:15],a[172:175],v[124:127],a[12:15]
ds_read_b64_tr_b16 a[172:173],v1 offset:30720
ds_read_b64_tr_b16 a[174:175],v1 offset:35840
v_mfma_f32_16x16x32_bf16 a[12:15],a[168:171],v[120:123],a[12:15]
buffer_load_dwordx4 v[164:167],v6,s[28:31],s77 offen offset:1024 sc1
v_mfma_f32_16x16x32_bf16 a[12:15],a[164:167],v[116:119],a[12:15]
ds_read_b64_tr_b16 a[164:165],v1 offset:10240
ds_read_b64_tr_b16 a[166:167],v1 offset:15360
v_mfma_f32_16x16x32_bf16 a[12:15],a[160:163],v[112:115],a[12:15]
ds_read_b64_tr_b16 a[160:161],v1
v_mfma_f32_16x16x32_bf16 a[16:19],a[176:179],v[64:67],a[16:19]
ds_read_b64_tr_b16 a[162:163],v1 offset:5120
v_mfma_f32_16x16x32_bf16 a[16:19],a[180:183],v[68:71],a[16:19]
ds_read_b64_tr_b16 a[168:169],v1 offset:20480
v_mfma_f32_16x16x32_bf16 a[16:19],a[184:187],v[72:75],a[16:19]
ds_read_b64_tr_b16 a[170:171],v1 offset:25600
v_mfma_f32_16x16x32_bf16 a[16:19],a[188:191],v[76:79],a[16:19]
buffer_load_dwordx4 v[196:199],v6,s[28:31],s77 offen offset:2048 sc1
v_mfma_f32_16x16x32_bf16 a[20:23],a[188:191],v[92:95],a[20:23]
v_mfma_f32_16x16x32_bf16 a[20:23],a[184:187],v[88:91],a[20:23]
v_mfma_f32_16x16x32_bf16 a[20:23],a[180:183],v[84:87],a[20:23]
v_mfma_f32_16x16x32_bf16 a[20:23],a[176:179],v[80:83],a[20:23]
v_mfma_f32_16x16x32_bf16 a[24:27],a[176:179],v[96:99],a[24:27]
v_mfma_f32_16x16x32_bf16 a[24:27],a[180:183],v[100:103],a[24:27]
buffer_load_dwordx4 v[228:231],v6,s[28:31],s77 offen offset:3072 sc1
v_mfma_f32_16x16x32_bf16 a[24:27],a[184:187],v[104:107],a[24:27]
v_mfma_f32_16x16x32_bf16 a[24:27],a[188:191],v[108:111],a[24:27]
v_mfma_f32_16x16x32_bf16 a[28:31],a[188:191],v[124:127],a[28:31]
ds_read_b64_tr_b16 a[188:189],v2 offset:30752
ds_read_b64_tr_b16 a[190:191],v2 offset:35872
v_mfma_f32_16x16x32_bf16 a[28:31],a[184:187],v[120:123],a[28:31]
ds_read_b64_tr_b16 a[184:185],v2 offset:20512
v_mfma_f32_16x16x32_bf16 a[28:31],a[180:183],v[116:119],a[28:31]
ds_read_b64_tr_b16 a[180:181],v2 offset:10272
v_mfma_f32_16x16x32_bf16 a[28:31],a[176:179],v[112:115],a[28:31]
buffer_load_dwordx4 v[144:147],v6,s[28:31],s78 offen sc1
v_mfma_f32_16x16x32_bf16 a[32:35],a[96:99],v[64:67],a[32:35]
ds_read_b64_tr_b16 a[176:177],v2 offset:32
ds_read_b64_tr_b16 a[178:179],v2 offset:5152
v_mfma_f32_16x16x32_bf16 a[32:35],a[100:103],v[68:71],a[32:35]
ds_read_b64_tr_b16 a[182:183],v2 offset:15392
v_mfma_f32_16x16x32_bf16 a[32:35],a[104:107],v[72:75],a[32:35]
ds_read_b64_tr_b16 a[186:187],v2 offset:25632
v_mfma_f32_16x16x32_bf16 a[32:35],a[108:111],v[76:79],a[32:35]
v_mfma_f32_16x16x32_bf16 a[36:39],a[108:111],v[92:95],a[36:39]
v_mfma_f32_16x16x32_bf16 a[36:39],a[104:107],v[88:91],a[36:39]
buffer_load_dwordx4 v[176:179],v6,s[28:31],s78 offen offset:1024 sc1
v_mfma_f32_16x16x32_bf16 a[36:39],a[100:103],v[84:87],a[36:39]
v_mfma_f32_16x16x32_bf16 a[36:39],a[96:99],v[80:83],a[36:39]
v_mfma_f32_16x16x32_bf16 a[40:43],a[96:99],v[96:99],a[40:43]
v_mfma_f32_16x16x32_bf16 a[40:43],a[100:103],v[100:103],a[40:43]
v_mfma_f32_16x16x32_bf16 a[40:43],a[104:107],v[104:107],a[40:43]
v_mfma_f32_16x16x32_bf16 a[40:43],a[108:111],v[108:111],a[40:43]
buffer_load_dwordx4 v[208:211],v6,s[28:31],s78 offen offset:2048 sc1
v_mfma_f32_16x16x32_bf16 a[44:47],a[108:111],v[124:127],a[44:47]
ds_read_b64_tr_b16 a[108:109],v1 offset:30784
ds_read_b64_tr_b16 a[110:111],v1 offset:35904
v_mfma_f32_16x16x32_bf16 a[44:47],a[104:107],v[120:123],a[44:47]
ds_read_b64_tr_b16 a[104:105],v1 offset:20544
v_mfma_f32_16x16x32_bf16 a[44:47],a[100:103],v[116:119],a[44:47]
ds_read_b64_tr_b16 a[100:101],v1 offset:10304
v_mfma_f32_16x16x32_bf16 a[44:47],a[96:99],v[112:115],a[44:47]
ds_read_b64_tr_b16 a[96:97],v1 offset:64
v_mfma_f32_16x16x32_bf16 a[48:51],a[112:115],v[64:67],a[48:51]
ds_read_b64_tr_b16 a[98:99],v1 offset:5184
ds_read_b64_tr_b16 a[102:103],v1 offset:15424
v_mfma_f32_16x16x32_bf16 a[48:51],a[116:119],v[68:71],a[48:51]
buffer_load_dwordx4 v[240:243],v6,s[28:31],s78 offen offset:3072 sc1
v_mfma_f32_16x16x32_bf16 a[48:51],a[120:123],v[72:75],a[48:51]
ds_read_b64_tr_b16 a[106:107],v1 offset:25664
v_mfma_f32_16x16x32_bf16 a[48:51],a[124:127],v[76:79],a[48:51]
v_mfma_f32_16x16x32_bf16 a[52:55],a[124:127],v[92:95],a[52:55]
v_mfma_f32_16x16x32_bf16 a[52:55],a[120:123],v[88:91],a[52:55]
v_mfma_f32_16x16x32_bf16 a[52:55],a[116:119],v[84:87],a[52:55]
v_mfma_f32_16x16x32_bf16 a[52:55],a[112:115],v[80:83],a[52:55]
buffer_load_dwordx4 v[148:151],v6,s[28:31],s79 offen sc1
v_mfma_f32_16x16x32_bf16 a[56:59],a[112:115],v[96:99],a[56:59]
v_mfma_f32_16x16x32_bf16 a[56:59],a[116:119],v[100:103],a[56:59]
s_cmp_ge_u32 1,s71
s_cselect_b32 s80,5,3
s_mul_i32 s80,s80,163840
v_mfma_f32_16x16x32_bf16 a[56:59],a[120:123],v[104:107],a[56:59]
s_add_u32 s80,s80,s81
s_add_u32 s83,s80,20480
v_mfma_f32_16x16x32_bf16 a[56:59],a[124:127],v[108:111],a[56:59]
v_mfma_f32_16x16x32_bf16 a[60:63],a[124:127],v[124:127],a[60:63]
ds_read_b64_tr_b16 a[124:125],v2 offset:30816
ds_read_b64_tr_b16 a[126:127],v2 offset:35936
v_mfma_f32_16x16x32_bf16 a[60:63],a[120:123],v[120:123],a[60:63]
buffer_load_dwordx4 v[180:183],v6,s[28:31],s79 offen offset:1024 sc1
v_mfma_f32_16x16x32_bf16 a[60:63],a[116:119],v[116:119],a[60:63]
ds_read_b64_tr_b16 a[116:117],v2 offset:10336
ds_read_b64_tr_b16 a[118:119],v2 offset:15456
v_mfma_f32_16x16x32_bf16 a[60:63],a[112:115],v[112:115],a[60:63]
s_waitcnt lgkmcnt(4)
ds_read_b64_tr_b16 a[112:113],v2 offset:96
v_mfma_f32_16x16x32_bf16 a[64:67],a[128:131],v[64:67],a[64:67]
ds_read_b64_tr_b16 a[114:115],v2 offset:5216
v_mfma_f32_16x16x32_bf16 a[64:67],a[132:135],v[68:71],a[64:67]
ds_read_b64_tr_b16 a[120:121],v2 offset:20576
v_mfma_f32_16x16x32_bf16 a[64:67],a[136:139],v[72:75],a[64:67]
ds_read_b64_tr_b16 a[122:123],v2 offset:25696
v_mfma_f32_16x16x32_bf16 a[64:67],a[140:143],v[76:79],a[64:67]
buffer_load_dwordx4 v[212:215],v6,s[28:31],s79 offen offset:2048 sc1
v_mfma_f32_16x16x32_bf16 a[68:71],a[140:143],v[92:95],a[68:71]
v_mfma_f32_16x16x32_bf16 a[68:71],a[136:139],v[88:91],a[68:71]
v_mfma_f32_16x16x32_bf16 a[68:71],a[132:135],v[84:87],a[68:71]
v_mfma_f32_16x16x32_bf16 a[68:71],a[128:131],v[80:83],a[68:71]
v_mfma_f32_16x16x32_bf16 a[72:75],a[128:131],v[96:99],a[72:75]
v_mfma_f32_16x16x32_bf16 a[72:75],a[132:135],v[100:103],a[72:75]
buffer_load_dwordx4 v[244:247],v6,s[28:31],s79 offen offset:3072 sc1
v_mfma_f32_16x16x32_bf16 a[72:75],a[136:139],v[104:107],a[72:75]
v_mfma_f32_16x16x32_bf16 a[72:75],a[140:143],v[108:111],a[72:75]
v_mfma_f32_16x16x32_bf16 a[76:79],a[140:143],v[124:127],a[76:79]
ds_read_b64_tr_b16 a[140:141],v1 offset:30848
ds_read_b64_tr_b16 a[142:143],v1 offset:35968
v_mfma_f32_16x16x32_bf16 a[76:79],a[136:139],v[120:123],a[76:79]
ds_read_b64_tr_b16 a[136:137],v1 offset:20608
v_mfma_f32_16x16x32_bf16 a[76:79],a[132:135],v[116:119],a[76:79]
s_add_u32 m0,s82,40960
ds_read_b64_tr_b16 a[132:133],v1 offset:10368
v_mfma_f32_16x16x32_bf16 a[76:79],a[128:131],v[112:115],a[76:79]
ds_read_b64_tr_b16 a[128:129],v1 offset:128
v_mfma_f32_16x16x32_bf16 a[80:83],a[144:147],v[64:67],a[80:83]
ds_read_b64_tr_b16 a[130:131],v1 offset:5248
ds_read_b64_tr_b16 a[134:135],v1 offset:15488
v_mfma_f32_16x16x32_bf16 a[80:83],a[148:151],v[68:71],a[80:83]
buffer_load_dwordx4 v7,s[72:75],s80 offen lds
v_mfma_f32_16x16x32_bf16 a[80:83],a[152:155],v[72:75],a[80:83]
ds_read_b64_tr_b16 a[138:139],v1 offset:25728
v_mfma_f32_16x16x32_bf16 a[80:83],a[156:159],v[76:79],a[80:83]
v_mfma_f32_16x16x32_bf16 a[84:87],a[156:159],v[92:95],a[84:87]
v_mfma_f32_16x16x32_bf16 a[84:87],a[152:155],v[88:91],a[84:87]
v_mfma_f32_16x16x32_bf16 a[84:87],a[148:151],v[84:87],a[84:87]
buffer_load_dwordx4 v8,s[72:75],s80 offen offset:1024 lds
v_mfma_f32_16x16x32_bf16 a[84:87],a[144:147],v[80:83],a[84:87]
v_mfma_f32_16x16x32_bf16 a[88:91],a[144:147],v[96:99],a[88:91]
v_mfma_f32_16x16x32_bf16 a[88:91],a[148:151],v[100:103],a[88:91]
v_mfma_f32_16x16x32_bf16 a[88:91],a[152:155],v[104:107],a[88:91]
v_mfma_f32_16x16x32_bf16 a[88:91],a[156:159],v[108:111],a[88:91]
buffer_load_dwordx4 v9,s[72:75],s80 offen offset:2048 lds
v_mfma_f32_16x16x32_bf16 a[92:95],a[156:159],v[124:127],a[92:95]
ds_read_b64_tr_b16 a[156:157],v2 offset:30880
ds_read_b64_tr_b16 a[158:159],v2 offset:36000
v_mfma_f32_16x16x32_bf16 a[92:95],a[152:155],v[120:123],a[92:95]
ds_read_b64_tr_b16 a[152:153],v2 offset:20640
v_mfma_f32_16x16x32_bf16 a[92:95],a[148:151],v[116:119],a[92:95]
ds_read_b64_tr_b16 a[148:149],v2 offset:10400
v_mfma_f32_16x16x32_bf16 a[92:95],a[144:147],v[112:115],a[92:95]
ds_read_b64_tr_b16 a[144:145],v2 offset:160
v_mfma_f32_16x16x32_bf16 a[192:195],a[160:163],v[136:139],a[192:195]
buffer_load_dwordx4 v10,s[72:75],s80 offen offset:3072 lds
v_mfma_f32_16x16x32_bf16 a[192:195],a[164:167],v[140:143],a[192:195]
ds_read_b64_tr_b16 a[146:147],v2 offset:5280
ds_read_b64_tr_b16 a[150:151],v2 offset:15520
v_mfma_f32_16x16x32_bf16 a[192:195],a[168:171],v[152:155],a[192:195]
s_addk_i32 m0,4096
ds_read_b64_tr_b16 a[154:155],v2 offset:25760
v_mfma_f32_16x16x32_bf16 a[192:195],a[172:175],v[156:159],a[192:195]
s_waitcnt vmcnt(12)
v_mfma_f32_16x16x32_bf16 a[196:199],a[172:175],v[188:191],a[196:199]
v_mfma_f32_16x16x32_bf16 a[196:199],a[168:171],v[184:187],a[196:199]
buffer_load_dwordx4 v11,s[72:75],s80 offen lds
v_mfma_f32_16x16x32_bf16 a[196:199],a[164:167],v[172:175],a[196:199]
v_mfma_f32_16x16x32_bf16 a[196:199],a[160:163],v[168:171],a[196:199]
s_addk_i32 m0,1024
v_mfma_f32_16x16x32_bf16 a[200:203],a[160:163],v[200:203],a[200:203]
v_mfma_f32_16x16x32_bf16 a[200:203],a[164:167],v[204:207],a[200:203]
v_mfma_f32_16x16x32_bf16 a[200:203],a[168:171],v[216:219],a[200:203]
buffer_load_dwordx4 v7,s[72:75],s83 offen lds
v_mfma_f32_16x16x32_bf16 a[200:203],a[172:175],v[220:223],a[200:203]
v_mfma_f32_16x16x32_bf16 a[204:207],a[172:175],v[252:255],a[204:207]
ds_read_b64_tr_b16 a[172:173],v1 offset:30912
ds_read_b64_tr_b16 a[174:175],v1 offset:36032
v_mfma_f32_16x16x32_bf16 a[204:207],a[168:171],v[248:251],a[204:207]
ds_read_b64_tr_b16 a[168:169],v1 offset:20672
v_mfma_f32_16x16x32_bf16 a[204:207],a[164:167],v[236:239],a[204:207]
ds_read_b64_tr_b16 a[164:165],v1 offset:10432
v_mfma_f32_16x16x32_bf16 a[204:207],a[160:163],v[232:235],a[204:207]
s_barrier
buffer_load_dwordx4 v8,s[72:75],s83 offen offset:1024 lds
v_mfma_f32_16x16x32_bf16 a[208:211],a[176:179],v[136:139],a[208:211]
s_cmp_ge_u32 1,s71
s_cselect_b32 s76,5,3
ds_read_b64_tr_b16 a[160:161],v1 offset:192
v_mfma_f32_16x16x32_bf16 a[208:211],a[180:183],v[140:143],a[208:211]
s_bitcmp1_b32 s54,s76
s_cselect_b32 s77,8192,0
s_lshl_b32 s76,s76,17
v_mfma_f32_16x16x32_bf16 a[208:211],a[184:187],v[152:155],a[208:211]
s_add_u32 s76,s76,s77
s_add_u32 s77,s76,4096
s_add_u32 s78,s76,16384
v_mfma_f32_16x16x32_bf16 a[208:211],a[188:191],v[156:159],a[208:211]
s_add_u32 s79,s76,20480
buffer_load_dwordx4 v9,s[72:75],s83 offen offset:2048 lds
v_mfma_f32_16x16x32_bf16 a[212:215],a[188:191],v[188:191],a[212:215]
s_max_i32 s76,s76,s18
s_max_i32 s77,s77,s19
s_max_i32 s78,s78,s100
v_mfma_f32_16x16x32_bf16 a[212:215],a[184:187],v[184:187],a[212:215]
s_max_i32 s79,s79,s101
ds_read_b64_tr_b16 a[162:163],v1 offset:5312
ds_read_b64_tr_b16 a[166:167],v1 offset:15552
v_mfma_f32_16x16x32_bf16 a[212:215],a[180:183],v[172:175],a[212:215]
ds_read_b64_tr_b16 a[170:171],v1 offset:25792
v_mfma_f32_16x16x32_bf16 a[212:215],a[176:179],v[168:171],a[212:215]
v_mfma_f32_16x16x32_bf16 a[216:219],a[176:179],v[200:203],a[216:219]
buffer_load_dwordx4 v10,s[72:75],s83 offen offset:3072 lds
v_mfma_f32_16x16x32_bf16 a[216:219],a[180:183],v[204:207],a[216:219]
v_mfma_f32_16x16x32_bf16 a[216:219],a[184:187],v[216:219],a[216:219]
s_addk_i32 m0,4096
v_mfma_f32_16x16x32_bf16 a[216:219],a[188:191],v[220:223],a[216:219]
v_mfma_f32_16x16x32_bf16 a[220:223],a[188:191],v[252:255],a[220:223]
ds_read_b64_tr_b16 a[188:189],v2 offset:30944
ds_read_b64_tr_b16 a[190:191],v2 offset:36064
)A"
R"A(v_mfma_f32_16x16x32_bf16 a[220:223],a[184:187],v[248:251],a[220:223]
buffer_load_dwordx4 v11,s[72:75],s83 offen lds
v_mfma_f32_16x16x32_bf16 a[220:223],a[180:183],v[236:239],a[220:223]
ds_read_b64_tr_b16 a[180:181],v2 offset:10464
ds_read_b64_tr_b16 a[182:183],v2 offset:15584
v_mfma_f32_16x16x32_bf16 a[220:223],a[176:179],v[232:235],a[220:223]
ds_read_b64_tr_b16 a[176:177],v2 offset:224
v_mfma_f32_16x16x32_bf16 a[224:227],a[96:99],v[136:139],a[224:227]
ds_read_b64_tr_b16 a[178:179],v2 offset:5344
v_mfma_f32_16x16x32_bf16 a[224:227],a[100:103],v[140:143],a[224:227]
ds_read_b64_tr_b16 a[184:185],v2 offset:20704
v_mfma_f32_16x16x32_bf16 a[224:227],a[104:107],v[152:155],a[224:227]
ds_read_b64_tr_b16 a[186:187],v2 offset:25824
v_mfma_f32_16x16x32_bf16 a[224:227],a[108:111],v[156:159],a[224:227]
v_mfma_f32_16x16x32_bf16 a[228:231],a[108:111],v[188:191],a[228:231]
v_mfma_f32_16x16x32_bf16 a[228:231],a[104:107],v[184:187],a[228:231]
v_mfma_f32_16x16x32_bf16 a[228:231],a[100:103],v[172:175],a[228:231]
v_mfma_f32_16x16x32_bf16 a[228:231],a[96:99],v[168:171],a[228:231]
buffer_load_dwordx4 v[64:67],v6,s[28:31],s76 offen sc1
v_mfma_f32_16x16x32_bf16 a[232:235],a[96:99],v[200:203],a[232:235]
v_mfma_f32_16x16x32_bf16 a[232:235],a[100:103],v[204:207],a[232:235]
v_mfma_f32_16x16x32_bf16 a[232:235],a[104:107],v[216:219],a[232:235]
v_mfma_f32_16x16x32_bf16 a[232:235],a[108:111],v[220:223],a[232:235]
v_mfma_f32_16x16x32_bf16 a[236:239],a[108:111],v[252:255],a[236:239]
buffer_load_dwordx4 v[68:71],v6,s[28:31],s76 offen offset:1024 sc1
v_mfma_f32_16x16x32_bf16 a[236:239],a[104:107],v[248:251],a[236:239]
ds_read_b64_tr_b16 a[104:105],v1 offset:20736
ds_read_b64_tr_b16 a[106:107],v1 offset:25856
v_mfma_f32_16x16x32_bf16 a[236:239],a[100:103],v[236:239],a[236:239]
ds_read_b64_tr_b16 a[100:101],v1 offset:10496
v_mfma_f32_16x16x32_bf16 a[236:239],a[96:99],v[232:235],a[236:239]
s_waitcnt lgkmcnt(3)
ds_read_b64_tr_b16 a[96:97],v1 offset:256
v_mfma_f32_16x16x32_bf16 a[240:243],a[112:115],v[136:139],a[240:243]
ds_read_b64_tr_b16 a[98:99],v1 offset:5376
v_mfma_f32_16x16x32_bf16 a[240:243],a[116:119],v[140:143],a[240:243]
buffer_load_dwordx4 v[72:75],v6,s[28:31],s76 offen offset:2048 sc1
v_mfma_f32_16x16x32_bf16 a[240:243],a[120:123],v[152:155],a[240:243]
ds_read_b64_tr_b16 a[102:103],v1 offset:15616
ds_read_b64_tr_b16 a[108:109],v1 offset:30976
v_mfma_f32_16x16x32_bf16 a[240:243],a[124:127],v[156:159],a[240:243]
ds_read_b64_tr_b16 a[110:111],v1 offset:36096
v_subrev_u32 v1,81920,v1
v_mfma_f32_16x16x32_bf16 a[244:247],a[124:127],v[188:191],a[244:247]
v_add_u32 v42,40960,v1
v_add_u32 v44,122880,v1
v_mfma_f32_16x16x32_bf16 a[244:247],a[120:123],v[184:187],a[244:247]
v_mfma_f32_16x16x32_bf16 a[244:247],a[116:119],v[172:175],a[244:247]
buffer_load_dwordx4 v[76:79],v6,s[28:31],s76 offen offset:3072 sc1
v_mfma_f32_16x16x32_bf16 a[244:247],a[112:115],v[168:171],a[244:247]
v_mfma_f32_16x16x32_bf16 a[248:251],a[112:115],v[200:203],a[248:251]
v_mfma_f32_16x16x32_bf16 a[248:251],a[116:119],v[204:207],a[248:251]
v_mfma_f32_16x16x32_bf16 a[248:251],a[120:123],v[216:219],a[248:251]
v_mfma_f32_16x16x32_bf16 a[248:251],a[124:127],v[220:223],a[248:251]
buffer_load_dwordx4 v[80:83],v6,s[28:31],s77 offen sc1
v_mfma_f32_16x16x32_bf16 a[252:255],a[124:127],v[252:255],a[252:255]
ds_read_b64_tr_b16 a[124:125],v2 offset:31008
ds_read_b64_tr_b16 a[126:127],v2 offset:36128
v_mfma_f32_16x16x32_bf16 a[252:255],a[120:123],v[248:251],a[252:255]
ds_read_b64_tr_b16 a[120:121],v2 offset:20768
v_mfma_f32_16x16x32_bf16 a[252:255],a[116:119],v[236:239],a[252:255]
ds_read_b64_tr_b16 a[116:117],v2 offset:10528
v_mfma_f32_16x16x32_bf16 a[252:255],a[112:115],v[232:235],a[252:255]
ds_read_b64_tr_b16 a[112:113],v2 offset:288
v_mfma_f32_16x16x32_bf16 a[0:3],a[128:131],v[136:139],a[0:3]
buffer_load_dwordx4 v[84:87],v6,s[28:31],s77 offen offset:1024 sc1
v_mfma_f32_16x16x32_bf16 a[0:3],a[132:135],v[140:143],a[0:3]
ds_read_b64_tr_b16 a[114:115],v2 offset:5408
ds_read_b64_tr_b16 a[118:119],v2 offset:15648
v_mfma_f32_16x16x32_bf16 a[0:3],a[136:139],v[152:155],a[0:3]
ds_read_b64_tr_b16 a[122:123],v2 offset:25888
v_subrev_u32 v2,81920,v2
v_mfma_f32_16x16x32_bf16 a[0:3],a[140:143],v[156:159],a[0:3]
v_add_u32 v43,40960,v2
v_add_u32 v45,122880,v2
v_mfma_f32_16x16x32_bf16 a[4:7],a[140:143],v[188:191],a[4:7]
v_mfma_f32_16x16x32_bf16 a[4:7],a[136:139],v[184:187],a[4:7]
buffer_load_dwordx4 v[88:91],v6,s[28:31],s77 offen offset:2048 sc1
v_mfma_f32_16x16x32_bf16 a[4:7],a[132:135],v[172:175],a[4:7]
v_mfma_f32_16x16x32_bf16 a[4:7],a[128:131],v[168:171],a[4:7]
v_mfma_f32_16x16x32_bf16 a[8:11],a[128:131],v[200:203],a[8:11]
v_mfma_f32_16x16x32_bf16 a[8:11],a[132:135],v[204:207],a[8:11]
v_mfma_f32_16x16x32_bf16 a[8:11],a[136:139],v[216:219],a[8:11]
buffer_load_dwordx4 v[92:95],v6,s[28:31],s77 offen offset:3072 sc1
v_mfma_f32_16x16x32_bf16 a[8:11],a[140:143],v[220:223],a[8:11]
v_mfma_f32_16x16x32_bf16 a[12:15],a[140:143],v[252:255],a[12:15]
ds_read_b64_tr_b16 a[140:141],v1 offset:30720
ds_read_b64_tr_b16 a[142:143],v1 offset:35840
v_mfma_f32_16x16x32_bf16 a[12:15],a[136:139],v[248:251],a[12:15]
ds_read_b64_tr_b16 a[136:137],v1 offset:20480
v_mfma_f32_16x16x32_bf16 a[12:15],a[132:135],v[236:239],a[12:15]
ds_read_b64_tr_b16 a[132:133],v1 offset:10240
v_mfma_f32_16x16x32_bf16 a[12:15],a[128:131],v[232:235],a[12:15]
buffer_load_dwordx4 v[96:99],v6,s[28:31],s78 offen sc1
v_mfma_f32_16x16x32_bf16 a[16:19],a[144:147],v[136:139],a[16:19]
ds_read_b64_tr_b16 a[128:129],v1
ds_read_b64_tr_b16 a[130:131],v1 offset:5120
v_mfma_f32_16x16x32_bf16 a[16:19],a[148:151],v[140:143],a[16:19]
ds_read_b64_tr_b16 a[134:135],v1 offset:15360
v_mfma_f32_16x16x32_bf16 a[16:19],a[152:155],v[152:155],a[16:19]
ds_read_b64_tr_b16 a[138:139],v1 offset:25600
v_mfma_f32_16x16x32_bf16 a[16:19],a[156:159],v[156:159],a[16:19]
v_mfma_f32_16x16x32_bf16 a[20:23],a[156:159],v[188:191],a[20:23]
buffer_load_dwordx4 v[100:103],v6,s[28:31],s78 offen offset:1024 sc1
v_mfma_f32_16x16x32_bf16 a[20:23],a[152:155],v[184:187],a[20:23]
v_mfma_f32_16x16x32_bf16 a[20:23],a[148:151],v[172:175],a[20:23]
v_mfma_f32_16x16x32_bf16 a[20:23],a[144:147],v[168:171],a[20:23]
v_mfma_f32_16x16x32_bf16 a[24:27],a[144:147],v[200:203],a[24:27]
v_mfma_f32_16x16x32_bf16 a[24:27],a[148:151],v[204:207],a[24:27]
buffer_load_dwordx4 v[104:107],v6,s[28:31],s78 offen offset:2048 sc1
v_mfma_f32_16x16x32_bf16 a[24:27],a[152:155],v[216:219],a[24:27]
v_mfma_f32_16x16x32_bf16 a[24:27],a[156:159],v[220:223],a[24:27]
v_mfma_f32_16x16x32_bf16 a[28:31],a[156:159],v[252:255],a[28:31]
ds_read_b64_tr_b16 a[156:157],v2 offset:30752
ds_read_b64_tr_b16 a[158:159],v2 offset:35872
v_mfma_f32_16x16x32_bf16 a[28:31],a[152:155],v[248:251],a[28:31]
ds_read_b64_tr_b16 a[152:153],v2 offset:20512
v_mfma_f32_16x16x32_bf16 a[28:31],a[148:151],v[236:239],a[28:31]
buffer_load_dwordx4 v[108:111],v6,s[28:31],s78 offen offset:3072 sc1
v_mfma_f32_16x16x32_bf16 a[28:31],a[144:147],v[232:235],a[28:31]
s_cmp_ge_u32 2,s71
s_cselect_b32 s80,7,5
s_mul_i32 s80,s80,163840
v_mfma_f32_16x16x32_bf16 a[32:35],a[160:163],v[136:139],a[32:35]
s_add_u32 s80,s80,s81
s_add_u32 s83,s80,20480
ds_read_b64_tr_b16 a[144:145],v2 offset:32
v_mfma_f32_16x16x32_bf16 a[32:35],a[164:167],v[140:143],a[32:35]
ds_read_b64_tr_b16 a[146:147],v2 offset:5152
ds_read_b64_tr_b16 a[148:149],v2 offset:10272
v_mfma_f32_16x16x32_bf16 a[32:35],a[168:171],v[152:155],a[32:35]
ds_read_b64_tr_b16 a[150:151],v2 offset:15392
v_mfma_f32_16x16x32_bf16 a[32:35],a[172:175],v[156:159],a[32:35]
buffer_load_dwordx4 v[112:115],v6,s[28:31],s79 offen sc1
v_mfma_f32_16x16x32_bf16 a[36:39],a[172:175],v[188:191],a[36:39]
ds_read_b64_tr_b16 a[154:155],v2 offset:25632
v_mfma_f32_16x16x32_bf16 a[36:39],a[168:171],v[184:187],a[36:39]
v_mfma_f32_16x16x32_bf16 a[36:39],a[164:167],v[172:175],a[36:39]
v_mfma_f32_16x16x32_bf16 a[36:39],a[160:163],v[168:171],a[36:39]
v_mfma_f32_16x16x32_bf16 a[40:43],a[160:163],v[200:203],a[40:43]
buffer_load_dwordx4 v[116:119],v6,s[28:31],s79 offen offset:1024 sc1
v_mfma_f32_16x16x32_bf16 a[40:43],a[164:167],v[204:207],a[40:43]
v_mfma_f32_16x16x32_bf16 a[40:43],a[168:171],v[216:219],a[40:43]
v_mfma_f32_16x16x32_bf16 a[40:43],a[172:175],v[220:223],a[40:43]
v_mfma_f32_16x16x32_bf16 a[44:47],a[172:175],v[252:255],a[44:47]
ds_read_b64_tr_b16 a[172:173],v1 offset:30784
ds_read_b64_tr_b16 a[174:175],v1 offset:35904
v_mfma_f32_16x16x32_bf16 a[44:47],a[168:171],v[248:251],a[44:47]
buffer_load_dwordx4 v[120:123],v6,s[28:31],s79 offen offset:2048 sc1
v_mfma_f32_16x16x32_bf16 a[44:47],a[164:167],v[236:239],a[44:47]
ds_read_b64_tr_b16 a[164:165],v1 offset:10304
ds_read_b64_tr_b16 a[166:167],v1 offset:15424
v_mfma_f32_16x16x32_bf16 a[44:47],a[160:163],v[232:235],a[44:47]
ds_read_b64_tr_b16 a[160:161],v1 offset:64
v_mfma_f32_16x16x32_bf16 a[48:51],a[176:179],v[136:139],a[48:51]
ds_read_b64_tr_b16 a[162:163],v1 offset:5184
v_mfma_f32_16x16x32_bf16 a[48:51],a[180:183],v[140:143],a[48:51]
ds_read_b64_tr_b16 a[168:169],v1 offset:20544
v_mfma_f32_16x16x32_bf16 a[48:51],a[184:187],v[152:155],a[48:51]
buffer_load_dwordx4 v[124:127],v6,s[28:31],s79 offen offset:3072 sc1
v_mfma_f32_16x16x32_bf16 a[48:51],a[188:191],v[156:159],a[48:51]
ds_read_b64_tr_b16 a[170:171],v1 offset:25664
v_mfma_f32_16x16x32_bf16 a[52:55],a[188:191],v[188:191],a[52:55]
s_add_u32 m0,s82,122880
v_mfma_f32_16x16x32_bf16 a[52:55],a[184:187],v[184:187],a[52:55]
v_mfma_f32_16x16x32_bf16 a[52:55],a[180:183],v[172:175],a[52:55]
v_mfma_f32_16x16x32_bf16 a[52:55],a[176:179],v[168:171],a[52:55]
buffer_load_dwordx4 v7,s[72:75],s80 offen lds
v_mfma_f32_16x16x32_bf16 a[56:59],a[176:179],v[200:203],a[56:59]
v_mfma_f32_16x16x32_bf16 a[56:59],a[180:183],v[204:207],a[56:59]
v_mfma_f32_16x16x32_bf16 a[56:59],a[184:187],v[216:219],a[56:59]
v_mfma_f32_16x16x32_bf16 a[56:59],a[188:191],v[220:223],a[56:59]
v_mfma_f32_16x16x32_bf16 a[60:63],a[188:191],v[252:255],a[60:63]
buffer_load_dwordx4 v8,s[72:75],s80 offen offset:1024 lds
v_mfma_f32_16x16x32_bf16 a[60:63],a[184:187],v[248:251],a[60:63]
ds_read_b64_tr_b16 a[184:185],v2 offset:20576
ds_read_b64_tr_b16 a[186:187],v2 offset:25696
v_mfma_f32_16x16x32_bf16 a[60:63],a[180:183],v[236:239],a[60:63]
ds_read_b64_tr_b16 a[180:181],v2 offset:10336
v_mfma_f32_16x16x32_bf16 a[60:63],a[176:179],v[232:235],a[60:63]
s_waitcnt lgkmcnt(3)
ds_read_b64_tr_b16 a[176:177],v2 offset:96
v_mfma_f32_16x16x32_bf16 a[64:67],a[96:99],v[136:139],a[64:67]
ds_read_b64_tr_b16 a[178:179],v2 offset:5216
v_mfma_f32_16x16x32_bf16 a[64:67],a[100:103],v[140:143],a[64:67]
buffer_load_dwordx4 v9,s[72:75],s80 offen offset:2048 lds
v_mfma_f32_16x16x32_bf16 a[64:67],a[104:107],v[152:155],a[64:67]
ds_read_b64_tr_b16 a[182:183],v2 offset:15456
ds_read_b64_tr_b16 a[188:189],v2 offset:30816
v_mfma_f32_16x16x32_bf16 a[64:67],a[108:111],v[156:159],a[64:67]
ds_read_b64_tr_b16 a[190:191],v2 offset:35936
v_mfma_f32_16x16x32_bf16 a[68:71],a[108:111],v[188:191],a[68:71]
v_mfma_f32_16x16x32_bf16 a[68:71],a[104:107],v[184:187],a[68:71]
v_mfma_f32_16x16x32_bf16 a[68:71],a[100:103],v[172:175],a[68:71]
buffer_load_dwordx4 v10,s[72:75],s80 offen offset:3072 lds
v_mfma_f32_16x16x32_bf16 a[68:71],a[96:99],v[168:171],a[68:71]
v_mfma_f32_16x16x32_bf16 a[72:75],a[96:99],v[200:203],a[72:75]
s_addk_i32 m0,4096
v_mfma_f32_16x16x32_bf16 a[72:75],a[100:103],v[204:207],a[72:75]
v_mfma_f32_16x16x32_bf16 a[72:75],a[104:107],v[216:219],a[72:75]
s_cmp_ge_u32 2,s71
s_cselect_b32 s76,7,5
v_mfma_f32_16x16x32_bf16 a[72:75],a[108:111],v[220:223],a[72:75]
buffer_load_dwordx4 v11,s[72:75],s80 offen lds
v_mfma_f32_16x16x32_bf16 a[76:79],a[108:111],v[252:255],a[76:79]
s_bitcmp1_b32 s54,s76
s_cselect_b32 s77,8192,0
s_lshl_b32 s76,s76,17
v_mfma_f32_16x16x32_bf16 a[76:79],a[104:107],v[248:251],a[76:79]
s_addk_i32 m0,1024
s_add_u32 s76,s76,s77
s_add_u32 s77,s76,4096
v_mfma_f32_16x16x32_bf16 a[76:79],a[100:103],v[236:239],a[76:79]
s_add_u32 s78,s76,16384
s_add_u32 s79,s76,20480
s_max_i32 s76,s76,s18
v_mfma_f32_16x16x32_bf16 a[76:79],a[96:99],v[232:235],a[76:79]
s_max_i32 s77,s77,s19
ds_read_b64_tr_b16 a[96:97],v1 offset:128
)A"
R"A(ds_read_b64_tr_b16 a[98:99],v1 offset:5248
v_mfma_f32_16x16x32_bf16 a[80:83],a[112:115],v[136:139],a[80:83]
s_max_i32 s78,s78,s100
buffer_load_dwordx4 v7,s[72:75],s83 offen lds
v_mfma_f32_16x16x32_bf16 a[80:83],a[116:119],v[140:143],a[80:83]
s_max_i32 s79,s79,s101
ds_read_b64_tr_b16 a[100:101],v1 offset:10368
ds_read_b64_tr_b16 a[102:103],v1 offset:15488
v_mfma_f32_16x16x32_bf16 a[80:83],a[120:123],v[152:155],a[80:83]
ds_read_b64_tr_b16 a[104:105],v1 offset:20608
v_mfma_f32_16x16x32_bf16 a[80:83],a[124:127],v[156:159],a[80:83]
ds_read_b64_tr_b16 a[106:107],v1 offset:25728
v_mfma_f32_16x16x32_bf16 a[84:87],a[124:127],v[188:191],a[84:87]
ds_read_b64_tr_b16 a[108:109],v1 offset:30848
v_mfma_f32_16x16x32_bf16 a[84:87],a[120:123],v[184:187],a[84:87]
buffer_load_dwordx4 v8,s[72:75],s83 offen offset:1024 lds
v_mfma_f32_16x16x32_bf16 a[84:87],a[116:119],v[172:175],a[84:87]
ds_read_b64_tr_b16 a[110:111],v1 offset:35968
v_mfma_f32_16x16x32_bf16 a[84:87],a[112:115],v[168:171],a[84:87]
v_mfma_f32_16x16x32_bf16 a[88:91],a[112:115],v[200:203],a[88:91]
v_mfma_f32_16x16x32_bf16 a[88:91],a[116:119],v[204:207],a[88:91]
v_mfma_f32_16x16x32_bf16 a[88:91],a[120:123],v[216:219],a[88:91]
buffer_load_dwordx4 v9,s[72:75],s83 offen offset:2048 lds
v_mfma_f32_16x16x32_bf16 a[88:91],a[124:127],v[220:223],a[88:91]
v_mfma_f32_16x16x32_bf16 a[92:95],a[124:127],v[252:255],a[92:95]
ds_read_b64_tr_b16 a[124:125],v2 offset:30880
ds_read_b64_tr_b16 a[126:127],v2 offset:36000
v_mfma_f32_16x16x32_bf16 a[92:95],a[120:123],v[248:251],a[92:95]
ds_read_b64_tr_b16 a[120:121],v2 offset:20640
v_mfma_f32_16x16x32_bf16 a[92:95],a[116:119],v[236:239],a[92:95]
ds_read_b64_tr_b16 a[116:117],v2 offset:10400
v_mfma_f32_16x16x32_bf16 a[92:95],a[112:115],v[232:235],a[92:95]
buffer_load_dwordx4 v10,s[72:75],s83 offen offset:3072 lds
v_mfma_f32_16x16x32_bf16 a[192:195],a[128:131],v[128:131],a[192:195]
ds_read_b64_tr_b16 a[112:113],v2 offset:160
ds_read_b64_tr_b16 a[114:115],v2 offset:5280
v_mfma_f32_16x16x32_bf16 a[192:195],a[132:135],v[160:163],a[192:195]
s_addk_i32 m0,4096
ds_read_b64_tr_b16 a[118:119],v2 offset:15520
v_mfma_f32_16x16x32_bf16 a[192:195],a[136:139],v[192:195],a[192:195]
ds_read_b64_tr_b16 a[122:123],v2 offset:25760
v_mfma_f32_16x16x32_bf16 a[192:195],a[140:143],v[224:227],a[192:195]
buffer_load_dwordx4 v[136:139],v6,s[28:31],s76 offen sc1
v_mfma_f32_16x16x32_bf16 a[196:199],a[140:143],v[228:231],a[196:199]
v_mfma_f32_16x16x32_bf16 a[196:199],a[136:139],v[196:199],a[196:199]
v_mfma_f32_16x16x32_bf16 a[196:199],a[132:135],v[164:167],a[196:199]
v_mfma_f32_16x16x32_bf16 a[196:199],a[128:131],v[132:135],a[196:199]
s_waitcnt vmcnt(14)
buffer_load_dwordx4 v11,s[72:75],s83 offen lds
v_mfma_f32_16x16x32_bf16 a[200:203],a[128:131],v[144:147],a[200:203]
ds_read_b64_tr_b16 a[128:129],v1 offset:192
ds_read_b64_tr_b16 a[130:131],v1 offset:5312
v_mfma_f32_16x16x32_bf16 a[200:203],a[132:135],v[176:179],a[200:203]
ds_read_b64_tr_b16 a[132:133],v1 offset:10432
v_mfma_f32_16x16x32_bf16 a[200:203],a[136:139],v[208:211],a[200:203]
ds_read_b64_tr_b16 a[134:135],v1 offset:15552
v_mfma_f32_16x16x32_bf16 a[200:203],a[140:143],v[240:243],a[200:203]
buffer_load_dwordx4 v[140:143],v6,s[28:31],s76 offen offset:1024 sc1
v_mfma_f32_16x16x32_bf16 a[208:211],a[144:147],v[128:131],a[208:211]
ds_read_b64_tr_b16 a[136:137],v1 offset:20672
ds_read_b64_tr_b16 a[138:139],v1 offset:25792
v_mfma_f32_16x16x32_bf16 a[208:211],a[148:151],v[160:163],a[208:211]
ds_read_b64_tr_b16 a[140:141],v1 offset:30912
v_mfma_f32_16x16x32_bf16 a[208:211],a[152:155],v[192:195],a[208:211]
ds_read_b64_tr_b16 a[142:143],v1 offset:36032
v_mfma_f32_16x16x32_bf16 a[208:211],a[156:159],v[224:227],a[208:211]
buffer_load_dwordx4 v[152:155],v6,s[28:31],s76 offen offset:2048 sc1
v_mfma_f32_16x16x32_bf16 a[212:215],a[156:159],v[228:231],a[212:215]
v_mfma_f32_16x16x32_bf16 a[212:215],a[152:155],v[196:199],a[212:215]
v_mfma_f32_16x16x32_bf16 a[212:215],a[148:151],v[164:167],a[212:215]
v_mfma_f32_16x16x32_bf16 a[212:215],a[144:147],v[132:135],a[212:215]
buffer_load_dwordx4 v[156:159],v6,s[28:31],s76 offen offset:3072 sc1
v_mfma_f32_16x16x32_bf16 a[216:219],a[144:147],v[144:147],a[216:219]
s_add_u32 s76,s70,3
ds_read_b64_tr_b16 a[144:145],v2 offset:224
ds_read_b64_tr_b16 a[146:147],v2 offset:5344
v_mfma_f32_16x16x32_bf16 a[216:219],a[148:151],v[176:179],a[216:219]
s_and_b32 s76,s76,3
s_lshl_b32 s76,s76,21
ds_read_b64_tr_b16 a[148:149],v2 offset:10464
v_mfma_f32_16x16x32_bf16 a[216:219],a[152:155],v[208:211],a[216:219]
s_add_u32 s76,s76,0
s_max_i32 s76,s76,s101
ds_read_b64_tr_b16 a[150:151],v2 offset:15584
v_mfma_f32_16x16x32_bf16 a[216:219],a[156:159],v[240:243],a[216:219]
buffer_load_dwordx4 v[168:171],v6,s[28:31],s77 offen sc1
v_mfma_f32_16x16x32_bf16 a[224:227],a[160:163],v[128:131],a[224:227]
ds_read_b64_tr_b16 a[152:153],v2 offset:20704
ds_read_b64_tr_b16 a[154:155],v2 offset:25824
v_mfma_f32_16x16x32_bf16 a[224:227],a[164:167],v[160:163],a[224:227]
ds_read_b64_tr_b16 a[156:157],v2 offset:30944
v_mfma_f32_16x16x32_bf16 a[224:227],a[168:171],v[192:195],a[224:227]
ds_read_b64_tr_b16 a[158:159],v2 offset:36064
v_mfma_f32_16x16x32_bf16 a[224:227],a[172:175],v[224:227],a[224:227]
buffer_load_dwordx4 v[172:175],v6,s[28:31],s77 offen offset:1024 sc1
v_mfma_f32_16x16x32_bf16 a[228:231],a[172:175],v[228:231],a[228:231]
v_mfma_f32_16x16x32_bf16 a[228:231],a[168:171],v[196:199],a[228:231]
v_mfma_f32_16x16x32_bf16 a[228:231],a[164:167],v[164:167],a[228:231]
v_mfma_f32_16x16x32_bf16 a[228:231],a[160:163],v[132:135],a[228:231]
v_mfma_f32_16x16x32_bf16 a[232:235],a[160:163],v[144:147],a[232:235]
buffer_load_dwordx4 v[184:187],v6,s[28:31],s77 offen offset:2048 sc1
v_mfma_f32_16x16x32_bf16 a[232:235],a[164:167],v[176:179],a[232:235]
ds_read_b64_tr_b16 a[160:161],v1 offset:256
ds_read_b64_tr_b16 a[162:163],v1 offset:5376
v_mfma_f32_16x16x32_bf16 a[232:235],a[168:171],v[208:211],a[232:235]
ds_read_b64_tr_b16 a[164:165],v1 offset:10496
v_mfma_f32_16x16x32_bf16 a[232:235],a[172:175],v[240:243],a[232:235]
s_waitcnt lgkmcnt(3)
ds_read_b64_tr_b16 a[166:167],v1 offset:15616
v_mfma_f32_16x16x32_bf16 a[240:243],a[176:179],v[128:131],a[240:243]
ds_read_b64_tr_b16 a[168:169],v1 offset:20736
v_mfma_f32_16x16x32_bf16 a[240:243],a[180:183],v[160:163],a[240:243]
buffer_load_dwordx4 v[188:191],v6,s[28:31],s77 offen offset:3072 sc1
v_mfma_f32_16x16x32_bf16 a[240:243],a[184:187],v[192:195],a[240:243]
s_add_u32 s77,s70,2
ds_read_b64_tr_b16 a[170:171],v1 offset:25856
ds_read_b64_tr_b16 a[172:173],v1 offset:30976
v_mfma_f32_16x16x32_bf16 a[240:243],a[188:191],v[224:227],a[240:243]
s_and_b32 s77,s77,3
s_lshl_b32 s77,s77,21
ds_read_b64_tr_b16 a[174:175],v1 offset:36096
v_mfma_f32_16x16x32_bf16 a[244:247],a[188:191],v[228:231],a[244:247]
s_add_u32 s77,s77,65536
s_max_i32 s77,s77,s101
v_mfma_f32_16x16x32_bf16 a[244:247],a[184:187],v[196:199],a[244:247]
v_mfma_f32_16x16x32_bf16 a[244:247],a[180:183],v[164:167],a[244:247]
buffer_load_dwordx4 v[200:203],v6,s[28:31],s78 offen sc1
v_mfma_f32_16x16x32_bf16 a[244:247],a[176:179],v[132:135],a[244:247]
v_mfma_f32_16x16x32_bf16 a[248:251],a[176:179],v[144:147],a[248:251]
ds_read_b64_tr_b16 a[176:177],v2 offset:288
ds_read_b64_tr_b16 a[178:179],v2 offset:5408
v_mfma_f32_16x16x32_bf16 a[248:251],a[180:183],v[176:179],a[248:251]
ds_read_b64_tr_b16 a[180:181],v2 offset:10528
v_mfma_f32_16x16x32_bf16 a[248:251],a[184:187],v[208:211],a[248:251]
ds_read_b64_tr_b16 a[182:183],v2 offset:15648
v_mfma_f32_16x16x32_bf16 a[248:251],a[188:191],v[240:243],a[248:251]
buffer_load_dwordx4 v[204:207],v6,s[28:31],s78 offen offset:1024 sc1
v_mfma_f32_16x16x32_bf16 a[0:3],a[96:99],v[128:131],a[0:3]
ds_read_b64_tr_b16 a[184:185],v2 offset:20768
ds_read_b64_tr_b16 a[186:187],v2 offset:25888
v_mfma_f32_16x16x32_bf16 a[0:3],a[100:103],v[160:163],a[0:3]
ds_read_b64_tr_b16 a[188:189],v2 offset:31008
v_mfma_f32_16x16x32_bf16 a[0:3],a[104:107],v[192:195],a[0:3]
ds_read_b64_tr_b16 a[190:191],v2 offset:36128
v_mfma_f32_16x16x32_bf16 a[0:3],a[108:111],v[224:227],a[0:3]
v_mfma_f32_16x16x32_bf16 a[4:7],a[108:111],v[228:231],a[4:7]
buffer_load_dwordx4 v[216:219],v6,s[28:31],s78 offen offset:2048 sc1
v_mfma_f32_16x16x32_bf16 a[4:7],a[104:107],v[196:199],a[4:7]
v_mfma_f32_16x16x32_bf16 a[4:7],a[100:103],v[164:167],a[4:7]
v_mfma_f32_16x16x32_bf16 a[4:7],a[96:99],v[132:135],a[4:7]
v_mfma_f32_16x16x32_bf16 a[8:11],a[96:99],v[144:147],a[8:11]
v_mfma_f32_16x16x32_bf16 a[8:11],a[100:103],v[176:179],a[8:11]
buffer_load_dwordx4 v[220:223],v6,s[28:31],s78 offen offset:3072 sc1
v_mfma_f32_16x16x32_bf16 a[8:11],a[104:107],v[208:211],a[8:11]
s_add_u32 s78,s70,1
s_and_b32 s78,s78,3
s_lshl_b32 s78,s78,21
v_mfma_f32_16x16x32_bf16 a[8:11],a[108:111],v[240:243],a[8:11]
s_add_u32 s78,s78,131072
s_max_i32 s78,s78,s101
v_mfma_f32_16x16x32_bf16 a[16:19],a[112:115],v[128:131],a[16:19]
v_mfma_f32_16x16x32_bf16 a[16:19],a[116:119],v[160:163],a[16:19]
v_mfma_f32_16x16x32_bf16 a[16:19],a[120:123],v[192:195],a[16:19]
buffer_load_dwordx4 v[232:235],v6,s[28:31],s79 offen sc1
v_mfma_f32_16x16x32_bf16 a[16:19],a[124:127],v[224:227],a[16:19]
v_mfma_f32_16x16x32_bf16 a[20:23],a[124:127],v[228:231],a[20:23]
v_mfma_f32_16x16x32_bf16 a[20:23],a[120:123],v[196:199],a[20:23]
v_mfma_f32_16x16x32_bf16 a[20:23],a[116:119],v[164:167],a[20:23]
v_mfma_f32_16x16x32_bf16 a[20:23],a[112:115],v[132:135],a[20:23]
buffer_load_dwordx4 v[236:239],v6,s[28:31],s79 offen offset:1024 sc1
v_mfma_f32_16x16x32_bf16 a[24:27],a[112:115],v[144:147],a[24:27]
v_mfma_f32_16x16x32_bf16 a[24:27],a[116:119],v[176:179],a[24:27]
v_mfma_f32_16x16x32_bf16 a[24:27],a[120:123],v[208:211],a[24:27]
v_mfma_f32_16x16x32_bf16 a[24:27],a[124:127],v[240:243],a[24:27]
v_mfma_f32_16x16x32_bf16 a[32:35],a[128:131],v[128:131],a[32:35]
buffer_load_dwordx4 v[248:251],v6,s[28:31],s79 offen offset:2048 sc1
v_mfma_f32_16x16x32_bf16 a[32:35],a[132:135],v[160:163],a[32:35]
v_mfma_f32_16x16x32_bf16 a[32:35],a[136:139],v[192:195],a[32:35]
v_mfma_f32_16x16x32_bf16 a[32:35],a[140:143],v[224:227],a[32:35]
v_mfma_f32_16x16x32_bf16 a[36:39],a[140:143],v[228:231],a[36:39]
v_mfma_f32_16x16x32_bf16 a[36:39],a[136:139],v[196:199],a[36:39]
buffer_load_dwordx4 v[252:255],v6,s[28:31],s79 offen offset:3072 sc1
v_mfma_f32_16x16x32_bf16 a[36:39],a[132:135],v[164:167],a[36:39]
v_mfma_f32_16x16x32_bf16 a[36:39],a[128:131],v[132:135],a[36:39]
v_mfma_f32_16x16x32_bf16 a[40:43],a[128:131],v[144:147],a[40:43]
v_mfma_f32_16x16x32_bf16 a[40:43],a[132:135],v[176:179],a[40:43]
v_mfma_f32_16x16x32_bf16 a[40:43],a[136:139],v[208:211],a[40:43]
s_waitcnt vmcnt(15)
v_mfma_f32_16x16x32_bf16 a[40:43],a[140:143],v[240:243],a[40:43]
s_barrier
ds_read_b64_tr_b16 a[96:97],v42
ds_read_b64_tr_b16 a[98:99],v42 offset:5120
v_mfma_f32_16x16x32_bf16 a[48:51],a[144:147],v[128:131],a[48:51]
ds_read_b64_tr_b16 a[100:101],v42 offset:10240
v_mfma_f32_16x16x32_bf16 a[48:51],a[148:151],v[160:163],a[48:51]
ds_read_b64_tr_b16 a[102:103],v42 offset:15360
v_mfma_f32_16x16x32_bf16 a[48:51],a[152:155],v[192:195],a[48:51]
ds_read_b64_tr_b16 a[104:105],v42 offset:20480
v_mfma_f32_16x16x32_bf16 a[48:51],a[156:159],v[224:227],a[48:51]
ds_read_b64_tr_b16 a[106:107],v42 offset:25600
ds_read_b64_tr_b16 a[108:109],v42 offset:30720
v_mfma_f32_16x16x32_bf16 a[52:55],a[156:159],v[228:231],a[52:55]
ds_read_b64_tr_b16 a[110:111],v42 offset:35840
v_mfma_f32_16x16x32_bf16 a[52:55],a[152:155],v[196:199],a[52:55]
ds_read_b64_tr_b16 a[112:113],v44
v_mfma_f32_16x16x32_bf16 a[52:55],a[148:151],v[164:167],a[52:55]
ds_read_b64_tr_b16 a[114:115],v44 offset:5120
v_mfma_f32_16x16x32_bf16 a[52:55],a[144:147],v[132:135],a[52:55]
ds_read_b64_tr_b16 a[116:117],v44 offset:10240
ds_read_b64_tr_b16 a[118:119],v44 offset:15360
v_mfma_f32_16x16x32_bf16 a[56:59],a[144:147],v[144:147],a[56:59]
ds_read_b64_tr_b16 a[120:121],v44 offset:20480
v_mfma_f32_16x16x32_bf16 a[56:59],a[148:151],v[176:179],a[56:59]
ds_read_b64_tr_b16 a[122:123],v44 offset:25600
v_mfma_f32_16x16x32_bf16 a[56:59],a[152:155],v[208:211],a[56:59]
ds_read_b64_tr_b16 a[124:125],v44 offset:30720
v_mfma_f32_16x16x32_bf16 a[56:59],a[156:159],v[240:243],a[56:59]
)A"
R"A(s_waitcnt lgkmcnt(8)
ds_read_b64_tr_b16 a[126:127],v44 offset:35840
ds_read_b64_tr_b16 a[128:129],v43 offset:32
v_mfma_f32_16x16x32_bf16 a[64:67],a[160:163],v[128:131],a[64:67]
ds_read_b64_tr_b16 a[130:131],v43 offset:5152
v_mfma_f32_16x16x32_bf16 a[64:67],a[164:167],v[160:163],a[64:67]
ds_read_b64_tr_b16 a[132:133],v43 offset:10272
v_mfma_f32_16x16x32_bf16 a[64:67],a[168:171],v[192:195],a[64:67]
ds_read_b64_tr_b16 a[134:135],v43 offset:15392
v_mfma_f32_16x16x32_bf16 a[64:67],a[172:175],v[224:227],a[64:67]
ds_read_b64_tr_b16 a[136:137],v43 offset:20512
ds_read_b64_tr_b16 a[138:139],v43 offset:25632
v_mfma_f32_16x16x32_bf16 a[68:71],a[172:175],v[228:231],a[68:71]
ds_read_b64_tr_b16 a[140:141],v43 offset:30752
v_mfma_f32_16x16x32_bf16 a[68:71],a[168:171],v[196:199],a[68:71]
ds_read_b64_tr_b16 a[142:143],v43 offset:35872
v_mfma_f32_16x16x32_bf16 a[68:71],a[164:167],v[164:167],a[68:71]
ds_read_b64_tr_b16 a[144:145],v45 offset:32
v_mfma_f32_16x16x32_bf16 a[68:71],a[160:163],v[132:135],a[68:71]
ds_read_b64_tr_b16 a[146:147],v45 offset:5152
ds_read_b64_tr_b16 a[148:149],v45 offset:10272
v_mfma_f32_16x16x32_bf16 a[72:75],a[160:163],v[144:147],a[72:75]
ds_read_b64_tr_b16 a[150:151],v45 offset:15392
v_mfma_f32_16x16x32_bf16 a[72:75],a[164:167],v[176:179],a[72:75]
ds_read_b64_tr_b16 a[152:153],v45 offset:20512
v_mfma_f32_16x16x32_bf16 a[72:75],a[168:171],v[208:211],a[72:75]
ds_read_b64_tr_b16 a[154:155],v45 offset:25632
v_mfma_f32_16x16x32_bf16 a[72:75],a[172:175],v[240:243],a[72:75]
ds_read_b64_tr_b16 a[156:157],v45 offset:30752
ds_read_b64_tr_b16 a[158:159],v45 offset:35872
v_mfma_f32_16x16x32_bf16 a[80:83],a[176:179],v[128:131],a[80:83]
ds_read_b64_tr_b16 a[160:161],v42 offset:64
v_mfma_f32_16x16x32_bf16 a[80:83],a[180:183],v[160:163],a[80:83]
ds_read_b64_tr_b16 a[162:163],v42 offset:5184
v_mfma_f32_16x16x32_bf16 a[80:83],a[184:187],v[192:195],a[80:83]
ds_read_b64_tr_b16 a[164:165],v42 offset:10304
v_mfma_f32_16x16x32_bf16 a[80:83],a[188:191],v[224:227],a[80:83]
ds_read_b64_tr_b16 a[166:167],v42 offset:15424
ds_read_b64_tr_b16 a[168:169],v42 offset:20544
v_mfma_f32_16x16x32_bf16 a[84:87],a[188:191],v[228:231],a[84:87]
ds_read_b64_tr_b16 a[170:171],v42 offset:25664
v_mfma_f32_16x16x32_bf16 a[84:87],a[184:187],v[196:199],a[84:87]
ds_read_b64_tr_b16 a[172:173],v42 offset:30784
v_mfma_f32_16x16x32_bf16 a[84:87],a[180:183],v[164:167],a[84:87]
ds_read_b64_tr_b16 a[174:175],v42 offset:35904
v_mfma_f32_16x16x32_bf16 a[84:87],a[176:179],v[132:135],a[84:87]
v_mfma_f32_16x16x32_bf16 a[88:91],a[176:179],v[144:147],a[88:91]
ds_read_b64_tr_b16 a[176:177],v44 offset:64
ds_read_b64_tr_b16 a[178:179],v44 offset:5184
v_mfma_f32_16x16x32_bf16 a[88:91],a[180:183],v[176:179],a[88:91]
ds_read_b64_tr_b16 a[180:181],v44 offset:10304
v_mfma_f32_16x16x32_bf16 a[88:91],a[184:187],v[208:211],a[88:91]
ds_read_b64_tr_b16 a[182:183],v44 offset:15424
v_mfma_f32_16x16x32_bf16 a[88:91],a[188:191],v[240:243],a[88:91]
ds_read_b64_tr_b16 a[184:185],v44 offset:20544
v_mfma_f32_16x16x32_bf16 a[192:195],a[96:99],v[64:67],a[192:195]
ds_read_b64_tr_b16 a[186:187],v44 offset:25664
ds_read_b64_tr_b16 a[188:189],v44 offset:30784
v_mfma_f32_16x16x32_bf16 a[192:195],a[100:103],v[68:71],a[192:195]
ds_read_b64_tr_b16 a[190:191],v44 offset:35904
v_mfma_f32_16x16x32_bf16 a[192:195],a[104:107],v[72:75],a[192:195]
s_waitcnt lgkmcnt(8)
v_mfma_f32_16x16x32_bf16 a[192:195],a[108:111],v[76:79],a[192:195]
v_mfma_f32_16x16x32_bf16 a[196:199],a[108:111],v[92:95],a[196:199]
v_mfma_f32_16x16x32_bf16 a[196:199],a[104:107],v[88:91],a[196:199]
v_mfma_f32_16x16x32_bf16 a[196:199],a[100:103],v[84:87],a[196:199]
v_mfma_f32_16x16x32_bf16 a[196:199],a[96:99],v[80:83],a[196:199]
v_mfma_f32_16x16x32_bf16 a[200:203],a[96:99],v[96:99],a[200:203]
ds_read_b64_tr_b16 a[96:97],v43 offset:96
ds_read_b64_tr_b16 a[98:99],v43 offset:5216
v_mfma_f32_16x16x32_bf16 a[200:203],a[100:103],v[100:103],a[200:203]
ds_read_b64_tr_b16 a[100:101],v43 offset:10336
v_mfma_f32_16x16x32_bf16 a[200:203],a[104:107],v[104:107],a[200:203]
ds_read_b64_tr_b16 a[102:103],v43 offset:15456
v_mfma_f32_16x16x32_bf16 a[200:203],a[108:111],v[108:111],a[200:203]
ds_read_b64_tr_b16 a[104:105],v43 offset:20576
v_mfma_f32_16x16x32_bf16 a[192:195],a[112:115],v[136:139],a[192:195]
s_waitcnt vmcnt(3)
ds_read_b64_tr_b16 a[106:107],v43 offset:25696
ds_read_b64_tr_b16 a[108:109],v43 offset:30816
v_mfma_f32_16x16x32_bf16 a[192:195],a[116:119],v[140:143],a[192:195]
ds_read_b64_tr_b16 a[110:111],v43 offset:35936
v_mfma_f32_16x16x32_bf16 a[192:195],a[120:123],v[152:155],a[192:195]
v_mfma_f32_16x16x32_bf16 a[192:195],a[124:127],v[156:159],a[192:195]
v_mfma_f32_16x16x32_bf16 a[196:199],a[124:127],v[188:191],a[196:199]
v_mfma_f32_16x16x32_bf16 a[196:199],a[120:123],v[184:187],a[196:199]
v_mfma_f32_16x16x32_bf16 a[196:199],a[116:119],v[172:175],a[196:199]
v_mfma_f32_16x16x32_bf16 a[196:199],a[112:115],v[168:171],a[196:199]
v_mfma_f32_16x16x32_bf16 a[200:203],a[112:115],v[200:203],a[200:203]
ds_read_b64_tr_b16 a[112:113],v45 offset:96
ds_read_b64_tr_b16 a[114:115],v45 offset:5216
v_mfma_f32_16x16x32_bf16 a[200:203],a[116:119],v[204:207],a[200:203]
v_accvgpr_read_b32 v128,a192
v_accvgpr_read_b32 v129,a193
v_mfma_f32_16x16x32_bf16 a[200:203],a[120:123],v[216:219],a[200:203]
v_cvt_pk_bf16_f32 v128,v128,v129
v_accvgpr_read_b32 v130,a194
v_mfma_f32_16x16x32_bf16 a[200:203],a[124:127],v[220:223],a[200:203]
v_accvgpr_read_b32 v131,a195
v_cvt_pk_bf16_f32 v129,v130,v131
v_mfma_f32_16x16x32_bf16 a[208:211],a[128:131],v[64:67],a[208:211]
ds_read_b64_tr_b16 a[116:117],v45 offset:10336
ds_read_b64_tr_b16 a[118:119],v45 offset:15456
v_mfma_f32_16x16x32_bf16 a[208:211],a[132:135],v[68:71],a[208:211]
v_accvgpr_read_b32 v132,a196
v_accvgpr_read_b32 v133,a197
v_mfma_f32_16x16x32_bf16 a[208:211],a[136:139],v[72:75],a[208:211]
v_cvt_pk_bf16_f32 v132,v132,v133
v_accvgpr_read_b32 v134,a198
v_mfma_f32_16x16x32_bf16 a[208:211],a[140:143],v[76:79],a[208:211]
v_accvgpr_read_b32 v135,a199
v_cvt_pk_bf16_f32 v133,v134,v135
v_mfma_f32_16x16x32_bf16 a[212:215],a[140:143],v[92:95],a[212:215]
ds_read_b64_tr_b16 a[120:121],v45 offset:20576
ds_read_b64_tr_b16 a[122:123],v45 offset:25696
v_mfma_f32_16x16x32_bf16 a[212:215],a[136:139],v[88:91],a[212:215]
ds_read_b64_tr_b16 a[124:125],v45 offset:30816
v_accvgpr_read_b32 v144,a200
v_mfma_f32_16x16x32_bf16 a[212:215],a[132:135],v[84:87],a[212:215]
ds_read_b64_tr_b16 a[126:127],v45 offset:35936
v_accvgpr_read_b32 v145,a201
v_mfma_f32_16x16x32_bf16 a[212:215],a[128:131],v[80:83],a[212:215]
v_cvt_pk_bf16_f32 v144,v144,v145
v_accvgpr_read_b32 v146,a202
v_mfma_f32_16x16x32_bf16 a[216:219],a[128:131],v[96:99],a[216:219]
v_accvgpr_read_b32 v147,a203
v_cvt_pk_bf16_f32 v145,v146,v147
v_mfma_f32_16x16x32_bf16 a[216:219],a[132:135],v[100:103],a[216:219]
ds_read_b64_tr_b16 a[128:129],v42 offset:128
ds_read_b64_tr_b16 a[130:131],v42 offset:5248
v_mfma_f32_16x16x32_bf16 a[216:219],a[136:139],v[104:107],a[216:219]
ds_read_b64_tr_b16 a[132:133],v42 offset:10368
v_mfma_f32_16x16x32_bf16 a[216:219],a[140:143],v[108:111],a[216:219]
ds_read_b64_tr_b16 a[134:135],v42 offset:15488
v_mfma_f32_16x16x32_bf16 a[208:211],a[144:147],v[136:139],a[208:211]
ds_read_b64_tr_b16 a[136:137],v42 offset:20608
v_mfma_f32_16x16x32_bf16 a[208:211],a[148:151],v[140:143],a[208:211]
ds_read_b64_tr_b16 a[138:139],v42 offset:25728
ds_read_b64_tr_b16 a[140:141],v42 offset:30848
v_mfma_f32_16x16x32_bf16 a[208:211],a[152:155],v[152:155],a[208:211]
ds_read_b64_tr_b16 a[142:143],v42 offset:35968
v_mfma_f32_16x16x32_bf16 a[208:211],a[156:159],v[156:159],a[208:211]
v_mfma_f32_16x16x32_bf16 a[212:215],a[156:159],v[188:191],a[212:215]
v_mfma_f32_16x16x32_bf16 a[212:215],a[152:155],v[184:187],a[212:215]
v_mfma_f32_16x16x32_bf16 a[212:215],a[148:151],v[172:175],a[212:215]
v_mfma_f32_16x16x32_bf16 a[212:215],a[144:147],v[168:171],a[212:215]
v_mfma_f32_16x16x32_bf16 a[216:219],a[144:147],v[200:203],a[216:219]
ds_read_b64_tr_b16 a[144:145],v44 offset:128
ds_read_b64_tr_b16 a[146:147],v44 offset:5248
v_mfma_f32_16x16x32_bf16 a[216:219],a[148:151],v[204:207],a[216:219]
v_accvgpr_read_b32 v130,a208
v_accvgpr_read_b32 v131,a209
v_mfma_f32_16x16x32_bf16 a[216:219],a[152:155],v[216:219],a[216:219]
v_accvgpr_read_b32 v16,a210
v_accvgpr_read_b32 v17,a211
v_mfma_f32_16x16x32_bf16 a[216:219],a[156:159],v[220:223],a[216:219]
v_cvt_pk_bf16_f32 v130,v130,v131
v_cvt_pk_bf16_f32 v131,v16,v17
v_mfma_f32_16x16x32_bf16 a[224:227],a[160:163],v[64:67],a[224:227]
buffer_store_dwordx4 v[128:131],v6,s[32:35],s84 offen sc1
v_mfma_f32_16x16x32_bf16 a[224:227],a[164:167],v[68:71],a[224:227]
v_accvgpr_read_b32 v134,a212
v_accvgpr_read_b32 v135,a213
v_mfma_f32_16x16x32_bf16 a[224:227],a[168:171],v[72:75],a[224:227]
v_accvgpr_read_b32 v12,a214
v_accvgpr_read_b32 v13,a215
v_mfma_f32_16x16x32_bf16 a[224:227],a[172:175],v[76:79],a[224:227]
v_cvt_pk_bf16_f32 v134,v134,v135
v_cvt_pk_bf16_f32 v135,v12,v13
v_mfma_f32_16x16x32_bf16 a[228:231],a[172:175],v[92:95],a[228:231]
buffer_store_dwordx4 v[132:135],v6,s[32:35],s85 offen sc1
v_mfma_f32_16x16x32_bf16 a[228:231],a[168:171],v[88:91],a[228:231]
v_accvgpr_read_b32 v146,a216
v_accvgpr_read_b32 v147,a217
v_mfma_f32_16x16x32_bf16 a[228:231],a[164:167],v[84:87],a[228:231]
v_accvgpr_read_b32 v14,a218
v_accvgpr_read_b32 v15,a219
v_mfma_f32_16x16x32_bf16 a[228:231],a[160:163],v[80:83],a[228:231]
v_cvt_pk_bf16_f32 v146,v146,v147
v_cvt_pk_bf16_f32 v147,v14,v15
v_mfma_f32_16x16x32_bf16 a[232:235],a[160:163],v[96:99],a[232:235]
buffer_store_dwordx4 v[144:147],v6,s[32:35],s86 offen sc1
v_mfma_f32_16x16x32_bf16 a[232:235],a[164:167],v[100:103],a[232:235]
ds_read_b64_tr_b16 a[148:149],v44 offset:10368
ds_read_b64_tr_b16 a[150:151],v44 offset:15488
v_mfma_f32_16x16x32_bf16 a[232:235],a[168:171],v[104:107],a[232:235]
ds_read_b64_tr_b16 a[152:153],v44 offset:20608
v_mfma_f32_16x16x32_bf16 a[232:235],a[172:175],v[108:111],a[232:235]
s_waitcnt lgkmcnt(3)
ds_read_b64_tr_b16 a[154:155],v44 offset:25728
v_mfma_f32_16x16x32_bf16 a[224:227],a[176:179],v[136:139],a[224:227]
ds_read_b64_tr_b16 a[156:157],v44 offset:30848
v_mfma_f32_16x16x32_bf16 a[224:227],a[180:183],v[140:143],a[224:227]
ds_read_b64_tr_b16 a[158:159],v44 offset:35968
ds_read_b64_tr_b16 a[160:161],v43 offset:160
v_mfma_f32_16x16x32_bf16 a[224:227],a[184:187],v[152:155],a[224:227]
ds_read_b64_tr_b16 a[162:163],v43 offset:5280
v_mfma_f32_16x16x32_bf16 a[224:227],a[188:191],v[156:159],a[224:227]
ds_read_b64_tr_b16 a[164:165],v43 offset:10400
v_mfma_f32_16x16x32_bf16 a[228:231],a[188:191],v[188:191],a[228:231]
ds_read_b64_tr_b16 a[166:167],v43 offset:15520
v_mfma_f32_16x16x32_bf16 a[228:231],a[184:187],v[184:187],a[228:231]
ds_read_b64_tr_b16 a[168:169],v43 offset:20640
ds_read_b64_tr_b16 a[170:171],v43 offset:25760
v_mfma_f32_16x16x32_bf16 a[228:231],a[180:183],v[172:175],a[228:231]
ds_read_b64_tr_b16 a[172:173],v43 offset:30880
v_mfma_f32_16x16x32_bf16 a[228:231],a[176:179],v[168:171],a[228:231]
ds_read_b64_tr_b16 a[174:175],v43 offset:36000
v_mfma_f32_16x16x32_bf16 a[232:235],a[176:179],v[200:203],a[232:235]
ds_read_b64_tr_b16 a[176:177],v45 offset:160
v_mfma_f32_16x16x32_bf16 a[232:235],a[180:183],v[204:207],a[232:235]
v_accvgpr_read_b32 v160,a224
v_accvgpr_read_b32 v161,a225
v_mfma_f32_16x16x32_bf16 a[232:235],a[184:187],v[216:219],a[232:235]
v_cvt_pk_bf16_f32 v160,v160,v161
v_accvgpr_read_b32 v162,a226
v_mfma_f32_16x16x32_bf16 a[232:235],a[188:191],v[220:223],a[232:235]
v_accvgpr_read_b32 v163,a227
v_cvt_pk_bf16_f32 v161,v162,v163
v_mfma_f32_16x16x32_bf16 a[240:243],a[96:99],v[64:67],a[240:243]
ds_read_b64_tr_b16 a[178:179],v45 offset:5280
ds_read_b64_tr_b16 a[180:181],v45 offset:10400
v_mfma_f32_16x16x32_bf16 a[240:243],a[100:103],v[68:71],a[240:243]
v_accvgpr_read_b32 v164,a228
v_accvgpr_read_b32 v165,a229
v_mfma_f32_16x16x32_bf16 a[240:243],a[104:107],v[72:75],a[240:243]
v_cvt_pk_bf16_f32 v164,v164,v165
v_accvgpr_read_b32 v166,a230
v_mfma_f32_16x16x32_bf16 a[240:243],a[108:111],v[76:79],a[240:243]
v_accvgpr_read_b32 v167,a231
v_cvt_pk_bf16_f32 v165,v166,v167
v_mfma_f32_16x16x32_bf16 a[244:247],a[108:111],v[92:95],a[244:247]
)A"
R"A(ds_read_b64_tr_b16 a[182:183],v45 offset:15520
ds_read_b64_tr_b16 a[184:185],v45 offset:20640
v_mfma_f32_16x16x32_bf16 a[244:247],a[104:107],v[88:91],a[244:247]
v_accvgpr_read_b32 v176,a232
v_accvgpr_read_b32 v177,a233
v_mfma_f32_16x16x32_bf16 a[244:247],a[100:103],v[84:87],a[244:247]
v_cvt_pk_bf16_f32 v176,v176,v177
v_accvgpr_read_b32 v178,a234
v_mfma_f32_16x16x32_bf16 a[244:247],a[96:99],v[80:83],a[244:247]
v_accvgpr_read_b32 v179,a235
v_cvt_pk_bf16_f32 v177,v178,v179
v_mfma_f32_16x16x32_bf16 a[248:251],a[96:99],v[96:99],a[248:251]
ds_read_b64_tr_b16 a[186:187],v45 offset:25760
ds_read_b64_tr_b16 a[188:189],v45 offset:30880
v_mfma_f32_16x16x32_bf16 a[248:251],a[100:103],v[100:103],a[248:251]
ds_read_b64_tr_b16 a[190:191],v45 offset:36000
v_mfma_f32_16x16x32_bf16 a[248:251],a[104:107],v[104:107],a[248:251]
ds_read_b64_tr_b16 a[96:97],v42 offset:192
v_mfma_f32_16x16x32_bf16 a[248:251],a[108:111],v[108:111],a[248:251]
ds_read_b64_tr_b16 a[98:99],v42 offset:5312
v_mfma_f32_16x16x32_bf16 a[240:243],a[112:115],v[136:139],a[240:243]
ds_read_b64_tr_b16 a[100:101],v42 offset:10432
ds_read_b64_tr_b16 a[102:103],v42 offset:15552
v_mfma_f32_16x16x32_bf16 a[240:243],a[116:119],v[140:143],a[240:243]
ds_read_b64_tr_b16 a[104:105],v42 offset:20672
v_mfma_f32_16x16x32_bf16 a[240:243],a[120:123],v[152:155],a[240:243]
ds_read_b64_tr_b16 a[106:107],v42 offset:25792
v_mfma_f32_16x16x32_bf16 a[240:243],a[124:127],v[156:159],a[240:243]
ds_read_b64_tr_b16 a[108:109],v42 offset:30912
v_mfma_f32_16x16x32_bf16 a[244:247],a[124:127],v[188:191],a[244:247]
ds_read_b64_tr_b16 a[110:111],v42 offset:36032
v_mfma_f32_16x16x32_bf16 a[244:247],a[120:123],v[184:187],a[244:247]
v_mfma_f32_16x16x32_bf16 a[244:247],a[116:119],v[172:175],a[244:247]
v_mfma_f32_16x16x32_bf16 a[244:247],a[112:115],v[168:171],a[244:247]
v_mfma_f32_16x16x32_bf16 a[248:251],a[112:115],v[200:203],a[248:251]
ds_read_b64_tr_b16 a[112:113],v44 offset:192
ds_read_b64_tr_b16 a[114:115],v44 offset:5312
v_mfma_f32_16x16x32_bf16 a[248:251],a[116:119],v[204:207],a[248:251]
v_accvgpr_read_b32 v162,a240
v_accvgpr_read_b32 v163,a241
v_mfma_f32_16x16x32_bf16 a[248:251],a[120:123],v[216:219],a[248:251]
v_accvgpr_read_b32 v16,a242
v_accvgpr_read_b32 v17,a243
v_mfma_f32_16x16x32_bf16 a[248:251],a[124:127],v[220:223],a[248:251]
v_cvt_pk_bf16_f32 v162,v162,v163
v_cvt_pk_bf16_f32 v163,v16,v17
v_mfma_f32_16x16x32_bf16 a[0:3],a[128:131],v[64:67],a[0:3]
buffer_store_dwordx4 v[160:163],v6,s[32:35],s84 offen offset:1024 sc1
v_mfma_f32_16x16x32_bf16 a[0:3],a[132:135],v[68:71],a[0:3]
v_accvgpr_read_b32 v166,a244
v_accvgpr_read_b32 v167,a245
v_mfma_f32_16x16x32_bf16 a[0:3],a[136:139],v[72:75],a[0:3]
v_accvgpr_read_b32 v12,a246
v_accvgpr_read_b32 v13,a247
v_mfma_f32_16x16x32_bf16 a[0:3],a[140:143],v[76:79],a[0:3]
v_cvt_pk_bf16_f32 v166,v166,v167
v_cvt_pk_bf16_f32 v167,v12,v13
v_mfma_f32_16x16x32_bf16 a[4:7],a[140:143],v[92:95],a[4:7]
buffer_store_dwordx4 v[164:167],v6,s[32:35],s85 offen offset:1024 sc1
v_mfma_f32_16x16x32_bf16 a[4:7],a[136:139],v[88:91],a[4:7]
v_accvgpr_read_b32 v178,a248
v_accvgpr_read_b32 v179,a249
v_mfma_f32_16x16x32_bf16 a[4:7],a[132:135],v[84:87],a[4:7]
v_accvgpr_read_b32 v14,a250
v_accvgpr_read_b32 v15,a251
v_cvt_pk_bf16_f32 v178,v178,v179
v_cvt_pk_bf16_f32 v179,v14,v15
v_mfma_f32_16x16x32_bf16 a[4:7],a[128:131],v[80:83],a[4:7]
ds_read_b64_tr_b16 a[116:117],v44 offset:10432
ds_read_b64_tr_b16 a[118:119],v44 offset:15552
v_mfma_f32_16x16x32_bf16 a[8:11],a[128:131],v[96:99],a[8:11]
buffer_store_dwordx4 v[176:179],v6,s[32:35],s86 offen offset:1024 sc1
v_mfma_f32_16x16x32_bf16 a[8:11],a[132:135],v[100:103],a[8:11]
ds_read_b64_tr_b16 a[120:121],v44 offset:20672
ds_read_b64_tr_b16 a[122:123],v44 offset:25792
v_mfma_f32_16x16x32_bf16 a[8:11],a[136:139],v[104:107],a[8:11]
ds_read_b64_tr_b16 a[124:125],v44 offset:30912
v_mfma_f32_16x16x32_bf16 a[8:11],a[140:143],v[108:111],a[8:11]
ds_read_b64_tr_b16 a[126:127],v44 offset:36032
v_mfma_f32_16x16x32_bf16 a[0:3],a[144:147],v[136:139],a[0:3]
s_waitcnt lgkmcnt(6)
ds_read_b64_tr_b16 a[128:129],v43 offset:224
v_mfma_f32_16x16x32_bf16 a[0:3],a[148:151],v[140:143],a[0:3]
ds_read_b64_tr_b16 a[130:131],v43 offset:5344
ds_read_b64_tr_b16 a[132:133],v43 offset:10464
v_mfma_f32_16x16x32_bf16 a[0:3],a[152:155],v[152:155],a[0:3]
ds_read_b64_tr_b16 a[134:135],v43 offset:15584
v_mfma_f32_16x16x32_bf16 a[0:3],a[156:159],v[156:159],a[0:3]
ds_read_b64_tr_b16 a[136:137],v43 offset:20704
v_mfma_f32_16x16x32_bf16 a[4:7],a[156:159],v[188:191],a[4:7]
ds_read_b64_tr_b16 a[138:139],v43 offset:25824
v_mfma_f32_16x16x32_bf16 a[4:7],a[152:155],v[184:187],a[4:7]
ds_read_b64_tr_b16 a[140:141],v43 offset:30944
ds_read_b64_tr_b16 a[142:143],v43 offset:36064
v_mfma_f32_16x16x32_bf16 a[4:7],a[148:151],v[172:175],a[4:7]
v_mfma_f32_16x16x32_bf16 a[4:7],a[144:147],v[168:171],a[4:7]
v_mfma_f32_16x16x32_bf16 a[8:11],a[144:147],v[200:203],a[8:11]
ds_read_b64_tr_b16 a[144:145],v45 offset:224
ds_read_b64_tr_b16 a[146:147],v45 offset:5344
v_mfma_f32_16x16x32_bf16 a[8:11],a[148:151],v[204:207],a[8:11]
v_accvgpr_read_b32 v192,a0
v_accvgpr_read_b32 v193,a1
v_mfma_f32_16x16x32_bf16 a[8:11],a[152:155],v[216:219],a[8:11]
v_cvt_pk_bf16_f32 v192,v192,v193
v_accvgpr_read_b32 v194,a2
v_mfma_f32_16x16x32_bf16 a[8:11],a[156:159],v[220:223],a[8:11]
v_accvgpr_read_b32 v195,a3
v_cvt_pk_bf16_f32 v193,v194,v195
v_mfma_f32_16x16x32_bf16 a[16:19],a[160:163],v[64:67],a[16:19]
ds_read_b64_tr_b16 a[148:149],v45 offset:10464
ds_read_b64_tr_b16 a[150:151],v45 offset:15584
v_mfma_f32_16x16x32_bf16 a[16:19],a[164:167],v[68:71],a[16:19]
v_accvgpr_read_b32 v196,a4
v_accvgpr_read_b32 v197,a5
v_mfma_f32_16x16x32_bf16 a[16:19],a[168:171],v[72:75],a[16:19]
v_cvt_pk_bf16_f32 v196,v196,v197
v_accvgpr_read_b32 v198,a6
v_mfma_f32_16x16x32_bf16 a[16:19],a[172:175],v[76:79],a[16:19]
v_accvgpr_read_b32 v199,a7
v_cvt_pk_bf16_f32 v197,v198,v199
v_mfma_f32_16x16x32_bf16 a[20:23],a[172:175],v[92:95],a[20:23]
ds_read_b64_tr_b16 a[152:153],v45 offset:20704
ds_read_b64_tr_b16 a[154:155],v45 offset:25824
v_mfma_f32_16x16x32_bf16 a[20:23],a[168:171],v[88:91],a[20:23]
ds_read_b64_tr_b16 a[156:157],v45 offset:30944
v_accvgpr_read_b32 v208,a8
v_mfma_f32_16x16x32_bf16 a[20:23],a[164:167],v[84:87],a[20:23]
ds_read_b64_tr_b16 a[158:159],v45 offset:36064
v_accvgpr_read_b32 v209,a9
v_mfma_f32_16x16x32_bf16 a[20:23],a[160:163],v[80:83],a[20:23]
v_cvt_pk_bf16_f32 v208,v208,v209
v_accvgpr_read_b32 v210,a10
v_mfma_f32_16x16x32_bf16 a[24:27],a[160:163],v[96:99],a[24:27]
v_accvgpr_read_b32 v211,a11
v_cvt_pk_bf16_f32 v209,v210,v211
v_mfma_f32_16x16x32_bf16 a[24:27],a[164:167],v[100:103],a[24:27]
ds_read_b64_tr_b16 a[160:161],v42 offset:256
ds_read_b64_tr_b16 a[162:163],v42 offset:5376
v_mfma_f32_16x16x32_bf16 a[24:27],a[168:171],v[104:107],a[24:27]
ds_read_b64_tr_b16 a[164:165],v42 offset:10496
v_mfma_f32_16x16x32_bf16 a[24:27],a[172:175],v[108:111],a[24:27]
ds_read_b64_tr_b16 a[166:167],v42 offset:15616
v_mfma_f32_16x16x32_bf16 a[16:19],a[176:179],v[136:139],a[16:19]
ds_read_b64_tr_b16 a[168:169],v42 offset:20736
v_mfma_f32_16x16x32_bf16 a[16:19],a[180:183],v[140:143],a[16:19]
ds_read_b64_tr_b16 a[170:171],v42 offset:25856
ds_read_b64_tr_b16 a[172:173],v42 offset:30976
v_mfma_f32_16x16x32_bf16 a[16:19],a[184:187],v[152:155],a[16:19]
ds_read_b64_tr_b16 a[174:175],v42 offset:36096
v_mfma_f32_16x16x32_bf16 a[16:19],a[188:191],v[156:159],a[16:19]
v_mfma_f32_16x16x32_bf16 a[20:23],a[188:191],v[188:191],a[20:23]
v_mfma_f32_16x16x32_bf16 a[20:23],a[184:187],v[184:187],a[20:23]
v_mfma_f32_16x16x32_bf16 a[20:23],a[180:183],v[172:175],a[20:23]
v_mfma_f32_16x16x32_bf16 a[20:23],a[176:179],v[168:171],a[20:23]
v_mfma_f32_16x16x32_bf16 a[24:27],a[176:179],v[200:203],a[24:27]
ds_read_b64_tr_b16 a[176:177],v44 offset:256
ds_read_b64_tr_b16 a[178:179],v44 offset:5376
v_mfma_f32_16x16x32_bf16 a[24:27],a[180:183],v[204:207],a[24:27]
s_lshl_b32 s95,s42,2
s_add_u32 s95,s95,s40
s_lshl_b32 s95,s95,4
v_mfma_f32_16x16x32_bf16 a[24:27],a[184:187],v[216:219],a[24:27]
s_addk_i32 s95,5120
v_bfe_u32 v19,s55,13,1
v_mov_b32 v18,s68
v_or_b32 v19,s69,v19
v_mfma_f32_16x16x32_bf16 a[24:27],a[188:191],v[220:223],a[24:27]
s_waitcnt vmcnt(0)
s_mov_b64 exec,1
buffer_store_dwordx2 v[18:19],v6,s[64:67],s95 offen sc1
s_mov_b64 exec,-1
v_mfma_f32_16x16x32_bf16 a[32:35],a[96:99],v[64:67],a[32:35]
v_accvgpr_read_b32 v194,a16
v_accvgpr_read_b32 v195,a17
v_mfma_f32_16x16x32_bf16 a[32:35],a[100:103],v[68:71],a[32:35]
v_accvgpr_read_b32 v16,a18
v_accvgpr_read_b32 v17,a19
v_mfma_f32_16x16x32_bf16 a[32:35],a[104:107],v[72:75],a[32:35]
v_cvt_pk_bf16_f32 v194,v194,v195
v_cvt_pk_bf16_f32 v195,v16,v17
v_mfma_f32_16x16x32_bf16 a[32:35],a[108:111],v[76:79],a[32:35]
buffer_store_dwordx4 v[192:195],v6,s[32:35],s84 offen offset:2048 sc1
v_mfma_f32_16x16x32_bf16 a[36:39],a[108:111],v[92:95],a[36:39]
v_accvgpr_read_b32 v198,a20
v_accvgpr_read_b32 v199,a21
v_mfma_f32_16x16x32_bf16 a[36:39],a[104:107],v[88:91],a[36:39]
v_accvgpr_read_b32 v12,a22
v_accvgpr_read_b32 v13,a23
v_mfma_f32_16x16x32_bf16 a[36:39],a[100:103],v[84:87],a[36:39]
v_cvt_pk_bf16_f32 v198,v198,v199
v_cvt_pk_bf16_f32 v199,v12,v13
v_mfma_f32_16x16x32_bf16 a[36:39],a[96:99],v[80:83],a[36:39]
buffer_store_dwordx4 v[196:199],v6,s[32:35],s85 offen offset:2048 sc1
v_mfma_f32_16x16x32_bf16 a[40:43],a[96:99],v[96:99],a[40:43]
v_accvgpr_read_b32 v210,a24
v_accvgpr_read_b32 v211,a25
v_mfma_f32_16x16x32_bf16 a[40:43],a[100:103],v[100:103],a[40:43]
v_accvgpr_read_b32 v14,a26
v_accvgpr_read_b32 v15,a27
v_mfma_f32_16x16x32_bf16 a[40:43],a[104:107],v[104:107],a[40:43]
v_cvt_pk_bf16_f32 v210,v210,v211
v_cvt_pk_bf16_f32 v211,v14,v15
v_mfma_f32_16x16x32_bf16 a[40:43],a[108:111],v[108:111],a[40:43]
buffer_store_dwordx4 v[208:211],v6,s[32:35],s86 offen offset:2048 sc1
v_mfma_f32_16x16x32_bf16 a[32:35],a[112:115],v[136:139],a[32:35]
s_waitcnt lgkmcnt(0)
ds_read_b64_tr_b16 a[180:181],v44 offset:10496
ds_read_b64_tr_b16 a[182:183],v44 offset:15616
v_mfma_f32_16x16x32_bf16 a[32:35],a[116:119],v[140:143],a[32:35]
ds_read_b64_tr_b16 a[184:185],v44 offset:20736
v_mfma_f32_16x16x32_bf16 a[32:35],a[120:123],v[152:155],a[32:35]
ds_read_b64_tr_b16 a[186:187],v44 offset:25856
v_mfma_f32_16x16x32_bf16 a[32:35],a[124:127],v[156:159],a[32:35]
ds_read_b64_tr_b16 a[188:189],v44 offset:30976
v_mfma_f32_16x16x32_bf16 a[36:39],a[124:127],v[188:191],a[36:39]
ds_read_b64_tr_b16 a[190:191],v44 offset:36096
ds_read_b64_tr_b16 a[96:97],v43 offset:288
v_mfma_f32_16x16x32_bf16 a[36:39],a[120:123],v[184:187],a[36:39]
ds_read_b64_tr_b16 a[98:99],v43 offset:5408
v_mfma_f32_16x16x32_bf16 a[36:39],a[116:119],v[172:175],a[36:39]
ds_read_b64_tr_b16 a[100:101],v43 offset:10528
v_mfma_f32_16x16x32_bf16 a[36:39],a[112:115],v[168:171],a[36:39]
ds_read_b64_tr_b16 a[102:103],v43 offset:15648
v_mfma_f32_16x16x32_bf16 a[40:43],a[112:115],v[200:203],a[40:43]
ds_read_b64_tr_b16 a[104:105],v43 offset:20768
ds_read_b64_tr_b16 a[106:107],v43 offset:25888
v_mfma_f32_16x16x32_bf16 a[40:43],a[116:119],v[204:207],a[40:43]
v_accvgpr_read_b32 v224,a32
v_accvgpr_read_b32 v225,a33
v_mfma_f32_16x16x32_bf16 a[40:43],a[120:123],v[216:219],a[40:43]
s_lshl_b32 s94,s71,2
v_cvt_pk_bf16_f32 v224,v224,v225
v_accvgpr_read_b32 v226,a34
v_mfma_f32_16x16x32_bf16 a[40:43],a[124:127],v[220:223],a[40:43]
s_add_u32 s94,s94,s40
v_accvgpr_read_b32 v227,a35
v_cvt_pk_bf16_f32 v225,v226,v227
v_mfma_f32_16x16x32_bf16 a[48:51],a[128:131],v[64:67],a[48:51]
s_lshl_b32 s94,s94,4
v_bfe_u32 v24,v6,4,2
v_min_u32 v24,2,v24
v_mfma_f32_16x16x32_bf16 a[48:51],a[132:135],v[68:71],a[48:51]
s_addk_i32 s94,5120
v_accvgpr_read_b32 v228,a36
v_accvgpr_read_b32 v229,a37
v_mfma_f32_16x16x32_bf16 a[48:51],a[136:139],v[72:75],a[48:51]
v_cvt_pk_bf16_f32 v228,v228,v229
v_accvgpr_read_b32 v230,a38
v_mfma_f32_16x16x32_bf16 a[48:51],a[140:143],v[76:79],a[48:51]
v_accvgpr_read_b32 v231,a39
v_cvt_pk_bf16_f32 v229,v230,v231
v_mfma_f32_16x16x32_bf16 a[52:55],a[140:143],v[92:95],a[52:55]
v_add_u32 v24,s70,v24
v_add_u32 v24,1,v24
v_mfma_f32_16x16x32_bf16 a[52:55],a[136:139],v[88:91],a[52:55]
v_accvgpr_read_b32 v240,a40
v_accvgpr_read_b32 v241,a41
)A"
R"A(v_mfma_f32_16x16x32_bf16 a[52:55],a[132:135],v[84:87],a[52:55]
v_cvt_pk_bf16_f32 v240,v240,v241
v_accvgpr_read_b32 v242,a42
v_mfma_f32_16x16x32_bf16 a[52:55],a[128:131],v[80:83],a[52:55]
v_accvgpr_read_b32 v243,a43
v_cvt_pk_bf16_f32 v241,v242,v243
v_mfma_f32_16x16x32_bf16 a[56:59],a[128:131],v[96:99],a[56:59]
v_and_b32 v24,3,v24
v_lshlrev_b32 v24,8,v24
v_mfma_f32_16x16x32_bf16 a[56:59],a[132:135],v[100:103],a[56:59]
ds_read_b64_tr_b16 a[108:109],v43 offset:31008
ds_read_b64_tr_b16 a[110:111],v43 offset:36128
v_mfma_f32_16x16x32_bf16 a[56:59],a[136:139],v[104:107],a[56:59]
ds_read_b64_tr_b16 a[112:113],v45 offset:288
v_mfma_f32_16x16x32_bf16 a[56:59],a[140:143],v[108:111],a[56:59]
ds_read_b64_tr_b16 a[114:115],v45 offset:5408
v_mfma_f32_16x16x32_bf16 a[48:51],a[144:147],v[136:139],a[48:51]
ds_read_b64_tr_b16 a[116:117],v45 offset:10528
v_mfma_f32_16x16x32_bf16 a[48:51],a[148:151],v[140:143],a[48:51]
ds_read_b64_tr_b16 a[118:119],v45 offset:15648
ds_read_b64_tr_b16 a[120:121],v45 offset:20768
v_mfma_f32_16x16x32_bf16 a[48:51],a[152:155],v[152:155],a[48:51]
ds_read_b64_tr_b16 a[122:123],v45 offset:25888
v_mfma_f32_16x16x32_bf16 a[48:51],a[156:159],v[156:159],a[48:51]
ds_read_b64_tr_b16 a[124:125],v45 offset:31008
v_mfma_f32_16x16x32_bf16 a[52:55],a[156:159],v[188:191],a[52:55]
ds_read_b64_tr_b16 a[126:127],v45 offset:36128
v_mfma_f32_16x16x32_bf16 a[52:55],a[152:155],v[184:187],a[52:55]
ds_read_b64_tr_b16 a[128:129],v1
ds_read_b64_tr_b16 a[130:131],v1 offset:5120
v_mfma_f32_16x16x32_bf16 a[52:55],a[148:151],v[172:175],a[52:55]
ds_read_b64_tr_b16 a[132:133],v1 offset:10240
v_mfma_f32_16x16x32_bf16 a[52:55],a[144:147],v[168:171],a[52:55]
ds_read_b64_tr_b16 a[134:135],v1 offset:15360
v_mfma_f32_16x16x32_bf16 a[56:59],a[144:147],v[200:203],a[56:59]
ds_read_b64_tr_b16 a[136:137],v1 offset:20480
v_mfma_f32_16x16x32_bf16 a[56:59],a[148:151],v[204:207],a[56:59]
v_accvgpr_read_b32 v226,a48
v_accvgpr_read_b32 v227,a49
v_mfma_f32_16x16x32_bf16 a[56:59],a[152:155],v[216:219],a[56:59]
v_accvgpr_read_b32 v16,a50
v_accvgpr_read_b32 v17,a51
v_mfma_f32_16x16x32_bf16 a[56:59],a[156:159],v[220:223],a[56:59]
v_cvt_pk_bf16_f32 v226,v226,v227
v_cvt_pk_bf16_f32 v227,v16,v17
v_mfma_f32_16x16x32_bf16 a[64:67],a[160:163],v[64:67],a[64:67]
buffer_store_dwordx4 v[224:227],v6,s[32:35],s84 offen offset:3072 sc1
v_mfma_f32_16x16x32_bf16 a[64:67],a[164:167],v[68:71],a[64:67]
v_accvgpr_read_b32 v230,a52
v_accvgpr_read_b32 v231,a53
v_mfma_f32_16x16x32_bf16 a[64:67],a[168:171],v[72:75],a[64:67]
v_accvgpr_read_b32 v12,a54
v_accvgpr_read_b32 v13,a55
v_mfma_f32_16x16x32_bf16 a[64:67],a[172:175],v[76:79],a[64:67]
v_cvt_pk_bf16_f32 v230,v230,v231
v_cvt_pk_bf16_f32 v231,v12,v13
v_mfma_f32_16x16x32_bf16 a[68:71],a[172:175],v[92:95],a[68:71]
buffer_store_dwordx4 v[228:231],v6,s[32:35],s85 offen offset:3072 sc1
v_mfma_f32_16x16x32_bf16 a[68:71],a[168:171],v[88:91],a[68:71]
v_accvgpr_read_b32 v242,a56
v_accvgpr_read_b32 v243,a57
v_mfma_f32_16x16x32_bf16 a[68:71],a[164:167],v[84:87],a[68:71]
v_accvgpr_read_b32 v14,a58
v_accvgpr_read_b32 v15,a59
v_mfma_f32_16x16x32_bf16 a[68:71],a[160:163],v[80:83],a[68:71]
v_cvt_pk_bf16_f32 v242,v242,v243
v_cvt_pk_bf16_f32 v243,v14,v15
v_mfma_f32_16x16x32_bf16 a[72:75],a[160:163],v[96:99],a[72:75]
buffer_store_dwordx4 v[240:243],v6,s[32:35],s86 offen offset:3072 sc1
v_mfma_f32_16x16x32_bf16 a[72:75],a[164:167],v[100:103],a[72:75]
ds_read_b64_tr_b16 a[138:139],v1 offset:25600
ds_read_b64_tr_b16 a[140:141],v1 offset:30720
v_mfma_f32_16x16x32_bf16 a[72:75],a[168:171],v[104:107],a[72:75]
ds_read_b64_tr_b16 a[142:143],v1 offset:35840
v_mfma_f32_16x16x32_bf16 a[72:75],a[172:175],v[108:111],a[72:75]
ds_read_b64_tr_b16 a[144:145],v42
v_mfma_f32_16x16x32_bf16 a[64:67],a[176:179],v[136:139],a[64:67]
s_waitcnt lgkmcnt(4)
ds_read_b64_tr_b16 a[146:147],v42 offset:5120
v_mfma_f32_16x16x32_bf16 a[64:67],a[180:183],v[140:143],a[64:67]
ds_read_b64_tr_b16 a[148:149],v42 offset:10240
ds_read_b64_tr_b16 a[150:151],v42 offset:15360
v_mfma_f32_16x16x32_bf16 a[64:67],a[184:187],v[152:155],a[64:67]
buffer_load_dwordx2 v[4:5],v24,s[64:67],s94 offen sc1
v_mfma_f32_16x16x32_bf16 a[64:67],a[188:191],v[156:159],a[64:67]
ds_read_b64_tr_b16 a[152:153],v42 offset:20480
ds_read_b64_tr_b16 a[154:155],v42 offset:25600
v_mfma_f32_16x16x32_bf16 a[68:71],a[188:191],v[188:191],a[68:71]
ds_read_b64_tr_b16 a[156:157],v42 offset:30720
v_mfma_f32_16x16x32_bf16 a[68:71],a[184:187],v[184:187],a[68:71]
ds_read_b64_tr_b16 a[158:159],v42 offset:35840
v_mfma_f32_16x16x32_bf16 a[68:71],a[180:183],v[172:175],a[68:71]
ds_read_b64_tr_b16 a[160:161],v44
v_mfma_f32_16x16x32_bf16 a[68:71],a[176:179],v[168:171],a[68:71]
ds_read_b64_tr_b16 a[162:163],v44 offset:5120
ds_read_b64_tr_b16 a[164:165],v44 offset:10240
v_mfma_f32_16x16x32_bf16 a[72:75],a[176:179],v[200:203],a[72:75]
ds_read_b64_tr_b16 a[166:167],v44 offset:15360
v_mfma_f32_16x16x32_bf16 a[72:75],a[180:183],v[204:207],a[72:75]
v_accvgpr_read_b32 v128,a64
v_accvgpr_read_b32 v129,a65
v_mfma_f32_16x16x32_bf16 a[72:75],a[184:187],v[216:219],a[72:75]
v_cvt_pk_bf16_f32 v128,v128,v129
v_accvgpr_read_b32 v130,a66
v_mfma_f32_16x16x32_bf16 a[72:75],a[188:191],v[220:223],a[72:75]
v_accvgpr_read_b32 v131,a67
v_cvt_pk_bf16_f32 v129,v130,v131
v_mfma_f32_16x16x32_bf16 a[80:83],a[96:99],v[64:67],a[80:83]
ds_read_b64_tr_b16 a[168:169],v44 offset:20480
ds_read_b64_tr_b16 a[170:171],v44 offset:25600
v_mfma_f32_16x16x32_bf16 a[80:83],a[100:103],v[68:71],a[80:83]
ds_read_b64_tr_b16 a[172:173],v44 offset:30720
v_accvgpr_read_b32 v132,a68
v_mfma_f32_16x16x32_bf16 a[80:83],a[104:107],v[72:75],a[80:83]
ds_read_b64_tr_b16 a[174:175],v44 offset:35840
v_accvgpr_read_b32 v133,a69
v_mfma_f32_16x16x32_bf16 a[80:83],a[108:111],v[76:79],a[80:83]
ds_read_b64_tr_b16 a[176:177],v2 offset:32
v_cvt_pk_bf16_f32 v132,v132,v133
v_mfma_f32_16x16x32_bf16 a[84:87],a[108:111],v[92:95],a[84:87]
ds_read_b64_tr_b16 a[178:179],v2 offset:5152
v_accvgpr_read_b32 v134,a70
v_mfma_f32_16x16x32_bf16 a[84:87],a[104:107],v[88:91],a[84:87]
v_accvgpr_read_b32 v144,a72
v_accvgpr_read_b32 v145,a73
v_mfma_f32_16x16x32_bf16 a[84:87],a[100:103],v[84:87],a[84:87]
v_accvgpr_read_b32 v135,a71
v_cvt_pk_bf16_f32 v144,v144,v145
v_mfma_f32_16x16x32_bf16 a[84:87],a[96:99],v[80:83],a[84:87]
v_accvgpr_read_b32 v146,a74
v_accvgpr_read_b32 v147,a75
v_mfma_f32_16x16x32_bf16 a[88:91],a[96:99],v[96:99],a[88:91]
ds_read_b64_tr_b16 a[180:181],v2 offset:10272
ds_read_b64_tr_b16 a[182:183],v2 offset:15392
v_mfma_f32_16x16x32_bf16 a[88:91],a[100:103],v[100:103],a[88:91]
v_cvt_pk_bf16_f32 v133,v134,v135
v_cvt_pk_bf16_f32 v145,v146,v147
v_mfma_f32_16x16x32_bf16 a[88:91],a[104:107],v[104:107],a[88:91]
ds_read_b64_tr_b16 a[184:185],v2 offset:20512
ds_read_b64_tr_b16 a[186:187],v2 offset:25632
v_mfma_f32_16x16x32_bf16 a[88:91],a[108:111],v[108:111],a[88:91]
ds_read_b64_tr_b16 a[188:189],v2 offset:30752
v_mfma_f32_16x16x32_bf16 a[80:83],a[112:115],v[136:139],a[80:83]
ds_read_b64_tr_b16 a[190:191],v2 offset:35872
v_mfma_f32_16x16x32_bf16 a[80:83],a[116:119],v[140:143],a[80:83]
ds_read_b64_tr_b16 a[96:97],v43 offset:32
v_mfma_f32_16x16x32_bf16 a[80:83],a[120:123],v[152:155],a[80:83]
ds_read_b64_tr_b16 a[98:99],v43 offset:5152
ds_read_b64_tr_b16 a[100:101],v43 offset:10272
v_mfma_f32_16x16x32_bf16 a[80:83],a[124:127],v[156:159],a[80:83]
ds_read_b64_tr_b16 a[102:103],v43 offset:15392
v_mfma_f32_16x16x32_bf16 a[84:87],a[124:127],v[188:191],a[84:87]
ds_read_b64_tr_b16 a[104:105],v43 offset:20512
v_mfma_f32_16x16x32_bf16 a[84:87],a[120:123],v[184:187],a[84:87]
ds_read_b64_tr_b16 a[106:107],v43 offset:25632
v_mfma_f32_16x16x32_bf16 a[84:87],a[116:119],v[172:175],a[84:87]
ds_read_b64_tr_b16 a[108:109],v43 offset:30752
ds_read_b64_tr_b16 a[110:111],v43 offset:35872
v_mfma_f32_16x16x32_bf16 a[84:87],a[112:115],v[168:171],a[84:87]
v_mfma_f32_16x16x32_bf16 a[88:91],a[112:115],v[200:203],a[88:91]
ds_read_b64_tr_b16 a[112:113],v45 offset:32
ds_read_b64_tr_b16 a[114:115],v45 offset:5152
v_mfma_f32_16x16x32_bf16 a[88:91],a[116:119],v[204:207],a[88:91]
v_accvgpr_read_b32 v130,a80
v_accvgpr_read_b32 v131,a81
v_mfma_f32_16x16x32_bf16 a[88:91],a[120:123],v[216:219],a[88:91]
v_accvgpr_read_b32 v16,a82
v_accvgpr_read_b32 v17,a83
v_mfma_f32_16x16x32_bf16 a[88:91],a[124:127],v[220:223],a[88:91]
v_cvt_pk_bf16_f32 v130,v130,v131
v_cvt_pk_bf16_f32 v131,v16,v17
v_mfma_f32_16x16x32_bf16 a[204:207],a[128:131],v[148:151],a[204:207]
buffer_store_dwordx4 v[128:131],v25,s[32:35],s84 offen sc1
v_mfma_f32_16x16x32_bf16 a[204:207],a[132:135],v[180:183],a[204:207]
s_waitcnt lgkmcnt(4)
v_accvgpr_read_b32 v134,a84
v_accvgpr_read_b32 v135,a85
v_mfma_f32_16x16x32_bf16 a[204:207],a[136:139],v[212:215],a[204:207]
s_lshl_b32 s95,s42,2
s_waitcnt vmcnt(1)
v_cmp_ne_u32 s[90:91],s68,v4
v_and_b32 v4,-2,v5
v_cmp_ne_u32 s[92:93],s69,v4
s_nop 0
s_or_b64 s[90:91],s[90:91],s[92:93]
s_cbranch_scc1 L_fr10_%=
L_fo10_%=:
v_lshlrev_b32 v19,13,v5
v_and_b32 v19,8192,v19
s_nop 0
v_readlane_b32 s90,v19,2
v_readlane_b32 s91,v19,1
v_readlane_b32 s92,v19,0
s_nop 1
s_or_b32 s76,s76,s90
s_or_b32 s77,s77,s91
s_or_b32 s78,s78,s92
v_mfma_f32_16x16x32_bf16 a[204:207],a[140:143],v[244:247],a[204:207]
s_add_u32 s95,s95,s40
s_lshl_b32 s95,s95,4
v_bfe_u32 v19,s55,13,1
v_mov_b32 v18,s68
v_or_b32 v19,s69,v19
v_mfma_f32_16x16x32_bf16 a[204:207],a[144:147],v[112:115],a[204:207]
s_lshl_b32 s94,s71,2
s_mov_b64 exec,1
buffer_store_dwordx2 v[18:19],v6,s[64:67],s95 offen offset:2048 sc1
s_mov_b64 exec,-1
v_mfma_f32_16x16x32_bf16 a[204:207],a[148:151],v[116:119],a[204:207]
s_add_u32 s94,s94,s40
v_accvgpr_read_b32 v146,a88
v_accvgpr_read_b32 v147,a89
v_mfma_f32_16x16x32_bf16 a[204:207],a[152:155],v[120:123],a[204:207]
v_accvgpr_read_b32 v12,a86
v_accvgpr_read_b32 v13,a87
v_cvt_pk_bf16_f32 v134,v134,v135
v_accvgpr_read_b32 v14,a90
v_accvgpr_read_b32 v15,a91
v_cvt_pk_bf16_f32 v146,v146,v147
v_cvt_pk_bf16_f32 v135,v12,v13
v_cvt_pk_bf16_f32 v147,v14,v15
v_mfma_f32_16x16x32_bf16 a[204:207],a[156:159],v[124:127],a[204:207]
s_lshl_b32 s94,s94,4
ds_read_b64_tr_b16 a[116:117],v45 offset:10272
ds_read_b64_tr_b16 a[118:119],v45 offset:15392
v_mfma_f32_16x16x32_bf16 a[204:207],a[160:163],v[232:235],a[204:207]
buffer_store_dwordx4 v[132:135],v25,s[32:35],s85 offen sc1
v_mfma_f32_16x16x32_bf16 a[204:207],a[164:167],v[236:239],a[204:207]
ds_read_b64_tr_b16 a[120:121],v45 offset:20512
ds_read_b64_tr_b16 a[122:123],v45 offset:25632
v_mfma_f32_16x16x32_bf16 a[204:207],a[168:171],v[248:251],a[204:207]
ds_read_b64_tr_b16 a[124:125],v45 offset:30752
ds_read_b64_tr_b16 a[126:127],v45 offset:35872
v_mfma_f32_16x16x32_bf16 a[204:207],a[172:175],v[252:255],a[204:207]
ds_read_b64_tr_b16 a[128:129],v1 offset:64
ds_read_b64_tr_b16 a[130:131],v1 offset:5184
v_mfma_f32_16x16x32_bf16 a[220:223],a[176:179],v[148:151],a[220:223]
buffer_store_dwordx4 v[144:147],v25,s[32:35],s86 offen sc1
v_mfma_f32_16x16x32_bf16 a[220:223],a[180:183],v[180:183],a[220:223]
ds_read_b64_tr_b16 a[132:133],v1 offset:10304
ds_read_b64_tr_b16 a[134:135],v1 offset:15424
v_mfma_f32_16x16x32_bf16 a[220:223],a[184:187],v[212:215],a[220:223]
ds_read_b64_tr_b16 a[136:137],v1 offset:20544
ds_read_b64_tr_b16 a[138:139],v1 offset:25664
v_mfma_f32_16x16x32_bf16 a[220:223],a[188:191],v[244:247],a[220:223]
ds_read_b64_tr_b16 a[140:141],v1 offset:30784
ds_read_b64_tr_b16 a[142:143],v1 offset:35904
v_mfma_f32_16x16x32_bf16 a[220:223],a[96:99],v[112:115],a[220:223]
buffer_load_dwordx4 a[192:195],v6,s[32:35],s76 offen sc1
v_mfma_f32_16x16x32_bf16 a[220:223],a[100:103],v[116:119],a[220:223]
ds_read_b64_tr_b16 a[144:145],v42 offset:64
ds_read_b64_tr_b16 a[146:147],v42 offset:5184
v_mfma_f32_16x16x32_bf16 a[220:223],a[104:107],v[120:123],a[220:223]
s_waitcnt lgkmcnt(10)
ds_read_b64_tr_b16 a[148:149],v42 offset:10304
ds_read_b64_tr_b16 a[150:151],v42 offset:15424
v_mfma_f32_16x16x32_bf16 a[220:223],a[108:111],v[124:127],a[220:223]
ds_read_b64_tr_b16 a[152:153],v42 offset:20544
ds_read_b64_tr_b16 a[154:155],v42 offset:25664
v_mfma_f32_16x16x32_bf16 a[220:223],a[112:115],v[232:235],a[220:223]
buffer_load_dwordx4 a[196:199],v6,s[32:35],s77 offen sc1
)A"
R"A(v_mfma_f32_16x16x32_bf16 a[220:223],a[116:119],v[236:239],a[220:223]
ds_read_b64_tr_b16 a[156:157],v42 offset:30784
ds_read_b64_tr_b16 a[158:159],v42 offset:35904
v_mfma_f32_16x16x32_bf16 a[220:223],a[120:123],v[248:251],a[220:223]
ds_read_b64_tr_b16 a[160:161],v44 offset:64
ds_read_b64_tr_b16 a[162:163],v44 offset:5184
v_mfma_f32_16x16x32_bf16 a[220:223],a[124:127],v[252:255],a[220:223]
s_waitcnt lgkmcnt(10)
ds_read_b64_tr_b16 a[164:165],v44 offset:10304
ds_read_b64_tr_b16 a[166:167],v44 offset:15424
v_mfma_f32_16x16x32_bf16 a[236:239],a[128:131],v[148:151],a[236:239]
buffer_load_dwordx4 a[200:203],v6,s[32:35],s78 offen sc1
v_mfma_f32_16x16x32_bf16 a[236:239],a[132:135],v[180:183],a[236:239]
ds_read_b64_tr_b16 a[168:169],v44 offset:20544
ds_read_b64_tr_b16 a[170:171],v44 offset:25664
v_mfma_f32_16x16x32_bf16 a[236:239],a[136:139],v[212:215],a[236:239]
ds_read_b64_tr_b16 a[172:173],v44 offset:30784
ds_read_b64_tr_b16 a[174:175],v44 offset:35904
v_mfma_f32_16x16x32_bf16 a[236:239],a[140:143],v[244:247],a[236:239]
s_waitcnt lgkmcnt(10)
ds_read_b64_tr_b16 a[176:177],v2 offset:96
ds_read_b64_tr_b16 a[178:179],v2 offset:5216
v_mfma_f32_16x16x32_bf16 a[236:239],a[144:147],v[112:115],a[236:239]
buffer_load_dwordx4 a[224:227],v6,s[32:35],s76 offen offset:1024 sc1
ds_read_b64_tr_b16 a[180:181],v2 offset:10336
ds_read_b64_tr_b16 a[182:183],v2 offset:15456
v_mfma_f32_16x16x32_bf16 a[236:239],a[148:151],v[116:119],a[236:239]
ds_read_b64_tr_b16 a[184:185],v2 offset:20576
ds_read_b64_tr_b16 a[186:187],v2 offset:25696
v_mfma_f32_16x16x32_bf16 a[236:239],a[152:155],v[120:123],a[236:239]
s_waitcnt lgkmcnt(12)
ds_read_b64_tr_b16 a[188:189],v2 offset:30816
ds_read_b64_tr_b16 a[190:191],v2 offset:35936
v_mfma_f32_16x16x32_bf16 a[236:239],a[156:159],v[124:127],a[236:239]
ds_read_b64_tr_b16 a[96:97],v43 offset:96
ds_read_b64_tr_b16 a[98:99],v43 offset:5216
v_mfma_f32_16x16x32_bf16 a[236:239],a[160:163],v[232:235],a[236:239]
s_waitcnt lgkmcnt(14)
buffer_load_dwordx4 a[228:231],v6,s[32:35],s77 offen offset:1024 sc1
ds_read_b64_tr_b16 a[100:101],v43 offset:10336
ds_read_b64_tr_b16 a[102:103],v43 offset:15456
v_mfma_f32_16x16x32_bf16 a[236:239],a[164:167],v[236:239],a[236:239]
s_waitcnt lgkmcnt(14)
ds_read_b64_tr_b16 a[104:105],v43 offset:20576
ds_read_b64_tr_b16 a[106:107],v43 offset:25696
v_mfma_f32_16x16x32_bf16 a[236:239],a[168:171],v[248:251],a[236:239]
s_waitcnt lgkmcnt(14)
ds_read_b64_tr_b16 a[108:109],v43 offset:30816
ds_read_b64_tr_b16 a[110:111],v43 offset:35936
v_mfma_f32_16x16x32_bf16 a[236:239],a[172:175],v[252:255],a[236:239]
s_waitcnt lgkmcnt(14)
ds_read_b64_tr_b16 a[112:113],v45 offset:96
ds_read_b64_tr_b16 a[114:115],v45 offset:5216
v_mfma_f32_16x16x32_bf16 a[252:255],a[176:179],v[148:151],a[252:255]
s_waitcnt lgkmcnt(14)
buffer_load_dwordx4 a[232:235],v6,s[32:35],s78 offen offset:1024 sc1
ds_read_b64_tr_b16 a[116:117],v45 offset:10336
ds_read_b64_tr_b16 a[118:119],v45 offset:15456
v_mfma_f32_16x16x32_bf16 a[252:255],a[180:183],v[180:183],a[252:255]
s_waitcnt lgkmcnt(14)
ds_read_b64_tr_b16 a[120:121],v45 offset:20576
ds_read_b64_tr_b16 a[122:123],v45 offset:25696
v_mfma_f32_16x16x32_bf16 a[252:255],a[184:187],v[212:215],a[252:255]
s_waitcnt lgkmcnt(14)
ds_read_b64_tr_b16 a[124:125],v45 offset:30816
ds_read_b64_tr_b16 a[126:127],v45 offset:35936
v_mfma_f32_16x16x32_bf16 a[252:255],a[188:191],v[244:247],a[252:255]
s_waitcnt lgkmcnt(14)
ds_read_b64_tr_b16 a[128:129],v1 offset:128
ds_read_b64_tr_b16 a[130:131],v1 offset:5248
v_bfe_u32 v24,v6,4,2
v_min_u32 v24,2,v24
v_mfma_f32_16x16x32_bf16 a[252:255],a[96:99],v[112:115],a[252:255]
s_waitcnt lgkmcnt(14)
ds_read_b64_tr_b16 a[132:133],v1 offset:10368
ds_read_b64_tr_b16 a[134:135],v1 offset:15488
v_add_u32 v24,s70,v24
v_add_u32 v24,1,v24
v_mfma_f32_16x16x32_bf16 a[252:255],a[100:103],v[116:119],a[252:255]
s_waitcnt lgkmcnt(14)
ds_read_b64_tr_b16 a[136:137],v1 offset:20608
ds_read_b64_tr_b16 a[138:139],v1 offset:25728
v_and_b32 v24,3,v24
v_lshlrev_b32 v24,8,v24
v_mfma_f32_16x16x32_bf16 a[252:255],a[104:107],v[120:123],a[252:255]
s_waitcnt lgkmcnt(14)
buffer_load_dwordx2 v[4:5],v24,s[64:67],s94 offen offset:2048 sc1
ds_read_b64_tr_b16 a[140:141],v1 offset:30848
ds_read_b64_tr_b16 a[142:143],v1 offset:35968
v_mfma_f32_16x16x32_bf16 a[252:255],a[108:111],v[124:127],a[252:255]
s_waitcnt lgkmcnt(14)
ds_read_b64_tr_b16 a[144:145],v42 offset:128
ds_read_b64_tr_b16 a[146:147],v42 offset:5248
v_mfma_f32_16x16x32_bf16 a[252:255],a[112:115],v[232:235],a[252:255]
s_waitcnt lgkmcnt(14)
ds_read_b64_tr_b16 a[148:149],v42 offset:10368
ds_read_b64_tr_b16 a[150:151],v42 offset:15488
v_mfma_f32_16x16x32_bf16 a[252:255],a[116:119],v[236:239],a[252:255]
s_lshl_b32 s95,s42,2
ds_read_b64_tr_b16 a[152:153],v42 offset:20608
ds_read_b64_tr_b16 a[154:155],v42 offset:25728
s_waitcnt lgkmcnt(15)
v_mfma_f32_16x16x32_bf16 a[252:255],a[120:123],v[248:251],a[252:255]
s_add_u32 s95,s95,s40
s_lshl_b32 s95,s95,4
ds_read_b64_tr_b16 a[156:157],v42 offset:30848
ds_read_b64_tr_b16 a[158:159],v42 offset:35968
v_bfe_u32 v19,s55,13,1
v_mov_b32 v18,s68
v_or_b32 v19,s69,v19
s_waitcnt lgkmcnt(15)
v_mfma_f32_16x16x32_bf16 a[252:255],a[124:127],v[252:255],a[252:255]
s_waitcnt vmcnt(7)
s_mov_b64 exec,1
buffer_store_dwordx2 v[18:19],v6,s[64:67],s95 offen offset:3072 sc1
s_mov_b64 exec,-1
ds_read_b64_tr_b16 a[160:161],v44 offset:128
ds_read_b64_tr_b16 a[162:163],v44 offset:5248
s_waitcnt lgkmcnt(15)
v_mfma_f32_16x16x32_bf16 a[12:15],a[128:131],v[148:151],a[12:15]
s_waitcnt lgkmcnt(14)
ds_read_b64_tr_b16 a[164:165],v44 offset:10368
ds_read_b64_tr_b16 a[166:167],v44 offset:15488
v_mfma_f32_16x16x32_bf16 a[12:15],a[132:135],v[180:183],a[12:15]
s_waitcnt lgkmcnt(14)
ds_read_b64_tr_b16 a[168:169],v44 offset:20608
ds_read_b64_tr_b16 a[170:171],v44 offset:25728
v_mfma_f32_16x16x32_bf16 a[12:15],a[136:139],v[212:215],a[12:15]
s_waitcnt lgkmcnt(14)
ds_read_b64_tr_b16 a[172:173],v44 offset:30848
ds_read_b64_tr_b16 a[174:175],v44 offset:35968
v_mfma_f32_16x16x32_bf16 a[12:15],a[140:143],v[244:247],a[12:15]
s_waitcnt lgkmcnt(14)
ds_read_b64_tr_b16 a[176:177],v2 offset:160
ds_read_b64_tr_b16 a[178:179],v2 offset:5280
v_mfma_f32_16x16x32_bf16 a[12:15],a[144:147],v[112:115],a[12:15]
s_waitcnt lgkmcnt(14)
ds_read_b64_tr_b16 a[180:181],v2 offset:10400
ds_read_b64_tr_b16 a[182:183],v2 offset:15520
v_mfma_f32_16x16x32_bf16 a[12:15],a[148:151],v[116:119],a[12:15]
s_waitcnt lgkmcnt(14)
ds_read_b64_tr_b16 a[184:185],v2 offset:20640
ds_read_b64_tr_b16 a[186:187],v2 offset:25760
v_mfma_f32_16x16x32_bf16 a[12:15],a[152:155],v[120:123],a[12:15]
s_waitcnt lgkmcnt(14)
ds_read_b64_tr_b16 a[188:189],v2 offset:30880
ds_read_b64_tr_b16 a[190:191],v2 offset:36000
v_mfma_f32_16x16x32_bf16 a[12:15],a[156:159],v[124:127],a[12:15]
s_waitcnt lgkmcnt(14)
ds_read_b64_tr_b16 a[96:97],v43 offset:160
ds_read_b64_tr_b16 a[98:99],v43 offset:5280
v_mfma_f32_16x16x32_bf16 a[12:15],a[160:163],v[232:235],a[12:15]
s_waitcnt lgkmcnt(14)
ds_read_b64_tr_b16 a[100:101],v43 offset:10400
ds_read_b64_tr_b16 a[102:103],v43 offset:15520
v_mfma_f32_16x16x32_bf16 a[12:15],a[164:167],v[236:239],a[12:15]
s_waitcnt lgkmcnt(14)
ds_read_b64_tr_b16 a[104:105],v43 offset:20640
ds_read_b64_tr_b16 a[106:107],v43 offset:25760
v_mfma_f32_16x16x32_bf16 a[12:15],a[168:171],v[248:251],a[12:15]
s_waitcnt lgkmcnt(14)
ds_read_b64_tr_b16 a[108:109],v43 offset:30880
ds_read_b64_tr_b16 a[110:111],v43 offset:36000
v_mfma_f32_16x16x32_bf16 a[12:15],a[172:175],v[252:255],a[12:15]
s_waitcnt lgkmcnt(14)
ds_read_b64_tr_b16 a[112:113],v45 offset:160
ds_read_b64_tr_b16 a[114:115],v45 offset:5280
v_mfma_f32_16x16x32_bf16 a[28:31],a[176:179],v[148:151],a[28:31]
s_waitcnt lgkmcnt(14)
ds_read_b64_tr_b16 a[116:117],v45 offset:10400
ds_read_b64_tr_b16 a[118:119],v45 offset:15520
v_mfma_f32_16x16x32_bf16 a[28:31],a[180:183],v[180:183],a[28:31]
s_waitcnt lgkmcnt(14)
ds_read_b64_tr_b16 a[120:121],v45 offset:20640
ds_read_b64_tr_b16 a[122:123],v45 offset:25760
v_mfma_f32_16x16x32_bf16 a[28:31],a[184:187],v[212:215],a[28:31]
s_waitcnt lgkmcnt(14)
ds_read_b64_tr_b16 a[124:125],v45 offset:30880
ds_read_b64_tr_b16 a[126:127],v45 offset:36000
v_mfma_f32_16x16x32_bf16 a[28:31],a[188:191],v[244:247],a[28:31]
s_waitcnt lgkmcnt(14)
ds_read_b64_tr_b16 a[128:129],v1 offset:192
ds_read_b64_tr_b16 a[130:131],v1 offset:5312
v_mfma_f32_16x16x32_bf16 a[28:31],a[96:99],v[112:115],a[28:31]
s_waitcnt lgkmcnt(14)
ds_read_b64_tr_b16 a[132:133],v1 offset:10432
ds_read_b64_tr_b16 a[134:135],v1 offset:15552
v_mfma_f32_16x16x32_bf16 a[28:31],a[100:103],v[116:119],a[28:31]
s_waitcnt lgkmcnt(14)
ds_read_b64_tr_b16 a[136:137],v1 offset:20672
ds_read_b64_tr_b16 a[138:139],v1 offset:25792
v_mfma_f32_16x16x32_bf16 a[28:31],a[104:107],v[120:123],a[28:31]
s_waitcnt lgkmcnt(14)
ds_read_b64_tr_b16 a[140:141],v1 offset:30912
ds_read_b64_tr_b16 a[142:143],v1 offset:36032
v_mfma_f32_16x16x32_bf16 a[28:31],a[108:111],v[124:127],a[28:31]
s_waitcnt lgkmcnt(14)
ds_read_b64_tr_b16 a[144:145],v42 offset:192
ds_read_b64_tr_b16 a[146:147],v42 offset:5312
v_mfma_f32_16x16x32_bf16 a[28:31],a[112:115],v[232:235],a[28:31]
s_waitcnt lgkmcnt(14)
ds_read_b64_tr_b16 a[148:149],v42 offset:10432
ds_read_b64_tr_b16 a[150:151],v42 offset:15552
v_mfma_f32_16x16x32_bf16 a[28:31],a[116:119],v[236:239],a[28:31]
s_waitcnt lgkmcnt(14)
ds_read_b64_tr_b16 a[152:153],v42 offset:20672
ds_read_b64_tr_b16 a[154:155],v42 offset:25792
v_mfma_f32_16x16x32_bf16 a[28:31],a[120:123],v[248:251],a[28:31]
s_waitcnt lgkmcnt(14)
ds_read_b64_tr_b16 a[156:157],v42 offset:30912
ds_read_b64_tr_b16 a[158:159],v42 offset:36032
v_mfma_f32_16x16x32_bf16 a[28:31],a[124:127],v[252:255],a[28:31]
s_waitcnt lgkmcnt(14)
ds_read_b64_tr_b16 a[160:161],v44 offset:192
ds_read_b64_tr_b16 a[162:163],v44 offset:5312
v_mfma_f32_16x16x32_bf16 a[44:47],a[128:131],v[148:151],a[44:47]
s_waitcnt lgkmcnt(14)
ds_read_b64_tr_b16 a[164:165],v44 offset:10432
ds_read_b64_tr_b16 a[166:167],v44 offset:15552
v_mfma_f32_16x16x32_bf16 a[44:47],a[132:135],v[180:183],a[44:47]
s_waitcnt lgkmcnt(14)
ds_read_b64_tr_b16 a[168:169],v44 offset:20672
ds_read_b64_tr_b16 a[170:171],v44 offset:25792
v_mfma_f32_16x16x32_bf16 a[44:47],a[136:139],v[212:215],a[44:47]
ds_read_b64_tr_b16 a[172:173],v44 offset:30912
ds_read_b64_tr_b16 a[174:175],v44 offset:36032
s_waitcnt vmcnt(1)
v_cmp_ne_u32 s[90:91],s68,v4
v_and_b32 v4,-2,v5
v_cmp_ne_u32 s[92:93],s69,v4
s_nop 0
s_or_b64 s[90:91],s[90:91],s[92:93]
s_cbranch_scc1 L_fr11_%=
L_fo11_%=:
s_waitcnt lgkmcnt(15)
v_mfma_f32_16x16x32_bf16 a[44:47],a[140:143],v[244:247],a[44:47]
s_waitcnt lgkmcnt(14)
buffer_load_dwordx4 v[72:75],v6,s[32:35],s76 offen offset:2048 sc1
ds_read_b64_tr_b16 a[176:177],v2 offset:224
ds_read_b64_tr_b16 a[178:179],v2 offset:5344
v_mfma_f32_16x16x32_bf16 a[44:47],a[144:147],v[112:115],a[44:47]
s_lshl_b32 s94,s71,2
ds_read_b64_tr_b16 a[180:181],v2 offset:10464
ds_read_b64_tr_b16 a[182:183],v2 offset:15584
s_waitcnt lgkmcnt(15)
v_mfma_f32_16x16x32_bf16 a[44:47],a[148:151],v[116:119],a[44:47]
s_add_u32 s94,s94,s40
ds_read_b64_tr_b16 a[184:185],v2 offset:20704
ds_read_b64_tr_b16 a[186:187],v2 offset:25824
s_waitcnt lgkmcnt(15)
v_mfma_f32_16x16x32_bf16 a[44:47],a[152:155],v[120:123],a[44:47]
s_lshl_b32 s94,s94,4
ds_read_b64_tr_b16 a[188:189],v2 offset:30944
ds_read_b64_tr_b16 a[190:191],v2 offset:36064
s_waitcnt lgkmcnt(15)
v_mfma_f32_16x16x32_bf16 a[44:47],a[156:159],v[124:127],a[44:47]
s_waitcnt lgkmcnt(14)
buffer_load_dwordx4 v[88:91],v6,s[32:35],s77 offen offset:2048 sc1
ds_read_b64_tr_b16 a[96:97],v43 offset:224
ds_read_b64_tr_b16 a[98:99],v43 offset:5344
v_mfma_f32_16x16x32_bf16 a[44:47],a[160:163],v[232:235],a[44:47]
s_waitcnt lgkmcnt(14)
ds_read_b64_tr_b16 a[100:101],v43 offset:10464
ds_read_b64_tr_b16 a[102:103],v43 offset:15584
v_mfma_f32_16x16x32_bf16 a[44:47],a[164:167],v[236:239],a[44:47]
s_waitcnt lgkmcnt(14)
ds_read_b64_tr_b16 a[104:105],v43 offset:20704
ds_read_b64_tr_b16 a[106:107],v43 offset:25824
v_mfma_f32_16x16x32_bf16 a[44:47],a[168:171],v[248:251],a[44:47]
s_waitcnt lgkmcnt(14)
ds_read_b64_tr_b16 a[108:109],v43 offset:30944
ds_read_b64_tr_b16 a[110:111],v43 offset:36064
)A"
R"A(v_mfma_f32_16x16x32_bf16 a[44:47],a[172:175],v[252:255],a[44:47]
s_waitcnt lgkmcnt(14)
buffer_load_dwordx4 v[104:107],v6,s[32:35],s78 offen offset:2048 sc1
ds_read_b64_tr_b16 a[112:113],v45 offset:224
ds_read_b64_tr_b16 a[114:115],v45 offset:5344
v_mfma_f32_16x16x32_bf16 a[60:63],a[176:179],v[148:151],a[60:63]
s_waitcnt lgkmcnt(14)
ds_read_b64_tr_b16 a[116:117],v45 offset:10464
ds_read_b64_tr_b16 a[118:119],v45 offset:15584
v_mfma_f32_16x16x32_bf16 a[60:63],a[180:183],v[180:183],a[60:63]
s_waitcnt lgkmcnt(14)
ds_read_b64_tr_b16 a[120:121],v45 offset:20704
ds_read_b64_tr_b16 a[122:123],v45 offset:25824
v_mfma_f32_16x16x32_bf16 a[60:63],a[184:187],v[212:215],a[60:63]
s_waitcnt lgkmcnt(14)
ds_read_b64_tr_b16 a[124:125],v45 offset:30944
ds_read_b64_tr_b16 a[126:127],v45 offset:36064
v_mfma_f32_16x16x32_bf16 a[60:63],a[188:191],v[244:247],a[60:63]
s_waitcnt lgkmcnt(14)
buffer_load_dwordx4 v[76:79],v6,s[32:35],s76 offen offset:3072 sc1
v_mfma_f32_16x16x32_bf16 a[60:63],a[96:99],v[112:115],a[60:63]
s_waitcnt lgkmcnt(12)
ds_read_b64_tr_b16 a[128:129],v1 offset:256
ds_read_b64_tr_b16 a[130:131],v1 offset:5376
v_mfma_f32_16x16x32_bf16 a[60:63],a[100:103],v[116:119],a[60:63]
s_waitcnt lgkmcnt(12)
ds_read_b64_tr_b16 a[132:133],v1 offset:10496
ds_read_b64_tr_b16 a[134:135],v1 offset:15616
v_mfma_f32_16x16x32_bf16 a[60:63],a[104:107],v[120:123],a[60:63]
s_waitcnt lgkmcnt(12)
ds_read_b64_tr_b16 a[136:137],v1 offset:20736
ds_read_b64_tr_b16 a[138:139],v1 offset:25856
v_mfma_f32_16x16x32_bf16 a[60:63],a[108:111],v[124:127],a[60:63]
s_waitcnt lgkmcnt(12)
buffer_load_dwordx4 v[92:95],v6,s[32:35],s77 offen offset:3072 sc1
v_mfma_f32_16x16x32_bf16 a[60:63],a[112:115],v[232:235],a[60:63]
s_waitcnt lgkmcnt(10)
ds_read_b64_tr_b16 a[140:141],v1 offset:30976
ds_read_b64_tr_b16 a[142:143],v1 offset:36096
v_mfma_f32_16x16x32_bf16 a[60:63],a[116:119],v[236:239],a[60:63]
s_waitcnt lgkmcnt(10)
ds_read_b64_tr_b16 a[144:145],v42 offset:256
ds_read_b64_tr_b16 a[146:147],v42 offset:5376
v_mfma_f32_16x16x32_bf16 a[60:63],a[120:123],v[248:251],a[60:63]
s_waitcnt lgkmcnt(10)
ds_read_b64_tr_b16 a[148:149],v42 offset:10496
ds_read_b64_tr_b16 a[150:151],v42 offset:15616
v_mfma_f32_16x16x32_bf16 a[60:63],a[124:127],v[252:255],a[60:63]
buffer_load_dwordx4 v[108:111],v6,s[32:35],s78 offen offset:3072 sc1
v_mfma_f32_16x16x32_bf16 a[204:207],v[34:37],a[192:195],a[204:207]
ds_read_b64_tr_b16 a[152:153],v42 offset:20736
ds_read_b64_tr_b16 a[154:155],v42 offset:25856
v_mfma_f32_16x16x32_bf16 a[204:207],v[34:37],a[196:199],a[204:207]
ds_read_b64_tr_b16 a[156:157],v42 offset:30976
ds_read_b64_tr_b16 a[158:159],v42 offset:36096
v_mfma_f32_16x16x32_bf16 a[204:207],v[34:37],a[200:203],a[204:207]
ds_read_b64_tr_b16 a[160:161],v44 offset:256
ds_read_b64_tr_b16 a[162:163],v44 offset:5376
v_mfma_f32_16x16x32_bf16 a[220:223],v[32:35],a[192:195],a[220:223]
ds_read_b64_tr_b16 a[164:165],v44 offset:10496
ds_read_b64_tr_b16 a[166:167],v44 offset:15616
v_mfma_f32_16x16x32_bf16 a[220:223],v[32:35],a[196:199],a[220:223]
ds_read_b64_tr_b16 a[168:169],v44 offset:20736
ds_read_b64_tr_b16 a[170:171],v44 offset:25856
v_mfma_f32_16x16x32_bf16 a[220:223],v[32:35],a[200:203],a[220:223]
s_waitcnt lgkmcnt(12)
ds_read_b64_tr_b16 a[172:173],v44 offset:30976
ds_read_b64_tr_b16 a[174:175],v44 offset:36096
v_mfma_f32_16x16x32_bf16 a[76:79],a[128:131],v[148:151],a[76:79]
v_bfe_u32 v24,v6,4,2
v_min_u32 v24,2,v24
v_mfma_f32_16x16x32_bf16 a[76:79],a[132:135],v[180:183],a[76:79]
v_add_u32 v24,s70,v24
v_add_u32 v24,1,v24
v_mfma_f32_16x16x32_bf16 a[76:79],a[136:139],v[212:215],a[76:79]
v_accvgpr_read_b32 v160,a204
v_accvgpr_read_b32 v161,a205
v_mfma_f32_16x16x32_bf16 a[76:79],a[140:143],v[244:247],a[76:79]
v_cvt_pk_bf16_f32 v160,v160,v161
v_accvgpr_read_b32 v162,a206
v_mfma_f32_16x16x32_bf16 a[76:79],a[144:147],v[112:115],a[76:79]
s_waitcnt lgkmcnt(6)
v_accvgpr_read_b32 v163,a207
v_cvt_pk_bf16_f32 v161,v162,v163
v_mfma_f32_16x16x32_bf16 a[76:79],a[148:151],v[116:119],a[76:79]
v_accvgpr_read_b32 v162,a220
v_accvgpr_read_b32 v163,a221
v_mfma_f32_16x16x32_bf16 a[76:79],a[152:155],v[120:123],a[76:79]
v_accvgpr_read_b32 v16,a222
v_accvgpr_read_b32 v17,a223
v_mfma_f32_16x16x32_bf16 a[76:79],a[156:159],v[124:127],a[76:79]
v_cvt_pk_bf16_f32 v162,v162,v163
v_cvt_pk_bf16_f32 v163,v16,v17
v_mfma_f32_16x16x32_bf16 a[76:79],a[160:163],v[232:235],a[76:79]
s_waitcnt lgkmcnt(0)
ds_read_b64_tr_b16 a[176:177],v2 offset:288
ds_read_b64_tr_b16 a[178:179],v2 offset:5408
v_mfma_f32_16x16x32_bf16 a[76:79],a[164:167],v[236:239],a[76:79]
v_permlane16_swap_b32 v160,v162
v_mfma_f32_16x16x32_bf16 a[76:79],a[168:171],v[248:251],a[76:79]
ds_read_b64_tr_b16 a[180:181],v2 offset:10528
ds_read_b64_tr_b16 a[182:183],v2 offset:15648
v_and_b32 v24,3,v24
v_lshlrev_b32 v24,8,v24
v_mfma_f32_16x16x32_bf16 a[76:79],a[172:175],v[252:255],a[76:79]
buffer_load_dwordx2 v[4:5],v24,s[64:67],s94 offen offset:3072 sc1
ds_read_b64_tr_b16 a[184:185],v2 offset:20768
ds_read_b64_tr_b16 a[186:187],v2 offset:25888
v_mfma_f32_16x16x32_bf16 a[236:239],v[34:37],a[224:227],a[236:239]
ds_read_b64_tr_b16 a[188:189],v2 offset:31008
ds_read_b64_tr_b16 a[190:191],v2 offset:36128
v_mfma_f32_16x16x32_bf16 a[236:239],v[34:37],a[228:231],a[236:239]
ds_read_b64_tr_b16 a[96:97],v43 offset:288
ds_read_b64_tr_b16 a[98:99],v43 offset:5408
v_mfma_f32_16x16x32_bf16 a[236:239],v[34:37],a[232:235],a[236:239]
ds_read_b64_tr_b16 a[100:101],v43 offset:10528
ds_read_b64_tr_b16 a[102:103],v43 offset:15648
v_mfma_f32_16x16x32_bf16 a[252:255],v[32:35],a[224:227],a[252:255]
ds_read_b64_tr_b16 a[104:105],v43 offset:20768
ds_read_b64_tr_b16 a[106:107],v43 offset:25888
v_mfma_f32_16x16x32_bf16 a[252:255],v[32:35],a[228:231],a[252:255]
ds_read_b64_tr_b16 a[108:109],v43 offset:31008
ds_read_b64_tr_b16 a[110:111],v43 offset:36128
v_mfma_f32_16x16x32_bf16 a[252:255],v[32:35],a[232:235],a[252:255]
s_waitcnt lgkmcnt(14)
ds_read_b64_tr_b16 a[112:113],v45 offset:288
ds_read_b64_tr_b16 a[114:115],v45 offset:5408
v_mfma_f32_16x16x32_bf16 a[92:95],a[176:179],v[148:151],a[92:95]
s_waitcnt lgkmcnt(14)
ds_read_b64_tr_b16 a[116:117],v45 offset:10528
ds_read_b64_tr_b16 a[118:119],v45 offset:15648
v_mfma_f32_16x16x32_bf16 a[92:95],a[180:183],v[180:183],a[92:95]
s_waitcnt lgkmcnt(14)
ds_read_b64_tr_b16 a[120:121],v45 offset:20768
ds_read_b64_tr_b16 a[122:123],v45 offset:25888
v_mfma_f32_16x16x32_bf16 a[92:95],a[184:187],v[212:215],a[92:95]
s_waitcnt lgkmcnt(14)
ds_read_b64_tr_b16 a[124:125],v45 offset:31008
ds_read_b64_tr_b16 a[126:127],v45 offset:36128
v_mfma_f32_16x16x32_bf16 a[92:95],a[188:191],v[244:247],a[92:95]
s_waitcnt lgkmcnt(14)
v_permlane16_swap_b32 v161,v163
v_mfma_f32_16x16x32_bf16 a[92:95],a[96:99],v[112:115],a[92:95]
s_cmp_lg_u32 %2,0
s_cbranch_scc1 L_ea0_%=
buffer_store_dwordx4 v[160:163],v40,s[20:23],s43 offen
s_branch L_eb0_%=
L_ea0_%=:
buffer_store_dwordx4 v[160:163],v40,s[20:23],s43 offen sc1
L_eb0_%=:
s_waitcnt lgkmcnt(12)
v_mfma_f32_16x16x32_bf16 a[92:95],a[100:103],v[116:119],a[92:95]
s_waitcnt lgkmcnt(10)
v_accvgpr_read_b32 v164,a236
v_accvgpr_read_b32 v165,a237
v_mfma_f32_16x16x32_bf16 a[92:95],a[104:107],v[120:123],a[92:95]
s_waitcnt lgkmcnt(8)
v_cvt_pk_bf16_f32 v164,v164,v165
v_accvgpr_read_b32 v166,a238
v_mfma_f32_16x16x32_bf16 a[92:95],a[108:111],v[124:127],a[92:95]
s_waitcnt lgkmcnt(6)
v_accvgpr_read_b32 v167,a239
v_cvt_pk_bf16_f32 v165,v166,v167
v_mfma_f32_16x16x32_bf16 a[92:95],a[112:115],v[232:235],a[92:95]
s_waitcnt lgkmcnt(4)
v_accvgpr_read_b32 v166,a252
v_accvgpr_read_b32 v167,a253
v_mfma_f32_16x16x32_bf16 a[92:95],a[116:119],v[236:239],a[92:95]
s_waitcnt lgkmcnt(2)
v_accvgpr_read_b32 v12,a254
v_accvgpr_read_b32 v13,a255
v_cvt_pk_bf16_f32 v166,v166,v167
v_cvt_pk_bf16_f32 v167,v12,v13
v_mfma_f32_16x16x32_bf16 a[92:95],a[120:123],v[248:251],a[92:95]
s_waitcnt lgkmcnt(0)
v_mfma_f32_16x16x32_bf16 a[92:95],a[124:127],v[252:255],a[92:95]
v_permlane16_swap_b32 v164,v166
v_permlane16_swap_b32 v165,v167
s_nop 3
s_cmp_lg_u32 %2,0
s_cbranch_scc1 L_ea1_%=
buffer_store_dwordx4 v[164:167],v40,s[20:23],s43 offen offset:64
s_branch L_eb1_%=
L_ea1_%=:
buffer_store_dwordx4 v[164:167],v40,s[20:23],s43 offen offset:64 sc1
L_eb1_%=:
s_waitcnt vmcnt(2)
v_cmp_ne_u32 s[90:91],s68,v4
v_and_b32 v4,-2,v5
v_cmp_ne_u32 s[92:93],s69,v4
s_nop 0
s_or_b64 s[90:91],s[90:91],s[92:93]
s_cbranch_scc1 L_fr12_%=
L_fo12_%=:
buffer_load_dwordx4 v[136:139],v25,s[32:35],s76 offen sc1
buffer_load_dwordx4 v[168:171],v25,s[32:35],s77 offen sc1
buffer_load_dwordx4 v[200:203],v25,s[32:35],s78 offen sc1
s_cmp_lg_u32 %2,0
s_cbranch_scc1 L_dual_%=
v_mfma_f32_16x16x32_bf16 a[12:15],v[34:37],v[72:75],a[12:15]
v_mfma_f32_16x16x32_bf16 a[12:15],v[34:37],v[88:91],a[12:15]
v_mfma_f32_16x16x32_bf16 a[12:15],v[34:37],v[104:107],a[12:15]
v_mfma_f32_16x16x32_bf16 a[28:31],v[32:35],v[72:75],a[28:31]
v_mfma_f32_16x16x32_bf16 a[28:31],v[32:35],v[88:91],a[28:31]
v_mfma_f32_16x16x32_bf16 a[28:31],v[32:35],v[104:107],a[28:31]
v_mfma_f32_16x16x32_bf16 a[44:47],v[34:37],v[76:79],a[44:47]
v_mfma_f32_16x16x32_bf16 a[44:47],v[34:37],v[92:95],a[44:47]
v_mfma_f32_16x16x32_bf16 a[44:47],v[34:37],v[108:111],a[44:47]
s_nop 1
v_accvgpr_read_b32 v176,a12
v_accvgpr_read_b32 v177,a13
v_cvt_pk_bf16_f32 v176,v176,v177
v_accvgpr_read_b32 v178,a14
v_accvgpr_read_b32 v179,a15
v_cvt_pk_bf16_f32 v177,v178,v179
v_mfma_f32_16x16x32_bf16 a[60:63],v[32:35],v[76:79],a[60:63]
v_mfma_f32_16x16x32_bf16 a[60:63],v[32:35],v[92:95],a[60:63]
v_mfma_f32_16x16x32_bf16 a[60:63],v[32:35],v[108:111],a[60:63]
v_accvgpr_read_b32 v178,a28
v_accvgpr_read_b32 v179,a29
v_accvgpr_read_b32 v14,a30
v_accvgpr_read_b32 v15,a31
v_cvt_pk_bf16_f32 v178,v178,v179
v_cvt_pk_bf16_f32 v179,v14,v15
s_nop 3
v_permlane16_swap_b32 v176,v178
v_permlane16_swap_b32 v177,v179
buffer_store_dwordx4 v[176:179],v40,s[20:23],s43 offen offset:128
v_accvgpr_read_b32 v192,a44
v_accvgpr_read_b32 v193,a45
v_cvt_pk_bf16_f32 v192,v192,v193
v_accvgpr_read_b32 v194,a46
v_accvgpr_read_b32 v195,a47
v_cvt_pk_bf16_f32 v193,v194,v195
v_accvgpr_read_b32 v194,a60
v_accvgpr_read_b32 v195,a61
v_accvgpr_read_b32 v16,a62
v_accvgpr_read_b32 v17,a63
v_cvt_pk_bf16_f32 v194,v194,v195
s_nop 1
v_permlane16_swap_b32 v192,v194
v_cvt_pk_bf16_f32 v195,v16,v17
s_nop 1
v_permlane16_swap_b32 v193,v195
buffer_store_dwordx4 v[192:195],v40,s[20:23],s43 offen offset:192
s_waitcnt vmcnt(4)
v_mfma_f32_16x16x32_bf16 a[76:79],v[34:37],v[136:139],a[76:79]
s_waitcnt vmcnt(3)
v_mfma_f32_16x16x32_bf16 a[76:79],v[34:37],v[168:171],a[76:79]
s_waitcnt vmcnt(2)
v_mfma_f32_16x16x32_bf16 a[76:79],v[34:37],v[200:203],a[76:79]
v_mfma_f32_16x16x32_bf16 a[92:95],v[32:35],v[136:139],a[92:95]
v_mfma_f32_16x16x32_bf16 a[92:95],v[32:35],v[168:171],a[92:95]
v_mfma_f32_16x16x32_bf16 a[92:95],v[32:35],v[200:203],a[92:95]
s_nop 3
s_nop 0
v_accvgpr_read_b32 v196,a76
v_accvgpr_read_b32 v197,a77
v_cvt_pk_bf16_f32 v196,v196,v197
v_accvgpr_read_b32 v198,a78
v_accvgpr_read_b32 v199,a79
v_cvt_pk_bf16_f32 v197,v198,v199
v_accvgpr_read_b32 v198,a92
v_accvgpr_read_b32 v199,a93
v_accvgpr_read_b32 v12,a94
v_accvgpr_read_b32 v13,a95
v_cvt_pk_bf16_f32 v198,v198,v199
s_nop 1
v_permlane16_swap_b32 v196,v198
v_cvt_pk_bf16_f32 v199,v12,v13
s_nop 1
v_permlane16_swap_b32 v197,v199
buffer_store_dwordx4 v[196:199],v40,s[20:23],s43 offen offset:256
s_or_b32 s62,s42,s40
s_cmp_lg_u32 s62,0
s_cbranch_scc1 L_noep_%=
v_mov_b32 v12,s68
s_mov_b64 exec,1
buffer_store_dword v12,v25,s[64:67],0 offen sc1
s_mov_b64 exec,-1
L_noep_%=:
s_endpgm
L_dual_%=:
v_mfma_f32_16x16x32_bf16 a[12:15],v[34:37],v[72:75],a[12:15]
v_mfma_f32_16x16x32_bf16 a[12:15],v[34:37],v[88:91],a[12:15]
v_mfma_f32_16x16x32_bf16 a[12:15],v[34:37],v[104:107],a[12:15]
v_mfma_f32_16x16x32_bf16 a[28:31],v[32:35],v[72:75],a[28:31]
v_mfma_f32_16x16x32_bf16 a[28:31],v[32:35],v[88:91],a[28:31]
v_mfma_f32_16x16x32_bf16 a[28:31],v[32:35],v[104:107],a[28:31]
v_mfma_f32_16x16x32_bf16 a[44:47],v[34:37],v[76:79],a[44:47]
v_mfma_f32_16x16x32_bf16 a[44:47],v[34:37],v[92:95],a[44:47]
v_mfma_f32_16x16x32_bf16 a[44:47],v[34:37],v[108:111],a[44:47]
s_nop 1
v_accvgpr_read_b32 v176,a12
v_accvgpr_read_b32 v177,a13
)A"
R"A(v_cvt_pk_bf16_f32 v176,v176,v177
v_accvgpr_read_b32 v178,a14
v_accvgpr_read_b32 v179,a15
v_cvt_pk_bf16_f32 v177,v178,v179
v_mfma_f32_16x16x32_bf16 a[60:63],v[32:35],v[76:79],a[60:63]
v_mfma_f32_16x16x32_bf16 a[60:63],v[32:35],v[92:95],a[60:63]
v_mfma_f32_16x16x32_bf16 a[60:63],v[32:35],v[108:111],a[60:63]
v_accvgpr_read_b32 v178,a28
v_accvgpr_read_b32 v179,a29
v_accvgpr_read_b32 v14,a30
v_accvgpr_read_b32 v15,a31
v_cvt_pk_bf16_f32 v178,v178,v179
v_cvt_pk_bf16_f32 v179,v14,v15
s_nop 3
v_permlane16_swap_b32 v176,v178
v_permlane16_swap_b32 v177,v179
buffer_store_dwordx4 v[176:179],v40,s[20:23],s43 offen offset:128 sc1
v_accvgpr_read_b32 v192,a44
v_accvgpr_read_b32 v193,a45
v_cvt_pk_bf16_f32 v192,v192,v193
v_accvgpr_read_b32 v194,a46
v_accvgpr_read_b32 v195,a47
v_cvt_pk_bf16_f32 v193,v194,v195
v_accvgpr_read_b32 v194,a60
v_accvgpr_read_b32 v195,a61
v_accvgpr_read_b32 v16,a62
v_accvgpr_read_b32 v17,a63
v_cvt_pk_bf16_f32 v194,v194,v195
s_nop 1
v_permlane16_swap_b32 v192,v194
v_cvt_pk_bf16_f32 v195,v16,v17
s_nop 1
v_permlane16_swap_b32 v193,v195
buffer_store_dwordx4 v[192:195],v40,s[20:23],s43 offen offset:192 sc1
s_waitcnt vmcnt(4)
v_mfma_f32_16x16x32_bf16 a[76:79],v[34:37],v[136:139],a[76:79]
s_waitcnt vmcnt(3)
v_mfma_f32_16x16x32_bf16 a[76:79],v[34:37],v[168:171],a[76:79]
s_waitcnt vmcnt(2)
v_mfma_f32_16x16x32_bf16 a[76:79],v[34:37],v[200:203],a[76:79]
v_mfma_f32_16x16x32_bf16 a[92:95],v[32:35],v[136:139],a[92:95]
v_mfma_f32_16x16x32_bf16 a[92:95],v[32:35],v[168:171],a[92:95]
v_mfma_f32_16x16x32_bf16 a[92:95],v[32:35],v[200:203],a[92:95]
s_nop 3
s_nop 0
v_accvgpr_read_b32 v196,a76
v_accvgpr_read_b32 v197,a77
v_cvt_pk_bf16_f32 v196,v196,v197
v_accvgpr_read_b32 v198,a78
v_accvgpr_read_b32 v199,a79
v_cvt_pk_bf16_f32 v197,v198,v199
v_accvgpr_read_b32 v198,a92
v_accvgpr_read_b32 v199,a93
v_accvgpr_read_b32 v12,a94
v_accvgpr_read_b32 v13,a95
v_cvt_pk_bf16_f32 v198,v198,v199
s_nop 1
v_permlane16_swap_b32 v196,v198
v_cvt_pk_bf16_f32 v199,v12,v13
s_nop 1
v_permlane16_swap_b32 v197,v199
buffer_store_dwordx4 v[196:199],v40,s[20:23],s43 offen offset:256 sc1
s_or_b32 s62,s42,s40
s_cmp_lg_u32 s62,0
s_cbranch_scc1 L_noep2_%=
v_mov_b32 v12,s68
s_mov_b64 exec,1
buffer_store_dword v12,v25,s[64:67],0 offen sc1
s_mov_b64 exec,-1
L_noep2_%=:
s_endpgm
L_ps2_%=:
s_min_i32 s79,s79,100
s_add_u32 s78,s78,s79
L_pl2_%=:
s_sleep 1
s_sub_u32 s79,s79,4
s_cmp_gt_i32 s79,1
s_cbranch_scc1 L_pl2_%=
s_branch L_pr2_%=
L_ps3_%=:
s_min_i32 s79,s79,100
s_add_u32 s78,s78,s79
L_pl3_%=:
s_sleep 1
s_sub_u32 s79,s79,4
s_cmp_gt_i32 s79,1
s_cbranch_scc1 L_pl3_%=
s_branch L_pr3_%=
L_ps4_%=:
s_min_i32 s79,s79,100
s_add_u32 s78,s78,s79
L_pl4_%=:
s_sleep 1
s_sub_u32 s79,s79,4
s_cmp_gt_i32 s79,1
s_cbranch_scc1 L_pl4_%=
s_branch L_pr4_%=
L_ps5_%=:
s_min_i32 s79,s79,100
s_add_u32 s78,s78,s79
L_pl5_%=:
s_sleep 1
s_sub_u32 s79,s79,4
s_cmp_gt_i32 s79,1
s_cbranch_scc1 L_pl5_%=
s_branch L_pr5_%=
L_ps6_%=:
s_min_i32 s79,s79,100
s_add_u32 s78,s78,s79
L_pl6_%=:
s_sleep 1
s_sub_u32 s79,s79,4
s_cmp_gt_i32 s79,1
s_cbranch_scc1 L_pl6_%=
s_branch L_pr6_%=
L_ps7_%=:
s_min_i32 s79,s79,100
s_add_u32 s78,s78,s79
L_pl7_%=:
s_sleep 1
s_sub_u32 s79,s79,4
s_cmp_gt_i32 s79,1
s_cbranch_scc1 L_pl7_%=
s_branch L_pr7_%=
L_fr8_%=:
s_mov_b32 s88,262144
L_fl8_%=:
buffer_load_dwordx2 v[4:5],v24,s[64:67],s94 offen sc1
s_waitcnt vmcnt(0)
s_sub_u32 s88,s88,1
s_cmp_eq_u32 s88,0
s_cbranch_scc1 L_fo8_%=
v_cmp_ne_u32 s[90:91],s68,v4
v_and_b32 v4,-2,v5
v_cmp_ne_u32 s[92:93],s69,v4
s_nop 0
s_or_b64 s[90:91],s[90:91],s[92:93]
s_cbranch_scc1 L_fl8_%=
s_branch L_fo8_%=
L_fr9_%=:
s_mov_b32 s88,262144
L_fl9_%=:
buffer_load_dwordx2 v[4:5],v24,s[64:67],s94 offen offset:1024 sc1
s_waitcnt vmcnt(0)
s_sub_u32 s88,s88,1
s_cmp_eq_u32 s88,0
s_cbranch_scc1 L_fo9_%=
v_cmp_ne_u32 s[90:91],s68,v4
v_and_b32 v4,-2,v5
v_cmp_ne_u32 s[92:93],s69,v4
s_nop 0
s_or_b64 s[90:91],s[90:91],s[92:93]
s_cbranch_scc1 L_fl9_%=
s_branch L_fo9_%=
L_fr10_%=:
s_mov_b32 s88,262144
L_fl10_%=:
buffer_load_dwordx2 v[4:5],v24,s[64:67],s94 offen sc1
s_waitcnt vmcnt(0)
s_sub_u32 s88,s88,1
s_cmp_eq_u32 s88,0
s_cbranch_scc1 L_fo10_%=
v_cmp_ne_u32 s[90:91],s68,v4
v_and_b32 v4,-2,v5
v_cmp_ne_u32 s[92:93],s69,v4
s_nop 0
s_or_b64 s[90:91],s[90:91],s[92:93]
s_cbranch_scc1 L_fl10_%=
s_branch L_fo10_%=
L_fr11_%=:
s_mov_b32 s88,262144
L_fl11_%=:
buffer_load_dwordx2 v[4:5],v24,s[64:67],s94 offen offset:2048 sc1
s_waitcnt vmcnt(0)
s_sub_u32 s88,s88,1
s_cmp_eq_u32 s88,0
s_cbranch_scc1 L_fo11_%=
v_cmp_ne_u32 s[90:91],s68,v4
v_and_b32 v4,-2,v5
v_cmp_ne_u32 s[92:93],s69,v4
s_nop 0
s_or_b64 s[90:91],s[90:91],s[92:93]
s_cbranch_scc1 L_fl11_%=
s_branch L_fo11_%=
L_fr12_%=:
s_mov_b32 s88,262144
L_fl12_%=:
buffer_load_dwordx2 v[4:5],v24,s[64:67],s94 offen offset:3072 sc1
s_waitcnt vmcnt(0)
s_sub_u32 s88,s88,1
s_cmp_eq_u32 s88,0
s_cbranch_scc1 L_fo12_%=
v_cmp_ne_u32 s[90:91],s68,v4
v_and_b32 v4,-2,v5
v_cmp_ne_u32 s[92:93],s69,v4
s_nop 0
s_or_b64 s[90:91],s[90:91],s[92:93]
s_cbranch_scc1 L_fl12_%=
s_branch L_fo12_%=
L_out_end_%=:
L_fast_end_%=:
L_slow0_%=:
s_mov_b32 s98,0
s_branch L_slowc_%=
L_slow1_%=:
s_mov_b32 s98,1
L_slowc_%=:
s_nop 7
s_nop 7
s_waitcnt vmcnt(0) lgkmcnt(0)
s_lshl_b32 s96,%1,2
s_add_u32 s96,s96,s40
s_lshl_b32 s96,s96,15
s_add_u32 s96,s96,167772160
s_mov_b64 s[56:57],s[12:13]
s_add_u32 s56,s56,s96
s_addc_u32 s57,s57,0
s_and_b32 s57,s57,0xffff
s_mov_b32 s58,32768
s_mov_b32 s59,0x20000
s_mov_b32 s96,0
buffer_store_dwordx4 v[64:67],v6,s[56:59],s96 offen
buffer_store_dwordx4 v[68:71],v6,s[56:59],s96 offen offset:1024
buffer_store_dwordx4 v[72:75],v6,s[56:59],s96 offen offset:2048
buffer_store_dwordx4 v[76:79],v6,s[56:59],s96 offen offset:3072
s_movk_i32 s96,4096
buffer_store_dwordx4 v[80:83],v6,s[56:59],s96 offen
buffer_store_dwordx4 v[84:87],v6,s[56:59],s96 offen offset:1024
buffer_store_dwordx4 v[88:91],v6,s[56:59],s96 offen offset:2048
buffer_store_dwordx4 v[92:95],v6,s[56:59],s96 offen offset:3072
s_movk_i32 s96,8192
buffer_store_dwordx4 v[96:99],v6,s[56:59],s96 offen
buffer_store_dwordx4 v[100:103],v6,s[56:59],s96 offen offset:1024
buffer_store_dwordx4 v[104:107],v6,s[56:59],s96 offen offset:2048
buffer_store_dwordx4 v[108:111],v6,s[56:59],s96 offen offset:3072
s_movk_i32 s96,12288
buffer_store_dwordx4 v[112:115],v6,s[56:59],s96 offen
buffer_store_dwordx4 v[116:119],v6,s[56:59],s96 offen offset:1024
buffer_store_dwordx4 v[120:123],v6,s[56:59],s96 offen offset:2048
buffer_store_dwordx4 v[124:127],v6,s[56:59],s96 offen offset:3072
s_movk_i32 s96,16384
buffer_store_dwordx4 v[128:131],v6,s[56:59],s96 offen
buffer_store_dwordx4 v[132:135],v6,s[56:59],s96 offen offset:1024
buffer_store_dwordx4 v[136:139],v6,s[56:59],s96 offen offset:2048
buffer_store_dwordx4 v[140:143],v6,s[56:59],s96 offen offset:3072
s_movk_i32 s96,20480
buffer_store_dwordx4 v[144:147],v6,s[56:59],s96 offen
buffer_store_dwordx4 v[148:151],v6,s[56:59],s96 offen offset:1024
buffer_store_dwordx4 v[152:155],v6,s[56:59],s96 offen offset:2048
buffer_store_dwordx4 v[156:159],v6,s[56:59],s96 offen offset:3072
s_movk_i32 s96,24576
buffer_store_dwordx4 v[160:163],v6,s[56:59],s96 offen
buffer_store_dwordx4 v[164:167],v6,s[56:59],s96 offen offset:1024
buffer_store_dwordx4 v[168:171],v6,s[56:59],s96 offen offset:2048
buffer_store_dwordx4 v[172:175],v6,s[56:59],s96 offen offset:3072
s_movk_i32 s96,28672
buffer_store_dwordx4 v[176:179],v6,s[56:59],s96 offen
buffer_store_dwordx4 v[180:183],v6,s[56:59],s96 offen offset:1024
buffer_store_dwordx4 v[184:187],v6,s[56:59],s96 offen offset:2048
buffer_store_dwordx4 v[188:191],v6,s[56:59],s96 offen offset:3072
s_waitcnt vmcnt(0)
v_writelane_b32 v173,s88,0
v_writelane_b32 v173,s89,1
v_writelane_b32 v173,s90,2
v_writelane_b32 v173,s91,3
v_writelane_b32 v173,s92,4
v_writelane_b32 v173,s93,5
v_writelane_b32 v173,s94,6
v_writelane_b32 v173,s95,7
s_mov_b32 s88,s98
s_mul_i32 s90,s41,307200
s_mov_b64 s[92:93],s[4:5]
s_add_u32 s92,s92,s90
s_addc_u32 s93,s93,0
s_and_b32 s93,s93,0xffff
s_mov_b32 s94,307200
s_mov_b32 s95,0x20000
s_mov_b64 s[96:97],s[10:11]
s_and_b32 s97,s97,0xffff
s_mov_b32 s98,5242880
s_mov_b32 s99,0x20000
v_bfe_u32 v171,v6,4,4
v_lshrrev_b32 v172,8,v6
v_lshlrev_b32 v164,8,v171
v_lshl_add_u32 v164,v172,4,v164
v_lshlrev_b32 v165,10,v172
v_lshl_add_u32 v165,v171,1,v165
v_add_u32 v166,4096,v165
v_lshlrev_b32 v168,16,v172
v_lshrrev_b32 v170,2,v171
v_lshl_add_u32 v168,v170,4,v168
v_and_b32 v170,3,v171
v_lshl_add_u32 v168,v170,1,v168
s_lshl_b32 s90,s42,1
s_add_u32 s90,s90,s88
s_lshl_b32 s90,s90,8
v_add_u32 v168,s90,v168
s_mov_b32 s89,0
L_sjb_%=:
s_add_u32 s90,s89,s70
s_add_u32 s90,s90,1
s_and_b32 s90,s90,3
s_lshl_b32 s91,s40,2
s_add_u32 s90,s90,s91
s_lshl_b32 s90,s90,4
v_bfe_u32 v171,v6,4,4
v_lshrrev_b32 v172,8,v6
v_add_u32 v167,s90,v171
v_mul_u32_u24 v167,1280,v167
v_lshl_add_u32 v167,v172,4,v167
s_lshl_b32 s90,s88,5
s_add_u32 s90,s90,s89
s_and_b32 s91,s89,2
s_add_u32 s90,s90,s91
s_lshl_b32 s90,s90,12
v_add_u32 v169,s90,v6
v_mov_b32 v64,0
v_mov_b32 v65,0
v_mov_b32 v66,0
v_mov_b32 v67,0
v_mov_b32 v68,0
v_mov_b32 v69,0
v_mov_b32 v70,0
v_mov_b32 v71,0
v_mov_b32 v72,0
v_mov_b32 v73,0
v_mov_b32 v74,0
v_mov_b32 v75,0
v_mov_b32 v76,0
v_mov_b32 v77,0
v_mov_b32 v78,0
v_mov_b32 v79,0
v_mov_b32 v80,0
v_mov_b32 v81,0
v_mov_b32 v82,0
v_mov_b32 v83,0
v_mov_b32 v84,0
v_mov_b32 v85,0
v_mov_b32 v86,0
v_mov_b32 v87,0
v_mov_b32 v88,0
v_mov_b32 v89,0
v_mov_b32 v90,0
v_mov_b32 v91,0
v_mov_b32 v92,0
v_mov_b32 v93,0
v_mov_b32 v94,0
v_mov_b32 v95,0
s_mov_b32 s90,0
L_sq_%=:
s_lshl_b32 s91,s90,6
buffer_load_dwordx4 v[160:163],v167,s[92:95],s91 offen
s_lshl_b32 s91,s90,18
buffer_load_ushort v112,v168,s[96:99],s91 offen
buffer_load_ushort v120,v168,s[96:99],s91 offen offset:8
buffer_load_ushort v128,v168,s[96:99],s91 offen offset:64
buffer_load_ushort v136,v168,s[96:99],s91 offen offset:72
s_addk_i32 s91,8192
buffer_load_ushort v113,v168,s[96:99],s91 offen
buffer_load_ushort v121,v168,s[96:99],s91 offen offset:8
buffer_load_ushort v129,v168,s[96:99],s91 offen offset:64
buffer_load_ushort v137,v168,s[96:99],s91 offen offset:72
s_addk_i32 s91,8192
buffer_load_ushort v114,v168,s[96:99],s91 offen
buffer_load_ushort v122,v168,s[96:99],s91 offen offset:8
buffer_load_ushort v130,v168,s[96:99],s91 offen offset:64
buffer_load_ushort v138,v168,s[96:99],s91 offen offset:72
s_addk_i32 s91,8192
buffer_load_ushort v115,v168,s[96:99],s91 offen
buffer_load_ushort v123,v168,s[96:99],s91 offen offset:8
buffer_load_ushort v131,v168,s[96:99],s91 offen offset:64
buffer_load_ushort v139,v168,s[96:99],s91 offen offset:72
s_addk_i32 s91,8192
buffer_load_ushort v116,v168,s[96:99],s91 offen
buffer_load_ushort v124,v168,s[96:99],s91 offen offset:8
buffer_load_ushort v132,v168,s[96:99],s91 offen offset:64
buffer_load_ushort v140,v168,s[96:99],s91 offen offset:72
s_addk_i32 s91,8192
buffer_load_ushort v117,v168,s[96:99],s91 offen
buffer_load_ushort v125,v168,s[96:99],s91 offen offset:8
buffer_load_ushort v133,v168,s[96:99],s91 offen offset:64
buffer_load_ushort v141,v168,s[96:99],s91 offen offset:72
s_addk_i32 s91,8192
buffer_load_ushort v118,v168,s[96:99],s91 offen
buffer_load_ushort v126,v168,s[96:99],s91 offen offset:8
buffer_load_ushort v134,v168,s[96:99],s91 offen offset:64
buffer_load_ushort v142,v168,s[96:99],s91 offen offset:72
s_addk_i32 s91,8192
buffer_load_ushort v119,v168,s[96:99],s91 offen
buffer_load_ushort v127,v168,s[96:99],s91 offen offset:8
buffer_load_ushort v135,v168,s[96:99],s91 offen offset:64
buffer_load_ushort v143,v168,s[96:99],s91 offen offset:72
s_addk_i32 s91,8192
s_waitcnt vmcnt(0)
v_lshl_or_b32 v112,v113,16,v112
v_lshl_or_b32 v113,v115,16,v114
v_lshl_or_b32 v114,v117,16,v116
v_lshl_or_b32 v115,v119,16,v118
v_lshl_or_b32 v120,v121,16,v120
v_lshl_or_b32 v121,v123,16,v122
v_lshl_or_b32 v122,v125,16,v124
v_lshl_or_b32 v123,v127,16,v126
v_lshl_or_b32 v128,v129,16,v128
v_lshl_or_b32 v129,v131,16,v130
v_lshl_or_b32 v130,v133,16,v132
v_lshl_or_b32 v131,v135,16,v134
v_lshl_or_b32 v136,v137,16,v136
v_lshl_or_b32 v137,v139,16,v138
v_lshl_or_b32 v138,v141,16,v140
v_lshl_or_b32 v139,v143,16,v142
s_nop 1
v_mfma_f32_16x16x32_bf16 v[64:67],v[112:115],v[160:163],v[64:67]
)A"
R"A(v_mfma_f32_16x16x32_bf16 v[68:71],v[120:123],v[160:163],v[68:71]
v_mfma_f32_16x16x32_bf16 v[72:75],v[128:131],v[160:163],v[72:75]
v_mfma_f32_16x16x32_bf16 v[76:79],v[136:139],v[160:163],v[76:79]
s_nop 7
s_lshl_b32 s91,s90,18
buffer_load_ushort v112,v168,s[96:99],s91 offen offset:128
buffer_load_ushort v120,v168,s[96:99],s91 offen offset:136
buffer_load_ushort v128,v168,s[96:99],s91 offen offset:192
buffer_load_ushort v136,v168,s[96:99],s91 offen offset:200
s_addk_i32 s91,8192
buffer_load_ushort v113,v168,s[96:99],s91 offen offset:128
buffer_load_ushort v121,v168,s[96:99],s91 offen offset:136
buffer_load_ushort v129,v168,s[96:99],s91 offen offset:192
buffer_load_ushort v137,v168,s[96:99],s91 offen offset:200
s_addk_i32 s91,8192
buffer_load_ushort v114,v168,s[96:99],s91 offen offset:128
buffer_load_ushort v122,v168,s[96:99],s91 offen offset:136
buffer_load_ushort v130,v168,s[96:99],s91 offen offset:192
buffer_load_ushort v138,v168,s[96:99],s91 offen offset:200
s_addk_i32 s91,8192
buffer_load_ushort v115,v168,s[96:99],s91 offen offset:128
buffer_load_ushort v123,v168,s[96:99],s91 offen offset:136
buffer_load_ushort v131,v168,s[96:99],s91 offen offset:192
buffer_load_ushort v139,v168,s[96:99],s91 offen offset:200
s_addk_i32 s91,8192
buffer_load_ushort v116,v168,s[96:99],s91 offen offset:128
buffer_load_ushort v124,v168,s[96:99],s91 offen offset:136
buffer_load_ushort v132,v168,s[96:99],s91 offen offset:192
buffer_load_ushort v140,v168,s[96:99],s91 offen offset:200
s_addk_i32 s91,8192
buffer_load_ushort v117,v168,s[96:99],s91 offen offset:128
buffer_load_ushort v125,v168,s[96:99],s91 offen offset:136
buffer_load_ushort v133,v168,s[96:99],s91 offen offset:192
buffer_load_ushort v141,v168,s[96:99],s91 offen offset:200
s_addk_i32 s91,8192
buffer_load_ushort v118,v168,s[96:99],s91 offen offset:128
buffer_load_ushort v126,v168,s[96:99],s91 offen offset:136
buffer_load_ushort v134,v168,s[96:99],s91 offen offset:192
buffer_load_ushort v142,v168,s[96:99],s91 offen offset:200
s_addk_i32 s91,8192
buffer_load_ushort v119,v168,s[96:99],s91 offen offset:128
buffer_load_ushort v127,v168,s[96:99],s91 offen offset:136
buffer_load_ushort v135,v168,s[96:99],s91 offen offset:192
buffer_load_ushort v143,v168,s[96:99],s91 offen offset:200
s_addk_i32 s91,8192
s_waitcnt vmcnt(0)
v_lshl_or_b32 v112,v113,16,v112
v_lshl_or_b32 v113,v115,16,v114
v_lshl_or_b32 v114,v117,16,v116
v_lshl_or_b32 v115,v119,16,v118
v_lshl_or_b32 v120,v121,16,v120
v_lshl_or_b32 v121,v123,16,v122
v_lshl_or_b32 v122,v125,16,v124
v_lshl_or_b32 v123,v127,16,v126
v_lshl_or_b32 v128,v129,16,v128
v_lshl_or_b32 v129,v131,16,v130
v_lshl_or_b32 v130,v133,16,v132
v_lshl_or_b32 v131,v135,16,v134
v_lshl_or_b32 v136,v137,16,v136
v_lshl_or_b32 v137,v139,16,v138
v_lshl_or_b32 v138,v141,16,v140
v_lshl_or_b32 v139,v143,16,v142
s_nop 1
v_mfma_f32_16x16x32_bf16 v[80:83],v[112:115],v[160:163],v[80:83]
v_mfma_f32_16x16x32_bf16 v[84:87],v[120:123],v[160:163],v[84:87]
v_mfma_f32_16x16x32_bf16 v[88:91],v[128:131],v[160:163],v[88:91]
v_mfma_f32_16x16x32_bf16 v[92:95],v[136:139],v[160:163],v[92:95]
s_nop 7
s_add_u32 s90,s90,1
s_cmp_lg_u32 s90,20
s_cbranch_scc1 L_sq_%=
s_nop 7
v_mul_f32 v64,s61,v64
v_mul_f32 v65,s61,v65
v_mul_f32 v66,s61,v66
v_mul_f32 v67,s61,v67
v_mul_f32 v68,s61,v68
v_mul_f32 v69,s61,v69
v_mul_f32 v70,s61,v70
v_mul_f32 v71,s61,v71
v_mul_f32 v72,s61,v72
v_mul_f32 v73,s61,v73
v_mul_f32 v74,s61,v74
v_mul_f32 v75,s61,v75
v_mul_f32 v76,s61,v76
v_mul_f32 v77,s61,v77
v_mul_f32 v78,s61,v78
v_mul_f32 v79,s61,v79
v_mul_f32 v80,s61,v80
v_mul_f32 v81,s61,v81
v_mul_f32 v82,s61,v82
v_mul_f32 v83,s61,v83
v_mul_f32 v84,s61,v84
v_mul_f32 v85,s61,v85
v_mul_f32 v86,s61,v86
v_mul_f32 v87,s61,v87
v_mul_f32 v88,s61,v88
v_mul_f32 v89,s61,v89
v_mul_f32 v90,s61,v90
v_mul_f32 v91,s61,v91
v_mul_f32 v92,s61,v92
v_mul_f32 v93,s61,v93
v_mul_f32 v94,s61,v94
v_mul_f32 v95,s61,v95
v_cvt_pk_bf16_f32 v96,v64,v65
v_cvt_pk_bf16_f32 v97,v66,v67
v_cvt_pk_bf16_f32 v98,v68,v69
v_cvt_pk_bf16_f32 v99,v70,v71
v_cvt_pk_bf16_f32 v100,v72,v73
v_cvt_pk_bf16_f32 v101,v74,v75
v_cvt_pk_bf16_f32 v102,v76,v77
v_cvt_pk_bf16_f32 v103,v78,v79
v_cvt_pk_bf16_f32 v104,v80,v81
v_cvt_pk_bf16_f32 v105,v82,v83
v_cvt_pk_bf16_f32 v106,v84,v85
v_cvt_pk_bf16_f32 v107,v86,v87
v_cvt_pk_bf16_f32 v108,v88,v89
v_cvt_pk_bf16_f32 v109,v90,v91
v_cvt_pk_bf16_f32 v110,v92,v93
v_cvt_pk_bf16_f32 v111,v94,v95
v_mov_b32 v170,0xff800000
s_lshl_b32 s91,s88,18
s_mov_b32 s90,64
L_s1_%=:
buffer_load_dwordx4 v[112:115],v164,s[20:23],s91 offen
buffer_load_dwordx4 v[116:119],v164,s[20:23],s91 offen offset:64
buffer_load_dwordx4 v[120:123],v164,s[20:23],s91 offen offset:128
buffer_load_dwordx4 v[124:127],v164,s[20:23],s91 offen offset:192
s_waitcnt vmcnt(0)
v_mfma_f32_16x16x32_bf16 v[144:147],v[112:115],v[96:99],0
v_mfma_f32_16x16x32_bf16 v[144:147],v[116:119],v[100:103],v[144:147]
v_mfma_f32_16x16x32_bf16 v[144:147],v[120:123],v[104:107],v[144:147]
v_mfma_f32_16x16x32_bf16 v[144:147],v[124:127],v[108:111],v[144:147]
s_nop 7
v_max3_f32 v170,v170,v144,v145
v_max3_f32 v170,v170,v146,v147
s_addk_i32 s91,4096
s_sub_u32 s90,s90,1
s_cmp_lg_u32 s90,0
s_cbranch_scc1 L_s1_%=
v_mov_b32 v171,v170
s_nop 1
v_permlane32_swap_b32 v170,v171
v_max_f32 v170,v170,v171
v_mov_b32 v171,v170
s_nop 1
v_permlane16_swap_b32 v170,v171
v_max_f32 v170,v170,v171
v_sub_f32 v152,0,v170
v_mov_b32 v156,0
v_sub_f32 v153,0,v170
v_mov_b32 v157,0
v_sub_f32 v154,0,v170
v_mov_b32 v158,0
v_sub_f32 v155,0,v170
v_mov_b32 v159,0
v_mov_b32 v64,0
v_mov_b32 v65,0
v_mov_b32 v66,0
v_mov_b32 v67,0
v_mov_b32 v68,0
v_mov_b32 v69,0
v_mov_b32 v70,0
v_mov_b32 v71,0
v_mov_b32 v72,0
v_mov_b32 v73,0
v_mov_b32 v74,0
v_mov_b32 v75,0
v_mov_b32 v76,0
v_mov_b32 v77,0
v_mov_b32 v78,0
v_mov_b32 v79,0
v_mov_b32 v80,0
v_mov_b32 v81,0
v_mov_b32 v82,0
v_mov_b32 v83,0
v_mov_b32 v84,0
v_mov_b32 v85,0
v_mov_b32 v86,0
v_mov_b32 v87,0
v_mov_b32 v88,0
v_mov_b32 v89,0
v_mov_b32 v90,0
v_mov_b32 v91,0
v_mov_b32 v92,0
v_mov_b32 v93,0
v_mov_b32 v94,0
v_mov_b32 v95,0
s_lshl_b32 s91,s88,18
s_mov_b32 s90,32
v_add_u32 v172,4096,v164
L_s2_%=:
buffer_load_dwordx4 v[112:115],v164,s[20:23],s91 offen
buffer_load_dwordx4 v[116:119],v164,s[20:23],s91 offen offset:64
buffer_load_dwordx4 v[120:123],v164,s[20:23],s91 offen offset:128
buffer_load_dwordx4 v[124:127],v164,s[20:23],s91 offen offset:192
buffer_load_dwordx4 v[128:131],v172,s[20:23],s91 offen
buffer_load_dwordx4 v[132:135],v172,s[20:23],s91 offen offset:64
buffer_load_dwordx4 v[136:139],v172,s[20:23],s91 offen offset:128
buffer_load_dwordx4 v[140:143],v172,s[20:23],s91 offen offset:192
s_waitcnt vmcnt(0)
v_mfma_f32_16x16x32_bf16 v[144:147],v[112:115],v[96:99],v[152:155]
v_mfma_f32_16x16x32_bf16 v[144:147],v[116:119],v[100:103],v[144:147]
v_mfma_f32_16x16x32_bf16 v[144:147],v[120:123],v[104:107],v[144:147]
v_mfma_f32_16x16x32_bf16 v[144:147],v[124:127],v[108:111],v[144:147]
v_mfma_f32_16x16x32_bf16 v[148:151],v[128:131],v[96:99],v[152:155]
v_mfma_f32_16x16x32_bf16 v[148:151],v[132:135],v[100:103],v[148:151]
v_mfma_f32_16x16x32_bf16 v[148:151],v[136:139],v[104:107],v[148:151]
v_mfma_f32_16x16x32_bf16 v[148:151],v[140:143],v[108:111],v[148:151]
s_nop 7
v_exp_f32 v144,v144
v_exp_f32 v145,v145
v_exp_f32 v146,v146
v_exp_f32 v147,v147
v_exp_f32 v148,v148
v_exp_f32 v149,v149
v_exp_f32 v150,v150
v_exp_f32 v151,v151
s_nop 0
v_cvt_pk_bf16_f32 v144,v144,v145
v_cvt_pk_bf16_f32 v145,v146,v147
v_cvt_pk_bf16_f32 v146,v148,v149
v_cvt_pk_bf16_f32 v147,v150,v151
buffer_load_ushort v112,v165,s[24:27],s91 offen
buffer_load_ushort v113,v165,s[24:27],s91 offen offset:256
buffer_load_ushort v114,v165,s[24:27],s91 offen offset:512
buffer_load_ushort v115,v165,s[24:27],s91 offen offset:768
buffer_load_ushort v116,v166,s[24:27],s91 offen
buffer_load_ushort v117,v166,s[24:27],s91 offen offset:256
buffer_load_ushort v118,v166,s[24:27],s91 offen offset:512
buffer_load_ushort v119,v166,s[24:27],s91 offen offset:768
buffer_load_ushort v120,v165,s[24:27],s91 offen offset:32
buffer_load_ushort v121,v165,s[24:27],s91 offen offset:288
buffer_load_ushort v122,v165,s[24:27],s91 offen offset:544
buffer_load_ushort v123,v165,s[24:27],s91 offen offset:800
buffer_load_ushort v124,v166,s[24:27],s91 offen offset:32
buffer_load_ushort v125,v166,s[24:27],s91 offen offset:288
buffer_load_ushort v126,v166,s[24:27],s91 offen offset:544
buffer_load_ushort v127,v166,s[24:27],s91 offen offset:800
buffer_load_ushort v128,v165,s[24:27],s91 offen offset:64
buffer_load_ushort v129,v165,s[24:27],s91 offen offset:320
buffer_load_ushort v130,v165,s[24:27],s91 offen offset:576
buffer_load_ushort v131,v165,s[24:27],s91 offen offset:832
buffer_load_ushort v132,v166,s[24:27],s91 offen offset:64
buffer_load_ushort v133,v166,s[24:27],s91 offen offset:320
buffer_load_ushort v134,v166,s[24:27],s91 offen offset:576
buffer_load_ushort v135,v166,s[24:27],s91 offen offset:832
buffer_load_ushort v136,v165,s[24:27],s91 offen offset:96
buffer_load_ushort v137,v165,s[24:27],s91 offen offset:352
buffer_load_ushort v138,v165,s[24:27],s91 offen offset:608
buffer_load_ushort v139,v165,s[24:27],s91 offen offset:864
buffer_load_ushort v140,v166,s[24:27],s91 offen offset:96
buffer_load_ushort v141,v166,s[24:27],s91 offen offset:352
buffer_load_ushort v142,v166,s[24:27],s91 offen offset:608
buffer_load_ushort v143,v166,s[24:27],s91 offen offset:864
s_waitcnt vmcnt(0)
v_lshl_or_b32 v112,v113,16,v112
v_lshl_or_b32 v113,v115,16,v114
v_lshl_or_b32 v114,v117,16,v116
v_lshl_or_b32 v115,v119,16,v118
v_lshl_or_b32 v120,v121,16,v120
v_lshl_or_b32 v121,v123,16,v122
v_lshl_or_b32 v122,v125,16,v124
v_lshl_or_b32 v123,v127,16,v126
v_lshl_or_b32 v128,v129,16,v128
v_lshl_or_b32 v129,v131,16,v130
v_lshl_or_b32 v130,v133,16,v132
v_lshl_or_b32 v131,v135,16,v134
v_lshl_or_b32 v136,v137,16,v136
v_lshl_or_b32 v137,v139,16,v138
v_lshl_or_b32 v138,v141,16,v140
v_lshl_or_b32 v139,v143,16,v142
s_nop 1
v_mfma_f32_16x16x32_bf16 v[64:67],v[112:115],v[144:147],v[64:67]
v_mfma_f32_16x16x32_bf16 v[68:71],v[120:123],v[144:147],v[68:71]
v_mfma_f32_16x16x32_bf16 v[72:75],v[128:131],v[144:147],v[72:75]
v_mfma_f32_16x16x32_bf16 v[76:79],v[136:139],v[144:147],v[76:79]
s_nop 7
buffer_load_ushort v112,v165,s[24:27],s91 offen offset:128
buffer_load_ushort v113,v165,s[24:27],s91 offen offset:384
buffer_load_ushort v114,v165,s[24:27],s91 offen offset:640
buffer_load_ushort v115,v165,s[24:27],s91 offen offset:896
buffer_load_ushort v116,v166,s[24:27],s91 offen offset:128
buffer_load_ushort v117,v166,s[24:27],s91 offen offset:384
buffer_load_ushort v118,v166,s[24:27],s91 offen offset:640
buffer_load_ushort v119,v166,s[24:27],s91 offen offset:896
buffer_load_ushort v120,v165,s[24:27],s91 offen offset:160
buffer_load_ushort v121,v165,s[24:27],s91 offen offset:416
buffer_load_ushort v122,v165,s[24:27],s91 offen offset:672
buffer_load_ushort v123,v165,s[24:27],s91 offen offset:928
buffer_load_ushort v124,v166,s[24:27],s91 offen offset:160
buffer_load_ushort v125,v166,s[24:27],s91 offen offset:416
buffer_load_ushort v126,v166,s[24:27],s91 offen offset:672
buffer_load_ushort v127,v166,s[24:27],s91 offen offset:928
buffer_load_ushort v128,v165,s[24:27],s91 offen offset:192
buffer_load_ushort v129,v165,s[24:27],s91 offen offset:448
buffer_load_ushort v130,v165,s[24:27],s91 offen offset:704
buffer_load_ushort v131,v165,s[24:27],s91 offen offset:960
buffer_load_ushort v132,v166,s[24:27],s91 offen offset:192
buffer_load_ushort v133,v166,s[24:27],s91 offen offset:448
buffer_load_ushort v134,v166,s[24:27],s91 offen offset:704
buffer_load_ushort v135,v166,s[24:27],s91 offen offset:960
buffer_load_ushort v136,v165,s[24:27],s91 offen offset:224
buffer_load_ushort v137,v165,s[24:27],s91 offen offset:480
buffer_load_ushort v138,v165,s[24:27],s91 offen offset:736
buffer_load_ushort v139,v165,s[24:27],s91 offen offset:992
buffer_load_ushort v140,v166,s[24:27],s91 offen offset:224
buffer_load_ushort v141,v166,s[24:27],s91 offen offset:480
buffer_load_ushort v142,v166,s[24:27],s91 offen offset:736
buffer_load_ushort v143,v166,s[24:27],s91 offen offset:992
s_waitcnt vmcnt(0)
v_lshl_or_b32 v112,v113,16,v112
)A"
R"A(v_lshl_or_b32 v113,v115,16,v114
v_lshl_or_b32 v114,v117,16,v116
v_lshl_or_b32 v115,v119,16,v118
v_lshl_or_b32 v120,v121,16,v120
v_lshl_or_b32 v121,v123,16,v122
v_lshl_or_b32 v122,v125,16,v124
v_lshl_or_b32 v123,v127,16,v126
v_lshl_or_b32 v128,v129,16,v128
v_lshl_or_b32 v129,v131,16,v130
v_lshl_or_b32 v130,v133,16,v132
v_lshl_or_b32 v131,v135,16,v134
v_lshl_or_b32 v136,v137,16,v136
v_lshl_or_b32 v137,v139,16,v138
v_lshl_or_b32 v138,v141,16,v140
v_lshl_or_b32 v139,v143,16,v142
s_nop 1
v_mfma_f32_16x16x32_bf16 v[80:83],v[112:115],v[144:147],v[80:83]
v_mfma_f32_16x16x32_bf16 v[84:87],v[120:123],v[144:147],v[84:87]
v_mfma_f32_16x16x32_bf16 v[88:91],v[128:131],v[144:147],v[88:91]
v_mfma_f32_16x16x32_bf16 v[92:95],v[136:139],v[144:147],v[92:95]
s_nop 7
v_mfma_f32_16x16x32_bf16 v[156:159],v[28:31],v[144:147],v[156:159]
s_nop 7
s_addk_i32 s91,8192
s_sub_u32 s90,s90,1
s_cmp_lg_u32 s90,0
s_cbranch_scc1 L_s2_%=
v_rcp_f32 v170,v156
s_nop 0
v_mul_f32 v64,v170,v64
v_mul_f32 v65,v170,v65
v_mul_f32 v66,v170,v66
v_mul_f32 v67,v170,v67
v_mul_f32 v68,v170,v68
v_mul_f32 v69,v170,v69
v_mul_f32 v70,v170,v70
v_mul_f32 v71,v170,v71
v_mul_f32 v72,v170,v72
v_mul_f32 v73,v170,v73
v_mul_f32 v74,v170,v74
v_mul_f32 v75,v170,v75
v_mul_f32 v76,v170,v76
v_mul_f32 v77,v170,v77
v_mul_f32 v78,v170,v78
v_mul_f32 v79,v170,v79
v_mul_f32 v80,v170,v80
v_mul_f32 v81,v170,v81
v_mul_f32 v82,v170,v82
v_mul_f32 v83,v170,v83
v_mul_f32 v84,v170,v84
v_mul_f32 v85,v170,v85
v_mul_f32 v86,v170,v86
v_mul_f32 v87,v170,v87
v_mul_f32 v88,v170,v88
v_mul_f32 v89,v170,v89
v_mul_f32 v90,v170,v90
v_mul_f32 v91,v170,v91
v_mul_f32 v92,v170,v92
v_mul_f32 v93,v170,v93
v_mul_f32 v94,v170,v94
v_mul_f32 v95,v170,v95
v_cvt_pk_bf16_f32 v96,v64,v65
v_cvt_pk_bf16_f32 v97,v66,v67
v_cvt_pk_bf16_f32 v98,v68,v69
v_cvt_pk_bf16_f32 v99,v70,v71
v_cvt_pk_bf16_f32 v100,v72,v73
v_cvt_pk_bf16_f32 v101,v74,v75
v_cvt_pk_bf16_f32 v102,v76,v77
v_cvt_pk_bf16_f32 v103,v78,v79
v_cvt_pk_bf16_f32 v104,v80,v81
v_cvt_pk_bf16_f32 v105,v82,v83
v_cvt_pk_bf16_f32 v106,v84,v85
v_cvt_pk_bf16_f32 v107,v86,v87
v_cvt_pk_bf16_f32 v108,v88,v89
v_cvt_pk_bf16_f32 v109,v90,v91
v_cvt_pk_bf16_f32 v110,v92,v93
v_cvt_pk_bf16_f32 v111,v94,v95
buffer_store_dwordx4 v[96:99],v169,s[36:39],s55 offen sc1
buffer_store_dwordx4 v[100:103],v169,s[36:39],s55 offen offset:1024 sc1
buffer_store_dwordx4 v[104:107],v169,s[36:39],s55 offen offset:2048 sc1
buffer_store_dwordx4 v[108:111],v169,s[36:39],s55 offen offset:3072 sc1
s_waitcnt vmcnt(0)
s_add_u32 s89,s89,1
s_cmp_lg_u32 s89,4
s_cbranch_scc1 L_sjb_%=
s_mov_b32 s98,s88
v_readlane_b32 s88,v173,0
v_readlane_b32 s89,v173,1
v_readlane_b32 s90,v173,2
v_readlane_b32 s91,v173,3
v_readlane_b32 s92,v173,4
v_readlane_b32 s93,v173,5
v_readlane_b32 s94,v173,6
v_readlane_b32 s95,v173,7
s_nop 4
s_mov_b32 s96,0
buffer_load_dwordx4 v[64:67],v6,s[56:59],s96 offen sc1
buffer_load_dwordx4 v[68:71],v6,s[56:59],s96 offen offset:1024 sc1
buffer_load_dwordx4 v[72:75],v6,s[56:59],s96 offen offset:2048 sc1
buffer_load_dwordx4 v[76:79],v6,s[56:59],s96 offen offset:3072 sc1
s_movk_i32 s96,4096
buffer_load_dwordx4 v[80:83],v6,s[56:59],s96 offen sc1
buffer_load_dwordx4 v[84:87],v6,s[56:59],s96 offen offset:1024 sc1
buffer_load_dwordx4 v[88:91],v6,s[56:59],s96 offen offset:2048 sc1
buffer_load_dwordx4 v[92:95],v6,s[56:59],s96 offen offset:3072 sc1
s_movk_i32 s96,8192
buffer_load_dwordx4 v[96:99],v6,s[56:59],s96 offen sc1
buffer_load_dwordx4 v[100:103],v6,s[56:59],s96 offen offset:1024 sc1
buffer_load_dwordx4 v[104:107],v6,s[56:59],s96 offen offset:2048 sc1
buffer_load_dwordx4 v[108:111],v6,s[56:59],s96 offen offset:3072 sc1
s_movk_i32 s96,12288
buffer_load_dwordx4 v[112:115],v6,s[56:59],s96 offen sc1
buffer_load_dwordx4 v[116:119],v6,s[56:59],s96 offen offset:1024 sc1
buffer_load_dwordx4 v[120:123],v6,s[56:59],s96 offen offset:2048 sc1
buffer_load_dwordx4 v[124:127],v6,s[56:59],s96 offen offset:3072 sc1
s_movk_i32 s96,16384
buffer_load_dwordx4 v[128:131],v6,s[56:59],s96 offen sc1
buffer_load_dwordx4 v[132:135],v6,s[56:59],s96 offen offset:1024 sc1
buffer_load_dwordx4 v[136:139],v6,s[56:59],s96 offen offset:2048 sc1
buffer_load_dwordx4 v[140:143],v6,s[56:59],s96 offen offset:3072 sc1
s_movk_i32 s96,20480
buffer_load_dwordx4 v[144:147],v6,s[56:59],s96 offen sc1
buffer_load_dwordx4 v[148:151],v6,s[56:59],s96 offen offset:1024 sc1
buffer_load_dwordx4 v[152:155],v6,s[56:59],s96 offen offset:2048 sc1
buffer_load_dwordx4 v[156:159],v6,s[56:59],s96 offen offset:3072 sc1
s_movk_i32 s96,24576
buffer_load_dwordx4 v[160:163],v6,s[56:59],s96 offen sc1
buffer_load_dwordx4 v[164:167],v6,s[56:59],s96 offen offset:1024 sc1
buffer_load_dwordx4 v[168:171],v6,s[56:59],s96 offen offset:2048 sc1
buffer_load_dwordx4 v[172:175],v6,s[56:59],s96 offen offset:3072 sc1
s_movk_i32 s96,28672
buffer_load_dwordx4 v[176:179],v6,s[56:59],s96 offen sc1
buffer_load_dwordx4 v[180:183],v6,s[56:59],s96 offen offset:1024 sc1
buffer_load_dwordx4 v[184:187],v6,s[56:59],s96 offen offset:2048 sc1
buffer_load_dwordx4 v[188:191],v6,s[56:59],s96 offen offset:3072 sc1
s_waitcnt vmcnt(0)
s_cmp_lg_u32 s98,0
s_cbranch_scc1 L_sr1_%=
buffer_load_dwordx4 v[64:67],v6,s[36:39],s84 offen sc1
buffer_load_dwordx4 v[68:71],v6,s[36:39],s84 offen offset:1024 sc1
buffer_load_dwordx4 v[72:75],v6,s[36:39],s84 offen offset:2048 sc1
buffer_load_dwordx4 v[76:79],v6,s[36:39],s84 offen offset:3072 sc1
buffer_load_dwordx4 v[80:83],v6,s[36:39],s85 offen sc1
buffer_load_dwordx4 v[84:87],v6,s[36:39],s85 offen offset:1024 sc1
buffer_load_dwordx4 v[88:91],v6,s[36:39],s85 offen offset:2048 sc1
buffer_load_dwordx4 v[92:95],v6,s[36:39],s85 offen offset:3072 sc1
buffer_load_dwordx4 v[96:99],v6,s[36:39],s86 offen sc1
buffer_load_dwordx4 v[100:103],v6,s[36:39],s86 offen offset:1024 sc1
buffer_load_dwordx4 v[104:107],v6,s[36:39],s86 offen offset:2048 sc1
buffer_load_dwordx4 v[108:111],v6,s[36:39],s86 offen offset:3072 sc1
buffer_load_dwordx4 v[112:115],v6,s[36:39],s87 offen sc1
buffer_load_dwordx4 v[116:119],v6,s[36:39],s87 offen offset:1024 sc1
buffer_load_dwordx4 v[120:123],v6,s[36:39],s87 offen offset:2048 sc1
buffer_load_dwordx4 v[124:127],v6,s[36:39],s87 offen offset:3072 sc1
s_waitcnt vmcnt(0)
s_nop 7
s_branch L_sret0_%=
L_sr1_%=:
buffer_load_dwordx4 v[136:139],v6,s[36:39],s84 offen sc1
buffer_load_dwordx4 v[140:143],v6,s[36:39],s84 offen offset:1024 sc1
buffer_load_dwordx4 v[152:155],v6,s[36:39],s84 offen offset:2048 sc1
buffer_load_dwordx4 v[156:159],v6,s[36:39],s84 offen offset:3072 sc1
buffer_load_dwordx4 v[168:171],v6,s[36:39],s85 offen sc1
buffer_load_dwordx4 v[172:175],v6,s[36:39],s85 offen offset:1024 sc1
buffer_load_dwordx4 v[184:187],v6,s[36:39],s85 offen offset:2048 sc1
buffer_load_dwordx4 v[188:191],v6,s[36:39],s85 offen offset:3072 sc1
buffer_load_dwordx4 v[200:203],v6,s[36:39],s86 offen sc1
buffer_load_dwordx4 v[204:207],v6,s[36:39],s86 offen offset:1024 sc1
buffer_load_dwordx4 v[216:219],v6,s[36:39],s86 offen offset:2048 sc1
buffer_load_dwordx4 v[220:223],v6,s[36:39],s86 offen offset:3072 sc1
buffer_load_dwordx4 v[232:235],v6,s[36:39],s87 offen sc1
buffer_load_dwordx4 v[236:239],v6,s[36:39],s87 offen offset:1024 sc1
buffer_load_dwordx4 v[248:251],v6,s[36:39],s87 offen offset:2048 sc1
buffer_load_dwordx4 v[252:255],v6,s[36:39],s87 offen offset:3072 sc1
s_waitcnt vmcnt(0)
s_nop 7
s_branch L_sret1_%=
L_end_%=:
)A"
    :: "s"(karg), "s"(blk), "s"(wid)
    : "v0", "v1", "v2", "v3", "v4", "v5", "v6", "v7", "v8", "v9", "v10", "v11", "v12", "v13", "v14", "v15", "v16", "v17", "v18", "v19", "v20", "v21", "v22", "v23", "v24", "v25", "v26", "v27", "v28", "v29", "v30", "v31", "v32", "v33", "v34", "v35", "v36", "v37", "v38", "v39", "v40", "v41", "v42", "v43", "v44", "v45", "v46", "v47", "v48", "v49", "v50", "v51", "v52", "v53", "v54", "v55", "v56", "v57", "v58", "v59", "v60", "v61", "v62", "v63", "v64", "v65", "v66", "v67", "v68", "v69", "v70", "v71", "v72", "v73", "v74", "v75", "v76", "v77", "v78", "v79", "v80", "v81", "v82", "v83", "v84", "v85", "v86", "v87", "v88", "v89", "v90", "v91", "v92", "v93", "v94", "v95", "v96", "v97", "v98", "v99", "v100", "v101", "v102", "v103", "v104", "v105", "v106", "v107", "v108", "v109", "v110", "v111", "v112", "v113", "v114", "v115", "v116", "v117", "v118", "v119", "v120", "v121", "v122", "v123", "v124", "v125", "v126", "v127", "v128", "v129", "v130", "v131", "v132", "v133", "v134", "v135", "v136", "v137", "v138", "v139", "v140", "v141", "v142", "v143", "v144", "v145", "v146", "v147", "v148", "v149", "v150", "v151", "v152", "v153", "v154", "v155", "v156", "v157", "v158", "v159", "v160", "v161", "v162", "v163", "v164", "v165", "v166", "v167", "v168", "v169", "v170", "v171", "v172", "v173", "v174", "v175", "v176", "v177", "v178", "v179", "v180", "v181", "v182", "v183", "v184", "v185", "v186", "v187", "v188", "v189", "v190", "v191", "v192", "v193", "v194", "v195", "v196", "v197", "v198", "v199", "v200", "v201", "v202", "v203", "v204", "v205", "v206", "v207", "v208", "v209", "v210", "v211", "v212", "v213", "v214", "v215", "v216", "v217", "v218", "v219", "v220", "v221", "v222", "v223", "v224", "v225", "v226", "v227", "v228", "v229", "v230", "v231", "v232", "v233", "v234", "v235", "v236", "v237", "v238", "v239", "v240", "v241", "v242", "v243", "v244", "v245", "v246", "v247", "v248", "v249", "v250", "v251", "v252", "v253", "v254", "v255", "a0", "a1", "a2", "a3", "a4", "a5", "a6", "a7", "a8", "a9", "a10", "a11", "a12", "a13", "a14", "a15", "a16", "a17", "a18", "a19", "a20", "a21", "a22", "a23", "a24", "a25", "a26", "a27", "a28", "a29", "a30", "a31", "a32", "a33", "a34", "a35", "a36", "a37", "a38", "a39", "a40", "a41", "a42", "a43", "a44", "a45", "a46", "a47", "a48", "a49", "a50", "a51", "a52", "a53", "a54", "a55", "a56", "a57", "a58", "a59", "a60", "a61", "a62", "a63", "a64", "a65", "a66", "a67", "a68", "a69", "a70", "a71", "a72", "a73", "a74", "a75", "a76", "a77", "a78", "a79", "a80", "a81", "a82", "a83", "a84", "a85", "a86", "a87", "a88", "a89", "a90", "a91", "a92", "a93", "a94", "a95", "a96", "a97", "a98", "a99", "a100", "a101", "a102", "a103", "a104", "a105", "a106", "a107", "a108", "a109", "a110", "a111", "a112", "a113", "a114", "a115", "a116", "a117", "a118", "a119", "a120", "a121", "a122", "a123", "a124", "a125", "a126", "a127", "a128", "a129", "a130", "a131", "a132", "a133", "a134", "a135", "a136", "a137", "a138", "a139", "a140", "a141", "a142", "a143", "a144", "a145", "a146", "a147", "a148", "a149", "a150", "a151", "a152", "a153", "a154", "a155", "a156", "a157", "a158", "a159", "a160", "a161", "a162", "a163", "a164", "a165", "a166", "a167", "a168", "a169", "a170", "a171", "a172", "a173", "a174", "a175", "a176", "a177", "a178", "a179", "a180", "a181", "a182", "a183", "a184", "a185", "a186", "a187", "a188", "a189", "a190", "a191", "a192", "a193", "a194", "a195", "a196", "a197", "a198", "a199", "a200", "a201", "a202", "a203", "a204", "a205", "a206", "a207", "a208", "a209", "a210", "a211", "a212", "a213", "a214", "a215", "a216", "a217", "a218", "a219", "a220", "a221", "a222", "a223", "a224", "a225", "a226", "a227", "a228", "a229", "a230", "a231", "a232", "a233", "a234", "a235", "a236", "a237", "a238", "a239", "a240", "a241", "a242", "a243", "a244", "a245", "a246", "a247", "a248", "a249", "a250", "a251", "a252", "a253", "a254", "a255", "s4", "s5", "s6", "s7", "s8", "s9", "s10", "s11", "s12", "s13", "s14", "s15", "s16", "s17", "s18", "s19", "s20", "s21", "s22", "s23", "s24", "s25", "s26", "s27", "s28", "s29", "s30", "s31", "s32", "s33", "s34", "s35", "s36", "s37", "s38", "s39", "s40", "s41", "s42", "s43", "s44", "s45", "s46", "s47", "s48", "s49", "s50", "s51", "s52", "s53", "s54", "s55", "s56", "s57", "s58", "s59", "s60", "s61", "s62", "s63", "s64", "s65", "s66", "s67", "s68", "s69", "s70", "s71", "s72", "s73", "s74", "s75", "s76", "s77", "s78", "s79", "s80", "s81", "s82", "s83", "s84", "s85", "s86", "s87", "s88", "s89", "s90", "s91", "s92", "s93", "s94", "s95", "s96", "s97", "s98", "s99", "s100", "s101", "vcc", "scc", "memory");
#endif
}

extern "C" void kernel_launch(void* const* d_in, const int* in_sizes, int n_in,
                              void* d_out, int out_size, void* d_ws, size_t ws_size,
                              hipStream_t stream) {
  (void)hipFuncSetAttribute((const void*)qattn_kernel, hipFuncAttributeMaxDynamicSharedMemorySize, LDS_ATT);
  qattn_kernel<<<256, 256, LDS_ATT, stream>>>(d_in[0], d_in[1], d_in[2], d_in[3], d_ws, d_in[4], d_out);
}
```

```cpp
#include <hip/hip_runtime.h>
#include <hip/hip_bf16.h>

constexpr int LDS_ATT = 163840;

__global__ __launch_bounds__(256, 1) void qattn_kernel(const void* __restrict__ x, const void* __restrict__ kc, const void* __restrict__ vc,
                                                       const void* __restrict__ wq, void* __restrict__ ws, const void* __restrict__ wo,
                                                       void* __restrict__ out) {
#if defined(__HIP_DEVICE_COMPILE__)
  const auto karg = __builtin_amdgcn_kernarg_segment_ptr();
  const unsigned blk = blockIdx.x;
  const unsigned wid = __builtin_amdgcn_readfirstlane(threadIdx.x >> 6);
  asm volatile(
R"A(s_memtime s[72:73]
s_memrealtime s[74:75]
s_load_dwordx8 s[4:11],%0,0x0
s_load_dwordx4 s[12:15],%0,0x20
s_load_dwordx2 s[16:17],%0,0x30
s_mov_b32 s40,%2
s_mov_b32 s43,%1
s_lshr_b32 s45,s43,3
v_mbcnt_lo_u32_b32 v12,-1,0
v_mbcnt_hi_u32_b32 v12,-1,v12
s_getpc_b64 s[88:89]
L_pc_%=:
s_and_b32 s88,s88,0xffffff80
s_and_b32 s89,s89,0xffff
s_mov_b32 s90,L_fast_end_%=-L_pc_%=
s_mov_b32 s91,0x20000
s_lshl_b32 s44,s45,2
s_add_u32 s44,s44,s40
s_mulk_i32 s44,896
v_lshlrev_b32 v67,7,v12
s_mov_b64 exec,127
buffer_load_dword v19,v67,s[88:91],s44 offen
s_mov_b64 exec,-1
s_and_b32 s70,s43,3
s_and_b32 s71,s45,3
s_lshl_b32 s42,s70,2
s_add_u32 s42,s42,s71
s_and_b32 s41,s43,4
s_lshl_b32 s41,s41,1
s_lshr_b32 s46,s45,2
s_add_u32 s41,s41,s46
s_lshl_b32 s56,s42,9
s_mov_b32 s57,0
s_lshl_b32 s59,s40,13
s_add_u32 s58,s59,65536
v_and_b32 v64,3,v12
v_bfe_u32 v65,v12,2,2
v_lshrrev_b32 v66,4,v12
s_lshl_b32 s44,s40,17
v_lshlrev_b32 v75,13,v65
v_lshl_add_u32 v75,v66,6,v75
v_lshl_add_u32 v75,v64,4,v75
v_add_u32 v75,s44,v75
v_add_u32 v56,0,v75
v_add_u32 v57,256,v75
v_add_u32 v58,32768,v75
v_add_u32 v59,33024,v75
v_add_u32 v60,65536,v75
v_add_u32 v61,65792,v75
v_add_u32 v62,98304,v75
v_add_u32 v63,98560,v75
s_waitcnt lgkmcnt(0)
s_mov_b64 s[32:33],s[10:11]
s_and_b32 s33,s33,0xffff
s_mov_b32 s34,5242880
s_mov_b32 s35,0x20000
s_add_u32 m0,s58,1024
s_nop 0
buffer_load_dwordx4 v57,s[32:35],s56 offen lds
s_add_u32 m0,s58,3072
s_nop 0
buffer_load_dwordx4 v59,s[32:35],s56 offen lds
s_add_u32 m0,s58,5120
s_nop 0
buffer_load_dwordx4 v61,s[32:35],s56 offen lds
s_add_u32 m0,s58,7168
s_nop 0
buffer_load_dwordx4 v63,s[32:35],s56 offen lds
v_lshrrev_b32 v75,3,v12
s_lshl_b32 s44,s40,6
v_add_u32 v75,s44,v75
v_mul_u32_u24 v75,1280,v75
v_and_b32 v76,7,v12
s_add_u32 s84,s70,1
s_and_b32 s84,s84,3
s_mulk_i32 s84,20480
s_addk_i32 s84,4096
s_add_u32 s85,s70,2
s_and_b32 s85,s85,3
s_mulk_i32 s85,20480
s_addk_i32 s85,2048
s_add_u32 s86,s70,3
s_and_b32 s86,s86,3
s_mulk_i32 s86,20480
s_addk_i32 s86,4096
s_add_u32 s87,s70,4
s_and_b32 s87,s87,3
s_mulk_i32 s87,20480
s_addk_i32 s87,2048
v_or_b32 v77,0,v66
v_xor_b32 v77,v77,v76
v_lshl_add_u32 v77,v77,4,v75
v_add_u32 v48,s84,v77
v_add_u32 v50,s85,v77
v_add_u32 v52,s86,v77
v_add_u32 v54,s87,v77
v_or_b32 v77,4,v66
v_xor_b32 v77,v77,v76
v_lshl_add_u32 v77,v77,4,v75
v_add_u32 v77,9216,v77
v_add_u32 v49,s84,v77
v_add_u32 v51,s85,v77
v_add_u32 v53,s86,v77
v_add_u32 v55,s87,v77
s_mul_i32 s44,s41,307200
s_mov_b64 s[28:29],s[4:5]
s_add_u32 s28,s28,s44
s_addc_u32 s29,s29,0
s_sub_u32 s28,s28,4096
s_subb_u32 s29,s29,0
s_and_b32 s29,s29,0xffff
s_mov_b32 s30,311296
s_mov_b32 s31,0x20000
s_add_u32 m0,s59,0
s_nop 0
buffer_load_dwordx4 v48,s[28:31],s57 offen lds
buffer_load_dwordx4 v49,s[28:31],s57 offen offset:1024 lds
buffer_load_dwordx4 v50,s[28:31],s57 offen offset:2048 lds
buffer_load_dwordx4 v51,s[28:31],s57 offen offset:3072 lds
s_addk_i32 m0,4096
s_nop 0
buffer_load_dwordx4 v52,s[28:31],s57 offen lds
buffer_load_dwordx4 v53,s[28:31],s57 offen offset:1024 lds
buffer_load_dwordx4 v54,s[28:31],s57 offen offset:2048 lds
buffer_load_dwordx4 v55,s[28:31],s57 offen offset:3072 lds
s_addk_i32 s57,128
s_xor_b32 s59,s59,131072
s_add_u32 m0,s58,0
s_nop 0
buffer_load_dwordx4 v56,s[32:35],s56 offen lds
s_add_u32 m0,s58,2048
s_nop 0
buffer_load_dwordx4 v58,s[32:35],s56 offen lds
s_add_u32 m0,s58,4096
s_nop 0
buffer_load_dwordx4 v60,s[32:35],s56 offen lds
s_add_u32 m0,s58,6144
s_nop 0
buffer_load_dwordx4 v62,s[32:35],s56 offen lds
s_add_u32 s56,s56,524288
s_add_u32 s58,s58,32768
s_cmp_ge_u32 s58,131072
s_cselect_b32 s62,98304,0
s_sub_u32 s58,s58,s62
s_add_u32 s12,s12,4194303
s_addc_u32 s13,s13,0
s_and_b32 s12,s12,0xffc00000
s_lshl_b32 s55,s40,15
s_lshl_b32 s84,s41,2
s_add_u32 s84,s84,s70
s_lshl_b32 s84,s84,7
s_lshl_b32 s85,s71,2
s_add_u32 s84,s84,s85
s_add_u32 s84,s84,s40
s_addk_i32 s84,112
s_lshl_b32 s84,s84,14
s_add_u32 s84,s84,s12
s_addc_u32 s85,s13,0
s_mov_b32 s87,0x13572468
s_mov_b32 s79,0
s_load_dword s78,s[84:85],0x1000 glc
s_xor_b32 s76,s84,2097152
s_mov_b32 s77,s85
s_load_dword s78,s[76:77],0x0 glc
s_load_dword s78,s[76:77],0x2000 glc
s_load_dword s78,s[76:77],0x100 glc
s_load_dword s78,s[76:77],0x2100 glc
s_load_dword s78,s[76:77],0x200 glc
s_load_dword s78,s[76:77],0x2200 glc
s_add_u32 m0,s58,0
s_nop 0
buffer_load_dwordx4 v56,s[32:35],s56 offen lds
s_addk_i32 m0,1024
s_nop 0
buffer_load_dwordx4 v57,s[32:35],s56 offen lds
s_addk_i32 m0,1024
s_nop 0
buffer_load_dwordx4 v58,s[32:35],s56 offen lds
s_addk_i32 m0,1024
s_nop 0
buffer_load_dwordx4 v59,s[32:35],s56 offen lds
s_addk_i32 m0,1024
s_nop 0
buffer_load_dwordx4 v60,s[32:35],s56 offen lds
s_addk_i32 m0,1024
s_nop 0
buffer_load_dwordx4 v61,s[32:35],s56 offen lds
s_addk_i32 m0,1024
s_nop 0
buffer_load_dwordx4 v62,s[32:35],s56 offen lds
s_addk_i32 m0,1024
s_nop 0
buffer_load_dwordx4 v63,s[32:35],s56 offen lds
s_add_u32 s56,s56,524288
s_add_u32 s58,s58,32768
s_cmp_ge_u32 s58,131072
s_cselect_b32 s62,98304,0
s_sub_u32 s58,s58,s62
s_mov_b32 s60,65536
v_lshlrev_b32 v6,4,v12
v_lshlrev_b32 v72,12,v66
v_lshl_add_u32 v72,v65,6,v72
v_lshl_add_u32 v23,v64,4,v72
v_add_u32 v22,65536,v23
v_and_b32 v74,15,v12
v_lshrrev_b32 v76,1,v74
v_lshlrev_b32 v75,7,v74
s_lshl_b32 s44,s40,13
v_add_u32 v75,s44,v75
v_or_b32 v77,0,v66
v_xor_b32 v77,v77,v76
v_lshl_add_u32 v20,v77,4,v75
v_or_b32 v77,4,v66
v_xor_b32 v77,v77,v76
v_lshl_add_u32 v21,v77,4,v75
s_waitcnt vmcnt(12)
s_nop 0
s_memtime s[64:65]
s_memrealtime s[66:67]
s_bfe_u32 s68,%1,327683
s_waitcnt lgkmcnt(0)
s_sub_u32 s64,s64,s72
s_sub_u32 s66,s66,s74
s_mul_i32 s64,s64,10
s_mulk_i32 s66,224
s_cmp_lt_u32 s64,s66
s_cselect_b32 s95,64,0
s_mov_b32 %2,s95
s_cbranch_scc1 L_se1_%=
L_sg1_%=:
s_cmp_eq_u32 s68,0
s_cbranch_scc1 L_se1_%=
s_sleep 2
s_sub_u32 s68,s68,1
s_branch L_sg1_%=
L_se1_%=:
s_barrier
ds_read_b128 a[192:195],v20
ds_read_b64_tr_b16 a[128:129],v22 offset:1024
ds_read_b64_tr_b16 a[130:131],v22 offset:3072
ds_read_b128 a[196:199],v21
ds_read_b64_tr_b16 a[132:133],v22 offset:17408
ds_read_b64_tr_b16 a[134:135],v22 offset:19456
ds_read_b128 a[204:207],v21 offset:2048
ds_read_b128 a[200:203],v20 offset:2048
ds_read_b128 a[208:211],v20 offset:4096
ds_read_b128 a[212:215],v21 offset:4096
ds_read_b128 a[220:223],v21 offset:6144
ds_read_b128 a[216:219],v20 offset:6144
ds_read_b64_tr_b16 a[136:137],v22 offset:1032
ds_read_b64_tr_b16 a[138:139],v22 offset:3080
s_waitcnt lgkmcnt(11)
v_mfma_f32_16x16x32_bf16 v[128:131],a[128:131],a[192:195],0
s_add_u32 m0,s59,0
ds_read_b64_tr_b16 a[140:141],v22 offset:17416
ds_read_b64_tr_b16 a[142:143],v22 offset:19464
s_waitcnt lgkmcnt(10)
v_mfma_f32_16x16x32_bf16 v[128:131],a[132:135],a[196:199],v[128:131]
s_waitcnt lgkmcnt(9)
v_mfma_f32_16x16x32_bf16 v[136:139],a[132:135],a[204:207],0
s_waitcnt lgkmcnt(8)
v_mfma_f32_16x16x32_bf16 v[136:139],a[128:131],a[200:203],v[136:139]
s_waitcnt lgkmcnt(7)
v_mfma_f32_16x16x32_bf16 v[144:147],a[128:131],a[208:211],0
s_waitcnt lgkmcnt(6)
buffer_load_dwordx4 v48,s[28:31],s57 offen lds
v_mfma_f32_16x16x32_bf16 v[144:147],a[132:135],a[212:215],v[144:147]
s_waitcnt lgkmcnt(5)
v_mfma_f32_16x16x32_bf16 v[152:155],a[132:135],a[220:223],0
s_waitcnt lgkmcnt(4)
v_mfma_f32_16x16x32_bf16 v[152:155],a[128:131],a[216:219],v[152:155]
s_waitcnt lgkmcnt(2)
ds_read_b64_tr_b16 a[144:145],v22 offset:1280
ds_read_b64_tr_b16 a[146:147],v22 offset:3328
v_mfma_f32_16x16x32_bf16 v[132:135],a[136:139],a[192:195],0
s_waitcnt lgkmcnt(2)
ds_read_b64_tr_b16 a[148:149],v22 offset:17664
ds_read_b64_tr_b16 a[150:151],v22 offset:19712
v_mfma_f32_16x16x32_bf16 v[132:135],a[140:143],a[196:199],v[132:135]
buffer_load_dwordx4 v49,s[28:31],s57 offen offset:1024 lds
v_mfma_f32_16x16x32_bf16 v[140:143],a[140:143],a[204:207],0
v_mfma_f32_16x16x32_bf16 v[140:143],a[136:139],a[200:203],v[140:143]
v_mfma_f32_16x16x32_bf16 v[148:151],a[136:139],a[208:211],0
v_mfma_f32_16x16x32_bf16 v[148:151],a[140:143],a[212:215],v[148:151]
v_mfma_f32_16x16x32_bf16 v[156:159],a[140:143],a[220:223],0
buffer_load_dwordx4 v50,s[28:31],s57 offen offset:2048 lds
v_mfma_f32_16x16x32_bf16 v[156:159],a[136:139],a[216:219],v[156:159]
s_waitcnt lgkmcnt(2)
ds_read_b64_tr_b16 a[152:153],v22 offset:1288
ds_read_b64_tr_b16 a[154:155],v22 offset:3336
v_mfma_f32_16x16x32_bf16 v[160:163],a[144:147],a[192:195],0
s_waitcnt lgkmcnt(2)
ds_read_b64_tr_b16 a[156:157],v22 offset:17672
ds_read_b64_tr_b16 a[158:159],v22 offset:19720
v_mfma_f32_16x16x32_bf16 v[160:163],a[148:151],a[196:199],v[160:163]
v_mfma_f32_16x16x32_bf16 v[168:171],a[148:151],a[204:207],0
v_mfma_f32_16x16x32_bf16 v[168:171],a[144:147],a[200:203],v[168:171]
buffer_load_dwordx4 v51,s[28:31],s57 offen offset:3072 lds
v_mfma_f32_16x16x32_bf16 v[176:179],a[144:147],a[208:211],0
s_addk_i32 m0,4096
v_mfma_f32_16x16x32_bf16 v[176:179],a[148:151],a[212:215],v[176:179]
v_mfma_f32_16x16x32_bf16 v[184:187],a[148:151],a[220:223],0
v_mfma_f32_16x16x32_bf16 v[184:187],a[144:147],a[216:219],v[184:187]
s_waitcnt lgkmcnt(2)
v_mfma_f32_16x16x32_bf16 v[164:167],a[152:155],a[192:195],0
s_waitcnt lgkmcnt(0)
buffer_load_dwordx4 v52,s[28:31],s57 offen lds
ds_read_b64_tr_b16 a[160:161],v22 offset:1536
ds_read_b64_tr_b16 a[162:163],v22 offset:3584
v_mfma_f32_16x16x32_bf16 v[164:167],a[156:159],a[196:199],v[164:167]
ds_read_b64_tr_b16 a[164:165],v22 offset:17920
ds_read_b64_tr_b16 a[166:167],v22 offset:19968
v_mfma_f32_16x16x32_bf16 v[172:175],a[156:159],a[204:207],0
v_mfma_f32_16x16x32_bf16 v[172:175],a[152:155],a[200:203],v[172:175]
v_mfma_f32_16x16x32_bf16 v[180:183],a[152:155],a[208:211],0
v_mfma_f32_16x16x32_bf16 v[180:183],a[156:159],a[212:215],v[180:183]
buffer_load_dwordx4 v53,s[28:31],s57 offen offset:1024 lds
v_mfma_f32_16x16x32_bf16 v[188:191],a[156:159],a[220:223],0
s_waitcnt vmcnt(14)
v_mfma_f32_16x16x32_bf16 v[188:191],a[152:155],a[216:219],v[188:191]
s_barrier
ds_read_b64_tr_b16 a[168:169],v22 offset:1544
ds_read_b64_tr_b16 a[170:171],v22 offset:3592
s_waitcnt lgkmcnt(4)
v_mfma_f32_16x16x32_bf16 v[192:195],a[160:163],a[192:195],0
s_waitcnt lgkmcnt(2)
ds_read_b64_tr_b16 a[172:173],v22 offset:17928
ds_read_b64_tr_b16 a[174:175],v22 offset:19976
v_mfma_f32_16x16x32_bf16 v[192:195],a[164:167],a[196:199],v[192:195]
buffer_load_dwordx4 v54,s[28:31],s57 offen offset:2048 lds
v_mfma_f32_16x16x32_bf16 v[200:203],a[164:167],a[204:207],0
s_lshl_b32 s44,s41,13
s_add_u32 s44,s44,134217728
s_mov_b64 s[64:65],s[12:13]
v_mfma_f32_16x16x32_bf16 v[200:203],a[160:163],a[200:203],v[200:203]
s_add_u32 s64,s64,s44
s_addc_u32 s65,s65,0
s_and_b32 s65,s65,0xffff
v_mfma_f32_16x16x32_bf16 v[208:211],a[160:163],a[208:211],0
s_movk_i32 s66,8192
s_mov_b32 s67,0x20000
v_mov_b32 v14,4096
v_mfma_f32_16x16x32_bf16 v[208:211],a[164:167],a[212:215],v[208:211]
s_mov_b64 s[72:73],s[14:15]
buffer_load_dwordx4 v55,s[28:31],s57 offen offset:3072 lds
v_mfma_f32_16x16x32_bf16 v[216:219],a[164:167],a[220:223],0
s_add_u32 m0,s58,0
s_and_b32 s73,s73,0xffff
ds_read_b64_tr_b16 a[176:177],v22 offset:1792
v_mfma_f32_16x16x32_bf16 v[216:219],a[160:163],a[216:219],v[216:219]
s_mov_b32 s74,5242880
s_mov_b32 s75,0x20000
ds_read_b64_tr_b16 a[178:179],v22 offset:3840
s_waitcnt lgkmcnt(4)
v_mfma_f32_16x16x32_bf16 v[196:199],a[168:171],a[192:195],0
s_waitcnt lgkmcnt(2)
ds_read_b64_tr_b16 a[180:181],v22 offset:18176
ds_read_b64_tr_b16 a[182:183],v22 offset:20224
v_mfma_f32_16x16x32_bf16 v[196:199],a[172:175],a[196:199],v[196:199]
s_lshl_b32 s83,%1,2
buffer_load_dwordx4 v56,s[32:35],s56 offen lds
v_mfma_f32_16x16x32_bf16 v[204:207],a[172:175],a[204:207],0
s_addk_i32 m0,1024
v_lshlrev_b32 v15,3,v6
v_add_u32 v16,128,v15
v_mfma_f32_16x16x32_bf16 v[204:207],a[168:171],a[200:203],v[204:207]
s_add_u32 s83,s83,s40
ds_read_b64_tr_b16 a[184:185],v22 offset:1800
v_lshrrev_b32 v16,10,v16
v_mfma_f32_16x16x32_bf16 v[212:215],a[168:171],a[208:211],0
s_mulk_i32 s83,5120
ds_read_b64_tr_b16 a[186:187],v22 offset:3848
v_lshl_add_u32 v15,v16,24,v15
v_mfma_f32_16x16x32_bf16 v[212:215],a[172:175],a[212:215],v[212:215]
s_lshl_b32 s43,s41,5
buffer_load_dwordx4 v57,s[32:35],s56 offen lds
v_mfma_f32_16x16x32_bf16 v[220:223],a[172:175],a[220:223],0
s_lshl_b32 s44,s42,1
s_add_u32 s43,s43,s44
)A"
R"A(ds_read_b64_tr_b16 a[188:189],v22 offset:18184
v_mfma_f32_16x16x32_bf16 v[220:223],a[168:171],a[216:219],v[220:223]
s_addk_i32 m0,1024
s_lshl_b32 s44,s43,18
ds_read_b64_tr_b16 a[190:191],v22 offset:20232
s_waitcnt lgkmcnt(6)
v_mfma_f32_16x16x32_bf16 v[224:227],a[176:179],a[192:195],0
s_mov_b64 s[20:21],s[6:7]
s_mov_b32 s22,524288
ds_read_b64_tr_b16 a[128:129],v22
s_waitcnt lgkmcnt(5)
v_mfma_f32_16x16x32_bf16 v[224:227],a[180:183],a[196:199],v[224:227]
s_add_u32 s20,s20,s44
s_addc_u32 s21,s21,0
ds_read_b64_tr_b16 a[130:131],v22 offset:2048
v_mfma_f32_16x16x32_bf16 v[232:235],a[180:183],a[204:207],0
s_and_b32 s21,s21,0xffff
buffer_load_dwordx4 v58,s[32:35],s56 offen lds
v_mfma_f32_16x16x32_bf16 v[232:235],a[176:179],a[200:203],v[232:235]
s_mov_b32 s23,0x20000
s_mov_b64 s[24:25],s[8:9]
ds_read_b64_tr_b16 a[132:133],v22 offset:16384
v_mfma_f32_16x16x32_bf16 v[240:243],a[176:179],a[208:211],0
s_addk_i32 m0,1024
s_mov_b32 s26,524288
ds_read_b64_tr_b16 a[134:135],v22 offset:18432
v_mfma_f32_16x16x32_bf16 v[240:243],a[180:183],a[212:215],v[240:243]
s_add_u32 s24,s24,s44
s_addc_u32 s25,s25,0
ds_read_b64_tr_b16 a[136:137],v22 offset:8
v_mfma_f32_16x16x32_bf16 v[248:251],a[180:183],a[220:223],0
s_and_b32 s25,s25,0xffff
s_mov_b32 s27,0x20000
ds_read_b64_tr_b16 a[138:139],v22 offset:2056
v_mfma_f32_16x16x32_bf16 v[248:251],a[176:179],a[216:219],v[248:251]
s_lshl_b32 s44,s41,2
buffer_load_dwordx4 v59,s[32:35],s56 offen lds
s_waitcnt lgkmcnt(6)
v_mfma_f32_16x16x32_bf16 v[228:231],a[184:187],a[192:195],0
s_add_u32 s44,s44,s70
s_lshl_b32 s44,s44,3
ds_read_b64_tr_b16 a[140:141],v22 offset:16392
v_mfma_f32_16x16x32_bf16 v[228:231],a[188:191],a[196:199],v[228:231]
s_addk_i32 m0,1024
s_add_u32 s44,s44,s71
ds_read_b64_tr_b16 a[142:143],v22 offset:18440
v_mfma_f32_16x16x32_bf16 v[236:239],a[188:191],a[204:207],0
s_lshl_b32 s44,s44,18
s_mov_b64 s[36:37],s[12:13]
ds_read_b64_tr_b16 a[144:145],v22 offset:256
v_mfma_f32_16x16x32_bf16 v[236:239],a[184:187],a[200:203],v[236:239]
s_add_u32 s36,s36,s44
s_addc_u32 s37,s37,0
ds_read_b64_tr_b16 a[146:147],v22 offset:2304
v_mfma_f32_16x16x32_bf16 v[244:247],a[184:187],a[208:211],0
s_and_b32 s37,s37,0xffff
buffer_load_dwordx4 v60,s[32:35],s56 offen lds
v_mfma_f32_16x16x32_bf16 v[244:247],a[188:191],a[212:215],v[244:247]
s_xor_b32 s59,s59,131072
s_mov_b32 s38,262144
ds_read_b64_tr_b16 a[148:149],v22 offset:16640
v_mfma_f32_16x16x32_bf16 v[252:255],a[188:191],a[220:223],0
s_addk_i32 m0,1024
s_mov_b32 s39,0x20000
ds_read_b64_tr_b16 a[150:151],v22 offset:18688
v_mfma_f32_16x16x32_bf16 v[252:255],a[184:187],a[216:219],v[252:255]
s_lshl_b32 s51,s40,12
s_addk_i32 s57,128
ds_read_b64_tr_b16 a[152:153],v22 offset:264
s_waitcnt lgkmcnt(7)
v_mfma_f32_16x16x32_bf16 a[0:3],a[128:131],a[192:195],0
s_add_u32 s52,s51,16384
s_mul_i32 s47,s41,5
ds_read_b64_tr_b16 a[154:155],v22 offset:2312
v_mfma_f32_16x16x32_bf16 a[0:3],a[132:135],a[196:199],a[0:3]
s_add_u32 s47,s47,s42
buffer_load_dwordx4 v61,s[32:35],s56 offen lds
v_mfma_f32_16x16x32_bf16 a[32:35],a[132:135],a[204:207],0
s_and_b32 s47,s47,15
s_mul_i32 s44,s41,5
ds_read_b64_tr_b16 a[156:157],v22 offset:16648
v_mfma_f32_16x16x32_bf16 a[32:35],a[128:131],a[200:203],a[32:35]
s_addk_i32 m0,1024
s_add_u32 s44,s44,s42
ds_read_b64_tr_b16 a[158:159],v22 offset:18696
v_mfma_f32_16x16x32_bf16 a[64:67],a[128:131],a[208:211],0
s_lshl_b32 s47,s44,12
s_and_b32 s47,s47,0x7000
ds_read_b64_tr_b16 a[160:161],v22 offset:512
v_mfma_f32_16x16x32_bf16 a[64:67],a[132:135],a[212:215],a[64:67]
s_lshl_b32 s44,s44,14
s_and_b32 s44,s44,0x20000
ds_read_b64_tr_b16 a[162:163],v22 offset:2560
v_mfma_f32_16x16x32_bf16 a[96:99],a[132:135],a[220:223],0
s_or_b32 s47,s47,s44
buffer_load_dwordx4 v62,s[32:35],s56 offen lds
v_mfma_f32_16x16x32_bf16 a[96:99],a[128:131],a[216:219],a[96:99]
s_mov_b32 s54,0
s_mov_b32 s61,0x3e0293ee
ds_read_b64_tr_b16 a[164:165],v22 offset:16896
v_mfma_f32_16x16x32_bf16 a[4:7],a[136:139],a[192:195],0
s_addk_i32 m0,1024
s_mov_b32 s63,0x5d800000
ds_read_b64_tr_b16 a[166:167],v22 offset:18944
s_waitcnt lgkmcnt(6)
v_mfma_f32_16x16x32_bf16 a[4:7],a[140:143],a[196:199],a[4:7]
s_lshl_b32 s44,s40,2
s_add_u32 s44,s44,s70
ds_read_b64_tr_b16 a[168:169],v22 offset:520
v_mfma_f32_16x16x32_bf16 a[36:39],a[140:143],a[204:207],0
s_mov_b32 s46,0xff800000
ds_read_b64_tr_b16 a[170:171],v22 offset:2568
v_lshlrev_b32 v70,15,v65
v_mfma_f32_16x16x32_bf16 a[36:39],a[136:139],a[200:203],a[36:39]
s_add_u32 s60,s60,32768
buffer_load_dwordx4 v63,s[32:35],s56 offen lds
v_mfma_f32_16x16x32_bf16 a[68:71],a[136:139],a[208:211],0
s_cmp_eq_u32 s44,14
s_cselect_b32 s18,0x7149f2ca,s46
ds_read_b64_tr_b16 a[172:173],v22 offset:16904
v_mfma_f32_16x16x32_bf16 a[68:71],a[140:143],a[212:215],a[68:71]
s_add_u32 m0,s59,0
ds_read_b64_tr_b16 a[174:175],v22 offset:18952
v_lshl_add_u32 v70,v66,6,v70
v_mfma_f32_16x16x32_bf16 a[100:103],a[140:143],a[220:223],0
s_cmp_eq_u32 s44,13
s_cselect_b32 s19,0x7149f2ca,s46
ds_read_b64_tr_b16 a[176:177],v22 offset:768
v_mfma_f32_16x16x32_bf16 a[100:103],a[136:139],a[216:219],a[100:103]
s_cmp_eq_u32 s44,12
s_cselect_b32 s100,0x7149f2ca,s46
ds_read_b64_tr_b16 a[178:179],v22 offset:2816
v_mfma_f32_16x16x32_bf16 a[8:11],a[144:147],a[192:195],0
s_add_u32 s58,s58,32768
buffer_load_dwordx4 v48,s[28:31],s57 offen lds
v_mfma_f32_16x16x32_bf16 a[8:11],a[148:151],a[196:199],a[8:11]
s_cmp_eq_u32 s44,15
s_cselect_b32 s101,0x7149f2ca,s46
ds_read_b64_tr_b16 a[180:181],v22 offset:17152
v_mfma_f32_16x16x32_bf16 a[40:43],a[148:151],a[204:207],0
s_add_u32 s56,s56,524288
ds_read_b64_tr_b16 a[182:183],v22 offset:19200
v_xor_b32 v71,s40,v65
v_mfma_f32_16x16x32_bf16 a[40:43],a[144:147],a[200:203],a[40:43]
s_mul_i32 s81,s70,1310720
ds_read_b64_tr_b16 a[184:185],v22 offset:776
v_lshl_add_u32 v70,v71,8,v70
v_mfma_f32_16x16x32_bf16 a[72:75],a[144:147],a[208:211],0
s_mul_i32 s44,s71,320
ds_read_b64_tr_b16 a[186:187],v22 offset:2824
v_lshl_add_u32 v4,v64,4,v70
v_mfma_f32_16x16x32_bf16 a[72:75],a[148:151],a[212:215],a[72:75]
s_add_u32 s81,s81,s44
buffer_load_dwordx4 v49,s[28:31],s57 offen offset:1024 lds
v_mfma_f32_16x16x32_bf16 a[104:107],a[148:151],a[220:223],0
s_mul_i32 s82,s40,10240
ds_read_b64_tr_b16 a[188:189],v22 offset:17160
v_xor_b32 v71,2,v64
v_mfma_f32_16x16x32_bf16 a[104:107],a[144:147],a[216:219],a[104:107]
s_mul_i32 s44,s40,40960
ds_read_b64_tr_b16 a[190:191],v22 offset:19208
v_lshl_add_u32 v5,v71,4,v70
v_mfma_f32_16x16x32_bf16 a[12:15],a[152:155],a[192:195],0
s_xor_b32 s59,s59,131072
v_lshlrev_b32 v72,10,v65
v_lshl_add_u32 v72,v64,6,v72
s_waitcnt lgkmcnt(6)
v_mfma_f32_16x16x32_bf16 a[12:15],a[156:159],a[196:199],a[12:15]
s_cmp_eq_u32 s60,131072
s_cselect_b32 s60,32768,s60
v_and_b32 v73,2,v65
v_mfma_f32_16x16x32_bf16 a[44:47],a[156:159],a[204:207],0
s_lshr_b32 s92,%1,3
buffer_load_dwordx4 v50,s[28:31],s57 offen offset:2048 lds
v_mfma_f32_16x16x32_bf16 a[44:47],a[152:155],a[200:203],a[44:47]
s_lshl_b32 s92,s92,2
v_add_u32 v22,s60,v23
v_xor_b32 v20,131072,v20
v_mfma_f32_16x16x32_bf16 a[76:79],a[152:155],a[208:211],0
s_add_u32 s92,s92,s40
v_xor_b32 v21,131072,v21
v_xor_b32 v73,v73,v66
v_mfma_f32_16x16x32_bf16 a[76:79],a[156:159],a[212:215],a[76:79]
s_lshl_b32 s92,s92,7
v_lshl_add_u32 v72,v73,4,v72
v_add_u32 v1,98304,v72
v_mfma_f32_16x16x32_bf16 a[108:111],a[156:159],a[220:223],0
s_add_u32 s93,s92,L_tailseg_%=-L_pc_%=
v_lshlrev_b32 v72,10,v66
v_lshl_add_u32 v72,v65,6,v72
v_mfma_f32_16x16x32_bf16 a[108:111],a[152:155],a[216:219],a[108:111]
s_waitcnt vmcnt(11) lgkmcnt(0)
s_add_u32 s60,s60,32768
buffer_load_dwordx4 v51,s[28:31],s57 offen offset:3072 lds
v_mfma_f32_16x16x32_bf16 a[16:19],a[160:163],a[192:195],0
s_barrier
s_addk_i32 m0,4096
ds_read_b128 a[224:227],v20
v_mfma_f32_16x16x32_bf16 a[16:19],a[164:167],a[196:199],a[16:19]
ds_read_b64_tr_b16 a[128:129],v22 offset:1024
v_lshl_add_u32 v72,v64,3,v72
v_mfma_f32_16x16x32_bf16 a[48:51],a[164:167],a[204:207],0
ds_read_b64_tr_b16 a[130:131],v22 offset:3072
v_add_u32 v72,114688,v72
v_mfma_f32_16x16x32_bf16 a[48:51],a[160:163],a[200:203],a[48:51]
buffer_load_dwordx4 v52,s[28:31],s57 offen lds
v_mfma_f32_16x16x32_bf16 a[80:83],a[160:163],a[208:211],0
s_cmp_ge_u32 s58,131072
s_cselect_b32 s62,98304,0
ds_read_b128 a[228:231],v21
v_mfma_f32_16x16x32_bf16 a[80:83],a[164:167],a[212:215],a[80:83]
s_sub_u32 s58,s58,s62
ds_read_b64_tr_b16 a[132:133],v22 offset:17408
v_and_b32 v73,1,v66
v_mfma_f32_16x16x32_bf16 a[112:115],a[164:167],a[220:223],0
ds_read_b64_tr_b16 a[134:135],v22 offset:19456
v_lshl_add_u32 v2,v73,5,v72
v_mfma_f32_16x16x32_bf16 a[112:115],a[160:163],a[216:219],a[112:115]
ds_read_b128 a[232:235],v20 offset:2048
v_xor_b32 v73,1,v73
v_mfma_f32_16x16x32_bf16 a[20:23],a[168:171],a[192:195],0
buffer_load_dwordx4 v53,s[28:31],s57 offen offset:1024 lds
v_mfma_f32_16x16x32_bf16 a[20:23],a[172:175],a[196:199],a[20:23]
ds_read_b128 a[236:239],v21 offset:2048
v_lshl_add_u32 v3,v73,5,v72
v_mfma_f32_16x16x32_bf16 a[52:55],a[172:175],a[204:207],0
ds_read_b128 a[240:243],v20 offset:4096
v_mov_b32 v73,0x3f803f80
v_mfma_f32_16x16x32_bf16 a[52:55],a[168:171],a[200:203],a[52:55]
ds_read_b128 a[244:247],v21 offset:4096
v_add_u32 v70,0,v12
v_mfma_f32_16x16x32_bf16 a[84:87],a[168:171],a[208:211],0
buffer_load_dword v13,v14,s[64:67],0 offen sc1
v_mfma_f32_16x16x32_bf16 a[84:87],a[172:175],a[212:215],a[84:87]
ds_read_b128 a[248:251],v20 offset:6144
v_mul_u32_u24 v71,3277,v70
v_mfma_f32_16x16x32_bf16 a[116:119],a[172:175],a[220:223],0
ds_read_b128 a[252:255],v21 offset:6144
v_lshrrev_b32 v71,16,v71
v_mfma_f32_16x16x32_bf16 a[116:119],a[168:171],a[216:219],a[116:119]
ds_read_b64_tr_b16 a[136:137],v22 offset:1032
v_mul_u32_u24 v72,20,v71
v_mfma_f32_16x16x32_bf16 a[24:27],a[176:179],a[192:195],0
buffer_load_dwordx4 v54,s[28:31],s57 offen offset:2048 lds
v_mfma_f32_16x16x32_bf16 a[24:27],a[180:183],a[196:199],a[24:27]
ds_read_b64_tr_b16 a[138:139],v22 offset:3080
v_sub_u32 v72,v70,v72
v_mfma_f32_16x16x32_bf16 a[56:59],a[180:183],a[204:207],0
ds_read_b64_tr_b16 a[140:141],v22 offset:17416
v_add_u32 v70,64,v12
v_mfma_f32_16x16x32_bf16 a[56:59],a[176:179],a[200:203],a[56:59]
ds_read_b64_tr_b16 a[142:143],v22 offset:19464
v_lshlrev_b32 v24,10,v6
v_mfma_f32_16x16x32_bf16 a[88:91],a[176:179],a[208:211],0
buffer_load_dwordx4 v55,s[28:31],s57 offen offset:3072 lds
v_mfma_f32_16x16x32_bf16 a[88:91],a[180:183],a[212:215],a[88:91]
s_addk_i32 s57,128
ds_read_b64_tr_b16 a[144:145],v22 offset:1280
v_xor_b32 v20,131072,v20
v_mfma_f32_16x16x32_bf16 a[120:123],a[180:183],a[220:223],0
s_cmp_eq_u32 s60,131072
s_cselect_b32 s60,32768,s60
ds_read_b64_tr_b16 a[146:147],v22 offset:3328
v_mfma_f32_16x16x32_bf16 a[120:123],a[176:179],a[216:219],a[120:123]
ds_read_b64_tr_b16 a[148:149],v22 offset:17664
v_xor_b32 v21,131072,v21
v_mfma_f32_16x16x32_bf16 a[28:31],a[184:187],a[192:195],0
ds_read_b64_tr_b16 a[150:151],v22 offset:19712
v_mfma_f32_16x16x32_bf16 a[28:31],a[188:191],a[196:199],a[28:31]
ds_read_b64_tr_b16 a[152:153],v22 offset:1288
ds_read_b64_tr_b16 a[154:155],v22 offset:3336
ds_read_b64_tr_b16 a[156:157],v22 offset:17672
ds_read_b64_tr_b16 a[158:159],v22 offset:19720
v_mfma_f32_16x16x32_bf16 a[60:63],a[188:191],a[204:207],0
v_mfma_f32_16x16x32_bf16 a[60:63],a[184:187],a[200:203],a[60:63]
v_cmp_gt_u32 vcc,2,v64
s_nop 1
v_cndmask_b32 v28,0,v73,vcc
v_mfma_f32_16x16x32_bf16 a[92:95],a[184:187],a[208:211],0
v_and_b32 v73,1,v64
v_mul_u32_u24 v73,0xaaaa,v73
v_mfma_f32_16x16x32_bf16 a[92:95],a[188:191],a[212:215],a[92:95]
v_add_u32 v73,0x4444,v73
v_lshl_or_b32 v27,v73,16,v73
v_mfma_f32_16x16x32_bf16 a[124:127],a[188:191],a[220:223],0
s_add_u32 m0,s58,0
v_mov_b32 v29,v28
v_mov_b32 v30,v28
v_mfma_f32_16x16x32_bf16 a[124:127],a[184:187],a[216:219],a[124:127]
s_add_u32 s58,s58,32768
ds_read_b64_tr_b16 a[160:161],v22 offset:1536
v_mov_b32 v31,v28
s_waitcnt lgkmcnt(6)
v_mfma_f32_16x16x32_bf16 v[128:131],a[128:131],a[224:227],v[128:131]
ds_read_b64_tr_b16 a[162:163],v22 offset:3584
v_bfe_u32 v73,v71,2,1
v_mfma_f32_16x16x32_bf16 v[128:131],a[132:135],a[228:231],v[128:131]
buffer_load_dwordx4 v56,s[32:35],s56 offen lds
v_mfma_f32_16x16x32_bf16 v[136:139],a[132:135],a[236:239],v[136:139]
)A"
R"A(ds_read_b64_tr_b16 a[164:165],v22 offset:17920
v_lshlrev_b32 v73,1,v73
v_mfma_f32_16x16x32_bf16 v[136:139],a[128:131],a[232:235],v[136:139]
s_addk_i32 m0,1024
ds_read_b64_tr_b16 a[166:167],v22 offset:19968
v_xor_b32 v72,v72,v73
v_mfma_f32_16x16x32_bf16 v[144:147],a[128:131],a[240:243],v[144:147]
ds_read_b64_tr_b16 a[168:169],v22 offset:1544
v_mul_u32_u24 v71,1280,v71
v_mfma_f32_16x16x32_bf16 v[144:147],a[132:135],a[244:247],v[144:147]
ds_read_b64_tr_b16 a[170:171],v22 offset:3592
v_lshl_add_u32 v71,v72,4,v71
v_mfma_f32_16x16x32_bf16 v[152:155],a[132:135],a[252:255],v[152:155]
buffer_load_dwordx4 v57,s[32:35],s56 offen lds
v_mfma_f32_16x16x32_bf16 v[152:155],a[128:131],a[248:251],v[152:155]
ds_read_b64_tr_b16 a[172:173],v22 offset:17928
v_add_u32 v71,s44,v71
v_mfma_f32_16x16x32_bf16 v[132:135],a[136:139],a[224:227],v[132:135]
ds_read_b64_tr_b16 a[174:175],v22 offset:19976
v_subrev_u32 v7,0,v71
v_mfma_f32_16x16x32_bf16 v[132:135],a[140:143],a[228:231],v[132:135]
s_addk_i32 m0,1024
v_mul_u32_u24 v71,3277,v70
v_lshrrev_b32 v71,16,v71
v_mfma_f32_16x16x32_bf16 v[140:143],a[140:143],a[236:239],v[140:143]
ds_read_b64_tr_b16 a[176:177],v22 offset:1792
v_mul_u32_u24 v72,20,v71
v_mfma_f32_16x16x32_bf16 v[140:143],a[136:139],a[232:235],v[140:143]
ds_read_b64_tr_b16 a[178:179],v22 offset:3840
v_sub_u32 v72,v70,v72
v_mfma_f32_16x16x32_bf16 v[148:151],a[136:139],a[240:243],v[148:151]
buffer_load_dwordx4 v58,s[32:35],s56 offen lds
v_mfma_f32_16x16x32_bf16 v[148:151],a[140:143],a[244:247],v[148:151]
ds_read_b64_tr_b16 a[180:181],v22 offset:18176
v_bfe_u32 v73,v71,2,1
v_mfma_f32_16x16x32_bf16 v[156:159],a[140:143],a[252:255],v[156:159]
ds_read_b64_tr_b16 a[182:183],v22 offset:20224
v_lshlrev_b32 v73,1,v73
v_mfma_f32_16x16x32_bf16 v[156:159],a[136:139],a[248:251],v[156:159]
s_addk_i32 m0,1024
v_xor_b32 v72,v72,v73
v_mul_u32_u24 v71,1280,v71
v_mfma_f32_16x16x32_bf16 v[160:163],a[144:147],a[224:227],v[160:163]
s_waitcnt lgkmcnt(4)
ds_read_b64_tr_b16 a[184:185],v22 offset:1800
v_lshl_add_u32 v71,v72,4,v71
v_mfma_f32_16x16x32_bf16 v[160:163],a[148:151],a[228:231],v[160:163]
ds_read_b64_tr_b16 a[186:187],v22 offset:3848
v_add_u32 v71,s44,v71
v_mfma_f32_16x16x32_bf16 v[168:171],a[148:151],a[236:239],v[168:171]
buffer_load_dwordx4 v59,s[32:35],s56 offen lds
v_mfma_f32_16x16x32_bf16 v[168:171],a[144:147],a[232:235],v[168:171]
ds_read_b64_tr_b16 a[188:189],v22 offset:18184
v_subrev_u32 v8,1024,v71
v_mfma_f32_16x16x32_bf16 v[176:179],a[144:147],a[240:243],v[176:179]
s_addk_i32 m0,1024
ds_read_b64_tr_b16 a[190:191],v22 offset:20232
v_add_u32 v70,128,v12
v_mfma_f32_16x16x32_bf16 v[176:179],a[148:151],a[244:247],v[176:179]
v_mul_u32_u24 v71,3277,v70
v_lshrrev_b32 v71,16,v71
v_mfma_f32_16x16x32_bf16 v[184:187],a[148:151],a[252:255],v[184:187]
v_mul_u32_u24 v72,20,v71
v_sub_u32 v72,v70,v72
v_mfma_f32_16x16x32_bf16 v[184:187],a[144:147],a[248:251],v[184:187]
buffer_load_dwordx4 v60,s[32:35],s56 offen lds
v_mfma_f32_16x16x32_bf16 v[164:167],a[152:155],a[224:227],v[164:167]
ds_read_b64_tr_b16 a[128:129],v22
v_bfe_u32 v73,v71,2,1
v_mfma_f32_16x16x32_bf16 v[164:167],a[156:159],a[228:231],v[164:167]
ds_read_b64_tr_b16 a[130:131],v22 offset:2048
v_lshlrev_b32 v73,1,v73
v_mfma_f32_16x16x32_bf16 v[172:175],a[156:159],a[236:239],v[172:175]
s_addk_i32 m0,1024
ds_read_b64_tr_b16 a[132:133],v22 offset:16384
v_xor_b32 v72,v72,v73
v_mfma_f32_16x16x32_bf16 v[172:175],a[152:155],a[232:235],v[172:175]
ds_read_b64_tr_b16 a[134:135],v22 offset:18432
v_mul_u32_u24 v71,1280,v71
v_mfma_f32_16x16x32_bf16 v[180:183],a[152:155],a[240:243],v[180:183]
v_lshl_add_u32 v71,v72,4,v71
v_add_u32 v71,s44,v71
v_mfma_f32_16x16x32_bf16 v[180:183],a[156:159],a[244:247],v[180:183]
buffer_load_dwordx4 v61,s[32:35],s56 offen lds
v_mfma_f32_16x16x32_bf16 v[188:191],a[156:159],a[252:255],v[188:191]
ds_read_b64_tr_b16 a[136:137],v22 offset:8
v_subrev_u32 v9,2048,v71
v_mfma_f32_16x16x32_bf16 v[188:191],a[152:155],a[248:251],v[188:191]
ds_read_b64_tr_b16 a[138:139],v22 offset:2056
v_add_u32 v70,192,v12
v_mfma_f32_16x16x32_bf16 v[192:195],a[160:163],a[224:227],v[192:195]
s_addk_i32 m0,1024
ds_read_b64_tr_b16 a[140:141],v22 offset:16392
v_mul_u32_u24 v71,3277,v70
v_mfma_f32_16x16x32_bf16 v[192:195],a[164:167],a[228:231],v[192:195]
ds_read_b64_tr_b16 a[142:143],v22 offset:18440
v_lshrrev_b32 v71,16,v71
v_mfma_f32_16x16x32_bf16 v[200:203],a[164:167],a[236:239],v[200:203]
v_mul_u32_u24 v72,20,v71
v_sub_u32 v72,v70,v72
v_mfma_f32_16x16x32_bf16 v[200:203],a[160:163],a[232:235],v[200:203]
buffer_load_dwordx4 v62,s[32:35],s56 offen lds
v_mfma_f32_16x16x32_bf16 v[208:211],a[160:163],a[240:243],v[208:211]
ds_read_b64_tr_b16 a[144:145],v22 offset:256
v_bfe_u32 v73,v71,2,1
v_mfma_f32_16x16x32_bf16 v[208:211],a[164:167],a[244:247],v[208:211]
s_addk_i32 m0,1024
ds_read_b64_tr_b16 a[146:147],v22 offset:2304
v_lshlrev_b32 v73,1,v73
v_mfma_f32_16x16x32_bf16 v[216:219],a[164:167],a[252:255],v[216:219]
ds_read_b64_tr_b16 a[148:149],v22 offset:16640
v_xor_b32 v72,v72,v73
v_mfma_f32_16x16x32_bf16 v[216:219],a[160:163],a[248:251],v[216:219]
ds_read_b64_tr_b16 a[150:151],v22 offset:18688
v_mul_u32_u24 v71,1280,v71
v_mfma_f32_16x16x32_bf16 v[196:199],a[168:171],a[224:227],v[196:199]
buffer_load_dwordx4 v63,s[32:35],s56 offen lds
v_mfma_f32_16x16x32_bf16 v[196:199],a[172:175],a[228:231],v[196:199]
s_add_u32 s56,s56,524288
v_lshl_add_u32 v71,v72,4,v71
v_add_u32 v71,s44,v71
v_mfma_f32_16x16x32_bf16 v[204:207],a[172:175],a[236:239],v[204:207]
v_subrev_u32 v10,3072,v71
v_add_u32 v70,256,v12
v_mfma_f32_16x16x32_bf16 v[204:207],a[168:171],a[232:235],v[204:207]
s_add_u32 m0,s59,0
ds_read_b64_tr_b16 a[152:153],v22 offset:264
v_mul_u32_u24 v71,3277,v70
v_mfma_f32_16x16x32_bf16 v[212:215],a[168:171],a[240:243],v[212:215]
s_xor_b32 s59,s59,131072
ds_read_b64_tr_b16 a[154:155],v22 offset:2312
v_lshrrev_b32 v71,16,v71
v_mfma_f32_16x16x32_bf16 v[212:215],a[172:175],a[244:247],v[212:215]
ds_read_b64_tr_b16 a[156:157],v22 offset:16648
v_mul_u32_u24 v72,20,v71
v_mfma_f32_16x16x32_bf16 v[220:223],a[172:175],a[252:255],v[220:223]
buffer_load_dwordx4 v48,s[28:31],s57 offen lds
v_mfma_f32_16x16x32_bf16 v[220:223],a[168:171],a[248:251],v[220:223]
s_waitcnt lgkmcnt(3)
ds_read_b64_tr_b16 a[158:159],v22 offset:18696
v_sub_u32 v72,v70,v72
v_mfma_f32_16x16x32_bf16 v[224:227],a[176:179],a[224:227],v[224:227]
v_bfe_u32 v73,v71,2,1
v_lshlrev_b32 v73,1,v73
v_mfma_f32_16x16x32_bf16 v[224:227],a[180:183],a[228:231],v[224:227]
ds_read_b64_tr_b16 a[160:161],v22 offset:512
v_xor_b32 v72,v72,v73
v_mfma_f32_16x16x32_bf16 v[232:235],a[180:183],a[236:239],v[232:235]
ds_read_b64_tr_b16 a[162:163],v22 offset:2560
v_mul_u32_u24 v71,1280,v71
v_mfma_f32_16x16x32_bf16 v[232:235],a[176:179],a[232:235],v[232:235]
ds_read_b64_tr_b16 a[164:165],v22 offset:16896
v_lshl_add_u32 v71,v72,4,v71
v_mfma_f32_16x16x32_bf16 v[240:243],a[176:179],a[240:243],v[240:243]
buffer_load_dwordx4 v49,s[28:31],s57 offen offset:1024 lds
v_mfma_f32_16x16x32_bf16 v[240:243],a[180:183],a[244:247],v[240:243]
ds_read_b64_tr_b16 a[166:167],v22 offset:18944
v_add_u32 v71,s44,v71
v_mfma_f32_16x16x32_bf16 v[248:251],a[180:183],a[252:255],v[248:251]
ds_read_b64_tr_b16 a[168:169],v22 offset:520
v_subrev_u32 v11,0,v71
v_mfma_f32_16x16x32_bf16 v[248:251],a[176:179],a[248:251],v[248:251]
s_cmp_ge_u32 s58,131072
s_cselect_b32 s62,98304,0
ds_read_b64_tr_b16 a[170:171],v22 offset:2568
v_mfma_f32_16x16x32_bf16 v[228:231],a[184:187],a[224:227],v[228:231]
s_sub_u32 s58,s58,s62
ds_read_b64_tr_b16 a[172:173],v22 offset:16904
v_mfma_f32_16x16x32_bf16 v[228:231],a[188:191],a[228:231],v[228:231]
buffer_load_dwordx4 v50,s[28:31],s57 offen offset:2048 lds
v_mfma_f32_16x16x32_bf16 v[236:239],a[188:191],a[236:239],v[236:239]
ds_read_b64_tr_b16 a[174:175],v22 offset:18952
v_mfma_f32_16x16x32_bf16 v[236:239],a[184:187],a[232:235],v[236:239]
ds_read_b64_tr_b16 a[176:177],v22 offset:768
v_mfma_f32_16x16x32_bf16 v[244:247],a[184:187],a[240:243],v[244:247]
ds_read_b64_tr_b16 a[178:179],v22 offset:2816
v_mfma_f32_16x16x32_bf16 v[244:247],a[188:191],a[244:247],v[244:247]
ds_read_b64_tr_b16 a[180:181],v22 offset:17152
v_mfma_f32_16x16x32_bf16 v[252:255],a[188:191],a[252:255],v[252:255]
ds_read_b64_tr_b16 a[182:183],v22 offset:19200
v_mfma_f32_16x16x32_bf16 v[252:255],a[184:187],a[248:251],v[252:255]
buffer_load_dwordx4 v51,s[28:31],s57 offen offset:3072 lds
v_mfma_f32_16x16x32_bf16 a[0:3],a[128:131],a[224:227],a[0:3]
ds_read_b64_tr_b16 a[184:185],v22 offset:776
v_mfma_f32_16x16x32_bf16 a[0:3],a[132:135],a[228:231],a[0:3]
ds_read_b64_tr_b16 a[186:187],v22 offset:2824
v_mfma_f32_16x16x32_bf16 a[32:35],a[132:135],a[236:239],a[32:35]
s_addk_i32 m0,4096
ds_read_b64_tr_b16 a[188:189],v22 offset:17160
v_mfma_f32_16x16x32_bf16 a[32:35],a[128:131],a[232:235],a[32:35]
ds_read_b64_tr_b16 a[190:191],v22 offset:19208
v_add_u32 v22,s60,v23
v_mfma_f32_16x16x32_bf16 a[64:67],a[128:131],a[240:243],a[64:67]
v_mfma_f32_16x16x32_bf16 a[64:67],a[132:135],a[244:247],a[64:67]
buffer_load_dwordx4 v52,s[28:31],s57 offen lds
v_mfma_f32_16x16x32_bf16 a[96:99],a[132:135],a[252:255],a[96:99]
v_mfma_f32_16x16x32_bf16 a[96:99],a[128:131],a[248:251],a[96:99]
v_mfma_f32_16x16x32_bf16 a[4:7],a[136:139],a[224:227],a[4:7]
v_mfma_f32_16x16x32_bf16 a[4:7],a[140:143],a[228:231],a[4:7]
v_mfma_f32_16x16x32_bf16 a[36:39],a[140:143],a[236:239],a[36:39]
buffer_load_dwordx4 v53,s[28:31],s57 offen offset:1024 lds
v_mfma_f32_16x16x32_bf16 a[36:39],a[136:139],a[232:235],a[36:39]
v_mfma_f32_16x16x32_bf16 a[68:71],a[136:139],a[240:243],a[68:71]
v_mfma_f32_16x16x32_bf16 a[68:71],a[140:143],a[244:247],a[68:71]
v_mfma_f32_16x16x32_bf16 a[100:103],a[140:143],a[252:255],a[100:103]
v_mfma_f32_16x16x32_bf16 a[100:103],a[136:139],a[248:251],a[100:103]
v_mfma_f32_16x16x32_bf16 a[8:11],a[144:147],a[224:227],a[8:11]
buffer_load_dwordx4 v54,s[28:31],s57 offen offset:2048 lds
v_mfma_f32_16x16x32_bf16 a[8:11],a[148:151],a[228:231],a[8:11]
v_mfma_f32_16x16x32_bf16 a[40:43],a[148:151],a[236:239],a[40:43]
v_mfma_f32_16x16x32_bf16 a[40:43],a[144:147],a[232:235],a[40:43]
v_mfma_f32_16x16x32_bf16 a[72:75],a[144:147],a[240:243],a[72:75]
v_mfma_f32_16x16x32_bf16 a[72:75],a[148:151],a[244:247],a[72:75]
v_mfma_f32_16x16x32_bf16 a[104:107],a[148:151],a[252:255],a[104:107]
buffer_load_dwordx4 v55,s[28:31],s57 offen offset:3072 lds
v_mfma_f32_16x16x32_bf16 a[104:107],a[144:147],a[248:251],a[104:107]
s_addk_i32 s57,128
s_waitcnt lgkmcnt(0)
v_mfma_f32_16x16x32_bf16 a[12:15],a[152:155],a[224:227],a[12:15]
v_mfma_f32_16x16x32_bf16 a[12:15],a[156:159],a[228:231],a[12:15]
v_mfma_f32_16x16x32_bf16 a[44:47],a[156:159],a[236:239],a[44:47]
v_mfma_f32_16x16x32_bf16 a[44:47],a[152:155],a[232:235],a[44:47]
v_mfma_f32_16x16x32_bf16 a[76:79],a[152:155],a[240:243],a[76:79]
v_mfma_f32_16x16x32_bf16 a[76:79],a[156:159],a[244:247],a[76:79]
v_mfma_f32_16x16x32_bf16 a[108:111],a[156:159],a[252:255],a[108:111]
v_mfma_f32_16x16x32_bf16 a[108:111],a[152:155],a[248:251],a[108:111]
s_waitcnt vmcnt(16)
v_mfma_f32_16x16x32_bf16 a[16:19],a[160:163],a[224:227],a[16:19]
s_barrier
ds_read_b128 a[192:195],v20
v_mfma_f32_16x16x32_bf16 a[16:19],a[164:167],a[228:231],a[16:19]
ds_read_b128 a[196:199],v21
v_mfma_f32_16x16x32_bf16 a[48:51],a[164:167],a[236:239],a[48:51]
ds_read_b128 a[200:203],v20 offset:2048
v_mfma_f32_16x16x32_bf16 a[48:51],a[160:163],a[232:235],a[48:51]
ds_read_b128 a[204:207],v21 offset:2048
v_mfma_f32_16x16x32_bf16 a[80:83],a[160:163],a[240:243],a[80:83]
ds_read_b128 a[208:211],v20 offset:4096
v_mfma_f32_16x16x32_bf16 a[80:83],a[164:167],a[244:247],a[80:83]
ds_read_b128 a[212:215],v21 offset:4096
v_mfma_f32_16x16x32_bf16 a[112:115],a[164:167],a[252:255],a[112:115]
ds_read_b128 a[216:219],v20 offset:6144
v_mfma_f32_16x16x32_bf16 a[112:115],a[160:163],a[248:251],a[112:115]
ds_read_b128 a[220:223],v21 offset:6144
v_mfma_f32_16x16x32_bf16 a[20:23],a[168:171],a[224:227],a[20:23]
ds_read_b64_tr_b16 a[128:129],v22 offset:1024
v_mfma_f32_16x16x32_bf16 a[20:23],a[172:175],a[228:231],a[20:23]
ds_read_b64_tr_b16 a[130:131],v22 offset:3072
v_mfma_f32_16x16x32_bf16 a[52:55],a[172:175],a[236:239],a[52:55]
)A"
R"A(ds_read_b64_tr_b16 a[132:133],v22 offset:17408
v_mfma_f32_16x16x32_bf16 a[52:55],a[168:171],a[232:235],a[52:55]
ds_read_b64_tr_b16 a[134:135],v22 offset:19456
v_mfma_f32_16x16x32_bf16 a[84:87],a[168:171],a[240:243],a[84:87]
ds_read_b64_tr_b16 a[136:137],v22 offset:1032
v_mfma_f32_16x16x32_bf16 a[84:87],a[172:175],a[244:247],a[84:87]
ds_read_b64_tr_b16 a[138:139],v22 offset:3080
v_mfma_f32_16x16x32_bf16 a[116:119],a[172:175],a[252:255],a[116:119]
ds_read_b64_tr_b16 a[140:141],v22 offset:17416
v_mfma_f32_16x16x32_bf16 a[116:119],a[168:171],a[248:251],a[116:119]
ds_read_b64_tr_b16 a[142:143],v22 offset:19464
v_mfma_f32_16x16x32_bf16 a[24:27],a[176:179],a[224:227],a[24:27]
ds_read_b64_tr_b16 a[144:145],v22 offset:1280
v_mfma_f32_16x16x32_bf16 a[24:27],a[180:183],a[228:231],a[24:27]
ds_read_b64_tr_b16 a[146:147],v22 offset:3328
v_mfma_f32_16x16x32_bf16 a[56:59],a[180:183],a[236:239],a[56:59]
ds_read_b64_tr_b16 a[148:149],v22 offset:17664
v_mfma_f32_16x16x32_bf16 a[56:59],a[176:179],a[232:235],a[56:59]
ds_read_b64_tr_b16 a[150:151],v22 offset:19712
v_mfma_f32_16x16x32_bf16 a[88:91],a[176:179],a[240:243],a[88:91]
ds_read_b64_tr_b16 a[152:153],v22 offset:1288
v_mfma_f32_16x16x32_bf16 a[88:91],a[180:183],a[244:247],a[88:91]
ds_read_b64_tr_b16 a[154:155],v22 offset:3336
v_mfma_f32_16x16x32_bf16 a[120:123],a[180:183],a[252:255],a[120:123]
ds_read_b64_tr_b16 a[156:157],v22 offset:17672
v_mfma_f32_16x16x32_bf16 a[120:123],a[176:179],a[248:251],a[120:123]
ds_read_b64_tr_b16 a[158:159],v22 offset:19720
v_mfma_f32_16x16x32_bf16 a[28:31],a[184:187],a[224:227],a[28:31]
v_mfma_f32_16x16x32_bf16 a[28:31],a[188:191],a[228:231],a[28:31]
v_mfma_f32_16x16x32_bf16 a[60:63],a[188:191],a[236:239],a[60:63]
v_mfma_f32_16x16x32_bf16 a[60:63],a[184:187],a[232:235],a[60:63]
v_mfma_f32_16x16x32_bf16 a[92:95],a[184:187],a[240:243],a[92:95]
v_mfma_f32_16x16x32_bf16 a[92:95],a[188:191],a[244:247],a[92:95]
v_mfma_f32_16x16x32_bf16 a[124:127],a[188:191],a[252:255],a[124:127]
v_mfma_f32_16x16x32_bf16 a[124:127],a[184:187],a[248:251],a[124:127]
s_mov_b32 s53,3
.p2align 5
L_qp_%=:
s_waitcnt lgkmcnt(1)
v_mfma_f32_16x16x32_bf16 v[128:131],a[128:131],a[192:195],v[128:131]
s_add_u32 m0,s58,0
s_mov_b32 s86,s87
ds_read_b64_tr_b16 a[160:161],v22 offset:1536
v_mfma_f32_16x16x32_bf16 v[128:131],a[132:135],a[196:199],v[128:131]
s_add_u32 s60,s60,32768
buffer_load_dwordx4 v56,s[32:35],s56 offen lds
v_mfma_f32_16x16x32_bf16 v[136:139],a[132:135],a[204:207],v[136:139]
s_cmp_eq_u32 s60,131072
s_cselect_b32 s60,32768,s60
ds_read_b64_tr_b16 a[162:163],v22 offset:3584
v_mfma_f32_16x16x32_bf16 v[136:139],a[128:131],a[200:203],v[136:139]
s_load_dword s86,s[84:85],0x0 glc
s_addk_i32 m0,1024
ds_read_b64_tr_b16 a[164:165],v22 offset:17920
v_mfma_f32_16x16x32_bf16 v[144:147],a[128:131],a[208:211],v[144:147]
s_add_u32 s58,s58,32768
ds_read_b64_tr_b16 a[166:167],v22 offset:19968
v_xor_b32 v20,131072,v20
v_mfma_f32_16x16x32_bf16 v[144:147],a[132:135],a[212:215],v[144:147]
s_mov_b32 s90,L_att0x_%=-L_pc_%=+128
ds_read_b64_tr_b16 a[168:169],v22 offset:1544
v_xor_b32 v21,131072,v21
v_mfma_f32_16x16x32_bf16 v[152:155],a[132:135],a[220:223],v[152:155]
s_sub_u32 s53,s53,1
buffer_load_dwordx4 v57,s[32:35],s56 offen lds
v_mfma_f32_16x16x32_bf16 v[152:155],a[128:131],a[216:219],v[152:155]
s_cmp_ge_u32 s58,131072
s_cselect_b32 s62,98304,0
ds_read_b64_tr_b16 a[170:171],v22 offset:3592
v_mfma_f32_16x16x32_bf16 v[132:135],a[136:139],a[192:195],v[132:135]
s_sub_u32 s58,s58,s62
ds_read_b64_tr_b16 a[172:173],v22 offset:17928
v_mfma_f32_16x16x32_bf16 v[132:135],a[140:143],a[196:199],v[132:135]
s_addk_i32 m0,1024
ds_read_b64_tr_b16 a[174:175],v22 offset:19976
v_mfma_f32_16x16x32_bf16 v[140:143],a[140:143],a[204:207],v[140:143]
ds_read_b64_tr_b16 a[176:177],v22 offset:1792
v_mfma_f32_16x16x32_bf16 v[140:143],a[136:139],a[200:203],v[140:143]
ds_read_b64_tr_b16 a[178:179],v22 offset:3840
v_mfma_f32_16x16x32_bf16 v[148:151],a[136:139],a[208:211],v[148:151]
buffer_load_dwordx4 v58,s[32:35],s56 offen lds
v_mfma_f32_16x16x32_bf16 v[148:151],a[140:143],a[212:215],v[148:151]
ds_read_b64_tr_b16 a[180:181],v22 offset:18176
v_mfma_f32_16x16x32_bf16 v[156:159],a[140:143],a[220:223],v[156:159]
s_cmp_eq_u32 s86,s87
s_addc_u32 s79,s79,0
s_addk_i32 m0,1024
v_mfma_f32_16x16x32_bf16 v[156:159],a[136:139],a[216:219],v[156:159]
ds_read_b64_tr_b16 a[182:183],v22 offset:20224
v_mfma_f32_16x16x32_bf16 v[160:163],a[144:147],a[192:195],v[160:163]
ds_read_b64_tr_b16 a[184:185],v22 offset:1800
v_mfma_f32_16x16x32_bf16 v[160:163],a[148:151],a[196:199],v[160:163]
s_cmp_eq_u32 s86,s87
s_addc_u32 s79,s79,0
ds_read_b64_tr_b16 a[186:187],v22 offset:3848
v_mfma_f32_16x16x32_bf16 v[168:171],a[148:151],a[204:207],v[168:171]
buffer_load_dwordx4 v59,s[32:35],s56 offen lds
v_mfma_f32_16x16x32_bf16 v[168:171],a[144:147],a[200:203],v[168:171]
ds_read_b64_tr_b16 a[188:189],v22 offset:18184
v_mfma_f32_16x16x32_bf16 v[176:179],a[144:147],a[208:211],v[176:179]
s_cmp_eq_u32 s86,s87
s_addc_u32 s79,s79,0
s_addk_i32 m0,1024
v_mfma_f32_16x16x32_bf16 v[176:179],a[148:151],a[212:215],v[176:179]
ds_read_b64_tr_b16 a[190:191],v22 offset:20232
v_mfma_f32_16x16x32_bf16 v[184:187],a[148:151],a[220:223],v[184:187]
ds_read_b64_tr_b16 a[128:129],v22
v_mfma_f32_16x16x32_bf16 v[184:187],a[144:147],a[216:219],v[184:187]
s_cmp_eq_u32 s86,s87
s_addc_u32 s79,s79,0
ds_read_b64_tr_b16 a[130:131],v22 offset:2048
v_mfma_f32_16x16x32_bf16 v[164:167],a[152:155],a[192:195],v[164:167]
s_waitcnt lgkmcnt(5)
buffer_load_dwordx4 v60,s[32:35],s56 offen lds
v_mfma_f32_16x16x32_bf16 v[164:167],a[156:159],a[196:199],v[164:167]
s_addk_i32 m0,1024
ds_read_b64_tr_b16 a[132:133],v22 offset:16384
v_mfma_f32_16x16x32_bf16 v[172:175],a[156:159],a[204:207],v[172:175]
s_cmp_eq_u32 s86,s87
s_addc_u32 s79,s79,0
ds_read_b64_tr_b16 a[134:135],v22 offset:18432
v_mfma_f32_16x16x32_bf16 v[172:175],a[152:155],a[200:203],v[172:175]
ds_read_b64_tr_b16 a[136:137],v22 offset:8
v_mfma_f32_16x16x32_bf16 v[180:183],a[152:155],a[208:211],v[180:183]
buffer_load_dwordx4 v61,s[32:35],s56 offen lds
v_mfma_f32_16x16x32_bf16 v[180:183],a[156:159],a[212:215],v[180:183]
s_cmp_eq_u32 s86,s87
s_addc_u32 s79,s79,0
ds_read_b64_tr_b16 a[138:139],v22 offset:2056
v_mfma_f32_16x16x32_bf16 v[188:191],a[156:159],a[220:223],v[188:191]
ds_read_b64_tr_b16 a[140:141],v22 offset:16392
v_mfma_f32_16x16x32_bf16 v[188:191],a[152:155],a[216:219],v[188:191]
s_addk_i32 m0,1024
ds_read_b64_tr_b16 a[142:143],v22 offset:18440
v_mfma_f32_16x16x32_bf16 v[192:195],a[160:163],a[192:195],v[192:195]
s_cmp_eq_u32 s86,s87
s_addc_u32 s79,s79,0
ds_read_b64_tr_b16 a[144:145],v22 offset:256
v_mfma_f32_16x16x32_bf16 v[192:195],a[164:167],a[196:199],v[192:195]
ds_read_b64_tr_b16 a[146:147],v22 offset:2304
v_mfma_f32_16x16x32_bf16 v[200:203],a[164:167],a[204:207],v[200:203]
buffer_load_dwordx4 v62,s[32:35],s56 offen lds
v_mfma_f32_16x16x32_bf16 v[200:203],a[160:163],a[200:203],v[200:203]
s_cmp_eq_u32 s86,s87
s_addc_u32 s79,s79,0
ds_read_b64_tr_b16 a[148:149],v22 offset:16640
v_mfma_f32_16x16x32_bf16 v[208:211],a[160:163],a[208:211],v[208:211]
s_addk_i32 m0,1024
ds_read_b64_tr_b16 a[150:151],v22 offset:18688
v_mfma_f32_16x16x32_bf16 v[208:211],a[164:167],a[212:215],v[208:211]
ds_read_b64_tr_b16 a[152:153],v22 offset:264
v_mfma_f32_16x16x32_bf16 v[216:219],a[164:167],a[220:223],v[216:219]
s_cmp_eq_u32 s86,s87
s_addc_u32 s79,s79,0
ds_read_b64_tr_b16 a[154:155],v22 offset:2312
v_mfma_f32_16x16x32_bf16 v[216:219],a[160:163],a[216:219],v[216:219]
buffer_load_dwordx4 v63,s[32:35],s56 offen lds
v_mfma_f32_16x16x32_bf16 v[196:199],a[168:171],a[192:195],v[196:199]
s_add_u32 s56,s56,524288
ds_read_b64_tr_b16 a[156:157],v22 offset:16648
v_mfma_f32_16x16x32_bf16 v[196:199],a[172:175],a[196:199],v[196:199]
s_cmp_eq_u32 s86,s87
s_addc_u32 s79,s79,0
ds_read_b64_tr_b16 a[158:159],v22 offset:18696
v_mfma_f32_16x16x32_bf16 v[204:207],a[172:175],a[204:207],v[204:207]
s_add_u32 m0,s59,0
s_xor_b32 s59,s59,131072
ds_read_b64_tr_b16 a[160:161],v22 offset:512
v_mfma_f32_16x16x32_bf16 v[204:207],a[168:171],a[200:203],v[204:207]
ds_read_b64_tr_b16 a[162:163],v22 offset:2560
v_mfma_f32_16x16x32_bf16 v[212:215],a[168:171],a[208:211],v[212:215]
s_cmp_eq_u32 s86,s87
s_addc_u32 s79,s79,0
ds_read_b64_tr_b16 a[164:165],v22 offset:16896
v_mfma_f32_16x16x32_bf16 v[212:215],a[172:175],a[212:215],v[212:215]
buffer_load_dwordx4 v48,s[28:31],s57 offen lds
v_mfma_f32_16x16x32_bf16 v[220:223],a[172:175],a[220:223],v[220:223]
ds_read_b64_tr_b16 a[166:167],v22 offset:18944
v_mfma_f32_16x16x32_bf16 v[220:223],a[168:171],a[216:219],v[220:223]
s_cmp_eq_u32 s86,s87
s_addc_u32 s79,s79,0
ds_read_b64_tr_b16 a[168:169],v22 offset:520
v_mfma_f32_16x16x32_bf16 v[224:227],a[176:179],a[192:195],v[224:227]
ds_read_b64_tr_b16 a[170:171],v22 offset:2568
v_mfma_f32_16x16x32_bf16 v[224:227],a[180:183],a[196:199],v[224:227]
ds_read_b64_tr_b16 a[172:173],v22 offset:16904
v_mfma_f32_16x16x32_bf16 v[232:235],a[180:183],a[204:207],v[232:235]
s_cmp_eq_u32 s86,s87
s_addc_u32 s79,s79,0
ds_read_b64_tr_b16 a[174:175],v22 offset:18952
v_mfma_f32_16x16x32_bf16 v[232:235],a[176:179],a[200:203],v[232:235]
buffer_load_dwordx4 v49,s[28:31],s57 offen offset:1024 lds
v_mfma_f32_16x16x32_bf16 v[240:243],a[176:179],a[208:211],v[240:243]
v_mfma_f32_16x16x32_bf16 v[240:243],a[180:183],a[212:215],v[240:243]
s_cmp_eq_u32 s86,s87
s_addc_u32 s79,s79,0
v_mfma_f32_16x16x32_bf16 v[248:251],a[180:183],a[220:223],v[248:251]
ds_read_b64_tr_b16 a[180:181],v22 offset:17152
v_mfma_f32_16x16x32_bf16 v[248:251],a[176:179],a[216:219],v[248:251]
s_waitcnt lgkmcnt(4)
ds_read_b64_tr_b16 a[176:177],v22 offset:768
v_mfma_f32_16x16x32_bf16 v[228:231],a[184:187],a[192:195],v[228:231]
s_cmp_eq_u32 s86,s87
s_addc_u32 s79,s79,0
ds_read_b64_tr_b16 a[178:179],v22 offset:2816
v_mfma_f32_16x16x32_bf16 v[228:231],a[188:191],a[196:199],v[228:231]
buffer_load_dwordx4 v50,s[28:31],s57 offen offset:2048 lds
v_mfma_f32_16x16x32_bf16 v[236:239],a[188:191],a[204:207],v[236:239]
ds_read_b64_tr_b16 a[182:183],v22 offset:19200
v_mfma_f32_16x16x32_bf16 v[236:239],a[184:187],a[200:203],v[236:239]
s_cmp_eq_u32 s86,s87
s_addc_u32 s79,s79,0
v_mfma_f32_16x16x32_bf16 v[244:247],a[184:187],a[208:211],v[244:247]
v_mfma_f32_16x16x32_bf16 v[244:247],a[188:191],a[212:215],v[244:247]
buffer_load_dwordx4 v51,s[28:31],s57 offen offset:3072 lds
v_mfma_f32_16x16x32_bf16 v[252:255],a[188:191],a[220:223],v[252:255]
ds_read_b64_tr_b16 a[188:189],v22 offset:17160
v_mfma_f32_16x16x32_bf16 v[252:255],a[184:187],a[216:219],v[252:255]
ds_read_b64_tr_b16 a[184:185],v22 offset:776
v_mfma_f32_16x16x32_bf16 a[0:3],a[128:131],a[192:195],a[0:3]
s_addk_i32 m0,4096
ds_read_b64_tr_b16 a[186:187],v22 offset:2824
v_mfma_f32_16x16x32_bf16 a[0:3],a[132:135],a[196:199],a[0:3]
ds_read_b64_tr_b16 a[190:191],v22 offset:19208
v_add_u32 v22,s60,v23
v_mfma_f32_16x16x32_bf16 a[32:35],a[132:135],a[204:207],a[32:35]
s_add_u32 s60,s60,32768
s_cmp_eq_u32 s60,131072
s_cselect_b32 s60,32768,s60
v_mfma_f32_16x16x32_bf16 a[32:35],a[128:131],a[200:203],a[32:35]
buffer_load_dwordx4 v52,s[28:31],s57 offen lds
v_mfma_f32_16x16x32_bf16 a[64:67],a[128:131],a[208:211],a[64:67]
v_mfma_f32_16x16x32_bf16 a[64:67],a[132:135],a[212:215],a[64:67]
v_mfma_f32_16x16x32_bf16 a[96:99],a[132:135],a[220:223],a[96:99]
v_mfma_f32_16x16x32_bf16 a[96:99],a[128:131],a[216:219],a[96:99]
v_mfma_f32_16x16x32_bf16 a[4:7],a[136:139],a[192:195],a[4:7]
buffer_load_dwordx4 v53,s[28:31],s57 offen offset:1024 lds
v_mfma_f32_16x16x32_bf16 a[4:7],a[140:143],a[196:199],a[4:7]
v_mfma_f32_16x16x32_bf16 a[36:39],a[140:143],a[204:207],a[36:39]
v_mfma_f32_16x16x32_bf16 a[36:39],a[136:139],a[200:203],a[36:39]
v_mfma_f32_16x16x32_bf16 a[68:71],a[136:139],a[208:211],a[68:71]
v_mfma_f32_16x16x32_bf16 a[68:71],a[140:143],a[212:215],a[68:71]
v_mfma_f32_16x16x32_bf16 a[100:103],a[140:143],a[220:223],a[100:103]
buffer_load_dwordx4 v54,s[28:31],s57 offen offset:2048 lds
v_mfma_f32_16x16x32_bf16 a[100:103],a[136:139],a[216:219],a[100:103]
v_mfma_f32_16x16x32_bf16 a[8:11],a[144:147],a[192:195],a[8:11]
v_mfma_f32_16x16x32_bf16 a[8:11],a[148:151],a[196:199],a[8:11]
)A"
R"A(v_mfma_f32_16x16x32_bf16 a[40:43],a[148:151],a[204:207],a[40:43]
v_mfma_f32_16x16x32_bf16 a[40:43],a[144:147],a[200:203],a[40:43]
buffer_load_dwordx4 v55,s[28:31],s57 offen offset:3072 lds
v_mfma_f32_16x16x32_bf16 a[72:75],a[144:147],a[208:211],a[72:75]
s_addk_i32 s57,128
v_mfma_f32_16x16x32_bf16 a[72:75],a[148:151],a[212:215],a[72:75]
v_mfma_f32_16x16x32_bf16 a[104:107],a[148:151],a[220:223],a[104:107]
v_mfma_f32_16x16x32_bf16 a[104:107],a[144:147],a[216:219],a[104:107]
v_mfma_f32_16x16x32_bf16 a[12:15],a[152:155],a[192:195],a[12:15]
v_mfma_f32_16x16x32_bf16 a[12:15],a[156:159],a[196:199],a[12:15]
v_mfma_f32_16x16x32_bf16 a[44:47],a[156:159],a[204:207],a[44:47]
buffer_load_dword v16,v15,s[72:75],s83 offen
v_mfma_f32_16x16x32_bf16 a[44:47],a[152:155],a[200:203],a[44:47]
s_addk_i32 s83,896
v_mfma_f32_16x16x32_bf16 a[76:79],a[152:155],a[208:211],a[76:79]
v_mfma_f32_16x16x32_bf16 a[76:79],a[156:159],a[212:215],a[76:79]
v_mfma_f32_16x16x32_bf16 a[108:111],a[156:159],a[220:223],a[108:111]
v_mfma_f32_16x16x32_bf16 a[108:111],a[152:155],a[216:219],a[108:111]
s_waitcnt vmcnt(17) lgkmcnt(0)
v_mfma_f32_16x16x32_bf16 a[16:19],a[160:163],a[192:195],a[16:19]
s_barrier
ds_read_b128 a[224:227],v20
v_mfma_f32_16x16x32_bf16 a[16:19],a[164:167],a[196:199],a[16:19]
ds_read_b64_tr_b16 a[128:129],v22 offset:1024
v_mfma_f32_16x16x32_bf16 a[48:51],a[164:167],a[204:207],a[48:51]
ds_read_b64_tr_b16 a[130:131],v22 offset:3072
v_mfma_f32_16x16x32_bf16 a[48:51],a[160:163],a[200:203],a[48:51]
ds_read_b128 a[228:231],v21
v_mfma_f32_16x16x32_bf16 a[80:83],a[160:163],a[208:211],a[80:83]
ds_read_b64_tr_b16 a[132:133],v22 offset:17408
v_mfma_f32_16x16x32_bf16 a[80:83],a[164:167],a[212:215],a[80:83]
ds_read_b64_tr_b16 a[134:135],v22 offset:19456
v_mfma_f32_16x16x32_bf16 a[112:115],a[164:167],a[220:223],a[112:115]
ds_read_b128 a[232:235],v20 offset:2048
v_mfma_f32_16x16x32_bf16 a[112:115],a[160:163],a[216:219],a[112:115]
ds_read_b128 a[236:239],v21 offset:2048
v_mfma_f32_16x16x32_bf16 a[20:23],a[168:171],a[192:195],a[20:23]
ds_read_b128 a[240:243],v20 offset:4096
v_mfma_f32_16x16x32_bf16 a[20:23],a[172:175],a[196:199],a[20:23]
ds_read_b128 a[244:247],v21 offset:4096
v_mfma_f32_16x16x32_bf16 a[52:55],a[172:175],a[204:207],a[52:55]
ds_read_b128 a[248:251],v20 offset:6144
v_xor_b32 v20,131072,v20
v_mfma_f32_16x16x32_bf16 a[52:55],a[168:171],a[200:203],a[52:55]
ds_read_b128 a[252:255],v21 offset:6144
v_xor_b32 v21,131072,v21
v_mfma_f32_16x16x32_bf16 a[84:87],a[168:171],a[208:211],a[84:87]
ds_read_b64_tr_b16 a[136:137],v22 offset:1032
v_mfma_f32_16x16x32_bf16 a[84:87],a[172:175],a[212:215],a[84:87]
ds_read_b64_tr_b16 a[138:139],v22 offset:3080
v_mfma_f32_16x16x32_bf16 a[116:119],a[172:175],a[220:223],a[116:119]
ds_read_b64_tr_b16 a[140:141],v22 offset:17416
v_mfma_f32_16x16x32_bf16 a[116:119],a[168:171],a[216:219],a[116:119]
ds_read_b64_tr_b16 a[142:143],v22 offset:19464
v_mfma_f32_16x16x32_bf16 a[24:27],a[176:179],a[192:195],a[24:27]
ds_read_b64_tr_b16 a[144:145],v22 offset:1280
v_mfma_f32_16x16x32_bf16 a[24:27],a[180:183],a[196:199],a[24:27]
ds_read_b64_tr_b16 a[146:147],v22 offset:3328
v_mfma_f32_16x16x32_bf16 a[56:59],a[180:183],a[204:207],a[56:59]
ds_read_b64_tr_b16 a[148:149],v22 offset:17664
v_mfma_f32_16x16x32_bf16 a[56:59],a[176:179],a[200:203],a[56:59]
ds_read_b64_tr_b16 a[150:151],v22 offset:19712
v_mfma_f32_16x16x32_bf16 a[88:91],a[176:179],a[208:211],a[88:91]
ds_read_b64_tr_b16 a[152:153],v22 offset:1288
v_mfma_f32_16x16x32_bf16 a[88:91],a[180:183],a[212:215],a[88:91]
ds_read_b64_tr_b16 a[154:155],v22 offset:3336
v_mfma_f32_16x16x32_bf16 a[120:123],a[180:183],a[220:223],a[120:123]
ds_read_b64_tr_b16 a[156:157],v22 offset:17672
v_mfma_f32_16x16x32_bf16 a[120:123],a[176:179],a[216:219],a[120:123]
ds_read_b64_tr_b16 a[158:159],v22 offset:19720
v_mfma_f32_16x16x32_bf16 a[28:31],a[184:187],a[192:195],a[28:31]
ds_read_b64_tr_b16 a[160:161],v22 offset:1536
v_mfma_f32_16x16x32_bf16 a[28:31],a[188:191],a[196:199],a[28:31]
ds_read_b64_tr_b16 a[162:163],v22 offset:3584
v_mfma_f32_16x16x32_bf16 a[60:63],a[188:191],a[204:207],a[60:63]
ds_read_b64_tr_b16 a[164:165],v22 offset:17920
v_mfma_f32_16x16x32_bf16 a[60:63],a[184:187],a[200:203],a[60:63]
ds_read_b64_tr_b16 a[166:167],v22 offset:19968
v_mfma_f32_16x16x32_bf16 a[92:95],a[184:187],a[208:211],a[92:95]
ds_read_b64_tr_b16 a[168:169],v22 offset:1544
v_mfma_f32_16x16x32_bf16 a[92:95],a[188:191],a[212:215],a[92:95]
ds_read_b64_tr_b16 a[170:171],v22 offset:3592
v_mfma_f32_16x16x32_bf16 a[124:127],a[188:191],a[220:223],a[124:127]
s_add_u32 m0,s58,0
s_add_u32 s58,s58,32768
ds_read_b64_tr_b16 a[172:173],v22 offset:17928
v_mfma_f32_16x16x32_bf16 a[124:127],a[184:187],a[216:219],a[124:127]
s_cmp_ge_u32 s58,131072
s_cselect_b32 s62,98304,0
ds_read_b64_tr_b16 a[174:175],v22 offset:19976
s_waitcnt lgkmcnt(8)
v_mfma_f32_16x16x32_bf16 v[128:131],a[128:131],a[224:227],v[128:131]
s_mov_b32 s86,s87
s_sub_u32 s58,s58,s62
ds_read_b64_tr_b16 a[176:177],v22 offset:1792
v_mfma_f32_16x16x32_bf16 v[128:131],a[132:135],a[228:231],v[128:131]
buffer_load_dwordx4 v56,s[32:35],s56 offen lds
v_mfma_f32_16x16x32_bf16 v[136:139],a[132:135],a[236:239],v[136:139]
ds_read_b64_tr_b16 a[178:179],v22 offset:3840
v_mfma_f32_16x16x32_bf16 v[136:139],a[128:131],a[232:235],v[136:139]
s_load_dword s86,s[84:85],0x2000 glc
s_addk_i32 m0,1024
ds_read_b64_tr_b16 a[180:181],v22 offset:18176
v_mfma_f32_16x16x32_bf16 v[144:147],a[128:131],a[240:243],v[144:147]
s_addk_i32 s84,256
ds_read_b64_tr_b16 a[182:183],v22 offset:20224
v_mfma_f32_16x16x32_bf16 v[144:147],a[132:135],a[244:247],v[144:147]
ds_read_b64_tr_b16 a[184:185],v22 offset:1800
v_mfma_f32_16x16x32_bf16 v[152:155],a[132:135],a[252:255],v[152:155]
buffer_load_dwordx4 v57,s[32:35],s56 offen lds
v_mfma_f32_16x16x32_bf16 v[152:155],a[128:131],a[248:251],v[152:155]
ds_read_b64_tr_b16 a[186:187],v22 offset:3848
v_mfma_f32_16x16x32_bf16 v[132:135],a[136:139],a[224:227],v[132:135]
ds_read_b64_tr_b16 a[188:189],v22 offset:18184
v_mfma_f32_16x16x32_bf16 v[132:135],a[140:143],a[228:231],v[132:135]
s_addk_i32 m0,1024
ds_read_b64_tr_b16 a[190:191],v22 offset:20232
v_mfma_f32_16x16x32_bf16 v[140:143],a[140:143],a[236:239],v[140:143]
ds_read_b64_tr_b16 a[128:129],v22
v_mfma_f32_16x16x32_bf16 v[140:143],a[136:139],a[232:235],v[140:143]
ds_read_b64_tr_b16 a[130:131],v22 offset:2048
v_mfma_f32_16x16x32_bf16 v[148:151],a[136:139],a[240:243],v[148:151]
buffer_load_dwordx4 v58,s[32:35],s56 offen lds
v_mfma_f32_16x16x32_bf16 v[148:151],a[140:143],a[244:247],v[148:151]
ds_read_b64_tr_b16 a[132:133],v22 offset:16384
v_mfma_f32_16x16x32_bf16 v[156:159],a[140:143],a[252:255],v[156:159]
s_cmp_eq_u32 s86,s87
s_subb_u32 s79,s79,0
s_addk_i32 m0,1024
v_mfma_f32_16x16x32_bf16 v[156:159],a[136:139],a[248:251],v[156:159]
ds_read_b64_tr_b16 a[134:135],v22 offset:18432
v_mfma_f32_16x16x32_bf16 v[160:163],a[144:147],a[224:227],v[160:163]
ds_read_b64_tr_b16 a[136:137],v22 offset:8
v_mfma_f32_16x16x32_bf16 v[160:163],a[148:151],a[228:231],v[160:163]
s_cmp_eq_u32 s86,s87
s_subb_u32 s79,s79,0
ds_read_b64_tr_b16 a[138:139],v22 offset:2056
v_mfma_f32_16x16x32_bf16 v[168:171],a[148:151],a[236:239],v[168:171]
buffer_load_dwordx4 v59,s[32:35],s56 offen lds
v_mfma_f32_16x16x32_bf16 v[168:171],a[144:147],a[232:235],v[168:171]
ds_read_b64_tr_b16 a[140:141],v22 offset:16392
v_mfma_f32_16x16x32_bf16 v[176:179],a[144:147],a[240:243],v[176:179]
s_cmp_eq_u32 s86,s87
s_subb_u32 s79,s79,0
s_addk_i32 m0,1024
v_mfma_f32_16x16x32_bf16 v[176:179],a[148:151],a[244:247],v[176:179]
ds_read_b64_tr_b16 a[142:143],v22 offset:18440
v_mfma_f32_16x16x32_bf16 v[184:187],a[148:151],a[252:255],v[184:187]
ds_read_b64_tr_b16 a[148:149],v22 offset:16640
v_mfma_f32_16x16x32_bf16 v[184:187],a[144:147],a[248:251],v[184:187]
s_cmp_eq_u32 s86,s87
s_subb_u32 s79,s79,0
ds_read_b64_tr_b16 a[144:145],v22 offset:256
v_mfma_f32_16x16x32_bf16 v[164:167],a[152:155],a[224:227],v[164:167]
buffer_load_dwordx4 v60,s[32:35],s56 offen lds
v_mfma_f32_16x16x32_bf16 v[164:167],a[156:159],a[228:231],v[164:167]
s_addk_i32 m0,1024
ds_read_b64_tr_b16 a[146:147],v22 offset:2304
v_mfma_f32_16x16x32_bf16 v[172:175],a[156:159],a[236:239],v[172:175]
s_cmp_eq_u32 s86,s87
s_subb_u32 s79,s79,0
ds_read_b64_tr_b16 a[150:151],v22 offset:18688
v_mfma_f32_16x16x32_bf16 v[172:175],a[152:155],a[232:235],v[172:175]
v_mfma_f32_16x16x32_bf16 v[180:183],a[152:155],a[240:243],v[180:183]
buffer_load_dwordx4 v61,s[32:35],s56 offen lds
v_mfma_f32_16x16x32_bf16 v[180:183],a[156:159],a[244:247],v[180:183]
s_cmp_eq_u32 s86,s87
s_subb_u32 s79,s79,0
v_mfma_f32_16x16x32_bf16 v[188:191],a[156:159],a[252:255],v[188:191]
ds_read_b64_tr_b16 a[156:157],v22 offset:16648
v_mfma_f32_16x16x32_bf16 v[188:191],a[152:155],a[248:251],v[188:191]
s_addk_i32 m0,1024
ds_read_b64_tr_b16 a[152:153],v22 offset:264
s_waitcnt lgkmcnt(4)
v_mfma_f32_16x16x32_bf16 v[192:195],a[160:163],a[224:227],v[192:195]
s_cmp_eq_u32 s86,s87
s_subb_u32 s79,s79,0
ds_read_b64_tr_b16 a[154:155],v22 offset:2312
v_mfma_f32_16x16x32_bf16 v[192:195],a[164:167],a[228:231],v[192:195]
ds_read_b64_tr_b16 a[158:159],v22 offset:18696
v_mfma_f32_16x16x32_bf16 v[200:203],a[164:167],a[236:239],v[200:203]
buffer_load_dwordx4 v62,s[32:35],s56 offen lds
v_mfma_f32_16x16x32_bf16 v[200:203],a[160:163],a[232:235],v[200:203]
s_cmp_eq_u32 s86,s87
s_subb_u32 s79,s79,0
v_mfma_f32_16x16x32_bf16 v[208:211],a[160:163],a[240:243],v[208:211]
s_addk_i32 m0,1024
v_mfma_f32_16x16x32_bf16 v[208:211],a[164:167],a[244:247],v[208:211]
v_mfma_f32_16x16x32_bf16 v[216:219],a[164:167],a[252:255],v[216:219]
s_cmp_eq_u32 s86,s87
s_subb_u32 s79,s79,0
ds_read_b64_tr_b16 a[164:165],v22 offset:16896
v_mfma_f32_16x16x32_bf16 v[216:219],a[160:163],a[248:251],v[216:219]
buffer_load_dwordx4 v63,s[32:35],s56 offen lds
v_mfma_f32_16x16x32_bf16 v[196:199],a[168:171],a[224:227],v[196:199]
s_add_u32 s56,s56,524288
ds_read_b64_tr_b16 a[160:161],v22 offset:512
v_mfma_f32_16x16x32_bf16 v[196:199],a[172:175],a[228:231],v[196:199]
s_cmp_eq_u32 s86,s87
s_subb_u32 s79,s79,0
ds_read_b64_tr_b16 a[162:163],v22 offset:2560
v_mfma_f32_16x16x32_bf16 v[204:207],a[172:175],a[236:239],v[204:207]
s_add_u32 m0,s59,0
s_xor_b32 s59,s59,131072
ds_read_b64_tr_b16 a[166:167],v22 offset:18944
v_mfma_f32_16x16x32_bf16 v[204:207],a[168:171],a[232:235],v[204:207]
v_mfma_f32_16x16x32_bf16 v[212:215],a[168:171],a[240:243],v[212:215]
s_cmp_eq_u32 s86,s87
s_subb_u32 s79,s79,0
v_mfma_f32_16x16x32_bf16 v[212:215],a[172:175],a[244:247],v[212:215]
buffer_load_dwordx4 v48,s[28:31],s57 offen lds
v_mfma_f32_16x16x32_bf16 v[220:223],a[172:175],a[252:255],v[220:223]
ds_read_b64_tr_b16 a[172:173],v22 offset:16904
v_mfma_f32_16x16x32_bf16 v[220:223],a[168:171],a[248:251],v[220:223]
s_cmp_eq_u32 s86,s87
s_subb_u32 s79,s79,0
ds_read_b64_tr_b16 a[168:169],v22 offset:520
v_mfma_f32_16x16x32_bf16 v[224:227],a[176:179],a[224:227],v[224:227]
ds_read_b64_tr_b16 a[170:171],v22 offset:2568
v_mfma_f32_16x16x32_bf16 v[224:227],a[180:183],a[228:231],v[224:227]
ds_read_b64_tr_b16 a[174:175],v22 offset:18952
v_mfma_f32_16x16x32_bf16 v[232:235],a[180:183],a[236:239],v[232:235]
s_cmp_eq_u32 s86,s87
s_subb_u32 s79,s79,0
v_mfma_f32_16x16x32_bf16 v[232:235],a[176:179],a[232:235],v[232:235]
buffer_load_dwordx4 v49,s[28:31],s57 offen offset:1024 lds
v_mfma_f32_16x16x32_bf16 v[240:243],a[176:179],a[240:243],v[240:243]
v_mfma_f32_16x16x32_bf16 v[240:243],a[180:183],a[244:247],v[240:243]
s_cmp_eq_u32 s86,s87
s_subb_u32 s79,s79,0
v_mfma_f32_16x16x32_bf16 v[248:251],a[180:183],a[252:255],v[248:251]
ds_read_b64_tr_b16 a[180:181],v22 offset:17152
v_mfma_f32_16x16x32_bf16 v[248:251],a[176:179],a[248:251],v[248:251]
ds_read_b64_tr_b16 a[176:177],v22 offset:768
v_mfma_f32_16x16x32_bf16 v[228:231],a[184:187],a[224:227],v[228:231]
s_cmp_eq_u32 s86,s87
s_subb_u32 s79,s79,0
ds_read_b64_tr_b16 a[178:179],v22 offset:2816
v_mfma_f32_16x16x32_bf16 v[228:231],a[188:191],a[228:231],v[228:231]
buffer_load_dwordx4 v50,s[28:31],s57 offen offset:2048 lds
v_mfma_f32_16x16x32_bf16 v[236:239],a[188:191],a[236:239],v[236:239]
ds_read_b64_tr_b16 a[182:183],v22 offset:19200
)A"
R"A(v_mfma_f32_16x16x32_bf16 v[236:239],a[184:187],a[232:235],v[236:239]
s_cmp_eq_u32 s86,s87
s_subb_u32 s79,s79,0
v_mfma_f32_16x16x32_bf16 v[244:247],a[184:187],a[240:243],v[244:247]
v_mfma_f32_16x16x32_bf16 v[244:247],a[188:191],a[244:247],v[244:247]
buffer_load_dwordx4 v51,s[28:31],s57 offen offset:3072 lds
v_mfma_f32_16x16x32_bf16 v[252:255],a[188:191],a[252:255],v[252:255]
ds_read_b64_tr_b16 a[188:189],v22 offset:17160
v_mfma_f32_16x16x32_bf16 v[252:255],a[184:187],a[248:251],v[252:255]
ds_read_b64_tr_b16 a[184:185],v22 offset:776
v_mfma_f32_16x16x32_bf16 a[0:3],a[128:131],a[224:227],a[0:3]
s_addk_i32 m0,4096
ds_read_b64_tr_b16 a[186:187],v22 offset:2824
v_mfma_f32_16x16x32_bf16 a[0:3],a[132:135],a[228:231],a[0:3]
ds_read_b64_tr_b16 a[190:191],v22 offset:19208
v_add_u32 v22,s60,v23
v_mfma_f32_16x16x32_bf16 a[32:35],a[132:135],a[236:239],a[32:35]
v_mfma_f32_16x16x32_bf16 a[32:35],a[128:131],a[232:235],a[32:35]
buffer_load_dwordx4 v52,s[28:31],s57 offen lds
v_mfma_f32_16x16x32_bf16 a[64:67],a[128:131],a[240:243],a[64:67]
v_mfma_f32_16x16x32_bf16 a[64:67],a[132:135],a[244:247],a[64:67]
v_mfma_f32_16x16x32_bf16 a[96:99],a[132:135],a[252:255],a[96:99]
v_mfma_f32_16x16x32_bf16 a[96:99],a[128:131],a[248:251],a[96:99]
v_mfma_f32_16x16x32_bf16 a[4:7],a[136:139],a[224:227],a[4:7]
buffer_load_dwordx4 v53,s[28:31],s57 offen offset:1024 lds
v_mfma_f32_16x16x32_bf16 a[4:7],a[140:143],a[228:231],a[4:7]
v_mfma_f32_16x16x32_bf16 a[36:39],a[140:143],a[236:239],a[36:39]
v_mfma_f32_16x16x32_bf16 a[36:39],a[136:139],a[232:235],a[36:39]
v_mfma_f32_16x16x32_bf16 a[68:71],a[136:139],a[240:243],a[68:71]
v_mfma_f32_16x16x32_bf16 a[68:71],a[140:143],a[244:247],a[68:71]
v_mfma_f32_16x16x32_bf16 a[100:103],a[140:143],a[252:255],a[100:103]
buffer_load_dwordx4 v54,s[28:31],s57 offen offset:2048 lds
v_mfma_f32_16x16x32_bf16 a[100:103],a[136:139],a[248:251],a[100:103]
s_waitcnt lgkmcnt(0)
v_mfma_f32_16x16x32_bf16 a[8:11],a[144:147],a[224:227],a[8:11]
v_mfma_f32_16x16x32_bf16 a[8:11],a[148:151],a[228:231],a[8:11]
v_mfma_f32_16x16x32_bf16 a[40:43],a[148:151],a[236:239],a[40:43]
v_mfma_f32_16x16x32_bf16 a[40:43],a[144:147],a[232:235],a[40:43]
buffer_load_dwordx4 v55,s[28:31],s57 offen offset:3072 lds
v_mfma_f32_16x16x32_bf16 a[72:75],a[144:147],a[240:243],a[72:75]
s_addk_i32 s57,128
v_mfma_f32_16x16x32_bf16 a[72:75],a[148:151],a[244:247],a[72:75]
v_mfma_f32_16x16x32_bf16 a[104:107],a[148:151],a[252:255],a[104:107]
v_mfma_f32_16x16x32_bf16 a[104:107],a[144:147],a[248:251],a[104:107]
v_mfma_f32_16x16x32_bf16 a[12:15],a[152:155],a[224:227],a[12:15]
v_mfma_f32_16x16x32_bf16 a[12:15],a[156:159],a[228:231],a[12:15]
v_mfma_f32_16x16x32_bf16 a[44:47],a[156:159],a[236:239],a[44:47]
buffer_load_dword v16,v15,s[72:75],s83 offen
v_mfma_f32_16x16x32_bf16 a[44:47],a[152:155],a[232:235],a[44:47]
s_addk_i32 s83,896
v_mfma_f32_16x16x32_bf16 a[76:79],a[152:155],a[240:243],a[76:79]
v_mfma_f32_16x16x32_bf16 a[76:79],a[156:159],a[244:247],a[76:79]
v_mfma_f32_16x16x32_bf16 a[108:111],a[156:159],a[252:255],a[108:111]
v_mfma_f32_16x16x32_bf16 a[108:111],a[152:155],a[248:251],a[108:111]
s_waitcnt vmcnt(17) lgkmcnt(0)
v_mfma_f32_16x16x32_bf16 a[16:19],a[160:163],a[224:227],a[16:19]
s_barrier
ds_read_b128 a[192:195],v20
v_mfma_f32_16x16x32_bf16 a[16:19],a[164:167],a[228:231],a[16:19]
ds_read_b128 a[196:199],v21
v_mfma_f32_16x16x32_bf16 a[48:51],a[164:167],a[236:239],a[48:51]
ds_read_b128 a[200:203],v20 offset:2048
v_mfma_f32_16x16x32_bf16 a[48:51],a[160:163],a[232:235],a[48:51]
ds_read_b128 a[204:207],v21 offset:2048
v_mfma_f32_16x16x32_bf16 a[80:83],a[160:163],a[240:243],a[80:83]
ds_read_b128 a[208:211],v20 offset:4096
v_mfma_f32_16x16x32_bf16 a[80:83],a[164:167],a[244:247],a[80:83]
ds_read_b128 a[212:215],v21 offset:4096
v_mfma_f32_16x16x32_bf16 a[112:115],a[164:167],a[252:255],a[112:115]
buffer_load_dword v26,v24,s[88:91],s93 offen
v_mfma_f32_16x16x32_bf16 a[112:115],a[160:163],a[248:251],a[112:115]
ds_read_b128 a[216:219],v20 offset:6144
v_mfma_f32_16x16x32_bf16 a[20:23],a[168:171],a[224:227],a[20:23]
ds_read_b128 a[220:223],v21 offset:6144
v_mfma_f32_16x16x32_bf16 a[20:23],a[172:175],a[228:231],a[20:23]
ds_read_b64_tr_b16 a[128:129],v22 offset:1024
v_mfma_f32_16x16x32_bf16 a[52:55],a[172:175],a[236:239],a[52:55]
ds_read_b64_tr_b16 a[130:131],v22 offset:3072
v_mfma_f32_16x16x32_bf16 a[52:55],a[168:171],a[232:235],a[52:55]
ds_read_b64_tr_b16 a[132:133],v22 offset:17408
v_mfma_f32_16x16x32_bf16 a[84:87],a[168:171],a[240:243],a[84:87]
ds_read_b64_tr_b16 a[134:135],v22 offset:19456
v_mfma_f32_16x16x32_bf16 a[84:87],a[172:175],a[244:247],a[84:87]
ds_read_b64_tr_b16 a[136:137],v22 offset:1032
v_mfma_f32_16x16x32_bf16 a[116:119],a[172:175],a[252:255],a[116:119]
ds_read_b64_tr_b16 a[138:139],v22 offset:3080
v_mfma_f32_16x16x32_bf16 a[116:119],a[168:171],a[248:251],a[116:119]
ds_read_b64_tr_b16 a[140:141],v22 offset:17416
v_mfma_f32_16x16x32_bf16 a[24:27],a[176:179],a[224:227],a[24:27]
ds_read_b64_tr_b16 a[142:143],v22 offset:19464
v_mfma_f32_16x16x32_bf16 a[24:27],a[180:183],a[228:231],a[24:27]
ds_read_b64_tr_b16 a[144:145],v22 offset:1280
v_mfma_f32_16x16x32_bf16 a[56:59],a[180:183],a[236:239],a[56:59]
ds_read_b64_tr_b16 a[146:147],v22 offset:3328
v_mfma_f32_16x16x32_bf16 a[56:59],a[176:179],a[232:235],a[56:59]
ds_read_b64_tr_b16 a[148:149],v22 offset:17664
v_mfma_f32_16x16x32_bf16 a[88:91],a[176:179],a[240:243],a[88:91]
ds_read_b64_tr_b16 a[150:151],v22 offset:19712
v_mfma_f32_16x16x32_bf16 a[88:91],a[180:183],a[244:247],a[88:91]
ds_read_b64_tr_b16 a[152:153],v22 offset:1288
v_mfma_f32_16x16x32_bf16 a[120:123],a[180:183],a[252:255],a[120:123]
ds_read_b64_tr_b16 a[154:155],v22 offset:3336
v_mfma_f32_16x16x32_bf16 a[120:123],a[176:179],a[248:251],a[120:123]
ds_read_b64_tr_b16 a[156:157],v22 offset:17672
v_mfma_f32_16x16x32_bf16 a[28:31],a[184:187],a[224:227],a[28:31]
ds_read_b64_tr_b16 a[158:159],v22 offset:19720
v_mfma_f32_16x16x32_bf16 a[28:31],a[188:191],a[228:231],a[28:31]
v_mfma_f32_16x16x32_bf16 a[60:63],a[188:191],a[236:239],a[60:63]
v_mfma_f32_16x16x32_bf16 a[60:63],a[184:187],a[232:235],a[60:63]
v_mfma_f32_16x16x32_bf16 a[92:95],a[184:187],a[240:243],a[92:95]
v_mfma_f32_16x16x32_bf16 a[92:95],a[188:191],a[244:247],a[92:95]
v_mfma_f32_16x16x32_bf16 a[124:127],a[188:191],a[252:255],a[124:127]
s_cmp_lg_u32 s53,0
v_mfma_f32_16x16x32_bf16 a[124:127],a[184:187],a[248:251],a[124:127]
s_cbranch_scc1 L_qp_%=
L_qpx_%=:
L_tailseg_%=:
s_waitcnt lgkmcnt(1)
v_mfma_f32_16x16x32_bf16 v[128:131],a[128:131],a[192:195],v[128:131]
s_add_u32 m0,98304,s51
ds_read_b64_tr_b16 a[160:161],v22 offset:1536
v_xor_b32 v20,131072,v20
v_mfma_f32_16x16x32_bf16 v[128:131],a[132:135],a[196:199],v[128:131]
s_addk_i32 s54,16384
buffer_load_dwordx4 v4,s[20:23],s47 offen nt lds
v_mfma_f32_16x16x32_bf16 v[136:139],a[132:135],a[204:207],v[136:139]
s_and_b32 s46,s54,0xfffc0000
ds_read_b64_tr_b16 a[162:163],v22 offset:3584
v_xor_b32 v21,131072,v21
v_mfma_f32_16x16x32_bf16 v[136:139],a[128:131],a[200:203],v[136:139]
s_addk_i32 s54,16384
ds_read_b64_tr_b16 a[164:165],v22 offset:17920
v_and_b32 v18,48,v6
v_mfma_f32_16x16x32_bf16 v[144:147],a[128:131],a[208:211],v[144:147]
s_mov_b32 s48,-98304
ds_read_b64_tr_b16 a[166:167],v22 offset:19968
v_lshrrev_b32 v19,4,v6
v_mfma_f32_16x16x32_bf16 v[144:147],a[132:135],a[212:215],v[144:147]
s_movk_i32 s60,141
ds_read_b64_tr_b16 a[168:169],v22 offset:1544
v_lshlrev_b32 v18,2,v18
v_mfma_f32_16x16x32_bf16 v[152:155],a[132:135],a[220:223],v[152:155]
s_mul_i32 s83,s41,5
ds_read_b64_tr_b16 a[170:171],v22 offset:3592
v_and_b32 v19,48,v19
v_mfma_f32_16x16x32_bf16 v[152:155],a[128:131],a[216:219],v[152:155]
s_add_u32 s83,s83,s42
ds_read_b64_tr_b16 a[172:173],v22 offset:17928
v_add_u32 v18,v18,v19
v_mfma_f32_16x16x32_bf16 v[132:135],a[136:139],a[192:195],v[132:135]
s_add_u32 s83,s83,15
ds_read_b64_tr_b16 a[174:175],v22 offset:19976
v_add_u32 v18,98304,v18
v_mfma_f32_16x16x32_bf16 v[132:135],a[140:143],a[196:199],v[132:135]
s_cmp_gt_i32 s79,0
s_cselect_b32 s86,8192,0
ds_read_b64_tr_b16 a[176:177],v22 offset:1792
v_mfma_f32_16x16x32_bf16 v[140:143],a[140:143],a[204:207],v[140:143]
s_lshr_b32 s79,%1,3
v_readfirstlane_b32 s68,v13
s_nop 1
s_add_u32 s68,s68,1
v_mfma_f32_16x16x32_bf16 v[140:143],a[136:139],a[200:203],v[140:143]
s_mul_i32 s79,s79,4
s_lshr_b32 s79,s79,5
ds_read_b64_tr_b16 a[178:179],v22 offset:3840
v_mfma_f32_16x16x32_bf16 v[148:151],a[136:139],a[208:211],v[148:151]
s_sub_u32 s60,s60,s79
s_lshl_b32 s94,s60,2
ds_read_b64_tr_b16 a[180:181],v22 offset:18176
v_mfma_f32_16x16x32_bf16 v[148:151],a[140:143],a[212:215],v[148:151]
s_sub_u32 s94,s94,3
buffer_load_dwordx4 v4,s[20:23],s47 offen offset:1024 nt lds
v_mfma_f32_16x16x32_bf16 v[156:159],a[140:143],a[220:223],v[156:159]
s_lshl_b32 s43,s83,14
s_lshl_b32 s83,s83,12
ds_read_b64_tr_b16 a[182:183],v22 offset:20224
v_mfma_f32_16x16x32_bf16 v[156:159],a[136:139],a[216:219],v[156:159]
s_lshl_b32 s44,s40,4
s_and_b32 s43,s43,0x20000
ds_read_b64_tr_b16 a[184:185],v22 offset:1800
v_mfma_f32_16x16x32_bf16 v[160:163],a[144:147],a[192:195],v[160:163]
s_and_b32 s83,s83,0x7000
s_xor_b32 s69,s68,0x9e3779b9
ds_read_b64_tr_b16 a[186:187],v22 offset:3848
v_mfma_f32_16x16x32_bf16 v[160:163],a[148:151],a[196:199],v[160:163]
s_lshl_b32 s80,s40,10
s_and_b32 s44,s44,32
ds_read_b64_tr_b16 a[188:189],v22 offset:18184
v_mfma_f32_16x16x32_bf16 v[168:171],a[148:151],a[204:207],v[168:171]
s_or_b32 s83,s83,s43
s_and_b32 s69,s69,-2
ds_read_b64_tr_b16 a[190:191],v22 offset:20232
v_mfma_f32_16x16x32_bf16 v[168:171],a[144:147],a[200:203],v[168:171]
s_add_u32 s55,s55,s86
s_add_u32 s93,s92,L_att0x_%=-L_pc_%=
ds_read_b64_tr_b16 a[128:129],v22
v_mfma_f32_16x16x32_bf16 v[176:179],a[144:147],a[208:211],v[176:179]
s_mov_b32 s50,32768
s_mov_b32 s49,98304
ds_read_b64_tr_b16 a[130:131],v22 offset:2048
v_mfma_f32_16x16x32_bf16 v[176:179],a[148:151],a[212:215],v[176:179]
s_mov_b32 s45,0x1249
ds_read_b64_tr_b16 a[132:133],v22 offset:16384
v_xor_b32 v25,s44,v4
v_mfma_f32_16x16x32_bf16 v[184:187],a[148:151],a[220:223],v[184:187]
s_add_u32 s80,s80,131072
s_or_b32 s83,s83,0x40000
ds_read_b64_tr_b16 a[134:135],v22 offset:18432
v_mfma_f32_16x16x32_bf16 v[184:187],a[144:147],a[216:219],v[184:187]
ds_read_b64_tr_b16 a[144:145],v22 offset:8
v_mfma_f32_16x16x32_bf16 v[164:167],a[152:155],a[192:195],v[164:167]
s_waitcnt lgkmcnt(7)
ds_read_b64_tr_b16 a[146:147],v22 offset:2056
v_mfma_f32_16x16x32_bf16 v[164:167],a[156:159],a[196:199],v[164:167]
buffer_load_dwordx4 v5,s[20:23],s47 offen offset:2048 nt lds
v_mfma_f32_16x16x32_bf16 v[172:175],a[156:159],a[204:207],v[172:175]
s_waitcnt vmcnt(5)
ds_read_b128 a[224:227],v20
v_mfma_f32_16x16x32_bf16 v[172:175],a[152:155],a[200:203],v[172:175]
ds_read_b64_tr_b16 a[148:149],v22 offset:16392
v_mfma_f32_16x16x32_bf16 v[180:183],a[152:155],a[208:211],v[180:183]
ds_read_b64_tr_b16 a[150:151],v22 offset:18440
v_mfma_f32_16x16x32_bf16 v[180:183],a[156:159],a[212:215],v[180:183]
v_mfma_f32_16x16x32_bf16 v[188:191],a[156:159],a[220:223],v[188:191]
ds_read_b128 a[228:231],v21
v_mfma_f32_16x16x32_bf16 v[188:191],a[152:155],a[216:219],v[188:191]
v_mfma_f32_16x16x32_bf16 v[192:195],a[160:163],a[192:195],v[192:195]
ds_read_b128 a[232:235],v20 offset:2048
v_mfma_f32_16x16x32_bf16 v[192:195],a[164:167],a[196:199],v[192:195]
v_mfma_f32_16x16x32_bf16 v[200:203],a[164:167],a[204:207],v[200:203]
ds_read_b128 a[236:239],v21 offset:2048
v_mfma_f32_16x16x32_bf16 v[200:203],a[160:163],a[200:203],v[200:203]
v_mfma_f32_16x16x32_bf16 v[208:211],a[160:163],a[208:211],v[208:211]
ds_read_b128 a[240:243],v20 offset:4096
v_mfma_f32_16x16x32_bf16 v[208:211],a[164:167],a[212:215],v[208:211]
buffer_load_dwordx4 v5,s[20:23],s47 offen offset:3072 nt lds
v_mfma_f32_16x16x32_bf16 v[216:219],a[164:167],a[220:223],v[216:219]
ds_read_b128 a[244:247],v21 offset:4096
v_mfma_f32_16x16x32_bf16 v[216:219],a[160:163],a[216:219],v[216:219]
ds_read_b64_tr_b16 a[160:161],v22 offset:256
v_mfma_f32_16x16x32_bf16 v[196:199],a[168:171],a[192:195],v[196:199]
ds_read_b128 a[248:251],v20 offset:6144
ds_read_b128 a[252:255],v21 offset:6144
v_mfma_f32_16x16x32_bf16 v[196:199],a[172:175],a[196:199],v[196:199]
)A"
R"A(v_mfma_f32_16x16x32_bf16 v[204:207],a[172:175],a[204:207],v[204:207]
v_mfma_f32_16x16x32_bf16 v[204:207],a[168:171],a[200:203],v[204:207]
v_mfma_f32_16x16x32_bf16 v[212:215],a[168:171],a[208:211],v[212:215]
ds_read_b64_tr_b16 a[162:163],v22 offset:2304
v_mfma_f32_16x16x32_bf16 v[212:215],a[172:175],a[212:215],v[212:215]
ds_read_b64_tr_b16 a[164:165],v22 offset:16640
v_mfma_f32_16x16x32_bf16 v[220:223],a[172:175],a[220:223],v[220:223]
s_waitcnt vmcnt(5) lgkmcnt(2)
s_addk_i32 m0,16384
ds_read_b64_tr_b16 a[166:167],v22 offset:18688
v_mfma_f32_16x16x32_bf16 v[220:223],a[168:171],a[216:219],v[220:223]
s_barrier
ds_read_b64_tr_b16 a[136:137],v22 offset:32768
v_mfma_f32_16x16x32_bf16 v[224:227],a[176:179],a[192:195],v[224:227]
buffer_load_dwordx4 v4,s[24:27],s47 offen nt lds
v_mfma_f32_16x16x32_bf16 v[224:227],a[180:183],a[196:199],v[224:227]
ds_read_b64_tr_b16 a[138:139],v22 offset:34816
v_mfma_f32_16x16x32_bf16 v[232:235],a[180:183],a[204:207],v[232:235]
ds_read_b64_tr_b16 a[140:141],v22 offset:49152
v_mfma_f32_16x16x32_bf16 v[232:235],a[176:179],a[200:203],v[232:235]
ds_read_b64_tr_b16 a[142:143],v22 offset:51200
v_mfma_f32_16x16x32_bf16 v[240:243],a[176:179],a[208:211],v[240:243]
ds_read_b64_tr_b16 a[152:153],v22 offset:32776
v_mfma_f32_16x16x32_bf16 v[240:243],a[180:183],a[212:215],v[240:243]
ds_read_b64_tr_b16 a[154:155],v22 offset:34824
v_mfma_f32_16x16x32_bf16 v[248:251],a[180:183],a[220:223],v[248:251]
ds_read_b64_tr_b16 a[156:157],v22 offset:49160
v_mfma_f32_16x16x32_bf16 v[248:251],a[176:179],a[216:219],v[248:251]
ds_read_b64_tr_b16 a[158:159],v22 offset:51208
v_mfma_f32_16x16x32_bf16 v[228:231],a[184:187],a[192:195],v[228:231]
ds_read_b64_tr_b16 a[168:169],v22 offset:33024
v_mfma_f32_16x16x32_bf16 v[228:231],a[188:191],a[196:199],v[228:231]
ds_read_b64_tr_b16 a[170:171],v22 offset:35072
v_mfma_f32_16x16x32_bf16 v[236:239],a[188:191],a[204:207],v[236:239]
ds_read_b64_tr_b16 a[172:173],v22 offset:49408
v_mfma_f32_16x16x32_bf16 v[236:239],a[184:187],a[200:203],v[236:239]
ds_read_b64_tr_b16 a[174:175],v22 offset:51456
v_mfma_f32_16x16x32_bf16 v[244:247],a[184:187],a[208:211],v[244:247]
buffer_load_dwordx4 v5,s[24:27],s47 offen offset:1024 nt lds
v_mfma_f32_16x16x32_bf16 v[244:247],a[188:191],a[212:215],v[244:247]
ds_read_b64_tr_b16 a[176:177],v22 offset:264
v_mfma_f32_16x16x32_bf16 v[252:255],a[188:191],a[220:223],v[252:255]
ds_read_b64_tr_b16 a[178:179],v22 offset:2312
v_mfma_f32_16x16x32_bf16 v[252:255],a[184:187],a[216:219],v[252:255]
ds_read_b64_tr_b16 a[180:181],v22 offset:16648
v_mfma_f32_16x16x32_bf16 a[0:3],a[128:131],a[192:195],a[0:3]
ds_read_b64_tr_b16 a[182:183],v22 offset:18696
v_mfma_f32_16x16x32_bf16 a[0:3],a[132:135],a[196:199],a[0:3]
s_waitcnt lgkmcnt(6)
ds_read_b64_tr_b16 a[184:185],v22 offset:33032
v_mfma_f32_16x16x32_bf16 a[0:3],a[136:139],a[224:227],a[0:3]
ds_read_b64_tr_b16 a[186:187],v22 offset:35080
v_mfma_f32_16x16x32_bf16 a[0:3],a[140:143],a[228:231],a[0:3]
ds_read_b64_tr_b16 a[188:189],v22 offset:49416
v_mfma_f32_16x16x32_bf16 a[32:35],a[140:143],a[236:239],a[32:35]
ds_read_b64_tr_b16 a[190:191],v22 offset:51464
v_mfma_f32_16x16x32_bf16 a[32:35],a[136:139],a[232:235],a[32:35]
v_mfma_f32_16x16x32_bf16 a[32:35],a[132:135],a[204:207],a[32:35]
v_mfma_f32_16x16x32_bf16 a[32:35],a[128:131],a[200:203],a[32:35]
v_mfma_f32_16x16x32_bf16 a[64:67],a[128:131],a[208:211],a[64:67]
buffer_load_dwordx4 v4,s[24:27],s47 offen offset:2048 nt lds
v_mfma_f32_16x16x32_bf16 a[64:67],a[132:135],a[212:215],a[64:67]
v_accvgpr_read_b32 v64,a0
v_accvgpr_read_b32 v65,a1
v_mfma_f32_16x16x32_bf16 a[64:67],a[136:139],a[240:243],a[64:67]
v_mul_f32 v64,s61,v64
v_mul_f32 v65,s61,v65
v_mfma_f32_16x16x32_bf16 a[64:67],a[140:143],a[244:247],a[64:67]
v_accvgpr_read_b32 v66,a2
v_accvgpr_read_b32 v67,a3
v_mfma_f32_16x16x32_bf16 a[96:99],a[140:143],a[252:255],a[96:99]
ds_read_b64_tr_b16 a[140:141],v22 offset:49664
v_cvt_pk_bf16_f32 v64,v64,v65
v_mfma_f32_16x16x32_bf16 a[96:99],a[136:139],a[248:251],a[96:99]
ds_read_b64_tr_b16 a[136:137],v22 offset:33280
v_accvgpr_read_b32 v80,a32
v_mfma_f32_16x16x32_bf16 a[96:99],a[132:135],a[220:223],a[96:99]
ds_read_b64_tr_b16 a[132:133],v22 offset:16896
v_accvgpr_read_b32 v81,a33
v_mfma_f32_16x16x32_bf16 a[96:99],a[128:131],a[216:219],a[96:99]
ds_read_b64_tr_b16 a[128:129],v22 offset:512
v_mul_f32 v80,s61,v80
v_mfma_f32_16x16x32_bf16 a[4:7],a[144:147],a[192:195],a[4:7]
ds_read_b64_tr_b16 a[130:131],v22 offset:2560
v_mul_f32 v81,s61,v81
v_mfma_f32_16x16x32_bf16 a[4:7],a[148:151],a[196:199],a[4:7]
ds_read_b64_tr_b16 a[134:135],v22 offset:18944
v_accvgpr_read_b32 v82,a34
v_mfma_f32_16x16x32_bf16 a[4:7],a[152:155],a[224:227],a[4:7]
ds_read_b64_tr_b16 a[138:139],v22 offset:35328
v_accvgpr_read_b32 v83,a35
v_mfma_f32_16x16x32_bf16 a[4:7],a[156:159],a[228:231],a[4:7]
ds_read_b64_tr_b16 a[142:143],v22 offset:51712
v_mul_f32 v66,s61,v66
v_mfma_f32_16x16x32_bf16 a[36:39],a[156:159],a[236:239],a[36:39]
buffer_load_dwordx4 v5,s[24:27],s47 offen offset:3072 nt lds
v_mfma_f32_16x16x32_bf16 a[36:39],a[152:155],a[232:235],a[36:39]
s_or_b32 s47,s47,0x18000
s_addk_i32 s47,4096
s_and_b32 s47,s47,0x27000
v_mfma_f32_16x16x32_bf16 a[36:39],a[148:151],a[204:207],a[36:39]
s_or_b32 s47,s47,s46
s_and_b32 s46,s54,0xfffc0000
v_mul_f32 v67,s61,v67
v_mfma_f32_16x16x32_bf16 a[36:39],a[144:147],a[200:203],a[36:39]
s_addk_i32 s54,16384
v_cvt_pk_bf16_f32 v80,v80,v81
v_mul_f32 v82,s61,v82
v_mfma_f32_16x16x32_bf16 a[68:71],a[144:147],a[208:211],a[68:71]
v_mul_f32 v83,s61,v83
v_cvt_pk_bf16_f32 v65,v66,v67
v_mfma_f32_16x16x32_bf16 a[68:71],a[148:151],a[212:215],a[68:71]
v_cvt_pk_bf16_f32 v81,v82,v83
v_accvgpr_read_b32 v112,a96
v_mfma_f32_16x16x32_bf16 a[68:71],a[152:155],a[240:243],a[68:71]
v_accvgpr_read_b32 v113,a97
v_accvgpr_read_b32 v66,a4
v_mfma_f32_16x16x32_bf16 a[68:71],a[156:159],a[244:247],a[68:71]
v_accvgpr_read_b32 v67,a5
v_mul_f32 v112,s61,v112
v_mfma_f32_16x16x32_bf16 a[100:103],a[156:159],a[252:255],a[100:103]
ds_read_b64_tr_b16 a[156:157],v22 offset:49672
v_mul_f32 v113,s61,v113
v_mfma_f32_16x16x32_bf16 a[100:103],a[152:155],a[248:251],a[100:103]
ds_read_b64_tr_b16 a[152:153],v22 offset:33288
v_accvgpr_read_b32 v82,a36
v_mfma_f32_16x16x32_bf16 a[100:103],a[148:151],a[220:223],a[100:103]
ds_read_b64_tr_b16 a[148:149],v22 offset:16904
v_accvgpr_read_b32 v83,a37
v_mfma_f32_16x16x32_bf16 a[100:103],a[144:147],a[216:219],a[100:103]
ds_read_b64_tr_b16 a[144:145],v22 offset:520
v_accvgpr_read_b32 v114,a98
v_mfma_f32_16x16x32_bf16 a[8:11],a[160:163],a[192:195],a[8:11]
s_add_u32 m0,0,s51
ds_read_b64_tr_b16 a[146:147],v22 offset:2568
v_accvgpr_read_b32 v115,a99
v_mfma_f32_16x16x32_bf16 a[8:11],a[164:167],a[196:199],a[8:11]
ds_read_b64_tr_b16 a[150:151],v22 offset:18952
v_accvgpr_read_b32 v12,a6
v_mfma_f32_16x16x32_bf16 a[8:11],a[168:171],a[224:227],a[8:11]
s_waitcnt lgkmcnt(6)
ds_read_b64_tr_b16 a[154:155],v22 offset:35336
v_accvgpr_read_b32 v13,a7
v_mfma_f32_16x16x32_bf16 a[8:11],a[172:175],a[228:231],a[8:11]
buffer_load_dwordx4 v4,s[20:23],s47 offen nt lds
v_mfma_f32_16x16x32_bf16 a[40:43],a[172:175],a[236:239],a[40:43]
ds_read_b64_tr_b16 a[158:159],v22 offset:51720
v_mul_f32 v66,s61,v66
v_mfma_f32_16x16x32_bf16 a[40:43],a[168:171],a[232:235],a[40:43]
v_mul_f32 v67,s61,v67
v_accvgpr_read_b32 v14,a38
v_mfma_f32_16x16x32_bf16 a[40:43],a[164:167],a[204:207],a[40:43]
v_accvgpr_read_b32 v15,a39
v_mul_f32 v82,s61,v82
v_mfma_f32_16x16x32_bf16 a[40:43],a[160:163],a[200:203],a[40:43]
v_mul_f32 v83,s61,v83
v_cvt_pk_bf16_f32 v112,v112,v113
v_mfma_f32_16x16x32_bf16 a[72:75],a[160:163],a[208:211],a[72:75]
v_mul_f32 v114,s61,v114
v_mul_f32 v115,s61,v115
v_mfma_f32_16x16x32_bf16 a[72:75],a[164:167],a[212:215],a[72:75]
v_accvgpr_read_b32 v68,a8
v_accvgpr_read_b32 v69,a9
v_mfma_f32_16x16x32_bf16 a[72:75],a[168:171],a[240:243],a[72:75]
v_mul_f32 v68,s61,v68
v_mul_f32 v69,s61,v69
v_mfma_f32_16x16x32_bf16 a[72:75],a[172:175],a[244:247],a[72:75]
v_accvgpr_read_b32 v70,a10
v_accvgpr_read_b32 v71,a11
v_mfma_f32_16x16x32_bf16 a[104:107],a[172:175],a[252:255],a[104:107]
ds_read_b64_tr_b16 a[172:173],v22 offset:49920
v_cvt_pk_bf16_f32 v68,v68,v69
v_mfma_f32_16x16x32_bf16 a[104:107],a[168:171],a[248:251],a[104:107]
ds_read_b64_tr_b16 a[168:169],v22 offset:33536
v_mul_f32 v70,s61,v70
v_mfma_f32_16x16x32_bf16 a[104:107],a[164:167],a[220:223],a[104:107]
ds_read_b64_tr_b16 a[164:165],v22 offset:17152
v_mul_f32 v71,s61,v71
v_mfma_f32_16x16x32_bf16 a[104:107],a[160:163],a[216:219],a[104:107]
ds_read_b64_tr_b16 a[160:161],v22 offset:768
v_mul_f32 v12,s61,v12
v_mfma_f32_16x16x32_bf16 a[12:15],a[176:179],a[192:195],a[12:15]
ds_read_b64_tr_b16 a[162:163],v22 offset:2816
v_mul_f32 v13,s61,v13
v_mfma_f32_16x16x32_bf16 a[12:15],a[180:183],a[196:199],a[12:15]
buffer_load_dwordx4 v4,s[20:23],s47 offen offset:1024 nt lds
v_mfma_f32_16x16x32_bf16 a[12:15],a[184:187],a[224:227],a[12:15]
ds_read_b64_tr_b16 a[166:167],v22 offset:19200
v_cvt_pk_bf16_f32 v66,v66,v67
v_mfma_f32_16x16x32_bf16 a[12:15],a[188:191],a[228:231],a[12:15]
ds_read_b64_tr_b16 a[170:171],v22 offset:35584
v_mul_f32 v14,s61,v14
v_mfma_f32_16x16x32_bf16 a[44:47],a[188:191],a[236:239],a[44:47]
ds_read_b64_tr_b16 a[174:175],v22 offset:51968
v_mul_f32 v15,s61,v15
v_mfma_f32_16x16x32_bf16 a[44:47],a[184:187],a[232:235],a[44:47]
v_cvt_pk_bf16_f32 v82,v82,v83
v_cvt_pk_bf16_f32 v113,v114,v115
v_mfma_f32_16x16x32_bf16 a[44:47],a[180:183],a[204:207],a[44:47]
v_cvt_pk_bf16_f32 v69,v70,v71
v_accvgpr_read_b32 v100,a72
v_mfma_f32_16x16x32_bf16 a[44:47],a[176:179],a[200:203],a[44:47]
v_accvgpr_read_b32 v101,a73
v_accvgpr_read_b32 v116,a104
v_mfma_f32_16x16x32_bf16 a[76:79],a[176:179],a[208:211],a[76:79]
v_accvgpr_read_b32 v117,a105
v_cvt_pk_bf16_f32 v67,v12,v13
v_mfma_f32_16x16x32_bf16 a[76:79],a[180:183],a[212:215],a[76:79]
v_cvt_pk_bf16_f32 v83,v14,v15
v_accvgpr_read_b32 v114,a100
v_mfma_f32_16x16x32_bf16 a[76:79],a[184:187],a[240:243],a[76:79]
v_accvgpr_read_b32 v115,a101
v_accvgpr_read_b32 v70,a12
v_mfma_f32_16x16x32_bf16 a[76:79],a[188:191],a[244:247],a[76:79]
v_accvgpr_read_b32 v71,a13
v_mul_f32 v100,s61,v100
v_mfma_f32_16x16x32_bf16 a[108:111],a[188:191],a[252:255],a[108:111]
ds_read_b64_tr_b16 a[188:189],v22 offset:49928
v_mul_f32 v101,s61,v101
v_mfma_f32_16x16x32_bf16 a[108:111],a[184:187],a[248:251],a[108:111]
ds_read_b64_tr_b16 a[184:185],v22 offset:33544
v_accvgpr_read_b32 v102,a74
v_mfma_f32_16x16x32_bf16 a[108:111],a[180:183],a[220:223],a[108:111]
ds_read_b64_tr_b16 a[180:181],v22 offset:17160
v_accvgpr_read_b32 v103,a75
v_mfma_f32_16x16x32_bf16 a[108:111],a[176:179],a[216:219],a[108:111]
buffer_load_dwordx4 v5,s[20:23],s47 offen offset:2048 nt lds
v_mfma_f32_16x16x32_bf16 a[16:19],a[128:131],a[192:195],a[16:19]
ds_read_b64_tr_b16 a[176:177],v22 offset:776
v_mul_f32 v116,s61,v116
v_mfma_f32_16x16x32_bf16 a[16:19],a[132:135],a[196:199],a[16:19]
ds_read_b64_tr_b16 a[178:179],v22 offset:2824
v_mul_f32 v117,s61,v117
v_mfma_f32_16x16x32_bf16 a[16:19],a[136:139],a[224:227],a[16:19]
ds_read_b64_tr_b16 a[182:183],v22 offset:19208
v_accvgpr_read_b32 v118,a106
v_mfma_f32_16x16x32_bf16 a[16:19],a[140:143],a[228:231],a[16:19]
ds_read_b64_tr_b16 a[186:187],v22 offset:35592
v_accvgpr_read_b32 v119,a107
v_mfma_f32_16x16x32_bf16 a[48:51],a[140:143],a[236:239],a[48:51]
ds_read_b64_tr_b16 a[190:191],v22 offset:51976
v_accvgpr_read_b32 v12,a102
v_mfma_f32_16x16x32_bf16 a[48:51],a[136:139],a[232:235],a[48:51]
v_accvgpr_read_b32 v13,a103
v_mul_f32 v114,s61,v114
v_mfma_f32_16x16x32_bf16 a[48:51],a[132:135],a[204:207],a[48:51]
v_mul_f32 v115,s61,v115
v_accvgpr_read_b32 v14,a14
v_mfma_f32_16x16x32_bf16 a[48:51],a[128:131],a[200:203],a[48:51]
v_accvgpr_read_b32 v15,a15
v_mul_f32 v70,s61,v70
v_mfma_f32_16x16x32_bf16 a[80:83],a[128:131],a[208:211],a[80:83]
v_mul_f32 v71,s61,v71
v_cvt_pk_bf16_f32 v100,v100,v101
v_mfma_f32_16x16x32_bf16 a[80:83],a[132:135],a[212:215],a[80:83]
v_mul_f32 v102,s61,v102
v_mul_f32 v103,s61,v103
v_mfma_f32_16x16x32_bf16 a[80:83],a[136:139],a[240:243],a[80:83]
v_cvt_pk_bf16_f32 v116,v116,v117
v_mul_f32 v118,s61,v118
v_mfma_f32_16x16x32_bf16 a[80:83],a[140:143],a[244:247],a[80:83]
)A"
R"A(v_mul_f32 v119,s61,v119
v_mul_f32 v12,s61,v12
v_mfma_f32_16x16x32_bf16 a[112:115],a[140:143],a[252:255],a[112:115]
ds_read_b64_tr_b16 a[140:141],v22 offset:50184
v_mul_f32 v13,s61,v13
v_mfma_f32_16x16x32_bf16 a[112:115],a[136:139],a[248:251],a[112:115]
buffer_load_dwordx4 v5,s[20:23],s47 offen offset:3072 nt lds
v_mfma_f32_16x16x32_bf16 a[112:115],a[132:135],a[220:223],a[112:115]
ds_read_b64_tr_b16 a[132:133],v22 offset:50176
v_cvt_pk_bf16_f32 v114,v114,v115
v_mfma_f32_16x16x32_bf16 a[112:115],a[128:131],a[216:219],a[112:115]
s_waitcnt lgkmcnt(2)
ds_read_b64_tr_b16 a[128:129],v22 offset:33792
v_mul_f32 v14,s61,v14
v_mfma_f32_16x16x32_bf16 a[20:23],a[144:147],a[192:195],a[20:23]
ds_read_b64_tr_b16 a[130:131],v22 offset:35840
v_mul_f32 v15,s61,v15
v_mfma_f32_16x16x32_bf16 a[20:23],a[148:151],a[196:199],a[20:23]
ds_read_b64_tr_b16 a[134:135],v22 offset:52224
v_cvt_pk_bf16_f32 v70,v70,v71
v_mfma_f32_16x16x32_bf16 a[20:23],a[152:155],a[224:227],a[20:23]
ds_read_b64_tr_b16 a[136:137],v22 offset:33800
v_cvt_pk_bf16_f32 v101,v102,v103
v_mfma_f32_16x16x32_bf16 a[20:23],a[156:159],a[228:231],a[20:23]
ds_read_b64_tr_b16 a[138:139],v22 offset:35848
v_cvt_pk_bf16_f32 v117,v118,v119
v_mfma_f32_16x16x32_bf16 a[52:55],a[156:159],a[236:239],a[52:55]
ds_read_b64_tr_b16 a[142:143],v22 offset:52232
v_cvt_pk_bf16_f32 v115,v12,v13
v_mfma_f32_16x16x32_bf16 a[52:55],a[152:155],a[232:235],a[52:55]
v_cvt_pk_bf16_f32 v71,v14,v15
v_accvgpr_read_b32 v102,a76
v_mfma_f32_16x16x32_bf16 a[52:55],a[148:151],a[204:207],a[52:55]
v_accvgpr_read_b32 v103,a77
v_accvgpr_read_b32 v118,a108
v_mfma_f32_16x16x32_bf16 a[52:55],a[144:147],a[200:203],a[52:55]
v_accvgpr_read_b32 v119,a109
v_accvgpr_read_b32 v12,a78
v_mfma_f32_16x16x32_bf16 a[84:87],a[144:147],a[208:211],a[84:87]
s_addk_i32 m0,16384
v_accvgpr_read_b32 v13,a79
v_mul_f32 v102,s61,v102
v_mfma_f32_16x16x32_bf16 a[84:87],a[148:151],a[212:215],a[84:87]
v_mul_f32 v103,s61,v103
v_accvgpr_read_b32 v14,a110
v_mfma_f32_16x16x32_bf16 a[84:87],a[152:155],a[240:243],a[84:87]
v_accvgpr_read_b32 v15,a111
v_mul_f32 v118,s61,v118
v_mfma_f32_16x16x32_bf16 a[84:87],a[156:159],a[244:247],a[84:87]
buffer_load_dwordx4 v4,s[24:27],s47 offen nt lds
v_mfma_f32_16x16x32_bf16 a[116:119],a[156:159],a[252:255],a[116:119]
ds_read_b64_tr_b16 a[156:157],v22 offset:50440
v_mul_f32 v119,s61,v119
v_mfma_f32_16x16x32_bf16 a[116:119],a[152:155],a[248:251],a[116:119]
ds_read_b64_tr_b16 a[152:153],v22 offset:34056
v_mul_f32 v12,s61,v12
v_mfma_f32_16x16x32_bf16 a[116:119],a[148:151],a[220:223],a[116:119]
ds_read_b64_tr_b16 a[148:149],v22 offset:50432
v_mul_f32 v13,s61,v13
v_mfma_f32_16x16x32_bf16 a[116:119],a[144:147],a[216:219],a[116:119]
ds_read_b64_tr_b16 a[144:145],v22 offset:34048
v_cvt_pk_bf16_f32 v102,v102,v103
v_mfma_f32_16x16x32_bf16 a[24:27],a[160:163],a[192:195],a[24:27]
ds_read_b64_tr_b16 a[146:147],v22 offset:36096
v_mul_f32 v14,s61,v14
v_mfma_f32_16x16x32_bf16 a[24:27],a[164:167],a[196:199],a[24:27]
ds_read_b64_tr_b16 a[150:151],v22 offset:52480
v_mul_f32 v15,s61,v15
v_mfma_f32_16x16x32_bf16 a[24:27],a[168:171],a[224:227],a[24:27]
ds_read_b64_tr_b16 a[154:155],v22 offset:36104
v_cvt_pk_bf16_f32 v118,v118,v119
v_mfma_f32_16x16x32_bf16 a[24:27],a[172:175],a[228:231],a[24:27]
ds_read_b64_tr_b16 a[158:159],v22 offset:52488
v_cvt_pk_bf16_f32 v103,v12,v13
v_mfma_f32_16x16x32_bf16 a[56:59],a[172:175],a[236:239],a[56:59]
v_cvt_pk_bf16_f32 v119,v14,v15
v_accvgpr_read_b32 v96,a64
v_mfma_f32_16x16x32_bf16 a[56:59],a[168:171],a[232:235],a[56:59]
v_accvgpr_read_b32 v72,a16
v_accvgpr_read_b32 v73,a17
v_accvgpr_read_b32 v74,a18
v_accvgpr_read_b32 v75,a19
v_accvgpr_read_b32 v12,a20
v_accvgpr_read_b32 v13,a21
v_accvgpr_read_b32 v14,a22
v_accvgpr_read_b32 v15,a23
v_pk_mul_f32 v[72:73],s[60:61],v[72:73] op_sel:[1,0] op_sel_hi:[1,1]
v_pk_mul_f32 v[74:75],s[60:61],v[74:75] op_sel:[1,0] op_sel_hi:[1,1]
v_pk_mul_f32 v[12:13],s[60:61],v[12:13] op_sel:[1,0] op_sel_hi:[1,1]
v_pk_mul_f32 v[14:15],s[60:61],v[14:15] op_sel:[1,0] op_sel_hi:[1,1]
v_cvt_pk_bf16_f32 v72,v72,v73
v_cvt_pk_bf16_f32 v73,v74,v75
v_cvt_pk_bf16_f32 v74,v12,v13
v_cvt_pk_bf16_f32 v75,v14,v15
v_mfma_f32_16x16x32_bf16 a[56:59],a[164:167],a[204:207],a[56:59]
v_accvgpr_read_b32 v97,a65
v_mul_f32 v96,s61,v96
v_mfma_f32_16x16x32_bf16 a[56:59],a[160:163],a[200:203],a[56:59]
v_accvgpr_read_b32 v88,a48
v_accvgpr_read_b32 v89,a49
v_accvgpr_read_b32 v90,a50
v_accvgpr_read_b32 v91,a51
v_accvgpr_read_b32 v12,a52
v_accvgpr_read_b32 v13,a53
v_accvgpr_read_b32 v14,a54
v_accvgpr_read_b32 v15,a55
v_pk_mul_f32 v[88:89],s[60:61],v[88:89] op_sel:[1,0] op_sel_hi:[1,1]
v_pk_mul_f32 v[90:91],s[60:61],v[90:91] op_sel:[1,0] op_sel_hi:[1,1]
v_pk_mul_f32 v[12:13],s[60:61],v[12:13] op_sel:[1,0] op_sel_hi:[1,1]
v_pk_mul_f32 v[14:15],s[60:61],v[14:15] op_sel:[1,0] op_sel_hi:[1,1]
v_cvt_pk_bf16_f32 v88,v88,v89
v_cvt_pk_bf16_f32 v89,v90,v91
v_cvt_pk_bf16_f32 v90,v12,v13
v_cvt_pk_bf16_f32 v91,v14,v15
v_mfma_f32_16x16x32_bf16 a[88:91],a[160:163],a[208:211],a[88:91]
v_mul_f32 v97,s61,v97
v_accvgpr_read_b32 v98,a66
v_mfma_f32_16x16x32_bf16 a[88:91],a[164:167],a[212:215],a[88:91]
buffer_load_dwordx4 v5,s[24:27],s47 offen offset:1024 nt lds
v_mfma_f32_16x16x32_bf16 a[88:91],a[168:171],a[240:243],a[88:91]
v_accvgpr_read_b32 v99,a67
v_cvt_pk_bf16_f32 v96,v96,v97
v_mfma_f32_16x16x32_bf16 a[88:91],a[172:175],a[244:247],a[88:91]
v_mul_f32 v98,s61,v98
v_mul_f32 v99,s61,v99
v_mfma_f32_16x16x32_bf16 a[120:123],a[172:175],a[252:255],a[120:123]
ds_read_b64_tr_b16 a[172:173],v22 offset:50696
v_cvt_pk_bf16_f32 v97,v98,v99
v_mfma_f32_16x16x32_bf16 a[120:123],a[168:171],a[248:251],a[120:123]
ds_read_b64_tr_b16 a[168:169],v22 offset:34312
v_accvgpr_read_b32 v84,a40
v_mfma_f32_16x16x32_bf16 a[120:123],a[164:167],a[220:223],a[120:123]
ds_read_b64_tr_b16 a[164:165],v22 offset:50688
v_accvgpr_read_b32 v85,a41
v_mfma_f32_16x16x32_bf16 a[120:123],a[160:163],a[216:219],a[120:123]
ds_read_b64_tr_b16 a[160:161],v22 offset:34304
v_accvgpr_read_b32 v98,a68
v_mfma_f32_16x16x32_bf16 a[28:31],a[176:179],a[192:195],a[28:31]
ds_read_b64_tr_b16 a[162:163],v22 offset:36352
v_accvgpr_read_b32 v99,a69
v_mfma_f32_16x16x32_bf16 a[28:31],a[180:183],a[196:199],a[28:31]
ds_read_b64_tr_b16 a[166:167],v22 offset:52736
v_mul_f32 v84,s61,v84
v_mfma_f32_16x16x32_bf16 a[28:31],a[184:187],a[224:227],a[28:31]
ds_read_b64_tr_b16 a[170:171],v22 offset:36360
v_mul_f32 v85,s61,v85
v_mfma_f32_16x16x32_bf16 a[28:31],a[188:191],a[228:231],a[28:31]
ds_read_b64_tr_b16 a[174:175],v22 offset:52744
v_accvgpr_read_b32 v86,a42
v_mfma_f32_16x16x32_bf16 a[60:63],a[188:191],a[236:239],a[60:63]
v_accvgpr_read_b32 v104,a80
v_accvgpr_read_b32 v105,a81
v_accvgpr_read_b32 v106,a82
v_accvgpr_read_b32 v107,a83
v_accvgpr_read_b32 v12,a84
v_accvgpr_read_b32 v13,a85
v_accvgpr_read_b32 v14,a86
v_accvgpr_read_b32 v15,a87
v_pk_mul_f32 v[104:105],s[60:61],v[104:105] op_sel:[1,0] op_sel_hi:[1,1]
v_pk_mul_f32 v[106:107],s[60:61],v[106:107] op_sel:[1,0] op_sel_hi:[1,1]
v_pk_mul_f32 v[12:13],s[60:61],v[12:13] op_sel:[1,0] op_sel_hi:[1,1]
v_pk_mul_f32 v[14:15],s[60:61],v[14:15] op_sel:[1,0] op_sel_hi:[1,1]
v_cvt_pk_bf16_f32 v104,v104,v105
v_cvt_pk_bf16_f32 v105,v106,v107
v_cvt_pk_bf16_f32 v106,v12,v13
v_cvt_pk_bf16_f32 v107,v14,v15
v_mfma_f32_16x16x32_bf16 a[60:63],a[184:187],a[232:235],a[60:63]
v_accvgpr_read_b32 v87,a43
v_accvgpr_read_b32 v16,a70
v_mfma_f32_16x16x32_bf16 a[60:63],a[180:183],a[204:207],a[60:63]
v_accvgpr_read_b32 v120,a112
v_accvgpr_read_b32 v121,a113
v_accvgpr_read_b32 v122,a114
v_accvgpr_read_b32 v123,a115
v_accvgpr_read_b32 v12,a116
v_accvgpr_read_b32 v13,a117
v_accvgpr_read_b32 v14,a118
v_accvgpr_read_b32 v15,a119
v_pk_mul_f32 v[120:121],s[60:61],v[120:121] op_sel:[1,0] op_sel_hi:[1,1]
v_pk_mul_f32 v[122:123],s[60:61],v[122:123] op_sel:[1,0] op_sel_hi:[1,1]
v_pk_mul_f32 v[12:13],s[60:61],v[12:13] op_sel:[1,0] op_sel_hi:[1,1]
v_pk_mul_f32 v[14:15],s[60:61],v[14:15] op_sel:[1,0] op_sel_hi:[1,1]
v_cvt_pk_bf16_f32 v120,v120,v121
v_cvt_pk_bf16_f32 v121,v122,v123
v_cvt_pk_bf16_f32 v122,v12,v13
v_cvt_pk_bf16_f32 v123,v14,v15
v_mfma_f32_16x16x32_bf16 a[60:63],a[176:179],a[200:203],a[60:63]
buffer_load_dwordx4 v4,s[24:27],s47 offen offset:2048 nt lds
v_mfma_f32_16x16x32_bf16 a[92:95],a[176:179],a[208:211],a[92:95]
v_accvgpr_read_b32 v17,a71
v_mul_f32 v98,s61,v98
v_mfma_f32_16x16x32_bf16 a[92:95],a[180:183],a[212:215],a[92:95]
v_mul_f32 v99,s61,v99
v_cvt_pk_bf16_f32 v84,v84,v85
v_mfma_f32_16x16x32_bf16 a[92:95],a[184:187],a[240:243],a[92:95]
v_mul_f32 v86,s61,v86
v_mul_f32 v87,s61,v87
v_mfma_f32_16x16x32_bf16 a[92:95],a[188:191],a[244:247],a[92:95]
v_mul_f32 v16,s61,v16
v_mul_f32 v17,s61,v17
v_mfma_f32_16x16x32_bf16 a[124:127],a[188:191],a[252:255],a[124:127]
ds_read_b64_tr_b16 a[188:189],v22 offset:50952
v_cvt_pk_bf16_f32 v98,v98,v99
v_mfma_f32_16x16x32_bf16 a[124:127],a[184:187],a[248:251],a[124:127]
ds_read_b64_tr_b16 a[184:185],v22 offset:34568
v_cvt_pk_bf16_f32 v85,v86,v87
v_mfma_f32_16x16x32_bf16 a[124:127],a[180:183],a[220:223],a[124:127]
ds_read_b64_tr_b16 a[180:181],v22 offset:50944
v_cvt_pk_bf16_f32 v99,v16,v17
v_mfma_f32_16x16x32_bf16 a[124:127],a[176:179],a[216:219],a[124:127]
s_waitcnt lgkmcnt(3)
ds_read_b64_tr_b16 a[176:177],v22 offset:34560
v_accvgpr_read_b32 v86,a44
v_mfma_f32_16x16x32_bf16 v[128:131],a[128:131],a[224:227],v[128:131]
ds_read_b64_tr_b16 a[178:179],v22 offset:36608
v_accvgpr_read_b32 v87,a45
v_mfma_f32_16x16x32_bf16 v[128:131],a[132:135],a[228:231],v[128:131]
ds_read_b64_tr_b16 a[182:183],v22 offset:52992
v_accvgpr_read_b32 v16,a46
v_mfma_f32_16x16x32_bf16 v[136:139],a[132:135],a[236:239],v[136:139]
ds_read_b64_tr_b16 a[186:187],v22 offset:36616
v_accvgpr_read_b32 v17,a47
v_mfma_f32_16x16x32_bf16 v[136:139],a[128:131],a[232:235],v[136:139]
ds_read_b64_tr_b16 a[190:191],v22 offset:53000
v_mul_f32 v86,s61,v86
v_mfma_f32_16x16x32_bf16 v[144:147],a[128:131],a[240:243],v[144:147]
v_accvgpr_read_b32 v76,a24
v_accvgpr_read_b32 v77,a25
v_accvgpr_read_b32 v78,a26
v_accvgpr_read_b32 v79,a27
v_accvgpr_read_b32 v12,a28
v_accvgpr_read_b32 v13,a29
v_accvgpr_read_b32 v14,a30
v_accvgpr_read_b32 v15,a31
v_pk_mul_f32 v[76:77],s[60:61],v[76:77] op_sel:[1,0] op_sel_hi:[1,1]
v_pk_mul_f32 v[78:79],s[60:61],v[78:79] op_sel:[1,0] op_sel_hi:[1,1]
v_pk_mul_f32 v[12:13],s[60:61],v[12:13] op_sel:[1,0] op_sel_hi:[1,1]
v_pk_mul_f32 v[14:15],s[60:61],v[14:15] op_sel:[1,0] op_sel_hi:[1,1]
v_cvt_pk_bf16_f32 v76,v76,v77
v_cvt_pk_bf16_f32 v77,v78,v79
v_cvt_pk_bf16_f32 v78,v12,v13
v_cvt_pk_bf16_f32 v79,v14,v15
v_mfma_f32_16x16x32_bf16 v[144:147],a[132:135],a[244:247],v[144:147]
buffer_load_dwordx4 v5,s[24:27],s47 offen offset:3072 nt lds
v_mfma_f32_16x16x32_bf16 v[152:155],a[132:135],a[252:255],v[152:155]
s_or_b32 s47,s47,0x18000
s_addk_i32 s47,4096
s_and_b32 s47,s47,0x27000
v_mfma_f32_16x16x32_bf16 v[152:155],a[128:131],a[248:251],v[152:155]
s_or_b32 s47,s47,s46
v_mul_f32 v87,s61,v87
v_mul_f32 v16,s61,v16
v_mfma_f32_16x16x32_bf16 v[132:135],a[136:139],a[224:227],v[132:135]
s_and_b32 s46,s54,0xfffc0000
v_mul_f32 v17,s61,v17
v_cvt_pk_bf16_f32 v86,v86,v87
v_mfma_f32_16x16x32_bf16 v[132:135],a[140:143],a[228:231],v[132:135]
s_addk_i32 s54,16384
v_cvt_pk_bf16_f32 v87,v16,v17
v_mfma_f32_16x16x32_bf16 v[140:143],a[140:143],a[236:239],v[140:143]
v_accvgpr_read_b32 v92,a56
v_accvgpr_read_b32 v93,a57
v_accvgpr_read_b32 v94,a58
v_accvgpr_read_b32 v95,a59
v_accvgpr_read_b32 v12,a60
v_accvgpr_read_b32 v13,a61
v_accvgpr_read_b32 v14,a62
v_accvgpr_read_b32 v15,a63
v_pk_mul_f32 v[92:93],s[60:61],v[92:93] op_sel:[1,0] op_sel_hi:[1,1]
v_pk_mul_f32 v[94:95],s[60:61],v[94:95] op_sel:[1,0] op_sel_hi:[1,1]
v_pk_mul_f32 v[12:13],s[60:61],v[12:13] op_sel:[1,0] op_sel_hi:[1,1]
v_pk_mul_f32 v[14:15],s[60:61],v[14:15] op_sel:[1,0] op_sel_hi:[1,1]
v_cvt_pk_bf16_f32 v92,v92,v93
v_cvt_pk_bf16_f32 v93,v94,v95
v_cvt_pk_bf16_f32 v94,v12,v13
v_cvt_pk_bf16_f32 v95,v14,v15
v_mfma_f32_16x16x32_bf16 v[140:143],a[136:139],a[232:235],v[140:143]
v_accvgpr_read_b32 v108,a88
v_accvgpr_read_b32 v109,a89
v_accvgpr_read_b32 v110,a90
v_accvgpr_read_b32 v111,a91
v_accvgpr_read_b32 v12,a92
v_accvgpr_read_b32 v13,a93
)A"
R"A(v_accvgpr_read_b32 v14,a94
v_accvgpr_read_b32 v15,a95
v_pk_mul_f32 v[108:109],s[60:61],v[108:109] op_sel:[1,0] op_sel_hi:[1,1]
v_pk_mul_f32 v[110:111],s[60:61],v[110:111] op_sel:[1,0] op_sel_hi:[1,1]
v_pk_mul_f32 v[12:13],s[60:61],v[12:13] op_sel:[1,0] op_sel_hi:[1,1]
v_pk_mul_f32 v[14:15],s[60:61],v[14:15] op_sel:[1,0] op_sel_hi:[1,1]
v_cvt_pk_bf16_f32 v108,v108,v109
v_cvt_pk_bf16_f32 v109,v110,v111
v_cvt_pk_bf16_f32 v110,v12,v13
v_cvt_pk_bf16_f32 v111,v14,v15
v_mfma_f32_16x16x32_bf16 v[148:151],a[136:139],a[240:243],v[148:151]
v_accvgpr_read_b32 v124,a120
v_accvgpr_read_b32 v125,a121
v_accvgpr_read_b32 v126,a122
v_accvgpr_read_b32 v127,a123
v_accvgpr_read_b32 v12,a124
v_accvgpr_read_b32 v13,a125
v_accvgpr_read_b32 v14,a126
v_accvgpr_read_b32 v15,a127
v_pk_mul_f32 v[124:125],s[60:61],v[124:125] op_sel:[1,0] op_sel_hi:[1,1]
v_pk_mul_f32 v[126:127],s[60:61],v[126:127] op_sel:[1,0] op_sel_hi:[1,1]
v_pk_mul_f32 v[12:13],s[60:61],v[12:13] op_sel:[1,0] op_sel_hi:[1,1]
v_pk_mul_f32 v[14:15],s[60:61],v[14:15] op_sel:[1,0] op_sel_hi:[1,1]
v_cvt_pk_bf16_f32 v124,v124,v125
v_cvt_pk_bf16_f32 v125,v126,v127
v_cvt_pk_bf16_f32 v126,v12,v13
v_cvt_pk_bf16_f32 v127,v14,v15
v_mfma_f32_16x16x32_bf16 v[148:151],a[140:143],a[244:247],v[148:151]
v_mfma_f32_16x16x32_bf16 v[156:159],a[140:143],a[252:255],v[156:159]
s_waitcnt vmcnt(8) lgkmcnt(0)
v_mfma_f32_16x16x32_bf16 v[156:159],a[136:139],a[248:251],v[156:159]
s_barrier
ds_read_b128 v[48:51],v18
v_mfma_f32_16x16x32_bf16 v[160:163],a[144:147],a[224:227],v[160:163]
ds_read_b64_tr_b16 a[192:193],v2
v_mfma_f32_16x16x32_bf16 v[160:163],a[148:151],a[228:231],v[160:163]
ds_read_b64_tr_b16 a[194:195],v2 offset:4096
v_mfma_f32_16x16x32_bf16 v[168:171],a[148:151],a[236:239],v[168:171]
ds_read_b64_tr_b16 a[196:197],v2 offset:8192
v_mfma_f32_16x16x32_bf16 v[168:171],a[144:147],a[232:235],v[168:171]
ds_read_b128 v[52:55],v18 offset:256
v_mfma_f32_16x16x32_bf16 v[176:179],a[144:147],a[240:243],v[176:179]
v_mul_f32 v128,s61,v128
v_mul_f32 v129,s61,v129
v_mul_f32 v130,s61,v130
v_mul_f32 v131,s61,v131
v_cvt_pk_bf16_f32 v128,v128,v129
v_cvt_pk_bf16_f32 v129,v130,v131
v_pk_mul_f32 v[132:133],s[60:61],v[132:133] op_sel:[1,0] op_sel_hi:[1,1]
v_pk_mul_f32 v[134:135],s[60:61],v[134:135] op_sel:[1,0] op_sel_hi:[1,1]
v_pk_mul_f32 v[136:137],s[60:61],v[136:137] op_sel:[1,0] op_sel_hi:[1,1]
v_pk_mul_f32 v[138:139],s[60:61],v[138:139] op_sel:[1,0] op_sel_hi:[1,1]
v_pk_mul_f32 v[140:141],s[60:61],v[140:141] op_sel:[1,0] op_sel_hi:[1,1]
v_pk_mul_f32 v[142:143],s[60:61],v[142:143] op_sel:[1,0] op_sel_hi:[1,1]
v_pk_mul_f32 v[144:145],s[60:61],v[144:145] op_sel:[1,0] op_sel_hi:[1,1]
v_pk_mul_f32 v[146:147],s[60:61],v[146:147] op_sel:[1,0] op_sel_hi:[1,1]
v_pk_mul_f32 v[148:149],s[60:61],v[148:149] op_sel:[1,0] op_sel_hi:[1,1]
v_pk_mul_f32 v[150:151],s[60:61],v[150:151] op_sel:[1,0] op_sel_hi:[1,1]
v_pk_mul_f32 v[152:153],s[60:61],v[152:153] op_sel:[1,0] op_sel_hi:[1,1]
v_pk_mul_f32 v[154:155],s[60:61],v[154:155] op_sel:[1,0] op_sel_hi:[1,1]
v_pk_mul_f32 v[156:157],s[60:61],v[156:157] op_sel:[1,0] op_sel_hi:[1,1]
v_pk_mul_f32 v[158:159],s[60:61],v[158:159] op_sel:[1,0] op_sel_hi:[1,1]
v_mfma_f32_16x16x32_bf16 v[176:179],a[148:151],a[244:247],v[176:179]
ds_read_b128 v[56:59],v18 offset:512
v_cvt_pk_bf16_f32 v130,v132,v133
v_mfma_f32_16x16x32_bf16 v[184:187],a[148:151],a[252:255],v[184:187]
v_cvt_pk_bf16_f32 v132,v136,v137
v_cvt_pk_bf16_f32 v133,v138,v139
v_mfma_f32_16x16x32_bf16 v[184:187],a[144:147],a[248:251],v[184:187]
ds_read_b128 v[60:63],v18 offset:768
v_cvt_pk_bf16_f32 v131,v134,v135
v_mfma_f32_16x16x32_bf16 v[164:167],a[152:155],a[224:227],v[164:167]
v_cvt_pk_bf16_f32 v134,v140,v141
v_cvt_pk_bf16_f32 v135,v142,v143
v_mfma_f32_16x16x32_bf16 v[164:167],a[156:159],a[228:231],v[164:167]
ds_read_b128 a[128:131],v1
v_cvt_pk_bf16_f32 v144,v144,v145
v_mfma_f32_16x16x32_bf16 v[172:175],a[156:159],a[236:239],v[172:175]
v_cvt_pk_bf16_f32 v145,v146,v147
v_cvt_pk_bf16_f32 v146,v148,v149
v_mfma_f32_16x16x32_bf16 v[172:175],a[152:155],a[232:235],v[172:175]
ds_read_b128 a[132:135],v1 offset:256
v_cvt_pk_bf16_f32 v148,v152,v153
v_mfma_f32_16x16x32_bf16 v[180:183],a[152:155],a[240:243],v[180:183]
v_cvt_pk_bf16_f32 v149,v154,v155
v_cvt_pk_bf16_f32 v147,v150,v151
v_mfma_f32_16x16x32_bf16 v[180:183],a[156:159],a[244:247],v[180:183]
ds_read_b128 a[136:139],v1 offset:512
v_cvt_pk_bf16_f32 v150,v156,v157
v_mfma_f32_16x16x32_bf16 v[188:191],a[156:159],a[252:255],v[188:191]
ds_read_b64_tr_b16 a[198:199],v2 offset:12288
v_cvt_pk_bf16_f32 v151,v158,v159
v_mfma_f32_16x16x32_bf16 v[188:191],a[152:155],a[248:251],v[188:191]
ds_read_b64_tr_b16 a[204:205],v3 offset:8192
v_mfma_f32_16x16x32_bf16 v[192:195],a[160:163],a[224:227],v[192:195]
ds_read_b64_tr_b16 a[206:207],v3 offset:12288
v_mfma_f32_16x16x32_bf16 v[192:195],a[164:167],a[228:231],v[192:195]
s_add_u32 m0,32768,s51
ds_read_b128 a[140:143],v1 offset:768
v_mfma_f32_16x16x32_bf16 v[200:203],a[164:167],a[236:239],v[200:203]
ds_read_b64_tr_b16 a[200:201],v3
v_mfma_f32_16x16x32_bf16 v[200:203],a[160:163],a[232:235],v[200:203]
ds_read_b64_tr_b16 a[202:203],v3 offset:4096
v_mfma_f32_16x16x32_bf16 v[208:211],a[160:163],a[240:243],v[208:211]
buffer_load_dwordx4 v4,s[20:23],s47 offen nt lds
v_mfma_f32_16x16x32_bf16 v[208:211],a[164:167],a[244:247],v[208:211]
ds_read_b128 a[156:159],v1 offset:4864
v_mfma_f32_16x16x32_bf16 v[216:219],a[164:167],a[252:255],v[216:219]
v_mul_f32 v160,s61,v160
v_mul_f32 v161,s61,v161
v_mul_f32 v162,s61,v162
v_mul_f32 v163,s61,v163
v_cvt_pk_bf16_f32 v160,v160,v161
v_cvt_pk_bf16_f32 v161,v162,v163
v_pk_mul_f32 v[164:165],s[60:61],v[164:165] op_sel:[1,0] op_sel_hi:[1,1]
v_pk_mul_f32 v[166:167],s[60:61],v[166:167] op_sel:[1,0] op_sel_hi:[1,1]
v_pk_mul_f32 v[168:169],s[60:61],v[168:169] op_sel:[1,0] op_sel_hi:[1,1]
v_pk_mul_f32 v[170:171],s[60:61],v[170:171] op_sel:[1,0] op_sel_hi:[1,1]
v_pk_mul_f32 v[172:173],s[60:61],v[172:173] op_sel:[1,0] op_sel_hi:[1,1]
v_pk_mul_f32 v[174:175],s[60:61],v[174:175] op_sel:[1,0] op_sel_hi:[1,1]
v_pk_mul_f32 v[176:177],s[60:61],v[176:177] op_sel:[1,0] op_sel_hi:[1,1]
v_pk_mul_f32 v[178:179],s[60:61],v[178:179] op_sel:[1,0] op_sel_hi:[1,1]
v_pk_mul_f32 v[180:181],s[60:61],v[180:181] op_sel:[1,0] op_sel_hi:[1,1]
v_pk_mul_f32 v[182:183],s[60:61],v[182:183] op_sel:[1,0] op_sel_hi:[1,1]
v_pk_mul_f32 v[184:185],s[60:61],v[184:185] op_sel:[1,0] op_sel_hi:[1,1]
v_pk_mul_f32 v[186:187],s[60:61],v[186:187] op_sel:[1,0] op_sel_hi:[1,1]
v_pk_mul_f32 v[188:189],s[60:61],v[188:189] op_sel:[1,0] op_sel_hi:[1,1]
v_pk_mul_f32 v[190:191],s[60:61],v[190:191] op_sel:[1,0] op_sel_hi:[1,1]
v_mfma_f32_16x16x32_bf16 v[216:219],a[160:163],a[248:251],v[216:219]
ds_read_b128 a[152:155],v1 offset:4608
v_cvt_pk_bf16_f32 v162,v164,v165
v_mfma_f32_16x16x32_bf16 v[196:199],a[168:171],a[224:227],v[196:199]
v_cvt_pk_bf16_f32 v164,v168,v169
v_cvt_pk_bf16_f32 v165,v170,v171
v_mfma_f32_16x16x32_bf16 v[196:199],a[172:175],a[228:231],v[196:199]
ds_read_b128 a[148:151],v1 offset:4352
v_cvt_pk_bf16_f32 v163,v166,v167
v_mfma_f32_16x16x32_bf16 v[204:207],a[172:175],a[236:239],v[204:207]
v_cvt_pk_bf16_f32 v166,v172,v173
v_cvt_pk_bf16_f32 v167,v174,v175
v_mfma_f32_16x16x32_bf16 v[204:207],a[168:171],a[232:235],v[204:207]
ds_read_b128 a[144:147],v1 offset:4096
v_cvt_pk_bf16_f32 v176,v176,v177
v_mfma_f32_16x16x32_bf16 v[212:215],a[168:171],a[240:243],v[212:215]
v_cvt_pk_bf16_f32 v177,v178,v179
v_cvt_pk_bf16_f32 v178,v180,v181
v_mfma_f32_16x16x32_bf16 v[212:215],a[172:175],a[244:247],v[212:215]
ds_read_b128 a[160:163],v1 offset:8192
v_cvt_pk_bf16_f32 v180,v184,v185
v_mfma_f32_16x16x32_bf16 v[220:223],a[172:175],a[252:255],v[220:223]
v_cvt_pk_bf16_f32 v181,v186,v187
v_cvt_pk_bf16_f32 v179,v182,v183
v_mfma_f32_16x16x32_bf16 v[220:223],a[168:171],a[248:251],v[220:223]
ds_read_b128 a[164:167],v1 offset:8448
v_cvt_pk_bf16_f32 v182,v188,v189
v_mfma_f32_16x16x32_bf16 v[224:227],a[176:179],a[224:227],v[224:227]
ds_read_b64_tr_b16 a[208:209],v2 offset:256
v_cvt_pk_bf16_f32 v183,v190,v191
v_mfma_f32_16x16x32_bf16 v[224:227],a[180:183],a[228:231],v[224:227]
buffer_load_dwordx4 v4,s[20:23],s47 offen offset:1024 nt lds
v_mfma_f32_16x16x32_bf16 v[232:235],a[180:183],a[236:239],v[232:235]
ds_read_b128 a[168:171],v1 offset:8704
v_mfma_f32_16x16x32_bf16 v[232:235],a[176:179],a[232:235],v[232:235]
ds_read_b64_tr_b16 a[210:211],v2 offset:4352
v_mfma_f32_16x16x32_bf16 v[240:243],a[176:179],a[240:243],v[240:243]
ds_read_b64_tr_b16 a[212:213],v2 offset:8448
v_mfma_f32_16x16x32_bf16 v[240:243],a[180:183],a[244:247],v[240:243]
v_mul_f32 v192,s61,v192
v_mul_f32 v193,s61,v193
v_mul_f32 v194,s61,v194
v_mul_f32 v195,s61,v195
v_cvt_pk_bf16_f32 v192,v192,v193
v_cvt_pk_bf16_f32 v193,v194,v195
v_pk_mul_f32 v[196:197],s[60:61],v[196:197] op_sel:[1,0] op_sel_hi:[1,1]
v_pk_mul_f32 v[198:199],s[60:61],v[198:199] op_sel:[1,0] op_sel_hi:[1,1]
v_pk_mul_f32 v[200:201],s[60:61],v[200:201] op_sel:[1,0] op_sel_hi:[1,1]
v_pk_mul_f32 v[202:203],s[60:61],v[202:203] op_sel:[1,0] op_sel_hi:[1,1]
v_pk_mul_f32 v[204:205],s[60:61],v[204:205] op_sel:[1,0] op_sel_hi:[1,1]
v_pk_mul_f32 v[206:207],s[60:61],v[206:207] op_sel:[1,0] op_sel_hi:[1,1]
v_pk_mul_f32 v[208:209],s[60:61],v[208:209] op_sel:[1,0] op_sel_hi:[1,1]
v_pk_mul_f32 v[210:211],s[60:61],v[210:211] op_sel:[1,0] op_sel_hi:[1,1]
v_pk_mul_f32 v[212:213],s[60:61],v[212:213] op_sel:[1,0] op_sel_hi:[1,1]
v_pk_mul_f32 v[214:215],s[60:61],v[214:215] op_sel:[1,0] op_sel_hi:[1,1]
v_pk_mul_f32 v[216:217],s[60:61],v[216:217] op_sel:[1,0] op_sel_hi:[1,1]
v_pk_mul_f32 v[218:219],s[60:61],v[218:219] op_sel:[1,0] op_sel_hi:[1,1]
v_pk_mul_f32 v[220:221],s[60:61],v[220:221] op_sel:[1,0] op_sel_hi:[1,1]
v_pk_mul_f32 v[222:223],s[60:61],v[222:223] op_sel:[1,0] op_sel_hi:[1,1]
v_mfma_f32_16x16x32_bf16 v[248:251],a[180:183],a[252:255],v[248:251]
ds_read_b128 a[172:175],v1 offset:8960
v_cvt_pk_bf16_f32 v194,v196,v197
v_mfma_f32_16x16x32_bf16 v[248:251],a[176:179],a[248:251],v[248:251]
v_cvt_pk_bf16_f32 v196,v200,v201
v_cvt_pk_bf16_f32 v197,v202,v203
v_mfma_f32_16x16x32_bf16 v[228:231],a[184:187],a[224:227],v[228:231]
ds_read_b128 a[180:183],v1 offset:12544
v_cvt_pk_bf16_f32 v195,v198,v199
v_mfma_f32_16x16x32_bf16 v[228:231],a[188:191],a[228:231],v[228:231]
v_cvt_pk_bf16_f32 v198,v204,v205
v_cvt_pk_bf16_f32 v199,v206,v207
v_mfma_f32_16x16x32_bf16 v[236:239],a[188:191],a[236:239],v[236:239]
ds_read_b128 a[176:179],v1 offset:12288
v_cvt_pk_bf16_f32 v208,v208,v209
v_mfma_f32_16x16x32_bf16 v[236:239],a[184:187],a[232:235],v[236:239]
v_cvt_pk_bf16_f32 v209,v210,v211
v_cvt_pk_bf16_f32 v210,v212,v213
v_mfma_f32_16x16x32_bf16 v[244:247],a[184:187],a[240:243],v[244:247]
v_cvt_pk_bf16_f32 v212,v216,v217
v_cvt_pk_bf16_f32 v213,v218,v219
v_mfma_f32_16x16x32_bf16 v[244:247],a[188:191],a[244:247],v[244:247]
v_cvt_pk_bf16_f32 v211,v214,v215
v_cvt_pk_bf16_f32 v214,v220,v221
v_mfma_f32_16x16x32_bf16 v[252:255],a[188:191],a[252:255],v[252:255]
buffer_load_dwordx4 v5,s[20:23],s47 offen offset:2048 nt lds
v_mfma_f32_16x16x32_bf16 v[252:255],a[184:187],a[248:251],v[252:255]
s_waitcnt lgkmcnt(2)
ds_read_b128 a[188:191],v1 offset:13056
v_cvt_pk_bf16_f32 v215,v222,v223
v_mfma_f32_16x16x32_bf16 v[32:35],v[48:51],v[64:67],0
ds_read_b64_tr_b16 a[214:215],v2 offset:12544
v_mfma_f32_16x16x32_bf16 v[32:35],v[52:55],v[68:71],v[32:35]
ds_read_b64_tr_b16 a[220:221],v3 offset:8448
v_mfma_f32_16x16x32_bf16 v[32:35],v[56:59],v[72:75],v[32:35]
ds_read_b64_tr_b16 a[222:223],v3 offset:12544
v_mfma_f32_16x16x32_bf16 v[32:35],v[60:63],v[76:79],v[32:35]
ds_read_b128 a[184:187],v1 offset:12800
v_add_u32 v1,s48,v1
v_mfma_f32_16x16x32_bf16 v[36:39],v[48:51],v[80:83],0
ds_read_b64_tr_b16 a[216:217],v3 offset:256
v_mfma_f32_16x16x32_bf16 v[36:39],v[52:55],v[84:87],v[36:39]
ds_read_b64_tr_b16 a[218:219],v3 offset:4352
v_mfma_f32_16x16x32_bf16 v[36:39],v[56:59],v[88:91],v[36:39]
ds_read_b64_tr_b16 a[224:225],v2 offset:512
v_mfma_f32_16x16x32_bf16 v[36:39],v[60:63],v[92:95],v[36:39]
ds_read_b64_tr_b16 a[226:227],v2 offset:4608
ds_read_b64_tr_b16 a[228:229],v2 offset:8704
v_mfma_f32_16x16x32_bf16 v[40:43],v[48:51],v[96:99],0
)A"
R"A(ds_read_b64_tr_b16 a[230:231],v2 offset:12800
v_mfma_f32_16x16x32_bf16 v[40:43],v[52:55],v[100:103],v[40:43]
v_max3_f32 v32,v32,v33,v34
v_max3_f32 v32,v32,v35,s18
v_mfma_f32_16x16x32_bf16 v[40:43],v[56:59],v[104:107],v[40:43]
v_sub_f32 v35,0,v32
v_sub_f32 v34,0,v32
v_mfma_f32_16x16x32_bf16 v[40:43],v[60:63],v[108:111],v[40:43]
v_sub_f32 v33,0,v32
v_sub_f32 v32,0,v32
v_mfma_f32_16x16x32_bf16 v[44:47],v[48:51],v[112:115],0
buffer_load_dwordx4 v5,s[20:23],s47 offen offset:3072 nt lds
v_mfma_f32_16x16x32_bf16 v[44:47],v[52:55],v[116:119],v[44:47]
v_max3_f32 v36,v36,v37,v38
v_max3_f32 v36,v36,v39,s19
v_mfma_f32_16x16x32_bf16 v[44:47],v[56:59],v[120:123],v[44:47]
v_sub_f32 v39,0,v36
v_sub_f32 v38,0,v36
v_mfma_f32_16x16x32_bf16 v[44:47],v[60:63],v[124:127],v[44:47]
v_sub_f32 v37,0,v36
v_sub_f32 v36,0,v36
v_mfma_f32_16x16x32_bf16 v[136:139],a[128:131],v[64:67],v[32:35]
ds_read_b64_tr_b16 a[236:237],v3 offset:8704
ds_read_b64_tr_b16 a[238:239],v3 offset:12800
v_mfma_f32_16x16x32_bf16 v[136:139],a[132:135],v[68:71],v[136:139]
v_max3_f32 v40,v40,v41,v42
v_max3_f32 v40,v40,v43,s100
v_mfma_f32_16x16x32_bf16 v[136:139],a[136:139],v[72:75],v[136:139]
v_sub_f32 v43,0,v40
v_sub_f32 v42,0,v40
v_mfma_f32_16x16x32_bf16 v[136:139],a[140:143],v[76:79],v[136:139]
v_sub_f32 v41,0,v40
v_sub_f32 v40,0,v40
v_mfma_f32_16x16x32_bf16 v[140:143],a[156:159],v[76:79],v[32:35]
ds_read_b64_tr_b16 a[232:233],v3 offset:512
ds_read_b64_tr_b16 a[234:235],v3 offset:4608
v_mfma_f32_16x16x32_bf16 v[140:143],a[152:155],v[72:75],v[140:143]
s_addk_i32 m0,16384
ds_read_b64_tr_b16 a[240:241],v2 offset:768
v_max3_f32 v44,v44,v45,v46
v_mfma_f32_16x16x32_bf16 v[140:143],a[148:151],v[68:71],v[140:143]
ds_read_b64_tr_b16 a[242:243],v2 offset:4864
v_max3_f32 v44,v44,v47,s101
v_mfma_f32_16x16x32_bf16 v[140:143],a[144:147],v[64:67],v[140:143]
ds_read_b64_tr_b16 a[244:245],v2 offset:8960
v_mov_b32 v48,0
v_mfma_f32_16x16x32_bf16 v[152:155],a[160:163],v[64:67],v[32:35]
buffer_load_dwordx4 v4,s[24:27],s47 offen nt lds
v_mfma_f32_16x16x32_bf16 v[152:155],a[164:167],v[68:71],v[152:155]
v_exp_f32 v136,v136
v_mfma_f32_16x16x32_bf16 v[152:155],a[168:171],v[72:75],v[152:155]
v_exp_f32 v137,v137
v_mfma_f32_16x16x32_bf16 v[152:155],a[172:175],v[76:79],v[152:155]
s_waitcnt lgkmcnt(5)
v_exp_f32 v138,v138
v_mfma_f32_16x16x32_bf16 v[156:159],a[188:191],v[76:79],v[32:35]
v_exp_f32 v139,v139
v_mfma_f32_16x16x32_bf16 v[156:159],a[184:187],v[72:75],v[156:159]
v_cvt_pk_bf16_f32 v136,v136,v137
v_cvt_pk_bf16_f32 v137,v138,v139
v_mfma_f32_16x16x32_bf16 v[156:159],a[180:183],v[68:71],v[156:159]
v_exp_f32 v140,v140
v_mfma_f32_16x16x32_bf16 v[156:159],a[176:179],v[64:67],v[156:159]
v_exp_f32 v141,v141
v_mfma_f32_16x16x32_bf16 v[168:171],a[128:131],v[80:83],v[36:39]
v_exp_f32 v142,v142
v_mfma_f32_16x16x32_bf16 v[168:171],a[132:135],v[84:87],v[168:171]
v_exp_f32 v143,v143
v_mfma_f32_16x16x32_bf16 v[168:171],a[136:139],v[88:91],v[168:171]
v_cvt_pk_bf16_f32 v138,v140,v141
v_cvt_pk_bf16_f32 v139,v142,v143
v_mfma_f32_16x16x32_bf16 v[168:171],a[140:143],v[92:95],v[168:171]
v_exp_f32 v152,v152
v_mfma_f32_16x16x32_bf16 v[172:175],a[156:159],v[92:95],v[36:39]
v_exp_f32 v153,v153
v_mfma_f32_16x16x32_bf16 v[172:175],a[152:155],v[88:91],v[172:175]
buffer_load_dwordx4 v5,s[24:27],s47 offen offset:1024 nt lds
v_mfma_f32_16x16x32_bf16 v[172:175],a[148:151],v[84:87],v[172:175]
v_exp_f32 v154,v154
v_mfma_f32_16x16x32_bf16 v[172:175],a[144:147],v[80:83],v[172:175]
v_exp_f32 v155,v155
v_mfma_f32_16x16x32_bf16 v[184:187],a[160:163],v[80:83],v[36:39]
v_exp_f32 v156,v156
v_mfma_f32_16x16x32_bf16 v[184:187],a[164:167],v[84:87],v[184:187]
v_exp_f32 v157,v157
v_mfma_f32_16x16x32_bf16 v[184:187],a[168:171],v[88:91],v[184:187]
v_exp_f32 v158,v158
v_mfma_f32_16x16x32_bf16 v[184:187],a[172:175],v[92:95],v[184:187]
v_exp_f32 v159,v159
v_mfma_f32_16x16x32_bf16 v[188:191],a[188:191],v[92:95],v[36:39]
v_cvt_pk_bf16_f32 v140,v152,v153
v_cvt_pk_bf16_f32 v141,v154,v155
v_mfma_f32_16x16x32_bf16 v[188:191],a[184:187],v[88:91],v[188:191]
v_cvt_pk_bf16_f32 v142,v156,v157
v_cvt_pk_bf16_f32 v143,v158,v159
v_mfma_f32_16x16x32_bf16 v[188:191],a[180:183],v[84:87],v[188:191]
ds_read_b64_tr_b16 a[246:247],v2 offset:13056
ds_read_b64_tr_b16 a[252:253],v3 offset:8960
v_mfma_f32_16x16x32_bf16 v[188:191],a[176:179],v[80:83],v[188:191]
ds_read_b64_tr_b16 a[254:255],v3 offset:13056
v_mov_b32 v49,0
v_mfma_f32_16x16x32_bf16 v[200:203],a[128:131],v[96:99],v[40:43]
ds_read_b64_tr_b16 a[248:249],v3 offset:768
v_mov_b32 v50,0
v_mfma_f32_16x16x32_bf16 v[200:203],a[132:135],v[100:103],v[200:203]
ds_read_b64_tr_b16 a[250:251],v3 offset:4864
v_mov_b32 v51,0
v_mfma_f32_16x16x32_bf16 v[200:203],a[136:139],v[104:107],v[200:203]
buffer_load_dwordx4 v4,s[24:27],s47 offen offset:2048 nt lds
v_mfma_f32_16x16x32_bf16 v[200:203],a[140:143],v[108:111],v[200:203]
v_sub_f32 v47,0,v44
v_sub_f32 v46,0,v44
v_mfma_f32_16x16x32_bf16 v[204:207],a[156:159],v[108:111],v[40:43]
v_sub_f32 v45,0,v44
v_sub_f32 v44,0,v44
v_mfma_f32_16x16x32_bf16 v[204:207],a[152:155],v[104:107],v[204:207]
v_mul_f32 v224,s61,v224
v_mul_f32 v225,s61,v225
v_mul_f32 v226,s61,v226
v_mul_f32 v227,s61,v227
v_cvt_pk_bf16_f32 v224,v224,v225
v_cvt_pk_bf16_f32 v225,v226,v227
v_pk_mul_f32 v[228:229],s[60:61],v[228:229] op_sel:[1,0] op_sel_hi:[1,1]
v_pk_mul_f32 v[230:231],s[60:61],v[230:231] op_sel:[1,0] op_sel_hi:[1,1]
v_pk_mul_f32 v[232:233],s[60:61],v[232:233] op_sel:[1,0] op_sel_hi:[1,1]
v_pk_mul_f32 v[234:235],s[60:61],v[234:235] op_sel:[1,0] op_sel_hi:[1,1]
v_pk_mul_f32 v[236:237],s[60:61],v[236:237] op_sel:[1,0] op_sel_hi:[1,1]
v_pk_mul_f32 v[238:239],s[60:61],v[238:239] op_sel:[1,0] op_sel_hi:[1,1]
v_pk_mul_f32 v[240:241],s[60:61],v[240:241] op_sel:[1,0] op_sel_hi:[1,1]
v_pk_mul_f32 v[242:243],s[60:61],v[242:243] op_sel:[1,0] op_sel_hi:[1,1]
v_pk_mul_f32 v[244:245],s[60:61],v[244:245] op_sel:[1,0] op_sel_hi:[1,1]
v_pk_mul_f32 v[246:247],s[60:61],v[246:247] op_sel:[1,0] op_sel_hi:[1,1]
v_pk_mul_f32 v[248:249],s[60:61],v[248:249] op_sel:[1,0] op_sel_hi:[1,1]
v_pk_mul_f32 v[250:251],s[60:61],v[250:251] op_sel:[1,0] op_sel_hi:[1,1]
v_pk_mul_f32 v[252:253],s[60:61],v[252:253] op_sel:[1,0] op_sel_hi:[1,1]
v_pk_mul_f32 v[254:255],s[60:61],v[254:255] op_sel:[1,0] op_sel_hi:[1,1]
v_mfma_f32_16x16x32_bf16 v[204:207],a[148:151],v[100:103],v[204:207]
v_cvt_pk_bf16_f32 v226,v228,v229
v_cvt_pk_bf16_f32 v228,v232,v233
v_mfma_f32_16x16x32_bf16 v[204:207],a[144:147],v[96:99],v[204:207]
v_cvt_pk_bf16_f32 v229,v234,v235
v_cvt_pk_bf16_f32 v227,v230,v231
v_mfma_f32_16x16x32_bf16 v[216:219],a[160:163],v[96:99],v[40:43]
v_cvt_pk_bf16_f32 v230,v236,v237
v_cvt_pk_bf16_f32 v231,v238,v239
v_mfma_f32_16x16x32_bf16 v[216:219],a[164:167],v[100:103],v[216:219]
v_cvt_pk_bf16_f32 v240,v240,v241
v_cvt_pk_bf16_f32 v241,v242,v243
v_mfma_f32_16x16x32_bf16 v[216:219],a[168:171],v[104:107],v[216:219]
v_cvt_pk_bf16_f32 v242,v244,v245
v_cvt_pk_bf16_f32 v244,v248,v249
v_mfma_f32_16x16x32_bf16 v[216:219],a[172:175],v[108:111],v[216:219]
v_cvt_pk_bf16_f32 v245,v250,v251
v_cvt_pk_bf16_f32 v243,v246,v247
v_mfma_f32_16x16x32_bf16 v[220:223],a[188:191],v[108:111],v[40:43]
v_cvt_pk_bf16_f32 v246,v252,v253
v_cvt_pk_bf16_f32 v247,v254,v255
v_mfma_f32_16x16x32_bf16 v[220:223],a[184:187],v[104:107],v[220:223]
v_exp_f32 v168,v168
v_mfma_f32_16x16x32_bf16 v[220:223],a[180:183],v[100:103],v[220:223]
v_exp_f32 v169,v169
v_mfma_f32_16x16x32_bf16 v[220:223],a[176:179],v[96:99],v[220:223]
buffer_load_dwordx4 v5,s[24:27],s47 offen offset:3072 nt lds
v_mfma_f32_16x16x32_bf16 a[0:3],a[192:195],v[136:139],0
s_or_b32 s47,s47,0x18000
s_addk_i32 s47,4096
s_and_b32 s47,s47,0x27000
v_mfma_f32_16x16x32_bf16 a[0:3],a[196:199],v[140:143],a[0:3]
s_or_b32 s47,s47,s46
v_exp_f32 v170,v170
v_mfma_f32_16x16x32_bf16 a[4:7],a[204:207],v[140:143],0
s_and_b32 s46,s54,0xfffc0000
v_exp_f32 v171,v171
v_mfma_f32_16x16x32_bf16 a[4:7],a[200:203],v[136:139],a[4:7]
s_memrealtime s[76:77]
v_cvt_pk_bf16_f32 v168,v168,v169
v_cvt_pk_bf16_f32 v169,v170,v171
v_mfma_f32_16x16x32_bf16 a[8:11],a[208:211],v[136:139],0
v_exp_f32 v172,v172
v_mfma_f32_16x16x32_bf16 a[8:11],a[212:215],v[140:143],a[8:11]
v_exp_f32 v173,v173
v_mfma_f32_16x16x32_bf16 a[12:15],a[220:223],v[140:143],0
v_exp_f32 v174,v174
v_mfma_f32_16x16x32_bf16 a[12:15],a[216:219],v[136:139],a[12:15]
v_exp_f32 v175,v175
v_mfma_f32_16x16x32_bf16 a[16:19],a[224:227],v[136:139],0
v_cvt_pk_bf16_f32 v170,v172,v173
v_cvt_pk_bf16_f32 v171,v174,v175
v_mfma_f32_16x16x32_bf16 a[16:19],a[228:231],v[140:143],a[16:19]
v_exp_f32 v184,v184
v_mfma_f32_16x16x32_bf16 a[20:23],a[236:239],v[140:143],0
s_waitcnt lgkmcnt(0)
v_exp_f32 v185,v185
v_mfma_f32_16x16x32_bf16 a[20:23],a[232:235],v[136:139],a[20:23]
s_add_u32 s78,s76,s60
v_exp_f32 v186,v186
v_mfma_f32_16x16x32_bf16 a[24:27],a[240:243],v[136:139],0
s_lshr_b32 s60,s94,2
v_exp_f32 v187,v187
v_mfma_f32_16x16x32_bf16 a[24:27],a[244:247],v[140:143],a[24:27]
s_max_i32 s60,s60,124
v_exp_f32 v188,v188
v_mfma_f32_16x16x32_bf16 a[28:31],a[252:255],v[140:143],0
v_exp_f32 v189,v189
v_mfma_f32_16x16x32_bf16 a[28:31],a[248:251],v[136:139],a[28:31]
v_exp_f32 v190,v190
v_smfmac_f32_16x16x64_bf16 v[48:51],v[28:31],v[136:143],v27
v_cvt_pk_bf16_f32 v172,v184,v185
v_mfma_f32_16x16x32_bf16 v[232:235],a[128:131],v[112:115],v[44:47]
s_waitcnt vmcnt(8)
v_exp_f32 v191,v191
v_mfma_f32_16x16x32_bf16 v[232:235],a[132:135],v[116:119],v[232:235]
s_barrier
s_add_u32 m0,65536,s51
v_cvt_pk_bf16_f32 v173,v186,v187
v_mfma_f32_16x16x32_bf16 v[232:235],a[136:139],v[120:123],v[232:235]
v_cvt_pk_bf16_f32 v174,v188,v189
v_cvt_pk_bf16_f32 v175,v190,v191
v_mfma_f32_16x16x32_bf16 v[232:235],a[140:143],v[124:127],v[232:235]
ds_read_b128 a[128:131],v1
v_mov_b32 v52,0
v_mfma_f32_16x16x32_bf16 v[236:239],a[156:159],v[124:127],v[44:47]
buffer_load_dwordx4 v4,s[20:23],s47 offen nt lds
v_mfma_f32_16x16x32_bf16 v[236:239],a[152:155],v[120:123],v[236:239]
ds_read_b128 a[132:135],v1 offset:256
v_mov_b32 v53,0
v_mfma_f32_16x16x32_bf16 v[236:239],a[148:151],v[116:119],v[236:239]
v_mov_b32 v54,0
v_mov_b32 v55,0
v_mfma_f32_16x16x32_bf16 v[236:239],a[144:147],v[112:115],v[236:239]
ds_read_b128 a[136:139],v1 offset:512
v_mov_b32 v56,0
v_mfma_f32_16x16x32_bf16 v[248:251],a[160:163],v[112:115],v[44:47]
v_exp_f32 v200,v200
v_mfma_f32_16x16x32_bf16 v[248:251],a[164:167],v[116:119],v[248:251]
ds_read_b128 a[140:143],v1 offset:768
v_mov_b32 v57,0
v_mfma_f32_16x16x32_bf16 v[248:251],a[168:171],v[120:123],v[248:251]
v_exp_f32 v201,v201
v_mfma_f32_16x16x32_bf16 v[248:251],a[172:175],v[124:127],v[248:251]
ds_read_b128 a[156:159],v1 offset:4864
v_cvt_pk_bf16_f32 v200,v200,v201
v_mfma_f32_16x16x32_bf16 v[252:255],a[188:191],v[124:127],v[44:47]
v_exp_f32 v202,v202
v_mfma_f32_16x16x32_bf16 v[252:255],a[184:187],v[120:123],v[252:255]
ds_read_b128 a[152:155],v1 offset:4608
v_mov_b32 v58,0
v_mfma_f32_16x16x32_bf16 v[252:255],a[180:183],v[116:119],v[252:255]
v_exp_f32 v203,v203
v_mfma_f32_16x16x32_bf16 v[252:255],a[176:179],v[112:115],v[252:255]
ds_read_b128 a[148:151],v1 offset:4352
v_cvt_pk_bf16_f32 v201,v202,v203
v_mfma_f32_16x16x32_bf16 a[32:35],a[192:195],v[168:171],0
v_exp_f32 v204,v204
v_mfma_f32_16x16x32_bf16 a[32:35],a[196:199],v[172:175],a[32:35]
ds_read_b128 a[144:147],v1 offset:4096
v_mov_b32 v59,0
v_mfma_f32_16x16x32_bf16 a[36:39],a[204:207],v[172:175],0
buffer_load_dwordx4 v4,s[20:23],s47 offen offset:1024 nt lds
v_mfma_f32_16x16x32_bf16 a[36:39],a[200:203],v[168:171],a[36:39]
ds_read_b128 a[160:163],v1 offset:8192
v_mov_b32 v60,0
v_mfma_f32_16x16x32_bf16 a[40:43],a[208:211],v[168:171],0
v_exp_f32 v205,v205
v_mfma_f32_16x16x32_bf16 a[40:43],a[212:215],v[172:175],a[40:43]
ds_read_b128 a[164:167],v1 offset:8448
v_cvt_pk_bf16_f32 v202,v204,v205
v_mfma_f32_16x16x32_bf16 a[44:47],a[220:223],v[172:175],0
v_exp_f32 v206,v206
v_mfma_f32_16x16x32_bf16 a[44:47],a[216:219],v[168:171],a[44:47]
ds_read_b128 a[168:171],v1 offset:8704
v_mov_b32 v61,0
v_mfma_f32_16x16x32_bf16 a[48:51],a[224:227],v[168:171],0
v_exp_f32 v207,v207
v_mfma_f32_16x16x32_bf16 a[48:51],a[228:231],v[172:175],a[48:51]
ds_read_b128 a[172:175],v1 offset:8960
v_cvt_pk_bf16_f32 v203,v206,v207
)A"
R"A(v_mfma_f32_16x16x32_bf16 a[52:55],a[236:239],v[172:175],0
v_exp_f32 v216,v216
v_mfma_f32_16x16x32_bf16 a[52:55],a[232:235],v[168:171],a[52:55]
ds_read_b128 a[188:191],v1 offset:13056
v_mov_b32 v62,0
v_mfma_f32_16x16x32_bf16 a[56:59],a[240:243],v[168:171],0
v_exp_f32 v217,v217
v_mfma_f32_16x16x32_bf16 a[56:59],a[244:247],v[172:175],a[56:59]
ds_read_b128 a[184:187],v1 offset:12800
v_cvt_pk_bf16_f32 v204,v216,v217
v_mfma_f32_16x16x32_bf16 a[60:63],a[252:255],v[172:175],0
v_exp_f32 v218,v218
v_mfma_f32_16x16x32_bf16 a[60:63],a[248:251],v[168:171],a[60:63]
ds_read_b128 a[180:183],v1 offset:12544
v_mov_b32 v63,0
v_smfmac_f32_16x16x64_bf16 v[52:55],v[28:31],v[168:175],v27
s_waitcnt lgkmcnt(4)
v_add_u32 v2,s48,v2
v_mfma_f32_16x16x32_bf16 v[136:139],a[128:131],v[64:67],v[32:35]
buffer_load_dwordx4 v5,s[20:23],s47 offen offset:2048 nt lds
v_mfma_f32_16x16x32_bf16 v[136:139],a[132:135],v[68:71],v[136:139]
ds_read_b128 a[176:179],v1 offset:12288
v_add_u32 v3,s48,v3
v_mfma_f32_16x16x32_bf16 v[136:139],a[136:139],v[72:75],v[136:139]
s_mov_b32 s48,32768
v_exp_f32 v219,v219
v_mfma_f32_16x16x32_bf16 v[136:139],a[140:143],v[76:79],v[136:139]
v_exp_f32 v220,v220
v_mfma_f32_16x16x32_bf16 v[140:143],a[156:159],v[76:79],v[32:35]
v_exp_f32 v221,v221
v_mfma_f32_16x16x32_bf16 v[140:143],a[152:155],v[72:75],v[140:143]
v_exp_f32 v222,v222
v_mfma_f32_16x16x32_bf16 v[140:143],a[148:151],v[68:71],v[140:143]
v_exp_f32 v223,v223
v_mfma_f32_16x16x32_bf16 v[140:143],a[144:147],v[64:67],v[140:143]
v_cvt_pk_bf16_f32 v205,v218,v219
v_cvt_pk_bf16_f32 v206,v220,v221
v_mfma_f32_16x16x32_bf16 v[152:155],a[160:163],v[64:67],v[32:35]
v_cvt_pk_bf16_f32 v207,v222,v223
v_add_u32 v1,s48,v1
v_mfma_f32_16x16x32_bf16 v[152:155],a[164:167],v[68:71],v[152:155]
v_exp_f32 v232,v232
v_mfma_f32_16x16x32_bf16 v[152:155],a[168:171],v[72:75],v[152:155]
s_waitcnt lgkmcnt(0)
v_exp_f32 v233,v233
v_mfma_f32_16x16x32_bf16 v[152:155],a[172:175],v[76:79],v[152:155]
v_exp_f32 v234,v234
v_mfma_f32_16x16x32_bf16 v[156:159],a[188:191],v[76:79],v[32:35]
v_exp_f32 v235,v235
v_mfma_f32_16x16x32_bf16 v[156:159],a[184:187],v[72:75],v[156:159]
buffer_load_dwordx4 v5,s[20:23],s47 offen offset:3072 nt lds
v_mfma_f32_16x16x32_bf16 v[156:159],a[180:183],v[68:71],v[156:159]
v_exp_f32 v236,v236
v_mfma_f32_16x16x32_bf16 v[156:159],a[176:179],v[64:67],v[156:159]
v_exp_f32 v237,v237
v_mfma_f32_16x16x32_bf16 a[64:67],a[192:195],v[200:203],0
v_exp_f32 v238,v238
v_mfma_f32_16x16x32_bf16 a[64:67],a[196:199],v[204:207],a[64:67]
v_exp_f32 v239,v239
v_mfma_f32_16x16x32_bf16 a[68:71],a[204:207],v[204:207],0
v_exp_f32 v248,v248
v_mfma_f32_16x16x32_bf16 a[68:71],a[200:203],v[200:203],a[68:71]
v_exp_f32 v249,v249
v_mfma_f32_16x16x32_bf16 a[72:75],a[208:211],v[200:203],0
v_exp_f32 v250,v250
v_mfma_f32_16x16x32_bf16 a[72:75],a[212:215],v[204:207],a[72:75]
v_exp_f32 v251,v251
v_mfma_f32_16x16x32_bf16 a[76:79],a[220:223],v[204:207],0
v_exp_f32 v252,v252
v_mfma_f32_16x16x32_bf16 a[76:79],a[216:219],v[200:203],a[76:79]
v_exp_f32 v253,v253
v_mfma_f32_16x16x32_bf16 a[80:83],a[224:227],v[200:203],0
s_addk_i32 m0,16384
v_exp_f32 v254,v254
v_mfma_f32_16x16x32_bf16 a[80:83],a[228:231],v[204:207],a[80:83]
v_exp_f32 v255,v255
v_mfma_f32_16x16x32_bf16 a[84:87],a[236:239],v[204:207],0
v_exp_f32 v136,v136
v_mfma_f32_16x16x32_bf16 a[84:87],a[232:235],v[200:203],a[84:87]
buffer_load_dwordx4 v4,s[24:27],s47 offen nt lds
v_mfma_f32_16x16x32_bf16 a[88:91],a[240:243],v[200:203],0
v_cvt_pk_bf16_f32 v232,v232,v233
v_cvt_pk_bf16_f32 v233,v234,v235
v_mfma_f32_16x16x32_bf16 a[88:91],a[244:247],v[204:207],a[88:91]
v_cvt_pk_bf16_f32 v234,v236,v237
v_cvt_pk_bf16_f32 v235,v238,v239
v_mfma_f32_16x16x32_bf16 a[92:95],a[252:255],v[204:207],0
v_cvt_pk_bf16_f32 v236,v248,v249
v_cvt_pk_bf16_f32 v237,v250,v251
v_mfma_f32_16x16x32_bf16 a[92:95],a[248:251],v[200:203],a[92:95]
v_cvt_pk_bf16_f32 v238,v252,v253
v_cvt_pk_bf16_f32 v239,v254,v255
v_smfmac_f32_16x16x64_bf16 v[56:59],v[28:31],v[200:207],v27
v_mfma_f32_16x16x32_bf16 v[168:171],a[128:131],v[80:83],v[36:39]
v_exp_f32 v137,v137
v_mfma_f32_16x16x32_bf16 v[168:171],a[132:135],v[84:87],v[168:171]
v_exp_f32 v138,v138
v_mfma_f32_16x16x32_bf16 v[168:171],a[136:139],v[88:91],v[168:171]
v_exp_f32 v139,v139
v_mfma_f32_16x16x32_bf16 v[168:171],a[140:143],v[92:95],v[168:171]
v_exp_f32 v140,v140
v_mfma_f32_16x16x32_bf16 v[172:175],a[156:159],v[92:95],v[36:39]
v_exp_f32 v141,v141
v_mfma_f32_16x16x32_bf16 v[172:175],a[152:155],v[88:91],v[172:175]
v_exp_f32 v142,v142
v_mfma_f32_16x16x32_bf16 v[172:175],a[148:151],v[84:87],v[172:175]
v_exp_f32 v143,v143
v_mfma_f32_16x16x32_bf16 v[172:175],a[144:147],v[80:83],v[172:175]
v_exp_f32 v152,v152
v_mfma_f32_16x16x32_bf16 v[184:187],a[160:163],v[80:83],v[36:39]
buffer_load_dwordx4 v5,s[24:27],s47 offen offset:1024 nt lds
v_mfma_f32_16x16x32_bf16 v[184:187],a[164:167],v[84:87],v[184:187]
v_exp_f32 v153,v153
v_mfma_f32_16x16x32_bf16 v[184:187],a[168:171],v[88:91],v[184:187]
v_exp_f32 v154,v154
v_mfma_f32_16x16x32_bf16 v[184:187],a[172:175],v[92:95],v[184:187]
v_exp_f32 v155,v155
v_mfma_f32_16x16x32_bf16 v[188:191],a[188:191],v[92:95],v[36:39]
v_exp_f32 v156,v156
v_mfma_f32_16x16x32_bf16 v[188:191],a[184:187],v[88:91],v[188:191]
v_exp_f32 v157,v157
v_mfma_f32_16x16x32_bf16 v[188:191],a[180:183],v[84:87],v[188:191]
v_exp_f32 v158,v158
v_mfma_f32_16x16x32_bf16 v[188:191],a[176:179],v[80:83],v[188:191]
v_exp_f32 v159,v159
v_mfma_f32_16x16x32_bf16 a[96:99],a[192:195],v[232:235],0
ds_read_b64_tr_b16 a[192:193],v2
ds_read_b64_tr_b16 a[194:195],v2 offset:4096
v_mfma_f32_16x16x32_bf16 a[96:99],a[196:199],v[236:239],a[96:99]
ds_read_b64_tr_b16 a[196:197],v2 offset:8192
v_cvt_pk_bf16_f32 v136,v136,v137
v_mfma_f32_16x16x32_bf16 a[100:103],a[204:207],v[236:239],0
ds_read_b64_tr_b16 a[198:199],v2 offset:12288
v_cvt_pk_bf16_f32 v137,v138,v139
v_mfma_f32_16x16x32_bf16 a[100:103],a[200:203],v[232:235],a[100:103]
ds_read_b64_tr_b16 a[204:205],v3 offset:8192
v_cvt_pk_bf16_f32 v138,v140,v141
v_mfma_f32_16x16x32_bf16 a[104:107],a[208:211],v[232:235],0
ds_read_b64_tr_b16 a[206:207],v3 offset:12288
ds_read_b64_tr_b16 a[200:201],v3
v_mfma_f32_16x16x32_bf16 a[104:107],a[212:215],v[236:239],a[104:107]
ds_read_b64_tr_b16 a[202:203],v3 offset:4096
v_cvt_pk_bf16_f32 v139,v142,v143
v_mfma_f32_16x16x32_bf16 a[108:111],a[220:223],v[236:239],0
buffer_load_dwordx4 v4,s[24:27],s47 offen offset:2048 nt lds
v_mfma_f32_16x16x32_bf16 a[108:111],a[216:219],v[232:235],a[108:111]
ds_read_b64_tr_b16 a[208:209],v2 offset:256
ds_read_b64_tr_b16 a[210:211],v2 offset:4352
v_mfma_f32_16x16x32_bf16 a[112:115],a[224:227],v[232:235],0
ds_read_b64_tr_b16 a[212:213],v2 offset:8448
v_cvt_pk_bf16_f32 v140,v152,v153
v_mfma_f32_16x16x32_bf16 a[112:115],a[228:231],v[236:239],a[112:115]
ds_read_b64_tr_b16 a[214:215],v2 offset:12544
v_cvt_pk_bf16_f32 v141,v154,v155
v_mfma_f32_16x16x32_bf16 a[116:119],a[236:239],v[236:239],0
ds_read_b64_tr_b16 a[220:221],v3 offset:8448
v_cvt_pk_bf16_f32 v142,v156,v157
v_mfma_f32_16x16x32_bf16 a[116:119],a[232:235],v[232:235],a[116:119]
ds_read_b64_tr_b16 a[222:223],v3 offset:12544
v_cvt_pk_bf16_f32 v143,v158,v159
v_mfma_f32_16x16x32_bf16 a[120:123],a[240:243],v[232:235],0
ds_read_b64_tr_b16 a[216:217],v3 offset:256
ds_read_b64_tr_b16 a[218:219],v3 offset:4352
v_mfma_f32_16x16x32_bf16 a[120:123],a[244:247],v[236:239],a[120:123]
v_exp_f32 v168,v168
v_mfma_f32_16x16x32_bf16 a[124:127],a[252:255],v[236:239],0
ds_read_b64_tr_b16 a[224:225],v2 offset:512
ds_read_b64_tr_b16 a[226:227],v2 offset:4608
v_mfma_f32_16x16x32_bf16 a[124:127],a[248:251],v[232:235],a[124:127]
v_exp_f32 v169,v169
v_smfmac_f32_16x16x64_bf16 v[60:63],v[28:31],v[232:239],v27
ds_read_b64_tr_b16 a[228:229],v2 offset:8704
v_mfma_f32_16x16x32_bf16 v[200:203],a[128:131],v[96:99],v[40:43]
ds_read_b64_tr_b16 a[230:231],v2 offset:12800
ds_read_b64_tr_b16 a[236:237],v3 offset:8704
v_mfma_f32_16x16x32_bf16 v[200:203],a[132:135],v[100:103],v[200:203]
ds_read_b64_tr_b16 a[238:239],v3 offset:12800
v_cvt_pk_bf16_f32 v168,v168,v169
v_mfma_f32_16x16x32_bf16 v[200:203],a[136:139],v[104:107],v[200:203]
ds_read_b64_tr_b16 a[232:233],v3 offset:512
v_mfma_f32_16x16x32_bf16 v[200:203],a[140:143],v[108:111],v[200:203]
buffer_load_dwordx4 v5,s[24:27],s47 offen offset:3072 nt lds
v_mfma_f32_16x16x32_bf16 v[204:207],a[156:159],v[108:111],v[40:43]
s_or_b32 s47,s47,0x18000
ds_read_b64_tr_b16 a[234:235],v3 offset:4608
ds_read_b64_tr_b16 a[240:241],v2 offset:768
v_mfma_f32_16x16x32_bf16 v[204:207],a[152:155],v[104:107],v[204:207]
s_addk_i32 s47,4096
s_and_b32 s47,s47,0x27000
ds_read_b64_tr_b16 a[242:243],v2 offset:4864
v_mfma_f32_16x16x32_bf16 v[204:207],a[148:151],v[100:103],v[204:207]
s_or_b32 s47,s47,s46
ds_read_b64_tr_b16 a[244:245],v2 offset:8960
v_mfma_f32_16x16x32_bf16 v[204:207],a[144:147],v[96:99],v[204:207]
ds_read_b64_tr_b16 a[246:247],v2 offset:13056
v_mfma_f32_16x16x32_bf16 v[216:219],a[160:163],v[96:99],v[40:43]
ds_read_b64_tr_b16 a[252:253],v3 offset:8960
ds_read_b64_tr_b16 a[254:255],v3 offset:13056
v_mfma_f32_16x16x32_bf16 v[216:219],a[164:167],v[100:103],v[216:219]
ds_read_b64_tr_b16 a[248:249],v3 offset:768
v_mfma_f32_16x16x32_bf16 v[216:219],a[168:171],v[104:107],v[216:219]
ds_read_b64_tr_b16 a[250:251],v3 offset:4864
v_mfma_f32_16x16x32_bf16 v[216:219],a[172:175],v[108:111],v[216:219]
v_exp_f32 v170,v170
v_mfma_f32_16x16x32_bf16 v[220:223],a[188:191],v[108:111],v[40:43]
v_exp_f32 v171,v171
v_mfma_f32_16x16x32_bf16 v[220:223],a[184:187],v[104:107],v[220:223]
v_exp_f32 v172,v172
v_mfma_f32_16x16x32_bf16 v[220:223],a[180:183],v[100:103],v[220:223]
v_exp_f32 v173,v173
v_mfma_f32_16x16x32_bf16 v[220:223],a[176:179],v[96:99],v[220:223]
s_waitcnt lgkmcnt(4)
v_exp_f32 v174,v174
v_mfma_f32_16x16x32_bf16 a[0:3],a[192:195],v[136:139],a[0:3]
v_exp_f32 v175,v175
v_mfma_f32_16x16x32_bf16 a[0:3],a[196:199],v[140:143],a[0:3]
v_cvt_pk_bf16_f32 v169,v170,v171
v_cvt_pk_bf16_f32 v170,v172,v173
v_mfma_f32_16x16x32_bf16 a[4:7],a[204:207],v[140:143],a[4:7]
v_exp_f32 v184,v184
v_mfma_f32_16x16x32_bf16 a[4:7],a[200:203],v[136:139],a[4:7]
s_memrealtime s[76:77]
v_exp_f32 v185,v185
v_mfma_f32_16x16x32_bf16 a[8:11],a[208:211],v[136:139],a[8:11]
v_exp_f32 v186,v186
v_mfma_f32_16x16x32_bf16 a[8:11],a[212:215],v[140:143],a[8:11]
v_exp_f32 v187,v187
v_mfma_f32_16x16x32_bf16 a[12:15],a[220:223],v[140:143],a[12:15]
v_exp_f32 v188,v188
v_mfma_f32_16x16x32_bf16 a[12:15],a[216:219],v[136:139],a[12:15]
v_exp_f32 v189,v189
v_mfma_f32_16x16x32_bf16 a[16:19],a[224:227],v[136:139],a[16:19]
v_exp_f32 v190,v190
v_mfma_f32_16x16x32_bf16 a[16:19],a[228:231],v[140:143],a[16:19]
v_exp_f32 v191,v191
v_mfma_f32_16x16x32_bf16 a[20:23],a[236:239],v[140:143],a[20:23]
s_waitcnt lgkmcnt(0)
v_cvt_pk_bf16_f32 v171,v174,v175
v_cvt_pk_bf16_f32 v172,v184,v185
v_mfma_f32_16x16x32_bf16 a[20:23],a[232:235],v[136:139],a[20:23]
s_sub_u32 s79,s78,s76
v_cvt_pk_bf16_f32 v173,v186,v187
v_cvt_pk_bf16_f32 v174,v188,v189
v_mfma_f32_16x16x32_bf16 a[24:27],a[240:243],v[136:139],a[24:27]
s_add_u32 s78,s76,s60
v_cvt_pk_bf16_f32 v175,v190,v191
v_mfma_f32_16x16x32_bf16 a[24:27],a[244:247],v[140:143],a[24:27]
s_cmp_gt_i32 s79,s95
s_cbranch_scc1 L_ps2_%=
L_pr2_%=:
v_mfma_f32_16x16x32_bf16 a[28:31],a[252:255],v[140:143],a[28:31]
v_mfma_f32_16x16x32_bf16 a[28:31],a[248:251],v[136:139],a[28:31]
v_smfmac_f32_16x16x64_bf16 v[48:51],v[28:31],v[136:143],v27
s_mov_b32 s53,13
s_cmp_eq_u32 s40,3
s_cbranch_scc0 L_att0_%=
s_cmp_gt_i32 s18,0
s_cbranch_scc1 L_att0v0_%=
s_cmp_gt_i32 s19,0
s_cbranch_scc1 L_att0v1_%=
s_cmp_gt_i32 s100,0
s_cbranch_scc1 L_att0v2_%=
.p2align 5
L_att0v3_%=:
s_waitcnt vmcnt(8) lgkmcnt(0)
v_exp_f32 v200,v200
s_barrier
s_add_u32 m0,s49,s51
ds_read_b128 a[128:131],v1
s_mov_b32 s90,L_endseg_%=-L_pc_%=+128
v_exp_f32 v201,v201
s_lshr_b32 s45,s45,1
ds_read_b128 a[132:135],v1 offset:256
v_cvt_pk_bf16_f32 v200,v200,v201
buffer_load_dwordx4 v4,s[20:23],s47 offen nt lds
ds_read_b128 a[136:139],v1 offset:512
v_add_u32 v2,s48,v2
v_exp_f32 v202,v202
s_add_u32 s62,s50,32768
ds_read_b128 a[140:143],v1 offset:768
v_add_u32 v3,s48,v3
s_and_b32 s62,s62,131071
v_exp_f32 v203,v203
s_addk_i32 s54,16384
)A"
R"A(ds_read_b128 a[156:159],v1 offset:4864
v_cvt_pk_bf16_f32 v201,v202,v203
s_sub_u32 s48,s62,s50
v_exp_f32 v204,v204
s_sub_u32 s53,s53,1
v_exp_f32 v205,v205
s_and_b32 s46,s54,0xfffc0000
ds_read_b128 a[152:155],v1 offset:4608
v_cvt_pk_bf16_f32 v202,v204,v205
s_mov_b32 s50,s62
v_exp_f32 v206,v206
s_xor_b32 s49,s50,65536
v_exp_f32 v207,v207
ds_read_b128 a[148:151],v1 offset:4352
v_cvt_pk_bf16_f32 v203,v206,v207
v_mfma_f32_16x16x32_bf16 a[32:35],a[192:195],v[168:171],a[32:35]
v_exp_f32 v216,v216
v_mfma_f32_16x16x32_bf16 a[32:35],a[196:199],v[172:175],a[32:35]
ds_read_b128 a[144:147],v1 offset:4096
v_mfma_f32_16x16x32_bf16 a[36:39],a[204:207],v[172:175],a[36:39]
buffer_load_dwordx4 v4,s[20:23],s47 offen offset:1024 nt lds
v_mfma_f32_16x16x32_bf16 a[36:39],a[200:203],v[168:171],a[36:39]
ds_read_b128 a[160:163],v1 offset:8192
v_mfma_f32_16x16x32_bf16 a[40:43],a[208:211],v[168:171],a[40:43]
v_exp_f32 v217,v217
v_mfma_f32_16x16x32_bf16 a[40:43],a[212:215],v[172:175],a[40:43]
ds_read_b128 a[164:167],v1 offset:8448
v_cvt_pk_bf16_f32 v204,v216,v217
v_mfma_f32_16x16x32_bf16 a[44:47],a[220:223],v[172:175],a[44:47]
v_exp_f32 v218,v218
v_mfma_f32_16x16x32_bf16 a[44:47],a[216:219],v[168:171],a[44:47]
ds_read_b128 a[168:171],v1 offset:8704
v_mfma_f32_16x16x32_bf16 a[48:51],a[224:227],v[168:171],a[48:51]
v_exp_f32 v219,v219
v_mfma_f32_16x16x32_bf16 a[48:51],a[228:231],v[172:175],a[48:51]
ds_read_b128 a[172:175],v1 offset:8960
v_cvt_pk_bf16_f32 v205,v218,v219
v_mfma_f32_16x16x32_bf16 a[52:55],a[236:239],v[172:175],a[52:55]
v_exp_f32 v220,v220
v_mfma_f32_16x16x32_bf16 a[52:55],a[232:235],v[168:171],a[52:55]
ds_read_b128 a[188:191],v1 offset:13056
v_mfma_f32_16x16x32_bf16 a[56:59],a[240:243],v[168:171],a[56:59]
v_exp_f32 v221,v221
v_mfma_f32_16x16x32_bf16 a[56:59],a[244:247],v[172:175],a[56:59]
ds_read_b128 a[184:187],v1 offset:12800
v_cvt_pk_bf16_f32 v206,v220,v221
v_mfma_f32_16x16x32_bf16 a[60:63],a[252:255],v[172:175],a[60:63]
v_exp_f32 v222,v222
v_mfma_f32_16x16x32_bf16 a[60:63],a[248:251],v[168:171],a[60:63]
ds_read_b128 a[180:183],v1 offset:12544
v_smfmac_f32_16x16x64_bf16 v[52:55],v[28:31],v[168:175],v27
s_waitcnt lgkmcnt(4)
v_mfma_f32_16x16x32_bf16 v[136:139],a[128:131],v[64:67],v[32:35]
buffer_load_dwordx4 v5,s[20:23],s47 offen offset:2048 nt lds
v_mfma_f32_16x16x32_bf16 v[136:139],a[132:135],v[68:71],v[136:139]
ds_read_b128 a[176:179],v1 offset:12288
v_add_u32 v1,s48,v1
v_mfma_f32_16x16x32_bf16 v[136:139],a[136:139],v[72:75],v[136:139]
v_exp_f32 v223,v223
v_mfma_f32_16x16x32_bf16 v[136:139],a[140:143],v[76:79],v[136:139]
v_cvt_pk_bf16_f32 v207,v222,v223
v_mfma_f32_16x16x32_bf16 v[140:143],a[156:159],v[76:79],v[32:35]
v_mfma_f32_16x16x32_bf16 v[140:143],a[152:155],v[72:75],v[140:143]
v_mfma_f32_16x16x32_bf16 v[140:143],a[148:151],v[68:71],v[140:143]
v_mfma_f32_16x16x32_bf16 v[140:143],a[144:147],v[64:67],v[140:143]
v_mfma_f32_16x16x32_bf16 v[152:155],a[160:163],v[64:67],v[32:35]
v_mfma_f32_16x16x32_bf16 v[152:155],a[164:167],v[68:71],v[152:155]
v_mfma_f32_16x16x32_bf16 v[152:155],a[168:171],v[72:75],v[152:155]
s_waitcnt lgkmcnt(0)
v_mfma_f32_16x16x32_bf16 v[152:155],a[172:175],v[76:79],v[152:155]
v_mfma_f32_16x16x32_bf16 v[156:159],a[188:191],v[76:79],v[32:35]
v_mfma_f32_16x16x32_bf16 v[156:159],a[184:187],v[72:75],v[156:159]
buffer_load_dwordx4 v5,s[20:23],s47 offen offset:3072 nt lds
v_mfma_f32_16x16x32_bf16 v[156:159],a[180:183],v[68:71],v[156:159]
v_mfma_f32_16x16x32_bf16 v[156:159],a[176:179],v[64:67],v[156:159]
v_mfma_f32_16x16x32_bf16 a[64:67],a[192:195],v[200:203],a[64:67]
v_mfma_f32_16x16x32_bf16 a[64:67],a[196:199],v[204:207],a[64:67]
v_mfma_f32_16x16x32_bf16 a[68:71],a[204:207],v[204:207],a[68:71]
v_mfma_f32_16x16x32_bf16 a[68:71],a[200:203],v[200:203],a[68:71]
v_mfma_f32_16x16x32_bf16 a[72:75],a[208:211],v[200:203],a[72:75]
v_mfma_f32_16x16x32_bf16 a[72:75],a[212:215],v[204:207],a[72:75]
v_exp_f32 v136,v136
v_mfma_f32_16x16x32_bf16 a[76:79],a[220:223],v[204:207],a[76:79]
v_exp_f32 v137,v137
v_mfma_f32_16x16x32_bf16 a[76:79],a[216:219],v[200:203],a[76:79]
v_exp_f32 v138,v138
v_mfma_f32_16x16x32_bf16 a[80:83],a[224:227],v[200:203],a[80:83]
s_addk_i32 m0,16384
v_exp_f32 v139,v139
v_mfma_f32_16x16x32_bf16 a[80:83],a[228:231],v[204:207],a[80:83]
v_exp_f32 v140,v140
v_mfma_f32_16x16x32_bf16 a[84:87],a[236:239],v[204:207],a[84:87]
v_exp_f32 v141,v141
v_mfma_f32_16x16x32_bf16 a[84:87],a[232:235],v[200:203],a[84:87]
buffer_load_dwordx4 v4,s[24:27],s47 offen nt lds
v_mfma_f32_16x16x32_bf16 a[88:91],a[240:243],v[200:203],a[88:91]
v_exp_f32 v142,v142
v_mfma_f32_16x16x32_bf16 a[88:91],a[244:247],v[204:207],a[88:91]
v_exp_f32 v143,v143
v_mfma_f32_16x16x32_bf16 a[92:95],a[252:255],v[204:207],a[92:95]
v_mfma_f32_16x16x32_bf16 a[92:95],a[248:251],v[200:203],a[92:95]
v_smfmac_f32_16x16x64_bf16 v[56:59],v[28:31],v[200:207],v27
v_mfma_f32_16x16x32_bf16 v[168:171],a[128:131],v[80:83],v[36:39]
v_mfma_f32_16x16x32_bf16 v[168:171],a[132:135],v[84:87],v[168:171]
v_mfma_f32_16x16x32_bf16 v[168:171],a[136:139],v[88:91],v[168:171]
v_exp_f32 v152,v152
v_mfma_f32_16x16x32_bf16 v[168:171],a[140:143],v[92:95],v[168:171]
v_exp_f32 v153,v153
v_mfma_f32_16x16x32_bf16 v[172:175],a[156:159],v[92:95],v[36:39]
v_exp_f32 v154,v154
v_mfma_f32_16x16x32_bf16 v[172:175],a[152:155],v[88:91],v[172:175]
v_exp_f32 v155,v155
v_mfma_f32_16x16x32_bf16 v[172:175],a[148:151],v[84:87],v[172:175]
v_exp_f32 v156,v156
v_mfma_f32_16x16x32_bf16 v[172:175],a[144:147],v[80:83],v[172:175]
v_exp_f32 v157,v157
v_mfma_f32_16x16x32_bf16 v[184:187],a[160:163],v[80:83],v[36:39]
buffer_load_dwordx4 v5,s[24:27],s47 offen offset:1024 nt lds
v_mfma_f32_16x16x32_bf16 v[184:187],a[164:167],v[84:87],v[184:187]
v_exp_f32 v158,v158
v_mfma_f32_16x16x32_bf16 v[184:187],a[168:171],v[88:91],v[184:187]
v_exp_f32 v159,v159
v_mfma_f32_16x16x32_bf16 v[184:187],a[172:175],v[92:95],v[184:187]
v_exp_f32 v168,v168
v_mfma_f32_16x16x32_bf16 v[188:191],a[188:191],v[92:95],v[36:39]
v_exp_f32 v169,v169
v_mfma_f32_16x16x32_bf16 v[188:191],a[184:187],v[88:91],v[188:191]
v_exp_f32 v170,v170
v_mfma_f32_16x16x32_bf16 v[188:191],a[180:183],v[84:87],v[188:191]
v_exp_f32 v171,v171
v_mfma_f32_16x16x32_bf16 v[188:191],a[176:179],v[80:83],v[188:191]
v_exp_f32 v172,v172
ds_read_b64_tr_b16 a[192:193],v2
v_cvt_pk_bf16_f32 v168,v168,v169
ds_read_b64_tr_b16 a[194:195],v2 offset:4096
v_cvt_pk_bf16_f32 v169,v170,v171
v_exp_f32 v173,v173
ds_read_b64_tr_b16 a[196:197],v2 offset:8192
v_cvt_pk_bf16_f32 v170,v172,v173
v_exp_f32 v174,v174
v_exp_f32 v175,v175
buffer_load_dwordx4 v4,s[24:27],s47 offen offset:2048 nt lds
ds_read_b64_tr_b16 a[198:199],v2 offset:12288
v_cvt_pk_bf16_f32 v171,v174,v175
v_exp_f32 v184,v184
v_exp_f32 v185,v185
ds_read_b64_tr_b16 a[204:205],v3 offset:8192
v_cvt_pk_bf16_f32 v136,v136,v137
ds_read_b64_tr_b16 a[206:207],v3 offset:12288
v_cvt_pk_bf16_f32 v172,v184,v185
ds_read_b64_tr_b16 a[200:201],v3
ds_read_b64_tr_b16 a[202:203],v3 offset:4096
v_cvt_pk_bf16_f32 v137,v138,v139
ds_read_b64_tr_b16 a[208:209],v2 offset:256
ds_read_b64_tr_b16 a[210:211],v2 offset:4352
ds_read_b64_tr_b16 a[212:213],v2 offset:8448
v_mfma_f32_16x16x32_bf16 v[200:203],a[128:131],v[96:99],v[40:43]
ds_read_b64_tr_b16 a[214:215],v2 offset:12544
v_cvt_pk_bf16_f32 v138,v140,v141
v_mfma_f32_16x16x32_bf16 v[200:203],a[132:135],v[100:103],v[200:203]
ds_read_b64_tr_b16 a[220:221],v3 offset:8448
v_mfma_f32_16x16x32_bf16 v[200:203],a[136:139],v[104:107],v[200:203]
ds_read_b64_tr_b16 a[222:223],v3 offset:12544
v_cvt_pk_bf16_f32 v139,v142,v143
v_mfma_f32_16x16x32_bf16 v[200:203],a[140:143],v[108:111],v[200:203]
buffer_load_dwordx4 v5,s[24:27],s47 offen offset:3072 nt lds
v_mfma_f32_16x16x32_bf16 v[204:207],a[156:159],v[108:111],v[40:43]
s_or_b32 s47,s47,0x18000
s_addk_i32 s47,4096
ds_read_b64_tr_b16 a[216:217],v3 offset:256
v_mfma_f32_16x16x32_bf16 v[204:207],a[152:155],v[104:107],v[204:207]
s_and_b32 s47,s47,0x27000
ds_read_b64_tr_b16 a[218:219],v3 offset:4352
v_cvt_pk_bf16_f32 v140,v152,v153
v_mfma_f32_16x16x32_bf16 v[204:207],a[148:151],v[100:103],v[204:207]
s_or_b32 s47,s47,s46
ds_read_b64_tr_b16 a[224:225],v2 offset:512
v_mfma_f32_16x16x32_bf16 v[204:207],a[144:147],v[96:99],v[204:207]
ds_read_b64_tr_b16 a[226:227],v2 offset:4608
v_mfma_f32_16x16x32_bf16 v[216:219],a[160:163],v[96:99],v[40:43]
ds_read_b64_tr_b16 a[228:229],v2 offset:8704
v_cvt_pk_bf16_f32 v141,v154,v155
v_mfma_f32_16x16x32_bf16 v[216:219],a[164:167],v[100:103],v[216:219]
ds_read_b64_tr_b16 a[230:231],v2 offset:12800
v_mfma_f32_16x16x32_bf16 v[216:219],a[168:171],v[104:107],v[216:219]
ds_read_b64_tr_b16 a[236:237],v3 offset:8704
v_mfma_f32_16x16x32_bf16 v[216:219],a[172:175],v[108:111],v[216:219]
ds_read_b64_tr_b16 a[238:239],v3 offset:12800
v_cvt_pk_bf16_f32 v142,v156,v157
v_mfma_f32_16x16x32_bf16 v[220:223],a[188:191],v[108:111],v[40:43]
ds_read_b64_tr_b16 a[232:233],v3 offset:512
v_mfma_f32_16x16x32_bf16 v[220:223],a[184:187],v[104:107],v[220:223]
ds_read_b64_tr_b16 a[234:235],v3 offset:4608
v_mfma_f32_16x16x32_bf16 v[220:223],a[180:183],v[100:103],v[220:223]
ds_read_b64_tr_b16 a[240:241],v2 offset:768
v_cvt_pk_bf16_f32 v143,v158,v159
v_mfma_f32_16x16x32_bf16 v[220:223],a[176:179],v[96:99],v[220:223]
s_waitcnt lgkmcnt(7)
buffer_load_dword v26,v24,s[88:91],s93 offen
v_mfma_f32_16x16x32_bf16 a[0:3],a[192:195],v[136:139],a[0:3]
ds_read_b64_tr_b16 a[242:243],v2 offset:4864
v_mfma_f32_16x16x32_bf16 a[0:3],a[196:199],v[140:143],a[0:3]
ds_read_b64_tr_b16 a[244:245],v2 offset:8960
v_mfma_f32_16x16x32_bf16 a[4:7],a[204:207],v[140:143],a[4:7]
ds_read_b64_tr_b16 a[246:247],v2 offset:13056
v_mfma_f32_16x16x32_bf16 a[4:7],a[200:203],v[136:139],a[4:7]
ds_read_b64_tr_b16 a[252:253],v3 offset:8960
v_mfma_f32_16x16x32_bf16 a[8:11],a[208:211],v[136:139],a[8:11]
ds_read_b64_tr_b16 a[254:255],v3 offset:13056
v_mfma_f32_16x16x32_bf16 a[8:11],a[212:215],v[140:143],a[8:11]
ds_read_b64_tr_b16 a[248:249],v3 offset:768
v_mfma_f32_16x16x32_bf16 a[12:15],a[220:223],v[140:143],a[12:15]
ds_read_b64_tr_b16 a[250:251],v3 offset:4864
v_mfma_f32_16x16x32_bf16 a[12:15],a[216:219],v[136:139],a[12:15]
s_bitcmp1_b32 s45,0
s_cbranch_scc0 L_t31v30_%=
s_mov_b32 m0,s80
s_addk_i32 s80,4096
buffer_load_dwordx4 v25,s[20:23],s83 offen nt lds
s_addk_i32 s83,1024
L_t31v30_%=:
v_mfma_f32_16x16x32_bf16 a[16:19],a[224:227],v[136:139],a[16:19]
s_waitcnt lgkmcnt(6)
v_exp_f32 v186,v186
v_mfma_f32_16x16x32_bf16 a[16:19],a[228:231],v[140:143],a[16:19]
v_exp_f32 v187,v187
v_mfma_f32_16x16x32_bf16 a[20:23],a[236:239],v[140:143],a[20:23]
v_exp_f32 v188,v188
v_mfma_f32_16x16x32_bf16 a[20:23],a[232:235],v[136:139],a[20:23]
v_exp_f32 v189,v189
v_mfma_f32_16x16x32_bf16 a[24:27],a[240:243],v[136:139],a[24:27]
s_waitcnt lgkmcnt(2)
v_exp_f32 v190,v190
v_mfma_f32_16x16x32_bf16 a[24:27],a[244:247],v[140:143],a[24:27]
v_cvt_pk_bf16_f32 v173,v186,v187
v_mfma_f32_16x16x32_bf16 a[28:31],a[252:255],v[140:143],a[28:31]
s_waitcnt lgkmcnt(0)
v_exp_f32 v191,v191
v_mfma_f32_16x16x32_bf16 a[28:31],a[248:251],v[136:139],a[28:31]
v_cvt_pk_bf16_f32 v174,v188,v189
v_cvt_pk_bf16_f32 v175,v190,v191
s_cmp_lg_u32 s53,0
v_smfmac_f32_16x16x64_bf16 v[48:51],v[28:31],v[136:143],v27
s_cbranch_scc1 L_att0v3_%=
s_waitcnt lgkmcnt(0)
s_nop 7
s_branch L_att0x_%=
.p2align 5
L_att0v0_%=:
v_mfma_f32_16x16x32_bf16 v[232:235],a[128:131],v[112:115],v[44:47]
s_waitcnt vmcnt(8) lgkmcnt(0)
v_exp_f32 v200,v200
v_mfma_f32_16x16x32_bf16 v[232:235],a[132:135],v[116:119],v[232:235]
s_barrier
s_add_u32 m0,s49,s51
ds_read_b128 a[128:131],v1
v_mfma_f32_16x16x32_bf16 v[232:235],a[136:139],v[120:123],v[232:235]
s_mov_b32 s90,L_endseg_%=-L_pc_%=+128
v_exp_f32 v201,v201
v_mfma_f32_16x16x32_bf16 v[232:235],a[140:143],v[124:127],v[232:235]
s_lshr_b32 s45,s45,1
ds_read_b128 a[132:135],v1 offset:256
v_cvt_pk_bf16_f32 v200,v200,v201
v_mfma_f32_16x16x32_bf16 v[236:239],a[156:159],v[124:127],v[44:47]
buffer_load_dwordx4 v4,s[20:23],s47 offen nt lds
v_mfma_f32_16x16x32_bf16 v[236:239],a[152:155],v[120:123],v[236:239]
ds_read_b128 a[136:139],v1 offset:512
v_add_u32 v2,s48,v2
v_mfma_f32_16x16x32_bf16 v[236:239],a[148:151],v[116:119],v[236:239]
v_exp_f32 v202,v202
)A"
R"A(v_mfma_f32_16x16x32_bf16 v[236:239],a[144:147],v[112:115],v[236:239]
s_add_u32 s62,s50,32768
ds_read_b128 a[140:143],v1 offset:768
v_add_u32 v3,s48,v3
v_mfma_f32_16x16x32_bf16 v[248:251],a[160:163],v[112:115],v[44:47]
s_and_b32 s62,s62,131071
v_exp_f32 v203,v203
v_mfma_f32_16x16x32_bf16 v[248:251],a[164:167],v[116:119],v[248:251]
s_addk_i32 s54,16384
ds_read_b128 a[156:159],v1 offset:4864
v_cvt_pk_bf16_f32 v201,v202,v203
v_mfma_f32_16x16x32_bf16 v[248:251],a[168:171],v[120:123],v[248:251]
s_sub_u32 s48,s62,s50
v_exp_f32 v204,v204
v_mfma_f32_16x16x32_bf16 v[248:251],a[172:175],v[124:127],v[248:251]
s_sub_u32 s53,s53,1
v_exp_f32 v205,v205
v_mfma_f32_16x16x32_bf16 v[252:255],a[188:191],v[124:127],v[44:47]
s_and_b32 s46,s54,0xfffc0000
ds_read_b128 a[152:155],v1 offset:4608
v_cvt_pk_bf16_f32 v202,v204,v205
v_mfma_f32_16x16x32_bf16 v[252:255],a[184:187],v[120:123],v[252:255]
s_mov_b32 s50,s62
v_exp_f32 v206,v206
v_mfma_f32_16x16x32_bf16 v[252:255],a[180:183],v[116:119],v[252:255]
s_xor_b32 s49,s50,65536
v_exp_f32 v207,v207
v_mfma_f32_16x16x32_bf16 v[252:255],a[176:179],v[112:115],v[252:255]
ds_read_b128 a[148:151],v1 offset:4352
v_cvt_pk_bf16_f32 v203,v206,v207
v_mfma_f32_16x16x32_bf16 a[32:35],a[192:195],v[168:171],a[32:35]
v_exp_f32 v216,v216
v_mfma_f32_16x16x32_bf16 a[32:35],a[196:199],v[172:175],a[32:35]
ds_read_b128 a[144:147],v1 offset:4096
v_mfma_f32_16x16x32_bf16 a[36:39],a[204:207],v[172:175],a[36:39]
buffer_load_dwordx4 v4,s[20:23],s47 offen offset:1024 nt lds
v_mfma_f32_16x16x32_bf16 a[36:39],a[200:203],v[168:171],a[36:39]
ds_read_b128 a[160:163],v1 offset:8192
v_mfma_f32_16x16x32_bf16 a[40:43],a[208:211],v[168:171],a[40:43]
v_exp_f32 v217,v217
v_mfma_f32_16x16x32_bf16 a[40:43],a[212:215],v[172:175],a[40:43]
ds_read_b128 a[164:167],v1 offset:8448
v_cvt_pk_bf16_f32 v204,v216,v217
v_mfma_f32_16x16x32_bf16 a[44:47],a[220:223],v[172:175],a[44:47]
v_exp_f32 v218,v218
v_mfma_f32_16x16x32_bf16 a[44:47],a[216:219],v[168:171],a[44:47]
ds_read_b128 a[168:171],v1 offset:8704
v_mfma_f32_16x16x32_bf16 a[48:51],a[224:227],v[168:171],a[48:51]
v_exp_f32 v219,v219
v_mfma_f32_16x16x32_bf16 a[48:51],a[228:231],v[172:175],a[48:51]
ds_read_b128 a[172:175],v1 offset:8960
v_cvt_pk_bf16_f32 v205,v218,v219
v_mfma_f32_16x16x32_bf16 a[52:55],a[236:239],v[172:175],a[52:55]
v_exp_f32 v220,v220
v_mfma_f32_16x16x32_bf16 a[52:55],a[232:235],v[168:171],a[52:55]
ds_read_b128 a[188:191],v1 offset:13056
v_mfma_f32_16x16x32_bf16 a[56:59],a[240:243],v[168:171],a[56:59]
v_exp_f32 v221,v221
v_mfma_f32_16x16x32_bf16 a[56:59],a[244:247],v[172:175],a[56:59]
ds_read_b128 a[184:187],v1 offset:12800
v_cvt_pk_bf16_f32 v206,v220,v221
v_mfma_f32_16x16x32_bf16 a[60:63],a[252:255],v[172:175],a[60:63]
v_exp_f32 v222,v222
v_mfma_f32_16x16x32_bf16 a[60:63],a[248:251],v[168:171],a[60:63]
ds_read_b128 a[180:183],v1 offset:12544
v_smfmac_f32_16x16x64_bf16 v[52:55],v[28:31],v[168:175],v27
buffer_load_dwordx4 v5,s[20:23],s47 offen offset:2048 nt lds
ds_read_b128 a[176:179],v1 offset:12288
v_add_u32 v1,s48,v1
v_exp_f32 v223,v223
s_nop 0
v_cvt_pk_bf16_f32 v207,v222,v223
v_exp_f32 v232,v232
v_exp_f32 v233,v233
v_exp_f32 v234,v234
v_exp_f32 v235,v235
v_exp_f32 v236,v236
v_exp_f32 v237,v237
v_exp_f32 v238,v238
v_exp_f32 v239,v239
v_exp_f32 v248,v248
buffer_load_dwordx4 v5,s[20:23],s47 offen offset:3072 nt lds
v_exp_f32 v249,v249
v_exp_f32 v250,v250
v_mfma_f32_16x16x32_bf16 a[64:67],a[192:195],v[200:203],a[64:67]
v_exp_f32 v251,v251
v_mfma_f32_16x16x32_bf16 a[64:67],a[196:199],v[204:207],a[64:67]
v_exp_f32 v252,v252
v_mfma_f32_16x16x32_bf16 a[68:71],a[204:207],v[204:207],a[68:71]
v_exp_f32 v253,v253
v_mfma_f32_16x16x32_bf16 a[68:71],a[200:203],v[200:203],a[68:71]
v_exp_f32 v254,v254
v_mfma_f32_16x16x32_bf16 a[72:75],a[208:211],v[200:203],a[72:75]
v_exp_f32 v255,v255
v_mfma_f32_16x16x32_bf16 a[72:75],a[212:215],v[204:207],a[72:75]
v_mfma_f32_16x16x32_bf16 a[76:79],a[220:223],v[204:207],a[76:79]
v_mfma_f32_16x16x32_bf16 a[76:79],a[216:219],v[200:203],a[76:79]
v_mfma_f32_16x16x32_bf16 a[80:83],a[224:227],v[200:203],a[80:83]
s_addk_i32 m0,16384
v_mfma_f32_16x16x32_bf16 a[80:83],a[228:231],v[204:207],a[80:83]
v_mfma_f32_16x16x32_bf16 a[84:87],a[236:239],v[204:207],a[84:87]
v_mfma_f32_16x16x32_bf16 a[84:87],a[232:235],v[200:203],a[84:87]
buffer_load_dwordx4 v4,s[24:27],s47 offen nt lds
v_mfma_f32_16x16x32_bf16 a[88:91],a[240:243],v[200:203],a[88:91]
v_mfma_f32_16x16x32_bf16 a[88:91],a[244:247],v[204:207],a[88:91]
v_mfma_f32_16x16x32_bf16 a[92:95],a[252:255],v[204:207],a[92:95]
v_cvt_pk_bf16_f32 v232,v232,v233
v_cvt_pk_bf16_f32 v233,v234,v235
v_mfma_f32_16x16x32_bf16 a[92:95],a[248:251],v[200:203],a[92:95]
v_cvt_pk_bf16_f32 v234,v236,v237
v_cvt_pk_bf16_f32 v235,v238,v239
v_smfmac_f32_16x16x64_bf16 v[56:59],v[28:31],v[200:207],v27
s_waitcnt lgkmcnt(0)
v_cvt_pk_bf16_f32 v236,v248,v249
v_mfma_f32_16x16x32_bf16 v[168:171],a[128:131],v[80:83],v[36:39]
v_cvt_pk_bf16_f32 v237,v250,v251
v_cvt_pk_bf16_f32 v238,v252,v253
v_mfma_f32_16x16x32_bf16 v[168:171],a[132:135],v[84:87],v[168:171]
v_cvt_pk_bf16_f32 v239,v254,v255
v_mfma_f32_16x16x32_bf16 v[168:171],a[136:139],v[88:91],v[168:171]
v_mfma_f32_16x16x32_bf16 v[168:171],a[140:143],v[92:95],v[168:171]
v_mfma_f32_16x16x32_bf16 v[172:175],a[156:159],v[92:95],v[36:39]
v_mfma_f32_16x16x32_bf16 v[172:175],a[152:155],v[88:91],v[172:175]
v_mfma_f32_16x16x32_bf16 v[172:175],a[148:151],v[84:87],v[172:175]
v_mfma_f32_16x16x32_bf16 v[172:175],a[144:147],v[80:83],v[172:175]
v_mfma_f32_16x16x32_bf16 v[184:187],a[160:163],v[80:83],v[36:39]
buffer_load_dwordx4 v5,s[24:27],s47 offen offset:1024 nt lds
v_mfma_f32_16x16x32_bf16 v[184:187],a[164:167],v[84:87],v[184:187]
v_mfma_f32_16x16x32_bf16 v[184:187],a[168:171],v[88:91],v[184:187]
v_mfma_f32_16x16x32_bf16 v[184:187],a[172:175],v[92:95],v[184:187]
v_exp_f32 v168,v168
v_mfma_f32_16x16x32_bf16 v[188:191],a[188:191],v[92:95],v[36:39]
v_exp_f32 v169,v169
v_mfma_f32_16x16x32_bf16 v[188:191],a[184:187],v[88:91],v[188:191]
v_exp_f32 v170,v170
v_mfma_f32_16x16x32_bf16 v[188:191],a[180:183],v[84:87],v[188:191]
v_exp_f32 v171,v171
v_mfma_f32_16x16x32_bf16 v[188:191],a[176:179],v[80:83],v[188:191]
v_exp_f32 v172,v172
v_mfma_f32_16x16x32_bf16 a[96:99],a[192:195],v[232:235],a[96:99]
ds_read_b64_tr_b16 a[192:193],v2
v_cvt_pk_bf16_f32 v168,v168,v169
v_mfma_f32_16x16x32_bf16 a[96:99],a[196:199],v[236:239],a[96:99]
ds_read_b64_tr_b16 a[194:195],v2 offset:4096
v_cvt_pk_bf16_f32 v169,v170,v171
v_mfma_f32_16x16x32_bf16 a[100:103],a[204:207],v[236:239],a[100:103]
v_exp_f32 v173,v173
v_mfma_f32_16x16x32_bf16 a[100:103],a[200:203],v[232:235],a[100:103]
ds_read_b64_tr_b16 a[196:197],v2 offset:8192
v_cvt_pk_bf16_f32 v170,v172,v173
v_mfma_f32_16x16x32_bf16 a[104:107],a[208:211],v[232:235],a[104:107]
v_exp_f32 v174,v174
v_mfma_f32_16x16x32_bf16 a[104:107],a[212:215],v[236:239],a[104:107]
v_exp_f32 v175,v175
v_mfma_f32_16x16x32_bf16 a[108:111],a[220:223],v[236:239],a[108:111]
buffer_load_dwordx4 v4,s[24:27],s47 offen offset:2048 nt lds
v_mfma_f32_16x16x32_bf16 a[108:111],a[216:219],v[232:235],a[108:111]
ds_read_b64_tr_b16 a[198:199],v2 offset:12288
v_cvt_pk_bf16_f32 v171,v174,v175
v_mfma_f32_16x16x32_bf16 a[112:115],a[224:227],v[232:235],a[112:115]
v_exp_f32 v184,v184
v_mfma_f32_16x16x32_bf16 a[112:115],a[228:231],v[236:239],a[112:115]
v_exp_f32 v185,v185
v_mfma_f32_16x16x32_bf16 a[116:119],a[236:239],v[236:239],a[116:119]
ds_read_b64_tr_b16 a[204:205],v3 offset:8192
v_mfma_f32_16x16x32_bf16 a[116:119],a[232:235],v[232:235],a[116:119]
ds_read_b64_tr_b16 a[206:207],v3 offset:12288
v_cvt_pk_bf16_f32 v172,v184,v185
v_mfma_f32_16x16x32_bf16 a[120:123],a[240:243],v[232:235],a[120:123]
ds_read_b64_tr_b16 a[200:201],v3
v_mfma_f32_16x16x32_bf16 a[120:123],a[244:247],v[236:239],a[120:123]
ds_read_b64_tr_b16 a[202:203],v3 offset:4096
v_mfma_f32_16x16x32_bf16 a[124:127],a[252:255],v[236:239],a[124:127]
ds_read_b64_tr_b16 a[208:209],v2 offset:256
v_mfma_f32_16x16x32_bf16 a[124:127],a[248:251],v[232:235],a[124:127]
ds_read_b64_tr_b16 a[210:211],v2 offset:4352
v_smfmac_f32_16x16x64_bf16 v[60:63],v[28:31],v[232:239],v27
ds_read_b64_tr_b16 a[212:213],v2 offset:8448
v_mfma_f32_16x16x32_bf16 v[200:203],a[128:131],v[96:99],v[40:43]
ds_read_b64_tr_b16 a[214:215],v2 offset:12544
v_mfma_f32_16x16x32_bf16 v[200:203],a[132:135],v[100:103],v[200:203]
ds_read_b64_tr_b16 a[220:221],v3 offset:8448
v_mfma_f32_16x16x32_bf16 v[200:203],a[136:139],v[104:107],v[200:203]
ds_read_b64_tr_b16 a[222:223],v3 offset:12544
v_mfma_f32_16x16x32_bf16 v[200:203],a[140:143],v[108:111],v[200:203]
buffer_load_dwordx4 v5,s[24:27],s47 offen offset:3072 nt lds
v_mfma_f32_16x16x32_bf16 v[204:207],a[156:159],v[108:111],v[40:43]
s_or_b32 s47,s47,0x18000
s_addk_i32 s47,4096
ds_read_b64_tr_b16 a[216:217],v3 offset:256
v_mfma_f32_16x16x32_bf16 v[204:207],a[152:155],v[104:107],v[204:207]
s_and_b32 s47,s47,0x27000
ds_read_b64_tr_b16 a[218:219],v3 offset:4352
v_mfma_f32_16x16x32_bf16 v[204:207],a[148:151],v[100:103],v[204:207]
s_or_b32 s47,s47,s46
ds_read_b64_tr_b16 a[224:225],v2 offset:512
v_mfma_f32_16x16x32_bf16 v[204:207],a[144:147],v[96:99],v[204:207]
ds_read_b64_tr_b16 a[226:227],v2 offset:4608
v_mfma_f32_16x16x32_bf16 v[216:219],a[160:163],v[96:99],v[40:43]
ds_read_b64_tr_b16 a[228:229],v2 offset:8704
v_mfma_f32_16x16x32_bf16 v[216:219],a[164:167],v[100:103],v[216:219]
ds_read_b64_tr_b16 a[230:231],v2 offset:12800
v_mfma_f32_16x16x32_bf16 v[216:219],a[168:171],v[104:107],v[216:219]
ds_read_b64_tr_b16 a[236:237],v3 offset:8704
v_mfma_f32_16x16x32_bf16 v[216:219],a[172:175],v[108:111],v[216:219]
ds_read_b64_tr_b16 a[238:239],v3 offset:12800
v_mfma_f32_16x16x32_bf16 v[220:223],a[188:191],v[108:111],v[40:43]
ds_read_b64_tr_b16 a[232:233],v3 offset:512
v_mfma_f32_16x16x32_bf16 v[220:223],a[184:187],v[104:107],v[220:223]
ds_read_b64_tr_b16 a[234:235],v3 offset:4608
v_mfma_f32_16x16x32_bf16 v[220:223],a[180:183],v[100:103],v[220:223]
ds_read_b64_tr_b16 a[240:241],v2 offset:768
v_mfma_f32_16x16x32_bf16 v[220:223],a[176:179],v[96:99],v[220:223]
buffer_load_dword v26,v24,s[88:91],s93 offen
ds_read_b64_tr_b16 a[242:243],v2 offset:4864
ds_read_b64_tr_b16 a[244:245],v2 offset:8960
ds_read_b64_tr_b16 a[246:247],v2 offset:13056
ds_read_b64_tr_b16 a[252:253],v3 offset:8960
ds_read_b64_tr_b16 a[254:255],v3 offset:13056
ds_read_b64_tr_b16 a[248:249],v3 offset:768
ds_read_b64_tr_b16 a[250:251],v3 offset:4864
s_bitcmp1_b32 s45,0
s_cbranch_scc0 L_t31v00_%=
s_mov_b32 m0,s80
s_addk_i32 s80,4096
buffer_load_dwordx4 v25,s[20:23],s83 offen nt lds
s_addk_i32 s83,1024
L_t31v00_%=:
v_exp_f32 v186,v186
v_exp_f32 v187,v187
v_exp_f32 v188,v188
v_exp_f32 v189,v189
v_exp_f32 v190,v190
v_cvt_pk_bf16_f32 v173,v186,v187
v_exp_f32 v191,v191
v_cvt_pk_bf16_f32 v174,v188,v189
v_cvt_pk_bf16_f32 v175,v190,v191
s_cmp_lg_u32 s53,0
s_cbranch_scc1 L_att0v0_%=
s_waitcnt lgkmcnt(0)
s_nop 7
s_branch L_att0x_%=
.p2align 5
L_att0v1_%=:
v_mfma_f32_16x16x32_bf16 v[232:235],a[128:131],v[112:115],v[44:47]
s_waitcnt vmcnt(8) lgkmcnt(0)
v_exp_f32 v200,v200
v_mfma_f32_16x16x32_bf16 v[232:235],a[132:135],v[116:119],v[232:235]
s_barrier
s_add_u32 m0,s49,s51
ds_read_b128 a[128:131],v1
v_mfma_f32_16x16x32_bf16 v[232:235],a[136:139],v[120:123],v[232:235]
s_mov_b32 s90,L_endseg_%=-L_pc_%=+128
v_exp_f32 v201,v201
v_mfma_f32_16x16x32_bf16 v[232:235],a[140:143],v[124:127],v[232:235]
s_lshr_b32 s45,s45,1
ds_read_b128 a[132:135],v1 offset:256
v_cvt_pk_bf16_f32 v200,v200,v201
v_mfma_f32_16x16x32_bf16 v[236:239],a[156:159],v[124:127],v[44:47]
buffer_load_dwordx4 v4,s[20:23],s47 offen nt lds
v_mfma_f32_16x16x32_bf16 v[236:239],a[152:155],v[120:123],v[236:239]
ds_read_b128 a[136:139],v1 offset:512
v_add_u32 v2,s48,v2
v_mfma_f32_16x16x32_bf16 v[236:239],a[148:151],v[116:119],v[236:239]
v_exp_f32 v202,v202
v_mfma_f32_16x16x32_bf16 v[236:239],a[144:147],v[112:115],v[236:239]
s_add_u32 s62,s50,32768
ds_read_b128 a[140:143],v1 offset:768
v_add_u32 v3,s48,v3
v_mfma_f32_16x16x32_bf16 v[248:251],a[160:163],v[112:115],v[44:47]
s_and_b32 s62,s62,131071
v_exp_f32 v203,v203
v_mfma_f32_16x16x32_bf16 v[248:251],a[164:167],v[116:119],v[248:251]
)A"
R"A(s_addk_i32 s54,16384
ds_read_b128 a[156:159],v1 offset:4864
v_cvt_pk_bf16_f32 v201,v202,v203
v_mfma_f32_16x16x32_bf16 v[248:251],a[168:171],v[120:123],v[248:251]
s_sub_u32 s48,s62,s50
v_exp_f32 v204,v204
v_mfma_f32_16x16x32_bf16 v[248:251],a[172:175],v[124:127],v[248:251]
s_sub_u32 s53,s53,1
v_exp_f32 v205,v205
v_mfma_f32_16x16x32_bf16 v[252:255],a[188:191],v[124:127],v[44:47]
s_and_b32 s46,s54,0xfffc0000
ds_read_b128 a[152:155],v1 offset:4608
v_cvt_pk_bf16_f32 v202,v204,v205
v_mfma_f32_16x16x32_bf16 v[252:255],a[184:187],v[120:123],v[252:255]
s_mov_b32 s50,s62
v_exp_f32 v206,v206
v_mfma_f32_16x16x32_bf16 v[252:255],a[180:183],v[116:119],v[252:255]
s_xor_b32 s49,s50,65536
v_exp_f32 v207,v207
v_mfma_f32_16x16x32_bf16 v[252:255],a[176:179],v[112:115],v[252:255]
ds_read_b128 a[148:151],v1 offset:4352
v_cvt_pk_bf16_f32 v203,v206,v207
v_exp_f32 v216,v216
ds_read_b128 a[144:147],v1 offset:4096
buffer_load_dwordx4 v4,s[20:23],s47 offen offset:1024 nt lds
ds_read_b128 a[160:163],v1 offset:8192
v_exp_f32 v217,v217
ds_read_b128 a[164:167],v1 offset:8448
v_cvt_pk_bf16_f32 v204,v216,v217
v_exp_f32 v218,v218
ds_read_b128 a[168:171],v1 offset:8704
v_exp_f32 v219,v219
ds_read_b128 a[172:175],v1 offset:8960
v_cvt_pk_bf16_f32 v205,v218,v219
v_exp_f32 v220,v220
ds_read_b128 a[188:191],v1 offset:13056
v_exp_f32 v221,v221
ds_read_b128 a[184:187],v1 offset:12800
v_cvt_pk_bf16_f32 v206,v220,v221
v_exp_f32 v222,v222
ds_read_b128 a[180:183],v1 offset:12544
s_waitcnt lgkmcnt(11)
v_mfma_f32_16x16x32_bf16 v[136:139],a[128:131],v[64:67],v[32:35]
buffer_load_dwordx4 v5,s[20:23],s47 offen offset:2048 nt lds
v_mfma_f32_16x16x32_bf16 v[136:139],a[132:135],v[68:71],v[136:139]
ds_read_b128 a[176:179],v1 offset:12288
v_add_u32 v1,s48,v1
v_mfma_f32_16x16x32_bf16 v[136:139],a[136:139],v[72:75],v[136:139]
v_exp_f32 v223,v223
v_mfma_f32_16x16x32_bf16 v[136:139],a[140:143],v[76:79],v[136:139]
s_waitcnt lgkmcnt(11)
v_cvt_pk_bf16_f32 v207,v222,v223
v_mfma_f32_16x16x32_bf16 v[140:143],a[156:159],v[76:79],v[32:35]
s_waitcnt lgkmcnt(10)
v_exp_f32 v232,v232
v_mfma_f32_16x16x32_bf16 v[140:143],a[152:155],v[72:75],v[140:143]
s_waitcnt lgkmcnt(9)
v_exp_f32 v233,v233
v_mfma_f32_16x16x32_bf16 v[140:143],a[148:151],v[68:71],v[140:143]
s_waitcnt lgkmcnt(8)
v_exp_f32 v234,v234
v_mfma_f32_16x16x32_bf16 v[140:143],a[144:147],v[64:67],v[140:143]
s_waitcnt lgkmcnt(1)
v_exp_f32 v235,v235
v_mfma_f32_16x16x32_bf16 v[152:155],a[160:163],v[64:67],v[32:35]
v_exp_f32 v236,v236
v_mfma_f32_16x16x32_bf16 v[152:155],a[164:167],v[68:71],v[152:155]
v_exp_f32 v237,v237
v_mfma_f32_16x16x32_bf16 v[152:155],a[168:171],v[72:75],v[152:155]
v_exp_f32 v238,v238
v_mfma_f32_16x16x32_bf16 v[152:155],a[172:175],v[76:79],v[152:155]
v_exp_f32 v239,v239
v_mfma_f32_16x16x32_bf16 v[156:159],a[188:191],v[76:79],v[32:35]
v_exp_f32 v248,v248
v_mfma_f32_16x16x32_bf16 v[156:159],a[184:187],v[72:75],v[156:159]
buffer_load_dwordx4 v5,s[20:23],s47 offen offset:3072 nt lds
v_mfma_f32_16x16x32_bf16 v[156:159],a[180:183],v[68:71],v[156:159]
s_waitcnt lgkmcnt(0)
v_exp_f32 v249,v249
v_mfma_f32_16x16x32_bf16 v[156:159],a[176:179],v[64:67],v[156:159]
v_exp_f32 v250,v250
v_mfma_f32_16x16x32_bf16 a[64:67],a[192:195],v[200:203],a[64:67]
v_exp_f32 v251,v251
v_mfma_f32_16x16x32_bf16 a[64:67],a[196:199],v[204:207],a[64:67]
v_exp_f32 v252,v252
v_mfma_f32_16x16x32_bf16 a[68:71],a[204:207],v[204:207],a[68:71]
v_exp_f32 v253,v253
v_mfma_f32_16x16x32_bf16 a[68:71],a[200:203],v[200:203],a[68:71]
v_exp_f32 v254,v254
v_mfma_f32_16x16x32_bf16 a[72:75],a[208:211],v[200:203],a[72:75]
v_exp_f32 v255,v255
v_mfma_f32_16x16x32_bf16 a[72:75],a[212:215],v[204:207],a[72:75]
v_exp_f32 v136,v136
v_mfma_f32_16x16x32_bf16 a[76:79],a[220:223],v[204:207],a[76:79]
v_exp_f32 v137,v137
v_mfma_f32_16x16x32_bf16 a[76:79],a[216:219],v[200:203],a[76:79]
v_exp_f32 v138,v138
v_mfma_f32_16x16x32_bf16 a[80:83],a[224:227],v[200:203],a[80:83]
s_addk_i32 m0,16384
v_exp_f32 v139,v139
v_mfma_f32_16x16x32_bf16 a[80:83],a[228:231],v[204:207],a[80:83]
v_exp_f32 v140,v140
v_mfma_f32_16x16x32_bf16 a[84:87],a[236:239],v[204:207],a[84:87]
v_exp_f32 v141,v141
v_mfma_f32_16x16x32_bf16 a[84:87],a[232:235],v[200:203],a[84:87]
buffer_load_dwordx4 v4,s[24:27],s47 offen nt lds
v_mfma_f32_16x16x32_bf16 a[88:91],a[240:243],v[200:203],a[88:91]
v_exp_f32 v142,v142
v_mfma_f32_16x16x32_bf16 a[88:91],a[244:247],v[204:207],a[88:91]
v_exp_f32 v143,v143
v_mfma_f32_16x16x32_bf16 a[92:95],a[252:255],v[204:207],a[92:95]
v_cvt_pk_bf16_f32 v232,v232,v233
v_cvt_pk_bf16_f32 v233,v234,v235
v_mfma_f32_16x16x32_bf16 a[92:95],a[248:251],v[200:203],a[92:95]
v_cvt_pk_bf16_f32 v234,v236,v237
v_cvt_pk_bf16_f32 v235,v238,v239
v_smfmac_f32_16x16x64_bf16 v[56:59],v[28:31],v[200:207],v27
v_cvt_pk_bf16_f32 v236,v248,v249
v_cvt_pk_bf16_f32 v237,v250,v251
v_cvt_pk_bf16_f32 v238,v252,v253
v_cvt_pk_bf16_f32 v239,v254,v255
v_exp_f32 v152,v152
v_exp_f32 v153,v153
v_exp_f32 v154,v154
v_exp_f32 v155,v155
v_exp_f32 v156,v156
v_exp_f32 v157,v157
buffer_load_dwordx4 v5,s[24:27],s47 offen offset:1024 nt lds
v_exp_f32 v158,v158
v_exp_f32 v159,v159
v_mfma_f32_16x16x32_bf16 a[96:99],a[192:195],v[232:235],a[96:99]
ds_read_b64_tr_b16 a[192:193],v2
v_mfma_f32_16x16x32_bf16 a[96:99],a[196:199],v[236:239],a[96:99]
ds_read_b64_tr_b16 a[194:195],v2 offset:4096
v_mfma_f32_16x16x32_bf16 a[100:103],a[204:207],v[236:239],a[100:103]
v_mfma_f32_16x16x32_bf16 a[100:103],a[200:203],v[232:235],a[100:103]
ds_read_b64_tr_b16 a[196:197],v2 offset:8192
v_mfma_f32_16x16x32_bf16 a[104:107],a[208:211],v[232:235],a[104:107]
v_mfma_f32_16x16x32_bf16 a[104:107],a[212:215],v[236:239],a[104:107]
v_mfma_f32_16x16x32_bf16 a[108:111],a[220:223],v[236:239],a[108:111]
buffer_load_dwordx4 v4,s[24:27],s47 offen offset:2048 nt lds
v_mfma_f32_16x16x32_bf16 a[108:111],a[216:219],v[232:235],a[108:111]
ds_read_b64_tr_b16 a[198:199],v2 offset:12288
v_mfma_f32_16x16x32_bf16 a[112:115],a[224:227],v[232:235],a[112:115]
v_mfma_f32_16x16x32_bf16 a[112:115],a[228:231],v[236:239],a[112:115]
v_mfma_f32_16x16x32_bf16 a[116:119],a[236:239],v[236:239],a[116:119]
ds_read_b64_tr_b16 a[204:205],v3 offset:8192
v_cvt_pk_bf16_f32 v136,v136,v137
v_mfma_f32_16x16x32_bf16 a[116:119],a[232:235],v[232:235],a[116:119]
ds_read_b64_tr_b16 a[206:207],v3 offset:12288
v_mfma_f32_16x16x32_bf16 a[120:123],a[240:243],v[232:235],a[120:123]
ds_read_b64_tr_b16 a[200:201],v3
v_mfma_f32_16x16x32_bf16 a[120:123],a[244:247],v[236:239],a[120:123]
ds_read_b64_tr_b16 a[202:203],v3 offset:4096
v_cvt_pk_bf16_f32 v137,v138,v139
v_mfma_f32_16x16x32_bf16 a[124:127],a[252:255],v[236:239],a[124:127]
ds_read_b64_tr_b16 a[208:209],v2 offset:256
v_mfma_f32_16x16x32_bf16 a[124:127],a[248:251],v[232:235],a[124:127]
ds_read_b64_tr_b16 a[210:211],v2 offset:4352
v_smfmac_f32_16x16x64_bf16 v[60:63],v[28:31],v[232:239],v27
ds_read_b64_tr_b16 a[212:213],v2 offset:8448
v_mfma_f32_16x16x32_bf16 v[200:203],a[128:131],v[96:99],v[40:43]
ds_read_b64_tr_b16 a[214:215],v2 offset:12544
v_cvt_pk_bf16_f32 v138,v140,v141
v_mfma_f32_16x16x32_bf16 v[200:203],a[132:135],v[100:103],v[200:203]
ds_read_b64_tr_b16 a[220:221],v3 offset:8448
v_mfma_f32_16x16x32_bf16 v[200:203],a[136:139],v[104:107],v[200:203]
ds_read_b64_tr_b16 a[222:223],v3 offset:12544
v_cvt_pk_bf16_f32 v139,v142,v143
v_mfma_f32_16x16x32_bf16 v[200:203],a[140:143],v[108:111],v[200:203]
buffer_load_dwordx4 v5,s[24:27],s47 offen offset:3072 nt lds
v_mfma_f32_16x16x32_bf16 v[204:207],a[156:159],v[108:111],v[40:43]
s_or_b32 s47,s47,0x18000
s_addk_i32 s47,4096
ds_read_b64_tr_b16 a[216:217],v3 offset:256
v_mfma_f32_16x16x32_bf16 v[204:207],a[152:155],v[104:107],v[204:207]
s_and_b32 s47,s47,0x27000
ds_read_b64_tr_b16 a[218:219],v3 offset:4352
v_cvt_pk_bf16_f32 v140,v152,v153
v_mfma_f32_16x16x32_bf16 v[204:207],a[148:151],v[100:103],v[204:207]
s_or_b32 s47,s47,s46
ds_read_b64_tr_b16 a[224:225],v2 offset:512
v_mfma_f32_16x16x32_bf16 v[204:207],a[144:147],v[96:99],v[204:207]
ds_read_b64_tr_b16 a[226:227],v2 offset:4608
v_mfma_f32_16x16x32_bf16 v[216:219],a[160:163],v[96:99],v[40:43]
ds_read_b64_tr_b16 a[228:229],v2 offset:8704
v_cvt_pk_bf16_f32 v141,v154,v155
v_mfma_f32_16x16x32_bf16 v[216:219],a[164:167],v[100:103],v[216:219]
ds_read_b64_tr_b16 a[230:231],v2 offset:12800
v_mfma_f32_16x16x32_bf16 v[216:219],a[168:171],v[104:107],v[216:219]
ds_read_b64_tr_b16 a[236:237],v3 offset:8704
v_mfma_f32_16x16x32_bf16 v[216:219],a[172:175],v[108:111],v[216:219]
ds_read_b64_tr_b16 a[238:239],v3 offset:12800
v_cvt_pk_bf16_f32 v142,v156,v157
v_mfma_f32_16x16x32_bf16 v[220:223],a[188:191],v[108:111],v[40:43]
ds_read_b64_tr_b16 a[232:233],v3 offset:512
v_mfma_f32_16x16x32_bf16 v[220:223],a[184:187],v[104:107],v[220:223]
ds_read_b64_tr_b16 a[234:235],v3 offset:4608
v_mfma_f32_16x16x32_bf16 v[220:223],a[180:183],v[100:103],v[220:223]
ds_read_b64_tr_b16 a[240:241],v2 offset:768
v_cvt_pk_bf16_f32 v143,v158,v159
v_mfma_f32_16x16x32_bf16 v[220:223],a[176:179],v[96:99],v[220:223]
s_waitcnt lgkmcnt(7)
buffer_load_dword v26,v24,s[88:91],s93 offen
v_mfma_f32_16x16x32_bf16 a[0:3],a[192:195],v[136:139],a[0:3]
ds_read_b64_tr_b16 a[242:243],v2 offset:4864
v_mfma_f32_16x16x32_bf16 a[0:3],a[196:199],v[140:143],a[0:3]
ds_read_b64_tr_b16 a[244:245],v2 offset:8960
v_mfma_f32_16x16x32_bf16 a[4:7],a[204:207],v[140:143],a[4:7]
ds_read_b64_tr_b16 a[246:247],v2 offset:13056
v_mfma_f32_16x16x32_bf16 a[4:7],a[200:203],v[136:139],a[4:7]
ds_read_b64_tr_b16 a[252:253],v3 offset:8960
v_mfma_f32_16x16x32_bf16 a[8:11],a[208:211],v[136:139],a[8:11]
ds_read_b64_tr_b16 a[254:255],v3 offset:13056
v_mfma_f32_16x16x32_bf16 a[8:11],a[212:215],v[140:143],a[8:11]
ds_read_b64_tr_b16 a[248:249],v3 offset:768
v_mfma_f32_16x16x32_bf16 a[12:15],a[220:223],v[140:143],a[12:15]
ds_read_b64_tr_b16 a[250:251],v3 offset:4864
v_mfma_f32_16x16x32_bf16 a[12:15],a[216:219],v[136:139],a[12:15]
s_bitcmp1_b32 s45,0
s_cbranch_scc0 L_t31v10_%=
s_mov_b32 m0,s80
s_addk_i32 s80,4096
buffer_load_dwordx4 v25,s[20:23],s83 offen nt lds
s_addk_i32 s83,1024
L_t31v10_%=:
v_mfma_f32_16x16x32_bf16 a[16:19],a[224:227],v[136:139],a[16:19]
s_waitcnt lgkmcnt(6)
v_mfma_f32_16x16x32_bf16 a[16:19],a[228:231],v[140:143],a[16:19]
v_mfma_f32_16x16x32_bf16 a[20:23],a[236:239],v[140:143],a[20:23]
v_mfma_f32_16x16x32_bf16 a[20:23],a[232:235],v[136:139],a[20:23]
v_mfma_f32_16x16x32_bf16 a[24:27],a[240:243],v[136:139],a[24:27]
s_waitcnt lgkmcnt(2)
v_mfma_f32_16x16x32_bf16 a[24:27],a[244:247],v[140:143],a[24:27]
v_mfma_f32_16x16x32_bf16 a[28:31],a[252:255],v[140:143],a[28:31]
s_waitcnt lgkmcnt(0)
v_mfma_f32_16x16x32_bf16 a[28:31],a[248:251],v[136:139],a[28:31]
s_cmp_lg_u32 s53,0
v_smfmac_f32_16x16x64_bf16 v[48:51],v[28:31],v[136:143],v27
s_cbranch_scc1 L_att0v1_%=
s_waitcnt lgkmcnt(0)
s_nop 7
s_branch L_att0x_%=
.p2align 5
L_att0v2_%=:
v_mfma_f32_16x16x32_bf16 v[232:235],a[128:131],v[112:115],v[44:47]
s_waitcnt vmcnt(8) lgkmcnt(0)
v_mfma_f32_16x16x32_bf16 v[232:235],a[132:135],v[116:119],v[232:235]
s_barrier
s_add_u32 m0,s49,s51
ds_read_b128 a[128:131],v1
v_mfma_f32_16x16x32_bf16 v[232:235],a[136:139],v[120:123],v[232:235]
s_mov_b32 s90,L_endseg_%=-L_pc_%=+128
v_mfma_f32_16x16x32_bf16 v[232:235],a[140:143],v[124:127],v[232:235]
s_lshr_b32 s45,s45,1
ds_read_b128 a[132:135],v1 offset:256
v_mfma_f32_16x16x32_bf16 v[236:239],a[156:159],v[124:127],v[44:47]
buffer_load_dwordx4 v4,s[20:23],s47 offen nt lds
v_mfma_f32_16x16x32_bf16 v[236:239],a[152:155],v[120:123],v[236:239]
ds_read_b128 a[136:139],v1 offset:512
v_add_u32 v2,s48,v2
v_mfma_f32_16x16x32_bf16 v[236:239],a[148:151],v[116:119],v[236:239]
v_mfma_f32_16x16x32_bf16 v[236:239],a[144:147],v[112:115],v[236:239]
s_add_u32 s62,s50,32768
ds_read_b128 a[140:143],v1 offset:768
v_add_u32 v3,s48,v3
v_mfma_f32_16x16x32_bf16 v[248:251],a[160:163],v[112:115],v[44:47]
s_and_b32 s62,s62,131071
v_mfma_f32_16x16x32_bf16 v[248:251],a[164:167],v[116:119],v[248:251]
s_addk_i32 s54,16384
ds_read_b128 a[156:159],v1 offset:4864
v_mfma_f32_16x16x32_bf16 v[248:251],a[168:171],v[120:123],v[248:251]
s_sub_u32 s48,s62,s50
v_mfma_f32_16x16x32_bf16 v[248:251],a[172:175],v[124:127],v[248:251]
s_sub_u32 s53,s53,1
v_mfma_f32_16x16x32_bf16 v[252:255],a[188:191],v[124:127],v[44:47]
)A"
R"A(s_and_b32 s46,s54,0xfffc0000
ds_read_b128 a[152:155],v1 offset:4608
v_mfma_f32_16x16x32_bf16 v[252:255],a[184:187],v[120:123],v[252:255]
s_mov_b32 s50,s62
v_mfma_f32_16x16x32_bf16 v[252:255],a[180:183],v[116:119],v[252:255]
s_xor_b32 s49,s50,65536
v_mfma_f32_16x16x32_bf16 v[252:255],a[176:179],v[112:115],v[252:255]
ds_read_b128 a[148:151],v1 offset:4352
v_mfma_f32_16x16x32_bf16 a[32:35],a[192:195],v[168:171],a[32:35]
v_mfma_f32_16x16x32_bf16 a[32:35],a[196:199],v[172:175],a[32:35]
ds_read_b128 a[144:147],v1 offset:4096
v_mfma_f32_16x16x32_bf16 a[36:39],a[204:207],v[172:175],a[36:39]
buffer_load_dwordx4 v4,s[20:23],s47 offen offset:1024 nt lds
v_mfma_f32_16x16x32_bf16 a[36:39],a[200:203],v[168:171],a[36:39]
ds_read_b128 a[160:163],v1 offset:8192
v_mfma_f32_16x16x32_bf16 a[40:43],a[208:211],v[168:171],a[40:43]
v_mfma_f32_16x16x32_bf16 a[40:43],a[212:215],v[172:175],a[40:43]
ds_read_b128 a[164:167],v1 offset:8448
v_mfma_f32_16x16x32_bf16 a[44:47],a[220:223],v[172:175],a[44:47]
v_mfma_f32_16x16x32_bf16 a[44:47],a[216:219],v[168:171],a[44:47]
ds_read_b128 a[168:171],v1 offset:8704
v_mfma_f32_16x16x32_bf16 a[48:51],a[224:227],v[168:171],a[48:51]
v_mfma_f32_16x16x32_bf16 a[48:51],a[228:231],v[172:175],a[48:51]
ds_read_b128 a[172:175],v1 offset:8960
v_mfma_f32_16x16x32_bf16 a[52:55],a[236:239],v[172:175],a[52:55]
v_mfma_f32_16x16x32_bf16 a[52:55],a[232:235],v[168:171],a[52:55]
ds_read_b128 a[188:191],v1 offset:13056
v_mfma_f32_16x16x32_bf16 a[56:59],a[240:243],v[168:171],a[56:59]
v_mfma_f32_16x16x32_bf16 a[56:59],a[244:247],v[172:175],a[56:59]
ds_read_b128 a[184:187],v1 offset:12800
v_mfma_f32_16x16x32_bf16 a[60:63],a[252:255],v[172:175],a[60:63]
v_mfma_f32_16x16x32_bf16 a[60:63],a[248:251],v[168:171],a[60:63]
ds_read_b128 a[180:183],v1 offset:12544
v_smfmac_f32_16x16x64_bf16 v[52:55],v[28:31],v[168:175],v27
s_waitcnt lgkmcnt(4)
v_mfma_f32_16x16x32_bf16 v[136:139],a[128:131],v[64:67],v[32:35]
buffer_load_dwordx4 v5,s[20:23],s47 offen offset:2048 nt lds
v_mfma_f32_16x16x32_bf16 v[136:139],a[132:135],v[68:71],v[136:139]
ds_read_b128 a[176:179],v1 offset:12288
v_add_u32 v1,s48,v1
v_mfma_f32_16x16x32_bf16 v[136:139],a[136:139],v[72:75],v[136:139]
v_mfma_f32_16x16x32_bf16 v[136:139],a[140:143],v[76:79],v[136:139]
v_mfma_f32_16x16x32_bf16 v[140:143],a[156:159],v[76:79],v[32:35]
v_exp_f32 v232,v232
v_mfma_f32_16x16x32_bf16 v[140:143],a[152:155],v[72:75],v[140:143]
v_exp_f32 v233,v233
v_mfma_f32_16x16x32_bf16 v[140:143],a[148:151],v[68:71],v[140:143]
v_exp_f32 v234,v234
v_mfma_f32_16x16x32_bf16 v[140:143],a[144:147],v[64:67],v[140:143]
v_exp_f32 v235,v235
v_mfma_f32_16x16x32_bf16 v[152:155],a[160:163],v[64:67],v[32:35]
v_exp_f32 v236,v236
v_mfma_f32_16x16x32_bf16 v[152:155],a[164:167],v[68:71],v[152:155]
v_exp_f32 v237,v237
v_mfma_f32_16x16x32_bf16 v[152:155],a[168:171],v[72:75],v[152:155]
s_waitcnt lgkmcnt(0)
v_exp_f32 v238,v238
v_mfma_f32_16x16x32_bf16 v[152:155],a[172:175],v[76:79],v[152:155]
v_exp_f32 v239,v239
v_mfma_f32_16x16x32_bf16 v[156:159],a[188:191],v[76:79],v[32:35]
v_exp_f32 v248,v248
v_mfma_f32_16x16x32_bf16 v[156:159],a[184:187],v[72:75],v[156:159]
buffer_load_dwordx4 v5,s[20:23],s47 offen offset:3072 nt lds
v_mfma_f32_16x16x32_bf16 v[156:159],a[180:183],v[68:71],v[156:159]
v_exp_f32 v249,v249
v_mfma_f32_16x16x32_bf16 v[156:159],a[176:179],v[64:67],v[156:159]
v_exp_f32 v250,v250
v_exp_f32 v251,v251
v_exp_f32 v252,v252
v_exp_f32 v253,v253
v_exp_f32 v254,v254
v_exp_f32 v255,v255
v_exp_f32 v136,v136
v_exp_f32 v137,v137
v_exp_f32 v138,v138
s_addk_i32 m0,16384
v_exp_f32 v139,v139
v_exp_f32 v140,v140
v_exp_f32 v141,v141
buffer_load_dwordx4 v4,s[24:27],s47 offen nt lds
v_exp_f32 v142,v142
v_exp_f32 v143,v143
v_cvt_pk_bf16_f32 v232,v232,v233
v_cvt_pk_bf16_f32 v233,v234,v235
v_cvt_pk_bf16_f32 v234,v236,v237
v_cvt_pk_bf16_f32 v235,v238,v239
v_cvt_pk_bf16_f32 v236,v248,v249
v_mfma_f32_16x16x32_bf16 v[168:171],a[128:131],v[80:83],v[36:39]
v_cvt_pk_bf16_f32 v237,v250,v251
v_cvt_pk_bf16_f32 v238,v252,v253
v_mfma_f32_16x16x32_bf16 v[168:171],a[132:135],v[84:87],v[168:171]
v_cvt_pk_bf16_f32 v239,v254,v255
v_mfma_f32_16x16x32_bf16 v[168:171],a[136:139],v[88:91],v[168:171]
v_exp_f32 v152,v152
v_mfma_f32_16x16x32_bf16 v[168:171],a[140:143],v[92:95],v[168:171]
v_exp_f32 v153,v153
v_mfma_f32_16x16x32_bf16 v[172:175],a[156:159],v[92:95],v[36:39]
v_exp_f32 v154,v154
v_mfma_f32_16x16x32_bf16 v[172:175],a[152:155],v[88:91],v[172:175]
v_exp_f32 v155,v155
v_mfma_f32_16x16x32_bf16 v[172:175],a[148:151],v[84:87],v[172:175]
v_exp_f32 v156,v156
v_mfma_f32_16x16x32_bf16 v[172:175],a[144:147],v[80:83],v[172:175]
v_exp_f32 v157,v157
v_mfma_f32_16x16x32_bf16 v[184:187],a[160:163],v[80:83],v[36:39]
buffer_load_dwordx4 v5,s[24:27],s47 offen offset:1024 nt lds
v_mfma_f32_16x16x32_bf16 v[184:187],a[164:167],v[84:87],v[184:187]
v_exp_f32 v158,v158
v_mfma_f32_16x16x32_bf16 v[184:187],a[168:171],v[88:91],v[184:187]
v_exp_f32 v159,v159
v_mfma_f32_16x16x32_bf16 v[184:187],a[172:175],v[92:95],v[184:187]
v_exp_f32 v168,v168
v_mfma_f32_16x16x32_bf16 v[188:191],a[188:191],v[92:95],v[36:39]
v_exp_f32 v169,v169
v_mfma_f32_16x16x32_bf16 v[188:191],a[184:187],v[88:91],v[188:191]
v_exp_f32 v170,v170
v_mfma_f32_16x16x32_bf16 v[188:191],a[180:183],v[84:87],v[188:191]
v_exp_f32 v171,v171
v_mfma_f32_16x16x32_bf16 v[188:191],a[176:179],v[80:83],v[188:191]
v_exp_f32 v172,v172
v_mfma_f32_16x16x32_bf16 a[96:99],a[192:195],v[232:235],a[96:99]
ds_read_b64_tr_b16 a[192:193],v2
v_cvt_pk_bf16_f32 v168,v168,v169
v_mfma_f32_16x16x32_bf16 a[96:99],a[196:199],v[236:239],a[96:99]
ds_read_b64_tr_b16 a[194:195],v2 offset:4096
v_cvt_pk_bf16_f32 v169,v170,v171
v_mfma_f32_16x16x32_bf16 a[100:103],a[204:207],v[236:239],a[100:103]
v_exp_f32 v173,v173
v_mfma_f32_16x16x32_bf16 a[100:103],a[200:203],v[232:235],a[100:103]
ds_read_b64_tr_b16 a[196:197],v2 offset:8192
v_cvt_pk_bf16_f32 v170,v172,v173
v_mfma_f32_16x16x32_bf16 a[104:107],a[208:211],v[232:235],a[104:107]
v_exp_f32 v174,v174
v_mfma_f32_16x16x32_bf16 a[104:107],a[212:215],v[236:239],a[104:107]
v_exp_f32 v175,v175
v_mfma_f32_16x16x32_bf16 a[108:111],a[220:223],v[236:239],a[108:111]
buffer_load_dwordx4 v4,s[24:27],s47 offen offset:2048 nt lds
v_mfma_f32_16x16x32_bf16 a[108:111],a[216:219],v[232:235],a[108:111]
ds_read_b64_tr_b16 a[198:199],v2 offset:12288
v_cvt_pk_bf16_f32 v171,v174,v175
v_mfma_f32_16x16x32_bf16 a[112:115],a[224:227],v[232:235],a[112:115]
v_exp_f32 v184,v184
v_mfma_f32_16x16x32_bf16 a[112:115],a[228:231],v[236:239],a[112:115]
v_exp_f32 v185,v185
v_mfma_f32_16x16x32_bf16 a[116:119],a[236:239],v[236:239],a[116:119]
ds_read_b64_tr_b16 a[204:205],v3 offset:8192
v_cvt_pk_bf16_f32 v136,v136,v137
v_mfma_f32_16x16x32_bf16 a[116:119],a[232:235],v[232:235],a[116:119]
ds_read_b64_tr_b16 a[206:207],v3 offset:12288
v_cvt_pk_bf16_f32 v172,v184,v185
v_mfma_f32_16x16x32_bf16 a[120:123],a[240:243],v[232:235],a[120:123]
ds_read_b64_tr_b16 a[200:201],v3
v_mfma_f32_16x16x32_bf16 a[120:123],a[244:247],v[236:239],a[120:123]
ds_read_b64_tr_b16 a[202:203],v3 offset:4096
v_cvt_pk_bf16_f32 v137,v138,v139
v_mfma_f32_16x16x32_bf16 a[124:127],a[252:255],v[236:239],a[124:127]
ds_read_b64_tr_b16 a[208:209],v2 offset:256
v_mfma_f32_16x16x32_bf16 a[124:127],a[248:251],v[232:235],a[124:127]
ds_read_b64_tr_b16 a[210:211],v2 offset:4352
v_smfmac_f32_16x16x64_bf16 v[60:63],v[28:31],v[232:239],v27
ds_read_b64_tr_b16 a[212:213],v2 offset:8448
ds_read_b64_tr_b16 a[214:215],v2 offset:12544
v_cvt_pk_bf16_f32 v138,v140,v141
ds_read_b64_tr_b16 a[220:221],v3 offset:8448
ds_read_b64_tr_b16 a[222:223],v3 offset:12544
v_cvt_pk_bf16_f32 v139,v142,v143
buffer_load_dwordx4 v5,s[24:27],s47 offen offset:3072 nt lds
s_or_b32 s47,s47,0x18000
s_addk_i32 s47,4096
ds_read_b64_tr_b16 a[216:217],v3 offset:256
s_and_b32 s47,s47,0x27000
ds_read_b64_tr_b16 a[218:219],v3 offset:4352
v_cvt_pk_bf16_f32 v140,v152,v153
s_or_b32 s47,s47,s46
ds_read_b64_tr_b16 a[224:225],v2 offset:512
ds_read_b64_tr_b16 a[226:227],v2 offset:4608
ds_read_b64_tr_b16 a[228:229],v2 offset:8704
v_cvt_pk_bf16_f32 v141,v154,v155
ds_read_b64_tr_b16 a[230:231],v2 offset:12800
ds_read_b64_tr_b16 a[236:237],v3 offset:8704
ds_read_b64_tr_b16 a[238:239],v3 offset:12800
v_cvt_pk_bf16_f32 v142,v156,v157
ds_read_b64_tr_b16 a[232:233],v3 offset:512
ds_read_b64_tr_b16 a[234:235],v3 offset:4608
ds_read_b64_tr_b16 a[240:241],v2 offset:768
v_cvt_pk_bf16_f32 v143,v158,v159
buffer_load_dword v26,v24,s[88:91],s93 offen
s_waitcnt lgkmcnt(15)
v_mfma_f32_16x16x32_bf16 a[0:3],a[192:195],v[136:139],a[0:3]
ds_read_b64_tr_b16 a[242:243],v2 offset:4864
v_mfma_f32_16x16x32_bf16 a[0:3],a[196:199],v[140:143],a[0:3]
ds_read_b64_tr_b16 a[244:245],v2 offset:8960
v_mfma_f32_16x16x32_bf16 a[4:7],a[204:207],v[140:143],a[4:7]
ds_read_b64_tr_b16 a[246:247],v2 offset:13056
v_mfma_f32_16x16x32_bf16 a[4:7],a[200:203],v[136:139],a[4:7]
ds_read_b64_tr_b16 a[252:253],v3 offset:8960
v_mfma_f32_16x16x32_bf16 a[8:11],a[208:211],v[136:139],a[8:11]
s_waitcnt lgkmcnt(15)
ds_read_b64_tr_b16 a[254:255],v3 offset:13056
v_mfma_f32_16x16x32_bf16 a[8:11],a[212:215],v[140:143],a[8:11]
ds_read_b64_tr_b16 a[248:249],v3 offset:768
v_mfma_f32_16x16x32_bf16 a[12:15],a[220:223],v[140:143],a[12:15]
s_waitcnt lgkmcnt(15)
ds_read_b64_tr_b16 a[250:251],v3 offset:4864
v_mfma_f32_16x16x32_bf16 a[12:15],a[216:219],v[136:139],a[12:15]
s_bitcmp1_b32 s45,0
s_cbranch_scc0 L_t31v20_%=
s_mov_b32 m0,s80
s_addk_i32 s80,4096
buffer_load_dwordx4 v25,s[20:23],s83 offen nt lds
s_addk_i32 s83,1024
L_t31v20_%=:
s_waitcnt lgkmcnt(7)
v_mfma_f32_16x16x32_bf16 a[16:19],a[224:227],v[136:139],a[16:19]
v_exp_f32 v186,v186
v_mfma_f32_16x16x32_bf16 a[16:19],a[228:231],v[140:143],a[16:19]
v_exp_f32 v187,v187
v_mfma_f32_16x16x32_bf16 a[20:23],a[236:239],v[140:143],a[20:23]
v_exp_f32 v188,v188
v_mfma_f32_16x16x32_bf16 a[20:23],a[232:235],v[136:139],a[20:23]
s_waitcnt lgkmcnt(3)
v_exp_f32 v189,v189
v_mfma_f32_16x16x32_bf16 a[24:27],a[240:243],v[136:139],a[24:27]
v_exp_f32 v190,v190
v_mfma_f32_16x16x32_bf16 a[24:27],a[244:247],v[140:143],a[24:27]
s_waitcnt lgkmcnt(1)
v_cvt_pk_bf16_f32 v173,v186,v187
v_mfma_f32_16x16x32_bf16 a[28:31],a[252:255],v[140:143],a[28:31]
s_waitcnt lgkmcnt(0)
v_exp_f32 v191,v191
v_mfma_f32_16x16x32_bf16 a[28:31],a[248:251],v[136:139],a[28:31]
v_cvt_pk_bf16_f32 v174,v188,v189
v_cvt_pk_bf16_f32 v175,v190,v191
s_cmp_lg_u32 s53,0
v_smfmac_f32_16x16x64_bf16 v[48:51],v[28:31],v[136:143],v27
s_cbranch_scc1 L_att0v2_%=
s_waitcnt lgkmcnt(0)
s_nop 7
s_branch L_att0x_%=
.p2align 5
L_att0_%=:
v_mfma_f32_16x16x32_bf16 v[232:235],a[128:131],v[112:115],v[44:47]
s_waitcnt vmcnt(8) lgkmcnt(0)
v_exp_f32 v200,v200
v_mfma_f32_16x16x32_bf16 v[232:235],a[132:135],v[116:119],v[232:235]
s_barrier
s_add_u32 m0,s49,s51
ds_read_b128 a[128:131],v1
v_mfma_f32_16x16x32_bf16 v[232:235],a[136:139],v[120:123],v[232:235]
s_mov_b32 s90,L_endseg_%=-L_pc_%=+128
v_exp_f32 v201,v201
v_mfma_f32_16x16x32_bf16 v[232:235],a[140:143],v[124:127],v[232:235]
s_lshr_b32 s45,s45,1
ds_read_b128 a[132:135],v1 offset:256
v_cvt_pk_bf16_f32 v200,v200,v201
v_mfma_f32_16x16x32_bf16 v[236:239],a[156:159],v[124:127],v[44:47]
s_sub_u32 s94,s94,3
buffer_load_dwordx4 v4,s[20:23],s47 offen nt lds
v_mfma_f32_16x16x32_bf16 v[236:239],a[152:155],v[120:123],v[236:239]
s_lshr_b32 s60,s94,2
ds_read_b128 a[136:139],v1 offset:512
v_add_u32 v2,s48,v2
v_mfma_f32_16x16x32_bf16 v[236:239],a[148:151],v[116:119],v[236:239]
s_max_i32 s60,s60,124
v_exp_f32 v202,v202
v_mfma_f32_16x16x32_bf16 v[236:239],a[144:147],v[112:115],v[236:239]
s_add_u32 s62,s50,32768
ds_read_b128 a[140:143],v1 offset:768
v_add_u32 v3,s48,v3
v_mfma_f32_16x16x32_bf16 v[248:251],a[160:163],v[112:115],v[44:47]
s_and_b32 s62,s62,131071
v_exp_f32 v203,v203
v_mfma_f32_16x16x32_bf16 v[248:251],a[164:167],v[116:119],v[248:251]
s_addk_i32 s54,16384
ds_read_b128 a[156:159],v1 offset:4864
v_cvt_pk_bf16_f32 v201,v202,v203
v_mfma_f32_16x16x32_bf16 v[248:251],a[168:171],v[120:123],v[248:251]
s_sub_u32 s48,s62,s50
v_exp_f32 v204,v204
v_mfma_f32_16x16x32_bf16 v[248:251],a[172:175],v[124:127],v[248:251]
s_sub_u32 s53,s53,1
v_exp_f32 v205,v205
v_mfma_f32_16x16x32_bf16 v[252:255],a[188:191],v[124:127],v[44:47]
)A"
R"A(s_and_b32 s46,s54,0xfffc0000
ds_read_b128 a[152:155],v1 offset:4608
v_cvt_pk_bf16_f32 v202,v204,v205
v_mfma_f32_16x16x32_bf16 v[252:255],a[184:187],v[120:123],v[252:255]
s_mov_b32 s50,s62
v_exp_f32 v206,v206
v_mfma_f32_16x16x32_bf16 v[252:255],a[180:183],v[116:119],v[252:255]
s_xor_b32 s49,s50,65536
v_exp_f32 v207,v207
v_mfma_f32_16x16x32_bf16 v[252:255],a[176:179],v[112:115],v[252:255]
ds_read_b128 a[148:151],v1 offset:4352
v_cvt_pk_bf16_f32 v203,v206,v207
v_mfma_f32_16x16x32_bf16 a[32:35],a[192:195],v[168:171],a[32:35]
v_exp_f32 v216,v216
v_mfma_f32_16x16x32_bf16 a[32:35],a[196:199],v[172:175],a[32:35]
ds_read_b128 a[144:147],v1 offset:4096
v_mfma_f32_16x16x32_bf16 a[36:39],a[204:207],v[172:175],a[36:39]
buffer_load_dwordx4 v4,s[20:23],s47 offen offset:1024 nt lds
v_mfma_f32_16x16x32_bf16 a[36:39],a[200:203],v[168:171],a[36:39]
ds_read_b128 a[160:163],v1 offset:8192
v_mfma_f32_16x16x32_bf16 a[40:43],a[208:211],v[168:171],a[40:43]
v_exp_f32 v217,v217
v_mfma_f32_16x16x32_bf16 a[40:43],a[212:215],v[172:175],a[40:43]
ds_read_b128 a[164:167],v1 offset:8448
v_cvt_pk_bf16_f32 v204,v216,v217
v_mfma_f32_16x16x32_bf16 a[44:47],a[220:223],v[172:175],a[44:47]
v_exp_f32 v218,v218
v_mfma_f32_16x16x32_bf16 a[44:47],a[216:219],v[168:171],a[44:47]
ds_read_b128 a[168:171],v1 offset:8704
v_mfma_f32_16x16x32_bf16 a[48:51],a[224:227],v[168:171],a[48:51]
v_exp_f32 v219,v219
v_mfma_f32_16x16x32_bf16 a[48:51],a[228:231],v[172:175],a[48:51]
ds_read_b128 a[172:175],v1 offset:8960
v_cvt_pk_bf16_f32 v205,v218,v219
v_mfma_f32_16x16x32_bf16 a[52:55],a[236:239],v[172:175],a[52:55]
v_exp_f32 v220,v220
v_mfma_f32_16x16x32_bf16 a[52:55],a[232:235],v[168:171],a[52:55]
ds_read_b128 a[188:191],v1 offset:13056
v_mfma_f32_16x16x32_bf16 a[56:59],a[240:243],v[168:171],a[56:59]
v_exp_f32 v221,v221
v_mfma_f32_16x16x32_bf16 a[56:59],a[244:247],v[172:175],a[56:59]
ds_read_b128 a[184:187],v1 offset:12800
v_cvt_pk_bf16_f32 v206,v220,v221
v_mfma_f32_16x16x32_bf16 a[60:63],a[252:255],v[172:175],a[60:63]
v_exp_f32 v222,v222
v_mfma_f32_16x16x32_bf16 a[60:63],a[248:251],v[168:171],a[60:63]
ds_read_b128 a[180:183],v1 offset:12544
v_smfmac_f32_16x16x64_bf16 v[52:55],v[28:31],v[168:175],v27
s_waitcnt lgkmcnt(4)
v_mfma_f32_16x16x32_bf16 v[136:139],a[128:131],v[64:67],v[32:35]
buffer_load_dwordx4 v5,s[20:23],s47 offen offset:2048 nt lds
v_mfma_f32_16x16x32_bf16 v[136:139],a[132:135],v[68:71],v[136:139]
ds_read_b128 a[176:179],v1 offset:12288
v_add_u32 v1,s48,v1
v_mfma_f32_16x16x32_bf16 v[136:139],a[136:139],v[72:75],v[136:139]
v_exp_f32 v223,v223
v_mfma_f32_16x16x32_bf16 v[136:139],a[140:143],v[76:79],v[136:139]
v_cvt_pk_bf16_f32 v207,v222,v223
v_mfma_f32_16x16x32_bf16 v[140:143],a[156:159],v[76:79],v[32:35]
v_exp_f32 v232,v232
v_mfma_f32_16x16x32_bf16 v[140:143],a[152:155],v[72:75],v[140:143]
v_exp_f32 v233,v233
v_mfma_f32_16x16x32_bf16 v[140:143],a[148:151],v[68:71],v[140:143]
v_exp_f32 v234,v234
v_mfma_f32_16x16x32_bf16 v[140:143],a[144:147],v[64:67],v[140:143]
v_exp_f32 v235,v235
v_mfma_f32_16x16x32_bf16 v[152:155],a[160:163],v[64:67],v[32:35]
v_exp_f32 v236,v236
v_mfma_f32_16x16x32_bf16 v[152:155],a[164:167],v[68:71],v[152:155]
v_exp_f32 v237,v237
v_mfma_f32_16x16x32_bf16 v[152:155],a[168:171],v[72:75],v[152:155]
s_waitcnt lgkmcnt(0)
v_exp_f32 v238,v238
v_mfma_f32_16x16x32_bf16 v[152:155],a[172:175],v[76:79],v[152:155]
v_exp_f32 v239,v239
v_mfma_f32_16x16x32_bf16 v[156:159],a[188:191],v[76:79],v[32:35]
v_exp_f32 v248,v248
v_mfma_f32_16x16x32_bf16 v[156:159],a[184:187],v[72:75],v[156:159]
buffer_load_dwordx4 v5,s[20:23],s47 offen offset:3072 nt lds
v_mfma_f32_16x16x32_bf16 v[156:159],a[180:183],v[68:71],v[156:159]
v_exp_f32 v249,v249
v_mfma_f32_16x16x32_bf16 v[156:159],a[176:179],v[64:67],v[156:159]
v_exp_f32 v250,v250
v_mfma_f32_16x16x32_bf16 a[64:67],a[192:195],v[200:203],a[64:67]
v_exp_f32 v251,v251
v_mfma_f32_16x16x32_bf16 a[64:67],a[196:199],v[204:207],a[64:67]
v_exp_f32 v252,v252
v_mfma_f32_16x16x32_bf16 a[68:71],a[204:207],v[204:207],a[68:71]
v_exp_f32 v253,v253
v_mfma_f32_16x16x32_bf16 a[68:71],a[200:203],v[200:203],a[68:71]
v_exp_f32 v254,v254
v_mfma_f32_16x16x32_bf16 a[72:75],a[208:211],v[200:203],a[72:75]
v_exp_f32 v255,v255
v_mfma_f32_16x16x32_bf16 a[72:75],a[212:215],v[204:207],a[72:75]
v_exp_f32 v136,v136
v_mfma_f32_16x16x32_bf16 a[76:79],a[220:223],v[204:207],a[76:79]
v_exp_f32 v137,v137
v_mfma_f32_16x16x32_bf16 a[76:79],a[216:219],v[200:203],a[76:79]
v_exp_f32 v138,v138
v_mfma_f32_16x16x32_bf16 a[80:83],a[224:227],v[200:203],a[80:83]
s_addk_i32 m0,16384
v_exp_f32 v139,v139
v_mfma_f32_16x16x32_bf16 a[80:83],a[228:231],v[204:207],a[80:83]
v_exp_f32 v140,v140
v_mfma_f32_16x16x32_bf16 a[84:87],a[236:239],v[204:207],a[84:87]
v_exp_f32 v141,v141
v_mfma_f32_16x16x32_bf16 a[84:87],a[232:235],v[200:203],a[84:87]
buffer_load_dwordx4 v4,s[24:27],s47 offen nt lds
v_mfma_f32_16x16x32_bf16 a[88:91],a[240:243],v[200:203],a[88:91]
v_exp_f32 v142,v142
v_mfma_f32_16x16x32_bf16 a[88:91],a[244:247],v[204:207],a[88:91]
v_exp_f32 v143,v143
v_mfma_f32_16x16x32_bf16 a[92:95],a[252:255],v[204:207],a[92:95]
v_cvt_pk_bf16_f32 v232,v232,v233
v_cvt_pk_bf16_f32 v233,v234,v235
v_mfma_f32_16x16x32_bf16 a[92:95],a[248:251],v[200:203],a[92:95]
v_cvt_pk_bf16_f32 v234,v236,v237
v_cvt_pk_bf16_f32 v235,v238,v239
v_smfmac_f32_16x16x64_bf16 v[56:59],v[28:31],v[200:207],v27
v_cvt_pk_bf16_f32 v236,v248,v249
v_mfma_f32_16x16x32_bf16 v[168:171],a[128:131],v[80:83],v[36:39]
v_cvt_pk_bf16_f32 v237,v250,v251
v_cvt_pk_bf16_f32 v238,v252,v253
v_mfma_f32_16x16x32_bf16 v[168:171],a[132:135],v[84:87],v[168:171]
v_cvt_pk_bf16_f32 v239,v254,v255
v_mfma_f32_16x16x32_bf16 v[168:171],a[136:139],v[88:91],v[168:171]
v_exp_f32 v152,v152
v_mfma_f32_16x16x32_bf16 v[168:171],a[140:143],v[92:95],v[168:171]
v_exp_f32 v153,v153
v_mfma_f32_16x16x32_bf16 v[172:175],a[156:159],v[92:95],v[36:39]
v_exp_f32 v154,v154
v_mfma_f32_16x16x32_bf16 v[172:175],a[152:155],v[88:91],v[172:175]
v_exp_f32 v155,v155
v_mfma_f32_16x16x32_bf16 v[172:175],a[148:151],v[84:87],v[172:175]
v_exp_f32 v156,v156
v_mfma_f32_16x16x32_bf16 v[172:175],a[144:147],v[80:83],v[172:175]
v_exp_f32 v157,v157
v_mfma_f32_16x16x32_bf16 v[184:187],a[160:163],v[80:83],v[36:39]
buffer_load_dwordx4 v5,s[24:27],s47 offen offset:1024 nt lds
v_mfma_f32_16x16x32_bf16 v[184:187],a[164:167],v[84:87],v[184:187]
v_exp_f32 v158,v158
v_mfma_f32_16x16x32_bf16 v[184:187],a[168:171],v[88:91],v[184:187]
v_exp_f32 v159,v159
v_mfma_f32_16x16x32_bf16 v[184:187],a[172:175],v[92:95],v[184:187]
v_exp_f32 v168,v168
v_mfma_f32_16x16x32_bf16 v[188:191],a[188:191],v[92:95],v[36:39]
v_exp_f32 v169,v169
v_mfma_f32_16x16x32_bf16 v[188:191],a[184:187],v[88:91],v[188:191]
v_exp_f32 v170,v170
v_mfma_f32_16x16x32_bf16 v[188:191],a[180:183],v[84:87],v[188:191]
v_exp_f32 v171,v171
v_mfma_f32_16x16x32_bf16 v[188:191],a[176:179],v[80:83],v[188:191]
v_exp_f32 v172,v172
v_mfma_f32_16x16x32_bf16 a[96:99],a[192:195],v[232:235],a[96:99]
ds_read_b64_tr_b16 a[192:193],v2
v_cvt_pk_bf16_f32 v168,v168,v169
v_mfma_f32_16x16x32_bf16 a[96:99],a[196:199],v[236:239],a[96:99]
ds_read_b64_tr_b16 a[194:195],v2 offset:4096
v_cvt_pk_bf16_f32 v169,v170,v171
v_mfma_f32_16x16x32_bf16 a[100:103],a[204:207],v[236:239],a[100:103]
v_exp_f32 v173,v173
v_mfma_f32_16x16x32_bf16 a[100:103],a[200:203],v[232:235],a[100:103]
ds_read_b64_tr_b16 a[196:197],v2 offset:8192
v_cvt_pk_bf16_f32 v170,v172,v173
v_mfma_f32_16x16x32_bf16 a[104:107],a[208:211],v[232:235],a[104:107]
v_exp_f32 v174,v174
v_mfma_f32_16x16x32_bf16 a[104:107],a[212:215],v[236:239],a[104:107]
v_exp_f32 v175,v175
v_mfma_f32_16x16x32_bf16 a[108:111],a[220:223],v[236:239],a[108:111]
buffer_load_dwordx4 v4,s[24:27],s47 offen offset:2048 nt lds
v_mfma_f32_16x16x32_bf16 a[108:111],a[216:219],v[232:235],a[108:111]
ds_read_b64_tr_b16 a[198:199],v2 offset:12288
v_cvt_pk_bf16_f32 v171,v174,v175
v_mfma_f32_16x16x32_bf16 a[112:115],a[224:227],v[232:235],a[112:115]
v_exp_f32 v184,v184
v_mfma_f32_16x16x32_bf16 a[112:115],a[228:231],v[236:239],a[112:115]
v_exp_f32 v185,v185
v_mfma_f32_16x16x32_bf16 a[116:119],a[236:239],v[236:239],a[116:119]
ds_read_b64_tr_b16 a[204:205],v3 offset:8192
v_cvt_pk_bf16_f32 v136,v136,v137
v_mfma_f32_16x16x32_bf16 a[116:119],a[232:235],v[232:235],a[116:119]
ds_read_b64_tr_b16 a[206:207],v3 offset:12288
v_cvt_pk_bf16_f32 v172,v184,v185
v_mfma_f32_16x16x32_bf16 a[120:123],a[240:243],v[232:235],a[120:123]
ds_read_b64_tr_b16 a[200:201],v3
v_mfma_f32_16x16x32_bf16 a[120:123],a[244:247],v[236:239],a[120:123]
ds_read_b64_tr_b16 a[202:203],v3 offset:4096
v_cvt_pk_bf16_f32 v137,v138,v139
v_mfma_f32_16x16x32_bf16 a[124:127],a[252:255],v[236:239],a[124:127]
ds_read_b64_tr_b16 a[208:209],v2 offset:256
v_mfma_f32_16x16x32_bf16 a[124:127],a[248:251],v[232:235],a[124:127]
ds_read_b64_tr_b16 a[210:211],v2 offset:4352
v_smfmac_f32_16x16x64_bf16 v[60:63],v[28:31],v[232:239],v27
ds_read_b64_tr_b16 a[212:213],v2 offset:8448
v_mfma_f32_16x16x32_bf16 v[200:203],a[128:131],v[96:99],v[40:43]
ds_read_b64_tr_b16 a[214:215],v2 offset:12544
v_cvt_pk_bf16_f32 v138,v140,v141
v_mfma_f32_16x16x32_bf16 v[200:203],a[132:135],v[100:103],v[200:203]
ds_read_b64_tr_b16 a[220:221],v3 offset:8448
v_mfma_f32_16x16x32_bf16 v[200:203],a[136:139],v[104:107],v[200:203]
ds_read_b64_tr_b16 a[222:223],v3 offset:12544
v_cvt_pk_bf16_f32 v139,v142,v143
v_mfma_f32_16x16x32_bf16 v[200:203],a[140:143],v[108:111],v[200:203]
buffer_load_dwordx4 v5,s[24:27],s47 offen offset:3072 nt lds
v_mfma_f32_16x16x32_bf16 v[204:207],a[156:159],v[108:111],v[40:43]
s_or_b32 s47,s47,0x18000
s_addk_i32 s47,4096
ds_read_b64_tr_b16 a[216:217],v3 offset:256
v_mfma_f32_16x16x32_bf16 v[204:207],a[152:155],v[104:107],v[204:207]
s_and_b32 s47,s47,0x27000
ds_read_b64_tr_b16 a[218:219],v3 offset:4352
v_cvt_pk_bf16_f32 v140,v152,v153
v_mfma_f32_16x16x32_bf16 v[204:207],a[148:151],v[100:103],v[204:207]
s_or_b32 s47,s47,s46
ds_read_b64_tr_b16 a[224:225],v2 offset:512
v_mfma_f32_16x16x32_bf16 v[204:207],a[144:147],v[96:99],v[204:207]
ds_read_b64_tr_b16 a[226:227],v2 offset:4608
v_mfma_f32_16x16x32_bf16 v[216:219],a[160:163],v[96:99],v[40:43]
ds_read_b64_tr_b16 a[228:229],v2 offset:8704
v_cvt_pk_bf16_f32 v141,v154,v155
v_mfma_f32_16x16x32_bf16 v[216:219],a[164:167],v[100:103],v[216:219]
ds_read_b64_tr_b16 a[230:231],v2 offset:12800
v_mfma_f32_16x16x32_bf16 v[216:219],a[168:171],v[104:107],v[216:219]
ds_read_b64_tr_b16 a[236:237],v3 offset:8704
v_mfma_f32_16x16x32_bf16 v[216:219],a[172:175],v[108:111],v[216:219]
ds_read_b64_tr_b16 a[238:239],v3 offset:12800
v_cvt_pk_bf16_f32 v142,v156,v157
v_mfma_f32_16x16x32_bf16 v[220:223],a[188:191],v[108:111],v[40:43]
ds_read_b64_tr_b16 a[232:233],v3 offset:512
v_mfma_f32_16x16x32_bf16 v[220:223],a[184:187],v[104:107],v[220:223]
ds_read_b64_tr_b16 a[234:235],v3 offset:4608
v_mfma_f32_16x16x32_bf16 v[220:223],a[180:183],v[100:103],v[220:223]
ds_read_b64_tr_b16 a[240:241],v2 offset:768
v_cvt_pk_bf16_f32 v143,v158,v159
v_mfma_f32_16x16x32_bf16 v[220:223],a[176:179],v[96:99],v[220:223]
s_waitcnt lgkmcnt(7)
buffer_load_dword v26,v24,s[88:91],s93 offen
v_mfma_f32_16x16x32_bf16 a[0:3],a[192:195],v[136:139],a[0:3]
ds_read_b64_tr_b16 a[242:243],v2 offset:4864
v_mfma_f32_16x16x32_bf16 a[0:3],a[196:199],v[140:143],a[0:3]
ds_read_b64_tr_b16 a[244:245],v2 offset:8960
v_mfma_f32_16x16x32_bf16 a[4:7],a[204:207],v[140:143],a[4:7]
ds_read_b64_tr_b16 a[246:247],v2 offset:13056
v_mfma_f32_16x16x32_bf16 a[4:7],a[200:203],v[136:139],a[4:7]
s_memrealtime s[76:77]
ds_read_b64_tr_b16 a[252:253],v3 offset:8960
v_mfma_f32_16x16x32_bf16 a[8:11],a[208:211],v[136:139],a[8:11]
ds_read_b64_tr_b16 a[254:255],v3 offset:13056
v_mfma_f32_16x16x32_bf16 a[8:11],a[212:215],v[140:143],a[8:11]
ds_read_b64_tr_b16 a[248:249],v3 offset:768
v_mfma_f32_16x16x32_bf16 a[12:15],a[220:223],v[140:143],a[12:15]
ds_read_b64_tr_b16 a[250:251],v3 offset:4864
v_mfma_f32_16x16x32_bf16 a[12:15],a[216:219],v[136:139],a[12:15]
s_bitcmp1_b32 s45,0
s_cbranch_scc0 L_t310_%=
s_mov_b32 m0,s80
s_addk_i32 s80,4096
buffer_load_dwordx4 v25,s[20:23],s83 offen nt lds
s_addk_i32 s83,1024
L_t310_%=:
v_mfma_f32_16x16x32_bf16 a[16:19],a[224:227],v[136:139],a[16:19]
)A"
R"A(s_waitcnt lgkmcnt(6)
v_exp_f32 v186,v186
v_mfma_f32_16x16x32_bf16 a[16:19],a[228:231],v[140:143],a[16:19]
v_exp_f32 v187,v187
v_mfma_f32_16x16x32_bf16 a[20:23],a[236:239],v[140:143],a[20:23]
s_waitcnt lgkmcnt(0)
v_exp_f32 v188,v188
v_mfma_f32_16x16x32_bf16 a[20:23],a[232:235],v[136:139],a[20:23]
s_sub_u32 s79,s78,s76
v_exp_f32 v189,v189
v_mfma_f32_16x16x32_bf16 a[24:27],a[240:243],v[136:139],a[24:27]
s_add_u32 s78,s76,s60
v_exp_f32 v190,v190
v_mfma_f32_16x16x32_bf16 a[24:27],a[244:247],v[140:143],a[24:27]
s_cmp_gt_i32 s79,s95
s_cbranch_scc1 L_ps3_%=
L_pr3_%=:
v_cvt_pk_bf16_f32 v173,v186,v187
v_mfma_f32_16x16x32_bf16 a[28:31],a[252:255],v[140:143],a[28:31]
v_exp_f32 v191,v191
v_mfma_f32_16x16x32_bf16 a[28:31],a[248:251],v[136:139],a[28:31]
v_cvt_pk_bf16_f32 v174,v188,v189
v_cvt_pk_bf16_f32 v175,v190,v191
s_cmp_lg_u32 s53,0
v_smfmac_f32_16x16x64_bf16 v[48:51],v[28:31],v[136:143],v27
s_cbranch_scc1 L_att0_%=
L_att0x_%=:
v_mfma_f32_16x16x32_bf16 v[232:235],a[128:131],v[112:115],v[44:47]
s_waitcnt vmcnt(10)
s_sub_u32 s94,s94,3
v_exp_f32 v200,v200
v_mfma_f32_16x16x32_bf16 v[232:235],a[132:135],v[116:119],v[232:235]
s_barrier
s_add_u32 m0,0,s51
ds_read_b128 a[128:131],v1
v_mfma_f32_16x16x32_bf16 v[232:235],a[136:139],v[120:123],v[232:235]
s_lshr_b32 s60,s94,2
v_exp_f32 v201,v201
v_mfma_f32_16x16x32_bf16 v[232:235],a[140:143],v[124:127],v[232:235]
s_max_i32 s60,s60,124
ds_read_b128 a[132:135],v1 offset:256
v_cvt_pk_bf16_f32 v200,v200,v201
v_mfma_f32_16x16x32_bf16 v[236:239],a[156:159],v[124:127],v[44:47]
s_addk_i32 s54,16384
buffer_load_dwordx4 v4,s[20:23],s47 offen nt lds
v_mfma_f32_16x16x32_bf16 v[236:239],a[152:155],v[120:123],v[236:239]
s_and_b32 s46,s54,0xfffc0000
ds_read_b128 a[136:139],v1 offset:512
v_and_b32 v18,48,v6
v_mfma_f32_16x16x32_bf16 v[236:239],a[148:151],v[116:119],v[236:239]
s_sub_u32 s94,s94,3
v_exp_f32 v202,v202
v_mfma_f32_16x16x32_bf16 v[236:239],a[144:147],v[112:115],v[236:239]
s_addk_i32 s54,16384
ds_read_b128 a[140:143],v1 offset:768
v_lshrrev_b32 v19,4,v6
v_mfma_f32_16x16x32_bf16 v[248:251],a[160:163],v[112:115],v[44:47]
s_mul_i32 s83,s41,5
v_exp_f32 v203,v203
v_mfma_f32_16x16x32_bf16 v[248:251],a[164:167],v[116:119],v[248:251]
s_add_u32 s83,s83,s42
ds_read_b128 a[156:159],v1 offset:4864
v_cvt_pk_bf16_f32 v201,v202,v203
v_mfma_f32_16x16x32_bf16 v[248:251],a[168:171],v[120:123],v[248:251]
s_add_u32 s83,s83,15
v_exp_f32 v204,v204
v_mfma_f32_16x16x32_bf16 v[248:251],a[172:175],v[124:127],v[248:251]
s_lshl_b32 s43,s83,14
ds_read_b128 a[152:155],v1 offset:4608
v_lshlrev_b32 v18,2,v18
v_mfma_f32_16x16x32_bf16 v[252:255],a[188:191],v[124:127],v[44:47]
s_add_u32 s84,s55,0
v_exp_f32 v205,v205
v_mfma_f32_16x16x32_bf16 v[252:255],a[184:187],v[120:123],v[252:255]
s_add_u32 s85,s55,4096
ds_read_b128 a[148:151],v1 offset:4352
v_cvt_pk_bf16_f32 v202,v204,v205
v_mfma_f32_16x16x32_bf16 v[252:255],a[180:183],v[116:119],v[252:255]
s_add_u32 s86,s55,16384
v_exp_f32 v206,v206
v_mfma_f32_16x16x32_bf16 v[252:255],a[176:179],v[112:115],v[252:255]
s_add_u32 s87,s55,20480
ds_read_b128 a[144:147],v1 offset:4096
v_and_b32 v19,48,v19
v_mfma_f32_16x16x32_bf16 a[32:35],a[192:195],v[168:171],a[32:35]
s_lshl_b32 s83,s83,12
v_exp_f32 v207,v207
v_mfma_f32_16x16x32_bf16 a[32:35],a[196:199],v[172:175],a[32:35]
s_max_i32 s84,s84,s18
ds_read_b128 a[160:163],v1 offset:8192
v_cvt_pk_bf16_f32 v203,v206,v207
v_mfma_f32_16x16x32_bf16 a[36:39],a[204:207],v[172:175],a[36:39]
s_max_i32 s85,s85,s19
buffer_load_dwordx4 v4,s[20:23],s47 offen offset:1024 nt lds
v_mfma_f32_16x16x32_bf16 a[36:39],a[200:203],v[168:171],a[36:39]
s_max_i32 s86,s86,s100
ds_read_b128 a[164:167],v1 offset:8448
v_add_u32 v18,v18,v19
v_mfma_f32_16x16x32_bf16 a[40:43],a[208:211],v[168:171],a[40:43]
s_max_i32 s87,s87,s101
v_exp_f32 v216,v216
v_mfma_f32_16x16x32_bf16 a[40:43],a[212:215],v[172:175],a[40:43]
s_lshl_b32 s44,s40,5
ds_read_b128 a[168:171],v1 offset:8704
v_add_u32 v18,98304,v18
v_mfma_f32_16x16x32_bf16 a[44:47],a[220:223],v[172:175],a[44:47]
s_and_b32 s43,s43,0x20000
v_exp_f32 v217,v217
v_mfma_f32_16x16x32_bf16 a[44:47],a[216:219],v[168:171],a[44:47]
s_and_b32 s83,s83,0x7000
ds_read_b128 a[172:175],v1 offset:8960
v_cvt_pk_bf16_f32 v204,v216,v217
v_mfma_f32_16x16x32_bf16 a[48:51],a[224:227],v[168:171],a[48:51]
s_lshl_b32 s80,s40,10
v_exp_f32 v218,v218
v_mfma_f32_16x16x32_bf16 a[48:51],a[228:231],v[172:175],a[48:51]
s_and_b32 s44,s44,32
ds_read_b128 a[188:191],v1 offset:13056
v_add_u32 v2,s48,v2
v_mfma_f32_16x16x32_bf16 a[52:55],a[236:239],v[172:175],a[52:55]
s_or_b32 s83,s83,s43
v_exp_f32 v219,v219
v_mfma_f32_16x16x32_bf16 a[52:55],a[232:235],v[168:171],a[52:55]
s_mov_b32 s50,32768
ds_read_b128 a[184:187],v1 offset:12800
v_cvt_pk_bf16_f32 v205,v218,v219
v_mfma_f32_16x16x32_bf16 a[56:59],a[240:243],v[168:171],a[56:59]
s_mov_b32 s49,98304
v_exp_f32 v220,v220
v_mfma_f32_16x16x32_bf16 a[56:59],a[244:247],v[172:175],a[56:59]
s_mov_b32 s45,0x2a8
ds_read_b128 a[180:183],v1 offset:12544
v_add_u32 v3,s48,v3
v_mfma_f32_16x16x32_bf16 a[60:63],a[252:255],v[172:175],a[60:63]
s_mov_b32 s48,32768
v_exp_f32 v221,v221
v_mfma_f32_16x16x32_bf16 a[60:63],a[248:251],v[168:171],a[60:63]
s_add_u32 s80,s80,147456
ds_read_b128 a[176:179],v1 offset:12288
v_cvt_pk_bf16_f32 v206,v220,v221
v_smfmac_f32_16x16x64_bf16 v[52:55],v[28:31],v[168:175],v27
s_or_b32 s83,s83,0x40000
v_add_u32 v1,s48,v1
s_waitcnt lgkmcnt(4)
v_mfma_f32_16x16x32_bf16 v[136:139],a[128:131],v[64:67],v[32:35]
s_add_u32 s93,s92,L_endseg_%=-L_pc_%=
buffer_load_dwordx4 v5,s[20:23],s47 offen offset:2048 nt lds
v_mfma_f32_16x16x32_bf16 v[136:139],a[132:135],v[68:71],v[136:139]
v_exp_f32 v222,v222
v_mfma_f32_16x16x32_bf16 v[136:139],a[136:139],v[72:75],v[136:139]
v_exp_f32 v223,v223
v_mfma_f32_16x16x32_bf16 v[136:139],a[140:143],v[76:79],v[136:139]
v_cvt_pk_bf16_f32 v207,v222,v223
v_xor_b32 v25,s44,v4
v_mfma_f32_16x16x32_bf16 v[140:143],a[156:159],v[76:79],v[32:35]
v_exp_f32 v232,v232
v_mfma_f32_16x16x32_bf16 v[140:143],a[152:155],v[72:75],v[140:143]
v_exp_f32 v233,v233
v_mfma_f32_16x16x32_bf16 v[140:143],a[148:151],v[68:71],v[140:143]
v_exp_f32 v234,v234
v_mfma_f32_16x16x32_bf16 v[140:143],a[144:147],v[64:67],v[140:143]
v_exp_f32 v235,v235
v_mfma_f32_16x16x32_bf16 v[152:155],a[160:163],v[64:67],v[32:35]
v_exp_f32 v236,v236
v_mfma_f32_16x16x32_bf16 v[152:155],a[164:167],v[68:71],v[152:155]
v_exp_f32 v237,v237
v_mfma_f32_16x16x32_bf16 v[152:155],a[168:171],v[72:75],v[152:155]
v_exp_f32 v238,v238
v_mfma_f32_16x16x32_bf16 v[152:155],a[172:175],v[76:79],v[152:155]
s_waitcnt lgkmcnt(0)
v_exp_f32 v239,v239
v_mfma_f32_16x16x32_bf16 v[156:159],a[188:191],v[76:79],v[32:35]
v_exp_f32 v248,v248
v_mfma_f32_16x16x32_bf16 v[156:159],a[184:187],v[72:75],v[156:159]
buffer_load_dwordx4 v5,s[20:23],s47 offen offset:3072 nt lds
v_mfma_f32_16x16x32_bf16 v[156:159],a[180:183],v[68:71],v[156:159]
v_exp_f32 v249,v249
v_mfma_f32_16x16x32_bf16 v[156:159],a[176:179],v[64:67],v[156:159]
v_exp_f32 v250,v250
v_mfma_f32_16x16x32_bf16 a[64:67],a[192:195],v[200:203],a[64:67]
v_exp_f32 v251,v251
v_mfma_f32_16x16x32_bf16 a[64:67],a[196:199],v[204:207],a[64:67]
v_exp_f32 v252,v252
v_mfma_f32_16x16x32_bf16 a[68:71],a[204:207],v[204:207],a[68:71]
v_exp_f32 v253,v253
v_mfma_f32_16x16x32_bf16 a[68:71],a[200:203],v[200:203],a[68:71]
v_exp_f32 v254,v254
v_mfma_f32_16x16x32_bf16 a[72:75],a[208:211],v[200:203],a[72:75]
v_exp_f32 v255,v255
v_mfma_f32_16x16x32_bf16 a[72:75],a[212:215],v[204:207],a[72:75]
v_exp_f32 v136,v136
v_mfma_f32_16x16x32_bf16 a[76:79],a[220:223],v[204:207],a[76:79]
v_exp_f32 v137,v137
v_mfma_f32_16x16x32_bf16 a[76:79],a[216:219],v[200:203],a[76:79]
v_cvt_pk_bf16_f32 v232,v232,v233
v_cvt_pk_bf16_f32 v233,v234,v235
v_mfma_f32_16x16x32_bf16 a[80:83],a[224:227],v[200:203],a[80:83]
s_addk_i32 m0,16384
v_cvt_pk_bf16_f32 v234,v236,v237
v_cvt_pk_bf16_f32 v235,v238,v239
v_mfma_f32_16x16x32_bf16 a[80:83],a[228:231],v[204:207],a[80:83]
v_cvt_pk_bf16_f32 v236,v248,v249
v_cvt_pk_bf16_f32 v237,v250,v251
v_mfma_f32_16x16x32_bf16 a[84:87],a[236:239],v[204:207],a[84:87]
v_cvt_pk_bf16_f32 v238,v252,v253
v_cvt_pk_bf16_f32 v239,v254,v255
v_mfma_f32_16x16x32_bf16 a[84:87],a[232:235],v[200:203],a[84:87]
buffer_load_dwordx4 v4,s[24:27],s47 offen nt lds
v_mfma_f32_16x16x32_bf16 a[88:91],a[240:243],v[200:203],a[88:91]
v_exp_f32 v138,v138
v_mfma_f32_16x16x32_bf16 a[88:91],a[244:247],v[204:207],a[88:91]
v_exp_f32 v139,v139
v_mfma_f32_16x16x32_bf16 a[92:95],a[252:255],v[204:207],a[92:95]
v_exp_f32 v140,v140
v_mfma_f32_16x16x32_bf16 a[92:95],a[248:251],v[200:203],a[92:95]
v_exp_f32 v141,v141
v_smfmac_f32_16x16x64_bf16 v[56:59],v[28:31],v[200:207],v27
v_cvt_pk_bf16_f32 v136,v136,v137
v_mfma_f32_16x16x32_bf16 v[168:171],a[128:131],v[80:83],v[36:39]
v_exp_f32 v142,v142
v_mfma_f32_16x16x32_bf16 v[168:171],a[132:135],v[84:87],v[168:171]
v_exp_f32 v143,v143
v_mfma_f32_16x16x32_bf16 v[168:171],a[136:139],v[88:91],v[168:171]
v_exp_f32 v152,v152
v_mfma_f32_16x16x32_bf16 v[168:171],a[140:143],v[92:95],v[168:171]
v_exp_f32 v153,v153
v_mfma_f32_16x16x32_bf16 v[172:175],a[156:159],v[92:95],v[36:39]
v_exp_f32 v154,v154
v_mfma_f32_16x16x32_bf16 v[172:175],a[152:155],v[88:91],v[172:175]
v_exp_f32 v155,v155
v_mfma_f32_16x16x32_bf16 v[172:175],a[148:151],v[84:87],v[172:175]
v_exp_f32 v156,v156
v_mfma_f32_16x16x32_bf16 v[172:175],a[144:147],v[80:83],v[172:175]
v_exp_f32 v157,v157
v_mfma_f32_16x16x32_bf16 v[184:187],a[160:163],v[80:83],v[36:39]
buffer_load_dwordx4 v5,s[24:27],s47 offen offset:1024 nt lds
v_mfma_f32_16x16x32_bf16 v[184:187],a[164:167],v[84:87],v[184:187]
v_exp_f32 v158,v158
v_mfma_f32_16x16x32_bf16 v[184:187],a[168:171],v[88:91],v[184:187]
v_exp_f32 v159,v159
v_mfma_f32_16x16x32_bf16 v[184:187],a[172:175],v[92:95],v[184:187]
v_exp_f32 v168,v168
v_mfma_f32_16x16x32_bf16 v[188:191],a[188:191],v[92:95],v[36:39]
v_exp_f32 v169,v169
v_mfma_f32_16x16x32_bf16 v[188:191],a[184:187],v[88:91],v[188:191]
v_cvt_pk_bf16_f32 v137,v138,v139
v_cvt_pk_bf16_f32 v138,v140,v141
v_mfma_f32_16x16x32_bf16 v[188:191],a[180:183],v[84:87],v[188:191]
s_waitcnt vmcnt(14)
v_cvt_pk_bf16_f32 v139,v142,v143
v_cvt_pk_bf16_f32 v140,v152,v153
v_mfma_f32_16x16x32_bf16 v[188:191],a[176:179],v[80:83],v[188:191]
s_barrier
ds_read_b128 v[80:83],v18
v_cvt_pk_bf16_f32 v141,v154,v155
v_mfma_f32_16x16x32_bf16 a[96:99],a[192:195],v[232:235],a[96:99]
ds_read_b64_tr_b16 a[192:193],v2
v_cvt_pk_bf16_f32 v142,v156,v157
v_mfma_f32_16x16x32_bf16 a[96:99],a[196:199],v[236:239],a[96:99]
ds_read_b64_tr_b16 a[194:195],v2 offset:4096
v_cvt_pk_bf16_f32 v143,v158,v159
v_mfma_f32_16x16x32_bf16 a[100:103],a[204:207],v[236:239],a[100:103]
ds_read_b64_tr_b16 a[196:197],v2 offset:8192
v_cvt_pk_bf16_f32 v168,v168,v169
v_mfma_f32_16x16x32_bf16 a[100:103],a[200:203],v[232:235],a[100:103]
v_exp_f32 v170,v170
v_mfma_f32_16x16x32_bf16 a[104:107],a[208:211],v[232:235],a[104:107]
v_exp_f32 v171,v171
v_mfma_f32_16x16x32_bf16 a[104:107],a[212:215],v[236:239],a[104:107]
buffer_load_dwordx4 v4,s[24:27],s47 offen offset:2048 nt lds
v_mfma_f32_16x16x32_bf16 a[108:111],a[220:223],v[236:239],a[108:111]
ds_read_b128 v[84:87],v18 offset:256
v_cvt_pk_bf16_f32 v169,v170,v171
v_mfma_f32_16x16x32_bf16 a[108:111],a[216:219],v[232:235],a[108:111]
v_exp_f32 v172,v172
v_mfma_f32_16x16x32_bf16 a[112:115],a[224:227],v[232:235],a[112:115]
v_exp_f32 v173,v173
v_mfma_f32_16x16x32_bf16 a[112:115],a[228:231],v[236:239],a[112:115]
ds_read_b128 v[88:91],v18 offset:512
v_cvt_pk_bf16_f32 v170,v172,v173
v_mfma_f32_16x16x32_bf16 a[116:119],a[236:239],v[236:239],a[116:119]
v_exp_f32 v174,v174
v_mfma_f32_16x16x32_bf16 a[116:119],a[232:235],v[232:235],a[116:119]
ds_read_b128 v[92:95],v18 offset:768
v_mfma_f32_16x16x32_bf16 a[120:123],a[240:243],v[232:235],a[120:123]
v_exp_f32 v175,v175
v_mfma_f32_16x16x32_bf16 a[120:123],a[244:247],v[236:239],a[120:123]
ds_read_b64_tr_b16 a[198:199],v2 offset:12288
ds_read_b64_tr_b16 a[204:205],v3 offset:8192
v_mfma_f32_16x16x32_bf16 a[124:127],a[252:255],v[236:239],a[124:127]
ds_read_b64_tr_b16 a[206:207],v3 offset:12288
v_cvt_pk_bf16_f32 v171,v174,v175
v_mfma_f32_16x16x32_bf16 a[124:127],a[248:251],v[232:235],a[124:127]
)A"
R"A(v_exp_f32 v184,v184
v_smfmac_f32_16x16x64_bf16 v[60:63],v[28:31],v[232:239],v27
ds_read_b64_tr_b16 a[200:201],v3
v_mfma_f32_16x16x32_bf16 v[200:203],a[128:131],v[96:99],v[40:43]
ds_read_b64_tr_b16 a[202:203],v3 offset:4096
ds_read_b64_tr_b16 a[208:209],v2 offset:256
v_mfma_f32_16x16x32_bf16 v[200:203],a[132:135],v[100:103],v[200:203]
v_exp_f32 v185,v185
v_mfma_f32_16x16x32_bf16 v[200:203],a[136:139],v[104:107],v[200:203]
buffer_load_dwordx4 v5,s[24:27],s47 offen offset:3072 nt lds
v_mfma_f32_16x16x32_bf16 v[200:203],a[140:143],v[108:111],v[200:203]
s_or_b32 s47,s47,0x18000
ds_read_b64_tr_b16 a[210:211],v2 offset:4352
ds_read_b64_tr_b16 a[212:213],v2 offset:8448
v_mfma_f32_16x16x32_bf16 v[204:207],a[156:159],v[108:111],v[40:43]
s_addk_i32 s47,4096
s_and_b32 s47,s47,0x27000
ds_read_b64_tr_b16 a[214:215],v2 offset:12544
v_mfma_f32_16x16x32_bf16 v[204:207],a[152:155],v[104:107],v[204:207]
s_or_b32 s47,s47,s46
ds_read_b64_tr_b16 a[220:221],v3 offset:8448
v_cvt_pk_bf16_f32 v172,v184,v185
v_mfma_f32_16x16x32_bf16 v[204:207],a[148:151],v[100:103],v[204:207]
s_and_b32 s46,s54,0xfffc0000
v_exp_f32 v186,v186
v_mfma_f32_16x16x32_bf16 v[204:207],a[144:147],v[96:99],v[204:207]
s_addk_i32 s54,16384
ds_read_b64_tr_b16 a[222:223],v3 offset:12544
ds_read_b64_tr_b16 a[216:217],v3 offset:256
v_mfma_f32_16x16x32_bf16 v[216:219],a[160:163],v[96:99],v[40:43]
v_exp_f32 v187,v187
v_mfma_f32_16x16x32_bf16 v[216:219],a[164:167],v[100:103],v[216:219]
ds_read_b64_tr_b16 a[218:219],v3 offset:4352
ds_read_b64_tr_b16 a[224:225],v2 offset:512
v_mfma_f32_16x16x32_bf16 v[216:219],a[168:171],v[104:107],v[216:219]
ds_read_b64_tr_b16 a[226:227],v2 offset:4608
v_cvt_pk_bf16_f32 v173,v186,v187
v_mfma_f32_16x16x32_bf16 v[216:219],a[172:175],v[108:111],v[216:219]
ds_read_b64_tr_b16 a[228:229],v2 offset:8704
v_mfma_f32_16x16x32_bf16 v[220:223],a[188:191],v[108:111],v[40:43]
ds_read_b64_tr_b16 a[230:231],v2 offset:12800
v_mfma_f32_16x16x32_bf16 v[220:223],a[184:187],v[104:107],v[220:223]
ds_read_b64_tr_b16 a[236:237],v3 offset:8704
ds_read_b64_tr_b16 a[238:239],v3 offset:12800
v_mfma_f32_16x16x32_bf16 v[220:223],a[180:183],v[100:103],v[220:223]
ds_read_b64_tr_b16 a[232:233],v3 offset:512
v_mfma_f32_16x16x32_bf16 v[220:223],a[176:179],v[96:99],v[220:223]
s_waitcnt lgkmcnt(8)
ds_read_b64_tr_b16 a[234:235],v3 offset:4608
v_mfma_f32_16x16x32_bf16 v[32:35],v[80:83],v[128:131],0
ds_read_b64_tr_b16 a[240:241],v2 offset:768
v_mfma_f32_16x16x32_bf16 v[32:35],v[84:87],v[160:163],v[32:35]
ds_read_b64_tr_b16 a[242:243],v2 offset:4864
ds_read_b64_tr_b16 a[244:245],v2 offset:8960
v_mfma_f32_16x16x32_bf16 v[32:35],v[88:91],v[192:195],v[32:35]
ds_read_b64_tr_b16 a[246:247],v2 offset:13056
v_add_u32 v2,s48,v2
v_mfma_f32_16x16x32_bf16 v[32:35],v[92:95],v[224:227],v[32:35]
ds_read_b64_tr_b16 a[252:253],v3 offset:8960
v_mfma_f32_16x16x32_bf16 v[36:39],v[80:83],v[132:135],0
ds_read_b64_tr_b16 a[254:255],v3 offset:13056
v_mfma_f32_16x16x32_bf16 v[36:39],v[84:87],v[164:167],v[36:39]
ds_read_b64_tr_b16 a[248:249],v3 offset:768
ds_read_b64_tr_b16 a[250:251],v3 offset:4864
v_mfma_f32_16x16x32_bf16 v[36:39],v[88:91],v[196:199],v[36:39]
v_exp_f32 v188,v188
v_mfma_f32_16x16x32_bf16 v[36:39],v[92:95],v[228:231],v[36:39]
v_exp_f32 v189,v189
v_mfma_f32_16x16x32_bf16 v[40:43],v[80:83],v[144:147],0
v_exp_f32 v190,v190
v_mfma_f32_16x16x32_bf16 v[40:43],v[84:87],v[176:179],v[40:43]
v_exp_f32 v191,v191
v_mfma_f32_16x16x32_bf16 v[40:43],v[88:91],v[208:211],v[40:43]
v_cvt_pk_bf16_f32 v174,v188,v189
v_cvt_pk_bf16_f32 v175,v190,v191
v_mfma_f32_16x16x32_bf16 v[40:43],v[92:95],v[240:243],v[40:43]
v_max3_f32 v32,v32,v33,v34
v_max3_f32 v32,v32,v35,s18
v_mfma_f32_16x16x32_bf16 a[0:3],a[192:195],v[136:139],a[0:3]
v_sub_f32 v35,0,v32
v_sub_f32 v34,0,v32
v_mfma_f32_16x16x32_bf16 a[0:3],a[196:199],v[140:143],a[0:3]
v_sub_f32 v33,0,v32
v_sub_f32 v32,0,v32
v_mfma_f32_16x16x32_bf16 a[4:7],a[204:207],v[140:143],a[4:7]
v_exp_f32 v200,v200
v_mfma_f32_16x16x32_bf16 a[4:7],a[200:203],v[136:139],a[4:7]
s_memrealtime s[76:77]
v_exp_f32 v201,v201
v_mfma_f32_16x16x32_bf16 a[8:11],a[208:211],v[136:139],a[8:11]
v_exp_f32 v202,v202
v_mfma_f32_16x16x32_bf16 a[8:11],a[212:215],v[140:143],a[8:11]
v_exp_f32 v203,v203
v_mfma_f32_16x16x32_bf16 a[12:15],a[220:223],v[140:143],a[12:15]
s_waitcnt lgkmcnt(0)
v_cvt_pk_bf16_f32 v200,v200,v201
v_cvt_pk_bf16_f32 v201,v202,v203
v_mfma_f32_16x16x32_bf16 a[12:15],a[216:219],v[136:139],a[12:15]
v_exp_f32 v204,v204
v_mfma_f32_16x16x32_bf16 a[16:19],a[224:227],v[136:139],a[16:19]
v_exp_f32 v205,v205
v_mfma_f32_16x16x32_bf16 a[16:19],a[228:231],v[140:143],a[16:19]
v_exp_f32 v206,v206
v_mfma_f32_16x16x32_bf16 a[20:23],a[236:239],v[140:143],a[20:23]
s_waitcnt lgkmcnt(0)
v_exp_f32 v207,v207
v_mfma_f32_16x16x32_bf16 a[20:23],a[232:235],v[136:139],a[20:23]
s_sub_u32 s79,s78,s76
v_cvt_pk_bf16_f32 v202,v204,v205
v_cvt_pk_bf16_f32 v203,v206,v207
v_mfma_f32_16x16x32_bf16 a[24:27],a[240:243],v[136:139],a[24:27]
s_add_u32 s78,s76,s60
v_exp_f32 v216,v216
v_mfma_f32_16x16x32_bf16 a[24:27],a[244:247],v[140:143],a[24:27]
s_cmp_gt_i32 s79,s95
s_cbranch_scc1 L_ps4_%=
L_pr4_%=:
v_max3_f32 v36,v36,v37,v38
v_mfma_f32_16x16x32_bf16 a[28:31],a[252:255],v[140:143],a[28:31]
s_lshr_b32 s60,s94,2
v_exp_f32 v217,v217
v_mfma_f32_16x16x32_bf16 a[28:31],a[248:251],v[136:139],a[28:31]
s_max_i32 s60,s60,124
v_exp_f32 v218,v218
v_smfmac_f32_16x16x64_bf16 v[48:51],v[28:31],v[136:143],v27
s_sub_u32 s94,s94,3
v_cvt_pk_bf16_f32 v204,v216,v217
v_mfma_f32_16x16x32_bf16 v[232:235],a[128:131],v[112:115],v[44:47]
s_waitcnt vmcnt(9)
v_exp_f32 v219,v219
v_mfma_f32_16x16x32_bf16 v[232:235],a[132:135],v[116:119],v[232:235]
s_barrier
s_add_u32 m0,32768,s51
ds_read_b128 a[128:131],v1
v_mfma_f32_16x16x32_bf16 v[232:235],a[136:139],v[120:123],v[232:235]
v_exp_f32 v220,v220
v_mfma_f32_16x16x32_bf16 v[232:235],a[140:143],v[124:127],v[232:235]
ds_read_b128 a[132:135],v1 offset:256
v_cvt_pk_bf16_f32 v205,v218,v219
v_mfma_f32_16x16x32_bf16 v[236:239],a[156:159],v[124:127],v[44:47]
buffer_load_dwordx4 v4,s[20:23],s47 offen nt lds
v_mfma_f32_16x16x32_bf16 v[236:239],a[152:155],v[120:123],v[236:239]
ds_read_b128 a[136:139],v1 offset:512
v_max3_f32 v36,v36,v39,s19
v_mfma_f32_16x16x32_bf16 v[236:239],a[148:151],v[116:119],v[236:239]
v_exp_f32 v221,v221
v_mfma_f32_16x16x32_bf16 v[236:239],a[144:147],v[112:115],v[236:239]
ds_read_b128 a[140:143],v1 offset:768
v_cvt_pk_bf16_f32 v206,v220,v221
v_mfma_f32_16x16x32_bf16 v[248:251],a[160:163],v[112:115],v[44:47]
v_exp_f32 v222,v222
v_mfma_f32_16x16x32_bf16 v[248:251],a[164:167],v[116:119],v[248:251]
ds_read_b128 a[156:159],v1 offset:4864
v_sub_f32 v39,0,v36
v_mfma_f32_16x16x32_bf16 v[248:251],a[168:171],v[120:123],v[248:251]
v_exp_f32 v223,v223
v_mfma_f32_16x16x32_bf16 v[248:251],a[172:175],v[124:127],v[248:251]
ds_read_b128 a[152:155],v1 offset:4608
v_cvt_pk_bf16_f32 v207,v222,v223
v_mfma_f32_16x16x32_bf16 v[252:255],a[188:191],v[124:127],v[44:47]
v_sub_f32 v38,0,v36
v_sub_f32 v37,0,v36
v_mfma_f32_16x16x32_bf16 v[252:255],a[184:187],v[120:123],v[252:255]
ds_read_b128 a[148:151],v1 offset:4352
v_sub_f32 v36,0,v36
v_mfma_f32_16x16x32_bf16 v[252:255],a[180:183],v[116:119],v[252:255]
v_exp_f32 v232,v232
v_mfma_f32_16x16x32_bf16 v[252:255],a[176:179],v[112:115],v[252:255]
ds_read_b128 a[144:147],v1 offset:4096
v_max3_f32 v40,v40,v41,v42
v_mfma_f32_16x16x32_bf16 v[44:47],v[80:83],v[148:151],0
v_exp_f32 v233,v233
v_mfma_f32_16x16x32_bf16 v[44:47],v[84:87],v[180:183],v[44:47]
ds_read_b128 a[160:163],v1 offset:8192
v_cvt_pk_bf16_f32 v232,v232,v233
v_mfma_f32_16x16x32_bf16 v[44:47],v[88:91],v[212:215],v[44:47]
buffer_load_dwordx4 v4,s[20:23],s47 offen offset:1024 nt lds
v_mfma_f32_16x16x32_bf16 v[44:47],v[92:95],v[244:247],v[44:47]
ds_read_b128 a[164:167],v1 offset:8448
v_max3_f32 v40,v40,v43,s100
v_mfma_f32_16x16x32_bf16 a[32:35],a[192:195],v[168:171],a[32:35]
v_exp_f32 v234,v234
v_mfma_f32_16x16x32_bf16 a[32:35],a[196:199],v[172:175],a[32:35]
ds_read_b128 a[168:171],v1 offset:8704
v_sub_f32 v43,0,v40
v_mfma_f32_16x16x32_bf16 a[36:39],a[204:207],v[172:175],a[36:39]
v_exp_f32 v235,v235
v_mfma_f32_16x16x32_bf16 a[36:39],a[200:203],v[168:171],a[36:39]
ds_read_b128 a[172:175],v1 offset:8960
v_cvt_pk_bf16_f32 v233,v234,v235
v_mfma_f32_16x16x32_bf16 a[40:43],a[208:211],v[168:171],a[40:43]
v_exp_f32 v236,v236
v_mfma_f32_16x16x32_bf16 a[40:43],a[212:215],v[172:175],a[40:43]
ds_read_b128 a[188:191],v1 offset:13056
v_sub_f32 v42,0,v40
v_mfma_f32_16x16x32_bf16 a[44:47],a[220:223],v[172:175],a[44:47]
v_exp_f32 v237,v237
v_mfma_f32_16x16x32_bf16 a[44:47],a[216:219],v[168:171],a[44:47]
ds_read_b128 a[184:187],v1 offset:12800
v_cvt_pk_bf16_f32 v234,v236,v237
v_mfma_f32_16x16x32_bf16 a[48:51],a[224:227],v[168:171],a[48:51]
v_exp_f32 v238,v238
v_mfma_f32_16x16x32_bf16 a[48:51],a[228:231],v[172:175],a[48:51]
ds_read_b128 a[180:183],v1 offset:12544
v_sub_f32 v41,0,v40
v_mfma_f32_16x16x32_bf16 a[52:55],a[236:239],v[172:175],a[52:55]
v_exp_f32 v239,v239
v_mfma_f32_16x16x32_bf16 a[52:55],a[232:235],v[168:171],a[52:55]
ds_read_b128 a[176:179],v1 offset:12288
v_cvt_pk_bf16_f32 v235,v238,v239
v_mfma_f32_16x16x32_bf16 a[56:59],a[240:243],v[168:171],a[56:59]
buffer_load_dwordx4 v5,s[20:23],s47 offen offset:2048 nt lds
v_mfma_f32_16x16x32_bf16 a[56:59],a[244:247],v[172:175],a[56:59]
v_exp_f32 v248,v248
v_mfma_f32_16x16x32_bf16 a[60:63],a[252:255],v[172:175],a[60:63]
v_exp_f32 v249,v249
v_mfma_f32_16x16x32_bf16 a[60:63],a[248:251],v[168:171],a[60:63]
v_exp_f32 v250,v250
v_smfmac_f32_16x16x64_bf16 v[52:55],v[28:31],v[168:175],v27
s_waitcnt lgkmcnt(2)
v_cvt_pk_bf16_f32 v236,v248,v249
v_mfma_f32_16x16x32_bf16 v[136:139],a[128:131],v[128:131],v[32:35]
v_exp_f32 v251,v251
v_mfma_f32_16x16x32_bf16 v[136:139],a[132:135],v[160:163],v[136:139]
v_exp_f32 v252,v252
v_mfma_f32_16x16x32_bf16 v[136:139],a[136:139],v[192:195],v[136:139]
v_exp_f32 v253,v253
v_mfma_f32_16x16x32_bf16 v[136:139],a[140:143],v[224:227],v[136:139]
v_exp_f32 v254,v254
v_mfma_f32_16x16x32_bf16 v[140:143],a[156:159],v[224:227],v[32:35]
v_exp_f32 v255,v255
v_mfma_f32_16x16x32_bf16 v[140:143],a[152:155],v[192:195],v[140:143]
v_cvt_pk_bf16_f32 v237,v250,v251
v_cvt_pk_bf16_f32 v238,v252,v253
v_mfma_f32_16x16x32_bf16 v[140:143],a[148:151],v[160:163],v[140:143]
v_cvt_pk_bf16_f32 v239,v254,v255
v_sub_f32 v40,0,v40
v_mfma_f32_16x16x32_bf16 v[140:143],a[144:147],v[128:131],v[140:143]
v_add_u32 v3,s48,v3
v_add_f32 v48,v49,v48
v_mfma_f32_16x16x32_bf16 v[152:155],a[160:163],v[128:131],v[32:35]
s_mov_b32 s48,-98304
v_max_f32 v20,0x00800000,v48
v_accvgpr_read_b32 v64,a0
v_mfma_f32_16x16x32_bf16 v[152:155],a[164:167],v[160:163],v[152:155]
buffer_load_dwordx4 v5,s[20:23],s47 offen offset:3072 nt lds
v_mfma_f32_16x16x32_bf16 v[152:155],a[168:171],v[192:195],v[152:155]
v_exp_f32 v136,v136
v_mfma_f32_16x16x32_bf16 v[152:155],a[172:175],v[224:227],v[152:155]
v_exp_f32 v137,v137
v_mfma_f32_16x16x32_bf16 v[156:159],a[188:191],v[224:227],v[32:35]
v_exp_f32 v138,v138
v_mfma_f32_16x16x32_bf16 v[156:159],a[184:187],v[192:195],v[156:159]
s_waitcnt lgkmcnt(0)
v_exp_f32 v139,v139
v_mfma_f32_16x16x32_bf16 v[156:159],a[180:183],v[160:163],v[156:159]
v_cvt_pk_bf16_f32 v136,v136,v137
v_cvt_pk_bf16_f32 v137,v138,v139
v_mfma_f32_16x16x32_bf16 v[156:159],a[176:179],v[128:131],v[156:159]
v_exp_f32 v140,v140
v_mfma_f32_16x16x32_bf16 a[64:67],a[192:195],v[200:203],a[64:67]
v_exp_f32 v141,v141
v_mfma_f32_16x16x32_bf16 a[64:67],a[196:199],v[204:207],a[64:67]
v_exp_f32 v142,v142
v_mfma_f32_16x16x32_bf16 a[68:71],a[204:207],v[204:207],a[68:71]
v_exp_f32 v143,v143
v_mfma_f32_16x16x32_bf16 a[68:71],a[200:203],v[200:203],a[68:71]
v_cvt_pk_bf16_f32 v138,v140,v141
v_cvt_pk_bf16_f32 v139,v142,v143
v_mfma_f32_16x16x32_bf16 a[72:75],a[208:211],v[200:203],a[72:75]
s_addk_i32 m0,16384
v_exp_f32 v152,v152
v_mfma_f32_16x16x32_bf16 a[72:75],a[212:215],v[204:207],a[72:75]
v_exp_f32 v153,v153
v_mfma_f32_16x16x32_bf16 a[76:79],a[220:223],v[204:207],a[76:79]
v_exp_f32 v154,v154
v_mfma_f32_16x16x32_bf16 a[76:79],a[216:219],v[200:203],a[76:79]
buffer_load_dwordx4 v4,s[24:27],s47 offen nt lds
v_mfma_f32_16x16x32_bf16 a[80:83],a[224:227],v[200:203],a[80:83]
)A"
R"A(v_exp_f32 v155,v155
v_mfma_f32_16x16x32_bf16 a[80:83],a[228:231],v[204:207],a[80:83]
v_exp_f32 v156,v156
v_mfma_f32_16x16x32_bf16 a[84:87],a[236:239],v[204:207],a[84:87]
v_exp_f32 v157,v157
v_mfma_f32_16x16x32_bf16 a[84:87],a[232:235],v[200:203],a[84:87]
v_exp_f32 v158,v158
v_mfma_f32_16x16x32_bf16 a[88:91],a[240:243],v[200:203],a[88:91]
v_exp_f32 v159,v159
v_mfma_f32_16x16x32_bf16 a[88:91],a[244:247],v[204:207],a[88:91]
v_cvt_pk_bf16_f32 v140,v152,v153
v_cvt_pk_bf16_f32 v141,v154,v155
v_mfma_f32_16x16x32_bf16 a[92:95],a[252:255],v[204:207],a[92:95]
v_cvt_pk_bf16_f32 v142,v156,v157
v_cvt_pk_bf16_f32 v143,v158,v159
v_mfma_f32_16x16x32_bf16 a[92:95],a[248:251],v[200:203],a[92:95]
v_rcp_f32 v20,v20
v_smfmac_f32_16x16x64_bf16 v[56:59],v[28:31],v[200:207],v27
v_accvgpr_read_b32 v65,a1
v_mfma_f32_16x16x32_bf16 v[168:171],a[128:131],v[132:135],v[36:39]
v_accvgpr_read_b32 v66,a2
v_accvgpr_read_b32 v67,a3
v_mfma_f32_16x16x32_bf16 v[168:171],a[132:135],v[164:167],v[168:171]
v_mul_f32 v64,v20,v64
v_mul_f32 v65,v20,v65
v_mfma_f32_16x16x32_bf16 v[168:171],a[136:139],v[196:199],v[168:171]
v_cvt_pk_bf16_f32 v64,v64,v65
v_mul_f32 v66,v20,v66
v_mfma_f32_16x16x32_bf16 v[168:171],a[140:143],v[228:231],v[168:171]
v_mul_f32 v67,v20,v67
v_cvt_pk_bf16_f32 v65,v66,v67
v_mfma_f32_16x16x32_bf16 v[172:175],a[156:159],v[228:231],v[36:39]
buffer_load_dwordx4 v5,s[24:27],s47 offen offset:1024 nt lds
v_mfma_f32_16x16x32_bf16 v[172:175],a[152:155],v[196:199],v[172:175]
v_accvgpr_read_b32 v16,a6
v_accvgpr_read_b32 v17,a7
v_mfma_f32_16x16x32_bf16 v[172:175],a[148:151],v[164:167],v[172:175]
v_accvgpr_read_b32 v66,a4
v_accvgpr_read_b32 v67,a5
v_mfma_f32_16x16x32_bf16 v[172:175],a[144:147],v[132:135],v[172:175]
v_accvgpr_read_b32 v68,a8
v_accvgpr_read_b32 v69,a9
v_accvgpr_read_b32 v70,a10
v_accvgpr_read_b32 v71,a11
v_accvgpr_read_b32 v12,a12
v_accvgpr_read_b32 v13,a13
v_accvgpr_read_b32 v14,a14
v_accvgpr_read_b32 v15,a15
v_pk_mul_f32 v[68:69],v[20:21],v[68:69] op_sel:[0,0] op_sel_hi:[0,1]
v_pk_mul_f32 v[70:71],v[20:21],v[70:71] op_sel:[0,0] op_sel_hi:[0,1]
v_pk_mul_f32 v[12:13],v[20:21],v[12:13] op_sel:[0,0] op_sel_hi:[0,1]
v_pk_mul_f32 v[14:15],v[20:21],v[14:15] op_sel:[0,0] op_sel_hi:[0,1]
v_cvt_pk_bf16_f32 v68,v68,v69
v_cvt_pk_bf16_f32 v69,v70,v71
v_cvt_pk_bf16_f32 v70,v12,v13
v_cvt_pk_bf16_f32 v71,v14,v15
v_mfma_f32_16x16x32_bf16 v[184:187],a[160:163],v[132:135],v[36:39]
v_accvgpr_read_b32 v72,a16
v_accvgpr_read_b32 v73,a17
v_accvgpr_read_b32 v74,a18
v_accvgpr_read_b32 v75,a19
v_accvgpr_read_b32 v12,a20
v_accvgpr_read_b32 v13,a21
v_accvgpr_read_b32 v14,a22
v_accvgpr_read_b32 v15,a23
v_pk_mul_f32 v[72:73],v[20:21],v[72:73] op_sel:[0,0] op_sel_hi:[0,1]
v_pk_mul_f32 v[74:75],v[20:21],v[74:75] op_sel:[0,0] op_sel_hi:[0,1]
v_pk_mul_f32 v[12:13],v[20:21],v[12:13] op_sel:[0,0] op_sel_hi:[0,1]
v_pk_mul_f32 v[14:15],v[20:21],v[14:15] op_sel:[0,0] op_sel_hi:[0,1]
v_cvt_pk_bf16_f32 v72,v72,v73
v_cvt_pk_bf16_f32 v73,v74,v75
v_cvt_pk_bf16_f32 v74,v12,v13
v_cvt_pk_bf16_f32 v75,v14,v15
v_mfma_f32_16x16x32_bf16 v[184:187],a[164:167],v[164:167],v[184:187]
v_accvgpr_read_b32 v76,a24
v_accvgpr_read_b32 v77,a25
v_accvgpr_read_b32 v78,a26
v_accvgpr_read_b32 v79,a27
v_accvgpr_read_b32 v12,a28
v_accvgpr_read_b32 v13,a29
v_accvgpr_read_b32 v14,a30
v_accvgpr_read_b32 v15,a31
v_pk_mul_f32 v[76:77],v[20:21],v[76:77] op_sel:[0,0] op_sel_hi:[0,1]
v_pk_mul_f32 v[78:79],v[20:21],v[78:79] op_sel:[0,0] op_sel_hi:[0,1]
v_pk_mul_f32 v[12:13],v[20:21],v[12:13] op_sel:[0,0] op_sel_hi:[0,1]
v_pk_mul_f32 v[14:15],v[20:21],v[14:15] op_sel:[0,0] op_sel_hi:[0,1]
v_cvt_pk_bf16_f32 v76,v76,v77
v_cvt_pk_bf16_f32 v77,v78,v79
v_cvt_pk_bf16_f32 v78,v12,v13
v_cvt_pk_bf16_f32 v79,v14,v15
v_mfma_f32_16x16x32_bf16 v[184:187],a[168:171],v[196:199],v[184:187]
v_cmp_nlt_f32 s[56:57],v48,s63
v_max3_f32 v44,v44,v45,v46
v_mfma_f32_16x16x32_bf16 v[184:187],a[172:175],v[228:231],v[184:187]
v_max3_f32 v44,v44,v47,s101
v_mov_b32 v48,0
v_mfma_f32_16x16x32_bf16 v[188:191],a[188:191],v[228:231],v[36:39]
v_mov_b32 v49,0
v_mov_b32 v50,0
v_mfma_f32_16x16x32_bf16 v[188:191],a[184:187],v[196:199],v[188:191]
v_mov_b32 v51,0
v_sub_f32 v47,0,v44
v_mfma_f32_16x16x32_bf16 v[188:191],a[180:183],v[164:167],v[188:191]
v_sub_f32 v46,0,v44
v_sub_f32 v45,0,v44
v_mfma_f32_16x16x32_bf16 v[188:191],a[176:179],v[132:135],v[188:191]
v_sub_f32 v44,0,v44
v_add_f32 v52,v53,v52
v_mfma_f32_16x16x32_bf16 a[96:99],a[192:195],v[232:235],a[96:99]
ds_read_b64_tr_b16 a[192:193],v2
ds_read_b64_tr_b16 a[194:195],v2 offset:4096
v_mfma_f32_16x16x32_bf16 a[96:99],a[196:199],v[236:239],a[96:99]
ds_read_b64_tr_b16 a[196:197],v2 offset:8192
v_max_f32 v21,0x00800000,v52
v_mfma_f32_16x16x32_bf16 a[100:103],a[204:207],v[236:239],a[100:103]
buffer_load_dwordx4 v4,s[24:27],s47 offen offset:2048 nt lds
v_mfma_f32_16x16x32_bf16 a[100:103],a[200:203],v[232:235],a[100:103]
ds_read_b64_tr_b16 a[198:199],v2 offset:12288
ds_read_b64_tr_b16 a[204:205],v3 offset:8192
v_mfma_f32_16x16x32_bf16 a[104:107],a[208:211],v[232:235],a[104:107]
ds_read_b64_tr_b16 a[206:207],v3 offset:12288
v_accvgpr_read_b32 v80,a32
v_mfma_f32_16x16x32_bf16 a[104:107],a[212:215],v[236:239],a[104:107]
ds_read_b64_tr_b16 a[200:201],v3
v_accvgpr_read_b32 v81,a33
v_mfma_f32_16x16x32_bf16 a[108:111],a[220:223],v[236:239],a[108:111]
ds_read_b64_tr_b16 a[202:203],v3 offset:4096
v_accvgpr_read_b32 v82,a34
v_mfma_f32_16x16x32_bf16 a[108:111],a[216:219],v[232:235],a[108:111]
ds_read_b64_tr_b16 a[208:209],v2 offset:256
ds_read_b64_tr_b16 a[210:211],v2 offset:4352
v_mfma_f32_16x16x32_bf16 a[112:115],a[224:227],v[232:235],a[112:115]
ds_read_b64_tr_b16 a[212:213],v2 offset:8448
v_accvgpr_read_b32 v83,a35
v_mfma_f32_16x16x32_bf16 a[112:115],a[228:231],v[236:239],a[112:115]
ds_read_b64_tr_b16 a[214:215],v2 offset:12544
v_accvgpr_read_b32 v12,a38
v_mfma_f32_16x16x32_bf16 a[116:119],a[236:239],v[236:239],a[116:119]
ds_read_b64_tr_b16 a[220:221],v3 offset:8448
v_accvgpr_read_b32 v13,a39
v_mfma_f32_16x16x32_bf16 a[116:119],a[232:235],v[232:235],a[116:119]
ds_read_b64_tr_b16 a[222:223],v3 offset:12544
ds_read_b64_tr_b16 a[216:217],v3 offset:256
v_mfma_f32_16x16x32_bf16 a[120:123],a[240:243],v[232:235],a[120:123]
ds_read_b64_tr_b16 a[218:219],v3 offset:4352
v_add_u32 v1,s48,v1
v_mfma_f32_16x16x32_bf16 a[120:123],a[244:247],v[236:239],a[120:123]
ds_read_b64_tr_b16 a[224:225],v2 offset:512
v_mov_b32 v53,0
v_mfma_f32_16x16x32_bf16 a[124:127],a[252:255],v[236:239],a[124:127]
ds_read_b64_tr_b16 a[226:227],v2 offset:4608
v_mov_b32 v54,0
v_mfma_f32_16x16x32_bf16 a[124:127],a[248:251],v[232:235],a[124:127]
ds_read_b64_tr_b16 a[228:229],v2 offset:8704
ds_read_b64_tr_b16 a[230:231],v2 offset:12800
v_smfmac_f32_16x16x64_bf16 v[60:63],v[28:31],v[232:239],v27
ds_read_b64_tr_b16 a[236:237],v3 offset:8704
v_mfma_f32_16x16x32_bf16 v[200:203],a[128:131],v[144:147],v[40:43]
buffer_load_dwordx4 v5,s[24:27],s47 offen offset:3072 nt lds
v_mfma_f32_16x16x32_bf16 v[200:203],a[132:135],v[176:179],v[200:203]
s_or_b32 s47,s47,0x18000
ds_read_b64_tr_b16 a[238:239],v3 offset:12800
ds_read_b64_tr_b16 a[232:233],v3 offset:512
v_mfma_f32_16x16x32_bf16 v[200:203],a[136:139],v[208:211],v[200:203]
s_addk_i32 s47,4096
s_and_b32 s47,s47,0x27000
ds_read_b64_tr_b16 a[234:235],v3 offset:4608
v_mfma_f32_16x16x32_bf16 v[200:203],a[140:143],v[240:243],v[200:203]
s_or_b32 s47,s47,s46
ds_read_b64_tr_b16 a[240:241],v2 offset:768
v_mov_b32 v55,0
v_mfma_f32_16x16x32_bf16 v[204:207],a[156:159],v[240:243],v[40:43]
s_and_b32 s46,s54,0xfffc0000
ds_read_b64_tr_b16 a[242:243],v2 offset:4864
v_add_f32 v56,v57,v56
v_mfma_f32_16x16x32_bf16 v[204:207],a[152:155],v[208:211],v[204:207]
ds_read_b64_tr_b16 a[244:245],v2 offset:8960
ds_read_b64_tr_b16 a[246:247],v2 offset:13056
v_mfma_f32_16x16x32_bf16 v[204:207],a[148:151],v[176:179],v[204:207]
ds_read_b64_tr_b16 a[252:253],v3 offset:8960
v_max_f32 v22,0x00800000,v56
v_mfma_f32_16x16x32_bf16 v[204:207],a[144:147],v[144:147],v[204:207]
ds_read_b64_tr_b16 a[254:255],v3 offset:13056
v_accvgpr_read_b32 v96,a64
v_mfma_f32_16x16x32_bf16 v[216:219],a[160:163],v[144:147],v[40:43]
ds_read_b64_tr_b16 a[248:249],v3 offset:768
v_accvgpr_read_b32 v97,a65
v_mfma_f32_16x16x32_bf16 v[216:219],a[164:167],v[176:179],v[216:219]
ds_read_b64_tr_b16 a[250:251],v3 offset:4864
v_accvgpr_read_b32 v98,a66
v_mfma_f32_16x16x32_bf16 v[216:219],a[168:171],v[208:211],v[216:219]
v_exp_f32 v168,v168
v_mfma_f32_16x16x32_bf16 v[216:219],a[172:175],v[240:243],v[216:219]
v_exp_f32 v169,v169
v_mfma_f32_16x16x32_bf16 v[220:223],a[188:191],v[240:243],v[40:43]
v_exp_f32 v170,v170
v_mfma_f32_16x16x32_bf16 v[220:223],a[184:187],v[208:211],v[220:223]
v_exp_f32 v171,v171
v_mfma_f32_16x16x32_bf16 v[220:223],a[180:183],v[176:179],v[220:223]
v_cvt_pk_bf16_f32 v168,v168,v169
v_cvt_pk_bf16_f32 v169,v170,v171
v_mfma_f32_16x16x32_bf16 v[220:223],a[176:179],v[144:147],v[220:223]
s_waitcnt lgkmcnt(2)
v_exp_f32 v172,v172
v_mfma_f32_16x16x32_bf16 a[0:3],a[192:195],v[136:139],0
v_exp_f32 v173,v173
v_mfma_f32_16x16x32_bf16 a[0:3],a[196:199],v[140:143],a[0:3]
v_exp_f32 v174,v174
v_mfma_f32_16x16x32_bf16 a[4:7],a[204:207],v[140:143],0
v_exp_f32 v175,v175
v_mfma_f32_16x16x32_bf16 a[4:7],a[200:203],v[136:139],a[4:7]
s_memrealtime s[76:77]
v_cvt_pk_bf16_f32 v170,v172,v173
v_cvt_pk_bf16_f32 v171,v174,v175
v_mfma_f32_16x16x32_bf16 a[8:11],a[208:211],v[136:139],0
v_exp_f32 v184,v184
v_mfma_f32_16x16x32_bf16 a[8:11],a[212:215],v[140:143],a[8:11]
v_exp_f32 v185,v185
v_mfma_f32_16x16x32_bf16 a[12:15],a[220:223],v[140:143],0
v_exp_f32 v186,v186
v_mfma_f32_16x16x32_bf16 a[12:15],a[216:219],v[136:139],a[12:15]
v_exp_f32 v187,v187
v_mfma_f32_16x16x32_bf16 a[16:19],a[224:227],v[136:139],0
v_exp_f32 v188,v188
v_mfma_f32_16x16x32_bf16 a[16:19],a[228:231],v[140:143],a[16:19]
v_exp_f32 v189,v189
v_mfma_f32_16x16x32_bf16 a[20:23],a[236:239],v[140:143],0
s_waitcnt lgkmcnt(0)
v_exp_f32 v190,v190
v_mfma_f32_16x16x32_bf16 a[20:23],a[232:235],v[136:139],a[20:23]
s_sub_u32 s79,s78,s76
v_exp_f32 v191,v191
v_mfma_f32_16x16x32_bf16 a[24:27],a[240:243],v[136:139],0
s_add_u32 s78,s76,s60
v_rcp_f32 v21,v21
v_mfma_f32_16x16x32_bf16 a[24:27],a[244:247],v[140:143],a[24:27]
s_cmp_gt_i32 s79,s95
s_cbranch_scc1 L_ps5_%=
L_pr5_%=:
v_cvt_pk_bf16_f32 v172,v184,v185
v_mfma_f32_16x16x32_bf16 a[28:31],a[252:255],v[140:143],0
s_lshr_b32 s60,s94,2
v_cvt_pk_bf16_f32 v173,v186,v187
v_cvt_pk_bf16_f32 v174,v188,v189
v_mfma_f32_16x16x32_bf16 a[28:31],a[248:251],v[136:139],a[28:31]
s_max_i32 s60,s60,124
v_cvt_pk_bf16_f32 v175,v190,v191
v_mul_f32 v80,v21,v80
v_smfmac_f32_16x16x64_bf16 v[48:51],v[28:31],v[136:143],v27
v_mul_f32 v81,v21,v81
v_mfma_f32_16x16x32_bf16 v[232:235],a[128:131],v[148:151],v[44:47]
s_waitcnt vmcnt(8)
v_cvt_pk_bf16_f32 v80,v80,v81
v_mul_f32 v82,v21,v82
v_mfma_f32_16x16x32_bf16 v[232:235],a[132:135],v[180:183],v[232:235]
s_barrier
s_add_u32 m0,65536,s51
v_mul_f32 v83,v21,v83
v_mfma_f32_16x16x32_bf16 v[232:235],a[136:139],v[212:215],v[232:235]
v_cvt_pk_bf16_f32 v81,v82,v83
v_accvgpr_read_b32 v82,a36
v_mfma_f32_16x16x32_bf16 v[232:235],a[140:143],v[244:247],v[232:235]
v_accvgpr_read_b32 v83,a37
v_mul_f32 v82,v21,v82
v_mfma_f32_16x16x32_bf16 v[236:239],a[156:159],v[244:247],v[44:47]
buffer_load_dwordx4 v4,s[20:23],s47 offen nt lds
v_mfma_f32_16x16x32_bf16 v[236:239],a[152:155],v[212:215],v[236:239]
v_mul_f32 v83,v21,v83
v_mul_f32 v12,v21,v12
v_mfma_f32_16x16x32_bf16 v[236:239],a[148:151],v[180:183],v[236:239]
v_mul_f32 v13,v21,v13
v_cvt_pk_bf16_f32 v82,v82,v83
v_mfma_f32_16x16x32_bf16 v[236:239],a[144:147],v[148:151],v[236:239]
ds_read_b128 a[128:131],v1
v_cvt_pk_bf16_f32 v83,v12,v13
v_mfma_f32_16x16x32_bf16 v[248:251],a[160:163],v[148:151],v[44:47]
v_accvgpr_read_b32 v84,a40
v_accvgpr_read_b32 v85,a41
v_accvgpr_read_b32 v86,a42
v_accvgpr_read_b32 v87,a43
v_accvgpr_read_b32 v12,a44
v_accvgpr_read_b32 v13,a45
v_accvgpr_read_b32 v14,a46
v_accvgpr_read_b32 v15,a47
v_pk_mul_f32 v[84:85],v[20:21],v[84:85] op_sel:[1,0] op_sel_hi:[1,1]
v_pk_mul_f32 v[86:87],v[20:21],v[86:87] op_sel:[1,0] op_sel_hi:[1,1]
v_pk_mul_f32 v[12:13],v[20:21],v[12:13] op_sel:[1,0] op_sel_hi:[1,1]
v_pk_mul_f32 v[14:15],v[20:21],v[14:15] op_sel:[1,0] op_sel_hi:[1,1]
)A"
R"A(v_cvt_pk_bf16_f32 v84,v84,v85
v_cvt_pk_bf16_f32 v85,v86,v87
v_cvt_pk_bf16_f32 v86,v12,v13
v_cvt_pk_bf16_f32 v87,v14,v15
v_mfma_f32_16x16x32_bf16 v[248:251],a[164:167],v[180:183],v[248:251]
v_accvgpr_read_b32 v88,a48
v_accvgpr_read_b32 v89,a49
v_accvgpr_read_b32 v90,a50
v_accvgpr_read_b32 v91,a51
v_accvgpr_read_b32 v12,a52
v_accvgpr_read_b32 v13,a53
v_accvgpr_read_b32 v14,a54
v_accvgpr_read_b32 v15,a55
v_pk_mul_f32 v[88:89],v[20:21],v[88:89] op_sel:[1,0] op_sel_hi:[1,1]
v_pk_mul_f32 v[90:91],v[20:21],v[90:91] op_sel:[1,0] op_sel_hi:[1,1]
v_pk_mul_f32 v[12:13],v[20:21],v[12:13] op_sel:[1,0] op_sel_hi:[1,1]
v_pk_mul_f32 v[14:15],v[20:21],v[14:15] op_sel:[1,0] op_sel_hi:[1,1]
v_cvt_pk_bf16_f32 v88,v88,v89
v_cvt_pk_bf16_f32 v89,v90,v91
v_cvt_pk_bf16_f32 v90,v12,v13
v_cvt_pk_bf16_f32 v91,v14,v15
v_mfma_f32_16x16x32_bf16 v[248:251],a[168:171],v[212:215],v[248:251]
ds_read_b128 a[132:135],v1 offset:256
v_accvgpr_read_b32 v99,a67
v_mfma_f32_16x16x32_bf16 v[248:251],a[172:175],v[244:247],v[248:251]
v_accvgpr_read_b32 v92,a56
v_accvgpr_read_b32 v93,a57
v_accvgpr_read_b32 v94,a58
v_accvgpr_read_b32 v95,a59
v_accvgpr_read_b32 v12,a60
v_accvgpr_read_b32 v13,a61
v_accvgpr_read_b32 v14,a62
v_accvgpr_read_b32 v15,a63
v_pk_mul_f32 v[92:93],v[20:21],v[92:93] op_sel:[1,0] op_sel_hi:[1,1]
v_pk_mul_f32 v[94:95],v[20:21],v[94:95] op_sel:[1,0] op_sel_hi:[1,1]
v_pk_mul_f32 v[12:13],v[20:21],v[12:13] op_sel:[1,0] op_sel_hi:[1,1]
v_pk_mul_f32 v[14:15],v[20:21],v[14:15] op_sel:[1,0] op_sel_hi:[1,1]
v_cvt_pk_bf16_f32 v92,v92,v93
v_cvt_pk_bf16_f32 v93,v94,v95
v_cvt_pk_bf16_f32 v94,v12,v13
v_cvt_pk_bf16_f32 v95,v14,v15
v_mfma_f32_16x16x32_bf16 v[252:255],a[188:191],v[244:247],v[44:47]
ds_read_b128 a[136:139],v1 offset:512
v_accvgpr_read_b32 v14,a70
v_mfma_f32_16x16x32_bf16 v[252:255],a[184:187],v[212:215],v[252:255]
v_cmp_nlt_f32 s[58:59],v52,s63
s_nop 1
s_or_b64 s[56:57],s[56:57],s[58:59]
v_mfma_f32_16x16x32_bf16 v[252:255],a[180:183],v[180:183],v[252:255]
ds_read_b128 a[140:143],v1 offset:768
v_mov_b32 v52,0
v_mfma_f32_16x16x32_bf16 v[252:255],a[176:179],v[148:151],v[252:255]
v_exp_f32 v200,v200
v_mfma_f32_16x16x32_bf16 a[32:35],a[192:195],v[168:171],0
ds_read_b128 a[156:159],v1 offset:4864
v_accvgpr_read_b32 v15,a71
v_mfma_f32_16x16x32_bf16 a[32:35],a[196:199],v[172:175],a[32:35]
v_exp_f32 v201,v201
v_mfma_f32_16x16x32_bf16 a[36:39],a[204:207],v[172:175],0
buffer_load_dwordx4 v4,s[20:23],s47 offen offset:1024 nt lds
v_mfma_f32_16x16x32_bf16 a[36:39],a[200:203],v[168:171],a[36:39]
ds_read_b128 a[152:155],v1 offset:4608
v_cvt_pk_bf16_f32 v200,v200,v201
v_mfma_f32_16x16x32_bf16 a[40:43],a[208:211],v[168:171],0
v_exp_f32 v202,v202
v_mfma_f32_16x16x32_bf16 a[40:43],a[212:215],v[172:175],a[40:43]
ds_read_b128 a[148:151],v1 offset:4352
v_mov_b32 v57,0
v_mfma_f32_16x16x32_bf16 a[44:47],a[220:223],v[172:175],0
v_exp_f32 v203,v203
v_mfma_f32_16x16x32_bf16 a[44:47],a[216:219],v[168:171],a[44:47]
ds_read_b128 a[144:147],v1 offset:4096
v_cvt_pk_bf16_f32 v201,v202,v203
v_mfma_f32_16x16x32_bf16 a[48:51],a[224:227],v[168:171],0
v_exp_f32 v204,v204
v_mfma_f32_16x16x32_bf16 a[48:51],a[228:231],v[172:175],a[48:51]
ds_read_b128 a[160:163],v1 offset:8192
v_mov_b32 v58,0
v_mfma_f32_16x16x32_bf16 a[52:55],a[236:239],v[172:175],0
v_exp_f32 v205,v205
v_mfma_f32_16x16x32_bf16 a[52:55],a[232:235],v[168:171],a[52:55]
ds_read_b128 a[164:167],v1 offset:8448
v_cvt_pk_bf16_f32 v202,v204,v205
v_mfma_f32_16x16x32_bf16 a[56:59],a[240:243],v[168:171],0
v_exp_f32 v206,v206
v_mfma_f32_16x16x32_bf16 a[56:59],a[244:247],v[172:175],a[56:59]
ds_read_b128 a[168:171],v1 offset:8704
v_mov_b32 v59,0
v_mfma_f32_16x16x32_bf16 a[60:63],a[252:255],v[172:175],0
v_exp_f32 v207,v207
v_mfma_f32_16x16x32_bf16 a[60:63],a[248:251],v[168:171],a[60:63]
ds_read_b128 a[172:175],v1 offset:8960
v_cvt_pk_bf16_f32 v203,v206,v207
v_smfmac_f32_16x16x64_bf16 v[52:55],v[28:31],v[168:175],v27
s_waitcnt lgkmcnt(4)
v_add_f32 v60,v61,v60
v_mfma_f32_16x16x32_bf16 v[136:139],a[128:131],v[128:131],v[32:35]
buffer_load_dwordx4 v5,s[20:23],s47 offen offset:2048 nt lds
v_mfma_f32_16x16x32_bf16 v[136:139],a[132:135],v[160:163],v[136:139]
ds_read_b128 a[188:191],v1 offset:13056
v_max_f32 v23,0x00800000,v60
v_mfma_f32_16x16x32_bf16 v[136:139],a[136:139],v[192:195],v[136:139]
v_exp_f32 v216,v216
v_mfma_f32_16x16x32_bf16 v[136:139],a[140:143],v[224:227],v[136:139]
ds_read_b128 a[184:187],v1 offset:12800
v_accvgpr_read_b32 v112,a96
v_mfma_f32_16x16x32_bf16 v[140:143],a[156:159],v[224:227],v[32:35]
v_exp_f32 v217,v217
v_mfma_f32_16x16x32_bf16 v[140:143],a[152:155],v[192:195],v[140:143]
ds_read_b128 a[180:183],v1 offset:12544
v_cvt_pk_bf16_f32 v204,v216,v217
v_mfma_f32_16x16x32_bf16 v[140:143],a[148:151],v[160:163],v[140:143]
ds_read_b128 a[176:179],v1 offset:12288
v_mfma_f32_16x16x32_bf16 v[140:143],a[144:147],v[128:131],v[140:143]
s_waitcnt lgkmcnt(4)
v_exp_f32 v218,v218
v_mfma_f32_16x16x32_bf16 v[152:155],a[160:163],v[128:131],v[32:35]
v_exp_f32 v219,v219
v_mfma_f32_16x16x32_bf16 v[152:155],a[164:167],v[160:163],v[152:155]
v_exp_f32 v220,v220
v_mfma_f32_16x16x32_bf16 v[152:155],a[168:171],v[192:195],v[152:155]
v_exp_f32 v221,v221
v_mfma_f32_16x16x32_bf16 v[152:155],a[172:175],v[224:227],v[152:155]
s_waitcnt lgkmcnt(2)
v_exp_f32 v222,v222
v_mfma_f32_16x16x32_bf16 v[156:159],a[188:191],v[224:227],v[32:35]
v_exp_f32 v223,v223
v_mfma_f32_16x16x32_bf16 v[156:159],a[184:187],v[192:195],v[156:159]
s_waitcnt lgkmcnt(1)
v_rcp_f32 v22,v22
v_mfma_f32_16x16x32_bf16 v[156:159],a[180:183],v[160:163],v[156:159]
s_waitcnt lgkmcnt(0)
v_cvt_pk_bf16_f32 v205,v218,v219
v_cvt_pk_bf16_f32 v206,v220,v221
v_cvt_pk_bf16_f32 v207,v222,v223
v_mfma_f32_16x16x32_bf16 v[156:159],a[176:179],v[128:131],v[156:159]
v_mul_f32 v96,v22,v96
v_mul_f32 v97,v22,v97
v_mfma_f32_16x16x32_bf16 a[64:67],a[192:195],v[200:203],0
buffer_load_dwordx4 v5,s[20:23],s47 offen offset:3072 nt lds
v_cvt_pk_bf16_f32 v96,v96,v97
v_mul_f32 v98,v22,v98
v_mul_f32 v99,v22,v99
v_cvt_pk_bf16_f32 v97,v98,v99
v_mfma_f32_16x16x32_bf16 a[64:67],a[196:199],v[204:207],a[64:67]
v_accvgpr_read_b32 v98,a68
v_accvgpr_read_b32 v99,a69
v_mul_f32 v98,v22,v98
v_mul_f32 v99,v22,v99
v_mfma_f32_16x16x32_bf16 a[68:71],a[204:207],v[204:207],0
v_mul_f32 v14,v22,v14
v_mul_f32 v15,v22,v15
v_cvt_pk_bf16_f32 v98,v98,v99
v_cvt_pk_bf16_f32 v99,v14,v15
v_mfma_f32_16x16x32_bf16 a[68:71],a[200:203],v[200:203],a[68:71]
v_accvgpr_read_b32 v100,a72
v_accvgpr_read_b32 v101,a73
v_accvgpr_read_b32 v102,a74
v_accvgpr_read_b32 v103,a75
v_accvgpr_read_b32 v12,a76
v_accvgpr_read_b32 v13,a77
v_accvgpr_read_b32 v14,a78
v_accvgpr_read_b32 v15,a79
v_pk_mul_f32 v[100:101],v[22:23],v[100:101] op_sel:[0,0] op_sel_hi:[0,1]
v_pk_mul_f32 v[102:103],v[22:23],v[102:103] op_sel:[0,0] op_sel_hi:[0,1]
v_pk_mul_f32 v[12:13],v[22:23],v[12:13] op_sel:[0,0] op_sel_hi:[0,1]
v_pk_mul_f32 v[14:15],v[22:23],v[14:15] op_sel:[0,0] op_sel_hi:[0,1]
v_cvt_pk_bf16_f32 v100,v100,v101
v_cvt_pk_bf16_f32 v101,v102,v103
v_cvt_pk_bf16_f32 v102,v12,v13
v_cvt_pk_bf16_f32 v103,v14,v15
v_mfma_f32_16x16x32_bf16 a[72:75],a[208:211],v[200:203],0
v_accvgpr_read_b32 v104,a80
v_accvgpr_read_b32 v105,a81
v_accvgpr_read_b32 v106,a82
v_accvgpr_read_b32 v107,a83
v_accvgpr_read_b32 v12,a84
v_accvgpr_read_b32 v13,a85
v_accvgpr_read_b32 v14,a86
v_accvgpr_read_b32 v15,a87
v_pk_mul_f32 v[104:105],v[22:23],v[104:105] op_sel:[0,0] op_sel_hi:[0,1]
v_pk_mul_f32 v[106:107],v[22:23],v[106:107] op_sel:[0,0] op_sel_hi:[0,1]
v_pk_mul_f32 v[12:13],v[22:23],v[12:13] op_sel:[0,0] op_sel_hi:[0,1]
v_pk_mul_f32 v[14:15],v[22:23],v[14:15] op_sel:[0,0] op_sel_hi:[0,1]
v_cvt_pk_bf16_f32 v104,v104,v105
v_cvt_pk_bf16_f32 v105,v106,v107
v_cvt_pk_bf16_f32 v106,v12,v13
v_cvt_pk_bf16_f32 v107,v14,v15
v_mfma_f32_16x16x32_bf16 a[72:75],a[212:215],v[204:207],a[72:75]
v_accvgpr_read_b32 v108,a88
v_accvgpr_read_b32 v109,a89
v_accvgpr_read_b32 v110,a90
v_accvgpr_read_b32 v111,a91
v_accvgpr_read_b32 v12,a92
v_accvgpr_read_b32 v13,a93
v_accvgpr_read_b32 v14,a94
v_accvgpr_read_b32 v15,a95
v_pk_mul_f32 v[108:109],v[22:23],v[108:109] op_sel:[0,0] op_sel_hi:[0,1]
v_pk_mul_f32 v[110:111],v[22:23],v[110:111] op_sel:[0,0] op_sel_hi:[0,1]
v_pk_mul_f32 v[12:13],v[22:23],v[12:13] op_sel:[0,0] op_sel_hi:[0,1]
v_pk_mul_f32 v[14:15],v[22:23],v[14:15] op_sel:[0,0] op_sel_hi:[0,1]
v_cvt_pk_bf16_f32 v108,v108,v109
v_cvt_pk_bf16_f32 v109,v110,v111
v_cvt_pk_bf16_f32 v110,v12,v13
v_cvt_pk_bf16_f32 v111,v14,v15
v_mfma_f32_16x16x32_bf16 a[76:79],a[220:223],v[204:207],0
v_cmp_nlt_f32 s[58:59],v56,s63
s_nop 1
s_or_b64 s[56:57],s[56:57],s[58:59]
v_mfma_f32_16x16x32_bf16 a[76:79],a[216:219],v[200:203],a[76:79]
v_mov_b32 v56,0
v_accvgpr_read_b32 v113,a97
v_mfma_f32_16x16x32_bf16 a[80:83],a[224:227],v[200:203],0
s_addk_i32 m0,16384
v_exp_f32 v232,v232
v_mfma_f32_16x16x32_bf16 a[80:83],a[228:231],v[204:207],a[80:83]
v_exp_f32 v233,v233
v_mfma_f32_16x16x32_bf16 a[84:87],a[236:239],v[204:207],0
v_exp_f32 v234,v234
v_mfma_f32_16x16x32_bf16 a[84:87],a[232:235],v[200:203],a[84:87]
buffer_load_dwordx4 v4,s[24:27],s47 offen nt lds
v_mfma_f32_16x16x32_bf16 a[88:91],a[240:243],v[200:203],0
v_exp_f32 v235,v235
v_mfma_f32_16x16x32_bf16 a[88:91],a[244:247],v[204:207],a[88:91]
v_cvt_pk_bf16_f32 v232,v232,v233
v_cvt_pk_bf16_f32 v233,v234,v235
v_mfma_f32_16x16x32_bf16 a[92:95],a[252:255],v[204:207],0
v_exp_f32 v236,v236
v_mfma_f32_16x16x32_bf16 a[92:95],a[248:251],v[200:203],a[92:95]
v_exp_f32 v237,v237
v_smfmac_f32_16x16x64_bf16 v[56:59],v[28:31],v[200:207],v27
v_cvt_pk_bf16_f32 v234,v236,v237
v_mfma_f32_16x16x32_bf16 v[168:171],a[128:131],v[132:135],v[36:39]
v_exp_f32 v238,v238
v_mfma_f32_16x16x32_bf16 v[168:171],a[132:135],v[164:167],v[168:171]
v_exp_f32 v239,v239
v_mfma_f32_16x16x32_bf16 v[168:171],a[136:139],v[196:199],v[168:171]
v_cvt_pk_bf16_f32 v235,v238,v239
v_accvgpr_read_b32 v114,a98
v_mfma_f32_16x16x32_bf16 v[168:171],a[140:143],v[228:231],v[168:171]
v_exp_f32 v248,v248
v_mfma_f32_16x16x32_bf16 v[172:175],a[156:159],v[228:231],v[36:39]
v_exp_f32 v249,v249
v_mfma_f32_16x16x32_bf16 v[172:175],a[152:155],v[196:199],v[172:175]
v_exp_f32 v250,v250
v_mfma_f32_16x16x32_bf16 v[172:175],a[148:151],v[164:167],v[172:175]
v_exp_f32 v251,v251
v_mfma_f32_16x16x32_bf16 v[172:175],a[144:147],v[132:135],v[172:175]
v_exp_f32 v252,v252
v_mfma_f32_16x16x32_bf16 v[184:187],a[160:163],v[132:135],v[36:39]
buffer_load_dwordx4 v5,s[24:27],s47 offen offset:1024 nt lds
v_mfma_f32_16x16x32_bf16 v[184:187],a[164:167],v[164:167],v[184:187]
v_exp_f32 v253,v253
v_mfma_f32_16x16x32_bf16 v[184:187],a[168:171],v[196:199],v[184:187]
v_exp_f32 v254,v254
v_mfma_f32_16x16x32_bf16 v[184:187],a[172:175],v[228:231],v[184:187]
v_exp_f32 v255,v255
v_mfma_f32_16x16x32_bf16 v[188:191],a[188:191],v[228:231],v[36:39]
v_cvt_pk_bf16_f32 v236,v248,v249
v_cvt_pk_bf16_f32 v237,v250,v251
v_mfma_f32_16x16x32_bf16 v[188:191],a[184:187],v[196:199],v[188:191]
v_cvt_pk_bf16_f32 v238,v252,v253
v_cvt_pk_bf16_f32 v239,v254,v255
v_mfma_f32_16x16x32_bf16 v[188:191],a[180:183],v[164:167],v[188:191]
v_rcp_f32 v23,v23
v_mfma_f32_16x16x32_bf16 v[188:191],a[176:179],v[132:135],v[188:191]
v_accvgpr_read_b32 v115,a99
v_mul_f32 v66,v20,v66
v_mfma_f32_16x16x32_bf16 a[96:99],a[192:195],v[232:235],0
v_mul_f32 v67,v20,v67
v_mul_f32 v112,v23,v112
v_mul_f32 v113,v23,v113
v_mul_f32 v16,v20,v16
v_mul_f32 v17,v20,v17
v_cvt_pk_bf16_f32 v66,v66,v67
v_cvt_pk_bf16_f32 v112,v112,v113
v_mul_f32 v114,v23,v114
v_mul_f32 v115,v23,v115
v_mfma_f32_16x16x32_bf16 a[96:99],a[196:199],v[236:239],a[96:99]
v_cvt_pk_bf16_f32 v67,v16,v17
v_cvt_pk_bf16_f32 v113,v114,v115
v_accvgpr_read_b32 v16,a102
v_accvgpr_read_b32 v17,a103
v_accvgpr_read_b32 v114,a100
v_accvgpr_read_b32 v115,a101
v_mfma_f32_16x16x32_bf16 a[100:103],a[204:207],v[236:239],0
v_accvgpr_read_b32 v116,a104
v_accvgpr_read_b32 v117,a105
v_accvgpr_read_b32 v118,a106
v_accvgpr_read_b32 v119,a107
v_accvgpr_read_b32 v12,a108
v_accvgpr_read_b32 v13,a109
v_accvgpr_read_b32 v14,a110
v_accvgpr_read_b32 v15,a111
v_pk_mul_f32 v[116:117],v[22:23],v[116:117] op_sel:[1,0] op_sel_hi:[1,1]
v_pk_mul_f32 v[118:119],v[22:23],v[118:119] op_sel:[1,0] op_sel_hi:[1,1]
)A"
R"A(v_pk_mul_f32 v[12:13],v[22:23],v[12:13] op_sel:[1,0] op_sel_hi:[1,1]
v_pk_mul_f32 v[14:15],v[22:23],v[14:15] op_sel:[1,0] op_sel_hi:[1,1]
v_cvt_pk_bf16_f32 v116,v116,v117
v_cvt_pk_bf16_f32 v117,v118,v119
v_cvt_pk_bf16_f32 v118,v12,v13
v_cvt_pk_bf16_f32 v119,v14,v15
v_mfma_f32_16x16x32_bf16 a[100:103],a[200:203],v[232:235],a[100:103]
v_accvgpr_read_b32 v120,a112
v_accvgpr_read_b32 v121,a113
v_accvgpr_read_b32 v122,a114
v_accvgpr_read_b32 v123,a115
v_accvgpr_read_b32 v12,a116
v_accvgpr_read_b32 v13,a117
v_accvgpr_read_b32 v14,a118
v_accvgpr_read_b32 v15,a119
v_pk_mul_f32 v[120:121],v[22:23],v[120:121] op_sel:[1,0] op_sel_hi:[1,1]
v_pk_mul_f32 v[122:123],v[22:23],v[122:123] op_sel:[1,0] op_sel_hi:[1,1]
v_pk_mul_f32 v[12:13],v[22:23],v[12:13] op_sel:[1,0] op_sel_hi:[1,1]
v_pk_mul_f32 v[14:15],v[22:23],v[14:15] op_sel:[1,0] op_sel_hi:[1,1]
v_cvt_pk_bf16_f32 v120,v120,v121
v_cvt_pk_bf16_f32 v121,v122,v123
v_cvt_pk_bf16_f32 v122,v12,v13
v_cvt_pk_bf16_f32 v123,v14,v15
v_mfma_f32_16x16x32_bf16 a[104:107],a[208:211],v[232:235],0
v_accvgpr_read_b32 v124,a120
v_accvgpr_read_b32 v125,a121
v_accvgpr_read_b32 v126,a122
v_accvgpr_read_b32 v127,a123
v_accvgpr_read_b32 v12,a124
v_accvgpr_read_b32 v13,a125
v_accvgpr_read_b32 v14,a126
v_accvgpr_read_b32 v15,a127
v_pk_mul_f32 v[124:125],v[22:23],v[124:125] op_sel:[1,0] op_sel_hi:[1,1]
v_pk_mul_f32 v[126:127],v[22:23],v[126:127] op_sel:[1,0] op_sel_hi:[1,1]
v_pk_mul_f32 v[12:13],v[22:23],v[12:13] op_sel:[1,0] op_sel_hi:[1,1]
v_pk_mul_f32 v[14:15],v[22:23],v[14:15] op_sel:[1,0] op_sel_hi:[1,1]
v_cvt_pk_bf16_f32 v124,v124,v125
v_cvt_pk_bf16_f32 v125,v126,v127
v_cvt_pk_bf16_f32 v126,v12,v13
v_cvt_pk_bf16_f32 v127,v14,v15
v_mfma_f32_16x16x32_bf16 a[104:107],a[212:215],v[236:239],a[104:107]
v_cmp_nlt_f32 s[58:59],v60,s63
s_nop 1
s_or_b64 s[56:57],s[56:57],s[58:59]
v_mfma_f32_16x16x32_bf16 a[108:111],a[220:223],v[236:239],0
buffer_load_dwordx4 v4,s[24:27],s47 offen offset:2048 nt lds
v_mfma_f32_16x16x32_bf16 a[108:111],a[216:219],v[232:235],a[108:111]
v_mov_b32 v60,0
v_mov_b32 v61,0
v_mfma_f32_16x16x32_bf16 a[112:115],a[224:227],v[232:235],0
v_mov_b32 v62,0
v_mov_b32 v63,0
v_mfma_f32_16x16x32_bf16 a[112:115],a[228:231],v[236:239],a[112:115]
v_add_u32 v2,s48,v2
v_add_u32 v3,s48,v3
v_mfma_f32_16x16x32_bf16 a[116:119],a[236:239],v[236:239],0
s_mov_b32 s48,32768
ds_read_b64_tr_b16 a[192:193],v2
ds_read_b64_tr_b16 a[194:195],v2 offset:4096
v_mfma_f32_16x16x32_bf16 a[116:119],a[232:235],v[232:235],a[116:119]
ds_read_b64_tr_b16 a[196:197],v2 offset:8192
v_mul_f32 v114,v23,v114
v_mfma_f32_16x16x32_bf16 a[120:123],a[240:243],v[232:235],0
ds_read_b64_tr_b16 a[198:199],v2 offset:12288
v_mul_f32 v115,v23,v115
v_mfma_f32_16x16x32_bf16 a[120:123],a[244:247],v[236:239],a[120:123]
ds_read_b64_tr_b16 a[204:205],v3 offset:8192
v_mul_f32 v16,v23,v16
v_mfma_f32_16x16x32_bf16 a[124:127],a[252:255],v[236:239],0
ds_read_b64_tr_b16 a[206:207],v3 offset:12288
ds_read_b64_tr_b16 a[200:201],v3
v_mfma_f32_16x16x32_bf16 a[124:127],a[248:251],v[232:235],a[124:127]
ds_read_b64_tr_b16 a[202:203],v3 offset:4096
v_mul_f32 v17,v23,v17
v_smfmac_f32_16x16x64_bf16 v[60:63],v[28:31],v[232:239],v27
ds_read_b64_tr_b16 a[208:209],v2 offset:256
v_mfma_f32_16x16x32_bf16 v[200:203],a[128:131],v[144:147],v[40:43]
v_exp_f32 v136,v136
v_mfma_f32_16x16x32_bf16 v[200:203],a[132:135],v[176:179],v[200:203]
v_exp_f32 v137,v137
v_mfma_f32_16x16x32_bf16 v[200:203],a[136:139],v[208:211],v[200:203]
ds_read_b64_tr_b16 a[210:211],v2 offset:4352
v_cvt_pk_bf16_f32 v136,v136,v137
v_mfma_f32_16x16x32_bf16 v[200:203],a[140:143],v[240:243],v[200:203]
buffer_load_dwordx4 v5,s[24:27],s47 offen offset:3072 nt lds
v_mfma_f32_16x16x32_bf16 v[204:207],a[156:159],v[240:243],v[40:43]
s_or_b32 s47,s47,0x18000
v_exp_f32 v138,v138
v_mfma_f32_16x16x32_bf16 v[204:207],a[152:155],v[208:211],v[204:207]
s_addk_i32 s47,4096
v_exp_f32 v139,v139
v_mfma_f32_16x16x32_bf16 v[204:207],a[148:151],v[176:179],v[204:207]
s_and_b32 s47,s47,0x27000
v_exp_f32 v140,v140
v_mfma_f32_16x16x32_bf16 v[204:207],a[144:147],v[144:147],v[204:207]
s_or_b32 s47,s47,s46
v_exp_f32 v141,v141
v_mfma_f32_16x16x32_bf16 v[216:219],a[160:163],v[144:147],v[40:43]
v_exp_f32 v142,v142
v_mfma_f32_16x16x32_bf16 v[216:219],a[164:167],v[176:179],v[216:219]
v_exp_f32 v143,v143
v_mfma_f32_16x16x32_bf16 v[216:219],a[168:171],v[208:211],v[216:219]
ds_read_b64_tr_b16 a[212:213],v2 offset:8448
ds_read_b64_tr_b16 a[214:215],v2 offset:12544
v_mfma_f32_16x16x32_bf16 v[216:219],a[172:175],v[240:243],v[216:219]
v_cvt_pk_bf16_f32 v137,v138,v139
v_cvt_pk_bf16_f32 v138,v140,v141
v_mfma_f32_16x16x32_bf16 v[220:223],a[188:191],v[240:243],v[40:43]
ds_read_b64_tr_b16 a[220:221],v3 offset:8448
v_cvt_pk_bf16_f32 v139,v142,v143
v_mfma_f32_16x16x32_bf16 v[220:223],a[184:187],v[208:211],v[220:223]
ds_read_b64_tr_b16 a[222:223],v3 offset:12544
v_exp_f32 v152,v152
v_exp_f32 v153,v153
v_exp_f32 v154,v154
v_exp_f32 v155,v155
v_exp_f32 v156,v156
v_exp_f32 v157,v157
v_exp_f32 v158,v158
v_exp_f32 v159,v159
v_mfma_f32_16x16x32_bf16 v[220:223],a[180:183],v[176:179],v[220:223]
ds_read_b64_tr_b16 a[216:217],v3 offset:256
ds_read_b64_tr_b16 a[218:219],v3 offset:4352
v_cvt_pk_bf16_f32 v140,v152,v153
v_cvt_pk_bf16_f32 v141,v154,v155
v_cvt_pk_bf16_f32 v142,v156,v157
v_cvt_pk_bf16_f32 v143,v158,v159
v_mfma_f32_16x16x32_bf16 v[220:223],a[176:179],v[144:147],v[220:223]
s_waitcnt lgkmcnt(6)
ds_read_b64_tr_b16 a[224:225],v2 offset:512
ds_read_b64_tr_b16 a[226:227],v2 offset:4608
v_mfma_f32_16x16x32_bf16 a[0:3],a[192:195],v[136:139],a[0:3]
ds_read_b64_tr_b16 a[228:229],v2 offset:8704
ds_read_b64_tr_b16 a[230:231],v2 offset:12800
v_mfma_f32_16x16x32_bf16 a[0:3],a[196:199],v[140:143],a[0:3]
ds_read_b64_tr_b16 a[236:237],v3 offset:8704
ds_read_b64_tr_b16 a[238:239],v3 offset:12800
v_mfma_f32_16x16x32_bf16 a[4:7],a[204:207],v[140:143],a[4:7]
ds_read_b64_tr_b16 a[232:233],v3 offset:512
ds_read_b64_tr_b16 a[234:235],v3 offset:4608
v_mfma_f32_16x16x32_bf16 a[4:7],a[200:203],v[136:139],a[4:7]
ds_read_b64_tr_b16 a[240:241],v2 offset:768
ds_read_b64_tr_b16 a[242:243],v2 offset:4864
v_mfma_f32_16x16x32_bf16 a[8:11],a[208:211],v[136:139],a[8:11]
s_memrealtime s[76:77]
ds_read_b64_tr_b16 a[244:245],v2 offset:8960
ds_read_b64_tr_b16 a[246:247],v2 offset:13056
s_waitcnt lgkmcnt(15)
v_mfma_f32_16x16x32_bf16 a[8:11],a[212:215],v[140:143],a[8:11]
s_waitcnt lgkmcnt(14)
ds_read_b64_tr_b16 a[252:253],v3 offset:8960
ds_read_b64_tr_b16 a[254:255],v3 offset:13056
v_mfma_f32_16x16x32_bf16 a[12:15],a[220:223],v[140:143],a[12:15]
s_waitcnt lgkmcnt(14)
ds_read_b64_tr_b16 a[248:249],v3 offset:768
ds_read_b64_tr_b16 a[250:251],v3 offset:4864
v_mfma_f32_16x16x32_bf16 a[12:15],a[216:219],v[136:139],a[12:15]
s_waitcnt lgkmcnt(14)
v_mfma_f32_16x16x32_bf16 a[16:19],a[224:227],v[136:139],a[16:19]
s_waitcnt lgkmcnt(12)
v_mfma_f32_16x16x32_bf16 a[16:19],a[228:231],v[140:143],a[16:19]
s_waitcnt lgkmcnt(10)
v_mfma_f32_16x16x32_bf16 a[20:23],a[236:239],v[140:143],a[20:23]
s_waitcnt lgkmcnt(0)
v_mfma_f32_16x16x32_bf16 a[20:23],a[232:235],v[136:139],a[20:23]
s_sub_u32 s79,s78,s76
v_exp_f32 v168,v168
v_mfma_f32_16x16x32_bf16 a[24:27],a[240:243],v[136:139],a[24:27]
s_add_u32 s78,s76,s60
v_exp_f32 v169,v169
v_mfma_f32_16x16x32_bf16 a[24:27],a[244:247],v[140:143],a[24:27]
s_cmp_gt_i32 s79,s95
s_cbranch_scc1 L_ps6_%=
L_pr6_%=:
v_exp_f32 v170,v170
v_exp_f32 v171,v171
v_mfma_f32_16x16x32_bf16 a[28:31],a[252:255],v[140:143],a[28:31]
v_cvt_pk_bf16_f32 v114,v114,v115
v_cvt_pk_bf16_f32 v168,v168,v169
v_exp_f32 v172,v172
v_exp_f32 v173,v173
v_exp_f32 v174,v174
v_exp_f32 v175,v175
v_cvt_pk_bf16_f32 v115,v16,v17
v_cvt_pk_bf16_f32 v169,v170,v171
v_exp_f32 v184,v184
v_exp_f32 v185,v185
v_exp_f32 v186,v186
v_exp_f32 v187,v187
v_exp_f32 v188,v188
v_exp_f32 v189,v189
v_exp_f32 v190,v190
v_exp_f32 v191,v191
v_mfma_f32_16x16x32_bf16 a[28:31],a[248:251],v[136:139],a[28:31]
s_cmp_lg_u64 s[56:57],0
s_cbranch_scc1 L_slow0_%=
L_sret0_%=:
v_cvt_pk_bf16_f32 v170,v172,v173
v_cvt_pk_bf16_f32 v171,v174,v175
v_add_u32 v1,s48,v1
v_cvt_pk_bf16_f32 v172,v184,v185
v_cvt_pk_bf16_f32 v173,v186,v187
v_cvt_pk_bf16_f32 v174,v188,v189
v_cvt_pk_bf16_f32 v175,v190,v191
v_smfmac_f32_16x16x64_bf16 v[48:51],v[28:31],v[136:143],v27
s_mov_b32 s53,11
s_cmp_eq_u32 s40,3
s_cbranch_scc0 L_att1_%=
s_cmp_gt_i32 s18,0
s_cbranch_scc1 L_att1v0_%=
s_cmp_gt_i32 s19,0
s_cbranch_scc1 L_att1v1_%=
s_cmp_gt_i32 s100,0
s_cbranch_scc1 L_att1v2_%=
.p2align 5
L_att1v3_%=:
s_waitcnt vmcnt(8) lgkmcnt(0)
v_exp_f32 v200,v200
s_barrier
s_add_u32 m0,s49,s51
ds_read_b128 a[128:131],v1
s_mov_b32 s90,L_out_end_%=-L_pc_%=+128
v_exp_f32 v201,v201
s_lshr_b32 s45,s45,1
ds_read_b128 a[132:135],v1 offset:256
v_cvt_pk_bf16_f32 v200,v200,v201
buffer_load_dwordx4 v4,s[20:23],s47 offen nt lds
ds_read_b128 a[136:139],v1 offset:512
v_add_u32 v2,s48,v2
v_exp_f32 v202,v202
s_add_u32 s62,s50,32768
ds_read_b128 a[140:143],v1 offset:768
v_add_u32 v3,s48,v3
s_and_b32 s62,s62,131071
v_exp_f32 v203,v203
s_addk_i32 s54,16384
ds_read_b128 a[156:159],v1 offset:4864
v_cvt_pk_bf16_f32 v201,v202,v203
s_sub_u32 s48,s62,s50
v_exp_f32 v204,v204
s_sub_u32 s53,s53,1
v_exp_f32 v205,v205
s_and_b32 s46,s54,0xfffc0000
ds_read_b128 a[152:155],v1 offset:4608
v_cvt_pk_bf16_f32 v202,v204,v205
s_mov_b32 s50,s62
v_exp_f32 v206,v206
s_xor_b32 s49,s50,65536
v_exp_f32 v207,v207
ds_read_b128 a[148:151],v1 offset:4352
v_cvt_pk_bf16_f32 v203,v206,v207
v_mfma_f32_16x16x32_bf16 a[32:35],a[192:195],v[168:171],a[32:35]
v_exp_f32 v216,v216
v_mfma_f32_16x16x32_bf16 a[32:35],a[196:199],v[172:175],a[32:35]
ds_read_b128 a[144:147],v1 offset:4096
v_mfma_f32_16x16x32_bf16 a[36:39],a[204:207],v[172:175],a[36:39]
buffer_load_dwordx4 v4,s[20:23],s47 offen offset:1024 nt lds
v_mfma_f32_16x16x32_bf16 a[36:39],a[200:203],v[168:171],a[36:39]
ds_read_b128 a[160:163],v1 offset:8192
v_mfma_f32_16x16x32_bf16 a[40:43],a[208:211],v[168:171],a[40:43]
v_exp_f32 v217,v217
v_mfma_f32_16x16x32_bf16 a[40:43],a[212:215],v[172:175],a[40:43]
ds_read_b128 a[164:167],v1 offset:8448
v_cvt_pk_bf16_f32 v204,v216,v217
v_mfma_f32_16x16x32_bf16 a[44:47],a[220:223],v[172:175],a[44:47]
v_exp_f32 v218,v218
v_mfma_f32_16x16x32_bf16 a[44:47],a[216:219],v[168:171],a[44:47]
ds_read_b128 a[168:171],v1 offset:8704
v_mfma_f32_16x16x32_bf16 a[48:51],a[224:227],v[168:171],a[48:51]
v_exp_f32 v219,v219
v_mfma_f32_16x16x32_bf16 a[48:51],a[228:231],v[172:175],a[48:51]
ds_read_b128 a[172:175],v1 offset:8960
v_cvt_pk_bf16_f32 v205,v218,v219
v_mfma_f32_16x16x32_bf16 a[52:55],a[236:239],v[172:175],a[52:55]
v_exp_f32 v220,v220
v_mfma_f32_16x16x32_bf16 a[52:55],a[232:235],v[168:171],a[52:55]
ds_read_b128 a[188:191],v1 offset:13056
v_mfma_f32_16x16x32_bf16 a[56:59],a[240:243],v[168:171],a[56:59]
v_exp_f32 v221,v221
v_mfma_f32_16x16x32_bf16 a[56:59],a[244:247],v[172:175],a[56:59]
ds_read_b128 a[184:187],v1 offset:12800
v_cvt_pk_bf16_f32 v206,v220,v221
v_mfma_f32_16x16x32_bf16 a[60:63],a[252:255],v[172:175],a[60:63]
v_exp_f32 v222,v222
v_mfma_f32_16x16x32_bf16 a[60:63],a[248:251],v[168:171],a[60:63]
ds_read_b128 a[180:183],v1 offset:12544
v_smfmac_f32_16x16x64_bf16 v[52:55],v[28:31],v[168:175],v27
s_waitcnt lgkmcnt(4)
v_mfma_f32_16x16x32_bf16 v[136:139],a[128:131],v[128:131],v[32:35]
buffer_load_dwordx4 v5,s[20:23],s47 offen offset:2048 nt lds
v_mfma_f32_16x16x32_bf16 v[136:139],a[132:135],v[160:163],v[136:139]
ds_read_b128 a[176:179],v1 offset:12288
v_add_u32 v1,s48,v1
v_mfma_f32_16x16x32_bf16 v[136:139],a[136:139],v[192:195],v[136:139]
v_exp_f32 v223,v223
v_mfma_f32_16x16x32_bf16 v[136:139],a[140:143],v[224:227],v[136:139]
v_cvt_pk_bf16_f32 v207,v222,v223
v_mfma_f32_16x16x32_bf16 v[140:143],a[156:159],v[224:227],v[32:35]
v_mfma_f32_16x16x32_bf16 v[140:143],a[152:155],v[192:195],v[140:143]
v_mfma_f32_16x16x32_bf16 v[140:143],a[148:151],v[160:163],v[140:143]
v_mfma_f32_16x16x32_bf16 v[140:143],a[144:147],v[128:131],v[140:143]
v_mfma_f32_16x16x32_bf16 v[152:155],a[160:163],v[128:131],v[32:35]
v_mfma_f32_16x16x32_bf16 v[152:155],a[164:167],v[160:163],v[152:155]
v_mfma_f32_16x16x32_bf16 v[152:155],a[168:171],v[192:195],v[152:155]
s_waitcnt lgkmcnt(0)
)A"
R"A(v_mfma_f32_16x16x32_bf16 v[152:155],a[172:175],v[224:227],v[152:155]
v_mfma_f32_16x16x32_bf16 v[156:159],a[188:191],v[224:227],v[32:35]
v_mfma_f32_16x16x32_bf16 v[156:159],a[184:187],v[192:195],v[156:159]
buffer_load_dwordx4 v5,s[20:23],s47 offen offset:3072 nt lds
v_mfma_f32_16x16x32_bf16 v[156:159],a[180:183],v[160:163],v[156:159]
v_mfma_f32_16x16x32_bf16 v[156:159],a[176:179],v[128:131],v[156:159]
v_mfma_f32_16x16x32_bf16 a[64:67],a[192:195],v[200:203],a[64:67]
v_mfma_f32_16x16x32_bf16 a[64:67],a[196:199],v[204:207],a[64:67]
v_mfma_f32_16x16x32_bf16 a[68:71],a[204:207],v[204:207],a[68:71]
v_mfma_f32_16x16x32_bf16 a[68:71],a[200:203],v[200:203],a[68:71]
v_mfma_f32_16x16x32_bf16 a[72:75],a[208:211],v[200:203],a[72:75]
v_mfma_f32_16x16x32_bf16 a[72:75],a[212:215],v[204:207],a[72:75]
v_exp_f32 v136,v136
v_mfma_f32_16x16x32_bf16 a[76:79],a[220:223],v[204:207],a[76:79]
v_exp_f32 v137,v137
v_mfma_f32_16x16x32_bf16 a[76:79],a[216:219],v[200:203],a[76:79]
v_exp_f32 v138,v138
v_mfma_f32_16x16x32_bf16 a[80:83],a[224:227],v[200:203],a[80:83]
s_addk_i32 m0,16384
v_exp_f32 v139,v139
v_mfma_f32_16x16x32_bf16 a[80:83],a[228:231],v[204:207],a[80:83]
v_exp_f32 v140,v140
v_mfma_f32_16x16x32_bf16 a[84:87],a[236:239],v[204:207],a[84:87]
v_exp_f32 v141,v141
v_mfma_f32_16x16x32_bf16 a[84:87],a[232:235],v[200:203],a[84:87]
buffer_load_dwordx4 v4,s[24:27],s47 offen nt lds
v_mfma_f32_16x16x32_bf16 a[88:91],a[240:243],v[200:203],a[88:91]
v_exp_f32 v142,v142
v_mfma_f32_16x16x32_bf16 a[88:91],a[244:247],v[204:207],a[88:91]
v_exp_f32 v143,v143
v_mfma_f32_16x16x32_bf16 a[92:95],a[252:255],v[204:207],a[92:95]
v_mfma_f32_16x16x32_bf16 a[92:95],a[248:251],v[200:203],a[92:95]
v_smfmac_f32_16x16x64_bf16 v[56:59],v[28:31],v[200:207],v27
v_mfma_f32_16x16x32_bf16 v[168:171],a[128:131],v[132:135],v[36:39]
v_mfma_f32_16x16x32_bf16 v[168:171],a[132:135],v[164:167],v[168:171]
v_mfma_f32_16x16x32_bf16 v[168:171],a[136:139],v[196:199],v[168:171]
v_exp_f32 v152,v152
v_mfma_f32_16x16x32_bf16 v[168:171],a[140:143],v[228:231],v[168:171]
v_exp_f32 v153,v153
v_mfma_f32_16x16x32_bf16 v[172:175],a[156:159],v[228:231],v[36:39]
v_exp_f32 v154,v154
v_mfma_f32_16x16x32_bf16 v[172:175],a[152:155],v[196:199],v[172:175]
v_exp_f32 v155,v155
v_mfma_f32_16x16x32_bf16 v[172:175],a[148:151],v[164:167],v[172:175]
v_exp_f32 v156,v156
v_mfma_f32_16x16x32_bf16 v[172:175],a[144:147],v[132:135],v[172:175]
v_exp_f32 v157,v157
v_mfma_f32_16x16x32_bf16 v[184:187],a[160:163],v[132:135],v[36:39]
buffer_load_dwordx4 v5,s[24:27],s47 offen offset:1024 nt lds
v_mfma_f32_16x16x32_bf16 v[184:187],a[164:167],v[164:167],v[184:187]
v_exp_f32 v158,v158
v_mfma_f32_16x16x32_bf16 v[184:187],a[168:171],v[196:199],v[184:187]
v_exp_f32 v159,v159
v_mfma_f32_16x16x32_bf16 v[184:187],a[172:175],v[228:231],v[184:187]
v_exp_f32 v168,v168
v_mfma_f32_16x16x32_bf16 v[188:191],a[188:191],v[228:231],v[36:39]
v_exp_f32 v169,v169
v_mfma_f32_16x16x32_bf16 v[188:191],a[184:187],v[196:199],v[188:191]
v_exp_f32 v170,v170
v_mfma_f32_16x16x32_bf16 v[188:191],a[180:183],v[164:167],v[188:191]
v_exp_f32 v171,v171
v_mfma_f32_16x16x32_bf16 v[188:191],a[176:179],v[132:135],v[188:191]
v_exp_f32 v172,v172
ds_read_b64_tr_b16 a[192:193],v2
v_cvt_pk_bf16_f32 v168,v168,v169
ds_read_b64_tr_b16 a[194:195],v2 offset:4096
v_cvt_pk_bf16_f32 v169,v170,v171
v_exp_f32 v173,v173
ds_read_b64_tr_b16 a[196:197],v2 offset:8192
v_cvt_pk_bf16_f32 v170,v172,v173
v_exp_f32 v174,v174
v_exp_f32 v175,v175
buffer_load_dwordx4 v4,s[24:27],s47 offen offset:2048 nt lds
ds_read_b64_tr_b16 a[198:199],v2 offset:12288
v_cvt_pk_bf16_f32 v171,v174,v175
v_exp_f32 v184,v184
v_exp_f32 v185,v185
ds_read_b64_tr_b16 a[204:205],v3 offset:8192
v_cvt_pk_bf16_f32 v136,v136,v137
ds_read_b64_tr_b16 a[206:207],v3 offset:12288
v_cvt_pk_bf16_f32 v172,v184,v185
ds_read_b64_tr_b16 a[200:201],v3
ds_read_b64_tr_b16 a[202:203],v3 offset:4096
v_cvt_pk_bf16_f32 v137,v138,v139
ds_read_b64_tr_b16 a[208:209],v2 offset:256
ds_read_b64_tr_b16 a[210:211],v2 offset:4352
ds_read_b64_tr_b16 a[212:213],v2 offset:8448
v_mfma_f32_16x16x32_bf16 v[200:203],a[128:131],v[144:147],v[40:43]
ds_read_b64_tr_b16 a[214:215],v2 offset:12544
v_cvt_pk_bf16_f32 v138,v140,v141
v_mfma_f32_16x16x32_bf16 v[200:203],a[132:135],v[176:179],v[200:203]
ds_read_b64_tr_b16 a[220:221],v3 offset:8448
v_mfma_f32_16x16x32_bf16 v[200:203],a[136:139],v[208:211],v[200:203]
ds_read_b64_tr_b16 a[222:223],v3 offset:12544
v_cvt_pk_bf16_f32 v139,v142,v143
v_mfma_f32_16x16x32_bf16 v[200:203],a[140:143],v[240:243],v[200:203]
buffer_load_dwordx4 v5,s[24:27],s47 offen offset:3072 nt lds
v_mfma_f32_16x16x32_bf16 v[204:207],a[156:159],v[240:243],v[40:43]
s_or_b32 s47,s47,0x18000
s_addk_i32 s47,4096
ds_read_b64_tr_b16 a[216:217],v3 offset:256
v_mfma_f32_16x16x32_bf16 v[204:207],a[152:155],v[208:211],v[204:207]
s_and_b32 s47,s47,0x27000
ds_read_b64_tr_b16 a[218:219],v3 offset:4352
v_cvt_pk_bf16_f32 v140,v152,v153
v_mfma_f32_16x16x32_bf16 v[204:207],a[148:151],v[176:179],v[204:207]
s_or_b32 s47,s47,s46
ds_read_b64_tr_b16 a[224:225],v2 offset:512
v_mfma_f32_16x16x32_bf16 v[204:207],a[144:147],v[144:147],v[204:207]
ds_read_b64_tr_b16 a[226:227],v2 offset:4608
v_mfma_f32_16x16x32_bf16 v[216:219],a[160:163],v[144:147],v[40:43]
ds_read_b64_tr_b16 a[228:229],v2 offset:8704
v_cvt_pk_bf16_f32 v141,v154,v155
v_mfma_f32_16x16x32_bf16 v[216:219],a[164:167],v[176:179],v[216:219]
ds_read_b64_tr_b16 a[230:231],v2 offset:12800
v_mfma_f32_16x16x32_bf16 v[216:219],a[168:171],v[208:211],v[216:219]
ds_read_b64_tr_b16 a[236:237],v3 offset:8704
v_mfma_f32_16x16x32_bf16 v[216:219],a[172:175],v[240:243],v[216:219]
ds_read_b64_tr_b16 a[238:239],v3 offset:12800
v_cvt_pk_bf16_f32 v142,v156,v157
v_mfma_f32_16x16x32_bf16 v[220:223],a[188:191],v[240:243],v[40:43]
ds_read_b64_tr_b16 a[232:233],v3 offset:512
v_mfma_f32_16x16x32_bf16 v[220:223],a[184:187],v[208:211],v[220:223]
ds_read_b64_tr_b16 a[234:235],v3 offset:4608
v_mfma_f32_16x16x32_bf16 v[220:223],a[180:183],v[176:179],v[220:223]
ds_read_b64_tr_b16 a[240:241],v2 offset:768
v_cvt_pk_bf16_f32 v143,v158,v159
v_mfma_f32_16x16x32_bf16 v[220:223],a[176:179],v[144:147],v[220:223]
s_waitcnt lgkmcnt(7)
buffer_load_dword v26,v24,s[88:91],s93 offen
v_mfma_f32_16x16x32_bf16 a[0:3],a[192:195],v[136:139],a[0:3]
ds_read_b64_tr_b16 a[242:243],v2 offset:4864
v_mfma_f32_16x16x32_bf16 a[0:3],a[196:199],v[140:143],a[0:3]
ds_read_b64_tr_b16 a[244:245],v2 offset:8960
v_mfma_f32_16x16x32_bf16 a[4:7],a[204:207],v[140:143],a[4:7]
ds_read_b64_tr_b16 a[246:247],v2 offset:13056
v_mfma_f32_16x16x32_bf16 a[4:7],a[200:203],v[136:139],a[4:7]
ds_read_b64_tr_b16 a[252:253],v3 offset:8960
v_mfma_f32_16x16x32_bf16 a[8:11],a[208:211],v[136:139],a[8:11]
ds_read_b64_tr_b16 a[254:255],v3 offset:13056
v_mfma_f32_16x16x32_bf16 a[8:11],a[212:215],v[140:143],a[8:11]
ds_read_b64_tr_b16 a[248:249],v3 offset:768
v_mfma_f32_16x16x32_bf16 a[12:15],a[220:223],v[140:143],a[12:15]
ds_read_b64_tr_b16 a[250:251],v3 offset:4864
v_mfma_f32_16x16x32_bf16 a[12:15],a[216:219],v[136:139],a[12:15]
s_bitcmp1_b32 s45,0
s_cbranch_scc0 L_t31v31_%=
s_mov_b32 m0,s80
s_addk_i32 s80,4096
buffer_load_dwordx4 v25,s[24:27],s83 offen nt lds
s_addk_i32 s83,1024
L_t31v31_%=:
v_mfma_f32_16x16x32_bf16 a[16:19],a[224:227],v[136:139],a[16:19]
s_waitcnt lgkmcnt(6)
v_exp_f32 v186,v186
v_mfma_f32_16x16x32_bf16 a[16:19],a[228:231],v[140:143],a[16:19]
v_exp_f32 v187,v187
v_mfma_f32_16x16x32_bf16 a[20:23],a[236:239],v[140:143],a[20:23]
v_exp_f32 v188,v188
v_mfma_f32_16x16x32_bf16 a[20:23],a[232:235],v[136:139],a[20:23]
v_exp_f32 v189,v189
v_mfma_f32_16x16x32_bf16 a[24:27],a[240:243],v[136:139],a[24:27]
s_waitcnt lgkmcnt(2)
v_exp_f32 v190,v190
v_mfma_f32_16x16x32_bf16 a[24:27],a[244:247],v[140:143],a[24:27]
v_cvt_pk_bf16_f32 v173,v186,v187
v_mfma_f32_16x16x32_bf16 a[28:31],a[252:255],v[140:143],a[28:31]
s_waitcnt lgkmcnt(0)
v_exp_f32 v191,v191
v_mfma_f32_16x16x32_bf16 a[28:31],a[248:251],v[136:139],a[28:31]
v_cvt_pk_bf16_f32 v174,v188,v189
v_cvt_pk_bf16_f32 v175,v190,v191
s_cmp_lg_u32 s53,0
v_smfmac_f32_16x16x64_bf16 v[48:51],v[28:31],v[136:143],v27
s_cbranch_scc1 L_att1v3_%=
s_waitcnt lgkmcnt(0)
s_nop 7
s_branch L_att1x_%=
.p2align 5
L_att1v0_%=:
v_mfma_f32_16x16x32_bf16 v[232:235],a[128:131],v[148:151],v[44:47]
s_waitcnt vmcnt(8) lgkmcnt(0)
v_exp_f32 v200,v200
v_mfma_f32_16x16x32_bf16 v[232:235],a[132:135],v[180:183],v[232:235]
s_barrier
s_add_u32 m0,s49,s51
ds_read_b128 a[128:131],v1
v_mfma_f32_16x16x32_bf16 v[232:235],a[136:139],v[212:215],v[232:235]
s_mov_b32 s90,L_out_end_%=-L_pc_%=+128
v_exp_f32 v201,v201
v_mfma_f32_16x16x32_bf16 v[232:235],a[140:143],v[244:247],v[232:235]
s_lshr_b32 s45,s45,1
ds_read_b128 a[132:135],v1 offset:256
v_cvt_pk_bf16_f32 v200,v200,v201
v_mfma_f32_16x16x32_bf16 v[236:239],a[156:159],v[244:247],v[44:47]
buffer_load_dwordx4 v4,s[20:23],s47 offen nt lds
v_mfma_f32_16x16x32_bf16 v[236:239],a[152:155],v[212:215],v[236:239]
ds_read_b128 a[136:139],v1 offset:512
v_add_u32 v2,s48,v2
v_mfma_f32_16x16x32_bf16 v[236:239],a[148:151],v[180:183],v[236:239]
v_exp_f32 v202,v202
v_mfma_f32_16x16x32_bf16 v[236:239],a[144:147],v[148:151],v[236:239]
s_add_u32 s62,s50,32768
ds_read_b128 a[140:143],v1 offset:768
v_add_u32 v3,s48,v3
v_mfma_f32_16x16x32_bf16 v[248:251],a[160:163],v[148:151],v[44:47]
s_and_b32 s62,s62,131071
v_exp_f32 v203,v203
v_mfma_f32_16x16x32_bf16 v[248:251],a[164:167],v[180:183],v[248:251]
s_addk_i32 s54,16384
ds_read_b128 a[156:159],v1 offset:4864
v_cvt_pk_bf16_f32 v201,v202,v203
v_mfma_f32_16x16x32_bf16 v[248:251],a[168:171],v[212:215],v[248:251]
s_sub_u32 s48,s62,s50
v_exp_f32 v204,v204
v_mfma_f32_16x16x32_bf16 v[248:251],a[172:175],v[244:247],v[248:251]
s_sub_u32 s53,s53,1
v_exp_f32 v205,v205
v_mfma_f32_16x16x32_bf16 v[252:255],a[188:191],v[244:247],v[44:47]
s_and_b32 s46,s54,0xfffc0000
ds_read_b128 a[152:155],v1 offset:4608
v_cvt_pk_bf16_f32 v202,v204,v205
v_mfma_f32_16x16x32_bf16 v[252:255],a[184:187],v[212:215],v[252:255]
s_mov_b32 s50,s62
v_exp_f32 v206,v206
v_mfma_f32_16x16x32_bf16 v[252:255],a[180:183],v[180:183],v[252:255]
s_xor_b32 s49,s50,65536
v_exp_f32 v207,v207
v_mfma_f32_16x16x32_bf16 v[252:255],a[176:179],v[148:151],v[252:255]
ds_read_b128 a[148:151],v1 offset:4352
v_cvt_pk_bf16_f32 v203,v206,v207
v_mfma_f32_16x16x32_bf16 a[32:35],a[192:195],v[168:171],a[32:35]
v_exp_f32 v216,v216
v_mfma_f32_16x16x32_bf16 a[32:35],a[196:199],v[172:175],a[32:35]
ds_read_b128 a[144:147],v1 offset:4096
v_mfma_f32_16x16x32_bf16 a[36:39],a[204:207],v[172:175],a[36:39]
buffer_load_dwordx4 v4,s[20:23],s47 offen offset:1024 nt lds
v_mfma_f32_16x16x32_bf16 a[36:39],a[200:203],v[168:171],a[36:39]
ds_read_b128 a[160:163],v1 offset:8192
v_mfma_f32_16x16x32_bf16 a[40:43],a[208:211],v[168:171],a[40:43]
v_exp_f32 v217,v217
v_mfma_f32_16x16x32_bf16 a[40:43],a[212:215],v[172:175],a[40:43]
ds_read_b128 a[164:167],v1 offset:8448
v_cvt_pk_bf16_f32 v204,v216,v217
v_mfma_f32_16x16x32_bf16 a[44:47],a[220:223],v[172:175],a[44:47]
v_exp_f32 v218,v218
v_mfma_f32_16x16x32_bf16 a[44:47],a[216:219],v[168:171],a[44:47]
ds_read_b128 a[168:171],v1 offset:8704
v_mfma_f32_16x16x32_bf16 a[48:51],a[224:227],v[168:171],a[48:51]
v_exp_f32 v219,v219
v_mfma_f32_16x16x32_bf16 a[48:51],a[228:231],v[172:175],a[48:51]
ds_read_b128 a[172:175],v1 offset:8960
v_cvt_pk_bf16_f32 v205,v218,v219
v_mfma_f32_16x16x32_bf16 a[52:55],a[236:239],v[172:175],a[52:55]
v_exp_f32 v220,v220
v_mfma_f32_16x16x32_bf16 a[52:55],a[232:235],v[168:171],a[52:55]
ds_read_b128 a[188:191],v1 offset:13056
v_mfma_f32_16x16x32_bf16 a[56:59],a[240:243],v[168:171],a[56:59]
v_exp_f32 v221,v221
v_mfma_f32_16x16x32_bf16 a[56:59],a[244:247],v[172:175],a[56:59]
ds_read_b128 a[184:187],v1 offset:12800
v_cvt_pk_bf16_f32 v206,v220,v221
v_mfma_f32_16x16x32_bf16 a[60:63],a[252:255],v[172:175],a[60:63]
v_exp_f32 v222,v222
v_mfma_f32_16x16x32_bf16 a[60:63],a[248:251],v[168:171],a[60:63]
ds_read_b128 a[180:183],v1 offset:12544
v_smfmac_f32_16x16x64_bf16 v[52:55],v[28:31],v[168:175],v27
buffer_load_dwordx4 v5,s[20:23],s47 offen offset:2048 nt lds
ds_read_b128 a[176:179],v1 offset:12288
v_add_u32 v1,s48,v1
v_exp_f32 v223,v223
)A"
R"A(s_nop 0
v_cvt_pk_bf16_f32 v207,v222,v223
v_exp_f32 v232,v232
v_exp_f32 v233,v233
v_exp_f32 v234,v234
v_exp_f32 v235,v235
v_exp_f32 v236,v236
v_exp_f32 v237,v237
v_exp_f32 v238,v238
v_exp_f32 v239,v239
v_exp_f32 v248,v248
buffer_load_dwordx4 v5,s[20:23],s47 offen offset:3072 nt lds
v_exp_f32 v249,v249
v_exp_f32 v250,v250
v_mfma_f32_16x16x32_bf16 a[64:67],a[192:195],v[200:203],a[64:67]
v_exp_f32 v251,v251
v_mfma_f32_16x16x32_bf16 a[64:67],a[196:199],v[204:207],a[64:67]
v_exp_f32 v252,v252
v_mfma_f32_16x16x32_bf16 a[68:71],a[204:207],v[204:207],a[68:71]
v_exp_f32 v253,v253
v_mfma_f32_16x16x32_bf16 a[68:71],a[200:203],v[200:203],a[68:71]
v_exp_f32 v254,v254
v_mfma_f32_16x16x32_bf16 a[72:75],a[208:211],v[200:203],a[72:75]
v_exp_f32 v255,v255
v_mfma_f32_16x16x32_bf16 a[72:75],a[212:215],v[204:207],a[72:75]
v_mfma_f32_16x16x32_bf16 a[76:79],a[220:223],v[204:207],a[76:79]
v_mfma_f32_16x16x32_bf16 a[76:79],a[216:219],v[200:203],a[76:79]
v_mfma_f32_16x16x32_bf16 a[80:83],a[224:227],v[200:203],a[80:83]
s_addk_i32 m0,16384
v_mfma_f32_16x16x32_bf16 a[80:83],a[228:231],v[204:207],a[80:83]
v_mfma_f32_16x16x32_bf16 a[84:87],a[236:239],v[204:207],a[84:87]
v_mfma_f32_16x16x32_bf16 a[84:87],a[232:235],v[200:203],a[84:87]
buffer_load_dwordx4 v4,s[24:27],s47 offen nt lds
v_mfma_f32_16x16x32_bf16 a[88:91],a[240:243],v[200:203],a[88:91]
v_mfma_f32_16x16x32_bf16 a[88:91],a[244:247],v[204:207],a[88:91]
v_mfma_f32_16x16x32_bf16 a[92:95],a[252:255],v[204:207],a[92:95]
v_cvt_pk_bf16_f32 v232,v232,v233
v_cvt_pk_bf16_f32 v233,v234,v235
v_mfma_f32_16x16x32_bf16 a[92:95],a[248:251],v[200:203],a[92:95]
v_cvt_pk_bf16_f32 v234,v236,v237
v_cvt_pk_bf16_f32 v235,v238,v239
v_smfmac_f32_16x16x64_bf16 v[56:59],v[28:31],v[200:207],v27
s_waitcnt lgkmcnt(0)
v_cvt_pk_bf16_f32 v236,v248,v249
v_mfma_f32_16x16x32_bf16 v[168:171],a[128:131],v[132:135],v[36:39]
v_cvt_pk_bf16_f32 v237,v250,v251
v_cvt_pk_bf16_f32 v238,v252,v253
v_mfma_f32_16x16x32_bf16 v[168:171],a[132:135],v[164:167],v[168:171]
v_cvt_pk_bf16_f32 v239,v254,v255
v_mfma_f32_16x16x32_bf16 v[168:171],a[136:139],v[196:199],v[168:171]
v_mfma_f32_16x16x32_bf16 v[168:171],a[140:143],v[228:231],v[168:171]
v_mfma_f32_16x16x32_bf16 v[172:175],a[156:159],v[228:231],v[36:39]
v_mfma_f32_16x16x32_bf16 v[172:175],a[152:155],v[196:199],v[172:175]
v_mfma_f32_16x16x32_bf16 v[172:175],a[148:151],v[164:167],v[172:175]
v_mfma_f32_16x16x32_bf16 v[172:175],a[144:147],v[132:135],v[172:175]
v_mfma_f32_16x16x32_bf16 v[184:187],a[160:163],v[132:135],v[36:39]
buffer_load_dwordx4 v5,s[24:27],s47 offen offset:1024 nt lds
v_mfma_f32_16x16x32_bf16 v[184:187],a[164:167],v[164:167],v[184:187]
v_mfma_f32_16x16x32_bf16 v[184:187],a[168:171],v[196:199],v[184:187]
v_mfma_f32_16x16x32_bf16 v[184:187],a[172:175],v[228:231],v[184:187]
v_exp_f32 v168,v168
v_mfma_f32_16x16x32_bf16 v[188:191],a[188:191],v[228:231],v[36:39]
v_exp_f32 v169,v169
v_mfma_f32_16x16x32_bf16 v[188:191],a[184:187],v[196:199],v[188:191]
v_exp_f32 v170,v170
v_mfma_f32_16x16x32_bf16 v[188:191],a[180:183],v[164:167],v[188:191]
v_exp_f32 v171,v171
v_mfma_f32_16x16x32_bf16 v[188:191],a[176:179],v[132:135],v[188:191]
v_exp_f32 v172,v172
v_mfma_f32_16x16x32_bf16 a[96:99],a[192:195],v[232:235],a[96:99]
ds_read_b64_tr_b16 a[192:193],v2
v_cvt_pk_bf16_f32 v168,v168,v169
v_mfma_f32_16x16x32_bf16 a[96:99],a[196:199],v[236:239],a[96:99]
ds_read_b64_tr_b16 a[194:195],v2 offset:4096
v_cvt_pk_bf16_f32 v169,v170,v171
v_mfma_f32_16x16x32_bf16 a[100:103],a[204:207],v[236:239],a[100:103]
v_exp_f32 v173,v173
v_mfma_f32_16x16x32_bf16 a[100:103],a[200:203],v[232:235],a[100:103]
ds_read_b64_tr_b16 a[196:197],v2 offset:8192
v_cvt_pk_bf16_f32 v170,v172,v173
v_mfma_f32_16x16x32_bf16 a[104:107],a[208:211],v[232:235],a[104:107]
v_exp_f32 v174,v174
v_mfma_f32_16x16x32_bf16 a[104:107],a[212:215],v[236:239],a[104:107]
v_exp_f32 v175,v175
v_mfma_f32_16x16x32_bf16 a[108:111],a[220:223],v[236:239],a[108:111]
buffer_load_dwordx4 v4,s[24:27],s47 offen offset:2048 nt lds
v_mfma_f32_16x16x32_bf16 a[108:111],a[216:219],v[232:235],a[108:111]
ds_read_b64_tr_b16 a[198:199],v2 offset:12288
v_cvt_pk_bf16_f32 v171,v174,v175
v_mfma_f32_16x16x32_bf16 a[112:115],a[224:227],v[232:235],a[112:115]
v_exp_f32 v184,v184
v_mfma_f32_16x16x32_bf16 a[112:115],a[228:231],v[236:239],a[112:115]
v_exp_f32 v185,v185
v_mfma_f32_16x16x32_bf16 a[116:119],a[236:239],v[236:239],a[116:119]
ds_read_b64_tr_b16 a[204:205],v3 offset:8192
v_mfma_f32_16x16x32_bf16 a[116:119],a[232:235],v[232:235],a[116:119]
ds_read_b64_tr_b16 a[206:207],v3 offset:12288
v_cvt_pk_bf16_f32 v172,v184,v185
v_mfma_f32_16x16x32_bf16 a[120:123],a[240:243],v[232:235],a[120:123]
ds_read_b64_tr_b16 a[200:201],v3
v_mfma_f32_16x16x32_bf16 a[120:123],a[244:247],v[236:239],a[120:123]
ds_read_b64_tr_b16 a[202:203],v3 offset:4096
v_mfma_f32_16x16x32_bf16 a[124:127],a[252:255],v[236:239],a[124:127]
ds_read_b64_tr_b16 a[208:209],v2 offset:256
v_mfma_f32_16x16x32_bf16 a[124:127],a[248:251],v[232:235],a[124:127]
ds_read_b64_tr_b16 a[210:211],v2 offset:4352
v_smfmac_f32_16x16x64_bf16 v[60:63],v[28:31],v[232:239],v27
ds_read_b64_tr_b16 a[212:213],v2 offset:8448
v_mfma_f32_16x16x32_bf16 v[200:203],a[128:131],v[144:147],v[40:43]
ds_read_b64_tr_b16 a[214:215],v2 offset:12544
v_mfma_f32_16x16x32_bf16 v[200:203],a[132:135],v[176:179],v[200:203]
ds_read_b64_tr_b16 a[220:221],v3 offset:8448
v_mfma_f32_16x16x32_bf16 v[200:203],a[136:139],v[208:211],v[200:203]
ds_read_b64_tr_b16 a[222:223],v3 offset:12544
v_mfma_f32_16x16x32_bf16 v[200:203],a[140:143],v[240:243],v[200:203]
buffer_load_dwordx4 v5,s[24:27],s47 offen offset:3072 nt lds
v_mfma_f32_16x16x32_bf16 v[204:207],a[156:159],v[240:243],v[40:43]
s_or_b32 s47,s47,0x18000
s_addk_i32 s47,4096
ds_read_b64_tr_b16 a[216:217],v3 offset:256
v_mfma_f32_16x16x32_bf16 v[204:207],a[152:155],v[208:211],v[204:207]
s_and_b32 s47,s47,0x27000
ds_read_b64_tr_b16 a[218:219],v3 offset:4352
v_mfma_f32_16x16x32_bf16 v[204:207],a[148:151],v[176:179],v[204:207]
s_or_b32 s47,s47,s46
ds_read_b64_tr_b16 a[224:225],v2 offset:512
v_mfma_f32_16x16x32_bf16 v[204:207],a[144:147],v[144:147],v[204:207]
ds_read_b64_tr_b16 a[226:227],v2 offset:4608
v_mfma_f32_16x16x32_bf16 v[216:219],a[160:163],v[144:147],v[40:43]
ds_read_b64_tr_b16 a[228:229],v2 offset:8704
v_mfma_f32_16x16x32_bf16 v[216:219],a[164:167],v[176:179],v[216:219]
ds_read_b64_tr_b16 a[230:231],v2 offset:12800
v_mfma_f32_16x16x32_bf16 v[216:219],a[168:171],v[208:211],v[216:219]
ds_read_b64_tr_b16 a[236:237],v3 offset:8704
v_mfma_f32_16x16x32_bf16 v[216:219],a[172:175],v[240:243],v[216:219]
ds_read_b64_tr_b16 a[238:239],v3 offset:12800
v_mfma_f32_16x16x32_bf16 v[220:223],a[188:191],v[240:243],v[40:43]
ds_read_b64_tr_b16 a[232:233],v3 offset:512
v_mfma_f32_16x16x32_bf16 v[220:223],a[184:187],v[208:211],v[220:223]
ds_read_b64_tr_b16 a[234:235],v3 offset:4608
v_mfma_f32_16x16x32_bf16 v[220:223],a[180:183],v[176:179],v[220:223]
ds_read_b64_tr_b16 a[240:241],v2 offset:768
v_mfma_f32_16x16x32_bf16 v[220:223],a[176:179],v[144:147],v[220:223]
buffer_load_dword v26,v24,s[88:91],s93 offen
ds_read_b64_tr_b16 a[242:243],v2 offset:4864
ds_read_b64_tr_b16 a[244:245],v2 offset:8960
ds_read_b64_tr_b16 a[246:247],v2 offset:13056
ds_read_b64_tr_b16 a[252:253],v3 offset:8960
ds_read_b64_tr_b16 a[254:255],v3 offset:13056
ds_read_b64_tr_b16 a[248:249],v3 offset:768
ds_read_b64_tr_b16 a[250:251],v3 offset:4864
s_bitcmp1_b32 s45,0
s_cbranch_scc0 L_t31v01_%=
s_mov_b32 m0,s80
s_addk_i32 s80,4096
buffer_load_dwordx4 v25,s[24:27],s83 offen nt lds
s_addk_i32 s83,1024
L_t31v01_%=:
v_exp_f32 v186,v186
v_exp_f32 v187,v187
v_exp_f32 v188,v188
v_exp_f32 v189,v189
v_exp_f32 v190,v190
v_cvt_pk_bf16_f32 v173,v186,v187
v_exp_f32 v191,v191
v_cvt_pk_bf16_f32 v174,v188,v189
v_cvt_pk_bf16_f32 v175,v190,v191
s_cmp_lg_u32 s53,0
s_cbranch_scc1 L_att1v0_%=
s_waitcnt lgkmcnt(0)
s_nop 7
s_branch L_att1x_%=
.p2align 5
L_att1v1_%=:
v_mfma_f32_16x16x32_bf16 v[232:235],a[128:131],v[148:151],v[44:47]
s_waitcnt vmcnt(8) lgkmcnt(0)
v_exp_f32 v200,v200
v_mfma_f32_16x16x32_bf16 v[232:235],a[132:135],v[180:183],v[232:235]
s_barrier
s_add_u32 m0,s49,s51
ds_read_b128 a[128:131],v1
v_mfma_f32_16x16x32_bf16 v[232:235],a[136:139],v[212:215],v[232:235]
s_mov_b32 s90,L_out_end_%=-L_pc_%=+128
v_exp_f32 v201,v201
v_mfma_f32_16x16x32_bf16 v[232:235],a[140:143],v[244:247],v[232:235]
s_lshr_b32 s45,s45,1
ds_read_b128 a[132:135],v1 offset:256
v_cvt_pk_bf16_f32 v200,v200,v201
v_mfma_f32_16x16x32_bf16 v[236:239],a[156:159],v[244:247],v[44:47]
buffer_load_dwordx4 v4,s[20:23],s47 offen nt lds
v_mfma_f32_16x16x32_bf16 v[236:239],a[152:155],v[212:215],v[236:239]
ds_read_b128 a[136:139],v1 offset:512
v_add_u32 v2,s48,v2
v_mfma_f32_16x16x32_bf16 v[236:239],a[148:151],v[180:183],v[236:239]
v_exp_f32 v202,v202
v_mfma_f32_16x16x32_bf16 v[236:239],a[144:147],v[148:151],v[236:239]
s_add_u32 s62,s50,32768
ds_read_b128 a[140:143],v1 offset:768
v_add_u32 v3,s48,v3
v_mfma_f32_16x16x32_bf16 v[248:251],a[160:163],v[148:151],v[44:47]
s_and_b32 s62,s62,131071
v_exp_f32 v203,v203
v_mfma_f32_16x16x32_bf16 v[248:251],a[164:167],v[180:183],v[248:251]
s_addk_i32 s54,16384
ds_read_b128 a[156:159],v1 offset:4864
v_cvt_pk_bf16_f32 v201,v202,v203
v_mfma_f32_16x16x32_bf16 v[248:251],a[168:171],v[212:215],v[248:251]
s_sub_u32 s48,s62,s50
v_exp_f32 v204,v204
v_mfma_f32_16x16x32_bf16 v[248:251],a[172:175],v[244:247],v[248:251]
s_sub_u32 s53,s53,1
v_exp_f32 v205,v205
v_mfma_f32_16x16x32_bf16 v[252:255],a[188:191],v[244:247],v[44:47]
s_and_b32 s46,s54,0xfffc0000
ds_read_b128 a[152:155],v1 offset:4608
v_cvt_pk_bf16_f32 v202,v204,v205
v_mfma_f32_16x16x32_bf16 v[252:255],a[184:187],v[212:215],v[252:255]
s_mov_b32 s50,s62
v_exp_f32 v206,v206
v_mfma_f32_16x16x32_bf16 v[252:255],a[180:183],v[180:183],v[252:255]
s_xor_b32 s49,s50,65536
v_exp_f32 v207,v207
v_mfma_f32_16x16x32_bf16 v[252:255],a[176:179],v[148:151],v[252:255]
ds_read_b128 a[148:151],v1 offset:4352
v_cvt_pk_bf16_f32 v203,v206,v207
v_exp_f32 v216,v216
ds_read_b128 a[144:147],v1 offset:4096
buffer_load_dwordx4 v4,s[20:23],s47 offen offset:1024 nt lds
ds_read_b128 a[160:163],v1 offset:8192
v_exp_f32 v217,v217
ds_read_b128 a[164:167],v1 offset:8448
v_cvt_pk_bf16_f32 v204,v216,v217
v_exp_f32 v218,v218
ds_read_b128 a[168:171],v1 offset:8704
v_exp_f32 v219,v219
ds_read_b128 a[172:175],v1 offset:8960
v_cvt_pk_bf16_f32 v205,v218,v219
v_exp_f32 v220,v220
ds_read_b128 a[188:191],v1 offset:13056
v_exp_f32 v221,v221
ds_read_b128 a[184:187],v1 offset:12800
v_cvt_pk_bf16_f32 v206,v220,v221
v_exp_f32 v222,v222
ds_read_b128 a[180:183],v1 offset:12544
s_waitcnt lgkmcnt(11)
v_mfma_f32_16x16x32_bf16 v[136:139],a[128:131],v[128:131],v[32:35]
buffer_load_dwordx4 v5,s[20:23],s47 offen offset:2048 nt lds
v_mfma_f32_16x16x32_bf16 v[136:139],a[132:135],v[160:163],v[136:139]
ds_read_b128 a[176:179],v1 offset:12288
v_add_u32 v1,s48,v1
v_mfma_f32_16x16x32_bf16 v[136:139],a[136:139],v[192:195],v[136:139]
v_exp_f32 v223,v223
v_mfma_f32_16x16x32_bf16 v[136:139],a[140:143],v[224:227],v[136:139]
s_waitcnt lgkmcnt(11)
v_cvt_pk_bf16_f32 v207,v222,v223
v_mfma_f32_16x16x32_bf16 v[140:143],a[156:159],v[224:227],v[32:35]
s_waitcnt lgkmcnt(10)
v_exp_f32 v232,v232
v_mfma_f32_16x16x32_bf16 v[140:143],a[152:155],v[192:195],v[140:143]
s_waitcnt lgkmcnt(9)
v_exp_f32 v233,v233
v_mfma_f32_16x16x32_bf16 v[140:143],a[148:151],v[160:163],v[140:143]
s_waitcnt lgkmcnt(8)
v_exp_f32 v234,v234
v_mfma_f32_16x16x32_bf16 v[140:143],a[144:147],v[128:131],v[140:143]
s_waitcnt lgkmcnt(1)
v_exp_f32 v235,v235
v_mfma_f32_16x16x32_bf16 v[152:155],a[160:163],v[128:131],v[32:35]
v_exp_f32 v236,v236
v_mfma_f32_16x16x32_bf16 v[152:155],a[164:167],v[160:163],v[152:155]
v_exp_f32 v237,v237
v_mfma_f32_16x16x32_bf16 v[152:155],a[168:171],v[192:195],v[152:155]
v_exp_f32 v238,v238
v_mfma_f32_16x16x32_bf16 v[152:155],a[172:175],v[224:227],v[152:155]
v_exp_f32 v239,v239
v_mfma_f32_16x16x32_bf16 v[156:159],a[188:191],v[224:227],v[32:35]
v_exp_f32 v248,v248
v_mfma_f32_16x16x32_bf16 v[156:159],a[184:187],v[192:195],v[156:159]
buffer_load_dwordx4 v5,s[20:23],s47 offen offset:3072 nt lds
v_mfma_f32_16x16x32_bf16 v[156:159],a[180:183],v[160:163],v[156:159]
)A"
R"A(s_waitcnt lgkmcnt(0)
v_exp_f32 v249,v249
v_mfma_f32_16x16x32_bf16 v[156:159],a[176:179],v[128:131],v[156:159]
v_exp_f32 v250,v250
v_mfma_f32_16x16x32_bf16 a[64:67],a[192:195],v[200:203],a[64:67]
v_exp_f32 v251,v251
v_mfma_f32_16x16x32_bf16 a[64:67],a[196:199],v[204:207],a[64:67]
v_exp_f32 v252,v252
v_mfma_f32_16x16x32_bf16 a[68:71],a[204:207],v[204:207],a[68:71]
v_exp_f32 v253,v253
v_mfma_f32_16x16x32_bf16 a[68:71],a[200:203],v[200:203],a[68:71]
v_exp_f32 v254,v254
v_mfma_f32_16x16x32_bf16 a[72:75],a[208:211],v[200:203],a[72:75]
v_exp_f32 v255,v255
v_mfma_f32_16x16x32_bf16 a[72:75],a[212:215],v[204:207],a[72:75]
v_exp_f32 v136,v136
v_mfma_f32_16x16x32_bf16 a[76:79],a[220:223],v[204:207],a[76:79]
v_exp_f32 v137,v137
v_mfma_f32_16x16x32_bf16 a[76:79],a[216:219],v[200:203],a[76:79]
v_exp_f32 v138,v138
v_mfma_f32_16x16x32_bf16 a[80:83],a[224:227],v[200:203],a[80:83]
s_addk_i32 m0,16384
v_exp_f32 v139,v139
v_mfma_f32_16x16x32_bf16 a[80:83],a[228:231],v[204:207],a[80:83]
v_exp_f32 v140,v140
v_mfma_f32_16x16x32_bf16 a[84:87],a[236:239],v[204:207],a[84:87]
v_exp_f32 v141,v141
v_mfma_f32_16x16x32_bf16 a[84:87],a[232:235],v[200:203],a[84:87]
buffer_load_dwordx4 v4,s[24:27],s47 offen nt lds
v_mfma_f32_16x16x32_bf16 a[88:91],a[240:243],v[200:203],a[88:91]
v_exp_f32 v142,v142
v_mfma_f32_16x16x32_bf16 a[88:91],a[244:247],v[204:207],a[88:91]
v_exp_f32 v143,v143
v_mfma_f32_16x16x32_bf16 a[92:95],a[252:255],v[204:207],a[92:95]
v_cvt_pk_bf16_f32 v232,v232,v233
v_cvt_pk_bf16_f32 v233,v234,v235
v_mfma_f32_16x16x32_bf16 a[92:95],a[248:251],v[200:203],a[92:95]
v_cvt_pk_bf16_f32 v234,v236,v237
v_cvt_pk_bf16_f32 v235,v238,v239
v_smfmac_f32_16x16x64_bf16 v[56:59],v[28:31],v[200:207],v27
v_cvt_pk_bf16_f32 v236,v248,v249
v_cvt_pk_bf16_f32 v237,v250,v251
v_cvt_pk_bf16_f32 v238,v252,v253
v_cvt_pk_bf16_f32 v239,v254,v255
v_exp_f32 v152,v152
v_exp_f32 v153,v153
v_exp_f32 v154,v154
v_exp_f32 v155,v155
v_exp_f32 v156,v156
v_exp_f32 v157,v157
buffer_load_dwordx4 v5,s[24:27],s47 offen offset:1024 nt lds
v_exp_f32 v158,v158
v_exp_f32 v159,v159
v_mfma_f32_16x16x32_bf16 a[96:99],a[192:195],v[232:235],a[96:99]
ds_read_b64_tr_b16 a[192:193],v2
v_mfma_f32_16x16x32_bf16 a[96:99],a[196:199],v[236:239],a[96:99]
ds_read_b64_tr_b16 a[194:195],v2 offset:4096
v_mfma_f32_16x16x32_bf16 a[100:103],a[204:207],v[236:239],a[100:103]
v_mfma_f32_16x16x32_bf16 a[100:103],a[200:203],v[232:235],a[100:103]
ds_read_b64_tr_b16 a[196:197],v2 offset:8192
v_mfma_f32_16x16x32_bf16 a[104:107],a[208:211],v[232:235],a[104:107]
v_mfma_f32_16x16x32_bf16 a[104:107],a[212:215],v[236:239],a[104:107]
v_mfma_f32_16x16x32_bf16 a[108:111],a[220:223],v[236:239],a[108:111]
buffer_load_dwordx4 v4,s[24:27],s47 offen offset:2048 nt lds
v_mfma_f32_16x16x32_bf16 a[108:111],a[216:219],v[232:235],a[108:111]
ds_read_b64_tr_b16 a[198:199],v2 offset:12288
v_mfma_f32_16x16x32_bf16 a[112:115],a[224:227],v[232:235],a[112:115]
v_mfma_f32_16x16x32_bf16 a[112:115],a[228:231],v[236:239],a[112:115]
v_mfma_f32_16x16x32_bf16 a[116:119],a[236:239],v[236:239],a[116:119]
ds_read_b64_tr_b16 a[204:205],v3 offset:8192
v_cvt_pk_bf16_f32 v136,v136,v137
v_mfma_f32_16x16x32_bf16 a[116:119],a[232:235],v[232:235],a[116:119]
ds_read_b64_tr_b16 a[206:207],v3 offset:12288
v_mfma_f32_16x16x32_bf16 a[120:123],a[240:243],v[232:235],a[120:123]
ds_read_b64_tr_b16 a[200:201],v3
v_mfma_f32_16x16x32_bf16 a[120:123],a[244:247],v[236:239],a[120:123]
ds_read_b64_tr_b16 a[202:203],v3 offset:4096
v_cvt_pk_bf16_f32 v137,v138,v139
v_mfma_f32_16x16x32_bf16 a[124:127],a[252:255],v[236:239],a[124:127]
ds_read_b64_tr_b16 a[208:209],v2 offset:256
v_mfma_f32_16x16x32_bf16 a[124:127],a[248:251],v[232:235],a[124:127]
ds_read_b64_tr_b16 a[210:211],v2 offset:4352
v_smfmac_f32_16x16x64_bf16 v[60:63],v[28:31],v[232:239],v27
ds_read_b64_tr_b16 a[212:213],v2 offset:8448
v_mfma_f32_16x16x32_bf16 v[200:203],a[128:131],v[144:147],v[40:43]
ds_read_b64_tr_b16 a[214:215],v2 offset:12544
v_cvt_pk_bf16_f32 v138,v140,v141
v_mfma_f32_16x16x32_bf16 v[200:203],a[132:135],v[176:179],v[200:203]
ds_read_b64_tr_b16 a[220:221],v3 offset:8448
v_mfma_f32_16x16x32_bf16 v[200:203],a[136:139],v[208:211],v[200:203]
ds_read_b64_tr_b16 a[222:223],v3 offset:12544
v_cvt_pk_bf16_f32 v139,v142,v143
v_mfma_f32_16x16x32_bf16 v[200:203],a[140:143],v[240:243],v[200:203]
buffer_load_dwordx4 v5,s[24:27],s47 offen offset:3072 nt lds
v_mfma_f32_16x16x32_bf16 v[204:207],a[156:159],v[240:243],v[40:43]
s_or_b32 s47,s47,0x18000
s_addk_i32 s47,4096
ds_read_b64_tr_b16 a[216:217],v3 offset:256
v_mfma_f32_16x16x32_bf16 v[204:207],a[152:155],v[208:211],v[204:207]
s_and_b32 s47,s47,0x27000
ds_read_b64_tr_b16 a[218:219],v3 offset:4352
v_cvt_pk_bf16_f32 v140,v152,v153
v_mfma_f32_16x16x32_bf16 v[204:207],a[148:151],v[176:179],v[204:207]
s_or_b32 s47,s47,s46
ds_read_b64_tr_b16 a[224:225],v2 offset:512
v_mfma_f32_16x16x32_bf16 v[204:207],a[144:147],v[144:147],v[204:207]
ds_read_b64_tr_b16 a[226:227],v2 offset:4608
v_mfma_f32_16x16x32_bf16 v[216:219],a[160:163],v[144:147],v[40:43]
ds_read_b64_tr_b16 a[228:229],v2 offset:8704
v_cvt_pk_bf16_f32 v141,v154,v155
v_mfma_f32_16x16x32_bf16 v[216:219],a[164:167],v[176:179],v[216:219]
ds_read_b64_tr_b16 a[230:231],v2 offset:12800
v_mfma_f32_16x16x32_bf16 v[216:219],a[168:171],v[208:211],v[216:219]
ds_read_b64_tr_b16 a[236:237],v3 offset:8704
v_mfma_f32_16x16x32_bf16 v[216:219],a[172:175],v[240:243],v[216:219]
ds_read_b64_tr_b16 a[238:239],v3 offset:12800
v_cvt_pk_bf16_f32 v142,v156,v157
v_mfma_f32_16x16x32_bf16 v[220:223],a[188:191],v[240:243],v[40:43]
ds_read_b64_tr_b16 a[232:233],v3 offset:512
v_mfma_f32_16x16x32_bf16 v[220:223],a[184:187],v[208:211],v[220:223]
ds_read_b64_tr_b16 a[234:235],v3 offset:4608
v_mfma_f32_16x16x32_bf16 v[220:223],a[180:183],v[176:179],v[220:223]
ds_read_b64_tr_b16 a[240:241],v2 offset:768
v_cvt_pk_bf16_f32 v143,v158,v159
v_mfma_f32_16x16x32_bf16 v[220:223],a[176:179],v[144:147],v[220:223]
s_waitcnt lgkmcnt(7)
buffer_load_dword v26,v24,s[88:91],s93 offen
v_mfma_f32_16x16x32_bf16 a[0:3],a[192:195],v[136:139],a[0:3]
ds_read_b64_tr_b16 a[242:243],v2 offset:4864
v_mfma_f32_16x16x32_bf16 a[0:3],a[196:199],v[140:143],a[0:3]
ds_read_b64_tr_b16 a[244:245],v2 offset:8960
v_mfma_f32_16x16x32_bf16 a[4:7],a[204:207],v[140:143],a[4:7]
ds_read_b64_tr_b16 a[246:247],v2 offset:13056
v_mfma_f32_16x16x32_bf16 a[4:7],a[200:203],v[136:139],a[4:7]
ds_read_b64_tr_b16 a[252:253],v3 offset:8960
v_mfma_f32_16x16x32_bf16 a[8:11],a[208:211],v[136:139],a[8:11]
ds_read_b64_tr_b16 a[254:255],v3 offset:13056
v_mfma_f32_16x16x32_bf16 a[8:11],a[212:215],v[140:143],a[8:11]
ds_read_b64_tr_b16 a[248:249],v3 offset:768
v_mfma_f32_16x16x32_bf16 a[12:15],a[220:223],v[140:143],a[12:15]
ds_read_b64_tr_b16 a[250:251],v3 offset:4864
v_mfma_f32_16x16x32_bf16 a[12:15],a[216:219],v[136:139],a[12:15]
s_bitcmp1_b32 s45,0
s_cbranch_scc0 L_t31v11_%=
s_mov_b32 m0,s80
s_addk_i32 s80,4096
buffer_load_dwordx4 v25,s[24:27],s83 offen nt lds
s_addk_i32 s83,1024
L_t31v11_%=:
v_mfma_f32_16x16x32_bf16 a[16:19],a[224:227],v[136:139],a[16:19]
s_waitcnt lgkmcnt(6)
v_mfma_f32_16x16x32_bf16 a[16:19],a[228:231],v[140:143],a[16:19]
v_mfma_f32_16x16x32_bf16 a[20:23],a[236:239],v[140:143],a[20:23]
v_mfma_f32_16x16x32_bf16 a[20:23],a[232:235],v[136:139],a[20:23]
v_mfma_f32_16x16x32_bf16 a[24:27],a[240:243],v[136:139],a[24:27]
s_waitcnt lgkmcnt(2)
v_mfma_f32_16x16x32_bf16 a[24:27],a[244:247],v[140:143],a[24:27]
v_mfma_f32_16x16x32_bf16 a[28:31],a[252:255],v[140:143],a[28:31]
s_waitcnt lgkmcnt(0)
v_mfma_f32_16x16x32_bf16 a[28:31],a[248:251],v[136:139],a[28:31]
s_cmp_lg_u32 s53,0
v_smfmac_f32_16x16x64_bf16 v[48:51],v[28:31],v[136:143],v27
s_cbranch_scc1 L_att1v1_%=
s_waitcnt lgkmcnt(0)
s_nop 7
s_branch L_att1x_%=
.p2align 5
L_att1v2_%=:
v_mfma_f32_16x16x32_bf16 v[232:235],a[128:131],v[148:151],v[44:47]
s_waitcnt vmcnt(8) lgkmcnt(0)
v_mfma_f32_16x16x32_bf16 v[232:235],a[132:135],v[180:183],v[232:235]
s_barrier
s_add_u32 m0,s49,s51
ds_read_b128 a[128:131],v1
v_mfma_f32_16x16x32_bf16 v[232:235],a[136:139],v[212:215],v[232:235]
s_mov_b32 s90,L_out_end_%=-L_pc_%=+128
v_mfma_f32_16x16x32_bf16 v[232:235],a[140:143],v[244:247],v[232:235]
s_lshr_b32 s45,s45,1
ds_read_b128 a[132:135],v1 offset:256
v_mfma_f32_16x16x32_bf16 v[236:239],a[156:159],v[244:247],v[44:47]
buffer_load_dwordx4 v4,s[20:23],s47 offen nt lds
v_mfma_f32_16x16x32_bf16 v[236:239],a[152:155],v[212:215],v[236:239]
ds_read_b128 a[136:139],v1 offset:512
v_add_u32 v2,s48,v2
v_mfma_f32_16x16x32_bf16 v[236:239],a[148:151],v[180:183],v[236:239]
v_mfma_f32_16x16x32_bf16 v[236:239],a[144:147],v[148:151],v[236:239]
s_add_u32 s62,s50,32768
ds_read_b128 a[140:143],v1 offset:768
v_add_u32 v3,s48,v3
v_mfma_f32_16x16x32_bf16 v[248:251],a[160:163],v[148:151],v[44:47]
s_and_b32 s62,s62,131071
v_mfma_f32_16x16x32_bf16 v[248:251],a[164:167],v[180:183],v[248:251]
s_addk_i32 s54,16384
ds_read_b128 a[156:159],v1 offset:4864
v_mfma_f32_16x16x32_bf16 v[248:251],a[168:171],v[212:215],v[248:251]
s_sub_u32 s48,s62,s50
v_mfma_f32_16x16x32_bf16 v[248:251],a[172:175],v[244:247],v[248:251]
s_sub_u32 s53,s53,1
v_mfma_f32_16x16x32_bf16 v[252:255],a[188:191],v[244:247],v[44:47]
s_and_b32 s46,s54,0xfffc0000
ds_read_b128 a[152:155],v1 offset:4608
v_mfma_f32_16x16x32_bf16 v[252:255],a[184:187],v[212:215],v[252:255]
s_mov_b32 s50,s62
v_mfma_f32_16x16x32_bf16 v[252:255],a[180:183],v[180:183],v[252:255]
s_xor_b32 s49,s50,65536
v_mfma_f32_16x16x32_bf16 v[252:255],a[176:179],v[148:151],v[252:255]
ds_read_b128 a[148:151],v1 offset:4352
v_mfma_f32_16x16x32_bf16 a[32:35],a[192:195],v[168:171],a[32:35]
v_mfma_f32_16x16x32_bf16 a[32:35],a[196:199],v[172:175],a[32:35]
ds_read_b128 a[144:147],v1 offset:4096
v_mfma_f32_16x16x32_bf16 a[36:39],a[204:207],v[172:175],a[36:39]
buffer_load_dwordx4 v4,s[20:23],s47 offen offset:1024 nt lds
v_mfma_f32_16x16x32_bf16 a[36:39],a[200:203],v[168:171],a[36:39]
ds_read_b128 a[160:163],v1 offset:8192
v_mfma_f32_16x16x32_bf16 a[40:43],a[208:211],v[168:171],a[40:43]
v_mfma_f32_16x16x32_bf16 a[40:43],a[212:215],v[172:175],a[40:43]
ds_read_b128 a[164:167],v1 offset:8448
v_mfma_f32_16x16x32_bf16 a[44:47],a[220:223],v[172:175],a[44:47]
v_mfma_f32_16x16x32_bf16 a[44:47],a[216:219],v[168:171],a[44:47]
ds_read_b128 a[168:171],v1 offset:8704
v_mfma_f32_16x16x32_bf16 a[48:51],a[224:227],v[168:171],a[48:51]
v_mfma_f32_16x16x32_bf16 a[48:51],a[228:231],v[172:175],a[48:51]
ds_read_b128 a[172:175],v1 offset:8960
v_mfma_f32_16x16x32_bf16 a[52:55],a[236:239],v[172:175],a[52:55]
v_mfma_f32_16x16x32_bf16 a[52:55],a[232:235],v[168:171],a[52:55]
ds_read_b128 a[188:191],v1 offset:13056
v_mfma_f32_16x16x32_bf16 a[56:59],a[240:243],v[168:171],a[56:59]
v_mfma_f32_16x16x32_bf16 a[56:59],a[244:247],v[172:175],a[56:59]
ds_read_b128 a[184:187],v1 offset:12800
v_mfma_f32_16x16x32_bf16 a[60:63],a[252:255],v[172:175],a[60:63]
v_mfma_f32_16x16x32_bf16 a[60:63],a[248:251],v[168:171],a[60:63]
ds_read_b128 a[180:183],v1 offset:12544
v_smfmac_f32_16x16x64_bf16 v[52:55],v[28:31],v[168:175],v27
s_waitcnt lgkmcnt(4)
v_mfma_f32_16x16x32_bf16 v[136:139],a[128:131],v[128:131],v[32:35]
buffer_load_dwordx4 v5,s[20:23],s47 offen offset:2048 nt lds
v_mfma_f32_16x16x32_bf16 v[136:139],a[132:135],v[160:163],v[136:139]
ds_read_b128 a[176:179],v1 offset:12288
v_add_u32 v1,s48,v1
v_mfma_f32_16x16x32_bf16 v[136:139],a[136:139],v[192:195],v[136:139]
v_mfma_f32_16x16x32_bf16 v[136:139],a[140:143],v[224:227],v[136:139]
v_mfma_f32_16x16x32_bf16 v[140:143],a[156:159],v[224:227],v[32:35]
v_exp_f32 v232,v232
v_mfma_f32_16x16x32_bf16 v[140:143],a[152:155],v[192:195],v[140:143]
v_exp_f32 v233,v233
v_mfma_f32_16x16x32_bf16 v[140:143],a[148:151],v[160:163],v[140:143]
v_exp_f32 v234,v234
v_mfma_f32_16x16x32_bf16 v[140:143],a[144:147],v[128:131],v[140:143]
v_exp_f32 v235,v235
v_mfma_f32_16x16x32_bf16 v[152:155],a[160:163],v[128:131],v[32:35]
v_exp_f32 v236,v236
v_mfma_f32_16x16x32_bf16 v[152:155],a[164:167],v[160:163],v[152:155]
v_exp_f32 v237,v237
v_mfma_f32_16x16x32_bf16 v[152:155],a[168:171],v[192:195],v[152:155]
s_waitcnt lgkmcnt(0)
v_exp_f32 v238,v238
v_mfma_f32_16x16x32_bf16 v[152:155],a[172:175],v[224:227],v[152:155]
)A"
R"A(v_exp_f32 v239,v239
v_mfma_f32_16x16x32_bf16 v[156:159],a[188:191],v[224:227],v[32:35]
v_exp_f32 v248,v248
v_mfma_f32_16x16x32_bf16 v[156:159],a[184:187],v[192:195],v[156:159]
buffer_load_dwordx4 v5,s[20:23],s47 offen offset:3072 nt lds
v_mfma_f32_16x16x32_bf16 v[156:159],a[180:183],v[160:163],v[156:159]
v_exp_f32 v249,v249
v_mfma_f32_16x16x32_bf16 v[156:159],a[176:179],v[128:131],v[156:159]
v_exp_f32 v250,v250
v_exp_f32 v251,v251
v_exp_f32 v252,v252
v_exp_f32 v253,v253
v_exp_f32 v254,v254
v_exp_f32 v255,v255
v_exp_f32 v136,v136
v_exp_f32 v137,v137
v_exp_f32 v138,v138
s_addk_i32 m0,16384
v_exp_f32 v139,v139
v_exp_f32 v140,v140
v_exp_f32 v141,v141
buffer_load_dwordx4 v4,s[24:27],s47 offen nt lds
v_exp_f32 v142,v142
v_exp_f32 v143,v143
v_cvt_pk_bf16_f32 v232,v232,v233
v_cvt_pk_bf16_f32 v233,v234,v235
v_cvt_pk_bf16_f32 v234,v236,v237
v_cvt_pk_bf16_f32 v235,v238,v239
v_cvt_pk_bf16_f32 v236,v248,v249
v_mfma_f32_16x16x32_bf16 v[168:171],a[128:131],v[132:135],v[36:39]
v_cvt_pk_bf16_f32 v237,v250,v251
v_cvt_pk_bf16_f32 v238,v252,v253
v_mfma_f32_16x16x32_bf16 v[168:171],a[132:135],v[164:167],v[168:171]
v_cvt_pk_bf16_f32 v239,v254,v255
v_mfma_f32_16x16x32_bf16 v[168:171],a[136:139],v[196:199],v[168:171]
v_exp_f32 v152,v152
v_mfma_f32_16x16x32_bf16 v[168:171],a[140:143],v[228:231],v[168:171]
v_exp_f32 v153,v153
v_mfma_f32_16x16x32_bf16 v[172:175],a[156:159],v[228:231],v[36:39]
v_exp_f32 v154,v154
v_mfma_f32_16x16x32_bf16 v[172:175],a[152:155],v[196:199],v[172:175]
v_exp_f32 v155,v155
v_mfma_f32_16x16x32_bf16 v[172:175],a[148:151],v[164:167],v[172:175]
v_exp_f32 v156,v156
v_mfma_f32_16x16x32_bf16 v[172:175],a[144:147],v[132:135],v[172:175]
v_exp_f32 v157,v157
v_mfma_f32_16x16x32_bf16 v[184:187],a[160:163],v[132:135],v[36:39]
buffer_load_dwordx4 v5,s[24:27],s47 offen offset:1024 nt lds
v_mfma_f32_16x16x32_bf16 v[184:187],a[164:167],v[164:167],v[184:187]
v_exp_f32 v158,v158
v_mfma_f32_16x16x32_bf16 v[184:187],a[168:171],v[196:199],v[184:187]
v_exp_f32 v159,v159
v_mfma_f32_16x16x32_bf16 v[184:187],a[172:175],v[228:231],v[184:187]
v_exp_f32 v168,v168
v_mfma_f32_16x16x32_bf16 v[188:191],a[188:191],v[228:231],v[36:39]
v_exp_f32 v169,v169
v_mfma_f32_16x16x32_bf16 v[188:191],a[184:187],v[196:199],v[188:191]
v_exp_f32 v170,v170
v_mfma_f32_16x16x32_bf16 v[188:191],a[180:183],v[164:167],v[188:191]
v_exp_f32 v171,v171
v_mfma_f32_16x16x32_bf16 v[188:191],a[176:179],v[132:135],v[188:191]
v_exp_f32 v172,v172
v_mfma_f32_16x16x32_bf16 a[96:99],a[192:195],v[232:235],a[96:99]
ds_read_b64_tr_b16 a[192:193],v2
v_cvt_pk_bf16_f32 v168,v168,v169
v_mfma_f32_16x16x32_bf16 a[96:99],a[196:199],v[236:239],a[96:99]
ds_read_b64_tr_b16 a[194:195],v2 offset:4096
v_cvt_pk_bf16_f32 v169,v170,v171
v_mfma_f32_16x16x32_bf16 a[100:103],a[204:207],v[236:239],a[100:103]
v_exp_f32 v173,v173
v_mfma_f32_16x16x32_bf16 a[100:103],a[200:203],v[232:235],a[100:103]
ds_read_b64_tr_b16 a[196:197],v2 offset:8192
v_cvt_pk_bf16_f32 v170,v172,v173
v_mfma_f32_16x16x32_bf16 a[104:107],a[208:211],v[232:235],a[104:107]
v_exp_f32 v174,v174
v_mfma_f32_16x16x32_bf16 a[104:107],a[212:215],v[236:239],a[104:107]
v_exp_f32 v175,v175
v_mfma_f32_16x16x32_bf16 a[108:111],a[220:223],v[236:239],a[108:111]
buffer_load_dwordx4 v4,s[24:27],s47 offen offset:2048 nt lds
v_mfma_f32_16x16x32_bf16 a[108:111],a[216:219],v[232:235],a[108:111]
ds_read_b64_tr_b16 a[198:199],v2 offset:12288
v_cvt_pk_bf16_f32 v171,v174,v175
v_mfma_f32_16x16x32_bf16 a[112:115],a[224:227],v[232:235],a[112:115]
v_exp_f32 v184,v184
v_mfma_f32_16x16x32_bf16 a[112:115],a[228:231],v[236:239],a[112:115]
v_exp_f32 v185,v185
v_mfma_f32_16x16x32_bf16 a[116:119],a[236:239],v[236:239],a[116:119]
ds_read_b64_tr_b16 a[204:205],v3 offset:8192
v_cvt_pk_bf16_f32 v136,v136,v137
v_mfma_f32_16x16x32_bf16 a[116:119],a[232:235],v[232:235],a[116:119]
ds_read_b64_tr_b16 a[206:207],v3 offset:12288
v_cvt_pk_bf16_f32 v172,v184,v185
v_mfma_f32_16x16x32_bf16 a[120:123],a[240:243],v[232:235],a[120:123]
ds_read_b64_tr_b16 a[200:201],v3
v_mfma_f32_16x16x32_bf16 a[120:123],a[244:247],v[236:239],a[120:123]
ds_read_b64_tr_b16 a[202:203],v3 offset:4096
v_cvt_pk_bf16_f32 v137,v138,v139
v_mfma_f32_16x16x32_bf16 a[124:127],a[252:255],v[236:239],a[124:127]
ds_read_b64_tr_b16 a[208:209],v2 offset:256
v_mfma_f32_16x16x32_bf16 a[124:127],a[248:251],v[232:235],a[124:127]
ds_read_b64_tr_b16 a[210:211],v2 offset:4352
v_smfmac_f32_16x16x64_bf16 v[60:63],v[28:31],v[232:239],v27
ds_read_b64_tr_b16 a[212:213],v2 offset:8448
ds_read_b64_tr_b16 a[214:215],v2 offset:12544
v_cvt_pk_bf16_f32 v138,v140,v141
ds_read_b64_tr_b16 a[220:221],v3 offset:8448
ds_read_b64_tr_b16 a[222:223],v3 offset:12544
v_cvt_pk_bf16_f32 v139,v142,v143
buffer_load_dwordx4 v5,s[24:27],s47 offen offset:3072 nt lds
s_or_b32 s47,s47,0x18000
s_addk_i32 s47,4096
ds_read_b64_tr_b16 a[216:217],v3 offset:256
s_and_b32 s47,s47,0x27000
ds_read_b64_tr_b16 a[218:219],v3 offset:4352
v_cvt_pk_bf16_f32 v140,v152,v153
s_or_b32 s47,s47,s46
ds_read_b64_tr_b16 a[224:225],v2 offset:512
ds_read_b64_tr_b16 a[226:227],v2 offset:4608
ds_read_b64_tr_b16 a[228:229],v2 offset:8704
v_cvt_pk_bf16_f32 v141,v154,v155
ds_read_b64_tr_b16 a[230:231],v2 offset:12800
ds_read_b64_tr_b16 a[236:237],v3 offset:8704
ds_read_b64_tr_b16 a[238:239],v3 offset:12800
v_cvt_pk_bf16_f32 v142,v156,v157
ds_read_b64_tr_b16 a[232:233],v3 offset:512
ds_read_b64_tr_b16 a[234:235],v3 offset:4608
ds_read_b64_tr_b16 a[240:241],v2 offset:768
v_cvt_pk_bf16_f32 v143,v158,v159
buffer_load_dword v26,v24,s[88:91],s93 offen
s_waitcnt lgkmcnt(15)
v_mfma_f32_16x16x32_bf16 a[0:3],a[192:195],v[136:139],a[0:3]
ds_read_b64_tr_b16 a[242:243],v2 offset:4864
v_mfma_f32_16x16x32_bf16 a[0:3],a[196:199],v[140:143],a[0:3]
ds_read_b64_tr_b16 a[244:245],v2 offset:8960
v_mfma_f32_16x16x32_bf16 a[4:7],a[204:207],v[140:143],a[4:7]
ds_read_b64_tr_b16 a[246:247],v2 offset:13056
v_mfma_f32_16x16x32_bf16 a[4:7],a[200:203],v[136:139],a[4:7]
ds_read_b64_tr_b16 a[252:253],v3 offset:8960
v_mfma_f32_16x16x32_bf16 a[8:11],a[208:211],v[136:139],a[8:11]
s_waitcnt lgkmcnt(15)
ds_read_b64_tr_b16 a[254:255],v3 offset:13056
v_mfma_f32_16x16x32_bf16 a[8:11],a[212:215],v[140:143],a[8:11]
ds_read_b64_tr_b16 a[248:249],v3 offset:768
v_mfma_f32_16x16x32_bf16 a[12:15],a[220:223],v[140:143],a[12:15]
s_waitcnt lgkmcnt(15)
ds_read_b64_tr_b16 a[250:251],v3 offset:4864
v_mfma_f32_16x16x32_bf16 a[12:15],a[216:219],v[136:139],a[12:15]
s_bitcmp1_b32 s45,0
s_cbranch_scc0 L_t31v21_%=
s_mov_b32 m0,s80
s_addk_i32 s80,4096
buffer_load_dwordx4 v25,s[24:27],s83 offen nt lds
s_addk_i32 s83,1024
L_t31v21_%=:
s_waitcnt lgkmcnt(7)
v_mfma_f32_16x16x32_bf16 a[16:19],a[224:227],v[136:139],a[16:19]
v_exp_f32 v186,v186
v_mfma_f32_16x16x32_bf16 a[16:19],a[228:231],v[140:143],a[16:19]
v_exp_f32 v187,v187
v_mfma_f32_16x16x32_bf16 a[20:23],a[236:239],v[140:143],a[20:23]
v_exp_f32 v188,v188
v_mfma_f32_16x16x32_bf16 a[20:23],a[232:235],v[136:139],a[20:23]
s_waitcnt lgkmcnt(3)
v_exp_f32 v189,v189
v_mfma_f32_16x16x32_bf16 a[24:27],a[240:243],v[136:139],a[24:27]
v_exp_f32 v190,v190
v_mfma_f32_16x16x32_bf16 a[24:27],a[244:247],v[140:143],a[24:27]
s_waitcnt lgkmcnt(1)
v_cvt_pk_bf16_f32 v173,v186,v187
v_mfma_f32_16x16x32_bf16 a[28:31],a[252:255],v[140:143],a[28:31]
s_waitcnt lgkmcnt(0)
v_exp_f32 v191,v191
v_mfma_f32_16x16x32_bf16 a[28:31],a[248:251],v[136:139],a[28:31]
v_cvt_pk_bf16_f32 v174,v188,v189
v_cvt_pk_bf16_f32 v175,v190,v191
s_cmp_lg_u32 s53,0
v_smfmac_f32_16x16x64_bf16 v[48:51],v[28:31],v[136:143],v27
s_cbranch_scc1 L_att1v2_%=
s_waitcnt lgkmcnt(0)
s_nop 7
s_branch L_att1x_%=
.p2align 5
L_att1_%=:
v_mfma_f32_16x16x32_bf16 v[232:235],a[128:131],v[148:151],v[44:47]
s_waitcnt vmcnt(8) lgkmcnt(0)
v_exp_f32 v200,v200
v_mfma_f32_16x16x32_bf16 v[232:235],a[132:135],v[180:183],v[232:235]
s_barrier
s_add_u32 m0,s49,s51
ds_read_b128 a[128:131],v1
v_mfma_f32_16x16x32_bf16 v[232:235],a[136:139],v[212:215],v[232:235]
s_mov_b32 s90,L_out_end_%=-L_pc_%=+128
v_exp_f32 v201,v201
v_mfma_f32_16x16x32_bf16 v[232:235],a[140:143],v[244:247],v[232:235]
s_lshr_b32 s45,s45,1
ds_read_b128 a[132:135],v1 offset:256
v_cvt_pk_bf16_f32 v200,v200,v201
v_mfma_f32_16x16x32_bf16 v[236:239],a[156:159],v[244:247],v[44:47]
s_sub_u32 s94,s94,3
buffer_load_dwordx4 v4,s[20:23],s47 offen nt lds
v_mfma_f32_16x16x32_bf16 v[236:239],a[152:155],v[212:215],v[236:239]
s_lshr_b32 s60,s94,2
ds_read_b128 a[136:139],v1 offset:512
v_add_u32 v2,s48,v2
v_mfma_f32_16x16x32_bf16 v[236:239],a[148:151],v[180:183],v[236:239]
s_max_i32 s60,s60,124
v_exp_f32 v202,v202
v_mfma_f32_16x16x32_bf16 v[236:239],a[144:147],v[148:151],v[236:239]
s_add_u32 s62,s50,32768
ds_read_b128 a[140:143],v1 offset:768
v_add_u32 v3,s48,v3
v_mfma_f32_16x16x32_bf16 v[248:251],a[160:163],v[148:151],v[44:47]
s_and_b32 s62,s62,131071
v_exp_f32 v203,v203
v_mfma_f32_16x16x32_bf16 v[248:251],a[164:167],v[180:183],v[248:251]
s_addk_i32 s54,16384
ds_read_b128 a[156:159],v1 offset:4864
v_cvt_pk_bf16_f32 v201,v202,v203
v_mfma_f32_16x16x32_bf16 v[248:251],a[168:171],v[212:215],v[248:251]
s_sub_u32 s48,s62,s50
v_exp_f32 v204,v204
v_mfma_f32_16x16x32_bf16 v[248:251],a[172:175],v[244:247],v[248:251]
s_sub_u32 s53,s53,1
v_exp_f32 v205,v205
v_mfma_f32_16x16x32_bf16 v[252:255],a[188:191],v[244:247],v[44:47]
s_and_b32 s46,s54,0xfffc0000
ds_read_b128 a[152:155],v1 offset:4608
v_cvt_pk_bf16_f32 v202,v204,v205
v_mfma_f32_16x16x32_bf16 v[252:255],a[184:187],v[212:215],v[252:255]
s_mov_b32 s50,s62
v_exp_f32 v206,v206
v_mfma_f32_16x16x32_bf16 v[252:255],a[180:183],v[180:183],v[252:255]
s_xor_b32 s49,s50,65536
v_exp_f32 v207,v207
v_mfma_f32_16x16x32_bf16 v[252:255],a[176:179],v[148:151],v[252:255]
ds_read_b128 a[148:151],v1 offset:4352
v_cvt_pk_bf16_f32 v203,v206,v207
v_mfma_f32_16x16x32_bf16 a[32:35],a[192:195],v[168:171],a[32:35]
v_exp_f32 v216,v216
v_mfma_f32_16x16x32_bf16 a[32:35],a[196:199],v[172:175],a[32:35]
ds_read_b128 a[144:147],v1 offset:4096
v_mfma_f32_16x16x32_bf16 a[36:39],a[204:207],v[172:175],a[36:39]
buffer_load_dwordx4 v4,s[20:23],s47 offen offset:1024 nt lds
v_mfma_f32_16x16x32_bf16 a[36:39],a[200:203],v[168:171],a[36:39]
ds_read_b128 a[160:163],v1 offset:8192
v_mfma_f32_16x16x32_bf16 a[40:43],a[208:211],v[168:171],a[40:43]
v_exp_f32 v217,v217
v_mfma_f32_16x16x32_bf16 a[40:43],a[212:215],v[172:175],a[40:43]
ds_read_b128 a[164:167],v1 offset:8448
v_cvt_pk_bf16_f32 v204,v216,v217
v_mfma_f32_16x16x32_bf16 a[44:47],a[220:223],v[172:175],a[44:47]
v_exp_f32 v218,v218
v_mfma_f32_16x16x32_bf16 a[44:47],a[216:219],v[168:171],a[44:47]
ds_read_b128 a[168:171],v1 offset:8704
v_mfma_f32_16x16x32_bf16 a[48:51],a[224:227],v[168:171],a[48:51]
v_exp_f32 v219,v219
v_mfma_f32_16x16x32_bf16 a[48:51],a[228:231],v[172:175],a[48:51]
ds_read_b128 a[172:175],v1 offset:8960
v_cvt_pk_bf16_f32 v205,v218,v219
v_mfma_f32_16x16x32_bf16 a[52:55],a[236:239],v[172:175],a[52:55]
v_exp_f32 v220,v220
v_mfma_f32_16x16x32_bf16 a[52:55],a[232:235],v[168:171],a[52:55]
ds_read_b128 a[188:191],v1 offset:13056
v_mfma_f32_16x16x32_bf16 a[56:59],a[240:243],v[168:171],a[56:59]
v_exp_f32 v221,v221
v_mfma_f32_16x16x32_bf16 a[56:59],a[244:247],v[172:175],a[56:59]
ds_read_b128 a[184:187],v1 offset:12800
v_cvt_pk_bf16_f32 v206,v220,v221
v_mfma_f32_16x16x32_bf16 a[60:63],a[252:255],v[172:175],a[60:63]
v_exp_f32 v222,v222
v_mfma_f32_16x16x32_bf16 a[60:63],a[248:251],v[168:171],a[60:63]
ds_read_b128 a[180:183],v1 offset:12544
v_smfmac_f32_16x16x64_bf16 v[52:55],v[28:31],v[168:175],v27
s_waitcnt lgkmcnt(4)
v_mfma_f32_16x16x32_bf16 v[136:139],a[128:131],v[128:131],v[32:35]
buffer_load_dwordx4 v5,s[20:23],s47 offen offset:2048 nt lds
v_mfma_f32_16x16x32_bf16 v[136:139],a[132:135],v[160:163],v[136:139]
ds_read_b128 a[176:179],v1 offset:12288
v_add_u32 v1,s48,v1
v_mfma_f32_16x16x32_bf16 v[136:139],a[136:139],v[192:195],v[136:139]
v_exp_f32 v223,v223
v_mfma_f32_16x16x32_bf16 v[136:139],a[140:143],v[224:227],v[136:139]
v_cvt_pk_bf16_f32 v207,v222,v223
v_mfma_f32_16x16x32_bf16 v[140:143],a[156:159],v[224:227],v[32:35]
v_exp_f32 v232,v232
v_mfma_f32_16x16x32_bf16 v[140:143],a[152:155],v[192:195],v[140:143]
v_exp_f32 v233,v233
v_mfma_f32_16x16x32_bf16 v[140:143],a[148:151],v[160:163],v[140:143]
)A"
R"A(v_exp_f32 v234,v234
v_mfma_f32_16x16x32_bf16 v[140:143],a[144:147],v[128:131],v[140:143]
v_exp_f32 v235,v235
v_mfma_f32_16x16x32_bf16 v[152:155],a[160:163],v[128:131],v[32:35]
v_exp_f32 v236,v236
v_mfma_f32_16x16x32_bf16 v[152:155],a[164:167],v[160:163],v[152:155]
v_exp_f32 v237,v237
v_mfma_f32_16x16x32_bf16 v[152:155],a[168:171],v[192:195],v[152:155]
s_waitcnt lgkmcnt(0)
v_exp_f32 v238,v238
v_mfma_f32_16x16x32_bf16 v[152:155],a[172:175],v[224:227],v[152:155]
v_exp_f32 v239,v239
v_mfma_f32_16x16x32_bf16 v[156:159],a[188:191],v[224:227],v[32:35]
v_exp_f32 v248,v248
v_mfma_f32_16x16x32_bf16 v[156:159],a[184:187],v[192:195],v[156:159]
buffer_load_dwordx4 v5,s[20:23],s47 offen offset:3072 nt lds
v_mfma_f32_16x16x32_bf16 v[156:159],a[180:183],v[160:163],v[156:159]
v_exp_f32 v249,v249
v_mfma_f32_16x16x32_bf16 v[156:159],a[176:179],v[128:131],v[156:159]
v_exp_f32 v250,v250
v_mfma_f32_16x16x32_bf16 a[64:67],a[192:195],v[200:203],a[64:67]
v_exp_f32 v251,v251
v_mfma_f32_16x16x32_bf16 a[64:67],a[196:199],v[204:207],a[64:67]
v_exp_f32 v252,v252
v_mfma_f32_16x16x32_bf16 a[68:71],a[204:207],v[204:207],a[68:71]
v_exp_f32 v253,v253
v_mfma_f32_16x16x32_bf16 a[68:71],a[200:203],v[200:203],a[68:71]
v_exp_f32 v254,v254
v_mfma_f32_16x16x32_bf16 a[72:75],a[208:211],v[200:203],a[72:75]
v_exp_f32 v255,v255
v_mfma_f32_16x16x32_bf16 a[72:75],a[212:215],v[204:207],a[72:75]
v_exp_f32 v136,v136
v_mfma_f32_16x16x32_bf16 a[76:79],a[220:223],v[204:207],a[76:79]
v_exp_f32 v137,v137
v_mfma_f32_16x16x32_bf16 a[76:79],a[216:219],v[200:203],a[76:79]
v_exp_f32 v138,v138
v_mfma_f32_16x16x32_bf16 a[80:83],a[224:227],v[200:203],a[80:83]
s_addk_i32 m0,16384
v_exp_f32 v139,v139
v_mfma_f32_16x16x32_bf16 a[80:83],a[228:231],v[204:207],a[80:83]
v_exp_f32 v140,v140
v_mfma_f32_16x16x32_bf16 a[84:87],a[236:239],v[204:207],a[84:87]
v_exp_f32 v141,v141
v_mfma_f32_16x16x32_bf16 a[84:87],a[232:235],v[200:203],a[84:87]
buffer_load_dwordx4 v4,s[24:27],s47 offen nt lds
v_mfma_f32_16x16x32_bf16 a[88:91],a[240:243],v[200:203],a[88:91]
v_exp_f32 v142,v142
v_mfma_f32_16x16x32_bf16 a[88:91],a[244:247],v[204:207],a[88:91]
v_exp_f32 v143,v143
v_mfma_f32_16x16x32_bf16 a[92:95],a[252:255],v[204:207],a[92:95]
v_cvt_pk_bf16_f32 v232,v232,v233
v_cvt_pk_bf16_f32 v233,v234,v235
v_mfma_f32_16x16x32_bf16 a[92:95],a[248:251],v[200:203],a[92:95]
v_cvt_pk_bf16_f32 v234,v236,v237
v_cvt_pk_bf16_f32 v235,v238,v239
v_smfmac_f32_16x16x64_bf16 v[56:59],v[28:31],v[200:207],v27
v_cvt_pk_bf16_f32 v236,v248,v249
v_mfma_f32_16x16x32_bf16 v[168:171],a[128:131],v[132:135],v[36:39]
v_cvt_pk_bf16_f32 v237,v250,v251
v_cvt_pk_bf16_f32 v238,v252,v253
v_mfma_f32_16x16x32_bf16 v[168:171],a[132:135],v[164:167],v[168:171]
v_cvt_pk_bf16_f32 v239,v254,v255
v_mfma_f32_16x16x32_bf16 v[168:171],a[136:139],v[196:199],v[168:171]
v_exp_f32 v152,v152
v_mfma_f32_16x16x32_bf16 v[168:171],a[140:143],v[228:231],v[168:171]
v_exp_f32 v153,v153
v_mfma_f32_16x16x32_bf16 v[172:175],a[156:159],v[228:231],v[36:39]
v_exp_f32 v154,v154
v_mfma_f32_16x16x32_bf16 v[172:175],a[152:155],v[196:199],v[172:175]
v_exp_f32 v155,v155
v_mfma_f32_16x16x32_bf16 v[172:175],a[148:151],v[164:167],v[172:175]
v_exp_f32 v156,v156
v_mfma_f32_16x16x32_bf16 v[172:175],a[144:147],v[132:135],v[172:175]
v_exp_f32 v157,v157
v_mfma_f32_16x16x32_bf16 v[184:187],a[160:163],v[132:135],v[36:39]
buffer_load_dwordx4 v5,s[24:27],s47 offen offset:1024 nt lds
v_mfma_f32_16x16x32_bf16 v[184:187],a[164:167],v[164:167],v[184:187]
v_exp_f32 v158,v158
v_mfma_f32_16x16x32_bf16 v[184:187],a[168:171],v[196:199],v[184:187]
v_exp_f32 v159,v159
v_mfma_f32_16x16x32_bf16 v[184:187],a[172:175],v[228:231],v[184:187]
v_exp_f32 v168,v168
v_mfma_f32_16x16x32_bf16 v[188:191],a[188:191],v[228:231],v[36:39]
v_exp_f32 v169,v169
v_mfma_f32_16x16x32_bf16 v[188:191],a[184:187],v[196:199],v[188:191]
v_exp_f32 v170,v170
v_mfma_f32_16x16x32_bf16 v[188:191],a[180:183],v[164:167],v[188:191]
v_exp_f32 v171,v171
v_mfma_f32_16x16x32_bf16 v[188:191],a[176:179],v[132:135],v[188:191]
v_exp_f32 v172,v172
v_mfma_f32_16x16x32_bf16 a[96:99],a[192:195],v[232:235],a[96:99]
ds_read_b64_tr_b16 a[192:193],v2
v_cvt_pk_bf16_f32 v168,v168,v169
v_mfma_f32_16x16x32_bf16 a[96:99],a[196:199],v[236:239],a[96:99]
ds_read_b64_tr_b16 a[194:195],v2 offset:4096
v_cvt_pk_bf16_f32 v169,v170,v171
v_mfma_f32_16x16x32_bf16 a[100:103],a[204:207],v[236:239],a[100:103]
v_exp_f32 v173,v173
v_mfma_f32_16x16x32_bf16 a[100:103],a[200:203],v[232:235],a[100:103]
ds_read_b64_tr_b16 a[196:197],v2 offset:8192
v_cvt_pk_bf16_f32 v170,v172,v173
v_mfma_f32_16x16x32_bf16 a[104:107],a[208:211],v[232:235],a[104:107]
v_exp_f32 v174,v174
v_mfma_f32_16x16x32_bf16 a[104:107],a[212:215],v[236:239],a[104:107]
v_exp_f32 v175,v175
v_mfma_f32_16x16x32_bf16 a[108:111],a[220:223],v[236:239],a[108:111]
buffer_load_dwordx4 v4,s[24:27],s47 offen offset:2048 nt lds
v_mfma_f32_16x16x32_bf16 a[108:111],a[216:219],v[232:235],a[108:111]
ds_read_b64_tr_b16 a[198:199],v2 offset:12288
v_cvt_pk_bf16_f32 v171,v174,v175
v_mfma_f32_16x16x32_bf16 a[112:115],a[224:227],v[232:235],a[112:115]
v_exp_f32 v184,v184
v_mfma_f32_16x16x32_bf16 a[112:115],a[228:231],v[236:239],a[112:115]
v_exp_f32 v185,v185
v_mfma_f32_16x16x32_bf16 a[116:119],a[236:239],v[236:239],a[116:119]
ds_read_b64_tr_b16 a[204:205],v3 offset:8192
v_cvt_pk_bf16_f32 v136,v136,v137
v_mfma_f32_16x16x32_bf16 a[116:119],a[232:235],v[232:235],a[116:119]
ds_read_b64_tr_b16 a[206:207],v3 offset:12288
v_cvt_pk_bf16_f32 v172,v184,v185
v_mfma_f32_16x16x32_bf16 a[120:123],a[240:243],v[232:235],a[120:123]
ds_read_b64_tr_b16 a[200:201],v3
v_mfma_f32_16x16x32_bf16 a[120:123],a[244:247],v[236:239],a[120:123]
ds_read_b64_tr_b16 a[202:203],v3 offset:4096
v_cvt_pk_bf16_f32 v137,v138,v139
v_mfma_f32_16x16x32_bf16 a[124:127],a[252:255],v[236:239],a[124:127]
ds_read_b64_tr_b16 a[208:209],v2 offset:256
v_mfma_f32_16x16x32_bf16 a[124:127],a[248:251],v[232:235],a[124:127]
ds_read_b64_tr_b16 a[210:211],v2 offset:4352
v_smfmac_f32_16x16x64_bf16 v[60:63],v[28:31],v[232:239],v27
ds_read_b64_tr_b16 a[212:213],v2 offset:8448
v_mfma_f32_16x16x32_bf16 v[200:203],a[128:131],v[144:147],v[40:43]
ds_read_b64_tr_b16 a[214:215],v2 offset:12544
v_cvt_pk_bf16_f32 v138,v140,v141
v_mfma_f32_16x16x32_bf16 v[200:203],a[132:135],v[176:179],v[200:203]
ds_read_b64_tr_b16 a[220:221],v3 offset:8448
v_mfma_f32_16x16x32_bf16 v[200:203],a[136:139],v[208:211],v[200:203]
ds_read_b64_tr_b16 a[222:223],v3 offset:12544
v_cvt_pk_bf16_f32 v139,v142,v143
v_mfma_f32_16x16x32_bf16 v[200:203],a[140:143],v[240:243],v[200:203]
buffer_load_dwordx4 v5,s[24:27],s47 offen offset:3072 nt lds
v_mfma_f32_16x16x32_bf16 v[204:207],a[156:159],v[240:243],v[40:43]
s_or_b32 s47,s47,0x18000
s_addk_i32 s47,4096
ds_read_b64_tr_b16 a[216:217],v3 offset:256
v_mfma_f32_16x16x32_bf16 v[204:207],a[152:155],v[208:211],v[204:207]
s_and_b32 s47,s47,0x27000
ds_read_b64_tr_b16 a[218:219],v3 offset:4352
v_cvt_pk_bf16_f32 v140,v152,v153
v_mfma_f32_16x16x32_bf16 v[204:207],a[148:151],v[176:179],v[204:207]
s_or_b32 s47,s47,s46
ds_read_b64_tr_b16 a[224:225],v2 offset:512
v_mfma_f32_16x16x32_bf16 v[204:207],a[144:147],v[144:147],v[204:207]
ds_read_b64_tr_b16 a[226:227],v2 offset:4608
v_mfma_f32_16x16x32_bf16 v[216:219],a[160:163],v[144:147],v[40:43]
ds_read_b64_tr_b16 a[228:229],v2 offset:8704
v_cvt_pk_bf16_f32 v141,v154,v155
v_mfma_f32_16x16x32_bf16 v[216:219],a[164:167],v[176:179],v[216:219]
ds_read_b64_tr_b16 a[230:231],v2 offset:12800
v_mfma_f32_16x16x32_bf16 v[216:219],a[168:171],v[208:211],v[216:219]
ds_read_b64_tr_b16 a[236:237],v3 offset:8704
v_mfma_f32_16x16x32_bf16 v[216:219],a[172:175],v[240:243],v[216:219]
ds_read_b64_tr_b16 a[238:239],v3 offset:12800
v_cvt_pk_bf16_f32 v142,v156,v157
v_mfma_f32_16x16x32_bf16 v[220:223],a[188:191],v[240:243],v[40:43]
ds_read_b64_tr_b16 a[232:233],v3 offset:512
v_mfma_f32_16x16x32_bf16 v[220:223],a[184:187],v[208:211],v[220:223]
ds_read_b64_tr_b16 a[234:235],v3 offset:4608
v_mfma_f32_16x16x32_bf16 v[220:223],a[180:183],v[176:179],v[220:223]
ds_read_b64_tr_b16 a[240:241],v2 offset:768
v_cvt_pk_bf16_f32 v143,v158,v159
v_mfma_f32_16x16x32_bf16 v[220:223],a[176:179],v[144:147],v[220:223]
s_waitcnt lgkmcnt(7)
buffer_load_dword v26,v24,s[88:91],s93 offen
v_mfma_f32_16x16x32_bf16 a[0:3],a[192:195],v[136:139],a[0:3]
ds_read_b64_tr_b16 a[242:243],v2 offset:4864
v_mfma_f32_16x16x32_bf16 a[0:3],a[196:199],v[140:143],a[0:3]
ds_read_b64_tr_b16 a[244:245],v2 offset:8960
v_mfma_f32_16x16x32_bf16 a[4:7],a[204:207],v[140:143],a[4:7]
ds_read_b64_tr_b16 a[246:247],v2 offset:13056
v_mfma_f32_16x16x32_bf16 a[4:7],a[200:203],v[136:139],a[4:7]
s_memrealtime s[76:77]
ds_read_b64_tr_b16 a[252:253],v3 offset:8960
v_mfma_f32_16x16x32_bf16 a[8:11],a[208:211],v[136:139],a[8:11]
ds_read_b64_tr_b16 a[254:255],v3 offset:13056
v_mfma_f32_16x16x32_bf16 a[8:11],a[212:215],v[140:143],a[8:11]
ds_read_b64_tr_b16 a[248:249],v3 offset:768
v_mfma_f32_16x16x32_bf16 a[12:15],a[220:223],v[140:143],a[12:15]
ds_read_b64_tr_b16 a[250:251],v3 offset:4864
v_mfma_f32_16x16x32_bf16 a[12:15],a[216:219],v[136:139],a[12:15]
s_bitcmp1_b32 s45,0
s_cbranch_scc0 L_t311_%=
s_mov_b32 m0,s80
s_addk_i32 s80,4096
buffer_load_dwordx4 v25,s[24:27],s83 offen nt lds
s_addk_i32 s83,1024
L_t311_%=:
v_mfma_f32_16x16x32_bf16 a[16:19],a[224:227],v[136:139],a[16:19]
s_waitcnt lgkmcnt(6)
v_exp_f32 v186,v186
v_mfma_f32_16x16x32_bf16 a[16:19],a[228:231],v[140:143],a[16:19]
v_exp_f32 v187,v187
v_mfma_f32_16x16x32_bf16 a[20:23],a[236:239],v[140:143],a[20:23]
s_waitcnt lgkmcnt(0)
v_exp_f32 v188,v188
v_mfma_f32_16x16x32_bf16 a[20:23],a[232:235],v[136:139],a[20:23]
s_sub_u32 s79,s78,s76
v_exp_f32 v189,v189
v_mfma_f32_16x16x32_bf16 a[24:27],a[240:243],v[136:139],a[24:27]
s_add_u32 s78,s76,s60
v_exp_f32 v190,v190
v_mfma_f32_16x16x32_bf16 a[24:27],a[244:247],v[140:143],a[24:27]
s_cmp_gt_i32 s79,s95
s_cbranch_scc1 L_ps7_%=
L_pr7_%=:
v_cvt_pk_bf16_f32 v173,v186,v187
v_mfma_f32_16x16x32_bf16 a[28:31],a[252:255],v[140:143],a[28:31]
v_exp_f32 v191,v191
v_mfma_f32_16x16x32_bf16 a[28:31],a[248:251],v[136:139],a[28:31]
v_cvt_pk_bf16_f32 v174,v188,v189
v_cvt_pk_bf16_f32 v175,v190,v191
s_cmp_lg_u32 s53,0
v_smfmac_f32_16x16x64_bf16 v[48:51],v[28:31],v[136:143],v27
s_cbranch_scc1 L_att1_%=
L_att1x_%=:
L_endseg_%=:
v_mfma_f32_16x16x32_bf16 v[232:235],a[128:131],v[148:151],v[44:47]
s_waitcnt vmcnt(10)
s_lshl_b32 s43,s41,2
v_exp_f32 v200,v200
v_mfma_f32_16x16x32_bf16 v[232:235],a[132:135],v[180:183],v[232:235]
s_barrier
ds_read_b128 a[128:131],v1
v_add_u32 v2,s48,v2
v_mfma_f32_16x16x32_bf16 v[232:235],a[136:139],v[212:215],v[232:235]
s_add_u32 s43,s43,s70
v_exp_f32 v201,v201
v_mfma_f32_16x16x32_bf16 v[232:235],a[140:143],v[244:247],v[232:235]
s_lshl_b32 s43,s43,6
ds_read_b128 a[132:135],v1 offset:256
v_cvt_pk_bf16_f32 v200,v200,v201
v_mfma_f32_16x16x32_bf16 v[236:239],a[156:159],v[244:247],v[44:47]
s_add_u32 s43,s43,s40
v_exp_f32 v202,v202
v_mfma_f32_16x16x32_bf16 v[236:239],a[152:155],v[212:215],v[236:239]
s_lshl_b32 s43,s43,15
ds_read_b128 a[136:139],v1 offset:512
v_add_u32 v3,s48,v3
v_mfma_f32_16x16x32_bf16 v[236:239],a[148:151],v[180:183],v[236:239]
s_mov_b32 s48,32768
v_exp_f32 v203,v203
v_mfma_f32_16x16x32_bf16 v[236:239],a[144:147],v[148:151],v[236:239]
s_mov_b64 s[28:29],s[12:13]
ds_read_b128 a[140:143],v1 offset:768
v_cvt_pk_bf16_f32 v201,v202,v203
v_mfma_f32_16x16x32_bf16 v[248:251],a[160:163],v[148:151],v[44:47]
s_mov_b32 s30,950272
v_exp_f32 v204,v204
v_mfma_f32_16x16x32_bf16 v[248:251],a[164:167],v[180:183],v[248:251]
s_mov_b32 s31,0x20000
ds_read_b128 a[156:159],v1 offset:4864
v_lshrrev_b32 v25,6,v6
v_mfma_f32_16x16x32_bf16 v[248:251],a[168:171],v[212:215],v[248:251]
v_exp_f32 v205,v205
v_mfma_f32_16x16x32_bf16 v[248:251],a[172:175],v[244:247],v[248:251]
ds_read_b128 a[152:155],v1 offset:4608
v_cvt_pk_bf16_f32 v202,v204,v205
v_mfma_f32_16x16x32_bf16 v[252:255],a[188:191],v[244:247],v[44:47]
v_exp_f32 v206,v206
v_mfma_f32_16x16x32_bf16 v[252:255],a[184:187],v[212:215],v[252:255]
ds_read_b128 a[148:151],v1 offset:4352
v_mul_u32_u24 v25,320,v25
v_mfma_f32_16x16x32_bf16 v[252:255],a[180:183],v[180:183],v[252:255]
)A"
R"A(v_exp_f32 v207,v207
v_mfma_f32_16x16x32_bf16 v[252:255],a[176:179],v[148:151],v[252:255]
ds_read_b128 a[144:147],v1 offset:4096
v_cvt_pk_bf16_f32 v203,v206,v207
v_mfma_f32_16x16x32_bf16 a[32:35],a[192:195],v[168:171],a[32:35]
v_exp_f32 v216,v216
v_mfma_f32_16x16x32_bf16 a[32:35],a[196:199],v[172:175],a[32:35]
s_add_u32 s28,s28,s43
s_addc_u32 s29,s29,0
ds_read_b128 a[160:163],v1 offset:8192
v_mfma_f32_16x16x32_bf16 a[36:39],a[204:207],v[172:175],a[36:39]
s_and_b32 s29,s29,0xffff
v_exp_f32 v217,v217
v_mfma_f32_16x16x32_bf16 a[36:39],a[200:203],v[168:171],a[36:39]
ds_read_b128 a[164:167],v1 offset:8448
v_cvt_pk_bf16_f32 v204,v216,v217
v_mfma_f32_16x16x32_bf16 a[40:43],a[208:211],v[168:171],a[40:43]
v_exp_f32 v218,v218
v_mfma_f32_16x16x32_bf16 a[40:43],a[212:215],v[172:175],a[40:43]
ds_read_b128 a[168:171],v1 offset:8704
v_mfma_f32_16x16x32_bf16 a[44:47],a[220:223],v[172:175],a[44:47]
v_exp_f32 v219,v219
v_mfma_f32_16x16x32_bf16 a[44:47],a[216:219],v[168:171],a[44:47]
ds_read_b128 a[172:175],v1 offset:8960
v_cvt_pk_bf16_f32 v205,v218,v219
v_mfma_f32_16x16x32_bf16 a[48:51],a[224:227],v[168:171],a[48:51]
v_exp_f32 v220,v220
v_mfma_f32_16x16x32_bf16 a[48:51],a[228:231],v[172:175],a[48:51]
ds_read_b128 a[188:191],v1 offset:13056
v_mfma_f32_16x16x32_bf16 a[52:55],a[236:239],v[172:175],a[52:55]
v_exp_f32 v221,v221
v_mfma_f32_16x16x32_bf16 a[52:55],a[232:235],v[168:171],a[52:55]
ds_read_b128 a[184:187],v1 offset:12800
v_cvt_pk_bf16_f32 v206,v220,v221
v_mfma_f32_16x16x32_bf16 a[56:59],a[240:243],v[168:171],a[56:59]
v_exp_f32 v222,v222
v_mfma_f32_16x16x32_bf16 a[56:59],a[244:247],v[172:175],a[56:59]
ds_read_b128 a[180:183],v1 offset:12544
v_mfma_f32_16x16x32_bf16 a[60:63],a[252:255],v[172:175],a[60:63]
v_exp_f32 v223,v223
v_mfma_f32_16x16x32_bf16 a[60:63],a[248:251],v[168:171],a[60:63]
ds_read_b128 a[176:179],v1 offset:12288
v_cvt_pk_bf16_f32 v207,v222,v223
v_smfmac_f32_16x16x64_bf16 v[52:55],v[28:31],v[168:175],v27
s_waitcnt lgkmcnt(4)
v_add_u32 v1,s48,v1
v_mfma_f32_16x16x32_bf16 v[136:139],a[128:131],v[128:131],v[32:35]
v_exp_f32 v232,v232
v_mfma_f32_16x16x32_bf16 v[136:139],a[132:135],v[160:163],v[136:139]
v_exp_f32 v233,v233
v_mfma_f32_16x16x32_bf16 v[136:139],a[136:139],v[192:195],v[136:139]
v_exp_f32 v234,v234
v_mfma_f32_16x16x32_bf16 v[136:139],a[140:143],v[224:227],v[136:139]
v_exp_f32 v235,v235
v_mfma_f32_16x16x32_bf16 v[140:143],a[156:159],v[224:227],v[32:35]
v_exp_f32 v236,v236
v_mfma_f32_16x16x32_bf16 v[140:143],a[152:155],v[192:195],v[140:143]
s_add_u32 s80,s71,0
s_and_b32 s80,s80,3
s_lshl_b32 s80,s80,1
v_mfma_f32_16x16x32_bf16 v[140:143],a[148:151],v[160:163],v[140:143]
s_mul_i32 s80,s80,163840
s_add_u32 s80,s80,s81
v_cvt_pk_bf16_f32 v232,v232,v233
v_mfma_f32_16x16x32_bf16 v[140:143],a[144:147],v[128:131],v[140:143]
s_add_u32 s83,s80,20480
v_exp_f32 v237,v237
v_mfma_f32_16x16x32_bf16 v[152:155],a[160:163],v[128:131],v[32:35]
v_exp_f32 v238,v238
v_mfma_f32_16x16x32_bf16 v[152:155],a[164:167],v[160:163],v[152:155]
buffer_store_dwordx4 v[64:67],v6,s[36:39],s84 offen sc1
v_mfma_f32_16x16x32_bf16 v[152:155],a[168:171],v[192:195],v[152:155]
v_exp_f32 v239,v239
v_mfma_f32_16x16x32_bf16 v[152:155],a[172:175],v[224:227],v[152:155]
s_waitcnt lgkmcnt(0)
v_exp_f32 v248,v248
v_mfma_f32_16x16x32_bf16 v[156:159],a[188:191],v[224:227],v[32:35]
v_exp_f32 v249,v249
v_mfma_f32_16x16x32_bf16 v[156:159],a[184:187],v[192:195],v[156:159]
v_exp_f32 v250,v250
v_mfma_f32_16x16x32_bf16 v[156:159],a[180:183],v[160:163],v[156:159]
v_exp_f32 v251,v251
v_mfma_f32_16x16x32_bf16 v[156:159],a[176:179],v[128:131],v[156:159]
v_exp_f32 v252,v252
v_mfma_f32_16x16x32_bf16 a[64:67],a[192:195],v[200:203],a[64:67]
v_exp_f32 v253,v253
v_mfma_f32_16x16x32_bf16 a[64:67],a[196:199],v[204:207],a[64:67]
v_exp_f32 v254,v254
v_mfma_f32_16x16x32_bf16 a[68:71],a[204:207],v[204:207],a[68:71]
v_exp_f32 v255,v255
v_mfma_f32_16x16x32_bf16 a[68:71],a[200:203],v[200:203],a[68:71]
v_exp_f32 v136,v136
v_mfma_f32_16x16x32_bf16 a[72:75],a[208:211],v[200:203],a[72:75]
v_exp_f32 v137,v137
v_mfma_f32_16x16x32_bf16 a[72:75],a[212:215],v[204:207],a[72:75]
v_cvt_pk_bf16_f32 v233,v234,v235
v_cvt_pk_bf16_f32 v234,v236,v237
v_mfma_f32_16x16x32_bf16 a[76:79],a[220:223],v[204:207],a[76:79]
v_cvt_pk_bf16_f32 v235,v238,v239
v_cvt_pk_bf16_f32 v236,v248,v249
v_mfma_f32_16x16x32_bf16 a[76:79],a[216:219],v[200:203],a[76:79]
v_cvt_pk_bf16_f32 v237,v250,v251
v_cvt_pk_bf16_f32 v238,v252,v253
v_mfma_f32_16x16x32_bf16 a[80:83],a[224:227],v[200:203],a[80:83]
v_cvt_pk_bf16_f32 v239,v254,v255
v_cvt_pk_bf16_f32 v136,v136,v137
v_mfma_f32_16x16x32_bf16 a[80:83],a[228:231],v[204:207],a[80:83]
buffer_store_dwordx4 v[68:71],v6,s[36:39],s84 offen offset:1024 sc1
v_mfma_f32_16x16x32_bf16 a[84:87],a[236:239],v[204:207],a[84:87]
s_add_u32 m0,s82,81920
v_exp_f32 v138,v138
v_mfma_f32_16x16x32_bf16 a[84:87],a[232:235],v[200:203],a[84:87]
v_exp_f32 v139,v139
v_mfma_f32_16x16x32_bf16 a[88:91],a[240:243],v[200:203],a[88:91]
v_exp_f32 v140,v140
v_mfma_f32_16x16x32_bf16 a[88:91],a[244:247],v[204:207],a[88:91]
buffer_load_dwordx4 v7,s[72:75],s80 offen lds
v_mfma_f32_16x16x32_bf16 a[92:95],a[252:255],v[204:207],a[92:95]
v_exp_f32 v141,v141
v_mfma_f32_16x16x32_bf16 a[92:95],a[248:251],v[200:203],a[92:95]
v_exp_f32 v142,v142
v_smfmac_f32_16x16x64_bf16 v[56:59],v[28:31],v[200:207],v27
v_cvt_pk_bf16_f32 v137,v138,v139
v_mfma_f32_16x16x32_bf16 v[168:171],a[128:131],v[132:135],v[36:39]
v_exp_f32 v143,v143
v_mfma_f32_16x16x32_bf16 v[168:171],a[132:135],v[164:167],v[168:171]
v_cvt_pk_bf16_f32 v138,v140,v141
v_cvt_pk_bf16_f32 v139,v142,v143
v_mfma_f32_16x16x32_bf16 v[168:171],a[136:139],v[196:199],v[168:171]
v_exp_f32 v152,v152
v_mfma_f32_16x16x32_bf16 v[168:171],a[140:143],v[228:231],v[168:171]
v_exp_f32 v153,v153
v_mfma_f32_16x16x32_bf16 v[172:175],a[156:159],v[228:231],v[36:39]
v_exp_f32 v154,v154
v_mfma_f32_16x16x32_bf16 v[172:175],a[152:155],v[196:199],v[172:175]
v_exp_f32 v155,v155
v_mfma_f32_16x16x32_bf16 v[172:175],a[148:151],v[164:167],v[172:175]
v_exp_f32 v156,v156
v_mfma_f32_16x16x32_bf16 v[172:175],a[144:147],v[132:135],v[172:175]
v_exp_f32 v157,v157
v_mfma_f32_16x16x32_bf16 v[184:187],a[160:163],v[132:135],v[36:39]
buffer_store_dwordx4 v[72:75],v6,s[36:39],s84 offen offset:2048 sc1
v_mfma_f32_16x16x32_bf16 v[184:187],a[164:167],v[164:167],v[184:187]
v_exp_f32 v158,v158
v_mfma_f32_16x16x32_bf16 v[184:187],a[168:171],v[196:199],v[184:187]
v_exp_f32 v159,v159
v_mfma_f32_16x16x32_bf16 v[184:187],a[172:175],v[228:231],v[184:187]
v_cvt_pk_bf16_f32 v140,v152,v153
v_cvt_pk_bf16_f32 v141,v154,v155
v_mfma_f32_16x16x32_bf16 v[188:191],a[188:191],v[228:231],v[36:39]
v_cvt_pk_bf16_f32 v142,v156,v157
v_cvt_pk_bf16_f32 v143,v158,v159
v_mfma_f32_16x16x32_bf16 v[188:191],a[184:187],v[196:199],v[188:191]
v_exp_f32 v168,v168
v_mfma_f32_16x16x32_bf16 v[188:191],a[180:183],v[164:167],v[188:191]
v_exp_f32 v169,v169
v_mfma_f32_16x16x32_bf16 v[188:191],a[176:179],v[132:135],v[188:191]
v_exp_f32 v170,v170
v_mfma_f32_16x16x32_bf16 a[96:99],a[192:195],v[232:235],a[96:99]
buffer_load_dwordx4 v8,s[72:75],s80 offen offset:1024 lds
v_mfma_f32_16x16x32_bf16 a[96:99],a[196:199],v[236:239],a[96:99]
ds_read_b64_tr_b16 a[192:193],v2
ds_read_b64_tr_b16 a[194:195],v2 offset:4096
v_mfma_f32_16x16x32_bf16 a[100:103],a[204:207],v[236:239],a[100:103]
ds_read_b64_tr_b16 a[196:197],v2 offset:8192
v_cvt_pk_bf16_f32 v168,v168,v169
v_mfma_f32_16x16x32_bf16 a[100:103],a[200:203],v[232:235],a[100:103]
v_exp_f32 v171,v171
v_mfma_f32_16x16x32_bf16 a[104:107],a[208:211],v[232:235],a[104:107]
ds_read_b64_tr_b16 a[198:199],v2 offset:12288
ds_read_b64_tr_b16 a[204:205],v3 offset:8192
v_mfma_f32_16x16x32_bf16 a[104:107],a[212:215],v[236:239],a[104:107]
ds_read_b64_tr_b16 a[206:207],v3 offset:12288
v_cvt_pk_bf16_f32 v169,v170,v171
v_mfma_f32_16x16x32_bf16 a[108:111],a[220:223],v[236:239],a[108:111]
v_exp_f32 v172,v172
v_mfma_f32_16x16x32_bf16 a[108:111],a[216:219],v[232:235],a[108:111]
ds_read_b64_tr_b16 a[200:201],v3
ds_read_b64_tr_b16 a[202:203],v3 offset:4096
v_mfma_f32_16x16x32_bf16 a[112:115],a[224:227],v[232:235],a[112:115]
buffer_store_dwordx4 v[76:79],v6,s[36:39],s84 offen offset:3072 sc1
v_mfma_f32_16x16x32_bf16 a[112:115],a[228:231],v[236:239],a[112:115]
s_add_u32 s84,s55,131072
ds_read_b64_tr_b16 a[208:209],v2 offset:256
ds_read_b64_tr_b16 a[210:211],v2 offset:4352
v_mfma_f32_16x16x32_bf16 a[116:119],a[236:239],v[236:239],a[116:119]
s_max_i32 s84,s84,s18
v_exp_f32 v173,v173
v_mfma_f32_16x16x32_bf16 a[116:119],a[232:235],v[232:235],a[116:119]
ds_read_b64_tr_b16 a[212:213],v2 offset:8448
ds_read_b64_tr_b16 a[214:215],v2 offset:12544
v_mfma_f32_16x16x32_bf16 a[120:123],a[240:243],v[232:235],a[120:123]
ds_read_b64_tr_b16 a[220:221],v3 offset:8448
v_cvt_pk_bf16_f32 v170,v172,v173
v_mfma_f32_16x16x32_bf16 a[120:123],a[244:247],v[236:239],a[120:123]
ds_read_b64_tr_b16 a[222:223],v3 offset:12544
v_mfma_f32_16x16x32_bf16 a[124:127],a[252:255],v[236:239],a[124:127]
ds_read_b64_tr_b16 a[216:217],v3 offset:256
v_mfma_f32_16x16x32_bf16 a[124:127],a[248:251],v[232:235],a[124:127]
ds_read_b64_tr_b16 a[218:219],v3 offset:4352
ds_read_b64_tr_b16 a[224:225],v2 offset:512
v_smfmac_f32_16x16x64_bf16 v[60:63],v[28:31],v[232:239],v27
ds_read_b64_tr_b16 a[226:227],v2 offset:4608
v_mfma_f32_16x16x32_bf16 v[200:203],a[128:131],v[144:147],v[40:43]
ds_read_b64_tr_b16 a[228:229],v2 offset:8704
v_mfma_f32_16x16x32_bf16 v[200:203],a[132:135],v[176:179],v[200:203]
ds_read_b64_tr_b16 a[230:231],v2 offset:12800
v_mfma_f32_16x16x32_bf16 v[200:203],a[136:139],v[208:211],v[200:203]
ds_read_b64_tr_b16 a[236:237],v3 offset:8704
ds_read_b64_tr_b16 a[238:239],v3 offset:12800
v_mfma_f32_16x16x32_bf16 v[200:203],a[140:143],v[240:243],v[200:203]
buffer_load_dwordx4 v9,s[72:75],s80 offen offset:2048 lds
v_mfma_f32_16x16x32_bf16 v[204:207],a[156:159],v[240:243],v[40:43]
ds_read_b64_tr_b16 a[232:233],v3 offset:512
ds_read_b64_tr_b16 a[234:235],v3 offset:4608
v_mfma_f32_16x16x32_bf16 v[204:207],a[152:155],v[208:211],v[204:207]
ds_read_b64_tr_b16 a[240:241],v2 offset:768
v_mfma_f32_16x16x32_bf16 v[204:207],a[148:151],v[176:179],v[204:207]
ds_read_b64_tr_b16 a[242:243],v2 offset:4864
v_mfma_f32_16x16x32_bf16 v[204:207],a[144:147],v[144:147],v[204:207]
buffer_store_dwordx4 v[80:83],v6,s[36:39],s85 offen sc1
v_mfma_f32_16x16x32_bf16 v[216:219],a[160:163],v[144:147],v[40:43]
ds_read_b64_tr_b16 a[244:245],v2 offset:8960
ds_read_b64_tr_b16 a[246:247],v2 offset:13056
v_mfma_f32_16x16x32_bf16 v[216:219],a[164:167],v[176:179],v[216:219]
ds_read_b64_tr_b16 a[252:253],v3 offset:8960
v_add_u32 v2,s48,v2
v_mfma_f32_16x16x32_bf16 v[216:219],a[168:171],v[208:211],v[216:219]
ds_read_b64_tr_b16 a[254:255],v3 offset:13056
v_mfma_f32_16x16x32_bf16 v[216:219],a[172:175],v[240:243],v[216:219]
ds_read_b64_tr_b16 a[248:249],v3 offset:768
v_mfma_f32_16x16x32_bf16 v[220:223],a[188:191],v[240:243],v[40:43]
ds_read_b64_tr_b16 a[250:251],v3 offset:4864
v_add_u32 v3,s48,v3
v_mfma_f32_16x16x32_bf16 v[220:223],a[184:187],v[208:211],v[220:223]
s_mov_b32 s48,98304
v_exp_f32 v174,v174
v_mfma_f32_16x16x32_bf16 v[220:223],a[180:183],v[176:179],v[220:223]
v_exp_f32 v175,v175
v_mfma_f32_16x16x32_bf16 v[220:223],a[176:179],v[144:147],v[220:223]
s_waitcnt lgkmcnt(6)
v_exp_f32 v184,v184
v_mfma_f32_16x16x32_bf16 a[0:3],a[192:195],v[136:139],a[0:3]
v_exp_f32 v185,v185
v_mfma_f32_16x16x32_bf16 a[0:3],a[196:199],v[140:143],a[0:3]
v_cvt_pk_bf16_f32 v171,v174,v175
v_cvt_pk_bf16_f32 v172,v184,v185
v_mfma_f32_16x16x32_bf16 a[4:7],a[204:207],v[140:143],a[4:7]
v_exp_f32 v186,v186
v_mfma_f32_16x16x32_bf16 a[4:7],a[200:203],v[136:139],a[4:7]
v_exp_f32 v187,v187
v_mfma_f32_16x16x32_bf16 a[8:11],a[208:211],v[136:139],a[8:11]
v_exp_f32 v188,v188
v_mfma_f32_16x16x32_bf16 a[8:11],a[212:215],v[140:143],a[8:11]
v_exp_f32 v189,v189
v_mfma_f32_16x16x32_bf16 a[12:15],a[220:223],v[140:143],a[12:15]
v_exp_f32 v190,v190
v_mfma_f32_16x16x32_bf16 a[12:15],a[216:219],v[136:139],a[12:15]
buffer_store_dwordx4 v[84:87],v6,s[36:39],s85 offen offset:1024 sc1
v_mfma_f32_16x16x32_bf16 a[16:19],a[224:227],v[136:139],a[16:19]
v_exp_f32 v191,v191
)A"
R"A(v_mfma_f32_16x16x32_bf16 a[16:19],a[228:231],v[140:143],a[16:19]
v_cvt_pk_bf16_f32 v173,v186,v187
v_cvt_pk_bf16_f32 v174,v188,v189
v_mfma_f32_16x16x32_bf16 a[20:23],a[236:239],v[140:143],a[20:23]
v_exp_f32 v200,v200
v_mfma_f32_16x16x32_bf16 a[20:23],a[232:235],v[136:139],a[20:23]
buffer_load_dwordx4 v10,s[72:75],s80 offen offset:3072 lds
v_mfma_f32_16x16x32_bf16 a[24:27],a[240:243],v[136:139],a[24:27]
s_waitcnt lgkmcnt(0)
v_exp_f32 v201,v201
v_mfma_f32_16x16x32_bf16 a[24:27],a[244:247],v[140:143],a[24:27]
v_cvt_pk_bf16_f32 v175,v190,v191
v_cvt_pk_bf16_f32 v200,v200,v201
v_mfma_f32_16x16x32_bf16 a[28:31],a[252:255],v[140:143],a[28:31]
v_exp_f32 v202,v202
v_mfma_f32_16x16x32_bf16 a[28:31],a[248:251],v[136:139],a[28:31]
v_exp_f32 v203,v203
v_smfmac_f32_16x16x64_bf16 v[48:51],v[28:31],v[136:143],v27
v_cvt_pk_bf16_f32 v201,v202,v203
v_mfma_f32_16x16x32_bf16 v[232:235],a[128:131],v[148:151],v[44:47]
s_waitcnt vmcnt(11)
v_exp_f32 v204,v204
v_mfma_f32_16x16x32_bf16 v[232:235],a[132:135],v[180:183],v[232:235]
s_barrier
v_exp_f32 v205,v205
v_mfma_f32_16x16x32_bf16 v[232:235],a[136:139],v[212:215],v[232:235]
ds_read_b128 a[128:131],v1
v_cvt_pk_bf16_f32 v202,v204,v205
v_mfma_f32_16x16x32_bf16 v[232:235],a[140:143],v[244:247],v[232:235]
v_exp_f32 v206,v206
v_mfma_f32_16x16x32_bf16 v[236:239],a[156:159],v[244:247],v[44:47]
v_exp_f32 v207,v207
v_mfma_f32_16x16x32_bf16 v[236:239],a[152:155],v[212:215],v[236:239]
buffer_store_dwordx4 v[88:91],v6,s[36:39],s85 offen offset:2048 sc1
v_mfma_f32_16x16x32_bf16 v[236:239],a[148:151],v[180:183],v[236:239]
s_addk_i32 m0,4096
ds_read_b128 a[132:135],v1 offset:256
v_cvt_pk_bf16_f32 v203,v206,v207
v_mfma_f32_16x16x32_bf16 v[236:239],a[144:147],v[148:151],v[236:239]
v_exp_f32 v216,v216
v_mfma_f32_16x16x32_bf16 v[248:251],a[160:163],v[148:151],v[44:47]
v_exp_f32 v217,v217
v_mfma_f32_16x16x32_bf16 v[248:251],a[164:167],v[180:183],v[248:251]
buffer_load_dwordx4 v11,s[72:75],s80 offen lds
v_mfma_f32_16x16x32_bf16 v[248:251],a[168:171],v[212:215],v[248:251]
ds_read_b128 a[136:139],v1 offset:512
v_cvt_pk_bf16_f32 v204,v216,v217
v_mfma_f32_16x16x32_bf16 v[248:251],a[172:175],v[244:247],v[248:251]
v_exp_f32 v218,v218
v_mfma_f32_16x16x32_bf16 v[252:255],a[188:191],v[244:247],v[44:47]
ds_read_b128 a[140:143],v1 offset:768
v_mfma_f32_16x16x32_bf16 v[252:255],a[184:187],v[212:215],v[252:255]
v_exp_f32 v219,v219
v_mfma_f32_16x16x32_bf16 v[252:255],a[180:183],v[180:183],v[252:255]
ds_read_b128 a[156:159],v1 offset:4864
v_cvt_pk_bf16_f32 v205,v218,v219
v_mfma_f32_16x16x32_bf16 v[252:255],a[176:179],v[148:151],v[252:255]
v_exp_f32 v220,v220
v_mfma_f32_16x16x32_bf16 a[32:35],a[192:195],v[168:171],a[32:35]
ds_read_b128 a[152:155],v1 offset:4608
v_mfma_f32_16x16x32_bf16 a[32:35],a[196:199],v[172:175],a[32:35]
v_exp_f32 v221,v221
v_mfma_f32_16x16x32_bf16 a[36:39],a[204:207],v[172:175],a[36:39]
ds_read_b128 a[148:151],v1 offset:4352
v_cvt_pk_bf16_f32 v206,v220,v221
v_mfma_f32_16x16x32_bf16 a[36:39],a[200:203],v[168:171],a[36:39]
v_exp_f32 v222,v222
v_mfma_f32_16x16x32_bf16 a[40:43],a[208:211],v[168:171],a[40:43]
ds_read_b128 a[144:147],v1 offset:4096
v_mfma_f32_16x16x32_bf16 a[40:43],a[212:215],v[172:175],a[40:43]
buffer_store_dwordx4 v[92:95],v6,s[36:39],s85 offen offset:3072 sc1
v_mfma_f32_16x16x32_bf16 a[44:47],a[220:223],v[172:175],a[44:47]
s_add_u32 s85,s55,135168
s_max_i32 s85,s85,s19
ds_read_b128 a[160:163],v1 offset:8192
v_mfma_f32_16x16x32_bf16 a[44:47],a[216:219],v[168:171],a[44:47]
v_exp_f32 v223,v223
v_mfma_f32_16x16x32_bf16 a[48:51],a[224:227],v[168:171],a[48:51]
ds_read_b128 a[164:167],v1 offset:8448
v_cvt_pk_bf16_f32 v207,v222,v223
v_mfma_f32_16x16x32_bf16 a[48:51],a[228:231],v[172:175],a[48:51]
v_exp_f32 v232,v232
v_mfma_f32_16x16x32_bf16 a[52:55],a[236:239],v[172:175],a[52:55]
s_addk_i32 m0,1024
ds_read_b128 a[168:171],v1 offset:8704
v_mfma_f32_16x16x32_bf16 a[52:55],a[232:235],v[168:171],a[52:55]
v_exp_f32 v233,v233
v_mfma_f32_16x16x32_bf16 a[56:59],a[240:243],v[168:171],a[56:59]
ds_read_b128 a[172:175],v1 offset:8960
v_cvt_pk_bf16_f32 v232,v232,v233
v_mfma_f32_16x16x32_bf16 a[56:59],a[244:247],v[172:175],a[56:59]
buffer_load_dwordx4 v7,s[72:75],s83 offen lds
v_mfma_f32_16x16x32_bf16 a[60:63],a[252:255],v[172:175],a[60:63]
ds_read_b128 a[188:191],v1 offset:13056
v_mfma_f32_16x16x32_bf16 a[60:63],a[248:251],v[168:171],a[60:63]
v_exp_f32 v234,v234
v_smfmac_f32_16x16x64_bf16 v[52:55],v[28:31],v[168:175],v27
s_waitcnt lgkmcnt(3)
ds_read_b128 a[184:187],v1 offset:12800
v_mfma_f32_16x16x32_bf16 v[136:139],a[128:131],v[128:131],v[32:35]
v_exp_f32 v235,v235
v_mfma_f32_16x16x32_bf16 v[136:139],a[132:135],v[160:163],v[136:139]
ds_read_b128 a[180:183],v1 offset:12544
v_cvt_pk_bf16_f32 v233,v234,v235
v_mfma_f32_16x16x32_bf16 v[136:139],a[136:139],v[192:195],v[136:139]
v_exp_f32 v236,v236
v_mfma_f32_16x16x32_bf16 v[136:139],a[140:143],v[224:227],v[136:139]
ds_read_b128 a[176:179],v1 offset:12288
v_add_u32 v1,s48,v1
v_mfma_f32_16x16x32_bf16 v[140:143],a[156:159],v[224:227],v[32:35]
buffer_store_dwordx4 v[96:99],v6,s[36:39],s86 offen sc1
v_mfma_f32_16x16x32_bf16 v[140:143],a[152:155],v[192:195],v[140:143]
v_exp_f32 v237,v237
v_mfma_f32_16x16x32_bf16 v[140:143],a[148:151],v[160:163],v[140:143]
v_exp_f32 v238,v238
v_mfma_f32_16x16x32_bf16 v[140:143],a[144:147],v[128:131],v[140:143]
v_exp_f32 v239,v239
v_mfma_f32_16x16x32_bf16 v[152:155],a[160:163],v[128:131],v[32:35]
v_cvt_pk_bf16_f32 v234,v236,v237
v_cvt_pk_bf16_f32 v235,v238,v239
v_mfma_f32_16x16x32_bf16 v[152:155],a[164:167],v[160:163],v[152:155]
s_waitcnt lgkmcnt(1)
v_exp_f32 v248,v248
v_mfma_f32_16x16x32_bf16 v[152:155],a[168:171],v[192:195],v[152:155]
v_exp_f32 v249,v249
v_mfma_f32_16x16x32_bf16 v[152:155],a[172:175],v[224:227],v[152:155]
v_exp_f32 v250,v250
v_mfma_f32_16x16x32_bf16 v[156:159],a[188:191],v[224:227],v[32:35]
v_exp_f32 v251,v251
v_mfma_f32_16x16x32_bf16 v[156:159],a[184:187],v[192:195],v[156:159]
v_exp_f32 v252,v252
v_mfma_f32_16x16x32_bf16 v[156:159],a[180:183],v[160:163],v[156:159]
s_waitcnt lgkmcnt(0)
v_exp_f32 v253,v253
v_mfma_f32_16x16x32_bf16 v[156:159],a[176:179],v[128:131],v[156:159]
v_exp_f32 v254,v254
v_mfma_f32_16x16x32_bf16 a[64:67],a[192:195],v[200:203],a[64:67]
buffer_load_dwordx4 v8,s[72:75],s83 offen offset:1024 lds
v_mfma_f32_16x16x32_bf16 a[64:67],a[196:199],v[204:207],a[64:67]
v_exp_f32 v255,v255
v_mfma_f32_16x16x32_bf16 a[68:71],a[204:207],v[204:207],a[68:71]
v_cvt_pk_bf16_f32 v236,v248,v249
v_cvt_pk_bf16_f32 v237,v250,v251
v_mfma_f32_16x16x32_bf16 a[68:71],a[200:203],v[200:203],a[68:71]
v_cvt_pk_bf16_f32 v238,v252,v253
v_cvt_pk_bf16_f32 v239,v254,v255
v_mfma_f32_16x16x32_bf16 a[72:75],a[208:211],v[200:203],a[72:75]
buffer_store_dwordx4 v[100:103],v6,s[36:39],s86 offen offset:1024 sc1
v_mfma_f32_16x16x32_bf16 a[72:75],a[212:215],v[204:207],a[72:75]
v_exp_f32 v136,v136
v_mfma_f32_16x16x32_bf16 a[76:79],a[220:223],v[204:207],a[76:79]
v_exp_f32 v137,v137
v_mfma_f32_16x16x32_bf16 a[76:79],a[216:219],v[200:203],a[76:79]
v_exp_f32 v138,v138
v_mfma_f32_16x16x32_bf16 a[80:83],a[224:227],v[200:203],a[80:83]
v_exp_f32 v139,v139
v_mfma_f32_16x16x32_bf16 a[80:83],a[228:231],v[204:207],a[80:83]
v_exp_f32 v140,v140
v_mfma_f32_16x16x32_bf16 a[84:87],a[236:239],v[204:207],a[84:87]
v_exp_f32 v141,v141
v_mfma_f32_16x16x32_bf16 a[84:87],a[232:235],v[200:203],a[84:87]
v_exp_f32 v142,v142
v_mfma_f32_16x16x32_bf16 a[88:91],a[240:243],v[200:203],a[88:91]
v_exp_f32 v143,v143
v_mfma_f32_16x16x32_bf16 a[88:91],a[244:247],v[204:207],a[88:91]
v_exp_f32 v152,v152
v_mfma_f32_16x16x32_bf16 a[92:95],a[252:255],v[204:207],a[92:95]
v_exp_f32 v153,v153
v_mfma_f32_16x16x32_bf16 a[92:95],a[248:251],v[200:203],a[92:95]
v_exp_f32 v154,v154
v_smfmac_f32_16x16x64_bf16 v[56:59],v[28:31],v[200:207],v27
v_cvt_pk_bf16_f32 v136,v136,v137
v_mfma_f32_16x16x32_bf16 v[168:171],a[128:131],v[132:135],v[36:39]
v_exp_f32 v155,v155
v_mfma_f32_16x16x32_bf16 v[168:171],a[132:135],v[164:167],v[168:171]
v_exp_f32 v156,v156
v_mfma_f32_16x16x32_bf16 v[168:171],a[136:139],v[196:199],v[168:171]
v_exp_f32 v157,v157
v_mfma_f32_16x16x32_bf16 v[168:171],a[140:143],v[228:231],v[168:171]
buffer_store_dwordx4 v[104:107],v6,s[36:39],s86 offen offset:2048 sc1
v_mfma_f32_16x16x32_bf16 v[172:175],a[156:159],v[228:231],v[36:39]
v_exp_f32 v158,v158
v_mfma_f32_16x16x32_bf16 v[172:175],a[152:155],v[196:199],v[172:175]
v_exp_f32 v159,v159
v_mfma_f32_16x16x32_bf16 v[172:175],a[148:151],v[164:167],v[172:175]
v_cvt_pk_bf16_f32 v137,v138,v139
v_cvt_pk_bf16_f32 v138,v140,v141
v_mfma_f32_16x16x32_bf16 v[172:175],a[144:147],v[132:135],v[172:175]
buffer_load_dwordx4 v9,s[72:75],s83 offen offset:2048 lds
v_mfma_f32_16x16x32_bf16 v[184:187],a[160:163],v[132:135],v[36:39]
v_cvt_pk_bf16_f32 v139,v142,v143
v_cvt_pk_bf16_f32 v140,v152,v153
v_mfma_f32_16x16x32_bf16 v[184:187],a[164:167],v[164:167],v[184:187]
v_cvt_pk_bf16_f32 v141,v154,v155
v_cvt_pk_bf16_f32 v142,v156,v157
v_mfma_f32_16x16x32_bf16 v[184:187],a[168:171],v[196:199],v[184:187]
v_exp_f32 v168,v168
v_mfma_f32_16x16x32_bf16 v[184:187],a[172:175],v[228:231],v[184:187]
v_exp_f32 v169,v169
v_mfma_f32_16x16x32_bf16 v[188:191],a[188:191],v[228:231],v[36:39]
v_cvt_pk_bf16_f32 v143,v158,v159
v_cvt_pk_bf16_f32 v168,v168,v169
v_mfma_f32_16x16x32_bf16 v[188:191],a[184:187],v[196:199],v[188:191]
v_exp_f32 v170,v170
v_mfma_f32_16x16x32_bf16 v[188:191],a[180:183],v[164:167],v[188:191]
v_exp_f32 v171,v171
v_mfma_f32_16x16x32_bf16 v[188:191],a[176:179],v[132:135],v[188:191]
v_exp_f32 v172,v172
v_mfma_f32_16x16x32_bf16 a[96:99],a[192:195],v[232:235],a[96:99]
ds_read_b64_tr_b16 a[192:193],v2
ds_read_b64_tr_b16 a[194:195],v2 offset:4096
v_mfma_f32_16x16x32_bf16 a[96:99],a[196:199],v[236:239],a[96:99]
ds_read_b64_tr_b16 a[196:197],v2 offset:8192
v_cvt_pk_bf16_f32 v169,v170,v171
v_mfma_f32_16x16x32_bf16 a[100:103],a[204:207],v[236:239],a[100:103]
v_exp_f32 v173,v173
v_mfma_f32_16x16x32_bf16 a[100:103],a[200:203],v[232:235],a[100:103]
buffer_store_dwordx4 v[108:111],v6,s[36:39],s86 offen offset:3072 sc1
v_mfma_f32_16x16x32_bf16 a[104:107],a[208:211],v[232:235],a[104:107]
s_add_u32 s86,s55,147456
ds_read_b64_tr_b16 a[198:199],v2 offset:12288
ds_read_b64_tr_b16 a[204:205],v3 offset:8192
v_mfma_f32_16x16x32_bf16 a[104:107],a[212:215],v[236:239],a[104:107]
s_max_i32 s86,s86,s100
ds_read_b64_tr_b16 a[206:207],v3 offset:12288
v_cvt_pk_bf16_f32 v170,v172,v173
v_mfma_f32_16x16x32_bf16 a[108:111],a[220:223],v[236:239],a[108:111]
v_exp_f32 v174,v174
v_mfma_f32_16x16x32_bf16 a[108:111],a[216:219],v[232:235],a[108:111]
buffer_load_dwordx4 v10,s[72:75],s83 offen offset:3072 lds
v_mfma_f32_16x16x32_bf16 a[112:115],a[224:227],v[232:235],a[112:115]
ds_read_b64_tr_b16 a[200:201],v3
ds_read_b64_tr_b16 a[202:203],v3 offset:4096
v_mfma_f32_16x16x32_bf16 a[112:115],a[228:231],v[236:239],a[112:115]
v_exp_f32 v175,v175
v_mfma_f32_16x16x32_bf16 a[116:119],a[236:239],v[236:239],a[116:119]
ds_read_b64_tr_b16 a[208:209],v2 offset:256
ds_read_b64_tr_b16 a[210:211],v2 offset:4352
v_mfma_f32_16x16x32_bf16 a[116:119],a[232:235],v[232:235],a[116:119]
ds_read_b64_tr_b16 a[212:213],v2 offset:8448
v_cvt_pk_bf16_f32 v171,v174,v175
v_mfma_f32_16x16x32_bf16 a[120:123],a[240:243],v[232:235],a[120:123]
ds_read_b64_tr_b16 a[214:215],v2 offset:12544
v_mfma_f32_16x16x32_bf16 a[120:123],a[244:247],v[236:239],a[120:123]
ds_read_b64_tr_b16 a[220:221],v3 offset:8448
v_mfma_f32_16x16x32_bf16 a[124:127],a[252:255],v[236:239],a[124:127]
ds_read_b64_tr_b16 a[222:223],v3 offset:12544
ds_read_b64_tr_b16 a[216:217],v3 offset:256
v_mfma_f32_16x16x32_bf16 a[124:127],a[248:251],v[232:235],a[124:127]
ds_read_b64_tr_b16 a[218:219],v3 offset:4352
v_smfmac_f32_16x16x64_bf16 v[60:63],v[28:31],v[232:239],v27
ds_read_b64_tr_b16 a[224:225],v2 offset:512
v_mfma_f32_16x16x32_bf16 v[200:203],a[128:131],v[144:147],v[40:43]
ds_read_b64_tr_b16 a[226:227],v2 offset:4608
v_mfma_f32_16x16x32_bf16 v[200:203],a[132:135],v[176:179],v[200:203]
ds_read_b64_tr_b16 a[228:229],v2 offset:8704
ds_read_b64_tr_b16 a[230:231],v2 offset:12800
v_mfma_f32_16x16x32_bf16 v[200:203],a[136:139],v[208:211],v[200:203]
buffer_store_dwordx4 v[112:115],v6,s[36:39],s87 offen sc1
)A"
R"A(v_mfma_f32_16x16x32_bf16 v[200:203],a[140:143],v[240:243],v[200:203]
ds_read_b64_tr_b16 a[236:237],v3 offset:8704
ds_read_b64_tr_b16 a[238:239],v3 offset:12800
v_mfma_f32_16x16x32_bf16 v[204:207],a[156:159],v[240:243],v[40:43]
ds_read_b64_tr_b16 a[232:233],v3 offset:512
v_mfma_f32_16x16x32_bf16 v[204:207],a[152:155],v[208:211],v[204:207]
ds_read_b64_tr_b16 a[234:235],v3 offset:4608
v_mfma_f32_16x16x32_bf16 v[204:207],a[148:151],v[176:179],v[204:207]
ds_read_b64_tr_b16 a[240:241],v2 offset:768
v_mfma_f32_16x16x32_bf16 v[204:207],a[144:147],v[144:147],v[204:207]
s_addk_i32 m0,4096
ds_read_b64_tr_b16 a[242:243],v2 offset:4864
ds_read_b64_tr_b16 a[244:245],v2 offset:8960
v_mfma_f32_16x16x32_bf16 v[216:219],a[160:163],v[144:147],v[40:43]
ds_read_b64_tr_b16 a[246:247],v2 offset:13056
v_add_u32 v2,s48,v2
v_mfma_f32_16x16x32_bf16 v[216:219],a[164:167],v[176:179],v[216:219]
ds_read_b64_tr_b16 a[252:253],v3 offset:8960
v_mfma_f32_16x16x32_bf16 v[216:219],a[168:171],v[208:211],v[216:219]
buffer_load_dwordx4 v11,s[72:75],s83 offen lds
v_mfma_f32_16x16x32_bf16 v[216:219],a[172:175],v[240:243],v[216:219]
ds_read_b64_tr_b16 a[254:255],v3 offset:13056
ds_read_b64_tr_b16 a[248:249],v3 offset:768
v_mfma_f32_16x16x32_bf16 v[220:223],a[188:191],v[240:243],v[40:43]
ds_read_b64_tr_b16 a[250:251],v3 offset:4864
v_add_u32 v3,s48,v3
v_mfma_f32_16x16x32_bf16 v[220:223],a[184:187],v[208:211],v[220:223]
v_exp_f32 v184,v184
v_mfma_f32_16x16x32_bf16 v[220:223],a[180:183],v[176:179],v[220:223]
v_exp_f32 v185,v185
v_mfma_f32_16x16x32_bf16 v[220:223],a[176:179],v[144:147],v[220:223]
s_waitcnt lgkmcnt(5)
v_exp_f32 v186,v186
v_mfma_f32_16x16x32_bf16 a[0:3],a[192:195],v[136:139],a[0:3]
v_exp_f32 v187,v187
v_mfma_f32_16x16x32_bf16 a[0:3],a[196:199],v[140:143],a[0:3]
s_add_u32 s80,s71,0
s_and_b32 s80,s80,3
s_lshl_b32 s80,s80,1
v_mfma_f32_16x16x32_bf16 a[4:7],a[204:207],v[140:143],a[4:7]
s_or_b32 s80,s80,1
buffer_store_dwordx4 v[116:119],v6,s[36:39],s87 offen offset:1024 sc1
v_mfma_f32_16x16x32_bf16 a[4:7],a[200:203],v[136:139],a[4:7]
s_mul_i32 s80,s80,163840
s_add_u32 s80,s80,s81
v_cvt_pk_bf16_f32 v172,v184,v185
v_mfma_f32_16x16x32_bf16 a[8:11],a[208:211],v[136:139],a[8:11]
s_add_u32 s83,s80,20480
v_exp_f32 v188,v188
v_mfma_f32_16x16x32_bf16 a[8:11],a[212:215],v[140:143],a[8:11]
v_exp_f32 v189,v189
v_mfma_f32_16x16x32_bf16 a[12:15],a[220:223],v[140:143],a[12:15]
v_exp_f32 v190,v190
v_mfma_f32_16x16x32_bf16 a[12:15],a[216:219],v[136:139],a[12:15]
v_exp_f32 v191,v191
v_mfma_f32_16x16x32_bf16 a[16:19],a[224:227],v[136:139],a[16:19]
v_cvt_pk_bf16_f32 v173,v186,v187
v_cvt_pk_bf16_f32 v174,v188,v189
v_mfma_f32_16x16x32_bf16 a[16:19],a[228:231],v[140:143],a[16:19]
v_exp_f32 v200,v200
v_mfma_f32_16x16x32_bf16 a[20:23],a[236:239],v[140:143],a[20:23]
v_exp_f32 v201,v201
v_mfma_f32_16x16x32_bf16 a[20:23],a[232:235],v[136:139],a[20:23]
v_cvt_pk_bf16_f32 v175,v190,v191
v_cvt_pk_bf16_f32 v200,v200,v201
v_mfma_f32_16x16x32_bf16 a[24:27],a[240:243],v[136:139],a[24:27]
s_waitcnt lgkmcnt(0)
v_exp_f32 v202,v202
v_mfma_f32_16x16x32_bf16 a[24:27],a[244:247],v[140:143],a[24:27]
v_exp_f32 v203,v203
v_mfma_f32_16x16x32_bf16 a[28:31],a[252:255],v[140:143],a[28:31]
v_exp_f32 v204,v204
v_mfma_f32_16x16x32_bf16 a[28:31],a[248:251],v[136:139],a[28:31]
v_exp_f32 v205,v205
v_smfmac_f32_16x16x64_bf16 v[48:51],v[28:31],v[136:143],v27
v_cvt_pk_bf16_f32 v201,v202,v203
v_mfma_f32_16x16x32_bf16 v[232:235],a[128:131],v[148:151],v[44:47]
v_exp_f32 v206,v206
v_mfma_f32_16x16x32_bf16 v[232:235],a[132:135],v[180:183],v[232:235]
s_barrier
buffer_store_dwordx4 v[120:123],v6,s[36:39],s87 offen offset:2048 sc1
v_mfma_f32_16x16x32_bf16 v[232:235],a[136:139],v[212:215],v[232:235]
ds_read_b128 a[128:131],v1
v_cvt_pk_bf16_f32 v202,v204,v205
v_mfma_f32_16x16x32_bf16 v[232:235],a[140:143],v[244:247],v[232:235]
v_exp_f32 v207,v207
v_mfma_f32_16x16x32_bf16 v[236:239],a[156:159],v[244:247],v[44:47]
s_add_u32 m0,s82,0
ds_read_b128 a[132:135],v1 offset:256
v_cvt_pk_bf16_f32 v203,v206,v207
v_mfma_f32_16x16x32_bf16 v[236:239],a[152:155],v[212:215],v[236:239]
v_exp_f32 v216,v216
v_mfma_f32_16x16x32_bf16 v[236:239],a[148:151],v[180:183],v[236:239]
v_exp_f32 v217,v217
v_mfma_f32_16x16x32_bf16 v[236:239],a[144:147],v[148:151],v[236:239]
buffer_load_dwordx4 v7,s[72:75],s80 offen lds
v_mfma_f32_16x16x32_bf16 v[248:251],a[160:163],v[148:151],v[44:47]
ds_read_b128 a[136:139],v1 offset:512
v_cvt_pk_bf16_f32 v204,v216,v217
v_mfma_f32_16x16x32_bf16 v[248:251],a[164:167],v[180:183],v[248:251]
v_exp_f32 v218,v218
v_mfma_f32_16x16x32_bf16 v[248:251],a[168:171],v[212:215],v[248:251]
ds_read_b128 a[140:143],v1 offset:768
v_mfma_f32_16x16x32_bf16 v[248:251],a[172:175],v[244:247],v[248:251]
v_exp_f32 v219,v219
v_mfma_f32_16x16x32_bf16 v[252:255],a[188:191],v[244:247],v[44:47]
ds_read_b128 a[156:159],v1 offset:4864
v_cvt_pk_bf16_f32 v205,v218,v219
v_mfma_f32_16x16x32_bf16 v[252:255],a[184:187],v[212:215],v[252:255]
v_exp_f32 v220,v220
v_mfma_f32_16x16x32_bf16 v[252:255],a[180:183],v[180:183],v[252:255]
ds_read_b128 a[152:155],v1 offset:4608
v_mfma_f32_16x16x32_bf16 v[252:255],a[176:179],v[148:151],v[252:255]
v_exp_f32 v221,v221
v_mfma_f32_16x16x32_bf16 a[32:35],a[192:195],v[168:171],a[32:35]
buffer_store_dwordx4 v[124:127],v6,s[36:39],s87 offen offset:3072 sc1
v_mfma_f32_16x16x32_bf16 a[32:35],a[196:199],v[172:175],a[32:35]
s_add_u32 s87,s55,151552
ds_read_b128 a[148:151],v1 offset:4352
v_cvt_pk_bf16_f32 v206,v220,v221
v_mfma_f32_16x16x32_bf16 a[36:39],a[204:207],v[172:175],a[36:39]
s_max_i32 s87,s87,s101
v_exp_f32 v222,v222
v_mfma_f32_16x16x32_bf16 a[36:39],a[200:203],v[168:171],a[36:39]
ds_read_b128 a[144:147],v1 offset:4096
v_mfma_f32_16x16x32_bf16 a[40:43],a[208:211],v[168:171],a[40:43]
buffer_load_dwordx4 v8,s[72:75],s80 offen offset:1024 lds
v_mfma_f32_16x16x32_bf16 a[40:43],a[212:215],v[172:175],a[40:43]
ds_read_b128 a[160:163],v1 offset:8192
v_mfma_f32_16x16x32_bf16 a[44:47],a[220:223],v[172:175],a[44:47]
v_exp_f32 v223,v223
v_mfma_f32_16x16x32_bf16 a[44:47],a[216:219],v[168:171],a[44:47]
ds_read_b128 a[164:167],v1 offset:8448
v_cvt_pk_bf16_f32 v207,v222,v223
v_mfma_f32_16x16x32_bf16 a[48:51],a[224:227],v[168:171],a[48:51]
v_exp_f32 v232,v232
v_mfma_f32_16x16x32_bf16 a[48:51],a[228:231],v[172:175],a[48:51]
ds_read_b128 a[168:171],v1 offset:8704
v_mfma_f32_16x16x32_bf16 a[52:55],a[236:239],v[172:175],a[52:55]
v_exp_f32 v233,v233
v_mfma_f32_16x16x32_bf16 a[52:55],a[232:235],v[168:171],a[52:55]
ds_read_b128 a[172:175],v1 offset:8960
v_cvt_pk_bf16_f32 v232,v232,v233
v_mfma_f32_16x16x32_bf16 a[56:59],a[240:243],v[168:171],a[56:59]
v_exp_f32 v234,v234
v_mfma_f32_16x16x32_bf16 a[56:59],a[244:247],v[172:175],a[56:59]
buffer_load_dwordx4 v9,s[72:75],s80 offen offset:2048 lds
v_mfma_f32_16x16x32_bf16 a[60:63],a[252:255],v[172:175],a[60:63]
ds_read_b128 a[188:191],v1 offset:13056
v_mfma_f32_16x16x32_bf16 a[60:63],a[248:251],v[168:171],a[60:63]
v_exp_f32 v235,v235
v_smfmac_f32_16x16x64_bf16 v[52:55],v[28:31],v[168:175],v27
s_waitcnt lgkmcnt(3)
ds_read_b128 a[184:187],v1 offset:12800
v_mfma_f32_16x16x32_bf16 v[136:139],a[128:131],v[128:131],v[32:35]
v_exp_f32 v236,v236
v_mfma_f32_16x16x32_bf16 v[136:139],a[132:135],v[160:163],v[136:139]
ds_read_b128 a[180:183],v1 offset:12544
v_cvt_pk_bf16_f32 v233,v234,v235
v_mfma_f32_16x16x32_bf16 v[136:139],a[136:139],v[192:195],v[136:139]
v_exp_f32 v237,v237
v_mfma_f32_16x16x32_bf16 v[136:139],a[140:143],v[224:227],v[136:139]
ds_read_b128 a[176:179],v1 offset:12288
v_cvt_pk_bf16_f32 v234,v236,v237
v_mfma_f32_16x16x32_bf16 v[140:143],a[156:159],v[224:227],v[32:35]
v_exp_f32 v238,v238
v_mfma_f32_16x16x32_bf16 v[140:143],a[152:155],v[192:195],v[140:143]
v_exp_f32 v239,v239
v_mfma_f32_16x16x32_bf16 v[140:143],a[148:151],v[160:163],v[140:143]
v_exp_f32 v248,v248
v_mfma_f32_16x16x32_bf16 v[140:143],a[144:147],v[128:131],v[140:143]
buffer_load_dwordx4 v10,s[72:75],s80 offen offset:3072 lds
v_mfma_f32_16x16x32_bf16 v[152:155],a[160:163],v[128:131],v[32:35]
v_exp_f32 v249,v249
v_mfma_f32_16x16x32_bf16 v[152:155],a[164:167],v[160:163],v[152:155]
s_waitcnt lgkmcnt(1)
v_cvt_pk_bf16_f32 v235,v238,v239
v_cvt_pk_bf16_f32 v236,v248,v249
v_mfma_f32_16x16x32_bf16 v[152:155],a[168:171],v[192:195],v[152:155]
v_exp_f32 v250,v250
v_mfma_f32_16x16x32_bf16 v[152:155],a[172:175],v[224:227],v[152:155]
v_exp_f32 v251,v251
v_mfma_f32_16x16x32_bf16 v[156:159],a[188:191],v[224:227],v[32:35]
v_exp_f32 v252,v252
v_mfma_f32_16x16x32_bf16 v[156:159],a[184:187],v[192:195],v[156:159]
v_exp_f32 v253,v253
v_mfma_f32_16x16x32_bf16 v[156:159],a[180:183],v[160:163],v[156:159]
s_waitcnt lgkmcnt(0)
v_exp_f32 v254,v254
v_mfma_f32_16x16x32_bf16 v[156:159],a[176:179],v[128:131],v[156:159]
s_addk_i32 m0,4096
v_exp_f32 v255,v255
v_mfma_f32_16x16x32_bf16 a[64:67],a[192:195],v[200:203],a[64:67]
v_cvt_pk_bf16_f32 v237,v250,v251
v_cvt_pk_bf16_f32 v238,v252,v253
v_mfma_f32_16x16x32_bf16 a[64:67],a[196:199],v[204:207],a[64:67]
v_cvt_pk_bf16_f32 v239,v254,v255
v_bfe_u32 v32,v6,4,2
v_mfma_f32_16x16x32_bf16 a[68:71],a[204:207],v[204:207],a[68:71]
buffer_load_dwordx4 v11,s[72:75],s80 offen lds
v_mfma_f32_16x16x32_bf16 a[68:71],a[200:203],v[200:203],a[68:71]
v_exp_f32 v136,v136
v_mfma_f32_16x16x32_bf16 a[72:75],a[208:211],v[200:203],a[72:75]
v_exp_f32 v137,v137
v_mfma_f32_16x16x32_bf16 a[72:75],a[212:215],v[204:207],a[72:75]
v_exp_f32 v138,v138
v_mfma_f32_16x16x32_bf16 a[76:79],a[220:223],v[204:207],a[76:79]
v_exp_f32 v139,v139
v_mfma_f32_16x16x32_bf16 a[76:79],a[216:219],v[200:203],a[76:79]
v_cvt_pk_bf16_f32 v136,v136,v137
v_cvt_pk_bf16_f32 v137,v138,v139
v_mfma_f32_16x16x32_bf16 a[80:83],a[224:227],v[200:203],a[80:83]
v_exp_f32 v140,v140
v_mfma_f32_16x16x32_bf16 a[80:83],a[228:231],v[204:207],a[80:83]
v_exp_f32 v141,v141
v_mfma_f32_16x16x32_bf16 a[84:87],a[236:239],v[204:207],a[84:87]
s_addk_i32 m0,1024
v_exp_f32 v142,v142
v_mfma_f32_16x16x32_bf16 a[84:87],a[232:235],v[200:203],a[84:87]
v_exp_f32 v143,v143
v_mfma_f32_16x16x32_bf16 a[88:91],a[240:243],v[200:203],a[88:91]
v_cvt_pk_bf16_f32 v138,v140,v141
v_cvt_pk_bf16_f32 v139,v142,v143
v_mfma_f32_16x16x32_bf16 a[88:91],a[244:247],v[204:207],a[88:91]
buffer_load_dwordx4 v7,s[72:75],s83 offen lds
v_mfma_f32_16x16x32_bf16 a[92:95],a[252:255],v[204:207],a[92:95]
v_exp_f32 v152,v152
v_mfma_f32_16x16x32_bf16 a[92:95],a[248:251],v[200:203],a[92:95]
v_exp_f32 v153,v153
v_smfmac_f32_16x16x64_bf16 v[56:59],v[28:31],v[200:207],v27
v_cvt_pk_bf16_f32 v140,v152,v153
v_mfma_f32_16x16x32_bf16 v[168:171],a[128:131],v[132:135],v[36:39]
s_lshl_b32 s95,s42,2
s_add_u32 s95,s95,s40
v_bfe_u32 v19,s55,13,1
v_mfma_f32_16x16x32_bf16 v[168:171],a[132:135],v[164:167],v[168:171]
s_lshl_b32 s95,s95,4
v_mov_b32 v18,s68
v_or_b32 v19,s69,v19
v_mfma_f32_16x16x32_bf16 v[168:171],a[136:139],v[196:199],v[168:171]
v_exp_f32 v154,v154
v_mfma_f32_16x16x32_bf16 v[168:171],a[140:143],v[228:231],v[168:171]
v_exp_f32 v155,v155
v_mfma_f32_16x16x32_bf16 v[172:175],a[156:159],v[228:231],v[36:39]
v_exp_f32 v156,v156
v_mfma_f32_16x16x32_bf16 v[172:175],a[152:155],v[196:199],v[172:175]
v_exp_f32 v157,v157
v_mfma_f32_16x16x32_bf16 v[172:175],a[148:151],v[164:167],v[172:175]
v_exp_f32 v158,v158
v_mfma_f32_16x16x32_bf16 v[172:175],a[144:147],v[132:135],v[172:175]
buffer_load_dwordx4 v8,s[72:75],s83 offen offset:1024 lds
v_mfma_f32_16x16x32_bf16 v[184:187],a[160:163],v[132:135],v[36:39]
v_exp_f32 v159,v159
v_mfma_f32_16x16x32_bf16 v[184:187],a[164:167],v[164:167],v[184:187]
v_cvt_pk_bf16_f32 v141,v154,v155
v_cvt_pk_bf16_f32 v142,v156,v157
v_mfma_f32_16x16x32_bf16 v[184:187],a[168:171],v[196:199],v[184:187]
v_cvt_pk_bf16_f32 v143,v158,v159
v_lshl_add_u32 v25,v32,3,v25
v_mfma_f32_16x16x32_bf16 v[184:187],a[172:175],v[228:231],v[184:187]
s_waitcnt vmcnt(6)
s_mov_b64 exec,1
buffer_store_dwordx2 v[18:19],v6,s[64:67],s95 offen sc1
s_mov_b64 exec,-1
v_mfma_f32_16x16x32_bf16 v[188:191],a[188:191],v[228:231],v[36:39]
v_exp_f32 v168,v168
v_mfma_f32_16x16x32_bf16 v[188:191],a[184:187],v[196:199],v[188:191]
v_exp_f32 v169,v169
v_mfma_f32_16x16x32_bf16 v[188:191],a[180:183],v[164:167],v[188:191]
v_exp_f32 v170,v170
v_mfma_f32_16x16x32_bf16 v[188:191],a[176:179],v[132:135],v[188:191]
)A"
R"A(v_exp_f32 v171,v171
v_mfma_f32_16x16x32_bf16 a[96:99],a[192:195],v[232:235],a[96:99]
ds_read_b64_tr_b16 a[192:193],v2
ds_read_b64_tr_b16 a[194:195],v2 offset:4096
v_mfma_f32_16x16x32_bf16 a[96:99],a[196:199],v[236:239],a[96:99]
ds_read_b64_tr_b16 a[196:197],v2 offset:8192
v_cvt_pk_bf16_f32 v168,v168,v169
v_mfma_f32_16x16x32_bf16 a[100:103],a[204:207],v[236:239],a[100:103]
buffer_load_dwordx4 v9,s[72:75],s83 offen offset:2048 lds
v_mfma_f32_16x16x32_bf16 a[100:103],a[200:203],v[232:235],a[100:103]
ds_read_b64_tr_b16 a[198:199],v2 offset:12288
ds_read_b64_tr_b16 a[204:205],v3 offset:8192
v_mfma_f32_16x16x32_bf16 a[104:107],a[208:211],v[232:235],a[104:107]
ds_read_b64_tr_b16 a[206:207],v3 offset:12288
v_cvt_pk_bf16_f32 v169,v170,v171
v_mfma_f32_16x16x32_bf16 a[104:107],a[212:215],v[236:239],a[104:107]
ds_read_b64_tr_b16 a[200:201],v3
v_bfe_u32 v32,v6,8,1
v_mfma_f32_16x16x32_bf16 a[108:111],a[220:223],v[236:239],a[108:111]
ds_read_b64_tr_b16 a[202:203],v3 offset:4096
v_lshlrev_b32 v32,5,v32
v_mfma_f32_16x16x32_bf16 a[108:111],a[216:219],v[232:235],a[108:111]
ds_read_b64_tr_b16 a[208:209],v2 offset:256
ds_read_b64_tr_b16 a[210:211],v2 offset:4352
v_mfma_f32_16x16x32_bf16 a[112:115],a[224:227],v[232:235],a[112:115]
ds_read_b64_tr_b16 a[212:213],v2 offset:8448
v_add_u32 v1,v25,v32
v_mfma_f32_16x16x32_bf16 a[112:115],a[228:231],v[236:239],a[112:115]
ds_read_b64_tr_b16 a[214:215],v2 offset:12544
v_add_u32 v1,81920,v1
v_mfma_f32_16x16x32_bf16 a[116:119],a[236:239],v[236:239],a[116:119]
v_exp_f32 v172,v172
v_mfma_f32_16x16x32_bf16 a[116:119],a[232:235],v[232:235],a[116:119]
ds_read_b64_tr_b16 a[220:221],v3 offset:8448
ds_read_b64_tr_b16 a[222:223],v3 offset:12544
v_mfma_f32_16x16x32_bf16 a[120:123],a[240:243],v[232:235],a[120:123]
s_cmp_ge_u32 0,s71
s_cselect_b32 s80,2,0
ds_read_b64_tr_b16 a[216:217],v3 offset:256
v_mfma_f32_16x16x32_bf16 a[120:123],a[244:247],v[236:239],a[120:123]
s_mul_i32 s80,s80,163840
buffer_load_dwordx4 v10,s[72:75],s83 offen offset:3072 lds
v_mfma_f32_16x16x32_bf16 a[124:127],a[252:255],v[236:239],a[124:127]
s_add_u32 s80,s80,s81
ds_read_b64_tr_b16 a[218:219],v3 offset:4352
ds_read_b64_tr_b16 a[224:225],v2 offset:512
v_mfma_f32_16x16x32_bf16 a[124:127],a[248:251],v[232:235],a[124:127]
ds_read_b64_tr_b16 a[226:227],v2 offset:4608
v_smfmac_f32_16x16x64_bf16 v[60:63],v[28:31],v[232:239],v27
ds_read_b64_tr_b16 a[228:229],v2 offset:8704
v_mfma_f32_16x16x32_bf16 v[200:203],a[128:131],v[144:147],v[40:43]
ds_read_b64_tr_b16 a[230:231],v2 offset:12800
v_mfma_f32_16x16x32_bf16 v[200:203],a[132:135],v[176:179],v[200:203]
ds_read_b64_tr_b16 a[236:237],v3 offset:8704
ds_read_b64_tr_b16 a[238:239],v3 offset:12800
v_mfma_f32_16x16x32_bf16 v[200:203],a[136:139],v[208:211],v[200:203]
ds_read_b64_tr_b16 a[232:233],v3 offset:512
v_mfma_f32_16x16x32_bf16 v[200:203],a[140:143],v[240:243],v[200:203]
ds_read_b64_tr_b16 a[234:235],v3 offset:4608
v_mfma_f32_16x16x32_bf16 v[204:207],a[156:159],v[240:243],v[40:43]
s_addk_i32 m0,4096
ds_read_b64_tr_b16 a[240:241],v2 offset:768
v_mfma_f32_16x16x32_bf16 v[204:207],a[152:155],v[208:211],v[204:207]
ds_read_b64_tr_b16 a[242:243],v2 offset:4864
ds_read_b64_tr_b16 a[244:245],v2 offset:8960
v_mfma_f32_16x16x32_bf16 v[204:207],a[148:151],v[176:179],v[204:207]
ds_read_b64_tr_b16 a[246:247],v2 offset:13056
v_sub_u32 v2,v25,v32
v_mfma_f32_16x16x32_bf16 v[204:207],a[144:147],v[144:147],v[204:207]
buffer_load_dwordx4 v11,s[72:75],s83 offen lds
v_mfma_f32_16x16x32_bf16 v[216:219],a[160:163],v[144:147],v[40:43]
s_add_u32 s83,s80,20480
ds_read_b64_tr_b16 a[252:253],v3 offset:8960
ds_read_b64_tr_b16 a[254:255],v3 offset:13056
v_mfma_f32_16x16x32_bf16 v[216:219],a[164:167],v[176:179],v[216:219]
ds_read_b64_tr_b16 a[248:249],v3 offset:768
v_add_u32 v2,81920,v2
v_mfma_f32_16x16x32_bf16 v[216:219],a[168:171],v[208:211],v[216:219]
ds_read_b64_tr_b16 a[250:251],v3 offset:4864
v_mfma_f32_16x16x32_bf16 v[216:219],a[172:175],v[240:243],v[216:219]
v_exp_f32 v173,v173
v_mfma_f32_16x16x32_bf16 v[220:223],a[188:191],v[240:243],v[40:43]
v_exp_f32 v174,v174
v_mfma_f32_16x16x32_bf16 v[220:223],a[184:187],v[208:211],v[220:223]
v_exp_f32 v175,v175
v_mfma_f32_16x16x32_bf16 v[220:223],a[180:183],v[176:179],v[220:223]
v_cvt_pk_bf16_f32 v170,v172,v173
v_cvt_pk_bf16_f32 v171,v174,v175
v_mfma_f32_16x16x32_bf16 v[220:223],a[176:179],v[144:147],v[220:223]
s_add_u32 m0,s82,40960
v_exp_f32 v184,v184
s_waitcnt lgkmcnt(4)
v_mfma_f32_16x16x32_bf16 a[0:3],a[192:195],v[136:139],a[0:3]
v_exp_f32 v185,v185
v_mfma_f32_16x16x32_bf16 a[0:3],a[196:199],v[140:143],a[0:3]
v_exp_f32 v186,v186
v_mfma_f32_16x16x32_bf16 a[4:7],a[204:207],v[140:143],a[4:7]
buffer_load_dwordx4 v7,s[72:75],s80 offen lds
v_mfma_f32_16x16x32_bf16 a[4:7],a[200:203],v[136:139],a[4:7]
v_exp_f32 v187,v187
v_mfma_f32_16x16x32_bf16 a[8:11],a[208:211],v[136:139],a[8:11]
v_exp_f32 v188,v188
v_mfma_f32_16x16x32_bf16 a[8:11],a[212:215],v[140:143],a[8:11]
v_exp_f32 v189,v189
v_mfma_f32_16x16x32_bf16 a[12:15],a[220:223],v[140:143],a[12:15]
v_exp_f32 v190,v190
v_mfma_f32_16x16x32_bf16 a[12:15],a[216:219],v[136:139],a[12:15]
v_exp_f32 v191,v191
v_mfma_f32_16x16x32_bf16 a[16:19],a[224:227],v[136:139],a[16:19]
v_cvt_pk_bf16_f32 v172,v184,v185
v_cvt_pk_bf16_f32 v173,v186,v187
v_mfma_f32_16x16x32_bf16 a[16:19],a[228:231],v[140:143],a[16:19]
v_cvt_pk_bf16_f32 v174,v188,v189
v_cvt_pk_bf16_f32 v175,v190,v191
v_mfma_f32_16x16x32_bf16 a[20:23],a[236:239],v[140:143],a[20:23]
v_exp_f32 v200,v200
v_mfma_f32_16x16x32_bf16 a[20:23],a[232:235],v[136:139],a[20:23]
v_exp_f32 v201,v201
v_mfma_f32_16x16x32_bf16 a[24:27],a[240:243],v[136:139],a[24:27]
v_exp_f32 v202,v202
v_mfma_f32_16x16x32_bf16 a[24:27],a[244:247],v[140:143],a[24:27]
s_waitcnt lgkmcnt(0)
buffer_load_dwordx4 v8,s[72:75],s80 offen offset:1024 lds
v_mfma_f32_16x16x32_bf16 a[28:31],a[252:255],v[140:143],a[28:31]
v_exp_f32 v203,v203
v_mfma_f32_16x16x32_bf16 a[28:31],a[248:251],v[136:139],a[28:31]
v_cvt_pk_bf16_f32 v200,v200,v201
v_cvt_pk_bf16_f32 v201,v202,v203
v_smfmac_f32_16x16x64_bf16 v[48:51],v[28:31],v[136:143],v27
s_barrier
v_exp_f32 v204,v204
v_mfma_f32_16x16x32_bf16 v[232:235],a[128:131],v[148:151],v[44:47]
v_exp_f32 v205,v205
v_mfma_f32_16x16x32_bf16 v[232:235],a[132:135],v[180:183],v[232:235]
v_exp_f32 v206,v206
v_mfma_f32_16x16x32_bf16 v[232:235],a[136:139],v[212:215],v[232:235]
v_exp_f32 v207,v207
v_mfma_f32_16x16x32_bf16 v[232:235],a[140:143],v[244:247],v[232:235]
v_cvt_pk_bf16_f32 v202,v204,v205
v_cvt_pk_bf16_f32 v203,v206,v207
v_mfma_f32_16x16x32_bf16 v[236:239],a[156:159],v[244:247],v[44:47]
v_exp_f32 v216,v216
v_mfma_f32_16x16x32_bf16 v[236:239],a[152:155],v[212:215],v[236:239]
v_exp_f32 v217,v217
v_mfma_f32_16x16x32_bf16 v[236:239],a[148:151],v[180:183],v[236:239]
buffer_load_dwordx4 v9,s[72:75],s80 offen offset:2048 lds
v_mfma_f32_16x16x32_bf16 v[236:239],a[144:147],v[148:151],v[236:239]
v_exp_f32 v218,v218
v_mfma_f32_16x16x32_bf16 v[248:251],a[160:163],v[148:151],v[44:47]
v_exp_f32 v219,v219
v_mfma_f32_16x16x32_bf16 v[248:251],a[164:167],v[180:183],v[248:251]
v_exp_f32 v220,v220
v_mfma_f32_16x16x32_bf16 v[248:251],a[168:171],v[212:215],v[248:251]
v_exp_f32 v221,v221
v_mfma_f32_16x16x32_bf16 v[248:251],a[172:175],v[244:247],v[248:251]
v_exp_f32 v222,v222
v_mfma_f32_16x16x32_bf16 v[252:255],a[188:191],v[244:247],v[44:47]
v_exp_f32 v223,v223
v_mfma_f32_16x16x32_bf16 v[252:255],a[184:187],v[212:215],v[252:255]
v_cvt_pk_bf16_f32 v204,v216,v217
v_cvt_pk_bf16_f32 v205,v218,v219
v_mfma_f32_16x16x32_bf16 v[252:255],a[180:183],v[180:183],v[252:255]
v_cvt_pk_bf16_f32 v206,v220,v221
v_cvt_pk_bf16_f32 v207,v222,v223
v_mfma_f32_16x16x32_bf16 v[252:255],a[176:179],v[148:151],v[252:255]
v_exp_f32 v232,v232
v_mfma_f32_16x16x32_bf16 a[32:35],a[192:195],v[168:171],a[32:35]
v_exp_f32 v233,v233
v_mfma_f32_16x16x32_bf16 a[32:35],a[196:199],v[172:175],a[32:35]
buffer_load_dwordx4 v10,s[72:75],s80 offen offset:3072 lds
v_mfma_f32_16x16x32_bf16 a[36:39],a[204:207],v[172:175],a[36:39]
v_exp_f32 v234,v234
v_mfma_f32_16x16x32_bf16 a[36:39],a[200:203],v[168:171],a[36:39]
v_exp_f32 v235,v235
v_mfma_f32_16x16x32_bf16 a[40:43],a[208:211],v[168:171],a[40:43]
v_cvt_pk_bf16_f32 v232,v232,v233
v_add_f32 v48,v49,v48
v_mfma_f32_16x16x32_bf16 a[40:43],a[212:215],v[172:175],a[40:43]
v_exp_f32 v236,v236
v_mfma_f32_16x16x32_bf16 a[44:47],a[220:223],v[172:175],a[44:47]
v_exp_f32 v237,v237
v_mfma_f32_16x16x32_bf16 a[44:47],a[216:219],v[168:171],a[44:47]
v_exp_f32 v238,v238
v_mfma_f32_16x16x32_bf16 a[48:51],a[224:227],v[168:171],a[48:51]
v_exp_f32 v239,v239
v_mfma_f32_16x16x32_bf16 a[48:51],a[228:231],v[172:175],a[48:51]
s_addk_i32 m0,4096
v_cvt_pk_bf16_f32 v233,v234,v235
v_max_f32 v20,0x00800000,v48
v_mfma_f32_16x16x32_bf16 a[52:55],a[236:239],v[172:175],a[52:55]
v_cvt_pk_bf16_f32 v234,v236,v237
v_cvt_pk_bf16_f32 v235,v238,v239
v_mfma_f32_16x16x32_bf16 a[52:55],a[232:235],v[168:171],a[52:55]
v_exp_f32 v248,v248
v_mfma_f32_16x16x32_bf16 a[56:59],a[240:243],v[168:171],a[56:59]
buffer_load_dwordx4 v11,s[72:75],s80 offen lds
v_mfma_f32_16x16x32_bf16 a[56:59],a[244:247],v[172:175],a[56:59]
v_exp_f32 v249,v249
v_mfma_f32_16x16x32_bf16 a[60:63],a[252:255],v[172:175],a[60:63]
v_exp_f32 v250,v250
v_mfma_f32_16x16x32_bf16 a[60:63],a[248:251],v[168:171],a[60:63]
v_exp_f32 v251,v251
v_smfmac_f32_16x16x64_bf16 v[52:55],v[28:31],v[168:175],v27
v_cvt_pk_bf16_f32 v236,v248,v249
v_mfma_f32_16x16x32_bf16 a[64:67],a[192:195],v[200:203],a[64:67]
v_exp_f32 v252,v252
v_mfma_f32_16x16x32_bf16 a[64:67],a[196:199],v[204:207],a[64:67]
v_exp_f32 v253,v253
v_mfma_f32_16x16x32_bf16 a[68:71],a[204:207],v[204:207],a[68:71]
v_exp_f32 v254,v254
v_mfma_f32_16x16x32_bf16 a[68:71],a[200:203],v[200:203],a[68:71]
s_addk_i32 m0,1024
v_exp_f32 v255,v255
v_mfma_f32_16x16x32_bf16 a[72:75],a[208:211],v[200:203],a[72:75]
v_rcp_f32 v20,v20
v_mfma_f32_16x16x32_bf16 a[72:75],a[212:215],v[204:207],a[72:75]
v_cvt_pk_bf16_f32 v237,v250,v251
v_cvt_pk_bf16_f32 v238,v252,v253
v_mfma_f32_16x16x32_bf16 a[76:79],a[220:223],v[204:207],a[76:79]
buffer_load_dwordx4 v7,s[72:75],s83 offen lds
v_mfma_f32_16x16x32_bf16 a[76:79],a[216:219],v[200:203],a[76:79]
ds_read_b64_tr_b16 a[128:129],v1
v_cvt_pk_bf16_f32 v239,v254,v255
v_mfma_f32_16x16x32_bf16 a[80:83],a[224:227],v[200:203],a[80:83]
v_accvgpr_read_b32 v136,a0
v_accvgpr_read_b32 v137,a1
v_accvgpr_read_b32 v138,a2
v_accvgpr_read_b32 v139,a3
v_accvgpr_read_b32 v12,a4
v_accvgpr_read_b32 v13,a5
v_accvgpr_read_b32 v14,a6
v_accvgpr_read_b32 v15,a7
v_pk_mul_f32 v[136:137],v[20:21],v[136:137] op_sel:[0,0] op_sel_hi:[0,1]
v_pk_mul_f32 v[138:139],v[20:21],v[138:139] op_sel:[0,0] op_sel_hi:[0,1]
v_pk_mul_f32 v[12:13],v[20:21],v[12:13] op_sel:[0,0] op_sel_hi:[0,1]
v_pk_mul_f32 v[14:15],v[20:21],v[14:15] op_sel:[0,0] op_sel_hi:[0,1]
v_cvt_pk_bf16_f32 v136,v136,v137
v_cvt_pk_bf16_f32 v137,v138,v139
v_cvt_pk_bf16_f32 v138,v12,v13
v_cvt_pk_bf16_f32 v139,v14,v15
v_mfma_f32_16x16x32_bf16 a[80:83],a[228:231],v[204:207],a[80:83]
ds_read_b64_tr_b16 a[130:131],v1 offset:5120
ds_read_b64_tr_b16 a[132:133],v1 offset:10240
v_mfma_f32_16x16x32_bf16 a[84:87],a[236:239],v[204:207],a[84:87]
ds_read_b64_tr_b16 a[134:135],v1 offset:15360
v_accvgpr_read_b32 v152,a16
v_mfma_f32_16x16x32_bf16 a[84:87],a[232:235],v[200:203],a[84:87]
ds_read_b64_tr_b16 a[136:137],v1 offset:20480
v_accvgpr_read_b32 v153,a17
v_mfma_f32_16x16x32_bf16 a[88:91],a[240:243],v[200:203],a[88:91]
ds_read_b64_tr_b16 a[138:139],v1 offset:25600
v_mul_f32 v152,v20,v152
v_mfma_f32_16x16x32_bf16 a[88:91],a[244:247],v[204:207],a[88:91]
v_accvgpr_read_b32 v140,a8
v_accvgpr_read_b32 v141,a9
v_accvgpr_read_b32 v142,a10
v_accvgpr_read_b32 v143,a11
v_accvgpr_read_b32 v12,a12
v_accvgpr_read_b32 v13,a13
v_accvgpr_read_b32 v14,a14
v_accvgpr_read_b32 v15,a15
v_pk_mul_f32 v[140:141],v[20:21],v[140:141] op_sel:[0,0] op_sel_hi:[0,1]
v_pk_mul_f32 v[142:143],v[20:21],v[142:143] op_sel:[0,0] op_sel_hi:[0,1]
v_pk_mul_f32 v[12:13],v[20:21],v[12:13] op_sel:[0,0] op_sel_hi:[0,1]
v_pk_mul_f32 v[14:15],v[20:21],v[14:15] op_sel:[0,0] op_sel_hi:[0,1]
v_cvt_pk_bf16_f32 v140,v140,v141
v_cvt_pk_bf16_f32 v141,v142,v143
)A"
R"A(v_cvt_pk_bf16_f32 v142,v12,v13
v_cvt_pk_bf16_f32 v143,v14,v15
v_mfma_f32_16x16x32_bf16 a[92:95],a[252:255],v[204:207],a[92:95]
ds_read_b64_tr_b16 a[140:141],v1 offset:30720
ds_read_b64_tr_b16 a[142:143],v1 offset:35840
v_mfma_f32_16x16x32_bf16 a[92:95],a[248:251],v[200:203],a[92:95]
v_mul_f32 v153,v20,v153
v_accvgpr_read_b32 v154,a18
v_smfmac_f32_16x16x64_bf16 v[56:59],v[28:31],v[200:207],v27
v_accvgpr_read_b32 v155,a19
v_mfma_f32_16x16x32_bf16 a[96:99],a[192:195],v[232:235],a[96:99]
buffer_store_dwordx4 v[136:139],v6,s[36:39],s84 offen sc1
v_mfma_f32_16x16x32_bf16 a[96:99],a[196:199],v[236:239],a[96:99]
v_cvt_pk_bf16_f32 v152,v152,v153
v_mul_f32 v154,v20,v154
v_mfma_f32_16x16x32_bf16 a[100:103],a[204:207],v[236:239],a[100:103]
v_mul_f32 v155,v20,v155
v_cvt_pk_bf16_f32 v153,v154,v155
v_mfma_f32_16x16x32_bf16 a[100:103],a[200:203],v[232:235],a[100:103]
v_accvgpr_read_b32 v154,a20
v_accvgpr_read_b32 v155,a21
v_mfma_f32_16x16x32_bf16 a[104:107],a[208:211],v[232:235],a[104:107]
buffer_load_dwordx4 v8,s[72:75],s83 offen offset:1024 lds
v_mfma_f32_16x16x32_bf16 a[104:107],a[212:215],v[236:239],a[104:107]
v_accvgpr_read_b32 v12,a22
v_accvgpr_read_b32 v13,a23
v_mfma_f32_16x16x32_bf16 a[108:111],a[220:223],v[236:239],a[108:111]
v_mul_f32 v154,v20,v154
v_mul_f32 v155,v20,v155
v_mfma_f32_16x16x32_bf16 a[108:111],a[216:219],v[232:235],a[108:111]
v_mul_f32 v12,v20,v12
v_mul_f32 v13,v20,v13
v_mfma_f32_16x16x32_bf16 a[112:115],a[224:227],v[232:235],a[112:115]
v_cvt_pk_bf16_f32 v154,v154,v155
v_cvt_pk_bf16_f32 v155,v12,v13
v_mfma_f32_16x16x32_bf16 a[112:115],a[228:231],v[236:239],a[112:115]
ds_read_b64_tr_b16 a[144:145],v2 offset:32
ds_read_b64_tr_b16 a[146:147],v2 offset:5152
v_mfma_f32_16x16x32_bf16 a[116:119],a[236:239],v[236:239],a[116:119]
ds_read_b64_tr_b16 a[148:149],v2 offset:10272
v_accvgpr_read_b32 v156,a24
v_mfma_f32_16x16x32_bf16 a[116:119],a[232:235],v[232:235],a[116:119]
buffer_load_dwordx4 v9,s[72:75],s83 offen offset:2048 lds
v_mfma_f32_16x16x32_bf16 a[120:123],a[240:243],v[232:235],a[120:123]
ds_read_b64_tr_b16 a[150:151],v2 offset:15392
ds_read_b64_tr_b16 a[152:153],v2 offset:20512
v_mfma_f32_16x16x32_bf16 a[120:123],a[244:247],v[236:239],a[120:123]
ds_read_b64_tr_b16 a[154:155],v2 offset:25632
v_accvgpr_read_b32 v157,a25
v_mfma_f32_16x16x32_bf16 a[124:127],a[252:255],v[236:239],a[124:127]
ds_read_b64_tr_b16 a[156:157],v2 offset:30752
v_mul_f32 v156,v20,v156
v_mfma_f32_16x16x32_bf16 a[124:127],a[248:251],v[232:235],a[124:127]
ds_read_b64_tr_b16 a[158:159],v2 offset:35872
v_mul_f32 v157,v20,v157
v_smfmac_f32_16x16x64_bf16 v[60:63],v[28:31],v[232:239],v27
s_waitcnt lgkmcnt(8)
v_accvgpr_read_b32 v158,a26
v_mfma_f32_16x16x32_bf16 a[192:195],a[128:131],v[64:67],0
s_lshl_b32 s94,s70,4
v_accvgpr_read_b32 v159,a27
v_cvt_pk_bf16_f32 v156,v156,v157
v_mfma_f32_16x16x32_bf16 a[192:195],a[132:135],v[68:71],a[192:195]
s_add_u32 s94,s94,s40
v_mul_f32 v158,v20,v158
v_mul_f32 v159,v20,v159
v_mfma_f32_16x16x32_bf16 a[192:195],a[136:139],v[72:75],a[192:195]
s_lshl_b32 s94,s94,4
buffer_store_dwordx4 v[140:143],v6,s[36:39],s84 offen offset:1024 sc1
v_mfma_f32_16x16x32_bf16 a[192:195],a[140:143],v[76:79],a[192:195]
v_cvt_pk_bf16_f32 v157,v158,v159
v_accvgpr_read_b32 v158,a28
v_mfma_f32_16x16x32_bf16 a[196:199],a[140:143],v[92:95],0
v_accvgpr_read_b32 v159,a29
v_accvgpr_read_b32 v14,a30
v_mfma_f32_16x16x32_bf16 a[196:199],a[136:139],v[88:91],a[196:199]
v_accvgpr_read_b32 v15,a31
v_mul_f32 v158,v20,v158
v_mfma_f32_16x16x32_bf16 a[196:199],a[132:135],v[84:87],a[196:199]
buffer_load_dwordx4 v10,s[72:75],s83 offen offset:3072 lds
v_mfma_f32_16x16x32_bf16 a[196:199],a[128:131],v[80:83],a[196:199]
v_mul_f32 v159,v20,v159
v_mul_f32 v14,v20,v14
v_mfma_f32_16x16x32_bf16 a[200:203],a[128:131],v[96:99],0
v_mul_f32 v15,v20,v15
v_cvt_pk_bf16_f32 v158,v158,v159
v_mfma_f32_16x16x32_bf16 a[200:203],a[132:135],v[100:103],a[200:203]
ds_read_b64_tr_b16 a[160:161],v1 offset:64
v_cvt_pk_bf16_f32 v159,v14,v15
v_mfma_f32_16x16x32_bf16 a[200:203],a[136:139],v[104:107],a[200:203]
ds_read_b64_tr_b16 a[162:163],v1 offset:5184
ds_read_b64_tr_b16 a[164:165],v1 offset:10304
v_mfma_f32_16x16x32_bf16 a[200:203],a[140:143],v[108:111],a[200:203]
ds_read_b64_tr_b16 a[166:167],v1 offset:15424
v_add_f32 v52,v53,v52
v_mfma_f32_16x16x32_bf16 a[204:207],a[140:143],v[124:127],0
ds_read_b64_tr_b16 a[168:169],v1 offset:20544
v_max_f32 v21,0x00800000,v52
v_mfma_f32_16x16x32_bf16 a[204:207],a[136:139],v[120:123],a[204:207]
s_addk_i32 m0,4096
ds_read_b64_tr_b16 a[170:171],v1 offset:25664
v_bfe_u32 v24,v6,4,2
v_mfma_f32_16x16x32_bf16 a[204:207],a[132:135],v[116:119],a[204:207]
ds_read_b64_tr_b16 a[172:173],v1 offset:30784
ds_read_b64_tr_b16 a[174:175],v1 offset:35904
v_mfma_f32_16x16x32_bf16 a[204:207],a[128:131],v[112:115],a[204:207]
s_waitcnt lgkmcnt(8)
v_rcp_f32 v21,v21
v_mfma_f32_16x16x32_bf16 a[208:211],a[144:147],v[64:67],0
buffer_load_dwordx4 v11,s[72:75],s83 offen lds
v_mfma_f32_16x16x32_bf16 a[208:211],a[148:151],v[68:71],a[208:211]
v_accvgpr_read_b32 v168,a32
v_accvgpr_read_b32 v169,a33
v_accvgpr_read_b32 v170,a34
v_accvgpr_read_b32 v171,a35
v_accvgpr_read_b32 v12,a36
v_accvgpr_read_b32 v13,a37
v_accvgpr_read_b32 v14,a38
v_accvgpr_read_b32 v15,a39
v_pk_mul_f32 v[168:169],v[20:21],v[168:169] op_sel:[1,0] op_sel_hi:[1,1]
v_pk_mul_f32 v[170:171],v[20:21],v[170:171] op_sel:[1,0] op_sel_hi:[1,1]
v_pk_mul_f32 v[12:13],v[20:21],v[12:13] op_sel:[1,0] op_sel_hi:[1,1]
v_pk_mul_f32 v[14:15],v[20:21],v[14:15] op_sel:[1,0] op_sel_hi:[1,1]
v_cvt_pk_bf16_f32 v168,v168,v169
v_cvt_pk_bf16_f32 v169,v170,v171
v_cvt_pk_bf16_f32 v170,v12,v13
v_cvt_pk_bf16_f32 v171,v14,v15
v_mfma_f32_16x16x32_bf16 a[208:211],a[152:155],v[72:75],a[208:211]
v_min_u32 v24,2,v24
v_add_u32 v24,s71,v24
v_mfma_f32_16x16x32_bf16 a[208:211],a[156:159],v[76:79],a[208:211]
s_cmp_ge_u32 1,s71
s_cselect_b32 s80,4,2
v_add_u32 v24,1,v24
v_mfma_f32_16x16x32_bf16 a[212:215],a[156:159],v[92:95],0
s_mul_i32 s80,s80,163840
s_add_u32 s80,s80,s81
v_and_b32 v24,3,v24
v_mfma_f32_16x16x32_bf16 a[212:215],a[152:155],v[88:91],a[212:215]
s_add_u32 s83,s80,20480
ds_read_b64_tr_b16 a[176:177],v2 offset:96
v_lshlrev_b32 v24,6,v24
v_mfma_f32_16x16x32_bf16 a[212:215],a[148:151],v[84:87],a[212:215]
buffer_store_dwordx4 v[152:155],v6,s[36:39],s84 offen offset:2048 sc1
v_mfma_f32_16x16x32_bf16 a[212:215],a[144:147],v[80:83],a[212:215]
ds_read_b64_tr_b16 a[178:179],v2 offset:5216
ds_read_b64_tr_b16 a[180:181],v2 offset:10336
v_mfma_f32_16x16x32_bf16 a[216:219],a[144:147],v[96:99],0
ds_read_b64_tr_b16 a[182:183],v2 offset:15456
v_accvgpr_read_b32 v184,a48
v_mfma_f32_16x16x32_bf16 a[216:219],a[148:151],v[100:103],a[216:219]
ds_read_b64_tr_b16 a[184:185],v2 offset:20576
v_accvgpr_read_b32 v185,a49
v_mfma_f32_16x16x32_bf16 a[216:219],a[152:155],v[104:107],a[216:219]
ds_read_b64_tr_b16 a[186:187],v2 offset:25696
v_mul_f32 v184,v21,v184
v_mfma_f32_16x16x32_bf16 a[216:219],a[156:159],v[108:111],a[216:219]
ds_read_b64_tr_b16 a[188:189],v2 offset:30816
ds_read_b64_tr_b16 a[190:191],v2 offset:35936
v_mfma_f32_16x16x32_bf16 a[220:223],a[156:159],v[124:127],0
v_accvgpr_read_b32 v172,a40
v_accvgpr_read_b32 v173,a41
v_accvgpr_read_b32 v174,a42
v_accvgpr_read_b32 v175,a43
v_accvgpr_read_b32 v12,a44
v_accvgpr_read_b32 v13,a45
v_accvgpr_read_b32 v14,a46
v_accvgpr_read_b32 v15,a47
v_pk_mul_f32 v[172:173],v[20:21],v[172:173] op_sel:[1,0] op_sel_hi:[1,1]
v_pk_mul_f32 v[174:175],v[20:21],v[174:175] op_sel:[1,0] op_sel_hi:[1,1]
v_pk_mul_f32 v[12:13],v[20:21],v[12:13] op_sel:[1,0] op_sel_hi:[1,1]
v_pk_mul_f32 v[14:15],v[20:21],v[14:15] op_sel:[1,0] op_sel_hi:[1,1]
v_cvt_pk_bf16_f32 v172,v172,v173
v_cvt_pk_bf16_f32 v173,v174,v175
v_cvt_pk_bf16_f32 v174,v12,v13
v_cvt_pk_bf16_f32 v175,v14,v15
v_mfma_f32_16x16x32_bf16 a[220:223],a[152:155],v[120:123],a[220:223]
v_mul_f32 v185,v21,v185
v_accvgpr_read_b32 v186,a50
v_mfma_f32_16x16x32_bf16 a[220:223],a[148:151],v[116:119],a[220:223]
s_add_u32 m0,s82,122880
v_accvgpr_read_b32 v187,a51
v_cvt_pk_bf16_f32 v184,v184,v185
v_mfma_f32_16x16x32_bf16 a[220:223],a[144:147],v[112:115],a[220:223]
s_waitcnt lgkmcnt(5)
v_mul_f32 v186,v21,v186
v_mul_f32 v187,v21,v187
v_mfma_f32_16x16x32_bf16 a[224:227],a[160:163],v[64:67],0
v_cvt_pk_bf16_f32 v185,v186,v187
v_accvgpr_read_b32 v186,a52
v_mfma_f32_16x16x32_bf16 a[224:227],a[164:167],v[68:71],a[224:227]
buffer_load_dwordx4 v7,s[72:75],s80 offen lds
v_mfma_f32_16x16x32_bf16 a[224:227],a[168:171],v[72:75],a[224:227]
v_accvgpr_read_b32 v187,a53
v_accvgpr_read_b32 v16,a54
v_mfma_f32_16x16x32_bf16 a[224:227],a[172:175],v[76:79],a[224:227]
v_accvgpr_read_b32 v17,a55
v_mul_f32 v186,v21,v186
v_mfma_f32_16x16x32_bf16 a[228:231],a[172:175],v[92:95],0
v_mul_f32 v187,v21,v187
v_mul_f32 v16,v21,v16
v_mfma_f32_16x16x32_bf16 a[228:231],a[168:171],v[88:91],a[228:231]
v_mul_f32 v17,v21,v17
v_cvt_pk_bf16_f32 v186,v186,v187
v_mfma_f32_16x16x32_bf16 a[228:231],a[164:167],v[84:87],a[228:231]
ds_read_b64_tr_b16 a[128:129],v1 offset:128
v_cvt_pk_bf16_f32 v187,v16,v17
v_mfma_f32_16x16x32_bf16 a[228:231],a[160:163],v[80:83],a[228:231]
ds_read_b64_tr_b16 a[130:131],v1 offset:5248
ds_read_b64_tr_b16 a[132:133],v1 offset:10368
v_mfma_f32_16x16x32_bf16 a[232:235],a[160:163],v[96:99],0
buffer_load_dwordx2 v[4:5],v24,s[64:67],s94 offen sc1
v_mfma_f32_16x16x32_bf16 a[232:235],a[164:167],v[100:103],a[232:235]
ds_read_b64_tr_b16 a[134:135],v1 offset:15488
ds_read_b64_tr_b16 a[136:137],v1 offset:20608
v_mfma_f32_16x16x32_bf16 a[232:235],a[168:171],v[104:107],a[232:235]
ds_read_b64_tr_b16 a[138:139],v1 offset:25728
v_accvgpr_read_b32 v188,a56
v_mfma_f32_16x16x32_bf16 a[232:235],a[172:175],v[108:111],a[232:235]
ds_read_b64_tr_b16 a[140:141],v1 offset:30848
v_accvgpr_read_b32 v189,a57
v_mfma_f32_16x16x32_bf16 a[236:239],a[172:175],v[124:127],0
buffer_store_dwordx4 v[156:159],v6,s[36:39],s84 offen offset:3072 sc1
v_mfma_f32_16x16x32_bf16 a[236:239],a[168:171],v[120:123],a[236:239]
ds_read_b64_tr_b16 a[142:143],v1 offset:35968
v_mul_f32 v188,v21,v188
v_mfma_f32_16x16x32_bf16 a[236:239],a[164:167],v[116:119],a[236:239]
v_mul_f32 v189,v21,v189
v_accvgpr_read_b32 v190,a58
v_mfma_f32_16x16x32_bf16 a[236:239],a[160:163],v[112:115],a[236:239]
v_accvgpr_read_b32 v191,a59
v_cvt_pk_bf16_f32 v188,v188,v189
v_mfma_f32_16x16x32_bf16 a[240:243],a[176:179],v[64:67],0
s_waitcnt lgkmcnt(5)
buffer_load_dwordx4 v8,s[72:75],s80 offen offset:1024 lds
v_mfma_f32_16x16x32_bf16 a[240:243],a[180:183],v[68:71],a[240:243]
v_mul_f32 v190,v21,v190
v_mul_f32 v191,v21,v191
v_mfma_f32_16x16x32_bf16 a[240:243],a[184:187],v[72:75],a[240:243]
v_cvt_pk_bf16_f32 v189,v190,v191
v_accvgpr_read_b32 v190,a60
v_mfma_f32_16x16x32_bf16 a[240:243],a[188:191],v[76:79],a[240:243]
v_accvgpr_read_b32 v191,a61
v_accvgpr_read_b32 v12,a62
v_mfma_f32_16x16x32_bf16 a[244:247],a[188:191],v[92:95],0
v_accvgpr_read_b32 v13,a63
v_mul_f32 v190,v21,v190
v_mfma_f32_16x16x32_bf16 a[244:247],a[184:187],v[88:91],a[244:247]
v_mul_f32 v191,v21,v191
v_mul_f32 v12,v21,v12
v_mfma_f32_16x16x32_bf16 a[244:247],a[180:183],v[84:87],a[244:247]
v_mul_f32 v13,v21,v13
v_cvt_pk_bf16_f32 v190,v190,v191
v_mfma_f32_16x16x32_bf16 a[244:247],a[176:179],v[80:83],a[244:247]
ds_read_b64_tr_b16 a[144:145],v2 offset:160
v_cvt_pk_bf16_f32 v191,v12,v13
v_mfma_f32_16x16x32_bf16 a[248:251],a[176:179],v[96:99],0
ds_read_b64_tr_b16 a[146:147],v2 offset:5280
ds_read_b64_tr_b16 a[148:149],v2 offset:10400
v_mfma_f32_16x16x32_bf16 a[248:251],a[180:183],v[100:103],a[248:251]
buffer_load_dwordx4 v9,s[72:75],s80 offen offset:2048 lds
v_mfma_f32_16x16x32_bf16 a[248:251],a[184:187],v[104:107],a[248:251]
ds_read_b64_tr_b16 a[150:151],v2 offset:15520
ds_read_b64_tr_b16 a[152:153],v2 offset:20640
v_mfma_f32_16x16x32_bf16 a[248:251],a[188:191],v[108:111],a[248:251]
ds_read_b64_tr_b16 a[154:155],v2 offset:25760
v_add_f32 v56,v57,v56
v_mfma_f32_16x16x32_bf16 a[252:255],a[188:191],v[124:127],0
ds_read_b64_tr_b16 a[156:157],v2 offset:30880
v_max_f32 v22,0x00800000,v56
v_mfma_f32_16x16x32_bf16 a[252:255],a[184:187],v[120:123],a[252:255]
ds_read_b64_tr_b16 a[158:159],v2 offset:36000
v_accvgpr_read_b32 v216,a80
)A"
R"A(v_mfma_f32_16x16x32_bf16 a[252:255],a[180:183],v[116:119],a[252:255]
s_waitcnt vmcnt(18)
v_rcp_f32 v22,v22
v_mfma_f32_16x16x32_bf16 a[252:255],a[176:179],v[112:115],a[252:255]
s_barrier
buffer_store_dwordx4 v[168:171],v6,s[36:39],s85 offen sc1
v_mfma_f32_16x16x32_bf16 a[0:3],a[128:131],v[64:67],0
v_accvgpr_read_b32 v200,a64
v_accvgpr_read_b32 v201,a65
v_accvgpr_read_b32 v202,a66
v_accvgpr_read_b32 v203,a67
v_accvgpr_read_b32 v12,a68
v_accvgpr_read_b32 v13,a69
v_accvgpr_read_b32 v14,a70
v_accvgpr_read_b32 v15,a71
v_pk_mul_f32 v[200:201],v[22:23],v[200:201] op_sel:[0,0] op_sel_hi:[0,1]
v_pk_mul_f32 v[202:203],v[22:23],v[202:203] op_sel:[0,0] op_sel_hi:[0,1]
v_pk_mul_f32 v[12:13],v[22:23],v[12:13] op_sel:[0,0] op_sel_hi:[0,1]
v_pk_mul_f32 v[14:15],v[22:23],v[14:15] op_sel:[0,0] op_sel_hi:[0,1]
v_cvt_pk_bf16_f32 v200,v200,v201
v_cvt_pk_bf16_f32 v201,v202,v203
v_cvt_pk_bf16_f32 v202,v12,v13
v_cvt_pk_bf16_f32 v203,v14,v15
s_waitcnt lgkmcnt(5)
v_mfma_f32_16x16x32_bf16 a[0:3],a[132:135],v[68:71],a[0:3]
ds_read_b64_tr_b16 a[160:161],v1 offset:192
ds_read_b64_tr_b16 a[162:163],v1 offset:5312
v_mfma_f32_16x16x32_bf16 a[0:3],a[136:139],v[72:75],a[0:3]
ds_read_b64_tr_b16 a[164:165],v1 offset:10432
v_accvgpr_read_b32 v217,a81
v_mfma_f32_16x16x32_bf16 a[0:3],a[140:143],v[76:79],a[0:3]
ds_read_b64_tr_b16 a[166:167],v1 offset:15552
v_mul_f32 v216,v22,v216
v_mfma_f32_16x16x32_bf16 a[4:7],a[140:143],v[92:95],0
buffer_load_dwordx4 v10,s[72:75],s80 offen offset:3072 lds
v_mfma_f32_16x16x32_bf16 a[4:7],a[136:139],v[88:91],a[4:7]
ds_read_b64_tr_b16 a[168:169],v1 offset:20672
ds_read_b64_tr_b16 a[170:171],v1 offset:25792
v_mfma_f32_16x16x32_bf16 a[4:7],a[132:135],v[84:87],a[4:7]
v_accvgpr_read_b32 v204,a72
v_accvgpr_read_b32 v205,a73
v_accvgpr_read_b32 v206,a74
v_accvgpr_read_b32 v207,a75
v_accvgpr_read_b32 v12,a76
v_accvgpr_read_b32 v13,a77
v_accvgpr_read_b32 v14,a78
v_accvgpr_read_b32 v15,a79
v_pk_mul_f32 v[204:205],v[22:23],v[204:205] op_sel:[0,0] op_sel_hi:[0,1]
v_pk_mul_f32 v[206:207],v[22:23],v[206:207] op_sel:[0,0] op_sel_hi:[0,1]
v_pk_mul_f32 v[12:13],v[22:23],v[12:13] op_sel:[0,0] op_sel_hi:[0,1]
v_pk_mul_f32 v[14:15],v[22:23],v[14:15] op_sel:[0,0] op_sel_hi:[0,1]
v_cvt_pk_bf16_f32 v204,v204,v205
v_cvt_pk_bf16_f32 v205,v206,v207
v_cvt_pk_bf16_f32 v206,v12,v13
v_cvt_pk_bf16_f32 v207,v14,v15
v_mfma_f32_16x16x32_bf16 a[4:7],a[128:131],v[80:83],a[4:7]
ds_read_b64_tr_b16 a[172:173],v1 offset:30912
ds_read_b64_tr_b16 a[174:175],v1 offset:36032
v_mfma_f32_16x16x32_bf16 a[8:11],a[128:131],v[96:99],0
v_mul_f32 v217,v22,v217
v_accvgpr_read_b32 v218,a82
v_mfma_f32_16x16x32_bf16 a[8:11],a[132:135],v[100:103],a[8:11]
v_accvgpr_read_b32 v219,a83
v_cvt_pk_bf16_f32 v216,v216,v217
v_mfma_f32_16x16x32_bf16 a[8:11],a[136:139],v[104:107],a[8:11]
v_mul_f32 v218,v22,v218
v_mul_f32 v219,v22,v219
v_mfma_f32_16x16x32_bf16 a[8:11],a[140:143],v[108:111],a[8:11]
s_waitcnt vmcnt(5)
v_cmp_ne_u32 s[90:91],s68,v4
v_and_b32 v4,-2,v5
v_cmp_ne_u32 s[92:93],s69,v4
s_nop 0
s_or_b64 s[90:91],s[90:91],s[92:93]
s_cbranch_scc1 L_fr8_%=
L_fo8_%=:
v_mfma_f32_16x16x32_bf16 a[12:15],a[140:143],v[124:127],0
s_nop 0
v_readlane_b32 s76,v5,0
v_readlane_b32 s77,v5,1
v_readlane_b32 s78,v5,2
v_mfma_f32_16x16x32_bf16 a[12:15],a[136:139],v[120:123],a[12:15]
s_addk_i32 m0,4096
s_and_b32 s77,s77,1
s_lshl_b32 s77,s77,2
v_mfma_f32_16x16x32_bf16 a[12:15],a[132:135],v[116:119],a[12:15]
s_nop 1
s_and_b32 s54,s76,1
v_mfma_f32_16x16x32_bf16 a[12:15],a[128:131],v[112:115],a[12:15]
s_and_b32 s78,s78,1
s_or_b32 s54,s54,s77
s_lshl_b32 s78,s78,4
v_mfma_f32_16x16x32_bf16 a[16:19],a[144:147],v[64:67],0
s_or_b32 s54,s54,s78
buffer_load_dwordx4 v11,s[72:75],s80 offen lds
s_waitcnt lgkmcnt(0)
v_mfma_f32_16x16x32_bf16 a[16:19],a[148:151],v[68:71],a[16:19]
s_mulk_i32 s54,3084
s_lshl_b32 s76,s71,1
s_lshl_b32 s54,s54,s76
v_mfma_f32_16x16x32_bf16 a[16:19],a[152:155],v[72:75],a[16:19]
s_lshr_b32 s54,s54,8
s_cmp_ge_u32 0,s71
s_cselect_b32 s76,2,0
v_mfma_f32_16x16x32_bf16 a[16:19],a[156:159],v[76:79],a[16:19]
s_bitcmp1_b32 s54,s76
s_cselect_b32 s77,8192,0
s_lshl_b32 s76,s76,17
v_mfma_f32_16x16x32_bf16 a[20:23],a[156:159],v[92:95],0
s_add_u32 s76,s76,s77
buffer_store_dwordx4 v[172:175],v6,s[36:39],s85 offen offset:1024 sc1
v_mfma_f32_16x16x32_bf16 a[20:23],a[152:155],v[88:91],a[20:23]
s_add_u32 s77,s76,4096
s_add_u32 s78,s76,16384
s_add_u32 s79,s76,20480
v_mfma_f32_16x16x32_bf16 a[20:23],a[148:151],v[84:87],a[20:23]
s_max_i32 s76,s76,s18
v_cvt_pk_bf16_f32 v217,v218,v219
v_accvgpr_read_b32 v218,a84
v_mfma_f32_16x16x32_bf16 a[20:23],a[144:147],v[80:83],a[20:23]
s_max_i32 s77,s77,s19
v_accvgpr_read_b32 v219,a85
v_accvgpr_read_b32 v14,a86
v_mfma_f32_16x16x32_bf16 a[24:27],a[144:147],v[96:99],0
s_max_i32 s78,s78,s100
v_accvgpr_read_b32 v15,a87
v_mul_f32 v218,v22,v218
v_mfma_f32_16x16x32_bf16 a[24:27],a[148:151],v[100:103],a[24:27]
s_addk_i32 m0,1024
v_mul_f32 v219,v22,v219
v_mul_f32 v14,v22,v14
v_mfma_f32_16x16x32_bf16 a[24:27],a[152:155],v[104:107],a[24:27]
s_max_i32 s79,s79,s101
v_mul_f32 v15,v22,v15
v_cvt_pk_bf16_f32 v218,v218,v219
v_mfma_f32_16x16x32_bf16 a[24:27],a[156:159],v[108:111],a[24:27]
ds_read_b64_tr_b16 a[176:177],v2 offset:224
v_cvt_pk_bf16_f32 v219,v14,v15
v_mfma_f32_16x16x32_bf16 a[28:31],a[156:159],v[124:127],0
buffer_load_dwordx4 v7,s[72:75],s83 offen lds
v_mfma_f32_16x16x32_bf16 a[28:31],a[152:155],v[120:123],a[28:31]
ds_read_b64_tr_b16 a[178:179],v2 offset:5344
ds_read_b64_tr_b16 a[180:181],v2 offset:10464
v_mfma_f32_16x16x32_bf16 a[28:31],a[148:151],v[116:119],a[28:31]
ds_read_b64_tr_b16 a[182:183],v2 offset:15584
v_accvgpr_read_b32 v220,a88
v_mfma_f32_16x16x32_bf16 a[28:31],a[144:147],v[112:115],a[28:31]
ds_read_b64_tr_b16 a[184:185],v2 offset:20704
v_accvgpr_read_b32 v221,a89
v_mfma_f32_16x16x32_bf16 a[32:35],a[160:163],v[64:67],0
buffer_load_dwordx4 v[128:131],v6,s[28:31],s76 offen sc1
v_mfma_f32_16x16x32_bf16 a[32:35],a[164:167],v[68:71],a[32:35]
ds_read_b64_tr_b16 a[186:187],v2 offset:25824
ds_read_b64_tr_b16 a[188:189],v2 offset:30944
v_mfma_f32_16x16x32_bf16 a[32:35],a[168:171],v[72:75],a[32:35]
ds_read_b64_tr_b16 a[190:191],v2 offset:36064
v_mul_f32 v220,v22,v220
v_mfma_f32_16x16x32_bf16 a[32:35],a[172:175],v[76:79],a[32:35]
v_mul_f32 v221,v22,v221
v_accvgpr_read_b32 v222,a90
v_mfma_f32_16x16x32_bf16 a[36:39],a[172:175],v[92:95],0
v_accvgpr_read_b32 v223,a91
v_cvt_pk_bf16_f32 v220,v220,v221
v_mfma_f32_16x16x32_bf16 a[36:39],a[168:171],v[88:91],a[36:39]
v_mul_f32 v222,v22,v222
v_mul_f32 v223,v22,v223
v_mfma_f32_16x16x32_bf16 a[36:39],a[164:167],v[84:87],a[36:39]
buffer_store_dwordx4 v[184:187],v6,s[36:39],s85 offen offset:2048 sc1
v_mfma_f32_16x16x32_bf16 a[36:39],a[160:163],v[80:83],a[36:39]
v_cvt_pk_bf16_f32 v221,v222,v223
v_accvgpr_read_b32 v222,a92
v_mfma_f32_16x16x32_bf16 a[40:43],a[160:163],v[96:99],0
v_accvgpr_read_b32 v223,a93
v_accvgpr_read_b32 v16,a94
v_mfma_f32_16x16x32_bf16 a[40:43],a[164:167],v[100:103],a[40:43]
v_accvgpr_read_b32 v17,a95
v_mul_f32 v222,v22,v222
v_mfma_f32_16x16x32_bf16 a[40:43],a[168:171],v[104:107],a[40:43]
buffer_load_dwordx4 v8,s[72:75],s83 offen offset:1024 lds
v_mfma_f32_16x16x32_bf16 a[40:43],a[172:175],v[108:111],a[40:43]
v_mul_f32 v223,v22,v223
v_mul_f32 v16,v22,v16
v_mfma_f32_16x16x32_bf16 a[44:47],a[172:175],v[124:127],0
v_mul_f32 v17,v22,v17
v_cvt_pk_bf16_f32 v222,v222,v223
v_mfma_f32_16x16x32_bf16 a[44:47],a[168:171],v[120:123],a[44:47]
ds_read_b64_tr_b16 a[128:129],v1 offset:256
v_cvt_pk_bf16_f32 v223,v16,v17
v_mfma_f32_16x16x32_bf16 a[44:47],a[164:167],v[116:119],a[44:47]
buffer_load_dwordx4 v[160:163],v6,s[28:31],s76 offen offset:1024 sc1
v_mfma_f32_16x16x32_bf16 a[44:47],a[160:163],v[112:115],a[44:47]
s_waitcnt lgkmcnt(1)
ds_read_b64_tr_b16 a[130:131],v1 offset:5376
ds_read_b64_tr_b16 a[132:133],v1 offset:10496
v_mfma_f32_16x16x32_bf16 a[48:51],a[176:179],v[64:67],0
ds_read_b64_tr_b16 a[134:135],v1 offset:15616
v_add_f32 v60,v61,v60
v_mfma_f32_16x16x32_bf16 a[48:51],a[180:183],v[68:71],a[48:51]
ds_read_b64_tr_b16 a[136:137],v1 offset:20736
v_max_f32 v23,0x00800000,v60
v_mfma_f32_16x16x32_bf16 a[48:51],a[184:187],v[72:75],a[48:51]
ds_read_b64_tr_b16 a[138:139],v1 offset:25856
v_accvgpr_read_b32 v248,a112
v_mfma_f32_16x16x32_bf16 a[48:51],a[188:191],v[76:79],a[48:51]
buffer_load_dwordx4 v[192:195],v6,s[28:31],s76 offen offset:2048 sc1
v_mfma_f32_16x16x32_bf16 a[52:55],a[188:191],v[92:95],0
ds_read_b64_tr_b16 a[140:141],v1 offset:30976
ds_read_b64_tr_b16 a[142:143],v1 offset:36096
v_mfma_f32_16x16x32_bf16 a[52:55],a[184:187],v[88:91],a[52:55]
v_rcp_f32 v23,v23
v_mfma_f32_16x16x32_bf16 a[52:55],a[180:183],v[84:87],a[52:55]
v_accvgpr_read_b32 v232,a96
v_accvgpr_read_b32 v233,a97
v_accvgpr_read_b32 v234,a98
v_accvgpr_read_b32 v235,a99
v_accvgpr_read_b32 v12,a100
v_accvgpr_read_b32 v13,a101
v_accvgpr_read_b32 v14,a102
v_accvgpr_read_b32 v15,a103
v_pk_mul_f32 v[232:233],v[22:23],v[232:233] op_sel:[1,0] op_sel_hi:[1,1]
v_pk_mul_f32 v[234:235],v[22:23],v[234:235] op_sel:[1,0] op_sel_hi:[1,1]
v_pk_mul_f32 v[12:13],v[22:23],v[12:13] op_sel:[1,0] op_sel_hi:[1,1]
v_pk_mul_f32 v[14:15],v[22:23],v[14:15] op_sel:[1,0] op_sel_hi:[1,1]
v_cvt_pk_bf16_f32 v232,v232,v233
v_cvt_pk_bf16_f32 v233,v234,v235
v_cvt_pk_bf16_f32 v234,v12,v13
v_cvt_pk_bf16_f32 v235,v14,v15
v_mfma_f32_16x16x32_bf16 a[52:55],a[176:179],v[80:83],a[52:55]
buffer_load_dwordx4 v9,s[72:75],s83 offen offset:2048 lds
v_mfma_f32_16x16x32_bf16 a[56:59],a[176:179],v[96:99],0
ds_read_b64_tr_b16 a[144:145],v2 offset:288
ds_read_b64_tr_b16 a[146:147],v2 offset:5408
v_mfma_f32_16x16x32_bf16 a[56:59],a[180:183],v[100:103],a[56:59]
ds_read_b64_tr_b16 a[148:149],v2 offset:10528
v_accvgpr_read_b32 v249,a113
v_mfma_f32_16x16x32_bf16 a[56:59],a[184:187],v[104:107],a[56:59]
ds_read_b64_tr_b16 a[150:151],v2 offset:15648
v_mul_f32 v248,v23,v248
v_mfma_f32_16x16x32_bf16 a[56:59],a[188:191],v[108:111],a[56:59]
buffer_store_dwordx4 v[188:191],v6,s[36:39],s85 offen offset:3072 sc1
v_mfma_f32_16x16x32_bf16 a[60:63],a[188:191],v[124:127],0
ds_read_b64_tr_b16 a[152:153],v2 offset:20768
ds_read_b64_tr_b16 a[154:155],v2 offset:25888
v_mfma_f32_16x16x32_bf16 a[60:63],a[184:187],v[120:123],a[60:63]
v_accvgpr_read_b32 v236,a104
v_accvgpr_read_b32 v237,a105
v_accvgpr_read_b32 v238,a106
v_accvgpr_read_b32 v239,a107
v_accvgpr_read_b32 v12,a108
v_accvgpr_read_b32 v13,a109
v_accvgpr_read_b32 v14,a110
v_accvgpr_read_b32 v15,a111
v_pk_mul_f32 v[236:237],v[22:23],v[236:237] op_sel:[1,0] op_sel_hi:[1,1]
v_pk_mul_f32 v[238:239],v[22:23],v[238:239] op_sel:[1,0] op_sel_hi:[1,1]
v_pk_mul_f32 v[12:13],v[22:23],v[12:13] op_sel:[1,0] op_sel_hi:[1,1]
v_pk_mul_f32 v[14:15],v[22:23],v[14:15] op_sel:[1,0] op_sel_hi:[1,1]
v_cvt_pk_bf16_f32 v236,v236,v237
v_cvt_pk_bf16_f32 v237,v238,v239
v_cvt_pk_bf16_f32 v238,v12,v13
v_cvt_pk_bf16_f32 v239,v14,v15
v_mfma_f32_16x16x32_bf16 a[60:63],a[180:183],v[116:119],a[60:63]
ds_read_b64_tr_b16 a[156:157],v2 offset:31008
ds_read_b64_tr_b16 a[158:159],v2 offset:36128
v_mfma_f32_16x16x32_bf16 a[60:63],a[176:179],v[112:115],a[60:63]
s_waitcnt lgkmcnt(8)
buffer_load_dwordx4 v[224:227],v6,s[28:31],s76 offen offset:3072 sc1
v_mfma_f32_16x16x32_bf16 a[64:67],a[128:131],v[64:67],0
v_mul_f32 v249,v23,v249
v_accvgpr_read_b32 v250,a114
v_mfma_f32_16x16x32_bf16 a[64:67],a[132:135],v[68:71],a[64:67]
v_accvgpr_read_b32 v251,a115
v_cvt_pk_bf16_f32 v248,v248,v249
v_mfma_f32_16x16x32_bf16 a[64:67],a[136:139],v[72:75],a[64:67]
v_mul_f32 v250,v23,v250
v_mul_f32 v251,v23,v251
v_mfma_f32_16x16x32_bf16 a[64:67],a[140:143],v[76:79],a[64:67]
buffer_load_dwordx4 v10,s[72:75],s83 offen offset:3072 lds
v_mfma_f32_16x16x32_bf16 a[68:71],a[140:143],v[92:95],0
v_cvt_pk_bf16_f32 v249,v250,v251
v_accvgpr_read_b32 v250,a116
v_mfma_f32_16x16x32_bf16 a[68:71],a[136:139],v[88:91],a[68:71]
v_accvgpr_read_b32 v251,a117
v_accvgpr_read_b32 v12,a118
v_mfma_f32_16x16x32_bf16 a[68:71],a[132:135],v[84:87],a[68:71]
v_accvgpr_read_b32 v13,a119
v_mul_f32 v250,v23,v250
v_mfma_f32_16x16x32_bf16 a[68:71],a[128:131],v[80:83],a[68:71]
buffer_load_dwordx4 v[132:135],v6,s[28:31],s77 offen sc1
v_mfma_f32_16x16x32_bf16 a[72:75],a[128:131],v[96:99],0
)A"
R"A(v_mul_f32 v251,v23,v251
v_mul_f32 v12,v23,v12
v_mfma_f32_16x16x32_bf16 a[72:75],a[132:135],v[100:103],a[72:75]
s_addk_i32 m0,4096
v_mul_f32 v13,v23,v13
v_cvt_pk_bf16_f32 v250,v250,v251
v_mfma_f32_16x16x32_bf16 a[72:75],a[136:139],v[104:107],a[72:75]
v_cvt_pk_bf16_f32 v251,v12,v13
v_subrev_u32 v1,81920,v1
v_mfma_f32_16x16x32_bf16 a[72:75],a[140:143],v[108:111],a[72:75]
ds_read_b64_tr_b16 a[160:161],v1
ds_read_b64_tr_b16 a[162:163],v1 offset:5120
v_mfma_f32_16x16x32_bf16 a[76:79],a[140:143],v[124:127],0
buffer_load_dwordx4 v11,s[72:75],s83 offen lds
v_mfma_f32_16x16x32_bf16 a[76:79],a[136:139],v[120:123],a[76:79]
ds_read_b64_tr_b16 a[164:165],v1 offset:10240
ds_read_b64_tr_b16 a[166:167],v1 offset:15360
v_mfma_f32_16x16x32_bf16 a[76:79],a[132:135],v[116:119],a[76:79]
ds_read_b64_tr_b16 a[168:169],v1 offset:20480
v_accvgpr_read_b32 v252,a120
v_mfma_f32_16x16x32_bf16 a[76:79],a[128:131],v[112:115],a[76:79]
s_waitcnt lgkmcnt(5)
ds_read_b64_tr_b16 a[170:171],v1 offset:25600
v_accvgpr_read_b32 v253,a121
v_mfma_f32_16x16x32_bf16 a[80:83],a[144:147],v[64:67],0
ds_read_b64_tr_b16 a[172:173],v1 offset:30720
v_cmp_nlt_f32 s[56:57],v48,s63
v_mfma_f32_16x16x32_bf16 a[80:83],a[148:151],v[68:71],a[80:83]
buffer_load_dwordx4 v[164:167],v6,s[28:31],s77 offen offset:1024 sc1
v_mfma_f32_16x16x32_bf16 a[80:83],a[152:155],v[72:75],a[80:83]
ds_read_b64_tr_b16 a[174:175],v1 offset:35840
v_mul_f32 v252,v23,v252
v_mfma_f32_16x16x32_bf16 a[80:83],a[156:159],v[76:79],a[80:83]
v_mul_f32 v253,v23,v253
v_accvgpr_read_b32 v254,a122
v_mfma_f32_16x16x32_bf16 a[84:87],a[156:159],v[92:95],0
v_accvgpr_read_b32 v255,a123
v_cvt_pk_bf16_f32 v252,v252,v253
v_mfma_f32_16x16x32_bf16 a[84:87],a[152:155],v[88:91],a[84:87]
v_mul_f32 v254,v23,v254
v_mul_f32 v255,v23,v255
v_mfma_f32_16x16x32_bf16 a[84:87],a[148:151],v[84:87],a[84:87]
v_cmp_nlt_f32 s[58:59],v52,s63
s_nop 1
s_or_b64 s[56:57],s[56:57],s[58:59]
v_mfma_f32_16x16x32_bf16 a[84:87],a[144:147],v[80:83],a[84:87]
v_cvt_pk_bf16_f32 v253,v254,v255
v_accvgpr_read_b32 v254,a124
v_mfma_f32_16x16x32_bf16 a[88:91],a[144:147],v[96:99],0
v_accvgpr_read_b32 v255,a125
v_accvgpr_read_b32 v14,a126
v_mfma_f32_16x16x32_bf16 a[88:91],a[148:151],v[100:103],a[88:91]
v_cmp_nlt_f32 s[58:59],v56,s63
s_nop 1
s_or_b64 s[56:57],s[56:57],s[58:59]
v_mfma_f32_16x16x32_bf16 a[88:91],a[152:155],v[104:107],a[88:91]
v_accvgpr_read_b32 v15,a127
v_mul_f32 v254,v23,v254
v_mfma_f32_16x16x32_bf16 a[88:91],a[156:159],v[108:111],a[88:91]
v_mul_f32 v255,v23,v255
v_mul_f32 v14,v23,v14
v_mfma_f32_16x16x32_bf16 a[92:95],a[156:159],v[124:127],0
v_mul_f32 v15,v23,v15
v_cvt_pk_bf16_f32 v254,v254,v255
v_mfma_f32_16x16x32_bf16 a[92:95],a[152:155],v[120:123],a[92:95]
v_cmp_nlt_f32 s[58:59],v60,s63
s_nop 1
s_or_b64 s[56:57],s[56:57],s[58:59]
v_mfma_f32_16x16x32_bf16 a[92:95],a[148:151],v[116:119],a[92:95]
buffer_load_dwordx4 v[196:199],v6,s[28:31],s77 offen offset:2048 sc1
v_cvt_pk_bf16_f32 v255,v14,v15
v_mfma_f32_16x16x32_bf16 a[92:95],a[144:147],v[112:115],a[92:95]
s_cmp_lg_u64 s[56:57],0
s_cbranch_scc1 L_slow1_%=
L_sret1_%=:
v_subrev_u32 v2,81920,v2
s_waitcnt lgkmcnt(0)
v_mfma_f32_16x16x32_bf16 a[192:195],a[160:163],v[136:139],a[192:195]
ds_read_b64_tr_b16 a[176:177],v2 offset:32
ds_read_b64_tr_b16 a[178:179],v2 offset:5152
v_mfma_f32_16x16x32_bf16 a[192:195],a[164:167],v[140:143],a[192:195]
s_cmp_ge_u32 2,s71
s_cselect_b32 s80,6,4
ds_read_b64_tr_b16 a[180:181],v2 offset:10272
v_mfma_f32_16x16x32_bf16 a[192:195],a[168:171],v[152:155],a[192:195]
s_mul_i32 s80,s80,163840
buffer_store_dwordx4 v[200:203],v6,s[36:39],s86 offen sc1
v_mfma_f32_16x16x32_bf16 a[192:195],a[172:175],v[156:159],a[192:195]
s_add_u32 s80,s80,s81
ds_read_b64_tr_b16 a[182:183],v2 offset:15392
ds_read_b64_tr_b16 a[184:185],v2 offset:20512
v_mfma_f32_16x16x32_bf16 a[196:199],a[172:175],v[188:191],a[196:199]
s_add_u32 s83,s80,20480
ds_read_b64_tr_b16 a[186:187],v2 offset:25632
v_mfma_f32_16x16x32_bf16 a[196:199],a[168:171],v[184:187],a[196:199]
ds_read_b64_tr_b16 a[188:189],v2 offset:30752
v_mfma_f32_16x16x32_bf16 a[196:199],a[164:167],v[172:175],a[196:199]
buffer_store_dwordx4 v[204:207],v6,s[36:39],s86 offen offset:1024 sc1
v_mfma_f32_16x16x32_bf16 a[196:199],a[160:163],v[168:171],a[196:199]
ds_read_b64_tr_b16 a[190:191],v2 offset:35872
ds_read_b64_tr_b16 a[128:129],v1 offset:64
v_mfma_f32_16x16x32_bf16 a[200:203],a[160:163],v[200:203],a[200:203]
ds_read_b64_tr_b16 a[130:131],v1 offset:5184
v_mfma_f32_16x16x32_bf16 a[200:203],a[164:167],v[204:207],a[200:203]
ds_read_b64_tr_b16 a[132:133],v1 offset:10304
v_mfma_f32_16x16x32_bf16 a[200:203],a[168:171],v[216:219],a[200:203]
buffer_load_dwordx4 v[228:231],v6,s[28:31],s77 offen offset:3072 sc1
v_mfma_f32_16x16x32_bf16 a[200:203],a[172:175],v[220:223],a[200:203]
ds_read_b64_tr_b16 a[134:135],v1 offset:15424
ds_read_b64_tr_b16 a[136:137],v1 offset:20544
v_mfma_f32_16x16x32_bf16 a[204:207],a[172:175],v[252:255],a[204:207]
ds_read_b64_tr_b16 a[138:139],v1 offset:25664
v_mfma_f32_16x16x32_bf16 a[204:207],a[168:171],v[248:251],a[204:207]
ds_read_b64_tr_b16 a[140:141],v1 offset:30784
v_mfma_f32_16x16x32_bf16 a[204:207],a[164:167],v[236:239],a[204:207]
ds_read_b64_tr_b16 a[142:143],v1 offset:35904
v_mfma_f32_16x16x32_bf16 a[204:207],a[160:163],v[232:235],a[204:207]
s_barrier
buffer_load_dwordx4 v[144:147],v6,s[28:31],s78 offen sc1
s_waitcnt lgkmcnt(7)
v_mfma_f32_16x16x32_bf16 a[208:211],a[176:179],v[136:139],a[208:211]
ds_read_b64_tr_b16 a[144:145],v2 offset:96
ds_read_b64_tr_b16 a[146:147],v2 offset:5216
v_mfma_f32_16x16x32_bf16 a[208:211],a[180:183],v[140:143],a[208:211]
ds_read_b64_tr_b16 a[148:149],v2 offset:10336
v_mfma_f32_16x16x32_bf16 a[208:211],a[184:187],v[152:155],a[208:211]
s_add_u32 m0,s82,81920
ds_read_b64_tr_b16 a[150:151],v2 offset:15456
v_mfma_f32_16x16x32_bf16 a[208:211],a[188:191],v[156:159],a[208:211]
ds_read_b64_tr_b16 a[152:153],v2 offset:20576
v_mfma_f32_16x16x32_bf16 a[212:215],a[188:191],v[188:191],a[212:215]
ds_read_b64_tr_b16 a[154:155],v2 offset:25696
ds_read_b64_tr_b16 a[156:157],v2 offset:30816
v_mfma_f32_16x16x32_bf16 a[212:215],a[184:187],v[184:187],a[212:215]
buffer_store_dwordx4 v[216:219],v6,s[36:39],s86 offen offset:2048 sc1
v_mfma_f32_16x16x32_bf16 a[212:215],a[180:183],v[172:175],a[212:215]
ds_read_b64_tr_b16 a[158:159],v2 offset:35936
ds_read_b64_tr_b16 a[160:161],v1 offset:128
v_mfma_f32_16x16x32_bf16 a[212:215],a[176:179],v[168:171],a[212:215]
ds_read_b64_tr_b16 a[162:163],v1 offset:5248
v_mfma_f32_16x16x32_bf16 a[216:219],a[176:179],v[200:203],a[216:219]
ds_read_b64_tr_b16 a[164:165],v1 offset:10368
v_mfma_f32_16x16x32_bf16 a[216:219],a[180:183],v[204:207],a[216:219]
buffer_load_dwordx4 v7,s[72:75],s80 offen lds
v_mfma_f32_16x16x32_bf16 a[216:219],a[184:187],v[216:219],a[216:219]
ds_read_b64_tr_b16 a[166:167],v1 offset:15488
ds_read_b64_tr_b16 a[168:169],v1 offset:20608
v_mfma_f32_16x16x32_bf16 a[216:219],a[188:191],v[220:223],a[216:219]
ds_read_b64_tr_b16 a[170:171],v1 offset:25728
v_mfma_f32_16x16x32_bf16 a[220:223],a[188:191],v[252:255],a[220:223]
ds_read_b64_tr_b16 a[172:173],v1 offset:30848
v_mfma_f32_16x16x32_bf16 a[220:223],a[184:187],v[248:251],a[220:223]
buffer_load_dwordx4 v[176:179],v6,s[28:31],s78 offen offset:1024 sc1
v_mfma_f32_16x16x32_bf16 a[220:223],a[180:183],v[236:239],a[220:223]
ds_read_b64_tr_b16 a[174:175],v1 offset:35968
ds_read_b64_tr_b16 a[180:181],v2 offset:10400
v_mfma_f32_16x16x32_bf16 a[220:223],a[176:179],v[232:235],a[220:223]
s_waitcnt lgkmcnt(7)
ds_read_b64_tr_b16 a[176:177],v2 offset:160
v_mfma_f32_16x16x32_bf16 a[224:227],a[128:131],v[136:139],a[224:227]
ds_read_b64_tr_b16 a[178:179],v2 offset:5280
v_mfma_f32_16x16x32_bf16 a[224:227],a[132:135],v[140:143],a[224:227]
ds_read_b64_tr_b16 a[182:183],v2 offset:15520
v_mfma_f32_16x16x32_bf16 a[224:227],a[136:139],v[152:155],a[224:227]
buffer_load_dwordx4 v[208:211],v6,s[28:31],s78 offen offset:2048 sc1
v_mfma_f32_16x16x32_bf16 a[224:227],a[140:143],v[156:159],a[224:227]
ds_read_b64_tr_b16 a[184:185],v2 offset:20640
ds_read_b64_tr_b16 a[186:187],v2 offset:25760
v_mfma_f32_16x16x32_bf16 a[228:231],a[140:143],v[188:191],a[228:231]
ds_read_b64_tr_b16 a[188:189],v2 offset:30880
v_mfma_f32_16x16x32_bf16 a[228:231],a[136:139],v[184:187],a[228:231]
ds_read_b64_tr_b16 a[190:191],v2 offset:36000
v_mfma_f32_16x16x32_bf16 a[228:231],a[132:135],v[172:175],a[228:231]
buffer_load_dwordx4 v8,s[72:75],s80 offen offset:1024 lds
v_mfma_f32_16x16x32_bf16 a[228:231],a[128:131],v[168:171],a[228:231]
v_mfma_f32_16x16x32_bf16 a[232:235],a[128:131],v[200:203],a[232:235]
v_mfma_f32_16x16x32_bf16 a[232:235],a[132:135],v[204:207],a[232:235]
v_mfma_f32_16x16x32_bf16 a[232:235],a[136:139],v[216:219],a[232:235]
buffer_store_dwordx4 v[220:223],v6,s[36:39],s86 offen offset:3072 sc1
v_mfma_f32_16x16x32_bf16 a[232:235],a[140:143],v[220:223],a[232:235]
v_mfma_f32_16x16x32_bf16 a[236:239],a[140:143],v[252:255],a[236:239]
ds_read_b64_tr_b16 a[140:141],v1 offset:30912
ds_read_b64_tr_b16 a[142:143],v1 offset:36032
v_mfma_f32_16x16x32_bf16 a[236:239],a[136:139],v[248:251],a[236:239]
ds_read_b64_tr_b16 a[136:137],v1 offset:20672
v_mfma_f32_16x16x32_bf16 a[236:239],a[132:135],v[236:239],a[236:239]
buffer_load_dwordx4 v[240:243],v6,s[28:31],s78 offen offset:3072 sc1
v_mfma_f32_16x16x32_bf16 a[236:239],a[128:131],v[232:235],a[236:239]
ds_read_b64_tr_b16 a[128:129],v1 offset:192
ds_read_b64_tr_b16 a[130:131],v1 offset:5312
v_mfma_f32_16x16x32_bf16 a[240:243],a[144:147],v[136:139],a[240:243]
ds_read_b64_tr_b16 a[132:133],v1 offset:10432
v_mfma_f32_16x16x32_bf16 a[240:243],a[148:151],v[140:143],a[240:243]
ds_read_b64_tr_b16 a[134:135],v1 offset:15552
v_mfma_f32_16x16x32_bf16 a[240:243],a[152:155],v[152:155],a[240:243]
ds_read_b64_tr_b16 a[138:139],v1 offset:25792
v_mfma_f32_16x16x32_bf16 a[240:243],a[156:159],v[156:159],a[240:243]
buffer_load_dwordx4 v9,s[72:75],s80 offen offset:2048 lds
v_mfma_f32_16x16x32_bf16 a[244:247],a[156:159],v[188:191],a[244:247]
v_mfma_f32_16x16x32_bf16 a[244:247],a[152:155],v[184:187],a[244:247]
v_mfma_f32_16x16x32_bf16 a[244:247],a[148:151],v[172:175],a[244:247]
v_mfma_f32_16x16x32_bf16 a[244:247],a[144:147],v[168:171],a[244:247]
buffer_load_dwordx4 v[148:151],v6,s[28:31],s79 offen sc1
v_mfma_f32_16x16x32_bf16 a[248:251],a[144:147],v[200:203],a[248:251]
v_mfma_f32_16x16x32_bf16 a[248:251],a[148:151],v[204:207],a[248:251]
v_mfma_f32_16x16x32_bf16 a[248:251],a[152:155],v[216:219],a[248:251]
v_mfma_f32_16x16x32_bf16 a[248:251],a[156:159],v[220:223],a[248:251]
v_mfma_f32_16x16x32_bf16 a[252:255],a[156:159],v[252:255],a[252:255]
ds_read_b64_tr_b16 a[156:157],v2 offset:30944
ds_read_b64_tr_b16 a[158:159],v2 offset:36064
v_mfma_f32_16x16x32_bf16 a[252:255],a[152:155],v[248:251],a[252:255]
buffer_store_dwordx4 v[232:235],v6,s[36:39],s87 offen sc1
v_mfma_f32_16x16x32_bf16 a[252:255],a[148:151],v[236:239],a[252:255]
ds_read_b64_tr_b16 a[148:149],v2 offset:10464
ds_read_b64_tr_b16 a[150:151],v2 offset:15584
v_mfma_f32_16x16x32_bf16 a[252:255],a[144:147],v[232:235],a[252:255]
ds_read_b64_tr_b16 a[144:145],v2 offset:224
v_mfma_f32_16x16x32_bf16 a[0:3],a[160:163],v[136:139],a[0:3]
s_waitcnt lgkmcnt(5)
ds_read_b64_tr_b16 a[146:147],v2 offset:5344
v_mfma_f32_16x16x32_bf16 a[0:3],a[164:167],v[140:143],a[0:3]
buffer_load_dwordx4 v[180:183],v6,s[28:31],s79 offen offset:1024 sc1
v_mfma_f32_16x16x32_bf16 a[0:3],a[168:171],v[152:155],a[0:3]
ds_read_b64_tr_b16 a[152:153],v2 offset:20704
ds_read_b64_tr_b16 a[154:155],v2 offset:25824
v_mfma_f32_16x16x32_bf16 a[0:3],a[172:175],v[156:159],a[0:3]
v_mfma_f32_16x16x32_bf16 a[4:7],a[172:175],v[188:191],a[4:7]
v_mfma_f32_16x16x32_bf16 a[4:7],a[168:171],v[184:187],a[4:7]
buffer_load_dwordx4 v10,s[72:75],s80 offen offset:3072 lds
v_mfma_f32_16x16x32_bf16 a[4:7],a[164:167],v[172:175],a[4:7]
v_mfma_f32_16x16x32_bf16 a[4:7],a[160:163],v[168:171],a[4:7]
v_mfma_f32_16x16x32_bf16 a[8:11],a[160:163],v[200:203],a[8:11]
v_mfma_f32_16x16x32_bf16 a[8:11],a[164:167],v[204:207],a[8:11]
v_mfma_f32_16x16x32_bf16 a[8:11],a[168:171],v[216:219],a[8:11]
buffer_load_dwordx4 v[212:215],v6,s[28:31],s79 offen offset:2048 sc1
)A"
R"A(v_mfma_f32_16x16x32_bf16 a[8:11],a[172:175],v[220:223],a[8:11]
v_mfma_f32_16x16x32_bf16 a[12:15],a[172:175],v[252:255],a[12:15]
ds_read_b64_tr_b16 a[172:173],v1 offset:30976
ds_read_b64_tr_b16 a[174:175],v1 offset:36096
v_mfma_f32_16x16x32_bf16 a[12:15],a[168:171],v[248:251],a[12:15]
ds_read_b64_tr_b16 a[168:169],v1 offset:20736
v_mfma_f32_16x16x32_bf16 a[12:15],a[164:167],v[236:239],a[12:15]
s_addk_i32 m0,4096
ds_read_b64_tr_b16 a[164:165],v1 offset:10496
v_mfma_f32_16x16x32_bf16 a[12:15],a[160:163],v[232:235],a[12:15]
ds_read_b64_tr_b16 a[160:161],v1 offset:256
v_mfma_f32_16x16x32_bf16 a[16:19],a[176:179],v[136:139],a[16:19]
ds_read_b64_tr_b16 a[162:163],v1 offset:5376
ds_read_b64_tr_b16 a[166:167],v1 offset:15616
v_mfma_f32_16x16x32_bf16 a[16:19],a[180:183],v[140:143],a[16:19]
buffer_store_dwordx4 v[236:239],v6,s[36:39],s87 offen offset:1024 sc1
v_mfma_f32_16x16x32_bf16 a[16:19],a[184:187],v[152:155],a[16:19]
ds_read_b64_tr_b16 a[170:171],v1 offset:25856
v_add_u32 v1,40960,v1
v_mfma_f32_16x16x32_bf16 a[16:19],a[188:191],v[156:159],a[16:19]
ds_read_b64_tr_b16 a[96:97],v1 offset:128
ds_read_b64_tr_b16 a[98:99],v1 offset:5248
v_mfma_f32_16x16x32_bf16 a[20:23],a[188:191],v[188:191],a[20:23]
ds_read_b64_tr_b16 a[100:101],v1 offset:10368
v_mfma_f32_16x16x32_bf16 a[20:23],a[184:187],v[184:187],a[20:23]
buffer_load_dwordx4 v11,s[72:75],s80 offen lds
v_mfma_f32_16x16x32_bf16 a[20:23],a[180:183],v[172:175],a[20:23]
ds_read_b64_tr_b16 a[102:103],v1 offset:15488
ds_read_b64_tr_b16 a[104:105],v1 offset:20608
v_mfma_f32_16x16x32_bf16 a[20:23],a[176:179],v[168:171],a[20:23]
ds_read_b64_tr_b16 a[106:107],v1 offset:25728
v_mfma_f32_16x16x32_bf16 a[24:27],a[176:179],v[200:203],a[24:27]
ds_read_b64_tr_b16 a[108:109],v1 offset:30848
v_mfma_f32_16x16x32_bf16 a[24:27],a[180:183],v[204:207],a[24:27]
buffer_load_dwordx4 v[244:247],v6,s[28:31],s79 offen offset:3072 sc1
v_mfma_f32_16x16x32_bf16 a[24:27],a[184:187],v[216:219],a[24:27]
ds_read_b64_tr_b16 a[110:111],v1 offset:35968
v_mfma_f32_16x16x32_bf16 a[24:27],a[188:191],v[220:223],a[24:27]
v_mfma_f32_16x16x32_bf16 a[28:31],a[188:191],v[252:255],a[28:31]
ds_read_b64_tr_b16 a[188:189],v2 offset:31008
ds_read_b64_tr_b16 a[190:191],v2 offset:36128
v_mfma_f32_16x16x32_bf16 a[28:31],a[184:187],v[248:251],a[28:31]
s_addk_i32 m0,1024
ds_read_b64_tr_b16 a[184:185],v2 offset:20768
v_mfma_f32_16x16x32_bf16 a[28:31],a[180:183],v[236:239],a[28:31]
ds_read_b64_tr_b16 a[180:181],v2 offset:10528
v_mfma_f32_16x16x32_bf16 a[28:31],a[176:179],v[232:235],a[28:31]
ds_read_b64_tr_b16 a[176:177],v2 offset:288
v_mfma_f32_16x16x32_bf16 a[32:35],a[128:131],v[136:139],a[32:35]
buffer_load_dwordx4 v7,s[72:75],s83 offen lds
v_mfma_f32_16x16x32_bf16 a[32:35],a[132:135],v[140:143],a[32:35]
ds_read_b64_tr_b16 a[178:179],v2 offset:5408
ds_read_b64_tr_b16 a[182:183],v2 offset:15648
v_mfma_f32_16x16x32_bf16 a[32:35],a[136:139],v[152:155],a[32:35]
ds_read_b64_tr_b16 a[186:187],v2 offset:25888
v_add_u32 v2,40960,v2
v_mfma_f32_16x16x32_bf16 a[32:35],a[140:143],v[156:159],a[32:35]
ds_read_b64_tr_b16 a[112:113],v2 offset:160
v_mfma_f32_16x16x32_bf16 a[36:39],a[140:143],v[188:191],a[36:39]
ds_read_b64_tr_b16 a[114:115],v2 offset:5280
v_mfma_f32_16x16x32_bf16 a[36:39],a[136:139],v[184:187],a[36:39]
buffer_store_dwordx4 v[248:251],v6,s[36:39],s87 offen offset:2048 sc1
v_mfma_f32_16x16x32_bf16 a[36:39],a[132:135],v[172:175],a[36:39]
ds_read_b64_tr_b16 a[116:117],v2 offset:10400
ds_read_b64_tr_b16 a[118:119],v2 offset:15520
v_mfma_f32_16x16x32_bf16 a[36:39],a[128:131],v[168:171],a[36:39]
ds_read_b64_tr_b16 a[120:121],v2 offset:20640
v_mfma_f32_16x16x32_bf16 a[40:43],a[128:131],v[200:203],a[40:43]
ds_read_b64_tr_b16 a[122:123],v2 offset:25760
v_mfma_f32_16x16x32_bf16 a[40:43],a[132:135],v[204:207],a[40:43]
ds_read_b64_tr_b16 a[124:125],v2 offset:30880
v_mfma_f32_16x16x32_bf16 a[40:43],a[136:139],v[216:219],a[40:43]
ds_read_b64_tr_b16 a[126:127],v2 offset:36000
v_mfma_f32_16x16x32_bf16 a[40:43],a[140:143],v[220:223],a[40:43]
v_mfma_f32_16x16x32_bf16 a[44:47],a[140:143],v[252:255],a[44:47]
ds_read_b64_tr_b16 a[140:141],v1 offset:30720
ds_read_b64_tr_b16 a[142:143],v1 offset:35840
v_mfma_f32_16x16x32_bf16 a[44:47],a[136:139],v[248:251],a[44:47]
ds_read_b64_tr_b16 a[136:137],v1 offset:20480
v_mfma_f32_16x16x32_bf16 a[44:47],a[132:135],v[236:239],a[44:47]
ds_read_b64_tr_b16 a[132:133],v1 offset:10240
v_mfma_f32_16x16x32_bf16 a[44:47],a[128:131],v[232:235],a[44:47]
s_waitcnt lgkmcnt(7)
buffer_load_dwordx4 v8,s[72:75],s83 offen offset:1024 lds
v_mfma_f32_16x16x32_bf16 a[48:51],a[144:147],v[136:139],a[48:51]
ds_read_b64_tr_b16 a[128:129],v1
ds_read_b64_tr_b16 a[130:131],v1 offset:5120
v_mfma_f32_16x16x32_bf16 a[48:51],a[148:151],v[140:143],a[48:51]
ds_read_b64_tr_b16 a[134:135],v1 offset:15360
v_mfma_f32_16x16x32_bf16 a[48:51],a[152:155],v[152:155],a[48:51]
ds_read_b64_tr_b16 a[138:139],v1 offset:25600
v_mfma_f32_16x16x32_bf16 a[48:51],a[156:159],v[156:159],a[48:51]
s_cmp_ge_u32 1,s71
s_cselect_b32 s76,4,2
v_mfma_f32_16x16x32_bf16 a[52:55],a[156:159],v[188:191],a[52:55]
s_bitcmp1_b32 s54,s76
s_cselect_b32 s77,8192,0
s_lshl_b32 s76,s76,17
v_mfma_f32_16x16x32_bf16 a[52:55],a[152:155],v[184:187],a[52:55]
s_add_u32 s76,s76,s77
s_add_u32 s77,s76,4096
s_add_u32 s78,s76,16384
v_mfma_f32_16x16x32_bf16 a[52:55],a[148:151],v[172:175],a[52:55]
s_add_u32 s79,s76,20480
s_max_i32 s76,s76,s18
s_max_i32 s77,s77,s19
v_mfma_f32_16x16x32_bf16 a[52:55],a[144:147],v[168:171],a[52:55]
s_max_i32 s78,s78,s100
s_max_i32 s79,s79,s101
v_mfma_f32_16x16x32_bf16 a[56:59],a[144:147],v[200:203],a[56:59]
v_mfma_f32_16x16x32_bf16 a[56:59],a[148:151],v[204:207],a[56:59]
buffer_store_dwordx4 v[252:255],v6,s[36:39],s87 offen offset:3072 sc1
v_mfma_f32_16x16x32_bf16 a[56:59],a[152:155],v[216:219],a[56:59]
v_mfma_f32_16x16x32_bf16 a[56:59],a[156:159],v[220:223],a[56:59]
v_mfma_f32_16x16x32_bf16 a[60:63],a[156:159],v[252:255],a[60:63]
ds_read_b64_tr_b16 a[156:157],v2 offset:30752
ds_read_b64_tr_b16 a[158:159],v2 offset:35872
v_mfma_f32_16x16x32_bf16 a[60:63],a[152:155],v[248:251],a[60:63]
ds_read_b64_tr_b16 a[152:153],v2 offset:20512
v_mfma_f32_16x16x32_bf16 a[60:63],a[148:151],v[236:239],a[60:63]
buffer_load_dwordx4 v9,s[72:75],s83 offen offset:2048 lds
v_mfma_f32_16x16x32_bf16 a[60:63],a[144:147],v[232:235],a[60:63]
ds_read_b64_tr_b16 a[144:145],v2 offset:32
ds_read_b64_tr_b16 a[146:147],v2 offset:5152
v_mfma_f32_16x16x32_bf16 a[64:67],a[160:163],v[136:139],a[64:67]
ds_read_b64_tr_b16 a[148:149],v2 offset:10272
v_mfma_f32_16x16x32_bf16 a[64:67],a[164:167],v[140:143],a[64:67]
ds_read_b64_tr_b16 a[150:151],v2 offset:15392
v_mfma_f32_16x16x32_bf16 a[64:67],a[168:171],v[152:155],a[64:67]
ds_read_b64_tr_b16 a[154:155],v2 offset:25632
v_mfma_f32_16x16x32_bf16 a[64:67],a[172:175],v[156:159],a[64:67]
v_mfma_f32_16x16x32_bf16 a[68:71],a[172:175],v[188:191],a[68:71]
v_mfma_f32_16x16x32_bf16 a[68:71],a[168:171],v[184:187],a[68:71]
v_mfma_f32_16x16x32_bf16 a[68:71],a[164:167],v[172:175],a[68:71]
v_mfma_f32_16x16x32_bf16 a[68:71],a[160:163],v[168:171],a[68:71]
v_mfma_f32_16x16x32_bf16 a[72:75],a[160:163],v[200:203],a[72:75]
v_mfma_f32_16x16x32_bf16 a[72:75],a[164:167],v[204:207],a[72:75]
v_mfma_f32_16x16x32_bf16 a[72:75],a[168:171],v[216:219],a[72:75]
v_mfma_f32_16x16x32_bf16 a[72:75],a[172:175],v[220:223],a[72:75]
buffer_load_dwordx4 v[64:67],v6,s[28:31],s76 offen sc1
v_mfma_f32_16x16x32_bf16 a[76:79],a[172:175],v[252:255],a[76:79]
ds_read_b64_tr_b16 a[172:173],v1 offset:30784
ds_read_b64_tr_b16 a[174:175],v1 offset:35904
v_mfma_f32_16x16x32_bf16 a[76:79],a[168:171],v[248:251],a[76:79]
ds_read_b64_tr_b16 a[168:169],v1 offset:20544
v_mfma_f32_16x16x32_bf16 a[76:79],a[164:167],v[236:239],a[76:79]
ds_read_b64_tr_b16 a[164:165],v1 offset:10304
v_mfma_f32_16x16x32_bf16 a[76:79],a[160:163],v[232:235],a[76:79]
buffer_load_dwordx4 v10,s[72:75],s83 offen offset:3072 lds
v_mfma_f32_16x16x32_bf16 a[80:83],a[176:179],v[136:139],a[80:83]
ds_read_b64_tr_b16 a[160:161],v1 offset:64
ds_read_b64_tr_b16 a[162:163],v1 offset:5184
v_mfma_f32_16x16x32_bf16 a[80:83],a[180:183],v[140:143],a[80:83]
ds_read_b64_tr_b16 a[166:167],v1 offset:15424
v_mfma_f32_16x16x32_bf16 a[80:83],a[184:187],v[152:155],a[80:83]
ds_read_b64_tr_b16 a[170:171],v1 offset:25664
v_mfma_f32_16x16x32_bf16 a[80:83],a[188:191],v[156:159],a[80:83]
buffer_load_dwordx4 v[68:71],v6,s[28:31],s76 offen offset:1024 sc1
v_mfma_f32_16x16x32_bf16 a[84:87],a[188:191],v[188:191],a[84:87]
v_mfma_f32_16x16x32_bf16 a[84:87],a[184:187],v[184:187],a[84:87]
v_mfma_f32_16x16x32_bf16 a[84:87],a[180:183],v[172:175],a[84:87]
v_mfma_f32_16x16x32_bf16 a[84:87],a[176:179],v[168:171],a[84:87]
v_mfma_f32_16x16x32_bf16 a[88:91],a[176:179],v[200:203],a[88:91]
v_mfma_f32_16x16x32_bf16 a[88:91],a[180:183],v[204:207],a[88:91]
s_addk_i32 m0,4096
v_mfma_f32_16x16x32_bf16 a[88:91],a[184:187],v[216:219],a[88:91]
v_mfma_f32_16x16x32_bf16 a[88:91],a[188:191],v[220:223],a[88:91]
buffer_load_dwordx4 v[72:75],v6,s[28:31],s76 offen offset:2048 sc1
v_mfma_f32_16x16x32_bf16 a[92:95],a[188:191],v[252:255],a[92:95]
ds_read_b64_tr_b16 a[188:189],v2 offset:30816
ds_read_b64_tr_b16 a[190:191],v2 offset:35936
v_mfma_f32_16x16x32_bf16 a[92:95],a[184:187],v[248:251],a[92:95]
ds_read_b64_tr_b16 a[184:185],v2 offset:20576
v_mfma_f32_16x16x32_bf16 a[92:95],a[180:183],v[236:239],a[92:95]
s_cmp_ge_u32 0,s71
s_cselect_b32 s80,3,1
s_mul_i32 s80,s80,163840
v_mfma_f32_16x16x32_bf16 a[92:95],a[176:179],v[232:235],a[92:95]
s_add_u32 s80,s80,s81
buffer_load_dwordx4 v11,s[72:75],s83 offen lds
s_waitcnt vmcnt(10) lgkmcnt(3)
v_mfma_f32_16x16x32_bf16 a[192:195],a[128:131],v[128:131],a[192:195]
s_add_u32 s83,s80,20480
ds_read_b64_tr_b16 a[176:177],v2 offset:96
ds_read_b64_tr_b16 a[178:179],v2 offset:5216
v_mfma_f32_16x16x32_bf16 a[192:195],a[132:135],v[160:163],a[192:195]
ds_read_b64_tr_b16 a[180:181],v2 offset:10336
v_mfma_f32_16x16x32_bf16 a[192:195],a[136:139],v[192:195],a[192:195]
ds_read_b64_tr_b16 a[182:183],v2 offset:15456
v_mfma_f32_16x16x32_bf16 a[192:195],a[140:143],v[224:227],a[192:195]
buffer_load_dwordx4 v[76:79],v6,s[28:31],s76 offen offset:3072 sc1
v_mfma_f32_16x16x32_bf16 a[196:199],a[140:143],v[228:231],a[196:199]
ds_read_b64_tr_b16 a[186:187],v2 offset:25696
v_mfma_f32_16x16x32_bf16 a[196:199],a[136:139],v[196:199],a[196:199]
v_mfma_f32_16x16x32_bf16 a[196:199],a[132:135],v[164:167],a[196:199]
v_mfma_f32_16x16x32_bf16 a[196:199],a[128:131],v[132:135],a[196:199]
v_mfma_f32_16x16x32_bf16 a[200:203],a[128:131],v[144:147],a[200:203]
v_mfma_f32_16x16x32_bf16 a[200:203],a[132:135],v[176:179],a[200:203]
v_mfma_f32_16x16x32_bf16 a[200:203],a[136:139],v[208:211],a[200:203]
v_mfma_f32_16x16x32_bf16 a[200:203],a[140:143],v[240:243],a[200:203]
s_lshl_b32 s95,s42,2
buffer_load_dwordx4 v[80:83],v6,s[28:31],s77 offen sc1
v_mfma_f32_16x16x32_bf16 a[204:207],a[140:143],v[244:247],a[204:207]
s_add_u32 s95,s95,s40
s_lshl_b32 s95,s95,4
v_bfe_u32 v19,s55,13,1
v_mfma_f32_16x16x32_bf16 a[204:207],a[136:139],v[212:215],a[204:207]
v_mov_b32 v18,s68
v_or_b32 v19,s69,v19
v_mfma_f32_16x16x32_bf16 a[204:207],a[132:135],v[180:183],a[204:207]
ds_read_b64_tr_b16 a[132:133],v1 offset:10432
ds_read_b64_tr_b16 a[134:135],v1 offset:15552
v_mfma_f32_16x16x32_bf16 a[204:207],a[128:131],v[148:151],a[204:207]
s_barrier
ds_read_b64_tr_b16 a[128:129],v1 offset:192
ds_read_b64_tr_b16 a[130:131],v1 offset:5312
v_mfma_f32_16x16x32_bf16 a[208:211],a[144:147],v[128:131],a[208:211]
ds_read_b64_tr_b16 a[136:137],v1 offset:20672
v_mfma_f32_16x16x32_bf16 a[208:211],a[148:151],v[160:163],a[208:211]
ds_read_b64_tr_b16 a[138:139],v1 offset:25792
v_mfma_f32_16x16x32_bf16 a[208:211],a[152:155],v[192:195],a[208:211]
s_add_u32 m0,s82,0
s_waitcnt vmcnt(8)
s_mov_b64 exec,1
buffer_store_dwordx2 v[18:19],v6,s[64:67],s95 offen offset:1024 sc1
s_mov_b64 exec,-1
v_mfma_f32_16x16x32_bf16 a[208:211],a[156:159],v[224:227],a[208:211]
ds_read_b64_tr_b16 a[140:141],v1 offset:30912
ds_read_b64_tr_b16 a[142:143],v1 offset:36032
v_mfma_f32_16x16x32_bf16 a[212:215],a[156:159],v[228:231],a[212:215]
v_mfma_f32_16x16x32_bf16 a[212:215],a[152:155],v[196:199],a[212:215]
)A"
R"A(v_mfma_f32_16x16x32_bf16 a[212:215],a[148:151],v[164:167],a[212:215]
buffer_load_dwordx4 v[84:87],v6,s[28:31],s77 offen offset:1024 sc1
v_mfma_f32_16x16x32_bf16 a[212:215],a[144:147],v[132:135],a[212:215]
v_mfma_f32_16x16x32_bf16 a[216:219],a[144:147],v[144:147],a[216:219]
v_mfma_f32_16x16x32_bf16 a[216:219],a[148:151],v[176:179],a[216:219]
v_mfma_f32_16x16x32_bf16 a[216:219],a[152:155],v[208:211],a[216:219]
buffer_load_dwordx4 v7,s[72:75],s80 offen lds
v_mfma_f32_16x16x32_bf16 a[216:219],a[156:159],v[240:243],a[216:219]
v_mfma_f32_16x16x32_bf16 a[220:223],a[156:159],v[244:247],a[220:223]
ds_read_b64_tr_b16 a[156:157],v2 offset:30944
ds_read_b64_tr_b16 a[158:159],v2 offset:36064
v_mfma_f32_16x16x32_bf16 a[220:223],a[152:155],v[212:215],a[220:223]
ds_read_b64_tr_b16 a[152:153],v2 offset:20704
v_mfma_f32_16x16x32_bf16 a[220:223],a[148:151],v[180:183],a[220:223]
buffer_load_dwordx4 v[88:91],v6,s[28:31],s77 offen offset:2048 sc1
v_mfma_f32_16x16x32_bf16 a[220:223],a[144:147],v[148:151],a[220:223]
ds_read_b64_tr_b16 a[144:145],v2 offset:224
ds_read_b64_tr_b16 a[146:147],v2 offset:5344
v_mfma_f32_16x16x32_bf16 a[224:227],a[160:163],v[128:131],a[224:227]
ds_read_b64_tr_b16 a[148:149],v2 offset:10464
v_mfma_f32_16x16x32_bf16 a[224:227],a[164:167],v[160:163],a[224:227]
ds_read_b64_tr_b16 a[150:151],v2 offset:15584
v_mfma_f32_16x16x32_bf16 a[224:227],a[168:171],v[192:195],a[224:227]
buffer_load_dwordx4 v[92:95],v6,s[28:31],s77 offen offset:3072 sc1
v_mfma_f32_16x16x32_bf16 a[224:227],a[172:175],v[224:227],a[224:227]
ds_read_b64_tr_b16 a[154:155],v2 offset:25824
v_mfma_f32_16x16x32_bf16 a[228:231],a[172:175],v[228:231],a[228:231]
v_mfma_f32_16x16x32_bf16 a[228:231],a[168:171],v[196:199],a[228:231]
v_mfma_f32_16x16x32_bf16 a[228:231],a[164:167],v[164:167],a[228:231]
buffer_load_dwordx4 v8,s[72:75],s80 offen offset:1024 lds
v_mfma_f32_16x16x32_bf16 a[228:231],a[160:163],v[132:135],a[228:231]
v_mfma_f32_16x16x32_bf16 a[232:235],a[160:163],v[144:147],a[232:235]
v_mfma_f32_16x16x32_bf16 a[232:235],a[164:167],v[176:179],a[232:235]
v_mfma_f32_16x16x32_bf16 a[232:235],a[168:171],v[208:211],a[232:235]
buffer_load_dwordx4 v[96:99],v6,s[28:31],s78 offen sc1
v_mfma_f32_16x16x32_bf16 a[232:235],a[172:175],v[240:243],a[232:235]
v_mfma_f32_16x16x32_bf16 a[236:239],a[172:175],v[244:247],a[236:239]
ds_read_b64_tr_b16 a[172:173],v1 offset:30976
ds_read_b64_tr_b16 a[174:175],v1 offset:36096
v_mfma_f32_16x16x32_bf16 a[236:239],a[168:171],v[212:215],a[236:239]
ds_read_b64_tr_b16 a[168:169],v1 offset:20736
v_mfma_f32_16x16x32_bf16 a[236:239],a[164:167],v[180:183],a[236:239]
ds_read_b64_tr_b16 a[164:165],v1 offset:10496
v_mfma_f32_16x16x32_bf16 a[236:239],a[160:163],v[148:151],a[236:239]
s_waitcnt lgkmcnt(4)
ds_read_b64_tr_b16 a[160:161],v1 offset:256
v_mfma_f32_16x16x32_bf16 a[240:243],a[176:179],v[128:131],a[240:243]
ds_read_b64_tr_b16 a[162:163],v1 offset:5376
ds_read_b64_tr_b16 a[166:167],v1 offset:15616
v_mfma_f32_16x16x32_bf16 a[240:243],a[180:183],v[160:163],a[240:243]
ds_read_b64_tr_b16 a[170:171],v1 offset:25856
v_add_u32 v1,81920,v1
v_mfma_f32_16x16x32_bf16 a[240:243],a[184:187],v[192:195],a[240:243]
buffer_load_dwordx4 v[100:103],v6,s[28:31],s78 offen offset:1024 sc1
v_mfma_f32_16x16x32_bf16 a[240:243],a[188:191],v[224:227],a[240:243]
v_mfma_f32_16x16x32_bf16 a[244:247],a[188:191],v[228:231],a[244:247]
v_mfma_f32_16x16x32_bf16 a[244:247],a[184:187],v[196:199],a[244:247]
v_mfma_f32_16x16x32_bf16 a[244:247],a[180:183],v[164:167],a[244:247]
buffer_load_dwordx4 v9,s[72:75],s80 offen offset:2048 lds
v_mfma_f32_16x16x32_bf16 a[244:247],a[176:179],v[132:135],a[244:247]
v_mfma_f32_16x16x32_bf16 a[248:251],a[176:179],v[144:147],a[248:251]
v_mfma_f32_16x16x32_bf16 a[248:251],a[180:183],v[176:179],a[248:251]
v_mfma_f32_16x16x32_bf16 a[248:251],a[184:187],v[208:211],a[248:251]
buffer_load_dwordx4 v[104:107],v6,s[28:31],s78 offen offset:2048 sc1
v_mfma_f32_16x16x32_bf16 a[248:251],a[188:191],v[240:243],a[248:251]
v_mfma_f32_16x16x32_bf16 a[252:255],a[188:191],v[244:247],a[252:255]
ds_read_b64_tr_b16 a[188:189],v2 offset:31008
ds_read_b64_tr_b16 a[190:191],v2 offset:36128
v_mfma_f32_16x16x32_bf16 a[252:255],a[184:187],v[212:215],a[252:255]
ds_read_b64_tr_b16 a[184:185],v2 offset:20768
v_mfma_f32_16x16x32_bf16 a[252:255],a[180:183],v[180:183],a[252:255]
ds_read_b64_tr_b16 a[180:181],v2 offset:10528
v_mfma_f32_16x16x32_bf16 a[252:255],a[176:179],v[148:151],a[252:255]
ds_read_b64_tr_b16 a[176:177],v2 offset:288
v_mfma_f32_16x16x32_bf16 a[0:3],a[96:99],v[128:131],a[0:3]
ds_read_b64_tr_b16 a[178:179],v2 offset:5408
ds_read_b64_tr_b16 a[182:183],v2 offset:15648
v_mfma_f32_16x16x32_bf16 a[0:3],a[100:103],v[160:163],a[0:3]
ds_read_b64_tr_b16 a[186:187],v2 offset:25888
v_add_u32 v2,81920,v2
v_mfma_f32_16x16x32_bf16 a[0:3],a[104:107],v[192:195],a[0:3]
buffer_load_dwordx4 v[108:111],v6,s[28:31],s78 offen offset:3072 sc1
v_mfma_f32_16x16x32_bf16 a[0:3],a[108:111],v[224:227],a[0:3]
v_mfma_f32_16x16x32_bf16 a[4:7],a[108:111],v[228:231],a[4:7]
v_mfma_f32_16x16x32_bf16 a[4:7],a[104:107],v[196:199],a[4:7]
v_mfma_f32_16x16x32_bf16 a[4:7],a[100:103],v[164:167],a[4:7]
buffer_load_dwordx4 v10,s[72:75],s80 offen offset:3072 lds
v_mfma_f32_16x16x32_bf16 a[4:7],a[96:99],v[132:135],a[4:7]
v_mfma_f32_16x16x32_bf16 a[8:11],a[96:99],v[144:147],a[8:11]
v_mfma_f32_16x16x32_bf16 a[8:11],a[100:103],v[176:179],a[8:11]
v_mfma_f32_16x16x32_bf16 a[8:11],a[104:107],v[208:211],a[8:11]
buffer_load_dwordx4 v[112:115],v6,s[28:31],s79 offen sc1
v_mfma_f32_16x16x32_bf16 a[8:11],a[108:111],v[240:243],a[8:11]
v_mfma_f32_16x16x32_bf16 a[12:15],a[108:111],v[244:247],a[12:15]
ds_read_b64_tr_b16 a[108:109],v1 offset:30720
ds_read_b64_tr_b16 a[110:111],v1 offset:35840
v_mfma_f32_16x16x32_bf16 a[12:15],a[104:107],v[212:215],a[12:15]
ds_read_b64_tr_b16 a[104:105],v1 offset:20480
v_mfma_f32_16x16x32_bf16 a[12:15],a[100:103],v[180:183],a[12:15]
s_addk_i32 m0,4096
ds_read_b64_tr_b16 a[100:101],v1 offset:10240
v_mfma_f32_16x16x32_bf16 a[12:15],a[96:99],v[148:151],a[12:15]
ds_read_b64_tr_b16 a[96:97],v1
v_mfma_f32_16x16x32_bf16 a[16:19],a[112:115],v[128:131],a[16:19]
ds_read_b64_tr_b16 a[98:99],v1 offset:5120
ds_read_b64_tr_b16 a[102:103],v1 offset:15360
v_mfma_f32_16x16x32_bf16 a[16:19],a[116:119],v[160:163],a[16:19]
buffer_load_dwordx4 v11,s[72:75],s80 offen lds
v_mfma_f32_16x16x32_bf16 a[16:19],a[120:123],v[192:195],a[16:19]
ds_read_b64_tr_b16 a[106:107],v1 offset:25600
v_mfma_f32_16x16x32_bf16 a[16:19],a[124:127],v[224:227],a[16:19]
v_mfma_f32_16x16x32_bf16 a[20:23],a[124:127],v[228:231],a[20:23]
v_mfma_f32_16x16x32_bf16 a[20:23],a[120:123],v[196:199],a[20:23]
buffer_load_dwordx4 v[116:119],v6,s[28:31],s79 offen offset:1024 sc1
v_mfma_f32_16x16x32_bf16 a[20:23],a[116:119],v[164:167],a[20:23]
v_mfma_f32_16x16x32_bf16 a[20:23],a[112:115],v[132:135],a[20:23]
v_mfma_f32_16x16x32_bf16 a[24:27],a[112:115],v[144:147],a[24:27]
v_mfma_f32_16x16x32_bf16 a[24:27],a[116:119],v[176:179],a[24:27]
v_mfma_f32_16x16x32_bf16 a[24:27],a[120:123],v[208:211],a[24:27]
buffer_load_dwordx4 v[120:123],v6,s[28:31],s79 offen offset:2048 sc1
v_mfma_f32_16x16x32_bf16 a[24:27],a[124:127],v[240:243],a[24:27]
v_mfma_f32_16x16x32_bf16 a[28:31],a[124:127],v[244:247],a[28:31]
ds_read_b64_tr_b16 a[124:125],v2 offset:30752
ds_read_b64_tr_b16 a[126:127],v2 offset:35872
v_mfma_f32_16x16x32_bf16 a[28:31],a[120:123],v[212:215],a[28:31]
s_addk_i32 m0,1024
ds_read_b64_tr_b16 a[120:121],v2 offset:20512
v_mfma_f32_16x16x32_bf16 a[28:31],a[116:119],v[180:183],a[28:31]
ds_read_b64_tr_b16 a[116:117],v2 offset:10272
v_mfma_f32_16x16x32_bf16 a[28:31],a[112:115],v[148:151],a[28:31]
s_cmp_ge_u32 2,s71
s_cselect_b32 s76,6,4
ds_read_b64_tr_b16 a[112:113],v2 offset:32
v_mfma_f32_16x16x32_bf16 a[32:35],a[128:131],v[128:131],a[32:35]
buffer_load_dwordx4 v7,s[72:75],s83 offen lds
v_mfma_f32_16x16x32_bf16 a[32:35],a[132:135],v[160:163],a[32:35]
s_bitcmp1_b32 s54,s76
s_cselect_b32 s77,8192,0
s_lshl_b32 s76,s76,17
v_mfma_f32_16x16x32_bf16 a[32:35],a[136:139],v[192:195],a[32:35]
s_add_u32 s76,s76,s77
s_add_u32 s77,s76,4096
s_add_u32 s78,s76,16384
v_mfma_f32_16x16x32_bf16 a[32:35],a[140:143],v[224:227],a[32:35]
s_max_i32 s77,s77,s19
s_max_i32 s78,s78,s100
ds_read_b64_tr_b16 a[114:115],v2 offset:5152
v_mfma_f32_16x16x32_bf16 a[36:39],a[140:143],v[228:231],a[36:39]
buffer_load_dwordx4 v[124:127],v6,s[28:31],s79 offen offset:3072 sc1
v_mfma_f32_16x16x32_bf16 a[36:39],a[136:139],v[196:199],a[36:39]
s_add_u32 s79,s76,20480
s_max_i32 s76,s76,s18
ds_read_b64_tr_b16 a[118:119],v2 offset:15392
v_mfma_f32_16x16x32_bf16 a[36:39],a[132:135],v[164:167],a[36:39]
s_max_i32 s79,s79,s101
ds_read_b64_tr_b16 a[122:123],v2 offset:25632
v_mfma_f32_16x16x32_bf16 a[36:39],a[128:131],v[132:135],a[36:39]
v_mfma_f32_16x16x32_bf16 a[40:43],a[128:131],v[144:147],a[40:43]
v_mfma_f32_16x16x32_bf16 a[40:43],a[132:135],v[176:179],a[40:43]
v_mfma_f32_16x16x32_bf16 a[40:43],a[136:139],v[208:211],a[40:43]
v_mfma_f32_16x16x32_bf16 a[40:43],a[140:143],v[240:243],a[40:43]
v_mfma_f32_16x16x32_bf16 a[44:47],a[140:143],v[244:247],a[44:47]
ds_read_b64_tr_b16 a[140:141],v1 offset:30784
ds_read_b64_tr_b16 a[142:143],v1 offset:35904
v_mfma_f32_16x16x32_bf16 a[44:47],a[136:139],v[212:215],a[44:47]
ds_read_b64_tr_b16 a[136:137],v1 offset:20544
v_mfma_f32_16x16x32_bf16 a[44:47],a[132:135],v[180:183],a[44:47]
ds_read_b64_tr_b16 a[132:133],v1 offset:10304
v_mfma_f32_16x16x32_bf16 a[44:47],a[128:131],v[148:151],a[44:47]
buffer_load_dwordx4 v8,s[72:75],s83 offen offset:1024 lds
v_mfma_f32_16x16x32_bf16 a[48:51],a[144:147],v[128:131],a[48:51]
ds_read_b64_tr_b16 a[128:129],v1 offset:64
ds_read_b64_tr_b16 a[130:131],v1 offset:5184
v_mfma_f32_16x16x32_bf16 a[48:51],a[148:151],v[160:163],a[48:51]
ds_read_b64_tr_b16 a[134:135],v1 offset:15424
v_mfma_f32_16x16x32_bf16 a[48:51],a[152:155],v[192:195],a[48:51]
ds_read_b64_tr_b16 a[138:139],v1 offset:25664
v_mfma_f32_16x16x32_bf16 a[48:51],a[156:159],v[224:227],a[48:51]
s_lshl_b32 s94,s70,4
v_bfe_u32 v24,v6,4,2
v_min_u32 v24,2,v24
v_mfma_f32_16x16x32_bf16 a[52:55],a[156:159],v[228:231],a[52:55]
s_add_u32 s94,s94,s40
v_add_u32 v24,s71,v24
v_add_u32 v24,1,v24
v_mfma_f32_16x16x32_bf16 a[52:55],a[152:155],v[196:199],a[52:55]
s_lshl_b32 s94,s94,4
v_and_b32 v24,3,v24
v_lshlrev_b32 v24,6,v24
v_mfma_f32_16x16x32_bf16 a[52:55],a[148:151],v[164:167],a[52:55]
v_mfma_f32_16x16x32_bf16 a[52:55],a[144:147],v[132:135],a[52:55]
buffer_load_dwordx4 v[136:139],v6,s[28:31],s76 offen sc1
v_mfma_f32_16x16x32_bf16 a[56:59],a[144:147],v[144:147],a[56:59]
v_mfma_f32_16x16x32_bf16 a[56:59],a[148:151],v[176:179],a[56:59]
v_mfma_f32_16x16x32_bf16 a[56:59],a[152:155],v[208:211],a[56:59]
v_mfma_f32_16x16x32_bf16 a[56:59],a[156:159],v[240:243],a[56:59]
v_mfma_f32_16x16x32_bf16 a[60:63],a[156:159],v[244:247],a[60:63]
ds_read_b64_tr_b16 a[156:157],v2 offset:30816
ds_read_b64_tr_b16 a[158:159],v2 offset:35936
v_mfma_f32_16x16x32_bf16 a[60:63],a[152:155],v[212:215],a[60:63]
buffer_load_dwordx4 v[140:143],v6,s[28:31],s76 offen offset:1024 sc1
v_mfma_f32_16x16x32_bf16 a[60:63],a[148:151],v[180:183],a[60:63]
ds_read_b64_tr_b16 a[148:149],v2 offset:10336
ds_read_b64_tr_b16 a[150:151],v2 offset:15456
v_mfma_f32_16x16x32_bf16 a[60:63],a[144:147],v[148:151],a[60:63]
s_waitcnt lgkmcnt(4)
ds_read_b64_tr_b16 a[144:145],v2 offset:96
v_mfma_f32_16x16x32_bf16 a[64:67],a[160:163],v[128:131],a[64:67]
ds_read_b64_tr_b16 a[146:147],v2 offset:5216
v_mfma_f32_16x16x32_bf16 a[64:67],a[164:167],v[160:163],a[64:67]
buffer_load_dwordx4 v9,s[72:75],s83 offen offset:2048 lds
v_mfma_f32_16x16x32_bf16 a[64:67],a[168:171],v[192:195],a[64:67]
ds_read_b64_tr_b16 a[152:153],v2 offset:20576
ds_read_b64_tr_b16 a[154:155],v2 offset:25696
v_mfma_f32_16x16x32_bf16 a[64:67],a[172:175],v[224:227],a[64:67]
v_mfma_f32_16x16x32_bf16 a[68:71],a[172:175],v[228:231],a[68:71]
v_mfma_f32_16x16x32_bf16 a[68:71],a[168:171],v[196:199],a[68:71]
buffer_load_dwordx4 v[152:155],v6,s[28:31],s76 offen offset:2048 sc1
v_mfma_f32_16x16x32_bf16 a[68:71],a[164:167],v[164:167],a[68:71]
v_mfma_f32_16x16x32_bf16 a[68:71],a[160:163],v[132:135],a[68:71]
)A"
R"A(v_mfma_f32_16x16x32_bf16 a[72:75],a[160:163],v[144:147],a[72:75]
v_mfma_f32_16x16x32_bf16 a[72:75],a[164:167],v[176:179],a[72:75]
buffer_load_dwordx4 v[156:159],v6,s[28:31],s76 offen offset:3072 sc1
v_mfma_f32_16x16x32_bf16 a[72:75],a[168:171],v[208:211],a[72:75]
v_mfma_f32_16x16x32_bf16 a[72:75],a[172:175],v[240:243],a[72:75]
v_mfma_f32_16x16x32_bf16 a[76:79],a[172:175],v[244:247],a[76:79]
ds_read_b64_tr_b16 a[172:173],v1 offset:30848
ds_read_b64_tr_b16 a[174:175],v1 offset:35968
v_mfma_f32_16x16x32_bf16 a[76:79],a[168:171],v[212:215],a[76:79]
buffer_load_dwordx4 v10,s[72:75],s83 offen offset:3072 lds
v_mfma_f32_16x16x32_bf16 a[76:79],a[164:167],v[180:183],a[76:79]
ds_read_b64_tr_b16 a[164:165],v1 offset:10368
ds_read_b64_tr_b16 a[166:167],v1 offset:15488
v_mfma_f32_16x16x32_bf16 a[76:79],a[160:163],v[148:151],a[76:79]
ds_read_b64_tr_b16 a[160:161],v1 offset:128
v_mfma_f32_16x16x32_bf16 a[80:83],a[176:179],v[128:131],a[80:83]
ds_read_b64_tr_b16 a[162:163],v1 offset:5248
v_mfma_f32_16x16x32_bf16 a[80:83],a[180:183],v[160:163],a[80:83]
buffer_load_dwordx4 v[168:171],v6,s[28:31],s77 offen sc1
v_mfma_f32_16x16x32_bf16 a[80:83],a[184:187],v[192:195],a[80:83]
ds_read_b64_tr_b16 a[168:169],v1 offset:20608
ds_read_b64_tr_b16 a[170:171],v1 offset:25728
v_mfma_f32_16x16x32_bf16 a[80:83],a[188:191],v[224:227],a[80:83]
v_mfma_f32_16x16x32_bf16 a[84:87],a[188:191],v[228:231],a[84:87]
v_mfma_f32_16x16x32_bf16 a[84:87],a[184:187],v[196:199],a[84:87]
buffer_load_dwordx4 v[172:175],v6,s[28:31],s77 offen offset:1024 sc1
v_mfma_f32_16x16x32_bf16 a[84:87],a[180:183],v[164:167],a[84:87]
v_mfma_f32_16x16x32_bf16 a[84:87],a[176:179],v[132:135],a[84:87]
v_mfma_f32_16x16x32_bf16 a[88:91],a[176:179],v[144:147],a[88:91]
v_mfma_f32_16x16x32_bf16 a[88:91],a[180:183],v[176:179],a[88:91]
s_addk_i32 m0,4096
v_mfma_f32_16x16x32_bf16 a[88:91],a[184:187],v[208:211],a[88:91]
v_mfma_f32_16x16x32_bf16 a[88:91],a[188:191],v[240:243],a[88:91]
buffer_load_dwordx2 v[4:5],v24,s[64:67],s94 offen offset:1024 sc1
v_mfma_f32_16x16x32_bf16 a[92:95],a[188:191],v[244:247],a[92:95]
ds_read_b64_tr_b16 a[188:189],v2 offset:30880
ds_read_b64_tr_b16 a[190:191],v2 offset:36000
v_mfma_f32_16x16x32_bf16 a[92:95],a[184:187],v[212:215],a[92:95]
ds_read_b64_tr_b16 a[184:185],v2 offset:20640
v_mfma_f32_16x16x32_bf16 a[92:95],a[180:183],v[180:183],a[92:95]
ds_read_b64_tr_b16 a[180:181],v2 offset:10400
v_mfma_f32_16x16x32_bf16 a[92:95],a[176:179],v[148:151],a[92:95]
s_waitcnt vmcnt(13)
buffer_load_dwordx4 v[184:187],v6,s[28:31],s77 offen offset:2048 sc1
v_mfma_f32_16x16x32_bf16 a[192:195],a[96:99],v[64:67],a[192:195]
s_lshr_b32 s44,s71,1
ds_read_b64_tr_b16 a[176:177],v2 offset:160
ds_read_b64_tr_b16 a[178:179],v2 offset:5280
v_mfma_f32_16x16x32_bf16 a[192:195],a[100:103],v[68:71],a[192:195]
s_lshl_b32 s43,s41,9
s_mul_i32 s44,s44,22
ds_read_b64_tr_b16 a[182:183],v2 offset:15520
v_mfma_f32_16x16x32_bf16 a[192:195],a[104:107],v[72:75],a[192:195]
s_add_u32 s43,s43,s44
s_add_u32 s43,s43,s71
ds_read_b64_tr_b16 a[186:187],v2 offset:25760
v_mfma_f32_16x16x32_bf16 a[192:195],a[108:111],v[76:79],a[192:195]
s_add_u32 s43,s43,s40
buffer_load_dwordx4 v11,s[72:75],s83 offen lds
v_mfma_f32_16x16x32_bf16 a[196:199],a[108:111],v[92:95],a[196:199]
s_add_u32 s43,s43,s40
s_add_u32 s43,s43,64
s_lshl_b32 s43,s43,14
v_mfma_f32_16x16x32_bf16 a[196:199],a[104:107],v[88:91],a[196:199]
s_mov_b64 s[32:33],s[12:13]
s_and_b32 s87,s55,8192
s_lshl_b32 s84,s70,21
v_mfma_f32_16x16x32_bf16 a[196:199],a[100:103],v[84:87],a[196:199]
s_add_u32 s87,s87,s84
s_add_u32 s32,s32,s43
s_addc_u32 s33,s33,0
v_mfma_f32_16x16x32_bf16 a[196:199],a[96:99],v[80:83],a[196:199]
s_add_u32 s84,s87,0
buffer_load_dwordx4 v[188:191],v6,s[28:31],s77 offen offset:3072 sc1
v_mfma_f32_16x16x32_bf16 a[200:203],a[96:99],v[96:99],a[200:203]
s_add_u32 s85,s87,131072
s_add_u32 s86,s87,262144
s_and_b32 s33,s33,0xffff
v_mfma_f32_16x16x32_bf16 a[200:203],a[100:103],v[100:103],a[200:203]
s_mov_b32 s34,6569984
s_mov_b32 s35,0x20000
s_max_i32 s84,s84,s18
v_mfma_f32_16x16x32_bf16 a[200:203],a[104:107],v[104:107],a[200:203]
s_max_i32 s85,s85,s19
s_max_i32 s86,s86,s100
v_bfe_u32 v18,v6,6,2
v_mfma_f32_16x16x32_bf16 a[200:203],a[108:111],v[108:111],a[200:203]
s_mul_i32 s44,s41,307200
buffer_load_dwordx4 v[200:203],v6,s[28:31],s78 offen sc1
s_waitcnt vmcnt(14)
v_mfma_f32_16x16x32_bf16 a[204:207],a[108:111],v[124:127],a[204:207]
s_mov_b64 s[20:21],s[16:17]
ds_read_b64_tr_b16 a[108:109],v1 offset:30912
ds_read_b64_tr_b16 a[110:111],v1 offset:36032
v_mfma_f32_16x16x32_bf16 a[204:207],a[104:107],v[120:123],a[204:207]
s_lshl_b32 s43,s40,2
ds_read_b64_tr_b16 a[104:105],v1 offset:20672
v_lshrrev_b32 v19,8,v6
v_mfma_f32_16x16x32_bf16 a[204:207],a[100:103],v[116:119],a[204:207]
s_add_u32 s43,s43,s70
ds_read_b64_tr_b16 a[100:101],v1 offset:10432
v_xor_b32 v18,v18,v19
v_mfma_f32_16x16x32_bf16 a[204:207],a[96:99],v[112:115],a[204:207]
s_barrier
buffer_load_dwordx4 v[204:207],v6,s[28:31],s78 offen offset:1024 sc1
v_mfma_f32_16x16x32_bf16 a[208:211],a[112:115],v[64:67],a[208:211]
s_mulk_i32 s43,20480
ds_read_b64_tr_b16 a[96:97],v1 offset:192
ds_read_b64_tr_b16 a[98:99],v1 offset:5312
v_mfma_f32_16x16x32_bf16 a[208:211],a[116:119],v[68:71],a[208:211]
s_mov_b32 s22,307200
ds_read_b64_tr_b16 a[102:103],v1 offset:15552
v_lshlrev_b32 v18,2,v18
v_mfma_f32_16x16x32_bf16 a[208:211],a[120:123],v[72:75],a[208:211]
s_mov_b32 s23,0x20000
ds_read_b64_tr_b16 a[106:107],v1 offset:25792
v_bfe_u32 v19,v6,4,2
v_mfma_f32_16x16x32_bf16 a[208:211],a[124:127],v[76:79],a[208:211]
buffer_load_dwordx4 v[216:219],v6,s[28:31],s78 offen offset:2048 sc1
v_mfma_f32_16x16x32_bf16 a[212:215],a[124:127],v[92:95],a[212:215]
v_or_b32 v18,v18,v19
v_and_b32 v19,16,v6
v_mfma_f32_16x16x32_bf16 a[212:215],a[120:123],v[88:91],a[212:215]
v_mov_b32 v34,0x3f80
v_lshlrev_b32 v19,v19,v34
v_mfma_f32_16x16x32_bf16 a[212:215],a[116:119],v[84:87],a[212:215]
v_lshrrev_b32 v18,1,v18
v_add_u32 v25,4096,v6
v_mfma_f32_16x16x32_bf16 a[212:215],a[112:115],v[80:83],a[212:215]
s_cmp_ge_u32 0,s71
s_cselect_b32 s76,3,1
v_mov_b32 v36,0
v_mfma_f32_16x16x32_bf16 a[216:219],a[112:115],v[96:99],a[216:219]
buffer_load_dwordx4 v[220:223],v6,s[28:31],s78 offen offset:3072 sc1
v_mfma_f32_16x16x32_bf16 a[216:219],a[116:119],v[100:103],a[216:219]
s_bitcmp1_b32 s54,s76
s_cselect_b32 s77,8192,0
s_lshl_b32 s76,s76,17
v_mfma_f32_16x16x32_bf16 a[216:219],a[120:123],v[104:107],a[216:219]
s_add_u32 s76,s76,s77
s_add_u32 s77,s76,4096
s_add_u32 s78,s76,16384
v_mfma_f32_16x16x32_bf16 a[216:219],a[124:127],v[108:111],a[216:219]
s_max_i32 s77,s77,s19
s_max_i32 s78,s78,s100
v_mov_b32 v37,0
v_mfma_f32_16x16x32_bf16 a[220:223],a[124:127],v[124:127],a[220:223]
ds_read_b64_tr_b16 a[124:125],v2 offset:30944
ds_read_b64_tr_b16 a[126:127],v2 offset:36064
v_mfma_f32_16x16x32_bf16 a[220:223],a[120:123],v[120:123],a[220:223]
ds_read_b64_tr_b16 a[120:121],v2 offset:20704
v_mov_b32 v32,0
v_mfma_f32_16x16x32_bf16 a[220:223],a[116:119],v[116:119],a[220:223]
buffer_load_dwordx4 v[232:235],v6,s[28:31],s79 offen sc1
v_mfma_f32_16x16x32_bf16 a[220:223],a[112:115],v[112:115],a[220:223]
ds_read_b64_tr_b16 a[112:113],v2 offset:224
ds_read_b64_tr_b16 a[114:115],v2 offset:5344
v_mfma_f32_16x16x32_bf16 a[224:227],a[128:131],v[64:67],a[224:227]
ds_read_b64_tr_b16 a[116:117],v2 offset:10464
v_mov_b32 v33,0
v_mfma_f32_16x16x32_bf16 a[224:227],a[132:135],v[68:71],a[224:227]
ds_read_b64_tr_b16 a[118:119],v2 offset:15584
v_bfe_u32 v40,v6,4,4
v_mfma_f32_16x16x32_bf16 a[224:227],a[136:139],v[72:75],a[224:227]
ds_read_b64_tr_b16 a[122:123],v2 offset:25824
v_mul_u32_u24 v40,1280,v40
v_mfma_f32_16x16x32_bf16 a[224:227],a[140:143],v[76:79],a[224:227]
v_cmp_eq_u32 vcc,0,v18
s_nop 1
v_cndmask_b32 v34,0,v19,vcc
v_mfma_f32_16x16x32_bf16 a[228:231],a[140:143],v[92:95],a[228:231]
buffer_load_dwordx4 v[236:239],v6,s[28:31],s79 offen offset:1024 sc1
v_mfma_f32_16x16x32_bf16 a[228:231],a[136:139],v[88:91],a[228:231]
v_cmp_eq_u32 vcc,1,v18
s_nop 1
v_cndmask_b32 v35,0,v19,vcc
v_mfma_f32_16x16x32_bf16 a[228:231],a[132:135],v[84:87],a[228:231]
s_add_u32 s20,s20,s44
s_addc_u32 s21,s21,0
v_bfe_u32 v41,v6,8,1
v_mfma_f32_16x16x32_bf16 a[228:231],a[128:131],v[80:83],a[228:231]
s_waitcnt vmcnt(9)
v_cmp_ne_u32 s[90:91],s68,v4
v_and_b32 v4,-2,v5
v_cmp_ne_u32 s[92:93],s69,v4
s_nop 0
s_or_b64 s[90:91],s[90:91],s[92:93]
s_cbranch_scc1 L_fr9_%=
L_fo9_%=:
v_mfma_f32_16x16x32_bf16 a[232:235],a[128:131],v[96:99],a[232:235]
s_mul_i32 s44,s71,320
v_lshl_add_u32 v40,v41,5,v40
v_lshrrev_b32 v41,9,v6
v_mfma_f32_16x16x32_bf16 a[232:235],a[132:135],v[100:103],a[232:235]
s_and_b32 s21,s21,0xffff
s_add_u32 s43,s43,s44
v_lshl_add_u32 v40,v41,4,v40
v_mfma_f32_16x16x32_bf16 a[232:235],a[136:139],v[104:107],a[232:235]
buffer_load_dwordx4 v[248:251],v6,s[28:31],s79 offen offset:2048 sc1
v_mfma_f32_16x16x32_bf16 a[232:235],a[140:143],v[108:111],a[232:235]
v_mfma_f32_16x16x32_bf16 a[236:239],a[140:143],v[124:127],a[236:239]
ds_read_b64_tr_b16 a[140:141],v1 offset:30976
ds_read_b64_tr_b16 a[142:143],v1 offset:36096
v_mfma_f32_16x16x32_bf16 a[236:239],a[136:139],v[120:123],a[236:239]
ds_read_b64_tr_b16 a[136:137],v1 offset:20736
v_mfma_f32_16x16x32_bf16 a[236:239],a[132:135],v[116:119],a[236:239]
ds_read_b64_tr_b16 a[132:133],v1 offset:10496
v_mfma_f32_16x16x32_bf16 a[236:239],a[128:131],v[112:115],a[236:239]
s_waitcnt lgkmcnt(4)
ds_read_b64_tr_b16 a[128:129],v1 offset:256
v_mfma_f32_16x16x32_bf16 a[240:243],a[144:147],v[64:67],a[240:243]
buffer_load_dwordx4 v[252:255],v6,s[28:31],s79 offen offset:3072 sc1
v_mfma_f32_16x16x32_bf16 a[240:243],a[148:151],v[68:71],a[240:243]
s_add_u32 s79,s76,20480
s_max_i32 s76,s76,s18
ds_read_b64_tr_b16 a[130:131],v1 offset:5376
v_mfma_f32_16x16x32_bf16 a[240:243],a[152:155],v[72:75],a[240:243]
s_max_i32 s79,s79,s101
ds_read_b64_tr_b16 a[134:135],v1 offset:15616
ds_read_b64_tr_b16 a[138:139],v1 offset:25856
v_mfma_f32_16x16x32_bf16 a[240:243],a[156:159],v[76:79],a[240:243]
v_subrev_u32 v1,40960,v1
v_mfma_f32_16x16x32_bf16 a[244:247],a[156:159],v[92:95],a[244:247]
buffer_load_dwordx4 v[128:131],v6,s[28:31],s76 offen sc1
v_mfma_f32_16x16x32_bf16 a[244:247],a[152:155],v[88:91],a[244:247]
v_mfma_f32_16x16x32_bf16 a[244:247],a[148:151],v[84:87],a[244:247]
v_mfma_f32_16x16x32_bf16 a[244:247],a[144:147],v[80:83],a[244:247]
v_mfma_f32_16x16x32_bf16 a[248:251],a[144:147],v[96:99],a[248:251]
buffer_load_dwordx4 v[160:163],v6,s[28:31],s76 offen offset:1024 sc1
v_mfma_f32_16x16x32_bf16 a[248:251],a[148:151],v[100:103],a[248:251]
v_mfma_f32_16x16x32_bf16 a[248:251],a[152:155],v[104:107],a[248:251]
v_mfma_f32_16x16x32_bf16 a[248:251],a[156:159],v[108:111],a[248:251]
v_mfma_f32_16x16x32_bf16 a[252:255],a[156:159],v[124:127],a[252:255]
buffer_load_dwordx4 v[192:195],v6,s[28:31],s76 offen offset:2048 sc1
v_mfma_f32_16x16x32_bf16 a[252:255],a[152:155],v[120:123],a[252:255]
ds_read_b64_tr_b16 a[152:153],v2 offset:20768
ds_read_b64_tr_b16 a[154:155],v2 offset:25888
v_mfma_f32_16x16x32_bf16 a[252:255],a[148:151],v[116:119],a[252:255]
ds_read_b64_tr_b16 a[148:149],v2 offset:10528
v_mfma_f32_16x16x32_bf16 a[252:255],a[144:147],v[112:115],a[252:255]
ds_read_b64_tr_b16 a[144:145],v2 offset:288
v_mfma_f32_16x16x32_bf16 a[0:3],a[160:163],v[64:67],a[0:3]
ds_read_b64_tr_b16 a[146:147],v2 offset:5408
v_mfma_f32_16x16x32_bf16 a[0:3],a[164:167],v[68:71],a[0:3]
buffer_load_dwordx4 v[224:227],v6,s[28:31],s76 offen offset:3072 sc1
v_mfma_f32_16x16x32_bf16 a[0:3],a[168:171],v[72:75],a[0:3]
ds_read_b64_tr_b16 a[150:151],v2 offset:15648
ds_read_b64_tr_b16 a[156:157],v2 offset:31008
v_mfma_f32_16x16x32_bf16 a[0:3],a[172:175],v[76:79],a[0:3]
ds_read_b64_tr_b16 a[158:159],v2 offset:36128
v_subrev_u32 v2,40960,v2
v_mfma_f32_16x16x32_bf16 a[4:7],a[172:175],v[92:95],a[4:7]
v_mfma_f32_16x16x32_bf16 a[4:7],a[168:171],v[88:91],a[4:7]
v_mfma_f32_16x16x32_bf16 a[4:7],a[164:167],v[84:87],a[4:7]
v_mfma_f32_16x16x32_bf16 a[4:7],a[160:163],v[80:83],a[4:7]
buffer_load_dwordx4 v[132:135],v6,s[28:31],s77 offen sc1
v_mfma_f32_16x16x32_bf16 a[8:11],a[160:163],v[96:99],a[8:11]
v_mfma_f32_16x16x32_bf16 a[8:11],a[164:167],v[100:103],a[8:11]
)A"
R"A(v_mfma_f32_16x16x32_bf16 a[8:11],a[168:171],v[104:107],a[8:11]
v_mfma_f32_16x16x32_bf16 a[8:11],a[172:175],v[108:111],a[8:11]
v_mfma_f32_16x16x32_bf16 a[12:15],a[172:175],v[124:127],a[12:15]
ds_read_b64_tr_b16 a[172:173],v1 offset:30720
ds_read_b64_tr_b16 a[174:175],v1 offset:35840
v_mfma_f32_16x16x32_bf16 a[12:15],a[168:171],v[120:123],a[12:15]
buffer_load_dwordx4 v[164:167],v6,s[28:31],s77 offen offset:1024 sc1
v_mfma_f32_16x16x32_bf16 a[12:15],a[164:167],v[116:119],a[12:15]
ds_read_b64_tr_b16 a[164:165],v1 offset:10240
ds_read_b64_tr_b16 a[166:167],v1 offset:15360
v_mfma_f32_16x16x32_bf16 a[12:15],a[160:163],v[112:115],a[12:15]
ds_read_b64_tr_b16 a[160:161],v1
v_mfma_f32_16x16x32_bf16 a[16:19],a[176:179],v[64:67],a[16:19]
ds_read_b64_tr_b16 a[162:163],v1 offset:5120
v_mfma_f32_16x16x32_bf16 a[16:19],a[180:183],v[68:71],a[16:19]
ds_read_b64_tr_b16 a[168:169],v1 offset:20480
v_mfma_f32_16x16x32_bf16 a[16:19],a[184:187],v[72:75],a[16:19]
ds_read_b64_tr_b16 a[170:171],v1 offset:25600
v_mfma_f32_16x16x32_bf16 a[16:19],a[188:191],v[76:79],a[16:19]
buffer_load_dwordx4 v[196:199],v6,s[28:31],s77 offen offset:2048 sc1
v_mfma_f32_16x16x32_bf16 a[20:23],a[188:191],v[92:95],a[20:23]
v_mfma_f32_16x16x32_bf16 a[20:23],a[184:187],v[88:91],a[20:23]
v_mfma_f32_16x16x32_bf16 a[20:23],a[180:183],v[84:87],a[20:23]
v_mfma_f32_16x16x32_bf16 a[20:23],a[176:179],v[80:83],a[20:23]
v_mfma_f32_16x16x32_bf16 a[24:27],a[176:179],v[96:99],a[24:27]
v_mfma_f32_16x16x32_bf16 a[24:27],a[180:183],v[100:103],a[24:27]
buffer_load_dwordx4 v[228:231],v6,s[28:31],s77 offen offset:3072 sc1
v_mfma_f32_16x16x32_bf16 a[24:27],a[184:187],v[104:107],a[24:27]
v_mfma_f32_16x16x32_bf16 a[24:27],a[188:191],v[108:111],a[24:27]
v_mfma_f32_16x16x32_bf16 a[28:31],a[188:191],v[124:127],a[28:31]
ds_read_b64_tr_b16 a[188:189],v2 offset:30752
ds_read_b64_tr_b16 a[190:191],v2 offset:35872
v_mfma_f32_16x16x32_bf16 a[28:31],a[184:187],v[120:123],a[28:31]
ds_read_b64_tr_b16 a[184:185],v2 offset:20512
v_mfma_f32_16x16x32_bf16 a[28:31],a[180:183],v[116:119],a[28:31]
ds_read_b64_tr_b16 a[180:181],v2 offset:10272
v_mfma_f32_16x16x32_bf16 a[28:31],a[176:179],v[112:115],a[28:31]
buffer_load_dwordx4 v[144:147],v6,s[28:31],s78 offen sc1
v_mfma_f32_16x16x32_bf16 a[32:35],a[96:99],v[64:67],a[32:35]
ds_read_b64_tr_b16 a[176:177],v2 offset:32
ds_read_b64_tr_b16 a[178:179],v2 offset:5152
v_mfma_f32_16x16x32_bf16 a[32:35],a[100:103],v[68:71],a[32:35]
ds_read_b64_tr_b16 a[182:183],v2 offset:15392
v_mfma_f32_16x16x32_bf16 a[32:35],a[104:107],v[72:75],a[32:35]
ds_read_b64_tr_b16 a[186:187],v2 offset:25632
v_mfma_f32_16x16x32_bf16 a[32:35],a[108:111],v[76:79],a[32:35]
v_mfma_f32_16x16x32_bf16 a[36:39],a[108:111],v[92:95],a[36:39]
v_mfma_f32_16x16x32_bf16 a[36:39],a[104:107],v[88:91],a[36:39]
buffer_load_dwordx4 v[176:179],v6,s[28:31],s78 offen offset:1024 sc1
v_mfma_f32_16x16x32_bf16 a[36:39],a[100:103],v[84:87],a[36:39]
v_mfma_f32_16x16x32_bf16 a[36:39],a[96:99],v[80:83],a[36:39]
v_mfma_f32_16x16x32_bf16 a[40:43],a[96:99],v[96:99],a[40:43]
v_mfma_f32_16x16x32_bf16 a[40:43],a[100:103],v[100:103],a[40:43]
v_mfma_f32_16x16x32_bf16 a[40:43],a[104:107],v[104:107],a[40:43]
v_mfma_f32_16x16x32_bf16 a[40:43],a[108:111],v[108:111],a[40:43]
buffer_load_dwordx4 v[208:211],v6,s[28:31],s78 offen offset:2048 sc1
v_mfma_f32_16x16x32_bf16 a[44:47],a[108:111],v[124:127],a[44:47]
ds_read_b64_tr_b16 a[108:109],v1 offset:30784
ds_read_b64_tr_b16 a[110:111],v1 offset:35904
v_mfma_f32_16x16x32_bf16 a[44:47],a[104:107],v[120:123],a[44:47]
ds_read_b64_tr_b16 a[104:105],v1 offset:20544
v_mfma_f32_16x16x32_bf16 a[44:47],a[100:103],v[116:119],a[44:47]
ds_read_b64_tr_b16 a[100:101],v1 offset:10304
v_mfma_f32_16x16x32_bf16 a[44:47],a[96:99],v[112:115],a[44:47]
ds_read_b64_tr_b16 a[96:97],v1 offset:64
v_mfma_f32_16x16x32_bf16 a[48:51],a[112:115],v[64:67],a[48:51]
ds_read_b64_tr_b16 a[98:99],v1 offset:5184
ds_read_b64_tr_b16 a[102:103],v1 offset:15424
v_mfma_f32_16x16x32_bf16 a[48:51],a[116:119],v[68:71],a[48:51]
buffer_load_dwordx4 v[240:243],v6,s[28:31],s78 offen offset:3072 sc1
v_mfma_f32_16x16x32_bf16 a[48:51],a[120:123],v[72:75],a[48:51]
ds_read_b64_tr_b16 a[106:107],v1 offset:25664
v_mfma_f32_16x16x32_bf16 a[48:51],a[124:127],v[76:79],a[48:51]
v_mfma_f32_16x16x32_bf16 a[52:55],a[124:127],v[92:95],a[52:55]
v_mfma_f32_16x16x32_bf16 a[52:55],a[120:123],v[88:91],a[52:55]
v_mfma_f32_16x16x32_bf16 a[52:55],a[116:119],v[84:87],a[52:55]
v_mfma_f32_16x16x32_bf16 a[52:55],a[112:115],v[80:83],a[52:55]
buffer_load_dwordx4 v[148:151],v6,s[28:31],s79 offen sc1
v_mfma_f32_16x16x32_bf16 a[56:59],a[112:115],v[96:99],a[56:59]
v_mfma_f32_16x16x32_bf16 a[56:59],a[116:119],v[100:103],a[56:59]
s_cmp_ge_u32 1,s71
s_cselect_b32 s80,5,3
s_mul_i32 s80,s80,163840
v_mfma_f32_16x16x32_bf16 a[56:59],a[120:123],v[104:107],a[56:59]
s_add_u32 s80,s80,s81
s_add_u32 s83,s80,20480
v_mfma_f32_16x16x32_bf16 a[56:59],a[124:127],v[108:111],a[56:59]
v_mfma_f32_16x16x32_bf16 a[60:63],a[124:127],v[124:127],a[60:63]
ds_read_b64_tr_b16 a[124:125],v2 offset:30816
ds_read_b64_tr_b16 a[126:127],v2 offset:35936
v_mfma_f32_16x16x32_bf16 a[60:63],a[120:123],v[120:123],a[60:63]
buffer_load_dwordx4 v[180:183],v6,s[28:31],s79 offen offset:1024 sc1
v_mfma_f32_16x16x32_bf16 a[60:63],a[116:119],v[116:119],a[60:63]
ds_read_b64_tr_b16 a[116:117],v2 offset:10336
ds_read_b64_tr_b16 a[118:119],v2 offset:15456
v_mfma_f32_16x16x32_bf16 a[60:63],a[112:115],v[112:115],a[60:63]
s_waitcnt lgkmcnt(4)
ds_read_b64_tr_b16 a[112:113],v2 offset:96
v_mfma_f32_16x16x32_bf16 a[64:67],a[128:131],v[64:67],a[64:67]
ds_read_b64_tr_b16 a[114:115],v2 offset:5216
v_mfma_f32_16x16x32_bf16 a[64:67],a[132:135],v[68:71],a[64:67]
ds_read_b64_tr_b16 a[120:121],v2 offset:20576
v_mfma_f32_16x16x32_bf16 a[64:67],a[136:139],v[72:75],a[64:67]
ds_read_b64_tr_b16 a[122:123],v2 offset:25696
v_mfma_f32_16x16x32_bf16 a[64:67],a[140:143],v[76:79],a[64:67]
buffer_load_dwordx4 v[212:215],v6,s[28:31],s79 offen offset:2048 sc1
v_mfma_f32_16x16x32_bf16 a[68:71],a[140:143],v[92:95],a[68:71]
v_mfma_f32_16x16x32_bf16 a[68:71],a[136:139],v[88:91],a[68:71]
v_mfma_f32_16x16x32_bf16 a[68:71],a[132:135],v[84:87],a[68:71]
v_mfma_f32_16x16x32_bf16 a[68:71],a[128:131],v[80:83],a[68:71]
v_mfma_f32_16x16x32_bf16 a[72:75],a[128:131],v[96:99],a[72:75]
v_mfma_f32_16x16x32_bf16 a[72:75],a[132:135],v[100:103],a[72:75]
buffer_load_dwordx4 v[244:247],v6,s[28:31],s79 offen offset:3072 sc1
v_mfma_f32_16x16x32_bf16 a[72:75],a[136:139],v[104:107],a[72:75]
v_mfma_f32_16x16x32_bf16 a[72:75],a[140:143],v[108:111],a[72:75]
v_mfma_f32_16x16x32_bf16 a[76:79],a[140:143],v[124:127],a[76:79]
ds_read_b64_tr_b16 a[140:141],v1 offset:30848
ds_read_b64_tr_b16 a[142:143],v1 offset:35968
v_mfma_f32_16x16x32_bf16 a[76:79],a[136:139],v[120:123],a[76:79]
ds_read_b64_tr_b16 a[136:137],v1 offset:20608
v_mfma_f32_16x16x32_bf16 a[76:79],a[132:135],v[116:119],a[76:79]
s_add_u32 m0,s82,40960
ds_read_b64_tr_b16 a[132:133],v1 offset:10368
v_mfma_f32_16x16x32_bf16 a[76:79],a[128:131],v[112:115],a[76:79]
ds_read_b64_tr_b16 a[128:129],v1 offset:128
v_mfma_f32_16x16x32_bf16 a[80:83],a[144:147],v[64:67],a[80:83]
ds_read_b64_tr_b16 a[130:131],v1 offset:5248
ds_read_b64_tr_b16 a[134:135],v1 offset:15488
v_mfma_f32_16x16x32_bf16 a[80:83],a[148:151],v[68:71],a[80:83]
buffer_load_dwordx4 v7,s[72:75],s80 offen lds
v_mfma_f32_16x16x32_bf16 a[80:83],a[152:155],v[72:75],a[80:83]
ds_read_b64_tr_b16 a[138:139],v1 offset:25728
v_mfma_f32_16x16x32_bf16 a[80:83],a[156:159],v[76:79],a[80:83]
v_mfma_f32_16x16x32_bf16 a[84:87],a[156:159],v[92:95],a[84:87]
v_mfma_f32_16x16x32_bf16 a[84:87],a[152:155],v[88:91],a[84:87]
v_mfma_f32_16x16x32_bf16 a[84:87],a[148:151],v[84:87],a[84:87]
buffer_load_dwordx4 v8,s[72:75],s80 offen offset:1024 lds
v_mfma_f32_16x16x32_bf16 a[84:87],a[144:147],v[80:83],a[84:87]
v_mfma_f32_16x16x32_bf16 a[88:91],a[144:147],v[96:99],a[88:91]
v_mfma_f32_16x16x32_bf16 a[88:91],a[148:151],v[100:103],a[88:91]
v_mfma_f32_16x16x32_bf16 a[88:91],a[152:155],v[104:107],a[88:91]
v_mfma_f32_16x16x32_bf16 a[88:91],a[156:159],v[108:111],a[88:91]
buffer_load_dwordx4 v9,s[72:75],s80 offen offset:2048 lds
v_mfma_f32_16x16x32_bf16 a[92:95],a[156:159],v[124:127],a[92:95]
ds_read_b64_tr_b16 a[156:157],v2 offset:30880
ds_read_b64_tr_b16 a[158:159],v2 offset:36000
v_mfma_f32_16x16x32_bf16 a[92:95],a[152:155],v[120:123],a[92:95]
ds_read_b64_tr_b16 a[152:153],v2 offset:20640
v_mfma_f32_16x16x32_bf16 a[92:95],a[148:151],v[116:119],a[92:95]
ds_read_b64_tr_b16 a[148:149],v2 offset:10400
v_mfma_f32_16x16x32_bf16 a[92:95],a[144:147],v[112:115],a[92:95]
ds_read_b64_tr_b16 a[144:145],v2 offset:160
v_mfma_f32_16x16x32_bf16 a[192:195],a[160:163],v[136:139],a[192:195]
buffer_load_dwordx4 v10,s[72:75],s80 offen offset:3072 lds
v_mfma_f32_16x16x32_bf16 a[192:195],a[164:167],v[140:143],a[192:195]
ds_read_b64_tr_b16 a[146:147],v2 offset:5280
ds_read_b64_tr_b16 a[150:151],v2 offset:15520
v_mfma_f32_16x16x32_bf16 a[192:195],a[168:171],v[152:155],a[192:195]
s_addk_i32 m0,4096
ds_read_b64_tr_b16 a[154:155],v2 offset:25760
v_mfma_f32_16x16x32_bf16 a[192:195],a[172:175],v[156:159],a[192:195]
s_waitcnt vmcnt(12)
v_mfma_f32_16x16x32_bf16 a[196:199],a[172:175],v[188:191],a[196:199]
v_mfma_f32_16x16x32_bf16 a[196:199],a[168:171],v[184:187],a[196:199]
buffer_load_dwordx4 v11,s[72:75],s80 offen lds
v_mfma_f32_16x16x32_bf16 a[196:199],a[164:167],v[172:175],a[196:199]
v_mfma_f32_16x16x32_bf16 a[196:199],a[160:163],v[168:171],a[196:199]
s_addk_i32 m0,1024
v_mfma_f32_16x16x32_bf16 a[200:203],a[160:163],v[200:203],a[200:203]
v_mfma_f32_16x16x32_bf16 a[200:203],a[164:167],v[204:207],a[200:203]
v_mfma_f32_16x16x32_bf16 a[200:203],a[168:171],v[216:219],a[200:203]
buffer_load_dwordx4 v7,s[72:75],s83 offen lds
v_mfma_f32_16x16x32_bf16 a[200:203],a[172:175],v[220:223],a[200:203]
v_mfma_f32_16x16x32_bf16 a[204:207],a[172:175],v[252:255],a[204:207]
ds_read_b64_tr_b16 a[172:173],v1 offset:30912
ds_read_b64_tr_b16 a[174:175],v1 offset:36032
v_mfma_f32_16x16x32_bf16 a[204:207],a[168:171],v[248:251],a[204:207]
ds_read_b64_tr_b16 a[168:169],v1 offset:20672
v_mfma_f32_16x16x32_bf16 a[204:207],a[164:167],v[236:239],a[204:207]
ds_read_b64_tr_b16 a[164:165],v1 offset:10432
v_mfma_f32_16x16x32_bf16 a[204:207],a[160:163],v[232:235],a[204:207]
s_barrier
buffer_load_dwordx4 v8,s[72:75],s83 offen offset:1024 lds
v_mfma_f32_16x16x32_bf16 a[208:211],a[176:179],v[136:139],a[208:211]
s_cmp_ge_u32 1,s71
s_cselect_b32 s76,5,3
ds_read_b64_tr_b16 a[160:161],v1 offset:192
v_mfma_f32_16x16x32_bf16 a[208:211],a[180:183],v[140:143],a[208:211]
s_bitcmp1_b32 s54,s76
s_cselect_b32 s77,8192,0
s_lshl_b32 s76,s76,17
v_mfma_f32_16x16x32_bf16 a[208:211],a[184:187],v[152:155],a[208:211]
s_add_u32 s76,s76,s77
s_add_u32 s77,s76,4096
s_add_u32 s78,s76,16384
v_mfma_f32_16x16x32_bf16 a[208:211],a[188:191],v[156:159],a[208:211]
s_add_u32 s79,s76,20480
buffer_load_dwordx4 v9,s[72:75],s83 offen offset:2048 lds
v_mfma_f32_16x16x32_bf16 a[212:215],a[188:191],v[188:191],a[212:215]
s_max_i32 s76,s76,s18
s_max_i32 s77,s77,s19
s_max_i32 s78,s78,s100
v_mfma_f32_16x16x32_bf16 a[212:215],a[184:187],v[184:187],a[212:215]
s_max_i32 s79,s79,s101
ds_read_b64_tr_b16 a[162:163],v1 offset:5312
ds_read_b64_tr_b16 a[166:167],v1 offset:15552
v_mfma_f32_16x16x32_bf16 a[212:215],a[180:183],v[172:175],a[212:215]
ds_read_b64_tr_b16 a[170:171],v1 offset:25792
v_mfma_f32_16x16x32_bf16 a[212:215],a[176:179],v[168:171],a[212:215]
v_mfma_f32_16x16x32_bf16 a[216:219],a[176:179],v[200:203],a[216:219]
buffer_load_dwordx4 v10,s[72:75],s83 offen offset:3072 lds
v_mfma_f32_16x16x32_bf16 a[216:219],a[180:183],v[204:207],a[216:219]
v_mfma_f32_16x16x32_bf16 a[216:219],a[184:187],v[216:219],a[216:219]
s_addk_i32 m0,4096
v_mfma_f32_16x16x32_bf16 a[216:219],a[188:191],v[220:223],a[216:219]
v_mfma_f32_16x16x32_bf16 a[220:223],a[188:191],v[252:255],a[220:223]
ds_read_b64_tr_b16 a[188:189],v2 offset:30944
ds_read_b64_tr_b16 a[190:191],v2 offset:36064
v_mfma_f32_16x16x32_bf16 a[220:223],a[184:187],v[248:251],a[220:223]
buffer_load_dwordx4 v11,s[72:75],s83 offen lds
v_mfma_f32_16x16x32_bf16 a[220:223],a[180:183],v[236:239],a[220:223]
)A"
R"A(ds_read_b64_tr_b16 a[180:181],v2 offset:10464
ds_read_b64_tr_b16 a[182:183],v2 offset:15584
v_mfma_f32_16x16x32_bf16 a[220:223],a[176:179],v[232:235],a[220:223]
ds_read_b64_tr_b16 a[176:177],v2 offset:224
v_mfma_f32_16x16x32_bf16 a[224:227],a[96:99],v[136:139],a[224:227]
ds_read_b64_tr_b16 a[178:179],v2 offset:5344
v_mfma_f32_16x16x32_bf16 a[224:227],a[100:103],v[140:143],a[224:227]
ds_read_b64_tr_b16 a[184:185],v2 offset:20704
v_mfma_f32_16x16x32_bf16 a[224:227],a[104:107],v[152:155],a[224:227]
ds_read_b64_tr_b16 a[186:187],v2 offset:25824
v_mfma_f32_16x16x32_bf16 a[224:227],a[108:111],v[156:159],a[224:227]
v_mfma_f32_16x16x32_bf16 a[228:231],a[108:111],v[188:191],a[228:231]
v_mfma_f32_16x16x32_bf16 a[228:231],a[104:107],v[184:187],a[228:231]
v_mfma_f32_16x16x32_bf16 a[228:231],a[100:103],v[172:175],a[228:231]
v_mfma_f32_16x16x32_bf16 a[228:231],a[96:99],v[168:171],a[228:231]
buffer_load_dwordx4 v[64:67],v6,s[28:31],s76 offen sc1
v_mfma_f32_16x16x32_bf16 a[232:235],a[96:99],v[200:203],a[232:235]
v_mfma_f32_16x16x32_bf16 a[232:235],a[100:103],v[204:207],a[232:235]
v_mfma_f32_16x16x32_bf16 a[232:235],a[104:107],v[216:219],a[232:235]
v_mfma_f32_16x16x32_bf16 a[232:235],a[108:111],v[220:223],a[232:235]
v_mfma_f32_16x16x32_bf16 a[236:239],a[108:111],v[252:255],a[236:239]
buffer_load_dwordx4 v[68:71],v6,s[28:31],s76 offen offset:1024 sc1
v_mfma_f32_16x16x32_bf16 a[236:239],a[104:107],v[248:251],a[236:239]
ds_read_b64_tr_b16 a[104:105],v1 offset:20736
ds_read_b64_tr_b16 a[106:107],v1 offset:25856
v_mfma_f32_16x16x32_bf16 a[236:239],a[100:103],v[236:239],a[236:239]
ds_read_b64_tr_b16 a[100:101],v1 offset:10496
v_mfma_f32_16x16x32_bf16 a[236:239],a[96:99],v[232:235],a[236:239]
s_waitcnt lgkmcnt(3)
ds_read_b64_tr_b16 a[96:97],v1 offset:256
v_mfma_f32_16x16x32_bf16 a[240:243],a[112:115],v[136:139],a[240:243]
ds_read_b64_tr_b16 a[98:99],v1 offset:5376
v_mfma_f32_16x16x32_bf16 a[240:243],a[116:119],v[140:143],a[240:243]
buffer_load_dwordx4 v[72:75],v6,s[28:31],s76 offen offset:2048 sc1
v_mfma_f32_16x16x32_bf16 a[240:243],a[120:123],v[152:155],a[240:243]
ds_read_b64_tr_b16 a[102:103],v1 offset:15616
ds_read_b64_tr_b16 a[108:109],v1 offset:30976
v_mfma_f32_16x16x32_bf16 a[240:243],a[124:127],v[156:159],a[240:243]
ds_read_b64_tr_b16 a[110:111],v1 offset:36096
v_subrev_u32 v1,81920,v1
v_mfma_f32_16x16x32_bf16 a[244:247],a[124:127],v[188:191],a[244:247]
v_add_u32 v42,40960,v1
v_add_u32 v44,122880,v1
v_mfma_f32_16x16x32_bf16 a[244:247],a[120:123],v[184:187],a[244:247]
v_mfma_f32_16x16x32_bf16 a[244:247],a[116:119],v[172:175],a[244:247]
buffer_load_dwordx4 v[76:79],v6,s[28:31],s76 offen offset:3072 sc1
v_mfma_f32_16x16x32_bf16 a[244:247],a[112:115],v[168:171],a[244:247]
v_mfma_f32_16x16x32_bf16 a[248:251],a[112:115],v[200:203],a[248:251]
v_mfma_f32_16x16x32_bf16 a[248:251],a[116:119],v[204:207],a[248:251]
v_mfma_f32_16x16x32_bf16 a[248:251],a[120:123],v[216:219],a[248:251]
v_mfma_f32_16x16x32_bf16 a[248:251],a[124:127],v[220:223],a[248:251]
buffer_load_dwordx4 v[80:83],v6,s[28:31],s77 offen sc1
v_mfma_f32_16x16x32_bf16 a[252:255],a[124:127],v[252:255],a[252:255]
ds_read_b64_tr_b16 a[124:125],v2 offset:31008
ds_read_b64_tr_b16 a[126:127],v2 offset:36128
v_mfma_f32_16x16x32_bf16 a[252:255],a[120:123],v[248:251],a[252:255]
ds_read_b64_tr_b16 a[120:121],v2 offset:20768
v_mfma_f32_16x16x32_bf16 a[252:255],a[116:119],v[236:239],a[252:255]
ds_read_b64_tr_b16 a[116:117],v2 offset:10528
v_mfma_f32_16x16x32_bf16 a[252:255],a[112:115],v[232:235],a[252:255]
ds_read_b64_tr_b16 a[112:113],v2 offset:288
v_mfma_f32_16x16x32_bf16 a[0:3],a[128:131],v[136:139],a[0:3]
buffer_load_dwordx4 v[84:87],v6,s[28:31],s77 offen offset:1024 sc1
v_mfma_f32_16x16x32_bf16 a[0:3],a[132:135],v[140:143],a[0:3]
ds_read_b64_tr_b16 a[114:115],v2 offset:5408
ds_read_b64_tr_b16 a[118:119],v2 offset:15648
v_mfma_f32_16x16x32_bf16 a[0:3],a[136:139],v[152:155],a[0:3]
ds_read_b64_tr_b16 a[122:123],v2 offset:25888
v_subrev_u32 v2,81920,v2
v_mfma_f32_16x16x32_bf16 a[0:3],a[140:143],v[156:159],a[0:3]
v_add_u32 v43,40960,v2
v_add_u32 v45,122880,v2
v_mfma_f32_16x16x32_bf16 a[4:7],a[140:143],v[188:191],a[4:7]
v_mfma_f32_16x16x32_bf16 a[4:7],a[136:139],v[184:187],a[4:7]
buffer_load_dwordx4 v[88:91],v6,s[28:31],s77 offen offset:2048 sc1
v_mfma_f32_16x16x32_bf16 a[4:7],a[132:135],v[172:175],a[4:7]
v_mfma_f32_16x16x32_bf16 a[4:7],a[128:131],v[168:171],a[4:7]
v_mfma_f32_16x16x32_bf16 a[8:11],a[128:131],v[200:203],a[8:11]
v_mfma_f32_16x16x32_bf16 a[8:11],a[132:135],v[204:207],a[8:11]
v_mfma_f32_16x16x32_bf16 a[8:11],a[136:139],v[216:219],a[8:11]
buffer_load_dwordx4 v[92:95],v6,s[28:31],s77 offen offset:3072 sc1
v_mfma_f32_16x16x32_bf16 a[8:11],a[140:143],v[220:223],a[8:11]
v_mfma_f32_16x16x32_bf16 a[12:15],a[140:143],v[252:255],a[12:15]
ds_read_b64_tr_b16 a[140:141],v1 offset:30720
ds_read_b64_tr_b16 a[142:143],v1 offset:35840
v_mfma_f32_16x16x32_bf16 a[12:15],a[136:139],v[248:251],a[12:15]
ds_read_b64_tr_b16 a[136:137],v1 offset:20480
v_mfma_f32_16x16x32_bf16 a[12:15],a[132:135],v[236:239],a[12:15]
ds_read_b64_tr_b16 a[132:133],v1 offset:10240
v_mfma_f32_16x16x32_bf16 a[12:15],a[128:131],v[232:235],a[12:15]
buffer_load_dwordx4 v[96:99],v6,s[28:31],s78 offen sc1
v_mfma_f32_16x16x32_bf16 a[16:19],a[144:147],v[136:139],a[16:19]
ds_read_b64_tr_b16 a[128:129],v1
ds_read_b64_tr_b16 a[130:131],v1 offset:5120
v_mfma_f32_16x16x32_bf16 a[16:19],a[148:151],v[140:143],a[16:19]
ds_read_b64_tr_b16 a[134:135],v1 offset:15360
v_mfma_f32_16x16x32_bf16 a[16:19],a[152:155],v[152:155],a[16:19]
ds_read_b64_tr_b16 a[138:139],v1 offset:25600
v_mfma_f32_16x16x32_bf16 a[16:19],a[156:159],v[156:159],a[16:19]
v_mfma_f32_16x16x32_bf16 a[20:23],a[156:159],v[188:191],a[20:23]
buffer_load_dwordx4 v[100:103],v6,s[28:31],s78 offen offset:1024 sc1
v_mfma_f32_16x16x32_bf16 a[20:23],a[152:155],v[184:187],a[20:23]
v_mfma_f32_16x16x32_bf16 a[20:23],a[148:151],v[172:175],a[20:23]
v_mfma_f32_16x16x32_bf16 a[20:23],a[144:147],v[168:171],a[20:23]
v_mfma_f32_16x16x32_bf16 a[24:27],a[144:147],v[200:203],a[24:27]
v_mfma_f32_16x16x32_bf16 a[24:27],a[148:151],v[204:207],a[24:27]
buffer_load_dwordx4 v[104:107],v6,s[28:31],s78 offen offset:2048 sc1
v_mfma_f32_16x16x32_bf16 a[24:27],a[152:155],v[216:219],a[24:27]
v_mfma_f32_16x16x32_bf16 a[24:27],a[156:159],v[220:223],a[24:27]
v_mfma_f32_16x16x32_bf16 a[28:31],a[156:159],v[252:255],a[28:31]
ds_read_b64_tr_b16 a[156:157],v2 offset:30752
ds_read_b64_tr_b16 a[158:159],v2 offset:35872
v_mfma_f32_16x16x32_bf16 a[28:31],a[152:155],v[248:251],a[28:31]
ds_read_b64_tr_b16 a[152:153],v2 offset:20512
v_mfma_f32_16x16x32_bf16 a[28:31],a[148:151],v[236:239],a[28:31]
buffer_load_dwordx4 v[108:111],v6,s[28:31],s78 offen offset:3072 sc1
v_mfma_f32_16x16x32_bf16 a[28:31],a[144:147],v[232:235],a[28:31]
s_cmp_ge_u32 2,s71
s_cselect_b32 s80,7,5
s_mul_i32 s80,s80,163840
v_mfma_f32_16x16x32_bf16 a[32:35],a[160:163],v[136:139],a[32:35]
s_add_u32 s80,s80,s81
s_add_u32 s83,s80,20480
ds_read_b64_tr_b16 a[144:145],v2 offset:32
v_mfma_f32_16x16x32_bf16 a[32:35],a[164:167],v[140:143],a[32:35]
ds_read_b64_tr_b16 a[146:147],v2 offset:5152
ds_read_b64_tr_b16 a[148:149],v2 offset:10272
v_mfma_f32_16x16x32_bf16 a[32:35],a[168:171],v[152:155],a[32:35]
ds_read_b64_tr_b16 a[150:151],v2 offset:15392
v_mfma_f32_16x16x32_bf16 a[32:35],a[172:175],v[156:159],a[32:35]
buffer_load_dwordx4 v[112:115],v6,s[28:31],s79 offen sc1
v_mfma_f32_16x16x32_bf16 a[36:39],a[172:175],v[188:191],a[36:39]
ds_read_b64_tr_b16 a[154:155],v2 offset:25632
v_mfma_f32_16x16x32_bf16 a[36:39],a[168:171],v[184:187],a[36:39]
v_mfma_f32_16x16x32_bf16 a[36:39],a[164:167],v[172:175],a[36:39]
v_mfma_f32_16x16x32_bf16 a[36:39],a[160:163],v[168:171],a[36:39]
v_mfma_f32_16x16x32_bf16 a[40:43],a[160:163],v[200:203],a[40:43]
buffer_load_dwordx4 v[116:119],v6,s[28:31],s79 offen offset:1024 sc1
v_mfma_f32_16x16x32_bf16 a[40:43],a[164:167],v[204:207],a[40:43]
v_mfma_f32_16x16x32_bf16 a[40:43],a[168:171],v[216:219],a[40:43]
v_mfma_f32_16x16x32_bf16 a[40:43],a[172:175],v[220:223],a[40:43]
v_mfma_f32_16x16x32_bf16 a[44:47],a[172:175],v[252:255],a[44:47]
ds_read_b64_tr_b16 a[172:173],v1 offset:30784
ds_read_b64_tr_b16 a[174:175],v1 offset:35904
v_mfma_f32_16x16x32_bf16 a[44:47],a[168:171],v[248:251],a[44:47]
buffer_load_dwordx4 v[120:123],v6,s[28:31],s79 offen offset:2048 sc1
v_mfma_f32_16x16x32_bf16 a[44:47],a[164:167],v[236:239],a[44:47]
ds_read_b64_tr_b16 a[164:165],v1 offset:10304
ds_read_b64_tr_b16 a[166:167],v1 offset:15424
v_mfma_f32_16x16x32_bf16 a[44:47],a[160:163],v[232:235],a[44:47]
ds_read_b64_tr_b16 a[160:161],v1 offset:64
v_mfma_f32_16x16x32_bf16 a[48:51],a[176:179],v[136:139],a[48:51]
ds_read_b64_tr_b16 a[162:163],v1 offset:5184
v_mfma_f32_16x16x32_bf16 a[48:51],a[180:183],v[140:143],a[48:51]
ds_read_b64_tr_b16 a[168:169],v1 offset:20544
v_mfma_f32_16x16x32_bf16 a[48:51],a[184:187],v[152:155],a[48:51]
buffer_load_dwordx4 v[124:127],v6,s[28:31],s79 offen offset:3072 sc1
v_mfma_f32_16x16x32_bf16 a[48:51],a[188:191],v[156:159],a[48:51]
ds_read_b64_tr_b16 a[170:171],v1 offset:25664
v_mfma_f32_16x16x32_bf16 a[52:55],a[188:191],v[188:191],a[52:55]
s_add_u32 m0,s82,122880
v_mfma_f32_16x16x32_bf16 a[52:55],a[184:187],v[184:187],a[52:55]
v_mfma_f32_16x16x32_bf16 a[52:55],a[180:183],v[172:175],a[52:55]
v_mfma_f32_16x16x32_bf16 a[52:55],a[176:179],v[168:171],a[52:55]
buffer_load_dwordx4 v7,s[72:75],s80 offen lds
v_mfma_f32_16x16x32_bf16 a[56:59],a[176:179],v[200:203],a[56:59]
v_mfma_f32_16x16x32_bf16 a[56:59],a[180:183],v[204:207],a[56:59]
v_mfma_f32_16x16x32_bf16 a[56:59],a[184:187],v[216:219],a[56:59]
v_mfma_f32_16x16x32_bf16 a[56:59],a[188:191],v[220:223],a[56:59]
v_mfma_f32_16x16x32_bf16 a[60:63],a[188:191],v[252:255],a[60:63]
buffer_load_dwordx4 v8,s[72:75],s80 offen offset:1024 lds
v_mfma_f32_16x16x32_bf16 a[60:63],a[184:187],v[248:251],a[60:63]
ds_read_b64_tr_b16 a[184:185],v2 offset:20576
ds_read_b64_tr_b16 a[186:187],v2 offset:25696
v_mfma_f32_16x16x32_bf16 a[60:63],a[180:183],v[236:239],a[60:63]
ds_read_b64_tr_b16 a[180:181],v2 offset:10336
v_mfma_f32_16x16x32_bf16 a[60:63],a[176:179],v[232:235],a[60:63]
s_waitcnt lgkmcnt(3)
ds_read_b64_tr_b16 a[176:177],v2 offset:96
v_mfma_f32_16x16x32_bf16 a[64:67],a[96:99],v[136:139],a[64:67]
ds_read_b64_tr_b16 a[178:179],v2 offset:5216
v_mfma_f32_16x16x32_bf16 a[64:67],a[100:103],v[140:143],a[64:67]
buffer_load_dwordx4 v9,s[72:75],s80 offen offset:2048 lds
v_mfma_f32_16x16x32_bf16 a[64:67],a[104:107],v[152:155],a[64:67]
ds_read_b64_tr_b16 a[182:183],v2 offset:15456
ds_read_b64_tr_b16 a[188:189],v2 offset:30816
v_mfma_f32_16x16x32_bf16 a[64:67],a[108:111],v[156:159],a[64:67]
ds_read_b64_tr_b16 a[190:191],v2 offset:35936
v_mfma_f32_16x16x32_bf16 a[68:71],a[108:111],v[188:191],a[68:71]
v_mfma_f32_16x16x32_bf16 a[68:71],a[104:107],v[184:187],a[68:71]
v_mfma_f32_16x16x32_bf16 a[68:71],a[100:103],v[172:175],a[68:71]
buffer_load_dwordx4 v10,s[72:75],s80 offen offset:3072 lds
v_mfma_f32_16x16x32_bf16 a[68:71],a[96:99],v[168:171],a[68:71]
v_mfma_f32_16x16x32_bf16 a[72:75],a[96:99],v[200:203],a[72:75]
s_addk_i32 m0,4096
v_mfma_f32_16x16x32_bf16 a[72:75],a[100:103],v[204:207],a[72:75]
v_mfma_f32_16x16x32_bf16 a[72:75],a[104:107],v[216:219],a[72:75]
s_cmp_ge_u32 2,s71
s_cselect_b32 s76,7,5
v_mfma_f32_16x16x32_bf16 a[72:75],a[108:111],v[220:223],a[72:75]
buffer_load_dwordx4 v11,s[72:75],s80 offen lds
v_mfma_f32_16x16x32_bf16 a[76:79],a[108:111],v[252:255],a[76:79]
s_bitcmp1_b32 s54,s76
s_cselect_b32 s77,8192,0
s_lshl_b32 s76,s76,17
v_mfma_f32_16x16x32_bf16 a[76:79],a[104:107],v[248:251],a[76:79]
s_addk_i32 m0,1024
s_add_u32 s76,s76,s77
s_add_u32 s77,s76,4096
v_mfma_f32_16x16x32_bf16 a[76:79],a[100:103],v[236:239],a[76:79]
s_add_u32 s78,s76,16384
s_add_u32 s79,s76,20480
s_max_i32 s76,s76,s18
v_mfma_f32_16x16x32_bf16 a[76:79],a[96:99],v[232:235],a[76:79]
s_max_i32 s77,s77,s19
ds_read_b64_tr_b16 a[96:97],v1 offset:128
ds_read_b64_tr_b16 a[98:99],v1 offset:5248
v_mfma_f32_16x16x32_bf16 a[80:83],a[112:115],v[136:139],a[80:83]
s_max_i32 s78,s78,s100
buffer_load_dwordx4 v7,s[72:75],s83 offen lds
v_mfma_f32_16x16x32_bf16 a[80:83],a[116:119],v[140:143],a[80:83]
)A"
R"A(s_max_i32 s79,s79,s101
ds_read_b64_tr_b16 a[100:101],v1 offset:10368
ds_read_b64_tr_b16 a[102:103],v1 offset:15488
v_mfma_f32_16x16x32_bf16 a[80:83],a[120:123],v[152:155],a[80:83]
ds_read_b64_tr_b16 a[104:105],v1 offset:20608
v_mfma_f32_16x16x32_bf16 a[80:83],a[124:127],v[156:159],a[80:83]
ds_read_b64_tr_b16 a[106:107],v1 offset:25728
v_mfma_f32_16x16x32_bf16 a[84:87],a[124:127],v[188:191],a[84:87]
ds_read_b64_tr_b16 a[108:109],v1 offset:30848
v_mfma_f32_16x16x32_bf16 a[84:87],a[120:123],v[184:187],a[84:87]
buffer_load_dwordx4 v8,s[72:75],s83 offen offset:1024 lds
v_mfma_f32_16x16x32_bf16 a[84:87],a[116:119],v[172:175],a[84:87]
ds_read_b64_tr_b16 a[110:111],v1 offset:35968
v_mfma_f32_16x16x32_bf16 a[84:87],a[112:115],v[168:171],a[84:87]
v_mfma_f32_16x16x32_bf16 a[88:91],a[112:115],v[200:203],a[88:91]
v_mfma_f32_16x16x32_bf16 a[88:91],a[116:119],v[204:207],a[88:91]
v_mfma_f32_16x16x32_bf16 a[88:91],a[120:123],v[216:219],a[88:91]
buffer_load_dwordx4 v9,s[72:75],s83 offen offset:2048 lds
v_mfma_f32_16x16x32_bf16 a[88:91],a[124:127],v[220:223],a[88:91]
v_mfma_f32_16x16x32_bf16 a[92:95],a[124:127],v[252:255],a[92:95]
ds_read_b64_tr_b16 a[124:125],v2 offset:30880
ds_read_b64_tr_b16 a[126:127],v2 offset:36000
v_mfma_f32_16x16x32_bf16 a[92:95],a[120:123],v[248:251],a[92:95]
ds_read_b64_tr_b16 a[120:121],v2 offset:20640
v_mfma_f32_16x16x32_bf16 a[92:95],a[116:119],v[236:239],a[92:95]
ds_read_b64_tr_b16 a[116:117],v2 offset:10400
v_mfma_f32_16x16x32_bf16 a[92:95],a[112:115],v[232:235],a[92:95]
buffer_load_dwordx4 v10,s[72:75],s83 offen offset:3072 lds
v_mfma_f32_16x16x32_bf16 a[192:195],a[128:131],v[128:131],a[192:195]
ds_read_b64_tr_b16 a[112:113],v2 offset:160
ds_read_b64_tr_b16 a[114:115],v2 offset:5280
v_mfma_f32_16x16x32_bf16 a[192:195],a[132:135],v[160:163],a[192:195]
s_addk_i32 m0,4096
ds_read_b64_tr_b16 a[118:119],v2 offset:15520
v_mfma_f32_16x16x32_bf16 a[192:195],a[136:139],v[192:195],a[192:195]
ds_read_b64_tr_b16 a[122:123],v2 offset:25760
v_mfma_f32_16x16x32_bf16 a[192:195],a[140:143],v[224:227],a[192:195]
buffer_load_dwordx4 v[136:139],v6,s[28:31],s76 offen sc1
v_mfma_f32_16x16x32_bf16 a[196:199],a[140:143],v[228:231],a[196:199]
v_mfma_f32_16x16x32_bf16 a[196:199],a[136:139],v[196:199],a[196:199]
v_mfma_f32_16x16x32_bf16 a[196:199],a[132:135],v[164:167],a[196:199]
v_mfma_f32_16x16x32_bf16 a[196:199],a[128:131],v[132:135],a[196:199]
s_waitcnt vmcnt(14)
buffer_load_dwordx4 v11,s[72:75],s83 offen lds
v_mfma_f32_16x16x32_bf16 a[200:203],a[128:131],v[144:147],a[200:203]
ds_read_b64_tr_b16 a[128:129],v1 offset:192
ds_read_b64_tr_b16 a[130:131],v1 offset:5312
v_mfma_f32_16x16x32_bf16 a[200:203],a[132:135],v[176:179],a[200:203]
ds_read_b64_tr_b16 a[132:133],v1 offset:10432
v_mfma_f32_16x16x32_bf16 a[200:203],a[136:139],v[208:211],a[200:203]
ds_read_b64_tr_b16 a[134:135],v1 offset:15552
v_mfma_f32_16x16x32_bf16 a[200:203],a[140:143],v[240:243],a[200:203]
buffer_load_dwordx4 v[140:143],v6,s[28:31],s76 offen offset:1024 sc1
v_mfma_f32_16x16x32_bf16 a[208:211],a[144:147],v[128:131],a[208:211]
ds_read_b64_tr_b16 a[136:137],v1 offset:20672
ds_read_b64_tr_b16 a[138:139],v1 offset:25792
v_mfma_f32_16x16x32_bf16 a[208:211],a[148:151],v[160:163],a[208:211]
ds_read_b64_tr_b16 a[140:141],v1 offset:30912
v_mfma_f32_16x16x32_bf16 a[208:211],a[152:155],v[192:195],a[208:211]
ds_read_b64_tr_b16 a[142:143],v1 offset:36032
v_mfma_f32_16x16x32_bf16 a[208:211],a[156:159],v[224:227],a[208:211]
buffer_load_dwordx4 v[152:155],v6,s[28:31],s76 offen offset:2048 sc1
v_mfma_f32_16x16x32_bf16 a[212:215],a[156:159],v[228:231],a[212:215]
v_mfma_f32_16x16x32_bf16 a[212:215],a[152:155],v[196:199],a[212:215]
v_mfma_f32_16x16x32_bf16 a[212:215],a[148:151],v[164:167],a[212:215]
v_mfma_f32_16x16x32_bf16 a[212:215],a[144:147],v[132:135],a[212:215]
buffer_load_dwordx4 v[156:159],v6,s[28:31],s76 offen offset:3072 sc1
v_mfma_f32_16x16x32_bf16 a[216:219],a[144:147],v[144:147],a[216:219]
s_add_u32 s76,s70,3
ds_read_b64_tr_b16 a[144:145],v2 offset:224
ds_read_b64_tr_b16 a[146:147],v2 offset:5344
v_mfma_f32_16x16x32_bf16 a[216:219],a[148:151],v[176:179],a[216:219]
s_and_b32 s76,s76,3
s_lshl_b32 s76,s76,21
ds_read_b64_tr_b16 a[148:149],v2 offset:10464
v_mfma_f32_16x16x32_bf16 a[216:219],a[152:155],v[208:211],a[216:219]
s_add_u32 s76,s76,0
s_max_i32 s76,s76,s101
ds_read_b64_tr_b16 a[150:151],v2 offset:15584
v_mfma_f32_16x16x32_bf16 a[216:219],a[156:159],v[240:243],a[216:219]
buffer_load_dwordx4 v[168:171],v6,s[28:31],s77 offen sc1
v_mfma_f32_16x16x32_bf16 a[224:227],a[160:163],v[128:131],a[224:227]
ds_read_b64_tr_b16 a[152:153],v2 offset:20704
ds_read_b64_tr_b16 a[154:155],v2 offset:25824
v_mfma_f32_16x16x32_bf16 a[224:227],a[164:167],v[160:163],a[224:227]
ds_read_b64_tr_b16 a[156:157],v2 offset:30944
v_mfma_f32_16x16x32_bf16 a[224:227],a[168:171],v[192:195],a[224:227]
ds_read_b64_tr_b16 a[158:159],v2 offset:36064
v_mfma_f32_16x16x32_bf16 a[224:227],a[172:175],v[224:227],a[224:227]
buffer_load_dwordx4 v[172:175],v6,s[28:31],s77 offen offset:1024 sc1
v_mfma_f32_16x16x32_bf16 a[228:231],a[172:175],v[228:231],a[228:231]
v_mfma_f32_16x16x32_bf16 a[228:231],a[168:171],v[196:199],a[228:231]
v_mfma_f32_16x16x32_bf16 a[228:231],a[164:167],v[164:167],a[228:231]
v_mfma_f32_16x16x32_bf16 a[228:231],a[160:163],v[132:135],a[228:231]
v_mfma_f32_16x16x32_bf16 a[232:235],a[160:163],v[144:147],a[232:235]
buffer_load_dwordx4 v[184:187],v6,s[28:31],s77 offen offset:2048 sc1
v_mfma_f32_16x16x32_bf16 a[232:235],a[164:167],v[176:179],a[232:235]
ds_read_b64_tr_b16 a[160:161],v1 offset:256
ds_read_b64_tr_b16 a[162:163],v1 offset:5376
v_mfma_f32_16x16x32_bf16 a[232:235],a[168:171],v[208:211],a[232:235]
ds_read_b64_tr_b16 a[164:165],v1 offset:10496
v_mfma_f32_16x16x32_bf16 a[232:235],a[172:175],v[240:243],a[232:235]
s_waitcnt lgkmcnt(3)
ds_read_b64_tr_b16 a[166:167],v1 offset:15616
v_mfma_f32_16x16x32_bf16 a[240:243],a[176:179],v[128:131],a[240:243]
ds_read_b64_tr_b16 a[168:169],v1 offset:20736
v_mfma_f32_16x16x32_bf16 a[240:243],a[180:183],v[160:163],a[240:243]
buffer_load_dwordx4 v[188:191],v6,s[28:31],s77 offen offset:3072 sc1
v_mfma_f32_16x16x32_bf16 a[240:243],a[184:187],v[192:195],a[240:243]
s_add_u32 s77,s70,2
ds_read_b64_tr_b16 a[170:171],v1 offset:25856
ds_read_b64_tr_b16 a[172:173],v1 offset:30976
v_mfma_f32_16x16x32_bf16 a[240:243],a[188:191],v[224:227],a[240:243]
s_and_b32 s77,s77,3
s_lshl_b32 s77,s77,21
ds_read_b64_tr_b16 a[174:175],v1 offset:36096
v_mfma_f32_16x16x32_bf16 a[244:247],a[188:191],v[228:231],a[244:247]
s_add_u32 s77,s77,131072
s_max_i32 s77,s77,s101
v_mfma_f32_16x16x32_bf16 a[244:247],a[184:187],v[196:199],a[244:247]
v_mfma_f32_16x16x32_bf16 a[244:247],a[180:183],v[164:167],a[244:247]
buffer_load_dwordx4 v[200:203],v6,s[28:31],s78 offen sc1
v_mfma_f32_16x16x32_bf16 a[244:247],a[176:179],v[132:135],a[244:247]
v_mfma_f32_16x16x32_bf16 a[248:251],a[176:179],v[144:147],a[248:251]
ds_read_b64_tr_b16 a[176:177],v2 offset:288
ds_read_b64_tr_b16 a[178:179],v2 offset:5408
v_mfma_f32_16x16x32_bf16 a[248:251],a[180:183],v[176:179],a[248:251]
ds_read_b64_tr_b16 a[180:181],v2 offset:10528
v_mfma_f32_16x16x32_bf16 a[248:251],a[184:187],v[208:211],a[248:251]
ds_read_b64_tr_b16 a[182:183],v2 offset:15648
v_mfma_f32_16x16x32_bf16 a[248:251],a[188:191],v[240:243],a[248:251]
buffer_load_dwordx4 v[204:207],v6,s[28:31],s78 offen offset:1024 sc1
v_mfma_f32_16x16x32_bf16 a[0:3],a[96:99],v[128:131],a[0:3]
ds_read_b64_tr_b16 a[184:185],v2 offset:20768
ds_read_b64_tr_b16 a[186:187],v2 offset:25888
v_mfma_f32_16x16x32_bf16 a[0:3],a[100:103],v[160:163],a[0:3]
ds_read_b64_tr_b16 a[188:189],v2 offset:31008
v_mfma_f32_16x16x32_bf16 a[0:3],a[104:107],v[192:195],a[0:3]
ds_read_b64_tr_b16 a[190:191],v2 offset:36128
v_mfma_f32_16x16x32_bf16 a[0:3],a[108:111],v[224:227],a[0:3]
v_mfma_f32_16x16x32_bf16 a[4:7],a[108:111],v[228:231],a[4:7]
buffer_load_dwordx4 v[216:219],v6,s[28:31],s78 offen offset:2048 sc1
v_mfma_f32_16x16x32_bf16 a[4:7],a[104:107],v[196:199],a[4:7]
v_mfma_f32_16x16x32_bf16 a[4:7],a[100:103],v[164:167],a[4:7]
v_mfma_f32_16x16x32_bf16 a[4:7],a[96:99],v[132:135],a[4:7]
v_mfma_f32_16x16x32_bf16 a[8:11],a[96:99],v[144:147],a[8:11]
v_mfma_f32_16x16x32_bf16 a[8:11],a[100:103],v[176:179],a[8:11]
buffer_load_dwordx4 v[220:223],v6,s[28:31],s78 offen offset:3072 sc1
v_mfma_f32_16x16x32_bf16 a[8:11],a[104:107],v[208:211],a[8:11]
s_add_u32 s78,s70,1
s_and_b32 s78,s78,3
s_lshl_b32 s78,s78,21
v_mfma_f32_16x16x32_bf16 a[8:11],a[108:111],v[240:243],a[8:11]
s_add_u32 s78,s78,262144
s_max_i32 s78,s78,s101
v_mfma_f32_16x16x32_bf16 a[16:19],a[112:115],v[128:131],a[16:19]
v_mfma_f32_16x16x32_bf16 a[16:19],a[116:119],v[160:163],a[16:19]
v_mfma_f32_16x16x32_bf16 a[16:19],a[120:123],v[192:195],a[16:19]
buffer_load_dwordx4 v[232:235],v6,s[28:31],s79 offen sc1
v_mfma_f32_16x16x32_bf16 a[16:19],a[124:127],v[224:227],a[16:19]
v_mfma_f32_16x16x32_bf16 a[20:23],a[124:127],v[228:231],a[20:23]
v_mfma_f32_16x16x32_bf16 a[20:23],a[120:123],v[196:199],a[20:23]
v_mfma_f32_16x16x32_bf16 a[20:23],a[116:119],v[164:167],a[20:23]
v_mfma_f32_16x16x32_bf16 a[20:23],a[112:115],v[132:135],a[20:23]
buffer_load_dwordx4 v[236:239],v6,s[28:31],s79 offen offset:1024 sc1
v_mfma_f32_16x16x32_bf16 a[24:27],a[112:115],v[144:147],a[24:27]
v_mfma_f32_16x16x32_bf16 a[24:27],a[116:119],v[176:179],a[24:27]
v_mfma_f32_16x16x32_bf16 a[24:27],a[120:123],v[208:211],a[24:27]
v_mfma_f32_16x16x32_bf16 a[24:27],a[124:127],v[240:243],a[24:27]
v_mfma_f32_16x16x32_bf16 a[32:35],a[128:131],v[128:131],a[32:35]
buffer_load_dwordx4 v[248:251],v6,s[28:31],s79 offen offset:2048 sc1
v_mfma_f32_16x16x32_bf16 a[32:35],a[132:135],v[160:163],a[32:35]
v_mfma_f32_16x16x32_bf16 a[32:35],a[136:139],v[192:195],a[32:35]
v_mfma_f32_16x16x32_bf16 a[32:35],a[140:143],v[224:227],a[32:35]
v_mfma_f32_16x16x32_bf16 a[36:39],a[140:143],v[228:231],a[36:39]
v_mfma_f32_16x16x32_bf16 a[36:39],a[136:139],v[196:199],a[36:39]
buffer_load_dwordx4 v[252:255],v6,s[28:31],s79 offen offset:3072 sc1
v_mfma_f32_16x16x32_bf16 a[36:39],a[132:135],v[164:167],a[36:39]
v_mfma_f32_16x16x32_bf16 a[36:39],a[128:131],v[132:135],a[36:39]
v_mfma_f32_16x16x32_bf16 a[40:43],a[128:131],v[144:147],a[40:43]
v_mfma_f32_16x16x32_bf16 a[40:43],a[132:135],v[176:179],a[40:43]
v_mfma_f32_16x16x32_bf16 a[40:43],a[136:139],v[208:211],a[40:43]
s_waitcnt vmcnt(15)
v_mfma_f32_16x16x32_bf16 a[40:43],a[140:143],v[240:243],a[40:43]
s_barrier
ds_read_b64_tr_b16 a[96:97],v42
ds_read_b64_tr_b16 a[98:99],v42 offset:5120
v_mfma_f32_16x16x32_bf16 a[48:51],a[144:147],v[128:131],a[48:51]
ds_read_b64_tr_b16 a[100:101],v42 offset:10240
v_mfma_f32_16x16x32_bf16 a[48:51],a[148:151],v[160:163],a[48:51]
ds_read_b64_tr_b16 a[102:103],v42 offset:15360
v_mfma_f32_16x16x32_bf16 a[48:51],a[152:155],v[192:195],a[48:51]
ds_read_b64_tr_b16 a[104:105],v42 offset:20480
v_mfma_f32_16x16x32_bf16 a[48:51],a[156:159],v[224:227],a[48:51]
ds_read_b64_tr_b16 a[106:107],v42 offset:25600
ds_read_b64_tr_b16 a[108:109],v42 offset:30720
v_mfma_f32_16x16x32_bf16 a[52:55],a[156:159],v[228:231],a[52:55]
ds_read_b64_tr_b16 a[110:111],v42 offset:35840
v_mfma_f32_16x16x32_bf16 a[52:55],a[152:155],v[196:199],a[52:55]
ds_read_b64_tr_b16 a[112:113],v44
v_mfma_f32_16x16x32_bf16 a[52:55],a[148:151],v[164:167],a[52:55]
ds_read_b64_tr_b16 a[114:115],v44 offset:5120
v_mfma_f32_16x16x32_bf16 a[52:55],a[144:147],v[132:135],a[52:55]
ds_read_b64_tr_b16 a[116:117],v44 offset:10240
ds_read_b64_tr_b16 a[118:119],v44 offset:15360
v_mfma_f32_16x16x32_bf16 a[56:59],a[144:147],v[144:147],a[56:59]
ds_read_b64_tr_b16 a[120:121],v44 offset:20480
v_mfma_f32_16x16x32_bf16 a[56:59],a[148:151],v[176:179],a[56:59]
ds_read_b64_tr_b16 a[122:123],v44 offset:25600
v_mfma_f32_16x16x32_bf16 a[56:59],a[152:155],v[208:211],a[56:59]
ds_read_b64_tr_b16 a[124:125],v44 offset:30720
v_mfma_f32_16x16x32_bf16 a[56:59],a[156:159],v[240:243],a[56:59]
s_waitcnt lgkmcnt(8)
ds_read_b64_tr_b16 a[126:127],v44 offset:35840
ds_read_b64_tr_b16 a[128:129],v43 offset:32
v_mfma_f32_16x16x32_bf16 a[64:67],a[160:163],v[128:131],a[64:67]
ds_read_b64_tr_b16 a[130:131],v43 offset:5152
)A"
R"A(v_mfma_f32_16x16x32_bf16 a[64:67],a[164:167],v[160:163],a[64:67]
ds_read_b64_tr_b16 a[132:133],v43 offset:10272
v_mfma_f32_16x16x32_bf16 a[64:67],a[168:171],v[192:195],a[64:67]
ds_read_b64_tr_b16 a[134:135],v43 offset:15392
v_mfma_f32_16x16x32_bf16 a[64:67],a[172:175],v[224:227],a[64:67]
ds_read_b64_tr_b16 a[136:137],v43 offset:20512
ds_read_b64_tr_b16 a[138:139],v43 offset:25632
v_mfma_f32_16x16x32_bf16 a[68:71],a[172:175],v[228:231],a[68:71]
ds_read_b64_tr_b16 a[140:141],v43 offset:30752
v_mfma_f32_16x16x32_bf16 a[68:71],a[168:171],v[196:199],a[68:71]
ds_read_b64_tr_b16 a[142:143],v43 offset:35872
v_mfma_f32_16x16x32_bf16 a[68:71],a[164:167],v[164:167],a[68:71]
ds_read_b64_tr_b16 a[144:145],v45 offset:32
v_mfma_f32_16x16x32_bf16 a[68:71],a[160:163],v[132:135],a[68:71]
ds_read_b64_tr_b16 a[146:147],v45 offset:5152
ds_read_b64_tr_b16 a[148:149],v45 offset:10272
v_mfma_f32_16x16x32_bf16 a[72:75],a[160:163],v[144:147],a[72:75]
ds_read_b64_tr_b16 a[150:151],v45 offset:15392
v_mfma_f32_16x16x32_bf16 a[72:75],a[164:167],v[176:179],a[72:75]
ds_read_b64_tr_b16 a[152:153],v45 offset:20512
v_mfma_f32_16x16x32_bf16 a[72:75],a[168:171],v[208:211],a[72:75]
ds_read_b64_tr_b16 a[154:155],v45 offset:25632
v_mfma_f32_16x16x32_bf16 a[72:75],a[172:175],v[240:243],a[72:75]
ds_read_b64_tr_b16 a[156:157],v45 offset:30752
ds_read_b64_tr_b16 a[158:159],v45 offset:35872
v_mfma_f32_16x16x32_bf16 a[80:83],a[176:179],v[128:131],a[80:83]
ds_read_b64_tr_b16 a[160:161],v42 offset:64
v_mfma_f32_16x16x32_bf16 a[80:83],a[180:183],v[160:163],a[80:83]
ds_read_b64_tr_b16 a[162:163],v42 offset:5184
v_mfma_f32_16x16x32_bf16 a[80:83],a[184:187],v[192:195],a[80:83]
ds_read_b64_tr_b16 a[164:165],v42 offset:10304
v_mfma_f32_16x16x32_bf16 a[80:83],a[188:191],v[224:227],a[80:83]
ds_read_b64_tr_b16 a[166:167],v42 offset:15424
ds_read_b64_tr_b16 a[168:169],v42 offset:20544
v_mfma_f32_16x16x32_bf16 a[84:87],a[188:191],v[228:231],a[84:87]
ds_read_b64_tr_b16 a[170:171],v42 offset:25664
v_mfma_f32_16x16x32_bf16 a[84:87],a[184:187],v[196:199],a[84:87]
ds_read_b64_tr_b16 a[172:173],v42 offset:30784
v_mfma_f32_16x16x32_bf16 a[84:87],a[180:183],v[164:167],a[84:87]
ds_read_b64_tr_b16 a[174:175],v42 offset:35904
v_mfma_f32_16x16x32_bf16 a[84:87],a[176:179],v[132:135],a[84:87]
v_mfma_f32_16x16x32_bf16 a[88:91],a[176:179],v[144:147],a[88:91]
ds_read_b64_tr_b16 a[176:177],v44 offset:64
ds_read_b64_tr_b16 a[178:179],v44 offset:5184
v_mfma_f32_16x16x32_bf16 a[88:91],a[180:183],v[176:179],a[88:91]
ds_read_b64_tr_b16 a[180:181],v44 offset:10304
v_mfma_f32_16x16x32_bf16 a[88:91],a[184:187],v[208:211],a[88:91]
ds_read_b64_tr_b16 a[182:183],v44 offset:15424
v_mfma_f32_16x16x32_bf16 a[88:91],a[188:191],v[240:243],a[88:91]
ds_read_b64_tr_b16 a[184:185],v44 offset:20544
v_mfma_f32_16x16x32_bf16 a[192:195],a[96:99],v[64:67],a[192:195]
ds_read_b64_tr_b16 a[186:187],v44 offset:25664
ds_read_b64_tr_b16 a[188:189],v44 offset:30784
v_mfma_f32_16x16x32_bf16 a[192:195],a[100:103],v[68:71],a[192:195]
ds_read_b64_tr_b16 a[190:191],v44 offset:35904
v_mfma_f32_16x16x32_bf16 a[192:195],a[104:107],v[72:75],a[192:195]
s_waitcnt lgkmcnt(8)
v_mfma_f32_16x16x32_bf16 a[192:195],a[108:111],v[76:79],a[192:195]
v_mfma_f32_16x16x32_bf16 a[196:199],a[108:111],v[92:95],a[196:199]
v_mfma_f32_16x16x32_bf16 a[196:199],a[104:107],v[88:91],a[196:199]
v_mfma_f32_16x16x32_bf16 a[196:199],a[100:103],v[84:87],a[196:199]
v_mfma_f32_16x16x32_bf16 a[196:199],a[96:99],v[80:83],a[196:199]
v_mfma_f32_16x16x32_bf16 a[200:203],a[96:99],v[96:99],a[200:203]
ds_read_b64_tr_b16 a[96:97],v43 offset:96
ds_read_b64_tr_b16 a[98:99],v43 offset:5216
v_mfma_f32_16x16x32_bf16 a[200:203],a[100:103],v[100:103],a[200:203]
ds_read_b64_tr_b16 a[100:101],v43 offset:10336
v_mfma_f32_16x16x32_bf16 a[200:203],a[104:107],v[104:107],a[200:203]
ds_read_b64_tr_b16 a[102:103],v43 offset:15456
v_mfma_f32_16x16x32_bf16 a[200:203],a[108:111],v[108:111],a[200:203]
ds_read_b64_tr_b16 a[104:105],v43 offset:20576
v_mfma_f32_16x16x32_bf16 a[192:195],a[112:115],v[136:139],a[192:195]
s_waitcnt vmcnt(3)
ds_read_b64_tr_b16 a[106:107],v43 offset:25696
ds_read_b64_tr_b16 a[108:109],v43 offset:30816
v_mfma_f32_16x16x32_bf16 a[192:195],a[116:119],v[140:143],a[192:195]
ds_read_b64_tr_b16 a[110:111],v43 offset:35936
v_mfma_f32_16x16x32_bf16 a[192:195],a[120:123],v[152:155],a[192:195]
v_mfma_f32_16x16x32_bf16 a[192:195],a[124:127],v[156:159],a[192:195]
v_mfma_f32_16x16x32_bf16 a[196:199],a[124:127],v[188:191],a[196:199]
v_mfma_f32_16x16x32_bf16 a[196:199],a[120:123],v[184:187],a[196:199]
v_mfma_f32_16x16x32_bf16 a[196:199],a[116:119],v[172:175],a[196:199]
v_mfma_f32_16x16x32_bf16 a[196:199],a[112:115],v[168:171],a[196:199]
v_mfma_f32_16x16x32_bf16 a[200:203],a[112:115],v[200:203],a[200:203]
ds_read_b64_tr_b16 a[112:113],v45 offset:96
ds_read_b64_tr_b16 a[114:115],v45 offset:5216
v_mfma_f32_16x16x32_bf16 a[200:203],a[116:119],v[204:207],a[200:203]
v_accvgpr_read_b32 v128,a192
v_accvgpr_read_b32 v129,a193
v_mfma_f32_16x16x32_bf16 a[200:203],a[120:123],v[216:219],a[200:203]
v_cvt_pk_bf16_f32 v128,v128,v129
v_accvgpr_read_b32 v130,a194
v_mfma_f32_16x16x32_bf16 a[200:203],a[124:127],v[220:223],a[200:203]
v_accvgpr_read_b32 v131,a195
v_cvt_pk_bf16_f32 v129,v130,v131
v_mfma_f32_16x16x32_bf16 a[208:211],a[128:131],v[64:67],a[208:211]
ds_read_b64_tr_b16 a[116:117],v45 offset:10336
ds_read_b64_tr_b16 a[118:119],v45 offset:15456
v_mfma_f32_16x16x32_bf16 a[208:211],a[132:135],v[68:71],a[208:211]
v_accvgpr_read_b32 v132,a196
v_accvgpr_read_b32 v133,a197
v_mfma_f32_16x16x32_bf16 a[208:211],a[136:139],v[72:75],a[208:211]
v_cvt_pk_bf16_f32 v132,v132,v133
v_accvgpr_read_b32 v134,a198
v_mfma_f32_16x16x32_bf16 a[208:211],a[140:143],v[76:79],a[208:211]
v_accvgpr_read_b32 v135,a199
v_cvt_pk_bf16_f32 v133,v134,v135
v_mfma_f32_16x16x32_bf16 a[212:215],a[140:143],v[92:95],a[212:215]
ds_read_b64_tr_b16 a[120:121],v45 offset:20576
ds_read_b64_tr_b16 a[122:123],v45 offset:25696
v_mfma_f32_16x16x32_bf16 a[212:215],a[136:139],v[88:91],a[212:215]
ds_read_b64_tr_b16 a[124:125],v45 offset:30816
v_accvgpr_read_b32 v144,a200
v_mfma_f32_16x16x32_bf16 a[212:215],a[132:135],v[84:87],a[212:215]
ds_read_b64_tr_b16 a[126:127],v45 offset:35936
v_accvgpr_read_b32 v145,a201
v_mfma_f32_16x16x32_bf16 a[212:215],a[128:131],v[80:83],a[212:215]
v_cvt_pk_bf16_f32 v144,v144,v145
v_accvgpr_read_b32 v146,a202
v_mfma_f32_16x16x32_bf16 a[216:219],a[128:131],v[96:99],a[216:219]
v_accvgpr_read_b32 v147,a203
v_cvt_pk_bf16_f32 v145,v146,v147
v_mfma_f32_16x16x32_bf16 a[216:219],a[132:135],v[100:103],a[216:219]
ds_read_b64_tr_b16 a[128:129],v42 offset:128
ds_read_b64_tr_b16 a[130:131],v42 offset:5248
v_mfma_f32_16x16x32_bf16 a[216:219],a[136:139],v[104:107],a[216:219]
ds_read_b64_tr_b16 a[132:133],v42 offset:10368
v_mfma_f32_16x16x32_bf16 a[216:219],a[140:143],v[108:111],a[216:219]
ds_read_b64_tr_b16 a[134:135],v42 offset:15488
v_mfma_f32_16x16x32_bf16 a[208:211],a[144:147],v[136:139],a[208:211]
ds_read_b64_tr_b16 a[136:137],v42 offset:20608
v_mfma_f32_16x16x32_bf16 a[208:211],a[148:151],v[140:143],a[208:211]
ds_read_b64_tr_b16 a[138:139],v42 offset:25728
ds_read_b64_tr_b16 a[140:141],v42 offset:30848
v_mfma_f32_16x16x32_bf16 a[208:211],a[152:155],v[152:155],a[208:211]
ds_read_b64_tr_b16 a[142:143],v42 offset:35968
v_mfma_f32_16x16x32_bf16 a[208:211],a[156:159],v[156:159],a[208:211]
v_mfma_f32_16x16x32_bf16 a[212:215],a[156:159],v[188:191],a[212:215]
v_mfma_f32_16x16x32_bf16 a[212:215],a[152:155],v[184:187],a[212:215]
v_mfma_f32_16x16x32_bf16 a[212:215],a[148:151],v[172:175],a[212:215]
v_mfma_f32_16x16x32_bf16 a[212:215],a[144:147],v[168:171],a[212:215]
v_mfma_f32_16x16x32_bf16 a[216:219],a[144:147],v[200:203],a[216:219]
ds_read_b64_tr_b16 a[144:145],v44 offset:128
ds_read_b64_tr_b16 a[146:147],v44 offset:5248
v_mfma_f32_16x16x32_bf16 a[216:219],a[148:151],v[204:207],a[216:219]
v_accvgpr_read_b32 v130,a208
v_accvgpr_read_b32 v131,a209
v_mfma_f32_16x16x32_bf16 a[216:219],a[152:155],v[216:219],a[216:219]
v_accvgpr_read_b32 v16,a210
v_accvgpr_read_b32 v17,a211
v_mfma_f32_16x16x32_bf16 a[216:219],a[156:159],v[220:223],a[216:219]
v_cvt_pk_bf16_f32 v130,v130,v131
v_cvt_pk_bf16_f32 v131,v16,v17
v_mfma_f32_16x16x32_bf16 a[224:227],a[160:163],v[64:67],a[224:227]
buffer_store_dwordx4 v[128:131],v6,s[32:35],s84 offen sc1
v_mfma_f32_16x16x32_bf16 a[224:227],a[164:167],v[68:71],a[224:227]
v_accvgpr_read_b32 v134,a212
v_accvgpr_read_b32 v135,a213
v_mfma_f32_16x16x32_bf16 a[224:227],a[168:171],v[72:75],a[224:227]
v_accvgpr_read_b32 v12,a214
v_accvgpr_read_b32 v13,a215
v_mfma_f32_16x16x32_bf16 a[224:227],a[172:175],v[76:79],a[224:227]
v_cvt_pk_bf16_f32 v134,v134,v135
v_cvt_pk_bf16_f32 v135,v12,v13
v_mfma_f32_16x16x32_bf16 a[228:231],a[172:175],v[92:95],a[228:231]
buffer_store_dwordx4 v[132:135],v6,s[32:35],s85 offen sc1
v_mfma_f32_16x16x32_bf16 a[228:231],a[168:171],v[88:91],a[228:231]
v_accvgpr_read_b32 v146,a216
v_accvgpr_read_b32 v147,a217
v_mfma_f32_16x16x32_bf16 a[228:231],a[164:167],v[84:87],a[228:231]
v_accvgpr_read_b32 v14,a218
v_accvgpr_read_b32 v15,a219
v_mfma_f32_16x16x32_bf16 a[228:231],a[160:163],v[80:83],a[228:231]
v_cvt_pk_bf16_f32 v146,v146,v147
v_cvt_pk_bf16_f32 v147,v14,v15
v_mfma_f32_16x16x32_bf16 a[232:235],a[160:163],v[96:99],a[232:235]
buffer_store_dwordx4 v[144:147],v6,s[32:35],s86 offen sc1
v_mfma_f32_16x16x32_bf16 a[232:235],a[164:167],v[100:103],a[232:235]
ds_read_b64_tr_b16 a[148:149],v44 offset:10368
ds_read_b64_tr_b16 a[150:151],v44 offset:15488
v_mfma_f32_16x16x32_bf16 a[232:235],a[168:171],v[104:107],a[232:235]
ds_read_b64_tr_b16 a[152:153],v44 offset:20608
v_mfma_f32_16x16x32_bf16 a[232:235],a[172:175],v[108:111],a[232:235]
s_waitcnt lgkmcnt(3)
ds_read_b64_tr_b16 a[154:155],v44 offset:25728
v_mfma_f32_16x16x32_bf16 a[224:227],a[176:179],v[136:139],a[224:227]
ds_read_b64_tr_b16 a[156:157],v44 offset:30848
v_mfma_f32_16x16x32_bf16 a[224:227],a[180:183],v[140:143],a[224:227]
ds_read_b64_tr_b16 a[158:159],v44 offset:35968
ds_read_b64_tr_b16 a[160:161],v43 offset:160
v_mfma_f32_16x16x32_bf16 a[224:227],a[184:187],v[152:155],a[224:227]
ds_read_b64_tr_b16 a[162:163],v43 offset:5280
v_mfma_f32_16x16x32_bf16 a[224:227],a[188:191],v[156:159],a[224:227]
ds_read_b64_tr_b16 a[164:165],v43 offset:10400
v_mfma_f32_16x16x32_bf16 a[228:231],a[188:191],v[188:191],a[228:231]
ds_read_b64_tr_b16 a[166:167],v43 offset:15520
v_mfma_f32_16x16x32_bf16 a[228:231],a[184:187],v[184:187],a[228:231]
ds_read_b64_tr_b16 a[168:169],v43 offset:20640
ds_read_b64_tr_b16 a[170:171],v43 offset:25760
v_mfma_f32_16x16x32_bf16 a[228:231],a[180:183],v[172:175],a[228:231]
ds_read_b64_tr_b16 a[172:173],v43 offset:30880
v_mfma_f32_16x16x32_bf16 a[228:231],a[176:179],v[168:171],a[228:231]
ds_read_b64_tr_b16 a[174:175],v43 offset:36000
v_mfma_f32_16x16x32_bf16 a[232:235],a[176:179],v[200:203],a[232:235]
ds_read_b64_tr_b16 a[176:177],v45 offset:160
v_mfma_f32_16x16x32_bf16 a[232:235],a[180:183],v[204:207],a[232:235]
v_accvgpr_read_b32 v160,a224
v_accvgpr_read_b32 v161,a225
v_mfma_f32_16x16x32_bf16 a[232:235],a[184:187],v[216:219],a[232:235]
v_cvt_pk_bf16_f32 v160,v160,v161
v_accvgpr_read_b32 v162,a226
v_mfma_f32_16x16x32_bf16 a[232:235],a[188:191],v[220:223],a[232:235]
v_accvgpr_read_b32 v163,a227
v_cvt_pk_bf16_f32 v161,v162,v163
v_mfma_f32_16x16x32_bf16 a[240:243],a[96:99],v[64:67],a[240:243]
ds_read_b64_tr_b16 a[178:179],v45 offset:5280
ds_read_b64_tr_b16 a[180:181],v45 offset:10400
v_mfma_f32_16x16x32_bf16 a[240:243],a[100:103],v[68:71],a[240:243]
v_accvgpr_read_b32 v164,a228
v_accvgpr_read_b32 v165,a229
v_mfma_f32_16x16x32_bf16 a[240:243],a[104:107],v[72:75],a[240:243]
v_cvt_pk_bf16_f32 v164,v164,v165
v_accvgpr_read_b32 v166,a230
v_mfma_f32_16x16x32_bf16 a[240:243],a[108:111],v[76:79],a[240:243]
v_accvgpr_read_b32 v167,a231
v_cvt_pk_bf16_f32 v165,v166,v167
v_mfma_f32_16x16x32_bf16 a[244:247],a[108:111],v[92:95],a[244:247]
ds_read_b64_tr_b16 a[182:183],v45 offset:15520
ds_read_b64_tr_b16 a[184:185],v45 offset:20640
v_mfma_f32_16x16x32_bf16 a[244:247],a[104:107],v[88:91],a[244:247]
v_accvgpr_read_b32 v176,a232
v_accvgpr_read_b32 v177,a233
)A"
R"A(v_mfma_f32_16x16x32_bf16 a[244:247],a[100:103],v[84:87],a[244:247]
v_cvt_pk_bf16_f32 v176,v176,v177
v_accvgpr_read_b32 v178,a234
v_mfma_f32_16x16x32_bf16 a[244:247],a[96:99],v[80:83],a[244:247]
v_accvgpr_read_b32 v179,a235
v_cvt_pk_bf16_f32 v177,v178,v179
v_mfma_f32_16x16x32_bf16 a[248:251],a[96:99],v[96:99],a[248:251]
ds_read_b64_tr_b16 a[186:187],v45 offset:25760
ds_read_b64_tr_b16 a[188:189],v45 offset:30880
v_mfma_f32_16x16x32_bf16 a[248:251],a[100:103],v[100:103],a[248:251]
ds_read_b64_tr_b16 a[190:191],v45 offset:36000
v_mfma_f32_16x16x32_bf16 a[248:251],a[104:107],v[104:107],a[248:251]
ds_read_b64_tr_b16 a[96:97],v42 offset:192
v_mfma_f32_16x16x32_bf16 a[248:251],a[108:111],v[108:111],a[248:251]
ds_read_b64_tr_b16 a[98:99],v42 offset:5312
v_mfma_f32_16x16x32_bf16 a[240:243],a[112:115],v[136:139],a[240:243]
ds_read_b64_tr_b16 a[100:101],v42 offset:10432
ds_read_b64_tr_b16 a[102:103],v42 offset:15552
v_mfma_f32_16x16x32_bf16 a[240:243],a[116:119],v[140:143],a[240:243]
ds_read_b64_tr_b16 a[104:105],v42 offset:20672
v_mfma_f32_16x16x32_bf16 a[240:243],a[120:123],v[152:155],a[240:243]
ds_read_b64_tr_b16 a[106:107],v42 offset:25792
v_mfma_f32_16x16x32_bf16 a[240:243],a[124:127],v[156:159],a[240:243]
ds_read_b64_tr_b16 a[108:109],v42 offset:30912
v_mfma_f32_16x16x32_bf16 a[244:247],a[124:127],v[188:191],a[244:247]
ds_read_b64_tr_b16 a[110:111],v42 offset:36032
v_mfma_f32_16x16x32_bf16 a[244:247],a[120:123],v[184:187],a[244:247]
v_mfma_f32_16x16x32_bf16 a[244:247],a[116:119],v[172:175],a[244:247]
v_mfma_f32_16x16x32_bf16 a[244:247],a[112:115],v[168:171],a[244:247]
v_mfma_f32_16x16x32_bf16 a[248:251],a[112:115],v[200:203],a[248:251]
ds_read_b64_tr_b16 a[112:113],v44 offset:192
ds_read_b64_tr_b16 a[114:115],v44 offset:5312
v_mfma_f32_16x16x32_bf16 a[248:251],a[116:119],v[204:207],a[248:251]
v_accvgpr_read_b32 v162,a240
v_accvgpr_read_b32 v163,a241
v_mfma_f32_16x16x32_bf16 a[248:251],a[120:123],v[216:219],a[248:251]
v_accvgpr_read_b32 v16,a242
v_accvgpr_read_b32 v17,a243
v_mfma_f32_16x16x32_bf16 a[248:251],a[124:127],v[220:223],a[248:251]
v_cvt_pk_bf16_f32 v162,v162,v163
v_cvt_pk_bf16_f32 v163,v16,v17
v_mfma_f32_16x16x32_bf16 a[0:3],a[128:131],v[64:67],a[0:3]
buffer_store_dwordx4 v[160:163],v6,s[32:35],s84 offen offset:1024 sc1
v_mfma_f32_16x16x32_bf16 a[0:3],a[132:135],v[68:71],a[0:3]
v_accvgpr_read_b32 v166,a244
v_accvgpr_read_b32 v167,a245
v_mfma_f32_16x16x32_bf16 a[0:3],a[136:139],v[72:75],a[0:3]
v_accvgpr_read_b32 v12,a246
v_accvgpr_read_b32 v13,a247
v_mfma_f32_16x16x32_bf16 a[0:3],a[140:143],v[76:79],a[0:3]
v_cvt_pk_bf16_f32 v166,v166,v167
v_cvt_pk_bf16_f32 v167,v12,v13
v_mfma_f32_16x16x32_bf16 a[4:7],a[140:143],v[92:95],a[4:7]
buffer_store_dwordx4 v[164:167],v6,s[32:35],s85 offen offset:1024 sc1
v_mfma_f32_16x16x32_bf16 a[4:7],a[136:139],v[88:91],a[4:7]
v_accvgpr_read_b32 v178,a248
v_accvgpr_read_b32 v179,a249
v_mfma_f32_16x16x32_bf16 a[4:7],a[132:135],v[84:87],a[4:7]
v_accvgpr_read_b32 v14,a250
v_accvgpr_read_b32 v15,a251
v_cvt_pk_bf16_f32 v178,v178,v179
v_cvt_pk_bf16_f32 v179,v14,v15
v_mfma_f32_16x16x32_bf16 a[4:7],a[128:131],v[80:83],a[4:7]
ds_read_b64_tr_b16 a[116:117],v44 offset:10432
ds_read_b64_tr_b16 a[118:119],v44 offset:15552
v_mfma_f32_16x16x32_bf16 a[8:11],a[128:131],v[96:99],a[8:11]
buffer_store_dwordx4 v[176:179],v6,s[32:35],s86 offen offset:1024 sc1
v_mfma_f32_16x16x32_bf16 a[8:11],a[132:135],v[100:103],a[8:11]
ds_read_b64_tr_b16 a[120:121],v44 offset:20672
ds_read_b64_tr_b16 a[122:123],v44 offset:25792
v_mfma_f32_16x16x32_bf16 a[8:11],a[136:139],v[104:107],a[8:11]
ds_read_b64_tr_b16 a[124:125],v44 offset:30912
v_mfma_f32_16x16x32_bf16 a[8:11],a[140:143],v[108:111],a[8:11]
ds_read_b64_tr_b16 a[126:127],v44 offset:36032
v_mfma_f32_16x16x32_bf16 a[0:3],a[144:147],v[136:139],a[0:3]
s_waitcnt lgkmcnt(6)
ds_read_b64_tr_b16 a[128:129],v43 offset:224
v_mfma_f32_16x16x32_bf16 a[0:3],a[148:151],v[140:143],a[0:3]
ds_read_b64_tr_b16 a[130:131],v43 offset:5344
ds_read_b64_tr_b16 a[132:133],v43 offset:10464
v_mfma_f32_16x16x32_bf16 a[0:3],a[152:155],v[152:155],a[0:3]
ds_read_b64_tr_b16 a[134:135],v43 offset:15584
v_mfma_f32_16x16x32_bf16 a[0:3],a[156:159],v[156:159],a[0:3]
ds_read_b64_tr_b16 a[136:137],v43 offset:20704
v_mfma_f32_16x16x32_bf16 a[4:7],a[156:159],v[188:191],a[4:7]
ds_read_b64_tr_b16 a[138:139],v43 offset:25824
v_mfma_f32_16x16x32_bf16 a[4:7],a[152:155],v[184:187],a[4:7]
ds_read_b64_tr_b16 a[140:141],v43 offset:30944
ds_read_b64_tr_b16 a[142:143],v43 offset:36064
v_mfma_f32_16x16x32_bf16 a[4:7],a[148:151],v[172:175],a[4:7]
v_mfma_f32_16x16x32_bf16 a[4:7],a[144:147],v[168:171],a[4:7]
v_mfma_f32_16x16x32_bf16 a[8:11],a[144:147],v[200:203],a[8:11]
ds_read_b64_tr_b16 a[144:145],v45 offset:224
ds_read_b64_tr_b16 a[146:147],v45 offset:5344
v_mfma_f32_16x16x32_bf16 a[8:11],a[148:151],v[204:207],a[8:11]
v_accvgpr_read_b32 v192,a0
v_accvgpr_read_b32 v193,a1
v_mfma_f32_16x16x32_bf16 a[8:11],a[152:155],v[216:219],a[8:11]
v_cvt_pk_bf16_f32 v192,v192,v193
v_accvgpr_read_b32 v194,a2
v_mfma_f32_16x16x32_bf16 a[8:11],a[156:159],v[220:223],a[8:11]
v_accvgpr_read_b32 v195,a3
v_cvt_pk_bf16_f32 v193,v194,v195
v_mfma_f32_16x16x32_bf16 a[16:19],a[160:163],v[64:67],a[16:19]
ds_read_b64_tr_b16 a[148:149],v45 offset:10464
ds_read_b64_tr_b16 a[150:151],v45 offset:15584
v_mfma_f32_16x16x32_bf16 a[16:19],a[164:167],v[68:71],a[16:19]
v_accvgpr_read_b32 v196,a4
v_accvgpr_read_b32 v197,a5
v_mfma_f32_16x16x32_bf16 a[16:19],a[168:171],v[72:75],a[16:19]
v_cvt_pk_bf16_f32 v196,v196,v197
v_accvgpr_read_b32 v198,a6
v_mfma_f32_16x16x32_bf16 a[16:19],a[172:175],v[76:79],a[16:19]
v_accvgpr_read_b32 v199,a7
v_cvt_pk_bf16_f32 v197,v198,v199
v_mfma_f32_16x16x32_bf16 a[20:23],a[172:175],v[92:95],a[20:23]
ds_read_b64_tr_b16 a[152:153],v45 offset:20704
ds_read_b64_tr_b16 a[154:155],v45 offset:25824
v_mfma_f32_16x16x32_bf16 a[20:23],a[168:171],v[88:91],a[20:23]
ds_read_b64_tr_b16 a[156:157],v45 offset:30944
v_accvgpr_read_b32 v208,a8
v_mfma_f32_16x16x32_bf16 a[20:23],a[164:167],v[84:87],a[20:23]
ds_read_b64_tr_b16 a[158:159],v45 offset:36064
v_accvgpr_read_b32 v209,a9
v_mfma_f32_16x16x32_bf16 a[20:23],a[160:163],v[80:83],a[20:23]
v_cvt_pk_bf16_f32 v208,v208,v209
v_accvgpr_read_b32 v210,a10
v_mfma_f32_16x16x32_bf16 a[24:27],a[160:163],v[96:99],a[24:27]
v_accvgpr_read_b32 v211,a11
v_cvt_pk_bf16_f32 v209,v210,v211
v_mfma_f32_16x16x32_bf16 a[24:27],a[164:167],v[100:103],a[24:27]
ds_read_b64_tr_b16 a[160:161],v42 offset:256
ds_read_b64_tr_b16 a[162:163],v42 offset:5376
v_mfma_f32_16x16x32_bf16 a[24:27],a[168:171],v[104:107],a[24:27]
ds_read_b64_tr_b16 a[164:165],v42 offset:10496
v_mfma_f32_16x16x32_bf16 a[24:27],a[172:175],v[108:111],a[24:27]
ds_read_b64_tr_b16 a[166:167],v42 offset:15616
v_mfma_f32_16x16x32_bf16 a[16:19],a[176:179],v[136:139],a[16:19]
ds_read_b64_tr_b16 a[168:169],v42 offset:20736
v_mfma_f32_16x16x32_bf16 a[16:19],a[180:183],v[140:143],a[16:19]
ds_read_b64_tr_b16 a[170:171],v42 offset:25856
ds_read_b64_tr_b16 a[172:173],v42 offset:30976
v_mfma_f32_16x16x32_bf16 a[16:19],a[184:187],v[152:155],a[16:19]
ds_read_b64_tr_b16 a[174:175],v42 offset:36096
v_mfma_f32_16x16x32_bf16 a[16:19],a[188:191],v[156:159],a[16:19]
v_mfma_f32_16x16x32_bf16 a[20:23],a[188:191],v[188:191],a[20:23]
v_mfma_f32_16x16x32_bf16 a[20:23],a[184:187],v[184:187],a[20:23]
v_mfma_f32_16x16x32_bf16 a[20:23],a[180:183],v[172:175],a[20:23]
v_mfma_f32_16x16x32_bf16 a[20:23],a[176:179],v[168:171],a[20:23]
v_mfma_f32_16x16x32_bf16 a[24:27],a[176:179],v[200:203],a[24:27]
ds_read_b64_tr_b16 a[176:177],v44 offset:256
ds_read_b64_tr_b16 a[178:179],v44 offset:5376
v_mfma_f32_16x16x32_bf16 a[24:27],a[180:183],v[204:207],a[24:27]
s_lshl_b32 s95,s42,2
s_add_u32 s95,s95,s40
s_lshl_b32 s95,s95,4
v_mfma_f32_16x16x32_bf16 a[24:27],a[184:187],v[216:219],a[24:27]
s_addk_i32 s95,5120
v_bfe_u32 v19,s55,13,1
v_mov_b32 v18,s68
v_or_b32 v19,s69,v19
v_mfma_f32_16x16x32_bf16 a[24:27],a[188:191],v[220:223],a[24:27]
s_waitcnt vmcnt(0)
s_mov_b64 exec,1
buffer_store_dwordx2 v[18:19],v6,s[64:67],s95 offen sc1
s_mov_b64 exec,-1
v_mfma_f32_16x16x32_bf16 a[32:35],a[96:99],v[64:67],a[32:35]
v_accvgpr_read_b32 v194,a16
v_accvgpr_read_b32 v195,a17
v_mfma_f32_16x16x32_bf16 a[32:35],a[100:103],v[68:71],a[32:35]
v_accvgpr_read_b32 v16,a18
v_accvgpr_read_b32 v17,a19
v_mfma_f32_16x16x32_bf16 a[32:35],a[104:107],v[72:75],a[32:35]
v_cvt_pk_bf16_f32 v194,v194,v195
v_cvt_pk_bf16_f32 v195,v16,v17
v_mfma_f32_16x16x32_bf16 a[32:35],a[108:111],v[76:79],a[32:35]
buffer_store_dwordx4 v[192:195],v6,s[32:35],s84 offen offset:2048 sc1
v_mfma_f32_16x16x32_bf16 a[36:39],a[108:111],v[92:95],a[36:39]
v_accvgpr_read_b32 v198,a20
v_accvgpr_read_b32 v199,a21
v_mfma_f32_16x16x32_bf16 a[36:39],a[104:107],v[88:91],a[36:39]
v_accvgpr_read_b32 v12,a22
v_accvgpr_read_b32 v13,a23
v_mfma_f32_16x16x32_bf16 a[36:39],a[100:103],v[84:87],a[36:39]
v_cvt_pk_bf16_f32 v198,v198,v199
v_cvt_pk_bf16_f32 v199,v12,v13
v_mfma_f32_16x16x32_bf16 a[36:39],a[96:99],v[80:83],a[36:39]
buffer_store_dwordx4 v[196:199],v6,s[32:35],s85 offen offset:2048 sc1
v_mfma_f32_16x16x32_bf16 a[40:43],a[96:99],v[96:99],a[40:43]
v_accvgpr_read_b32 v210,a24
v_accvgpr_read_b32 v211,a25
v_mfma_f32_16x16x32_bf16 a[40:43],a[100:103],v[100:103],a[40:43]
v_accvgpr_read_b32 v14,a26
v_accvgpr_read_b32 v15,a27
v_mfma_f32_16x16x32_bf16 a[40:43],a[104:107],v[104:107],a[40:43]
v_cvt_pk_bf16_f32 v210,v210,v211
v_cvt_pk_bf16_f32 v211,v14,v15
v_mfma_f32_16x16x32_bf16 a[40:43],a[108:111],v[108:111],a[40:43]
buffer_store_dwordx4 v[208:211],v6,s[32:35],s86 offen offset:2048 sc1
v_mfma_f32_16x16x32_bf16 a[32:35],a[112:115],v[136:139],a[32:35]
s_waitcnt lgkmcnt(0)
ds_read_b64_tr_b16 a[180:181],v44 offset:10496
ds_read_b64_tr_b16 a[182:183],v44 offset:15616
v_mfma_f32_16x16x32_bf16 a[32:35],a[116:119],v[140:143],a[32:35]
ds_read_b64_tr_b16 a[184:185],v44 offset:20736
v_mfma_f32_16x16x32_bf16 a[32:35],a[120:123],v[152:155],a[32:35]
ds_read_b64_tr_b16 a[186:187],v44 offset:25856
v_mfma_f32_16x16x32_bf16 a[32:35],a[124:127],v[156:159],a[32:35]
ds_read_b64_tr_b16 a[188:189],v44 offset:30976
v_mfma_f32_16x16x32_bf16 a[36:39],a[124:127],v[188:191],a[36:39]
ds_read_b64_tr_b16 a[190:191],v44 offset:36096
ds_read_b64_tr_b16 a[96:97],v43 offset:288
v_mfma_f32_16x16x32_bf16 a[36:39],a[120:123],v[184:187],a[36:39]
ds_read_b64_tr_b16 a[98:99],v43 offset:5408
v_mfma_f32_16x16x32_bf16 a[36:39],a[116:119],v[172:175],a[36:39]
ds_read_b64_tr_b16 a[100:101],v43 offset:10528
v_mfma_f32_16x16x32_bf16 a[36:39],a[112:115],v[168:171],a[36:39]
ds_read_b64_tr_b16 a[102:103],v43 offset:15648
v_mfma_f32_16x16x32_bf16 a[40:43],a[112:115],v[200:203],a[40:43]
ds_read_b64_tr_b16 a[104:105],v43 offset:20768
ds_read_b64_tr_b16 a[106:107],v43 offset:25888
v_mfma_f32_16x16x32_bf16 a[40:43],a[116:119],v[204:207],a[40:43]
v_accvgpr_read_b32 v224,a32
v_accvgpr_read_b32 v225,a33
v_mfma_f32_16x16x32_bf16 a[40:43],a[120:123],v[216:219],a[40:43]
s_lshl_b32 s94,s71,2
v_cvt_pk_bf16_f32 v224,v224,v225
v_accvgpr_read_b32 v226,a34
v_mfma_f32_16x16x32_bf16 a[40:43],a[124:127],v[220:223],a[40:43]
s_add_u32 s94,s94,s40
v_accvgpr_read_b32 v227,a35
v_cvt_pk_bf16_f32 v225,v226,v227
v_mfma_f32_16x16x32_bf16 a[48:51],a[128:131],v[64:67],a[48:51]
s_lshl_b32 s94,s94,4
v_bfe_u32 v24,v6,4,2
v_min_u32 v24,2,v24
v_mfma_f32_16x16x32_bf16 a[48:51],a[132:135],v[68:71],a[48:51]
s_addk_i32 s94,5120
v_accvgpr_read_b32 v228,a36
v_accvgpr_read_b32 v229,a37
v_mfma_f32_16x16x32_bf16 a[48:51],a[136:139],v[72:75],a[48:51]
v_cvt_pk_bf16_f32 v228,v228,v229
v_accvgpr_read_b32 v230,a38
v_mfma_f32_16x16x32_bf16 a[48:51],a[140:143],v[76:79],a[48:51]
v_accvgpr_read_b32 v231,a39
v_cvt_pk_bf16_f32 v229,v230,v231
v_mfma_f32_16x16x32_bf16 a[52:55],a[140:143],v[92:95],a[52:55]
v_add_u32 v24,s70,v24
v_add_u32 v24,1,v24
v_mfma_f32_16x16x32_bf16 a[52:55],a[136:139],v[88:91],a[52:55]
v_accvgpr_read_b32 v240,a40
v_accvgpr_read_b32 v241,a41
v_mfma_f32_16x16x32_bf16 a[52:55],a[132:135],v[84:87],a[52:55]
v_cvt_pk_bf16_f32 v240,v240,v241
v_accvgpr_read_b32 v242,a42
v_mfma_f32_16x16x32_bf16 a[52:55],a[128:131],v[80:83],a[52:55]
v_accvgpr_read_b32 v243,a43
)A"
R"A(v_cvt_pk_bf16_f32 v241,v242,v243
v_mfma_f32_16x16x32_bf16 a[56:59],a[128:131],v[96:99],a[56:59]
v_and_b32 v24,3,v24
v_lshlrev_b32 v24,8,v24
v_mfma_f32_16x16x32_bf16 a[56:59],a[132:135],v[100:103],a[56:59]
ds_read_b64_tr_b16 a[108:109],v43 offset:31008
ds_read_b64_tr_b16 a[110:111],v43 offset:36128
v_mfma_f32_16x16x32_bf16 a[56:59],a[136:139],v[104:107],a[56:59]
ds_read_b64_tr_b16 a[112:113],v45 offset:288
v_mfma_f32_16x16x32_bf16 a[56:59],a[140:143],v[108:111],a[56:59]
ds_read_b64_tr_b16 a[114:115],v45 offset:5408
v_mfma_f32_16x16x32_bf16 a[48:51],a[144:147],v[136:139],a[48:51]
ds_read_b64_tr_b16 a[116:117],v45 offset:10528
v_mfma_f32_16x16x32_bf16 a[48:51],a[148:151],v[140:143],a[48:51]
ds_read_b64_tr_b16 a[118:119],v45 offset:15648
ds_read_b64_tr_b16 a[120:121],v45 offset:20768
v_mfma_f32_16x16x32_bf16 a[48:51],a[152:155],v[152:155],a[48:51]
ds_read_b64_tr_b16 a[122:123],v45 offset:25888
v_mfma_f32_16x16x32_bf16 a[48:51],a[156:159],v[156:159],a[48:51]
ds_read_b64_tr_b16 a[124:125],v45 offset:31008
v_mfma_f32_16x16x32_bf16 a[52:55],a[156:159],v[188:191],a[52:55]
ds_read_b64_tr_b16 a[126:127],v45 offset:36128
v_mfma_f32_16x16x32_bf16 a[52:55],a[152:155],v[184:187],a[52:55]
ds_read_b64_tr_b16 a[128:129],v1
ds_read_b64_tr_b16 a[130:131],v1 offset:5120
v_mfma_f32_16x16x32_bf16 a[52:55],a[148:151],v[172:175],a[52:55]
ds_read_b64_tr_b16 a[132:133],v1 offset:10240
v_mfma_f32_16x16x32_bf16 a[52:55],a[144:147],v[168:171],a[52:55]
ds_read_b64_tr_b16 a[134:135],v1 offset:15360
v_mfma_f32_16x16x32_bf16 a[56:59],a[144:147],v[200:203],a[56:59]
ds_read_b64_tr_b16 a[136:137],v1 offset:20480
v_mfma_f32_16x16x32_bf16 a[56:59],a[148:151],v[204:207],a[56:59]
v_accvgpr_read_b32 v226,a48
v_accvgpr_read_b32 v227,a49
v_mfma_f32_16x16x32_bf16 a[56:59],a[152:155],v[216:219],a[56:59]
v_accvgpr_read_b32 v16,a50
v_accvgpr_read_b32 v17,a51
v_mfma_f32_16x16x32_bf16 a[56:59],a[156:159],v[220:223],a[56:59]
v_cvt_pk_bf16_f32 v226,v226,v227
v_cvt_pk_bf16_f32 v227,v16,v17
v_mfma_f32_16x16x32_bf16 a[64:67],a[160:163],v[64:67],a[64:67]
buffer_store_dwordx4 v[224:227],v6,s[32:35],s84 offen offset:3072 sc1
v_mfma_f32_16x16x32_bf16 a[64:67],a[164:167],v[68:71],a[64:67]
v_accvgpr_read_b32 v230,a52
v_accvgpr_read_b32 v231,a53
v_mfma_f32_16x16x32_bf16 a[64:67],a[168:171],v[72:75],a[64:67]
v_accvgpr_read_b32 v12,a54
v_accvgpr_read_b32 v13,a55
v_mfma_f32_16x16x32_bf16 a[64:67],a[172:175],v[76:79],a[64:67]
v_cvt_pk_bf16_f32 v230,v230,v231
v_cvt_pk_bf16_f32 v231,v12,v13
v_mfma_f32_16x16x32_bf16 a[68:71],a[172:175],v[92:95],a[68:71]
buffer_store_dwordx4 v[228:231],v6,s[32:35],s85 offen offset:3072 sc1
v_mfma_f32_16x16x32_bf16 a[68:71],a[168:171],v[88:91],a[68:71]
v_accvgpr_read_b32 v242,a56
v_accvgpr_read_b32 v243,a57
v_mfma_f32_16x16x32_bf16 a[68:71],a[164:167],v[84:87],a[68:71]
v_accvgpr_read_b32 v14,a58
v_accvgpr_read_b32 v15,a59
v_mfma_f32_16x16x32_bf16 a[68:71],a[160:163],v[80:83],a[68:71]
v_cvt_pk_bf16_f32 v242,v242,v243
v_cvt_pk_bf16_f32 v243,v14,v15
v_mfma_f32_16x16x32_bf16 a[72:75],a[160:163],v[96:99],a[72:75]
buffer_store_dwordx4 v[240:243],v6,s[32:35],s86 offen offset:3072 sc1
v_mfma_f32_16x16x32_bf16 a[72:75],a[164:167],v[100:103],a[72:75]
ds_read_b64_tr_b16 a[138:139],v1 offset:25600
ds_read_b64_tr_b16 a[140:141],v1 offset:30720
v_mfma_f32_16x16x32_bf16 a[72:75],a[168:171],v[104:107],a[72:75]
ds_read_b64_tr_b16 a[142:143],v1 offset:35840
v_mfma_f32_16x16x32_bf16 a[72:75],a[172:175],v[108:111],a[72:75]
ds_read_b64_tr_b16 a[144:145],v42
v_mfma_f32_16x16x32_bf16 a[64:67],a[176:179],v[136:139],a[64:67]
s_waitcnt lgkmcnt(4)
ds_read_b64_tr_b16 a[146:147],v42 offset:5120
v_mfma_f32_16x16x32_bf16 a[64:67],a[180:183],v[140:143],a[64:67]
ds_read_b64_tr_b16 a[148:149],v42 offset:10240
ds_read_b64_tr_b16 a[150:151],v42 offset:15360
v_mfma_f32_16x16x32_bf16 a[64:67],a[184:187],v[152:155],a[64:67]
buffer_load_dwordx2 v[4:5],v24,s[64:67],s94 offen sc1
v_mfma_f32_16x16x32_bf16 a[64:67],a[188:191],v[156:159],a[64:67]
ds_read_b64_tr_b16 a[152:153],v42 offset:20480
ds_read_b64_tr_b16 a[154:155],v42 offset:25600
v_mfma_f32_16x16x32_bf16 a[68:71],a[188:191],v[188:191],a[68:71]
ds_read_b64_tr_b16 a[156:157],v42 offset:30720
v_mfma_f32_16x16x32_bf16 a[68:71],a[184:187],v[184:187],a[68:71]
ds_read_b64_tr_b16 a[158:159],v42 offset:35840
v_mfma_f32_16x16x32_bf16 a[68:71],a[180:183],v[172:175],a[68:71]
ds_read_b64_tr_b16 a[160:161],v44
v_mfma_f32_16x16x32_bf16 a[68:71],a[176:179],v[168:171],a[68:71]
ds_read_b64_tr_b16 a[162:163],v44 offset:5120
ds_read_b64_tr_b16 a[164:165],v44 offset:10240
v_mfma_f32_16x16x32_bf16 a[72:75],a[176:179],v[200:203],a[72:75]
ds_read_b64_tr_b16 a[166:167],v44 offset:15360
v_mfma_f32_16x16x32_bf16 a[72:75],a[180:183],v[204:207],a[72:75]
v_accvgpr_read_b32 v128,a64
v_accvgpr_read_b32 v129,a65
v_mfma_f32_16x16x32_bf16 a[72:75],a[184:187],v[216:219],a[72:75]
v_cvt_pk_bf16_f32 v128,v128,v129
v_accvgpr_read_b32 v130,a66
v_mfma_f32_16x16x32_bf16 a[72:75],a[188:191],v[220:223],a[72:75]
v_accvgpr_read_b32 v131,a67
v_cvt_pk_bf16_f32 v129,v130,v131
v_mfma_f32_16x16x32_bf16 a[80:83],a[96:99],v[64:67],a[80:83]
ds_read_b64_tr_b16 a[168:169],v44 offset:20480
ds_read_b64_tr_b16 a[170:171],v44 offset:25600
v_mfma_f32_16x16x32_bf16 a[80:83],a[100:103],v[68:71],a[80:83]
ds_read_b64_tr_b16 a[172:173],v44 offset:30720
v_accvgpr_read_b32 v132,a68
v_mfma_f32_16x16x32_bf16 a[80:83],a[104:107],v[72:75],a[80:83]
ds_read_b64_tr_b16 a[174:175],v44 offset:35840
v_accvgpr_read_b32 v133,a69
v_mfma_f32_16x16x32_bf16 a[80:83],a[108:111],v[76:79],a[80:83]
ds_read_b64_tr_b16 a[176:177],v2 offset:32
v_cvt_pk_bf16_f32 v132,v132,v133
v_mfma_f32_16x16x32_bf16 a[84:87],a[108:111],v[92:95],a[84:87]
ds_read_b64_tr_b16 a[178:179],v2 offset:5152
v_accvgpr_read_b32 v134,a70
v_mfma_f32_16x16x32_bf16 a[84:87],a[104:107],v[88:91],a[84:87]
v_accvgpr_read_b32 v144,a72
v_accvgpr_read_b32 v145,a73
v_mfma_f32_16x16x32_bf16 a[84:87],a[100:103],v[84:87],a[84:87]
v_accvgpr_read_b32 v135,a71
v_cvt_pk_bf16_f32 v144,v144,v145
v_mfma_f32_16x16x32_bf16 a[84:87],a[96:99],v[80:83],a[84:87]
v_accvgpr_read_b32 v146,a74
v_accvgpr_read_b32 v147,a75
v_mfma_f32_16x16x32_bf16 a[88:91],a[96:99],v[96:99],a[88:91]
ds_read_b64_tr_b16 a[180:181],v2 offset:10272
ds_read_b64_tr_b16 a[182:183],v2 offset:15392
v_mfma_f32_16x16x32_bf16 a[88:91],a[100:103],v[100:103],a[88:91]
v_cvt_pk_bf16_f32 v133,v134,v135
v_cvt_pk_bf16_f32 v145,v146,v147
v_mfma_f32_16x16x32_bf16 a[88:91],a[104:107],v[104:107],a[88:91]
ds_read_b64_tr_b16 a[184:185],v2 offset:20512
ds_read_b64_tr_b16 a[186:187],v2 offset:25632
v_mfma_f32_16x16x32_bf16 a[88:91],a[108:111],v[108:111],a[88:91]
ds_read_b64_tr_b16 a[188:189],v2 offset:30752
v_mfma_f32_16x16x32_bf16 a[80:83],a[112:115],v[136:139],a[80:83]
ds_read_b64_tr_b16 a[190:191],v2 offset:35872
v_mfma_f32_16x16x32_bf16 a[80:83],a[116:119],v[140:143],a[80:83]
ds_read_b64_tr_b16 a[96:97],v43 offset:32
v_mfma_f32_16x16x32_bf16 a[80:83],a[120:123],v[152:155],a[80:83]
ds_read_b64_tr_b16 a[98:99],v43 offset:5152
ds_read_b64_tr_b16 a[100:101],v43 offset:10272
v_mfma_f32_16x16x32_bf16 a[80:83],a[124:127],v[156:159],a[80:83]
ds_read_b64_tr_b16 a[102:103],v43 offset:15392
v_mfma_f32_16x16x32_bf16 a[84:87],a[124:127],v[188:191],a[84:87]
ds_read_b64_tr_b16 a[104:105],v43 offset:20512
v_mfma_f32_16x16x32_bf16 a[84:87],a[120:123],v[184:187],a[84:87]
ds_read_b64_tr_b16 a[106:107],v43 offset:25632
v_mfma_f32_16x16x32_bf16 a[84:87],a[116:119],v[172:175],a[84:87]
ds_read_b64_tr_b16 a[108:109],v43 offset:30752
ds_read_b64_tr_b16 a[110:111],v43 offset:35872
v_mfma_f32_16x16x32_bf16 a[84:87],a[112:115],v[168:171],a[84:87]
v_mfma_f32_16x16x32_bf16 a[88:91],a[112:115],v[200:203],a[88:91]
ds_read_b64_tr_b16 a[112:113],v45 offset:32
ds_read_b64_tr_b16 a[114:115],v45 offset:5152
v_mfma_f32_16x16x32_bf16 a[88:91],a[116:119],v[204:207],a[88:91]
v_accvgpr_read_b32 v130,a80
v_accvgpr_read_b32 v131,a81
v_mfma_f32_16x16x32_bf16 a[88:91],a[120:123],v[216:219],a[88:91]
v_accvgpr_read_b32 v16,a82
v_accvgpr_read_b32 v17,a83
v_mfma_f32_16x16x32_bf16 a[88:91],a[124:127],v[220:223],a[88:91]
v_cvt_pk_bf16_f32 v130,v130,v131
v_cvt_pk_bf16_f32 v131,v16,v17
v_mfma_f32_16x16x32_bf16 a[204:207],a[128:131],v[148:151],a[204:207]
buffer_store_dwordx4 v[128:131],v25,s[32:35],s84 offen sc1
v_mfma_f32_16x16x32_bf16 a[204:207],a[132:135],v[180:183],a[204:207]
s_waitcnt lgkmcnt(4)
v_accvgpr_read_b32 v134,a84
v_accvgpr_read_b32 v135,a85
v_mfma_f32_16x16x32_bf16 a[204:207],a[136:139],v[212:215],a[204:207]
s_lshl_b32 s95,s42,2
s_waitcnt vmcnt(1)
v_cmp_ne_u32 s[90:91],s68,v4
v_and_b32 v4,-2,v5
v_cmp_ne_u32 s[92:93],s69,v4
s_nop 0
s_or_b64 s[90:91],s[90:91],s[92:93]
s_cbranch_scc1 L_fr10_%=
L_fo10_%=:
v_lshlrev_b32 v19,13,v5
v_and_b32 v19,8192,v19
s_nop 0
v_readlane_b32 s90,v19,2
v_readlane_b32 s91,v19,1
v_readlane_b32 s92,v19,0
s_nop 1
s_or_b32 s76,s76,s90
s_or_b32 s77,s77,s91
s_or_b32 s78,s78,s92
v_mfma_f32_16x16x32_bf16 a[204:207],a[140:143],v[244:247],a[204:207]
s_add_u32 s95,s95,s40
s_lshl_b32 s95,s95,4
v_bfe_u32 v19,s55,13,1
v_mov_b32 v18,s68
v_or_b32 v19,s69,v19
v_mfma_f32_16x16x32_bf16 a[204:207],a[144:147],v[112:115],a[204:207]
s_lshl_b32 s94,s71,2
s_mov_b64 exec,1
buffer_store_dwordx2 v[18:19],v6,s[64:67],s95 offen offset:2048 sc1
s_mov_b64 exec,-1
v_mfma_f32_16x16x32_bf16 a[204:207],a[148:151],v[116:119],a[204:207]
s_add_u32 s94,s94,s40
v_accvgpr_read_b32 v146,a88
v_accvgpr_read_b32 v147,a89
v_mfma_f32_16x16x32_bf16 a[204:207],a[152:155],v[120:123],a[204:207]
v_accvgpr_read_b32 v12,a86
v_accvgpr_read_b32 v13,a87
v_cvt_pk_bf16_f32 v134,v134,v135
v_accvgpr_read_b32 v14,a90
v_accvgpr_read_b32 v15,a91
v_cvt_pk_bf16_f32 v146,v146,v147
v_cvt_pk_bf16_f32 v135,v12,v13
v_cvt_pk_bf16_f32 v147,v14,v15
v_mfma_f32_16x16x32_bf16 a[204:207],a[156:159],v[124:127],a[204:207]
s_lshl_b32 s94,s94,4
ds_read_b64_tr_b16 a[116:117],v45 offset:10272
ds_read_b64_tr_b16 a[118:119],v45 offset:15392
v_mfma_f32_16x16x32_bf16 a[204:207],a[160:163],v[232:235],a[204:207]
buffer_store_dwordx4 v[132:135],v25,s[32:35],s85 offen sc1
v_mfma_f32_16x16x32_bf16 a[204:207],a[164:167],v[236:239],a[204:207]
ds_read_b64_tr_b16 a[120:121],v45 offset:20512
ds_read_b64_tr_b16 a[122:123],v45 offset:25632
v_mfma_f32_16x16x32_bf16 a[204:207],a[168:171],v[248:251],a[204:207]
ds_read_b64_tr_b16 a[124:125],v45 offset:30752
ds_read_b64_tr_b16 a[126:127],v45 offset:35872
v_mfma_f32_16x16x32_bf16 a[204:207],a[172:175],v[252:255],a[204:207]
ds_read_b64_tr_b16 a[128:129],v1 offset:64
ds_read_b64_tr_b16 a[130:131],v1 offset:5184
v_mfma_f32_16x16x32_bf16 a[220:223],a[176:179],v[148:151],a[220:223]
buffer_store_dwordx4 v[144:147],v25,s[32:35],s86 offen sc1
v_mfma_f32_16x16x32_bf16 a[220:223],a[180:183],v[180:183],a[220:223]
ds_read_b64_tr_b16 a[132:133],v1 offset:10304
ds_read_b64_tr_b16 a[134:135],v1 offset:15424
v_mfma_f32_16x16x32_bf16 a[220:223],a[184:187],v[212:215],a[220:223]
ds_read_b64_tr_b16 a[136:137],v1 offset:20544
ds_read_b64_tr_b16 a[138:139],v1 offset:25664
v_mfma_f32_16x16x32_bf16 a[220:223],a[188:191],v[244:247],a[220:223]
ds_read_b64_tr_b16 a[140:141],v1 offset:30784
ds_read_b64_tr_b16 a[142:143],v1 offset:35904
v_mfma_f32_16x16x32_bf16 a[220:223],a[96:99],v[112:115],a[220:223]
buffer_load_dwordx4 a[192:195],v6,s[32:35],s76 offen sc1
v_mfma_f32_16x16x32_bf16 a[220:223],a[100:103],v[116:119],a[220:223]
ds_read_b64_tr_b16 a[144:145],v42 offset:64
ds_read_b64_tr_b16 a[146:147],v42 offset:5184
v_mfma_f32_16x16x32_bf16 a[220:223],a[104:107],v[120:123],a[220:223]
s_waitcnt lgkmcnt(10)
ds_read_b64_tr_b16 a[148:149],v42 offset:10304
ds_read_b64_tr_b16 a[150:151],v42 offset:15424
v_mfma_f32_16x16x32_bf16 a[220:223],a[108:111],v[124:127],a[220:223]
ds_read_b64_tr_b16 a[152:153],v42 offset:20544
ds_read_b64_tr_b16 a[154:155],v42 offset:25664
v_mfma_f32_16x16x32_bf16 a[220:223],a[112:115],v[232:235],a[220:223]
buffer_load_dwordx4 a[196:199],v6,s[32:35],s77 offen sc1
v_mfma_f32_16x16x32_bf16 a[220:223],a[116:119],v[236:239],a[220:223]
ds_read_b64_tr_b16 a[156:157],v42 offset:30784
ds_read_b64_tr_b16 a[158:159],v42 offset:35904
v_mfma_f32_16x16x32_bf16 a[220:223],a[120:123],v[248:251],a[220:223]
)A"
R"A(ds_read_b64_tr_b16 a[160:161],v44 offset:64
ds_read_b64_tr_b16 a[162:163],v44 offset:5184
v_mfma_f32_16x16x32_bf16 a[220:223],a[124:127],v[252:255],a[220:223]
s_waitcnt lgkmcnt(10)
ds_read_b64_tr_b16 a[164:165],v44 offset:10304
ds_read_b64_tr_b16 a[166:167],v44 offset:15424
v_mfma_f32_16x16x32_bf16 a[236:239],a[128:131],v[148:151],a[236:239]
buffer_load_dwordx4 a[200:203],v6,s[32:35],s78 offen sc1
v_mfma_f32_16x16x32_bf16 a[236:239],a[132:135],v[180:183],a[236:239]
ds_read_b64_tr_b16 a[168:169],v44 offset:20544
ds_read_b64_tr_b16 a[170:171],v44 offset:25664
v_mfma_f32_16x16x32_bf16 a[236:239],a[136:139],v[212:215],a[236:239]
ds_read_b64_tr_b16 a[172:173],v44 offset:30784
ds_read_b64_tr_b16 a[174:175],v44 offset:35904
v_mfma_f32_16x16x32_bf16 a[236:239],a[140:143],v[244:247],a[236:239]
s_waitcnt lgkmcnt(10)
ds_read_b64_tr_b16 a[176:177],v2 offset:96
ds_read_b64_tr_b16 a[178:179],v2 offset:5216
v_mfma_f32_16x16x32_bf16 a[236:239],a[144:147],v[112:115],a[236:239]
buffer_load_dwordx4 a[224:227],v6,s[32:35],s76 offen offset:1024 sc1
ds_read_b64_tr_b16 a[180:181],v2 offset:10336
ds_read_b64_tr_b16 a[182:183],v2 offset:15456
v_mfma_f32_16x16x32_bf16 a[236:239],a[148:151],v[116:119],a[236:239]
ds_read_b64_tr_b16 a[184:185],v2 offset:20576
ds_read_b64_tr_b16 a[186:187],v2 offset:25696
v_mfma_f32_16x16x32_bf16 a[236:239],a[152:155],v[120:123],a[236:239]
s_waitcnt lgkmcnt(12)
ds_read_b64_tr_b16 a[188:189],v2 offset:30816
ds_read_b64_tr_b16 a[190:191],v2 offset:35936
v_mfma_f32_16x16x32_bf16 a[236:239],a[156:159],v[124:127],a[236:239]
ds_read_b64_tr_b16 a[96:97],v43 offset:96
ds_read_b64_tr_b16 a[98:99],v43 offset:5216
v_mfma_f32_16x16x32_bf16 a[236:239],a[160:163],v[232:235],a[236:239]
s_waitcnt lgkmcnt(14)
buffer_load_dwordx4 a[228:231],v6,s[32:35],s77 offen offset:1024 sc1
ds_read_b64_tr_b16 a[100:101],v43 offset:10336
ds_read_b64_tr_b16 a[102:103],v43 offset:15456
v_mfma_f32_16x16x32_bf16 a[236:239],a[164:167],v[236:239],a[236:239]
s_waitcnt lgkmcnt(14)
ds_read_b64_tr_b16 a[104:105],v43 offset:20576
ds_read_b64_tr_b16 a[106:107],v43 offset:25696
v_mfma_f32_16x16x32_bf16 a[236:239],a[168:171],v[248:251],a[236:239]
s_waitcnt lgkmcnt(14)
ds_read_b64_tr_b16 a[108:109],v43 offset:30816
ds_read_b64_tr_b16 a[110:111],v43 offset:35936
v_mfma_f32_16x16x32_bf16 a[236:239],a[172:175],v[252:255],a[236:239]
s_waitcnt lgkmcnt(14)
ds_read_b64_tr_b16 a[112:113],v45 offset:96
ds_read_b64_tr_b16 a[114:115],v45 offset:5216
v_mfma_f32_16x16x32_bf16 a[252:255],a[176:179],v[148:151],a[252:255]
s_waitcnt lgkmcnt(14)
buffer_load_dwordx4 a[232:235],v6,s[32:35],s78 offen offset:1024 sc1
ds_read_b64_tr_b16 a[116:117],v45 offset:10336
ds_read_b64_tr_b16 a[118:119],v45 offset:15456
v_mfma_f32_16x16x32_bf16 a[252:255],a[180:183],v[180:183],a[252:255]
s_waitcnt lgkmcnt(14)
ds_read_b64_tr_b16 a[120:121],v45 offset:20576
ds_read_b64_tr_b16 a[122:123],v45 offset:25696
v_mfma_f32_16x16x32_bf16 a[252:255],a[184:187],v[212:215],a[252:255]
s_waitcnt lgkmcnt(14)
ds_read_b64_tr_b16 a[124:125],v45 offset:30816
ds_read_b64_tr_b16 a[126:127],v45 offset:35936
v_mfma_f32_16x16x32_bf16 a[252:255],a[188:191],v[244:247],a[252:255]
s_waitcnt lgkmcnt(14)
ds_read_b64_tr_b16 a[128:129],v1 offset:128
ds_read_b64_tr_b16 a[130:131],v1 offset:5248
v_bfe_u32 v24,v6,4,2
v_min_u32 v24,2,v24
v_mfma_f32_16x16x32_bf16 a[252:255],a[96:99],v[112:115],a[252:255]
s_waitcnt lgkmcnt(14)
ds_read_b64_tr_b16 a[132:133],v1 offset:10368
ds_read_b64_tr_b16 a[134:135],v1 offset:15488
v_add_u32 v24,s70,v24
v_add_u32 v24,1,v24
v_mfma_f32_16x16x32_bf16 a[252:255],a[100:103],v[116:119],a[252:255]
s_waitcnt lgkmcnt(14)
ds_read_b64_tr_b16 a[136:137],v1 offset:20608
ds_read_b64_tr_b16 a[138:139],v1 offset:25728
v_and_b32 v24,3,v24
v_lshlrev_b32 v24,8,v24
v_mfma_f32_16x16x32_bf16 a[252:255],a[104:107],v[120:123],a[252:255]
s_waitcnt lgkmcnt(14)
buffer_load_dwordx2 v[4:5],v24,s[64:67],s94 offen offset:2048 sc1
ds_read_b64_tr_b16 a[140:141],v1 offset:30848
ds_read_b64_tr_b16 a[142:143],v1 offset:35968
v_mfma_f32_16x16x32_bf16 a[252:255],a[108:111],v[124:127],a[252:255]
s_waitcnt lgkmcnt(14)
ds_read_b64_tr_b16 a[144:145],v42 offset:128
ds_read_b64_tr_b16 a[146:147],v42 offset:5248
v_mfma_f32_16x16x32_bf16 a[252:255],a[112:115],v[232:235],a[252:255]
s_waitcnt lgkmcnt(14)
ds_read_b64_tr_b16 a[148:149],v42 offset:10368
ds_read_b64_tr_b16 a[150:151],v42 offset:15488
v_mfma_f32_16x16x32_bf16 a[252:255],a[116:119],v[236:239],a[252:255]
s_lshl_b32 s95,s42,2
ds_read_b64_tr_b16 a[152:153],v42 offset:20608
ds_read_b64_tr_b16 a[154:155],v42 offset:25728
s_waitcnt lgkmcnt(15)
v_mfma_f32_16x16x32_bf16 a[252:255],a[120:123],v[248:251],a[252:255]
s_add_u32 s95,s95,s40
s_lshl_b32 s95,s95,4
ds_read_b64_tr_b16 a[156:157],v42 offset:30848
ds_read_b64_tr_b16 a[158:159],v42 offset:35968
v_bfe_u32 v19,s55,13,1
v_mov_b32 v18,s68
v_or_b32 v19,s69,v19
s_waitcnt lgkmcnt(15)
v_mfma_f32_16x16x32_bf16 a[252:255],a[124:127],v[252:255],a[252:255]
s_waitcnt vmcnt(7)
s_mov_b64 exec,1
buffer_store_dwordx2 v[18:19],v6,s[64:67],s95 offen offset:3072 sc1
s_mov_b64 exec,-1
ds_read_b64_tr_b16 a[160:161],v44 offset:128
ds_read_b64_tr_b16 a[162:163],v44 offset:5248
s_waitcnt lgkmcnt(15)
v_mfma_f32_16x16x32_bf16 a[12:15],a[128:131],v[148:151],a[12:15]
s_waitcnt lgkmcnt(14)
ds_read_b64_tr_b16 a[164:165],v44 offset:10368
ds_read_b64_tr_b16 a[166:167],v44 offset:15488
v_mfma_f32_16x16x32_bf16 a[12:15],a[132:135],v[180:183],a[12:15]
s_waitcnt lgkmcnt(14)
ds_read_b64_tr_b16 a[168:169],v44 offset:20608
ds_read_b64_tr_b16 a[170:171],v44 offset:25728
v_mfma_f32_16x16x32_bf16 a[12:15],a[136:139],v[212:215],a[12:15]
s_waitcnt lgkmcnt(14)
ds_read_b64_tr_b16 a[172:173],v44 offset:30848
ds_read_b64_tr_b16 a[174:175],v44 offset:35968
v_mfma_f32_16x16x32_bf16 a[12:15],a[140:143],v[244:247],a[12:15]
s_waitcnt lgkmcnt(14)
ds_read_b64_tr_b16 a[176:177],v2 offset:160
ds_read_b64_tr_b16 a[178:179],v2 offset:5280
v_mfma_f32_16x16x32_bf16 a[12:15],a[144:147],v[112:115],a[12:15]
s_waitcnt lgkmcnt(14)
ds_read_b64_tr_b16 a[180:181],v2 offset:10400
ds_read_b64_tr_b16 a[182:183],v2 offset:15520
v_mfma_f32_16x16x32_bf16 a[12:15],a[148:151],v[116:119],a[12:15]
s_waitcnt lgkmcnt(14)
ds_read_b64_tr_b16 a[184:185],v2 offset:20640
ds_read_b64_tr_b16 a[186:187],v2 offset:25760
v_mfma_f32_16x16x32_bf16 a[12:15],a[152:155],v[120:123],a[12:15]
s_waitcnt lgkmcnt(14)
ds_read_b64_tr_b16 a[188:189],v2 offset:30880
ds_read_b64_tr_b16 a[190:191],v2 offset:36000
v_mfma_f32_16x16x32_bf16 a[12:15],a[156:159],v[124:127],a[12:15]
s_waitcnt lgkmcnt(14)
ds_read_b64_tr_b16 a[96:97],v43 offset:160
ds_read_b64_tr_b16 a[98:99],v43 offset:5280
v_mfma_f32_16x16x32_bf16 a[12:15],a[160:163],v[232:235],a[12:15]
s_waitcnt lgkmcnt(14)
ds_read_b64_tr_b16 a[100:101],v43 offset:10400
ds_read_b64_tr_b16 a[102:103],v43 offset:15520
v_mfma_f32_16x16x32_bf16 a[12:15],a[164:167],v[236:239],a[12:15]
s_waitcnt lgkmcnt(14)
ds_read_b64_tr_b16 a[104:105],v43 offset:20640
ds_read_b64_tr_b16 a[106:107],v43 offset:25760
v_mfma_f32_16x16x32_bf16 a[12:15],a[168:171],v[248:251],a[12:15]
s_waitcnt lgkmcnt(14)
ds_read_b64_tr_b16 a[108:109],v43 offset:30880
ds_read_b64_tr_b16 a[110:111],v43 offset:36000
v_mfma_f32_16x16x32_bf16 a[12:15],a[172:175],v[252:255],a[12:15]
s_waitcnt lgkmcnt(14)
ds_read_b64_tr_b16 a[112:113],v45 offset:160
ds_read_b64_tr_b16 a[114:115],v45 offset:5280
v_mfma_f32_16x16x32_bf16 a[28:31],a[176:179],v[148:151],a[28:31]
s_waitcnt lgkmcnt(14)
ds_read_b64_tr_b16 a[116:117],v45 offset:10400
ds_read_b64_tr_b16 a[118:119],v45 offset:15520
v_mfma_f32_16x16x32_bf16 a[28:31],a[180:183],v[180:183],a[28:31]
s_waitcnt lgkmcnt(14)
ds_read_b64_tr_b16 a[120:121],v45 offset:20640
ds_read_b64_tr_b16 a[122:123],v45 offset:25760
v_mfma_f32_16x16x32_bf16 a[28:31],a[184:187],v[212:215],a[28:31]
s_waitcnt lgkmcnt(14)
ds_read_b64_tr_b16 a[124:125],v45 offset:30880
ds_read_b64_tr_b16 a[126:127],v45 offset:36000
v_mfma_f32_16x16x32_bf16 a[28:31],a[188:191],v[244:247],a[28:31]
s_waitcnt lgkmcnt(14)
ds_read_b64_tr_b16 a[128:129],v1 offset:192
ds_read_b64_tr_b16 a[130:131],v1 offset:5312
v_mfma_f32_16x16x32_bf16 a[28:31],a[96:99],v[112:115],a[28:31]
s_waitcnt lgkmcnt(14)
ds_read_b64_tr_b16 a[132:133],v1 offset:10432
ds_read_b64_tr_b16 a[134:135],v1 offset:15552
v_mfma_f32_16x16x32_bf16 a[28:31],a[100:103],v[116:119],a[28:31]
s_waitcnt lgkmcnt(14)
ds_read_b64_tr_b16 a[136:137],v1 offset:20672
ds_read_b64_tr_b16 a[138:139],v1 offset:25792
v_mfma_f32_16x16x32_bf16 a[28:31],a[104:107],v[120:123],a[28:31]
s_waitcnt lgkmcnt(14)
ds_read_b64_tr_b16 a[140:141],v1 offset:30912
ds_read_b64_tr_b16 a[142:143],v1 offset:36032
v_mfma_f32_16x16x32_bf16 a[28:31],a[108:111],v[124:127],a[28:31]
s_waitcnt lgkmcnt(14)
ds_read_b64_tr_b16 a[144:145],v42 offset:192
ds_read_b64_tr_b16 a[146:147],v42 offset:5312
v_mfma_f32_16x16x32_bf16 a[28:31],a[112:115],v[232:235],a[28:31]
s_waitcnt lgkmcnt(14)
ds_read_b64_tr_b16 a[148:149],v42 offset:10432
ds_read_b64_tr_b16 a[150:151],v42 offset:15552
v_mfma_f32_16x16x32_bf16 a[28:31],a[116:119],v[236:239],a[28:31]
s_waitcnt lgkmcnt(14)
ds_read_b64_tr_b16 a[152:153],v42 offset:20672
ds_read_b64_tr_b16 a[154:155],v42 offset:25792
v_mfma_f32_16x16x32_bf16 a[28:31],a[120:123],v[248:251],a[28:31]
s_waitcnt lgkmcnt(14)
ds_read_b64_tr_b16 a[156:157],v42 offset:30912
ds_read_b64_tr_b16 a[158:159],v42 offset:36032
v_mfma_f32_16x16x32_bf16 a[28:31],a[124:127],v[252:255],a[28:31]
s_waitcnt lgkmcnt(14)
ds_read_b64_tr_b16 a[160:161],v44 offset:192
ds_read_b64_tr_b16 a[162:163],v44 offset:5312
v_mfma_f32_16x16x32_bf16 a[44:47],a[128:131],v[148:151],a[44:47]
s_waitcnt lgkmcnt(14)
ds_read_b64_tr_b16 a[164:165],v44 offset:10432
ds_read_b64_tr_b16 a[166:167],v44 offset:15552
v_mfma_f32_16x16x32_bf16 a[44:47],a[132:135],v[180:183],a[44:47]
s_waitcnt lgkmcnt(14)
ds_read_b64_tr_b16 a[168:169],v44 offset:20672
ds_read_b64_tr_b16 a[170:171],v44 offset:25792
v_mfma_f32_16x16x32_bf16 a[44:47],a[136:139],v[212:215],a[44:47]
ds_read_b64_tr_b16 a[172:173],v44 offset:30912
ds_read_b64_tr_b16 a[174:175],v44 offset:36032
s_waitcnt vmcnt(1)
v_cmp_ne_u32 s[90:91],s68,v4
v_and_b32 v4,-2,v5
v_cmp_ne_u32 s[92:93],s69,v4
s_nop 0
s_or_b64 s[90:91],s[90:91],s[92:93]
s_cbranch_scc1 L_fr11_%=
L_fo11_%=:
s_waitcnt lgkmcnt(15)
v_mfma_f32_16x16x32_bf16 a[44:47],a[140:143],v[244:247],a[44:47]
s_waitcnt lgkmcnt(14)
buffer_load_dwordx4 v[72:75],v6,s[32:35],s76 offen offset:2048 sc1
ds_read_b64_tr_b16 a[176:177],v2 offset:224
ds_read_b64_tr_b16 a[178:179],v2 offset:5344
v_mfma_f32_16x16x32_bf16 a[44:47],a[144:147],v[112:115],a[44:47]
s_lshl_b32 s94,s71,2
ds_read_b64_tr_b16 a[180:181],v2 offset:10464
ds_read_b64_tr_b16 a[182:183],v2 offset:15584
s_waitcnt lgkmcnt(15)
v_mfma_f32_16x16x32_bf16 a[44:47],a[148:151],v[116:119],a[44:47]
s_add_u32 s94,s94,s40
ds_read_b64_tr_b16 a[184:185],v2 offset:20704
ds_read_b64_tr_b16 a[186:187],v2 offset:25824
s_waitcnt lgkmcnt(15)
v_mfma_f32_16x16x32_bf16 a[44:47],a[152:155],v[120:123],a[44:47]
s_lshl_b32 s94,s94,4
ds_read_b64_tr_b16 a[188:189],v2 offset:30944
ds_read_b64_tr_b16 a[190:191],v2 offset:36064
s_waitcnt lgkmcnt(15)
v_mfma_f32_16x16x32_bf16 a[44:47],a[156:159],v[124:127],a[44:47]
s_waitcnt lgkmcnt(14)
buffer_load_dwordx4 v[88:91],v6,s[32:35],s77 offen offset:2048 sc1
ds_read_b64_tr_b16 a[96:97],v43 offset:224
ds_read_b64_tr_b16 a[98:99],v43 offset:5344
v_mfma_f32_16x16x32_bf16 a[44:47],a[160:163],v[232:235],a[44:47]
s_waitcnt lgkmcnt(14)
ds_read_b64_tr_b16 a[100:101],v43 offset:10464
ds_read_b64_tr_b16 a[102:103],v43 offset:15584
v_mfma_f32_16x16x32_bf16 a[44:47],a[164:167],v[236:239],a[44:47]
s_waitcnt lgkmcnt(14)
ds_read_b64_tr_b16 a[104:105],v43 offset:20704
ds_read_b64_tr_b16 a[106:107],v43 offset:25824
v_mfma_f32_16x16x32_bf16 a[44:47],a[168:171],v[248:251],a[44:47]
s_waitcnt lgkmcnt(14)
ds_read_b64_tr_b16 a[108:109],v43 offset:30944
ds_read_b64_tr_b16 a[110:111],v43 offset:36064
v_mfma_f32_16x16x32_bf16 a[44:47],a[172:175],v[252:255],a[44:47]
s_waitcnt lgkmcnt(14)
buffer_load_dwordx4 v[104:107],v6,s[32:35],s78 offen offset:2048 sc1
ds_read_b64_tr_b16 a[112:113],v45 offset:224
)A"
R"A(ds_read_b64_tr_b16 a[114:115],v45 offset:5344
v_mfma_f32_16x16x32_bf16 a[60:63],a[176:179],v[148:151],a[60:63]
s_waitcnt lgkmcnt(14)
ds_read_b64_tr_b16 a[116:117],v45 offset:10464
ds_read_b64_tr_b16 a[118:119],v45 offset:15584
v_mfma_f32_16x16x32_bf16 a[60:63],a[180:183],v[180:183],a[60:63]
s_waitcnt lgkmcnt(14)
ds_read_b64_tr_b16 a[120:121],v45 offset:20704
ds_read_b64_tr_b16 a[122:123],v45 offset:25824
v_mfma_f32_16x16x32_bf16 a[60:63],a[184:187],v[212:215],a[60:63]
s_waitcnt lgkmcnt(14)
ds_read_b64_tr_b16 a[124:125],v45 offset:30944
ds_read_b64_tr_b16 a[126:127],v45 offset:36064
v_mfma_f32_16x16x32_bf16 a[60:63],a[188:191],v[244:247],a[60:63]
s_waitcnt lgkmcnt(14)
buffer_load_dwordx4 v[76:79],v6,s[32:35],s76 offen offset:3072 sc1
v_mfma_f32_16x16x32_bf16 a[60:63],a[96:99],v[112:115],a[60:63]
s_waitcnt lgkmcnt(12)
ds_read_b64_tr_b16 a[128:129],v1 offset:256
ds_read_b64_tr_b16 a[130:131],v1 offset:5376
v_mfma_f32_16x16x32_bf16 a[60:63],a[100:103],v[116:119],a[60:63]
s_waitcnt lgkmcnt(12)
ds_read_b64_tr_b16 a[132:133],v1 offset:10496
ds_read_b64_tr_b16 a[134:135],v1 offset:15616
v_mfma_f32_16x16x32_bf16 a[60:63],a[104:107],v[120:123],a[60:63]
s_waitcnt lgkmcnt(12)
ds_read_b64_tr_b16 a[136:137],v1 offset:20736
ds_read_b64_tr_b16 a[138:139],v1 offset:25856
v_mfma_f32_16x16x32_bf16 a[60:63],a[108:111],v[124:127],a[60:63]
s_waitcnt lgkmcnt(12)
buffer_load_dwordx4 v[92:95],v6,s[32:35],s77 offen offset:3072 sc1
v_mfma_f32_16x16x32_bf16 a[60:63],a[112:115],v[232:235],a[60:63]
s_waitcnt lgkmcnt(10)
ds_read_b64_tr_b16 a[140:141],v1 offset:30976
ds_read_b64_tr_b16 a[142:143],v1 offset:36096
v_mfma_f32_16x16x32_bf16 a[60:63],a[116:119],v[236:239],a[60:63]
s_waitcnt lgkmcnt(10)
ds_read_b64_tr_b16 a[144:145],v42 offset:256
ds_read_b64_tr_b16 a[146:147],v42 offset:5376
v_mfma_f32_16x16x32_bf16 a[60:63],a[120:123],v[248:251],a[60:63]
s_waitcnt lgkmcnt(10)
ds_read_b64_tr_b16 a[148:149],v42 offset:10496
ds_read_b64_tr_b16 a[150:151],v42 offset:15616
v_mfma_f32_16x16x32_bf16 a[60:63],a[124:127],v[252:255],a[60:63]
buffer_load_dwordx4 v[108:111],v6,s[32:35],s78 offen offset:3072 sc1
v_mfma_f32_16x16x32_bf16 a[204:207],v[34:37],a[192:195],a[204:207]
ds_read_b64_tr_b16 a[152:153],v42 offset:20736
ds_read_b64_tr_b16 a[154:155],v42 offset:25856
v_mfma_f32_16x16x32_bf16 a[204:207],v[34:37],a[196:199],a[204:207]
ds_read_b64_tr_b16 a[156:157],v42 offset:30976
ds_read_b64_tr_b16 a[158:159],v42 offset:36096
v_mfma_f32_16x16x32_bf16 a[204:207],v[34:37],a[200:203],a[204:207]
ds_read_b64_tr_b16 a[160:161],v44 offset:256
ds_read_b64_tr_b16 a[162:163],v44 offset:5376
v_mfma_f32_16x16x32_bf16 a[220:223],v[32:35],a[192:195],a[220:223]
ds_read_b64_tr_b16 a[164:165],v44 offset:10496
ds_read_b64_tr_b16 a[166:167],v44 offset:15616
v_mfma_f32_16x16x32_bf16 a[220:223],v[32:35],a[196:199],a[220:223]
ds_read_b64_tr_b16 a[168:169],v44 offset:20736
ds_read_b64_tr_b16 a[170:171],v44 offset:25856
v_mfma_f32_16x16x32_bf16 a[220:223],v[32:35],a[200:203],a[220:223]
s_waitcnt lgkmcnt(12)
ds_read_b64_tr_b16 a[172:173],v44 offset:30976
ds_read_b64_tr_b16 a[174:175],v44 offset:36096
v_mfma_f32_16x16x32_bf16 a[76:79],a[128:131],v[148:151],a[76:79]
v_bfe_u32 v24,v6,4,2
v_min_u32 v24,2,v24
v_mfma_f32_16x16x32_bf16 a[76:79],a[132:135],v[180:183],a[76:79]
v_add_u32 v24,s70,v24
v_add_u32 v24,1,v24
v_mfma_f32_16x16x32_bf16 a[76:79],a[136:139],v[212:215],a[76:79]
v_accvgpr_read_b32 v160,a204
v_accvgpr_read_b32 v161,a205
v_mfma_f32_16x16x32_bf16 a[76:79],a[140:143],v[244:247],a[76:79]
v_cvt_pk_bf16_f32 v160,v160,v161
v_accvgpr_read_b32 v162,a206
v_mfma_f32_16x16x32_bf16 a[76:79],a[144:147],v[112:115],a[76:79]
s_waitcnt lgkmcnt(6)
v_accvgpr_read_b32 v163,a207
v_cvt_pk_bf16_f32 v161,v162,v163
v_mfma_f32_16x16x32_bf16 a[76:79],a[148:151],v[116:119],a[76:79]
v_accvgpr_read_b32 v162,a220
v_accvgpr_read_b32 v163,a221
v_mfma_f32_16x16x32_bf16 a[76:79],a[152:155],v[120:123],a[76:79]
v_accvgpr_read_b32 v16,a222
v_accvgpr_read_b32 v17,a223
v_mfma_f32_16x16x32_bf16 a[76:79],a[156:159],v[124:127],a[76:79]
v_cvt_pk_bf16_f32 v162,v162,v163
v_cvt_pk_bf16_f32 v163,v16,v17
v_mfma_f32_16x16x32_bf16 a[76:79],a[160:163],v[232:235],a[76:79]
s_waitcnt lgkmcnt(0)
ds_read_b64_tr_b16 a[176:177],v2 offset:288
ds_read_b64_tr_b16 a[178:179],v2 offset:5408
v_mfma_f32_16x16x32_bf16 a[76:79],a[164:167],v[236:239],a[76:79]
v_permlane16_swap_b32 v160,v162
v_mfma_f32_16x16x32_bf16 a[76:79],a[168:171],v[248:251],a[76:79]
ds_read_b64_tr_b16 a[180:181],v2 offset:10528
ds_read_b64_tr_b16 a[182:183],v2 offset:15648
v_and_b32 v24,3,v24
v_lshlrev_b32 v24,8,v24
v_mfma_f32_16x16x32_bf16 a[76:79],a[172:175],v[252:255],a[76:79]
buffer_load_dwordx2 v[4:5],v24,s[64:67],s94 offen offset:3072 sc1
ds_read_b64_tr_b16 a[184:185],v2 offset:20768
ds_read_b64_tr_b16 a[186:187],v2 offset:25888
v_mfma_f32_16x16x32_bf16 a[236:239],v[34:37],a[224:227],a[236:239]
ds_read_b64_tr_b16 a[188:189],v2 offset:31008
ds_read_b64_tr_b16 a[190:191],v2 offset:36128
v_mfma_f32_16x16x32_bf16 a[236:239],v[34:37],a[228:231],a[236:239]
ds_read_b64_tr_b16 a[96:97],v43 offset:288
ds_read_b64_tr_b16 a[98:99],v43 offset:5408
v_mfma_f32_16x16x32_bf16 a[236:239],v[34:37],a[232:235],a[236:239]
ds_read_b64_tr_b16 a[100:101],v43 offset:10528
ds_read_b64_tr_b16 a[102:103],v43 offset:15648
v_mfma_f32_16x16x32_bf16 a[252:255],v[32:35],a[224:227],a[252:255]
ds_read_b64_tr_b16 a[104:105],v43 offset:20768
ds_read_b64_tr_b16 a[106:107],v43 offset:25888
v_mfma_f32_16x16x32_bf16 a[252:255],v[32:35],a[228:231],a[252:255]
ds_read_b64_tr_b16 a[108:109],v43 offset:31008
ds_read_b64_tr_b16 a[110:111],v43 offset:36128
v_mfma_f32_16x16x32_bf16 a[252:255],v[32:35],a[232:235],a[252:255]
s_waitcnt lgkmcnt(14)
ds_read_b64_tr_b16 a[112:113],v45 offset:288
ds_read_b64_tr_b16 a[114:115],v45 offset:5408
v_mfma_f32_16x16x32_bf16 a[92:95],a[176:179],v[148:151],a[92:95]
s_waitcnt lgkmcnt(14)
ds_read_b64_tr_b16 a[116:117],v45 offset:10528
ds_read_b64_tr_b16 a[118:119],v45 offset:15648
v_mfma_f32_16x16x32_bf16 a[92:95],a[180:183],v[180:183],a[92:95]
s_waitcnt lgkmcnt(14)
ds_read_b64_tr_b16 a[120:121],v45 offset:20768
ds_read_b64_tr_b16 a[122:123],v45 offset:25888
v_mfma_f32_16x16x32_bf16 a[92:95],a[184:187],v[212:215],a[92:95]
s_waitcnt lgkmcnt(14)
ds_read_b64_tr_b16 a[124:125],v45 offset:31008
ds_read_b64_tr_b16 a[126:127],v45 offset:36128
v_mfma_f32_16x16x32_bf16 a[92:95],a[188:191],v[244:247],a[92:95]
s_waitcnt lgkmcnt(14)
v_permlane16_swap_b32 v161,v163
v_mfma_f32_16x16x32_bf16 a[92:95],a[96:99],v[112:115],a[92:95]
s_cmp_lg_u32 %2,0
s_cbranch_scc1 L_ea0_%=
buffer_store_dwordx4 v[160:163],v40,s[20:23],s43 offen
s_branch L_eb0_%=
L_ea0_%=:
buffer_store_dwordx4 v[160:163],v40,s[20:23],s43 offen sc1
L_eb0_%=:
s_waitcnt lgkmcnt(12)
v_mfma_f32_16x16x32_bf16 a[92:95],a[100:103],v[116:119],a[92:95]
s_waitcnt lgkmcnt(10)
v_accvgpr_read_b32 v164,a236
v_accvgpr_read_b32 v165,a237
v_mfma_f32_16x16x32_bf16 a[92:95],a[104:107],v[120:123],a[92:95]
s_waitcnt lgkmcnt(8)
v_cvt_pk_bf16_f32 v164,v164,v165
v_accvgpr_read_b32 v166,a238
v_mfma_f32_16x16x32_bf16 a[92:95],a[108:111],v[124:127],a[92:95]
s_waitcnt lgkmcnt(6)
v_accvgpr_read_b32 v167,a239
v_cvt_pk_bf16_f32 v165,v166,v167
v_mfma_f32_16x16x32_bf16 a[92:95],a[112:115],v[232:235],a[92:95]
s_waitcnt lgkmcnt(4)
v_accvgpr_read_b32 v166,a252
v_accvgpr_read_b32 v167,a253
v_mfma_f32_16x16x32_bf16 a[92:95],a[116:119],v[236:239],a[92:95]
s_waitcnt lgkmcnt(2)
v_accvgpr_read_b32 v12,a254
v_accvgpr_read_b32 v13,a255
v_cvt_pk_bf16_f32 v166,v166,v167
v_cvt_pk_bf16_f32 v167,v12,v13
v_mfma_f32_16x16x32_bf16 a[92:95],a[120:123],v[248:251],a[92:95]
s_waitcnt lgkmcnt(0)
v_mfma_f32_16x16x32_bf16 a[92:95],a[124:127],v[252:255],a[92:95]
v_permlane16_swap_b32 v164,v166
v_permlane16_swap_b32 v165,v167
s_nop 3
s_cmp_lg_u32 %2,0
s_cbranch_scc1 L_ea1_%=
buffer_store_dwordx4 v[164:167],v40,s[20:23],s43 offen offset:64
s_branch L_eb1_%=
L_ea1_%=:
buffer_store_dwordx4 v[164:167],v40,s[20:23],s43 offen offset:64 sc1
L_eb1_%=:
s_waitcnt vmcnt(2)
v_cmp_ne_u32 s[90:91],s68,v4
v_and_b32 v4,-2,v5
v_cmp_ne_u32 s[92:93],s69,v4
s_nop 0
s_or_b64 s[90:91],s[90:91],s[92:93]
s_cbranch_scc1 L_fr12_%=
L_fo12_%=:
buffer_load_dwordx4 v[136:139],v25,s[32:35],s76 offen sc1
buffer_load_dwordx4 v[168:171],v25,s[32:35],s77 offen sc1
buffer_load_dwordx4 v[200:203],v25,s[32:35],s78 offen sc1
s_cmp_lg_u32 %2,0
s_cbranch_scc1 L_dual_%=
v_mfma_f32_16x16x32_bf16 a[12:15],v[34:37],v[72:75],a[12:15]
v_mfma_f32_16x16x32_bf16 a[12:15],v[34:37],v[88:91],a[12:15]
v_mfma_f32_16x16x32_bf16 a[12:15],v[34:37],v[104:107],a[12:15]
v_mfma_f32_16x16x32_bf16 a[28:31],v[32:35],v[72:75],a[28:31]
v_mfma_f32_16x16x32_bf16 a[28:31],v[32:35],v[88:91],a[28:31]
v_mfma_f32_16x16x32_bf16 a[28:31],v[32:35],v[104:107],a[28:31]
v_mfma_f32_16x16x32_bf16 a[44:47],v[34:37],v[76:79],a[44:47]
v_mfma_f32_16x16x32_bf16 a[44:47],v[34:37],v[92:95],a[44:47]
v_mfma_f32_16x16x32_bf16 a[44:47],v[34:37],v[108:111],a[44:47]
s_nop 1
v_accvgpr_read_b32 v176,a12
v_accvgpr_read_b32 v177,a13
v_cvt_pk_bf16_f32 v176,v176,v177
v_accvgpr_read_b32 v178,a14
v_accvgpr_read_b32 v179,a15
v_cvt_pk_bf16_f32 v177,v178,v179
v_mfma_f32_16x16x32_bf16 a[60:63],v[32:35],v[76:79],a[60:63]
v_mfma_f32_16x16x32_bf16 a[60:63],v[32:35],v[92:95],a[60:63]
v_mfma_f32_16x16x32_bf16 a[60:63],v[32:35],v[108:111],a[60:63]
v_accvgpr_read_b32 v178,a28
v_accvgpr_read_b32 v179,a29
v_accvgpr_read_b32 v14,a30
v_accvgpr_read_b32 v15,a31
v_cvt_pk_bf16_f32 v178,v178,v179
v_cvt_pk_bf16_f32 v179,v14,v15
s_nop 3
v_permlane16_swap_b32 v176,v178
v_permlane16_swap_b32 v177,v179
buffer_store_dwordx4 v[176:179],v40,s[20:23],s43 offen offset:128
v_accvgpr_read_b32 v192,a44
v_accvgpr_read_b32 v193,a45
v_cvt_pk_bf16_f32 v192,v192,v193
v_accvgpr_read_b32 v194,a46
v_accvgpr_read_b32 v195,a47
v_cvt_pk_bf16_f32 v193,v194,v195
v_accvgpr_read_b32 v194,a60
v_accvgpr_read_b32 v195,a61
v_accvgpr_read_b32 v16,a62
v_accvgpr_read_b32 v17,a63
v_cvt_pk_bf16_f32 v194,v194,v195
s_nop 1
v_permlane16_swap_b32 v192,v194
v_cvt_pk_bf16_f32 v195,v16,v17
s_nop 1
v_permlane16_swap_b32 v193,v195
buffer_store_dwordx4 v[192:195],v40,s[20:23],s43 offen offset:192
s_waitcnt vmcnt(4)
v_mfma_f32_16x16x32_bf16 a[76:79],v[34:37],v[136:139],a[76:79]
s_waitcnt vmcnt(3)
v_mfma_f32_16x16x32_bf16 a[76:79],v[34:37],v[168:171],a[76:79]
s_waitcnt vmcnt(2)
v_mfma_f32_16x16x32_bf16 a[76:79],v[34:37],v[200:203],a[76:79]
v_mfma_f32_16x16x32_bf16 a[92:95],v[32:35],v[136:139],a[92:95]
v_mfma_f32_16x16x32_bf16 a[92:95],v[32:35],v[168:171],a[92:95]
v_mfma_f32_16x16x32_bf16 a[92:95],v[32:35],v[200:203],a[92:95]
s_nop 3
s_nop 0
v_accvgpr_read_b32 v196,a76
v_accvgpr_read_b32 v197,a77
v_cvt_pk_bf16_f32 v196,v196,v197
v_accvgpr_read_b32 v198,a78
v_accvgpr_read_b32 v199,a79
v_cvt_pk_bf16_f32 v197,v198,v199
v_accvgpr_read_b32 v198,a92
v_accvgpr_read_b32 v199,a93
v_accvgpr_read_b32 v12,a94
v_accvgpr_read_b32 v13,a95
v_cvt_pk_bf16_f32 v198,v198,v199
s_nop 1
v_permlane16_swap_b32 v196,v198
v_cvt_pk_bf16_f32 v199,v12,v13
s_nop 1
v_permlane16_swap_b32 v197,v199
buffer_store_dwordx4 v[196:199],v40,s[20:23],s43 offen offset:256
s_or_b32 s62,s42,s40
s_cmp_lg_u32 s62,0
s_cbranch_scc1 L_noep_%=
v_mov_b32 v12,s68
s_mov_b64 exec,1
buffer_store_dword v12,v25,s[64:67],0 offen sc1
s_mov_b64 exec,-1
L_noep_%=:
s_endpgm
L_dual_%=:
v_mfma_f32_16x16x32_bf16 a[12:15],v[34:37],v[72:75],a[12:15]
v_mfma_f32_16x16x32_bf16 a[12:15],v[34:37],v[88:91],a[12:15]
v_mfma_f32_16x16x32_bf16 a[12:15],v[34:37],v[104:107],a[12:15]
v_mfma_f32_16x16x32_bf16 a[28:31],v[32:35],v[72:75],a[28:31]
v_mfma_f32_16x16x32_bf16 a[28:31],v[32:35],v[88:91],a[28:31]
v_mfma_f32_16x16x32_bf16 a[28:31],v[32:35],v[104:107],a[28:31]
v_mfma_f32_16x16x32_bf16 a[44:47],v[34:37],v[76:79],a[44:47]
v_mfma_f32_16x16x32_bf16 a[44:47],v[34:37],v[92:95],a[44:47]
v_mfma_f32_16x16x32_bf16 a[44:47],v[34:37],v[108:111],a[44:47]
s_nop 1
v_accvgpr_read_b32 v176,a12
v_accvgpr_read_b32 v177,a13
v_cvt_pk_bf16_f32 v176,v176,v177
v_accvgpr_read_b32 v178,a14
v_accvgpr_read_b32 v179,a15
v_cvt_pk_bf16_f32 v177,v178,v179
v_mfma_f32_16x16x32_bf16 a[60:63],v[32:35],v[76:79],a[60:63]
v_mfma_f32_16x16x32_bf16 a[60:63],v[32:35],v[92:95],a[60:63]
)A"
R"A(v_mfma_f32_16x16x32_bf16 a[60:63],v[32:35],v[108:111],a[60:63]
v_accvgpr_read_b32 v178,a28
v_accvgpr_read_b32 v179,a29
v_accvgpr_read_b32 v14,a30
v_accvgpr_read_b32 v15,a31
v_cvt_pk_bf16_f32 v178,v178,v179
v_cvt_pk_bf16_f32 v179,v14,v15
s_nop 3
v_permlane16_swap_b32 v176,v178
v_permlane16_swap_b32 v177,v179
buffer_store_dwordx4 v[176:179],v40,s[20:23],s43 offen offset:128 sc1
v_accvgpr_read_b32 v192,a44
v_accvgpr_read_b32 v193,a45
v_cvt_pk_bf16_f32 v192,v192,v193
v_accvgpr_read_b32 v194,a46
v_accvgpr_read_b32 v195,a47
v_cvt_pk_bf16_f32 v193,v194,v195
v_accvgpr_read_b32 v194,a60
v_accvgpr_read_b32 v195,a61
v_accvgpr_read_b32 v16,a62
v_accvgpr_read_b32 v17,a63
v_cvt_pk_bf16_f32 v194,v194,v195
s_nop 1
v_permlane16_swap_b32 v192,v194
v_cvt_pk_bf16_f32 v195,v16,v17
s_nop 1
v_permlane16_swap_b32 v193,v195
buffer_store_dwordx4 v[192:195],v40,s[20:23],s43 offen offset:192 sc1
s_waitcnt vmcnt(4)
v_mfma_f32_16x16x32_bf16 a[76:79],v[34:37],v[136:139],a[76:79]
s_waitcnt vmcnt(3)
v_mfma_f32_16x16x32_bf16 a[76:79],v[34:37],v[168:171],a[76:79]
s_waitcnt vmcnt(2)
v_mfma_f32_16x16x32_bf16 a[76:79],v[34:37],v[200:203],a[76:79]
v_mfma_f32_16x16x32_bf16 a[92:95],v[32:35],v[136:139],a[92:95]
v_mfma_f32_16x16x32_bf16 a[92:95],v[32:35],v[168:171],a[92:95]
v_mfma_f32_16x16x32_bf16 a[92:95],v[32:35],v[200:203],a[92:95]
s_nop 3
s_nop 0
v_accvgpr_read_b32 v196,a76
v_accvgpr_read_b32 v197,a77
v_cvt_pk_bf16_f32 v196,v196,v197
v_accvgpr_read_b32 v198,a78
v_accvgpr_read_b32 v199,a79
v_cvt_pk_bf16_f32 v197,v198,v199
v_accvgpr_read_b32 v198,a92
v_accvgpr_read_b32 v199,a93
v_accvgpr_read_b32 v12,a94
v_accvgpr_read_b32 v13,a95
v_cvt_pk_bf16_f32 v198,v198,v199
s_nop 1
v_permlane16_swap_b32 v196,v198
v_cvt_pk_bf16_f32 v199,v12,v13
s_nop 1
v_permlane16_swap_b32 v197,v199
buffer_store_dwordx4 v[196:199],v40,s[20:23],s43 offen offset:256 sc1
s_or_b32 s62,s42,s40
s_cmp_lg_u32 s62,0
s_cbranch_scc1 L_noep2_%=
v_mov_b32 v12,s68
s_mov_b64 exec,1
buffer_store_dword v12,v25,s[64:67],0 offen sc1
s_mov_b64 exec,-1
L_noep2_%=:
s_endpgm
L_ps2_%=:
s_min_i32 s79,s79,100
s_add_u32 s78,s78,s79
L_pl2_%=:
s_sleep 1
s_sub_u32 s79,s79,4
s_cmp_gt_i32 s79,1
s_cbranch_scc1 L_pl2_%=
s_branch L_pr2_%=
L_ps3_%=:
s_min_i32 s79,s79,100
s_add_u32 s78,s78,s79
L_pl3_%=:
s_sleep 1
s_sub_u32 s79,s79,4
s_cmp_gt_i32 s79,1
s_cbranch_scc1 L_pl3_%=
s_branch L_pr3_%=
L_ps4_%=:
s_min_i32 s79,s79,100
s_add_u32 s78,s78,s79
L_pl4_%=:
s_sleep 1
s_sub_u32 s79,s79,4
s_cmp_gt_i32 s79,1
s_cbranch_scc1 L_pl4_%=
s_branch L_pr4_%=
L_ps5_%=:
s_min_i32 s79,s79,100
s_add_u32 s78,s78,s79
L_pl5_%=:
s_sleep 1
s_sub_u32 s79,s79,4
s_cmp_gt_i32 s79,1
s_cbranch_scc1 L_pl5_%=
s_branch L_pr5_%=
L_ps6_%=:
s_min_i32 s79,s79,100
s_add_u32 s78,s78,s79
L_pl6_%=:
s_sleep 1
s_sub_u32 s79,s79,4
s_cmp_gt_i32 s79,1
s_cbranch_scc1 L_pl6_%=
s_branch L_pr6_%=
L_ps7_%=:
s_min_i32 s79,s79,100
s_add_u32 s78,s78,s79
L_pl7_%=:
s_sleep 1
s_sub_u32 s79,s79,4
s_cmp_gt_i32 s79,1
s_cbranch_scc1 L_pl7_%=
s_branch L_pr7_%=
L_fr8_%=:
s_mov_b32 s88,262144
L_fl8_%=:
buffer_load_dwordx2 v[4:5],v24,s[64:67],s94 offen sc1
s_waitcnt vmcnt(0)
s_sub_u32 s88,s88,1
s_cmp_eq_u32 s88,0
s_cbranch_scc1 L_fo8_%=
v_cmp_ne_u32 s[90:91],s68,v4
v_and_b32 v4,-2,v5
v_cmp_ne_u32 s[92:93],s69,v4
s_nop 0
s_or_b64 s[90:91],s[90:91],s[92:93]
s_cbranch_scc1 L_fl8_%=
s_branch L_fo8_%=
L_fr9_%=:
s_mov_b32 s88,262144
L_fl9_%=:
buffer_load_dwordx2 v[4:5],v24,s[64:67],s94 offen offset:1024 sc1
s_waitcnt vmcnt(0)
s_sub_u32 s88,s88,1
s_cmp_eq_u32 s88,0
s_cbranch_scc1 L_fo9_%=
v_cmp_ne_u32 s[90:91],s68,v4
v_and_b32 v4,-2,v5
v_cmp_ne_u32 s[92:93],s69,v4
s_nop 0
s_or_b64 s[90:91],s[90:91],s[92:93]
s_cbranch_scc1 L_fl9_%=
s_branch L_fo9_%=
L_fr10_%=:
s_mov_b32 s88,262144
L_fl10_%=:
buffer_load_dwordx2 v[4:5],v24,s[64:67],s94 offen sc1
s_waitcnt vmcnt(0)
s_sub_u32 s88,s88,1
s_cmp_eq_u32 s88,0
s_cbranch_scc1 L_fo10_%=
v_cmp_ne_u32 s[90:91],s68,v4
v_and_b32 v4,-2,v5
v_cmp_ne_u32 s[92:93],s69,v4
s_nop 0
s_or_b64 s[90:91],s[90:91],s[92:93]
s_cbranch_scc1 L_fl10_%=
s_branch L_fo10_%=
L_fr11_%=:
s_mov_b32 s88,262144
L_fl11_%=:
buffer_load_dwordx2 v[4:5],v24,s[64:67],s94 offen offset:2048 sc1
s_waitcnt vmcnt(0)
s_sub_u32 s88,s88,1
s_cmp_eq_u32 s88,0
s_cbranch_scc1 L_fo11_%=
v_cmp_ne_u32 s[90:91],s68,v4
v_and_b32 v4,-2,v5
v_cmp_ne_u32 s[92:93],s69,v4
s_nop 0
s_or_b64 s[90:91],s[90:91],s[92:93]
s_cbranch_scc1 L_fl11_%=
s_branch L_fo11_%=
L_fr12_%=:
s_mov_b32 s88,262144
L_fl12_%=:
buffer_load_dwordx2 v[4:5],v24,s[64:67],s94 offen offset:3072 sc1
s_waitcnt vmcnt(0)
s_sub_u32 s88,s88,1
s_cmp_eq_u32 s88,0
s_cbranch_scc1 L_fo12_%=
v_cmp_ne_u32 s[90:91],s68,v4
v_and_b32 v4,-2,v5
v_cmp_ne_u32 s[92:93],s69,v4
s_nop 0
s_or_b64 s[90:91],s[90:91],s[92:93]
s_cbranch_scc1 L_fl12_%=
s_branch L_fo12_%=
L_out_end_%=:
L_fast_end_%=:
L_slow0_%=:
s_mov_b32 s98,0
s_branch L_slowc_%=
L_slow1_%=:
s_mov_b32 s98,1
L_slowc_%=:
s_nop 7
s_nop 7
s_waitcnt vmcnt(0) lgkmcnt(0)
s_lshl_b32 s96,%1,2
s_add_u32 s96,s96,s40
s_lshl_b32 s96,s96,15
s_add_u32 s96,s96,167772160
s_mov_b64 s[56:57],s[12:13]
s_add_u32 s56,s56,s96
s_addc_u32 s57,s57,0
s_and_b32 s57,s57,0xffff
s_mov_b32 s58,32768
s_mov_b32 s59,0x20000
s_mov_b32 s96,0
buffer_store_dwordx4 v[64:67],v6,s[56:59],s96 offen
buffer_store_dwordx4 v[68:71],v6,s[56:59],s96 offen offset:1024
buffer_store_dwordx4 v[72:75],v6,s[56:59],s96 offen offset:2048
buffer_store_dwordx4 v[76:79],v6,s[56:59],s96 offen offset:3072
s_movk_i32 s96,4096
buffer_store_dwordx4 v[80:83],v6,s[56:59],s96 offen
buffer_store_dwordx4 v[84:87],v6,s[56:59],s96 offen offset:1024
buffer_store_dwordx4 v[88:91],v6,s[56:59],s96 offen offset:2048
buffer_store_dwordx4 v[92:95],v6,s[56:59],s96 offen offset:3072
s_movk_i32 s96,8192
buffer_store_dwordx4 v[96:99],v6,s[56:59],s96 offen
buffer_store_dwordx4 v[100:103],v6,s[56:59],s96 offen offset:1024
buffer_store_dwordx4 v[104:107],v6,s[56:59],s96 offen offset:2048
buffer_store_dwordx4 v[108:111],v6,s[56:59],s96 offen offset:3072
s_movk_i32 s96,12288
buffer_store_dwordx4 v[112:115],v6,s[56:59],s96 offen
buffer_store_dwordx4 v[116:119],v6,s[56:59],s96 offen offset:1024
buffer_store_dwordx4 v[120:123],v6,s[56:59],s96 offen offset:2048
buffer_store_dwordx4 v[124:127],v6,s[56:59],s96 offen offset:3072
s_movk_i32 s96,16384
buffer_store_dwordx4 v[128:131],v6,s[56:59],s96 offen
buffer_store_dwordx4 v[132:135],v6,s[56:59],s96 offen offset:1024
buffer_store_dwordx4 v[136:139],v6,s[56:59],s96 offen offset:2048
buffer_store_dwordx4 v[140:143],v6,s[56:59],s96 offen offset:3072
s_movk_i32 s96,20480
buffer_store_dwordx4 v[144:147],v6,s[56:59],s96 offen
buffer_store_dwordx4 v[148:151],v6,s[56:59],s96 offen offset:1024
buffer_store_dwordx4 v[152:155],v6,s[56:59],s96 offen offset:2048
buffer_store_dwordx4 v[156:159],v6,s[56:59],s96 offen offset:3072
s_movk_i32 s96,24576
buffer_store_dwordx4 v[160:163],v6,s[56:59],s96 offen
buffer_store_dwordx4 v[164:167],v6,s[56:59],s96 offen offset:1024
buffer_store_dwordx4 v[168:171],v6,s[56:59],s96 offen offset:2048
buffer_store_dwordx4 v[172:175],v6,s[56:59],s96 offen offset:3072
s_movk_i32 s96,28672
buffer_store_dwordx4 v[176:179],v6,s[56:59],s96 offen
buffer_store_dwordx4 v[180:183],v6,s[56:59],s96 offen offset:1024
buffer_store_dwordx4 v[184:187],v6,s[56:59],s96 offen offset:2048
buffer_store_dwordx4 v[188:191],v6,s[56:59],s96 offen offset:3072
s_waitcnt vmcnt(0)
v_writelane_b32 v173,s88,0
v_writelane_b32 v173,s89,1
v_writelane_b32 v173,s90,2
v_writelane_b32 v173,s91,3
v_writelane_b32 v173,s92,4
v_writelane_b32 v173,s93,5
v_writelane_b32 v173,s94,6
v_writelane_b32 v173,s95,7
s_mov_b32 s88,s98
s_mul_i32 s90,s41,307200
s_mov_b64 s[92:93],s[4:5]
s_add_u32 s92,s92,s90
s_addc_u32 s93,s93,0
s_and_b32 s93,s93,0xffff
s_mov_b32 s94,307200
s_mov_b32 s95,0x20000
s_mov_b64 s[96:97],s[10:11]
s_and_b32 s97,s97,0xffff
s_mov_b32 s98,5242880
s_mov_b32 s99,0x20000
v_bfe_u32 v171,v6,4,4
v_lshrrev_b32 v172,8,v6
v_lshlrev_b32 v164,8,v171
v_lshl_add_u32 v164,v172,4,v164
v_lshlrev_b32 v165,10,v172
v_lshl_add_u32 v165,v171,1,v165
v_add_u32 v166,4096,v165
v_lshlrev_b32 v168,16,v172
v_lshrrev_b32 v170,2,v171
v_lshl_add_u32 v168,v170,4,v168
v_and_b32 v170,3,v171
v_lshl_add_u32 v168,v170,1,v168
s_lshl_b32 s90,s42,1
s_add_u32 s90,s90,s88
s_lshl_b32 s90,s90,8
v_add_u32 v168,s90,v168
s_mov_b32 s89,0
L_sjb_%=:
s_add_u32 s90,s89,s70
s_add_u32 s90,s90,1
s_and_b32 s90,s90,3
s_lshl_b32 s91,s40,2
s_add_u32 s90,s90,s91
s_lshl_b32 s90,s90,4
v_bfe_u32 v171,v6,4,4
v_lshrrev_b32 v172,8,v6
v_add_u32 v167,s90,v171
v_mul_u32_u24 v167,1280,v167
v_lshl_add_u32 v167,v172,4,v167
s_lshl_b32 s90,s88,5
s_add_u32 s90,s90,s89
s_and_b32 s91,s89,2
s_add_u32 s90,s90,s91
s_lshl_b32 s90,s90,12
v_add_u32 v169,s90,v6
v_mov_b32 v64,0
v_mov_b32 v65,0
v_mov_b32 v66,0
v_mov_b32 v67,0
v_mov_b32 v68,0
v_mov_b32 v69,0
v_mov_b32 v70,0
v_mov_b32 v71,0
v_mov_b32 v72,0
v_mov_b32 v73,0
v_mov_b32 v74,0
v_mov_b32 v75,0
v_mov_b32 v76,0
v_mov_b32 v77,0
v_mov_b32 v78,0
v_mov_b32 v79,0
v_mov_b32 v80,0
v_mov_b32 v81,0
v_mov_b32 v82,0
v_mov_b32 v83,0
v_mov_b32 v84,0
v_mov_b32 v85,0
v_mov_b32 v86,0
v_mov_b32 v87,0
v_mov_b32 v88,0
v_mov_b32 v89,0
v_mov_b32 v90,0
v_mov_b32 v91,0
v_mov_b32 v92,0
v_mov_b32 v93,0
v_mov_b32 v94,0
v_mov_b32 v95,0
s_mov_b32 s90,0
L_sq_%=:
s_lshl_b32 s91,s90,6
buffer_load_dwordx4 v[160:163],v167,s[92:95],s91 offen
s_lshl_b32 s91,s90,18
buffer_load_ushort v112,v168,s[96:99],s91 offen
buffer_load_ushort v120,v168,s[96:99],s91 offen offset:8
buffer_load_ushort v128,v168,s[96:99],s91 offen offset:64
buffer_load_ushort v136,v168,s[96:99],s91 offen offset:72
s_addk_i32 s91,8192
buffer_load_ushort v113,v168,s[96:99],s91 offen
buffer_load_ushort v121,v168,s[96:99],s91 offen offset:8
buffer_load_ushort v129,v168,s[96:99],s91 offen offset:64
buffer_load_ushort v137,v168,s[96:99],s91 offen offset:72
s_addk_i32 s91,8192
buffer_load_ushort v114,v168,s[96:99],s91 offen
buffer_load_ushort v122,v168,s[96:99],s91 offen offset:8
buffer_load_ushort v130,v168,s[96:99],s91 offen offset:64
buffer_load_ushort v138,v168,s[96:99],s91 offen offset:72
s_addk_i32 s91,8192
buffer_load_ushort v115,v168,s[96:99],s91 offen
buffer_load_ushort v123,v168,s[96:99],s91 offen offset:8
buffer_load_ushort v131,v168,s[96:99],s91 offen offset:64
buffer_load_ushort v139,v168,s[96:99],s91 offen offset:72
s_addk_i32 s91,8192
buffer_load_ushort v116,v168,s[96:99],s91 offen
buffer_load_ushort v124,v168,s[96:99],s91 offen offset:8
buffer_load_ushort v132,v168,s[96:99],s91 offen offset:64
buffer_load_ushort v140,v168,s[96:99],s91 offen offset:72
s_addk_i32 s91,8192
buffer_load_ushort v117,v168,s[96:99],s91 offen
buffer_load_ushort v125,v168,s[96:99],s91 offen offset:8
buffer_load_ushort v133,v168,s[96:99],s91 offen offset:64
buffer_load_ushort v141,v168,s[96:99],s91 offen offset:72
s_addk_i32 s91,8192
buffer_load_ushort v118,v168,s[96:99],s91 offen
buffer_load_ushort v126,v168,s[96:99],s91 offen offset:8
buffer_load_ushort v134,v168,s[96:99],s91 offen offset:64
buffer_load_ushort v142,v168,s[96:99],s91 offen offset:72
s_addk_i32 s91,8192
buffer_load_ushort v119,v168,s[96:99],s91 offen
buffer_load_ushort v127,v168,s[96:99],s91 offen offset:8
buffer_load_ushort v135,v168,s[96:99],s91 offen offset:64
buffer_load_ushort v143,v168,s[96:99],s91 offen offset:72
s_addk_i32 s91,8192
s_waitcnt vmcnt(0)
v_lshl_or_b32 v112,v113,16,v112
v_lshl_or_b32 v113,v115,16,v114
v_lshl_or_b32 v114,v117,16,v116
v_lshl_or_b32 v115,v119,16,v118
v_lshl_or_b32 v120,v121,16,v120
v_lshl_or_b32 v121,v123,16,v122
v_lshl_or_b32 v122,v125,16,v124
v_lshl_or_b32 v123,v127,16,v126
v_lshl_or_b32 v128,v129,16,v128
v_lshl_or_b32 v129,v131,16,v130
v_lshl_or_b32 v130,v133,16,v132
v_lshl_or_b32 v131,v135,16,v134
v_lshl_or_b32 v136,v137,16,v136
v_lshl_or_b32 v137,v139,16,v138
v_lshl_or_b32 v138,v141,16,v140
v_lshl_or_b32 v139,v143,16,v142
s_nop 1
v_mfma_f32_16x16x32_bf16 v[64:67],v[112:115],v[160:163],v[64:67]
v_mfma_f32_16x16x32_bf16 v[68:71],v[120:123],v[160:163],v[68:71]
v_mfma_f32_16x16x32_bf16 v[72:75],v[128:131],v[160:163],v[72:75]
v_mfma_f32_16x16x32_bf16 v[76:79],v[136:139],v[160:163],v[76:79]
s_nop 7
s_lshl_b32 s91,s90,18
buffer_load_ushort v112,v168,s[96:99],s91 offen offset:128
)A"
R"A(buffer_load_ushort v120,v168,s[96:99],s91 offen offset:136
buffer_load_ushort v128,v168,s[96:99],s91 offen offset:192
buffer_load_ushort v136,v168,s[96:99],s91 offen offset:200
s_addk_i32 s91,8192
buffer_load_ushort v113,v168,s[96:99],s91 offen offset:128
buffer_load_ushort v121,v168,s[96:99],s91 offen offset:136
buffer_load_ushort v129,v168,s[96:99],s91 offen offset:192
buffer_load_ushort v137,v168,s[96:99],s91 offen offset:200
s_addk_i32 s91,8192
buffer_load_ushort v114,v168,s[96:99],s91 offen offset:128
buffer_load_ushort v122,v168,s[96:99],s91 offen offset:136
buffer_load_ushort v130,v168,s[96:99],s91 offen offset:192
buffer_load_ushort v138,v168,s[96:99],s91 offen offset:200
s_addk_i32 s91,8192
buffer_load_ushort v115,v168,s[96:99],s91 offen offset:128
buffer_load_ushort v123,v168,s[96:99],s91 offen offset:136
buffer_load_ushort v131,v168,s[96:99],s91 offen offset:192
buffer_load_ushort v139,v168,s[96:99],s91 offen offset:200
s_addk_i32 s91,8192
buffer_load_ushort v116,v168,s[96:99],s91 offen offset:128
buffer_load_ushort v124,v168,s[96:99],s91 offen offset:136
buffer_load_ushort v132,v168,s[96:99],s91 offen offset:192
buffer_load_ushort v140,v168,s[96:99],s91 offen offset:200
s_addk_i32 s91,8192
buffer_load_ushort v117,v168,s[96:99],s91 offen offset:128
buffer_load_ushort v125,v168,s[96:99],s91 offen offset:136
buffer_load_ushort v133,v168,s[96:99],s91 offen offset:192
buffer_load_ushort v141,v168,s[96:99],s91 offen offset:200
s_addk_i32 s91,8192
buffer_load_ushort v118,v168,s[96:99],s91 offen offset:128
buffer_load_ushort v126,v168,s[96:99],s91 offen offset:136
buffer_load_ushort v134,v168,s[96:99],s91 offen offset:192
buffer_load_ushort v142,v168,s[96:99],s91 offen offset:200
s_addk_i32 s91,8192
buffer_load_ushort v119,v168,s[96:99],s91 offen offset:128
buffer_load_ushort v127,v168,s[96:99],s91 offen offset:136
buffer_load_ushort v135,v168,s[96:99],s91 offen offset:192
buffer_load_ushort v143,v168,s[96:99],s91 offen offset:200
s_addk_i32 s91,8192
s_waitcnt vmcnt(0)
v_lshl_or_b32 v112,v113,16,v112
v_lshl_or_b32 v113,v115,16,v114
v_lshl_or_b32 v114,v117,16,v116
v_lshl_or_b32 v115,v119,16,v118
v_lshl_or_b32 v120,v121,16,v120
v_lshl_or_b32 v121,v123,16,v122
v_lshl_or_b32 v122,v125,16,v124
v_lshl_or_b32 v123,v127,16,v126
v_lshl_or_b32 v128,v129,16,v128
v_lshl_or_b32 v129,v131,16,v130
v_lshl_or_b32 v130,v133,16,v132
v_lshl_or_b32 v131,v135,16,v134
v_lshl_or_b32 v136,v137,16,v136
v_lshl_or_b32 v137,v139,16,v138
v_lshl_or_b32 v138,v141,16,v140
v_lshl_or_b32 v139,v143,16,v142
s_nop 1
v_mfma_f32_16x16x32_bf16 v[80:83],v[112:115],v[160:163],v[80:83]
v_mfma_f32_16x16x32_bf16 v[84:87],v[120:123],v[160:163],v[84:87]
v_mfma_f32_16x16x32_bf16 v[88:91],v[128:131],v[160:163],v[88:91]
v_mfma_f32_16x16x32_bf16 v[92:95],v[136:139],v[160:163],v[92:95]
s_nop 7
s_add_u32 s90,s90,1
s_cmp_lg_u32 s90,20
s_cbranch_scc1 L_sq_%=
s_nop 7
v_mul_f32 v64,s61,v64
v_mul_f32 v65,s61,v65
v_mul_f32 v66,s61,v66
v_mul_f32 v67,s61,v67
v_mul_f32 v68,s61,v68
v_mul_f32 v69,s61,v69
v_mul_f32 v70,s61,v70
v_mul_f32 v71,s61,v71
v_mul_f32 v72,s61,v72
v_mul_f32 v73,s61,v73
v_mul_f32 v74,s61,v74
v_mul_f32 v75,s61,v75
v_mul_f32 v76,s61,v76
v_mul_f32 v77,s61,v77
v_mul_f32 v78,s61,v78
v_mul_f32 v79,s61,v79
v_mul_f32 v80,s61,v80
v_mul_f32 v81,s61,v81
v_mul_f32 v82,s61,v82
v_mul_f32 v83,s61,v83
v_mul_f32 v84,s61,v84
v_mul_f32 v85,s61,v85
v_mul_f32 v86,s61,v86
v_mul_f32 v87,s61,v87
v_mul_f32 v88,s61,v88
v_mul_f32 v89,s61,v89
v_mul_f32 v90,s61,v90
v_mul_f32 v91,s61,v91
v_mul_f32 v92,s61,v92
v_mul_f32 v93,s61,v93
v_mul_f32 v94,s61,v94
v_mul_f32 v95,s61,v95
v_cvt_pk_bf16_f32 v96,v64,v65
v_cvt_pk_bf16_f32 v97,v66,v67
v_cvt_pk_bf16_f32 v98,v68,v69
v_cvt_pk_bf16_f32 v99,v70,v71
v_cvt_pk_bf16_f32 v100,v72,v73
v_cvt_pk_bf16_f32 v101,v74,v75
v_cvt_pk_bf16_f32 v102,v76,v77
v_cvt_pk_bf16_f32 v103,v78,v79
v_cvt_pk_bf16_f32 v104,v80,v81
v_cvt_pk_bf16_f32 v105,v82,v83
v_cvt_pk_bf16_f32 v106,v84,v85
v_cvt_pk_bf16_f32 v107,v86,v87
v_cvt_pk_bf16_f32 v108,v88,v89
v_cvt_pk_bf16_f32 v109,v90,v91
v_cvt_pk_bf16_f32 v110,v92,v93
v_cvt_pk_bf16_f32 v111,v94,v95
v_mov_b32 v170,0xff800000
s_lshl_b32 s91,s88,18
s_mov_b32 s90,64
L_s1_%=:
buffer_load_dwordx4 v[112:115],v164,s[20:23],s91 offen
buffer_load_dwordx4 v[116:119],v164,s[20:23],s91 offen offset:64
buffer_load_dwordx4 v[120:123],v164,s[20:23],s91 offen offset:128
buffer_load_dwordx4 v[124:127],v164,s[20:23],s91 offen offset:192
s_waitcnt vmcnt(0)
v_mfma_f32_16x16x32_bf16 v[144:147],v[112:115],v[96:99],0
v_mfma_f32_16x16x32_bf16 v[144:147],v[116:119],v[100:103],v[144:147]
v_mfma_f32_16x16x32_bf16 v[144:147],v[120:123],v[104:107],v[144:147]
v_mfma_f32_16x16x32_bf16 v[144:147],v[124:127],v[108:111],v[144:147]
s_nop 7
v_max3_f32 v170,v170,v144,v145
v_max3_f32 v170,v170,v146,v147
s_addk_i32 s91,4096
s_sub_u32 s90,s90,1
s_cmp_lg_u32 s90,0
s_cbranch_scc1 L_s1_%=
v_mov_b32 v171,v170
s_nop 1
v_permlane32_swap_b32 v170,v171
v_max_f32 v170,v170,v171
v_mov_b32 v171,v170
s_nop 1
v_permlane16_swap_b32 v170,v171
v_max_f32 v170,v170,v171
v_sub_f32 v152,0,v170
v_mov_b32 v156,0
v_sub_f32 v153,0,v170
v_mov_b32 v157,0
v_sub_f32 v154,0,v170
v_mov_b32 v158,0
v_sub_f32 v155,0,v170
v_mov_b32 v159,0
v_mov_b32 v64,0
v_mov_b32 v65,0
v_mov_b32 v66,0
v_mov_b32 v67,0
v_mov_b32 v68,0
v_mov_b32 v69,0
v_mov_b32 v70,0
v_mov_b32 v71,0
v_mov_b32 v72,0
v_mov_b32 v73,0
v_mov_b32 v74,0
v_mov_b32 v75,0
v_mov_b32 v76,0
v_mov_b32 v77,0
v_mov_b32 v78,0
v_mov_b32 v79,0
v_mov_b32 v80,0
v_mov_b32 v81,0
v_mov_b32 v82,0
v_mov_b32 v83,0
v_mov_b32 v84,0
v_mov_b32 v85,0
v_mov_b32 v86,0
v_mov_b32 v87,0
v_mov_b32 v88,0
v_mov_b32 v89,0
v_mov_b32 v90,0
v_mov_b32 v91,0
v_mov_b32 v92,0
v_mov_b32 v93,0
v_mov_b32 v94,0
v_mov_b32 v95,0
s_lshl_b32 s91,s88,18
s_mov_b32 s90,32
v_add_u32 v172,4096,v164
L_s2_%=:
buffer_load_dwordx4 v[112:115],v164,s[20:23],s91 offen
buffer_load_dwordx4 v[116:119],v164,s[20:23],s91 offen offset:64
buffer_load_dwordx4 v[120:123],v164,s[20:23],s91 offen offset:128
buffer_load_dwordx4 v[124:127],v164,s[20:23],s91 offen offset:192
buffer_load_dwordx4 v[128:131],v172,s[20:23],s91 offen
buffer_load_dwordx4 v[132:135],v172,s[20:23],s91 offen offset:64
buffer_load_dwordx4 v[136:139],v172,s[20:23],s91 offen offset:128
buffer_load_dwordx4 v[140:143],v172,s[20:23],s91 offen offset:192
s_waitcnt vmcnt(0)
v_mfma_f32_16x16x32_bf16 v[144:147],v[112:115],v[96:99],v[152:155]
v_mfma_f32_16x16x32_bf16 v[144:147],v[116:119],v[100:103],v[144:147]
v_mfma_f32_16x16x32_bf16 v[144:147],v[120:123],v[104:107],v[144:147]
v_mfma_f32_16x16x32_bf16 v[144:147],v[124:127],v[108:111],v[144:147]
v_mfma_f32_16x16x32_bf16 v[148:151],v[128:131],v[96:99],v[152:155]
v_mfma_f32_16x16x32_bf16 v[148:151],v[132:135],v[100:103],v[148:151]
v_mfma_f32_16x16x32_bf16 v[148:151],v[136:139],v[104:107],v[148:151]
v_mfma_f32_16x16x32_bf16 v[148:151],v[140:143],v[108:111],v[148:151]
s_nop 7
v_exp_f32 v144,v144
v_exp_f32 v145,v145
v_exp_f32 v146,v146
v_exp_f32 v147,v147
v_exp_f32 v148,v148
v_exp_f32 v149,v149
v_exp_f32 v150,v150
v_exp_f32 v151,v151
s_nop 0
v_cvt_pk_bf16_f32 v144,v144,v145
v_cvt_pk_bf16_f32 v145,v146,v147
v_cvt_pk_bf16_f32 v146,v148,v149
v_cvt_pk_bf16_f32 v147,v150,v151
buffer_load_ushort v112,v165,s[24:27],s91 offen
buffer_load_ushort v113,v165,s[24:27],s91 offen offset:256
buffer_load_ushort v114,v165,s[24:27],s91 offen offset:512
buffer_load_ushort v115,v165,s[24:27],s91 offen offset:768
buffer_load_ushort v116,v166,s[24:27],s91 offen
buffer_load_ushort v117,v166,s[24:27],s91 offen offset:256
buffer_load_ushort v118,v166,s[24:27],s91 offen offset:512
buffer_load_ushort v119,v166,s[24:27],s91 offen offset:768
buffer_load_ushort v120,v165,s[24:27],s91 offen offset:32
buffer_load_ushort v121,v165,s[24:27],s91 offen offset:288
buffer_load_ushort v122,v165,s[24:27],s91 offen offset:544
buffer_load_ushort v123,v165,s[24:27],s91 offen offset:800
buffer_load_ushort v124,v166,s[24:27],s91 offen offset:32
buffer_load_ushort v125,v166,s[24:27],s91 offen offset:288
buffer_load_ushort v126,v166,s[24:27],s91 offen offset:544
buffer_load_ushort v127,v166,s[24:27],s91 offen offset:800
buffer_load_ushort v128,v165,s[24:27],s91 offen offset:64
buffer_load_ushort v129,v165,s[24:27],s91 offen offset:320
buffer_load_ushort v130,v165,s[24:27],s91 offen offset:576
buffer_load_ushort v131,v165,s[24:27],s91 offen offset:832
buffer_load_ushort v132,v166,s[24:27],s91 offen offset:64
buffer_load_ushort v133,v166,s[24:27],s91 offen offset:320
buffer_load_ushort v134,v166,s[24:27],s91 offen offset:576
buffer_load_ushort v135,v166,s[24:27],s91 offen offset:832
buffer_load_ushort v136,v165,s[24:27],s91 offen offset:96
buffer_load_ushort v137,v165,s[24:27],s91 offen offset:352
buffer_load_ushort v138,v165,s[24:27],s91 offen offset:608
buffer_load_ushort v139,v165,s[24:27],s91 offen offset:864
buffer_load_ushort v140,v166,s[24:27],s91 offen offset:96
buffer_load_ushort v141,v166,s[24:27],s91 offen offset:352
buffer_load_ushort v142,v166,s[24:27],s91 offen offset:608
buffer_load_ushort v143,v166,s[24:27],s91 offen offset:864
s_waitcnt vmcnt(0)
v_lshl_or_b32 v112,v113,16,v112
v_lshl_or_b32 v113,v115,16,v114
v_lshl_or_b32 v114,v117,16,v116
v_lshl_or_b32 v115,v119,16,v118
v_lshl_or_b32 v120,v121,16,v120
v_lshl_or_b32 v121,v123,16,v122
v_lshl_or_b32 v122,v125,16,v124
v_lshl_or_b32 v123,v127,16,v126
v_lshl_or_b32 v128,v129,16,v128
v_lshl_or_b32 v129,v131,16,v130
v_lshl_or_b32 v130,v133,16,v132
v_lshl_or_b32 v131,v135,16,v134
v_lshl_or_b32 v136,v137,16,v136
v_lshl_or_b32 v137,v139,16,v138
v_lshl_or_b32 v138,v141,16,v140
v_lshl_or_b32 v139,v143,16,v142
s_nop 1
v_mfma_f32_16x16x32_bf16 v[64:67],v[112:115],v[144:147],v[64:67]
v_mfma_f32_16x16x32_bf16 v[68:71],v[120:123],v[144:147],v[68:71]
v_mfma_f32_16x16x32_bf16 v[72:75],v[128:131],v[144:147],v[72:75]
v_mfma_f32_16x16x32_bf16 v[76:79],v[136:139],v[144:147],v[76:79]
s_nop 7
buffer_load_ushort v112,v165,s[24:27],s91 offen offset:128
buffer_load_ushort v113,v165,s[24:27],s91 offen offset:384
buffer_load_ushort v114,v165,s[24:27],s91 offen offset:640
buffer_load_ushort v115,v165,s[24:27],s91 offen offset:896
buffer_load_ushort v116,v166,s[24:27],s91 offen offset:128
buffer_load_ushort v117,v166,s[24:27],s91 offen offset:384
buffer_load_ushort v118,v166,s[24:27],s91 offen offset:640
buffer_load_ushort v119,v166,s[24:27],s91 offen offset:896
buffer_load_ushort v120,v165,s[24:27],s91 offen offset:160
buffer_load_ushort v121,v165,s[24:27],s91 offen offset:416
buffer_load_ushort v122,v165,s[24:27],s91 offen offset:672
buffer_load_ushort v123,v165,s[24:27],s91 offen offset:928
buffer_load_ushort v124,v166,s[24:27],s91 offen offset:160
buffer_load_ushort v125,v166,s[24:27],s91 offen offset:416
buffer_load_ushort v126,v166,s[24:27],s91 offen offset:672
buffer_load_ushort v127,v166,s[24:27],s91 offen offset:928
buffer_load_ushort v128,v165,s[24:27],s91 offen offset:192
buffer_load_ushort v129,v165,s[24:27],s91 offen offset:448
buffer_load_ushort v130,v165,s[24:27],s91 offen offset:704
buffer_load_ushort v131,v165,s[24:27],s91 offen offset:960
buffer_load_ushort v132,v166,s[24:27],s91 offen offset:192
buffer_load_ushort v133,v166,s[24:27],s91 offen offset:448
buffer_load_ushort v134,v166,s[24:27],s91 offen offset:704
buffer_load_ushort v135,v166,s[24:27],s91 offen offset:960
buffer_load_ushort v136,v165,s[24:27],s91 offen offset:224
buffer_load_ushort v137,v165,s[24:27],s91 offen offset:480
buffer_load_ushort v138,v165,s[24:27],s91 offen offset:736
buffer_load_ushort v139,v165,s[24:27],s91 offen offset:992
buffer_load_ushort v140,v166,s[24:27],s91 offen offset:224
buffer_load_ushort v141,v166,s[24:27],s91 offen offset:480
buffer_load_ushort v142,v166,s[24:27],s91 offen offset:736
buffer_load_ushort v143,v166,s[24:27],s91 offen offset:992
s_waitcnt vmcnt(0)
v_lshl_or_b32 v112,v113,16,v112
v_lshl_or_b32 v113,v115,16,v114
v_lshl_or_b32 v114,v117,16,v116
v_lshl_or_b32 v115,v119,16,v118
v_lshl_or_b32 v120,v121,16,v120
v_lshl_or_b32 v121,v123,16,v122
v_lshl_or_b32 v122,v125,16,v124
v_lshl_or_b32 v123,v127,16,v126
v_lshl_or_b32 v128,v129,16,v128
v_lshl_or_b32 v129,v131,16,v130
)A"
R"A(v_lshl_or_b32 v130,v133,16,v132
v_lshl_or_b32 v131,v135,16,v134
v_lshl_or_b32 v136,v137,16,v136
v_lshl_or_b32 v137,v139,16,v138
v_lshl_or_b32 v138,v141,16,v140
v_lshl_or_b32 v139,v143,16,v142
s_nop 1
v_mfma_f32_16x16x32_bf16 v[80:83],v[112:115],v[144:147],v[80:83]
v_mfma_f32_16x16x32_bf16 v[84:87],v[120:123],v[144:147],v[84:87]
v_mfma_f32_16x16x32_bf16 v[88:91],v[128:131],v[144:147],v[88:91]
v_mfma_f32_16x16x32_bf16 v[92:95],v[136:139],v[144:147],v[92:95]
s_nop 7
v_mfma_f32_16x16x32_bf16 v[156:159],v[28:31],v[144:147],v[156:159]
s_nop 7
s_addk_i32 s91,8192
s_sub_u32 s90,s90,1
s_cmp_lg_u32 s90,0
s_cbranch_scc1 L_s2_%=
v_rcp_f32 v170,v156
s_nop 0
v_mul_f32 v64,v170,v64
v_mul_f32 v65,v170,v65
v_mul_f32 v66,v170,v66
v_mul_f32 v67,v170,v67
v_mul_f32 v68,v170,v68
v_mul_f32 v69,v170,v69
v_mul_f32 v70,v170,v70
v_mul_f32 v71,v170,v71
v_mul_f32 v72,v170,v72
v_mul_f32 v73,v170,v73
v_mul_f32 v74,v170,v74
v_mul_f32 v75,v170,v75
v_mul_f32 v76,v170,v76
v_mul_f32 v77,v170,v77
v_mul_f32 v78,v170,v78
v_mul_f32 v79,v170,v79
v_mul_f32 v80,v170,v80
v_mul_f32 v81,v170,v81
v_mul_f32 v82,v170,v82
v_mul_f32 v83,v170,v83
v_mul_f32 v84,v170,v84
v_mul_f32 v85,v170,v85
v_mul_f32 v86,v170,v86
v_mul_f32 v87,v170,v87
v_mul_f32 v88,v170,v88
v_mul_f32 v89,v170,v89
v_mul_f32 v90,v170,v90
v_mul_f32 v91,v170,v91
v_mul_f32 v92,v170,v92
v_mul_f32 v93,v170,v93
v_mul_f32 v94,v170,v94
v_mul_f32 v95,v170,v95
v_cvt_pk_bf16_f32 v96,v64,v65
v_cvt_pk_bf16_f32 v97,v66,v67
v_cvt_pk_bf16_f32 v98,v68,v69
v_cvt_pk_bf16_f32 v99,v70,v71
v_cvt_pk_bf16_f32 v100,v72,v73
v_cvt_pk_bf16_f32 v101,v74,v75
v_cvt_pk_bf16_f32 v102,v76,v77
v_cvt_pk_bf16_f32 v103,v78,v79
v_cvt_pk_bf16_f32 v104,v80,v81
v_cvt_pk_bf16_f32 v105,v82,v83
v_cvt_pk_bf16_f32 v106,v84,v85
v_cvt_pk_bf16_f32 v107,v86,v87
v_cvt_pk_bf16_f32 v108,v88,v89
v_cvt_pk_bf16_f32 v109,v90,v91
v_cvt_pk_bf16_f32 v110,v92,v93
v_cvt_pk_bf16_f32 v111,v94,v95
buffer_store_dwordx4 v[96:99],v169,s[36:39],s55 offen sc1
buffer_store_dwordx4 v[100:103],v169,s[36:39],s55 offen offset:1024 sc1
buffer_store_dwordx4 v[104:107],v169,s[36:39],s55 offen offset:2048 sc1
buffer_store_dwordx4 v[108:111],v169,s[36:39],s55 offen offset:3072 sc1
s_waitcnt vmcnt(0)
s_add_u32 s89,s89,1
s_cmp_lg_u32 s89,4
s_cbranch_scc1 L_sjb_%=
s_mov_b32 s98,s88
v_readlane_b32 s88,v173,0
v_readlane_b32 s89,v173,1
v_readlane_b32 s90,v173,2
v_readlane_b32 s91,v173,3
v_readlane_b32 s92,v173,4
v_readlane_b32 s93,v173,5
v_readlane_b32 s94,v173,6
v_readlane_b32 s95,v173,7
s_nop 4
s_mov_b32 s96,0
buffer_load_dwordx4 v[64:67],v6,s[56:59],s96 offen sc1
buffer_load_dwordx4 v[68:71],v6,s[56:59],s96 offen offset:1024 sc1
buffer_load_dwordx4 v[72:75],v6,s[56:59],s96 offen offset:2048 sc1
buffer_load_dwordx4 v[76:79],v6,s[56:59],s96 offen offset:3072 sc1
s_movk_i32 s96,4096
buffer_load_dwordx4 v[80:83],v6,s[56:59],s96 offen sc1
buffer_load_dwordx4 v[84:87],v6,s[56:59],s96 offen offset:1024 sc1
buffer_load_dwordx4 v[88:91],v6,s[56:59],s96 offen offset:2048 sc1
buffer_load_dwordx4 v[92:95],v6,s[56:59],s96 offen offset:3072 sc1
s_movk_i32 s96,8192
buffer_load_dwordx4 v[96:99],v6,s[56:59],s96 offen sc1
buffer_load_dwordx4 v[100:103],v6,s[56:59],s96 offen offset:1024 sc1
buffer_load_dwordx4 v[104:107],v6,s[56:59],s96 offen offset:2048 sc1
buffer_load_dwordx4 v[108:111],v6,s[56:59],s96 offen offset:3072 sc1
s_movk_i32 s96,12288
buffer_load_dwordx4 v[112:115],v6,s[56:59],s96 offen sc1
buffer_load_dwordx4 v[116:119],v6,s[56:59],s96 offen offset:1024 sc1
buffer_load_dwordx4 v[120:123],v6,s[56:59],s96 offen offset:2048 sc1
buffer_load_dwordx4 v[124:127],v6,s[56:59],s96 offen offset:3072 sc1
s_movk_i32 s96,16384
buffer_load_dwordx4 v[128:131],v6,s[56:59],s96 offen sc1
buffer_load_dwordx4 v[132:135],v6,s[56:59],s96 offen offset:1024 sc1
buffer_load_dwordx4 v[136:139],v6,s[56:59],s96 offen offset:2048 sc1
buffer_load_dwordx4 v[140:143],v6,s[56:59],s96 offen offset:3072 sc1
s_movk_i32 s96,20480
buffer_load_dwordx4 v[144:147],v6,s[56:59],s96 offen sc1
buffer_load_dwordx4 v[148:151],v6,s[56:59],s96 offen offset:1024 sc1
buffer_load_dwordx4 v[152:155],v6,s[56:59],s96 offen offset:2048 sc1
buffer_load_dwordx4 v[156:159],v6,s[56:59],s96 offen offset:3072 sc1
s_movk_i32 s96,24576
buffer_load_dwordx4 v[160:163],v6,s[56:59],s96 offen sc1
buffer_load_dwordx4 v[164:167],v6,s[56:59],s96 offen offset:1024 sc1
buffer_load_dwordx4 v[168:171],v6,s[56:59],s96 offen offset:2048 sc1
buffer_load_dwordx4 v[172:175],v6,s[56:59],s96 offen offset:3072 sc1
s_movk_i32 s96,28672
buffer_load_dwordx4 v[176:179],v6,s[56:59],s96 offen sc1
buffer_load_dwordx4 v[180:183],v6,s[56:59],s96 offen offset:1024 sc1
buffer_load_dwordx4 v[184:187],v6,s[56:59],s96 offen offset:2048 sc1
buffer_load_dwordx4 v[188:191],v6,s[56:59],s96 offen offset:3072 sc1
s_waitcnt vmcnt(0)
s_cmp_lg_u32 s98,0
s_cbranch_scc1 L_sr1_%=
buffer_load_dwordx4 v[64:67],v6,s[36:39],s84 offen sc1
buffer_load_dwordx4 v[68:71],v6,s[36:39],s84 offen offset:1024 sc1
buffer_load_dwordx4 v[72:75],v6,s[36:39],s84 offen offset:2048 sc1
buffer_load_dwordx4 v[76:79],v6,s[36:39],s84 offen offset:3072 sc1
buffer_load_dwordx4 v[80:83],v6,s[36:39],s85 offen sc1
buffer_load_dwordx4 v[84:87],v6,s[36:39],s85 offen offset:1024 sc1
buffer_load_dwordx4 v[88:91],v6,s[36:39],s85 offen offset:2048 sc1
buffer_load_dwordx4 v[92:95],v6,s[36:39],s85 offen offset:3072 sc1
buffer_load_dwordx4 v[96:99],v6,s[36:39],s86 offen sc1
buffer_load_dwordx4 v[100:103],v6,s[36:39],s86 offen offset:1024 sc1
buffer_load_dwordx4 v[104:107],v6,s[36:39],s86 offen offset:2048 sc1
buffer_load_dwordx4 v[108:111],v6,s[36:39],s86 offen offset:3072 sc1
buffer_load_dwordx4 v[112:115],v6,s[36:39],s87 offen sc1
buffer_load_dwordx4 v[116:119],v6,s[36:39],s87 offen offset:1024 sc1
buffer_load_dwordx4 v[120:123],v6,s[36:39],s87 offen offset:2048 sc1
buffer_load_dwordx4 v[124:127],v6,s[36:39],s87 offen offset:3072 sc1
s_waitcnt vmcnt(0)
s_nop 7
s_branch L_sret0_%=
L_sr1_%=:
buffer_load_dwordx4 v[136:139],v6,s[36:39],s84 offen sc1
buffer_load_dwordx4 v[140:143],v6,s[36:39],s84 offen offset:1024 sc1
buffer_load_dwordx4 v[152:155],v6,s[36:39],s84 offen offset:2048 sc1
buffer_load_dwordx4 v[156:159],v6,s[36:39],s84 offen offset:3072 sc1
buffer_load_dwordx4 v[168:171],v6,s[36:39],s85 offen sc1
buffer_load_dwordx4 v[172:175],v6,s[36:39],s85 offen offset:1024 sc1
buffer_load_dwordx4 v[184:187],v6,s[36:39],s85 offen offset:2048 sc1
buffer_load_dwordx4 v[188:191],v6,s[36:39],s85 offen offset:3072 sc1
buffer_load_dwordx4 v[200:203],v6,s[36:39],s86 offen sc1
buffer_load_dwordx4 v[204:207],v6,s[36:39],s86 offen offset:1024 sc1
buffer_load_dwordx4 v[216:219],v6,s[36:39],s86 offen offset:2048 sc1
buffer_load_dwordx4 v[220:223],v6,s[36:39],s86 offen offset:3072 sc1
buffer_load_dwordx4 v[232:235],v6,s[36:39],s87 offen sc1
buffer_load_dwordx4 v[236:239],v6,s[36:39],s87 offen offset:1024 sc1
buffer_load_dwordx4 v[248:251],v6,s[36:39],s87 offen offset:2048 sc1
buffer_load_dwordx4 v[252:255],v6,s[36:39],s87 offen offset:3072 sc1
s_waitcnt vmcnt(0)
s_nop 7
s_branch L_sret1_%=
L_end_%=:
)A"
    :: "s"(karg), "s"(blk), "s"(wid)
    : "v0", "v1", "v2", "v3", "v4", "v5", "v6", "v7", "v8", "v9", "v10", "v11", "v12", "v13", "v14", "v15", "v16", "v17", "v18", "v19", "v20", "v21", "v22", "v23", "v24", "v25", "v26", "v27", "v28", "v29", "v30", "v31", "v32", "v33", "v34", "v35", "v36", "v37", "v38", "v39", "v40", "v41", "v42", "v43", "v44", "v45", "v46", "v47", "v48", "v49", "v50", "v51", "v52", "v53", "v54", "v55", "v56", "v57", "v58", "v59", "v60", "v61", "v62", "v63", "v64", "v65", "v66", "v67", "v68", "v69", "v70", "v71", "v72", "v73", "v74", "v75", "v76", "v77", "v78", "v79", "v80", "v81", "v82", "v83", "v84", "v85", "v86", "v87", "v88", "v89", "v90", "v91", "v92", "v93", "v94", "v95", "v96", "v97", "v98", "v99", "v100", "v101", "v102", "v103", "v104", "v105", "v106", "v107", "v108", "v109", "v110", "v111", "v112", "v113", "v114", "v115", "v116", "v117", "v118", "v119", "v120", "v121", "v122", "v123", "v124", "v125", "v126", "v127", "v128", "v129", "v130", "v131", "v132", "v133", "v134", "v135", "v136", "v137", "v138", "v139", "v140", "v141", "v142", "v143", "v144", "v145", "v146", "v147", "v148", "v149", "v150", "v151", "v152", "v153", "v154", "v155", "v156", "v157", "v158", "v159", "v160", "v161", "v162", "v163", "v164", "v165", "v166", "v167", "v168", "v169", "v170", "v171", "v172", "v173", "v174", "v175", "v176", "v177", "v178", "v179", "v180", "v181", "v182", "v183", "v184", "v185", "v186", "v187", "v188", "v189", "v190", "v191", "v192", "v193", "v194", "v195", "v196", "v197", "v198", "v199", "v200", "v201", "v202", "v203", "v204", "v205", "v206", "v207", "v208", "v209", "v210", "v211", "v212", "v213", "v214", "v215", "v216", "v217", "v218", "v219", "v220", "v221", "v222", "v223", "v224", "v225", "v226", "v227", "v228", "v229", "v230", "v231", "v232", "v233", "v234", "v235", "v236", "v237", "v238", "v239", "v240", "v241", "v242", "v243", "v244", "v245", "v246", "v247", "v248", "v249", "v250", "v251", "v252", "v253", "v254", "v255", "a0", "a1", "a2", "a3", "a4", "a5", "a6", "a7", "a8", "a9", "a10", "a11", "a12", "a13", "a14", "a15", "a16", "a17", "a18", "a19", "a20", "a21", "a22", "a23", "a24", "a25", "a26", "a27", "a28", "a29", "a30", "a31", "a32", "a33", "a34", "a35", "a36", "a37", "a38", "a39", "a40", "a41", "a42", "a43", "a44", "a45", "a46", "a47", "a48", "a49", "a50", "a51", "a52", "a53", "a54", "a55", "a56", "a57", "a58", "a59", "a60", "a61", "a62", "a63", "a64", "a65", "a66", "a67", "a68", "a69", "a70", "a71", "a72", "a73", "a74", "a75", "a76", "a77", "a78", "a79", "a80", "a81", "a82", "a83", "a84", "a85", "a86", "a87", "a88", "a89", "a90", "a91", "a92", "a93", "a94", "a95", "a96", "a97", "a98", "a99", "a100", "a101", "a102", "a103", "a104", "a105", "a106", "a107", "a108", "a109", "a110", "a111", "a112", "a113", "a114", "a115", "a116", "a117", "a118", "a119", "a120", "a121", "a122", "a123", "a124", "a125", "a126", "a127", "a128", "a129", "a130", "a131", "a132", "a133", "a134", "a135", "a136", "a137", "a138", "a139", "a140", "a141", "a142", "a143", "a144", "a145", "a146", "a147", "a148", "a149", "a150", "a151", "a152", "a153", "a154", "a155", "a156", "a157", "a158", "a159", "a160", "a161", "a162", "a163", "a164", "a165", "a166", "a167", "a168", "a169", "a170", "a171", "a172", "a173", "a174", "a175", "a176", "a177", "a178", "a179", "a180", "a181", "a182", "a183", "a184", "a185", "a186", "a187", "a188", "a189", "a190", "a191", "a192", "a193", "a194", "a195", "a196", "a197", "a198", "a199", "a200", "a201", "a202", "a203", "a204", "a205", "a206", "a207", "a208", "a209", "a210", "a211", "a212", "a213", "a214", "a215", "a216", "a217", "a218", "a219", "a220", "a221", "a222", "a223", "a224", "a225", "a226", "a227", "a228", "a229", "a230", "a231", "a232", "a233", "a234", "a235", "a236", "a237", "a238", "a239", "a240", "a241", "a242", "a243", "a244", "a245", "a246", "a247", "a248", "a249", "a250", "a251", "a252", "a253", "a254", "a255", "s4", "s5", "s6", "s7", "s8", "s9", "s10", "s11", "s12", "s13", "s14", "s15", "s16", "s17", "s18", "s19", "s20", "s21", "s22", "s23", "s24", "s25", "s26", "s27", "s28", "s29", "s30", "s31", "s32", "s33", "s34", "s35", "s36", "s37", "s38", "s39", "s40", "s41", "s42", "s43", "s44", "s45", "s46", "s47", "s48", "s49", "s50", "s51", "s52", "s53", "s54", "s55", "s56", "s57", "s58", "s59", "s60", "s61", "s62", "s63", "s64", "s65", "s66", "s67", "s68", "s69", "s70", "s71", "s72", "s73", "s74", "s75", "s76", "s77", "s78", "s79", "s80", "s81", "s82", "s83", "s84", "s85", "s86", "s87", "s88", "s89", "s90", "s91", "s92", "s93", "s94", "s95", "s96", "s97", "s98", "s99", "s100", "s101", "vcc", "scc", "memory");
#endif
}

extern "C" void kernel_launch(void* const* d_in, const int* in_sizes, int n_in,
                              void* d_out, int out_size, void* d_ws, size_t ws_size,
                              hipStream_t stream) {
  (void)hipFuncSetAttribute((const void*)qattn_kernel, hipFuncAttributeMaxDynamicSharedMemorySize, LDS_ATT);
  qattn_kernel<<<256, 256, LDS_ATT, stream>>>(d_in[0], d_in[1], d_in[2], d_in[3], d_ws, d_in[4], d_out);
}
```

```cpp
#include <hip/hip_runtime.h>
#include <hip/hip_bf16.h>

constexpr int LDS_ATT = 163840;

__global__ __launch_bounds__(256, 1) void qattn_kernel(const void* __restrict__ x, const void* __restrict__ kc, const void* __restrict__ vc,
                                                       const void* __restrict__ wq, void* __restrict__ ws, const void* __restrict__ wo,
                                                       void* __restrict__ out) {
#if defined(__HIP_DEVICE_COMPILE__)
  const auto karg = __builtin_amdgcn_kernarg_segment_ptr();
  const unsigned blk = blockIdx.x;
  const unsigned wid = __builtin_amdgcn_readfirstlane(threadIdx.x >> 6);
  asm volatile(
R"A(s_memtime s[72:73]
s_memrealtime s[74:75]
s_load_dwordx8 s[4:11],%0,0x0
s_load_dwordx4 s[12:15],%0,0x20
s_load_dwordx2 s[16:17],%0,0x30
s_mov_b32 s40,%2
s_mov_b32 s43,%1
s_lshr_b32 s45,s43,3
v_mbcnt_lo_u32_b32 v12,-1,0
v_mbcnt_hi_u32_b32 v12,-1,v12
s_getpc_b64 s[88:89]
L_pc_%=:
s_and_b32 s88,s88,0xffffff80
s_and_b32 s89,s89,0xffff
s_mov_b32 s90,L_fast_end_%=-L_pc_%=
s_mov_b32 s91,0x20000
s_lshl_b32 s44,s45,2
s_add_u32 s44,s44,s40
s_mulk_i32 s44,896
v_lshlrev_b32 v67,7,v12
s_mov_b64 exec,127
buffer_load_dword v19,v67,s[88:91],s44 offen
s_mov_b64 exec,-1
s_and_b32 s70,s43,3
s_and_b32 s71,s45,3
s_lshl_b32 s42,s70,2
s_add_u32 s42,s42,s71
s_and_b32 s41,s43,4
s_lshl_b32 s41,s41,1
s_lshr_b32 s46,s45,2
s_add_u32 s41,s41,s46
s_lshl_b32 s56,s42,9
s_mov_b32 s57,0
s_lshl_b32 s59,s40,13
s_add_u32 s58,s59,65536
v_and_b32 v64,3,v12
v_bfe_u32 v65,v12,2,2
v_lshrrev_b32 v66,4,v12
s_lshl_b32 s44,s40,17
v_lshlrev_b32 v75,13,v65
v_lshl_add_u32 v75,v66,6,v75
v_lshl_add_u32 v75,v64,4,v75
v_add_u32 v75,s44,v75
v_add_u32 v56,0,v75
v_add_u32 v57,256,v75
v_add_u32 v58,32768,v75
v_add_u32 v59,33024,v75
v_add_u32 v60,65536,v75
v_add_u32 v61,65792,v75
v_add_u32 v62,98304,v75
v_add_u32 v63,98560,v75
s_waitcnt lgkmcnt(0)
s_mov_b64 s[32:33],s[10:11]
s_and_b32 s33,s33,0xffff
s_mov_b32 s34,5242880
s_mov_b32 s35,0x20000
s_add_u32 m0,s58,1024
s_nop 0
buffer_load_dwordx4 v57,s[32:35],s56 offen lds
s_add_u32 m0,s58,3072
s_nop 0
buffer_load_dwordx4 v59,s[32:35],s56 offen lds
s_add_u32 m0,s58,5120
s_nop 0
buffer_load_dwordx4 v61,s[32:35],s56 offen lds
s_add_u32 m0,s58,7168
s_nop 0
buffer_load_dwordx4 v63,s[32:35],s56 offen lds
v_lshrrev_b32 v75,3,v12
s_lshl_b32 s44,s40,6
v_add_u32 v75,s44,v75
v_mul_u32_u24 v75,1280,v75
v_and_b32 v76,7,v12
s_add_u32 s84,s70,1
s_and_b32 s84,s84,3
s_mulk_i32 s84,20480
s_addk_i32 s84,4096
s_add_u32 s85,s70,2
s_and_b32 s85,s85,3
s_mulk_i32 s85,20480
s_addk_i32 s85,2048
s_add_u32 s86,s70,3
s_and_b32 s86,s86,3
s_mulk_i32 s86,20480
s_addk_i32 s86,4096
s_add_u32 s87,s70,4
s_and_b32 s87,s87,3
s_mulk_i32 s87,20480
s_addk_i32 s87,2048
v_or_b32 v77,0,v66
v_xor_b32 v77,v77,v76
v_lshl_add_u32 v77,v77,4,v75
v_add_u32 v48,s84,v77
v_add_u32 v50,s85,v77
v_add_u32 v52,s86,v77
v_add_u32 v54,s87,v77
v_or_b32 v77,4,v66
v_xor_b32 v77,v77,v76
v_lshl_add_u32 v77,v77,4,v75
v_add_u32 v77,9216,v77
v_add_u32 v49,s84,v77
v_add_u32 v51,s85,v77
v_add_u32 v53,s86,v77
v_add_u32 v55,s87,v77
s_mul_i32 s44,s41,307200
s_mov_b64 s[28:29],s[4:5]
s_add_u32 s28,s28,s44
s_addc_u32 s29,s29,0
s_sub_u32 s28,s28,4096
s_subb_u32 s29,s29,0
s_and_b32 s29,s29,0xffff
s_mov_b32 s30,311296
s_mov_b32 s31,0x20000
s_add_u32 m0,s59,0
s_nop 0
buffer_load_dwordx4 v48,s[28:31],s57 offen lds
buffer_load_dwordx4 v49,s[28:31],s57 offen offset:1024 lds
buffer_load_dwordx4 v50,s[28:31],s57 offen offset:2048 lds
buffer_load_dwordx4 v51,s[28:31],s57 offen offset:3072 lds
s_addk_i32 m0,4096
s_nop 0
buffer_load_dwordx4 v52,s[28:31],s57 offen lds
buffer_load_dwordx4 v53,s[28:31],s57 offen offset:1024 lds
buffer_load_dwordx4 v54,s[28:31],s57 offen offset:2048 lds
buffer_load_dwordx4 v55,s[28:31],s57 offen offset:3072 lds
s_addk_i32 s57,128
s_xor_b32 s59,s59,131072
s_add_u32 m0,s58,0
s_nop 0
buffer_load_dwordx4 v56,s[32:35],s56 offen lds
s_add_u32 m0,s58,2048
s_nop 0
buffer_load_dwordx4 v58,s[32:35],s56 offen lds
s_add_u32 m0,s58,4096
s_nop 0
buffer_load_dwordx4 v60,s[32:35],s56 offen lds
s_add_u32 m0,s58,6144
s_nop 0
buffer_load_dwordx4 v62,s[32:35],s56 offen lds
s_add_u32 s56,s56,524288
s_add_u32 s58,s58,32768
s_cmp_ge_u32 s58,131072
s_cselect_b32 s62,98304,0
s_sub_u32 s58,s58,s62
s_add_u32 s12,s12,4194303
s_addc_u32 s13,s13,0
s_and_b32 s12,s12,0xffc00000
s_lshl_b32 s55,s40,15
s_lshl_b32 s84,s41,2
s_add_u32 s84,s84,s70
s_lshl_b32 s84,s84,7
s_lshl_b32 s85,s71,2
s_add_u32 s84,s84,s85
s_add_u32 s84,s84,s40
s_addk_i32 s84,112
s_lshl_b32 s84,s84,14
s_add_u32 s84,s84,s12
s_addc_u32 s85,s13,0
s_mov_b32 s87,0x13572468
s_mov_b32 s79,0
s_load_dword s78,s[84:85],0x1000 glc
s_xor_b32 s76,s84,2097152
s_mov_b32 s77,s85
s_load_dword s78,s[76:77],0x0 glc
s_load_dword s78,s[76:77],0x2000 glc
s_load_dword s78,s[76:77],0x100 glc
s_load_dword s78,s[76:77],0x2100 glc
s_load_dword s78,s[76:77],0x200 glc
s_load_dword s78,s[76:77],0x2200 glc
s_add_u32 m0,s58,0
s_nop 0
buffer_load_dwordx4 v56,s[32:35],s56 offen lds
s_addk_i32 m0,1024
s_nop 0
buffer_load_dwordx4 v57,s[32:35],s56 offen lds
s_addk_i32 m0,1024
s_nop 0
buffer_load_dwordx4 v58,s[32:35],s56 offen lds
s_addk_i32 m0,1024
s_nop 0
buffer_load_dwordx4 v59,s[32:35],s56 offen lds
s_addk_i32 m0,1024
s_nop 0
buffer_load_dwordx4 v60,s[32:35],s56 offen lds
s_addk_i32 m0,1024
s_nop 0
buffer_load_dwordx4 v61,s[32:35],s56 offen lds
s_addk_i32 m0,1024
s_nop 0
buffer_load_dwordx4 v62,s[32:35],s56 offen lds
s_addk_i32 m0,1024
s_nop 0
buffer_load_dwordx4 v63,s[32:35],s56 offen lds
s_add_u32 s56,s56,524288
s_add_u32 s58,s58,32768
s_cmp_ge_u32 s58,131072
s_cselect_b32 s62,98304,0
s_sub_u32 s58,s58,s62
s_mov_b32 s60,65536
v_lshlrev_b32 v6,4,v12
v_lshlrev_b32 v72,12,v66
v_lshl_add_u32 v72,v65,6,v72
v_lshl_add_u32 v23,v64,4,v72
v_add_u32 v22,65536,v23
v_and_b32 v74,15,v12
v_lshrrev_b32 v76,1,v74
v_lshlrev_b32 v75,7,v74
s_lshl_b32 s44,s40,13
v_add_u32 v75,s44,v75
v_or_b32 v77,0,v66
v_xor_b32 v77,v77,v76
v_lshl_add_u32 v20,v77,4,v75
v_or_b32 v77,4,v66
v_xor_b32 v77,v77,v76
v_lshl_add_u32 v21,v77,4,v75
s_waitcnt vmcnt(12)
s_nop 0
s_memtime s[64:65]
s_memrealtime s[66:67]
s_bfe_u32 s68,%1,327683
s_waitcnt lgkmcnt(0)
s_sub_u32 s64,s64,s72
s_sub_u32 s66,s66,s74
s_mul_i32 s64,s64,10
s_mulk_i32 s66,224
s_cmp_lt_u32 s64,s66
s_cselect_b32 s95,64,0
s_mov_b32 %2,s95
s_cbranch_scc1 L_se1_%=
L_sg1_%=:
s_cmp_eq_u32 s68,0
s_cbranch_scc1 L_se1_%=
s_sleep 2
s_sub_u32 s68,s68,1
s_branch L_sg1_%=
L_se1_%=:
s_barrier
ds_read_b128 a[192:195],v20
ds_read_b64_tr_b16 a[128:129],v22 offset:1024
ds_read_b64_tr_b16 a[130:131],v22 offset:3072
ds_read_b128 a[196:199],v21
ds_read_b64_tr_b16 a[132:133],v22 offset:17408
ds_read_b64_tr_b16 a[134:135],v22 offset:19456
ds_read_b128 a[204:207],v21 offset:2048
ds_read_b128 a[200:203],v20 offset:2048
ds_read_b128 a[208:211],v20 offset:4096
ds_read_b128 a[212:215],v21 offset:4096
ds_read_b128 a[220:223],v21 offset:6144
ds_read_b128 a[216:219],v20 offset:6144
ds_read_b64_tr_b16 a[136:137],v22 offset:1032
ds_read_b64_tr_b16 a[138:139],v22 offset:3080
s_waitcnt lgkmcnt(11)
v_mfma_f32_16x16x32_bf16 v[128:131],a[128:131],a[192:195],0
s_add_u32 m0,s59,0
ds_read_b64_tr_b16 a[140:141],v22 offset:17416
ds_read_b64_tr_b16 a[142:143],v22 offset:19464
s_waitcnt lgkmcnt(10)
v_mfma_f32_16x16x32_bf16 v[128:131],a[132:135],a[196:199],v[128:131]
s_waitcnt lgkmcnt(9)
v_mfma_f32_16x16x32_bf16 v[136:139],a[132:135],a[204:207],0
s_waitcnt lgkmcnt(8)
v_mfma_f32_16x16x32_bf16 v[136:139],a[128:131],a[200:203],v[136:139]
s_waitcnt lgkmcnt(7)
v_mfma_f32_16x16x32_bf16 v[144:147],a[128:131],a[208:211],0
s_waitcnt lgkmcnt(6)
buffer_load_dwordx4 v48,s[28:31],s57 offen lds
v_mfma_f32_16x16x32_bf16 v[144:147],a[132:135],a[212:215],v[144:147]
s_waitcnt lgkmcnt(5)
v_mfma_f32_16x16x32_bf16 v[152:155],a[132:135],a[220:223],0
s_waitcnt lgkmcnt(4)
v_mfma_f32_16x16x32_bf16 v[152:155],a[128:131],a[216:219],v[152:155]
s_waitcnt lgkmcnt(2)
ds_read_b64_tr_b16 a[144:145],v22 offset:1280
ds_read_b64_tr_b16 a[146:147],v22 offset:3328
v_mfma_f32_16x16x32_bf16 v[132:135],a[136:139],a[192:195],0
s_waitcnt lgkmcnt(2)
ds_read_b64_tr_b16 a[148:149],v22 offset:17664
ds_read_b64_tr_b16 a[150:151],v22 offset:19712
v_mfma_f32_16x16x32_bf16 v[132:135],a[140:143],a[196:199],v[132:135]
buffer_load_dwordx4 v49,s[28:31],s57 offen offset:1024 lds
v_mfma_f32_16x16x32_bf16 v[140:143],a[140:143],a[204:207],0
v_mfma_f32_16x16x32_bf16 v[140:143],a[136:139],a[200:203],v[140:143]
v_mfma_f32_16x16x32_bf16 v[148:151],a[136:139],a[208:211],0
v_mfma_f32_16x16x32_bf16 v[148:151],a[140:143],a[212:215],v[148:151]
v_mfma_f32_16x16x32_bf16 v[156:159],a[140:143],a[220:223],0
buffer_load_dwordx4 v50,s[28:31],s57 offen offset:2048 lds
v_mfma_f32_16x16x32_bf16 v[156:159],a[136:139],a[216:219],v[156:159]
s_waitcnt lgkmcnt(2)
ds_read_b64_tr_b16 a[152:153],v22 offset:1288
ds_read_b64_tr_b16 a[154:155],v22 offset:3336
v_mfma_f32_16x16x32_bf16 v[160:163],a[144:147],a[192:195],0
s_waitcnt lgkmcnt(2)
ds_read_b64_tr_b16 a[156:157],v22 offset:17672
ds_read_b64_tr_b16 a[158:159],v22 offset:19720
v_mfma_f32_16x16x32_bf16 v[160:163],a[148:151],a[196:199],v[160:163]
v_mfma_f32_16x16x32_bf16 v[168:171],a[148:151],a[204:207],0
v_mfma_f32_16x16x32_bf16 v[168:171],a[144:147],a[200:203],v[168:171]
buffer_load_dwordx4 v51,s[28:31],s57 offen offset:3072 lds
v_mfma_f32_16x16x32_bf16 v[176:179],a[144:147],a[208:211],0
s_addk_i32 m0,4096
v_mfma_f32_16x16x32_bf16 v[176:179],a[148:151],a[212:215],v[176:179]
v_mfma_f32_16x16x32_bf16 v[184:187],a[148:151],a[220:223],0
v_mfma_f32_16x16x32_bf16 v[184:187],a[144:147],a[216:219],v[184:187]
s_waitcnt lgkmcnt(2)
v_mfma_f32_16x16x32_bf16 v[164:167],a[152:155],a[192:195],0
s_waitcnt lgkmcnt(0)
buffer_load_dwordx4 v52,s[28:31],s57 offen lds
ds_read_b64_tr_b16 a[160:161],v22 offset:1536
ds_read_b64_tr_b16 a[162:163],v22 offset:3584
v_mfma_f32_16x16x32_bf16 v[164:167],a[156:159],a[196:199],v[164:167]
ds_read_b64_tr_b16 a[164:165],v22 offset:17920
ds_read_b64_tr_b16 a[166:167],v22 offset:19968
v_mfma_f32_16x16x32_bf16 v[172:175],a[156:159],a[204:207],0
v_mfma_f32_16x16x32_bf16 v[172:175],a[152:155],a[200:203],v[172:175]
v_mfma_f32_16x16x32_bf16 v[180:183],a[152:155],a[208:211],0
v_mfma_f32_16x16x32_bf16 v[180:183],a[156:159],a[212:215],v[180:183]
buffer_load_dwordx4 v53,s[28:31],s57 offen offset:1024 lds
v_mfma_f32_16x16x32_bf16 v[188:191],a[156:159],a[220:223],0
s_waitcnt vmcnt(14)
v_mfma_f32_16x16x32_bf16 v[188:191],a[152:155],a[216:219],v[188:191]
s_barrier
ds_read_b64_tr_b16 a[168:169],v22 offset:1544
ds_read_b64_tr_b16 a[170:171],v22 offset:3592
s_waitcnt lgkmcnt(4)
v_mfma_f32_16x16x32_bf16 v[192:195],a[160:163],a[192:195],0
s_waitcnt lgkmcnt(2)
ds_read_b64_tr_b16 a[172:173],v22 offset:17928
ds_read_b64_tr_b16 a[174:175],v22 offset:19976
v_mfma_f32_16x16x32_bf16 v[192:195],a[164:167],a[196:199],v[192:195]
buffer_load_dwordx4 v54,s[28:31],s57 offen offset:2048 lds
v_mfma_f32_16x16x32_bf16 v[200:203],a[164:167],a[204:207],0
s_lshl_b32 s44,s41,13
s_add_u32 s44,s44,134217728
s_mov_b64 s[64:65],s[12:13]
v_mfma_f32_16x16x32_bf16 v[200:203],a[160:163],a[200:203],v[200:203]
s_add_u32 s64,s64,s44
s_addc_u32 s65,s65,0
s_and_b32 s65,s65,0xffff
v_mfma_f32_16x16x32_bf16 v[208:211],a[160:163],a[208:211],0
s_movk_i32 s66,8192
s_mov_b32 s67,0x20000
v_mov_b32 v14,4096
v_mfma_f32_16x16x32_bf16 v[208:211],a[164:167],a[212:215],v[208:211]
s_mov_b64 s[72:73],s[14:15]
buffer_load_dwordx4 v55,s[28:31],s57 offen offset:3072 lds
v_mfma_f32_16x16x32_bf16 v[216:219],a[164:167],a[220:223],0
s_add_u32 m0,s58,0
s_and_b32 s73,s73,0xffff
ds_read_b64_tr_b16 a[176:177],v22 offset:1792
v_mfma_f32_16x16x32_bf16 v[216:219],a[160:163],a[216:219],v[216:219]
s_mov_b32 s74,5242880
s_mov_b32 s75,0x20000
ds_read_b64_tr_b16 a[178:179],v22 offset:3840
s_waitcnt lgkmcnt(4)
v_mfma_f32_16x16x32_bf16 v[196:199],a[168:171],a[192:195],0
s_waitcnt lgkmcnt(2)
ds_read_b64_tr_b16 a[180:181],v22 offset:18176
ds_read_b64_tr_b16 a[182:183],v22 offset:20224
v_mfma_f32_16x16x32_bf16 v[196:199],a[172:175],a[196:199],v[196:199]
s_lshl_b32 s83,%1,2
buffer_load_dwordx4 v56,s[32:35],s56 offen lds
v_mfma_f32_16x16x32_bf16 v[204:207],a[172:175],a[204:207],0
s_addk_i32 m0,1024
v_lshlrev_b32 v15,3,v6
v_add_u32 v16,128,v15
v_mfma_f32_16x16x32_bf16 v[204:207],a[168:171],a[200:203],v[204:207]
s_add_u32 s83,s83,s40
ds_read_b64_tr_b16 a[184:185],v22 offset:1800
v_lshrrev_b32 v16,10,v16
v_mfma_f32_16x16x32_bf16 v[212:215],a[168:171],a[208:211],0
s_mulk_i32 s83,5120
ds_read_b64_tr_b16 a[186:187],v22 offset:3848
v_lshl_add_u32 v15,v16,24,v15
v_mfma_f32_16x16x32_bf16 v[212:215],a[172:175],a[212:215],v[212:215]
s_lshl_b32 s43,s41,5
buffer_load_dwordx4 v57,s[32:35],s56 offen lds
v_mfma_f32_16x16x32_bf16 v[220:223],a[172:175],a[220:223],0
s_lshl_b32 s44,s42,1
s_add_u32 s43,s43,s44
)A"
R"A(ds_read_b64_tr_b16 a[188:189],v22 offset:18184
v_mfma_f32_16x16x32_bf16 v[220:223],a[168:171],a[216:219],v[220:223]
s_addk_i32 m0,1024
s_lshl_b32 s44,s43,18
ds_read_b64_tr_b16 a[190:191],v22 offset:20232
s_waitcnt lgkmcnt(6)
v_mfma_f32_16x16x32_bf16 v[224:227],a[176:179],a[192:195],0
s_mov_b64 s[20:21],s[6:7]
s_mov_b32 s22,524288
ds_read_b64_tr_b16 a[128:129],v22
s_waitcnt lgkmcnt(5)
v_mfma_f32_16x16x32_bf16 v[224:227],a[180:183],a[196:199],v[224:227]
s_add_u32 s20,s20,s44
s_addc_u32 s21,s21,0
ds_read_b64_tr_b16 a[130:131],v22 offset:2048
v_mfma_f32_16x16x32_bf16 v[232:235],a[180:183],a[204:207],0
s_and_b32 s21,s21,0xffff
buffer_load_dwordx4 v58,s[32:35],s56 offen lds
v_mfma_f32_16x16x32_bf16 v[232:235],a[176:179],a[200:203],v[232:235]
s_mov_b32 s23,0x20000
s_mov_b64 s[24:25],s[8:9]
ds_read_b64_tr_b16 a[132:133],v22 offset:16384
v_mfma_f32_16x16x32_bf16 v[240:243],a[176:179],a[208:211],0
s_addk_i32 m0,1024
s_mov_b32 s26,524288
ds_read_b64_tr_b16 a[134:135],v22 offset:18432
v_mfma_f32_16x16x32_bf16 v[240:243],a[180:183],a[212:215],v[240:243]
s_add_u32 s24,s24,s44
s_addc_u32 s25,s25,0
ds_read_b64_tr_b16 a[136:137],v22 offset:8
v_mfma_f32_16x16x32_bf16 v[248:251],a[180:183],a[220:223],0
s_and_b32 s25,s25,0xffff
s_mov_b32 s27,0x20000
ds_read_b64_tr_b16 a[138:139],v22 offset:2056
v_mfma_f32_16x16x32_bf16 v[248:251],a[176:179],a[216:219],v[248:251]
s_lshl_b32 s44,s41,2
buffer_load_dwordx4 v59,s[32:35],s56 offen lds
s_waitcnt lgkmcnt(6)
v_mfma_f32_16x16x32_bf16 v[228:231],a[184:187],a[192:195],0
s_add_u32 s44,s44,s70
s_lshl_b32 s44,s44,3
ds_read_b64_tr_b16 a[140:141],v22 offset:16392
v_mfma_f32_16x16x32_bf16 v[228:231],a[188:191],a[196:199],v[228:231]
s_addk_i32 m0,1024
s_add_u32 s44,s44,s71
ds_read_b64_tr_b16 a[142:143],v22 offset:18440
v_mfma_f32_16x16x32_bf16 v[236:239],a[188:191],a[204:207],0
s_lshl_b32 s44,s44,18
s_mov_b64 s[36:37],s[12:13]
ds_read_b64_tr_b16 a[144:145],v22 offset:256
v_mfma_f32_16x16x32_bf16 v[236:239],a[184:187],a[200:203],v[236:239]
s_add_u32 s36,s36,s44
s_addc_u32 s37,s37,0
ds_read_b64_tr_b16 a[146:147],v22 offset:2304
v_mfma_f32_16x16x32_bf16 v[244:247],a[184:187],a[208:211],0
s_and_b32 s37,s37,0xffff
buffer_load_dwordx4 v60,s[32:35],s56 offen lds
v_mfma_f32_16x16x32_bf16 v[244:247],a[188:191],a[212:215],v[244:247]
s_xor_b32 s59,s59,131072
s_mov_b32 s38,262144
ds_read_b64_tr_b16 a[148:149],v22 offset:16640
v_mfma_f32_16x16x32_bf16 v[252:255],a[188:191],a[220:223],0
s_addk_i32 m0,1024
s_mov_b32 s39,0x20000
ds_read_b64_tr_b16 a[150:151],v22 offset:18688
v_mfma_f32_16x16x32_bf16 v[252:255],a[184:187],a[216:219],v[252:255]
s_lshl_b32 s51,s40,12
s_addk_i32 s57,128
ds_read_b64_tr_b16 a[152:153],v22 offset:264
s_waitcnt lgkmcnt(7)
v_mfma_f32_16x16x32_bf16 a[0:3],a[128:131],a[192:195],0
s_add_u32 s52,s51,16384
s_mul_i32 s47,s41,5
ds_read_b64_tr_b16 a[154:155],v22 offset:2312
v_mfma_f32_16x16x32_bf16 a[0:3],a[132:135],a[196:199],a[0:3]
s_add_u32 s47,s47,s42
buffer_load_dwordx4 v61,s[32:35],s56 offen lds
v_mfma_f32_16x16x32_bf16 a[32:35],a[132:135],a[204:207],0
s_and_b32 s47,s47,15
s_mul_i32 s44,s41,5
ds_read_b64_tr_b16 a[156:157],v22 offset:16648
v_mfma_f32_16x16x32_bf16 a[32:35],a[128:131],a[200:203],a[32:35]
s_addk_i32 m0,1024
s_add_u32 s44,s44,s42
ds_read_b64_tr_b16 a[158:159],v22 offset:18696
v_mfma_f32_16x16x32_bf16 a[64:67],a[128:131],a[208:211],0
s_lshl_b32 s47,s44,12
s_and_b32 s47,s47,0x7000
ds_read_b64_tr_b16 a[160:161],v22 offset:512
v_mfma_f32_16x16x32_bf16 a[64:67],a[132:135],a[212:215],a[64:67]
s_lshl_b32 s44,s44,14
s_and_b32 s44,s44,0x20000
ds_read_b64_tr_b16 a[162:163],v22 offset:2560
v_mfma_f32_16x16x32_bf16 a[96:99],a[132:135],a[220:223],0
s_or_b32 s47,s47,s44
buffer_load_dwordx4 v62,s[32:35],s56 offen lds
v_mfma_f32_16x16x32_bf16 a[96:99],a[128:131],a[216:219],a[96:99]
s_mov_b32 s54,0
s_mov_b32 s61,0x3e0293ee
ds_read_b64_tr_b16 a[164:165],v22 offset:16896
v_mfma_f32_16x16x32_bf16 a[4:7],a[136:139],a[192:195],0
s_addk_i32 m0,1024
s_mov_b32 s63,0x5d800000
ds_read_b64_tr_b16 a[166:167],v22 offset:18944
s_waitcnt lgkmcnt(6)
v_mfma_f32_16x16x32_bf16 a[4:7],a[140:143],a[196:199],a[4:7]
s_lshl_b32 s44,s40,2
s_add_u32 s44,s44,s70
ds_read_b64_tr_b16 a[168:169],v22 offset:520
v_mfma_f32_16x16x32_bf16 a[36:39],a[140:143],a[204:207],0
s_mov_b32 s46,0xff800000
ds_read_b64_tr_b16 a[170:171],v22 offset:2568
v_lshlrev_b32 v70,15,v65
v_mfma_f32_16x16x32_bf16 a[36:39],a[136:139],a[200:203],a[36:39]
s_add_u32 s60,s60,32768
buffer_load_dwordx4 v63,s[32:35],s56 offen lds
v_mfma_f32_16x16x32_bf16 a[68:71],a[136:139],a[208:211],0
s_cmp_eq_u32 s44,14
s_cselect_b32 s18,0x7149f2ca,s46
ds_read_b64_tr_b16 a[172:173],v22 offset:16904
v_mfma_f32_16x16x32_bf16 a[68:71],a[140:143],a[212:215],a[68:71]
s_add_u32 m0,s59,0
ds_read_b64_tr_b16 a[174:175],v22 offset:18952
v_lshl_add_u32 v70,v66,6,v70
v_mfma_f32_16x16x32_bf16 a[100:103],a[140:143],a[220:223],0
s_cmp_eq_u32 s44,13
s_cselect_b32 s19,0x7149f2ca,s46
ds_read_b64_tr_b16 a[176:177],v22 offset:768
v_mfma_f32_16x16x32_bf16 a[100:103],a[136:139],a[216:219],a[100:103]
s_cmp_eq_u32 s44,12
s_cselect_b32 s100,0x7149f2ca,s46
ds_read_b64_tr_b16 a[178:179],v22 offset:2816
v_mfma_f32_16x16x32_bf16 a[8:11],a[144:147],a[192:195],0
s_add_u32 s58,s58,32768
buffer_load_dwordx4 v48,s[28:31],s57 offen lds
v_mfma_f32_16x16x32_bf16 a[8:11],a[148:151],a[196:199],a[8:11]
s_cmp_eq_u32 s44,15
s_cselect_b32 s101,0x7149f2ca,s46
ds_read_b64_tr_b16 a[180:181],v22 offset:17152
v_mfma_f32_16x16x32_bf16 a[40:43],a[148:151],a[204:207],0
s_add_u32 s56,s56,524288
ds_read_b64_tr_b16 a[182:183],v22 offset:19200
v_xor_b32 v71,s40,v65
v_mfma_f32_16x16x32_bf16 a[40:43],a[144:147],a[200:203],a[40:43]
s_mul_i32 s81,s70,1310720
ds_read_b64_tr_b16 a[184:185],v22 offset:776
v_lshl_add_u32 v70,v71,8,v70
v_mfma_f32_16x16x32_bf16 a[72:75],a[144:147],a[208:211],0
s_mul_i32 s44,s71,320
ds_read_b64_tr_b16 a[186:187],v22 offset:2824
v_lshl_add_u32 v4,v64,4,v70
v_mfma_f32_16x16x32_bf16 a[72:75],a[148:151],a[212:215],a[72:75]
s_add_u32 s81,s81,s44
buffer_load_dwordx4 v49,s[28:31],s57 offen offset:1024 lds
v_mfma_f32_16x16x32_bf16 a[104:107],a[148:151],a[220:223],0
s_mul_i32 s82,s40,10240
ds_read_b64_tr_b16 a[188:189],v22 offset:17160
v_xor_b32 v71,2,v64
v_mfma_f32_16x16x32_bf16 a[104:107],a[144:147],a[216:219],a[104:107]
s_mul_i32 s44,s40,40960
ds_read_b64_tr_b16 a[190:191],v22 offset:19208
v_lshl_add_u32 v5,v71,4,v70
v_mfma_f32_16x16x32_bf16 a[12:15],a[152:155],a[192:195],0
s_xor_b32 s59,s59,131072
v_lshlrev_b32 v72,10,v65
v_lshl_add_u32 v72,v64,6,v72
s_waitcnt lgkmcnt(6)
v_mfma_f32_16x16x32_bf16 a[12:15],a[156:159],a[196:199],a[12:15]
s_cmp_eq_u32 s60,131072
s_cselect_b32 s60,32768,s60
v_and_b32 v73,2,v65
v_mfma_f32_16x16x32_bf16 a[44:47],a[156:159],a[204:207],0
s_lshr_b32 s92,%1,3
buffer_load_dwordx4 v50,s[28:31],s57 offen offset:2048 lds
v_mfma_f32_16x16x32_bf16 a[44:47],a[152:155],a[200:203],a[44:47]
s_lshl_b32 s92,s92,2
v_add_u32 v22,s60,v23
v_xor_b32 v20,131072,v20
v_mfma_f32_16x16x32_bf16 a[76:79],a[152:155],a[208:211],0
s_add_u32 s92,s92,s40
v_xor_b32 v21,131072,v21
v_xor_b32 v73,v73,v66
v_mfma_f32_16x16x32_bf16 a[76:79],a[156:159],a[212:215],a[76:79]
s_lshl_b32 s92,s92,7
v_lshl_add_u32 v72,v73,4,v72
v_add_u32 v1,98304,v72
v_mfma_f32_16x16x32_bf16 a[108:111],a[156:159],a[220:223],0
s_add_u32 s93,s92,L_tailseg_%=-L_pc_%=
v_lshlrev_b32 v72,10,v66
v_lshl_add_u32 v72,v65,6,v72
v_mfma_f32_16x16x32_bf16 a[108:111],a[152:155],a[216:219],a[108:111]
s_waitcnt vmcnt(11) lgkmcnt(0)
s_add_u32 s60,s60,32768
buffer_load_dwordx4 v51,s[28:31],s57 offen offset:3072 lds
v_mfma_f32_16x16x32_bf16 a[16:19],a[160:163],a[192:195],0
s_barrier
s_addk_i32 m0,4096
ds_read_b128 a[224:227],v20
v_mfma_f32_16x16x32_bf16 a[16:19],a[164:167],a[196:199],a[16:19]
ds_read_b64_tr_b16 a[128:129],v22 offset:1024
v_lshl_add_u32 v72,v64,3,v72
v_mfma_f32_16x16x32_bf16 a[48:51],a[164:167],a[204:207],0
ds_read_b64_tr_b16 a[130:131],v22 offset:3072
v_add_u32 v72,114688,v72
v_mfma_f32_16x16x32_bf16 a[48:51],a[160:163],a[200:203],a[48:51]
buffer_load_dwordx4 v52,s[28:31],s57 offen lds
v_mfma_f32_16x16x32_bf16 a[80:83],a[160:163],a[208:211],0
s_cmp_ge_u32 s58,131072
s_cselect_b32 s62,98304,0
ds_read_b128 a[228:231],v21
v_mfma_f32_16x16x32_bf16 a[80:83],a[164:167],a[212:215],a[80:83]
s_sub_u32 s58,s58,s62
ds_read_b64_tr_b16 a[132:133],v22 offset:17408
v_and_b32 v73,1,v66
v_mfma_f32_16x16x32_bf16 a[112:115],a[164:167],a[220:223],0
ds_read_b64_tr_b16 a[134:135],v22 offset:19456
v_lshl_add_u32 v2,v73,5,v72
v_mfma_f32_16x16x32_bf16 a[112:115],a[160:163],a[216:219],a[112:115]
ds_read_b128 a[232:235],v20 offset:2048
v_xor_b32 v73,1,v73
v_mfma_f32_16x16x32_bf16 a[20:23],a[168:171],a[192:195],0
buffer_load_dwordx4 v53,s[28:31],s57 offen offset:1024 lds
v_mfma_f32_16x16x32_bf16 a[20:23],a[172:175],a[196:199],a[20:23]
ds_read_b128 a[236:239],v21 offset:2048
v_lshl_add_u32 v3,v73,5,v72
v_mfma_f32_16x16x32_bf16 a[52:55],a[172:175],a[204:207],0
ds_read_b128 a[240:243],v20 offset:4096
v_mov_b32 v73,0x3f803f80
v_mfma_f32_16x16x32_bf16 a[52:55],a[168:171],a[200:203],a[52:55]
ds_read_b128 a[244:247],v21 offset:4096
v_add_u32 v70,0,v12
v_mfma_f32_16x16x32_bf16 a[84:87],a[168:171],a[208:211],0
buffer_load_dword v13,v14,s[64:67],0 offen sc1
v_mfma_f32_16x16x32_bf16 a[84:87],a[172:175],a[212:215],a[84:87]
ds_read_b128 a[248:251],v20 offset:6144
v_mul_u32_u24 v71,3277,v70
v_mfma_f32_16x16x32_bf16 a[116:119],a[172:175],a[220:223],0
ds_read_b128 a[252:255],v21 offset:6144
v_lshrrev_b32 v71,16,v71
v_mfma_f32_16x16x32_bf16 a[116:119],a[168:171],a[216:219],a[116:119]
ds_read_b64_tr_b16 a[136:137],v22 offset:1032
v_mul_u32_u24 v72,20,v71
v_mfma_f32_16x16x32_bf16 a[24:27],a[176:179],a[192:195],0
buffer_load_dwordx4 v54,s[28:31],s57 offen offset:2048 lds
v_mfma_f32_16x16x32_bf16 a[24:27],a[180:183],a[196:199],a[24:27]
ds_read_b64_tr_b16 a[138:139],v22 offset:3080
v_sub_u32 v72,v70,v72
v_mfma_f32_16x16x32_bf16 a[56:59],a[180:183],a[204:207],0
ds_read_b64_tr_b16 a[140:141],v22 offset:17416
v_add_u32 v70,64,v12
v_mfma_f32_16x16x32_bf16 a[56:59],a[176:179],a[200:203],a[56:59]
ds_read_b64_tr_b16 a[142:143],v22 offset:19464
v_lshlrev_b32 v24,10,v6
v_mfma_f32_16x16x32_bf16 a[88:91],a[176:179],a[208:211],0
buffer_load_dwordx4 v55,s[28:31],s57 offen offset:3072 lds
v_mfma_f32_16x16x32_bf16 a[88:91],a[180:183],a[212:215],a[88:91]
s_addk_i32 s57,128
ds_read_b64_tr_b16 a[144:145],v22 offset:1280
v_xor_b32 v20,131072,v20
v_mfma_f32_16x16x32_bf16 a[120:123],a[180:183],a[220:223],0
s_cmp_eq_u32 s60,131072
s_cselect_b32 s60,32768,s60
ds_read_b64_tr_b16 a[146:147],v22 offset:3328
v_mfma_f32_16x16x32_bf16 a[120:123],a[176:179],a[216:219],a[120:123]
ds_read_b64_tr_b16 a[148:149],v22 offset:17664
v_xor_b32 v21,131072,v21
v_mfma_f32_16x16x32_bf16 a[28:31],a[184:187],a[192:195],0
ds_read_b64_tr_b16 a[150:151],v22 offset:19712
v_mfma_f32_16x16x32_bf16 a[28:31],a[188:191],a[196:199],a[28:31]
ds_read_b64_tr_b16 a[152:153],v22 offset:1288
ds_read_b64_tr_b16 a[154:155],v22 offset:3336
ds_read_b64_tr_b16 a[156:157],v22 offset:17672
ds_read_b64_tr_b16 a[158:159],v22 offset:19720
v_mfma_f32_16x16x32_bf16 a[60:63],a[188:191],a[204:207],0
v_mfma_f32_16x16x32_bf16 a[60:63],a[184:187],a[200:203],a[60:63]
v_cmp_gt_u32 vcc,2,v64
s_nop 1
v_cndmask_b32 v28,0,v73,vcc
v_mfma_f32_16x16x32_bf16 a[92:95],a[184:187],a[208:211],0
v_and_b32 v73,1,v64
v_mul_u32_u24 v73,0xaaaa,v73
v_mfma_f32_16x16x32_bf16 a[92:95],a[188:191],a[212:215],a[92:95]
v_add_u32 v73,0x4444,v73
v_lshl_or_b32 v27,v73,16,v73
v_mfma_f32_16x16x32_bf16 a[124:127],a[188:191],a[220:223],0
s_add_u32 m0,s58,0
v_mov_b32 v29,v28
v_mov_b32 v30,v28
v_mfma_f32_16x16x32_bf16 a[124:127],a[184:187],a[216:219],a[124:127]
s_add_u32 s58,s58,32768
ds_read_b64_tr_b16 a[160:161],v22 offset:1536
v_mov_b32 v31,v28
s_waitcnt lgkmcnt(6)
v_mfma_f32_16x16x32_bf16 v[128:131],a[128:131],a[224:227],v[128:131]
ds_read_b64_tr_b16 a[162:163],v22 offset:3584
v_bfe_u32 v73,v71,2,1
v_mfma_f32_16x16x32_bf16 v[128:131],a[132:135],a[228:231],v[128:131]
buffer_load_dwordx4 v56,s[32:35],s56 offen lds
v_mfma_f32_16x16x32_bf16 v[136:139],a[132:135],a[236:239],v[136:139]
)A"
R"A(ds_read_b64_tr_b16 a[164:165],v22 offset:17920
v_lshlrev_b32 v73,1,v73
v_mfma_f32_16x16x32_bf16 v[136:139],a[128:131],a[232:235],v[136:139]
s_addk_i32 m0,1024
ds_read_b64_tr_b16 a[166:167],v22 offset:19968
v_xor_b32 v72,v72,v73
v_mfma_f32_16x16x32_bf16 v[144:147],a[128:131],a[240:243],v[144:147]
ds_read_b64_tr_b16 a[168:169],v22 offset:1544
v_mul_u32_u24 v71,1280,v71
v_mfma_f32_16x16x32_bf16 v[144:147],a[132:135],a[244:247],v[144:147]
ds_read_b64_tr_b16 a[170:171],v22 offset:3592
v_lshl_add_u32 v71,v72,4,v71
v_mfma_f32_16x16x32_bf16 v[152:155],a[132:135],a[252:255],v[152:155]
buffer_load_dwordx4 v57,s[32:35],s56 offen lds
v_mfma_f32_16x16x32_bf16 v[152:155],a[128:131],a[248:251],v[152:155]
ds_read_b64_tr_b16 a[172:173],v22 offset:17928
v_add_u32 v71,s44,v71
v_mfma_f32_16x16x32_bf16 v[132:135],a[136:139],a[224:227],v[132:135]
ds_read_b64_tr_b16 a[174:175],v22 offset:19976
v_subrev_u32 v7,0,v71
v_mfma_f32_16x16x32_bf16 v[132:135],a[140:143],a[228:231],v[132:135]
s_addk_i32 m0,1024
v_mul_u32_u24 v71,3277,v70
v_lshrrev_b32 v71,16,v71
v_mfma_f32_16x16x32_bf16 v[140:143],a[140:143],a[236:239],v[140:143]
ds_read_b64_tr_b16 a[176:177],v22 offset:1792
v_mul_u32_u24 v72,20,v71
v_mfma_f32_16x16x32_bf16 v[140:143],a[136:139],a[232:235],v[140:143]
ds_read_b64_tr_b16 a[178:179],v22 offset:3840
v_sub_u32 v72,v70,v72
v_mfma_f32_16x16x32_bf16 v[148:151],a[136:139],a[240:243],v[148:151]
buffer_load_dwordx4 v58,s[32:35],s56 offen lds
v_mfma_f32_16x16x32_bf16 v[148:151],a[140:143],a[244:247],v[148:151]
ds_read_b64_tr_b16 a[180:181],v22 offset:18176
v_bfe_u32 v73,v71,2,1
v_mfma_f32_16x16x32_bf16 v[156:159],a[140:143],a[252:255],v[156:159]
ds_read_b64_tr_b16 a[182:183],v22 offset:20224
v_lshlrev_b32 v73,1,v73
v_mfma_f32_16x16x32_bf16 v[156:159],a[136:139],a[248:251],v[156:159]
s_addk_i32 m0,1024
v_xor_b32 v72,v72,v73
v_mul_u32_u24 v71,1280,v71
v_mfma_f32_16x16x32_bf16 v[160:163],a[144:147],a[224:227],v[160:163]
s_waitcnt lgkmcnt(4)
ds_read_b64_tr_b16 a[184:185],v22 offset:1800
v_lshl_add_u32 v71,v72,4,v71
v_mfma_f32_16x16x32_bf16 v[160:163],a[148:151],a[228:231],v[160:163]
ds_read_b64_tr_b16 a[186:187],v22 offset:3848
v_add_u32 v71,s44,v71
v_mfma_f32_16x16x32_bf16 v[168:171],a[148:151],a[236:239],v[168:171]
buffer_load_dwordx4 v59,s[32:35],s56 offen lds
v_mfma_f32_16x16x32_bf16 v[168:171],a[144:147],a[232:235],v[168:171]
ds_read_b64_tr_b16 a[188:189],v22 offset:18184
v_subrev_u32 v8,1024,v71
v_mfma_f32_16x16x32_bf16 v[176:179],a[144:147],a[240:243],v[176:179]
s_addk_i32 m0,1024
ds_read_b64_tr_b16 a[190:191],v22 offset:20232
v_add_u32 v70,128,v12
v_mfma_f32_16x16x32_bf16 v[176:179],a[148:151],a[244:247],v[176:179]
v_mul_u32_u24 v71,3277,v70
v_lshrrev_b32 v71,16,v71
v_mfma_f32_16x16x32_bf16 v[184:187],a[148:151],a[252:255],v[184:187]
v_mul_u32_u24 v72,20,v71
v_sub_u32 v72,v70,v72
v_mfma_f32_16x16x32_bf16 v[184:187],a[144:147],a[248:251],v[184:187]
buffer_load_dwordx4 v60,s[32:35],s56 offen lds
v_mfma_f32_16x16x32_bf16 v[164:167],a[152:155],a[224:227],v[164:167]
ds_read_b64_tr_b16 a[128:129],v22
v_bfe_u32 v73,v71,2,1
v_mfma_f32_16x16x32_bf16 v[164:167],a[156:159],a[228:231],v[164:167]
ds_read_b64_tr_b16 a[130:131],v22 offset:2048
v_lshlrev_b32 v73,1,v73
v_mfma_f32_16x16x32_bf16 v[172:175],a[156:159],a[236:239],v[172:175]
s_addk_i32 m0,1024
ds_read_b64_tr_b16 a[132:133],v22 offset:16384
v_xor_b32 v72,v72,v73
v_mfma_f32_16x16x32_bf16 v[172:175],a[152:155],a[232:235],v[172:175]
ds_read_b64_tr_b16 a[134:135],v22 offset:18432
v_mul_u32_u24 v71,1280,v71
v_mfma_f32_16x16x32_bf16 v[180:183],a[152:155],a[240:243],v[180:183]
v_lshl_add_u32 v71,v72,4,v71
v_add_u32 v71,s44,v71
v_mfma_f32_16x16x32_bf16 v[180:183],a[156:159],a[244:247],v[180:183]
buffer_load_dwordx4 v61,s[32:35],s56 offen lds
v_mfma_f32_16x16x32_bf16 v[188:191],a[156:159],a[252:255],v[188:191]
ds_read_b64_tr_b16 a[136:137],v22 offset:8
v_subrev_u32 v9,2048,v71
v_mfma_f32_16x16x32_bf16 v[188:191],a[152:155],a[248:251],v[188:191]
ds_read_b64_tr_b16 a[138:139],v22 offset:2056
v_add_u32 v70,192,v12
v_mfma_f32_16x16x32_bf16 v[192:195],a[160:163],a[224:227],v[192:195]
s_addk_i32 m0,1024
ds_read_b64_tr_b16 a[140:141],v22 offset:16392
v_mul_u32_u24 v71,3277,v70
v_mfma_f32_16x16x32_bf16 v[192:195],a[164:167],a[228:231],v[192:195]
ds_read_b64_tr_b16 a[142:143],v22 offset:18440
v_lshrrev_b32 v71,16,v71
v_mfma_f32_16x16x32_bf16 v[200:203],a[164:167],a[236:239],v[200:203]
v_mul_u32_u24 v72,20,v71
v_sub_u32 v72,v70,v72
v_mfma_f32_16x16x32_bf16 v[200:203],a[160:163],a[232:235],v[200:203]
buffer_load_dwordx4 v62,s[32:35],s56 offen lds
v_mfma_f32_16x16x32_bf16 v[208:211],a[160:163],a[240:243],v[208:211]
ds_read_b64_tr_b16 a[144:145],v22 offset:256
v_bfe_u32 v73,v71,2,1
v_mfma_f32_16x16x32_bf16 v[208:211],a[164:167],a[244:247],v[208:211]
s_addk_i32 m0,1024
ds_read_b64_tr_b16 a[146:147],v22 offset:2304
v_lshlrev_b32 v73,1,v73
v_mfma_f32_16x16x32_bf16 v[216:219],a[164:167],a[252:255],v[216:219]
ds_read_b64_tr_b16 a[148:149],v22 offset:16640
v_xor_b32 v72,v72,v73
v_mfma_f32_16x16x32_bf16 v[216:219],a[160:163],a[248:251],v[216:219]
ds_read_b64_tr_b16 a[150:151],v22 offset:18688
v_mul_u32_u24 v71,1280,v71
v_mfma_f32_16x16x32_bf16 v[196:199],a[168:171],a[224:227],v[196:199]
buffer_load_dwordx4 v63,s[32:35],s56 offen lds
v_mfma_f32_16x16x32_bf16 v[196:199],a[172:175],a[228:231],v[196:199]
s_add_u32 s56,s56,524288
v_lshl_add_u32 v71,v72,4,v71
v_add_u32 v71,s44,v71
v_mfma_f32_16x16x32_bf16 v[204:207],a[172:175],a[236:239],v[204:207]
v_subrev_u32 v10,3072,v71
v_add_u32 v70,256,v12
v_mfma_f32_16x16x32_bf16 v[204:207],a[168:171],a[232:235],v[204:207]
s_add_u32 m0,s59,0
ds_read_b64_tr_b16 a[152:153],v22 offset:264
v_mul_u32_u24 v71,3277,v70
v_mfma_f32_16x16x32_bf16 v[212:215],a[168:171],a[240:243],v[212:215]
s_xor_b32 s59,s59,131072
ds_read_b64_tr_b16 a[154:155],v22 offset:2312
v_lshrrev_b32 v71,16,v71
v_mfma_f32_16x16x32_bf16 v[212:215],a[172:175],a[244:247],v[212:215]
ds_read_b64_tr_b16 a[156:157],v22 offset:16648
v_mul_u32_u24 v72,20,v71
v_mfma_f32_16x16x32_bf16 v[220:223],a[172:175],a[252:255],v[220:223]
buffer_load_dwordx4 v48,s[28:31],s57 offen lds
v_mfma_f32_16x16x32_bf16 v[220:223],a[168:171],a[248:251],v[220:223]
s_waitcnt lgkmcnt(3)
ds_read_b64_tr_b16 a[158:159],v22 offset:18696
v_sub_u32 v72,v70,v72
v_mfma_f32_16x16x32_bf16 v[224:227],a[176:179],a[224:227],v[224:227]
v_bfe_u32 v73,v71,2,1
v_lshlrev_b32 v73,1,v73
v_mfma_f32_16x16x32_bf16 v[224:227],a[180:183],a[228:231],v[224:227]
ds_read_b64_tr_b16 a[160:161],v22 offset:512
v_xor_b32 v72,v72,v73
v_mfma_f32_16x16x32_bf16 v[232:235],a[180:183],a[236:239],v[232:235]
ds_read_b64_tr_b16 a[162:163],v22 offset:2560
v_mul_u32_u24 v71,1280,v71
v_mfma_f32_16x16x32_bf16 v[232:235],a[176:179],a[232:235],v[232:235]
ds_read_b64_tr_b16 a[164:165],v22 offset:16896
v_lshl_add_u32 v71,v72,4,v71
v_mfma_f32_16x16x32_bf16 v[240:243],a[176:179],a[240:243],v[240:243]
buffer_load_dwordx4 v49,s[28:31],s57 offen offset:1024 lds
v_mfma_f32_16x16x32_bf16 v[240:243],a[180:183],a[244:247],v[240:243]
ds_read_b64_tr_b16 a[166:167],v22 offset:18944
v_add_u32 v71,s44,v71
v_mfma_f32_16x16x32_bf16 v[248:251],a[180:183],a[252:255],v[248:251]
ds_read_b64_tr_b16 a[168:169],v22 offset:520
v_subrev_u32 v11,0,v71
v_mfma_f32_16x16x32_bf16 v[248:251],a[176:179],a[248:251],v[248:251]
s_cmp_ge_u32 s58,131072
s_cselect_b32 s62,98304,0
ds_read_b64_tr_b16 a[170:171],v22 offset:2568
v_mfma_f32_16x16x32_bf16 v[228:231],a[184:187],a[224:227],v[228:231]
s_sub_u32 s58,s58,s62
ds_read_b64_tr_b16 a[172:173],v22 offset:16904
v_mfma_f32_16x16x32_bf16 v[228:231],a[188:191],a[228:231],v[228:231]
buffer_load_dwordx4 v50,s[28:31],s57 offen offset:2048 lds
v_mfma_f32_16x16x32_bf16 v[236:239],a[188:191],a[236:239],v[236:239]
ds_read_b64_tr_b16 a[174:175],v22 offset:18952
v_mfma_f32_16x16x32_bf16 v[236:239],a[184:187],a[232:235],v[236:239]
ds_read_b64_tr_b16 a[176:177],v22 offset:768
v_mfma_f32_16x16x32_bf16 v[244:247],a[184:187],a[240:243],v[244:247]
ds_read_b64_tr_b16 a[178:179],v22 offset:2816
v_mfma_f32_16x16x32_bf16 v[244:247],a[188:191],a[244:247],v[244:247]
ds_read_b64_tr_b16 a[180:181],v22 offset:17152
v_mfma_f32_16x16x32_bf16 v[252:255],a[188:191],a[252:255],v[252:255]
ds_read_b64_tr_b16 a[182:183],v22 offset:19200
v_mfma_f32_16x16x32_bf16 v[252:255],a[184:187],a[248:251],v[252:255]
buffer_load_dwordx4 v51,s[28:31],s57 offen offset:3072 lds
v_mfma_f32_16x16x32_bf16 a[0:3],a[128:131],a[224:227],a[0:3]
ds_read_b64_tr_b16 a[184:185],v22 offset:776
v_mfma_f32_16x16x32_bf16 a[0:3],a[132:135],a[228:231],a[0:3]
ds_read_b64_tr_b16 a[186:187],v22 offset:2824
v_mfma_f32_16x16x32_bf16 a[32:35],a[132:135],a[236:239],a[32:35]
s_addk_i32 m0,4096
ds_read_b64_tr_b16 a[188:189],v22 offset:17160
v_mfma_f32_16x16x32_bf16 a[32:35],a[128:131],a[232:235],a[32:35]
ds_read_b64_tr_b16 a[190:191],v22 offset:19208
v_add_u32 v22,s60,v23
v_mfma_f32_16x16x32_bf16 a[64:67],a[128:131],a[240:243],a[64:67]
v_mfma_f32_16x16x32_bf16 a[64:67],a[132:135],a[244:247],a[64:67]
buffer_load_dwordx4 v52,s[28:31],s57 offen lds
v_mfma_f32_16x16x32_bf16 a[96:99],a[132:135],a[252:255],a[96:99]
v_mfma_f32_16x16x32_bf16 a[96:99],a[128:131],a[248:251],a[96:99]
v_mfma_f32_16x16x32_bf16 a[4:7],a[136:139],a[224:227],a[4:7]
v_mfma_f32_16x16x32_bf16 a[4:7],a[140:143],a[228:231],a[4:7]
v_mfma_f32_16x16x32_bf16 a[36:39],a[140:143],a[236:239],a[36:39]
buffer_load_dwordx4 v53,s[28:31],s57 offen offset:1024 lds
v_mfma_f32_16x16x32_bf16 a[36:39],a[136:139],a[232:235],a[36:39]
v_mfma_f32_16x16x32_bf16 a[68:71],a[136:139],a[240:243],a[68:71]
v_mfma_f32_16x16x32_bf16 a[68:71],a[140:143],a[244:247],a[68:71]
v_mfma_f32_16x16x32_bf16 a[100:103],a[140:143],a[252:255],a[100:103]
v_mfma_f32_16x16x32_bf16 a[100:103],a[136:139],a[248:251],a[100:103]
v_mfma_f32_16x16x32_bf16 a[8:11],a[144:147],a[224:227],a[8:11]
buffer_load_dwordx4 v54,s[28:31],s57 offen offset:2048 lds
v_mfma_f32_16x16x32_bf16 a[8:11],a[148:151],a[228:231],a[8:11]
v_mfma_f32_16x16x32_bf16 a[40:43],a[148:151],a[236:239],a[40:43]
v_mfma_f32_16x16x32_bf16 a[40:43],a[144:147],a[232:235],a[40:43]
v_mfma_f32_16x16x32_bf16 a[72:75],a[144:147],a[240:243],a[72:75]
v_mfma_f32_16x16x32_bf16 a[72:75],a[148:151],a[244:247],a[72:75]
v_mfma_f32_16x16x32_bf16 a[104:107],a[148:151],a[252:255],a[104:107]
buffer_load_dwordx4 v55,s[28:31],s57 offen offset:3072 lds
v_mfma_f32_16x16x32_bf16 a[104:107],a[144:147],a[248:251],a[104:107]
s_addk_i32 s57,128
s_waitcnt lgkmcnt(0)
v_mfma_f32_16x16x32_bf16 a[12:15],a[152:155],a[224:227],a[12:15]
v_mfma_f32_16x16x32_bf16 a[12:15],a[156:159],a[228:231],a[12:15]
v_mfma_f32_16x16x32_bf16 a[44:47],a[156:159],a[236:239],a[44:47]
v_mfma_f32_16x16x32_bf16 a[44:47],a[152:155],a[232:235],a[44:47]
v_mfma_f32_16x16x32_bf16 a[76:79],a[152:155],a[240:243],a[76:79]
v_mfma_f32_16x16x32_bf16 a[76:79],a[156:159],a[244:247],a[76:79]
v_mfma_f32_16x16x32_bf16 a[108:111],a[156:159],a[252:255],a[108:111]
v_mfma_f32_16x16x32_bf16 a[108:111],a[152:155],a[248:251],a[108:111]
s_waitcnt vmcnt(16)
v_mfma_f32_16x16x32_bf16 a[16:19],a[160:163],a[224:227],a[16:19]
s_barrier
ds_read_b128 a[192:195],v20
v_mfma_f32_16x16x32_bf16 a[16:19],a[164:167],a[228:231],a[16:19]
ds_read_b128 a[196:199],v21
v_mfma_f32_16x16x32_bf16 a[48:51],a[164:167],a[236:239],a[48:51]
ds_read_b128 a[200:203],v20 offset:2048
v_mfma_f32_16x16x32_bf16 a[48:51],a[160:163],a[232:235],a[48:51]
ds_read_b128 a[204:207],v21 offset:2048
v_mfma_f32_16x16x32_bf16 a[80:83],a[160:163],a[240:243],a[80:83]
ds_read_b128 a[208:211],v20 offset:4096
v_mfma_f32_16x16x32_bf16 a[80:83],a[164:167],a[244:247],a[80:83]
ds_read_b128 a[212:215],v21 offset:4096
v_mfma_f32_16x16x32_bf16 a[112:115],a[164:167],a[252:255],a[112:115]
ds_read_b128 a[216:219],v20 offset:6144
v_mfma_f32_16x16x32_bf16 a[112:115],a[160:163],a[248:251],a[112:115]
ds_read_b128 a[220:223],v21 offset:6144
v_mfma_f32_16x16x32_bf16 a[20:23],a[168:171],a[224:227],a[20:23]
ds_read_b64_tr_b16 a[128:129],v22 offset:1024
v_mfma_f32_16x16x32_bf16 a[20:23],a[172:175],a[228:231],a[20:23]
ds_read_b64_tr_b16 a[130:131],v22 offset:3072
v_mfma_f32_16x16x32_bf16 a[52:55],a[172:175],a[236:239],a[52:55]
)A"
R"A(ds_read_b64_tr_b16 a[132:133],v22 offset:17408
v_mfma_f32_16x16x32_bf16 a[52:55],a[168:171],a[232:235],a[52:55]
ds_read_b64_tr_b16 a[134:135],v22 offset:19456
v_mfma_f32_16x16x32_bf16 a[84:87],a[168:171],a[240:243],a[84:87]
ds_read_b64_tr_b16 a[136:137],v22 offset:1032
v_mfma_f32_16x16x32_bf16 a[84:87],a[172:175],a[244:247],a[84:87]
ds_read_b64_tr_b16 a[138:139],v22 offset:3080
v_mfma_f32_16x16x32_bf16 a[116:119],a[172:175],a[252:255],a[116:119]
ds_read_b64_tr_b16 a[140:141],v22 offset:17416
v_mfma_f32_16x16x32_bf16 a[116:119],a[168:171],a[248:251],a[116:119]
ds_read_b64_tr_b16 a[142:143],v22 offset:19464
v_mfma_f32_16x16x32_bf16 a[24:27],a[176:179],a[224:227],a[24:27]
ds_read_b64_tr_b16 a[144:145],v22 offset:1280
v_mfma_f32_16x16x32_bf16 a[24:27],a[180:183],a[228:231],a[24:27]
ds_read_b64_tr_b16 a[146:147],v22 offset:3328
v_mfma_f32_16x16x32_bf16 a[56:59],a[180:183],a[236:239],a[56:59]
ds_read_b64_tr_b16 a[148:149],v22 offset:17664
v_mfma_f32_16x16x32_bf16 a[56:59],a[176:179],a[232:235],a[56:59]
ds_read_b64_tr_b16 a[150:151],v22 offset:19712
v_mfma_f32_16x16x32_bf16 a[88:91],a[176:179],a[240:243],a[88:91]
ds_read_b64_tr_b16 a[152:153],v22 offset:1288
v_mfma_f32_16x16x32_bf16 a[88:91],a[180:183],a[244:247],a[88:91]
ds_read_b64_tr_b16 a[154:155],v22 offset:3336
v_mfma_f32_16x16x32_bf16 a[120:123],a[180:183],a[252:255],a[120:123]
ds_read_b64_tr_b16 a[156:157],v22 offset:17672
v_mfma_f32_16x16x32_bf16 a[120:123],a[176:179],a[248:251],a[120:123]
ds_read_b64_tr_b16 a[158:159],v22 offset:19720
v_mfma_f32_16x16x32_bf16 a[28:31],a[184:187],a[224:227],a[28:31]
v_mfma_f32_16x16x32_bf16 a[28:31],a[188:191],a[228:231],a[28:31]
v_mfma_f32_16x16x32_bf16 a[60:63],a[188:191],a[236:239],a[60:63]
v_mfma_f32_16x16x32_bf16 a[60:63],a[184:187],a[232:235],a[60:63]
v_mfma_f32_16x16x32_bf16 a[92:95],a[184:187],a[240:243],a[92:95]
v_mfma_f32_16x16x32_bf16 a[92:95],a[188:191],a[244:247],a[92:95]
v_mfma_f32_16x16x32_bf16 a[124:127],a[188:191],a[252:255],a[124:127]
v_mfma_f32_16x16x32_bf16 a[124:127],a[184:187],a[248:251],a[124:127]
s_mov_b32 s53,3
.p2align 5
L_qp_%=:
s_waitcnt lgkmcnt(1)
v_mfma_f32_16x16x32_bf16 v[128:131],a[128:131],a[192:195],v[128:131]
s_add_u32 m0,s58,0
s_mov_b32 s86,s87
ds_read_b64_tr_b16 a[160:161],v22 offset:1536
v_mfma_f32_16x16x32_bf16 v[128:131],a[132:135],a[196:199],v[128:131]
s_add_u32 s60,s60,32768
buffer_load_dwordx4 v56,s[32:35],s56 offen lds
v_mfma_f32_16x16x32_bf16 v[136:139],a[132:135],a[204:207],v[136:139]
s_cmp_eq_u32 s60,131072
s_cselect_b32 s60,32768,s60
ds_read_b64_tr_b16 a[162:163],v22 offset:3584
v_mfma_f32_16x16x32_bf16 v[136:139],a[128:131],a[200:203],v[136:139]
s_load_dword s86,s[84:85],0x0 glc
s_addk_i32 m0,1024
ds_read_b64_tr_b16 a[164:165],v22 offset:17920
v_mfma_f32_16x16x32_bf16 v[144:147],a[128:131],a[208:211],v[144:147]
s_add_u32 s58,s58,32768
ds_read_b64_tr_b16 a[166:167],v22 offset:19968
v_xor_b32 v20,131072,v20
v_mfma_f32_16x16x32_bf16 v[144:147],a[132:135],a[212:215],v[144:147]
s_mov_b32 s90,L_att0x_%=-L_pc_%=+128
ds_read_b64_tr_b16 a[168:169],v22 offset:1544
v_xor_b32 v21,131072,v21
v_mfma_f32_16x16x32_bf16 v[152:155],a[132:135],a[220:223],v[152:155]
s_sub_u32 s53,s53,1
buffer_load_dwordx4 v57,s[32:35],s56 offen lds
v_mfma_f32_16x16x32_bf16 v[152:155],a[128:131],a[216:219],v[152:155]
s_cmp_ge_u32 s58,131072
s_cselect_b32 s62,98304,0
ds_read_b64_tr_b16 a[170:171],v22 offset:3592
v_mfma_f32_16x16x32_bf16 v[132:135],a[136:139],a[192:195],v[132:135]
s_sub_u32 s58,s58,s62
ds_read_b64_tr_b16 a[172:173],v22 offset:17928
v_mfma_f32_16x16x32_bf16 v[132:135],a[140:143],a[196:199],v[132:135]
s_addk_i32 m0,1024
ds_read_b64_tr_b16 a[174:175],v22 offset:19976
v_mfma_f32_16x16x32_bf16 v[140:143],a[140:143],a[204:207],v[140:143]
ds_read_b64_tr_b16 a[176:177],v22 offset:1792
v_mfma_f32_16x16x32_bf16 v[140:143],a[136:139],a[200:203],v[140:143]
ds_read_b64_tr_b16 a[178:179],v22 offset:3840
v_mfma_f32_16x16x32_bf16 v[148:151],a[136:139],a[208:211],v[148:151]
buffer_load_dwordx4 v58,s[32:35],s56 offen lds
v_mfma_f32_16x16x32_bf16 v[148:151],a[140:143],a[212:215],v[148:151]
ds_read_b64_tr_b16 a[180:181],v22 offset:18176
v_mfma_f32_16x16x32_bf16 v[156:159],a[140:143],a[220:223],v[156:159]
s_cmp_eq_u32 s86,s87
s_addc_u32 s79,s79,0
s_addk_i32 m0,1024
v_mfma_f32_16x16x32_bf16 v[156:159],a[136:139],a[216:219],v[156:159]
ds_read_b64_tr_b16 a[182:183],v22 offset:20224
v_mfma_f32_16x16x32_bf16 v[160:163],a[144:147],a[192:195],v[160:163]
ds_read_b64_tr_b16 a[184:185],v22 offset:1800
v_mfma_f32_16x16x32_bf16 v[160:163],a[148:151],a[196:199],v[160:163]
s_cmp_eq_u32 s86,s87
s_addc_u32 s79,s79,0
ds_read_b64_tr_b16 a[186:187],v22 offset:3848
v_mfma_f32_16x16x32_bf16 v[168:171],a[148:151],a[204:207],v[168:171]
buffer_load_dwordx4 v59,s[32:35],s56 offen lds
v_mfma_f32_16x16x32_bf16 v[168:171],a[144:147],a[200:203],v[168:171]
ds_read_b64_tr_b16 a[188:189],v22 offset:18184
v_mfma_f32_16x16x32_bf16 v[176:179],a[144:147],a[208:211],v[176:179]
s_cmp_eq_u32 s86,s87
s_addc_u32 s79,s79,0
s_addk_i32 m0,1024
v_mfma_f32_16x16x32_bf16 v[176:179],a[148:151],a[212:215],v[176:179]
ds_read_b64_tr_b16 a[190:191],v22 offset:20232
v_mfma_f32_16x16x32_bf16 v[184:187],a[148:151],a[220:223],v[184:187]
ds_read_b64_tr_b16 a[128:129],v22
v_mfma_f32_16x16x32_bf16 v[184:187],a[144:147],a[216:219],v[184:187]
s_cmp_eq_u32 s86,s87
s_addc_u32 s79,s79,0
ds_read_b64_tr_b16 a[130:131],v22 offset:2048
v_mfma_f32_16x16x32_bf16 v[164:167],a[152:155],a[192:195],v[164:167]
s_waitcnt lgkmcnt(5)
buffer_load_dwordx4 v60,s[32:35],s56 offen lds
v_mfma_f32_16x16x32_bf16 v[164:167],a[156:159],a[196:199],v[164:167]
s_addk_i32 m0,1024
ds_read_b64_tr_b16 a[132:133],v22 offset:16384
v_mfma_f32_16x16x32_bf16 v[172:175],a[156:159],a[204:207],v[172:175]
s_cmp_eq_u32 s86,s87
s_addc_u32 s79,s79,0
ds_read_b64_tr_b16 a[134:135],v22 offset:18432
v_mfma_f32_16x16x32_bf16 v[172:175],a[152:155],a[200:203],v[172:175]
ds_read_b64_tr_b16 a[136:137],v22 offset:8
v_mfma_f32_16x16x32_bf16 v[180:183],a[152:155],a[208:211],v[180:183]
buffer_load_dwordx4 v61,s[32:35],s56 offen lds
v_mfma_f32_16x16x32_bf16 v[180:183],a[156:159],a[212:215],v[180:183]
s_cmp_eq_u32 s86,s87
s_addc_u32 s79,s79,0
ds_read_b64_tr_b16 a[138:139],v22 offset:2056
v_mfma_f32_16x16x32_bf16 v[188:191],a[156:159],a[220:223],v[188:191]
ds_read_b64_tr_b16 a[140:141],v22 offset:16392
v_mfma_f32_16x16x32_bf16 v[188:191],a[152:155],a[216:219],v[188:191]
s_addk_i32 m0,1024
ds_read_b64_tr_b16 a[142:143],v22 offset:18440
v_mfma_f32_16x16x32_bf16 v[192:195],a[160:163],a[192:195],v[192:195]
s_cmp_eq_u32 s86,s87
s_addc_u32 s79,s79,0
ds_read_b64_tr_b16 a[144:145],v22 offset:256
v_mfma_f32_16x16x32_bf16 v[192:195],a[164:167],a[196:199],v[192:195]
ds_read_b64_tr_b16 a[146:147],v22 offset:2304
v_mfma_f32_16x16x32_bf16 v[200:203],a[164:167],a[204:207],v[200:203]
buffer_load_dwordx4 v62,s[32:35],s56 offen lds
v_mfma_f32_16x16x32_bf16 v[200:203],a[160:163],a[200:203],v[200:203]
s_cmp_eq_u32 s86,s87
s_addc_u32 s79,s79,0
ds_read_b64_tr_b16 a[148:149],v22 offset:16640
v_mfma_f32_16x16x32_bf16 v[208:211],a[160:163],a[208:211],v[208:211]
s_addk_i32 m0,1024
ds_read_b64_tr_b16 a[150:151],v22 offset:18688
v_mfma_f32_16x16x32_bf16 v[208:211],a[164:167],a[212:215],v[208:211]
ds_read_b64_tr_b16 a[152:153],v22 offset:264
v_mfma_f32_16x16x32_bf16 v[216:219],a[164:167],a[220:223],v[216:219]
s_cmp_eq_u32 s86,s87
s_addc_u32 s79,s79,0
ds_read_b64_tr_b16 a[154:155],v22 offset:2312
v_mfma_f32_16x16x32_bf16 v[216:219],a[160:163],a[216:219],v[216:219]
buffer_load_dwordx4 v63,s[32:35],s56 offen lds
v_mfma_f32_16x16x32_bf16 v[196:199],a[168:171],a[192:195],v[196:199]
s_add_u32 s56,s56,524288
ds_read_b64_tr_b16 a[156:157],v22 offset:16648
v_mfma_f32_16x16x32_bf16 v[196:199],a[172:175],a[196:199],v[196:199]
s_cmp_eq_u32 s86,s87
s_addc_u32 s79,s79,0
ds_read_b64_tr_b16 a[158:159],v22 offset:18696
v_mfma_f32_16x16x32_bf16 v[204:207],a[172:175],a[204:207],v[204:207]
s_add_u32 m0,s59,0
s_xor_b32 s59,s59,131072
ds_read_b64_tr_b16 a[160:161],v22 offset:512
v_mfma_f32_16x16x32_bf16 v[204:207],a[168:171],a[200:203],v[204:207]
ds_read_b64_tr_b16 a[162:163],v22 offset:2560
v_mfma_f32_16x16x32_bf16 v[212:215],a[168:171],a[208:211],v[212:215]
s_cmp_eq_u32 s86,s87
s_addc_u32 s79,s79,0
ds_read_b64_tr_b16 a[164:165],v22 offset:16896
v_mfma_f32_16x16x32_bf16 v[212:215],a[172:175],a[212:215],v[212:215]
buffer_load_dwordx4 v48,s[28:31],s57 offen lds
v_mfma_f32_16x16x32_bf16 v[220:223],a[172:175],a[220:223],v[220:223]
ds_read_b64_tr_b16 a[166:167],v22 offset:18944
v_mfma_f32_16x16x32_bf16 v[220:223],a[168:171],a[216:219],v[220:223]
s_cmp_eq_u32 s86,s87
s_addc_u32 s79,s79,0
ds_read_b64_tr_b16 a[168:169],v22 offset:520
v_mfma_f32_16x16x32_bf16 v[224:227],a[176:179],a[192:195],v[224:227]
ds_read_b64_tr_b16 a[170:171],v22 offset:2568
v_mfma_f32_16x16x32_bf16 v[224:227],a[180:183],a[196:199],v[224:227]
ds_read_b64_tr_b16 a[172:173],v22 offset:16904
v_mfma_f32_16x16x32_bf16 v[232:235],a[180:183],a[204:207],v[232:235]
s_cmp_eq_u32 s86,s87
s_addc_u32 s79,s79,0
ds_read_b64_tr_b16 a[174:175],v22 offset:18952
v_mfma_f32_16x16x32_bf16 v[232:235],a[176:179],a[200:203],v[232:235]
buffer_load_dwordx4 v49,s[28:31],s57 offen offset:1024 lds
v_mfma_f32_16x16x32_bf16 v[240:243],a[176:179],a[208:211],v[240:243]
v_mfma_f32_16x16x32_bf16 v[240:243],a[180:183],a[212:215],v[240:243]
s_cmp_eq_u32 s86,s87
s_addc_u32 s79,s79,0
v_mfma_f32_16x16x32_bf16 v[248:251],a[180:183],a[220:223],v[248:251]
ds_read_b64_tr_b16 a[180:181],v22 offset:17152
v_mfma_f32_16x16x32_bf16 v[248:251],a[176:179],a[216:219],v[248:251]
s_waitcnt lgkmcnt(4)
ds_read_b64_tr_b16 a[176:177],v22 offset:768
v_mfma_f32_16x16x32_bf16 v[228:231],a[184:187],a[192:195],v[228:231]
s_cmp_eq_u32 s86,s87
s_addc_u32 s79,s79,0
ds_read_b64_tr_b16 a[178:179],v22 offset:2816
v_mfma_f32_16x16x32_bf16 v[228:231],a[188:191],a[196:199],v[228:231]
buffer_load_dwordx4 v50,s[28:31],s57 offen offset:2048 lds
v_mfma_f32_16x16x32_bf16 v[236:239],a[188:191],a[204:207],v[236:239]
ds_read_b64_tr_b16 a[182:183],v22 offset:19200
v_mfma_f32_16x16x32_bf16 v[236:239],a[184:187],a[200:203],v[236:239]
s_cmp_eq_u32 s86,s87
s_addc_u32 s79,s79,0
v_mfma_f32_16x16x32_bf16 v[244:247],a[184:187],a[208:211],v[244:247]
v_mfma_f32_16x16x32_bf16 v[244:247],a[188:191],a[212:215],v[244:247]
buffer_load_dwordx4 v51,s[28:31],s57 offen offset:3072 lds
v_mfma_f32_16x16x32_bf16 v[252:255],a[188:191],a[220:223],v[252:255]
ds_read_b64_tr_b16 a[188:189],v22 offset:17160
v_mfma_f32_16x16x32_bf16 v[252:255],a[184:187],a[216:219],v[252:255]
ds_read_b64_tr_b16 a[184:185],v22 offset:776
v_mfma_f32_16x16x32_bf16 a[0:3],a[128:131],a[192:195],a[0:3]
s_addk_i32 m0,4096
ds_read_b64_tr_b16 a[186:187],v22 offset:2824
v_mfma_f32_16x16x32_bf16 a[0:3],a[132:135],a[196:199],a[0:3]
ds_read_b64_tr_b16 a[190:191],v22 offset:19208
v_add_u32 v22,s60,v23
v_mfma_f32_16x16x32_bf16 a[32:35],a[132:135],a[204:207],a[32:35]
s_add_u32 s60,s60,32768
s_cmp_eq_u32 s60,131072
s_cselect_b32 s60,32768,s60
v_mfma_f32_16x16x32_bf16 a[32:35],a[128:131],a[200:203],a[32:35]
buffer_load_dwordx4 v52,s[28:31],s57 offen lds
v_mfma_f32_16x16x32_bf16 a[64:67],a[128:131],a[208:211],a[64:67]
v_mfma_f32_16x16x32_bf16 a[64:67],a[132:135],a[212:215],a[64:67]
v_mfma_f32_16x16x32_bf16 a[96:99],a[132:135],a[220:223],a[96:99]
v_mfma_f32_16x16x32_bf16 a[96:99],a[128:131],a[216:219],a[96:99]
v_mfma_f32_16x16x32_bf16 a[4:7],a[136:139],a[192:195],a[4:7]
buffer_load_dwordx4 v53,s[28:31],s57 offen offset:1024 lds
v_mfma_f32_16x16x32_bf16 a[4:7],a[140:143],a[196:199],a[4:7]
v_mfma_f32_16x16x32_bf16 a[36:39],a[140:143],a[204:207],a[36:39]
v_mfma_f32_16x16x32_bf16 a[36:39],a[136:139],a[200:203],a[36:39]
v_mfma_f32_16x16x32_bf16 a[68:71],a[136:139],a[208:211],a[68:71]
v_mfma_f32_16x16x32_bf16 a[68:71],a[140:143],a[212:215],a[68:71]
v_mfma_f32_16x16x32_bf16 a[100:103],a[140:143],a[220:223],a[100:103]
buffer_load_dwordx4 v54,s[28:31],s57 offen offset:2048 lds
v_mfma_f32_16x16x32_bf16 a[100:103],a[136:139],a[216:219],a[100:103]
v_mfma_f32_16x16x32_bf16 a[8:11],a[144:147],a[192:195],a[8:11]
v_mfma_f32_16x16x32_bf16 a[8:11],a[148:151],a[196:199],a[8:11]
)A"
R"A(v_mfma_f32_16x16x32_bf16 a[40:43],a[148:151],a[204:207],a[40:43]
v_mfma_f32_16x16x32_bf16 a[40:43],a[144:147],a[200:203],a[40:43]
buffer_load_dwordx4 v55,s[28:31],s57 offen offset:3072 lds
v_mfma_f32_16x16x32_bf16 a[72:75],a[144:147],a[208:211],a[72:75]
s_addk_i32 s57,128
v_mfma_f32_16x16x32_bf16 a[72:75],a[148:151],a[212:215],a[72:75]
v_mfma_f32_16x16x32_bf16 a[104:107],a[148:151],a[220:223],a[104:107]
v_mfma_f32_16x16x32_bf16 a[104:107],a[144:147],a[216:219],a[104:107]
v_mfma_f32_16x16x32_bf16 a[12:15],a[152:155],a[192:195],a[12:15]
v_mfma_f32_16x16x32_bf16 a[12:15],a[156:159],a[196:199],a[12:15]
v_mfma_f32_16x16x32_bf16 a[44:47],a[156:159],a[204:207],a[44:47]
buffer_load_dword v16,v15,s[72:75],s83 offen
v_mfma_f32_16x16x32_bf16 a[44:47],a[152:155],a[200:203],a[44:47]
s_addk_i32 s83,896
v_mfma_f32_16x16x32_bf16 a[76:79],a[152:155],a[208:211],a[76:79]
v_mfma_f32_16x16x32_bf16 a[76:79],a[156:159],a[212:215],a[76:79]
v_mfma_f32_16x16x32_bf16 a[108:111],a[156:159],a[220:223],a[108:111]
v_mfma_f32_16x16x32_bf16 a[108:111],a[152:155],a[216:219],a[108:111]
s_waitcnt vmcnt(17) lgkmcnt(0)
v_mfma_f32_16x16x32_bf16 a[16:19],a[160:163],a[192:195],a[16:19]
s_barrier
ds_read_b128 a[224:227],v20
v_mfma_f32_16x16x32_bf16 a[16:19],a[164:167],a[196:199],a[16:19]
ds_read_b64_tr_b16 a[128:129],v22 offset:1024
v_mfma_f32_16x16x32_bf16 a[48:51],a[164:167],a[204:207],a[48:51]
ds_read_b64_tr_b16 a[130:131],v22 offset:3072
v_mfma_f32_16x16x32_bf16 a[48:51],a[160:163],a[200:203],a[48:51]
ds_read_b128 a[228:231],v21
v_mfma_f32_16x16x32_bf16 a[80:83],a[160:163],a[208:211],a[80:83]
ds_read_b64_tr_b16 a[132:133],v22 offset:17408
v_mfma_f32_16x16x32_bf16 a[80:83],a[164:167],a[212:215],a[80:83]
ds_read_b64_tr_b16 a[134:135],v22 offset:19456
v_mfma_f32_16x16x32_bf16 a[112:115],a[164:167],a[220:223],a[112:115]
ds_read_b128 a[232:235],v20 offset:2048
v_mfma_f32_16x16x32_bf16 a[112:115],a[160:163],a[216:219],a[112:115]
ds_read_b128 a[236:239],v21 offset:2048
v_mfma_f32_16x16x32_bf16 a[20:23],a[168:171],a[192:195],a[20:23]
ds_read_b128 a[240:243],v20 offset:4096
v_mfma_f32_16x16x32_bf16 a[20:23],a[172:175],a[196:199],a[20:23]
ds_read_b128 a[244:247],v21 offset:4096
v_mfma_f32_16x16x32_bf16 a[52:55],a[172:175],a[204:207],a[52:55]
ds_read_b128 a[248:251],v20 offset:6144
v_xor_b32 v20,131072,v20
v_mfma_f32_16x16x32_bf16 a[52:55],a[168:171],a[200:203],a[52:55]
ds_read_b128 a[252:255],v21 offset:6144
v_xor_b32 v21,131072,v21
v_mfma_f32_16x16x32_bf16 a[84:87],a[168:171],a[208:211],a[84:87]
ds_read_b64_tr_b16 a[136:137],v22 offset:1032
v_mfma_f32_16x16x32_bf16 a[84:87],a[172:175],a[212:215],a[84:87]
ds_read_b64_tr_b16 a[138:139],v22 offset:3080
v_mfma_f32_16x16x32_bf16 a[116:119],a[172:175],a[220:223],a[116:119]
ds_read_b64_tr_b16 a[140:141],v22 offset:17416
v_mfma_f32_16x16x32_bf16 a[116:119],a[168:171],a[216:219],a[116:119]
ds_read_b64_tr_b16 a[142:143],v22 offset:19464
v_mfma_f32_16x16x32_bf16 a[24:27],a[176:179],a[192:195],a[24:27]
ds_read_b64_tr_b16 a[144:145],v22 offset:1280
v_mfma_f32_16x16x32_bf16 a[24:27],a[180:183],a[196:199],a[24:27]
ds_read_b64_tr_b16 a[146:147],v22 offset:3328
v_mfma_f32_16x16x32_bf16 a[56:59],a[180:183],a[204:207],a[56:59]
ds_read_b64_tr_b16 a[148:149],v22 offset:17664
v_mfma_f32_16x16x32_bf16 a[56:59],a[176:179],a[200:203],a[56:59]
ds_read_b64_tr_b16 a[150:151],v22 offset:19712
v_mfma_f32_16x16x32_bf16 a[88:91],a[176:179],a[208:211],a[88:91]
ds_read_b64_tr_b16 a[152:153],v22 offset:1288
v_mfma_f32_16x16x32_bf16 a[88:91],a[180:183],a[212:215],a[88:91]
ds_read_b64_tr_b16 a[154:155],v22 offset:3336
v_mfma_f32_16x16x32_bf16 a[120:123],a[180:183],a[220:223],a[120:123]
ds_read_b64_tr_b16 a[156:157],v22 offset:17672
v_mfma_f32_16x16x32_bf16 a[120:123],a[176:179],a[216:219],a[120:123]
ds_read_b64_tr_b16 a[158:159],v22 offset:19720
v_mfma_f32_16x16x32_bf16 a[28:31],a[184:187],a[192:195],a[28:31]
ds_read_b64_tr_b16 a[160:161],v22 offset:1536
v_mfma_f32_16x16x32_bf16 a[28:31],a[188:191],a[196:199],a[28:31]
ds_read_b64_tr_b16 a[162:163],v22 offset:3584
v_mfma_f32_16x16x32_bf16 a[60:63],a[188:191],a[204:207],a[60:63]
ds_read_b64_tr_b16 a[164:165],v22 offset:17920
v_mfma_f32_16x16x32_bf16 a[60:63],a[184:187],a[200:203],a[60:63]
ds_read_b64_tr_b16 a[166:167],v22 offset:19968
v_mfma_f32_16x16x32_bf16 a[92:95],a[184:187],a[208:211],a[92:95]
ds_read_b64_tr_b16 a[168:169],v22 offset:1544
v_mfma_f32_16x16x32_bf16 a[92:95],a[188:191],a[212:215],a[92:95]
ds_read_b64_tr_b16 a[170:171],v22 offset:3592
v_mfma_f32_16x16x32_bf16 a[124:127],a[188:191],a[220:223],a[124:127]
s_add_u32 m0,s58,0
s_add_u32 s58,s58,32768
ds_read_b64_tr_b16 a[172:173],v22 offset:17928
v_mfma_f32_16x16x32_bf16 a[124:127],a[184:187],a[216:219],a[124:127]
s_cmp_ge_u32 s58,131072
s_cselect_b32 s62,98304,0
ds_read_b64_tr_b16 a[174:175],v22 offset:19976
s_waitcnt lgkmcnt(8)
v_mfma_f32_16x16x32_bf16 v[128:131],a[128:131],a[224:227],v[128:131]
s_mov_b32 s86,s87
s_sub_u32 s58,s58,s62
ds_read_b64_tr_b16 a[176:177],v22 offset:1792
v_mfma_f32_16x16x32_bf16 v[128:131],a[132:135],a[228:231],v[128:131]
buffer_load_dwordx4 v56,s[32:35],s56 offen lds
v_mfma_f32_16x16x32_bf16 v[136:139],a[132:135],a[236:239],v[136:139]
ds_read_b64_tr_b16 a[178:179],v22 offset:3840
v_mfma_f32_16x16x32_bf16 v[136:139],a[128:131],a[232:235],v[136:139]
s_load_dword s86,s[84:85],0x2000 glc
s_addk_i32 m0,1024
ds_read_b64_tr_b16 a[180:181],v22 offset:18176
v_mfma_f32_16x16x32_bf16 v[144:147],a[128:131],a[240:243],v[144:147]
s_addk_i32 s84,256
ds_read_b64_tr_b16 a[182:183],v22 offset:20224
v_mfma_f32_16x16x32_bf16 v[144:147],a[132:135],a[244:247],v[144:147]
ds_read_b64_tr_b16 a[184:185],v22 offset:1800
v_mfma_f32_16x16x32_bf16 v[152:155],a[132:135],a[252:255],v[152:155]
buffer_load_dwordx4 v57,s[32:35],s56 offen lds
v_mfma_f32_16x16x32_bf16 v[152:155],a[128:131],a[248:251],v[152:155]
ds_read_b64_tr_b16 a[186:187],v22 offset:3848
v_mfma_f32_16x16x32_bf16 v[132:135],a[136:139],a[224:227],v[132:135]
ds_read_b64_tr_b16 a[188:189],v22 offset:18184
v_mfma_f32_16x16x32_bf16 v[132:135],a[140:143],a[228:231],v[132:135]
s_addk_i32 m0,1024
ds_read_b64_tr_b16 a[190:191],v22 offset:20232
v_mfma_f32_16x16x32_bf16 v[140:143],a[140:143],a[236:239],v[140:143]
ds_read_b64_tr_b16 a[128:129],v22
v_mfma_f32_16x16x32_bf16 v[140:143],a[136:139],a[232:235],v[140:143]
ds_read_b64_tr_b16 a[130:131],v22 offset:2048
v_mfma_f32_16x16x32_bf16 v[148:151],a[136:139],a[240:243],v[148:151]
buffer_load_dwordx4 v58,s[32:35],s56 offen lds
v_mfma_f32_16x16x32_bf16 v[148:151],a[140:143],a[244:247],v[148:151]
ds_read_b64_tr_b16 a[132:133],v22 offset:16384
v_mfma_f32_16x16x32_bf16 v[156:159],a[140:143],a[252:255],v[156:159]
s_cmp_eq_u32 s86,s87
s_subb_u32 s79,s79,0
s_addk_i32 m0,1024
v_mfma_f32_16x16x32_bf16 v[156:159],a[136:139],a[248:251],v[156:159]
ds_read_b64_tr_b16 a[134:135],v22 offset:18432
v_mfma_f32_16x16x32_bf16 v[160:163],a[144:147],a[224:227],v[160:163]
ds_read_b64_tr_b16 a[136:137],v22 offset:8
v_mfma_f32_16x16x32_bf16 v[160:163],a[148:151],a[228:231],v[160:163]
s_cmp_eq_u32 s86,s87
s_subb_u32 s79,s79,0
ds_read_b64_tr_b16 a[138:139],v22 offset:2056
v_mfma_f32_16x16x32_bf16 v[168:171],a[148:151],a[236:239],v[168:171]
buffer_load_dwordx4 v59,s[32:35],s56 offen lds
v_mfma_f32_16x16x32_bf16 v[168:171],a[144:147],a[232:235],v[168:171]
ds_read_b64_tr_b16 a[140:141],v22 offset:16392
v_mfma_f32_16x16x32_bf16 v[176:179],a[144:147],a[240:243],v[176:179]
s_cmp_eq_u32 s86,s87
s_subb_u32 s79,s79,0
s_addk_i32 m0,1024
v_mfma_f32_16x16x32_bf16 v[176:179],a[148:151],a[244:247],v[176:179]
ds_read_b64_tr_b16 a[142:143],v22 offset:18440
v_mfma_f32_16x16x32_bf16 v[184:187],a[148:151],a[252:255],v[184:187]
ds_read_b64_tr_b16 a[148:149],v22 offset:16640
v_mfma_f32_16x16x32_bf16 v[184:187],a[144:147],a[248:251],v[184:187]
s_cmp_eq_u32 s86,s87
s_subb_u32 s79,s79,0
ds_read_b64_tr_b16 a[144:145],v22 offset:256
v_mfma_f32_16x16x32_bf16 v[164:167],a[152:155],a[224:227],v[164:167]
buffer_load_dwordx4 v60,s[32:35],s56 offen lds
v_mfma_f32_16x16x32_bf16 v[164:167],a[156:159],a[228:231],v[164:167]
s_addk_i32 m0,1024
ds_read_b64_tr_b16 a[146:147],v22 offset:2304
v_mfma_f32_16x16x32_bf16 v[172:175],a[156:159],a[236:239],v[172:175]
s_cmp_eq_u32 s86,s87
s_subb_u32 s79,s79,0
ds_read_b64_tr_b16 a[150:151],v22 offset:18688
v_mfma_f32_16x16x32_bf16 v[172:175],a[152:155],a[232:235],v[172:175]
v_mfma_f32_16x16x32_bf16 v[180:183],a[152:155],a[240:243],v[180:183]
buffer_load_dwordx4 v61,s[32:35],s56 offen lds
v_mfma_f32_16x16x32_bf16 v[180:183],a[156:159],a[244:247],v[180:183]
s_cmp_eq_u32 s86,s87
s_subb_u32 s79,s79,0
v_mfma_f32_16x16x32_bf16 v[188:191],a[156:159],a[252:255],v[188:191]
ds_read_b64_tr_b16 a[156:157],v22 offset:16648
v_mfma_f32_16x16x32_bf16 v[188:191],a[152:155],a[248:251],v[188:191]
s_addk_i32 m0,1024
ds_read_b64_tr_b16 a[152:153],v22 offset:264
s_waitcnt lgkmcnt(4)
v_mfma_f32_16x16x32_bf16 v[192:195],a[160:163],a[224:227],v[192:195]
s_cmp_eq_u32 s86,s87
s_subb_u32 s79,s79,0
ds_read_b64_tr_b16 a[154:155],v22 offset:2312
v_mfma_f32_16x16x32_bf16 v[192:195],a[164:167],a[228:231],v[192:195]
ds_read_b64_tr_b16 a[158:159],v22 offset:18696
v_mfma_f32_16x16x32_bf16 v[200:203],a[164:167],a[236:239],v[200:203]
buffer_load_dwordx4 v62,s[32:35],s56 offen lds
v_mfma_f32_16x16x32_bf16 v[200:203],a[160:163],a[232:235],v[200:203]
s_cmp_eq_u32 s86,s87
s_subb_u32 s79,s79,0
v_mfma_f32_16x16x32_bf16 v[208:211],a[160:163],a[240:243],v[208:211]
s_addk_i32 m0,1024
v_mfma_f32_16x16x32_bf16 v[208:211],a[164:167],a[244:247],v[208:211]
v_mfma_f32_16x16x32_bf16 v[216:219],a[164:167],a[252:255],v[216:219]
s_cmp_eq_u32 s86,s87
s_subb_u32 s79,s79,0
ds_read_b64_tr_b16 a[164:165],v22 offset:16896
v_mfma_f32_16x16x32_bf16 v[216:219],a[160:163],a[248:251],v[216:219]
buffer_load_dwordx4 v63,s[32:35],s56 offen lds
v_mfma_f32_16x16x32_bf16 v[196:199],a[168:171],a[224:227],v[196:199]
s_add_u32 s56,s56,524288
ds_read_b64_tr_b16 a[160:161],v22 offset:512
v_mfma_f32_16x16x32_bf16 v[196:199],a[172:175],a[228:231],v[196:199]
s_cmp_eq_u32 s86,s87
s_subb_u32 s79,s79,0
ds_read_b64_tr_b16 a[162:163],v22 offset:2560
v_mfma_f32_16x16x32_bf16 v[204:207],a[172:175],a[236:239],v[204:207]
s_add_u32 m0,s59,0
s_xor_b32 s59,s59,131072
ds_read_b64_tr_b16 a[166:167],v22 offset:18944
v_mfma_f32_16x16x32_bf16 v[204:207],a[168:171],a[232:235],v[204:207]
v_mfma_f32_16x16x32_bf16 v[212:215],a[168:171],a[240:243],v[212:215]
s_cmp_eq_u32 s86,s87
s_subb_u32 s79,s79,0
v_mfma_f32_16x16x32_bf16 v[212:215],a[172:175],a[244:247],v[212:215]
buffer_load_dwordx4 v48,s[28:31],s57 offen lds
v_mfma_f32_16x16x32_bf16 v[220:223],a[172:175],a[252:255],v[220:223]
ds_read_b64_tr_b16 a[172:173],v22 offset:16904
v_mfma_f32_16x16x32_bf16 v[220:223],a[168:171],a[248:251],v[220:223]
s_cmp_eq_u32 s86,s87
s_subb_u32 s79,s79,0
ds_read_b64_tr_b16 a[168:169],v22 offset:520
v_mfma_f32_16x16x32_bf16 v[224:227],a[176:179],a[224:227],v[224:227]
ds_read_b64_tr_b16 a[170:171],v22 offset:2568
v_mfma_f32_16x16x32_bf16 v[224:227],a[180:183],a[228:231],v[224:227]
ds_read_b64_tr_b16 a[174:175],v22 offset:18952
v_mfma_f32_16x16x32_bf16 v[232:235],a[180:183],a[236:239],v[232:235]
s_cmp_eq_u32 s86,s87
s_subb_u32 s79,s79,0
v_mfma_f32_16x16x32_bf16 v[232:235],a[176:179],a[232:235],v[232:235]
buffer_load_dwordx4 v49,s[28:31],s57 offen offset:1024 lds
v_mfma_f32_16x16x32_bf16 v[240:243],a[176:179],a[240:243],v[240:243]
v_mfma_f32_16x16x32_bf16 v[240:243],a[180:183],a[244:247],v[240:243]
s_cmp_eq_u32 s86,s87
s_subb_u32 s79,s79,0
v_mfma_f32_16x16x32_bf16 v[248:251],a[180:183],a[252:255],v[248:251]
ds_read_b64_tr_b16 a[180:181],v22 offset:17152
v_mfma_f32_16x16x32_bf16 v[248:251],a[176:179],a[248:251],v[248:251]
ds_read_b64_tr_b16 a[176:177],v22 offset:768
v_mfma_f32_16x16x32_bf16 v[228:231],a[184:187],a[224:227],v[228:231]
s_cmp_eq_u32 s86,s87
s_subb_u32 s79,s79,0
ds_read_b64_tr_b16 a[178:179],v22 offset:2816
v_mfma_f32_16x16x32_bf16 v[228:231],a[188:191],a[228:231],v[228:231]
buffer_load_dwordx4 v50,s[28:31],s57 offen offset:2048 lds
v_mfma_f32_16x16x32_bf16 v[236:239],a[188:191],a[236:239],v[236:239]
ds_read_b64_tr_b16 a[182:183],v22 offset:19200
)A"
R"A(v_mfma_f32_16x16x32_bf16 v[236:239],a[184:187],a[232:235],v[236:239]
s_cmp_eq_u32 s86,s87
s_subb_u32 s79,s79,0
v_mfma_f32_16x16x32_bf16 v[244:247],a[184:187],a[240:243],v[244:247]
v_mfma_f32_16x16x32_bf16 v[244:247],a[188:191],a[244:247],v[244:247]
buffer_load_dwordx4 v51,s[28:31],s57 offen offset:3072 lds
v_mfma_f32_16x16x32_bf16 v[252:255],a[188:191],a[252:255],v[252:255]
ds_read_b64_tr_b16 a[188:189],v22 offset:17160
v_mfma_f32_16x16x32_bf16 v[252:255],a[184:187],a[248:251],v[252:255]
ds_read_b64_tr_b16 a[184:185],v22 offset:776
v_mfma_f32_16x16x32_bf16 a[0:3],a[128:131],a[224:227],a[0:3]
s_addk_i32 m0,4096
ds_read_b64_tr_b16 a[186:187],v22 offset:2824
v_mfma_f32_16x16x32_bf16 a[0:3],a[132:135],a[228:231],a[0:3]
ds_read_b64_tr_b16 a[190:191],v22 offset:19208
v_add_u32 v22,s60,v23
v_mfma_f32_16x16x32_bf16 a[32:35],a[132:135],a[236:239],a[32:35]
v_mfma_f32_16x16x32_bf16 a[32:35],a[128:131],a[232:235],a[32:35]
buffer_load_dwordx4 v52,s[28:31],s57 offen lds
v_mfma_f32_16x16x32_bf16 a[64:67],a[128:131],a[240:243],a[64:67]
v_mfma_f32_16x16x32_bf16 a[64:67],a[132:135],a[244:247],a[64:67]
v_mfma_f32_16x16x32_bf16 a[96:99],a[132:135],a[252:255],a[96:99]
v_mfma_f32_16x16x32_bf16 a[96:99],a[128:131],a[248:251],a[96:99]
v_mfma_f32_16x16x32_bf16 a[4:7],a[136:139],a[224:227],a[4:7]
buffer_load_dwordx4 v53,s[28:31],s57 offen offset:1024 lds
v_mfma_f32_16x16x32_bf16 a[4:7],a[140:143],a[228:231],a[4:7]
v_mfma_f32_16x16x32_bf16 a[36:39],a[140:143],a[236:239],a[36:39]
v_mfma_f32_16x16x32_bf16 a[36:39],a[136:139],a[232:235],a[36:39]
v_mfma_f32_16x16x32_bf16 a[68:71],a[136:139],a[240:243],a[68:71]
v_mfma_f32_16x16x32_bf16 a[68:71],a[140:143],a[244:247],a[68:71]
v_mfma_f32_16x16x32_bf16 a[100:103],a[140:143],a[252:255],a[100:103]
buffer_load_dwordx4 v54,s[28:31],s57 offen offset:2048 lds
v_mfma_f32_16x16x32_bf16 a[100:103],a[136:139],a[248:251],a[100:103]
s_waitcnt lgkmcnt(0)
v_mfma_f32_16x16x32_bf16 a[8:11],a[144:147],a[224:227],a[8:11]
v_mfma_f32_16x16x32_bf16 a[8:11],a[148:151],a[228:231],a[8:11]
v_mfma_f32_16x16x32_bf16 a[40:43],a[148:151],a[236:239],a[40:43]
v_mfma_f32_16x16x32_bf16 a[40:43],a[144:147],a[232:235],a[40:43]
buffer_load_dwordx4 v55,s[28:31],s57 offen offset:3072 lds
v_mfma_f32_16x16x32_bf16 a[72:75],a[144:147],a[240:243],a[72:75]
s_addk_i32 s57,128
v_mfma_f32_16x16x32_bf16 a[72:75],a[148:151],a[244:247],a[72:75]
v_mfma_f32_16x16x32_bf16 a[104:107],a[148:151],a[252:255],a[104:107]
v_mfma_f32_16x16x32_bf16 a[104:107],a[144:147],a[248:251],a[104:107]
v_mfma_f32_16x16x32_bf16 a[12:15],a[152:155],a[224:227],a[12:15]
v_mfma_f32_16x16x32_bf16 a[12:15],a[156:159],a[228:231],a[12:15]
v_mfma_f32_16x16x32_bf16 a[44:47],a[156:159],a[236:239],a[44:47]
buffer_load_dword v16,v15,s[72:75],s83 offen
v_mfma_f32_16x16x32_bf16 a[44:47],a[152:155],a[232:235],a[44:47]
s_addk_i32 s83,896
v_mfma_f32_16x16x32_bf16 a[76:79],a[152:155],a[240:243],a[76:79]
v_mfma_f32_16x16x32_bf16 a[76:79],a[156:159],a[244:247],a[76:79]
v_mfma_f32_16x16x32_bf16 a[108:111],a[156:159],a[252:255],a[108:111]
v_mfma_f32_16x16x32_bf16 a[108:111],a[152:155],a[248:251],a[108:111]
s_waitcnt vmcnt(17) lgkmcnt(0)
v_mfma_f32_16x16x32_bf16 a[16:19],a[160:163],a[224:227],a[16:19]
s_barrier
ds_read_b128 a[192:195],v20
v_mfma_f32_16x16x32_bf16 a[16:19],a[164:167],a[228:231],a[16:19]
ds_read_b128 a[196:199],v21
v_mfma_f32_16x16x32_bf16 a[48:51],a[164:167],a[236:239],a[48:51]
ds_read_b128 a[200:203],v20 offset:2048
v_mfma_f32_16x16x32_bf16 a[48:51],a[160:163],a[232:235],a[48:51]
ds_read_b128 a[204:207],v21 offset:2048
v_mfma_f32_16x16x32_bf16 a[80:83],a[160:163],a[240:243],a[80:83]
ds_read_b128 a[208:211],v20 offset:4096
v_mfma_f32_16x16x32_bf16 a[80:83],a[164:167],a[244:247],a[80:83]
ds_read_b128 a[212:215],v21 offset:4096
v_mfma_f32_16x16x32_bf16 a[112:115],a[164:167],a[252:255],a[112:115]
buffer_load_dword v26,v24,s[88:91],s93 offen
v_mfma_f32_16x16x32_bf16 a[112:115],a[160:163],a[248:251],a[112:115]
ds_read_b128 a[216:219],v20 offset:6144
v_mfma_f32_16x16x32_bf16 a[20:23],a[168:171],a[224:227],a[20:23]
ds_read_b128 a[220:223],v21 offset:6144
v_mfma_f32_16x16x32_bf16 a[20:23],a[172:175],a[228:231],a[20:23]
ds_read_b64_tr_b16 a[128:129],v22 offset:1024
v_mfma_f32_16x16x32_bf16 a[52:55],a[172:175],a[236:239],a[52:55]
ds_read_b64_tr_b16 a[130:131],v22 offset:3072
v_mfma_f32_16x16x32_bf16 a[52:55],a[168:171],a[232:235],a[52:55]
ds_read_b64_tr_b16 a[132:133],v22 offset:17408
v_mfma_f32_16x16x32_bf16 a[84:87],a[168:171],a[240:243],a[84:87]
ds_read_b64_tr_b16 a[134:135],v22 offset:19456
v_mfma_f32_16x16x32_bf16 a[84:87],a[172:175],a[244:247],a[84:87]
ds_read_b64_tr_b16 a[136:137],v22 offset:1032
v_mfma_f32_16x16x32_bf16 a[116:119],a[172:175],a[252:255],a[116:119]
ds_read_b64_tr_b16 a[138:139],v22 offset:3080
v_mfma_f32_16x16x32_bf16 a[116:119],a[168:171],a[248:251],a[116:119]
ds_read_b64_tr_b16 a[140:141],v22 offset:17416
v_mfma_f32_16x16x32_bf16 a[24:27],a[176:179],a[224:227],a[24:27]
ds_read_b64_tr_b16 a[142:143],v22 offset:19464
v_mfma_f32_16x16x32_bf16 a[24:27],a[180:183],a[228:231],a[24:27]
ds_read_b64_tr_b16 a[144:145],v22 offset:1280
v_mfma_f32_16x16x32_bf16 a[56:59],a[180:183],a[236:239],a[56:59]
ds_read_b64_tr_b16 a[146:147],v22 offset:3328
v_mfma_f32_16x16x32_bf16 a[56:59],a[176:179],a[232:235],a[56:59]
ds_read_b64_tr_b16 a[148:149],v22 offset:17664
v_mfma_f32_16x16x32_bf16 a[88:91],a[176:179],a[240:243],a[88:91]
ds_read_b64_tr_b16 a[150:151],v22 offset:19712
v_mfma_f32_16x16x32_bf16 a[88:91],a[180:183],a[244:247],a[88:91]
ds_read_b64_tr_b16 a[152:153],v22 offset:1288
v_mfma_f32_16x16x32_bf16 a[120:123],a[180:183],a[252:255],a[120:123]
ds_read_b64_tr_b16 a[154:155],v22 offset:3336
v_mfma_f32_16x16x32_bf16 a[120:123],a[176:179],a[248:251],a[120:123]
ds_read_b64_tr_b16 a[156:157],v22 offset:17672
v_mfma_f32_16x16x32_bf16 a[28:31],a[184:187],a[224:227],a[28:31]
ds_read_b64_tr_b16 a[158:159],v22 offset:19720
v_mfma_f32_16x16x32_bf16 a[28:31],a[188:191],a[228:231],a[28:31]
v_mfma_f32_16x16x32_bf16 a[60:63],a[188:191],a[236:239],a[60:63]
v_mfma_f32_16x16x32_bf16 a[60:63],a[184:187],a[232:235],a[60:63]
v_mfma_f32_16x16x32_bf16 a[92:95],a[184:187],a[240:243],a[92:95]
v_mfma_f32_16x16x32_bf16 a[92:95],a[188:191],a[244:247],a[92:95]
v_mfma_f32_16x16x32_bf16 a[124:127],a[188:191],a[252:255],a[124:127]
s_cmp_lg_u32 s53,0
v_mfma_f32_16x16x32_bf16 a[124:127],a[184:187],a[248:251],a[124:127]
s_cbranch_scc1 L_qp_%=
L_qpx_%=:
L_tailseg_%=:
s_waitcnt lgkmcnt(1)
v_mfma_f32_16x16x32_bf16 v[128:131],a[128:131],a[192:195],v[128:131]
s_add_u32 m0,98304,s51
ds_read_b64_tr_b16 a[160:161],v22 offset:1536
v_xor_b32 v20,131072,v20
v_mfma_f32_16x16x32_bf16 v[128:131],a[132:135],a[196:199],v[128:131]
s_addk_i32 s54,16384
buffer_load_dwordx4 v4,s[20:23],s47 offen nt lds
v_mfma_f32_16x16x32_bf16 v[136:139],a[132:135],a[204:207],v[136:139]
s_and_b32 s46,s54,0xfffc0000
ds_read_b64_tr_b16 a[162:163],v22 offset:3584
v_xor_b32 v21,131072,v21
v_mfma_f32_16x16x32_bf16 v[136:139],a[128:131],a[200:203],v[136:139]
s_addk_i32 s54,16384
ds_read_b64_tr_b16 a[164:165],v22 offset:17920
v_and_b32 v18,48,v6
v_mfma_f32_16x16x32_bf16 v[144:147],a[128:131],a[208:211],v[144:147]
s_mov_b32 s48,-98304
ds_read_b64_tr_b16 a[166:167],v22 offset:19968
v_lshrrev_b32 v19,4,v6
v_mfma_f32_16x16x32_bf16 v[144:147],a[132:135],a[212:215],v[144:147]
s_movk_i32 s60,141
ds_read_b64_tr_b16 a[168:169],v22 offset:1544
v_lshlrev_b32 v18,2,v18
v_mfma_f32_16x16x32_bf16 v[152:155],a[132:135],a[220:223],v[152:155]
s_mul_i32 s83,s41,5
ds_read_b64_tr_b16 a[170:171],v22 offset:3592
v_and_b32 v19,48,v19
v_mfma_f32_16x16x32_bf16 v[152:155],a[128:131],a[216:219],v[152:155]
s_add_u32 s83,s83,s42
ds_read_b64_tr_b16 a[172:173],v22 offset:17928
v_add_u32 v18,v18,v19
v_mfma_f32_16x16x32_bf16 v[132:135],a[136:139],a[192:195],v[132:135]
s_add_u32 s83,s83,15
ds_read_b64_tr_b16 a[174:175],v22 offset:19976
v_add_u32 v18,98304,v18
v_mfma_f32_16x16x32_bf16 v[132:135],a[140:143],a[196:199],v[132:135]
s_cmp_gt_i32 s79,0
s_cselect_b32 s86,8192,0
ds_read_b64_tr_b16 a[176:177],v22 offset:1792
v_mfma_f32_16x16x32_bf16 v[140:143],a[140:143],a[204:207],v[140:143]
s_lshr_b32 s79,%1,3
v_readfirstlane_b32 s68,v13
s_nop 1
s_add_u32 s68,s68,1
v_mfma_f32_16x16x32_bf16 v[140:143],a[136:139],a[200:203],v[140:143]
s_mul_i32 s79,s79,4
s_lshr_b32 s79,s79,5
ds_read_b64_tr_b16 a[178:179],v22 offset:3840
v_mfma_f32_16x16x32_bf16 v[148:151],a[136:139],a[208:211],v[148:151]
s_sub_u32 s60,s60,s79
s_lshl_b32 s94,s60,2
ds_read_b64_tr_b16 a[180:181],v22 offset:18176
v_mfma_f32_16x16x32_bf16 v[148:151],a[140:143],a[212:215],v[148:151]
s_sub_u32 s94,s94,3
buffer_load_dwordx4 v4,s[20:23],s47 offen offset:1024 nt lds
v_mfma_f32_16x16x32_bf16 v[156:159],a[140:143],a[220:223],v[156:159]
s_lshl_b32 s43,s83,14
s_lshl_b32 s83,s83,12
ds_read_b64_tr_b16 a[182:183],v22 offset:20224
v_mfma_f32_16x16x32_bf16 v[156:159],a[136:139],a[216:219],v[156:159]
s_lshl_b32 s44,s40,4
s_and_b32 s43,s43,0x20000
ds_read_b64_tr_b16 a[184:185],v22 offset:1800
v_mfma_f32_16x16x32_bf16 v[160:163],a[144:147],a[192:195],v[160:163]
s_and_b32 s83,s83,0x7000
s_xor_b32 s69,s68,0x9e3779b9
ds_read_b64_tr_b16 a[186:187],v22 offset:3848
v_mfma_f32_16x16x32_bf16 v[160:163],a[148:151],a[196:199],v[160:163]
s_lshl_b32 s80,s40,10
s_and_b32 s44,s44,32
ds_read_b64_tr_b16 a[188:189],v22 offset:18184
v_mfma_f32_16x16x32_bf16 v[168:171],a[148:151],a[204:207],v[168:171]
s_or_b32 s83,s83,s43
s_and_b32 s69,s69,-2
ds_read_b64_tr_b16 a[190:191],v22 offset:20232
v_mfma_f32_16x16x32_bf16 v[168:171],a[144:147],a[200:203],v[168:171]
s_add_u32 s55,s55,s86
s_add_u32 s93,s92,L_att0x_%=-L_pc_%=
ds_read_b64_tr_b16 a[128:129],v22
v_mfma_f32_16x16x32_bf16 v[176:179],a[144:147],a[208:211],v[176:179]
s_mov_b32 s50,32768
s_mov_b32 s49,98304
ds_read_b64_tr_b16 a[130:131],v22 offset:2048
v_mfma_f32_16x16x32_bf16 v[176:179],a[148:151],a[212:215],v[176:179]
s_mov_b32 s45,0x1249
ds_read_b64_tr_b16 a[132:133],v22 offset:16384
v_xor_b32 v25,s44,v4
v_mfma_f32_16x16x32_bf16 v[184:187],a[148:151],a[220:223],v[184:187]
s_add_u32 s80,s80,131072
s_or_b32 s83,s83,0x40000
ds_read_b64_tr_b16 a[134:135],v22 offset:18432
v_mfma_f32_16x16x32_bf16 v[184:187],a[144:147],a[216:219],v[184:187]
ds_read_b64_tr_b16 a[144:145],v22 offset:8
v_mfma_f32_16x16x32_bf16 v[164:167],a[152:155],a[192:195],v[164:167]
s_waitcnt lgkmcnt(7)
ds_read_b64_tr_b16 a[146:147],v22 offset:2056
v_mfma_f32_16x16x32_bf16 v[164:167],a[156:159],a[196:199],v[164:167]
buffer_load_dwordx4 v5,s[20:23],s47 offen offset:2048 nt lds
v_mfma_f32_16x16x32_bf16 v[172:175],a[156:159],a[204:207],v[172:175]
s_waitcnt vmcnt(5)
ds_read_b128 a[224:227],v20
v_mfma_f32_16x16x32_bf16 v[172:175],a[152:155],a[200:203],v[172:175]
ds_read_b64_tr_b16 a[148:149],v22 offset:16392
v_mfma_f32_16x16x32_bf16 v[180:183],a[152:155],a[208:211],v[180:183]
ds_read_b64_tr_b16 a[150:151],v22 offset:18440
v_mfma_f32_16x16x32_bf16 v[180:183],a[156:159],a[212:215],v[180:183]
v_mfma_f32_16x16x32_bf16 v[188:191],a[156:159],a[220:223],v[188:191]
ds_read_b128 a[228:231],v21
v_mfma_f32_16x16x32_bf16 v[188:191],a[152:155],a[216:219],v[188:191]
v_mfma_f32_16x16x32_bf16 v[192:195],a[160:163],a[192:195],v[192:195]
ds_read_b128 a[232:235],v20 offset:2048
v_mfma_f32_16x16x32_bf16 v[192:195],a[164:167],a[196:199],v[192:195]
v_mfma_f32_16x16x32_bf16 v[200:203],a[164:167],a[204:207],v[200:203]
ds_read_b128 a[236:239],v21 offset:2048
v_mfma_f32_16x16x32_bf16 v[200:203],a[160:163],a[200:203],v[200:203]
v_mfma_f32_16x16x32_bf16 v[208:211],a[160:163],a[208:211],v[208:211]
ds_read_b128 a[240:243],v20 offset:4096
v_mfma_f32_16x16x32_bf16 v[208:211],a[164:167],a[212:215],v[208:211]
buffer_load_dwordx4 v5,s[20:23],s47 offen offset:3072 nt lds
v_mfma_f32_16x16x32_bf16 v[216:219],a[164:167],a[220:223],v[216:219]
ds_read_b128 a[244:247],v21 offset:4096
v_mfma_f32_16x16x32_bf16 v[216:219],a[160:163],a[216:219],v[216:219]
ds_read_b64_tr_b16 a[160:161],v22 offset:256
v_mfma_f32_16x16x32_bf16 v[196:199],a[168:171],a[192:195],v[196:199]
ds_read_b128 a[248:251],v20 offset:6144
ds_read_b128 a[252:255],v21 offset:6144
v_mfma_f32_16x16x32_bf16 v[196:199],a[172:175],a[196:199],v[196:199]
)A"
R"A(v_mfma_f32_16x16x32_bf16 v[204:207],a[172:175],a[204:207],v[204:207]
v_mfma_f32_16x16x32_bf16 v[204:207],a[168:171],a[200:203],v[204:207]
v_mfma_f32_16x16x32_bf16 v[212:215],a[168:171],a[208:211],v[212:215]
ds_read_b64_tr_b16 a[162:163],v22 offset:2304
v_mfma_f32_16x16x32_bf16 v[212:215],a[172:175],a[212:215],v[212:215]
ds_read_b64_tr_b16 a[164:165],v22 offset:16640
v_mfma_f32_16x16x32_bf16 v[220:223],a[172:175],a[220:223],v[220:223]
s_waitcnt vmcnt(5) lgkmcnt(2)
s_addk_i32 m0,16384
ds_read_b64_tr_b16 a[166:167],v22 offset:18688
v_mfma_f32_16x16x32_bf16 v[220:223],a[168:171],a[216:219],v[220:223]
s_barrier
ds_read_b64_tr_b16 a[136:137],v22 offset:32768
v_mfma_f32_16x16x32_bf16 v[224:227],a[176:179],a[192:195],v[224:227]
buffer_load_dwordx4 v4,s[24:27],s47 offen nt lds
v_mfma_f32_16x16x32_bf16 v[224:227],a[180:183],a[196:199],v[224:227]
ds_read_b64_tr_b16 a[138:139],v22 offset:34816
v_mfma_f32_16x16x32_bf16 v[232:235],a[180:183],a[204:207],v[232:235]
ds_read_b64_tr_b16 a[140:141],v22 offset:49152
v_mfma_f32_16x16x32_bf16 v[232:235],a[176:179],a[200:203],v[232:235]
ds_read_b64_tr_b16 a[142:143],v22 offset:51200
v_mfma_f32_16x16x32_bf16 v[240:243],a[176:179],a[208:211],v[240:243]
ds_read_b64_tr_b16 a[152:153],v22 offset:32776
v_mfma_f32_16x16x32_bf16 v[240:243],a[180:183],a[212:215],v[240:243]
ds_read_b64_tr_b16 a[154:155],v22 offset:34824
v_mfma_f32_16x16x32_bf16 v[248:251],a[180:183],a[220:223],v[248:251]
ds_read_b64_tr_b16 a[156:157],v22 offset:49160
v_mfma_f32_16x16x32_bf16 v[248:251],a[176:179],a[216:219],v[248:251]
ds_read_b64_tr_b16 a[158:159],v22 offset:51208
v_mfma_f32_16x16x32_bf16 v[228:231],a[184:187],a[192:195],v[228:231]
ds_read_b64_tr_b16 a[168:169],v22 offset:33024
v_mfma_f32_16x16x32_bf16 v[228:231],a[188:191],a[196:199],v[228:231]
ds_read_b64_tr_b16 a[170:171],v22 offset:35072
v_mfma_f32_16x16x32_bf16 v[236:239],a[188:191],a[204:207],v[236:239]
ds_read_b64_tr_b16 a[172:173],v22 offset:49408
v_mfma_f32_16x16x32_bf16 v[236:239],a[184:187],a[200:203],v[236:239]
ds_read_b64_tr_b16 a[174:175],v22 offset:51456
v_mfma_f32_16x16x32_bf16 v[244:247],a[184:187],a[208:211],v[244:247]
buffer_load_dwordx4 v5,s[24:27],s47 offen offset:1024 nt lds
v_mfma_f32_16x16x32_bf16 v[244:247],a[188:191],a[212:215],v[244:247]
ds_read_b64_tr_b16 a[176:177],v22 offset:264
v_mfma_f32_16x16x32_bf16 v[252:255],a[188:191],a[220:223],v[252:255]
ds_read_b64_tr_b16 a[178:179],v22 offset:2312
v_mfma_f32_16x16x32_bf16 v[252:255],a[184:187],a[216:219],v[252:255]
ds_read_b64_tr_b16 a[180:181],v22 offset:16648
v_mfma_f32_16x16x32_bf16 a[0:3],a[128:131],a[192:195],a[0:3]
ds_read_b64_tr_b16 a[182:183],v22 offset:18696
v_mfma_f32_16x16x32_bf16 a[0:3],a[132:135],a[196:199],a[0:3]
s_waitcnt lgkmcnt(6)
ds_read_b64_tr_b16 a[184:185],v22 offset:33032
v_mfma_f32_16x16x32_bf16 a[0:3],a[136:139],a[224:227],a[0:3]
ds_read_b64_tr_b16 a[186:187],v22 offset:35080
v_mfma_f32_16x16x32_bf16 a[0:3],a[140:143],a[228:231],a[0:3]
ds_read_b64_tr_b16 a[188:189],v22 offset:49416
v_mfma_f32_16x16x32_bf16 a[32:35],a[140:143],a[236:239],a[32:35]
ds_read_b64_tr_b16 a[190:191],v22 offset:51464
v_mfma_f32_16x16x32_bf16 a[32:35],a[136:139],a[232:235],a[32:35]
v_mfma_f32_16x16x32_bf16 a[32:35],a[132:135],a[204:207],a[32:35]
v_mfma_f32_16x16x32_bf16 a[32:35],a[128:131],a[200:203],a[32:35]
v_mfma_f32_16x16x32_bf16 a[64:67],a[128:131],a[208:211],a[64:67]
buffer_load_dwordx4 v4,s[24:27],s47 offen offset:2048 nt lds
v_mfma_f32_16x16x32_bf16 a[64:67],a[132:135],a[212:215],a[64:67]
v_accvgpr_read_b32 v64,a0
v_accvgpr_read_b32 v65,a1
v_mfma_f32_16x16x32_bf16 a[64:67],a[136:139],a[240:243],a[64:67]
v_mul_f32 v64,s61,v64
v_mul_f32 v65,s61,v65
v_mfma_f32_16x16x32_bf16 a[64:67],a[140:143],a[244:247],a[64:67]
v_accvgpr_read_b32 v66,a2
v_accvgpr_read_b32 v67,a3
v_mfma_f32_16x16x32_bf16 a[96:99],a[140:143],a[252:255],a[96:99]
ds_read_b64_tr_b16 a[140:141],v22 offset:49664
v_cvt_pk_bf16_f32 v64,v64,v65
v_mfma_f32_16x16x32_bf16 a[96:99],a[136:139],a[248:251],a[96:99]
ds_read_b64_tr_b16 a[136:137],v22 offset:33280
v_accvgpr_read_b32 v80,a32
v_mfma_f32_16x16x32_bf16 a[96:99],a[132:135],a[220:223],a[96:99]
ds_read_b64_tr_b16 a[132:133],v22 offset:16896
v_accvgpr_read_b32 v81,a33
v_mfma_f32_16x16x32_bf16 a[96:99],a[128:131],a[216:219],a[96:99]
ds_read_b64_tr_b16 a[128:129],v22 offset:512
v_mul_f32 v80,s61,v80
v_mfma_f32_16x16x32_bf16 a[4:7],a[144:147],a[192:195],a[4:7]
ds_read_b64_tr_b16 a[130:131],v22 offset:2560
v_mul_f32 v81,s61,v81
v_mfma_f32_16x16x32_bf16 a[4:7],a[148:151],a[196:199],a[4:7]
ds_read_b64_tr_b16 a[134:135],v22 offset:18944
v_accvgpr_read_b32 v82,a34
v_mfma_f32_16x16x32_bf16 a[4:7],a[152:155],a[224:227],a[4:7]
ds_read_b64_tr_b16 a[138:139],v22 offset:35328
v_accvgpr_read_b32 v83,a35
v_mfma_f32_16x16x32_bf16 a[4:7],a[156:159],a[228:231],a[4:7]
ds_read_b64_tr_b16 a[142:143],v22 offset:51712
v_mul_f32 v66,s61,v66
v_mfma_f32_16x16x32_bf16 a[36:39],a[156:159],a[236:239],a[36:39]
buffer_load_dwordx4 v5,s[24:27],s47 offen offset:3072 nt lds
v_mfma_f32_16x16x32_bf16 a[36:39],a[152:155],a[232:235],a[36:39]
s_or_b32 s47,s47,0x18000
s_addk_i32 s47,4096
s_and_b32 s47,s47,0x27000
v_mfma_f32_16x16x32_bf16 a[36:39],a[148:151],a[204:207],a[36:39]
s_or_b32 s47,s47,s46
s_and_b32 s46,s54,0xfffc0000
v_mul_f32 v67,s61,v67
v_mfma_f32_16x16x32_bf16 a[36:39],a[144:147],a[200:203],a[36:39]
s_addk_i32 s54,16384
v_cvt_pk_bf16_f32 v80,v80,v81
v_mul_f32 v82,s61,v82
v_mfma_f32_16x16x32_bf16 a[68:71],a[144:147],a[208:211],a[68:71]
v_mul_f32 v83,s61,v83
v_cvt_pk_bf16_f32 v65,v66,v67
v_mfma_f32_16x16x32_bf16 a[68:71],a[148:151],a[212:215],a[68:71]
v_cvt_pk_bf16_f32 v81,v82,v83
v_accvgpr_read_b32 v112,a96
v_mfma_f32_16x16x32_bf16 a[68:71],a[152:155],a[240:243],a[68:71]
v_accvgpr_read_b32 v113,a97
v_accvgpr_read_b32 v66,a4
v_mfma_f32_16x16x32_bf16 a[68:71],a[156:159],a[244:247],a[68:71]
v_accvgpr_read_b32 v67,a5
v_mul_f32 v112,s61,v112
v_mfma_f32_16x16x32_bf16 a[100:103],a[156:159],a[252:255],a[100:103]
ds_read_b64_tr_b16 a[156:157],v22 offset:49672
v_mul_f32 v113,s61,v113
v_mfma_f32_16x16x32_bf16 a[100:103],a[152:155],a[248:251],a[100:103]
ds_read_b64_tr_b16 a[152:153],v22 offset:33288
v_accvgpr_read_b32 v82,a36
v_mfma_f32_16x16x32_bf16 a[100:103],a[148:151],a[220:223],a[100:103]
ds_read_b64_tr_b16 a[148:149],v22 offset:16904
v_accvgpr_read_b32 v83,a37
v_mfma_f32_16x16x32_bf16 a[100:103],a[144:147],a[216:219],a[100:103]
ds_read_b64_tr_b16 a[144:145],v22 offset:520
v_accvgpr_read_b32 v114,a98
v_mfma_f32_16x16x32_bf16 a[8:11],a[160:163],a[192:195],a[8:11]
s_add_u32 m0,0,s51
ds_read_b64_tr_b16 a[146:147],v22 offset:2568
v_accvgpr_read_b32 v115,a99
v_mfma_f32_16x16x32_bf16 a[8:11],a[164:167],a[196:199],a[8:11]
ds_read_b64_tr_b16 a[150:151],v22 offset:18952
v_accvgpr_read_b32 v12,a6
v_mfma_f32_16x16x32_bf16 a[8:11],a[168:171],a[224:227],a[8:11]
s_waitcnt lgkmcnt(6)
ds_read_b64_tr_b16 a[154:155],v22 offset:35336
v_accvgpr_read_b32 v13,a7
v_mfma_f32_16x16x32_bf16 a[8:11],a[172:175],a[228:231],a[8:11]
buffer_load_dwordx4 v4,s[20:23],s47 offen nt lds
v_mfma_f32_16x16x32_bf16 a[40:43],a[172:175],a[236:239],a[40:43]
ds_read_b64_tr_b16 a[158:159],v22 offset:51720
v_mul_f32 v66,s61,v66
v_mfma_f32_16x16x32_bf16 a[40:43],a[168:171],a[232:235],a[40:43]
v_mul_f32 v67,s61,v67
v_accvgpr_read_b32 v14,a38
v_mfma_f32_16x16x32_bf16 a[40:43],a[164:167],a[204:207],a[40:43]
v_accvgpr_read_b32 v15,a39
v_mul_f32 v82,s61,v82
v_mfma_f32_16x16x32_bf16 a[40:43],a[160:163],a[200:203],a[40:43]
v_mul_f32 v83,s61,v83
v_cvt_pk_bf16_f32 v112,v112,v113
v_mfma_f32_16x16x32_bf16 a[72:75],a[160:163],a[208:211],a[72:75]
v_mul_f32 v114,s61,v114
v_mul_f32 v115,s61,v115
v_mfma_f32_16x16x32_bf16 a[72:75],a[164:167],a[212:215],a[72:75]
v_accvgpr_read_b32 v68,a8
v_accvgpr_read_b32 v69,a9
v_mfma_f32_16x16x32_bf16 a[72:75],a[168:171],a[240:243],a[72:75]
v_mul_f32 v68,s61,v68
v_mul_f32 v69,s61,v69
v_mfma_f32_16x16x32_bf16 a[72:75],a[172:175],a[244:247],a[72:75]
v_accvgpr_read_b32 v70,a10
v_accvgpr_read_b32 v71,a11
v_mfma_f32_16x16x32_bf16 a[104:107],a[172:175],a[252:255],a[104:107]
ds_read_b64_tr_b16 a[172:173],v22 offset:49920
v_cvt_pk_bf16_f32 v68,v68,v69
v_mfma_f32_16x16x32_bf16 a[104:107],a[168:171],a[248:251],a[104:107]
ds_read_b64_tr_b16 a[168:169],v22 offset:33536
v_mul_f32 v70,s61,v70
v_mfma_f32_16x16x32_bf16 a[104:107],a[164:167],a[220:223],a[104:107]
ds_read_b64_tr_b16 a[164:165],v22 offset:17152
v_mul_f32 v71,s61,v71
v_mfma_f32_16x16x32_bf16 a[104:107],a[160:163],a[216:219],a[104:107]
ds_read_b64_tr_b16 a[160:161],v22 offset:768
v_mul_f32 v12,s61,v12
v_mfma_f32_16x16x32_bf16 a[12:15],a[176:179],a[192:195],a[12:15]
ds_read_b64_tr_b16 a[162:163],v22 offset:2816
v_mul_f32 v13,s61,v13
v_mfma_f32_16x16x32_bf16 a[12:15],a[180:183],a[196:199],a[12:15]
buffer_load_dwordx4 v4,s[20:23],s47 offen offset:1024 nt lds
v_mfma_f32_16x16x32_bf16 a[12:15],a[184:187],a[224:227],a[12:15]
ds_read_b64_tr_b16 a[166:167],v22 offset:19200
v_cvt_pk_bf16_f32 v66,v66,v67
v_mfma_f32_16x16x32_bf16 a[12:15],a[188:191],a[228:231],a[12:15]
ds_read_b64_tr_b16 a[170:171],v22 offset:35584
v_mul_f32 v14,s61,v14
v_mfma_f32_16x16x32_bf16 a[44:47],a[188:191],a[236:239],a[44:47]
ds_read_b64_tr_b16 a[174:175],v22 offset:51968
v_mul_f32 v15,s61,v15
v_mfma_f32_16x16x32_bf16 a[44:47],a[184:187],a[232:235],a[44:47]
v_cvt_pk_bf16_f32 v82,v82,v83
v_cvt_pk_bf16_f32 v113,v114,v115
v_mfma_f32_16x16x32_bf16 a[44:47],a[180:183],a[204:207],a[44:47]
v_cvt_pk_bf16_f32 v69,v70,v71
v_accvgpr_read_b32 v100,a72
v_mfma_f32_16x16x32_bf16 a[44:47],a[176:179],a[200:203],a[44:47]
v_accvgpr_read_b32 v101,a73
v_accvgpr_read_b32 v116,a104
v_mfma_f32_16x16x32_bf16 a[76:79],a[176:179],a[208:211],a[76:79]
v_accvgpr_read_b32 v117,a105
v_cvt_pk_bf16_f32 v67,v12,v13
v_mfma_f32_16x16x32_bf16 a[76:79],a[180:183],a[212:215],a[76:79]
v_cvt_pk_bf16_f32 v83,v14,v15
v_accvgpr_read_b32 v114,a100
v_mfma_f32_16x16x32_bf16 a[76:79],a[184:187],a[240:243],a[76:79]
v_accvgpr_read_b32 v115,a101
v_accvgpr_read_b32 v70,a12
v_mfma_f32_16x16x32_bf16 a[76:79],a[188:191],a[244:247],a[76:79]
v_accvgpr_read_b32 v71,a13
v_mul_f32 v100,s61,v100
v_mfma_f32_16x16x32_bf16 a[108:111],a[188:191],a[252:255],a[108:111]
ds_read_b64_tr_b16 a[188:189],v22 offset:49928
v_mul_f32 v101,s61,v101
v_mfma_f32_16x16x32_bf16 a[108:111],a[184:187],a[248:251],a[108:111]
ds_read_b64_tr_b16 a[184:185],v22 offset:33544
v_accvgpr_read_b32 v102,a74
v_mfma_f32_16x16x32_bf16 a[108:111],a[180:183],a[220:223],a[108:111]
ds_read_b64_tr_b16 a[180:181],v22 offset:17160
v_accvgpr_read_b32 v103,a75
v_mfma_f32_16x16x32_bf16 a[108:111],a[176:179],a[216:219],a[108:111]
buffer_load_dwordx4 v5,s[20:23],s47 offen offset:2048 nt lds
v_mfma_f32_16x16x32_bf16 a[16:19],a[128:131],a[192:195],a[16:19]
ds_read_b64_tr_b16 a[176:177],v22 offset:776
v_mul_f32 v116,s61,v116
v_mfma_f32_16x16x32_bf16 a[16:19],a[132:135],a[196:199],a[16:19]
ds_read_b64_tr_b16 a[178:179],v22 offset:2824
v_mul_f32 v117,s61,v117
v_mfma_f32_16x16x32_bf16 a[16:19],a[136:139],a[224:227],a[16:19]
ds_read_b64_tr_b16 a[182:183],v22 offset:19208
v_accvgpr_read_b32 v118,a106
v_mfma_f32_16x16x32_bf16 a[16:19],a[140:143],a[228:231],a[16:19]
ds_read_b64_tr_b16 a[186:187],v22 offset:35592
v_accvgpr_read_b32 v119,a107
v_mfma_f32_16x16x32_bf16 a[48:51],a[140:143],a[236:239],a[48:51]
ds_read_b64_tr_b16 a[190:191],v22 offset:51976
v_accvgpr_read_b32 v12,a102
v_mfma_f32_16x16x32_bf16 a[48:51],a[136:139],a[232:235],a[48:51]
v_accvgpr_read_b32 v13,a103
v_mul_f32 v114,s61,v114
v_mfma_f32_16x16x32_bf16 a[48:51],a[132:135],a[204:207],a[48:51]
v_mul_f32 v115,s61,v115
v_accvgpr_read_b32 v14,a14
v_mfma_f32_16x16x32_bf16 a[48:51],a[128:131],a[200:203],a[48:51]
v_accvgpr_read_b32 v15,a15
v_mul_f32 v70,s61,v70
v_mfma_f32_16x16x32_bf16 a[80:83],a[128:131],a[208:211],a[80:83]
v_mul_f32 v71,s61,v71
v_cvt_pk_bf16_f32 v100,v100,v101
v_mfma_f32_16x16x32_bf16 a[80:83],a[132:135],a[212:215],a[80:83]
v_mul_f32 v102,s61,v102
v_mul_f32 v103,s61,v103
v_mfma_f32_16x16x32_bf16 a[80:83],a[136:139],a[240:243],a[80:83]
v_cvt_pk_bf16_f32 v116,v116,v117
v_mul_f32 v118,s61,v118
v_mfma_f32_16x16x32_bf16 a[80:83],a[140:143],a[244:247],a[80:83]
)A"
R"A(v_mul_f32 v119,s61,v119
v_mul_f32 v12,s61,v12
v_mfma_f32_16x16x32_bf16 a[112:115],a[140:143],a[252:255],a[112:115]
ds_read_b64_tr_b16 a[140:141],v22 offset:50184
v_mul_f32 v13,s61,v13
v_mfma_f32_16x16x32_bf16 a[112:115],a[136:139],a[248:251],a[112:115]
buffer_load_dwordx4 v5,s[20:23],s47 offen offset:3072 nt lds
v_mfma_f32_16x16x32_bf16 a[112:115],a[132:135],a[220:223],a[112:115]
ds_read_b64_tr_b16 a[132:133],v22 offset:50176
v_cvt_pk_bf16_f32 v114,v114,v115
v_mfma_f32_16x16x32_bf16 a[112:115],a[128:131],a[216:219],a[112:115]
s_waitcnt lgkmcnt(2)
ds_read_b64_tr_b16 a[128:129],v22 offset:33792
v_mul_f32 v14,s61,v14
v_mfma_f32_16x16x32_bf16 a[20:23],a[144:147],a[192:195],a[20:23]
ds_read_b64_tr_b16 a[130:131],v22 offset:35840
v_mul_f32 v15,s61,v15
v_mfma_f32_16x16x32_bf16 a[20:23],a[148:151],a[196:199],a[20:23]
ds_read_b64_tr_b16 a[134:135],v22 offset:52224
v_cvt_pk_bf16_f32 v70,v70,v71
v_mfma_f32_16x16x32_bf16 a[20:23],a[152:155],a[224:227],a[20:23]
ds_read_b64_tr_b16 a[136:137],v22 offset:33800
v_cvt_pk_bf16_f32 v101,v102,v103
v_mfma_f32_16x16x32_bf16 a[20:23],a[156:159],a[228:231],a[20:23]
ds_read_b64_tr_b16 a[138:139],v22 offset:35848
v_cvt_pk_bf16_f32 v117,v118,v119
v_mfma_f32_16x16x32_bf16 a[52:55],a[156:159],a[236:239],a[52:55]
ds_read_b64_tr_b16 a[142:143],v22 offset:52232
v_cvt_pk_bf16_f32 v115,v12,v13
v_mfma_f32_16x16x32_bf16 a[52:55],a[152:155],a[232:235],a[52:55]
v_cvt_pk_bf16_f32 v71,v14,v15
v_accvgpr_read_b32 v102,a76
v_mfma_f32_16x16x32_bf16 a[52:55],a[148:151],a[204:207],a[52:55]
v_accvgpr_read_b32 v103,a77
v_accvgpr_read_b32 v118,a108
v_mfma_f32_16x16x32_bf16 a[52:55],a[144:147],a[200:203],a[52:55]
v_accvgpr_read_b32 v119,a109
v_accvgpr_read_b32 v12,a78
v_mfma_f32_16x16x32_bf16 a[84:87],a[144:147],a[208:211],a[84:87]
s_addk_i32 m0,16384
v_accvgpr_read_b32 v13,a79
v_mul_f32 v102,s61,v102
v_mfma_f32_16x16x32_bf16 a[84:87],a[148:151],a[212:215],a[84:87]
v_mul_f32 v103,s61,v103
v_accvgpr_read_b32 v14,a110
v_mfma_f32_16x16x32_bf16 a[84:87],a[152:155],a[240:243],a[84:87]
v_accvgpr_read_b32 v15,a111
v_mul_f32 v118,s61,v118
v_mfma_f32_16x16x32_bf16 a[84:87],a[156:159],a[244:247],a[84:87]
buffer_load_dwordx4 v4,s[24:27],s47 offen nt lds
v_mfma_f32_16x16x32_bf16 a[116:119],a[156:159],a[252:255],a[116:119]
ds_read_b64_tr_b16 a[156:157],v22 offset:50440
v_mul_f32 v119,s61,v119
v_mfma_f32_16x16x32_bf16 a[116:119],a[152:155],a[248:251],a[116:119]
ds_read_b64_tr_b16 a[152:153],v22 offset:34056
v_mul_f32 v12,s61,v12
v_mfma_f32_16x16x32_bf16 a[116:119],a[148:151],a[220:223],a[116:119]
ds_read_b64_tr_b16 a[148:149],v22 offset:50432
v_mul_f32 v13,s61,v13
v_mfma_f32_16x16x32_bf16 a[116:119],a[144:147],a[216:219],a[116:119]
ds_read_b64_tr_b16 a[144:145],v22 offset:34048
v_cvt_pk_bf16_f32 v102,v102,v103
v_mfma_f32_16x16x32_bf16 a[24:27],a[160:163],a[192:195],a[24:27]
ds_read_b64_tr_b16 a[146:147],v22 offset:36096
v_mul_f32 v14,s61,v14
v_mfma_f32_16x16x32_bf16 a[24:27],a[164:167],a[196:199],a[24:27]
ds_read_b64_tr_b16 a[150:151],v22 offset:52480
v_mul_f32 v15,s61,v15
v_mfma_f32_16x16x32_bf16 a[24:27],a[168:171],a[224:227],a[24:27]
ds_read_b64_tr_b16 a[154:155],v22 offset:36104
v_cvt_pk_bf16_f32 v118,v118,v119
v_mfma_f32_16x16x32_bf16 a[24:27],a[172:175],a[228:231],a[24:27]
ds_read_b64_tr_b16 a[158:159],v22 offset:52488
v_cvt_pk_bf16_f32 v103,v12,v13
v_mfma_f32_16x16x32_bf16 a[56:59],a[172:175],a[236:239],a[56:59]
v_cvt_pk_bf16_f32 v119,v14,v15
v_accvgpr_read_b32 v96,a64
v_mfma_f32_16x16x32_bf16 a[56:59],a[168:171],a[232:235],a[56:59]
.p2align 3
v_accvgpr_read_b32 v72,a16
v_accvgpr_read_b32 v73,a17
v_accvgpr_read_b32 v74,a18
v_accvgpr_read_b32 v75,a19
v_accvgpr_read_b32 v12,a20
v_accvgpr_read_b32 v13,a21
v_accvgpr_read_b32 v14,a22
v_accvgpr_read_b32 v15,a23
v_pk_mul_f32 v[72:73],s[60:61],v[72:73] op_sel:[1,0] op_sel_hi:[1,1]
v_pk_mul_f32 v[74:75],s[60:61],v[74:75] op_sel:[1,0] op_sel_hi:[1,1]
v_pk_mul_f32 v[12:13],s[60:61],v[12:13] op_sel:[1,0] op_sel_hi:[1,1]
v_pk_mul_f32 v[14:15],s[60:61],v[14:15] op_sel:[1,0] op_sel_hi:[1,1]
v_cvt_pk_bf16_f32 v72,v72,v73
v_cvt_pk_bf16_f32 v73,v74,v75
v_cvt_pk_bf16_f32 v74,v12,v13
v_cvt_pk_bf16_f32 v75,v14,v15
v_mfma_f32_16x16x32_bf16 a[56:59],a[164:167],a[204:207],a[56:59]
v_accvgpr_read_b32 v97,a65
v_mul_f32 v96,s61,v96
v_mfma_f32_16x16x32_bf16 a[56:59],a[160:163],a[200:203],a[56:59]
.p2align 3
v_accvgpr_read_b32 v88,a48
v_accvgpr_read_b32 v89,a49
v_accvgpr_read_b32 v90,a50
v_accvgpr_read_b32 v91,a51
v_accvgpr_read_b32 v12,a52
v_accvgpr_read_b32 v13,a53
v_accvgpr_read_b32 v14,a54
v_accvgpr_read_b32 v15,a55
v_pk_mul_f32 v[88:89],s[60:61],v[88:89] op_sel:[1,0] op_sel_hi:[1,1]
v_pk_mul_f32 v[90:91],s[60:61],v[90:91] op_sel:[1,0] op_sel_hi:[1,1]
v_pk_mul_f32 v[12:13],s[60:61],v[12:13] op_sel:[1,0] op_sel_hi:[1,1]
v_pk_mul_f32 v[14:15],s[60:61],v[14:15] op_sel:[1,0] op_sel_hi:[1,1]
v_cvt_pk_bf16_f32 v88,v88,v89
v_cvt_pk_bf16_f32 v89,v90,v91
v_cvt_pk_bf16_f32 v90,v12,v13
v_cvt_pk_bf16_f32 v91,v14,v15
v_mfma_f32_16x16x32_bf16 a[88:91],a[160:163],a[208:211],a[88:91]
v_mul_f32 v97,s61,v97
v_accvgpr_read_b32 v98,a66
v_mfma_f32_16x16x32_bf16 a[88:91],a[164:167],a[212:215],a[88:91]
buffer_load_dwordx4 v5,s[24:27],s47 offen offset:1024 nt lds
v_mfma_f32_16x16x32_bf16 a[88:91],a[168:171],a[240:243],a[88:91]
v_accvgpr_read_b32 v99,a67
v_cvt_pk_bf16_f32 v96,v96,v97
v_mfma_f32_16x16x32_bf16 a[88:91],a[172:175],a[244:247],a[88:91]
v_mul_f32 v98,s61,v98
v_mul_f32 v99,s61,v99
v_mfma_f32_16x16x32_bf16 a[120:123],a[172:175],a[252:255],a[120:123]
ds_read_b64_tr_b16 a[172:173],v22 offset:50696
v_cvt_pk_bf16_f32 v97,v98,v99
v_mfma_f32_16x16x32_bf16 a[120:123],a[168:171],a[248:251],a[120:123]
ds_read_b64_tr_b16 a[168:169],v22 offset:34312
v_accvgpr_read_b32 v84,a40
v_mfma_f32_16x16x32_bf16 a[120:123],a[164:167],a[220:223],a[120:123]
ds_read_b64_tr_b16 a[164:165],v22 offset:50688
v_accvgpr_read_b32 v85,a41
v_mfma_f32_16x16x32_bf16 a[120:123],a[160:163],a[216:219],a[120:123]
ds_read_b64_tr_b16 a[160:161],v22 offset:34304
v_accvgpr_read_b32 v98,a68
v_mfma_f32_16x16x32_bf16 a[28:31],a[176:179],a[192:195],a[28:31]
ds_read_b64_tr_b16 a[162:163],v22 offset:36352
v_accvgpr_read_b32 v99,a69
v_mfma_f32_16x16x32_bf16 a[28:31],a[180:183],a[196:199],a[28:31]
ds_read_b64_tr_b16 a[166:167],v22 offset:52736
v_mul_f32 v84,s61,v84
v_mfma_f32_16x16x32_bf16 a[28:31],a[184:187],a[224:227],a[28:31]
ds_read_b64_tr_b16 a[170:171],v22 offset:36360
v_mul_f32 v85,s61,v85
v_mfma_f32_16x16x32_bf16 a[28:31],a[188:191],a[228:231],a[28:31]
ds_read_b64_tr_b16 a[174:175],v22 offset:52744
v_accvgpr_read_b32 v86,a42
v_mfma_f32_16x16x32_bf16 a[60:63],a[188:191],a[236:239],a[60:63]
.p2align 3
v_accvgpr_read_b32 v104,a80
v_accvgpr_read_b32 v105,a81
v_accvgpr_read_b32 v106,a82
v_accvgpr_read_b32 v107,a83
v_accvgpr_read_b32 v12,a84
v_accvgpr_read_b32 v13,a85
v_accvgpr_read_b32 v14,a86
v_accvgpr_read_b32 v15,a87
v_pk_mul_f32 v[104:105],s[60:61],v[104:105] op_sel:[1,0] op_sel_hi:[1,1]
v_pk_mul_f32 v[106:107],s[60:61],v[106:107] op_sel:[1,0] op_sel_hi:[1,1]
v_pk_mul_f32 v[12:13],s[60:61],v[12:13] op_sel:[1,0] op_sel_hi:[1,1]
v_pk_mul_f32 v[14:15],s[60:61],v[14:15] op_sel:[1,0] op_sel_hi:[1,1]
v_cvt_pk_bf16_f32 v104,v104,v105
v_cvt_pk_bf16_f32 v105,v106,v107
v_cvt_pk_bf16_f32 v106,v12,v13
v_cvt_pk_bf16_f32 v107,v14,v15
v_mfma_f32_16x16x32_bf16 a[60:63],a[184:187],a[232:235],a[60:63]
v_accvgpr_read_b32 v87,a43
v_accvgpr_read_b32 v16,a70
v_mfma_f32_16x16x32_bf16 a[60:63],a[180:183],a[204:207],a[60:63]
.p2align 3
v_accvgpr_read_b32 v120,a112
v_accvgpr_read_b32 v121,a113
v_accvgpr_read_b32 v122,a114
v_accvgpr_read_b32 v123,a115
v_accvgpr_read_b32 v12,a116
v_accvgpr_read_b32 v13,a117
v_accvgpr_read_b32 v14,a118
v_accvgpr_read_b32 v15,a119
v_pk_mul_f32 v[120:121],s[60:61],v[120:121] op_sel:[1,0] op_sel_hi:[1,1]
v_pk_mul_f32 v[122:123],s[60:61],v[122:123] op_sel:[1,0] op_sel_hi:[1,1]
v_pk_mul_f32 v[12:13],s[60:61],v[12:13] op_sel:[1,0] op_sel_hi:[1,1]
v_pk_mul_f32 v[14:15],s[60:61],v[14:15] op_sel:[1,0] op_sel_hi:[1,1]
v_cvt_pk_bf16_f32 v120,v120,v121
v_cvt_pk_bf16_f32 v121,v122,v123
v_cvt_pk_bf16_f32 v122,v12,v13
v_cvt_pk_bf16_f32 v123,v14,v15
v_mfma_f32_16x16x32_bf16 a[60:63],a[176:179],a[200:203],a[60:63]
buffer_load_dwordx4 v4,s[24:27],s47 offen offset:2048 nt lds
v_mfma_f32_16x16x32_bf16 a[92:95],a[176:179],a[208:211],a[92:95]
v_accvgpr_read_b32 v17,a71
v_mul_f32 v98,s61,v98
v_mfma_f32_16x16x32_bf16 a[92:95],a[180:183],a[212:215],a[92:95]
v_mul_f32 v99,s61,v99
v_cvt_pk_bf16_f32 v84,v84,v85
v_mfma_f32_16x16x32_bf16 a[92:95],a[184:187],a[240:243],a[92:95]
v_mul_f32 v86,s61,v86
v_mul_f32 v87,s61,v87
v_mfma_f32_16x16x32_bf16 a[92:95],a[188:191],a[244:247],a[92:95]
v_mul_f32 v16,s61,v16
v_mul_f32 v17,s61,v17
v_mfma_f32_16x16x32_bf16 a[124:127],a[188:191],a[252:255],a[124:127]
ds_read_b64_tr_b16 a[188:189],v22 offset:50952
v_cvt_pk_bf16_f32 v98,v98,v99
v_mfma_f32_16x16x32_bf16 a[124:127],a[184:187],a[248:251],a[124:127]
ds_read_b64_tr_b16 a[184:185],v22 offset:34568
v_cvt_pk_bf16_f32 v85,v86,v87
v_mfma_f32_16x16x32_bf16 a[124:127],a[180:183],a[220:223],a[124:127]
ds_read_b64_tr_b16 a[180:181],v22 offset:50944
v_cvt_pk_bf16_f32 v99,v16,v17
v_mfma_f32_16x16x32_bf16 a[124:127],a[176:179],a[216:219],a[124:127]
s_waitcnt lgkmcnt(3)
ds_read_b64_tr_b16 a[176:177],v22 offset:34560
v_accvgpr_read_b32 v86,a44
v_mfma_f32_16x16x32_bf16 v[128:131],a[128:131],a[224:227],v[128:131]
ds_read_b64_tr_b16 a[178:179],v22 offset:36608
v_accvgpr_read_b32 v87,a45
v_mfma_f32_16x16x32_bf16 v[128:131],a[132:135],a[228:231],v[128:131]
ds_read_b64_tr_b16 a[182:183],v22 offset:52992
v_accvgpr_read_b32 v16,a46
v_mfma_f32_16x16x32_bf16 v[136:139],a[132:135],a[236:239],v[136:139]
ds_read_b64_tr_b16 a[186:187],v22 offset:36616
v_accvgpr_read_b32 v17,a47
v_mfma_f32_16x16x32_bf16 v[136:139],a[128:131],a[232:235],v[136:139]
ds_read_b64_tr_b16 a[190:191],v22 offset:53000
v_mul_f32 v86,s61,v86
v_mfma_f32_16x16x32_bf16 v[144:147],a[128:131],a[240:243],v[144:147]
.p2align 3
v_accvgpr_read_b32 v76,a24
v_accvgpr_read_b32 v77,a25
v_accvgpr_read_b32 v78,a26
v_accvgpr_read_b32 v79,a27
v_accvgpr_read_b32 v12,a28
v_accvgpr_read_b32 v13,a29
v_accvgpr_read_b32 v14,a30
v_accvgpr_read_b32 v15,a31
v_pk_mul_f32 v[76:77],s[60:61],v[76:77] op_sel:[1,0] op_sel_hi:[1,1]
v_pk_mul_f32 v[78:79],s[60:61],v[78:79] op_sel:[1,0] op_sel_hi:[1,1]
v_pk_mul_f32 v[12:13],s[60:61],v[12:13] op_sel:[1,0] op_sel_hi:[1,1]
v_pk_mul_f32 v[14:15],s[60:61],v[14:15] op_sel:[1,0] op_sel_hi:[1,1]
v_cvt_pk_bf16_f32 v76,v76,v77
v_cvt_pk_bf16_f32 v77,v78,v79
v_cvt_pk_bf16_f32 v78,v12,v13
v_cvt_pk_bf16_f32 v79,v14,v15
v_mfma_f32_16x16x32_bf16 v[144:147],a[132:135],a[244:247],v[144:147]
buffer_load_dwordx4 v5,s[24:27],s47 offen offset:3072 nt lds
v_mfma_f32_16x16x32_bf16 v[152:155],a[132:135],a[252:255],v[152:155]
s_or_b32 s47,s47,0x18000
s_addk_i32 s47,4096
s_and_b32 s47,s47,0x27000
v_mfma_f32_16x16x32_bf16 v[152:155],a[128:131],a[248:251],v[152:155]
s_or_b32 s47,s47,s46
v_mul_f32 v87,s61,v87
v_mul_f32 v16,s61,v16
v_mfma_f32_16x16x32_bf16 v[132:135],a[136:139],a[224:227],v[132:135]
s_and_b32 s46,s54,0xfffc0000
v_mul_f32 v17,s61,v17
v_cvt_pk_bf16_f32 v86,v86,v87
v_mfma_f32_16x16x32_bf16 v[132:135],a[140:143],a[228:231],v[132:135]
s_addk_i32 s54,16384
v_cvt_pk_bf16_f32 v87,v16,v17
v_mfma_f32_16x16x32_bf16 v[140:143],a[140:143],a[236:239],v[140:143]
.p2align 3
v_accvgpr_read_b32 v92,a56
v_accvgpr_read_b32 v93,a57
v_accvgpr_read_b32 v94,a58
v_accvgpr_read_b32 v95,a59
v_accvgpr_read_b32 v12,a60
v_accvgpr_read_b32 v13,a61
v_accvgpr_read_b32 v14,a62
v_accvgpr_read_b32 v15,a63
v_pk_mul_f32 v[92:93],s[60:61],v[92:93] op_sel:[1,0] op_sel_hi:[1,1]
v_pk_mul_f32 v[94:95],s[60:61],v[94:95] op_sel:[1,0] op_sel_hi:[1,1]
v_pk_mul_f32 v[12:13],s[60:61],v[12:13] op_sel:[1,0] op_sel_hi:[1,1]
v_pk_mul_f32 v[14:15],s[60:61],v[14:15] op_sel:[1,0] op_sel_hi:[1,1]
v_cvt_pk_bf16_f32 v92,v92,v93
v_cvt_pk_bf16_f32 v93,v94,v95
v_cvt_pk_bf16_f32 v94,v12,v13
v_cvt_pk_bf16_f32 v95,v14,v15
v_mfma_f32_16x16x32_bf16 v[140:143],a[136:139],a[232:235],v[140:143]
.p2align 3
v_accvgpr_read_b32 v108,a88
v_accvgpr_read_b32 v109,a89
v_accvgpr_read_b32 v110,a90
)A"
R"A(v_accvgpr_read_b32 v111,a91
v_accvgpr_read_b32 v12,a92
v_accvgpr_read_b32 v13,a93
v_accvgpr_read_b32 v14,a94
v_accvgpr_read_b32 v15,a95
v_pk_mul_f32 v[108:109],s[60:61],v[108:109] op_sel:[1,0] op_sel_hi:[1,1]
v_pk_mul_f32 v[110:111],s[60:61],v[110:111] op_sel:[1,0] op_sel_hi:[1,1]
v_pk_mul_f32 v[12:13],s[60:61],v[12:13] op_sel:[1,0] op_sel_hi:[1,1]
v_pk_mul_f32 v[14:15],s[60:61],v[14:15] op_sel:[1,0] op_sel_hi:[1,1]
v_cvt_pk_bf16_f32 v108,v108,v109
v_cvt_pk_bf16_f32 v109,v110,v111
v_cvt_pk_bf16_f32 v110,v12,v13
v_cvt_pk_bf16_f32 v111,v14,v15
v_mfma_f32_16x16x32_bf16 v[148:151],a[136:139],a[240:243],v[148:151]
.p2align 3
v_accvgpr_read_b32 v124,a120
v_accvgpr_read_b32 v125,a121
v_accvgpr_read_b32 v126,a122
v_accvgpr_read_b32 v127,a123
v_accvgpr_read_b32 v12,a124
v_accvgpr_read_b32 v13,a125
v_accvgpr_read_b32 v14,a126
v_accvgpr_read_b32 v15,a127
v_pk_mul_f32 v[124:125],s[60:61],v[124:125] op_sel:[1,0] op_sel_hi:[1,1]
v_pk_mul_f32 v[126:127],s[60:61],v[126:127] op_sel:[1,0] op_sel_hi:[1,1]
v_pk_mul_f32 v[12:13],s[60:61],v[12:13] op_sel:[1,0] op_sel_hi:[1,1]
v_pk_mul_f32 v[14:15],s[60:61],v[14:15] op_sel:[1,0] op_sel_hi:[1,1]
v_cvt_pk_bf16_f32 v124,v124,v125
v_cvt_pk_bf16_f32 v125,v126,v127
v_cvt_pk_bf16_f32 v126,v12,v13
v_cvt_pk_bf16_f32 v127,v14,v15
v_mfma_f32_16x16x32_bf16 v[148:151],a[140:143],a[244:247],v[148:151]
v_mfma_f32_16x16x32_bf16 v[156:159],a[140:143],a[252:255],v[156:159]
s_waitcnt vmcnt(8) lgkmcnt(0)
v_mfma_f32_16x16x32_bf16 v[156:159],a[136:139],a[248:251],v[156:159]
s_barrier
ds_read_b128 v[48:51],v18
v_mfma_f32_16x16x32_bf16 v[160:163],a[144:147],a[224:227],v[160:163]
ds_read_b64_tr_b16 a[192:193],v2
v_mfma_f32_16x16x32_bf16 v[160:163],a[148:151],a[228:231],v[160:163]
ds_read_b64_tr_b16 a[194:195],v2 offset:4096
v_mfma_f32_16x16x32_bf16 v[168:171],a[148:151],a[236:239],v[168:171]
ds_read_b64_tr_b16 a[196:197],v2 offset:8192
v_mfma_f32_16x16x32_bf16 v[168:171],a[144:147],a[232:235],v[168:171]
ds_read_b128 v[52:55],v18 offset:256
v_mfma_f32_16x16x32_bf16 v[176:179],a[144:147],a[240:243],v[176:179]
v_mul_f32 v128,s61,v128
v_mul_f32 v129,s61,v129
v_mul_f32 v130,s61,v130
v_mul_f32 v131,s61,v131
.p2align 3
v_cvt_pk_bf16_f32 v128,v128,v129
v_cvt_pk_bf16_f32 v129,v130,v131
v_pk_mul_f32 v[132:133],s[60:61],v[132:133] op_sel:[1,0] op_sel_hi:[1,1]
v_pk_mul_f32 v[134:135],s[60:61],v[134:135] op_sel:[1,0] op_sel_hi:[1,1]
v_pk_mul_f32 v[136:137],s[60:61],v[136:137] op_sel:[1,0] op_sel_hi:[1,1]
v_pk_mul_f32 v[138:139],s[60:61],v[138:139] op_sel:[1,0] op_sel_hi:[1,1]
v_pk_mul_f32 v[140:141],s[60:61],v[140:141] op_sel:[1,0] op_sel_hi:[1,1]
v_pk_mul_f32 v[142:143],s[60:61],v[142:143] op_sel:[1,0] op_sel_hi:[1,1]
v_pk_mul_f32 v[144:145],s[60:61],v[144:145] op_sel:[1,0] op_sel_hi:[1,1]
v_pk_mul_f32 v[146:147],s[60:61],v[146:147] op_sel:[1,0] op_sel_hi:[1,1]
v_pk_mul_f32 v[148:149],s[60:61],v[148:149] op_sel:[1,0] op_sel_hi:[1,1]
v_pk_mul_f32 v[150:151],s[60:61],v[150:151] op_sel:[1,0] op_sel_hi:[1,1]
v_pk_mul_f32 v[152:153],s[60:61],v[152:153] op_sel:[1,0] op_sel_hi:[1,1]
v_pk_mul_f32 v[154:155],s[60:61],v[154:155] op_sel:[1,0] op_sel_hi:[1,1]
v_pk_mul_f32 v[156:157],s[60:61],v[156:157] op_sel:[1,0] op_sel_hi:[1,1]
v_pk_mul_f32 v[158:159],s[60:61],v[158:159] op_sel:[1,0] op_sel_hi:[1,1]
v_mfma_f32_16x16x32_bf16 v[176:179],a[148:151],a[244:247],v[176:179]
ds_read_b128 v[56:59],v18 offset:512
v_cvt_pk_bf16_f32 v130,v132,v133
v_mfma_f32_16x16x32_bf16 v[184:187],a[148:151],a[252:255],v[184:187]
v_cvt_pk_bf16_f32 v132,v136,v137
v_cvt_pk_bf16_f32 v133,v138,v139
v_mfma_f32_16x16x32_bf16 v[184:187],a[144:147],a[248:251],v[184:187]
ds_read_b128 v[60:63],v18 offset:768
v_cvt_pk_bf16_f32 v131,v134,v135
v_mfma_f32_16x16x32_bf16 v[164:167],a[152:155],a[224:227],v[164:167]
v_cvt_pk_bf16_f32 v134,v140,v141
v_cvt_pk_bf16_f32 v135,v142,v143
v_mfma_f32_16x16x32_bf16 v[164:167],a[156:159],a[228:231],v[164:167]
ds_read_b128 a[128:131],v1
v_cvt_pk_bf16_f32 v144,v144,v145
v_mfma_f32_16x16x32_bf16 v[172:175],a[156:159],a[236:239],v[172:175]
v_cvt_pk_bf16_f32 v145,v146,v147
v_cvt_pk_bf16_f32 v146,v148,v149
v_mfma_f32_16x16x32_bf16 v[172:175],a[152:155],a[232:235],v[172:175]
ds_read_b128 a[132:135],v1 offset:256
v_cvt_pk_bf16_f32 v148,v152,v153
v_mfma_f32_16x16x32_bf16 v[180:183],a[152:155],a[240:243],v[180:183]
v_cvt_pk_bf16_f32 v149,v154,v155
v_cvt_pk_bf16_f32 v147,v150,v151
v_mfma_f32_16x16x32_bf16 v[180:183],a[156:159],a[244:247],v[180:183]
ds_read_b128 a[136:139],v1 offset:512
v_cvt_pk_bf16_f32 v150,v156,v157
v_mfma_f32_16x16x32_bf16 v[188:191],a[156:159],a[252:255],v[188:191]
ds_read_b64_tr_b16 a[198:199],v2 offset:12288
v_cvt_pk_bf16_f32 v151,v158,v159
v_mfma_f32_16x16x32_bf16 v[188:191],a[152:155],a[248:251],v[188:191]
ds_read_b64_tr_b16 a[204:205],v3 offset:8192
v_mfma_f32_16x16x32_bf16 v[192:195],a[160:163],a[224:227],v[192:195]
ds_read_b64_tr_b16 a[206:207],v3 offset:12288
v_mfma_f32_16x16x32_bf16 v[192:195],a[164:167],a[228:231],v[192:195]
s_add_u32 m0,32768,s51
ds_read_b128 a[140:143],v1 offset:768
v_mfma_f32_16x16x32_bf16 v[200:203],a[164:167],a[236:239],v[200:203]
ds_read_b64_tr_b16 a[200:201],v3
v_mfma_f32_16x16x32_bf16 v[200:203],a[160:163],a[232:235],v[200:203]
ds_read_b64_tr_b16 a[202:203],v3 offset:4096
v_mfma_f32_16x16x32_bf16 v[208:211],a[160:163],a[240:243],v[208:211]
buffer_load_dwordx4 v4,s[20:23],s47 offen nt lds
v_mfma_f32_16x16x32_bf16 v[208:211],a[164:167],a[244:247],v[208:211]
ds_read_b128 a[156:159],v1 offset:4864
v_mfma_f32_16x16x32_bf16 v[216:219],a[164:167],a[252:255],v[216:219]
v_mul_f32 v160,s61,v160
v_mul_f32 v161,s61,v161
v_mul_f32 v162,s61,v162
v_mul_f32 v163,s61,v163
.p2align 3
v_cvt_pk_bf16_f32 v160,v160,v161
v_cvt_pk_bf16_f32 v161,v162,v163
v_pk_mul_f32 v[164:165],s[60:61],v[164:165] op_sel:[1,0] op_sel_hi:[1,1]
v_pk_mul_f32 v[166:167],s[60:61],v[166:167] op_sel:[1,0] op_sel_hi:[1,1]
v_pk_mul_f32 v[168:169],s[60:61],v[168:169] op_sel:[1,0] op_sel_hi:[1,1]
v_pk_mul_f32 v[170:171],s[60:61],v[170:171] op_sel:[1,0] op_sel_hi:[1,1]
v_pk_mul_f32 v[172:173],s[60:61],v[172:173] op_sel:[1,0] op_sel_hi:[1,1]
v_pk_mul_f32 v[174:175],s[60:61],v[174:175] op_sel:[1,0] op_sel_hi:[1,1]
v_pk_mul_f32 v[176:177],s[60:61],v[176:177] op_sel:[1,0] op_sel_hi:[1,1]
v_pk_mul_f32 v[178:179],s[60:61],v[178:179] op_sel:[1,0] op_sel_hi:[1,1]
v_pk_mul_f32 v[180:181],s[60:61],v[180:181] op_sel:[1,0] op_sel_hi:[1,1]
v_pk_mul_f32 v[182:183],s[60:61],v[182:183] op_sel:[1,0] op_sel_hi:[1,1]
v_pk_mul_f32 v[184:185],s[60:61],v[184:185] op_sel:[1,0] op_sel_hi:[1,1]
v_pk_mul_f32 v[186:187],s[60:61],v[186:187] op_sel:[1,0] op_sel_hi:[1,1]
v_pk_mul_f32 v[188:189],s[60:61],v[188:189] op_sel:[1,0] op_sel_hi:[1,1]
v_pk_mul_f32 v[190:191],s[60:61],v[190:191] op_sel:[1,0] op_sel_hi:[1,1]
v_mfma_f32_16x16x32_bf16 v[216:219],a[160:163],a[248:251],v[216:219]
ds_read_b128 a[152:155],v1 offset:4608
v_cvt_pk_bf16_f32 v162,v164,v165
v_mfma_f32_16x16x32_bf16 v[196:199],a[168:171],a[224:227],v[196:199]
v_cvt_pk_bf16_f32 v164,v168,v169
v_cvt_pk_bf16_f32 v165,v170,v171
v_mfma_f32_16x16x32_bf16 v[196:199],a[172:175],a[228:231],v[196:199]
ds_read_b128 a[148:151],v1 offset:4352
v_cvt_pk_bf16_f32 v163,v166,v167
v_mfma_f32_16x16x32_bf16 v[204:207],a[172:175],a[236:239],v[204:207]
v_cvt_pk_bf16_f32 v166,v172,v173
v_cvt_pk_bf16_f32 v167,v174,v175
v_mfma_f32_16x16x32_bf16 v[204:207],a[168:171],a[232:235],v[204:207]
ds_read_b128 a[144:147],v1 offset:4096
v_cvt_pk_bf16_f32 v176,v176,v177
v_mfma_f32_16x16x32_bf16 v[212:215],a[168:171],a[240:243],v[212:215]
v_cvt_pk_bf16_f32 v177,v178,v179
v_cvt_pk_bf16_f32 v178,v180,v181
v_mfma_f32_16x16x32_bf16 v[212:215],a[172:175],a[244:247],v[212:215]
ds_read_b128 a[160:163],v1 offset:8192
v_cvt_pk_bf16_f32 v180,v184,v185
v_mfma_f32_16x16x32_bf16 v[220:223],a[172:175],a[252:255],v[220:223]
v_cvt_pk_bf16_f32 v181,v186,v187
v_cvt_pk_bf16_f32 v179,v182,v183
v_mfma_f32_16x16x32_bf16 v[220:223],a[168:171],a[248:251],v[220:223]
ds_read_b128 a[164:167],v1 offset:8448
v_cvt_pk_bf16_f32 v182,v188,v189
v_mfma_f32_16x16x32_bf16 v[224:227],a[176:179],a[224:227],v[224:227]
ds_read_b64_tr_b16 a[208:209],v2 offset:256
v_cvt_pk_bf16_f32 v183,v190,v191
v_mfma_f32_16x16x32_bf16 v[224:227],a[180:183],a[228:231],v[224:227]
buffer_load_dwordx4 v4,s[20:23],s47 offen offset:1024 nt lds
v_mfma_f32_16x16x32_bf16 v[232:235],a[180:183],a[236:239],v[232:235]
ds_read_b128 a[168:171],v1 offset:8704
v_mfma_f32_16x16x32_bf16 v[232:235],a[176:179],a[232:235],v[232:235]
ds_read_b64_tr_b16 a[210:211],v2 offset:4352
v_mfma_f32_16x16x32_bf16 v[240:243],a[176:179],a[240:243],v[240:243]
ds_read_b64_tr_b16 a[212:213],v2 offset:8448
v_mfma_f32_16x16x32_bf16 v[240:243],a[180:183],a[244:247],v[240:243]
v_mul_f32 v192,s61,v192
v_mul_f32 v193,s61,v193
v_mul_f32 v194,s61,v194
v_mul_f32 v195,s61,v195
.p2align 3
v_cvt_pk_bf16_f32 v192,v192,v193
v_cvt_pk_bf16_f32 v193,v194,v195
v_pk_mul_f32 v[196:197],s[60:61],v[196:197] op_sel:[1,0] op_sel_hi:[1,1]
v_pk_mul_f32 v[198:199],s[60:61],v[198:199] op_sel:[1,0] op_sel_hi:[1,1]
v_pk_mul_f32 v[200:201],s[60:61],v[200:201] op_sel:[1,0] op_sel_hi:[1,1]
v_pk_mul_f32 v[202:203],s[60:61],v[202:203] op_sel:[1,0] op_sel_hi:[1,1]
v_pk_mul_f32 v[204:205],s[60:61],v[204:205] op_sel:[1,0] op_sel_hi:[1,1]
v_pk_mul_f32 v[206:207],s[60:61],v[206:207] op_sel:[1,0] op_sel_hi:[1,1]
v_pk_mul_f32 v[208:209],s[60:61],v[208:209] op_sel:[1,0] op_sel_hi:[1,1]
v_pk_mul_f32 v[210:211],s[60:61],v[210:211] op_sel:[1,0] op_sel_hi:[1,1]
v_pk_mul_f32 v[212:213],s[60:61],v[212:213] op_sel:[1,0] op_sel_hi:[1,1]
v_pk_mul_f32 v[214:215],s[60:61],v[214:215] op_sel:[1,0] op_sel_hi:[1,1]
v_pk_mul_f32 v[216:217],s[60:61],v[216:217] op_sel:[1,0] op_sel_hi:[1,1]
v_pk_mul_f32 v[218:219],s[60:61],v[218:219] op_sel:[1,0] op_sel_hi:[1,1]
v_pk_mul_f32 v[220:221],s[60:61],v[220:221] op_sel:[1,0] op_sel_hi:[1,1]
v_pk_mul_f32 v[222:223],s[60:61],v[222:223] op_sel:[1,0] op_sel_hi:[1,1]
v_mfma_f32_16x16x32_bf16 v[248:251],a[180:183],a[252:255],v[248:251]
ds_read_b128 a[172:175],v1 offset:8960
v_cvt_pk_bf16_f32 v194,v196,v197
v_mfma_f32_16x16x32_bf16 v[248:251],a[176:179],a[248:251],v[248:251]
v_cvt_pk_bf16_f32 v196,v200,v201
v_cvt_pk_bf16_f32 v197,v202,v203
v_mfma_f32_16x16x32_bf16 v[228:231],a[184:187],a[224:227],v[228:231]
ds_read_b128 a[180:183],v1 offset:12544
v_cvt_pk_bf16_f32 v195,v198,v199
v_mfma_f32_16x16x32_bf16 v[228:231],a[188:191],a[228:231],v[228:231]
v_cvt_pk_bf16_f32 v198,v204,v205
v_cvt_pk_bf16_f32 v199,v206,v207
v_mfma_f32_16x16x32_bf16 v[236:239],a[188:191],a[236:239],v[236:239]
ds_read_b128 a[176:179],v1 offset:12288
v_cvt_pk_bf16_f32 v208,v208,v209
v_mfma_f32_16x16x32_bf16 v[236:239],a[184:187],a[232:235],v[236:239]
v_cvt_pk_bf16_f32 v209,v210,v211
v_cvt_pk_bf16_f32 v210,v212,v213
v_mfma_f32_16x16x32_bf16 v[244:247],a[184:187],a[240:243],v[244:247]
v_cvt_pk_bf16_f32 v212,v216,v217
v_cvt_pk_bf16_f32 v213,v218,v219
v_mfma_f32_16x16x32_bf16 v[244:247],a[188:191],a[244:247],v[244:247]
v_cvt_pk_bf16_f32 v211,v214,v215
v_cvt_pk_bf16_f32 v214,v220,v221
v_mfma_f32_16x16x32_bf16 v[252:255],a[188:191],a[252:255],v[252:255]
buffer_load_dwordx4 v5,s[20:23],s47 offen offset:2048 nt lds
v_mfma_f32_16x16x32_bf16 v[252:255],a[184:187],a[248:251],v[252:255]
s_waitcnt lgkmcnt(2)
ds_read_b128 a[188:191],v1 offset:13056
v_cvt_pk_bf16_f32 v215,v222,v223
v_mfma_f32_16x16x32_bf16 v[32:35],v[48:51],v[64:67],0
ds_read_b64_tr_b16 a[214:215],v2 offset:12544
v_mfma_f32_16x16x32_bf16 v[32:35],v[52:55],v[68:71],v[32:35]
ds_read_b64_tr_b16 a[220:221],v3 offset:8448
v_mfma_f32_16x16x32_bf16 v[32:35],v[56:59],v[72:75],v[32:35]
ds_read_b64_tr_b16 a[222:223],v3 offset:12544
v_mfma_f32_16x16x32_bf16 v[32:35],v[60:63],v[76:79],v[32:35]
ds_read_b128 a[184:187],v1 offset:12800
v_add_u32 v1,s48,v1
v_mfma_f32_16x16x32_bf16 v[36:39],v[48:51],v[80:83],0
ds_read_b64_tr_b16 a[216:217],v3 offset:256
v_mfma_f32_16x16x32_bf16 v[36:39],v[52:55],v[84:87],v[36:39]
ds_read_b64_tr_b16 a[218:219],v3 offset:4352
v_mfma_f32_16x16x32_bf16 v[36:39],v[56:59],v[88:91],v[36:39]
ds_read_b64_tr_b16 a[224:225],v2 offset:512
v_mfma_f32_16x16x32_bf16 v[36:39],v[60:63],v[92:95],v[36:39]
ds_read_b64_tr_b16 a[226:227],v2 offset:4608
)A"
R"A(ds_read_b64_tr_b16 a[228:229],v2 offset:8704
v_mfma_f32_16x16x32_bf16 v[40:43],v[48:51],v[96:99],0
ds_read_b64_tr_b16 a[230:231],v2 offset:12800
v_mfma_f32_16x16x32_bf16 v[40:43],v[52:55],v[100:103],v[40:43]
v_max3_f32 v32,v32,v33,v34
v_max3_f32 v32,v32,v35,s18
v_mfma_f32_16x16x32_bf16 v[40:43],v[56:59],v[104:107],v[40:43]
v_sub_f32 v35,0,v32
v_sub_f32 v34,0,v32
v_mfma_f32_16x16x32_bf16 v[40:43],v[60:63],v[108:111],v[40:43]
v_sub_f32 v33,0,v32
v_sub_f32 v32,0,v32
v_mfma_f32_16x16x32_bf16 v[44:47],v[48:51],v[112:115],0
buffer_load_dwordx4 v5,s[20:23],s47 offen offset:3072 nt lds
v_mfma_f32_16x16x32_bf16 v[44:47],v[52:55],v[116:119],v[44:47]
v_max3_f32 v36,v36,v37,v38
v_max3_f32 v36,v36,v39,s19
v_mfma_f32_16x16x32_bf16 v[44:47],v[56:59],v[120:123],v[44:47]
v_sub_f32 v39,0,v36
v_sub_f32 v38,0,v36
v_mfma_f32_16x16x32_bf16 v[44:47],v[60:63],v[124:127],v[44:47]
v_sub_f32 v37,0,v36
v_sub_f32 v36,0,v36
v_mfma_f32_16x16x32_bf16 v[136:139],a[128:131],v[64:67],v[32:35]
ds_read_b64_tr_b16 a[236:237],v3 offset:8704
ds_read_b64_tr_b16 a[238:239],v3 offset:12800
v_mfma_f32_16x16x32_bf16 v[136:139],a[132:135],v[68:71],v[136:139]
v_max3_f32 v40,v40,v41,v42
v_max3_f32 v40,v40,v43,s100
v_mfma_f32_16x16x32_bf16 v[136:139],a[136:139],v[72:75],v[136:139]
v_sub_f32 v43,0,v40
v_sub_f32 v42,0,v40
v_mfma_f32_16x16x32_bf16 v[136:139],a[140:143],v[76:79],v[136:139]
v_sub_f32 v41,0,v40
v_sub_f32 v40,0,v40
v_mfma_f32_16x16x32_bf16 v[140:143],a[156:159],v[76:79],v[32:35]
ds_read_b64_tr_b16 a[232:233],v3 offset:512
ds_read_b64_tr_b16 a[234:235],v3 offset:4608
v_mfma_f32_16x16x32_bf16 v[140:143],a[152:155],v[72:75],v[140:143]
s_addk_i32 m0,16384
ds_read_b64_tr_b16 a[240:241],v2 offset:768
v_max3_f32 v44,v44,v45,v46
v_mfma_f32_16x16x32_bf16 v[140:143],a[148:151],v[68:71],v[140:143]
ds_read_b64_tr_b16 a[242:243],v2 offset:4864
v_max3_f32 v44,v44,v47,s101
v_mfma_f32_16x16x32_bf16 v[140:143],a[144:147],v[64:67],v[140:143]
ds_read_b64_tr_b16 a[244:245],v2 offset:8960
v_mov_b32 v48,0
v_mfma_f32_16x16x32_bf16 v[152:155],a[160:163],v[64:67],v[32:35]
buffer_load_dwordx4 v4,s[24:27],s47 offen nt lds
v_mfma_f32_16x16x32_bf16 v[152:155],a[164:167],v[68:71],v[152:155]
v_exp_f32 v136,v136
v_mfma_f32_16x16x32_bf16 v[152:155],a[168:171],v[72:75],v[152:155]
v_exp_f32 v137,v137
v_mfma_f32_16x16x32_bf16 v[152:155],a[172:175],v[76:79],v[152:155]
s_waitcnt lgkmcnt(5)
v_exp_f32 v138,v138
v_mfma_f32_16x16x32_bf16 v[156:159],a[188:191],v[76:79],v[32:35]
v_exp_f32 v139,v139
v_mfma_f32_16x16x32_bf16 v[156:159],a[184:187],v[72:75],v[156:159]
v_cvt_pk_bf16_f32 v136,v136,v137
v_cvt_pk_bf16_f32 v137,v138,v139
v_mfma_f32_16x16x32_bf16 v[156:159],a[180:183],v[68:71],v[156:159]
v_exp_f32 v140,v140
v_mfma_f32_16x16x32_bf16 v[156:159],a[176:179],v[64:67],v[156:159]
v_exp_f32 v141,v141
v_mfma_f32_16x16x32_bf16 v[168:171],a[128:131],v[80:83],v[36:39]
v_exp_f32 v142,v142
v_mfma_f32_16x16x32_bf16 v[168:171],a[132:135],v[84:87],v[168:171]
v_exp_f32 v143,v143
v_mfma_f32_16x16x32_bf16 v[168:171],a[136:139],v[88:91],v[168:171]
v_cvt_pk_bf16_f32 v138,v140,v141
v_cvt_pk_bf16_f32 v139,v142,v143
v_mfma_f32_16x16x32_bf16 v[168:171],a[140:143],v[92:95],v[168:171]
v_exp_f32 v152,v152
v_mfma_f32_16x16x32_bf16 v[172:175],a[156:159],v[92:95],v[36:39]
v_exp_f32 v153,v153
v_mfma_f32_16x16x32_bf16 v[172:175],a[152:155],v[88:91],v[172:175]
buffer_load_dwordx4 v5,s[24:27],s47 offen offset:1024 nt lds
v_mfma_f32_16x16x32_bf16 v[172:175],a[148:151],v[84:87],v[172:175]
v_exp_f32 v154,v154
v_mfma_f32_16x16x32_bf16 v[172:175],a[144:147],v[80:83],v[172:175]
v_exp_f32 v155,v155
v_mfma_f32_16x16x32_bf16 v[184:187],a[160:163],v[80:83],v[36:39]
v_exp_f32 v156,v156
v_mfma_f32_16x16x32_bf16 v[184:187],a[164:167],v[84:87],v[184:187]
v_exp_f32 v157,v157
v_mfma_f32_16x16x32_bf16 v[184:187],a[168:171],v[88:91],v[184:187]
v_exp_f32 v158,v158
v_mfma_f32_16x16x32_bf16 v[184:187],a[172:175],v[92:95],v[184:187]
v_exp_f32 v159,v159
v_mfma_f32_16x16x32_bf16 v[188:191],a[188:191],v[92:95],v[36:39]
v_cvt_pk_bf16_f32 v140,v152,v153
v_cvt_pk_bf16_f32 v141,v154,v155
v_mfma_f32_16x16x32_bf16 v[188:191],a[184:187],v[88:91],v[188:191]
v_cvt_pk_bf16_f32 v142,v156,v157
v_cvt_pk_bf16_f32 v143,v158,v159
v_mfma_f32_16x16x32_bf16 v[188:191],a[180:183],v[84:87],v[188:191]
ds_read_b64_tr_b16 a[246:247],v2 offset:13056
ds_read_b64_tr_b16 a[252:253],v3 offset:8960
v_mfma_f32_16x16x32_bf16 v[188:191],a[176:179],v[80:83],v[188:191]
ds_read_b64_tr_b16 a[254:255],v3 offset:13056
v_mov_b32 v49,0
v_mfma_f32_16x16x32_bf16 v[200:203],a[128:131],v[96:99],v[40:43]
ds_read_b64_tr_b16 a[248:249],v3 offset:768
v_mov_b32 v50,0
v_mfma_f32_16x16x32_bf16 v[200:203],a[132:135],v[100:103],v[200:203]
ds_read_b64_tr_b16 a[250:251],v3 offset:4864
v_mov_b32 v51,0
v_mfma_f32_16x16x32_bf16 v[200:203],a[136:139],v[104:107],v[200:203]
buffer_load_dwordx4 v4,s[24:27],s47 offen offset:2048 nt lds
v_mfma_f32_16x16x32_bf16 v[200:203],a[140:143],v[108:111],v[200:203]
v_sub_f32 v47,0,v44
v_sub_f32 v46,0,v44
v_mfma_f32_16x16x32_bf16 v[204:207],a[156:159],v[108:111],v[40:43]
v_sub_f32 v45,0,v44
v_sub_f32 v44,0,v44
v_mfma_f32_16x16x32_bf16 v[204:207],a[152:155],v[104:107],v[204:207]
v_mul_f32 v224,s61,v224
v_mul_f32 v225,s61,v225
v_mul_f32 v226,s61,v226
v_mul_f32 v227,s61,v227
.p2align 3
v_cvt_pk_bf16_f32 v224,v224,v225
v_cvt_pk_bf16_f32 v225,v226,v227
v_pk_mul_f32 v[228:229],s[60:61],v[228:229] op_sel:[1,0] op_sel_hi:[1,1]
v_pk_mul_f32 v[230:231],s[60:61],v[230:231] op_sel:[1,0] op_sel_hi:[1,1]
v_pk_mul_f32 v[232:233],s[60:61],v[232:233] op_sel:[1,0] op_sel_hi:[1,1]
v_pk_mul_f32 v[234:235],s[60:61],v[234:235] op_sel:[1,0] op_sel_hi:[1,1]
v_pk_mul_f32 v[236:237],s[60:61],v[236:237] op_sel:[1,0] op_sel_hi:[1,1]
v_pk_mul_f32 v[238:239],s[60:61],v[238:239] op_sel:[1,0] op_sel_hi:[1,1]
v_pk_mul_f32 v[240:241],s[60:61],v[240:241] op_sel:[1,0] op_sel_hi:[1,1]
v_pk_mul_f32 v[242:243],s[60:61],v[242:243] op_sel:[1,0] op_sel_hi:[1,1]
v_pk_mul_f32 v[244:245],s[60:61],v[244:245] op_sel:[1,0] op_sel_hi:[1,1]
v_pk_mul_f32 v[246:247],s[60:61],v[246:247] op_sel:[1,0] op_sel_hi:[1,1]
v_pk_mul_f32 v[248:249],s[60:61],v[248:249] op_sel:[1,0] op_sel_hi:[1,1]
v_pk_mul_f32 v[250:251],s[60:61],v[250:251] op_sel:[1,0] op_sel_hi:[1,1]
v_pk_mul_f32 v[252:253],s[60:61],v[252:253] op_sel:[1,0] op_sel_hi:[1,1]
v_pk_mul_f32 v[254:255],s[60:61],v[254:255] op_sel:[1,0] op_sel_hi:[1,1]
v_mfma_f32_16x16x32_bf16 v[204:207],a[148:151],v[100:103],v[204:207]
v_cvt_pk_bf16_f32 v226,v228,v229
v_cvt_pk_bf16_f32 v228,v232,v233
v_mfma_f32_16x16x32_bf16 v[204:207],a[144:147],v[96:99],v[204:207]
v_cvt_pk_bf16_f32 v229,v234,v235
v_cvt_pk_bf16_f32 v227,v230,v231
v_mfma_f32_16x16x32_bf16 v[216:219],a[160:163],v[96:99],v[40:43]
v_cvt_pk_bf16_f32 v230,v236,v237
v_cvt_pk_bf16_f32 v231,v238,v239
v_mfma_f32_16x16x32_bf16 v[216:219],a[164:167],v[100:103],v[216:219]
v_cvt_pk_bf16_f32 v240,v240,v241
v_cvt_pk_bf16_f32 v241,v242,v243
v_mfma_f32_16x16x32_bf16 v[216:219],a[168:171],v[104:107],v[216:219]
v_cvt_pk_bf16_f32 v242,v244,v245
v_cvt_pk_bf16_f32 v244,v248,v249
v_mfma_f32_16x16x32_bf16 v[216:219],a[172:175],v[108:111],v[216:219]
v_cvt_pk_bf16_f32 v245,v250,v251
v_cvt_pk_bf16_f32 v243,v246,v247
v_mfma_f32_16x16x32_bf16 v[220:223],a[188:191],v[108:111],v[40:43]
v_cvt_pk_bf16_f32 v246,v252,v253
v_cvt_pk_bf16_f32 v247,v254,v255
v_mfma_f32_16x16x32_bf16 v[220:223],a[184:187],v[104:107],v[220:223]
v_exp_f32 v168,v168
v_mfma_f32_16x16x32_bf16 v[220:223],a[180:183],v[100:103],v[220:223]
v_exp_f32 v169,v169
v_mfma_f32_16x16x32_bf16 v[220:223],a[176:179],v[96:99],v[220:223]
buffer_load_dwordx4 v5,s[24:27],s47 offen offset:3072 nt lds
v_mfma_f32_16x16x32_bf16 a[0:3],a[192:195],v[136:139],0
s_or_b32 s47,s47,0x18000
s_addk_i32 s47,4096
s_and_b32 s47,s47,0x27000
v_mfma_f32_16x16x32_bf16 a[0:3],a[196:199],v[140:143],a[0:3]
s_or_b32 s47,s47,s46
v_exp_f32 v170,v170
v_mfma_f32_16x16x32_bf16 a[4:7],a[204:207],v[140:143],0
s_and_b32 s46,s54,0xfffc0000
v_exp_f32 v171,v171
v_mfma_f32_16x16x32_bf16 a[4:7],a[200:203],v[136:139],a[4:7]
s_memrealtime s[76:77]
v_cvt_pk_bf16_f32 v168,v168,v169
v_cvt_pk_bf16_f32 v169,v170,v171
v_mfma_f32_16x16x32_bf16 a[8:11],a[208:211],v[136:139],0
v_exp_f32 v172,v172
v_mfma_f32_16x16x32_bf16 a[8:11],a[212:215],v[140:143],a[8:11]
v_exp_f32 v173,v173
v_mfma_f32_16x16x32_bf16 a[12:15],a[220:223],v[140:143],0
v_exp_f32 v174,v174
v_mfma_f32_16x16x32_bf16 a[12:15],a[216:219],v[136:139],a[12:15]
v_exp_f32 v175,v175
v_mfma_f32_16x16x32_bf16 a[16:19],a[224:227],v[136:139],0
v_cvt_pk_bf16_f32 v170,v172,v173
v_cvt_pk_bf16_f32 v171,v174,v175
v_mfma_f32_16x16x32_bf16 a[16:19],a[228:231],v[140:143],a[16:19]
v_exp_f32 v184,v184
v_mfma_f32_16x16x32_bf16 a[20:23],a[236:239],v[140:143],0
s_waitcnt lgkmcnt(0)
v_exp_f32 v185,v185
v_mfma_f32_16x16x32_bf16 a[20:23],a[232:235],v[136:139],a[20:23]
s_add_u32 s78,s76,s60
v_exp_f32 v186,v186
v_mfma_f32_16x16x32_bf16 a[24:27],a[240:243],v[136:139],0
s_lshr_b32 s60,s94,2
v_exp_f32 v187,v187
v_mfma_f32_16x16x32_bf16 a[24:27],a[244:247],v[140:143],a[24:27]
s_max_i32 s60,s60,124
v_exp_f32 v188,v188
v_mfma_f32_16x16x32_bf16 a[28:31],a[252:255],v[140:143],0
v_exp_f32 v189,v189
v_mfma_f32_16x16x32_bf16 a[28:31],a[248:251],v[136:139],a[28:31]
v_exp_f32 v190,v190
v_smfmac_f32_16x16x64_bf16 v[48:51],v[28:31],v[136:143],v27
v_cvt_pk_bf16_f32 v172,v184,v185
v_mfma_f32_16x16x32_bf16 v[232:235],a[128:131],v[112:115],v[44:47]
s_waitcnt vmcnt(8)
v_exp_f32 v191,v191
v_mfma_f32_16x16x32_bf16 v[232:235],a[132:135],v[116:119],v[232:235]
s_barrier
v_cvt_pk_bf16_f32 v173,v186,v187
v_cvt_pk_bf16_f32 v174,v188,v189
v_mfma_f32_16x16x32_bf16 v[232:235],a[136:139],v[120:123],v[232:235]
ds_read_b128 a[128:131],v1
v_cvt_pk_bf16_f32 v175,v190,v191
v_mfma_f32_16x16x32_bf16 v[232:235],a[140:143],v[124:127],v[232:235]
s_add_u32 m0,65536,s51
v_mov_b32 v52,0
v_mov_b32 v53,0
v_mfma_f32_16x16x32_bf16 v[236:239],a[156:159],v[124:127],v[44:47]
ds_read_b128 a[132:135],v1 offset:256
v_mov_b32 v54,0
v_mfma_f32_16x16x32_bf16 v[236:239],a[152:155],v[120:123],v[236:239]
v_mov_b32 v55,0
v_mov_b32 v56,0
v_mfma_f32_16x16x32_bf16 v[236:239],a[148:151],v[116:119],v[236:239]
buffer_load_dwordx4 v4,s[20:23],s47 offen nt lds
v_mfma_f32_16x16x32_bf16 v[236:239],a[144:147],v[112:115],v[236:239]
ds_read_b128 a[136:139],v1 offset:512
v_mov_b32 v57,0
v_mfma_f32_16x16x32_bf16 v[248:251],a[160:163],v[112:115],v[44:47]
v_exp_f32 v200,v200
v_mfma_f32_16x16x32_bf16 v[248:251],a[164:167],v[116:119],v[248:251]
ds_read_b128 a[140:143],v1 offset:768
v_mov_b32 v58,0
v_mfma_f32_16x16x32_bf16 v[248:251],a[168:171],v[120:123],v[248:251]
v_exp_f32 v201,v201
v_mfma_f32_16x16x32_bf16 v[248:251],a[172:175],v[124:127],v[248:251]
ds_read_b128 a[156:159],v1 offset:4864
v_cvt_pk_bf16_f32 v200,v200,v201
v_mfma_f32_16x16x32_bf16 v[252:255],a[188:191],v[124:127],v[44:47]
v_exp_f32 v202,v202
v_mfma_f32_16x16x32_bf16 v[252:255],a[184:187],v[120:123],v[252:255]
ds_read_b128 a[152:155],v1 offset:4608
v_mov_b32 v59,0
v_mfma_f32_16x16x32_bf16 v[252:255],a[180:183],v[116:119],v[252:255]
v_exp_f32 v203,v203
v_mfma_f32_16x16x32_bf16 v[252:255],a[176:179],v[112:115],v[252:255]
ds_read_b128 a[148:151],v1 offset:4352
v_cvt_pk_bf16_f32 v201,v202,v203
v_mfma_f32_16x16x32_bf16 a[32:35],a[192:195],v[168:171],0
v_exp_f32 v204,v204
v_mfma_f32_16x16x32_bf16 a[32:35],a[196:199],v[172:175],a[32:35]
ds_read_b128 a[144:147],v1 offset:4096
v_mov_b32 v60,0
v_mfma_f32_16x16x32_bf16 a[36:39],a[204:207],v[172:175],0
v_exp_f32 v205,v205
v_mfma_f32_16x16x32_bf16 a[36:39],a[200:203],v[168:171],a[36:39]
ds_read_b128 a[160:163],v1 offset:8192
v_cvt_pk_bf16_f32 v202,v204,v205
v_mfma_f32_16x16x32_bf16 a[40:43],a[208:211],v[168:171],0
buffer_load_dwordx4 v4,s[20:23],s47 offen offset:1024 nt lds
v_mfma_f32_16x16x32_bf16 a[40:43],a[212:215],v[172:175],a[40:43]
ds_read_b128 a[164:167],v1 offset:8448
v_mov_b32 v61,0
v_mfma_f32_16x16x32_bf16 a[44:47],a[220:223],v[172:175],0
v_exp_f32 v206,v206
v_mfma_f32_16x16x32_bf16 a[44:47],a[216:219],v[168:171],a[44:47]
ds_read_b128 a[168:171],v1 offset:8704
v_mov_b32 v62,0
v_mfma_f32_16x16x32_bf16 a[48:51],a[224:227],v[168:171],0
v_exp_f32 v207,v207
)A"
R"A(v_mfma_f32_16x16x32_bf16 a[48:51],a[228:231],v[172:175],a[48:51]
ds_read_b128 a[172:175],v1 offset:8960
v_cvt_pk_bf16_f32 v203,v206,v207
v_mfma_f32_16x16x32_bf16 a[52:55],a[236:239],v[172:175],0
v_exp_f32 v216,v216
v_mfma_f32_16x16x32_bf16 a[52:55],a[232:235],v[168:171],a[52:55]
ds_read_b128 a[188:191],v1 offset:13056
v_mov_b32 v63,0
v_mfma_f32_16x16x32_bf16 a[56:59],a[240:243],v[168:171],0
v_exp_f32 v217,v217
v_mfma_f32_16x16x32_bf16 a[56:59],a[244:247],v[172:175],a[56:59]
ds_read_b128 a[184:187],v1 offset:12800
v_cvt_pk_bf16_f32 v204,v216,v217
v_mfma_f32_16x16x32_bf16 a[60:63],a[252:255],v[172:175],0
v_exp_f32 v218,v218
v_mfma_f32_16x16x32_bf16 a[60:63],a[248:251],v[168:171],a[60:63]
ds_read_b128 a[180:183],v1 offset:12544
v_add_u32 v2,s48,v2
v_smfmac_f32_16x16x64_bf16 v[52:55],v[28:31],v[168:175],v27
s_waitcnt lgkmcnt(4)
v_add_u32 v3,s48,v3
v_mfma_f32_16x16x32_bf16 v[136:139],a[128:131],v[64:67],v[32:35]
s_mov_b32 s48,32768
ds_read_b128 a[176:179],v1 offset:12288
v_add_u32 v1,s48,v1
v_mfma_f32_16x16x32_bf16 v[136:139],a[132:135],v[68:71],v[136:139]
buffer_load_dwordx4 v5,s[20:23],s47 offen offset:2048 nt lds
v_mfma_f32_16x16x32_bf16 v[136:139],a[136:139],v[72:75],v[136:139]
v_exp_f32 v219,v219
v_mfma_f32_16x16x32_bf16 v[136:139],a[140:143],v[76:79],v[136:139]
v_exp_f32 v220,v220
v_mfma_f32_16x16x32_bf16 v[140:143],a[156:159],v[76:79],v[32:35]
v_exp_f32 v221,v221
v_mfma_f32_16x16x32_bf16 v[140:143],a[152:155],v[72:75],v[140:143]
v_exp_f32 v222,v222
v_mfma_f32_16x16x32_bf16 v[140:143],a[148:151],v[68:71],v[140:143]
v_exp_f32 v223,v223
v_mfma_f32_16x16x32_bf16 v[140:143],a[144:147],v[64:67],v[140:143]
v_cvt_pk_bf16_f32 v205,v218,v219
v_cvt_pk_bf16_f32 v206,v220,v221
v_mfma_f32_16x16x32_bf16 v[152:155],a[160:163],v[64:67],v[32:35]
v_cvt_pk_bf16_f32 v207,v222,v223
v_mfma_f32_16x16x32_bf16 v[152:155],a[164:167],v[68:71],v[152:155]
v_exp_f32 v232,v232
v_mfma_f32_16x16x32_bf16 v[152:155],a[168:171],v[72:75],v[152:155]
s_waitcnt lgkmcnt(0)
v_exp_f32 v233,v233
v_mfma_f32_16x16x32_bf16 v[152:155],a[172:175],v[76:79],v[152:155]
v_exp_f32 v234,v234
v_mfma_f32_16x16x32_bf16 v[156:159],a[188:191],v[76:79],v[32:35]
v_exp_f32 v235,v235
v_mfma_f32_16x16x32_bf16 v[156:159],a[184:187],v[72:75],v[156:159]
v_exp_f32 v236,v236
v_mfma_f32_16x16x32_bf16 v[156:159],a[180:183],v[68:71],v[156:159]
v_exp_f32 v237,v237
v_mfma_f32_16x16x32_bf16 v[156:159],a[176:179],v[64:67],v[156:159]
buffer_load_dwordx4 v5,s[20:23],s47 offen offset:3072 nt lds
v_mfma_f32_16x16x32_bf16 a[64:67],a[192:195],v[200:203],0
v_exp_f32 v238,v238
v_mfma_f32_16x16x32_bf16 a[64:67],a[196:199],v[204:207],a[64:67]
v_exp_f32 v239,v239
v_mfma_f32_16x16x32_bf16 a[68:71],a[204:207],v[204:207],0
v_exp_f32 v248,v248
v_mfma_f32_16x16x32_bf16 a[68:71],a[200:203],v[200:203],a[68:71]
v_exp_f32 v249,v249
v_mfma_f32_16x16x32_bf16 a[72:75],a[208:211],v[200:203],0
v_exp_f32 v250,v250
v_mfma_f32_16x16x32_bf16 a[72:75],a[212:215],v[204:207],a[72:75]
v_exp_f32 v251,v251
v_mfma_f32_16x16x32_bf16 a[76:79],a[220:223],v[204:207],0
v_exp_f32 v252,v252
v_mfma_f32_16x16x32_bf16 a[76:79],a[216:219],v[200:203],a[76:79]
v_exp_f32 v253,v253
v_mfma_f32_16x16x32_bf16 a[80:83],a[224:227],v[200:203],0
v_exp_f32 v254,v254
v_mfma_f32_16x16x32_bf16 a[80:83],a[228:231],v[204:207],a[80:83]
v_exp_f32 v255,v255
v_mfma_f32_16x16x32_bf16 a[84:87],a[236:239],v[204:207],0
s_addk_i32 m0,16384
v_exp_f32 v136,v136
v_mfma_f32_16x16x32_bf16 a[84:87],a[232:235],v[200:203],a[84:87]
v_exp_f32 v137,v137
v_mfma_f32_16x16x32_bf16 a[88:91],a[240:243],v[200:203],0
v_cvt_pk_bf16_f32 v232,v232,v233
v_cvt_pk_bf16_f32 v233,v234,v235
v_mfma_f32_16x16x32_bf16 a[88:91],a[244:247],v[204:207],a[88:91]
buffer_load_dwordx4 v4,s[24:27],s47 offen nt lds
v_mfma_f32_16x16x32_bf16 a[92:95],a[252:255],v[204:207],0
v_cvt_pk_bf16_f32 v234,v236,v237
v_cvt_pk_bf16_f32 v235,v238,v239
v_mfma_f32_16x16x32_bf16 a[92:95],a[248:251],v[200:203],a[92:95]
v_cvt_pk_bf16_f32 v236,v248,v249
v_cvt_pk_bf16_f32 v237,v250,v251
v_smfmac_f32_16x16x64_bf16 v[56:59],v[28:31],v[200:207],v27
v_cvt_pk_bf16_f32 v238,v252,v253
v_mfma_f32_16x16x32_bf16 v[168:171],a[128:131],v[80:83],v[36:39]
v_cvt_pk_bf16_f32 v239,v254,v255
v_cvt_pk_bf16_f32 v136,v136,v137
v_mfma_f32_16x16x32_bf16 v[168:171],a[132:135],v[84:87],v[168:171]
v_exp_f32 v138,v138
v_mfma_f32_16x16x32_bf16 v[168:171],a[136:139],v[88:91],v[168:171]
v_exp_f32 v139,v139
v_mfma_f32_16x16x32_bf16 v[168:171],a[140:143],v[92:95],v[168:171]
v_exp_f32 v140,v140
v_mfma_f32_16x16x32_bf16 v[172:175],a[156:159],v[92:95],v[36:39]
v_exp_f32 v141,v141
v_mfma_f32_16x16x32_bf16 v[172:175],a[152:155],v[88:91],v[172:175]
v_exp_f32 v142,v142
v_mfma_f32_16x16x32_bf16 v[172:175],a[148:151],v[84:87],v[172:175]
v_exp_f32 v143,v143
v_mfma_f32_16x16x32_bf16 v[172:175],a[144:147],v[80:83],v[172:175]
v_exp_f32 v152,v152
v_mfma_f32_16x16x32_bf16 v[184:187],a[160:163],v[80:83],v[36:39]
v_exp_f32 v153,v153
v_mfma_f32_16x16x32_bf16 v[184:187],a[164:167],v[84:87],v[184:187]
v_exp_f32 v154,v154
v_mfma_f32_16x16x32_bf16 v[184:187],a[168:171],v[88:91],v[184:187]
buffer_load_dwordx4 v5,s[24:27],s47 offen offset:1024 nt lds
v_mfma_f32_16x16x32_bf16 v[184:187],a[172:175],v[92:95],v[184:187]
v_exp_f32 v155,v155
v_mfma_f32_16x16x32_bf16 v[188:191],a[188:191],v[92:95],v[36:39]
v_exp_f32 v156,v156
v_mfma_f32_16x16x32_bf16 v[188:191],a[184:187],v[88:91],v[188:191]
v_exp_f32 v157,v157
v_mfma_f32_16x16x32_bf16 v[188:191],a[180:183],v[84:87],v[188:191]
v_exp_f32 v158,v158
v_mfma_f32_16x16x32_bf16 v[188:191],a[176:179],v[80:83],v[188:191]
v_exp_f32 v159,v159
v_mfma_f32_16x16x32_bf16 a[96:99],a[192:195],v[232:235],0
ds_read_b64_tr_b16 a[192:193],v2
ds_read_b64_tr_b16 a[194:195],v2 offset:4096
v_mfma_f32_16x16x32_bf16 a[96:99],a[196:199],v[236:239],a[96:99]
ds_read_b64_tr_b16 a[196:197],v2 offset:8192
v_cvt_pk_bf16_f32 v137,v138,v139
v_mfma_f32_16x16x32_bf16 a[100:103],a[204:207],v[236:239],0
ds_read_b64_tr_b16 a[198:199],v2 offset:12288
v_cvt_pk_bf16_f32 v138,v140,v141
v_mfma_f32_16x16x32_bf16 a[100:103],a[200:203],v[232:235],a[100:103]
ds_read_b64_tr_b16 a[204:205],v3 offset:8192
v_cvt_pk_bf16_f32 v139,v142,v143
v_mfma_f32_16x16x32_bf16 a[104:107],a[208:211],v[232:235],0
ds_read_b64_tr_b16 a[206:207],v3 offset:12288
ds_read_b64_tr_b16 a[200:201],v3
v_mfma_f32_16x16x32_bf16 a[104:107],a[212:215],v[236:239],a[104:107]
ds_read_b64_tr_b16 a[202:203],v3 offset:4096
v_cvt_pk_bf16_f32 v140,v152,v153
v_mfma_f32_16x16x32_bf16 a[108:111],a[220:223],v[236:239],0
ds_read_b64_tr_b16 a[208:209],v2 offset:256
v_cvt_pk_bf16_f32 v141,v154,v155
v_mfma_f32_16x16x32_bf16 a[108:111],a[216:219],v[232:235],a[108:111]
ds_read_b64_tr_b16 a[210:211],v2 offset:4352
v_cvt_pk_bf16_f32 v142,v156,v157
v_mfma_f32_16x16x32_bf16 a[112:115],a[224:227],v[232:235],0
buffer_load_dwordx4 v4,s[24:27],s47 offen offset:2048 nt lds
v_mfma_f32_16x16x32_bf16 a[112:115],a[228:231],v[236:239],a[112:115]
ds_read_b64_tr_b16 a[212:213],v2 offset:8448
ds_read_b64_tr_b16 a[214:215],v2 offset:12544
v_mfma_f32_16x16x32_bf16 a[116:119],a[236:239],v[236:239],0
ds_read_b64_tr_b16 a[220:221],v3 offset:8448
v_cvt_pk_bf16_f32 v143,v158,v159
v_mfma_f32_16x16x32_bf16 a[116:119],a[232:235],v[232:235],a[116:119]
v_exp_f32 v168,v168
v_mfma_f32_16x16x32_bf16 a[120:123],a[240:243],v[232:235],0
ds_read_b64_tr_b16 a[222:223],v3 offset:12544
ds_read_b64_tr_b16 a[216:217],v3 offset:256
v_mfma_f32_16x16x32_bf16 a[120:123],a[244:247],v[236:239],a[120:123]
v_exp_f32 v169,v169
v_mfma_f32_16x16x32_bf16 a[124:127],a[252:255],v[236:239],0
ds_read_b64_tr_b16 a[218:219],v3 offset:4352
ds_read_b64_tr_b16 a[224:225],v2 offset:512
v_mfma_f32_16x16x32_bf16 a[124:127],a[248:251],v[232:235],a[124:127]
ds_read_b64_tr_b16 a[226:227],v2 offset:4608
v_cvt_pk_bf16_f32 v168,v168,v169
v_smfmac_f32_16x16x64_bf16 v[60:63],v[28:31],v[232:239],v27
ds_read_b64_tr_b16 a[228:229],v2 offset:8704
v_mfma_f32_16x16x32_bf16 v[200:203],a[128:131],v[96:99],v[40:43]
ds_read_b64_tr_b16 a[230:231],v2 offset:12800
v_mfma_f32_16x16x32_bf16 v[200:203],a[132:135],v[100:103],v[200:203]
ds_read_b64_tr_b16 a[236:237],v3 offset:8704
ds_read_b64_tr_b16 a[238:239],v3 offset:12800
v_mfma_f32_16x16x32_bf16 v[200:203],a[136:139],v[104:107],v[200:203]
ds_read_b64_tr_b16 a[232:233],v3 offset:512
v_mfma_f32_16x16x32_bf16 v[200:203],a[140:143],v[108:111],v[200:203]
ds_read_b64_tr_b16 a[234:235],v3 offset:4608
v_mfma_f32_16x16x32_bf16 v[204:207],a[156:159],v[108:111],v[40:43]
ds_read_b64_tr_b16 a[240:241],v2 offset:768
v_mfma_f32_16x16x32_bf16 v[204:207],a[152:155],v[104:107],v[204:207]
buffer_load_dwordx4 v5,s[24:27],s47 offen offset:3072 nt lds
v_mfma_f32_16x16x32_bf16 v[204:207],a[148:151],v[100:103],v[204:207]
s_or_b32 s47,s47,0x18000
ds_read_b64_tr_b16 a[242:243],v2 offset:4864
ds_read_b64_tr_b16 a[244:245],v2 offset:8960
v_mfma_f32_16x16x32_bf16 v[204:207],a[144:147],v[96:99],v[204:207]
s_addk_i32 s47,4096
s_and_b32 s47,s47,0x27000
ds_read_b64_tr_b16 a[246:247],v2 offset:13056
v_mfma_f32_16x16x32_bf16 v[216:219],a[160:163],v[96:99],v[40:43]
s_or_b32 s47,s47,s46
ds_read_b64_tr_b16 a[252:253],v3 offset:8960
v_mfma_f32_16x16x32_bf16 v[216:219],a[164:167],v[100:103],v[216:219]
ds_read_b64_tr_b16 a[254:255],v3 offset:13056
v_mfma_f32_16x16x32_bf16 v[216:219],a[168:171],v[104:107],v[216:219]
ds_read_b64_tr_b16 a[248:249],v3 offset:768
ds_read_b64_tr_b16 a[250:251],v3 offset:4864
v_mfma_f32_16x16x32_bf16 v[216:219],a[172:175],v[108:111],v[216:219]
v_exp_f32 v170,v170
v_mfma_f32_16x16x32_bf16 v[220:223],a[188:191],v[108:111],v[40:43]
v_exp_f32 v171,v171
v_mfma_f32_16x16x32_bf16 v[220:223],a[184:187],v[104:107],v[220:223]
v_exp_f32 v172,v172
v_mfma_f32_16x16x32_bf16 v[220:223],a[180:183],v[100:103],v[220:223]
v_exp_f32 v173,v173
v_mfma_f32_16x16x32_bf16 v[220:223],a[176:179],v[96:99],v[220:223]
s_waitcnt lgkmcnt(4)
v_exp_f32 v174,v174
v_mfma_f32_16x16x32_bf16 a[0:3],a[192:195],v[136:139],a[0:3]
v_exp_f32 v175,v175
v_mfma_f32_16x16x32_bf16 a[0:3],a[196:199],v[140:143],a[0:3]
v_cvt_pk_bf16_f32 v169,v170,v171
v_cvt_pk_bf16_f32 v170,v172,v173
v_mfma_f32_16x16x32_bf16 a[4:7],a[204:207],v[140:143],a[4:7]
v_exp_f32 v184,v184
v_mfma_f32_16x16x32_bf16 a[4:7],a[200:203],v[136:139],a[4:7]
s_memrealtime s[76:77]
v_exp_f32 v185,v185
v_mfma_f32_16x16x32_bf16 a[8:11],a[208:211],v[136:139],a[8:11]
v_exp_f32 v186,v186
v_mfma_f32_16x16x32_bf16 a[8:11],a[212:215],v[140:143],a[8:11]
v_exp_f32 v187,v187
v_mfma_f32_16x16x32_bf16 a[12:15],a[220:223],v[140:143],a[12:15]
v_exp_f32 v188,v188
v_mfma_f32_16x16x32_bf16 a[12:15],a[216:219],v[136:139],a[12:15]
v_exp_f32 v189,v189
v_mfma_f32_16x16x32_bf16 a[16:19],a[224:227],v[136:139],a[16:19]
v_exp_f32 v190,v190
v_mfma_f32_16x16x32_bf16 a[16:19],a[228:231],v[140:143],a[16:19]
v_exp_f32 v191,v191
v_mfma_f32_16x16x32_bf16 a[20:23],a[236:239],v[140:143],a[20:23]
s_waitcnt lgkmcnt(0)
v_cvt_pk_bf16_f32 v171,v174,v175
v_cvt_pk_bf16_f32 v172,v184,v185
v_mfma_f32_16x16x32_bf16 a[20:23],a[232:235],v[136:139],a[20:23]
s_sub_u32 s79,s78,s76
v_cvt_pk_bf16_f32 v173,v186,v187
v_cvt_pk_bf16_f32 v174,v188,v189
v_mfma_f32_16x16x32_bf16 a[24:27],a[240:243],v[136:139],a[24:27]
s_add_u32 s78,s76,s60
v_cvt_pk_bf16_f32 v175,v190,v191
v_mfma_f32_16x16x32_bf16 a[24:27],a[244:247],v[140:143],a[24:27]
s_cmp_gt_i32 s79,s95
s_cbranch_scc1 L_ps2_%=
L_pr2_%=:
v_mfma_f32_16x16x32_bf16 a[28:31],a[252:255],v[140:143],a[28:31]
v_mfma_f32_16x16x32_bf16 a[28:31],a[248:251],v[136:139],a[28:31]
v_smfmac_f32_16x16x64_bf16 v[48:51],v[28:31],v[136:143],v27
s_mov_b32 s53,13
s_cmp_eq_u32 s40,3
s_cbranch_scc0 L_att0_%=
s_cmp_gt_i32 s18,0
s_cbranch_scc1 L_att0v0_%=
s_cmp_gt_i32 s19,0
s_cbranch_scc1 L_att0v1_%=
s_cmp_gt_i32 s100,0
s_cbranch_scc1 L_att0v2_%=
.p2align 5
L_att0v3_%=:
s_waitcnt vmcnt(8) lgkmcnt(0)
v_exp_f32 v200,v200
s_barrier
ds_read_b128 a[128:131],v1
v_add_u32 v2,s48,v2
s_mov_b32 s90,L_endseg_%=-L_pc_%=+128
v_exp_f32 v201,v201
s_add_u32 m0,s49,s51
ds_read_b128 a[132:135],v1 offset:256
v_cvt_pk_bf16_f32 v200,v200,v201
s_lshr_b32 s45,s45,1
v_exp_f32 v202,v202
ds_read_b128 a[136:139],v1 offset:512
v_add_u32 v3,s48,v3
buffer_load_dwordx4 v4,s[20:23],s47 offen nt lds
)A"
R"A(v_exp_f32 v203,v203
s_add_u32 s62,s50,32768
ds_read_b128 a[140:143],v1 offset:768
v_cvt_pk_bf16_f32 v201,v202,v203
s_and_b32 s62,s62,131071
v_exp_f32 v204,v204
s_addk_i32 s54,16384
v_exp_f32 v205,v205
s_sub_u32 s48,s62,s50
ds_read_b128 a[156:159],v1 offset:4864
v_cvt_pk_bf16_f32 v202,v204,v205
s_sub_u32 s53,s53,1
v_exp_f32 v206,v206
s_and_b32 s46,s54,0xfffc0000
v_exp_f32 v207,v207
s_mov_b32 s50,s62
ds_read_b128 a[152:155],v1 offset:4608
v_cvt_pk_bf16_f32 v203,v206,v207
s_xor_b32 s49,s50,65536
v_exp_f32 v216,v216
v_mfma_f32_16x16x32_bf16 a[32:35],a[192:195],v[168:171],a[32:35]
ds_read_b128 a[148:151],v1 offset:4352
v_mfma_f32_16x16x32_bf16 a[32:35],a[196:199],v[172:175],a[32:35]
v_exp_f32 v217,v217
v_mfma_f32_16x16x32_bf16 a[36:39],a[204:207],v[172:175],a[36:39]
ds_read_b128 a[144:147],v1 offset:4096
v_cvt_pk_bf16_f32 v204,v216,v217
v_mfma_f32_16x16x32_bf16 a[36:39],a[200:203],v[168:171],a[36:39]
v_exp_f32 v218,v218
v_mfma_f32_16x16x32_bf16 a[40:43],a[208:211],v[168:171],a[40:43]
buffer_load_dwordx4 v4,s[20:23],s47 offen offset:1024 nt lds
v_mfma_f32_16x16x32_bf16 a[40:43],a[212:215],v[172:175],a[40:43]
ds_read_b128 a[160:163],v1 offset:8192
v_mfma_f32_16x16x32_bf16 a[44:47],a[220:223],v[172:175],a[44:47]
v_exp_f32 v219,v219
v_mfma_f32_16x16x32_bf16 a[44:47],a[216:219],v[168:171],a[44:47]
ds_read_b128 a[164:167],v1 offset:8448
v_cvt_pk_bf16_f32 v205,v218,v219
v_mfma_f32_16x16x32_bf16 a[48:51],a[224:227],v[168:171],a[48:51]
v_exp_f32 v220,v220
v_mfma_f32_16x16x32_bf16 a[48:51],a[228:231],v[172:175],a[48:51]
ds_read_b128 a[168:171],v1 offset:8704
v_mfma_f32_16x16x32_bf16 a[52:55],a[236:239],v[172:175],a[52:55]
v_exp_f32 v221,v221
v_mfma_f32_16x16x32_bf16 a[52:55],a[232:235],v[168:171],a[52:55]
ds_read_b128 a[172:175],v1 offset:8960
v_cvt_pk_bf16_f32 v206,v220,v221
v_mfma_f32_16x16x32_bf16 a[56:59],a[240:243],v[168:171],a[56:59]
v_exp_f32 v222,v222
v_mfma_f32_16x16x32_bf16 a[56:59],a[244:247],v[172:175],a[56:59]
ds_read_b128 a[188:191],v1 offset:13056
v_mfma_f32_16x16x32_bf16 a[60:63],a[252:255],v[172:175],a[60:63]
v_exp_f32 v223,v223
v_mfma_f32_16x16x32_bf16 a[60:63],a[248:251],v[168:171],a[60:63]
ds_read_b128 a[184:187],v1 offset:12800
v_cvt_pk_bf16_f32 v207,v222,v223
v_smfmac_f32_16x16x64_bf16 v[52:55],v[28:31],v[168:175],v27
s_waitcnt lgkmcnt(4)
v_mfma_f32_16x16x32_bf16 v[136:139],a[128:131],v[64:67],v[32:35]
ds_read_b128 a[180:183],v1 offset:12544
v_mfma_f32_16x16x32_bf16 v[136:139],a[132:135],v[68:71],v[136:139]
buffer_load_dwordx4 v5,s[20:23],s47 offen offset:2048 nt lds
v_mfma_f32_16x16x32_bf16 v[136:139],a[136:139],v[72:75],v[136:139]
ds_read_b128 a[176:179],v1 offset:12288
v_add_u32 v1,s48,v1
v_mfma_f32_16x16x32_bf16 v[136:139],a[140:143],v[76:79],v[136:139]
v_mfma_f32_16x16x32_bf16 v[140:143],a[156:159],v[76:79],v[32:35]
v_mfma_f32_16x16x32_bf16 v[140:143],a[152:155],v[72:75],v[140:143]
v_mfma_f32_16x16x32_bf16 v[140:143],a[148:151],v[68:71],v[140:143]
v_mfma_f32_16x16x32_bf16 v[140:143],a[144:147],v[64:67],v[140:143]
v_mfma_f32_16x16x32_bf16 v[152:155],a[160:163],v[64:67],v[32:35]
v_mfma_f32_16x16x32_bf16 v[152:155],a[164:167],v[68:71],v[152:155]
s_waitcnt lgkmcnt(1)
v_mfma_f32_16x16x32_bf16 v[152:155],a[168:171],v[72:75],v[152:155]
v_mfma_f32_16x16x32_bf16 v[152:155],a[172:175],v[76:79],v[152:155]
v_mfma_f32_16x16x32_bf16 v[156:159],a[188:191],v[76:79],v[32:35]
v_mfma_f32_16x16x32_bf16 v[156:159],a[184:187],v[72:75],v[156:159]
v_mfma_f32_16x16x32_bf16 v[156:159],a[180:183],v[68:71],v[156:159]
s_waitcnt lgkmcnt(0)
v_mfma_f32_16x16x32_bf16 v[156:159],a[176:179],v[64:67],v[156:159]
buffer_load_dwordx4 v5,s[20:23],s47 offen offset:3072 nt lds
v_mfma_f32_16x16x32_bf16 a[64:67],a[192:195],v[200:203],a[64:67]
v_mfma_f32_16x16x32_bf16 a[64:67],a[196:199],v[204:207],a[64:67]
v_mfma_f32_16x16x32_bf16 a[68:71],a[204:207],v[204:207],a[68:71]
v_mfma_f32_16x16x32_bf16 a[68:71],a[200:203],v[200:203],a[68:71]
v_mfma_f32_16x16x32_bf16 a[72:75],a[208:211],v[200:203],a[72:75]
v_exp_f32 v136,v136
v_mfma_f32_16x16x32_bf16 a[72:75],a[212:215],v[204:207],a[72:75]
v_exp_f32 v137,v137
v_mfma_f32_16x16x32_bf16 a[76:79],a[220:223],v[204:207],a[76:79]
v_exp_f32 v138,v138
v_mfma_f32_16x16x32_bf16 a[76:79],a[216:219],v[200:203],a[76:79]
v_exp_f32 v139,v139
v_mfma_f32_16x16x32_bf16 a[80:83],a[224:227],v[200:203],a[80:83]
v_exp_f32 v140,v140
v_mfma_f32_16x16x32_bf16 a[80:83],a[228:231],v[204:207],a[80:83]
v_exp_f32 v141,v141
v_mfma_f32_16x16x32_bf16 a[84:87],a[236:239],v[204:207],a[84:87]
s_addk_i32 m0,16384
v_exp_f32 v142,v142
v_mfma_f32_16x16x32_bf16 a[84:87],a[232:235],v[200:203],a[84:87]
v_exp_f32 v143,v143
v_mfma_f32_16x16x32_bf16 a[88:91],a[240:243],v[200:203],a[88:91]
v_exp_f32 v152,v152
v_mfma_f32_16x16x32_bf16 a[88:91],a[244:247],v[204:207],a[88:91]
buffer_load_dwordx4 v4,s[24:27],s47 offen nt lds
v_mfma_f32_16x16x32_bf16 a[92:95],a[252:255],v[204:207],a[92:95]
v_mfma_f32_16x16x32_bf16 a[92:95],a[248:251],v[200:203],a[92:95]
v_smfmac_f32_16x16x64_bf16 v[56:59],v[28:31],v[200:207],v27
v_mfma_f32_16x16x32_bf16 v[168:171],a[128:131],v[80:83],v[36:39]
v_mfma_f32_16x16x32_bf16 v[168:171],a[132:135],v[84:87],v[168:171]
v_mfma_f32_16x16x32_bf16 v[168:171],a[136:139],v[88:91],v[168:171]
v_exp_f32 v153,v153
v_mfma_f32_16x16x32_bf16 v[168:171],a[140:143],v[92:95],v[168:171]
v_exp_f32 v154,v154
v_mfma_f32_16x16x32_bf16 v[172:175],a[156:159],v[92:95],v[36:39]
v_exp_f32 v155,v155
v_mfma_f32_16x16x32_bf16 v[172:175],a[152:155],v[88:91],v[172:175]
v_exp_f32 v156,v156
v_mfma_f32_16x16x32_bf16 v[172:175],a[148:151],v[84:87],v[172:175]
v_exp_f32 v157,v157
v_mfma_f32_16x16x32_bf16 v[172:175],a[144:147],v[80:83],v[172:175]
v_exp_f32 v158,v158
v_mfma_f32_16x16x32_bf16 v[184:187],a[160:163],v[80:83],v[36:39]
v_exp_f32 v159,v159
v_mfma_f32_16x16x32_bf16 v[184:187],a[164:167],v[84:87],v[184:187]
v_exp_f32 v168,v168
v_mfma_f32_16x16x32_bf16 v[184:187],a[168:171],v[88:91],v[184:187]
buffer_load_dwordx4 v5,s[24:27],s47 offen offset:1024 nt lds
v_mfma_f32_16x16x32_bf16 v[184:187],a[172:175],v[92:95],v[184:187]
v_exp_f32 v169,v169
v_mfma_f32_16x16x32_bf16 v[188:191],a[188:191],v[92:95],v[36:39]
v_exp_f32 v170,v170
v_mfma_f32_16x16x32_bf16 v[188:191],a[184:187],v[88:91],v[188:191]
v_exp_f32 v171,v171
v_mfma_f32_16x16x32_bf16 v[188:191],a[180:183],v[84:87],v[188:191]
v_exp_f32 v172,v172
v_mfma_f32_16x16x32_bf16 v[188:191],a[176:179],v[80:83],v[188:191]
v_exp_f32 v173,v173
ds_read_b64_tr_b16 a[192:193],v2
v_cvt_pk_bf16_f32 v168,v168,v169
ds_read_b64_tr_b16 a[194:195],v2 offset:4096
v_cvt_pk_bf16_f32 v169,v170,v171
ds_read_b64_tr_b16 a[196:197],v2 offset:8192
v_cvt_pk_bf16_f32 v170,v172,v173
v_exp_f32 v174,v174
v_exp_f32 v175,v175
ds_read_b64_tr_b16 a[198:199],v2 offset:12288
v_cvt_pk_bf16_f32 v171,v174,v175
v_exp_f32 v184,v184
v_exp_f32 v185,v185
buffer_load_dwordx4 v4,s[24:27],s47 offen offset:2048 nt lds
ds_read_b64_tr_b16 a[204:205],v3 offset:8192
v_cvt_pk_bf16_f32 v172,v184,v185
ds_read_b64_tr_b16 a[206:207],v3 offset:12288
v_cvt_pk_bf16_f32 v136,v136,v137
v_exp_f32 v186,v186
ds_read_b64_tr_b16 a[200:201],v3
ds_read_b64_tr_b16 a[202:203],v3 offset:4096
v_cvt_pk_bf16_f32 v137,v138,v139
ds_read_b64_tr_b16 a[208:209],v2 offset:256
ds_read_b64_tr_b16 a[210:211],v2 offset:4352
ds_read_b64_tr_b16 a[212:213],v2 offset:8448
v_mfma_f32_16x16x32_bf16 v[200:203],a[128:131],v[96:99],v[40:43]
ds_read_b64_tr_b16 a[214:215],v2 offset:12544
v_cvt_pk_bf16_f32 v138,v140,v141
v_mfma_f32_16x16x32_bf16 v[200:203],a[132:135],v[100:103],v[200:203]
ds_read_b64_tr_b16 a[220:221],v3 offset:8448
v_mfma_f32_16x16x32_bf16 v[200:203],a[136:139],v[104:107],v[200:203]
ds_read_b64_tr_b16 a[222:223],v3 offset:12544
v_cvt_pk_bf16_f32 v139,v142,v143
v_mfma_f32_16x16x32_bf16 v[200:203],a[140:143],v[108:111],v[200:203]
ds_read_b64_tr_b16 a[216:217],v3 offset:256
v_mfma_f32_16x16x32_bf16 v[204:207],a[156:159],v[108:111],v[40:43]
ds_read_b64_tr_b16 a[218:219],v3 offset:4352
v_mfma_f32_16x16x32_bf16 v[204:207],a[152:155],v[104:107],v[204:207]
buffer_load_dwordx4 v5,s[24:27],s47 offen offset:3072 nt lds
v_mfma_f32_16x16x32_bf16 v[204:207],a[148:151],v[100:103],v[204:207]
s_or_b32 s47,s47,0x18000
ds_read_b64_tr_b16 a[224:225],v2 offset:512
v_cvt_pk_bf16_f32 v140,v152,v153
v_mfma_f32_16x16x32_bf16 v[204:207],a[144:147],v[96:99],v[204:207]
s_addk_i32 s47,4096
s_and_b32 s47,s47,0x27000
ds_read_b64_tr_b16 a[226:227],v2 offset:4608
v_mfma_f32_16x16x32_bf16 v[216:219],a[160:163],v[96:99],v[40:43]
s_or_b32 s47,s47,s46
ds_read_b64_tr_b16 a[228:229],v2 offset:8704
v_cvt_pk_bf16_f32 v141,v154,v155
v_mfma_f32_16x16x32_bf16 v[216:219],a[164:167],v[100:103],v[216:219]
ds_read_b64_tr_b16 a[230:231],v2 offset:12800
v_mfma_f32_16x16x32_bf16 v[216:219],a[168:171],v[104:107],v[216:219]
ds_read_b64_tr_b16 a[236:237],v3 offset:8704
v_mfma_f32_16x16x32_bf16 v[216:219],a[172:175],v[108:111],v[216:219]
ds_read_b64_tr_b16 a[238:239],v3 offset:12800
v_cvt_pk_bf16_f32 v142,v156,v157
v_mfma_f32_16x16x32_bf16 v[220:223],a[188:191],v[108:111],v[40:43]
ds_read_b64_tr_b16 a[232:233],v3 offset:512
v_mfma_f32_16x16x32_bf16 v[220:223],a[184:187],v[104:107],v[220:223]
ds_read_b64_tr_b16 a[234:235],v3 offset:4608
v_mfma_f32_16x16x32_bf16 v[220:223],a[180:183],v[100:103],v[220:223]
ds_read_b64_tr_b16 a[240:241],v2 offset:768
v_cvt_pk_bf16_f32 v143,v158,v159
v_mfma_f32_16x16x32_bf16 v[220:223],a[176:179],v[96:99],v[220:223]
s_waitcnt lgkmcnt(7)
buffer_load_dword v26,v24,s[88:91],s93 offen
v_mfma_f32_16x16x32_bf16 a[0:3],a[192:195],v[136:139],a[0:3]
ds_read_b64_tr_b16 a[242:243],v2 offset:4864
v_mfma_f32_16x16x32_bf16 a[0:3],a[196:199],v[140:143],a[0:3]
ds_read_b64_tr_b16 a[244:245],v2 offset:8960
v_mfma_f32_16x16x32_bf16 a[4:7],a[204:207],v[140:143],a[4:7]
ds_read_b64_tr_b16 a[246:247],v2 offset:13056
v_mfma_f32_16x16x32_bf16 a[4:7],a[200:203],v[136:139],a[4:7]
ds_read_b64_tr_b16 a[252:253],v3 offset:8960
v_mfma_f32_16x16x32_bf16 a[8:11],a[208:211],v[136:139],a[8:11]
ds_read_b64_tr_b16 a[254:255],v3 offset:13056
v_mfma_f32_16x16x32_bf16 a[8:11],a[212:215],v[140:143],a[8:11]
ds_read_b64_tr_b16 a[248:249],v3 offset:768
v_mfma_f32_16x16x32_bf16 a[12:15],a[220:223],v[140:143],a[12:15]
ds_read_b64_tr_b16 a[250:251],v3 offset:4864
v_mfma_f32_16x16x32_bf16 a[12:15],a[216:219],v[136:139],a[12:15]
s_bitcmp1_b32 s45,0
s_cbranch_scc0 L_t31v30_%=
s_mov_b32 m0,s80
s_addk_i32 s80,4096
buffer_load_dwordx4 v25,s[20:23],s83 offen nt lds
s_addk_i32 s83,1024
L_t31v30_%=:
v_mfma_f32_16x16x32_bf16 a[16:19],a[224:227],v[136:139],a[16:19]
s_waitcnt lgkmcnt(6)
v_exp_f32 v187,v187
v_mfma_f32_16x16x32_bf16 a[16:19],a[228:231],v[140:143],a[16:19]
v_exp_f32 v188,v188
v_mfma_f32_16x16x32_bf16 a[20:23],a[236:239],v[140:143],a[20:23]
v_exp_f32 v189,v189
v_mfma_f32_16x16x32_bf16 a[20:23],a[232:235],v[136:139],a[20:23]
v_exp_f32 v190,v190
v_mfma_f32_16x16x32_bf16 a[24:27],a[240:243],v[136:139],a[24:27]
s_waitcnt lgkmcnt(2)
v_exp_f32 v191,v191
v_mfma_f32_16x16x32_bf16 a[24:27],a[244:247],v[140:143],a[24:27]
v_cvt_pk_bf16_f32 v173,v186,v187
v_mfma_f32_16x16x32_bf16 a[28:31],a[252:255],v[140:143],a[28:31]
s_waitcnt lgkmcnt(0)
v_cvt_pk_bf16_f32 v174,v188,v189
v_cvt_pk_bf16_f32 v175,v190,v191
v_mfma_f32_16x16x32_bf16 a[28:31],a[248:251],v[136:139],a[28:31]
s_cmp_lg_u32 s53,0
v_smfmac_f32_16x16x64_bf16 v[48:51],v[28:31],v[136:143],v27
s_cbranch_scc1 L_att0v3_%=
s_waitcnt lgkmcnt(0)
s_nop 7
s_branch L_att0x_%=
.p2align 5
L_att0v0_%=:
v_mfma_f32_16x16x32_bf16 v[232:235],a[128:131],v[112:115],v[44:47]
s_waitcnt vmcnt(8) lgkmcnt(0)
v_exp_f32 v200,v200
v_mfma_f32_16x16x32_bf16 v[232:235],a[132:135],v[116:119],v[232:235]
s_barrier
ds_read_b128 a[128:131],v1
v_add_u32 v2,s48,v2
v_mfma_f32_16x16x32_bf16 v[232:235],a[136:139],v[120:123],v[232:235]
s_mov_b32 s90,L_endseg_%=-L_pc_%=+128
v_exp_f32 v201,v201
v_mfma_f32_16x16x32_bf16 v[232:235],a[140:143],v[124:127],v[232:235]
s_add_u32 m0,s49,s51
ds_read_b128 a[132:135],v1 offset:256
v_cvt_pk_bf16_f32 v200,v200,v201
v_mfma_f32_16x16x32_bf16 v[236:239],a[156:159],v[124:127],v[44:47]
s_lshr_b32 s45,s45,1
v_exp_f32 v202,v202
v_mfma_f32_16x16x32_bf16 v[236:239],a[152:155],v[120:123],v[236:239]
ds_read_b128 a[136:139],v1 offset:512
)A"
R"A(v_add_u32 v3,s48,v3
v_mfma_f32_16x16x32_bf16 v[236:239],a[148:151],v[116:119],v[236:239]
buffer_load_dwordx4 v4,s[20:23],s47 offen nt lds
v_mfma_f32_16x16x32_bf16 v[236:239],a[144:147],v[112:115],v[236:239]
v_exp_f32 v203,v203
v_mfma_f32_16x16x32_bf16 v[248:251],a[160:163],v[112:115],v[44:47]
s_add_u32 s62,s50,32768
ds_read_b128 a[140:143],v1 offset:768
v_cvt_pk_bf16_f32 v201,v202,v203
v_mfma_f32_16x16x32_bf16 v[248:251],a[164:167],v[116:119],v[248:251]
s_and_b32 s62,s62,131071
v_exp_f32 v204,v204
v_mfma_f32_16x16x32_bf16 v[248:251],a[168:171],v[120:123],v[248:251]
s_addk_i32 s54,16384
v_exp_f32 v205,v205
v_mfma_f32_16x16x32_bf16 v[248:251],a[172:175],v[124:127],v[248:251]
s_sub_u32 s48,s62,s50
ds_read_b128 a[156:159],v1 offset:4864
v_cvt_pk_bf16_f32 v202,v204,v205
v_mfma_f32_16x16x32_bf16 v[252:255],a[188:191],v[124:127],v[44:47]
s_sub_u32 s53,s53,1
v_exp_f32 v206,v206
v_mfma_f32_16x16x32_bf16 v[252:255],a[184:187],v[120:123],v[252:255]
s_and_b32 s46,s54,0xfffc0000
v_exp_f32 v207,v207
v_mfma_f32_16x16x32_bf16 v[252:255],a[180:183],v[116:119],v[252:255]
s_mov_b32 s50,s62
ds_read_b128 a[152:155],v1 offset:4608
v_cvt_pk_bf16_f32 v203,v206,v207
v_mfma_f32_16x16x32_bf16 v[252:255],a[176:179],v[112:115],v[252:255]
s_xor_b32 s49,s50,65536
v_exp_f32 v216,v216
v_mfma_f32_16x16x32_bf16 a[32:35],a[192:195],v[168:171],a[32:35]
ds_read_b128 a[148:151],v1 offset:4352
v_mfma_f32_16x16x32_bf16 a[32:35],a[196:199],v[172:175],a[32:35]
v_exp_f32 v217,v217
v_mfma_f32_16x16x32_bf16 a[36:39],a[204:207],v[172:175],a[36:39]
ds_read_b128 a[144:147],v1 offset:4096
v_cvt_pk_bf16_f32 v204,v216,v217
v_mfma_f32_16x16x32_bf16 a[36:39],a[200:203],v[168:171],a[36:39]
v_exp_f32 v218,v218
v_mfma_f32_16x16x32_bf16 a[40:43],a[208:211],v[168:171],a[40:43]
buffer_load_dwordx4 v4,s[20:23],s47 offen offset:1024 nt lds
v_mfma_f32_16x16x32_bf16 a[40:43],a[212:215],v[172:175],a[40:43]
ds_read_b128 a[160:163],v1 offset:8192
v_mfma_f32_16x16x32_bf16 a[44:47],a[220:223],v[172:175],a[44:47]
v_exp_f32 v219,v219
v_mfma_f32_16x16x32_bf16 a[44:47],a[216:219],v[168:171],a[44:47]
ds_read_b128 a[164:167],v1 offset:8448
v_cvt_pk_bf16_f32 v205,v218,v219
v_mfma_f32_16x16x32_bf16 a[48:51],a[224:227],v[168:171],a[48:51]
v_exp_f32 v220,v220
v_mfma_f32_16x16x32_bf16 a[48:51],a[228:231],v[172:175],a[48:51]
ds_read_b128 a[168:171],v1 offset:8704
v_mfma_f32_16x16x32_bf16 a[52:55],a[236:239],v[172:175],a[52:55]
v_exp_f32 v221,v221
v_mfma_f32_16x16x32_bf16 a[52:55],a[232:235],v[168:171],a[52:55]
ds_read_b128 a[172:175],v1 offset:8960
v_cvt_pk_bf16_f32 v206,v220,v221
v_mfma_f32_16x16x32_bf16 a[56:59],a[240:243],v[168:171],a[56:59]
v_exp_f32 v222,v222
v_mfma_f32_16x16x32_bf16 a[56:59],a[244:247],v[172:175],a[56:59]
ds_read_b128 a[188:191],v1 offset:13056
v_mfma_f32_16x16x32_bf16 a[60:63],a[252:255],v[172:175],a[60:63]
v_exp_f32 v223,v223
v_mfma_f32_16x16x32_bf16 a[60:63],a[248:251],v[168:171],a[60:63]
ds_read_b128 a[184:187],v1 offset:12800
v_cvt_pk_bf16_f32 v207,v222,v223
v_smfmac_f32_16x16x64_bf16 v[52:55],v[28:31],v[168:175],v27
ds_read_b128 a[180:183],v1 offset:12544
buffer_load_dwordx4 v5,s[20:23],s47 offen offset:2048 nt lds
ds_read_b128 a[176:179],v1 offset:12288
v_add_u32 v1,s48,v1
v_exp_f32 v232,v232
v_exp_f32 v233,v233
v_exp_f32 v234,v234
v_exp_f32 v235,v235
v_exp_f32 v236,v236
v_exp_f32 v237,v237
v_exp_f32 v238,v238
v_exp_f32 v239,v239
v_exp_f32 v248,v248
v_exp_f32 v249,v249
v_exp_f32 v250,v250
v_exp_f32 v251,v251
buffer_load_dwordx4 v5,s[20:23],s47 offen offset:3072 nt lds
v_mfma_f32_16x16x32_bf16 a[64:67],a[192:195],v[200:203],a[64:67]
v_exp_f32 v252,v252
v_mfma_f32_16x16x32_bf16 a[64:67],a[196:199],v[204:207],a[64:67]
v_exp_f32 v253,v253
v_mfma_f32_16x16x32_bf16 a[68:71],a[204:207],v[204:207],a[68:71]
v_exp_f32 v254,v254
v_mfma_f32_16x16x32_bf16 a[68:71],a[200:203],v[200:203],a[68:71]
v_exp_f32 v255,v255
v_mfma_f32_16x16x32_bf16 a[72:75],a[208:211],v[200:203],a[72:75]
v_mfma_f32_16x16x32_bf16 a[72:75],a[212:215],v[204:207],a[72:75]
v_mfma_f32_16x16x32_bf16 a[76:79],a[220:223],v[204:207],a[76:79]
v_mfma_f32_16x16x32_bf16 a[76:79],a[216:219],v[200:203],a[76:79]
v_mfma_f32_16x16x32_bf16 a[80:83],a[224:227],v[200:203],a[80:83]
v_mfma_f32_16x16x32_bf16 a[80:83],a[228:231],v[204:207],a[80:83]
v_mfma_f32_16x16x32_bf16 a[84:87],a[236:239],v[204:207],a[84:87]
s_addk_i32 m0,16384
v_mfma_f32_16x16x32_bf16 a[84:87],a[232:235],v[200:203],a[84:87]
v_mfma_f32_16x16x32_bf16 a[88:91],a[240:243],v[200:203],a[88:91]
v_mfma_f32_16x16x32_bf16 a[88:91],a[244:247],v[204:207],a[88:91]
buffer_load_dwordx4 v4,s[24:27],s47 offen nt lds
v_mfma_f32_16x16x32_bf16 a[92:95],a[252:255],v[204:207],a[92:95]
v_cvt_pk_bf16_f32 v232,v232,v233
v_cvt_pk_bf16_f32 v233,v234,v235
v_mfma_f32_16x16x32_bf16 a[92:95],a[248:251],v[200:203],a[92:95]
v_cvt_pk_bf16_f32 v234,v236,v237
v_cvt_pk_bf16_f32 v235,v238,v239
v_smfmac_f32_16x16x64_bf16 v[56:59],v[28:31],v[200:207],v27
s_waitcnt lgkmcnt(0)
v_cvt_pk_bf16_f32 v236,v248,v249
v_mfma_f32_16x16x32_bf16 v[168:171],a[128:131],v[80:83],v[36:39]
v_cvt_pk_bf16_f32 v237,v250,v251
v_cvt_pk_bf16_f32 v238,v252,v253
v_mfma_f32_16x16x32_bf16 v[168:171],a[132:135],v[84:87],v[168:171]
v_cvt_pk_bf16_f32 v239,v254,v255
v_mfma_f32_16x16x32_bf16 v[168:171],a[136:139],v[88:91],v[168:171]
v_mfma_f32_16x16x32_bf16 v[168:171],a[140:143],v[92:95],v[168:171]
v_mfma_f32_16x16x32_bf16 v[172:175],a[156:159],v[92:95],v[36:39]
v_mfma_f32_16x16x32_bf16 v[172:175],a[152:155],v[88:91],v[172:175]
v_mfma_f32_16x16x32_bf16 v[172:175],a[148:151],v[84:87],v[172:175]
v_mfma_f32_16x16x32_bf16 v[172:175],a[144:147],v[80:83],v[172:175]
v_mfma_f32_16x16x32_bf16 v[184:187],a[160:163],v[80:83],v[36:39]
v_mfma_f32_16x16x32_bf16 v[184:187],a[164:167],v[84:87],v[184:187]
s_nop 1
v_exp_f32 v168,v168
v_mfma_f32_16x16x32_bf16 v[184:187],a[168:171],v[88:91],v[184:187]
buffer_load_dwordx4 v5,s[24:27],s47 offen offset:1024 nt lds
v_mfma_f32_16x16x32_bf16 v[184:187],a[172:175],v[92:95],v[184:187]
v_exp_f32 v169,v169
v_mfma_f32_16x16x32_bf16 v[188:191],a[188:191],v[92:95],v[36:39]
v_exp_f32 v170,v170
v_mfma_f32_16x16x32_bf16 v[188:191],a[184:187],v[88:91],v[188:191]
v_exp_f32 v171,v171
v_mfma_f32_16x16x32_bf16 v[188:191],a[180:183],v[84:87],v[188:191]
v_exp_f32 v172,v172
v_mfma_f32_16x16x32_bf16 v[188:191],a[176:179],v[80:83],v[188:191]
v_exp_f32 v173,v173
v_mfma_f32_16x16x32_bf16 a[96:99],a[192:195],v[232:235],a[96:99]
ds_read_b64_tr_b16 a[192:193],v2
v_cvt_pk_bf16_f32 v168,v168,v169
v_mfma_f32_16x16x32_bf16 a[96:99],a[196:199],v[236:239],a[96:99]
ds_read_b64_tr_b16 a[194:195],v2 offset:4096
v_cvt_pk_bf16_f32 v169,v170,v171
v_mfma_f32_16x16x32_bf16 a[100:103],a[204:207],v[236:239],a[100:103]
ds_read_b64_tr_b16 a[196:197],v2 offset:8192
v_cvt_pk_bf16_f32 v170,v172,v173
v_mfma_f32_16x16x32_bf16 a[100:103],a[200:203],v[232:235],a[100:103]
v_exp_f32 v174,v174
v_mfma_f32_16x16x32_bf16 a[104:107],a[208:211],v[232:235],a[104:107]
v_exp_f32 v175,v175
v_mfma_f32_16x16x32_bf16 a[104:107],a[212:215],v[236:239],a[104:107]
ds_read_b64_tr_b16 a[198:199],v2 offset:12288
v_cvt_pk_bf16_f32 v171,v174,v175
v_mfma_f32_16x16x32_bf16 a[108:111],a[220:223],v[236:239],a[108:111]
v_exp_f32 v184,v184
v_mfma_f32_16x16x32_bf16 a[108:111],a[216:219],v[232:235],a[108:111]
v_exp_f32 v185,v185
v_mfma_f32_16x16x32_bf16 a[112:115],a[224:227],v[232:235],a[112:115]
buffer_load_dwordx4 v4,s[24:27],s47 offen offset:2048 nt lds
v_mfma_f32_16x16x32_bf16 a[112:115],a[228:231],v[236:239],a[112:115]
ds_read_b64_tr_b16 a[204:205],v3 offset:8192
v_cvt_pk_bf16_f32 v172,v184,v185
v_mfma_f32_16x16x32_bf16 a[116:119],a[236:239],v[236:239],a[116:119]
ds_read_b64_tr_b16 a[206:207],v3 offset:12288
v_mfma_f32_16x16x32_bf16 a[116:119],a[232:235],v[232:235],a[116:119]
v_exp_f32 v186,v186
v_mfma_f32_16x16x32_bf16 a[120:123],a[240:243],v[232:235],a[120:123]
ds_read_b64_tr_b16 a[200:201],v3
v_mfma_f32_16x16x32_bf16 a[120:123],a[244:247],v[236:239],a[120:123]
ds_read_b64_tr_b16 a[202:203],v3 offset:4096
v_mfma_f32_16x16x32_bf16 a[124:127],a[252:255],v[236:239],a[124:127]
ds_read_b64_tr_b16 a[208:209],v2 offset:256
v_mfma_f32_16x16x32_bf16 a[124:127],a[248:251],v[232:235],a[124:127]
ds_read_b64_tr_b16 a[210:211],v2 offset:4352
v_smfmac_f32_16x16x64_bf16 v[60:63],v[28:31],v[232:239],v27
ds_read_b64_tr_b16 a[212:213],v2 offset:8448
v_mfma_f32_16x16x32_bf16 v[200:203],a[128:131],v[96:99],v[40:43]
ds_read_b64_tr_b16 a[214:215],v2 offset:12544
v_mfma_f32_16x16x32_bf16 v[200:203],a[132:135],v[100:103],v[200:203]
ds_read_b64_tr_b16 a[220:221],v3 offset:8448
v_mfma_f32_16x16x32_bf16 v[200:203],a[136:139],v[104:107],v[200:203]
ds_read_b64_tr_b16 a[222:223],v3 offset:12544
v_mfma_f32_16x16x32_bf16 v[200:203],a[140:143],v[108:111],v[200:203]
ds_read_b64_tr_b16 a[216:217],v3 offset:256
v_mfma_f32_16x16x32_bf16 v[204:207],a[156:159],v[108:111],v[40:43]
ds_read_b64_tr_b16 a[218:219],v3 offset:4352
v_mfma_f32_16x16x32_bf16 v[204:207],a[152:155],v[104:107],v[204:207]
buffer_load_dwordx4 v5,s[24:27],s47 offen offset:3072 nt lds
v_mfma_f32_16x16x32_bf16 v[204:207],a[148:151],v[100:103],v[204:207]
s_or_b32 s47,s47,0x18000
ds_read_b64_tr_b16 a[224:225],v2 offset:512
v_mfma_f32_16x16x32_bf16 v[204:207],a[144:147],v[96:99],v[204:207]
s_addk_i32 s47,4096
s_and_b32 s47,s47,0x27000
ds_read_b64_tr_b16 a[226:227],v2 offset:4608
v_mfma_f32_16x16x32_bf16 v[216:219],a[160:163],v[96:99],v[40:43]
s_or_b32 s47,s47,s46
ds_read_b64_tr_b16 a[228:229],v2 offset:8704
v_mfma_f32_16x16x32_bf16 v[216:219],a[164:167],v[100:103],v[216:219]
ds_read_b64_tr_b16 a[230:231],v2 offset:12800
v_mfma_f32_16x16x32_bf16 v[216:219],a[168:171],v[104:107],v[216:219]
ds_read_b64_tr_b16 a[236:237],v3 offset:8704
v_mfma_f32_16x16x32_bf16 v[216:219],a[172:175],v[108:111],v[216:219]
ds_read_b64_tr_b16 a[238:239],v3 offset:12800
v_mfma_f32_16x16x32_bf16 v[220:223],a[188:191],v[108:111],v[40:43]
ds_read_b64_tr_b16 a[232:233],v3 offset:512
v_mfma_f32_16x16x32_bf16 v[220:223],a[184:187],v[104:107],v[220:223]
ds_read_b64_tr_b16 a[234:235],v3 offset:4608
v_mfma_f32_16x16x32_bf16 v[220:223],a[180:183],v[100:103],v[220:223]
ds_read_b64_tr_b16 a[240:241],v2 offset:768
v_mfma_f32_16x16x32_bf16 v[220:223],a[176:179],v[96:99],v[220:223]
buffer_load_dword v26,v24,s[88:91],s93 offen
ds_read_b64_tr_b16 a[242:243],v2 offset:4864
ds_read_b64_tr_b16 a[244:245],v2 offset:8960
ds_read_b64_tr_b16 a[246:247],v2 offset:13056
ds_read_b64_tr_b16 a[252:253],v3 offset:8960
ds_read_b64_tr_b16 a[254:255],v3 offset:13056
ds_read_b64_tr_b16 a[248:249],v3 offset:768
ds_read_b64_tr_b16 a[250:251],v3 offset:4864
s_bitcmp1_b32 s45,0
s_cbranch_scc0 L_t31v00_%=
s_mov_b32 m0,s80
s_addk_i32 s80,4096
buffer_load_dwordx4 v25,s[20:23],s83 offen nt lds
s_addk_i32 s83,1024
L_t31v00_%=:
v_exp_f32 v187,v187
v_exp_f32 v188,v188
v_exp_f32 v189,v189
v_exp_f32 v190,v190
v_exp_f32 v191,v191
.p2align 3
v_cvt_pk_bf16_f32 v173,v186,v187
v_cvt_pk_bf16_f32 v174,v188,v189
v_cvt_pk_bf16_f32 v175,v190,v191
s_cmp_lg_u32 s53,0
s_cbranch_scc1 L_att0v0_%=
s_waitcnt lgkmcnt(0)
s_nop 7
s_branch L_att0x_%=
.p2align 5
L_att0v1_%=:
v_mfma_f32_16x16x32_bf16 v[232:235],a[128:131],v[112:115],v[44:47]
s_waitcnt vmcnt(8) lgkmcnt(0)
v_exp_f32 v200,v200
v_mfma_f32_16x16x32_bf16 v[232:235],a[132:135],v[116:119],v[232:235]
s_barrier
ds_read_b128 a[128:131],v1
v_add_u32 v2,s48,v2
v_mfma_f32_16x16x32_bf16 v[232:235],a[136:139],v[120:123],v[232:235]
s_mov_b32 s90,L_endseg_%=-L_pc_%=+128
v_exp_f32 v201,v201
v_mfma_f32_16x16x32_bf16 v[232:235],a[140:143],v[124:127],v[232:235]
s_add_u32 m0,s49,s51
ds_read_b128 a[132:135],v1 offset:256
v_cvt_pk_bf16_f32 v200,v200,v201
v_mfma_f32_16x16x32_bf16 v[236:239],a[156:159],v[124:127],v[44:47]
s_lshr_b32 s45,s45,1
v_exp_f32 v202,v202
v_mfma_f32_16x16x32_bf16 v[236:239],a[152:155],v[120:123],v[236:239]
ds_read_b128 a[136:139],v1 offset:512
v_add_u32 v3,s48,v3
v_mfma_f32_16x16x32_bf16 v[236:239],a[148:151],v[116:119],v[236:239]
buffer_load_dwordx4 v4,s[20:23],s47 offen nt lds
v_mfma_f32_16x16x32_bf16 v[236:239],a[144:147],v[112:115],v[236:239]
v_exp_f32 v203,v203
v_mfma_f32_16x16x32_bf16 v[248:251],a[160:163],v[112:115],v[44:47]
s_add_u32 s62,s50,32768
)A"
R"A(ds_read_b128 a[140:143],v1 offset:768
v_cvt_pk_bf16_f32 v201,v202,v203
v_mfma_f32_16x16x32_bf16 v[248:251],a[164:167],v[116:119],v[248:251]
s_and_b32 s62,s62,131071
v_exp_f32 v204,v204
v_mfma_f32_16x16x32_bf16 v[248:251],a[168:171],v[120:123],v[248:251]
s_addk_i32 s54,16384
v_exp_f32 v205,v205
v_mfma_f32_16x16x32_bf16 v[248:251],a[172:175],v[124:127],v[248:251]
s_sub_u32 s48,s62,s50
ds_read_b128 a[156:159],v1 offset:4864
v_cvt_pk_bf16_f32 v202,v204,v205
v_mfma_f32_16x16x32_bf16 v[252:255],a[188:191],v[124:127],v[44:47]
s_sub_u32 s53,s53,1
v_exp_f32 v206,v206
v_mfma_f32_16x16x32_bf16 v[252:255],a[184:187],v[120:123],v[252:255]
s_and_b32 s46,s54,0xfffc0000
v_exp_f32 v207,v207
v_mfma_f32_16x16x32_bf16 v[252:255],a[180:183],v[116:119],v[252:255]
s_mov_b32 s50,s62
ds_read_b128 a[152:155],v1 offset:4608
v_cvt_pk_bf16_f32 v203,v206,v207
v_mfma_f32_16x16x32_bf16 v[252:255],a[176:179],v[112:115],v[252:255]
s_xor_b32 s49,s50,65536
v_exp_f32 v216,v216
ds_read_b128 a[148:151],v1 offset:4352
v_exp_f32 v217,v217
ds_read_b128 a[144:147],v1 offset:4096
v_cvt_pk_bf16_f32 v204,v216,v217
v_exp_f32 v218,v218
buffer_load_dwordx4 v4,s[20:23],s47 offen offset:1024 nt lds
ds_read_b128 a[160:163],v1 offset:8192
v_exp_f32 v219,v219
ds_read_b128 a[164:167],v1 offset:8448
v_cvt_pk_bf16_f32 v205,v218,v219
v_exp_f32 v220,v220
ds_read_b128 a[168:171],v1 offset:8704
v_exp_f32 v221,v221
ds_read_b128 a[172:175],v1 offset:8960
v_cvt_pk_bf16_f32 v206,v220,v221
v_exp_f32 v222,v222
ds_read_b128 a[188:191],v1 offset:13056
v_exp_f32 v223,v223
ds_read_b128 a[184:187],v1 offset:12800
v_cvt_pk_bf16_f32 v207,v222,v223
s_waitcnt lgkmcnt(11)
v_mfma_f32_16x16x32_bf16 v[136:139],a[128:131],v[64:67],v[32:35]
ds_read_b128 a[180:183],v1 offset:12544
v_mfma_f32_16x16x32_bf16 v[136:139],a[132:135],v[68:71],v[136:139]
buffer_load_dwordx4 v5,s[20:23],s47 offen offset:2048 nt lds
v_mfma_f32_16x16x32_bf16 v[136:139],a[136:139],v[72:75],v[136:139]
s_waitcnt lgkmcnt(11)
ds_read_b128 a[176:179],v1 offset:12288
v_add_u32 v1,s48,v1
v_mfma_f32_16x16x32_bf16 v[136:139],a[140:143],v[76:79],v[136:139]
s_waitcnt lgkmcnt(11)
v_exp_f32 v232,v232
v_mfma_f32_16x16x32_bf16 v[140:143],a[156:159],v[76:79],v[32:35]
s_waitcnt lgkmcnt(10)
v_exp_f32 v233,v233
v_mfma_f32_16x16x32_bf16 v[140:143],a[152:155],v[72:75],v[140:143]
s_waitcnt lgkmcnt(9)
v_exp_f32 v234,v234
v_mfma_f32_16x16x32_bf16 v[140:143],a[148:151],v[68:71],v[140:143]
s_waitcnt lgkmcnt(8)
v_exp_f32 v235,v235
v_mfma_f32_16x16x32_bf16 v[140:143],a[144:147],v[64:67],v[140:143]
s_waitcnt lgkmcnt(2)
v_exp_f32 v236,v236
v_mfma_f32_16x16x32_bf16 v[152:155],a[160:163],v[64:67],v[32:35]
v_exp_f32 v237,v237
v_mfma_f32_16x16x32_bf16 v[152:155],a[164:167],v[68:71],v[152:155]
v_exp_f32 v238,v238
v_mfma_f32_16x16x32_bf16 v[152:155],a[168:171],v[72:75],v[152:155]
v_exp_f32 v239,v239
v_mfma_f32_16x16x32_bf16 v[152:155],a[172:175],v[76:79],v[152:155]
v_exp_f32 v248,v248
v_mfma_f32_16x16x32_bf16 v[156:159],a[188:191],v[76:79],v[32:35]
v_exp_f32 v249,v249
v_mfma_f32_16x16x32_bf16 v[156:159],a[184:187],v[72:75],v[156:159]
s_waitcnt lgkmcnt(0)
v_exp_f32 v250,v250
v_mfma_f32_16x16x32_bf16 v[156:159],a[180:183],v[68:71],v[156:159]
v_exp_f32 v251,v251
v_mfma_f32_16x16x32_bf16 v[156:159],a[176:179],v[64:67],v[156:159]
buffer_load_dwordx4 v5,s[20:23],s47 offen offset:3072 nt lds
v_mfma_f32_16x16x32_bf16 a[64:67],a[192:195],v[200:203],a[64:67]
v_exp_f32 v252,v252
v_mfma_f32_16x16x32_bf16 a[64:67],a[196:199],v[204:207],a[64:67]
v_exp_f32 v253,v253
v_mfma_f32_16x16x32_bf16 a[68:71],a[204:207],v[204:207],a[68:71]
v_exp_f32 v254,v254
v_mfma_f32_16x16x32_bf16 a[68:71],a[200:203],v[200:203],a[68:71]
v_exp_f32 v255,v255
v_mfma_f32_16x16x32_bf16 a[72:75],a[208:211],v[200:203],a[72:75]
v_exp_f32 v136,v136
v_mfma_f32_16x16x32_bf16 a[72:75],a[212:215],v[204:207],a[72:75]
v_exp_f32 v137,v137
v_mfma_f32_16x16x32_bf16 a[76:79],a[220:223],v[204:207],a[76:79]
v_exp_f32 v138,v138
v_mfma_f32_16x16x32_bf16 a[76:79],a[216:219],v[200:203],a[76:79]
v_exp_f32 v139,v139
v_mfma_f32_16x16x32_bf16 a[80:83],a[224:227],v[200:203],a[80:83]
v_exp_f32 v140,v140
v_mfma_f32_16x16x32_bf16 a[80:83],a[228:231],v[204:207],a[80:83]
v_exp_f32 v141,v141
v_mfma_f32_16x16x32_bf16 a[84:87],a[236:239],v[204:207],a[84:87]
s_addk_i32 m0,16384
v_exp_f32 v142,v142
v_mfma_f32_16x16x32_bf16 a[84:87],a[232:235],v[200:203],a[84:87]
v_exp_f32 v143,v143
v_mfma_f32_16x16x32_bf16 a[88:91],a[240:243],v[200:203],a[88:91]
v_exp_f32 v152,v152
v_mfma_f32_16x16x32_bf16 a[88:91],a[244:247],v[204:207],a[88:91]
buffer_load_dwordx4 v4,s[24:27],s47 offen nt lds
v_mfma_f32_16x16x32_bf16 a[92:95],a[252:255],v[204:207],a[92:95]
v_cvt_pk_bf16_f32 v232,v232,v233
v_cvt_pk_bf16_f32 v233,v234,v235
v_mfma_f32_16x16x32_bf16 a[92:95],a[248:251],v[200:203],a[92:95]
v_cvt_pk_bf16_f32 v234,v236,v237
v_cvt_pk_bf16_f32 v235,v238,v239
v_smfmac_f32_16x16x64_bf16 v[56:59],v[28:31],v[200:207],v27
.p2align 3
v_cvt_pk_bf16_f32 v236,v248,v249
v_cvt_pk_bf16_f32 v237,v250,v251
v_cvt_pk_bf16_f32 v238,v252,v253
v_cvt_pk_bf16_f32 v239,v254,v255
v_exp_f32 v153,v153
v_exp_f32 v154,v154
v_exp_f32 v155,v155
v_exp_f32 v156,v156
v_exp_f32 v157,v157
v_exp_f32 v158,v158
v_exp_f32 v159,v159
buffer_load_dwordx4 v5,s[24:27],s47 offen offset:1024 nt lds
v_mfma_f32_16x16x32_bf16 a[96:99],a[192:195],v[232:235],a[96:99]
ds_read_b64_tr_b16 a[192:193],v2
v_mfma_f32_16x16x32_bf16 a[96:99],a[196:199],v[236:239],a[96:99]
ds_read_b64_tr_b16 a[194:195],v2 offset:4096
v_mfma_f32_16x16x32_bf16 a[100:103],a[204:207],v[236:239],a[100:103]
ds_read_b64_tr_b16 a[196:197],v2 offset:8192
v_mfma_f32_16x16x32_bf16 a[100:103],a[200:203],v[232:235],a[100:103]
v_mfma_f32_16x16x32_bf16 a[104:107],a[208:211],v[232:235],a[104:107]
v_mfma_f32_16x16x32_bf16 a[104:107],a[212:215],v[236:239],a[104:107]
ds_read_b64_tr_b16 a[198:199],v2 offset:12288
v_mfma_f32_16x16x32_bf16 a[108:111],a[220:223],v[236:239],a[108:111]
v_mfma_f32_16x16x32_bf16 a[108:111],a[216:219],v[232:235],a[108:111]
v_mfma_f32_16x16x32_bf16 a[112:115],a[224:227],v[232:235],a[112:115]
buffer_load_dwordx4 v4,s[24:27],s47 offen offset:2048 nt lds
v_mfma_f32_16x16x32_bf16 a[112:115],a[228:231],v[236:239],a[112:115]
ds_read_b64_tr_b16 a[204:205],v3 offset:8192
v_mfma_f32_16x16x32_bf16 a[116:119],a[236:239],v[236:239],a[116:119]
ds_read_b64_tr_b16 a[206:207],v3 offset:12288
v_cvt_pk_bf16_f32 v136,v136,v137
v_mfma_f32_16x16x32_bf16 a[116:119],a[232:235],v[232:235],a[116:119]
v_mfma_f32_16x16x32_bf16 a[120:123],a[240:243],v[232:235],a[120:123]
ds_read_b64_tr_b16 a[200:201],v3
v_mfma_f32_16x16x32_bf16 a[120:123],a[244:247],v[236:239],a[120:123]
ds_read_b64_tr_b16 a[202:203],v3 offset:4096
v_cvt_pk_bf16_f32 v137,v138,v139
v_mfma_f32_16x16x32_bf16 a[124:127],a[252:255],v[236:239],a[124:127]
ds_read_b64_tr_b16 a[208:209],v2 offset:256
v_mfma_f32_16x16x32_bf16 a[124:127],a[248:251],v[232:235],a[124:127]
ds_read_b64_tr_b16 a[210:211],v2 offset:4352
v_smfmac_f32_16x16x64_bf16 v[60:63],v[28:31],v[232:239],v27
ds_read_b64_tr_b16 a[212:213],v2 offset:8448
v_mfma_f32_16x16x32_bf16 v[200:203],a[128:131],v[96:99],v[40:43]
ds_read_b64_tr_b16 a[214:215],v2 offset:12544
v_cvt_pk_bf16_f32 v138,v140,v141
v_mfma_f32_16x16x32_bf16 v[200:203],a[132:135],v[100:103],v[200:203]
ds_read_b64_tr_b16 a[220:221],v3 offset:8448
v_mfma_f32_16x16x32_bf16 v[200:203],a[136:139],v[104:107],v[200:203]
ds_read_b64_tr_b16 a[222:223],v3 offset:12544
v_cvt_pk_bf16_f32 v139,v142,v143
v_mfma_f32_16x16x32_bf16 v[200:203],a[140:143],v[108:111],v[200:203]
ds_read_b64_tr_b16 a[216:217],v3 offset:256
v_mfma_f32_16x16x32_bf16 v[204:207],a[156:159],v[108:111],v[40:43]
ds_read_b64_tr_b16 a[218:219],v3 offset:4352
v_mfma_f32_16x16x32_bf16 v[204:207],a[152:155],v[104:107],v[204:207]
buffer_load_dwordx4 v5,s[24:27],s47 offen offset:3072 nt lds
v_mfma_f32_16x16x32_bf16 v[204:207],a[148:151],v[100:103],v[204:207]
s_or_b32 s47,s47,0x18000
ds_read_b64_tr_b16 a[224:225],v2 offset:512
v_cvt_pk_bf16_f32 v140,v152,v153
v_mfma_f32_16x16x32_bf16 v[204:207],a[144:147],v[96:99],v[204:207]
s_addk_i32 s47,4096
s_and_b32 s47,s47,0x27000
ds_read_b64_tr_b16 a[226:227],v2 offset:4608
v_mfma_f32_16x16x32_bf16 v[216:219],a[160:163],v[96:99],v[40:43]
s_or_b32 s47,s47,s46
ds_read_b64_tr_b16 a[228:229],v2 offset:8704
v_cvt_pk_bf16_f32 v141,v154,v155
v_mfma_f32_16x16x32_bf16 v[216:219],a[164:167],v[100:103],v[216:219]
ds_read_b64_tr_b16 a[230:231],v2 offset:12800
v_mfma_f32_16x16x32_bf16 v[216:219],a[168:171],v[104:107],v[216:219]
ds_read_b64_tr_b16 a[236:237],v3 offset:8704
v_mfma_f32_16x16x32_bf16 v[216:219],a[172:175],v[108:111],v[216:219]
ds_read_b64_tr_b16 a[238:239],v3 offset:12800
v_cvt_pk_bf16_f32 v142,v156,v157
v_mfma_f32_16x16x32_bf16 v[220:223],a[188:191],v[108:111],v[40:43]
ds_read_b64_tr_b16 a[232:233],v3 offset:512
v_mfma_f32_16x16x32_bf16 v[220:223],a[184:187],v[104:107],v[220:223]
ds_read_b64_tr_b16 a[234:235],v3 offset:4608
v_mfma_f32_16x16x32_bf16 v[220:223],a[180:183],v[100:103],v[220:223]
ds_read_b64_tr_b16 a[240:241],v2 offset:768
v_cvt_pk_bf16_f32 v143,v158,v159
v_mfma_f32_16x16x32_bf16 v[220:223],a[176:179],v[96:99],v[220:223]
s_waitcnt lgkmcnt(7)
buffer_load_dword v26,v24,s[88:91],s93 offen
v_mfma_f32_16x16x32_bf16 a[0:3],a[192:195],v[136:139],a[0:3]
ds_read_b64_tr_b16 a[242:243],v2 offset:4864
v_mfma_f32_16x16x32_bf16 a[0:3],a[196:199],v[140:143],a[0:3]
ds_read_b64_tr_b16 a[244:245],v2 offset:8960
v_mfma_f32_16x16x32_bf16 a[4:7],a[204:207],v[140:143],a[4:7]
ds_read_b64_tr_b16 a[246:247],v2 offset:13056
v_mfma_f32_16x16x32_bf16 a[4:7],a[200:203],v[136:139],a[4:7]
ds_read_b64_tr_b16 a[252:253],v3 offset:8960
v_mfma_f32_16x16x32_bf16 a[8:11],a[208:211],v[136:139],a[8:11]
ds_read_b64_tr_b16 a[254:255],v3 offset:13056
v_mfma_f32_16x16x32_bf16 a[8:11],a[212:215],v[140:143],a[8:11]
ds_read_b64_tr_b16 a[248:249],v3 offset:768
v_mfma_f32_16x16x32_bf16 a[12:15],a[220:223],v[140:143],a[12:15]
ds_read_b64_tr_b16 a[250:251],v3 offset:4864
v_mfma_f32_16x16x32_bf16 a[12:15],a[216:219],v[136:139],a[12:15]
s_bitcmp1_b32 s45,0
s_cbranch_scc0 L_t31v10_%=
s_mov_b32 m0,s80
s_addk_i32 s80,4096
buffer_load_dwordx4 v25,s[20:23],s83 offen nt lds
s_addk_i32 s83,1024
L_t31v10_%=:
v_mfma_f32_16x16x32_bf16 a[16:19],a[224:227],v[136:139],a[16:19]
s_waitcnt lgkmcnt(6)
v_mfma_f32_16x16x32_bf16 a[16:19],a[228:231],v[140:143],a[16:19]
v_mfma_f32_16x16x32_bf16 a[20:23],a[236:239],v[140:143],a[20:23]
v_mfma_f32_16x16x32_bf16 a[20:23],a[232:235],v[136:139],a[20:23]
v_mfma_f32_16x16x32_bf16 a[24:27],a[240:243],v[136:139],a[24:27]
s_waitcnt lgkmcnt(2)
v_mfma_f32_16x16x32_bf16 a[24:27],a[244:247],v[140:143],a[24:27]
v_mfma_f32_16x16x32_bf16 a[28:31],a[252:255],v[140:143],a[28:31]
s_waitcnt lgkmcnt(0)
v_mfma_f32_16x16x32_bf16 a[28:31],a[248:251],v[136:139],a[28:31]
s_cmp_lg_u32 s53,0
v_smfmac_f32_16x16x64_bf16 v[48:51],v[28:31],v[136:143],v27
s_cbranch_scc1 L_att0v1_%=
s_waitcnt lgkmcnt(0)
s_nop 7
s_branch L_att0x_%=
.p2align 5
L_att0v2_%=:
v_mfma_f32_16x16x32_bf16 v[232:235],a[128:131],v[112:115],v[44:47]
s_waitcnt vmcnt(8) lgkmcnt(0)
v_mfma_f32_16x16x32_bf16 v[232:235],a[132:135],v[116:119],v[232:235]
s_barrier
ds_read_b128 a[128:131],v1
v_add_u32 v2,s48,v2
v_mfma_f32_16x16x32_bf16 v[232:235],a[136:139],v[120:123],v[232:235]
s_mov_b32 s90,L_endseg_%=-L_pc_%=+128
v_mfma_f32_16x16x32_bf16 v[232:235],a[140:143],v[124:127],v[232:235]
s_add_u32 m0,s49,s51
ds_read_b128 a[132:135],v1 offset:256
v_mfma_f32_16x16x32_bf16 v[236:239],a[156:159],v[124:127],v[44:47]
s_lshr_b32 s45,s45,1
v_mfma_f32_16x16x32_bf16 v[236:239],a[152:155],v[120:123],v[236:239]
ds_read_b128 a[136:139],v1 offset:512
v_add_u32 v3,s48,v3
v_mfma_f32_16x16x32_bf16 v[236:239],a[148:151],v[116:119],v[236:239]
buffer_load_dwordx4 v4,s[20:23],s47 offen nt lds
v_mfma_f32_16x16x32_bf16 v[236:239],a[144:147],v[112:115],v[236:239]
v_mfma_f32_16x16x32_bf16 v[248:251],a[160:163],v[112:115],v[44:47]
s_add_u32 s62,s50,32768
ds_read_b128 a[140:143],v1 offset:768
v_mfma_f32_16x16x32_bf16 v[248:251],a[164:167],v[116:119],v[248:251]
s_and_b32 s62,s62,131071
v_mfma_f32_16x16x32_bf16 v[248:251],a[168:171],v[120:123],v[248:251]
s_addk_i32 s54,16384
)A"
R"A(v_mfma_f32_16x16x32_bf16 v[248:251],a[172:175],v[124:127],v[248:251]
s_sub_u32 s48,s62,s50
ds_read_b128 a[156:159],v1 offset:4864
v_mfma_f32_16x16x32_bf16 v[252:255],a[188:191],v[124:127],v[44:47]
s_sub_u32 s53,s53,1
v_mfma_f32_16x16x32_bf16 v[252:255],a[184:187],v[120:123],v[252:255]
s_and_b32 s46,s54,0xfffc0000
v_mfma_f32_16x16x32_bf16 v[252:255],a[180:183],v[116:119],v[252:255]
s_mov_b32 s50,s62
ds_read_b128 a[152:155],v1 offset:4608
v_mfma_f32_16x16x32_bf16 v[252:255],a[176:179],v[112:115],v[252:255]
s_xor_b32 s49,s50,65536
v_mfma_f32_16x16x32_bf16 a[32:35],a[192:195],v[168:171],a[32:35]
ds_read_b128 a[148:151],v1 offset:4352
v_mfma_f32_16x16x32_bf16 a[32:35],a[196:199],v[172:175],a[32:35]
v_mfma_f32_16x16x32_bf16 a[36:39],a[204:207],v[172:175],a[36:39]
ds_read_b128 a[144:147],v1 offset:4096
v_mfma_f32_16x16x32_bf16 a[36:39],a[200:203],v[168:171],a[36:39]
v_mfma_f32_16x16x32_bf16 a[40:43],a[208:211],v[168:171],a[40:43]
buffer_load_dwordx4 v4,s[20:23],s47 offen offset:1024 nt lds
v_mfma_f32_16x16x32_bf16 a[40:43],a[212:215],v[172:175],a[40:43]
ds_read_b128 a[160:163],v1 offset:8192
v_mfma_f32_16x16x32_bf16 a[44:47],a[220:223],v[172:175],a[44:47]
v_mfma_f32_16x16x32_bf16 a[44:47],a[216:219],v[168:171],a[44:47]
ds_read_b128 a[164:167],v1 offset:8448
v_mfma_f32_16x16x32_bf16 a[48:51],a[224:227],v[168:171],a[48:51]
v_mfma_f32_16x16x32_bf16 a[48:51],a[228:231],v[172:175],a[48:51]
ds_read_b128 a[168:171],v1 offset:8704
v_mfma_f32_16x16x32_bf16 a[52:55],a[236:239],v[172:175],a[52:55]
v_mfma_f32_16x16x32_bf16 a[52:55],a[232:235],v[168:171],a[52:55]
ds_read_b128 a[172:175],v1 offset:8960
v_mfma_f32_16x16x32_bf16 a[56:59],a[240:243],v[168:171],a[56:59]
v_mfma_f32_16x16x32_bf16 a[56:59],a[244:247],v[172:175],a[56:59]
ds_read_b128 a[188:191],v1 offset:13056
v_mfma_f32_16x16x32_bf16 a[60:63],a[252:255],v[172:175],a[60:63]
v_mfma_f32_16x16x32_bf16 a[60:63],a[248:251],v[168:171],a[60:63]
ds_read_b128 a[184:187],v1 offset:12800
v_smfmac_f32_16x16x64_bf16 v[52:55],v[28:31],v[168:175],v27
s_waitcnt lgkmcnt(4)
v_mfma_f32_16x16x32_bf16 v[136:139],a[128:131],v[64:67],v[32:35]
ds_read_b128 a[180:183],v1 offset:12544
v_mfma_f32_16x16x32_bf16 v[136:139],a[132:135],v[68:71],v[136:139]
buffer_load_dwordx4 v5,s[20:23],s47 offen offset:2048 nt lds
v_mfma_f32_16x16x32_bf16 v[136:139],a[136:139],v[72:75],v[136:139]
ds_read_b128 a[176:179],v1 offset:12288
v_add_u32 v1,s48,v1
v_mfma_f32_16x16x32_bf16 v[136:139],a[140:143],v[76:79],v[136:139]
v_exp_f32 v232,v232
v_mfma_f32_16x16x32_bf16 v[140:143],a[156:159],v[76:79],v[32:35]
v_exp_f32 v233,v233
v_mfma_f32_16x16x32_bf16 v[140:143],a[152:155],v[72:75],v[140:143]
v_exp_f32 v234,v234
v_mfma_f32_16x16x32_bf16 v[140:143],a[148:151],v[68:71],v[140:143]
v_exp_f32 v235,v235
v_mfma_f32_16x16x32_bf16 v[140:143],a[144:147],v[64:67],v[140:143]
v_exp_f32 v236,v236
v_mfma_f32_16x16x32_bf16 v[152:155],a[160:163],v[64:67],v[32:35]
v_exp_f32 v237,v237
v_mfma_f32_16x16x32_bf16 v[152:155],a[164:167],v[68:71],v[152:155]
s_waitcnt lgkmcnt(1)
v_exp_f32 v238,v238
v_mfma_f32_16x16x32_bf16 v[152:155],a[168:171],v[72:75],v[152:155]
v_exp_f32 v239,v239
v_mfma_f32_16x16x32_bf16 v[152:155],a[172:175],v[76:79],v[152:155]
v_exp_f32 v248,v248
v_mfma_f32_16x16x32_bf16 v[156:159],a[188:191],v[76:79],v[32:35]
v_exp_f32 v249,v249
v_mfma_f32_16x16x32_bf16 v[156:159],a[184:187],v[72:75],v[156:159]
v_exp_f32 v250,v250
v_mfma_f32_16x16x32_bf16 v[156:159],a[180:183],v[68:71],v[156:159]
s_waitcnt lgkmcnt(0)
v_exp_f32 v251,v251
v_mfma_f32_16x16x32_bf16 v[156:159],a[176:179],v[64:67],v[156:159]
buffer_load_dwordx4 v5,s[20:23],s47 offen offset:3072 nt lds
v_exp_f32 v252,v252
v_exp_f32 v253,v253
v_exp_f32 v254,v254
v_exp_f32 v255,v255
v_exp_f32 v136,v136
v_exp_f32 v137,v137
v_exp_f32 v138,v138
v_exp_f32 v139,v139
v_exp_f32 v140,v140
v_exp_f32 v141,v141
s_addk_i32 m0,16384
v_exp_f32 v142,v142
v_exp_f32 v143,v143
v_exp_f32 v152,v152
buffer_load_dwordx4 v4,s[24:27],s47 offen nt lds
.p2align 3
v_cvt_pk_bf16_f32 v232,v232,v233
v_cvt_pk_bf16_f32 v233,v234,v235
v_cvt_pk_bf16_f32 v234,v236,v237
v_cvt_pk_bf16_f32 v235,v238,v239
v_cvt_pk_bf16_f32 v236,v248,v249
v_mfma_f32_16x16x32_bf16 v[168:171],a[128:131],v[80:83],v[36:39]
v_cvt_pk_bf16_f32 v237,v250,v251
v_cvt_pk_bf16_f32 v238,v252,v253
v_mfma_f32_16x16x32_bf16 v[168:171],a[132:135],v[84:87],v[168:171]
v_cvt_pk_bf16_f32 v239,v254,v255
v_mfma_f32_16x16x32_bf16 v[168:171],a[136:139],v[88:91],v[168:171]
v_exp_f32 v153,v153
v_mfma_f32_16x16x32_bf16 v[168:171],a[140:143],v[92:95],v[168:171]
v_exp_f32 v154,v154
v_mfma_f32_16x16x32_bf16 v[172:175],a[156:159],v[92:95],v[36:39]
v_exp_f32 v155,v155
v_mfma_f32_16x16x32_bf16 v[172:175],a[152:155],v[88:91],v[172:175]
v_exp_f32 v156,v156
v_mfma_f32_16x16x32_bf16 v[172:175],a[148:151],v[84:87],v[172:175]
v_exp_f32 v157,v157
v_mfma_f32_16x16x32_bf16 v[172:175],a[144:147],v[80:83],v[172:175]
v_exp_f32 v158,v158
v_mfma_f32_16x16x32_bf16 v[184:187],a[160:163],v[80:83],v[36:39]
v_exp_f32 v159,v159
v_mfma_f32_16x16x32_bf16 v[184:187],a[164:167],v[84:87],v[184:187]
v_exp_f32 v168,v168
v_mfma_f32_16x16x32_bf16 v[184:187],a[168:171],v[88:91],v[184:187]
buffer_load_dwordx4 v5,s[24:27],s47 offen offset:1024 nt lds
v_mfma_f32_16x16x32_bf16 v[184:187],a[172:175],v[92:95],v[184:187]
v_exp_f32 v169,v169
v_mfma_f32_16x16x32_bf16 v[188:191],a[188:191],v[92:95],v[36:39]
v_exp_f32 v170,v170
v_mfma_f32_16x16x32_bf16 v[188:191],a[184:187],v[88:91],v[188:191]
v_exp_f32 v171,v171
v_mfma_f32_16x16x32_bf16 v[188:191],a[180:183],v[84:87],v[188:191]
v_exp_f32 v172,v172
v_mfma_f32_16x16x32_bf16 v[188:191],a[176:179],v[80:83],v[188:191]
v_exp_f32 v173,v173
v_mfma_f32_16x16x32_bf16 a[96:99],a[192:195],v[232:235],a[96:99]
ds_read_b64_tr_b16 a[192:193],v2
v_cvt_pk_bf16_f32 v168,v168,v169
v_mfma_f32_16x16x32_bf16 a[96:99],a[196:199],v[236:239],a[96:99]
ds_read_b64_tr_b16 a[194:195],v2 offset:4096
v_cvt_pk_bf16_f32 v169,v170,v171
v_mfma_f32_16x16x32_bf16 a[100:103],a[204:207],v[236:239],a[100:103]
ds_read_b64_tr_b16 a[196:197],v2 offset:8192
v_cvt_pk_bf16_f32 v170,v172,v173
v_mfma_f32_16x16x32_bf16 a[100:103],a[200:203],v[232:235],a[100:103]
v_exp_f32 v174,v174
v_mfma_f32_16x16x32_bf16 a[104:107],a[208:211],v[232:235],a[104:107]
v_exp_f32 v175,v175
v_mfma_f32_16x16x32_bf16 a[104:107],a[212:215],v[236:239],a[104:107]
ds_read_b64_tr_b16 a[198:199],v2 offset:12288
v_cvt_pk_bf16_f32 v171,v174,v175
v_mfma_f32_16x16x32_bf16 a[108:111],a[220:223],v[236:239],a[108:111]
v_exp_f32 v184,v184
v_mfma_f32_16x16x32_bf16 a[108:111],a[216:219],v[232:235],a[108:111]
v_exp_f32 v185,v185
v_mfma_f32_16x16x32_bf16 a[112:115],a[224:227],v[232:235],a[112:115]
buffer_load_dwordx4 v4,s[24:27],s47 offen offset:2048 nt lds
v_mfma_f32_16x16x32_bf16 a[112:115],a[228:231],v[236:239],a[112:115]
ds_read_b64_tr_b16 a[204:205],v3 offset:8192
v_cvt_pk_bf16_f32 v172,v184,v185
v_mfma_f32_16x16x32_bf16 a[116:119],a[236:239],v[236:239],a[116:119]
ds_read_b64_tr_b16 a[206:207],v3 offset:12288
v_cvt_pk_bf16_f32 v136,v136,v137
v_mfma_f32_16x16x32_bf16 a[116:119],a[232:235],v[232:235],a[116:119]
v_exp_f32 v186,v186
v_mfma_f32_16x16x32_bf16 a[120:123],a[240:243],v[232:235],a[120:123]
ds_read_b64_tr_b16 a[200:201],v3
v_mfma_f32_16x16x32_bf16 a[120:123],a[244:247],v[236:239],a[120:123]
ds_read_b64_tr_b16 a[202:203],v3 offset:4096
v_cvt_pk_bf16_f32 v137,v138,v139
v_mfma_f32_16x16x32_bf16 a[124:127],a[252:255],v[236:239],a[124:127]
ds_read_b64_tr_b16 a[208:209],v2 offset:256
v_mfma_f32_16x16x32_bf16 a[124:127],a[248:251],v[232:235],a[124:127]
ds_read_b64_tr_b16 a[210:211],v2 offset:4352
v_smfmac_f32_16x16x64_bf16 v[60:63],v[28:31],v[232:239],v27
ds_read_b64_tr_b16 a[212:213],v2 offset:8448
ds_read_b64_tr_b16 a[214:215],v2 offset:12544
v_cvt_pk_bf16_f32 v138,v140,v141
ds_read_b64_tr_b16 a[220:221],v3 offset:8448
ds_read_b64_tr_b16 a[222:223],v3 offset:12544
v_cvt_pk_bf16_f32 v139,v142,v143
ds_read_b64_tr_b16 a[216:217],v3 offset:256
ds_read_b64_tr_b16 a[218:219],v3 offset:4352
buffer_load_dwordx4 v5,s[24:27],s47 offen offset:3072 nt lds
s_or_b32 s47,s47,0x18000
ds_read_b64_tr_b16 a[224:225],v2 offset:512
v_cvt_pk_bf16_f32 v140,v152,v153
s_addk_i32 s47,4096
s_and_b32 s47,s47,0x27000
ds_read_b64_tr_b16 a[226:227],v2 offset:4608
s_or_b32 s47,s47,s46
ds_read_b64_tr_b16 a[228:229],v2 offset:8704
v_cvt_pk_bf16_f32 v141,v154,v155
ds_read_b64_tr_b16 a[230:231],v2 offset:12800
ds_read_b64_tr_b16 a[236:237],v3 offset:8704
ds_read_b64_tr_b16 a[238:239],v3 offset:12800
v_cvt_pk_bf16_f32 v142,v156,v157
ds_read_b64_tr_b16 a[232:233],v3 offset:512
ds_read_b64_tr_b16 a[234:235],v3 offset:4608
ds_read_b64_tr_b16 a[240:241],v2 offset:768
v_cvt_pk_bf16_f32 v143,v158,v159
buffer_load_dword v26,v24,s[88:91],s93 offen
s_waitcnt lgkmcnt(15)
v_mfma_f32_16x16x32_bf16 a[0:3],a[192:195],v[136:139],a[0:3]
ds_read_b64_tr_b16 a[242:243],v2 offset:4864
v_mfma_f32_16x16x32_bf16 a[0:3],a[196:199],v[140:143],a[0:3]
ds_read_b64_tr_b16 a[244:245],v2 offset:8960
v_mfma_f32_16x16x32_bf16 a[4:7],a[204:207],v[140:143],a[4:7]
ds_read_b64_tr_b16 a[246:247],v2 offset:13056
v_mfma_f32_16x16x32_bf16 a[4:7],a[200:203],v[136:139],a[4:7]
ds_read_b64_tr_b16 a[252:253],v3 offset:8960
v_mfma_f32_16x16x32_bf16 a[8:11],a[208:211],v[136:139],a[8:11]
s_waitcnt lgkmcnt(15)
ds_read_b64_tr_b16 a[254:255],v3 offset:13056
v_mfma_f32_16x16x32_bf16 a[8:11],a[212:215],v[140:143],a[8:11]
ds_read_b64_tr_b16 a[248:249],v3 offset:768
v_mfma_f32_16x16x32_bf16 a[12:15],a[220:223],v[140:143],a[12:15]
s_waitcnt lgkmcnt(15)
ds_read_b64_tr_b16 a[250:251],v3 offset:4864
v_mfma_f32_16x16x32_bf16 a[12:15],a[216:219],v[136:139],a[12:15]
s_bitcmp1_b32 s45,0
s_cbranch_scc0 L_t31v20_%=
s_mov_b32 m0,s80
s_addk_i32 s80,4096
buffer_load_dwordx4 v25,s[20:23],s83 offen nt lds
s_addk_i32 s83,1024
L_t31v20_%=:
s_waitcnt lgkmcnt(7)
v_mfma_f32_16x16x32_bf16 a[16:19],a[224:227],v[136:139],a[16:19]
v_exp_f32 v187,v187
v_mfma_f32_16x16x32_bf16 a[16:19],a[228:231],v[140:143],a[16:19]
v_exp_f32 v188,v188
v_mfma_f32_16x16x32_bf16 a[20:23],a[236:239],v[140:143],a[20:23]
v_exp_f32 v189,v189
v_mfma_f32_16x16x32_bf16 a[20:23],a[232:235],v[136:139],a[20:23]
s_waitcnt lgkmcnt(3)
v_exp_f32 v190,v190
v_mfma_f32_16x16x32_bf16 a[24:27],a[240:243],v[136:139],a[24:27]
v_exp_f32 v191,v191
v_mfma_f32_16x16x32_bf16 a[24:27],a[244:247],v[140:143],a[24:27]
s_waitcnt lgkmcnt(1)
v_cvt_pk_bf16_f32 v173,v186,v187
v_mfma_f32_16x16x32_bf16 a[28:31],a[252:255],v[140:143],a[28:31]
s_waitcnt lgkmcnt(0)
v_cvt_pk_bf16_f32 v174,v188,v189
v_cvt_pk_bf16_f32 v175,v190,v191
v_mfma_f32_16x16x32_bf16 a[28:31],a[248:251],v[136:139],a[28:31]
s_cmp_lg_u32 s53,0
v_smfmac_f32_16x16x64_bf16 v[48:51],v[28:31],v[136:143],v27
s_cbranch_scc1 L_att0v2_%=
s_waitcnt lgkmcnt(0)
s_nop 7
s_branch L_att0x_%=
.p2align 5
L_att0_%=:
v_mfma_f32_16x16x32_bf16 v[232:235],a[128:131],v[112:115],v[44:47]
s_waitcnt vmcnt(8) lgkmcnt(0)
v_exp_f32 v200,v200
v_mfma_f32_16x16x32_bf16 v[232:235],a[132:135],v[116:119],v[232:235]
s_barrier
ds_read_b128 a[128:131],v1
v_add_u32 v2,s48,v2
v_mfma_f32_16x16x32_bf16 v[232:235],a[136:139],v[120:123],v[232:235]
s_mov_b32 s90,L_endseg_%=-L_pc_%=+128
v_exp_f32 v201,v201
v_mfma_f32_16x16x32_bf16 v[232:235],a[140:143],v[124:127],v[232:235]
s_add_u32 m0,s49,s51
ds_read_b128 a[132:135],v1 offset:256
v_cvt_pk_bf16_f32 v200,v200,v201
v_mfma_f32_16x16x32_bf16 v[236:239],a[156:159],v[124:127],v[44:47]
s_lshr_b32 s45,s45,1
v_exp_f32 v202,v202
v_mfma_f32_16x16x32_bf16 v[236:239],a[152:155],v[120:123],v[236:239]
s_sub_u32 s94,s94,3
ds_read_b128 a[136:139],v1 offset:512
v_add_u32 v3,s48,v3
v_mfma_f32_16x16x32_bf16 v[236:239],a[148:151],v[116:119],v[236:239]
s_lshr_b32 s60,s94,2
buffer_load_dwordx4 v4,s[20:23],s47 offen nt lds
v_mfma_f32_16x16x32_bf16 v[236:239],a[144:147],v[112:115],v[236:239]
s_max_i32 s60,s60,124
v_exp_f32 v203,v203
v_mfma_f32_16x16x32_bf16 v[248:251],a[160:163],v[112:115],v[44:47]
s_add_u32 s62,s50,32768
ds_read_b128 a[140:143],v1 offset:768
v_cvt_pk_bf16_f32 v201,v202,v203
v_mfma_f32_16x16x32_bf16 v[248:251],a[164:167],v[116:119],v[248:251]
s_and_b32 s62,s62,131071
v_exp_f32 v204,v204
v_mfma_f32_16x16x32_bf16 v[248:251],a[168:171],v[120:123],v[248:251]
)A"
R"A(s_addk_i32 s54,16384
v_exp_f32 v205,v205
v_mfma_f32_16x16x32_bf16 v[248:251],a[172:175],v[124:127],v[248:251]
s_sub_u32 s48,s62,s50
ds_read_b128 a[156:159],v1 offset:4864
v_cvt_pk_bf16_f32 v202,v204,v205
v_mfma_f32_16x16x32_bf16 v[252:255],a[188:191],v[124:127],v[44:47]
s_sub_u32 s53,s53,1
v_exp_f32 v206,v206
v_mfma_f32_16x16x32_bf16 v[252:255],a[184:187],v[120:123],v[252:255]
s_and_b32 s46,s54,0xfffc0000
v_exp_f32 v207,v207
v_mfma_f32_16x16x32_bf16 v[252:255],a[180:183],v[116:119],v[252:255]
s_mov_b32 s50,s62
ds_read_b128 a[152:155],v1 offset:4608
v_cvt_pk_bf16_f32 v203,v206,v207
v_mfma_f32_16x16x32_bf16 v[252:255],a[176:179],v[112:115],v[252:255]
s_xor_b32 s49,s50,65536
v_exp_f32 v216,v216
v_mfma_f32_16x16x32_bf16 a[32:35],a[192:195],v[168:171],a[32:35]
ds_read_b128 a[148:151],v1 offset:4352
v_mfma_f32_16x16x32_bf16 a[32:35],a[196:199],v[172:175],a[32:35]
v_exp_f32 v217,v217
v_mfma_f32_16x16x32_bf16 a[36:39],a[204:207],v[172:175],a[36:39]
ds_read_b128 a[144:147],v1 offset:4096
v_cvt_pk_bf16_f32 v204,v216,v217
v_mfma_f32_16x16x32_bf16 a[36:39],a[200:203],v[168:171],a[36:39]
v_exp_f32 v218,v218
v_mfma_f32_16x16x32_bf16 a[40:43],a[208:211],v[168:171],a[40:43]
buffer_load_dwordx4 v4,s[20:23],s47 offen offset:1024 nt lds
v_mfma_f32_16x16x32_bf16 a[40:43],a[212:215],v[172:175],a[40:43]
ds_read_b128 a[160:163],v1 offset:8192
v_mfma_f32_16x16x32_bf16 a[44:47],a[220:223],v[172:175],a[44:47]
v_exp_f32 v219,v219
v_mfma_f32_16x16x32_bf16 a[44:47],a[216:219],v[168:171],a[44:47]
ds_read_b128 a[164:167],v1 offset:8448
v_cvt_pk_bf16_f32 v205,v218,v219
v_mfma_f32_16x16x32_bf16 a[48:51],a[224:227],v[168:171],a[48:51]
v_exp_f32 v220,v220
v_mfma_f32_16x16x32_bf16 a[48:51],a[228:231],v[172:175],a[48:51]
ds_read_b128 a[168:171],v1 offset:8704
v_mfma_f32_16x16x32_bf16 a[52:55],a[236:239],v[172:175],a[52:55]
v_exp_f32 v221,v221
v_mfma_f32_16x16x32_bf16 a[52:55],a[232:235],v[168:171],a[52:55]
ds_read_b128 a[172:175],v1 offset:8960
v_cvt_pk_bf16_f32 v206,v220,v221
v_mfma_f32_16x16x32_bf16 a[56:59],a[240:243],v[168:171],a[56:59]
v_exp_f32 v222,v222
v_mfma_f32_16x16x32_bf16 a[56:59],a[244:247],v[172:175],a[56:59]
ds_read_b128 a[188:191],v1 offset:13056
v_mfma_f32_16x16x32_bf16 a[60:63],a[252:255],v[172:175],a[60:63]
v_exp_f32 v223,v223
v_mfma_f32_16x16x32_bf16 a[60:63],a[248:251],v[168:171],a[60:63]
ds_read_b128 a[184:187],v1 offset:12800
v_cvt_pk_bf16_f32 v207,v222,v223
v_smfmac_f32_16x16x64_bf16 v[52:55],v[28:31],v[168:175],v27
s_waitcnt lgkmcnt(4)
v_mfma_f32_16x16x32_bf16 v[136:139],a[128:131],v[64:67],v[32:35]
ds_read_b128 a[180:183],v1 offset:12544
v_mfma_f32_16x16x32_bf16 v[136:139],a[132:135],v[68:71],v[136:139]
buffer_load_dwordx4 v5,s[20:23],s47 offen offset:2048 nt lds
v_mfma_f32_16x16x32_bf16 v[136:139],a[136:139],v[72:75],v[136:139]
ds_read_b128 a[176:179],v1 offset:12288
v_add_u32 v1,s48,v1
v_mfma_f32_16x16x32_bf16 v[136:139],a[140:143],v[76:79],v[136:139]
v_exp_f32 v232,v232
v_mfma_f32_16x16x32_bf16 v[140:143],a[156:159],v[76:79],v[32:35]
v_exp_f32 v233,v233
v_mfma_f32_16x16x32_bf16 v[140:143],a[152:155],v[72:75],v[140:143]
v_exp_f32 v234,v234
v_mfma_f32_16x16x32_bf16 v[140:143],a[148:151],v[68:71],v[140:143]
v_exp_f32 v235,v235
v_mfma_f32_16x16x32_bf16 v[140:143],a[144:147],v[64:67],v[140:143]
v_exp_f32 v236,v236
v_mfma_f32_16x16x32_bf16 v[152:155],a[160:163],v[64:67],v[32:35]
v_exp_f32 v237,v237
v_mfma_f32_16x16x32_bf16 v[152:155],a[164:167],v[68:71],v[152:155]
s_waitcnt lgkmcnt(1)
v_exp_f32 v238,v238
v_mfma_f32_16x16x32_bf16 v[152:155],a[168:171],v[72:75],v[152:155]
v_exp_f32 v239,v239
v_mfma_f32_16x16x32_bf16 v[152:155],a[172:175],v[76:79],v[152:155]
v_exp_f32 v248,v248
v_mfma_f32_16x16x32_bf16 v[156:159],a[188:191],v[76:79],v[32:35]
v_exp_f32 v249,v249
v_mfma_f32_16x16x32_bf16 v[156:159],a[184:187],v[72:75],v[156:159]
v_exp_f32 v250,v250
v_mfma_f32_16x16x32_bf16 v[156:159],a[180:183],v[68:71],v[156:159]
s_waitcnt lgkmcnt(0)
v_exp_f32 v251,v251
v_mfma_f32_16x16x32_bf16 v[156:159],a[176:179],v[64:67],v[156:159]
buffer_load_dwordx4 v5,s[20:23],s47 offen offset:3072 nt lds
v_mfma_f32_16x16x32_bf16 a[64:67],a[192:195],v[200:203],a[64:67]
v_exp_f32 v252,v252
v_mfma_f32_16x16x32_bf16 a[64:67],a[196:199],v[204:207],a[64:67]
v_exp_f32 v253,v253
v_mfma_f32_16x16x32_bf16 a[68:71],a[204:207],v[204:207],a[68:71]
v_exp_f32 v254,v254
v_mfma_f32_16x16x32_bf16 a[68:71],a[200:203],v[200:203],a[68:71]
v_exp_f32 v255,v255
v_mfma_f32_16x16x32_bf16 a[72:75],a[208:211],v[200:203],a[72:75]
v_exp_f32 v136,v136
v_mfma_f32_16x16x32_bf16 a[72:75],a[212:215],v[204:207],a[72:75]
v_exp_f32 v137,v137
v_mfma_f32_16x16x32_bf16 a[76:79],a[220:223],v[204:207],a[76:79]
v_exp_f32 v138,v138
v_mfma_f32_16x16x32_bf16 a[76:79],a[216:219],v[200:203],a[76:79]
v_exp_f32 v139,v139
v_mfma_f32_16x16x32_bf16 a[80:83],a[224:227],v[200:203],a[80:83]
v_exp_f32 v140,v140
v_mfma_f32_16x16x32_bf16 a[80:83],a[228:231],v[204:207],a[80:83]
v_exp_f32 v141,v141
v_mfma_f32_16x16x32_bf16 a[84:87],a[236:239],v[204:207],a[84:87]
s_addk_i32 m0,16384
v_exp_f32 v142,v142
v_mfma_f32_16x16x32_bf16 a[84:87],a[232:235],v[200:203],a[84:87]
v_exp_f32 v143,v143
v_mfma_f32_16x16x32_bf16 a[88:91],a[240:243],v[200:203],a[88:91]
v_exp_f32 v152,v152
v_mfma_f32_16x16x32_bf16 a[88:91],a[244:247],v[204:207],a[88:91]
buffer_load_dwordx4 v4,s[24:27],s47 offen nt lds
v_mfma_f32_16x16x32_bf16 a[92:95],a[252:255],v[204:207],a[92:95]
v_cvt_pk_bf16_f32 v232,v232,v233
v_cvt_pk_bf16_f32 v233,v234,v235
v_mfma_f32_16x16x32_bf16 a[92:95],a[248:251],v[200:203],a[92:95]
v_cvt_pk_bf16_f32 v234,v236,v237
v_cvt_pk_bf16_f32 v235,v238,v239
v_smfmac_f32_16x16x64_bf16 v[56:59],v[28:31],v[200:207],v27
v_cvt_pk_bf16_f32 v236,v248,v249
v_mfma_f32_16x16x32_bf16 v[168:171],a[128:131],v[80:83],v[36:39]
v_cvt_pk_bf16_f32 v237,v250,v251
v_cvt_pk_bf16_f32 v238,v252,v253
v_mfma_f32_16x16x32_bf16 v[168:171],a[132:135],v[84:87],v[168:171]
v_cvt_pk_bf16_f32 v239,v254,v255
v_mfma_f32_16x16x32_bf16 v[168:171],a[136:139],v[88:91],v[168:171]
v_exp_f32 v153,v153
v_mfma_f32_16x16x32_bf16 v[168:171],a[140:143],v[92:95],v[168:171]
v_exp_f32 v154,v154
v_mfma_f32_16x16x32_bf16 v[172:175],a[156:159],v[92:95],v[36:39]
v_exp_f32 v155,v155
v_mfma_f32_16x16x32_bf16 v[172:175],a[152:155],v[88:91],v[172:175]
v_exp_f32 v156,v156
v_mfma_f32_16x16x32_bf16 v[172:175],a[148:151],v[84:87],v[172:175]
v_exp_f32 v157,v157
v_mfma_f32_16x16x32_bf16 v[172:175],a[144:147],v[80:83],v[172:175]
v_exp_f32 v158,v158
v_mfma_f32_16x16x32_bf16 v[184:187],a[160:163],v[80:83],v[36:39]
v_exp_f32 v159,v159
v_mfma_f32_16x16x32_bf16 v[184:187],a[164:167],v[84:87],v[184:187]
v_exp_f32 v168,v168
v_mfma_f32_16x16x32_bf16 v[184:187],a[168:171],v[88:91],v[184:187]
buffer_load_dwordx4 v5,s[24:27],s47 offen offset:1024 nt lds
v_mfma_f32_16x16x32_bf16 v[184:187],a[172:175],v[92:95],v[184:187]
v_exp_f32 v169,v169
v_mfma_f32_16x16x32_bf16 v[188:191],a[188:191],v[92:95],v[36:39]
v_exp_f32 v170,v170
v_mfma_f32_16x16x32_bf16 v[188:191],a[184:187],v[88:91],v[188:191]
v_exp_f32 v171,v171
v_mfma_f32_16x16x32_bf16 v[188:191],a[180:183],v[84:87],v[188:191]
v_exp_f32 v172,v172
v_mfma_f32_16x16x32_bf16 v[188:191],a[176:179],v[80:83],v[188:191]
v_exp_f32 v173,v173
v_mfma_f32_16x16x32_bf16 a[96:99],a[192:195],v[232:235],a[96:99]
ds_read_b64_tr_b16 a[192:193],v2
v_cvt_pk_bf16_f32 v168,v168,v169
v_mfma_f32_16x16x32_bf16 a[96:99],a[196:199],v[236:239],a[96:99]
ds_read_b64_tr_b16 a[194:195],v2 offset:4096
v_cvt_pk_bf16_f32 v169,v170,v171
v_mfma_f32_16x16x32_bf16 a[100:103],a[204:207],v[236:239],a[100:103]
ds_read_b64_tr_b16 a[196:197],v2 offset:8192
v_cvt_pk_bf16_f32 v170,v172,v173
v_mfma_f32_16x16x32_bf16 a[100:103],a[200:203],v[232:235],a[100:103]
v_exp_f32 v174,v174
v_mfma_f32_16x16x32_bf16 a[104:107],a[208:211],v[232:235],a[104:107]
v_exp_f32 v175,v175
v_mfma_f32_16x16x32_bf16 a[104:107],a[212:215],v[236:239],a[104:107]
ds_read_b64_tr_b16 a[198:199],v2 offset:12288
v_cvt_pk_bf16_f32 v171,v174,v175
v_mfma_f32_16x16x32_bf16 a[108:111],a[220:223],v[236:239],a[108:111]
v_exp_f32 v184,v184
v_mfma_f32_16x16x32_bf16 a[108:111],a[216:219],v[232:235],a[108:111]
v_exp_f32 v185,v185
v_mfma_f32_16x16x32_bf16 a[112:115],a[224:227],v[232:235],a[112:115]
buffer_load_dwordx4 v4,s[24:27],s47 offen offset:2048 nt lds
v_mfma_f32_16x16x32_bf16 a[112:115],a[228:231],v[236:239],a[112:115]
ds_read_b64_tr_b16 a[204:205],v3 offset:8192
v_cvt_pk_bf16_f32 v172,v184,v185
v_mfma_f32_16x16x32_bf16 a[116:119],a[236:239],v[236:239],a[116:119]
ds_read_b64_tr_b16 a[206:207],v3 offset:12288
v_cvt_pk_bf16_f32 v136,v136,v137
v_mfma_f32_16x16x32_bf16 a[116:119],a[232:235],v[232:235],a[116:119]
v_exp_f32 v186,v186
v_mfma_f32_16x16x32_bf16 a[120:123],a[240:243],v[232:235],a[120:123]
ds_read_b64_tr_b16 a[200:201],v3
v_mfma_f32_16x16x32_bf16 a[120:123],a[244:247],v[236:239],a[120:123]
ds_read_b64_tr_b16 a[202:203],v3 offset:4096
v_cvt_pk_bf16_f32 v137,v138,v139
v_mfma_f32_16x16x32_bf16 a[124:127],a[252:255],v[236:239],a[124:127]
ds_read_b64_tr_b16 a[208:209],v2 offset:256
v_mfma_f32_16x16x32_bf16 a[124:127],a[248:251],v[232:235],a[124:127]
ds_read_b64_tr_b16 a[210:211],v2 offset:4352
v_smfmac_f32_16x16x64_bf16 v[60:63],v[28:31],v[232:239],v27
ds_read_b64_tr_b16 a[212:213],v2 offset:8448
v_mfma_f32_16x16x32_bf16 v[200:203],a[128:131],v[96:99],v[40:43]
ds_read_b64_tr_b16 a[214:215],v2 offset:12544
v_cvt_pk_bf16_f32 v138,v140,v141
v_mfma_f32_16x16x32_bf16 v[200:203],a[132:135],v[100:103],v[200:203]
ds_read_b64_tr_b16 a[220:221],v3 offset:8448
v_mfma_f32_16x16x32_bf16 v[200:203],a[136:139],v[104:107],v[200:203]
ds_read_b64_tr_b16 a[222:223],v3 offset:12544
v_cvt_pk_bf16_f32 v139,v142,v143
v_mfma_f32_16x16x32_bf16 v[200:203],a[140:143],v[108:111],v[200:203]
ds_read_b64_tr_b16 a[216:217],v3 offset:256
v_mfma_f32_16x16x32_bf16 v[204:207],a[156:159],v[108:111],v[40:43]
ds_read_b64_tr_b16 a[218:219],v3 offset:4352
v_mfma_f32_16x16x32_bf16 v[204:207],a[152:155],v[104:107],v[204:207]
buffer_load_dwordx4 v5,s[24:27],s47 offen offset:3072 nt lds
v_mfma_f32_16x16x32_bf16 v[204:207],a[148:151],v[100:103],v[204:207]
s_or_b32 s47,s47,0x18000
ds_read_b64_tr_b16 a[224:225],v2 offset:512
v_cvt_pk_bf16_f32 v140,v152,v153
v_mfma_f32_16x16x32_bf16 v[204:207],a[144:147],v[96:99],v[204:207]
s_addk_i32 s47,4096
s_and_b32 s47,s47,0x27000
ds_read_b64_tr_b16 a[226:227],v2 offset:4608
v_mfma_f32_16x16x32_bf16 v[216:219],a[160:163],v[96:99],v[40:43]
s_or_b32 s47,s47,s46
ds_read_b64_tr_b16 a[228:229],v2 offset:8704
v_cvt_pk_bf16_f32 v141,v154,v155
v_mfma_f32_16x16x32_bf16 v[216:219],a[164:167],v[100:103],v[216:219]
ds_read_b64_tr_b16 a[230:231],v2 offset:12800
v_mfma_f32_16x16x32_bf16 v[216:219],a[168:171],v[104:107],v[216:219]
ds_read_b64_tr_b16 a[236:237],v3 offset:8704
v_mfma_f32_16x16x32_bf16 v[216:219],a[172:175],v[108:111],v[216:219]
ds_read_b64_tr_b16 a[238:239],v3 offset:12800
v_cvt_pk_bf16_f32 v142,v156,v157
v_mfma_f32_16x16x32_bf16 v[220:223],a[188:191],v[108:111],v[40:43]
ds_read_b64_tr_b16 a[232:233],v3 offset:512
v_mfma_f32_16x16x32_bf16 v[220:223],a[184:187],v[104:107],v[220:223]
ds_read_b64_tr_b16 a[234:235],v3 offset:4608
v_mfma_f32_16x16x32_bf16 v[220:223],a[180:183],v[100:103],v[220:223]
ds_read_b64_tr_b16 a[240:241],v2 offset:768
v_cvt_pk_bf16_f32 v143,v158,v159
v_mfma_f32_16x16x32_bf16 v[220:223],a[176:179],v[96:99],v[220:223]
s_waitcnt lgkmcnt(7)
buffer_load_dword v26,v24,s[88:91],s93 offen
v_mfma_f32_16x16x32_bf16 a[0:3],a[192:195],v[136:139],a[0:3]
ds_read_b64_tr_b16 a[242:243],v2 offset:4864
v_mfma_f32_16x16x32_bf16 a[0:3],a[196:199],v[140:143],a[0:3]
ds_read_b64_tr_b16 a[244:245],v2 offset:8960
v_mfma_f32_16x16x32_bf16 a[4:7],a[204:207],v[140:143],a[4:7]
ds_read_b64_tr_b16 a[246:247],v2 offset:13056
v_mfma_f32_16x16x32_bf16 a[4:7],a[200:203],v[136:139],a[4:7]
s_memrealtime s[76:77]
ds_read_b64_tr_b16 a[252:253],v3 offset:8960
v_mfma_f32_16x16x32_bf16 a[8:11],a[208:211],v[136:139],a[8:11]
ds_read_b64_tr_b16 a[254:255],v3 offset:13056
v_mfma_f32_16x16x32_bf16 a[8:11],a[212:215],v[140:143],a[8:11]
ds_read_b64_tr_b16 a[248:249],v3 offset:768
v_mfma_f32_16x16x32_bf16 a[12:15],a[220:223],v[140:143],a[12:15]
ds_read_b64_tr_b16 a[250:251],v3 offset:4864
)A"
R"A(v_mfma_f32_16x16x32_bf16 a[12:15],a[216:219],v[136:139],a[12:15]
s_bitcmp1_b32 s45,0
s_cbranch_scc0 L_t310_%=
s_mov_b32 m0,s80
s_addk_i32 s80,4096
buffer_load_dwordx4 v25,s[20:23],s83 offen nt lds
s_addk_i32 s83,1024
L_t310_%=:
v_mfma_f32_16x16x32_bf16 a[16:19],a[224:227],v[136:139],a[16:19]
s_waitcnt lgkmcnt(6)
v_exp_f32 v187,v187
v_mfma_f32_16x16x32_bf16 a[16:19],a[228:231],v[140:143],a[16:19]
v_exp_f32 v188,v188
v_mfma_f32_16x16x32_bf16 a[20:23],a[236:239],v[140:143],a[20:23]
s_waitcnt lgkmcnt(0)
v_exp_f32 v189,v189
v_mfma_f32_16x16x32_bf16 a[20:23],a[232:235],v[136:139],a[20:23]
s_sub_u32 s79,s78,s76
v_exp_f32 v190,v190
v_mfma_f32_16x16x32_bf16 a[24:27],a[240:243],v[136:139],a[24:27]
s_add_u32 s78,s76,s60
v_exp_f32 v191,v191
v_mfma_f32_16x16x32_bf16 a[24:27],a[244:247],v[140:143],a[24:27]
s_cmp_gt_i32 s79,s95
s_cbranch_scc1 L_ps3_%=
L_pr3_%=:
v_cvt_pk_bf16_f32 v173,v186,v187
v_mfma_f32_16x16x32_bf16 a[28:31],a[252:255],v[140:143],a[28:31]
v_cvt_pk_bf16_f32 v174,v188,v189
v_cvt_pk_bf16_f32 v175,v190,v191
v_mfma_f32_16x16x32_bf16 a[28:31],a[248:251],v[136:139],a[28:31]
s_cmp_lg_u32 s53,0
v_smfmac_f32_16x16x64_bf16 v[48:51],v[28:31],v[136:143],v27
s_cbranch_scc1 L_att0_%=
L_att0x_%=:
v_mfma_f32_16x16x32_bf16 v[232:235],a[128:131],v[112:115],v[44:47]
s_waitcnt vmcnt(10)
s_sub_u32 s94,s94,3
v_exp_f32 v200,v200
v_mfma_f32_16x16x32_bf16 v[232:235],a[132:135],v[116:119],v[232:235]
s_barrier
ds_read_b128 a[128:131],v1
v_and_b32 v18,48,v6
v_mfma_f32_16x16x32_bf16 v[232:235],a[136:139],v[120:123],v[232:235]
s_lshr_b32 s60,s94,2
v_exp_f32 v201,v201
v_mfma_f32_16x16x32_bf16 v[232:235],a[140:143],v[124:127],v[232:235]
s_add_u32 m0,0,s51
ds_read_b128 a[132:135],v1 offset:256
v_cvt_pk_bf16_f32 v200,v200,v201
v_mfma_f32_16x16x32_bf16 v[236:239],a[156:159],v[124:127],v[44:47]
s_max_i32 s60,s60,124
v_exp_f32 v202,v202
v_mfma_f32_16x16x32_bf16 v[236:239],a[152:155],v[120:123],v[236:239]
s_addk_i32 s54,16384
ds_read_b128 a[136:139],v1 offset:512
v_lshrrev_b32 v19,4,v6
v_mfma_f32_16x16x32_bf16 v[236:239],a[148:151],v[116:119],v[236:239]
s_and_b32 s46,s54,0xfffc0000
buffer_load_dwordx4 v4,s[20:23],s47 offen nt lds
v_mfma_f32_16x16x32_bf16 v[236:239],a[144:147],v[112:115],v[236:239]
s_sub_u32 s94,s94,3
ds_read_b128 a[140:143],v1 offset:768
v_lshlrev_b32 v18,2,v18
v_mfma_f32_16x16x32_bf16 v[248:251],a[160:163],v[112:115],v[44:47]
s_addk_i32 s54,16384
v_exp_f32 v203,v203
v_mfma_f32_16x16x32_bf16 v[248:251],a[164:167],v[116:119],v[248:251]
s_mul_i32 s83,s41,5
ds_read_b128 a[156:159],v1 offset:4864
v_cvt_pk_bf16_f32 v201,v202,v203
v_mfma_f32_16x16x32_bf16 v[248:251],a[168:171],v[120:123],v[248:251]
s_add_u32 s83,s83,s42
v_exp_f32 v204,v204
v_mfma_f32_16x16x32_bf16 v[248:251],a[172:175],v[124:127],v[248:251]
s_add_u32 s83,s83,15
ds_read_b128 a[152:155],v1 offset:4608
v_and_b32 v19,48,v19
v_mfma_f32_16x16x32_bf16 v[252:255],a[188:191],v[124:127],v[44:47]
s_lshl_b32 s43,s83,14
v_exp_f32 v205,v205
v_mfma_f32_16x16x32_bf16 v[252:255],a[184:187],v[120:123],v[252:255]
s_add_u32 s84,s55,0
ds_read_b128 a[148:151],v1 offset:4352
v_cvt_pk_bf16_f32 v202,v204,v205
v_mfma_f32_16x16x32_bf16 v[252:255],a[180:183],v[116:119],v[252:255]
s_add_u32 s85,s55,4096
v_exp_f32 v206,v206
v_mfma_f32_16x16x32_bf16 v[252:255],a[176:179],v[112:115],v[252:255]
s_add_u32 s86,s55,16384
ds_read_b128 a[144:147],v1 offset:4096
v_add_u32 v18,v18,v19
v_mfma_f32_16x16x32_bf16 a[32:35],a[192:195],v[168:171],a[32:35]
s_add_u32 s87,s55,20480
v_exp_f32 v207,v207
v_mfma_f32_16x16x32_bf16 a[32:35],a[196:199],v[172:175],a[32:35]
s_lshl_b32 s83,s83,12
ds_read_b128 a[160:163],v1 offset:8192
v_cvt_pk_bf16_f32 v203,v206,v207
v_mfma_f32_16x16x32_bf16 a[36:39],a[204:207],v[172:175],a[36:39]
s_max_i32 s84,s84,s18
v_exp_f32 v216,v216
v_mfma_f32_16x16x32_bf16 a[36:39],a[200:203],v[168:171],a[36:39]
s_max_i32 s85,s85,s19
ds_read_b128 a[164:167],v1 offset:8448
v_add_u32 v18,98304,v18
v_mfma_f32_16x16x32_bf16 a[40:43],a[208:211],v[168:171],a[40:43]
s_max_i32 s86,s86,s100
buffer_load_dwordx4 v4,s[20:23],s47 offen offset:1024 nt lds
v_mfma_f32_16x16x32_bf16 a[40:43],a[212:215],v[172:175],a[40:43]
s_max_i32 s87,s87,s101
ds_read_b128 a[168:171],v1 offset:8704
v_add_u32 v2,s48,v2
v_mfma_f32_16x16x32_bf16 a[44:47],a[220:223],v[172:175],a[44:47]
s_lshl_b32 s44,s40,5
v_exp_f32 v217,v217
v_mfma_f32_16x16x32_bf16 a[44:47],a[216:219],v[168:171],a[44:47]
s_and_b32 s43,s43,0x20000
ds_read_b128 a[172:175],v1 offset:8960
v_cvt_pk_bf16_f32 v204,v216,v217
v_mfma_f32_16x16x32_bf16 a[48:51],a[224:227],v[168:171],a[48:51]
s_and_b32 s83,s83,0x7000
v_exp_f32 v218,v218
v_mfma_f32_16x16x32_bf16 a[48:51],a[228:231],v[172:175],a[48:51]
s_lshl_b32 s80,s40,10
ds_read_b128 a[188:191],v1 offset:13056
v_add_u32 v3,s48,v3
v_mfma_f32_16x16x32_bf16 a[52:55],a[236:239],v[172:175],a[52:55]
s_mov_b32 s48,32768
v_exp_f32 v219,v219
v_mfma_f32_16x16x32_bf16 a[52:55],a[232:235],v[168:171],a[52:55]
s_and_b32 s44,s44,32
ds_read_b128 a[184:187],v1 offset:12800
v_cvt_pk_bf16_f32 v205,v218,v219
v_mfma_f32_16x16x32_bf16 a[56:59],a[240:243],v[168:171],a[56:59]
s_or_b32 s83,s83,s43
v_exp_f32 v220,v220
v_mfma_f32_16x16x32_bf16 a[56:59],a[244:247],v[172:175],a[56:59]
s_mov_b32 s50,32768
s_mov_b32 s49,98304
ds_read_b128 a[180:183],v1 offset:12544
v_mfma_f32_16x16x32_bf16 a[60:63],a[252:255],v[172:175],a[60:63]
s_mov_b32 s45,0x2a8
v_exp_f32 v221,v221
v_mfma_f32_16x16x32_bf16 a[60:63],a[248:251],v[168:171],a[60:63]
s_add_u32 s80,s80,147456
ds_read_b128 a[176:179],v1 offset:12288
v_cvt_pk_bf16_f32 v206,v220,v221
v_smfmac_f32_16x16x64_bf16 v[52:55],v[28:31],v[168:175],v27
s_or_b32 s83,s83,0x40000
v_add_u32 v1,s48,v1
s_waitcnt lgkmcnt(4)
v_mfma_f32_16x16x32_bf16 v[136:139],a[128:131],v[64:67],v[32:35]
s_add_u32 s93,s92,L_endseg_%=-L_pc_%=
v_exp_f32 v222,v222
v_mfma_f32_16x16x32_bf16 v[136:139],a[132:135],v[68:71],v[136:139]
buffer_load_dwordx4 v5,s[20:23],s47 offen offset:2048 nt lds
v_mfma_f32_16x16x32_bf16 v[136:139],a[136:139],v[72:75],v[136:139]
v_exp_f32 v223,v223
v_mfma_f32_16x16x32_bf16 v[136:139],a[140:143],v[76:79],v[136:139]
v_cvt_pk_bf16_f32 v207,v222,v223
v_xor_b32 v25,s44,v4
v_mfma_f32_16x16x32_bf16 v[140:143],a[156:159],v[76:79],v[32:35]
v_exp_f32 v232,v232
v_mfma_f32_16x16x32_bf16 v[140:143],a[152:155],v[72:75],v[140:143]
v_exp_f32 v233,v233
v_mfma_f32_16x16x32_bf16 v[140:143],a[148:151],v[68:71],v[140:143]
v_exp_f32 v234,v234
v_mfma_f32_16x16x32_bf16 v[140:143],a[144:147],v[64:67],v[140:143]
v_exp_f32 v235,v235
v_mfma_f32_16x16x32_bf16 v[152:155],a[160:163],v[64:67],v[32:35]
v_exp_f32 v236,v236
v_mfma_f32_16x16x32_bf16 v[152:155],a[164:167],v[68:71],v[152:155]
v_exp_f32 v237,v237
v_mfma_f32_16x16x32_bf16 v[152:155],a[168:171],v[72:75],v[152:155]
v_exp_f32 v238,v238
v_mfma_f32_16x16x32_bf16 v[152:155],a[172:175],v[76:79],v[152:155]
s_waitcnt lgkmcnt(0)
v_exp_f32 v239,v239
v_mfma_f32_16x16x32_bf16 v[156:159],a[188:191],v[76:79],v[32:35]
v_exp_f32 v248,v248
v_mfma_f32_16x16x32_bf16 v[156:159],a[184:187],v[72:75],v[156:159]
v_exp_f32 v249,v249
v_mfma_f32_16x16x32_bf16 v[156:159],a[180:183],v[68:71],v[156:159]
v_exp_f32 v250,v250
v_mfma_f32_16x16x32_bf16 v[156:159],a[176:179],v[64:67],v[156:159]
buffer_load_dwordx4 v5,s[20:23],s47 offen offset:3072 nt lds
v_mfma_f32_16x16x32_bf16 a[64:67],a[192:195],v[200:203],a[64:67]
v_exp_f32 v251,v251
v_mfma_f32_16x16x32_bf16 a[64:67],a[196:199],v[204:207],a[64:67]
v_exp_f32 v252,v252
v_mfma_f32_16x16x32_bf16 a[68:71],a[204:207],v[204:207],a[68:71]
v_exp_f32 v253,v253
v_mfma_f32_16x16x32_bf16 a[68:71],a[200:203],v[200:203],a[68:71]
v_exp_f32 v254,v254
v_mfma_f32_16x16x32_bf16 a[72:75],a[208:211],v[200:203],a[72:75]
v_exp_f32 v255,v255
v_mfma_f32_16x16x32_bf16 a[72:75],a[212:215],v[204:207],a[72:75]
v_exp_f32 v136,v136
v_mfma_f32_16x16x32_bf16 a[76:79],a[220:223],v[204:207],a[76:79]
v_exp_f32 v137,v137
v_mfma_f32_16x16x32_bf16 a[76:79],a[216:219],v[200:203],a[76:79]
v_cvt_pk_bf16_f32 v232,v232,v233
v_cvt_pk_bf16_f32 v233,v234,v235
v_mfma_f32_16x16x32_bf16 a[80:83],a[224:227],v[200:203],a[80:83]
v_cvt_pk_bf16_f32 v234,v236,v237
v_cvt_pk_bf16_f32 v235,v238,v239
v_mfma_f32_16x16x32_bf16 a[80:83],a[228:231],v[204:207],a[80:83]
v_cvt_pk_bf16_f32 v236,v248,v249
v_cvt_pk_bf16_f32 v237,v250,v251
v_mfma_f32_16x16x32_bf16 a[84:87],a[236:239],v[204:207],a[84:87]
s_addk_i32 m0,16384
v_cvt_pk_bf16_f32 v238,v252,v253
v_cvt_pk_bf16_f32 v239,v254,v255
v_mfma_f32_16x16x32_bf16 a[84:87],a[232:235],v[200:203],a[84:87]
v_exp_f32 v138,v138
v_mfma_f32_16x16x32_bf16 a[88:91],a[240:243],v[200:203],a[88:91]
v_exp_f32 v139,v139
v_mfma_f32_16x16x32_bf16 a[88:91],a[244:247],v[204:207],a[88:91]
buffer_load_dwordx4 v4,s[24:27],s47 offen nt lds
v_mfma_f32_16x16x32_bf16 a[92:95],a[252:255],v[204:207],a[92:95]
v_exp_f32 v140,v140
v_mfma_f32_16x16x32_bf16 a[92:95],a[248:251],v[200:203],a[92:95]
v_exp_f32 v141,v141
v_smfmac_f32_16x16x64_bf16 v[56:59],v[28:31],v[200:207],v27
v_cvt_pk_bf16_f32 v136,v136,v137
v_mfma_f32_16x16x32_bf16 v[168:171],a[128:131],v[80:83],v[36:39]
v_exp_f32 v142,v142
v_mfma_f32_16x16x32_bf16 v[168:171],a[132:135],v[84:87],v[168:171]
v_exp_f32 v143,v143
v_mfma_f32_16x16x32_bf16 v[168:171],a[136:139],v[88:91],v[168:171]
v_exp_f32 v152,v152
v_mfma_f32_16x16x32_bf16 v[168:171],a[140:143],v[92:95],v[168:171]
v_exp_f32 v153,v153
v_mfma_f32_16x16x32_bf16 v[172:175],a[156:159],v[92:95],v[36:39]
v_exp_f32 v154,v154
v_mfma_f32_16x16x32_bf16 v[172:175],a[152:155],v[88:91],v[172:175]
v_exp_f32 v155,v155
v_mfma_f32_16x16x32_bf16 v[172:175],a[148:151],v[84:87],v[172:175]
v_exp_f32 v156,v156
v_mfma_f32_16x16x32_bf16 v[172:175],a[144:147],v[80:83],v[172:175]
v_exp_f32 v157,v157
v_mfma_f32_16x16x32_bf16 v[184:187],a[160:163],v[80:83],v[36:39]
v_exp_f32 v158,v158
v_mfma_f32_16x16x32_bf16 v[184:187],a[164:167],v[84:87],v[184:187]
v_exp_f32 v159,v159
v_mfma_f32_16x16x32_bf16 v[184:187],a[168:171],v[88:91],v[184:187]
buffer_load_dwordx4 v5,s[24:27],s47 offen offset:1024 nt lds
v_mfma_f32_16x16x32_bf16 v[184:187],a[172:175],v[92:95],v[184:187]
v_exp_f32 v168,v168
v_mfma_f32_16x16x32_bf16 v[188:191],a[188:191],v[92:95],v[36:39]
v_exp_f32 v169,v169
v_mfma_f32_16x16x32_bf16 v[188:191],a[184:187],v[88:91],v[188:191]
v_cvt_pk_bf16_f32 v137,v138,v139
v_cvt_pk_bf16_f32 v138,v140,v141
v_mfma_f32_16x16x32_bf16 v[188:191],a[180:183],v[84:87],v[188:191]
s_waitcnt vmcnt(14)
v_cvt_pk_bf16_f32 v139,v142,v143
v_cvt_pk_bf16_f32 v140,v152,v153
v_mfma_f32_16x16x32_bf16 v[188:191],a[176:179],v[80:83],v[188:191]
s_barrier
ds_read_b128 v[80:83],v18
v_cvt_pk_bf16_f32 v141,v154,v155
v_mfma_f32_16x16x32_bf16 a[96:99],a[192:195],v[232:235],a[96:99]
ds_read_b64_tr_b16 a[192:193],v2
v_cvt_pk_bf16_f32 v142,v156,v157
v_mfma_f32_16x16x32_bf16 a[96:99],a[196:199],v[236:239],a[96:99]
ds_read_b64_tr_b16 a[194:195],v2 offset:4096
v_cvt_pk_bf16_f32 v143,v158,v159
v_mfma_f32_16x16x32_bf16 a[100:103],a[204:207],v[236:239],a[100:103]
ds_read_b64_tr_b16 a[196:197],v2 offset:8192
v_cvt_pk_bf16_f32 v168,v168,v169
v_mfma_f32_16x16x32_bf16 a[100:103],a[200:203],v[232:235],a[100:103]
v_exp_f32 v170,v170
v_mfma_f32_16x16x32_bf16 a[104:107],a[208:211],v[232:235],a[104:107]
v_exp_f32 v171,v171
v_mfma_f32_16x16x32_bf16 a[104:107],a[212:215],v[236:239],a[104:107]
ds_read_b128 v[84:87],v18 offset:256
v_cvt_pk_bf16_f32 v169,v170,v171
v_mfma_f32_16x16x32_bf16 a[108:111],a[220:223],v[236:239],a[108:111]
v_exp_f32 v172,v172
v_mfma_f32_16x16x32_bf16 a[108:111],a[216:219],v[232:235],a[108:111]
buffer_load_dwordx4 v4,s[24:27],s47 offen offset:2048 nt lds
v_mfma_f32_16x16x32_bf16 a[112:115],a[224:227],v[232:235],a[112:115]
v_exp_f32 v173,v173
v_mfma_f32_16x16x32_bf16 a[112:115],a[228:231],v[236:239],a[112:115]
ds_read_b128 v[88:91],v18 offset:512
v_cvt_pk_bf16_f32 v170,v172,v173
v_mfma_f32_16x16x32_bf16 a[116:119],a[236:239],v[236:239],a[116:119]
v_exp_f32 v174,v174
v_mfma_f32_16x16x32_bf16 a[116:119],a[232:235],v[232:235],a[116:119]
ds_read_b128 v[92:95],v18 offset:768
v_mfma_f32_16x16x32_bf16 a[120:123],a[240:243],v[232:235],a[120:123]
v_exp_f32 v175,v175
v_mfma_f32_16x16x32_bf16 a[120:123],a[244:247],v[236:239],a[120:123]
ds_read_b64_tr_b16 a[198:199],v2 offset:12288
)A"
R"A(ds_read_b64_tr_b16 a[204:205],v3 offset:8192
v_mfma_f32_16x16x32_bf16 a[124:127],a[252:255],v[236:239],a[124:127]
ds_read_b64_tr_b16 a[206:207],v3 offset:12288
v_cvt_pk_bf16_f32 v171,v174,v175
v_mfma_f32_16x16x32_bf16 a[124:127],a[248:251],v[232:235],a[124:127]
v_exp_f32 v184,v184
v_smfmac_f32_16x16x64_bf16 v[60:63],v[28:31],v[232:239],v27
ds_read_b64_tr_b16 a[200:201],v3
v_mfma_f32_16x16x32_bf16 v[200:203],a[128:131],v[96:99],v[40:43]
ds_read_b64_tr_b16 a[202:203],v3 offset:4096
ds_read_b64_tr_b16 a[208:209],v2 offset:256
v_mfma_f32_16x16x32_bf16 v[200:203],a[132:135],v[100:103],v[200:203]
v_exp_f32 v185,v185
v_mfma_f32_16x16x32_bf16 v[200:203],a[136:139],v[104:107],v[200:203]
ds_read_b64_tr_b16 a[210:211],v2 offset:4352
ds_read_b64_tr_b16 a[212:213],v2 offset:8448
v_mfma_f32_16x16x32_bf16 v[200:203],a[140:143],v[108:111],v[200:203]
ds_read_b64_tr_b16 a[214:215],v2 offset:12544
v_cvt_pk_bf16_f32 v172,v184,v185
v_mfma_f32_16x16x32_bf16 v[204:207],a[156:159],v[108:111],v[40:43]
buffer_load_dwordx4 v5,s[24:27],s47 offen offset:3072 nt lds
v_mfma_f32_16x16x32_bf16 v[204:207],a[152:155],v[104:107],v[204:207]
s_or_b32 s47,s47,0x18000
ds_read_b64_tr_b16 a[220:221],v3 offset:8448
ds_read_b64_tr_b16 a[222:223],v3 offset:12544
v_mfma_f32_16x16x32_bf16 v[204:207],a[148:151],v[100:103],v[204:207]
s_addk_i32 s47,4096
v_exp_f32 v186,v186
v_mfma_f32_16x16x32_bf16 v[204:207],a[144:147],v[96:99],v[204:207]
s_and_b32 s47,s47,0x27000
ds_read_b64_tr_b16 a[216:217],v3 offset:256
ds_read_b64_tr_b16 a[218:219],v3 offset:4352
v_mfma_f32_16x16x32_bf16 v[216:219],a[160:163],v[96:99],v[40:43]
s_or_b32 s47,s47,s46
v_exp_f32 v187,v187
v_mfma_f32_16x16x32_bf16 v[216:219],a[164:167],v[100:103],v[216:219]
s_and_b32 s46,s54,0xfffc0000
ds_read_b64_tr_b16 a[224:225],v2 offset:512
ds_read_b64_tr_b16 a[226:227],v2 offset:4608
v_mfma_f32_16x16x32_bf16 v[216:219],a[168:171],v[104:107],v[216:219]
s_addk_i32 s54,16384
ds_read_b64_tr_b16 a[228:229],v2 offset:8704
v_cvt_pk_bf16_f32 v173,v186,v187
v_mfma_f32_16x16x32_bf16 v[216:219],a[172:175],v[108:111],v[216:219]
ds_read_b64_tr_b16 a[230:231],v2 offset:12800
v_mfma_f32_16x16x32_bf16 v[220:223],a[188:191],v[108:111],v[40:43]
ds_read_b64_tr_b16 a[236:237],v3 offset:8704
v_mfma_f32_16x16x32_bf16 v[220:223],a[184:187],v[104:107],v[220:223]
ds_read_b64_tr_b16 a[238:239],v3 offset:12800
ds_read_b64_tr_b16 a[232:233],v3 offset:512
v_mfma_f32_16x16x32_bf16 v[220:223],a[180:183],v[100:103],v[220:223]
ds_read_b64_tr_b16 a[234:235],v3 offset:4608
v_mfma_f32_16x16x32_bf16 v[220:223],a[176:179],v[96:99],v[220:223]
s_waitcnt lgkmcnt(8)
ds_read_b64_tr_b16 a[240:241],v2 offset:768
v_mfma_f32_16x16x32_bf16 v[32:35],v[80:83],v[128:131],0
ds_read_b64_tr_b16 a[242:243],v2 offset:4864
v_mfma_f32_16x16x32_bf16 v[32:35],v[84:87],v[160:163],v[32:35]
ds_read_b64_tr_b16 a[244:245],v2 offset:8960
ds_read_b64_tr_b16 a[246:247],v2 offset:13056
v_mfma_f32_16x16x32_bf16 v[32:35],v[88:91],v[192:195],v[32:35]
ds_read_b64_tr_b16 a[252:253],v3 offset:8960
v_add_u32 v2,s48,v2
v_mfma_f32_16x16x32_bf16 v[32:35],v[92:95],v[224:227],v[32:35]
ds_read_b64_tr_b16 a[254:255],v3 offset:13056
v_mfma_f32_16x16x32_bf16 v[36:39],v[80:83],v[132:135],0
ds_read_b64_tr_b16 a[248:249],v3 offset:768
v_mfma_f32_16x16x32_bf16 v[36:39],v[84:87],v[164:167],v[36:39]
ds_read_b64_tr_b16 a[250:251],v3 offset:4864
v_add_u32 v3,s48,v3
v_mfma_f32_16x16x32_bf16 v[36:39],v[88:91],v[196:199],v[36:39]
s_mov_b32 s48,-98304
v_exp_f32 v188,v188
v_mfma_f32_16x16x32_bf16 v[36:39],v[92:95],v[228:231],v[36:39]
v_exp_f32 v189,v189
v_mfma_f32_16x16x32_bf16 v[40:43],v[80:83],v[144:147],0
v_exp_f32 v190,v190
v_mfma_f32_16x16x32_bf16 v[40:43],v[84:87],v[176:179],v[40:43]
v_exp_f32 v191,v191
v_mfma_f32_16x16x32_bf16 v[40:43],v[88:91],v[208:211],v[40:43]
v_cvt_pk_bf16_f32 v174,v188,v189
v_cvt_pk_bf16_f32 v175,v190,v191
v_mfma_f32_16x16x32_bf16 v[40:43],v[92:95],v[240:243],v[40:43]
v_exp_f32 v200,v200
v_mfma_f32_16x16x32_bf16 a[0:3],a[192:195],v[136:139],a[0:3]
v_exp_f32 v201,v201
v_mfma_f32_16x16x32_bf16 a[0:3],a[196:199],v[140:143],a[0:3]
v_max3_f32 v32,v32,v33,v34
v_max3_f32 v32,v32,v35,s18
v_mfma_f32_16x16x32_bf16 a[4:7],a[204:207],v[140:143],a[4:7]
v_sub_f32 v35,0,v32
v_sub_f32 v34,0,v32
v_mfma_f32_16x16x32_bf16 a[4:7],a[200:203],v[136:139],a[4:7]
s_memrealtime s[76:77]
v_sub_f32 v33,0,v32
v_sub_f32 v32,0,v32
v_mfma_f32_16x16x32_bf16 a[8:11],a[208:211],v[136:139],a[8:11]
v_exp_f32 v202,v202
v_mfma_f32_16x16x32_bf16 a[8:11],a[212:215],v[140:143],a[8:11]
v_exp_f32 v203,v203
v_mfma_f32_16x16x32_bf16 a[12:15],a[220:223],v[140:143],a[12:15]
v_cvt_pk_bf16_f32 v200,v200,v201
v_cvt_pk_bf16_f32 v201,v202,v203
v_mfma_f32_16x16x32_bf16 a[12:15],a[216:219],v[136:139],a[12:15]
s_waitcnt lgkmcnt(0)
v_exp_f32 v204,v204
v_mfma_f32_16x16x32_bf16 a[16:19],a[224:227],v[136:139],a[16:19]
v_exp_f32 v205,v205
v_mfma_f32_16x16x32_bf16 a[16:19],a[228:231],v[140:143],a[16:19]
v_exp_f32 v206,v206
v_mfma_f32_16x16x32_bf16 a[20:23],a[236:239],v[140:143],a[20:23]
s_waitcnt lgkmcnt(0)
v_exp_f32 v207,v207
v_mfma_f32_16x16x32_bf16 a[20:23],a[232:235],v[136:139],a[20:23]
s_sub_u32 s79,s78,s76
v_cvt_pk_bf16_f32 v202,v204,v205
v_cvt_pk_bf16_f32 v203,v206,v207
v_mfma_f32_16x16x32_bf16 a[24:27],a[240:243],v[136:139],a[24:27]
s_add_u32 s78,s76,s60
v_exp_f32 v216,v216
v_mfma_f32_16x16x32_bf16 a[24:27],a[244:247],v[140:143],a[24:27]
s_cmp_gt_i32 s79,s95
s_cbranch_scc1 L_ps4_%=
L_pr4_%=:
v_max3_f32 v36,v36,v37,v38
v_mfma_f32_16x16x32_bf16 a[28:31],a[252:255],v[140:143],a[28:31]
s_lshr_b32 s60,s94,2
v_exp_f32 v217,v217
v_mfma_f32_16x16x32_bf16 a[28:31],a[248:251],v[136:139],a[28:31]
s_max_i32 s60,s60,124
v_exp_f32 v218,v218
v_smfmac_f32_16x16x64_bf16 v[48:51],v[28:31],v[136:143],v27
s_sub_u32 s94,s94,3
v_cvt_pk_bf16_f32 v204,v216,v217
v_mfma_f32_16x16x32_bf16 v[232:235],a[128:131],v[112:115],v[44:47]
s_waitcnt vmcnt(9)
v_exp_f32 v219,v219
v_mfma_f32_16x16x32_bf16 v[232:235],a[132:135],v[116:119],v[232:235]
s_barrier
ds_read_b128 a[128:131],v1
v_cvt_pk_bf16_f32 v205,v218,v219
v_mfma_f32_16x16x32_bf16 v[232:235],a[136:139],v[120:123],v[232:235]
v_exp_f32 v220,v220
v_mfma_f32_16x16x32_bf16 v[232:235],a[140:143],v[124:127],v[232:235]
s_add_u32 m0,32768,s51
ds_read_b128 a[132:135],v1 offset:256
v_max3_f32 v36,v36,v39,s19
v_mfma_f32_16x16x32_bf16 v[236:239],a[156:159],v[124:127],v[44:47]
v_exp_f32 v221,v221
v_mfma_f32_16x16x32_bf16 v[236:239],a[152:155],v[120:123],v[236:239]
ds_read_b128 a[136:139],v1 offset:512
v_cvt_pk_bf16_f32 v206,v220,v221
v_mfma_f32_16x16x32_bf16 v[236:239],a[148:151],v[116:119],v[236:239]
buffer_load_dwordx4 v4,s[20:23],s47 offen nt lds
v_mfma_f32_16x16x32_bf16 v[236:239],a[144:147],v[112:115],v[236:239]
ds_read_b128 a[140:143],v1 offset:768
v_sub_f32 v39,0,v36
v_mfma_f32_16x16x32_bf16 v[248:251],a[160:163],v[112:115],v[44:47]
v_exp_f32 v222,v222
v_mfma_f32_16x16x32_bf16 v[248:251],a[164:167],v[116:119],v[248:251]
ds_read_b128 a[156:159],v1 offset:4864
v_sub_f32 v38,0,v36
v_mfma_f32_16x16x32_bf16 v[248:251],a[168:171],v[120:123],v[248:251]
v_exp_f32 v223,v223
v_mfma_f32_16x16x32_bf16 v[248:251],a[172:175],v[124:127],v[248:251]
ds_read_b128 a[152:155],v1 offset:4608
v_cvt_pk_bf16_f32 v207,v222,v223
v_mfma_f32_16x16x32_bf16 v[252:255],a[188:191],v[124:127],v[44:47]
v_sub_f32 v37,0,v36
v_sub_f32 v36,0,v36
v_mfma_f32_16x16x32_bf16 v[252:255],a[184:187],v[120:123],v[252:255]
ds_read_b128 a[148:151],v1 offset:4352
v_max3_f32 v40,v40,v41,v42
v_mfma_f32_16x16x32_bf16 v[252:255],a[180:183],v[116:119],v[252:255]
v_exp_f32 v232,v232
v_mfma_f32_16x16x32_bf16 v[252:255],a[176:179],v[112:115],v[252:255]
ds_read_b128 a[144:147],v1 offset:4096
v_max3_f32 v40,v40,v43,s100
v_mfma_f32_16x16x32_bf16 v[44:47],v[80:83],v[148:151],0
v_exp_f32 v233,v233
v_mfma_f32_16x16x32_bf16 v[44:47],v[84:87],v[180:183],v[44:47]
ds_read_b128 a[160:163],v1 offset:8192
v_cvt_pk_bf16_f32 v232,v232,v233
v_mfma_f32_16x16x32_bf16 v[44:47],v[88:91],v[212:215],v[44:47]
v_exp_f32 v234,v234
v_mfma_f32_16x16x32_bf16 v[44:47],v[92:95],v[244:247],v[44:47]
ds_read_b128 a[164:167],v1 offset:8448
v_sub_f32 v43,0,v40
v_mfma_f32_16x16x32_bf16 a[32:35],a[192:195],v[168:171],a[32:35]
buffer_load_dwordx4 v4,s[20:23],s47 offen offset:1024 nt lds
v_mfma_f32_16x16x32_bf16 a[32:35],a[196:199],v[172:175],a[32:35]
ds_read_b128 a[168:171],v1 offset:8704
v_sub_f32 v42,0,v40
v_mfma_f32_16x16x32_bf16 a[36:39],a[204:207],v[172:175],a[36:39]
v_exp_f32 v235,v235
v_mfma_f32_16x16x32_bf16 a[36:39],a[200:203],v[168:171],a[36:39]
ds_read_b128 a[172:175],v1 offset:8960
v_cvt_pk_bf16_f32 v233,v234,v235
v_mfma_f32_16x16x32_bf16 a[40:43],a[208:211],v[168:171],a[40:43]
v_exp_f32 v236,v236
v_mfma_f32_16x16x32_bf16 a[40:43],a[212:215],v[172:175],a[40:43]
ds_read_b128 a[188:191],v1 offset:13056
v_sub_f32 v41,0,v40
v_mfma_f32_16x16x32_bf16 a[44:47],a[220:223],v[172:175],a[44:47]
v_exp_f32 v237,v237
v_mfma_f32_16x16x32_bf16 a[44:47],a[216:219],v[168:171],a[44:47]
ds_read_b128 a[184:187],v1 offset:12800
v_cvt_pk_bf16_f32 v234,v236,v237
v_mfma_f32_16x16x32_bf16 a[48:51],a[224:227],v[168:171],a[48:51]
v_exp_f32 v238,v238
v_mfma_f32_16x16x32_bf16 a[48:51],a[228:231],v[172:175],a[48:51]
ds_read_b128 a[180:183],v1 offset:12544
v_sub_f32 v40,0,v40
v_mfma_f32_16x16x32_bf16 a[52:55],a[236:239],v[172:175],a[52:55]
v_exp_f32 v239,v239
v_mfma_f32_16x16x32_bf16 a[52:55],a[232:235],v[168:171],a[52:55]
ds_read_b128 a[176:179],v1 offset:12288
v_cvt_pk_bf16_f32 v235,v238,v239
v_mfma_f32_16x16x32_bf16 a[56:59],a[240:243],v[168:171],a[56:59]
v_exp_f32 v248,v248
v_mfma_f32_16x16x32_bf16 a[56:59],a[244:247],v[172:175],a[56:59]
v_exp_f32 v249,v249
v_mfma_f32_16x16x32_bf16 a[60:63],a[252:255],v[172:175],a[60:63]
buffer_load_dwordx4 v5,s[20:23],s47 offen offset:2048 nt lds
v_mfma_f32_16x16x32_bf16 a[60:63],a[248:251],v[168:171],a[60:63]
v_exp_f32 v250,v250
v_smfmac_f32_16x16x64_bf16 v[52:55],v[28:31],v[168:175],v27
s_waitcnt lgkmcnt(2)
v_cvt_pk_bf16_f32 v236,v248,v249
v_mfma_f32_16x16x32_bf16 v[136:139],a[128:131],v[128:131],v[32:35]
v_exp_f32 v251,v251
v_mfma_f32_16x16x32_bf16 v[136:139],a[132:135],v[160:163],v[136:139]
v_exp_f32 v252,v252
v_mfma_f32_16x16x32_bf16 v[136:139],a[136:139],v[192:195],v[136:139]
v_exp_f32 v253,v253
v_mfma_f32_16x16x32_bf16 v[136:139],a[140:143],v[224:227],v[136:139]
v_exp_f32 v254,v254
v_mfma_f32_16x16x32_bf16 v[140:143],a[156:159],v[224:227],v[32:35]
v_exp_f32 v255,v255
v_mfma_f32_16x16x32_bf16 v[140:143],a[152:155],v[192:195],v[140:143]
v_cvt_pk_bf16_f32 v237,v250,v251
v_cvt_pk_bf16_f32 v238,v252,v253
v_mfma_f32_16x16x32_bf16 v[140:143],a[148:151],v[160:163],v[140:143]
v_cvt_pk_bf16_f32 v239,v254,v255
v_add_f32 v48,v49,v48
v_mfma_f32_16x16x32_bf16 v[140:143],a[144:147],v[128:131],v[140:143]
v_max_f32 v20,0x00800000,v48
v_accvgpr_read_b32 v64,a0
v_mfma_f32_16x16x32_bf16 v[152:155],a[160:163],v[128:131],v[32:35]
v_rcp_f32 v20,v20
v_mfma_f32_16x16x32_bf16 v[152:155],a[164:167],v[160:163],v[152:155]
v_exp_f32 v136,v136
v_mfma_f32_16x16x32_bf16 v[152:155],a[168:171],v[192:195],v[152:155]
v_exp_f32 v137,v137
v_mfma_f32_16x16x32_bf16 v[152:155],a[172:175],v[224:227],v[152:155]
buffer_load_dwordx4 v5,s[20:23],s47 offen offset:3072 nt lds
v_mfma_f32_16x16x32_bf16 v[156:159],a[188:191],v[224:227],v[32:35]
v_exp_f32 v138,v138
v_mfma_f32_16x16x32_bf16 v[156:159],a[184:187],v[192:195],v[156:159]
s_waitcnt lgkmcnt(0)
v_exp_f32 v139,v139
v_mfma_f32_16x16x32_bf16 v[156:159],a[180:183],v[160:163],v[156:159]
v_cvt_pk_bf16_f32 v136,v136,v137
v_cvt_pk_bf16_f32 v137,v138,v139
v_mfma_f32_16x16x32_bf16 v[156:159],a[176:179],v[128:131],v[156:159]
v_exp_f32 v140,v140
v_mfma_f32_16x16x32_bf16 a[64:67],a[192:195],v[200:203],a[64:67]
v_exp_f32 v141,v141
v_mfma_f32_16x16x32_bf16 a[64:67],a[196:199],v[204:207],a[64:67]
v_exp_f32 v142,v142
v_mfma_f32_16x16x32_bf16 a[68:71],a[204:207],v[204:207],a[68:71]
v_exp_f32 v143,v143
v_mfma_f32_16x16x32_bf16 a[68:71],a[200:203],v[200:203],a[68:71]
v_cvt_pk_bf16_f32 v138,v140,v141
v_cvt_pk_bf16_f32 v139,v142,v143
v_mfma_f32_16x16x32_bf16 a[72:75],a[208:211],v[200:203],a[72:75]
v_exp_f32 v152,v152
v_mfma_f32_16x16x32_bf16 a[72:75],a[212:215],v[204:207],a[72:75]
)A"
R"A(v_exp_f32 v153,v153
v_mfma_f32_16x16x32_bf16 a[76:79],a[220:223],v[204:207],a[76:79]
s_addk_i32 m0,16384
v_exp_f32 v154,v154
v_mfma_f32_16x16x32_bf16 a[76:79],a[216:219],v[200:203],a[76:79]
v_exp_f32 v155,v155
v_mfma_f32_16x16x32_bf16 a[80:83],a[224:227],v[200:203],a[80:83]
v_exp_f32 v156,v156
v_mfma_f32_16x16x32_bf16 a[80:83],a[228:231],v[204:207],a[80:83]
buffer_load_dwordx4 v4,s[24:27],s47 offen nt lds
v_mfma_f32_16x16x32_bf16 a[84:87],a[236:239],v[204:207],a[84:87]
v_exp_f32 v157,v157
v_mfma_f32_16x16x32_bf16 a[84:87],a[232:235],v[200:203],a[84:87]
v_exp_f32 v158,v158
v_mfma_f32_16x16x32_bf16 a[88:91],a[240:243],v[200:203],a[88:91]
v_exp_f32 v159,v159
v_mfma_f32_16x16x32_bf16 a[88:91],a[244:247],v[204:207],a[88:91]
v_cvt_pk_bf16_f32 v140,v152,v153
v_cvt_pk_bf16_f32 v141,v154,v155
v_mfma_f32_16x16x32_bf16 a[92:95],a[252:255],v[204:207],a[92:95]
v_cvt_pk_bf16_f32 v142,v156,v157
v_cvt_pk_bf16_f32 v143,v158,v159
v_mfma_f32_16x16x32_bf16 a[92:95],a[248:251],v[200:203],a[92:95]
v_accvgpr_read_b32 v65,a1
v_accvgpr_read_b32 v66,a2
v_smfmac_f32_16x16x64_bf16 v[56:59],v[28:31],v[200:207],v27
v_accvgpr_read_b32 v67,a3
v_mfma_f32_16x16x32_bf16 v[168:171],a[128:131],v[132:135],v[36:39]
v_mul_f32 v64,v20,v64
v_mul_f32 v65,v20,v65
v_mfma_f32_16x16x32_bf16 v[168:171],a[132:135],v[164:167],v[168:171]
v_cvt_pk_bf16_f32 v64,v64,v65
v_mul_f32 v66,v20,v66
v_mfma_f32_16x16x32_bf16 v[168:171],a[136:139],v[196:199],v[168:171]
v_mul_f32 v67,v20,v67
v_cvt_pk_bf16_f32 v65,v66,v67
v_mfma_f32_16x16x32_bf16 v[168:171],a[140:143],v[228:231],v[168:171]
v_accvgpr_read_b32 v16,a6
v_accvgpr_read_b32 v17,a7
v_mfma_f32_16x16x32_bf16 v[172:175],a[156:159],v[228:231],v[36:39]
v_accvgpr_read_b32 v66,a4
v_accvgpr_read_b32 v67,a5
v_mfma_f32_16x16x32_bf16 v[172:175],a[152:155],v[196:199],v[172:175]
.p2align 3
v_accvgpr_read_b32 v68,a8
v_accvgpr_read_b32 v69,a9
v_accvgpr_read_b32 v70,a10
v_accvgpr_read_b32 v71,a11
v_accvgpr_read_b32 v12,a12
v_accvgpr_read_b32 v13,a13
v_accvgpr_read_b32 v14,a14
v_accvgpr_read_b32 v15,a15
v_pk_mul_f32 v[68:69],v[20:21],v[68:69] op_sel:[0,0] op_sel_hi:[0,1]
v_pk_mul_f32 v[70:71],v[20:21],v[70:71] op_sel:[0,0] op_sel_hi:[0,1]
v_pk_mul_f32 v[12:13],v[20:21],v[12:13] op_sel:[0,0] op_sel_hi:[0,1]
v_pk_mul_f32 v[14:15],v[20:21],v[14:15] op_sel:[0,0] op_sel_hi:[0,1]
v_cvt_pk_bf16_f32 v68,v68,v69
v_cvt_pk_bf16_f32 v69,v70,v71
v_cvt_pk_bf16_f32 v70,v12,v13
v_cvt_pk_bf16_f32 v71,v14,v15
v_mfma_f32_16x16x32_bf16 v[172:175],a[148:151],v[164:167],v[172:175]
buffer_load_dwordx4 v5,s[24:27],s47 offen offset:1024 nt lds
v_mfma_f32_16x16x32_bf16 v[172:175],a[144:147],v[132:135],v[172:175]
.p2align 3
v_accvgpr_read_b32 v72,a16
v_accvgpr_read_b32 v73,a17
v_accvgpr_read_b32 v74,a18
v_accvgpr_read_b32 v75,a19
v_accvgpr_read_b32 v12,a20
v_accvgpr_read_b32 v13,a21
v_accvgpr_read_b32 v14,a22
v_accvgpr_read_b32 v15,a23
v_pk_mul_f32 v[72:73],v[20:21],v[72:73] op_sel:[0,0] op_sel_hi:[0,1]
v_pk_mul_f32 v[74:75],v[20:21],v[74:75] op_sel:[0,0] op_sel_hi:[0,1]
v_pk_mul_f32 v[12:13],v[20:21],v[12:13] op_sel:[0,0] op_sel_hi:[0,1]
v_pk_mul_f32 v[14:15],v[20:21],v[14:15] op_sel:[0,0] op_sel_hi:[0,1]
v_cvt_pk_bf16_f32 v72,v72,v73
v_cvt_pk_bf16_f32 v73,v74,v75
v_cvt_pk_bf16_f32 v74,v12,v13
v_cvt_pk_bf16_f32 v75,v14,v15
v_mfma_f32_16x16x32_bf16 v[184:187],a[160:163],v[132:135],v[36:39]
.p2align 3
v_accvgpr_read_b32 v76,a24
v_accvgpr_read_b32 v77,a25
v_accvgpr_read_b32 v78,a26
v_accvgpr_read_b32 v79,a27
v_accvgpr_read_b32 v12,a28
v_accvgpr_read_b32 v13,a29
v_accvgpr_read_b32 v14,a30
v_accvgpr_read_b32 v15,a31
v_pk_mul_f32 v[76:77],v[20:21],v[76:77] op_sel:[0,0] op_sel_hi:[0,1]
v_pk_mul_f32 v[78:79],v[20:21],v[78:79] op_sel:[0,0] op_sel_hi:[0,1]
v_pk_mul_f32 v[12:13],v[20:21],v[12:13] op_sel:[0,0] op_sel_hi:[0,1]
v_pk_mul_f32 v[14:15],v[20:21],v[14:15] op_sel:[0,0] op_sel_hi:[0,1]
v_cvt_pk_bf16_f32 v76,v76,v77
v_cvt_pk_bf16_f32 v77,v78,v79
v_cvt_pk_bf16_f32 v78,v12,v13
v_cvt_pk_bf16_f32 v79,v14,v15
v_mfma_f32_16x16x32_bf16 v[184:187],a[164:167],v[164:167],v[184:187]
v_cmp_nlt_f32 s[56:57],v48,s63
v_max3_f32 v44,v44,v45,v46
v_mfma_f32_16x16x32_bf16 v[184:187],a[168:171],v[196:199],v[184:187]
v_max3_f32 v44,v44,v47,s101
v_mov_b32 v48,0
v_mfma_f32_16x16x32_bf16 v[184:187],a[172:175],v[228:231],v[184:187]
v_mov_b32 v49,0
v_mov_b32 v50,0
v_mfma_f32_16x16x32_bf16 v[188:191],a[188:191],v[228:231],v[36:39]
v_mov_b32 v51,0
v_sub_f32 v47,0,v44
v_mfma_f32_16x16x32_bf16 v[188:191],a[184:187],v[196:199],v[188:191]
v_sub_f32 v46,0,v44
v_sub_f32 v45,0,v44
v_mfma_f32_16x16x32_bf16 v[188:191],a[180:183],v[164:167],v[188:191]
v_sub_f32 v44,0,v44
v_add_f32 v52,v53,v52
v_mfma_f32_16x16x32_bf16 v[188:191],a[176:179],v[132:135],v[188:191]
v_exp_f32 v168,v168
v_mfma_f32_16x16x32_bf16 a[96:99],a[192:195],v[232:235],a[96:99]
ds_read_b64_tr_b16 a[192:193],v2
ds_read_b64_tr_b16 a[194:195],v2 offset:4096
v_mfma_f32_16x16x32_bf16 a[96:99],a[196:199],v[236:239],a[96:99]
ds_read_b64_tr_b16 a[196:197],v2 offset:8192
v_max_f32 v21,0x00800000,v52
v_mfma_f32_16x16x32_bf16 a[100:103],a[204:207],v[236:239],a[100:103]
ds_read_b64_tr_b16 a[198:199],v2 offset:12288
v_accvgpr_read_b32 v80,a32
v_mfma_f32_16x16x32_bf16 a[100:103],a[200:203],v[232:235],a[100:103]
ds_read_b64_tr_b16 a[204:205],v3 offset:8192
v_accvgpr_read_b32 v81,a33
v_mfma_f32_16x16x32_bf16 a[104:107],a[208:211],v[232:235],a[104:107]
buffer_load_dwordx4 v4,s[24:27],s47 offen offset:2048 nt lds
v_mfma_f32_16x16x32_bf16 a[104:107],a[212:215],v[236:239],a[104:107]
ds_read_b64_tr_b16 a[206:207],v3 offset:12288
ds_read_b64_tr_b16 a[200:201],v3
v_mfma_f32_16x16x32_bf16 a[108:111],a[220:223],v[236:239],a[108:111]
ds_read_b64_tr_b16 a[202:203],v3 offset:4096
v_accvgpr_read_b32 v82,a34
v_mfma_f32_16x16x32_bf16 a[108:111],a[216:219],v[232:235],a[108:111]
ds_read_b64_tr_b16 a[208:209],v2 offset:256
v_accvgpr_read_b32 v83,a35
v_mfma_f32_16x16x32_bf16 a[112:115],a[224:227],v[232:235],a[112:115]
ds_read_b64_tr_b16 a[210:211],v2 offset:4352
v_accvgpr_read_b32 v12,a38
v_mfma_f32_16x16x32_bf16 a[112:115],a[228:231],v[236:239],a[112:115]
ds_read_b64_tr_b16 a[212:213],v2 offset:8448
ds_read_b64_tr_b16 a[214:215],v2 offset:12544
v_mfma_f32_16x16x32_bf16 a[116:119],a[236:239],v[236:239],a[116:119]
ds_read_b64_tr_b16 a[220:221],v3 offset:8448
v_accvgpr_read_b32 v13,a39
v_mfma_f32_16x16x32_bf16 a[116:119],a[232:235],v[232:235],a[116:119]
ds_read_b64_tr_b16 a[222:223],v3 offset:12544
v_add_u32 v1,s48,v1
v_mfma_f32_16x16x32_bf16 a[120:123],a[240:243],v[232:235],a[120:123]
ds_read_b64_tr_b16 a[216:217],v3 offset:256
v_mov_b32 v53,0
v_mfma_f32_16x16x32_bf16 a[120:123],a[244:247],v[236:239],a[120:123]
ds_read_b64_tr_b16 a[218:219],v3 offset:4352
ds_read_b64_tr_b16 a[224:225],v2 offset:512
v_mfma_f32_16x16x32_bf16 a[124:127],a[252:255],v[236:239],a[124:127]
ds_read_b64_tr_b16 a[226:227],v2 offset:4608
v_mov_b32 v54,0
v_mfma_f32_16x16x32_bf16 a[124:127],a[248:251],v[232:235],a[124:127]
ds_read_b64_tr_b16 a[228:229],v2 offset:8704
v_mov_b32 v55,0
v_smfmac_f32_16x16x64_bf16 v[60:63],v[28:31],v[232:239],v27
ds_read_b64_tr_b16 a[230:231],v2 offset:12800
v_mfma_f32_16x16x32_bf16 v[200:203],a[128:131],v[144:147],v[40:43]
ds_read_b64_tr_b16 a[236:237],v3 offset:8704
ds_read_b64_tr_b16 a[238:239],v3 offset:12800
v_mfma_f32_16x16x32_bf16 v[200:203],a[132:135],v[176:179],v[200:203]
buffer_load_dwordx4 v5,s[24:27],s47 offen offset:3072 nt lds
v_mfma_f32_16x16x32_bf16 v[200:203],a[136:139],v[208:211],v[200:203]
s_or_b32 s47,s47,0x18000
ds_read_b64_tr_b16 a[232:233],v3 offset:512
ds_read_b64_tr_b16 a[234:235],v3 offset:4608
v_mfma_f32_16x16x32_bf16 v[200:203],a[140:143],v[240:243],v[200:203]
s_addk_i32 s47,4096
s_and_b32 s47,s47,0x27000
ds_read_b64_tr_b16 a[240:241],v2 offset:768
v_mfma_f32_16x16x32_bf16 v[204:207],a[156:159],v[240:243],v[40:43]
s_or_b32 s47,s47,s46
ds_read_b64_tr_b16 a[242:243],v2 offset:4864
v_add_f32 v56,v57,v56
v_mfma_f32_16x16x32_bf16 v[204:207],a[152:155],v[208:211],v[204:207]
s_and_b32 s46,s54,0xfffc0000
ds_read_b64_tr_b16 a[244:245],v2 offset:8960
v_max_f32 v22,0x00800000,v56
v_mfma_f32_16x16x32_bf16 v[204:207],a[148:151],v[176:179],v[204:207]
ds_read_b64_tr_b16 a[246:247],v2 offset:13056
ds_read_b64_tr_b16 a[252:253],v3 offset:8960
v_mfma_f32_16x16x32_bf16 v[204:207],a[144:147],v[144:147],v[204:207]
ds_read_b64_tr_b16 a[254:255],v3 offset:13056
v_accvgpr_read_b32 v96,a64
v_mfma_f32_16x16x32_bf16 v[216:219],a[160:163],v[144:147],v[40:43]
ds_read_b64_tr_b16 a[248:249],v3 offset:768
v_accvgpr_read_b32 v97,a65
v_mfma_f32_16x16x32_bf16 v[216:219],a[164:167],v[176:179],v[216:219]
ds_read_b64_tr_b16 a[250:251],v3 offset:4864
v_accvgpr_read_b32 v98,a66
v_mfma_f32_16x16x32_bf16 v[216:219],a[168:171],v[208:211],v[216:219]
v_exp_f32 v169,v169
v_mfma_f32_16x16x32_bf16 v[216:219],a[172:175],v[240:243],v[216:219]
v_exp_f32 v170,v170
v_mfma_f32_16x16x32_bf16 v[220:223],a[188:191],v[240:243],v[40:43]
v_exp_f32 v171,v171
v_mfma_f32_16x16x32_bf16 v[220:223],a[184:187],v[208:211],v[220:223]
v_cvt_pk_bf16_f32 v168,v168,v169
v_cvt_pk_bf16_f32 v169,v170,v171
v_mfma_f32_16x16x32_bf16 v[220:223],a[180:183],v[176:179],v[220:223]
v_exp_f32 v172,v172
v_mfma_f32_16x16x32_bf16 v[220:223],a[176:179],v[144:147],v[220:223]
s_waitcnt lgkmcnt(2)
v_exp_f32 v173,v173
v_mfma_f32_16x16x32_bf16 a[0:3],a[192:195],v[136:139],0
v_exp_f32 v174,v174
v_mfma_f32_16x16x32_bf16 a[0:3],a[196:199],v[140:143],a[0:3]
v_exp_f32 v175,v175
v_mfma_f32_16x16x32_bf16 a[4:7],a[204:207],v[140:143],0
v_cvt_pk_bf16_f32 v170,v172,v173
v_cvt_pk_bf16_f32 v171,v174,v175
v_mfma_f32_16x16x32_bf16 a[4:7],a[200:203],v[136:139],a[4:7]
s_memrealtime s[76:77]
v_exp_f32 v184,v184
v_mfma_f32_16x16x32_bf16 a[8:11],a[208:211],v[136:139],0
v_exp_f32 v185,v185
v_mfma_f32_16x16x32_bf16 a[8:11],a[212:215],v[140:143],a[8:11]
v_exp_f32 v186,v186
v_mfma_f32_16x16x32_bf16 a[12:15],a[220:223],v[140:143],0
v_exp_f32 v187,v187
v_mfma_f32_16x16x32_bf16 a[12:15],a[216:219],v[136:139],a[12:15]
v_exp_f32 v188,v188
v_mfma_f32_16x16x32_bf16 a[16:19],a[224:227],v[136:139],0
v_exp_f32 v189,v189
v_mfma_f32_16x16x32_bf16 a[16:19],a[228:231],v[140:143],a[16:19]
v_exp_f32 v190,v190
v_mfma_f32_16x16x32_bf16 a[20:23],a[236:239],v[140:143],0
s_waitcnt lgkmcnt(0)
v_exp_f32 v191,v191
v_mfma_f32_16x16x32_bf16 a[20:23],a[232:235],v[136:139],a[20:23]
s_sub_u32 s79,s78,s76
v_rcp_f32 v21,v21
v_mfma_f32_16x16x32_bf16 a[24:27],a[240:243],v[136:139],0
s_add_u32 s78,s76,s60
v_cvt_pk_bf16_f32 v172,v184,v185
v_cvt_pk_bf16_f32 v173,v186,v187
v_mfma_f32_16x16x32_bf16 a[24:27],a[244:247],v[140:143],a[24:27]
s_cmp_gt_i32 s79,s95
s_cbranch_scc1 L_ps5_%=
L_pr5_%=:
v_cvt_pk_bf16_f32 v174,v188,v189
v_mfma_f32_16x16x32_bf16 a[28:31],a[252:255],v[140:143],0
s_lshr_b32 s60,s94,2
v_cvt_pk_bf16_f32 v175,v190,v191
v_mul_f32 v80,v21,v80
v_mfma_f32_16x16x32_bf16 a[28:31],a[248:251],v[136:139],a[28:31]
s_max_i32 s60,s60,124
v_mul_f32 v81,v21,v81
v_cvt_pk_bf16_f32 v80,v80,v81
v_smfmac_f32_16x16x64_bf16 v[48:51],v[28:31],v[136:143],v27
v_mul_f32 v82,v21,v82
v_mfma_f32_16x16x32_bf16 v[232:235],a[128:131],v[148:151],v[44:47]
s_waitcnt vmcnt(8)
v_mul_f32 v83,v21,v83
v_cvt_pk_bf16_f32 v81,v82,v83
v_mfma_f32_16x16x32_bf16 v[232:235],a[132:135],v[180:183],v[232:235]
s_barrier
v_accvgpr_read_b32 v82,a36
v_accvgpr_read_b32 v83,a37
v_mfma_f32_16x16x32_bf16 v[232:235],a[136:139],v[212:215],v[232:235]
v_mul_f32 v82,v21,v82
v_mul_f32 v83,v21,v83
v_mfma_f32_16x16x32_bf16 v[232:235],a[140:143],v[244:247],v[232:235]
s_add_u32 m0,65536,s51
v_mul_f32 v12,v21,v12
v_mul_f32 v13,v21,v13
v_mfma_f32_16x16x32_bf16 v[236:239],a[156:159],v[244:247],v[44:47]
v_cvt_pk_bf16_f32 v82,v82,v83
v_cvt_pk_bf16_f32 v83,v12,v13
v_mfma_f32_16x16x32_bf16 v[236:239],a[152:155],v[212:215],v[236:239]
.p2align 3
v_accvgpr_read_b32 v84,a40
v_accvgpr_read_b32 v85,a41
v_accvgpr_read_b32 v86,a42
v_accvgpr_read_b32 v87,a43
v_accvgpr_read_b32 v12,a44
v_accvgpr_read_b32 v13,a45
v_accvgpr_read_b32 v14,a46
v_accvgpr_read_b32 v15,a47
v_pk_mul_f32 v[84:85],v[20:21],v[84:85] op_sel:[1,0] op_sel_hi:[1,1]
v_pk_mul_f32 v[86:87],v[20:21],v[86:87] op_sel:[1,0] op_sel_hi:[1,1]
v_pk_mul_f32 v[12:13],v[20:21],v[12:13] op_sel:[1,0] op_sel_hi:[1,1]
)A"
R"A(v_pk_mul_f32 v[14:15],v[20:21],v[14:15] op_sel:[1,0] op_sel_hi:[1,1]
v_cvt_pk_bf16_f32 v84,v84,v85
v_cvt_pk_bf16_f32 v85,v86,v87
v_cvt_pk_bf16_f32 v86,v12,v13
v_cvt_pk_bf16_f32 v87,v14,v15
v_mfma_f32_16x16x32_bf16 v[236:239],a[148:151],v[180:183],v[236:239]
buffer_load_dwordx4 v4,s[20:23],s47 offen nt lds
v_mfma_f32_16x16x32_bf16 v[236:239],a[144:147],v[148:151],v[236:239]
.p2align 3
v_accvgpr_read_b32 v88,a48
v_accvgpr_read_b32 v89,a49
v_accvgpr_read_b32 v90,a50
v_accvgpr_read_b32 v91,a51
v_accvgpr_read_b32 v12,a52
v_accvgpr_read_b32 v13,a53
v_accvgpr_read_b32 v14,a54
v_accvgpr_read_b32 v15,a55
v_pk_mul_f32 v[88:89],v[20:21],v[88:89] op_sel:[1,0] op_sel_hi:[1,1]
v_pk_mul_f32 v[90:91],v[20:21],v[90:91] op_sel:[1,0] op_sel_hi:[1,1]
v_pk_mul_f32 v[12:13],v[20:21],v[12:13] op_sel:[1,0] op_sel_hi:[1,1]
v_pk_mul_f32 v[14:15],v[20:21],v[14:15] op_sel:[1,0] op_sel_hi:[1,1]
v_cvt_pk_bf16_f32 v88,v88,v89
v_cvt_pk_bf16_f32 v89,v90,v91
v_cvt_pk_bf16_f32 v90,v12,v13
v_cvt_pk_bf16_f32 v91,v14,v15
v_mfma_f32_16x16x32_bf16 v[248:251],a[160:163],v[148:151],v[44:47]
ds_read_b128 a[128:131],v1
v_accvgpr_read_b32 v99,a67
v_mfma_f32_16x16x32_bf16 v[248:251],a[164:167],v[180:183],v[248:251]
.p2align 3
v_accvgpr_read_b32 v92,a56
v_accvgpr_read_b32 v93,a57
v_accvgpr_read_b32 v94,a58
v_accvgpr_read_b32 v95,a59
v_accvgpr_read_b32 v12,a60
v_accvgpr_read_b32 v13,a61
v_accvgpr_read_b32 v14,a62
v_accvgpr_read_b32 v15,a63
v_pk_mul_f32 v[92:93],v[20:21],v[92:93] op_sel:[1,0] op_sel_hi:[1,1]
v_pk_mul_f32 v[94:95],v[20:21],v[94:95] op_sel:[1,0] op_sel_hi:[1,1]
v_pk_mul_f32 v[12:13],v[20:21],v[12:13] op_sel:[1,0] op_sel_hi:[1,1]
v_pk_mul_f32 v[14:15],v[20:21],v[14:15] op_sel:[1,0] op_sel_hi:[1,1]
v_cvt_pk_bf16_f32 v92,v92,v93
v_cvt_pk_bf16_f32 v93,v94,v95
v_cvt_pk_bf16_f32 v94,v12,v13
v_cvt_pk_bf16_f32 v95,v14,v15
v_mfma_f32_16x16x32_bf16 v[248:251],a[168:171],v[212:215],v[248:251]
ds_read_b128 a[132:135],v1 offset:256
v_accvgpr_read_b32 v14,a70
v_mfma_f32_16x16x32_bf16 v[248:251],a[172:175],v[244:247],v[248:251]
v_cmp_nlt_f32 s[58:59],v52,s63
s_nop 1
s_or_b64 s[56:57],s[56:57],s[58:59]
v_mfma_f32_16x16x32_bf16 v[252:255],a[188:191],v[244:247],v[44:47]
ds_read_b128 a[136:139],v1 offset:512
v_mov_b32 v52,0
v_mfma_f32_16x16x32_bf16 v[252:255],a[184:187],v[212:215],v[252:255]
v_exp_f32 v200,v200
v_mfma_f32_16x16x32_bf16 v[252:255],a[180:183],v[180:183],v[252:255]
ds_read_b128 a[140:143],v1 offset:768
v_accvgpr_read_b32 v15,a71
v_mfma_f32_16x16x32_bf16 v[252:255],a[176:179],v[148:151],v[252:255]
v_exp_f32 v201,v201
v_mfma_f32_16x16x32_bf16 a[32:35],a[192:195],v[168:171],0
ds_read_b128 a[156:159],v1 offset:4864
v_cvt_pk_bf16_f32 v200,v200,v201
v_mfma_f32_16x16x32_bf16 a[32:35],a[196:199],v[172:175],a[32:35]
v_exp_f32 v202,v202
v_mfma_f32_16x16x32_bf16 a[36:39],a[204:207],v[172:175],0
ds_read_b128 a[152:155],v1 offset:4608
v_mov_b32 v57,0
v_mfma_f32_16x16x32_bf16 a[36:39],a[200:203],v[168:171],a[36:39]
v_exp_f32 v203,v203
v_mfma_f32_16x16x32_bf16 a[40:43],a[208:211],v[168:171],0
buffer_load_dwordx4 v4,s[20:23],s47 offen offset:1024 nt lds
v_mfma_f32_16x16x32_bf16 a[40:43],a[212:215],v[172:175],a[40:43]
ds_read_b128 a[148:151],v1 offset:4352
v_cvt_pk_bf16_f32 v201,v202,v203
v_mfma_f32_16x16x32_bf16 a[44:47],a[220:223],v[172:175],0
v_exp_f32 v204,v204
v_mfma_f32_16x16x32_bf16 a[44:47],a[216:219],v[168:171],a[44:47]
ds_read_b128 a[144:147],v1 offset:4096
v_mov_b32 v58,0
v_mfma_f32_16x16x32_bf16 a[48:51],a[224:227],v[168:171],0
v_exp_f32 v205,v205
v_mfma_f32_16x16x32_bf16 a[48:51],a[228:231],v[172:175],a[48:51]
ds_read_b128 a[160:163],v1 offset:8192
v_cvt_pk_bf16_f32 v202,v204,v205
v_mfma_f32_16x16x32_bf16 a[52:55],a[236:239],v[172:175],0
v_exp_f32 v206,v206
v_mfma_f32_16x16x32_bf16 a[52:55],a[232:235],v[168:171],a[52:55]
ds_read_b128 a[164:167],v1 offset:8448
v_mov_b32 v59,0
v_mfma_f32_16x16x32_bf16 a[56:59],a[240:243],v[168:171],0
v_exp_f32 v207,v207
v_mfma_f32_16x16x32_bf16 a[56:59],a[244:247],v[172:175],a[56:59]
ds_read_b128 a[168:171],v1 offset:8704
v_cvt_pk_bf16_f32 v203,v206,v207
v_mfma_f32_16x16x32_bf16 a[60:63],a[252:255],v[172:175],0
v_exp_f32 v216,v216
v_mfma_f32_16x16x32_bf16 a[60:63],a[248:251],v[168:171],a[60:63]
ds_read_b128 a[172:175],v1 offset:8960
v_add_f32 v60,v61,v60
v_smfmac_f32_16x16x64_bf16 v[52:55],v[28:31],v[168:175],v27
s_waitcnt lgkmcnt(4)
v_max_f32 v23,0x00800000,v60
v_mfma_f32_16x16x32_bf16 v[136:139],a[128:131],v[128:131],v[32:35]
ds_read_b128 a[188:191],v1 offset:13056
v_accvgpr_read_b32 v112,a96
v_mfma_f32_16x16x32_bf16 v[136:139],a[132:135],v[160:163],v[136:139]
buffer_load_dwordx4 v5,s[20:23],s47 offen offset:2048 nt lds
v_mfma_f32_16x16x32_bf16 v[136:139],a[136:139],v[192:195],v[136:139]
ds_read_b128 a[184:187],v1 offset:12800
v_accvgpr_read_b32 v113,a97
v_mfma_f32_16x16x32_bf16 v[136:139],a[140:143],v[224:227],v[136:139]
v_exp_f32 v217,v217
v_mfma_f32_16x16x32_bf16 v[140:143],a[156:159],v[224:227],v[32:35]
ds_read_b128 a[180:183],v1 offset:12544
v_cvt_pk_bf16_f32 v204,v216,v217
v_mfma_f32_16x16x32_bf16 v[140:143],a[152:155],v[192:195],v[140:143]
v_exp_f32 v218,v218
v_mfma_f32_16x16x32_bf16 v[140:143],a[148:151],v[160:163],v[140:143]
ds_read_b128 a[176:179],v1 offset:12288
v_accvgpr_read_b32 v114,a98
v_mfma_f32_16x16x32_bf16 v[140:143],a[144:147],v[128:131],v[140:143]
s_waitcnt lgkmcnt(4)
v_exp_f32 v219,v219
v_mfma_f32_16x16x32_bf16 v[152:155],a[160:163],v[128:131],v[32:35]
v_exp_f32 v220,v220
v_mfma_f32_16x16x32_bf16 v[152:155],a[164:167],v[160:163],v[152:155]
v_exp_f32 v221,v221
v_mfma_f32_16x16x32_bf16 v[152:155],a[168:171],v[192:195],v[152:155]
v_exp_f32 v222,v222
v_mfma_f32_16x16x32_bf16 v[152:155],a[172:175],v[224:227],v[152:155]
s_waitcnt lgkmcnt(2)
v_exp_f32 v223,v223
v_mfma_f32_16x16x32_bf16 v[156:159],a[188:191],v[224:227],v[32:35]
v_rcp_f32 v22,v22
v_mfma_f32_16x16x32_bf16 v[156:159],a[184:187],v[192:195],v[156:159]
s_waitcnt lgkmcnt(1)
v_cvt_pk_bf16_f32 v205,v218,v219
v_cvt_pk_bf16_f32 v206,v220,v221
v_mfma_f32_16x16x32_bf16 v[156:159],a[180:183],v[160:163],v[156:159]
s_waitcnt lgkmcnt(0)
v_cvt_pk_bf16_f32 v207,v222,v223
v_mul_f32 v96,v22,v96
v_mfma_f32_16x16x32_bf16 v[156:159],a[176:179],v[128:131],v[156:159]
v_mul_f32 v97,v22,v97
v_cvt_pk_bf16_f32 v96,v96,v97
v_mfma_f32_16x16x32_bf16 a[64:67],a[192:195],v[200:203],0
v_mul_f32 v98,v22,v98
v_mul_f32 v99,v22,v99
v_cvt_pk_bf16_f32 v97,v98,v99
v_mfma_f32_16x16x32_bf16 a[64:67],a[196:199],v[204:207],a[64:67]
v_accvgpr_read_b32 v98,a68
v_accvgpr_read_b32 v99,a69
v_mul_f32 v98,v22,v98
v_mul_f32 v99,v22,v99
v_mfma_f32_16x16x32_bf16 a[68:71],a[204:207],v[204:207],0
buffer_load_dwordx4 v5,s[20:23],s47 offen offset:3072 nt lds
v_mul_f32 v14,v22,v14
v_mul_f32 v15,v22,v15
v_cvt_pk_bf16_f32 v98,v98,v99
v_cvt_pk_bf16_f32 v99,v14,v15
v_mfma_f32_16x16x32_bf16 a[68:71],a[200:203],v[200:203],a[68:71]
.p2align 3
v_accvgpr_read_b32 v100,a72
v_accvgpr_read_b32 v101,a73
v_accvgpr_read_b32 v102,a74
v_accvgpr_read_b32 v103,a75
v_accvgpr_read_b32 v12,a76
v_accvgpr_read_b32 v13,a77
v_accvgpr_read_b32 v14,a78
v_accvgpr_read_b32 v15,a79
v_pk_mul_f32 v[100:101],v[22:23],v[100:101] op_sel:[0,0] op_sel_hi:[0,1]
v_pk_mul_f32 v[102:103],v[22:23],v[102:103] op_sel:[0,0] op_sel_hi:[0,1]
v_pk_mul_f32 v[12:13],v[22:23],v[12:13] op_sel:[0,0] op_sel_hi:[0,1]
v_pk_mul_f32 v[14:15],v[22:23],v[14:15] op_sel:[0,0] op_sel_hi:[0,1]
v_cvt_pk_bf16_f32 v100,v100,v101
v_cvt_pk_bf16_f32 v101,v102,v103
v_cvt_pk_bf16_f32 v102,v12,v13
v_cvt_pk_bf16_f32 v103,v14,v15
v_mfma_f32_16x16x32_bf16 a[72:75],a[208:211],v[200:203],0
.p2align 3
v_accvgpr_read_b32 v104,a80
v_accvgpr_read_b32 v105,a81
v_accvgpr_read_b32 v106,a82
v_accvgpr_read_b32 v107,a83
v_accvgpr_read_b32 v12,a84
v_accvgpr_read_b32 v13,a85
v_accvgpr_read_b32 v14,a86
v_accvgpr_read_b32 v15,a87
v_pk_mul_f32 v[104:105],v[22:23],v[104:105] op_sel:[0,0] op_sel_hi:[0,1]
v_pk_mul_f32 v[106:107],v[22:23],v[106:107] op_sel:[0,0] op_sel_hi:[0,1]
v_pk_mul_f32 v[12:13],v[22:23],v[12:13] op_sel:[0,0] op_sel_hi:[0,1]
v_pk_mul_f32 v[14:15],v[22:23],v[14:15] op_sel:[0,0] op_sel_hi:[0,1]
v_cvt_pk_bf16_f32 v104,v104,v105
v_cvt_pk_bf16_f32 v105,v106,v107
v_cvt_pk_bf16_f32 v106,v12,v13
v_cvt_pk_bf16_f32 v107,v14,v15
v_mfma_f32_16x16x32_bf16 a[72:75],a[212:215],v[204:207],a[72:75]
.p2align 3
v_accvgpr_read_b32 v108,a88
v_accvgpr_read_b32 v109,a89
v_accvgpr_read_b32 v110,a90
v_accvgpr_read_b32 v111,a91
v_accvgpr_read_b32 v12,a92
v_accvgpr_read_b32 v13,a93
v_accvgpr_read_b32 v14,a94
v_accvgpr_read_b32 v15,a95
v_pk_mul_f32 v[108:109],v[22:23],v[108:109] op_sel:[0,0] op_sel_hi:[0,1]
v_pk_mul_f32 v[110:111],v[22:23],v[110:111] op_sel:[0,0] op_sel_hi:[0,1]
v_pk_mul_f32 v[12:13],v[22:23],v[12:13] op_sel:[0,0] op_sel_hi:[0,1]
v_pk_mul_f32 v[14:15],v[22:23],v[14:15] op_sel:[0,0] op_sel_hi:[0,1]
v_cvt_pk_bf16_f32 v108,v108,v109
v_cvt_pk_bf16_f32 v109,v110,v111
v_cvt_pk_bf16_f32 v110,v12,v13
v_cvt_pk_bf16_f32 v111,v14,v15
v_mfma_f32_16x16x32_bf16 a[76:79],a[220:223],v[204:207],0
v_cmp_nlt_f32 s[58:59],v56,s63
s_nop 1
s_or_b64 s[56:57],s[56:57],s[58:59]
v_mfma_f32_16x16x32_bf16 a[76:79],a[216:219],v[200:203],a[76:79]
v_mov_b32 v56,0
v_accvgpr_read_b32 v115,a99
v_mfma_f32_16x16x32_bf16 a[80:83],a[224:227],v[200:203],0
v_exp_f32 v232,v232
v_mfma_f32_16x16x32_bf16 a[80:83],a[228:231],v[204:207],a[80:83]
v_exp_f32 v233,v233
v_mfma_f32_16x16x32_bf16 a[84:87],a[236:239],v[204:207],0
s_addk_i32 m0,16384
v_exp_f32 v234,v234
v_mfma_f32_16x16x32_bf16 a[84:87],a[232:235],v[200:203],a[84:87]
v_exp_f32 v235,v235
v_mfma_f32_16x16x32_bf16 a[88:91],a[240:243],v[200:203],0
v_cvt_pk_bf16_f32 v232,v232,v233
v_cvt_pk_bf16_f32 v233,v234,v235
v_mfma_f32_16x16x32_bf16 a[88:91],a[244:247],v[204:207],a[88:91]
buffer_load_dwordx4 v4,s[24:27],s47 offen nt lds
v_mfma_f32_16x16x32_bf16 a[92:95],a[252:255],v[204:207],0
v_exp_f32 v236,v236
v_mfma_f32_16x16x32_bf16 a[92:95],a[248:251],v[200:203],a[92:95]
v_exp_f32 v237,v237
v_smfmac_f32_16x16x64_bf16 v[56:59],v[28:31],v[200:207],v27
v_cvt_pk_bf16_f32 v234,v236,v237
v_mfma_f32_16x16x32_bf16 v[168:171],a[128:131],v[132:135],v[36:39]
v_exp_f32 v238,v238
v_mfma_f32_16x16x32_bf16 v[168:171],a[132:135],v[164:167],v[168:171]
v_exp_f32 v239,v239
v_mfma_f32_16x16x32_bf16 v[168:171],a[136:139],v[196:199],v[168:171]
v_cvt_pk_bf16_f32 v235,v238,v239
v_mul_f32 v66,v20,v66
v_mfma_f32_16x16x32_bf16 v[168:171],a[140:143],v[228:231],v[168:171]
v_exp_f32 v248,v248
v_mfma_f32_16x16x32_bf16 v[172:175],a[156:159],v[228:231],v[36:39]
v_exp_f32 v249,v249
v_mfma_f32_16x16x32_bf16 v[172:175],a[152:155],v[196:199],v[172:175]
v_exp_f32 v250,v250
v_mfma_f32_16x16x32_bf16 v[172:175],a[148:151],v[164:167],v[172:175]
v_exp_f32 v251,v251
v_mfma_f32_16x16x32_bf16 v[172:175],a[144:147],v[132:135],v[172:175]
v_exp_f32 v252,v252
v_mfma_f32_16x16x32_bf16 v[184:187],a[160:163],v[132:135],v[36:39]
v_exp_f32 v253,v253
v_mfma_f32_16x16x32_bf16 v[184:187],a[164:167],v[164:167],v[184:187]
v_exp_f32 v254,v254
v_mfma_f32_16x16x32_bf16 v[184:187],a[168:171],v[196:199],v[184:187]
buffer_load_dwordx4 v5,s[24:27],s47 offen offset:1024 nt lds
v_mfma_f32_16x16x32_bf16 v[184:187],a[172:175],v[228:231],v[184:187]
v_exp_f32 v255,v255
v_mfma_f32_16x16x32_bf16 v[188:191],a[188:191],v[228:231],v[36:39]
v_cvt_pk_bf16_f32 v236,v248,v249
v_cvt_pk_bf16_f32 v237,v250,v251
v_mfma_f32_16x16x32_bf16 v[188:191],a[184:187],v[196:199],v[188:191]
v_cvt_pk_bf16_f32 v238,v252,v253
v_cvt_pk_bf16_f32 v239,v254,v255
v_mfma_f32_16x16x32_bf16 v[188:191],a[180:183],v[164:167],v[188:191]
v_rcp_f32 v23,v23
v_mfma_f32_16x16x32_bf16 v[188:191],a[176:179],v[132:135],v[188:191]
v_mul_f32 v67,v20,v67
v_mul_f32 v112,v23,v112
v_mfma_f32_16x16x32_bf16 a[96:99],a[192:195],v[232:235],0
v_mul_f32 v113,v23,v113
v_mul_f32 v16,v20,v16
v_mul_f32 v17,v20,v17
v_cvt_pk_bf16_f32 v66,v66,v67
v_cvt_pk_bf16_f32 v112,v112,v113
v_mul_f32 v114,v23,v114
v_mul_f32 v115,v23,v115
v_mfma_f32_16x16x32_bf16 a[96:99],a[196:199],v[236:239],a[96:99]
.p2align 3
v_cvt_pk_bf16_f32 v67,v16,v17
v_cvt_pk_bf16_f32 v113,v114,v115
v_accvgpr_read_b32 v16,a102
v_accvgpr_read_b32 v17,a103
v_accvgpr_read_b32 v114,a100
v_accvgpr_read_b32 v115,a101
v_mfma_f32_16x16x32_bf16 a[100:103],a[204:207],v[236:239],0
)A"
R"A(.p2align 3
v_accvgpr_read_b32 v116,a104
v_accvgpr_read_b32 v117,a105
v_accvgpr_read_b32 v118,a106
v_accvgpr_read_b32 v119,a107
v_accvgpr_read_b32 v12,a108
v_accvgpr_read_b32 v13,a109
v_accvgpr_read_b32 v14,a110
v_accvgpr_read_b32 v15,a111
v_pk_mul_f32 v[116:117],v[22:23],v[116:117] op_sel:[1,0] op_sel_hi:[1,1]
v_pk_mul_f32 v[118:119],v[22:23],v[118:119] op_sel:[1,0] op_sel_hi:[1,1]
v_pk_mul_f32 v[12:13],v[22:23],v[12:13] op_sel:[1,0] op_sel_hi:[1,1]
v_pk_mul_f32 v[14:15],v[22:23],v[14:15] op_sel:[1,0] op_sel_hi:[1,1]
v_cvt_pk_bf16_f32 v116,v116,v117
v_cvt_pk_bf16_f32 v117,v118,v119
v_cvt_pk_bf16_f32 v118,v12,v13
v_cvt_pk_bf16_f32 v119,v14,v15
v_mfma_f32_16x16x32_bf16 a[100:103],a[200:203],v[232:235],a[100:103]
.p2align 3
v_accvgpr_read_b32 v120,a112
v_accvgpr_read_b32 v121,a113
v_accvgpr_read_b32 v122,a114
v_accvgpr_read_b32 v123,a115
v_accvgpr_read_b32 v12,a116
v_accvgpr_read_b32 v13,a117
v_accvgpr_read_b32 v14,a118
v_accvgpr_read_b32 v15,a119
v_pk_mul_f32 v[120:121],v[22:23],v[120:121] op_sel:[1,0] op_sel_hi:[1,1]
v_pk_mul_f32 v[122:123],v[22:23],v[122:123] op_sel:[1,0] op_sel_hi:[1,1]
v_pk_mul_f32 v[12:13],v[22:23],v[12:13] op_sel:[1,0] op_sel_hi:[1,1]
v_pk_mul_f32 v[14:15],v[22:23],v[14:15] op_sel:[1,0] op_sel_hi:[1,1]
v_cvt_pk_bf16_f32 v120,v120,v121
v_cvt_pk_bf16_f32 v121,v122,v123
v_cvt_pk_bf16_f32 v122,v12,v13
v_cvt_pk_bf16_f32 v123,v14,v15
v_mfma_f32_16x16x32_bf16 a[104:107],a[208:211],v[232:235],0
.p2align 3
v_accvgpr_read_b32 v124,a120
v_accvgpr_read_b32 v125,a121
v_accvgpr_read_b32 v126,a122
v_accvgpr_read_b32 v127,a123
v_accvgpr_read_b32 v12,a124
v_accvgpr_read_b32 v13,a125
v_accvgpr_read_b32 v14,a126
v_accvgpr_read_b32 v15,a127
v_pk_mul_f32 v[124:125],v[22:23],v[124:125] op_sel:[1,0] op_sel_hi:[1,1]
v_pk_mul_f32 v[126:127],v[22:23],v[126:127] op_sel:[1,0] op_sel_hi:[1,1]
v_pk_mul_f32 v[12:13],v[22:23],v[12:13] op_sel:[1,0] op_sel_hi:[1,1]
v_pk_mul_f32 v[14:15],v[22:23],v[14:15] op_sel:[1,0] op_sel_hi:[1,1]
v_cvt_pk_bf16_f32 v124,v124,v125
v_cvt_pk_bf16_f32 v125,v126,v127
v_cvt_pk_bf16_f32 v126,v12,v13
v_cvt_pk_bf16_f32 v127,v14,v15
v_mfma_f32_16x16x32_bf16 a[104:107],a[212:215],v[236:239],a[104:107]
v_cmp_nlt_f32 s[58:59],v60,s63
s_nop 1
s_or_b64 s[56:57],s[56:57],s[58:59]
v_mfma_f32_16x16x32_bf16 a[108:111],a[220:223],v[236:239],0
v_mov_b32 v60,0
v_mov_b32 v61,0
v_mfma_f32_16x16x32_bf16 a[108:111],a[216:219],v[232:235],a[108:111]
v_mov_b32 v62,0
v_mov_b32 v63,0
v_mfma_f32_16x16x32_bf16 a[112:115],a[224:227],v[232:235],0
buffer_load_dwordx4 v4,s[24:27],s47 offen offset:2048 nt lds
v_mfma_f32_16x16x32_bf16 a[112:115],a[228:231],v[236:239],a[112:115]
v_add_u32 v2,s48,v2
v_add_u32 v3,s48,v3
v_mfma_f32_16x16x32_bf16 a[116:119],a[236:239],v[236:239],0
s_mov_b32 s48,32768
ds_read_b64_tr_b16 a[192:193],v2
ds_read_b64_tr_b16 a[194:195],v2 offset:4096
v_mfma_f32_16x16x32_bf16 a[116:119],a[232:235],v[232:235],a[116:119]
ds_read_b64_tr_b16 a[196:197],v2 offset:8192
v_mul_f32 v114,v23,v114
v_mfma_f32_16x16x32_bf16 a[120:123],a[240:243],v[232:235],0
ds_read_b64_tr_b16 a[198:199],v2 offset:12288
v_mul_f32 v115,v23,v115
v_mfma_f32_16x16x32_bf16 a[120:123],a[244:247],v[236:239],a[120:123]
ds_read_b64_tr_b16 a[204:205],v3 offset:8192
v_mul_f32 v16,v23,v16
v_mfma_f32_16x16x32_bf16 a[124:127],a[252:255],v[236:239],0
ds_read_b64_tr_b16 a[206:207],v3 offset:12288
ds_read_b64_tr_b16 a[200:201],v3
v_mfma_f32_16x16x32_bf16 a[124:127],a[248:251],v[232:235],a[124:127]
ds_read_b64_tr_b16 a[202:203],v3 offset:4096
v_mul_f32 v17,v23,v17
v_smfmac_f32_16x16x64_bf16 v[60:63],v[28:31],v[232:239],v27
ds_read_b64_tr_b16 a[208:209],v2 offset:256
v_mfma_f32_16x16x32_bf16 v[200:203],a[128:131],v[144:147],v[40:43]
v_exp_f32 v136,v136
v_mfma_f32_16x16x32_bf16 v[200:203],a[132:135],v[176:179],v[200:203]
v_exp_f32 v137,v137
v_mfma_f32_16x16x32_bf16 v[200:203],a[136:139],v[208:211],v[200:203]
ds_read_b64_tr_b16 a[210:211],v2 offset:4352
v_cvt_pk_bf16_f32 v136,v136,v137
v_mfma_f32_16x16x32_bf16 v[200:203],a[140:143],v[240:243],v[200:203]
v_exp_f32 v138,v138
v_mfma_f32_16x16x32_bf16 v[204:207],a[156:159],v[240:243],v[40:43]
v_exp_f32 v139,v139
v_mfma_f32_16x16x32_bf16 v[204:207],a[152:155],v[208:211],v[204:207]
buffer_load_dwordx4 v5,s[24:27],s47 offen offset:3072 nt lds
v_mfma_f32_16x16x32_bf16 v[204:207],a[148:151],v[176:179],v[204:207]
s_or_b32 s47,s47,0x18000
v_exp_f32 v140,v140
v_mfma_f32_16x16x32_bf16 v[204:207],a[144:147],v[144:147],v[204:207]
s_addk_i32 s47,4096
v_exp_f32 v141,v141
v_mfma_f32_16x16x32_bf16 v[216:219],a[160:163],v[144:147],v[40:43]
s_and_b32 s47,s47,0x27000
v_exp_f32 v142,v142
v_mfma_f32_16x16x32_bf16 v[216:219],a[164:167],v[176:179],v[216:219]
s_or_b32 s47,s47,s46
v_exp_f32 v143,v143
v_mfma_f32_16x16x32_bf16 v[216:219],a[168:171],v[208:211],v[216:219]
ds_read_b64_tr_b16 a[212:213],v2 offset:8448
ds_read_b64_tr_b16 a[214:215],v2 offset:12544
v_mfma_f32_16x16x32_bf16 v[216:219],a[172:175],v[240:243],v[216:219]
v_cvt_pk_bf16_f32 v137,v138,v139
v_cvt_pk_bf16_f32 v138,v140,v141
v_mfma_f32_16x16x32_bf16 v[220:223],a[188:191],v[240:243],v[40:43]
ds_read_b64_tr_b16 a[220:221],v3 offset:8448
v_cvt_pk_bf16_f32 v139,v142,v143
v_mfma_f32_16x16x32_bf16 v[220:223],a[184:187],v[208:211],v[220:223]
ds_read_b64_tr_b16 a[222:223],v3 offset:12544
v_exp_f32 v152,v152
v_exp_f32 v153,v153
v_exp_f32 v154,v154
v_exp_f32 v155,v155
v_exp_f32 v156,v156
v_exp_f32 v157,v157
v_exp_f32 v158,v158
v_exp_f32 v159,v159
v_mfma_f32_16x16x32_bf16 v[220:223],a[180:183],v[176:179],v[220:223]
ds_read_b64_tr_b16 a[216:217],v3 offset:256
ds_read_b64_tr_b16 a[218:219],v3 offset:4352
.p2align 3
v_cvt_pk_bf16_f32 v140,v152,v153
v_cvt_pk_bf16_f32 v141,v154,v155
v_cvt_pk_bf16_f32 v142,v156,v157
v_cvt_pk_bf16_f32 v143,v158,v159
v_mfma_f32_16x16x32_bf16 v[220:223],a[176:179],v[144:147],v[220:223]
s_waitcnt lgkmcnt(6)
ds_read_b64_tr_b16 a[224:225],v2 offset:512
ds_read_b64_tr_b16 a[226:227],v2 offset:4608
v_mfma_f32_16x16x32_bf16 a[0:3],a[192:195],v[136:139],a[0:3]
ds_read_b64_tr_b16 a[228:229],v2 offset:8704
ds_read_b64_tr_b16 a[230:231],v2 offset:12800
v_mfma_f32_16x16x32_bf16 a[0:3],a[196:199],v[140:143],a[0:3]
ds_read_b64_tr_b16 a[236:237],v3 offset:8704
ds_read_b64_tr_b16 a[238:239],v3 offset:12800
v_mfma_f32_16x16x32_bf16 a[4:7],a[204:207],v[140:143],a[4:7]
ds_read_b64_tr_b16 a[232:233],v3 offset:512
ds_read_b64_tr_b16 a[234:235],v3 offset:4608
v_mfma_f32_16x16x32_bf16 a[4:7],a[200:203],v[136:139],a[4:7]
ds_read_b64_tr_b16 a[240:241],v2 offset:768
ds_read_b64_tr_b16 a[242:243],v2 offset:4864
v_mfma_f32_16x16x32_bf16 a[8:11],a[208:211],v[136:139],a[8:11]
s_memrealtime s[76:77]
ds_read_b64_tr_b16 a[244:245],v2 offset:8960
ds_read_b64_tr_b16 a[246:247],v2 offset:13056
s_waitcnt lgkmcnt(15)
v_mfma_f32_16x16x32_bf16 a[8:11],a[212:215],v[140:143],a[8:11]
s_waitcnt lgkmcnt(14)
ds_read_b64_tr_b16 a[252:253],v3 offset:8960
ds_read_b64_tr_b16 a[254:255],v3 offset:13056
v_mfma_f32_16x16x32_bf16 a[12:15],a[220:223],v[140:143],a[12:15]
s_waitcnt lgkmcnt(14)
ds_read_b64_tr_b16 a[248:249],v3 offset:768
ds_read_b64_tr_b16 a[250:251],v3 offset:4864
v_mfma_f32_16x16x32_bf16 a[12:15],a[216:219],v[136:139],a[12:15]
s_waitcnt lgkmcnt(14)
v_mfma_f32_16x16x32_bf16 a[16:19],a[224:227],v[136:139],a[16:19]
s_waitcnt lgkmcnt(12)
v_mfma_f32_16x16x32_bf16 a[16:19],a[228:231],v[140:143],a[16:19]
s_waitcnt lgkmcnt(10)
v_mfma_f32_16x16x32_bf16 a[20:23],a[236:239],v[140:143],a[20:23]
s_waitcnt lgkmcnt(0)
v_mfma_f32_16x16x32_bf16 a[20:23],a[232:235],v[136:139],a[20:23]
s_sub_u32 s79,s78,s76
v_exp_f32 v168,v168
v_mfma_f32_16x16x32_bf16 a[24:27],a[240:243],v[136:139],a[24:27]
s_add_u32 s78,s76,s60
v_exp_f32 v169,v169
v_mfma_f32_16x16x32_bf16 a[24:27],a[244:247],v[140:143],a[24:27]
s_cmp_gt_i32 s79,s95
s_cbranch_scc1 L_ps6_%=
L_pr6_%=:
v_exp_f32 v170,v170
v_exp_f32 v171,v171
v_mfma_f32_16x16x32_bf16 a[28:31],a[252:255],v[140:143],a[28:31]
v_cvt_pk_bf16_f32 v114,v114,v115
v_cvt_pk_bf16_f32 v168,v168,v169
v_exp_f32 v172,v172
v_exp_f32 v173,v173
v_exp_f32 v174,v174
v_exp_f32 v175,v175
v_cvt_pk_bf16_f32 v115,v16,v17
v_cvt_pk_bf16_f32 v169,v170,v171
v_exp_f32 v184,v184
v_exp_f32 v185,v185
v_exp_f32 v186,v186
v_exp_f32 v187,v187
v_exp_f32 v188,v188
v_exp_f32 v189,v189
v_exp_f32 v190,v190
v_exp_f32 v191,v191
v_mfma_f32_16x16x32_bf16 a[28:31],a[248:251],v[136:139],a[28:31]
s_cmp_lg_u64 s[56:57],0
s_cbranch_scc1 L_slow0_%=
L_sret0_%=:
v_cvt_pk_bf16_f32 v170,v172,v173
v_cvt_pk_bf16_f32 v171,v174,v175
v_add_u32 v1,s48,v1
.p2align 3
v_cvt_pk_bf16_f32 v172,v184,v185
v_cvt_pk_bf16_f32 v173,v186,v187
v_cvt_pk_bf16_f32 v174,v188,v189
v_cvt_pk_bf16_f32 v175,v190,v191
v_smfmac_f32_16x16x64_bf16 v[48:51],v[28:31],v[136:143],v27
s_mov_b32 s53,11
s_cmp_eq_u32 s40,3
s_cbranch_scc0 L_att1_%=
s_cmp_gt_i32 s18,0
s_cbranch_scc1 L_att1v0_%=
s_cmp_gt_i32 s19,0
s_cbranch_scc1 L_att1v1_%=
s_cmp_gt_i32 s100,0
s_cbranch_scc1 L_att1v2_%=
.p2align 5
L_att1v3_%=:
s_waitcnt vmcnt(8) lgkmcnt(0)
v_exp_f32 v200,v200
s_barrier
ds_read_b128 a[128:131],v1
v_add_u32 v2,s48,v2
s_mov_b32 s90,L_out_end_%=-L_pc_%=+128
v_exp_f32 v201,v201
s_add_u32 m0,s49,s51
ds_read_b128 a[132:135],v1 offset:256
v_cvt_pk_bf16_f32 v200,v200,v201
s_lshr_b32 s45,s45,1
v_exp_f32 v202,v202
ds_read_b128 a[136:139],v1 offset:512
v_add_u32 v3,s48,v3
buffer_load_dwordx4 v4,s[20:23],s47 offen nt lds
v_exp_f32 v203,v203
s_add_u32 s62,s50,32768
ds_read_b128 a[140:143],v1 offset:768
v_cvt_pk_bf16_f32 v201,v202,v203
s_and_b32 s62,s62,131071
v_exp_f32 v204,v204
s_addk_i32 s54,16384
v_exp_f32 v205,v205
s_sub_u32 s48,s62,s50
ds_read_b128 a[156:159],v1 offset:4864
v_cvt_pk_bf16_f32 v202,v204,v205
s_sub_u32 s53,s53,1
v_exp_f32 v206,v206
s_and_b32 s46,s54,0xfffc0000
v_exp_f32 v207,v207
s_mov_b32 s50,s62
ds_read_b128 a[152:155],v1 offset:4608
v_cvt_pk_bf16_f32 v203,v206,v207
s_xor_b32 s49,s50,65536
v_exp_f32 v216,v216
v_mfma_f32_16x16x32_bf16 a[32:35],a[192:195],v[168:171],a[32:35]
ds_read_b128 a[148:151],v1 offset:4352
v_mfma_f32_16x16x32_bf16 a[32:35],a[196:199],v[172:175],a[32:35]
v_exp_f32 v217,v217
v_mfma_f32_16x16x32_bf16 a[36:39],a[204:207],v[172:175],a[36:39]
ds_read_b128 a[144:147],v1 offset:4096
v_cvt_pk_bf16_f32 v204,v216,v217
v_mfma_f32_16x16x32_bf16 a[36:39],a[200:203],v[168:171],a[36:39]
v_exp_f32 v218,v218
v_mfma_f32_16x16x32_bf16 a[40:43],a[208:211],v[168:171],a[40:43]
buffer_load_dwordx4 v4,s[20:23],s47 offen offset:1024 nt lds
v_mfma_f32_16x16x32_bf16 a[40:43],a[212:215],v[172:175],a[40:43]
ds_read_b128 a[160:163],v1 offset:8192
v_mfma_f32_16x16x32_bf16 a[44:47],a[220:223],v[172:175],a[44:47]
v_exp_f32 v219,v219
v_mfma_f32_16x16x32_bf16 a[44:47],a[216:219],v[168:171],a[44:47]
ds_read_b128 a[164:167],v1 offset:8448
v_cvt_pk_bf16_f32 v205,v218,v219
v_mfma_f32_16x16x32_bf16 a[48:51],a[224:227],v[168:171],a[48:51]
v_exp_f32 v220,v220
v_mfma_f32_16x16x32_bf16 a[48:51],a[228:231],v[172:175],a[48:51]
ds_read_b128 a[168:171],v1 offset:8704
v_mfma_f32_16x16x32_bf16 a[52:55],a[236:239],v[172:175],a[52:55]
v_exp_f32 v221,v221
v_mfma_f32_16x16x32_bf16 a[52:55],a[232:235],v[168:171],a[52:55]
ds_read_b128 a[172:175],v1 offset:8960
v_cvt_pk_bf16_f32 v206,v220,v221
v_mfma_f32_16x16x32_bf16 a[56:59],a[240:243],v[168:171],a[56:59]
v_exp_f32 v222,v222
v_mfma_f32_16x16x32_bf16 a[56:59],a[244:247],v[172:175],a[56:59]
ds_read_b128 a[188:191],v1 offset:13056
v_mfma_f32_16x16x32_bf16 a[60:63],a[252:255],v[172:175],a[60:63]
v_exp_f32 v223,v223
v_mfma_f32_16x16x32_bf16 a[60:63],a[248:251],v[168:171],a[60:63]
ds_read_b128 a[184:187],v1 offset:12800
v_cvt_pk_bf16_f32 v207,v222,v223
v_smfmac_f32_16x16x64_bf16 v[52:55],v[28:31],v[168:175],v27
s_waitcnt lgkmcnt(4)
v_mfma_f32_16x16x32_bf16 v[136:139],a[128:131],v[128:131],v[32:35]
ds_read_b128 a[180:183],v1 offset:12544
v_mfma_f32_16x16x32_bf16 v[136:139],a[132:135],v[160:163],v[136:139]
buffer_load_dwordx4 v5,s[20:23],s47 offen offset:2048 nt lds
v_mfma_f32_16x16x32_bf16 v[136:139],a[136:139],v[192:195],v[136:139]
ds_read_b128 a[176:179],v1 offset:12288
v_add_u32 v1,s48,v1
v_mfma_f32_16x16x32_bf16 v[136:139],a[140:143],v[224:227],v[136:139]
v_mfma_f32_16x16x32_bf16 v[140:143],a[156:159],v[224:227],v[32:35]
)A"
R"A(v_mfma_f32_16x16x32_bf16 v[140:143],a[152:155],v[192:195],v[140:143]
v_mfma_f32_16x16x32_bf16 v[140:143],a[148:151],v[160:163],v[140:143]
v_mfma_f32_16x16x32_bf16 v[140:143],a[144:147],v[128:131],v[140:143]
v_mfma_f32_16x16x32_bf16 v[152:155],a[160:163],v[128:131],v[32:35]
v_mfma_f32_16x16x32_bf16 v[152:155],a[164:167],v[160:163],v[152:155]
s_waitcnt lgkmcnt(1)
v_mfma_f32_16x16x32_bf16 v[152:155],a[168:171],v[192:195],v[152:155]
v_mfma_f32_16x16x32_bf16 v[152:155],a[172:175],v[224:227],v[152:155]
v_mfma_f32_16x16x32_bf16 v[156:159],a[188:191],v[224:227],v[32:35]
v_mfma_f32_16x16x32_bf16 v[156:159],a[184:187],v[192:195],v[156:159]
v_mfma_f32_16x16x32_bf16 v[156:159],a[180:183],v[160:163],v[156:159]
s_waitcnt lgkmcnt(0)
v_mfma_f32_16x16x32_bf16 v[156:159],a[176:179],v[128:131],v[156:159]
buffer_load_dwordx4 v5,s[20:23],s47 offen offset:3072 nt lds
v_mfma_f32_16x16x32_bf16 a[64:67],a[192:195],v[200:203],a[64:67]
v_mfma_f32_16x16x32_bf16 a[64:67],a[196:199],v[204:207],a[64:67]
v_mfma_f32_16x16x32_bf16 a[68:71],a[204:207],v[204:207],a[68:71]
v_mfma_f32_16x16x32_bf16 a[68:71],a[200:203],v[200:203],a[68:71]
v_mfma_f32_16x16x32_bf16 a[72:75],a[208:211],v[200:203],a[72:75]
v_exp_f32 v136,v136
v_mfma_f32_16x16x32_bf16 a[72:75],a[212:215],v[204:207],a[72:75]
v_exp_f32 v137,v137
v_mfma_f32_16x16x32_bf16 a[76:79],a[220:223],v[204:207],a[76:79]
v_exp_f32 v138,v138
v_mfma_f32_16x16x32_bf16 a[76:79],a[216:219],v[200:203],a[76:79]
v_exp_f32 v139,v139
v_mfma_f32_16x16x32_bf16 a[80:83],a[224:227],v[200:203],a[80:83]
v_exp_f32 v140,v140
v_mfma_f32_16x16x32_bf16 a[80:83],a[228:231],v[204:207],a[80:83]
v_exp_f32 v141,v141
v_mfma_f32_16x16x32_bf16 a[84:87],a[236:239],v[204:207],a[84:87]
s_addk_i32 m0,16384
v_exp_f32 v142,v142
v_mfma_f32_16x16x32_bf16 a[84:87],a[232:235],v[200:203],a[84:87]
v_exp_f32 v143,v143
v_mfma_f32_16x16x32_bf16 a[88:91],a[240:243],v[200:203],a[88:91]
v_exp_f32 v152,v152
v_mfma_f32_16x16x32_bf16 a[88:91],a[244:247],v[204:207],a[88:91]
buffer_load_dwordx4 v4,s[24:27],s47 offen nt lds
v_mfma_f32_16x16x32_bf16 a[92:95],a[252:255],v[204:207],a[92:95]
v_mfma_f32_16x16x32_bf16 a[92:95],a[248:251],v[200:203],a[92:95]
v_smfmac_f32_16x16x64_bf16 v[56:59],v[28:31],v[200:207],v27
v_mfma_f32_16x16x32_bf16 v[168:171],a[128:131],v[132:135],v[36:39]
v_mfma_f32_16x16x32_bf16 v[168:171],a[132:135],v[164:167],v[168:171]
v_mfma_f32_16x16x32_bf16 v[168:171],a[136:139],v[196:199],v[168:171]
v_exp_f32 v153,v153
v_mfma_f32_16x16x32_bf16 v[168:171],a[140:143],v[228:231],v[168:171]
v_exp_f32 v154,v154
v_mfma_f32_16x16x32_bf16 v[172:175],a[156:159],v[228:231],v[36:39]
v_exp_f32 v155,v155
v_mfma_f32_16x16x32_bf16 v[172:175],a[152:155],v[196:199],v[172:175]
v_exp_f32 v156,v156
v_mfma_f32_16x16x32_bf16 v[172:175],a[148:151],v[164:167],v[172:175]
v_exp_f32 v157,v157
v_mfma_f32_16x16x32_bf16 v[172:175],a[144:147],v[132:135],v[172:175]
v_exp_f32 v158,v158
v_mfma_f32_16x16x32_bf16 v[184:187],a[160:163],v[132:135],v[36:39]
v_exp_f32 v159,v159
v_mfma_f32_16x16x32_bf16 v[184:187],a[164:167],v[164:167],v[184:187]
v_exp_f32 v168,v168
v_mfma_f32_16x16x32_bf16 v[184:187],a[168:171],v[196:199],v[184:187]
buffer_load_dwordx4 v5,s[24:27],s47 offen offset:1024 nt lds
v_mfma_f32_16x16x32_bf16 v[184:187],a[172:175],v[228:231],v[184:187]
v_exp_f32 v169,v169
v_mfma_f32_16x16x32_bf16 v[188:191],a[188:191],v[228:231],v[36:39]
v_exp_f32 v170,v170
v_mfma_f32_16x16x32_bf16 v[188:191],a[184:187],v[196:199],v[188:191]
v_exp_f32 v171,v171
v_mfma_f32_16x16x32_bf16 v[188:191],a[180:183],v[164:167],v[188:191]
v_exp_f32 v172,v172
v_mfma_f32_16x16x32_bf16 v[188:191],a[176:179],v[132:135],v[188:191]
v_exp_f32 v173,v173
ds_read_b64_tr_b16 a[192:193],v2
v_cvt_pk_bf16_f32 v168,v168,v169
ds_read_b64_tr_b16 a[194:195],v2 offset:4096
v_cvt_pk_bf16_f32 v169,v170,v171
ds_read_b64_tr_b16 a[196:197],v2 offset:8192
v_cvt_pk_bf16_f32 v170,v172,v173
v_exp_f32 v174,v174
v_exp_f32 v175,v175
ds_read_b64_tr_b16 a[198:199],v2 offset:12288
v_cvt_pk_bf16_f32 v171,v174,v175
v_exp_f32 v184,v184
v_exp_f32 v185,v185
buffer_load_dwordx4 v4,s[24:27],s47 offen offset:2048 nt lds
ds_read_b64_tr_b16 a[204:205],v3 offset:8192
v_cvt_pk_bf16_f32 v172,v184,v185
ds_read_b64_tr_b16 a[206:207],v3 offset:12288
v_cvt_pk_bf16_f32 v136,v136,v137
v_exp_f32 v186,v186
ds_read_b64_tr_b16 a[200:201],v3
ds_read_b64_tr_b16 a[202:203],v3 offset:4096
v_cvt_pk_bf16_f32 v137,v138,v139
ds_read_b64_tr_b16 a[208:209],v2 offset:256
ds_read_b64_tr_b16 a[210:211],v2 offset:4352
ds_read_b64_tr_b16 a[212:213],v2 offset:8448
v_mfma_f32_16x16x32_bf16 v[200:203],a[128:131],v[144:147],v[40:43]
ds_read_b64_tr_b16 a[214:215],v2 offset:12544
v_cvt_pk_bf16_f32 v138,v140,v141
v_mfma_f32_16x16x32_bf16 v[200:203],a[132:135],v[176:179],v[200:203]
ds_read_b64_tr_b16 a[220:221],v3 offset:8448
v_mfma_f32_16x16x32_bf16 v[200:203],a[136:139],v[208:211],v[200:203]
ds_read_b64_tr_b16 a[222:223],v3 offset:12544
v_cvt_pk_bf16_f32 v139,v142,v143
v_mfma_f32_16x16x32_bf16 v[200:203],a[140:143],v[240:243],v[200:203]
ds_read_b64_tr_b16 a[216:217],v3 offset:256
v_mfma_f32_16x16x32_bf16 v[204:207],a[156:159],v[240:243],v[40:43]
ds_read_b64_tr_b16 a[218:219],v3 offset:4352
v_mfma_f32_16x16x32_bf16 v[204:207],a[152:155],v[208:211],v[204:207]
buffer_load_dwordx4 v5,s[24:27],s47 offen offset:3072 nt lds
v_mfma_f32_16x16x32_bf16 v[204:207],a[148:151],v[176:179],v[204:207]
s_or_b32 s47,s47,0x18000
ds_read_b64_tr_b16 a[224:225],v2 offset:512
v_cvt_pk_bf16_f32 v140,v152,v153
v_mfma_f32_16x16x32_bf16 v[204:207],a[144:147],v[144:147],v[204:207]
s_addk_i32 s47,4096
s_and_b32 s47,s47,0x27000
ds_read_b64_tr_b16 a[226:227],v2 offset:4608
v_mfma_f32_16x16x32_bf16 v[216:219],a[160:163],v[144:147],v[40:43]
s_or_b32 s47,s47,s46
ds_read_b64_tr_b16 a[228:229],v2 offset:8704
v_cvt_pk_bf16_f32 v141,v154,v155
v_mfma_f32_16x16x32_bf16 v[216:219],a[164:167],v[176:179],v[216:219]
ds_read_b64_tr_b16 a[230:231],v2 offset:12800
v_mfma_f32_16x16x32_bf16 v[216:219],a[168:171],v[208:211],v[216:219]
ds_read_b64_tr_b16 a[236:237],v3 offset:8704
v_mfma_f32_16x16x32_bf16 v[216:219],a[172:175],v[240:243],v[216:219]
ds_read_b64_tr_b16 a[238:239],v3 offset:12800
v_cvt_pk_bf16_f32 v142,v156,v157
v_mfma_f32_16x16x32_bf16 v[220:223],a[188:191],v[240:243],v[40:43]
ds_read_b64_tr_b16 a[232:233],v3 offset:512
v_mfma_f32_16x16x32_bf16 v[220:223],a[184:187],v[208:211],v[220:223]
ds_read_b64_tr_b16 a[234:235],v3 offset:4608
v_mfma_f32_16x16x32_bf16 v[220:223],a[180:183],v[176:179],v[220:223]
ds_read_b64_tr_b16 a[240:241],v2 offset:768
v_cvt_pk_bf16_f32 v143,v158,v159
v_mfma_f32_16x16x32_bf16 v[220:223],a[176:179],v[144:147],v[220:223]
s_waitcnt lgkmcnt(7)
buffer_load_dword v26,v24,s[88:91],s93 offen
v_mfma_f32_16x16x32_bf16 a[0:3],a[192:195],v[136:139],a[0:3]
ds_read_b64_tr_b16 a[242:243],v2 offset:4864
v_mfma_f32_16x16x32_bf16 a[0:3],a[196:199],v[140:143],a[0:3]
ds_read_b64_tr_b16 a[244:245],v2 offset:8960
v_mfma_f32_16x16x32_bf16 a[4:7],a[204:207],v[140:143],a[4:7]
ds_read_b64_tr_b16 a[246:247],v2 offset:13056
v_mfma_f32_16x16x32_bf16 a[4:7],a[200:203],v[136:139],a[4:7]
ds_read_b64_tr_b16 a[252:253],v3 offset:8960
v_mfma_f32_16x16x32_bf16 a[8:11],a[208:211],v[136:139],a[8:11]
ds_read_b64_tr_b16 a[254:255],v3 offset:13056
v_mfma_f32_16x16x32_bf16 a[8:11],a[212:215],v[140:143],a[8:11]
ds_read_b64_tr_b16 a[248:249],v3 offset:768
v_mfma_f32_16x16x32_bf16 a[12:15],a[220:223],v[140:143],a[12:15]
ds_read_b64_tr_b16 a[250:251],v3 offset:4864
v_mfma_f32_16x16x32_bf16 a[12:15],a[216:219],v[136:139],a[12:15]
s_bitcmp1_b32 s45,0
s_cbranch_scc0 L_t31v31_%=
s_mov_b32 m0,s80
s_addk_i32 s80,4096
buffer_load_dwordx4 v25,s[24:27],s83 offen nt lds
s_addk_i32 s83,1024
L_t31v31_%=:
v_mfma_f32_16x16x32_bf16 a[16:19],a[224:227],v[136:139],a[16:19]
s_waitcnt lgkmcnt(6)
v_exp_f32 v187,v187
v_mfma_f32_16x16x32_bf16 a[16:19],a[228:231],v[140:143],a[16:19]
v_exp_f32 v188,v188
v_mfma_f32_16x16x32_bf16 a[20:23],a[236:239],v[140:143],a[20:23]
v_exp_f32 v189,v189
v_mfma_f32_16x16x32_bf16 a[20:23],a[232:235],v[136:139],a[20:23]
v_exp_f32 v190,v190
v_mfma_f32_16x16x32_bf16 a[24:27],a[240:243],v[136:139],a[24:27]
s_waitcnt lgkmcnt(2)
v_exp_f32 v191,v191
v_mfma_f32_16x16x32_bf16 a[24:27],a[244:247],v[140:143],a[24:27]
v_cvt_pk_bf16_f32 v173,v186,v187
v_mfma_f32_16x16x32_bf16 a[28:31],a[252:255],v[140:143],a[28:31]
s_waitcnt lgkmcnt(0)
v_cvt_pk_bf16_f32 v174,v188,v189
v_cvt_pk_bf16_f32 v175,v190,v191
v_mfma_f32_16x16x32_bf16 a[28:31],a[248:251],v[136:139],a[28:31]
s_cmp_lg_u32 s53,0
v_smfmac_f32_16x16x64_bf16 v[48:51],v[28:31],v[136:143],v27
s_cbranch_scc1 L_att1v3_%=
s_waitcnt lgkmcnt(0)
s_nop 7
s_branch L_att1x_%=
.p2align 5
L_att1v0_%=:
v_mfma_f32_16x16x32_bf16 v[232:235],a[128:131],v[148:151],v[44:47]
s_waitcnt vmcnt(8) lgkmcnt(0)
v_exp_f32 v200,v200
v_mfma_f32_16x16x32_bf16 v[232:235],a[132:135],v[180:183],v[232:235]
s_barrier
ds_read_b128 a[128:131],v1
v_add_u32 v2,s48,v2
v_mfma_f32_16x16x32_bf16 v[232:235],a[136:139],v[212:215],v[232:235]
s_mov_b32 s90,L_out_end_%=-L_pc_%=+128
v_exp_f32 v201,v201
v_mfma_f32_16x16x32_bf16 v[232:235],a[140:143],v[244:247],v[232:235]
s_add_u32 m0,s49,s51
ds_read_b128 a[132:135],v1 offset:256
v_cvt_pk_bf16_f32 v200,v200,v201
v_mfma_f32_16x16x32_bf16 v[236:239],a[156:159],v[244:247],v[44:47]
s_lshr_b32 s45,s45,1
v_exp_f32 v202,v202
v_mfma_f32_16x16x32_bf16 v[236:239],a[152:155],v[212:215],v[236:239]
ds_read_b128 a[136:139],v1 offset:512
v_add_u32 v3,s48,v3
v_mfma_f32_16x16x32_bf16 v[236:239],a[148:151],v[180:183],v[236:239]
buffer_load_dwordx4 v4,s[20:23],s47 offen nt lds
v_mfma_f32_16x16x32_bf16 v[236:239],a[144:147],v[148:151],v[236:239]
v_exp_f32 v203,v203
v_mfma_f32_16x16x32_bf16 v[248:251],a[160:163],v[148:151],v[44:47]
s_add_u32 s62,s50,32768
ds_read_b128 a[140:143],v1 offset:768
v_cvt_pk_bf16_f32 v201,v202,v203
v_mfma_f32_16x16x32_bf16 v[248:251],a[164:167],v[180:183],v[248:251]
s_and_b32 s62,s62,131071
v_exp_f32 v204,v204
v_mfma_f32_16x16x32_bf16 v[248:251],a[168:171],v[212:215],v[248:251]
s_addk_i32 s54,16384
v_exp_f32 v205,v205
v_mfma_f32_16x16x32_bf16 v[248:251],a[172:175],v[244:247],v[248:251]
s_sub_u32 s48,s62,s50
ds_read_b128 a[156:159],v1 offset:4864
v_cvt_pk_bf16_f32 v202,v204,v205
v_mfma_f32_16x16x32_bf16 v[252:255],a[188:191],v[244:247],v[44:47]
s_sub_u32 s53,s53,1
v_exp_f32 v206,v206
v_mfma_f32_16x16x32_bf16 v[252:255],a[184:187],v[212:215],v[252:255]
s_and_b32 s46,s54,0xfffc0000
v_exp_f32 v207,v207
v_mfma_f32_16x16x32_bf16 v[252:255],a[180:183],v[180:183],v[252:255]
s_mov_b32 s50,s62
ds_read_b128 a[152:155],v1 offset:4608
v_cvt_pk_bf16_f32 v203,v206,v207
v_mfma_f32_16x16x32_bf16 v[252:255],a[176:179],v[148:151],v[252:255]
s_xor_b32 s49,s50,65536
v_exp_f32 v216,v216
v_mfma_f32_16x16x32_bf16 a[32:35],a[192:195],v[168:171],a[32:35]
ds_read_b128 a[148:151],v1 offset:4352
v_mfma_f32_16x16x32_bf16 a[32:35],a[196:199],v[172:175],a[32:35]
v_exp_f32 v217,v217
v_mfma_f32_16x16x32_bf16 a[36:39],a[204:207],v[172:175],a[36:39]
ds_read_b128 a[144:147],v1 offset:4096
v_cvt_pk_bf16_f32 v204,v216,v217
v_mfma_f32_16x16x32_bf16 a[36:39],a[200:203],v[168:171],a[36:39]
v_exp_f32 v218,v218
v_mfma_f32_16x16x32_bf16 a[40:43],a[208:211],v[168:171],a[40:43]
buffer_load_dwordx4 v4,s[20:23],s47 offen offset:1024 nt lds
v_mfma_f32_16x16x32_bf16 a[40:43],a[212:215],v[172:175],a[40:43]
ds_read_b128 a[160:163],v1 offset:8192
v_mfma_f32_16x16x32_bf16 a[44:47],a[220:223],v[172:175],a[44:47]
v_exp_f32 v219,v219
v_mfma_f32_16x16x32_bf16 a[44:47],a[216:219],v[168:171],a[44:47]
ds_read_b128 a[164:167],v1 offset:8448
v_cvt_pk_bf16_f32 v205,v218,v219
v_mfma_f32_16x16x32_bf16 a[48:51],a[224:227],v[168:171],a[48:51]
v_exp_f32 v220,v220
v_mfma_f32_16x16x32_bf16 a[48:51],a[228:231],v[172:175],a[48:51]
ds_read_b128 a[168:171],v1 offset:8704
v_mfma_f32_16x16x32_bf16 a[52:55],a[236:239],v[172:175],a[52:55]
v_exp_f32 v221,v221
v_mfma_f32_16x16x32_bf16 a[52:55],a[232:235],v[168:171],a[52:55]
ds_read_b128 a[172:175],v1 offset:8960
v_cvt_pk_bf16_f32 v206,v220,v221
v_mfma_f32_16x16x32_bf16 a[56:59],a[240:243],v[168:171],a[56:59]
v_exp_f32 v222,v222
v_mfma_f32_16x16x32_bf16 a[56:59],a[244:247],v[172:175],a[56:59]
)A"
R"A(ds_read_b128 a[188:191],v1 offset:13056
v_mfma_f32_16x16x32_bf16 a[60:63],a[252:255],v[172:175],a[60:63]
v_exp_f32 v223,v223
v_mfma_f32_16x16x32_bf16 a[60:63],a[248:251],v[168:171],a[60:63]
ds_read_b128 a[184:187],v1 offset:12800
v_cvt_pk_bf16_f32 v207,v222,v223
v_smfmac_f32_16x16x64_bf16 v[52:55],v[28:31],v[168:175],v27
ds_read_b128 a[180:183],v1 offset:12544
buffer_load_dwordx4 v5,s[20:23],s47 offen offset:2048 nt lds
ds_read_b128 a[176:179],v1 offset:12288
v_add_u32 v1,s48,v1
v_exp_f32 v232,v232
v_exp_f32 v233,v233
v_exp_f32 v234,v234
v_exp_f32 v235,v235
v_exp_f32 v236,v236
v_exp_f32 v237,v237
v_exp_f32 v238,v238
v_exp_f32 v239,v239
v_exp_f32 v248,v248
v_exp_f32 v249,v249
v_exp_f32 v250,v250
v_exp_f32 v251,v251
buffer_load_dwordx4 v5,s[20:23],s47 offen offset:3072 nt lds
v_mfma_f32_16x16x32_bf16 a[64:67],a[192:195],v[200:203],a[64:67]
v_exp_f32 v252,v252
v_mfma_f32_16x16x32_bf16 a[64:67],a[196:199],v[204:207],a[64:67]
v_exp_f32 v253,v253
v_mfma_f32_16x16x32_bf16 a[68:71],a[204:207],v[204:207],a[68:71]
v_exp_f32 v254,v254
v_mfma_f32_16x16x32_bf16 a[68:71],a[200:203],v[200:203],a[68:71]
v_exp_f32 v255,v255
v_mfma_f32_16x16x32_bf16 a[72:75],a[208:211],v[200:203],a[72:75]
v_mfma_f32_16x16x32_bf16 a[72:75],a[212:215],v[204:207],a[72:75]
v_mfma_f32_16x16x32_bf16 a[76:79],a[220:223],v[204:207],a[76:79]
v_mfma_f32_16x16x32_bf16 a[76:79],a[216:219],v[200:203],a[76:79]
v_mfma_f32_16x16x32_bf16 a[80:83],a[224:227],v[200:203],a[80:83]
v_mfma_f32_16x16x32_bf16 a[80:83],a[228:231],v[204:207],a[80:83]
v_mfma_f32_16x16x32_bf16 a[84:87],a[236:239],v[204:207],a[84:87]
s_addk_i32 m0,16384
v_mfma_f32_16x16x32_bf16 a[84:87],a[232:235],v[200:203],a[84:87]
v_mfma_f32_16x16x32_bf16 a[88:91],a[240:243],v[200:203],a[88:91]
v_mfma_f32_16x16x32_bf16 a[88:91],a[244:247],v[204:207],a[88:91]
buffer_load_dwordx4 v4,s[24:27],s47 offen nt lds
v_mfma_f32_16x16x32_bf16 a[92:95],a[252:255],v[204:207],a[92:95]
v_cvt_pk_bf16_f32 v232,v232,v233
v_cvt_pk_bf16_f32 v233,v234,v235
v_mfma_f32_16x16x32_bf16 a[92:95],a[248:251],v[200:203],a[92:95]
v_cvt_pk_bf16_f32 v234,v236,v237
v_cvt_pk_bf16_f32 v235,v238,v239
v_smfmac_f32_16x16x64_bf16 v[56:59],v[28:31],v[200:207],v27
s_waitcnt lgkmcnt(0)
v_cvt_pk_bf16_f32 v236,v248,v249
v_mfma_f32_16x16x32_bf16 v[168:171],a[128:131],v[132:135],v[36:39]
v_cvt_pk_bf16_f32 v237,v250,v251
v_cvt_pk_bf16_f32 v238,v252,v253
v_mfma_f32_16x16x32_bf16 v[168:171],a[132:135],v[164:167],v[168:171]
v_cvt_pk_bf16_f32 v239,v254,v255
v_mfma_f32_16x16x32_bf16 v[168:171],a[136:139],v[196:199],v[168:171]
v_mfma_f32_16x16x32_bf16 v[168:171],a[140:143],v[228:231],v[168:171]
v_mfma_f32_16x16x32_bf16 v[172:175],a[156:159],v[228:231],v[36:39]
v_mfma_f32_16x16x32_bf16 v[172:175],a[152:155],v[196:199],v[172:175]
v_mfma_f32_16x16x32_bf16 v[172:175],a[148:151],v[164:167],v[172:175]
v_mfma_f32_16x16x32_bf16 v[172:175],a[144:147],v[132:135],v[172:175]
v_mfma_f32_16x16x32_bf16 v[184:187],a[160:163],v[132:135],v[36:39]
v_mfma_f32_16x16x32_bf16 v[184:187],a[164:167],v[164:167],v[184:187]
s_nop 1
v_exp_f32 v168,v168
v_mfma_f32_16x16x32_bf16 v[184:187],a[168:171],v[196:199],v[184:187]
buffer_load_dwordx4 v5,s[24:27],s47 offen offset:1024 nt lds
v_mfma_f32_16x16x32_bf16 v[184:187],a[172:175],v[228:231],v[184:187]
v_exp_f32 v169,v169
v_mfma_f32_16x16x32_bf16 v[188:191],a[188:191],v[228:231],v[36:39]
v_exp_f32 v170,v170
v_mfma_f32_16x16x32_bf16 v[188:191],a[184:187],v[196:199],v[188:191]
v_exp_f32 v171,v171
v_mfma_f32_16x16x32_bf16 v[188:191],a[180:183],v[164:167],v[188:191]
v_exp_f32 v172,v172
v_mfma_f32_16x16x32_bf16 v[188:191],a[176:179],v[132:135],v[188:191]
v_exp_f32 v173,v173
v_mfma_f32_16x16x32_bf16 a[96:99],a[192:195],v[232:235],a[96:99]
ds_read_b64_tr_b16 a[192:193],v2
v_cvt_pk_bf16_f32 v168,v168,v169
v_mfma_f32_16x16x32_bf16 a[96:99],a[196:199],v[236:239],a[96:99]
ds_read_b64_tr_b16 a[194:195],v2 offset:4096
v_cvt_pk_bf16_f32 v169,v170,v171
v_mfma_f32_16x16x32_bf16 a[100:103],a[204:207],v[236:239],a[100:103]
ds_read_b64_tr_b16 a[196:197],v2 offset:8192
v_cvt_pk_bf16_f32 v170,v172,v173
v_mfma_f32_16x16x32_bf16 a[100:103],a[200:203],v[232:235],a[100:103]
v_exp_f32 v174,v174
v_mfma_f32_16x16x32_bf16 a[104:107],a[208:211],v[232:235],a[104:107]
v_exp_f32 v175,v175
v_mfma_f32_16x16x32_bf16 a[104:107],a[212:215],v[236:239],a[104:107]
ds_read_b64_tr_b16 a[198:199],v2 offset:12288
v_cvt_pk_bf16_f32 v171,v174,v175
v_mfma_f32_16x16x32_bf16 a[108:111],a[220:223],v[236:239],a[108:111]
v_exp_f32 v184,v184
v_mfma_f32_16x16x32_bf16 a[108:111],a[216:219],v[232:235],a[108:111]
v_exp_f32 v185,v185
v_mfma_f32_16x16x32_bf16 a[112:115],a[224:227],v[232:235],a[112:115]
buffer_load_dwordx4 v4,s[24:27],s47 offen offset:2048 nt lds
v_mfma_f32_16x16x32_bf16 a[112:115],a[228:231],v[236:239],a[112:115]
ds_read_b64_tr_b16 a[204:205],v3 offset:8192
v_cvt_pk_bf16_f32 v172,v184,v185
v_mfma_f32_16x16x32_bf16 a[116:119],a[236:239],v[236:239],a[116:119]
ds_read_b64_tr_b16 a[206:207],v3 offset:12288
v_mfma_f32_16x16x32_bf16 a[116:119],a[232:235],v[232:235],a[116:119]
v_exp_f32 v186,v186
v_mfma_f32_16x16x32_bf16 a[120:123],a[240:243],v[232:235],a[120:123]
ds_read_b64_tr_b16 a[200:201],v3
v_mfma_f32_16x16x32_bf16 a[120:123],a[244:247],v[236:239],a[120:123]
ds_read_b64_tr_b16 a[202:203],v3 offset:4096
v_mfma_f32_16x16x32_bf16 a[124:127],a[252:255],v[236:239],a[124:127]
ds_read_b64_tr_b16 a[208:209],v2 offset:256
v_mfma_f32_16x16x32_bf16 a[124:127],a[248:251],v[232:235],a[124:127]
ds_read_b64_tr_b16 a[210:211],v2 offset:4352
v_smfmac_f32_16x16x64_bf16 v[60:63],v[28:31],v[232:239],v27
ds_read_b64_tr_b16 a[212:213],v2 offset:8448
v_mfma_f32_16x16x32_bf16 v[200:203],a[128:131],v[144:147],v[40:43]
ds_read_b64_tr_b16 a[214:215],v2 offset:12544
v_mfma_f32_16x16x32_bf16 v[200:203],a[132:135],v[176:179],v[200:203]
ds_read_b64_tr_b16 a[220:221],v3 offset:8448
v_mfma_f32_16x16x32_bf16 v[200:203],a[136:139],v[208:211],v[200:203]
ds_read_b64_tr_b16 a[222:223],v3 offset:12544
v_mfma_f32_16x16x32_bf16 v[200:203],a[140:143],v[240:243],v[200:203]
ds_read_b64_tr_b16 a[216:217],v3 offset:256
v_mfma_f32_16x16x32_bf16 v[204:207],a[156:159],v[240:243],v[40:43]
ds_read_b64_tr_b16 a[218:219],v3 offset:4352
v_mfma_f32_16x16x32_bf16 v[204:207],a[152:155],v[208:211],v[204:207]
buffer_load_dwordx4 v5,s[24:27],s47 offen offset:3072 nt lds
v_mfma_f32_16x16x32_bf16 v[204:207],a[148:151],v[176:179],v[204:207]
s_or_b32 s47,s47,0x18000
ds_read_b64_tr_b16 a[224:225],v2 offset:512
v_mfma_f32_16x16x32_bf16 v[204:207],a[144:147],v[144:147],v[204:207]
s_addk_i32 s47,4096
s_and_b32 s47,s47,0x27000
ds_read_b64_tr_b16 a[226:227],v2 offset:4608
v_mfma_f32_16x16x32_bf16 v[216:219],a[160:163],v[144:147],v[40:43]
s_or_b32 s47,s47,s46
ds_read_b64_tr_b16 a[228:229],v2 offset:8704
v_mfma_f32_16x16x32_bf16 v[216:219],a[164:167],v[176:179],v[216:219]
ds_read_b64_tr_b16 a[230:231],v2 offset:12800
v_mfma_f32_16x16x32_bf16 v[216:219],a[168:171],v[208:211],v[216:219]
ds_read_b64_tr_b16 a[236:237],v3 offset:8704
v_mfma_f32_16x16x32_bf16 v[216:219],a[172:175],v[240:243],v[216:219]
ds_read_b64_tr_b16 a[238:239],v3 offset:12800
v_mfma_f32_16x16x32_bf16 v[220:223],a[188:191],v[240:243],v[40:43]
ds_read_b64_tr_b16 a[232:233],v3 offset:512
v_mfma_f32_16x16x32_bf16 v[220:223],a[184:187],v[208:211],v[220:223]
ds_read_b64_tr_b16 a[234:235],v3 offset:4608
v_mfma_f32_16x16x32_bf16 v[220:223],a[180:183],v[176:179],v[220:223]
ds_read_b64_tr_b16 a[240:241],v2 offset:768
v_mfma_f32_16x16x32_bf16 v[220:223],a[176:179],v[144:147],v[220:223]
buffer_load_dword v26,v24,s[88:91],s93 offen
ds_read_b64_tr_b16 a[242:243],v2 offset:4864
ds_read_b64_tr_b16 a[244:245],v2 offset:8960
ds_read_b64_tr_b16 a[246:247],v2 offset:13056
ds_read_b64_tr_b16 a[252:253],v3 offset:8960
ds_read_b64_tr_b16 a[254:255],v3 offset:13056
ds_read_b64_tr_b16 a[248:249],v3 offset:768
ds_read_b64_tr_b16 a[250:251],v3 offset:4864
s_bitcmp1_b32 s45,0
s_cbranch_scc0 L_t31v01_%=
s_mov_b32 m0,s80
s_addk_i32 s80,4096
buffer_load_dwordx4 v25,s[24:27],s83 offen nt lds
s_addk_i32 s83,1024
L_t31v01_%=:
v_exp_f32 v187,v187
v_exp_f32 v188,v188
v_exp_f32 v189,v189
v_exp_f32 v190,v190
v_exp_f32 v191,v191
.p2align 3
v_cvt_pk_bf16_f32 v173,v186,v187
v_cvt_pk_bf16_f32 v174,v188,v189
v_cvt_pk_bf16_f32 v175,v190,v191
s_cmp_lg_u32 s53,0
s_cbranch_scc1 L_att1v0_%=
s_waitcnt lgkmcnt(0)
s_nop 7
s_branch L_att1x_%=
.p2align 5
L_att1v1_%=:
v_mfma_f32_16x16x32_bf16 v[232:235],a[128:131],v[148:151],v[44:47]
s_waitcnt vmcnt(8) lgkmcnt(0)
v_exp_f32 v200,v200
v_mfma_f32_16x16x32_bf16 v[232:235],a[132:135],v[180:183],v[232:235]
s_barrier
ds_read_b128 a[128:131],v1
v_add_u32 v2,s48,v2
v_mfma_f32_16x16x32_bf16 v[232:235],a[136:139],v[212:215],v[232:235]
s_mov_b32 s90,L_out_end_%=-L_pc_%=+128
v_exp_f32 v201,v201
v_mfma_f32_16x16x32_bf16 v[232:235],a[140:143],v[244:247],v[232:235]
s_add_u32 m0,s49,s51
ds_read_b128 a[132:135],v1 offset:256
v_cvt_pk_bf16_f32 v200,v200,v201
v_mfma_f32_16x16x32_bf16 v[236:239],a[156:159],v[244:247],v[44:47]
s_lshr_b32 s45,s45,1
v_exp_f32 v202,v202
v_mfma_f32_16x16x32_bf16 v[236:239],a[152:155],v[212:215],v[236:239]
ds_read_b128 a[136:139],v1 offset:512
v_add_u32 v3,s48,v3
v_mfma_f32_16x16x32_bf16 v[236:239],a[148:151],v[180:183],v[236:239]
buffer_load_dwordx4 v4,s[20:23],s47 offen nt lds
v_mfma_f32_16x16x32_bf16 v[236:239],a[144:147],v[148:151],v[236:239]
v_exp_f32 v203,v203
v_mfma_f32_16x16x32_bf16 v[248:251],a[160:163],v[148:151],v[44:47]
s_add_u32 s62,s50,32768
ds_read_b128 a[140:143],v1 offset:768
v_cvt_pk_bf16_f32 v201,v202,v203
v_mfma_f32_16x16x32_bf16 v[248:251],a[164:167],v[180:183],v[248:251]
s_and_b32 s62,s62,131071
v_exp_f32 v204,v204
v_mfma_f32_16x16x32_bf16 v[248:251],a[168:171],v[212:215],v[248:251]
s_addk_i32 s54,16384
v_exp_f32 v205,v205
v_mfma_f32_16x16x32_bf16 v[248:251],a[172:175],v[244:247],v[248:251]
s_sub_u32 s48,s62,s50
ds_read_b128 a[156:159],v1 offset:4864
v_cvt_pk_bf16_f32 v202,v204,v205
v_mfma_f32_16x16x32_bf16 v[252:255],a[188:191],v[244:247],v[44:47]
s_sub_u32 s53,s53,1
v_exp_f32 v206,v206
v_mfma_f32_16x16x32_bf16 v[252:255],a[184:187],v[212:215],v[252:255]
s_and_b32 s46,s54,0xfffc0000
v_exp_f32 v207,v207
v_mfma_f32_16x16x32_bf16 v[252:255],a[180:183],v[180:183],v[252:255]
s_mov_b32 s50,s62
ds_read_b128 a[152:155],v1 offset:4608
v_cvt_pk_bf16_f32 v203,v206,v207
v_mfma_f32_16x16x32_bf16 v[252:255],a[176:179],v[148:151],v[252:255]
s_xor_b32 s49,s50,65536
v_exp_f32 v216,v216
ds_read_b128 a[148:151],v1 offset:4352
v_exp_f32 v217,v217
ds_read_b128 a[144:147],v1 offset:4096
v_cvt_pk_bf16_f32 v204,v216,v217
v_exp_f32 v218,v218
buffer_load_dwordx4 v4,s[20:23],s47 offen offset:1024 nt lds
ds_read_b128 a[160:163],v1 offset:8192
v_exp_f32 v219,v219
ds_read_b128 a[164:167],v1 offset:8448
v_cvt_pk_bf16_f32 v205,v218,v219
v_exp_f32 v220,v220
ds_read_b128 a[168:171],v1 offset:8704
v_exp_f32 v221,v221
ds_read_b128 a[172:175],v1 offset:8960
v_cvt_pk_bf16_f32 v206,v220,v221
v_exp_f32 v222,v222
ds_read_b128 a[188:191],v1 offset:13056
v_exp_f32 v223,v223
ds_read_b128 a[184:187],v1 offset:12800
v_cvt_pk_bf16_f32 v207,v222,v223
s_waitcnt lgkmcnt(11)
v_mfma_f32_16x16x32_bf16 v[136:139],a[128:131],v[128:131],v[32:35]
ds_read_b128 a[180:183],v1 offset:12544
v_mfma_f32_16x16x32_bf16 v[136:139],a[132:135],v[160:163],v[136:139]
buffer_load_dwordx4 v5,s[20:23],s47 offen offset:2048 nt lds
v_mfma_f32_16x16x32_bf16 v[136:139],a[136:139],v[192:195],v[136:139]
s_waitcnt lgkmcnt(11)
ds_read_b128 a[176:179],v1 offset:12288
v_add_u32 v1,s48,v1
v_mfma_f32_16x16x32_bf16 v[136:139],a[140:143],v[224:227],v[136:139]
s_waitcnt lgkmcnt(11)
v_exp_f32 v232,v232
v_mfma_f32_16x16x32_bf16 v[140:143],a[156:159],v[224:227],v[32:35]
s_waitcnt lgkmcnt(10)
v_exp_f32 v233,v233
v_mfma_f32_16x16x32_bf16 v[140:143],a[152:155],v[192:195],v[140:143]
s_waitcnt lgkmcnt(9)
v_exp_f32 v234,v234
v_mfma_f32_16x16x32_bf16 v[140:143],a[148:151],v[160:163],v[140:143]
s_waitcnt lgkmcnt(8)
v_exp_f32 v235,v235
v_mfma_f32_16x16x32_bf16 v[140:143],a[144:147],v[128:131],v[140:143]
s_waitcnt lgkmcnt(2)
v_exp_f32 v236,v236
v_mfma_f32_16x16x32_bf16 v[152:155],a[160:163],v[128:131],v[32:35]
v_exp_f32 v237,v237
)A"
R"A(v_mfma_f32_16x16x32_bf16 v[152:155],a[164:167],v[160:163],v[152:155]
v_exp_f32 v238,v238
v_mfma_f32_16x16x32_bf16 v[152:155],a[168:171],v[192:195],v[152:155]
v_exp_f32 v239,v239
v_mfma_f32_16x16x32_bf16 v[152:155],a[172:175],v[224:227],v[152:155]
v_exp_f32 v248,v248
v_mfma_f32_16x16x32_bf16 v[156:159],a[188:191],v[224:227],v[32:35]
v_exp_f32 v249,v249
v_mfma_f32_16x16x32_bf16 v[156:159],a[184:187],v[192:195],v[156:159]
s_waitcnt lgkmcnt(0)
v_exp_f32 v250,v250
v_mfma_f32_16x16x32_bf16 v[156:159],a[180:183],v[160:163],v[156:159]
v_exp_f32 v251,v251
v_mfma_f32_16x16x32_bf16 v[156:159],a[176:179],v[128:131],v[156:159]
buffer_load_dwordx4 v5,s[20:23],s47 offen offset:3072 nt lds
v_mfma_f32_16x16x32_bf16 a[64:67],a[192:195],v[200:203],a[64:67]
v_exp_f32 v252,v252
v_mfma_f32_16x16x32_bf16 a[64:67],a[196:199],v[204:207],a[64:67]
v_exp_f32 v253,v253
v_mfma_f32_16x16x32_bf16 a[68:71],a[204:207],v[204:207],a[68:71]
v_exp_f32 v254,v254
v_mfma_f32_16x16x32_bf16 a[68:71],a[200:203],v[200:203],a[68:71]
v_exp_f32 v255,v255
v_mfma_f32_16x16x32_bf16 a[72:75],a[208:211],v[200:203],a[72:75]
v_exp_f32 v136,v136
v_mfma_f32_16x16x32_bf16 a[72:75],a[212:215],v[204:207],a[72:75]
v_exp_f32 v137,v137
v_mfma_f32_16x16x32_bf16 a[76:79],a[220:223],v[204:207],a[76:79]
v_exp_f32 v138,v138
v_mfma_f32_16x16x32_bf16 a[76:79],a[216:219],v[200:203],a[76:79]
v_exp_f32 v139,v139
v_mfma_f32_16x16x32_bf16 a[80:83],a[224:227],v[200:203],a[80:83]
v_exp_f32 v140,v140
v_mfma_f32_16x16x32_bf16 a[80:83],a[228:231],v[204:207],a[80:83]
v_exp_f32 v141,v141
v_mfma_f32_16x16x32_bf16 a[84:87],a[236:239],v[204:207],a[84:87]
s_addk_i32 m0,16384
v_exp_f32 v142,v142
v_mfma_f32_16x16x32_bf16 a[84:87],a[232:235],v[200:203],a[84:87]
v_exp_f32 v143,v143
v_mfma_f32_16x16x32_bf16 a[88:91],a[240:243],v[200:203],a[88:91]
v_exp_f32 v152,v152
v_mfma_f32_16x16x32_bf16 a[88:91],a[244:247],v[204:207],a[88:91]
buffer_load_dwordx4 v4,s[24:27],s47 offen nt lds
v_mfma_f32_16x16x32_bf16 a[92:95],a[252:255],v[204:207],a[92:95]
v_cvt_pk_bf16_f32 v232,v232,v233
v_cvt_pk_bf16_f32 v233,v234,v235
v_mfma_f32_16x16x32_bf16 a[92:95],a[248:251],v[200:203],a[92:95]
v_cvt_pk_bf16_f32 v234,v236,v237
v_cvt_pk_bf16_f32 v235,v238,v239
v_smfmac_f32_16x16x64_bf16 v[56:59],v[28:31],v[200:207],v27
.p2align 3
v_cvt_pk_bf16_f32 v236,v248,v249
v_cvt_pk_bf16_f32 v237,v250,v251
v_cvt_pk_bf16_f32 v238,v252,v253
v_cvt_pk_bf16_f32 v239,v254,v255
v_exp_f32 v153,v153
v_exp_f32 v154,v154
v_exp_f32 v155,v155
v_exp_f32 v156,v156
v_exp_f32 v157,v157
v_exp_f32 v158,v158
v_exp_f32 v159,v159
buffer_load_dwordx4 v5,s[24:27],s47 offen offset:1024 nt lds
v_mfma_f32_16x16x32_bf16 a[96:99],a[192:195],v[232:235],a[96:99]
ds_read_b64_tr_b16 a[192:193],v2
v_mfma_f32_16x16x32_bf16 a[96:99],a[196:199],v[236:239],a[96:99]
ds_read_b64_tr_b16 a[194:195],v2 offset:4096
v_mfma_f32_16x16x32_bf16 a[100:103],a[204:207],v[236:239],a[100:103]
ds_read_b64_tr_b16 a[196:197],v2 offset:8192
v_mfma_f32_16x16x32_bf16 a[100:103],a[200:203],v[232:235],a[100:103]
v_mfma_f32_16x16x32_bf16 a[104:107],a[208:211],v[232:235],a[104:107]
v_mfma_f32_16x16x32_bf16 a[104:107],a[212:215],v[236:239],a[104:107]
ds_read_b64_tr_b16 a[198:199],v2 offset:12288
v_mfma_f32_16x16x32_bf16 a[108:111],a[220:223],v[236:239],a[108:111]
v_mfma_f32_16x16x32_bf16 a[108:111],a[216:219],v[232:235],a[108:111]
v_mfma_f32_16x16x32_bf16 a[112:115],a[224:227],v[232:235],a[112:115]
buffer_load_dwordx4 v4,s[24:27],s47 offen offset:2048 nt lds
v_mfma_f32_16x16x32_bf16 a[112:115],a[228:231],v[236:239],a[112:115]
ds_read_b64_tr_b16 a[204:205],v3 offset:8192
v_mfma_f32_16x16x32_bf16 a[116:119],a[236:239],v[236:239],a[116:119]
ds_read_b64_tr_b16 a[206:207],v3 offset:12288
v_cvt_pk_bf16_f32 v136,v136,v137
v_mfma_f32_16x16x32_bf16 a[116:119],a[232:235],v[232:235],a[116:119]
v_mfma_f32_16x16x32_bf16 a[120:123],a[240:243],v[232:235],a[120:123]
ds_read_b64_tr_b16 a[200:201],v3
v_mfma_f32_16x16x32_bf16 a[120:123],a[244:247],v[236:239],a[120:123]
ds_read_b64_tr_b16 a[202:203],v3 offset:4096
v_cvt_pk_bf16_f32 v137,v138,v139
v_mfma_f32_16x16x32_bf16 a[124:127],a[252:255],v[236:239],a[124:127]
ds_read_b64_tr_b16 a[208:209],v2 offset:256
v_mfma_f32_16x16x32_bf16 a[124:127],a[248:251],v[232:235],a[124:127]
ds_read_b64_tr_b16 a[210:211],v2 offset:4352
v_smfmac_f32_16x16x64_bf16 v[60:63],v[28:31],v[232:239],v27
ds_read_b64_tr_b16 a[212:213],v2 offset:8448
v_mfma_f32_16x16x32_bf16 v[200:203],a[128:131],v[144:147],v[40:43]
ds_read_b64_tr_b16 a[214:215],v2 offset:12544
v_cvt_pk_bf16_f32 v138,v140,v141
v_mfma_f32_16x16x32_bf16 v[200:203],a[132:135],v[176:179],v[200:203]
ds_read_b64_tr_b16 a[220:221],v3 offset:8448
v_mfma_f32_16x16x32_bf16 v[200:203],a[136:139],v[208:211],v[200:203]
ds_read_b64_tr_b16 a[222:223],v3 offset:12544
v_cvt_pk_bf16_f32 v139,v142,v143
v_mfma_f32_16x16x32_bf16 v[200:203],a[140:143],v[240:243],v[200:203]
ds_read_b64_tr_b16 a[216:217],v3 offset:256
v_mfma_f32_16x16x32_bf16 v[204:207],a[156:159],v[240:243],v[40:43]
ds_read_b64_tr_b16 a[218:219],v3 offset:4352
v_mfma_f32_16x16x32_bf16 v[204:207],a[152:155],v[208:211],v[204:207]
buffer_load_dwordx4 v5,s[24:27],s47 offen offset:3072 nt lds
v_mfma_f32_16x16x32_bf16 v[204:207],a[148:151],v[176:179],v[204:207]
s_or_b32 s47,s47,0x18000
ds_read_b64_tr_b16 a[224:225],v2 offset:512
v_cvt_pk_bf16_f32 v140,v152,v153
v_mfma_f32_16x16x32_bf16 v[204:207],a[144:147],v[144:147],v[204:207]
s_addk_i32 s47,4096
s_and_b32 s47,s47,0x27000
ds_read_b64_tr_b16 a[226:227],v2 offset:4608
v_mfma_f32_16x16x32_bf16 v[216:219],a[160:163],v[144:147],v[40:43]
s_or_b32 s47,s47,s46
ds_read_b64_tr_b16 a[228:229],v2 offset:8704
v_cvt_pk_bf16_f32 v141,v154,v155
v_mfma_f32_16x16x32_bf16 v[216:219],a[164:167],v[176:179],v[216:219]
ds_read_b64_tr_b16 a[230:231],v2 offset:12800
v_mfma_f32_16x16x32_bf16 v[216:219],a[168:171],v[208:211],v[216:219]
ds_read_b64_tr_b16 a[236:237],v3 offset:8704
v_mfma_f32_16x16x32_bf16 v[216:219],a[172:175],v[240:243],v[216:219]
ds_read_b64_tr_b16 a[238:239],v3 offset:12800
v_cvt_pk_bf16_f32 v142,v156,v157
v_mfma_f32_16x16x32_bf16 v[220:223],a[188:191],v[240:243],v[40:43]
ds_read_b64_tr_b16 a[232:233],v3 offset:512
v_mfma_f32_16x16x32_bf16 v[220:223],a[184:187],v[208:211],v[220:223]
ds_read_b64_tr_b16 a[234:235],v3 offset:4608
v_mfma_f32_16x16x32_bf16 v[220:223],a[180:183],v[176:179],v[220:223]
ds_read_b64_tr_b16 a[240:241],v2 offset:768
v_cvt_pk_bf16_f32 v143,v158,v159
v_mfma_f32_16x16x32_bf16 v[220:223],a[176:179],v[144:147],v[220:223]
s_waitcnt lgkmcnt(7)
buffer_load_dword v26,v24,s[88:91],s93 offen
v_mfma_f32_16x16x32_bf16 a[0:3],a[192:195],v[136:139],a[0:3]
ds_read_b64_tr_b16 a[242:243],v2 offset:4864
v_mfma_f32_16x16x32_bf16 a[0:3],a[196:199],v[140:143],a[0:3]
ds_read_b64_tr_b16 a[244:245],v2 offset:8960
v_mfma_f32_16x16x32_bf16 a[4:7],a[204:207],v[140:143],a[4:7]
ds_read_b64_tr_b16 a[246:247],v2 offset:13056
v_mfma_f32_16x16x32_bf16 a[4:7],a[200:203],v[136:139],a[4:7]
ds_read_b64_tr_b16 a[252:253],v3 offset:8960
v_mfma_f32_16x16x32_bf16 a[8:11],a[208:211],v[136:139],a[8:11]
ds_read_b64_tr_b16 a[254:255],v3 offset:13056
v_mfma_f32_16x16x32_bf16 a[8:11],a[212:215],v[140:143],a[8:11]
ds_read_b64_tr_b16 a[248:249],v3 offset:768
v_mfma_f32_16x16x32_bf16 a[12:15],a[220:223],v[140:143],a[12:15]
ds_read_b64_tr_b16 a[250:251],v3 offset:4864
v_mfma_f32_16x16x32_bf16 a[12:15],a[216:219],v[136:139],a[12:15]
s_bitcmp1_b32 s45,0
s_cbranch_scc0 L_t31v11_%=
s_mov_b32 m0,s80
s_addk_i32 s80,4096
buffer_load_dwordx4 v25,s[24:27],s83 offen nt lds
s_addk_i32 s83,1024
L_t31v11_%=:
v_mfma_f32_16x16x32_bf16 a[16:19],a[224:227],v[136:139],a[16:19]
s_waitcnt lgkmcnt(6)
v_mfma_f32_16x16x32_bf16 a[16:19],a[228:231],v[140:143],a[16:19]
v_mfma_f32_16x16x32_bf16 a[20:23],a[236:239],v[140:143],a[20:23]
v_mfma_f32_16x16x32_bf16 a[20:23],a[232:235],v[136:139],a[20:23]
v_mfma_f32_16x16x32_bf16 a[24:27],a[240:243],v[136:139],a[24:27]
s_waitcnt lgkmcnt(2)
v_mfma_f32_16x16x32_bf16 a[24:27],a[244:247],v[140:143],a[24:27]
v_mfma_f32_16x16x32_bf16 a[28:31],a[252:255],v[140:143],a[28:31]
s_waitcnt lgkmcnt(0)
v_mfma_f32_16x16x32_bf16 a[28:31],a[248:251],v[136:139],a[28:31]
s_cmp_lg_u32 s53,0
v_smfmac_f32_16x16x64_bf16 v[48:51],v[28:31],v[136:143],v27
s_cbranch_scc1 L_att1v1_%=
s_waitcnt lgkmcnt(0)
s_nop 7
s_branch L_att1x_%=
.p2align 5
L_att1v2_%=:
v_mfma_f32_16x16x32_bf16 v[232:235],a[128:131],v[148:151],v[44:47]
s_waitcnt vmcnt(8) lgkmcnt(0)
v_mfma_f32_16x16x32_bf16 v[232:235],a[132:135],v[180:183],v[232:235]
s_barrier
ds_read_b128 a[128:131],v1
v_add_u32 v2,s48,v2
v_mfma_f32_16x16x32_bf16 v[232:235],a[136:139],v[212:215],v[232:235]
s_mov_b32 s90,L_out_end_%=-L_pc_%=+128
v_mfma_f32_16x16x32_bf16 v[232:235],a[140:143],v[244:247],v[232:235]
s_add_u32 m0,s49,s51
ds_read_b128 a[132:135],v1 offset:256
v_mfma_f32_16x16x32_bf16 v[236:239],a[156:159],v[244:247],v[44:47]
s_lshr_b32 s45,s45,1
v_mfma_f32_16x16x32_bf16 v[236:239],a[152:155],v[212:215],v[236:239]
ds_read_b128 a[136:139],v1 offset:512
v_add_u32 v3,s48,v3
v_mfma_f32_16x16x32_bf16 v[236:239],a[148:151],v[180:183],v[236:239]
buffer_load_dwordx4 v4,s[20:23],s47 offen nt lds
v_mfma_f32_16x16x32_bf16 v[236:239],a[144:147],v[148:151],v[236:239]
v_mfma_f32_16x16x32_bf16 v[248:251],a[160:163],v[148:151],v[44:47]
s_add_u32 s62,s50,32768
ds_read_b128 a[140:143],v1 offset:768
v_mfma_f32_16x16x32_bf16 v[248:251],a[164:167],v[180:183],v[248:251]
s_and_b32 s62,s62,131071
v_mfma_f32_16x16x32_bf16 v[248:251],a[168:171],v[212:215],v[248:251]
s_addk_i32 s54,16384
v_mfma_f32_16x16x32_bf16 v[248:251],a[172:175],v[244:247],v[248:251]
s_sub_u32 s48,s62,s50
ds_read_b128 a[156:159],v1 offset:4864
v_mfma_f32_16x16x32_bf16 v[252:255],a[188:191],v[244:247],v[44:47]
s_sub_u32 s53,s53,1
v_mfma_f32_16x16x32_bf16 v[252:255],a[184:187],v[212:215],v[252:255]
s_and_b32 s46,s54,0xfffc0000
v_mfma_f32_16x16x32_bf16 v[252:255],a[180:183],v[180:183],v[252:255]
s_mov_b32 s50,s62
ds_read_b128 a[152:155],v1 offset:4608
v_mfma_f32_16x16x32_bf16 v[252:255],a[176:179],v[148:151],v[252:255]
s_xor_b32 s49,s50,65536
v_mfma_f32_16x16x32_bf16 a[32:35],a[192:195],v[168:171],a[32:35]
ds_read_b128 a[148:151],v1 offset:4352
v_mfma_f32_16x16x32_bf16 a[32:35],a[196:199],v[172:175],a[32:35]
v_mfma_f32_16x16x32_bf16 a[36:39],a[204:207],v[172:175],a[36:39]
ds_read_b128 a[144:147],v1 offset:4096
v_mfma_f32_16x16x32_bf16 a[36:39],a[200:203],v[168:171],a[36:39]
v_mfma_f32_16x16x32_bf16 a[40:43],a[208:211],v[168:171],a[40:43]
buffer_load_dwordx4 v4,s[20:23],s47 offen offset:1024 nt lds
v_mfma_f32_16x16x32_bf16 a[40:43],a[212:215],v[172:175],a[40:43]
ds_read_b128 a[160:163],v1 offset:8192
v_mfma_f32_16x16x32_bf16 a[44:47],a[220:223],v[172:175],a[44:47]
v_mfma_f32_16x16x32_bf16 a[44:47],a[216:219],v[168:171],a[44:47]
ds_read_b128 a[164:167],v1 offset:8448
v_mfma_f32_16x16x32_bf16 a[48:51],a[224:227],v[168:171],a[48:51]
v_mfma_f32_16x16x32_bf16 a[48:51],a[228:231],v[172:175],a[48:51]
ds_read_b128 a[168:171],v1 offset:8704
v_mfma_f32_16x16x32_bf16 a[52:55],a[236:239],v[172:175],a[52:55]
v_mfma_f32_16x16x32_bf16 a[52:55],a[232:235],v[168:171],a[52:55]
ds_read_b128 a[172:175],v1 offset:8960
v_mfma_f32_16x16x32_bf16 a[56:59],a[240:243],v[168:171],a[56:59]
v_mfma_f32_16x16x32_bf16 a[56:59],a[244:247],v[172:175],a[56:59]
ds_read_b128 a[188:191],v1 offset:13056
v_mfma_f32_16x16x32_bf16 a[60:63],a[252:255],v[172:175],a[60:63]
v_mfma_f32_16x16x32_bf16 a[60:63],a[248:251],v[168:171],a[60:63]
ds_read_b128 a[184:187],v1 offset:12800
v_smfmac_f32_16x16x64_bf16 v[52:55],v[28:31],v[168:175],v27
s_waitcnt lgkmcnt(4)
v_mfma_f32_16x16x32_bf16 v[136:139],a[128:131],v[128:131],v[32:35]
ds_read_b128 a[180:183],v1 offset:12544
v_mfma_f32_16x16x32_bf16 v[136:139],a[132:135],v[160:163],v[136:139]
buffer_load_dwordx4 v5,s[20:23],s47 offen offset:2048 nt lds
v_mfma_f32_16x16x32_bf16 v[136:139],a[136:139],v[192:195],v[136:139]
ds_read_b128 a[176:179],v1 offset:12288
v_add_u32 v1,s48,v1
v_mfma_f32_16x16x32_bf16 v[136:139],a[140:143],v[224:227],v[136:139]
v_exp_f32 v232,v232
v_mfma_f32_16x16x32_bf16 v[140:143],a[156:159],v[224:227],v[32:35]
v_exp_f32 v233,v233
v_mfma_f32_16x16x32_bf16 v[140:143],a[152:155],v[192:195],v[140:143]
)A"
R"A(v_exp_f32 v234,v234
v_mfma_f32_16x16x32_bf16 v[140:143],a[148:151],v[160:163],v[140:143]
v_exp_f32 v235,v235
v_mfma_f32_16x16x32_bf16 v[140:143],a[144:147],v[128:131],v[140:143]
v_exp_f32 v236,v236
v_mfma_f32_16x16x32_bf16 v[152:155],a[160:163],v[128:131],v[32:35]
v_exp_f32 v237,v237
v_mfma_f32_16x16x32_bf16 v[152:155],a[164:167],v[160:163],v[152:155]
s_waitcnt lgkmcnt(1)
v_exp_f32 v238,v238
v_mfma_f32_16x16x32_bf16 v[152:155],a[168:171],v[192:195],v[152:155]
v_exp_f32 v239,v239
v_mfma_f32_16x16x32_bf16 v[152:155],a[172:175],v[224:227],v[152:155]
v_exp_f32 v248,v248
v_mfma_f32_16x16x32_bf16 v[156:159],a[188:191],v[224:227],v[32:35]
v_exp_f32 v249,v249
v_mfma_f32_16x16x32_bf16 v[156:159],a[184:187],v[192:195],v[156:159]
v_exp_f32 v250,v250
v_mfma_f32_16x16x32_bf16 v[156:159],a[180:183],v[160:163],v[156:159]
s_waitcnt lgkmcnt(0)
v_exp_f32 v251,v251
v_mfma_f32_16x16x32_bf16 v[156:159],a[176:179],v[128:131],v[156:159]
buffer_load_dwordx4 v5,s[20:23],s47 offen offset:3072 nt lds
v_exp_f32 v252,v252
v_exp_f32 v253,v253
v_exp_f32 v254,v254
v_exp_f32 v255,v255
v_exp_f32 v136,v136
v_exp_f32 v137,v137
v_exp_f32 v138,v138
v_exp_f32 v139,v139
v_exp_f32 v140,v140
v_exp_f32 v141,v141
s_addk_i32 m0,16384
v_exp_f32 v142,v142
v_exp_f32 v143,v143
v_exp_f32 v152,v152
buffer_load_dwordx4 v4,s[24:27],s47 offen nt lds
.p2align 3
v_cvt_pk_bf16_f32 v232,v232,v233
v_cvt_pk_bf16_f32 v233,v234,v235
v_cvt_pk_bf16_f32 v234,v236,v237
v_cvt_pk_bf16_f32 v235,v238,v239
v_cvt_pk_bf16_f32 v236,v248,v249
v_mfma_f32_16x16x32_bf16 v[168:171],a[128:131],v[132:135],v[36:39]
v_cvt_pk_bf16_f32 v237,v250,v251
v_cvt_pk_bf16_f32 v238,v252,v253
v_mfma_f32_16x16x32_bf16 v[168:171],a[132:135],v[164:167],v[168:171]
v_cvt_pk_bf16_f32 v239,v254,v255
v_mfma_f32_16x16x32_bf16 v[168:171],a[136:139],v[196:199],v[168:171]
v_exp_f32 v153,v153
v_mfma_f32_16x16x32_bf16 v[168:171],a[140:143],v[228:231],v[168:171]
v_exp_f32 v154,v154
v_mfma_f32_16x16x32_bf16 v[172:175],a[156:159],v[228:231],v[36:39]
v_exp_f32 v155,v155
v_mfma_f32_16x16x32_bf16 v[172:175],a[152:155],v[196:199],v[172:175]
v_exp_f32 v156,v156
v_mfma_f32_16x16x32_bf16 v[172:175],a[148:151],v[164:167],v[172:175]
v_exp_f32 v157,v157
v_mfma_f32_16x16x32_bf16 v[172:175],a[144:147],v[132:135],v[172:175]
v_exp_f32 v158,v158
v_mfma_f32_16x16x32_bf16 v[184:187],a[160:163],v[132:135],v[36:39]
v_exp_f32 v159,v159
v_mfma_f32_16x16x32_bf16 v[184:187],a[164:167],v[164:167],v[184:187]
v_exp_f32 v168,v168
v_mfma_f32_16x16x32_bf16 v[184:187],a[168:171],v[196:199],v[184:187]
buffer_load_dwordx4 v5,s[24:27],s47 offen offset:1024 nt lds
v_mfma_f32_16x16x32_bf16 v[184:187],a[172:175],v[228:231],v[184:187]
v_exp_f32 v169,v169
v_mfma_f32_16x16x32_bf16 v[188:191],a[188:191],v[228:231],v[36:39]
v_exp_f32 v170,v170
v_mfma_f32_16x16x32_bf16 v[188:191],a[184:187],v[196:199],v[188:191]
v_exp_f32 v171,v171
v_mfma_f32_16x16x32_bf16 v[188:191],a[180:183],v[164:167],v[188:191]
v_exp_f32 v172,v172
v_mfma_f32_16x16x32_bf16 v[188:191],a[176:179],v[132:135],v[188:191]
v_exp_f32 v173,v173
v_mfma_f32_16x16x32_bf16 a[96:99],a[192:195],v[232:235],a[96:99]
ds_read_b64_tr_b16 a[192:193],v2
v_cvt_pk_bf16_f32 v168,v168,v169
v_mfma_f32_16x16x32_bf16 a[96:99],a[196:199],v[236:239],a[96:99]
ds_read_b64_tr_b16 a[194:195],v2 offset:4096
v_cvt_pk_bf16_f32 v169,v170,v171
v_mfma_f32_16x16x32_bf16 a[100:103],a[204:207],v[236:239],a[100:103]
ds_read_b64_tr_b16 a[196:197],v2 offset:8192
v_cvt_pk_bf16_f32 v170,v172,v173
v_mfma_f32_16x16x32_bf16 a[100:103],a[200:203],v[232:235],a[100:103]
v_exp_f32 v174,v174
v_mfma_f32_16x16x32_bf16 a[104:107],a[208:211],v[232:235],a[104:107]
v_exp_f32 v175,v175
v_mfma_f32_16x16x32_bf16 a[104:107],a[212:215],v[236:239],a[104:107]
ds_read_b64_tr_b16 a[198:199],v2 offset:12288
v_cvt_pk_bf16_f32 v171,v174,v175
v_mfma_f32_16x16x32_bf16 a[108:111],a[220:223],v[236:239],a[108:111]
v_exp_f32 v184,v184
v_mfma_f32_16x16x32_bf16 a[108:111],a[216:219],v[232:235],a[108:111]
v_exp_f32 v185,v185
v_mfma_f32_16x16x32_bf16 a[112:115],a[224:227],v[232:235],a[112:115]
buffer_load_dwordx4 v4,s[24:27],s47 offen offset:2048 nt lds
v_mfma_f32_16x16x32_bf16 a[112:115],a[228:231],v[236:239],a[112:115]
ds_read_b64_tr_b16 a[204:205],v3 offset:8192
v_cvt_pk_bf16_f32 v172,v184,v185
v_mfma_f32_16x16x32_bf16 a[116:119],a[236:239],v[236:239],a[116:119]
ds_read_b64_tr_b16 a[206:207],v3 offset:12288
v_cvt_pk_bf16_f32 v136,v136,v137
v_mfma_f32_16x16x32_bf16 a[116:119],a[232:235],v[232:235],a[116:119]
v_exp_f32 v186,v186
v_mfma_f32_16x16x32_bf16 a[120:123],a[240:243],v[232:235],a[120:123]
ds_read_b64_tr_b16 a[200:201],v3
v_mfma_f32_16x16x32_bf16 a[120:123],a[244:247],v[236:239],a[120:123]
ds_read_b64_tr_b16 a[202:203],v3 offset:4096
v_cvt_pk_bf16_f32 v137,v138,v139
v_mfma_f32_16x16x32_bf16 a[124:127],a[252:255],v[236:239],a[124:127]
ds_read_b64_tr_b16 a[208:209],v2 offset:256
v_mfma_f32_16x16x32_bf16 a[124:127],a[248:251],v[232:235],a[124:127]
ds_read_b64_tr_b16 a[210:211],v2 offset:4352
v_smfmac_f32_16x16x64_bf16 v[60:63],v[28:31],v[232:239],v27
ds_read_b64_tr_b16 a[212:213],v2 offset:8448
ds_read_b64_tr_b16 a[214:215],v2 offset:12544
v_cvt_pk_bf16_f32 v138,v140,v141
ds_read_b64_tr_b16 a[220:221],v3 offset:8448
ds_read_b64_tr_b16 a[222:223],v3 offset:12544
v_cvt_pk_bf16_f32 v139,v142,v143
ds_read_b64_tr_b16 a[216:217],v3 offset:256
ds_read_b64_tr_b16 a[218:219],v3 offset:4352
buffer_load_dwordx4 v5,s[24:27],s47 offen offset:3072 nt lds
s_or_b32 s47,s47,0x18000
ds_read_b64_tr_b16 a[224:225],v2 offset:512
v_cvt_pk_bf16_f32 v140,v152,v153
s_addk_i32 s47,4096
s_and_b32 s47,s47,0x27000
ds_read_b64_tr_b16 a[226:227],v2 offset:4608
s_or_b32 s47,s47,s46
ds_read_b64_tr_b16 a[228:229],v2 offset:8704
v_cvt_pk_bf16_f32 v141,v154,v155
ds_read_b64_tr_b16 a[230:231],v2 offset:12800
ds_read_b64_tr_b16 a[236:237],v3 offset:8704
ds_read_b64_tr_b16 a[238:239],v3 offset:12800
v_cvt_pk_bf16_f32 v142,v156,v157
ds_read_b64_tr_b16 a[232:233],v3 offset:512
ds_read_b64_tr_b16 a[234:235],v3 offset:4608
ds_read_b64_tr_b16 a[240:241],v2 offset:768
v_cvt_pk_bf16_f32 v143,v158,v159
buffer_load_dword v26,v24,s[88:91],s93 offen
s_waitcnt lgkmcnt(15)
v_mfma_f32_16x16x32_bf16 a[0:3],a[192:195],v[136:139],a[0:3]
ds_read_b64_tr_b16 a[242:243],v2 offset:4864
v_mfma_f32_16x16x32_bf16 a[0:3],a[196:199],v[140:143],a[0:3]
ds_read_b64_tr_b16 a[244:245],v2 offset:8960
v_mfma_f32_16x16x32_bf16 a[4:7],a[204:207],v[140:143],a[4:7]
ds_read_b64_tr_b16 a[246:247],v2 offset:13056
v_mfma_f32_16x16x32_bf16 a[4:7],a[200:203],v[136:139],a[4:7]
ds_read_b64_tr_b16 a[252:253],v3 offset:8960
v_mfma_f32_16x16x32_bf16 a[8:11],a[208:211],v[136:139],a[8:11]
s_waitcnt lgkmcnt(15)
ds_read_b64_tr_b16 a[254:255],v3 offset:13056
v_mfma_f32_16x16x32_bf16 a[8:11],a[212:215],v[140:143],a[8:11]
ds_read_b64_tr_b16 a[248:249],v3 offset:768
v_mfma_f32_16x16x32_bf16 a[12:15],a[220:223],v[140:143],a[12:15]
s_waitcnt lgkmcnt(15)
ds_read_b64_tr_b16 a[250:251],v3 offset:4864
v_mfma_f32_16x16x32_bf16 a[12:15],a[216:219],v[136:139],a[12:15]
s_bitcmp1_b32 s45,0
s_cbranch_scc0 L_t31v21_%=
s_mov_b32 m0,s80
s_addk_i32 s80,4096
buffer_load_dwordx4 v25,s[24:27],s83 offen nt lds
s_addk_i32 s83,1024
L_t31v21_%=:
s_waitcnt lgkmcnt(7)
v_mfma_f32_16x16x32_bf16 a[16:19],a[224:227],v[136:139],a[16:19]
v_exp_f32 v187,v187
v_mfma_f32_16x16x32_bf16 a[16:19],a[228:231],v[140:143],a[16:19]
v_exp_f32 v188,v188
v_mfma_f32_16x16x32_bf16 a[20:23],a[236:239],v[140:143],a[20:23]
v_exp_f32 v189,v189
v_mfma_f32_16x16x32_bf16 a[20:23],a[232:235],v[136:139],a[20:23]
s_waitcnt lgkmcnt(3)
v_exp_f32 v190,v190
v_mfma_f32_16x16x32_bf16 a[24:27],a[240:243],v[136:139],a[24:27]
v_exp_f32 v191,v191
v_mfma_f32_16x16x32_bf16 a[24:27],a[244:247],v[140:143],a[24:27]
s_waitcnt lgkmcnt(1)
v_cvt_pk_bf16_f32 v173,v186,v187
v_mfma_f32_16x16x32_bf16 a[28:31],a[252:255],v[140:143],a[28:31]
s_waitcnt lgkmcnt(0)
v_cvt_pk_bf16_f32 v174,v188,v189
v_cvt_pk_bf16_f32 v175,v190,v191
v_mfma_f32_16x16x32_bf16 a[28:31],a[248:251],v[136:139],a[28:31]
s_cmp_lg_u32 s53,0
v_smfmac_f32_16x16x64_bf16 v[48:51],v[28:31],v[136:143],v27
s_cbranch_scc1 L_att1v2_%=
s_waitcnt lgkmcnt(0)
s_nop 7
s_branch L_att1x_%=
.p2align 5
L_att1_%=:
v_mfma_f32_16x16x32_bf16 v[232:235],a[128:131],v[148:151],v[44:47]
s_waitcnt vmcnt(8) lgkmcnt(0)
v_exp_f32 v200,v200
v_mfma_f32_16x16x32_bf16 v[232:235],a[132:135],v[180:183],v[232:235]
s_barrier
ds_read_b128 a[128:131],v1
v_add_u32 v2,s48,v2
v_mfma_f32_16x16x32_bf16 v[232:235],a[136:139],v[212:215],v[232:235]
s_mov_b32 s90,L_out_end_%=-L_pc_%=+128
v_exp_f32 v201,v201
v_mfma_f32_16x16x32_bf16 v[232:235],a[140:143],v[244:247],v[232:235]
s_add_u32 m0,s49,s51
ds_read_b128 a[132:135],v1 offset:256
v_cvt_pk_bf16_f32 v200,v200,v201
v_mfma_f32_16x16x32_bf16 v[236:239],a[156:159],v[244:247],v[44:47]
s_lshr_b32 s45,s45,1
v_exp_f32 v202,v202
v_mfma_f32_16x16x32_bf16 v[236:239],a[152:155],v[212:215],v[236:239]
s_sub_u32 s94,s94,3
ds_read_b128 a[136:139],v1 offset:512
v_add_u32 v3,s48,v3
v_mfma_f32_16x16x32_bf16 v[236:239],a[148:151],v[180:183],v[236:239]
s_lshr_b32 s60,s94,2
buffer_load_dwordx4 v4,s[20:23],s47 offen nt lds
v_mfma_f32_16x16x32_bf16 v[236:239],a[144:147],v[148:151],v[236:239]
s_max_i32 s60,s60,124
v_exp_f32 v203,v203
v_mfma_f32_16x16x32_bf16 v[248:251],a[160:163],v[148:151],v[44:47]
s_add_u32 s62,s50,32768
ds_read_b128 a[140:143],v1 offset:768
v_cvt_pk_bf16_f32 v201,v202,v203
v_mfma_f32_16x16x32_bf16 v[248:251],a[164:167],v[180:183],v[248:251]
s_and_b32 s62,s62,131071
v_exp_f32 v204,v204
v_mfma_f32_16x16x32_bf16 v[248:251],a[168:171],v[212:215],v[248:251]
s_addk_i32 s54,16384
v_exp_f32 v205,v205
v_mfma_f32_16x16x32_bf16 v[248:251],a[172:175],v[244:247],v[248:251]
s_sub_u32 s48,s62,s50
ds_read_b128 a[156:159],v1 offset:4864
v_cvt_pk_bf16_f32 v202,v204,v205
v_mfma_f32_16x16x32_bf16 v[252:255],a[188:191],v[244:247],v[44:47]
s_sub_u32 s53,s53,1
v_exp_f32 v206,v206
v_mfma_f32_16x16x32_bf16 v[252:255],a[184:187],v[212:215],v[252:255]
s_and_b32 s46,s54,0xfffc0000
v_exp_f32 v207,v207
v_mfma_f32_16x16x32_bf16 v[252:255],a[180:183],v[180:183],v[252:255]
s_mov_b32 s50,s62
ds_read_b128 a[152:155],v1 offset:4608
v_cvt_pk_bf16_f32 v203,v206,v207
v_mfma_f32_16x16x32_bf16 v[252:255],a[176:179],v[148:151],v[252:255]
s_xor_b32 s49,s50,65536
v_exp_f32 v216,v216
v_mfma_f32_16x16x32_bf16 a[32:35],a[192:195],v[168:171],a[32:35]
ds_read_b128 a[148:151],v1 offset:4352
v_mfma_f32_16x16x32_bf16 a[32:35],a[196:199],v[172:175],a[32:35]
v_exp_f32 v217,v217
v_mfma_f32_16x16x32_bf16 a[36:39],a[204:207],v[172:175],a[36:39]
ds_read_b128 a[144:147],v1 offset:4096
v_cvt_pk_bf16_f32 v204,v216,v217
v_mfma_f32_16x16x32_bf16 a[36:39],a[200:203],v[168:171],a[36:39]
v_exp_f32 v218,v218
v_mfma_f32_16x16x32_bf16 a[40:43],a[208:211],v[168:171],a[40:43]
buffer_load_dwordx4 v4,s[20:23],s47 offen offset:1024 nt lds
v_mfma_f32_16x16x32_bf16 a[40:43],a[212:215],v[172:175],a[40:43]
ds_read_b128 a[160:163],v1 offset:8192
v_mfma_f32_16x16x32_bf16 a[44:47],a[220:223],v[172:175],a[44:47]
v_exp_f32 v219,v219
v_mfma_f32_16x16x32_bf16 a[44:47],a[216:219],v[168:171],a[44:47]
ds_read_b128 a[164:167],v1 offset:8448
v_cvt_pk_bf16_f32 v205,v218,v219
v_mfma_f32_16x16x32_bf16 a[48:51],a[224:227],v[168:171],a[48:51]
v_exp_f32 v220,v220
v_mfma_f32_16x16x32_bf16 a[48:51],a[228:231],v[172:175],a[48:51]
ds_read_b128 a[168:171],v1 offset:8704
v_mfma_f32_16x16x32_bf16 a[52:55],a[236:239],v[172:175],a[52:55]
v_exp_f32 v221,v221
v_mfma_f32_16x16x32_bf16 a[52:55],a[232:235],v[168:171],a[52:55]
ds_read_b128 a[172:175],v1 offset:8960
v_cvt_pk_bf16_f32 v206,v220,v221
v_mfma_f32_16x16x32_bf16 a[56:59],a[240:243],v[168:171],a[56:59]
v_exp_f32 v222,v222
v_mfma_f32_16x16x32_bf16 a[56:59],a[244:247],v[172:175],a[56:59]
ds_read_b128 a[188:191],v1 offset:13056
v_mfma_f32_16x16x32_bf16 a[60:63],a[252:255],v[172:175],a[60:63]
v_exp_f32 v223,v223
v_mfma_f32_16x16x32_bf16 a[60:63],a[248:251],v[168:171],a[60:63]
ds_read_b128 a[184:187],v1 offset:12800
v_cvt_pk_bf16_f32 v207,v222,v223
v_smfmac_f32_16x16x64_bf16 v[52:55],v[28:31],v[168:175],v27
s_waitcnt lgkmcnt(4)
v_mfma_f32_16x16x32_bf16 v[136:139],a[128:131],v[128:131],v[32:35]
)A"
R"A(ds_read_b128 a[180:183],v1 offset:12544
v_mfma_f32_16x16x32_bf16 v[136:139],a[132:135],v[160:163],v[136:139]
buffer_load_dwordx4 v5,s[20:23],s47 offen offset:2048 nt lds
v_mfma_f32_16x16x32_bf16 v[136:139],a[136:139],v[192:195],v[136:139]
ds_read_b128 a[176:179],v1 offset:12288
v_add_u32 v1,s48,v1
v_mfma_f32_16x16x32_bf16 v[136:139],a[140:143],v[224:227],v[136:139]
v_exp_f32 v232,v232
v_mfma_f32_16x16x32_bf16 v[140:143],a[156:159],v[224:227],v[32:35]
v_exp_f32 v233,v233
v_mfma_f32_16x16x32_bf16 v[140:143],a[152:155],v[192:195],v[140:143]
v_exp_f32 v234,v234
v_mfma_f32_16x16x32_bf16 v[140:143],a[148:151],v[160:163],v[140:143]
v_exp_f32 v235,v235
v_mfma_f32_16x16x32_bf16 v[140:143],a[144:147],v[128:131],v[140:143]
v_exp_f32 v236,v236
v_mfma_f32_16x16x32_bf16 v[152:155],a[160:163],v[128:131],v[32:35]
v_exp_f32 v237,v237
v_mfma_f32_16x16x32_bf16 v[152:155],a[164:167],v[160:163],v[152:155]
s_waitcnt lgkmcnt(1)
v_exp_f32 v238,v238
v_mfma_f32_16x16x32_bf16 v[152:155],a[168:171],v[192:195],v[152:155]
v_exp_f32 v239,v239
v_mfma_f32_16x16x32_bf16 v[152:155],a[172:175],v[224:227],v[152:155]
v_exp_f32 v248,v248
v_mfma_f32_16x16x32_bf16 v[156:159],a[188:191],v[224:227],v[32:35]
v_exp_f32 v249,v249
v_mfma_f32_16x16x32_bf16 v[156:159],a[184:187],v[192:195],v[156:159]
v_exp_f32 v250,v250
v_mfma_f32_16x16x32_bf16 v[156:159],a[180:183],v[160:163],v[156:159]
s_waitcnt lgkmcnt(0)
v_exp_f32 v251,v251
v_mfma_f32_16x16x32_bf16 v[156:159],a[176:179],v[128:131],v[156:159]
buffer_load_dwordx4 v5,s[20:23],s47 offen offset:3072 nt lds
v_mfma_f32_16x16x32_bf16 a[64:67],a[192:195],v[200:203],a[64:67]
v_exp_f32 v252,v252
v_mfma_f32_16x16x32_bf16 a[64:67],a[196:199],v[204:207],a[64:67]
v_exp_f32 v253,v253
v_mfma_f32_16x16x32_bf16 a[68:71],a[204:207],v[204:207],a[68:71]
v_exp_f32 v254,v254
v_mfma_f32_16x16x32_bf16 a[68:71],a[200:203],v[200:203],a[68:71]
v_exp_f32 v255,v255
v_mfma_f32_16x16x32_bf16 a[72:75],a[208:211],v[200:203],a[72:75]
v_exp_f32 v136,v136
v_mfma_f32_16x16x32_bf16 a[72:75],a[212:215],v[204:207],a[72:75]
v_exp_f32 v137,v137
v_mfma_f32_16x16x32_bf16 a[76:79],a[220:223],v[204:207],a[76:79]
v_exp_f32 v138,v138
v_mfma_f32_16x16x32_bf16 a[76:79],a[216:219],v[200:203],a[76:79]
v_exp_f32 v139,v139
v_mfma_f32_16x16x32_bf16 a[80:83],a[224:227],v[200:203],a[80:83]
v_exp_f32 v140,v140
v_mfma_f32_16x16x32_bf16 a[80:83],a[228:231],v[204:207],a[80:83]
v_exp_f32 v141,v141
v_mfma_f32_16x16x32_bf16 a[84:87],a[236:239],v[204:207],a[84:87]
s_addk_i32 m0,16384
v_exp_f32 v142,v142
v_mfma_f32_16x16x32_bf16 a[84:87],a[232:235],v[200:203],a[84:87]
v_exp_f32 v143,v143
v_mfma_f32_16x16x32_bf16 a[88:91],a[240:243],v[200:203],a[88:91]
v_exp_f32 v152,v152
v_mfma_f32_16x16x32_bf16 a[88:91],a[244:247],v[204:207],a[88:91]
buffer_load_dwordx4 v4,s[24:27],s47 offen nt lds
v_mfma_f32_16x16x32_bf16 a[92:95],a[252:255],v[204:207],a[92:95]
v_cvt_pk_bf16_f32 v232,v232,v233
v_cvt_pk_bf16_f32 v233,v234,v235
v_mfma_f32_16x16x32_bf16 a[92:95],a[248:251],v[200:203],a[92:95]
v_cvt_pk_bf16_f32 v234,v236,v237
v_cvt_pk_bf16_f32 v235,v238,v239
v_smfmac_f32_16x16x64_bf16 v[56:59],v[28:31],v[200:207],v27
v_cvt_pk_bf16_f32 v236,v248,v249
v_mfma_f32_16x16x32_bf16 v[168:171],a[128:131],v[132:135],v[36:39]
v_cvt_pk_bf16_f32 v237,v250,v251
v_cvt_pk_bf16_f32 v238,v252,v253
v_mfma_f32_16x16x32_bf16 v[168:171],a[132:135],v[164:167],v[168:171]
v_cvt_pk_bf16_f32 v239,v254,v255
v_mfma_f32_16x16x32_bf16 v[168:171],a[136:139],v[196:199],v[168:171]
v_exp_f32 v153,v153
v_mfma_f32_16x16x32_bf16 v[168:171],a[140:143],v[228:231],v[168:171]
v_exp_f32 v154,v154
v_mfma_f32_16x16x32_bf16 v[172:175],a[156:159],v[228:231],v[36:39]
v_exp_f32 v155,v155
v_mfma_f32_16x16x32_bf16 v[172:175],a[152:155],v[196:199],v[172:175]
v_exp_f32 v156,v156
v_mfma_f32_16x16x32_bf16 v[172:175],a[148:151],v[164:167],v[172:175]
v_exp_f32 v157,v157
v_mfma_f32_16x16x32_bf16 v[172:175],a[144:147],v[132:135],v[172:175]
v_exp_f32 v158,v158
v_mfma_f32_16x16x32_bf16 v[184:187],a[160:163],v[132:135],v[36:39]
v_exp_f32 v159,v159
v_mfma_f32_16x16x32_bf16 v[184:187],a[164:167],v[164:167],v[184:187]
v_exp_f32 v168,v168
v_mfma_f32_16x16x32_bf16 v[184:187],a[168:171],v[196:199],v[184:187]
buffer_load_dwordx4 v5,s[24:27],s47 offen offset:1024 nt lds
v_mfma_f32_16x16x32_bf16 v[184:187],a[172:175],v[228:231],v[184:187]
v_exp_f32 v169,v169
v_mfma_f32_16x16x32_bf16 v[188:191],a[188:191],v[228:231],v[36:39]
v_exp_f32 v170,v170
v_mfma_f32_16x16x32_bf16 v[188:191],a[184:187],v[196:199],v[188:191]
v_exp_f32 v171,v171
v_mfma_f32_16x16x32_bf16 v[188:191],a[180:183],v[164:167],v[188:191]
v_exp_f32 v172,v172
v_mfma_f32_16x16x32_bf16 v[188:191],a[176:179],v[132:135],v[188:191]
v_exp_f32 v173,v173
v_mfma_f32_16x16x32_bf16 a[96:99],a[192:195],v[232:235],a[96:99]
ds_read_b64_tr_b16 a[192:193],v2
v_cvt_pk_bf16_f32 v168,v168,v169
v_mfma_f32_16x16x32_bf16 a[96:99],a[196:199],v[236:239],a[96:99]
ds_read_b64_tr_b16 a[194:195],v2 offset:4096
v_cvt_pk_bf16_f32 v169,v170,v171
v_mfma_f32_16x16x32_bf16 a[100:103],a[204:207],v[236:239],a[100:103]
ds_read_b64_tr_b16 a[196:197],v2 offset:8192
v_cvt_pk_bf16_f32 v170,v172,v173
v_mfma_f32_16x16x32_bf16 a[100:103],a[200:203],v[232:235],a[100:103]
v_exp_f32 v174,v174
v_mfma_f32_16x16x32_bf16 a[104:107],a[208:211],v[232:235],a[104:107]
v_exp_f32 v175,v175
v_mfma_f32_16x16x32_bf16 a[104:107],a[212:215],v[236:239],a[104:107]
ds_read_b64_tr_b16 a[198:199],v2 offset:12288
v_cvt_pk_bf16_f32 v171,v174,v175
v_mfma_f32_16x16x32_bf16 a[108:111],a[220:223],v[236:239],a[108:111]
v_exp_f32 v184,v184
v_mfma_f32_16x16x32_bf16 a[108:111],a[216:219],v[232:235],a[108:111]
v_exp_f32 v185,v185
v_mfma_f32_16x16x32_bf16 a[112:115],a[224:227],v[232:235],a[112:115]
buffer_load_dwordx4 v4,s[24:27],s47 offen offset:2048 nt lds
v_mfma_f32_16x16x32_bf16 a[112:115],a[228:231],v[236:239],a[112:115]
ds_read_b64_tr_b16 a[204:205],v3 offset:8192
v_cvt_pk_bf16_f32 v172,v184,v185
v_mfma_f32_16x16x32_bf16 a[116:119],a[236:239],v[236:239],a[116:119]
ds_read_b64_tr_b16 a[206:207],v3 offset:12288
v_cvt_pk_bf16_f32 v136,v136,v137
v_mfma_f32_16x16x32_bf16 a[116:119],a[232:235],v[232:235],a[116:119]
v_exp_f32 v186,v186
v_mfma_f32_16x16x32_bf16 a[120:123],a[240:243],v[232:235],a[120:123]
ds_read_b64_tr_b16 a[200:201],v3
v_mfma_f32_16x16x32_bf16 a[120:123],a[244:247],v[236:239],a[120:123]
ds_read_b64_tr_b16 a[202:203],v3 offset:4096
v_cvt_pk_bf16_f32 v137,v138,v139
v_mfma_f32_16x16x32_bf16 a[124:127],a[252:255],v[236:239],a[124:127]
ds_read_b64_tr_b16 a[208:209],v2 offset:256
v_mfma_f32_16x16x32_bf16 a[124:127],a[248:251],v[232:235],a[124:127]
ds_read_b64_tr_b16 a[210:211],v2 offset:4352
v_smfmac_f32_16x16x64_bf16 v[60:63],v[28:31],v[232:239],v27
ds_read_b64_tr_b16 a[212:213],v2 offset:8448
v_mfma_f32_16x16x32_bf16 v[200:203],a[128:131],v[144:147],v[40:43]
ds_read_b64_tr_b16 a[214:215],v2 offset:12544
v_cvt_pk_bf16_f32 v138,v140,v141
v_mfma_f32_16x16x32_bf16 v[200:203],a[132:135],v[176:179],v[200:203]
ds_read_b64_tr_b16 a[220:221],v3 offset:8448
v_mfma_f32_16x16x32_bf16 v[200:203],a[136:139],v[208:211],v[200:203]
ds_read_b64_tr_b16 a[222:223],v3 offset:12544
v_cvt_pk_bf16_f32 v139,v142,v143
v_mfma_f32_16x16x32_bf16 v[200:203],a[140:143],v[240:243],v[200:203]
ds_read_b64_tr_b16 a[216:217],v3 offset:256
v_mfma_f32_16x16x32_bf16 v[204:207],a[156:159],v[240:243],v[40:43]
ds_read_b64_tr_b16 a[218:219],v3 offset:4352
v_mfma_f32_16x16x32_bf16 v[204:207],a[152:155],v[208:211],v[204:207]
buffer_load_dwordx4 v5,s[24:27],s47 offen offset:3072 nt lds
v_mfma_f32_16x16x32_bf16 v[204:207],a[148:151],v[176:179],v[204:207]
s_or_b32 s47,s47,0x18000
ds_read_b64_tr_b16 a[224:225],v2 offset:512
v_cvt_pk_bf16_f32 v140,v152,v153
v_mfma_f32_16x16x32_bf16 v[204:207],a[144:147],v[144:147],v[204:207]
s_addk_i32 s47,4096
s_and_b32 s47,s47,0x27000
ds_read_b64_tr_b16 a[226:227],v2 offset:4608
v_mfma_f32_16x16x32_bf16 v[216:219],a[160:163],v[144:147],v[40:43]
s_or_b32 s47,s47,s46
ds_read_b64_tr_b16 a[228:229],v2 offset:8704
v_cvt_pk_bf16_f32 v141,v154,v155
v_mfma_f32_16x16x32_bf16 v[216:219],a[164:167],v[176:179],v[216:219]
ds_read_b64_tr_b16 a[230:231],v2 offset:12800
v_mfma_f32_16x16x32_bf16 v[216:219],a[168:171],v[208:211],v[216:219]
ds_read_b64_tr_b16 a[236:237],v3 offset:8704
v_mfma_f32_16x16x32_bf16 v[216:219],a[172:175],v[240:243],v[216:219]
ds_read_b64_tr_b16 a[238:239],v3 offset:12800
v_cvt_pk_bf16_f32 v142,v156,v157
v_mfma_f32_16x16x32_bf16 v[220:223],a[188:191],v[240:243],v[40:43]
ds_read_b64_tr_b16 a[232:233],v3 offset:512
v_mfma_f32_16x16x32_bf16 v[220:223],a[184:187],v[208:211],v[220:223]
ds_read_b64_tr_b16 a[234:235],v3 offset:4608
v_mfma_f32_16x16x32_bf16 v[220:223],a[180:183],v[176:179],v[220:223]
ds_read_b64_tr_b16 a[240:241],v2 offset:768
v_cvt_pk_bf16_f32 v143,v158,v159
v_mfma_f32_16x16x32_bf16 v[220:223],a[176:179],v[144:147],v[220:223]
s_waitcnt lgkmcnt(7)
buffer_load_dword v26,v24,s[88:91],s93 offen
v_mfma_f32_16x16x32_bf16 a[0:3],a[192:195],v[136:139],a[0:3]
ds_read_b64_tr_b16 a[242:243],v2 offset:4864
v_mfma_f32_16x16x32_bf16 a[0:3],a[196:199],v[140:143],a[0:3]
ds_read_b64_tr_b16 a[244:245],v2 offset:8960
v_mfma_f32_16x16x32_bf16 a[4:7],a[204:207],v[140:143],a[4:7]
ds_read_b64_tr_b16 a[246:247],v2 offset:13056
v_mfma_f32_16x16x32_bf16 a[4:7],a[200:203],v[136:139],a[4:7]
s_memrealtime s[76:77]
ds_read_b64_tr_b16 a[252:253],v3 offset:8960
v_mfma_f32_16x16x32_bf16 a[8:11],a[208:211],v[136:139],a[8:11]
ds_read_b64_tr_b16 a[254:255],v3 offset:13056
v_mfma_f32_16x16x32_bf16 a[8:11],a[212:215],v[140:143],a[8:11]
ds_read_b64_tr_b16 a[248:249],v3 offset:768
v_mfma_f32_16x16x32_bf16 a[12:15],a[220:223],v[140:143],a[12:15]
ds_read_b64_tr_b16 a[250:251],v3 offset:4864
v_mfma_f32_16x16x32_bf16 a[12:15],a[216:219],v[136:139],a[12:15]
s_bitcmp1_b32 s45,0
s_cbranch_scc0 L_t311_%=
s_mov_b32 m0,s80
s_addk_i32 s80,4096
buffer_load_dwordx4 v25,s[24:27],s83 offen nt lds
s_addk_i32 s83,1024
L_t311_%=:
v_mfma_f32_16x16x32_bf16 a[16:19],a[224:227],v[136:139],a[16:19]
s_waitcnt lgkmcnt(6)
v_exp_f32 v187,v187
v_mfma_f32_16x16x32_bf16 a[16:19],a[228:231],v[140:143],a[16:19]
v_exp_f32 v188,v188
v_mfma_f32_16x16x32_bf16 a[20:23],a[236:239],v[140:143],a[20:23]
s_waitcnt lgkmcnt(0)
v_exp_f32 v189,v189
v_mfma_f32_16x16x32_bf16 a[20:23],a[232:235],v[136:139],a[20:23]
s_sub_u32 s79,s78,s76
v_exp_f32 v190,v190
v_mfma_f32_16x16x32_bf16 a[24:27],a[240:243],v[136:139],a[24:27]
s_add_u32 s78,s76,s60
v_exp_f32 v191,v191
v_mfma_f32_16x16x32_bf16 a[24:27],a[244:247],v[140:143],a[24:27]
s_cmp_gt_i32 s79,s95
s_cbranch_scc1 L_ps7_%=
L_pr7_%=:
v_cvt_pk_bf16_f32 v173,v186,v187
v_mfma_f32_16x16x32_bf16 a[28:31],a[252:255],v[140:143],a[28:31]
v_cvt_pk_bf16_f32 v174,v188,v189
v_cvt_pk_bf16_f32 v175,v190,v191
v_mfma_f32_16x16x32_bf16 a[28:31],a[248:251],v[136:139],a[28:31]
s_cmp_lg_u32 s53,0
v_smfmac_f32_16x16x64_bf16 v[48:51],v[28:31],v[136:143],v27
s_cbranch_scc1 L_att1_%=
L_att1x_%=:
L_endseg_%=:
v_mfma_f32_16x16x32_bf16 v[232:235],a[128:131],v[148:151],v[44:47]
s_waitcnt vmcnt(10)
s_lshl_b32 s43,s41,2
v_exp_f32 v200,v200
v_mfma_f32_16x16x32_bf16 v[232:235],a[132:135],v[180:183],v[232:235]
s_barrier
ds_read_b128 a[128:131],v1
v_add_u32 v2,s48,v2
v_mfma_f32_16x16x32_bf16 v[232:235],a[136:139],v[212:215],v[232:235]
s_add_u32 s43,s43,s70
v_exp_f32 v201,v201
v_mfma_f32_16x16x32_bf16 v[232:235],a[140:143],v[244:247],v[232:235]
s_lshl_b32 s43,s43,6
ds_read_b128 a[132:135],v1 offset:256
v_cvt_pk_bf16_f32 v200,v200,v201
v_mfma_f32_16x16x32_bf16 v[236:239],a[156:159],v[244:247],v[44:47]
s_add_u32 s43,s43,s40
v_exp_f32 v202,v202
v_mfma_f32_16x16x32_bf16 v[236:239],a[152:155],v[212:215],v[236:239]
s_lshl_b32 s43,s43,15
ds_read_b128 a[136:139],v1 offset:512
v_add_u32 v3,s48,v3
v_mfma_f32_16x16x32_bf16 v[236:239],a[148:151],v[180:183],v[236:239]
s_mov_b32 s48,32768
v_exp_f32 v203,v203
v_mfma_f32_16x16x32_bf16 v[236:239],a[144:147],v[148:151],v[236:239]
s_mov_b64 s[28:29],s[12:13]
ds_read_b128 a[140:143],v1 offset:768
v_cvt_pk_bf16_f32 v201,v202,v203
v_mfma_f32_16x16x32_bf16 v[248:251],a[160:163],v[148:151],v[44:47]
s_mov_b32 s30,950272
v_exp_f32 v204,v204
v_mfma_f32_16x16x32_bf16 v[248:251],a[164:167],v[180:183],v[248:251]
)A"
R"A(s_mov_b32 s31,0x20000
ds_read_b128 a[156:159],v1 offset:4864
v_lshrrev_b32 v25,6,v6
v_mfma_f32_16x16x32_bf16 v[248:251],a[168:171],v[212:215],v[248:251]
v_exp_f32 v205,v205
v_mfma_f32_16x16x32_bf16 v[248:251],a[172:175],v[244:247],v[248:251]
ds_read_b128 a[152:155],v1 offset:4608
v_cvt_pk_bf16_f32 v202,v204,v205
v_mfma_f32_16x16x32_bf16 v[252:255],a[188:191],v[244:247],v[44:47]
v_exp_f32 v206,v206
v_mfma_f32_16x16x32_bf16 v[252:255],a[184:187],v[212:215],v[252:255]
ds_read_b128 a[148:151],v1 offset:4352
v_mul_u32_u24 v25,320,v25
v_mfma_f32_16x16x32_bf16 v[252:255],a[180:183],v[180:183],v[252:255]
v_exp_f32 v207,v207
v_mfma_f32_16x16x32_bf16 v[252:255],a[176:179],v[148:151],v[252:255]
ds_read_b128 a[144:147],v1 offset:4096
v_cvt_pk_bf16_f32 v203,v206,v207
v_mfma_f32_16x16x32_bf16 a[32:35],a[192:195],v[168:171],a[32:35]
v_exp_f32 v216,v216
v_mfma_f32_16x16x32_bf16 a[32:35],a[196:199],v[172:175],a[32:35]
s_add_u32 s28,s28,s43
s_addc_u32 s29,s29,0
ds_read_b128 a[160:163],v1 offset:8192
v_mfma_f32_16x16x32_bf16 a[36:39],a[204:207],v[172:175],a[36:39]
s_and_b32 s29,s29,0xffff
v_exp_f32 v217,v217
v_mfma_f32_16x16x32_bf16 a[36:39],a[200:203],v[168:171],a[36:39]
ds_read_b128 a[164:167],v1 offset:8448
v_cvt_pk_bf16_f32 v204,v216,v217
v_mfma_f32_16x16x32_bf16 a[40:43],a[208:211],v[168:171],a[40:43]
v_exp_f32 v218,v218
v_mfma_f32_16x16x32_bf16 a[40:43],a[212:215],v[172:175],a[40:43]
ds_read_b128 a[168:171],v1 offset:8704
v_mfma_f32_16x16x32_bf16 a[44:47],a[220:223],v[172:175],a[44:47]
v_exp_f32 v219,v219
v_mfma_f32_16x16x32_bf16 a[44:47],a[216:219],v[168:171],a[44:47]
ds_read_b128 a[172:175],v1 offset:8960
v_cvt_pk_bf16_f32 v205,v218,v219
v_mfma_f32_16x16x32_bf16 a[48:51],a[224:227],v[168:171],a[48:51]
v_exp_f32 v220,v220
v_mfma_f32_16x16x32_bf16 a[48:51],a[228:231],v[172:175],a[48:51]
ds_read_b128 a[188:191],v1 offset:13056
v_mfma_f32_16x16x32_bf16 a[52:55],a[236:239],v[172:175],a[52:55]
v_exp_f32 v221,v221
v_mfma_f32_16x16x32_bf16 a[52:55],a[232:235],v[168:171],a[52:55]
ds_read_b128 a[184:187],v1 offset:12800
v_cvt_pk_bf16_f32 v206,v220,v221
v_mfma_f32_16x16x32_bf16 a[56:59],a[240:243],v[168:171],a[56:59]
v_exp_f32 v222,v222
v_mfma_f32_16x16x32_bf16 a[56:59],a[244:247],v[172:175],a[56:59]
ds_read_b128 a[180:183],v1 offset:12544
v_mfma_f32_16x16x32_bf16 a[60:63],a[252:255],v[172:175],a[60:63]
v_exp_f32 v223,v223
v_mfma_f32_16x16x32_bf16 a[60:63],a[248:251],v[168:171],a[60:63]
ds_read_b128 a[176:179],v1 offset:12288
v_cvt_pk_bf16_f32 v207,v222,v223
v_smfmac_f32_16x16x64_bf16 v[52:55],v[28:31],v[168:175],v27
s_waitcnt lgkmcnt(4)
v_add_u32 v1,s48,v1
v_mfma_f32_16x16x32_bf16 v[136:139],a[128:131],v[128:131],v[32:35]
v_exp_f32 v232,v232
v_mfma_f32_16x16x32_bf16 v[136:139],a[132:135],v[160:163],v[136:139]
v_exp_f32 v233,v233
v_mfma_f32_16x16x32_bf16 v[136:139],a[136:139],v[192:195],v[136:139]
v_exp_f32 v234,v234
v_mfma_f32_16x16x32_bf16 v[136:139],a[140:143],v[224:227],v[136:139]
v_exp_f32 v235,v235
v_mfma_f32_16x16x32_bf16 v[140:143],a[156:159],v[224:227],v[32:35]
v_exp_f32 v236,v236
v_mfma_f32_16x16x32_bf16 v[140:143],a[152:155],v[192:195],v[140:143]
s_add_u32 s80,s71,0
s_and_b32 s80,s80,3
s_lshl_b32 s80,s80,1
v_mfma_f32_16x16x32_bf16 v[140:143],a[148:151],v[160:163],v[140:143]
s_mul_i32 s80,s80,163840
s_add_u32 s80,s80,s81
v_cvt_pk_bf16_f32 v232,v232,v233
v_mfma_f32_16x16x32_bf16 v[140:143],a[144:147],v[128:131],v[140:143]
s_add_u32 s83,s80,20480
v_exp_f32 v237,v237
v_mfma_f32_16x16x32_bf16 v[152:155],a[160:163],v[128:131],v[32:35]
v_exp_f32 v238,v238
v_mfma_f32_16x16x32_bf16 v[152:155],a[164:167],v[160:163],v[152:155]
buffer_store_dwordx4 v[64:67],v6,s[36:39],s84 offen sc1
v_mfma_f32_16x16x32_bf16 v[152:155],a[168:171],v[192:195],v[152:155]
v_exp_f32 v239,v239
v_mfma_f32_16x16x32_bf16 v[152:155],a[172:175],v[224:227],v[152:155]
s_waitcnt lgkmcnt(0)
v_exp_f32 v248,v248
v_mfma_f32_16x16x32_bf16 v[156:159],a[188:191],v[224:227],v[32:35]
v_exp_f32 v249,v249
v_mfma_f32_16x16x32_bf16 v[156:159],a[184:187],v[192:195],v[156:159]
v_exp_f32 v250,v250
v_mfma_f32_16x16x32_bf16 v[156:159],a[180:183],v[160:163],v[156:159]
v_exp_f32 v251,v251
v_mfma_f32_16x16x32_bf16 v[156:159],a[176:179],v[128:131],v[156:159]
v_exp_f32 v252,v252
v_mfma_f32_16x16x32_bf16 a[64:67],a[192:195],v[200:203],a[64:67]
v_exp_f32 v253,v253
v_mfma_f32_16x16x32_bf16 a[64:67],a[196:199],v[204:207],a[64:67]
v_exp_f32 v254,v254
v_mfma_f32_16x16x32_bf16 a[68:71],a[204:207],v[204:207],a[68:71]
v_exp_f32 v255,v255
v_mfma_f32_16x16x32_bf16 a[68:71],a[200:203],v[200:203],a[68:71]
v_exp_f32 v136,v136
v_mfma_f32_16x16x32_bf16 a[72:75],a[208:211],v[200:203],a[72:75]
v_exp_f32 v137,v137
v_mfma_f32_16x16x32_bf16 a[72:75],a[212:215],v[204:207],a[72:75]
v_cvt_pk_bf16_f32 v233,v234,v235
v_cvt_pk_bf16_f32 v234,v236,v237
v_mfma_f32_16x16x32_bf16 a[76:79],a[220:223],v[204:207],a[76:79]
v_cvt_pk_bf16_f32 v235,v238,v239
v_cvt_pk_bf16_f32 v236,v248,v249
v_mfma_f32_16x16x32_bf16 a[76:79],a[216:219],v[200:203],a[76:79]
v_cvt_pk_bf16_f32 v237,v250,v251
v_cvt_pk_bf16_f32 v238,v252,v253
v_mfma_f32_16x16x32_bf16 a[80:83],a[224:227],v[200:203],a[80:83]
v_cvt_pk_bf16_f32 v239,v254,v255
v_cvt_pk_bf16_f32 v136,v136,v137
v_mfma_f32_16x16x32_bf16 a[80:83],a[228:231],v[204:207],a[80:83]
buffer_store_dwordx4 v[68:71],v6,s[36:39],s84 offen offset:1024 sc1
v_mfma_f32_16x16x32_bf16 a[84:87],a[236:239],v[204:207],a[84:87]
s_add_u32 m0,s82,81920
v_exp_f32 v138,v138
v_mfma_f32_16x16x32_bf16 a[84:87],a[232:235],v[200:203],a[84:87]
v_exp_f32 v139,v139
v_mfma_f32_16x16x32_bf16 a[88:91],a[240:243],v[200:203],a[88:91]
v_exp_f32 v140,v140
v_mfma_f32_16x16x32_bf16 a[88:91],a[244:247],v[204:207],a[88:91]
buffer_load_dwordx4 v7,s[72:75],s80 offen lds
v_mfma_f32_16x16x32_bf16 a[92:95],a[252:255],v[204:207],a[92:95]
v_exp_f32 v141,v141
v_mfma_f32_16x16x32_bf16 a[92:95],a[248:251],v[200:203],a[92:95]
v_exp_f32 v142,v142
v_smfmac_f32_16x16x64_bf16 v[56:59],v[28:31],v[200:207],v27
v_cvt_pk_bf16_f32 v137,v138,v139
v_mfma_f32_16x16x32_bf16 v[168:171],a[128:131],v[132:135],v[36:39]
v_exp_f32 v143,v143
v_mfma_f32_16x16x32_bf16 v[168:171],a[132:135],v[164:167],v[168:171]
v_cvt_pk_bf16_f32 v138,v140,v141
v_cvt_pk_bf16_f32 v139,v142,v143
v_mfma_f32_16x16x32_bf16 v[168:171],a[136:139],v[196:199],v[168:171]
v_exp_f32 v152,v152
v_mfma_f32_16x16x32_bf16 v[168:171],a[140:143],v[228:231],v[168:171]
v_exp_f32 v153,v153
v_mfma_f32_16x16x32_bf16 v[172:175],a[156:159],v[228:231],v[36:39]
v_exp_f32 v154,v154
v_mfma_f32_16x16x32_bf16 v[172:175],a[152:155],v[196:199],v[172:175]
v_exp_f32 v155,v155
v_mfma_f32_16x16x32_bf16 v[172:175],a[148:151],v[164:167],v[172:175]
v_exp_f32 v156,v156
v_mfma_f32_16x16x32_bf16 v[172:175],a[144:147],v[132:135],v[172:175]
v_exp_f32 v157,v157
v_mfma_f32_16x16x32_bf16 v[184:187],a[160:163],v[132:135],v[36:39]
buffer_store_dwordx4 v[72:75],v6,s[36:39],s84 offen offset:2048 sc1
v_mfma_f32_16x16x32_bf16 v[184:187],a[164:167],v[164:167],v[184:187]
v_exp_f32 v158,v158
v_mfma_f32_16x16x32_bf16 v[184:187],a[168:171],v[196:199],v[184:187]
v_exp_f32 v159,v159
v_mfma_f32_16x16x32_bf16 v[184:187],a[172:175],v[228:231],v[184:187]
v_cvt_pk_bf16_f32 v140,v152,v153
v_cvt_pk_bf16_f32 v141,v154,v155
v_mfma_f32_16x16x32_bf16 v[188:191],a[188:191],v[228:231],v[36:39]
v_cvt_pk_bf16_f32 v142,v156,v157
v_cvt_pk_bf16_f32 v143,v158,v159
v_mfma_f32_16x16x32_bf16 v[188:191],a[184:187],v[196:199],v[188:191]
v_exp_f32 v168,v168
v_mfma_f32_16x16x32_bf16 v[188:191],a[180:183],v[164:167],v[188:191]
v_exp_f32 v169,v169
v_mfma_f32_16x16x32_bf16 v[188:191],a[176:179],v[132:135],v[188:191]
v_exp_f32 v170,v170
v_mfma_f32_16x16x32_bf16 a[96:99],a[192:195],v[232:235],a[96:99]
buffer_load_dwordx4 v8,s[72:75],s80 offen offset:1024 lds
v_mfma_f32_16x16x32_bf16 a[96:99],a[196:199],v[236:239],a[96:99]
ds_read_b64_tr_b16 a[192:193],v2
ds_read_b64_tr_b16 a[194:195],v2 offset:4096
v_mfma_f32_16x16x32_bf16 a[100:103],a[204:207],v[236:239],a[100:103]
ds_read_b64_tr_b16 a[196:197],v2 offset:8192
v_cvt_pk_bf16_f32 v168,v168,v169
v_mfma_f32_16x16x32_bf16 a[100:103],a[200:203],v[232:235],a[100:103]
v_exp_f32 v171,v171
v_mfma_f32_16x16x32_bf16 a[104:107],a[208:211],v[232:235],a[104:107]
ds_read_b64_tr_b16 a[198:199],v2 offset:12288
ds_read_b64_tr_b16 a[204:205],v3 offset:8192
v_mfma_f32_16x16x32_bf16 a[104:107],a[212:215],v[236:239],a[104:107]
ds_read_b64_tr_b16 a[206:207],v3 offset:12288
v_cvt_pk_bf16_f32 v169,v170,v171
v_mfma_f32_16x16x32_bf16 a[108:111],a[220:223],v[236:239],a[108:111]
v_exp_f32 v172,v172
v_mfma_f32_16x16x32_bf16 a[108:111],a[216:219],v[232:235],a[108:111]
ds_read_b64_tr_b16 a[200:201],v3
ds_read_b64_tr_b16 a[202:203],v3 offset:4096
v_mfma_f32_16x16x32_bf16 a[112:115],a[224:227],v[232:235],a[112:115]
buffer_store_dwordx4 v[76:79],v6,s[36:39],s84 offen offset:3072 sc1
v_mfma_f32_16x16x32_bf16 a[112:115],a[228:231],v[236:239],a[112:115]
s_add_u32 s84,s55,131072
ds_read_b64_tr_b16 a[208:209],v2 offset:256
ds_read_b64_tr_b16 a[210:211],v2 offset:4352
v_mfma_f32_16x16x32_bf16 a[116:119],a[236:239],v[236:239],a[116:119]
s_max_i32 s84,s84,s18
v_exp_f32 v173,v173
v_mfma_f32_16x16x32_bf16 a[116:119],a[232:235],v[232:235],a[116:119]
ds_read_b64_tr_b16 a[212:213],v2 offset:8448
ds_read_b64_tr_b16 a[214:215],v2 offset:12544
v_mfma_f32_16x16x32_bf16 a[120:123],a[240:243],v[232:235],a[120:123]
ds_read_b64_tr_b16 a[220:221],v3 offset:8448
v_cvt_pk_bf16_f32 v170,v172,v173
v_mfma_f32_16x16x32_bf16 a[120:123],a[244:247],v[236:239],a[120:123]
ds_read_b64_tr_b16 a[222:223],v3 offset:12544
v_mfma_f32_16x16x32_bf16 a[124:127],a[252:255],v[236:239],a[124:127]
ds_read_b64_tr_b16 a[216:217],v3 offset:256
v_mfma_f32_16x16x32_bf16 a[124:127],a[248:251],v[232:235],a[124:127]
ds_read_b64_tr_b16 a[218:219],v3 offset:4352
ds_read_b64_tr_b16 a[224:225],v2 offset:512
v_smfmac_f32_16x16x64_bf16 v[60:63],v[28:31],v[232:239],v27
ds_read_b64_tr_b16 a[226:227],v2 offset:4608
v_mfma_f32_16x16x32_bf16 v[200:203],a[128:131],v[144:147],v[40:43]
ds_read_b64_tr_b16 a[228:229],v2 offset:8704
v_mfma_f32_16x16x32_bf16 v[200:203],a[132:135],v[176:179],v[200:203]
ds_read_b64_tr_b16 a[230:231],v2 offset:12800
v_mfma_f32_16x16x32_bf16 v[200:203],a[136:139],v[208:211],v[200:203]
ds_read_b64_tr_b16 a[236:237],v3 offset:8704
ds_read_b64_tr_b16 a[238:239],v3 offset:12800
v_mfma_f32_16x16x32_bf16 v[200:203],a[140:143],v[240:243],v[200:203]
buffer_load_dwordx4 v9,s[72:75],s80 offen offset:2048 lds
v_mfma_f32_16x16x32_bf16 v[204:207],a[156:159],v[240:243],v[40:43]
ds_read_b64_tr_b16 a[232:233],v3 offset:512
ds_read_b64_tr_b16 a[234:235],v3 offset:4608
v_mfma_f32_16x16x32_bf16 v[204:207],a[152:155],v[208:211],v[204:207]
ds_read_b64_tr_b16 a[240:241],v2 offset:768
v_mfma_f32_16x16x32_bf16 v[204:207],a[148:151],v[176:179],v[204:207]
ds_read_b64_tr_b16 a[242:243],v2 offset:4864
v_mfma_f32_16x16x32_bf16 v[204:207],a[144:147],v[144:147],v[204:207]
buffer_store_dwordx4 v[80:83],v6,s[36:39],s85 offen sc1
v_mfma_f32_16x16x32_bf16 v[216:219],a[160:163],v[144:147],v[40:43]
ds_read_b64_tr_b16 a[244:245],v2 offset:8960
ds_read_b64_tr_b16 a[246:247],v2 offset:13056
v_mfma_f32_16x16x32_bf16 v[216:219],a[164:167],v[176:179],v[216:219]
ds_read_b64_tr_b16 a[252:253],v3 offset:8960
v_add_u32 v2,s48,v2
v_mfma_f32_16x16x32_bf16 v[216:219],a[168:171],v[208:211],v[216:219]
ds_read_b64_tr_b16 a[254:255],v3 offset:13056
v_mfma_f32_16x16x32_bf16 v[216:219],a[172:175],v[240:243],v[216:219]
ds_read_b64_tr_b16 a[248:249],v3 offset:768
v_mfma_f32_16x16x32_bf16 v[220:223],a[188:191],v[240:243],v[40:43]
ds_read_b64_tr_b16 a[250:251],v3 offset:4864
v_add_u32 v3,s48,v3
v_mfma_f32_16x16x32_bf16 v[220:223],a[184:187],v[208:211],v[220:223]
s_mov_b32 s48,98304
v_exp_f32 v174,v174
v_mfma_f32_16x16x32_bf16 v[220:223],a[180:183],v[176:179],v[220:223]
v_exp_f32 v175,v175
v_mfma_f32_16x16x32_bf16 v[220:223],a[176:179],v[144:147],v[220:223]
s_waitcnt lgkmcnt(6)
v_exp_f32 v184,v184
v_mfma_f32_16x16x32_bf16 a[0:3],a[192:195],v[136:139],a[0:3]
v_exp_f32 v185,v185
v_mfma_f32_16x16x32_bf16 a[0:3],a[196:199],v[140:143],a[0:3]
v_cvt_pk_bf16_f32 v171,v174,v175
v_cvt_pk_bf16_f32 v172,v184,v185
v_mfma_f32_16x16x32_bf16 a[4:7],a[204:207],v[140:143],a[4:7]
)A"
R"A(v_exp_f32 v186,v186
v_mfma_f32_16x16x32_bf16 a[4:7],a[200:203],v[136:139],a[4:7]
v_exp_f32 v187,v187
v_mfma_f32_16x16x32_bf16 a[8:11],a[208:211],v[136:139],a[8:11]
v_exp_f32 v188,v188
v_mfma_f32_16x16x32_bf16 a[8:11],a[212:215],v[140:143],a[8:11]
v_exp_f32 v189,v189
v_mfma_f32_16x16x32_bf16 a[12:15],a[220:223],v[140:143],a[12:15]
v_exp_f32 v190,v190
v_mfma_f32_16x16x32_bf16 a[12:15],a[216:219],v[136:139],a[12:15]
buffer_store_dwordx4 v[84:87],v6,s[36:39],s85 offen offset:1024 sc1
v_mfma_f32_16x16x32_bf16 a[16:19],a[224:227],v[136:139],a[16:19]
v_exp_f32 v191,v191
v_mfma_f32_16x16x32_bf16 a[16:19],a[228:231],v[140:143],a[16:19]
v_cvt_pk_bf16_f32 v173,v186,v187
v_cvt_pk_bf16_f32 v174,v188,v189
v_mfma_f32_16x16x32_bf16 a[20:23],a[236:239],v[140:143],a[20:23]
v_exp_f32 v200,v200
v_mfma_f32_16x16x32_bf16 a[20:23],a[232:235],v[136:139],a[20:23]
buffer_load_dwordx4 v10,s[72:75],s80 offen offset:3072 lds
v_mfma_f32_16x16x32_bf16 a[24:27],a[240:243],v[136:139],a[24:27]
s_waitcnt lgkmcnt(0)
v_exp_f32 v201,v201
v_mfma_f32_16x16x32_bf16 a[24:27],a[244:247],v[140:143],a[24:27]
v_cvt_pk_bf16_f32 v175,v190,v191
v_cvt_pk_bf16_f32 v200,v200,v201
v_mfma_f32_16x16x32_bf16 a[28:31],a[252:255],v[140:143],a[28:31]
v_exp_f32 v202,v202
v_mfma_f32_16x16x32_bf16 a[28:31],a[248:251],v[136:139],a[28:31]
v_exp_f32 v203,v203
v_smfmac_f32_16x16x64_bf16 v[48:51],v[28:31],v[136:143],v27
v_cvt_pk_bf16_f32 v201,v202,v203
v_mfma_f32_16x16x32_bf16 v[232:235],a[128:131],v[148:151],v[44:47]
s_waitcnt vmcnt(11)
v_exp_f32 v204,v204
v_mfma_f32_16x16x32_bf16 v[232:235],a[132:135],v[180:183],v[232:235]
s_barrier
v_exp_f32 v205,v205
v_mfma_f32_16x16x32_bf16 v[232:235],a[136:139],v[212:215],v[232:235]
ds_read_b128 a[128:131],v1
v_cvt_pk_bf16_f32 v202,v204,v205
v_mfma_f32_16x16x32_bf16 v[232:235],a[140:143],v[244:247],v[232:235]
v_exp_f32 v206,v206
v_mfma_f32_16x16x32_bf16 v[236:239],a[156:159],v[244:247],v[44:47]
v_exp_f32 v207,v207
v_mfma_f32_16x16x32_bf16 v[236:239],a[152:155],v[212:215],v[236:239]
buffer_store_dwordx4 v[88:91],v6,s[36:39],s85 offen offset:2048 sc1
v_mfma_f32_16x16x32_bf16 v[236:239],a[148:151],v[180:183],v[236:239]
s_addk_i32 m0,4096
ds_read_b128 a[132:135],v1 offset:256
v_cvt_pk_bf16_f32 v203,v206,v207
v_mfma_f32_16x16x32_bf16 v[236:239],a[144:147],v[148:151],v[236:239]
v_exp_f32 v216,v216
v_mfma_f32_16x16x32_bf16 v[248:251],a[160:163],v[148:151],v[44:47]
v_exp_f32 v217,v217
v_mfma_f32_16x16x32_bf16 v[248:251],a[164:167],v[180:183],v[248:251]
buffer_load_dwordx4 v11,s[72:75],s80 offen lds
v_mfma_f32_16x16x32_bf16 v[248:251],a[168:171],v[212:215],v[248:251]
ds_read_b128 a[136:139],v1 offset:512
v_cvt_pk_bf16_f32 v204,v216,v217
v_mfma_f32_16x16x32_bf16 v[248:251],a[172:175],v[244:247],v[248:251]
v_exp_f32 v218,v218
v_mfma_f32_16x16x32_bf16 v[252:255],a[188:191],v[244:247],v[44:47]
ds_read_b128 a[140:143],v1 offset:768
v_mfma_f32_16x16x32_bf16 v[252:255],a[184:187],v[212:215],v[252:255]
v_exp_f32 v219,v219
v_mfma_f32_16x16x32_bf16 v[252:255],a[180:183],v[180:183],v[252:255]
ds_read_b128 a[156:159],v1 offset:4864
v_cvt_pk_bf16_f32 v205,v218,v219
v_mfma_f32_16x16x32_bf16 v[252:255],a[176:179],v[148:151],v[252:255]
v_exp_f32 v220,v220
v_mfma_f32_16x16x32_bf16 a[32:35],a[192:195],v[168:171],a[32:35]
ds_read_b128 a[152:155],v1 offset:4608
v_mfma_f32_16x16x32_bf16 a[32:35],a[196:199],v[172:175],a[32:35]
v_exp_f32 v221,v221
v_mfma_f32_16x16x32_bf16 a[36:39],a[204:207],v[172:175],a[36:39]
ds_read_b128 a[148:151],v1 offset:4352
v_cvt_pk_bf16_f32 v206,v220,v221
v_mfma_f32_16x16x32_bf16 a[36:39],a[200:203],v[168:171],a[36:39]
v_exp_f32 v222,v222
v_mfma_f32_16x16x32_bf16 a[40:43],a[208:211],v[168:171],a[40:43]
ds_read_b128 a[144:147],v1 offset:4096
v_mfma_f32_16x16x32_bf16 a[40:43],a[212:215],v[172:175],a[40:43]
buffer_store_dwordx4 v[92:95],v6,s[36:39],s85 offen offset:3072 sc1
v_mfma_f32_16x16x32_bf16 a[44:47],a[220:223],v[172:175],a[44:47]
s_add_u32 s85,s55,135168
s_max_i32 s85,s85,s19
ds_read_b128 a[160:163],v1 offset:8192
v_mfma_f32_16x16x32_bf16 a[44:47],a[216:219],v[168:171],a[44:47]
v_exp_f32 v223,v223
v_mfma_f32_16x16x32_bf16 a[48:51],a[224:227],v[168:171],a[48:51]
ds_read_b128 a[164:167],v1 offset:8448
v_cvt_pk_bf16_f32 v207,v222,v223
v_mfma_f32_16x16x32_bf16 a[48:51],a[228:231],v[172:175],a[48:51]
v_exp_f32 v232,v232
v_mfma_f32_16x16x32_bf16 a[52:55],a[236:239],v[172:175],a[52:55]
s_addk_i32 m0,1024
ds_read_b128 a[168:171],v1 offset:8704
v_mfma_f32_16x16x32_bf16 a[52:55],a[232:235],v[168:171],a[52:55]
v_exp_f32 v233,v233
v_mfma_f32_16x16x32_bf16 a[56:59],a[240:243],v[168:171],a[56:59]
ds_read_b128 a[172:175],v1 offset:8960
v_cvt_pk_bf16_f32 v232,v232,v233
v_mfma_f32_16x16x32_bf16 a[56:59],a[244:247],v[172:175],a[56:59]
buffer_load_dwordx4 v7,s[72:75],s83 offen lds
v_mfma_f32_16x16x32_bf16 a[60:63],a[252:255],v[172:175],a[60:63]
ds_read_b128 a[188:191],v1 offset:13056
v_mfma_f32_16x16x32_bf16 a[60:63],a[248:251],v[168:171],a[60:63]
v_exp_f32 v234,v234
v_smfmac_f32_16x16x64_bf16 v[52:55],v[28:31],v[168:175],v27
s_waitcnt lgkmcnt(3)
ds_read_b128 a[184:187],v1 offset:12800
v_mfma_f32_16x16x32_bf16 v[136:139],a[128:131],v[128:131],v[32:35]
v_exp_f32 v235,v235
v_mfma_f32_16x16x32_bf16 v[136:139],a[132:135],v[160:163],v[136:139]
ds_read_b128 a[180:183],v1 offset:12544
v_cvt_pk_bf16_f32 v233,v234,v235
v_mfma_f32_16x16x32_bf16 v[136:139],a[136:139],v[192:195],v[136:139]
v_exp_f32 v236,v236
v_mfma_f32_16x16x32_bf16 v[136:139],a[140:143],v[224:227],v[136:139]
ds_read_b128 a[176:179],v1 offset:12288
v_add_u32 v1,s48,v1
v_mfma_f32_16x16x32_bf16 v[140:143],a[156:159],v[224:227],v[32:35]
buffer_store_dwordx4 v[96:99],v6,s[36:39],s86 offen sc1
v_mfma_f32_16x16x32_bf16 v[140:143],a[152:155],v[192:195],v[140:143]
v_exp_f32 v237,v237
v_mfma_f32_16x16x32_bf16 v[140:143],a[148:151],v[160:163],v[140:143]
v_exp_f32 v238,v238
v_mfma_f32_16x16x32_bf16 v[140:143],a[144:147],v[128:131],v[140:143]
v_exp_f32 v239,v239
v_mfma_f32_16x16x32_bf16 v[152:155],a[160:163],v[128:131],v[32:35]
v_cvt_pk_bf16_f32 v234,v236,v237
v_cvt_pk_bf16_f32 v235,v238,v239
v_mfma_f32_16x16x32_bf16 v[152:155],a[164:167],v[160:163],v[152:155]
s_waitcnt lgkmcnt(1)
v_exp_f32 v248,v248
v_mfma_f32_16x16x32_bf16 v[152:155],a[168:171],v[192:195],v[152:155]
v_exp_f32 v249,v249
v_mfma_f32_16x16x32_bf16 v[152:155],a[172:175],v[224:227],v[152:155]
v_exp_f32 v250,v250
v_mfma_f32_16x16x32_bf16 v[156:159],a[188:191],v[224:227],v[32:35]
v_exp_f32 v251,v251
v_mfma_f32_16x16x32_bf16 v[156:159],a[184:187],v[192:195],v[156:159]
v_exp_f32 v252,v252
v_mfma_f32_16x16x32_bf16 v[156:159],a[180:183],v[160:163],v[156:159]
s_waitcnt lgkmcnt(0)
v_exp_f32 v253,v253
v_mfma_f32_16x16x32_bf16 v[156:159],a[176:179],v[128:131],v[156:159]
v_exp_f32 v254,v254
v_mfma_f32_16x16x32_bf16 a[64:67],a[192:195],v[200:203],a[64:67]
buffer_load_dwordx4 v8,s[72:75],s83 offen offset:1024 lds
v_mfma_f32_16x16x32_bf16 a[64:67],a[196:199],v[204:207],a[64:67]
v_exp_f32 v255,v255
v_mfma_f32_16x16x32_bf16 a[68:71],a[204:207],v[204:207],a[68:71]
v_cvt_pk_bf16_f32 v236,v248,v249
v_cvt_pk_bf16_f32 v237,v250,v251
v_mfma_f32_16x16x32_bf16 a[68:71],a[200:203],v[200:203],a[68:71]
v_cvt_pk_bf16_f32 v238,v252,v253
v_cvt_pk_bf16_f32 v239,v254,v255
v_mfma_f32_16x16x32_bf16 a[72:75],a[208:211],v[200:203],a[72:75]
buffer_store_dwordx4 v[100:103],v6,s[36:39],s86 offen offset:1024 sc1
v_mfma_f32_16x16x32_bf16 a[72:75],a[212:215],v[204:207],a[72:75]
v_exp_f32 v136,v136
v_mfma_f32_16x16x32_bf16 a[76:79],a[220:223],v[204:207],a[76:79]
v_exp_f32 v137,v137
v_mfma_f32_16x16x32_bf16 a[76:79],a[216:219],v[200:203],a[76:79]
v_exp_f32 v138,v138
v_mfma_f32_16x16x32_bf16 a[80:83],a[224:227],v[200:203],a[80:83]
v_exp_f32 v139,v139
v_mfma_f32_16x16x32_bf16 a[80:83],a[228:231],v[204:207],a[80:83]
v_exp_f32 v140,v140
v_mfma_f32_16x16x32_bf16 a[84:87],a[236:239],v[204:207],a[84:87]
v_exp_f32 v141,v141
v_mfma_f32_16x16x32_bf16 a[84:87],a[232:235],v[200:203],a[84:87]
v_exp_f32 v142,v142
v_mfma_f32_16x16x32_bf16 a[88:91],a[240:243],v[200:203],a[88:91]
v_exp_f32 v143,v143
v_mfma_f32_16x16x32_bf16 a[88:91],a[244:247],v[204:207],a[88:91]
v_exp_f32 v152,v152
v_mfma_f32_16x16x32_bf16 a[92:95],a[252:255],v[204:207],a[92:95]
v_exp_f32 v153,v153
v_mfma_f32_16x16x32_bf16 a[92:95],a[248:251],v[200:203],a[92:95]
v_exp_f32 v154,v154
v_smfmac_f32_16x16x64_bf16 v[56:59],v[28:31],v[200:207],v27
v_cvt_pk_bf16_f32 v136,v136,v137
v_mfma_f32_16x16x32_bf16 v[168:171],a[128:131],v[132:135],v[36:39]
v_exp_f32 v155,v155
v_mfma_f32_16x16x32_bf16 v[168:171],a[132:135],v[164:167],v[168:171]
v_exp_f32 v156,v156
v_mfma_f32_16x16x32_bf16 v[168:171],a[136:139],v[196:199],v[168:171]
v_exp_f32 v157,v157
v_mfma_f32_16x16x32_bf16 v[168:171],a[140:143],v[228:231],v[168:171]
buffer_store_dwordx4 v[104:107],v6,s[36:39],s86 offen offset:2048 sc1
v_mfma_f32_16x16x32_bf16 v[172:175],a[156:159],v[228:231],v[36:39]
v_exp_f32 v158,v158
v_mfma_f32_16x16x32_bf16 v[172:175],a[152:155],v[196:199],v[172:175]
v_exp_f32 v159,v159
v_mfma_f32_16x16x32_bf16 v[172:175],a[148:151],v[164:167],v[172:175]
v_cvt_pk_bf16_f32 v137,v138,v139
v_cvt_pk_bf16_f32 v138,v140,v141
v_mfma_f32_16x16x32_bf16 v[172:175],a[144:147],v[132:135],v[172:175]
buffer_load_dwordx4 v9,s[72:75],s83 offen offset:2048 lds
v_mfma_f32_16x16x32_bf16 v[184:187],a[160:163],v[132:135],v[36:39]
v_cvt_pk_bf16_f32 v139,v142,v143
v_cvt_pk_bf16_f32 v140,v152,v153
v_mfma_f32_16x16x32_bf16 v[184:187],a[164:167],v[164:167],v[184:187]
v_cvt_pk_bf16_f32 v141,v154,v155
v_cvt_pk_bf16_f32 v142,v156,v157
v_mfma_f32_16x16x32_bf16 v[184:187],a[168:171],v[196:199],v[184:187]
v_exp_f32 v168,v168
v_mfma_f32_16x16x32_bf16 v[184:187],a[172:175],v[228:231],v[184:187]
v_exp_f32 v169,v169
v_mfma_f32_16x16x32_bf16 v[188:191],a[188:191],v[228:231],v[36:39]
v_cvt_pk_bf16_f32 v143,v158,v159
v_cvt_pk_bf16_f32 v168,v168,v169
v_mfma_f32_16x16x32_bf16 v[188:191],a[184:187],v[196:199],v[188:191]
v_exp_f32 v170,v170
v_mfma_f32_16x16x32_bf16 v[188:191],a[180:183],v[164:167],v[188:191]
v_exp_f32 v171,v171
v_mfma_f32_16x16x32_bf16 v[188:191],a[176:179],v[132:135],v[188:191]
v_exp_f32 v172,v172
v_mfma_f32_16x16x32_bf16 a[96:99],a[192:195],v[232:235],a[96:99]
ds_read_b64_tr_b16 a[192:193],v2
ds_read_b64_tr_b16 a[194:195],v2 offset:4096
v_mfma_f32_16x16x32_bf16 a[96:99],a[196:199],v[236:239],a[96:99]
ds_read_b64_tr_b16 a[196:197],v2 offset:8192
v_cvt_pk_bf16_f32 v169,v170,v171
v_mfma_f32_16x16x32_bf16 a[100:103],a[204:207],v[236:239],a[100:103]
v_exp_f32 v173,v173
v_mfma_f32_16x16x32_bf16 a[100:103],a[200:203],v[232:235],a[100:103]
buffer_store_dwordx4 v[108:111],v6,s[36:39],s86 offen offset:3072 sc1
v_mfma_f32_16x16x32_bf16 a[104:107],a[208:211],v[232:235],a[104:107]
s_add_u32 s86,s55,147456
ds_read_b64_tr_b16 a[198:199],v2 offset:12288
ds_read_b64_tr_b16 a[204:205],v3 offset:8192
v_mfma_f32_16x16x32_bf16 a[104:107],a[212:215],v[236:239],a[104:107]
s_max_i32 s86,s86,s100
ds_read_b64_tr_b16 a[206:207],v3 offset:12288
v_cvt_pk_bf16_f32 v170,v172,v173
v_mfma_f32_16x16x32_bf16 a[108:111],a[220:223],v[236:239],a[108:111]
v_exp_f32 v174,v174
v_mfma_f32_16x16x32_bf16 a[108:111],a[216:219],v[232:235],a[108:111]
buffer_load_dwordx4 v10,s[72:75],s83 offen offset:3072 lds
v_mfma_f32_16x16x32_bf16 a[112:115],a[224:227],v[232:235],a[112:115]
ds_read_b64_tr_b16 a[200:201],v3
ds_read_b64_tr_b16 a[202:203],v3 offset:4096
v_mfma_f32_16x16x32_bf16 a[112:115],a[228:231],v[236:239],a[112:115]
v_exp_f32 v175,v175
v_mfma_f32_16x16x32_bf16 a[116:119],a[236:239],v[236:239],a[116:119]
ds_read_b64_tr_b16 a[208:209],v2 offset:256
ds_read_b64_tr_b16 a[210:211],v2 offset:4352
v_mfma_f32_16x16x32_bf16 a[116:119],a[232:235],v[232:235],a[116:119]
ds_read_b64_tr_b16 a[212:213],v2 offset:8448
v_cvt_pk_bf16_f32 v171,v174,v175
v_mfma_f32_16x16x32_bf16 a[120:123],a[240:243],v[232:235],a[120:123]
ds_read_b64_tr_b16 a[214:215],v2 offset:12544
v_mfma_f32_16x16x32_bf16 a[120:123],a[244:247],v[236:239],a[120:123]
ds_read_b64_tr_b16 a[220:221],v3 offset:8448
v_mfma_f32_16x16x32_bf16 a[124:127],a[252:255],v[236:239],a[124:127]
ds_read_b64_tr_b16 a[222:223],v3 offset:12544
ds_read_b64_tr_b16 a[216:217],v3 offset:256
)A"
R"A(v_mfma_f32_16x16x32_bf16 a[124:127],a[248:251],v[232:235],a[124:127]
ds_read_b64_tr_b16 a[218:219],v3 offset:4352
v_smfmac_f32_16x16x64_bf16 v[60:63],v[28:31],v[232:239],v27
ds_read_b64_tr_b16 a[224:225],v2 offset:512
v_mfma_f32_16x16x32_bf16 v[200:203],a[128:131],v[144:147],v[40:43]
ds_read_b64_tr_b16 a[226:227],v2 offset:4608
v_mfma_f32_16x16x32_bf16 v[200:203],a[132:135],v[176:179],v[200:203]
ds_read_b64_tr_b16 a[228:229],v2 offset:8704
ds_read_b64_tr_b16 a[230:231],v2 offset:12800
v_mfma_f32_16x16x32_bf16 v[200:203],a[136:139],v[208:211],v[200:203]
buffer_store_dwordx4 v[112:115],v6,s[36:39],s87 offen sc1
v_mfma_f32_16x16x32_bf16 v[200:203],a[140:143],v[240:243],v[200:203]
ds_read_b64_tr_b16 a[236:237],v3 offset:8704
ds_read_b64_tr_b16 a[238:239],v3 offset:12800
v_mfma_f32_16x16x32_bf16 v[204:207],a[156:159],v[240:243],v[40:43]
ds_read_b64_tr_b16 a[232:233],v3 offset:512
v_mfma_f32_16x16x32_bf16 v[204:207],a[152:155],v[208:211],v[204:207]
ds_read_b64_tr_b16 a[234:235],v3 offset:4608
v_mfma_f32_16x16x32_bf16 v[204:207],a[148:151],v[176:179],v[204:207]
ds_read_b64_tr_b16 a[240:241],v2 offset:768
v_mfma_f32_16x16x32_bf16 v[204:207],a[144:147],v[144:147],v[204:207]
s_addk_i32 m0,4096
ds_read_b64_tr_b16 a[242:243],v2 offset:4864
ds_read_b64_tr_b16 a[244:245],v2 offset:8960
v_mfma_f32_16x16x32_bf16 v[216:219],a[160:163],v[144:147],v[40:43]
ds_read_b64_tr_b16 a[246:247],v2 offset:13056
v_add_u32 v2,s48,v2
v_mfma_f32_16x16x32_bf16 v[216:219],a[164:167],v[176:179],v[216:219]
ds_read_b64_tr_b16 a[252:253],v3 offset:8960
v_mfma_f32_16x16x32_bf16 v[216:219],a[168:171],v[208:211],v[216:219]
buffer_load_dwordx4 v11,s[72:75],s83 offen lds
v_mfma_f32_16x16x32_bf16 v[216:219],a[172:175],v[240:243],v[216:219]
ds_read_b64_tr_b16 a[254:255],v3 offset:13056
ds_read_b64_tr_b16 a[248:249],v3 offset:768
v_mfma_f32_16x16x32_bf16 v[220:223],a[188:191],v[240:243],v[40:43]
ds_read_b64_tr_b16 a[250:251],v3 offset:4864
v_add_u32 v3,s48,v3
v_mfma_f32_16x16x32_bf16 v[220:223],a[184:187],v[208:211],v[220:223]
v_exp_f32 v184,v184
v_mfma_f32_16x16x32_bf16 v[220:223],a[180:183],v[176:179],v[220:223]
v_exp_f32 v185,v185
v_mfma_f32_16x16x32_bf16 v[220:223],a[176:179],v[144:147],v[220:223]
s_waitcnt lgkmcnt(5)
v_exp_f32 v186,v186
v_mfma_f32_16x16x32_bf16 a[0:3],a[192:195],v[136:139],a[0:3]
v_exp_f32 v187,v187
v_mfma_f32_16x16x32_bf16 a[0:3],a[196:199],v[140:143],a[0:3]
s_add_u32 s80,s71,0
s_and_b32 s80,s80,3
s_lshl_b32 s80,s80,1
v_mfma_f32_16x16x32_bf16 a[4:7],a[204:207],v[140:143],a[4:7]
s_or_b32 s80,s80,1
buffer_store_dwordx4 v[116:119],v6,s[36:39],s87 offen offset:1024 sc1
v_mfma_f32_16x16x32_bf16 a[4:7],a[200:203],v[136:139],a[4:7]
s_mul_i32 s80,s80,163840
s_add_u32 s80,s80,s81
v_cvt_pk_bf16_f32 v172,v184,v185
v_mfma_f32_16x16x32_bf16 a[8:11],a[208:211],v[136:139],a[8:11]
s_add_u32 s83,s80,20480
v_exp_f32 v188,v188
v_mfma_f32_16x16x32_bf16 a[8:11],a[212:215],v[140:143],a[8:11]
v_exp_f32 v189,v189
v_mfma_f32_16x16x32_bf16 a[12:15],a[220:223],v[140:143],a[12:15]
v_exp_f32 v190,v190
v_mfma_f32_16x16x32_bf16 a[12:15],a[216:219],v[136:139],a[12:15]
v_exp_f32 v191,v191
v_mfma_f32_16x16x32_bf16 a[16:19],a[224:227],v[136:139],a[16:19]
v_cvt_pk_bf16_f32 v173,v186,v187
v_cvt_pk_bf16_f32 v174,v188,v189
v_mfma_f32_16x16x32_bf16 a[16:19],a[228:231],v[140:143],a[16:19]
v_exp_f32 v200,v200
v_mfma_f32_16x16x32_bf16 a[20:23],a[236:239],v[140:143],a[20:23]
v_exp_f32 v201,v201
v_mfma_f32_16x16x32_bf16 a[20:23],a[232:235],v[136:139],a[20:23]
v_cvt_pk_bf16_f32 v175,v190,v191
v_cvt_pk_bf16_f32 v200,v200,v201
v_mfma_f32_16x16x32_bf16 a[24:27],a[240:243],v[136:139],a[24:27]
s_waitcnt lgkmcnt(0)
v_exp_f32 v202,v202
v_mfma_f32_16x16x32_bf16 a[24:27],a[244:247],v[140:143],a[24:27]
v_exp_f32 v203,v203
v_mfma_f32_16x16x32_bf16 a[28:31],a[252:255],v[140:143],a[28:31]
v_exp_f32 v204,v204
v_mfma_f32_16x16x32_bf16 a[28:31],a[248:251],v[136:139],a[28:31]
v_exp_f32 v205,v205
v_smfmac_f32_16x16x64_bf16 v[48:51],v[28:31],v[136:143],v27
v_cvt_pk_bf16_f32 v201,v202,v203
v_mfma_f32_16x16x32_bf16 v[232:235],a[128:131],v[148:151],v[44:47]
v_exp_f32 v206,v206
v_mfma_f32_16x16x32_bf16 v[232:235],a[132:135],v[180:183],v[232:235]
s_barrier
buffer_store_dwordx4 v[120:123],v6,s[36:39],s87 offen offset:2048 sc1
v_mfma_f32_16x16x32_bf16 v[232:235],a[136:139],v[212:215],v[232:235]
ds_read_b128 a[128:131],v1
v_cvt_pk_bf16_f32 v202,v204,v205
v_mfma_f32_16x16x32_bf16 v[232:235],a[140:143],v[244:247],v[232:235]
v_exp_f32 v207,v207
v_mfma_f32_16x16x32_bf16 v[236:239],a[156:159],v[244:247],v[44:47]
s_add_u32 m0,s82,0
ds_read_b128 a[132:135],v1 offset:256
v_cvt_pk_bf16_f32 v203,v206,v207
v_mfma_f32_16x16x32_bf16 v[236:239],a[152:155],v[212:215],v[236:239]
v_exp_f32 v216,v216
v_mfma_f32_16x16x32_bf16 v[236:239],a[148:151],v[180:183],v[236:239]
v_exp_f32 v217,v217
v_mfma_f32_16x16x32_bf16 v[236:239],a[144:147],v[148:151],v[236:239]
buffer_load_dwordx4 v7,s[72:75],s80 offen lds
v_mfma_f32_16x16x32_bf16 v[248:251],a[160:163],v[148:151],v[44:47]
ds_read_b128 a[136:139],v1 offset:512
v_cvt_pk_bf16_f32 v204,v216,v217
v_mfma_f32_16x16x32_bf16 v[248:251],a[164:167],v[180:183],v[248:251]
v_exp_f32 v218,v218
v_mfma_f32_16x16x32_bf16 v[248:251],a[168:171],v[212:215],v[248:251]
ds_read_b128 a[140:143],v1 offset:768
v_mfma_f32_16x16x32_bf16 v[248:251],a[172:175],v[244:247],v[248:251]
v_exp_f32 v219,v219
v_mfma_f32_16x16x32_bf16 v[252:255],a[188:191],v[244:247],v[44:47]
ds_read_b128 a[156:159],v1 offset:4864
v_cvt_pk_bf16_f32 v205,v218,v219
v_mfma_f32_16x16x32_bf16 v[252:255],a[184:187],v[212:215],v[252:255]
v_exp_f32 v220,v220
v_mfma_f32_16x16x32_bf16 v[252:255],a[180:183],v[180:183],v[252:255]
ds_read_b128 a[152:155],v1 offset:4608
v_mfma_f32_16x16x32_bf16 v[252:255],a[176:179],v[148:151],v[252:255]
v_exp_f32 v221,v221
v_mfma_f32_16x16x32_bf16 a[32:35],a[192:195],v[168:171],a[32:35]
buffer_store_dwordx4 v[124:127],v6,s[36:39],s87 offen offset:3072 sc1
v_mfma_f32_16x16x32_bf16 a[32:35],a[196:199],v[172:175],a[32:35]
s_add_u32 s87,s55,151552
ds_read_b128 a[148:151],v1 offset:4352
v_cvt_pk_bf16_f32 v206,v220,v221
v_mfma_f32_16x16x32_bf16 a[36:39],a[204:207],v[172:175],a[36:39]
s_max_i32 s87,s87,s101
v_exp_f32 v222,v222
v_mfma_f32_16x16x32_bf16 a[36:39],a[200:203],v[168:171],a[36:39]
ds_read_b128 a[144:147],v1 offset:4096
v_mfma_f32_16x16x32_bf16 a[40:43],a[208:211],v[168:171],a[40:43]
buffer_load_dwordx4 v8,s[72:75],s80 offen offset:1024 lds
v_mfma_f32_16x16x32_bf16 a[40:43],a[212:215],v[172:175],a[40:43]
ds_read_b128 a[160:163],v1 offset:8192
v_mfma_f32_16x16x32_bf16 a[44:47],a[220:223],v[172:175],a[44:47]
v_exp_f32 v223,v223
v_mfma_f32_16x16x32_bf16 a[44:47],a[216:219],v[168:171],a[44:47]
ds_read_b128 a[164:167],v1 offset:8448
v_cvt_pk_bf16_f32 v207,v222,v223
v_mfma_f32_16x16x32_bf16 a[48:51],a[224:227],v[168:171],a[48:51]
v_exp_f32 v232,v232
v_mfma_f32_16x16x32_bf16 a[48:51],a[228:231],v[172:175],a[48:51]
ds_read_b128 a[168:171],v1 offset:8704
v_mfma_f32_16x16x32_bf16 a[52:55],a[236:239],v[172:175],a[52:55]
v_exp_f32 v233,v233
v_mfma_f32_16x16x32_bf16 a[52:55],a[232:235],v[168:171],a[52:55]
ds_read_b128 a[172:175],v1 offset:8960
v_cvt_pk_bf16_f32 v232,v232,v233
v_mfma_f32_16x16x32_bf16 a[56:59],a[240:243],v[168:171],a[56:59]
v_exp_f32 v234,v234
v_mfma_f32_16x16x32_bf16 a[56:59],a[244:247],v[172:175],a[56:59]
buffer_load_dwordx4 v9,s[72:75],s80 offen offset:2048 lds
v_mfma_f32_16x16x32_bf16 a[60:63],a[252:255],v[172:175],a[60:63]
ds_read_b128 a[188:191],v1 offset:13056
v_mfma_f32_16x16x32_bf16 a[60:63],a[248:251],v[168:171],a[60:63]
v_exp_f32 v235,v235
v_smfmac_f32_16x16x64_bf16 v[52:55],v[28:31],v[168:175],v27
s_waitcnt lgkmcnt(3)
ds_read_b128 a[184:187],v1 offset:12800
v_mfma_f32_16x16x32_bf16 v[136:139],a[128:131],v[128:131],v[32:35]
v_exp_f32 v236,v236
v_mfma_f32_16x16x32_bf16 v[136:139],a[132:135],v[160:163],v[136:139]
ds_read_b128 a[180:183],v1 offset:12544
v_cvt_pk_bf16_f32 v233,v234,v235
v_mfma_f32_16x16x32_bf16 v[136:139],a[136:139],v[192:195],v[136:139]
v_exp_f32 v237,v237
v_mfma_f32_16x16x32_bf16 v[136:139],a[140:143],v[224:227],v[136:139]
ds_read_b128 a[176:179],v1 offset:12288
v_cvt_pk_bf16_f32 v234,v236,v237
v_mfma_f32_16x16x32_bf16 v[140:143],a[156:159],v[224:227],v[32:35]
v_exp_f32 v238,v238
v_mfma_f32_16x16x32_bf16 v[140:143],a[152:155],v[192:195],v[140:143]
v_exp_f32 v239,v239
v_mfma_f32_16x16x32_bf16 v[140:143],a[148:151],v[160:163],v[140:143]
v_exp_f32 v248,v248
v_mfma_f32_16x16x32_bf16 v[140:143],a[144:147],v[128:131],v[140:143]
buffer_load_dwordx4 v10,s[72:75],s80 offen offset:3072 lds
v_mfma_f32_16x16x32_bf16 v[152:155],a[160:163],v[128:131],v[32:35]
v_exp_f32 v249,v249
v_mfma_f32_16x16x32_bf16 v[152:155],a[164:167],v[160:163],v[152:155]
s_waitcnt lgkmcnt(1)
v_cvt_pk_bf16_f32 v235,v238,v239
v_cvt_pk_bf16_f32 v236,v248,v249
v_mfma_f32_16x16x32_bf16 v[152:155],a[168:171],v[192:195],v[152:155]
v_exp_f32 v250,v250
v_mfma_f32_16x16x32_bf16 v[152:155],a[172:175],v[224:227],v[152:155]
v_exp_f32 v251,v251
v_mfma_f32_16x16x32_bf16 v[156:159],a[188:191],v[224:227],v[32:35]
v_exp_f32 v252,v252
v_mfma_f32_16x16x32_bf16 v[156:159],a[184:187],v[192:195],v[156:159]
v_exp_f32 v253,v253
v_mfma_f32_16x16x32_bf16 v[156:159],a[180:183],v[160:163],v[156:159]
s_waitcnt lgkmcnt(0)
v_exp_f32 v254,v254
v_mfma_f32_16x16x32_bf16 v[156:159],a[176:179],v[128:131],v[156:159]
s_addk_i32 m0,4096
v_exp_f32 v255,v255
v_mfma_f32_16x16x32_bf16 a[64:67],a[192:195],v[200:203],a[64:67]
v_cvt_pk_bf16_f32 v237,v250,v251
v_cvt_pk_bf16_f32 v238,v252,v253
v_mfma_f32_16x16x32_bf16 a[64:67],a[196:199],v[204:207],a[64:67]
v_cvt_pk_bf16_f32 v239,v254,v255
v_bfe_u32 v32,v6,4,2
v_mfma_f32_16x16x32_bf16 a[68:71],a[204:207],v[204:207],a[68:71]
buffer_load_dwordx4 v11,s[72:75],s80 offen lds
v_mfma_f32_16x16x32_bf16 a[68:71],a[200:203],v[200:203],a[68:71]
v_exp_f32 v136,v136
v_mfma_f32_16x16x32_bf16 a[72:75],a[208:211],v[200:203],a[72:75]
v_exp_f32 v137,v137
v_mfma_f32_16x16x32_bf16 a[72:75],a[212:215],v[204:207],a[72:75]
v_exp_f32 v138,v138
v_mfma_f32_16x16x32_bf16 a[76:79],a[220:223],v[204:207],a[76:79]
v_exp_f32 v139,v139
v_mfma_f32_16x16x32_bf16 a[76:79],a[216:219],v[200:203],a[76:79]
v_cvt_pk_bf16_f32 v136,v136,v137
v_cvt_pk_bf16_f32 v137,v138,v139
v_mfma_f32_16x16x32_bf16 a[80:83],a[224:227],v[200:203],a[80:83]
v_exp_f32 v140,v140
v_mfma_f32_16x16x32_bf16 a[80:83],a[228:231],v[204:207],a[80:83]
v_exp_f32 v141,v141
v_mfma_f32_16x16x32_bf16 a[84:87],a[236:239],v[204:207],a[84:87]
s_addk_i32 m0,1024
v_exp_f32 v142,v142
v_mfma_f32_16x16x32_bf16 a[84:87],a[232:235],v[200:203],a[84:87]
v_exp_f32 v143,v143
v_mfma_f32_16x16x32_bf16 a[88:91],a[240:243],v[200:203],a[88:91]
v_cvt_pk_bf16_f32 v138,v140,v141
v_cvt_pk_bf16_f32 v139,v142,v143
v_mfma_f32_16x16x32_bf16 a[88:91],a[244:247],v[204:207],a[88:91]
buffer_load_dwordx4 v7,s[72:75],s83 offen lds
v_mfma_f32_16x16x32_bf16 a[92:95],a[252:255],v[204:207],a[92:95]
v_exp_f32 v152,v152
v_mfma_f32_16x16x32_bf16 a[92:95],a[248:251],v[200:203],a[92:95]
v_exp_f32 v153,v153
v_smfmac_f32_16x16x64_bf16 v[56:59],v[28:31],v[200:207],v27
v_cvt_pk_bf16_f32 v140,v152,v153
v_mfma_f32_16x16x32_bf16 v[168:171],a[128:131],v[132:135],v[36:39]
s_lshl_b32 s95,s42,2
s_add_u32 s95,s95,s40
v_bfe_u32 v19,s55,13,1
v_mfma_f32_16x16x32_bf16 v[168:171],a[132:135],v[164:167],v[168:171]
s_lshl_b32 s95,s95,4
v_mov_b32 v18,s68
v_or_b32 v19,s69,v19
v_mfma_f32_16x16x32_bf16 v[168:171],a[136:139],v[196:199],v[168:171]
v_exp_f32 v154,v154
v_mfma_f32_16x16x32_bf16 v[168:171],a[140:143],v[228:231],v[168:171]
v_exp_f32 v155,v155
v_mfma_f32_16x16x32_bf16 v[172:175],a[156:159],v[228:231],v[36:39]
v_exp_f32 v156,v156
v_mfma_f32_16x16x32_bf16 v[172:175],a[152:155],v[196:199],v[172:175]
v_exp_f32 v157,v157
v_mfma_f32_16x16x32_bf16 v[172:175],a[148:151],v[164:167],v[172:175]
v_exp_f32 v158,v158
v_mfma_f32_16x16x32_bf16 v[172:175],a[144:147],v[132:135],v[172:175]
buffer_load_dwordx4 v8,s[72:75],s83 offen offset:1024 lds
v_mfma_f32_16x16x32_bf16 v[184:187],a[160:163],v[132:135],v[36:39]
v_exp_f32 v159,v159
v_mfma_f32_16x16x32_bf16 v[184:187],a[164:167],v[164:167],v[184:187]
v_cvt_pk_bf16_f32 v141,v154,v155
)A"
R"A(v_cvt_pk_bf16_f32 v142,v156,v157
v_mfma_f32_16x16x32_bf16 v[184:187],a[168:171],v[196:199],v[184:187]
v_cvt_pk_bf16_f32 v143,v158,v159
v_lshl_add_u32 v25,v32,3,v25
v_mfma_f32_16x16x32_bf16 v[184:187],a[172:175],v[228:231],v[184:187]
s_waitcnt vmcnt(6)
s_mov_b64 exec,1
buffer_store_dwordx2 v[18:19],v6,s[64:67],s95 offen sc1
s_mov_b64 exec,-1
v_mfma_f32_16x16x32_bf16 v[188:191],a[188:191],v[228:231],v[36:39]
v_exp_f32 v168,v168
v_mfma_f32_16x16x32_bf16 v[188:191],a[184:187],v[196:199],v[188:191]
v_exp_f32 v169,v169
v_mfma_f32_16x16x32_bf16 v[188:191],a[180:183],v[164:167],v[188:191]
v_exp_f32 v170,v170
v_mfma_f32_16x16x32_bf16 v[188:191],a[176:179],v[132:135],v[188:191]
v_exp_f32 v171,v171
v_mfma_f32_16x16x32_bf16 a[96:99],a[192:195],v[232:235],a[96:99]
ds_read_b64_tr_b16 a[192:193],v2
ds_read_b64_tr_b16 a[194:195],v2 offset:4096
v_mfma_f32_16x16x32_bf16 a[96:99],a[196:199],v[236:239],a[96:99]
ds_read_b64_tr_b16 a[196:197],v2 offset:8192
v_cvt_pk_bf16_f32 v168,v168,v169
v_mfma_f32_16x16x32_bf16 a[100:103],a[204:207],v[236:239],a[100:103]
buffer_load_dwordx4 v9,s[72:75],s83 offen offset:2048 lds
v_mfma_f32_16x16x32_bf16 a[100:103],a[200:203],v[232:235],a[100:103]
ds_read_b64_tr_b16 a[198:199],v2 offset:12288
ds_read_b64_tr_b16 a[204:205],v3 offset:8192
v_mfma_f32_16x16x32_bf16 a[104:107],a[208:211],v[232:235],a[104:107]
ds_read_b64_tr_b16 a[206:207],v3 offset:12288
v_cvt_pk_bf16_f32 v169,v170,v171
v_mfma_f32_16x16x32_bf16 a[104:107],a[212:215],v[236:239],a[104:107]
ds_read_b64_tr_b16 a[200:201],v3
v_bfe_u32 v32,v6,8,1
v_mfma_f32_16x16x32_bf16 a[108:111],a[220:223],v[236:239],a[108:111]
ds_read_b64_tr_b16 a[202:203],v3 offset:4096
v_lshlrev_b32 v32,5,v32
v_mfma_f32_16x16x32_bf16 a[108:111],a[216:219],v[232:235],a[108:111]
ds_read_b64_tr_b16 a[208:209],v2 offset:256
ds_read_b64_tr_b16 a[210:211],v2 offset:4352
v_mfma_f32_16x16x32_bf16 a[112:115],a[224:227],v[232:235],a[112:115]
ds_read_b64_tr_b16 a[212:213],v2 offset:8448
v_add_u32 v1,v25,v32
v_mfma_f32_16x16x32_bf16 a[112:115],a[228:231],v[236:239],a[112:115]
ds_read_b64_tr_b16 a[214:215],v2 offset:12544
v_add_u32 v1,81920,v1
v_mfma_f32_16x16x32_bf16 a[116:119],a[236:239],v[236:239],a[116:119]
v_exp_f32 v172,v172
v_mfma_f32_16x16x32_bf16 a[116:119],a[232:235],v[232:235],a[116:119]
ds_read_b64_tr_b16 a[220:221],v3 offset:8448
ds_read_b64_tr_b16 a[222:223],v3 offset:12544
v_mfma_f32_16x16x32_bf16 a[120:123],a[240:243],v[232:235],a[120:123]
s_cmp_ge_u32 0,s71
s_cselect_b32 s80,2,0
ds_read_b64_tr_b16 a[216:217],v3 offset:256
v_mfma_f32_16x16x32_bf16 a[120:123],a[244:247],v[236:239],a[120:123]
s_mul_i32 s80,s80,163840
buffer_load_dwordx4 v10,s[72:75],s83 offen offset:3072 lds
v_mfma_f32_16x16x32_bf16 a[124:127],a[252:255],v[236:239],a[124:127]
s_add_u32 s80,s80,s81
ds_read_b64_tr_b16 a[218:219],v3 offset:4352
ds_read_b64_tr_b16 a[224:225],v2 offset:512
v_mfma_f32_16x16x32_bf16 a[124:127],a[248:251],v[232:235],a[124:127]
ds_read_b64_tr_b16 a[226:227],v2 offset:4608
v_smfmac_f32_16x16x64_bf16 v[60:63],v[28:31],v[232:239],v27
ds_read_b64_tr_b16 a[228:229],v2 offset:8704
v_mfma_f32_16x16x32_bf16 v[200:203],a[128:131],v[144:147],v[40:43]
ds_read_b64_tr_b16 a[230:231],v2 offset:12800
v_mfma_f32_16x16x32_bf16 v[200:203],a[132:135],v[176:179],v[200:203]
ds_read_b64_tr_b16 a[236:237],v3 offset:8704
ds_read_b64_tr_b16 a[238:239],v3 offset:12800
v_mfma_f32_16x16x32_bf16 v[200:203],a[136:139],v[208:211],v[200:203]
ds_read_b64_tr_b16 a[232:233],v3 offset:512
v_mfma_f32_16x16x32_bf16 v[200:203],a[140:143],v[240:243],v[200:203]
ds_read_b64_tr_b16 a[234:235],v3 offset:4608
v_mfma_f32_16x16x32_bf16 v[204:207],a[156:159],v[240:243],v[40:43]
s_addk_i32 m0,4096
ds_read_b64_tr_b16 a[240:241],v2 offset:768
v_mfma_f32_16x16x32_bf16 v[204:207],a[152:155],v[208:211],v[204:207]
ds_read_b64_tr_b16 a[242:243],v2 offset:4864
ds_read_b64_tr_b16 a[244:245],v2 offset:8960
v_mfma_f32_16x16x32_bf16 v[204:207],a[148:151],v[176:179],v[204:207]
ds_read_b64_tr_b16 a[246:247],v2 offset:13056
v_sub_u32 v2,v25,v32
v_mfma_f32_16x16x32_bf16 v[204:207],a[144:147],v[144:147],v[204:207]
buffer_load_dwordx4 v11,s[72:75],s83 offen lds
v_mfma_f32_16x16x32_bf16 v[216:219],a[160:163],v[144:147],v[40:43]
s_add_u32 s83,s80,20480
ds_read_b64_tr_b16 a[252:253],v3 offset:8960
ds_read_b64_tr_b16 a[254:255],v3 offset:13056
v_mfma_f32_16x16x32_bf16 v[216:219],a[164:167],v[176:179],v[216:219]
ds_read_b64_tr_b16 a[248:249],v3 offset:768
v_add_u32 v2,81920,v2
v_mfma_f32_16x16x32_bf16 v[216:219],a[168:171],v[208:211],v[216:219]
ds_read_b64_tr_b16 a[250:251],v3 offset:4864
v_mfma_f32_16x16x32_bf16 v[216:219],a[172:175],v[240:243],v[216:219]
v_exp_f32 v173,v173
v_mfma_f32_16x16x32_bf16 v[220:223],a[188:191],v[240:243],v[40:43]
v_exp_f32 v174,v174
v_mfma_f32_16x16x32_bf16 v[220:223],a[184:187],v[208:211],v[220:223]
v_exp_f32 v175,v175
v_mfma_f32_16x16x32_bf16 v[220:223],a[180:183],v[176:179],v[220:223]
v_cvt_pk_bf16_f32 v170,v172,v173
v_cvt_pk_bf16_f32 v171,v174,v175
v_mfma_f32_16x16x32_bf16 v[220:223],a[176:179],v[144:147],v[220:223]
s_add_u32 m0,s82,40960
v_exp_f32 v184,v184
s_waitcnt lgkmcnt(4)
v_mfma_f32_16x16x32_bf16 a[0:3],a[192:195],v[136:139],a[0:3]
v_exp_f32 v185,v185
v_mfma_f32_16x16x32_bf16 a[0:3],a[196:199],v[140:143],a[0:3]
v_exp_f32 v186,v186
v_mfma_f32_16x16x32_bf16 a[4:7],a[204:207],v[140:143],a[4:7]
buffer_load_dwordx4 v7,s[72:75],s80 offen lds
v_mfma_f32_16x16x32_bf16 a[4:7],a[200:203],v[136:139],a[4:7]
v_exp_f32 v187,v187
v_mfma_f32_16x16x32_bf16 a[8:11],a[208:211],v[136:139],a[8:11]
v_exp_f32 v188,v188
v_mfma_f32_16x16x32_bf16 a[8:11],a[212:215],v[140:143],a[8:11]
v_exp_f32 v189,v189
v_mfma_f32_16x16x32_bf16 a[12:15],a[220:223],v[140:143],a[12:15]
v_exp_f32 v190,v190
v_mfma_f32_16x16x32_bf16 a[12:15],a[216:219],v[136:139],a[12:15]
v_exp_f32 v191,v191
v_mfma_f32_16x16x32_bf16 a[16:19],a[224:227],v[136:139],a[16:19]
v_cvt_pk_bf16_f32 v172,v184,v185
v_cvt_pk_bf16_f32 v173,v186,v187
v_mfma_f32_16x16x32_bf16 a[16:19],a[228:231],v[140:143],a[16:19]
v_cvt_pk_bf16_f32 v174,v188,v189
v_cvt_pk_bf16_f32 v175,v190,v191
v_mfma_f32_16x16x32_bf16 a[20:23],a[236:239],v[140:143],a[20:23]
v_exp_f32 v200,v200
v_mfma_f32_16x16x32_bf16 a[20:23],a[232:235],v[136:139],a[20:23]
v_exp_f32 v201,v201
v_mfma_f32_16x16x32_bf16 a[24:27],a[240:243],v[136:139],a[24:27]
v_exp_f32 v202,v202
v_mfma_f32_16x16x32_bf16 a[24:27],a[244:247],v[140:143],a[24:27]
s_waitcnt lgkmcnt(0)
buffer_load_dwordx4 v8,s[72:75],s80 offen offset:1024 lds
v_mfma_f32_16x16x32_bf16 a[28:31],a[252:255],v[140:143],a[28:31]
v_exp_f32 v203,v203
v_mfma_f32_16x16x32_bf16 a[28:31],a[248:251],v[136:139],a[28:31]
v_cvt_pk_bf16_f32 v200,v200,v201
v_cvt_pk_bf16_f32 v201,v202,v203
v_smfmac_f32_16x16x64_bf16 v[48:51],v[28:31],v[136:143],v27
s_barrier
v_exp_f32 v204,v204
v_mfma_f32_16x16x32_bf16 v[232:235],a[128:131],v[148:151],v[44:47]
v_exp_f32 v205,v205
v_mfma_f32_16x16x32_bf16 v[232:235],a[132:135],v[180:183],v[232:235]
v_exp_f32 v206,v206
v_mfma_f32_16x16x32_bf16 v[232:235],a[136:139],v[212:215],v[232:235]
v_exp_f32 v207,v207
v_mfma_f32_16x16x32_bf16 v[232:235],a[140:143],v[244:247],v[232:235]
v_cvt_pk_bf16_f32 v202,v204,v205
v_cvt_pk_bf16_f32 v203,v206,v207
v_mfma_f32_16x16x32_bf16 v[236:239],a[156:159],v[244:247],v[44:47]
v_exp_f32 v216,v216
v_mfma_f32_16x16x32_bf16 v[236:239],a[152:155],v[212:215],v[236:239]
v_exp_f32 v217,v217
v_mfma_f32_16x16x32_bf16 v[236:239],a[148:151],v[180:183],v[236:239]
buffer_load_dwordx4 v9,s[72:75],s80 offen offset:2048 lds
v_mfma_f32_16x16x32_bf16 v[236:239],a[144:147],v[148:151],v[236:239]
v_exp_f32 v218,v218
v_mfma_f32_16x16x32_bf16 v[248:251],a[160:163],v[148:151],v[44:47]
v_exp_f32 v219,v219
v_mfma_f32_16x16x32_bf16 v[248:251],a[164:167],v[180:183],v[248:251]
v_exp_f32 v220,v220
v_mfma_f32_16x16x32_bf16 v[248:251],a[168:171],v[212:215],v[248:251]
v_exp_f32 v221,v221
v_mfma_f32_16x16x32_bf16 v[248:251],a[172:175],v[244:247],v[248:251]
v_exp_f32 v222,v222
v_mfma_f32_16x16x32_bf16 v[252:255],a[188:191],v[244:247],v[44:47]
v_exp_f32 v223,v223
v_mfma_f32_16x16x32_bf16 v[252:255],a[184:187],v[212:215],v[252:255]
v_cvt_pk_bf16_f32 v204,v216,v217
v_cvt_pk_bf16_f32 v205,v218,v219
v_mfma_f32_16x16x32_bf16 v[252:255],a[180:183],v[180:183],v[252:255]
v_cvt_pk_bf16_f32 v206,v220,v221
v_cvt_pk_bf16_f32 v207,v222,v223
v_mfma_f32_16x16x32_bf16 v[252:255],a[176:179],v[148:151],v[252:255]
v_exp_f32 v232,v232
v_mfma_f32_16x16x32_bf16 a[32:35],a[192:195],v[168:171],a[32:35]
v_exp_f32 v233,v233
v_mfma_f32_16x16x32_bf16 a[32:35],a[196:199],v[172:175],a[32:35]
buffer_load_dwordx4 v10,s[72:75],s80 offen offset:3072 lds
v_mfma_f32_16x16x32_bf16 a[36:39],a[204:207],v[172:175],a[36:39]
v_exp_f32 v234,v234
v_mfma_f32_16x16x32_bf16 a[36:39],a[200:203],v[168:171],a[36:39]
v_exp_f32 v235,v235
v_mfma_f32_16x16x32_bf16 a[40:43],a[208:211],v[168:171],a[40:43]
v_cvt_pk_bf16_f32 v232,v232,v233
v_add_f32 v48,v49,v48
v_mfma_f32_16x16x32_bf16 a[40:43],a[212:215],v[172:175],a[40:43]
v_exp_f32 v236,v236
v_mfma_f32_16x16x32_bf16 a[44:47],a[220:223],v[172:175],a[44:47]
v_exp_f32 v237,v237
v_mfma_f32_16x16x32_bf16 a[44:47],a[216:219],v[168:171],a[44:47]
v_exp_f32 v238,v238
v_mfma_f32_16x16x32_bf16 a[48:51],a[224:227],v[168:171],a[48:51]
v_exp_f32 v239,v239
v_mfma_f32_16x16x32_bf16 a[48:51],a[228:231],v[172:175],a[48:51]
s_addk_i32 m0,4096
v_cvt_pk_bf16_f32 v233,v234,v235
v_max_f32 v20,0x00800000,v48
v_mfma_f32_16x16x32_bf16 a[52:55],a[236:239],v[172:175],a[52:55]
v_cvt_pk_bf16_f32 v234,v236,v237
v_cvt_pk_bf16_f32 v235,v238,v239
v_mfma_f32_16x16x32_bf16 a[52:55],a[232:235],v[168:171],a[52:55]
v_exp_f32 v248,v248
v_mfma_f32_16x16x32_bf16 a[56:59],a[240:243],v[168:171],a[56:59]
buffer_load_dwordx4 v11,s[72:75],s80 offen lds
v_mfma_f32_16x16x32_bf16 a[56:59],a[244:247],v[172:175],a[56:59]
v_exp_f32 v249,v249
v_mfma_f32_16x16x32_bf16 a[60:63],a[252:255],v[172:175],a[60:63]
v_exp_f32 v250,v250
v_mfma_f32_16x16x32_bf16 a[60:63],a[248:251],v[168:171],a[60:63]
v_exp_f32 v251,v251
v_smfmac_f32_16x16x64_bf16 v[52:55],v[28:31],v[168:175],v27
v_cvt_pk_bf16_f32 v236,v248,v249
v_mfma_f32_16x16x32_bf16 a[64:67],a[192:195],v[200:203],a[64:67]
v_exp_f32 v252,v252
v_mfma_f32_16x16x32_bf16 a[64:67],a[196:199],v[204:207],a[64:67]
v_exp_f32 v253,v253
v_mfma_f32_16x16x32_bf16 a[68:71],a[204:207],v[204:207],a[68:71]
v_exp_f32 v254,v254
v_mfma_f32_16x16x32_bf16 a[68:71],a[200:203],v[200:203],a[68:71]
s_addk_i32 m0,1024
v_exp_f32 v255,v255
v_mfma_f32_16x16x32_bf16 a[72:75],a[208:211],v[200:203],a[72:75]
v_rcp_f32 v20,v20
v_mfma_f32_16x16x32_bf16 a[72:75],a[212:215],v[204:207],a[72:75]
v_cvt_pk_bf16_f32 v237,v250,v251
v_cvt_pk_bf16_f32 v238,v252,v253
v_mfma_f32_16x16x32_bf16 a[76:79],a[220:223],v[204:207],a[76:79]
buffer_load_dwordx4 v7,s[72:75],s83 offen lds
v_mfma_f32_16x16x32_bf16 a[76:79],a[216:219],v[200:203],a[76:79]
ds_read_b64_tr_b16 a[128:129],v1
v_cvt_pk_bf16_f32 v239,v254,v255
v_mfma_f32_16x16x32_bf16 a[80:83],a[224:227],v[200:203],a[80:83]
.p2align 3
v_accvgpr_read_b32 v136,a0
v_accvgpr_read_b32 v137,a1
v_accvgpr_read_b32 v138,a2
v_accvgpr_read_b32 v139,a3
v_accvgpr_read_b32 v12,a4
v_accvgpr_read_b32 v13,a5
v_accvgpr_read_b32 v14,a6
v_accvgpr_read_b32 v15,a7
v_pk_mul_f32 v[136:137],v[20:21],v[136:137] op_sel:[0,0] op_sel_hi:[0,1]
v_pk_mul_f32 v[138:139],v[20:21],v[138:139] op_sel:[0,0] op_sel_hi:[0,1]
v_pk_mul_f32 v[12:13],v[20:21],v[12:13] op_sel:[0,0] op_sel_hi:[0,1]
v_pk_mul_f32 v[14:15],v[20:21],v[14:15] op_sel:[0,0] op_sel_hi:[0,1]
v_cvt_pk_bf16_f32 v136,v136,v137
v_cvt_pk_bf16_f32 v137,v138,v139
v_cvt_pk_bf16_f32 v138,v12,v13
v_cvt_pk_bf16_f32 v139,v14,v15
v_mfma_f32_16x16x32_bf16 a[80:83],a[228:231],v[204:207],a[80:83]
ds_read_b64_tr_b16 a[130:131],v1 offset:5120
ds_read_b64_tr_b16 a[132:133],v1 offset:10240
v_mfma_f32_16x16x32_bf16 a[84:87],a[236:239],v[204:207],a[84:87]
ds_read_b64_tr_b16 a[134:135],v1 offset:15360
v_accvgpr_read_b32 v152,a16
v_mfma_f32_16x16x32_bf16 a[84:87],a[232:235],v[200:203],a[84:87]
ds_read_b64_tr_b16 a[136:137],v1 offset:20480
v_accvgpr_read_b32 v153,a17
v_mfma_f32_16x16x32_bf16 a[88:91],a[240:243],v[200:203],a[88:91]
ds_read_b64_tr_b16 a[138:139],v1 offset:25600
)A"
R"A(v_mul_f32 v152,v20,v152
v_mfma_f32_16x16x32_bf16 a[88:91],a[244:247],v[204:207],a[88:91]
.p2align 3
v_accvgpr_read_b32 v140,a8
v_accvgpr_read_b32 v141,a9
v_accvgpr_read_b32 v142,a10
v_accvgpr_read_b32 v143,a11
v_accvgpr_read_b32 v12,a12
v_accvgpr_read_b32 v13,a13
v_accvgpr_read_b32 v14,a14
v_accvgpr_read_b32 v15,a15
v_pk_mul_f32 v[140:141],v[20:21],v[140:141] op_sel:[0,0] op_sel_hi:[0,1]
v_pk_mul_f32 v[142:143],v[20:21],v[142:143] op_sel:[0,0] op_sel_hi:[0,1]
v_pk_mul_f32 v[12:13],v[20:21],v[12:13] op_sel:[0,0] op_sel_hi:[0,1]
v_pk_mul_f32 v[14:15],v[20:21],v[14:15] op_sel:[0,0] op_sel_hi:[0,1]
v_cvt_pk_bf16_f32 v140,v140,v141
v_cvt_pk_bf16_f32 v141,v142,v143
v_cvt_pk_bf16_f32 v142,v12,v13
v_cvt_pk_bf16_f32 v143,v14,v15
v_mfma_f32_16x16x32_bf16 a[92:95],a[252:255],v[204:207],a[92:95]
ds_read_b64_tr_b16 a[140:141],v1 offset:30720
ds_read_b64_tr_b16 a[142:143],v1 offset:35840
v_mfma_f32_16x16x32_bf16 a[92:95],a[248:251],v[200:203],a[92:95]
v_mul_f32 v153,v20,v153
v_accvgpr_read_b32 v154,a18
v_smfmac_f32_16x16x64_bf16 v[56:59],v[28:31],v[200:207],v27
v_accvgpr_read_b32 v155,a19
v_mfma_f32_16x16x32_bf16 a[96:99],a[192:195],v[232:235],a[96:99]
buffer_store_dwordx4 v[136:139],v6,s[36:39],s84 offen sc1
v_mfma_f32_16x16x32_bf16 a[96:99],a[196:199],v[236:239],a[96:99]
v_cvt_pk_bf16_f32 v152,v152,v153
v_mul_f32 v154,v20,v154
v_mfma_f32_16x16x32_bf16 a[100:103],a[204:207],v[236:239],a[100:103]
v_mul_f32 v155,v20,v155
v_cvt_pk_bf16_f32 v153,v154,v155
v_mfma_f32_16x16x32_bf16 a[100:103],a[200:203],v[232:235],a[100:103]
v_accvgpr_read_b32 v154,a20
v_accvgpr_read_b32 v155,a21
v_mfma_f32_16x16x32_bf16 a[104:107],a[208:211],v[232:235],a[104:107]
buffer_load_dwordx4 v8,s[72:75],s83 offen offset:1024 lds
v_mfma_f32_16x16x32_bf16 a[104:107],a[212:215],v[236:239],a[104:107]
v_accvgpr_read_b32 v12,a22
v_accvgpr_read_b32 v13,a23
v_mfma_f32_16x16x32_bf16 a[108:111],a[220:223],v[236:239],a[108:111]
v_mul_f32 v154,v20,v154
v_mul_f32 v155,v20,v155
v_mfma_f32_16x16x32_bf16 a[108:111],a[216:219],v[232:235],a[108:111]
v_mul_f32 v12,v20,v12
v_mul_f32 v13,v20,v13
v_mfma_f32_16x16x32_bf16 a[112:115],a[224:227],v[232:235],a[112:115]
v_cvt_pk_bf16_f32 v154,v154,v155
v_cvt_pk_bf16_f32 v155,v12,v13
v_mfma_f32_16x16x32_bf16 a[112:115],a[228:231],v[236:239],a[112:115]
ds_read_b64_tr_b16 a[144:145],v2 offset:32
ds_read_b64_tr_b16 a[146:147],v2 offset:5152
v_mfma_f32_16x16x32_bf16 a[116:119],a[236:239],v[236:239],a[116:119]
ds_read_b64_tr_b16 a[148:149],v2 offset:10272
v_accvgpr_read_b32 v156,a24
v_mfma_f32_16x16x32_bf16 a[116:119],a[232:235],v[232:235],a[116:119]
buffer_load_dwordx4 v9,s[72:75],s83 offen offset:2048 lds
v_mfma_f32_16x16x32_bf16 a[120:123],a[240:243],v[232:235],a[120:123]
ds_read_b64_tr_b16 a[150:151],v2 offset:15392
ds_read_b64_tr_b16 a[152:153],v2 offset:20512
v_mfma_f32_16x16x32_bf16 a[120:123],a[244:247],v[236:239],a[120:123]
ds_read_b64_tr_b16 a[154:155],v2 offset:25632
v_accvgpr_read_b32 v157,a25
v_mfma_f32_16x16x32_bf16 a[124:127],a[252:255],v[236:239],a[124:127]
ds_read_b64_tr_b16 a[156:157],v2 offset:30752
v_mul_f32 v156,v20,v156
v_mfma_f32_16x16x32_bf16 a[124:127],a[248:251],v[232:235],a[124:127]
ds_read_b64_tr_b16 a[158:159],v2 offset:35872
v_mul_f32 v157,v20,v157
v_smfmac_f32_16x16x64_bf16 v[60:63],v[28:31],v[232:239],v27
s_waitcnt lgkmcnt(8)
v_accvgpr_read_b32 v158,a26
v_mfma_f32_16x16x32_bf16 a[192:195],a[128:131],v[64:67],0
s_lshl_b32 s94,s70,4
v_accvgpr_read_b32 v159,a27
v_cvt_pk_bf16_f32 v156,v156,v157
v_mfma_f32_16x16x32_bf16 a[192:195],a[132:135],v[68:71],a[192:195]
s_add_u32 s94,s94,s40
v_mul_f32 v158,v20,v158
v_mul_f32 v159,v20,v159
v_mfma_f32_16x16x32_bf16 a[192:195],a[136:139],v[72:75],a[192:195]
s_lshl_b32 s94,s94,4
buffer_store_dwordx4 v[140:143],v6,s[36:39],s84 offen offset:1024 sc1
v_mfma_f32_16x16x32_bf16 a[192:195],a[140:143],v[76:79],a[192:195]
v_cvt_pk_bf16_f32 v157,v158,v159
v_accvgpr_read_b32 v158,a28
v_mfma_f32_16x16x32_bf16 a[196:199],a[140:143],v[92:95],0
v_accvgpr_read_b32 v159,a29
v_accvgpr_read_b32 v14,a30
v_mfma_f32_16x16x32_bf16 a[196:199],a[136:139],v[88:91],a[196:199]
v_accvgpr_read_b32 v15,a31
v_mul_f32 v158,v20,v158
v_mfma_f32_16x16x32_bf16 a[196:199],a[132:135],v[84:87],a[196:199]
buffer_load_dwordx4 v10,s[72:75],s83 offen offset:3072 lds
v_mfma_f32_16x16x32_bf16 a[196:199],a[128:131],v[80:83],a[196:199]
v_mul_f32 v159,v20,v159
v_mul_f32 v14,v20,v14
v_mfma_f32_16x16x32_bf16 a[200:203],a[128:131],v[96:99],0
v_mul_f32 v15,v20,v15
v_cvt_pk_bf16_f32 v158,v158,v159
v_mfma_f32_16x16x32_bf16 a[200:203],a[132:135],v[100:103],a[200:203]
ds_read_b64_tr_b16 a[160:161],v1 offset:64
v_cvt_pk_bf16_f32 v159,v14,v15
v_mfma_f32_16x16x32_bf16 a[200:203],a[136:139],v[104:107],a[200:203]
ds_read_b64_tr_b16 a[162:163],v1 offset:5184
ds_read_b64_tr_b16 a[164:165],v1 offset:10304
v_mfma_f32_16x16x32_bf16 a[200:203],a[140:143],v[108:111],a[200:203]
ds_read_b64_tr_b16 a[166:167],v1 offset:15424
v_add_f32 v52,v53,v52
v_mfma_f32_16x16x32_bf16 a[204:207],a[140:143],v[124:127],0
ds_read_b64_tr_b16 a[168:169],v1 offset:20544
v_max_f32 v21,0x00800000,v52
v_mfma_f32_16x16x32_bf16 a[204:207],a[136:139],v[120:123],a[204:207]
s_addk_i32 m0,4096
ds_read_b64_tr_b16 a[170:171],v1 offset:25664
v_bfe_u32 v24,v6,4,2
v_mfma_f32_16x16x32_bf16 a[204:207],a[132:135],v[116:119],a[204:207]
ds_read_b64_tr_b16 a[172:173],v1 offset:30784
ds_read_b64_tr_b16 a[174:175],v1 offset:35904
v_mfma_f32_16x16x32_bf16 a[204:207],a[128:131],v[112:115],a[204:207]
s_waitcnt lgkmcnt(8)
v_rcp_f32 v21,v21
v_mfma_f32_16x16x32_bf16 a[208:211],a[144:147],v[64:67],0
buffer_load_dwordx4 v11,s[72:75],s83 offen lds
v_mfma_f32_16x16x32_bf16 a[208:211],a[148:151],v[68:71],a[208:211]
.p2align 3
v_accvgpr_read_b32 v168,a32
v_accvgpr_read_b32 v169,a33
v_accvgpr_read_b32 v170,a34
v_accvgpr_read_b32 v171,a35
v_accvgpr_read_b32 v12,a36
v_accvgpr_read_b32 v13,a37
v_accvgpr_read_b32 v14,a38
v_accvgpr_read_b32 v15,a39
v_pk_mul_f32 v[168:169],v[20:21],v[168:169] op_sel:[1,0] op_sel_hi:[1,1]
v_pk_mul_f32 v[170:171],v[20:21],v[170:171] op_sel:[1,0] op_sel_hi:[1,1]
v_pk_mul_f32 v[12:13],v[20:21],v[12:13] op_sel:[1,0] op_sel_hi:[1,1]
v_pk_mul_f32 v[14:15],v[20:21],v[14:15] op_sel:[1,0] op_sel_hi:[1,1]
v_cvt_pk_bf16_f32 v168,v168,v169
v_cvt_pk_bf16_f32 v169,v170,v171
v_cvt_pk_bf16_f32 v170,v12,v13
v_cvt_pk_bf16_f32 v171,v14,v15
v_mfma_f32_16x16x32_bf16 a[208:211],a[152:155],v[72:75],a[208:211]
v_min_u32 v24,2,v24
v_add_u32 v24,s71,v24
v_mfma_f32_16x16x32_bf16 a[208:211],a[156:159],v[76:79],a[208:211]
s_cmp_ge_u32 1,s71
s_cselect_b32 s80,4,2
v_add_u32 v24,1,v24
v_mfma_f32_16x16x32_bf16 a[212:215],a[156:159],v[92:95],0
s_mul_i32 s80,s80,163840
s_add_u32 s80,s80,s81
v_and_b32 v24,3,v24
v_mfma_f32_16x16x32_bf16 a[212:215],a[152:155],v[88:91],a[212:215]
s_add_u32 s83,s80,20480
ds_read_b64_tr_b16 a[176:177],v2 offset:96
v_lshlrev_b32 v24,6,v24
v_mfma_f32_16x16x32_bf16 a[212:215],a[148:151],v[84:87],a[212:215]
buffer_store_dwordx4 v[152:155],v6,s[36:39],s84 offen offset:2048 sc1
v_mfma_f32_16x16x32_bf16 a[212:215],a[144:147],v[80:83],a[212:215]
ds_read_b64_tr_b16 a[178:179],v2 offset:5216
ds_read_b64_tr_b16 a[180:181],v2 offset:10336
v_mfma_f32_16x16x32_bf16 a[216:219],a[144:147],v[96:99],0
ds_read_b64_tr_b16 a[182:183],v2 offset:15456
v_accvgpr_read_b32 v184,a48
v_mfma_f32_16x16x32_bf16 a[216:219],a[148:151],v[100:103],a[216:219]
ds_read_b64_tr_b16 a[184:185],v2 offset:20576
v_accvgpr_read_b32 v185,a49
v_mfma_f32_16x16x32_bf16 a[216:219],a[152:155],v[104:107],a[216:219]
ds_read_b64_tr_b16 a[186:187],v2 offset:25696
v_mul_f32 v184,v21,v184
v_mfma_f32_16x16x32_bf16 a[216:219],a[156:159],v[108:111],a[216:219]
ds_read_b64_tr_b16 a[188:189],v2 offset:30816
ds_read_b64_tr_b16 a[190:191],v2 offset:35936
v_mfma_f32_16x16x32_bf16 a[220:223],a[156:159],v[124:127],0
.p2align 3
v_accvgpr_read_b32 v172,a40
v_accvgpr_read_b32 v173,a41
v_accvgpr_read_b32 v174,a42
v_accvgpr_read_b32 v175,a43
v_accvgpr_read_b32 v12,a44
v_accvgpr_read_b32 v13,a45
v_accvgpr_read_b32 v14,a46
v_accvgpr_read_b32 v15,a47
v_pk_mul_f32 v[172:173],v[20:21],v[172:173] op_sel:[1,0] op_sel_hi:[1,1]
v_pk_mul_f32 v[174:175],v[20:21],v[174:175] op_sel:[1,0] op_sel_hi:[1,1]
v_pk_mul_f32 v[12:13],v[20:21],v[12:13] op_sel:[1,0] op_sel_hi:[1,1]
v_pk_mul_f32 v[14:15],v[20:21],v[14:15] op_sel:[1,0] op_sel_hi:[1,1]
v_cvt_pk_bf16_f32 v172,v172,v173
v_cvt_pk_bf16_f32 v173,v174,v175
v_cvt_pk_bf16_f32 v174,v12,v13
v_cvt_pk_bf16_f32 v175,v14,v15
v_mfma_f32_16x16x32_bf16 a[220:223],a[152:155],v[120:123],a[220:223]
v_mul_f32 v185,v21,v185
v_accvgpr_read_b32 v186,a50
v_mfma_f32_16x16x32_bf16 a[220:223],a[148:151],v[116:119],a[220:223]
s_add_u32 m0,s82,122880
v_accvgpr_read_b32 v187,a51
v_cvt_pk_bf16_f32 v184,v184,v185
v_mfma_f32_16x16x32_bf16 a[220:223],a[144:147],v[112:115],a[220:223]
s_waitcnt lgkmcnt(5)
v_mul_f32 v186,v21,v186
v_mul_f32 v187,v21,v187
v_mfma_f32_16x16x32_bf16 a[224:227],a[160:163],v[64:67],0
v_cvt_pk_bf16_f32 v185,v186,v187
v_accvgpr_read_b32 v186,a52
v_mfma_f32_16x16x32_bf16 a[224:227],a[164:167],v[68:71],a[224:227]
buffer_load_dwordx4 v7,s[72:75],s80 offen lds
v_mfma_f32_16x16x32_bf16 a[224:227],a[168:171],v[72:75],a[224:227]
v_accvgpr_read_b32 v187,a53
v_accvgpr_read_b32 v16,a54
v_mfma_f32_16x16x32_bf16 a[224:227],a[172:175],v[76:79],a[224:227]
v_accvgpr_read_b32 v17,a55
v_mul_f32 v186,v21,v186
v_mfma_f32_16x16x32_bf16 a[228:231],a[172:175],v[92:95],0
v_mul_f32 v187,v21,v187
v_mul_f32 v16,v21,v16
v_mfma_f32_16x16x32_bf16 a[228:231],a[168:171],v[88:91],a[228:231]
v_mul_f32 v17,v21,v17
v_cvt_pk_bf16_f32 v186,v186,v187
v_mfma_f32_16x16x32_bf16 a[228:231],a[164:167],v[84:87],a[228:231]
ds_read_b64_tr_b16 a[128:129],v1 offset:128
v_cvt_pk_bf16_f32 v187,v16,v17
v_mfma_f32_16x16x32_bf16 a[228:231],a[160:163],v[80:83],a[228:231]
ds_read_b64_tr_b16 a[130:131],v1 offset:5248
ds_read_b64_tr_b16 a[132:133],v1 offset:10368
v_mfma_f32_16x16x32_bf16 a[232:235],a[160:163],v[96:99],0
buffer_load_dwordx2 v[4:5],v24,s[64:67],s94 offen sc1
v_mfma_f32_16x16x32_bf16 a[232:235],a[164:167],v[100:103],a[232:235]
ds_read_b64_tr_b16 a[134:135],v1 offset:15488
ds_read_b64_tr_b16 a[136:137],v1 offset:20608
v_mfma_f32_16x16x32_bf16 a[232:235],a[168:171],v[104:107],a[232:235]
ds_read_b64_tr_b16 a[138:139],v1 offset:25728
v_accvgpr_read_b32 v188,a56
v_mfma_f32_16x16x32_bf16 a[232:235],a[172:175],v[108:111],a[232:235]
ds_read_b64_tr_b16 a[140:141],v1 offset:30848
v_accvgpr_read_b32 v189,a57
v_mfma_f32_16x16x32_bf16 a[236:239],a[172:175],v[124:127],0
buffer_store_dwordx4 v[156:159],v6,s[36:39],s84 offen offset:3072 sc1
v_mfma_f32_16x16x32_bf16 a[236:239],a[168:171],v[120:123],a[236:239]
ds_read_b64_tr_b16 a[142:143],v1 offset:35968
v_mul_f32 v188,v21,v188
v_mfma_f32_16x16x32_bf16 a[236:239],a[164:167],v[116:119],a[236:239]
v_mul_f32 v189,v21,v189
v_accvgpr_read_b32 v190,a58
v_mfma_f32_16x16x32_bf16 a[236:239],a[160:163],v[112:115],a[236:239]
v_accvgpr_read_b32 v191,a59
v_cvt_pk_bf16_f32 v188,v188,v189
v_mfma_f32_16x16x32_bf16 a[240:243],a[176:179],v[64:67],0
s_waitcnt lgkmcnt(5)
buffer_load_dwordx4 v8,s[72:75],s80 offen offset:1024 lds
v_mfma_f32_16x16x32_bf16 a[240:243],a[180:183],v[68:71],a[240:243]
v_mul_f32 v190,v21,v190
v_mul_f32 v191,v21,v191
v_mfma_f32_16x16x32_bf16 a[240:243],a[184:187],v[72:75],a[240:243]
v_cvt_pk_bf16_f32 v189,v190,v191
v_accvgpr_read_b32 v190,a60
v_mfma_f32_16x16x32_bf16 a[240:243],a[188:191],v[76:79],a[240:243]
v_accvgpr_read_b32 v191,a61
v_accvgpr_read_b32 v12,a62
v_mfma_f32_16x16x32_bf16 a[244:247],a[188:191],v[92:95],0
v_accvgpr_read_b32 v13,a63
v_mul_f32 v190,v21,v190
v_mfma_f32_16x16x32_bf16 a[244:247],a[184:187],v[88:91],a[244:247]
v_mul_f32 v191,v21,v191
v_mul_f32 v12,v21,v12
v_mfma_f32_16x16x32_bf16 a[244:247],a[180:183],v[84:87],a[244:247]
v_mul_f32 v13,v21,v13
v_cvt_pk_bf16_f32 v190,v190,v191
v_mfma_f32_16x16x32_bf16 a[244:247],a[176:179],v[80:83],a[244:247]
ds_read_b64_tr_b16 a[144:145],v2 offset:160
v_cvt_pk_bf16_f32 v191,v12,v13
v_mfma_f32_16x16x32_bf16 a[248:251],a[176:179],v[96:99],0
ds_read_b64_tr_b16 a[146:147],v2 offset:5280
ds_read_b64_tr_b16 a[148:149],v2 offset:10400
v_mfma_f32_16x16x32_bf16 a[248:251],a[180:183],v[100:103],a[248:251]
)A"
R"A(buffer_load_dwordx4 v9,s[72:75],s80 offen offset:2048 lds
v_mfma_f32_16x16x32_bf16 a[248:251],a[184:187],v[104:107],a[248:251]
ds_read_b64_tr_b16 a[150:151],v2 offset:15520
ds_read_b64_tr_b16 a[152:153],v2 offset:20640
v_mfma_f32_16x16x32_bf16 a[248:251],a[188:191],v[108:111],a[248:251]
ds_read_b64_tr_b16 a[154:155],v2 offset:25760
v_add_f32 v56,v57,v56
v_mfma_f32_16x16x32_bf16 a[252:255],a[188:191],v[124:127],0
ds_read_b64_tr_b16 a[156:157],v2 offset:30880
v_max_f32 v22,0x00800000,v56
v_mfma_f32_16x16x32_bf16 a[252:255],a[184:187],v[120:123],a[252:255]
ds_read_b64_tr_b16 a[158:159],v2 offset:36000
v_accvgpr_read_b32 v216,a80
v_mfma_f32_16x16x32_bf16 a[252:255],a[180:183],v[116:119],a[252:255]
s_waitcnt vmcnt(18)
v_rcp_f32 v22,v22
v_mfma_f32_16x16x32_bf16 a[252:255],a[176:179],v[112:115],a[252:255]
s_barrier
buffer_store_dwordx4 v[168:171],v6,s[36:39],s85 offen sc1
v_mfma_f32_16x16x32_bf16 a[0:3],a[128:131],v[64:67],0
.p2align 3
v_accvgpr_read_b32 v200,a64
v_accvgpr_read_b32 v201,a65
v_accvgpr_read_b32 v202,a66
v_accvgpr_read_b32 v203,a67
v_accvgpr_read_b32 v12,a68
v_accvgpr_read_b32 v13,a69
v_accvgpr_read_b32 v14,a70
v_accvgpr_read_b32 v15,a71
v_pk_mul_f32 v[200:201],v[22:23],v[200:201] op_sel:[0,0] op_sel_hi:[0,1]
v_pk_mul_f32 v[202:203],v[22:23],v[202:203] op_sel:[0,0] op_sel_hi:[0,1]
v_pk_mul_f32 v[12:13],v[22:23],v[12:13] op_sel:[0,0] op_sel_hi:[0,1]
v_pk_mul_f32 v[14:15],v[22:23],v[14:15] op_sel:[0,0] op_sel_hi:[0,1]
v_cvt_pk_bf16_f32 v200,v200,v201
v_cvt_pk_bf16_f32 v201,v202,v203
v_cvt_pk_bf16_f32 v202,v12,v13
v_cvt_pk_bf16_f32 v203,v14,v15
s_waitcnt lgkmcnt(5)
v_mfma_f32_16x16x32_bf16 a[0:3],a[132:135],v[68:71],a[0:3]
ds_read_b64_tr_b16 a[160:161],v1 offset:192
ds_read_b64_tr_b16 a[162:163],v1 offset:5312
v_mfma_f32_16x16x32_bf16 a[0:3],a[136:139],v[72:75],a[0:3]
ds_read_b64_tr_b16 a[164:165],v1 offset:10432
v_accvgpr_read_b32 v217,a81
v_mfma_f32_16x16x32_bf16 a[0:3],a[140:143],v[76:79],a[0:3]
ds_read_b64_tr_b16 a[166:167],v1 offset:15552
v_mul_f32 v216,v22,v216
v_mfma_f32_16x16x32_bf16 a[4:7],a[140:143],v[92:95],0
buffer_load_dwordx4 v10,s[72:75],s80 offen offset:3072 lds
v_mfma_f32_16x16x32_bf16 a[4:7],a[136:139],v[88:91],a[4:7]
ds_read_b64_tr_b16 a[168:169],v1 offset:20672
ds_read_b64_tr_b16 a[170:171],v1 offset:25792
v_mfma_f32_16x16x32_bf16 a[4:7],a[132:135],v[84:87],a[4:7]
.p2align 3
v_accvgpr_read_b32 v204,a72
v_accvgpr_read_b32 v205,a73
v_accvgpr_read_b32 v206,a74
v_accvgpr_read_b32 v207,a75
v_accvgpr_read_b32 v12,a76
v_accvgpr_read_b32 v13,a77
v_accvgpr_read_b32 v14,a78
v_accvgpr_read_b32 v15,a79
v_pk_mul_f32 v[204:205],v[22:23],v[204:205] op_sel:[0,0] op_sel_hi:[0,1]
v_pk_mul_f32 v[206:207],v[22:23],v[206:207] op_sel:[0,0] op_sel_hi:[0,1]
v_pk_mul_f32 v[12:13],v[22:23],v[12:13] op_sel:[0,0] op_sel_hi:[0,1]
v_pk_mul_f32 v[14:15],v[22:23],v[14:15] op_sel:[0,0] op_sel_hi:[0,1]
v_cvt_pk_bf16_f32 v204,v204,v205
v_cvt_pk_bf16_f32 v205,v206,v207
v_cvt_pk_bf16_f32 v206,v12,v13
v_cvt_pk_bf16_f32 v207,v14,v15
v_mfma_f32_16x16x32_bf16 a[4:7],a[128:131],v[80:83],a[4:7]
ds_read_b64_tr_b16 a[172:173],v1 offset:30912
ds_read_b64_tr_b16 a[174:175],v1 offset:36032
v_mfma_f32_16x16x32_bf16 a[8:11],a[128:131],v[96:99],0
v_mul_f32 v217,v22,v217
v_accvgpr_read_b32 v218,a82
v_mfma_f32_16x16x32_bf16 a[8:11],a[132:135],v[100:103],a[8:11]
v_accvgpr_read_b32 v219,a83
v_cvt_pk_bf16_f32 v216,v216,v217
v_mfma_f32_16x16x32_bf16 a[8:11],a[136:139],v[104:107],a[8:11]
v_mul_f32 v218,v22,v218
v_mul_f32 v219,v22,v219
v_mfma_f32_16x16x32_bf16 a[8:11],a[140:143],v[108:111],a[8:11]
s_waitcnt vmcnt(5)
v_cmp_ne_u32 s[90:91],s68,v4
v_and_b32 v4,-2,v5
v_cmp_ne_u32 s[92:93],s69,v4
s_nop 0
s_or_b64 s[90:91],s[90:91],s[92:93]
s_cbranch_scc1 L_fr8_%=
L_fo8_%=:
v_mfma_f32_16x16x32_bf16 a[12:15],a[140:143],v[124:127],0
s_nop 0
v_readlane_b32 s76,v5,0
v_readlane_b32 s77,v5,1
v_readlane_b32 s78,v5,2
v_mfma_f32_16x16x32_bf16 a[12:15],a[136:139],v[120:123],a[12:15]
s_addk_i32 m0,4096
s_and_b32 s77,s77,1
s_lshl_b32 s77,s77,2
v_mfma_f32_16x16x32_bf16 a[12:15],a[132:135],v[116:119],a[12:15]
s_nop 1
s_and_b32 s54,s76,1
v_mfma_f32_16x16x32_bf16 a[12:15],a[128:131],v[112:115],a[12:15]
s_and_b32 s78,s78,1
s_or_b32 s54,s54,s77
s_lshl_b32 s78,s78,4
v_mfma_f32_16x16x32_bf16 a[16:19],a[144:147],v[64:67],0
s_or_b32 s54,s54,s78
buffer_load_dwordx4 v11,s[72:75],s80 offen lds
s_waitcnt lgkmcnt(0)
v_mfma_f32_16x16x32_bf16 a[16:19],a[148:151],v[68:71],a[16:19]
s_mulk_i32 s54,3084
s_lshl_b32 s76,s71,1
s_lshl_b32 s54,s54,s76
v_mfma_f32_16x16x32_bf16 a[16:19],a[152:155],v[72:75],a[16:19]
s_lshr_b32 s54,s54,8
s_cmp_ge_u32 0,s71
s_cselect_b32 s76,2,0
v_mfma_f32_16x16x32_bf16 a[16:19],a[156:159],v[76:79],a[16:19]
s_bitcmp1_b32 s54,s76
s_cselect_b32 s77,8192,0
s_lshl_b32 s76,s76,17
v_mfma_f32_16x16x32_bf16 a[20:23],a[156:159],v[92:95],0
s_add_u32 s76,s76,s77
buffer_store_dwordx4 v[172:175],v6,s[36:39],s85 offen offset:1024 sc1
v_mfma_f32_16x16x32_bf16 a[20:23],a[152:155],v[88:91],a[20:23]
s_add_u32 s77,s76,4096
s_add_u32 s78,s76,16384
s_add_u32 s79,s76,20480
v_mfma_f32_16x16x32_bf16 a[20:23],a[148:151],v[84:87],a[20:23]
s_max_i32 s76,s76,s18
v_cvt_pk_bf16_f32 v217,v218,v219
v_accvgpr_read_b32 v218,a84
v_mfma_f32_16x16x32_bf16 a[20:23],a[144:147],v[80:83],a[20:23]
s_max_i32 s77,s77,s19
v_accvgpr_read_b32 v219,a85
v_accvgpr_read_b32 v14,a86
v_mfma_f32_16x16x32_bf16 a[24:27],a[144:147],v[96:99],0
s_max_i32 s78,s78,s100
v_accvgpr_read_b32 v15,a87
v_mul_f32 v218,v22,v218
v_mfma_f32_16x16x32_bf16 a[24:27],a[148:151],v[100:103],a[24:27]
s_addk_i32 m0,1024
v_mul_f32 v219,v22,v219
v_mul_f32 v14,v22,v14
v_mfma_f32_16x16x32_bf16 a[24:27],a[152:155],v[104:107],a[24:27]
s_max_i32 s79,s79,s101
v_mul_f32 v15,v22,v15
v_cvt_pk_bf16_f32 v218,v218,v219
v_mfma_f32_16x16x32_bf16 a[24:27],a[156:159],v[108:111],a[24:27]
ds_read_b64_tr_b16 a[176:177],v2 offset:224
v_cvt_pk_bf16_f32 v219,v14,v15
v_mfma_f32_16x16x32_bf16 a[28:31],a[156:159],v[124:127],0
buffer_load_dwordx4 v7,s[72:75],s83 offen lds
v_mfma_f32_16x16x32_bf16 a[28:31],a[152:155],v[120:123],a[28:31]
ds_read_b64_tr_b16 a[178:179],v2 offset:5344
ds_read_b64_tr_b16 a[180:181],v2 offset:10464
v_mfma_f32_16x16x32_bf16 a[28:31],a[148:151],v[116:119],a[28:31]
ds_read_b64_tr_b16 a[182:183],v2 offset:15584
v_accvgpr_read_b32 v220,a88
v_mfma_f32_16x16x32_bf16 a[28:31],a[144:147],v[112:115],a[28:31]
ds_read_b64_tr_b16 a[184:185],v2 offset:20704
v_accvgpr_read_b32 v221,a89
v_mfma_f32_16x16x32_bf16 a[32:35],a[160:163],v[64:67],0
buffer_load_dwordx4 v[128:131],v6,s[28:31],s76 offen sc1
v_mfma_f32_16x16x32_bf16 a[32:35],a[164:167],v[68:71],a[32:35]
ds_read_b64_tr_b16 a[186:187],v2 offset:25824
ds_read_b64_tr_b16 a[188:189],v2 offset:30944
v_mfma_f32_16x16x32_bf16 a[32:35],a[168:171],v[72:75],a[32:35]
ds_read_b64_tr_b16 a[190:191],v2 offset:36064
v_mul_f32 v220,v22,v220
v_mfma_f32_16x16x32_bf16 a[32:35],a[172:175],v[76:79],a[32:35]
v_mul_f32 v221,v22,v221
v_accvgpr_read_b32 v222,a90
v_mfma_f32_16x16x32_bf16 a[36:39],a[172:175],v[92:95],0
v_accvgpr_read_b32 v223,a91
v_cvt_pk_bf16_f32 v220,v220,v221
v_mfma_f32_16x16x32_bf16 a[36:39],a[168:171],v[88:91],a[36:39]
v_mul_f32 v222,v22,v222
v_mul_f32 v223,v22,v223
v_mfma_f32_16x16x32_bf16 a[36:39],a[164:167],v[84:87],a[36:39]
buffer_store_dwordx4 v[184:187],v6,s[36:39],s85 offen offset:2048 sc1
v_mfma_f32_16x16x32_bf16 a[36:39],a[160:163],v[80:83],a[36:39]
v_cvt_pk_bf16_f32 v221,v222,v223
v_accvgpr_read_b32 v222,a92
v_mfma_f32_16x16x32_bf16 a[40:43],a[160:163],v[96:99],0
v_accvgpr_read_b32 v223,a93
v_accvgpr_read_b32 v16,a94
v_mfma_f32_16x16x32_bf16 a[40:43],a[164:167],v[100:103],a[40:43]
v_accvgpr_read_b32 v17,a95
v_mul_f32 v222,v22,v222
v_mfma_f32_16x16x32_bf16 a[40:43],a[168:171],v[104:107],a[40:43]
buffer_load_dwordx4 v8,s[72:75],s83 offen offset:1024 lds
v_mfma_f32_16x16x32_bf16 a[40:43],a[172:175],v[108:111],a[40:43]
v_mul_f32 v223,v22,v223
v_mul_f32 v16,v22,v16
v_mfma_f32_16x16x32_bf16 a[44:47],a[172:175],v[124:127],0
v_mul_f32 v17,v22,v17
v_cvt_pk_bf16_f32 v222,v222,v223
v_mfma_f32_16x16x32_bf16 a[44:47],a[168:171],v[120:123],a[44:47]
ds_read_b64_tr_b16 a[128:129],v1 offset:256
v_cvt_pk_bf16_f32 v223,v16,v17
v_mfma_f32_16x16x32_bf16 a[44:47],a[164:167],v[116:119],a[44:47]
buffer_load_dwordx4 v[160:163],v6,s[28:31],s76 offen offset:1024 sc1
v_mfma_f32_16x16x32_bf16 a[44:47],a[160:163],v[112:115],a[44:47]
s_waitcnt lgkmcnt(1)
ds_read_b64_tr_b16 a[130:131],v1 offset:5376
ds_read_b64_tr_b16 a[132:133],v1 offset:10496
v_mfma_f32_16x16x32_bf16 a[48:51],a[176:179],v[64:67],0
ds_read_b64_tr_b16 a[134:135],v1 offset:15616
v_add_f32 v60,v61,v60
v_mfma_f32_16x16x32_bf16 a[48:51],a[180:183],v[68:71],a[48:51]
ds_read_b64_tr_b16 a[136:137],v1 offset:20736
v_max_f32 v23,0x00800000,v60
v_mfma_f32_16x16x32_bf16 a[48:51],a[184:187],v[72:75],a[48:51]
ds_read_b64_tr_b16 a[138:139],v1 offset:25856
v_accvgpr_read_b32 v248,a112
v_mfma_f32_16x16x32_bf16 a[48:51],a[188:191],v[76:79],a[48:51]
buffer_load_dwordx4 v[192:195],v6,s[28:31],s76 offen offset:2048 sc1
v_mfma_f32_16x16x32_bf16 a[52:55],a[188:191],v[92:95],0
ds_read_b64_tr_b16 a[140:141],v1 offset:30976
ds_read_b64_tr_b16 a[142:143],v1 offset:36096
v_mfma_f32_16x16x32_bf16 a[52:55],a[184:187],v[88:91],a[52:55]
v_rcp_f32 v23,v23
v_mfma_f32_16x16x32_bf16 a[52:55],a[180:183],v[84:87],a[52:55]
.p2align 3
v_accvgpr_read_b32 v232,a96
v_accvgpr_read_b32 v233,a97
v_accvgpr_read_b32 v234,a98
v_accvgpr_read_b32 v235,a99
v_accvgpr_read_b32 v12,a100
v_accvgpr_read_b32 v13,a101
v_accvgpr_read_b32 v14,a102
v_accvgpr_read_b32 v15,a103
v_pk_mul_f32 v[232:233],v[22:23],v[232:233] op_sel:[1,0] op_sel_hi:[1,1]
v_pk_mul_f32 v[234:235],v[22:23],v[234:235] op_sel:[1,0] op_sel_hi:[1,1]
v_pk_mul_f32 v[12:13],v[22:23],v[12:13] op_sel:[1,0] op_sel_hi:[1,1]
v_pk_mul_f32 v[14:15],v[22:23],v[14:15] op_sel:[1,0] op_sel_hi:[1,1]
v_cvt_pk_bf16_f32 v232,v232,v233
v_cvt_pk_bf16_f32 v233,v234,v235
v_cvt_pk_bf16_f32 v234,v12,v13
v_cvt_pk_bf16_f32 v235,v14,v15
v_mfma_f32_16x16x32_bf16 a[52:55],a[176:179],v[80:83],a[52:55]
buffer_load_dwordx4 v9,s[72:75],s83 offen offset:2048 lds
v_mfma_f32_16x16x32_bf16 a[56:59],a[176:179],v[96:99],0
ds_read_b64_tr_b16 a[144:145],v2 offset:288
ds_read_b64_tr_b16 a[146:147],v2 offset:5408
v_mfma_f32_16x16x32_bf16 a[56:59],a[180:183],v[100:103],a[56:59]
ds_read_b64_tr_b16 a[148:149],v2 offset:10528
v_accvgpr_read_b32 v249,a113
v_mfma_f32_16x16x32_bf16 a[56:59],a[184:187],v[104:107],a[56:59]
ds_read_b64_tr_b16 a[150:151],v2 offset:15648
v_mul_f32 v248,v23,v248
v_mfma_f32_16x16x32_bf16 a[56:59],a[188:191],v[108:111],a[56:59]
buffer_store_dwordx4 v[188:191],v6,s[36:39],s85 offen offset:3072 sc1
v_mfma_f32_16x16x32_bf16 a[60:63],a[188:191],v[124:127],0
ds_read_b64_tr_b16 a[152:153],v2 offset:20768
ds_read_b64_tr_b16 a[154:155],v2 offset:25888
v_mfma_f32_16x16x32_bf16 a[60:63],a[184:187],v[120:123],a[60:63]
.p2align 3
v_accvgpr_read_b32 v236,a104
v_accvgpr_read_b32 v237,a105
v_accvgpr_read_b32 v238,a106
v_accvgpr_read_b32 v239,a107
v_accvgpr_read_b32 v12,a108
v_accvgpr_read_b32 v13,a109
v_accvgpr_read_b32 v14,a110
v_accvgpr_read_b32 v15,a111
v_pk_mul_f32 v[236:237],v[22:23],v[236:237] op_sel:[1,0] op_sel_hi:[1,1]
v_pk_mul_f32 v[238:239],v[22:23],v[238:239] op_sel:[1,0] op_sel_hi:[1,1]
v_pk_mul_f32 v[12:13],v[22:23],v[12:13] op_sel:[1,0] op_sel_hi:[1,1]
v_pk_mul_f32 v[14:15],v[22:23],v[14:15] op_sel:[1,0] op_sel_hi:[1,1]
v_cvt_pk_bf16_f32 v236,v236,v237
v_cvt_pk_bf16_f32 v237,v238,v239
v_cvt_pk_bf16_f32 v238,v12,v13
v_cvt_pk_bf16_f32 v239,v14,v15
v_mfma_f32_16x16x32_bf16 a[60:63],a[180:183],v[116:119],a[60:63]
ds_read_b64_tr_b16 a[156:157],v2 offset:31008
ds_read_b64_tr_b16 a[158:159],v2 offset:36128
v_mfma_f32_16x16x32_bf16 a[60:63],a[176:179],v[112:115],a[60:63]
s_waitcnt lgkmcnt(8)
buffer_load_dwordx4 v[224:227],v6,s[28:31],s76 offen offset:3072 sc1
v_mfma_f32_16x16x32_bf16 a[64:67],a[128:131],v[64:67],0
v_mul_f32 v249,v23,v249
v_accvgpr_read_b32 v250,a114
v_mfma_f32_16x16x32_bf16 a[64:67],a[132:135],v[68:71],a[64:67]
v_accvgpr_read_b32 v251,a115
v_cvt_pk_bf16_f32 v248,v248,v249
v_mfma_f32_16x16x32_bf16 a[64:67],a[136:139],v[72:75],a[64:67]
)A"
R"A(v_mul_f32 v250,v23,v250
v_mul_f32 v251,v23,v251
v_mfma_f32_16x16x32_bf16 a[64:67],a[140:143],v[76:79],a[64:67]
buffer_load_dwordx4 v10,s[72:75],s83 offen offset:3072 lds
v_mfma_f32_16x16x32_bf16 a[68:71],a[140:143],v[92:95],0
v_cvt_pk_bf16_f32 v249,v250,v251
v_accvgpr_read_b32 v250,a116
v_mfma_f32_16x16x32_bf16 a[68:71],a[136:139],v[88:91],a[68:71]
v_accvgpr_read_b32 v251,a117
v_accvgpr_read_b32 v12,a118
v_mfma_f32_16x16x32_bf16 a[68:71],a[132:135],v[84:87],a[68:71]
v_accvgpr_read_b32 v13,a119
v_mul_f32 v250,v23,v250
v_mfma_f32_16x16x32_bf16 a[68:71],a[128:131],v[80:83],a[68:71]
buffer_load_dwordx4 v[132:135],v6,s[28:31],s77 offen sc1
v_mfma_f32_16x16x32_bf16 a[72:75],a[128:131],v[96:99],0
v_mul_f32 v251,v23,v251
v_mul_f32 v12,v23,v12
v_mfma_f32_16x16x32_bf16 a[72:75],a[132:135],v[100:103],a[72:75]
s_addk_i32 m0,4096
v_mul_f32 v13,v23,v13
v_cvt_pk_bf16_f32 v250,v250,v251
v_mfma_f32_16x16x32_bf16 a[72:75],a[136:139],v[104:107],a[72:75]
v_cvt_pk_bf16_f32 v251,v12,v13
v_subrev_u32 v1,81920,v1
v_mfma_f32_16x16x32_bf16 a[72:75],a[140:143],v[108:111],a[72:75]
ds_read_b64_tr_b16 a[160:161],v1
ds_read_b64_tr_b16 a[162:163],v1 offset:5120
v_mfma_f32_16x16x32_bf16 a[76:79],a[140:143],v[124:127],0
buffer_load_dwordx4 v11,s[72:75],s83 offen lds
v_mfma_f32_16x16x32_bf16 a[76:79],a[136:139],v[120:123],a[76:79]
ds_read_b64_tr_b16 a[164:165],v1 offset:10240
ds_read_b64_tr_b16 a[166:167],v1 offset:15360
v_mfma_f32_16x16x32_bf16 a[76:79],a[132:135],v[116:119],a[76:79]
ds_read_b64_tr_b16 a[168:169],v1 offset:20480
v_accvgpr_read_b32 v252,a120
v_mfma_f32_16x16x32_bf16 a[76:79],a[128:131],v[112:115],a[76:79]
s_waitcnt lgkmcnt(5)
ds_read_b64_tr_b16 a[170:171],v1 offset:25600
v_accvgpr_read_b32 v253,a121
v_mfma_f32_16x16x32_bf16 a[80:83],a[144:147],v[64:67],0
ds_read_b64_tr_b16 a[172:173],v1 offset:30720
v_cmp_nlt_f32 s[56:57],v48,s63
v_mfma_f32_16x16x32_bf16 a[80:83],a[148:151],v[68:71],a[80:83]
buffer_load_dwordx4 v[164:167],v6,s[28:31],s77 offen offset:1024 sc1
v_mfma_f32_16x16x32_bf16 a[80:83],a[152:155],v[72:75],a[80:83]
ds_read_b64_tr_b16 a[174:175],v1 offset:35840
v_mul_f32 v252,v23,v252
v_mfma_f32_16x16x32_bf16 a[80:83],a[156:159],v[76:79],a[80:83]
v_mul_f32 v253,v23,v253
v_accvgpr_read_b32 v254,a122
v_mfma_f32_16x16x32_bf16 a[84:87],a[156:159],v[92:95],0
v_accvgpr_read_b32 v255,a123
v_cvt_pk_bf16_f32 v252,v252,v253
v_mfma_f32_16x16x32_bf16 a[84:87],a[152:155],v[88:91],a[84:87]
v_mul_f32 v254,v23,v254
v_mul_f32 v255,v23,v255
v_mfma_f32_16x16x32_bf16 a[84:87],a[148:151],v[84:87],a[84:87]
v_cmp_nlt_f32 s[58:59],v52,s63
s_nop 1
s_or_b64 s[56:57],s[56:57],s[58:59]
v_mfma_f32_16x16x32_bf16 a[84:87],a[144:147],v[80:83],a[84:87]
v_cvt_pk_bf16_f32 v253,v254,v255
v_accvgpr_read_b32 v254,a124
v_mfma_f32_16x16x32_bf16 a[88:91],a[144:147],v[96:99],0
v_accvgpr_read_b32 v255,a125
v_accvgpr_read_b32 v14,a126
v_mfma_f32_16x16x32_bf16 a[88:91],a[148:151],v[100:103],a[88:91]
v_cmp_nlt_f32 s[58:59],v56,s63
s_nop 1
s_or_b64 s[56:57],s[56:57],s[58:59]
v_mfma_f32_16x16x32_bf16 a[88:91],a[152:155],v[104:107],a[88:91]
v_accvgpr_read_b32 v15,a127
v_mul_f32 v254,v23,v254
v_mfma_f32_16x16x32_bf16 a[88:91],a[156:159],v[108:111],a[88:91]
v_mul_f32 v255,v23,v255
v_mul_f32 v14,v23,v14
v_mfma_f32_16x16x32_bf16 a[92:95],a[156:159],v[124:127],0
v_mul_f32 v15,v23,v15
v_cvt_pk_bf16_f32 v254,v254,v255
v_mfma_f32_16x16x32_bf16 a[92:95],a[152:155],v[120:123],a[92:95]
v_cmp_nlt_f32 s[58:59],v60,s63
s_nop 1
s_or_b64 s[56:57],s[56:57],s[58:59]
v_mfma_f32_16x16x32_bf16 a[92:95],a[148:151],v[116:119],a[92:95]
buffer_load_dwordx4 v[196:199],v6,s[28:31],s77 offen offset:2048 sc1
v_cvt_pk_bf16_f32 v255,v14,v15
v_mfma_f32_16x16x32_bf16 a[92:95],a[144:147],v[112:115],a[92:95]
s_cmp_lg_u64 s[56:57],0
s_cbranch_scc1 L_slow1_%=
L_sret1_%=:
v_subrev_u32 v2,81920,v2
s_waitcnt lgkmcnt(0)
v_mfma_f32_16x16x32_bf16 a[192:195],a[160:163],v[136:139],a[192:195]
ds_read_b64_tr_b16 a[176:177],v2 offset:32
ds_read_b64_tr_b16 a[178:179],v2 offset:5152
v_mfma_f32_16x16x32_bf16 a[192:195],a[164:167],v[140:143],a[192:195]
s_cmp_ge_u32 2,s71
s_cselect_b32 s80,6,4
ds_read_b64_tr_b16 a[180:181],v2 offset:10272
v_mfma_f32_16x16x32_bf16 a[192:195],a[168:171],v[152:155],a[192:195]
s_mul_i32 s80,s80,163840
buffer_store_dwordx4 v[200:203],v6,s[36:39],s86 offen sc1
v_mfma_f32_16x16x32_bf16 a[192:195],a[172:175],v[156:159],a[192:195]
s_add_u32 s80,s80,s81
ds_read_b64_tr_b16 a[182:183],v2 offset:15392
ds_read_b64_tr_b16 a[184:185],v2 offset:20512
v_mfma_f32_16x16x32_bf16 a[196:199],a[172:175],v[188:191],a[196:199]
s_add_u32 s83,s80,20480
ds_read_b64_tr_b16 a[186:187],v2 offset:25632
v_mfma_f32_16x16x32_bf16 a[196:199],a[168:171],v[184:187],a[196:199]
ds_read_b64_tr_b16 a[188:189],v2 offset:30752
v_mfma_f32_16x16x32_bf16 a[196:199],a[164:167],v[172:175],a[196:199]
buffer_store_dwordx4 v[204:207],v6,s[36:39],s86 offen offset:1024 sc1
v_mfma_f32_16x16x32_bf16 a[196:199],a[160:163],v[168:171],a[196:199]
ds_read_b64_tr_b16 a[190:191],v2 offset:35872
ds_read_b64_tr_b16 a[128:129],v1 offset:64
v_mfma_f32_16x16x32_bf16 a[200:203],a[160:163],v[200:203],a[200:203]
ds_read_b64_tr_b16 a[130:131],v1 offset:5184
v_mfma_f32_16x16x32_bf16 a[200:203],a[164:167],v[204:207],a[200:203]
ds_read_b64_tr_b16 a[132:133],v1 offset:10304
v_mfma_f32_16x16x32_bf16 a[200:203],a[168:171],v[216:219],a[200:203]
buffer_load_dwordx4 v[228:231],v6,s[28:31],s77 offen offset:3072 sc1
v_mfma_f32_16x16x32_bf16 a[200:203],a[172:175],v[220:223],a[200:203]
ds_read_b64_tr_b16 a[134:135],v1 offset:15424
ds_read_b64_tr_b16 a[136:137],v1 offset:20544
v_mfma_f32_16x16x32_bf16 a[204:207],a[172:175],v[252:255],a[204:207]
ds_read_b64_tr_b16 a[138:139],v1 offset:25664
v_mfma_f32_16x16x32_bf16 a[204:207],a[168:171],v[248:251],a[204:207]
ds_read_b64_tr_b16 a[140:141],v1 offset:30784
v_mfma_f32_16x16x32_bf16 a[204:207],a[164:167],v[236:239],a[204:207]
ds_read_b64_tr_b16 a[142:143],v1 offset:35904
v_mfma_f32_16x16x32_bf16 a[204:207],a[160:163],v[232:235],a[204:207]
s_barrier
buffer_load_dwordx4 v[144:147],v6,s[28:31],s78 offen sc1
s_waitcnt lgkmcnt(7)
v_mfma_f32_16x16x32_bf16 a[208:211],a[176:179],v[136:139],a[208:211]
ds_read_b64_tr_b16 a[144:145],v2 offset:96
ds_read_b64_tr_b16 a[146:147],v2 offset:5216
v_mfma_f32_16x16x32_bf16 a[208:211],a[180:183],v[140:143],a[208:211]
ds_read_b64_tr_b16 a[148:149],v2 offset:10336
v_mfma_f32_16x16x32_bf16 a[208:211],a[184:187],v[152:155],a[208:211]
s_add_u32 m0,s82,81920
ds_read_b64_tr_b16 a[150:151],v2 offset:15456
v_mfma_f32_16x16x32_bf16 a[208:211],a[188:191],v[156:159],a[208:211]
ds_read_b64_tr_b16 a[152:153],v2 offset:20576
v_mfma_f32_16x16x32_bf16 a[212:215],a[188:191],v[188:191],a[212:215]
ds_read_b64_tr_b16 a[154:155],v2 offset:25696
ds_read_b64_tr_b16 a[156:157],v2 offset:30816
v_mfma_f32_16x16x32_bf16 a[212:215],a[184:187],v[184:187],a[212:215]
buffer_store_dwordx4 v[216:219],v6,s[36:39],s86 offen offset:2048 sc1
v_mfma_f32_16x16x32_bf16 a[212:215],a[180:183],v[172:175],a[212:215]
ds_read_b64_tr_b16 a[158:159],v2 offset:35936
ds_read_b64_tr_b16 a[160:161],v1 offset:128
v_mfma_f32_16x16x32_bf16 a[212:215],a[176:179],v[168:171],a[212:215]
ds_read_b64_tr_b16 a[162:163],v1 offset:5248
v_mfma_f32_16x16x32_bf16 a[216:219],a[176:179],v[200:203],a[216:219]
ds_read_b64_tr_b16 a[164:165],v1 offset:10368
v_mfma_f32_16x16x32_bf16 a[216:219],a[180:183],v[204:207],a[216:219]
buffer_load_dwordx4 v7,s[72:75],s80 offen lds
v_mfma_f32_16x16x32_bf16 a[216:219],a[184:187],v[216:219],a[216:219]
ds_read_b64_tr_b16 a[166:167],v1 offset:15488
ds_read_b64_tr_b16 a[168:169],v1 offset:20608
v_mfma_f32_16x16x32_bf16 a[216:219],a[188:191],v[220:223],a[216:219]
ds_read_b64_tr_b16 a[170:171],v1 offset:25728
v_mfma_f32_16x16x32_bf16 a[220:223],a[188:191],v[252:255],a[220:223]
ds_read_b64_tr_b16 a[172:173],v1 offset:30848
v_mfma_f32_16x16x32_bf16 a[220:223],a[184:187],v[248:251],a[220:223]
buffer_load_dwordx4 v[176:179],v6,s[28:31],s78 offen offset:1024 sc1
v_mfma_f32_16x16x32_bf16 a[220:223],a[180:183],v[236:239],a[220:223]
ds_read_b64_tr_b16 a[174:175],v1 offset:35968
ds_read_b64_tr_b16 a[180:181],v2 offset:10400
v_mfma_f32_16x16x32_bf16 a[220:223],a[176:179],v[232:235],a[220:223]
s_waitcnt lgkmcnt(7)
ds_read_b64_tr_b16 a[176:177],v2 offset:160
v_mfma_f32_16x16x32_bf16 a[224:227],a[128:131],v[136:139],a[224:227]
ds_read_b64_tr_b16 a[178:179],v2 offset:5280
v_mfma_f32_16x16x32_bf16 a[224:227],a[132:135],v[140:143],a[224:227]
ds_read_b64_tr_b16 a[182:183],v2 offset:15520
v_mfma_f32_16x16x32_bf16 a[224:227],a[136:139],v[152:155],a[224:227]
buffer_load_dwordx4 v[208:211],v6,s[28:31],s78 offen offset:2048 sc1
v_mfma_f32_16x16x32_bf16 a[224:227],a[140:143],v[156:159],a[224:227]
ds_read_b64_tr_b16 a[184:185],v2 offset:20640
ds_read_b64_tr_b16 a[186:187],v2 offset:25760
v_mfma_f32_16x16x32_bf16 a[228:231],a[140:143],v[188:191],a[228:231]
ds_read_b64_tr_b16 a[188:189],v2 offset:30880
v_mfma_f32_16x16x32_bf16 a[228:231],a[136:139],v[184:187],a[228:231]
ds_read_b64_tr_b16 a[190:191],v2 offset:36000
v_mfma_f32_16x16x32_bf16 a[228:231],a[132:135],v[172:175],a[228:231]
buffer_load_dwordx4 v8,s[72:75],s80 offen offset:1024 lds
v_mfma_f32_16x16x32_bf16 a[228:231],a[128:131],v[168:171],a[228:231]
v_mfma_f32_16x16x32_bf16 a[232:235],a[128:131],v[200:203],a[232:235]
v_mfma_f32_16x16x32_bf16 a[232:235],a[132:135],v[204:207],a[232:235]
v_mfma_f32_16x16x32_bf16 a[232:235],a[136:139],v[216:219],a[232:235]
buffer_store_dwordx4 v[220:223],v6,s[36:39],s86 offen offset:3072 sc1
v_mfma_f32_16x16x32_bf16 a[232:235],a[140:143],v[220:223],a[232:235]
v_mfma_f32_16x16x32_bf16 a[236:239],a[140:143],v[252:255],a[236:239]
ds_read_b64_tr_b16 a[140:141],v1 offset:30912
ds_read_b64_tr_b16 a[142:143],v1 offset:36032
v_mfma_f32_16x16x32_bf16 a[236:239],a[136:139],v[248:251],a[236:239]
ds_read_b64_tr_b16 a[136:137],v1 offset:20672
v_mfma_f32_16x16x32_bf16 a[236:239],a[132:135],v[236:239],a[236:239]
buffer_load_dwordx4 v[240:243],v6,s[28:31],s78 offen offset:3072 sc1
v_mfma_f32_16x16x32_bf16 a[236:239],a[128:131],v[232:235],a[236:239]
ds_read_b64_tr_b16 a[128:129],v1 offset:192
ds_read_b64_tr_b16 a[130:131],v1 offset:5312
v_mfma_f32_16x16x32_bf16 a[240:243],a[144:147],v[136:139],a[240:243]
ds_read_b64_tr_b16 a[132:133],v1 offset:10432
v_mfma_f32_16x16x32_bf16 a[240:243],a[148:151],v[140:143],a[240:243]
ds_read_b64_tr_b16 a[134:135],v1 offset:15552
v_mfma_f32_16x16x32_bf16 a[240:243],a[152:155],v[152:155],a[240:243]
ds_read_b64_tr_b16 a[138:139],v1 offset:25792
v_mfma_f32_16x16x32_bf16 a[240:243],a[156:159],v[156:159],a[240:243]
buffer_load_dwordx4 v9,s[72:75],s80 offen offset:2048 lds
v_mfma_f32_16x16x32_bf16 a[244:247],a[156:159],v[188:191],a[244:247]
v_mfma_f32_16x16x32_bf16 a[244:247],a[152:155],v[184:187],a[244:247]
v_mfma_f32_16x16x32_bf16 a[244:247],a[148:151],v[172:175],a[244:247]
v_mfma_f32_16x16x32_bf16 a[244:247],a[144:147],v[168:171],a[244:247]
buffer_load_dwordx4 v[148:151],v6,s[28:31],s79 offen sc1
v_mfma_f32_16x16x32_bf16 a[248:251],a[144:147],v[200:203],a[248:251]
v_mfma_f32_16x16x32_bf16 a[248:251],a[148:151],v[204:207],a[248:251]
v_mfma_f32_16x16x32_bf16 a[248:251],a[152:155],v[216:219],a[248:251]
v_mfma_f32_16x16x32_bf16 a[248:251],a[156:159],v[220:223],a[248:251]
v_mfma_f32_16x16x32_bf16 a[252:255],a[156:159],v[252:255],a[252:255]
ds_read_b64_tr_b16 a[156:157],v2 offset:30944
ds_read_b64_tr_b16 a[158:159],v2 offset:36064
v_mfma_f32_16x16x32_bf16 a[252:255],a[152:155],v[248:251],a[252:255]
buffer_store_dwordx4 v[232:235],v6,s[36:39],s87 offen sc1
v_mfma_f32_16x16x32_bf16 a[252:255],a[148:151],v[236:239],a[252:255]
ds_read_b64_tr_b16 a[148:149],v2 offset:10464
ds_read_b64_tr_b16 a[150:151],v2 offset:15584
v_mfma_f32_16x16x32_bf16 a[252:255],a[144:147],v[232:235],a[252:255]
ds_read_b64_tr_b16 a[144:145],v2 offset:224
v_mfma_f32_16x16x32_bf16 a[0:3],a[160:163],v[136:139],a[0:3]
s_waitcnt lgkmcnt(5)
ds_read_b64_tr_b16 a[146:147],v2 offset:5344
v_mfma_f32_16x16x32_bf16 a[0:3],a[164:167],v[140:143],a[0:3]
buffer_load_dwordx4 v[180:183],v6,s[28:31],s79 offen offset:1024 sc1
v_mfma_f32_16x16x32_bf16 a[0:3],a[168:171],v[152:155],a[0:3]
)A"
R"A(ds_read_b64_tr_b16 a[152:153],v2 offset:20704
ds_read_b64_tr_b16 a[154:155],v2 offset:25824
v_mfma_f32_16x16x32_bf16 a[0:3],a[172:175],v[156:159],a[0:3]
v_mfma_f32_16x16x32_bf16 a[4:7],a[172:175],v[188:191],a[4:7]
v_mfma_f32_16x16x32_bf16 a[4:7],a[168:171],v[184:187],a[4:7]
buffer_load_dwordx4 v10,s[72:75],s80 offen offset:3072 lds
v_mfma_f32_16x16x32_bf16 a[4:7],a[164:167],v[172:175],a[4:7]
v_mfma_f32_16x16x32_bf16 a[4:7],a[160:163],v[168:171],a[4:7]
v_mfma_f32_16x16x32_bf16 a[8:11],a[160:163],v[200:203],a[8:11]
v_mfma_f32_16x16x32_bf16 a[8:11],a[164:167],v[204:207],a[8:11]
v_mfma_f32_16x16x32_bf16 a[8:11],a[168:171],v[216:219],a[8:11]
buffer_load_dwordx4 v[212:215],v6,s[28:31],s79 offen offset:2048 sc1
v_mfma_f32_16x16x32_bf16 a[8:11],a[172:175],v[220:223],a[8:11]
v_mfma_f32_16x16x32_bf16 a[12:15],a[172:175],v[252:255],a[12:15]
ds_read_b64_tr_b16 a[172:173],v1 offset:30976
ds_read_b64_tr_b16 a[174:175],v1 offset:36096
v_mfma_f32_16x16x32_bf16 a[12:15],a[168:171],v[248:251],a[12:15]
ds_read_b64_tr_b16 a[168:169],v1 offset:20736
v_mfma_f32_16x16x32_bf16 a[12:15],a[164:167],v[236:239],a[12:15]
s_addk_i32 m0,4096
ds_read_b64_tr_b16 a[164:165],v1 offset:10496
v_mfma_f32_16x16x32_bf16 a[12:15],a[160:163],v[232:235],a[12:15]
ds_read_b64_tr_b16 a[160:161],v1 offset:256
v_mfma_f32_16x16x32_bf16 a[16:19],a[176:179],v[136:139],a[16:19]
ds_read_b64_tr_b16 a[162:163],v1 offset:5376
ds_read_b64_tr_b16 a[166:167],v1 offset:15616
v_mfma_f32_16x16x32_bf16 a[16:19],a[180:183],v[140:143],a[16:19]
buffer_store_dwordx4 v[236:239],v6,s[36:39],s87 offen offset:1024 sc1
v_mfma_f32_16x16x32_bf16 a[16:19],a[184:187],v[152:155],a[16:19]
ds_read_b64_tr_b16 a[170:171],v1 offset:25856
v_add_u32 v1,40960,v1
v_mfma_f32_16x16x32_bf16 a[16:19],a[188:191],v[156:159],a[16:19]
ds_read_b64_tr_b16 a[96:97],v1 offset:128
ds_read_b64_tr_b16 a[98:99],v1 offset:5248
v_mfma_f32_16x16x32_bf16 a[20:23],a[188:191],v[188:191],a[20:23]
ds_read_b64_tr_b16 a[100:101],v1 offset:10368
v_mfma_f32_16x16x32_bf16 a[20:23],a[184:187],v[184:187],a[20:23]
buffer_load_dwordx4 v11,s[72:75],s80 offen lds
v_mfma_f32_16x16x32_bf16 a[20:23],a[180:183],v[172:175],a[20:23]
ds_read_b64_tr_b16 a[102:103],v1 offset:15488
ds_read_b64_tr_b16 a[104:105],v1 offset:20608
v_mfma_f32_16x16x32_bf16 a[20:23],a[176:179],v[168:171],a[20:23]
ds_read_b64_tr_b16 a[106:107],v1 offset:25728
v_mfma_f32_16x16x32_bf16 a[24:27],a[176:179],v[200:203],a[24:27]
ds_read_b64_tr_b16 a[108:109],v1 offset:30848
v_mfma_f32_16x16x32_bf16 a[24:27],a[180:183],v[204:207],a[24:27]
buffer_load_dwordx4 v[244:247],v6,s[28:31],s79 offen offset:3072 sc1
v_mfma_f32_16x16x32_bf16 a[24:27],a[184:187],v[216:219],a[24:27]
ds_read_b64_tr_b16 a[110:111],v1 offset:35968
v_mfma_f32_16x16x32_bf16 a[24:27],a[188:191],v[220:223],a[24:27]
v_mfma_f32_16x16x32_bf16 a[28:31],a[188:191],v[252:255],a[28:31]
ds_read_b64_tr_b16 a[188:189],v2 offset:31008
ds_read_b64_tr_b16 a[190:191],v2 offset:36128
v_mfma_f32_16x16x32_bf16 a[28:31],a[184:187],v[248:251],a[28:31]
s_addk_i32 m0,1024
ds_read_b64_tr_b16 a[184:185],v2 offset:20768
v_mfma_f32_16x16x32_bf16 a[28:31],a[180:183],v[236:239],a[28:31]
ds_read_b64_tr_b16 a[180:181],v2 offset:10528
v_mfma_f32_16x16x32_bf16 a[28:31],a[176:179],v[232:235],a[28:31]
ds_read_b64_tr_b16 a[176:177],v2 offset:288
v_mfma_f32_16x16x32_bf16 a[32:35],a[128:131],v[136:139],a[32:35]
buffer_load_dwordx4 v7,s[72:75],s83 offen lds
v_mfma_f32_16x16x32_bf16 a[32:35],a[132:135],v[140:143],a[32:35]
ds_read_b64_tr_b16 a[178:179],v2 offset:5408
ds_read_b64_tr_b16 a[182:183],v2 offset:15648
v_mfma_f32_16x16x32_bf16 a[32:35],a[136:139],v[152:155],a[32:35]
ds_read_b64_tr_b16 a[186:187],v2 offset:25888
v_add_u32 v2,40960,v2
v_mfma_f32_16x16x32_bf16 a[32:35],a[140:143],v[156:159],a[32:35]
ds_read_b64_tr_b16 a[112:113],v2 offset:160
v_mfma_f32_16x16x32_bf16 a[36:39],a[140:143],v[188:191],a[36:39]
ds_read_b64_tr_b16 a[114:115],v2 offset:5280
v_mfma_f32_16x16x32_bf16 a[36:39],a[136:139],v[184:187],a[36:39]
buffer_store_dwordx4 v[248:251],v6,s[36:39],s87 offen offset:2048 sc1
v_mfma_f32_16x16x32_bf16 a[36:39],a[132:135],v[172:175],a[36:39]
ds_read_b64_tr_b16 a[116:117],v2 offset:10400
ds_read_b64_tr_b16 a[118:119],v2 offset:15520
v_mfma_f32_16x16x32_bf16 a[36:39],a[128:131],v[168:171],a[36:39]
ds_read_b64_tr_b16 a[120:121],v2 offset:20640
v_mfma_f32_16x16x32_bf16 a[40:43],a[128:131],v[200:203],a[40:43]
ds_read_b64_tr_b16 a[122:123],v2 offset:25760
v_mfma_f32_16x16x32_bf16 a[40:43],a[132:135],v[204:207],a[40:43]
ds_read_b64_tr_b16 a[124:125],v2 offset:30880
v_mfma_f32_16x16x32_bf16 a[40:43],a[136:139],v[216:219],a[40:43]
ds_read_b64_tr_b16 a[126:127],v2 offset:36000
v_mfma_f32_16x16x32_bf16 a[40:43],a[140:143],v[220:223],a[40:43]
v_mfma_f32_16x16x32_bf16 a[44:47],a[140:143],v[252:255],a[44:47]
ds_read_b64_tr_b16 a[140:141],v1 offset:30720
ds_read_b64_tr_b16 a[142:143],v1 offset:35840
v_mfma_f32_16x16x32_bf16 a[44:47],a[136:139],v[248:251],a[44:47]
ds_read_b64_tr_b16 a[136:137],v1 offset:20480
v_mfma_f32_16x16x32_bf16 a[44:47],a[132:135],v[236:239],a[44:47]
ds_read_b64_tr_b16 a[132:133],v1 offset:10240
v_mfma_f32_16x16x32_bf16 a[44:47],a[128:131],v[232:235],a[44:47]
s_waitcnt lgkmcnt(7)
buffer_load_dwordx4 v8,s[72:75],s83 offen offset:1024 lds
v_mfma_f32_16x16x32_bf16 a[48:51],a[144:147],v[136:139],a[48:51]
ds_read_b64_tr_b16 a[128:129],v1
ds_read_b64_tr_b16 a[130:131],v1 offset:5120
v_mfma_f32_16x16x32_bf16 a[48:51],a[148:151],v[140:143],a[48:51]
ds_read_b64_tr_b16 a[134:135],v1 offset:15360
v_mfma_f32_16x16x32_bf16 a[48:51],a[152:155],v[152:155],a[48:51]
ds_read_b64_tr_b16 a[138:139],v1 offset:25600
v_mfma_f32_16x16x32_bf16 a[48:51],a[156:159],v[156:159],a[48:51]
s_cmp_ge_u32 1,s71
s_cselect_b32 s76,4,2
v_mfma_f32_16x16x32_bf16 a[52:55],a[156:159],v[188:191],a[52:55]
s_bitcmp1_b32 s54,s76
s_cselect_b32 s77,8192,0
s_lshl_b32 s76,s76,17
v_mfma_f32_16x16x32_bf16 a[52:55],a[152:155],v[184:187],a[52:55]
s_add_u32 s76,s76,s77
s_add_u32 s77,s76,4096
s_add_u32 s78,s76,16384
v_mfma_f32_16x16x32_bf16 a[52:55],a[148:151],v[172:175],a[52:55]
s_add_u32 s79,s76,20480
s_max_i32 s76,s76,s18
s_max_i32 s77,s77,s19
v_mfma_f32_16x16x32_bf16 a[52:55],a[144:147],v[168:171],a[52:55]
s_max_i32 s78,s78,s100
s_max_i32 s79,s79,s101
v_mfma_f32_16x16x32_bf16 a[56:59],a[144:147],v[200:203],a[56:59]
v_mfma_f32_16x16x32_bf16 a[56:59],a[148:151],v[204:207],a[56:59]
buffer_store_dwordx4 v[252:255],v6,s[36:39],s87 offen offset:3072 sc1
v_mfma_f32_16x16x32_bf16 a[56:59],a[152:155],v[216:219],a[56:59]
v_mfma_f32_16x16x32_bf16 a[56:59],a[156:159],v[220:223],a[56:59]
v_mfma_f32_16x16x32_bf16 a[60:63],a[156:159],v[252:255],a[60:63]
ds_read_b64_tr_b16 a[156:157],v2 offset:30752
ds_read_b64_tr_b16 a[158:159],v2 offset:35872
v_mfma_f32_16x16x32_bf16 a[60:63],a[152:155],v[248:251],a[60:63]
ds_read_b64_tr_b16 a[152:153],v2 offset:20512
v_mfma_f32_16x16x32_bf16 a[60:63],a[148:151],v[236:239],a[60:63]
buffer_load_dwordx4 v9,s[72:75],s83 offen offset:2048 lds
v_mfma_f32_16x16x32_bf16 a[60:63],a[144:147],v[232:235],a[60:63]
ds_read_b64_tr_b16 a[144:145],v2 offset:32
ds_read_b64_tr_b16 a[146:147],v2 offset:5152
v_mfma_f32_16x16x32_bf16 a[64:67],a[160:163],v[136:139],a[64:67]
ds_read_b64_tr_b16 a[148:149],v2 offset:10272
v_mfma_f32_16x16x32_bf16 a[64:67],a[164:167],v[140:143],a[64:67]
ds_read_b64_tr_b16 a[150:151],v2 offset:15392
v_mfma_f32_16x16x32_bf16 a[64:67],a[168:171],v[152:155],a[64:67]
ds_read_b64_tr_b16 a[154:155],v2 offset:25632
v_mfma_f32_16x16x32_bf16 a[64:67],a[172:175],v[156:159],a[64:67]
v_mfma_f32_16x16x32_bf16 a[68:71],a[172:175],v[188:191],a[68:71]
v_mfma_f32_16x16x32_bf16 a[68:71],a[168:171],v[184:187],a[68:71]
v_mfma_f32_16x16x32_bf16 a[68:71],a[164:167],v[172:175],a[68:71]
v_mfma_f32_16x16x32_bf16 a[68:71],a[160:163],v[168:171],a[68:71]
v_mfma_f32_16x16x32_bf16 a[72:75],a[160:163],v[200:203],a[72:75]
v_mfma_f32_16x16x32_bf16 a[72:75],a[164:167],v[204:207],a[72:75]
v_mfma_f32_16x16x32_bf16 a[72:75],a[168:171],v[216:219],a[72:75]
v_mfma_f32_16x16x32_bf16 a[72:75],a[172:175],v[220:223],a[72:75]
buffer_load_dwordx4 v[64:67],v6,s[28:31],s76 offen sc1
v_mfma_f32_16x16x32_bf16 a[76:79],a[172:175],v[252:255],a[76:79]
ds_read_b64_tr_b16 a[172:173],v1 offset:30784
ds_read_b64_tr_b16 a[174:175],v1 offset:35904
v_mfma_f32_16x16x32_bf16 a[76:79],a[168:171],v[248:251],a[76:79]
ds_read_b64_tr_b16 a[168:169],v1 offset:20544
v_mfma_f32_16x16x32_bf16 a[76:79],a[164:167],v[236:239],a[76:79]
ds_read_b64_tr_b16 a[164:165],v1 offset:10304
v_mfma_f32_16x16x32_bf16 a[76:79],a[160:163],v[232:235],a[76:79]
buffer_load_dwordx4 v10,s[72:75],s83 offen offset:3072 lds
v_mfma_f32_16x16x32_bf16 a[80:83],a[176:179],v[136:139],a[80:83]
ds_read_b64_tr_b16 a[160:161],v1 offset:64
ds_read_b64_tr_b16 a[162:163],v1 offset:5184
v_mfma_f32_16x16x32_bf16 a[80:83],a[180:183],v[140:143],a[80:83]
ds_read_b64_tr_b16 a[166:167],v1 offset:15424
v_mfma_f32_16x16x32_bf16 a[80:83],a[184:187],v[152:155],a[80:83]
ds_read_b64_tr_b16 a[170:171],v1 offset:25664
v_mfma_f32_16x16x32_bf16 a[80:83],a[188:191],v[156:159],a[80:83]
buffer_load_dwordx4 v[68:71],v6,s[28:31],s76 offen offset:1024 sc1
v_mfma_f32_16x16x32_bf16 a[84:87],a[188:191],v[188:191],a[84:87]
v_mfma_f32_16x16x32_bf16 a[84:87],a[184:187],v[184:187],a[84:87]
v_mfma_f32_16x16x32_bf16 a[84:87],a[180:183],v[172:175],a[84:87]
v_mfma_f32_16x16x32_bf16 a[84:87],a[176:179],v[168:171],a[84:87]
v_mfma_f32_16x16x32_bf16 a[88:91],a[176:179],v[200:203],a[88:91]
v_mfma_f32_16x16x32_bf16 a[88:91],a[180:183],v[204:207],a[88:91]
s_addk_i32 m0,4096
v_mfma_f32_16x16x32_bf16 a[88:91],a[184:187],v[216:219],a[88:91]
v_mfma_f32_16x16x32_bf16 a[88:91],a[188:191],v[220:223],a[88:91]
buffer_load_dwordx4 v[72:75],v6,s[28:31],s76 offen offset:2048 sc1
v_mfma_f32_16x16x32_bf16 a[92:95],a[188:191],v[252:255],a[92:95]
ds_read_b64_tr_b16 a[188:189],v2 offset:30816
ds_read_b64_tr_b16 a[190:191],v2 offset:35936
v_mfma_f32_16x16x32_bf16 a[92:95],a[184:187],v[248:251],a[92:95]
ds_read_b64_tr_b16 a[184:185],v2 offset:20576
v_mfma_f32_16x16x32_bf16 a[92:95],a[180:183],v[236:239],a[92:95]
s_cmp_ge_u32 0,s71
s_cselect_b32 s80,3,1
s_mul_i32 s80,s80,163840
v_mfma_f32_16x16x32_bf16 a[92:95],a[176:179],v[232:235],a[92:95]
s_add_u32 s80,s80,s81
buffer_load_dwordx4 v11,s[72:75],s83 offen lds
s_waitcnt vmcnt(10) lgkmcnt(3)
v_mfma_f32_16x16x32_bf16 a[192:195],a[128:131],v[128:131],a[192:195]
s_add_u32 s83,s80,20480
ds_read_b64_tr_b16 a[176:177],v2 offset:96
ds_read_b64_tr_b16 a[178:179],v2 offset:5216
v_mfma_f32_16x16x32_bf16 a[192:195],a[132:135],v[160:163],a[192:195]
ds_read_b64_tr_b16 a[180:181],v2 offset:10336
v_mfma_f32_16x16x32_bf16 a[192:195],a[136:139],v[192:195],a[192:195]
ds_read_b64_tr_b16 a[182:183],v2 offset:15456
v_mfma_f32_16x16x32_bf16 a[192:195],a[140:143],v[224:227],a[192:195]
buffer_load_dwordx4 v[76:79],v6,s[28:31],s76 offen offset:3072 sc1
v_mfma_f32_16x16x32_bf16 a[196:199],a[140:143],v[228:231],a[196:199]
ds_read_b64_tr_b16 a[186:187],v2 offset:25696
v_mfma_f32_16x16x32_bf16 a[196:199],a[136:139],v[196:199],a[196:199]
v_mfma_f32_16x16x32_bf16 a[196:199],a[132:135],v[164:167],a[196:199]
v_mfma_f32_16x16x32_bf16 a[196:199],a[128:131],v[132:135],a[196:199]
v_mfma_f32_16x16x32_bf16 a[200:203],a[128:131],v[144:147],a[200:203]
v_mfma_f32_16x16x32_bf16 a[200:203],a[132:135],v[176:179],a[200:203]
v_mfma_f32_16x16x32_bf16 a[200:203],a[136:139],v[208:211],a[200:203]
v_mfma_f32_16x16x32_bf16 a[200:203],a[140:143],v[240:243],a[200:203]
s_lshl_b32 s95,s42,2
buffer_load_dwordx4 v[80:83],v6,s[28:31],s77 offen sc1
v_mfma_f32_16x16x32_bf16 a[204:207],a[140:143],v[244:247],a[204:207]
s_add_u32 s95,s95,s40
s_lshl_b32 s95,s95,4
v_bfe_u32 v19,s55,13,1
v_mfma_f32_16x16x32_bf16 a[204:207],a[136:139],v[212:215],a[204:207]
v_mov_b32 v18,s68
v_or_b32 v19,s69,v19
v_mfma_f32_16x16x32_bf16 a[204:207],a[132:135],v[180:183],a[204:207]
ds_read_b64_tr_b16 a[132:133],v1 offset:10432
ds_read_b64_tr_b16 a[134:135],v1 offset:15552
v_mfma_f32_16x16x32_bf16 a[204:207],a[128:131],v[148:151],a[204:207]
s_barrier
ds_read_b64_tr_b16 a[128:129],v1 offset:192
ds_read_b64_tr_b16 a[130:131],v1 offset:5312
)A"
R"A(v_mfma_f32_16x16x32_bf16 a[208:211],a[144:147],v[128:131],a[208:211]
ds_read_b64_tr_b16 a[136:137],v1 offset:20672
v_mfma_f32_16x16x32_bf16 a[208:211],a[148:151],v[160:163],a[208:211]
ds_read_b64_tr_b16 a[138:139],v1 offset:25792
v_mfma_f32_16x16x32_bf16 a[208:211],a[152:155],v[192:195],a[208:211]
s_add_u32 m0,s82,0
s_waitcnt vmcnt(8)
s_mov_b64 exec,1
buffer_store_dwordx2 v[18:19],v6,s[64:67],s95 offen offset:1024 sc1
s_mov_b64 exec,-1
v_mfma_f32_16x16x32_bf16 a[208:211],a[156:159],v[224:227],a[208:211]
ds_read_b64_tr_b16 a[140:141],v1 offset:30912
ds_read_b64_tr_b16 a[142:143],v1 offset:36032
v_mfma_f32_16x16x32_bf16 a[212:215],a[156:159],v[228:231],a[212:215]
v_mfma_f32_16x16x32_bf16 a[212:215],a[152:155],v[196:199],a[212:215]
v_mfma_f32_16x16x32_bf16 a[212:215],a[148:151],v[164:167],a[212:215]
buffer_load_dwordx4 v[84:87],v6,s[28:31],s77 offen offset:1024 sc1
v_mfma_f32_16x16x32_bf16 a[212:215],a[144:147],v[132:135],a[212:215]
v_mfma_f32_16x16x32_bf16 a[216:219],a[144:147],v[144:147],a[216:219]
v_mfma_f32_16x16x32_bf16 a[216:219],a[148:151],v[176:179],a[216:219]
v_mfma_f32_16x16x32_bf16 a[216:219],a[152:155],v[208:211],a[216:219]
buffer_load_dwordx4 v7,s[72:75],s80 offen lds
v_mfma_f32_16x16x32_bf16 a[216:219],a[156:159],v[240:243],a[216:219]
v_mfma_f32_16x16x32_bf16 a[220:223],a[156:159],v[244:247],a[220:223]
ds_read_b64_tr_b16 a[156:157],v2 offset:30944
ds_read_b64_tr_b16 a[158:159],v2 offset:36064
v_mfma_f32_16x16x32_bf16 a[220:223],a[152:155],v[212:215],a[220:223]
ds_read_b64_tr_b16 a[152:153],v2 offset:20704
v_mfma_f32_16x16x32_bf16 a[220:223],a[148:151],v[180:183],a[220:223]
buffer_load_dwordx4 v[88:91],v6,s[28:31],s77 offen offset:2048 sc1
v_mfma_f32_16x16x32_bf16 a[220:223],a[144:147],v[148:151],a[220:223]
ds_read_b64_tr_b16 a[144:145],v2 offset:224
ds_read_b64_tr_b16 a[146:147],v2 offset:5344
v_mfma_f32_16x16x32_bf16 a[224:227],a[160:163],v[128:131],a[224:227]
ds_read_b64_tr_b16 a[148:149],v2 offset:10464
v_mfma_f32_16x16x32_bf16 a[224:227],a[164:167],v[160:163],a[224:227]
ds_read_b64_tr_b16 a[150:151],v2 offset:15584
v_mfma_f32_16x16x32_bf16 a[224:227],a[168:171],v[192:195],a[224:227]
buffer_load_dwordx4 v[92:95],v6,s[28:31],s77 offen offset:3072 sc1
v_mfma_f32_16x16x32_bf16 a[224:227],a[172:175],v[224:227],a[224:227]
ds_read_b64_tr_b16 a[154:155],v2 offset:25824
v_mfma_f32_16x16x32_bf16 a[228:231],a[172:175],v[228:231],a[228:231]
v_mfma_f32_16x16x32_bf16 a[228:231],a[168:171],v[196:199],a[228:231]
v_mfma_f32_16x16x32_bf16 a[228:231],a[164:167],v[164:167],a[228:231]
buffer_load_dwordx4 v8,s[72:75],s80 offen offset:1024 lds
v_mfma_f32_16x16x32_bf16 a[228:231],a[160:163],v[132:135],a[228:231]
v_mfma_f32_16x16x32_bf16 a[232:235],a[160:163],v[144:147],a[232:235]
v_mfma_f32_16x16x32_bf16 a[232:235],a[164:167],v[176:179],a[232:235]
v_mfma_f32_16x16x32_bf16 a[232:235],a[168:171],v[208:211],a[232:235]
buffer_load_dwordx4 v[96:99],v6,s[28:31],s78 offen sc1
v_mfma_f32_16x16x32_bf16 a[232:235],a[172:175],v[240:243],a[232:235]
v_mfma_f32_16x16x32_bf16 a[236:239],a[172:175],v[244:247],a[236:239]
ds_read_b64_tr_b16 a[172:173],v1 offset:30976
ds_read_b64_tr_b16 a[174:175],v1 offset:36096
v_mfma_f32_16x16x32_bf16 a[236:239],a[168:171],v[212:215],a[236:239]
ds_read_b64_tr_b16 a[168:169],v1 offset:20736
v_mfma_f32_16x16x32_bf16 a[236:239],a[164:167],v[180:183],a[236:239]
ds_read_b64_tr_b16 a[164:165],v1 offset:10496
v_mfma_f32_16x16x32_bf16 a[236:239],a[160:163],v[148:151],a[236:239]
s_waitcnt lgkmcnt(4)
ds_read_b64_tr_b16 a[160:161],v1 offset:256
v_mfma_f32_16x16x32_bf16 a[240:243],a[176:179],v[128:131],a[240:243]
ds_read_b64_tr_b16 a[162:163],v1 offset:5376
ds_read_b64_tr_b16 a[166:167],v1 offset:15616
v_mfma_f32_16x16x32_bf16 a[240:243],a[180:183],v[160:163],a[240:243]
ds_read_b64_tr_b16 a[170:171],v1 offset:25856
v_add_u32 v1,81920,v1
v_mfma_f32_16x16x32_bf16 a[240:243],a[184:187],v[192:195],a[240:243]
buffer_load_dwordx4 v[100:103],v6,s[28:31],s78 offen offset:1024 sc1
v_mfma_f32_16x16x32_bf16 a[240:243],a[188:191],v[224:227],a[240:243]
v_mfma_f32_16x16x32_bf16 a[244:247],a[188:191],v[228:231],a[244:247]
v_mfma_f32_16x16x32_bf16 a[244:247],a[184:187],v[196:199],a[244:247]
v_mfma_f32_16x16x32_bf16 a[244:247],a[180:183],v[164:167],a[244:247]
buffer_load_dwordx4 v9,s[72:75],s80 offen offset:2048 lds
v_mfma_f32_16x16x32_bf16 a[244:247],a[176:179],v[132:135],a[244:247]
v_mfma_f32_16x16x32_bf16 a[248:251],a[176:179],v[144:147],a[248:251]
v_mfma_f32_16x16x32_bf16 a[248:251],a[180:183],v[176:179],a[248:251]
v_mfma_f32_16x16x32_bf16 a[248:251],a[184:187],v[208:211],a[248:251]
buffer_load_dwordx4 v[104:107],v6,s[28:31],s78 offen offset:2048 sc1
v_mfma_f32_16x16x32_bf16 a[248:251],a[188:191],v[240:243],a[248:251]
v_mfma_f32_16x16x32_bf16 a[252:255],a[188:191],v[244:247],a[252:255]
ds_read_b64_tr_b16 a[188:189],v2 offset:31008
ds_read_b64_tr_b16 a[190:191],v2 offset:36128
v_mfma_f32_16x16x32_bf16 a[252:255],a[184:187],v[212:215],a[252:255]
ds_read_b64_tr_b16 a[184:185],v2 offset:20768
v_mfma_f32_16x16x32_bf16 a[252:255],a[180:183],v[180:183],a[252:255]
ds_read_b64_tr_b16 a[180:181],v2 offset:10528
v_mfma_f32_16x16x32_bf16 a[252:255],a[176:179],v[148:151],a[252:255]
ds_read_b64_tr_b16 a[176:177],v2 offset:288
v_mfma_f32_16x16x32_bf16 a[0:3],a[96:99],v[128:131],a[0:3]
ds_read_b64_tr_b16 a[178:179],v2 offset:5408
ds_read_b64_tr_b16 a[182:183],v2 offset:15648
v_mfma_f32_16x16x32_bf16 a[0:3],a[100:103],v[160:163],a[0:3]
ds_read_b64_tr_b16 a[186:187],v2 offset:25888
v_add_u32 v2,81920,v2
v_mfma_f32_16x16x32_bf16 a[0:3],a[104:107],v[192:195],a[0:3]
buffer_load_dwordx4 v[108:111],v6,s[28:31],s78 offen offset:3072 sc1
v_mfma_f32_16x16x32_bf16 a[0:3],a[108:111],v[224:227],a[0:3]
v_mfma_f32_16x16x32_bf16 a[4:7],a[108:111],v[228:231],a[4:7]
v_mfma_f32_16x16x32_bf16 a[4:7],a[104:107],v[196:199],a[4:7]
v_mfma_f32_16x16x32_bf16 a[4:7],a[100:103],v[164:167],a[4:7]
buffer_load_dwordx4 v10,s[72:75],s80 offen offset:3072 lds
v_mfma_f32_16x16x32_bf16 a[4:7],a[96:99],v[132:135],a[4:7]
v_mfma_f32_16x16x32_bf16 a[8:11],a[96:99],v[144:147],a[8:11]
v_mfma_f32_16x16x32_bf16 a[8:11],a[100:103],v[176:179],a[8:11]
v_mfma_f32_16x16x32_bf16 a[8:11],a[104:107],v[208:211],a[8:11]
buffer_load_dwordx4 v[112:115],v6,s[28:31],s79 offen sc1
v_mfma_f32_16x16x32_bf16 a[8:11],a[108:111],v[240:243],a[8:11]
v_mfma_f32_16x16x32_bf16 a[12:15],a[108:111],v[244:247],a[12:15]
ds_read_b64_tr_b16 a[108:109],v1 offset:30720
ds_read_b64_tr_b16 a[110:111],v1 offset:35840
v_mfma_f32_16x16x32_bf16 a[12:15],a[104:107],v[212:215],a[12:15]
ds_read_b64_tr_b16 a[104:105],v1 offset:20480
v_mfma_f32_16x16x32_bf16 a[12:15],a[100:103],v[180:183],a[12:15]
s_addk_i32 m0,4096
ds_read_b64_tr_b16 a[100:101],v1 offset:10240
v_mfma_f32_16x16x32_bf16 a[12:15],a[96:99],v[148:151],a[12:15]
ds_read_b64_tr_b16 a[96:97],v1
v_mfma_f32_16x16x32_bf16 a[16:19],a[112:115],v[128:131],a[16:19]
ds_read_b64_tr_b16 a[98:99],v1 offset:5120
ds_read_b64_tr_b16 a[102:103],v1 offset:15360
v_mfma_f32_16x16x32_bf16 a[16:19],a[116:119],v[160:163],a[16:19]
buffer_load_dwordx4 v11,s[72:75],s80 offen lds
v_mfma_f32_16x16x32_bf16 a[16:19],a[120:123],v[192:195],a[16:19]
ds_read_b64_tr_b16 a[106:107],v1 offset:25600
v_mfma_f32_16x16x32_bf16 a[16:19],a[124:127],v[224:227],a[16:19]
v_mfma_f32_16x16x32_bf16 a[20:23],a[124:127],v[228:231],a[20:23]
v_mfma_f32_16x16x32_bf16 a[20:23],a[120:123],v[196:199],a[20:23]
buffer_load_dwordx4 v[116:119],v6,s[28:31],s79 offen offset:1024 sc1
v_mfma_f32_16x16x32_bf16 a[20:23],a[116:119],v[164:167],a[20:23]
v_mfma_f32_16x16x32_bf16 a[20:23],a[112:115],v[132:135],a[20:23]
v_mfma_f32_16x16x32_bf16 a[24:27],a[112:115],v[144:147],a[24:27]
v_mfma_f32_16x16x32_bf16 a[24:27],a[116:119],v[176:179],a[24:27]
v_mfma_f32_16x16x32_bf16 a[24:27],a[120:123],v[208:211],a[24:27]
buffer_load_dwordx4 v[120:123],v6,s[28:31],s79 offen offset:2048 sc1
v_mfma_f32_16x16x32_bf16 a[24:27],a[124:127],v[240:243],a[24:27]
v_mfma_f32_16x16x32_bf16 a[28:31],a[124:127],v[244:247],a[28:31]
ds_read_b64_tr_b16 a[124:125],v2 offset:30752
ds_read_b64_tr_b16 a[126:127],v2 offset:35872
v_mfma_f32_16x16x32_bf16 a[28:31],a[120:123],v[212:215],a[28:31]
s_addk_i32 m0,1024
ds_read_b64_tr_b16 a[120:121],v2 offset:20512
v_mfma_f32_16x16x32_bf16 a[28:31],a[116:119],v[180:183],a[28:31]
ds_read_b64_tr_b16 a[116:117],v2 offset:10272
v_mfma_f32_16x16x32_bf16 a[28:31],a[112:115],v[148:151],a[28:31]
s_cmp_ge_u32 2,s71
s_cselect_b32 s76,6,4
ds_read_b64_tr_b16 a[112:113],v2 offset:32
v_mfma_f32_16x16x32_bf16 a[32:35],a[128:131],v[128:131],a[32:35]
buffer_load_dwordx4 v7,s[72:75],s83 offen lds
v_mfma_f32_16x16x32_bf16 a[32:35],a[132:135],v[160:163],a[32:35]
s_bitcmp1_b32 s54,s76
s_cselect_b32 s77,8192,0
s_lshl_b32 s76,s76,17
v_mfma_f32_16x16x32_bf16 a[32:35],a[136:139],v[192:195],a[32:35]
s_add_u32 s76,s76,s77
s_add_u32 s77,s76,4096
s_add_u32 s78,s76,16384
v_mfma_f32_16x16x32_bf16 a[32:35],a[140:143],v[224:227],a[32:35]
s_max_i32 s77,s77,s19
s_max_i32 s78,s78,s100
ds_read_b64_tr_b16 a[114:115],v2 offset:5152
v_mfma_f32_16x16x32_bf16 a[36:39],a[140:143],v[228:231],a[36:39]
buffer_load_dwordx4 v[124:127],v6,s[28:31],s79 offen offset:3072 sc1
v_mfma_f32_16x16x32_bf16 a[36:39],a[136:139],v[196:199],a[36:39]
s_add_u32 s79,s76,20480
s_max_i32 s76,s76,s18
ds_read_b64_tr_b16 a[118:119],v2 offset:15392
v_mfma_f32_16x16x32_bf16 a[36:39],a[132:135],v[164:167],a[36:39]
s_max_i32 s79,s79,s101
ds_read_b64_tr_b16 a[122:123],v2 offset:25632
v_mfma_f32_16x16x32_bf16 a[36:39],a[128:131],v[132:135],a[36:39]
v_mfma_f32_16x16x32_bf16 a[40:43],a[128:131],v[144:147],a[40:43]
v_mfma_f32_16x16x32_bf16 a[40:43],a[132:135],v[176:179],a[40:43]
v_mfma_f32_16x16x32_bf16 a[40:43],a[136:139],v[208:211],a[40:43]
v_mfma_f32_16x16x32_bf16 a[40:43],a[140:143],v[240:243],a[40:43]
v_mfma_f32_16x16x32_bf16 a[44:47],a[140:143],v[244:247],a[44:47]
ds_read_b64_tr_b16 a[140:141],v1 offset:30784
ds_read_b64_tr_b16 a[142:143],v1 offset:35904
v_mfma_f32_16x16x32_bf16 a[44:47],a[136:139],v[212:215],a[44:47]
ds_read_b64_tr_b16 a[136:137],v1 offset:20544
v_mfma_f32_16x16x32_bf16 a[44:47],a[132:135],v[180:183],a[44:47]
ds_read_b64_tr_b16 a[132:133],v1 offset:10304
v_mfma_f32_16x16x32_bf16 a[44:47],a[128:131],v[148:151],a[44:47]
buffer_load_dwordx4 v8,s[72:75],s83 offen offset:1024 lds
v_mfma_f32_16x16x32_bf16 a[48:51],a[144:147],v[128:131],a[48:51]
ds_read_b64_tr_b16 a[128:129],v1 offset:64
ds_read_b64_tr_b16 a[130:131],v1 offset:5184
v_mfma_f32_16x16x32_bf16 a[48:51],a[148:151],v[160:163],a[48:51]
ds_read_b64_tr_b16 a[134:135],v1 offset:15424
v_mfma_f32_16x16x32_bf16 a[48:51],a[152:155],v[192:195],a[48:51]
ds_read_b64_tr_b16 a[138:139],v1 offset:25664
v_mfma_f32_16x16x32_bf16 a[48:51],a[156:159],v[224:227],a[48:51]
s_lshl_b32 s94,s70,4
v_bfe_u32 v24,v6,4,2
v_min_u32 v24,2,v24
v_mfma_f32_16x16x32_bf16 a[52:55],a[156:159],v[228:231],a[52:55]
s_add_u32 s94,s94,s40
v_add_u32 v24,s71,v24
v_add_u32 v24,1,v24
v_mfma_f32_16x16x32_bf16 a[52:55],a[152:155],v[196:199],a[52:55]
s_lshl_b32 s94,s94,4
v_and_b32 v24,3,v24
v_lshlrev_b32 v24,6,v24
v_mfma_f32_16x16x32_bf16 a[52:55],a[148:151],v[164:167],a[52:55]
v_mfma_f32_16x16x32_bf16 a[52:55],a[144:147],v[132:135],a[52:55]
buffer_load_dwordx4 v[136:139],v6,s[28:31],s76 offen sc1
v_mfma_f32_16x16x32_bf16 a[56:59],a[144:147],v[144:147],a[56:59]
v_mfma_f32_16x16x32_bf16 a[56:59],a[148:151],v[176:179],a[56:59]
v_mfma_f32_16x16x32_bf16 a[56:59],a[152:155],v[208:211],a[56:59]
v_mfma_f32_16x16x32_bf16 a[56:59],a[156:159],v[240:243],a[56:59]
v_mfma_f32_16x16x32_bf16 a[60:63],a[156:159],v[244:247],a[60:63]
ds_read_b64_tr_b16 a[156:157],v2 offset:30816
ds_read_b64_tr_b16 a[158:159],v2 offset:35936
v_mfma_f32_16x16x32_bf16 a[60:63],a[152:155],v[212:215],a[60:63]
buffer_load_dwordx4 v[140:143],v6,s[28:31],s76 offen offset:1024 sc1
v_mfma_f32_16x16x32_bf16 a[60:63],a[148:151],v[180:183],a[60:63]
ds_read_b64_tr_b16 a[148:149],v2 offset:10336
ds_read_b64_tr_b16 a[150:151],v2 offset:15456
v_mfma_f32_16x16x32_bf16 a[60:63],a[144:147],v[148:151],a[60:63]
s_waitcnt lgkmcnt(4)
ds_read_b64_tr_b16 a[144:145],v2 offset:96
v_mfma_f32_16x16x32_bf16 a[64:67],a[160:163],v[128:131],a[64:67]
)A"
R"A(ds_read_b64_tr_b16 a[146:147],v2 offset:5216
v_mfma_f32_16x16x32_bf16 a[64:67],a[164:167],v[160:163],a[64:67]
buffer_load_dwordx4 v9,s[72:75],s83 offen offset:2048 lds
v_mfma_f32_16x16x32_bf16 a[64:67],a[168:171],v[192:195],a[64:67]
ds_read_b64_tr_b16 a[152:153],v2 offset:20576
ds_read_b64_tr_b16 a[154:155],v2 offset:25696
v_mfma_f32_16x16x32_bf16 a[64:67],a[172:175],v[224:227],a[64:67]
v_mfma_f32_16x16x32_bf16 a[68:71],a[172:175],v[228:231],a[68:71]
v_mfma_f32_16x16x32_bf16 a[68:71],a[168:171],v[196:199],a[68:71]
buffer_load_dwordx4 v[152:155],v6,s[28:31],s76 offen offset:2048 sc1
v_mfma_f32_16x16x32_bf16 a[68:71],a[164:167],v[164:167],a[68:71]
v_mfma_f32_16x16x32_bf16 a[68:71],a[160:163],v[132:135],a[68:71]
v_mfma_f32_16x16x32_bf16 a[72:75],a[160:163],v[144:147],a[72:75]
v_mfma_f32_16x16x32_bf16 a[72:75],a[164:167],v[176:179],a[72:75]
buffer_load_dwordx4 v[156:159],v6,s[28:31],s76 offen offset:3072 sc1
v_mfma_f32_16x16x32_bf16 a[72:75],a[168:171],v[208:211],a[72:75]
v_mfma_f32_16x16x32_bf16 a[72:75],a[172:175],v[240:243],a[72:75]
v_mfma_f32_16x16x32_bf16 a[76:79],a[172:175],v[244:247],a[76:79]
ds_read_b64_tr_b16 a[172:173],v1 offset:30848
ds_read_b64_tr_b16 a[174:175],v1 offset:35968
v_mfma_f32_16x16x32_bf16 a[76:79],a[168:171],v[212:215],a[76:79]
buffer_load_dwordx4 v10,s[72:75],s83 offen offset:3072 lds
v_mfma_f32_16x16x32_bf16 a[76:79],a[164:167],v[180:183],a[76:79]
ds_read_b64_tr_b16 a[164:165],v1 offset:10368
ds_read_b64_tr_b16 a[166:167],v1 offset:15488
v_mfma_f32_16x16x32_bf16 a[76:79],a[160:163],v[148:151],a[76:79]
ds_read_b64_tr_b16 a[160:161],v1 offset:128
v_mfma_f32_16x16x32_bf16 a[80:83],a[176:179],v[128:131],a[80:83]
ds_read_b64_tr_b16 a[162:163],v1 offset:5248
v_mfma_f32_16x16x32_bf16 a[80:83],a[180:183],v[160:163],a[80:83]
buffer_load_dwordx4 v[168:171],v6,s[28:31],s77 offen sc1
v_mfma_f32_16x16x32_bf16 a[80:83],a[184:187],v[192:195],a[80:83]
ds_read_b64_tr_b16 a[168:169],v1 offset:20608
ds_read_b64_tr_b16 a[170:171],v1 offset:25728
v_mfma_f32_16x16x32_bf16 a[80:83],a[188:191],v[224:227],a[80:83]
v_mfma_f32_16x16x32_bf16 a[84:87],a[188:191],v[228:231],a[84:87]
v_mfma_f32_16x16x32_bf16 a[84:87],a[184:187],v[196:199],a[84:87]
buffer_load_dwordx4 v[172:175],v6,s[28:31],s77 offen offset:1024 sc1
v_mfma_f32_16x16x32_bf16 a[84:87],a[180:183],v[164:167],a[84:87]
v_mfma_f32_16x16x32_bf16 a[84:87],a[176:179],v[132:135],a[84:87]
v_mfma_f32_16x16x32_bf16 a[88:91],a[176:179],v[144:147],a[88:91]
v_mfma_f32_16x16x32_bf16 a[88:91],a[180:183],v[176:179],a[88:91]
s_addk_i32 m0,4096
v_mfma_f32_16x16x32_bf16 a[88:91],a[184:187],v[208:211],a[88:91]
v_mfma_f32_16x16x32_bf16 a[88:91],a[188:191],v[240:243],a[88:91]
buffer_load_dwordx2 v[4:5],v24,s[64:67],s94 offen offset:1024 sc1
v_mfma_f32_16x16x32_bf16 a[92:95],a[188:191],v[244:247],a[92:95]
ds_read_b64_tr_b16 a[188:189],v2 offset:30880
ds_read_b64_tr_b16 a[190:191],v2 offset:36000
v_mfma_f32_16x16x32_bf16 a[92:95],a[184:187],v[212:215],a[92:95]
ds_read_b64_tr_b16 a[184:185],v2 offset:20640
v_mfma_f32_16x16x32_bf16 a[92:95],a[180:183],v[180:183],a[92:95]
ds_read_b64_tr_b16 a[180:181],v2 offset:10400
v_mfma_f32_16x16x32_bf16 a[92:95],a[176:179],v[148:151],a[92:95]
s_waitcnt vmcnt(13)
buffer_load_dwordx4 v[184:187],v6,s[28:31],s77 offen offset:2048 sc1
v_mfma_f32_16x16x32_bf16 a[192:195],a[96:99],v[64:67],a[192:195]
s_lshr_b32 s44,s71,1
ds_read_b64_tr_b16 a[176:177],v2 offset:160
ds_read_b64_tr_b16 a[178:179],v2 offset:5280
v_mfma_f32_16x16x32_bf16 a[192:195],a[100:103],v[68:71],a[192:195]
s_lshl_b32 s43,s41,9
s_mul_i32 s44,s44,22
ds_read_b64_tr_b16 a[182:183],v2 offset:15520
v_mfma_f32_16x16x32_bf16 a[192:195],a[104:107],v[72:75],a[192:195]
s_add_u32 s43,s43,s44
s_add_u32 s43,s43,s71
ds_read_b64_tr_b16 a[186:187],v2 offset:25760
v_mfma_f32_16x16x32_bf16 a[192:195],a[108:111],v[76:79],a[192:195]
s_add_u32 s43,s43,s40
buffer_load_dwordx4 v11,s[72:75],s83 offen lds
v_mfma_f32_16x16x32_bf16 a[196:199],a[108:111],v[92:95],a[196:199]
s_add_u32 s43,s43,s40
s_add_u32 s43,s43,64
s_lshl_b32 s43,s43,14
v_mfma_f32_16x16x32_bf16 a[196:199],a[104:107],v[88:91],a[196:199]
s_mov_b64 s[32:33],s[12:13]
s_and_b32 s87,s55,8192
s_lshl_b32 s84,s70,21
v_mfma_f32_16x16x32_bf16 a[196:199],a[100:103],v[84:87],a[196:199]
s_add_u32 s87,s87,s84
s_add_u32 s32,s32,s43
s_addc_u32 s33,s33,0
v_mfma_f32_16x16x32_bf16 a[196:199],a[96:99],v[80:83],a[196:199]
s_add_u32 s84,s87,0
buffer_load_dwordx4 v[188:191],v6,s[28:31],s77 offen offset:3072 sc1
v_mfma_f32_16x16x32_bf16 a[200:203],a[96:99],v[96:99],a[200:203]
s_add_u32 s85,s87,131072
s_add_u32 s86,s87,262144
s_and_b32 s33,s33,0xffff
v_mfma_f32_16x16x32_bf16 a[200:203],a[100:103],v[100:103],a[200:203]
s_mov_b32 s34,6569984
s_mov_b32 s35,0x20000
s_max_i32 s84,s84,s18
v_mfma_f32_16x16x32_bf16 a[200:203],a[104:107],v[104:107],a[200:203]
s_max_i32 s85,s85,s19
s_max_i32 s86,s86,s100
v_bfe_u32 v18,v6,6,2
v_mfma_f32_16x16x32_bf16 a[200:203],a[108:111],v[108:111],a[200:203]
s_mul_i32 s44,s41,307200
buffer_load_dwordx4 v[200:203],v6,s[28:31],s78 offen sc1
s_waitcnt vmcnt(14)
v_mfma_f32_16x16x32_bf16 a[204:207],a[108:111],v[124:127],a[204:207]
s_mov_b64 s[20:21],s[16:17]
ds_read_b64_tr_b16 a[108:109],v1 offset:30912
ds_read_b64_tr_b16 a[110:111],v1 offset:36032
v_mfma_f32_16x16x32_bf16 a[204:207],a[104:107],v[120:123],a[204:207]
s_lshl_b32 s43,s40,2
ds_read_b64_tr_b16 a[104:105],v1 offset:20672
v_lshrrev_b32 v19,8,v6
v_mfma_f32_16x16x32_bf16 a[204:207],a[100:103],v[116:119],a[204:207]
s_add_u32 s43,s43,s70
ds_read_b64_tr_b16 a[100:101],v1 offset:10432
v_xor_b32 v18,v18,v19
v_mfma_f32_16x16x32_bf16 a[204:207],a[96:99],v[112:115],a[204:207]
s_barrier
buffer_load_dwordx4 v[204:207],v6,s[28:31],s78 offen offset:1024 sc1
v_mfma_f32_16x16x32_bf16 a[208:211],a[112:115],v[64:67],a[208:211]
s_mulk_i32 s43,20480
ds_read_b64_tr_b16 a[96:97],v1 offset:192
ds_read_b64_tr_b16 a[98:99],v1 offset:5312
v_mfma_f32_16x16x32_bf16 a[208:211],a[116:119],v[68:71],a[208:211]
s_mov_b32 s22,307200
ds_read_b64_tr_b16 a[102:103],v1 offset:15552
v_lshlrev_b32 v18,2,v18
v_mfma_f32_16x16x32_bf16 a[208:211],a[120:123],v[72:75],a[208:211]
s_mov_b32 s23,0x20000
ds_read_b64_tr_b16 a[106:107],v1 offset:25792
v_bfe_u32 v19,v6,4,2
v_mfma_f32_16x16x32_bf16 a[208:211],a[124:127],v[76:79],a[208:211]
buffer_load_dwordx4 v[216:219],v6,s[28:31],s78 offen offset:2048 sc1
v_mfma_f32_16x16x32_bf16 a[212:215],a[124:127],v[92:95],a[212:215]
v_or_b32 v18,v18,v19
v_and_b32 v19,16,v6
v_mfma_f32_16x16x32_bf16 a[212:215],a[120:123],v[88:91],a[212:215]
v_mov_b32 v34,0x3f80
v_lshlrev_b32 v19,v19,v34
v_mfma_f32_16x16x32_bf16 a[212:215],a[116:119],v[84:87],a[212:215]
v_lshrrev_b32 v18,1,v18
v_add_u32 v25,4096,v6
v_mfma_f32_16x16x32_bf16 a[212:215],a[112:115],v[80:83],a[212:215]
s_cmp_ge_u32 0,s71
s_cselect_b32 s76,3,1
v_mov_b32 v36,0
v_mfma_f32_16x16x32_bf16 a[216:219],a[112:115],v[96:99],a[216:219]
buffer_load_dwordx4 v[220:223],v6,s[28:31],s78 offen offset:3072 sc1
v_mfma_f32_16x16x32_bf16 a[216:219],a[116:119],v[100:103],a[216:219]
s_bitcmp1_b32 s54,s76
s_cselect_b32 s77,8192,0
s_lshl_b32 s76,s76,17
v_mfma_f32_16x16x32_bf16 a[216:219],a[120:123],v[104:107],a[216:219]
s_add_u32 s76,s76,s77
s_add_u32 s77,s76,4096
s_add_u32 s78,s76,16384
v_mfma_f32_16x16x32_bf16 a[216:219],a[124:127],v[108:111],a[216:219]
s_max_i32 s77,s77,s19
s_max_i32 s78,s78,s100
v_mov_b32 v37,0
v_mfma_f32_16x16x32_bf16 a[220:223],a[124:127],v[124:127],a[220:223]
ds_read_b64_tr_b16 a[124:125],v2 offset:30944
ds_read_b64_tr_b16 a[126:127],v2 offset:36064
v_mfma_f32_16x16x32_bf16 a[220:223],a[120:123],v[120:123],a[220:223]
ds_read_b64_tr_b16 a[120:121],v2 offset:20704
v_mov_b32 v32,0
v_mfma_f32_16x16x32_bf16 a[220:223],a[116:119],v[116:119],a[220:223]
buffer_load_dwordx4 v[232:235],v6,s[28:31],s79 offen sc1
v_mfma_f32_16x16x32_bf16 a[220:223],a[112:115],v[112:115],a[220:223]
ds_read_b64_tr_b16 a[112:113],v2 offset:224
ds_read_b64_tr_b16 a[114:115],v2 offset:5344
v_mfma_f32_16x16x32_bf16 a[224:227],a[128:131],v[64:67],a[224:227]
ds_read_b64_tr_b16 a[116:117],v2 offset:10464
v_mov_b32 v33,0
v_mfma_f32_16x16x32_bf16 a[224:227],a[132:135],v[68:71],a[224:227]
ds_read_b64_tr_b16 a[118:119],v2 offset:15584
v_bfe_u32 v40,v6,4,4
v_mfma_f32_16x16x32_bf16 a[224:227],a[136:139],v[72:75],a[224:227]
ds_read_b64_tr_b16 a[122:123],v2 offset:25824
v_mul_u32_u24 v40,1280,v40
v_mfma_f32_16x16x32_bf16 a[224:227],a[140:143],v[76:79],a[224:227]
v_cmp_eq_u32 vcc,0,v18
s_nop 1
v_cndmask_b32 v34,0,v19,vcc
v_mfma_f32_16x16x32_bf16 a[228:231],a[140:143],v[92:95],a[228:231]
buffer_load_dwordx4 v[236:239],v6,s[28:31],s79 offen offset:1024 sc1
v_mfma_f32_16x16x32_bf16 a[228:231],a[136:139],v[88:91],a[228:231]
v_cmp_eq_u32 vcc,1,v18
s_nop 1
v_cndmask_b32 v35,0,v19,vcc
v_mfma_f32_16x16x32_bf16 a[228:231],a[132:135],v[84:87],a[228:231]
s_add_u32 s20,s20,s44
s_addc_u32 s21,s21,0
v_bfe_u32 v41,v6,8,1
v_mfma_f32_16x16x32_bf16 a[228:231],a[128:131],v[80:83],a[228:231]
s_waitcnt vmcnt(9)
v_cmp_ne_u32 s[90:91],s68,v4
v_and_b32 v4,-2,v5
v_cmp_ne_u32 s[92:93],s69,v4
s_nop 0
s_or_b64 s[90:91],s[90:91],s[92:93]
s_cbranch_scc1 L_fr9_%=
L_fo9_%=:
v_mfma_f32_16x16x32_bf16 a[232:235],a[128:131],v[96:99],a[232:235]
s_mul_i32 s44,s71,320
v_lshl_add_u32 v40,v41,5,v40
v_lshrrev_b32 v41,9,v6
v_mfma_f32_16x16x32_bf16 a[232:235],a[132:135],v[100:103],a[232:235]
s_and_b32 s21,s21,0xffff
s_add_u32 s43,s43,s44
v_lshl_add_u32 v40,v41,4,v40
v_mfma_f32_16x16x32_bf16 a[232:235],a[136:139],v[104:107],a[232:235]
buffer_load_dwordx4 v[248:251],v6,s[28:31],s79 offen offset:2048 sc1
v_mfma_f32_16x16x32_bf16 a[232:235],a[140:143],v[108:111],a[232:235]
v_mfma_f32_16x16x32_bf16 a[236:239],a[140:143],v[124:127],a[236:239]
ds_read_b64_tr_b16 a[140:141],v1 offset:30976
ds_read_b64_tr_b16 a[142:143],v1 offset:36096
v_mfma_f32_16x16x32_bf16 a[236:239],a[136:139],v[120:123],a[236:239]
ds_read_b64_tr_b16 a[136:137],v1 offset:20736
v_mfma_f32_16x16x32_bf16 a[236:239],a[132:135],v[116:119],a[236:239]
ds_read_b64_tr_b16 a[132:133],v1 offset:10496
v_mfma_f32_16x16x32_bf16 a[236:239],a[128:131],v[112:115],a[236:239]
s_waitcnt lgkmcnt(4)
ds_read_b64_tr_b16 a[128:129],v1 offset:256
v_mfma_f32_16x16x32_bf16 a[240:243],a[144:147],v[64:67],a[240:243]
buffer_load_dwordx4 v[252:255],v6,s[28:31],s79 offen offset:3072 sc1
v_mfma_f32_16x16x32_bf16 a[240:243],a[148:151],v[68:71],a[240:243]
s_add_u32 s79,s76,20480
s_max_i32 s76,s76,s18
ds_read_b64_tr_b16 a[130:131],v1 offset:5376
v_mfma_f32_16x16x32_bf16 a[240:243],a[152:155],v[72:75],a[240:243]
s_max_i32 s79,s79,s101
ds_read_b64_tr_b16 a[134:135],v1 offset:15616
ds_read_b64_tr_b16 a[138:139],v1 offset:25856
v_mfma_f32_16x16x32_bf16 a[240:243],a[156:159],v[76:79],a[240:243]
v_subrev_u32 v1,40960,v1
v_mfma_f32_16x16x32_bf16 a[244:247],a[156:159],v[92:95],a[244:247]
buffer_load_dwordx4 v[128:131],v6,s[28:31],s76 offen sc1
v_mfma_f32_16x16x32_bf16 a[244:247],a[152:155],v[88:91],a[244:247]
v_mfma_f32_16x16x32_bf16 a[244:247],a[148:151],v[84:87],a[244:247]
v_mfma_f32_16x16x32_bf16 a[244:247],a[144:147],v[80:83],a[244:247]
v_mfma_f32_16x16x32_bf16 a[248:251],a[144:147],v[96:99],a[248:251]
buffer_load_dwordx4 v[160:163],v6,s[28:31],s76 offen offset:1024 sc1
v_mfma_f32_16x16x32_bf16 a[248:251],a[148:151],v[100:103],a[248:251]
v_mfma_f32_16x16x32_bf16 a[248:251],a[152:155],v[104:107],a[248:251]
v_mfma_f32_16x16x32_bf16 a[248:251],a[156:159],v[108:111],a[248:251]
v_mfma_f32_16x16x32_bf16 a[252:255],a[156:159],v[124:127],a[252:255]
buffer_load_dwordx4 v[192:195],v6,s[28:31],s76 offen offset:2048 sc1
v_mfma_f32_16x16x32_bf16 a[252:255],a[152:155],v[120:123],a[252:255]
ds_read_b64_tr_b16 a[152:153],v2 offset:20768
ds_read_b64_tr_b16 a[154:155],v2 offset:25888
v_mfma_f32_16x16x32_bf16 a[252:255],a[148:151],v[116:119],a[252:255]
ds_read_b64_tr_b16 a[148:149],v2 offset:10528
v_mfma_f32_16x16x32_bf16 a[252:255],a[144:147],v[112:115],a[252:255]
ds_read_b64_tr_b16 a[144:145],v2 offset:288
v_mfma_f32_16x16x32_bf16 a[0:3],a[160:163],v[64:67],a[0:3]
ds_read_b64_tr_b16 a[146:147],v2 offset:5408
v_mfma_f32_16x16x32_bf16 a[0:3],a[164:167],v[68:71],a[0:3]
buffer_load_dwordx4 v[224:227],v6,s[28:31],s76 offen offset:3072 sc1
)A"
R"A(v_mfma_f32_16x16x32_bf16 a[0:3],a[168:171],v[72:75],a[0:3]
ds_read_b64_tr_b16 a[150:151],v2 offset:15648
ds_read_b64_tr_b16 a[156:157],v2 offset:31008
v_mfma_f32_16x16x32_bf16 a[0:3],a[172:175],v[76:79],a[0:3]
ds_read_b64_tr_b16 a[158:159],v2 offset:36128
v_subrev_u32 v2,40960,v2
v_mfma_f32_16x16x32_bf16 a[4:7],a[172:175],v[92:95],a[4:7]
v_mfma_f32_16x16x32_bf16 a[4:7],a[168:171],v[88:91],a[4:7]
v_mfma_f32_16x16x32_bf16 a[4:7],a[164:167],v[84:87],a[4:7]
v_mfma_f32_16x16x32_bf16 a[4:7],a[160:163],v[80:83],a[4:7]
buffer_load_dwordx4 v[132:135],v6,s[28:31],s77 offen sc1
v_mfma_f32_16x16x32_bf16 a[8:11],a[160:163],v[96:99],a[8:11]
v_mfma_f32_16x16x32_bf16 a[8:11],a[164:167],v[100:103],a[8:11]
v_mfma_f32_16x16x32_bf16 a[8:11],a[168:171],v[104:107],a[8:11]
v_mfma_f32_16x16x32_bf16 a[8:11],a[172:175],v[108:111],a[8:11]
v_mfma_f32_16x16x32_bf16 a[12:15],a[172:175],v[124:127],a[12:15]
ds_read_b64_tr_b16 a[172:173],v1 offset:30720
ds_read_b64_tr_b16 a[174:175],v1 offset:35840
v_mfma_f32_16x16x32_bf16 a[12:15],a[168:171],v[120:123],a[12:15]
buffer_load_dwordx4 v[164:167],v6,s[28:31],s77 offen offset:1024 sc1
v_mfma_f32_16x16x32_bf16 a[12:15],a[164:167],v[116:119],a[12:15]
ds_read_b64_tr_b16 a[164:165],v1 offset:10240
ds_read_b64_tr_b16 a[166:167],v1 offset:15360
v_mfma_f32_16x16x32_bf16 a[12:15],a[160:163],v[112:115],a[12:15]
ds_read_b64_tr_b16 a[160:161],v1
v_mfma_f32_16x16x32_bf16 a[16:19],a[176:179],v[64:67],a[16:19]
ds_read_b64_tr_b16 a[162:163],v1 offset:5120
v_mfma_f32_16x16x32_bf16 a[16:19],a[180:183],v[68:71],a[16:19]
ds_read_b64_tr_b16 a[168:169],v1 offset:20480
v_mfma_f32_16x16x32_bf16 a[16:19],a[184:187],v[72:75],a[16:19]
ds_read_b64_tr_b16 a[170:171],v1 offset:25600
v_mfma_f32_16x16x32_bf16 a[16:19],a[188:191],v[76:79],a[16:19]
buffer_load_dwordx4 v[196:199],v6,s[28:31],s77 offen offset:2048 sc1
v_mfma_f32_16x16x32_bf16 a[20:23],a[188:191],v[92:95],a[20:23]
v_mfma_f32_16x16x32_bf16 a[20:23],a[184:187],v[88:91],a[20:23]
v_mfma_f32_16x16x32_bf16 a[20:23],a[180:183],v[84:87],a[20:23]
v_mfma_f32_16x16x32_bf16 a[20:23],a[176:179],v[80:83],a[20:23]
v_mfma_f32_16x16x32_bf16 a[24:27],a[176:179],v[96:99],a[24:27]
v_mfma_f32_16x16x32_bf16 a[24:27],a[180:183],v[100:103],a[24:27]
buffer_load_dwordx4 v[228:231],v6,s[28:31],s77 offen offset:3072 sc1
v_mfma_f32_16x16x32_bf16 a[24:27],a[184:187],v[104:107],a[24:27]
v_mfma_f32_16x16x32_bf16 a[24:27],a[188:191],v[108:111],a[24:27]
v_mfma_f32_16x16x32_bf16 a[28:31],a[188:191],v[124:127],a[28:31]
ds_read_b64_tr_b16 a[188:189],v2 offset:30752
ds_read_b64_tr_b16 a[190:191],v2 offset:35872
v_mfma_f32_16x16x32_bf16 a[28:31],a[184:187],v[120:123],a[28:31]
ds_read_b64_tr_b16 a[184:185],v2 offset:20512
v_mfma_f32_16x16x32_bf16 a[28:31],a[180:183],v[116:119],a[28:31]
ds_read_b64_tr_b16 a[180:181],v2 offset:10272
v_mfma_f32_16x16x32_bf16 a[28:31],a[176:179],v[112:115],a[28:31]
buffer_load_dwordx4 v[144:147],v6,s[28:31],s78 offen sc1
v_mfma_f32_16x16x32_bf16 a[32:35],a[96:99],v[64:67],a[32:35]
ds_read_b64_tr_b16 a[176:177],v2 offset:32
ds_read_b64_tr_b16 a[178:179],v2 offset:5152
v_mfma_f32_16x16x32_bf16 a[32:35],a[100:103],v[68:71],a[32:35]
ds_read_b64_tr_b16 a[182:183],v2 offset:15392
v_mfma_f32_16x16x32_bf16 a[32:35],a[104:107],v[72:75],a[32:35]
ds_read_b64_tr_b16 a[186:187],v2 offset:25632
v_mfma_f32_16x16x32_bf16 a[32:35],a[108:111],v[76:79],a[32:35]
v_mfma_f32_16x16x32_bf16 a[36:39],a[108:111],v[92:95],a[36:39]
v_mfma_f32_16x16x32_bf16 a[36:39],a[104:107],v[88:91],a[36:39]
buffer_load_dwordx4 v[176:179],v6,s[28:31],s78 offen offset:1024 sc1
v_mfma_f32_16x16x32_bf16 a[36:39],a[100:103],v[84:87],a[36:39]
v_mfma_f32_16x16x32_bf16 a[36:39],a[96:99],v[80:83],a[36:39]
v_mfma_f32_16x16x32_bf16 a[40:43],a[96:99],v[96:99],a[40:43]
v_mfma_f32_16x16x32_bf16 a[40:43],a[100:103],v[100:103],a[40:43]
v_mfma_f32_16x16x32_bf16 a[40:43],a[104:107],v[104:107],a[40:43]
v_mfma_f32_16x16x32_bf16 a[40:43],a[108:111],v[108:111],a[40:43]
buffer_load_dwordx4 v[208:211],v6,s[28:31],s78 offen offset:2048 sc1
v_mfma_f32_16x16x32_bf16 a[44:47],a[108:111],v[124:127],a[44:47]
ds_read_b64_tr_b16 a[108:109],v1 offset:30784
ds_read_b64_tr_b16 a[110:111],v1 offset:35904
v_mfma_f32_16x16x32_bf16 a[44:47],a[104:107],v[120:123],a[44:47]
ds_read_b64_tr_b16 a[104:105],v1 offset:20544
v_mfma_f32_16x16x32_bf16 a[44:47],a[100:103],v[116:119],a[44:47]
ds_read_b64_tr_b16 a[100:101],v1 offset:10304
v_mfma_f32_16x16x32_bf16 a[44:47],a[96:99],v[112:115],a[44:47]
ds_read_b64_tr_b16 a[96:97],v1 offset:64
v_mfma_f32_16x16x32_bf16 a[48:51],a[112:115],v[64:67],a[48:51]
ds_read_b64_tr_b16 a[98:99],v1 offset:5184
ds_read_b64_tr_b16 a[102:103],v1 offset:15424
v_mfma_f32_16x16x32_bf16 a[48:51],a[116:119],v[68:71],a[48:51]
buffer_load_dwordx4 v[240:243],v6,s[28:31],s78 offen offset:3072 sc1
v_mfma_f32_16x16x32_bf16 a[48:51],a[120:123],v[72:75],a[48:51]
ds_read_b64_tr_b16 a[106:107],v1 offset:25664
v_mfma_f32_16x16x32_bf16 a[48:51],a[124:127],v[76:79],a[48:51]
v_mfma_f32_16x16x32_bf16 a[52:55],a[124:127],v[92:95],a[52:55]
v_mfma_f32_16x16x32_bf16 a[52:55],a[120:123],v[88:91],a[52:55]
v_mfma_f32_16x16x32_bf16 a[52:55],a[116:119],v[84:87],a[52:55]
v_mfma_f32_16x16x32_bf16 a[52:55],a[112:115],v[80:83],a[52:55]
buffer_load_dwordx4 v[148:151],v6,s[28:31],s79 offen sc1
v_mfma_f32_16x16x32_bf16 a[56:59],a[112:115],v[96:99],a[56:59]
v_mfma_f32_16x16x32_bf16 a[56:59],a[116:119],v[100:103],a[56:59]
s_cmp_ge_u32 1,s71
s_cselect_b32 s80,5,3
s_mul_i32 s80,s80,163840
v_mfma_f32_16x16x32_bf16 a[56:59],a[120:123],v[104:107],a[56:59]
s_add_u32 s80,s80,s81
s_add_u32 s83,s80,20480
v_mfma_f32_16x16x32_bf16 a[56:59],a[124:127],v[108:111],a[56:59]
v_mfma_f32_16x16x32_bf16 a[60:63],a[124:127],v[124:127],a[60:63]
ds_read_b64_tr_b16 a[124:125],v2 offset:30816
ds_read_b64_tr_b16 a[126:127],v2 offset:35936
v_mfma_f32_16x16x32_bf16 a[60:63],a[120:123],v[120:123],a[60:63]
buffer_load_dwordx4 v[180:183],v6,s[28:31],s79 offen offset:1024 sc1
v_mfma_f32_16x16x32_bf16 a[60:63],a[116:119],v[116:119],a[60:63]
ds_read_b64_tr_b16 a[116:117],v2 offset:10336
ds_read_b64_tr_b16 a[118:119],v2 offset:15456
v_mfma_f32_16x16x32_bf16 a[60:63],a[112:115],v[112:115],a[60:63]
s_waitcnt lgkmcnt(4)
ds_read_b64_tr_b16 a[112:113],v2 offset:96
v_mfma_f32_16x16x32_bf16 a[64:67],a[128:131],v[64:67],a[64:67]
ds_read_b64_tr_b16 a[114:115],v2 offset:5216
v_mfma_f32_16x16x32_bf16 a[64:67],a[132:135],v[68:71],a[64:67]
ds_read_b64_tr_b16 a[120:121],v2 offset:20576
v_mfma_f32_16x16x32_bf16 a[64:67],a[136:139],v[72:75],a[64:67]
ds_read_b64_tr_b16 a[122:123],v2 offset:25696
v_mfma_f32_16x16x32_bf16 a[64:67],a[140:143],v[76:79],a[64:67]
buffer_load_dwordx4 v[212:215],v6,s[28:31],s79 offen offset:2048 sc1
v_mfma_f32_16x16x32_bf16 a[68:71],a[140:143],v[92:95],a[68:71]
v_mfma_f32_16x16x32_bf16 a[68:71],a[136:139],v[88:91],a[68:71]
v_mfma_f32_16x16x32_bf16 a[68:71],a[132:135],v[84:87],a[68:71]
v_mfma_f32_16x16x32_bf16 a[68:71],a[128:131],v[80:83],a[68:71]
v_mfma_f32_16x16x32_bf16 a[72:75],a[128:131],v[96:99],a[72:75]
v_mfma_f32_16x16x32_bf16 a[72:75],a[132:135],v[100:103],a[72:75]
buffer_load_dwordx4 v[244:247],v6,s[28:31],s79 offen offset:3072 sc1
v_mfma_f32_16x16x32_bf16 a[72:75],a[136:139],v[104:107],a[72:75]
v_mfma_f32_16x16x32_bf16 a[72:75],a[140:143],v[108:111],a[72:75]
v_mfma_f32_16x16x32_bf16 a[76:79],a[140:143],v[124:127],a[76:79]
ds_read_b64_tr_b16 a[140:141],v1 offset:30848
ds_read_b64_tr_b16 a[142:143],v1 offset:35968
v_mfma_f32_16x16x32_bf16 a[76:79],a[136:139],v[120:123],a[76:79]
ds_read_b64_tr_b16 a[136:137],v1 offset:20608
v_mfma_f32_16x16x32_bf16 a[76:79],a[132:135],v[116:119],a[76:79]
s_add_u32 m0,s82,40960
ds_read_b64_tr_b16 a[132:133],v1 offset:10368
v_mfma_f32_16x16x32_bf16 a[76:79],a[128:131],v[112:115],a[76:79]
ds_read_b64_tr_b16 a[128:129],v1 offset:128
v_mfma_f32_16x16x32_bf16 a[80:83],a[144:147],v[64:67],a[80:83]
ds_read_b64_tr_b16 a[130:131],v1 offset:5248
ds_read_b64_tr_b16 a[134:135],v1 offset:15488
v_mfma_f32_16x16x32_bf16 a[80:83],a[148:151],v[68:71],a[80:83]
buffer_load_dwordx4 v7,s[72:75],s80 offen lds
v_mfma_f32_16x16x32_bf16 a[80:83],a[152:155],v[72:75],a[80:83]
ds_read_b64_tr_b16 a[138:139],v1 offset:25728
v_mfma_f32_16x16x32_bf16 a[80:83],a[156:159],v[76:79],a[80:83]
v_mfma_f32_16x16x32_bf16 a[84:87],a[156:159],v[92:95],a[84:87]
v_mfma_f32_16x16x32_bf16 a[84:87],a[152:155],v[88:91],a[84:87]
v_mfma_f32_16x16x32_bf16 a[84:87],a[148:151],v[84:87],a[84:87]
buffer_load_dwordx4 v8,s[72:75],s80 offen offset:1024 lds
v_mfma_f32_16x16x32_bf16 a[84:87],a[144:147],v[80:83],a[84:87]
v_mfma_f32_16x16x32_bf16 a[88:91],a[144:147],v[96:99],a[88:91]
v_mfma_f32_16x16x32_bf16 a[88:91],a[148:151],v[100:103],a[88:91]
v_mfma_f32_16x16x32_bf16 a[88:91],a[152:155],v[104:107],a[88:91]
v_mfma_f32_16x16x32_bf16 a[88:91],a[156:159],v[108:111],a[88:91]
buffer_load_dwordx4 v9,s[72:75],s80 offen offset:2048 lds
v_mfma_f32_16x16x32_bf16 a[92:95],a[156:159],v[124:127],a[92:95]
ds_read_b64_tr_b16 a[156:157],v2 offset:30880
ds_read_b64_tr_b16 a[158:159],v2 offset:36000
v_mfma_f32_16x16x32_bf16 a[92:95],a[152:155],v[120:123],a[92:95]
ds_read_b64_tr_b16 a[152:153],v2 offset:20640
v_mfma_f32_16x16x32_bf16 a[92:95],a[148:151],v[116:119],a[92:95]
ds_read_b64_tr_b16 a[148:149],v2 offset:10400
v_mfma_f32_16x16x32_bf16 a[92:95],a[144:147],v[112:115],a[92:95]
ds_read_b64_tr_b16 a[144:145],v2 offset:160
v_mfma_f32_16x16x32_bf16 a[192:195],a[160:163],v[136:139],a[192:195]
buffer_load_dwordx4 v10,s[72:75],s80 offen offset:3072 lds
v_mfma_f32_16x16x32_bf16 a[192:195],a[164:167],v[140:143],a[192:195]
ds_read_b64_tr_b16 a[146:147],v2 offset:5280
ds_read_b64_tr_b16 a[150:151],v2 offset:15520
v_mfma_f32_16x16x32_bf16 a[192:195],a[168:171],v[152:155],a[192:195]
s_addk_i32 m0,4096
ds_read_b64_tr_b16 a[154:155],v2 offset:25760
v_mfma_f32_16x16x32_bf16 a[192:195],a[172:175],v[156:159],a[192:195]
s_waitcnt vmcnt(12)
v_mfma_f32_16x16x32_bf16 a[196:199],a[172:175],v[188:191],a[196:199]
v_mfma_f32_16x16x32_bf16 a[196:199],a[168:171],v[184:187],a[196:199]
buffer_load_dwordx4 v11,s[72:75],s80 offen lds
v_mfma_f32_16x16x32_bf16 a[196:199],a[164:167],v[172:175],a[196:199]
v_mfma_f32_16x16x32_bf16 a[196:199],a[160:163],v[168:171],a[196:199]
s_addk_i32 m0,1024
v_mfma_f32_16x16x32_bf16 a[200:203],a[160:163],v[200:203],a[200:203]
v_mfma_f32_16x16x32_bf16 a[200:203],a[164:167],v[204:207],a[200:203]
v_mfma_f32_16x16x32_bf16 a[200:203],a[168:171],v[216:219],a[200:203]
buffer_load_dwordx4 v7,s[72:75],s83 offen lds
v_mfma_f32_16x16x32_bf16 a[200:203],a[172:175],v[220:223],a[200:203]
v_mfma_f32_16x16x32_bf16 a[204:207],a[172:175],v[252:255],a[204:207]
ds_read_b64_tr_b16 a[172:173],v1 offset:30912
ds_read_b64_tr_b16 a[174:175],v1 offset:36032
v_mfma_f32_16x16x32_bf16 a[204:207],a[168:171],v[248:251],a[204:207]
ds_read_b64_tr_b16 a[168:169],v1 offset:20672
v_mfma_f32_16x16x32_bf16 a[204:207],a[164:167],v[236:239],a[204:207]
ds_read_b64_tr_b16 a[164:165],v1 offset:10432
v_mfma_f32_16x16x32_bf16 a[204:207],a[160:163],v[232:235],a[204:207]
s_barrier
buffer_load_dwordx4 v8,s[72:75],s83 offen offset:1024 lds
v_mfma_f32_16x16x32_bf16 a[208:211],a[176:179],v[136:139],a[208:211]
s_cmp_ge_u32 1,s71
s_cselect_b32 s76,5,3
ds_read_b64_tr_b16 a[160:161],v1 offset:192
v_mfma_f32_16x16x32_bf16 a[208:211],a[180:183],v[140:143],a[208:211]
s_bitcmp1_b32 s54,s76
s_cselect_b32 s77,8192,0
s_lshl_b32 s76,s76,17
v_mfma_f32_16x16x32_bf16 a[208:211],a[184:187],v[152:155],a[208:211]
s_add_u32 s76,s76,s77
s_add_u32 s77,s76,4096
s_add_u32 s78,s76,16384
v_mfma_f32_16x16x32_bf16 a[208:211],a[188:191],v[156:159],a[208:211]
s_add_u32 s79,s76,20480
buffer_load_dwordx4 v9,s[72:75],s83 offen offset:2048 lds
v_mfma_f32_16x16x32_bf16 a[212:215],a[188:191],v[188:191],a[212:215]
s_max_i32 s76,s76,s18
s_max_i32 s77,s77,s19
s_max_i32 s78,s78,s100
v_mfma_f32_16x16x32_bf16 a[212:215],a[184:187],v[184:187],a[212:215]
s_max_i32 s79,s79,s101
ds_read_b64_tr_b16 a[162:163],v1 offset:5312
ds_read_b64_tr_b16 a[166:167],v1 offset:15552
v_mfma_f32_16x16x32_bf16 a[212:215],a[180:183],v[172:175],a[212:215]
ds_read_b64_tr_b16 a[170:171],v1 offset:25792
v_mfma_f32_16x16x32_bf16 a[212:215],a[176:179],v[168:171],a[212:215]
)A"
R"A(v_mfma_f32_16x16x32_bf16 a[216:219],a[176:179],v[200:203],a[216:219]
buffer_load_dwordx4 v10,s[72:75],s83 offen offset:3072 lds
v_mfma_f32_16x16x32_bf16 a[216:219],a[180:183],v[204:207],a[216:219]
v_mfma_f32_16x16x32_bf16 a[216:219],a[184:187],v[216:219],a[216:219]
s_addk_i32 m0,4096
v_mfma_f32_16x16x32_bf16 a[216:219],a[188:191],v[220:223],a[216:219]
v_mfma_f32_16x16x32_bf16 a[220:223],a[188:191],v[252:255],a[220:223]
ds_read_b64_tr_b16 a[188:189],v2 offset:30944
ds_read_b64_tr_b16 a[190:191],v2 offset:36064
v_mfma_f32_16x16x32_bf16 a[220:223],a[184:187],v[248:251],a[220:223]
buffer_load_dwordx4 v11,s[72:75],s83 offen lds
v_mfma_f32_16x16x32_bf16 a[220:223],a[180:183],v[236:239],a[220:223]
ds_read_b64_tr_b16 a[180:181],v2 offset:10464
ds_read_b64_tr_b16 a[182:183],v2 offset:15584
v_mfma_f32_16x16x32_bf16 a[220:223],a[176:179],v[232:235],a[220:223]
ds_read_b64_tr_b16 a[176:177],v2 offset:224
v_mfma_f32_16x16x32_bf16 a[224:227],a[96:99],v[136:139],a[224:227]
ds_read_b64_tr_b16 a[178:179],v2 offset:5344
v_mfma_f32_16x16x32_bf16 a[224:227],a[100:103],v[140:143],a[224:227]
ds_read_b64_tr_b16 a[184:185],v2 offset:20704
v_mfma_f32_16x16x32_bf16 a[224:227],a[104:107],v[152:155],a[224:227]
ds_read_b64_tr_b16 a[186:187],v2 offset:25824
v_mfma_f32_16x16x32_bf16 a[224:227],a[108:111],v[156:159],a[224:227]
v_mfma_f32_16x16x32_bf16 a[228:231],a[108:111],v[188:191],a[228:231]
v_mfma_f32_16x16x32_bf16 a[228:231],a[104:107],v[184:187],a[228:231]
v_mfma_f32_16x16x32_bf16 a[228:231],a[100:103],v[172:175],a[228:231]
v_mfma_f32_16x16x32_bf16 a[228:231],a[96:99],v[168:171],a[228:231]
buffer_load_dwordx4 v[64:67],v6,s[28:31],s76 offen sc1
v_mfma_f32_16x16x32_bf16 a[232:235],a[96:99],v[200:203],a[232:235]
v_mfma_f32_16x16x32_bf16 a[232:235],a[100:103],v[204:207],a[232:235]
v_mfma_f32_16x16x32_bf16 a[232:235],a[104:107],v[216:219],a[232:235]
v_mfma_f32_16x16x32_bf16 a[232:235],a[108:111],v[220:223],a[232:235]
v_mfma_f32_16x16x32_bf16 a[236:239],a[108:111],v[252:255],a[236:239]
buffer_load_dwordx4 v[68:71],v6,s[28:31],s76 offen offset:1024 sc1
v_mfma_f32_16x16x32_bf16 a[236:239],a[104:107],v[248:251],a[236:239]
ds_read_b64_tr_b16 a[104:105],v1 offset:20736
ds_read_b64_tr_b16 a[106:107],v1 offset:25856
v_mfma_f32_16x16x32_bf16 a[236:239],a[100:103],v[236:239],a[236:239]
ds_read_b64_tr_b16 a[100:101],v1 offset:10496
v_mfma_f32_16x16x32_bf16 a[236:239],a[96:99],v[232:235],a[236:239]
s_waitcnt lgkmcnt(3)
ds_read_b64_tr_b16 a[96:97],v1 offset:256
v_mfma_f32_16x16x32_bf16 a[240:243],a[112:115],v[136:139],a[240:243]
ds_read_b64_tr_b16 a[98:99],v1 offset:5376
v_mfma_f32_16x16x32_bf16 a[240:243],a[116:119],v[140:143],a[240:243]
buffer_load_dwordx4 v[72:75],v6,s[28:31],s76 offen offset:2048 sc1
v_mfma_f32_16x16x32_bf16 a[240:243],a[120:123],v[152:155],a[240:243]
ds_read_b64_tr_b16 a[102:103],v1 offset:15616
ds_read_b64_tr_b16 a[108:109],v1 offset:30976
v_mfma_f32_16x16x32_bf16 a[240:243],a[124:127],v[156:159],a[240:243]
ds_read_b64_tr_b16 a[110:111],v1 offset:36096
v_subrev_u32 v1,81920,v1
v_mfma_f32_16x16x32_bf16 a[244:247],a[124:127],v[188:191],a[244:247]
v_add_u32 v42,40960,v1
v_add_u32 v44,122880,v1
v_mfma_f32_16x16x32_bf16 a[244:247],a[120:123],v[184:187],a[244:247]
v_mfma_f32_16x16x32_bf16 a[244:247],a[116:119],v[172:175],a[244:247]
buffer_load_dwordx4 v[76:79],v6,s[28:31],s76 offen offset:3072 sc1
v_mfma_f32_16x16x32_bf16 a[244:247],a[112:115],v[168:171],a[244:247]
v_mfma_f32_16x16x32_bf16 a[248:251],a[112:115],v[200:203],a[248:251]
v_mfma_f32_16x16x32_bf16 a[248:251],a[116:119],v[204:207],a[248:251]
v_mfma_f32_16x16x32_bf16 a[248:251],a[120:123],v[216:219],a[248:251]
v_mfma_f32_16x16x32_bf16 a[248:251],a[124:127],v[220:223],a[248:251]
buffer_load_dwordx4 v[80:83],v6,s[28:31],s77 offen sc1
v_mfma_f32_16x16x32_bf16 a[252:255],a[124:127],v[252:255],a[252:255]
ds_read_b64_tr_b16 a[124:125],v2 offset:31008
ds_read_b64_tr_b16 a[126:127],v2 offset:36128
v_mfma_f32_16x16x32_bf16 a[252:255],a[120:123],v[248:251],a[252:255]
ds_read_b64_tr_b16 a[120:121],v2 offset:20768
v_mfma_f32_16x16x32_bf16 a[252:255],a[116:119],v[236:239],a[252:255]
ds_read_b64_tr_b16 a[116:117],v2 offset:10528
v_mfma_f32_16x16x32_bf16 a[252:255],a[112:115],v[232:235],a[252:255]
ds_read_b64_tr_b16 a[112:113],v2 offset:288
v_mfma_f32_16x16x32_bf16 a[0:3],a[128:131],v[136:139],a[0:3]
buffer_load_dwordx4 v[84:87],v6,s[28:31],s77 offen offset:1024 sc1
v_mfma_f32_16x16x32_bf16 a[0:3],a[132:135],v[140:143],a[0:3]
ds_read_b64_tr_b16 a[114:115],v2 offset:5408
ds_read_b64_tr_b16 a[118:119],v2 offset:15648
v_mfma_f32_16x16x32_bf16 a[0:3],a[136:139],v[152:155],a[0:3]
ds_read_b64_tr_b16 a[122:123],v2 offset:25888
v_subrev_u32 v2,81920,v2
v_mfma_f32_16x16x32_bf16 a[0:3],a[140:143],v[156:159],a[0:3]
v_add_u32 v43,40960,v2
v_add_u32 v45,122880,v2
v_mfma_f32_16x16x32_bf16 a[4:7],a[140:143],v[188:191],a[4:7]
v_mfma_f32_16x16x32_bf16 a[4:7],a[136:139],v[184:187],a[4:7]
buffer_load_dwordx4 v[88:91],v6,s[28:31],s77 offen offset:2048 sc1
v_mfma_f32_16x16x32_bf16 a[4:7],a[132:135],v[172:175],a[4:7]
v_mfma_f32_16x16x32_bf16 a[4:7],a[128:131],v[168:171],a[4:7]
v_mfma_f32_16x16x32_bf16 a[8:11],a[128:131],v[200:203],a[8:11]
v_mfma_f32_16x16x32_bf16 a[8:11],a[132:135],v[204:207],a[8:11]
v_mfma_f32_16x16x32_bf16 a[8:11],a[136:139],v[216:219],a[8:11]
buffer_load_dwordx4 v[92:95],v6,s[28:31],s77 offen offset:3072 sc1
v_mfma_f32_16x16x32_bf16 a[8:11],a[140:143],v[220:223],a[8:11]
v_mfma_f32_16x16x32_bf16 a[12:15],a[140:143],v[252:255],a[12:15]
ds_read_b64_tr_b16 a[140:141],v1 offset:30720
ds_read_b64_tr_b16 a[142:143],v1 offset:35840
v_mfma_f32_16x16x32_bf16 a[12:15],a[136:139],v[248:251],a[12:15]
ds_read_b64_tr_b16 a[136:137],v1 offset:20480
v_mfma_f32_16x16x32_bf16 a[12:15],a[132:135],v[236:239],a[12:15]
ds_read_b64_tr_b16 a[132:133],v1 offset:10240
v_mfma_f32_16x16x32_bf16 a[12:15],a[128:131],v[232:235],a[12:15]
buffer_load_dwordx4 v[96:99],v6,s[28:31],s78 offen sc1
v_mfma_f32_16x16x32_bf16 a[16:19],a[144:147],v[136:139],a[16:19]
ds_read_b64_tr_b16 a[128:129],v1
ds_read_b64_tr_b16 a[130:131],v1 offset:5120
v_mfma_f32_16x16x32_bf16 a[16:19],a[148:151],v[140:143],a[16:19]
ds_read_b64_tr_b16 a[134:135],v1 offset:15360
v_mfma_f32_16x16x32_bf16 a[16:19],a[152:155],v[152:155],a[16:19]
ds_read_b64_tr_b16 a[138:139],v1 offset:25600
v_mfma_f32_16x16x32_bf16 a[16:19],a[156:159],v[156:159],a[16:19]
v_mfma_f32_16x16x32_bf16 a[20:23],a[156:159],v[188:191],a[20:23]
buffer_load_dwordx4 v[100:103],v6,s[28:31],s78 offen offset:1024 sc1
v_mfma_f32_16x16x32_bf16 a[20:23],a[152:155],v[184:187],a[20:23]
v_mfma_f32_16x16x32_bf16 a[20:23],a[148:151],v[172:175],a[20:23]
v_mfma_f32_16x16x32_bf16 a[20:23],a[144:147],v[168:171],a[20:23]
v_mfma_f32_16x16x32_bf16 a[24:27],a[144:147],v[200:203],a[24:27]
v_mfma_f32_16x16x32_bf16 a[24:27],a[148:151],v[204:207],a[24:27]
buffer_load_dwordx4 v[104:107],v6,s[28:31],s78 offen offset:2048 sc1
v_mfma_f32_16x16x32_bf16 a[24:27],a[152:155],v[216:219],a[24:27]
v_mfma_f32_16x16x32_bf16 a[24:27],a[156:159],v[220:223],a[24:27]
v_mfma_f32_16x16x32_bf16 a[28:31],a[156:159],v[252:255],a[28:31]
ds_read_b64_tr_b16 a[156:157],v2 offset:30752
ds_read_b64_tr_b16 a[158:159],v2 offset:35872
v_mfma_f32_16x16x32_bf16 a[28:31],a[152:155],v[248:251],a[28:31]
ds_read_b64_tr_b16 a[152:153],v2 offset:20512
v_mfma_f32_16x16x32_bf16 a[28:31],a[148:151],v[236:239],a[28:31]
buffer_load_dwordx4 v[108:111],v6,s[28:31],s78 offen offset:3072 sc1
v_mfma_f32_16x16x32_bf16 a[28:31],a[144:147],v[232:235],a[28:31]
s_cmp_ge_u32 2,s71
s_cselect_b32 s80,7,5
s_mul_i32 s80,s80,163840
v_mfma_f32_16x16x32_bf16 a[32:35],a[160:163],v[136:139],a[32:35]
s_add_u32 s80,s80,s81
s_add_u32 s83,s80,20480
ds_read_b64_tr_b16 a[144:145],v2 offset:32
v_mfma_f32_16x16x32_bf16 a[32:35],a[164:167],v[140:143],a[32:35]
ds_read_b64_tr_b16 a[146:147],v2 offset:5152
ds_read_b64_tr_b16 a[148:149],v2 offset:10272
v_mfma_f32_16x16x32_bf16 a[32:35],a[168:171],v[152:155],a[32:35]
ds_read_b64_tr_b16 a[150:151],v2 offset:15392
v_mfma_f32_16x16x32_bf16 a[32:35],a[172:175],v[156:159],a[32:35]
buffer_load_dwordx4 v[112:115],v6,s[28:31],s79 offen sc1
v_mfma_f32_16x16x32_bf16 a[36:39],a[172:175],v[188:191],a[36:39]
ds_read_b64_tr_b16 a[154:155],v2 offset:25632
v_mfma_f32_16x16x32_bf16 a[36:39],a[168:171],v[184:187],a[36:39]
v_mfma_f32_16x16x32_bf16 a[36:39],a[164:167],v[172:175],a[36:39]
v_mfma_f32_16x16x32_bf16 a[36:39],a[160:163],v[168:171],a[36:39]
v_mfma_f32_16x16x32_bf16 a[40:43],a[160:163],v[200:203],a[40:43]
buffer_load_dwordx4 v[116:119],v6,s[28:31],s79 offen offset:1024 sc1
v_mfma_f32_16x16x32_bf16 a[40:43],a[164:167],v[204:207],a[40:43]
v_mfma_f32_16x16x32_bf16 a[40:43],a[168:171],v[216:219],a[40:43]
v_mfma_f32_16x16x32_bf16 a[40:43],a[172:175],v[220:223],a[40:43]
v_mfma_f32_16x16x32_bf16 a[44:47],a[172:175],v[252:255],a[44:47]
ds_read_b64_tr_b16 a[172:173],v1 offset:30784
ds_read_b64_tr_b16 a[174:175],v1 offset:35904
v_mfma_f32_16x16x32_bf16 a[44:47],a[168:171],v[248:251],a[44:47]
buffer_load_dwordx4 v[120:123],v6,s[28:31],s79 offen offset:2048 sc1
v_mfma_f32_16x16x32_bf16 a[44:47],a[164:167],v[236:239],a[44:47]
ds_read_b64_tr_b16 a[164:165],v1 offset:10304
ds_read_b64_tr_b16 a[166:167],v1 offset:15424
v_mfma_f32_16x16x32_bf16 a[44:47],a[160:163],v[232:235],a[44:47]
ds_read_b64_tr_b16 a[160:161],v1 offset:64
v_mfma_f32_16x16x32_bf16 a[48:51],a[176:179],v[136:139],a[48:51]
ds_read_b64_tr_b16 a[162:163],v1 offset:5184
v_mfma_f32_16x16x32_bf16 a[48:51],a[180:183],v[140:143],a[48:51]
ds_read_b64_tr_b16 a[168:169],v1 offset:20544
v_mfma_f32_16x16x32_bf16 a[48:51],a[184:187],v[152:155],a[48:51]
buffer_load_dwordx4 v[124:127],v6,s[28:31],s79 offen offset:3072 sc1
v_mfma_f32_16x16x32_bf16 a[48:51],a[188:191],v[156:159],a[48:51]
ds_read_b64_tr_b16 a[170:171],v1 offset:25664
v_mfma_f32_16x16x32_bf16 a[52:55],a[188:191],v[188:191],a[52:55]
s_add_u32 m0,s82,122880
v_mfma_f32_16x16x32_bf16 a[52:55],a[184:187],v[184:187],a[52:55]
v_mfma_f32_16x16x32_bf16 a[52:55],a[180:183],v[172:175],a[52:55]
v_mfma_f32_16x16x32_bf16 a[52:55],a[176:179],v[168:171],a[52:55]
buffer_load_dwordx4 v7,s[72:75],s80 offen lds
v_mfma_f32_16x16x32_bf16 a[56:59],a[176:179],v[200:203],a[56:59]
v_mfma_f32_16x16x32_bf16 a[56:59],a[180:183],v[204:207],a[56:59]
v_mfma_f32_16x16x32_bf16 a[56:59],a[184:187],v[216:219],a[56:59]
v_mfma_f32_16x16x32_bf16 a[56:59],a[188:191],v[220:223],a[56:59]
v_mfma_f32_16x16x32_bf16 a[60:63],a[188:191],v[252:255],a[60:63]
buffer_load_dwordx4 v8,s[72:75],s80 offen offset:1024 lds
v_mfma_f32_16x16x32_bf16 a[60:63],a[184:187],v[248:251],a[60:63]
ds_read_b64_tr_b16 a[184:185],v2 offset:20576
ds_read_b64_tr_b16 a[186:187],v2 offset:25696
v_mfma_f32_16x16x32_bf16 a[60:63],a[180:183],v[236:239],a[60:63]
ds_read_b64_tr_b16 a[180:181],v2 offset:10336
v_mfma_f32_16x16x32_bf16 a[60:63],a[176:179],v[232:235],a[60:63]
s_waitcnt lgkmcnt(3)
ds_read_b64_tr_b16 a[176:177],v2 offset:96
v_mfma_f32_16x16x32_bf16 a[64:67],a[96:99],v[136:139],a[64:67]
ds_read_b64_tr_b16 a[178:179],v2 offset:5216
v_mfma_f32_16x16x32_bf16 a[64:67],a[100:103],v[140:143],a[64:67]
buffer_load_dwordx4 v9,s[72:75],s80 offen offset:2048 lds
v_mfma_f32_16x16x32_bf16 a[64:67],a[104:107],v[152:155],a[64:67]
ds_read_b64_tr_b16 a[182:183],v2 offset:15456
ds_read_b64_tr_b16 a[188:189],v2 offset:30816
v_mfma_f32_16x16x32_bf16 a[64:67],a[108:111],v[156:159],a[64:67]
ds_read_b64_tr_b16 a[190:191],v2 offset:35936
v_mfma_f32_16x16x32_bf16 a[68:71],a[108:111],v[188:191],a[68:71]
v_mfma_f32_16x16x32_bf16 a[68:71],a[104:107],v[184:187],a[68:71]
v_mfma_f32_16x16x32_bf16 a[68:71],a[100:103],v[172:175],a[68:71]
buffer_load_dwordx4 v10,s[72:75],s80 offen offset:3072 lds
v_mfma_f32_16x16x32_bf16 a[68:71],a[96:99],v[168:171],a[68:71]
v_mfma_f32_16x16x32_bf16 a[72:75],a[96:99],v[200:203],a[72:75]
s_addk_i32 m0,4096
v_mfma_f32_16x16x32_bf16 a[72:75],a[100:103],v[204:207],a[72:75]
v_mfma_f32_16x16x32_bf16 a[72:75],a[104:107],v[216:219],a[72:75]
s_cmp_ge_u32 2,s71
s_cselect_b32 s76,7,5
v_mfma_f32_16x16x32_bf16 a[72:75],a[108:111],v[220:223],a[72:75]
buffer_load_dwordx4 v11,s[72:75],s80 offen lds
v_mfma_f32_16x16x32_bf16 a[76:79],a[108:111],v[252:255],a[76:79]
)A"
R"A(s_bitcmp1_b32 s54,s76
s_cselect_b32 s77,8192,0
s_lshl_b32 s76,s76,17
v_mfma_f32_16x16x32_bf16 a[76:79],a[104:107],v[248:251],a[76:79]
s_addk_i32 m0,1024
s_add_u32 s76,s76,s77
s_add_u32 s77,s76,4096
v_mfma_f32_16x16x32_bf16 a[76:79],a[100:103],v[236:239],a[76:79]
s_add_u32 s78,s76,16384
s_add_u32 s79,s76,20480
s_max_i32 s76,s76,s18
v_mfma_f32_16x16x32_bf16 a[76:79],a[96:99],v[232:235],a[76:79]
s_max_i32 s77,s77,s19
ds_read_b64_tr_b16 a[96:97],v1 offset:128
ds_read_b64_tr_b16 a[98:99],v1 offset:5248
v_mfma_f32_16x16x32_bf16 a[80:83],a[112:115],v[136:139],a[80:83]
s_max_i32 s78,s78,s100
buffer_load_dwordx4 v7,s[72:75],s83 offen lds
v_mfma_f32_16x16x32_bf16 a[80:83],a[116:119],v[140:143],a[80:83]
s_max_i32 s79,s79,s101
ds_read_b64_tr_b16 a[100:101],v1 offset:10368
ds_read_b64_tr_b16 a[102:103],v1 offset:15488
v_mfma_f32_16x16x32_bf16 a[80:83],a[120:123],v[152:155],a[80:83]
ds_read_b64_tr_b16 a[104:105],v1 offset:20608
v_mfma_f32_16x16x32_bf16 a[80:83],a[124:127],v[156:159],a[80:83]
ds_read_b64_tr_b16 a[106:107],v1 offset:25728
v_mfma_f32_16x16x32_bf16 a[84:87],a[124:127],v[188:191],a[84:87]
ds_read_b64_tr_b16 a[108:109],v1 offset:30848
v_mfma_f32_16x16x32_bf16 a[84:87],a[120:123],v[184:187],a[84:87]
buffer_load_dwordx4 v8,s[72:75],s83 offen offset:1024 lds
v_mfma_f32_16x16x32_bf16 a[84:87],a[116:119],v[172:175],a[84:87]
ds_read_b64_tr_b16 a[110:111],v1 offset:35968
v_mfma_f32_16x16x32_bf16 a[84:87],a[112:115],v[168:171],a[84:87]
v_mfma_f32_16x16x32_bf16 a[88:91],a[112:115],v[200:203],a[88:91]
v_mfma_f32_16x16x32_bf16 a[88:91],a[116:119],v[204:207],a[88:91]
v_mfma_f32_16x16x32_bf16 a[88:91],a[120:123],v[216:219],a[88:91]
buffer_load_dwordx4 v9,s[72:75],s83 offen offset:2048 lds
v_mfma_f32_16x16x32_bf16 a[88:91],a[124:127],v[220:223],a[88:91]
v_mfma_f32_16x16x32_bf16 a[92:95],a[124:127],v[252:255],a[92:95]
ds_read_b64_tr_b16 a[124:125],v2 offset:30880
ds_read_b64_tr_b16 a[126:127],v2 offset:36000
v_mfma_f32_16x16x32_bf16 a[92:95],a[120:123],v[248:251],a[92:95]
ds_read_b64_tr_b16 a[120:121],v2 offset:20640
v_mfma_f32_16x16x32_bf16 a[92:95],a[116:119],v[236:239],a[92:95]
ds_read_b64_tr_b16 a[116:117],v2 offset:10400
v_mfma_f32_16x16x32_bf16 a[92:95],a[112:115],v[232:235],a[92:95]
buffer_load_dwordx4 v10,s[72:75],s83 offen offset:3072 lds
v_mfma_f32_16x16x32_bf16 a[192:195],a[128:131],v[128:131],a[192:195]
ds_read_b64_tr_b16 a[112:113],v2 offset:160
ds_read_b64_tr_b16 a[114:115],v2 offset:5280
v_mfma_f32_16x16x32_bf16 a[192:195],a[132:135],v[160:163],a[192:195]
s_addk_i32 m0,4096
ds_read_b64_tr_b16 a[118:119],v2 offset:15520
v_mfma_f32_16x16x32_bf16 a[192:195],a[136:139],v[192:195],a[192:195]
ds_read_b64_tr_b16 a[122:123],v2 offset:25760
v_mfma_f32_16x16x32_bf16 a[192:195],a[140:143],v[224:227],a[192:195]
buffer_load_dwordx4 v[136:139],v6,s[28:31],s76 offen sc1
v_mfma_f32_16x16x32_bf16 a[196:199],a[140:143],v[228:231],a[196:199]
v_mfma_f32_16x16x32_bf16 a[196:199],a[136:139],v[196:199],a[196:199]
v_mfma_f32_16x16x32_bf16 a[196:199],a[132:135],v[164:167],a[196:199]
v_mfma_f32_16x16x32_bf16 a[196:199],a[128:131],v[132:135],a[196:199]
s_waitcnt vmcnt(14)
buffer_load_dwordx4 v11,s[72:75],s83 offen lds
v_mfma_f32_16x16x32_bf16 a[200:203],a[128:131],v[144:147],a[200:203]
ds_read_b64_tr_b16 a[128:129],v1 offset:192
ds_read_b64_tr_b16 a[130:131],v1 offset:5312
v_mfma_f32_16x16x32_bf16 a[200:203],a[132:135],v[176:179],a[200:203]
ds_read_b64_tr_b16 a[132:133],v1 offset:10432
v_mfma_f32_16x16x32_bf16 a[200:203],a[136:139],v[208:211],a[200:203]
ds_read_b64_tr_b16 a[134:135],v1 offset:15552
v_mfma_f32_16x16x32_bf16 a[200:203],a[140:143],v[240:243],a[200:203]
buffer_load_dwordx4 v[140:143],v6,s[28:31],s76 offen offset:1024 sc1
v_mfma_f32_16x16x32_bf16 a[208:211],a[144:147],v[128:131],a[208:211]
ds_read_b64_tr_b16 a[136:137],v1 offset:20672
ds_read_b64_tr_b16 a[138:139],v1 offset:25792
v_mfma_f32_16x16x32_bf16 a[208:211],a[148:151],v[160:163],a[208:211]
ds_read_b64_tr_b16 a[140:141],v1 offset:30912
v_mfma_f32_16x16x32_bf16 a[208:211],a[152:155],v[192:195],a[208:211]
ds_read_b64_tr_b16 a[142:143],v1 offset:36032
v_mfma_f32_16x16x32_bf16 a[208:211],a[156:159],v[224:227],a[208:211]
buffer_load_dwordx4 v[152:155],v6,s[28:31],s76 offen offset:2048 sc1
v_mfma_f32_16x16x32_bf16 a[212:215],a[156:159],v[228:231],a[212:215]
v_mfma_f32_16x16x32_bf16 a[212:215],a[152:155],v[196:199],a[212:215]
v_mfma_f32_16x16x32_bf16 a[212:215],a[148:151],v[164:167],a[212:215]
v_mfma_f32_16x16x32_bf16 a[212:215],a[144:147],v[132:135],a[212:215]
buffer_load_dwordx4 v[156:159],v6,s[28:31],s76 offen offset:3072 sc1
v_mfma_f32_16x16x32_bf16 a[216:219],a[144:147],v[144:147],a[216:219]
s_add_u32 s76,s70,3
ds_read_b64_tr_b16 a[144:145],v2 offset:224
ds_read_b64_tr_b16 a[146:147],v2 offset:5344
v_mfma_f32_16x16x32_bf16 a[216:219],a[148:151],v[176:179],a[216:219]
s_and_b32 s76,s76,3
s_lshl_b32 s76,s76,21
ds_read_b64_tr_b16 a[148:149],v2 offset:10464
v_mfma_f32_16x16x32_bf16 a[216:219],a[152:155],v[208:211],a[216:219]
s_add_u32 s76,s76,0
s_max_i32 s76,s76,s101
ds_read_b64_tr_b16 a[150:151],v2 offset:15584
v_mfma_f32_16x16x32_bf16 a[216:219],a[156:159],v[240:243],a[216:219]
buffer_load_dwordx4 v[168:171],v6,s[28:31],s77 offen sc1
v_mfma_f32_16x16x32_bf16 a[224:227],a[160:163],v[128:131],a[224:227]
ds_read_b64_tr_b16 a[152:153],v2 offset:20704
ds_read_b64_tr_b16 a[154:155],v2 offset:25824
v_mfma_f32_16x16x32_bf16 a[224:227],a[164:167],v[160:163],a[224:227]
ds_read_b64_tr_b16 a[156:157],v2 offset:30944
v_mfma_f32_16x16x32_bf16 a[224:227],a[168:171],v[192:195],a[224:227]
ds_read_b64_tr_b16 a[158:159],v2 offset:36064
v_mfma_f32_16x16x32_bf16 a[224:227],a[172:175],v[224:227],a[224:227]
buffer_load_dwordx4 v[172:175],v6,s[28:31],s77 offen offset:1024 sc1
v_mfma_f32_16x16x32_bf16 a[228:231],a[172:175],v[228:231],a[228:231]
v_mfma_f32_16x16x32_bf16 a[228:231],a[168:171],v[196:199],a[228:231]
v_mfma_f32_16x16x32_bf16 a[228:231],a[164:167],v[164:167],a[228:231]
v_mfma_f32_16x16x32_bf16 a[228:231],a[160:163],v[132:135],a[228:231]
v_mfma_f32_16x16x32_bf16 a[232:235],a[160:163],v[144:147],a[232:235]
buffer_load_dwordx4 v[184:187],v6,s[28:31],s77 offen offset:2048 sc1
v_mfma_f32_16x16x32_bf16 a[232:235],a[164:167],v[176:179],a[232:235]
ds_read_b64_tr_b16 a[160:161],v1 offset:256
ds_read_b64_tr_b16 a[162:163],v1 offset:5376
v_mfma_f32_16x16x32_bf16 a[232:235],a[168:171],v[208:211],a[232:235]
ds_read_b64_tr_b16 a[164:165],v1 offset:10496
v_mfma_f32_16x16x32_bf16 a[232:235],a[172:175],v[240:243],a[232:235]
s_waitcnt lgkmcnt(3)
ds_read_b64_tr_b16 a[166:167],v1 offset:15616
v_mfma_f32_16x16x32_bf16 a[240:243],a[176:179],v[128:131],a[240:243]
ds_read_b64_tr_b16 a[168:169],v1 offset:20736
v_mfma_f32_16x16x32_bf16 a[240:243],a[180:183],v[160:163],a[240:243]
buffer_load_dwordx4 v[188:191],v6,s[28:31],s77 offen offset:3072 sc1
v_mfma_f32_16x16x32_bf16 a[240:243],a[184:187],v[192:195],a[240:243]
s_add_u32 s77,s70,2
ds_read_b64_tr_b16 a[170:171],v1 offset:25856
ds_read_b64_tr_b16 a[172:173],v1 offset:30976
v_mfma_f32_16x16x32_bf16 a[240:243],a[188:191],v[224:227],a[240:243]
s_and_b32 s77,s77,3
s_lshl_b32 s77,s77,21
ds_read_b64_tr_b16 a[174:175],v1 offset:36096
v_mfma_f32_16x16x32_bf16 a[244:247],a[188:191],v[228:231],a[244:247]
s_add_u32 s77,s77,131072
s_max_i32 s77,s77,s101
v_mfma_f32_16x16x32_bf16 a[244:247],a[184:187],v[196:199],a[244:247]
v_mfma_f32_16x16x32_bf16 a[244:247],a[180:183],v[164:167],a[244:247]
buffer_load_dwordx4 v[200:203],v6,s[28:31],s78 offen sc1
v_mfma_f32_16x16x32_bf16 a[244:247],a[176:179],v[132:135],a[244:247]
v_mfma_f32_16x16x32_bf16 a[248:251],a[176:179],v[144:147],a[248:251]
ds_read_b64_tr_b16 a[176:177],v2 offset:288
ds_read_b64_tr_b16 a[178:179],v2 offset:5408
v_mfma_f32_16x16x32_bf16 a[248:251],a[180:183],v[176:179],a[248:251]
ds_read_b64_tr_b16 a[180:181],v2 offset:10528
v_mfma_f32_16x16x32_bf16 a[248:251],a[184:187],v[208:211],a[248:251]
ds_read_b64_tr_b16 a[182:183],v2 offset:15648
v_mfma_f32_16x16x32_bf16 a[248:251],a[188:191],v[240:243],a[248:251]
buffer_load_dwordx4 v[204:207],v6,s[28:31],s78 offen offset:1024 sc1
v_mfma_f32_16x16x32_bf16 a[0:3],a[96:99],v[128:131],a[0:3]
ds_read_b64_tr_b16 a[184:185],v2 offset:20768
ds_read_b64_tr_b16 a[186:187],v2 offset:25888
v_mfma_f32_16x16x32_bf16 a[0:3],a[100:103],v[160:163],a[0:3]
ds_read_b64_tr_b16 a[188:189],v2 offset:31008
v_mfma_f32_16x16x32_bf16 a[0:3],a[104:107],v[192:195],a[0:3]
ds_read_b64_tr_b16 a[190:191],v2 offset:36128
v_mfma_f32_16x16x32_bf16 a[0:3],a[108:111],v[224:227],a[0:3]
v_mfma_f32_16x16x32_bf16 a[4:7],a[108:111],v[228:231],a[4:7]
buffer_load_dwordx4 v[216:219],v6,s[28:31],s78 offen offset:2048 sc1
v_mfma_f32_16x16x32_bf16 a[4:7],a[104:107],v[196:199],a[4:7]
v_mfma_f32_16x16x32_bf16 a[4:7],a[100:103],v[164:167],a[4:7]
v_mfma_f32_16x16x32_bf16 a[4:7],a[96:99],v[132:135],a[4:7]
v_mfma_f32_16x16x32_bf16 a[8:11],a[96:99],v[144:147],a[8:11]
v_mfma_f32_16x16x32_bf16 a[8:11],a[100:103],v[176:179],a[8:11]
buffer_load_dwordx4 v[220:223],v6,s[28:31],s78 offen offset:3072 sc1
v_mfma_f32_16x16x32_bf16 a[8:11],a[104:107],v[208:211],a[8:11]
s_add_u32 s78,s70,1
s_and_b32 s78,s78,3
s_lshl_b32 s78,s78,21
v_mfma_f32_16x16x32_bf16 a[8:11],a[108:111],v[240:243],a[8:11]
s_add_u32 s78,s78,262144
s_max_i32 s78,s78,s101
v_mfma_f32_16x16x32_bf16 a[16:19],a[112:115],v[128:131],a[16:19]
v_mfma_f32_16x16x32_bf16 a[16:19],a[116:119],v[160:163],a[16:19]
v_mfma_f32_16x16x32_bf16 a[16:19],a[120:123],v[192:195],a[16:19]
buffer_load_dwordx4 v[232:235],v6,s[28:31],s79 offen sc1
v_mfma_f32_16x16x32_bf16 a[16:19],a[124:127],v[224:227],a[16:19]
v_mfma_f32_16x16x32_bf16 a[20:23],a[124:127],v[228:231],a[20:23]
v_mfma_f32_16x16x32_bf16 a[20:23],a[120:123],v[196:199],a[20:23]
v_mfma_f32_16x16x32_bf16 a[20:23],a[116:119],v[164:167],a[20:23]
v_mfma_f32_16x16x32_bf16 a[20:23],a[112:115],v[132:135],a[20:23]
buffer_load_dwordx4 v[236:239],v6,s[28:31],s79 offen offset:1024 sc1
v_mfma_f32_16x16x32_bf16 a[24:27],a[112:115],v[144:147],a[24:27]
v_mfma_f32_16x16x32_bf16 a[24:27],a[116:119],v[176:179],a[24:27]
v_mfma_f32_16x16x32_bf16 a[24:27],a[120:123],v[208:211],a[24:27]
v_mfma_f32_16x16x32_bf16 a[24:27],a[124:127],v[240:243],a[24:27]
v_mfma_f32_16x16x32_bf16 a[32:35],a[128:131],v[128:131],a[32:35]
buffer_load_dwordx4 v[248:251],v6,s[28:31],s79 offen offset:2048 sc1
v_mfma_f32_16x16x32_bf16 a[32:35],a[132:135],v[160:163],a[32:35]
v_mfma_f32_16x16x32_bf16 a[32:35],a[136:139],v[192:195],a[32:35]
v_mfma_f32_16x16x32_bf16 a[32:35],a[140:143],v[224:227],a[32:35]
v_mfma_f32_16x16x32_bf16 a[36:39],a[140:143],v[228:231],a[36:39]
v_mfma_f32_16x16x32_bf16 a[36:39],a[136:139],v[196:199],a[36:39]
buffer_load_dwordx4 v[252:255],v6,s[28:31],s79 offen offset:3072 sc1
v_mfma_f32_16x16x32_bf16 a[36:39],a[132:135],v[164:167],a[36:39]
v_mfma_f32_16x16x32_bf16 a[36:39],a[128:131],v[132:135],a[36:39]
v_mfma_f32_16x16x32_bf16 a[40:43],a[128:131],v[144:147],a[40:43]
v_mfma_f32_16x16x32_bf16 a[40:43],a[132:135],v[176:179],a[40:43]
v_mfma_f32_16x16x32_bf16 a[40:43],a[136:139],v[208:211],a[40:43]
s_waitcnt vmcnt(15)
v_mfma_f32_16x16x32_bf16 a[40:43],a[140:143],v[240:243],a[40:43]
s_barrier
ds_read_b64_tr_b16 a[96:97],v42
ds_read_b64_tr_b16 a[98:99],v42 offset:5120
v_mfma_f32_16x16x32_bf16 a[48:51],a[144:147],v[128:131],a[48:51]
ds_read_b64_tr_b16 a[100:101],v42 offset:10240
v_mfma_f32_16x16x32_bf16 a[48:51],a[148:151],v[160:163],a[48:51]
ds_read_b64_tr_b16 a[102:103],v42 offset:15360
v_mfma_f32_16x16x32_bf16 a[48:51],a[152:155],v[192:195],a[48:51]
ds_read_b64_tr_b16 a[104:105],v42 offset:20480
v_mfma_f32_16x16x32_bf16 a[48:51],a[156:159],v[224:227],a[48:51]
ds_read_b64_tr_b16 a[106:107],v42 offset:25600
ds_read_b64_tr_b16 a[108:109],v42 offset:30720
v_mfma_f32_16x16x32_bf16 a[52:55],a[156:159],v[228:231],a[52:55]
ds_read_b64_tr_b16 a[110:111],v42 offset:35840
v_mfma_f32_16x16x32_bf16 a[52:55],a[152:155],v[196:199],a[52:55]
ds_read_b64_tr_b16 a[112:113],v44
v_mfma_f32_16x16x32_bf16 a[52:55],a[148:151],v[164:167],a[52:55]
ds_read_b64_tr_b16 a[114:115],v44 offset:5120
v_mfma_f32_16x16x32_bf16 a[52:55],a[144:147],v[132:135],a[52:55]
ds_read_b64_tr_b16 a[116:117],v44 offset:10240
)A"
R"A(ds_read_b64_tr_b16 a[118:119],v44 offset:15360
v_mfma_f32_16x16x32_bf16 a[56:59],a[144:147],v[144:147],a[56:59]
ds_read_b64_tr_b16 a[120:121],v44 offset:20480
v_mfma_f32_16x16x32_bf16 a[56:59],a[148:151],v[176:179],a[56:59]
ds_read_b64_tr_b16 a[122:123],v44 offset:25600
v_mfma_f32_16x16x32_bf16 a[56:59],a[152:155],v[208:211],a[56:59]
ds_read_b64_tr_b16 a[124:125],v44 offset:30720
v_mfma_f32_16x16x32_bf16 a[56:59],a[156:159],v[240:243],a[56:59]
s_waitcnt lgkmcnt(8)
ds_read_b64_tr_b16 a[126:127],v44 offset:35840
ds_read_b64_tr_b16 a[128:129],v43 offset:32
v_mfma_f32_16x16x32_bf16 a[64:67],a[160:163],v[128:131],a[64:67]
ds_read_b64_tr_b16 a[130:131],v43 offset:5152
v_mfma_f32_16x16x32_bf16 a[64:67],a[164:167],v[160:163],a[64:67]
ds_read_b64_tr_b16 a[132:133],v43 offset:10272
v_mfma_f32_16x16x32_bf16 a[64:67],a[168:171],v[192:195],a[64:67]
ds_read_b64_tr_b16 a[134:135],v43 offset:15392
v_mfma_f32_16x16x32_bf16 a[64:67],a[172:175],v[224:227],a[64:67]
ds_read_b64_tr_b16 a[136:137],v43 offset:20512
ds_read_b64_tr_b16 a[138:139],v43 offset:25632
v_mfma_f32_16x16x32_bf16 a[68:71],a[172:175],v[228:231],a[68:71]
ds_read_b64_tr_b16 a[140:141],v43 offset:30752
v_mfma_f32_16x16x32_bf16 a[68:71],a[168:171],v[196:199],a[68:71]
ds_read_b64_tr_b16 a[142:143],v43 offset:35872
v_mfma_f32_16x16x32_bf16 a[68:71],a[164:167],v[164:167],a[68:71]
ds_read_b64_tr_b16 a[144:145],v45 offset:32
v_mfma_f32_16x16x32_bf16 a[68:71],a[160:163],v[132:135],a[68:71]
ds_read_b64_tr_b16 a[146:147],v45 offset:5152
ds_read_b64_tr_b16 a[148:149],v45 offset:10272
v_mfma_f32_16x16x32_bf16 a[72:75],a[160:163],v[144:147],a[72:75]
ds_read_b64_tr_b16 a[150:151],v45 offset:15392
v_mfma_f32_16x16x32_bf16 a[72:75],a[164:167],v[176:179],a[72:75]
ds_read_b64_tr_b16 a[152:153],v45 offset:20512
v_mfma_f32_16x16x32_bf16 a[72:75],a[168:171],v[208:211],a[72:75]
ds_read_b64_tr_b16 a[154:155],v45 offset:25632
v_mfma_f32_16x16x32_bf16 a[72:75],a[172:175],v[240:243],a[72:75]
ds_read_b64_tr_b16 a[156:157],v45 offset:30752
ds_read_b64_tr_b16 a[158:159],v45 offset:35872
v_mfma_f32_16x16x32_bf16 a[80:83],a[176:179],v[128:131],a[80:83]
ds_read_b64_tr_b16 a[160:161],v42 offset:64
v_mfma_f32_16x16x32_bf16 a[80:83],a[180:183],v[160:163],a[80:83]
ds_read_b64_tr_b16 a[162:163],v42 offset:5184
v_mfma_f32_16x16x32_bf16 a[80:83],a[184:187],v[192:195],a[80:83]
ds_read_b64_tr_b16 a[164:165],v42 offset:10304
v_mfma_f32_16x16x32_bf16 a[80:83],a[188:191],v[224:227],a[80:83]
ds_read_b64_tr_b16 a[166:167],v42 offset:15424
ds_read_b64_tr_b16 a[168:169],v42 offset:20544
v_mfma_f32_16x16x32_bf16 a[84:87],a[188:191],v[228:231],a[84:87]
ds_read_b64_tr_b16 a[170:171],v42 offset:25664
v_mfma_f32_16x16x32_bf16 a[84:87],a[184:187],v[196:199],a[84:87]
ds_read_b64_tr_b16 a[172:173],v42 offset:30784
v_mfma_f32_16x16x32_bf16 a[84:87],a[180:183],v[164:167],a[84:87]
ds_read_b64_tr_b16 a[174:175],v42 offset:35904
v_mfma_f32_16x16x32_bf16 a[84:87],a[176:179],v[132:135],a[84:87]
v_mfma_f32_16x16x32_bf16 a[88:91],a[176:179],v[144:147],a[88:91]
ds_read_b64_tr_b16 a[176:177],v44 offset:64
ds_read_b64_tr_b16 a[178:179],v44 offset:5184
v_mfma_f32_16x16x32_bf16 a[88:91],a[180:183],v[176:179],a[88:91]
ds_read_b64_tr_b16 a[180:181],v44 offset:10304
v_mfma_f32_16x16x32_bf16 a[88:91],a[184:187],v[208:211],a[88:91]
ds_read_b64_tr_b16 a[182:183],v44 offset:15424
v_mfma_f32_16x16x32_bf16 a[88:91],a[188:191],v[240:243],a[88:91]
ds_read_b64_tr_b16 a[184:185],v44 offset:20544
v_mfma_f32_16x16x32_bf16 a[192:195],a[96:99],v[64:67],a[192:195]
ds_read_b64_tr_b16 a[186:187],v44 offset:25664
ds_read_b64_tr_b16 a[188:189],v44 offset:30784
v_mfma_f32_16x16x32_bf16 a[192:195],a[100:103],v[68:71],a[192:195]
ds_read_b64_tr_b16 a[190:191],v44 offset:35904
v_mfma_f32_16x16x32_bf16 a[192:195],a[104:107],v[72:75],a[192:195]
s_waitcnt lgkmcnt(8)
v_mfma_f32_16x16x32_bf16 a[192:195],a[108:111],v[76:79],a[192:195]
v_mfma_f32_16x16x32_bf16 a[196:199],a[108:111],v[92:95],a[196:199]
v_mfma_f32_16x16x32_bf16 a[196:199],a[104:107],v[88:91],a[196:199]
v_mfma_f32_16x16x32_bf16 a[196:199],a[100:103],v[84:87],a[196:199]
v_mfma_f32_16x16x32_bf16 a[196:199],a[96:99],v[80:83],a[196:199]
v_mfma_f32_16x16x32_bf16 a[200:203],a[96:99],v[96:99],a[200:203]
ds_read_b64_tr_b16 a[96:97],v43 offset:96
ds_read_b64_tr_b16 a[98:99],v43 offset:5216
v_mfma_f32_16x16x32_bf16 a[200:203],a[100:103],v[100:103],a[200:203]
ds_read_b64_tr_b16 a[100:101],v43 offset:10336
v_mfma_f32_16x16x32_bf16 a[200:203],a[104:107],v[104:107],a[200:203]
ds_read_b64_tr_b16 a[102:103],v43 offset:15456
v_mfma_f32_16x16x32_bf16 a[200:203],a[108:111],v[108:111],a[200:203]
ds_read_b64_tr_b16 a[104:105],v43 offset:20576
v_mfma_f32_16x16x32_bf16 a[192:195],a[112:115],v[136:139],a[192:195]
s_waitcnt vmcnt(3)
ds_read_b64_tr_b16 a[106:107],v43 offset:25696
ds_read_b64_tr_b16 a[108:109],v43 offset:30816
v_mfma_f32_16x16x32_bf16 a[192:195],a[116:119],v[140:143],a[192:195]
ds_read_b64_tr_b16 a[110:111],v43 offset:35936
v_mfma_f32_16x16x32_bf16 a[192:195],a[120:123],v[152:155],a[192:195]
v_mfma_f32_16x16x32_bf16 a[192:195],a[124:127],v[156:159],a[192:195]
v_mfma_f32_16x16x32_bf16 a[196:199],a[124:127],v[188:191],a[196:199]
v_mfma_f32_16x16x32_bf16 a[196:199],a[120:123],v[184:187],a[196:199]
v_mfma_f32_16x16x32_bf16 a[196:199],a[116:119],v[172:175],a[196:199]
v_mfma_f32_16x16x32_bf16 a[196:199],a[112:115],v[168:171],a[196:199]
v_mfma_f32_16x16x32_bf16 a[200:203],a[112:115],v[200:203],a[200:203]
ds_read_b64_tr_b16 a[112:113],v45 offset:96
ds_read_b64_tr_b16 a[114:115],v45 offset:5216
v_mfma_f32_16x16x32_bf16 a[200:203],a[116:119],v[204:207],a[200:203]
v_accvgpr_read_b32 v128,a192
v_accvgpr_read_b32 v129,a193
v_mfma_f32_16x16x32_bf16 a[200:203],a[120:123],v[216:219],a[200:203]
v_cvt_pk_bf16_f32 v128,v128,v129
v_accvgpr_read_b32 v130,a194
v_mfma_f32_16x16x32_bf16 a[200:203],a[124:127],v[220:223],a[200:203]
v_accvgpr_read_b32 v131,a195
v_cvt_pk_bf16_f32 v129,v130,v131
v_mfma_f32_16x16x32_bf16 a[208:211],a[128:131],v[64:67],a[208:211]
ds_read_b64_tr_b16 a[116:117],v45 offset:10336
ds_read_b64_tr_b16 a[118:119],v45 offset:15456
v_mfma_f32_16x16x32_bf16 a[208:211],a[132:135],v[68:71],a[208:211]
v_accvgpr_read_b32 v132,a196
v_accvgpr_read_b32 v133,a197
v_mfma_f32_16x16x32_bf16 a[208:211],a[136:139],v[72:75],a[208:211]
v_cvt_pk_bf16_f32 v132,v132,v133
v_accvgpr_read_b32 v134,a198
v_mfma_f32_16x16x32_bf16 a[208:211],a[140:143],v[76:79],a[208:211]
v_accvgpr_read_b32 v135,a199
v_cvt_pk_bf16_f32 v133,v134,v135
v_mfma_f32_16x16x32_bf16 a[212:215],a[140:143],v[92:95],a[212:215]
ds_read_b64_tr_b16 a[120:121],v45 offset:20576
ds_read_b64_tr_b16 a[122:123],v45 offset:25696
v_mfma_f32_16x16x32_bf16 a[212:215],a[136:139],v[88:91],a[212:215]
ds_read_b64_tr_b16 a[124:125],v45 offset:30816
v_accvgpr_read_b32 v144,a200
v_mfma_f32_16x16x32_bf16 a[212:215],a[132:135],v[84:87],a[212:215]
ds_read_b64_tr_b16 a[126:127],v45 offset:35936
v_accvgpr_read_b32 v145,a201
v_mfma_f32_16x16x32_bf16 a[212:215],a[128:131],v[80:83],a[212:215]
v_cvt_pk_bf16_f32 v144,v144,v145
v_accvgpr_read_b32 v146,a202
v_mfma_f32_16x16x32_bf16 a[216:219],a[128:131],v[96:99],a[216:219]
v_accvgpr_read_b32 v147,a203
v_cvt_pk_bf16_f32 v145,v146,v147
v_mfma_f32_16x16x32_bf16 a[216:219],a[132:135],v[100:103],a[216:219]
ds_read_b64_tr_b16 a[128:129],v42 offset:128
ds_read_b64_tr_b16 a[130:131],v42 offset:5248
v_mfma_f32_16x16x32_bf16 a[216:219],a[136:139],v[104:107],a[216:219]
ds_read_b64_tr_b16 a[132:133],v42 offset:10368
v_mfma_f32_16x16x32_bf16 a[216:219],a[140:143],v[108:111],a[216:219]
ds_read_b64_tr_b16 a[134:135],v42 offset:15488
v_mfma_f32_16x16x32_bf16 a[208:211],a[144:147],v[136:139],a[208:211]
ds_read_b64_tr_b16 a[136:137],v42 offset:20608
v_mfma_f32_16x16x32_bf16 a[208:211],a[148:151],v[140:143],a[208:211]
ds_read_b64_tr_b16 a[138:139],v42 offset:25728
ds_read_b64_tr_b16 a[140:141],v42 offset:30848
v_mfma_f32_16x16x32_bf16 a[208:211],a[152:155],v[152:155],a[208:211]
ds_read_b64_tr_b16 a[142:143],v42 offset:35968
v_mfma_f32_16x16x32_bf16 a[208:211],a[156:159],v[156:159],a[208:211]
v_mfma_f32_16x16x32_bf16 a[212:215],a[156:159],v[188:191],a[212:215]
v_mfma_f32_16x16x32_bf16 a[212:215],a[152:155],v[184:187],a[212:215]
v_mfma_f32_16x16x32_bf16 a[212:215],a[148:151],v[172:175],a[212:215]
v_mfma_f32_16x16x32_bf16 a[212:215],a[144:147],v[168:171],a[212:215]
v_mfma_f32_16x16x32_bf16 a[216:219],a[144:147],v[200:203],a[216:219]
ds_read_b64_tr_b16 a[144:145],v44 offset:128
ds_read_b64_tr_b16 a[146:147],v44 offset:5248
v_mfma_f32_16x16x32_bf16 a[216:219],a[148:151],v[204:207],a[216:219]
v_accvgpr_read_b32 v130,a208
v_accvgpr_read_b32 v131,a209
v_mfma_f32_16x16x32_bf16 a[216:219],a[152:155],v[216:219],a[216:219]
v_accvgpr_read_b32 v16,a210
v_accvgpr_read_b32 v17,a211
v_mfma_f32_16x16x32_bf16 a[216:219],a[156:159],v[220:223],a[216:219]
v_cvt_pk_bf16_f32 v130,v130,v131
v_cvt_pk_bf16_f32 v131,v16,v17
v_mfma_f32_16x16x32_bf16 a[224:227],a[160:163],v[64:67],a[224:227]
buffer_store_dwordx4 v[128:131],v6,s[32:35],s84 offen sc1
v_mfma_f32_16x16x32_bf16 a[224:227],a[164:167],v[68:71],a[224:227]
v_accvgpr_read_b32 v134,a212
v_accvgpr_read_b32 v135,a213
v_mfma_f32_16x16x32_bf16 a[224:227],a[168:171],v[72:75],a[224:227]
v_accvgpr_read_b32 v12,a214
v_accvgpr_read_b32 v13,a215
v_mfma_f32_16x16x32_bf16 a[224:227],a[172:175],v[76:79],a[224:227]
v_cvt_pk_bf16_f32 v134,v134,v135
v_cvt_pk_bf16_f32 v135,v12,v13
v_mfma_f32_16x16x32_bf16 a[228:231],a[172:175],v[92:95],a[228:231]
buffer_store_dwordx4 v[132:135],v6,s[32:35],s85 offen sc1
v_mfma_f32_16x16x32_bf16 a[228:231],a[168:171],v[88:91],a[228:231]
v_accvgpr_read_b32 v146,a216
v_accvgpr_read_b32 v147,a217
v_mfma_f32_16x16x32_bf16 a[228:231],a[164:167],v[84:87],a[228:231]
v_accvgpr_read_b32 v14,a218
v_accvgpr_read_b32 v15,a219
v_mfma_f32_16x16x32_bf16 a[228:231],a[160:163],v[80:83],a[228:231]
v_cvt_pk_bf16_f32 v146,v146,v147
v_cvt_pk_bf16_f32 v147,v14,v15
v_mfma_f32_16x16x32_bf16 a[232:235],a[160:163],v[96:99],a[232:235]
buffer_store_dwordx4 v[144:147],v6,s[32:35],s86 offen sc1
v_mfma_f32_16x16x32_bf16 a[232:235],a[164:167],v[100:103],a[232:235]
ds_read_b64_tr_b16 a[148:149],v44 offset:10368
ds_read_b64_tr_b16 a[150:151],v44 offset:15488
v_mfma_f32_16x16x32_bf16 a[232:235],a[168:171],v[104:107],a[232:235]
ds_read_b64_tr_b16 a[152:153],v44 offset:20608
v_mfma_f32_16x16x32_bf16 a[232:235],a[172:175],v[108:111],a[232:235]
s_waitcnt lgkmcnt(3)
ds_read_b64_tr_b16 a[154:155],v44 offset:25728
v_mfma_f32_16x16x32_bf16 a[224:227],a[176:179],v[136:139],a[224:227]
ds_read_b64_tr_b16 a[156:157],v44 offset:30848
v_mfma_f32_16x16x32_bf16 a[224:227],a[180:183],v[140:143],a[224:227]
ds_read_b64_tr_b16 a[158:159],v44 offset:35968
ds_read_b64_tr_b16 a[160:161],v43 offset:160
v_mfma_f32_16x16x32_bf16 a[224:227],a[184:187],v[152:155],a[224:227]
ds_read_b64_tr_b16 a[162:163],v43 offset:5280
v_mfma_f32_16x16x32_bf16 a[224:227],a[188:191],v[156:159],a[224:227]
ds_read_b64_tr_b16 a[164:165],v43 offset:10400
v_mfma_f32_16x16x32_bf16 a[228:231],a[188:191],v[188:191],a[228:231]
ds_read_b64_tr_b16 a[166:167],v43 offset:15520
v_mfma_f32_16x16x32_bf16 a[228:231],a[184:187],v[184:187],a[228:231]
ds_read_b64_tr_b16 a[168:169],v43 offset:20640
ds_read_b64_tr_b16 a[170:171],v43 offset:25760
v_mfma_f32_16x16x32_bf16 a[228:231],a[180:183],v[172:175],a[228:231]
ds_read_b64_tr_b16 a[172:173],v43 offset:30880
v_mfma_f32_16x16x32_bf16 a[228:231],a[176:179],v[168:171],a[228:231]
ds_read_b64_tr_b16 a[174:175],v43 offset:36000
v_mfma_f32_16x16x32_bf16 a[232:235],a[176:179],v[200:203],a[232:235]
ds_read_b64_tr_b16 a[176:177],v45 offset:160
v_mfma_f32_16x16x32_bf16 a[232:235],a[180:183],v[204:207],a[232:235]
v_accvgpr_read_b32 v160,a224
v_accvgpr_read_b32 v161,a225
v_mfma_f32_16x16x32_bf16 a[232:235],a[184:187],v[216:219],a[232:235]
v_cvt_pk_bf16_f32 v160,v160,v161
v_accvgpr_read_b32 v162,a226
v_mfma_f32_16x16x32_bf16 a[232:235],a[188:191],v[220:223],a[232:235]
v_accvgpr_read_b32 v163,a227
v_cvt_pk_bf16_f32 v161,v162,v163
v_mfma_f32_16x16x32_bf16 a[240:243],a[96:99],v[64:67],a[240:243]
ds_read_b64_tr_b16 a[178:179],v45 offset:5280
ds_read_b64_tr_b16 a[180:181],v45 offset:10400
)A"
R"A(v_mfma_f32_16x16x32_bf16 a[240:243],a[100:103],v[68:71],a[240:243]
v_accvgpr_read_b32 v164,a228
v_accvgpr_read_b32 v165,a229
v_mfma_f32_16x16x32_bf16 a[240:243],a[104:107],v[72:75],a[240:243]
v_cvt_pk_bf16_f32 v164,v164,v165
v_accvgpr_read_b32 v166,a230
v_mfma_f32_16x16x32_bf16 a[240:243],a[108:111],v[76:79],a[240:243]
v_accvgpr_read_b32 v167,a231
v_cvt_pk_bf16_f32 v165,v166,v167
v_mfma_f32_16x16x32_bf16 a[244:247],a[108:111],v[92:95],a[244:247]
ds_read_b64_tr_b16 a[182:183],v45 offset:15520
ds_read_b64_tr_b16 a[184:185],v45 offset:20640
v_mfma_f32_16x16x32_bf16 a[244:247],a[104:107],v[88:91],a[244:247]
v_accvgpr_read_b32 v176,a232
v_accvgpr_read_b32 v177,a233
v_mfma_f32_16x16x32_bf16 a[244:247],a[100:103],v[84:87],a[244:247]
v_cvt_pk_bf16_f32 v176,v176,v177
v_accvgpr_read_b32 v178,a234
v_mfma_f32_16x16x32_bf16 a[244:247],a[96:99],v[80:83],a[244:247]
v_accvgpr_read_b32 v179,a235
v_cvt_pk_bf16_f32 v177,v178,v179
v_mfma_f32_16x16x32_bf16 a[248:251],a[96:99],v[96:99],a[248:251]
ds_read_b64_tr_b16 a[186:187],v45 offset:25760
ds_read_b64_tr_b16 a[188:189],v45 offset:30880
v_mfma_f32_16x16x32_bf16 a[248:251],a[100:103],v[100:103],a[248:251]
ds_read_b64_tr_b16 a[190:191],v45 offset:36000
v_mfma_f32_16x16x32_bf16 a[248:251],a[104:107],v[104:107],a[248:251]
ds_read_b64_tr_b16 a[96:97],v42 offset:192
v_mfma_f32_16x16x32_bf16 a[248:251],a[108:111],v[108:111],a[248:251]
ds_read_b64_tr_b16 a[98:99],v42 offset:5312
v_mfma_f32_16x16x32_bf16 a[240:243],a[112:115],v[136:139],a[240:243]
ds_read_b64_tr_b16 a[100:101],v42 offset:10432
ds_read_b64_tr_b16 a[102:103],v42 offset:15552
v_mfma_f32_16x16x32_bf16 a[240:243],a[116:119],v[140:143],a[240:243]
ds_read_b64_tr_b16 a[104:105],v42 offset:20672
v_mfma_f32_16x16x32_bf16 a[240:243],a[120:123],v[152:155],a[240:243]
ds_read_b64_tr_b16 a[106:107],v42 offset:25792
v_mfma_f32_16x16x32_bf16 a[240:243],a[124:127],v[156:159],a[240:243]
ds_read_b64_tr_b16 a[108:109],v42 offset:30912
v_mfma_f32_16x16x32_bf16 a[244:247],a[124:127],v[188:191],a[244:247]
ds_read_b64_tr_b16 a[110:111],v42 offset:36032
v_mfma_f32_16x16x32_bf16 a[244:247],a[120:123],v[184:187],a[244:247]
v_mfma_f32_16x16x32_bf16 a[244:247],a[116:119],v[172:175],a[244:247]
v_mfma_f32_16x16x32_bf16 a[244:247],a[112:115],v[168:171],a[244:247]
v_mfma_f32_16x16x32_bf16 a[248:251],a[112:115],v[200:203],a[248:251]
ds_read_b64_tr_b16 a[112:113],v44 offset:192
ds_read_b64_tr_b16 a[114:115],v44 offset:5312
v_mfma_f32_16x16x32_bf16 a[248:251],a[116:119],v[204:207],a[248:251]
v_accvgpr_read_b32 v162,a240
v_accvgpr_read_b32 v163,a241
v_mfma_f32_16x16x32_bf16 a[248:251],a[120:123],v[216:219],a[248:251]
v_accvgpr_read_b32 v16,a242
v_accvgpr_read_b32 v17,a243
v_mfma_f32_16x16x32_bf16 a[248:251],a[124:127],v[220:223],a[248:251]
v_cvt_pk_bf16_f32 v162,v162,v163
v_cvt_pk_bf16_f32 v163,v16,v17
v_mfma_f32_16x16x32_bf16 a[0:3],a[128:131],v[64:67],a[0:3]
buffer_store_dwordx4 v[160:163],v6,s[32:35],s84 offen offset:1024 sc1
v_mfma_f32_16x16x32_bf16 a[0:3],a[132:135],v[68:71],a[0:3]
v_accvgpr_read_b32 v166,a244
v_accvgpr_read_b32 v167,a245
v_mfma_f32_16x16x32_bf16 a[0:3],a[136:139],v[72:75],a[0:3]
v_accvgpr_read_b32 v12,a246
v_accvgpr_read_b32 v13,a247
v_mfma_f32_16x16x32_bf16 a[0:3],a[140:143],v[76:79],a[0:3]
v_cvt_pk_bf16_f32 v166,v166,v167
v_cvt_pk_bf16_f32 v167,v12,v13
v_mfma_f32_16x16x32_bf16 a[4:7],a[140:143],v[92:95],a[4:7]
buffer_store_dwordx4 v[164:167],v6,s[32:35],s85 offen offset:1024 sc1
v_mfma_f32_16x16x32_bf16 a[4:7],a[136:139],v[88:91],a[4:7]
v_accvgpr_read_b32 v178,a248
v_accvgpr_read_b32 v179,a249
v_mfma_f32_16x16x32_bf16 a[4:7],a[132:135],v[84:87],a[4:7]
.p2align 3
v_accvgpr_read_b32 v14,a250
v_accvgpr_read_b32 v15,a251
v_cvt_pk_bf16_f32 v178,v178,v179
v_cvt_pk_bf16_f32 v179,v14,v15
v_mfma_f32_16x16x32_bf16 a[4:7],a[128:131],v[80:83],a[4:7]
ds_read_b64_tr_b16 a[116:117],v44 offset:10432
ds_read_b64_tr_b16 a[118:119],v44 offset:15552
v_mfma_f32_16x16x32_bf16 a[8:11],a[128:131],v[96:99],a[8:11]
buffer_store_dwordx4 v[176:179],v6,s[32:35],s86 offen offset:1024 sc1
v_mfma_f32_16x16x32_bf16 a[8:11],a[132:135],v[100:103],a[8:11]
ds_read_b64_tr_b16 a[120:121],v44 offset:20672
ds_read_b64_tr_b16 a[122:123],v44 offset:25792
v_mfma_f32_16x16x32_bf16 a[8:11],a[136:139],v[104:107],a[8:11]
ds_read_b64_tr_b16 a[124:125],v44 offset:30912
v_mfma_f32_16x16x32_bf16 a[8:11],a[140:143],v[108:111],a[8:11]
ds_read_b64_tr_b16 a[126:127],v44 offset:36032
v_mfma_f32_16x16x32_bf16 a[0:3],a[144:147],v[136:139],a[0:3]
s_waitcnt lgkmcnt(6)
ds_read_b64_tr_b16 a[128:129],v43 offset:224
v_mfma_f32_16x16x32_bf16 a[0:3],a[148:151],v[140:143],a[0:3]
ds_read_b64_tr_b16 a[130:131],v43 offset:5344
ds_read_b64_tr_b16 a[132:133],v43 offset:10464
v_mfma_f32_16x16x32_bf16 a[0:3],a[152:155],v[152:155],a[0:3]
ds_read_b64_tr_b16 a[134:135],v43 offset:15584
v_mfma_f32_16x16x32_bf16 a[0:3],a[156:159],v[156:159],a[0:3]
ds_read_b64_tr_b16 a[136:137],v43 offset:20704
v_mfma_f32_16x16x32_bf16 a[4:7],a[156:159],v[188:191],a[4:7]
ds_read_b64_tr_b16 a[138:139],v43 offset:25824
v_mfma_f32_16x16x32_bf16 a[4:7],a[152:155],v[184:187],a[4:7]
ds_read_b64_tr_b16 a[140:141],v43 offset:30944
ds_read_b64_tr_b16 a[142:143],v43 offset:36064
v_mfma_f32_16x16x32_bf16 a[4:7],a[148:151],v[172:175],a[4:7]
v_mfma_f32_16x16x32_bf16 a[4:7],a[144:147],v[168:171],a[4:7]
v_mfma_f32_16x16x32_bf16 a[8:11],a[144:147],v[200:203],a[8:11]
ds_read_b64_tr_b16 a[144:145],v45 offset:224
ds_read_b64_tr_b16 a[146:147],v45 offset:5344
v_mfma_f32_16x16x32_bf16 a[8:11],a[148:151],v[204:207],a[8:11]
v_accvgpr_read_b32 v192,a0
v_accvgpr_read_b32 v193,a1
v_mfma_f32_16x16x32_bf16 a[8:11],a[152:155],v[216:219],a[8:11]
v_cvt_pk_bf16_f32 v192,v192,v193
v_accvgpr_read_b32 v194,a2
v_mfma_f32_16x16x32_bf16 a[8:11],a[156:159],v[220:223],a[8:11]
v_accvgpr_read_b32 v195,a3
v_cvt_pk_bf16_f32 v193,v194,v195
v_mfma_f32_16x16x32_bf16 a[16:19],a[160:163],v[64:67],a[16:19]
ds_read_b64_tr_b16 a[148:149],v45 offset:10464
ds_read_b64_tr_b16 a[150:151],v45 offset:15584
v_mfma_f32_16x16x32_bf16 a[16:19],a[164:167],v[68:71],a[16:19]
v_accvgpr_read_b32 v196,a4
v_accvgpr_read_b32 v197,a5
v_mfma_f32_16x16x32_bf16 a[16:19],a[168:171],v[72:75],a[16:19]
v_cvt_pk_bf16_f32 v196,v196,v197
v_accvgpr_read_b32 v198,a6
v_mfma_f32_16x16x32_bf16 a[16:19],a[172:175],v[76:79],a[16:19]
v_accvgpr_read_b32 v199,a7
v_cvt_pk_bf16_f32 v197,v198,v199
v_mfma_f32_16x16x32_bf16 a[20:23],a[172:175],v[92:95],a[20:23]
ds_read_b64_tr_b16 a[152:153],v45 offset:20704
ds_read_b64_tr_b16 a[154:155],v45 offset:25824
v_mfma_f32_16x16x32_bf16 a[20:23],a[168:171],v[88:91],a[20:23]
ds_read_b64_tr_b16 a[156:157],v45 offset:30944
v_accvgpr_read_b32 v208,a8
v_mfma_f32_16x16x32_bf16 a[20:23],a[164:167],v[84:87],a[20:23]
ds_read_b64_tr_b16 a[158:159],v45 offset:36064
v_accvgpr_read_b32 v209,a9
v_mfma_f32_16x16x32_bf16 a[20:23],a[160:163],v[80:83],a[20:23]
v_cvt_pk_bf16_f32 v208,v208,v209
v_accvgpr_read_b32 v210,a10
v_mfma_f32_16x16x32_bf16 a[24:27],a[160:163],v[96:99],a[24:27]
v_accvgpr_read_b32 v211,a11
v_cvt_pk_bf16_f32 v209,v210,v211
v_mfma_f32_16x16x32_bf16 a[24:27],a[164:167],v[100:103],a[24:27]
ds_read_b64_tr_b16 a[160:161],v42 offset:256
ds_read_b64_tr_b16 a[162:163],v42 offset:5376
v_mfma_f32_16x16x32_bf16 a[24:27],a[168:171],v[104:107],a[24:27]
ds_read_b64_tr_b16 a[164:165],v42 offset:10496
v_mfma_f32_16x16x32_bf16 a[24:27],a[172:175],v[108:111],a[24:27]
ds_read_b64_tr_b16 a[166:167],v42 offset:15616
v_mfma_f32_16x16x32_bf16 a[16:19],a[176:179],v[136:139],a[16:19]
ds_read_b64_tr_b16 a[168:169],v42 offset:20736
v_mfma_f32_16x16x32_bf16 a[16:19],a[180:183],v[140:143],a[16:19]
ds_read_b64_tr_b16 a[170:171],v42 offset:25856
ds_read_b64_tr_b16 a[172:173],v42 offset:30976
v_mfma_f32_16x16x32_bf16 a[16:19],a[184:187],v[152:155],a[16:19]
ds_read_b64_tr_b16 a[174:175],v42 offset:36096
v_mfma_f32_16x16x32_bf16 a[16:19],a[188:191],v[156:159],a[16:19]
v_mfma_f32_16x16x32_bf16 a[20:23],a[188:191],v[188:191],a[20:23]
v_mfma_f32_16x16x32_bf16 a[20:23],a[184:187],v[184:187],a[20:23]
v_mfma_f32_16x16x32_bf16 a[20:23],a[180:183],v[172:175],a[20:23]
v_mfma_f32_16x16x32_bf16 a[20:23],a[176:179],v[168:171],a[20:23]
v_mfma_f32_16x16x32_bf16 a[24:27],a[176:179],v[200:203],a[24:27]
ds_read_b64_tr_b16 a[176:177],v44 offset:256
ds_read_b64_tr_b16 a[178:179],v44 offset:5376
v_mfma_f32_16x16x32_bf16 a[24:27],a[180:183],v[204:207],a[24:27]
s_lshl_b32 s95,s42,2
s_add_u32 s95,s95,s40
s_lshl_b32 s95,s95,4
v_mfma_f32_16x16x32_bf16 a[24:27],a[184:187],v[216:219],a[24:27]
s_addk_i32 s95,5120
v_bfe_u32 v19,s55,13,1
v_mov_b32 v18,s68
v_or_b32 v19,s69,v19
v_mfma_f32_16x16x32_bf16 a[24:27],a[188:191],v[220:223],a[24:27]
s_waitcnt vmcnt(0)
s_mov_b64 exec,1
buffer_store_dwordx2 v[18:19],v6,s[64:67],s95 offen sc1
s_mov_b64 exec,-1
v_mfma_f32_16x16x32_bf16 a[32:35],a[96:99],v[64:67],a[32:35]
v_accvgpr_read_b32 v194,a16
v_accvgpr_read_b32 v195,a17
v_mfma_f32_16x16x32_bf16 a[32:35],a[100:103],v[68:71],a[32:35]
v_accvgpr_read_b32 v16,a18
v_accvgpr_read_b32 v17,a19
v_mfma_f32_16x16x32_bf16 a[32:35],a[104:107],v[72:75],a[32:35]
v_cvt_pk_bf16_f32 v194,v194,v195
v_cvt_pk_bf16_f32 v195,v16,v17
v_mfma_f32_16x16x32_bf16 a[32:35],a[108:111],v[76:79],a[32:35]
buffer_store_dwordx4 v[192:195],v6,s[32:35],s84 offen offset:2048 sc1
v_mfma_f32_16x16x32_bf16 a[36:39],a[108:111],v[92:95],a[36:39]
v_accvgpr_read_b32 v198,a20
v_accvgpr_read_b32 v199,a21
v_mfma_f32_16x16x32_bf16 a[36:39],a[104:107],v[88:91],a[36:39]
v_accvgpr_read_b32 v12,a22
v_accvgpr_read_b32 v13,a23
v_mfma_f32_16x16x32_bf16 a[36:39],a[100:103],v[84:87],a[36:39]
v_cvt_pk_bf16_f32 v198,v198,v199
v_cvt_pk_bf16_f32 v199,v12,v13
v_mfma_f32_16x16x32_bf16 a[36:39],a[96:99],v[80:83],a[36:39]
buffer_store_dwordx4 v[196:199],v6,s[32:35],s85 offen offset:2048 sc1
v_mfma_f32_16x16x32_bf16 a[40:43],a[96:99],v[96:99],a[40:43]
v_accvgpr_read_b32 v210,a24
v_accvgpr_read_b32 v211,a25
v_mfma_f32_16x16x32_bf16 a[40:43],a[100:103],v[100:103],a[40:43]
v_accvgpr_read_b32 v14,a26
v_accvgpr_read_b32 v15,a27
v_mfma_f32_16x16x32_bf16 a[40:43],a[104:107],v[104:107],a[40:43]
v_cvt_pk_bf16_f32 v210,v210,v211
v_cvt_pk_bf16_f32 v211,v14,v15
v_mfma_f32_16x16x32_bf16 a[40:43],a[108:111],v[108:111],a[40:43]
buffer_store_dwordx4 v[208:211],v6,s[32:35],s86 offen offset:2048 sc1
v_mfma_f32_16x16x32_bf16 a[32:35],a[112:115],v[136:139],a[32:35]
s_waitcnt lgkmcnt(0)
ds_read_b64_tr_b16 a[180:181],v44 offset:10496
ds_read_b64_tr_b16 a[182:183],v44 offset:15616
v_mfma_f32_16x16x32_bf16 a[32:35],a[116:119],v[140:143],a[32:35]
ds_read_b64_tr_b16 a[184:185],v44 offset:20736
v_mfma_f32_16x16x32_bf16 a[32:35],a[120:123],v[152:155],a[32:35]
ds_read_b64_tr_b16 a[186:187],v44 offset:25856
v_mfma_f32_16x16x32_bf16 a[32:35],a[124:127],v[156:159],a[32:35]
ds_read_b64_tr_b16 a[188:189],v44 offset:30976
v_mfma_f32_16x16x32_bf16 a[36:39],a[124:127],v[188:191],a[36:39]
ds_read_b64_tr_b16 a[190:191],v44 offset:36096
ds_read_b64_tr_b16 a[96:97],v43 offset:288
v_mfma_f32_16x16x32_bf16 a[36:39],a[120:123],v[184:187],a[36:39]
ds_read_b64_tr_b16 a[98:99],v43 offset:5408
v_mfma_f32_16x16x32_bf16 a[36:39],a[116:119],v[172:175],a[36:39]
ds_read_b64_tr_b16 a[100:101],v43 offset:10528
v_mfma_f32_16x16x32_bf16 a[36:39],a[112:115],v[168:171],a[36:39]
ds_read_b64_tr_b16 a[102:103],v43 offset:15648
v_mfma_f32_16x16x32_bf16 a[40:43],a[112:115],v[200:203],a[40:43]
ds_read_b64_tr_b16 a[104:105],v43 offset:20768
ds_read_b64_tr_b16 a[106:107],v43 offset:25888
v_mfma_f32_16x16x32_bf16 a[40:43],a[116:119],v[204:207],a[40:43]
v_accvgpr_read_b32 v224,a32
v_accvgpr_read_b32 v225,a33
v_mfma_f32_16x16x32_bf16 a[40:43],a[120:123],v[216:219],a[40:43]
s_lshl_b32 s94,s71,2
v_cvt_pk_bf16_f32 v224,v224,v225
v_accvgpr_read_b32 v226,a34
v_mfma_f32_16x16x32_bf16 a[40:43],a[124:127],v[220:223],a[40:43]
s_add_u32 s94,s94,s40
v_accvgpr_read_b32 v227,a35
v_cvt_pk_bf16_f32 v225,v226,v227
v_mfma_f32_16x16x32_bf16 a[48:51],a[128:131],v[64:67],a[48:51]
s_lshl_b32 s94,s94,4
v_bfe_u32 v24,v6,4,2
v_min_u32 v24,2,v24
v_mfma_f32_16x16x32_bf16 a[48:51],a[132:135],v[68:71],a[48:51]
s_addk_i32 s94,5120
v_accvgpr_read_b32 v228,a36
v_accvgpr_read_b32 v229,a37
)A"
R"A(v_mfma_f32_16x16x32_bf16 a[48:51],a[136:139],v[72:75],a[48:51]
v_cvt_pk_bf16_f32 v228,v228,v229
v_accvgpr_read_b32 v230,a38
v_mfma_f32_16x16x32_bf16 a[48:51],a[140:143],v[76:79],a[48:51]
v_accvgpr_read_b32 v231,a39
v_cvt_pk_bf16_f32 v229,v230,v231
v_mfma_f32_16x16x32_bf16 a[52:55],a[140:143],v[92:95],a[52:55]
v_add_u32 v24,s70,v24
v_add_u32 v24,1,v24
v_mfma_f32_16x16x32_bf16 a[52:55],a[136:139],v[88:91],a[52:55]
v_accvgpr_read_b32 v240,a40
v_accvgpr_read_b32 v241,a41
v_mfma_f32_16x16x32_bf16 a[52:55],a[132:135],v[84:87],a[52:55]
v_cvt_pk_bf16_f32 v240,v240,v241
v_accvgpr_read_b32 v242,a42
v_mfma_f32_16x16x32_bf16 a[52:55],a[128:131],v[80:83],a[52:55]
v_accvgpr_read_b32 v243,a43
v_cvt_pk_bf16_f32 v241,v242,v243
v_mfma_f32_16x16x32_bf16 a[56:59],a[128:131],v[96:99],a[56:59]
v_and_b32 v24,3,v24
v_lshlrev_b32 v24,8,v24
v_mfma_f32_16x16x32_bf16 a[56:59],a[132:135],v[100:103],a[56:59]
ds_read_b64_tr_b16 a[108:109],v43 offset:31008
ds_read_b64_tr_b16 a[110:111],v43 offset:36128
v_mfma_f32_16x16x32_bf16 a[56:59],a[136:139],v[104:107],a[56:59]
ds_read_b64_tr_b16 a[112:113],v45 offset:288
v_mfma_f32_16x16x32_bf16 a[56:59],a[140:143],v[108:111],a[56:59]
ds_read_b64_tr_b16 a[114:115],v45 offset:5408
v_mfma_f32_16x16x32_bf16 a[48:51],a[144:147],v[136:139],a[48:51]
ds_read_b64_tr_b16 a[116:117],v45 offset:10528
v_mfma_f32_16x16x32_bf16 a[48:51],a[148:151],v[140:143],a[48:51]
ds_read_b64_tr_b16 a[118:119],v45 offset:15648
ds_read_b64_tr_b16 a[120:121],v45 offset:20768
v_mfma_f32_16x16x32_bf16 a[48:51],a[152:155],v[152:155],a[48:51]
ds_read_b64_tr_b16 a[122:123],v45 offset:25888
v_mfma_f32_16x16x32_bf16 a[48:51],a[156:159],v[156:159],a[48:51]
ds_read_b64_tr_b16 a[124:125],v45 offset:31008
v_mfma_f32_16x16x32_bf16 a[52:55],a[156:159],v[188:191],a[52:55]
ds_read_b64_tr_b16 a[126:127],v45 offset:36128
v_mfma_f32_16x16x32_bf16 a[52:55],a[152:155],v[184:187],a[52:55]
ds_read_b64_tr_b16 a[128:129],v1
ds_read_b64_tr_b16 a[130:131],v1 offset:5120
v_mfma_f32_16x16x32_bf16 a[52:55],a[148:151],v[172:175],a[52:55]
ds_read_b64_tr_b16 a[132:133],v1 offset:10240
v_mfma_f32_16x16x32_bf16 a[52:55],a[144:147],v[168:171],a[52:55]
ds_read_b64_tr_b16 a[134:135],v1 offset:15360
v_mfma_f32_16x16x32_bf16 a[56:59],a[144:147],v[200:203],a[56:59]
ds_read_b64_tr_b16 a[136:137],v1 offset:20480
v_mfma_f32_16x16x32_bf16 a[56:59],a[148:151],v[204:207],a[56:59]
v_accvgpr_read_b32 v226,a48
v_accvgpr_read_b32 v227,a49
v_mfma_f32_16x16x32_bf16 a[56:59],a[152:155],v[216:219],a[56:59]
v_accvgpr_read_b32 v16,a50
v_accvgpr_read_b32 v17,a51
v_mfma_f32_16x16x32_bf16 a[56:59],a[156:159],v[220:223],a[56:59]
v_cvt_pk_bf16_f32 v226,v226,v227
v_cvt_pk_bf16_f32 v227,v16,v17
v_mfma_f32_16x16x32_bf16 a[64:67],a[160:163],v[64:67],a[64:67]
buffer_store_dwordx4 v[224:227],v6,s[32:35],s84 offen offset:3072 sc1
v_mfma_f32_16x16x32_bf16 a[64:67],a[164:167],v[68:71],a[64:67]
v_accvgpr_read_b32 v230,a52
v_accvgpr_read_b32 v231,a53
v_mfma_f32_16x16x32_bf16 a[64:67],a[168:171],v[72:75],a[64:67]
v_accvgpr_read_b32 v12,a54
v_accvgpr_read_b32 v13,a55
v_mfma_f32_16x16x32_bf16 a[64:67],a[172:175],v[76:79],a[64:67]
v_cvt_pk_bf16_f32 v230,v230,v231
v_cvt_pk_bf16_f32 v231,v12,v13
v_mfma_f32_16x16x32_bf16 a[68:71],a[172:175],v[92:95],a[68:71]
buffer_store_dwordx4 v[228:231],v6,s[32:35],s85 offen offset:3072 sc1
v_mfma_f32_16x16x32_bf16 a[68:71],a[168:171],v[88:91],a[68:71]
v_accvgpr_read_b32 v242,a56
v_accvgpr_read_b32 v243,a57
v_mfma_f32_16x16x32_bf16 a[68:71],a[164:167],v[84:87],a[68:71]
v_accvgpr_read_b32 v14,a58
v_accvgpr_read_b32 v15,a59
v_mfma_f32_16x16x32_bf16 a[68:71],a[160:163],v[80:83],a[68:71]
v_cvt_pk_bf16_f32 v242,v242,v243
v_cvt_pk_bf16_f32 v243,v14,v15
v_mfma_f32_16x16x32_bf16 a[72:75],a[160:163],v[96:99],a[72:75]
buffer_store_dwordx4 v[240:243],v6,s[32:35],s86 offen offset:3072 sc1
v_mfma_f32_16x16x32_bf16 a[72:75],a[164:167],v[100:103],a[72:75]
ds_read_b64_tr_b16 a[138:139],v1 offset:25600
ds_read_b64_tr_b16 a[140:141],v1 offset:30720
v_mfma_f32_16x16x32_bf16 a[72:75],a[168:171],v[104:107],a[72:75]
ds_read_b64_tr_b16 a[142:143],v1 offset:35840
v_mfma_f32_16x16x32_bf16 a[72:75],a[172:175],v[108:111],a[72:75]
ds_read_b64_tr_b16 a[144:145],v42
v_mfma_f32_16x16x32_bf16 a[64:67],a[176:179],v[136:139],a[64:67]
s_waitcnt lgkmcnt(4)
ds_read_b64_tr_b16 a[146:147],v42 offset:5120
v_mfma_f32_16x16x32_bf16 a[64:67],a[180:183],v[140:143],a[64:67]
ds_read_b64_tr_b16 a[148:149],v42 offset:10240
ds_read_b64_tr_b16 a[150:151],v42 offset:15360
v_mfma_f32_16x16x32_bf16 a[64:67],a[184:187],v[152:155],a[64:67]
buffer_load_dwordx2 v[4:5],v24,s[64:67],s94 offen sc1
v_mfma_f32_16x16x32_bf16 a[64:67],a[188:191],v[156:159],a[64:67]
ds_read_b64_tr_b16 a[152:153],v42 offset:20480
ds_read_b64_tr_b16 a[154:155],v42 offset:25600
v_mfma_f32_16x16x32_bf16 a[68:71],a[188:191],v[188:191],a[68:71]
ds_read_b64_tr_b16 a[156:157],v42 offset:30720
v_mfma_f32_16x16x32_bf16 a[68:71],a[184:187],v[184:187],a[68:71]
ds_read_b64_tr_b16 a[158:159],v42 offset:35840
v_mfma_f32_16x16x32_bf16 a[68:71],a[180:183],v[172:175],a[68:71]
ds_read_b64_tr_b16 a[160:161],v44
v_mfma_f32_16x16x32_bf16 a[68:71],a[176:179],v[168:171],a[68:71]
ds_read_b64_tr_b16 a[162:163],v44 offset:5120
ds_read_b64_tr_b16 a[164:165],v44 offset:10240
v_mfma_f32_16x16x32_bf16 a[72:75],a[176:179],v[200:203],a[72:75]
ds_read_b64_tr_b16 a[166:167],v44 offset:15360
v_mfma_f32_16x16x32_bf16 a[72:75],a[180:183],v[204:207],a[72:75]
v_accvgpr_read_b32 v128,a64
v_accvgpr_read_b32 v129,a65
v_mfma_f32_16x16x32_bf16 a[72:75],a[184:187],v[216:219],a[72:75]
v_cvt_pk_bf16_f32 v128,v128,v129
v_accvgpr_read_b32 v130,a66
v_mfma_f32_16x16x32_bf16 a[72:75],a[188:191],v[220:223],a[72:75]
v_accvgpr_read_b32 v131,a67
v_cvt_pk_bf16_f32 v129,v130,v131
v_mfma_f32_16x16x32_bf16 a[80:83],a[96:99],v[64:67],a[80:83]
ds_read_b64_tr_b16 a[168:169],v44 offset:20480
ds_read_b64_tr_b16 a[170:171],v44 offset:25600
v_mfma_f32_16x16x32_bf16 a[80:83],a[100:103],v[68:71],a[80:83]
ds_read_b64_tr_b16 a[172:173],v44 offset:30720
v_accvgpr_read_b32 v132,a68
v_mfma_f32_16x16x32_bf16 a[80:83],a[104:107],v[72:75],a[80:83]
ds_read_b64_tr_b16 a[174:175],v44 offset:35840
v_accvgpr_read_b32 v133,a69
v_mfma_f32_16x16x32_bf16 a[80:83],a[108:111],v[76:79],a[80:83]
ds_read_b64_tr_b16 a[176:177],v2 offset:32
v_cvt_pk_bf16_f32 v132,v132,v133
v_mfma_f32_16x16x32_bf16 a[84:87],a[108:111],v[92:95],a[84:87]
ds_read_b64_tr_b16 a[178:179],v2 offset:5152
v_accvgpr_read_b32 v134,a70
v_mfma_f32_16x16x32_bf16 a[84:87],a[104:107],v[88:91],a[84:87]
v_accvgpr_read_b32 v144,a72
v_accvgpr_read_b32 v145,a73
v_mfma_f32_16x16x32_bf16 a[84:87],a[100:103],v[84:87],a[84:87]
v_accvgpr_read_b32 v135,a71
v_cvt_pk_bf16_f32 v144,v144,v145
v_mfma_f32_16x16x32_bf16 a[84:87],a[96:99],v[80:83],a[84:87]
v_accvgpr_read_b32 v146,a74
v_accvgpr_read_b32 v147,a75
v_mfma_f32_16x16x32_bf16 a[88:91],a[96:99],v[96:99],a[88:91]
ds_read_b64_tr_b16 a[180:181],v2 offset:10272
ds_read_b64_tr_b16 a[182:183],v2 offset:15392
v_mfma_f32_16x16x32_bf16 a[88:91],a[100:103],v[100:103],a[88:91]
v_cvt_pk_bf16_f32 v133,v134,v135
v_cvt_pk_bf16_f32 v145,v146,v147
v_mfma_f32_16x16x32_bf16 a[88:91],a[104:107],v[104:107],a[88:91]
ds_read_b64_tr_b16 a[184:185],v2 offset:20512
ds_read_b64_tr_b16 a[186:187],v2 offset:25632
v_mfma_f32_16x16x32_bf16 a[88:91],a[108:111],v[108:111],a[88:91]
ds_read_b64_tr_b16 a[188:189],v2 offset:30752
v_mfma_f32_16x16x32_bf16 a[80:83],a[112:115],v[136:139],a[80:83]
ds_read_b64_tr_b16 a[190:191],v2 offset:35872
v_mfma_f32_16x16x32_bf16 a[80:83],a[116:119],v[140:143],a[80:83]
ds_read_b64_tr_b16 a[96:97],v43 offset:32
v_mfma_f32_16x16x32_bf16 a[80:83],a[120:123],v[152:155],a[80:83]
ds_read_b64_tr_b16 a[98:99],v43 offset:5152
ds_read_b64_tr_b16 a[100:101],v43 offset:10272
v_mfma_f32_16x16x32_bf16 a[80:83],a[124:127],v[156:159],a[80:83]
ds_read_b64_tr_b16 a[102:103],v43 offset:15392
v_mfma_f32_16x16x32_bf16 a[84:87],a[124:127],v[188:191],a[84:87]
ds_read_b64_tr_b16 a[104:105],v43 offset:20512
v_mfma_f32_16x16x32_bf16 a[84:87],a[120:123],v[184:187],a[84:87]
ds_read_b64_tr_b16 a[106:107],v43 offset:25632
v_mfma_f32_16x16x32_bf16 a[84:87],a[116:119],v[172:175],a[84:87]
ds_read_b64_tr_b16 a[108:109],v43 offset:30752
ds_read_b64_tr_b16 a[110:111],v43 offset:35872
v_mfma_f32_16x16x32_bf16 a[84:87],a[112:115],v[168:171],a[84:87]
v_mfma_f32_16x16x32_bf16 a[88:91],a[112:115],v[200:203],a[88:91]
ds_read_b64_tr_b16 a[112:113],v45 offset:32
ds_read_b64_tr_b16 a[114:115],v45 offset:5152
v_mfma_f32_16x16x32_bf16 a[88:91],a[116:119],v[204:207],a[88:91]
v_accvgpr_read_b32 v130,a80
v_accvgpr_read_b32 v131,a81
v_mfma_f32_16x16x32_bf16 a[88:91],a[120:123],v[216:219],a[88:91]
v_accvgpr_read_b32 v16,a82
v_accvgpr_read_b32 v17,a83
v_mfma_f32_16x16x32_bf16 a[88:91],a[124:127],v[220:223],a[88:91]
v_cvt_pk_bf16_f32 v130,v130,v131
v_cvt_pk_bf16_f32 v131,v16,v17
v_mfma_f32_16x16x32_bf16 a[204:207],a[128:131],v[148:151],a[204:207]
buffer_store_dwordx4 v[128:131],v25,s[32:35],s84 offen sc1
v_mfma_f32_16x16x32_bf16 a[204:207],a[132:135],v[180:183],a[204:207]
s_waitcnt lgkmcnt(4)
v_accvgpr_read_b32 v134,a84
v_accvgpr_read_b32 v135,a85
v_mfma_f32_16x16x32_bf16 a[204:207],a[136:139],v[212:215],a[204:207]
s_lshl_b32 s95,s42,2
s_waitcnt vmcnt(1)
v_cmp_ne_u32 s[90:91],s68,v4
v_and_b32 v4,-2,v5
v_cmp_ne_u32 s[92:93],s69,v4
s_nop 0
s_or_b64 s[90:91],s[90:91],s[92:93]
s_cbranch_scc1 L_fr10_%=
L_fo10_%=:
v_lshlrev_b32 v19,13,v5
v_and_b32 v19,8192,v19
s_nop 0
v_readlane_b32 s90,v19,2
v_readlane_b32 s91,v19,1
v_readlane_b32 s92,v19,0
s_nop 1
s_or_b32 s76,s76,s90
s_or_b32 s77,s77,s91
s_or_b32 s78,s78,s92
v_mfma_f32_16x16x32_bf16 a[204:207],a[140:143],v[244:247],a[204:207]
s_add_u32 s95,s95,s40
s_lshl_b32 s95,s95,4
v_bfe_u32 v19,s55,13,1
v_mov_b32 v18,s68
v_or_b32 v19,s69,v19
v_mfma_f32_16x16x32_bf16 a[204:207],a[144:147],v[112:115],a[204:207]
s_lshl_b32 s94,s71,2
s_mov_b64 exec,1
buffer_store_dwordx2 v[18:19],v6,s[64:67],s95 offen offset:2048 sc1
s_mov_b64 exec,-1
v_mfma_f32_16x16x32_bf16 a[204:207],a[148:151],v[116:119],a[204:207]
s_add_u32 s94,s94,s40
v_accvgpr_read_b32 v146,a88
v_accvgpr_read_b32 v147,a89
v_mfma_f32_16x16x32_bf16 a[204:207],a[152:155],v[120:123],a[204:207]
.p2align 3
v_accvgpr_read_b32 v12,a86
v_accvgpr_read_b32 v13,a87
v_cvt_pk_bf16_f32 v134,v134,v135
v_accvgpr_read_b32 v14,a90
v_accvgpr_read_b32 v15,a91
v_cvt_pk_bf16_f32 v146,v146,v147
v_cvt_pk_bf16_f32 v135,v12,v13
v_cvt_pk_bf16_f32 v147,v14,v15
v_mfma_f32_16x16x32_bf16 a[204:207],a[156:159],v[124:127],a[204:207]
s_lshl_b32 s94,s94,4
ds_read_b64_tr_b16 a[116:117],v45 offset:10272
ds_read_b64_tr_b16 a[118:119],v45 offset:15392
v_mfma_f32_16x16x32_bf16 a[204:207],a[160:163],v[232:235],a[204:207]
buffer_store_dwordx4 v[132:135],v25,s[32:35],s85 offen sc1
v_mfma_f32_16x16x32_bf16 a[204:207],a[164:167],v[236:239],a[204:207]
ds_read_b64_tr_b16 a[120:121],v45 offset:20512
ds_read_b64_tr_b16 a[122:123],v45 offset:25632
v_mfma_f32_16x16x32_bf16 a[204:207],a[168:171],v[248:251],a[204:207]
ds_read_b64_tr_b16 a[124:125],v45 offset:30752
ds_read_b64_tr_b16 a[126:127],v45 offset:35872
v_mfma_f32_16x16x32_bf16 a[204:207],a[172:175],v[252:255],a[204:207]
ds_read_b64_tr_b16 a[128:129],v1 offset:64
ds_read_b64_tr_b16 a[130:131],v1 offset:5184
v_mfma_f32_16x16x32_bf16 a[220:223],a[176:179],v[148:151],a[220:223]
buffer_store_dwordx4 v[144:147],v25,s[32:35],s86 offen sc1
v_mfma_f32_16x16x32_bf16 a[220:223],a[180:183],v[180:183],a[220:223]
ds_read_b64_tr_b16 a[132:133],v1 offset:10304
ds_read_b64_tr_b16 a[134:135],v1 offset:15424
v_mfma_f32_16x16x32_bf16 a[220:223],a[184:187],v[212:215],a[220:223]
ds_read_b64_tr_b16 a[136:137],v1 offset:20544
ds_read_b64_tr_b16 a[138:139],v1 offset:25664
v_mfma_f32_16x16x32_bf16 a[220:223],a[188:191],v[244:247],a[220:223]
ds_read_b64_tr_b16 a[140:141],v1 offset:30784
ds_read_b64_tr_b16 a[142:143],v1 offset:35904
v_mfma_f32_16x16x32_bf16 a[220:223],a[96:99],v[112:115],a[220:223]
buffer_load_dwordx4 a[192:195],v6,s[32:35],s76 offen sc1
v_mfma_f32_16x16x32_bf16 a[220:223],a[100:103],v[116:119],a[220:223]
ds_read_b64_tr_b16 a[144:145],v42 offset:64
ds_read_b64_tr_b16 a[146:147],v42 offset:5184
)A"
R"A(v_mfma_f32_16x16x32_bf16 a[220:223],a[104:107],v[120:123],a[220:223]
s_waitcnt lgkmcnt(10)
ds_read_b64_tr_b16 a[148:149],v42 offset:10304
ds_read_b64_tr_b16 a[150:151],v42 offset:15424
v_mfma_f32_16x16x32_bf16 a[220:223],a[108:111],v[124:127],a[220:223]
ds_read_b64_tr_b16 a[152:153],v42 offset:20544
ds_read_b64_tr_b16 a[154:155],v42 offset:25664
v_mfma_f32_16x16x32_bf16 a[220:223],a[112:115],v[232:235],a[220:223]
buffer_load_dwordx4 a[196:199],v6,s[32:35],s77 offen sc1
v_mfma_f32_16x16x32_bf16 a[220:223],a[116:119],v[236:239],a[220:223]
ds_read_b64_tr_b16 a[156:157],v42 offset:30784
ds_read_b64_tr_b16 a[158:159],v42 offset:35904
v_mfma_f32_16x16x32_bf16 a[220:223],a[120:123],v[248:251],a[220:223]
ds_read_b64_tr_b16 a[160:161],v44 offset:64
ds_read_b64_tr_b16 a[162:163],v44 offset:5184
v_mfma_f32_16x16x32_bf16 a[220:223],a[124:127],v[252:255],a[220:223]
s_waitcnt lgkmcnt(10)
ds_read_b64_tr_b16 a[164:165],v44 offset:10304
ds_read_b64_tr_b16 a[166:167],v44 offset:15424
v_mfma_f32_16x16x32_bf16 a[236:239],a[128:131],v[148:151],a[236:239]
buffer_load_dwordx4 a[200:203],v6,s[32:35],s78 offen sc1
v_mfma_f32_16x16x32_bf16 a[236:239],a[132:135],v[180:183],a[236:239]
ds_read_b64_tr_b16 a[168:169],v44 offset:20544
ds_read_b64_tr_b16 a[170:171],v44 offset:25664
v_mfma_f32_16x16x32_bf16 a[236:239],a[136:139],v[212:215],a[236:239]
ds_read_b64_tr_b16 a[172:173],v44 offset:30784
ds_read_b64_tr_b16 a[174:175],v44 offset:35904
v_mfma_f32_16x16x32_bf16 a[236:239],a[140:143],v[244:247],a[236:239]
s_waitcnt lgkmcnt(10)
ds_read_b64_tr_b16 a[176:177],v2 offset:96
ds_read_b64_tr_b16 a[178:179],v2 offset:5216
v_mfma_f32_16x16x32_bf16 a[236:239],a[144:147],v[112:115],a[236:239]
buffer_load_dwordx4 a[224:227],v6,s[32:35],s76 offen offset:1024 sc1
ds_read_b64_tr_b16 a[180:181],v2 offset:10336
ds_read_b64_tr_b16 a[182:183],v2 offset:15456
v_mfma_f32_16x16x32_bf16 a[236:239],a[148:151],v[116:119],a[236:239]
ds_read_b64_tr_b16 a[184:185],v2 offset:20576
ds_read_b64_tr_b16 a[186:187],v2 offset:25696
v_mfma_f32_16x16x32_bf16 a[236:239],a[152:155],v[120:123],a[236:239]
s_waitcnt lgkmcnt(12)
ds_read_b64_tr_b16 a[188:189],v2 offset:30816
ds_read_b64_tr_b16 a[190:191],v2 offset:35936
v_mfma_f32_16x16x32_bf16 a[236:239],a[156:159],v[124:127],a[236:239]
ds_read_b64_tr_b16 a[96:97],v43 offset:96
ds_read_b64_tr_b16 a[98:99],v43 offset:5216
v_mfma_f32_16x16x32_bf16 a[236:239],a[160:163],v[232:235],a[236:239]
s_waitcnt lgkmcnt(14)
buffer_load_dwordx4 a[228:231],v6,s[32:35],s77 offen offset:1024 sc1
ds_read_b64_tr_b16 a[100:101],v43 offset:10336
ds_read_b64_tr_b16 a[102:103],v43 offset:15456
v_mfma_f32_16x16x32_bf16 a[236:239],a[164:167],v[236:239],a[236:239]
s_waitcnt lgkmcnt(14)
ds_read_b64_tr_b16 a[104:105],v43 offset:20576
ds_read_b64_tr_b16 a[106:107],v43 offset:25696
v_mfma_f32_16x16x32_bf16 a[236:239],a[168:171],v[248:251],a[236:239]
s_waitcnt lgkmcnt(14)
ds_read_b64_tr_b16 a[108:109],v43 offset:30816
ds_read_b64_tr_b16 a[110:111],v43 offset:35936
v_mfma_f32_16x16x32_bf16 a[236:239],a[172:175],v[252:255],a[236:239]
s_waitcnt lgkmcnt(14)
ds_read_b64_tr_b16 a[112:113],v45 offset:96
ds_read_b64_tr_b16 a[114:115],v45 offset:5216
v_mfma_f32_16x16x32_bf16 a[252:255],a[176:179],v[148:151],a[252:255]
s_waitcnt lgkmcnt(14)
buffer_load_dwordx4 a[232:235],v6,s[32:35],s78 offen offset:1024 sc1
ds_read_b64_tr_b16 a[116:117],v45 offset:10336
ds_read_b64_tr_b16 a[118:119],v45 offset:15456
v_mfma_f32_16x16x32_bf16 a[252:255],a[180:183],v[180:183],a[252:255]
s_waitcnt lgkmcnt(14)
ds_read_b64_tr_b16 a[120:121],v45 offset:20576
ds_read_b64_tr_b16 a[122:123],v45 offset:25696
v_mfma_f32_16x16x32_bf16 a[252:255],a[184:187],v[212:215],a[252:255]
s_waitcnt lgkmcnt(14)
ds_read_b64_tr_b16 a[124:125],v45 offset:30816
ds_read_b64_tr_b16 a[126:127],v45 offset:35936
v_mfma_f32_16x16x32_bf16 a[252:255],a[188:191],v[244:247],a[252:255]
s_waitcnt lgkmcnt(14)
ds_read_b64_tr_b16 a[128:129],v1 offset:128
ds_read_b64_tr_b16 a[130:131],v1 offset:5248
v_bfe_u32 v24,v6,4,2
v_min_u32 v24,2,v24
v_mfma_f32_16x16x32_bf16 a[252:255],a[96:99],v[112:115],a[252:255]
s_waitcnt lgkmcnt(14)
ds_read_b64_tr_b16 a[132:133],v1 offset:10368
ds_read_b64_tr_b16 a[134:135],v1 offset:15488
v_add_u32 v24,s70,v24
v_add_u32 v24,1,v24
v_mfma_f32_16x16x32_bf16 a[252:255],a[100:103],v[116:119],a[252:255]
s_waitcnt lgkmcnt(14)
ds_read_b64_tr_b16 a[136:137],v1 offset:20608
ds_read_b64_tr_b16 a[138:139],v1 offset:25728
v_and_b32 v24,3,v24
v_lshlrev_b32 v24,8,v24
v_mfma_f32_16x16x32_bf16 a[252:255],a[104:107],v[120:123],a[252:255]
s_waitcnt lgkmcnt(14)
buffer_load_dwordx2 v[4:5],v24,s[64:67],s94 offen offset:2048 sc1
ds_read_b64_tr_b16 a[140:141],v1 offset:30848
ds_read_b64_tr_b16 a[142:143],v1 offset:35968
v_mfma_f32_16x16x32_bf16 a[252:255],a[108:111],v[124:127],a[252:255]
s_waitcnt lgkmcnt(14)
ds_read_b64_tr_b16 a[144:145],v42 offset:128
ds_read_b64_tr_b16 a[146:147],v42 offset:5248
v_mfma_f32_16x16x32_bf16 a[252:255],a[112:115],v[232:235],a[252:255]
s_waitcnt lgkmcnt(14)
ds_read_b64_tr_b16 a[148:149],v42 offset:10368
ds_read_b64_tr_b16 a[150:151],v42 offset:15488
v_mfma_f32_16x16x32_bf16 a[252:255],a[116:119],v[236:239],a[252:255]
s_lshl_b32 s95,s42,2
ds_read_b64_tr_b16 a[152:153],v42 offset:20608
ds_read_b64_tr_b16 a[154:155],v42 offset:25728
s_waitcnt lgkmcnt(15)
v_mfma_f32_16x16x32_bf16 a[252:255],a[120:123],v[248:251],a[252:255]
s_add_u32 s95,s95,s40
s_lshl_b32 s95,s95,4
ds_read_b64_tr_b16 a[156:157],v42 offset:30848
ds_read_b64_tr_b16 a[158:159],v42 offset:35968
v_bfe_u32 v19,s55,13,1
v_mov_b32 v18,s68
v_or_b32 v19,s69,v19
s_waitcnt lgkmcnt(15)
v_mfma_f32_16x16x32_bf16 a[252:255],a[124:127],v[252:255],a[252:255]
s_waitcnt vmcnt(7)
s_mov_b64 exec,1
buffer_store_dwordx2 v[18:19],v6,s[64:67],s95 offen offset:3072 sc1
s_mov_b64 exec,-1
ds_read_b64_tr_b16 a[160:161],v44 offset:128
ds_read_b64_tr_b16 a[162:163],v44 offset:5248
s_waitcnt lgkmcnt(15)
v_mfma_f32_16x16x32_bf16 a[12:15],a[128:131],v[148:151],a[12:15]
s_waitcnt lgkmcnt(14)
ds_read_b64_tr_b16 a[164:165],v44 offset:10368
ds_read_b64_tr_b16 a[166:167],v44 offset:15488
v_mfma_f32_16x16x32_bf16 a[12:15],a[132:135],v[180:183],a[12:15]
s_waitcnt lgkmcnt(14)
ds_read_b64_tr_b16 a[168:169],v44 offset:20608
ds_read_b64_tr_b16 a[170:171],v44 offset:25728
v_mfma_f32_16x16x32_bf16 a[12:15],a[136:139],v[212:215],a[12:15]
s_waitcnt lgkmcnt(14)
ds_read_b64_tr_b16 a[172:173],v44 offset:30848
ds_read_b64_tr_b16 a[174:175],v44 offset:35968
v_mfma_f32_16x16x32_bf16 a[12:15],a[140:143],v[244:247],a[12:15]
s_waitcnt lgkmcnt(14)
ds_read_b64_tr_b16 a[176:177],v2 offset:160
ds_read_b64_tr_b16 a[178:179],v2 offset:5280
v_mfma_f32_16x16x32_bf16 a[12:15],a[144:147],v[112:115],a[12:15]
s_waitcnt lgkmcnt(14)
ds_read_b64_tr_b16 a[180:181],v2 offset:10400
ds_read_b64_tr_b16 a[182:183],v2 offset:15520
v_mfma_f32_16x16x32_bf16 a[12:15],a[148:151],v[116:119],a[12:15]
s_waitcnt lgkmcnt(14)
ds_read_b64_tr_b16 a[184:185],v2 offset:20640
ds_read_b64_tr_b16 a[186:187],v2 offset:25760
v_mfma_f32_16x16x32_bf16 a[12:15],a[152:155],v[120:123],a[12:15]
s_waitcnt lgkmcnt(14)
ds_read_b64_tr_b16 a[188:189],v2 offset:30880
ds_read_b64_tr_b16 a[190:191],v2 offset:36000
v_mfma_f32_16x16x32_bf16 a[12:15],a[156:159],v[124:127],a[12:15]
s_waitcnt lgkmcnt(14)
ds_read_b64_tr_b16 a[96:97],v43 offset:160
ds_read_b64_tr_b16 a[98:99],v43 offset:5280
v_mfma_f32_16x16x32_bf16 a[12:15],a[160:163],v[232:235],a[12:15]
s_waitcnt lgkmcnt(14)
ds_read_b64_tr_b16 a[100:101],v43 offset:10400
ds_read_b64_tr_b16 a[102:103],v43 offset:15520
v_mfma_f32_16x16x32_bf16 a[12:15],a[164:167],v[236:239],a[12:15]
s_waitcnt lgkmcnt(14)
ds_read_b64_tr_b16 a[104:105],v43 offset:20640
ds_read_b64_tr_b16 a[106:107],v43 offset:25760
v_mfma_f32_16x16x32_bf16 a[12:15],a[168:171],v[248:251],a[12:15]
s_waitcnt lgkmcnt(14)
ds_read_b64_tr_b16 a[108:109],v43 offset:30880
ds_read_b64_tr_b16 a[110:111],v43 offset:36000
v_mfma_f32_16x16x32_bf16 a[12:15],a[172:175],v[252:255],a[12:15]
s_waitcnt lgkmcnt(14)
ds_read_b64_tr_b16 a[112:113],v45 offset:160
ds_read_b64_tr_b16 a[114:115],v45 offset:5280
v_mfma_f32_16x16x32_bf16 a[28:31],a[176:179],v[148:151],a[28:31]
s_waitcnt lgkmcnt(14)
ds_read_b64_tr_b16 a[116:117],v45 offset:10400
ds_read_b64_tr_b16 a[118:119],v45 offset:15520
v_mfma_f32_16x16x32_bf16 a[28:31],a[180:183],v[180:183],a[28:31]
s_waitcnt lgkmcnt(14)
ds_read_b64_tr_b16 a[120:121],v45 offset:20640
ds_read_b64_tr_b16 a[122:123],v45 offset:25760
v_mfma_f32_16x16x32_bf16 a[28:31],a[184:187],v[212:215],a[28:31]
s_waitcnt lgkmcnt(14)
ds_read_b64_tr_b16 a[124:125],v45 offset:30880
ds_read_b64_tr_b16 a[126:127],v45 offset:36000
v_mfma_f32_16x16x32_bf16 a[28:31],a[188:191],v[244:247],a[28:31]
s_waitcnt lgkmcnt(14)
ds_read_b64_tr_b16 a[128:129],v1 offset:192
ds_read_b64_tr_b16 a[130:131],v1 offset:5312
v_mfma_f32_16x16x32_bf16 a[28:31],a[96:99],v[112:115],a[28:31]
s_waitcnt lgkmcnt(14)
ds_read_b64_tr_b16 a[132:133],v1 offset:10432
ds_read_b64_tr_b16 a[134:135],v1 offset:15552
v_mfma_f32_16x16x32_bf16 a[28:31],a[100:103],v[116:119],a[28:31]
s_waitcnt lgkmcnt(14)
ds_read_b64_tr_b16 a[136:137],v1 offset:20672
ds_read_b64_tr_b16 a[138:139],v1 offset:25792
v_mfma_f32_16x16x32_bf16 a[28:31],a[104:107],v[120:123],a[28:31]
s_waitcnt lgkmcnt(14)
ds_read_b64_tr_b16 a[140:141],v1 offset:30912
ds_read_b64_tr_b16 a[142:143],v1 offset:36032
v_mfma_f32_16x16x32_bf16 a[28:31],a[108:111],v[124:127],a[28:31]
s_waitcnt lgkmcnt(14)
ds_read_b64_tr_b16 a[144:145],v42 offset:192
ds_read_b64_tr_b16 a[146:147],v42 offset:5312
v_mfma_f32_16x16x32_bf16 a[28:31],a[112:115],v[232:235],a[28:31]
s_waitcnt lgkmcnt(14)
ds_read_b64_tr_b16 a[148:149],v42 offset:10432
ds_read_b64_tr_b16 a[150:151],v42 offset:15552
v_mfma_f32_16x16x32_bf16 a[28:31],a[116:119],v[236:239],a[28:31]
s_waitcnt lgkmcnt(14)
ds_read_b64_tr_b16 a[152:153],v42 offset:20672
ds_read_b64_tr_b16 a[154:155],v42 offset:25792
v_mfma_f32_16x16x32_bf16 a[28:31],a[120:123],v[248:251],a[28:31]
s_waitcnt lgkmcnt(14)
ds_read_b64_tr_b16 a[156:157],v42 offset:30912
ds_read_b64_tr_b16 a[158:159],v42 offset:36032
v_mfma_f32_16x16x32_bf16 a[28:31],a[124:127],v[252:255],a[28:31]
s_waitcnt lgkmcnt(14)
ds_read_b64_tr_b16 a[160:161],v44 offset:192
ds_read_b64_tr_b16 a[162:163],v44 offset:5312
v_mfma_f32_16x16x32_bf16 a[44:47],a[128:131],v[148:151],a[44:47]
s_waitcnt lgkmcnt(14)
ds_read_b64_tr_b16 a[164:165],v44 offset:10432
ds_read_b64_tr_b16 a[166:167],v44 offset:15552
v_mfma_f32_16x16x32_bf16 a[44:47],a[132:135],v[180:183],a[44:47]
s_waitcnt lgkmcnt(14)
ds_read_b64_tr_b16 a[168:169],v44 offset:20672
ds_read_b64_tr_b16 a[170:171],v44 offset:25792
v_mfma_f32_16x16x32_bf16 a[44:47],a[136:139],v[212:215],a[44:47]
ds_read_b64_tr_b16 a[172:173],v44 offset:30912
ds_read_b64_tr_b16 a[174:175],v44 offset:36032
s_waitcnt vmcnt(1)
v_cmp_ne_u32 s[90:91],s68,v4
v_and_b32 v4,-2,v5
v_cmp_ne_u32 s[92:93],s69,v4
s_nop 0
s_or_b64 s[90:91],s[90:91],s[92:93]
s_cbranch_scc1 L_fr11_%=
L_fo11_%=:
s_waitcnt lgkmcnt(15)
v_mfma_f32_16x16x32_bf16 a[44:47],a[140:143],v[244:247],a[44:47]
s_waitcnt lgkmcnt(14)
buffer_load_dwordx4 v[72:75],v6,s[32:35],s76 offen offset:2048 sc1
ds_read_b64_tr_b16 a[176:177],v2 offset:224
ds_read_b64_tr_b16 a[178:179],v2 offset:5344
v_mfma_f32_16x16x32_bf16 a[44:47],a[144:147],v[112:115],a[44:47]
s_lshl_b32 s94,s71,2
ds_read_b64_tr_b16 a[180:181],v2 offset:10464
ds_read_b64_tr_b16 a[182:183],v2 offset:15584
s_waitcnt lgkmcnt(15)
v_mfma_f32_16x16x32_bf16 a[44:47],a[148:151],v[116:119],a[44:47]
s_add_u32 s94,s94,s40
ds_read_b64_tr_b16 a[184:185],v2 offset:20704
ds_read_b64_tr_b16 a[186:187],v2 offset:25824
s_waitcnt lgkmcnt(15)
v_mfma_f32_16x16x32_bf16 a[44:47],a[152:155],v[120:123],a[44:47]
s_lshl_b32 s94,s94,4
ds_read_b64_tr_b16 a[188:189],v2 offset:30944
ds_read_b64_tr_b16 a[190:191],v2 offset:36064
s_waitcnt lgkmcnt(15)
v_mfma_f32_16x16x32_bf16 a[44:47],a[156:159],v[124:127],a[44:47]
s_waitcnt lgkmcnt(14)
buffer_load_dwordx4 v[88:91],v6,s[32:35],s77 offen offset:2048 sc1
ds_read_b64_tr_b16 a[96:97],v43 offset:224
ds_read_b64_tr_b16 a[98:99],v43 offset:5344
v_mfma_f32_16x16x32_bf16 a[44:47],a[160:163],v[232:235],a[44:47]
)A"
R"A(s_waitcnt lgkmcnt(14)
ds_read_b64_tr_b16 a[100:101],v43 offset:10464
ds_read_b64_tr_b16 a[102:103],v43 offset:15584
v_mfma_f32_16x16x32_bf16 a[44:47],a[164:167],v[236:239],a[44:47]
s_waitcnt lgkmcnt(14)
ds_read_b64_tr_b16 a[104:105],v43 offset:20704
ds_read_b64_tr_b16 a[106:107],v43 offset:25824
v_mfma_f32_16x16x32_bf16 a[44:47],a[168:171],v[248:251],a[44:47]
s_waitcnt lgkmcnt(14)
ds_read_b64_tr_b16 a[108:109],v43 offset:30944
ds_read_b64_tr_b16 a[110:111],v43 offset:36064
v_mfma_f32_16x16x32_bf16 a[44:47],a[172:175],v[252:255],a[44:47]
s_waitcnt lgkmcnt(14)
buffer_load_dwordx4 v[104:107],v6,s[32:35],s78 offen offset:2048 sc1
ds_read_b64_tr_b16 a[112:113],v45 offset:224
ds_read_b64_tr_b16 a[114:115],v45 offset:5344
v_mfma_f32_16x16x32_bf16 a[60:63],a[176:179],v[148:151],a[60:63]
s_waitcnt lgkmcnt(14)
ds_read_b64_tr_b16 a[116:117],v45 offset:10464
ds_read_b64_tr_b16 a[118:119],v45 offset:15584
v_mfma_f32_16x16x32_bf16 a[60:63],a[180:183],v[180:183],a[60:63]
s_waitcnt lgkmcnt(14)
ds_read_b64_tr_b16 a[120:121],v45 offset:20704
ds_read_b64_tr_b16 a[122:123],v45 offset:25824
v_mfma_f32_16x16x32_bf16 a[60:63],a[184:187],v[212:215],a[60:63]
s_waitcnt lgkmcnt(14)
ds_read_b64_tr_b16 a[124:125],v45 offset:30944
ds_read_b64_tr_b16 a[126:127],v45 offset:36064
v_mfma_f32_16x16x32_bf16 a[60:63],a[188:191],v[244:247],a[60:63]
s_waitcnt lgkmcnt(14)
buffer_load_dwordx4 v[76:79],v6,s[32:35],s76 offen offset:3072 sc1
v_mfma_f32_16x16x32_bf16 a[60:63],a[96:99],v[112:115],a[60:63]
s_waitcnt lgkmcnt(12)
ds_read_b64_tr_b16 a[128:129],v1 offset:256
ds_read_b64_tr_b16 a[130:131],v1 offset:5376
v_mfma_f32_16x16x32_bf16 a[60:63],a[100:103],v[116:119],a[60:63]
s_waitcnt lgkmcnt(12)
ds_read_b64_tr_b16 a[132:133],v1 offset:10496
ds_read_b64_tr_b16 a[134:135],v1 offset:15616
v_mfma_f32_16x16x32_bf16 a[60:63],a[104:107],v[120:123],a[60:63]
s_waitcnt lgkmcnt(12)
ds_read_b64_tr_b16 a[136:137],v1 offset:20736
ds_read_b64_tr_b16 a[138:139],v1 offset:25856
v_mfma_f32_16x16x32_bf16 a[60:63],a[108:111],v[124:127],a[60:63]
s_waitcnt lgkmcnt(12)
buffer_load_dwordx4 v[92:95],v6,s[32:35],s77 offen offset:3072 sc1
v_mfma_f32_16x16x32_bf16 a[60:63],a[112:115],v[232:235],a[60:63]
s_waitcnt lgkmcnt(10)
ds_read_b64_tr_b16 a[140:141],v1 offset:30976
ds_read_b64_tr_b16 a[142:143],v1 offset:36096
v_mfma_f32_16x16x32_bf16 a[60:63],a[116:119],v[236:239],a[60:63]
s_waitcnt lgkmcnt(10)
ds_read_b64_tr_b16 a[144:145],v42 offset:256
ds_read_b64_tr_b16 a[146:147],v42 offset:5376
v_mfma_f32_16x16x32_bf16 a[60:63],a[120:123],v[248:251],a[60:63]
s_waitcnt lgkmcnt(10)
ds_read_b64_tr_b16 a[148:149],v42 offset:10496
ds_read_b64_tr_b16 a[150:151],v42 offset:15616
v_mfma_f32_16x16x32_bf16 a[60:63],a[124:127],v[252:255],a[60:63]
buffer_load_dwordx4 v[108:111],v6,s[32:35],s78 offen offset:3072 sc1
v_mfma_f32_16x16x32_bf16 a[204:207],v[34:37],a[192:195],a[204:207]
ds_read_b64_tr_b16 a[152:153],v42 offset:20736
ds_read_b64_tr_b16 a[154:155],v42 offset:25856
v_mfma_f32_16x16x32_bf16 a[204:207],v[34:37],a[196:199],a[204:207]
ds_read_b64_tr_b16 a[156:157],v42 offset:30976
ds_read_b64_tr_b16 a[158:159],v42 offset:36096
v_mfma_f32_16x16x32_bf16 a[204:207],v[34:37],a[200:203],a[204:207]
ds_read_b64_tr_b16 a[160:161],v44 offset:256
ds_read_b64_tr_b16 a[162:163],v44 offset:5376
v_mfma_f32_16x16x32_bf16 a[220:223],v[32:35],a[192:195],a[220:223]
ds_read_b64_tr_b16 a[164:165],v44 offset:10496
ds_read_b64_tr_b16 a[166:167],v44 offset:15616
v_mfma_f32_16x16x32_bf16 a[220:223],v[32:35],a[196:199],a[220:223]
ds_read_b64_tr_b16 a[168:169],v44 offset:20736
ds_read_b64_tr_b16 a[170:171],v44 offset:25856
v_mfma_f32_16x16x32_bf16 a[220:223],v[32:35],a[200:203],a[220:223]
s_waitcnt lgkmcnt(12)
ds_read_b64_tr_b16 a[172:173],v44 offset:30976
ds_read_b64_tr_b16 a[174:175],v44 offset:36096
v_mfma_f32_16x16x32_bf16 a[76:79],a[128:131],v[148:151],a[76:79]
v_bfe_u32 v24,v6,4,2
v_min_u32 v24,2,v24
v_mfma_f32_16x16x32_bf16 a[76:79],a[132:135],v[180:183],a[76:79]
v_add_u32 v24,s70,v24
v_add_u32 v24,1,v24
v_mfma_f32_16x16x32_bf16 a[76:79],a[136:139],v[212:215],a[76:79]
v_accvgpr_read_b32 v160,a204
v_accvgpr_read_b32 v161,a205
v_mfma_f32_16x16x32_bf16 a[76:79],a[140:143],v[244:247],a[76:79]
v_cvt_pk_bf16_f32 v160,v160,v161
v_accvgpr_read_b32 v162,a206
v_mfma_f32_16x16x32_bf16 a[76:79],a[144:147],v[112:115],a[76:79]
s_waitcnt lgkmcnt(6)
v_accvgpr_read_b32 v163,a207
v_cvt_pk_bf16_f32 v161,v162,v163
v_mfma_f32_16x16x32_bf16 a[76:79],a[148:151],v[116:119],a[76:79]
v_accvgpr_read_b32 v162,a220
v_accvgpr_read_b32 v163,a221
v_mfma_f32_16x16x32_bf16 a[76:79],a[152:155],v[120:123],a[76:79]
v_accvgpr_read_b32 v16,a222
v_accvgpr_read_b32 v17,a223
v_mfma_f32_16x16x32_bf16 a[76:79],a[156:159],v[124:127],a[76:79]
v_cvt_pk_bf16_f32 v162,v162,v163
v_cvt_pk_bf16_f32 v163,v16,v17
v_mfma_f32_16x16x32_bf16 a[76:79],a[160:163],v[232:235],a[76:79]
s_waitcnt lgkmcnt(0)
ds_read_b64_tr_b16 a[176:177],v2 offset:288
ds_read_b64_tr_b16 a[178:179],v2 offset:5408
v_mfma_f32_16x16x32_bf16 a[76:79],a[164:167],v[236:239],a[76:79]
v_permlane16_swap_b32 v160,v162
v_mfma_f32_16x16x32_bf16 a[76:79],a[168:171],v[248:251],a[76:79]
ds_read_b64_tr_b16 a[180:181],v2 offset:10528
ds_read_b64_tr_b16 a[182:183],v2 offset:15648
v_and_b32 v24,3,v24
v_lshlrev_b32 v24,8,v24
v_mfma_f32_16x16x32_bf16 a[76:79],a[172:175],v[252:255],a[76:79]
buffer_load_dwordx2 v[4:5],v24,s[64:67],s94 offen offset:3072 sc1
ds_read_b64_tr_b16 a[184:185],v2 offset:20768
ds_read_b64_tr_b16 a[186:187],v2 offset:25888
v_mfma_f32_16x16x32_bf16 a[236:239],v[34:37],a[224:227],a[236:239]
ds_read_b64_tr_b16 a[188:189],v2 offset:31008
ds_read_b64_tr_b16 a[190:191],v2 offset:36128
v_mfma_f32_16x16x32_bf16 a[236:239],v[34:37],a[228:231],a[236:239]
ds_read_b64_tr_b16 a[96:97],v43 offset:288
ds_read_b64_tr_b16 a[98:99],v43 offset:5408
v_mfma_f32_16x16x32_bf16 a[236:239],v[34:37],a[232:235],a[236:239]
ds_read_b64_tr_b16 a[100:101],v43 offset:10528
ds_read_b64_tr_b16 a[102:103],v43 offset:15648
v_mfma_f32_16x16x32_bf16 a[252:255],v[32:35],a[224:227],a[252:255]
ds_read_b64_tr_b16 a[104:105],v43 offset:20768
ds_read_b64_tr_b16 a[106:107],v43 offset:25888
v_mfma_f32_16x16x32_bf16 a[252:255],v[32:35],a[228:231],a[252:255]
ds_read_b64_tr_b16 a[108:109],v43 offset:31008
ds_read_b64_tr_b16 a[110:111],v43 offset:36128
v_mfma_f32_16x16x32_bf16 a[252:255],v[32:35],a[232:235],a[252:255]
s_waitcnt lgkmcnt(14)
ds_read_b64_tr_b16 a[112:113],v45 offset:288
ds_read_b64_tr_b16 a[114:115],v45 offset:5408
v_mfma_f32_16x16x32_bf16 a[92:95],a[176:179],v[148:151],a[92:95]
s_waitcnt lgkmcnt(14)
ds_read_b64_tr_b16 a[116:117],v45 offset:10528
ds_read_b64_tr_b16 a[118:119],v45 offset:15648
v_mfma_f32_16x16x32_bf16 a[92:95],a[180:183],v[180:183],a[92:95]
s_waitcnt lgkmcnt(14)
ds_read_b64_tr_b16 a[120:121],v45 offset:20768
ds_read_b64_tr_b16 a[122:123],v45 offset:25888
v_mfma_f32_16x16x32_bf16 a[92:95],a[184:187],v[212:215],a[92:95]
s_waitcnt lgkmcnt(14)
ds_read_b64_tr_b16 a[124:125],v45 offset:31008
ds_read_b64_tr_b16 a[126:127],v45 offset:36128
v_mfma_f32_16x16x32_bf16 a[92:95],a[188:191],v[244:247],a[92:95]
s_waitcnt lgkmcnt(14)
v_permlane16_swap_b32 v161,v163
v_mfma_f32_16x16x32_bf16 a[92:95],a[96:99],v[112:115],a[92:95]
s_cmp_lg_u32 %2,0
s_cbranch_scc1 L_ea0_%=
buffer_store_dwordx4 v[160:163],v40,s[20:23],s43 offen
s_branch L_eb0_%=
L_ea0_%=:
buffer_store_dwordx4 v[160:163],v40,s[20:23],s43 offen sc1
L_eb0_%=:
s_waitcnt lgkmcnt(12)
v_mfma_f32_16x16x32_bf16 a[92:95],a[100:103],v[116:119],a[92:95]
s_waitcnt lgkmcnt(10)
v_accvgpr_read_b32 v164,a236
v_accvgpr_read_b32 v165,a237
v_mfma_f32_16x16x32_bf16 a[92:95],a[104:107],v[120:123],a[92:95]
s_waitcnt lgkmcnt(8)
v_cvt_pk_bf16_f32 v164,v164,v165
v_accvgpr_read_b32 v166,a238
v_mfma_f32_16x16x32_bf16 a[92:95],a[108:111],v[124:127],a[92:95]
s_waitcnt lgkmcnt(6)
v_accvgpr_read_b32 v167,a239
v_cvt_pk_bf16_f32 v165,v166,v167
v_mfma_f32_16x16x32_bf16 a[92:95],a[112:115],v[232:235],a[92:95]
s_waitcnt lgkmcnt(4)
v_accvgpr_read_b32 v166,a252
v_accvgpr_read_b32 v167,a253
v_mfma_f32_16x16x32_bf16 a[92:95],a[116:119],v[236:239],a[92:95]
s_waitcnt lgkmcnt(2)
.p2align 3
v_accvgpr_read_b32 v12,a254
v_accvgpr_read_b32 v13,a255
v_cvt_pk_bf16_f32 v166,v166,v167
v_cvt_pk_bf16_f32 v167,v12,v13
v_mfma_f32_16x16x32_bf16 a[92:95],a[120:123],v[248:251],a[92:95]
s_waitcnt lgkmcnt(0)
v_mfma_f32_16x16x32_bf16 a[92:95],a[124:127],v[252:255],a[92:95]
v_permlane16_swap_b32 v164,v166
v_permlane16_swap_b32 v165,v167
s_nop 3
s_cmp_lg_u32 %2,0
s_cbranch_scc1 L_ea1_%=
buffer_store_dwordx4 v[164:167],v40,s[20:23],s43 offen offset:64
s_branch L_eb1_%=
L_ea1_%=:
buffer_store_dwordx4 v[164:167],v40,s[20:23],s43 offen offset:64 sc1
L_eb1_%=:
s_waitcnt vmcnt(2)
v_cmp_ne_u32 s[90:91],s68,v4
v_and_b32 v4,-2,v5
v_cmp_ne_u32 s[92:93],s69,v4
s_nop 0
s_or_b64 s[90:91],s[90:91],s[92:93]
s_cbranch_scc1 L_fr12_%=
L_fo12_%=:
buffer_load_dwordx4 v[136:139],v25,s[32:35],s76 offen sc1
buffer_load_dwordx4 v[168:171],v25,s[32:35],s77 offen sc1
buffer_load_dwordx4 v[200:203],v25,s[32:35],s78 offen sc1
s_cmp_lg_u32 %2,0
s_cbranch_scc1 L_dual_%=
v_mfma_f32_16x16x32_bf16 a[12:15],v[34:37],v[72:75],a[12:15]
v_mfma_f32_16x16x32_bf16 a[12:15],v[34:37],v[88:91],a[12:15]
v_mfma_f32_16x16x32_bf16 a[12:15],v[34:37],v[104:107],a[12:15]
v_mfma_f32_16x16x32_bf16 a[28:31],v[32:35],v[72:75],a[28:31]
v_mfma_f32_16x16x32_bf16 a[28:31],v[32:35],v[88:91],a[28:31]
v_mfma_f32_16x16x32_bf16 a[28:31],v[32:35],v[104:107],a[28:31]
v_mfma_f32_16x16x32_bf16 a[44:47],v[34:37],v[76:79],a[44:47]
v_mfma_f32_16x16x32_bf16 a[44:47],v[34:37],v[92:95],a[44:47]
v_mfma_f32_16x16x32_bf16 a[44:47],v[34:37],v[108:111],a[44:47]
s_nop 1
.p2align 3
v_accvgpr_read_b32 v176,a12
v_accvgpr_read_b32 v177,a13
v_cvt_pk_bf16_f32 v176,v176,v177
v_accvgpr_read_b32 v178,a14
v_accvgpr_read_b32 v179,a15
v_cvt_pk_bf16_f32 v177,v178,v179
v_mfma_f32_16x16x32_bf16 a[60:63],v[32:35],v[76:79],a[60:63]
v_mfma_f32_16x16x32_bf16 a[60:63],v[32:35],v[92:95],a[60:63]
v_mfma_f32_16x16x32_bf16 a[60:63],v[32:35],v[108:111],a[60:63]
.p2align 3
v_accvgpr_read_b32 v178,a28
v_accvgpr_read_b32 v179,a29
v_accvgpr_read_b32 v14,a30
v_accvgpr_read_b32 v15,a31
v_cvt_pk_bf16_f32 v178,v178,v179
v_cvt_pk_bf16_f32 v179,v14,v15
s_nop 3
v_permlane16_swap_b32 v176,v178
v_permlane16_swap_b32 v177,v179
buffer_store_dwordx4 v[176:179],v40,s[20:23],s43 offen offset:128
.p2align 3
v_accvgpr_read_b32 v192,a44
v_accvgpr_read_b32 v193,a45
v_cvt_pk_bf16_f32 v192,v192,v193
v_accvgpr_read_b32 v194,a46
v_accvgpr_read_b32 v195,a47
v_cvt_pk_bf16_f32 v193,v194,v195
v_accvgpr_read_b32 v194,a60
v_accvgpr_read_b32 v195,a61
v_accvgpr_read_b32 v16,a62
v_accvgpr_read_b32 v17,a63
v_cvt_pk_bf16_f32 v194,v194,v195
s_nop 1
v_permlane16_swap_b32 v192,v194
v_cvt_pk_bf16_f32 v195,v16,v17
s_nop 1
v_permlane16_swap_b32 v193,v195
buffer_store_dwordx4 v[192:195],v40,s[20:23],s43 offen offset:192
s_waitcnt vmcnt(4)
v_mfma_f32_16x16x32_bf16 a[76:79],v[34:37],v[136:139],a[76:79]
s_waitcnt vmcnt(3)
v_mfma_f32_16x16x32_bf16 a[76:79],v[34:37],v[168:171],a[76:79]
s_waitcnt vmcnt(2)
v_mfma_f32_16x16x32_bf16 a[76:79],v[34:37],v[200:203],a[76:79]
v_mfma_f32_16x16x32_bf16 a[92:95],v[32:35],v[136:139],a[92:95]
v_mfma_f32_16x16x32_bf16 a[92:95],v[32:35],v[168:171],a[92:95]
v_mfma_f32_16x16x32_bf16 a[92:95],v[32:35],v[200:203],a[92:95]
s_nop 3
s_nop 0
.p2align 3
v_accvgpr_read_b32 v196,a76
v_accvgpr_read_b32 v197,a77
v_cvt_pk_bf16_f32 v196,v196,v197
v_accvgpr_read_b32 v198,a78
v_accvgpr_read_b32 v199,a79
v_cvt_pk_bf16_f32 v197,v198,v199
v_accvgpr_read_b32 v198,a92
v_accvgpr_read_b32 v199,a93
v_accvgpr_read_b32 v12,a94
v_accvgpr_read_b32 v13,a95
v_cvt_pk_bf16_f32 v198,v198,v199
s_nop 1
v_permlane16_swap_b32 v196,v198
v_cvt_pk_bf16_f32 v199,v12,v13
s_nop 1
v_permlane16_swap_b32 v197,v199
buffer_store_dwordx4 v[196:199],v40,s[20:23],s43 offen offset:256
s_or_b32 s62,s42,s40
s_cmp_lg_u32 s62,0
s_cbranch_scc1 L_noep_%=
v_mov_b32 v12,s68
s_mov_b64 exec,1
buffer_store_dword v12,v25,s[64:67],0 offen sc1
s_mov_b64 exec,-1
L_noep_%=:
s_endpgm
L_dual_%=:
v_mfma_f32_16x16x32_bf16 a[12:15],v[34:37],v[72:75],a[12:15]
v_mfma_f32_16x16x32_bf16 a[12:15],v[34:37],v[88:91],a[12:15]
)A"
R"A(v_mfma_f32_16x16x32_bf16 a[12:15],v[34:37],v[104:107],a[12:15]
v_mfma_f32_16x16x32_bf16 a[28:31],v[32:35],v[72:75],a[28:31]
v_mfma_f32_16x16x32_bf16 a[28:31],v[32:35],v[88:91],a[28:31]
v_mfma_f32_16x16x32_bf16 a[28:31],v[32:35],v[104:107],a[28:31]
v_mfma_f32_16x16x32_bf16 a[44:47],v[34:37],v[76:79],a[44:47]
v_mfma_f32_16x16x32_bf16 a[44:47],v[34:37],v[92:95],a[44:47]
v_mfma_f32_16x16x32_bf16 a[44:47],v[34:37],v[108:111],a[44:47]
s_nop 1
.p2align 3
v_accvgpr_read_b32 v176,a12
v_accvgpr_read_b32 v177,a13
v_cvt_pk_bf16_f32 v176,v176,v177
v_accvgpr_read_b32 v178,a14
v_accvgpr_read_b32 v179,a15
v_cvt_pk_bf16_f32 v177,v178,v179
v_mfma_f32_16x16x32_bf16 a[60:63],v[32:35],v[76:79],a[60:63]
v_mfma_f32_16x16x32_bf16 a[60:63],v[32:35],v[92:95],a[60:63]
v_mfma_f32_16x16x32_bf16 a[60:63],v[32:35],v[108:111],a[60:63]
.p2align 3
v_accvgpr_read_b32 v178,a28
v_accvgpr_read_b32 v179,a29
v_accvgpr_read_b32 v14,a30
v_accvgpr_read_b32 v15,a31
v_cvt_pk_bf16_f32 v178,v178,v179
v_cvt_pk_bf16_f32 v179,v14,v15
s_nop 3
v_permlane16_swap_b32 v176,v178
v_permlane16_swap_b32 v177,v179
buffer_store_dwordx4 v[176:179],v40,s[20:23],s43 offen offset:128 sc1
.p2align 3
v_accvgpr_read_b32 v192,a44
v_accvgpr_read_b32 v193,a45
v_cvt_pk_bf16_f32 v192,v192,v193
v_accvgpr_read_b32 v194,a46
v_accvgpr_read_b32 v195,a47
v_cvt_pk_bf16_f32 v193,v194,v195
v_accvgpr_read_b32 v194,a60
v_accvgpr_read_b32 v195,a61
v_accvgpr_read_b32 v16,a62
v_accvgpr_read_b32 v17,a63
v_cvt_pk_bf16_f32 v194,v194,v195
s_nop 1
v_permlane16_swap_b32 v192,v194
v_cvt_pk_bf16_f32 v195,v16,v17
s_nop 1
v_permlane16_swap_b32 v193,v195
buffer_store_dwordx4 v[192:195],v40,s[20:23],s43 offen offset:192 sc1
s_waitcnt vmcnt(4)
v_mfma_f32_16x16x32_bf16 a[76:79],v[34:37],v[136:139],a[76:79]
s_waitcnt vmcnt(3)
v_mfma_f32_16x16x32_bf16 a[76:79],v[34:37],v[168:171],a[76:79]
s_waitcnt vmcnt(2)
v_mfma_f32_16x16x32_bf16 a[76:79],v[34:37],v[200:203],a[76:79]
v_mfma_f32_16x16x32_bf16 a[92:95],v[32:35],v[136:139],a[92:95]
v_mfma_f32_16x16x32_bf16 a[92:95],v[32:35],v[168:171],a[92:95]
v_mfma_f32_16x16x32_bf16 a[92:95],v[32:35],v[200:203],a[92:95]
s_nop 3
s_nop 0
.p2align 3
v_accvgpr_read_b32 v196,a76
v_accvgpr_read_b32 v197,a77
v_cvt_pk_bf16_f32 v196,v196,v197
v_accvgpr_read_b32 v198,a78
v_accvgpr_read_b32 v199,a79
v_cvt_pk_bf16_f32 v197,v198,v199
v_accvgpr_read_b32 v198,a92
v_accvgpr_read_b32 v199,a93
v_accvgpr_read_b32 v12,a94
v_accvgpr_read_b32 v13,a95
v_cvt_pk_bf16_f32 v198,v198,v199
s_nop 1
v_permlane16_swap_b32 v196,v198
v_cvt_pk_bf16_f32 v199,v12,v13
s_nop 1
v_permlane16_swap_b32 v197,v199
buffer_store_dwordx4 v[196:199],v40,s[20:23],s43 offen offset:256 sc1
s_or_b32 s62,s42,s40
s_cmp_lg_u32 s62,0
s_cbranch_scc1 L_noep2_%=
v_mov_b32 v12,s68
s_mov_b64 exec,1
buffer_store_dword v12,v25,s[64:67],0 offen sc1
s_mov_b64 exec,-1
L_noep2_%=:
s_endpgm
L_ps2_%=:
s_min_i32 s79,s79,100
s_add_u32 s78,s78,s79
L_pl2_%=:
s_sleep 1
s_sub_u32 s79,s79,4
s_cmp_gt_i32 s79,1
s_cbranch_scc1 L_pl2_%=
s_branch L_pr2_%=
L_ps3_%=:
s_min_i32 s79,s79,100
s_add_u32 s78,s78,s79
L_pl3_%=:
s_sleep 1
s_sub_u32 s79,s79,4
s_cmp_gt_i32 s79,1
s_cbranch_scc1 L_pl3_%=
s_branch L_pr3_%=
L_ps4_%=:
s_min_i32 s79,s79,100
s_add_u32 s78,s78,s79
L_pl4_%=:
s_sleep 1
s_sub_u32 s79,s79,4
s_cmp_gt_i32 s79,1
s_cbranch_scc1 L_pl4_%=
s_branch L_pr4_%=
L_ps5_%=:
s_min_i32 s79,s79,100
s_add_u32 s78,s78,s79
L_pl5_%=:
s_sleep 1
s_sub_u32 s79,s79,4
s_cmp_gt_i32 s79,1
s_cbranch_scc1 L_pl5_%=
s_branch L_pr5_%=
L_ps6_%=:
s_min_i32 s79,s79,100
s_add_u32 s78,s78,s79
L_pl6_%=:
s_sleep 1
s_sub_u32 s79,s79,4
s_cmp_gt_i32 s79,1
s_cbranch_scc1 L_pl6_%=
s_branch L_pr6_%=
L_ps7_%=:
s_min_i32 s79,s79,100
s_add_u32 s78,s78,s79
L_pl7_%=:
s_sleep 1
s_sub_u32 s79,s79,4
s_cmp_gt_i32 s79,1
s_cbranch_scc1 L_pl7_%=
s_branch L_pr7_%=
L_fr8_%=:
s_mov_b32 s88,262144
L_fl8_%=:
buffer_load_dwordx2 v[4:5],v24,s[64:67],s94 offen sc1
s_waitcnt vmcnt(0)
s_sub_u32 s88,s88,1
s_cmp_eq_u32 s88,0
s_cbranch_scc1 L_fo8_%=
v_cmp_ne_u32 s[90:91],s68,v4
v_and_b32 v4,-2,v5
v_cmp_ne_u32 s[92:93],s69,v4
s_nop 0
s_or_b64 s[90:91],s[90:91],s[92:93]
s_cbranch_scc1 L_fl8_%=
s_branch L_fo8_%=
L_fr9_%=:
s_mov_b32 s88,262144
L_fl9_%=:
buffer_load_dwordx2 v[4:5],v24,s[64:67],s94 offen offset:1024 sc1
s_waitcnt vmcnt(0)
s_sub_u32 s88,s88,1
s_cmp_eq_u32 s88,0
s_cbranch_scc1 L_fo9_%=
v_cmp_ne_u32 s[90:91],s68,v4
v_and_b32 v4,-2,v5
v_cmp_ne_u32 s[92:93],s69,v4
s_nop 0
s_or_b64 s[90:91],s[90:91],s[92:93]
s_cbranch_scc1 L_fl9_%=
s_branch L_fo9_%=
L_fr10_%=:
s_mov_b32 s88,262144
L_fl10_%=:
buffer_load_dwordx2 v[4:5],v24,s[64:67],s94 offen sc1
s_waitcnt vmcnt(0)
s_sub_u32 s88,s88,1
s_cmp_eq_u32 s88,0
s_cbranch_scc1 L_fo10_%=
v_cmp_ne_u32 s[90:91],s68,v4
v_and_b32 v4,-2,v5
v_cmp_ne_u32 s[92:93],s69,v4
s_nop 0
s_or_b64 s[90:91],s[90:91],s[92:93]
s_cbranch_scc1 L_fl10_%=
s_branch L_fo10_%=
L_fr11_%=:
s_mov_b32 s88,262144
L_fl11_%=:
buffer_load_dwordx2 v[4:5],v24,s[64:67],s94 offen offset:2048 sc1
s_waitcnt vmcnt(0)
s_sub_u32 s88,s88,1
s_cmp_eq_u32 s88,0
s_cbranch_scc1 L_fo11_%=
v_cmp_ne_u32 s[90:91],s68,v4
v_and_b32 v4,-2,v5
v_cmp_ne_u32 s[92:93],s69,v4
s_nop 0
s_or_b64 s[90:91],s[90:91],s[92:93]
s_cbranch_scc1 L_fl11_%=
s_branch L_fo11_%=
L_fr12_%=:
s_mov_b32 s88,262144
L_fl12_%=:
buffer_load_dwordx2 v[4:5],v24,s[64:67],s94 offen offset:3072 sc1
s_waitcnt vmcnt(0)
s_sub_u32 s88,s88,1
s_cmp_eq_u32 s88,0
s_cbranch_scc1 L_fo12_%=
v_cmp_ne_u32 s[90:91],s68,v4
v_and_b32 v4,-2,v5
v_cmp_ne_u32 s[92:93],s69,v4
s_nop 0
s_or_b64 s[90:91],s[90:91],s[92:93]
s_cbranch_scc1 L_fl12_%=
s_branch L_fo12_%=
L_out_end_%=:
L_fast_end_%=:
L_slow0_%=:
s_mov_b32 s98,0
s_branch L_slowc_%=
L_slow1_%=:
s_mov_b32 s98,1
L_slowc_%=:
s_nop 7
s_nop 7
s_waitcnt vmcnt(0) lgkmcnt(0)
s_lshl_b32 s96,%1,2
s_add_u32 s96,s96,s40
s_lshl_b32 s96,s96,15
s_add_u32 s96,s96,167772160
s_mov_b64 s[56:57],s[12:13]
s_add_u32 s56,s56,s96
s_addc_u32 s57,s57,0
s_and_b32 s57,s57,0xffff
s_mov_b32 s58,32768
s_mov_b32 s59,0x20000
s_mov_b32 s96,0
buffer_store_dwordx4 v[64:67],v6,s[56:59],s96 offen
buffer_store_dwordx4 v[68:71],v6,s[56:59],s96 offen offset:1024
buffer_store_dwordx4 v[72:75],v6,s[56:59],s96 offen offset:2048
buffer_store_dwordx4 v[76:79],v6,s[56:59],s96 offen offset:3072
s_movk_i32 s96,4096
buffer_store_dwordx4 v[80:83],v6,s[56:59],s96 offen
buffer_store_dwordx4 v[84:87],v6,s[56:59],s96 offen offset:1024
buffer_store_dwordx4 v[88:91],v6,s[56:59],s96 offen offset:2048
buffer_store_dwordx4 v[92:95],v6,s[56:59],s96 offen offset:3072
s_movk_i32 s96,8192
buffer_store_dwordx4 v[96:99],v6,s[56:59],s96 offen
buffer_store_dwordx4 v[100:103],v6,s[56:59],s96 offen offset:1024
buffer_store_dwordx4 v[104:107],v6,s[56:59],s96 offen offset:2048
buffer_store_dwordx4 v[108:111],v6,s[56:59],s96 offen offset:3072
s_movk_i32 s96,12288
buffer_store_dwordx4 v[112:115],v6,s[56:59],s96 offen
buffer_store_dwordx4 v[116:119],v6,s[56:59],s96 offen offset:1024
buffer_store_dwordx4 v[120:123],v6,s[56:59],s96 offen offset:2048
buffer_store_dwordx4 v[124:127],v6,s[56:59],s96 offen offset:3072
s_movk_i32 s96,16384
buffer_store_dwordx4 v[128:131],v6,s[56:59],s96 offen
buffer_store_dwordx4 v[132:135],v6,s[56:59],s96 offen offset:1024
buffer_store_dwordx4 v[136:139],v6,s[56:59],s96 offen offset:2048
buffer_store_dwordx4 v[140:143],v6,s[56:59],s96 offen offset:3072
s_movk_i32 s96,20480
buffer_store_dwordx4 v[144:147],v6,s[56:59],s96 offen
buffer_store_dwordx4 v[148:151],v6,s[56:59],s96 offen offset:1024
buffer_store_dwordx4 v[152:155],v6,s[56:59],s96 offen offset:2048
buffer_store_dwordx4 v[156:159],v6,s[56:59],s96 offen offset:3072
s_movk_i32 s96,24576
buffer_store_dwordx4 v[160:163],v6,s[56:59],s96 offen
buffer_store_dwordx4 v[164:167],v6,s[56:59],s96 offen offset:1024
buffer_store_dwordx4 v[168:171],v6,s[56:59],s96 offen offset:2048
buffer_store_dwordx4 v[172:175],v6,s[56:59],s96 offen offset:3072
s_movk_i32 s96,28672
buffer_store_dwordx4 v[176:179],v6,s[56:59],s96 offen
buffer_store_dwordx4 v[180:183],v6,s[56:59],s96 offen offset:1024
buffer_store_dwordx4 v[184:187],v6,s[56:59],s96 offen offset:2048
buffer_store_dwordx4 v[188:191],v6,s[56:59],s96 offen offset:3072
s_waitcnt vmcnt(0)
v_writelane_b32 v173,s88,0
v_writelane_b32 v173,s89,1
v_writelane_b32 v173,s90,2
v_writelane_b32 v173,s91,3
v_writelane_b32 v173,s92,4
v_writelane_b32 v173,s93,5
v_writelane_b32 v173,s94,6
v_writelane_b32 v173,s95,7
s_mov_b32 s88,s98
s_mul_i32 s90,s41,307200
s_mov_b64 s[92:93],s[4:5]
s_add_u32 s92,s92,s90
s_addc_u32 s93,s93,0
s_and_b32 s93,s93,0xffff
s_mov_b32 s94,307200
s_mov_b32 s95,0x20000
s_mov_b64 s[96:97],s[10:11]
s_and_b32 s97,s97,0xffff
s_mov_b32 s98,5242880
s_mov_b32 s99,0x20000
v_bfe_u32 v171,v6,4,4
v_lshrrev_b32 v172,8,v6
v_lshlrev_b32 v164,8,v171
v_lshl_add_u32 v164,v172,4,v164
v_lshlrev_b32 v165,10,v172
v_lshl_add_u32 v165,v171,1,v165
v_add_u32 v166,4096,v165
v_lshlrev_b32 v168,16,v172
v_lshrrev_b32 v170,2,v171
v_lshl_add_u32 v168,v170,4,v168
v_and_b32 v170,3,v171
v_lshl_add_u32 v168,v170,1,v168
s_lshl_b32 s90,s42,1
s_add_u32 s90,s90,s88
s_lshl_b32 s90,s90,8
v_add_u32 v168,s90,v168
s_mov_b32 s89,0
L_sjb_%=:
s_add_u32 s90,s89,s70
s_add_u32 s90,s90,1
s_and_b32 s90,s90,3
s_lshl_b32 s91,s40,2
s_add_u32 s90,s90,s91
s_lshl_b32 s90,s90,4
v_bfe_u32 v171,v6,4,4
v_lshrrev_b32 v172,8,v6
v_add_u32 v167,s90,v171
v_mul_u32_u24 v167,1280,v167
v_lshl_add_u32 v167,v172,4,v167
s_lshl_b32 s90,s88,5
s_add_u32 s90,s90,s89
s_and_b32 s91,s89,2
s_add_u32 s90,s90,s91
s_lshl_b32 s90,s90,12
v_add_u32 v169,s90,v6
v_mov_b32 v64,0
v_mov_b32 v65,0
v_mov_b32 v66,0
v_mov_b32 v67,0
v_mov_b32 v68,0
v_mov_b32 v69,0
v_mov_b32 v70,0
v_mov_b32 v71,0
v_mov_b32 v72,0
v_mov_b32 v73,0
v_mov_b32 v74,0
v_mov_b32 v75,0
v_mov_b32 v76,0
v_mov_b32 v77,0
v_mov_b32 v78,0
v_mov_b32 v79,0
v_mov_b32 v80,0
v_mov_b32 v81,0
v_mov_b32 v82,0
v_mov_b32 v83,0
v_mov_b32 v84,0
v_mov_b32 v85,0
v_mov_b32 v86,0
v_mov_b32 v87,0
v_mov_b32 v88,0
v_mov_b32 v89,0
v_mov_b32 v90,0
v_mov_b32 v91,0
v_mov_b32 v92,0
v_mov_b32 v93,0
v_mov_b32 v94,0
v_mov_b32 v95,0
s_mov_b32 s90,0
L_sq_%=:
s_lshl_b32 s91,s90,6
buffer_load_dwordx4 v[160:163],v167,s[92:95],s91 offen
s_lshl_b32 s91,s90,18
buffer_load_ushort v112,v168,s[96:99],s91 offen
buffer_load_ushort v120,v168,s[96:99],s91 offen offset:8
buffer_load_ushort v128,v168,s[96:99],s91 offen offset:64
buffer_load_ushort v136,v168,s[96:99],s91 offen offset:72
s_addk_i32 s91,8192
buffer_load_ushort v113,v168,s[96:99],s91 offen
buffer_load_ushort v121,v168,s[96:99],s91 offen offset:8
buffer_load_ushort v129,v168,s[96:99],s91 offen offset:64
buffer_load_ushort v137,v168,s[96:99],s91 offen offset:72
s_addk_i32 s91,8192
buffer_load_ushort v114,v168,s[96:99],s91 offen
buffer_load_ushort v122,v168,s[96:99],s91 offen offset:8
buffer_load_ushort v130,v168,s[96:99],s91 offen offset:64
buffer_load_ushort v138,v168,s[96:99],s91 offen offset:72
s_addk_i32 s91,8192
buffer_load_ushort v115,v168,s[96:99],s91 offen
buffer_load_ushort v123,v168,s[96:99],s91 offen offset:8
buffer_load_ushort v131,v168,s[96:99],s91 offen offset:64
buffer_load_ushort v139,v168,s[96:99],s91 offen offset:72
s_addk_i32 s91,8192
buffer_load_ushort v116,v168,s[96:99],s91 offen
buffer_load_ushort v124,v168,s[96:99],s91 offen offset:8
buffer_load_ushort v132,v168,s[96:99],s91 offen offset:64
buffer_load_ushort v140,v168,s[96:99],s91 offen offset:72
s_addk_i32 s91,8192
buffer_load_ushort v117,v168,s[96:99],s91 offen
buffer_load_ushort v125,v168,s[96:99],s91 offen offset:8
buffer_load_ushort v133,v168,s[96:99],s91 offen offset:64
buffer_load_ushort v141,v168,s[96:99],s91 offen offset:72
s_addk_i32 s91,8192
buffer_load_ushort v118,v168,s[96:99],s91 offen
buffer_load_ushort v126,v168,s[96:99],s91 offen offset:8
buffer_load_ushort v134,v168,s[96:99],s91 offen offset:64
buffer_load_ushort v142,v168,s[96:99],s91 offen offset:72
s_addk_i32 s91,8192
buffer_load_ushort v119,v168,s[96:99],s91 offen
buffer_load_ushort v127,v168,s[96:99],s91 offen offset:8
buffer_load_ushort v135,v168,s[96:99],s91 offen offset:64
buffer_load_ushort v143,v168,s[96:99],s91 offen offset:72
s_addk_i32 s91,8192
s_waitcnt vmcnt(0)
v_lshl_or_b32 v112,v113,16,v112
v_lshl_or_b32 v113,v115,16,v114
)A"
R"A(v_lshl_or_b32 v114,v117,16,v116
v_lshl_or_b32 v115,v119,16,v118
v_lshl_or_b32 v120,v121,16,v120
v_lshl_or_b32 v121,v123,16,v122
v_lshl_or_b32 v122,v125,16,v124
v_lshl_or_b32 v123,v127,16,v126
v_lshl_or_b32 v128,v129,16,v128
v_lshl_or_b32 v129,v131,16,v130
v_lshl_or_b32 v130,v133,16,v132
v_lshl_or_b32 v131,v135,16,v134
v_lshl_or_b32 v136,v137,16,v136
v_lshl_or_b32 v137,v139,16,v138
v_lshl_or_b32 v138,v141,16,v140
v_lshl_or_b32 v139,v143,16,v142
s_nop 1
v_mfma_f32_16x16x32_bf16 v[64:67],v[112:115],v[160:163],v[64:67]
v_mfma_f32_16x16x32_bf16 v[68:71],v[120:123],v[160:163],v[68:71]
v_mfma_f32_16x16x32_bf16 v[72:75],v[128:131],v[160:163],v[72:75]
v_mfma_f32_16x16x32_bf16 v[76:79],v[136:139],v[160:163],v[76:79]
s_nop 7
s_lshl_b32 s91,s90,18
buffer_load_ushort v112,v168,s[96:99],s91 offen offset:128
buffer_load_ushort v120,v168,s[96:99],s91 offen offset:136
buffer_load_ushort v128,v168,s[96:99],s91 offen offset:192
buffer_load_ushort v136,v168,s[96:99],s91 offen offset:200
s_addk_i32 s91,8192
buffer_load_ushort v113,v168,s[96:99],s91 offen offset:128
buffer_load_ushort v121,v168,s[96:99],s91 offen offset:136
buffer_load_ushort v129,v168,s[96:99],s91 offen offset:192
buffer_load_ushort v137,v168,s[96:99],s91 offen offset:200
s_addk_i32 s91,8192
buffer_load_ushort v114,v168,s[96:99],s91 offen offset:128
buffer_load_ushort v122,v168,s[96:99],s91 offen offset:136
buffer_load_ushort v130,v168,s[96:99],s91 offen offset:192
buffer_load_ushort v138,v168,s[96:99],s91 offen offset:200
s_addk_i32 s91,8192
buffer_load_ushort v115,v168,s[96:99],s91 offen offset:128
buffer_load_ushort v123,v168,s[96:99],s91 offen offset:136
buffer_load_ushort v131,v168,s[96:99],s91 offen offset:192
buffer_load_ushort v139,v168,s[96:99],s91 offen offset:200
s_addk_i32 s91,8192
buffer_load_ushort v116,v168,s[96:99],s91 offen offset:128
buffer_load_ushort v124,v168,s[96:99],s91 offen offset:136
buffer_load_ushort v132,v168,s[96:99],s91 offen offset:192
buffer_load_ushort v140,v168,s[96:99],s91 offen offset:200
s_addk_i32 s91,8192
buffer_load_ushort v117,v168,s[96:99],s91 offen offset:128
buffer_load_ushort v125,v168,s[96:99],s91 offen offset:136
buffer_load_ushort v133,v168,s[96:99],s91 offen offset:192
buffer_load_ushort v141,v168,s[96:99],s91 offen offset:200
s_addk_i32 s91,8192
buffer_load_ushort v118,v168,s[96:99],s91 offen offset:128
buffer_load_ushort v126,v168,s[96:99],s91 offen offset:136
buffer_load_ushort v134,v168,s[96:99],s91 offen offset:192
buffer_load_ushort v142,v168,s[96:99],s91 offen offset:200
s_addk_i32 s91,8192
buffer_load_ushort v119,v168,s[96:99],s91 offen offset:128
buffer_load_ushort v127,v168,s[96:99],s91 offen offset:136
buffer_load_ushort v135,v168,s[96:99],s91 offen offset:192
buffer_load_ushort v143,v168,s[96:99],s91 offen offset:200
s_addk_i32 s91,8192
s_waitcnt vmcnt(0)
v_lshl_or_b32 v112,v113,16,v112
v_lshl_or_b32 v113,v115,16,v114
v_lshl_or_b32 v114,v117,16,v116
v_lshl_or_b32 v115,v119,16,v118
v_lshl_or_b32 v120,v121,16,v120
v_lshl_or_b32 v121,v123,16,v122
v_lshl_or_b32 v122,v125,16,v124
v_lshl_or_b32 v123,v127,16,v126
v_lshl_or_b32 v128,v129,16,v128
v_lshl_or_b32 v129,v131,16,v130
v_lshl_or_b32 v130,v133,16,v132
v_lshl_or_b32 v131,v135,16,v134
v_lshl_or_b32 v136,v137,16,v136
v_lshl_or_b32 v137,v139,16,v138
v_lshl_or_b32 v138,v141,16,v140
v_lshl_or_b32 v139,v143,16,v142
s_nop 1
v_mfma_f32_16x16x32_bf16 v[80:83],v[112:115],v[160:163],v[80:83]
v_mfma_f32_16x16x32_bf16 v[84:87],v[120:123],v[160:163],v[84:87]
v_mfma_f32_16x16x32_bf16 v[88:91],v[128:131],v[160:163],v[88:91]
v_mfma_f32_16x16x32_bf16 v[92:95],v[136:139],v[160:163],v[92:95]
s_nop 7
s_add_u32 s90,s90,1
s_cmp_lg_u32 s90,20
s_cbranch_scc1 L_sq_%=
s_nop 7
v_mul_f32 v64,s61,v64
v_mul_f32 v65,s61,v65
v_mul_f32 v66,s61,v66
v_mul_f32 v67,s61,v67
v_mul_f32 v68,s61,v68
v_mul_f32 v69,s61,v69
v_mul_f32 v70,s61,v70
v_mul_f32 v71,s61,v71
v_mul_f32 v72,s61,v72
v_mul_f32 v73,s61,v73
v_mul_f32 v74,s61,v74
v_mul_f32 v75,s61,v75
v_mul_f32 v76,s61,v76
v_mul_f32 v77,s61,v77
v_mul_f32 v78,s61,v78
v_mul_f32 v79,s61,v79
v_mul_f32 v80,s61,v80
v_mul_f32 v81,s61,v81
v_mul_f32 v82,s61,v82
v_mul_f32 v83,s61,v83
v_mul_f32 v84,s61,v84
v_mul_f32 v85,s61,v85
v_mul_f32 v86,s61,v86
v_mul_f32 v87,s61,v87
v_mul_f32 v88,s61,v88
v_mul_f32 v89,s61,v89
v_mul_f32 v90,s61,v90
v_mul_f32 v91,s61,v91
v_mul_f32 v92,s61,v92
v_mul_f32 v93,s61,v93
v_mul_f32 v94,s61,v94
v_mul_f32 v95,s61,v95
.p2align 3
v_cvt_pk_bf16_f32 v96,v64,v65
v_cvt_pk_bf16_f32 v97,v66,v67
v_cvt_pk_bf16_f32 v98,v68,v69
v_cvt_pk_bf16_f32 v99,v70,v71
v_cvt_pk_bf16_f32 v100,v72,v73
v_cvt_pk_bf16_f32 v101,v74,v75
v_cvt_pk_bf16_f32 v102,v76,v77
v_cvt_pk_bf16_f32 v103,v78,v79
v_cvt_pk_bf16_f32 v104,v80,v81
v_cvt_pk_bf16_f32 v105,v82,v83
v_cvt_pk_bf16_f32 v106,v84,v85
v_cvt_pk_bf16_f32 v107,v86,v87
v_cvt_pk_bf16_f32 v108,v88,v89
v_cvt_pk_bf16_f32 v109,v90,v91
v_cvt_pk_bf16_f32 v110,v92,v93
v_cvt_pk_bf16_f32 v111,v94,v95
v_mov_b32 v170,0xff800000
s_lshl_b32 s91,s88,18
s_mov_b32 s90,64
L_s1_%=:
buffer_load_dwordx4 v[112:115],v164,s[20:23],s91 offen
buffer_load_dwordx4 v[116:119],v164,s[20:23],s91 offen offset:64
buffer_load_dwordx4 v[120:123],v164,s[20:23],s91 offen offset:128
buffer_load_dwordx4 v[124:127],v164,s[20:23],s91 offen offset:192
s_waitcnt vmcnt(0)
v_mfma_f32_16x16x32_bf16 v[144:147],v[112:115],v[96:99],0
v_mfma_f32_16x16x32_bf16 v[144:147],v[116:119],v[100:103],v[144:147]
v_mfma_f32_16x16x32_bf16 v[144:147],v[120:123],v[104:107],v[144:147]
v_mfma_f32_16x16x32_bf16 v[144:147],v[124:127],v[108:111],v[144:147]
s_nop 7
v_max3_f32 v170,v170,v144,v145
v_max3_f32 v170,v170,v146,v147
s_addk_i32 s91,4096
s_sub_u32 s90,s90,1
s_cmp_lg_u32 s90,0
s_cbranch_scc1 L_s1_%=
v_mov_b32 v171,v170
s_nop 1
v_permlane32_swap_b32 v170,v171
v_max_f32 v170,v170,v171
v_mov_b32 v171,v170
s_nop 1
v_permlane16_swap_b32 v170,v171
v_max_f32 v170,v170,v171
v_sub_f32 v152,0,v170
v_mov_b32 v156,0
v_sub_f32 v153,0,v170
v_mov_b32 v157,0
v_sub_f32 v154,0,v170
v_mov_b32 v158,0
v_sub_f32 v155,0,v170
v_mov_b32 v159,0
v_mov_b32 v64,0
v_mov_b32 v65,0
v_mov_b32 v66,0
v_mov_b32 v67,0
v_mov_b32 v68,0
v_mov_b32 v69,0
v_mov_b32 v70,0
v_mov_b32 v71,0
v_mov_b32 v72,0
v_mov_b32 v73,0
v_mov_b32 v74,0
v_mov_b32 v75,0
v_mov_b32 v76,0
v_mov_b32 v77,0
v_mov_b32 v78,0
v_mov_b32 v79,0
v_mov_b32 v80,0
v_mov_b32 v81,0
v_mov_b32 v82,0
v_mov_b32 v83,0
v_mov_b32 v84,0
v_mov_b32 v85,0
v_mov_b32 v86,0
v_mov_b32 v87,0
v_mov_b32 v88,0
v_mov_b32 v89,0
v_mov_b32 v90,0
v_mov_b32 v91,0
v_mov_b32 v92,0
v_mov_b32 v93,0
v_mov_b32 v94,0
v_mov_b32 v95,0
s_lshl_b32 s91,s88,18
s_mov_b32 s90,32
v_add_u32 v172,4096,v164
L_s2_%=:
buffer_load_dwordx4 v[112:115],v164,s[20:23],s91 offen
buffer_load_dwordx4 v[116:119],v164,s[20:23],s91 offen offset:64
buffer_load_dwordx4 v[120:123],v164,s[20:23],s91 offen offset:128
buffer_load_dwordx4 v[124:127],v164,s[20:23],s91 offen offset:192
buffer_load_dwordx4 v[128:131],v172,s[20:23],s91 offen
buffer_load_dwordx4 v[132:135],v172,s[20:23],s91 offen offset:64
buffer_load_dwordx4 v[136:139],v172,s[20:23],s91 offen offset:128
buffer_load_dwordx4 v[140:143],v172,s[20:23],s91 offen offset:192
s_waitcnt vmcnt(0)
v_mfma_f32_16x16x32_bf16 v[144:147],v[112:115],v[96:99],v[152:155]
v_mfma_f32_16x16x32_bf16 v[144:147],v[116:119],v[100:103],v[144:147]
v_mfma_f32_16x16x32_bf16 v[144:147],v[120:123],v[104:107],v[144:147]
v_mfma_f32_16x16x32_bf16 v[144:147],v[124:127],v[108:111],v[144:147]
v_mfma_f32_16x16x32_bf16 v[148:151],v[128:131],v[96:99],v[152:155]
v_mfma_f32_16x16x32_bf16 v[148:151],v[132:135],v[100:103],v[148:151]
v_mfma_f32_16x16x32_bf16 v[148:151],v[136:139],v[104:107],v[148:151]
v_mfma_f32_16x16x32_bf16 v[148:151],v[140:143],v[108:111],v[148:151]
s_nop 7
v_exp_f32 v144,v144
v_exp_f32 v145,v145
v_exp_f32 v146,v146
v_exp_f32 v147,v147
v_exp_f32 v148,v148
v_exp_f32 v149,v149
v_exp_f32 v150,v150
v_exp_f32 v151,v151
s_nop 0
.p2align 3
v_cvt_pk_bf16_f32 v144,v144,v145
v_cvt_pk_bf16_f32 v145,v146,v147
v_cvt_pk_bf16_f32 v146,v148,v149
v_cvt_pk_bf16_f32 v147,v150,v151
buffer_load_ushort v112,v165,s[24:27],s91 offen
buffer_load_ushort v113,v165,s[24:27],s91 offen offset:256
buffer_load_ushort v114,v165,s[24:27],s91 offen offset:512
buffer_load_ushort v115,v165,s[24:27],s91 offen offset:768
buffer_load_ushort v116,v166,s[24:27],s91 offen
buffer_load_ushort v117,v166,s[24:27],s91 offen offset:256
buffer_load_ushort v118,v166,s[24:27],s91 offen offset:512
buffer_load_ushort v119,v166,s[24:27],s91 offen offset:768
buffer_load_ushort v120,v165,s[24:27],s91 offen offset:32
buffer_load_ushort v121,v165,s[24:27],s91 offen offset:288
buffer_load_ushort v122,v165,s[24:27],s91 offen offset:544
buffer_load_ushort v123,v165,s[24:27],s91 offen offset:800
buffer_load_ushort v124,v166,s[24:27],s91 offen offset:32
buffer_load_ushort v125,v166,s[24:27],s91 offen offset:288
buffer_load_ushort v126,v166,s[24:27],s91 offen offset:544
buffer_load_ushort v127,v166,s[24:27],s91 offen offset:800
buffer_load_ushort v128,v165,s[24:27],s91 offen offset:64
buffer_load_ushort v129,v165,s[24:27],s91 offen offset:320
buffer_load_ushort v130,v165,s[24:27],s91 offen offset:576
buffer_load_ushort v131,v165,s[24:27],s91 offen offset:832
buffer_load_ushort v132,v166,s[24:27],s91 offen offset:64
buffer_load_ushort v133,v166,s[24:27],s91 offen offset:320
buffer_load_ushort v134,v166,s[24:27],s91 offen offset:576
buffer_load_ushort v135,v166,s[24:27],s91 offen offset:832
buffer_load_ushort v136,v165,s[24:27],s91 offen offset:96
buffer_load_ushort v137,v165,s[24:27],s91 offen offset:352
buffer_load_ushort v138,v165,s[24:27],s91 offen offset:608
buffer_load_ushort v139,v165,s[24:27],s91 offen offset:864
buffer_load_ushort v140,v166,s[24:27],s91 offen offset:96
buffer_load_ushort v141,v166,s[24:27],s91 offen offset:352
buffer_load_ushort v142,v166,s[24:27],s91 offen offset:608
buffer_load_ushort v143,v166,s[24:27],s91 offen offset:864
s_waitcnt vmcnt(0)
v_lshl_or_b32 v112,v113,16,v112
v_lshl_or_b32 v113,v115,16,v114
v_lshl_or_b32 v114,v117,16,v116
v_lshl_or_b32 v115,v119,16,v118
v_lshl_or_b32 v120,v121,16,v120
v_lshl_or_b32 v121,v123,16,v122
v_lshl_or_b32 v122,v125,16,v124
v_lshl_or_b32 v123,v127,16,v126
v_lshl_or_b32 v128,v129,16,v128
v_lshl_or_b32 v129,v131,16,v130
v_lshl_or_b32 v130,v133,16,v132
v_lshl_or_b32 v131,v135,16,v134
v_lshl_or_b32 v136,v137,16,v136
v_lshl_or_b32 v137,v139,16,v138
v_lshl_or_b32 v138,v141,16,v140
v_lshl_or_b32 v139,v143,16,v142
s_nop 1
v_mfma_f32_16x16x32_bf16 v[64:67],v[112:115],v[144:147],v[64:67]
v_mfma_f32_16x16x32_bf16 v[68:71],v[120:123],v[144:147],v[68:71]
v_mfma_f32_16x16x32_bf16 v[72:75],v[128:131],v[144:147],v[72:75]
v_mfma_f32_16x16x32_bf16 v[76:79],v[136:139],v[144:147],v[76:79]
s_nop 7
buffer_load_ushort v112,v165,s[24:27],s91 offen offset:128
buffer_load_ushort v113,v165,s[24:27],s91 offen offset:384
buffer_load_ushort v114,v165,s[24:27],s91 offen offset:640
buffer_load_ushort v115,v165,s[24:27],s91 offen offset:896
buffer_load_ushort v116,v166,s[24:27],s91 offen offset:128
buffer_load_ushort v117,v166,s[24:27],s91 offen offset:384
buffer_load_ushort v118,v166,s[24:27],s91 offen offset:640
buffer_load_ushort v119,v166,s[24:27],s91 offen offset:896
buffer_load_ushort v120,v165,s[24:27],s91 offen offset:160
buffer_load_ushort v121,v165,s[24:27],s91 offen offset:416
buffer_load_ushort v122,v165,s[24:27],s91 offen offset:672
buffer_load_ushort v123,v165,s[24:27],s91 offen offset:928
buffer_load_ushort v124,v166,s[24:27],s91 offen offset:160
buffer_load_ushort v125,v166,s[24:27],s91 offen offset:416
buffer_load_ushort v126,v166,s[24:27],s91 offen offset:672
buffer_load_ushort v127,v166,s[24:27],s91 offen offset:928
buffer_load_ushort v128,v165,s[24:27],s91 offen offset:192
buffer_load_ushort v129,v165,s[24:27],s91 offen offset:448
buffer_load_ushort v130,v165,s[24:27],s91 offen offset:704
buffer_load_ushort v131,v165,s[24:27],s91 offen offset:960
buffer_load_ushort v132,v166,s[24:27],s91 offen offset:192
buffer_load_ushort v133,v166,s[24:27],s91 offen offset:448
buffer_load_ushort v134,v166,s[24:27],s91 offen offset:704
buffer_load_ushort v135,v166,s[24:27],s91 offen offset:960
)A"
R"A(buffer_load_ushort v136,v165,s[24:27],s91 offen offset:224
buffer_load_ushort v137,v165,s[24:27],s91 offen offset:480
buffer_load_ushort v138,v165,s[24:27],s91 offen offset:736
buffer_load_ushort v139,v165,s[24:27],s91 offen offset:992
buffer_load_ushort v140,v166,s[24:27],s91 offen offset:224
buffer_load_ushort v141,v166,s[24:27],s91 offen offset:480
buffer_load_ushort v142,v166,s[24:27],s91 offen offset:736
buffer_load_ushort v143,v166,s[24:27],s91 offen offset:992
s_waitcnt vmcnt(0)
v_lshl_or_b32 v112,v113,16,v112
v_lshl_or_b32 v113,v115,16,v114
v_lshl_or_b32 v114,v117,16,v116
v_lshl_or_b32 v115,v119,16,v118
v_lshl_or_b32 v120,v121,16,v120
v_lshl_or_b32 v121,v123,16,v122
v_lshl_or_b32 v122,v125,16,v124
v_lshl_or_b32 v123,v127,16,v126
v_lshl_or_b32 v128,v129,16,v128
v_lshl_or_b32 v129,v131,16,v130
v_lshl_or_b32 v130,v133,16,v132
v_lshl_or_b32 v131,v135,16,v134
v_lshl_or_b32 v136,v137,16,v136
v_lshl_or_b32 v137,v139,16,v138
v_lshl_or_b32 v138,v141,16,v140
v_lshl_or_b32 v139,v143,16,v142
s_nop 1
v_mfma_f32_16x16x32_bf16 v[80:83],v[112:115],v[144:147],v[80:83]
v_mfma_f32_16x16x32_bf16 v[84:87],v[120:123],v[144:147],v[84:87]
v_mfma_f32_16x16x32_bf16 v[88:91],v[128:131],v[144:147],v[88:91]
v_mfma_f32_16x16x32_bf16 v[92:95],v[136:139],v[144:147],v[92:95]
s_nop 7
v_mfma_f32_16x16x32_bf16 v[156:159],v[28:31],v[144:147],v[156:159]
s_nop 7
s_addk_i32 s91,8192
s_sub_u32 s90,s90,1
s_cmp_lg_u32 s90,0
s_cbranch_scc1 L_s2_%=
v_rcp_f32 v170,v156
s_nop 0
v_mul_f32 v64,v170,v64
v_mul_f32 v65,v170,v65
v_mul_f32 v66,v170,v66
v_mul_f32 v67,v170,v67
v_mul_f32 v68,v170,v68
v_mul_f32 v69,v170,v69
v_mul_f32 v70,v170,v70
v_mul_f32 v71,v170,v71
v_mul_f32 v72,v170,v72
v_mul_f32 v73,v170,v73
v_mul_f32 v74,v170,v74
v_mul_f32 v75,v170,v75
v_mul_f32 v76,v170,v76
v_mul_f32 v77,v170,v77
v_mul_f32 v78,v170,v78
v_mul_f32 v79,v170,v79
v_mul_f32 v80,v170,v80
v_mul_f32 v81,v170,v81
v_mul_f32 v82,v170,v82
v_mul_f32 v83,v170,v83
v_mul_f32 v84,v170,v84
v_mul_f32 v85,v170,v85
v_mul_f32 v86,v170,v86
v_mul_f32 v87,v170,v87
v_mul_f32 v88,v170,v88
v_mul_f32 v89,v170,v89
v_mul_f32 v90,v170,v90
v_mul_f32 v91,v170,v91
v_mul_f32 v92,v170,v92
v_mul_f32 v93,v170,v93
v_mul_f32 v94,v170,v94
v_mul_f32 v95,v170,v95
.p2align 3
v_cvt_pk_bf16_f32 v96,v64,v65
v_cvt_pk_bf16_f32 v97,v66,v67
v_cvt_pk_bf16_f32 v98,v68,v69
v_cvt_pk_bf16_f32 v99,v70,v71
v_cvt_pk_bf16_f32 v100,v72,v73
v_cvt_pk_bf16_f32 v101,v74,v75
v_cvt_pk_bf16_f32 v102,v76,v77
v_cvt_pk_bf16_f32 v103,v78,v79
v_cvt_pk_bf16_f32 v104,v80,v81
v_cvt_pk_bf16_f32 v105,v82,v83
v_cvt_pk_bf16_f32 v106,v84,v85
v_cvt_pk_bf16_f32 v107,v86,v87
v_cvt_pk_bf16_f32 v108,v88,v89
v_cvt_pk_bf16_f32 v109,v90,v91
v_cvt_pk_bf16_f32 v110,v92,v93
v_cvt_pk_bf16_f32 v111,v94,v95
buffer_store_dwordx4 v[96:99],v169,s[36:39],s55 offen sc1
buffer_store_dwordx4 v[100:103],v169,s[36:39],s55 offen offset:1024 sc1
buffer_store_dwordx4 v[104:107],v169,s[36:39],s55 offen offset:2048 sc1
buffer_store_dwordx4 v[108:111],v169,s[36:39],s55 offen offset:3072 sc1
s_waitcnt vmcnt(0)
s_add_u32 s89,s89,1
s_cmp_lg_u32 s89,4
s_cbranch_scc1 L_sjb_%=
s_mov_b32 s98,s88
v_readlane_b32 s88,v173,0
v_readlane_b32 s89,v173,1
v_readlane_b32 s90,v173,2
v_readlane_b32 s91,v173,3
v_readlane_b32 s92,v173,4
v_readlane_b32 s93,v173,5
v_readlane_b32 s94,v173,6
v_readlane_b32 s95,v173,7
s_nop 4
s_mov_b32 s96,0
buffer_load_dwordx4 v[64:67],v6,s[56:59],s96 offen sc1
buffer_load_dwordx4 v[68:71],v6,s[56:59],s96 offen offset:1024 sc1
buffer_load_dwordx4 v[72:75],v6,s[56:59],s96 offen offset:2048 sc1
buffer_load_dwordx4 v[76:79],v6,s[56:59],s96 offen offset:3072 sc1
s_movk_i32 s96,4096
buffer_load_dwordx4 v[80:83],v6,s[56:59],s96 offen sc1
buffer_load_dwordx4 v[84:87],v6,s[56:59],s96 offen offset:1024 sc1
buffer_load_dwordx4 v[88:91],v6,s[56:59],s96 offen offset:2048 sc1
buffer_load_dwordx4 v[92:95],v6,s[56:59],s96 offen offset:3072 sc1
s_movk_i32 s96,8192
buffer_load_dwordx4 v[96:99],v6,s[56:59],s96 offen sc1
buffer_load_dwordx4 v[100:103],v6,s[56:59],s96 offen offset:1024 sc1
buffer_load_dwordx4 v[104:107],v6,s[56:59],s96 offen offset:2048 sc1
buffer_load_dwordx4 v[108:111],v6,s[56:59],s96 offen offset:3072 sc1
s_movk_i32 s96,12288
buffer_load_dwordx4 v[112:115],v6,s[56:59],s96 offen sc1
buffer_load_dwordx4 v[116:119],v6,s[56:59],s96 offen offset:1024 sc1
buffer_load_dwordx4 v[120:123],v6,s[56:59],s96 offen offset:2048 sc1
buffer_load_dwordx4 v[124:127],v6,s[56:59],s96 offen offset:3072 sc1
s_movk_i32 s96,16384
buffer_load_dwordx4 v[128:131],v6,s[56:59],s96 offen sc1
buffer_load_dwordx4 v[132:135],v6,s[56:59],s96 offen offset:1024 sc1
buffer_load_dwordx4 v[136:139],v6,s[56:59],s96 offen offset:2048 sc1
buffer_load_dwordx4 v[140:143],v6,s[56:59],s96 offen offset:3072 sc1
s_movk_i32 s96,20480
buffer_load_dwordx4 v[144:147],v6,s[56:59],s96 offen sc1
buffer_load_dwordx4 v[148:151],v6,s[56:59],s96 offen offset:1024 sc1
buffer_load_dwordx4 v[152:155],v6,s[56:59],s96 offen offset:2048 sc1
buffer_load_dwordx4 v[156:159],v6,s[56:59],s96 offen offset:3072 sc1
s_movk_i32 s96,24576
buffer_load_dwordx4 v[160:163],v6,s[56:59],s96 offen sc1
buffer_load_dwordx4 v[164:167],v6,s[56:59],s96 offen offset:1024 sc1
buffer_load_dwordx4 v[168:171],v6,s[56:59],s96 offen offset:2048 sc1
buffer_load_dwordx4 v[172:175],v6,s[56:59],s96 offen offset:3072 sc1
s_movk_i32 s96,28672
buffer_load_dwordx4 v[176:179],v6,s[56:59],s96 offen sc1
buffer_load_dwordx4 v[180:183],v6,s[56:59],s96 offen offset:1024 sc1
buffer_load_dwordx4 v[184:187],v6,s[56:59],s96 offen offset:2048 sc1
buffer_load_dwordx4 v[188:191],v6,s[56:59],s96 offen offset:3072 sc1
s_waitcnt vmcnt(0)
s_cmp_lg_u32 s98,0
s_cbranch_scc1 L_sr1_%=
buffer_load_dwordx4 v[64:67],v6,s[36:39],s84 offen sc1
buffer_load_dwordx4 v[68:71],v6,s[36:39],s84 offen offset:1024 sc1
buffer_load_dwordx4 v[72:75],v6,s[36:39],s84 offen offset:2048 sc1
buffer_load_dwordx4 v[76:79],v6,s[36:39],s84 offen offset:3072 sc1
buffer_load_dwordx4 v[80:83],v6,s[36:39],s85 offen sc1
buffer_load_dwordx4 v[84:87],v6,s[36:39],s85 offen offset:1024 sc1
buffer_load_dwordx4 v[88:91],v6,s[36:39],s85 offen offset:2048 sc1
buffer_load_dwordx4 v[92:95],v6,s[36:39],s85 offen offset:3072 sc1
buffer_load_dwordx4 v[96:99],v6,s[36:39],s86 offen sc1
buffer_load_dwordx4 v[100:103],v6,s[36:39],s86 offen offset:1024 sc1
buffer_load_dwordx4 v[104:107],v6,s[36:39],s86 offen offset:2048 sc1
buffer_load_dwordx4 v[108:111],v6,s[36:39],s86 offen offset:3072 sc1
buffer_load_dwordx4 v[112:115],v6,s[36:39],s87 offen sc1
buffer_load_dwordx4 v[116:119],v6,s[36:39],s87 offen offset:1024 sc1
buffer_load_dwordx4 v[120:123],v6,s[36:39],s87 offen offset:2048 sc1
buffer_load_dwordx4 v[124:127],v6,s[36:39],s87 offen offset:3072 sc1
s_waitcnt vmcnt(0)
s_nop 7
s_branch L_sret0_%=
L_sr1_%=:
buffer_load_dwordx4 v[136:139],v6,s[36:39],s84 offen sc1
buffer_load_dwordx4 v[140:143],v6,s[36:39],s84 offen offset:1024 sc1
buffer_load_dwordx4 v[152:155],v6,s[36:39],s84 offen offset:2048 sc1
buffer_load_dwordx4 v[156:159],v6,s[36:39],s84 offen offset:3072 sc1
buffer_load_dwordx4 v[168:171],v6,s[36:39],s85 offen sc1
buffer_load_dwordx4 v[172:175],v6,s[36:39],s85 offen offset:1024 sc1
buffer_load_dwordx4 v[184:187],v6,s[36:39],s85 offen offset:2048 sc1
buffer_load_dwordx4 v[188:191],v6,s[36:39],s85 offen offset:3072 sc1
buffer_load_dwordx4 v[200:203],v6,s[36:39],s86 offen sc1
buffer_load_dwordx4 v[204:207],v6,s[36:39],s86 offen offset:1024 sc1
buffer_load_dwordx4 v[216:219],v6,s[36:39],s86 offen offset:2048 sc1
buffer_load_dwordx4 v[220:223],v6,s[36:39],s86 offen offset:3072 sc1
buffer_load_dwordx4 v[232:235],v6,s[36:39],s87 offen sc1
buffer_load_dwordx4 v[236:239],v6,s[36:39],s87 offen offset:1024 sc1
buffer_load_dwordx4 v[248:251],v6,s[36:39],s87 offen offset:2048 sc1
buffer_load_dwordx4 v[252:255],v6,s[36:39],s87 offen offset:3072 sc1
s_waitcnt vmcnt(0)
s_nop 7
s_branch L_sret1_%=
L_end_%=:
)A"
    :: "s"(karg), "s"(blk), "s"(wid)
    : "v0", "v1", "v2", "v3", "v4", "v5", "v6", "v7", "v8", "v9", "v10", "v11", "v12", "v13", "v14", "v15", "v16", "v17", "v18", "v19", "v20", "v21", "v22", "v23", "v24", "v25", "v26", "v27", "v28", "v29", "v30", "v31", "v32", "v33", "v34", "v35", "v36", "v37", "v38", "v39", "v40", "v41", "v42", "v43", "v44", "v45", "v46", "v47", "v48", "v49", "v50", "v51", "v52", "v53", "v54", "v55", "v56", "v57", "v58", "v59", "v60", "v61", "v62", "v63", "v64", "v65", "v66", "v67", "v68", "v69", "v70", "v71", "v72", "v73", "v74", "v75", "v76", "v77", "v78", "v79", "v80", "v81", "v82", "v83", "v84", "v85", "v86", "v87", "v88", "v89", "v90", "v91", "v92", "v93", "v94", "v95", "v96", "v97", "v98", "v99", "v100", "v101", "v102", "v103", "v104", "v105", "v106", "v107", "v108", "v109", "v110", "v111", "v112", "v113", "v114", "v115", "v116", "v117", "v118", "v119", "v120", "v121", "v122", "v123", "v124", "v125", "v126", "v127", "v128", "v129", "v130", "v131", "v132", "v133", "v134", "v135", "v136", "v137", "v138", "v139", "v140", "v141", "v142", "v143", "v144", "v145", "v146", "v147", "v148", "v149", "v150", "v151", "v152", "v153", "v154", "v155", "v156", "v157", "v158", "v159", "v160", "v161", "v162", "v163", "v164", "v165", "v166", "v167", "v168", "v169", "v170", "v171", "v172", "v173", "v174", "v175", "v176", "v177", "v178", "v179", "v180", "v181", "v182", "v183", "v184", "v185", "v186", "v187", "v188", "v189", "v190", "v191", "v192", "v193", "v194", "v195", "v196", "v197", "v198", "v199", "v200", "v201", "v202", "v203", "v204", "v205", "v206", "v207", "v208", "v209", "v210", "v211", "v212", "v213", "v214", "v215", "v216", "v217", "v218", "v219", "v220", "v221", "v222", "v223", "v224", "v225", "v226", "v227", "v228", "v229", "v230", "v231", "v232", "v233", "v234", "v235", "v236", "v237", "v238", "v239", "v240", "v241", "v242", "v243", "v244", "v245", "v246", "v247", "v248", "v249", "v250", "v251", "v252", "v253", "v254", "v255", "a0", "a1", "a2", "a3", "a4", "a5", "a6", "a7", "a8", "a9", "a10", "a11", "a12", "a13", "a14", "a15", "a16", "a17", "a18", "a19", "a20", "a21", "a22", "a23", "a24", "a25", "a26", "a27", "a28", "a29", "a30", "a31", "a32", "a33", "a34", "a35", "a36", "a37", "a38", "a39", "a40", "a41", "a42", "a43", "a44", "a45", "a46", "a47", "a48", "a49", "a50", "a51", "a52", "a53", "a54", "a55", "a56", "a57", "a58", "a59", "a60", "a61", "a62", "a63", "a64", "a65", "a66", "a67", "a68", "a69", "a70", "a71", "a72", "a73", "a74", "a75", "a76", "a77", "a78", "a79", "a80", "a81", "a82", "a83", "a84", "a85", "a86", "a87", "a88", "a89", "a90", "a91", "a92", "a93", "a94", "a95", "a96", "a97", "a98", "a99", "a100", "a101", "a102", "a103", "a104", "a105", "a106", "a107", "a108", "a109", "a110", "a111", "a112", "a113", "a114", "a115", "a116", "a117", "a118", "a119", "a120", "a121", "a122", "a123", "a124", "a125", "a126", "a127", "a128", "a129", "a130", "a131", "a132", "a133", "a134", "a135", "a136", "a137", "a138", "a139", "a140", "a141", "a142", "a143", "a144", "a145", "a146", "a147", "a148", "a149", "a150", "a151", "a152", "a153", "a154", "a155", "a156", "a157", "a158", "a159", "a160", "a161", "a162", "a163", "a164", "a165", "a166", "a167", "a168", "a169", "a170", "a171", "a172", "a173", "a174", "a175", "a176", "a177", "a178", "a179", "a180", "a181", "a182", "a183", "a184", "a185", "a186", "a187", "a188", "a189", "a190", "a191", "a192", "a193", "a194", "a195", "a196", "a197", "a198", "a199", "a200", "a201", "a202", "a203", "a204", "a205", "a206", "a207", "a208", "a209", "a210", "a211", "a212", "a213", "a214", "a215", "a216", "a217", "a218", "a219", "a220", "a221", "a222", "a223", "a224", "a225", "a226", "a227", "a228", "a229", "a230", "a231", "a232", "a233", "a234", "a235", "a236", "a237", "a238", "a239", "a240", "a241", "a242", "a243", "a244", "a245", "a246", "a247", "a248", "a249", "a250", "a251", "a252", "a253", "a254", "a255", "s4", "s5", "s6", "s7", "s8", "s9", "s10", "s11", "s12", "s13", "s14", "s15", "s16", "s17", "s18", "s19", "s20", "s21", "s22", "s23", "s24", "s25", "s26", "s27", "s28", "s29", "s30", "s31", "s32", "s33", "s34", "s35", "s36", "s37", "s38", "s39", "s40", "s41", "s42", "s43", "s44", "s45", "s46", "s47", "s48", "s49", "s50", "s51", "s52", "s53", "s54", "s55", "s56", "s57", "s58", "s59", "s60", "s61", "s62", "s63", "s64", "s65", "s66", "s67", "s68", "s69", "s70", "s71", "s72", "s73", "s74", "s75", "s76", "s77", "s78", "s79", "s80", "s81", "s82", "s83", "s84", "s85", "s86", "s87", "s88", "s89", "s90", "s91", "s92", "s93", "s94", "s95", "s96", "s97", "s98", "s99", "s100", "s101", "vcc", "scc", "memory");
#endif
}

extern "C" void kernel_launch(void* const* d_in, const int* in_sizes, int n_in,
                              void* d_out, int out_size, void* d_ws, size_t ws_size,
                              hipStream_t stream) {
  (void)hipFuncSetAttribute((const void*)qattn_kernel, hipFuncAttributeMaxDynamicSharedMemorySize, LDS_ATT);
  qattn_kernel<<<256, 256, LDS_ATT, stream>>>(d_in[0], d_in[1], d_in[2], d_in[3], d_ws, d_in[4], d_out);
}
```

```cpp
#include <hip/hip_runtime.h>
#include <hip/hip_bf16.h>

constexpr int LDS_ATT = 163840;

__global__ __launch_bounds__(256, 1) void qattn_kernel(const void* __restrict__ x, const void* __restrict__ kc, const void* __restrict__ vc,
                                                       const void* __restrict__ wq, void* __restrict__ ws, const void* __restrict__ wo,
                                                       void* __restrict__ out) {
#if defined(__HIP_DEVICE_COMPILE__)
  const auto karg = __builtin_amdgcn_kernarg_segment_ptr();
  const unsigned blk = blockIdx.x;
  const unsigned wid = __builtin_amdgcn_readfirstlane(threadIdx.x >> 6);
  asm volatile(
R"A(s_memtime s[72:73]
s_memrealtime s[74:75]
s_load_dwordx8 s[4:11],%0,0x0
s_load_dwordx4 s[12:15],%0,0x20
s_load_dwordx2 s[16:17],%0,0x30
s_mov_b32 s40,%2
s_mov_b32 s43,%1
s_lshr_b32 s45,s43,3
v_mbcnt_lo_u32_b32 v12,-1,0
v_mbcnt_hi_u32_b32 v12,-1,v12
s_getpc_b64 s[88:89]
L_pc_%=:
s_and_b32 s88,s88,0xffffff80
s_and_b32 s89,s89,0xffff
s_mov_b32 s90,L_fast_end_%=-L_pc_%=
s_mov_b32 s91,0x20000
s_lshl_b32 s44,s45,2
s_add_u32 s44,s44,s40
s_mulk_i32 s44,896
v_lshlrev_b32 v67,7,v12
s_mov_b64 exec,127
buffer_load_dword v19,v67,s[88:91],s44 offen
s_mov_b64 exec,-1
s_and_b32 s70,s43,3
s_and_b32 s71,s45,3
s_lshl_b32 s42,s70,2
s_add_u32 s42,s42,s71
s_and_b32 s41,s43,4
s_lshl_b32 s41,s41,1
s_lshr_b32 s46,s45,2
s_add_u32 s41,s41,s46
s_lshl_b32 s56,s42,9
s_mov_b32 s57,0
s_lshl_b32 s59,s40,13
s_add_u32 s58,s59,65536
v_and_b32 v64,3,v12
v_bfe_u32 v65,v12,2,2
v_lshrrev_b32 v66,4,v12
s_lshl_b32 s44,s40,17
v_lshlrev_b32 v75,13,v65
v_lshl_add_u32 v75,v66,6,v75
v_lshl_add_u32 v75,v64,4,v75
v_add_u32 v75,s44,v75
v_add_u32 v56,0,v75
v_add_u32 v57,256,v75
v_add_u32 v58,32768,v75
v_add_u32 v59,33024,v75
v_add_u32 v60,65536,v75
v_add_u32 v61,65792,v75
v_add_u32 v62,98304,v75
v_add_u32 v63,98560,v75
s_waitcnt lgkmcnt(0)
s_mov_b64 s[32:33],s[10:11]
s_and_b32 s33,s33,0xffff
s_mov_b32 s34,5242880
s_mov_b32 s35,0x20000
s_add_u32 m0,s58,1024
s_nop 0
buffer_load_dwordx4 v57,s[32:35],s56 offen lds
s_add_u32 m0,s58,3072
s_nop 0
buffer_load_dwordx4 v59,s[32:35],s56 offen lds
s_add_u32 m0,s58,5120
s_nop 0
buffer_load_dwordx4 v61,s[32:35],s56 offen lds
s_add_u32 m0,s58,7168
s_nop 0
buffer_load_dwordx4 v63,s[32:35],s56 offen lds
v_lshrrev_b32 v75,3,v12
s_lshl_b32 s44,s40,6
v_add_u32 v75,s44,v75
v_mul_u32_u24 v75,1280,v75
v_and_b32 v76,7,v12
s_add_u32 s84,s70,1
s_and_b32 s84,s84,3
s_mulk_i32 s84,20480
s_addk_i32 s84,4096
s_add_u32 s85,s70,2
s_and_b32 s85,s85,3
s_mulk_i32 s85,20480
s_addk_i32 s85,2048
s_add_u32 s86,s70,3
s_and_b32 s86,s86,3
s_mulk_i32 s86,20480
s_addk_i32 s86,4096
s_add_u32 s87,s70,4
s_and_b32 s87,s87,3
s_mulk_i32 s87,20480
s_addk_i32 s87,2048
v_or_b32 v77,0,v66
v_xor_b32 v77,v77,v76
v_lshl_add_u32 v77,v77,4,v75
v_add_u32 v48,s84,v77
v_add_u32 v50,s85,v77
v_add_u32 v52,s86,v77
v_add_u32 v54,s87,v77
v_or_b32 v77,4,v66
v_xor_b32 v77,v77,v76
v_lshl_add_u32 v77,v77,4,v75
v_add_u32 v77,9216,v77
v_add_u32 v49,s84,v77
v_add_u32 v51,s85,v77
v_add_u32 v53,s86,v77
v_add_u32 v55,s87,v77
s_mul_i32 s44,s41,307200
s_mov_b64 s[28:29],s[4:5]
s_add_u32 s28,s28,s44
s_addc_u32 s29,s29,0
s_sub_u32 s28,s28,4096
s_subb_u32 s29,s29,0
s_and_b32 s29,s29,0xffff
s_mov_b32 s30,311296
s_mov_b32 s31,0x20000
s_add_u32 m0,s59,0
s_nop 0
buffer_load_dwordx4 v48,s[28:31],s57 offen lds
buffer_load_dwordx4 v49,s[28:31],s57 offen offset:1024 lds
buffer_load_dwordx4 v50,s[28:31],s57 offen offset:2048 lds
buffer_load_dwordx4 v51,s[28:31],s57 offen offset:3072 lds
s_addk_i32 m0,4096
s_nop 0
buffer_load_dwordx4 v52,s[28:31],s57 offen lds
buffer_load_dwordx4 v53,s[28:31],s57 offen offset:1024 lds
buffer_load_dwordx4 v54,s[28:31],s57 offen offset:2048 lds
buffer_load_dwordx4 v55,s[28:31],s57 offen offset:3072 lds
s_addk_i32 s57,128
s_xor_b32 s59,s59,131072
s_add_u32 m0,s58,0
s_nop 0
buffer_load_dwordx4 v56,s[32:35],s56 offen lds
s_add_u32 m0,s58,2048
s_nop 0
buffer_load_dwordx4 v58,s[32:35],s56 offen lds
s_add_u32 m0,s58,4096
s_nop 0
buffer_load_dwordx4 v60,s[32:35],s56 offen lds
s_add_u32 m0,s58,6144
s_nop 0
buffer_load_dwordx4 v62,s[32:35],s56 offen lds
s_add_u32 s56,s56,524288
s_add_u32 s58,s58,32768
s_cmp_ge_u32 s58,131072
s_cselect_b32 s62,98304,0
s_sub_u32 s58,s58,s62
s_add_u32 s12,s12,4194303
s_addc_u32 s13,s13,0
s_and_b32 s12,s12,0xffc00000
s_lshl_b32 s55,s40,15
s_lshl_b32 s84,s41,2
s_add_u32 s84,s84,s70
s_lshl_b32 s84,s84,7
s_lshl_b32 s85,s71,2
s_add_u32 s84,s84,s85
s_add_u32 s84,s84,s40
s_addk_i32 s84,112
s_lshl_b32 s84,s84,14
s_add_u32 s84,s84,s12
s_addc_u32 s85,s13,0
s_mov_b32 s87,0x13572468
s_mov_b32 s79,0
s_load_dword s78,s[84:85],0x1000 glc
s_xor_b32 s76,s84,2097152
s_mov_b32 s77,s85
s_load_dword s78,s[76:77],0x0 glc
s_load_dword s78,s[76:77],0x2000 glc
s_load_dword s78,s[76:77],0x100 glc
s_load_dword s78,s[76:77],0x2100 glc
s_load_dword s78,s[76:77],0x200 glc
s_load_dword s78,s[76:77],0x2200 glc
s_add_u32 m0,s58,0
s_nop 0
buffer_load_dwordx4 v56,s[32:35],s56 offen lds
s_addk_i32 m0,1024
s_nop 0
buffer_load_dwordx4 v57,s[32:35],s56 offen lds
s_addk_i32 m0,1024
s_nop 0
buffer_load_dwordx4 v58,s[32:35],s56 offen lds
s_addk_i32 m0,1024
s_nop 0
buffer_load_dwordx4 v59,s[32:35],s56 offen lds
s_addk_i32 m0,1024
s_nop 0
buffer_load_dwordx4 v60,s[32:35],s56 offen lds
s_addk_i32 m0,1024
s_nop 0
buffer_load_dwordx4 v61,s[32:35],s56 offen lds
s_addk_i32 m0,1024
s_nop 0
buffer_load_dwordx4 v62,s[32:35],s56 offen lds
s_addk_i32 m0,1024
s_nop 0
buffer_load_dwordx4 v63,s[32:35],s56 offen lds
s_add_u32 s56,s56,524288
s_add_u32 s58,s58,32768
s_cmp_ge_u32 s58,131072
s_cselect_b32 s62,98304,0
s_sub_u32 s58,s58,s62
s_mov_b32 s60,65536
v_lshlrev_b32 v6,4,v12
v_lshlrev_b32 v72,12,v66
v_lshl_add_u32 v72,v65,6,v72
v_lshl_add_u32 v23,v64,4,v72
v_add_u32 v22,65536,v23
v_and_b32 v74,15,v12
v_lshrrev_b32 v76,1,v74
v_lshlrev_b32 v75,7,v74
s_lshl_b32 s44,s40,13
v_add_u32 v75,s44,v75
v_or_b32 v77,0,v66
v_xor_b32 v77,v77,v76
v_lshl_add_u32 v20,v77,4,v75
v_or_b32 v77,4,v66
v_xor_b32 v77,v77,v76
v_lshl_add_u32 v21,v77,4,v75
s_waitcnt vmcnt(12)
s_nop 0
s_memtime s[64:65]
s_memrealtime s[66:67]
s_bfe_u32 s68,%1,327683
s_waitcnt lgkmcnt(0)
s_sub_u32 s64,s64,s72
s_sub_u32 s66,s66,s74
s_mul_i32 s64,s64,10
s_mulk_i32 s66,224
s_cmp_lt_u32 s64,s66
s_cselect_b32 s95,64,0
s_mov_b32 %2,s95
s_cbranch_scc1 L_se1_%=
L_sg1_%=:
s_cmp_eq_u32 s68,0
s_cbranch_scc1 L_se1_%=
s_sleep 2
s_sub_u32 s68,s68,1
s_branch L_sg1_%=
L_se1_%=:
s_barrier
ds_read_b128 a[192:195],v20
ds_read_b64_tr_b16 a[128:129],v22 offset:1024
ds_read_b64_tr_b16 a[130:131],v22 offset:3072
ds_read_b128 a[196:199],v21
ds_read_b64_tr_b16 a[132:133],v22 offset:17408
ds_read_b64_tr_b16 a[134:135],v22 offset:19456
ds_read_b128 a[204:207],v21 offset:2048
ds_read_b128 a[200:203],v20 offset:2048
ds_read_b128 a[208:211],v20 offset:4096
ds_read_b128 a[212:215],v21 offset:4096
ds_read_b128 a[220:223],v21 offset:6144
ds_read_b128 a[216:219],v20 offset:6144
ds_read_b64_tr_b16 a[136:137],v22 offset:1032
ds_read_b64_tr_b16 a[138:139],v22 offset:3080
s_waitcnt lgkmcnt(11)
v_mfma_f32_16x16x32_bf16 v[128:131],a[128:131],a[192:195],0
s_add_u32 m0,s59,0
ds_read_b64_tr_b16 a[140:141],v22 offset:17416
ds_read_b64_tr_b16 a[142:143],v22 offset:19464
s_waitcnt lgkmcnt(10)
v_mfma_f32_16x16x32_bf16 v[128:131],a[132:135],a[196:199],v[128:131]
s_waitcnt lgkmcnt(9)
v_mfma_f32_16x16x32_bf16 v[136:139],a[132:135],a[204:207],0
s_waitcnt lgkmcnt(8)
v_mfma_f32_16x16x32_bf16 v[136:139],a[128:131],a[200:203],v[136:139]
s_waitcnt lgkmcnt(7)
v_mfma_f32_16x16x32_bf16 v[144:147],a[128:131],a[208:211],0
s_waitcnt lgkmcnt(6)
buffer_load_dwordx4 v48,s[28:31],s57 offen lds
v_mfma_f32_16x16x32_bf16 v[144:147],a[132:135],a[212:215],v[144:147]
s_waitcnt lgkmcnt(5)
v_mfma_f32_16x16x32_bf16 v[152:155],a[132:135],a[220:223],0
s_waitcnt lgkmcnt(4)
v_mfma_f32_16x16x32_bf16 v[152:155],a[128:131],a[216:219],v[152:155]
s_waitcnt lgkmcnt(2)
ds_read_b64_tr_b16 a[144:145],v22 offset:1280
ds_read_b64_tr_b16 a[146:147],v22 offset:3328
v_mfma_f32_16x16x32_bf16 v[132:135],a[136:139],a[192:195],0
s_waitcnt lgkmcnt(2)
ds_read_b64_tr_b16 a[148:149],v22 offset:17664
ds_read_b64_tr_b16 a[150:151],v22 offset:19712
v_mfma_f32_16x16x32_bf16 v[132:135],a[140:143],a[196:199],v[132:135]
buffer_load_dwordx4 v49,s[28:31],s57 offen offset:1024 lds
v_mfma_f32_16x16x32_bf16 v[140:143],a[140:143],a[204:207],0
v_mfma_f32_16x16x32_bf16 v[140:143],a[136:139],a[200:203],v[140:143]
v_mfma_f32_16x16x32_bf16 v[148:151],a[136:139],a[208:211],0
v_mfma_f32_16x16x32_bf16 v[148:151],a[140:143],a[212:215],v[148:151]
v_mfma_f32_16x16x32_bf16 v[156:159],a[140:143],a[220:223],0
buffer_load_dwordx4 v50,s[28:31],s57 offen offset:2048 lds
v_mfma_f32_16x16x32_bf16 v[156:159],a[136:139],a[216:219],v[156:159]
s_waitcnt lgkmcnt(2)
ds_read_b64_tr_b16 a[152:153],v22 offset:1288
ds_read_b64_tr_b16 a[154:155],v22 offset:3336
v_mfma_f32_16x16x32_bf16 v[160:163],a[144:147],a[192:195],0
s_waitcnt lgkmcnt(2)
ds_read_b64_tr_b16 a[156:157],v22 offset:17672
ds_read_b64_tr_b16 a[158:159],v22 offset:19720
v_mfma_f32_16x16x32_bf16 v[160:163],a[148:151],a[196:199],v[160:163]
v_mfma_f32_16x16x32_bf16 v[168:171],a[148:151],a[204:207],0
v_mfma_f32_16x16x32_bf16 v[168:171],a[144:147],a[200:203],v[168:171]
buffer_load_dwordx4 v51,s[28:31],s57 offen offset:3072 lds
v_mfma_f32_16x16x32_bf16 v[176:179],a[144:147],a[208:211],0
s_addk_i32 m0,4096
v_mfma_f32_16x16x32_bf16 v[176:179],a[148:151],a[212:215],v[176:179]
v_mfma_f32_16x16x32_bf16 v[184:187],a[148:151],a[220:223],0
v_mfma_f32_16x16x32_bf16 v[184:187],a[144:147],a[216:219],v[184:187]
s_waitcnt lgkmcnt(2)
v_mfma_f32_16x16x32_bf16 v[164:167],a[152:155],a[192:195],0
s_waitcnt lgkmcnt(0)
buffer_load_dwordx4 v52,s[28:31],s57 offen lds
ds_read_b64_tr_b16 a[160:161],v22 offset:1536
ds_read_b64_tr_b16 a[162:163],v22 offset:3584
v_mfma_f32_16x16x32_bf16 v[164:167],a[156:159],a[196:199],v[164:167]
ds_read_b64_tr_b16 a[164:165],v22 offset:17920
ds_read_b64_tr_b16 a[166:167],v22 offset:19968
v_mfma_f32_16x16x32_bf16 v[172:175],a[156:159],a[204:207],0
v_mfma_f32_16x16x32_bf16 v[172:175],a[152:155],a[200:203],v[172:175]
v_mfma_f32_16x16x32_bf16 v[180:183],a[152:155],a[208:211],0
v_mfma_f32_16x16x32_bf16 v[180:183],a[156:159],a[212:215],v[180:183]
buffer_load_dwordx4 v53,s[28:31],s57 offen offset:1024 lds
v_mfma_f32_16x16x32_bf16 v[188:191],a[156:159],a[220:223],0
s_waitcnt vmcnt(14)
v_mfma_f32_16x16x32_bf16 v[188:191],a[152:155],a[216:219],v[188:191]
s_barrier
ds_read_b64_tr_b16 a[168:169],v22 offset:1544
ds_read_b64_tr_b16 a[170:171],v22 offset:3592
s_waitcnt lgkmcnt(4)
v_mfma_f32_16x16x32_bf16 v[192:195],a[160:163],a[192:195],0
s_waitcnt lgkmcnt(2)
ds_read_b64_tr_b16 a[172:173],v22 offset:17928
ds_read_b64_tr_b16 a[174:175],v22 offset:19976
v_mfma_f32_16x16x32_bf16 v[192:195],a[164:167],a[196:199],v[192:195]
buffer_load_dwordx4 v54,s[28:31],s57 offen offset:2048 lds
v_mfma_f32_16x16x32_bf16 v[200:203],a[164:167],a[204:207],0
s_lshl_b32 s44,s41,13
s_add_u32 s44,s44,134217728
s_mov_b64 s[64:65],s[12:13]
v_mfma_f32_16x16x32_bf16 v[200:203],a[160:163],a[200:203],v[200:203]
s_add_u32 s64,s64,s44
s_addc_u32 s65,s65,0
s_and_b32 s65,s65,0xffff
v_mfma_f32_16x16x32_bf16 v[208:211],a[160:163],a[208:211],0
s_movk_i32 s66,8192
s_mov_b32 s67,0x20000
v_mov_b32 v14,4096
v_mfma_f32_16x16x32_bf16 v[208:211],a[164:167],a[212:215],v[208:211]
s_mov_b64 s[72:73],s[14:15]
buffer_load_dwordx4 v55,s[28:31],s57 offen offset:3072 lds
v_mfma_f32_16x16x32_bf16 v[216:219],a[164:167],a[220:223],0
s_add_u32 m0,s58,0
s_and_b32 s73,s73,0xffff
ds_read_b64_tr_b16 a[176:177],v22 offset:1792
v_mfma_f32_16x16x32_bf16 v[216:219],a[160:163],a[216:219],v[216:219]
s_mov_b32 s74,5242880
s_mov_b32 s75,0x20000
ds_read_b64_tr_b16 a[178:179],v22 offset:3840
s_waitcnt lgkmcnt(4)
v_mfma_f32_16x16x32_bf16 v[196:199],a[168:171],a[192:195],0
s_waitcnt lgkmcnt(2)
ds_read_b64_tr_b16 a[180:181],v22 offset:18176
ds_read_b64_tr_b16 a[182:183],v22 offset:20224
v_mfma_f32_16x16x32_bf16 v[196:199],a[172:175],a[196:199],v[196:199]
s_lshl_b32 s83,%1,2
buffer_load_dwordx4 v56,s[32:35],s56 offen lds
v_mfma_f32_16x16x32_bf16 v[204:207],a[172:175],a[204:207],0
s_addk_i32 m0,1024
v_lshlrev_b32 v15,3,v6
v_add_u32 v16,128,v15
v_mfma_f32_16x16x32_bf16 v[204:207],a[168:171],a[200:203],v[204:207]
s_add_u32 s83,s83,s40
ds_read_b64_tr_b16 a[184:185],v22 offset:1800
v_lshrrev_b32 v16,10,v16
v_mfma_f32_16x16x32_bf16 v[212:215],a[168:171],a[208:211],0
s_mulk_i32 s83,5120
ds_read_b64_tr_b16 a[186:187],v22 offset:3848
v_lshl_add_u32 v15,v16,24,v15
v_mfma_f32_16x16x32_bf16 v[212:215],a[172:175],a[212:215],v[212:215]
s_lshl_b32 s43,s41,5
buffer_load_dwordx4 v57,s[32:35],s56 offen lds
v_mfma_f32_16x16x32_bf16 v[220:223],a[172:175],a[220:223],0
s_lshl_b32 s44,s42,1
s_add_u32 s43,s43,s44
)A"
R"A(ds_read_b64_tr_b16 a[188:189],v22 offset:18184
v_mfma_f32_16x16x32_bf16 v[220:223],a[168:171],a[216:219],v[220:223]
s_addk_i32 m0,1024
s_lshl_b32 s44,s43,18
ds_read_b64_tr_b16 a[190:191],v22 offset:20232
s_waitcnt lgkmcnt(6)
v_mfma_f32_16x16x32_bf16 v[224:227],a[176:179],a[192:195],0
s_mov_b64 s[20:21],s[6:7]
s_mov_b32 s22,524288
ds_read_b64_tr_b16 a[128:129],v22
s_waitcnt lgkmcnt(5)
v_mfma_f32_16x16x32_bf16 v[224:227],a[180:183],a[196:199],v[224:227]
s_add_u32 s20,s20,s44
s_addc_u32 s21,s21,0
ds_read_b64_tr_b16 a[130:131],v22 offset:2048
v_mfma_f32_16x16x32_bf16 v[232:235],a[180:183],a[204:207],0
s_and_b32 s21,s21,0xffff
buffer_load_dwordx4 v58,s[32:35],s56 offen lds
v_mfma_f32_16x16x32_bf16 v[232:235],a[176:179],a[200:203],v[232:235]
s_mov_b32 s23,0x20000
s_mov_b64 s[24:25],s[8:9]
ds_read_b64_tr_b16 a[132:133],v22 offset:16384
v_mfma_f32_16x16x32_bf16 v[240:243],a[176:179],a[208:211],0
s_addk_i32 m0,1024
s_mov_b32 s26,524288
ds_read_b64_tr_b16 a[134:135],v22 offset:18432
v_mfma_f32_16x16x32_bf16 v[240:243],a[180:183],a[212:215],v[240:243]
s_add_u32 s24,s24,s44
s_addc_u32 s25,s25,0
ds_read_b64_tr_b16 a[136:137],v22 offset:8
v_mfma_f32_16x16x32_bf16 v[248:251],a[180:183],a[220:223],0
s_and_b32 s25,s25,0xffff
s_mov_b32 s27,0x20000
ds_read_b64_tr_b16 a[138:139],v22 offset:2056
v_mfma_f32_16x16x32_bf16 v[248:251],a[176:179],a[216:219],v[248:251]
s_lshl_b32 s44,s41,2
buffer_load_dwordx4 v59,s[32:35],s56 offen lds
s_waitcnt lgkmcnt(6)
v_mfma_f32_16x16x32_bf16 v[228:231],a[184:187],a[192:195],0
s_add_u32 s44,s44,s70
s_lshl_b32 s44,s44,3
ds_read_b64_tr_b16 a[140:141],v22 offset:16392
v_mfma_f32_16x16x32_bf16 v[228:231],a[188:191],a[196:199],v[228:231]
s_addk_i32 m0,1024
s_add_u32 s44,s44,s71
ds_read_b64_tr_b16 a[142:143],v22 offset:18440
v_mfma_f32_16x16x32_bf16 v[236:239],a[188:191],a[204:207],0
s_lshl_b32 s44,s44,18
s_mov_b64 s[36:37],s[12:13]
ds_read_b64_tr_b16 a[144:145],v22 offset:256
v_mfma_f32_16x16x32_bf16 v[236:239],a[184:187],a[200:203],v[236:239]
s_add_u32 s36,s36,s44
s_addc_u32 s37,s37,0
ds_read_b64_tr_b16 a[146:147],v22 offset:2304
v_mfma_f32_16x16x32_bf16 v[244:247],a[184:187],a[208:211],0
s_and_b32 s37,s37,0xffff
buffer_load_dwordx4 v60,s[32:35],s56 offen lds
v_mfma_f32_16x16x32_bf16 v[244:247],a[188:191],a[212:215],v[244:247]
s_xor_b32 s59,s59,131072
s_mov_b32 s38,262144
ds_read_b64_tr_b16 a[148:149],v22 offset:16640
v_mfma_f32_16x16x32_bf16 v[252:255],a[188:191],a[220:223],0
s_addk_i32 m0,1024
s_mov_b32 s39,0x20000
ds_read_b64_tr_b16 a[150:151],v22 offset:18688
v_mfma_f32_16x16x32_bf16 v[252:255],a[184:187],a[216:219],v[252:255]
s_lshl_b32 s51,s40,12
s_addk_i32 s57,128
ds_read_b64_tr_b16 a[152:153],v22 offset:264
s_waitcnt lgkmcnt(7)
v_mfma_f32_16x16x32_bf16 a[0:3],a[128:131],a[192:195],0
s_mul_i32 s47,s41,5
s_add_u32 s47,s47,s42
ds_read_b64_tr_b16 a[154:155],v22 offset:2312
v_mfma_f32_16x16x32_bf16 a[0:3],a[132:135],a[196:199],a[0:3]
s_and_b32 s47,s47,15
buffer_load_dwordx4 v61,s[32:35],s56 offen lds
v_mfma_f32_16x16x32_bf16 a[32:35],a[132:135],a[204:207],0
s_mul_i32 s44,s41,5
s_add_u32 s44,s44,s42
ds_read_b64_tr_b16 a[156:157],v22 offset:16648
v_mfma_f32_16x16x32_bf16 a[32:35],a[128:131],a[200:203],a[32:35]
s_addk_i32 m0,1024
s_lshl_b32 s47,s44,12
ds_read_b64_tr_b16 a[158:159],v22 offset:18696
v_mfma_f32_16x16x32_bf16 a[64:67],a[128:131],a[208:211],0
s_and_b32 s47,s47,0x7000
s_lshl_b32 s44,s44,14
ds_read_b64_tr_b16 a[160:161],v22 offset:512
v_mfma_f32_16x16x32_bf16 a[64:67],a[132:135],a[212:215],a[64:67]
s_and_b32 s44,s44,0x20000
s_or_b32 s47,s47,s44
ds_read_b64_tr_b16 a[162:163],v22 offset:2560
v_mfma_f32_16x16x32_bf16 a[96:99],a[132:135],a[220:223],0
s_mov_b32 s54,0
buffer_load_dwordx4 v62,s[32:35],s56 offen lds
v_mfma_f32_16x16x32_bf16 a[96:99],a[128:131],a[216:219],a[96:99]
s_mov_b32 s61,0x3e0293ee
s_mov_b32 s63,0x53800000
ds_read_b64_tr_b16 a[164:165],v22 offset:16896
v_mfma_f32_16x16x32_bf16 a[4:7],a[136:139],a[192:195],0
s_addk_i32 m0,1024
s_lshl_b32 s44,s40,2
ds_read_b64_tr_b16 a[166:167],v22 offset:18944
s_waitcnt lgkmcnt(6)
v_mfma_f32_16x16x32_bf16 a[4:7],a[140:143],a[196:199],a[4:7]
s_add_u32 s44,s44,s70
s_mov_b32 s46,0xff800000
ds_read_b64_tr_b16 a[168:169],v22 offset:520
v_mfma_f32_16x16x32_bf16 a[36:39],a[140:143],a[204:207],0
s_cmp_eq_u32 s44,14
s_cselect_b32 s18,0x7f61b1e6,s46
ds_read_b64_tr_b16 a[170:171],v22 offset:2568
v_mfma_f32_16x16x32_bf16 a[36:39],a[136:139],a[200:203],a[36:39]
s_add_u32 s60,s60,32768
buffer_load_dwordx4 v63,s[32:35],s56 offen lds
v_mfma_f32_16x16x32_bf16 a[68:71],a[136:139],a[208:211],0
s_cmp_eq_u32 s44,13
s_cselect_b32 s19,0x7f61b1e6,s46
ds_read_b64_tr_b16 a[172:173],v22 offset:16904
v_mfma_f32_16x16x32_bf16 a[68:71],a[140:143],a[212:215],a[68:71]
s_add_u32 m0,s59,0
ds_read_b64_tr_b16 a[174:175],v22 offset:18952
v_lshlrev_b32 v70,15,v65
v_mfma_f32_16x16x32_bf16 a[100:103],a[140:143],a[220:223],0
s_cmp_eq_u32 s44,12
s_cselect_b32 s100,0x7f61b1e6,s46
ds_read_b64_tr_b16 a[176:177],v22 offset:768
v_mfma_f32_16x16x32_bf16 a[100:103],a[136:139],a[216:219],a[100:103]
s_cmp_eq_u32 s44,15
s_cselect_b32 s101,0x7f61b1e6,s46
ds_read_b64_tr_b16 a[178:179],v22 offset:2816
v_mfma_f32_16x16x32_bf16 a[8:11],a[144:147],a[192:195],0
s_add_u32 s58,s58,32768
buffer_load_dwordx4 v48,s[28:31],s57 offen lds
v_mfma_f32_16x16x32_bf16 a[8:11],a[148:151],a[196:199],a[8:11]
s_add_u32 s56,s56,524288
ds_read_b64_tr_b16 a[180:181],v22 offset:17152
v_lshl_add_u32 v70,v66,6,v70
v_mfma_f32_16x16x32_bf16 a[40:43],a[148:151],a[204:207],0
s_mul_i32 s81,s70,1310720
ds_read_b64_tr_b16 a[182:183],v22 offset:19200
v_xor_b32 v71,s40,v65
v_mfma_f32_16x16x32_bf16 a[40:43],a[144:147],a[200:203],a[40:43]
s_mul_i32 s44,s71,320
ds_read_b64_tr_b16 a[184:185],v22 offset:776
v_lshl_add_u32 v70,v71,8,v70
v_mfma_f32_16x16x32_bf16 a[72:75],a[144:147],a[208:211],0
s_add_u32 s81,s81,s44
ds_read_b64_tr_b16 a[186:187],v22 offset:2824
v_lshl_add_u32 v4,v64,4,v70
v_mfma_f32_16x16x32_bf16 a[72:75],a[148:151],a[212:215],a[72:75]
s_mul_i32 s82,s40,10240
buffer_load_dwordx4 v49,s[28:31],s57 offen offset:1024 lds
v_mfma_f32_16x16x32_bf16 a[104:107],a[148:151],a[220:223],0
s_mul_i32 s44,s40,40960
ds_read_b64_tr_b16 a[188:189],v22 offset:17160
v_xor_b32 v71,2,v64
v_mfma_f32_16x16x32_bf16 a[104:107],a[144:147],a[216:219],a[104:107]
s_xor_b32 s59,s59,131072
ds_read_b64_tr_b16 a[190:191],v22 offset:19208
v_lshl_add_u32 v5,v71,4,v70
v_mfma_f32_16x16x32_bf16 a[12:15],a[152:155],a[192:195],0
s_lshr_b32 s92,%1,3
v_lshlrev_b32 v72,10,v65
v_lshl_add_u32 v72,v64,6,v72
s_waitcnt lgkmcnt(6)
v_mfma_f32_16x16x32_bf16 a[12:15],a[156:159],a[196:199],a[12:15]
s_cmp_eq_u32 s60,131072
s_cselect_b32 s60,32768,s60
v_and_b32 v73,2,v65
v_mfma_f32_16x16x32_bf16 a[44:47],a[156:159],a[204:207],0
s_lshl_b32 s92,s92,2
buffer_load_dwordx4 v50,s[28:31],s57 offen offset:2048 lds
v_mfma_f32_16x16x32_bf16 a[44:47],a[152:155],a[200:203],a[44:47]
s_add_u32 s92,s92,s40
v_xor_b32 v73,v73,v66
v_add_u32 v22,s60,v23
v_mfma_f32_16x16x32_bf16 a[76:79],a[152:155],a[208:211],0
s_lshl_b32 s92,s92,7
v_xor_b32 v20,131072,v20
v_xor_b32 v21,131072,v21
v_mfma_f32_16x16x32_bf16 a[76:79],a[156:159],a[212:215],a[76:79]
s_add_u32 s93,s92,L_tailseg_%=-L_pc_%=
v_lshl_add_u32 v72,v73,4,v72
v_add_u32 v1,98304,v72
v_mfma_f32_16x16x32_bf16 a[108:111],a[156:159],a[220:223],0
s_add_u32 s60,s60,32768
v_lshlrev_b32 v72,10,v66
v_lshl_add_u32 v72,v65,6,v72
v_mfma_f32_16x16x32_bf16 a[108:111],a[152:155],a[216:219],a[108:111]
s_waitcnt vmcnt(11) lgkmcnt(0)
buffer_load_dwordx4 v51,s[28:31],s57 offen offset:3072 lds
v_mfma_f32_16x16x32_bf16 a[16:19],a[160:163],a[192:195],0
s_barrier
s_addk_i32 m0,4096
ds_read_b128 a[224:227],v20
v_mfma_f32_16x16x32_bf16 a[16:19],a[164:167],a[196:199],a[16:19]
ds_read_b64_tr_b16 a[128:129],v22 offset:1024
v_lshl_add_u32 v72,v64,3,v72
v_mfma_f32_16x16x32_bf16 a[48:51],a[164:167],a[204:207],0
ds_read_b64_tr_b16 a[130:131],v22 offset:3072
v_add_u32 v72,114688,v72
v_mfma_f32_16x16x32_bf16 a[48:51],a[160:163],a[200:203],a[48:51]
buffer_load_dwordx4 v52,s[28:31],s57 offen lds
v_mfma_f32_16x16x32_bf16 a[80:83],a[160:163],a[208:211],0
ds_read_b128 a[228:231],v21
v_and_b32 v73,1,v66
v_mfma_f32_16x16x32_bf16 a[80:83],a[164:167],a[212:215],a[80:83]
s_cmp_ge_u32 s58,131072
s_cselect_b32 s62,98304,0
ds_read_b64_tr_b16 a[132:133],v22 offset:17408
v_mfma_f32_16x16x32_bf16 a[112:115],a[164:167],a[220:223],0
s_sub_u32 s58,s58,s62
ds_read_b64_tr_b16 a[134:135],v22 offset:19456
v_lshl_add_u32 v2,v73,5,v72
v_mfma_f32_16x16x32_bf16 a[112:115],a[160:163],a[216:219],a[112:115]
ds_read_b128 a[232:235],v20 offset:2048
v_xor_b32 v73,1,v73
v_mfma_f32_16x16x32_bf16 a[20:23],a[168:171],a[192:195],0
buffer_load_dwordx4 v53,s[28:31],s57 offen offset:1024 lds
v_mfma_f32_16x16x32_bf16 a[20:23],a[172:175],a[196:199],a[20:23]
ds_read_b128 a[236:239],v21 offset:2048
v_lshl_add_u32 v3,v73,5,v72
v_mfma_f32_16x16x32_bf16 a[52:55],a[172:175],a[204:207],0
ds_read_b128 a[240:243],v20 offset:4096
v_mov_b32 v73,0x3f803f80
v_mfma_f32_16x16x32_bf16 a[52:55],a[168:171],a[200:203],a[52:55]
ds_read_b128 a[244:247],v21 offset:4096
v_add_u32 v70,0,v12
v_mfma_f32_16x16x32_bf16 a[84:87],a[168:171],a[208:211],0
buffer_load_dword v13,v14,s[64:67],0 offen sc1
v_mfma_f32_16x16x32_bf16 a[84:87],a[172:175],a[212:215],a[84:87]
ds_read_b128 a[248:251],v20 offset:6144
v_mul_u32_u24 v71,3277,v70
v_mfma_f32_16x16x32_bf16 a[116:119],a[172:175],a[220:223],0
ds_read_b128 a[252:255],v21 offset:6144
v_lshrrev_b32 v71,16,v71
v_mfma_f32_16x16x32_bf16 a[116:119],a[168:171],a[216:219],a[116:119]
ds_read_b64_tr_b16 a[136:137],v22 offset:1032
v_mul_u32_u24 v72,20,v71
v_mfma_f32_16x16x32_bf16 a[24:27],a[176:179],a[192:195],0
buffer_load_dwordx4 v54,s[28:31],s57 offen offset:2048 lds
v_mfma_f32_16x16x32_bf16 a[24:27],a[180:183],a[196:199],a[24:27]
ds_read_b64_tr_b16 a[138:139],v22 offset:3080
v_sub_u32 v72,v70,v72
v_mfma_f32_16x16x32_bf16 a[56:59],a[180:183],a[204:207],0
ds_read_b64_tr_b16 a[140:141],v22 offset:17416
v_add_u32 v70,64,v12
v_mfma_f32_16x16x32_bf16 a[56:59],a[176:179],a[200:203],a[56:59]
ds_read_b64_tr_b16 a[142:143],v22 offset:19464
v_lshlrev_b32 v24,10,v6
v_mfma_f32_16x16x32_bf16 a[88:91],a[176:179],a[208:211],0
buffer_load_dwordx4 v55,s[28:31],s57 offen offset:3072 lds
v_mfma_f32_16x16x32_bf16 a[88:91],a[180:183],a[212:215],a[88:91]
s_addk_i32 s57,128
ds_read_b64_tr_b16 a[144:145],v22 offset:1280
v_xor_b32 v20,131072,v20
v_mfma_f32_16x16x32_bf16 a[120:123],a[180:183],a[220:223],0
s_cmp_eq_u32 s60,131072
s_cselect_b32 s60,32768,s60
ds_read_b64_tr_b16 a[146:147],v22 offset:3328
v_mfma_f32_16x16x32_bf16 a[120:123],a[176:179],a[216:219],a[120:123]
ds_read_b64_tr_b16 a[148:149],v22 offset:17664
v_xor_b32 v21,131072,v21
v_mfma_f32_16x16x32_bf16 a[28:31],a[184:187],a[192:195],0
ds_read_b64_tr_b16 a[150:151],v22 offset:19712
v_mfma_f32_16x16x32_bf16 a[28:31],a[188:191],a[196:199],a[28:31]
ds_read_b64_tr_b16 a[152:153],v22 offset:1288
ds_read_b64_tr_b16 a[154:155],v22 offset:3336
ds_read_b64_tr_b16 a[156:157],v22 offset:17672
ds_read_b64_tr_b16 a[158:159],v22 offset:19720
v_mfma_f32_16x16x32_bf16 a[60:63],a[188:191],a[204:207],0
v_mfma_f32_16x16x32_bf16 a[60:63],a[184:187],a[200:203],a[60:63]
v_cmp_gt_u32 vcc,2,v64
s_nop 1
v_cndmask_b32 v28,0,v73,vcc
v_mfma_f32_16x16x32_bf16 a[92:95],a[184:187],a[208:211],0
v_and_b32 v73,1,v64
v_mul_u32_u24 v73,0xaaaa,v73
v_mfma_f32_16x16x32_bf16 a[92:95],a[188:191],a[212:215],a[92:95]
v_add_u32 v73,0x4444,v73
v_lshl_or_b32 v27,v73,16,v73
v_mfma_f32_16x16x32_bf16 a[124:127],a[188:191],a[220:223],0
s_add_u32 m0,s58,0
v_mov_b32 v29,v28
v_mov_b32 v30,v28
v_mfma_f32_16x16x32_bf16 a[124:127],a[184:187],a[216:219],a[124:127]
s_add_u32 s58,s58,32768
ds_read_b64_tr_b16 a[160:161],v22 offset:1536
v_mov_b32 v31,v28
s_waitcnt lgkmcnt(6)
v_mfma_f32_16x16x32_bf16 v[128:131],a[128:131],a[224:227],v[128:131]
ds_read_b64_tr_b16 a[162:163],v22 offset:3584
v_bfe_u32 v73,v71,2,1
v_mfma_f32_16x16x32_bf16 v[128:131],a[132:135],a[228:231],v[128:131]
buffer_load_dwordx4 v56,s[32:35],s56 offen lds
v_mfma_f32_16x16x32_bf16 v[136:139],a[132:135],a[236:239],v[136:139]
ds_read_b64_tr_b16 a[164:165],v22 offset:17920
)A"
R"A(v_lshlrev_b32 v73,1,v73
v_mfma_f32_16x16x32_bf16 v[136:139],a[128:131],a[232:235],v[136:139]
s_addk_i32 m0,1024
ds_read_b64_tr_b16 a[166:167],v22 offset:19968
v_xor_b32 v72,v72,v73
v_mfma_f32_16x16x32_bf16 v[144:147],a[128:131],a[240:243],v[144:147]
v_mul_u32_u24 v71,1280,v71
v_lshl_add_u32 v71,v72,4,v71
v_mfma_f32_16x16x32_bf16 v[144:147],a[132:135],a[244:247],v[144:147]
ds_read_b64_tr_b16 a[168:169],v22 offset:1544
v_add_u32 v71,s44,v71
v_mfma_f32_16x16x32_bf16 v[152:155],a[132:135],a[252:255],v[152:155]
buffer_load_dwordx4 v57,s[32:35],s56 offen lds
v_mfma_f32_16x16x32_bf16 v[152:155],a[128:131],a[248:251],v[152:155]
ds_read_b64_tr_b16 a[170:171],v22 offset:3592
v_subrev_u32 v7,0,v71
v_mfma_f32_16x16x32_bf16 v[132:135],a[136:139],a[224:227],v[132:135]
ds_read_b64_tr_b16 a[172:173],v22 offset:17928
v_mul_u32_u24 v71,3277,v70
v_mfma_f32_16x16x32_bf16 v[132:135],a[140:143],a[228:231],v[132:135]
s_addk_i32 m0,1024
ds_read_b64_tr_b16 a[174:175],v22 offset:19976
v_lshrrev_b32 v71,16,v71
v_mfma_f32_16x16x32_bf16 v[140:143],a[140:143],a[236:239],v[140:143]
ds_read_b64_tr_b16 a[176:177],v22 offset:1792
v_mul_u32_u24 v72,20,v71
v_mfma_f32_16x16x32_bf16 v[140:143],a[136:139],a[232:235],v[140:143]
ds_read_b64_tr_b16 a[178:179],v22 offset:3840
v_sub_u32 v72,v70,v72
v_mfma_f32_16x16x32_bf16 v[148:151],a[136:139],a[240:243],v[148:151]
buffer_load_dwordx4 v58,s[32:35],s56 offen lds
v_mfma_f32_16x16x32_bf16 v[148:151],a[140:143],a[244:247],v[148:151]
ds_read_b64_tr_b16 a[180:181],v22 offset:18176
v_bfe_u32 v73,v71,2,1
v_mfma_f32_16x16x32_bf16 v[156:159],a[140:143],a[252:255],v[156:159]
ds_read_b64_tr_b16 a[182:183],v22 offset:20224
v_lshlrev_b32 v73,1,v73
v_mfma_f32_16x16x32_bf16 v[156:159],a[136:139],a[248:251],v[156:159]
s_addk_i32 m0,1024
v_xor_b32 v72,v72,v73
v_mul_u32_u24 v71,1280,v71
v_mfma_f32_16x16x32_bf16 v[160:163],a[144:147],a[224:227],v[160:163]
s_waitcnt lgkmcnt(5)
v_lshl_add_u32 v71,v72,4,v71
v_add_u32 v71,s44,v71
v_mfma_f32_16x16x32_bf16 v[160:163],a[148:151],a[228:231],v[160:163]
ds_read_b64_tr_b16 a[184:185],v22 offset:1800
v_subrev_u32 v8,1024,v71
v_mfma_f32_16x16x32_bf16 v[168:171],a[148:151],a[236:239],v[168:171]
buffer_load_dwordx4 v59,s[32:35],s56 offen lds
v_mfma_f32_16x16x32_bf16 v[168:171],a[144:147],a[232:235],v[168:171]
ds_read_b64_tr_b16 a[186:187],v22 offset:3848
v_add_u32 v70,128,v12
v_mfma_f32_16x16x32_bf16 v[176:179],a[144:147],a[240:243],v[176:179]
s_addk_i32 m0,1024
ds_read_b64_tr_b16 a[188:189],v22 offset:18184
v_mul_u32_u24 v71,3277,v70
v_mfma_f32_16x16x32_bf16 v[176:179],a[148:151],a[244:247],v[176:179]
ds_read_b64_tr_b16 a[190:191],v22 offset:20232
v_lshrrev_b32 v71,16,v71
v_mfma_f32_16x16x32_bf16 v[184:187],a[148:151],a[252:255],v[184:187]
v_mul_u32_u24 v72,20,v71
v_sub_u32 v72,v70,v72
v_mfma_f32_16x16x32_bf16 v[184:187],a[144:147],a[248:251],v[184:187]
buffer_load_dwordx4 v60,s[32:35],s56 offen lds
v_mfma_f32_16x16x32_bf16 v[164:167],a[152:155],a[224:227],v[164:167]
ds_read_b64_tr_b16 a[128:129],v22
v_bfe_u32 v73,v71,2,1
v_mfma_f32_16x16x32_bf16 v[164:167],a[156:159],a[228:231],v[164:167]
ds_read_b64_tr_b16 a[130:131],v22 offset:2048
v_lshlrev_b32 v73,1,v73
v_mfma_f32_16x16x32_bf16 v[172:175],a[156:159],a[236:239],v[172:175]
s_addk_i32 m0,1024
ds_read_b64_tr_b16 a[132:133],v22 offset:16384
v_xor_b32 v72,v72,v73
v_mfma_f32_16x16x32_bf16 v[172:175],a[152:155],a[232:235],v[172:175]
ds_read_b64_tr_b16 a[134:135],v22 offset:18432
v_mul_u32_u24 v71,1280,v71
v_mfma_f32_16x16x32_bf16 v[180:183],a[152:155],a[240:243],v[180:183]
v_lshl_add_u32 v71,v72,4,v71
v_add_u32 v71,s44,v71
v_mfma_f32_16x16x32_bf16 v[180:183],a[156:159],a[244:247],v[180:183]
buffer_load_dwordx4 v61,s[32:35],s56 offen lds
v_mfma_f32_16x16x32_bf16 v[188:191],a[156:159],a[252:255],v[188:191]
ds_read_b64_tr_b16 a[136:137],v22 offset:8
v_subrev_u32 v9,2048,v71
v_mfma_f32_16x16x32_bf16 v[188:191],a[152:155],a[248:251],v[188:191]
ds_read_b64_tr_b16 a[138:139],v22 offset:2056
v_add_u32 v70,192,v12
v_mfma_f32_16x16x32_bf16 v[192:195],a[160:163],a[224:227],v[192:195]
s_addk_i32 m0,1024
ds_read_b64_tr_b16 a[140:141],v22 offset:16392
v_mul_u32_u24 v71,3277,v70
v_mfma_f32_16x16x32_bf16 v[192:195],a[164:167],a[228:231],v[192:195]
ds_read_b64_tr_b16 a[142:143],v22 offset:18440
v_lshrrev_b32 v71,16,v71
v_mfma_f32_16x16x32_bf16 v[200:203],a[164:167],a[236:239],v[200:203]
v_mul_u32_u24 v72,20,v71
v_sub_u32 v72,v70,v72
v_mfma_f32_16x16x32_bf16 v[200:203],a[160:163],a[232:235],v[200:203]
buffer_load_dwordx4 v62,s[32:35],s56 offen lds
v_mfma_f32_16x16x32_bf16 v[208:211],a[160:163],a[240:243],v[208:211]
v_bfe_u32 v73,v71,2,1
v_lshlrev_b32 v73,1,v73
v_mfma_f32_16x16x32_bf16 v[208:211],a[164:167],a[244:247],v[208:211]
s_addk_i32 m0,1024
ds_read_b64_tr_b16 a[144:145],v22 offset:256
v_xor_b32 v72,v72,v73
v_mfma_f32_16x16x32_bf16 v[216:219],a[164:167],a[252:255],v[216:219]
ds_read_b64_tr_b16 a[146:147],v22 offset:2304
v_mul_u32_u24 v71,1280,v71
v_mfma_f32_16x16x32_bf16 v[216:219],a[160:163],a[248:251],v[216:219]
ds_read_b64_tr_b16 a[148:149],v22 offset:16640
v_lshl_add_u32 v71,v72,4,v71
v_mfma_f32_16x16x32_bf16 v[196:199],a[168:171],a[224:227],v[196:199]
s_waitcnt lgkmcnt(4)
buffer_load_dwordx4 v63,s[32:35],s56 offen lds
v_mfma_f32_16x16x32_bf16 v[196:199],a[172:175],a[228:231],v[196:199]
s_add_u32 s56,s56,524288
ds_read_b64_tr_b16 a[150:151],v22 offset:18688
v_add_u32 v71,s44,v71
v_mfma_f32_16x16x32_bf16 v[204:207],a[172:175],a[236:239],v[204:207]
v_subrev_u32 v10,3072,v71
v_add_u32 v70,256,v12
v_mfma_f32_16x16x32_bf16 v[204:207],a[168:171],a[232:235],v[204:207]
s_add_u32 m0,s59,0
ds_read_b64_tr_b16 a[152:153],v22 offset:264
v_mul_u32_u24 v71,3277,v70
v_mfma_f32_16x16x32_bf16 v[212:215],a[168:171],a[240:243],v[212:215]
s_xor_b32 s59,s59,131072
ds_read_b64_tr_b16 a[154:155],v22 offset:2312
v_lshrrev_b32 v71,16,v71
v_mfma_f32_16x16x32_bf16 v[212:215],a[172:175],a[244:247],v[212:215]
ds_read_b64_tr_b16 a[156:157],v22 offset:16648
v_mul_u32_u24 v72,20,v71
v_mfma_f32_16x16x32_bf16 v[220:223],a[172:175],a[252:255],v[220:223]
buffer_load_dwordx4 v48,s[28:31],s57 offen lds
v_mfma_f32_16x16x32_bf16 v[220:223],a[168:171],a[248:251],v[220:223]
ds_read_b64_tr_b16 a[158:159],v22 offset:18696
v_sub_u32 v72,v70,v72
v_mfma_f32_16x16x32_bf16 v[224:227],a[176:179],a[224:227],v[224:227]
v_bfe_u32 v73,v71,2,1
v_lshlrev_b32 v73,1,v73
v_mfma_f32_16x16x32_bf16 v[224:227],a[180:183],a[228:231],v[224:227]
ds_read_b64_tr_b16 a[160:161],v22 offset:512
v_xor_b32 v72,v72,v73
v_mfma_f32_16x16x32_bf16 v[232:235],a[180:183],a[236:239],v[232:235]
ds_read_b64_tr_b16 a[162:163],v22 offset:2560
v_mul_u32_u24 v71,1280,v71
v_mfma_f32_16x16x32_bf16 v[232:235],a[176:179],a[232:235],v[232:235]
ds_read_b64_tr_b16 a[164:165],v22 offset:16896
v_lshl_add_u32 v71,v72,4,v71
v_mfma_f32_16x16x32_bf16 v[240:243],a[176:179],a[240:243],v[240:243]
buffer_load_dwordx4 v49,s[28:31],s57 offen offset:1024 lds
v_mfma_f32_16x16x32_bf16 v[240:243],a[180:183],a[244:247],v[240:243]
ds_read_b64_tr_b16 a[166:167],v22 offset:18944
v_add_u32 v71,s44,v71
v_mfma_f32_16x16x32_bf16 v[248:251],a[180:183],a[252:255],v[248:251]
ds_read_b64_tr_b16 a[168:169],v22 offset:520
v_subrev_u32 v11,0,v71
v_mfma_f32_16x16x32_bf16 v[248:251],a[176:179],a[248:251],v[248:251]
s_cmp_ge_u32 s58,131072
s_cselect_b32 s62,98304,0
ds_read_b64_tr_b16 a[170:171],v22 offset:2568
v_mfma_f32_16x16x32_bf16 v[228:231],a[184:187],a[224:227],v[228:231]
s_sub_u32 s58,s58,s62
ds_read_b64_tr_b16 a[172:173],v22 offset:16904
v_mfma_f32_16x16x32_bf16 v[228:231],a[188:191],a[228:231],v[228:231]
buffer_load_dwordx4 v50,s[28:31],s57 offen offset:2048 lds
v_mfma_f32_16x16x32_bf16 v[236:239],a[188:191],a[236:239],v[236:239]
ds_read_b64_tr_b16 a[174:175],v22 offset:18952
v_mfma_f32_16x16x32_bf16 v[236:239],a[184:187],a[232:235],v[236:239]
ds_read_b64_tr_b16 a[176:177],v22 offset:768
v_mfma_f32_16x16x32_bf16 v[244:247],a[184:187],a[240:243],v[244:247]
ds_read_b64_tr_b16 a[178:179],v22 offset:2816
v_mfma_f32_16x16x32_bf16 v[244:247],a[188:191],a[244:247],v[244:247]
ds_read_b64_tr_b16 a[180:181],v22 offset:17152
v_mfma_f32_16x16x32_bf16 v[252:255],a[188:191],a[252:255],v[252:255]
ds_read_b64_tr_b16 a[182:183],v22 offset:19200
v_mfma_f32_16x16x32_bf16 v[252:255],a[184:187],a[248:251],v[252:255]
buffer_load_dwordx4 v51,s[28:31],s57 offen offset:3072 lds
v_mfma_f32_16x16x32_bf16 a[0:3],a[128:131],a[224:227],a[0:3]
ds_read_b64_tr_b16 a[184:185],v22 offset:776
v_mfma_f32_16x16x32_bf16 a[0:3],a[132:135],a[228:231],a[0:3]
ds_read_b64_tr_b16 a[186:187],v22 offset:2824
v_mfma_f32_16x16x32_bf16 a[32:35],a[132:135],a[236:239],a[32:35]
s_addk_i32 m0,4096
ds_read_b64_tr_b16 a[188:189],v22 offset:17160
v_mfma_f32_16x16x32_bf16 a[32:35],a[128:131],a[232:235],a[32:35]
ds_read_b64_tr_b16 a[190:191],v22 offset:19208
v_add_u32 v22,s60,v23
v_mfma_f32_16x16x32_bf16 a[64:67],a[128:131],a[240:243],a[64:67]
v_mfma_f32_16x16x32_bf16 a[64:67],a[132:135],a[244:247],a[64:67]
buffer_load_dwordx4 v52,s[28:31],s57 offen lds
v_mfma_f32_16x16x32_bf16 a[96:99],a[132:135],a[252:255],a[96:99]
v_mfma_f32_16x16x32_bf16 a[96:99],a[128:131],a[248:251],a[96:99]
v_mfma_f32_16x16x32_bf16 a[4:7],a[136:139],a[224:227],a[4:7]
s_waitcnt lgkmcnt(3)
v_mfma_f32_16x16x32_bf16 a[4:7],a[140:143],a[228:231],a[4:7]
v_mfma_f32_16x16x32_bf16 a[36:39],a[140:143],a[236:239],a[36:39]
buffer_load_dwordx4 v53,s[28:31],s57 offen offset:1024 lds
v_mfma_f32_16x16x32_bf16 a[36:39],a[136:139],a[232:235],a[36:39]
v_mfma_f32_16x16x32_bf16 a[68:71],a[136:139],a[240:243],a[68:71]
v_mfma_f32_16x16x32_bf16 a[68:71],a[140:143],a[244:247],a[68:71]
v_mfma_f32_16x16x32_bf16 a[100:103],a[140:143],a[252:255],a[100:103]
v_mfma_f32_16x16x32_bf16 a[100:103],a[136:139],a[248:251],a[100:103]
v_mfma_f32_16x16x32_bf16 a[8:11],a[144:147],a[224:227],a[8:11]
buffer_load_dwordx4 v54,s[28:31],s57 offen offset:2048 lds
v_mfma_f32_16x16x32_bf16 a[8:11],a[148:151],a[228:231],a[8:11]
v_mfma_f32_16x16x32_bf16 a[40:43],a[148:151],a[236:239],a[40:43]
v_mfma_f32_16x16x32_bf16 a[40:43],a[144:147],a[232:235],a[40:43]
v_mfma_f32_16x16x32_bf16 a[72:75],a[144:147],a[240:243],a[72:75]
v_mfma_f32_16x16x32_bf16 a[72:75],a[148:151],a[244:247],a[72:75]
v_mfma_f32_16x16x32_bf16 a[104:107],a[148:151],a[252:255],a[104:107]
buffer_load_dwordx4 v55,s[28:31],s57 offen offset:3072 lds
v_mfma_f32_16x16x32_bf16 a[104:107],a[144:147],a[248:251],a[104:107]
s_addk_i32 s57,128
v_mfma_f32_16x16x32_bf16 a[12:15],a[152:155],a[224:227],a[12:15]
v_mfma_f32_16x16x32_bf16 a[12:15],a[156:159],a[228:231],a[12:15]
v_mfma_f32_16x16x32_bf16 a[44:47],a[156:159],a[236:239],a[44:47]
v_mfma_f32_16x16x32_bf16 a[44:47],a[152:155],a[232:235],a[44:47]
v_mfma_f32_16x16x32_bf16 a[76:79],a[152:155],a[240:243],a[76:79]
v_mfma_f32_16x16x32_bf16 a[76:79],a[156:159],a[244:247],a[76:79]
v_mfma_f32_16x16x32_bf16 a[108:111],a[156:159],a[252:255],a[108:111]
v_mfma_f32_16x16x32_bf16 a[108:111],a[152:155],a[248:251],a[108:111]
s_waitcnt vmcnt(16) lgkmcnt(0)
v_mfma_f32_16x16x32_bf16 a[16:19],a[160:163],a[224:227],a[16:19]
s_barrier
ds_read_b128 a[192:195],v20
v_mfma_f32_16x16x32_bf16 a[16:19],a[164:167],a[228:231],a[16:19]
ds_read_b128 a[196:199],v21
v_mfma_f32_16x16x32_bf16 a[48:51],a[164:167],a[236:239],a[48:51]
ds_read_b128 a[200:203],v20 offset:2048
v_mfma_f32_16x16x32_bf16 a[48:51],a[160:163],a[232:235],a[48:51]
ds_read_b128 a[204:207],v21 offset:2048
v_mfma_f32_16x16x32_bf16 a[80:83],a[160:163],a[240:243],a[80:83]
ds_read_b128 a[208:211],v20 offset:4096
v_mfma_f32_16x16x32_bf16 a[80:83],a[164:167],a[244:247],a[80:83]
ds_read_b128 a[212:215],v21 offset:4096
v_mfma_f32_16x16x32_bf16 a[112:115],a[164:167],a[252:255],a[112:115]
ds_read_b128 a[216:219],v20 offset:6144
v_mfma_f32_16x16x32_bf16 a[112:115],a[160:163],a[248:251],a[112:115]
ds_read_b128 a[220:223],v21 offset:6144
v_mfma_f32_16x16x32_bf16 a[20:23],a[168:171],a[224:227],a[20:23]
ds_read_b64_tr_b16 a[128:129],v22 offset:1024
v_mfma_f32_16x16x32_bf16 a[20:23],a[172:175],a[228:231],a[20:23]
ds_read_b64_tr_b16 a[130:131],v22 offset:3072
v_mfma_f32_16x16x32_bf16 a[52:55],a[172:175],a[236:239],a[52:55]
ds_read_b64_tr_b16 a[132:133],v22 offset:17408
)A"
R"A(v_mfma_f32_16x16x32_bf16 a[52:55],a[168:171],a[232:235],a[52:55]
ds_read_b64_tr_b16 a[134:135],v22 offset:19456
v_mfma_f32_16x16x32_bf16 a[84:87],a[168:171],a[240:243],a[84:87]
ds_read_b64_tr_b16 a[136:137],v22 offset:1032
v_mfma_f32_16x16x32_bf16 a[84:87],a[172:175],a[244:247],a[84:87]
ds_read_b64_tr_b16 a[138:139],v22 offset:3080
v_mfma_f32_16x16x32_bf16 a[116:119],a[172:175],a[252:255],a[116:119]
ds_read_b64_tr_b16 a[140:141],v22 offset:17416
v_mfma_f32_16x16x32_bf16 a[116:119],a[168:171],a[248:251],a[116:119]
ds_read_b64_tr_b16 a[142:143],v22 offset:19464
v_mfma_f32_16x16x32_bf16 a[24:27],a[176:179],a[224:227],a[24:27]
ds_read_b64_tr_b16 a[144:145],v22 offset:1280
v_mfma_f32_16x16x32_bf16 a[24:27],a[180:183],a[228:231],a[24:27]
ds_read_b64_tr_b16 a[146:147],v22 offset:3328
v_mfma_f32_16x16x32_bf16 a[56:59],a[180:183],a[236:239],a[56:59]
ds_read_b64_tr_b16 a[148:149],v22 offset:17664
v_mfma_f32_16x16x32_bf16 a[56:59],a[176:179],a[232:235],a[56:59]
ds_read_b64_tr_b16 a[150:151],v22 offset:19712
v_mfma_f32_16x16x32_bf16 a[88:91],a[176:179],a[240:243],a[88:91]
ds_read_b64_tr_b16 a[152:153],v22 offset:1288
v_mfma_f32_16x16x32_bf16 a[88:91],a[180:183],a[244:247],a[88:91]
ds_read_b64_tr_b16 a[154:155],v22 offset:3336
v_mfma_f32_16x16x32_bf16 a[120:123],a[180:183],a[252:255],a[120:123]
ds_read_b64_tr_b16 a[156:157],v22 offset:17672
v_mfma_f32_16x16x32_bf16 a[120:123],a[176:179],a[248:251],a[120:123]
ds_read_b64_tr_b16 a[158:159],v22 offset:19720
v_mfma_f32_16x16x32_bf16 a[28:31],a[184:187],a[224:227],a[28:31]
v_mfma_f32_16x16x32_bf16 a[28:31],a[188:191],a[228:231],a[28:31]
v_mfma_f32_16x16x32_bf16 a[60:63],a[188:191],a[236:239],a[60:63]
v_mfma_f32_16x16x32_bf16 a[60:63],a[184:187],a[232:235],a[60:63]
v_mfma_f32_16x16x32_bf16 a[92:95],a[184:187],a[240:243],a[92:95]
v_mfma_f32_16x16x32_bf16 a[92:95],a[188:191],a[244:247],a[92:95]
v_mfma_f32_16x16x32_bf16 a[124:127],a[188:191],a[252:255],a[124:127]
v_mfma_f32_16x16x32_bf16 a[124:127],a[184:187],a[248:251],a[124:127]
s_mov_b32 s53,3
.p2align 5
L_qp_%=:
s_waitcnt lgkmcnt(1)
v_mfma_f32_16x16x32_bf16 v[128:131],a[128:131],a[192:195],v[128:131]
s_add_u32 m0,s58,0
s_mov_b32 s86,s87
ds_read_b64_tr_b16 a[160:161],v22 offset:1536
v_mfma_f32_16x16x32_bf16 v[128:131],a[132:135],a[196:199],v[128:131]
s_add_u32 s60,s60,32768
buffer_load_dwordx4 v56,s[32:35],s56 offen lds
v_mfma_f32_16x16x32_bf16 v[136:139],a[132:135],a[204:207],v[136:139]
s_cmp_eq_u32 s60,131072
s_cselect_b32 s60,32768,s60
ds_read_b64_tr_b16 a[162:163],v22 offset:3584
v_mfma_f32_16x16x32_bf16 v[136:139],a[128:131],a[200:203],v[136:139]
s_load_dword s86,s[84:85],0x0 glc
s_addk_i32 m0,1024
ds_read_b64_tr_b16 a[164:165],v22 offset:17920
v_mfma_f32_16x16x32_bf16 v[144:147],a[128:131],a[208:211],v[144:147]
s_add_u32 s58,s58,32768
ds_read_b64_tr_b16 a[166:167],v22 offset:19968
v_xor_b32 v20,131072,v20
v_mfma_f32_16x16x32_bf16 v[144:147],a[132:135],a[212:215],v[144:147]
s_mov_b32 s90,L_att0x_%=-L_pc_%=+128
ds_read_b64_tr_b16 a[168:169],v22 offset:1544
v_xor_b32 v21,131072,v21
v_mfma_f32_16x16x32_bf16 v[152:155],a[132:135],a[220:223],v[152:155]
s_sub_u32 s53,s53,1
buffer_load_dwordx4 v57,s[32:35],s56 offen lds
v_mfma_f32_16x16x32_bf16 v[152:155],a[128:131],a[216:219],v[152:155]
s_cmp_ge_u32 s58,131072
s_cselect_b32 s62,98304,0
ds_read_b64_tr_b16 a[170:171],v22 offset:3592
v_mfma_f32_16x16x32_bf16 v[132:135],a[136:139],a[192:195],v[132:135]
s_sub_u32 s58,s58,s62
ds_read_b64_tr_b16 a[172:173],v22 offset:17928
v_mfma_f32_16x16x32_bf16 v[132:135],a[140:143],a[196:199],v[132:135]
s_addk_i32 m0,1024
ds_read_b64_tr_b16 a[174:175],v22 offset:19976
v_mfma_f32_16x16x32_bf16 v[140:143],a[140:143],a[204:207],v[140:143]
ds_read_b64_tr_b16 a[176:177],v22 offset:1792
v_mfma_f32_16x16x32_bf16 v[140:143],a[136:139],a[200:203],v[140:143]
ds_read_b64_tr_b16 a[178:179],v22 offset:3840
v_mfma_f32_16x16x32_bf16 v[148:151],a[136:139],a[208:211],v[148:151]
buffer_load_dwordx4 v58,s[32:35],s56 offen lds
v_mfma_f32_16x16x32_bf16 v[148:151],a[140:143],a[212:215],v[148:151]
ds_read_b64_tr_b16 a[180:181],v22 offset:18176
v_mfma_f32_16x16x32_bf16 v[156:159],a[140:143],a[220:223],v[156:159]
s_cmp_eq_u32 s86,s87
s_addc_u32 s79,s79,0
s_addk_i32 m0,1024
v_mfma_f32_16x16x32_bf16 v[156:159],a[136:139],a[216:219],v[156:159]
ds_read_b64_tr_b16 a[182:183],v22 offset:20224
v_mfma_f32_16x16x32_bf16 v[160:163],a[144:147],a[192:195],v[160:163]
ds_read_b64_tr_b16 a[184:185],v22 offset:1800
v_mfma_f32_16x16x32_bf16 v[160:163],a[148:151],a[196:199],v[160:163]
s_cmp_eq_u32 s86,s87
s_addc_u32 s79,s79,0
ds_read_b64_tr_b16 a[186:187],v22 offset:3848
v_mfma_f32_16x16x32_bf16 v[168:171],a[148:151],a[204:207],v[168:171]
buffer_load_dwordx4 v59,s[32:35],s56 offen lds
v_mfma_f32_16x16x32_bf16 v[168:171],a[144:147],a[200:203],v[168:171]
ds_read_b64_tr_b16 a[188:189],v22 offset:18184
v_mfma_f32_16x16x32_bf16 v[176:179],a[144:147],a[208:211],v[176:179]
s_cmp_eq_u32 s86,s87
s_addc_u32 s79,s79,0
s_addk_i32 m0,1024
v_mfma_f32_16x16x32_bf16 v[176:179],a[148:151],a[212:215],v[176:179]
ds_read_b64_tr_b16 a[190:191],v22 offset:20232
v_mfma_f32_16x16x32_bf16 v[184:187],a[148:151],a[220:223],v[184:187]
ds_read_b64_tr_b16 a[128:129],v22
v_mfma_f32_16x16x32_bf16 v[184:187],a[144:147],a[216:219],v[184:187]
s_cmp_eq_u32 s86,s87
s_addc_u32 s79,s79,0
ds_read_b64_tr_b16 a[130:131],v22 offset:2048
v_mfma_f32_16x16x32_bf16 v[164:167],a[152:155],a[192:195],v[164:167]
s_waitcnt lgkmcnt(5)
buffer_load_dwordx4 v60,s[32:35],s56 offen lds
v_mfma_f32_16x16x32_bf16 v[164:167],a[156:159],a[196:199],v[164:167]
s_addk_i32 m0,1024
ds_read_b64_tr_b16 a[132:133],v22 offset:16384
v_mfma_f32_16x16x32_bf16 v[172:175],a[156:159],a[204:207],v[172:175]
s_cmp_eq_u32 s86,s87
s_addc_u32 s79,s79,0
ds_read_b64_tr_b16 a[134:135],v22 offset:18432
v_mfma_f32_16x16x32_bf16 v[172:175],a[152:155],a[200:203],v[172:175]
ds_read_b64_tr_b16 a[136:137],v22 offset:8
v_mfma_f32_16x16x32_bf16 v[180:183],a[152:155],a[208:211],v[180:183]
buffer_load_dwordx4 v61,s[32:35],s56 offen lds
v_mfma_f32_16x16x32_bf16 v[180:183],a[156:159],a[212:215],v[180:183]
s_cmp_eq_u32 s86,s87
s_addc_u32 s79,s79,0
ds_read_b64_tr_b16 a[138:139],v22 offset:2056
v_mfma_f32_16x16x32_bf16 v[188:191],a[156:159],a[220:223],v[188:191]
ds_read_b64_tr_b16 a[140:141],v22 offset:16392
v_mfma_f32_16x16x32_bf16 v[188:191],a[152:155],a[216:219],v[188:191]
s_addk_i32 m0,1024
ds_read_b64_tr_b16 a[142:143],v22 offset:18440
v_mfma_f32_16x16x32_bf16 v[192:195],a[160:163],a[192:195],v[192:195]
s_cmp_eq_u32 s86,s87
s_addc_u32 s79,s79,0
ds_read_b64_tr_b16 a[144:145],v22 offset:256
v_mfma_f32_16x16x32_bf16 v[192:195],a[164:167],a[196:199],v[192:195]
ds_read_b64_tr_b16 a[146:147],v22 offset:2304
v_mfma_f32_16x16x32_bf16 v[200:203],a[164:167],a[204:207],v[200:203]
buffer_load_dwordx4 v62,s[32:35],s56 offen lds
v_mfma_f32_16x16x32_bf16 v[200:203],a[160:163],a[200:203],v[200:203]
s_cmp_eq_u32 s86,s87
s_addc_u32 s79,s79,0
ds_read_b64_tr_b16 a[148:149],v22 offset:16640
v_mfma_f32_16x16x32_bf16 v[208:211],a[160:163],a[208:211],v[208:211]
s_addk_i32 m0,1024
ds_read_b64_tr_b16 a[150:151],v22 offset:18688
v_mfma_f32_16x16x32_bf16 v[208:211],a[164:167],a[212:215],v[208:211]
ds_read_b64_tr_b16 a[152:153],v22 offset:264
v_mfma_f32_16x16x32_bf16 v[216:219],a[164:167],a[220:223],v[216:219]
s_cmp_eq_u32 s86,s87
s_addc_u32 s79,s79,0
ds_read_b64_tr_b16 a[154:155],v22 offset:2312
v_mfma_f32_16x16x32_bf16 v[216:219],a[160:163],a[216:219],v[216:219]
buffer_load_dwordx4 v63,s[32:35],s56 offen lds
v_mfma_f32_16x16x32_bf16 v[196:199],a[168:171],a[192:195],v[196:199]
s_add_u32 s56,s56,524288
ds_read_b64_tr_b16 a[156:157],v22 offset:16648
v_mfma_f32_16x16x32_bf16 v[196:199],a[172:175],a[196:199],v[196:199]
s_cmp_eq_u32 s86,s87
s_addc_u32 s79,s79,0
ds_read_b64_tr_b16 a[158:159],v22 offset:18696
v_mfma_f32_16x16x32_bf16 v[204:207],a[172:175],a[204:207],v[204:207]
s_add_u32 m0,s59,0
s_xor_b32 s59,s59,131072
ds_read_b64_tr_b16 a[160:161],v22 offset:512
v_mfma_f32_16x16x32_bf16 v[204:207],a[168:171],a[200:203],v[204:207]
ds_read_b64_tr_b16 a[162:163],v22 offset:2560
v_mfma_f32_16x16x32_bf16 v[212:215],a[168:171],a[208:211],v[212:215]
s_cmp_eq_u32 s86,s87
s_addc_u32 s79,s79,0
ds_read_b64_tr_b16 a[164:165],v22 offset:16896
v_mfma_f32_16x16x32_bf16 v[212:215],a[172:175],a[212:215],v[212:215]
buffer_load_dwordx4 v48,s[28:31],s57 offen lds
v_mfma_f32_16x16x32_bf16 v[220:223],a[172:175],a[220:223],v[220:223]
ds_read_b64_tr_b16 a[166:167],v22 offset:18944
v_mfma_f32_16x16x32_bf16 v[220:223],a[168:171],a[216:219],v[220:223]
s_cmp_eq_u32 s86,s87
s_addc_u32 s79,s79,0
ds_read_b64_tr_b16 a[168:169],v22 offset:520
v_mfma_f32_16x16x32_bf16 v[224:227],a[176:179],a[192:195],v[224:227]
ds_read_b64_tr_b16 a[170:171],v22 offset:2568
v_mfma_f32_16x16x32_bf16 v[224:227],a[180:183],a[196:199],v[224:227]
ds_read_b64_tr_b16 a[172:173],v22 offset:16904
v_mfma_f32_16x16x32_bf16 v[232:235],a[180:183],a[204:207],v[232:235]
s_cmp_eq_u32 s86,s87
s_addc_u32 s79,s79,0
ds_read_b64_tr_b16 a[174:175],v22 offset:18952
v_mfma_f32_16x16x32_bf16 v[232:235],a[176:179],a[200:203],v[232:235]
buffer_load_dwordx4 v49,s[28:31],s57 offen offset:1024 lds
v_mfma_f32_16x16x32_bf16 v[240:243],a[176:179],a[208:211],v[240:243]
v_mfma_f32_16x16x32_bf16 v[240:243],a[180:183],a[212:215],v[240:243]
s_cmp_eq_u32 s86,s87
s_addc_u32 s79,s79,0
v_mfma_f32_16x16x32_bf16 v[248:251],a[180:183],a[220:223],v[248:251]
ds_read_b64_tr_b16 a[180:181],v22 offset:17152
v_mfma_f32_16x16x32_bf16 v[248:251],a[176:179],a[216:219],v[248:251]
s_waitcnt lgkmcnt(4)
ds_read_b64_tr_b16 a[176:177],v22 offset:768
v_mfma_f32_16x16x32_bf16 v[228:231],a[184:187],a[192:195],v[228:231]
s_cmp_eq_u32 s86,s87
s_addc_u32 s79,s79,0
ds_read_b64_tr_b16 a[178:179],v22 offset:2816
v_mfma_f32_16x16x32_bf16 v[228:231],a[188:191],a[196:199],v[228:231]
buffer_load_dwordx4 v50,s[28:31],s57 offen offset:2048 lds
v_mfma_f32_16x16x32_bf16 v[236:239],a[188:191],a[204:207],v[236:239]
ds_read_b64_tr_b16 a[182:183],v22 offset:19200
v_mfma_f32_16x16x32_bf16 v[236:239],a[184:187],a[200:203],v[236:239]
s_cmp_eq_u32 s86,s87
s_addc_u32 s79,s79,0
v_mfma_f32_16x16x32_bf16 v[244:247],a[184:187],a[208:211],v[244:247]
v_mfma_f32_16x16x32_bf16 v[244:247],a[188:191],a[212:215],v[244:247]
buffer_load_dwordx4 v51,s[28:31],s57 offen offset:3072 lds
v_mfma_f32_16x16x32_bf16 v[252:255],a[188:191],a[220:223],v[252:255]
ds_read_b64_tr_b16 a[188:189],v22 offset:17160
v_mfma_f32_16x16x32_bf16 v[252:255],a[184:187],a[216:219],v[252:255]
ds_read_b64_tr_b16 a[184:185],v22 offset:776
v_mfma_f32_16x16x32_bf16 a[0:3],a[128:131],a[192:195],a[0:3]
s_addk_i32 m0,4096
ds_read_b64_tr_b16 a[186:187],v22 offset:2824
v_mfma_f32_16x16x32_bf16 a[0:3],a[132:135],a[196:199],a[0:3]
ds_read_b64_tr_b16 a[190:191],v22 offset:19208
v_add_u32 v22,s60,v23
v_mfma_f32_16x16x32_bf16 a[32:35],a[132:135],a[204:207],a[32:35]
s_add_u32 s60,s60,32768
s_cmp_eq_u32 s60,131072
s_cselect_b32 s60,32768,s60
v_mfma_f32_16x16x32_bf16 a[32:35],a[128:131],a[200:203],a[32:35]
buffer_load_dwordx4 v52,s[28:31],s57 offen lds
v_mfma_f32_16x16x32_bf16 a[64:67],a[128:131],a[208:211],a[64:67]
v_mfma_f32_16x16x32_bf16 a[64:67],a[132:135],a[212:215],a[64:67]
v_mfma_f32_16x16x32_bf16 a[96:99],a[132:135],a[220:223],a[96:99]
v_mfma_f32_16x16x32_bf16 a[96:99],a[128:131],a[216:219],a[96:99]
v_mfma_f32_16x16x32_bf16 a[4:7],a[136:139],a[192:195],a[4:7]
buffer_load_dwordx4 v53,s[28:31],s57 offen offset:1024 lds
v_mfma_f32_16x16x32_bf16 a[4:7],a[140:143],a[196:199],a[4:7]
v_mfma_f32_16x16x32_bf16 a[36:39],a[140:143],a[204:207],a[36:39]
v_mfma_f32_16x16x32_bf16 a[36:39],a[136:139],a[200:203],a[36:39]
v_mfma_f32_16x16x32_bf16 a[68:71],a[136:139],a[208:211],a[68:71]
v_mfma_f32_16x16x32_bf16 a[68:71],a[140:143],a[212:215],a[68:71]
v_mfma_f32_16x16x32_bf16 a[100:103],a[140:143],a[220:223],a[100:103]
buffer_load_dwordx4 v54,s[28:31],s57 offen offset:2048 lds
v_mfma_f32_16x16x32_bf16 a[100:103],a[136:139],a[216:219],a[100:103]
v_mfma_f32_16x16x32_bf16 a[8:11],a[144:147],a[192:195],a[8:11]
v_mfma_f32_16x16x32_bf16 a[8:11],a[148:151],a[196:199],a[8:11]
v_mfma_f32_16x16x32_bf16 a[40:43],a[148:151],a[204:207],a[40:43]
)A"
R"A(v_mfma_f32_16x16x32_bf16 a[40:43],a[144:147],a[200:203],a[40:43]
buffer_load_dwordx4 v55,s[28:31],s57 offen offset:3072 lds
v_mfma_f32_16x16x32_bf16 a[72:75],a[144:147],a[208:211],a[72:75]
s_addk_i32 s57,128
v_mfma_f32_16x16x32_bf16 a[72:75],a[148:151],a[212:215],a[72:75]
v_mfma_f32_16x16x32_bf16 a[104:107],a[148:151],a[220:223],a[104:107]
v_mfma_f32_16x16x32_bf16 a[104:107],a[144:147],a[216:219],a[104:107]
v_mfma_f32_16x16x32_bf16 a[12:15],a[152:155],a[192:195],a[12:15]
v_mfma_f32_16x16x32_bf16 a[12:15],a[156:159],a[196:199],a[12:15]
v_mfma_f32_16x16x32_bf16 a[44:47],a[156:159],a[204:207],a[44:47]
buffer_load_dword v16,v15,s[72:75],s83 offen
v_mfma_f32_16x16x32_bf16 a[44:47],a[152:155],a[200:203],a[44:47]
s_addk_i32 s83,896
v_mfma_f32_16x16x32_bf16 a[76:79],a[152:155],a[208:211],a[76:79]
v_mfma_f32_16x16x32_bf16 a[76:79],a[156:159],a[212:215],a[76:79]
v_mfma_f32_16x16x32_bf16 a[108:111],a[156:159],a[220:223],a[108:111]
v_mfma_f32_16x16x32_bf16 a[108:111],a[152:155],a[216:219],a[108:111]
s_waitcnt vmcnt(17) lgkmcnt(0)
v_mfma_f32_16x16x32_bf16 a[16:19],a[160:163],a[192:195],a[16:19]
s_barrier
ds_read_b128 a[224:227],v20
v_mfma_f32_16x16x32_bf16 a[16:19],a[164:167],a[196:199],a[16:19]
ds_read_b64_tr_b16 a[128:129],v22 offset:1024
v_mfma_f32_16x16x32_bf16 a[48:51],a[164:167],a[204:207],a[48:51]
ds_read_b64_tr_b16 a[130:131],v22 offset:3072
v_mfma_f32_16x16x32_bf16 a[48:51],a[160:163],a[200:203],a[48:51]
ds_read_b128 a[228:231],v21
v_mfma_f32_16x16x32_bf16 a[80:83],a[160:163],a[208:211],a[80:83]
ds_read_b64_tr_b16 a[132:133],v22 offset:17408
v_mfma_f32_16x16x32_bf16 a[80:83],a[164:167],a[212:215],a[80:83]
ds_read_b64_tr_b16 a[134:135],v22 offset:19456
v_mfma_f32_16x16x32_bf16 a[112:115],a[164:167],a[220:223],a[112:115]
ds_read_b128 a[232:235],v20 offset:2048
v_mfma_f32_16x16x32_bf16 a[112:115],a[160:163],a[216:219],a[112:115]
ds_read_b128 a[236:239],v21 offset:2048
v_mfma_f32_16x16x32_bf16 a[20:23],a[168:171],a[192:195],a[20:23]
ds_read_b128 a[240:243],v20 offset:4096
v_mfma_f32_16x16x32_bf16 a[20:23],a[172:175],a[196:199],a[20:23]
ds_read_b128 a[244:247],v21 offset:4096
v_mfma_f32_16x16x32_bf16 a[52:55],a[172:175],a[204:207],a[52:55]
ds_read_b128 a[248:251],v20 offset:6144
v_xor_b32 v20,131072,v20
v_mfma_f32_16x16x32_bf16 a[52:55],a[168:171],a[200:203],a[52:55]
ds_read_b128 a[252:255],v21 offset:6144
v_xor_b32 v21,131072,v21
v_mfma_f32_16x16x32_bf16 a[84:87],a[168:171],a[208:211],a[84:87]
ds_read_b64_tr_b16 a[136:137],v22 offset:1032
v_mfma_f32_16x16x32_bf16 a[84:87],a[172:175],a[212:215],a[84:87]
ds_read_b64_tr_b16 a[138:139],v22 offset:3080
v_mfma_f32_16x16x32_bf16 a[116:119],a[172:175],a[220:223],a[116:119]
ds_read_b64_tr_b16 a[140:141],v22 offset:17416
v_mfma_f32_16x16x32_bf16 a[116:119],a[168:171],a[216:219],a[116:119]
ds_read_b64_tr_b16 a[142:143],v22 offset:19464
v_mfma_f32_16x16x32_bf16 a[24:27],a[176:179],a[192:195],a[24:27]
ds_read_b64_tr_b16 a[144:145],v22 offset:1280
v_mfma_f32_16x16x32_bf16 a[24:27],a[180:183],a[196:199],a[24:27]
ds_read_b64_tr_b16 a[146:147],v22 offset:3328
v_mfma_f32_16x16x32_bf16 a[56:59],a[180:183],a[204:207],a[56:59]
ds_read_b64_tr_b16 a[148:149],v22 offset:17664
v_mfma_f32_16x16x32_bf16 a[56:59],a[176:179],a[200:203],a[56:59]
ds_read_b64_tr_b16 a[150:151],v22 offset:19712
v_mfma_f32_16x16x32_bf16 a[88:91],a[176:179],a[208:211],a[88:91]
ds_read_b64_tr_b16 a[152:153],v22 offset:1288
v_mfma_f32_16x16x32_bf16 a[88:91],a[180:183],a[212:215],a[88:91]
ds_read_b64_tr_b16 a[154:155],v22 offset:3336
v_mfma_f32_16x16x32_bf16 a[120:123],a[180:183],a[220:223],a[120:123]
ds_read_b64_tr_b16 a[156:157],v22 offset:17672
v_mfma_f32_16x16x32_bf16 a[120:123],a[176:179],a[216:219],a[120:123]
ds_read_b64_tr_b16 a[158:159],v22 offset:19720
v_mfma_f32_16x16x32_bf16 a[28:31],a[184:187],a[192:195],a[28:31]
ds_read_b64_tr_b16 a[160:161],v22 offset:1536
v_mfma_f32_16x16x32_bf16 a[28:31],a[188:191],a[196:199],a[28:31]
ds_read_b64_tr_b16 a[162:163],v22 offset:3584
v_mfma_f32_16x16x32_bf16 a[60:63],a[188:191],a[204:207],a[60:63]
ds_read_b64_tr_b16 a[164:165],v22 offset:17920
v_mfma_f32_16x16x32_bf16 a[60:63],a[184:187],a[200:203],a[60:63]
ds_read_b64_tr_b16 a[166:167],v22 offset:19968
v_mfma_f32_16x16x32_bf16 a[92:95],a[184:187],a[208:211],a[92:95]
ds_read_b64_tr_b16 a[168:169],v22 offset:1544
v_mfma_f32_16x16x32_bf16 a[92:95],a[188:191],a[212:215],a[92:95]
ds_read_b64_tr_b16 a[170:171],v22 offset:3592
v_mfma_f32_16x16x32_bf16 a[124:127],a[188:191],a[220:223],a[124:127]
s_add_u32 m0,s58,0
s_add_u32 s58,s58,32768
ds_read_b64_tr_b16 a[172:173],v22 offset:17928
v_mfma_f32_16x16x32_bf16 a[124:127],a[184:187],a[216:219],a[124:127]
s_cmp_ge_u32 s58,131072
s_cselect_b32 s62,98304,0
ds_read_b64_tr_b16 a[174:175],v22 offset:19976
s_waitcnt lgkmcnt(8)
v_mfma_f32_16x16x32_bf16 v[128:131],a[128:131],a[224:227],v[128:131]
s_mov_b32 s86,s87
s_sub_u32 s58,s58,s62
ds_read_b64_tr_b16 a[176:177],v22 offset:1792
v_mfma_f32_16x16x32_bf16 v[128:131],a[132:135],a[228:231],v[128:131]
buffer_load_dwordx4 v56,s[32:35],s56 offen lds
v_mfma_f32_16x16x32_bf16 v[136:139],a[132:135],a[236:239],v[136:139]
ds_read_b64_tr_b16 a[178:179],v22 offset:3840
v_mfma_f32_16x16x32_bf16 v[136:139],a[128:131],a[232:235],v[136:139]
s_load_dword s86,s[84:85],0x2000 glc
s_addk_i32 m0,1024
ds_read_b64_tr_b16 a[180:181],v22 offset:18176
v_mfma_f32_16x16x32_bf16 v[144:147],a[128:131],a[240:243],v[144:147]
s_addk_i32 s84,256
ds_read_b64_tr_b16 a[182:183],v22 offset:20224
v_mfma_f32_16x16x32_bf16 v[144:147],a[132:135],a[244:247],v[144:147]
ds_read_b64_tr_b16 a[184:185],v22 offset:1800
v_mfma_f32_16x16x32_bf16 v[152:155],a[132:135],a[252:255],v[152:155]
buffer_load_dwordx4 v57,s[32:35],s56 offen lds
v_mfma_f32_16x16x32_bf16 v[152:155],a[128:131],a[248:251],v[152:155]
ds_read_b64_tr_b16 a[186:187],v22 offset:3848
v_mfma_f32_16x16x32_bf16 v[132:135],a[136:139],a[224:227],v[132:135]
ds_read_b64_tr_b16 a[188:189],v22 offset:18184
v_mfma_f32_16x16x32_bf16 v[132:135],a[140:143],a[228:231],v[132:135]
s_addk_i32 m0,1024
ds_read_b64_tr_b16 a[190:191],v22 offset:20232
v_mfma_f32_16x16x32_bf16 v[140:143],a[140:143],a[236:239],v[140:143]
ds_read_b64_tr_b16 a[128:129],v22
v_mfma_f32_16x16x32_bf16 v[140:143],a[136:139],a[232:235],v[140:143]
ds_read_b64_tr_b16 a[130:131],v22 offset:2048
v_mfma_f32_16x16x32_bf16 v[148:151],a[136:139],a[240:243],v[148:151]
buffer_load_dwordx4 v58,s[32:35],s56 offen lds
v_mfma_f32_16x16x32_bf16 v[148:151],a[140:143],a[244:247],v[148:151]
ds_read_b64_tr_b16 a[132:133],v22 offset:16384
v_mfma_f32_16x16x32_bf16 v[156:159],a[140:143],a[252:255],v[156:159]
s_cmp_eq_u32 s86,s87
s_subb_u32 s79,s79,0
s_addk_i32 m0,1024
v_mfma_f32_16x16x32_bf16 v[156:159],a[136:139],a[248:251],v[156:159]
ds_read_b64_tr_b16 a[134:135],v22 offset:18432
v_mfma_f32_16x16x32_bf16 v[160:163],a[144:147],a[224:227],v[160:163]
ds_read_b64_tr_b16 a[136:137],v22 offset:8
v_mfma_f32_16x16x32_bf16 v[160:163],a[148:151],a[228:231],v[160:163]
s_cmp_eq_u32 s86,s87
s_subb_u32 s79,s79,0
ds_read_b64_tr_b16 a[138:139],v22 offset:2056
v_mfma_f32_16x16x32_bf16 v[168:171],a[148:151],a[236:239],v[168:171]
buffer_load_dwordx4 v59,s[32:35],s56 offen lds
v_mfma_f32_16x16x32_bf16 v[168:171],a[144:147],a[232:235],v[168:171]
ds_read_b64_tr_b16 a[140:141],v22 offset:16392
v_mfma_f32_16x16x32_bf16 v[176:179],a[144:147],a[240:243],v[176:179]
s_cmp_eq_u32 s86,s87
s_subb_u32 s79,s79,0
s_addk_i32 m0,1024
v_mfma_f32_16x16x32_bf16 v[176:179],a[148:151],a[244:247],v[176:179]
ds_read_b64_tr_b16 a[142:143],v22 offset:18440
v_mfma_f32_16x16x32_bf16 v[184:187],a[148:151],a[252:255],v[184:187]
ds_read_b64_tr_b16 a[148:149],v22 offset:16640
v_mfma_f32_16x16x32_bf16 v[184:187],a[144:147],a[248:251],v[184:187]
s_cmp_eq_u32 s86,s87
s_subb_u32 s79,s79,0
ds_read_b64_tr_b16 a[144:145],v22 offset:256
v_mfma_f32_16x16x32_bf16 v[164:167],a[152:155],a[224:227],v[164:167]
buffer_load_dwordx4 v60,s[32:35],s56 offen lds
v_mfma_f32_16x16x32_bf16 v[164:167],a[156:159],a[228:231],v[164:167]
s_addk_i32 m0,1024
ds_read_b64_tr_b16 a[146:147],v22 offset:2304
v_mfma_f32_16x16x32_bf16 v[172:175],a[156:159],a[236:239],v[172:175]
s_cmp_eq_u32 s86,s87
s_subb_u32 s79,s79,0
ds_read_b64_tr_b16 a[150:151],v22 offset:18688
v_mfma_f32_16x16x32_bf16 v[172:175],a[152:155],a[232:235],v[172:175]
v_mfma_f32_16x16x32_bf16 v[180:183],a[152:155],a[240:243],v[180:183]
buffer_load_dwordx4 v61,s[32:35],s56 offen lds
v_mfma_f32_16x16x32_bf16 v[180:183],a[156:159],a[244:247],v[180:183]
s_cmp_eq_u32 s86,s87
s_subb_u32 s79,s79,0
v_mfma_f32_16x16x32_bf16 v[188:191],a[156:159],a[252:255],v[188:191]
ds_read_b64_tr_b16 a[156:157],v22 offset:16648
v_mfma_f32_16x16x32_bf16 v[188:191],a[152:155],a[248:251],v[188:191]
s_addk_i32 m0,1024
ds_read_b64_tr_b16 a[152:153],v22 offset:264
s_waitcnt lgkmcnt(4)
v_mfma_f32_16x16x32_bf16 v[192:195],a[160:163],a[224:227],v[192:195]
s_cmp_eq_u32 s86,s87
s_subb_u32 s79,s79,0
ds_read_b64_tr_b16 a[154:155],v22 offset:2312
v_mfma_f32_16x16x32_bf16 v[192:195],a[164:167],a[228:231],v[192:195]
ds_read_b64_tr_b16 a[158:159],v22 offset:18696
v_mfma_f32_16x16x32_bf16 v[200:203],a[164:167],a[236:239],v[200:203]
buffer_load_dwordx4 v62,s[32:35],s56 offen lds
v_mfma_f32_16x16x32_bf16 v[200:203],a[160:163],a[232:235],v[200:203]
s_cmp_eq_u32 s86,s87
s_subb_u32 s79,s79,0
v_mfma_f32_16x16x32_bf16 v[208:211],a[160:163],a[240:243],v[208:211]
s_addk_i32 m0,1024
v_mfma_f32_16x16x32_bf16 v[208:211],a[164:167],a[244:247],v[208:211]
v_mfma_f32_16x16x32_bf16 v[216:219],a[164:167],a[252:255],v[216:219]
s_cmp_eq_u32 s86,s87
s_subb_u32 s79,s79,0
ds_read_b64_tr_b16 a[164:165],v22 offset:16896
v_mfma_f32_16x16x32_bf16 v[216:219],a[160:163],a[248:251],v[216:219]
buffer_load_dwordx4 v63,s[32:35],s56 offen lds
v_mfma_f32_16x16x32_bf16 v[196:199],a[168:171],a[224:227],v[196:199]
s_add_u32 s56,s56,524288
ds_read_b64_tr_b16 a[160:161],v22 offset:512
v_mfma_f32_16x16x32_bf16 v[196:199],a[172:175],a[228:231],v[196:199]
s_cmp_eq_u32 s86,s87
s_subb_u32 s79,s79,0
ds_read_b64_tr_b16 a[162:163],v22 offset:2560
v_mfma_f32_16x16x32_bf16 v[204:207],a[172:175],a[236:239],v[204:207]
s_add_u32 m0,s59,0
s_xor_b32 s59,s59,131072
ds_read_b64_tr_b16 a[166:167],v22 offset:18944
v_mfma_f32_16x16x32_bf16 v[204:207],a[168:171],a[232:235],v[204:207]
v_mfma_f32_16x16x32_bf16 v[212:215],a[168:171],a[240:243],v[212:215]
s_cmp_eq_u32 s86,s87
s_subb_u32 s79,s79,0
v_mfma_f32_16x16x32_bf16 v[212:215],a[172:175],a[244:247],v[212:215]
buffer_load_dwordx4 v48,s[28:31],s57 offen lds
v_mfma_f32_16x16x32_bf16 v[220:223],a[172:175],a[252:255],v[220:223]
ds_read_b64_tr_b16 a[172:173],v22 offset:16904
v_mfma_f32_16x16x32_bf16 v[220:223],a[168:171],a[248:251],v[220:223]
s_cmp_eq_u32 s86,s87
s_subb_u32 s79,s79,0
ds_read_b64_tr_b16 a[168:169],v22 offset:520
v_mfma_f32_16x16x32_bf16 v[224:227],a[176:179],a[224:227],v[224:227]
ds_read_b64_tr_b16 a[170:171],v22 offset:2568
v_mfma_f32_16x16x32_bf16 v[224:227],a[180:183],a[228:231],v[224:227]
ds_read_b64_tr_b16 a[174:175],v22 offset:18952
v_mfma_f32_16x16x32_bf16 v[232:235],a[180:183],a[236:239],v[232:235]
s_cmp_eq_u32 s86,s87
s_subb_u32 s79,s79,0
v_mfma_f32_16x16x32_bf16 v[232:235],a[176:179],a[232:235],v[232:235]
buffer_load_dwordx4 v49,s[28:31],s57 offen offset:1024 lds
v_mfma_f32_16x16x32_bf16 v[240:243],a[176:179],a[240:243],v[240:243]
v_mfma_f32_16x16x32_bf16 v[240:243],a[180:183],a[244:247],v[240:243]
s_cmp_eq_u32 s86,s87
s_subb_u32 s79,s79,0
v_mfma_f32_16x16x32_bf16 v[248:251],a[180:183],a[252:255],v[248:251]
ds_read_b64_tr_b16 a[180:181],v22 offset:17152
v_mfma_f32_16x16x32_bf16 v[248:251],a[176:179],a[248:251],v[248:251]
ds_read_b64_tr_b16 a[176:177],v22 offset:768
v_mfma_f32_16x16x32_bf16 v[228:231],a[184:187],a[224:227],v[228:231]
s_cmp_eq_u32 s86,s87
s_subb_u32 s79,s79,0
ds_read_b64_tr_b16 a[178:179],v22 offset:2816
v_mfma_f32_16x16x32_bf16 v[228:231],a[188:191],a[228:231],v[228:231]
buffer_load_dwordx4 v50,s[28:31],s57 offen offset:2048 lds
v_mfma_f32_16x16x32_bf16 v[236:239],a[188:191],a[236:239],v[236:239]
ds_read_b64_tr_b16 a[182:183],v22 offset:19200
v_mfma_f32_16x16x32_bf16 v[236:239],a[184:187],a[232:235],v[236:239]
)A"
R"A(s_cmp_eq_u32 s86,s87
s_subb_u32 s79,s79,0
v_mfma_f32_16x16x32_bf16 v[244:247],a[184:187],a[240:243],v[244:247]
v_mfma_f32_16x16x32_bf16 v[244:247],a[188:191],a[244:247],v[244:247]
buffer_load_dwordx4 v51,s[28:31],s57 offen offset:3072 lds
v_mfma_f32_16x16x32_bf16 v[252:255],a[188:191],a[252:255],v[252:255]
ds_read_b64_tr_b16 a[188:189],v22 offset:17160
v_mfma_f32_16x16x32_bf16 v[252:255],a[184:187],a[248:251],v[252:255]
ds_read_b64_tr_b16 a[184:185],v22 offset:776
v_mfma_f32_16x16x32_bf16 a[0:3],a[128:131],a[224:227],a[0:3]
s_addk_i32 m0,4096
ds_read_b64_tr_b16 a[186:187],v22 offset:2824
v_mfma_f32_16x16x32_bf16 a[0:3],a[132:135],a[228:231],a[0:3]
ds_read_b64_tr_b16 a[190:191],v22 offset:19208
v_add_u32 v22,s60,v23
v_mfma_f32_16x16x32_bf16 a[32:35],a[132:135],a[236:239],a[32:35]
v_mfma_f32_16x16x32_bf16 a[32:35],a[128:131],a[232:235],a[32:35]
buffer_load_dwordx4 v52,s[28:31],s57 offen lds
v_mfma_f32_16x16x32_bf16 a[64:67],a[128:131],a[240:243],a[64:67]
v_mfma_f32_16x16x32_bf16 a[64:67],a[132:135],a[244:247],a[64:67]
v_mfma_f32_16x16x32_bf16 a[96:99],a[132:135],a[252:255],a[96:99]
v_mfma_f32_16x16x32_bf16 a[96:99],a[128:131],a[248:251],a[96:99]
v_mfma_f32_16x16x32_bf16 a[4:7],a[136:139],a[224:227],a[4:7]
buffer_load_dwordx4 v53,s[28:31],s57 offen offset:1024 lds
v_mfma_f32_16x16x32_bf16 a[4:7],a[140:143],a[228:231],a[4:7]
v_mfma_f32_16x16x32_bf16 a[36:39],a[140:143],a[236:239],a[36:39]
v_mfma_f32_16x16x32_bf16 a[36:39],a[136:139],a[232:235],a[36:39]
v_mfma_f32_16x16x32_bf16 a[68:71],a[136:139],a[240:243],a[68:71]
v_mfma_f32_16x16x32_bf16 a[68:71],a[140:143],a[244:247],a[68:71]
v_mfma_f32_16x16x32_bf16 a[100:103],a[140:143],a[252:255],a[100:103]
buffer_load_dwordx4 v54,s[28:31],s57 offen offset:2048 lds
v_mfma_f32_16x16x32_bf16 a[100:103],a[136:139],a[248:251],a[100:103]
s_waitcnt lgkmcnt(0)
v_mfma_f32_16x16x32_bf16 a[8:11],a[144:147],a[224:227],a[8:11]
v_mfma_f32_16x16x32_bf16 a[8:11],a[148:151],a[228:231],a[8:11]
v_mfma_f32_16x16x32_bf16 a[40:43],a[148:151],a[236:239],a[40:43]
v_mfma_f32_16x16x32_bf16 a[40:43],a[144:147],a[232:235],a[40:43]
buffer_load_dwordx4 v55,s[28:31],s57 offen offset:3072 lds
v_mfma_f32_16x16x32_bf16 a[72:75],a[144:147],a[240:243],a[72:75]
s_addk_i32 s57,128
v_mfma_f32_16x16x32_bf16 a[72:75],a[148:151],a[244:247],a[72:75]
v_mfma_f32_16x16x32_bf16 a[104:107],a[148:151],a[252:255],a[104:107]
v_mfma_f32_16x16x32_bf16 a[104:107],a[144:147],a[248:251],a[104:107]
v_mfma_f32_16x16x32_bf16 a[12:15],a[152:155],a[224:227],a[12:15]
v_mfma_f32_16x16x32_bf16 a[12:15],a[156:159],a[228:231],a[12:15]
v_mfma_f32_16x16x32_bf16 a[44:47],a[156:159],a[236:239],a[44:47]
buffer_load_dword v16,v15,s[72:75],s83 offen
v_mfma_f32_16x16x32_bf16 a[44:47],a[152:155],a[232:235],a[44:47]
s_addk_i32 s83,896
v_mfma_f32_16x16x32_bf16 a[76:79],a[152:155],a[240:243],a[76:79]
v_mfma_f32_16x16x32_bf16 a[76:79],a[156:159],a[244:247],a[76:79]
v_mfma_f32_16x16x32_bf16 a[108:111],a[156:159],a[252:255],a[108:111]
v_mfma_f32_16x16x32_bf16 a[108:111],a[152:155],a[248:251],a[108:111]
s_waitcnt vmcnt(17) lgkmcnt(0)
v_mfma_f32_16x16x32_bf16 a[16:19],a[160:163],a[224:227],a[16:19]
s_barrier
ds_read_b128 a[192:195],v20
v_mfma_f32_16x16x32_bf16 a[16:19],a[164:167],a[228:231],a[16:19]
ds_read_b128 a[196:199],v21
v_mfma_f32_16x16x32_bf16 a[48:51],a[164:167],a[236:239],a[48:51]
ds_read_b128 a[200:203],v20 offset:2048
v_mfma_f32_16x16x32_bf16 a[48:51],a[160:163],a[232:235],a[48:51]
ds_read_b128 a[204:207],v21 offset:2048
v_mfma_f32_16x16x32_bf16 a[80:83],a[160:163],a[240:243],a[80:83]
ds_read_b128 a[208:211],v20 offset:4096
v_mfma_f32_16x16x32_bf16 a[80:83],a[164:167],a[244:247],a[80:83]
ds_read_b128 a[212:215],v21 offset:4096
v_mfma_f32_16x16x32_bf16 a[112:115],a[164:167],a[252:255],a[112:115]
buffer_load_dword v26,v24,s[88:91],s93 offen
v_mfma_f32_16x16x32_bf16 a[112:115],a[160:163],a[248:251],a[112:115]
ds_read_b128 a[216:219],v20 offset:6144
v_mfma_f32_16x16x32_bf16 a[20:23],a[168:171],a[224:227],a[20:23]
ds_read_b128 a[220:223],v21 offset:6144
v_mfma_f32_16x16x32_bf16 a[20:23],a[172:175],a[228:231],a[20:23]
ds_read_b64_tr_b16 a[128:129],v22 offset:1024
v_mfma_f32_16x16x32_bf16 a[52:55],a[172:175],a[236:239],a[52:55]
ds_read_b64_tr_b16 a[130:131],v22 offset:3072
v_mfma_f32_16x16x32_bf16 a[52:55],a[168:171],a[232:235],a[52:55]
ds_read_b64_tr_b16 a[132:133],v22 offset:17408
v_mfma_f32_16x16x32_bf16 a[84:87],a[168:171],a[240:243],a[84:87]
ds_read_b64_tr_b16 a[134:135],v22 offset:19456
v_mfma_f32_16x16x32_bf16 a[84:87],a[172:175],a[244:247],a[84:87]
ds_read_b64_tr_b16 a[136:137],v22 offset:1032
v_mfma_f32_16x16x32_bf16 a[116:119],a[172:175],a[252:255],a[116:119]
ds_read_b64_tr_b16 a[138:139],v22 offset:3080
v_mfma_f32_16x16x32_bf16 a[116:119],a[168:171],a[248:251],a[116:119]
ds_read_b64_tr_b16 a[140:141],v22 offset:17416
v_mfma_f32_16x16x32_bf16 a[24:27],a[176:179],a[224:227],a[24:27]
ds_read_b64_tr_b16 a[142:143],v22 offset:19464
v_mfma_f32_16x16x32_bf16 a[24:27],a[180:183],a[228:231],a[24:27]
ds_read_b64_tr_b16 a[144:145],v22 offset:1280
v_mfma_f32_16x16x32_bf16 a[56:59],a[180:183],a[236:239],a[56:59]
ds_read_b64_tr_b16 a[146:147],v22 offset:3328
v_mfma_f32_16x16x32_bf16 a[56:59],a[176:179],a[232:235],a[56:59]
ds_read_b64_tr_b16 a[148:149],v22 offset:17664
v_mfma_f32_16x16x32_bf16 a[88:91],a[176:179],a[240:243],a[88:91]
ds_read_b64_tr_b16 a[150:151],v22 offset:19712
v_mfma_f32_16x16x32_bf16 a[88:91],a[180:183],a[244:247],a[88:91]
ds_read_b64_tr_b16 a[152:153],v22 offset:1288
v_mfma_f32_16x16x32_bf16 a[120:123],a[180:183],a[252:255],a[120:123]
ds_read_b64_tr_b16 a[154:155],v22 offset:3336
v_mfma_f32_16x16x32_bf16 a[120:123],a[176:179],a[248:251],a[120:123]
ds_read_b64_tr_b16 a[156:157],v22 offset:17672
v_mfma_f32_16x16x32_bf16 a[28:31],a[184:187],a[224:227],a[28:31]
ds_read_b64_tr_b16 a[158:159],v22 offset:19720
v_mfma_f32_16x16x32_bf16 a[28:31],a[188:191],a[228:231],a[28:31]
v_mfma_f32_16x16x32_bf16 a[60:63],a[188:191],a[236:239],a[60:63]
v_mfma_f32_16x16x32_bf16 a[60:63],a[184:187],a[232:235],a[60:63]
v_mfma_f32_16x16x32_bf16 a[92:95],a[184:187],a[240:243],a[92:95]
v_mfma_f32_16x16x32_bf16 a[92:95],a[188:191],a[244:247],a[92:95]
v_mfma_f32_16x16x32_bf16 a[124:127],a[188:191],a[252:255],a[124:127]
s_cmp_lg_u32 s53,0
v_mfma_f32_16x16x32_bf16 a[124:127],a[184:187],a[248:251],a[124:127]
s_cbranch_scc1 L_qp_%=
L_qpx_%=:
L_tailseg_%=:
s_waitcnt lgkmcnt(1)
v_mfma_f32_16x16x32_bf16 v[128:131],a[128:131],a[192:195],v[128:131]
s_add_u32 m0,98304,s51
ds_read_b64_tr_b16 a[160:161],v22 offset:1536
v_xor_b32 v20,131072,v20
v_mfma_f32_16x16x32_bf16 v[128:131],a[132:135],a[196:199],v[128:131]
s_addk_i32 s54,16384
buffer_load_dwordx4 v4,s[20:23],s47 offen nt lds
v_mfma_f32_16x16x32_bf16 v[136:139],a[132:135],a[204:207],v[136:139]
s_and_b32 s46,s54,0xfffc0000
ds_read_b64_tr_b16 a[162:163],v22 offset:3584
v_xor_b32 v21,131072,v21
v_mfma_f32_16x16x32_bf16 v[136:139],a[128:131],a[200:203],v[136:139]
s_addk_i32 s54,16384
ds_read_b64_tr_b16 a[164:165],v22 offset:17920
v_and_b32 v18,48,v6
v_mfma_f32_16x16x32_bf16 v[144:147],a[128:131],a[208:211],v[144:147]
s_mov_b32 s48,-98304
ds_read_b64_tr_b16 a[166:167],v22 offset:19968
v_lshrrev_b32 v19,4,v6
v_mfma_f32_16x16x32_bf16 v[144:147],a[132:135],a[212:215],v[144:147]
s_movk_i32 s60,141
ds_read_b64_tr_b16 a[168:169],v22 offset:1544
v_lshlrev_b32 v18,2,v18
v_mfma_f32_16x16x32_bf16 v[152:155],a[132:135],a[220:223],v[152:155]
s_mul_i32 s83,s41,5
ds_read_b64_tr_b16 a[170:171],v22 offset:3592
v_and_b32 v19,48,v19
v_mfma_f32_16x16x32_bf16 v[152:155],a[128:131],a[216:219],v[152:155]
s_add_u32 s83,s83,s42
ds_read_b64_tr_b16 a[172:173],v22 offset:17928
v_add_u32 v18,v18,v19
v_mfma_f32_16x16x32_bf16 v[132:135],a[136:139],a[192:195],v[132:135]
s_add_u32 s83,s83,15
ds_read_b64_tr_b16 a[174:175],v22 offset:19976
v_add_u32 v18,98304,v18
v_mfma_f32_16x16x32_bf16 v[132:135],a[140:143],a[196:199],v[132:135]
s_cmp_gt_i32 s79,0
s_cselect_b32 s86,8192,0
ds_read_b64_tr_b16 a[176:177],v22 offset:1792
v_mfma_f32_16x16x32_bf16 v[140:143],a[140:143],a[204:207],v[140:143]
s_lshr_b32 s79,%1,3
v_readfirstlane_b32 s68,v13
s_nop 1
s_add_u32 s68,s68,1
v_mfma_f32_16x16x32_bf16 v[140:143],a[136:139],a[200:203],v[140:143]
s_mul_i32 s79,s79,4
s_lshr_b32 s79,s79,5
ds_read_b64_tr_b16 a[178:179],v22 offset:3840
v_mfma_f32_16x16x32_bf16 v[148:151],a[136:139],a[208:211],v[148:151]
s_sub_u32 s60,s60,s79
s_lshl_b32 s94,s60,2
ds_read_b64_tr_b16 a[180:181],v22 offset:18176
v_mfma_f32_16x16x32_bf16 v[148:151],a[140:143],a[212:215],v[148:151]
s_sub_u32 s94,s94,3
buffer_load_dwordx4 v4,s[20:23],s47 offen offset:1024 nt lds
v_mfma_f32_16x16x32_bf16 v[156:159],a[140:143],a[220:223],v[156:159]
s_lshl_b32 s43,s83,14
s_lshl_b32 s83,s83,12
ds_read_b64_tr_b16 a[182:183],v22 offset:20224
v_mfma_f32_16x16x32_bf16 v[156:159],a[136:139],a[216:219],v[156:159]
s_lshl_b32 s44,s40,4
s_and_b32 s43,s43,0x20000
ds_read_b64_tr_b16 a[184:185],v22 offset:1800
v_mfma_f32_16x16x32_bf16 v[160:163],a[144:147],a[192:195],v[160:163]
s_and_b32 s83,s83,0x7000
s_xor_b32 s69,s68,0x9e3779b9
ds_read_b64_tr_b16 a[186:187],v22 offset:3848
v_mfma_f32_16x16x32_bf16 v[160:163],a[148:151],a[196:199],v[160:163]
s_lshl_b32 s80,s40,10
s_and_b32 s44,s44,32
ds_read_b64_tr_b16 a[188:189],v22 offset:18184
v_mfma_f32_16x16x32_bf16 v[168:171],a[148:151],a[204:207],v[168:171]
s_or_b32 s83,s83,s43
s_and_b32 s69,s69,-2
ds_read_b64_tr_b16 a[190:191],v22 offset:20232
v_mfma_f32_16x16x32_bf16 v[168:171],a[144:147],a[200:203],v[168:171]
s_add_u32 s55,s55,s86
s_add_u32 s93,s92,L_att0x_%=-L_pc_%=
ds_read_b64_tr_b16 a[128:129],v22
v_mfma_f32_16x16x32_bf16 v[176:179],a[144:147],a[208:211],v[176:179]
s_mov_b32 s50,32768
s_mov_b32 s49,98304
ds_read_b64_tr_b16 a[130:131],v22 offset:2048
v_mfma_f32_16x16x32_bf16 v[176:179],a[148:151],a[212:215],v[176:179]
s_mov_b32 s45,0x1249
ds_read_b64_tr_b16 a[132:133],v22 offset:16384
v_xor_b32 v25,s44,v4
v_mfma_f32_16x16x32_bf16 v[184:187],a[148:151],a[220:223],v[184:187]
s_add_u32 s80,s80,131072
s_or_b32 s83,s83,0x40000
ds_read_b64_tr_b16 a[134:135],v22 offset:18432
v_mfma_f32_16x16x32_bf16 v[184:187],a[144:147],a[216:219],v[184:187]
ds_read_b64_tr_b16 a[144:145],v22 offset:8
v_mfma_f32_16x16x32_bf16 v[164:167],a[152:155],a[192:195],v[164:167]
s_waitcnt lgkmcnt(7)
ds_read_b64_tr_b16 a[146:147],v22 offset:2056
v_mfma_f32_16x16x32_bf16 v[164:167],a[156:159],a[196:199],v[164:167]
buffer_load_dwordx4 v5,s[20:23],s47 offen offset:2048 nt lds
v_mfma_f32_16x16x32_bf16 v[172:175],a[156:159],a[204:207],v[172:175]
s_waitcnt vmcnt(5)
ds_read_b128 a[224:227],v20
v_mfma_f32_16x16x32_bf16 v[172:175],a[152:155],a[200:203],v[172:175]
ds_read_b64_tr_b16 a[148:149],v22 offset:16392
v_mfma_f32_16x16x32_bf16 v[180:183],a[152:155],a[208:211],v[180:183]
ds_read_b64_tr_b16 a[150:151],v22 offset:18440
v_mfma_f32_16x16x32_bf16 v[180:183],a[156:159],a[212:215],v[180:183]
v_mfma_f32_16x16x32_bf16 v[188:191],a[156:159],a[220:223],v[188:191]
ds_read_b128 a[228:231],v21
v_mfma_f32_16x16x32_bf16 v[188:191],a[152:155],a[216:219],v[188:191]
v_mfma_f32_16x16x32_bf16 v[192:195],a[160:163],a[192:195],v[192:195]
ds_read_b128 a[232:235],v20 offset:2048
v_mfma_f32_16x16x32_bf16 v[192:195],a[164:167],a[196:199],v[192:195]
v_mfma_f32_16x16x32_bf16 v[200:203],a[164:167],a[204:207],v[200:203]
ds_read_b128 a[236:239],v21 offset:2048
v_mfma_f32_16x16x32_bf16 v[200:203],a[160:163],a[200:203],v[200:203]
v_mfma_f32_16x16x32_bf16 v[208:211],a[160:163],a[208:211],v[208:211]
ds_read_b128 a[240:243],v20 offset:4096
v_mfma_f32_16x16x32_bf16 v[208:211],a[164:167],a[212:215],v[208:211]
buffer_load_dwordx4 v5,s[20:23],s47 offen offset:3072 nt lds
v_mfma_f32_16x16x32_bf16 v[216:219],a[164:167],a[220:223],v[216:219]
ds_read_b128 a[244:247],v21 offset:4096
v_mfma_f32_16x16x32_bf16 v[216:219],a[160:163],a[216:219],v[216:219]
ds_read_b64_tr_b16 a[160:161],v22 offset:256
v_mfma_f32_16x16x32_bf16 v[196:199],a[168:171],a[192:195],v[196:199]
ds_read_b128 a[248:251],v20 offset:6144
ds_read_b128 a[252:255],v21 offset:6144
v_mfma_f32_16x16x32_bf16 v[196:199],a[172:175],a[196:199],v[196:199]
v_mfma_f32_16x16x32_bf16 v[204:207],a[172:175],a[204:207],v[204:207]
)A"
R"A(v_mfma_f32_16x16x32_bf16 v[204:207],a[168:171],a[200:203],v[204:207]
v_mfma_f32_16x16x32_bf16 v[212:215],a[168:171],a[208:211],v[212:215]
ds_read_b64_tr_b16 a[162:163],v22 offset:2304
v_mfma_f32_16x16x32_bf16 v[212:215],a[172:175],a[212:215],v[212:215]
ds_read_b64_tr_b16 a[164:165],v22 offset:16640
v_mfma_f32_16x16x32_bf16 v[220:223],a[172:175],a[220:223],v[220:223]
s_waitcnt vmcnt(5) lgkmcnt(2)
s_addk_i32 m0,16384
ds_read_b64_tr_b16 a[166:167],v22 offset:18688
v_mfma_f32_16x16x32_bf16 v[220:223],a[168:171],a[216:219],v[220:223]
s_barrier
ds_read_b64_tr_b16 a[136:137],v22 offset:32768
v_mfma_f32_16x16x32_bf16 v[224:227],a[176:179],a[192:195],v[224:227]
buffer_load_dwordx4 v4,s[24:27],s47 offen nt lds
v_mfma_f32_16x16x32_bf16 v[224:227],a[180:183],a[196:199],v[224:227]
ds_read_b64_tr_b16 a[138:139],v22 offset:34816
v_mfma_f32_16x16x32_bf16 v[232:235],a[180:183],a[204:207],v[232:235]
ds_read_b64_tr_b16 a[140:141],v22 offset:49152
v_mfma_f32_16x16x32_bf16 v[232:235],a[176:179],a[200:203],v[232:235]
ds_read_b64_tr_b16 a[142:143],v22 offset:51200
v_mfma_f32_16x16x32_bf16 v[240:243],a[176:179],a[208:211],v[240:243]
ds_read_b64_tr_b16 a[152:153],v22 offset:32776
v_mfma_f32_16x16x32_bf16 v[240:243],a[180:183],a[212:215],v[240:243]
ds_read_b64_tr_b16 a[154:155],v22 offset:34824
v_mfma_f32_16x16x32_bf16 v[248:251],a[180:183],a[220:223],v[248:251]
ds_read_b64_tr_b16 a[156:157],v22 offset:49160
v_mfma_f32_16x16x32_bf16 v[248:251],a[176:179],a[216:219],v[248:251]
ds_read_b64_tr_b16 a[158:159],v22 offset:51208
v_mfma_f32_16x16x32_bf16 v[228:231],a[184:187],a[192:195],v[228:231]
ds_read_b64_tr_b16 a[168:169],v22 offset:33024
v_mfma_f32_16x16x32_bf16 v[228:231],a[188:191],a[196:199],v[228:231]
ds_read_b64_tr_b16 a[170:171],v22 offset:35072
v_mfma_f32_16x16x32_bf16 v[236:239],a[188:191],a[204:207],v[236:239]
ds_read_b64_tr_b16 a[172:173],v22 offset:49408
v_mfma_f32_16x16x32_bf16 v[236:239],a[184:187],a[200:203],v[236:239]
ds_read_b64_tr_b16 a[174:175],v22 offset:51456
v_mfma_f32_16x16x32_bf16 v[244:247],a[184:187],a[208:211],v[244:247]
buffer_load_dwordx4 v5,s[24:27],s47 offen offset:1024 nt lds
v_mfma_f32_16x16x32_bf16 v[244:247],a[188:191],a[212:215],v[244:247]
ds_read_b64_tr_b16 a[176:177],v22 offset:264
v_mfma_f32_16x16x32_bf16 v[252:255],a[188:191],a[220:223],v[252:255]
ds_read_b64_tr_b16 a[178:179],v22 offset:2312
v_mfma_f32_16x16x32_bf16 v[252:255],a[184:187],a[216:219],v[252:255]
ds_read_b64_tr_b16 a[180:181],v22 offset:16648
v_mfma_f32_16x16x32_bf16 a[0:3],a[128:131],a[192:195],a[0:3]
ds_read_b64_tr_b16 a[182:183],v22 offset:18696
v_mfma_f32_16x16x32_bf16 a[0:3],a[132:135],a[196:199],a[0:3]
s_waitcnt lgkmcnt(6)
ds_read_b64_tr_b16 a[184:185],v22 offset:33032
v_mfma_f32_16x16x32_bf16 a[0:3],a[136:139],a[224:227],a[0:3]
ds_read_b64_tr_b16 a[186:187],v22 offset:35080
v_mfma_f32_16x16x32_bf16 a[0:3],a[140:143],a[228:231],a[0:3]
ds_read_b64_tr_b16 a[188:189],v22 offset:49416
v_mfma_f32_16x16x32_bf16 a[32:35],a[140:143],a[236:239],a[32:35]
ds_read_b64_tr_b16 a[190:191],v22 offset:51464
v_mfma_f32_16x16x32_bf16 a[32:35],a[136:139],a[232:235],a[32:35]
v_mfma_f32_16x16x32_bf16 a[32:35],a[132:135],a[204:207],a[32:35]
v_mfma_f32_16x16x32_bf16 a[32:35],a[128:131],a[200:203],a[32:35]
v_mfma_f32_16x16x32_bf16 a[64:67],a[128:131],a[208:211],a[64:67]
buffer_load_dwordx4 v4,s[24:27],s47 offen offset:2048 nt lds
v_mfma_f32_16x16x32_bf16 a[64:67],a[132:135],a[212:215],a[64:67]
v_accvgpr_read_b32 v64,a0
v_accvgpr_read_b32 v65,a1
v_mfma_f32_16x16x32_bf16 a[64:67],a[136:139],a[240:243],a[64:67]
v_mul_f32 v64,s61,v64
v_mul_f32 v65,s61,v65
v_mfma_f32_16x16x32_bf16 a[64:67],a[140:143],a[244:247],a[64:67]
v_accvgpr_read_b32 v66,a2
v_accvgpr_read_b32 v67,a3
v_mfma_f32_16x16x32_bf16 a[96:99],a[140:143],a[252:255],a[96:99]
ds_read_b64_tr_b16 a[140:141],v22 offset:49664
v_cvt_pk_bf16_f32 v64,v64,v65
v_mfma_f32_16x16x32_bf16 a[96:99],a[136:139],a[248:251],a[96:99]
ds_read_b64_tr_b16 a[136:137],v22 offset:33280
v_accvgpr_read_b32 v80,a32
v_mfma_f32_16x16x32_bf16 a[96:99],a[132:135],a[220:223],a[96:99]
ds_read_b64_tr_b16 a[132:133],v22 offset:16896
v_accvgpr_read_b32 v81,a33
v_mfma_f32_16x16x32_bf16 a[96:99],a[128:131],a[216:219],a[96:99]
ds_read_b64_tr_b16 a[128:129],v22 offset:512
v_mul_f32 v80,s61,v80
v_mfma_f32_16x16x32_bf16 a[4:7],a[144:147],a[192:195],a[4:7]
ds_read_b64_tr_b16 a[130:131],v22 offset:2560
v_mul_f32 v81,s61,v81
v_mfma_f32_16x16x32_bf16 a[4:7],a[148:151],a[196:199],a[4:7]
ds_read_b64_tr_b16 a[134:135],v22 offset:18944
v_accvgpr_read_b32 v82,a34
v_mfma_f32_16x16x32_bf16 a[4:7],a[152:155],a[224:227],a[4:7]
ds_read_b64_tr_b16 a[138:139],v22 offset:35328
v_accvgpr_read_b32 v83,a35
v_mfma_f32_16x16x32_bf16 a[4:7],a[156:159],a[228:231],a[4:7]
ds_read_b64_tr_b16 a[142:143],v22 offset:51712
v_mul_f32 v66,s61,v66
v_mfma_f32_16x16x32_bf16 a[36:39],a[156:159],a[236:239],a[36:39]
buffer_load_dwordx4 v5,s[24:27],s47 offen offset:3072 nt lds
v_mfma_f32_16x16x32_bf16 a[36:39],a[152:155],a[232:235],a[36:39]
s_or_b32 s47,s47,0x18000
s_addk_i32 s47,4096
s_and_b32 s47,s47,0x27000
v_mfma_f32_16x16x32_bf16 a[36:39],a[148:151],a[204:207],a[36:39]
s_or_b32 s47,s47,s46
s_and_b32 s46,s54,0xfffc0000
v_mul_f32 v67,s61,v67
v_mfma_f32_16x16x32_bf16 a[36:39],a[144:147],a[200:203],a[36:39]
s_addk_i32 s54,16384
v_cvt_pk_bf16_f32 v80,v80,v81
v_mul_f32 v82,s61,v82
v_mfma_f32_16x16x32_bf16 a[68:71],a[144:147],a[208:211],a[68:71]
v_mul_f32 v83,s61,v83
v_cvt_pk_bf16_f32 v65,v66,v67
v_mfma_f32_16x16x32_bf16 a[68:71],a[148:151],a[212:215],a[68:71]
v_cvt_pk_bf16_f32 v81,v82,v83
v_accvgpr_read_b32 v112,a96
v_mfma_f32_16x16x32_bf16 a[68:71],a[152:155],a[240:243],a[68:71]
v_accvgpr_read_b32 v113,a97
v_accvgpr_read_b32 v66,a4
v_mfma_f32_16x16x32_bf16 a[68:71],a[156:159],a[244:247],a[68:71]
v_accvgpr_read_b32 v67,a5
v_mul_f32 v112,s61,v112
v_mfma_f32_16x16x32_bf16 a[100:103],a[156:159],a[252:255],a[100:103]
ds_read_b64_tr_b16 a[156:157],v22 offset:49672
v_mul_f32 v113,s61,v113
v_mfma_f32_16x16x32_bf16 a[100:103],a[152:155],a[248:251],a[100:103]
ds_read_b64_tr_b16 a[152:153],v22 offset:33288
v_accvgpr_read_b32 v82,a36
v_mfma_f32_16x16x32_bf16 a[100:103],a[148:151],a[220:223],a[100:103]
ds_read_b64_tr_b16 a[148:149],v22 offset:16904
v_accvgpr_read_b32 v83,a37
v_mfma_f32_16x16x32_bf16 a[100:103],a[144:147],a[216:219],a[100:103]
ds_read_b64_tr_b16 a[144:145],v22 offset:520
v_accvgpr_read_b32 v114,a98
v_mfma_f32_16x16x32_bf16 a[8:11],a[160:163],a[192:195],a[8:11]
s_add_u32 m0,0,s51
ds_read_b64_tr_b16 a[146:147],v22 offset:2568
v_accvgpr_read_b32 v115,a99
v_mfma_f32_16x16x32_bf16 a[8:11],a[164:167],a[196:199],a[8:11]
ds_read_b64_tr_b16 a[150:151],v22 offset:18952
v_accvgpr_read_b32 v12,a6
v_mfma_f32_16x16x32_bf16 a[8:11],a[168:171],a[224:227],a[8:11]
s_waitcnt lgkmcnt(6)
ds_read_b64_tr_b16 a[154:155],v22 offset:35336
v_accvgpr_read_b32 v13,a7
v_mfma_f32_16x16x32_bf16 a[8:11],a[172:175],a[228:231],a[8:11]
buffer_load_dwordx4 v4,s[20:23],s47 offen nt lds
v_mfma_f32_16x16x32_bf16 a[40:43],a[172:175],a[236:239],a[40:43]
ds_read_b64_tr_b16 a[158:159],v22 offset:51720
v_mul_f32 v66,s61,v66
v_mfma_f32_16x16x32_bf16 a[40:43],a[168:171],a[232:235],a[40:43]
v_mul_f32 v67,s61,v67
v_accvgpr_read_b32 v14,a38
v_mfma_f32_16x16x32_bf16 a[40:43],a[164:167],a[204:207],a[40:43]
v_accvgpr_read_b32 v15,a39
v_mul_f32 v82,s61,v82
v_mfma_f32_16x16x32_bf16 a[40:43],a[160:163],a[200:203],a[40:43]
v_mul_f32 v83,s61,v83
v_cvt_pk_bf16_f32 v112,v112,v113
v_mfma_f32_16x16x32_bf16 a[72:75],a[160:163],a[208:211],a[72:75]
v_mul_f32 v114,s61,v114
v_mul_f32 v115,s61,v115
v_mfma_f32_16x16x32_bf16 a[72:75],a[164:167],a[212:215],a[72:75]
v_accvgpr_read_b32 v68,a8
v_accvgpr_read_b32 v69,a9
v_mfma_f32_16x16x32_bf16 a[72:75],a[168:171],a[240:243],a[72:75]
v_mul_f32 v68,s61,v68
v_mul_f32 v69,s61,v69
v_mfma_f32_16x16x32_bf16 a[72:75],a[172:175],a[244:247],a[72:75]
v_accvgpr_read_b32 v70,a10
v_accvgpr_read_b32 v71,a11
v_mfma_f32_16x16x32_bf16 a[104:107],a[172:175],a[252:255],a[104:107]
ds_read_b64_tr_b16 a[172:173],v22 offset:49920
v_cvt_pk_bf16_f32 v68,v68,v69
v_mfma_f32_16x16x32_bf16 a[104:107],a[168:171],a[248:251],a[104:107]
ds_read_b64_tr_b16 a[168:169],v22 offset:33536
v_mul_f32 v70,s61,v70
v_mfma_f32_16x16x32_bf16 a[104:107],a[164:167],a[220:223],a[104:107]
ds_read_b64_tr_b16 a[164:165],v22 offset:17152
v_mul_f32 v71,s61,v71
v_mfma_f32_16x16x32_bf16 a[104:107],a[160:163],a[216:219],a[104:107]
ds_read_b64_tr_b16 a[160:161],v22 offset:768
v_mul_f32 v12,s61,v12
v_mfma_f32_16x16x32_bf16 a[12:15],a[176:179],a[192:195],a[12:15]
ds_read_b64_tr_b16 a[162:163],v22 offset:2816
v_mul_f32 v13,s61,v13
v_mfma_f32_16x16x32_bf16 a[12:15],a[180:183],a[196:199],a[12:15]
buffer_load_dwordx4 v4,s[20:23],s47 offen offset:1024 nt lds
v_mfma_f32_16x16x32_bf16 a[12:15],a[184:187],a[224:227],a[12:15]
ds_read_b64_tr_b16 a[166:167],v22 offset:19200
v_cvt_pk_bf16_f32 v66,v66,v67
v_mfma_f32_16x16x32_bf16 a[12:15],a[188:191],a[228:231],a[12:15]
ds_read_b64_tr_b16 a[170:171],v22 offset:35584
v_mul_f32 v14,s61,v14
v_mfma_f32_16x16x32_bf16 a[44:47],a[188:191],a[236:239],a[44:47]
ds_read_b64_tr_b16 a[174:175],v22 offset:51968
v_mul_f32 v15,s61,v15
v_mfma_f32_16x16x32_bf16 a[44:47],a[184:187],a[232:235],a[44:47]
v_cvt_pk_bf16_f32 v82,v82,v83
v_cvt_pk_bf16_f32 v113,v114,v115
v_mfma_f32_16x16x32_bf16 a[44:47],a[180:183],a[204:207],a[44:47]
v_cvt_pk_bf16_f32 v69,v70,v71
v_accvgpr_read_b32 v100,a72
v_mfma_f32_16x16x32_bf16 a[44:47],a[176:179],a[200:203],a[44:47]
v_accvgpr_read_b32 v101,a73
v_accvgpr_read_b32 v116,a104
v_mfma_f32_16x16x32_bf16 a[76:79],a[176:179],a[208:211],a[76:79]
v_accvgpr_read_b32 v117,a105
v_cvt_pk_bf16_f32 v67,v12,v13
v_mfma_f32_16x16x32_bf16 a[76:79],a[180:183],a[212:215],a[76:79]
v_cvt_pk_bf16_f32 v83,v14,v15
v_accvgpr_read_b32 v114,a100
v_mfma_f32_16x16x32_bf16 a[76:79],a[184:187],a[240:243],a[76:79]
v_accvgpr_read_b32 v115,a101
v_accvgpr_read_b32 v70,a12
v_mfma_f32_16x16x32_bf16 a[76:79],a[188:191],a[244:247],a[76:79]
v_accvgpr_read_b32 v71,a13
v_mul_f32 v100,s61,v100
v_mfma_f32_16x16x32_bf16 a[108:111],a[188:191],a[252:255],a[108:111]
ds_read_b64_tr_b16 a[188:189],v22 offset:49928
v_mul_f32 v101,s61,v101
v_mfma_f32_16x16x32_bf16 a[108:111],a[184:187],a[248:251],a[108:111]
ds_read_b64_tr_b16 a[184:185],v22 offset:33544
v_accvgpr_read_b32 v102,a74
v_mfma_f32_16x16x32_bf16 a[108:111],a[180:183],a[220:223],a[108:111]
ds_read_b64_tr_b16 a[180:181],v22 offset:17160
v_accvgpr_read_b32 v103,a75
v_mfma_f32_16x16x32_bf16 a[108:111],a[176:179],a[216:219],a[108:111]
buffer_load_dwordx4 v5,s[20:23],s47 offen offset:2048 nt lds
v_mfma_f32_16x16x32_bf16 a[16:19],a[128:131],a[192:195],a[16:19]
ds_read_b64_tr_b16 a[176:177],v22 offset:776
v_mul_f32 v116,s61,v116
v_mfma_f32_16x16x32_bf16 a[16:19],a[132:135],a[196:199],a[16:19]
ds_read_b64_tr_b16 a[178:179],v22 offset:2824
v_mul_f32 v117,s61,v117
v_mfma_f32_16x16x32_bf16 a[16:19],a[136:139],a[224:227],a[16:19]
ds_read_b64_tr_b16 a[182:183],v22 offset:19208
v_accvgpr_read_b32 v118,a106
v_mfma_f32_16x16x32_bf16 a[16:19],a[140:143],a[228:231],a[16:19]
ds_read_b64_tr_b16 a[186:187],v22 offset:35592
v_accvgpr_read_b32 v119,a107
v_mfma_f32_16x16x32_bf16 a[48:51],a[140:143],a[236:239],a[48:51]
ds_read_b64_tr_b16 a[190:191],v22 offset:51976
v_accvgpr_read_b32 v12,a102
v_mfma_f32_16x16x32_bf16 a[48:51],a[136:139],a[232:235],a[48:51]
v_accvgpr_read_b32 v13,a103
v_mul_f32 v114,s61,v114
v_mfma_f32_16x16x32_bf16 a[48:51],a[132:135],a[204:207],a[48:51]
v_mul_f32 v115,s61,v115
v_accvgpr_read_b32 v14,a14
v_mfma_f32_16x16x32_bf16 a[48:51],a[128:131],a[200:203],a[48:51]
v_accvgpr_read_b32 v15,a15
v_mul_f32 v70,s61,v70
v_mfma_f32_16x16x32_bf16 a[80:83],a[128:131],a[208:211],a[80:83]
v_mul_f32 v71,s61,v71
v_cvt_pk_bf16_f32 v100,v100,v101
v_mfma_f32_16x16x32_bf16 a[80:83],a[132:135],a[212:215],a[80:83]
v_mul_f32 v102,s61,v102
v_mul_f32 v103,s61,v103
v_mfma_f32_16x16x32_bf16 a[80:83],a[136:139],a[240:243],a[80:83]
v_cvt_pk_bf16_f32 v116,v116,v117
v_mul_f32 v118,s61,v118
v_mfma_f32_16x16x32_bf16 a[80:83],a[140:143],a[244:247],a[80:83]
v_mul_f32 v119,s61,v119
v_mul_f32 v12,s61,v12
v_mfma_f32_16x16x32_bf16 a[112:115],a[140:143],a[252:255],a[112:115]
)A"
R"A(ds_read_b64_tr_b16 a[140:141],v22 offset:50184
v_mul_f32 v13,s61,v13
v_mfma_f32_16x16x32_bf16 a[112:115],a[136:139],a[248:251],a[112:115]
buffer_load_dwordx4 v5,s[20:23],s47 offen offset:3072 nt lds
v_mfma_f32_16x16x32_bf16 a[112:115],a[132:135],a[220:223],a[112:115]
ds_read_b64_tr_b16 a[132:133],v22 offset:50176
v_cvt_pk_bf16_f32 v114,v114,v115
v_mfma_f32_16x16x32_bf16 a[112:115],a[128:131],a[216:219],a[112:115]
s_waitcnt lgkmcnt(2)
ds_read_b64_tr_b16 a[128:129],v22 offset:33792
v_mul_f32 v14,s61,v14
v_mfma_f32_16x16x32_bf16 a[20:23],a[144:147],a[192:195],a[20:23]
ds_read_b64_tr_b16 a[130:131],v22 offset:35840
v_mul_f32 v15,s61,v15
v_mfma_f32_16x16x32_bf16 a[20:23],a[148:151],a[196:199],a[20:23]
ds_read_b64_tr_b16 a[134:135],v22 offset:52224
v_cvt_pk_bf16_f32 v70,v70,v71
v_mfma_f32_16x16x32_bf16 a[20:23],a[152:155],a[224:227],a[20:23]
ds_read_b64_tr_b16 a[136:137],v22 offset:33800
v_cvt_pk_bf16_f32 v101,v102,v103
v_mfma_f32_16x16x32_bf16 a[20:23],a[156:159],a[228:231],a[20:23]
ds_read_b64_tr_b16 a[138:139],v22 offset:35848
v_cvt_pk_bf16_f32 v117,v118,v119
v_mfma_f32_16x16x32_bf16 a[52:55],a[156:159],a[236:239],a[52:55]
ds_read_b64_tr_b16 a[142:143],v22 offset:52232
v_cvt_pk_bf16_f32 v115,v12,v13
v_mfma_f32_16x16x32_bf16 a[52:55],a[152:155],a[232:235],a[52:55]
v_cvt_pk_bf16_f32 v71,v14,v15
v_accvgpr_read_b32 v102,a76
v_mfma_f32_16x16x32_bf16 a[52:55],a[148:151],a[204:207],a[52:55]
v_accvgpr_read_b32 v103,a77
v_accvgpr_read_b32 v118,a108
v_mfma_f32_16x16x32_bf16 a[52:55],a[144:147],a[200:203],a[52:55]
v_accvgpr_read_b32 v119,a109
v_accvgpr_read_b32 v12,a78
v_mfma_f32_16x16x32_bf16 a[84:87],a[144:147],a[208:211],a[84:87]
s_addk_i32 m0,16384
v_accvgpr_read_b32 v13,a79
v_mul_f32 v102,s61,v102
v_mfma_f32_16x16x32_bf16 a[84:87],a[148:151],a[212:215],a[84:87]
v_mul_f32 v103,s61,v103
v_accvgpr_read_b32 v14,a110
v_mfma_f32_16x16x32_bf16 a[84:87],a[152:155],a[240:243],a[84:87]
v_accvgpr_read_b32 v15,a111
v_mul_f32 v118,s61,v118
v_mfma_f32_16x16x32_bf16 a[84:87],a[156:159],a[244:247],a[84:87]
buffer_load_dwordx4 v4,s[24:27],s47 offen nt lds
v_mfma_f32_16x16x32_bf16 a[116:119],a[156:159],a[252:255],a[116:119]
ds_read_b64_tr_b16 a[156:157],v22 offset:50440
v_mul_f32 v119,s61,v119
v_mfma_f32_16x16x32_bf16 a[116:119],a[152:155],a[248:251],a[116:119]
ds_read_b64_tr_b16 a[152:153],v22 offset:34056
v_mul_f32 v12,s61,v12
v_mfma_f32_16x16x32_bf16 a[116:119],a[148:151],a[220:223],a[116:119]
ds_read_b64_tr_b16 a[148:149],v22 offset:50432
v_mul_f32 v13,s61,v13
v_mfma_f32_16x16x32_bf16 a[116:119],a[144:147],a[216:219],a[116:119]
ds_read_b64_tr_b16 a[144:145],v22 offset:34048
v_cvt_pk_bf16_f32 v102,v102,v103
v_mfma_f32_16x16x32_bf16 a[24:27],a[160:163],a[192:195],a[24:27]
ds_read_b64_tr_b16 a[146:147],v22 offset:36096
v_mul_f32 v14,s61,v14
v_mfma_f32_16x16x32_bf16 a[24:27],a[164:167],a[196:199],a[24:27]
ds_read_b64_tr_b16 a[150:151],v22 offset:52480
v_mul_f32 v15,s61,v15
v_mfma_f32_16x16x32_bf16 a[24:27],a[168:171],a[224:227],a[24:27]
ds_read_b64_tr_b16 a[154:155],v22 offset:36104
v_cvt_pk_bf16_f32 v118,v118,v119
v_mfma_f32_16x16x32_bf16 a[24:27],a[172:175],a[228:231],a[24:27]
ds_read_b64_tr_b16 a[158:159],v22 offset:52488
v_cvt_pk_bf16_f32 v103,v12,v13
v_mfma_f32_16x16x32_bf16 a[56:59],a[172:175],a[236:239],a[56:59]
v_cvt_pk_bf16_f32 v119,v14,v15
v_accvgpr_read_b32 v96,a64
v_mfma_f32_16x16x32_bf16 a[56:59],a[168:171],a[232:235],a[56:59]
.p2align 3
v_accvgpr_read_b32 v72,a16
v_accvgpr_read_b32 v73,a17
v_accvgpr_read_b32 v74,a18
v_accvgpr_read_b32 v75,a19
v_accvgpr_read_b32 v12,a20
v_accvgpr_read_b32 v13,a21
v_accvgpr_read_b32 v14,a22
v_accvgpr_read_b32 v15,a23
v_pk_mul_f32 v[72:73],s[60:61],v[72:73] op_sel:[1,0] op_sel_hi:[1,1]
v_pk_mul_f32 v[74:75],s[60:61],v[74:75] op_sel:[1,0] op_sel_hi:[1,1]
v_pk_mul_f32 v[12:13],s[60:61],v[12:13] op_sel:[1,0] op_sel_hi:[1,1]
v_pk_mul_f32 v[14:15],s[60:61],v[14:15] op_sel:[1,0] op_sel_hi:[1,1]
v_cvt_pk_bf16_f32 v72,v72,v73
v_cvt_pk_bf16_f32 v73,v74,v75
v_cvt_pk_bf16_f32 v74,v12,v13
v_cvt_pk_bf16_f32 v75,v14,v15
v_mfma_f32_16x16x32_bf16 a[56:59],a[164:167],a[204:207],a[56:59]
v_accvgpr_read_b32 v97,a65
v_mul_f32 v96,s61,v96
v_mfma_f32_16x16x32_bf16 a[56:59],a[160:163],a[200:203],a[56:59]
.p2align 3
v_accvgpr_read_b32 v88,a48
v_accvgpr_read_b32 v89,a49
v_accvgpr_read_b32 v90,a50
v_accvgpr_read_b32 v91,a51
v_accvgpr_read_b32 v12,a52
v_accvgpr_read_b32 v13,a53
v_accvgpr_read_b32 v14,a54
v_accvgpr_read_b32 v15,a55
v_pk_mul_f32 v[88:89],s[60:61],v[88:89] op_sel:[1,0] op_sel_hi:[1,1]
v_pk_mul_f32 v[90:91],s[60:61],v[90:91] op_sel:[1,0] op_sel_hi:[1,1]
v_pk_mul_f32 v[12:13],s[60:61],v[12:13] op_sel:[1,0] op_sel_hi:[1,1]
v_pk_mul_f32 v[14:15],s[60:61],v[14:15] op_sel:[1,0] op_sel_hi:[1,1]
v_cvt_pk_bf16_f32 v88,v88,v89
v_cvt_pk_bf16_f32 v89,v90,v91
v_cvt_pk_bf16_f32 v90,v12,v13
v_cvt_pk_bf16_f32 v91,v14,v15
v_mfma_f32_16x16x32_bf16 a[88:91],a[160:163],a[208:211],a[88:91]
v_mul_f32 v97,s61,v97
v_accvgpr_read_b32 v98,a66
v_mfma_f32_16x16x32_bf16 a[88:91],a[164:167],a[212:215],a[88:91]
buffer_load_dwordx4 v5,s[24:27],s47 offen offset:1024 nt lds
v_mfma_f32_16x16x32_bf16 a[88:91],a[168:171],a[240:243],a[88:91]
v_accvgpr_read_b32 v99,a67
v_cvt_pk_bf16_f32 v96,v96,v97
v_mfma_f32_16x16x32_bf16 a[88:91],a[172:175],a[244:247],a[88:91]
v_mul_f32 v98,s61,v98
v_mul_f32 v99,s61,v99
v_mfma_f32_16x16x32_bf16 a[120:123],a[172:175],a[252:255],a[120:123]
ds_read_b64_tr_b16 a[172:173],v22 offset:50696
v_cvt_pk_bf16_f32 v97,v98,v99
v_mfma_f32_16x16x32_bf16 a[120:123],a[168:171],a[248:251],a[120:123]
ds_read_b64_tr_b16 a[168:169],v22 offset:34312
v_accvgpr_read_b32 v84,a40
v_mfma_f32_16x16x32_bf16 a[120:123],a[164:167],a[220:223],a[120:123]
ds_read_b64_tr_b16 a[164:165],v22 offset:50688
v_accvgpr_read_b32 v85,a41
v_mfma_f32_16x16x32_bf16 a[120:123],a[160:163],a[216:219],a[120:123]
ds_read_b64_tr_b16 a[160:161],v22 offset:34304
v_accvgpr_read_b32 v98,a68
v_mfma_f32_16x16x32_bf16 a[28:31],a[176:179],a[192:195],a[28:31]
ds_read_b64_tr_b16 a[162:163],v22 offset:36352
v_accvgpr_read_b32 v99,a69
v_mfma_f32_16x16x32_bf16 a[28:31],a[180:183],a[196:199],a[28:31]
ds_read_b64_tr_b16 a[166:167],v22 offset:52736
v_mul_f32 v84,s61,v84
v_mfma_f32_16x16x32_bf16 a[28:31],a[184:187],a[224:227],a[28:31]
ds_read_b64_tr_b16 a[170:171],v22 offset:36360
v_mul_f32 v85,s61,v85
v_mfma_f32_16x16x32_bf16 a[28:31],a[188:191],a[228:231],a[28:31]
ds_read_b64_tr_b16 a[174:175],v22 offset:52744
v_accvgpr_read_b32 v86,a42
v_mfma_f32_16x16x32_bf16 a[60:63],a[188:191],a[236:239],a[60:63]
.p2align 3
v_accvgpr_read_b32 v104,a80
v_accvgpr_read_b32 v105,a81
v_accvgpr_read_b32 v106,a82
v_accvgpr_read_b32 v107,a83
v_accvgpr_read_b32 v12,a84
v_accvgpr_read_b32 v13,a85
v_accvgpr_read_b32 v14,a86
v_accvgpr_read_b32 v15,a87
v_pk_mul_f32 v[104:105],s[60:61],v[104:105] op_sel:[1,0] op_sel_hi:[1,1]
v_pk_mul_f32 v[106:107],s[60:61],v[106:107] op_sel:[1,0] op_sel_hi:[1,1]
v_pk_mul_f32 v[12:13],s[60:61],v[12:13] op_sel:[1,0] op_sel_hi:[1,1]
v_pk_mul_f32 v[14:15],s[60:61],v[14:15] op_sel:[1,0] op_sel_hi:[1,1]
v_cvt_pk_bf16_f32 v104,v104,v105
v_cvt_pk_bf16_f32 v105,v106,v107
v_cvt_pk_bf16_f32 v106,v12,v13
v_cvt_pk_bf16_f32 v107,v14,v15
v_mfma_f32_16x16x32_bf16 a[60:63],a[184:187],a[232:235],a[60:63]
v_accvgpr_read_b32 v87,a43
v_accvgpr_read_b32 v16,a70
v_mfma_f32_16x16x32_bf16 a[60:63],a[180:183],a[204:207],a[60:63]
.p2align 3
v_accvgpr_read_b32 v120,a112
v_accvgpr_read_b32 v121,a113
v_accvgpr_read_b32 v122,a114
v_accvgpr_read_b32 v123,a115
v_accvgpr_read_b32 v12,a116
v_accvgpr_read_b32 v13,a117
v_accvgpr_read_b32 v14,a118
v_accvgpr_read_b32 v15,a119
v_pk_mul_f32 v[120:121],s[60:61],v[120:121] op_sel:[1,0] op_sel_hi:[1,1]
v_pk_mul_f32 v[122:123],s[60:61],v[122:123] op_sel:[1,0] op_sel_hi:[1,1]
v_pk_mul_f32 v[12:13],s[60:61],v[12:13] op_sel:[1,0] op_sel_hi:[1,1]
v_pk_mul_f32 v[14:15],s[60:61],v[14:15] op_sel:[1,0] op_sel_hi:[1,1]
v_cvt_pk_bf16_f32 v120,v120,v121
v_cvt_pk_bf16_f32 v121,v122,v123
v_cvt_pk_bf16_f32 v122,v12,v13
v_cvt_pk_bf16_f32 v123,v14,v15
v_mfma_f32_16x16x32_bf16 a[60:63],a[176:179],a[200:203],a[60:63]
buffer_load_dwordx4 v4,s[24:27],s47 offen offset:2048 nt lds
v_mfma_f32_16x16x32_bf16 a[92:95],a[176:179],a[208:211],a[92:95]
v_accvgpr_read_b32 v17,a71
v_mul_f32 v98,s61,v98
v_mfma_f32_16x16x32_bf16 a[92:95],a[180:183],a[212:215],a[92:95]
v_mul_f32 v99,s61,v99
v_cvt_pk_bf16_f32 v84,v84,v85
v_mfma_f32_16x16x32_bf16 a[92:95],a[184:187],a[240:243],a[92:95]
v_mul_f32 v86,s61,v86
v_mul_f32 v87,s61,v87
v_mfma_f32_16x16x32_bf16 a[92:95],a[188:191],a[244:247],a[92:95]
v_mul_f32 v16,s61,v16
v_mul_f32 v17,s61,v17
v_mfma_f32_16x16x32_bf16 a[124:127],a[188:191],a[252:255],a[124:127]
ds_read_b64_tr_b16 a[188:189],v22 offset:50952
v_cvt_pk_bf16_f32 v98,v98,v99
v_mfma_f32_16x16x32_bf16 a[124:127],a[184:187],a[248:251],a[124:127]
ds_read_b64_tr_b16 a[184:185],v22 offset:34568
v_cvt_pk_bf16_f32 v85,v86,v87
v_mfma_f32_16x16x32_bf16 a[124:127],a[180:183],a[220:223],a[124:127]
ds_read_b64_tr_b16 a[180:181],v22 offset:50944
v_cvt_pk_bf16_f32 v99,v16,v17
v_mfma_f32_16x16x32_bf16 a[124:127],a[176:179],a[216:219],a[124:127]
s_waitcnt lgkmcnt(3)
ds_read_b64_tr_b16 a[176:177],v22 offset:34560
v_accvgpr_read_b32 v86,a44
v_mfma_f32_16x16x32_bf16 v[128:131],a[128:131],a[224:227],v[128:131]
ds_read_b64_tr_b16 a[178:179],v22 offset:36608
v_accvgpr_read_b32 v87,a45
v_mfma_f32_16x16x32_bf16 v[128:131],a[132:135],a[228:231],v[128:131]
ds_read_b64_tr_b16 a[182:183],v22 offset:52992
v_accvgpr_read_b32 v16,a46
v_mfma_f32_16x16x32_bf16 v[136:139],a[132:135],a[236:239],v[136:139]
ds_read_b64_tr_b16 a[186:187],v22 offset:36616
v_accvgpr_read_b32 v17,a47
v_mfma_f32_16x16x32_bf16 v[136:139],a[128:131],a[232:235],v[136:139]
ds_read_b64_tr_b16 a[190:191],v22 offset:53000
v_mul_f32 v86,s61,v86
v_mfma_f32_16x16x32_bf16 v[144:147],a[128:131],a[240:243],v[144:147]
.p2align 3
v_accvgpr_read_b32 v76,a24
v_accvgpr_read_b32 v77,a25
v_accvgpr_read_b32 v78,a26
v_accvgpr_read_b32 v79,a27
v_accvgpr_read_b32 v12,a28
v_accvgpr_read_b32 v13,a29
v_accvgpr_read_b32 v14,a30
v_accvgpr_read_b32 v15,a31
v_pk_mul_f32 v[76:77],s[60:61],v[76:77] op_sel:[1,0] op_sel_hi:[1,1]
v_pk_mul_f32 v[78:79],s[60:61],v[78:79] op_sel:[1,0] op_sel_hi:[1,1]
v_pk_mul_f32 v[12:13],s[60:61],v[12:13] op_sel:[1,0] op_sel_hi:[1,1]
v_pk_mul_f32 v[14:15],s[60:61],v[14:15] op_sel:[1,0] op_sel_hi:[1,1]
v_cvt_pk_bf16_f32 v76,v76,v77
v_cvt_pk_bf16_f32 v77,v78,v79
v_cvt_pk_bf16_f32 v78,v12,v13
v_cvt_pk_bf16_f32 v79,v14,v15
v_mfma_f32_16x16x32_bf16 v[144:147],a[132:135],a[244:247],v[144:147]
buffer_load_dwordx4 v5,s[24:27],s47 offen offset:3072 nt lds
v_mfma_f32_16x16x32_bf16 v[152:155],a[132:135],a[252:255],v[152:155]
s_or_b32 s47,s47,0x18000
s_addk_i32 s47,4096
s_and_b32 s47,s47,0x27000
v_mfma_f32_16x16x32_bf16 v[152:155],a[128:131],a[248:251],v[152:155]
s_or_b32 s47,s47,s46
v_mul_f32 v87,s61,v87
v_mul_f32 v16,s61,v16
v_mfma_f32_16x16x32_bf16 v[132:135],a[136:139],a[224:227],v[132:135]
s_and_b32 s46,s54,0xfffc0000
v_mul_f32 v17,s61,v17
v_cvt_pk_bf16_f32 v86,v86,v87
v_mfma_f32_16x16x32_bf16 v[132:135],a[140:143],a[228:231],v[132:135]
s_addk_i32 s54,16384
v_cvt_pk_bf16_f32 v87,v16,v17
v_mfma_f32_16x16x32_bf16 v[140:143],a[140:143],a[236:239],v[140:143]
.p2align 3
v_accvgpr_read_b32 v92,a56
v_accvgpr_read_b32 v93,a57
v_accvgpr_read_b32 v94,a58
v_accvgpr_read_b32 v95,a59
v_accvgpr_read_b32 v12,a60
v_accvgpr_read_b32 v13,a61
v_accvgpr_read_b32 v14,a62
v_accvgpr_read_b32 v15,a63
v_pk_mul_f32 v[92:93],s[60:61],v[92:93] op_sel:[1,0] op_sel_hi:[1,1]
v_pk_mul_f32 v[94:95],s[60:61],v[94:95] op_sel:[1,0] op_sel_hi:[1,1]
v_pk_mul_f32 v[12:13],s[60:61],v[12:13] op_sel:[1,0] op_sel_hi:[1,1]
v_pk_mul_f32 v[14:15],s[60:61],v[14:15] op_sel:[1,0] op_sel_hi:[1,1]
v_cvt_pk_bf16_f32 v92,v92,v93
v_cvt_pk_bf16_f32 v93,v94,v95
v_cvt_pk_bf16_f32 v94,v12,v13
v_cvt_pk_bf16_f32 v95,v14,v15
v_mfma_f32_16x16x32_bf16 v[140:143],a[136:139],a[232:235],v[140:143]
.p2align 3
v_accvgpr_read_b32 v108,a88
v_accvgpr_read_b32 v109,a89
v_accvgpr_read_b32 v110,a90
v_accvgpr_read_b32 v111,a91
v_accvgpr_read_b32 v12,a92
v_accvgpr_read_b32 v13,a93
v_accvgpr_read_b32 v14,a94
)A"
R"A(v_accvgpr_read_b32 v15,a95
v_pk_mul_f32 v[108:109],s[60:61],v[108:109] op_sel:[1,0] op_sel_hi:[1,1]
v_pk_mul_f32 v[110:111],s[60:61],v[110:111] op_sel:[1,0] op_sel_hi:[1,1]
v_pk_mul_f32 v[12:13],s[60:61],v[12:13] op_sel:[1,0] op_sel_hi:[1,1]
v_pk_mul_f32 v[14:15],s[60:61],v[14:15] op_sel:[1,0] op_sel_hi:[1,1]
v_cvt_pk_bf16_f32 v108,v108,v109
v_cvt_pk_bf16_f32 v109,v110,v111
v_cvt_pk_bf16_f32 v110,v12,v13
v_cvt_pk_bf16_f32 v111,v14,v15
v_mfma_f32_16x16x32_bf16 v[148:151],a[136:139],a[240:243],v[148:151]
.p2align 3
v_accvgpr_read_b32 v124,a120
v_accvgpr_read_b32 v125,a121
v_accvgpr_read_b32 v126,a122
v_accvgpr_read_b32 v127,a123
v_accvgpr_read_b32 v12,a124
v_accvgpr_read_b32 v13,a125
v_accvgpr_read_b32 v14,a126
v_accvgpr_read_b32 v15,a127
v_pk_mul_f32 v[124:125],s[60:61],v[124:125] op_sel:[1,0] op_sel_hi:[1,1]
v_pk_mul_f32 v[126:127],s[60:61],v[126:127] op_sel:[1,0] op_sel_hi:[1,1]
v_pk_mul_f32 v[12:13],s[60:61],v[12:13] op_sel:[1,0] op_sel_hi:[1,1]
v_pk_mul_f32 v[14:15],s[60:61],v[14:15] op_sel:[1,0] op_sel_hi:[1,1]
v_cvt_pk_bf16_f32 v124,v124,v125
v_cvt_pk_bf16_f32 v125,v126,v127
v_cvt_pk_bf16_f32 v126,v12,v13
v_cvt_pk_bf16_f32 v127,v14,v15
v_mfma_f32_16x16x32_bf16 v[148:151],a[140:143],a[244:247],v[148:151]
v_mfma_f32_16x16x32_bf16 v[156:159],a[140:143],a[252:255],v[156:159]
s_waitcnt vmcnt(8) lgkmcnt(0)
v_mfma_f32_16x16x32_bf16 v[156:159],a[136:139],a[248:251],v[156:159]
s_barrier
ds_read_b128 v[48:51],v18
v_mfma_f32_16x16x32_bf16 v[160:163],a[144:147],a[224:227],v[160:163]
ds_read_b64_tr_b16 a[192:193],v2
v_mfma_f32_16x16x32_bf16 v[160:163],a[148:151],a[228:231],v[160:163]
ds_read_b64_tr_b16 a[194:195],v2 offset:4096
v_mfma_f32_16x16x32_bf16 v[168:171],a[148:151],a[236:239],v[168:171]
ds_read_b64_tr_b16 a[196:197],v2 offset:8192
v_mfma_f32_16x16x32_bf16 v[168:171],a[144:147],a[232:235],v[168:171]
ds_read_b128 v[52:55],v18 offset:256
v_mfma_f32_16x16x32_bf16 v[176:179],a[144:147],a[240:243],v[176:179]
v_mul_f32 v128,s61,v128
v_mul_f32 v129,s61,v129
v_mul_f32 v130,s61,v130
v_mul_f32 v131,s61,v131
.p2align 3
v_cvt_pk_bf16_f32 v128,v128,v129
v_cvt_pk_bf16_f32 v129,v130,v131
v_pk_mul_f32 v[132:133],s[60:61],v[132:133] op_sel:[1,0] op_sel_hi:[1,1]
v_pk_mul_f32 v[134:135],s[60:61],v[134:135] op_sel:[1,0] op_sel_hi:[1,1]
v_pk_mul_f32 v[136:137],s[60:61],v[136:137] op_sel:[1,0] op_sel_hi:[1,1]
v_pk_mul_f32 v[138:139],s[60:61],v[138:139] op_sel:[1,0] op_sel_hi:[1,1]
v_pk_mul_f32 v[140:141],s[60:61],v[140:141] op_sel:[1,0] op_sel_hi:[1,1]
v_pk_mul_f32 v[142:143],s[60:61],v[142:143] op_sel:[1,0] op_sel_hi:[1,1]
v_pk_mul_f32 v[144:145],s[60:61],v[144:145] op_sel:[1,0] op_sel_hi:[1,1]
v_pk_mul_f32 v[146:147],s[60:61],v[146:147] op_sel:[1,0] op_sel_hi:[1,1]
v_pk_mul_f32 v[148:149],s[60:61],v[148:149] op_sel:[1,0] op_sel_hi:[1,1]
v_pk_mul_f32 v[150:151],s[60:61],v[150:151] op_sel:[1,0] op_sel_hi:[1,1]
v_pk_mul_f32 v[152:153],s[60:61],v[152:153] op_sel:[1,0] op_sel_hi:[1,1]
v_pk_mul_f32 v[154:155],s[60:61],v[154:155] op_sel:[1,0] op_sel_hi:[1,1]
v_pk_mul_f32 v[156:157],s[60:61],v[156:157] op_sel:[1,0] op_sel_hi:[1,1]
v_pk_mul_f32 v[158:159],s[60:61],v[158:159] op_sel:[1,0] op_sel_hi:[1,1]
v_mfma_f32_16x16x32_bf16 v[176:179],a[148:151],a[244:247],v[176:179]
ds_read_b128 v[56:59],v18 offset:512
v_cvt_pk_bf16_f32 v130,v132,v133
v_mfma_f32_16x16x32_bf16 v[184:187],a[148:151],a[252:255],v[184:187]
v_cvt_pk_bf16_f32 v132,v136,v137
v_cvt_pk_bf16_f32 v133,v138,v139
v_mfma_f32_16x16x32_bf16 v[184:187],a[144:147],a[248:251],v[184:187]
ds_read_b128 v[60:63],v18 offset:768
v_cvt_pk_bf16_f32 v131,v134,v135
v_mfma_f32_16x16x32_bf16 v[164:167],a[152:155],a[224:227],v[164:167]
v_cvt_pk_bf16_f32 v134,v140,v141
v_cvt_pk_bf16_f32 v135,v142,v143
v_mfma_f32_16x16x32_bf16 v[164:167],a[156:159],a[228:231],v[164:167]
ds_read_b128 v[20:23],v18 offset:16384
v_cvt_pk_bf16_f32 v144,v144,v145
v_mfma_f32_16x16x32_bf16 v[172:175],a[156:159],a[236:239],v[172:175]
v_cvt_pk_bf16_f32 v145,v146,v147
v_cvt_pk_bf16_f32 v146,v148,v149
v_mfma_f32_16x16x32_bf16 v[172:175],a[152:155],a[232:235],v[172:175]
ds_read_b128 a[128:131],v1
v_cvt_pk_bf16_f32 v148,v152,v153
v_mfma_f32_16x16x32_bf16 v[180:183],a[152:155],a[240:243],v[180:183]
v_cvt_pk_bf16_f32 v149,v154,v155
v_cvt_pk_bf16_f32 v147,v150,v151
v_mfma_f32_16x16x32_bf16 v[180:183],a[156:159],a[244:247],v[180:183]
ds_read_b128 a[132:135],v1 offset:256
v_cvt_pk_bf16_f32 v150,v156,v157
v_mfma_f32_16x16x32_bf16 v[188:191],a[156:159],a[252:255],v[188:191]
ds_read_b64_tr_b16 a[198:199],v2 offset:12288
v_cvt_pk_bf16_f32 v151,v158,v159
v_mfma_f32_16x16x32_bf16 v[188:191],a[152:155],a[248:251],v[188:191]
ds_read_b64_tr_b16 a[204:205],v3 offset:8192
v_mfma_f32_16x16x32_bf16 v[192:195],a[160:163],a[224:227],v[192:195]
ds_read_b64_tr_b16 a[206:207],v3 offset:12288
v_mfma_f32_16x16x32_bf16 v[192:195],a[164:167],a[228:231],v[192:195]
s_add_u32 m0,32768,s51
ds_read_b128 a[136:139],v1 offset:512
v_mfma_f32_16x16x32_bf16 v[200:203],a[164:167],a[236:239],v[200:203]
s_waitcnt lgkmcnt(6)
v_and_b32 v20,0x7fff7fff,v20
v_and_b32 v21,0x7fff7fff,v21
v_mfma_f32_16x16x32_bf16 v[200:203],a[160:163],a[232:235],v[200:203]
v_sad_u16 v20,v20,0,0
v_and_b32 v22,0x7fff7fff,v22
v_mfma_f32_16x16x32_bf16 v[208:211],a[160:163],a[240:243],v[208:211]
buffer_load_dwordx4 v4,s[20:23],s47 offen nt lds
v_mfma_f32_16x16x32_bf16 v[208:211],a[164:167],a[244:247],v[208:211]
v_sad_u16 v20,v21,0,v20
v_and_b32 v23,0x7fff7fff,v23
v_mfma_f32_16x16x32_bf16 v[216:219],a[164:167],a[252:255],v[216:219]
v_sad_u16 v20,v22,0,v20
v_sad_u16 v20,v23,0,v20
v_mfma_f32_16x16x32_bf16 v[216:219],a[160:163],a[248:251],v[216:219]
s_nop 1
v_add_u32_dpp v20,v20,v20 quad_perm:[1,0,3,2] row_mask:0xf bank_mask:0xf
v_mfma_f32_16x16x32_bf16 v[196:199],a[168:171],a[224:227],v[196:199]
s_nop 1
v_add_u32_dpp v20,v20,v20 quad_perm:[2,3,0,1] row_mask:0xf bank_mask:0xf
v_mfma_f32_16x16x32_bf16 v[196:199],a[172:175],a[228:231],v[196:199]
v_cvt_f32_u32 v20,v20
v_mov_b32 v21,0xb9800000
v_mfma_f32_16x16x32_bf16 v[204:207],a[172:175],a[236:239],v[204:207]
ds_read_b128 a[140:143],v1 offset:768
v_fmaak_f32 v20,v20,v21,0x42e8c937
v_mfma_f32_16x16x32_bf16 v[204:207],a[168:171],a[232:235],v[204:207]
s_nop 0
v_readfirstlane_b32 s52,v20
v_mfma_f32_16x16x32_bf16 v[212:215],a[168:171],a[240:243],v[212:215]
s_nop 1
s_min_u32 s52,s52,0xc2200000
v_mfma_f32_16x16x32_bf16 v[212:215],a[172:175],a[244:247],v[212:215]
s_min_i32 s52,s52,0x41a00000
ds_read_b128 a[156:159],v1 offset:4864
v_mfma_f32_16x16x32_bf16 v[220:223],a[172:175],a[252:255],v[220:223]
v_mul_f32 v160,s61,v160
v_mul_f32 v161,s61,v161
v_mul_f32 v162,s61,v162
v_mul_f32 v163,s61,v163
.p2align 3
v_cvt_pk_bf16_f32 v160,v160,v161
v_cvt_pk_bf16_f32 v161,v162,v163
v_pk_mul_f32 v[164:165],s[60:61],v[164:165] op_sel:[1,0] op_sel_hi:[1,1]
v_pk_mul_f32 v[166:167],s[60:61],v[166:167] op_sel:[1,0] op_sel_hi:[1,1]
v_pk_mul_f32 v[168:169],s[60:61],v[168:169] op_sel:[1,0] op_sel_hi:[1,1]
v_pk_mul_f32 v[170:171],s[60:61],v[170:171] op_sel:[1,0] op_sel_hi:[1,1]
v_pk_mul_f32 v[172:173],s[60:61],v[172:173] op_sel:[1,0] op_sel_hi:[1,1]
v_pk_mul_f32 v[174:175],s[60:61],v[174:175] op_sel:[1,0] op_sel_hi:[1,1]
v_pk_mul_f32 v[176:177],s[60:61],v[176:177] op_sel:[1,0] op_sel_hi:[1,1]
v_pk_mul_f32 v[178:179],s[60:61],v[178:179] op_sel:[1,0] op_sel_hi:[1,1]
v_pk_mul_f32 v[180:181],s[60:61],v[180:181] op_sel:[1,0] op_sel_hi:[1,1]
v_pk_mul_f32 v[182:183],s[60:61],v[182:183] op_sel:[1,0] op_sel_hi:[1,1]
v_pk_mul_f32 v[184:185],s[60:61],v[184:185] op_sel:[1,0] op_sel_hi:[1,1]
v_pk_mul_f32 v[186:187],s[60:61],v[186:187] op_sel:[1,0] op_sel_hi:[1,1]
v_pk_mul_f32 v[188:189],s[60:61],v[188:189] op_sel:[1,0] op_sel_hi:[1,1]
v_pk_mul_f32 v[190:191],s[60:61],v[190:191] op_sel:[1,0] op_sel_hi:[1,1]
v_mfma_f32_16x16x32_bf16 v[220:223],a[168:171],a[248:251],v[220:223]
ds_read_b128 a[152:155],v1 offset:4608
v_cvt_pk_bf16_f32 v162,v164,v165
v_mfma_f32_16x16x32_bf16 v[224:227],a[176:179],a[224:227],v[224:227]
v_cvt_pk_bf16_f32 v164,v168,v169
v_cvt_pk_bf16_f32 v165,v170,v171
v_mfma_f32_16x16x32_bf16 v[224:227],a[180:183],a[228:231],v[224:227]
buffer_load_dwordx4 v4,s[20:23],s47 offen offset:1024 nt lds
v_mfma_f32_16x16x32_bf16 v[232:235],a[180:183],a[236:239],v[232:235]
ds_read_b128 a[148:151],v1 offset:4352
v_cvt_pk_bf16_f32 v163,v166,v167
v_mfma_f32_16x16x32_bf16 v[232:235],a[176:179],a[232:235],v[232:235]
v_cvt_pk_bf16_f32 v166,v172,v173
v_cvt_pk_bf16_f32 v167,v174,v175
v_mfma_f32_16x16x32_bf16 v[240:243],a[176:179],a[240:243],v[240:243]
ds_read_b128 a[144:147],v1 offset:4096
v_cvt_pk_bf16_f32 v176,v176,v177
v_mfma_f32_16x16x32_bf16 v[240:243],a[180:183],a[244:247],v[240:243]
v_cvt_pk_bf16_f32 v177,v178,v179
v_cvt_pk_bf16_f32 v178,v180,v181
v_mfma_f32_16x16x32_bf16 v[248:251],a[180:183],a[252:255],v[248:251]
ds_read_b128 a[160:163],v1 offset:8192
v_cvt_pk_bf16_f32 v180,v184,v185
v_mfma_f32_16x16x32_bf16 v[248:251],a[176:179],a[248:251],v[248:251]
v_cvt_pk_bf16_f32 v181,v186,v187
v_cvt_pk_bf16_f32 v179,v182,v183
v_mfma_f32_16x16x32_bf16 v[228:231],a[184:187],a[224:227],v[228:231]
ds_read_b128 a[164:167],v1 offset:8448
v_cvt_pk_bf16_f32 v182,v188,v189
v_mfma_f32_16x16x32_bf16 v[228:231],a[188:191],a[228:231],v[228:231]
ds_read_b64_tr_b16 a[200:201],v3
v_cvt_pk_bf16_f32 v183,v190,v191
v_mfma_f32_16x16x32_bf16 v[236:239],a[188:191],a[236:239],v[236:239]
v_mul_f32 v192,s61,v192
v_mul_f32 v193,s61,v193
v_mul_f32 v194,s61,v194
v_mul_f32 v195,s61,v195
.p2align 3
v_cvt_pk_bf16_f32 v192,v192,v193
v_cvt_pk_bf16_f32 v193,v194,v195
v_pk_mul_f32 v[196:197],s[60:61],v[196:197] op_sel:[1,0] op_sel_hi:[1,1]
v_pk_mul_f32 v[198:199],s[60:61],v[198:199] op_sel:[1,0] op_sel_hi:[1,1]
v_pk_mul_f32 v[200:201],s[60:61],v[200:201] op_sel:[1,0] op_sel_hi:[1,1]
v_pk_mul_f32 v[202:203],s[60:61],v[202:203] op_sel:[1,0] op_sel_hi:[1,1]
v_pk_mul_f32 v[204:205],s[60:61],v[204:205] op_sel:[1,0] op_sel_hi:[1,1]
v_pk_mul_f32 v[206:207],s[60:61],v[206:207] op_sel:[1,0] op_sel_hi:[1,1]
v_pk_mul_f32 v[208:209],s[60:61],v[208:209] op_sel:[1,0] op_sel_hi:[1,1]
v_pk_mul_f32 v[210:211],s[60:61],v[210:211] op_sel:[1,0] op_sel_hi:[1,1]
v_pk_mul_f32 v[212:213],s[60:61],v[212:213] op_sel:[1,0] op_sel_hi:[1,1]
v_pk_mul_f32 v[214:215],s[60:61],v[214:215] op_sel:[1,0] op_sel_hi:[1,1]
v_pk_mul_f32 v[216:217],s[60:61],v[216:217] op_sel:[1,0] op_sel_hi:[1,1]
v_pk_mul_f32 v[218:219],s[60:61],v[218:219] op_sel:[1,0] op_sel_hi:[1,1]
v_pk_mul_f32 v[220:221],s[60:61],v[220:221] op_sel:[1,0] op_sel_hi:[1,1]
v_pk_mul_f32 v[222:223],s[60:61],v[222:223] op_sel:[1,0] op_sel_hi:[1,1]
v_mfma_f32_16x16x32_bf16 v[236:239],a[184:187],a[232:235],v[236:239]
ds_read_b128 a[168:171],v1 offset:8704
v_cvt_pk_bf16_f32 v194,v196,v197
v_mfma_f32_16x16x32_bf16 v[244:247],a[184:187],a[240:243],v[244:247]
v_cvt_pk_bf16_f32 v196,v200,v201
v_cvt_pk_bf16_f32 v197,v202,v203
v_mfma_f32_16x16x32_bf16 v[244:247],a[188:191],a[244:247],v[244:247]
ds_read_b128 a[172:175],v1 offset:8960
v_cvt_pk_bf16_f32 v195,v198,v199
v_mfma_f32_16x16x32_bf16 v[252:255],a[188:191],a[252:255],v[252:255]
buffer_load_dwordx4 v5,s[20:23],s47 offen offset:2048 nt lds
v_mfma_f32_16x16x32_bf16 v[252:255],a[184:187],a[248:251],v[252:255]
ds_read_b128 a[188:191],v1 offset:13056
v_cvt_pk_bf16_f32 v198,v204,v205
v_mfma_f32_16x16x32_bf16 v[32:35],v[48:51],v[64:67],0
v_cvt_pk_bf16_f32 v199,v206,v207
v_cvt_pk_bf16_f32 v208,v208,v209
v_mfma_f32_16x16x32_bf16 v[32:35],v[52:55],v[68:71],v[32:35]
ds_read_b128 a[184:187],v1 offset:12800
v_cvt_pk_bf16_f32 v209,v210,v211
v_mfma_f32_16x16x32_bf16 v[32:35],v[56:59],v[72:75],v[32:35]
v_cvt_pk_bf16_f32 v210,v212,v213
v_cvt_pk_bf16_f32 v212,v216,v217
v_mfma_f32_16x16x32_bf16 v[32:35],v[60:63],v[76:79],v[32:35]
ds_read_b128 a[180:183],v1 offset:12544
v_cvt_pk_bf16_f32 v213,v218,v219
v_mfma_f32_16x16x32_bf16 v[36:39],v[48:51],v[80:83],0
ds_read_b64_tr_b16 a[202:203],v3 offset:4096
v_cvt_pk_bf16_f32 v211,v214,v215
v_mfma_f32_16x16x32_bf16 v[36:39],v[52:55],v[84:87],v[36:39]
v_cvt_pk_bf16_f32 v214,v220,v221
v_cvt_pk_bf16_f32 v215,v222,v223
v_mfma_f32_16x16x32_bf16 v[36:39],v[56:59],v[88:91],v[36:39]
ds_read_b128 a[176:179],v1 offset:12288
v_add_u32 v1,s48,v1
)A"
R"A(v_mfma_f32_16x16x32_bf16 v[36:39],v[60:63],v[92:95],v[36:39]
ds_read_b64_tr_b16 a[208:209],v2 offset:256
v_mfma_f32_16x16x32_bf16 v[40:43],v[48:51],v[96:99],0
ds_read_b64_tr_b16 a[210:211],v2 offset:4352
v_mfma_f32_16x16x32_bf16 v[40:43],v[52:55],v[100:103],v[40:43]
v_max3_f32 v32,v32,v33,v34
v_max3_f32 v32,v32,v35,s18
v_mfma_f32_16x16x32_bf16 v[40:43],v[56:59],v[104:107],v[40:43]
v_sub_f32 v35,s52,v32
v_sub_f32 v34,s52,v32
v_mfma_f32_16x16x32_bf16 v[40:43],v[60:63],v[108:111],v[40:43]
v_sub_f32 v33,s52,v32
v_sub_f32 v32,s52,v32
v_mfma_f32_16x16x32_bf16 v[44:47],v[48:51],v[112:115],0
buffer_load_dwordx4 v5,s[20:23],s47 offen offset:3072 nt lds
v_mfma_f32_16x16x32_bf16 v[44:47],v[52:55],v[116:119],v[44:47]
v_max3_f32 v36,v36,v37,v38
v_max3_f32 v36,v36,v39,s19
v_mfma_f32_16x16x32_bf16 v[44:47],v[56:59],v[120:123],v[44:47]
v_sub_f32 v39,s52,v36
v_sub_f32 v38,s52,v36
v_mfma_f32_16x16x32_bf16 v[44:47],v[60:63],v[124:127],v[44:47]
s_waitcnt lgkmcnt(1)
v_sub_f32 v37,s52,v36
v_sub_f32 v36,s52,v36
v_mfma_f32_16x16x32_bf16 v[136:139],a[128:131],v[64:67],v[32:35]
ds_read_b64_tr_b16 a[212:213],v2 offset:8448
ds_read_b64_tr_b16 a[214:215],v2 offset:12544
v_mfma_f32_16x16x32_bf16 v[136:139],a[132:135],v[68:71],v[136:139]
v_max3_f32 v40,v40,v41,v42
v_max3_f32 v40,v40,v43,s100
v_mfma_f32_16x16x32_bf16 v[136:139],a[136:139],v[72:75],v[136:139]
v_sub_f32 v43,s52,v40
v_sub_f32 v42,s52,v40
v_mfma_f32_16x16x32_bf16 v[136:139],a[140:143],v[76:79],v[136:139]
v_sub_f32 v41,s52,v40
v_sub_f32 v40,s52,v40
v_mfma_f32_16x16x32_bf16 v[140:143],a[156:159],v[76:79],v[32:35]
ds_read_b64_tr_b16 a[220:221],v3 offset:8448
ds_read_b64_tr_b16 a[222:223],v3 offset:12544
v_mfma_f32_16x16x32_bf16 v[140:143],a[152:155],v[72:75],v[140:143]
s_addk_i32 m0,16384
ds_read_b64_tr_b16 a[216:217],v3 offset:256
v_max3_f32 v44,v44,v45,v46
v_mfma_f32_16x16x32_bf16 v[140:143],a[148:151],v[68:71],v[140:143]
ds_read_b64_tr_b16 a[218:219],v3 offset:4352
v_max3_f32 v44,v44,v47,s101
v_mfma_f32_16x16x32_bf16 v[140:143],a[144:147],v[64:67],v[140:143]
ds_read_b64_tr_b16 a[224:225],v2 offset:512
v_mov_b32 v48,0
v_mfma_f32_16x16x32_bf16 v[152:155],a[160:163],v[64:67],v[32:35]
buffer_load_dwordx4 v4,s[24:27],s47 offen nt lds
v_mfma_f32_16x16x32_bf16 v[152:155],a[164:167],v[68:71],v[152:155]
v_exp_f32 v136,v136
v_mfma_f32_16x16x32_bf16 v[152:155],a[168:171],v[72:75],v[152:155]
v_exp_f32 v137,v137
v_mfma_f32_16x16x32_bf16 v[152:155],a[172:175],v[76:79],v[152:155]
v_exp_f32 v138,v138
v_mfma_f32_16x16x32_bf16 v[156:159],a[188:191],v[76:79],v[32:35]
v_exp_f32 v139,v139
v_mfma_f32_16x16x32_bf16 v[156:159],a[184:187],v[72:75],v[156:159]
v_cvt_pk_bf16_f32 v136,v136,v137
v_cvt_pk_bf16_f32 v137,v138,v139
v_mfma_f32_16x16x32_bf16 v[156:159],a[180:183],v[68:71],v[156:159]
v_exp_f32 v140,v140
v_mfma_f32_16x16x32_bf16 v[156:159],a[176:179],v[64:67],v[156:159]
v_exp_f32 v141,v141
v_mfma_f32_16x16x32_bf16 v[168:171],a[128:131],v[80:83],v[36:39]
v_exp_f32 v142,v142
v_mfma_f32_16x16x32_bf16 v[168:171],a[132:135],v[84:87],v[168:171]
v_exp_f32 v143,v143
v_mfma_f32_16x16x32_bf16 v[168:171],a[136:139],v[88:91],v[168:171]
v_cvt_pk_bf16_f32 v138,v140,v141
v_cvt_pk_bf16_f32 v139,v142,v143
v_mfma_f32_16x16x32_bf16 v[168:171],a[140:143],v[92:95],v[168:171]
v_exp_f32 v152,v152
v_mfma_f32_16x16x32_bf16 v[172:175],a[156:159],v[92:95],v[36:39]
v_exp_f32 v153,v153
v_mfma_f32_16x16x32_bf16 v[172:175],a[152:155],v[88:91],v[172:175]
buffer_load_dwordx4 v5,s[24:27],s47 offen offset:1024 nt lds
v_mfma_f32_16x16x32_bf16 v[172:175],a[148:151],v[84:87],v[172:175]
v_exp_f32 v154,v154
v_mfma_f32_16x16x32_bf16 v[172:175],a[144:147],v[80:83],v[172:175]
v_exp_f32 v155,v155
v_mfma_f32_16x16x32_bf16 v[184:187],a[160:163],v[80:83],v[36:39]
v_exp_f32 v156,v156
v_mfma_f32_16x16x32_bf16 v[184:187],a[164:167],v[84:87],v[184:187]
v_exp_f32 v157,v157
v_mfma_f32_16x16x32_bf16 v[184:187],a[168:171],v[88:91],v[184:187]
v_exp_f32 v158,v158
v_mfma_f32_16x16x32_bf16 v[184:187],a[172:175],v[92:95],v[184:187]
v_exp_f32 v159,v159
v_mfma_f32_16x16x32_bf16 v[188:191],a[188:191],v[92:95],v[36:39]
v_cvt_pk_bf16_f32 v140,v152,v153
v_cvt_pk_bf16_f32 v141,v154,v155
v_mfma_f32_16x16x32_bf16 v[188:191],a[184:187],v[88:91],v[188:191]
v_cvt_pk_bf16_f32 v142,v156,v157
v_cvt_pk_bf16_f32 v143,v158,v159
v_mfma_f32_16x16x32_bf16 v[188:191],a[180:183],v[84:87],v[188:191]
ds_read_b64_tr_b16 a[226:227],v2 offset:4608
ds_read_b64_tr_b16 a[228:229],v2 offset:8704
v_mfma_f32_16x16x32_bf16 v[188:191],a[176:179],v[80:83],v[188:191]
ds_read_b64_tr_b16 a[230:231],v2 offset:12800
v_mov_b32 v49,0
v_mfma_f32_16x16x32_bf16 v[200:203],a[128:131],v[96:99],v[40:43]
ds_read_b64_tr_b16 a[236:237],v3 offset:8704
v_mov_b32 v50,0
v_mfma_f32_16x16x32_bf16 v[200:203],a[132:135],v[100:103],v[200:203]
ds_read_b64_tr_b16 a[238:239],v3 offset:12800
v_mov_b32 v51,0
v_mfma_f32_16x16x32_bf16 v[200:203],a[136:139],v[104:107],v[200:203]
buffer_load_dwordx4 v4,s[24:27],s47 offen offset:2048 nt lds
v_mfma_f32_16x16x32_bf16 v[200:203],a[140:143],v[108:111],v[200:203]
ds_read_b64_tr_b16 a[232:233],v3 offset:512
ds_read_b64_tr_b16 a[234:235],v3 offset:4608
v_mfma_f32_16x16x32_bf16 v[204:207],a[156:159],v[108:111],v[40:43]
ds_read_b64_tr_b16 a[240:241],v2 offset:768
v_sub_f32 v47,s52,v44
v_mfma_f32_16x16x32_bf16 v[204:207],a[152:155],v[104:107],v[204:207]
ds_read_b64_tr_b16 a[242:243],v2 offset:4864
v_sub_f32 v46,s52,v44
v_mfma_f32_16x16x32_bf16 v[204:207],a[148:151],v[100:103],v[204:207]
ds_read_b64_tr_b16 a[244:245],v2 offset:8960
v_sub_f32 v45,s52,v44
v_mfma_f32_16x16x32_bf16 v[204:207],a[144:147],v[96:99],v[204:207]
ds_read_b64_tr_b16 a[246:247],v2 offset:13056
ds_read_b64_tr_b16 a[252:253],v3 offset:8960
v_mfma_f32_16x16x32_bf16 v[216:219],a[160:163],v[96:99],v[40:43]
ds_read_b64_tr_b16 a[254:255],v3 offset:13056
v_sub_f32 v44,s52,v44
v_mfma_f32_16x16x32_bf16 v[216:219],a[164:167],v[100:103],v[216:219]
ds_read_b64_tr_b16 a[248:249],v3 offset:768
v_mov_b32 v52,0
v_mfma_f32_16x16x32_bf16 v[216:219],a[168:171],v[104:107],v[216:219]
ds_read_b64_tr_b16 a[250:251],v3 offset:4864
v_mov_b32 v53,0
v_mfma_f32_16x16x32_bf16 v[216:219],a[172:175],v[108:111],v[216:219]
v_mul_f32 v224,s61,v224
v_mul_f32 v225,s61,v225
v_mul_f32 v226,s61,v226
v_mul_f32 v227,s61,v227
.p2align 3
v_cvt_pk_bf16_f32 v224,v224,v225
v_cvt_pk_bf16_f32 v225,v226,v227
v_pk_mul_f32 v[228:229],s[60:61],v[228:229] op_sel:[1,0] op_sel_hi:[1,1]
v_pk_mul_f32 v[230:231],s[60:61],v[230:231] op_sel:[1,0] op_sel_hi:[1,1]
v_pk_mul_f32 v[232:233],s[60:61],v[232:233] op_sel:[1,0] op_sel_hi:[1,1]
v_pk_mul_f32 v[234:235],s[60:61],v[234:235] op_sel:[1,0] op_sel_hi:[1,1]
v_pk_mul_f32 v[236:237],s[60:61],v[236:237] op_sel:[1,0] op_sel_hi:[1,1]
v_pk_mul_f32 v[238:239],s[60:61],v[238:239] op_sel:[1,0] op_sel_hi:[1,1]
v_pk_mul_f32 v[240:241],s[60:61],v[240:241] op_sel:[1,0] op_sel_hi:[1,1]
v_pk_mul_f32 v[242:243],s[60:61],v[242:243] op_sel:[1,0] op_sel_hi:[1,1]
v_pk_mul_f32 v[244:245],s[60:61],v[244:245] op_sel:[1,0] op_sel_hi:[1,1]
v_pk_mul_f32 v[246:247],s[60:61],v[246:247] op_sel:[1,0] op_sel_hi:[1,1]
v_pk_mul_f32 v[248:249],s[60:61],v[248:249] op_sel:[1,0] op_sel_hi:[1,1]
v_pk_mul_f32 v[250:251],s[60:61],v[250:251] op_sel:[1,0] op_sel_hi:[1,1]
v_pk_mul_f32 v[252:253],s[60:61],v[252:253] op_sel:[1,0] op_sel_hi:[1,1]
v_pk_mul_f32 v[254:255],s[60:61],v[254:255] op_sel:[1,0] op_sel_hi:[1,1]
v_mfma_f32_16x16x32_bf16 v[220:223],a[188:191],v[108:111],v[40:43]
v_cvt_pk_bf16_f32 v226,v228,v229
v_cvt_pk_bf16_f32 v228,v232,v233
v_mfma_f32_16x16x32_bf16 v[220:223],a[184:187],v[104:107],v[220:223]
v_cvt_pk_bf16_f32 v229,v234,v235
v_cvt_pk_bf16_f32 v227,v230,v231
v_mfma_f32_16x16x32_bf16 v[220:223],a[180:183],v[100:103],v[220:223]
v_cvt_pk_bf16_f32 v230,v236,v237
v_cvt_pk_bf16_f32 v231,v238,v239
v_mfma_f32_16x16x32_bf16 v[220:223],a[176:179],v[96:99],v[220:223]
buffer_load_dwordx4 v5,s[24:27],s47 offen offset:3072 nt lds
v_mfma_f32_16x16x32_bf16 a[0:3],a[192:195],v[136:139],0.15915494
s_or_b32 s47,s47,0x18000
v_cvt_pk_bf16_f32 v240,v240,v241
v_cvt_pk_bf16_f32 v241,v242,v243
v_mfma_f32_16x16x32_bf16 a[0:3],a[196:199],v[140:143],a[0:3]
s_addk_i32 s47,4096
v_cvt_pk_bf16_f32 v242,v244,v245
v_cvt_pk_bf16_f32 v244,v248,v249
v_mfma_f32_16x16x32_bf16 a[4:7],a[204:207],v[140:143],0.15915494
s_and_b32 s47,s47,0x27000
s_or_b32 s47,s47,s46
v_cvt_pk_bf16_f32 v245,v250,v251
v_mfma_f32_16x16x32_bf16 a[4:7],a[200:203],v[136:139],a[4:7]
s_memrealtime s[76:77]
v_cvt_pk_bf16_f32 v243,v246,v247
v_cvt_pk_bf16_f32 v246,v252,v253
s_waitcnt lgkmcnt(0)
v_mfma_f32_16x16x32_bf16 a[8:11],a[208:211],v[136:139],0.15915494
s_and_b32 s46,s54,0xfffc0000
v_cvt_pk_bf16_f32 v247,v254,v255
v_mov_b32 v54,0
v_mfma_f32_16x16x32_bf16 a[8:11],a[212:215],v[140:143],a[8:11]
v_exp_f32 v168,v168
v_mfma_f32_16x16x32_bf16 a[12:15],a[220:223],v[140:143],0.15915494
v_exp_f32 v169,v169
v_mfma_f32_16x16x32_bf16 a[12:15],a[216:219],v[136:139],a[12:15]
v_exp_f32 v170,v170
v_mfma_f32_16x16x32_bf16 a[16:19],a[224:227],v[136:139],0.15915494
v_exp_f32 v171,v171
v_mfma_f32_16x16x32_bf16 a[16:19],a[228:231],v[140:143],a[16:19]
v_cvt_pk_bf16_f32 v168,v168,v169
v_cvt_pk_bf16_f32 v169,v170,v171
v_mfma_f32_16x16x32_bf16 a[20:23],a[236:239],v[140:143],0.15915494
s_waitcnt lgkmcnt(0)
v_exp_f32 v172,v172
v_mfma_f32_16x16x32_bf16 a[20:23],a[232:235],v[136:139],a[20:23]
s_add_u32 s78,s76,s60
v_exp_f32 v173,v173
v_mfma_f32_16x16x32_bf16 a[24:27],a[240:243],v[136:139],0.15915494
s_lshr_b32 s60,s94,2
v_exp_f32 v174,v174
v_mfma_f32_16x16x32_bf16 a[24:27],a[244:247],v[140:143],a[24:27]
s_max_i32 s60,s60,124
v_exp_f32 v175,v175
v_mfma_f32_16x16x32_bf16 a[28:31],a[252:255],v[140:143],0.15915494
v_cvt_pk_bf16_f32 v170,v172,v173
v_cvt_pk_bf16_f32 v171,v174,v175
v_mfma_f32_16x16x32_bf16 a[28:31],a[248:251],v[136:139],a[28:31]
v_exp_f32 v184,v184
v_smfmac_f32_16x16x64_bf16 v[48:51],v[28:31],v[136:143],v27
v_mov_b32 v55,0
v_mfma_f32_16x16x32_bf16 v[232:235],a[128:131],v[112:115],v[44:47]
s_waitcnt vmcnt(8)
v_exp_f32 v185,v185
v_mfma_f32_16x16x32_bf16 v[232:235],a[132:135],v[116:119],v[232:235]
s_barrier
v_exp_f32 v186,v186
v_mfma_f32_16x16x32_bf16 v[232:235],a[136:139],v[120:123],v[232:235]
v_exp_f32 v187,v187
v_mfma_f32_16x16x32_bf16 v[232:235],a[140:143],v[124:127],v[232:235]
s_add_u32 m0,65536,s51
v_exp_f32 v188,v188
v_mfma_f32_16x16x32_bf16 v[236:239],a[156:159],v[124:127],v[44:47]
v_exp_f32 v189,v189
v_mfma_f32_16x16x32_bf16 v[236:239],a[152:155],v[120:123],v[236:239]
v_exp_f32 v190,v190
v_mfma_f32_16x16x32_bf16 v[236:239],a[148:151],v[116:119],v[236:239]
buffer_load_dwordx4 v4,s[20:23],s47 offen nt lds
v_mfma_f32_16x16x32_bf16 v[236:239],a[144:147],v[112:115],v[236:239]
v_exp_f32 v191,v191
v_mfma_f32_16x16x32_bf16 v[248:251],a[160:163],v[112:115],v[44:47]
v_cvt_pk_bf16_f32 v172,v184,v185
v_cvt_pk_bf16_f32 v173,v186,v187
v_mfma_f32_16x16x32_bf16 v[248:251],a[164:167],v[116:119],v[248:251]
v_cvt_pk_bf16_f32 v174,v188,v189
v_cvt_pk_bf16_f32 v175,v190,v191
v_mfma_f32_16x16x32_bf16 v[248:251],a[168:171],v[120:123],v[248:251]
ds_read_b128 a[128:131],v1
v_mov_b32 v56,0
v_mfma_f32_16x16x32_bf16 v[248:251],a[172:175],v[124:127],v[248:251]
v_exp_f32 v200,v200
v_mfma_f32_16x16x32_bf16 v[252:255],a[188:191],v[124:127],v[44:47]
ds_read_b128 a[132:135],v1 offset:256
v_mov_b32 v57,0
v_mfma_f32_16x16x32_bf16 v[252:255],a[184:187],v[120:123],v[252:255]
v_exp_f32 v201,v201
v_mfma_f32_16x16x32_bf16 v[252:255],a[180:183],v[116:119],v[252:255]
ds_read_b128 a[136:139],v1 offset:512
v_cvt_pk_bf16_f32 v200,v200,v201
v_mfma_f32_16x16x32_bf16 v[252:255],a[176:179],v[112:115],v[252:255]
v_exp_f32 v202,v202
v_mfma_f32_16x16x32_bf16 a[32:35],a[192:195],v[168:171],0.15915494
ds_read_b128 a[140:143],v1 offset:768
v_mov_b32 v58,0
v_mfma_f32_16x16x32_bf16 a[32:35],a[196:199],v[172:175],a[32:35]
v_exp_f32 v203,v203
v_mfma_f32_16x16x32_bf16 a[36:39],a[204:207],v[172:175],0.15915494
ds_read_b128 a[156:159],v1 offset:4864
v_cvt_pk_bf16_f32 v201,v202,v203
v_mfma_f32_16x16x32_bf16 a[36:39],a[200:203],v[168:171],a[36:39]
v_exp_f32 v204,v204
v_mfma_f32_16x16x32_bf16 a[40:43],a[208:211],v[168:171],0.15915494
buffer_load_dwordx4 v4,s[20:23],s47 offen offset:1024 nt lds
)A"
R"A(v_mfma_f32_16x16x32_bf16 a[40:43],a[212:215],v[172:175],a[40:43]
ds_read_b128 a[152:155],v1 offset:4608
v_mov_b32 v59,0
v_mfma_f32_16x16x32_bf16 a[44:47],a[220:223],v[172:175],0.15915494
v_exp_f32 v205,v205
v_mfma_f32_16x16x32_bf16 a[44:47],a[216:219],v[168:171],a[44:47]
ds_read_b128 a[148:151],v1 offset:4352
v_cvt_pk_bf16_f32 v202,v204,v205
v_mfma_f32_16x16x32_bf16 a[48:51],a[224:227],v[168:171],0.15915494
v_exp_f32 v206,v206
v_mfma_f32_16x16x32_bf16 a[48:51],a[228:231],v[172:175],a[48:51]
ds_read_b128 a[144:147],v1 offset:4096
v_mov_b32 v60,0
v_mfma_f32_16x16x32_bf16 a[52:55],a[236:239],v[172:175],0.15915494
v_exp_f32 v207,v207
v_mfma_f32_16x16x32_bf16 a[52:55],a[232:235],v[168:171],a[52:55]
ds_read_b128 a[160:163],v1 offset:8192
v_cvt_pk_bf16_f32 v203,v206,v207
v_mfma_f32_16x16x32_bf16 a[56:59],a[240:243],v[168:171],0.15915494
v_exp_f32 v216,v216
v_mfma_f32_16x16x32_bf16 a[56:59],a[244:247],v[172:175],a[56:59]
ds_read_b128 a[164:167],v1 offset:8448
v_mov_b32 v61,0
v_mfma_f32_16x16x32_bf16 a[60:63],a[252:255],v[172:175],0.15915494
v_exp_f32 v217,v217
v_mfma_f32_16x16x32_bf16 a[60:63],a[248:251],v[168:171],a[60:63]
ds_read_b128 a[168:171],v1 offset:8704
v_cvt_pk_bf16_f32 v204,v216,v217
v_smfmac_f32_16x16x64_bf16 v[52:55],v[28:31],v[168:175],v27
s_waitcnt lgkmcnt(4)
v_mov_b32 v62,0
v_mfma_f32_16x16x32_bf16 v[136:139],a[128:131],v[64:67],v[32:35]
ds_read_b128 a[172:175],v1 offset:8960
v_mov_b32 v63,0
v_mfma_f32_16x16x32_bf16 v[136:139],a[132:135],v[68:71],v[136:139]
buffer_load_dwordx4 v5,s[20:23],s47 offen offset:2048 nt lds
v_mfma_f32_16x16x32_bf16 v[136:139],a[136:139],v[72:75],v[136:139]
ds_read_b128 a[188:191],v1 offset:13056
v_add_u32 v2,s48,v2
v_mfma_f32_16x16x32_bf16 v[136:139],a[140:143],v[76:79],v[136:139]
v_exp_f32 v218,v218
v_mfma_f32_16x16x32_bf16 v[140:143],a[156:159],v[76:79],v[32:35]
ds_read_b128 a[184:187],v1 offset:12800
v_add_u32 v3,s48,v3
v_mfma_f32_16x16x32_bf16 v[140:143],a[152:155],v[72:75],v[140:143]
ds_read_b128 a[180:183],v1 offset:12544
v_mfma_f32_16x16x32_bf16 v[140:143],a[148:151],v[68:71],v[140:143]
s_waitcnt lgkmcnt(4)
ds_read_b128 a[176:179],v1 offset:12288
v_mfma_f32_16x16x32_bf16 v[140:143],a[144:147],v[64:67],v[140:143]
v_mfma_f32_16x16x32_bf16 v[152:155],a[160:163],v[64:67],v[32:35]
s_mov_b32 s48,32768
v_exp_f32 v219,v219
v_mfma_f32_16x16x32_bf16 v[152:155],a[164:167],v[68:71],v[152:155]
v_exp_f32 v220,v220
v_mfma_f32_16x16x32_bf16 v[152:155],a[168:171],v[72:75],v[152:155]
s_waitcnt lgkmcnt(3)
v_exp_f32 v221,v221
v_mfma_f32_16x16x32_bf16 v[152:155],a[172:175],v[76:79],v[152:155]
v_exp_f32 v222,v222
v_mfma_f32_16x16x32_bf16 v[156:159],a[188:191],v[76:79],v[32:35]
s_waitcnt lgkmcnt(2)
v_exp_f32 v223,v223
v_mfma_f32_16x16x32_bf16 v[156:159],a[184:187],v[72:75],v[156:159]
s_waitcnt lgkmcnt(1)
v_cvt_pk_bf16_f32 v205,v218,v219
v_cvt_pk_bf16_f32 v206,v220,v221
v_mfma_f32_16x16x32_bf16 v[156:159],a[180:183],v[68:71],v[156:159]
s_waitcnt lgkmcnt(0)
v_cvt_pk_bf16_f32 v207,v222,v223
v_add_u32 v1,s48,v1
v_mfma_f32_16x16x32_bf16 v[156:159],a[176:179],v[64:67],v[156:159]
buffer_load_dwordx4 v5,s[20:23],s47 offen offset:3072 nt lds
v_mfma_f32_16x16x32_bf16 a[64:67],a[192:195],v[200:203],0.15915494
v_exp_f32 v232,v232
v_mfma_f32_16x16x32_bf16 a[64:67],a[196:199],v[204:207],a[64:67]
v_exp_f32 v233,v233
v_mfma_f32_16x16x32_bf16 a[68:71],a[204:207],v[204:207],0.15915494
v_exp_f32 v234,v234
v_mfma_f32_16x16x32_bf16 a[68:71],a[200:203],v[200:203],a[68:71]
v_exp_f32 v235,v235
v_mfma_f32_16x16x32_bf16 a[72:75],a[208:211],v[200:203],0.15915494
v_exp_f32 v236,v236
v_mfma_f32_16x16x32_bf16 a[72:75],a[212:215],v[204:207],a[72:75]
v_exp_f32 v237,v237
v_mfma_f32_16x16x32_bf16 a[76:79],a[220:223],v[204:207],0.15915494
v_exp_f32 v238,v238
v_mfma_f32_16x16x32_bf16 a[76:79],a[216:219],v[200:203],a[76:79]
v_exp_f32 v239,v239
v_mfma_f32_16x16x32_bf16 a[80:83],a[224:227],v[200:203],0.15915494
v_exp_f32 v248,v248
v_mfma_f32_16x16x32_bf16 a[80:83],a[228:231],v[204:207],a[80:83]
v_exp_f32 v249,v249
v_mfma_f32_16x16x32_bf16 a[84:87],a[236:239],v[204:207],0.15915494
s_addk_i32 m0,16384
v_exp_f32 v250,v250
v_mfma_f32_16x16x32_bf16 a[84:87],a[232:235],v[200:203],a[84:87]
v_exp_f32 v251,v251
v_mfma_f32_16x16x32_bf16 a[88:91],a[240:243],v[200:203],0.15915494
v_cvt_pk_bf16_f32 v232,v232,v233
v_cvt_pk_bf16_f32 v233,v234,v235
v_mfma_f32_16x16x32_bf16 a[88:91],a[244:247],v[204:207],a[88:91]
buffer_load_dwordx4 v4,s[24:27],s47 offen nt lds
v_mfma_f32_16x16x32_bf16 a[92:95],a[252:255],v[204:207],0.15915494
v_cvt_pk_bf16_f32 v234,v236,v237
v_cvt_pk_bf16_f32 v235,v238,v239
v_mfma_f32_16x16x32_bf16 a[92:95],a[248:251],v[200:203],a[92:95]
v_exp_f32 v252,v252
v_smfmac_f32_16x16x64_bf16 v[56:59],v[28:31],v[200:207],v27
v_cvt_pk_bf16_f32 v236,v248,v249
v_mfma_f32_16x16x32_bf16 v[168:171],a[128:131],v[80:83],v[36:39]
v_exp_f32 v253,v253
v_mfma_f32_16x16x32_bf16 v[168:171],a[132:135],v[84:87],v[168:171]
v_exp_f32 v254,v254
v_mfma_f32_16x16x32_bf16 v[168:171],a[136:139],v[88:91],v[168:171]
v_exp_f32 v255,v255
v_mfma_f32_16x16x32_bf16 v[168:171],a[140:143],v[92:95],v[168:171]
v_cvt_pk_bf16_f32 v237,v250,v251
v_cvt_pk_bf16_f32 v238,v252,v253
v_mfma_f32_16x16x32_bf16 v[172:175],a[156:159],v[92:95],v[36:39]
v_cvt_pk_bf16_f32 v239,v254,v255
v_mfma_f32_16x16x32_bf16 v[172:175],a[152:155],v[88:91],v[172:175]
v_exp_f32 v136,v136
v_mfma_f32_16x16x32_bf16 v[172:175],a[148:151],v[84:87],v[172:175]
v_exp_f32 v137,v137
v_mfma_f32_16x16x32_bf16 v[172:175],a[144:147],v[80:83],v[172:175]
v_exp_f32 v138,v138
v_mfma_f32_16x16x32_bf16 v[184:187],a[160:163],v[80:83],v[36:39]
v_exp_f32 v139,v139
v_mfma_f32_16x16x32_bf16 v[184:187],a[164:167],v[84:87],v[184:187]
v_exp_f32 v140,v140
v_mfma_f32_16x16x32_bf16 v[184:187],a[168:171],v[88:91],v[184:187]
buffer_load_dwordx4 v5,s[24:27],s47 offen offset:1024 nt lds
v_mfma_f32_16x16x32_bf16 v[184:187],a[172:175],v[92:95],v[184:187]
v_exp_f32 v141,v141
v_mfma_f32_16x16x32_bf16 v[188:191],a[188:191],v[92:95],v[36:39]
v_exp_f32 v142,v142
v_mfma_f32_16x16x32_bf16 v[188:191],a[184:187],v[88:91],v[188:191]
v_exp_f32 v143,v143
v_mfma_f32_16x16x32_bf16 v[188:191],a[180:183],v[84:87],v[188:191]
v_exp_f32 v152,v152
v_mfma_f32_16x16x32_bf16 v[188:191],a[176:179],v[80:83],v[188:191]
v_exp_f32 v153,v153
v_mfma_f32_16x16x32_bf16 a[96:99],a[192:195],v[232:235],0.15915494
ds_read_b64_tr_b16 a[192:193],v2
ds_read_b64_tr_b16 a[194:195],v2 offset:4096
v_mfma_f32_16x16x32_bf16 a[96:99],a[196:199],v[236:239],a[96:99]
ds_read_b64_tr_b16 a[196:197],v2 offset:8192
v_cvt_pk_bf16_f32 v136,v136,v137
v_mfma_f32_16x16x32_bf16 a[100:103],a[204:207],v[236:239],0.15915494
ds_read_b64_tr_b16 a[198:199],v2 offset:12288
v_cvt_pk_bf16_f32 v137,v138,v139
v_mfma_f32_16x16x32_bf16 a[100:103],a[200:203],v[232:235],a[100:103]
ds_read_b64_tr_b16 a[204:205],v3 offset:8192
v_cvt_pk_bf16_f32 v138,v140,v141
v_mfma_f32_16x16x32_bf16 a[104:107],a[208:211],v[232:235],0.15915494
ds_read_b64_tr_b16 a[206:207],v3 offset:12288
ds_read_b64_tr_b16 a[200:201],v3
v_mfma_f32_16x16x32_bf16 a[104:107],a[212:215],v[236:239],a[104:107]
ds_read_b64_tr_b16 a[202:203],v3 offset:4096
v_cvt_pk_bf16_f32 v139,v142,v143
v_mfma_f32_16x16x32_bf16 a[108:111],a[220:223],v[236:239],0.15915494
ds_read_b64_tr_b16 a[208:209],v2 offset:256
v_cvt_pk_bf16_f32 v140,v152,v153
v_mfma_f32_16x16x32_bf16 a[108:111],a[216:219],v[232:235],a[108:111]
v_exp_f32 v154,v154
v_mfma_f32_16x16x32_bf16 a[112:115],a[224:227],v[232:235],0.15915494
buffer_load_dwordx4 v4,s[24:27],s47 offen offset:2048 nt lds
v_mfma_f32_16x16x32_bf16 a[112:115],a[228:231],v[236:239],a[112:115]
ds_read_b64_tr_b16 a[210:211],v2 offset:4352
ds_read_b64_tr_b16 a[212:213],v2 offset:8448
v_mfma_f32_16x16x32_bf16 a[116:119],a[236:239],v[236:239],0.15915494
v_exp_f32 v155,v155
v_mfma_f32_16x16x32_bf16 a[116:119],a[232:235],v[232:235],a[116:119]
ds_read_b64_tr_b16 a[214:215],v2 offset:12544
ds_read_b64_tr_b16 a[220:221],v3 offset:8448
v_mfma_f32_16x16x32_bf16 a[120:123],a[240:243],v[232:235],0.15915494
v_exp_f32 v156,v156
v_mfma_f32_16x16x32_bf16 a[120:123],a[244:247],v[236:239],a[120:123]
v_exp_f32 v157,v157
v_mfma_f32_16x16x32_bf16 a[124:127],a[252:255],v[236:239],0.15915494
v_exp_f32 v158,v158
v_mfma_f32_16x16x32_bf16 a[124:127],a[248:251],v[232:235],a[124:127]
v_exp_f32 v159,v159
v_smfmac_f32_16x16x64_bf16 v[60:63],v[28:31],v[232:239],v27
ds_read_b64_tr_b16 a[222:223],v3 offset:12544
v_mfma_f32_16x16x32_bf16 v[200:203],a[128:131],v[96:99],v[40:43]
v_cvt_pk_bf16_f32 v141,v154,v155
v_cvt_pk_bf16_f32 v142,v156,v157
v_mfma_f32_16x16x32_bf16 v[200:203],a[132:135],v[100:103],v[200:203]
ds_read_b64_tr_b16 a[216:217],v3 offset:256
v_cvt_pk_bf16_f32 v143,v158,v159
v_mfma_f32_16x16x32_bf16 v[200:203],a[136:139],v[104:107],v[200:203]
ds_read_b64_tr_b16 a[218:219],v3 offset:4352
ds_read_b64_tr_b16 a[224:225],v2 offset:512
v_mfma_f32_16x16x32_bf16 v[200:203],a[140:143],v[108:111],v[200:203]
ds_read_b64_tr_b16 a[226:227],v2 offset:4608
v_mfma_f32_16x16x32_bf16 v[204:207],a[156:159],v[108:111],v[40:43]
ds_read_b64_tr_b16 a[228:229],v2 offset:8704
v_mfma_f32_16x16x32_bf16 v[204:207],a[152:155],v[104:107],v[204:207]
buffer_load_dwordx4 v5,s[24:27],s47 offen offset:3072 nt lds
v_mfma_f32_16x16x32_bf16 v[204:207],a[148:151],v[100:103],v[204:207]
s_or_b32 s47,s47,0x18000
ds_read_b64_tr_b16 a[230:231],v2 offset:12800
ds_read_b64_tr_b16 a[236:237],v3 offset:8704
v_mfma_f32_16x16x32_bf16 v[204:207],a[144:147],v[96:99],v[204:207]
s_addk_i32 s47,4096
s_and_b32 s47,s47,0x27000
ds_read_b64_tr_b16 a[238:239],v3 offset:12800
v_mfma_f32_16x16x32_bf16 v[216:219],a[160:163],v[96:99],v[40:43]
s_or_b32 s47,s47,s46
ds_read_b64_tr_b16 a[232:233],v3 offset:512
v_mfma_f32_16x16x32_bf16 v[216:219],a[164:167],v[100:103],v[216:219]
ds_read_b64_tr_b16 a[234:235],v3 offset:4608
v_mfma_f32_16x16x32_bf16 v[216:219],a[168:171],v[104:107],v[216:219]
ds_read_b64_tr_b16 a[240:241],v2 offset:768
ds_read_b64_tr_b16 a[242:243],v2 offset:4864
v_mfma_f32_16x16x32_bf16 v[216:219],a[172:175],v[108:111],v[216:219]
ds_read_b64_tr_b16 a[244:245],v2 offset:8960
v_mfma_f32_16x16x32_bf16 v[220:223],a[188:191],v[108:111],v[40:43]
ds_read_b64_tr_b16 a[246:247],v2 offset:13056
v_mfma_f32_16x16x32_bf16 v[220:223],a[184:187],v[104:107],v[220:223]
ds_read_b64_tr_b16 a[252:253],v3 offset:8960
v_mfma_f32_16x16x32_bf16 v[220:223],a[180:183],v[100:103],v[220:223]
ds_read_b64_tr_b16 a[254:255],v3 offset:13056
ds_read_b64_tr_b16 a[248:249],v3 offset:768
v_mfma_f32_16x16x32_bf16 v[220:223],a[176:179],v[96:99],v[220:223]
s_waitcnt lgkmcnt(9)
ds_read_b64_tr_b16 a[250:251],v3 offset:4864
v_mfma_f32_16x16x32_bf16 a[0:3],a[192:195],v[136:139],a[0:3]
v_exp_f32 v168,v168
v_mfma_f32_16x16x32_bf16 a[0:3],a[196:199],v[140:143],a[0:3]
v_exp_f32 v169,v169
v_mfma_f32_16x16x32_bf16 a[4:7],a[204:207],v[140:143],a[4:7]
v_exp_f32 v170,v170
v_mfma_f32_16x16x32_bf16 a[4:7],a[200:203],v[136:139],a[4:7]
s_memrealtime s[76:77]
v_exp_f32 v171,v171
v_mfma_f32_16x16x32_bf16 a[8:11],a[208:211],v[136:139],a[8:11]
v_cvt_pk_bf16_f32 v168,v168,v169
v_cvt_pk_bf16_f32 v169,v170,v171
v_mfma_f32_16x16x32_bf16 a[8:11],a[212:215],v[140:143],a[8:11]
v_exp_f32 v172,v172
v_mfma_f32_16x16x32_bf16 a[12:15],a[220:223],v[140:143],a[12:15]
v_exp_f32 v173,v173
v_mfma_f32_16x16x32_bf16 a[12:15],a[216:219],v[136:139],a[12:15]
v_exp_f32 v174,v174
v_mfma_f32_16x16x32_bf16 a[16:19],a[224:227],v[136:139],a[16:19]
v_exp_f32 v175,v175
v_mfma_f32_16x16x32_bf16 a[16:19],a[228:231],v[140:143],a[16:19]
v_exp_f32 v184,v184
v_mfma_f32_16x16x32_bf16 a[20:23],a[236:239],v[140:143],a[20:23]
s_waitcnt lgkmcnt(0)
v_exp_f32 v185,v185
v_mfma_f32_16x16x32_bf16 a[20:23],a[232:235],v[136:139],a[20:23]
s_sub_u32 s79,s78,s76
v_exp_f32 v186,v186
v_mfma_f32_16x16x32_bf16 a[24:27],a[240:243],v[136:139],a[24:27]
s_add_u32 s78,s76,s60
v_exp_f32 v187,v187
v_mfma_f32_16x16x32_bf16 a[24:27],a[244:247],v[140:143],a[24:27]
s_cmp_gt_i32 s79,s95
s_cbranch_scc1 L_ps2_%=
L_pr2_%=:
v_cvt_pk_bf16_f32 v170,v172,v173
v_mfma_f32_16x16x32_bf16 a[28:31],a[252:255],v[140:143],a[28:31]
v_exp_f32 v188,v188
v_exp_f32 v189,v189
v_exp_f32 v190,v190
v_exp_f32 v191,v191
)A"
R"A(v_mfma_f32_16x16x32_bf16 a[28:31],a[248:251],v[136:139],a[28:31]
.p2align 3
v_cvt_pk_bf16_f32 v171,v174,v175
v_cvt_pk_bf16_f32 v172,v184,v185
v_cvt_pk_bf16_f32 v173,v186,v187
v_cvt_pk_bf16_f32 v174,v188,v189
v_cvt_pk_bf16_f32 v175,v190,v191
v_smfmac_f32_16x16x64_bf16 v[48:51],v[28:31],v[136:143],v27
s_mov_b32 s53,13
s_cmp_eq_u32 s40,3
s_cbranch_scc0 L_att0_%=
s_cmp_gt_i32 s18,0
s_cbranch_scc1 L_att0v0_%=
s_cmp_gt_i32 s19,0
s_cbranch_scc1 L_att0v1_%=
s_cmp_gt_i32 s100,0
s_cbranch_scc1 L_att0v2_%=
.p2align 5
L_att0v3_%=:
s_waitcnt vmcnt(8) lgkmcnt(0)
v_exp_f32 v200,v200
s_barrier
ds_read_b128 a[128:131],v1
v_add_u32 v2,s48,v2
s_mov_b32 s90,L_endseg_%=-L_pc_%=+128
v_exp_f32 v201,v201
s_add_u32 m0,s49,s51
ds_read_b128 a[132:135],v1 offset:256
v_cvt_pk_bf16_f32 v200,v200,v201
s_lshr_b32 s45,s45,1
v_exp_f32 v202,v202
ds_read_b128 a[136:139],v1 offset:512
v_add_u32 v3,s48,v3
buffer_load_dwordx4 v4,s[20:23],s47 offen nt lds
v_exp_f32 v203,v203
s_add_u32 s62,s50,32768
ds_read_b128 a[140:143],v1 offset:768
v_cvt_pk_bf16_f32 v201,v202,v203
s_and_b32 s62,s62,131071
v_exp_f32 v204,v204
s_addk_i32 s54,16384
v_exp_f32 v205,v205
s_sub_u32 s48,s62,s50
ds_read_b128 a[156:159],v1 offset:4864
v_cvt_pk_bf16_f32 v202,v204,v205
s_sub_u32 s53,s53,1
v_exp_f32 v206,v206
s_and_b32 s46,s54,0xfffc0000
v_exp_f32 v207,v207
s_mov_b32 s50,s62
ds_read_b128 a[152:155],v1 offset:4608
v_cvt_pk_bf16_f32 v203,v206,v207
s_xor_b32 s49,s50,65536
v_exp_f32 v216,v216
v_mfma_f32_16x16x32_bf16 a[32:35],a[192:195],v[168:171],a[32:35]
ds_read_b128 a[148:151],v1 offset:4352
v_mfma_f32_16x16x32_bf16 a[32:35],a[196:199],v[172:175],a[32:35]
v_exp_f32 v217,v217
v_mfma_f32_16x16x32_bf16 a[36:39],a[204:207],v[172:175],a[36:39]
ds_read_b128 a[144:147],v1 offset:4096
v_cvt_pk_bf16_f32 v204,v216,v217
v_mfma_f32_16x16x32_bf16 a[36:39],a[200:203],v[168:171],a[36:39]
v_exp_f32 v218,v218
v_mfma_f32_16x16x32_bf16 a[40:43],a[208:211],v[168:171],a[40:43]
buffer_load_dwordx4 v4,s[20:23],s47 offen offset:1024 nt lds
v_mfma_f32_16x16x32_bf16 a[40:43],a[212:215],v[172:175],a[40:43]
ds_read_b128 a[160:163],v1 offset:8192
v_mfma_f32_16x16x32_bf16 a[44:47],a[220:223],v[172:175],a[44:47]
v_exp_f32 v219,v219
v_mfma_f32_16x16x32_bf16 a[44:47],a[216:219],v[168:171],a[44:47]
ds_read_b128 a[164:167],v1 offset:8448
v_cvt_pk_bf16_f32 v205,v218,v219
v_mfma_f32_16x16x32_bf16 a[48:51],a[224:227],v[168:171],a[48:51]
v_exp_f32 v220,v220
v_mfma_f32_16x16x32_bf16 a[48:51],a[228:231],v[172:175],a[48:51]
ds_read_b128 a[168:171],v1 offset:8704
v_mfma_f32_16x16x32_bf16 a[52:55],a[236:239],v[172:175],a[52:55]
v_exp_f32 v221,v221
v_mfma_f32_16x16x32_bf16 a[52:55],a[232:235],v[168:171],a[52:55]
ds_read_b128 a[172:175],v1 offset:8960
v_cvt_pk_bf16_f32 v206,v220,v221
v_mfma_f32_16x16x32_bf16 a[56:59],a[240:243],v[168:171],a[56:59]
v_exp_f32 v222,v222
v_mfma_f32_16x16x32_bf16 a[56:59],a[244:247],v[172:175],a[56:59]
ds_read_b128 a[188:191],v1 offset:13056
v_mfma_f32_16x16x32_bf16 a[60:63],a[252:255],v[172:175],a[60:63]
v_exp_f32 v223,v223
v_mfma_f32_16x16x32_bf16 a[60:63],a[248:251],v[168:171],a[60:63]
ds_read_b128 a[184:187],v1 offset:12800
v_cvt_pk_bf16_f32 v207,v222,v223
v_smfmac_f32_16x16x64_bf16 v[52:55],v[28:31],v[168:175],v27
s_waitcnt lgkmcnt(4)
v_mfma_f32_16x16x32_bf16 v[136:139],a[128:131],v[64:67],v[32:35]
ds_read_b128 a[180:183],v1 offset:12544
v_mfma_f32_16x16x32_bf16 v[136:139],a[132:135],v[68:71],v[136:139]
buffer_load_dwordx4 v5,s[20:23],s47 offen offset:2048 nt lds
v_mfma_f32_16x16x32_bf16 v[136:139],a[136:139],v[72:75],v[136:139]
ds_read_b128 a[176:179],v1 offset:12288
v_add_u32 v1,s48,v1
v_mfma_f32_16x16x32_bf16 v[136:139],a[140:143],v[76:79],v[136:139]
v_mfma_f32_16x16x32_bf16 v[140:143],a[156:159],v[76:79],v[32:35]
v_mfma_f32_16x16x32_bf16 v[140:143],a[152:155],v[72:75],v[140:143]
v_mfma_f32_16x16x32_bf16 v[140:143],a[148:151],v[68:71],v[140:143]
v_mfma_f32_16x16x32_bf16 v[140:143],a[144:147],v[64:67],v[140:143]
v_mfma_f32_16x16x32_bf16 v[152:155],a[160:163],v[64:67],v[32:35]
v_mfma_f32_16x16x32_bf16 v[152:155],a[164:167],v[68:71],v[152:155]
s_waitcnt lgkmcnt(1)
v_mfma_f32_16x16x32_bf16 v[152:155],a[168:171],v[72:75],v[152:155]
v_mfma_f32_16x16x32_bf16 v[152:155],a[172:175],v[76:79],v[152:155]
v_mfma_f32_16x16x32_bf16 v[156:159],a[188:191],v[76:79],v[32:35]
v_mfma_f32_16x16x32_bf16 v[156:159],a[184:187],v[72:75],v[156:159]
v_mfma_f32_16x16x32_bf16 v[156:159],a[180:183],v[68:71],v[156:159]
s_waitcnt lgkmcnt(0)
v_mfma_f32_16x16x32_bf16 v[156:159],a[176:179],v[64:67],v[156:159]
buffer_load_dwordx4 v5,s[20:23],s47 offen offset:3072 nt lds
v_mfma_f32_16x16x32_bf16 a[64:67],a[192:195],v[200:203],a[64:67]
v_mfma_f32_16x16x32_bf16 a[64:67],a[196:199],v[204:207],a[64:67]
v_mfma_f32_16x16x32_bf16 a[68:71],a[204:207],v[204:207],a[68:71]
v_mfma_f32_16x16x32_bf16 a[68:71],a[200:203],v[200:203],a[68:71]
v_mfma_f32_16x16x32_bf16 a[72:75],a[208:211],v[200:203],a[72:75]
v_exp_f32 v136,v136
v_mfma_f32_16x16x32_bf16 a[72:75],a[212:215],v[204:207],a[72:75]
v_exp_f32 v137,v137
v_mfma_f32_16x16x32_bf16 a[76:79],a[220:223],v[204:207],a[76:79]
v_exp_f32 v138,v138
v_mfma_f32_16x16x32_bf16 a[76:79],a[216:219],v[200:203],a[76:79]
v_exp_f32 v139,v139
v_mfma_f32_16x16x32_bf16 a[80:83],a[224:227],v[200:203],a[80:83]
v_exp_f32 v140,v140
v_mfma_f32_16x16x32_bf16 a[80:83],a[228:231],v[204:207],a[80:83]
v_exp_f32 v141,v141
v_mfma_f32_16x16x32_bf16 a[84:87],a[236:239],v[204:207],a[84:87]
s_addk_i32 m0,16384
v_exp_f32 v142,v142
v_mfma_f32_16x16x32_bf16 a[84:87],a[232:235],v[200:203],a[84:87]
v_exp_f32 v143,v143
v_mfma_f32_16x16x32_bf16 a[88:91],a[240:243],v[200:203],a[88:91]
v_exp_f32 v152,v152
v_mfma_f32_16x16x32_bf16 a[88:91],a[244:247],v[204:207],a[88:91]
buffer_load_dwordx4 v4,s[24:27],s47 offen nt lds
v_mfma_f32_16x16x32_bf16 a[92:95],a[252:255],v[204:207],a[92:95]
v_mfma_f32_16x16x32_bf16 a[92:95],a[248:251],v[200:203],a[92:95]
v_smfmac_f32_16x16x64_bf16 v[56:59],v[28:31],v[200:207],v27
v_mfma_f32_16x16x32_bf16 v[168:171],a[128:131],v[80:83],v[36:39]
v_mfma_f32_16x16x32_bf16 v[168:171],a[132:135],v[84:87],v[168:171]
v_mfma_f32_16x16x32_bf16 v[168:171],a[136:139],v[88:91],v[168:171]
v_exp_f32 v153,v153
v_mfma_f32_16x16x32_bf16 v[168:171],a[140:143],v[92:95],v[168:171]
v_exp_f32 v154,v154
v_mfma_f32_16x16x32_bf16 v[172:175],a[156:159],v[92:95],v[36:39]
v_exp_f32 v155,v155
v_mfma_f32_16x16x32_bf16 v[172:175],a[152:155],v[88:91],v[172:175]
v_exp_f32 v156,v156
v_mfma_f32_16x16x32_bf16 v[172:175],a[148:151],v[84:87],v[172:175]
v_exp_f32 v157,v157
v_mfma_f32_16x16x32_bf16 v[172:175],a[144:147],v[80:83],v[172:175]
v_exp_f32 v158,v158
v_mfma_f32_16x16x32_bf16 v[184:187],a[160:163],v[80:83],v[36:39]
v_exp_f32 v159,v159
v_mfma_f32_16x16x32_bf16 v[184:187],a[164:167],v[84:87],v[184:187]
v_exp_f32 v168,v168
v_mfma_f32_16x16x32_bf16 v[184:187],a[168:171],v[88:91],v[184:187]
buffer_load_dwordx4 v5,s[24:27],s47 offen offset:1024 nt lds
v_mfma_f32_16x16x32_bf16 v[184:187],a[172:175],v[92:95],v[184:187]
v_exp_f32 v169,v169
v_mfma_f32_16x16x32_bf16 v[188:191],a[188:191],v[92:95],v[36:39]
v_exp_f32 v170,v170
v_mfma_f32_16x16x32_bf16 v[188:191],a[184:187],v[88:91],v[188:191]
v_exp_f32 v171,v171
v_mfma_f32_16x16x32_bf16 v[188:191],a[180:183],v[84:87],v[188:191]
v_exp_f32 v172,v172
v_mfma_f32_16x16x32_bf16 v[188:191],a[176:179],v[80:83],v[188:191]
v_exp_f32 v173,v173
ds_read_b64_tr_b16 a[192:193],v2
v_cvt_pk_bf16_f32 v168,v168,v169
ds_read_b64_tr_b16 a[194:195],v2 offset:4096
v_cvt_pk_bf16_f32 v169,v170,v171
ds_read_b64_tr_b16 a[196:197],v2 offset:8192
v_cvt_pk_bf16_f32 v170,v172,v173
v_exp_f32 v174,v174
v_exp_f32 v175,v175
ds_read_b64_tr_b16 a[198:199],v2 offset:12288
v_cvt_pk_bf16_f32 v171,v174,v175
v_exp_f32 v184,v184
v_exp_f32 v185,v185
buffer_load_dwordx4 v4,s[24:27],s47 offen offset:2048 nt lds
ds_read_b64_tr_b16 a[204:205],v3 offset:8192
v_cvt_pk_bf16_f32 v172,v184,v185
ds_read_b64_tr_b16 a[206:207],v3 offset:12288
v_cvt_pk_bf16_f32 v136,v136,v137
v_exp_f32 v186,v186
ds_read_b64_tr_b16 a[200:201],v3
ds_read_b64_tr_b16 a[202:203],v3 offset:4096
v_cvt_pk_bf16_f32 v137,v138,v139
ds_read_b64_tr_b16 a[208:209],v2 offset:256
ds_read_b64_tr_b16 a[210:211],v2 offset:4352
ds_read_b64_tr_b16 a[212:213],v2 offset:8448
v_mfma_f32_16x16x32_bf16 v[200:203],a[128:131],v[96:99],v[40:43]
ds_read_b64_tr_b16 a[214:215],v2 offset:12544
v_cvt_pk_bf16_f32 v138,v140,v141
v_mfma_f32_16x16x32_bf16 v[200:203],a[132:135],v[100:103],v[200:203]
ds_read_b64_tr_b16 a[220:221],v3 offset:8448
v_mfma_f32_16x16x32_bf16 v[200:203],a[136:139],v[104:107],v[200:203]
ds_read_b64_tr_b16 a[222:223],v3 offset:12544
v_cvt_pk_bf16_f32 v139,v142,v143
v_mfma_f32_16x16x32_bf16 v[200:203],a[140:143],v[108:111],v[200:203]
ds_read_b64_tr_b16 a[216:217],v3 offset:256
v_mfma_f32_16x16x32_bf16 v[204:207],a[156:159],v[108:111],v[40:43]
ds_read_b64_tr_b16 a[218:219],v3 offset:4352
v_mfma_f32_16x16x32_bf16 v[204:207],a[152:155],v[104:107],v[204:207]
buffer_load_dwordx4 v5,s[24:27],s47 offen offset:3072 nt lds
v_mfma_f32_16x16x32_bf16 v[204:207],a[148:151],v[100:103],v[204:207]
s_or_b32 s47,s47,0x18000
ds_read_b64_tr_b16 a[224:225],v2 offset:512
v_cvt_pk_bf16_f32 v140,v152,v153
v_mfma_f32_16x16x32_bf16 v[204:207],a[144:147],v[96:99],v[204:207]
s_addk_i32 s47,4096
s_and_b32 s47,s47,0x27000
ds_read_b64_tr_b16 a[226:227],v2 offset:4608
v_mfma_f32_16x16x32_bf16 v[216:219],a[160:163],v[96:99],v[40:43]
s_or_b32 s47,s47,s46
ds_read_b64_tr_b16 a[228:229],v2 offset:8704
v_cvt_pk_bf16_f32 v141,v154,v155
v_mfma_f32_16x16x32_bf16 v[216:219],a[164:167],v[100:103],v[216:219]
ds_read_b64_tr_b16 a[230:231],v2 offset:12800
v_mfma_f32_16x16x32_bf16 v[216:219],a[168:171],v[104:107],v[216:219]
ds_read_b64_tr_b16 a[236:237],v3 offset:8704
v_mfma_f32_16x16x32_bf16 v[216:219],a[172:175],v[108:111],v[216:219]
ds_read_b64_tr_b16 a[238:239],v3 offset:12800
v_cvt_pk_bf16_f32 v142,v156,v157
v_mfma_f32_16x16x32_bf16 v[220:223],a[188:191],v[108:111],v[40:43]
ds_read_b64_tr_b16 a[232:233],v3 offset:512
v_mfma_f32_16x16x32_bf16 v[220:223],a[184:187],v[104:107],v[220:223]
ds_read_b64_tr_b16 a[234:235],v3 offset:4608
v_mfma_f32_16x16x32_bf16 v[220:223],a[180:183],v[100:103],v[220:223]
ds_read_b64_tr_b16 a[240:241],v2 offset:768
v_cvt_pk_bf16_f32 v143,v158,v159
v_mfma_f32_16x16x32_bf16 v[220:223],a[176:179],v[96:99],v[220:223]
s_waitcnt lgkmcnt(7)
buffer_load_dword v26,v24,s[88:91],s93 offen
v_mfma_f32_16x16x32_bf16 a[0:3],a[192:195],v[136:139],a[0:3]
ds_read_b64_tr_b16 a[242:243],v2 offset:4864
v_mfma_f32_16x16x32_bf16 a[0:3],a[196:199],v[140:143],a[0:3]
ds_read_b64_tr_b16 a[244:245],v2 offset:8960
v_mfma_f32_16x16x32_bf16 a[4:7],a[204:207],v[140:143],a[4:7]
ds_read_b64_tr_b16 a[246:247],v2 offset:13056
v_mfma_f32_16x16x32_bf16 a[4:7],a[200:203],v[136:139],a[4:7]
ds_read_b64_tr_b16 a[252:253],v3 offset:8960
v_mfma_f32_16x16x32_bf16 a[8:11],a[208:211],v[136:139],a[8:11]
ds_read_b64_tr_b16 a[254:255],v3 offset:13056
v_mfma_f32_16x16x32_bf16 a[8:11],a[212:215],v[140:143],a[8:11]
ds_read_b64_tr_b16 a[248:249],v3 offset:768
v_mfma_f32_16x16x32_bf16 a[12:15],a[220:223],v[140:143],a[12:15]
ds_read_b64_tr_b16 a[250:251],v3 offset:4864
v_mfma_f32_16x16x32_bf16 a[12:15],a[216:219],v[136:139],a[12:15]
s_bitcmp1_b32 s45,0
s_cbranch_scc0 L_t31v30_%=
s_mov_b32 m0,s80
s_addk_i32 s80,4096
buffer_load_dwordx4 v25,s[20:23],s83 offen nt lds
s_addk_i32 s83,1024
L_t31v30_%=:
v_mfma_f32_16x16x32_bf16 a[16:19],a[224:227],v[136:139],a[16:19]
s_waitcnt lgkmcnt(6)
v_exp_f32 v187,v187
v_mfma_f32_16x16x32_bf16 a[16:19],a[228:231],v[140:143],a[16:19]
v_exp_f32 v188,v188
v_mfma_f32_16x16x32_bf16 a[20:23],a[236:239],v[140:143],a[20:23]
v_exp_f32 v189,v189
v_mfma_f32_16x16x32_bf16 a[20:23],a[232:235],v[136:139],a[20:23]
v_exp_f32 v190,v190
v_mfma_f32_16x16x32_bf16 a[24:27],a[240:243],v[136:139],a[24:27]
s_waitcnt lgkmcnt(2)
v_exp_f32 v191,v191
v_mfma_f32_16x16x32_bf16 a[24:27],a[244:247],v[140:143],a[24:27]
v_cvt_pk_bf16_f32 v173,v186,v187
v_mfma_f32_16x16x32_bf16 a[28:31],a[252:255],v[140:143],a[28:31]
s_waitcnt lgkmcnt(0)
v_cvt_pk_bf16_f32 v174,v188,v189
v_cvt_pk_bf16_f32 v175,v190,v191
v_mfma_f32_16x16x32_bf16 a[28:31],a[248:251],v[136:139],a[28:31]
)A"
R"A(s_cmp_lg_u32 s53,0
v_smfmac_f32_16x16x64_bf16 v[48:51],v[28:31],v[136:143],v27
s_cbranch_scc1 L_att0v3_%=
s_waitcnt lgkmcnt(0)
s_nop 7
s_branch L_att0x_%=
.p2align 5
L_att0v0_%=:
v_mfma_f32_16x16x32_bf16 v[232:235],a[128:131],v[112:115],v[44:47]
s_waitcnt vmcnt(8) lgkmcnt(0)
v_exp_f32 v200,v200
v_mfma_f32_16x16x32_bf16 v[232:235],a[132:135],v[116:119],v[232:235]
s_barrier
ds_read_b128 a[128:131],v1
v_add_u32 v2,s48,v2
v_mfma_f32_16x16x32_bf16 v[232:235],a[136:139],v[120:123],v[232:235]
s_mov_b32 s90,L_endseg_%=-L_pc_%=+128
v_exp_f32 v201,v201
v_mfma_f32_16x16x32_bf16 v[232:235],a[140:143],v[124:127],v[232:235]
s_add_u32 m0,s49,s51
ds_read_b128 a[132:135],v1 offset:256
v_cvt_pk_bf16_f32 v200,v200,v201
v_mfma_f32_16x16x32_bf16 v[236:239],a[156:159],v[124:127],v[44:47]
s_lshr_b32 s45,s45,1
v_exp_f32 v202,v202
v_mfma_f32_16x16x32_bf16 v[236:239],a[152:155],v[120:123],v[236:239]
ds_read_b128 a[136:139],v1 offset:512
v_add_u32 v3,s48,v3
v_mfma_f32_16x16x32_bf16 v[236:239],a[148:151],v[116:119],v[236:239]
buffer_load_dwordx4 v4,s[20:23],s47 offen nt lds
v_mfma_f32_16x16x32_bf16 v[236:239],a[144:147],v[112:115],v[236:239]
v_exp_f32 v203,v203
v_mfma_f32_16x16x32_bf16 v[248:251],a[160:163],v[112:115],v[44:47]
s_add_u32 s62,s50,32768
ds_read_b128 a[140:143],v1 offset:768
v_cvt_pk_bf16_f32 v201,v202,v203
v_mfma_f32_16x16x32_bf16 v[248:251],a[164:167],v[116:119],v[248:251]
s_and_b32 s62,s62,131071
v_exp_f32 v204,v204
v_mfma_f32_16x16x32_bf16 v[248:251],a[168:171],v[120:123],v[248:251]
s_addk_i32 s54,16384
v_exp_f32 v205,v205
v_mfma_f32_16x16x32_bf16 v[248:251],a[172:175],v[124:127],v[248:251]
s_sub_u32 s48,s62,s50
ds_read_b128 a[156:159],v1 offset:4864
v_cvt_pk_bf16_f32 v202,v204,v205
v_mfma_f32_16x16x32_bf16 v[252:255],a[188:191],v[124:127],v[44:47]
s_sub_u32 s53,s53,1
v_exp_f32 v206,v206
v_mfma_f32_16x16x32_bf16 v[252:255],a[184:187],v[120:123],v[252:255]
s_and_b32 s46,s54,0xfffc0000
v_exp_f32 v207,v207
v_mfma_f32_16x16x32_bf16 v[252:255],a[180:183],v[116:119],v[252:255]
s_mov_b32 s50,s62
ds_read_b128 a[152:155],v1 offset:4608
v_cvt_pk_bf16_f32 v203,v206,v207
v_mfma_f32_16x16x32_bf16 v[252:255],a[176:179],v[112:115],v[252:255]
s_xor_b32 s49,s50,65536
v_exp_f32 v216,v216
v_mfma_f32_16x16x32_bf16 a[32:35],a[192:195],v[168:171],a[32:35]
ds_read_b128 a[148:151],v1 offset:4352
v_mfma_f32_16x16x32_bf16 a[32:35],a[196:199],v[172:175],a[32:35]
v_exp_f32 v217,v217
v_mfma_f32_16x16x32_bf16 a[36:39],a[204:207],v[172:175],a[36:39]
ds_read_b128 a[144:147],v1 offset:4096
v_cvt_pk_bf16_f32 v204,v216,v217
v_mfma_f32_16x16x32_bf16 a[36:39],a[200:203],v[168:171],a[36:39]
v_exp_f32 v218,v218
v_mfma_f32_16x16x32_bf16 a[40:43],a[208:211],v[168:171],a[40:43]
buffer_load_dwordx4 v4,s[20:23],s47 offen offset:1024 nt lds
v_mfma_f32_16x16x32_bf16 a[40:43],a[212:215],v[172:175],a[40:43]
ds_read_b128 a[160:163],v1 offset:8192
v_mfma_f32_16x16x32_bf16 a[44:47],a[220:223],v[172:175],a[44:47]
v_exp_f32 v219,v219
v_mfma_f32_16x16x32_bf16 a[44:47],a[216:219],v[168:171],a[44:47]
ds_read_b128 a[164:167],v1 offset:8448
v_cvt_pk_bf16_f32 v205,v218,v219
v_mfma_f32_16x16x32_bf16 a[48:51],a[224:227],v[168:171],a[48:51]
v_exp_f32 v220,v220
v_mfma_f32_16x16x32_bf16 a[48:51],a[228:231],v[172:175],a[48:51]
ds_read_b128 a[168:171],v1 offset:8704
v_mfma_f32_16x16x32_bf16 a[52:55],a[236:239],v[172:175],a[52:55]
v_exp_f32 v221,v221
v_mfma_f32_16x16x32_bf16 a[52:55],a[232:235],v[168:171],a[52:55]
ds_read_b128 a[172:175],v1 offset:8960
v_cvt_pk_bf16_f32 v206,v220,v221
v_mfma_f32_16x16x32_bf16 a[56:59],a[240:243],v[168:171],a[56:59]
v_exp_f32 v222,v222
v_mfma_f32_16x16x32_bf16 a[56:59],a[244:247],v[172:175],a[56:59]
ds_read_b128 a[188:191],v1 offset:13056
v_mfma_f32_16x16x32_bf16 a[60:63],a[252:255],v[172:175],a[60:63]
v_exp_f32 v223,v223
v_mfma_f32_16x16x32_bf16 a[60:63],a[248:251],v[168:171],a[60:63]
ds_read_b128 a[184:187],v1 offset:12800
v_cvt_pk_bf16_f32 v207,v222,v223
v_smfmac_f32_16x16x64_bf16 v[52:55],v[28:31],v[168:175],v27
ds_read_b128 a[180:183],v1 offset:12544
buffer_load_dwordx4 v5,s[20:23],s47 offen offset:2048 nt lds
ds_read_b128 a[176:179],v1 offset:12288
v_add_u32 v1,s48,v1
v_exp_f32 v232,v232
v_exp_f32 v233,v233
v_exp_f32 v234,v234
v_exp_f32 v235,v235
v_exp_f32 v236,v236
v_exp_f32 v237,v237
v_exp_f32 v238,v238
v_exp_f32 v239,v239
v_exp_f32 v248,v248
v_exp_f32 v249,v249
v_exp_f32 v250,v250
v_exp_f32 v251,v251
buffer_load_dwordx4 v5,s[20:23],s47 offen offset:3072 nt lds
v_mfma_f32_16x16x32_bf16 a[64:67],a[192:195],v[200:203],a[64:67]
v_exp_f32 v252,v252
v_mfma_f32_16x16x32_bf16 a[64:67],a[196:199],v[204:207],a[64:67]
v_exp_f32 v253,v253
v_mfma_f32_16x16x32_bf16 a[68:71],a[204:207],v[204:207],a[68:71]
v_exp_f32 v254,v254
v_mfma_f32_16x16x32_bf16 a[68:71],a[200:203],v[200:203],a[68:71]
v_exp_f32 v255,v255
v_mfma_f32_16x16x32_bf16 a[72:75],a[208:211],v[200:203],a[72:75]
v_mfma_f32_16x16x32_bf16 a[72:75],a[212:215],v[204:207],a[72:75]
v_mfma_f32_16x16x32_bf16 a[76:79],a[220:223],v[204:207],a[76:79]
v_mfma_f32_16x16x32_bf16 a[76:79],a[216:219],v[200:203],a[76:79]
v_mfma_f32_16x16x32_bf16 a[80:83],a[224:227],v[200:203],a[80:83]
v_mfma_f32_16x16x32_bf16 a[80:83],a[228:231],v[204:207],a[80:83]
v_mfma_f32_16x16x32_bf16 a[84:87],a[236:239],v[204:207],a[84:87]
s_addk_i32 m0,16384
v_mfma_f32_16x16x32_bf16 a[84:87],a[232:235],v[200:203],a[84:87]
v_mfma_f32_16x16x32_bf16 a[88:91],a[240:243],v[200:203],a[88:91]
v_mfma_f32_16x16x32_bf16 a[88:91],a[244:247],v[204:207],a[88:91]
buffer_load_dwordx4 v4,s[24:27],s47 offen nt lds
v_mfma_f32_16x16x32_bf16 a[92:95],a[252:255],v[204:207],a[92:95]
v_cvt_pk_bf16_f32 v232,v232,v233
v_cvt_pk_bf16_f32 v233,v234,v235
v_mfma_f32_16x16x32_bf16 a[92:95],a[248:251],v[200:203],a[92:95]
v_cvt_pk_bf16_f32 v234,v236,v237
v_cvt_pk_bf16_f32 v235,v238,v239
v_smfmac_f32_16x16x64_bf16 v[56:59],v[28:31],v[200:207],v27
s_waitcnt lgkmcnt(0)
v_cvt_pk_bf16_f32 v236,v248,v249
v_mfma_f32_16x16x32_bf16 v[168:171],a[128:131],v[80:83],v[36:39]
v_cvt_pk_bf16_f32 v237,v250,v251
v_cvt_pk_bf16_f32 v238,v252,v253
v_mfma_f32_16x16x32_bf16 v[168:171],a[132:135],v[84:87],v[168:171]
v_cvt_pk_bf16_f32 v239,v254,v255
v_mfma_f32_16x16x32_bf16 v[168:171],a[136:139],v[88:91],v[168:171]
v_mfma_f32_16x16x32_bf16 v[168:171],a[140:143],v[92:95],v[168:171]
v_mfma_f32_16x16x32_bf16 v[172:175],a[156:159],v[92:95],v[36:39]
v_mfma_f32_16x16x32_bf16 v[172:175],a[152:155],v[88:91],v[172:175]
v_mfma_f32_16x16x32_bf16 v[172:175],a[148:151],v[84:87],v[172:175]
v_mfma_f32_16x16x32_bf16 v[172:175],a[144:147],v[80:83],v[172:175]
v_mfma_f32_16x16x32_bf16 v[184:187],a[160:163],v[80:83],v[36:39]
v_mfma_f32_16x16x32_bf16 v[184:187],a[164:167],v[84:87],v[184:187]
s_nop 1
v_exp_f32 v168,v168
v_mfma_f32_16x16x32_bf16 v[184:187],a[168:171],v[88:91],v[184:187]
buffer_load_dwordx4 v5,s[24:27],s47 offen offset:1024 nt lds
v_mfma_f32_16x16x32_bf16 v[184:187],a[172:175],v[92:95],v[184:187]
v_exp_f32 v169,v169
v_mfma_f32_16x16x32_bf16 v[188:191],a[188:191],v[92:95],v[36:39]
v_exp_f32 v170,v170
v_mfma_f32_16x16x32_bf16 v[188:191],a[184:187],v[88:91],v[188:191]
v_exp_f32 v171,v171
v_mfma_f32_16x16x32_bf16 v[188:191],a[180:183],v[84:87],v[188:191]
v_exp_f32 v172,v172
v_mfma_f32_16x16x32_bf16 v[188:191],a[176:179],v[80:83],v[188:191]
v_exp_f32 v173,v173
v_mfma_f32_16x16x32_bf16 a[96:99],a[192:195],v[232:235],a[96:99]
ds_read_b64_tr_b16 a[192:193],v2
v_cvt_pk_bf16_f32 v168,v168,v169
v_mfma_f32_16x16x32_bf16 a[96:99],a[196:199],v[236:239],a[96:99]
ds_read_b64_tr_b16 a[194:195],v2 offset:4096
v_cvt_pk_bf16_f32 v169,v170,v171
v_mfma_f32_16x16x32_bf16 a[100:103],a[204:207],v[236:239],a[100:103]
ds_read_b64_tr_b16 a[196:197],v2 offset:8192
v_cvt_pk_bf16_f32 v170,v172,v173
v_mfma_f32_16x16x32_bf16 a[100:103],a[200:203],v[232:235],a[100:103]
v_exp_f32 v174,v174
v_mfma_f32_16x16x32_bf16 a[104:107],a[208:211],v[232:235],a[104:107]
v_exp_f32 v175,v175
v_mfma_f32_16x16x32_bf16 a[104:107],a[212:215],v[236:239],a[104:107]
ds_read_b64_tr_b16 a[198:199],v2 offset:12288
v_cvt_pk_bf16_f32 v171,v174,v175
v_mfma_f32_16x16x32_bf16 a[108:111],a[220:223],v[236:239],a[108:111]
v_exp_f32 v184,v184
v_mfma_f32_16x16x32_bf16 a[108:111],a[216:219],v[232:235],a[108:111]
v_exp_f32 v185,v185
v_mfma_f32_16x16x32_bf16 a[112:115],a[224:227],v[232:235],a[112:115]
buffer_load_dwordx4 v4,s[24:27],s47 offen offset:2048 nt lds
v_mfma_f32_16x16x32_bf16 a[112:115],a[228:231],v[236:239],a[112:115]
ds_read_b64_tr_b16 a[204:205],v3 offset:8192
v_cvt_pk_bf16_f32 v172,v184,v185
v_mfma_f32_16x16x32_bf16 a[116:119],a[236:239],v[236:239],a[116:119]
ds_read_b64_tr_b16 a[206:207],v3 offset:12288
v_mfma_f32_16x16x32_bf16 a[116:119],a[232:235],v[232:235],a[116:119]
v_exp_f32 v186,v186
v_mfma_f32_16x16x32_bf16 a[120:123],a[240:243],v[232:235],a[120:123]
ds_read_b64_tr_b16 a[200:201],v3
v_mfma_f32_16x16x32_bf16 a[120:123],a[244:247],v[236:239],a[120:123]
ds_read_b64_tr_b16 a[202:203],v3 offset:4096
v_mfma_f32_16x16x32_bf16 a[124:127],a[252:255],v[236:239],a[124:127]
ds_read_b64_tr_b16 a[208:209],v2 offset:256
v_mfma_f32_16x16x32_bf16 a[124:127],a[248:251],v[232:235],a[124:127]
ds_read_b64_tr_b16 a[210:211],v2 offset:4352
v_smfmac_f32_16x16x64_bf16 v[60:63],v[28:31],v[232:239],v27
ds_read_b64_tr_b16 a[212:213],v2 offset:8448
v_mfma_f32_16x16x32_bf16 v[200:203],a[128:131],v[96:99],v[40:43]
ds_read_b64_tr_b16 a[214:215],v2 offset:12544
v_mfma_f32_16x16x32_bf16 v[200:203],a[132:135],v[100:103],v[200:203]
ds_read_b64_tr_b16 a[220:221],v3 offset:8448
v_mfma_f32_16x16x32_bf16 v[200:203],a[136:139],v[104:107],v[200:203]
ds_read_b64_tr_b16 a[222:223],v3 offset:12544
v_mfma_f32_16x16x32_bf16 v[200:203],a[140:143],v[108:111],v[200:203]
ds_read_b64_tr_b16 a[216:217],v3 offset:256
v_mfma_f32_16x16x32_bf16 v[204:207],a[156:159],v[108:111],v[40:43]
ds_read_b64_tr_b16 a[218:219],v3 offset:4352
v_mfma_f32_16x16x32_bf16 v[204:207],a[152:155],v[104:107],v[204:207]
buffer_load_dwordx4 v5,s[24:27],s47 offen offset:3072 nt lds
v_mfma_f32_16x16x32_bf16 v[204:207],a[148:151],v[100:103],v[204:207]
s_or_b32 s47,s47,0x18000
ds_read_b64_tr_b16 a[224:225],v2 offset:512
v_mfma_f32_16x16x32_bf16 v[204:207],a[144:147],v[96:99],v[204:207]
s_addk_i32 s47,4096
s_and_b32 s47,s47,0x27000
ds_read_b64_tr_b16 a[226:227],v2 offset:4608
v_mfma_f32_16x16x32_bf16 v[216:219],a[160:163],v[96:99],v[40:43]
s_or_b32 s47,s47,s46
ds_read_b64_tr_b16 a[228:229],v2 offset:8704
v_mfma_f32_16x16x32_bf16 v[216:219],a[164:167],v[100:103],v[216:219]
ds_read_b64_tr_b16 a[230:231],v2 offset:12800
v_mfma_f32_16x16x32_bf16 v[216:219],a[168:171],v[104:107],v[216:219]
ds_read_b64_tr_b16 a[236:237],v3 offset:8704
v_mfma_f32_16x16x32_bf16 v[216:219],a[172:175],v[108:111],v[216:219]
ds_read_b64_tr_b16 a[238:239],v3 offset:12800
v_mfma_f32_16x16x32_bf16 v[220:223],a[188:191],v[108:111],v[40:43]
ds_read_b64_tr_b16 a[232:233],v3 offset:512
v_mfma_f32_16x16x32_bf16 v[220:223],a[184:187],v[104:107],v[220:223]
ds_read_b64_tr_b16 a[234:235],v3 offset:4608
v_mfma_f32_16x16x32_bf16 v[220:223],a[180:183],v[100:103],v[220:223]
ds_read_b64_tr_b16 a[240:241],v2 offset:768
v_mfma_f32_16x16x32_bf16 v[220:223],a[176:179],v[96:99],v[220:223]
buffer_load_dword v26,v24,s[88:91],s93 offen
ds_read_b64_tr_b16 a[242:243],v2 offset:4864
ds_read_b64_tr_b16 a[244:245],v2 offset:8960
ds_read_b64_tr_b16 a[246:247],v2 offset:13056
ds_read_b64_tr_b16 a[252:253],v3 offset:8960
ds_read_b64_tr_b16 a[254:255],v3 offset:13056
ds_read_b64_tr_b16 a[248:249],v3 offset:768
ds_read_b64_tr_b16 a[250:251],v3 offset:4864
s_bitcmp1_b32 s45,0
s_cbranch_scc0 L_t31v00_%=
s_mov_b32 m0,s80
s_addk_i32 s80,4096
buffer_load_dwordx4 v25,s[20:23],s83 offen nt lds
s_addk_i32 s83,1024
L_t31v00_%=:
v_exp_f32 v187,v187
v_exp_f32 v188,v188
v_exp_f32 v189,v189
v_exp_f32 v190,v190
v_exp_f32 v191,v191
.p2align 3
v_cvt_pk_bf16_f32 v173,v186,v187
v_cvt_pk_bf16_f32 v174,v188,v189
v_cvt_pk_bf16_f32 v175,v190,v191
s_cmp_lg_u32 s53,0
s_cbranch_scc1 L_att0v0_%=
s_waitcnt lgkmcnt(0)
s_nop 7
s_branch L_att0x_%=
.p2align 5
L_att0v1_%=:
v_mfma_f32_16x16x32_bf16 v[232:235],a[128:131],v[112:115],v[44:47]
s_waitcnt vmcnt(8) lgkmcnt(0)
v_exp_f32 v200,v200
v_mfma_f32_16x16x32_bf16 v[232:235],a[132:135],v[116:119],v[232:235]
)A"
R"A(s_barrier
ds_read_b128 a[128:131],v1
v_add_u32 v2,s48,v2
v_mfma_f32_16x16x32_bf16 v[232:235],a[136:139],v[120:123],v[232:235]
s_mov_b32 s90,L_endseg_%=-L_pc_%=+128
v_exp_f32 v201,v201
v_mfma_f32_16x16x32_bf16 v[232:235],a[140:143],v[124:127],v[232:235]
s_add_u32 m0,s49,s51
ds_read_b128 a[132:135],v1 offset:256
v_cvt_pk_bf16_f32 v200,v200,v201
v_mfma_f32_16x16x32_bf16 v[236:239],a[156:159],v[124:127],v[44:47]
s_lshr_b32 s45,s45,1
v_exp_f32 v202,v202
v_mfma_f32_16x16x32_bf16 v[236:239],a[152:155],v[120:123],v[236:239]
ds_read_b128 a[136:139],v1 offset:512
v_add_u32 v3,s48,v3
v_mfma_f32_16x16x32_bf16 v[236:239],a[148:151],v[116:119],v[236:239]
buffer_load_dwordx4 v4,s[20:23],s47 offen nt lds
v_mfma_f32_16x16x32_bf16 v[236:239],a[144:147],v[112:115],v[236:239]
v_exp_f32 v203,v203
v_mfma_f32_16x16x32_bf16 v[248:251],a[160:163],v[112:115],v[44:47]
s_add_u32 s62,s50,32768
ds_read_b128 a[140:143],v1 offset:768
v_cvt_pk_bf16_f32 v201,v202,v203
v_mfma_f32_16x16x32_bf16 v[248:251],a[164:167],v[116:119],v[248:251]
s_and_b32 s62,s62,131071
v_exp_f32 v204,v204
v_mfma_f32_16x16x32_bf16 v[248:251],a[168:171],v[120:123],v[248:251]
s_addk_i32 s54,16384
v_exp_f32 v205,v205
v_mfma_f32_16x16x32_bf16 v[248:251],a[172:175],v[124:127],v[248:251]
s_sub_u32 s48,s62,s50
ds_read_b128 a[156:159],v1 offset:4864
v_cvt_pk_bf16_f32 v202,v204,v205
v_mfma_f32_16x16x32_bf16 v[252:255],a[188:191],v[124:127],v[44:47]
s_sub_u32 s53,s53,1
v_exp_f32 v206,v206
v_mfma_f32_16x16x32_bf16 v[252:255],a[184:187],v[120:123],v[252:255]
s_and_b32 s46,s54,0xfffc0000
v_exp_f32 v207,v207
v_mfma_f32_16x16x32_bf16 v[252:255],a[180:183],v[116:119],v[252:255]
s_mov_b32 s50,s62
ds_read_b128 a[152:155],v1 offset:4608
v_cvt_pk_bf16_f32 v203,v206,v207
v_mfma_f32_16x16x32_bf16 v[252:255],a[176:179],v[112:115],v[252:255]
s_xor_b32 s49,s50,65536
v_exp_f32 v216,v216
ds_read_b128 a[148:151],v1 offset:4352
v_exp_f32 v217,v217
ds_read_b128 a[144:147],v1 offset:4096
v_cvt_pk_bf16_f32 v204,v216,v217
v_exp_f32 v218,v218
buffer_load_dwordx4 v4,s[20:23],s47 offen offset:1024 nt lds
ds_read_b128 a[160:163],v1 offset:8192
v_exp_f32 v219,v219
ds_read_b128 a[164:167],v1 offset:8448
v_cvt_pk_bf16_f32 v205,v218,v219
v_exp_f32 v220,v220
ds_read_b128 a[168:171],v1 offset:8704
v_exp_f32 v221,v221
ds_read_b128 a[172:175],v1 offset:8960
v_cvt_pk_bf16_f32 v206,v220,v221
v_exp_f32 v222,v222
ds_read_b128 a[188:191],v1 offset:13056
v_exp_f32 v223,v223
ds_read_b128 a[184:187],v1 offset:12800
v_cvt_pk_bf16_f32 v207,v222,v223
s_waitcnt lgkmcnt(11)
v_mfma_f32_16x16x32_bf16 v[136:139],a[128:131],v[64:67],v[32:35]
ds_read_b128 a[180:183],v1 offset:12544
v_mfma_f32_16x16x32_bf16 v[136:139],a[132:135],v[68:71],v[136:139]
buffer_load_dwordx4 v5,s[20:23],s47 offen offset:2048 nt lds
v_mfma_f32_16x16x32_bf16 v[136:139],a[136:139],v[72:75],v[136:139]
s_waitcnt lgkmcnt(11)
ds_read_b128 a[176:179],v1 offset:12288
v_add_u32 v1,s48,v1
v_mfma_f32_16x16x32_bf16 v[136:139],a[140:143],v[76:79],v[136:139]
s_waitcnt lgkmcnt(11)
v_exp_f32 v232,v232
v_mfma_f32_16x16x32_bf16 v[140:143],a[156:159],v[76:79],v[32:35]
s_waitcnt lgkmcnt(10)
v_exp_f32 v233,v233
v_mfma_f32_16x16x32_bf16 v[140:143],a[152:155],v[72:75],v[140:143]
s_waitcnt lgkmcnt(9)
v_exp_f32 v234,v234
v_mfma_f32_16x16x32_bf16 v[140:143],a[148:151],v[68:71],v[140:143]
s_waitcnt lgkmcnt(8)
v_exp_f32 v235,v235
v_mfma_f32_16x16x32_bf16 v[140:143],a[144:147],v[64:67],v[140:143]
s_waitcnt lgkmcnt(2)
v_exp_f32 v236,v236
v_mfma_f32_16x16x32_bf16 v[152:155],a[160:163],v[64:67],v[32:35]
v_exp_f32 v237,v237
v_mfma_f32_16x16x32_bf16 v[152:155],a[164:167],v[68:71],v[152:155]
v_exp_f32 v238,v238
v_mfma_f32_16x16x32_bf16 v[152:155],a[168:171],v[72:75],v[152:155]
v_exp_f32 v239,v239
v_mfma_f32_16x16x32_bf16 v[152:155],a[172:175],v[76:79],v[152:155]
v_exp_f32 v248,v248
v_mfma_f32_16x16x32_bf16 v[156:159],a[188:191],v[76:79],v[32:35]
v_exp_f32 v249,v249
v_mfma_f32_16x16x32_bf16 v[156:159],a[184:187],v[72:75],v[156:159]
s_waitcnt lgkmcnt(0)
v_exp_f32 v250,v250
v_mfma_f32_16x16x32_bf16 v[156:159],a[180:183],v[68:71],v[156:159]
v_exp_f32 v251,v251
v_mfma_f32_16x16x32_bf16 v[156:159],a[176:179],v[64:67],v[156:159]
buffer_load_dwordx4 v5,s[20:23],s47 offen offset:3072 nt lds
v_mfma_f32_16x16x32_bf16 a[64:67],a[192:195],v[200:203],a[64:67]
v_exp_f32 v252,v252
v_mfma_f32_16x16x32_bf16 a[64:67],a[196:199],v[204:207],a[64:67]
v_exp_f32 v253,v253
v_mfma_f32_16x16x32_bf16 a[68:71],a[204:207],v[204:207],a[68:71]
v_exp_f32 v254,v254
v_mfma_f32_16x16x32_bf16 a[68:71],a[200:203],v[200:203],a[68:71]
v_exp_f32 v255,v255
v_mfma_f32_16x16x32_bf16 a[72:75],a[208:211],v[200:203],a[72:75]
v_exp_f32 v136,v136
v_mfma_f32_16x16x32_bf16 a[72:75],a[212:215],v[204:207],a[72:75]
v_exp_f32 v137,v137
v_mfma_f32_16x16x32_bf16 a[76:79],a[220:223],v[204:207],a[76:79]
v_exp_f32 v138,v138
v_mfma_f32_16x16x32_bf16 a[76:79],a[216:219],v[200:203],a[76:79]
v_exp_f32 v139,v139
v_mfma_f32_16x16x32_bf16 a[80:83],a[224:227],v[200:203],a[80:83]
v_exp_f32 v140,v140
v_mfma_f32_16x16x32_bf16 a[80:83],a[228:231],v[204:207],a[80:83]
v_exp_f32 v141,v141
v_mfma_f32_16x16x32_bf16 a[84:87],a[236:239],v[204:207],a[84:87]
s_addk_i32 m0,16384
v_exp_f32 v142,v142
v_mfma_f32_16x16x32_bf16 a[84:87],a[232:235],v[200:203],a[84:87]
v_exp_f32 v143,v143
v_mfma_f32_16x16x32_bf16 a[88:91],a[240:243],v[200:203],a[88:91]
v_exp_f32 v152,v152
v_mfma_f32_16x16x32_bf16 a[88:91],a[244:247],v[204:207],a[88:91]
buffer_load_dwordx4 v4,s[24:27],s47 offen nt lds
v_mfma_f32_16x16x32_bf16 a[92:95],a[252:255],v[204:207],a[92:95]
v_cvt_pk_bf16_f32 v232,v232,v233
v_cvt_pk_bf16_f32 v233,v234,v235
v_mfma_f32_16x16x32_bf16 a[92:95],a[248:251],v[200:203],a[92:95]
v_cvt_pk_bf16_f32 v234,v236,v237
v_cvt_pk_bf16_f32 v235,v238,v239
v_smfmac_f32_16x16x64_bf16 v[56:59],v[28:31],v[200:207],v27
.p2align 3
v_cvt_pk_bf16_f32 v236,v248,v249
v_cvt_pk_bf16_f32 v237,v250,v251
v_cvt_pk_bf16_f32 v238,v252,v253
v_cvt_pk_bf16_f32 v239,v254,v255
v_exp_f32 v153,v153
v_exp_f32 v154,v154
v_exp_f32 v155,v155
v_exp_f32 v156,v156
v_exp_f32 v157,v157
v_exp_f32 v158,v158
v_exp_f32 v159,v159
buffer_load_dwordx4 v5,s[24:27],s47 offen offset:1024 nt lds
v_mfma_f32_16x16x32_bf16 a[96:99],a[192:195],v[232:235],a[96:99]
ds_read_b64_tr_b16 a[192:193],v2
v_mfma_f32_16x16x32_bf16 a[96:99],a[196:199],v[236:239],a[96:99]
ds_read_b64_tr_b16 a[194:195],v2 offset:4096
v_mfma_f32_16x16x32_bf16 a[100:103],a[204:207],v[236:239],a[100:103]
ds_read_b64_tr_b16 a[196:197],v2 offset:8192
v_mfma_f32_16x16x32_bf16 a[100:103],a[200:203],v[232:235],a[100:103]
v_mfma_f32_16x16x32_bf16 a[104:107],a[208:211],v[232:235],a[104:107]
v_mfma_f32_16x16x32_bf16 a[104:107],a[212:215],v[236:239],a[104:107]
ds_read_b64_tr_b16 a[198:199],v2 offset:12288
v_mfma_f32_16x16x32_bf16 a[108:111],a[220:223],v[236:239],a[108:111]
v_mfma_f32_16x16x32_bf16 a[108:111],a[216:219],v[232:235],a[108:111]
v_mfma_f32_16x16x32_bf16 a[112:115],a[224:227],v[232:235],a[112:115]
buffer_load_dwordx4 v4,s[24:27],s47 offen offset:2048 nt lds
v_mfma_f32_16x16x32_bf16 a[112:115],a[228:231],v[236:239],a[112:115]
ds_read_b64_tr_b16 a[204:205],v3 offset:8192
v_mfma_f32_16x16x32_bf16 a[116:119],a[236:239],v[236:239],a[116:119]
ds_read_b64_tr_b16 a[206:207],v3 offset:12288
v_cvt_pk_bf16_f32 v136,v136,v137
v_mfma_f32_16x16x32_bf16 a[116:119],a[232:235],v[232:235],a[116:119]
v_mfma_f32_16x16x32_bf16 a[120:123],a[240:243],v[232:235],a[120:123]
ds_read_b64_tr_b16 a[200:201],v3
v_mfma_f32_16x16x32_bf16 a[120:123],a[244:247],v[236:239],a[120:123]
ds_read_b64_tr_b16 a[202:203],v3 offset:4096
v_cvt_pk_bf16_f32 v137,v138,v139
v_mfma_f32_16x16x32_bf16 a[124:127],a[252:255],v[236:239],a[124:127]
ds_read_b64_tr_b16 a[208:209],v2 offset:256
v_mfma_f32_16x16x32_bf16 a[124:127],a[248:251],v[232:235],a[124:127]
ds_read_b64_tr_b16 a[210:211],v2 offset:4352
v_smfmac_f32_16x16x64_bf16 v[60:63],v[28:31],v[232:239],v27
ds_read_b64_tr_b16 a[212:213],v2 offset:8448
v_mfma_f32_16x16x32_bf16 v[200:203],a[128:131],v[96:99],v[40:43]
ds_read_b64_tr_b16 a[214:215],v2 offset:12544
v_cvt_pk_bf16_f32 v138,v140,v141
v_mfma_f32_16x16x32_bf16 v[200:203],a[132:135],v[100:103],v[200:203]
ds_read_b64_tr_b16 a[220:221],v3 offset:8448
v_mfma_f32_16x16x32_bf16 v[200:203],a[136:139],v[104:107],v[200:203]
ds_read_b64_tr_b16 a[222:223],v3 offset:12544
v_cvt_pk_bf16_f32 v139,v142,v143
v_mfma_f32_16x16x32_bf16 v[200:203],a[140:143],v[108:111],v[200:203]
ds_read_b64_tr_b16 a[216:217],v3 offset:256
v_mfma_f32_16x16x32_bf16 v[204:207],a[156:159],v[108:111],v[40:43]
ds_read_b64_tr_b16 a[218:219],v3 offset:4352
v_mfma_f32_16x16x32_bf16 v[204:207],a[152:155],v[104:107],v[204:207]
buffer_load_dwordx4 v5,s[24:27],s47 offen offset:3072 nt lds
v_mfma_f32_16x16x32_bf16 v[204:207],a[148:151],v[100:103],v[204:207]
s_or_b32 s47,s47,0x18000
ds_read_b64_tr_b16 a[224:225],v2 offset:512
v_cvt_pk_bf16_f32 v140,v152,v153
v_mfma_f32_16x16x32_bf16 v[204:207],a[144:147],v[96:99],v[204:207]
s_addk_i32 s47,4096
s_and_b32 s47,s47,0x27000
ds_read_b64_tr_b16 a[226:227],v2 offset:4608
v_mfma_f32_16x16x32_bf16 v[216:219],a[160:163],v[96:99],v[40:43]
s_or_b32 s47,s47,s46
ds_read_b64_tr_b16 a[228:229],v2 offset:8704
v_cvt_pk_bf16_f32 v141,v154,v155
v_mfma_f32_16x16x32_bf16 v[216:219],a[164:167],v[100:103],v[216:219]
ds_read_b64_tr_b16 a[230:231],v2 offset:12800
v_mfma_f32_16x16x32_bf16 v[216:219],a[168:171],v[104:107],v[216:219]
ds_read_b64_tr_b16 a[236:237],v3 offset:8704
v_mfma_f32_16x16x32_bf16 v[216:219],a[172:175],v[108:111],v[216:219]
ds_read_b64_tr_b16 a[238:239],v3 offset:12800
v_cvt_pk_bf16_f32 v142,v156,v157
v_mfma_f32_16x16x32_bf16 v[220:223],a[188:191],v[108:111],v[40:43]
ds_read_b64_tr_b16 a[232:233],v3 offset:512
v_mfma_f32_16x16x32_bf16 v[220:223],a[184:187],v[104:107],v[220:223]
ds_read_b64_tr_b16 a[234:235],v3 offset:4608
v_mfma_f32_16x16x32_bf16 v[220:223],a[180:183],v[100:103],v[220:223]
ds_read_b64_tr_b16 a[240:241],v2 offset:768
v_cvt_pk_bf16_f32 v143,v158,v159
v_mfma_f32_16x16x32_bf16 v[220:223],a[176:179],v[96:99],v[220:223]
s_waitcnt lgkmcnt(7)
buffer_load_dword v26,v24,s[88:91],s93 offen
v_mfma_f32_16x16x32_bf16 a[0:3],a[192:195],v[136:139],a[0:3]
ds_read_b64_tr_b16 a[242:243],v2 offset:4864
v_mfma_f32_16x16x32_bf16 a[0:3],a[196:199],v[140:143],a[0:3]
ds_read_b64_tr_b16 a[244:245],v2 offset:8960
v_mfma_f32_16x16x32_bf16 a[4:7],a[204:207],v[140:143],a[4:7]
ds_read_b64_tr_b16 a[246:247],v2 offset:13056
v_mfma_f32_16x16x32_bf16 a[4:7],a[200:203],v[136:139],a[4:7]
ds_read_b64_tr_b16 a[252:253],v3 offset:8960
v_mfma_f32_16x16x32_bf16 a[8:11],a[208:211],v[136:139],a[8:11]
ds_read_b64_tr_b16 a[254:255],v3 offset:13056
v_mfma_f32_16x16x32_bf16 a[8:11],a[212:215],v[140:143],a[8:11]
ds_read_b64_tr_b16 a[248:249],v3 offset:768
v_mfma_f32_16x16x32_bf16 a[12:15],a[220:223],v[140:143],a[12:15]
ds_read_b64_tr_b16 a[250:251],v3 offset:4864
v_mfma_f32_16x16x32_bf16 a[12:15],a[216:219],v[136:139],a[12:15]
s_bitcmp1_b32 s45,0
s_cbranch_scc0 L_t31v10_%=
s_mov_b32 m0,s80
s_addk_i32 s80,4096
buffer_load_dwordx4 v25,s[20:23],s83 offen nt lds
s_addk_i32 s83,1024
L_t31v10_%=:
v_mfma_f32_16x16x32_bf16 a[16:19],a[224:227],v[136:139],a[16:19]
s_waitcnt lgkmcnt(6)
v_mfma_f32_16x16x32_bf16 a[16:19],a[228:231],v[140:143],a[16:19]
v_mfma_f32_16x16x32_bf16 a[20:23],a[236:239],v[140:143],a[20:23]
v_mfma_f32_16x16x32_bf16 a[20:23],a[232:235],v[136:139],a[20:23]
v_mfma_f32_16x16x32_bf16 a[24:27],a[240:243],v[136:139],a[24:27]
s_waitcnt lgkmcnt(2)
v_mfma_f32_16x16x32_bf16 a[24:27],a[244:247],v[140:143],a[24:27]
v_mfma_f32_16x16x32_bf16 a[28:31],a[252:255],v[140:143],a[28:31]
s_waitcnt lgkmcnt(0)
v_mfma_f32_16x16x32_bf16 a[28:31],a[248:251],v[136:139],a[28:31]
s_cmp_lg_u32 s53,0
v_smfmac_f32_16x16x64_bf16 v[48:51],v[28:31],v[136:143],v27
s_cbranch_scc1 L_att0v1_%=
s_waitcnt lgkmcnt(0)
s_nop 7
s_branch L_att0x_%=
.p2align 5
L_att0v2_%=:
v_mfma_f32_16x16x32_bf16 v[232:235],a[128:131],v[112:115],v[44:47]
s_waitcnt vmcnt(8) lgkmcnt(0)
v_mfma_f32_16x16x32_bf16 v[232:235],a[132:135],v[116:119],v[232:235]
s_barrier
ds_read_b128 a[128:131],v1
v_add_u32 v2,s48,v2
v_mfma_f32_16x16x32_bf16 v[232:235],a[136:139],v[120:123],v[232:235]
)A"
R"A(s_mov_b32 s90,L_endseg_%=-L_pc_%=+128
v_mfma_f32_16x16x32_bf16 v[232:235],a[140:143],v[124:127],v[232:235]
s_add_u32 m0,s49,s51
ds_read_b128 a[132:135],v1 offset:256
v_mfma_f32_16x16x32_bf16 v[236:239],a[156:159],v[124:127],v[44:47]
s_lshr_b32 s45,s45,1
v_mfma_f32_16x16x32_bf16 v[236:239],a[152:155],v[120:123],v[236:239]
ds_read_b128 a[136:139],v1 offset:512
v_add_u32 v3,s48,v3
v_mfma_f32_16x16x32_bf16 v[236:239],a[148:151],v[116:119],v[236:239]
buffer_load_dwordx4 v4,s[20:23],s47 offen nt lds
v_mfma_f32_16x16x32_bf16 v[236:239],a[144:147],v[112:115],v[236:239]
v_mfma_f32_16x16x32_bf16 v[248:251],a[160:163],v[112:115],v[44:47]
s_add_u32 s62,s50,32768
ds_read_b128 a[140:143],v1 offset:768
v_mfma_f32_16x16x32_bf16 v[248:251],a[164:167],v[116:119],v[248:251]
s_and_b32 s62,s62,131071
v_mfma_f32_16x16x32_bf16 v[248:251],a[168:171],v[120:123],v[248:251]
s_addk_i32 s54,16384
v_mfma_f32_16x16x32_bf16 v[248:251],a[172:175],v[124:127],v[248:251]
s_sub_u32 s48,s62,s50
ds_read_b128 a[156:159],v1 offset:4864
v_mfma_f32_16x16x32_bf16 v[252:255],a[188:191],v[124:127],v[44:47]
s_sub_u32 s53,s53,1
v_mfma_f32_16x16x32_bf16 v[252:255],a[184:187],v[120:123],v[252:255]
s_and_b32 s46,s54,0xfffc0000
v_mfma_f32_16x16x32_bf16 v[252:255],a[180:183],v[116:119],v[252:255]
s_mov_b32 s50,s62
ds_read_b128 a[152:155],v1 offset:4608
v_mfma_f32_16x16x32_bf16 v[252:255],a[176:179],v[112:115],v[252:255]
s_xor_b32 s49,s50,65536
v_mfma_f32_16x16x32_bf16 a[32:35],a[192:195],v[168:171],a[32:35]
ds_read_b128 a[148:151],v1 offset:4352
v_mfma_f32_16x16x32_bf16 a[32:35],a[196:199],v[172:175],a[32:35]
v_mfma_f32_16x16x32_bf16 a[36:39],a[204:207],v[172:175],a[36:39]
ds_read_b128 a[144:147],v1 offset:4096
v_mfma_f32_16x16x32_bf16 a[36:39],a[200:203],v[168:171],a[36:39]
v_mfma_f32_16x16x32_bf16 a[40:43],a[208:211],v[168:171],a[40:43]
buffer_load_dwordx4 v4,s[20:23],s47 offen offset:1024 nt lds
v_mfma_f32_16x16x32_bf16 a[40:43],a[212:215],v[172:175],a[40:43]
ds_read_b128 a[160:163],v1 offset:8192
v_mfma_f32_16x16x32_bf16 a[44:47],a[220:223],v[172:175],a[44:47]
v_mfma_f32_16x16x32_bf16 a[44:47],a[216:219],v[168:171],a[44:47]
ds_read_b128 a[164:167],v1 offset:8448
v_mfma_f32_16x16x32_bf16 a[48:51],a[224:227],v[168:171],a[48:51]
v_mfma_f32_16x16x32_bf16 a[48:51],a[228:231],v[172:175],a[48:51]
ds_read_b128 a[168:171],v1 offset:8704
v_mfma_f32_16x16x32_bf16 a[52:55],a[236:239],v[172:175],a[52:55]
v_mfma_f32_16x16x32_bf16 a[52:55],a[232:235],v[168:171],a[52:55]
ds_read_b128 a[172:175],v1 offset:8960
v_mfma_f32_16x16x32_bf16 a[56:59],a[240:243],v[168:171],a[56:59]
v_mfma_f32_16x16x32_bf16 a[56:59],a[244:247],v[172:175],a[56:59]
ds_read_b128 a[188:191],v1 offset:13056
v_mfma_f32_16x16x32_bf16 a[60:63],a[252:255],v[172:175],a[60:63]
v_mfma_f32_16x16x32_bf16 a[60:63],a[248:251],v[168:171],a[60:63]
ds_read_b128 a[184:187],v1 offset:12800
v_smfmac_f32_16x16x64_bf16 v[52:55],v[28:31],v[168:175],v27
s_waitcnt lgkmcnt(4)
v_mfma_f32_16x16x32_bf16 v[136:139],a[128:131],v[64:67],v[32:35]
ds_read_b128 a[180:183],v1 offset:12544
v_mfma_f32_16x16x32_bf16 v[136:139],a[132:135],v[68:71],v[136:139]
buffer_load_dwordx4 v5,s[20:23],s47 offen offset:2048 nt lds
v_mfma_f32_16x16x32_bf16 v[136:139],a[136:139],v[72:75],v[136:139]
ds_read_b128 a[176:179],v1 offset:12288
v_add_u32 v1,s48,v1
v_mfma_f32_16x16x32_bf16 v[136:139],a[140:143],v[76:79],v[136:139]
v_exp_f32 v232,v232
v_mfma_f32_16x16x32_bf16 v[140:143],a[156:159],v[76:79],v[32:35]
v_exp_f32 v233,v233
v_mfma_f32_16x16x32_bf16 v[140:143],a[152:155],v[72:75],v[140:143]
v_exp_f32 v234,v234
v_mfma_f32_16x16x32_bf16 v[140:143],a[148:151],v[68:71],v[140:143]
v_exp_f32 v235,v235
v_mfma_f32_16x16x32_bf16 v[140:143],a[144:147],v[64:67],v[140:143]
v_exp_f32 v236,v236
v_mfma_f32_16x16x32_bf16 v[152:155],a[160:163],v[64:67],v[32:35]
v_exp_f32 v237,v237
v_mfma_f32_16x16x32_bf16 v[152:155],a[164:167],v[68:71],v[152:155]
s_waitcnt lgkmcnt(1)
v_exp_f32 v238,v238
v_mfma_f32_16x16x32_bf16 v[152:155],a[168:171],v[72:75],v[152:155]
v_exp_f32 v239,v239
v_mfma_f32_16x16x32_bf16 v[152:155],a[172:175],v[76:79],v[152:155]
v_exp_f32 v248,v248
v_mfma_f32_16x16x32_bf16 v[156:159],a[188:191],v[76:79],v[32:35]
v_exp_f32 v249,v249
v_mfma_f32_16x16x32_bf16 v[156:159],a[184:187],v[72:75],v[156:159]
v_exp_f32 v250,v250
v_mfma_f32_16x16x32_bf16 v[156:159],a[180:183],v[68:71],v[156:159]
s_waitcnt lgkmcnt(0)
v_exp_f32 v251,v251
v_mfma_f32_16x16x32_bf16 v[156:159],a[176:179],v[64:67],v[156:159]
buffer_load_dwordx4 v5,s[20:23],s47 offen offset:3072 nt lds
v_exp_f32 v252,v252
v_exp_f32 v253,v253
v_exp_f32 v254,v254
v_exp_f32 v255,v255
v_exp_f32 v136,v136
v_exp_f32 v137,v137
v_exp_f32 v138,v138
v_exp_f32 v139,v139
v_exp_f32 v140,v140
v_exp_f32 v141,v141
s_addk_i32 m0,16384
v_exp_f32 v142,v142
v_exp_f32 v143,v143
v_exp_f32 v152,v152
buffer_load_dwordx4 v4,s[24:27],s47 offen nt lds
.p2align 3
v_cvt_pk_bf16_f32 v232,v232,v233
v_cvt_pk_bf16_f32 v233,v234,v235
v_cvt_pk_bf16_f32 v234,v236,v237
v_cvt_pk_bf16_f32 v235,v238,v239
v_cvt_pk_bf16_f32 v236,v248,v249
v_mfma_f32_16x16x32_bf16 v[168:171],a[128:131],v[80:83],v[36:39]
v_cvt_pk_bf16_f32 v237,v250,v251
v_cvt_pk_bf16_f32 v238,v252,v253
v_mfma_f32_16x16x32_bf16 v[168:171],a[132:135],v[84:87],v[168:171]
v_cvt_pk_bf16_f32 v239,v254,v255
v_mfma_f32_16x16x32_bf16 v[168:171],a[136:139],v[88:91],v[168:171]
v_exp_f32 v153,v153
v_mfma_f32_16x16x32_bf16 v[168:171],a[140:143],v[92:95],v[168:171]
v_exp_f32 v154,v154
v_mfma_f32_16x16x32_bf16 v[172:175],a[156:159],v[92:95],v[36:39]
v_exp_f32 v155,v155
v_mfma_f32_16x16x32_bf16 v[172:175],a[152:155],v[88:91],v[172:175]
v_exp_f32 v156,v156
v_mfma_f32_16x16x32_bf16 v[172:175],a[148:151],v[84:87],v[172:175]
v_exp_f32 v157,v157
v_mfma_f32_16x16x32_bf16 v[172:175],a[144:147],v[80:83],v[172:175]
v_exp_f32 v158,v158
v_mfma_f32_16x16x32_bf16 v[184:187],a[160:163],v[80:83],v[36:39]
v_exp_f32 v159,v159
v_mfma_f32_16x16x32_bf16 v[184:187],a[164:167],v[84:87],v[184:187]
v_exp_f32 v168,v168
v_mfma_f32_16x16x32_bf16 v[184:187],a[168:171],v[88:91],v[184:187]
buffer_load_dwordx4 v5,s[24:27],s47 offen offset:1024 nt lds
v_mfma_f32_16x16x32_bf16 v[184:187],a[172:175],v[92:95],v[184:187]
v_exp_f32 v169,v169
v_mfma_f32_16x16x32_bf16 v[188:191],a[188:191],v[92:95],v[36:39]
v_exp_f32 v170,v170
v_mfma_f32_16x16x32_bf16 v[188:191],a[184:187],v[88:91],v[188:191]
v_exp_f32 v171,v171
v_mfma_f32_16x16x32_bf16 v[188:191],a[180:183],v[84:87],v[188:191]
v_exp_f32 v172,v172
v_mfma_f32_16x16x32_bf16 v[188:191],a[176:179],v[80:83],v[188:191]
v_exp_f32 v173,v173
v_mfma_f32_16x16x32_bf16 a[96:99],a[192:195],v[232:235],a[96:99]
ds_read_b64_tr_b16 a[192:193],v2
v_cvt_pk_bf16_f32 v168,v168,v169
v_mfma_f32_16x16x32_bf16 a[96:99],a[196:199],v[236:239],a[96:99]
ds_read_b64_tr_b16 a[194:195],v2 offset:4096
v_cvt_pk_bf16_f32 v169,v170,v171
v_mfma_f32_16x16x32_bf16 a[100:103],a[204:207],v[236:239],a[100:103]
ds_read_b64_tr_b16 a[196:197],v2 offset:8192
v_cvt_pk_bf16_f32 v170,v172,v173
v_mfma_f32_16x16x32_bf16 a[100:103],a[200:203],v[232:235],a[100:103]
v_exp_f32 v174,v174
v_mfma_f32_16x16x32_bf16 a[104:107],a[208:211],v[232:235],a[104:107]
v_exp_f32 v175,v175
v_mfma_f32_16x16x32_bf16 a[104:107],a[212:215],v[236:239],a[104:107]
ds_read_b64_tr_b16 a[198:199],v2 offset:12288
v_cvt_pk_bf16_f32 v171,v174,v175
v_mfma_f32_16x16x32_bf16 a[108:111],a[220:223],v[236:239],a[108:111]
v_exp_f32 v184,v184
v_mfma_f32_16x16x32_bf16 a[108:111],a[216:219],v[232:235],a[108:111]
v_exp_f32 v185,v185
v_mfma_f32_16x16x32_bf16 a[112:115],a[224:227],v[232:235],a[112:115]
buffer_load_dwordx4 v4,s[24:27],s47 offen offset:2048 nt lds
v_mfma_f32_16x16x32_bf16 a[112:115],a[228:231],v[236:239],a[112:115]
ds_read_b64_tr_b16 a[204:205],v3 offset:8192
v_cvt_pk_bf16_f32 v172,v184,v185
v_mfma_f32_16x16x32_bf16 a[116:119],a[236:239],v[236:239],a[116:119]
ds_read_b64_tr_b16 a[206:207],v3 offset:12288
v_cvt_pk_bf16_f32 v136,v136,v137
v_mfma_f32_16x16x32_bf16 a[116:119],a[232:235],v[232:235],a[116:119]
v_exp_f32 v186,v186
v_mfma_f32_16x16x32_bf16 a[120:123],a[240:243],v[232:235],a[120:123]
ds_read_b64_tr_b16 a[200:201],v3
v_mfma_f32_16x16x32_bf16 a[120:123],a[244:247],v[236:239],a[120:123]
ds_read_b64_tr_b16 a[202:203],v3 offset:4096
v_cvt_pk_bf16_f32 v137,v138,v139
v_mfma_f32_16x16x32_bf16 a[124:127],a[252:255],v[236:239],a[124:127]
ds_read_b64_tr_b16 a[208:209],v2 offset:256
v_mfma_f32_16x16x32_bf16 a[124:127],a[248:251],v[232:235],a[124:127]
ds_read_b64_tr_b16 a[210:211],v2 offset:4352
v_smfmac_f32_16x16x64_bf16 v[60:63],v[28:31],v[232:239],v27
ds_read_b64_tr_b16 a[212:213],v2 offset:8448
ds_read_b64_tr_b16 a[214:215],v2 offset:12544
v_cvt_pk_bf16_f32 v138,v140,v141
ds_read_b64_tr_b16 a[220:221],v3 offset:8448
ds_read_b64_tr_b16 a[222:223],v3 offset:12544
v_cvt_pk_bf16_f32 v139,v142,v143
ds_read_b64_tr_b16 a[216:217],v3 offset:256
ds_read_b64_tr_b16 a[218:219],v3 offset:4352
buffer_load_dwordx4 v5,s[24:27],s47 offen offset:3072 nt lds
s_or_b32 s47,s47,0x18000
ds_read_b64_tr_b16 a[224:225],v2 offset:512
v_cvt_pk_bf16_f32 v140,v152,v153
s_addk_i32 s47,4096
s_and_b32 s47,s47,0x27000
ds_read_b64_tr_b16 a[226:227],v2 offset:4608
s_or_b32 s47,s47,s46
ds_read_b64_tr_b16 a[228:229],v2 offset:8704
v_cvt_pk_bf16_f32 v141,v154,v155
ds_read_b64_tr_b16 a[230:231],v2 offset:12800
ds_read_b64_tr_b16 a[236:237],v3 offset:8704
ds_read_b64_tr_b16 a[238:239],v3 offset:12800
v_cvt_pk_bf16_f32 v142,v156,v157
ds_read_b64_tr_b16 a[232:233],v3 offset:512
ds_read_b64_tr_b16 a[234:235],v3 offset:4608
ds_read_b64_tr_b16 a[240:241],v2 offset:768
v_cvt_pk_bf16_f32 v143,v158,v159
buffer_load_dword v26,v24,s[88:91],s93 offen
s_waitcnt lgkmcnt(15)
v_mfma_f32_16x16x32_bf16 a[0:3],a[192:195],v[136:139],a[0:3]
ds_read_b64_tr_b16 a[242:243],v2 offset:4864
v_mfma_f32_16x16x32_bf16 a[0:3],a[196:199],v[140:143],a[0:3]
ds_read_b64_tr_b16 a[244:245],v2 offset:8960
v_mfma_f32_16x16x32_bf16 a[4:7],a[204:207],v[140:143],a[4:7]
ds_read_b64_tr_b16 a[246:247],v2 offset:13056
v_mfma_f32_16x16x32_bf16 a[4:7],a[200:203],v[136:139],a[4:7]
ds_read_b64_tr_b16 a[252:253],v3 offset:8960
v_mfma_f32_16x16x32_bf16 a[8:11],a[208:211],v[136:139],a[8:11]
s_waitcnt lgkmcnt(15)
ds_read_b64_tr_b16 a[254:255],v3 offset:13056
v_mfma_f32_16x16x32_bf16 a[8:11],a[212:215],v[140:143],a[8:11]
ds_read_b64_tr_b16 a[248:249],v3 offset:768
v_mfma_f32_16x16x32_bf16 a[12:15],a[220:223],v[140:143],a[12:15]
s_waitcnt lgkmcnt(15)
ds_read_b64_tr_b16 a[250:251],v3 offset:4864
v_mfma_f32_16x16x32_bf16 a[12:15],a[216:219],v[136:139],a[12:15]
s_bitcmp1_b32 s45,0
s_cbranch_scc0 L_t31v20_%=
s_mov_b32 m0,s80
s_addk_i32 s80,4096
buffer_load_dwordx4 v25,s[20:23],s83 offen nt lds
s_addk_i32 s83,1024
L_t31v20_%=:
s_waitcnt lgkmcnt(7)
v_mfma_f32_16x16x32_bf16 a[16:19],a[224:227],v[136:139],a[16:19]
v_exp_f32 v187,v187
v_mfma_f32_16x16x32_bf16 a[16:19],a[228:231],v[140:143],a[16:19]
v_exp_f32 v188,v188
v_mfma_f32_16x16x32_bf16 a[20:23],a[236:239],v[140:143],a[20:23]
v_exp_f32 v189,v189
v_mfma_f32_16x16x32_bf16 a[20:23],a[232:235],v[136:139],a[20:23]
s_waitcnt lgkmcnt(3)
v_exp_f32 v190,v190
v_mfma_f32_16x16x32_bf16 a[24:27],a[240:243],v[136:139],a[24:27]
v_exp_f32 v191,v191
v_mfma_f32_16x16x32_bf16 a[24:27],a[244:247],v[140:143],a[24:27]
s_waitcnt lgkmcnt(1)
v_cvt_pk_bf16_f32 v173,v186,v187
v_mfma_f32_16x16x32_bf16 a[28:31],a[252:255],v[140:143],a[28:31]
s_waitcnt lgkmcnt(0)
v_cvt_pk_bf16_f32 v174,v188,v189
v_cvt_pk_bf16_f32 v175,v190,v191
v_mfma_f32_16x16x32_bf16 a[28:31],a[248:251],v[136:139],a[28:31]
s_cmp_lg_u32 s53,0
v_smfmac_f32_16x16x64_bf16 v[48:51],v[28:31],v[136:143],v27
s_cbranch_scc1 L_att0v2_%=
s_waitcnt lgkmcnt(0)
s_nop 7
s_branch L_att0x_%=
.p2align 5
L_att0_%=:
v_mfma_f32_16x16x32_bf16 v[232:235],a[128:131],v[112:115],v[44:47]
s_waitcnt vmcnt(8) lgkmcnt(0)
v_exp_f32 v200,v200
v_mfma_f32_16x16x32_bf16 v[232:235],a[132:135],v[116:119],v[232:235]
s_barrier
ds_read_b128 a[128:131],v1
v_add_u32 v2,s48,v2
v_mfma_f32_16x16x32_bf16 v[232:235],a[136:139],v[120:123],v[232:235]
s_mov_b32 s90,L_endseg_%=-L_pc_%=+128
v_exp_f32 v201,v201
v_mfma_f32_16x16x32_bf16 v[232:235],a[140:143],v[124:127],v[232:235]
s_add_u32 m0,s49,s51
)A"
R"A(ds_read_b128 a[132:135],v1 offset:256
v_cvt_pk_bf16_f32 v200,v200,v201
v_mfma_f32_16x16x32_bf16 v[236:239],a[156:159],v[124:127],v[44:47]
s_lshr_b32 s45,s45,1
v_exp_f32 v202,v202
v_mfma_f32_16x16x32_bf16 v[236:239],a[152:155],v[120:123],v[236:239]
s_sub_u32 s94,s94,3
ds_read_b128 a[136:139],v1 offset:512
v_add_u32 v3,s48,v3
v_mfma_f32_16x16x32_bf16 v[236:239],a[148:151],v[116:119],v[236:239]
s_lshr_b32 s60,s94,2
buffer_load_dwordx4 v4,s[20:23],s47 offen nt lds
v_mfma_f32_16x16x32_bf16 v[236:239],a[144:147],v[112:115],v[236:239]
s_max_i32 s60,s60,124
v_exp_f32 v203,v203
v_mfma_f32_16x16x32_bf16 v[248:251],a[160:163],v[112:115],v[44:47]
s_add_u32 s62,s50,32768
ds_read_b128 a[140:143],v1 offset:768
v_cvt_pk_bf16_f32 v201,v202,v203
v_mfma_f32_16x16x32_bf16 v[248:251],a[164:167],v[116:119],v[248:251]
s_and_b32 s62,s62,131071
v_exp_f32 v204,v204
v_mfma_f32_16x16x32_bf16 v[248:251],a[168:171],v[120:123],v[248:251]
s_addk_i32 s54,16384
v_exp_f32 v205,v205
v_mfma_f32_16x16x32_bf16 v[248:251],a[172:175],v[124:127],v[248:251]
s_sub_u32 s48,s62,s50
ds_read_b128 a[156:159],v1 offset:4864
v_cvt_pk_bf16_f32 v202,v204,v205
v_mfma_f32_16x16x32_bf16 v[252:255],a[188:191],v[124:127],v[44:47]
s_sub_u32 s53,s53,1
v_exp_f32 v206,v206
v_mfma_f32_16x16x32_bf16 v[252:255],a[184:187],v[120:123],v[252:255]
s_and_b32 s46,s54,0xfffc0000
v_exp_f32 v207,v207
v_mfma_f32_16x16x32_bf16 v[252:255],a[180:183],v[116:119],v[252:255]
s_mov_b32 s50,s62
ds_read_b128 a[152:155],v1 offset:4608
v_cvt_pk_bf16_f32 v203,v206,v207
v_mfma_f32_16x16x32_bf16 v[252:255],a[176:179],v[112:115],v[252:255]
s_xor_b32 s49,s50,65536
v_exp_f32 v216,v216
v_mfma_f32_16x16x32_bf16 a[32:35],a[192:195],v[168:171],a[32:35]
ds_read_b128 a[148:151],v1 offset:4352
v_mfma_f32_16x16x32_bf16 a[32:35],a[196:199],v[172:175],a[32:35]
v_exp_f32 v217,v217
v_mfma_f32_16x16x32_bf16 a[36:39],a[204:207],v[172:175],a[36:39]
ds_read_b128 a[144:147],v1 offset:4096
v_cvt_pk_bf16_f32 v204,v216,v217
v_mfma_f32_16x16x32_bf16 a[36:39],a[200:203],v[168:171],a[36:39]
v_exp_f32 v218,v218
v_mfma_f32_16x16x32_bf16 a[40:43],a[208:211],v[168:171],a[40:43]
buffer_load_dwordx4 v4,s[20:23],s47 offen offset:1024 nt lds
v_mfma_f32_16x16x32_bf16 a[40:43],a[212:215],v[172:175],a[40:43]
ds_read_b128 a[160:163],v1 offset:8192
v_mfma_f32_16x16x32_bf16 a[44:47],a[220:223],v[172:175],a[44:47]
v_exp_f32 v219,v219
v_mfma_f32_16x16x32_bf16 a[44:47],a[216:219],v[168:171],a[44:47]
ds_read_b128 a[164:167],v1 offset:8448
v_cvt_pk_bf16_f32 v205,v218,v219
v_mfma_f32_16x16x32_bf16 a[48:51],a[224:227],v[168:171],a[48:51]
v_exp_f32 v220,v220
v_mfma_f32_16x16x32_bf16 a[48:51],a[228:231],v[172:175],a[48:51]
ds_read_b128 a[168:171],v1 offset:8704
v_mfma_f32_16x16x32_bf16 a[52:55],a[236:239],v[172:175],a[52:55]
v_exp_f32 v221,v221
v_mfma_f32_16x16x32_bf16 a[52:55],a[232:235],v[168:171],a[52:55]
ds_read_b128 a[172:175],v1 offset:8960
v_cvt_pk_bf16_f32 v206,v220,v221
v_mfma_f32_16x16x32_bf16 a[56:59],a[240:243],v[168:171],a[56:59]
v_exp_f32 v222,v222
v_mfma_f32_16x16x32_bf16 a[56:59],a[244:247],v[172:175],a[56:59]
ds_read_b128 a[188:191],v1 offset:13056
v_mfma_f32_16x16x32_bf16 a[60:63],a[252:255],v[172:175],a[60:63]
v_exp_f32 v223,v223
v_mfma_f32_16x16x32_bf16 a[60:63],a[248:251],v[168:171],a[60:63]
ds_read_b128 a[184:187],v1 offset:12800
v_cvt_pk_bf16_f32 v207,v222,v223
v_smfmac_f32_16x16x64_bf16 v[52:55],v[28:31],v[168:175],v27
s_waitcnt lgkmcnt(4)
v_mfma_f32_16x16x32_bf16 v[136:139],a[128:131],v[64:67],v[32:35]
ds_read_b128 a[180:183],v1 offset:12544
v_mfma_f32_16x16x32_bf16 v[136:139],a[132:135],v[68:71],v[136:139]
buffer_load_dwordx4 v5,s[20:23],s47 offen offset:2048 nt lds
v_mfma_f32_16x16x32_bf16 v[136:139],a[136:139],v[72:75],v[136:139]
ds_read_b128 a[176:179],v1 offset:12288
v_add_u32 v1,s48,v1
v_mfma_f32_16x16x32_bf16 v[136:139],a[140:143],v[76:79],v[136:139]
v_exp_f32 v232,v232
v_mfma_f32_16x16x32_bf16 v[140:143],a[156:159],v[76:79],v[32:35]
v_exp_f32 v233,v233
v_mfma_f32_16x16x32_bf16 v[140:143],a[152:155],v[72:75],v[140:143]
v_exp_f32 v234,v234
v_mfma_f32_16x16x32_bf16 v[140:143],a[148:151],v[68:71],v[140:143]
v_exp_f32 v235,v235
v_mfma_f32_16x16x32_bf16 v[140:143],a[144:147],v[64:67],v[140:143]
v_exp_f32 v236,v236
v_mfma_f32_16x16x32_bf16 v[152:155],a[160:163],v[64:67],v[32:35]
v_exp_f32 v237,v237
v_mfma_f32_16x16x32_bf16 v[152:155],a[164:167],v[68:71],v[152:155]
s_waitcnt lgkmcnt(1)
v_exp_f32 v238,v238
v_mfma_f32_16x16x32_bf16 v[152:155],a[168:171],v[72:75],v[152:155]
v_exp_f32 v239,v239
v_mfma_f32_16x16x32_bf16 v[152:155],a[172:175],v[76:79],v[152:155]
v_exp_f32 v248,v248
v_mfma_f32_16x16x32_bf16 v[156:159],a[188:191],v[76:79],v[32:35]
v_exp_f32 v249,v249
v_mfma_f32_16x16x32_bf16 v[156:159],a[184:187],v[72:75],v[156:159]
v_exp_f32 v250,v250
v_mfma_f32_16x16x32_bf16 v[156:159],a[180:183],v[68:71],v[156:159]
s_waitcnt lgkmcnt(0)
v_exp_f32 v251,v251
v_mfma_f32_16x16x32_bf16 v[156:159],a[176:179],v[64:67],v[156:159]
buffer_load_dwordx4 v5,s[20:23],s47 offen offset:3072 nt lds
v_mfma_f32_16x16x32_bf16 a[64:67],a[192:195],v[200:203],a[64:67]
v_exp_f32 v252,v252
v_mfma_f32_16x16x32_bf16 a[64:67],a[196:199],v[204:207],a[64:67]
v_exp_f32 v253,v253
v_mfma_f32_16x16x32_bf16 a[68:71],a[204:207],v[204:207],a[68:71]
v_exp_f32 v254,v254
v_mfma_f32_16x16x32_bf16 a[68:71],a[200:203],v[200:203],a[68:71]
v_exp_f32 v255,v255
v_mfma_f32_16x16x32_bf16 a[72:75],a[208:211],v[200:203],a[72:75]
v_exp_f32 v136,v136
v_mfma_f32_16x16x32_bf16 a[72:75],a[212:215],v[204:207],a[72:75]
v_exp_f32 v137,v137
v_mfma_f32_16x16x32_bf16 a[76:79],a[220:223],v[204:207],a[76:79]
v_exp_f32 v138,v138
v_mfma_f32_16x16x32_bf16 a[76:79],a[216:219],v[200:203],a[76:79]
v_exp_f32 v139,v139
v_mfma_f32_16x16x32_bf16 a[80:83],a[224:227],v[200:203],a[80:83]
v_exp_f32 v140,v140
v_mfma_f32_16x16x32_bf16 a[80:83],a[228:231],v[204:207],a[80:83]
v_exp_f32 v141,v141
v_mfma_f32_16x16x32_bf16 a[84:87],a[236:239],v[204:207],a[84:87]
s_addk_i32 m0,16384
v_exp_f32 v142,v142
v_mfma_f32_16x16x32_bf16 a[84:87],a[232:235],v[200:203],a[84:87]
v_exp_f32 v143,v143
v_mfma_f32_16x16x32_bf16 a[88:91],a[240:243],v[200:203],a[88:91]
v_exp_f32 v152,v152
v_mfma_f32_16x16x32_bf16 a[88:91],a[244:247],v[204:207],a[88:91]
buffer_load_dwordx4 v4,s[24:27],s47 offen nt lds
v_mfma_f32_16x16x32_bf16 a[92:95],a[252:255],v[204:207],a[92:95]
v_cvt_pk_bf16_f32 v232,v232,v233
v_cvt_pk_bf16_f32 v233,v234,v235
v_mfma_f32_16x16x32_bf16 a[92:95],a[248:251],v[200:203],a[92:95]
v_cvt_pk_bf16_f32 v234,v236,v237
v_cvt_pk_bf16_f32 v235,v238,v239
v_smfmac_f32_16x16x64_bf16 v[56:59],v[28:31],v[200:207],v27
v_cvt_pk_bf16_f32 v236,v248,v249
v_mfma_f32_16x16x32_bf16 v[168:171],a[128:131],v[80:83],v[36:39]
v_cvt_pk_bf16_f32 v237,v250,v251
v_cvt_pk_bf16_f32 v238,v252,v253
v_mfma_f32_16x16x32_bf16 v[168:171],a[132:135],v[84:87],v[168:171]
v_cvt_pk_bf16_f32 v239,v254,v255
v_mfma_f32_16x16x32_bf16 v[168:171],a[136:139],v[88:91],v[168:171]
v_exp_f32 v153,v153
v_mfma_f32_16x16x32_bf16 v[168:171],a[140:143],v[92:95],v[168:171]
v_exp_f32 v154,v154
v_mfma_f32_16x16x32_bf16 v[172:175],a[156:159],v[92:95],v[36:39]
v_exp_f32 v155,v155
v_mfma_f32_16x16x32_bf16 v[172:175],a[152:155],v[88:91],v[172:175]
v_exp_f32 v156,v156
v_mfma_f32_16x16x32_bf16 v[172:175],a[148:151],v[84:87],v[172:175]
v_exp_f32 v157,v157
v_mfma_f32_16x16x32_bf16 v[172:175],a[144:147],v[80:83],v[172:175]
v_exp_f32 v158,v158
v_mfma_f32_16x16x32_bf16 v[184:187],a[160:163],v[80:83],v[36:39]
v_exp_f32 v159,v159
v_mfma_f32_16x16x32_bf16 v[184:187],a[164:167],v[84:87],v[184:187]
v_exp_f32 v168,v168
v_mfma_f32_16x16x32_bf16 v[184:187],a[168:171],v[88:91],v[184:187]
buffer_load_dwordx4 v5,s[24:27],s47 offen offset:1024 nt lds
v_mfma_f32_16x16x32_bf16 v[184:187],a[172:175],v[92:95],v[184:187]
v_exp_f32 v169,v169
v_mfma_f32_16x16x32_bf16 v[188:191],a[188:191],v[92:95],v[36:39]
v_exp_f32 v170,v170
v_mfma_f32_16x16x32_bf16 v[188:191],a[184:187],v[88:91],v[188:191]
v_exp_f32 v171,v171
v_mfma_f32_16x16x32_bf16 v[188:191],a[180:183],v[84:87],v[188:191]
v_exp_f32 v172,v172
v_mfma_f32_16x16x32_bf16 v[188:191],a[176:179],v[80:83],v[188:191]
v_exp_f32 v173,v173
v_mfma_f32_16x16x32_bf16 a[96:99],a[192:195],v[232:235],a[96:99]
ds_read_b64_tr_b16 a[192:193],v2
v_cvt_pk_bf16_f32 v168,v168,v169
v_mfma_f32_16x16x32_bf16 a[96:99],a[196:199],v[236:239],a[96:99]
ds_read_b64_tr_b16 a[194:195],v2 offset:4096
v_cvt_pk_bf16_f32 v169,v170,v171
v_mfma_f32_16x16x32_bf16 a[100:103],a[204:207],v[236:239],a[100:103]
ds_read_b64_tr_b16 a[196:197],v2 offset:8192
v_cvt_pk_bf16_f32 v170,v172,v173
v_mfma_f32_16x16x32_bf16 a[100:103],a[200:203],v[232:235],a[100:103]
v_exp_f32 v174,v174
v_mfma_f32_16x16x32_bf16 a[104:107],a[208:211],v[232:235],a[104:107]
v_exp_f32 v175,v175
v_mfma_f32_16x16x32_bf16 a[104:107],a[212:215],v[236:239],a[104:107]
ds_read_b64_tr_b16 a[198:199],v2 offset:12288
v_cvt_pk_bf16_f32 v171,v174,v175
v_mfma_f32_16x16x32_bf16 a[108:111],a[220:223],v[236:239],a[108:111]
v_exp_f32 v184,v184
v_mfma_f32_16x16x32_bf16 a[108:111],a[216:219],v[232:235],a[108:111]
v_exp_f32 v185,v185
v_mfma_f32_16x16x32_bf16 a[112:115],a[224:227],v[232:235],a[112:115]
buffer_load_dwordx4 v4,s[24:27],s47 offen offset:2048 nt lds
v_mfma_f32_16x16x32_bf16 a[112:115],a[228:231],v[236:239],a[112:115]
ds_read_b64_tr_b16 a[204:205],v3 offset:8192
v_cvt_pk_bf16_f32 v172,v184,v185
v_mfma_f32_16x16x32_bf16 a[116:119],a[236:239],v[236:239],a[116:119]
ds_read_b64_tr_b16 a[206:207],v3 offset:12288
v_cvt_pk_bf16_f32 v136,v136,v137
v_mfma_f32_16x16x32_bf16 a[116:119],a[232:235],v[232:235],a[116:119]
v_exp_f32 v186,v186
v_mfma_f32_16x16x32_bf16 a[120:123],a[240:243],v[232:235],a[120:123]
ds_read_b64_tr_b16 a[200:201],v3
v_mfma_f32_16x16x32_bf16 a[120:123],a[244:247],v[236:239],a[120:123]
ds_read_b64_tr_b16 a[202:203],v3 offset:4096
v_cvt_pk_bf16_f32 v137,v138,v139
v_mfma_f32_16x16x32_bf16 a[124:127],a[252:255],v[236:239],a[124:127]
ds_read_b64_tr_b16 a[208:209],v2 offset:256
v_mfma_f32_16x16x32_bf16 a[124:127],a[248:251],v[232:235],a[124:127]
ds_read_b64_tr_b16 a[210:211],v2 offset:4352
v_smfmac_f32_16x16x64_bf16 v[60:63],v[28:31],v[232:239],v27
ds_read_b64_tr_b16 a[212:213],v2 offset:8448
v_mfma_f32_16x16x32_bf16 v[200:203],a[128:131],v[96:99],v[40:43]
ds_read_b64_tr_b16 a[214:215],v2 offset:12544
v_cvt_pk_bf16_f32 v138,v140,v141
v_mfma_f32_16x16x32_bf16 v[200:203],a[132:135],v[100:103],v[200:203]
ds_read_b64_tr_b16 a[220:221],v3 offset:8448
v_mfma_f32_16x16x32_bf16 v[200:203],a[136:139],v[104:107],v[200:203]
ds_read_b64_tr_b16 a[222:223],v3 offset:12544
v_cvt_pk_bf16_f32 v139,v142,v143
v_mfma_f32_16x16x32_bf16 v[200:203],a[140:143],v[108:111],v[200:203]
ds_read_b64_tr_b16 a[216:217],v3 offset:256
v_mfma_f32_16x16x32_bf16 v[204:207],a[156:159],v[108:111],v[40:43]
ds_read_b64_tr_b16 a[218:219],v3 offset:4352
v_mfma_f32_16x16x32_bf16 v[204:207],a[152:155],v[104:107],v[204:207]
buffer_load_dwordx4 v5,s[24:27],s47 offen offset:3072 nt lds
v_mfma_f32_16x16x32_bf16 v[204:207],a[148:151],v[100:103],v[204:207]
s_or_b32 s47,s47,0x18000
ds_read_b64_tr_b16 a[224:225],v2 offset:512
v_cvt_pk_bf16_f32 v140,v152,v153
v_mfma_f32_16x16x32_bf16 v[204:207],a[144:147],v[96:99],v[204:207]
s_addk_i32 s47,4096
s_and_b32 s47,s47,0x27000
ds_read_b64_tr_b16 a[226:227],v2 offset:4608
v_mfma_f32_16x16x32_bf16 v[216:219],a[160:163],v[96:99],v[40:43]
s_or_b32 s47,s47,s46
ds_read_b64_tr_b16 a[228:229],v2 offset:8704
v_cvt_pk_bf16_f32 v141,v154,v155
v_mfma_f32_16x16x32_bf16 v[216:219],a[164:167],v[100:103],v[216:219]
ds_read_b64_tr_b16 a[230:231],v2 offset:12800
v_mfma_f32_16x16x32_bf16 v[216:219],a[168:171],v[104:107],v[216:219]
ds_read_b64_tr_b16 a[236:237],v3 offset:8704
v_mfma_f32_16x16x32_bf16 v[216:219],a[172:175],v[108:111],v[216:219]
ds_read_b64_tr_b16 a[238:239],v3 offset:12800
v_cvt_pk_bf16_f32 v142,v156,v157
v_mfma_f32_16x16x32_bf16 v[220:223],a[188:191],v[108:111],v[40:43]
ds_read_b64_tr_b16 a[232:233],v3 offset:512
v_mfma_f32_16x16x32_bf16 v[220:223],a[184:187],v[104:107],v[220:223]
ds_read_b64_tr_b16 a[234:235],v3 offset:4608
v_mfma_f32_16x16x32_bf16 v[220:223],a[180:183],v[100:103],v[220:223]
ds_read_b64_tr_b16 a[240:241],v2 offset:768
)A"
R"A(v_cvt_pk_bf16_f32 v143,v158,v159
v_mfma_f32_16x16x32_bf16 v[220:223],a[176:179],v[96:99],v[220:223]
s_waitcnt lgkmcnt(7)
buffer_load_dword v26,v24,s[88:91],s93 offen
v_mfma_f32_16x16x32_bf16 a[0:3],a[192:195],v[136:139],a[0:3]
ds_read_b64_tr_b16 a[242:243],v2 offset:4864
v_mfma_f32_16x16x32_bf16 a[0:3],a[196:199],v[140:143],a[0:3]
ds_read_b64_tr_b16 a[244:245],v2 offset:8960
v_mfma_f32_16x16x32_bf16 a[4:7],a[204:207],v[140:143],a[4:7]
ds_read_b64_tr_b16 a[246:247],v2 offset:13056
v_mfma_f32_16x16x32_bf16 a[4:7],a[200:203],v[136:139],a[4:7]
s_memrealtime s[76:77]
ds_read_b64_tr_b16 a[252:253],v3 offset:8960
v_mfma_f32_16x16x32_bf16 a[8:11],a[208:211],v[136:139],a[8:11]
ds_read_b64_tr_b16 a[254:255],v3 offset:13056
v_mfma_f32_16x16x32_bf16 a[8:11],a[212:215],v[140:143],a[8:11]
ds_read_b64_tr_b16 a[248:249],v3 offset:768
v_mfma_f32_16x16x32_bf16 a[12:15],a[220:223],v[140:143],a[12:15]
ds_read_b64_tr_b16 a[250:251],v3 offset:4864
v_mfma_f32_16x16x32_bf16 a[12:15],a[216:219],v[136:139],a[12:15]
s_bitcmp1_b32 s45,0
s_cbranch_scc0 L_t310_%=
s_mov_b32 m0,s80
s_addk_i32 s80,4096
buffer_load_dwordx4 v25,s[20:23],s83 offen nt lds
s_addk_i32 s83,1024
L_t310_%=:
v_mfma_f32_16x16x32_bf16 a[16:19],a[224:227],v[136:139],a[16:19]
s_waitcnt lgkmcnt(6)
v_exp_f32 v187,v187
v_mfma_f32_16x16x32_bf16 a[16:19],a[228:231],v[140:143],a[16:19]
v_exp_f32 v188,v188
v_mfma_f32_16x16x32_bf16 a[20:23],a[236:239],v[140:143],a[20:23]
s_waitcnt lgkmcnt(0)
v_exp_f32 v189,v189
v_mfma_f32_16x16x32_bf16 a[20:23],a[232:235],v[136:139],a[20:23]
s_sub_u32 s79,s78,s76
v_exp_f32 v190,v190
v_mfma_f32_16x16x32_bf16 a[24:27],a[240:243],v[136:139],a[24:27]
s_add_u32 s78,s76,s60
v_exp_f32 v191,v191
v_mfma_f32_16x16x32_bf16 a[24:27],a[244:247],v[140:143],a[24:27]
s_cmp_gt_i32 s79,s95
s_cbranch_scc1 L_ps3_%=
L_pr3_%=:
v_cvt_pk_bf16_f32 v173,v186,v187
v_mfma_f32_16x16x32_bf16 a[28:31],a[252:255],v[140:143],a[28:31]
v_cvt_pk_bf16_f32 v174,v188,v189
v_cvt_pk_bf16_f32 v175,v190,v191
v_mfma_f32_16x16x32_bf16 a[28:31],a[248:251],v[136:139],a[28:31]
s_cmp_lg_u32 s53,0
v_smfmac_f32_16x16x64_bf16 v[48:51],v[28:31],v[136:143],v27
s_cbranch_scc1 L_att0_%=
L_att0x_%=:
v_mfma_f32_16x16x32_bf16 v[232:235],a[128:131],v[112:115],v[44:47]
s_waitcnt vmcnt(10)
s_sub_u32 s94,s94,3
v_exp_f32 v200,v200
v_mfma_f32_16x16x32_bf16 v[232:235],a[132:135],v[116:119],v[232:235]
s_barrier
ds_read_b128 a[128:131],v1
v_and_b32 v18,48,v6
v_mfma_f32_16x16x32_bf16 v[232:235],a[136:139],v[120:123],v[232:235]
s_lshr_b32 s60,s94,2
v_exp_f32 v201,v201
v_mfma_f32_16x16x32_bf16 v[232:235],a[140:143],v[124:127],v[232:235]
s_add_u32 m0,0,s51
ds_read_b128 a[132:135],v1 offset:256
v_cvt_pk_bf16_f32 v200,v200,v201
v_mfma_f32_16x16x32_bf16 v[236:239],a[156:159],v[124:127],v[44:47]
s_max_i32 s60,s60,124
v_exp_f32 v202,v202
v_mfma_f32_16x16x32_bf16 v[236:239],a[152:155],v[120:123],v[236:239]
s_addk_i32 s54,16384
ds_read_b128 a[136:139],v1 offset:512
v_lshrrev_b32 v19,4,v6
v_mfma_f32_16x16x32_bf16 v[236:239],a[148:151],v[116:119],v[236:239]
s_and_b32 s46,s54,0xfffc0000
buffer_load_dwordx4 v4,s[20:23],s47 offen nt lds
v_mfma_f32_16x16x32_bf16 v[236:239],a[144:147],v[112:115],v[236:239]
s_sub_u32 s94,s94,3
ds_read_b128 a[140:143],v1 offset:768
v_lshlrev_b32 v18,2,v18
v_mfma_f32_16x16x32_bf16 v[248:251],a[160:163],v[112:115],v[44:47]
s_addk_i32 s54,16384
v_exp_f32 v203,v203
v_mfma_f32_16x16x32_bf16 v[248:251],a[164:167],v[116:119],v[248:251]
s_mul_i32 s83,s41,5
ds_read_b128 a[156:159],v1 offset:4864
v_cvt_pk_bf16_f32 v201,v202,v203
v_mfma_f32_16x16x32_bf16 v[248:251],a[168:171],v[120:123],v[248:251]
s_add_u32 s83,s83,s42
v_exp_f32 v204,v204
v_mfma_f32_16x16x32_bf16 v[248:251],a[172:175],v[124:127],v[248:251]
s_add_u32 s83,s83,15
ds_read_b128 a[152:155],v1 offset:4608
v_and_b32 v19,48,v19
v_mfma_f32_16x16x32_bf16 v[252:255],a[188:191],v[124:127],v[44:47]
s_lshl_b32 s43,s83,14
v_exp_f32 v205,v205
v_mfma_f32_16x16x32_bf16 v[252:255],a[184:187],v[120:123],v[252:255]
s_add_u32 s84,s55,0
ds_read_b128 a[148:151],v1 offset:4352
v_cvt_pk_bf16_f32 v202,v204,v205
v_mfma_f32_16x16x32_bf16 v[252:255],a[180:183],v[116:119],v[252:255]
s_add_u32 s85,s55,4096
v_exp_f32 v206,v206
v_mfma_f32_16x16x32_bf16 v[252:255],a[176:179],v[112:115],v[252:255]
s_add_u32 s86,s55,16384
ds_read_b128 a[144:147],v1 offset:4096
v_add_u32 v18,v18,v19
v_mfma_f32_16x16x32_bf16 a[32:35],a[192:195],v[168:171],a[32:35]
s_add_u32 s87,s55,20480
v_exp_f32 v207,v207
v_mfma_f32_16x16x32_bf16 a[32:35],a[196:199],v[172:175],a[32:35]
s_lshl_b32 s83,s83,12
ds_read_b128 a[160:163],v1 offset:8192
v_cvt_pk_bf16_f32 v203,v206,v207
v_mfma_f32_16x16x32_bf16 a[36:39],a[204:207],v[172:175],a[36:39]
s_max_i32 s84,s84,s18
v_exp_f32 v216,v216
v_mfma_f32_16x16x32_bf16 a[36:39],a[200:203],v[168:171],a[36:39]
s_max_i32 s85,s85,s19
ds_read_b128 a[164:167],v1 offset:8448
v_add_u32 v18,98304,v18
v_mfma_f32_16x16x32_bf16 a[40:43],a[208:211],v[168:171],a[40:43]
s_max_i32 s86,s86,s100
buffer_load_dwordx4 v4,s[20:23],s47 offen offset:1024 nt lds
v_mfma_f32_16x16x32_bf16 a[40:43],a[212:215],v[172:175],a[40:43]
s_max_i32 s87,s87,s101
ds_read_b128 a[168:171],v1 offset:8704
v_add_u32 v2,s48,v2
v_mfma_f32_16x16x32_bf16 a[44:47],a[220:223],v[172:175],a[44:47]
s_lshl_b32 s44,s40,5
v_exp_f32 v217,v217
v_mfma_f32_16x16x32_bf16 a[44:47],a[216:219],v[168:171],a[44:47]
s_and_b32 s43,s43,0x20000
ds_read_b128 a[172:175],v1 offset:8960
v_cvt_pk_bf16_f32 v204,v216,v217
v_mfma_f32_16x16x32_bf16 a[48:51],a[224:227],v[168:171],a[48:51]
s_and_b32 s83,s83,0x7000
v_exp_f32 v218,v218
v_mfma_f32_16x16x32_bf16 a[48:51],a[228:231],v[172:175],a[48:51]
s_lshl_b32 s80,s40,10
ds_read_b128 a[188:191],v1 offset:13056
v_add_u32 v3,s48,v3
v_mfma_f32_16x16x32_bf16 a[52:55],a[236:239],v[172:175],a[52:55]
s_mov_b32 s48,32768
v_exp_f32 v219,v219
v_mfma_f32_16x16x32_bf16 a[52:55],a[232:235],v[168:171],a[52:55]
s_and_b32 s44,s44,32
ds_read_b128 a[184:187],v1 offset:12800
v_cvt_pk_bf16_f32 v205,v218,v219
v_mfma_f32_16x16x32_bf16 a[56:59],a[240:243],v[168:171],a[56:59]
s_or_b32 s83,s83,s43
v_exp_f32 v220,v220
v_mfma_f32_16x16x32_bf16 a[56:59],a[244:247],v[172:175],a[56:59]
s_mov_b32 s50,32768
s_mov_b32 s49,98304
ds_read_b128 a[180:183],v1 offset:12544
v_mfma_f32_16x16x32_bf16 a[60:63],a[252:255],v[172:175],a[60:63]
s_mov_b32 s45,0x2a8
v_exp_f32 v221,v221
v_mfma_f32_16x16x32_bf16 a[60:63],a[248:251],v[168:171],a[60:63]
s_add_u32 s80,s80,147456
ds_read_b128 a[176:179],v1 offset:12288
v_cvt_pk_bf16_f32 v206,v220,v221
v_smfmac_f32_16x16x64_bf16 v[52:55],v[28:31],v[168:175],v27
s_or_b32 s83,s83,0x40000
v_add_u32 v1,s48,v1
s_waitcnt lgkmcnt(4)
v_mfma_f32_16x16x32_bf16 v[136:139],a[128:131],v[64:67],v[32:35]
s_add_u32 s93,s92,L_endseg_%=-L_pc_%=
v_exp_f32 v222,v222
v_mfma_f32_16x16x32_bf16 v[136:139],a[132:135],v[68:71],v[136:139]
buffer_load_dwordx4 v5,s[20:23],s47 offen offset:2048 nt lds
v_mfma_f32_16x16x32_bf16 v[136:139],a[136:139],v[72:75],v[136:139]
v_exp_f32 v223,v223
v_mfma_f32_16x16x32_bf16 v[136:139],a[140:143],v[76:79],v[136:139]
v_cvt_pk_bf16_f32 v207,v222,v223
v_xor_b32 v25,s44,v4
v_mfma_f32_16x16x32_bf16 v[140:143],a[156:159],v[76:79],v[32:35]
v_exp_f32 v232,v232
v_mfma_f32_16x16x32_bf16 v[140:143],a[152:155],v[72:75],v[140:143]
v_exp_f32 v233,v233
v_mfma_f32_16x16x32_bf16 v[140:143],a[148:151],v[68:71],v[140:143]
v_exp_f32 v234,v234
v_mfma_f32_16x16x32_bf16 v[140:143],a[144:147],v[64:67],v[140:143]
v_exp_f32 v235,v235
v_mfma_f32_16x16x32_bf16 v[152:155],a[160:163],v[64:67],v[32:35]
v_exp_f32 v236,v236
v_mfma_f32_16x16x32_bf16 v[152:155],a[164:167],v[68:71],v[152:155]
v_exp_f32 v237,v237
v_mfma_f32_16x16x32_bf16 v[152:155],a[168:171],v[72:75],v[152:155]
v_exp_f32 v238,v238
v_mfma_f32_16x16x32_bf16 v[152:155],a[172:175],v[76:79],v[152:155]
s_waitcnt lgkmcnt(0)
v_exp_f32 v239,v239
v_mfma_f32_16x16x32_bf16 v[156:159],a[188:191],v[76:79],v[32:35]
v_exp_f32 v248,v248
v_mfma_f32_16x16x32_bf16 v[156:159],a[184:187],v[72:75],v[156:159]
v_exp_f32 v249,v249
v_mfma_f32_16x16x32_bf16 v[156:159],a[180:183],v[68:71],v[156:159]
v_exp_f32 v250,v250
v_mfma_f32_16x16x32_bf16 v[156:159],a[176:179],v[64:67],v[156:159]
buffer_load_dwordx4 v5,s[20:23],s47 offen offset:3072 nt lds
v_mfma_f32_16x16x32_bf16 a[64:67],a[192:195],v[200:203],a[64:67]
v_exp_f32 v251,v251
v_mfma_f32_16x16x32_bf16 a[64:67],a[196:199],v[204:207],a[64:67]
v_exp_f32 v252,v252
v_mfma_f32_16x16x32_bf16 a[68:71],a[204:207],v[204:207],a[68:71]
v_exp_f32 v253,v253
v_mfma_f32_16x16x32_bf16 a[68:71],a[200:203],v[200:203],a[68:71]
v_exp_f32 v254,v254
v_mfma_f32_16x16x32_bf16 a[72:75],a[208:211],v[200:203],a[72:75]
v_exp_f32 v255,v255
v_mfma_f32_16x16x32_bf16 a[72:75],a[212:215],v[204:207],a[72:75]
v_exp_f32 v136,v136
v_mfma_f32_16x16x32_bf16 a[76:79],a[220:223],v[204:207],a[76:79]
v_exp_f32 v137,v137
v_mfma_f32_16x16x32_bf16 a[76:79],a[216:219],v[200:203],a[76:79]
v_cvt_pk_bf16_f32 v232,v232,v233
v_cvt_pk_bf16_f32 v233,v234,v235
v_mfma_f32_16x16x32_bf16 a[80:83],a[224:227],v[200:203],a[80:83]
v_cvt_pk_bf16_f32 v234,v236,v237
v_cvt_pk_bf16_f32 v235,v238,v239
v_mfma_f32_16x16x32_bf16 a[80:83],a[228:231],v[204:207],a[80:83]
v_cvt_pk_bf16_f32 v236,v248,v249
v_cvt_pk_bf16_f32 v237,v250,v251
v_mfma_f32_16x16x32_bf16 a[84:87],a[236:239],v[204:207],a[84:87]
s_addk_i32 m0,16384
v_cvt_pk_bf16_f32 v238,v252,v253
v_cvt_pk_bf16_f32 v239,v254,v255
v_mfma_f32_16x16x32_bf16 a[84:87],a[232:235],v[200:203],a[84:87]
v_exp_f32 v138,v138
v_mfma_f32_16x16x32_bf16 a[88:91],a[240:243],v[200:203],a[88:91]
v_exp_f32 v139,v139
v_mfma_f32_16x16x32_bf16 a[88:91],a[244:247],v[204:207],a[88:91]
buffer_load_dwordx4 v4,s[24:27],s47 offen nt lds
v_mfma_f32_16x16x32_bf16 a[92:95],a[252:255],v[204:207],a[92:95]
v_exp_f32 v140,v140
v_mfma_f32_16x16x32_bf16 a[92:95],a[248:251],v[200:203],a[92:95]
v_exp_f32 v141,v141
v_smfmac_f32_16x16x64_bf16 v[56:59],v[28:31],v[200:207],v27
v_cvt_pk_bf16_f32 v136,v136,v137
v_mfma_f32_16x16x32_bf16 v[168:171],a[128:131],v[80:83],v[36:39]
v_exp_f32 v142,v142
v_mfma_f32_16x16x32_bf16 v[168:171],a[132:135],v[84:87],v[168:171]
v_exp_f32 v143,v143
v_mfma_f32_16x16x32_bf16 v[168:171],a[136:139],v[88:91],v[168:171]
v_exp_f32 v152,v152
v_mfma_f32_16x16x32_bf16 v[168:171],a[140:143],v[92:95],v[168:171]
v_exp_f32 v153,v153
v_mfma_f32_16x16x32_bf16 v[172:175],a[156:159],v[92:95],v[36:39]
v_exp_f32 v154,v154
v_mfma_f32_16x16x32_bf16 v[172:175],a[152:155],v[88:91],v[172:175]
v_exp_f32 v155,v155
v_mfma_f32_16x16x32_bf16 v[172:175],a[148:151],v[84:87],v[172:175]
v_exp_f32 v156,v156
v_mfma_f32_16x16x32_bf16 v[172:175],a[144:147],v[80:83],v[172:175]
v_exp_f32 v157,v157
v_mfma_f32_16x16x32_bf16 v[184:187],a[160:163],v[80:83],v[36:39]
v_exp_f32 v158,v158
v_mfma_f32_16x16x32_bf16 v[184:187],a[164:167],v[84:87],v[184:187]
v_exp_f32 v159,v159
v_mfma_f32_16x16x32_bf16 v[184:187],a[168:171],v[88:91],v[184:187]
buffer_load_dwordx4 v5,s[24:27],s47 offen offset:1024 nt lds
v_mfma_f32_16x16x32_bf16 v[184:187],a[172:175],v[92:95],v[184:187]
v_exp_f32 v168,v168
v_mfma_f32_16x16x32_bf16 v[188:191],a[188:191],v[92:95],v[36:39]
v_exp_f32 v169,v169
v_mfma_f32_16x16x32_bf16 v[188:191],a[184:187],v[88:91],v[188:191]
v_cvt_pk_bf16_f32 v137,v138,v139
v_cvt_pk_bf16_f32 v138,v140,v141
v_mfma_f32_16x16x32_bf16 v[188:191],a[180:183],v[84:87],v[188:191]
s_waitcnt vmcnt(10)
v_cvt_pk_bf16_f32 v139,v142,v143
v_cvt_pk_bf16_f32 v140,v152,v153
v_mfma_f32_16x16x32_bf16 v[188:191],a[176:179],v[80:83],v[188:191]
s_barrier
ds_read_b128 v[80:83],v18
v_cvt_pk_bf16_f32 v141,v154,v155
v_mfma_f32_16x16x32_bf16 a[96:99],a[192:195],v[232:235],a[96:99]
ds_read_b64_tr_b16 a[192:193],v2
v_cvt_pk_bf16_f32 v142,v156,v157
v_mfma_f32_16x16x32_bf16 a[96:99],a[196:199],v[236:239],a[96:99]
ds_read_b64_tr_b16 a[194:195],v2 offset:4096
v_cvt_pk_bf16_f32 v143,v158,v159
v_mfma_f32_16x16x32_bf16 a[100:103],a[204:207],v[236:239],a[100:103]
ds_read_b64_tr_b16 a[196:197],v2 offset:8192
v_cvt_pk_bf16_f32 v168,v168,v169
v_mfma_f32_16x16x32_bf16 a[100:103],a[200:203],v[232:235],a[100:103]
v_exp_f32 v170,v170
v_mfma_f32_16x16x32_bf16 a[104:107],a[208:211],v[232:235],a[104:107]
v_exp_f32 v171,v171
v_mfma_f32_16x16x32_bf16 a[104:107],a[212:215],v[236:239],a[104:107]
)A"
R"A(ds_read_b128 v[84:87],v18 offset:256
v_cvt_pk_bf16_f32 v169,v170,v171
v_mfma_f32_16x16x32_bf16 a[108:111],a[220:223],v[236:239],a[108:111]
v_exp_f32 v172,v172
v_mfma_f32_16x16x32_bf16 a[108:111],a[216:219],v[232:235],a[108:111]
buffer_load_dwordx4 v4,s[24:27],s47 offen offset:2048 nt lds
v_mfma_f32_16x16x32_bf16 a[112:115],a[224:227],v[232:235],a[112:115]
v_exp_f32 v173,v173
v_mfma_f32_16x16x32_bf16 a[112:115],a[228:231],v[236:239],a[112:115]
ds_read_b128 v[88:91],v18 offset:512
v_cvt_pk_bf16_f32 v170,v172,v173
v_mfma_f32_16x16x32_bf16 a[116:119],a[236:239],v[236:239],a[116:119]
v_exp_f32 v174,v174
v_mfma_f32_16x16x32_bf16 a[116:119],a[232:235],v[232:235],a[116:119]
ds_read_b128 v[92:95],v18 offset:768
v_mfma_f32_16x16x32_bf16 a[120:123],a[240:243],v[232:235],a[120:123]
v_exp_f32 v175,v175
v_mfma_f32_16x16x32_bf16 a[120:123],a[244:247],v[236:239],a[120:123]
ds_read_b64_tr_b16 a[198:199],v2 offset:12288
ds_read_b64_tr_b16 a[204:205],v3 offset:8192
v_mfma_f32_16x16x32_bf16 a[124:127],a[252:255],v[236:239],a[124:127]
ds_read_b64_tr_b16 a[206:207],v3 offset:12288
v_cvt_pk_bf16_f32 v171,v174,v175
v_mfma_f32_16x16x32_bf16 a[124:127],a[248:251],v[232:235],a[124:127]
v_exp_f32 v184,v184
v_smfmac_f32_16x16x64_bf16 v[60:63],v[28:31],v[232:239],v27
ds_read_b64_tr_b16 a[200:201],v3
v_mfma_f32_16x16x32_bf16 v[200:203],a[128:131],v[96:99],v[40:43]
ds_read_b64_tr_b16 a[202:203],v3 offset:4096
ds_read_b64_tr_b16 a[208:209],v2 offset:256
v_mfma_f32_16x16x32_bf16 v[200:203],a[132:135],v[100:103],v[200:203]
v_exp_f32 v185,v185
v_mfma_f32_16x16x32_bf16 v[200:203],a[136:139],v[104:107],v[200:203]
ds_read_b64_tr_b16 a[210:211],v2 offset:4352
ds_read_b64_tr_b16 a[212:213],v2 offset:8448
v_mfma_f32_16x16x32_bf16 v[200:203],a[140:143],v[108:111],v[200:203]
ds_read_b64_tr_b16 a[214:215],v2 offset:12544
v_cvt_pk_bf16_f32 v172,v184,v185
v_mfma_f32_16x16x32_bf16 v[204:207],a[156:159],v[108:111],v[40:43]
buffer_load_dwordx4 v5,s[24:27],s47 offen offset:3072 nt lds
v_mfma_f32_16x16x32_bf16 v[204:207],a[152:155],v[104:107],v[204:207]
s_or_b32 s47,s47,0x18000
ds_read_b64_tr_b16 a[220:221],v3 offset:8448
ds_read_b64_tr_b16 a[222:223],v3 offset:12544
v_mfma_f32_16x16x32_bf16 v[204:207],a[148:151],v[100:103],v[204:207]
s_addk_i32 s47,4096
v_exp_f32 v186,v186
v_mfma_f32_16x16x32_bf16 v[204:207],a[144:147],v[96:99],v[204:207]
s_and_b32 s47,s47,0x27000
ds_read_b64_tr_b16 a[216:217],v3 offset:256
ds_read_b64_tr_b16 a[218:219],v3 offset:4352
v_mfma_f32_16x16x32_bf16 v[216:219],a[160:163],v[96:99],v[40:43]
s_or_b32 s47,s47,s46
v_exp_f32 v187,v187
v_mfma_f32_16x16x32_bf16 v[216:219],a[164:167],v[100:103],v[216:219]
s_and_b32 s46,s54,0xfffc0000
ds_read_b64_tr_b16 a[224:225],v2 offset:512
ds_read_b64_tr_b16 a[226:227],v2 offset:4608
v_mfma_f32_16x16x32_bf16 v[216:219],a[168:171],v[104:107],v[216:219]
s_addk_i32 s54,16384
ds_read_b64_tr_b16 a[228:229],v2 offset:8704
v_cvt_pk_bf16_f32 v173,v186,v187
v_mfma_f32_16x16x32_bf16 v[216:219],a[172:175],v[108:111],v[216:219]
ds_read_b64_tr_b16 a[230:231],v2 offset:12800
v_mfma_f32_16x16x32_bf16 v[220:223],a[188:191],v[108:111],v[40:43]
ds_read_b64_tr_b16 a[236:237],v3 offset:8704
v_mfma_f32_16x16x32_bf16 v[220:223],a[184:187],v[104:107],v[220:223]
ds_read_b64_tr_b16 a[238:239],v3 offset:12800
ds_read_b64_tr_b16 a[232:233],v3 offset:512
v_mfma_f32_16x16x32_bf16 v[220:223],a[180:183],v[100:103],v[220:223]
ds_read_b64_tr_b16 a[234:235],v3 offset:4608
v_mfma_f32_16x16x32_bf16 v[220:223],a[176:179],v[96:99],v[220:223]
s_waitcnt lgkmcnt(8)
ds_read_b64_tr_b16 a[240:241],v2 offset:768
v_mfma_f32_16x16x32_bf16 v[32:35],v[80:83],v[128:131],0
ds_read_b64_tr_b16 a[242:243],v2 offset:4864
v_mfma_f32_16x16x32_bf16 v[32:35],v[84:87],v[160:163],v[32:35]
ds_read_b64_tr_b16 a[244:245],v2 offset:8960
ds_read_b64_tr_b16 a[246:247],v2 offset:13056
v_mfma_f32_16x16x32_bf16 v[32:35],v[88:91],v[192:195],v[32:35]
ds_read_b64_tr_b16 a[252:253],v3 offset:8960
v_add_u32 v2,s48,v2
v_mfma_f32_16x16x32_bf16 v[32:35],v[92:95],v[224:227],v[32:35]
ds_read_b64_tr_b16 a[254:255],v3 offset:13056
v_mfma_f32_16x16x32_bf16 v[36:39],v[80:83],v[132:135],0
ds_read_b64_tr_b16 a[248:249],v3 offset:768
v_mfma_f32_16x16x32_bf16 v[36:39],v[84:87],v[164:167],v[36:39]
ds_read_b64_tr_b16 a[250:251],v3 offset:4864
v_add_u32 v3,s48,v3
v_mfma_f32_16x16x32_bf16 v[36:39],v[88:91],v[196:199],v[36:39]
s_mov_b32 s48,-98304
v_exp_f32 v188,v188
v_mfma_f32_16x16x32_bf16 v[36:39],v[92:95],v[228:231],v[36:39]
v_exp_f32 v189,v189
v_mfma_f32_16x16x32_bf16 v[40:43],v[80:83],v[144:147],0
ds_read_b128 v[20:23],v18 offset:16384
v_cvt_pk_bf16_f32 v174,v188,v189
v_mfma_f32_16x16x32_bf16 v[40:43],v[84:87],v[176:179],v[40:43]
v_exp_f32 v190,v190
v_mfma_f32_16x16x32_bf16 v[40:43],v[88:91],v[208:211],v[40:43]
v_exp_f32 v191,v191
v_mfma_f32_16x16x32_bf16 v[40:43],v[92:95],v[240:243],v[40:43]
v_cvt_pk_bf16_f32 v175,v190,v191
v_max3_f32 v32,v32,v33,v34
v_mfma_f32_16x16x32_bf16 a[0:3],a[192:195],v[136:139],a[0:3]
v_exp_f32 v200,v200
v_mfma_f32_16x16x32_bf16 a[0:3],a[196:199],v[140:143],a[0:3]
v_max3_f32 v32,v32,v35,s18
v_max3_f32 v36,v36,v37,v38
v_mfma_f32_16x16x32_bf16 a[4:7],a[204:207],v[140:143],a[4:7]
v_exp_f32 v201,v201
v_mfma_f32_16x16x32_bf16 a[4:7],a[200:203],v[136:139],a[4:7]
s_memrealtime s[76:77]
v_exp_f32 v202,v202
v_mfma_f32_16x16x32_bf16 a[8:11],a[208:211],v[136:139],a[8:11]
v_exp_f32 v203,v203
v_mfma_f32_16x16x32_bf16 a[8:11],a[212:215],v[140:143],a[8:11]
s_waitcnt lgkmcnt(0)
v_and_b32 v20,0x7fff7fff,v20
v_and_b32 v21,0x7fff7fff,v21
v_mfma_f32_16x16x32_bf16 a[12:15],a[220:223],v[140:143],a[12:15]
v_sad_u16 v20,v20,0,0
v_and_b32 v22,0x7fff7fff,v22
v_mfma_f32_16x16x32_bf16 a[12:15],a[216:219],v[136:139],a[12:15]
v_sad_u16 v20,v21,0,v20
v_and_b32 v23,0x7fff7fff,v23
v_mfma_f32_16x16x32_bf16 a[16:19],a[224:227],v[136:139],a[16:19]
v_sad_u16 v20,v22,0,v20
v_sad_u16 v20,v23,0,v20
v_mfma_f32_16x16x32_bf16 a[16:19],a[228:231],v[140:143],a[16:19]
s_nop 1
v_add_u32_dpp v20,v20,v20 quad_perm:[1,0,3,2] row_mask:0xf bank_mask:0xf
v_mfma_f32_16x16x32_bf16 a[20:23],a[236:239],v[140:143],a[20:23]
s_waitcnt lgkmcnt(0)
s_nop 1
v_add_u32_dpp v20,v20,v20 quad_perm:[2,3,0,1] row_mask:0xf bank_mask:0xf
v_mfma_f32_16x16x32_bf16 a[20:23],a[232:235],v[136:139],a[20:23]
s_sub_u32 s79,s78,s76
v_cvt_f32_u32 v20,v20
v_mov_b32 v21,0xb9800000
v_mfma_f32_16x16x32_bf16 a[24:27],a[240:243],v[136:139],a[24:27]
s_add_u32 s78,s76,s60
v_fmaak_f32 v20,v20,v21,0x42e8c937
v_cvt_pk_bf16_f32 v200,v200,v201
v_mfma_f32_16x16x32_bf16 a[24:27],a[244:247],v[140:143],a[24:27]
s_cmp_gt_i32 s79,s95
s_cbranch_scc1 L_ps4_%=
L_pr4_%=:
v_cvt_pk_bf16_f32 v201,v202,v203
v_mfma_f32_16x16x32_bf16 a[28:31],a[252:255],v[140:143],a[28:31]
s_lshr_b32 s60,s94,2
s_nop 0
v_readfirstlane_b32 s52,v20
v_mfma_f32_16x16x32_bf16 a[28:31],a[248:251],v[136:139],a[28:31]
s_nop 1
s_min_u32 s52,s52,0xc2200000
v_smfmac_f32_16x16x64_bf16 v[48:51],v[28:31],v[136:143],v27
s_min_i32 s52,s52,0x41a00000
v_sub_f32 v35,s52,v32
v_mfma_f32_16x16x32_bf16 v[232:235],a[128:131],v[112:115],v[44:47]
s_waitcnt vmcnt(9)
s_max_i32 s60,s60,124
v_sub_f32 v34,s52,v32
v_sub_f32 v33,s52,v32
v_mfma_f32_16x16x32_bf16 v[232:235],a[132:135],v[116:119],v[232:235]
s_barrier
ds_read_b128 a[128:131],v1
v_sub_f32 v32,s52,v32
v_mfma_f32_16x16x32_bf16 v[232:235],a[136:139],v[120:123],v[232:235]
s_sub_u32 s94,s94,3
v_exp_f32 v204,v204
v_mfma_f32_16x16x32_bf16 v[232:235],a[140:143],v[124:127],v[232:235]
s_add_u32 m0,32768,s51
ds_read_b128 a[132:135],v1 offset:256
v_max3_f32 v36,v36,v39,s19
v_mfma_f32_16x16x32_bf16 v[236:239],a[156:159],v[124:127],v[44:47]
v_exp_f32 v205,v205
v_mfma_f32_16x16x32_bf16 v[236:239],a[152:155],v[120:123],v[236:239]
ds_read_b128 a[136:139],v1 offset:512
v_cvt_pk_bf16_f32 v202,v204,v205
v_mfma_f32_16x16x32_bf16 v[236:239],a[148:151],v[116:119],v[236:239]
buffer_load_dwordx4 v4,s[20:23],s47 offen nt lds
v_mfma_f32_16x16x32_bf16 v[236:239],a[144:147],v[112:115],v[236:239]
ds_read_b128 a[140:143],v1 offset:768
v_sub_f32 v39,s52,v36
v_mfma_f32_16x16x32_bf16 v[248:251],a[160:163],v[112:115],v[44:47]
v_exp_f32 v206,v206
v_mfma_f32_16x16x32_bf16 v[248:251],a[164:167],v[116:119],v[248:251]
ds_read_b128 a[156:159],v1 offset:4864
v_sub_f32 v38,s52,v36
v_mfma_f32_16x16x32_bf16 v[248:251],a[168:171],v[120:123],v[248:251]
v_exp_f32 v207,v207
v_mfma_f32_16x16x32_bf16 v[248:251],a[172:175],v[124:127],v[248:251]
ds_read_b128 a[152:155],v1 offset:4608
v_cvt_pk_bf16_f32 v203,v206,v207
v_mfma_f32_16x16x32_bf16 v[252:255],a[188:191],v[124:127],v[44:47]
v_exp_f32 v216,v216
v_mfma_f32_16x16x32_bf16 v[252:255],a[184:187],v[120:123],v[252:255]
ds_read_b128 a[148:151],v1 offset:4352
v_sub_f32 v37,s52,v36
v_mfma_f32_16x16x32_bf16 v[252:255],a[180:183],v[116:119],v[252:255]
v_exp_f32 v217,v217
v_mfma_f32_16x16x32_bf16 v[252:255],a[176:179],v[112:115],v[252:255]
ds_read_b128 a[144:147],v1 offset:4096
v_cvt_pk_bf16_f32 v204,v216,v217
v_mfma_f32_16x16x32_bf16 v[44:47],v[80:83],v[148:151],0
v_exp_f32 v218,v218
v_mfma_f32_16x16x32_bf16 v[44:47],v[84:87],v[180:183],v[44:47]
ds_read_b128 a[160:163],v1 offset:8192
v_sub_f32 v36,s52,v36
v_mfma_f32_16x16x32_bf16 v[44:47],v[88:91],v[212:215],v[44:47]
v_exp_f32 v219,v219
v_mfma_f32_16x16x32_bf16 v[44:47],v[92:95],v[244:247],v[44:47]
ds_read_b128 a[164:167],v1 offset:8448
v_cvt_pk_bf16_f32 v205,v218,v219
v_mfma_f32_16x16x32_bf16 a[32:35],a[192:195],v[168:171],a[32:35]
buffer_load_dwordx4 v4,s[20:23],s47 offen offset:1024 nt lds
v_mfma_f32_16x16x32_bf16 a[32:35],a[196:199],v[172:175],a[32:35]
ds_read_b128 a[168:171],v1 offset:8704
v_max3_f32 v40,v40,v41,v42
v_mfma_f32_16x16x32_bf16 a[36:39],a[204:207],v[172:175],a[36:39]
v_exp_f32 v220,v220
v_mfma_f32_16x16x32_bf16 a[36:39],a[200:203],v[168:171],a[36:39]
ds_read_b128 a[172:175],v1 offset:8960
v_max3_f32 v40,v40,v43,s100
v_mfma_f32_16x16x32_bf16 a[40:43],a[208:211],v[168:171],a[40:43]
v_exp_f32 v221,v221
v_mfma_f32_16x16x32_bf16 a[40:43],a[212:215],v[172:175],a[40:43]
ds_read_b128 a[188:191],v1 offset:13056
v_cvt_pk_bf16_f32 v206,v220,v221
v_mfma_f32_16x16x32_bf16 a[44:47],a[220:223],v[172:175],a[44:47]
v_exp_f32 v222,v222
v_mfma_f32_16x16x32_bf16 a[44:47],a[216:219],v[168:171],a[44:47]
ds_read_b128 a[184:187],v1 offset:12800
v_sub_f32 v43,s52,v40
v_mfma_f32_16x16x32_bf16 a[48:51],a[224:227],v[168:171],a[48:51]
v_exp_f32 v223,v223
v_mfma_f32_16x16x32_bf16 a[48:51],a[228:231],v[172:175],a[48:51]
ds_read_b128 a[180:183],v1 offset:12544
v_cvt_pk_bf16_f32 v207,v222,v223
v_mfma_f32_16x16x32_bf16 a[52:55],a[236:239],v[172:175],a[52:55]
v_exp_f32 v232,v232
v_mfma_f32_16x16x32_bf16 a[52:55],a[232:235],v[168:171],a[52:55]
ds_read_b128 a[176:179],v1 offset:12288
v_sub_f32 v42,s52,v40
v_mfma_f32_16x16x32_bf16 a[56:59],a[240:243],v[168:171],a[56:59]
v_exp_f32 v233,v233
v_mfma_f32_16x16x32_bf16 a[56:59],a[244:247],v[172:175],a[56:59]
v_exp_f32 v234,v234
v_mfma_f32_16x16x32_bf16 a[60:63],a[252:255],v[172:175],a[60:63]
buffer_load_dwordx4 v5,s[20:23],s47 offen offset:2048 nt lds
v_mfma_f32_16x16x32_bf16 a[60:63],a[248:251],v[168:171],a[60:63]
v_exp_f32 v235,v235
v_smfmac_f32_16x16x64_bf16 v[52:55],v[28:31],v[168:175],v27
s_waitcnt lgkmcnt(2)
v_cvt_pk_bf16_f32 v232,v232,v233
v_mfma_f32_16x16x32_bf16 v[136:139],a[128:131],v[128:131],v[32:35]
v_exp_f32 v236,v236
v_mfma_f32_16x16x32_bf16 v[136:139],a[132:135],v[160:163],v[136:139]
v_exp_f32 v237,v237
v_mfma_f32_16x16x32_bf16 v[136:139],a[136:139],v[192:195],v[136:139]
v_exp_f32 v238,v238
v_mfma_f32_16x16x32_bf16 v[136:139],a[140:143],v[224:227],v[136:139]
v_exp_f32 v239,v239
v_mfma_f32_16x16x32_bf16 v[140:143],a[156:159],v[224:227],v[32:35]
v_cvt_pk_bf16_f32 v233,v234,v235
v_cvt_pk_bf16_f32 v234,v236,v237
v_mfma_f32_16x16x32_bf16 v[140:143],a[152:155],v[192:195],v[140:143]
v_cvt_pk_bf16_f32 v235,v238,v239
v_sub_f32 v41,s52,v40
v_mfma_f32_16x16x32_bf16 v[140:143],a[148:151],v[160:163],v[140:143]
v_exp_f32 v248,v248
v_mfma_f32_16x16x32_bf16 v[140:143],a[144:147],v[128:131],v[140:143]
v_exp_f32 v249,v249
v_mfma_f32_16x16x32_bf16 v[152:155],a[160:163],v[128:131],v[32:35]
v_exp_f32 v250,v250
v_mfma_f32_16x16x32_bf16 v[152:155],a[164:167],v[160:163],v[152:155]
)A"
R"A(v_exp_f32 v251,v251
v_mfma_f32_16x16x32_bf16 v[152:155],a[168:171],v[192:195],v[152:155]
v_exp_f32 v252,v252
v_mfma_f32_16x16x32_bf16 v[152:155],a[172:175],v[224:227],v[152:155]
buffer_load_dwordx4 v5,s[20:23],s47 offen offset:3072 nt lds
v_mfma_f32_16x16x32_bf16 v[156:159],a[188:191],v[224:227],v[32:35]
v_exp_f32 v253,v253
v_mfma_f32_16x16x32_bf16 v[156:159],a[184:187],v[192:195],v[156:159]
s_waitcnt lgkmcnt(0)
v_exp_f32 v254,v254
v_mfma_f32_16x16x32_bf16 v[156:159],a[180:183],v[160:163],v[156:159]
v_exp_f32 v255,v255
v_mfma_f32_16x16x32_bf16 v[156:159],a[176:179],v[128:131],v[156:159]
v_cvt_pk_bf16_f32 v236,v248,v249
v_cvt_pk_bf16_f32 v237,v250,v251
v_mfma_f32_16x16x32_bf16 a[64:67],a[192:195],v[200:203],a[64:67]
v_cvt_pk_bf16_f32 v238,v252,v253
v_cvt_pk_bf16_f32 v239,v254,v255
v_mfma_f32_16x16x32_bf16 a[64:67],a[196:199],v[204:207],a[64:67]
v_sub_f32 v40,s52,v40
v_add_f32 v48,v49,v48
v_mfma_f32_16x16x32_bf16 a[68:71],a[204:207],v[204:207],a[68:71]
v_exp_f32 v136,v136
v_mfma_f32_16x16x32_bf16 a[68:71],a[200:203],v[200:203],a[68:71]
v_exp_f32 v137,v137
v_mfma_f32_16x16x32_bf16 a[72:75],a[208:211],v[200:203],a[72:75]
v_exp_f32 v138,v138
v_mfma_f32_16x16x32_bf16 a[72:75],a[212:215],v[204:207],a[72:75]
v_exp_f32 v139,v139
v_mfma_f32_16x16x32_bf16 a[76:79],a[220:223],v[204:207],a[76:79]
s_addk_i32 m0,16384
v_max_f32 v20,0x00800000,v48
v_cvt_pk_bf16_f32 v136,v136,v137
v_mfma_f32_16x16x32_bf16 a[76:79],a[216:219],v[200:203],a[76:79]
v_exp_f32 v140,v140
v_mfma_f32_16x16x32_bf16 a[80:83],a[224:227],v[200:203],a[80:83]
v_exp_f32 v141,v141
v_mfma_f32_16x16x32_bf16 a[80:83],a[228:231],v[204:207],a[80:83]
buffer_load_dwordx4 v4,s[24:27],s47 offen nt lds
v_mfma_f32_16x16x32_bf16 a[84:87],a[236:239],v[204:207],a[84:87]
v_exp_f32 v142,v142
v_mfma_f32_16x16x32_bf16 a[84:87],a[232:235],v[200:203],a[84:87]
v_exp_f32 v143,v143
v_mfma_f32_16x16x32_bf16 a[88:91],a[240:243],v[200:203],a[88:91]
v_cvt_pk_bf16_f32 v137,v138,v139
v_cvt_pk_bf16_f32 v138,v140,v141
v_mfma_f32_16x16x32_bf16 a[88:91],a[244:247],v[204:207],a[88:91]
v_rcp_f32 v20,v20
v_mfma_f32_16x16x32_bf16 a[92:95],a[252:255],v[204:207],a[92:95]
v_cvt_pk_bf16_f32 v139,v142,v143
v_mul_f32 v21,0xbe22f983,v20
v_mfma_f32_16x16x32_bf16 a[92:95],a[248:251],v[200:203],a[92:95]
v_exp_f32 v152,v152
v_smfmac_f32_16x16x64_bf16 v[56:59],v[28:31],v[200:207],v27
v_cmp_nlt_f32 s[56:57],v48,s63
v_mfma_f32_16x16x32_bf16 v[168:171],a[128:131],v[132:135],v[36:39]
v_exp_f32 v153,v153
v_mfma_f32_16x16x32_bf16 v[168:171],a[132:135],v[164:167],v[168:171]
v_exp_f32 v154,v154
v_mfma_f32_16x16x32_bf16 v[168:171],a[136:139],v[196:199],v[168:171]
v_exp_f32 v155,v155
v_mfma_f32_16x16x32_bf16 v[168:171],a[140:143],v[228:231],v[168:171]
v_exp_f32 v156,v156
v_mfma_f32_16x16x32_bf16 v[172:175],a[156:159],v[228:231],v[36:39]
v_exp_f32 v157,v157
v_mfma_f32_16x16x32_bf16 v[172:175],a[152:155],v[196:199],v[172:175]
v_exp_f32 v158,v158
v_mfma_f32_16x16x32_bf16 v[172:175],a[148:151],v[164:167],v[172:175]
buffer_load_dwordx4 v5,s[24:27],s47 offen offset:1024 nt lds
v_mfma_f32_16x16x32_bf16 v[172:175],a[144:147],v[132:135],v[172:175]
v_exp_f32 v159,v159
v_mfma_f32_16x16x32_bf16 v[184:187],a[160:163],v[132:135],v[36:39]
v_cvt_pk_bf16_f32 v140,v152,v153
v_cvt_pk_bf16_f32 v141,v154,v155
v_mfma_f32_16x16x32_bf16 v[184:187],a[164:167],v[164:167],v[184:187]
v_cvt_pk_bf16_f32 v142,v156,v157
v_cvt_pk_bf16_f32 v143,v158,v159
v_mfma_f32_16x16x32_bf16 v[184:187],a[168:171],v[196:199],v[184:187]
.p2align 3
v_accvgpr_read_b32 v64,a0
v_accvgpr_read_b32 v65,a1
v_accvgpr_read_b32 v66,a2
v_accvgpr_read_b32 v67,a3
v_accvgpr_read_b32 v12,a4
v_accvgpr_read_b32 v13,a5
v_accvgpr_read_b32 v14,a6
v_accvgpr_read_b32 v15,a7
v_pk_fma_f32 v[64:65],v[20:21],v[64:65],v[20:21] op_sel:[0,0,1] op_sel_hi:[0,1,1]
v_pk_fma_f32 v[66:67],v[20:21],v[66:67],v[20:21] op_sel:[0,0,1] op_sel_hi:[0,1,1]
v_pk_fma_f32 v[12:13],v[20:21],v[12:13],v[20:21] op_sel:[0,0,1] op_sel_hi:[0,1,1]
v_pk_fma_f32 v[14:15],v[20:21],v[14:15],v[20:21] op_sel:[0,0,1] op_sel_hi:[0,1,1]
v_cvt_pk_bf16_f32 v64,v64,v65
v_cvt_pk_bf16_f32 v65,v66,v67
v_cvt_pk_bf16_f32 v66,v12,v13
v_cvt_pk_bf16_f32 v67,v14,v15
v_mfma_f32_16x16x32_bf16 v[184:187],a[172:175],v[228:231],v[184:187]
.p2align 3
v_accvgpr_read_b32 v68,a8
v_accvgpr_read_b32 v69,a9
v_accvgpr_read_b32 v70,a10
v_accvgpr_read_b32 v71,a11
v_accvgpr_read_b32 v12,a12
v_accvgpr_read_b32 v13,a13
v_accvgpr_read_b32 v14,a14
v_accvgpr_read_b32 v15,a15
v_pk_fma_f32 v[68:69],v[20:21],v[68:69],v[20:21] op_sel:[0,0,1] op_sel_hi:[0,1,1]
v_pk_fma_f32 v[70:71],v[20:21],v[70:71],v[20:21] op_sel:[0,0,1] op_sel_hi:[0,1,1]
v_pk_fma_f32 v[12:13],v[20:21],v[12:13],v[20:21] op_sel:[0,0,1] op_sel_hi:[0,1,1]
v_pk_fma_f32 v[14:15],v[20:21],v[14:15],v[20:21] op_sel:[0,0,1] op_sel_hi:[0,1,1]
v_cvt_pk_bf16_f32 v68,v68,v69
v_cvt_pk_bf16_f32 v69,v70,v71
v_cvt_pk_bf16_f32 v70,v12,v13
v_cvt_pk_bf16_f32 v71,v14,v15
v_mfma_f32_16x16x32_bf16 v[188:191],a[188:191],v[228:231],v[36:39]
.p2align 3
v_accvgpr_read_b32 v72,a16
v_accvgpr_read_b32 v73,a17
v_accvgpr_read_b32 v74,a18
v_accvgpr_read_b32 v75,a19
v_accvgpr_read_b32 v12,a20
v_accvgpr_read_b32 v13,a21
v_accvgpr_read_b32 v14,a22
v_accvgpr_read_b32 v15,a23
v_pk_fma_f32 v[72:73],v[20:21],v[72:73],v[20:21] op_sel:[0,0,1] op_sel_hi:[0,1,1]
v_pk_fma_f32 v[74:75],v[20:21],v[74:75],v[20:21] op_sel:[0,0,1] op_sel_hi:[0,1,1]
v_pk_fma_f32 v[12:13],v[20:21],v[12:13],v[20:21] op_sel:[0,0,1] op_sel_hi:[0,1,1]
v_pk_fma_f32 v[14:15],v[20:21],v[14:15],v[20:21] op_sel:[0,0,1] op_sel_hi:[0,1,1]
v_cvt_pk_bf16_f32 v72,v72,v73
v_cvt_pk_bf16_f32 v73,v74,v75
v_cvt_pk_bf16_f32 v74,v12,v13
v_cvt_pk_bf16_f32 v75,v14,v15
v_mfma_f32_16x16x32_bf16 v[188:191],a[184:187],v[196:199],v[188:191]
.p2align 3
v_accvgpr_read_b32 v76,a24
v_accvgpr_read_b32 v77,a25
v_accvgpr_read_b32 v78,a26
v_accvgpr_read_b32 v79,a27
v_accvgpr_read_b32 v12,a28
v_accvgpr_read_b32 v13,a29
v_accvgpr_read_b32 v14,a30
v_accvgpr_read_b32 v15,a31
v_pk_fma_f32 v[76:77],v[20:21],v[76:77],v[20:21] op_sel:[0,0,1] op_sel_hi:[0,1,1]
v_pk_fma_f32 v[78:79],v[20:21],v[78:79],v[20:21] op_sel:[0,0,1] op_sel_hi:[0,1,1]
v_pk_fma_f32 v[12:13],v[20:21],v[12:13],v[20:21] op_sel:[0,0,1] op_sel_hi:[0,1,1]
v_pk_fma_f32 v[14:15],v[20:21],v[14:15],v[20:21] op_sel:[0,0,1] op_sel_hi:[0,1,1]
v_cvt_pk_bf16_f32 v76,v76,v77
v_cvt_pk_bf16_f32 v77,v78,v79
v_cvt_pk_bf16_f32 v78,v12,v13
v_cvt_pk_bf16_f32 v79,v14,v15
v_mfma_f32_16x16x32_bf16 v[188:191],a[180:183],v[164:167],v[188:191]
v_max3_f32 v44,v44,v45,v46
v_max3_f32 v44,v44,v47,s101
v_mfma_f32_16x16x32_bf16 v[188:191],a[176:179],v[132:135],v[188:191]
v_mov_b32 v48,0
v_mov_b32 v49,0
v_mfma_f32_16x16x32_bf16 a[96:99],a[192:195],v[232:235],a[96:99]
ds_read_b64_tr_b16 a[192:193],v2
ds_read_b64_tr_b16 a[194:195],v2 offset:4096
v_mfma_f32_16x16x32_bf16 a[96:99],a[196:199],v[236:239],a[96:99]
ds_read_b64_tr_b16 a[196:197],v2 offset:8192
v_mov_b32 v50,0
v_mfma_f32_16x16x32_bf16 a[100:103],a[204:207],v[236:239],a[100:103]
ds_read_b64_tr_b16 a[198:199],v2 offset:12288
v_mov_b32 v51,0
v_mfma_f32_16x16x32_bf16 a[100:103],a[200:203],v[232:235],a[100:103]
ds_read_b64_tr_b16 a[204:205],v3 offset:8192
v_sub_f32 v47,s52,v44
v_mfma_f32_16x16x32_bf16 a[104:107],a[208:211],v[232:235],a[104:107]
buffer_load_dwordx4 v4,s[24:27],s47 offen offset:2048 nt lds
v_mfma_f32_16x16x32_bf16 a[104:107],a[212:215],v[236:239],a[104:107]
ds_read_b64_tr_b16 a[206:207],v3 offset:12288
ds_read_b64_tr_b16 a[200:201],v3
v_mfma_f32_16x16x32_bf16 a[108:111],a[220:223],v[236:239],a[108:111]
ds_read_b64_tr_b16 a[202:203],v3 offset:4096
v_sub_f32 v46,s52,v44
v_mfma_f32_16x16x32_bf16 a[108:111],a[216:219],v[232:235],a[108:111]
ds_read_b64_tr_b16 a[208:209],v2 offset:256
v_sub_f32 v45,s52,v44
v_mfma_f32_16x16x32_bf16 a[112:115],a[224:227],v[232:235],a[112:115]
ds_read_b64_tr_b16 a[210:211],v2 offset:4352
v_sub_f32 v44,s52,v44
v_mfma_f32_16x16x32_bf16 a[112:115],a[228:231],v[236:239],a[112:115]
ds_read_b64_tr_b16 a[212:213],v2 offset:8448
ds_read_b64_tr_b16 a[214:215],v2 offset:12544
v_mfma_f32_16x16x32_bf16 a[116:119],a[236:239],v[236:239],a[116:119]
ds_read_b64_tr_b16 a[220:221],v3 offset:8448
v_add_f32 v52,v53,v52
v_mfma_f32_16x16x32_bf16 a[116:119],a[232:235],v[232:235],a[116:119]
ds_read_b64_tr_b16 a[222:223],v3 offset:12544
v_max_f32 v22,0x00800000,v52
v_mfma_f32_16x16x32_bf16 a[120:123],a[240:243],v[232:235],a[120:123]
ds_read_b64_tr_b16 a[216:217],v3 offset:256
v_add_u32 v1,s48,v1
v_mfma_f32_16x16x32_bf16 a[120:123],a[244:247],v[236:239],a[120:123]
ds_read_b64_tr_b16 a[218:219],v3 offset:4352
ds_read_b64_tr_b16 a[224:225],v2 offset:512
v_mfma_f32_16x16x32_bf16 a[124:127],a[252:255],v[236:239],a[124:127]
ds_read_b64_tr_b16 a[226:227],v2 offset:4608
v_mov_b32 v53,0
v_mfma_f32_16x16x32_bf16 a[124:127],a[248:251],v[232:235],a[124:127]
ds_read_b64_tr_b16 a[228:229],v2 offset:8704
v_mov_b32 v54,0
v_smfmac_f32_16x16x64_bf16 v[60:63],v[28:31],v[232:239],v27
ds_read_b64_tr_b16 a[230:231],v2 offset:12800
v_mfma_f32_16x16x32_bf16 v[200:203],a[128:131],v[144:147],v[40:43]
ds_read_b64_tr_b16 a[236:237],v3 offset:8704
ds_read_b64_tr_b16 a[238:239],v3 offset:12800
v_mfma_f32_16x16x32_bf16 v[200:203],a[132:135],v[176:179],v[200:203]
buffer_load_dwordx4 v5,s[24:27],s47 offen offset:3072 nt lds
v_mfma_f32_16x16x32_bf16 v[200:203],a[136:139],v[208:211],v[200:203]
s_or_b32 s47,s47,0x18000
ds_read_b64_tr_b16 a[232:233],v3 offset:512
ds_read_b64_tr_b16 a[234:235],v3 offset:4608
v_mfma_f32_16x16x32_bf16 v[200:203],a[140:143],v[240:243],v[200:203]
s_addk_i32 s47,4096
s_and_b32 s47,s47,0x27000
ds_read_b64_tr_b16 a[240:241],v2 offset:768
v_mfma_f32_16x16x32_bf16 v[204:207],a[156:159],v[240:243],v[40:43]
s_or_b32 s47,s47,s46
ds_read_b64_tr_b16 a[242:243],v2 offset:4864
v_mov_b32 v55,0
v_mfma_f32_16x16x32_bf16 v[204:207],a[152:155],v[208:211],v[204:207]
s_and_b32 s46,s54,0xfffc0000
ds_read_b64_tr_b16 a[244:245],v2 offset:8960
v_add_f32 v56,v57,v56
v_mfma_f32_16x16x32_bf16 v[204:207],a[148:151],v[176:179],v[204:207]
ds_read_b64_tr_b16 a[246:247],v2 offset:13056
ds_read_b64_tr_b16 a[252:253],v3 offset:8960
v_mfma_f32_16x16x32_bf16 v[204:207],a[144:147],v[144:147],v[204:207]
ds_read_b64_tr_b16 a[254:255],v3 offset:13056
v_max_f32 v20,0x00800000,v56
v_mfma_f32_16x16x32_bf16 v[216:219],a[160:163],v[144:147],v[40:43]
ds_read_b64_tr_b16 a[248:249],v3 offset:768
v_max3_f32 v18,|v64|,|v65|,|v66|
v_mfma_f32_16x16x32_bf16 v[216:219],a[164:167],v[176:179],v[216:219]
ds_read_b64_tr_b16 a[250:251],v3 offset:4864
v_max3_f32 v18,v18,|v67|,s18
v_mfma_f32_16x16x32_bf16 v[216:219],a[168:171],v[208:211],v[216:219]
v_exp_f32 v168,v168
v_mfma_f32_16x16x32_bf16 v[216:219],a[172:175],v[240:243],v[216:219]
v_exp_f32 v169,v169
v_mfma_f32_16x16x32_bf16 v[220:223],a[188:191],v[240:243],v[40:43]
v_exp_f32 v170,v170
v_mfma_f32_16x16x32_bf16 v[220:223],a[184:187],v[208:211],v[220:223]
v_exp_f32 v171,v171
v_mfma_f32_16x16x32_bf16 v[220:223],a[180:183],v[176:179],v[220:223]
v_cvt_pk_bf16_f32 v168,v168,v169
v_cvt_pk_bf16_f32 v169,v170,v171
v_mfma_f32_16x16x32_bf16 v[220:223],a[176:179],v[144:147],v[220:223]
s_waitcnt lgkmcnt(2)
v_exp_f32 v172,v172
v_mfma_f32_16x16x32_bf16 a[0:3],a[192:195],v[136:139],0.15915494
v_exp_f32 v173,v173
v_mfma_f32_16x16x32_bf16 a[0:3],a[196:199],v[140:143],a[0:3]
v_exp_f32 v174,v174
v_mfma_f32_16x16x32_bf16 a[4:7],a[204:207],v[140:143],0.15915494
v_exp_f32 v175,v175
v_mfma_f32_16x16x32_bf16 a[4:7],a[200:203],v[136:139],a[4:7]
s_memrealtime s[76:77]
v_rcp_f32 v22,v22
v_mfma_f32_16x16x32_bf16 a[8:11],a[208:211],v[136:139],0.15915494
v_cvt_pk_bf16_f32 v170,v172,v173
v_cvt_pk_bf16_f32 v171,v174,v175
v_mfma_f32_16x16x32_bf16 a[8:11],a[212:215],v[140:143],a[8:11]
v_exp_f32 v184,v184
v_mfma_f32_16x16x32_bf16 a[12:15],a[220:223],v[140:143],0.15915494
v_exp_f32 v185,v185
v_mfma_f32_16x16x32_bf16 a[12:15],a[216:219],v[136:139],a[12:15]
v_exp_f32 v186,v186
v_mfma_f32_16x16x32_bf16 a[16:19],a[224:227],v[136:139],0.15915494
v_exp_f32 v187,v187
v_mfma_f32_16x16x32_bf16 a[16:19],a[228:231],v[140:143],a[16:19]
v_exp_f32 v188,v188
v_mfma_f32_16x16x32_bf16 a[20:23],a[236:239],v[140:143],0.15915494
s_waitcnt lgkmcnt(0)
)A"
R"A(v_exp_f32 v189,v189
v_mfma_f32_16x16x32_bf16 a[20:23],a[232:235],v[136:139],a[20:23]
s_sub_u32 s79,s78,s76
v_exp_f32 v190,v190
v_mfma_f32_16x16x32_bf16 a[24:27],a[240:243],v[136:139],0.15915494
s_add_u32 s78,s76,s60
v_exp_f32 v191,v191
v_mfma_f32_16x16x32_bf16 a[24:27],a[244:247],v[140:143],a[24:27]
s_cmp_gt_i32 s79,s95
s_cbranch_scc1 L_ps5_%=
L_pr5_%=:
v_mul_f32 v23,0xbe22f983,v22
v_mfma_f32_16x16x32_bf16 a[28:31],a[252:255],v[140:143],0.15915494
s_lshr_b32 s60,s94,2
v_cvt_pk_bf16_f32 v172,v184,v185
v_cvt_pk_bf16_f32 v173,v186,v187
v_mfma_f32_16x16x32_bf16 a[28:31],a[248:251],v[136:139],a[28:31]
s_max_i32 s60,s60,124
v_cvt_pk_bf16_f32 v174,v188,v189
v_cvt_pk_bf16_f32 v175,v190,v191
v_smfmac_f32_16x16x64_bf16 v[48:51],v[28:31],v[136:143],v27
v_fmac_f32 v18,0x39800000,v21
v_mfma_f32_16x16x32_bf16 v[232:235],a[128:131],v[148:151],v[44:47]
s_waitcnt vmcnt(8)
.p2align 3
v_accvgpr_read_b32 v80,a32
v_accvgpr_read_b32 v81,a33
v_accvgpr_read_b32 v82,a34
v_accvgpr_read_b32 v83,a35
v_accvgpr_read_b32 v12,a36
v_accvgpr_read_b32 v13,a37
v_accvgpr_read_b32 v14,a38
v_accvgpr_read_b32 v15,a39
v_pk_fma_f32 v[80:81],v[22:23],v[80:81],v[22:23] op_sel:[0,0,1] op_sel_hi:[0,1,1]
v_pk_fma_f32 v[82:83],v[22:23],v[82:83],v[22:23] op_sel:[0,0,1] op_sel_hi:[0,1,1]
v_pk_fma_f32 v[12:13],v[22:23],v[12:13],v[22:23] op_sel:[0,0,1] op_sel_hi:[0,1,1]
v_pk_fma_f32 v[14:15],v[22:23],v[14:15],v[22:23] op_sel:[0,0,1] op_sel_hi:[0,1,1]
v_cvt_pk_bf16_f32 v80,v80,v81
v_cvt_pk_bf16_f32 v81,v82,v83
v_cvt_pk_bf16_f32 v82,v12,v13
v_cvt_pk_bf16_f32 v83,v14,v15
v_mfma_f32_16x16x32_bf16 v[232:235],a[132:135],v[180:183],v[232:235]
s_barrier
.p2align 3
v_accvgpr_read_b32 v84,a40
v_accvgpr_read_b32 v85,a41
v_accvgpr_read_b32 v86,a42
v_accvgpr_read_b32 v87,a43
v_accvgpr_read_b32 v12,a44
v_accvgpr_read_b32 v13,a45
v_accvgpr_read_b32 v14,a46
v_accvgpr_read_b32 v15,a47
v_pk_fma_f32 v[84:85],v[22:23],v[84:85],v[22:23] op_sel:[0,0,1] op_sel_hi:[0,1,1]
v_pk_fma_f32 v[86:87],v[22:23],v[86:87],v[22:23] op_sel:[0,0,1] op_sel_hi:[0,1,1]
v_pk_fma_f32 v[12:13],v[22:23],v[12:13],v[22:23] op_sel:[0,0,1] op_sel_hi:[0,1,1]
v_pk_fma_f32 v[14:15],v[22:23],v[14:15],v[22:23] op_sel:[0,0,1] op_sel_hi:[0,1,1]
v_cvt_pk_bf16_f32 v84,v84,v85
v_cvt_pk_bf16_f32 v85,v86,v87
v_cvt_pk_bf16_f32 v86,v12,v13
v_cvt_pk_bf16_f32 v87,v14,v15
v_mfma_f32_16x16x32_bf16 v[232:235],a[136:139],v[212:215],v[232:235]
.p2align 3
v_accvgpr_read_b32 v88,a48
v_accvgpr_read_b32 v89,a49
v_accvgpr_read_b32 v90,a50
v_accvgpr_read_b32 v91,a51
v_accvgpr_read_b32 v12,a52
v_accvgpr_read_b32 v13,a53
v_accvgpr_read_b32 v14,a54
v_accvgpr_read_b32 v15,a55
v_pk_fma_f32 v[88:89],v[22:23],v[88:89],v[22:23] op_sel:[0,0,1] op_sel_hi:[0,1,1]
v_pk_fma_f32 v[90:91],v[22:23],v[90:91],v[22:23] op_sel:[0,0,1] op_sel_hi:[0,1,1]
v_pk_fma_f32 v[12:13],v[22:23],v[12:13],v[22:23] op_sel:[0,0,1] op_sel_hi:[0,1,1]
v_pk_fma_f32 v[14:15],v[22:23],v[14:15],v[22:23] op_sel:[0,0,1] op_sel_hi:[0,1,1]
v_cvt_pk_bf16_f32 v88,v88,v89
v_cvt_pk_bf16_f32 v89,v90,v91
v_cvt_pk_bf16_f32 v90,v12,v13
v_cvt_pk_bf16_f32 v91,v14,v15
v_mfma_f32_16x16x32_bf16 v[232:235],a[140:143],v[244:247],v[232:235]
s_add_u32 m0,65536,s51
ds_read_b128 a[128:131],v1
v_mov_b32 v57,0
v_mfma_f32_16x16x32_bf16 v[236:239],a[156:159],v[244:247],v[44:47]
.p2align 3
v_accvgpr_read_b32 v92,a56
v_accvgpr_read_b32 v93,a57
v_accvgpr_read_b32 v94,a58
v_accvgpr_read_b32 v95,a59
v_accvgpr_read_b32 v12,a60
v_accvgpr_read_b32 v13,a61
v_accvgpr_read_b32 v14,a62
v_accvgpr_read_b32 v15,a63
v_pk_fma_f32 v[92:93],v[22:23],v[92:93],v[22:23] op_sel:[0,0,1] op_sel_hi:[0,1,1]
v_pk_fma_f32 v[94:95],v[22:23],v[94:95],v[22:23] op_sel:[0,0,1] op_sel_hi:[0,1,1]
v_pk_fma_f32 v[12:13],v[22:23],v[12:13],v[22:23] op_sel:[0,0,1] op_sel_hi:[0,1,1]
v_pk_fma_f32 v[14:15],v[22:23],v[14:15],v[22:23] op_sel:[0,0,1] op_sel_hi:[0,1,1]
v_cvt_pk_bf16_f32 v92,v92,v93
v_cvt_pk_bf16_f32 v93,v94,v95
v_cvt_pk_bf16_f32 v94,v12,v13
v_cvt_pk_bf16_f32 v95,v14,v15
v_mfma_f32_16x16x32_bf16 v[236:239],a[152:155],v[212:215],v[236:239]
ds_read_b128 a[132:135],v1 offset:256
v_mov_b32 v58,0
v_mfma_f32_16x16x32_bf16 v[236:239],a[148:151],v[180:183],v[236:239]
buffer_load_dwordx4 v4,s[20:23],s47 offen nt lds
v_mfma_f32_16x16x32_bf16 v[236:239],a[144:147],v[148:151],v[236:239]
ds_read_b128 a[136:139],v1 offset:512
v_mov_b32 v59,0
v_mfma_f32_16x16x32_bf16 v[248:251],a[160:163],v[148:151],v[44:47]
v_cmp_nlt_f32 s[58:59],v52,s63
s_nop 1
s_or_b64 s[56:57],s[56:57],s[58:59]
v_mfma_f32_16x16x32_bf16 v[248:251],a[164:167],v[180:183],v[248:251]
ds_read_b128 a[140:143],v1 offset:768
v_mov_b32 v52,0
v_mfma_f32_16x16x32_bf16 v[248:251],a[168:171],v[212:215],v[248:251]
v_exp_f32 v200,v200
v_mfma_f32_16x16x32_bf16 v[248:251],a[172:175],v[244:247],v[248:251]
ds_read_b128 a[156:159],v1 offset:4864
v_add_f32 v60,v61,v60
v_mfma_f32_16x16x32_bf16 v[252:255],a[188:191],v[244:247],v[44:47]
v_exp_f32 v201,v201
v_mfma_f32_16x16x32_bf16 v[252:255],a[184:187],v[212:215],v[252:255]
ds_read_b128 a[152:155],v1 offset:4608
v_cvt_pk_bf16_f32 v200,v200,v201
v_mfma_f32_16x16x32_bf16 v[252:255],a[180:183],v[180:183],v[252:255]
v_exp_f32 v202,v202
v_mfma_f32_16x16x32_bf16 v[252:255],a[176:179],v[148:151],v[252:255]
ds_read_b128 a[148:151],v1 offset:4352
v_max_f32 v22,0x00800000,v60
v_mfma_f32_16x16x32_bf16 a[32:35],a[192:195],v[168:171],0.15915494
v_exp_f32 v203,v203
v_mfma_f32_16x16x32_bf16 a[32:35],a[196:199],v[172:175],a[32:35]
ds_read_b128 a[144:147],v1 offset:4096
v_cvt_pk_bf16_f32 v201,v202,v203
v_mfma_f32_16x16x32_bf16 a[36:39],a[204:207],v[172:175],0.15915494
v_exp_f32 v204,v204
v_mfma_f32_16x16x32_bf16 a[36:39],a[200:203],v[168:171],a[36:39]
ds_read_b128 a[160:163],v1 offset:8192
v_max3_f32 v19,|v80|,|v81|,|v82|
v_mfma_f32_16x16x32_bf16 a[40:43],a[208:211],v[168:171],0.15915494
buffer_load_dwordx4 v4,s[20:23],s47 offen offset:1024 nt lds
v_mfma_f32_16x16x32_bf16 a[40:43],a[212:215],v[172:175],a[40:43]
ds_read_b128 a[164:167],v1 offset:8448
v_max3_f32 v19,v19,|v83|,s19
v_mfma_f32_16x16x32_bf16 a[44:47],a[220:223],v[172:175],0.15915494
v_exp_f32 v205,v205
v_mfma_f32_16x16x32_bf16 a[44:47],a[216:219],v[168:171],a[44:47]
ds_read_b128 a[168:171],v1 offset:8704
v_cvt_pk_bf16_f32 v202,v204,v205
v_mfma_f32_16x16x32_bf16 a[48:51],a[224:227],v[168:171],0.15915494
v_exp_f32 v206,v206
v_mfma_f32_16x16x32_bf16 a[48:51],a[228:231],v[172:175],a[48:51]
ds_read_b128 a[172:175],v1 offset:8960
v_fmac_f32 v19,0x39800000,v23
v_mfma_f32_16x16x32_bf16 a[52:55],a[236:239],v[172:175],0.15915494
v_exp_f32 v207,v207
v_mfma_f32_16x16x32_bf16 a[52:55],a[232:235],v[168:171],a[52:55]
ds_read_b128 a[188:191],v1 offset:13056
v_cvt_pk_bf16_f32 v203,v206,v207
v_mfma_f32_16x16x32_bf16 a[56:59],a[240:243],v[168:171],0.15915494
v_rcp_f32 v20,v20
v_mfma_f32_16x16x32_bf16 a[56:59],a[244:247],v[172:175],a[56:59]
ds_read_b128 a[184:187],v1 offset:12800
v_mul_f32 v21,0xbe22f983,v20
v_mfma_f32_16x16x32_bf16 a[60:63],a[252:255],v[172:175],0.15915494
v_exp_f32 v216,v216
v_mfma_f32_16x16x32_bf16 a[60:63],a[248:251],v[168:171],a[60:63]
ds_read_b128 a[180:183],v1 offset:12544
v_mov_b32 v61,0
v_smfmac_f32_16x16x64_bf16 v[52:55],v[28:31],v[168:175],v27
s_waitcnt lgkmcnt(4)
v_mov_b32 v62,0
v_mfma_f32_16x16x32_bf16 v[136:139],a[128:131],v[128:131],v[32:35]
ds_read_b128 a[176:179],v1 offset:12288
v_mov_b32 v63,0
v_mfma_f32_16x16x32_bf16 v[136:139],a[132:135],v[160:163],v[136:139]
buffer_load_dwordx4 v5,s[20:23],s47 offen offset:2048 nt lds
v_mfma_f32_16x16x32_bf16 v[136:139],a[136:139],v[192:195],v[136:139]
v_exp_f32 v217,v217
v_mfma_f32_16x16x32_bf16 v[136:139],a[140:143],v[224:227],v[136:139]
v_exp_f32 v218,v218
v_mfma_f32_16x16x32_bf16 v[140:143],a[156:159],v[224:227],v[32:35]
v_exp_f32 v219,v219
v_mfma_f32_16x16x32_bf16 v[140:143],a[152:155],v[192:195],v[140:143]
v_exp_f32 v220,v220
v_mfma_f32_16x16x32_bf16 v[140:143],a[148:151],v[160:163],v[140:143]
v_exp_f32 v221,v221
v_mfma_f32_16x16x32_bf16 v[140:143],a[144:147],v[128:131],v[140:143]
v_exp_f32 v222,v222
v_mfma_f32_16x16x32_bf16 v[152:155],a[160:163],v[128:131],v[32:35]
v_exp_f32 v223,v223
v_mfma_f32_16x16x32_bf16 v[152:155],a[164:167],v[160:163],v[152:155]
v_cvt_pk_bf16_f32 v204,v216,v217
v_cvt_pk_bf16_f32 v205,v218,v219
v_mfma_f32_16x16x32_bf16 v[152:155],a[168:171],v[192:195],v[152:155]
s_waitcnt lgkmcnt(0)
v_cvt_pk_bf16_f32 v206,v220,v221
v_cvt_pk_bf16_f32 v207,v222,v223
v_mfma_f32_16x16x32_bf16 v[152:155],a[172:175],v[224:227],v[152:155]
.p2align 3
v_accvgpr_read_b32 v96,a64
v_accvgpr_read_b32 v97,a65
v_accvgpr_read_b32 v98,a66
v_accvgpr_read_b32 v99,a67
v_accvgpr_read_b32 v12,a68
v_accvgpr_read_b32 v13,a69
v_accvgpr_read_b32 v14,a70
v_accvgpr_read_b32 v15,a71
v_pk_fma_f32 v[96:97],v[20:21],v[96:97],v[20:21] op_sel:[0,0,1] op_sel_hi:[0,1,1]
v_pk_fma_f32 v[98:99],v[20:21],v[98:99],v[20:21] op_sel:[0,0,1] op_sel_hi:[0,1,1]
v_pk_fma_f32 v[12:13],v[20:21],v[12:13],v[20:21] op_sel:[0,0,1] op_sel_hi:[0,1,1]
v_pk_fma_f32 v[14:15],v[20:21],v[14:15],v[20:21] op_sel:[0,0,1] op_sel_hi:[0,1,1]
v_cvt_pk_bf16_f32 v96,v96,v97
v_cvt_pk_bf16_f32 v97,v98,v99
v_cvt_pk_bf16_f32 v98,v12,v13
v_cvt_pk_bf16_f32 v99,v14,v15
v_mfma_f32_16x16x32_bf16 v[156:159],a[188:191],v[224:227],v[32:35]
.p2align 3
v_accvgpr_read_b32 v100,a72
v_accvgpr_read_b32 v101,a73
v_accvgpr_read_b32 v102,a74
v_accvgpr_read_b32 v103,a75
v_accvgpr_read_b32 v12,a76
v_accvgpr_read_b32 v13,a77
v_accvgpr_read_b32 v14,a78
v_accvgpr_read_b32 v15,a79
v_pk_fma_f32 v[100:101],v[20:21],v[100:101],v[20:21] op_sel:[0,0,1] op_sel_hi:[0,1,1]
v_pk_fma_f32 v[102:103],v[20:21],v[102:103],v[20:21] op_sel:[0,0,1] op_sel_hi:[0,1,1]
v_pk_fma_f32 v[12:13],v[20:21],v[12:13],v[20:21] op_sel:[0,0,1] op_sel_hi:[0,1,1]
v_pk_fma_f32 v[14:15],v[20:21],v[14:15],v[20:21] op_sel:[0,0,1] op_sel_hi:[0,1,1]
v_cvt_pk_bf16_f32 v100,v100,v101
v_cvt_pk_bf16_f32 v101,v102,v103
v_cvt_pk_bf16_f32 v102,v12,v13
v_cvt_pk_bf16_f32 v103,v14,v15
v_mfma_f32_16x16x32_bf16 v[156:159],a[184:187],v[192:195],v[156:159]
.p2align 3
v_accvgpr_read_b32 v104,a80
v_accvgpr_read_b32 v105,a81
v_accvgpr_read_b32 v106,a82
v_accvgpr_read_b32 v107,a83
v_accvgpr_read_b32 v12,a84
v_accvgpr_read_b32 v13,a85
v_accvgpr_read_b32 v14,a86
v_accvgpr_read_b32 v15,a87
v_pk_fma_f32 v[104:105],v[20:21],v[104:105],v[20:21] op_sel:[0,0,1] op_sel_hi:[0,1,1]
v_pk_fma_f32 v[106:107],v[20:21],v[106:107],v[20:21] op_sel:[0,0,1] op_sel_hi:[0,1,1]
v_pk_fma_f32 v[12:13],v[20:21],v[12:13],v[20:21] op_sel:[0,0,1] op_sel_hi:[0,1,1]
v_pk_fma_f32 v[14:15],v[20:21],v[14:15],v[20:21] op_sel:[0,0,1] op_sel_hi:[0,1,1]
v_cvt_pk_bf16_f32 v104,v104,v105
v_cvt_pk_bf16_f32 v105,v106,v107
v_cvt_pk_bf16_f32 v106,v12,v13
v_cvt_pk_bf16_f32 v107,v14,v15
v_mfma_f32_16x16x32_bf16 v[156:159],a[180:183],v[160:163],v[156:159]
.p2align 3
v_accvgpr_read_b32 v108,a88
v_accvgpr_read_b32 v109,a89
v_accvgpr_read_b32 v110,a90
v_accvgpr_read_b32 v111,a91
v_accvgpr_read_b32 v12,a92
v_accvgpr_read_b32 v13,a93
v_accvgpr_read_b32 v14,a94
v_accvgpr_read_b32 v15,a95
v_pk_fma_f32 v[108:109],v[20:21],v[108:109],v[20:21] op_sel:[0,0,1] op_sel_hi:[0,1,1]
v_pk_fma_f32 v[110:111],v[20:21],v[110:111],v[20:21] op_sel:[0,0,1] op_sel_hi:[0,1,1]
v_pk_fma_f32 v[12:13],v[20:21],v[12:13],v[20:21] op_sel:[0,0,1] op_sel_hi:[0,1,1]
v_pk_fma_f32 v[14:15],v[20:21],v[14:15],v[20:21] op_sel:[0,0,1] op_sel_hi:[0,1,1]
v_cvt_pk_bf16_f32 v108,v108,v109
v_cvt_pk_bf16_f32 v109,v110,v111
v_cvt_pk_bf16_f32 v110,v12,v13
v_cvt_pk_bf16_f32 v111,v14,v15
v_mfma_f32_16x16x32_bf16 v[156:159],a[176:179],v[128:131],v[156:159]
buffer_load_dwordx4 v5,s[20:23],s47 offen offset:3072 nt lds
v_mfma_f32_16x16x32_bf16 a[64:67],a[192:195],v[200:203],0.15915494
v_cmp_nlt_f32 s[58:59],v56,s63
s_nop 1
s_or_b64 s[56:57],s[56:57],s[58:59]
v_mfma_f32_16x16x32_bf16 a[64:67],a[196:199],v[204:207],a[64:67]
v_mov_b32 v56,0
v_add_u32 v2,s48,v2
v_mfma_f32_16x16x32_bf16 a[68:71],a[204:207],v[204:207],0.15915494
v_exp_f32 v232,v232
v_mfma_f32_16x16x32_bf16 a[68:71],a[200:203],v[200:203],a[68:71]
v_exp_f32 v233,v233
v_mfma_f32_16x16x32_bf16 a[72:75],a[208:211],v[200:203],0.15915494
v_exp_f32 v234,v234
v_mfma_f32_16x16x32_bf16 a[72:75],a[212:215],v[204:207],a[72:75]
)A"
R"A(v_exp_f32 v235,v235
v_mfma_f32_16x16x32_bf16 a[76:79],a[220:223],v[204:207],0.15915494
v_cvt_pk_bf16_f32 v232,v232,v233
v_cvt_pk_bf16_f32 v233,v234,v235
v_mfma_f32_16x16x32_bf16 a[76:79],a[216:219],v[200:203],a[76:79]
v_exp_f32 v236,v236
v_mfma_f32_16x16x32_bf16 a[80:83],a[224:227],v[200:203],0.15915494
v_exp_f32 v237,v237
v_mfma_f32_16x16x32_bf16 a[80:83],a[228:231],v[204:207],a[80:83]
v_exp_f32 v238,v238
v_mfma_f32_16x16x32_bf16 a[84:87],a[236:239],v[204:207],0.15915494
s_addk_i32 m0,16384
v_exp_f32 v239,v239
v_mfma_f32_16x16x32_bf16 a[84:87],a[232:235],v[200:203],a[84:87]
v_rcp_f32 v22,v22
v_mfma_f32_16x16x32_bf16 a[88:91],a[240:243],v[200:203],0.15915494
v_cvt_pk_bf16_f32 v234,v236,v237
v_cvt_pk_bf16_f32 v235,v238,v239
v_mfma_f32_16x16x32_bf16 a[88:91],a[244:247],v[204:207],a[88:91]
buffer_load_dwordx4 v4,s[24:27],s47 offen nt lds
v_mfma_f32_16x16x32_bf16 a[92:95],a[252:255],v[204:207],0.15915494
v_exp_f32 v248,v248
v_mfma_f32_16x16x32_bf16 a[92:95],a[248:251],v[200:203],a[92:95]
v_exp_f32 v249,v249
v_smfmac_f32_16x16x64_bf16 v[56:59],v[28:31],v[200:207],v27
v_mul_f32 v23,0xbe22f983,v22
v_mfma_f32_16x16x32_bf16 v[168:171],a[128:131],v[132:135],v[36:39]
v_exp_f32 v250,v250
v_mfma_f32_16x16x32_bf16 v[168:171],a[132:135],v[164:167],v[168:171]
v_exp_f32 v251,v251
v_mfma_f32_16x16x32_bf16 v[168:171],a[136:139],v[196:199],v[168:171]
v_exp_f32 v252,v252
v_mfma_f32_16x16x32_bf16 v[168:171],a[140:143],v[228:231],v[168:171]
v_exp_f32 v253,v253
v_mfma_f32_16x16x32_bf16 v[172:175],a[156:159],v[228:231],v[36:39]
v_exp_f32 v254,v254
v_mfma_f32_16x16x32_bf16 v[172:175],a[152:155],v[196:199],v[172:175]
v_exp_f32 v255,v255
v_mfma_f32_16x16x32_bf16 v[172:175],a[148:151],v[164:167],v[172:175]
v_cvt_pk_bf16_f32 v236,v248,v249
v_cvt_pk_bf16_f32 v237,v250,v251
v_mfma_f32_16x16x32_bf16 v[172:175],a[144:147],v[132:135],v[172:175]
v_cvt_pk_bf16_f32 v238,v252,v253
v_cvt_pk_bf16_f32 v239,v254,v255
v_mfma_f32_16x16x32_bf16 v[184:187],a[160:163],v[132:135],v[36:39]
.p2align 3
v_accvgpr_read_b32 v112,a96
v_accvgpr_read_b32 v113,a97
v_accvgpr_read_b32 v114,a98
v_accvgpr_read_b32 v115,a99
v_accvgpr_read_b32 v12,a100
v_accvgpr_read_b32 v13,a101
v_accvgpr_read_b32 v14,a102
v_accvgpr_read_b32 v15,a103
v_pk_fma_f32 v[112:113],v[22:23],v[112:113],v[22:23] op_sel:[0,0,1] op_sel_hi:[0,1,1]
v_pk_fma_f32 v[114:115],v[22:23],v[114:115],v[22:23] op_sel:[0,0,1] op_sel_hi:[0,1,1]
v_pk_fma_f32 v[12:13],v[22:23],v[12:13],v[22:23] op_sel:[0,0,1] op_sel_hi:[0,1,1]
v_pk_fma_f32 v[14:15],v[22:23],v[14:15],v[22:23] op_sel:[0,0,1] op_sel_hi:[0,1,1]
v_cvt_pk_bf16_f32 v112,v112,v113
v_cvt_pk_bf16_f32 v113,v114,v115
v_cvt_pk_bf16_f32 v114,v12,v13
v_cvt_pk_bf16_f32 v115,v14,v15
v_mfma_f32_16x16x32_bf16 v[184:187],a[164:167],v[164:167],v[184:187]
.p2align 3
v_accvgpr_read_b32 v116,a104
v_accvgpr_read_b32 v117,a105
v_accvgpr_read_b32 v118,a106
v_accvgpr_read_b32 v119,a107
v_accvgpr_read_b32 v12,a108
v_accvgpr_read_b32 v13,a109
v_accvgpr_read_b32 v14,a110
v_accvgpr_read_b32 v15,a111
v_pk_fma_f32 v[116:117],v[22:23],v[116:117],v[22:23] op_sel:[0,0,1] op_sel_hi:[0,1,1]
v_pk_fma_f32 v[118:119],v[22:23],v[118:119],v[22:23] op_sel:[0,0,1] op_sel_hi:[0,1,1]
v_pk_fma_f32 v[12:13],v[22:23],v[12:13],v[22:23] op_sel:[0,0,1] op_sel_hi:[0,1,1]
v_pk_fma_f32 v[14:15],v[22:23],v[14:15],v[22:23] op_sel:[0,0,1] op_sel_hi:[0,1,1]
v_cvt_pk_bf16_f32 v116,v116,v117
v_cvt_pk_bf16_f32 v117,v118,v119
v_cvt_pk_bf16_f32 v118,v12,v13
v_cvt_pk_bf16_f32 v119,v14,v15
v_mfma_f32_16x16x32_bf16 v[184:187],a[168:171],v[196:199],v[184:187]
buffer_load_dwordx4 v5,s[24:27],s47 offen offset:1024 nt lds
v_mfma_f32_16x16x32_bf16 v[184:187],a[172:175],v[228:231],v[184:187]
.p2align 3
v_accvgpr_read_b32 v120,a112
v_accvgpr_read_b32 v121,a113
v_accvgpr_read_b32 v122,a114
v_accvgpr_read_b32 v123,a115
v_accvgpr_read_b32 v12,a116
v_accvgpr_read_b32 v13,a117
v_accvgpr_read_b32 v14,a118
v_accvgpr_read_b32 v15,a119
v_pk_fma_f32 v[120:121],v[22:23],v[120:121],v[22:23] op_sel:[0,0,1] op_sel_hi:[0,1,1]
v_pk_fma_f32 v[122:123],v[22:23],v[122:123],v[22:23] op_sel:[0,0,1] op_sel_hi:[0,1,1]
v_pk_fma_f32 v[12:13],v[22:23],v[12:13],v[22:23] op_sel:[0,0,1] op_sel_hi:[0,1,1]
v_pk_fma_f32 v[14:15],v[22:23],v[14:15],v[22:23] op_sel:[0,0,1] op_sel_hi:[0,1,1]
v_cvt_pk_bf16_f32 v120,v120,v121
v_cvt_pk_bf16_f32 v121,v122,v123
v_cvt_pk_bf16_f32 v122,v12,v13
v_cvt_pk_bf16_f32 v123,v14,v15
v_mfma_f32_16x16x32_bf16 v[188:191],a[188:191],v[228:231],v[36:39]
.p2align 3
v_accvgpr_read_b32 v124,a120
v_accvgpr_read_b32 v125,a121
v_accvgpr_read_b32 v126,a122
v_accvgpr_read_b32 v127,a123
v_accvgpr_read_b32 v12,a124
v_accvgpr_read_b32 v13,a125
v_accvgpr_read_b32 v14,a126
v_accvgpr_read_b32 v15,a127
v_pk_fma_f32 v[124:125],v[22:23],v[124:125],v[22:23] op_sel:[0,0,1] op_sel_hi:[0,1,1]
v_pk_fma_f32 v[126:127],v[22:23],v[126:127],v[22:23] op_sel:[0,0,1] op_sel_hi:[0,1,1]
v_pk_fma_f32 v[12:13],v[22:23],v[12:13],v[22:23] op_sel:[0,0,1] op_sel_hi:[0,1,1]
v_pk_fma_f32 v[14:15],v[22:23],v[14:15],v[22:23] op_sel:[0,0,1] op_sel_hi:[0,1,1]
v_cvt_pk_bf16_f32 v124,v124,v125
v_cvt_pk_bf16_f32 v125,v126,v127
v_cvt_pk_bf16_f32 v126,v12,v13
v_cvt_pk_bf16_f32 v127,v14,v15
v_mfma_f32_16x16x32_bf16 v[188:191],a[184:187],v[196:199],v[188:191]
v_cmp_nlt_f32 s[58:59],v60,s63
s_nop 1
s_or_b64 s[56:57],s[56:57],s[58:59]
v_mfma_f32_16x16x32_bf16 v[188:191],a[180:183],v[164:167],v[188:191]
v_mov_b32 v60,0
v_add_u32 v3,s48,v3
v_mfma_f32_16x16x32_bf16 v[188:191],a[176:179],v[132:135],v[188:191]
s_mov_b32 s48,32768
v_exp_f32 v136,v136
v_mfma_f32_16x16x32_bf16 a[96:99],a[192:195],v[232:235],0.15915494
ds_read_b64_tr_b16 a[192:193],v2
ds_read_b64_tr_b16 a[194:195],v2 offset:4096
v_mfma_f32_16x16x32_bf16 a[96:99],a[196:199],v[236:239],a[96:99]
ds_read_b64_tr_b16 a[196:197],v2 offset:8192
v_min_f32 v18,v18,v19
v_mfma_f32_16x16x32_bf16 a[100:103],a[204:207],v[236:239],0.15915494
ds_read_b64_tr_b16 a[198:199],v2 offset:12288
v_max3_f32 v19,|v96|,|v97|,|v98|
v_mfma_f32_16x16x32_bf16 a[100:103],a[200:203],v[232:235],a[100:103]
ds_read_b64_tr_b16 a[204:205],v3 offset:8192
v_max3_f32 v19,v19,|v99|,s100
v_mfma_f32_16x16x32_bf16 a[104:107],a[208:211],v[232:235],0.15915494
ds_read_b64_tr_b16 a[206:207],v3 offset:12288
ds_read_b64_tr_b16 a[200:201],v3
v_mfma_f32_16x16x32_bf16 a[104:107],a[212:215],v[236:239],a[104:107]
ds_read_b64_tr_b16 a[202:203],v3 offset:4096
v_fmac_f32 v19,0x39800000,v21
v_mfma_f32_16x16x32_bf16 a[108:111],a[220:223],v[236:239],0.15915494
v_exp_f32 v137,v137
v_mfma_f32_16x16x32_bf16 a[108:111],a[216:219],v[232:235],a[108:111]
ds_read_b64_tr_b16 a[208:209],v2 offset:256
ds_read_b64_tr_b16 a[210:211],v2 offset:4352
v_mfma_f32_16x16x32_bf16 a[112:115],a[224:227],v[232:235],0.15915494
buffer_load_dwordx4 v4,s[24:27],s47 offen offset:2048 nt lds
v_mfma_f32_16x16x32_bf16 a[112:115],a[228:231],v[236:239],a[112:115]
v_exp_f32 v138,v138
v_mfma_f32_16x16x32_bf16 a[116:119],a[236:239],v[236:239],0.15915494
v_exp_f32 v139,v139
v_mfma_f32_16x16x32_bf16 a[116:119],a[232:235],v[232:235],a[116:119]
ds_read_b64_tr_b16 a[212:213],v2 offset:8448
v_cvt_pk_bf16_f32 v136,v136,v137
v_mfma_f32_16x16x32_bf16 a[120:123],a[240:243],v[232:235],0.15915494
v_exp_f32 v140,v140
v_mfma_f32_16x16x32_bf16 a[120:123],a[244:247],v[236:239],a[120:123]
v_exp_f32 v141,v141
v_mfma_f32_16x16x32_bf16 a[124:127],a[252:255],v[236:239],0.15915494
v_exp_f32 v142,v142
v_mfma_f32_16x16x32_bf16 a[124:127],a[248:251],v[232:235],a[124:127]
v_exp_f32 v143,v143
v_smfmac_f32_16x16x64_bf16 v[60:63],v[28:31],v[232:239],v27
ds_read_b64_tr_b16 a[214:215],v2 offset:12544
v_mfma_f32_16x16x32_bf16 v[200:203],a[128:131],v[144:147],v[40:43]
v_cvt_pk_bf16_f32 v137,v138,v139
v_cvt_pk_bf16_f32 v138,v140,v141
v_mfma_f32_16x16x32_bf16 v[200:203],a[132:135],v[176:179],v[200:203]
ds_read_b64_tr_b16 a[220:221],v3 offset:8448
v_cvt_pk_bf16_f32 v139,v142,v143
v_mfma_f32_16x16x32_bf16 v[200:203],a[136:139],v[208:211],v[200:203]
v_exp_f32 v152,v152
v_mfma_f32_16x16x32_bf16 v[200:203],a[140:143],v[240:243],v[200:203]
v_exp_f32 v153,v153
v_mfma_f32_16x16x32_bf16 v[204:207],a[156:159],v[240:243],v[40:43]
v_exp_f32 v154,v154
v_mfma_f32_16x16x32_bf16 v[204:207],a[152:155],v[208:211],v[204:207]
buffer_load_dwordx4 v5,s[24:27],s47 offen offset:3072 nt lds
v_mfma_f32_16x16x32_bf16 v[204:207],a[148:151],v[176:179],v[204:207]
s_or_b32 s47,s47,0x18000
v_exp_f32 v155,v155
v_mfma_f32_16x16x32_bf16 v[204:207],a[144:147],v[144:147],v[204:207]
s_addk_i32 s47,4096
v_exp_f32 v156,v156
v_mfma_f32_16x16x32_bf16 v[216:219],a[160:163],v[144:147],v[40:43]
s_and_b32 s47,s47,0x27000
v_exp_f32 v157,v157
v_mfma_f32_16x16x32_bf16 v[216:219],a[164:167],v[176:179],v[216:219]
s_or_b32 s47,s47,s46
v_exp_f32 v158,v158
v_mfma_f32_16x16x32_bf16 v[216:219],a[168:171],v[208:211],v[216:219]
v_exp_f32 v159,v159
v_mfma_f32_16x16x32_bf16 v[216:219],a[172:175],v[240:243],v[216:219]
ds_read_b64_tr_b16 a[222:223],v3 offset:12544
v_cvt_pk_bf16_f32 v140,v152,v153
v_mfma_f32_16x16x32_bf16 v[220:223],a[188:191],v[240:243],v[40:43]
v_cvt_pk_bf16_f32 v141,v154,v155
v_cvt_pk_bf16_f32 v142,v156,v157
v_mfma_f32_16x16x32_bf16 v[220:223],a[184:187],v[208:211],v[220:223]
ds_read_b64_tr_b16 a[216:217],v3 offset:256
v_cvt_pk_bf16_f32 v143,v158,v159
v_mfma_f32_16x16x32_bf16 v[220:223],a[180:183],v[176:179],v[220:223]
ds_read_b64_tr_b16 a[218:219],v3 offset:4352
ds_read_b64_tr_b16 a[224:225],v2 offset:512
v_mfma_f32_16x16x32_bf16 v[220:223],a[176:179],v[144:147],v[220:223]
s_waitcnt lgkmcnt(4)
ds_read_b64_tr_b16 a[226:227],v2 offset:4608
v_min_f32 v18,v18,v19
v_mfma_f32_16x16x32_bf16 a[0:3],a[192:195],v[136:139],a[0:3]
ds_read_b64_tr_b16 a[228:229],v2 offset:8704
ds_read_b64_tr_b16 a[230:231],v2 offset:12800
v_mfma_f32_16x16x32_bf16 a[0:3],a[196:199],v[140:143],a[0:3]
ds_read_b64_tr_b16 a[236:237],v3 offset:8704
ds_read_b64_tr_b16 a[238:239],v3 offset:12800
v_mfma_f32_16x16x32_bf16 a[4:7],a[204:207],v[140:143],a[4:7]
ds_read_b64_tr_b16 a[232:233],v3 offset:512
ds_read_b64_tr_b16 a[234:235],v3 offset:4608
v_mfma_f32_16x16x32_bf16 a[4:7],a[200:203],v[136:139],a[4:7]
ds_read_b64_tr_b16 a[240:241],v2 offset:768
ds_read_b64_tr_b16 a[242:243],v2 offset:4864
v_mfma_f32_16x16x32_bf16 a[8:11],a[208:211],v[136:139],a[8:11]
s_memrealtime s[76:77]
ds_read_b64_tr_b16 a[244:245],v2 offset:8960
ds_read_b64_tr_b16 a[246:247],v2 offset:13056
v_mfma_f32_16x16x32_bf16 a[8:11],a[212:215],v[140:143],a[8:11]
s_waitcnt lgkmcnt(13)
ds_read_b64_tr_b16 a[252:253],v3 offset:8960
ds_read_b64_tr_b16 a[254:255],v3 offset:13056
v_mfma_f32_16x16x32_bf16 a[12:15],a[220:223],v[140:143],a[12:15]
s_waitcnt lgkmcnt(13)
ds_read_b64_tr_b16 a[248:249],v3 offset:768
ds_read_b64_tr_b16 a[250:251],v3 offset:4864
v_mfma_f32_16x16x32_bf16 a[12:15],a[216:219],v[136:139],a[12:15]
s_waitcnt lgkmcnt(14)
v_mfma_f32_16x16x32_bf16 a[16:19],a[224:227],v[136:139],a[16:19]
s_waitcnt lgkmcnt(12)
v_mfma_f32_16x16x32_bf16 a[16:19],a[228:231],v[140:143],a[16:19]
s_waitcnt lgkmcnt(10)
v_mfma_f32_16x16x32_bf16 a[20:23],a[236:239],v[140:143],a[20:23]
s_waitcnt lgkmcnt(0)
v_max3_f32 v19,|v112|,|v113|,|v114|
v_max3_f32 v19,v19,|v115|,s101
v_mfma_f32_16x16x32_bf16 a[20:23],a[232:235],v[136:139],a[20:23]
s_sub_u32 s79,s78,s76
v_fmac_f32 v19,0x39800000,v23
v_min_f32 v18,v18,v19
v_mfma_f32_16x16x32_bf16 a[24:27],a[240:243],v[136:139],a[24:27]
s_add_u32 s78,s76,s60
v_exp_f32 v168,v168
v_mfma_f32_16x16x32_bf16 a[24:27],a[244:247],v[140:143],a[24:27]
s_cmp_gt_i32 s79,s95
s_cbranch_scc1 L_ps6_%=
L_pr6_%=:
v_exp_f32 v169,v169
v_exp_f32 v170,v170
v_exp_f32 v171,v171
v_mfma_f32_16x16x32_bf16 a[28:31],a[252:255],v[140:143],a[28:31]
v_cvt_pk_bf16_f32 v168,v168,v169
v_exp_f32 v172,v172
v_exp_f32 v173,v173
v_exp_f32 v174,v174
v_exp_f32 v175,v175
v_cmp_gt_f32 s[58:59],0,v18
s_nop 1
s_or_b64 s[56:57],s[56:57],s[58:59]
v_cvt_pk_bf16_f32 v169,v170,v171
v_exp_f32 v184,v184
v_exp_f32 v185,v185
v_exp_f32 v186,v186
v_exp_f32 v187,v187
v_exp_f32 v188,v188
v_exp_f32 v189,v189
v_exp_f32 v190,v190
v_exp_f32 v191,v191
v_mfma_f32_16x16x32_bf16 a[28:31],a[248:251],v[136:139],a[28:31]
s_cmp_lg_u64 s[56:57],0
s_cbranch_scc1 L_slow0_%=
L_sret0_%=:
v_cvt_pk_bf16_f32 v170,v172,v173
)A"
R"A(v_cvt_pk_bf16_f32 v171,v174,v175
v_add_u32 v1,s48,v1
.p2align 3
v_cvt_pk_bf16_f32 v172,v184,v185
v_cvt_pk_bf16_f32 v173,v186,v187
v_cvt_pk_bf16_f32 v174,v188,v189
v_cvt_pk_bf16_f32 v175,v190,v191
v_smfmac_f32_16x16x64_bf16 v[48:51],v[28:31],v[136:143],v27
s_mov_b32 s53,11
s_cmp_eq_u32 s40,3
s_cbranch_scc0 L_att1_%=
s_cmp_gt_i32 s18,0
s_cbranch_scc1 L_att1v0_%=
s_cmp_gt_i32 s19,0
s_cbranch_scc1 L_att1v1_%=
s_cmp_gt_i32 s100,0
s_cbranch_scc1 L_att1v2_%=
.p2align 5
L_att1v3_%=:
s_waitcnt vmcnt(8) lgkmcnt(0)
v_exp_f32 v200,v200
s_barrier
ds_read_b128 a[128:131],v1
v_add_u32 v2,s48,v2
s_mov_b32 s90,L_out_end_%=-L_pc_%=+128
v_exp_f32 v201,v201
s_add_u32 m0,s49,s51
ds_read_b128 a[132:135],v1 offset:256
v_cvt_pk_bf16_f32 v200,v200,v201
s_lshr_b32 s45,s45,1
v_exp_f32 v202,v202
ds_read_b128 a[136:139],v1 offset:512
v_add_u32 v3,s48,v3
buffer_load_dwordx4 v4,s[20:23],s47 offen nt lds
v_exp_f32 v203,v203
s_add_u32 s62,s50,32768
ds_read_b128 a[140:143],v1 offset:768
v_cvt_pk_bf16_f32 v201,v202,v203
s_and_b32 s62,s62,131071
v_exp_f32 v204,v204
s_addk_i32 s54,16384
v_exp_f32 v205,v205
s_sub_u32 s48,s62,s50
ds_read_b128 a[156:159],v1 offset:4864
v_cvt_pk_bf16_f32 v202,v204,v205
s_sub_u32 s53,s53,1
v_exp_f32 v206,v206
s_and_b32 s46,s54,0xfffc0000
v_exp_f32 v207,v207
s_mov_b32 s50,s62
ds_read_b128 a[152:155],v1 offset:4608
v_cvt_pk_bf16_f32 v203,v206,v207
s_xor_b32 s49,s50,65536
v_exp_f32 v216,v216
v_mfma_f32_16x16x32_bf16 a[32:35],a[192:195],v[168:171],a[32:35]
ds_read_b128 a[148:151],v1 offset:4352
v_mfma_f32_16x16x32_bf16 a[32:35],a[196:199],v[172:175],a[32:35]
v_exp_f32 v217,v217
v_mfma_f32_16x16x32_bf16 a[36:39],a[204:207],v[172:175],a[36:39]
ds_read_b128 a[144:147],v1 offset:4096
v_cvt_pk_bf16_f32 v204,v216,v217
v_mfma_f32_16x16x32_bf16 a[36:39],a[200:203],v[168:171],a[36:39]
v_exp_f32 v218,v218
v_mfma_f32_16x16x32_bf16 a[40:43],a[208:211],v[168:171],a[40:43]
buffer_load_dwordx4 v4,s[20:23],s47 offen offset:1024 nt lds
v_mfma_f32_16x16x32_bf16 a[40:43],a[212:215],v[172:175],a[40:43]
ds_read_b128 a[160:163],v1 offset:8192
v_mfma_f32_16x16x32_bf16 a[44:47],a[220:223],v[172:175],a[44:47]
v_exp_f32 v219,v219
v_mfma_f32_16x16x32_bf16 a[44:47],a[216:219],v[168:171],a[44:47]
ds_read_b128 a[164:167],v1 offset:8448
v_cvt_pk_bf16_f32 v205,v218,v219
v_mfma_f32_16x16x32_bf16 a[48:51],a[224:227],v[168:171],a[48:51]
v_exp_f32 v220,v220
v_mfma_f32_16x16x32_bf16 a[48:51],a[228:231],v[172:175],a[48:51]
ds_read_b128 a[168:171],v1 offset:8704
v_mfma_f32_16x16x32_bf16 a[52:55],a[236:239],v[172:175],a[52:55]
v_exp_f32 v221,v221
v_mfma_f32_16x16x32_bf16 a[52:55],a[232:235],v[168:171],a[52:55]
ds_read_b128 a[172:175],v1 offset:8960
v_cvt_pk_bf16_f32 v206,v220,v221
v_mfma_f32_16x16x32_bf16 a[56:59],a[240:243],v[168:171],a[56:59]
v_exp_f32 v222,v222
v_mfma_f32_16x16x32_bf16 a[56:59],a[244:247],v[172:175],a[56:59]
ds_read_b128 a[188:191],v1 offset:13056
v_mfma_f32_16x16x32_bf16 a[60:63],a[252:255],v[172:175],a[60:63]
v_exp_f32 v223,v223
v_mfma_f32_16x16x32_bf16 a[60:63],a[248:251],v[168:171],a[60:63]
ds_read_b128 a[184:187],v1 offset:12800
v_cvt_pk_bf16_f32 v207,v222,v223
v_smfmac_f32_16x16x64_bf16 v[52:55],v[28:31],v[168:175],v27
s_waitcnt lgkmcnt(4)
v_mfma_f32_16x16x32_bf16 v[136:139],a[128:131],v[128:131],v[32:35]
ds_read_b128 a[180:183],v1 offset:12544
v_mfma_f32_16x16x32_bf16 v[136:139],a[132:135],v[160:163],v[136:139]
buffer_load_dwordx4 v5,s[20:23],s47 offen offset:2048 nt lds
v_mfma_f32_16x16x32_bf16 v[136:139],a[136:139],v[192:195],v[136:139]
ds_read_b128 a[176:179],v1 offset:12288
v_add_u32 v1,s48,v1
v_mfma_f32_16x16x32_bf16 v[136:139],a[140:143],v[224:227],v[136:139]
v_mfma_f32_16x16x32_bf16 v[140:143],a[156:159],v[224:227],v[32:35]
v_mfma_f32_16x16x32_bf16 v[140:143],a[152:155],v[192:195],v[140:143]
v_mfma_f32_16x16x32_bf16 v[140:143],a[148:151],v[160:163],v[140:143]
v_mfma_f32_16x16x32_bf16 v[140:143],a[144:147],v[128:131],v[140:143]
v_mfma_f32_16x16x32_bf16 v[152:155],a[160:163],v[128:131],v[32:35]
v_mfma_f32_16x16x32_bf16 v[152:155],a[164:167],v[160:163],v[152:155]
s_waitcnt lgkmcnt(1)
v_mfma_f32_16x16x32_bf16 v[152:155],a[168:171],v[192:195],v[152:155]
v_mfma_f32_16x16x32_bf16 v[152:155],a[172:175],v[224:227],v[152:155]
v_mfma_f32_16x16x32_bf16 v[156:159],a[188:191],v[224:227],v[32:35]
v_mfma_f32_16x16x32_bf16 v[156:159],a[184:187],v[192:195],v[156:159]
v_mfma_f32_16x16x32_bf16 v[156:159],a[180:183],v[160:163],v[156:159]
s_waitcnt lgkmcnt(0)
v_mfma_f32_16x16x32_bf16 v[156:159],a[176:179],v[128:131],v[156:159]
buffer_load_dwordx4 v5,s[20:23],s47 offen offset:3072 nt lds
v_mfma_f32_16x16x32_bf16 a[64:67],a[192:195],v[200:203],a[64:67]
v_mfma_f32_16x16x32_bf16 a[64:67],a[196:199],v[204:207],a[64:67]
v_mfma_f32_16x16x32_bf16 a[68:71],a[204:207],v[204:207],a[68:71]
v_mfma_f32_16x16x32_bf16 a[68:71],a[200:203],v[200:203],a[68:71]
v_mfma_f32_16x16x32_bf16 a[72:75],a[208:211],v[200:203],a[72:75]
v_exp_f32 v136,v136
v_mfma_f32_16x16x32_bf16 a[72:75],a[212:215],v[204:207],a[72:75]
v_exp_f32 v137,v137
v_mfma_f32_16x16x32_bf16 a[76:79],a[220:223],v[204:207],a[76:79]
v_exp_f32 v138,v138
v_mfma_f32_16x16x32_bf16 a[76:79],a[216:219],v[200:203],a[76:79]
v_exp_f32 v139,v139
v_mfma_f32_16x16x32_bf16 a[80:83],a[224:227],v[200:203],a[80:83]
v_exp_f32 v140,v140
v_mfma_f32_16x16x32_bf16 a[80:83],a[228:231],v[204:207],a[80:83]
v_exp_f32 v141,v141
v_mfma_f32_16x16x32_bf16 a[84:87],a[236:239],v[204:207],a[84:87]
s_addk_i32 m0,16384
v_exp_f32 v142,v142
v_mfma_f32_16x16x32_bf16 a[84:87],a[232:235],v[200:203],a[84:87]
v_exp_f32 v143,v143
v_mfma_f32_16x16x32_bf16 a[88:91],a[240:243],v[200:203],a[88:91]
v_exp_f32 v152,v152
v_mfma_f32_16x16x32_bf16 a[88:91],a[244:247],v[204:207],a[88:91]
buffer_load_dwordx4 v4,s[24:27],s47 offen nt lds
v_mfma_f32_16x16x32_bf16 a[92:95],a[252:255],v[204:207],a[92:95]
v_mfma_f32_16x16x32_bf16 a[92:95],a[248:251],v[200:203],a[92:95]
v_smfmac_f32_16x16x64_bf16 v[56:59],v[28:31],v[200:207],v27
v_mfma_f32_16x16x32_bf16 v[168:171],a[128:131],v[132:135],v[36:39]
v_mfma_f32_16x16x32_bf16 v[168:171],a[132:135],v[164:167],v[168:171]
v_mfma_f32_16x16x32_bf16 v[168:171],a[136:139],v[196:199],v[168:171]
v_exp_f32 v153,v153
v_mfma_f32_16x16x32_bf16 v[168:171],a[140:143],v[228:231],v[168:171]
v_exp_f32 v154,v154
v_mfma_f32_16x16x32_bf16 v[172:175],a[156:159],v[228:231],v[36:39]
v_exp_f32 v155,v155
v_mfma_f32_16x16x32_bf16 v[172:175],a[152:155],v[196:199],v[172:175]
v_exp_f32 v156,v156
v_mfma_f32_16x16x32_bf16 v[172:175],a[148:151],v[164:167],v[172:175]
v_exp_f32 v157,v157
v_mfma_f32_16x16x32_bf16 v[172:175],a[144:147],v[132:135],v[172:175]
v_exp_f32 v158,v158
v_mfma_f32_16x16x32_bf16 v[184:187],a[160:163],v[132:135],v[36:39]
v_exp_f32 v159,v159
v_mfma_f32_16x16x32_bf16 v[184:187],a[164:167],v[164:167],v[184:187]
v_exp_f32 v168,v168
v_mfma_f32_16x16x32_bf16 v[184:187],a[168:171],v[196:199],v[184:187]
buffer_load_dwordx4 v5,s[24:27],s47 offen offset:1024 nt lds
v_mfma_f32_16x16x32_bf16 v[184:187],a[172:175],v[228:231],v[184:187]
v_exp_f32 v169,v169
v_mfma_f32_16x16x32_bf16 v[188:191],a[188:191],v[228:231],v[36:39]
v_exp_f32 v170,v170
v_mfma_f32_16x16x32_bf16 v[188:191],a[184:187],v[196:199],v[188:191]
v_exp_f32 v171,v171
v_mfma_f32_16x16x32_bf16 v[188:191],a[180:183],v[164:167],v[188:191]
v_exp_f32 v172,v172
v_mfma_f32_16x16x32_bf16 v[188:191],a[176:179],v[132:135],v[188:191]
v_exp_f32 v173,v173
ds_read_b64_tr_b16 a[192:193],v2
v_cvt_pk_bf16_f32 v168,v168,v169
ds_read_b64_tr_b16 a[194:195],v2 offset:4096
v_cvt_pk_bf16_f32 v169,v170,v171
ds_read_b64_tr_b16 a[196:197],v2 offset:8192
v_cvt_pk_bf16_f32 v170,v172,v173
v_exp_f32 v174,v174
v_exp_f32 v175,v175
ds_read_b64_tr_b16 a[198:199],v2 offset:12288
v_cvt_pk_bf16_f32 v171,v174,v175
v_exp_f32 v184,v184
v_exp_f32 v185,v185
buffer_load_dwordx4 v4,s[24:27],s47 offen offset:2048 nt lds
ds_read_b64_tr_b16 a[204:205],v3 offset:8192
v_cvt_pk_bf16_f32 v172,v184,v185
ds_read_b64_tr_b16 a[206:207],v3 offset:12288
v_cvt_pk_bf16_f32 v136,v136,v137
v_exp_f32 v186,v186
ds_read_b64_tr_b16 a[200:201],v3
ds_read_b64_tr_b16 a[202:203],v3 offset:4096
v_cvt_pk_bf16_f32 v137,v138,v139
ds_read_b64_tr_b16 a[208:209],v2 offset:256
ds_read_b64_tr_b16 a[210:211],v2 offset:4352
ds_read_b64_tr_b16 a[212:213],v2 offset:8448
v_mfma_f32_16x16x32_bf16 v[200:203],a[128:131],v[144:147],v[40:43]
ds_read_b64_tr_b16 a[214:215],v2 offset:12544
v_cvt_pk_bf16_f32 v138,v140,v141
v_mfma_f32_16x16x32_bf16 v[200:203],a[132:135],v[176:179],v[200:203]
ds_read_b64_tr_b16 a[220:221],v3 offset:8448
v_mfma_f32_16x16x32_bf16 v[200:203],a[136:139],v[208:211],v[200:203]
ds_read_b64_tr_b16 a[222:223],v3 offset:12544
v_cvt_pk_bf16_f32 v139,v142,v143
v_mfma_f32_16x16x32_bf16 v[200:203],a[140:143],v[240:243],v[200:203]
ds_read_b64_tr_b16 a[216:217],v3 offset:256
v_mfma_f32_16x16x32_bf16 v[204:207],a[156:159],v[240:243],v[40:43]
ds_read_b64_tr_b16 a[218:219],v3 offset:4352
v_mfma_f32_16x16x32_bf16 v[204:207],a[152:155],v[208:211],v[204:207]
buffer_load_dwordx4 v5,s[24:27],s47 offen offset:3072 nt lds
v_mfma_f32_16x16x32_bf16 v[204:207],a[148:151],v[176:179],v[204:207]
s_or_b32 s47,s47,0x18000
ds_read_b64_tr_b16 a[224:225],v2 offset:512
v_cvt_pk_bf16_f32 v140,v152,v153
v_mfma_f32_16x16x32_bf16 v[204:207],a[144:147],v[144:147],v[204:207]
s_addk_i32 s47,4096
s_and_b32 s47,s47,0x27000
ds_read_b64_tr_b16 a[226:227],v2 offset:4608
v_mfma_f32_16x16x32_bf16 v[216:219],a[160:163],v[144:147],v[40:43]
s_or_b32 s47,s47,s46
ds_read_b64_tr_b16 a[228:229],v2 offset:8704
v_cvt_pk_bf16_f32 v141,v154,v155
v_mfma_f32_16x16x32_bf16 v[216:219],a[164:167],v[176:179],v[216:219]
ds_read_b64_tr_b16 a[230:231],v2 offset:12800
v_mfma_f32_16x16x32_bf16 v[216:219],a[168:171],v[208:211],v[216:219]
ds_read_b64_tr_b16 a[236:237],v3 offset:8704
v_mfma_f32_16x16x32_bf16 v[216:219],a[172:175],v[240:243],v[216:219]
ds_read_b64_tr_b16 a[238:239],v3 offset:12800
v_cvt_pk_bf16_f32 v142,v156,v157
v_mfma_f32_16x16x32_bf16 v[220:223],a[188:191],v[240:243],v[40:43]
ds_read_b64_tr_b16 a[232:233],v3 offset:512
v_mfma_f32_16x16x32_bf16 v[220:223],a[184:187],v[208:211],v[220:223]
ds_read_b64_tr_b16 a[234:235],v3 offset:4608
v_mfma_f32_16x16x32_bf16 v[220:223],a[180:183],v[176:179],v[220:223]
ds_read_b64_tr_b16 a[240:241],v2 offset:768
v_cvt_pk_bf16_f32 v143,v158,v159
v_mfma_f32_16x16x32_bf16 v[220:223],a[176:179],v[144:147],v[220:223]
s_waitcnt lgkmcnt(7)
buffer_load_dword v26,v24,s[88:91],s93 offen
v_mfma_f32_16x16x32_bf16 a[0:3],a[192:195],v[136:139],a[0:3]
ds_read_b64_tr_b16 a[242:243],v2 offset:4864
v_mfma_f32_16x16x32_bf16 a[0:3],a[196:199],v[140:143],a[0:3]
ds_read_b64_tr_b16 a[244:245],v2 offset:8960
v_mfma_f32_16x16x32_bf16 a[4:7],a[204:207],v[140:143],a[4:7]
ds_read_b64_tr_b16 a[246:247],v2 offset:13056
v_mfma_f32_16x16x32_bf16 a[4:7],a[200:203],v[136:139],a[4:7]
ds_read_b64_tr_b16 a[252:253],v3 offset:8960
v_mfma_f32_16x16x32_bf16 a[8:11],a[208:211],v[136:139],a[8:11]
ds_read_b64_tr_b16 a[254:255],v3 offset:13056
v_mfma_f32_16x16x32_bf16 a[8:11],a[212:215],v[140:143],a[8:11]
ds_read_b64_tr_b16 a[248:249],v3 offset:768
v_mfma_f32_16x16x32_bf16 a[12:15],a[220:223],v[140:143],a[12:15]
ds_read_b64_tr_b16 a[250:251],v3 offset:4864
v_mfma_f32_16x16x32_bf16 a[12:15],a[216:219],v[136:139],a[12:15]
s_bitcmp1_b32 s45,0
s_cbranch_scc0 L_t31v31_%=
s_mov_b32 m0,s80
s_addk_i32 s80,4096
buffer_load_dwordx4 v25,s[24:27],s83 offen nt lds
s_addk_i32 s83,1024
L_t31v31_%=:
v_mfma_f32_16x16x32_bf16 a[16:19],a[224:227],v[136:139],a[16:19]
s_waitcnt lgkmcnt(6)
v_exp_f32 v187,v187
v_mfma_f32_16x16x32_bf16 a[16:19],a[228:231],v[140:143],a[16:19]
v_exp_f32 v188,v188
v_mfma_f32_16x16x32_bf16 a[20:23],a[236:239],v[140:143],a[20:23]
v_exp_f32 v189,v189
v_mfma_f32_16x16x32_bf16 a[20:23],a[232:235],v[136:139],a[20:23]
v_exp_f32 v190,v190
v_mfma_f32_16x16x32_bf16 a[24:27],a[240:243],v[136:139],a[24:27]
s_waitcnt lgkmcnt(2)
v_exp_f32 v191,v191
v_mfma_f32_16x16x32_bf16 a[24:27],a[244:247],v[140:143],a[24:27]
v_cvt_pk_bf16_f32 v173,v186,v187
v_mfma_f32_16x16x32_bf16 a[28:31],a[252:255],v[140:143],a[28:31]
s_waitcnt lgkmcnt(0)
v_cvt_pk_bf16_f32 v174,v188,v189
v_cvt_pk_bf16_f32 v175,v190,v191
)A"
R"A(v_mfma_f32_16x16x32_bf16 a[28:31],a[248:251],v[136:139],a[28:31]
s_cmp_lg_u32 s53,0
v_smfmac_f32_16x16x64_bf16 v[48:51],v[28:31],v[136:143],v27
s_cbranch_scc1 L_att1v3_%=
s_waitcnt lgkmcnt(0)
s_nop 7
s_branch L_att1x_%=
.p2align 5
L_att1v0_%=:
v_mfma_f32_16x16x32_bf16 v[232:235],a[128:131],v[148:151],v[44:47]
s_waitcnt vmcnt(8) lgkmcnt(0)
v_exp_f32 v200,v200
v_mfma_f32_16x16x32_bf16 v[232:235],a[132:135],v[180:183],v[232:235]
s_barrier
ds_read_b128 a[128:131],v1
v_add_u32 v2,s48,v2
v_mfma_f32_16x16x32_bf16 v[232:235],a[136:139],v[212:215],v[232:235]
s_mov_b32 s90,L_out_end_%=-L_pc_%=+128
v_exp_f32 v201,v201
v_mfma_f32_16x16x32_bf16 v[232:235],a[140:143],v[244:247],v[232:235]
s_add_u32 m0,s49,s51
ds_read_b128 a[132:135],v1 offset:256
v_cvt_pk_bf16_f32 v200,v200,v201
v_mfma_f32_16x16x32_bf16 v[236:239],a[156:159],v[244:247],v[44:47]
s_lshr_b32 s45,s45,1
v_exp_f32 v202,v202
v_mfma_f32_16x16x32_bf16 v[236:239],a[152:155],v[212:215],v[236:239]
ds_read_b128 a[136:139],v1 offset:512
v_add_u32 v3,s48,v3
v_mfma_f32_16x16x32_bf16 v[236:239],a[148:151],v[180:183],v[236:239]
buffer_load_dwordx4 v4,s[20:23],s47 offen nt lds
v_mfma_f32_16x16x32_bf16 v[236:239],a[144:147],v[148:151],v[236:239]
v_exp_f32 v203,v203
v_mfma_f32_16x16x32_bf16 v[248:251],a[160:163],v[148:151],v[44:47]
s_add_u32 s62,s50,32768
ds_read_b128 a[140:143],v1 offset:768
v_cvt_pk_bf16_f32 v201,v202,v203
v_mfma_f32_16x16x32_bf16 v[248:251],a[164:167],v[180:183],v[248:251]
s_and_b32 s62,s62,131071
v_exp_f32 v204,v204
v_mfma_f32_16x16x32_bf16 v[248:251],a[168:171],v[212:215],v[248:251]
s_addk_i32 s54,16384
v_exp_f32 v205,v205
v_mfma_f32_16x16x32_bf16 v[248:251],a[172:175],v[244:247],v[248:251]
s_sub_u32 s48,s62,s50
ds_read_b128 a[156:159],v1 offset:4864
v_cvt_pk_bf16_f32 v202,v204,v205
v_mfma_f32_16x16x32_bf16 v[252:255],a[188:191],v[244:247],v[44:47]
s_sub_u32 s53,s53,1
v_exp_f32 v206,v206
v_mfma_f32_16x16x32_bf16 v[252:255],a[184:187],v[212:215],v[252:255]
s_and_b32 s46,s54,0xfffc0000
v_exp_f32 v207,v207
v_mfma_f32_16x16x32_bf16 v[252:255],a[180:183],v[180:183],v[252:255]
s_mov_b32 s50,s62
ds_read_b128 a[152:155],v1 offset:4608
v_cvt_pk_bf16_f32 v203,v206,v207
v_mfma_f32_16x16x32_bf16 v[252:255],a[176:179],v[148:151],v[252:255]
s_xor_b32 s49,s50,65536
v_exp_f32 v216,v216
v_mfma_f32_16x16x32_bf16 a[32:35],a[192:195],v[168:171],a[32:35]
ds_read_b128 a[148:151],v1 offset:4352
v_mfma_f32_16x16x32_bf16 a[32:35],a[196:199],v[172:175],a[32:35]
v_exp_f32 v217,v217
v_mfma_f32_16x16x32_bf16 a[36:39],a[204:207],v[172:175],a[36:39]
ds_read_b128 a[144:147],v1 offset:4096
v_cvt_pk_bf16_f32 v204,v216,v217
v_mfma_f32_16x16x32_bf16 a[36:39],a[200:203],v[168:171],a[36:39]
v_exp_f32 v218,v218
v_mfma_f32_16x16x32_bf16 a[40:43],a[208:211],v[168:171],a[40:43]
buffer_load_dwordx4 v4,s[20:23],s47 offen offset:1024 nt lds
v_mfma_f32_16x16x32_bf16 a[40:43],a[212:215],v[172:175],a[40:43]
ds_read_b128 a[160:163],v1 offset:8192
v_mfma_f32_16x16x32_bf16 a[44:47],a[220:223],v[172:175],a[44:47]
v_exp_f32 v219,v219
v_mfma_f32_16x16x32_bf16 a[44:47],a[216:219],v[168:171],a[44:47]
ds_read_b128 a[164:167],v1 offset:8448
v_cvt_pk_bf16_f32 v205,v218,v219
v_mfma_f32_16x16x32_bf16 a[48:51],a[224:227],v[168:171],a[48:51]
v_exp_f32 v220,v220
v_mfma_f32_16x16x32_bf16 a[48:51],a[228:231],v[172:175],a[48:51]
ds_read_b128 a[168:171],v1 offset:8704
v_mfma_f32_16x16x32_bf16 a[52:55],a[236:239],v[172:175],a[52:55]
v_exp_f32 v221,v221
v_mfma_f32_16x16x32_bf16 a[52:55],a[232:235],v[168:171],a[52:55]
ds_read_b128 a[172:175],v1 offset:8960
v_cvt_pk_bf16_f32 v206,v220,v221
v_mfma_f32_16x16x32_bf16 a[56:59],a[240:243],v[168:171],a[56:59]
v_exp_f32 v222,v222
v_mfma_f32_16x16x32_bf16 a[56:59],a[244:247],v[172:175],a[56:59]
ds_read_b128 a[188:191],v1 offset:13056
v_mfma_f32_16x16x32_bf16 a[60:63],a[252:255],v[172:175],a[60:63]
v_exp_f32 v223,v223
v_mfma_f32_16x16x32_bf16 a[60:63],a[248:251],v[168:171],a[60:63]
ds_read_b128 a[184:187],v1 offset:12800
v_cvt_pk_bf16_f32 v207,v222,v223
v_smfmac_f32_16x16x64_bf16 v[52:55],v[28:31],v[168:175],v27
ds_read_b128 a[180:183],v1 offset:12544
buffer_load_dwordx4 v5,s[20:23],s47 offen offset:2048 nt lds
ds_read_b128 a[176:179],v1 offset:12288
v_add_u32 v1,s48,v1
v_exp_f32 v232,v232
v_exp_f32 v233,v233
v_exp_f32 v234,v234
v_exp_f32 v235,v235
v_exp_f32 v236,v236
v_exp_f32 v237,v237
v_exp_f32 v238,v238
v_exp_f32 v239,v239
v_exp_f32 v248,v248
v_exp_f32 v249,v249
v_exp_f32 v250,v250
v_exp_f32 v251,v251
buffer_load_dwordx4 v5,s[20:23],s47 offen offset:3072 nt lds
v_mfma_f32_16x16x32_bf16 a[64:67],a[192:195],v[200:203],a[64:67]
v_exp_f32 v252,v252
v_mfma_f32_16x16x32_bf16 a[64:67],a[196:199],v[204:207],a[64:67]
v_exp_f32 v253,v253
v_mfma_f32_16x16x32_bf16 a[68:71],a[204:207],v[204:207],a[68:71]
v_exp_f32 v254,v254
v_mfma_f32_16x16x32_bf16 a[68:71],a[200:203],v[200:203],a[68:71]
v_exp_f32 v255,v255
v_mfma_f32_16x16x32_bf16 a[72:75],a[208:211],v[200:203],a[72:75]
v_mfma_f32_16x16x32_bf16 a[72:75],a[212:215],v[204:207],a[72:75]
v_mfma_f32_16x16x32_bf16 a[76:79],a[220:223],v[204:207],a[76:79]
v_mfma_f32_16x16x32_bf16 a[76:79],a[216:219],v[200:203],a[76:79]
v_mfma_f32_16x16x32_bf16 a[80:83],a[224:227],v[200:203],a[80:83]
v_mfma_f32_16x16x32_bf16 a[80:83],a[228:231],v[204:207],a[80:83]
v_mfma_f32_16x16x32_bf16 a[84:87],a[236:239],v[204:207],a[84:87]
s_addk_i32 m0,16384
v_mfma_f32_16x16x32_bf16 a[84:87],a[232:235],v[200:203],a[84:87]
v_mfma_f32_16x16x32_bf16 a[88:91],a[240:243],v[200:203],a[88:91]
v_mfma_f32_16x16x32_bf16 a[88:91],a[244:247],v[204:207],a[88:91]
buffer_load_dwordx4 v4,s[24:27],s47 offen nt lds
v_mfma_f32_16x16x32_bf16 a[92:95],a[252:255],v[204:207],a[92:95]
v_cvt_pk_bf16_f32 v232,v232,v233
v_cvt_pk_bf16_f32 v233,v234,v235
v_mfma_f32_16x16x32_bf16 a[92:95],a[248:251],v[200:203],a[92:95]
v_cvt_pk_bf16_f32 v234,v236,v237
v_cvt_pk_bf16_f32 v235,v238,v239
v_smfmac_f32_16x16x64_bf16 v[56:59],v[28:31],v[200:207],v27
s_waitcnt lgkmcnt(0)
v_cvt_pk_bf16_f32 v236,v248,v249
v_mfma_f32_16x16x32_bf16 v[168:171],a[128:131],v[132:135],v[36:39]
v_cvt_pk_bf16_f32 v237,v250,v251
v_cvt_pk_bf16_f32 v238,v252,v253
v_mfma_f32_16x16x32_bf16 v[168:171],a[132:135],v[164:167],v[168:171]
v_cvt_pk_bf16_f32 v239,v254,v255
v_mfma_f32_16x16x32_bf16 v[168:171],a[136:139],v[196:199],v[168:171]
v_mfma_f32_16x16x32_bf16 v[168:171],a[140:143],v[228:231],v[168:171]
v_mfma_f32_16x16x32_bf16 v[172:175],a[156:159],v[228:231],v[36:39]
v_mfma_f32_16x16x32_bf16 v[172:175],a[152:155],v[196:199],v[172:175]
v_mfma_f32_16x16x32_bf16 v[172:175],a[148:151],v[164:167],v[172:175]
v_mfma_f32_16x16x32_bf16 v[172:175],a[144:147],v[132:135],v[172:175]
v_mfma_f32_16x16x32_bf16 v[184:187],a[160:163],v[132:135],v[36:39]
v_mfma_f32_16x16x32_bf16 v[184:187],a[164:167],v[164:167],v[184:187]
s_nop 1
v_exp_f32 v168,v168
v_mfma_f32_16x16x32_bf16 v[184:187],a[168:171],v[196:199],v[184:187]
buffer_load_dwordx4 v5,s[24:27],s47 offen offset:1024 nt lds
v_mfma_f32_16x16x32_bf16 v[184:187],a[172:175],v[228:231],v[184:187]
v_exp_f32 v169,v169
v_mfma_f32_16x16x32_bf16 v[188:191],a[188:191],v[228:231],v[36:39]
v_exp_f32 v170,v170
v_mfma_f32_16x16x32_bf16 v[188:191],a[184:187],v[196:199],v[188:191]
v_exp_f32 v171,v171
v_mfma_f32_16x16x32_bf16 v[188:191],a[180:183],v[164:167],v[188:191]
v_exp_f32 v172,v172
v_mfma_f32_16x16x32_bf16 v[188:191],a[176:179],v[132:135],v[188:191]
v_exp_f32 v173,v173
v_mfma_f32_16x16x32_bf16 a[96:99],a[192:195],v[232:235],a[96:99]
ds_read_b64_tr_b16 a[192:193],v2
v_cvt_pk_bf16_f32 v168,v168,v169
v_mfma_f32_16x16x32_bf16 a[96:99],a[196:199],v[236:239],a[96:99]
ds_read_b64_tr_b16 a[194:195],v2 offset:4096
v_cvt_pk_bf16_f32 v169,v170,v171
v_mfma_f32_16x16x32_bf16 a[100:103],a[204:207],v[236:239],a[100:103]
ds_read_b64_tr_b16 a[196:197],v2 offset:8192
v_cvt_pk_bf16_f32 v170,v172,v173
v_mfma_f32_16x16x32_bf16 a[100:103],a[200:203],v[232:235],a[100:103]
v_exp_f32 v174,v174
v_mfma_f32_16x16x32_bf16 a[104:107],a[208:211],v[232:235],a[104:107]
v_exp_f32 v175,v175
v_mfma_f32_16x16x32_bf16 a[104:107],a[212:215],v[236:239],a[104:107]
ds_read_b64_tr_b16 a[198:199],v2 offset:12288
v_cvt_pk_bf16_f32 v171,v174,v175
v_mfma_f32_16x16x32_bf16 a[108:111],a[220:223],v[236:239],a[108:111]
v_exp_f32 v184,v184
v_mfma_f32_16x16x32_bf16 a[108:111],a[216:219],v[232:235],a[108:111]
v_exp_f32 v185,v185
v_mfma_f32_16x16x32_bf16 a[112:115],a[224:227],v[232:235],a[112:115]
buffer_load_dwordx4 v4,s[24:27],s47 offen offset:2048 nt lds
v_mfma_f32_16x16x32_bf16 a[112:115],a[228:231],v[236:239],a[112:115]
ds_read_b64_tr_b16 a[204:205],v3 offset:8192
v_cvt_pk_bf16_f32 v172,v184,v185
v_mfma_f32_16x16x32_bf16 a[116:119],a[236:239],v[236:239],a[116:119]
ds_read_b64_tr_b16 a[206:207],v3 offset:12288
v_mfma_f32_16x16x32_bf16 a[116:119],a[232:235],v[232:235],a[116:119]
v_exp_f32 v186,v186
v_mfma_f32_16x16x32_bf16 a[120:123],a[240:243],v[232:235],a[120:123]
ds_read_b64_tr_b16 a[200:201],v3
v_mfma_f32_16x16x32_bf16 a[120:123],a[244:247],v[236:239],a[120:123]
ds_read_b64_tr_b16 a[202:203],v3 offset:4096
v_mfma_f32_16x16x32_bf16 a[124:127],a[252:255],v[236:239],a[124:127]
ds_read_b64_tr_b16 a[208:209],v2 offset:256
v_mfma_f32_16x16x32_bf16 a[124:127],a[248:251],v[232:235],a[124:127]
ds_read_b64_tr_b16 a[210:211],v2 offset:4352
v_smfmac_f32_16x16x64_bf16 v[60:63],v[28:31],v[232:239],v27
ds_read_b64_tr_b16 a[212:213],v2 offset:8448
v_mfma_f32_16x16x32_bf16 v[200:203],a[128:131],v[144:147],v[40:43]
ds_read_b64_tr_b16 a[214:215],v2 offset:12544
v_mfma_f32_16x16x32_bf16 v[200:203],a[132:135],v[176:179],v[200:203]
ds_read_b64_tr_b16 a[220:221],v3 offset:8448
v_mfma_f32_16x16x32_bf16 v[200:203],a[136:139],v[208:211],v[200:203]
ds_read_b64_tr_b16 a[222:223],v3 offset:12544
v_mfma_f32_16x16x32_bf16 v[200:203],a[140:143],v[240:243],v[200:203]
ds_read_b64_tr_b16 a[216:217],v3 offset:256
v_mfma_f32_16x16x32_bf16 v[204:207],a[156:159],v[240:243],v[40:43]
ds_read_b64_tr_b16 a[218:219],v3 offset:4352
v_mfma_f32_16x16x32_bf16 v[204:207],a[152:155],v[208:211],v[204:207]
buffer_load_dwordx4 v5,s[24:27],s47 offen offset:3072 nt lds
v_mfma_f32_16x16x32_bf16 v[204:207],a[148:151],v[176:179],v[204:207]
s_or_b32 s47,s47,0x18000
ds_read_b64_tr_b16 a[224:225],v2 offset:512
v_mfma_f32_16x16x32_bf16 v[204:207],a[144:147],v[144:147],v[204:207]
s_addk_i32 s47,4096
s_and_b32 s47,s47,0x27000
ds_read_b64_tr_b16 a[226:227],v2 offset:4608
v_mfma_f32_16x16x32_bf16 v[216:219],a[160:163],v[144:147],v[40:43]
s_or_b32 s47,s47,s46
ds_read_b64_tr_b16 a[228:229],v2 offset:8704
v_mfma_f32_16x16x32_bf16 v[216:219],a[164:167],v[176:179],v[216:219]
ds_read_b64_tr_b16 a[230:231],v2 offset:12800
v_mfma_f32_16x16x32_bf16 v[216:219],a[168:171],v[208:211],v[216:219]
ds_read_b64_tr_b16 a[236:237],v3 offset:8704
v_mfma_f32_16x16x32_bf16 v[216:219],a[172:175],v[240:243],v[216:219]
ds_read_b64_tr_b16 a[238:239],v3 offset:12800
v_mfma_f32_16x16x32_bf16 v[220:223],a[188:191],v[240:243],v[40:43]
ds_read_b64_tr_b16 a[232:233],v3 offset:512
v_mfma_f32_16x16x32_bf16 v[220:223],a[184:187],v[208:211],v[220:223]
ds_read_b64_tr_b16 a[234:235],v3 offset:4608
v_mfma_f32_16x16x32_bf16 v[220:223],a[180:183],v[176:179],v[220:223]
ds_read_b64_tr_b16 a[240:241],v2 offset:768
v_mfma_f32_16x16x32_bf16 v[220:223],a[176:179],v[144:147],v[220:223]
buffer_load_dword v26,v24,s[88:91],s93 offen
ds_read_b64_tr_b16 a[242:243],v2 offset:4864
ds_read_b64_tr_b16 a[244:245],v2 offset:8960
ds_read_b64_tr_b16 a[246:247],v2 offset:13056
ds_read_b64_tr_b16 a[252:253],v3 offset:8960
ds_read_b64_tr_b16 a[254:255],v3 offset:13056
ds_read_b64_tr_b16 a[248:249],v3 offset:768
ds_read_b64_tr_b16 a[250:251],v3 offset:4864
s_bitcmp1_b32 s45,0
s_cbranch_scc0 L_t31v01_%=
s_mov_b32 m0,s80
s_addk_i32 s80,4096
buffer_load_dwordx4 v25,s[24:27],s83 offen nt lds
s_addk_i32 s83,1024
L_t31v01_%=:
v_exp_f32 v187,v187
v_exp_f32 v188,v188
v_exp_f32 v189,v189
v_exp_f32 v190,v190
v_exp_f32 v191,v191
.p2align 3
v_cvt_pk_bf16_f32 v173,v186,v187
v_cvt_pk_bf16_f32 v174,v188,v189
v_cvt_pk_bf16_f32 v175,v190,v191
s_cmp_lg_u32 s53,0
s_cbranch_scc1 L_att1v0_%=
s_waitcnt lgkmcnt(0)
s_nop 7
s_branch L_att1x_%=
.p2align 5
L_att1v1_%=:
v_mfma_f32_16x16x32_bf16 v[232:235],a[128:131],v[148:151],v[44:47]
)A"
R"A(s_waitcnt vmcnt(8) lgkmcnt(0)
v_exp_f32 v200,v200
v_mfma_f32_16x16x32_bf16 v[232:235],a[132:135],v[180:183],v[232:235]
s_barrier
ds_read_b128 a[128:131],v1
v_add_u32 v2,s48,v2
v_mfma_f32_16x16x32_bf16 v[232:235],a[136:139],v[212:215],v[232:235]
s_mov_b32 s90,L_out_end_%=-L_pc_%=+128
v_exp_f32 v201,v201
v_mfma_f32_16x16x32_bf16 v[232:235],a[140:143],v[244:247],v[232:235]
s_add_u32 m0,s49,s51
ds_read_b128 a[132:135],v1 offset:256
v_cvt_pk_bf16_f32 v200,v200,v201
v_mfma_f32_16x16x32_bf16 v[236:239],a[156:159],v[244:247],v[44:47]
s_lshr_b32 s45,s45,1
v_exp_f32 v202,v202
v_mfma_f32_16x16x32_bf16 v[236:239],a[152:155],v[212:215],v[236:239]
ds_read_b128 a[136:139],v1 offset:512
v_add_u32 v3,s48,v3
v_mfma_f32_16x16x32_bf16 v[236:239],a[148:151],v[180:183],v[236:239]
buffer_load_dwordx4 v4,s[20:23],s47 offen nt lds
v_mfma_f32_16x16x32_bf16 v[236:239],a[144:147],v[148:151],v[236:239]
v_exp_f32 v203,v203
v_mfma_f32_16x16x32_bf16 v[248:251],a[160:163],v[148:151],v[44:47]
s_add_u32 s62,s50,32768
ds_read_b128 a[140:143],v1 offset:768
v_cvt_pk_bf16_f32 v201,v202,v203
v_mfma_f32_16x16x32_bf16 v[248:251],a[164:167],v[180:183],v[248:251]
s_and_b32 s62,s62,131071
v_exp_f32 v204,v204
v_mfma_f32_16x16x32_bf16 v[248:251],a[168:171],v[212:215],v[248:251]
s_addk_i32 s54,16384
v_exp_f32 v205,v205
v_mfma_f32_16x16x32_bf16 v[248:251],a[172:175],v[244:247],v[248:251]
s_sub_u32 s48,s62,s50
ds_read_b128 a[156:159],v1 offset:4864
v_cvt_pk_bf16_f32 v202,v204,v205
v_mfma_f32_16x16x32_bf16 v[252:255],a[188:191],v[244:247],v[44:47]
s_sub_u32 s53,s53,1
v_exp_f32 v206,v206
v_mfma_f32_16x16x32_bf16 v[252:255],a[184:187],v[212:215],v[252:255]
s_and_b32 s46,s54,0xfffc0000
v_exp_f32 v207,v207
v_mfma_f32_16x16x32_bf16 v[252:255],a[180:183],v[180:183],v[252:255]
s_mov_b32 s50,s62
ds_read_b128 a[152:155],v1 offset:4608
v_cvt_pk_bf16_f32 v203,v206,v207
v_mfma_f32_16x16x32_bf16 v[252:255],a[176:179],v[148:151],v[252:255]
s_xor_b32 s49,s50,65536
v_exp_f32 v216,v216
ds_read_b128 a[148:151],v1 offset:4352
v_exp_f32 v217,v217
ds_read_b128 a[144:147],v1 offset:4096
v_cvt_pk_bf16_f32 v204,v216,v217
v_exp_f32 v218,v218
buffer_load_dwordx4 v4,s[20:23],s47 offen offset:1024 nt lds
ds_read_b128 a[160:163],v1 offset:8192
v_exp_f32 v219,v219
ds_read_b128 a[164:167],v1 offset:8448
v_cvt_pk_bf16_f32 v205,v218,v219
v_exp_f32 v220,v220
ds_read_b128 a[168:171],v1 offset:8704
v_exp_f32 v221,v221
ds_read_b128 a[172:175],v1 offset:8960
v_cvt_pk_bf16_f32 v206,v220,v221
v_exp_f32 v222,v222
ds_read_b128 a[188:191],v1 offset:13056
v_exp_f32 v223,v223
ds_read_b128 a[184:187],v1 offset:12800
v_cvt_pk_bf16_f32 v207,v222,v223
s_waitcnt lgkmcnt(11)
v_mfma_f32_16x16x32_bf16 v[136:139],a[128:131],v[128:131],v[32:35]
ds_read_b128 a[180:183],v1 offset:12544
v_mfma_f32_16x16x32_bf16 v[136:139],a[132:135],v[160:163],v[136:139]
buffer_load_dwordx4 v5,s[20:23],s47 offen offset:2048 nt lds
v_mfma_f32_16x16x32_bf16 v[136:139],a[136:139],v[192:195],v[136:139]
s_waitcnt lgkmcnt(11)
ds_read_b128 a[176:179],v1 offset:12288
v_add_u32 v1,s48,v1
v_mfma_f32_16x16x32_bf16 v[136:139],a[140:143],v[224:227],v[136:139]
s_waitcnt lgkmcnt(11)
v_exp_f32 v232,v232
v_mfma_f32_16x16x32_bf16 v[140:143],a[156:159],v[224:227],v[32:35]
s_waitcnt lgkmcnt(10)
v_exp_f32 v233,v233
v_mfma_f32_16x16x32_bf16 v[140:143],a[152:155],v[192:195],v[140:143]
s_waitcnt lgkmcnt(9)
v_exp_f32 v234,v234
v_mfma_f32_16x16x32_bf16 v[140:143],a[148:151],v[160:163],v[140:143]
s_waitcnt lgkmcnt(8)
v_exp_f32 v235,v235
v_mfma_f32_16x16x32_bf16 v[140:143],a[144:147],v[128:131],v[140:143]
s_waitcnt lgkmcnt(2)
v_exp_f32 v236,v236
v_mfma_f32_16x16x32_bf16 v[152:155],a[160:163],v[128:131],v[32:35]
v_exp_f32 v237,v237
v_mfma_f32_16x16x32_bf16 v[152:155],a[164:167],v[160:163],v[152:155]
v_exp_f32 v238,v238
v_mfma_f32_16x16x32_bf16 v[152:155],a[168:171],v[192:195],v[152:155]
v_exp_f32 v239,v239
v_mfma_f32_16x16x32_bf16 v[152:155],a[172:175],v[224:227],v[152:155]
v_exp_f32 v248,v248
v_mfma_f32_16x16x32_bf16 v[156:159],a[188:191],v[224:227],v[32:35]
v_exp_f32 v249,v249
v_mfma_f32_16x16x32_bf16 v[156:159],a[184:187],v[192:195],v[156:159]
s_waitcnt lgkmcnt(0)
v_exp_f32 v250,v250
v_mfma_f32_16x16x32_bf16 v[156:159],a[180:183],v[160:163],v[156:159]
v_exp_f32 v251,v251
v_mfma_f32_16x16x32_bf16 v[156:159],a[176:179],v[128:131],v[156:159]
buffer_load_dwordx4 v5,s[20:23],s47 offen offset:3072 nt lds
v_mfma_f32_16x16x32_bf16 a[64:67],a[192:195],v[200:203],a[64:67]
v_exp_f32 v252,v252
v_mfma_f32_16x16x32_bf16 a[64:67],a[196:199],v[204:207],a[64:67]
v_exp_f32 v253,v253
v_mfma_f32_16x16x32_bf16 a[68:71],a[204:207],v[204:207],a[68:71]
v_exp_f32 v254,v254
v_mfma_f32_16x16x32_bf16 a[68:71],a[200:203],v[200:203],a[68:71]
v_exp_f32 v255,v255
v_mfma_f32_16x16x32_bf16 a[72:75],a[208:211],v[200:203],a[72:75]
v_exp_f32 v136,v136
v_mfma_f32_16x16x32_bf16 a[72:75],a[212:215],v[204:207],a[72:75]
v_exp_f32 v137,v137
v_mfma_f32_16x16x32_bf16 a[76:79],a[220:223],v[204:207],a[76:79]
v_exp_f32 v138,v138
v_mfma_f32_16x16x32_bf16 a[76:79],a[216:219],v[200:203],a[76:79]
v_exp_f32 v139,v139
v_mfma_f32_16x16x32_bf16 a[80:83],a[224:227],v[200:203],a[80:83]
v_exp_f32 v140,v140
v_mfma_f32_16x16x32_bf16 a[80:83],a[228:231],v[204:207],a[80:83]
v_exp_f32 v141,v141
v_mfma_f32_16x16x32_bf16 a[84:87],a[236:239],v[204:207],a[84:87]
s_addk_i32 m0,16384
v_exp_f32 v142,v142
v_mfma_f32_16x16x32_bf16 a[84:87],a[232:235],v[200:203],a[84:87]
v_exp_f32 v143,v143
v_mfma_f32_16x16x32_bf16 a[88:91],a[240:243],v[200:203],a[88:91]
v_exp_f32 v152,v152
v_mfma_f32_16x16x32_bf16 a[88:91],a[244:247],v[204:207],a[88:91]
buffer_load_dwordx4 v4,s[24:27],s47 offen nt lds
v_mfma_f32_16x16x32_bf16 a[92:95],a[252:255],v[204:207],a[92:95]
v_cvt_pk_bf16_f32 v232,v232,v233
v_cvt_pk_bf16_f32 v233,v234,v235
v_mfma_f32_16x16x32_bf16 a[92:95],a[248:251],v[200:203],a[92:95]
v_cvt_pk_bf16_f32 v234,v236,v237
v_cvt_pk_bf16_f32 v235,v238,v239
v_smfmac_f32_16x16x64_bf16 v[56:59],v[28:31],v[200:207],v27
.p2align 3
v_cvt_pk_bf16_f32 v236,v248,v249
v_cvt_pk_bf16_f32 v237,v250,v251
v_cvt_pk_bf16_f32 v238,v252,v253
v_cvt_pk_bf16_f32 v239,v254,v255
v_exp_f32 v153,v153
v_exp_f32 v154,v154
v_exp_f32 v155,v155
v_exp_f32 v156,v156
v_exp_f32 v157,v157
v_exp_f32 v158,v158
v_exp_f32 v159,v159
buffer_load_dwordx4 v5,s[24:27],s47 offen offset:1024 nt lds
v_mfma_f32_16x16x32_bf16 a[96:99],a[192:195],v[232:235],a[96:99]
ds_read_b64_tr_b16 a[192:193],v2
v_mfma_f32_16x16x32_bf16 a[96:99],a[196:199],v[236:239],a[96:99]
ds_read_b64_tr_b16 a[194:195],v2 offset:4096
v_mfma_f32_16x16x32_bf16 a[100:103],a[204:207],v[236:239],a[100:103]
ds_read_b64_tr_b16 a[196:197],v2 offset:8192
v_mfma_f32_16x16x32_bf16 a[100:103],a[200:203],v[232:235],a[100:103]
v_mfma_f32_16x16x32_bf16 a[104:107],a[208:211],v[232:235],a[104:107]
v_mfma_f32_16x16x32_bf16 a[104:107],a[212:215],v[236:239],a[104:107]
ds_read_b64_tr_b16 a[198:199],v2 offset:12288
v_mfma_f32_16x16x32_bf16 a[108:111],a[220:223],v[236:239],a[108:111]
v_mfma_f32_16x16x32_bf16 a[108:111],a[216:219],v[232:235],a[108:111]
v_mfma_f32_16x16x32_bf16 a[112:115],a[224:227],v[232:235],a[112:115]
buffer_load_dwordx4 v4,s[24:27],s47 offen offset:2048 nt lds
v_mfma_f32_16x16x32_bf16 a[112:115],a[228:231],v[236:239],a[112:115]
ds_read_b64_tr_b16 a[204:205],v3 offset:8192
v_mfma_f32_16x16x32_bf16 a[116:119],a[236:239],v[236:239],a[116:119]
ds_read_b64_tr_b16 a[206:207],v3 offset:12288
v_cvt_pk_bf16_f32 v136,v136,v137
v_mfma_f32_16x16x32_bf16 a[116:119],a[232:235],v[232:235],a[116:119]
v_mfma_f32_16x16x32_bf16 a[120:123],a[240:243],v[232:235],a[120:123]
ds_read_b64_tr_b16 a[200:201],v3
v_mfma_f32_16x16x32_bf16 a[120:123],a[244:247],v[236:239],a[120:123]
ds_read_b64_tr_b16 a[202:203],v3 offset:4096
v_cvt_pk_bf16_f32 v137,v138,v139
v_mfma_f32_16x16x32_bf16 a[124:127],a[252:255],v[236:239],a[124:127]
ds_read_b64_tr_b16 a[208:209],v2 offset:256
v_mfma_f32_16x16x32_bf16 a[124:127],a[248:251],v[232:235],a[124:127]
ds_read_b64_tr_b16 a[210:211],v2 offset:4352
v_smfmac_f32_16x16x64_bf16 v[60:63],v[28:31],v[232:239],v27
ds_read_b64_tr_b16 a[212:213],v2 offset:8448
v_mfma_f32_16x16x32_bf16 v[200:203],a[128:131],v[144:147],v[40:43]
ds_read_b64_tr_b16 a[214:215],v2 offset:12544
v_cvt_pk_bf16_f32 v138,v140,v141
v_mfma_f32_16x16x32_bf16 v[200:203],a[132:135],v[176:179],v[200:203]
ds_read_b64_tr_b16 a[220:221],v3 offset:8448
v_mfma_f32_16x16x32_bf16 v[200:203],a[136:139],v[208:211],v[200:203]
ds_read_b64_tr_b16 a[222:223],v3 offset:12544
v_cvt_pk_bf16_f32 v139,v142,v143
v_mfma_f32_16x16x32_bf16 v[200:203],a[140:143],v[240:243],v[200:203]
ds_read_b64_tr_b16 a[216:217],v3 offset:256
v_mfma_f32_16x16x32_bf16 v[204:207],a[156:159],v[240:243],v[40:43]
ds_read_b64_tr_b16 a[218:219],v3 offset:4352
v_mfma_f32_16x16x32_bf16 v[204:207],a[152:155],v[208:211],v[204:207]
buffer_load_dwordx4 v5,s[24:27],s47 offen offset:3072 nt lds
v_mfma_f32_16x16x32_bf16 v[204:207],a[148:151],v[176:179],v[204:207]
s_or_b32 s47,s47,0x18000
ds_read_b64_tr_b16 a[224:225],v2 offset:512
v_cvt_pk_bf16_f32 v140,v152,v153
v_mfma_f32_16x16x32_bf16 v[204:207],a[144:147],v[144:147],v[204:207]
s_addk_i32 s47,4096
s_and_b32 s47,s47,0x27000
ds_read_b64_tr_b16 a[226:227],v2 offset:4608
v_mfma_f32_16x16x32_bf16 v[216:219],a[160:163],v[144:147],v[40:43]
s_or_b32 s47,s47,s46
ds_read_b64_tr_b16 a[228:229],v2 offset:8704
v_cvt_pk_bf16_f32 v141,v154,v155
v_mfma_f32_16x16x32_bf16 v[216:219],a[164:167],v[176:179],v[216:219]
ds_read_b64_tr_b16 a[230:231],v2 offset:12800
v_mfma_f32_16x16x32_bf16 v[216:219],a[168:171],v[208:211],v[216:219]
ds_read_b64_tr_b16 a[236:237],v3 offset:8704
v_mfma_f32_16x16x32_bf16 v[216:219],a[172:175],v[240:243],v[216:219]
ds_read_b64_tr_b16 a[238:239],v3 offset:12800
v_cvt_pk_bf16_f32 v142,v156,v157
v_mfma_f32_16x16x32_bf16 v[220:223],a[188:191],v[240:243],v[40:43]
ds_read_b64_tr_b16 a[232:233],v3 offset:512
v_mfma_f32_16x16x32_bf16 v[220:223],a[184:187],v[208:211],v[220:223]
ds_read_b64_tr_b16 a[234:235],v3 offset:4608
v_mfma_f32_16x16x32_bf16 v[220:223],a[180:183],v[176:179],v[220:223]
ds_read_b64_tr_b16 a[240:241],v2 offset:768
v_cvt_pk_bf16_f32 v143,v158,v159
v_mfma_f32_16x16x32_bf16 v[220:223],a[176:179],v[144:147],v[220:223]
s_waitcnt lgkmcnt(7)
buffer_load_dword v26,v24,s[88:91],s93 offen
v_mfma_f32_16x16x32_bf16 a[0:3],a[192:195],v[136:139],a[0:3]
ds_read_b64_tr_b16 a[242:243],v2 offset:4864
v_mfma_f32_16x16x32_bf16 a[0:3],a[196:199],v[140:143],a[0:3]
ds_read_b64_tr_b16 a[244:245],v2 offset:8960
v_mfma_f32_16x16x32_bf16 a[4:7],a[204:207],v[140:143],a[4:7]
ds_read_b64_tr_b16 a[246:247],v2 offset:13056
v_mfma_f32_16x16x32_bf16 a[4:7],a[200:203],v[136:139],a[4:7]
ds_read_b64_tr_b16 a[252:253],v3 offset:8960
v_mfma_f32_16x16x32_bf16 a[8:11],a[208:211],v[136:139],a[8:11]
ds_read_b64_tr_b16 a[254:255],v3 offset:13056
v_mfma_f32_16x16x32_bf16 a[8:11],a[212:215],v[140:143],a[8:11]
ds_read_b64_tr_b16 a[248:249],v3 offset:768
v_mfma_f32_16x16x32_bf16 a[12:15],a[220:223],v[140:143],a[12:15]
ds_read_b64_tr_b16 a[250:251],v3 offset:4864
v_mfma_f32_16x16x32_bf16 a[12:15],a[216:219],v[136:139],a[12:15]
s_bitcmp1_b32 s45,0
s_cbranch_scc0 L_t31v11_%=
s_mov_b32 m0,s80
s_addk_i32 s80,4096
buffer_load_dwordx4 v25,s[24:27],s83 offen nt lds
s_addk_i32 s83,1024
L_t31v11_%=:
v_mfma_f32_16x16x32_bf16 a[16:19],a[224:227],v[136:139],a[16:19]
s_waitcnt lgkmcnt(6)
v_mfma_f32_16x16x32_bf16 a[16:19],a[228:231],v[140:143],a[16:19]
v_mfma_f32_16x16x32_bf16 a[20:23],a[236:239],v[140:143],a[20:23]
v_mfma_f32_16x16x32_bf16 a[20:23],a[232:235],v[136:139],a[20:23]
v_mfma_f32_16x16x32_bf16 a[24:27],a[240:243],v[136:139],a[24:27]
s_waitcnt lgkmcnt(2)
v_mfma_f32_16x16x32_bf16 a[24:27],a[244:247],v[140:143],a[24:27]
v_mfma_f32_16x16x32_bf16 a[28:31],a[252:255],v[140:143],a[28:31]
s_waitcnt lgkmcnt(0)
v_mfma_f32_16x16x32_bf16 a[28:31],a[248:251],v[136:139],a[28:31]
s_cmp_lg_u32 s53,0
v_smfmac_f32_16x16x64_bf16 v[48:51],v[28:31],v[136:143],v27
s_cbranch_scc1 L_att1v1_%=
s_waitcnt lgkmcnt(0)
s_nop 7
s_branch L_att1x_%=
.p2align 5
L_att1v2_%=:
v_mfma_f32_16x16x32_bf16 v[232:235],a[128:131],v[148:151],v[44:47]
s_waitcnt vmcnt(8) lgkmcnt(0)
v_mfma_f32_16x16x32_bf16 v[232:235],a[132:135],v[180:183],v[232:235]
)A"
R"A(s_barrier
ds_read_b128 a[128:131],v1
v_add_u32 v2,s48,v2
v_mfma_f32_16x16x32_bf16 v[232:235],a[136:139],v[212:215],v[232:235]
s_mov_b32 s90,L_out_end_%=-L_pc_%=+128
v_mfma_f32_16x16x32_bf16 v[232:235],a[140:143],v[244:247],v[232:235]
s_add_u32 m0,s49,s51
ds_read_b128 a[132:135],v1 offset:256
v_mfma_f32_16x16x32_bf16 v[236:239],a[156:159],v[244:247],v[44:47]
s_lshr_b32 s45,s45,1
v_mfma_f32_16x16x32_bf16 v[236:239],a[152:155],v[212:215],v[236:239]
ds_read_b128 a[136:139],v1 offset:512
v_add_u32 v3,s48,v3
v_mfma_f32_16x16x32_bf16 v[236:239],a[148:151],v[180:183],v[236:239]
buffer_load_dwordx4 v4,s[20:23],s47 offen nt lds
v_mfma_f32_16x16x32_bf16 v[236:239],a[144:147],v[148:151],v[236:239]
v_mfma_f32_16x16x32_bf16 v[248:251],a[160:163],v[148:151],v[44:47]
s_add_u32 s62,s50,32768
ds_read_b128 a[140:143],v1 offset:768
v_mfma_f32_16x16x32_bf16 v[248:251],a[164:167],v[180:183],v[248:251]
s_and_b32 s62,s62,131071
v_mfma_f32_16x16x32_bf16 v[248:251],a[168:171],v[212:215],v[248:251]
s_addk_i32 s54,16384
v_mfma_f32_16x16x32_bf16 v[248:251],a[172:175],v[244:247],v[248:251]
s_sub_u32 s48,s62,s50
ds_read_b128 a[156:159],v1 offset:4864
v_mfma_f32_16x16x32_bf16 v[252:255],a[188:191],v[244:247],v[44:47]
s_sub_u32 s53,s53,1
v_mfma_f32_16x16x32_bf16 v[252:255],a[184:187],v[212:215],v[252:255]
s_and_b32 s46,s54,0xfffc0000
v_mfma_f32_16x16x32_bf16 v[252:255],a[180:183],v[180:183],v[252:255]
s_mov_b32 s50,s62
ds_read_b128 a[152:155],v1 offset:4608
v_mfma_f32_16x16x32_bf16 v[252:255],a[176:179],v[148:151],v[252:255]
s_xor_b32 s49,s50,65536
v_mfma_f32_16x16x32_bf16 a[32:35],a[192:195],v[168:171],a[32:35]
ds_read_b128 a[148:151],v1 offset:4352
v_mfma_f32_16x16x32_bf16 a[32:35],a[196:199],v[172:175],a[32:35]
v_mfma_f32_16x16x32_bf16 a[36:39],a[204:207],v[172:175],a[36:39]
ds_read_b128 a[144:147],v1 offset:4096
v_mfma_f32_16x16x32_bf16 a[36:39],a[200:203],v[168:171],a[36:39]
v_mfma_f32_16x16x32_bf16 a[40:43],a[208:211],v[168:171],a[40:43]
buffer_load_dwordx4 v4,s[20:23],s47 offen offset:1024 nt lds
v_mfma_f32_16x16x32_bf16 a[40:43],a[212:215],v[172:175],a[40:43]
ds_read_b128 a[160:163],v1 offset:8192
v_mfma_f32_16x16x32_bf16 a[44:47],a[220:223],v[172:175],a[44:47]
v_mfma_f32_16x16x32_bf16 a[44:47],a[216:219],v[168:171],a[44:47]
ds_read_b128 a[164:167],v1 offset:8448
v_mfma_f32_16x16x32_bf16 a[48:51],a[224:227],v[168:171],a[48:51]
v_mfma_f32_16x16x32_bf16 a[48:51],a[228:231],v[172:175],a[48:51]
ds_read_b128 a[168:171],v1 offset:8704
v_mfma_f32_16x16x32_bf16 a[52:55],a[236:239],v[172:175],a[52:55]
v_mfma_f32_16x16x32_bf16 a[52:55],a[232:235],v[168:171],a[52:55]
ds_read_b128 a[172:175],v1 offset:8960
v_mfma_f32_16x16x32_bf16 a[56:59],a[240:243],v[168:171],a[56:59]
v_mfma_f32_16x16x32_bf16 a[56:59],a[244:247],v[172:175],a[56:59]
ds_read_b128 a[188:191],v1 offset:13056
v_mfma_f32_16x16x32_bf16 a[60:63],a[252:255],v[172:175],a[60:63]
v_mfma_f32_16x16x32_bf16 a[60:63],a[248:251],v[168:171],a[60:63]
ds_read_b128 a[184:187],v1 offset:12800
v_smfmac_f32_16x16x64_bf16 v[52:55],v[28:31],v[168:175],v27
s_waitcnt lgkmcnt(4)
v_mfma_f32_16x16x32_bf16 v[136:139],a[128:131],v[128:131],v[32:35]
ds_read_b128 a[180:183],v1 offset:12544
v_mfma_f32_16x16x32_bf16 v[136:139],a[132:135],v[160:163],v[136:139]
buffer_load_dwordx4 v5,s[20:23],s47 offen offset:2048 nt lds
v_mfma_f32_16x16x32_bf16 v[136:139],a[136:139],v[192:195],v[136:139]
ds_read_b128 a[176:179],v1 offset:12288
v_add_u32 v1,s48,v1
v_mfma_f32_16x16x32_bf16 v[136:139],a[140:143],v[224:227],v[136:139]
v_exp_f32 v232,v232
v_mfma_f32_16x16x32_bf16 v[140:143],a[156:159],v[224:227],v[32:35]
v_exp_f32 v233,v233
v_mfma_f32_16x16x32_bf16 v[140:143],a[152:155],v[192:195],v[140:143]
v_exp_f32 v234,v234
v_mfma_f32_16x16x32_bf16 v[140:143],a[148:151],v[160:163],v[140:143]
v_exp_f32 v235,v235
v_mfma_f32_16x16x32_bf16 v[140:143],a[144:147],v[128:131],v[140:143]
v_exp_f32 v236,v236
v_mfma_f32_16x16x32_bf16 v[152:155],a[160:163],v[128:131],v[32:35]
v_exp_f32 v237,v237
v_mfma_f32_16x16x32_bf16 v[152:155],a[164:167],v[160:163],v[152:155]
s_waitcnt lgkmcnt(1)
v_exp_f32 v238,v238
v_mfma_f32_16x16x32_bf16 v[152:155],a[168:171],v[192:195],v[152:155]
v_exp_f32 v239,v239
v_mfma_f32_16x16x32_bf16 v[152:155],a[172:175],v[224:227],v[152:155]
v_exp_f32 v248,v248
v_mfma_f32_16x16x32_bf16 v[156:159],a[188:191],v[224:227],v[32:35]
v_exp_f32 v249,v249
v_mfma_f32_16x16x32_bf16 v[156:159],a[184:187],v[192:195],v[156:159]
v_exp_f32 v250,v250
v_mfma_f32_16x16x32_bf16 v[156:159],a[180:183],v[160:163],v[156:159]
s_waitcnt lgkmcnt(0)
v_exp_f32 v251,v251
v_mfma_f32_16x16x32_bf16 v[156:159],a[176:179],v[128:131],v[156:159]
buffer_load_dwordx4 v5,s[20:23],s47 offen offset:3072 nt lds
v_exp_f32 v252,v252
v_exp_f32 v253,v253
v_exp_f32 v254,v254
v_exp_f32 v255,v255
v_exp_f32 v136,v136
v_exp_f32 v137,v137
v_exp_f32 v138,v138
v_exp_f32 v139,v139
v_exp_f32 v140,v140
v_exp_f32 v141,v141
s_addk_i32 m0,16384
v_exp_f32 v142,v142
v_exp_f32 v143,v143
v_exp_f32 v152,v152
buffer_load_dwordx4 v4,s[24:27],s47 offen nt lds
.p2align 3
v_cvt_pk_bf16_f32 v232,v232,v233
v_cvt_pk_bf16_f32 v233,v234,v235
v_cvt_pk_bf16_f32 v234,v236,v237
v_cvt_pk_bf16_f32 v235,v238,v239
v_cvt_pk_bf16_f32 v236,v248,v249
v_mfma_f32_16x16x32_bf16 v[168:171],a[128:131],v[132:135],v[36:39]
v_cvt_pk_bf16_f32 v237,v250,v251
v_cvt_pk_bf16_f32 v238,v252,v253
v_mfma_f32_16x16x32_bf16 v[168:171],a[132:135],v[164:167],v[168:171]
v_cvt_pk_bf16_f32 v239,v254,v255
v_mfma_f32_16x16x32_bf16 v[168:171],a[136:139],v[196:199],v[168:171]
v_exp_f32 v153,v153
v_mfma_f32_16x16x32_bf16 v[168:171],a[140:143],v[228:231],v[168:171]
v_exp_f32 v154,v154
v_mfma_f32_16x16x32_bf16 v[172:175],a[156:159],v[228:231],v[36:39]
v_exp_f32 v155,v155
v_mfma_f32_16x16x32_bf16 v[172:175],a[152:155],v[196:199],v[172:175]
v_exp_f32 v156,v156
v_mfma_f32_16x16x32_bf16 v[172:175],a[148:151],v[164:167],v[172:175]
v_exp_f32 v157,v157
v_mfma_f32_16x16x32_bf16 v[172:175],a[144:147],v[132:135],v[172:175]
v_exp_f32 v158,v158
v_mfma_f32_16x16x32_bf16 v[184:187],a[160:163],v[132:135],v[36:39]
v_exp_f32 v159,v159
v_mfma_f32_16x16x32_bf16 v[184:187],a[164:167],v[164:167],v[184:187]
v_exp_f32 v168,v168
v_mfma_f32_16x16x32_bf16 v[184:187],a[168:171],v[196:199],v[184:187]
buffer_load_dwordx4 v5,s[24:27],s47 offen offset:1024 nt lds
v_mfma_f32_16x16x32_bf16 v[184:187],a[172:175],v[228:231],v[184:187]
v_exp_f32 v169,v169
v_mfma_f32_16x16x32_bf16 v[188:191],a[188:191],v[228:231],v[36:39]
v_exp_f32 v170,v170
v_mfma_f32_16x16x32_bf16 v[188:191],a[184:187],v[196:199],v[188:191]
v_exp_f32 v171,v171
v_mfma_f32_16x16x32_bf16 v[188:191],a[180:183],v[164:167],v[188:191]
v_exp_f32 v172,v172
v_mfma_f32_16x16x32_bf16 v[188:191],a[176:179],v[132:135],v[188:191]
v_exp_f32 v173,v173
v_mfma_f32_16x16x32_bf16 a[96:99],a[192:195],v[232:235],a[96:99]
ds_read_b64_tr_b16 a[192:193],v2
v_cvt_pk_bf16_f32 v168,v168,v169
v_mfma_f32_16x16x32_bf16 a[96:99],a[196:199],v[236:239],a[96:99]
ds_read_b64_tr_b16 a[194:195],v2 offset:4096
v_cvt_pk_bf16_f32 v169,v170,v171
v_mfma_f32_16x16x32_bf16 a[100:103],a[204:207],v[236:239],a[100:103]
ds_read_b64_tr_b16 a[196:197],v2 offset:8192
v_cvt_pk_bf16_f32 v170,v172,v173
v_mfma_f32_16x16x32_bf16 a[100:103],a[200:203],v[232:235],a[100:103]
v_exp_f32 v174,v174
v_mfma_f32_16x16x32_bf16 a[104:107],a[208:211],v[232:235],a[104:107]
v_exp_f32 v175,v175
v_mfma_f32_16x16x32_bf16 a[104:107],a[212:215],v[236:239],a[104:107]
ds_read_b64_tr_b16 a[198:199],v2 offset:12288
v_cvt_pk_bf16_f32 v171,v174,v175
v_mfma_f32_16x16x32_bf16 a[108:111],a[220:223],v[236:239],a[108:111]
v_exp_f32 v184,v184
v_mfma_f32_16x16x32_bf16 a[108:111],a[216:219],v[232:235],a[108:111]
v_exp_f32 v185,v185
v_mfma_f32_16x16x32_bf16 a[112:115],a[224:227],v[232:235],a[112:115]
buffer_load_dwordx4 v4,s[24:27],s47 offen offset:2048 nt lds
v_mfma_f32_16x16x32_bf16 a[112:115],a[228:231],v[236:239],a[112:115]
ds_read_b64_tr_b16 a[204:205],v3 offset:8192
v_cvt_pk_bf16_f32 v172,v184,v185
v_mfma_f32_16x16x32_bf16 a[116:119],a[236:239],v[236:239],a[116:119]
ds_read_b64_tr_b16 a[206:207],v3 offset:12288
v_cvt_pk_bf16_f32 v136,v136,v137
v_mfma_f32_16x16x32_bf16 a[116:119],a[232:235],v[232:235],a[116:119]
v_exp_f32 v186,v186
v_mfma_f32_16x16x32_bf16 a[120:123],a[240:243],v[232:235],a[120:123]
ds_read_b64_tr_b16 a[200:201],v3
v_mfma_f32_16x16x32_bf16 a[120:123],a[244:247],v[236:239],a[120:123]
ds_read_b64_tr_b16 a[202:203],v3 offset:4096
v_cvt_pk_bf16_f32 v137,v138,v139
v_mfma_f32_16x16x32_bf16 a[124:127],a[252:255],v[236:239],a[124:127]
ds_read_b64_tr_b16 a[208:209],v2 offset:256
v_mfma_f32_16x16x32_bf16 a[124:127],a[248:251],v[232:235],a[124:127]
ds_read_b64_tr_b16 a[210:211],v2 offset:4352
v_smfmac_f32_16x16x64_bf16 v[60:63],v[28:31],v[232:239],v27
ds_read_b64_tr_b16 a[212:213],v2 offset:8448
ds_read_b64_tr_b16 a[214:215],v2 offset:12544
v_cvt_pk_bf16_f32 v138,v140,v141
ds_read_b64_tr_b16 a[220:221],v3 offset:8448
ds_read_b64_tr_b16 a[222:223],v3 offset:12544
v_cvt_pk_bf16_f32 v139,v142,v143
ds_read_b64_tr_b16 a[216:217],v3 offset:256
ds_read_b64_tr_b16 a[218:219],v3 offset:4352
buffer_load_dwordx4 v5,s[24:27],s47 offen offset:3072 nt lds
s_or_b32 s47,s47,0x18000
ds_read_b64_tr_b16 a[224:225],v2 offset:512
v_cvt_pk_bf16_f32 v140,v152,v153
s_addk_i32 s47,4096
s_and_b32 s47,s47,0x27000
ds_read_b64_tr_b16 a[226:227],v2 offset:4608
s_or_b32 s47,s47,s46
ds_read_b64_tr_b16 a[228:229],v2 offset:8704
v_cvt_pk_bf16_f32 v141,v154,v155
ds_read_b64_tr_b16 a[230:231],v2 offset:12800
ds_read_b64_tr_b16 a[236:237],v3 offset:8704
ds_read_b64_tr_b16 a[238:239],v3 offset:12800
v_cvt_pk_bf16_f32 v142,v156,v157
ds_read_b64_tr_b16 a[232:233],v3 offset:512
ds_read_b64_tr_b16 a[234:235],v3 offset:4608
ds_read_b64_tr_b16 a[240:241],v2 offset:768
v_cvt_pk_bf16_f32 v143,v158,v159
buffer_load_dword v26,v24,s[88:91],s93 offen
s_waitcnt lgkmcnt(15)
v_mfma_f32_16x16x32_bf16 a[0:3],a[192:195],v[136:139],a[0:3]
ds_read_b64_tr_b16 a[242:243],v2 offset:4864
v_mfma_f32_16x16x32_bf16 a[0:3],a[196:199],v[140:143],a[0:3]
ds_read_b64_tr_b16 a[244:245],v2 offset:8960
v_mfma_f32_16x16x32_bf16 a[4:7],a[204:207],v[140:143],a[4:7]
ds_read_b64_tr_b16 a[246:247],v2 offset:13056
v_mfma_f32_16x16x32_bf16 a[4:7],a[200:203],v[136:139],a[4:7]
ds_read_b64_tr_b16 a[252:253],v3 offset:8960
v_mfma_f32_16x16x32_bf16 a[8:11],a[208:211],v[136:139],a[8:11]
s_waitcnt lgkmcnt(15)
ds_read_b64_tr_b16 a[254:255],v3 offset:13056
v_mfma_f32_16x16x32_bf16 a[8:11],a[212:215],v[140:143],a[8:11]
ds_read_b64_tr_b16 a[248:249],v3 offset:768
v_mfma_f32_16x16x32_bf16 a[12:15],a[220:223],v[140:143],a[12:15]
s_waitcnt lgkmcnt(15)
ds_read_b64_tr_b16 a[250:251],v3 offset:4864
v_mfma_f32_16x16x32_bf16 a[12:15],a[216:219],v[136:139],a[12:15]
s_bitcmp1_b32 s45,0
s_cbranch_scc0 L_t31v21_%=
s_mov_b32 m0,s80
s_addk_i32 s80,4096
buffer_load_dwordx4 v25,s[24:27],s83 offen nt lds
s_addk_i32 s83,1024
L_t31v21_%=:
s_waitcnt lgkmcnt(7)
v_mfma_f32_16x16x32_bf16 a[16:19],a[224:227],v[136:139],a[16:19]
v_exp_f32 v187,v187
v_mfma_f32_16x16x32_bf16 a[16:19],a[228:231],v[140:143],a[16:19]
v_exp_f32 v188,v188
v_mfma_f32_16x16x32_bf16 a[20:23],a[236:239],v[140:143],a[20:23]
v_exp_f32 v189,v189
v_mfma_f32_16x16x32_bf16 a[20:23],a[232:235],v[136:139],a[20:23]
s_waitcnt lgkmcnt(3)
v_exp_f32 v190,v190
v_mfma_f32_16x16x32_bf16 a[24:27],a[240:243],v[136:139],a[24:27]
v_exp_f32 v191,v191
v_mfma_f32_16x16x32_bf16 a[24:27],a[244:247],v[140:143],a[24:27]
s_waitcnt lgkmcnt(1)
v_cvt_pk_bf16_f32 v173,v186,v187
v_mfma_f32_16x16x32_bf16 a[28:31],a[252:255],v[140:143],a[28:31]
s_waitcnt lgkmcnt(0)
v_cvt_pk_bf16_f32 v174,v188,v189
v_cvt_pk_bf16_f32 v175,v190,v191
v_mfma_f32_16x16x32_bf16 a[28:31],a[248:251],v[136:139],a[28:31]
s_cmp_lg_u32 s53,0
v_smfmac_f32_16x16x64_bf16 v[48:51],v[28:31],v[136:143],v27
s_cbranch_scc1 L_att1v2_%=
s_waitcnt lgkmcnt(0)
s_nop 7
s_branch L_att1x_%=
.p2align 5
L_att1_%=:
v_mfma_f32_16x16x32_bf16 v[232:235],a[128:131],v[148:151],v[44:47]
s_waitcnt vmcnt(8) lgkmcnt(0)
v_exp_f32 v200,v200
v_mfma_f32_16x16x32_bf16 v[232:235],a[132:135],v[180:183],v[232:235]
s_barrier
ds_read_b128 a[128:131],v1
v_add_u32 v2,s48,v2
v_mfma_f32_16x16x32_bf16 v[232:235],a[136:139],v[212:215],v[232:235]
)A"
R"A(s_mov_b32 s90,L_out_end_%=-L_pc_%=+128
v_exp_f32 v201,v201
v_mfma_f32_16x16x32_bf16 v[232:235],a[140:143],v[244:247],v[232:235]
s_add_u32 m0,s49,s51
ds_read_b128 a[132:135],v1 offset:256
v_cvt_pk_bf16_f32 v200,v200,v201
v_mfma_f32_16x16x32_bf16 v[236:239],a[156:159],v[244:247],v[44:47]
s_lshr_b32 s45,s45,1
v_exp_f32 v202,v202
v_mfma_f32_16x16x32_bf16 v[236:239],a[152:155],v[212:215],v[236:239]
s_sub_u32 s94,s94,3
ds_read_b128 a[136:139],v1 offset:512
v_add_u32 v3,s48,v3
v_mfma_f32_16x16x32_bf16 v[236:239],a[148:151],v[180:183],v[236:239]
s_lshr_b32 s60,s94,2
buffer_load_dwordx4 v4,s[20:23],s47 offen nt lds
v_mfma_f32_16x16x32_bf16 v[236:239],a[144:147],v[148:151],v[236:239]
s_max_i32 s60,s60,124
v_exp_f32 v203,v203
v_mfma_f32_16x16x32_bf16 v[248:251],a[160:163],v[148:151],v[44:47]
s_add_u32 s62,s50,32768
ds_read_b128 a[140:143],v1 offset:768
v_cvt_pk_bf16_f32 v201,v202,v203
v_mfma_f32_16x16x32_bf16 v[248:251],a[164:167],v[180:183],v[248:251]
s_and_b32 s62,s62,131071
v_exp_f32 v204,v204
v_mfma_f32_16x16x32_bf16 v[248:251],a[168:171],v[212:215],v[248:251]
s_addk_i32 s54,16384
v_exp_f32 v205,v205
v_mfma_f32_16x16x32_bf16 v[248:251],a[172:175],v[244:247],v[248:251]
s_sub_u32 s48,s62,s50
ds_read_b128 a[156:159],v1 offset:4864
v_cvt_pk_bf16_f32 v202,v204,v205
v_mfma_f32_16x16x32_bf16 v[252:255],a[188:191],v[244:247],v[44:47]
s_sub_u32 s53,s53,1
v_exp_f32 v206,v206
v_mfma_f32_16x16x32_bf16 v[252:255],a[184:187],v[212:215],v[252:255]
s_and_b32 s46,s54,0xfffc0000
v_exp_f32 v207,v207
v_mfma_f32_16x16x32_bf16 v[252:255],a[180:183],v[180:183],v[252:255]
s_mov_b32 s50,s62
ds_read_b128 a[152:155],v1 offset:4608
v_cvt_pk_bf16_f32 v203,v206,v207
v_mfma_f32_16x16x32_bf16 v[252:255],a[176:179],v[148:151],v[252:255]
s_xor_b32 s49,s50,65536
v_exp_f32 v216,v216
v_mfma_f32_16x16x32_bf16 a[32:35],a[192:195],v[168:171],a[32:35]
ds_read_b128 a[148:151],v1 offset:4352
v_mfma_f32_16x16x32_bf16 a[32:35],a[196:199],v[172:175],a[32:35]
v_exp_f32 v217,v217
v_mfma_f32_16x16x32_bf16 a[36:39],a[204:207],v[172:175],a[36:39]
ds_read_b128 a[144:147],v1 offset:4096
v_cvt_pk_bf16_f32 v204,v216,v217
v_mfma_f32_16x16x32_bf16 a[36:39],a[200:203],v[168:171],a[36:39]
v_exp_f32 v218,v218
v_mfma_f32_16x16x32_bf16 a[40:43],a[208:211],v[168:171],a[40:43]
buffer_load_dwordx4 v4,s[20:23],s47 offen offset:1024 nt lds
v_mfma_f32_16x16x32_bf16 a[40:43],a[212:215],v[172:175],a[40:43]
ds_read_b128 a[160:163],v1 offset:8192
v_mfma_f32_16x16x32_bf16 a[44:47],a[220:223],v[172:175],a[44:47]
v_exp_f32 v219,v219
v_mfma_f32_16x16x32_bf16 a[44:47],a[216:219],v[168:171],a[44:47]
ds_read_b128 a[164:167],v1 offset:8448
v_cvt_pk_bf16_f32 v205,v218,v219
v_mfma_f32_16x16x32_bf16 a[48:51],a[224:227],v[168:171],a[48:51]
v_exp_f32 v220,v220
v_mfma_f32_16x16x32_bf16 a[48:51],a[228:231],v[172:175],a[48:51]
ds_read_b128 a[168:171],v1 offset:8704
v_mfma_f32_16x16x32_bf16 a[52:55],a[236:239],v[172:175],a[52:55]
v_exp_f32 v221,v221
v_mfma_f32_16x16x32_bf16 a[52:55],a[232:235],v[168:171],a[52:55]
ds_read_b128 a[172:175],v1 offset:8960
v_cvt_pk_bf16_f32 v206,v220,v221
v_mfma_f32_16x16x32_bf16 a[56:59],a[240:243],v[168:171],a[56:59]
v_exp_f32 v222,v222
v_mfma_f32_16x16x32_bf16 a[56:59],a[244:247],v[172:175],a[56:59]
ds_read_b128 a[188:191],v1 offset:13056
v_mfma_f32_16x16x32_bf16 a[60:63],a[252:255],v[172:175],a[60:63]
v_exp_f32 v223,v223
v_mfma_f32_16x16x32_bf16 a[60:63],a[248:251],v[168:171],a[60:63]
ds_read_b128 a[184:187],v1 offset:12800
v_cvt_pk_bf16_f32 v207,v222,v223
v_smfmac_f32_16x16x64_bf16 v[52:55],v[28:31],v[168:175],v27
s_waitcnt lgkmcnt(4)
v_mfma_f32_16x16x32_bf16 v[136:139],a[128:131],v[128:131],v[32:35]
ds_read_b128 a[180:183],v1 offset:12544
v_mfma_f32_16x16x32_bf16 v[136:139],a[132:135],v[160:163],v[136:139]
buffer_load_dwordx4 v5,s[20:23],s47 offen offset:2048 nt lds
v_mfma_f32_16x16x32_bf16 v[136:139],a[136:139],v[192:195],v[136:139]
ds_read_b128 a[176:179],v1 offset:12288
v_add_u32 v1,s48,v1
v_mfma_f32_16x16x32_bf16 v[136:139],a[140:143],v[224:227],v[136:139]
v_exp_f32 v232,v232
v_mfma_f32_16x16x32_bf16 v[140:143],a[156:159],v[224:227],v[32:35]
v_exp_f32 v233,v233
v_mfma_f32_16x16x32_bf16 v[140:143],a[152:155],v[192:195],v[140:143]
v_exp_f32 v234,v234
v_mfma_f32_16x16x32_bf16 v[140:143],a[148:151],v[160:163],v[140:143]
v_exp_f32 v235,v235
v_mfma_f32_16x16x32_bf16 v[140:143],a[144:147],v[128:131],v[140:143]
v_exp_f32 v236,v236
v_mfma_f32_16x16x32_bf16 v[152:155],a[160:163],v[128:131],v[32:35]
v_exp_f32 v237,v237
v_mfma_f32_16x16x32_bf16 v[152:155],a[164:167],v[160:163],v[152:155]
s_waitcnt lgkmcnt(1)
v_exp_f32 v238,v238
v_mfma_f32_16x16x32_bf16 v[152:155],a[168:171],v[192:195],v[152:155]
v_exp_f32 v239,v239
v_mfma_f32_16x16x32_bf16 v[152:155],a[172:175],v[224:227],v[152:155]
v_exp_f32 v248,v248
v_mfma_f32_16x16x32_bf16 v[156:159],a[188:191],v[224:227],v[32:35]
v_exp_f32 v249,v249
v_mfma_f32_16x16x32_bf16 v[156:159],a[184:187],v[192:195],v[156:159]
v_exp_f32 v250,v250
v_mfma_f32_16x16x32_bf16 v[156:159],a[180:183],v[160:163],v[156:159]
s_waitcnt lgkmcnt(0)
v_exp_f32 v251,v251
v_mfma_f32_16x16x32_bf16 v[156:159],a[176:179],v[128:131],v[156:159]
buffer_load_dwordx4 v5,s[20:23],s47 offen offset:3072 nt lds
v_mfma_f32_16x16x32_bf16 a[64:67],a[192:195],v[200:203],a[64:67]
v_exp_f32 v252,v252
v_mfma_f32_16x16x32_bf16 a[64:67],a[196:199],v[204:207],a[64:67]
v_exp_f32 v253,v253
v_mfma_f32_16x16x32_bf16 a[68:71],a[204:207],v[204:207],a[68:71]
v_exp_f32 v254,v254
v_mfma_f32_16x16x32_bf16 a[68:71],a[200:203],v[200:203],a[68:71]
v_exp_f32 v255,v255
v_mfma_f32_16x16x32_bf16 a[72:75],a[208:211],v[200:203],a[72:75]
v_exp_f32 v136,v136
v_mfma_f32_16x16x32_bf16 a[72:75],a[212:215],v[204:207],a[72:75]
v_exp_f32 v137,v137
v_mfma_f32_16x16x32_bf16 a[76:79],a[220:223],v[204:207],a[76:79]
v_exp_f32 v138,v138
v_mfma_f32_16x16x32_bf16 a[76:79],a[216:219],v[200:203],a[76:79]
v_exp_f32 v139,v139
v_mfma_f32_16x16x32_bf16 a[80:83],a[224:227],v[200:203],a[80:83]
v_exp_f32 v140,v140
v_mfma_f32_16x16x32_bf16 a[80:83],a[228:231],v[204:207],a[80:83]
v_exp_f32 v141,v141
v_mfma_f32_16x16x32_bf16 a[84:87],a[236:239],v[204:207],a[84:87]
s_addk_i32 m0,16384
v_exp_f32 v142,v142
v_mfma_f32_16x16x32_bf16 a[84:87],a[232:235],v[200:203],a[84:87]
v_exp_f32 v143,v143
v_mfma_f32_16x16x32_bf16 a[88:91],a[240:243],v[200:203],a[88:91]
v_exp_f32 v152,v152
v_mfma_f32_16x16x32_bf16 a[88:91],a[244:247],v[204:207],a[88:91]
buffer_load_dwordx4 v4,s[24:27],s47 offen nt lds
v_mfma_f32_16x16x32_bf16 a[92:95],a[252:255],v[204:207],a[92:95]
v_cvt_pk_bf16_f32 v232,v232,v233
v_cvt_pk_bf16_f32 v233,v234,v235
v_mfma_f32_16x16x32_bf16 a[92:95],a[248:251],v[200:203],a[92:95]
v_cvt_pk_bf16_f32 v234,v236,v237
v_cvt_pk_bf16_f32 v235,v238,v239
v_smfmac_f32_16x16x64_bf16 v[56:59],v[28:31],v[200:207],v27
v_cvt_pk_bf16_f32 v236,v248,v249
v_mfma_f32_16x16x32_bf16 v[168:171],a[128:131],v[132:135],v[36:39]
v_cvt_pk_bf16_f32 v237,v250,v251
v_cvt_pk_bf16_f32 v238,v252,v253
v_mfma_f32_16x16x32_bf16 v[168:171],a[132:135],v[164:167],v[168:171]
v_cvt_pk_bf16_f32 v239,v254,v255
v_mfma_f32_16x16x32_bf16 v[168:171],a[136:139],v[196:199],v[168:171]
v_exp_f32 v153,v153
v_mfma_f32_16x16x32_bf16 v[168:171],a[140:143],v[228:231],v[168:171]
v_exp_f32 v154,v154
v_mfma_f32_16x16x32_bf16 v[172:175],a[156:159],v[228:231],v[36:39]
v_exp_f32 v155,v155
v_mfma_f32_16x16x32_bf16 v[172:175],a[152:155],v[196:199],v[172:175]
v_exp_f32 v156,v156
v_mfma_f32_16x16x32_bf16 v[172:175],a[148:151],v[164:167],v[172:175]
v_exp_f32 v157,v157
v_mfma_f32_16x16x32_bf16 v[172:175],a[144:147],v[132:135],v[172:175]
v_exp_f32 v158,v158
v_mfma_f32_16x16x32_bf16 v[184:187],a[160:163],v[132:135],v[36:39]
v_exp_f32 v159,v159
v_mfma_f32_16x16x32_bf16 v[184:187],a[164:167],v[164:167],v[184:187]
v_exp_f32 v168,v168
v_mfma_f32_16x16x32_bf16 v[184:187],a[168:171],v[196:199],v[184:187]
buffer_load_dwordx4 v5,s[24:27],s47 offen offset:1024 nt lds
v_mfma_f32_16x16x32_bf16 v[184:187],a[172:175],v[228:231],v[184:187]
v_exp_f32 v169,v169
v_mfma_f32_16x16x32_bf16 v[188:191],a[188:191],v[228:231],v[36:39]
v_exp_f32 v170,v170
v_mfma_f32_16x16x32_bf16 v[188:191],a[184:187],v[196:199],v[188:191]
v_exp_f32 v171,v171
v_mfma_f32_16x16x32_bf16 v[188:191],a[180:183],v[164:167],v[188:191]
v_exp_f32 v172,v172
v_mfma_f32_16x16x32_bf16 v[188:191],a[176:179],v[132:135],v[188:191]
v_exp_f32 v173,v173
v_mfma_f32_16x16x32_bf16 a[96:99],a[192:195],v[232:235],a[96:99]
ds_read_b64_tr_b16 a[192:193],v2
v_cvt_pk_bf16_f32 v168,v168,v169
v_mfma_f32_16x16x32_bf16 a[96:99],a[196:199],v[236:239],a[96:99]
ds_read_b64_tr_b16 a[194:195],v2 offset:4096
v_cvt_pk_bf16_f32 v169,v170,v171
v_mfma_f32_16x16x32_bf16 a[100:103],a[204:207],v[236:239],a[100:103]
ds_read_b64_tr_b16 a[196:197],v2 offset:8192
v_cvt_pk_bf16_f32 v170,v172,v173
v_mfma_f32_16x16x32_bf16 a[100:103],a[200:203],v[232:235],a[100:103]
v_exp_f32 v174,v174
v_mfma_f32_16x16x32_bf16 a[104:107],a[208:211],v[232:235],a[104:107]
v_exp_f32 v175,v175
v_mfma_f32_16x16x32_bf16 a[104:107],a[212:215],v[236:239],a[104:107]
ds_read_b64_tr_b16 a[198:199],v2 offset:12288
v_cvt_pk_bf16_f32 v171,v174,v175
v_mfma_f32_16x16x32_bf16 a[108:111],a[220:223],v[236:239],a[108:111]
v_exp_f32 v184,v184
v_mfma_f32_16x16x32_bf16 a[108:111],a[216:219],v[232:235],a[108:111]
v_exp_f32 v185,v185
v_mfma_f32_16x16x32_bf16 a[112:115],a[224:227],v[232:235],a[112:115]
buffer_load_dwordx4 v4,s[24:27],s47 offen offset:2048 nt lds
v_mfma_f32_16x16x32_bf16 a[112:115],a[228:231],v[236:239],a[112:115]
ds_read_b64_tr_b16 a[204:205],v3 offset:8192
v_cvt_pk_bf16_f32 v172,v184,v185
v_mfma_f32_16x16x32_bf16 a[116:119],a[236:239],v[236:239],a[116:119]
ds_read_b64_tr_b16 a[206:207],v3 offset:12288
v_cvt_pk_bf16_f32 v136,v136,v137
v_mfma_f32_16x16x32_bf16 a[116:119],a[232:235],v[232:235],a[116:119]
v_exp_f32 v186,v186
v_mfma_f32_16x16x32_bf16 a[120:123],a[240:243],v[232:235],a[120:123]
ds_read_b64_tr_b16 a[200:201],v3
v_mfma_f32_16x16x32_bf16 a[120:123],a[244:247],v[236:239],a[120:123]
ds_read_b64_tr_b16 a[202:203],v3 offset:4096
v_cvt_pk_bf16_f32 v137,v138,v139
v_mfma_f32_16x16x32_bf16 a[124:127],a[252:255],v[236:239],a[124:127]
ds_read_b64_tr_b16 a[208:209],v2 offset:256
v_mfma_f32_16x16x32_bf16 a[124:127],a[248:251],v[232:235],a[124:127]
ds_read_b64_tr_b16 a[210:211],v2 offset:4352
v_smfmac_f32_16x16x64_bf16 v[60:63],v[28:31],v[232:239],v27
ds_read_b64_tr_b16 a[212:213],v2 offset:8448
v_mfma_f32_16x16x32_bf16 v[200:203],a[128:131],v[144:147],v[40:43]
ds_read_b64_tr_b16 a[214:215],v2 offset:12544
v_cvt_pk_bf16_f32 v138,v140,v141
v_mfma_f32_16x16x32_bf16 v[200:203],a[132:135],v[176:179],v[200:203]
ds_read_b64_tr_b16 a[220:221],v3 offset:8448
v_mfma_f32_16x16x32_bf16 v[200:203],a[136:139],v[208:211],v[200:203]
ds_read_b64_tr_b16 a[222:223],v3 offset:12544
v_cvt_pk_bf16_f32 v139,v142,v143
v_mfma_f32_16x16x32_bf16 v[200:203],a[140:143],v[240:243],v[200:203]
ds_read_b64_tr_b16 a[216:217],v3 offset:256
v_mfma_f32_16x16x32_bf16 v[204:207],a[156:159],v[240:243],v[40:43]
ds_read_b64_tr_b16 a[218:219],v3 offset:4352
v_mfma_f32_16x16x32_bf16 v[204:207],a[152:155],v[208:211],v[204:207]
buffer_load_dwordx4 v5,s[24:27],s47 offen offset:3072 nt lds
v_mfma_f32_16x16x32_bf16 v[204:207],a[148:151],v[176:179],v[204:207]
s_or_b32 s47,s47,0x18000
ds_read_b64_tr_b16 a[224:225],v2 offset:512
v_cvt_pk_bf16_f32 v140,v152,v153
v_mfma_f32_16x16x32_bf16 v[204:207],a[144:147],v[144:147],v[204:207]
s_addk_i32 s47,4096
s_and_b32 s47,s47,0x27000
ds_read_b64_tr_b16 a[226:227],v2 offset:4608
v_mfma_f32_16x16x32_bf16 v[216:219],a[160:163],v[144:147],v[40:43]
s_or_b32 s47,s47,s46
ds_read_b64_tr_b16 a[228:229],v2 offset:8704
v_cvt_pk_bf16_f32 v141,v154,v155
v_mfma_f32_16x16x32_bf16 v[216:219],a[164:167],v[176:179],v[216:219]
ds_read_b64_tr_b16 a[230:231],v2 offset:12800
v_mfma_f32_16x16x32_bf16 v[216:219],a[168:171],v[208:211],v[216:219]
ds_read_b64_tr_b16 a[236:237],v3 offset:8704
v_mfma_f32_16x16x32_bf16 v[216:219],a[172:175],v[240:243],v[216:219]
ds_read_b64_tr_b16 a[238:239],v3 offset:12800
v_cvt_pk_bf16_f32 v142,v156,v157
v_mfma_f32_16x16x32_bf16 v[220:223],a[188:191],v[240:243],v[40:43]
ds_read_b64_tr_b16 a[232:233],v3 offset:512
)A"
R"A(v_mfma_f32_16x16x32_bf16 v[220:223],a[184:187],v[208:211],v[220:223]
ds_read_b64_tr_b16 a[234:235],v3 offset:4608
v_mfma_f32_16x16x32_bf16 v[220:223],a[180:183],v[176:179],v[220:223]
ds_read_b64_tr_b16 a[240:241],v2 offset:768
v_cvt_pk_bf16_f32 v143,v158,v159
v_mfma_f32_16x16x32_bf16 v[220:223],a[176:179],v[144:147],v[220:223]
s_waitcnt lgkmcnt(7)
buffer_load_dword v26,v24,s[88:91],s93 offen
v_mfma_f32_16x16x32_bf16 a[0:3],a[192:195],v[136:139],a[0:3]
ds_read_b64_tr_b16 a[242:243],v2 offset:4864
v_mfma_f32_16x16x32_bf16 a[0:3],a[196:199],v[140:143],a[0:3]
ds_read_b64_tr_b16 a[244:245],v2 offset:8960
v_mfma_f32_16x16x32_bf16 a[4:7],a[204:207],v[140:143],a[4:7]
ds_read_b64_tr_b16 a[246:247],v2 offset:13056
v_mfma_f32_16x16x32_bf16 a[4:7],a[200:203],v[136:139],a[4:7]
s_memrealtime s[76:77]
ds_read_b64_tr_b16 a[252:253],v3 offset:8960
v_mfma_f32_16x16x32_bf16 a[8:11],a[208:211],v[136:139],a[8:11]
ds_read_b64_tr_b16 a[254:255],v3 offset:13056
v_mfma_f32_16x16x32_bf16 a[8:11],a[212:215],v[140:143],a[8:11]
ds_read_b64_tr_b16 a[248:249],v3 offset:768
v_mfma_f32_16x16x32_bf16 a[12:15],a[220:223],v[140:143],a[12:15]
ds_read_b64_tr_b16 a[250:251],v3 offset:4864
v_mfma_f32_16x16x32_bf16 a[12:15],a[216:219],v[136:139],a[12:15]
s_bitcmp1_b32 s45,0
s_cbranch_scc0 L_t311_%=
s_mov_b32 m0,s80
s_addk_i32 s80,4096
buffer_load_dwordx4 v25,s[24:27],s83 offen nt lds
s_addk_i32 s83,1024
L_t311_%=:
v_mfma_f32_16x16x32_bf16 a[16:19],a[224:227],v[136:139],a[16:19]
s_waitcnt lgkmcnt(6)
v_exp_f32 v187,v187
v_mfma_f32_16x16x32_bf16 a[16:19],a[228:231],v[140:143],a[16:19]
v_exp_f32 v188,v188
v_mfma_f32_16x16x32_bf16 a[20:23],a[236:239],v[140:143],a[20:23]
s_waitcnt lgkmcnt(0)
v_exp_f32 v189,v189
v_mfma_f32_16x16x32_bf16 a[20:23],a[232:235],v[136:139],a[20:23]
s_sub_u32 s79,s78,s76
v_exp_f32 v190,v190
v_mfma_f32_16x16x32_bf16 a[24:27],a[240:243],v[136:139],a[24:27]
s_add_u32 s78,s76,s60
v_exp_f32 v191,v191
v_mfma_f32_16x16x32_bf16 a[24:27],a[244:247],v[140:143],a[24:27]
s_cmp_gt_i32 s79,s95
s_cbranch_scc1 L_ps7_%=
L_pr7_%=:
v_cvt_pk_bf16_f32 v173,v186,v187
v_mfma_f32_16x16x32_bf16 a[28:31],a[252:255],v[140:143],a[28:31]
v_cvt_pk_bf16_f32 v174,v188,v189
v_cvt_pk_bf16_f32 v175,v190,v191
v_mfma_f32_16x16x32_bf16 a[28:31],a[248:251],v[136:139],a[28:31]
s_cmp_lg_u32 s53,0
v_smfmac_f32_16x16x64_bf16 v[48:51],v[28:31],v[136:143],v27
s_cbranch_scc1 L_att1_%=
L_att1x_%=:
L_endseg_%=:
v_mfma_f32_16x16x32_bf16 v[232:235],a[128:131],v[148:151],v[44:47]
s_waitcnt vmcnt(10)
s_lshl_b32 s43,s41,2
v_exp_f32 v200,v200
v_mfma_f32_16x16x32_bf16 v[232:235],a[132:135],v[180:183],v[232:235]
s_barrier
ds_read_b128 a[128:131],v1
v_add_u32 v2,s48,v2
v_mfma_f32_16x16x32_bf16 v[232:235],a[136:139],v[212:215],v[232:235]
s_add_u32 s43,s43,s70
v_exp_f32 v201,v201
v_mfma_f32_16x16x32_bf16 v[232:235],a[140:143],v[244:247],v[232:235]
s_lshl_b32 s43,s43,6
ds_read_b128 a[132:135],v1 offset:256
v_cvt_pk_bf16_f32 v200,v200,v201
v_mfma_f32_16x16x32_bf16 v[236:239],a[156:159],v[244:247],v[44:47]
s_add_u32 s43,s43,s40
v_exp_f32 v202,v202
v_mfma_f32_16x16x32_bf16 v[236:239],a[152:155],v[212:215],v[236:239]
s_lshl_b32 s43,s43,15
ds_read_b128 a[136:139],v1 offset:512
v_add_u32 v3,s48,v3
v_mfma_f32_16x16x32_bf16 v[236:239],a[148:151],v[180:183],v[236:239]
s_mov_b32 s48,32768
v_exp_f32 v203,v203
v_mfma_f32_16x16x32_bf16 v[236:239],a[144:147],v[148:151],v[236:239]
s_mov_b64 s[28:29],s[12:13]
ds_read_b128 a[140:143],v1 offset:768
v_cvt_pk_bf16_f32 v201,v202,v203
v_mfma_f32_16x16x32_bf16 v[248:251],a[160:163],v[148:151],v[44:47]
s_mov_b32 s30,950272
v_exp_f32 v204,v204
v_mfma_f32_16x16x32_bf16 v[248:251],a[164:167],v[180:183],v[248:251]
s_mov_b32 s31,0x20000
ds_read_b128 a[156:159],v1 offset:4864
v_lshrrev_b32 v25,6,v6
v_mfma_f32_16x16x32_bf16 v[248:251],a[168:171],v[212:215],v[248:251]
v_exp_f32 v205,v205
v_mfma_f32_16x16x32_bf16 v[248:251],a[172:175],v[244:247],v[248:251]
ds_read_b128 a[152:155],v1 offset:4608
v_cvt_pk_bf16_f32 v202,v204,v205
v_mfma_f32_16x16x32_bf16 v[252:255],a[188:191],v[244:247],v[44:47]
v_exp_f32 v206,v206
v_mfma_f32_16x16x32_bf16 v[252:255],a[184:187],v[212:215],v[252:255]
ds_read_b128 a[148:151],v1 offset:4352
v_mul_u32_u24 v25,320,v25
v_mfma_f32_16x16x32_bf16 v[252:255],a[180:183],v[180:183],v[252:255]
v_exp_f32 v207,v207
v_mfma_f32_16x16x32_bf16 v[252:255],a[176:179],v[148:151],v[252:255]
ds_read_b128 a[144:147],v1 offset:4096
v_cvt_pk_bf16_f32 v203,v206,v207
v_mfma_f32_16x16x32_bf16 a[32:35],a[192:195],v[168:171],a[32:35]
v_exp_f32 v216,v216
v_mfma_f32_16x16x32_bf16 a[32:35],a[196:199],v[172:175],a[32:35]
s_add_u32 s28,s28,s43
s_addc_u32 s29,s29,0
ds_read_b128 a[160:163],v1 offset:8192
v_mfma_f32_16x16x32_bf16 a[36:39],a[204:207],v[172:175],a[36:39]
s_and_b32 s29,s29,0xffff
v_exp_f32 v217,v217
v_mfma_f32_16x16x32_bf16 a[36:39],a[200:203],v[168:171],a[36:39]
ds_read_b128 a[164:167],v1 offset:8448
v_cvt_pk_bf16_f32 v204,v216,v217
v_mfma_f32_16x16x32_bf16 a[40:43],a[208:211],v[168:171],a[40:43]
v_exp_f32 v218,v218
v_mfma_f32_16x16x32_bf16 a[40:43],a[212:215],v[172:175],a[40:43]
ds_read_b128 a[168:171],v1 offset:8704
v_mfma_f32_16x16x32_bf16 a[44:47],a[220:223],v[172:175],a[44:47]
v_exp_f32 v219,v219
v_mfma_f32_16x16x32_bf16 a[44:47],a[216:219],v[168:171],a[44:47]
ds_read_b128 a[172:175],v1 offset:8960
v_cvt_pk_bf16_f32 v205,v218,v219
v_mfma_f32_16x16x32_bf16 a[48:51],a[224:227],v[168:171],a[48:51]
v_exp_f32 v220,v220
v_mfma_f32_16x16x32_bf16 a[48:51],a[228:231],v[172:175],a[48:51]
ds_read_b128 a[188:191],v1 offset:13056
v_mfma_f32_16x16x32_bf16 a[52:55],a[236:239],v[172:175],a[52:55]
v_exp_f32 v221,v221
v_mfma_f32_16x16x32_bf16 a[52:55],a[232:235],v[168:171],a[52:55]
ds_read_b128 a[184:187],v1 offset:12800
v_cvt_pk_bf16_f32 v206,v220,v221
v_mfma_f32_16x16x32_bf16 a[56:59],a[240:243],v[168:171],a[56:59]
v_exp_f32 v222,v222
v_mfma_f32_16x16x32_bf16 a[56:59],a[244:247],v[172:175],a[56:59]
ds_read_b128 a[180:183],v1 offset:12544
v_mfma_f32_16x16x32_bf16 a[60:63],a[252:255],v[172:175],a[60:63]
v_exp_f32 v223,v223
v_mfma_f32_16x16x32_bf16 a[60:63],a[248:251],v[168:171],a[60:63]
ds_read_b128 a[176:179],v1 offset:12288
v_cvt_pk_bf16_f32 v207,v222,v223
v_smfmac_f32_16x16x64_bf16 v[52:55],v[28:31],v[168:175],v27
s_waitcnt lgkmcnt(4)
v_add_u32 v1,s48,v1
v_mfma_f32_16x16x32_bf16 v[136:139],a[128:131],v[128:131],v[32:35]
v_exp_f32 v232,v232
v_mfma_f32_16x16x32_bf16 v[136:139],a[132:135],v[160:163],v[136:139]
v_exp_f32 v233,v233
v_mfma_f32_16x16x32_bf16 v[136:139],a[136:139],v[192:195],v[136:139]
v_exp_f32 v234,v234
v_mfma_f32_16x16x32_bf16 v[136:139],a[140:143],v[224:227],v[136:139]
v_exp_f32 v235,v235
v_mfma_f32_16x16x32_bf16 v[140:143],a[156:159],v[224:227],v[32:35]
v_exp_f32 v236,v236
v_mfma_f32_16x16x32_bf16 v[140:143],a[152:155],v[192:195],v[140:143]
s_add_u32 s80,s71,0
s_and_b32 s80,s80,3
s_lshl_b32 s80,s80,1
v_mfma_f32_16x16x32_bf16 v[140:143],a[148:151],v[160:163],v[140:143]
s_mul_i32 s80,s80,163840
s_add_u32 s80,s80,s81
v_cvt_pk_bf16_f32 v232,v232,v233
v_mfma_f32_16x16x32_bf16 v[140:143],a[144:147],v[128:131],v[140:143]
s_add_u32 s83,s80,20480
v_exp_f32 v237,v237
v_mfma_f32_16x16x32_bf16 v[152:155],a[160:163],v[128:131],v[32:35]
v_exp_f32 v238,v238
v_mfma_f32_16x16x32_bf16 v[152:155],a[164:167],v[160:163],v[152:155]
buffer_store_dwordx4 v[64:67],v6,s[36:39],s84 offen sc1
v_mfma_f32_16x16x32_bf16 v[152:155],a[168:171],v[192:195],v[152:155]
v_exp_f32 v239,v239
v_mfma_f32_16x16x32_bf16 v[152:155],a[172:175],v[224:227],v[152:155]
s_waitcnt lgkmcnt(0)
v_exp_f32 v248,v248
v_mfma_f32_16x16x32_bf16 v[156:159],a[188:191],v[224:227],v[32:35]
v_exp_f32 v249,v249
v_mfma_f32_16x16x32_bf16 v[156:159],a[184:187],v[192:195],v[156:159]
v_exp_f32 v250,v250
v_mfma_f32_16x16x32_bf16 v[156:159],a[180:183],v[160:163],v[156:159]
v_exp_f32 v251,v251
v_mfma_f32_16x16x32_bf16 v[156:159],a[176:179],v[128:131],v[156:159]
v_exp_f32 v252,v252
v_mfma_f32_16x16x32_bf16 a[64:67],a[192:195],v[200:203],a[64:67]
v_exp_f32 v253,v253
v_mfma_f32_16x16x32_bf16 a[64:67],a[196:199],v[204:207],a[64:67]
v_exp_f32 v254,v254
v_mfma_f32_16x16x32_bf16 a[68:71],a[204:207],v[204:207],a[68:71]
v_exp_f32 v255,v255
v_mfma_f32_16x16x32_bf16 a[68:71],a[200:203],v[200:203],a[68:71]
v_exp_f32 v136,v136
v_mfma_f32_16x16x32_bf16 a[72:75],a[208:211],v[200:203],a[72:75]
v_exp_f32 v137,v137
v_mfma_f32_16x16x32_bf16 a[72:75],a[212:215],v[204:207],a[72:75]
v_cvt_pk_bf16_f32 v233,v234,v235
v_cvt_pk_bf16_f32 v234,v236,v237
v_mfma_f32_16x16x32_bf16 a[76:79],a[220:223],v[204:207],a[76:79]
v_cvt_pk_bf16_f32 v235,v238,v239
v_cvt_pk_bf16_f32 v236,v248,v249
v_mfma_f32_16x16x32_bf16 a[76:79],a[216:219],v[200:203],a[76:79]
v_cvt_pk_bf16_f32 v237,v250,v251
v_cvt_pk_bf16_f32 v238,v252,v253
v_mfma_f32_16x16x32_bf16 a[80:83],a[224:227],v[200:203],a[80:83]
v_cvt_pk_bf16_f32 v239,v254,v255
v_cvt_pk_bf16_f32 v136,v136,v137
v_mfma_f32_16x16x32_bf16 a[80:83],a[228:231],v[204:207],a[80:83]
buffer_store_dwordx4 v[68:71],v6,s[36:39],s84 offen offset:1024 sc1
v_mfma_f32_16x16x32_bf16 a[84:87],a[236:239],v[204:207],a[84:87]
s_add_u32 m0,s82,81920
v_exp_f32 v138,v138
v_mfma_f32_16x16x32_bf16 a[84:87],a[232:235],v[200:203],a[84:87]
v_exp_f32 v139,v139
v_mfma_f32_16x16x32_bf16 a[88:91],a[240:243],v[200:203],a[88:91]
v_exp_f32 v140,v140
v_mfma_f32_16x16x32_bf16 a[88:91],a[244:247],v[204:207],a[88:91]
buffer_load_dwordx4 v7,s[72:75],s80 offen lds
v_mfma_f32_16x16x32_bf16 a[92:95],a[252:255],v[204:207],a[92:95]
v_exp_f32 v141,v141
v_mfma_f32_16x16x32_bf16 a[92:95],a[248:251],v[200:203],a[92:95]
v_exp_f32 v142,v142
v_smfmac_f32_16x16x64_bf16 v[56:59],v[28:31],v[200:207],v27
v_cvt_pk_bf16_f32 v137,v138,v139
v_mfma_f32_16x16x32_bf16 v[168:171],a[128:131],v[132:135],v[36:39]
v_exp_f32 v143,v143
v_mfma_f32_16x16x32_bf16 v[168:171],a[132:135],v[164:167],v[168:171]
v_cvt_pk_bf16_f32 v138,v140,v141
v_cvt_pk_bf16_f32 v139,v142,v143
v_mfma_f32_16x16x32_bf16 v[168:171],a[136:139],v[196:199],v[168:171]
v_exp_f32 v152,v152
v_mfma_f32_16x16x32_bf16 v[168:171],a[140:143],v[228:231],v[168:171]
v_exp_f32 v153,v153
v_mfma_f32_16x16x32_bf16 v[172:175],a[156:159],v[228:231],v[36:39]
v_exp_f32 v154,v154
v_mfma_f32_16x16x32_bf16 v[172:175],a[152:155],v[196:199],v[172:175]
v_exp_f32 v155,v155
v_mfma_f32_16x16x32_bf16 v[172:175],a[148:151],v[164:167],v[172:175]
v_exp_f32 v156,v156
v_mfma_f32_16x16x32_bf16 v[172:175],a[144:147],v[132:135],v[172:175]
v_exp_f32 v157,v157
v_mfma_f32_16x16x32_bf16 v[184:187],a[160:163],v[132:135],v[36:39]
buffer_store_dwordx4 v[72:75],v6,s[36:39],s84 offen offset:2048 sc1
v_mfma_f32_16x16x32_bf16 v[184:187],a[164:167],v[164:167],v[184:187]
v_exp_f32 v158,v158
v_mfma_f32_16x16x32_bf16 v[184:187],a[168:171],v[196:199],v[184:187]
v_exp_f32 v159,v159
v_mfma_f32_16x16x32_bf16 v[184:187],a[172:175],v[228:231],v[184:187]
v_cvt_pk_bf16_f32 v140,v152,v153
v_cvt_pk_bf16_f32 v141,v154,v155
v_mfma_f32_16x16x32_bf16 v[188:191],a[188:191],v[228:231],v[36:39]
v_cvt_pk_bf16_f32 v142,v156,v157
v_cvt_pk_bf16_f32 v143,v158,v159
v_mfma_f32_16x16x32_bf16 v[188:191],a[184:187],v[196:199],v[188:191]
v_exp_f32 v168,v168
v_mfma_f32_16x16x32_bf16 v[188:191],a[180:183],v[164:167],v[188:191]
v_exp_f32 v169,v169
v_mfma_f32_16x16x32_bf16 v[188:191],a[176:179],v[132:135],v[188:191]
v_exp_f32 v170,v170
v_mfma_f32_16x16x32_bf16 a[96:99],a[192:195],v[232:235],a[96:99]
buffer_load_dwordx4 v8,s[72:75],s80 offen offset:1024 lds
v_mfma_f32_16x16x32_bf16 a[96:99],a[196:199],v[236:239],a[96:99]
ds_read_b64_tr_b16 a[192:193],v2
ds_read_b64_tr_b16 a[194:195],v2 offset:4096
v_mfma_f32_16x16x32_bf16 a[100:103],a[204:207],v[236:239],a[100:103]
ds_read_b64_tr_b16 a[196:197],v2 offset:8192
v_cvt_pk_bf16_f32 v168,v168,v169
v_mfma_f32_16x16x32_bf16 a[100:103],a[200:203],v[232:235],a[100:103]
v_exp_f32 v171,v171
v_mfma_f32_16x16x32_bf16 a[104:107],a[208:211],v[232:235],a[104:107]
ds_read_b64_tr_b16 a[198:199],v2 offset:12288
ds_read_b64_tr_b16 a[204:205],v3 offset:8192
v_mfma_f32_16x16x32_bf16 a[104:107],a[212:215],v[236:239],a[104:107]
ds_read_b64_tr_b16 a[206:207],v3 offset:12288
v_cvt_pk_bf16_f32 v169,v170,v171
v_mfma_f32_16x16x32_bf16 a[108:111],a[220:223],v[236:239],a[108:111]
)A"
R"A(v_exp_f32 v172,v172
v_mfma_f32_16x16x32_bf16 a[108:111],a[216:219],v[232:235],a[108:111]
ds_read_b64_tr_b16 a[200:201],v3
ds_read_b64_tr_b16 a[202:203],v3 offset:4096
v_mfma_f32_16x16x32_bf16 a[112:115],a[224:227],v[232:235],a[112:115]
buffer_store_dwordx4 v[76:79],v6,s[36:39],s84 offen offset:3072 sc1
v_mfma_f32_16x16x32_bf16 a[112:115],a[228:231],v[236:239],a[112:115]
s_add_u32 s84,s55,131072
ds_read_b64_tr_b16 a[208:209],v2 offset:256
ds_read_b64_tr_b16 a[210:211],v2 offset:4352
v_mfma_f32_16x16x32_bf16 a[116:119],a[236:239],v[236:239],a[116:119]
s_max_i32 s84,s84,s18
v_exp_f32 v173,v173
v_mfma_f32_16x16x32_bf16 a[116:119],a[232:235],v[232:235],a[116:119]
ds_read_b64_tr_b16 a[212:213],v2 offset:8448
ds_read_b64_tr_b16 a[214:215],v2 offset:12544
v_mfma_f32_16x16x32_bf16 a[120:123],a[240:243],v[232:235],a[120:123]
ds_read_b64_tr_b16 a[220:221],v3 offset:8448
v_cvt_pk_bf16_f32 v170,v172,v173
v_mfma_f32_16x16x32_bf16 a[120:123],a[244:247],v[236:239],a[120:123]
ds_read_b64_tr_b16 a[222:223],v3 offset:12544
v_mfma_f32_16x16x32_bf16 a[124:127],a[252:255],v[236:239],a[124:127]
ds_read_b64_tr_b16 a[216:217],v3 offset:256
v_mfma_f32_16x16x32_bf16 a[124:127],a[248:251],v[232:235],a[124:127]
ds_read_b64_tr_b16 a[218:219],v3 offset:4352
ds_read_b64_tr_b16 a[224:225],v2 offset:512
v_smfmac_f32_16x16x64_bf16 v[60:63],v[28:31],v[232:239],v27
ds_read_b64_tr_b16 a[226:227],v2 offset:4608
v_mfma_f32_16x16x32_bf16 v[200:203],a[128:131],v[144:147],v[40:43]
ds_read_b64_tr_b16 a[228:229],v2 offset:8704
v_mfma_f32_16x16x32_bf16 v[200:203],a[132:135],v[176:179],v[200:203]
ds_read_b64_tr_b16 a[230:231],v2 offset:12800
v_mfma_f32_16x16x32_bf16 v[200:203],a[136:139],v[208:211],v[200:203]
ds_read_b64_tr_b16 a[236:237],v3 offset:8704
ds_read_b64_tr_b16 a[238:239],v3 offset:12800
v_mfma_f32_16x16x32_bf16 v[200:203],a[140:143],v[240:243],v[200:203]
buffer_load_dwordx4 v9,s[72:75],s80 offen offset:2048 lds
v_mfma_f32_16x16x32_bf16 v[204:207],a[156:159],v[240:243],v[40:43]
ds_read_b64_tr_b16 a[232:233],v3 offset:512
ds_read_b64_tr_b16 a[234:235],v3 offset:4608
v_mfma_f32_16x16x32_bf16 v[204:207],a[152:155],v[208:211],v[204:207]
ds_read_b64_tr_b16 a[240:241],v2 offset:768
v_mfma_f32_16x16x32_bf16 v[204:207],a[148:151],v[176:179],v[204:207]
ds_read_b64_tr_b16 a[242:243],v2 offset:4864
v_mfma_f32_16x16x32_bf16 v[204:207],a[144:147],v[144:147],v[204:207]
buffer_store_dwordx4 v[80:83],v6,s[36:39],s85 offen sc1
v_mfma_f32_16x16x32_bf16 v[216:219],a[160:163],v[144:147],v[40:43]
ds_read_b64_tr_b16 a[244:245],v2 offset:8960
ds_read_b64_tr_b16 a[246:247],v2 offset:13056
v_mfma_f32_16x16x32_bf16 v[216:219],a[164:167],v[176:179],v[216:219]
ds_read_b64_tr_b16 a[252:253],v3 offset:8960
v_add_u32 v2,s48,v2
v_mfma_f32_16x16x32_bf16 v[216:219],a[168:171],v[208:211],v[216:219]
ds_read_b64_tr_b16 a[254:255],v3 offset:13056
v_mfma_f32_16x16x32_bf16 v[216:219],a[172:175],v[240:243],v[216:219]
ds_read_b64_tr_b16 a[248:249],v3 offset:768
v_mfma_f32_16x16x32_bf16 v[220:223],a[188:191],v[240:243],v[40:43]
ds_read_b64_tr_b16 a[250:251],v3 offset:4864
v_add_u32 v3,s48,v3
v_mfma_f32_16x16x32_bf16 v[220:223],a[184:187],v[208:211],v[220:223]
s_mov_b32 s48,98304
v_exp_f32 v174,v174
v_mfma_f32_16x16x32_bf16 v[220:223],a[180:183],v[176:179],v[220:223]
v_exp_f32 v175,v175
v_mfma_f32_16x16x32_bf16 v[220:223],a[176:179],v[144:147],v[220:223]
s_waitcnt lgkmcnt(6)
v_exp_f32 v184,v184
v_mfma_f32_16x16x32_bf16 a[0:3],a[192:195],v[136:139],a[0:3]
v_exp_f32 v185,v185
v_mfma_f32_16x16x32_bf16 a[0:3],a[196:199],v[140:143],a[0:3]
v_cvt_pk_bf16_f32 v171,v174,v175
v_cvt_pk_bf16_f32 v172,v184,v185
v_mfma_f32_16x16x32_bf16 a[4:7],a[204:207],v[140:143],a[4:7]
v_exp_f32 v186,v186
v_mfma_f32_16x16x32_bf16 a[4:7],a[200:203],v[136:139],a[4:7]
v_exp_f32 v187,v187
v_mfma_f32_16x16x32_bf16 a[8:11],a[208:211],v[136:139],a[8:11]
v_exp_f32 v188,v188
v_mfma_f32_16x16x32_bf16 a[8:11],a[212:215],v[140:143],a[8:11]
v_exp_f32 v189,v189
v_mfma_f32_16x16x32_bf16 a[12:15],a[220:223],v[140:143],a[12:15]
v_exp_f32 v190,v190
v_mfma_f32_16x16x32_bf16 a[12:15],a[216:219],v[136:139],a[12:15]
buffer_store_dwordx4 v[84:87],v6,s[36:39],s85 offen offset:1024 sc1
v_mfma_f32_16x16x32_bf16 a[16:19],a[224:227],v[136:139],a[16:19]
v_exp_f32 v191,v191
v_mfma_f32_16x16x32_bf16 a[16:19],a[228:231],v[140:143],a[16:19]
v_cvt_pk_bf16_f32 v173,v186,v187
v_cvt_pk_bf16_f32 v174,v188,v189
v_mfma_f32_16x16x32_bf16 a[20:23],a[236:239],v[140:143],a[20:23]
v_exp_f32 v200,v200
v_mfma_f32_16x16x32_bf16 a[20:23],a[232:235],v[136:139],a[20:23]
buffer_load_dwordx4 v10,s[72:75],s80 offen offset:3072 lds
v_mfma_f32_16x16x32_bf16 a[24:27],a[240:243],v[136:139],a[24:27]
s_waitcnt lgkmcnt(0)
v_exp_f32 v201,v201
v_mfma_f32_16x16x32_bf16 a[24:27],a[244:247],v[140:143],a[24:27]
v_cvt_pk_bf16_f32 v175,v190,v191
v_cvt_pk_bf16_f32 v200,v200,v201
v_mfma_f32_16x16x32_bf16 a[28:31],a[252:255],v[140:143],a[28:31]
v_exp_f32 v202,v202
v_mfma_f32_16x16x32_bf16 a[28:31],a[248:251],v[136:139],a[28:31]
v_exp_f32 v203,v203
v_smfmac_f32_16x16x64_bf16 v[48:51],v[28:31],v[136:143],v27
v_cvt_pk_bf16_f32 v201,v202,v203
v_mfma_f32_16x16x32_bf16 v[232:235],a[128:131],v[148:151],v[44:47]
s_waitcnt vmcnt(11)
v_exp_f32 v204,v204
v_mfma_f32_16x16x32_bf16 v[232:235],a[132:135],v[180:183],v[232:235]
s_barrier
v_exp_f32 v205,v205
v_mfma_f32_16x16x32_bf16 v[232:235],a[136:139],v[212:215],v[232:235]
ds_read_b128 a[128:131],v1
v_cvt_pk_bf16_f32 v202,v204,v205
v_mfma_f32_16x16x32_bf16 v[232:235],a[140:143],v[244:247],v[232:235]
v_exp_f32 v206,v206
v_mfma_f32_16x16x32_bf16 v[236:239],a[156:159],v[244:247],v[44:47]
v_exp_f32 v207,v207
v_mfma_f32_16x16x32_bf16 v[236:239],a[152:155],v[212:215],v[236:239]
buffer_store_dwordx4 v[88:91],v6,s[36:39],s85 offen offset:2048 sc1
v_mfma_f32_16x16x32_bf16 v[236:239],a[148:151],v[180:183],v[236:239]
s_addk_i32 m0,4096
ds_read_b128 a[132:135],v1 offset:256
v_cvt_pk_bf16_f32 v203,v206,v207
v_mfma_f32_16x16x32_bf16 v[236:239],a[144:147],v[148:151],v[236:239]
v_exp_f32 v216,v216
v_mfma_f32_16x16x32_bf16 v[248:251],a[160:163],v[148:151],v[44:47]
v_exp_f32 v217,v217
v_mfma_f32_16x16x32_bf16 v[248:251],a[164:167],v[180:183],v[248:251]
buffer_load_dwordx4 v11,s[72:75],s80 offen lds
v_mfma_f32_16x16x32_bf16 v[248:251],a[168:171],v[212:215],v[248:251]
ds_read_b128 a[136:139],v1 offset:512
v_cvt_pk_bf16_f32 v204,v216,v217
v_mfma_f32_16x16x32_bf16 v[248:251],a[172:175],v[244:247],v[248:251]
v_exp_f32 v218,v218
v_mfma_f32_16x16x32_bf16 v[252:255],a[188:191],v[244:247],v[44:47]
ds_read_b128 a[140:143],v1 offset:768
v_mfma_f32_16x16x32_bf16 v[252:255],a[184:187],v[212:215],v[252:255]
v_exp_f32 v219,v219
v_mfma_f32_16x16x32_bf16 v[252:255],a[180:183],v[180:183],v[252:255]
ds_read_b128 a[156:159],v1 offset:4864
v_cvt_pk_bf16_f32 v205,v218,v219
v_mfma_f32_16x16x32_bf16 v[252:255],a[176:179],v[148:151],v[252:255]
v_exp_f32 v220,v220
v_mfma_f32_16x16x32_bf16 a[32:35],a[192:195],v[168:171],a[32:35]
ds_read_b128 a[152:155],v1 offset:4608
v_mfma_f32_16x16x32_bf16 a[32:35],a[196:199],v[172:175],a[32:35]
v_exp_f32 v221,v221
v_mfma_f32_16x16x32_bf16 a[36:39],a[204:207],v[172:175],a[36:39]
ds_read_b128 a[148:151],v1 offset:4352
v_cvt_pk_bf16_f32 v206,v220,v221
v_mfma_f32_16x16x32_bf16 a[36:39],a[200:203],v[168:171],a[36:39]
v_exp_f32 v222,v222
v_mfma_f32_16x16x32_bf16 a[40:43],a[208:211],v[168:171],a[40:43]
ds_read_b128 a[144:147],v1 offset:4096
v_mfma_f32_16x16x32_bf16 a[40:43],a[212:215],v[172:175],a[40:43]
buffer_store_dwordx4 v[92:95],v6,s[36:39],s85 offen offset:3072 sc1
v_mfma_f32_16x16x32_bf16 a[44:47],a[220:223],v[172:175],a[44:47]
s_add_u32 s85,s55,135168
s_max_i32 s85,s85,s19
ds_read_b128 a[160:163],v1 offset:8192
v_mfma_f32_16x16x32_bf16 a[44:47],a[216:219],v[168:171],a[44:47]
v_exp_f32 v223,v223
v_mfma_f32_16x16x32_bf16 a[48:51],a[224:227],v[168:171],a[48:51]
ds_read_b128 a[164:167],v1 offset:8448
v_cvt_pk_bf16_f32 v207,v222,v223
v_mfma_f32_16x16x32_bf16 a[48:51],a[228:231],v[172:175],a[48:51]
v_exp_f32 v232,v232
v_mfma_f32_16x16x32_bf16 a[52:55],a[236:239],v[172:175],a[52:55]
s_addk_i32 m0,1024
ds_read_b128 a[168:171],v1 offset:8704
v_mfma_f32_16x16x32_bf16 a[52:55],a[232:235],v[168:171],a[52:55]
v_exp_f32 v233,v233
v_mfma_f32_16x16x32_bf16 a[56:59],a[240:243],v[168:171],a[56:59]
ds_read_b128 a[172:175],v1 offset:8960
v_cvt_pk_bf16_f32 v232,v232,v233
v_mfma_f32_16x16x32_bf16 a[56:59],a[244:247],v[172:175],a[56:59]
buffer_load_dwordx4 v7,s[72:75],s83 offen lds
v_mfma_f32_16x16x32_bf16 a[60:63],a[252:255],v[172:175],a[60:63]
ds_read_b128 a[188:191],v1 offset:13056
v_mfma_f32_16x16x32_bf16 a[60:63],a[248:251],v[168:171],a[60:63]
v_exp_f32 v234,v234
v_smfmac_f32_16x16x64_bf16 v[52:55],v[28:31],v[168:175],v27
s_waitcnt lgkmcnt(3)
ds_read_b128 a[184:187],v1 offset:12800
v_mfma_f32_16x16x32_bf16 v[136:139],a[128:131],v[128:131],v[32:35]
v_exp_f32 v235,v235
v_mfma_f32_16x16x32_bf16 v[136:139],a[132:135],v[160:163],v[136:139]
ds_read_b128 a[180:183],v1 offset:12544
v_cvt_pk_bf16_f32 v233,v234,v235
v_mfma_f32_16x16x32_bf16 v[136:139],a[136:139],v[192:195],v[136:139]
v_exp_f32 v236,v236
v_mfma_f32_16x16x32_bf16 v[136:139],a[140:143],v[224:227],v[136:139]
ds_read_b128 a[176:179],v1 offset:12288
v_add_u32 v1,s48,v1
v_mfma_f32_16x16x32_bf16 v[140:143],a[156:159],v[224:227],v[32:35]
buffer_store_dwordx4 v[96:99],v6,s[36:39],s86 offen sc1
v_mfma_f32_16x16x32_bf16 v[140:143],a[152:155],v[192:195],v[140:143]
v_exp_f32 v237,v237
v_mfma_f32_16x16x32_bf16 v[140:143],a[148:151],v[160:163],v[140:143]
v_exp_f32 v238,v238
v_mfma_f32_16x16x32_bf16 v[140:143],a[144:147],v[128:131],v[140:143]
v_exp_f32 v239,v239
v_mfma_f32_16x16x32_bf16 v[152:155],a[160:163],v[128:131],v[32:35]
v_cvt_pk_bf16_f32 v234,v236,v237
v_cvt_pk_bf16_f32 v235,v238,v239
v_mfma_f32_16x16x32_bf16 v[152:155],a[164:167],v[160:163],v[152:155]
s_waitcnt lgkmcnt(1)
v_exp_f32 v248,v248
v_mfma_f32_16x16x32_bf16 v[152:155],a[168:171],v[192:195],v[152:155]
v_exp_f32 v249,v249
v_mfma_f32_16x16x32_bf16 v[152:155],a[172:175],v[224:227],v[152:155]
v_exp_f32 v250,v250
v_mfma_f32_16x16x32_bf16 v[156:159],a[188:191],v[224:227],v[32:35]
v_exp_f32 v251,v251
v_mfma_f32_16x16x32_bf16 v[156:159],a[184:187],v[192:195],v[156:159]
v_exp_f32 v252,v252
v_mfma_f32_16x16x32_bf16 v[156:159],a[180:183],v[160:163],v[156:159]
s_waitcnt lgkmcnt(0)
v_exp_f32 v253,v253
v_mfma_f32_16x16x32_bf16 v[156:159],a[176:179],v[128:131],v[156:159]
v_exp_f32 v254,v254
v_mfma_f32_16x16x32_bf16 a[64:67],a[192:195],v[200:203],a[64:67]
buffer_load_dwordx4 v8,s[72:75],s83 offen offset:1024 lds
v_mfma_f32_16x16x32_bf16 a[64:67],a[196:199],v[204:207],a[64:67]
v_exp_f32 v255,v255
v_mfma_f32_16x16x32_bf16 a[68:71],a[204:207],v[204:207],a[68:71]
v_cvt_pk_bf16_f32 v236,v248,v249
v_cvt_pk_bf16_f32 v237,v250,v251
v_mfma_f32_16x16x32_bf16 a[68:71],a[200:203],v[200:203],a[68:71]
v_cvt_pk_bf16_f32 v238,v252,v253
v_cvt_pk_bf16_f32 v239,v254,v255
v_mfma_f32_16x16x32_bf16 a[72:75],a[208:211],v[200:203],a[72:75]
buffer_store_dwordx4 v[100:103],v6,s[36:39],s86 offen offset:1024 sc1
v_mfma_f32_16x16x32_bf16 a[72:75],a[212:215],v[204:207],a[72:75]
v_exp_f32 v136,v136
v_mfma_f32_16x16x32_bf16 a[76:79],a[220:223],v[204:207],a[76:79]
v_exp_f32 v137,v137
v_mfma_f32_16x16x32_bf16 a[76:79],a[216:219],v[200:203],a[76:79]
v_exp_f32 v138,v138
v_mfma_f32_16x16x32_bf16 a[80:83],a[224:227],v[200:203],a[80:83]
v_exp_f32 v139,v139
v_mfma_f32_16x16x32_bf16 a[80:83],a[228:231],v[204:207],a[80:83]
v_exp_f32 v140,v140
v_mfma_f32_16x16x32_bf16 a[84:87],a[236:239],v[204:207],a[84:87]
v_exp_f32 v141,v141
v_mfma_f32_16x16x32_bf16 a[84:87],a[232:235],v[200:203],a[84:87]
v_exp_f32 v142,v142
v_mfma_f32_16x16x32_bf16 a[88:91],a[240:243],v[200:203],a[88:91]
v_exp_f32 v143,v143
v_mfma_f32_16x16x32_bf16 a[88:91],a[244:247],v[204:207],a[88:91]
v_exp_f32 v152,v152
v_mfma_f32_16x16x32_bf16 a[92:95],a[252:255],v[204:207],a[92:95]
v_exp_f32 v153,v153
v_mfma_f32_16x16x32_bf16 a[92:95],a[248:251],v[200:203],a[92:95]
v_exp_f32 v154,v154
v_smfmac_f32_16x16x64_bf16 v[56:59],v[28:31],v[200:207],v27
v_cvt_pk_bf16_f32 v136,v136,v137
v_mfma_f32_16x16x32_bf16 v[168:171],a[128:131],v[132:135],v[36:39]
)A"
R"A(v_exp_f32 v155,v155
v_mfma_f32_16x16x32_bf16 v[168:171],a[132:135],v[164:167],v[168:171]
v_exp_f32 v156,v156
v_mfma_f32_16x16x32_bf16 v[168:171],a[136:139],v[196:199],v[168:171]
v_exp_f32 v157,v157
v_mfma_f32_16x16x32_bf16 v[168:171],a[140:143],v[228:231],v[168:171]
buffer_store_dwordx4 v[104:107],v6,s[36:39],s86 offen offset:2048 sc1
v_mfma_f32_16x16x32_bf16 v[172:175],a[156:159],v[228:231],v[36:39]
v_exp_f32 v158,v158
v_mfma_f32_16x16x32_bf16 v[172:175],a[152:155],v[196:199],v[172:175]
v_exp_f32 v159,v159
v_mfma_f32_16x16x32_bf16 v[172:175],a[148:151],v[164:167],v[172:175]
v_cvt_pk_bf16_f32 v137,v138,v139
v_cvt_pk_bf16_f32 v138,v140,v141
v_mfma_f32_16x16x32_bf16 v[172:175],a[144:147],v[132:135],v[172:175]
buffer_load_dwordx4 v9,s[72:75],s83 offen offset:2048 lds
v_mfma_f32_16x16x32_bf16 v[184:187],a[160:163],v[132:135],v[36:39]
v_cvt_pk_bf16_f32 v139,v142,v143
v_cvt_pk_bf16_f32 v140,v152,v153
v_mfma_f32_16x16x32_bf16 v[184:187],a[164:167],v[164:167],v[184:187]
v_cvt_pk_bf16_f32 v141,v154,v155
v_cvt_pk_bf16_f32 v142,v156,v157
v_mfma_f32_16x16x32_bf16 v[184:187],a[168:171],v[196:199],v[184:187]
v_exp_f32 v168,v168
v_mfma_f32_16x16x32_bf16 v[184:187],a[172:175],v[228:231],v[184:187]
v_exp_f32 v169,v169
v_mfma_f32_16x16x32_bf16 v[188:191],a[188:191],v[228:231],v[36:39]
v_cvt_pk_bf16_f32 v143,v158,v159
v_cvt_pk_bf16_f32 v168,v168,v169
v_mfma_f32_16x16x32_bf16 v[188:191],a[184:187],v[196:199],v[188:191]
v_exp_f32 v170,v170
v_mfma_f32_16x16x32_bf16 v[188:191],a[180:183],v[164:167],v[188:191]
v_exp_f32 v171,v171
v_mfma_f32_16x16x32_bf16 v[188:191],a[176:179],v[132:135],v[188:191]
v_exp_f32 v172,v172
v_mfma_f32_16x16x32_bf16 a[96:99],a[192:195],v[232:235],a[96:99]
ds_read_b64_tr_b16 a[192:193],v2
ds_read_b64_tr_b16 a[194:195],v2 offset:4096
v_mfma_f32_16x16x32_bf16 a[96:99],a[196:199],v[236:239],a[96:99]
ds_read_b64_tr_b16 a[196:197],v2 offset:8192
v_cvt_pk_bf16_f32 v169,v170,v171
v_mfma_f32_16x16x32_bf16 a[100:103],a[204:207],v[236:239],a[100:103]
v_exp_f32 v173,v173
v_mfma_f32_16x16x32_bf16 a[100:103],a[200:203],v[232:235],a[100:103]
buffer_store_dwordx4 v[108:111],v6,s[36:39],s86 offen offset:3072 sc1
v_mfma_f32_16x16x32_bf16 a[104:107],a[208:211],v[232:235],a[104:107]
s_add_u32 s86,s55,147456
ds_read_b64_tr_b16 a[198:199],v2 offset:12288
ds_read_b64_tr_b16 a[204:205],v3 offset:8192
v_mfma_f32_16x16x32_bf16 a[104:107],a[212:215],v[236:239],a[104:107]
s_max_i32 s86,s86,s100
ds_read_b64_tr_b16 a[206:207],v3 offset:12288
v_cvt_pk_bf16_f32 v170,v172,v173
v_mfma_f32_16x16x32_bf16 a[108:111],a[220:223],v[236:239],a[108:111]
v_exp_f32 v174,v174
v_mfma_f32_16x16x32_bf16 a[108:111],a[216:219],v[232:235],a[108:111]
buffer_load_dwordx4 v10,s[72:75],s83 offen offset:3072 lds
v_mfma_f32_16x16x32_bf16 a[112:115],a[224:227],v[232:235],a[112:115]
ds_read_b64_tr_b16 a[200:201],v3
ds_read_b64_tr_b16 a[202:203],v3 offset:4096
v_mfma_f32_16x16x32_bf16 a[112:115],a[228:231],v[236:239],a[112:115]
v_exp_f32 v175,v175
v_mfma_f32_16x16x32_bf16 a[116:119],a[236:239],v[236:239],a[116:119]
ds_read_b64_tr_b16 a[208:209],v2 offset:256
ds_read_b64_tr_b16 a[210:211],v2 offset:4352
v_mfma_f32_16x16x32_bf16 a[116:119],a[232:235],v[232:235],a[116:119]
ds_read_b64_tr_b16 a[212:213],v2 offset:8448
v_cvt_pk_bf16_f32 v171,v174,v175
v_mfma_f32_16x16x32_bf16 a[120:123],a[240:243],v[232:235],a[120:123]
ds_read_b64_tr_b16 a[214:215],v2 offset:12544
v_mfma_f32_16x16x32_bf16 a[120:123],a[244:247],v[236:239],a[120:123]
ds_read_b64_tr_b16 a[220:221],v3 offset:8448
v_mfma_f32_16x16x32_bf16 a[124:127],a[252:255],v[236:239],a[124:127]
ds_read_b64_tr_b16 a[222:223],v3 offset:12544
ds_read_b64_tr_b16 a[216:217],v3 offset:256
v_mfma_f32_16x16x32_bf16 a[124:127],a[248:251],v[232:235],a[124:127]
ds_read_b64_tr_b16 a[218:219],v3 offset:4352
v_smfmac_f32_16x16x64_bf16 v[60:63],v[28:31],v[232:239],v27
ds_read_b64_tr_b16 a[224:225],v2 offset:512
v_mfma_f32_16x16x32_bf16 v[200:203],a[128:131],v[144:147],v[40:43]
ds_read_b64_tr_b16 a[226:227],v2 offset:4608
v_mfma_f32_16x16x32_bf16 v[200:203],a[132:135],v[176:179],v[200:203]
ds_read_b64_tr_b16 a[228:229],v2 offset:8704
ds_read_b64_tr_b16 a[230:231],v2 offset:12800
v_mfma_f32_16x16x32_bf16 v[200:203],a[136:139],v[208:211],v[200:203]
buffer_store_dwordx4 v[112:115],v6,s[36:39],s87 offen sc1
v_mfma_f32_16x16x32_bf16 v[200:203],a[140:143],v[240:243],v[200:203]
ds_read_b64_tr_b16 a[236:237],v3 offset:8704
ds_read_b64_tr_b16 a[238:239],v3 offset:12800
v_mfma_f32_16x16x32_bf16 v[204:207],a[156:159],v[240:243],v[40:43]
ds_read_b64_tr_b16 a[232:233],v3 offset:512
v_mfma_f32_16x16x32_bf16 v[204:207],a[152:155],v[208:211],v[204:207]
ds_read_b64_tr_b16 a[234:235],v3 offset:4608
v_mfma_f32_16x16x32_bf16 v[204:207],a[148:151],v[176:179],v[204:207]
ds_read_b64_tr_b16 a[240:241],v2 offset:768
v_mfma_f32_16x16x32_bf16 v[204:207],a[144:147],v[144:147],v[204:207]
s_addk_i32 m0,4096
ds_read_b64_tr_b16 a[242:243],v2 offset:4864
ds_read_b64_tr_b16 a[244:245],v2 offset:8960
v_mfma_f32_16x16x32_bf16 v[216:219],a[160:163],v[144:147],v[40:43]
ds_read_b64_tr_b16 a[246:247],v2 offset:13056
v_add_u32 v2,s48,v2
v_mfma_f32_16x16x32_bf16 v[216:219],a[164:167],v[176:179],v[216:219]
ds_read_b64_tr_b16 a[252:253],v3 offset:8960
v_mfma_f32_16x16x32_bf16 v[216:219],a[168:171],v[208:211],v[216:219]
buffer_load_dwordx4 v11,s[72:75],s83 offen lds
v_mfma_f32_16x16x32_bf16 v[216:219],a[172:175],v[240:243],v[216:219]
ds_read_b64_tr_b16 a[254:255],v3 offset:13056
ds_read_b64_tr_b16 a[248:249],v3 offset:768
v_mfma_f32_16x16x32_bf16 v[220:223],a[188:191],v[240:243],v[40:43]
ds_read_b64_tr_b16 a[250:251],v3 offset:4864
v_add_u32 v3,s48,v3
v_mfma_f32_16x16x32_bf16 v[220:223],a[184:187],v[208:211],v[220:223]
v_exp_f32 v184,v184
v_mfma_f32_16x16x32_bf16 v[220:223],a[180:183],v[176:179],v[220:223]
v_exp_f32 v185,v185
v_mfma_f32_16x16x32_bf16 v[220:223],a[176:179],v[144:147],v[220:223]
s_waitcnt lgkmcnt(5)
v_exp_f32 v186,v186
v_mfma_f32_16x16x32_bf16 a[0:3],a[192:195],v[136:139],a[0:3]
v_exp_f32 v187,v187
v_mfma_f32_16x16x32_bf16 a[0:3],a[196:199],v[140:143],a[0:3]
s_add_u32 s80,s71,0
s_and_b32 s80,s80,3
s_lshl_b32 s80,s80,1
v_mfma_f32_16x16x32_bf16 a[4:7],a[204:207],v[140:143],a[4:7]
s_or_b32 s80,s80,1
buffer_store_dwordx4 v[116:119],v6,s[36:39],s87 offen offset:1024 sc1
v_mfma_f32_16x16x32_bf16 a[4:7],a[200:203],v[136:139],a[4:7]
s_mul_i32 s80,s80,163840
s_add_u32 s80,s80,s81
v_cvt_pk_bf16_f32 v172,v184,v185
v_mfma_f32_16x16x32_bf16 a[8:11],a[208:211],v[136:139],a[8:11]
s_add_u32 s83,s80,20480
v_exp_f32 v188,v188
v_mfma_f32_16x16x32_bf16 a[8:11],a[212:215],v[140:143],a[8:11]
v_exp_f32 v189,v189
v_mfma_f32_16x16x32_bf16 a[12:15],a[220:223],v[140:143],a[12:15]
v_exp_f32 v190,v190
v_mfma_f32_16x16x32_bf16 a[12:15],a[216:219],v[136:139],a[12:15]
v_exp_f32 v191,v191
v_mfma_f32_16x16x32_bf16 a[16:19],a[224:227],v[136:139],a[16:19]
v_cvt_pk_bf16_f32 v173,v186,v187
v_cvt_pk_bf16_f32 v174,v188,v189
v_mfma_f32_16x16x32_bf16 a[16:19],a[228:231],v[140:143],a[16:19]
v_exp_f32 v200,v200
v_mfma_f32_16x16x32_bf16 a[20:23],a[236:239],v[140:143],a[20:23]
v_exp_f32 v201,v201
v_mfma_f32_16x16x32_bf16 a[20:23],a[232:235],v[136:139],a[20:23]
v_cvt_pk_bf16_f32 v175,v190,v191
v_cvt_pk_bf16_f32 v200,v200,v201
v_mfma_f32_16x16x32_bf16 a[24:27],a[240:243],v[136:139],a[24:27]
s_waitcnt lgkmcnt(0)
v_exp_f32 v202,v202
v_mfma_f32_16x16x32_bf16 a[24:27],a[244:247],v[140:143],a[24:27]
v_exp_f32 v203,v203
v_mfma_f32_16x16x32_bf16 a[28:31],a[252:255],v[140:143],a[28:31]
v_exp_f32 v204,v204
v_mfma_f32_16x16x32_bf16 a[28:31],a[248:251],v[136:139],a[28:31]
v_exp_f32 v205,v205
v_smfmac_f32_16x16x64_bf16 v[48:51],v[28:31],v[136:143],v27
v_cvt_pk_bf16_f32 v201,v202,v203
v_mfma_f32_16x16x32_bf16 v[232:235],a[128:131],v[148:151],v[44:47]
v_exp_f32 v206,v206
v_mfma_f32_16x16x32_bf16 v[232:235],a[132:135],v[180:183],v[232:235]
s_barrier
buffer_store_dwordx4 v[120:123],v6,s[36:39],s87 offen offset:2048 sc1
v_mfma_f32_16x16x32_bf16 v[232:235],a[136:139],v[212:215],v[232:235]
ds_read_b128 a[128:131],v1
v_cvt_pk_bf16_f32 v202,v204,v205
v_mfma_f32_16x16x32_bf16 v[232:235],a[140:143],v[244:247],v[232:235]
v_exp_f32 v207,v207
v_mfma_f32_16x16x32_bf16 v[236:239],a[156:159],v[244:247],v[44:47]
s_add_u32 m0,s82,0
ds_read_b128 a[132:135],v1 offset:256
v_cvt_pk_bf16_f32 v203,v206,v207
v_mfma_f32_16x16x32_bf16 v[236:239],a[152:155],v[212:215],v[236:239]
v_exp_f32 v216,v216
v_mfma_f32_16x16x32_bf16 v[236:239],a[148:151],v[180:183],v[236:239]
v_exp_f32 v217,v217
v_mfma_f32_16x16x32_bf16 v[236:239],a[144:147],v[148:151],v[236:239]
buffer_load_dwordx4 v7,s[72:75],s80 offen lds
v_mfma_f32_16x16x32_bf16 v[248:251],a[160:163],v[148:151],v[44:47]
ds_read_b128 a[136:139],v1 offset:512
v_cvt_pk_bf16_f32 v204,v216,v217
v_mfma_f32_16x16x32_bf16 v[248:251],a[164:167],v[180:183],v[248:251]
v_exp_f32 v218,v218
v_mfma_f32_16x16x32_bf16 v[248:251],a[168:171],v[212:215],v[248:251]
ds_read_b128 a[140:143],v1 offset:768
v_mfma_f32_16x16x32_bf16 v[248:251],a[172:175],v[244:247],v[248:251]
v_exp_f32 v219,v219
v_mfma_f32_16x16x32_bf16 v[252:255],a[188:191],v[244:247],v[44:47]
ds_read_b128 a[156:159],v1 offset:4864
v_cvt_pk_bf16_f32 v205,v218,v219
v_mfma_f32_16x16x32_bf16 v[252:255],a[184:187],v[212:215],v[252:255]
v_exp_f32 v220,v220
v_mfma_f32_16x16x32_bf16 v[252:255],a[180:183],v[180:183],v[252:255]
ds_read_b128 a[152:155],v1 offset:4608
v_mfma_f32_16x16x32_bf16 v[252:255],a[176:179],v[148:151],v[252:255]
v_exp_f32 v221,v221
v_mfma_f32_16x16x32_bf16 a[32:35],a[192:195],v[168:171],a[32:35]
buffer_store_dwordx4 v[124:127],v6,s[36:39],s87 offen offset:3072 sc1
v_mfma_f32_16x16x32_bf16 a[32:35],a[196:199],v[172:175],a[32:35]
s_add_u32 s87,s55,151552
ds_read_b128 a[148:151],v1 offset:4352
v_cvt_pk_bf16_f32 v206,v220,v221
v_mfma_f32_16x16x32_bf16 a[36:39],a[204:207],v[172:175],a[36:39]
s_max_i32 s87,s87,s101
v_exp_f32 v222,v222
v_mfma_f32_16x16x32_bf16 a[36:39],a[200:203],v[168:171],a[36:39]
ds_read_b128 a[144:147],v1 offset:4096
v_mfma_f32_16x16x32_bf16 a[40:43],a[208:211],v[168:171],a[40:43]
buffer_load_dwordx4 v8,s[72:75],s80 offen offset:1024 lds
v_mfma_f32_16x16x32_bf16 a[40:43],a[212:215],v[172:175],a[40:43]
ds_read_b128 a[160:163],v1 offset:8192
v_mfma_f32_16x16x32_bf16 a[44:47],a[220:223],v[172:175],a[44:47]
v_exp_f32 v223,v223
v_mfma_f32_16x16x32_bf16 a[44:47],a[216:219],v[168:171],a[44:47]
ds_read_b128 a[164:167],v1 offset:8448
v_cvt_pk_bf16_f32 v207,v222,v223
v_mfma_f32_16x16x32_bf16 a[48:51],a[224:227],v[168:171],a[48:51]
v_exp_f32 v232,v232
v_mfma_f32_16x16x32_bf16 a[48:51],a[228:231],v[172:175],a[48:51]
ds_read_b128 a[168:171],v1 offset:8704
v_mfma_f32_16x16x32_bf16 a[52:55],a[236:239],v[172:175],a[52:55]
v_exp_f32 v233,v233
v_mfma_f32_16x16x32_bf16 a[52:55],a[232:235],v[168:171],a[52:55]
ds_read_b128 a[172:175],v1 offset:8960
v_cvt_pk_bf16_f32 v232,v232,v233
v_mfma_f32_16x16x32_bf16 a[56:59],a[240:243],v[168:171],a[56:59]
v_exp_f32 v234,v234
v_mfma_f32_16x16x32_bf16 a[56:59],a[244:247],v[172:175],a[56:59]
buffer_load_dwordx4 v9,s[72:75],s80 offen offset:2048 lds
v_mfma_f32_16x16x32_bf16 a[60:63],a[252:255],v[172:175],a[60:63]
ds_read_b128 a[188:191],v1 offset:13056
v_mfma_f32_16x16x32_bf16 a[60:63],a[248:251],v[168:171],a[60:63]
v_exp_f32 v235,v235
v_smfmac_f32_16x16x64_bf16 v[52:55],v[28:31],v[168:175],v27
s_waitcnt lgkmcnt(3)
ds_read_b128 a[184:187],v1 offset:12800
v_mfma_f32_16x16x32_bf16 v[136:139],a[128:131],v[128:131],v[32:35]
v_exp_f32 v236,v236
v_mfma_f32_16x16x32_bf16 v[136:139],a[132:135],v[160:163],v[136:139]
ds_read_b128 a[180:183],v1 offset:12544
v_cvt_pk_bf16_f32 v233,v234,v235
v_mfma_f32_16x16x32_bf16 v[136:139],a[136:139],v[192:195],v[136:139]
v_exp_f32 v237,v237
v_mfma_f32_16x16x32_bf16 v[136:139],a[140:143],v[224:227],v[136:139]
ds_read_b128 a[176:179],v1 offset:12288
v_cvt_pk_bf16_f32 v234,v236,v237
v_mfma_f32_16x16x32_bf16 v[140:143],a[156:159],v[224:227],v[32:35]
v_exp_f32 v238,v238
v_mfma_f32_16x16x32_bf16 v[140:143],a[152:155],v[192:195],v[140:143]
v_exp_f32 v239,v239
v_mfma_f32_16x16x32_bf16 v[140:143],a[148:151],v[160:163],v[140:143]
v_exp_f32 v248,v248
v_mfma_f32_16x16x32_bf16 v[140:143],a[144:147],v[128:131],v[140:143]
)A"
R"A(buffer_load_dwordx4 v10,s[72:75],s80 offen offset:3072 lds
v_mfma_f32_16x16x32_bf16 v[152:155],a[160:163],v[128:131],v[32:35]
v_exp_f32 v249,v249
v_mfma_f32_16x16x32_bf16 v[152:155],a[164:167],v[160:163],v[152:155]
s_waitcnt lgkmcnt(1)
v_cvt_pk_bf16_f32 v235,v238,v239
v_cvt_pk_bf16_f32 v236,v248,v249
v_mfma_f32_16x16x32_bf16 v[152:155],a[168:171],v[192:195],v[152:155]
v_exp_f32 v250,v250
v_mfma_f32_16x16x32_bf16 v[152:155],a[172:175],v[224:227],v[152:155]
v_exp_f32 v251,v251
v_mfma_f32_16x16x32_bf16 v[156:159],a[188:191],v[224:227],v[32:35]
v_exp_f32 v252,v252
v_mfma_f32_16x16x32_bf16 v[156:159],a[184:187],v[192:195],v[156:159]
v_exp_f32 v253,v253
v_mfma_f32_16x16x32_bf16 v[156:159],a[180:183],v[160:163],v[156:159]
s_waitcnt lgkmcnt(0)
v_exp_f32 v254,v254
v_mfma_f32_16x16x32_bf16 v[156:159],a[176:179],v[128:131],v[156:159]
s_addk_i32 m0,4096
v_exp_f32 v255,v255
v_mfma_f32_16x16x32_bf16 a[64:67],a[192:195],v[200:203],a[64:67]
v_cvt_pk_bf16_f32 v237,v250,v251
v_cvt_pk_bf16_f32 v238,v252,v253
v_mfma_f32_16x16x32_bf16 a[64:67],a[196:199],v[204:207],a[64:67]
v_cvt_pk_bf16_f32 v239,v254,v255
v_bfe_u32 v32,v6,4,2
v_mfma_f32_16x16x32_bf16 a[68:71],a[204:207],v[204:207],a[68:71]
buffer_load_dwordx4 v11,s[72:75],s80 offen lds
v_mfma_f32_16x16x32_bf16 a[68:71],a[200:203],v[200:203],a[68:71]
v_exp_f32 v136,v136
v_mfma_f32_16x16x32_bf16 a[72:75],a[208:211],v[200:203],a[72:75]
v_exp_f32 v137,v137
v_mfma_f32_16x16x32_bf16 a[72:75],a[212:215],v[204:207],a[72:75]
v_exp_f32 v138,v138
v_mfma_f32_16x16x32_bf16 a[76:79],a[220:223],v[204:207],a[76:79]
v_exp_f32 v139,v139
v_mfma_f32_16x16x32_bf16 a[76:79],a[216:219],v[200:203],a[76:79]
v_cvt_pk_bf16_f32 v136,v136,v137
v_cvt_pk_bf16_f32 v137,v138,v139
v_mfma_f32_16x16x32_bf16 a[80:83],a[224:227],v[200:203],a[80:83]
v_exp_f32 v140,v140
v_mfma_f32_16x16x32_bf16 a[80:83],a[228:231],v[204:207],a[80:83]
v_exp_f32 v141,v141
v_mfma_f32_16x16x32_bf16 a[84:87],a[236:239],v[204:207],a[84:87]
s_addk_i32 m0,1024
v_exp_f32 v142,v142
v_mfma_f32_16x16x32_bf16 a[84:87],a[232:235],v[200:203],a[84:87]
v_exp_f32 v143,v143
v_mfma_f32_16x16x32_bf16 a[88:91],a[240:243],v[200:203],a[88:91]
v_cvt_pk_bf16_f32 v138,v140,v141
v_cvt_pk_bf16_f32 v139,v142,v143
v_mfma_f32_16x16x32_bf16 a[88:91],a[244:247],v[204:207],a[88:91]
buffer_load_dwordx4 v7,s[72:75],s83 offen lds
v_mfma_f32_16x16x32_bf16 a[92:95],a[252:255],v[204:207],a[92:95]
v_exp_f32 v152,v152
v_mfma_f32_16x16x32_bf16 a[92:95],a[248:251],v[200:203],a[92:95]
v_exp_f32 v153,v153
v_smfmac_f32_16x16x64_bf16 v[56:59],v[28:31],v[200:207],v27
v_cvt_pk_bf16_f32 v140,v152,v153
v_mfma_f32_16x16x32_bf16 v[168:171],a[128:131],v[132:135],v[36:39]
s_lshl_b32 s95,s42,2
s_add_u32 s95,s95,s40
v_bfe_u32 v19,s55,13,1
v_mfma_f32_16x16x32_bf16 v[168:171],a[132:135],v[164:167],v[168:171]
s_lshl_b32 s95,s95,4
v_mov_b32 v18,s68
v_or_b32 v19,s69,v19
v_mfma_f32_16x16x32_bf16 v[168:171],a[136:139],v[196:199],v[168:171]
v_exp_f32 v154,v154
v_mfma_f32_16x16x32_bf16 v[168:171],a[140:143],v[228:231],v[168:171]
v_exp_f32 v155,v155
v_mfma_f32_16x16x32_bf16 v[172:175],a[156:159],v[228:231],v[36:39]
v_exp_f32 v156,v156
v_mfma_f32_16x16x32_bf16 v[172:175],a[152:155],v[196:199],v[172:175]
v_exp_f32 v157,v157
v_mfma_f32_16x16x32_bf16 v[172:175],a[148:151],v[164:167],v[172:175]
v_exp_f32 v158,v158
v_mfma_f32_16x16x32_bf16 v[172:175],a[144:147],v[132:135],v[172:175]
buffer_load_dwordx4 v8,s[72:75],s83 offen offset:1024 lds
v_mfma_f32_16x16x32_bf16 v[184:187],a[160:163],v[132:135],v[36:39]
v_exp_f32 v159,v159
v_mfma_f32_16x16x32_bf16 v[184:187],a[164:167],v[164:167],v[184:187]
v_cvt_pk_bf16_f32 v141,v154,v155
v_cvt_pk_bf16_f32 v142,v156,v157
v_mfma_f32_16x16x32_bf16 v[184:187],a[168:171],v[196:199],v[184:187]
v_cvt_pk_bf16_f32 v143,v158,v159
v_lshl_add_u32 v25,v32,3,v25
v_mfma_f32_16x16x32_bf16 v[184:187],a[172:175],v[228:231],v[184:187]
s_waitcnt vmcnt(6)
s_mov_b64 exec,1
buffer_store_dwordx2 v[18:19],v6,s[64:67],s95 offen sc1
s_mov_b64 exec,-1
v_mfma_f32_16x16x32_bf16 v[188:191],a[188:191],v[228:231],v[36:39]
v_exp_f32 v168,v168
v_mfma_f32_16x16x32_bf16 v[188:191],a[184:187],v[196:199],v[188:191]
v_exp_f32 v169,v169
v_mfma_f32_16x16x32_bf16 v[188:191],a[180:183],v[164:167],v[188:191]
v_exp_f32 v170,v170
v_mfma_f32_16x16x32_bf16 v[188:191],a[176:179],v[132:135],v[188:191]
v_exp_f32 v171,v171
v_mfma_f32_16x16x32_bf16 a[96:99],a[192:195],v[232:235],a[96:99]
ds_read_b64_tr_b16 a[192:193],v2
ds_read_b64_tr_b16 a[194:195],v2 offset:4096
v_mfma_f32_16x16x32_bf16 a[96:99],a[196:199],v[236:239],a[96:99]
ds_read_b64_tr_b16 a[196:197],v2 offset:8192
v_cvt_pk_bf16_f32 v168,v168,v169
v_mfma_f32_16x16x32_bf16 a[100:103],a[204:207],v[236:239],a[100:103]
buffer_load_dwordx4 v9,s[72:75],s83 offen offset:2048 lds
v_mfma_f32_16x16x32_bf16 a[100:103],a[200:203],v[232:235],a[100:103]
ds_read_b64_tr_b16 a[198:199],v2 offset:12288
ds_read_b64_tr_b16 a[204:205],v3 offset:8192
v_mfma_f32_16x16x32_bf16 a[104:107],a[208:211],v[232:235],a[104:107]
ds_read_b64_tr_b16 a[206:207],v3 offset:12288
v_cvt_pk_bf16_f32 v169,v170,v171
v_mfma_f32_16x16x32_bf16 a[104:107],a[212:215],v[236:239],a[104:107]
ds_read_b64_tr_b16 a[200:201],v3
v_bfe_u32 v32,v6,8,1
v_mfma_f32_16x16x32_bf16 a[108:111],a[220:223],v[236:239],a[108:111]
ds_read_b64_tr_b16 a[202:203],v3 offset:4096
v_lshlrev_b32 v32,5,v32
v_mfma_f32_16x16x32_bf16 a[108:111],a[216:219],v[232:235],a[108:111]
ds_read_b64_tr_b16 a[208:209],v2 offset:256
ds_read_b64_tr_b16 a[210:211],v2 offset:4352
v_mfma_f32_16x16x32_bf16 a[112:115],a[224:227],v[232:235],a[112:115]
ds_read_b64_tr_b16 a[212:213],v2 offset:8448
v_add_u32 v1,v25,v32
v_mfma_f32_16x16x32_bf16 a[112:115],a[228:231],v[236:239],a[112:115]
ds_read_b64_tr_b16 a[214:215],v2 offset:12544
v_add_u32 v1,81920,v1
v_mfma_f32_16x16x32_bf16 a[116:119],a[236:239],v[236:239],a[116:119]
v_exp_f32 v172,v172
v_mfma_f32_16x16x32_bf16 a[116:119],a[232:235],v[232:235],a[116:119]
ds_read_b64_tr_b16 a[220:221],v3 offset:8448
ds_read_b64_tr_b16 a[222:223],v3 offset:12544
v_mfma_f32_16x16x32_bf16 a[120:123],a[240:243],v[232:235],a[120:123]
s_cmp_ge_u32 0,s71
s_cselect_b32 s80,2,0
ds_read_b64_tr_b16 a[216:217],v3 offset:256
v_mfma_f32_16x16x32_bf16 a[120:123],a[244:247],v[236:239],a[120:123]
s_mul_i32 s80,s80,163840
buffer_load_dwordx4 v10,s[72:75],s83 offen offset:3072 lds
v_mfma_f32_16x16x32_bf16 a[124:127],a[252:255],v[236:239],a[124:127]
s_add_u32 s80,s80,s81
ds_read_b64_tr_b16 a[218:219],v3 offset:4352
ds_read_b64_tr_b16 a[224:225],v2 offset:512
v_mfma_f32_16x16x32_bf16 a[124:127],a[248:251],v[232:235],a[124:127]
ds_read_b64_tr_b16 a[226:227],v2 offset:4608
v_smfmac_f32_16x16x64_bf16 v[60:63],v[28:31],v[232:239],v27
ds_read_b64_tr_b16 a[228:229],v2 offset:8704
v_mfma_f32_16x16x32_bf16 v[200:203],a[128:131],v[144:147],v[40:43]
ds_read_b64_tr_b16 a[230:231],v2 offset:12800
v_mfma_f32_16x16x32_bf16 v[200:203],a[132:135],v[176:179],v[200:203]
ds_read_b64_tr_b16 a[236:237],v3 offset:8704
ds_read_b64_tr_b16 a[238:239],v3 offset:12800
v_mfma_f32_16x16x32_bf16 v[200:203],a[136:139],v[208:211],v[200:203]
ds_read_b64_tr_b16 a[232:233],v3 offset:512
v_mfma_f32_16x16x32_bf16 v[200:203],a[140:143],v[240:243],v[200:203]
ds_read_b64_tr_b16 a[234:235],v3 offset:4608
v_mfma_f32_16x16x32_bf16 v[204:207],a[156:159],v[240:243],v[40:43]
s_addk_i32 m0,4096
ds_read_b64_tr_b16 a[240:241],v2 offset:768
v_mfma_f32_16x16x32_bf16 v[204:207],a[152:155],v[208:211],v[204:207]
ds_read_b64_tr_b16 a[242:243],v2 offset:4864
ds_read_b64_tr_b16 a[244:245],v2 offset:8960
v_mfma_f32_16x16x32_bf16 v[204:207],a[148:151],v[176:179],v[204:207]
ds_read_b64_tr_b16 a[246:247],v2 offset:13056
v_sub_u32 v2,v25,v32
v_mfma_f32_16x16x32_bf16 v[204:207],a[144:147],v[144:147],v[204:207]
buffer_load_dwordx4 v11,s[72:75],s83 offen lds
v_mfma_f32_16x16x32_bf16 v[216:219],a[160:163],v[144:147],v[40:43]
s_add_u32 s83,s80,20480
ds_read_b64_tr_b16 a[252:253],v3 offset:8960
ds_read_b64_tr_b16 a[254:255],v3 offset:13056
v_mfma_f32_16x16x32_bf16 v[216:219],a[164:167],v[176:179],v[216:219]
ds_read_b64_tr_b16 a[248:249],v3 offset:768
v_add_u32 v2,81920,v2
v_mfma_f32_16x16x32_bf16 v[216:219],a[168:171],v[208:211],v[216:219]
ds_read_b64_tr_b16 a[250:251],v3 offset:4864
v_mfma_f32_16x16x32_bf16 v[216:219],a[172:175],v[240:243],v[216:219]
v_exp_f32 v173,v173
v_mfma_f32_16x16x32_bf16 v[220:223],a[188:191],v[240:243],v[40:43]
v_exp_f32 v174,v174
v_mfma_f32_16x16x32_bf16 v[220:223],a[184:187],v[208:211],v[220:223]
v_exp_f32 v175,v175
v_mfma_f32_16x16x32_bf16 v[220:223],a[180:183],v[176:179],v[220:223]
v_cvt_pk_bf16_f32 v170,v172,v173
v_cvt_pk_bf16_f32 v171,v174,v175
v_mfma_f32_16x16x32_bf16 v[220:223],a[176:179],v[144:147],v[220:223]
s_add_u32 m0,s82,40960
v_exp_f32 v184,v184
s_waitcnt lgkmcnt(4)
v_mfma_f32_16x16x32_bf16 a[0:3],a[192:195],v[136:139],a[0:3]
v_exp_f32 v185,v185
v_mfma_f32_16x16x32_bf16 a[0:3],a[196:199],v[140:143],a[0:3]
v_exp_f32 v186,v186
v_mfma_f32_16x16x32_bf16 a[4:7],a[204:207],v[140:143],a[4:7]
buffer_load_dwordx4 v7,s[72:75],s80 offen lds
v_mfma_f32_16x16x32_bf16 a[4:7],a[200:203],v[136:139],a[4:7]
v_exp_f32 v187,v187
v_mfma_f32_16x16x32_bf16 a[8:11],a[208:211],v[136:139],a[8:11]
v_exp_f32 v188,v188
v_mfma_f32_16x16x32_bf16 a[8:11],a[212:215],v[140:143],a[8:11]
v_exp_f32 v189,v189
v_mfma_f32_16x16x32_bf16 a[12:15],a[220:223],v[140:143],a[12:15]
v_exp_f32 v190,v190
v_mfma_f32_16x16x32_bf16 a[12:15],a[216:219],v[136:139],a[12:15]
v_exp_f32 v191,v191
v_mfma_f32_16x16x32_bf16 a[16:19],a[224:227],v[136:139],a[16:19]
v_cvt_pk_bf16_f32 v172,v184,v185
v_cvt_pk_bf16_f32 v173,v186,v187
v_mfma_f32_16x16x32_bf16 a[16:19],a[228:231],v[140:143],a[16:19]
v_cvt_pk_bf16_f32 v174,v188,v189
v_cvt_pk_bf16_f32 v175,v190,v191
v_mfma_f32_16x16x32_bf16 a[20:23],a[236:239],v[140:143],a[20:23]
v_exp_f32 v200,v200
v_mfma_f32_16x16x32_bf16 a[20:23],a[232:235],v[136:139],a[20:23]
v_exp_f32 v201,v201
v_mfma_f32_16x16x32_bf16 a[24:27],a[240:243],v[136:139],a[24:27]
v_exp_f32 v202,v202
v_mfma_f32_16x16x32_bf16 a[24:27],a[244:247],v[140:143],a[24:27]
s_waitcnt lgkmcnt(0)
buffer_load_dwordx4 v8,s[72:75],s80 offen offset:1024 lds
v_mfma_f32_16x16x32_bf16 a[28:31],a[252:255],v[140:143],a[28:31]
v_exp_f32 v203,v203
v_mfma_f32_16x16x32_bf16 a[28:31],a[248:251],v[136:139],a[28:31]
v_cvt_pk_bf16_f32 v200,v200,v201
v_cvt_pk_bf16_f32 v201,v202,v203
v_smfmac_f32_16x16x64_bf16 v[48:51],v[28:31],v[136:143],v27
s_barrier
v_exp_f32 v204,v204
v_mfma_f32_16x16x32_bf16 v[232:235],a[128:131],v[148:151],v[44:47]
v_exp_f32 v205,v205
v_mfma_f32_16x16x32_bf16 v[232:235],a[132:135],v[180:183],v[232:235]
v_exp_f32 v206,v206
v_mfma_f32_16x16x32_bf16 v[232:235],a[136:139],v[212:215],v[232:235]
v_exp_f32 v207,v207
v_mfma_f32_16x16x32_bf16 v[232:235],a[140:143],v[244:247],v[232:235]
v_cvt_pk_bf16_f32 v202,v204,v205
v_cvt_pk_bf16_f32 v203,v206,v207
v_mfma_f32_16x16x32_bf16 v[236:239],a[156:159],v[244:247],v[44:47]
v_exp_f32 v216,v216
v_mfma_f32_16x16x32_bf16 v[236:239],a[152:155],v[212:215],v[236:239]
v_exp_f32 v217,v217
v_mfma_f32_16x16x32_bf16 v[236:239],a[148:151],v[180:183],v[236:239]
buffer_load_dwordx4 v9,s[72:75],s80 offen offset:2048 lds
v_mfma_f32_16x16x32_bf16 v[236:239],a[144:147],v[148:151],v[236:239]
v_exp_f32 v218,v218
v_mfma_f32_16x16x32_bf16 v[248:251],a[160:163],v[148:151],v[44:47]
v_exp_f32 v219,v219
v_mfma_f32_16x16x32_bf16 v[248:251],a[164:167],v[180:183],v[248:251]
v_exp_f32 v220,v220
v_mfma_f32_16x16x32_bf16 v[248:251],a[168:171],v[212:215],v[248:251]
v_exp_f32 v221,v221
v_mfma_f32_16x16x32_bf16 v[248:251],a[172:175],v[244:247],v[248:251]
v_exp_f32 v222,v222
v_mfma_f32_16x16x32_bf16 v[252:255],a[188:191],v[244:247],v[44:47]
v_exp_f32 v223,v223
v_mfma_f32_16x16x32_bf16 v[252:255],a[184:187],v[212:215],v[252:255]
v_cvt_pk_bf16_f32 v204,v216,v217
v_cvt_pk_bf16_f32 v205,v218,v219
v_mfma_f32_16x16x32_bf16 v[252:255],a[180:183],v[180:183],v[252:255]
v_cvt_pk_bf16_f32 v206,v220,v221
v_cvt_pk_bf16_f32 v207,v222,v223
v_mfma_f32_16x16x32_bf16 v[252:255],a[176:179],v[148:151],v[252:255]
v_exp_f32 v232,v232
v_mfma_f32_16x16x32_bf16 a[32:35],a[192:195],v[168:171],a[32:35]
v_exp_f32 v233,v233
v_mfma_f32_16x16x32_bf16 a[32:35],a[196:199],v[172:175],a[32:35]
)A"
R"A(buffer_load_dwordx4 v10,s[72:75],s80 offen offset:3072 lds
v_mfma_f32_16x16x32_bf16 a[36:39],a[204:207],v[172:175],a[36:39]
v_add_f32 v48,v49,v48
v_max_f32 v20,0x00800000,v48
v_mfma_f32_16x16x32_bf16 a[36:39],a[200:203],v[168:171],a[36:39]
v_exp_f32 v234,v234
v_mfma_f32_16x16x32_bf16 a[40:43],a[208:211],v[168:171],a[40:43]
v_exp_f32 v235,v235
v_mfma_f32_16x16x32_bf16 a[40:43],a[212:215],v[172:175],a[40:43]
v_cvt_pk_bf16_f32 v232,v232,v233
v_cvt_pk_bf16_f32 v233,v234,v235
v_mfma_f32_16x16x32_bf16 a[44:47],a[220:223],v[172:175],a[44:47]
v_exp_f32 v236,v236
v_mfma_f32_16x16x32_bf16 a[44:47],a[216:219],v[168:171],a[44:47]
v_exp_f32 v237,v237
v_mfma_f32_16x16x32_bf16 a[48:51],a[224:227],v[168:171],a[48:51]
v_exp_f32 v238,v238
v_mfma_f32_16x16x32_bf16 a[48:51],a[228:231],v[172:175],a[48:51]
s_addk_i32 m0,4096
v_exp_f32 v239,v239
v_mfma_f32_16x16x32_bf16 a[52:55],a[236:239],v[172:175],a[52:55]
v_rcp_f32 v20,v20
v_mfma_f32_16x16x32_bf16 a[52:55],a[232:235],v[168:171],a[52:55]
v_cvt_pk_bf16_f32 v234,v236,v237
v_cvt_pk_bf16_f32 v235,v238,v239
v_mfma_f32_16x16x32_bf16 a[56:59],a[240:243],v[168:171],a[56:59]
buffer_load_dwordx4 v11,s[72:75],s80 offen lds
v_mfma_f32_16x16x32_bf16 a[56:59],a[244:247],v[172:175],a[56:59]
v_exp_f32 v248,v248
v_mfma_f32_16x16x32_bf16 a[60:63],a[252:255],v[172:175],a[60:63]
v_exp_f32 v249,v249
v_mfma_f32_16x16x32_bf16 a[60:63],a[248:251],v[168:171],a[60:63]
v_exp_f32 v250,v250
v_smfmac_f32_16x16x64_bf16 v[52:55],v[28:31],v[168:175],v27
v_mul_f32 v21,0xbe22f983,v20
v_mfma_f32_16x16x32_bf16 a[64:67],a[192:195],v[200:203],a[64:67]
v_exp_f32 v251,v251
v_mfma_f32_16x16x32_bf16 a[64:67],a[196:199],v[204:207],a[64:67]
v_exp_f32 v252,v252
v_mfma_f32_16x16x32_bf16 a[68:71],a[204:207],v[204:207],a[68:71]
v_exp_f32 v253,v253
v_mfma_f32_16x16x32_bf16 a[68:71],a[200:203],v[200:203],a[68:71]
s_addk_i32 m0,1024
v_exp_f32 v254,v254
v_mfma_f32_16x16x32_bf16 a[72:75],a[208:211],v[200:203],a[72:75]
v_exp_f32 v255,v255
v_mfma_f32_16x16x32_bf16 a[72:75],a[212:215],v[204:207],a[72:75]
v_cvt_pk_bf16_f32 v236,v248,v249
v_cvt_pk_bf16_f32 v237,v250,v251
v_mfma_f32_16x16x32_bf16 a[76:79],a[220:223],v[204:207],a[76:79]
buffer_load_dwordx4 v7,s[72:75],s83 offen lds
v_mfma_f32_16x16x32_bf16 a[76:79],a[216:219],v[200:203],a[76:79]
v_cvt_pk_bf16_f32 v238,v252,v253
v_cvt_pk_bf16_f32 v239,v254,v255
v_mfma_f32_16x16x32_bf16 a[80:83],a[224:227],v[200:203],a[80:83]
.p2align 3
v_accvgpr_read_b32 v136,a0
v_accvgpr_read_b32 v137,a1
v_accvgpr_read_b32 v138,a2
v_accvgpr_read_b32 v139,a3
v_accvgpr_read_b32 v12,a4
v_accvgpr_read_b32 v13,a5
v_accvgpr_read_b32 v14,a6
v_accvgpr_read_b32 v15,a7
v_pk_fma_f32 v[136:137],v[20:21],v[136:137],v[20:21] op_sel:[0,0,1] op_sel_hi:[0,1,1]
v_pk_fma_f32 v[138:139],v[20:21],v[138:139],v[20:21] op_sel:[0,0,1] op_sel_hi:[0,1,1]
v_pk_fma_f32 v[12:13],v[20:21],v[12:13],v[20:21] op_sel:[0,0,1] op_sel_hi:[0,1,1]
v_pk_fma_f32 v[14:15],v[20:21],v[14:15],v[20:21] op_sel:[0,0,1] op_sel_hi:[0,1,1]
v_cvt_pk_bf16_f32 v136,v136,v137
v_cvt_pk_bf16_f32 v137,v138,v139
v_cvt_pk_bf16_f32 v138,v12,v13
v_cvt_pk_bf16_f32 v139,v14,v15
v_mfma_f32_16x16x32_bf16 a[80:83],a[228:231],v[204:207],a[80:83]
ds_read_b64_tr_b16 a[128:129],v1
ds_read_b64_tr_b16 a[130:131],v1 offset:5120
v_mfma_f32_16x16x32_bf16 a[84:87],a[236:239],v[204:207],a[84:87]
ds_read_b64_tr_b16 a[132:133],v1 offset:10240
v_accvgpr_read_b32 v152,a16
v_mfma_f32_16x16x32_bf16 a[84:87],a[232:235],v[200:203],a[84:87]
ds_read_b64_tr_b16 a[134:135],v1 offset:15360
v_accvgpr_read_b32 v153,a17
v_mfma_f32_16x16x32_bf16 a[88:91],a[240:243],v[200:203],a[88:91]
ds_read_b64_tr_b16 a[136:137],v1 offset:20480
v_fma_f32 v152,v20,v152,v21
v_mfma_f32_16x16x32_bf16 a[88:91],a[244:247],v[204:207],a[88:91]
ds_read_b64_tr_b16 a[138:139],v1 offset:25600
ds_read_b64_tr_b16 a[140:141],v1 offset:30720
v_mfma_f32_16x16x32_bf16 a[92:95],a[252:255],v[204:207],a[92:95]
.p2align 3
v_accvgpr_read_b32 v140,a8
v_accvgpr_read_b32 v141,a9
v_accvgpr_read_b32 v142,a10
v_accvgpr_read_b32 v143,a11
v_accvgpr_read_b32 v12,a12
v_accvgpr_read_b32 v13,a13
v_accvgpr_read_b32 v14,a14
v_accvgpr_read_b32 v15,a15
v_pk_fma_f32 v[140:141],v[20:21],v[140:141],v[20:21] op_sel:[0,0,1] op_sel_hi:[0,1,1]
v_pk_fma_f32 v[142:143],v[20:21],v[142:143],v[20:21] op_sel:[0,0,1] op_sel_hi:[0,1,1]
v_pk_fma_f32 v[12:13],v[20:21],v[12:13],v[20:21] op_sel:[0,0,1] op_sel_hi:[0,1,1]
v_pk_fma_f32 v[14:15],v[20:21],v[14:15],v[20:21] op_sel:[0,0,1] op_sel_hi:[0,1,1]
v_cvt_pk_bf16_f32 v140,v140,v141
v_cvt_pk_bf16_f32 v141,v142,v143
v_cvt_pk_bf16_f32 v142,v12,v13
v_cvt_pk_bf16_f32 v143,v14,v15
v_mfma_f32_16x16x32_bf16 a[92:95],a[248:251],v[200:203],a[92:95]
ds_read_b64_tr_b16 a[142:143],v1 offset:35840
v_fma_f32 v153,v20,v153,v21
v_smfmac_f32_16x16x64_bf16 v[56:59],v[28:31],v[200:207],v27
v_accvgpr_read_b32 v154,a18
v_mfma_f32_16x16x32_bf16 a[96:99],a[192:195],v[232:235],a[96:99]
buffer_store_dwordx4 v[136:139],v6,s[36:39],s84 offen sc1
v_mfma_f32_16x16x32_bf16 a[96:99],a[196:199],v[236:239],a[96:99]
v_accvgpr_read_b32 v155,a19
v_cvt_pk_bf16_f32 v152,v152,v153
v_mfma_f32_16x16x32_bf16 a[100:103],a[204:207],v[236:239],a[100:103]
v_fma_f32 v154,v20,v154,v21
v_fma_f32 v155,v20,v155,v21
v_mfma_f32_16x16x32_bf16 a[100:103],a[200:203],v[232:235],a[100:103]
v_cvt_pk_bf16_f32 v153,v154,v155
v_accvgpr_read_b32 v154,a20
v_mfma_f32_16x16x32_bf16 a[104:107],a[208:211],v[232:235],a[104:107]
buffer_load_dwordx4 v8,s[72:75],s83 offen offset:1024 lds
v_mfma_f32_16x16x32_bf16 a[104:107],a[212:215],v[236:239],a[104:107]
v_accvgpr_read_b32 v155,a21
v_accvgpr_read_b32 v16,a22
v_mfma_f32_16x16x32_bf16 a[108:111],a[220:223],v[236:239],a[108:111]
v_accvgpr_read_b32 v17,a23
v_fma_f32 v154,v20,v154,v21
v_mfma_f32_16x16x32_bf16 a[108:111],a[216:219],v[232:235],a[108:111]
v_fma_f32 v155,v20,v155,v21
v_fma_f32 v16,v20,v16,v21
v_mfma_f32_16x16x32_bf16 a[112:115],a[224:227],v[232:235],a[112:115]
v_fma_f32 v17,v20,v17,v21
v_cvt_pk_bf16_f32 v154,v154,v155
v_mfma_f32_16x16x32_bf16 a[112:115],a[228:231],v[236:239],a[112:115]
ds_read_b64_tr_b16 a[144:145],v2 offset:32
v_cvt_pk_bf16_f32 v155,v16,v17
v_mfma_f32_16x16x32_bf16 a[116:119],a[236:239],v[236:239],a[116:119]
ds_read_b64_tr_b16 a[146:147],v2 offset:5152
ds_read_b64_tr_b16 a[148:149],v2 offset:10272
v_mfma_f32_16x16x32_bf16 a[116:119],a[232:235],v[232:235],a[116:119]
buffer_load_dwordx4 v9,s[72:75],s83 offen offset:2048 lds
v_mfma_f32_16x16x32_bf16 a[120:123],a[240:243],v[232:235],a[120:123]
ds_read_b64_tr_b16 a[150:151],v2 offset:15392
ds_read_b64_tr_b16 a[152:153],v2 offset:20512
v_mfma_f32_16x16x32_bf16 a[120:123],a[244:247],v[236:239],a[120:123]
ds_read_b64_tr_b16 a[154:155],v2 offset:25632
v_accvgpr_read_b32 v156,a24
v_mfma_f32_16x16x32_bf16 a[124:127],a[252:255],v[236:239],a[124:127]
ds_read_b64_tr_b16 a[156:157],v2 offset:30752
v_accvgpr_read_b32 v157,a25
v_mfma_f32_16x16x32_bf16 a[124:127],a[248:251],v[232:235],a[124:127]
ds_read_b64_tr_b16 a[158:159],v2 offset:35872
v_fma_f32 v156,v20,v156,v21
v_smfmac_f32_16x16x64_bf16 v[60:63],v[28:31],v[232:239],v27
s_waitcnt lgkmcnt(8)
v_fma_f32 v157,v20,v157,v21
v_mfma_f32_16x16x32_bf16 a[192:195],a[128:131],v[64:67],0
s_lshl_b32 s94,s70,4
v_accvgpr_read_b32 v158,a26
v_accvgpr_read_b32 v159,a27
v_mfma_f32_16x16x32_bf16 a[192:195],a[132:135],v[68:71],a[192:195]
s_add_u32 s94,s94,s40
v_cvt_pk_bf16_f32 v156,v156,v157
v_fma_f32 v158,v20,v158,v21
v_mfma_f32_16x16x32_bf16 a[192:195],a[136:139],v[72:75],a[192:195]
s_lshl_b32 s94,s94,4
buffer_store_dwordx4 v[140:143],v6,s[36:39],s84 offen offset:1024 sc1
v_mfma_f32_16x16x32_bf16 a[192:195],a[140:143],v[76:79],a[192:195]
v_fma_f32 v159,v20,v159,v21
v_cvt_pk_bf16_f32 v157,v158,v159
v_mfma_f32_16x16x32_bf16 a[196:199],a[140:143],v[92:95],0
v_accvgpr_read_b32 v158,a28
v_accvgpr_read_b32 v159,a29
v_mfma_f32_16x16x32_bf16 a[196:199],a[136:139],v[88:91],a[196:199]
v_accvgpr_read_b32 v12,a30
v_accvgpr_read_b32 v13,a31
v_mfma_f32_16x16x32_bf16 a[196:199],a[132:135],v[84:87],a[196:199]
buffer_load_dwordx4 v10,s[72:75],s83 offen offset:3072 lds
v_mfma_f32_16x16x32_bf16 a[196:199],a[128:131],v[80:83],a[196:199]
v_fma_f32 v158,v20,v158,v21
v_fma_f32 v159,v20,v159,v21
v_mfma_f32_16x16x32_bf16 a[200:203],a[128:131],v[96:99],0
v_fma_f32 v12,v20,v12,v21
v_fma_f32 v13,v20,v13,v21
v_mfma_f32_16x16x32_bf16 a[200:203],a[132:135],v[100:103],a[200:203]
v_cvt_pk_bf16_f32 v158,v158,v159
v_cvt_pk_bf16_f32 v159,v12,v13
v_mfma_f32_16x16x32_bf16 a[200:203],a[136:139],v[104:107],a[200:203]
ds_read_b64_tr_b16 a[160:161],v1 offset:64
ds_read_b64_tr_b16 a[162:163],v1 offset:5184
v_mfma_f32_16x16x32_bf16 a[200:203],a[140:143],v[108:111],a[200:203]
ds_read_b64_tr_b16 a[164:165],v1 offset:10304
v_add_f32 v52,v53,v52
v_mfma_f32_16x16x32_bf16 a[204:207],a[140:143],v[124:127],0
ds_read_b64_tr_b16 a[166:167],v1 offset:15424
v_max_f32 v22,0x00800000,v52
v_mfma_f32_16x16x32_bf16 a[204:207],a[136:139],v[120:123],a[204:207]
s_addk_i32 m0,4096
ds_read_b64_tr_b16 a[168:169],v1 offset:20544
v_bfe_u32 v24,v6,4,2
v_mfma_f32_16x16x32_bf16 a[204:207],a[132:135],v[116:119],a[204:207]
ds_read_b64_tr_b16 a[170:171],v1 offset:25664
ds_read_b64_tr_b16 a[172:173],v1 offset:30784
v_mfma_f32_16x16x32_bf16 a[204:207],a[128:131],v[112:115],a[204:207]
s_waitcnt lgkmcnt(7)
ds_read_b64_tr_b16 a[174:175],v1 offset:35904
v_min_u32 v24,2,v24
v_mfma_f32_16x16x32_bf16 a[208:211],a[144:147],v[64:67],0
buffer_load_dwordx4 v11,s[72:75],s83 offen lds
v_mfma_f32_16x16x32_bf16 a[208:211],a[148:151],v[68:71],a[208:211]
v_rcp_f32 v22,v22
v_mfma_f32_16x16x32_bf16 a[208:211],a[152:155],v[72:75],a[208:211]
v_mul_f32 v23,0xbe22f983,v22
v_add_u32 v24,s71,v24
v_mfma_f32_16x16x32_bf16 a[208:211],a[156:159],v[76:79],a[208:211]
.p2align 3
v_accvgpr_read_b32 v168,a32
v_accvgpr_read_b32 v169,a33
v_accvgpr_read_b32 v170,a34
v_accvgpr_read_b32 v171,a35
v_accvgpr_read_b32 v12,a36
v_accvgpr_read_b32 v13,a37
v_accvgpr_read_b32 v14,a38
v_accvgpr_read_b32 v15,a39
v_pk_fma_f32 v[168:169],v[22:23],v[168:169],v[22:23] op_sel:[0,0,1] op_sel_hi:[0,1,1]
v_pk_fma_f32 v[170:171],v[22:23],v[170:171],v[22:23] op_sel:[0,0,1] op_sel_hi:[0,1,1]
v_pk_fma_f32 v[12:13],v[22:23],v[12:13],v[22:23] op_sel:[0,0,1] op_sel_hi:[0,1,1]
v_pk_fma_f32 v[14:15],v[22:23],v[14:15],v[22:23] op_sel:[0,0,1] op_sel_hi:[0,1,1]
v_cvt_pk_bf16_f32 v168,v168,v169
v_cvt_pk_bf16_f32 v169,v170,v171
v_cvt_pk_bf16_f32 v170,v12,v13
v_cvt_pk_bf16_f32 v171,v14,v15
v_mfma_f32_16x16x32_bf16 a[212:215],a[156:159],v[92:95],0
s_cmp_ge_u32 1,s71
s_cselect_b32 s80,4,2
v_add_u32 v24,1,v24
v_mfma_f32_16x16x32_bf16 a[212:215],a[152:155],v[88:91],a[212:215]
s_mul_i32 s80,s80,163840
s_add_u32 s80,s80,s81
v_and_b32 v24,3,v24
v_mfma_f32_16x16x32_bf16 a[212:215],a[148:151],v[84:87],a[212:215]
s_add_u32 s83,s80,20480
buffer_store_dwordx4 v[152:155],v6,s[36:39],s84 offen offset:2048 sc1
v_mfma_f32_16x16x32_bf16 a[212:215],a[144:147],v[80:83],a[212:215]
ds_read_b64_tr_b16 a[176:177],v2 offset:96
v_lshlrev_b32 v24,6,v24
v_mfma_f32_16x16x32_bf16 a[216:219],a[144:147],v[96:99],0
ds_read_b64_tr_b16 a[178:179],v2 offset:5216
ds_read_b64_tr_b16 a[180:181],v2 offset:10336
v_mfma_f32_16x16x32_bf16 a[216:219],a[148:151],v[100:103],a[216:219]
ds_read_b64_tr_b16 a[182:183],v2 offset:15456
v_accvgpr_read_b32 v184,a48
v_mfma_f32_16x16x32_bf16 a[216:219],a[152:155],v[104:107],a[216:219]
ds_read_b64_tr_b16 a[184:185],v2 offset:20576
v_accvgpr_read_b32 v185,a49
v_mfma_f32_16x16x32_bf16 a[216:219],a[156:159],v[108:111],a[216:219]
ds_read_b64_tr_b16 a[186:187],v2 offset:25696
v_fma_f32 v184,v22,v184,v23
v_mfma_f32_16x16x32_bf16 a[220:223],a[156:159],v[124:127],0
ds_read_b64_tr_b16 a[188:189],v2 offset:30816
ds_read_b64_tr_b16 a[190:191],v2 offset:35936
v_mfma_f32_16x16x32_bf16 a[220:223],a[152:155],v[120:123],a[220:223]
.p2align 3
v_accvgpr_read_b32 v172,a40
v_accvgpr_read_b32 v173,a41
v_accvgpr_read_b32 v174,a42
v_accvgpr_read_b32 v175,a43
v_accvgpr_read_b32 v12,a44
v_accvgpr_read_b32 v13,a45
v_accvgpr_read_b32 v14,a46
v_accvgpr_read_b32 v15,a47
v_pk_fma_f32 v[172:173],v[22:23],v[172:173],v[22:23] op_sel:[0,0,1] op_sel_hi:[0,1,1]
v_pk_fma_f32 v[174:175],v[22:23],v[174:175],v[22:23] op_sel:[0,0,1] op_sel_hi:[0,1,1]
)A"
R"A(v_pk_fma_f32 v[12:13],v[22:23],v[12:13],v[22:23] op_sel:[0,0,1] op_sel_hi:[0,1,1]
v_pk_fma_f32 v[14:15],v[22:23],v[14:15],v[22:23] op_sel:[0,0,1] op_sel_hi:[0,1,1]
v_cvt_pk_bf16_f32 v172,v172,v173
v_cvt_pk_bf16_f32 v173,v174,v175
v_cvt_pk_bf16_f32 v174,v12,v13
v_cvt_pk_bf16_f32 v175,v14,v15
v_mfma_f32_16x16x32_bf16 a[220:223],a[148:151],v[116:119],a[220:223]
s_add_u32 m0,s82,122880
v_fma_f32 v185,v22,v185,v23
v_accvgpr_read_b32 v186,a50
v_mfma_f32_16x16x32_bf16 a[220:223],a[144:147],v[112:115],a[220:223]
s_waitcnt lgkmcnt(7)
v_accvgpr_read_b32 v187,a51
v_cvt_pk_bf16_f32 v184,v184,v185
v_mfma_f32_16x16x32_bf16 a[224:227],a[160:163],v[64:67],0
v_fma_f32 v186,v22,v186,v23
v_fma_f32 v187,v22,v187,v23
v_mfma_f32_16x16x32_bf16 a[224:227],a[164:167],v[68:71],a[224:227]
buffer_load_dwordx4 v7,s[72:75],s80 offen lds
v_mfma_f32_16x16x32_bf16 a[224:227],a[168:171],v[72:75],a[224:227]
v_cvt_pk_bf16_f32 v185,v186,v187
v_accvgpr_read_b32 v186,a52
v_mfma_f32_16x16x32_bf16 a[224:227],a[172:175],v[76:79],a[224:227]
v_accvgpr_read_b32 v187,a53
v_accvgpr_read_b32 v14,a54
v_mfma_f32_16x16x32_bf16 a[228:231],a[172:175],v[92:95],0
v_accvgpr_read_b32 v15,a55
v_fma_f32 v186,v22,v186,v23
v_mfma_f32_16x16x32_bf16 a[228:231],a[168:171],v[88:91],a[228:231]
v_fma_f32 v187,v22,v187,v23
v_fma_f32 v14,v22,v14,v23
v_mfma_f32_16x16x32_bf16 a[228:231],a[164:167],v[84:87],a[228:231]
v_fma_f32 v15,v22,v15,v23
v_cvt_pk_bf16_f32 v186,v186,v187
v_mfma_f32_16x16x32_bf16 a[228:231],a[160:163],v[80:83],a[228:231]
ds_read_b64_tr_b16 a[128:129],v1 offset:128
v_cvt_pk_bf16_f32 v187,v14,v15
v_mfma_f32_16x16x32_bf16 a[232:235],a[160:163],v[96:99],0
buffer_load_dwordx2 v[4:5],v24,s[64:67],s94 offen sc1
v_mfma_f32_16x16x32_bf16 a[232:235],a[164:167],v[100:103],a[232:235]
ds_read_b64_tr_b16 a[130:131],v1 offset:5248
ds_read_b64_tr_b16 a[132:133],v1 offset:10368
v_mfma_f32_16x16x32_bf16 a[232:235],a[168:171],v[104:107],a[232:235]
ds_read_b64_tr_b16 a[134:135],v1 offset:15488
v_accvgpr_read_b32 v188,a56
v_mfma_f32_16x16x32_bf16 a[232:235],a[172:175],v[108:111],a[232:235]
ds_read_b64_tr_b16 a[136:137],v1 offset:20608
v_accvgpr_read_b32 v189,a57
v_mfma_f32_16x16x32_bf16 a[236:239],a[172:175],v[124:127],0
buffer_store_dwordx4 v[156:159],v6,s[36:39],s84 offen offset:3072 sc1
v_mfma_f32_16x16x32_bf16 a[236:239],a[168:171],v[120:123],a[236:239]
ds_read_b64_tr_b16 a[138:139],v1 offset:25728
ds_read_b64_tr_b16 a[140:141],v1 offset:30848
v_mfma_f32_16x16x32_bf16 a[236:239],a[164:167],v[116:119],a[236:239]
ds_read_b64_tr_b16 a[142:143],v1 offset:35968
v_fma_f32 v188,v22,v188,v23
v_mfma_f32_16x16x32_bf16 a[236:239],a[160:163],v[112:115],a[236:239]
s_waitcnt lgkmcnt(7)
v_fma_f32 v189,v22,v189,v23
v_accvgpr_read_b32 v190,a58
v_mfma_f32_16x16x32_bf16 a[240:243],a[176:179],v[64:67],0
buffer_load_dwordx4 v8,s[72:75],s80 offen offset:1024 lds
v_mfma_f32_16x16x32_bf16 a[240:243],a[180:183],v[68:71],a[240:243]
v_accvgpr_read_b32 v191,a59
v_cvt_pk_bf16_f32 v188,v188,v189
v_mfma_f32_16x16x32_bf16 a[240:243],a[184:187],v[72:75],a[240:243]
v_fma_f32 v190,v22,v190,v23
v_fma_f32 v191,v22,v191,v23
v_mfma_f32_16x16x32_bf16 a[240:243],a[188:191],v[76:79],a[240:243]
v_cvt_pk_bf16_f32 v189,v190,v191
v_accvgpr_read_b32 v190,a60
v_mfma_f32_16x16x32_bf16 a[244:247],a[188:191],v[92:95],0
v_accvgpr_read_b32 v191,a61
v_accvgpr_read_b32 v16,a62
v_mfma_f32_16x16x32_bf16 a[244:247],a[184:187],v[88:91],a[244:247]
v_accvgpr_read_b32 v17,a63
v_fma_f32 v190,v22,v190,v23
v_mfma_f32_16x16x32_bf16 a[244:247],a[180:183],v[84:87],a[244:247]
v_fma_f32 v191,v22,v191,v23
v_fma_f32 v16,v22,v16,v23
v_mfma_f32_16x16x32_bf16 a[244:247],a[176:179],v[80:83],a[244:247]
v_fma_f32 v17,v22,v17,v23
v_cvt_pk_bf16_f32 v190,v190,v191
v_mfma_f32_16x16x32_bf16 a[248:251],a[176:179],v[96:99],0
ds_read_b64_tr_b16 a[144:145],v2 offset:160
v_cvt_pk_bf16_f32 v191,v16,v17
v_mfma_f32_16x16x32_bf16 a[248:251],a[180:183],v[100:103],a[248:251]
buffer_load_dwordx4 v9,s[72:75],s80 offen offset:2048 lds
v_mfma_f32_16x16x32_bf16 a[248:251],a[184:187],v[104:107],a[248:251]
ds_read_b64_tr_b16 a[146:147],v2 offset:5280
ds_read_b64_tr_b16 a[148:149],v2 offset:10400
v_mfma_f32_16x16x32_bf16 a[248:251],a[188:191],v[108:111],a[248:251]
ds_read_b64_tr_b16 a[150:151],v2 offset:15520
v_add_f32 v56,v57,v56
v_mfma_f32_16x16x32_bf16 a[252:255],a[188:191],v[124:127],0
ds_read_b64_tr_b16 a[152:153],v2 offset:20640
v_max_f32 v20,0x00800000,v56
v_mfma_f32_16x16x32_bf16 a[252:255],a[184:187],v[120:123],a[252:255]
ds_read_b64_tr_b16 a[154:155],v2 offset:25760
v_max3_f32 v18,|v136|,|v137|,|v138|
v_mfma_f32_16x16x32_bf16 a[252:255],a[180:183],v[116:119],a[252:255]
s_waitcnt vmcnt(18)
ds_read_b64_tr_b16 a[156:157],v2 offset:30880
ds_read_b64_tr_b16 a[158:159],v2 offset:36000
v_mfma_f32_16x16x32_bf16 a[252:255],a[176:179],v[112:115],a[252:255]
s_barrier
buffer_store_dwordx4 v[168:171],v6,s[36:39],s85 offen sc1
s_waitcnt lgkmcnt(8)
v_mfma_f32_16x16x32_bf16 a[0:3],a[128:131],v[64:67],0
v_max3_f32 v18,v18,|v139|,s18
v_fmac_f32 v18,0x39800000,v21
v_mfma_f32_16x16x32_bf16 a[0:3],a[132:135],v[68:71],a[0:3]
v_rcp_f32 v20,v20
v_mfma_f32_16x16x32_bf16 a[0:3],a[136:139],v[72:75],a[0:3]
ds_read_b64_tr_b16 a[160:161],v1 offset:192
v_mul_f32 v21,0xbe22f983,v20
v_mfma_f32_16x16x32_bf16 a[0:3],a[140:143],v[76:79],a[0:3]
.p2align 3
v_accvgpr_read_b32 v200,a64
v_accvgpr_read_b32 v201,a65
v_accvgpr_read_b32 v202,a66
v_accvgpr_read_b32 v203,a67
v_accvgpr_read_b32 v12,a68
v_accvgpr_read_b32 v13,a69
v_accvgpr_read_b32 v14,a70
v_accvgpr_read_b32 v15,a71
v_pk_fma_f32 v[200:201],v[20:21],v[200:201],v[20:21] op_sel:[0,0,1] op_sel_hi:[0,1,1]
v_pk_fma_f32 v[202:203],v[20:21],v[202:203],v[20:21] op_sel:[0,0,1] op_sel_hi:[0,1,1]
v_pk_fma_f32 v[12:13],v[20:21],v[12:13],v[20:21] op_sel:[0,0,1] op_sel_hi:[0,1,1]
v_pk_fma_f32 v[14:15],v[20:21],v[14:15],v[20:21] op_sel:[0,0,1] op_sel_hi:[0,1,1]
v_cvt_pk_bf16_f32 v200,v200,v201
v_cvt_pk_bf16_f32 v201,v202,v203
v_cvt_pk_bf16_f32 v202,v12,v13
v_cvt_pk_bf16_f32 v203,v14,v15
v_mfma_f32_16x16x32_bf16 a[4:7],a[140:143],v[92:95],0
buffer_load_dwordx4 v10,s[72:75],s80 offen offset:3072 lds
v_mfma_f32_16x16x32_bf16 a[4:7],a[136:139],v[88:91],a[4:7]
ds_read_b64_tr_b16 a[162:163],v1 offset:5312
ds_read_b64_tr_b16 a[164:165],v1 offset:10432
v_mfma_f32_16x16x32_bf16 a[4:7],a[132:135],v[84:87],a[4:7]
ds_read_b64_tr_b16 a[166:167],v1 offset:15552
v_accvgpr_read_b32 v216,a80
v_mfma_f32_16x16x32_bf16 a[4:7],a[128:131],v[80:83],a[4:7]
ds_read_b64_tr_b16 a[168:169],v1 offset:20672
v_accvgpr_read_b32 v217,a81
v_mfma_f32_16x16x32_bf16 a[8:11],a[128:131],v[96:99],0
ds_read_b64_tr_b16 a[170:171],v1 offset:25792
v_fma_f32 v216,v20,v216,v21
v_mfma_f32_16x16x32_bf16 a[8:11],a[132:135],v[100:103],a[8:11]
.p2align 3
v_accvgpr_read_b32 v204,a72
v_accvgpr_read_b32 v205,a73
v_accvgpr_read_b32 v206,a74
v_accvgpr_read_b32 v207,a75
v_accvgpr_read_b32 v12,a76
v_accvgpr_read_b32 v13,a77
v_accvgpr_read_b32 v14,a78
v_accvgpr_read_b32 v15,a79
v_pk_fma_f32 v[204:205],v[20:21],v[204:205],v[20:21] op_sel:[0,0,1] op_sel_hi:[0,1,1]
v_pk_fma_f32 v[206:207],v[20:21],v[206:207],v[20:21] op_sel:[0,0,1] op_sel_hi:[0,1,1]
v_pk_fma_f32 v[12:13],v[20:21],v[12:13],v[20:21] op_sel:[0,0,1] op_sel_hi:[0,1,1]
v_pk_fma_f32 v[14:15],v[20:21],v[14:15],v[20:21] op_sel:[0,0,1] op_sel_hi:[0,1,1]
v_cvt_pk_bf16_f32 v204,v204,v205
v_cvt_pk_bf16_f32 v205,v206,v207
v_cvt_pk_bf16_f32 v206,v12,v13
v_cvt_pk_bf16_f32 v207,v14,v15
v_mfma_f32_16x16x32_bf16 a[8:11],a[136:139],v[104:107],a[8:11]
ds_read_b64_tr_b16 a[172:173],v1 offset:30912
ds_read_b64_tr_b16 a[174:175],v1 offset:36032
v_mfma_f32_16x16x32_bf16 a[8:11],a[140:143],v[108:111],a[8:11]
s_waitcnt vmcnt(5)
v_cmp_ne_u32 s[90:91],s68,v4
v_and_b32 v4,-2,v5
v_cmp_ne_u32 s[92:93],s69,v4
s_nop 0
s_or_b64 s[90:91],s[90:91],s[92:93]
s_cbranch_scc1 L_fr8_%=
L_fo8_%=:
v_mfma_f32_16x16x32_bf16 a[12:15],a[140:143],v[124:127],0
s_nop 0
v_readlane_b32 s76,v5,0
v_readlane_b32 s77,v5,1
v_readlane_b32 s78,v5,2
v_mfma_f32_16x16x32_bf16 a[12:15],a[136:139],v[120:123],a[12:15]
s_addk_i32 m0,4096
s_and_b32 s77,s77,1
s_lshl_b32 s77,s77,2
v_mfma_f32_16x16x32_bf16 a[12:15],a[132:135],v[116:119],a[12:15]
s_nop 1
s_and_b32 s54,s76,1
v_mfma_f32_16x16x32_bf16 a[12:15],a[128:131],v[112:115],a[12:15]
s_and_b32 s78,s78,1
s_or_b32 s54,s54,s77
s_lshl_b32 s78,s78,4
s_waitcnt lgkmcnt(3)
v_mfma_f32_16x16x32_bf16 a[16:19],a[144:147],v[64:67],0
s_or_b32 s54,s54,s78
buffer_load_dwordx4 v11,s[72:75],s80 offen lds
v_mfma_f32_16x16x32_bf16 a[16:19],a[148:151],v[68:71],a[16:19]
s_mulk_i32 s54,3084
s_lshl_b32 s76,s71,1
s_lshl_b32 s54,s54,s76
v_mfma_f32_16x16x32_bf16 a[16:19],a[152:155],v[72:75],a[16:19]
s_lshr_b32 s54,s54,8
s_cmp_ge_u32 0,s71
s_cselect_b32 s76,2,0
v_mfma_f32_16x16x32_bf16 a[16:19],a[156:159],v[76:79],a[16:19]
s_bitcmp1_b32 s54,s76
s_cselect_b32 s77,8192,0
s_lshl_b32 s76,s76,17
v_mfma_f32_16x16x32_bf16 a[20:23],a[156:159],v[92:95],0
s_add_u32 s76,s76,s77
buffer_store_dwordx4 v[172:175],v6,s[36:39],s85 offen offset:1024 sc1
v_mfma_f32_16x16x32_bf16 a[20:23],a[152:155],v[88:91],a[20:23]
s_add_u32 s77,s76,4096
s_add_u32 s78,s76,16384
s_add_u32 s79,s76,20480
v_mfma_f32_16x16x32_bf16 a[20:23],a[148:151],v[84:87],a[20:23]
s_max_i32 s76,s76,s18
v_fma_f32 v217,v20,v217,v21
v_accvgpr_read_b32 v218,a82
v_mfma_f32_16x16x32_bf16 a[20:23],a[144:147],v[80:83],a[20:23]
s_max_i32 s77,s77,s19
v_accvgpr_read_b32 v219,a83
v_cvt_pk_bf16_f32 v216,v216,v217
v_mfma_f32_16x16x32_bf16 a[24:27],a[144:147],v[96:99],0
s_max_i32 s78,s78,s100
v_fma_f32 v218,v20,v218,v21
v_fma_f32 v219,v20,v219,v21
v_mfma_f32_16x16x32_bf16 a[24:27],a[148:151],v[100:103],a[24:27]
s_addk_i32 m0,1024
v_cvt_pk_bf16_f32 v217,v218,v219
v_accvgpr_read_b32 v218,a84
v_mfma_f32_16x16x32_bf16 a[24:27],a[152:155],v[104:107],a[24:27]
s_max_i32 s79,s79,s101
v_accvgpr_read_b32 v219,a85
v_accvgpr_read_b32 v12,a86
v_mfma_f32_16x16x32_bf16 a[24:27],a[156:159],v[108:111],a[24:27]
v_accvgpr_read_b32 v13,a87
v_fma_f32 v218,v20,v218,v21
v_mfma_f32_16x16x32_bf16 a[28:31],a[156:159],v[124:127],0
buffer_load_dwordx4 v7,s[72:75],s83 offen lds
v_mfma_f32_16x16x32_bf16 a[28:31],a[152:155],v[120:123],a[28:31]
v_fma_f32 v219,v20,v219,v21
v_fma_f32 v12,v20,v12,v21
v_mfma_f32_16x16x32_bf16 a[28:31],a[148:151],v[116:119],a[28:31]
v_fma_f32 v13,v20,v13,v21
v_cvt_pk_bf16_f32 v218,v218,v219
v_mfma_f32_16x16x32_bf16 a[28:31],a[144:147],v[112:115],a[28:31]
ds_read_b64_tr_b16 a[176:177],v2 offset:224
v_cvt_pk_bf16_f32 v219,v12,v13
v_mfma_f32_16x16x32_bf16 a[32:35],a[160:163],v[64:67],0
buffer_load_dwordx4 v[128:131],v6,s[28:31],s76 offen sc1
v_mfma_f32_16x16x32_bf16 a[32:35],a[164:167],v[68:71],a[32:35]
s_waitcnt lgkmcnt(1)
ds_read_b64_tr_b16 a[178:179],v2 offset:5344
ds_read_b64_tr_b16 a[180:181],v2 offset:10464
v_mfma_f32_16x16x32_bf16 a[32:35],a[168:171],v[72:75],a[32:35]
ds_read_b64_tr_b16 a[182:183],v2 offset:15584
v_accvgpr_read_b32 v220,a88
v_mfma_f32_16x16x32_bf16 a[32:35],a[172:175],v[76:79],a[32:35]
ds_read_b64_tr_b16 a[184:185],v2 offset:20704
v_accvgpr_read_b32 v221,a89
v_mfma_f32_16x16x32_bf16 a[36:39],a[172:175],v[92:95],0
ds_read_b64_tr_b16 a[186:187],v2 offset:25824
v_fma_f32 v220,v20,v220,v21
v_mfma_f32_16x16x32_bf16 a[36:39],a[168:171],v[88:91],a[36:39]
ds_read_b64_tr_b16 a[188:189],v2 offset:30944
ds_read_b64_tr_b16 a[190:191],v2 offset:36064
v_mfma_f32_16x16x32_bf16 a[36:39],a[164:167],v[84:87],a[36:39]
buffer_store_dwordx4 v[184:187],v6,s[36:39],s85 offen offset:2048 sc1
v_mfma_f32_16x16x32_bf16 a[36:39],a[160:163],v[80:83],a[36:39]
v_fma_f32 v221,v20,v221,v21
v_accvgpr_read_b32 v222,a90
v_mfma_f32_16x16x32_bf16 a[40:43],a[160:163],v[96:99],0
v_accvgpr_read_b32 v223,a91
v_cvt_pk_bf16_f32 v220,v220,v221
v_mfma_f32_16x16x32_bf16 a[40:43],a[164:167],v[100:103],a[40:43]
v_fma_f32 v222,v20,v222,v21
v_fma_f32 v223,v20,v223,v21
v_mfma_f32_16x16x32_bf16 a[40:43],a[168:171],v[104:107],a[40:43]
buffer_load_dwordx4 v8,s[72:75],s83 offen offset:1024 lds
v_mfma_f32_16x16x32_bf16 a[40:43],a[172:175],v[108:111],a[40:43]
v_cvt_pk_bf16_f32 v221,v222,v223
v_accvgpr_read_b32 v222,a92
v_mfma_f32_16x16x32_bf16 a[44:47],a[172:175],v[124:127],0
)A"
R"A(v_accvgpr_read_b32 v223,a93
v_accvgpr_read_b32 v14,a94
v_mfma_f32_16x16x32_bf16 a[44:47],a[168:171],v[120:123],a[44:47]
v_accvgpr_read_b32 v15,a95
v_fma_f32 v222,v20,v222,v21
v_mfma_f32_16x16x32_bf16 a[44:47],a[164:167],v[116:119],a[44:47]
buffer_load_dwordx4 v[160:163],v6,s[28:31],s76 offen offset:1024 sc1
v_mfma_f32_16x16x32_bf16 a[44:47],a[160:163],v[112:115],a[44:47]
s_waitcnt lgkmcnt(0)
v_fma_f32 v223,v20,v223,v21
v_fma_f32 v14,v20,v14,v21
v_mfma_f32_16x16x32_bf16 a[48:51],a[176:179],v[64:67],0
v_fma_f32 v15,v20,v15,v21
v_cvt_pk_bf16_f32 v222,v222,v223
v_mfma_f32_16x16x32_bf16 a[48:51],a[180:183],v[68:71],a[48:51]
ds_read_b64_tr_b16 a[128:129],v1 offset:256
v_cvt_pk_bf16_f32 v223,v14,v15
v_mfma_f32_16x16x32_bf16 a[48:51],a[184:187],v[72:75],a[48:51]
ds_read_b64_tr_b16 a[130:131],v1 offset:5376
ds_read_b64_tr_b16 a[132:133],v1 offset:10496
v_mfma_f32_16x16x32_bf16 a[48:51],a[188:191],v[76:79],a[48:51]
buffer_load_dwordx4 v[192:195],v6,s[28:31],s76 offen offset:2048 sc1
v_mfma_f32_16x16x32_bf16 a[52:55],a[188:191],v[92:95],0
ds_read_b64_tr_b16 a[134:135],v1 offset:15616
ds_read_b64_tr_b16 a[136:137],v1 offset:20736
v_mfma_f32_16x16x32_bf16 a[52:55],a[184:187],v[88:91],a[52:55]
ds_read_b64_tr_b16 a[138:139],v1 offset:25856
v_add_f32 v60,v61,v60
v_mfma_f32_16x16x32_bf16 a[52:55],a[180:183],v[84:87],a[52:55]
ds_read_b64_tr_b16 a[140:141],v1 offset:30976
v_max_f32 v22,0x00800000,v60
v_mfma_f32_16x16x32_bf16 a[52:55],a[176:179],v[80:83],a[52:55]
buffer_load_dwordx4 v9,s[72:75],s83 offen offset:2048 lds
v_mfma_f32_16x16x32_bf16 a[56:59],a[176:179],v[96:99],0
ds_read_b64_tr_b16 a[142:143],v1 offset:36096
v_max3_f32 v19,|v168|,|v169|,|v170|
v_mfma_f32_16x16x32_bf16 a[56:59],a[180:183],v[100:103],a[56:59]
v_max3_f32 v19,v19,|v171|,s19
v_fmac_f32 v19,0x39800000,v23
v_mfma_f32_16x16x32_bf16 a[56:59],a[184:187],v[104:107],a[56:59]
v_rcp_f32 v22,v22
v_mfma_f32_16x16x32_bf16 a[56:59],a[188:191],v[108:111],a[56:59]
buffer_store_dwordx4 v[188:191],v6,s[36:39],s85 offen offset:3072 sc1
v_mfma_f32_16x16x32_bf16 a[60:63],a[188:191],v[124:127],0
ds_read_b64_tr_b16 a[144:145],v2 offset:288
v_mul_f32 v23,0xbe22f983,v22
v_mfma_f32_16x16x32_bf16 a[60:63],a[184:187],v[120:123],a[60:63]
.p2align 3
v_accvgpr_read_b32 v232,a96
v_accvgpr_read_b32 v233,a97
v_accvgpr_read_b32 v234,a98
v_accvgpr_read_b32 v235,a99
v_accvgpr_read_b32 v12,a100
v_accvgpr_read_b32 v13,a101
v_accvgpr_read_b32 v14,a102
v_accvgpr_read_b32 v15,a103
v_pk_fma_f32 v[232:233],v[22:23],v[232:233],v[22:23] op_sel:[0,0,1] op_sel_hi:[0,1,1]
v_pk_fma_f32 v[234:235],v[22:23],v[234:235],v[22:23] op_sel:[0,0,1] op_sel_hi:[0,1,1]
v_pk_fma_f32 v[12:13],v[22:23],v[12:13],v[22:23] op_sel:[0,0,1] op_sel_hi:[0,1,1]
v_pk_fma_f32 v[14:15],v[22:23],v[14:15],v[22:23] op_sel:[0,0,1] op_sel_hi:[0,1,1]
v_cvt_pk_bf16_f32 v232,v232,v233
v_cvt_pk_bf16_f32 v233,v234,v235
v_cvt_pk_bf16_f32 v234,v12,v13
v_cvt_pk_bf16_f32 v235,v14,v15
v_mfma_f32_16x16x32_bf16 a[60:63],a[180:183],v[116:119],a[60:63]
ds_read_b64_tr_b16 a[146:147],v2 offset:5408
ds_read_b64_tr_b16 a[148:149],v2 offset:10528
v_mfma_f32_16x16x32_bf16 a[60:63],a[176:179],v[112:115],a[60:63]
s_waitcnt lgkmcnt(4)
buffer_load_dwordx4 v[224:227],v6,s[28:31],s76 offen offset:3072 sc1
v_mfma_f32_16x16x32_bf16 a[64:67],a[128:131],v[64:67],0
ds_read_b64_tr_b16 a[150:151],v2 offset:15648
ds_read_b64_tr_b16 a[152:153],v2 offset:20768
v_mfma_f32_16x16x32_bf16 a[64:67],a[132:135],v[68:71],a[64:67]
ds_read_b64_tr_b16 a[154:155],v2 offset:25888
v_accvgpr_read_b32 v248,a112
v_mfma_f32_16x16x32_bf16 a[64:67],a[136:139],v[72:75],a[64:67]
.p2align 3
v_accvgpr_read_b32 v236,a104
v_accvgpr_read_b32 v237,a105
v_accvgpr_read_b32 v238,a106
v_accvgpr_read_b32 v239,a107
v_accvgpr_read_b32 v12,a108
v_accvgpr_read_b32 v13,a109
v_accvgpr_read_b32 v14,a110
v_accvgpr_read_b32 v15,a111
v_pk_fma_f32 v[236:237],v[22:23],v[236:237],v[22:23] op_sel:[0,0,1] op_sel_hi:[0,1,1]
v_pk_fma_f32 v[238:239],v[22:23],v[238:239],v[22:23] op_sel:[0,0,1] op_sel_hi:[0,1,1]
v_pk_fma_f32 v[12:13],v[22:23],v[12:13],v[22:23] op_sel:[0,0,1] op_sel_hi:[0,1,1]
v_pk_fma_f32 v[14:15],v[22:23],v[14:15],v[22:23] op_sel:[0,0,1] op_sel_hi:[0,1,1]
v_cvt_pk_bf16_f32 v236,v236,v237
v_cvt_pk_bf16_f32 v237,v238,v239
v_cvt_pk_bf16_f32 v238,v12,v13
v_cvt_pk_bf16_f32 v239,v14,v15
s_waitcnt lgkmcnt(6)
v_mfma_f32_16x16x32_bf16 a[64:67],a[140:143],v[76:79],a[64:67]
buffer_load_dwordx4 v10,s[72:75],s83 offen offset:3072 lds
v_mfma_f32_16x16x32_bf16 a[68:71],a[140:143],v[92:95],0
ds_read_b64_tr_b16 a[156:157],v2 offset:31008
ds_read_b64_tr_b16 a[158:159],v2 offset:36128
v_mfma_f32_16x16x32_bf16 a[68:71],a[136:139],v[88:91],a[68:71]
v_accvgpr_read_b32 v249,a113
v_fma_f32 v248,v22,v248,v23
v_mfma_f32_16x16x32_bf16 a[68:71],a[132:135],v[84:87],a[68:71]
v_fma_f32 v249,v22,v249,v23
v_accvgpr_read_b32 v250,a114
v_mfma_f32_16x16x32_bf16 a[68:71],a[128:131],v[80:83],a[68:71]
buffer_load_dwordx4 v[132:135],v6,s[28:31],s77 offen sc1
v_mfma_f32_16x16x32_bf16 a[72:75],a[128:131],v[96:99],0
v_accvgpr_read_b32 v251,a115
v_cvt_pk_bf16_f32 v248,v248,v249
v_mfma_f32_16x16x32_bf16 a[72:75],a[132:135],v[100:103],a[72:75]
s_addk_i32 m0,4096
v_fma_f32 v250,v22,v250,v23
v_fma_f32 v251,v22,v251,v23
v_mfma_f32_16x16x32_bf16 a[72:75],a[136:139],v[104:107],a[72:75]
v_cvt_pk_bf16_f32 v249,v250,v251
v_accvgpr_read_b32 v250,a116
v_mfma_f32_16x16x32_bf16 a[72:75],a[140:143],v[108:111],a[72:75]
v_accvgpr_read_b32 v251,a117
v_accvgpr_read_b32 v16,a118
v_mfma_f32_16x16x32_bf16 a[76:79],a[140:143],v[124:127],0
buffer_load_dwordx4 v11,s[72:75],s83 offen lds
v_mfma_f32_16x16x32_bf16 a[76:79],a[136:139],v[120:123],a[76:79]
v_accvgpr_read_b32 v17,a119
v_fma_f32 v250,v22,v250,v23
v_mfma_f32_16x16x32_bf16 a[76:79],a[132:135],v[116:119],a[76:79]
v_fma_f32 v251,v22,v251,v23
v_fma_f32 v16,v22,v16,v23
v_mfma_f32_16x16x32_bf16 a[76:79],a[128:131],v[112:115],a[76:79]
s_waitcnt lgkmcnt(0)
v_fma_f32 v17,v22,v17,v23
v_cvt_pk_bf16_f32 v250,v250,v251
v_mfma_f32_16x16x32_bf16 a[80:83],a[144:147],v[64:67],0
v_cvt_pk_bf16_f32 v251,v16,v17
v_subrev_u32 v1,81920,v1
v_mfma_f32_16x16x32_bf16 a[80:83],a[148:151],v[68:71],a[80:83]
buffer_load_dwordx4 v[164:167],v6,s[28:31],s77 offen offset:1024 sc1
v_mfma_f32_16x16x32_bf16 a[80:83],a[152:155],v[72:75],a[80:83]
ds_read_b64_tr_b16 a[160:161],v1
ds_read_b64_tr_b16 a[162:163],v1 offset:5120
v_mfma_f32_16x16x32_bf16 a[80:83],a[156:159],v[76:79],a[80:83]
ds_read_b64_tr_b16 a[164:165],v1 offset:10240
v_min_f32 v18,v18,v19
v_mfma_f32_16x16x32_bf16 a[84:87],a[156:159],v[92:95],0
ds_read_b64_tr_b16 a[166:167],v1 offset:15360
v_max3_f32 v19,|v200|,|v201|,|v202|
v_mfma_f32_16x16x32_bf16 a[84:87],a[152:155],v[88:91],a[84:87]
ds_read_b64_tr_b16 a[168:169],v1 offset:20480
v_cmp_nlt_f32 s[56:57],v48,s63
v_mfma_f32_16x16x32_bf16 a[84:87],a[148:151],v[84:87],a[84:87]
ds_read_b64_tr_b16 a[170:171],v1 offset:25600
v_max3_f32 v19,v19,|v203|,s100
v_mfma_f32_16x16x32_bf16 a[84:87],a[144:147],v[80:83],a[84:87]
ds_read_b64_tr_b16 a[172:173],v1 offset:30720
ds_read_b64_tr_b16 a[174:175],v1 offset:35840
v_mfma_f32_16x16x32_bf16 a[88:91],a[144:147],v[96:99],0
v_fmac_f32 v19,0x39800000,v21
v_accvgpr_read_b32 v252,a120
v_mfma_f32_16x16x32_bf16 a[88:91],a[148:151],v[100:103],a[88:91]
v_accvgpr_read_b32 v253,a121
v_min_f32 v18,v18,v19
v_mfma_f32_16x16x32_bf16 a[88:91],a[152:155],v[104:107],a[88:91]
v_cmp_nlt_f32 s[58:59],v52,s63
s_nop 1
s_or_b64 s[56:57],s[56:57],s[58:59]
v_mfma_f32_16x16x32_bf16 a[88:91],a[156:159],v[108:111],a[88:91]
.p2align 3
v_fma_f32 v252,v22,v252,v23
v_fma_f32 v253,v22,v253,v23
v_accvgpr_read_b32 v254,a122
v_accvgpr_read_b32 v255,a123
v_mfma_f32_16x16x32_bf16 a[92:95],a[156:159],v[124:127],0
v_max3_f32 v19,|v232|,|v233|,|v234|
.p2align 3
v_cvt_pk_bf16_f32 v252,v252,v253
v_fma_f32 v254,v22,v254,v23
v_fma_f32 v255,v22,v255,v23
v_cmp_nlt_f32 s[58:59],v56,s63
s_nop 1
s_or_b64 s[56:57],s[56:57],s[58:59]
v_max3_f32 v19,v19,|v235|,s101
v_cvt_pk_bf16_f32 v253,v254,v255
v_mfma_f32_16x16x32_bf16 a[92:95],a[152:155],v[120:123],a[92:95]
.p2align 3
v_fmac_f32 v19,0x39800000,v23
v_accvgpr_read_b32 v254,a124
v_accvgpr_read_b32 v255,a125
v_cmp_nlt_f32 s[58:59],v60,s63
s_nop 1
s_or_b64 s[56:57],s[56:57],s[58:59]
v_min_f32 v18,v18,v19
.p2align 3
v_accvgpr_read_b32 v12,a126
v_accvgpr_read_b32 v13,a127
v_fma_f32 v254,v22,v254,v23
v_fma_f32 v255,v22,v255,v23
v_mfma_f32_16x16x32_bf16 a[92:95],a[148:151],v[116:119],a[92:95]
buffer_load_dwordx4 v[196:199],v6,s[28:31],s77 offen offset:2048 sc1
.p2align 3
v_fma_f32 v12,v22,v12,v23
v_fma_f32 v13,v22,v13,v23
v_cvt_pk_bf16_f32 v254,v254,v255
v_cmp_gt_f32 s[58:59],0,v18
s_nop 1
s_or_b64 s[56:57],s[56:57],s[58:59]
v_cvt_pk_bf16_f32 v255,v12,v13
v_mfma_f32_16x16x32_bf16 a[92:95],a[144:147],v[112:115],a[92:95]
s_cmp_lg_u64 s[56:57],0
s_cbranch_scc1 L_slow1_%=
L_sret1_%=:
v_subrev_u32 v2,81920,v2
s_waitcnt lgkmcnt(0)
v_mfma_f32_16x16x32_bf16 a[192:195],a[160:163],v[136:139],a[192:195]
ds_read_b64_tr_b16 a[176:177],v2 offset:32
ds_read_b64_tr_b16 a[178:179],v2 offset:5152
v_mfma_f32_16x16x32_bf16 a[192:195],a[164:167],v[140:143],a[192:195]
s_cmp_ge_u32 2,s71
s_cselect_b32 s80,6,4
ds_read_b64_tr_b16 a[180:181],v2 offset:10272
v_mfma_f32_16x16x32_bf16 a[192:195],a[168:171],v[152:155],a[192:195]
s_mul_i32 s80,s80,163840
buffer_store_dwordx4 v[200:203],v6,s[36:39],s86 offen sc1
v_mfma_f32_16x16x32_bf16 a[192:195],a[172:175],v[156:159],a[192:195]
s_add_u32 s80,s80,s81
ds_read_b64_tr_b16 a[182:183],v2 offset:15392
ds_read_b64_tr_b16 a[184:185],v2 offset:20512
v_mfma_f32_16x16x32_bf16 a[196:199],a[172:175],v[188:191],a[196:199]
s_add_u32 s83,s80,20480
ds_read_b64_tr_b16 a[186:187],v2 offset:25632
v_mfma_f32_16x16x32_bf16 a[196:199],a[168:171],v[184:187],a[196:199]
ds_read_b64_tr_b16 a[188:189],v2 offset:30752
v_mfma_f32_16x16x32_bf16 a[196:199],a[164:167],v[172:175],a[196:199]
buffer_store_dwordx4 v[204:207],v6,s[36:39],s86 offen offset:1024 sc1
v_mfma_f32_16x16x32_bf16 a[196:199],a[160:163],v[168:171],a[196:199]
ds_read_b64_tr_b16 a[190:191],v2 offset:35872
ds_read_b64_tr_b16 a[128:129],v1 offset:64
v_mfma_f32_16x16x32_bf16 a[200:203],a[160:163],v[200:203],a[200:203]
ds_read_b64_tr_b16 a[130:131],v1 offset:5184
v_mfma_f32_16x16x32_bf16 a[200:203],a[164:167],v[204:207],a[200:203]
ds_read_b64_tr_b16 a[132:133],v1 offset:10304
v_mfma_f32_16x16x32_bf16 a[200:203],a[168:171],v[216:219],a[200:203]
buffer_load_dwordx4 v[228:231],v6,s[28:31],s77 offen offset:3072 sc1
v_mfma_f32_16x16x32_bf16 a[200:203],a[172:175],v[220:223],a[200:203]
ds_read_b64_tr_b16 a[134:135],v1 offset:15424
ds_read_b64_tr_b16 a[136:137],v1 offset:20544
v_mfma_f32_16x16x32_bf16 a[204:207],a[172:175],v[252:255],a[204:207]
ds_read_b64_tr_b16 a[138:139],v1 offset:25664
v_mfma_f32_16x16x32_bf16 a[204:207],a[168:171],v[248:251],a[204:207]
ds_read_b64_tr_b16 a[140:141],v1 offset:30784
v_mfma_f32_16x16x32_bf16 a[204:207],a[164:167],v[236:239],a[204:207]
ds_read_b64_tr_b16 a[142:143],v1 offset:35904
v_mfma_f32_16x16x32_bf16 a[204:207],a[160:163],v[232:235],a[204:207]
s_barrier
buffer_load_dwordx4 v[144:147],v6,s[28:31],s78 offen sc1
s_waitcnt lgkmcnt(7)
v_mfma_f32_16x16x32_bf16 a[208:211],a[176:179],v[136:139],a[208:211]
ds_read_b64_tr_b16 a[144:145],v2 offset:96
ds_read_b64_tr_b16 a[146:147],v2 offset:5216
v_mfma_f32_16x16x32_bf16 a[208:211],a[180:183],v[140:143],a[208:211]
ds_read_b64_tr_b16 a[148:149],v2 offset:10336
v_mfma_f32_16x16x32_bf16 a[208:211],a[184:187],v[152:155],a[208:211]
s_add_u32 m0,s82,81920
ds_read_b64_tr_b16 a[150:151],v2 offset:15456
v_mfma_f32_16x16x32_bf16 a[208:211],a[188:191],v[156:159],a[208:211]
ds_read_b64_tr_b16 a[152:153],v2 offset:20576
v_mfma_f32_16x16x32_bf16 a[212:215],a[188:191],v[188:191],a[212:215]
ds_read_b64_tr_b16 a[154:155],v2 offset:25696
ds_read_b64_tr_b16 a[156:157],v2 offset:30816
v_mfma_f32_16x16x32_bf16 a[212:215],a[184:187],v[184:187],a[212:215]
buffer_store_dwordx4 v[216:219],v6,s[36:39],s86 offen offset:2048 sc1
v_mfma_f32_16x16x32_bf16 a[212:215],a[180:183],v[172:175],a[212:215]
ds_read_b64_tr_b16 a[158:159],v2 offset:35936
ds_read_b64_tr_b16 a[160:161],v1 offset:128
v_mfma_f32_16x16x32_bf16 a[212:215],a[176:179],v[168:171],a[212:215]
)A"
R"A(ds_read_b64_tr_b16 a[162:163],v1 offset:5248
v_mfma_f32_16x16x32_bf16 a[216:219],a[176:179],v[200:203],a[216:219]
ds_read_b64_tr_b16 a[164:165],v1 offset:10368
v_mfma_f32_16x16x32_bf16 a[216:219],a[180:183],v[204:207],a[216:219]
buffer_load_dwordx4 v7,s[72:75],s80 offen lds
v_mfma_f32_16x16x32_bf16 a[216:219],a[184:187],v[216:219],a[216:219]
ds_read_b64_tr_b16 a[166:167],v1 offset:15488
ds_read_b64_tr_b16 a[168:169],v1 offset:20608
v_mfma_f32_16x16x32_bf16 a[216:219],a[188:191],v[220:223],a[216:219]
ds_read_b64_tr_b16 a[170:171],v1 offset:25728
v_mfma_f32_16x16x32_bf16 a[220:223],a[188:191],v[252:255],a[220:223]
ds_read_b64_tr_b16 a[172:173],v1 offset:30848
v_mfma_f32_16x16x32_bf16 a[220:223],a[184:187],v[248:251],a[220:223]
buffer_load_dwordx4 v[176:179],v6,s[28:31],s78 offen offset:1024 sc1
v_mfma_f32_16x16x32_bf16 a[220:223],a[180:183],v[236:239],a[220:223]
ds_read_b64_tr_b16 a[174:175],v1 offset:35968
ds_read_b64_tr_b16 a[180:181],v2 offset:10400
v_mfma_f32_16x16x32_bf16 a[220:223],a[176:179],v[232:235],a[220:223]
s_waitcnt lgkmcnt(7)
ds_read_b64_tr_b16 a[176:177],v2 offset:160
v_mfma_f32_16x16x32_bf16 a[224:227],a[128:131],v[136:139],a[224:227]
ds_read_b64_tr_b16 a[178:179],v2 offset:5280
v_mfma_f32_16x16x32_bf16 a[224:227],a[132:135],v[140:143],a[224:227]
ds_read_b64_tr_b16 a[182:183],v2 offset:15520
v_mfma_f32_16x16x32_bf16 a[224:227],a[136:139],v[152:155],a[224:227]
buffer_load_dwordx4 v[208:211],v6,s[28:31],s78 offen offset:2048 sc1
v_mfma_f32_16x16x32_bf16 a[224:227],a[140:143],v[156:159],a[224:227]
ds_read_b64_tr_b16 a[184:185],v2 offset:20640
ds_read_b64_tr_b16 a[186:187],v2 offset:25760
v_mfma_f32_16x16x32_bf16 a[228:231],a[140:143],v[188:191],a[228:231]
ds_read_b64_tr_b16 a[188:189],v2 offset:30880
v_mfma_f32_16x16x32_bf16 a[228:231],a[136:139],v[184:187],a[228:231]
ds_read_b64_tr_b16 a[190:191],v2 offset:36000
v_mfma_f32_16x16x32_bf16 a[228:231],a[132:135],v[172:175],a[228:231]
buffer_load_dwordx4 v8,s[72:75],s80 offen offset:1024 lds
v_mfma_f32_16x16x32_bf16 a[228:231],a[128:131],v[168:171],a[228:231]
v_mfma_f32_16x16x32_bf16 a[232:235],a[128:131],v[200:203],a[232:235]
v_mfma_f32_16x16x32_bf16 a[232:235],a[132:135],v[204:207],a[232:235]
v_mfma_f32_16x16x32_bf16 a[232:235],a[136:139],v[216:219],a[232:235]
buffer_store_dwordx4 v[220:223],v6,s[36:39],s86 offen offset:3072 sc1
v_mfma_f32_16x16x32_bf16 a[232:235],a[140:143],v[220:223],a[232:235]
v_mfma_f32_16x16x32_bf16 a[236:239],a[140:143],v[252:255],a[236:239]
ds_read_b64_tr_b16 a[140:141],v1 offset:30912
ds_read_b64_tr_b16 a[142:143],v1 offset:36032
v_mfma_f32_16x16x32_bf16 a[236:239],a[136:139],v[248:251],a[236:239]
ds_read_b64_tr_b16 a[136:137],v1 offset:20672
v_mfma_f32_16x16x32_bf16 a[236:239],a[132:135],v[236:239],a[236:239]
buffer_load_dwordx4 v[240:243],v6,s[28:31],s78 offen offset:3072 sc1
v_mfma_f32_16x16x32_bf16 a[236:239],a[128:131],v[232:235],a[236:239]
ds_read_b64_tr_b16 a[128:129],v1 offset:192
ds_read_b64_tr_b16 a[130:131],v1 offset:5312
v_mfma_f32_16x16x32_bf16 a[240:243],a[144:147],v[136:139],a[240:243]
ds_read_b64_tr_b16 a[132:133],v1 offset:10432
v_mfma_f32_16x16x32_bf16 a[240:243],a[148:151],v[140:143],a[240:243]
ds_read_b64_tr_b16 a[134:135],v1 offset:15552
v_mfma_f32_16x16x32_bf16 a[240:243],a[152:155],v[152:155],a[240:243]
ds_read_b64_tr_b16 a[138:139],v1 offset:25792
v_mfma_f32_16x16x32_bf16 a[240:243],a[156:159],v[156:159],a[240:243]
buffer_load_dwordx4 v9,s[72:75],s80 offen offset:2048 lds
v_mfma_f32_16x16x32_bf16 a[244:247],a[156:159],v[188:191],a[244:247]
v_mfma_f32_16x16x32_bf16 a[244:247],a[152:155],v[184:187],a[244:247]
v_mfma_f32_16x16x32_bf16 a[244:247],a[148:151],v[172:175],a[244:247]
v_mfma_f32_16x16x32_bf16 a[244:247],a[144:147],v[168:171],a[244:247]
buffer_load_dwordx4 v[148:151],v6,s[28:31],s79 offen sc1
v_mfma_f32_16x16x32_bf16 a[248:251],a[144:147],v[200:203],a[248:251]
v_mfma_f32_16x16x32_bf16 a[248:251],a[148:151],v[204:207],a[248:251]
v_mfma_f32_16x16x32_bf16 a[248:251],a[152:155],v[216:219],a[248:251]
v_mfma_f32_16x16x32_bf16 a[248:251],a[156:159],v[220:223],a[248:251]
v_mfma_f32_16x16x32_bf16 a[252:255],a[156:159],v[252:255],a[252:255]
ds_read_b64_tr_b16 a[156:157],v2 offset:30944
ds_read_b64_tr_b16 a[158:159],v2 offset:36064
v_mfma_f32_16x16x32_bf16 a[252:255],a[152:155],v[248:251],a[252:255]
buffer_store_dwordx4 v[232:235],v6,s[36:39],s87 offen sc1
v_mfma_f32_16x16x32_bf16 a[252:255],a[148:151],v[236:239],a[252:255]
ds_read_b64_tr_b16 a[148:149],v2 offset:10464
ds_read_b64_tr_b16 a[150:151],v2 offset:15584
v_mfma_f32_16x16x32_bf16 a[252:255],a[144:147],v[232:235],a[252:255]
ds_read_b64_tr_b16 a[144:145],v2 offset:224
v_mfma_f32_16x16x32_bf16 a[0:3],a[160:163],v[136:139],a[0:3]
s_waitcnt lgkmcnt(5)
ds_read_b64_tr_b16 a[146:147],v2 offset:5344
v_mfma_f32_16x16x32_bf16 a[0:3],a[164:167],v[140:143],a[0:3]
buffer_load_dwordx4 v[180:183],v6,s[28:31],s79 offen offset:1024 sc1
v_mfma_f32_16x16x32_bf16 a[0:3],a[168:171],v[152:155],a[0:3]
ds_read_b64_tr_b16 a[152:153],v2 offset:20704
ds_read_b64_tr_b16 a[154:155],v2 offset:25824
v_mfma_f32_16x16x32_bf16 a[0:3],a[172:175],v[156:159],a[0:3]
v_mfma_f32_16x16x32_bf16 a[4:7],a[172:175],v[188:191],a[4:7]
v_mfma_f32_16x16x32_bf16 a[4:7],a[168:171],v[184:187],a[4:7]
buffer_load_dwordx4 v10,s[72:75],s80 offen offset:3072 lds
v_mfma_f32_16x16x32_bf16 a[4:7],a[164:167],v[172:175],a[4:7]
v_mfma_f32_16x16x32_bf16 a[4:7],a[160:163],v[168:171],a[4:7]
v_mfma_f32_16x16x32_bf16 a[8:11],a[160:163],v[200:203],a[8:11]
v_mfma_f32_16x16x32_bf16 a[8:11],a[164:167],v[204:207],a[8:11]
v_mfma_f32_16x16x32_bf16 a[8:11],a[168:171],v[216:219],a[8:11]
buffer_load_dwordx4 v[212:215],v6,s[28:31],s79 offen offset:2048 sc1
v_mfma_f32_16x16x32_bf16 a[8:11],a[172:175],v[220:223],a[8:11]
v_mfma_f32_16x16x32_bf16 a[12:15],a[172:175],v[252:255],a[12:15]
ds_read_b64_tr_b16 a[172:173],v1 offset:30976
ds_read_b64_tr_b16 a[174:175],v1 offset:36096
v_mfma_f32_16x16x32_bf16 a[12:15],a[168:171],v[248:251],a[12:15]
ds_read_b64_tr_b16 a[168:169],v1 offset:20736
v_mfma_f32_16x16x32_bf16 a[12:15],a[164:167],v[236:239],a[12:15]
s_addk_i32 m0,4096
ds_read_b64_tr_b16 a[164:165],v1 offset:10496
v_mfma_f32_16x16x32_bf16 a[12:15],a[160:163],v[232:235],a[12:15]
ds_read_b64_tr_b16 a[160:161],v1 offset:256
v_mfma_f32_16x16x32_bf16 a[16:19],a[176:179],v[136:139],a[16:19]
ds_read_b64_tr_b16 a[162:163],v1 offset:5376
ds_read_b64_tr_b16 a[166:167],v1 offset:15616
v_mfma_f32_16x16x32_bf16 a[16:19],a[180:183],v[140:143],a[16:19]
buffer_store_dwordx4 v[236:239],v6,s[36:39],s87 offen offset:1024 sc1
v_mfma_f32_16x16x32_bf16 a[16:19],a[184:187],v[152:155],a[16:19]
ds_read_b64_tr_b16 a[170:171],v1 offset:25856
v_add_u32 v1,40960,v1
v_mfma_f32_16x16x32_bf16 a[16:19],a[188:191],v[156:159],a[16:19]
ds_read_b64_tr_b16 a[96:97],v1 offset:128
ds_read_b64_tr_b16 a[98:99],v1 offset:5248
v_mfma_f32_16x16x32_bf16 a[20:23],a[188:191],v[188:191],a[20:23]
ds_read_b64_tr_b16 a[100:101],v1 offset:10368
v_mfma_f32_16x16x32_bf16 a[20:23],a[184:187],v[184:187],a[20:23]
buffer_load_dwordx4 v11,s[72:75],s80 offen lds
v_mfma_f32_16x16x32_bf16 a[20:23],a[180:183],v[172:175],a[20:23]
ds_read_b64_tr_b16 a[102:103],v1 offset:15488
ds_read_b64_tr_b16 a[104:105],v1 offset:20608
v_mfma_f32_16x16x32_bf16 a[20:23],a[176:179],v[168:171],a[20:23]
ds_read_b64_tr_b16 a[106:107],v1 offset:25728
v_mfma_f32_16x16x32_bf16 a[24:27],a[176:179],v[200:203],a[24:27]
ds_read_b64_tr_b16 a[108:109],v1 offset:30848
v_mfma_f32_16x16x32_bf16 a[24:27],a[180:183],v[204:207],a[24:27]
buffer_load_dwordx4 v[244:247],v6,s[28:31],s79 offen offset:3072 sc1
v_mfma_f32_16x16x32_bf16 a[24:27],a[184:187],v[216:219],a[24:27]
ds_read_b64_tr_b16 a[110:111],v1 offset:35968
v_mfma_f32_16x16x32_bf16 a[24:27],a[188:191],v[220:223],a[24:27]
v_mfma_f32_16x16x32_bf16 a[28:31],a[188:191],v[252:255],a[28:31]
ds_read_b64_tr_b16 a[188:189],v2 offset:31008
ds_read_b64_tr_b16 a[190:191],v2 offset:36128
v_mfma_f32_16x16x32_bf16 a[28:31],a[184:187],v[248:251],a[28:31]
s_addk_i32 m0,1024
ds_read_b64_tr_b16 a[184:185],v2 offset:20768
v_mfma_f32_16x16x32_bf16 a[28:31],a[180:183],v[236:239],a[28:31]
ds_read_b64_tr_b16 a[180:181],v2 offset:10528
v_mfma_f32_16x16x32_bf16 a[28:31],a[176:179],v[232:235],a[28:31]
ds_read_b64_tr_b16 a[176:177],v2 offset:288
v_mfma_f32_16x16x32_bf16 a[32:35],a[128:131],v[136:139],a[32:35]
buffer_load_dwordx4 v7,s[72:75],s83 offen lds
v_mfma_f32_16x16x32_bf16 a[32:35],a[132:135],v[140:143],a[32:35]
ds_read_b64_tr_b16 a[178:179],v2 offset:5408
ds_read_b64_tr_b16 a[182:183],v2 offset:15648
v_mfma_f32_16x16x32_bf16 a[32:35],a[136:139],v[152:155],a[32:35]
ds_read_b64_tr_b16 a[186:187],v2 offset:25888
v_add_u32 v2,40960,v2
v_mfma_f32_16x16x32_bf16 a[32:35],a[140:143],v[156:159],a[32:35]
ds_read_b64_tr_b16 a[112:113],v2 offset:160
v_mfma_f32_16x16x32_bf16 a[36:39],a[140:143],v[188:191],a[36:39]
ds_read_b64_tr_b16 a[114:115],v2 offset:5280
v_mfma_f32_16x16x32_bf16 a[36:39],a[136:139],v[184:187],a[36:39]
buffer_store_dwordx4 v[248:251],v6,s[36:39],s87 offen offset:2048 sc1
v_mfma_f32_16x16x32_bf16 a[36:39],a[132:135],v[172:175],a[36:39]
ds_read_b64_tr_b16 a[116:117],v2 offset:10400
ds_read_b64_tr_b16 a[118:119],v2 offset:15520
v_mfma_f32_16x16x32_bf16 a[36:39],a[128:131],v[168:171],a[36:39]
ds_read_b64_tr_b16 a[120:121],v2 offset:20640
v_mfma_f32_16x16x32_bf16 a[40:43],a[128:131],v[200:203],a[40:43]
ds_read_b64_tr_b16 a[122:123],v2 offset:25760
v_mfma_f32_16x16x32_bf16 a[40:43],a[132:135],v[204:207],a[40:43]
ds_read_b64_tr_b16 a[124:125],v2 offset:30880
v_mfma_f32_16x16x32_bf16 a[40:43],a[136:139],v[216:219],a[40:43]
ds_read_b64_tr_b16 a[126:127],v2 offset:36000
v_mfma_f32_16x16x32_bf16 a[40:43],a[140:143],v[220:223],a[40:43]
v_mfma_f32_16x16x32_bf16 a[44:47],a[140:143],v[252:255],a[44:47]
ds_read_b64_tr_b16 a[140:141],v1 offset:30720
ds_read_b64_tr_b16 a[142:143],v1 offset:35840
v_mfma_f32_16x16x32_bf16 a[44:47],a[136:139],v[248:251],a[44:47]
ds_read_b64_tr_b16 a[136:137],v1 offset:20480
v_mfma_f32_16x16x32_bf16 a[44:47],a[132:135],v[236:239],a[44:47]
ds_read_b64_tr_b16 a[132:133],v1 offset:10240
v_mfma_f32_16x16x32_bf16 a[44:47],a[128:131],v[232:235],a[44:47]
s_waitcnt lgkmcnt(7)
buffer_load_dwordx4 v8,s[72:75],s83 offen offset:1024 lds
v_mfma_f32_16x16x32_bf16 a[48:51],a[144:147],v[136:139],a[48:51]
ds_read_b64_tr_b16 a[128:129],v1
ds_read_b64_tr_b16 a[130:131],v1 offset:5120
v_mfma_f32_16x16x32_bf16 a[48:51],a[148:151],v[140:143],a[48:51]
ds_read_b64_tr_b16 a[134:135],v1 offset:15360
v_mfma_f32_16x16x32_bf16 a[48:51],a[152:155],v[152:155],a[48:51]
ds_read_b64_tr_b16 a[138:139],v1 offset:25600
v_mfma_f32_16x16x32_bf16 a[48:51],a[156:159],v[156:159],a[48:51]
s_cmp_ge_u32 1,s71
s_cselect_b32 s76,4,2
v_mfma_f32_16x16x32_bf16 a[52:55],a[156:159],v[188:191],a[52:55]
s_bitcmp1_b32 s54,s76
s_cselect_b32 s77,8192,0
s_lshl_b32 s76,s76,17
v_mfma_f32_16x16x32_bf16 a[52:55],a[152:155],v[184:187],a[52:55]
s_add_u32 s76,s76,s77
s_add_u32 s77,s76,4096
s_add_u32 s78,s76,16384
v_mfma_f32_16x16x32_bf16 a[52:55],a[148:151],v[172:175],a[52:55]
s_add_u32 s79,s76,20480
s_max_i32 s76,s76,s18
s_max_i32 s77,s77,s19
v_mfma_f32_16x16x32_bf16 a[52:55],a[144:147],v[168:171],a[52:55]
s_max_i32 s78,s78,s100
s_max_i32 s79,s79,s101
v_mfma_f32_16x16x32_bf16 a[56:59],a[144:147],v[200:203],a[56:59]
v_mfma_f32_16x16x32_bf16 a[56:59],a[148:151],v[204:207],a[56:59]
buffer_store_dwordx4 v[252:255],v6,s[36:39],s87 offen offset:3072 sc1
v_mfma_f32_16x16x32_bf16 a[56:59],a[152:155],v[216:219],a[56:59]
v_mfma_f32_16x16x32_bf16 a[56:59],a[156:159],v[220:223],a[56:59]
v_mfma_f32_16x16x32_bf16 a[60:63],a[156:159],v[252:255],a[60:63]
ds_read_b64_tr_b16 a[156:157],v2 offset:30752
ds_read_b64_tr_b16 a[158:159],v2 offset:35872
v_mfma_f32_16x16x32_bf16 a[60:63],a[152:155],v[248:251],a[60:63]
ds_read_b64_tr_b16 a[152:153],v2 offset:20512
v_mfma_f32_16x16x32_bf16 a[60:63],a[148:151],v[236:239],a[60:63]
buffer_load_dwordx4 v9,s[72:75],s83 offen offset:2048 lds
v_mfma_f32_16x16x32_bf16 a[60:63],a[144:147],v[232:235],a[60:63]
ds_read_b64_tr_b16 a[144:145],v2 offset:32
ds_read_b64_tr_b16 a[146:147],v2 offset:5152
)A"
R"A(v_mfma_f32_16x16x32_bf16 a[64:67],a[160:163],v[136:139],a[64:67]
ds_read_b64_tr_b16 a[148:149],v2 offset:10272
v_mfma_f32_16x16x32_bf16 a[64:67],a[164:167],v[140:143],a[64:67]
ds_read_b64_tr_b16 a[150:151],v2 offset:15392
v_mfma_f32_16x16x32_bf16 a[64:67],a[168:171],v[152:155],a[64:67]
ds_read_b64_tr_b16 a[154:155],v2 offset:25632
v_mfma_f32_16x16x32_bf16 a[64:67],a[172:175],v[156:159],a[64:67]
v_mfma_f32_16x16x32_bf16 a[68:71],a[172:175],v[188:191],a[68:71]
v_mfma_f32_16x16x32_bf16 a[68:71],a[168:171],v[184:187],a[68:71]
v_mfma_f32_16x16x32_bf16 a[68:71],a[164:167],v[172:175],a[68:71]
v_mfma_f32_16x16x32_bf16 a[68:71],a[160:163],v[168:171],a[68:71]
v_mfma_f32_16x16x32_bf16 a[72:75],a[160:163],v[200:203],a[72:75]
v_mfma_f32_16x16x32_bf16 a[72:75],a[164:167],v[204:207],a[72:75]
v_mfma_f32_16x16x32_bf16 a[72:75],a[168:171],v[216:219],a[72:75]
v_mfma_f32_16x16x32_bf16 a[72:75],a[172:175],v[220:223],a[72:75]
buffer_load_dwordx4 v[64:67],v6,s[28:31],s76 offen sc1
v_mfma_f32_16x16x32_bf16 a[76:79],a[172:175],v[252:255],a[76:79]
ds_read_b64_tr_b16 a[172:173],v1 offset:30784
ds_read_b64_tr_b16 a[174:175],v1 offset:35904
v_mfma_f32_16x16x32_bf16 a[76:79],a[168:171],v[248:251],a[76:79]
ds_read_b64_tr_b16 a[168:169],v1 offset:20544
v_mfma_f32_16x16x32_bf16 a[76:79],a[164:167],v[236:239],a[76:79]
ds_read_b64_tr_b16 a[164:165],v1 offset:10304
v_mfma_f32_16x16x32_bf16 a[76:79],a[160:163],v[232:235],a[76:79]
buffer_load_dwordx4 v10,s[72:75],s83 offen offset:3072 lds
v_mfma_f32_16x16x32_bf16 a[80:83],a[176:179],v[136:139],a[80:83]
ds_read_b64_tr_b16 a[160:161],v1 offset:64
ds_read_b64_tr_b16 a[162:163],v1 offset:5184
v_mfma_f32_16x16x32_bf16 a[80:83],a[180:183],v[140:143],a[80:83]
ds_read_b64_tr_b16 a[166:167],v1 offset:15424
v_mfma_f32_16x16x32_bf16 a[80:83],a[184:187],v[152:155],a[80:83]
ds_read_b64_tr_b16 a[170:171],v1 offset:25664
v_mfma_f32_16x16x32_bf16 a[80:83],a[188:191],v[156:159],a[80:83]
buffer_load_dwordx4 v[68:71],v6,s[28:31],s76 offen offset:1024 sc1
v_mfma_f32_16x16x32_bf16 a[84:87],a[188:191],v[188:191],a[84:87]
v_mfma_f32_16x16x32_bf16 a[84:87],a[184:187],v[184:187],a[84:87]
v_mfma_f32_16x16x32_bf16 a[84:87],a[180:183],v[172:175],a[84:87]
v_mfma_f32_16x16x32_bf16 a[84:87],a[176:179],v[168:171],a[84:87]
v_mfma_f32_16x16x32_bf16 a[88:91],a[176:179],v[200:203],a[88:91]
v_mfma_f32_16x16x32_bf16 a[88:91],a[180:183],v[204:207],a[88:91]
s_addk_i32 m0,4096
v_mfma_f32_16x16x32_bf16 a[88:91],a[184:187],v[216:219],a[88:91]
v_mfma_f32_16x16x32_bf16 a[88:91],a[188:191],v[220:223],a[88:91]
buffer_load_dwordx4 v[72:75],v6,s[28:31],s76 offen offset:2048 sc1
v_mfma_f32_16x16x32_bf16 a[92:95],a[188:191],v[252:255],a[92:95]
ds_read_b64_tr_b16 a[188:189],v2 offset:30816
ds_read_b64_tr_b16 a[190:191],v2 offset:35936
v_mfma_f32_16x16x32_bf16 a[92:95],a[184:187],v[248:251],a[92:95]
ds_read_b64_tr_b16 a[184:185],v2 offset:20576
v_mfma_f32_16x16x32_bf16 a[92:95],a[180:183],v[236:239],a[92:95]
s_cmp_ge_u32 0,s71
s_cselect_b32 s80,3,1
s_mul_i32 s80,s80,163840
v_mfma_f32_16x16x32_bf16 a[92:95],a[176:179],v[232:235],a[92:95]
s_add_u32 s80,s80,s81
buffer_load_dwordx4 v11,s[72:75],s83 offen lds
s_waitcnt vmcnt(10) lgkmcnt(3)
v_mfma_f32_16x16x32_bf16 a[192:195],a[128:131],v[128:131],a[192:195]
s_add_u32 s83,s80,20480
ds_read_b64_tr_b16 a[176:177],v2 offset:96
ds_read_b64_tr_b16 a[178:179],v2 offset:5216
v_mfma_f32_16x16x32_bf16 a[192:195],a[132:135],v[160:163],a[192:195]
ds_read_b64_tr_b16 a[180:181],v2 offset:10336
v_mfma_f32_16x16x32_bf16 a[192:195],a[136:139],v[192:195],a[192:195]
ds_read_b64_tr_b16 a[182:183],v2 offset:15456
v_mfma_f32_16x16x32_bf16 a[192:195],a[140:143],v[224:227],a[192:195]
buffer_load_dwordx4 v[76:79],v6,s[28:31],s76 offen offset:3072 sc1
v_mfma_f32_16x16x32_bf16 a[196:199],a[140:143],v[228:231],a[196:199]
ds_read_b64_tr_b16 a[186:187],v2 offset:25696
v_mfma_f32_16x16x32_bf16 a[196:199],a[136:139],v[196:199],a[196:199]
v_mfma_f32_16x16x32_bf16 a[196:199],a[132:135],v[164:167],a[196:199]
v_mfma_f32_16x16x32_bf16 a[196:199],a[128:131],v[132:135],a[196:199]
v_mfma_f32_16x16x32_bf16 a[200:203],a[128:131],v[144:147],a[200:203]
v_mfma_f32_16x16x32_bf16 a[200:203],a[132:135],v[176:179],a[200:203]
v_mfma_f32_16x16x32_bf16 a[200:203],a[136:139],v[208:211],a[200:203]
v_mfma_f32_16x16x32_bf16 a[200:203],a[140:143],v[240:243],a[200:203]
s_lshl_b32 s95,s42,2
buffer_load_dwordx4 v[80:83],v6,s[28:31],s77 offen sc1
v_mfma_f32_16x16x32_bf16 a[204:207],a[140:143],v[244:247],a[204:207]
s_add_u32 s95,s95,s40
s_lshl_b32 s95,s95,4
v_bfe_u32 v19,s55,13,1
v_mfma_f32_16x16x32_bf16 a[204:207],a[136:139],v[212:215],a[204:207]
v_mov_b32 v18,s68
v_or_b32 v19,s69,v19
v_mfma_f32_16x16x32_bf16 a[204:207],a[132:135],v[180:183],a[204:207]
ds_read_b64_tr_b16 a[132:133],v1 offset:10432
ds_read_b64_tr_b16 a[134:135],v1 offset:15552
v_mfma_f32_16x16x32_bf16 a[204:207],a[128:131],v[148:151],a[204:207]
s_barrier
ds_read_b64_tr_b16 a[128:129],v1 offset:192
ds_read_b64_tr_b16 a[130:131],v1 offset:5312
v_mfma_f32_16x16x32_bf16 a[208:211],a[144:147],v[128:131],a[208:211]
ds_read_b64_tr_b16 a[136:137],v1 offset:20672
v_mfma_f32_16x16x32_bf16 a[208:211],a[148:151],v[160:163],a[208:211]
ds_read_b64_tr_b16 a[138:139],v1 offset:25792
v_mfma_f32_16x16x32_bf16 a[208:211],a[152:155],v[192:195],a[208:211]
s_add_u32 m0,s82,0
s_waitcnt vmcnt(8)
s_mov_b64 exec,1
buffer_store_dwordx2 v[18:19],v6,s[64:67],s95 offen offset:1024 sc1
s_mov_b64 exec,-1
v_mfma_f32_16x16x32_bf16 a[208:211],a[156:159],v[224:227],a[208:211]
ds_read_b64_tr_b16 a[140:141],v1 offset:30912
ds_read_b64_tr_b16 a[142:143],v1 offset:36032
v_mfma_f32_16x16x32_bf16 a[212:215],a[156:159],v[228:231],a[212:215]
v_mfma_f32_16x16x32_bf16 a[212:215],a[152:155],v[196:199],a[212:215]
v_mfma_f32_16x16x32_bf16 a[212:215],a[148:151],v[164:167],a[212:215]
buffer_load_dwordx4 v[84:87],v6,s[28:31],s77 offen offset:1024 sc1
v_mfma_f32_16x16x32_bf16 a[212:215],a[144:147],v[132:135],a[212:215]
v_mfma_f32_16x16x32_bf16 a[216:219],a[144:147],v[144:147],a[216:219]
v_mfma_f32_16x16x32_bf16 a[216:219],a[148:151],v[176:179],a[216:219]
v_mfma_f32_16x16x32_bf16 a[216:219],a[152:155],v[208:211],a[216:219]
buffer_load_dwordx4 v7,s[72:75],s80 offen lds
v_mfma_f32_16x16x32_bf16 a[216:219],a[156:159],v[240:243],a[216:219]
v_mfma_f32_16x16x32_bf16 a[220:223],a[156:159],v[244:247],a[220:223]
ds_read_b64_tr_b16 a[156:157],v2 offset:30944
ds_read_b64_tr_b16 a[158:159],v2 offset:36064
v_mfma_f32_16x16x32_bf16 a[220:223],a[152:155],v[212:215],a[220:223]
ds_read_b64_tr_b16 a[152:153],v2 offset:20704
v_mfma_f32_16x16x32_bf16 a[220:223],a[148:151],v[180:183],a[220:223]
buffer_load_dwordx4 v[88:91],v6,s[28:31],s77 offen offset:2048 sc1
v_mfma_f32_16x16x32_bf16 a[220:223],a[144:147],v[148:151],a[220:223]
ds_read_b64_tr_b16 a[144:145],v2 offset:224
ds_read_b64_tr_b16 a[146:147],v2 offset:5344
v_mfma_f32_16x16x32_bf16 a[224:227],a[160:163],v[128:131],a[224:227]
ds_read_b64_tr_b16 a[148:149],v2 offset:10464
v_mfma_f32_16x16x32_bf16 a[224:227],a[164:167],v[160:163],a[224:227]
ds_read_b64_tr_b16 a[150:151],v2 offset:15584
v_mfma_f32_16x16x32_bf16 a[224:227],a[168:171],v[192:195],a[224:227]
buffer_load_dwordx4 v[92:95],v6,s[28:31],s77 offen offset:3072 sc1
v_mfma_f32_16x16x32_bf16 a[224:227],a[172:175],v[224:227],a[224:227]
ds_read_b64_tr_b16 a[154:155],v2 offset:25824
v_mfma_f32_16x16x32_bf16 a[228:231],a[172:175],v[228:231],a[228:231]
v_mfma_f32_16x16x32_bf16 a[228:231],a[168:171],v[196:199],a[228:231]
v_mfma_f32_16x16x32_bf16 a[228:231],a[164:167],v[164:167],a[228:231]
buffer_load_dwordx4 v8,s[72:75],s80 offen offset:1024 lds
v_mfma_f32_16x16x32_bf16 a[228:231],a[160:163],v[132:135],a[228:231]
v_mfma_f32_16x16x32_bf16 a[232:235],a[160:163],v[144:147],a[232:235]
v_mfma_f32_16x16x32_bf16 a[232:235],a[164:167],v[176:179],a[232:235]
v_mfma_f32_16x16x32_bf16 a[232:235],a[168:171],v[208:211],a[232:235]
buffer_load_dwordx4 v[96:99],v6,s[28:31],s78 offen sc1
v_mfma_f32_16x16x32_bf16 a[232:235],a[172:175],v[240:243],a[232:235]
v_mfma_f32_16x16x32_bf16 a[236:239],a[172:175],v[244:247],a[236:239]
ds_read_b64_tr_b16 a[172:173],v1 offset:30976
ds_read_b64_tr_b16 a[174:175],v1 offset:36096
v_mfma_f32_16x16x32_bf16 a[236:239],a[168:171],v[212:215],a[236:239]
ds_read_b64_tr_b16 a[168:169],v1 offset:20736
v_mfma_f32_16x16x32_bf16 a[236:239],a[164:167],v[180:183],a[236:239]
ds_read_b64_tr_b16 a[164:165],v1 offset:10496
v_mfma_f32_16x16x32_bf16 a[236:239],a[160:163],v[148:151],a[236:239]
s_waitcnt lgkmcnt(4)
ds_read_b64_tr_b16 a[160:161],v1 offset:256
v_mfma_f32_16x16x32_bf16 a[240:243],a[176:179],v[128:131],a[240:243]
ds_read_b64_tr_b16 a[162:163],v1 offset:5376
ds_read_b64_tr_b16 a[166:167],v1 offset:15616
v_mfma_f32_16x16x32_bf16 a[240:243],a[180:183],v[160:163],a[240:243]
ds_read_b64_tr_b16 a[170:171],v1 offset:25856
v_add_u32 v1,81920,v1
v_mfma_f32_16x16x32_bf16 a[240:243],a[184:187],v[192:195],a[240:243]
buffer_load_dwordx4 v[100:103],v6,s[28:31],s78 offen offset:1024 sc1
v_mfma_f32_16x16x32_bf16 a[240:243],a[188:191],v[224:227],a[240:243]
v_mfma_f32_16x16x32_bf16 a[244:247],a[188:191],v[228:231],a[244:247]
v_mfma_f32_16x16x32_bf16 a[244:247],a[184:187],v[196:199],a[244:247]
v_mfma_f32_16x16x32_bf16 a[244:247],a[180:183],v[164:167],a[244:247]
buffer_load_dwordx4 v9,s[72:75],s80 offen offset:2048 lds
v_mfma_f32_16x16x32_bf16 a[244:247],a[176:179],v[132:135],a[244:247]
v_mfma_f32_16x16x32_bf16 a[248:251],a[176:179],v[144:147],a[248:251]
v_mfma_f32_16x16x32_bf16 a[248:251],a[180:183],v[176:179],a[248:251]
v_mfma_f32_16x16x32_bf16 a[248:251],a[184:187],v[208:211],a[248:251]
buffer_load_dwordx4 v[104:107],v6,s[28:31],s78 offen offset:2048 sc1
v_mfma_f32_16x16x32_bf16 a[248:251],a[188:191],v[240:243],a[248:251]
v_mfma_f32_16x16x32_bf16 a[252:255],a[188:191],v[244:247],a[252:255]
ds_read_b64_tr_b16 a[188:189],v2 offset:31008
ds_read_b64_tr_b16 a[190:191],v2 offset:36128
v_mfma_f32_16x16x32_bf16 a[252:255],a[184:187],v[212:215],a[252:255]
ds_read_b64_tr_b16 a[184:185],v2 offset:20768
v_mfma_f32_16x16x32_bf16 a[252:255],a[180:183],v[180:183],a[252:255]
ds_read_b64_tr_b16 a[180:181],v2 offset:10528
v_mfma_f32_16x16x32_bf16 a[252:255],a[176:179],v[148:151],a[252:255]
ds_read_b64_tr_b16 a[176:177],v2 offset:288
v_mfma_f32_16x16x32_bf16 a[0:3],a[96:99],v[128:131],a[0:3]
ds_read_b64_tr_b16 a[178:179],v2 offset:5408
ds_read_b64_tr_b16 a[182:183],v2 offset:15648
v_mfma_f32_16x16x32_bf16 a[0:3],a[100:103],v[160:163],a[0:3]
ds_read_b64_tr_b16 a[186:187],v2 offset:25888
v_add_u32 v2,81920,v2
v_mfma_f32_16x16x32_bf16 a[0:3],a[104:107],v[192:195],a[0:3]
buffer_load_dwordx4 v[108:111],v6,s[28:31],s78 offen offset:3072 sc1
v_mfma_f32_16x16x32_bf16 a[0:3],a[108:111],v[224:227],a[0:3]
v_mfma_f32_16x16x32_bf16 a[4:7],a[108:111],v[228:231],a[4:7]
v_mfma_f32_16x16x32_bf16 a[4:7],a[104:107],v[196:199],a[4:7]
v_mfma_f32_16x16x32_bf16 a[4:7],a[100:103],v[164:167],a[4:7]
buffer_load_dwordx4 v10,s[72:75],s80 offen offset:3072 lds
v_mfma_f32_16x16x32_bf16 a[4:7],a[96:99],v[132:135],a[4:7]
v_mfma_f32_16x16x32_bf16 a[8:11],a[96:99],v[144:147],a[8:11]
v_mfma_f32_16x16x32_bf16 a[8:11],a[100:103],v[176:179],a[8:11]
v_mfma_f32_16x16x32_bf16 a[8:11],a[104:107],v[208:211],a[8:11]
buffer_load_dwordx4 v[112:115],v6,s[28:31],s79 offen sc1
v_mfma_f32_16x16x32_bf16 a[8:11],a[108:111],v[240:243],a[8:11]
v_mfma_f32_16x16x32_bf16 a[12:15],a[108:111],v[244:247],a[12:15]
ds_read_b64_tr_b16 a[108:109],v1 offset:30720
ds_read_b64_tr_b16 a[110:111],v1 offset:35840
v_mfma_f32_16x16x32_bf16 a[12:15],a[104:107],v[212:215],a[12:15]
ds_read_b64_tr_b16 a[104:105],v1 offset:20480
v_mfma_f32_16x16x32_bf16 a[12:15],a[100:103],v[180:183],a[12:15]
s_addk_i32 m0,4096
ds_read_b64_tr_b16 a[100:101],v1 offset:10240
v_mfma_f32_16x16x32_bf16 a[12:15],a[96:99],v[148:151],a[12:15]
ds_read_b64_tr_b16 a[96:97],v1
v_mfma_f32_16x16x32_bf16 a[16:19],a[112:115],v[128:131],a[16:19]
ds_read_b64_tr_b16 a[98:99],v1 offset:5120
ds_read_b64_tr_b16 a[102:103],v1 offset:15360
v_mfma_f32_16x16x32_bf16 a[16:19],a[116:119],v[160:163],a[16:19]
buffer_load_dwordx4 v11,s[72:75],s80 offen lds
v_mfma_f32_16x16x32_bf16 a[16:19],a[120:123],v[192:195],a[16:19]
)A"
R"A(ds_read_b64_tr_b16 a[106:107],v1 offset:25600
v_mfma_f32_16x16x32_bf16 a[16:19],a[124:127],v[224:227],a[16:19]
v_mfma_f32_16x16x32_bf16 a[20:23],a[124:127],v[228:231],a[20:23]
v_mfma_f32_16x16x32_bf16 a[20:23],a[120:123],v[196:199],a[20:23]
buffer_load_dwordx4 v[116:119],v6,s[28:31],s79 offen offset:1024 sc1
v_mfma_f32_16x16x32_bf16 a[20:23],a[116:119],v[164:167],a[20:23]
v_mfma_f32_16x16x32_bf16 a[20:23],a[112:115],v[132:135],a[20:23]
v_mfma_f32_16x16x32_bf16 a[24:27],a[112:115],v[144:147],a[24:27]
v_mfma_f32_16x16x32_bf16 a[24:27],a[116:119],v[176:179],a[24:27]
v_mfma_f32_16x16x32_bf16 a[24:27],a[120:123],v[208:211],a[24:27]
buffer_load_dwordx4 v[120:123],v6,s[28:31],s79 offen offset:2048 sc1
v_mfma_f32_16x16x32_bf16 a[24:27],a[124:127],v[240:243],a[24:27]
v_mfma_f32_16x16x32_bf16 a[28:31],a[124:127],v[244:247],a[28:31]
ds_read_b64_tr_b16 a[124:125],v2 offset:30752
ds_read_b64_tr_b16 a[126:127],v2 offset:35872
v_mfma_f32_16x16x32_bf16 a[28:31],a[120:123],v[212:215],a[28:31]
s_addk_i32 m0,1024
ds_read_b64_tr_b16 a[120:121],v2 offset:20512
v_mfma_f32_16x16x32_bf16 a[28:31],a[116:119],v[180:183],a[28:31]
ds_read_b64_tr_b16 a[116:117],v2 offset:10272
v_mfma_f32_16x16x32_bf16 a[28:31],a[112:115],v[148:151],a[28:31]
s_cmp_ge_u32 2,s71
s_cselect_b32 s76,6,4
ds_read_b64_tr_b16 a[112:113],v2 offset:32
v_mfma_f32_16x16x32_bf16 a[32:35],a[128:131],v[128:131],a[32:35]
buffer_load_dwordx4 v7,s[72:75],s83 offen lds
v_mfma_f32_16x16x32_bf16 a[32:35],a[132:135],v[160:163],a[32:35]
s_bitcmp1_b32 s54,s76
s_cselect_b32 s77,8192,0
s_lshl_b32 s76,s76,17
v_mfma_f32_16x16x32_bf16 a[32:35],a[136:139],v[192:195],a[32:35]
s_add_u32 s76,s76,s77
s_add_u32 s77,s76,4096
s_add_u32 s78,s76,16384
v_mfma_f32_16x16x32_bf16 a[32:35],a[140:143],v[224:227],a[32:35]
s_max_i32 s77,s77,s19
s_max_i32 s78,s78,s100
ds_read_b64_tr_b16 a[114:115],v2 offset:5152
v_mfma_f32_16x16x32_bf16 a[36:39],a[140:143],v[228:231],a[36:39]
buffer_load_dwordx4 v[124:127],v6,s[28:31],s79 offen offset:3072 sc1
v_mfma_f32_16x16x32_bf16 a[36:39],a[136:139],v[196:199],a[36:39]
s_add_u32 s79,s76,20480
s_max_i32 s76,s76,s18
ds_read_b64_tr_b16 a[118:119],v2 offset:15392
v_mfma_f32_16x16x32_bf16 a[36:39],a[132:135],v[164:167],a[36:39]
s_max_i32 s79,s79,s101
ds_read_b64_tr_b16 a[122:123],v2 offset:25632
v_mfma_f32_16x16x32_bf16 a[36:39],a[128:131],v[132:135],a[36:39]
v_mfma_f32_16x16x32_bf16 a[40:43],a[128:131],v[144:147],a[40:43]
v_mfma_f32_16x16x32_bf16 a[40:43],a[132:135],v[176:179],a[40:43]
v_mfma_f32_16x16x32_bf16 a[40:43],a[136:139],v[208:211],a[40:43]
v_mfma_f32_16x16x32_bf16 a[40:43],a[140:143],v[240:243],a[40:43]
v_mfma_f32_16x16x32_bf16 a[44:47],a[140:143],v[244:247],a[44:47]
ds_read_b64_tr_b16 a[140:141],v1 offset:30784
ds_read_b64_tr_b16 a[142:143],v1 offset:35904
v_mfma_f32_16x16x32_bf16 a[44:47],a[136:139],v[212:215],a[44:47]
ds_read_b64_tr_b16 a[136:137],v1 offset:20544
v_mfma_f32_16x16x32_bf16 a[44:47],a[132:135],v[180:183],a[44:47]
ds_read_b64_tr_b16 a[132:133],v1 offset:10304
v_mfma_f32_16x16x32_bf16 a[44:47],a[128:131],v[148:151],a[44:47]
buffer_load_dwordx4 v8,s[72:75],s83 offen offset:1024 lds
v_mfma_f32_16x16x32_bf16 a[48:51],a[144:147],v[128:131],a[48:51]
ds_read_b64_tr_b16 a[128:129],v1 offset:64
ds_read_b64_tr_b16 a[130:131],v1 offset:5184
v_mfma_f32_16x16x32_bf16 a[48:51],a[148:151],v[160:163],a[48:51]
ds_read_b64_tr_b16 a[134:135],v1 offset:15424
v_mfma_f32_16x16x32_bf16 a[48:51],a[152:155],v[192:195],a[48:51]
ds_read_b64_tr_b16 a[138:139],v1 offset:25664
v_mfma_f32_16x16x32_bf16 a[48:51],a[156:159],v[224:227],a[48:51]
s_lshl_b32 s94,s70,4
v_bfe_u32 v24,v6,4,2
v_min_u32 v24,2,v24
v_mfma_f32_16x16x32_bf16 a[52:55],a[156:159],v[228:231],a[52:55]
s_add_u32 s94,s94,s40
v_add_u32 v24,s71,v24
v_add_u32 v24,1,v24
v_mfma_f32_16x16x32_bf16 a[52:55],a[152:155],v[196:199],a[52:55]
s_lshl_b32 s94,s94,4
v_and_b32 v24,3,v24
v_lshlrev_b32 v24,6,v24
v_mfma_f32_16x16x32_bf16 a[52:55],a[148:151],v[164:167],a[52:55]
v_mfma_f32_16x16x32_bf16 a[52:55],a[144:147],v[132:135],a[52:55]
buffer_load_dwordx4 v[136:139],v6,s[28:31],s76 offen sc1
v_mfma_f32_16x16x32_bf16 a[56:59],a[144:147],v[144:147],a[56:59]
v_mfma_f32_16x16x32_bf16 a[56:59],a[148:151],v[176:179],a[56:59]
v_mfma_f32_16x16x32_bf16 a[56:59],a[152:155],v[208:211],a[56:59]
v_mfma_f32_16x16x32_bf16 a[56:59],a[156:159],v[240:243],a[56:59]
v_mfma_f32_16x16x32_bf16 a[60:63],a[156:159],v[244:247],a[60:63]
ds_read_b64_tr_b16 a[156:157],v2 offset:30816
ds_read_b64_tr_b16 a[158:159],v2 offset:35936
v_mfma_f32_16x16x32_bf16 a[60:63],a[152:155],v[212:215],a[60:63]
buffer_load_dwordx4 v[140:143],v6,s[28:31],s76 offen offset:1024 sc1
v_mfma_f32_16x16x32_bf16 a[60:63],a[148:151],v[180:183],a[60:63]
ds_read_b64_tr_b16 a[148:149],v2 offset:10336
ds_read_b64_tr_b16 a[150:151],v2 offset:15456
v_mfma_f32_16x16x32_bf16 a[60:63],a[144:147],v[148:151],a[60:63]
s_waitcnt lgkmcnt(4)
ds_read_b64_tr_b16 a[144:145],v2 offset:96
v_mfma_f32_16x16x32_bf16 a[64:67],a[160:163],v[128:131],a[64:67]
ds_read_b64_tr_b16 a[146:147],v2 offset:5216
v_mfma_f32_16x16x32_bf16 a[64:67],a[164:167],v[160:163],a[64:67]
buffer_load_dwordx4 v9,s[72:75],s83 offen offset:2048 lds
v_mfma_f32_16x16x32_bf16 a[64:67],a[168:171],v[192:195],a[64:67]
ds_read_b64_tr_b16 a[152:153],v2 offset:20576
ds_read_b64_tr_b16 a[154:155],v2 offset:25696
v_mfma_f32_16x16x32_bf16 a[64:67],a[172:175],v[224:227],a[64:67]
v_mfma_f32_16x16x32_bf16 a[68:71],a[172:175],v[228:231],a[68:71]
v_mfma_f32_16x16x32_bf16 a[68:71],a[168:171],v[196:199],a[68:71]
buffer_load_dwordx4 v[152:155],v6,s[28:31],s76 offen offset:2048 sc1
v_mfma_f32_16x16x32_bf16 a[68:71],a[164:167],v[164:167],a[68:71]
v_mfma_f32_16x16x32_bf16 a[68:71],a[160:163],v[132:135],a[68:71]
v_mfma_f32_16x16x32_bf16 a[72:75],a[160:163],v[144:147],a[72:75]
v_mfma_f32_16x16x32_bf16 a[72:75],a[164:167],v[176:179],a[72:75]
buffer_load_dwordx4 v[156:159],v6,s[28:31],s76 offen offset:3072 sc1
v_mfma_f32_16x16x32_bf16 a[72:75],a[168:171],v[208:211],a[72:75]
v_mfma_f32_16x16x32_bf16 a[72:75],a[172:175],v[240:243],a[72:75]
v_mfma_f32_16x16x32_bf16 a[76:79],a[172:175],v[244:247],a[76:79]
ds_read_b64_tr_b16 a[172:173],v1 offset:30848
ds_read_b64_tr_b16 a[174:175],v1 offset:35968
v_mfma_f32_16x16x32_bf16 a[76:79],a[168:171],v[212:215],a[76:79]
buffer_load_dwordx4 v10,s[72:75],s83 offen offset:3072 lds
v_mfma_f32_16x16x32_bf16 a[76:79],a[164:167],v[180:183],a[76:79]
ds_read_b64_tr_b16 a[164:165],v1 offset:10368
ds_read_b64_tr_b16 a[166:167],v1 offset:15488
v_mfma_f32_16x16x32_bf16 a[76:79],a[160:163],v[148:151],a[76:79]
ds_read_b64_tr_b16 a[160:161],v1 offset:128
v_mfma_f32_16x16x32_bf16 a[80:83],a[176:179],v[128:131],a[80:83]
ds_read_b64_tr_b16 a[162:163],v1 offset:5248
v_mfma_f32_16x16x32_bf16 a[80:83],a[180:183],v[160:163],a[80:83]
buffer_load_dwordx4 v[168:171],v6,s[28:31],s77 offen sc1
v_mfma_f32_16x16x32_bf16 a[80:83],a[184:187],v[192:195],a[80:83]
ds_read_b64_tr_b16 a[168:169],v1 offset:20608
ds_read_b64_tr_b16 a[170:171],v1 offset:25728
v_mfma_f32_16x16x32_bf16 a[80:83],a[188:191],v[224:227],a[80:83]
v_mfma_f32_16x16x32_bf16 a[84:87],a[188:191],v[228:231],a[84:87]
v_mfma_f32_16x16x32_bf16 a[84:87],a[184:187],v[196:199],a[84:87]
buffer_load_dwordx4 v[172:175],v6,s[28:31],s77 offen offset:1024 sc1
v_mfma_f32_16x16x32_bf16 a[84:87],a[180:183],v[164:167],a[84:87]
v_mfma_f32_16x16x32_bf16 a[84:87],a[176:179],v[132:135],a[84:87]
v_mfma_f32_16x16x32_bf16 a[88:91],a[176:179],v[144:147],a[88:91]
v_mfma_f32_16x16x32_bf16 a[88:91],a[180:183],v[176:179],a[88:91]
s_addk_i32 m0,4096
v_mfma_f32_16x16x32_bf16 a[88:91],a[184:187],v[208:211],a[88:91]
v_mfma_f32_16x16x32_bf16 a[88:91],a[188:191],v[240:243],a[88:91]
buffer_load_dwordx2 v[4:5],v24,s[64:67],s94 offen offset:1024 sc1
v_mfma_f32_16x16x32_bf16 a[92:95],a[188:191],v[244:247],a[92:95]
ds_read_b64_tr_b16 a[188:189],v2 offset:30880
ds_read_b64_tr_b16 a[190:191],v2 offset:36000
v_mfma_f32_16x16x32_bf16 a[92:95],a[184:187],v[212:215],a[92:95]
ds_read_b64_tr_b16 a[184:185],v2 offset:20640
v_mfma_f32_16x16x32_bf16 a[92:95],a[180:183],v[180:183],a[92:95]
ds_read_b64_tr_b16 a[180:181],v2 offset:10400
v_mfma_f32_16x16x32_bf16 a[92:95],a[176:179],v[148:151],a[92:95]
s_waitcnt vmcnt(13)
buffer_load_dwordx4 v[184:187],v6,s[28:31],s77 offen offset:2048 sc1
v_mfma_f32_16x16x32_bf16 a[192:195],a[96:99],v[64:67],a[192:195]
s_lshr_b32 s44,s71,1
ds_read_b64_tr_b16 a[176:177],v2 offset:160
ds_read_b64_tr_b16 a[178:179],v2 offset:5280
v_mfma_f32_16x16x32_bf16 a[192:195],a[100:103],v[68:71],a[192:195]
s_lshl_b32 s43,s41,9
s_mul_i32 s44,s44,22
ds_read_b64_tr_b16 a[182:183],v2 offset:15520
v_mfma_f32_16x16x32_bf16 a[192:195],a[104:107],v[72:75],a[192:195]
s_add_u32 s43,s43,s44
s_add_u32 s43,s43,s71
ds_read_b64_tr_b16 a[186:187],v2 offset:25760
v_mfma_f32_16x16x32_bf16 a[192:195],a[108:111],v[76:79],a[192:195]
s_add_u32 s43,s43,s40
buffer_load_dwordx4 v11,s[72:75],s83 offen lds
v_mfma_f32_16x16x32_bf16 a[196:199],a[108:111],v[92:95],a[196:199]
s_add_u32 s43,s43,s40
s_add_u32 s43,s43,64
s_lshl_b32 s43,s43,14
v_mfma_f32_16x16x32_bf16 a[196:199],a[104:107],v[88:91],a[196:199]
s_mov_b64 s[32:33],s[12:13]
s_and_b32 s87,s55,8192
s_lshl_b32 s84,s70,21
v_mfma_f32_16x16x32_bf16 a[196:199],a[100:103],v[84:87],a[196:199]
s_add_u32 s87,s87,s84
s_add_u32 s32,s32,s43
s_addc_u32 s33,s33,0
v_mfma_f32_16x16x32_bf16 a[196:199],a[96:99],v[80:83],a[196:199]
s_add_u32 s84,s87,0
buffer_load_dwordx4 v[188:191],v6,s[28:31],s77 offen offset:3072 sc1
v_mfma_f32_16x16x32_bf16 a[200:203],a[96:99],v[96:99],a[200:203]
s_add_u32 s85,s87,131072
s_add_u32 s86,s87,262144
s_and_b32 s33,s33,0xffff
v_mfma_f32_16x16x32_bf16 a[200:203],a[100:103],v[100:103],a[200:203]
s_mov_b32 s34,6569984
s_mov_b32 s35,0x20000
s_max_i32 s84,s84,s18
v_mfma_f32_16x16x32_bf16 a[200:203],a[104:107],v[104:107],a[200:203]
s_max_i32 s85,s85,s19
s_max_i32 s86,s86,s100
v_bfe_u32 v18,v6,6,2
v_mfma_f32_16x16x32_bf16 a[200:203],a[108:111],v[108:111],a[200:203]
s_mul_i32 s44,s41,307200
buffer_load_dwordx4 v[200:203],v6,s[28:31],s78 offen sc1
s_waitcnt vmcnt(14)
v_mfma_f32_16x16x32_bf16 a[204:207],a[108:111],v[124:127],a[204:207]
s_mov_b64 s[20:21],s[16:17]
ds_read_b64_tr_b16 a[108:109],v1 offset:30912
ds_read_b64_tr_b16 a[110:111],v1 offset:36032
v_mfma_f32_16x16x32_bf16 a[204:207],a[104:107],v[120:123],a[204:207]
s_lshl_b32 s43,s40,2
ds_read_b64_tr_b16 a[104:105],v1 offset:20672
v_lshrrev_b32 v19,8,v6
v_mfma_f32_16x16x32_bf16 a[204:207],a[100:103],v[116:119],a[204:207]
s_add_u32 s43,s43,s70
ds_read_b64_tr_b16 a[100:101],v1 offset:10432
v_xor_b32 v18,v18,v19
v_mfma_f32_16x16x32_bf16 a[204:207],a[96:99],v[112:115],a[204:207]
s_barrier
buffer_load_dwordx4 v[204:207],v6,s[28:31],s78 offen offset:1024 sc1
v_mfma_f32_16x16x32_bf16 a[208:211],a[112:115],v[64:67],a[208:211]
s_mulk_i32 s43,20480
ds_read_b64_tr_b16 a[96:97],v1 offset:192
ds_read_b64_tr_b16 a[98:99],v1 offset:5312
v_mfma_f32_16x16x32_bf16 a[208:211],a[116:119],v[68:71],a[208:211]
s_mov_b32 s22,307200
ds_read_b64_tr_b16 a[102:103],v1 offset:15552
v_lshlrev_b32 v18,2,v18
v_mfma_f32_16x16x32_bf16 a[208:211],a[120:123],v[72:75],a[208:211]
s_mov_b32 s23,0x20000
ds_read_b64_tr_b16 a[106:107],v1 offset:25792
v_bfe_u32 v19,v6,4,2
v_mfma_f32_16x16x32_bf16 a[208:211],a[124:127],v[76:79],a[208:211]
buffer_load_dwordx4 v[216:219],v6,s[28:31],s78 offen offset:2048 sc1
v_mfma_f32_16x16x32_bf16 a[212:215],a[124:127],v[92:95],a[212:215]
v_or_b32 v18,v18,v19
v_and_b32 v19,16,v6
v_mfma_f32_16x16x32_bf16 a[212:215],a[120:123],v[88:91],a[212:215]
v_mov_b32 v34,0x3f80
v_lshlrev_b32 v19,v19,v34
v_mfma_f32_16x16x32_bf16 a[212:215],a[116:119],v[84:87],a[212:215]
v_lshrrev_b32 v18,1,v18
v_add_u32 v25,4096,v6
v_mfma_f32_16x16x32_bf16 a[212:215],a[112:115],v[80:83],a[212:215]
s_cmp_ge_u32 0,s71
s_cselect_b32 s76,3,1
v_mov_b32 v36,0
v_mfma_f32_16x16x32_bf16 a[216:219],a[112:115],v[96:99],a[216:219]
buffer_load_dwordx4 v[220:223],v6,s[28:31],s78 offen offset:3072 sc1
v_mfma_f32_16x16x32_bf16 a[216:219],a[116:119],v[100:103],a[216:219]
s_bitcmp1_b32 s54,s76
s_cselect_b32 s77,8192,0
s_lshl_b32 s76,s76,17
v_mfma_f32_16x16x32_bf16 a[216:219],a[120:123],v[104:107],a[216:219]
)A"
R"A(s_add_u32 s76,s76,s77
s_add_u32 s77,s76,4096
s_add_u32 s78,s76,16384
v_mfma_f32_16x16x32_bf16 a[216:219],a[124:127],v[108:111],a[216:219]
s_max_i32 s77,s77,s19
s_max_i32 s78,s78,s100
v_mov_b32 v37,0
v_mfma_f32_16x16x32_bf16 a[220:223],a[124:127],v[124:127],a[220:223]
ds_read_b64_tr_b16 a[124:125],v2 offset:30944
ds_read_b64_tr_b16 a[126:127],v2 offset:36064
v_mfma_f32_16x16x32_bf16 a[220:223],a[120:123],v[120:123],a[220:223]
ds_read_b64_tr_b16 a[120:121],v2 offset:20704
v_mov_b32 v32,0
v_mfma_f32_16x16x32_bf16 a[220:223],a[116:119],v[116:119],a[220:223]
buffer_load_dwordx4 v[232:235],v6,s[28:31],s79 offen sc1
v_mfma_f32_16x16x32_bf16 a[220:223],a[112:115],v[112:115],a[220:223]
ds_read_b64_tr_b16 a[112:113],v2 offset:224
ds_read_b64_tr_b16 a[114:115],v2 offset:5344
v_mfma_f32_16x16x32_bf16 a[224:227],a[128:131],v[64:67],a[224:227]
ds_read_b64_tr_b16 a[116:117],v2 offset:10464
v_mov_b32 v33,0
v_mfma_f32_16x16x32_bf16 a[224:227],a[132:135],v[68:71],a[224:227]
ds_read_b64_tr_b16 a[118:119],v2 offset:15584
v_bfe_u32 v40,v6,4,4
v_mfma_f32_16x16x32_bf16 a[224:227],a[136:139],v[72:75],a[224:227]
ds_read_b64_tr_b16 a[122:123],v2 offset:25824
v_mul_u32_u24 v40,1280,v40
v_mfma_f32_16x16x32_bf16 a[224:227],a[140:143],v[76:79],a[224:227]
v_cmp_eq_u32 vcc,0,v18
s_nop 1
v_cndmask_b32 v34,0,v19,vcc
v_mfma_f32_16x16x32_bf16 a[228:231],a[140:143],v[92:95],a[228:231]
buffer_load_dwordx4 v[236:239],v6,s[28:31],s79 offen offset:1024 sc1
v_mfma_f32_16x16x32_bf16 a[228:231],a[136:139],v[88:91],a[228:231]
v_cmp_eq_u32 vcc,1,v18
s_nop 1
v_cndmask_b32 v35,0,v19,vcc
v_mfma_f32_16x16x32_bf16 a[228:231],a[132:135],v[84:87],a[228:231]
s_add_u32 s20,s20,s44
s_addc_u32 s21,s21,0
v_bfe_u32 v41,v6,8,1
v_mfma_f32_16x16x32_bf16 a[228:231],a[128:131],v[80:83],a[228:231]
s_waitcnt vmcnt(9)
v_cmp_ne_u32 s[90:91],s68,v4
v_and_b32 v4,-2,v5
v_cmp_ne_u32 s[92:93],s69,v4
s_nop 0
s_or_b64 s[90:91],s[90:91],s[92:93]
s_cbranch_scc1 L_fr9_%=
L_fo9_%=:
v_mfma_f32_16x16x32_bf16 a[232:235],a[128:131],v[96:99],a[232:235]
s_mul_i32 s44,s71,320
v_lshl_add_u32 v40,v41,5,v40
v_lshrrev_b32 v41,9,v6
v_mfma_f32_16x16x32_bf16 a[232:235],a[132:135],v[100:103],a[232:235]
s_and_b32 s21,s21,0xffff
s_add_u32 s43,s43,s44
v_lshl_add_u32 v40,v41,4,v40
v_mfma_f32_16x16x32_bf16 a[232:235],a[136:139],v[104:107],a[232:235]
buffer_load_dwordx4 v[248:251],v6,s[28:31],s79 offen offset:2048 sc1
v_mfma_f32_16x16x32_bf16 a[232:235],a[140:143],v[108:111],a[232:235]
v_mfma_f32_16x16x32_bf16 a[236:239],a[140:143],v[124:127],a[236:239]
ds_read_b64_tr_b16 a[140:141],v1 offset:30976
ds_read_b64_tr_b16 a[142:143],v1 offset:36096
v_mfma_f32_16x16x32_bf16 a[236:239],a[136:139],v[120:123],a[236:239]
ds_read_b64_tr_b16 a[136:137],v1 offset:20736
v_mfma_f32_16x16x32_bf16 a[236:239],a[132:135],v[116:119],a[236:239]
ds_read_b64_tr_b16 a[132:133],v1 offset:10496
v_mfma_f32_16x16x32_bf16 a[236:239],a[128:131],v[112:115],a[236:239]
s_waitcnt lgkmcnt(4)
ds_read_b64_tr_b16 a[128:129],v1 offset:256
v_mfma_f32_16x16x32_bf16 a[240:243],a[144:147],v[64:67],a[240:243]
buffer_load_dwordx4 v[252:255],v6,s[28:31],s79 offen offset:3072 sc1
v_mfma_f32_16x16x32_bf16 a[240:243],a[148:151],v[68:71],a[240:243]
s_add_u32 s79,s76,20480
s_max_i32 s76,s76,s18
ds_read_b64_tr_b16 a[130:131],v1 offset:5376
v_mfma_f32_16x16x32_bf16 a[240:243],a[152:155],v[72:75],a[240:243]
s_max_i32 s79,s79,s101
ds_read_b64_tr_b16 a[134:135],v1 offset:15616
ds_read_b64_tr_b16 a[138:139],v1 offset:25856
v_mfma_f32_16x16x32_bf16 a[240:243],a[156:159],v[76:79],a[240:243]
v_subrev_u32 v1,40960,v1
v_mfma_f32_16x16x32_bf16 a[244:247],a[156:159],v[92:95],a[244:247]
buffer_load_dwordx4 v[128:131],v6,s[28:31],s76 offen sc1
v_mfma_f32_16x16x32_bf16 a[244:247],a[152:155],v[88:91],a[244:247]
v_mfma_f32_16x16x32_bf16 a[244:247],a[148:151],v[84:87],a[244:247]
v_mfma_f32_16x16x32_bf16 a[244:247],a[144:147],v[80:83],a[244:247]
v_mfma_f32_16x16x32_bf16 a[248:251],a[144:147],v[96:99],a[248:251]
buffer_load_dwordx4 v[160:163],v6,s[28:31],s76 offen offset:1024 sc1
v_mfma_f32_16x16x32_bf16 a[248:251],a[148:151],v[100:103],a[248:251]
v_mfma_f32_16x16x32_bf16 a[248:251],a[152:155],v[104:107],a[248:251]
v_mfma_f32_16x16x32_bf16 a[248:251],a[156:159],v[108:111],a[248:251]
v_mfma_f32_16x16x32_bf16 a[252:255],a[156:159],v[124:127],a[252:255]
buffer_load_dwordx4 v[192:195],v6,s[28:31],s76 offen offset:2048 sc1
v_mfma_f32_16x16x32_bf16 a[252:255],a[152:155],v[120:123],a[252:255]
ds_read_b64_tr_b16 a[152:153],v2 offset:20768
ds_read_b64_tr_b16 a[154:155],v2 offset:25888
v_mfma_f32_16x16x32_bf16 a[252:255],a[148:151],v[116:119],a[252:255]
ds_read_b64_tr_b16 a[148:149],v2 offset:10528
v_mfma_f32_16x16x32_bf16 a[252:255],a[144:147],v[112:115],a[252:255]
ds_read_b64_tr_b16 a[144:145],v2 offset:288
v_mfma_f32_16x16x32_bf16 a[0:3],a[160:163],v[64:67],a[0:3]
ds_read_b64_tr_b16 a[146:147],v2 offset:5408
v_mfma_f32_16x16x32_bf16 a[0:3],a[164:167],v[68:71],a[0:3]
buffer_load_dwordx4 v[224:227],v6,s[28:31],s76 offen offset:3072 sc1
v_mfma_f32_16x16x32_bf16 a[0:3],a[168:171],v[72:75],a[0:3]
ds_read_b64_tr_b16 a[150:151],v2 offset:15648
ds_read_b64_tr_b16 a[156:157],v2 offset:31008
v_mfma_f32_16x16x32_bf16 a[0:3],a[172:175],v[76:79],a[0:3]
ds_read_b64_tr_b16 a[158:159],v2 offset:36128
v_subrev_u32 v2,40960,v2
v_mfma_f32_16x16x32_bf16 a[4:7],a[172:175],v[92:95],a[4:7]
v_mfma_f32_16x16x32_bf16 a[4:7],a[168:171],v[88:91],a[4:7]
v_mfma_f32_16x16x32_bf16 a[4:7],a[164:167],v[84:87],a[4:7]
v_mfma_f32_16x16x32_bf16 a[4:7],a[160:163],v[80:83],a[4:7]
buffer_load_dwordx4 v[132:135],v6,s[28:31],s77 offen sc1
v_mfma_f32_16x16x32_bf16 a[8:11],a[160:163],v[96:99],a[8:11]
v_mfma_f32_16x16x32_bf16 a[8:11],a[164:167],v[100:103],a[8:11]
v_mfma_f32_16x16x32_bf16 a[8:11],a[168:171],v[104:107],a[8:11]
v_mfma_f32_16x16x32_bf16 a[8:11],a[172:175],v[108:111],a[8:11]
v_mfma_f32_16x16x32_bf16 a[12:15],a[172:175],v[124:127],a[12:15]
ds_read_b64_tr_b16 a[172:173],v1 offset:30720
ds_read_b64_tr_b16 a[174:175],v1 offset:35840
v_mfma_f32_16x16x32_bf16 a[12:15],a[168:171],v[120:123],a[12:15]
buffer_load_dwordx4 v[164:167],v6,s[28:31],s77 offen offset:1024 sc1
v_mfma_f32_16x16x32_bf16 a[12:15],a[164:167],v[116:119],a[12:15]
ds_read_b64_tr_b16 a[164:165],v1 offset:10240
ds_read_b64_tr_b16 a[166:167],v1 offset:15360
v_mfma_f32_16x16x32_bf16 a[12:15],a[160:163],v[112:115],a[12:15]
ds_read_b64_tr_b16 a[160:161],v1
v_mfma_f32_16x16x32_bf16 a[16:19],a[176:179],v[64:67],a[16:19]
ds_read_b64_tr_b16 a[162:163],v1 offset:5120
v_mfma_f32_16x16x32_bf16 a[16:19],a[180:183],v[68:71],a[16:19]
ds_read_b64_tr_b16 a[168:169],v1 offset:20480
v_mfma_f32_16x16x32_bf16 a[16:19],a[184:187],v[72:75],a[16:19]
ds_read_b64_tr_b16 a[170:171],v1 offset:25600
v_mfma_f32_16x16x32_bf16 a[16:19],a[188:191],v[76:79],a[16:19]
buffer_load_dwordx4 v[196:199],v6,s[28:31],s77 offen offset:2048 sc1
v_mfma_f32_16x16x32_bf16 a[20:23],a[188:191],v[92:95],a[20:23]
v_mfma_f32_16x16x32_bf16 a[20:23],a[184:187],v[88:91],a[20:23]
v_mfma_f32_16x16x32_bf16 a[20:23],a[180:183],v[84:87],a[20:23]
v_mfma_f32_16x16x32_bf16 a[20:23],a[176:179],v[80:83],a[20:23]
v_mfma_f32_16x16x32_bf16 a[24:27],a[176:179],v[96:99],a[24:27]
v_mfma_f32_16x16x32_bf16 a[24:27],a[180:183],v[100:103],a[24:27]
buffer_load_dwordx4 v[228:231],v6,s[28:31],s77 offen offset:3072 sc1
v_mfma_f32_16x16x32_bf16 a[24:27],a[184:187],v[104:107],a[24:27]
v_mfma_f32_16x16x32_bf16 a[24:27],a[188:191],v[108:111],a[24:27]
v_mfma_f32_16x16x32_bf16 a[28:31],a[188:191],v[124:127],a[28:31]
ds_read_b64_tr_b16 a[188:189],v2 offset:30752
ds_read_b64_tr_b16 a[190:191],v2 offset:35872
v_mfma_f32_16x16x32_bf16 a[28:31],a[184:187],v[120:123],a[28:31]
ds_read_b64_tr_b16 a[184:185],v2 offset:20512
v_mfma_f32_16x16x32_bf16 a[28:31],a[180:183],v[116:119],a[28:31]
ds_read_b64_tr_b16 a[180:181],v2 offset:10272
v_mfma_f32_16x16x32_bf16 a[28:31],a[176:179],v[112:115],a[28:31]
buffer_load_dwordx4 v[144:147],v6,s[28:31],s78 offen sc1
v_mfma_f32_16x16x32_bf16 a[32:35],a[96:99],v[64:67],a[32:35]
ds_read_b64_tr_b16 a[176:177],v2 offset:32
ds_read_b64_tr_b16 a[178:179],v2 offset:5152
v_mfma_f32_16x16x32_bf16 a[32:35],a[100:103],v[68:71],a[32:35]
ds_read_b64_tr_b16 a[182:183],v2 offset:15392
v_mfma_f32_16x16x32_bf16 a[32:35],a[104:107],v[72:75],a[32:35]
ds_read_b64_tr_b16 a[186:187],v2 offset:25632
v_mfma_f32_16x16x32_bf16 a[32:35],a[108:111],v[76:79],a[32:35]
v_mfma_f32_16x16x32_bf16 a[36:39],a[108:111],v[92:95],a[36:39]
v_mfma_f32_16x16x32_bf16 a[36:39],a[104:107],v[88:91],a[36:39]
buffer_load_dwordx4 v[176:179],v6,s[28:31],s78 offen offset:1024 sc1
v_mfma_f32_16x16x32_bf16 a[36:39],a[100:103],v[84:87],a[36:39]
v_mfma_f32_16x16x32_bf16 a[36:39],a[96:99],v[80:83],a[36:39]
v_mfma_f32_16x16x32_bf16 a[40:43],a[96:99],v[96:99],a[40:43]
v_mfma_f32_16x16x32_bf16 a[40:43],a[100:103],v[100:103],a[40:43]
v_mfma_f32_16x16x32_bf16 a[40:43],a[104:107],v[104:107],a[40:43]
v_mfma_f32_16x16x32_bf16 a[40:43],a[108:111],v[108:111],a[40:43]
buffer_load_dwordx4 v[208:211],v6,s[28:31],s78 offen offset:2048 sc1
v_mfma_f32_16x16x32_bf16 a[44:47],a[108:111],v[124:127],a[44:47]
ds_read_b64_tr_b16 a[108:109],v1 offset:30784
ds_read_b64_tr_b16 a[110:111],v1 offset:35904
v_mfma_f32_16x16x32_bf16 a[44:47],a[104:107],v[120:123],a[44:47]
ds_read_b64_tr_b16 a[104:105],v1 offset:20544
v_mfma_f32_16x16x32_bf16 a[44:47],a[100:103],v[116:119],a[44:47]
ds_read_b64_tr_b16 a[100:101],v1 offset:10304
v_mfma_f32_16x16x32_bf16 a[44:47],a[96:99],v[112:115],a[44:47]
ds_read_b64_tr_b16 a[96:97],v1 offset:64
v_mfma_f32_16x16x32_bf16 a[48:51],a[112:115],v[64:67],a[48:51]
ds_read_b64_tr_b16 a[98:99],v1 offset:5184
ds_read_b64_tr_b16 a[102:103],v1 offset:15424
v_mfma_f32_16x16x32_bf16 a[48:51],a[116:119],v[68:71],a[48:51]
buffer_load_dwordx4 v[240:243],v6,s[28:31],s78 offen offset:3072 sc1
v_mfma_f32_16x16x32_bf16 a[48:51],a[120:123],v[72:75],a[48:51]
ds_read_b64_tr_b16 a[106:107],v1 offset:25664
v_mfma_f32_16x16x32_bf16 a[48:51],a[124:127],v[76:79],a[48:51]
v_mfma_f32_16x16x32_bf16 a[52:55],a[124:127],v[92:95],a[52:55]
v_mfma_f32_16x16x32_bf16 a[52:55],a[120:123],v[88:91],a[52:55]
v_mfma_f32_16x16x32_bf16 a[52:55],a[116:119],v[84:87],a[52:55]
v_mfma_f32_16x16x32_bf16 a[52:55],a[112:115],v[80:83],a[52:55]
buffer_load_dwordx4 v[148:151],v6,s[28:31],s79 offen sc1
v_mfma_f32_16x16x32_bf16 a[56:59],a[112:115],v[96:99],a[56:59]
v_mfma_f32_16x16x32_bf16 a[56:59],a[116:119],v[100:103],a[56:59]
s_cmp_ge_u32 1,s71
s_cselect_b32 s80,5,3
s_mul_i32 s80,s80,163840
v_mfma_f32_16x16x32_bf16 a[56:59],a[120:123],v[104:107],a[56:59]
s_add_u32 s80,s80,s81
s_add_u32 s83,s80,20480
v_mfma_f32_16x16x32_bf16 a[56:59],a[124:127],v[108:111],a[56:59]
v_mfma_f32_16x16x32_bf16 a[60:63],a[124:127],v[124:127],a[60:63]
ds_read_b64_tr_b16 a[124:125],v2 offset:30816
ds_read_b64_tr_b16 a[126:127],v2 offset:35936
v_mfma_f32_16x16x32_bf16 a[60:63],a[120:123],v[120:123],a[60:63]
buffer_load_dwordx4 v[180:183],v6,s[28:31],s79 offen offset:1024 sc1
v_mfma_f32_16x16x32_bf16 a[60:63],a[116:119],v[116:119],a[60:63]
ds_read_b64_tr_b16 a[116:117],v2 offset:10336
ds_read_b64_tr_b16 a[118:119],v2 offset:15456
v_mfma_f32_16x16x32_bf16 a[60:63],a[112:115],v[112:115],a[60:63]
s_waitcnt lgkmcnt(4)
ds_read_b64_tr_b16 a[112:113],v2 offset:96
v_mfma_f32_16x16x32_bf16 a[64:67],a[128:131],v[64:67],a[64:67]
ds_read_b64_tr_b16 a[114:115],v2 offset:5216
v_mfma_f32_16x16x32_bf16 a[64:67],a[132:135],v[68:71],a[64:67]
ds_read_b64_tr_b16 a[120:121],v2 offset:20576
v_mfma_f32_16x16x32_bf16 a[64:67],a[136:139],v[72:75],a[64:67]
ds_read_b64_tr_b16 a[122:123],v2 offset:25696
v_mfma_f32_16x16x32_bf16 a[64:67],a[140:143],v[76:79],a[64:67]
buffer_load_dwordx4 v[212:215],v6,s[28:31],s79 offen offset:2048 sc1
v_mfma_f32_16x16x32_bf16 a[68:71],a[140:143],v[92:95],a[68:71]
v_mfma_f32_16x16x32_bf16 a[68:71],a[136:139],v[88:91],a[68:71]
v_mfma_f32_16x16x32_bf16 a[68:71],a[132:135],v[84:87],a[68:71]
v_mfma_f32_16x16x32_bf16 a[68:71],a[128:131],v[80:83],a[68:71]
v_mfma_f32_16x16x32_bf16 a[72:75],a[128:131],v[96:99],a[72:75]
v_mfma_f32_16x16x32_bf16 a[72:75],a[132:135],v[100:103],a[72:75]
buffer_load_dwordx4 v[244:247],v6,s[28:31],s79 offen offset:3072 sc1
)A"
R"A(v_mfma_f32_16x16x32_bf16 a[72:75],a[136:139],v[104:107],a[72:75]
v_mfma_f32_16x16x32_bf16 a[72:75],a[140:143],v[108:111],a[72:75]
v_mfma_f32_16x16x32_bf16 a[76:79],a[140:143],v[124:127],a[76:79]
ds_read_b64_tr_b16 a[140:141],v1 offset:30848
ds_read_b64_tr_b16 a[142:143],v1 offset:35968
v_mfma_f32_16x16x32_bf16 a[76:79],a[136:139],v[120:123],a[76:79]
ds_read_b64_tr_b16 a[136:137],v1 offset:20608
v_mfma_f32_16x16x32_bf16 a[76:79],a[132:135],v[116:119],a[76:79]
s_add_u32 m0,s82,40960
ds_read_b64_tr_b16 a[132:133],v1 offset:10368
v_mfma_f32_16x16x32_bf16 a[76:79],a[128:131],v[112:115],a[76:79]
ds_read_b64_tr_b16 a[128:129],v1 offset:128
v_mfma_f32_16x16x32_bf16 a[80:83],a[144:147],v[64:67],a[80:83]
ds_read_b64_tr_b16 a[130:131],v1 offset:5248
ds_read_b64_tr_b16 a[134:135],v1 offset:15488
v_mfma_f32_16x16x32_bf16 a[80:83],a[148:151],v[68:71],a[80:83]
buffer_load_dwordx4 v7,s[72:75],s80 offen lds
v_mfma_f32_16x16x32_bf16 a[80:83],a[152:155],v[72:75],a[80:83]
ds_read_b64_tr_b16 a[138:139],v1 offset:25728
v_mfma_f32_16x16x32_bf16 a[80:83],a[156:159],v[76:79],a[80:83]
v_mfma_f32_16x16x32_bf16 a[84:87],a[156:159],v[92:95],a[84:87]
v_mfma_f32_16x16x32_bf16 a[84:87],a[152:155],v[88:91],a[84:87]
v_mfma_f32_16x16x32_bf16 a[84:87],a[148:151],v[84:87],a[84:87]
buffer_load_dwordx4 v8,s[72:75],s80 offen offset:1024 lds
v_mfma_f32_16x16x32_bf16 a[84:87],a[144:147],v[80:83],a[84:87]
v_mfma_f32_16x16x32_bf16 a[88:91],a[144:147],v[96:99],a[88:91]
v_mfma_f32_16x16x32_bf16 a[88:91],a[148:151],v[100:103],a[88:91]
v_mfma_f32_16x16x32_bf16 a[88:91],a[152:155],v[104:107],a[88:91]
v_mfma_f32_16x16x32_bf16 a[88:91],a[156:159],v[108:111],a[88:91]
buffer_load_dwordx4 v9,s[72:75],s80 offen offset:2048 lds
v_mfma_f32_16x16x32_bf16 a[92:95],a[156:159],v[124:127],a[92:95]
ds_read_b64_tr_b16 a[156:157],v2 offset:30880
ds_read_b64_tr_b16 a[158:159],v2 offset:36000
v_mfma_f32_16x16x32_bf16 a[92:95],a[152:155],v[120:123],a[92:95]
ds_read_b64_tr_b16 a[152:153],v2 offset:20640
v_mfma_f32_16x16x32_bf16 a[92:95],a[148:151],v[116:119],a[92:95]
ds_read_b64_tr_b16 a[148:149],v2 offset:10400
v_mfma_f32_16x16x32_bf16 a[92:95],a[144:147],v[112:115],a[92:95]
ds_read_b64_tr_b16 a[144:145],v2 offset:160
v_mfma_f32_16x16x32_bf16 a[192:195],a[160:163],v[136:139],a[192:195]
buffer_load_dwordx4 v10,s[72:75],s80 offen offset:3072 lds
v_mfma_f32_16x16x32_bf16 a[192:195],a[164:167],v[140:143],a[192:195]
ds_read_b64_tr_b16 a[146:147],v2 offset:5280
ds_read_b64_tr_b16 a[150:151],v2 offset:15520
v_mfma_f32_16x16x32_bf16 a[192:195],a[168:171],v[152:155],a[192:195]
s_addk_i32 m0,4096
ds_read_b64_tr_b16 a[154:155],v2 offset:25760
v_mfma_f32_16x16x32_bf16 a[192:195],a[172:175],v[156:159],a[192:195]
s_waitcnt vmcnt(12)
v_mfma_f32_16x16x32_bf16 a[196:199],a[172:175],v[188:191],a[196:199]
v_mfma_f32_16x16x32_bf16 a[196:199],a[168:171],v[184:187],a[196:199]
buffer_load_dwordx4 v11,s[72:75],s80 offen lds
v_mfma_f32_16x16x32_bf16 a[196:199],a[164:167],v[172:175],a[196:199]
v_mfma_f32_16x16x32_bf16 a[196:199],a[160:163],v[168:171],a[196:199]
s_addk_i32 m0,1024
v_mfma_f32_16x16x32_bf16 a[200:203],a[160:163],v[200:203],a[200:203]
v_mfma_f32_16x16x32_bf16 a[200:203],a[164:167],v[204:207],a[200:203]
v_mfma_f32_16x16x32_bf16 a[200:203],a[168:171],v[216:219],a[200:203]
buffer_load_dwordx4 v7,s[72:75],s83 offen lds
v_mfma_f32_16x16x32_bf16 a[200:203],a[172:175],v[220:223],a[200:203]
v_mfma_f32_16x16x32_bf16 a[204:207],a[172:175],v[252:255],a[204:207]
ds_read_b64_tr_b16 a[172:173],v1 offset:30912
ds_read_b64_tr_b16 a[174:175],v1 offset:36032
v_mfma_f32_16x16x32_bf16 a[204:207],a[168:171],v[248:251],a[204:207]
ds_read_b64_tr_b16 a[168:169],v1 offset:20672
v_mfma_f32_16x16x32_bf16 a[204:207],a[164:167],v[236:239],a[204:207]
ds_read_b64_tr_b16 a[164:165],v1 offset:10432
v_mfma_f32_16x16x32_bf16 a[204:207],a[160:163],v[232:235],a[204:207]
s_barrier
buffer_load_dwordx4 v8,s[72:75],s83 offen offset:1024 lds
v_mfma_f32_16x16x32_bf16 a[208:211],a[176:179],v[136:139],a[208:211]
s_cmp_ge_u32 1,s71
s_cselect_b32 s76,5,3
ds_read_b64_tr_b16 a[160:161],v1 offset:192
v_mfma_f32_16x16x32_bf16 a[208:211],a[180:183],v[140:143],a[208:211]
s_bitcmp1_b32 s54,s76
s_cselect_b32 s77,8192,0
s_lshl_b32 s76,s76,17
v_mfma_f32_16x16x32_bf16 a[208:211],a[184:187],v[152:155],a[208:211]
s_add_u32 s76,s76,s77
s_add_u32 s77,s76,4096
s_add_u32 s78,s76,16384
v_mfma_f32_16x16x32_bf16 a[208:211],a[188:191],v[156:159],a[208:211]
s_add_u32 s79,s76,20480
buffer_load_dwordx4 v9,s[72:75],s83 offen offset:2048 lds
v_mfma_f32_16x16x32_bf16 a[212:215],a[188:191],v[188:191],a[212:215]
s_max_i32 s76,s76,s18
s_max_i32 s77,s77,s19
s_max_i32 s78,s78,s100
v_mfma_f32_16x16x32_bf16 a[212:215],a[184:187],v[184:187],a[212:215]
s_max_i32 s79,s79,s101
ds_read_b64_tr_b16 a[162:163],v1 offset:5312
ds_read_b64_tr_b16 a[166:167],v1 offset:15552
v_mfma_f32_16x16x32_bf16 a[212:215],a[180:183],v[172:175],a[212:215]
ds_read_b64_tr_b16 a[170:171],v1 offset:25792
v_mfma_f32_16x16x32_bf16 a[212:215],a[176:179],v[168:171],a[212:215]
v_mfma_f32_16x16x32_bf16 a[216:219],a[176:179],v[200:203],a[216:219]
buffer_load_dwordx4 v10,s[72:75],s83 offen offset:3072 lds
v_mfma_f32_16x16x32_bf16 a[216:219],a[180:183],v[204:207],a[216:219]
v_mfma_f32_16x16x32_bf16 a[216:219],a[184:187],v[216:219],a[216:219]
s_addk_i32 m0,4096
v_mfma_f32_16x16x32_bf16 a[216:219],a[188:191],v[220:223],a[216:219]
v_mfma_f32_16x16x32_bf16 a[220:223],a[188:191],v[252:255],a[220:223]
ds_read_b64_tr_b16 a[188:189],v2 offset:30944
ds_read_b64_tr_b16 a[190:191],v2 offset:36064
v_mfma_f32_16x16x32_bf16 a[220:223],a[184:187],v[248:251],a[220:223]
buffer_load_dwordx4 v11,s[72:75],s83 offen lds
v_mfma_f32_16x16x32_bf16 a[220:223],a[180:183],v[236:239],a[220:223]
ds_read_b64_tr_b16 a[180:181],v2 offset:10464
ds_read_b64_tr_b16 a[182:183],v2 offset:15584
v_mfma_f32_16x16x32_bf16 a[220:223],a[176:179],v[232:235],a[220:223]
ds_read_b64_tr_b16 a[176:177],v2 offset:224
v_mfma_f32_16x16x32_bf16 a[224:227],a[96:99],v[136:139],a[224:227]
ds_read_b64_tr_b16 a[178:179],v2 offset:5344
v_mfma_f32_16x16x32_bf16 a[224:227],a[100:103],v[140:143],a[224:227]
ds_read_b64_tr_b16 a[184:185],v2 offset:20704
v_mfma_f32_16x16x32_bf16 a[224:227],a[104:107],v[152:155],a[224:227]
ds_read_b64_tr_b16 a[186:187],v2 offset:25824
v_mfma_f32_16x16x32_bf16 a[224:227],a[108:111],v[156:159],a[224:227]
v_mfma_f32_16x16x32_bf16 a[228:231],a[108:111],v[188:191],a[228:231]
v_mfma_f32_16x16x32_bf16 a[228:231],a[104:107],v[184:187],a[228:231]
v_mfma_f32_16x16x32_bf16 a[228:231],a[100:103],v[172:175],a[228:231]
v_mfma_f32_16x16x32_bf16 a[228:231],a[96:99],v[168:171],a[228:231]
buffer_load_dwordx4 v[64:67],v6,s[28:31],s76 offen sc1
v_mfma_f32_16x16x32_bf16 a[232:235],a[96:99],v[200:203],a[232:235]
v_mfma_f32_16x16x32_bf16 a[232:235],a[100:103],v[204:207],a[232:235]
v_mfma_f32_16x16x32_bf16 a[232:235],a[104:107],v[216:219],a[232:235]
v_mfma_f32_16x16x32_bf16 a[232:235],a[108:111],v[220:223],a[232:235]
v_mfma_f32_16x16x32_bf16 a[236:239],a[108:111],v[252:255],a[236:239]
buffer_load_dwordx4 v[68:71],v6,s[28:31],s76 offen offset:1024 sc1
v_mfma_f32_16x16x32_bf16 a[236:239],a[104:107],v[248:251],a[236:239]
ds_read_b64_tr_b16 a[104:105],v1 offset:20736
ds_read_b64_tr_b16 a[106:107],v1 offset:25856
v_mfma_f32_16x16x32_bf16 a[236:239],a[100:103],v[236:239],a[236:239]
ds_read_b64_tr_b16 a[100:101],v1 offset:10496
v_mfma_f32_16x16x32_bf16 a[236:239],a[96:99],v[232:235],a[236:239]
s_waitcnt lgkmcnt(3)
ds_read_b64_tr_b16 a[96:97],v1 offset:256
v_mfma_f32_16x16x32_bf16 a[240:243],a[112:115],v[136:139],a[240:243]
ds_read_b64_tr_b16 a[98:99],v1 offset:5376
v_mfma_f32_16x16x32_bf16 a[240:243],a[116:119],v[140:143],a[240:243]
buffer_load_dwordx4 v[72:75],v6,s[28:31],s76 offen offset:2048 sc1
v_mfma_f32_16x16x32_bf16 a[240:243],a[120:123],v[152:155],a[240:243]
ds_read_b64_tr_b16 a[102:103],v1 offset:15616
ds_read_b64_tr_b16 a[108:109],v1 offset:30976
v_mfma_f32_16x16x32_bf16 a[240:243],a[124:127],v[156:159],a[240:243]
ds_read_b64_tr_b16 a[110:111],v1 offset:36096
v_subrev_u32 v1,81920,v1
v_mfma_f32_16x16x32_bf16 a[244:247],a[124:127],v[188:191],a[244:247]
v_add_u32 v42,40960,v1
v_add_u32 v44,122880,v1
v_mfma_f32_16x16x32_bf16 a[244:247],a[120:123],v[184:187],a[244:247]
v_mfma_f32_16x16x32_bf16 a[244:247],a[116:119],v[172:175],a[244:247]
buffer_load_dwordx4 v[76:79],v6,s[28:31],s76 offen offset:3072 sc1
v_mfma_f32_16x16x32_bf16 a[244:247],a[112:115],v[168:171],a[244:247]
v_mfma_f32_16x16x32_bf16 a[248:251],a[112:115],v[200:203],a[248:251]
v_mfma_f32_16x16x32_bf16 a[248:251],a[116:119],v[204:207],a[248:251]
v_mfma_f32_16x16x32_bf16 a[248:251],a[120:123],v[216:219],a[248:251]
v_mfma_f32_16x16x32_bf16 a[248:251],a[124:127],v[220:223],a[248:251]
buffer_load_dwordx4 v[80:83],v6,s[28:31],s77 offen sc1
v_mfma_f32_16x16x32_bf16 a[252:255],a[124:127],v[252:255],a[252:255]
ds_read_b64_tr_b16 a[124:125],v2 offset:31008
ds_read_b64_tr_b16 a[126:127],v2 offset:36128
v_mfma_f32_16x16x32_bf16 a[252:255],a[120:123],v[248:251],a[252:255]
ds_read_b64_tr_b16 a[120:121],v2 offset:20768
v_mfma_f32_16x16x32_bf16 a[252:255],a[116:119],v[236:239],a[252:255]
ds_read_b64_tr_b16 a[116:117],v2 offset:10528
v_mfma_f32_16x16x32_bf16 a[252:255],a[112:115],v[232:235],a[252:255]
ds_read_b64_tr_b16 a[112:113],v2 offset:288
v_mfma_f32_16x16x32_bf16 a[0:3],a[128:131],v[136:139],a[0:3]
buffer_load_dwordx4 v[84:87],v6,s[28:31],s77 offen offset:1024 sc1
v_mfma_f32_16x16x32_bf16 a[0:3],a[132:135],v[140:143],a[0:3]
ds_read_b64_tr_b16 a[114:115],v2 offset:5408
ds_read_b64_tr_b16 a[118:119],v2 offset:15648
v_mfma_f32_16x16x32_bf16 a[0:3],a[136:139],v[152:155],a[0:3]
ds_read_b64_tr_b16 a[122:123],v2 offset:25888
v_subrev_u32 v2,81920,v2
v_mfma_f32_16x16x32_bf16 a[0:3],a[140:143],v[156:159],a[0:3]
v_add_u32 v43,40960,v2
v_add_u32 v45,122880,v2
v_mfma_f32_16x16x32_bf16 a[4:7],a[140:143],v[188:191],a[4:7]
v_mfma_f32_16x16x32_bf16 a[4:7],a[136:139],v[184:187],a[4:7]
buffer_load_dwordx4 v[88:91],v6,s[28:31],s77 offen offset:2048 sc1
v_mfma_f32_16x16x32_bf16 a[4:7],a[132:135],v[172:175],a[4:7]
v_mfma_f32_16x16x32_bf16 a[4:7],a[128:131],v[168:171],a[4:7]
v_mfma_f32_16x16x32_bf16 a[8:11],a[128:131],v[200:203],a[8:11]
v_mfma_f32_16x16x32_bf16 a[8:11],a[132:135],v[204:207],a[8:11]
v_mfma_f32_16x16x32_bf16 a[8:11],a[136:139],v[216:219],a[8:11]
buffer_load_dwordx4 v[92:95],v6,s[28:31],s77 offen offset:3072 sc1
v_mfma_f32_16x16x32_bf16 a[8:11],a[140:143],v[220:223],a[8:11]
v_mfma_f32_16x16x32_bf16 a[12:15],a[140:143],v[252:255],a[12:15]
ds_read_b64_tr_b16 a[140:141],v1 offset:30720
ds_read_b64_tr_b16 a[142:143],v1 offset:35840
v_mfma_f32_16x16x32_bf16 a[12:15],a[136:139],v[248:251],a[12:15]
ds_read_b64_tr_b16 a[136:137],v1 offset:20480
v_mfma_f32_16x16x32_bf16 a[12:15],a[132:135],v[236:239],a[12:15]
ds_read_b64_tr_b16 a[132:133],v1 offset:10240
v_mfma_f32_16x16x32_bf16 a[12:15],a[128:131],v[232:235],a[12:15]
buffer_load_dwordx4 v[96:99],v6,s[28:31],s78 offen sc1
v_mfma_f32_16x16x32_bf16 a[16:19],a[144:147],v[136:139],a[16:19]
ds_read_b64_tr_b16 a[128:129],v1
ds_read_b64_tr_b16 a[130:131],v1 offset:5120
v_mfma_f32_16x16x32_bf16 a[16:19],a[148:151],v[140:143],a[16:19]
ds_read_b64_tr_b16 a[134:135],v1 offset:15360
v_mfma_f32_16x16x32_bf16 a[16:19],a[152:155],v[152:155],a[16:19]
ds_read_b64_tr_b16 a[138:139],v1 offset:25600
v_mfma_f32_16x16x32_bf16 a[16:19],a[156:159],v[156:159],a[16:19]
v_mfma_f32_16x16x32_bf16 a[20:23],a[156:159],v[188:191],a[20:23]
buffer_load_dwordx4 v[100:103],v6,s[28:31],s78 offen offset:1024 sc1
v_mfma_f32_16x16x32_bf16 a[20:23],a[152:155],v[184:187],a[20:23]
v_mfma_f32_16x16x32_bf16 a[20:23],a[148:151],v[172:175],a[20:23]
v_mfma_f32_16x16x32_bf16 a[20:23],a[144:147],v[168:171],a[20:23]
v_mfma_f32_16x16x32_bf16 a[24:27],a[144:147],v[200:203],a[24:27]
v_mfma_f32_16x16x32_bf16 a[24:27],a[148:151],v[204:207],a[24:27]
buffer_load_dwordx4 v[104:107],v6,s[28:31],s78 offen offset:2048 sc1
v_mfma_f32_16x16x32_bf16 a[24:27],a[152:155],v[216:219],a[24:27]
v_mfma_f32_16x16x32_bf16 a[24:27],a[156:159],v[220:223],a[24:27]
v_mfma_f32_16x16x32_bf16 a[28:31],a[156:159],v[252:255],a[28:31]
)A"
R"A(ds_read_b64_tr_b16 a[156:157],v2 offset:30752
ds_read_b64_tr_b16 a[158:159],v2 offset:35872
v_mfma_f32_16x16x32_bf16 a[28:31],a[152:155],v[248:251],a[28:31]
ds_read_b64_tr_b16 a[152:153],v2 offset:20512
v_mfma_f32_16x16x32_bf16 a[28:31],a[148:151],v[236:239],a[28:31]
buffer_load_dwordx4 v[108:111],v6,s[28:31],s78 offen offset:3072 sc1
v_mfma_f32_16x16x32_bf16 a[28:31],a[144:147],v[232:235],a[28:31]
s_cmp_ge_u32 2,s71
s_cselect_b32 s80,7,5
s_mul_i32 s80,s80,163840
v_mfma_f32_16x16x32_bf16 a[32:35],a[160:163],v[136:139],a[32:35]
s_add_u32 s80,s80,s81
s_add_u32 s83,s80,20480
ds_read_b64_tr_b16 a[144:145],v2 offset:32
v_mfma_f32_16x16x32_bf16 a[32:35],a[164:167],v[140:143],a[32:35]
ds_read_b64_tr_b16 a[146:147],v2 offset:5152
ds_read_b64_tr_b16 a[148:149],v2 offset:10272
v_mfma_f32_16x16x32_bf16 a[32:35],a[168:171],v[152:155],a[32:35]
ds_read_b64_tr_b16 a[150:151],v2 offset:15392
v_mfma_f32_16x16x32_bf16 a[32:35],a[172:175],v[156:159],a[32:35]
buffer_load_dwordx4 v[112:115],v6,s[28:31],s79 offen sc1
v_mfma_f32_16x16x32_bf16 a[36:39],a[172:175],v[188:191],a[36:39]
ds_read_b64_tr_b16 a[154:155],v2 offset:25632
v_mfma_f32_16x16x32_bf16 a[36:39],a[168:171],v[184:187],a[36:39]
v_mfma_f32_16x16x32_bf16 a[36:39],a[164:167],v[172:175],a[36:39]
v_mfma_f32_16x16x32_bf16 a[36:39],a[160:163],v[168:171],a[36:39]
v_mfma_f32_16x16x32_bf16 a[40:43],a[160:163],v[200:203],a[40:43]
buffer_load_dwordx4 v[116:119],v6,s[28:31],s79 offen offset:1024 sc1
v_mfma_f32_16x16x32_bf16 a[40:43],a[164:167],v[204:207],a[40:43]
v_mfma_f32_16x16x32_bf16 a[40:43],a[168:171],v[216:219],a[40:43]
v_mfma_f32_16x16x32_bf16 a[40:43],a[172:175],v[220:223],a[40:43]
v_mfma_f32_16x16x32_bf16 a[44:47],a[172:175],v[252:255],a[44:47]
ds_read_b64_tr_b16 a[172:173],v1 offset:30784
ds_read_b64_tr_b16 a[174:175],v1 offset:35904
v_mfma_f32_16x16x32_bf16 a[44:47],a[168:171],v[248:251],a[44:47]
buffer_load_dwordx4 v[120:123],v6,s[28:31],s79 offen offset:2048 sc1
v_mfma_f32_16x16x32_bf16 a[44:47],a[164:167],v[236:239],a[44:47]
ds_read_b64_tr_b16 a[164:165],v1 offset:10304
ds_read_b64_tr_b16 a[166:167],v1 offset:15424
v_mfma_f32_16x16x32_bf16 a[44:47],a[160:163],v[232:235],a[44:47]
ds_read_b64_tr_b16 a[160:161],v1 offset:64
v_mfma_f32_16x16x32_bf16 a[48:51],a[176:179],v[136:139],a[48:51]
ds_read_b64_tr_b16 a[162:163],v1 offset:5184
v_mfma_f32_16x16x32_bf16 a[48:51],a[180:183],v[140:143],a[48:51]
ds_read_b64_tr_b16 a[168:169],v1 offset:20544
v_mfma_f32_16x16x32_bf16 a[48:51],a[184:187],v[152:155],a[48:51]
buffer_load_dwordx4 v[124:127],v6,s[28:31],s79 offen offset:3072 sc1
v_mfma_f32_16x16x32_bf16 a[48:51],a[188:191],v[156:159],a[48:51]
ds_read_b64_tr_b16 a[170:171],v1 offset:25664
v_mfma_f32_16x16x32_bf16 a[52:55],a[188:191],v[188:191],a[52:55]
s_add_u32 m0,s82,122880
v_mfma_f32_16x16x32_bf16 a[52:55],a[184:187],v[184:187],a[52:55]
v_mfma_f32_16x16x32_bf16 a[52:55],a[180:183],v[172:175],a[52:55]
v_mfma_f32_16x16x32_bf16 a[52:55],a[176:179],v[168:171],a[52:55]
buffer_load_dwordx4 v7,s[72:75],s80 offen lds
v_mfma_f32_16x16x32_bf16 a[56:59],a[176:179],v[200:203],a[56:59]
v_mfma_f32_16x16x32_bf16 a[56:59],a[180:183],v[204:207],a[56:59]
v_mfma_f32_16x16x32_bf16 a[56:59],a[184:187],v[216:219],a[56:59]
v_mfma_f32_16x16x32_bf16 a[56:59],a[188:191],v[220:223],a[56:59]
v_mfma_f32_16x16x32_bf16 a[60:63],a[188:191],v[252:255],a[60:63]
buffer_load_dwordx4 v8,s[72:75],s80 offen offset:1024 lds
v_mfma_f32_16x16x32_bf16 a[60:63],a[184:187],v[248:251],a[60:63]
ds_read_b64_tr_b16 a[184:185],v2 offset:20576
ds_read_b64_tr_b16 a[186:187],v2 offset:25696
v_mfma_f32_16x16x32_bf16 a[60:63],a[180:183],v[236:239],a[60:63]
ds_read_b64_tr_b16 a[180:181],v2 offset:10336
v_mfma_f32_16x16x32_bf16 a[60:63],a[176:179],v[232:235],a[60:63]
s_waitcnt lgkmcnt(3)
ds_read_b64_tr_b16 a[176:177],v2 offset:96
v_mfma_f32_16x16x32_bf16 a[64:67],a[96:99],v[136:139],a[64:67]
ds_read_b64_tr_b16 a[178:179],v2 offset:5216
v_mfma_f32_16x16x32_bf16 a[64:67],a[100:103],v[140:143],a[64:67]
buffer_load_dwordx4 v9,s[72:75],s80 offen offset:2048 lds
v_mfma_f32_16x16x32_bf16 a[64:67],a[104:107],v[152:155],a[64:67]
ds_read_b64_tr_b16 a[182:183],v2 offset:15456
ds_read_b64_tr_b16 a[188:189],v2 offset:30816
v_mfma_f32_16x16x32_bf16 a[64:67],a[108:111],v[156:159],a[64:67]
ds_read_b64_tr_b16 a[190:191],v2 offset:35936
v_mfma_f32_16x16x32_bf16 a[68:71],a[108:111],v[188:191],a[68:71]
v_mfma_f32_16x16x32_bf16 a[68:71],a[104:107],v[184:187],a[68:71]
v_mfma_f32_16x16x32_bf16 a[68:71],a[100:103],v[172:175],a[68:71]
buffer_load_dwordx4 v10,s[72:75],s80 offen offset:3072 lds
v_mfma_f32_16x16x32_bf16 a[68:71],a[96:99],v[168:171],a[68:71]
v_mfma_f32_16x16x32_bf16 a[72:75],a[96:99],v[200:203],a[72:75]
s_addk_i32 m0,4096
v_mfma_f32_16x16x32_bf16 a[72:75],a[100:103],v[204:207],a[72:75]
v_mfma_f32_16x16x32_bf16 a[72:75],a[104:107],v[216:219],a[72:75]
s_cmp_ge_u32 2,s71
s_cselect_b32 s76,7,5
v_mfma_f32_16x16x32_bf16 a[72:75],a[108:111],v[220:223],a[72:75]
buffer_load_dwordx4 v11,s[72:75],s80 offen lds
v_mfma_f32_16x16x32_bf16 a[76:79],a[108:111],v[252:255],a[76:79]
s_bitcmp1_b32 s54,s76
s_cselect_b32 s77,8192,0
s_lshl_b32 s76,s76,17
v_mfma_f32_16x16x32_bf16 a[76:79],a[104:107],v[248:251],a[76:79]
s_addk_i32 m0,1024
s_add_u32 s76,s76,s77
s_add_u32 s77,s76,4096
v_mfma_f32_16x16x32_bf16 a[76:79],a[100:103],v[236:239],a[76:79]
s_add_u32 s78,s76,16384
s_add_u32 s79,s76,20480
s_max_i32 s76,s76,s18
v_mfma_f32_16x16x32_bf16 a[76:79],a[96:99],v[232:235],a[76:79]
s_max_i32 s77,s77,s19
ds_read_b64_tr_b16 a[96:97],v1 offset:128
ds_read_b64_tr_b16 a[98:99],v1 offset:5248
v_mfma_f32_16x16x32_bf16 a[80:83],a[112:115],v[136:139],a[80:83]
s_max_i32 s78,s78,s100
buffer_load_dwordx4 v7,s[72:75],s83 offen lds
v_mfma_f32_16x16x32_bf16 a[80:83],a[116:119],v[140:143],a[80:83]
s_max_i32 s79,s79,s101
ds_read_b64_tr_b16 a[100:101],v1 offset:10368
ds_read_b64_tr_b16 a[102:103],v1 offset:15488
v_mfma_f32_16x16x32_bf16 a[80:83],a[120:123],v[152:155],a[80:83]
ds_read_b64_tr_b16 a[104:105],v1 offset:20608
v_mfma_f32_16x16x32_bf16 a[80:83],a[124:127],v[156:159],a[80:83]
ds_read_b64_tr_b16 a[106:107],v1 offset:25728
v_mfma_f32_16x16x32_bf16 a[84:87],a[124:127],v[188:191],a[84:87]
ds_read_b64_tr_b16 a[108:109],v1 offset:30848
v_mfma_f32_16x16x32_bf16 a[84:87],a[120:123],v[184:187],a[84:87]
buffer_load_dwordx4 v8,s[72:75],s83 offen offset:1024 lds
v_mfma_f32_16x16x32_bf16 a[84:87],a[116:119],v[172:175],a[84:87]
ds_read_b64_tr_b16 a[110:111],v1 offset:35968
v_mfma_f32_16x16x32_bf16 a[84:87],a[112:115],v[168:171],a[84:87]
v_mfma_f32_16x16x32_bf16 a[88:91],a[112:115],v[200:203],a[88:91]
v_mfma_f32_16x16x32_bf16 a[88:91],a[116:119],v[204:207],a[88:91]
v_mfma_f32_16x16x32_bf16 a[88:91],a[120:123],v[216:219],a[88:91]
buffer_load_dwordx4 v9,s[72:75],s83 offen offset:2048 lds
v_mfma_f32_16x16x32_bf16 a[88:91],a[124:127],v[220:223],a[88:91]
v_mfma_f32_16x16x32_bf16 a[92:95],a[124:127],v[252:255],a[92:95]
ds_read_b64_tr_b16 a[124:125],v2 offset:30880
ds_read_b64_tr_b16 a[126:127],v2 offset:36000
v_mfma_f32_16x16x32_bf16 a[92:95],a[120:123],v[248:251],a[92:95]
ds_read_b64_tr_b16 a[120:121],v2 offset:20640
v_mfma_f32_16x16x32_bf16 a[92:95],a[116:119],v[236:239],a[92:95]
ds_read_b64_tr_b16 a[116:117],v2 offset:10400
v_mfma_f32_16x16x32_bf16 a[92:95],a[112:115],v[232:235],a[92:95]
buffer_load_dwordx4 v10,s[72:75],s83 offen offset:3072 lds
v_mfma_f32_16x16x32_bf16 a[192:195],a[128:131],v[128:131],a[192:195]
ds_read_b64_tr_b16 a[112:113],v2 offset:160
ds_read_b64_tr_b16 a[114:115],v2 offset:5280
v_mfma_f32_16x16x32_bf16 a[192:195],a[132:135],v[160:163],a[192:195]
s_addk_i32 m0,4096
ds_read_b64_tr_b16 a[118:119],v2 offset:15520
v_mfma_f32_16x16x32_bf16 a[192:195],a[136:139],v[192:195],a[192:195]
ds_read_b64_tr_b16 a[122:123],v2 offset:25760
v_mfma_f32_16x16x32_bf16 a[192:195],a[140:143],v[224:227],a[192:195]
buffer_load_dwordx4 v[136:139],v6,s[28:31],s76 offen sc1
v_mfma_f32_16x16x32_bf16 a[196:199],a[140:143],v[228:231],a[196:199]
v_mfma_f32_16x16x32_bf16 a[196:199],a[136:139],v[196:199],a[196:199]
v_mfma_f32_16x16x32_bf16 a[196:199],a[132:135],v[164:167],a[196:199]
v_mfma_f32_16x16x32_bf16 a[196:199],a[128:131],v[132:135],a[196:199]
s_waitcnt vmcnt(14)
buffer_load_dwordx4 v11,s[72:75],s83 offen lds
v_mfma_f32_16x16x32_bf16 a[200:203],a[128:131],v[144:147],a[200:203]
ds_read_b64_tr_b16 a[128:129],v1 offset:192
ds_read_b64_tr_b16 a[130:131],v1 offset:5312
v_mfma_f32_16x16x32_bf16 a[200:203],a[132:135],v[176:179],a[200:203]
ds_read_b64_tr_b16 a[132:133],v1 offset:10432
v_mfma_f32_16x16x32_bf16 a[200:203],a[136:139],v[208:211],a[200:203]
ds_read_b64_tr_b16 a[134:135],v1 offset:15552
v_mfma_f32_16x16x32_bf16 a[200:203],a[140:143],v[240:243],a[200:203]
buffer_load_dwordx4 v[140:143],v6,s[28:31],s76 offen offset:1024 sc1
v_mfma_f32_16x16x32_bf16 a[208:211],a[144:147],v[128:131],a[208:211]
ds_read_b64_tr_b16 a[136:137],v1 offset:20672
ds_read_b64_tr_b16 a[138:139],v1 offset:25792
v_mfma_f32_16x16x32_bf16 a[208:211],a[148:151],v[160:163],a[208:211]
ds_read_b64_tr_b16 a[140:141],v1 offset:30912
v_mfma_f32_16x16x32_bf16 a[208:211],a[152:155],v[192:195],a[208:211]
ds_read_b64_tr_b16 a[142:143],v1 offset:36032
v_mfma_f32_16x16x32_bf16 a[208:211],a[156:159],v[224:227],a[208:211]
buffer_load_dwordx4 v[152:155],v6,s[28:31],s76 offen offset:2048 sc1
v_mfma_f32_16x16x32_bf16 a[212:215],a[156:159],v[228:231],a[212:215]
v_mfma_f32_16x16x32_bf16 a[212:215],a[152:155],v[196:199],a[212:215]
v_mfma_f32_16x16x32_bf16 a[212:215],a[148:151],v[164:167],a[212:215]
v_mfma_f32_16x16x32_bf16 a[212:215],a[144:147],v[132:135],a[212:215]
buffer_load_dwordx4 v[156:159],v6,s[28:31],s76 offen offset:3072 sc1
v_mfma_f32_16x16x32_bf16 a[216:219],a[144:147],v[144:147],a[216:219]
s_add_u32 s76,s70,3
ds_read_b64_tr_b16 a[144:145],v2 offset:224
ds_read_b64_tr_b16 a[146:147],v2 offset:5344
v_mfma_f32_16x16x32_bf16 a[216:219],a[148:151],v[176:179],a[216:219]
s_and_b32 s76,s76,3
s_lshl_b32 s76,s76,21
ds_read_b64_tr_b16 a[148:149],v2 offset:10464
v_mfma_f32_16x16x32_bf16 a[216:219],a[152:155],v[208:211],a[216:219]
s_add_u32 s76,s76,0
s_max_i32 s76,s76,s101
ds_read_b64_tr_b16 a[150:151],v2 offset:15584
v_mfma_f32_16x16x32_bf16 a[216:219],a[156:159],v[240:243],a[216:219]
buffer_load_dwordx4 v[168:171],v6,s[28:31],s77 offen sc1
v_mfma_f32_16x16x32_bf16 a[224:227],a[160:163],v[128:131],a[224:227]
ds_read_b64_tr_b16 a[152:153],v2 offset:20704
ds_read_b64_tr_b16 a[154:155],v2 offset:25824
v_mfma_f32_16x16x32_bf16 a[224:227],a[164:167],v[160:163],a[224:227]
ds_read_b64_tr_b16 a[156:157],v2 offset:30944
v_mfma_f32_16x16x32_bf16 a[224:227],a[168:171],v[192:195],a[224:227]
ds_read_b64_tr_b16 a[158:159],v2 offset:36064
v_mfma_f32_16x16x32_bf16 a[224:227],a[172:175],v[224:227],a[224:227]
buffer_load_dwordx4 v[172:175],v6,s[28:31],s77 offen offset:1024 sc1
v_mfma_f32_16x16x32_bf16 a[228:231],a[172:175],v[228:231],a[228:231]
v_mfma_f32_16x16x32_bf16 a[228:231],a[168:171],v[196:199],a[228:231]
v_mfma_f32_16x16x32_bf16 a[228:231],a[164:167],v[164:167],a[228:231]
v_mfma_f32_16x16x32_bf16 a[228:231],a[160:163],v[132:135],a[228:231]
v_mfma_f32_16x16x32_bf16 a[232:235],a[160:163],v[144:147],a[232:235]
buffer_load_dwordx4 v[184:187],v6,s[28:31],s77 offen offset:2048 sc1
v_mfma_f32_16x16x32_bf16 a[232:235],a[164:167],v[176:179],a[232:235]
ds_read_b64_tr_b16 a[160:161],v1 offset:256
ds_read_b64_tr_b16 a[162:163],v1 offset:5376
v_mfma_f32_16x16x32_bf16 a[232:235],a[168:171],v[208:211],a[232:235]
ds_read_b64_tr_b16 a[164:165],v1 offset:10496
v_mfma_f32_16x16x32_bf16 a[232:235],a[172:175],v[240:243],a[232:235]
s_waitcnt lgkmcnt(3)
ds_read_b64_tr_b16 a[166:167],v1 offset:15616
v_mfma_f32_16x16x32_bf16 a[240:243],a[176:179],v[128:131],a[240:243]
ds_read_b64_tr_b16 a[168:169],v1 offset:20736
v_mfma_f32_16x16x32_bf16 a[240:243],a[180:183],v[160:163],a[240:243]
buffer_load_dwordx4 v[188:191],v6,s[28:31],s77 offen offset:3072 sc1
v_mfma_f32_16x16x32_bf16 a[240:243],a[184:187],v[192:195],a[240:243]
s_add_u32 s77,s70,2
ds_read_b64_tr_b16 a[170:171],v1 offset:25856
ds_read_b64_tr_b16 a[172:173],v1 offset:30976
v_mfma_f32_16x16x32_bf16 a[240:243],a[188:191],v[224:227],a[240:243]
)A"
R"A(s_and_b32 s77,s77,3
s_lshl_b32 s77,s77,21
ds_read_b64_tr_b16 a[174:175],v1 offset:36096
v_mfma_f32_16x16x32_bf16 a[244:247],a[188:191],v[228:231],a[244:247]
s_add_u32 s77,s77,131072
s_max_i32 s77,s77,s101
v_mfma_f32_16x16x32_bf16 a[244:247],a[184:187],v[196:199],a[244:247]
v_mfma_f32_16x16x32_bf16 a[244:247],a[180:183],v[164:167],a[244:247]
buffer_load_dwordx4 v[200:203],v6,s[28:31],s78 offen sc1
v_mfma_f32_16x16x32_bf16 a[244:247],a[176:179],v[132:135],a[244:247]
v_mfma_f32_16x16x32_bf16 a[248:251],a[176:179],v[144:147],a[248:251]
ds_read_b64_tr_b16 a[176:177],v2 offset:288
ds_read_b64_tr_b16 a[178:179],v2 offset:5408
v_mfma_f32_16x16x32_bf16 a[248:251],a[180:183],v[176:179],a[248:251]
ds_read_b64_tr_b16 a[180:181],v2 offset:10528
v_mfma_f32_16x16x32_bf16 a[248:251],a[184:187],v[208:211],a[248:251]
ds_read_b64_tr_b16 a[182:183],v2 offset:15648
v_mfma_f32_16x16x32_bf16 a[248:251],a[188:191],v[240:243],a[248:251]
buffer_load_dwordx4 v[204:207],v6,s[28:31],s78 offen offset:1024 sc1
v_mfma_f32_16x16x32_bf16 a[0:3],a[96:99],v[128:131],a[0:3]
ds_read_b64_tr_b16 a[184:185],v2 offset:20768
ds_read_b64_tr_b16 a[186:187],v2 offset:25888
v_mfma_f32_16x16x32_bf16 a[0:3],a[100:103],v[160:163],a[0:3]
ds_read_b64_tr_b16 a[188:189],v2 offset:31008
v_mfma_f32_16x16x32_bf16 a[0:3],a[104:107],v[192:195],a[0:3]
ds_read_b64_tr_b16 a[190:191],v2 offset:36128
v_mfma_f32_16x16x32_bf16 a[0:3],a[108:111],v[224:227],a[0:3]
v_mfma_f32_16x16x32_bf16 a[4:7],a[108:111],v[228:231],a[4:7]
buffer_load_dwordx4 v[216:219],v6,s[28:31],s78 offen offset:2048 sc1
v_mfma_f32_16x16x32_bf16 a[4:7],a[104:107],v[196:199],a[4:7]
v_mfma_f32_16x16x32_bf16 a[4:7],a[100:103],v[164:167],a[4:7]
v_mfma_f32_16x16x32_bf16 a[4:7],a[96:99],v[132:135],a[4:7]
v_mfma_f32_16x16x32_bf16 a[8:11],a[96:99],v[144:147],a[8:11]
v_mfma_f32_16x16x32_bf16 a[8:11],a[100:103],v[176:179],a[8:11]
buffer_load_dwordx4 v[220:223],v6,s[28:31],s78 offen offset:3072 sc1
v_mfma_f32_16x16x32_bf16 a[8:11],a[104:107],v[208:211],a[8:11]
s_add_u32 s78,s70,1
s_and_b32 s78,s78,3
s_lshl_b32 s78,s78,21
v_mfma_f32_16x16x32_bf16 a[8:11],a[108:111],v[240:243],a[8:11]
s_add_u32 s78,s78,262144
s_max_i32 s78,s78,s101
v_mfma_f32_16x16x32_bf16 a[16:19],a[112:115],v[128:131],a[16:19]
v_mfma_f32_16x16x32_bf16 a[16:19],a[116:119],v[160:163],a[16:19]
v_mfma_f32_16x16x32_bf16 a[16:19],a[120:123],v[192:195],a[16:19]
buffer_load_dwordx4 v[232:235],v6,s[28:31],s79 offen sc1
v_mfma_f32_16x16x32_bf16 a[16:19],a[124:127],v[224:227],a[16:19]
v_mfma_f32_16x16x32_bf16 a[20:23],a[124:127],v[228:231],a[20:23]
v_mfma_f32_16x16x32_bf16 a[20:23],a[120:123],v[196:199],a[20:23]
v_mfma_f32_16x16x32_bf16 a[20:23],a[116:119],v[164:167],a[20:23]
v_mfma_f32_16x16x32_bf16 a[20:23],a[112:115],v[132:135],a[20:23]
buffer_load_dwordx4 v[236:239],v6,s[28:31],s79 offen offset:1024 sc1
v_mfma_f32_16x16x32_bf16 a[24:27],a[112:115],v[144:147],a[24:27]
v_mfma_f32_16x16x32_bf16 a[24:27],a[116:119],v[176:179],a[24:27]
v_mfma_f32_16x16x32_bf16 a[24:27],a[120:123],v[208:211],a[24:27]
v_mfma_f32_16x16x32_bf16 a[24:27],a[124:127],v[240:243],a[24:27]
v_mfma_f32_16x16x32_bf16 a[32:35],a[128:131],v[128:131],a[32:35]
buffer_load_dwordx4 v[248:251],v6,s[28:31],s79 offen offset:2048 sc1
v_mfma_f32_16x16x32_bf16 a[32:35],a[132:135],v[160:163],a[32:35]
v_mfma_f32_16x16x32_bf16 a[32:35],a[136:139],v[192:195],a[32:35]
v_mfma_f32_16x16x32_bf16 a[32:35],a[140:143],v[224:227],a[32:35]
v_mfma_f32_16x16x32_bf16 a[36:39],a[140:143],v[228:231],a[36:39]
v_mfma_f32_16x16x32_bf16 a[36:39],a[136:139],v[196:199],a[36:39]
buffer_load_dwordx4 v[252:255],v6,s[28:31],s79 offen offset:3072 sc1
v_mfma_f32_16x16x32_bf16 a[36:39],a[132:135],v[164:167],a[36:39]
v_mfma_f32_16x16x32_bf16 a[36:39],a[128:131],v[132:135],a[36:39]
v_mfma_f32_16x16x32_bf16 a[40:43],a[128:131],v[144:147],a[40:43]
v_mfma_f32_16x16x32_bf16 a[40:43],a[132:135],v[176:179],a[40:43]
v_mfma_f32_16x16x32_bf16 a[40:43],a[136:139],v[208:211],a[40:43]
s_waitcnt vmcnt(15)
v_mfma_f32_16x16x32_bf16 a[40:43],a[140:143],v[240:243],a[40:43]
s_barrier
ds_read_b64_tr_b16 a[96:97],v42
ds_read_b64_tr_b16 a[98:99],v42 offset:5120
v_mfma_f32_16x16x32_bf16 a[48:51],a[144:147],v[128:131],a[48:51]
ds_read_b64_tr_b16 a[100:101],v42 offset:10240
v_mfma_f32_16x16x32_bf16 a[48:51],a[148:151],v[160:163],a[48:51]
ds_read_b64_tr_b16 a[102:103],v42 offset:15360
v_mfma_f32_16x16x32_bf16 a[48:51],a[152:155],v[192:195],a[48:51]
ds_read_b64_tr_b16 a[104:105],v42 offset:20480
v_mfma_f32_16x16x32_bf16 a[48:51],a[156:159],v[224:227],a[48:51]
ds_read_b64_tr_b16 a[106:107],v42 offset:25600
ds_read_b64_tr_b16 a[108:109],v42 offset:30720
v_mfma_f32_16x16x32_bf16 a[52:55],a[156:159],v[228:231],a[52:55]
ds_read_b64_tr_b16 a[110:111],v42 offset:35840
v_mfma_f32_16x16x32_bf16 a[52:55],a[152:155],v[196:199],a[52:55]
ds_read_b64_tr_b16 a[112:113],v44
v_mfma_f32_16x16x32_bf16 a[52:55],a[148:151],v[164:167],a[52:55]
ds_read_b64_tr_b16 a[114:115],v44 offset:5120
v_mfma_f32_16x16x32_bf16 a[52:55],a[144:147],v[132:135],a[52:55]
ds_read_b64_tr_b16 a[116:117],v44 offset:10240
ds_read_b64_tr_b16 a[118:119],v44 offset:15360
v_mfma_f32_16x16x32_bf16 a[56:59],a[144:147],v[144:147],a[56:59]
ds_read_b64_tr_b16 a[120:121],v44 offset:20480
v_mfma_f32_16x16x32_bf16 a[56:59],a[148:151],v[176:179],a[56:59]
ds_read_b64_tr_b16 a[122:123],v44 offset:25600
v_mfma_f32_16x16x32_bf16 a[56:59],a[152:155],v[208:211],a[56:59]
ds_read_b64_tr_b16 a[124:125],v44 offset:30720
v_mfma_f32_16x16x32_bf16 a[56:59],a[156:159],v[240:243],a[56:59]
s_waitcnt lgkmcnt(8)
ds_read_b64_tr_b16 a[126:127],v44 offset:35840
ds_read_b64_tr_b16 a[128:129],v43 offset:32
v_mfma_f32_16x16x32_bf16 a[64:67],a[160:163],v[128:131],a[64:67]
ds_read_b64_tr_b16 a[130:131],v43 offset:5152
v_mfma_f32_16x16x32_bf16 a[64:67],a[164:167],v[160:163],a[64:67]
ds_read_b64_tr_b16 a[132:133],v43 offset:10272
v_mfma_f32_16x16x32_bf16 a[64:67],a[168:171],v[192:195],a[64:67]
ds_read_b64_tr_b16 a[134:135],v43 offset:15392
v_mfma_f32_16x16x32_bf16 a[64:67],a[172:175],v[224:227],a[64:67]
ds_read_b64_tr_b16 a[136:137],v43 offset:20512
ds_read_b64_tr_b16 a[138:139],v43 offset:25632
v_mfma_f32_16x16x32_bf16 a[68:71],a[172:175],v[228:231],a[68:71]
ds_read_b64_tr_b16 a[140:141],v43 offset:30752
v_mfma_f32_16x16x32_bf16 a[68:71],a[168:171],v[196:199],a[68:71]
ds_read_b64_tr_b16 a[142:143],v43 offset:35872
v_mfma_f32_16x16x32_bf16 a[68:71],a[164:167],v[164:167],a[68:71]
ds_read_b64_tr_b16 a[144:145],v45 offset:32
v_mfma_f32_16x16x32_bf16 a[68:71],a[160:163],v[132:135],a[68:71]
ds_read_b64_tr_b16 a[146:147],v45 offset:5152
ds_read_b64_tr_b16 a[148:149],v45 offset:10272
v_mfma_f32_16x16x32_bf16 a[72:75],a[160:163],v[144:147],a[72:75]
ds_read_b64_tr_b16 a[150:151],v45 offset:15392
v_mfma_f32_16x16x32_bf16 a[72:75],a[164:167],v[176:179],a[72:75]
ds_read_b64_tr_b16 a[152:153],v45 offset:20512
v_mfma_f32_16x16x32_bf16 a[72:75],a[168:171],v[208:211],a[72:75]
ds_read_b64_tr_b16 a[154:155],v45 offset:25632
v_mfma_f32_16x16x32_bf16 a[72:75],a[172:175],v[240:243],a[72:75]
ds_read_b64_tr_b16 a[156:157],v45 offset:30752
ds_read_b64_tr_b16 a[158:159],v45 offset:35872
v_mfma_f32_16x16x32_bf16 a[80:83],a[176:179],v[128:131],a[80:83]
ds_read_b64_tr_b16 a[160:161],v42 offset:64
v_mfma_f32_16x16x32_bf16 a[80:83],a[180:183],v[160:163],a[80:83]
ds_read_b64_tr_b16 a[162:163],v42 offset:5184
v_mfma_f32_16x16x32_bf16 a[80:83],a[184:187],v[192:195],a[80:83]
ds_read_b64_tr_b16 a[164:165],v42 offset:10304
v_mfma_f32_16x16x32_bf16 a[80:83],a[188:191],v[224:227],a[80:83]
ds_read_b64_tr_b16 a[166:167],v42 offset:15424
ds_read_b64_tr_b16 a[168:169],v42 offset:20544
v_mfma_f32_16x16x32_bf16 a[84:87],a[188:191],v[228:231],a[84:87]
ds_read_b64_tr_b16 a[170:171],v42 offset:25664
v_mfma_f32_16x16x32_bf16 a[84:87],a[184:187],v[196:199],a[84:87]
ds_read_b64_tr_b16 a[172:173],v42 offset:30784
v_mfma_f32_16x16x32_bf16 a[84:87],a[180:183],v[164:167],a[84:87]
ds_read_b64_tr_b16 a[174:175],v42 offset:35904
v_mfma_f32_16x16x32_bf16 a[84:87],a[176:179],v[132:135],a[84:87]
v_mfma_f32_16x16x32_bf16 a[88:91],a[176:179],v[144:147],a[88:91]
ds_read_b64_tr_b16 a[176:177],v44 offset:64
ds_read_b64_tr_b16 a[178:179],v44 offset:5184
v_mfma_f32_16x16x32_bf16 a[88:91],a[180:183],v[176:179],a[88:91]
ds_read_b64_tr_b16 a[180:181],v44 offset:10304
v_mfma_f32_16x16x32_bf16 a[88:91],a[184:187],v[208:211],a[88:91]
ds_read_b64_tr_b16 a[182:183],v44 offset:15424
v_mfma_f32_16x16x32_bf16 a[88:91],a[188:191],v[240:243],a[88:91]
ds_read_b64_tr_b16 a[184:185],v44 offset:20544
v_mfma_f32_16x16x32_bf16 a[192:195],a[96:99],v[64:67],a[192:195]
ds_read_b64_tr_b16 a[186:187],v44 offset:25664
ds_read_b64_tr_b16 a[188:189],v44 offset:30784
v_mfma_f32_16x16x32_bf16 a[192:195],a[100:103],v[68:71],a[192:195]
ds_read_b64_tr_b16 a[190:191],v44 offset:35904
v_mfma_f32_16x16x32_bf16 a[192:195],a[104:107],v[72:75],a[192:195]
s_waitcnt lgkmcnt(8)
v_mfma_f32_16x16x32_bf16 a[192:195],a[108:111],v[76:79],a[192:195]
v_mfma_f32_16x16x32_bf16 a[196:199],a[108:111],v[92:95],a[196:199]
v_mfma_f32_16x16x32_bf16 a[196:199],a[104:107],v[88:91],a[196:199]
v_mfma_f32_16x16x32_bf16 a[196:199],a[100:103],v[84:87],a[196:199]
v_mfma_f32_16x16x32_bf16 a[196:199],a[96:99],v[80:83],a[196:199]
v_mfma_f32_16x16x32_bf16 a[200:203],a[96:99],v[96:99],a[200:203]
ds_read_b64_tr_b16 a[96:97],v43 offset:96
ds_read_b64_tr_b16 a[98:99],v43 offset:5216
v_mfma_f32_16x16x32_bf16 a[200:203],a[100:103],v[100:103],a[200:203]
ds_read_b64_tr_b16 a[100:101],v43 offset:10336
v_mfma_f32_16x16x32_bf16 a[200:203],a[104:107],v[104:107],a[200:203]
ds_read_b64_tr_b16 a[102:103],v43 offset:15456
v_mfma_f32_16x16x32_bf16 a[200:203],a[108:111],v[108:111],a[200:203]
ds_read_b64_tr_b16 a[104:105],v43 offset:20576
v_mfma_f32_16x16x32_bf16 a[192:195],a[112:115],v[136:139],a[192:195]
s_waitcnt vmcnt(3)
ds_read_b64_tr_b16 a[106:107],v43 offset:25696
ds_read_b64_tr_b16 a[108:109],v43 offset:30816
v_mfma_f32_16x16x32_bf16 a[192:195],a[116:119],v[140:143],a[192:195]
ds_read_b64_tr_b16 a[110:111],v43 offset:35936
v_mfma_f32_16x16x32_bf16 a[192:195],a[120:123],v[152:155],a[192:195]
v_mfma_f32_16x16x32_bf16 a[192:195],a[124:127],v[156:159],a[192:195]
v_mfma_f32_16x16x32_bf16 a[196:199],a[124:127],v[188:191],a[196:199]
v_mfma_f32_16x16x32_bf16 a[196:199],a[120:123],v[184:187],a[196:199]
v_mfma_f32_16x16x32_bf16 a[196:199],a[116:119],v[172:175],a[196:199]
v_mfma_f32_16x16x32_bf16 a[196:199],a[112:115],v[168:171],a[196:199]
v_mfma_f32_16x16x32_bf16 a[200:203],a[112:115],v[200:203],a[200:203]
ds_read_b64_tr_b16 a[112:113],v45 offset:96
ds_read_b64_tr_b16 a[114:115],v45 offset:5216
v_mfma_f32_16x16x32_bf16 a[200:203],a[116:119],v[204:207],a[200:203]
v_accvgpr_read_b32 v128,a192
v_accvgpr_read_b32 v129,a193
v_mfma_f32_16x16x32_bf16 a[200:203],a[120:123],v[216:219],a[200:203]
v_cvt_pk_bf16_f32 v128,v128,v129
v_accvgpr_read_b32 v130,a194
v_mfma_f32_16x16x32_bf16 a[200:203],a[124:127],v[220:223],a[200:203]
v_accvgpr_read_b32 v131,a195
v_cvt_pk_bf16_f32 v129,v130,v131
v_mfma_f32_16x16x32_bf16 a[208:211],a[128:131],v[64:67],a[208:211]
ds_read_b64_tr_b16 a[116:117],v45 offset:10336
ds_read_b64_tr_b16 a[118:119],v45 offset:15456
v_mfma_f32_16x16x32_bf16 a[208:211],a[132:135],v[68:71],a[208:211]
v_accvgpr_read_b32 v132,a196
v_accvgpr_read_b32 v133,a197
v_mfma_f32_16x16x32_bf16 a[208:211],a[136:139],v[72:75],a[208:211]
v_cvt_pk_bf16_f32 v132,v132,v133
v_accvgpr_read_b32 v134,a198
v_mfma_f32_16x16x32_bf16 a[208:211],a[140:143],v[76:79],a[208:211]
v_accvgpr_read_b32 v135,a199
v_cvt_pk_bf16_f32 v133,v134,v135
v_mfma_f32_16x16x32_bf16 a[212:215],a[140:143],v[92:95],a[212:215]
ds_read_b64_tr_b16 a[120:121],v45 offset:20576
ds_read_b64_tr_b16 a[122:123],v45 offset:25696
v_mfma_f32_16x16x32_bf16 a[212:215],a[136:139],v[88:91],a[212:215]
ds_read_b64_tr_b16 a[124:125],v45 offset:30816
v_accvgpr_read_b32 v144,a200
v_mfma_f32_16x16x32_bf16 a[212:215],a[132:135],v[84:87],a[212:215]
ds_read_b64_tr_b16 a[126:127],v45 offset:35936
v_accvgpr_read_b32 v145,a201
v_mfma_f32_16x16x32_bf16 a[212:215],a[128:131],v[80:83],a[212:215]
v_cvt_pk_bf16_f32 v144,v144,v145
v_accvgpr_read_b32 v146,a202
)A"
R"A(v_mfma_f32_16x16x32_bf16 a[216:219],a[128:131],v[96:99],a[216:219]
v_accvgpr_read_b32 v147,a203
v_cvt_pk_bf16_f32 v145,v146,v147
v_mfma_f32_16x16x32_bf16 a[216:219],a[132:135],v[100:103],a[216:219]
ds_read_b64_tr_b16 a[128:129],v42 offset:128
ds_read_b64_tr_b16 a[130:131],v42 offset:5248
v_mfma_f32_16x16x32_bf16 a[216:219],a[136:139],v[104:107],a[216:219]
ds_read_b64_tr_b16 a[132:133],v42 offset:10368
v_mfma_f32_16x16x32_bf16 a[216:219],a[140:143],v[108:111],a[216:219]
ds_read_b64_tr_b16 a[134:135],v42 offset:15488
v_mfma_f32_16x16x32_bf16 a[208:211],a[144:147],v[136:139],a[208:211]
ds_read_b64_tr_b16 a[136:137],v42 offset:20608
v_mfma_f32_16x16x32_bf16 a[208:211],a[148:151],v[140:143],a[208:211]
ds_read_b64_tr_b16 a[138:139],v42 offset:25728
ds_read_b64_tr_b16 a[140:141],v42 offset:30848
v_mfma_f32_16x16x32_bf16 a[208:211],a[152:155],v[152:155],a[208:211]
ds_read_b64_tr_b16 a[142:143],v42 offset:35968
v_mfma_f32_16x16x32_bf16 a[208:211],a[156:159],v[156:159],a[208:211]
v_mfma_f32_16x16x32_bf16 a[212:215],a[156:159],v[188:191],a[212:215]
v_mfma_f32_16x16x32_bf16 a[212:215],a[152:155],v[184:187],a[212:215]
v_mfma_f32_16x16x32_bf16 a[212:215],a[148:151],v[172:175],a[212:215]
v_mfma_f32_16x16x32_bf16 a[212:215],a[144:147],v[168:171],a[212:215]
v_mfma_f32_16x16x32_bf16 a[216:219],a[144:147],v[200:203],a[216:219]
ds_read_b64_tr_b16 a[144:145],v44 offset:128
ds_read_b64_tr_b16 a[146:147],v44 offset:5248
v_mfma_f32_16x16x32_bf16 a[216:219],a[148:151],v[204:207],a[216:219]
v_accvgpr_read_b32 v130,a208
v_accvgpr_read_b32 v131,a209
v_mfma_f32_16x16x32_bf16 a[216:219],a[152:155],v[216:219],a[216:219]
v_accvgpr_read_b32 v14,a210
v_accvgpr_read_b32 v15,a211
v_mfma_f32_16x16x32_bf16 a[216:219],a[156:159],v[220:223],a[216:219]
v_cvt_pk_bf16_f32 v130,v130,v131
v_cvt_pk_bf16_f32 v131,v14,v15
v_mfma_f32_16x16x32_bf16 a[224:227],a[160:163],v[64:67],a[224:227]
buffer_store_dwordx4 v[128:131],v6,s[32:35],s84 offen sc1
v_mfma_f32_16x16x32_bf16 a[224:227],a[164:167],v[68:71],a[224:227]
v_accvgpr_read_b32 v134,a212
v_accvgpr_read_b32 v135,a213
v_mfma_f32_16x16x32_bf16 a[224:227],a[168:171],v[72:75],a[224:227]
v_accvgpr_read_b32 v16,a214
v_accvgpr_read_b32 v17,a215
v_mfma_f32_16x16x32_bf16 a[224:227],a[172:175],v[76:79],a[224:227]
v_cvt_pk_bf16_f32 v134,v134,v135
v_cvt_pk_bf16_f32 v135,v16,v17
v_mfma_f32_16x16x32_bf16 a[228:231],a[172:175],v[92:95],a[228:231]
buffer_store_dwordx4 v[132:135],v6,s[32:35],s85 offen sc1
v_mfma_f32_16x16x32_bf16 a[228:231],a[168:171],v[88:91],a[228:231]
v_accvgpr_read_b32 v146,a216
v_accvgpr_read_b32 v147,a217
v_mfma_f32_16x16x32_bf16 a[228:231],a[164:167],v[84:87],a[228:231]
v_accvgpr_read_b32 v12,a218
v_accvgpr_read_b32 v13,a219
v_mfma_f32_16x16x32_bf16 a[228:231],a[160:163],v[80:83],a[228:231]
v_cvt_pk_bf16_f32 v146,v146,v147
v_cvt_pk_bf16_f32 v147,v12,v13
v_mfma_f32_16x16x32_bf16 a[232:235],a[160:163],v[96:99],a[232:235]
buffer_store_dwordx4 v[144:147],v6,s[32:35],s86 offen sc1
v_mfma_f32_16x16x32_bf16 a[232:235],a[164:167],v[100:103],a[232:235]
ds_read_b64_tr_b16 a[148:149],v44 offset:10368
ds_read_b64_tr_b16 a[150:151],v44 offset:15488
v_mfma_f32_16x16x32_bf16 a[232:235],a[168:171],v[104:107],a[232:235]
ds_read_b64_tr_b16 a[152:153],v44 offset:20608
v_mfma_f32_16x16x32_bf16 a[232:235],a[172:175],v[108:111],a[232:235]
s_waitcnt lgkmcnt(3)
ds_read_b64_tr_b16 a[154:155],v44 offset:25728
v_mfma_f32_16x16x32_bf16 a[224:227],a[176:179],v[136:139],a[224:227]
ds_read_b64_tr_b16 a[156:157],v44 offset:30848
v_mfma_f32_16x16x32_bf16 a[224:227],a[180:183],v[140:143],a[224:227]
ds_read_b64_tr_b16 a[158:159],v44 offset:35968
ds_read_b64_tr_b16 a[160:161],v43 offset:160
v_mfma_f32_16x16x32_bf16 a[224:227],a[184:187],v[152:155],a[224:227]
ds_read_b64_tr_b16 a[162:163],v43 offset:5280
v_mfma_f32_16x16x32_bf16 a[224:227],a[188:191],v[156:159],a[224:227]
ds_read_b64_tr_b16 a[164:165],v43 offset:10400
v_mfma_f32_16x16x32_bf16 a[228:231],a[188:191],v[188:191],a[228:231]
ds_read_b64_tr_b16 a[166:167],v43 offset:15520
v_mfma_f32_16x16x32_bf16 a[228:231],a[184:187],v[184:187],a[228:231]
ds_read_b64_tr_b16 a[168:169],v43 offset:20640
ds_read_b64_tr_b16 a[170:171],v43 offset:25760
v_mfma_f32_16x16x32_bf16 a[228:231],a[180:183],v[172:175],a[228:231]
ds_read_b64_tr_b16 a[172:173],v43 offset:30880
v_mfma_f32_16x16x32_bf16 a[228:231],a[176:179],v[168:171],a[228:231]
ds_read_b64_tr_b16 a[174:175],v43 offset:36000
v_mfma_f32_16x16x32_bf16 a[232:235],a[176:179],v[200:203],a[232:235]
ds_read_b64_tr_b16 a[176:177],v45 offset:160
v_mfma_f32_16x16x32_bf16 a[232:235],a[180:183],v[204:207],a[232:235]
v_accvgpr_read_b32 v160,a224
v_accvgpr_read_b32 v161,a225
v_mfma_f32_16x16x32_bf16 a[232:235],a[184:187],v[216:219],a[232:235]
v_cvt_pk_bf16_f32 v160,v160,v161
v_accvgpr_read_b32 v162,a226
v_mfma_f32_16x16x32_bf16 a[232:235],a[188:191],v[220:223],a[232:235]
v_accvgpr_read_b32 v163,a227
v_cvt_pk_bf16_f32 v161,v162,v163
v_mfma_f32_16x16x32_bf16 a[240:243],a[96:99],v[64:67],a[240:243]
ds_read_b64_tr_b16 a[178:179],v45 offset:5280
ds_read_b64_tr_b16 a[180:181],v45 offset:10400
v_mfma_f32_16x16x32_bf16 a[240:243],a[100:103],v[68:71],a[240:243]
v_accvgpr_read_b32 v164,a228
v_accvgpr_read_b32 v165,a229
v_mfma_f32_16x16x32_bf16 a[240:243],a[104:107],v[72:75],a[240:243]
v_cvt_pk_bf16_f32 v164,v164,v165
v_accvgpr_read_b32 v166,a230
v_mfma_f32_16x16x32_bf16 a[240:243],a[108:111],v[76:79],a[240:243]
v_accvgpr_read_b32 v167,a231
v_cvt_pk_bf16_f32 v165,v166,v167
v_mfma_f32_16x16x32_bf16 a[244:247],a[108:111],v[92:95],a[244:247]
ds_read_b64_tr_b16 a[182:183],v45 offset:15520
ds_read_b64_tr_b16 a[184:185],v45 offset:20640
v_mfma_f32_16x16x32_bf16 a[244:247],a[104:107],v[88:91],a[244:247]
v_accvgpr_read_b32 v176,a232
v_accvgpr_read_b32 v177,a233
v_mfma_f32_16x16x32_bf16 a[244:247],a[100:103],v[84:87],a[244:247]
v_cvt_pk_bf16_f32 v176,v176,v177
v_accvgpr_read_b32 v178,a234
v_mfma_f32_16x16x32_bf16 a[244:247],a[96:99],v[80:83],a[244:247]
v_accvgpr_read_b32 v179,a235
v_cvt_pk_bf16_f32 v177,v178,v179
v_mfma_f32_16x16x32_bf16 a[248:251],a[96:99],v[96:99],a[248:251]
ds_read_b64_tr_b16 a[186:187],v45 offset:25760
ds_read_b64_tr_b16 a[188:189],v45 offset:30880
v_mfma_f32_16x16x32_bf16 a[248:251],a[100:103],v[100:103],a[248:251]
ds_read_b64_tr_b16 a[190:191],v45 offset:36000
v_mfma_f32_16x16x32_bf16 a[248:251],a[104:107],v[104:107],a[248:251]
ds_read_b64_tr_b16 a[96:97],v42 offset:192
v_mfma_f32_16x16x32_bf16 a[248:251],a[108:111],v[108:111],a[248:251]
ds_read_b64_tr_b16 a[98:99],v42 offset:5312
v_mfma_f32_16x16x32_bf16 a[240:243],a[112:115],v[136:139],a[240:243]
ds_read_b64_tr_b16 a[100:101],v42 offset:10432
ds_read_b64_tr_b16 a[102:103],v42 offset:15552
v_mfma_f32_16x16x32_bf16 a[240:243],a[116:119],v[140:143],a[240:243]
ds_read_b64_tr_b16 a[104:105],v42 offset:20672
v_mfma_f32_16x16x32_bf16 a[240:243],a[120:123],v[152:155],a[240:243]
ds_read_b64_tr_b16 a[106:107],v42 offset:25792
v_mfma_f32_16x16x32_bf16 a[240:243],a[124:127],v[156:159],a[240:243]
ds_read_b64_tr_b16 a[108:109],v42 offset:30912
v_mfma_f32_16x16x32_bf16 a[244:247],a[124:127],v[188:191],a[244:247]
ds_read_b64_tr_b16 a[110:111],v42 offset:36032
v_mfma_f32_16x16x32_bf16 a[244:247],a[120:123],v[184:187],a[244:247]
v_mfma_f32_16x16x32_bf16 a[244:247],a[116:119],v[172:175],a[244:247]
v_mfma_f32_16x16x32_bf16 a[244:247],a[112:115],v[168:171],a[244:247]
v_mfma_f32_16x16x32_bf16 a[248:251],a[112:115],v[200:203],a[248:251]
ds_read_b64_tr_b16 a[112:113],v44 offset:192
ds_read_b64_tr_b16 a[114:115],v44 offset:5312
v_mfma_f32_16x16x32_bf16 a[248:251],a[116:119],v[204:207],a[248:251]
v_accvgpr_read_b32 v162,a240
v_accvgpr_read_b32 v163,a241
v_mfma_f32_16x16x32_bf16 a[248:251],a[120:123],v[216:219],a[248:251]
v_accvgpr_read_b32 v14,a242
v_accvgpr_read_b32 v15,a243
v_mfma_f32_16x16x32_bf16 a[248:251],a[124:127],v[220:223],a[248:251]
v_cvt_pk_bf16_f32 v162,v162,v163
v_cvt_pk_bf16_f32 v163,v14,v15
v_mfma_f32_16x16x32_bf16 a[0:3],a[128:131],v[64:67],a[0:3]
buffer_store_dwordx4 v[160:163],v6,s[32:35],s84 offen offset:1024 sc1
v_mfma_f32_16x16x32_bf16 a[0:3],a[132:135],v[68:71],a[0:3]
v_accvgpr_read_b32 v166,a244
v_accvgpr_read_b32 v167,a245
v_mfma_f32_16x16x32_bf16 a[0:3],a[136:139],v[72:75],a[0:3]
v_accvgpr_read_b32 v16,a246
v_accvgpr_read_b32 v17,a247
v_mfma_f32_16x16x32_bf16 a[0:3],a[140:143],v[76:79],a[0:3]
v_cvt_pk_bf16_f32 v166,v166,v167
v_cvt_pk_bf16_f32 v167,v16,v17
v_mfma_f32_16x16x32_bf16 a[4:7],a[140:143],v[92:95],a[4:7]
buffer_store_dwordx4 v[164:167],v6,s[32:35],s85 offen offset:1024 sc1
v_mfma_f32_16x16x32_bf16 a[4:7],a[136:139],v[88:91],a[4:7]
v_accvgpr_read_b32 v178,a248
v_accvgpr_read_b32 v179,a249
v_mfma_f32_16x16x32_bf16 a[4:7],a[132:135],v[84:87],a[4:7]
.p2align 3
v_accvgpr_read_b32 v12,a250
v_accvgpr_read_b32 v13,a251
v_cvt_pk_bf16_f32 v178,v178,v179
v_cvt_pk_bf16_f32 v179,v12,v13
v_mfma_f32_16x16x32_bf16 a[4:7],a[128:131],v[80:83],a[4:7]
ds_read_b64_tr_b16 a[116:117],v44 offset:10432
ds_read_b64_tr_b16 a[118:119],v44 offset:15552
v_mfma_f32_16x16x32_bf16 a[8:11],a[128:131],v[96:99],a[8:11]
buffer_store_dwordx4 v[176:179],v6,s[32:35],s86 offen offset:1024 sc1
v_mfma_f32_16x16x32_bf16 a[8:11],a[132:135],v[100:103],a[8:11]
ds_read_b64_tr_b16 a[120:121],v44 offset:20672
ds_read_b64_tr_b16 a[122:123],v44 offset:25792
v_mfma_f32_16x16x32_bf16 a[8:11],a[136:139],v[104:107],a[8:11]
ds_read_b64_tr_b16 a[124:125],v44 offset:30912
v_mfma_f32_16x16x32_bf16 a[8:11],a[140:143],v[108:111],a[8:11]
ds_read_b64_tr_b16 a[126:127],v44 offset:36032
v_mfma_f32_16x16x32_bf16 a[0:3],a[144:147],v[136:139],a[0:3]
s_waitcnt lgkmcnt(6)
ds_read_b64_tr_b16 a[128:129],v43 offset:224
v_mfma_f32_16x16x32_bf16 a[0:3],a[148:151],v[140:143],a[0:3]
ds_read_b64_tr_b16 a[130:131],v43 offset:5344
ds_read_b64_tr_b16 a[132:133],v43 offset:10464
v_mfma_f32_16x16x32_bf16 a[0:3],a[152:155],v[152:155],a[0:3]
ds_read_b64_tr_b16 a[134:135],v43 offset:15584
v_mfma_f32_16x16x32_bf16 a[0:3],a[156:159],v[156:159],a[0:3]
ds_read_b64_tr_b16 a[136:137],v43 offset:20704
v_mfma_f32_16x16x32_bf16 a[4:7],a[156:159],v[188:191],a[4:7]
ds_read_b64_tr_b16 a[138:139],v43 offset:25824
v_mfma_f32_16x16x32_bf16 a[4:7],a[152:155],v[184:187],a[4:7]
ds_read_b64_tr_b16 a[140:141],v43 offset:30944
ds_read_b64_tr_b16 a[142:143],v43 offset:36064
v_mfma_f32_16x16x32_bf16 a[4:7],a[148:151],v[172:175],a[4:7]
v_mfma_f32_16x16x32_bf16 a[4:7],a[144:147],v[168:171],a[4:7]
v_mfma_f32_16x16x32_bf16 a[8:11],a[144:147],v[200:203],a[8:11]
ds_read_b64_tr_b16 a[144:145],v45 offset:224
ds_read_b64_tr_b16 a[146:147],v45 offset:5344
v_mfma_f32_16x16x32_bf16 a[8:11],a[148:151],v[204:207],a[8:11]
v_accvgpr_read_b32 v192,a0
v_accvgpr_read_b32 v193,a1
v_mfma_f32_16x16x32_bf16 a[8:11],a[152:155],v[216:219],a[8:11]
v_cvt_pk_bf16_f32 v192,v192,v193
v_accvgpr_read_b32 v194,a2
v_mfma_f32_16x16x32_bf16 a[8:11],a[156:159],v[220:223],a[8:11]
v_accvgpr_read_b32 v195,a3
v_cvt_pk_bf16_f32 v193,v194,v195
v_mfma_f32_16x16x32_bf16 a[16:19],a[160:163],v[64:67],a[16:19]
ds_read_b64_tr_b16 a[148:149],v45 offset:10464
ds_read_b64_tr_b16 a[150:151],v45 offset:15584
v_mfma_f32_16x16x32_bf16 a[16:19],a[164:167],v[68:71],a[16:19]
v_accvgpr_read_b32 v196,a4
v_accvgpr_read_b32 v197,a5
v_mfma_f32_16x16x32_bf16 a[16:19],a[168:171],v[72:75],a[16:19]
v_cvt_pk_bf16_f32 v196,v196,v197
v_accvgpr_read_b32 v198,a6
v_mfma_f32_16x16x32_bf16 a[16:19],a[172:175],v[76:79],a[16:19]
v_accvgpr_read_b32 v199,a7
v_cvt_pk_bf16_f32 v197,v198,v199
v_mfma_f32_16x16x32_bf16 a[20:23],a[172:175],v[92:95],a[20:23]
ds_read_b64_tr_b16 a[152:153],v45 offset:20704
ds_read_b64_tr_b16 a[154:155],v45 offset:25824
v_mfma_f32_16x16x32_bf16 a[20:23],a[168:171],v[88:91],a[20:23]
ds_read_b64_tr_b16 a[156:157],v45 offset:30944
v_accvgpr_read_b32 v208,a8
v_mfma_f32_16x16x32_bf16 a[20:23],a[164:167],v[84:87],a[20:23]
ds_read_b64_tr_b16 a[158:159],v45 offset:36064
v_accvgpr_read_b32 v209,a9
v_mfma_f32_16x16x32_bf16 a[20:23],a[160:163],v[80:83],a[20:23]
v_cvt_pk_bf16_f32 v208,v208,v209
v_accvgpr_read_b32 v210,a10
v_mfma_f32_16x16x32_bf16 a[24:27],a[160:163],v[96:99],a[24:27]
v_accvgpr_read_b32 v211,a11
v_cvt_pk_bf16_f32 v209,v210,v211
)A"
R"A(v_mfma_f32_16x16x32_bf16 a[24:27],a[164:167],v[100:103],a[24:27]
ds_read_b64_tr_b16 a[160:161],v42 offset:256
ds_read_b64_tr_b16 a[162:163],v42 offset:5376
v_mfma_f32_16x16x32_bf16 a[24:27],a[168:171],v[104:107],a[24:27]
ds_read_b64_tr_b16 a[164:165],v42 offset:10496
v_mfma_f32_16x16x32_bf16 a[24:27],a[172:175],v[108:111],a[24:27]
ds_read_b64_tr_b16 a[166:167],v42 offset:15616
v_mfma_f32_16x16x32_bf16 a[16:19],a[176:179],v[136:139],a[16:19]
ds_read_b64_tr_b16 a[168:169],v42 offset:20736
v_mfma_f32_16x16x32_bf16 a[16:19],a[180:183],v[140:143],a[16:19]
ds_read_b64_tr_b16 a[170:171],v42 offset:25856
ds_read_b64_tr_b16 a[172:173],v42 offset:30976
v_mfma_f32_16x16x32_bf16 a[16:19],a[184:187],v[152:155],a[16:19]
ds_read_b64_tr_b16 a[174:175],v42 offset:36096
v_mfma_f32_16x16x32_bf16 a[16:19],a[188:191],v[156:159],a[16:19]
v_mfma_f32_16x16x32_bf16 a[20:23],a[188:191],v[188:191],a[20:23]
v_mfma_f32_16x16x32_bf16 a[20:23],a[184:187],v[184:187],a[20:23]
v_mfma_f32_16x16x32_bf16 a[20:23],a[180:183],v[172:175],a[20:23]
v_mfma_f32_16x16x32_bf16 a[20:23],a[176:179],v[168:171],a[20:23]
v_mfma_f32_16x16x32_bf16 a[24:27],a[176:179],v[200:203],a[24:27]
ds_read_b64_tr_b16 a[176:177],v44 offset:256
ds_read_b64_tr_b16 a[178:179],v44 offset:5376
v_mfma_f32_16x16x32_bf16 a[24:27],a[180:183],v[204:207],a[24:27]
s_lshl_b32 s95,s42,2
s_add_u32 s95,s95,s40
s_lshl_b32 s95,s95,4
v_mfma_f32_16x16x32_bf16 a[24:27],a[184:187],v[216:219],a[24:27]
s_addk_i32 s95,5120
v_bfe_u32 v19,s55,13,1
v_mov_b32 v18,s68
v_or_b32 v19,s69,v19
v_mfma_f32_16x16x32_bf16 a[24:27],a[188:191],v[220:223],a[24:27]
s_waitcnt vmcnt(0)
s_mov_b64 exec,1
buffer_store_dwordx2 v[18:19],v6,s[64:67],s95 offen sc1
s_mov_b64 exec,-1
v_mfma_f32_16x16x32_bf16 a[32:35],a[96:99],v[64:67],a[32:35]
v_accvgpr_read_b32 v194,a16
v_accvgpr_read_b32 v195,a17
v_mfma_f32_16x16x32_bf16 a[32:35],a[100:103],v[68:71],a[32:35]
v_accvgpr_read_b32 v14,a18
v_accvgpr_read_b32 v15,a19
v_mfma_f32_16x16x32_bf16 a[32:35],a[104:107],v[72:75],a[32:35]
v_cvt_pk_bf16_f32 v194,v194,v195
v_cvt_pk_bf16_f32 v195,v14,v15
v_mfma_f32_16x16x32_bf16 a[32:35],a[108:111],v[76:79],a[32:35]
buffer_store_dwordx4 v[192:195],v6,s[32:35],s84 offen offset:2048 sc1
v_mfma_f32_16x16x32_bf16 a[36:39],a[108:111],v[92:95],a[36:39]
v_accvgpr_read_b32 v198,a20
v_accvgpr_read_b32 v199,a21
v_mfma_f32_16x16x32_bf16 a[36:39],a[104:107],v[88:91],a[36:39]
v_accvgpr_read_b32 v16,a22
v_accvgpr_read_b32 v17,a23
v_mfma_f32_16x16x32_bf16 a[36:39],a[100:103],v[84:87],a[36:39]
v_cvt_pk_bf16_f32 v198,v198,v199
v_cvt_pk_bf16_f32 v199,v16,v17
v_mfma_f32_16x16x32_bf16 a[36:39],a[96:99],v[80:83],a[36:39]
buffer_store_dwordx4 v[196:199],v6,s[32:35],s85 offen offset:2048 sc1
v_mfma_f32_16x16x32_bf16 a[40:43],a[96:99],v[96:99],a[40:43]
v_accvgpr_read_b32 v210,a24
v_accvgpr_read_b32 v211,a25
v_mfma_f32_16x16x32_bf16 a[40:43],a[100:103],v[100:103],a[40:43]
v_accvgpr_read_b32 v12,a26
v_accvgpr_read_b32 v13,a27
v_mfma_f32_16x16x32_bf16 a[40:43],a[104:107],v[104:107],a[40:43]
v_cvt_pk_bf16_f32 v210,v210,v211
v_cvt_pk_bf16_f32 v211,v12,v13
v_mfma_f32_16x16x32_bf16 a[40:43],a[108:111],v[108:111],a[40:43]
buffer_store_dwordx4 v[208:211],v6,s[32:35],s86 offen offset:2048 sc1
v_mfma_f32_16x16x32_bf16 a[32:35],a[112:115],v[136:139],a[32:35]
s_waitcnt lgkmcnt(0)
ds_read_b64_tr_b16 a[180:181],v44 offset:10496
ds_read_b64_tr_b16 a[182:183],v44 offset:15616
v_mfma_f32_16x16x32_bf16 a[32:35],a[116:119],v[140:143],a[32:35]
ds_read_b64_tr_b16 a[184:185],v44 offset:20736
v_mfma_f32_16x16x32_bf16 a[32:35],a[120:123],v[152:155],a[32:35]
ds_read_b64_tr_b16 a[186:187],v44 offset:25856
v_mfma_f32_16x16x32_bf16 a[32:35],a[124:127],v[156:159],a[32:35]
ds_read_b64_tr_b16 a[188:189],v44 offset:30976
v_mfma_f32_16x16x32_bf16 a[36:39],a[124:127],v[188:191],a[36:39]
ds_read_b64_tr_b16 a[190:191],v44 offset:36096
ds_read_b64_tr_b16 a[96:97],v43 offset:288
v_mfma_f32_16x16x32_bf16 a[36:39],a[120:123],v[184:187],a[36:39]
ds_read_b64_tr_b16 a[98:99],v43 offset:5408
v_mfma_f32_16x16x32_bf16 a[36:39],a[116:119],v[172:175],a[36:39]
ds_read_b64_tr_b16 a[100:101],v43 offset:10528
v_mfma_f32_16x16x32_bf16 a[36:39],a[112:115],v[168:171],a[36:39]
ds_read_b64_tr_b16 a[102:103],v43 offset:15648
v_mfma_f32_16x16x32_bf16 a[40:43],a[112:115],v[200:203],a[40:43]
ds_read_b64_tr_b16 a[104:105],v43 offset:20768
ds_read_b64_tr_b16 a[106:107],v43 offset:25888
v_mfma_f32_16x16x32_bf16 a[40:43],a[116:119],v[204:207],a[40:43]
v_accvgpr_read_b32 v224,a32
v_accvgpr_read_b32 v225,a33
v_mfma_f32_16x16x32_bf16 a[40:43],a[120:123],v[216:219],a[40:43]
s_lshl_b32 s94,s71,2
v_cvt_pk_bf16_f32 v224,v224,v225
v_accvgpr_read_b32 v226,a34
v_mfma_f32_16x16x32_bf16 a[40:43],a[124:127],v[220:223],a[40:43]
s_add_u32 s94,s94,s40
v_accvgpr_read_b32 v227,a35
v_cvt_pk_bf16_f32 v225,v226,v227
v_mfma_f32_16x16x32_bf16 a[48:51],a[128:131],v[64:67],a[48:51]
s_lshl_b32 s94,s94,4
v_bfe_u32 v24,v6,4,2
v_min_u32 v24,2,v24
v_mfma_f32_16x16x32_bf16 a[48:51],a[132:135],v[68:71],a[48:51]
s_addk_i32 s94,5120
v_accvgpr_read_b32 v228,a36
v_accvgpr_read_b32 v229,a37
v_mfma_f32_16x16x32_bf16 a[48:51],a[136:139],v[72:75],a[48:51]
v_cvt_pk_bf16_f32 v228,v228,v229
v_accvgpr_read_b32 v230,a38
v_mfma_f32_16x16x32_bf16 a[48:51],a[140:143],v[76:79],a[48:51]
v_accvgpr_read_b32 v231,a39
v_cvt_pk_bf16_f32 v229,v230,v231
v_mfma_f32_16x16x32_bf16 a[52:55],a[140:143],v[92:95],a[52:55]
v_add_u32 v24,s70,v24
v_add_u32 v24,1,v24
v_mfma_f32_16x16x32_bf16 a[52:55],a[136:139],v[88:91],a[52:55]
v_accvgpr_read_b32 v240,a40
v_accvgpr_read_b32 v241,a41
v_mfma_f32_16x16x32_bf16 a[52:55],a[132:135],v[84:87],a[52:55]
v_cvt_pk_bf16_f32 v240,v240,v241
v_accvgpr_read_b32 v242,a42
v_mfma_f32_16x16x32_bf16 a[52:55],a[128:131],v[80:83],a[52:55]
v_accvgpr_read_b32 v243,a43
v_cvt_pk_bf16_f32 v241,v242,v243
v_mfma_f32_16x16x32_bf16 a[56:59],a[128:131],v[96:99],a[56:59]
v_and_b32 v24,3,v24
v_lshlrev_b32 v24,8,v24
v_mfma_f32_16x16x32_bf16 a[56:59],a[132:135],v[100:103],a[56:59]
ds_read_b64_tr_b16 a[108:109],v43 offset:31008
ds_read_b64_tr_b16 a[110:111],v43 offset:36128
v_mfma_f32_16x16x32_bf16 a[56:59],a[136:139],v[104:107],a[56:59]
ds_read_b64_tr_b16 a[112:113],v45 offset:288
v_mfma_f32_16x16x32_bf16 a[56:59],a[140:143],v[108:111],a[56:59]
ds_read_b64_tr_b16 a[114:115],v45 offset:5408
v_mfma_f32_16x16x32_bf16 a[48:51],a[144:147],v[136:139],a[48:51]
ds_read_b64_tr_b16 a[116:117],v45 offset:10528
v_mfma_f32_16x16x32_bf16 a[48:51],a[148:151],v[140:143],a[48:51]
ds_read_b64_tr_b16 a[118:119],v45 offset:15648
ds_read_b64_tr_b16 a[120:121],v45 offset:20768
v_mfma_f32_16x16x32_bf16 a[48:51],a[152:155],v[152:155],a[48:51]
ds_read_b64_tr_b16 a[122:123],v45 offset:25888
v_mfma_f32_16x16x32_bf16 a[48:51],a[156:159],v[156:159],a[48:51]
ds_read_b64_tr_b16 a[124:125],v45 offset:31008
v_mfma_f32_16x16x32_bf16 a[52:55],a[156:159],v[188:191],a[52:55]
ds_read_b64_tr_b16 a[126:127],v45 offset:36128
v_mfma_f32_16x16x32_bf16 a[52:55],a[152:155],v[184:187],a[52:55]
ds_read_b64_tr_b16 a[128:129],v1
ds_read_b64_tr_b16 a[130:131],v1 offset:5120
v_mfma_f32_16x16x32_bf16 a[52:55],a[148:151],v[172:175],a[52:55]
ds_read_b64_tr_b16 a[132:133],v1 offset:10240
v_mfma_f32_16x16x32_bf16 a[52:55],a[144:147],v[168:171],a[52:55]
ds_read_b64_tr_b16 a[134:135],v1 offset:15360
v_mfma_f32_16x16x32_bf16 a[56:59],a[144:147],v[200:203],a[56:59]
ds_read_b64_tr_b16 a[136:137],v1 offset:20480
v_mfma_f32_16x16x32_bf16 a[56:59],a[148:151],v[204:207],a[56:59]
v_accvgpr_read_b32 v226,a48
v_accvgpr_read_b32 v227,a49
v_mfma_f32_16x16x32_bf16 a[56:59],a[152:155],v[216:219],a[56:59]
v_accvgpr_read_b32 v14,a50
v_accvgpr_read_b32 v15,a51
v_mfma_f32_16x16x32_bf16 a[56:59],a[156:159],v[220:223],a[56:59]
v_cvt_pk_bf16_f32 v226,v226,v227
v_cvt_pk_bf16_f32 v227,v14,v15
v_mfma_f32_16x16x32_bf16 a[64:67],a[160:163],v[64:67],a[64:67]
buffer_store_dwordx4 v[224:227],v6,s[32:35],s84 offen offset:3072 sc1
v_mfma_f32_16x16x32_bf16 a[64:67],a[164:167],v[68:71],a[64:67]
v_accvgpr_read_b32 v230,a52
v_accvgpr_read_b32 v231,a53
v_mfma_f32_16x16x32_bf16 a[64:67],a[168:171],v[72:75],a[64:67]
v_accvgpr_read_b32 v16,a54
v_accvgpr_read_b32 v17,a55
v_mfma_f32_16x16x32_bf16 a[64:67],a[172:175],v[76:79],a[64:67]
v_cvt_pk_bf16_f32 v230,v230,v231
v_cvt_pk_bf16_f32 v231,v16,v17
v_mfma_f32_16x16x32_bf16 a[68:71],a[172:175],v[92:95],a[68:71]
buffer_store_dwordx4 v[228:231],v6,s[32:35],s85 offen offset:3072 sc1
v_mfma_f32_16x16x32_bf16 a[68:71],a[168:171],v[88:91],a[68:71]
v_accvgpr_read_b32 v242,a56
v_accvgpr_read_b32 v243,a57
v_mfma_f32_16x16x32_bf16 a[68:71],a[164:167],v[84:87],a[68:71]
v_accvgpr_read_b32 v12,a58
v_accvgpr_read_b32 v13,a59
v_mfma_f32_16x16x32_bf16 a[68:71],a[160:163],v[80:83],a[68:71]
v_cvt_pk_bf16_f32 v242,v242,v243
v_cvt_pk_bf16_f32 v243,v12,v13
v_mfma_f32_16x16x32_bf16 a[72:75],a[160:163],v[96:99],a[72:75]
buffer_store_dwordx4 v[240:243],v6,s[32:35],s86 offen offset:3072 sc1
v_mfma_f32_16x16x32_bf16 a[72:75],a[164:167],v[100:103],a[72:75]
ds_read_b64_tr_b16 a[138:139],v1 offset:25600
ds_read_b64_tr_b16 a[140:141],v1 offset:30720
v_mfma_f32_16x16x32_bf16 a[72:75],a[168:171],v[104:107],a[72:75]
ds_read_b64_tr_b16 a[142:143],v1 offset:35840
v_mfma_f32_16x16x32_bf16 a[72:75],a[172:175],v[108:111],a[72:75]
ds_read_b64_tr_b16 a[144:145],v42
v_mfma_f32_16x16x32_bf16 a[64:67],a[176:179],v[136:139],a[64:67]
s_waitcnt lgkmcnt(4)
ds_read_b64_tr_b16 a[146:147],v42 offset:5120
v_mfma_f32_16x16x32_bf16 a[64:67],a[180:183],v[140:143],a[64:67]
ds_read_b64_tr_b16 a[148:149],v42 offset:10240
ds_read_b64_tr_b16 a[150:151],v42 offset:15360
v_mfma_f32_16x16x32_bf16 a[64:67],a[184:187],v[152:155],a[64:67]
buffer_load_dwordx2 v[4:5],v24,s[64:67],s94 offen sc1
v_mfma_f32_16x16x32_bf16 a[64:67],a[188:191],v[156:159],a[64:67]
ds_read_b64_tr_b16 a[152:153],v42 offset:20480
ds_read_b64_tr_b16 a[154:155],v42 offset:25600
v_mfma_f32_16x16x32_bf16 a[68:71],a[188:191],v[188:191],a[68:71]
ds_read_b64_tr_b16 a[156:157],v42 offset:30720
v_mfma_f32_16x16x32_bf16 a[68:71],a[184:187],v[184:187],a[68:71]
ds_read_b64_tr_b16 a[158:159],v42 offset:35840
v_mfma_f32_16x16x32_bf16 a[68:71],a[180:183],v[172:175],a[68:71]
ds_read_b64_tr_b16 a[160:161],v44
v_mfma_f32_16x16x32_bf16 a[68:71],a[176:179],v[168:171],a[68:71]
ds_read_b64_tr_b16 a[162:163],v44 offset:5120
ds_read_b64_tr_b16 a[164:165],v44 offset:10240
v_mfma_f32_16x16x32_bf16 a[72:75],a[176:179],v[200:203],a[72:75]
ds_read_b64_tr_b16 a[166:167],v44 offset:15360
v_mfma_f32_16x16x32_bf16 a[72:75],a[180:183],v[204:207],a[72:75]
v_accvgpr_read_b32 v128,a64
v_accvgpr_read_b32 v129,a65
v_mfma_f32_16x16x32_bf16 a[72:75],a[184:187],v[216:219],a[72:75]
v_cvt_pk_bf16_f32 v128,v128,v129
v_accvgpr_read_b32 v130,a66
v_mfma_f32_16x16x32_bf16 a[72:75],a[188:191],v[220:223],a[72:75]
v_accvgpr_read_b32 v131,a67
v_cvt_pk_bf16_f32 v129,v130,v131
v_mfma_f32_16x16x32_bf16 a[80:83],a[96:99],v[64:67],a[80:83]
ds_read_b64_tr_b16 a[168:169],v44 offset:20480
ds_read_b64_tr_b16 a[170:171],v44 offset:25600
v_mfma_f32_16x16x32_bf16 a[80:83],a[100:103],v[68:71],a[80:83]
ds_read_b64_tr_b16 a[172:173],v44 offset:30720
v_accvgpr_read_b32 v132,a68
v_mfma_f32_16x16x32_bf16 a[80:83],a[104:107],v[72:75],a[80:83]
ds_read_b64_tr_b16 a[174:175],v44 offset:35840
v_accvgpr_read_b32 v133,a69
v_mfma_f32_16x16x32_bf16 a[80:83],a[108:111],v[76:79],a[80:83]
ds_read_b64_tr_b16 a[176:177],v2 offset:32
v_cvt_pk_bf16_f32 v132,v132,v133
v_mfma_f32_16x16x32_bf16 a[84:87],a[108:111],v[92:95],a[84:87]
ds_read_b64_tr_b16 a[178:179],v2 offset:5152
v_accvgpr_read_b32 v134,a70
v_mfma_f32_16x16x32_bf16 a[84:87],a[104:107],v[88:91],a[84:87]
v_accvgpr_read_b32 v144,a72
v_accvgpr_read_b32 v145,a73
v_mfma_f32_16x16x32_bf16 a[84:87],a[100:103],v[84:87],a[84:87]
v_accvgpr_read_b32 v135,a71
v_cvt_pk_bf16_f32 v144,v144,v145
v_mfma_f32_16x16x32_bf16 a[84:87],a[96:99],v[80:83],a[84:87]
v_accvgpr_read_b32 v146,a74
v_accvgpr_read_b32 v147,a75
v_mfma_f32_16x16x32_bf16 a[88:91],a[96:99],v[96:99],a[88:91]
ds_read_b64_tr_b16 a[180:181],v2 offset:10272
ds_read_b64_tr_b16 a[182:183],v2 offset:15392
v_mfma_f32_16x16x32_bf16 a[88:91],a[100:103],v[100:103],a[88:91]
v_cvt_pk_bf16_f32 v133,v134,v135
)A"
R"A(v_cvt_pk_bf16_f32 v145,v146,v147
v_mfma_f32_16x16x32_bf16 a[88:91],a[104:107],v[104:107],a[88:91]
ds_read_b64_tr_b16 a[184:185],v2 offset:20512
ds_read_b64_tr_b16 a[186:187],v2 offset:25632
v_mfma_f32_16x16x32_bf16 a[88:91],a[108:111],v[108:111],a[88:91]
ds_read_b64_tr_b16 a[188:189],v2 offset:30752
v_mfma_f32_16x16x32_bf16 a[80:83],a[112:115],v[136:139],a[80:83]
ds_read_b64_tr_b16 a[190:191],v2 offset:35872
v_mfma_f32_16x16x32_bf16 a[80:83],a[116:119],v[140:143],a[80:83]
ds_read_b64_tr_b16 a[96:97],v43 offset:32
v_mfma_f32_16x16x32_bf16 a[80:83],a[120:123],v[152:155],a[80:83]
ds_read_b64_tr_b16 a[98:99],v43 offset:5152
ds_read_b64_tr_b16 a[100:101],v43 offset:10272
v_mfma_f32_16x16x32_bf16 a[80:83],a[124:127],v[156:159],a[80:83]
ds_read_b64_tr_b16 a[102:103],v43 offset:15392
v_mfma_f32_16x16x32_bf16 a[84:87],a[124:127],v[188:191],a[84:87]
ds_read_b64_tr_b16 a[104:105],v43 offset:20512
v_mfma_f32_16x16x32_bf16 a[84:87],a[120:123],v[184:187],a[84:87]
ds_read_b64_tr_b16 a[106:107],v43 offset:25632
v_mfma_f32_16x16x32_bf16 a[84:87],a[116:119],v[172:175],a[84:87]
ds_read_b64_tr_b16 a[108:109],v43 offset:30752
ds_read_b64_tr_b16 a[110:111],v43 offset:35872
v_mfma_f32_16x16x32_bf16 a[84:87],a[112:115],v[168:171],a[84:87]
v_mfma_f32_16x16x32_bf16 a[88:91],a[112:115],v[200:203],a[88:91]
ds_read_b64_tr_b16 a[112:113],v45 offset:32
ds_read_b64_tr_b16 a[114:115],v45 offset:5152
v_mfma_f32_16x16x32_bf16 a[88:91],a[116:119],v[204:207],a[88:91]
v_accvgpr_read_b32 v130,a80
v_accvgpr_read_b32 v131,a81
v_mfma_f32_16x16x32_bf16 a[88:91],a[120:123],v[216:219],a[88:91]
v_accvgpr_read_b32 v14,a82
v_accvgpr_read_b32 v15,a83
v_mfma_f32_16x16x32_bf16 a[88:91],a[124:127],v[220:223],a[88:91]
v_cvt_pk_bf16_f32 v130,v130,v131
v_cvt_pk_bf16_f32 v131,v14,v15
v_mfma_f32_16x16x32_bf16 a[204:207],a[128:131],v[148:151],a[204:207]
buffer_store_dwordx4 v[128:131],v25,s[32:35],s84 offen sc1
v_mfma_f32_16x16x32_bf16 a[204:207],a[132:135],v[180:183],a[204:207]
s_waitcnt lgkmcnt(4)
v_accvgpr_read_b32 v134,a84
v_accvgpr_read_b32 v135,a85
v_mfma_f32_16x16x32_bf16 a[204:207],a[136:139],v[212:215],a[204:207]
s_lshl_b32 s95,s42,2
s_waitcnt vmcnt(1)
v_cmp_ne_u32 s[90:91],s68,v4
v_and_b32 v4,-2,v5
v_cmp_ne_u32 s[92:93],s69,v4
s_nop 0
s_or_b64 s[90:91],s[90:91],s[92:93]
s_cbranch_scc1 L_fr10_%=
L_fo10_%=:
v_lshlrev_b32 v19,13,v5
v_and_b32 v19,8192,v19
s_nop 0
v_readlane_b32 s90,v19,2
v_readlane_b32 s91,v19,1
v_readlane_b32 s92,v19,0
s_nop 1
s_or_b32 s76,s76,s90
s_or_b32 s77,s77,s91
s_or_b32 s78,s78,s92
v_mfma_f32_16x16x32_bf16 a[204:207],a[140:143],v[244:247],a[204:207]
s_add_u32 s95,s95,s40
s_lshl_b32 s95,s95,4
v_bfe_u32 v19,s55,13,1
v_mov_b32 v18,s68
v_or_b32 v19,s69,v19
v_mfma_f32_16x16x32_bf16 a[204:207],a[144:147],v[112:115],a[204:207]
s_lshl_b32 s94,s71,2
s_mov_b64 exec,1
buffer_store_dwordx2 v[18:19],v6,s[64:67],s95 offen offset:2048 sc1
s_mov_b64 exec,-1
v_mfma_f32_16x16x32_bf16 a[204:207],a[148:151],v[116:119],a[204:207]
s_add_u32 s94,s94,s40
v_accvgpr_read_b32 v146,a88
v_accvgpr_read_b32 v147,a89
v_mfma_f32_16x16x32_bf16 a[204:207],a[152:155],v[120:123],a[204:207]
.p2align 3
v_accvgpr_read_b32 v16,a86
v_accvgpr_read_b32 v17,a87
v_cvt_pk_bf16_f32 v134,v134,v135
v_accvgpr_read_b32 v12,a90
v_accvgpr_read_b32 v13,a91
v_cvt_pk_bf16_f32 v146,v146,v147
v_cvt_pk_bf16_f32 v135,v16,v17
v_cvt_pk_bf16_f32 v147,v12,v13
v_mfma_f32_16x16x32_bf16 a[204:207],a[156:159],v[124:127],a[204:207]
s_lshl_b32 s94,s94,4
ds_read_b64_tr_b16 a[116:117],v45 offset:10272
ds_read_b64_tr_b16 a[118:119],v45 offset:15392
v_mfma_f32_16x16x32_bf16 a[204:207],a[160:163],v[232:235],a[204:207]
buffer_store_dwordx4 v[132:135],v25,s[32:35],s85 offen sc1
v_mfma_f32_16x16x32_bf16 a[204:207],a[164:167],v[236:239],a[204:207]
ds_read_b64_tr_b16 a[120:121],v45 offset:20512
ds_read_b64_tr_b16 a[122:123],v45 offset:25632
v_mfma_f32_16x16x32_bf16 a[204:207],a[168:171],v[248:251],a[204:207]
ds_read_b64_tr_b16 a[124:125],v45 offset:30752
ds_read_b64_tr_b16 a[126:127],v45 offset:35872
v_mfma_f32_16x16x32_bf16 a[204:207],a[172:175],v[252:255],a[204:207]
ds_read_b64_tr_b16 a[128:129],v1 offset:64
ds_read_b64_tr_b16 a[130:131],v1 offset:5184
v_mfma_f32_16x16x32_bf16 a[220:223],a[176:179],v[148:151],a[220:223]
buffer_store_dwordx4 v[144:147],v25,s[32:35],s86 offen sc1
v_mfma_f32_16x16x32_bf16 a[220:223],a[180:183],v[180:183],a[220:223]
ds_read_b64_tr_b16 a[132:133],v1 offset:10304
ds_read_b64_tr_b16 a[134:135],v1 offset:15424
v_mfma_f32_16x16x32_bf16 a[220:223],a[184:187],v[212:215],a[220:223]
ds_read_b64_tr_b16 a[136:137],v1 offset:20544
ds_read_b64_tr_b16 a[138:139],v1 offset:25664
v_mfma_f32_16x16x32_bf16 a[220:223],a[188:191],v[244:247],a[220:223]
ds_read_b64_tr_b16 a[140:141],v1 offset:30784
ds_read_b64_tr_b16 a[142:143],v1 offset:35904
v_mfma_f32_16x16x32_bf16 a[220:223],a[96:99],v[112:115],a[220:223]
buffer_load_dwordx4 a[192:195],v6,s[32:35],s76 offen sc1
v_mfma_f32_16x16x32_bf16 a[220:223],a[100:103],v[116:119],a[220:223]
ds_read_b64_tr_b16 a[144:145],v42 offset:64
ds_read_b64_tr_b16 a[146:147],v42 offset:5184
v_mfma_f32_16x16x32_bf16 a[220:223],a[104:107],v[120:123],a[220:223]
s_waitcnt lgkmcnt(10)
ds_read_b64_tr_b16 a[148:149],v42 offset:10304
ds_read_b64_tr_b16 a[150:151],v42 offset:15424
v_mfma_f32_16x16x32_bf16 a[220:223],a[108:111],v[124:127],a[220:223]
ds_read_b64_tr_b16 a[152:153],v42 offset:20544
ds_read_b64_tr_b16 a[154:155],v42 offset:25664
v_mfma_f32_16x16x32_bf16 a[220:223],a[112:115],v[232:235],a[220:223]
buffer_load_dwordx4 a[196:199],v6,s[32:35],s77 offen sc1
v_mfma_f32_16x16x32_bf16 a[220:223],a[116:119],v[236:239],a[220:223]
ds_read_b64_tr_b16 a[156:157],v42 offset:30784
ds_read_b64_tr_b16 a[158:159],v42 offset:35904
v_mfma_f32_16x16x32_bf16 a[220:223],a[120:123],v[248:251],a[220:223]
ds_read_b64_tr_b16 a[160:161],v44 offset:64
ds_read_b64_tr_b16 a[162:163],v44 offset:5184
v_mfma_f32_16x16x32_bf16 a[220:223],a[124:127],v[252:255],a[220:223]
s_waitcnt lgkmcnt(10)
ds_read_b64_tr_b16 a[164:165],v44 offset:10304
ds_read_b64_tr_b16 a[166:167],v44 offset:15424
v_mfma_f32_16x16x32_bf16 a[236:239],a[128:131],v[148:151],a[236:239]
buffer_load_dwordx4 a[200:203],v6,s[32:35],s78 offen sc1
v_mfma_f32_16x16x32_bf16 a[236:239],a[132:135],v[180:183],a[236:239]
ds_read_b64_tr_b16 a[168:169],v44 offset:20544
ds_read_b64_tr_b16 a[170:171],v44 offset:25664
v_mfma_f32_16x16x32_bf16 a[236:239],a[136:139],v[212:215],a[236:239]
ds_read_b64_tr_b16 a[172:173],v44 offset:30784
ds_read_b64_tr_b16 a[174:175],v44 offset:35904
v_mfma_f32_16x16x32_bf16 a[236:239],a[140:143],v[244:247],a[236:239]
s_waitcnt lgkmcnt(10)
ds_read_b64_tr_b16 a[176:177],v2 offset:96
ds_read_b64_tr_b16 a[178:179],v2 offset:5216
v_mfma_f32_16x16x32_bf16 a[236:239],a[144:147],v[112:115],a[236:239]
buffer_load_dwordx4 a[224:227],v6,s[32:35],s76 offen offset:1024 sc1
ds_read_b64_tr_b16 a[180:181],v2 offset:10336
ds_read_b64_tr_b16 a[182:183],v2 offset:15456
v_mfma_f32_16x16x32_bf16 a[236:239],a[148:151],v[116:119],a[236:239]
ds_read_b64_tr_b16 a[184:185],v2 offset:20576
ds_read_b64_tr_b16 a[186:187],v2 offset:25696
v_mfma_f32_16x16x32_bf16 a[236:239],a[152:155],v[120:123],a[236:239]
s_waitcnt lgkmcnt(12)
ds_read_b64_tr_b16 a[188:189],v2 offset:30816
ds_read_b64_tr_b16 a[190:191],v2 offset:35936
v_mfma_f32_16x16x32_bf16 a[236:239],a[156:159],v[124:127],a[236:239]
ds_read_b64_tr_b16 a[96:97],v43 offset:96
ds_read_b64_tr_b16 a[98:99],v43 offset:5216
v_mfma_f32_16x16x32_bf16 a[236:239],a[160:163],v[232:235],a[236:239]
s_waitcnt lgkmcnt(14)
buffer_load_dwordx4 a[228:231],v6,s[32:35],s77 offen offset:1024 sc1
ds_read_b64_tr_b16 a[100:101],v43 offset:10336
ds_read_b64_tr_b16 a[102:103],v43 offset:15456
v_mfma_f32_16x16x32_bf16 a[236:239],a[164:167],v[236:239],a[236:239]
s_waitcnt lgkmcnt(14)
ds_read_b64_tr_b16 a[104:105],v43 offset:20576
ds_read_b64_tr_b16 a[106:107],v43 offset:25696
v_mfma_f32_16x16x32_bf16 a[236:239],a[168:171],v[248:251],a[236:239]
s_waitcnt lgkmcnt(14)
ds_read_b64_tr_b16 a[108:109],v43 offset:30816
ds_read_b64_tr_b16 a[110:111],v43 offset:35936
v_mfma_f32_16x16x32_bf16 a[236:239],a[172:175],v[252:255],a[236:239]
s_waitcnt lgkmcnt(14)
ds_read_b64_tr_b16 a[112:113],v45 offset:96
ds_read_b64_tr_b16 a[114:115],v45 offset:5216
v_mfma_f32_16x16x32_bf16 a[252:255],a[176:179],v[148:151],a[252:255]
s_waitcnt lgkmcnt(14)
buffer_load_dwordx4 a[232:235],v6,s[32:35],s78 offen offset:1024 sc1
ds_read_b64_tr_b16 a[116:117],v45 offset:10336
ds_read_b64_tr_b16 a[118:119],v45 offset:15456
v_mfma_f32_16x16x32_bf16 a[252:255],a[180:183],v[180:183],a[252:255]
s_waitcnt lgkmcnt(14)
ds_read_b64_tr_b16 a[120:121],v45 offset:20576
ds_read_b64_tr_b16 a[122:123],v45 offset:25696
v_mfma_f32_16x16x32_bf16 a[252:255],a[184:187],v[212:215],a[252:255]
s_waitcnt lgkmcnt(14)
ds_read_b64_tr_b16 a[124:125],v45 offset:30816
ds_read_b64_tr_b16 a[126:127],v45 offset:35936
v_mfma_f32_16x16x32_bf16 a[252:255],a[188:191],v[244:247],a[252:255]
s_waitcnt lgkmcnt(14)
ds_read_b64_tr_b16 a[128:129],v1 offset:128
ds_read_b64_tr_b16 a[130:131],v1 offset:5248
v_bfe_u32 v24,v6,4,2
v_min_u32 v24,2,v24
v_mfma_f32_16x16x32_bf16 a[252:255],a[96:99],v[112:115],a[252:255]
s_waitcnt lgkmcnt(14)
ds_read_b64_tr_b16 a[132:133],v1 offset:10368
ds_read_b64_tr_b16 a[134:135],v1 offset:15488
v_add_u32 v24,s70,v24
v_add_u32 v24,1,v24
v_mfma_f32_16x16x32_bf16 a[252:255],a[100:103],v[116:119],a[252:255]
s_waitcnt lgkmcnt(14)
ds_read_b64_tr_b16 a[136:137],v1 offset:20608
ds_read_b64_tr_b16 a[138:139],v1 offset:25728
v_and_b32 v24,3,v24
v_lshlrev_b32 v24,8,v24
v_mfma_f32_16x16x32_bf16 a[252:255],a[104:107],v[120:123],a[252:255]
s_waitcnt lgkmcnt(14)
buffer_load_dwordx2 v[4:5],v24,s[64:67],s94 offen offset:2048 sc1
ds_read_b64_tr_b16 a[140:141],v1 offset:30848
ds_read_b64_tr_b16 a[142:143],v1 offset:35968
v_mfma_f32_16x16x32_bf16 a[252:255],a[108:111],v[124:127],a[252:255]
s_waitcnt lgkmcnt(14)
ds_read_b64_tr_b16 a[144:145],v42 offset:128
ds_read_b64_tr_b16 a[146:147],v42 offset:5248
v_mfma_f32_16x16x32_bf16 a[252:255],a[112:115],v[232:235],a[252:255]
s_waitcnt lgkmcnt(14)
ds_read_b64_tr_b16 a[148:149],v42 offset:10368
ds_read_b64_tr_b16 a[150:151],v42 offset:15488
v_mfma_f32_16x16x32_bf16 a[252:255],a[116:119],v[236:239],a[252:255]
s_lshl_b32 s95,s42,2
ds_read_b64_tr_b16 a[152:153],v42 offset:20608
ds_read_b64_tr_b16 a[154:155],v42 offset:25728
s_waitcnt lgkmcnt(15)
v_mfma_f32_16x16x32_bf16 a[252:255],a[120:123],v[248:251],a[252:255]
s_add_u32 s95,s95,s40
s_lshl_b32 s95,s95,4
ds_read_b64_tr_b16 a[156:157],v42 offset:30848
ds_read_b64_tr_b16 a[158:159],v42 offset:35968
v_bfe_u32 v19,s55,13,1
v_mov_b32 v18,s68
v_or_b32 v19,s69,v19
s_waitcnt lgkmcnt(15)
v_mfma_f32_16x16x32_bf16 a[252:255],a[124:127],v[252:255],a[252:255]
s_waitcnt vmcnt(7)
s_mov_b64 exec,1
buffer_store_dwordx2 v[18:19],v6,s[64:67],s95 offen offset:3072 sc1
s_mov_b64 exec,-1
ds_read_b64_tr_b16 a[160:161],v44 offset:128
ds_read_b64_tr_b16 a[162:163],v44 offset:5248
s_waitcnt lgkmcnt(15)
v_mfma_f32_16x16x32_bf16 a[12:15],a[128:131],v[148:151],a[12:15]
s_waitcnt lgkmcnt(14)
ds_read_b64_tr_b16 a[164:165],v44 offset:10368
ds_read_b64_tr_b16 a[166:167],v44 offset:15488
v_mfma_f32_16x16x32_bf16 a[12:15],a[132:135],v[180:183],a[12:15]
s_waitcnt lgkmcnt(14)
ds_read_b64_tr_b16 a[168:169],v44 offset:20608
ds_read_b64_tr_b16 a[170:171],v44 offset:25728
v_mfma_f32_16x16x32_bf16 a[12:15],a[136:139],v[212:215],a[12:15]
s_waitcnt lgkmcnt(14)
ds_read_b64_tr_b16 a[172:173],v44 offset:30848
ds_read_b64_tr_b16 a[174:175],v44 offset:35968
v_mfma_f32_16x16x32_bf16 a[12:15],a[140:143],v[244:247],a[12:15]
s_waitcnt lgkmcnt(14)
ds_read_b64_tr_b16 a[176:177],v2 offset:160
ds_read_b64_tr_b16 a[178:179],v2 offset:5280
v_mfma_f32_16x16x32_bf16 a[12:15],a[144:147],v[112:115],a[12:15]
s_waitcnt lgkmcnt(14)
ds_read_b64_tr_b16 a[180:181],v2 offset:10400
ds_read_b64_tr_b16 a[182:183],v2 offset:15520
v_mfma_f32_16x16x32_bf16 a[12:15],a[148:151],v[116:119],a[12:15]
s_waitcnt lgkmcnt(14)
ds_read_b64_tr_b16 a[184:185],v2 offset:20640
ds_read_b64_tr_b16 a[186:187],v2 offset:25760
v_mfma_f32_16x16x32_bf16 a[12:15],a[152:155],v[120:123],a[12:15]
)A"
R"A(s_waitcnt lgkmcnt(14)
ds_read_b64_tr_b16 a[188:189],v2 offset:30880
ds_read_b64_tr_b16 a[190:191],v2 offset:36000
v_mfma_f32_16x16x32_bf16 a[12:15],a[156:159],v[124:127],a[12:15]
s_waitcnt lgkmcnt(14)
ds_read_b64_tr_b16 a[96:97],v43 offset:160
ds_read_b64_tr_b16 a[98:99],v43 offset:5280
v_mfma_f32_16x16x32_bf16 a[12:15],a[160:163],v[232:235],a[12:15]
s_waitcnt lgkmcnt(14)
ds_read_b64_tr_b16 a[100:101],v43 offset:10400
ds_read_b64_tr_b16 a[102:103],v43 offset:15520
v_mfma_f32_16x16x32_bf16 a[12:15],a[164:167],v[236:239],a[12:15]
s_waitcnt lgkmcnt(14)
ds_read_b64_tr_b16 a[104:105],v43 offset:20640
ds_read_b64_tr_b16 a[106:107],v43 offset:25760
v_mfma_f32_16x16x32_bf16 a[12:15],a[168:171],v[248:251],a[12:15]
s_waitcnt lgkmcnt(14)
ds_read_b64_tr_b16 a[108:109],v43 offset:30880
ds_read_b64_tr_b16 a[110:111],v43 offset:36000
v_mfma_f32_16x16x32_bf16 a[12:15],a[172:175],v[252:255],a[12:15]
s_waitcnt lgkmcnt(14)
ds_read_b64_tr_b16 a[112:113],v45 offset:160
ds_read_b64_tr_b16 a[114:115],v45 offset:5280
v_mfma_f32_16x16x32_bf16 a[28:31],a[176:179],v[148:151],a[28:31]
s_waitcnt lgkmcnt(14)
ds_read_b64_tr_b16 a[116:117],v45 offset:10400
ds_read_b64_tr_b16 a[118:119],v45 offset:15520
v_mfma_f32_16x16x32_bf16 a[28:31],a[180:183],v[180:183],a[28:31]
s_waitcnt lgkmcnt(14)
ds_read_b64_tr_b16 a[120:121],v45 offset:20640
ds_read_b64_tr_b16 a[122:123],v45 offset:25760
v_mfma_f32_16x16x32_bf16 a[28:31],a[184:187],v[212:215],a[28:31]
s_waitcnt lgkmcnt(14)
ds_read_b64_tr_b16 a[124:125],v45 offset:30880
ds_read_b64_tr_b16 a[126:127],v45 offset:36000
v_mfma_f32_16x16x32_bf16 a[28:31],a[188:191],v[244:247],a[28:31]
s_waitcnt lgkmcnt(14)
ds_read_b64_tr_b16 a[128:129],v1 offset:192
ds_read_b64_tr_b16 a[130:131],v1 offset:5312
v_mfma_f32_16x16x32_bf16 a[28:31],a[96:99],v[112:115],a[28:31]
s_waitcnt lgkmcnt(14)
ds_read_b64_tr_b16 a[132:133],v1 offset:10432
ds_read_b64_tr_b16 a[134:135],v1 offset:15552
v_mfma_f32_16x16x32_bf16 a[28:31],a[100:103],v[116:119],a[28:31]
s_waitcnt lgkmcnt(14)
ds_read_b64_tr_b16 a[136:137],v1 offset:20672
ds_read_b64_tr_b16 a[138:139],v1 offset:25792
v_mfma_f32_16x16x32_bf16 a[28:31],a[104:107],v[120:123],a[28:31]
s_waitcnt lgkmcnt(14)
ds_read_b64_tr_b16 a[140:141],v1 offset:30912
ds_read_b64_tr_b16 a[142:143],v1 offset:36032
v_mfma_f32_16x16x32_bf16 a[28:31],a[108:111],v[124:127],a[28:31]
s_waitcnt lgkmcnt(14)
ds_read_b64_tr_b16 a[144:145],v42 offset:192
ds_read_b64_tr_b16 a[146:147],v42 offset:5312
v_mfma_f32_16x16x32_bf16 a[28:31],a[112:115],v[232:235],a[28:31]
s_waitcnt lgkmcnt(14)
ds_read_b64_tr_b16 a[148:149],v42 offset:10432
ds_read_b64_tr_b16 a[150:151],v42 offset:15552
v_mfma_f32_16x16x32_bf16 a[28:31],a[116:119],v[236:239],a[28:31]
s_waitcnt lgkmcnt(14)
ds_read_b64_tr_b16 a[152:153],v42 offset:20672
ds_read_b64_tr_b16 a[154:155],v42 offset:25792
v_mfma_f32_16x16x32_bf16 a[28:31],a[120:123],v[248:251],a[28:31]
s_waitcnt lgkmcnt(14)
ds_read_b64_tr_b16 a[156:157],v42 offset:30912
ds_read_b64_tr_b16 a[158:159],v42 offset:36032
v_mfma_f32_16x16x32_bf16 a[28:31],a[124:127],v[252:255],a[28:31]
s_waitcnt lgkmcnt(14)
ds_read_b64_tr_b16 a[160:161],v44 offset:192
ds_read_b64_tr_b16 a[162:163],v44 offset:5312
v_mfma_f32_16x16x32_bf16 a[44:47],a[128:131],v[148:151],a[44:47]
s_waitcnt lgkmcnt(14)
ds_read_b64_tr_b16 a[164:165],v44 offset:10432
ds_read_b64_tr_b16 a[166:167],v44 offset:15552
v_mfma_f32_16x16x32_bf16 a[44:47],a[132:135],v[180:183],a[44:47]
s_waitcnt lgkmcnt(14)
ds_read_b64_tr_b16 a[168:169],v44 offset:20672
ds_read_b64_tr_b16 a[170:171],v44 offset:25792
v_mfma_f32_16x16x32_bf16 a[44:47],a[136:139],v[212:215],a[44:47]
ds_read_b64_tr_b16 a[172:173],v44 offset:30912
ds_read_b64_tr_b16 a[174:175],v44 offset:36032
s_waitcnt vmcnt(1)
v_cmp_ne_u32 s[90:91],s68,v4
v_and_b32 v4,-2,v5
v_cmp_ne_u32 s[92:93],s69,v4
s_nop 0
s_or_b64 s[90:91],s[90:91],s[92:93]
s_cbranch_scc1 L_fr11_%=
L_fo11_%=:
s_waitcnt lgkmcnt(15)
v_mfma_f32_16x16x32_bf16 a[44:47],a[140:143],v[244:247],a[44:47]
s_waitcnt lgkmcnt(14)
buffer_load_dwordx4 v[72:75],v6,s[32:35],s76 offen offset:2048 sc1
ds_read_b64_tr_b16 a[176:177],v2 offset:224
ds_read_b64_tr_b16 a[178:179],v2 offset:5344
v_mfma_f32_16x16x32_bf16 a[44:47],a[144:147],v[112:115],a[44:47]
s_lshl_b32 s94,s71,2
ds_read_b64_tr_b16 a[180:181],v2 offset:10464
ds_read_b64_tr_b16 a[182:183],v2 offset:15584
s_waitcnt lgkmcnt(15)
v_mfma_f32_16x16x32_bf16 a[44:47],a[148:151],v[116:119],a[44:47]
s_add_u32 s94,s94,s40
ds_read_b64_tr_b16 a[184:185],v2 offset:20704
ds_read_b64_tr_b16 a[186:187],v2 offset:25824
s_waitcnt lgkmcnt(15)
v_mfma_f32_16x16x32_bf16 a[44:47],a[152:155],v[120:123],a[44:47]
s_lshl_b32 s94,s94,4
ds_read_b64_tr_b16 a[188:189],v2 offset:30944
ds_read_b64_tr_b16 a[190:191],v2 offset:36064
s_waitcnt lgkmcnt(15)
v_mfma_f32_16x16x32_bf16 a[44:47],a[156:159],v[124:127],a[44:47]
s_waitcnt lgkmcnt(14)
buffer_load_dwordx4 v[88:91],v6,s[32:35],s77 offen offset:2048 sc1
ds_read_b64_tr_b16 a[96:97],v43 offset:224
ds_read_b64_tr_b16 a[98:99],v43 offset:5344
v_mfma_f32_16x16x32_bf16 a[44:47],a[160:163],v[232:235],a[44:47]
s_waitcnt lgkmcnt(14)
ds_read_b64_tr_b16 a[100:101],v43 offset:10464
ds_read_b64_tr_b16 a[102:103],v43 offset:15584
v_mfma_f32_16x16x32_bf16 a[44:47],a[164:167],v[236:239],a[44:47]
s_waitcnt lgkmcnt(14)
ds_read_b64_tr_b16 a[104:105],v43 offset:20704
ds_read_b64_tr_b16 a[106:107],v43 offset:25824
v_mfma_f32_16x16x32_bf16 a[44:47],a[168:171],v[248:251],a[44:47]
s_waitcnt lgkmcnt(14)
ds_read_b64_tr_b16 a[108:109],v43 offset:30944
ds_read_b64_tr_b16 a[110:111],v43 offset:36064
v_mfma_f32_16x16x32_bf16 a[44:47],a[172:175],v[252:255],a[44:47]
s_waitcnt lgkmcnt(14)
buffer_load_dwordx4 v[104:107],v6,s[32:35],s78 offen offset:2048 sc1
ds_read_b64_tr_b16 a[112:113],v45 offset:224
ds_read_b64_tr_b16 a[114:115],v45 offset:5344
v_mfma_f32_16x16x32_bf16 a[60:63],a[176:179],v[148:151],a[60:63]
s_waitcnt lgkmcnt(14)
ds_read_b64_tr_b16 a[116:117],v45 offset:10464
ds_read_b64_tr_b16 a[118:119],v45 offset:15584
v_mfma_f32_16x16x32_bf16 a[60:63],a[180:183],v[180:183],a[60:63]
s_waitcnt lgkmcnt(14)
ds_read_b64_tr_b16 a[120:121],v45 offset:20704
ds_read_b64_tr_b16 a[122:123],v45 offset:25824
v_mfma_f32_16x16x32_bf16 a[60:63],a[184:187],v[212:215],a[60:63]
s_waitcnt lgkmcnt(14)
ds_read_b64_tr_b16 a[124:125],v45 offset:30944
ds_read_b64_tr_b16 a[126:127],v45 offset:36064
v_mfma_f32_16x16x32_bf16 a[60:63],a[188:191],v[244:247],a[60:63]
s_waitcnt lgkmcnt(14)
buffer_load_dwordx4 v[76:79],v6,s[32:35],s76 offen offset:3072 sc1
v_mfma_f32_16x16x32_bf16 a[60:63],a[96:99],v[112:115],a[60:63]
s_waitcnt lgkmcnt(12)
ds_read_b64_tr_b16 a[128:129],v1 offset:256
ds_read_b64_tr_b16 a[130:131],v1 offset:5376
v_mfma_f32_16x16x32_bf16 a[60:63],a[100:103],v[116:119],a[60:63]
s_waitcnt lgkmcnt(12)
ds_read_b64_tr_b16 a[132:133],v1 offset:10496
ds_read_b64_tr_b16 a[134:135],v1 offset:15616
v_mfma_f32_16x16x32_bf16 a[60:63],a[104:107],v[120:123],a[60:63]
s_waitcnt lgkmcnt(12)
ds_read_b64_tr_b16 a[136:137],v1 offset:20736
ds_read_b64_tr_b16 a[138:139],v1 offset:25856
v_mfma_f32_16x16x32_bf16 a[60:63],a[108:111],v[124:127],a[60:63]
s_waitcnt lgkmcnt(12)
buffer_load_dwordx4 v[92:95],v6,s[32:35],s77 offen offset:3072 sc1
v_mfma_f32_16x16x32_bf16 a[60:63],a[112:115],v[232:235],a[60:63]
s_waitcnt lgkmcnt(10)
ds_read_b64_tr_b16 a[140:141],v1 offset:30976
ds_read_b64_tr_b16 a[142:143],v1 offset:36096
v_mfma_f32_16x16x32_bf16 a[60:63],a[116:119],v[236:239],a[60:63]
s_waitcnt lgkmcnt(10)
ds_read_b64_tr_b16 a[144:145],v42 offset:256
ds_read_b64_tr_b16 a[146:147],v42 offset:5376
v_mfma_f32_16x16x32_bf16 a[60:63],a[120:123],v[248:251],a[60:63]
s_waitcnt lgkmcnt(10)
ds_read_b64_tr_b16 a[148:149],v42 offset:10496
ds_read_b64_tr_b16 a[150:151],v42 offset:15616
v_mfma_f32_16x16x32_bf16 a[60:63],a[124:127],v[252:255],a[60:63]
buffer_load_dwordx4 v[108:111],v6,s[32:35],s78 offen offset:3072 sc1
v_mfma_f32_16x16x32_bf16 a[204:207],v[34:37],a[192:195],a[204:207]
ds_read_b64_tr_b16 a[152:153],v42 offset:20736
ds_read_b64_tr_b16 a[154:155],v42 offset:25856
v_mfma_f32_16x16x32_bf16 a[204:207],v[34:37],a[196:199],a[204:207]
ds_read_b64_tr_b16 a[156:157],v42 offset:30976
ds_read_b64_tr_b16 a[158:159],v42 offset:36096
v_mfma_f32_16x16x32_bf16 a[204:207],v[34:37],a[200:203],a[204:207]
ds_read_b64_tr_b16 a[160:161],v44 offset:256
ds_read_b64_tr_b16 a[162:163],v44 offset:5376
v_mfma_f32_16x16x32_bf16 a[220:223],v[32:35],a[192:195],a[220:223]
ds_read_b64_tr_b16 a[164:165],v44 offset:10496
ds_read_b64_tr_b16 a[166:167],v44 offset:15616
v_mfma_f32_16x16x32_bf16 a[220:223],v[32:35],a[196:199],a[220:223]
ds_read_b64_tr_b16 a[168:169],v44 offset:20736
ds_read_b64_tr_b16 a[170:171],v44 offset:25856
v_mfma_f32_16x16x32_bf16 a[220:223],v[32:35],a[200:203],a[220:223]
s_waitcnt lgkmcnt(12)
ds_read_b64_tr_b16 a[172:173],v44 offset:30976
ds_read_b64_tr_b16 a[174:175],v44 offset:36096
v_mfma_f32_16x16x32_bf16 a[76:79],a[128:131],v[148:151],a[76:79]
v_bfe_u32 v24,v6,4,2
v_min_u32 v24,2,v24
v_mfma_f32_16x16x32_bf16 a[76:79],a[132:135],v[180:183],a[76:79]
v_add_u32 v24,s70,v24
v_add_u32 v24,1,v24
v_mfma_f32_16x16x32_bf16 a[76:79],a[136:139],v[212:215],a[76:79]
v_accvgpr_read_b32 v160,a204
v_accvgpr_read_b32 v161,a205
v_mfma_f32_16x16x32_bf16 a[76:79],a[140:143],v[244:247],a[76:79]
v_cvt_pk_bf16_f32 v160,v160,v161
v_accvgpr_read_b32 v162,a206
v_mfma_f32_16x16x32_bf16 a[76:79],a[144:147],v[112:115],a[76:79]
s_waitcnt lgkmcnt(6)
v_accvgpr_read_b32 v163,a207
v_cvt_pk_bf16_f32 v161,v162,v163
v_mfma_f32_16x16x32_bf16 a[76:79],a[148:151],v[116:119],a[76:79]
v_accvgpr_read_b32 v162,a220
v_accvgpr_read_b32 v163,a221
v_mfma_f32_16x16x32_bf16 a[76:79],a[152:155],v[120:123],a[76:79]
v_accvgpr_read_b32 v14,a222
v_accvgpr_read_b32 v15,a223
v_mfma_f32_16x16x32_bf16 a[76:79],a[156:159],v[124:127],a[76:79]
v_cvt_pk_bf16_f32 v162,v162,v163
v_cvt_pk_bf16_f32 v163,v14,v15
v_mfma_f32_16x16x32_bf16 a[76:79],a[160:163],v[232:235],a[76:79]
s_waitcnt lgkmcnt(0)
ds_read_b64_tr_b16 a[176:177],v2 offset:288
ds_read_b64_tr_b16 a[178:179],v2 offset:5408
v_mfma_f32_16x16x32_bf16 a[76:79],a[164:167],v[236:239],a[76:79]
v_permlane16_swap_b32 v160,v162
v_mfma_f32_16x16x32_bf16 a[76:79],a[168:171],v[248:251],a[76:79]
ds_read_b64_tr_b16 a[180:181],v2 offset:10528
ds_read_b64_tr_b16 a[182:183],v2 offset:15648
v_and_b32 v24,3,v24
v_lshlrev_b32 v24,8,v24
v_mfma_f32_16x16x32_bf16 a[76:79],a[172:175],v[252:255],a[76:79]
buffer_load_dwordx2 v[4:5],v24,s[64:67],s94 offen offset:3072 sc1
ds_read_b64_tr_b16 a[184:185],v2 offset:20768
ds_read_b64_tr_b16 a[186:187],v2 offset:25888
v_mfma_f32_16x16x32_bf16 a[236:239],v[34:37],a[224:227],a[236:239]
ds_read_b64_tr_b16 a[188:189],v2 offset:31008
ds_read_b64_tr_b16 a[190:191],v2 offset:36128
v_mfma_f32_16x16x32_bf16 a[236:239],v[34:37],a[228:231],a[236:239]
ds_read_b64_tr_b16 a[96:97],v43 offset:288
ds_read_b64_tr_b16 a[98:99],v43 offset:5408
v_mfma_f32_16x16x32_bf16 a[236:239],v[34:37],a[232:235],a[236:239]
ds_read_b64_tr_b16 a[100:101],v43 offset:10528
ds_read_b64_tr_b16 a[102:103],v43 offset:15648
v_mfma_f32_16x16x32_bf16 a[252:255],v[32:35],a[224:227],a[252:255]
ds_read_b64_tr_b16 a[104:105],v43 offset:20768
ds_read_b64_tr_b16 a[106:107],v43 offset:25888
v_mfma_f32_16x16x32_bf16 a[252:255],v[32:35],a[228:231],a[252:255]
ds_read_b64_tr_b16 a[108:109],v43 offset:31008
ds_read_b64_tr_b16 a[110:111],v43 offset:36128
v_mfma_f32_16x16x32_bf16 a[252:255],v[32:35],a[232:235],a[252:255]
s_waitcnt lgkmcnt(14)
ds_read_b64_tr_b16 a[112:113],v45 offset:288
ds_read_b64_tr_b16 a[114:115],v45 offset:5408
v_mfma_f32_16x16x32_bf16 a[92:95],a[176:179],v[148:151],a[92:95]
s_waitcnt lgkmcnt(14)
ds_read_b64_tr_b16 a[116:117],v45 offset:10528
ds_read_b64_tr_b16 a[118:119],v45 offset:15648
v_mfma_f32_16x16x32_bf16 a[92:95],a[180:183],v[180:183],a[92:95]
s_waitcnt lgkmcnt(14)
ds_read_b64_tr_b16 a[120:121],v45 offset:20768
ds_read_b64_tr_b16 a[122:123],v45 offset:25888
v_mfma_f32_16x16x32_bf16 a[92:95],a[184:187],v[212:215],a[92:95]
s_waitcnt lgkmcnt(14)
ds_read_b64_tr_b16 a[124:125],v45 offset:31008
ds_read_b64_tr_b16 a[126:127],v45 offset:36128
v_mfma_f32_16x16x32_bf16 a[92:95],a[188:191],v[244:247],a[92:95]
)A"
R"A(s_waitcnt lgkmcnt(14)
v_permlane16_swap_b32 v161,v163
v_mfma_f32_16x16x32_bf16 a[92:95],a[96:99],v[112:115],a[92:95]
s_cmp_lg_u32 %2,0
s_cbranch_scc1 L_ea0_%=
buffer_store_dwordx4 v[160:163],v40,s[20:23],s43 offen
s_branch L_eb0_%=
L_ea0_%=:
buffer_store_dwordx4 v[160:163],v40,s[20:23],s43 offen sc1
L_eb0_%=:
s_waitcnt lgkmcnt(12)
v_mfma_f32_16x16x32_bf16 a[92:95],a[100:103],v[116:119],a[92:95]
s_waitcnt lgkmcnt(10)
v_accvgpr_read_b32 v164,a236
v_accvgpr_read_b32 v165,a237
v_mfma_f32_16x16x32_bf16 a[92:95],a[104:107],v[120:123],a[92:95]
s_waitcnt lgkmcnt(8)
v_cvt_pk_bf16_f32 v164,v164,v165
v_accvgpr_read_b32 v166,a238
v_mfma_f32_16x16x32_bf16 a[92:95],a[108:111],v[124:127],a[92:95]
s_waitcnt lgkmcnt(6)
v_accvgpr_read_b32 v167,a239
v_cvt_pk_bf16_f32 v165,v166,v167
v_mfma_f32_16x16x32_bf16 a[92:95],a[112:115],v[232:235],a[92:95]
s_waitcnt lgkmcnt(4)
v_accvgpr_read_b32 v166,a252
v_accvgpr_read_b32 v167,a253
v_mfma_f32_16x16x32_bf16 a[92:95],a[116:119],v[236:239],a[92:95]
s_waitcnt lgkmcnt(2)
.p2align 3
v_accvgpr_read_b32 v16,a254
v_accvgpr_read_b32 v17,a255
v_cvt_pk_bf16_f32 v166,v166,v167
v_cvt_pk_bf16_f32 v167,v16,v17
v_mfma_f32_16x16x32_bf16 a[92:95],a[120:123],v[248:251],a[92:95]
s_waitcnt lgkmcnt(0)
v_mfma_f32_16x16x32_bf16 a[92:95],a[124:127],v[252:255],a[92:95]
v_permlane16_swap_b32 v164,v166
v_permlane16_swap_b32 v165,v167
s_nop 3
s_cmp_lg_u32 %2,0
s_cbranch_scc1 L_ea1_%=
buffer_store_dwordx4 v[164:167],v40,s[20:23],s43 offen offset:64
s_branch L_eb1_%=
L_ea1_%=:
buffer_store_dwordx4 v[164:167],v40,s[20:23],s43 offen offset:64 sc1
L_eb1_%=:
s_waitcnt vmcnt(2)
v_cmp_ne_u32 s[90:91],s68,v4
v_and_b32 v4,-2,v5
v_cmp_ne_u32 s[92:93],s69,v4
s_nop 0
s_or_b64 s[90:91],s[90:91],s[92:93]
s_cbranch_scc1 L_fr12_%=
L_fo12_%=:
buffer_load_dwordx4 v[136:139],v25,s[32:35],s76 offen sc1
buffer_load_dwordx4 v[168:171],v25,s[32:35],s77 offen sc1
buffer_load_dwordx4 v[200:203],v25,s[32:35],s78 offen sc1
s_cmp_lg_u32 %2,0
s_cbranch_scc1 L_dual_%=
v_mfma_f32_16x16x32_bf16 a[12:15],v[34:37],v[72:75],a[12:15]
v_mfma_f32_16x16x32_bf16 a[12:15],v[34:37],v[88:91],a[12:15]
v_mfma_f32_16x16x32_bf16 a[12:15],v[34:37],v[104:107],a[12:15]
v_mfma_f32_16x16x32_bf16 a[28:31],v[32:35],v[72:75],a[28:31]
v_mfma_f32_16x16x32_bf16 a[28:31],v[32:35],v[88:91],a[28:31]
v_mfma_f32_16x16x32_bf16 a[28:31],v[32:35],v[104:107],a[28:31]
v_mfma_f32_16x16x32_bf16 a[44:47],v[34:37],v[76:79],a[44:47]
v_mfma_f32_16x16x32_bf16 a[44:47],v[34:37],v[92:95],a[44:47]
v_mfma_f32_16x16x32_bf16 a[44:47],v[34:37],v[108:111],a[44:47]
s_nop 1
.p2align 3
v_accvgpr_read_b32 v176,a12
v_accvgpr_read_b32 v177,a13
v_cvt_pk_bf16_f32 v176,v176,v177
v_accvgpr_read_b32 v178,a14
v_accvgpr_read_b32 v179,a15
v_cvt_pk_bf16_f32 v177,v178,v179
v_mfma_f32_16x16x32_bf16 a[60:63],v[32:35],v[76:79],a[60:63]
v_mfma_f32_16x16x32_bf16 a[60:63],v[32:35],v[92:95],a[60:63]
v_mfma_f32_16x16x32_bf16 a[60:63],v[32:35],v[108:111],a[60:63]
.p2align 3
v_accvgpr_read_b32 v178,a28
v_accvgpr_read_b32 v179,a29
v_accvgpr_read_b32 v12,a30
v_accvgpr_read_b32 v13,a31
v_cvt_pk_bf16_f32 v178,v178,v179
v_cvt_pk_bf16_f32 v179,v12,v13
s_nop 3
v_permlane16_swap_b32 v176,v178
v_permlane16_swap_b32 v177,v179
buffer_store_dwordx4 v[176:179],v40,s[20:23],s43 offen offset:128
.p2align 3
v_accvgpr_read_b32 v192,a44
v_accvgpr_read_b32 v193,a45
v_cvt_pk_bf16_f32 v192,v192,v193
v_accvgpr_read_b32 v194,a46
v_accvgpr_read_b32 v195,a47
v_cvt_pk_bf16_f32 v193,v194,v195
v_accvgpr_read_b32 v194,a60
v_accvgpr_read_b32 v195,a61
v_accvgpr_read_b32 v14,a62
v_accvgpr_read_b32 v15,a63
v_cvt_pk_bf16_f32 v194,v194,v195
s_nop 1
v_permlane16_swap_b32 v192,v194
v_cvt_pk_bf16_f32 v195,v14,v15
s_nop 1
v_permlane16_swap_b32 v193,v195
buffer_store_dwordx4 v[192:195],v40,s[20:23],s43 offen offset:192
s_waitcnt vmcnt(4)
v_mfma_f32_16x16x32_bf16 a[76:79],v[34:37],v[136:139],a[76:79]
s_waitcnt vmcnt(3)
v_mfma_f32_16x16x32_bf16 a[76:79],v[34:37],v[168:171],a[76:79]
s_waitcnt vmcnt(2)
v_mfma_f32_16x16x32_bf16 a[76:79],v[34:37],v[200:203],a[76:79]
v_mfma_f32_16x16x32_bf16 a[92:95],v[32:35],v[136:139],a[92:95]
v_mfma_f32_16x16x32_bf16 a[92:95],v[32:35],v[168:171],a[92:95]
v_mfma_f32_16x16x32_bf16 a[92:95],v[32:35],v[200:203],a[92:95]
s_nop 3
s_nop 0
.p2align 3
v_accvgpr_read_b32 v196,a76
v_accvgpr_read_b32 v197,a77
v_cvt_pk_bf16_f32 v196,v196,v197
v_accvgpr_read_b32 v198,a78
v_accvgpr_read_b32 v199,a79
v_cvt_pk_bf16_f32 v197,v198,v199
v_accvgpr_read_b32 v198,a92
v_accvgpr_read_b32 v199,a93
v_accvgpr_read_b32 v16,a94
v_accvgpr_read_b32 v17,a95
v_cvt_pk_bf16_f32 v198,v198,v199
s_nop 1
v_permlane16_swap_b32 v196,v198
v_cvt_pk_bf16_f32 v199,v16,v17
s_nop 1
v_permlane16_swap_b32 v197,v199
buffer_store_dwordx4 v[196:199],v40,s[20:23],s43 offen offset:256
s_or_b32 s62,s42,s40
s_cmp_lg_u32 s62,0
s_cbranch_scc1 L_noep_%=
v_mov_b32 v12,s68
s_mov_b64 exec,1
buffer_store_dword v12,v25,s[64:67],0 offen sc1
s_mov_b64 exec,-1
L_noep_%=:
s_endpgm
L_dual_%=:
v_mfma_f32_16x16x32_bf16 a[12:15],v[34:37],v[72:75],a[12:15]
v_mfma_f32_16x16x32_bf16 a[12:15],v[34:37],v[88:91],a[12:15]
v_mfma_f32_16x16x32_bf16 a[12:15],v[34:37],v[104:107],a[12:15]
v_mfma_f32_16x16x32_bf16 a[28:31],v[32:35],v[72:75],a[28:31]
v_mfma_f32_16x16x32_bf16 a[28:31],v[32:35],v[88:91],a[28:31]
v_mfma_f32_16x16x32_bf16 a[28:31],v[32:35],v[104:107],a[28:31]
v_mfma_f32_16x16x32_bf16 a[44:47],v[34:37],v[76:79],a[44:47]
v_mfma_f32_16x16x32_bf16 a[44:47],v[34:37],v[92:95],a[44:47]
v_mfma_f32_16x16x32_bf16 a[44:47],v[34:37],v[108:111],a[44:47]
s_nop 1
.p2align 3
v_accvgpr_read_b32 v176,a12
v_accvgpr_read_b32 v177,a13
v_cvt_pk_bf16_f32 v176,v176,v177
v_accvgpr_read_b32 v178,a14
v_accvgpr_read_b32 v179,a15
v_cvt_pk_bf16_f32 v177,v178,v179
v_mfma_f32_16x16x32_bf16 a[60:63],v[32:35],v[76:79],a[60:63]
v_mfma_f32_16x16x32_bf16 a[60:63],v[32:35],v[92:95],a[60:63]
v_mfma_f32_16x16x32_bf16 a[60:63],v[32:35],v[108:111],a[60:63]
.p2align 3
v_accvgpr_read_b32 v178,a28
v_accvgpr_read_b32 v179,a29
v_accvgpr_read_b32 v12,a30
v_accvgpr_read_b32 v13,a31
v_cvt_pk_bf16_f32 v178,v178,v179
v_cvt_pk_bf16_f32 v179,v12,v13
s_nop 3
v_permlane16_swap_b32 v176,v178
v_permlane16_swap_b32 v177,v179
buffer_store_dwordx4 v[176:179],v40,s[20:23],s43 offen offset:128 sc1
.p2align 3
v_accvgpr_read_b32 v192,a44
v_accvgpr_read_b32 v193,a45
v_cvt_pk_bf16_f32 v192,v192,v193
v_accvgpr_read_b32 v194,a46
v_accvgpr_read_b32 v195,a47
v_cvt_pk_bf16_f32 v193,v194,v195
v_accvgpr_read_b32 v194,a60
v_accvgpr_read_b32 v195,a61
v_accvgpr_read_b32 v14,a62
v_accvgpr_read_b32 v15,a63
v_cvt_pk_bf16_f32 v194,v194,v195
s_nop 1
v_permlane16_swap_b32 v192,v194
v_cvt_pk_bf16_f32 v195,v14,v15
s_nop 1
v_permlane16_swap_b32 v193,v195
buffer_store_dwordx4 v[192:195],v40,s[20:23],s43 offen offset:192 sc1
s_waitcnt vmcnt(4)
v_mfma_f32_16x16x32_bf16 a[76:79],v[34:37],v[136:139],a[76:79]
s_waitcnt vmcnt(3)
v_mfma_f32_16x16x32_bf16 a[76:79],v[34:37],v[168:171],a[76:79]
s_waitcnt vmcnt(2)
v_mfma_f32_16x16x32_bf16 a[76:79],v[34:37],v[200:203],a[76:79]
v_mfma_f32_16x16x32_bf16 a[92:95],v[32:35],v[136:139],a[92:95]
v_mfma_f32_16x16x32_bf16 a[92:95],v[32:35],v[168:171],a[92:95]
v_mfma_f32_16x16x32_bf16 a[92:95],v[32:35],v[200:203],a[92:95]
s_nop 3
s_nop 0
.p2align 3
v_accvgpr_read_b32 v196,a76
v_accvgpr_read_b32 v197,a77
v_cvt_pk_bf16_f32 v196,v196,v197
v_accvgpr_read_b32 v198,a78
v_accvgpr_read_b32 v199,a79
v_cvt_pk_bf16_f32 v197,v198,v199
v_accvgpr_read_b32 v198,a92
v_accvgpr_read_b32 v199,a93
v_accvgpr_read_b32 v16,a94
v_accvgpr_read_b32 v17,a95
v_cvt_pk_bf16_f32 v198,v198,v199
s_nop 1
v_permlane16_swap_b32 v196,v198
v_cvt_pk_bf16_f32 v199,v16,v17
s_nop 1
v_permlane16_swap_b32 v197,v199
buffer_store_dwordx4 v[196:199],v40,s[20:23],s43 offen offset:256 sc1
s_or_b32 s62,s42,s40
s_cmp_lg_u32 s62,0
s_cbranch_scc1 L_noep2_%=
v_mov_b32 v12,s68
s_mov_b64 exec,1
buffer_store_dword v12,v25,s[64:67],0 offen sc1
s_mov_b64 exec,-1
L_noep2_%=:
s_endpgm
L_ps2_%=:
s_min_i32 s79,s79,100
s_add_u32 s78,s78,s79
L_pl2_%=:
s_sleep 1
s_sub_u32 s79,s79,4
s_cmp_gt_i32 s79,1
s_cbranch_scc1 L_pl2_%=
s_branch L_pr2_%=
L_ps3_%=:
s_min_i32 s79,s79,100
s_add_u32 s78,s78,s79
L_pl3_%=:
s_sleep 1
s_sub_u32 s79,s79,4
s_cmp_gt_i32 s79,1
s_cbranch_scc1 L_pl3_%=
s_branch L_pr3_%=
L_ps4_%=:
s_min_i32 s79,s79,100
s_add_u32 s78,s78,s79
L_pl4_%=:
s_sleep 1
s_sub_u32 s79,s79,4
s_cmp_gt_i32 s79,1
s_cbranch_scc1 L_pl4_%=
s_branch L_pr4_%=
L_ps5_%=:
s_min_i32 s79,s79,100
s_add_u32 s78,s78,s79
L_pl5_%=:
s_sleep 1
s_sub_u32 s79,s79,4
s_cmp_gt_i32 s79,1
s_cbranch_scc1 L_pl5_%=
s_branch L_pr5_%=
L_ps6_%=:
s_min_i32 s79,s79,100
s_add_u32 s78,s78,s79
L_pl6_%=:
s_sleep 1
s_sub_u32 s79,s79,4
s_cmp_gt_i32 s79,1
s_cbranch_scc1 L_pl6_%=
s_branch L_pr6_%=
L_ps7_%=:
s_min_i32 s79,s79,100
s_add_u32 s78,s78,s79
L_pl7_%=:
s_sleep 1
s_sub_u32 s79,s79,4
s_cmp_gt_i32 s79,1
s_cbranch_scc1 L_pl7_%=
s_branch L_pr7_%=
L_fr8_%=:
s_mov_b32 s88,262144
L_fl8_%=:
buffer_load_dwordx2 v[4:5],v24,s[64:67],s94 offen sc1
s_waitcnt vmcnt(0)
s_sub_u32 s88,s88,1
s_cmp_eq_u32 s88,0
s_cbranch_scc1 L_fo8_%=
v_cmp_ne_u32 s[90:91],s68,v4
v_and_b32 v4,-2,v5
v_cmp_ne_u32 s[92:93],s69,v4
s_nop 0
s_or_b64 s[90:91],s[90:91],s[92:93]
s_cbranch_scc1 L_fl8_%=
s_branch L_fo8_%=
L_fr9_%=:
s_mov_b32 s88,262144
L_fl9_%=:
buffer_load_dwordx2 v[4:5],v24,s[64:67],s94 offen offset:1024 sc1
s_waitcnt vmcnt(0)
s_sub_u32 s88,s88,1
s_cmp_eq_u32 s88,0
s_cbranch_scc1 L_fo9_%=
v_cmp_ne_u32 s[90:91],s68,v4
v_and_b32 v4,-2,v5
v_cmp_ne_u32 s[92:93],s69,v4
s_nop 0
s_or_b64 s[90:91],s[90:91],s[92:93]
s_cbranch_scc1 L_fl9_%=
s_branch L_fo9_%=
L_fr10_%=:
s_mov_b32 s88,262144
L_fl10_%=:
buffer_load_dwordx2 v[4:5],v24,s[64:67],s94 offen sc1
s_waitcnt vmcnt(0)
s_sub_u32 s88,s88,1
s_cmp_eq_u32 s88,0
s_cbranch_scc1 L_fo10_%=
v_cmp_ne_u32 s[90:91],s68,v4
v_and_b32 v4,-2,v5
v_cmp_ne_u32 s[92:93],s69,v4
s_nop 0
s_or_b64 s[90:91],s[90:91],s[92:93]
s_cbranch_scc1 L_fl10_%=
s_branch L_fo10_%=
L_fr11_%=:
s_mov_b32 s88,262144
L_fl11_%=:
buffer_load_dwordx2 v[4:5],v24,s[64:67],s94 offen offset:2048 sc1
s_waitcnt vmcnt(0)
s_sub_u32 s88,s88,1
s_cmp_eq_u32 s88,0
s_cbranch_scc1 L_fo11_%=
v_cmp_ne_u32 s[90:91],s68,v4
v_and_b32 v4,-2,v5
v_cmp_ne_u32 s[92:93],s69,v4
s_nop 0
s_or_b64 s[90:91],s[90:91],s[92:93]
s_cbranch_scc1 L_fl11_%=
s_branch L_fo11_%=
L_fr12_%=:
s_mov_b32 s88,262144
L_fl12_%=:
buffer_load_dwordx2 v[4:5],v24,s[64:67],s94 offen offset:3072 sc1
s_waitcnt vmcnt(0)
s_sub_u32 s88,s88,1
s_cmp_eq_u32 s88,0
s_cbranch_scc1 L_fo12_%=
v_cmp_ne_u32 s[90:91],s68,v4
v_and_b32 v4,-2,v5
v_cmp_ne_u32 s[92:93],s69,v4
s_nop 0
s_or_b64 s[90:91],s[90:91],s[92:93]
s_cbranch_scc1 L_fl12_%=
s_branch L_fo12_%=
L_out_end_%=:
L_fast_end_%=:
L_slow0_%=:
s_mov_b32 s98,0
s_branch L_slowc_%=
L_slow1_%=:
s_mov_b32 s98,1
L_slowc_%=:
s_nop 7
s_nop 7
s_waitcnt vmcnt(0) lgkmcnt(0)
s_lshl_b32 s96,%1,2
s_add_u32 s96,s96,s40
s_lshl_b32 s96,s96,15
s_add_u32 s96,s96,167772160
s_mov_b64 s[56:57],s[12:13]
s_add_u32 s56,s56,s96
s_addc_u32 s57,s57,0
s_and_b32 s57,s57,0xffff
s_mov_b32 s58,32768
s_mov_b32 s59,0x20000
s_mov_b32 s96,0
buffer_store_dwordx4 v[64:67],v6,s[56:59],s96 offen
buffer_store_dwordx4 v[68:71],v6,s[56:59],s96 offen offset:1024
buffer_store_dwordx4 v[72:75],v6,s[56:59],s96 offen offset:2048
buffer_store_dwordx4 v[76:79],v6,s[56:59],s96 offen offset:3072
s_movk_i32 s96,4096
buffer_store_dwordx4 v[80:83],v6,s[56:59],s96 offen
buffer_store_dwordx4 v[84:87],v6,s[56:59],s96 offen offset:1024
buffer_store_dwordx4 v[88:91],v6,s[56:59],s96 offen offset:2048
buffer_store_dwordx4 v[92:95],v6,s[56:59],s96 offen offset:3072
s_movk_i32 s96,8192
buffer_store_dwordx4 v[96:99],v6,s[56:59],s96 offen
buffer_store_dwordx4 v[100:103],v6,s[56:59],s96 offen offset:1024
buffer_store_dwordx4 v[104:107],v6,s[56:59],s96 offen offset:2048
buffer_store_dwordx4 v[108:111],v6,s[56:59],s96 offen offset:3072
s_movk_i32 s96,12288
buffer_store_dwordx4 v[112:115],v6,s[56:59],s96 offen
buffer_store_dwordx4 v[116:119],v6,s[56:59],s96 offen offset:1024
buffer_store_dwordx4 v[120:123],v6,s[56:59],s96 offen offset:2048
buffer_store_dwordx4 v[124:127],v6,s[56:59],s96 offen offset:3072
s_movk_i32 s96,16384
buffer_store_dwordx4 v[128:131],v6,s[56:59],s96 offen
)A"
R"A(buffer_store_dwordx4 v[132:135],v6,s[56:59],s96 offen offset:1024
buffer_store_dwordx4 v[136:139],v6,s[56:59],s96 offen offset:2048
buffer_store_dwordx4 v[140:143],v6,s[56:59],s96 offen offset:3072
s_movk_i32 s96,20480
buffer_store_dwordx4 v[144:147],v6,s[56:59],s96 offen
buffer_store_dwordx4 v[148:151],v6,s[56:59],s96 offen offset:1024
buffer_store_dwordx4 v[152:155],v6,s[56:59],s96 offen offset:2048
buffer_store_dwordx4 v[156:159],v6,s[56:59],s96 offen offset:3072
s_movk_i32 s96,24576
buffer_store_dwordx4 v[160:163],v6,s[56:59],s96 offen
buffer_store_dwordx4 v[164:167],v6,s[56:59],s96 offen offset:1024
buffer_store_dwordx4 v[168:171],v6,s[56:59],s96 offen offset:2048
buffer_store_dwordx4 v[172:175],v6,s[56:59],s96 offen offset:3072
s_movk_i32 s96,28672
buffer_store_dwordx4 v[176:179],v6,s[56:59],s96 offen
buffer_store_dwordx4 v[180:183],v6,s[56:59],s96 offen offset:1024
buffer_store_dwordx4 v[184:187],v6,s[56:59],s96 offen offset:2048
buffer_store_dwordx4 v[188:191],v6,s[56:59],s96 offen offset:3072
s_waitcnt vmcnt(0)
v_writelane_b32 v173,s88,0
v_writelane_b32 v173,s89,1
v_writelane_b32 v173,s90,2
v_writelane_b32 v173,s91,3
v_writelane_b32 v173,s92,4
v_writelane_b32 v173,s93,5
v_writelane_b32 v173,s94,6
v_writelane_b32 v173,s95,7
s_mov_b32 s88,s98
s_mul_i32 s90,s41,307200
s_mov_b64 s[92:93],s[4:5]
s_add_u32 s92,s92,s90
s_addc_u32 s93,s93,0
s_and_b32 s93,s93,0xffff
s_mov_b32 s94,307200
s_mov_b32 s95,0x20000
s_mov_b64 s[96:97],s[10:11]
s_and_b32 s97,s97,0xffff
s_mov_b32 s98,5242880
s_mov_b32 s99,0x20000
v_bfe_u32 v171,v6,4,4
v_lshrrev_b32 v172,8,v6
v_lshlrev_b32 v164,8,v171
v_lshl_add_u32 v164,v172,4,v164
v_lshlrev_b32 v165,10,v172
v_lshl_add_u32 v165,v171,1,v165
v_add_u32 v166,4096,v165
v_lshlrev_b32 v168,16,v172
v_lshrrev_b32 v170,2,v171
v_lshl_add_u32 v168,v170,4,v168
v_and_b32 v170,3,v171
v_lshl_add_u32 v168,v170,1,v168
s_lshl_b32 s90,s42,1
s_add_u32 s90,s90,s88
s_lshl_b32 s90,s90,8
v_add_u32 v168,s90,v168
s_mov_b32 s89,0
L_sjb_%=:
s_add_u32 s90,s89,s70
s_add_u32 s90,s90,1
s_and_b32 s90,s90,3
s_lshl_b32 s91,s40,2
s_add_u32 s90,s90,s91
s_lshl_b32 s90,s90,4
v_bfe_u32 v171,v6,4,4
v_lshrrev_b32 v172,8,v6
v_add_u32 v167,s90,v171
v_mul_u32_u24 v167,1280,v167
v_lshl_add_u32 v167,v172,4,v167
s_lshl_b32 s90,s88,5
s_add_u32 s90,s90,s89
s_and_b32 s91,s89,2
s_add_u32 s90,s90,s91
s_lshl_b32 s90,s90,12
v_add_u32 v169,s90,v6
v_mov_b32 v64,0
v_mov_b32 v65,0
v_mov_b32 v66,0
v_mov_b32 v67,0
v_mov_b32 v68,0
v_mov_b32 v69,0
v_mov_b32 v70,0
v_mov_b32 v71,0
v_mov_b32 v72,0
v_mov_b32 v73,0
v_mov_b32 v74,0
v_mov_b32 v75,0
v_mov_b32 v76,0
v_mov_b32 v77,0
v_mov_b32 v78,0
v_mov_b32 v79,0
v_mov_b32 v80,0
v_mov_b32 v81,0
v_mov_b32 v82,0
v_mov_b32 v83,0
v_mov_b32 v84,0
v_mov_b32 v85,0
v_mov_b32 v86,0
v_mov_b32 v87,0
v_mov_b32 v88,0
v_mov_b32 v89,0
v_mov_b32 v90,0
v_mov_b32 v91,0
v_mov_b32 v92,0
v_mov_b32 v93,0
v_mov_b32 v94,0
v_mov_b32 v95,0
s_mov_b32 s90,0
L_sq_%=:
s_lshl_b32 s91,s90,6
buffer_load_dwordx4 v[160:163],v167,s[92:95],s91 offen
s_lshl_b32 s91,s90,18
buffer_load_ushort v112,v168,s[96:99],s91 offen
buffer_load_ushort v120,v168,s[96:99],s91 offen offset:8
buffer_load_ushort v128,v168,s[96:99],s91 offen offset:64
buffer_load_ushort v136,v168,s[96:99],s91 offen offset:72
s_addk_i32 s91,8192
buffer_load_ushort v113,v168,s[96:99],s91 offen
buffer_load_ushort v121,v168,s[96:99],s91 offen offset:8
buffer_load_ushort v129,v168,s[96:99],s91 offen offset:64
buffer_load_ushort v137,v168,s[96:99],s91 offen offset:72
s_addk_i32 s91,8192
buffer_load_ushort v114,v168,s[96:99],s91 offen
buffer_load_ushort v122,v168,s[96:99],s91 offen offset:8
buffer_load_ushort v130,v168,s[96:99],s91 offen offset:64
buffer_load_ushort v138,v168,s[96:99],s91 offen offset:72
s_addk_i32 s91,8192
buffer_load_ushort v115,v168,s[96:99],s91 offen
buffer_load_ushort v123,v168,s[96:99],s91 offen offset:8
buffer_load_ushort v131,v168,s[96:99],s91 offen offset:64
buffer_load_ushort v139,v168,s[96:99],s91 offen offset:72
s_addk_i32 s91,8192
buffer_load_ushort v116,v168,s[96:99],s91 offen
buffer_load_ushort v124,v168,s[96:99],s91 offen offset:8
buffer_load_ushort v132,v168,s[96:99],s91 offen offset:64
buffer_load_ushort v140,v168,s[96:99],s91 offen offset:72
s_addk_i32 s91,8192
buffer_load_ushort v117,v168,s[96:99],s91 offen
buffer_load_ushort v125,v168,s[96:99],s91 offen offset:8
buffer_load_ushort v133,v168,s[96:99],s91 offen offset:64
buffer_load_ushort v141,v168,s[96:99],s91 offen offset:72
s_addk_i32 s91,8192
buffer_load_ushort v118,v168,s[96:99],s91 offen
buffer_load_ushort v126,v168,s[96:99],s91 offen offset:8
buffer_load_ushort v134,v168,s[96:99],s91 offen offset:64
buffer_load_ushort v142,v168,s[96:99],s91 offen offset:72
s_addk_i32 s91,8192
buffer_load_ushort v119,v168,s[96:99],s91 offen
buffer_load_ushort v127,v168,s[96:99],s91 offen offset:8
buffer_load_ushort v135,v168,s[96:99],s91 offen offset:64
buffer_load_ushort v143,v168,s[96:99],s91 offen offset:72
s_addk_i32 s91,8192
s_waitcnt vmcnt(0)
v_lshl_or_b32 v112,v113,16,v112
v_lshl_or_b32 v113,v115,16,v114
v_lshl_or_b32 v114,v117,16,v116
v_lshl_or_b32 v115,v119,16,v118
v_lshl_or_b32 v120,v121,16,v120
v_lshl_or_b32 v121,v123,16,v122
v_lshl_or_b32 v122,v125,16,v124
v_lshl_or_b32 v123,v127,16,v126
v_lshl_or_b32 v128,v129,16,v128
v_lshl_or_b32 v129,v131,16,v130
v_lshl_or_b32 v130,v133,16,v132
v_lshl_or_b32 v131,v135,16,v134
v_lshl_or_b32 v136,v137,16,v136
v_lshl_or_b32 v137,v139,16,v138
v_lshl_or_b32 v138,v141,16,v140
v_lshl_or_b32 v139,v143,16,v142
s_nop 1
v_mfma_f32_16x16x32_bf16 v[64:67],v[112:115],v[160:163],v[64:67]
v_mfma_f32_16x16x32_bf16 v[68:71],v[120:123],v[160:163],v[68:71]
v_mfma_f32_16x16x32_bf16 v[72:75],v[128:131],v[160:163],v[72:75]
v_mfma_f32_16x16x32_bf16 v[76:79],v[136:139],v[160:163],v[76:79]
s_nop 7
s_lshl_b32 s91,s90,18
buffer_load_ushort v112,v168,s[96:99],s91 offen offset:128
buffer_load_ushort v120,v168,s[96:99],s91 offen offset:136
buffer_load_ushort v128,v168,s[96:99],s91 offen offset:192
buffer_load_ushort v136,v168,s[96:99],s91 offen offset:200
s_addk_i32 s91,8192
buffer_load_ushort v113,v168,s[96:99],s91 offen offset:128
buffer_load_ushort v121,v168,s[96:99],s91 offen offset:136
buffer_load_ushort v129,v168,s[96:99],s91 offen offset:192
buffer_load_ushort v137,v168,s[96:99],s91 offen offset:200
s_addk_i32 s91,8192
buffer_load_ushort v114,v168,s[96:99],s91 offen offset:128
buffer_load_ushort v122,v168,s[96:99],s91 offen offset:136
buffer_load_ushort v130,v168,s[96:99],s91 offen offset:192
buffer_load_ushort v138,v168,s[96:99],s91 offen offset:200
s_addk_i32 s91,8192
buffer_load_ushort v115,v168,s[96:99],s91 offen offset:128
buffer_load_ushort v123,v168,s[96:99],s91 offen offset:136
buffer_load_ushort v131,v168,s[96:99],s91 offen offset:192
buffer_load_ushort v139,v168,s[96:99],s91 offen offset:200
s_addk_i32 s91,8192
buffer_load_ushort v116,v168,s[96:99],s91 offen offset:128
buffer_load_ushort v124,v168,s[96:99],s91 offen offset:136
buffer_load_ushort v132,v168,s[96:99],s91 offen offset:192
buffer_load_ushort v140,v168,s[96:99],s91 offen offset:200
s_addk_i32 s91,8192
buffer_load_ushort v117,v168,s[96:99],s91 offen offset:128
buffer_load_ushort v125,v168,s[96:99],s91 offen offset:136
buffer_load_ushort v133,v168,s[96:99],s91 offen offset:192
buffer_load_ushort v141,v168,s[96:99],s91 offen offset:200
s_addk_i32 s91,8192
buffer_load_ushort v118,v168,s[96:99],s91 offen offset:128
buffer_load_ushort v126,v168,s[96:99],s91 offen offset:136
buffer_load_ushort v134,v168,s[96:99],s91 offen offset:192
buffer_load_ushort v142,v168,s[96:99],s91 offen offset:200
s_addk_i32 s91,8192
buffer_load_ushort v119,v168,s[96:99],s91 offen offset:128
buffer_load_ushort v127,v168,s[96:99],s91 offen offset:136
buffer_load_ushort v135,v168,s[96:99],s91 offen offset:192
buffer_load_ushort v143,v168,s[96:99],s91 offen offset:200
s_addk_i32 s91,8192
s_waitcnt vmcnt(0)
v_lshl_or_b32 v112,v113,16,v112
v_lshl_or_b32 v113,v115,16,v114
v_lshl_or_b32 v114,v117,16,v116
v_lshl_or_b32 v115,v119,16,v118
v_lshl_or_b32 v120,v121,16,v120
v_lshl_or_b32 v121,v123,16,v122
v_lshl_or_b32 v122,v125,16,v124
v_lshl_or_b32 v123,v127,16,v126
v_lshl_or_b32 v128,v129,16,v128
v_lshl_or_b32 v129,v131,16,v130
v_lshl_or_b32 v130,v133,16,v132
v_lshl_or_b32 v131,v135,16,v134
v_lshl_or_b32 v136,v137,16,v136
v_lshl_or_b32 v137,v139,16,v138
v_lshl_or_b32 v138,v141,16,v140
v_lshl_or_b32 v139,v143,16,v142
s_nop 1
v_mfma_f32_16x16x32_bf16 v[80:83],v[112:115],v[160:163],v[80:83]
v_mfma_f32_16x16x32_bf16 v[84:87],v[120:123],v[160:163],v[84:87]
v_mfma_f32_16x16x32_bf16 v[88:91],v[128:131],v[160:163],v[88:91]
v_mfma_f32_16x16x32_bf16 v[92:95],v[136:139],v[160:163],v[92:95]
s_nop 7
s_add_u32 s90,s90,1
s_cmp_lg_u32 s90,20
s_cbranch_scc1 L_sq_%=
s_nop 7
v_mul_f32 v64,s61,v64
v_mul_f32 v65,s61,v65
v_mul_f32 v66,s61,v66
v_mul_f32 v67,s61,v67
v_mul_f32 v68,s61,v68
v_mul_f32 v69,s61,v69
v_mul_f32 v70,s61,v70
v_mul_f32 v71,s61,v71
v_mul_f32 v72,s61,v72
v_mul_f32 v73,s61,v73
v_mul_f32 v74,s61,v74
v_mul_f32 v75,s61,v75
v_mul_f32 v76,s61,v76
v_mul_f32 v77,s61,v77
v_mul_f32 v78,s61,v78
v_mul_f32 v79,s61,v79
v_mul_f32 v80,s61,v80
v_mul_f32 v81,s61,v81
v_mul_f32 v82,s61,v82
v_mul_f32 v83,s61,v83
v_mul_f32 v84,s61,v84
v_mul_f32 v85,s61,v85
v_mul_f32 v86,s61,v86
v_mul_f32 v87,s61,v87
v_mul_f32 v88,s61,v88
v_mul_f32 v89,s61,v89
v_mul_f32 v90,s61,v90
v_mul_f32 v91,s61,v91
v_mul_f32 v92,s61,v92
v_mul_f32 v93,s61,v93
v_mul_f32 v94,s61,v94
v_mul_f32 v95,s61,v95
.p2align 3
v_cvt_pk_bf16_f32 v96,v64,v65
v_cvt_pk_bf16_f32 v97,v66,v67
v_cvt_pk_bf16_f32 v98,v68,v69
v_cvt_pk_bf16_f32 v99,v70,v71
v_cvt_pk_bf16_f32 v100,v72,v73
v_cvt_pk_bf16_f32 v101,v74,v75
v_cvt_pk_bf16_f32 v102,v76,v77
v_cvt_pk_bf16_f32 v103,v78,v79
v_cvt_pk_bf16_f32 v104,v80,v81
v_cvt_pk_bf16_f32 v105,v82,v83
v_cvt_pk_bf16_f32 v106,v84,v85
v_cvt_pk_bf16_f32 v107,v86,v87
v_cvt_pk_bf16_f32 v108,v88,v89
v_cvt_pk_bf16_f32 v109,v90,v91
v_cvt_pk_bf16_f32 v110,v92,v93
v_cvt_pk_bf16_f32 v111,v94,v95
v_mov_b32 v170,0xff800000
s_lshl_b32 s91,s88,18
s_mov_b32 s90,64
L_s1_%=:
buffer_load_dwordx4 v[112:115],v164,s[20:23],s91 offen
buffer_load_dwordx4 v[116:119],v164,s[20:23],s91 offen offset:64
buffer_load_dwordx4 v[120:123],v164,s[20:23],s91 offen offset:128
buffer_load_dwordx4 v[124:127],v164,s[20:23],s91 offen offset:192
s_waitcnt vmcnt(0)
v_mfma_f32_16x16x32_bf16 v[144:147],v[112:115],v[96:99],0
v_mfma_f32_16x16x32_bf16 v[144:147],v[116:119],v[100:103],v[144:147]
v_mfma_f32_16x16x32_bf16 v[144:147],v[120:123],v[104:107],v[144:147]
v_mfma_f32_16x16x32_bf16 v[144:147],v[124:127],v[108:111],v[144:147]
s_nop 7
v_max3_f32 v170,v170,v144,v145
v_max3_f32 v170,v170,v146,v147
s_addk_i32 s91,4096
s_sub_u32 s90,s90,1
s_cmp_lg_u32 s90,0
s_cbranch_scc1 L_s1_%=
v_mov_b32 v171,v170
s_nop 1
v_permlane32_swap_b32 v170,v171
v_max_f32 v170,v170,v171
v_mov_b32 v171,v170
s_nop 1
v_permlane16_swap_b32 v170,v171
v_max_f32 v170,v170,v171
v_sub_f32 v152,0,v170
v_mov_b32 v156,0
v_sub_f32 v153,0,v170
v_mov_b32 v157,0
v_sub_f32 v154,0,v170
v_mov_b32 v158,0
v_sub_f32 v155,0,v170
v_mov_b32 v159,0
v_mov_b32 v64,0
v_mov_b32 v65,0
v_mov_b32 v66,0
v_mov_b32 v67,0
v_mov_b32 v68,0
v_mov_b32 v69,0
v_mov_b32 v70,0
v_mov_b32 v71,0
v_mov_b32 v72,0
v_mov_b32 v73,0
v_mov_b32 v74,0
v_mov_b32 v75,0
v_mov_b32 v76,0
v_mov_b32 v77,0
v_mov_b32 v78,0
v_mov_b32 v79,0
v_mov_b32 v80,0
v_mov_b32 v81,0
v_mov_b32 v82,0
v_mov_b32 v83,0
v_mov_b32 v84,0
v_mov_b32 v85,0
v_mov_b32 v86,0
v_mov_b32 v87,0
v_mov_b32 v88,0
v_mov_b32 v89,0
v_mov_b32 v90,0
v_mov_b32 v91,0
v_mov_b32 v92,0
v_mov_b32 v93,0
v_mov_b32 v94,0
v_mov_b32 v95,0
s_lshl_b32 s91,s88,18
s_mov_b32 s90,32
v_add_u32 v172,4096,v164
L_s2_%=:
buffer_load_dwordx4 v[112:115],v164,s[20:23],s91 offen
buffer_load_dwordx4 v[116:119],v164,s[20:23],s91 offen offset:64
buffer_load_dwordx4 v[120:123],v164,s[20:23],s91 offen offset:128
buffer_load_dwordx4 v[124:127],v164,s[20:23],s91 offen offset:192
buffer_load_dwordx4 v[128:131],v172,s[20:23],s91 offen
buffer_load_dwordx4 v[132:135],v172,s[20:23],s91 offen offset:64
buffer_load_dwordx4 v[136:139],v172,s[20:23],s91 offen offset:128
buffer_load_dwordx4 v[140:143],v172,s[20:23],s91 offen offset:192
)A"
R"A(s_waitcnt vmcnt(0)
v_mfma_f32_16x16x32_bf16 v[144:147],v[112:115],v[96:99],v[152:155]
v_mfma_f32_16x16x32_bf16 v[144:147],v[116:119],v[100:103],v[144:147]
v_mfma_f32_16x16x32_bf16 v[144:147],v[120:123],v[104:107],v[144:147]
v_mfma_f32_16x16x32_bf16 v[144:147],v[124:127],v[108:111],v[144:147]
v_mfma_f32_16x16x32_bf16 v[148:151],v[128:131],v[96:99],v[152:155]
v_mfma_f32_16x16x32_bf16 v[148:151],v[132:135],v[100:103],v[148:151]
v_mfma_f32_16x16x32_bf16 v[148:151],v[136:139],v[104:107],v[148:151]
v_mfma_f32_16x16x32_bf16 v[148:151],v[140:143],v[108:111],v[148:151]
s_nop 7
v_exp_f32 v144,v144
v_exp_f32 v145,v145
v_exp_f32 v146,v146
v_exp_f32 v147,v147
v_exp_f32 v148,v148
v_exp_f32 v149,v149
v_exp_f32 v150,v150
v_exp_f32 v151,v151
s_nop 0
.p2align 3
v_cvt_pk_bf16_f32 v144,v144,v145
v_cvt_pk_bf16_f32 v145,v146,v147
v_cvt_pk_bf16_f32 v146,v148,v149
v_cvt_pk_bf16_f32 v147,v150,v151
buffer_load_ushort v112,v165,s[24:27],s91 offen
buffer_load_ushort v113,v165,s[24:27],s91 offen offset:256
buffer_load_ushort v114,v165,s[24:27],s91 offen offset:512
buffer_load_ushort v115,v165,s[24:27],s91 offen offset:768
buffer_load_ushort v116,v166,s[24:27],s91 offen
buffer_load_ushort v117,v166,s[24:27],s91 offen offset:256
buffer_load_ushort v118,v166,s[24:27],s91 offen offset:512
buffer_load_ushort v119,v166,s[24:27],s91 offen offset:768
buffer_load_ushort v120,v165,s[24:27],s91 offen offset:32
buffer_load_ushort v121,v165,s[24:27],s91 offen offset:288
buffer_load_ushort v122,v165,s[24:27],s91 offen offset:544
buffer_load_ushort v123,v165,s[24:27],s91 offen offset:800
buffer_load_ushort v124,v166,s[24:27],s91 offen offset:32
buffer_load_ushort v125,v166,s[24:27],s91 offen offset:288
buffer_load_ushort v126,v166,s[24:27],s91 offen offset:544
buffer_load_ushort v127,v166,s[24:27],s91 offen offset:800
buffer_load_ushort v128,v165,s[24:27],s91 offen offset:64
buffer_load_ushort v129,v165,s[24:27],s91 offen offset:320
buffer_load_ushort v130,v165,s[24:27],s91 offen offset:576
buffer_load_ushort v131,v165,s[24:27],s91 offen offset:832
buffer_load_ushort v132,v166,s[24:27],s91 offen offset:64
buffer_load_ushort v133,v166,s[24:27],s91 offen offset:320
buffer_load_ushort v134,v166,s[24:27],s91 offen offset:576
buffer_load_ushort v135,v166,s[24:27],s91 offen offset:832
buffer_load_ushort v136,v165,s[24:27],s91 offen offset:96
buffer_load_ushort v137,v165,s[24:27],s91 offen offset:352
buffer_load_ushort v138,v165,s[24:27],s91 offen offset:608
buffer_load_ushort v139,v165,s[24:27],s91 offen offset:864
buffer_load_ushort v140,v166,s[24:27],s91 offen offset:96
buffer_load_ushort v141,v166,s[24:27],s91 offen offset:352
buffer_load_ushort v142,v166,s[24:27],s91 offen offset:608
buffer_load_ushort v143,v166,s[24:27],s91 offen offset:864
s_waitcnt vmcnt(0)
v_lshl_or_b32 v112,v113,16,v112
v_lshl_or_b32 v113,v115,16,v114
v_lshl_or_b32 v114,v117,16,v116
v_lshl_or_b32 v115,v119,16,v118
v_lshl_or_b32 v120,v121,16,v120
v_lshl_or_b32 v121,v123,16,v122
v_lshl_or_b32 v122,v125,16,v124
v_lshl_or_b32 v123,v127,16,v126
v_lshl_or_b32 v128,v129,16,v128
v_lshl_or_b32 v129,v131,16,v130
v_lshl_or_b32 v130,v133,16,v132
v_lshl_or_b32 v131,v135,16,v134
v_lshl_or_b32 v136,v137,16,v136
v_lshl_or_b32 v137,v139,16,v138
v_lshl_or_b32 v138,v141,16,v140
v_lshl_or_b32 v139,v143,16,v142
s_nop 1
v_mfma_f32_16x16x32_bf16 v[64:67],v[112:115],v[144:147],v[64:67]
v_mfma_f32_16x16x32_bf16 v[68:71],v[120:123],v[144:147],v[68:71]
v_mfma_f32_16x16x32_bf16 v[72:75],v[128:131],v[144:147],v[72:75]
v_mfma_f32_16x16x32_bf16 v[76:79],v[136:139],v[144:147],v[76:79]
s_nop 7
buffer_load_ushort v112,v165,s[24:27],s91 offen offset:128
buffer_load_ushort v113,v165,s[24:27],s91 offen offset:384
buffer_load_ushort v114,v165,s[24:27],s91 offen offset:640
buffer_load_ushort v115,v165,s[24:27],s91 offen offset:896
buffer_load_ushort v116,v166,s[24:27],s91 offen offset:128
buffer_load_ushort v117,v166,s[24:27],s91 offen offset:384
buffer_load_ushort v118,v166,s[24:27],s91 offen offset:640
buffer_load_ushort v119,v166,s[24:27],s91 offen offset:896
buffer_load_ushort v120,v165,s[24:27],s91 offen offset:160
buffer_load_ushort v121,v165,s[24:27],s91 offen offset:416
buffer_load_ushort v122,v165,s[24:27],s91 offen offset:672
buffer_load_ushort v123,v165,s[24:27],s91 offen offset:928
buffer_load_ushort v124,v166,s[24:27],s91 offen offset:160
buffer_load_ushort v125,v166,s[24:27],s91 offen offset:416
buffer_load_ushort v126,v166,s[24:27],s91 offen offset:672
buffer_load_ushort v127,v166,s[24:27],s91 offen offset:928
buffer_load_ushort v128,v165,s[24:27],s91 offen offset:192
buffer_load_ushort v129,v165,s[24:27],s91 offen offset:448
buffer_load_ushort v130,v165,s[24:27],s91 offen offset:704
buffer_load_ushort v131,v165,s[24:27],s91 offen offset:960
buffer_load_ushort v132,v166,s[24:27],s91 offen offset:192
buffer_load_ushort v133,v166,s[24:27],s91 offen offset:448
buffer_load_ushort v134,v166,s[24:27],s91 offen offset:704
buffer_load_ushort v135,v166,s[24:27],s91 offen offset:960
buffer_load_ushort v136,v165,s[24:27],s91 offen offset:224
buffer_load_ushort v137,v165,s[24:27],s91 offen offset:480
buffer_load_ushort v138,v165,s[24:27],s91 offen offset:736
buffer_load_ushort v139,v165,s[24:27],s91 offen offset:992
buffer_load_ushort v140,v166,s[24:27],s91 offen offset:224
buffer_load_ushort v141,v166,s[24:27],s91 offen offset:480
buffer_load_ushort v142,v166,s[24:27],s91 offen offset:736
buffer_load_ushort v143,v166,s[24:27],s91 offen offset:992
s_waitcnt vmcnt(0)
v_lshl_or_b32 v112,v113,16,v112
v_lshl_or_b32 v113,v115,16,v114
v_lshl_or_b32 v114,v117,16,v116
v_lshl_or_b32 v115,v119,16,v118
v_lshl_or_b32 v120,v121,16,v120
v_lshl_or_b32 v121,v123,16,v122
v_lshl_or_b32 v122,v125,16,v124
v_lshl_or_b32 v123,v127,16,v126
v_lshl_or_b32 v128,v129,16,v128
v_lshl_or_b32 v129,v131,16,v130
v_lshl_or_b32 v130,v133,16,v132
v_lshl_or_b32 v131,v135,16,v134
v_lshl_or_b32 v136,v137,16,v136
v_lshl_or_b32 v137,v139,16,v138
v_lshl_or_b32 v138,v141,16,v140
v_lshl_or_b32 v139,v143,16,v142
s_nop 1
v_mfma_f32_16x16x32_bf16 v[80:83],v[112:115],v[144:147],v[80:83]
v_mfma_f32_16x16x32_bf16 v[84:87],v[120:123],v[144:147],v[84:87]
v_mfma_f32_16x16x32_bf16 v[88:91],v[128:131],v[144:147],v[88:91]
v_mfma_f32_16x16x32_bf16 v[92:95],v[136:139],v[144:147],v[92:95]
s_nop 7
v_mfma_f32_16x16x32_bf16 v[156:159],v[28:31],v[144:147],v[156:159]
s_nop 7
s_addk_i32 s91,8192
s_sub_u32 s90,s90,1
s_cmp_lg_u32 s90,0
s_cbranch_scc1 L_s2_%=
v_rcp_f32 v170,v156
s_nop 0
v_mul_f32 v64,v170,v64
v_mul_f32 v65,v170,v65
v_mul_f32 v66,v170,v66
v_mul_f32 v67,v170,v67
v_mul_f32 v68,v170,v68
v_mul_f32 v69,v170,v69
v_mul_f32 v70,v170,v70
v_mul_f32 v71,v170,v71
v_mul_f32 v72,v170,v72
v_mul_f32 v73,v170,v73
v_mul_f32 v74,v170,v74
v_mul_f32 v75,v170,v75
v_mul_f32 v76,v170,v76
v_mul_f32 v77,v170,v77
v_mul_f32 v78,v170,v78
v_mul_f32 v79,v170,v79
v_mul_f32 v80,v170,v80
v_mul_f32 v81,v170,v81
v_mul_f32 v82,v170,v82
v_mul_f32 v83,v170,v83
v_mul_f32 v84,v170,v84
v_mul_f32 v85,v170,v85
v_mul_f32 v86,v170,v86
v_mul_f32 v87,v170,v87
v_mul_f32 v88,v170,v88
v_mul_f32 v89,v170,v89
v_mul_f32 v90,v170,v90
v_mul_f32 v91,v170,v91
v_mul_f32 v92,v170,v92
v_mul_f32 v93,v170,v93
v_mul_f32 v94,v170,v94
v_mul_f32 v95,v170,v95
.p2align 3
v_cvt_pk_bf16_f32 v96,v64,v65
v_cvt_pk_bf16_f32 v97,v66,v67
v_cvt_pk_bf16_f32 v98,v68,v69
v_cvt_pk_bf16_f32 v99,v70,v71
v_cvt_pk_bf16_f32 v100,v72,v73
v_cvt_pk_bf16_f32 v101,v74,v75
v_cvt_pk_bf16_f32 v102,v76,v77
v_cvt_pk_bf16_f32 v103,v78,v79
v_cvt_pk_bf16_f32 v104,v80,v81
v_cvt_pk_bf16_f32 v105,v82,v83
v_cvt_pk_bf16_f32 v106,v84,v85
v_cvt_pk_bf16_f32 v107,v86,v87
v_cvt_pk_bf16_f32 v108,v88,v89
v_cvt_pk_bf16_f32 v109,v90,v91
v_cvt_pk_bf16_f32 v110,v92,v93
v_cvt_pk_bf16_f32 v111,v94,v95
buffer_store_dwordx4 v[96:99],v169,s[36:39],s55 offen sc1
buffer_store_dwordx4 v[100:103],v169,s[36:39],s55 offen offset:1024 sc1
buffer_store_dwordx4 v[104:107],v169,s[36:39],s55 offen offset:2048 sc1
buffer_store_dwordx4 v[108:111],v169,s[36:39],s55 offen offset:3072 sc1
s_waitcnt vmcnt(0)
s_add_u32 s89,s89,1
s_cmp_lg_u32 s89,4
s_cbranch_scc1 L_sjb_%=
s_mov_b32 s98,s88
v_readlane_b32 s88,v173,0
v_readlane_b32 s89,v173,1
v_readlane_b32 s90,v173,2
v_readlane_b32 s91,v173,3
v_readlane_b32 s92,v173,4
v_readlane_b32 s93,v173,5
v_readlane_b32 s94,v173,6
v_readlane_b32 s95,v173,7
s_nop 4
s_mov_b32 s96,0
buffer_load_dwordx4 v[64:67],v6,s[56:59],s96 offen sc1
buffer_load_dwordx4 v[68:71],v6,s[56:59],s96 offen offset:1024 sc1
buffer_load_dwordx4 v[72:75],v6,s[56:59],s96 offen offset:2048 sc1
buffer_load_dwordx4 v[76:79],v6,s[56:59],s96 offen offset:3072 sc1
s_movk_i32 s96,4096
buffer_load_dwordx4 v[80:83],v6,s[56:59],s96 offen sc1
buffer_load_dwordx4 v[84:87],v6,s[56:59],s96 offen offset:1024 sc1
buffer_load_dwordx4 v[88:91],v6,s[56:59],s96 offen offset:2048 sc1
buffer_load_dwordx4 v[92:95],v6,s[56:59],s96 offen offset:3072 sc1
s_movk_i32 s96,8192
buffer_load_dwordx4 v[96:99],v6,s[56:59],s96 offen sc1
buffer_load_dwordx4 v[100:103],v6,s[56:59],s96 offen offset:1024 sc1
buffer_load_dwordx4 v[104:107],v6,s[56:59],s96 offen offset:2048 sc1
buffer_load_dwordx4 v[108:111],v6,s[56:59],s96 offen offset:3072 sc1
s_movk_i32 s96,12288
buffer_load_dwordx4 v[112:115],v6,s[56:59],s96 offen sc1
buffer_load_dwordx4 v[116:119],v6,s[56:59],s96 offen offset:1024 sc1
buffer_load_dwordx4 v[120:123],v6,s[56:59],s96 offen offset:2048 sc1
buffer_load_dwordx4 v[124:127],v6,s[56:59],s96 offen offset:3072 sc1
s_movk_i32 s96,16384
buffer_load_dwordx4 v[128:131],v6,s[56:59],s96 offen sc1
buffer_load_dwordx4 v[132:135],v6,s[56:59],s96 offen offset:1024 sc1
buffer_load_dwordx4 v[136:139],v6,s[56:59],s96 offen offset:2048 sc1
buffer_load_dwordx4 v[140:143],v6,s[56:59],s96 offen offset:3072 sc1
s_movk_i32 s96,20480
buffer_load_dwordx4 v[144:147],v6,s[56:59],s96 offen sc1
buffer_load_dwordx4 v[148:151],v6,s[56:59],s96 offen offset:1024 sc1
buffer_load_dwordx4 v[152:155],v6,s[56:59],s96 offen offset:2048 sc1
buffer_load_dwordx4 v[156:159],v6,s[56:59],s96 offen offset:3072 sc1
s_movk_i32 s96,24576
buffer_load_dwordx4 v[160:163],v6,s[56:59],s96 offen sc1
buffer_load_dwordx4 v[164:167],v6,s[56:59],s96 offen offset:1024 sc1
buffer_load_dwordx4 v[168:171],v6,s[56:59],s96 offen offset:2048 sc1
buffer_load_dwordx4 v[172:175],v6,s[56:59],s96 offen offset:3072 sc1
s_movk_i32 s96,28672
buffer_load_dwordx4 v[176:179],v6,s[56:59],s96 offen sc1
buffer_load_dwordx4 v[180:183],v6,s[56:59],s96 offen offset:1024 sc1
buffer_load_dwordx4 v[184:187],v6,s[56:59],s96 offen offset:2048 sc1
buffer_load_dwordx4 v[188:191],v6,s[56:59],s96 offen offset:3072 sc1
s_waitcnt vmcnt(0)
s_cmp_lg_u32 s98,0
s_cbranch_scc1 L_sr1_%=
buffer_load_dwordx4 v[64:67],v6,s[36:39],s84 offen sc1
buffer_load_dwordx4 v[68:71],v6,s[36:39],s84 offen offset:1024 sc1
buffer_load_dwordx4 v[72:75],v6,s[36:39],s84 offen offset:2048 sc1
buffer_load_dwordx4 v[76:79],v6,s[36:39],s84 offen offset:3072 sc1
buffer_load_dwordx4 v[80:83],v6,s[36:39],s85 offen sc1
buffer_load_dwordx4 v[84:87],v6,s[36:39],s85 offen offset:1024 sc1
buffer_load_dwordx4 v[88:91],v6,s[36:39],s85 offen offset:2048 sc1
buffer_load_dwordx4 v[92:95],v6,s[36:39],s85 offen offset:3072 sc1
buffer_load_dwordx4 v[96:99],v6,s[36:39],s86 offen sc1
buffer_load_dwordx4 v[100:103],v6,s[36:39],s86 offen offset:1024 sc1
buffer_load_dwordx4 v[104:107],v6,s[36:39],s86 offen offset:2048 sc1
buffer_load_dwordx4 v[108:111],v6,s[36:39],s86 offen offset:3072 sc1
buffer_load_dwordx4 v[112:115],v6,s[36:39],s87 offen sc1
buffer_load_dwordx4 v[116:119],v6,s[36:39],s87 offen offset:1024 sc1
buffer_load_dwordx4 v[120:123],v6,s[36:39],s87 offen offset:2048 sc1
buffer_load_dwordx4 v[124:127],v6,s[36:39],s87 offen offset:3072 sc1
s_waitcnt vmcnt(0)
s_nop 7
s_branch L_sret0_%=
L_sr1_%=:
buffer_load_dwordx4 v[136:139],v6,s[36:39],s84 offen sc1
buffer_load_dwordx4 v[140:143],v6,s[36:39],s84 offen offset:1024 sc1
buffer_load_dwordx4 v[152:155],v6,s[36:39],s84 offen offset:2048 sc1
buffer_load_dwordx4 v[156:159],v6,s[36:39],s84 offen offset:3072 sc1
buffer_load_dwordx4 v[168:171],v6,s[36:39],s85 offen sc1
buffer_load_dwordx4 v[172:175],v6,s[36:39],s85 offen offset:1024 sc1
)A"
R"A(buffer_load_dwordx4 v[184:187],v6,s[36:39],s85 offen offset:2048 sc1
buffer_load_dwordx4 v[188:191],v6,s[36:39],s85 offen offset:3072 sc1
buffer_load_dwordx4 v[200:203],v6,s[36:39],s86 offen sc1
buffer_load_dwordx4 v[204:207],v6,s[36:39],s86 offen offset:1024 sc1
buffer_load_dwordx4 v[216:219],v6,s[36:39],s86 offen offset:2048 sc1
buffer_load_dwordx4 v[220:223],v6,s[36:39],s86 offen offset:3072 sc1
buffer_load_dwordx4 v[232:235],v6,s[36:39],s87 offen sc1
buffer_load_dwordx4 v[236:239],v6,s[36:39],s87 offen offset:1024 sc1
buffer_load_dwordx4 v[248:251],v6,s[36:39],s87 offen offset:2048 sc1
buffer_load_dwordx4 v[252:255],v6,s[36:39],s87 offen offset:3072 sc1
s_waitcnt vmcnt(0)
s_nop 7
s_branch L_sret1_%=
L_end_%=:
)A"
    :: "s"(karg), "s"(blk), "s"(wid)
    : "v0", "v1", "v2", "v3", "v4", "v5", "v6", "v7", "v8", "v9", "v10", "v11", "v12", "v13", "v14", "v15", "v16", "v17", "v18", "v19", "v20", "v21", "v22", "v23", "v24", "v25", "v26", "v27", "v28", "v29", "v30", "v31", "v32", "v33", "v34", "v35", "v36", "v37", "v38", "v39", "v40", "v41", "v42", "v43", "v44", "v45", "v46", "v47", "v48", "v49", "v50", "v51", "v52", "v53", "v54", "v55", "v56", "v57", "v58", "v59", "v60", "v61", "v62", "v63", "v64", "v65", "v66", "v67", "v68", "v69", "v70", "v71", "v72", "v73", "v74", "v75", "v76", "v77", "v78", "v79", "v80", "v81", "v82", "v83", "v84", "v85", "v86", "v87", "v88", "v89", "v90", "v91", "v92", "v93", "v94", "v95", "v96", "v97", "v98", "v99", "v100", "v101", "v102", "v103", "v104", "v105", "v106", "v107", "v108", "v109", "v110", "v111", "v112", "v113", "v114", "v115", "v116", "v117", "v118", "v119", "v120", "v121", "v122", "v123", "v124", "v125", "v126", "v127", "v128", "v129", "v130", "v131", "v132", "v133", "v134", "v135", "v136", "v137", "v138", "v139", "v140", "v141", "v142", "v143", "v144", "v145", "v146", "v147", "v148", "v149", "v150", "v151", "v152", "v153", "v154", "v155", "v156", "v157", "v158", "v159", "v160", "v161", "v162", "v163", "v164", "v165", "v166", "v167", "v168", "v169", "v170", "v171", "v172", "v173", "v174", "v175", "v176", "v177", "v178", "v179", "v180", "v181", "v182", "v183", "v184", "v185", "v186", "v187", "v188", "v189", "v190", "v191", "v192", "v193", "v194", "v195", "v196", "v197", "v198", "v199", "v200", "v201", "v202", "v203", "v204", "v205", "v206", "v207", "v208", "v209", "v210", "v211", "v212", "v213", "v214", "v215", "v216", "v217", "v218", "v219", "v220", "v221", "v222", "v223", "v224", "v225", "v226", "v227", "v228", "v229", "v230", "v231", "v232", "v233", "v234", "v235", "v236", "v237", "v238", "v239", "v240", "v241", "v242", "v243", "v244", "v245", "v246", "v247", "v248", "v249", "v250", "v251", "v252", "v253", "v254", "v255", "a0", "a1", "a2", "a3", "a4", "a5", "a6", "a7", "a8", "a9", "a10", "a11", "a12", "a13", "a14", "a15", "a16", "a17", "a18", "a19", "a20", "a21", "a22", "a23", "a24", "a25", "a26", "a27", "a28", "a29", "a30", "a31", "a32", "a33", "a34", "a35", "a36", "a37", "a38", "a39", "a40", "a41", "a42", "a43", "a44", "a45", "a46", "a47", "a48", "a49", "a50", "a51", "a52", "a53", "a54", "a55", "a56", "a57", "a58", "a59", "a60", "a61", "a62", "a63", "a64", "a65", "a66", "a67", "a68", "a69", "a70", "a71", "a72", "a73", "a74", "a75", "a76", "a77", "a78", "a79", "a80", "a81", "a82", "a83", "a84", "a85", "a86", "a87", "a88", "a89", "a90", "a91", "a92", "a93", "a94", "a95", "a96", "a97", "a98", "a99", "a100", "a101", "a102", "a103", "a104", "a105", "a106", "a107", "a108", "a109", "a110", "a111", "a112", "a113", "a114", "a115", "a116", "a117", "a118", "a119", "a120", "a121", "a122", "a123", "a124", "a125", "a126", "a127", "a128", "a129", "a130", "a131", "a132", "a133", "a134", "a135", "a136", "a137", "a138", "a139", "a140", "a141", "a142", "a143", "a144", "a145", "a146", "a147", "a148", "a149", "a150", "a151", "a152", "a153", "a154", "a155", "a156", "a157", "a158", "a159", "a160", "a161", "a162", "a163", "a164", "a165", "a166", "a167", "a168", "a169", "a170", "a171", "a172", "a173", "a174", "a175", "a176", "a177", "a178", "a179", "a180", "a181", "a182", "a183", "a184", "a185", "a186", "a187", "a188", "a189", "a190", "a191", "a192", "a193", "a194", "a195", "a196", "a197", "a198", "a199", "a200", "a201", "a202", "a203", "a204", "a205", "a206", "a207", "a208", "a209", "a210", "a211", "a212", "a213", "a214", "a215", "a216", "a217", "a218", "a219", "a220", "a221", "a222", "a223", "a224", "a225", "a226", "a227", "a228", "a229", "a230", "a231", "a232", "a233", "a234", "a235", "a236", "a237", "a238", "a239", "a240", "a241", "a242", "a243", "a244", "a245", "a246", "a247", "a248", "a249", "a250", "a251", "a252", "a253", "a254", "a255", "s4", "s5", "s6", "s7", "s8", "s9", "s10", "s11", "s12", "s13", "s14", "s15", "s16", "s17", "s18", "s19", "s20", "s21", "s22", "s23", "s24", "s25", "s26", "s27", "s28", "s29", "s30", "s31", "s32", "s33", "s34", "s35", "s36", "s37", "s38", "s39", "s40", "s41", "s42", "s43", "s44", "s45", "s46", "s47", "s48", "s49", "s50", "s51", "s52", "s53", "s54", "s55", "s56", "s57", "s58", "s59", "s60", "s61", "s62", "s63", "s64", "s65", "s66", "s67", "s68", "s69", "s70", "s71", "s72", "s73", "s74", "s75", "s76", "s77", "s78", "s79", "s80", "s81", "s82", "s83", "s84", "s85", "s86", "s87", "s88", "s89", "s90", "s91", "s92", "s93", "s94", "s95", "s96", "s97", "s98", "s99", "s100", "s101", "vcc", "scc", "memory");
#endif
}

extern "C" void kernel_launch(void* const* d_in, const int* in_sizes, int n_in,
                              void* d_out, int out_size, void* d_ws, size_t ws_size,
                              hipStream_t stream) {
  (void)hipFuncSetAttribute((const void*)qattn_kernel, hipFuncAttributeMaxDynamicSharedMemorySize, LDS_ATT);
  qattn_kernel<<<256, 256, LDS_ATT, stream>>>(d_in[0], d_in[1], d_in[2], d_in[3], d_ws, d_in[4], d_out);
}
```

```cpp
#include <hip/hip_runtime.h>
#include <hip/hip_bf16.h>

constexpr int LDS_ATT = 163840;

__global__ __launch_bounds__(256, 1) void qattn_kernel(const void* __restrict__ x, const void* __restrict__ kc, const void* __restrict__ vc,
                                                       const void* __restrict__ wq, void* __restrict__ ws, const void* __restrict__ wo,
                                                       void* __restrict__ out) {
#if defined(__HIP_DEVICE_COMPILE__)
  const auto karg = __builtin_amdgcn_kernarg_segment_ptr();
  const unsigned blk = blockIdx.x;
  const unsigned wid = __builtin_amdgcn_readfirstlane(threadIdx.x >> 6);
  asm volatile(
R"A(s_memtime s[72:73]
s_memrealtime s[74:75]
s_load_dwordx8 s[4:11],%0,0x0
s_load_dwordx4 s[12:15],%0,0x20
s_load_dwordx2 s[16:17],%0,0x30
s_mov_b32 s40,%2
s_mov_b32 s43,%1
s_lshr_b32 s45,s43,3
v_mbcnt_lo_u32_b32 v12,-1,0
v_mbcnt_hi_u32_b32 v12,-1,v12
s_getpc_b64 s[88:89]
L_pc_%=:
s_and_b32 s88,s88,0xffffff80
s_and_b32 s89,s89,0xffff
s_mov_b32 s90,L_fast_end_%=-L_pc_%=
s_mov_b32 s91,0x20000
s_lshl_b32 s44,s45,2
s_add_u32 s44,s44,s40
s_mulk_i32 s44,896
v_lshlrev_b32 v67,7,v12
s_mov_b64 exec,127
buffer_load_dword v19,v67,s[88:91],s44 offen
s_mov_b64 exec,-1
s_and_b32 s70,s43,3
s_and_b32 s71,s45,3
s_lshl_b32 s42,s70,2
s_add_u32 s42,s42,s71
s_and_b32 s41,s43,4
s_lshl_b32 s41,s41,1
s_lshr_b32 s46,s45,2
s_add_u32 s41,s41,s46
s_lshl_b32 s56,s42,9
s_mov_b32 s57,0
s_lshl_b32 s59,s40,13
s_add_u32 s58,s59,65536
v_and_b32 v64,3,v12
v_bfe_u32 v65,v12,2,2
v_lshrrev_b32 v66,4,v12
s_lshl_b32 s44,s40,17
v_lshlrev_b32 v75,13,v65
v_lshl_add_u32 v75,v66,6,v75
v_lshl_add_u32 v75,v64,4,v75
v_add_u32 v75,s44,v75
v_add_u32 v56,0,v75
v_add_u32 v57,256,v75
v_add_u32 v58,32768,v75
v_add_u32 v59,33024,v75
v_add_u32 v60,65536,v75
v_add_u32 v61,65792,v75
v_add_u32 v62,98304,v75
v_add_u32 v63,98560,v75
s_waitcnt lgkmcnt(0)
s_mov_b64 s[32:33],s[10:11]
s_and_b32 s33,s33,0xffff
s_mov_b32 s34,5242880
s_mov_b32 s35,0x20000
s_add_u32 m0,s58,1024
s_nop 0
buffer_load_dwordx4 v57,s[32:35],s56 offen lds
s_add_u32 m0,s58,3072
s_nop 0
buffer_load_dwordx4 v59,s[32:35],s56 offen lds
s_add_u32 m0,s58,5120
s_nop 0
buffer_load_dwordx4 v61,s[32:35],s56 offen lds
s_add_u32 m0,s58,7168
s_nop 0
buffer_load_dwordx4 v63,s[32:35],s56 offen lds
v_lshrrev_b32 v75,3,v12
s_lshl_b32 s44,s40,6
v_add_u32 v75,s44,v75
v_mul_u32_u24 v75,1280,v75
v_and_b32 v76,7,v12
s_add_u32 s84,s70,1
s_and_b32 s84,s84,3
s_mulk_i32 s84,20480
s_addk_i32 s84,4096
s_add_u32 s85,s70,2
s_and_b32 s85,s85,3
s_mulk_i32 s85,20480
s_addk_i32 s85,2048
s_add_u32 s86,s70,3
s_and_b32 s86,s86,3
s_mulk_i32 s86,20480
s_addk_i32 s86,4096
s_add_u32 s87,s70,4
s_and_b32 s87,s87,3
s_mulk_i32 s87,20480
s_addk_i32 s87,2048
v_or_b32 v77,0,v66
v_xor_b32 v77,v77,v76
v_lshl_add_u32 v77,v77,4,v75
v_add_u32 v48,s84,v77
v_add_u32 v50,s85,v77
v_add_u32 v52,s86,v77
v_add_u32 v54,s87,v77
v_or_b32 v77,4,v66
v_xor_b32 v77,v77,v76
v_lshl_add_u32 v77,v77,4,v75
v_add_u32 v77,9216,v77
v_add_u32 v49,s84,v77
v_add_u32 v51,s85,v77
v_add_u32 v53,s86,v77
v_add_u32 v55,s87,v77
s_mul_i32 s44,s41,307200
s_mov_b64 s[28:29],s[4:5]
s_add_u32 s28,s28,s44
s_addc_u32 s29,s29,0
s_sub_u32 s28,s28,4096
s_subb_u32 s29,s29,0
s_and_b32 s29,s29,0xffff
s_mov_b32 s30,311296
s_mov_b32 s31,0x20000
s_add_u32 m0,s59,0
s_nop 0
buffer_load_dwordx4 v48,s[28:31],s57 offen lds
buffer_load_dwordx4 v49,s[28:31],s57 offen offset:1024 lds
buffer_load_dwordx4 v50,s[28:31],s57 offen offset:2048 lds
buffer_load_dwordx4 v51,s[28:31],s57 offen offset:3072 lds
s_addk_i32 m0,4096
s_nop 0
buffer_load_dwordx4 v52,s[28:31],s57 offen lds
buffer_load_dwordx4 v53,s[28:31],s57 offen offset:1024 lds
buffer_load_dwordx4 v54,s[28:31],s57 offen offset:2048 lds
buffer_load_dwordx4 v55,s[28:31],s57 offen offset:3072 lds
s_addk_i32 s57,128
s_xor_b32 s59,s59,131072
s_add_u32 m0,s58,0
s_nop 0
buffer_load_dwordx4 v56,s[32:35],s56 offen lds
s_add_u32 m0,s58,2048
s_nop 0
buffer_load_dwordx4 v58,s[32:35],s56 offen lds
s_add_u32 m0,s58,4096
s_nop 0
buffer_load_dwordx4 v60,s[32:35],s56 offen lds
s_add_u32 m0,s58,6144
s_nop 0
buffer_load_dwordx4 v62,s[32:35],s56 offen lds
s_add_u32 s56,s56,524288
s_add_u32 s58,s58,32768
s_cmp_ge_u32 s58,131072
s_cselect_b32 s62,98304,0
s_sub_u32 s58,s58,s62
s_add_u32 s12,s12,4194303
s_addc_u32 s13,s13,0
s_and_b32 s12,s12,0xffc00000
s_lshl_b32 s55,s40,15
s_lshl_b32 s84,s41,2
s_add_u32 s84,s84,s70
s_lshl_b32 s84,s84,7
s_lshl_b32 s85,s71,2
s_add_u32 s84,s84,s85
s_add_u32 s84,s84,s40
s_addk_i32 s84,112
s_lshl_b32 s84,s84,14
s_add_u32 s84,s84,s12
s_addc_u32 s85,s13,0
s_mov_b32 s87,0x13572468
s_mov_b32 s79,0
s_load_dword s78,s[84:85],0x1000 glc
s_xor_b32 s76,s84,2097152
s_mov_b32 s77,s85
s_load_dword s78,s[76:77],0x0 glc
s_load_dword s78,s[76:77],0x2000 glc
s_load_dword s78,s[76:77],0x100 glc
s_load_dword s78,s[76:77],0x2100 glc
s_load_dword s78,s[76:77],0x200 glc
s_load_dword s78,s[76:77],0x2200 glc
s_add_u32 m0,s58,0
s_nop 0
buffer_load_dwordx4 v56,s[32:35],s56 offen lds
s_addk_i32 m0,1024
s_nop 0
buffer_load_dwordx4 v57,s[32:35],s56 offen lds
s_addk_i32 m0,1024
s_nop 0
buffer_load_dwordx4 v58,s[32:35],s56 offen lds
s_addk_i32 m0,1024
s_nop 0
buffer_load_dwordx4 v59,s[32:35],s56 offen lds
s_addk_i32 m0,1024
s_nop 0
buffer_load_dwordx4 v60,s[32:35],s56 offen lds
s_addk_i32 m0,1024
s_nop 0
buffer_load_dwordx4 v61,s[32:35],s56 offen lds
s_addk_i32 m0,1024
s_nop 0
buffer_load_dwordx4 v62,s[32:35],s56 offen lds
s_addk_i32 m0,1024
s_nop 0
buffer_load_dwordx4 v63,s[32:35],s56 offen lds
s_add_u32 s56,s56,524288
s_add_u32 s58,s58,32768
s_cmp_ge_u32 s58,131072
s_cselect_b32 s62,98304,0
s_sub_u32 s58,s58,s62
s_mov_b32 s60,65536
v_lshlrev_b32 v6,4,v12
v_lshlrev_b32 v72,12,v66
v_lshl_add_u32 v72,v65,6,v72
v_lshl_add_u32 v23,v64,4,v72
v_add_u32 v22,65536,v23
v_and_b32 v74,15,v12
v_lshrrev_b32 v76,1,v74
v_lshlrev_b32 v75,7,v74
s_lshl_b32 s44,s40,13
v_add_u32 v75,s44,v75
v_or_b32 v77,0,v66
v_xor_b32 v77,v77,v76
v_lshl_add_u32 v20,v77,4,v75
v_or_b32 v77,4,v66
v_xor_b32 v77,v77,v76
v_lshl_add_u32 v21,v77,4,v75
s_waitcnt vmcnt(12)
s_nop 0
s_memtime s[64:65]
s_memrealtime s[66:67]
s_bfe_u32 s68,%1,327683
s_waitcnt lgkmcnt(0)
s_sub_u32 s64,s64,s72
s_sub_u32 s66,s66,s74
s_mul_i32 s64,s64,10
s_mulk_i32 s66,224
s_cmp_lt_u32 s64,s66
s_cselect_b32 s95,64,0
s_mov_b32 %2,s95
s_cbranch_scc1 L_se1_%=
L_sg1_%=:
s_cmp_eq_u32 s68,0
s_cbranch_scc1 L_se1_%=
s_sleep 2
s_sub_u32 s68,s68,1
s_branch L_sg1_%=
L_se1_%=:
s_barrier
ds_read_b128 a[192:195],v20
ds_read_b64_tr_b16 a[128:129],v22 offset:1024
ds_read_b64_tr_b16 a[130:131],v22 offset:3072
ds_read_b128 a[196:199],v21
ds_read_b64_tr_b16 a[132:133],v22 offset:17408
ds_read_b64_tr_b16 a[134:135],v22 offset:19456
ds_read_b128 a[204:207],v21 offset:2048
ds_read_b128 a[200:203],v20 offset:2048
ds_read_b128 a[208:211],v20 offset:4096
ds_read_b128 a[212:215],v21 offset:4096
ds_read_b128 a[220:223],v21 offset:6144
ds_read_b128 a[216:219],v20 offset:6144
ds_read_b64_tr_b16 a[136:137],v22 offset:1032
ds_read_b64_tr_b16 a[138:139],v22 offset:3080
s_waitcnt lgkmcnt(11)
v_mfma_f32_16x16x32_bf16 v[128:131],a[128:131],a[192:195],0
s_add_u32 m0,s59,0
ds_read_b64_tr_b16 a[140:141],v22 offset:17416
ds_read_b64_tr_b16 a[142:143],v22 offset:19464
s_waitcnt lgkmcnt(10)
v_mfma_f32_16x16x32_bf16 v[128:131],a[132:135],a[196:199],v[128:131]
s_waitcnt lgkmcnt(9)
v_mfma_f32_16x16x32_bf16 v[136:139],a[132:135],a[204:207],0
s_waitcnt lgkmcnt(8)
v_mfma_f32_16x16x32_bf16 v[136:139],a[128:131],a[200:203],v[136:139]
s_waitcnt lgkmcnt(7)
v_mfma_f32_16x16x32_bf16 v[144:147],a[128:131],a[208:211],0
s_waitcnt lgkmcnt(6)
buffer_load_dwordx4 v48,s[28:31],s57 offen lds
v_mfma_f32_16x16x32_bf16 v[144:147],a[132:135],a[212:215],v[144:147]
s_waitcnt lgkmcnt(5)
v_mfma_f32_16x16x32_bf16 v[152:155],a[132:135],a[220:223],0
s_waitcnt lgkmcnt(4)
v_mfma_f32_16x16x32_bf16 v[152:155],a[128:131],a[216:219],v[152:155]
s_waitcnt lgkmcnt(2)
ds_read_b64_tr_b16 a[144:145],v22 offset:1280
ds_read_b64_tr_b16 a[146:147],v22 offset:3328
v_mfma_f32_16x16x32_bf16 v[132:135],a[136:139],a[192:195],0
s_waitcnt lgkmcnt(2)
ds_read_b64_tr_b16 a[148:149],v22 offset:17664
ds_read_b64_tr_b16 a[150:151],v22 offset:19712
v_mfma_f32_16x16x32_bf16 v[132:135],a[140:143],a[196:199],v[132:135]
buffer_load_dwordx4 v49,s[28:31],s57 offen offset:1024 lds
v_mfma_f32_16x16x32_bf16 v[140:143],a[140:143],a[204:207],0
v_mfma_f32_16x16x32_bf16 v[140:143],a[136:139],a[200:203],v[140:143]
v_mfma_f32_16x16x32_bf16 v[148:151],a[136:139],a[208:211],0
v_mfma_f32_16x16x32_bf16 v[148:151],a[140:143],a[212:215],v[148:151]
v_mfma_f32_16x16x32_bf16 v[156:159],a[140:143],a[220:223],0
buffer_load_dwordx4 v50,s[28:31],s57 offen offset:2048 lds
v_mfma_f32_16x16x32_bf16 v[156:159],a[136:139],a[216:219],v[156:159]
s_waitcnt lgkmcnt(2)
ds_read_b64_tr_b16 a[152:153],v22 offset:1288
ds_read_b64_tr_b16 a[154:155],v22 offset:3336
v_mfma_f32_16x16x32_bf16 v[160:163],a[144:147],a[192:195],0
s_waitcnt lgkmcnt(2)
ds_read_b64_tr_b16 a[156:157],v22 offset:17672
ds_read_b64_tr_b16 a[158:159],v22 offset:19720
v_mfma_f32_16x16x32_bf16 v[160:163],a[148:151],a[196:199],v[160:163]
v_mfma_f32_16x16x32_bf16 v[168:171],a[148:151],a[204:207],0
v_mfma_f32_16x16x32_bf16 v[168:171],a[144:147],a[200:203],v[168:171]
buffer_load_dwordx4 v51,s[28:31],s57 offen offset:3072 lds
v_mfma_f32_16x16x32_bf16 v[176:179],a[144:147],a[208:211],0
s_addk_i32 m0,4096
v_mfma_f32_16x16x32_bf16 v[176:179],a[148:151],a[212:215],v[176:179]
v_mfma_f32_16x16x32_bf16 v[184:187],a[148:151],a[220:223],0
v_mfma_f32_16x16x32_bf16 v[184:187],a[144:147],a[216:219],v[184:187]
s_waitcnt lgkmcnt(2)
v_mfma_f32_16x16x32_bf16 v[164:167],a[152:155],a[192:195],0
s_waitcnt lgkmcnt(0)
buffer_load_dwordx4 v52,s[28:31],s57 offen lds
ds_read_b64_tr_b16 a[160:161],v22 offset:1536
ds_read_b64_tr_b16 a[162:163],v22 offset:3584
v_mfma_f32_16x16x32_bf16 v[164:167],a[156:159],a[196:199],v[164:167]
ds_read_b64_tr_b16 a[164:165],v22 offset:17920
ds_read_b64_tr_b16 a[166:167],v22 offset:19968
v_mfma_f32_16x16x32_bf16 v[172:175],a[156:159],a[204:207],0
v_mfma_f32_16x16x32_bf16 v[172:175],a[152:155],a[200:203],v[172:175]
v_mfma_f32_16x16x32_bf16 v[180:183],a[152:155],a[208:211],0
v_mfma_f32_16x16x32_bf16 v[180:183],a[156:159],a[212:215],v[180:183]
buffer_load_dwordx4 v53,s[28:31],s57 offen offset:1024 lds
v_mfma_f32_16x16x32_bf16 v[188:191],a[156:159],a[220:223],0
s_waitcnt vmcnt(14)
v_mfma_f32_16x16x32_bf16 v[188:191],a[152:155],a[216:219],v[188:191]
s_barrier
ds_read_b64_tr_b16 a[168:169],v22 offset:1544
ds_read_b64_tr_b16 a[170:171],v22 offset:3592
s_waitcnt lgkmcnt(4)
v_mfma_f32_16x16x32_bf16 v[192:195],a[160:163],a[192:195],0
s_waitcnt lgkmcnt(2)
ds_read_b64_tr_b16 a[172:173],v22 offset:17928
ds_read_b64_tr_b16 a[174:175],v22 offset:19976
v_mfma_f32_16x16x32_bf16 v[192:195],a[164:167],a[196:199],v[192:195]
buffer_load_dwordx4 v54,s[28:31],s57 offen offset:2048 lds
v_mfma_f32_16x16x32_bf16 v[200:203],a[164:167],a[204:207],0
s_lshl_b32 s44,s41,13
s_add_u32 s44,s44,134217728
s_mov_b64 s[64:65],s[12:13]
v_mfma_f32_16x16x32_bf16 v[200:203],a[160:163],a[200:203],v[200:203]
s_add_u32 s64,s64,s44
s_addc_u32 s65,s65,0
s_and_b32 s65,s65,0xffff
v_mfma_f32_16x16x32_bf16 v[208:211],a[160:163],a[208:211],0
s_movk_i32 s66,8192
s_mov_b32 s67,0x20000
v_mov_b32 v14,4096
v_mfma_f32_16x16x32_bf16 v[208:211],a[164:167],a[212:215],v[208:211]
s_mov_b64 s[72:73],s[14:15]
buffer_load_dwordx4 v55,s[28:31],s57 offen offset:3072 lds
v_mfma_f32_16x16x32_bf16 v[216:219],a[164:167],a[220:223],0
s_add_u32 m0,s58,0
s_and_b32 s73,s73,0xffff
ds_read_b64_tr_b16 a[176:177],v22 offset:1792
v_mfma_f32_16x16x32_bf16 v[216:219],a[160:163],a[216:219],v[216:219]
s_mov_b32 s74,5242880
s_mov_b32 s75,0x20000
ds_read_b64_tr_b16 a[178:179],v22 offset:3840
s_waitcnt lgkmcnt(4)
v_mfma_f32_16x16x32_bf16 v[196:199],a[168:171],a[192:195],0
s_waitcnt lgkmcnt(2)
ds_read_b64_tr_b16 a[180:181],v22 offset:18176
ds_read_b64_tr_b16 a[182:183],v22 offset:20224
v_mfma_f32_16x16x32_bf16 v[196:199],a[172:175],a[196:199],v[196:199]
s_lshl_b32 s83,%1,2
buffer_load_dwordx4 v56,s[32:35],s56 offen lds
v_mfma_f32_16x16x32_bf16 v[204:207],a[172:175],a[204:207],0
s_addk_i32 m0,1024
v_lshlrev_b32 v15,3,v6
v_add_u32 v16,128,v15
v_mfma_f32_16x16x32_bf16 v[204:207],a[168:171],a[200:203],v[204:207]
s_add_u32 s83,s83,s40
ds_read_b64_tr_b16 a[184:185],v22 offset:1800
v_lshrrev_b32 v16,10,v16
v_mfma_f32_16x16x32_bf16 v[212:215],a[168:171],a[208:211],0
s_mulk_i32 s83,5120
ds_read_b64_tr_b16 a[186:187],v22 offset:3848
v_lshl_add_u32 v15,v16,24,v15
v_mfma_f32_16x16x32_bf16 v[212:215],a[172:175],a[212:215],v[212:215]
s_lshl_b32 s43,s41,5
buffer_load_dwordx4 v57,s[32:35],s56 offen lds
v_mfma_f32_16x16x32_bf16 v[220:223],a[172:175],a[220:223],0
s_lshl_b32 s44,s42,1
s_add_u32 s43,s43,s44
)A"
R"A(ds_read_b64_tr_b16 a[188:189],v22 offset:18184
v_mfma_f32_16x16x32_bf16 v[220:223],a[168:171],a[216:219],v[220:223]
s_addk_i32 m0,1024
s_lshl_b32 s44,s43,18
ds_read_b64_tr_b16 a[190:191],v22 offset:20232
s_waitcnt lgkmcnt(6)
v_mfma_f32_16x16x32_bf16 v[224:227],a[176:179],a[192:195],0
s_mov_b64 s[20:21],s[6:7]
s_mov_b32 s22,524288
ds_read_b64_tr_b16 a[128:129],v22
s_waitcnt lgkmcnt(5)
v_mfma_f32_16x16x32_bf16 v[224:227],a[180:183],a[196:199],v[224:227]
s_add_u32 s20,s20,s44
s_addc_u32 s21,s21,0
ds_read_b64_tr_b16 a[130:131],v22 offset:2048
v_mfma_f32_16x16x32_bf16 v[232:235],a[180:183],a[204:207],0
s_and_b32 s21,s21,0xffff
buffer_load_dwordx4 v58,s[32:35],s56 offen lds
v_mfma_f32_16x16x32_bf16 v[232:235],a[176:179],a[200:203],v[232:235]
s_mov_b32 s23,0x20000
s_mov_b64 s[24:25],s[8:9]
ds_read_b64_tr_b16 a[132:133],v22 offset:16384
v_mfma_f32_16x16x32_bf16 v[240:243],a[176:179],a[208:211],0
s_addk_i32 m0,1024
s_mov_b32 s26,524288
ds_read_b64_tr_b16 a[134:135],v22 offset:18432
v_mfma_f32_16x16x32_bf16 v[240:243],a[180:183],a[212:215],v[240:243]
s_add_u32 s24,s24,s44
s_addc_u32 s25,s25,0
ds_read_b64_tr_b16 a[136:137],v22 offset:8
v_mfma_f32_16x16x32_bf16 v[248:251],a[180:183],a[220:223],0
s_and_b32 s25,s25,0xffff
s_mov_b32 s27,0x20000
ds_read_b64_tr_b16 a[138:139],v22 offset:2056
v_mfma_f32_16x16x32_bf16 v[248:251],a[176:179],a[216:219],v[248:251]
s_lshl_b32 s44,s41,2
buffer_load_dwordx4 v59,s[32:35],s56 offen lds
s_waitcnt lgkmcnt(6)
v_mfma_f32_16x16x32_bf16 v[228:231],a[184:187],a[192:195],0
s_add_u32 s44,s44,s70
s_lshl_b32 s44,s44,3
ds_read_b64_tr_b16 a[140:141],v22 offset:16392
v_mfma_f32_16x16x32_bf16 v[228:231],a[188:191],a[196:199],v[228:231]
s_addk_i32 m0,1024
s_add_u32 s44,s44,s71
ds_read_b64_tr_b16 a[142:143],v22 offset:18440
v_mfma_f32_16x16x32_bf16 v[236:239],a[188:191],a[204:207],0
s_lshl_b32 s44,s44,18
s_mov_b64 s[36:37],s[12:13]
ds_read_b64_tr_b16 a[144:145],v22 offset:256
v_mfma_f32_16x16x32_bf16 v[236:239],a[184:187],a[200:203],v[236:239]
s_add_u32 s36,s36,s44
s_addc_u32 s37,s37,0
ds_read_b64_tr_b16 a[146:147],v22 offset:2304
v_mfma_f32_16x16x32_bf16 v[244:247],a[184:187],a[208:211],0
s_and_b32 s37,s37,0xffff
buffer_load_dwordx4 v60,s[32:35],s56 offen lds
v_mfma_f32_16x16x32_bf16 v[244:247],a[188:191],a[212:215],v[244:247]
s_xor_b32 s59,s59,131072
s_mov_b32 s38,262144
ds_read_b64_tr_b16 a[148:149],v22 offset:16640
v_mfma_f32_16x16x32_bf16 v[252:255],a[188:191],a[220:223],0
s_addk_i32 m0,1024
s_mov_b32 s39,0x20000
ds_read_b64_tr_b16 a[150:151],v22 offset:18688
v_mfma_f32_16x16x32_bf16 v[252:255],a[184:187],a[216:219],v[252:255]
s_lshl_b32 s51,s40,12
s_addk_i32 s57,128
ds_read_b64_tr_b16 a[152:153],v22 offset:264
s_waitcnt lgkmcnt(7)
v_mfma_f32_16x16x32_bf16 a[0:3],a[128:131],a[192:195],0
s_mul_i32 s47,s41,5
s_add_u32 s47,s47,s42
ds_read_b64_tr_b16 a[154:155],v22 offset:2312
v_mfma_f32_16x16x32_bf16 a[0:3],a[132:135],a[196:199],a[0:3]
s_and_b32 s47,s47,15
buffer_load_dwordx4 v61,s[32:35],s56 offen lds
v_mfma_f32_16x16x32_bf16 a[32:35],a[132:135],a[204:207],0
s_mul_i32 s44,s41,5
s_add_u32 s44,s44,s42
ds_read_b64_tr_b16 a[156:157],v22 offset:16648
v_mfma_f32_16x16x32_bf16 a[32:35],a[128:131],a[200:203],a[32:35]
s_addk_i32 m0,1024
s_lshl_b32 s47,s44,12
ds_read_b64_tr_b16 a[158:159],v22 offset:18696
v_mfma_f32_16x16x32_bf16 a[64:67],a[128:131],a[208:211],0
s_and_b32 s47,s47,0x7000
s_lshl_b32 s44,s44,14
ds_read_b64_tr_b16 a[160:161],v22 offset:512
v_mfma_f32_16x16x32_bf16 a[64:67],a[132:135],a[212:215],a[64:67]
s_and_b32 s44,s44,0x20000
s_or_b32 s47,s47,s44
ds_read_b64_tr_b16 a[162:163],v22 offset:2560
v_mfma_f32_16x16x32_bf16 a[96:99],a[132:135],a[220:223],0
s_mov_b32 s54,0
buffer_load_dwordx4 v62,s[32:35],s56 offen lds
v_mfma_f32_16x16x32_bf16 a[96:99],a[128:131],a[216:219],a[96:99]
s_mov_b32 s61,0x3e0293f0
s_mov_b32 s63,0x53800000
ds_read_b64_tr_b16 a[164:165],v22 offset:16896
v_mfma_f32_16x16x32_bf16 a[4:7],a[136:139],a[192:195],0
s_addk_i32 m0,1024
s_lshl_b32 s44,s40,2
ds_read_b64_tr_b16 a[166:167],v22 offset:18944
s_waitcnt lgkmcnt(6)
v_mfma_f32_16x16x32_bf16 a[4:7],a[140:143],a[196:199],a[4:7]
s_add_u32 s44,s44,s70
s_mov_b32 s46,0xff800000
ds_read_b64_tr_b16 a[168:169],v22 offset:520
v_mfma_f32_16x16x32_bf16 a[36:39],a[140:143],a[204:207],0
s_cmp_eq_u32 s44,14
s_cselect_b32 s18,0x7f61b1e6,s46
ds_read_b64_tr_b16 a[170:171],v22 offset:2568
v_mfma_f32_16x16x32_bf16 a[36:39],a[136:139],a[200:203],a[36:39]
s_add_u32 s60,s60,32768
buffer_load_dwordx4 v63,s[32:35],s56 offen lds
v_mfma_f32_16x16x32_bf16 a[68:71],a[136:139],a[208:211],0
s_cmp_eq_u32 s44,13
s_cselect_b32 s19,0x7f61b1e6,s46
ds_read_b64_tr_b16 a[172:173],v22 offset:16904
v_mfma_f32_16x16x32_bf16 a[68:71],a[140:143],a[212:215],a[68:71]
s_add_u32 m0,s59,0
ds_read_b64_tr_b16 a[174:175],v22 offset:18952
v_lshlrev_b32 v70,15,v65
v_mfma_f32_16x16x32_bf16 a[100:103],a[140:143],a[220:223],0
s_cmp_eq_u32 s44,12
s_cselect_b32 s100,0x7f61b1e6,s46
ds_read_b64_tr_b16 a[176:177],v22 offset:768
v_mfma_f32_16x16x32_bf16 a[100:103],a[136:139],a[216:219],a[100:103]
s_cmp_eq_u32 s44,15
s_cselect_b32 s101,0x7f61b1e6,s46
ds_read_b64_tr_b16 a[178:179],v22 offset:2816
v_mfma_f32_16x16x32_bf16 a[8:11],a[144:147],a[192:195],0
s_add_u32 s58,s58,32768
buffer_load_dwordx4 v48,s[28:31],s57 offen lds
v_mfma_f32_16x16x32_bf16 a[8:11],a[148:151],a[196:199],a[8:11]
s_add_u32 s56,s56,524288
ds_read_b64_tr_b16 a[180:181],v22 offset:17152
v_lshl_add_u32 v70,v66,6,v70
v_mfma_f32_16x16x32_bf16 a[40:43],a[148:151],a[204:207],0
s_mul_i32 s81,s70,1310720
ds_read_b64_tr_b16 a[182:183],v22 offset:19200
v_xor_b32 v71,s40,v65
v_mfma_f32_16x16x32_bf16 a[40:43],a[144:147],a[200:203],a[40:43]
s_mul_i32 s44,s71,320
ds_read_b64_tr_b16 a[184:185],v22 offset:776
v_lshl_add_u32 v70,v71,8,v70
v_mfma_f32_16x16x32_bf16 a[72:75],a[144:147],a[208:211],0
s_add_u32 s81,s81,s44
ds_read_b64_tr_b16 a[186:187],v22 offset:2824
v_lshl_add_u32 v4,v64,4,v70
v_mfma_f32_16x16x32_bf16 a[72:75],a[148:151],a[212:215],a[72:75]
s_mul_i32 s82,s40,10240
buffer_load_dwordx4 v49,s[28:31],s57 offen offset:1024 lds
v_mfma_f32_16x16x32_bf16 a[104:107],a[148:151],a[220:223],0
s_mul_i32 s44,s40,40960
ds_read_b64_tr_b16 a[188:189],v22 offset:17160
v_xor_b32 v71,2,v64
v_mfma_f32_16x16x32_bf16 a[104:107],a[144:147],a[216:219],a[104:107]
s_xor_b32 s59,s59,131072
ds_read_b64_tr_b16 a[190:191],v22 offset:19208
v_lshl_add_u32 v5,v71,4,v70
v_mfma_f32_16x16x32_bf16 a[12:15],a[152:155],a[192:195],0
s_lshr_b32 s92,%1,3
v_lshlrev_b32 v72,10,v65
v_lshl_add_u32 v72,v64,6,v72
s_waitcnt lgkmcnt(6)
v_mfma_f32_16x16x32_bf16 a[12:15],a[156:159],a[196:199],a[12:15]
s_cmp_eq_u32 s60,131072
s_cselect_b32 s60,32768,s60
v_and_b32 v73,2,v65
v_mfma_f32_16x16x32_bf16 a[44:47],a[156:159],a[204:207],0
s_lshl_b32 s92,s92,2
buffer_load_dwordx4 v50,s[28:31],s57 offen offset:2048 lds
v_mfma_f32_16x16x32_bf16 a[44:47],a[152:155],a[200:203],a[44:47]
s_add_u32 s92,s92,s40
v_xor_b32 v73,v73,v66
v_add_u32 v22,s60,v23
v_mfma_f32_16x16x32_bf16 a[76:79],a[152:155],a[208:211],0
s_lshl_b32 s92,s92,7
v_xor_b32 v20,131072,v20
v_xor_b32 v21,131072,v21
v_mfma_f32_16x16x32_bf16 a[76:79],a[156:159],a[212:215],a[76:79]
s_add_u32 s93,s92,L_tailseg_%=-L_pc_%=
v_lshl_add_u32 v72,v73,4,v72
v_add_u32 v1,98304,v72
v_mfma_f32_16x16x32_bf16 a[108:111],a[156:159],a[220:223],0
s_add_u32 s60,s60,32768
v_lshlrev_b32 v72,10,v66
v_lshl_add_u32 v72,v65,6,v72
v_mfma_f32_16x16x32_bf16 a[108:111],a[152:155],a[216:219],a[108:111]
s_waitcnt vmcnt(11) lgkmcnt(0)
buffer_load_dwordx4 v51,s[28:31],s57 offen offset:3072 lds
v_mfma_f32_16x16x32_bf16 a[16:19],a[160:163],a[192:195],0
s_barrier
s_addk_i32 m0,4096
ds_read_b128 a[224:227],v20
v_mfma_f32_16x16x32_bf16 a[16:19],a[164:167],a[196:199],a[16:19]
ds_read_b64_tr_b16 a[128:129],v22 offset:1024
v_lshl_add_u32 v72,v64,3,v72
v_mfma_f32_16x16x32_bf16 a[48:51],a[164:167],a[204:207],0
ds_read_b64_tr_b16 a[130:131],v22 offset:3072
v_add_u32 v72,114688,v72
v_mfma_f32_16x16x32_bf16 a[48:51],a[160:163],a[200:203],a[48:51]
buffer_load_dwordx4 v52,s[28:31],s57 offen lds
v_mfma_f32_16x16x32_bf16 a[80:83],a[160:163],a[208:211],0
ds_read_b128 a[228:231],v21
v_and_b32 v73,1,v66
v_mfma_f32_16x16x32_bf16 a[80:83],a[164:167],a[212:215],a[80:83]
s_cmp_ge_u32 s58,131072
s_cselect_b32 s62,98304,0
ds_read_b64_tr_b16 a[132:133],v22 offset:17408
v_mfma_f32_16x16x32_bf16 a[112:115],a[164:167],a[220:223],0
s_sub_u32 s58,s58,s62
ds_read_b64_tr_b16 a[134:135],v22 offset:19456
v_lshl_add_u32 v2,v73,5,v72
v_mfma_f32_16x16x32_bf16 a[112:115],a[160:163],a[216:219],a[112:115]
ds_read_b128 a[232:235],v20 offset:2048
v_xor_b32 v73,1,v73
v_mfma_f32_16x16x32_bf16 a[20:23],a[168:171],a[192:195],0
buffer_load_dwordx4 v53,s[28:31],s57 offen offset:1024 lds
v_mfma_f32_16x16x32_bf16 a[20:23],a[172:175],a[196:199],a[20:23]
ds_read_b128 a[236:239],v21 offset:2048
v_lshl_add_u32 v3,v73,5,v72
v_mfma_f32_16x16x32_bf16 a[52:55],a[172:175],a[204:207],0
ds_read_b128 a[240:243],v20 offset:4096
v_mov_b32 v73,0x3f803f80
v_mfma_f32_16x16x32_bf16 a[52:55],a[168:171],a[200:203],a[52:55]
ds_read_b128 a[244:247],v21 offset:4096
v_add_u32 v70,0,v12
v_mfma_f32_16x16x32_bf16 a[84:87],a[168:171],a[208:211],0
buffer_load_dword v13,v14,s[64:67],0 offen sc1
v_mfma_f32_16x16x32_bf16 a[84:87],a[172:175],a[212:215],a[84:87]
ds_read_b128 a[248:251],v20 offset:6144
v_mul_u32_u24 v71,3277,v70
v_mfma_f32_16x16x32_bf16 a[116:119],a[172:175],a[220:223],0
ds_read_b128 a[252:255],v21 offset:6144
v_lshrrev_b32 v71,16,v71
v_mfma_f32_16x16x32_bf16 a[116:119],a[168:171],a[216:219],a[116:119]
ds_read_b64_tr_b16 a[136:137],v22 offset:1032
v_mul_u32_u24 v72,20,v71
v_mfma_f32_16x16x32_bf16 a[24:27],a[176:179],a[192:195],0
buffer_load_dwordx4 v54,s[28:31],s57 offen offset:2048 lds
v_mfma_f32_16x16x32_bf16 a[24:27],a[180:183],a[196:199],a[24:27]
ds_read_b64_tr_b16 a[138:139],v22 offset:3080
v_sub_u32 v72,v70,v72
v_mfma_f32_16x16x32_bf16 a[56:59],a[180:183],a[204:207],0
ds_read_b64_tr_b16 a[140:141],v22 offset:17416
v_add_u32 v70,64,v12
v_mfma_f32_16x16x32_bf16 a[56:59],a[176:179],a[200:203],a[56:59]
ds_read_b64_tr_b16 a[142:143],v22 offset:19464
v_lshlrev_b32 v24,10,v6
v_mfma_f32_16x16x32_bf16 a[88:91],a[176:179],a[208:211],0
buffer_load_dwordx4 v55,s[28:31],s57 offen offset:3072 lds
v_mfma_f32_16x16x32_bf16 a[88:91],a[180:183],a[212:215],a[88:91]
s_addk_i32 s57,128
ds_read_b64_tr_b16 a[144:145],v22 offset:1280
v_xor_b32 v20,131072,v20
v_mfma_f32_16x16x32_bf16 a[120:123],a[180:183],a[220:223],0
s_cmp_eq_u32 s60,131072
s_cselect_b32 s60,32768,s60
ds_read_b64_tr_b16 a[146:147],v22 offset:3328
v_mfma_f32_16x16x32_bf16 a[120:123],a[176:179],a[216:219],a[120:123]
ds_read_b64_tr_b16 a[148:149],v22 offset:17664
v_xor_b32 v21,131072,v21
v_mfma_f32_16x16x32_bf16 a[28:31],a[184:187],a[192:195],0
ds_read_b64_tr_b16 a[150:151],v22 offset:19712
v_mfma_f32_16x16x32_bf16 a[28:31],a[188:191],a[196:199],a[28:31]
ds_read_b64_tr_b16 a[152:153],v22 offset:1288
ds_read_b64_tr_b16 a[154:155],v22 offset:3336
ds_read_b64_tr_b16 a[156:157],v22 offset:17672
ds_read_b64_tr_b16 a[158:159],v22 offset:19720
v_mfma_f32_16x16x32_bf16 a[60:63],a[188:191],a[204:207],0
v_mfma_f32_16x16x32_bf16 a[60:63],a[184:187],a[200:203],a[60:63]
v_cmp_gt_u32 vcc,2,v64
s_nop 1
v_cndmask_b32 v28,0,v73,vcc
v_mfma_f32_16x16x32_bf16 a[92:95],a[184:187],a[208:211],0
v_and_b32 v73,1,v64
v_mul_u32_u24 v73,0xaaaa,v73
v_mfma_f32_16x16x32_bf16 a[92:95],a[188:191],a[212:215],a[92:95]
v_add_u32 v73,0x4444,v73
v_lshl_or_b32 v27,v73,16,v73
v_mfma_f32_16x16x32_bf16 a[124:127],a[188:191],a[220:223],0
s_add_u32 m0,s58,0
v_mov_b32 v29,v28
v_mov_b32 v30,v28
v_mfma_f32_16x16x32_bf16 a[124:127],a[184:187],a[216:219],a[124:127]
s_add_u32 s58,s58,32768
ds_read_b64_tr_b16 a[160:161],v22 offset:1536
v_mov_b32 v31,v28
s_waitcnt lgkmcnt(6)
v_mfma_f32_16x16x32_bf16 v[128:131],a[128:131],a[224:227],v[128:131]
ds_read_b64_tr_b16 a[162:163],v22 offset:3584
v_bfe_u32 v73,v71,2,1
v_mfma_f32_16x16x32_bf16 v[128:131],a[132:135],a[228:231],v[128:131]
buffer_load_dwordx4 v56,s[32:35],s56 offen lds
v_mfma_f32_16x16x32_bf16 v[136:139],a[132:135],a[236:239],v[136:139]
ds_read_b64_tr_b16 a[164:165],v22 offset:17920
)A"
R"A(v_lshlrev_b32 v73,1,v73
v_mfma_f32_16x16x32_bf16 v[136:139],a[128:131],a[232:235],v[136:139]
s_addk_i32 m0,1024
ds_read_b64_tr_b16 a[166:167],v22 offset:19968
v_xor_b32 v72,v72,v73
v_mfma_f32_16x16x32_bf16 v[144:147],a[128:131],a[240:243],v[144:147]
v_mul_u32_u24 v71,1280,v71
v_lshl_add_u32 v71,v72,4,v71
v_mfma_f32_16x16x32_bf16 v[144:147],a[132:135],a[244:247],v[144:147]
ds_read_b64_tr_b16 a[168:169],v22 offset:1544
v_add_u32 v71,s44,v71
v_mfma_f32_16x16x32_bf16 v[152:155],a[132:135],a[252:255],v[152:155]
buffer_load_dwordx4 v57,s[32:35],s56 offen lds
v_mfma_f32_16x16x32_bf16 v[152:155],a[128:131],a[248:251],v[152:155]
ds_read_b64_tr_b16 a[170:171],v22 offset:3592
v_subrev_u32 v7,0,v71
v_mfma_f32_16x16x32_bf16 v[132:135],a[136:139],a[224:227],v[132:135]
ds_read_b64_tr_b16 a[172:173],v22 offset:17928
v_mul_u32_u24 v71,3277,v70
v_mfma_f32_16x16x32_bf16 v[132:135],a[140:143],a[228:231],v[132:135]
s_addk_i32 m0,1024
ds_read_b64_tr_b16 a[174:175],v22 offset:19976
v_lshrrev_b32 v71,16,v71
v_mfma_f32_16x16x32_bf16 v[140:143],a[140:143],a[236:239],v[140:143]
ds_read_b64_tr_b16 a[176:177],v22 offset:1792
v_mul_u32_u24 v72,20,v71
v_mfma_f32_16x16x32_bf16 v[140:143],a[136:139],a[232:235],v[140:143]
ds_read_b64_tr_b16 a[178:179],v22 offset:3840
v_sub_u32 v72,v70,v72
v_mfma_f32_16x16x32_bf16 v[148:151],a[136:139],a[240:243],v[148:151]
buffer_load_dwordx4 v58,s[32:35],s56 offen lds
v_mfma_f32_16x16x32_bf16 v[148:151],a[140:143],a[244:247],v[148:151]
ds_read_b64_tr_b16 a[180:181],v22 offset:18176
v_bfe_u32 v73,v71,2,1
v_mfma_f32_16x16x32_bf16 v[156:159],a[140:143],a[252:255],v[156:159]
ds_read_b64_tr_b16 a[182:183],v22 offset:20224
v_lshlrev_b32 v73,1,v73
v_mfma_f32_16x16x32_bf16 v[156:159],a[136:139],a[248:251],v[156:159]
s_addk_i32 m0,1024
v_xor_b32 v72,v72,v73
v_mul_u32_u24 v71,1280,v71
v_mfma_f32_16x16x32_bf16 v[160:163],a[144:147],a[224:227],v[160:163]
s_waitcnt lgkmcnt(5)
v_lshl_add_u32 v71,v72,4,v71
v_add_u32 v71,s44,v71
v_mfma_f32_16x16x32_bf16 v[160:163],a[148:151],a[228:231],v[160:163]
ds_read_b64_tr_b16 a[184:185],v22 offset:1800
v_subrev_u32 v8,1024,v71
v_mfma_f32_16x16x32_bf16 v[168:171],a[148:151],a[236:239],v[168:171]
buffer_load_dwordx4 v59,s[32:35],s56 offen lds
v_mfma_f32_16x16x32_bf16 v[168:171],a[144:147],a[232:235],v[168:171]
ds_read_b64_tr_b16 a[186:187],v22 offset:3848
v_add_u32 v70,128,v12
v_mfma_f32_16x16x32_bf16 v[176:179],a[144:147],a[240:243],v[176:179]
s_addk_i32 m0,1024
ds_read_b64_tr_b16 a[188:189],v22 offset:18184
v_mul_u32_u24 v71,3277,v70
v_mfma_f32_16x16x32_bf16 v[176:179],a[148:151],a[244:247],v[176:179]
ds_read_b64_tr_b16 a[190:191],v22 offset:20232
v_lshrrev_b32 v71,16,v71
v_mfma_f32_16x16x32_bf16 v[184:187],a[148:151],a[252:255],v[184:187]
v_mul_u32_u24 v72,20,v71
v_sub_u32 v72,v70,v72
v_mfma_f32_16x16x32_bf16 v[184:187],a[144:147],a[248:251],v[184:187]
buffer_load_dwordx4 v60,s[32:35],s56 offen lds
v_mfma_f32_16x16x32_bf16 v[164:167],a[152:155],a[224:227],v[164:167]
ds_read_b64_tr_b16 a[128:129],v22
v_bfe_u32 v73,v71,2,1
v_mfma_f32_16x16x32_bf16 v[164:167],a[156:159],a[228:231],v[164:167]
ds_read_b64_tr_b16 a[130:131],v22 offset:2048
v_lshlrev_b32 v73,1,v73
v_mfma_f32_16x16x32_bf16 v[172:175],a[156:159],a[236:239],v[172:175]
s_addk_i32 m0,1024
ds_read_b64_tr_b16 a[132:133],v22 offset:16384
v_xor_b32 v72,v72,v73
v_mfma_f32_16x16x32_bf16 v[172:175],a[152:155],a[232:235],v[172:175]
ds_read_b64_tr_b16 a[134:135],v22 offset:18432
v_mul_u32_u24 v71,1280,v71
v_mfma_f32_16x16x32_bf16 v[180:183],a[152:155],a[240:243],v[180:183]
v_lshl_add_u32 v71,v72,4,v71
v_add_u32 v71,s44,v71
v_mfma_f32_16x16x32_bf16 v[180:183],a[156:159],a[244:247],v[180:183]
buffer_load_dwordx4 v61,s[32:35],s56 offen lds
v_mfma_f32_16x16x32_bf16 v[188:191],a[156:159],a[252:255],v[188:191]
ds_read_b64_tr_b16 a[136:137],v22 offset:8
v_subrev_u32 v9,2048,v71
v_mfma_f32_16x16x32_bf16 v[188:191],a[152:155],a[248:251],v[188:191]
ds_read_b64_tr_b16 a[138:139],v22 offset:2056
v_add_u32 v70,192,v12
v_mfma_f32_16x16x32_bf16 v[192:195],a[160:163],a[224:227],v[192:195]
s_addk_i32 m0,1024
ds_read_b64_tr_b16 a[140:141],v22 offset:16392
v_mul_u32_u24 v71,3277,v70
v_mfma_f32_16x16x32_bf16 v[192:195],a[164:167],a[228:231],v[192:195]
ds_read_b64_tr_b16 a[142:143],v22 offset:18440
v_lshrrev_b32 v71,16,v71
v_mfma_f32_16x16x32_bf16 v[200:203],a[164:167],a[236:239],v[200:203]
v_mul_u32_u24 v72,20,v71
v_sub_u32 v72,v70,v72
v_mfma_f32_16x16x32_bf16 v[200:203],a[160:163],a[232:235],v[200:203]
buffer_load_dwordx4 v62,s[32:35],s56 offen lds
v_mfma_f32_16x16x32_bf16 v[208:211],a[160:163],a[240:243],v[208:211]
v_bfe_u32 v73,v71,2,1
v_lshlrev_b32 v73,1,v73
v_mfma_f32_16x16x32_bf16 v[208:211],a[164:167],a[244:247],v[208:211]
s_addk_i32 m0,1024
ds_read_b64_tr_b16 a[144:145],v22 offset:256
v_xor_b32 v72,v72,v73
v_mfma_f32_16x16x32_bf16 v[216:219],a[164:167],a[252:255],v[216:219]
ds_read_b64_tr_b16 a[146:147],v22 offset:2304
v_mul_u32_u24 v71,1280,v71
v_mfma_f32_16x16x32_bf16 v[216:219],a[160:163],a[248:251],v[216:219]
ds_read_b64_tr_b16 a[148:149],v22 offset:16640
v_lshl_add_u32 v71,v72,4,v71
v_mfma_f32_16x16x32_bf16 v[196:199],a[168:171],a[224:227],v[196:199]
s_waitcnt lgkmcnt(4)
buffer_load_dwordx4 v63,s[32:35],s56 offen lds
v_mfma_f32_16x16x32_bf16 v[196:199],a[172:175],a[228:231],v[196:199]
s_add_u32 s56,s56,524288
ds_read_b64_tr_b16 a[150:151],v22 offset:18688
v_add_u32 v71,s44,v71
v_mfma_f32_16x16x32_bf16 v[204:207],a[172:175],a[236:239],v[204:207]
v_subrev_u32 v10,3072,v71
v_add_u32 v70,256,v12
v_mfma_f32_16x16x32_bf16 v[204:207],a[168:171],a[232:235],v[204:207]
s_add_u32 m0,s59,0
ds_read_b64_tr_b16 a[152:153],v22 offset:264
v_mul_u32_u24 v71,3277,v70
v_mfma_f32_16x16x32_bf16 v[212:215],a[168:171],a[240:243],v[212:215]
s_xor_b32 s59,s59,131072
ds_read_b64_tr_b16 a[154:155],v22 offset:2312
v_lshrrev_b32 v71,16,v71
v_mfma_f32_16x16x32_bf16 v[212:215],a[172:175],a[244:247],v[212:215]
ds_read_b64_tr_b16 a[156:157],v22 offset:16648
v_mul_u32_u24 v72,20,v71
v_mfma_f32_16x16x32_bf16 v[220:223],a[172:175],a[252:255],v[220:223]
buffer_load_dwordx4 v48,s[28:31],s57 offen lds
v_mfma_f32_16x16x32_bf16 v[220:223],a[168:171],a[248:251],v[220:223]
ds_read_b64_tr_b16 a[158:159],v22 offset:18696
v_sub_u32 v72,v70,v72
v_mfma_f32_16x16x32_bf16 v[224:227],a[176:179],a[224:227],v[224:227]
v_bfe_u32 v73,v71,2,1
v_lshlrev_b32 v73,1,v73
v_mfma_f32_16x16x32_bf16 v[224:227],a[180:183],a[228:231],v[224:227]
ds_read_b64_tr_b16 a[160:161],v22 offset:512
v_xor_b32 v72,v72,v73
v_mfma_f32_16x16x32_bf16 v[232:235],a[180:183],a[236:239],v[232:235]
ds_read_b64_tr_b16 a[162:163],v22 offset:2560
v_mul_u32_u24 v71,1280,v71
v_mfma_f32_16x16x32_bf16 v[232:235],a[176:179],a[232:235],v[232:235]
ds_read_b64_tr_b16 a[164:165],v22 offset:16896
v_lshl_add_u32 v71,v72,4,v71
v_mfma_f32_16x16x32_bf16 v[240:243],a[176:179],a[240:243],v[240:243]
buffer_load_dwordx4 v49,s[28:31],s57 offen offset:1024 lds
v_mfma_f32_16x16x32_bf16 v[240:243],a[180:183],a[244:247],v[240:243]
ds_read_b64_tr_b16 a[166:167],v22 offset:18944
v_add_u32 v71,s44,v71
v_mfma_f32_16x16x32_bf16 v[248:251],a[180:183],a[252:255],v[248:251]
ds_read_b64_tr_b16 a[168:169],v22 offset:520
v_subrev_u32 v11,0,v71
v_mfma_f32_16x16x32_bf16 v[248:251],a[176:179],a[248:251],v[248:251]
s_cmp_ge_u32 s58,131072
s_cselect_b32 s62,98304,0
ds_read_b64_tr_b16 a[170:171],v22 offset:2568
v_mfma_f32_16x16x32_bf16 v[228:231],a[184:187],a[224:227],v[228:231]
s_sub_u32 s58,s58,s62
ds_read_b64_tr_b16 a[172:173],v22 offset:16904
v_mfma_f32_16x16x32_bf16 v[228:231],a[188:191],a[228:231],v[228:231]
buffer_load_dwordx4 v50,s[28:31],s57 offen offset:2048 lds
v_mfma_f32_16x16x32_bf16 v[236:239],a[188:191],a[236:239],v[236:239]
ds_read_b64_tr_b16 a[174:175],v22 offset:18952
v_mfma_f32_16x16x32_bf16 v[236:239],a[184:187],a[232:235],v[236:239]
ds_read_b64_tr_b16 a[176:177],v22 offset:768
v_mfma_f32_16x16x32_bf16 v[244:247],a[184:187],a[240:243],v[244:247]
ds_read_b64_tr_b16 a[178:179],v22 offset:2816
v_mfma_f32_16x16x32_bf16 v[244:247],a[188:191],a[244:247],v[244:247]
ds_read_b64_tr_b16 a[180:181],v22 offset:17152
v_mfma_f32_16x16x32_bf16 v[252:255],a[188:191],a[252:255],v[252:255]
ds_read_b64_tr_b16 a[182:183],v22 offset:19200
v_mfma_f32_16x16x32_bf16 v[252:255],a[184:187],a[248:251],v[252:255]
buffer_load_dwordx4 v51,s[28:31],s57 offen offset:3072 lds
v_mfma_f32_16x16x32_bf16 a[0:3],a[128:131],a[224:227],a[0:3]
ds_read_b64_tr_b16 a[184:185],v22 offset:776
v_mfma_f32_16x16x32_bf16 a[0:3],a[132:135],a[228:231],a[0:3]
ds_read_b64_tr_b16 a[186:187],v22 offset:2824
v_mfma_f32_16x16x32_bf16 a[32:35],a[132:135],a[236:239],a[32:35]
s_addk_i32 m0,4096
ds_read_b64_tr_b16 a[188:189],v22 offset:17160
v_mfma_f32_16x16x32_bf16 a[32:35],a[128:131],a[232:235],a[32:35]
ds_read_b64_tr_b16 a[190:191],v22 offset:19208
v_add_u32 v22,s60,v23
v_mfma_f32_16x16x32_bf16 a[64:67],a[128:131],a[240:243],a[64:67]
v_mfma_f32_16x16x32_bf16 a[64:67],a[132:135],a[244:247],a[64:67]
buffer_load_dwordx4 v52,s[28:31],s57 offen lds
v_mfma_f32_16x16x32_bf16 a[96:99],a[132:135],a[252:255],a[96:99]
v_mfma_f32_16x16x32_bf16 a[96:99],a[128:131],a[248:251],a[96:99]
v_mfma_f32_16x16x32_bf16 a[4:7],a[136:139],a[224:227],a[4:7]
s_waitcnt lgkmcnt(3)
v_mfma_f32_16x16x32_bf16 a[4:7],a[140:143],a[228:231],a[4:7]
v_mfma_f32_16x16x32_bf16 a[36:39],a[140:143],a[236:239],a[36:39]
buffer_load_dwordx4 v53,s[28:31],s57 offen offset:1024 lds
v_mfma_f32_16x16x32_bf16 a[36:39],a[136:139],a[232:235],a[36:39]
v_mfma_f32_16x16x32_bf16 a[68:71],a[136:139],a[240:243],a[68:71]
v_mfma_f32_16x16x32_bf16 a[68:71],a[140:143],a[244:247],a[68:71]
v_mfma_f32_16x16x32_bf16 a[100:103],a[140:143],a[252:255],a[100:103]
v_mfma_f32_16x16x32_bf16 a[100:103],a[136:139],a[248:251],a[100:103]
v_mfma_f32_16x16x32_bf16 a[8:11],a[144:147],a[224:227],a[8:11]
buffer_load_dwordx4 v54,s[28:31],s57 offen offset:2048 lds
v_mfma_f32_16x16x32_bf16 a[8:11],a[148:151],a[228:231],a[8:11]
v_mfma_f32_16x16x32_bf16 a[40:43],a[148:151],a[236:239],a[40:43]
v_mfma_f32_16x16x32_bf16 a[40:43],a[144:147],a[232:235],a[40:43]
v_mfma_f32_16x16x32_bf16 a[72:75],a[144:147],a[240:243],a[72:75]
v_mfma_f32_16x16x32_bf16 a[72:75],a[148:151],a[244:247],a[72:75]
v_mfma_f32_16x16x32_bf16 a[104:107],a[148:151],a[252:255],a[104:107]
buffer_load_dwordx4 v55,s[28:31],s57 offen offset:3072 lds
v_mfma_f32_16x16x32_bf16 a[104:107],a[144:147],a[248:251],a[104:107]
s_addk_i32 s57,128
v_mfma_f32_16x16x32_bf16 a[12:15],a[152:155],a[224:227],a[12:15]
v_mfma_f32_16x16x32_bf16 a[12:15],a[156:159],a[228:231],a[12:15]
v_mfma_f32_16x16x32_bf16 a[44:47],a[156:159],a[236:239],a[44:47]
v_mfma_f32_16x16x32_bf16 a[44:47],a[152:155],a[232:235],a[44:47]
v_mfma_f32_16x16x32_bf16 a[76:79],a[152:155],a[240:243],a[76:79]
v_mfma_f32_16x16x32_bf16 a[76:79],a[156:159],a[244:247],a[76:79]
v_mfma_f32_16x16x32_bf16 a[108:111],a[156:159],a[252:255],a[108:111]
v_mfma_f32_16x16x32_bf16 a[108:111],a[152:155],a[248:251],a[108:111]
s_waitcnt vmcnt(16) lgkmcnt(0)
v_mfma_f32_16x16x32_bf16 a[16:19],a[160:163],a[224:227],a[16:19]
s_barrier
ds_read_b128 a[192:195],v20
v_mfma_f32_16x16x32_bf16 a[16:19],a[164:167],a[228:231],a[16:19]
ds_read_b128 a[196:199],v21
v_mfma_f32_16x16x32_bf16 a[48:51],a[164:167],a[236:239],a[48:51]
ds_read_b128 a[200:203],v20 offset:2048
v_mfma_f32_16x16x32_bf16 a[48:51],a[160:163],a[232:235],a[48:51]
ds_read_b128 a[204:207],v21 offset:2048
v_mfma_f32_16x16x32_bf16 a[80:83],a[160:163],a[240:243],a[80:83]
ds_read_b128 a[208:211],v20 offset:4096
v_mfma_f32_16x16x32_bf16 a[80:83],a[164:167],a[244:247],a[80:83]
ds_read_b128 a[212:215],v21 offset:4096
v_mfma_f32_16x16x32_bf16 a[112:115],a[164:167],a[252:255],a[112:115]
ds_read_b128 a[216:219],v20 offset:6144
v_mfma_f32_16x16x32_bf16 a[112:115],a[160:163],a[248:251],a[112:115]
ds_read_b128 a[220:223],v21 offset:6144
v_mfma_f32_16x16x32_bf16 a[20:23],a[168:171],a[224:227],a[20:23]
ds_read_b64_tr_b16 a[128:129],v22 offset:1024
v_mfma_f32_16x16x32_bf16 a[20:23],a[172:175],a[228:231],a[20:23]
ds_read_b64_tr_b16 a[130:131],v22 offset:3072
v_mfma_f32_16x16x32_bf16 a[52:55],a[172:175],a[236:239],a[52:55]
ds_read_b64_tr_b16 a[132:133],v22 offset:17408
)A"
R"A(v_mfma_f32_16x16x32_bf16 a[52:55],a[168:171],a[232:235],a[52:55]
ds_read_b64_tr_b16 a[134:135],v22 offset:19456
v_mfma_f32_16x16x32_bf16 a[84:87],a[168:171],a[240:243],a[84:87]
ds_read_b64_tr_b16 a[136:137],v22 offset:1032
v_mfma_f32_16x16x32_bf16 a[84:87],a[172:175],a[244:247],a[84:87]
ds_read_b64_tr_b16 a[138:139],v22 offset:3080
v_mfma_f32_16x16x32_bf16 a[116:119],a[172:175],a[252:255],a[116:119]
ds_read_b64_tr_b16 a[140:141],v22 offset:17416
v_mfma_f32_16x16x32_bf16 a[116:119],a[168:171],a[248:251],a[116:119]
ds_read_b64_tr_b16 a[142:143],v22 offset:19464
v_mfma_f32_16x16x32_bf16 a[24:27],a[176:179],a[224:227],a[24:27]
ds_read_b64_tr_b16 a[144:145],v22 offset:1280
v_mfma_f32_16x16x32_bf16 a[24:27],a[180:183],a[228:231],a[24:27]
ds_read_b64_tr_b16 a[146:147],v22 offset:3328
v_mfma_f32_16x16x32_bf16 a[56:59],a[180:183],a[236:239],a[56:59]
ds_read_b64_tr_b16 a[148:149],v22 offset:17664
v_mfma_f32_16x16x32_bf16 a[56:59],a[176:179],a[232:235],a[56:59]
ds_read_b64_tr_b16 a[150:151],v22 offset:19712
v_mfma_f32_16x16x32_bf16 a[88:91],a[176:179],a[240:243],a[88:91]
ds_read_b64_tr_b16 a[152:153],v22 offset:1288
v_mfma_f32_16x16x32_bf16 a[88:91],a[180:183],a[244:247],a[88:91]
ds_read_b64_tr_b16 a[154:155],v22 offset:3336
v_mfma_f32_16x16x32_bf16 a[120:123],a[180:183],a[252:255],a[120:123]
ds_read_b64_tr_b16 a[156:157],v22 offset:17672
v_mfma_f32_16x16x32_bf16 a[120:123],a[176:179],a[248:251],a[120:123]
ds_read_b64_tr_b16 a[158:159],v22 offset:19720
v_mfma_f32_16x16x32_bf16 a[28:31],a[184:187],a[224:227],a[28:31]
v_mfma_f32_16x16x32_bf16 a[28:31],a[188:191],a[228:231],a[28:31]
v_mfma_f32_16x16x32_bf16 a[60:63],a[188:191],a[236:239],a[60:63]
v_mfma_f32_16x16x32_bf16 a[60:63],a[184:187],a[232:235],a[60:63]
v_mfma_f32_16x16x32_bf16 a[92:95],a[184:187],a[240:243],a[92:95]
v_mfma_f32_16x16x32_bf16 a[92:95],a[188:191],a[244:247],a[92:95]
v_mfma_f32_16x16x32_bf16 a[124:127],a[188:191],a[252:255],a[124:127]
v_mfma_f32_16x16x32_bf16 a[124:127],a[184:187],a[248:251],a[124:127]
s_mov_b32 s53,3
.p2align 5
L_qp_%=:
s_waitcnt lgkmcnt(1)
v_mfma_f32_16x16x32_bf16 v[128:131],a[128:131],a[192:195],v[128:131]
s_add_u32 m0,s58,0
s_mov_b32 s86,s87
ds_read_b64_tr_b16 a[160:161],v22 offset:1536
v_mfma_f32_16x16x32_bf16 v[128:131],a[132:135],a[196:199],v[128:131]
s_add_u32 s60,s60,32768
buffer_load_dwordx4 v56,s[32:35],s56 offen lds
v_mfma_f32_16x16x32_bf16 v[136:139],a[132:135],a[204:207],v[136:139]
s_cmp_eq_u32 s60,131072
s_cselect_b32 s60,32768,s60
ds_read_b64_tr_b16 a[162:163],v22 offset:3584
v_mfma_f32_16x16x32_bf16 v[136:139],a[128:131],a[200:203],v[136:139]
s_load_dword s86,s[84:85],0x0 glc
s_addk_i32 m0,1024
ds_read_b64_tr_b16 a[164:165],v22 offset:17920
v_mfma_f32_16x16x32_bf16 v[144:147],a[128:131],a[208:211],v[144:147]
s_add_u32 s58,s58,32768
ds_read_b64_tr_b16 a[166:167],v22 offset:19968
v_xor_b32 v20,131072,v20
v_mfma_f32_16x16x32_bf16 v[144:147],a[132:135],a[212:215],v[144:147]
s_mov_b32 s90,L_att0x_%=-L_pc_%=+128
ds_read_b64_tr_b16 a[168:169],v22 offset:1544
v_xor_b32 v21,131072,v21
v_mfma_f32_16x16x32_bf16 v[152:155],a[132:135],a[220:223],v[152:155]
s_sub_u32 s53,s53,1
buffer_load_dwordx4 v57,s[32:35],s56 offen lds
v_mfma_f32_16x16x32_bf16 v[152:155],a[128:131],a[216:219],v[152:155]
s_cmp_ge_u32 s58,131072
s_cselect_b32 s62,98304,0
ds_read_b64_tr_b16 a[170:171],v22 offset:3592
v_mfma_f32_16x16x32_bf16 v[132:135],a[136:139],a[192:195],v[132:135]
s_sub_u32 s58,s58,s62
ds_read_b64_tr_b16 a[172:173],v22 offset:17928
v_mfma_f32_16x16x32_bf16 v[132:135],a[140:143],a[196:199],v[132:135]
s_addk_i32 m0,1024
ds_read_b64_tr_b16 a[174:175],v22 offset:19976
v_mfma_f32_16x16x32_bf16 v[140:143],a[140:143],a[204:207],v[140:143]
ds_read_b64_tr_b16 a[176:177],v22 offset:1792
v_mfma_f32_16x16x32_bf16 v[140:143],a[136:139],a[200:203],v[140:143]
ds_read_b64_tr_b16 a[178:179],v22 offset:3840
v_mfma_f32_16x16x32_bf16 v[148:151],a[136:139],a[208:211],v[148:151]
buffer_load_dwordx4 v58,s[32:35],s56 offen lds
v_mfma_f32_16x16x32_bf16 v[148:151],a[140:143],a[212:215],v[148:151]
ds_read_b64_tr_b16 a[180:181],v22 offset:18176
v_mfma_f32_16x16x32_bf16 v[156:159],a[140:143],a[220:223],v[156:159]
s_cmp_eq_u32 s86,s87
s_addc_u32 s79,s79,0
s_addk_i32 m0,1024
v_mfma_f32_16x16x32_bf16 v[156:159],a[136:139],a[216:219],v[156:159]
ds_read_b64_tr_b16 a[182:183],v22 offset:20224
v_mfma_f32_16x16x32_bf16 v[160:163],a[144:147],a[192:195],v[160:163]
ds_read_b64_tr_b16 a[184:185],v22 offset:1800
v_mfma_f32_16x16x32_bf16 v[160:163],a[148:151],a[196:199],v[160:163]
s_cmp_eq_u32 s86,s87
s_addc_u32 s79,s79,0
ds_read_b64_tr_b16 a[186:187],v22 offset:3848
v_mfma_f32_16x16x32_bf16 v[168:171],a[148:151],a[204:207],v[168:171]
buffer_load_dwordx4 v59,s[32:35],s56 offen lds
v_mfma_f32_16x16x32_bf16 v[168:171],a[144:147],a[200:203],v[168:171]
ds_read_b64_tr_b16 a[188:189],v22 offset:18184
v_mfma_f32_16x16x32_bf16 v[176:179],a[144:147],a[208:211],v[176:179]
s_cmp_eq_u32 s86,s87
s_addc_u32 s79,s79,0
s_addk_i32 m0,1024
v_mfma_f32_16x16x32_bf16 v[176:179],a[148:151],a[212:215],v[176:179]
ds_read_b64_tr_b16 a[190:191],v22 offset:20232
v_mfma_f32_16x16x32_bf16 v[184:187],a[148:151],a[220:223],v[184:187]
ds_read_b64_tr_b16 a[128:129],v22
v_mfma_f32_16x16x32_bf16 v[184:187],a[144:147],a[216:219],v[184:187]
s_cmp_eq_u32 s86,s87
s_addc_u32 s79,s79,0
ds_read_b64_tr_b16 a[130:131],v22 offset:2048
v_mfma_f32_16x16x32_bf16 v[164:167],a[152:155],a[192:195],v[164:167]
s_waitcnt lgkmcnt(5)
buffer_load_dwordx4 v60,s[32:35],s56 offen lds
v_mfma_f32_16x16x32_bf16 v[164:167],a[156:159],a[196:199],v[164:167]
s_addk_i32 m0,1024
ds_read_b64_tr_b16 a[132:133],v22 offset:16384
v_mfma_f32_16x16x32_bf16 v[172:175],a[156:159],a[204:207],v[172:175]
s_cmp_eq_u32 s86,s87
s_addc_u32 s79,s79,0
ds_read_b64_tr_b16 a[134:135],v22 offset:18432
v_mfma_f32_16x16x32_bf16 v[172:175],a[152:155],a[200:203],v[172:175]
ds_read_b64_tr_b16 a[136:137],v22 offset:8
v_mfma_f32_16x16x32_bf16 v[180:183],a[152:155],a[208:211],v[180:183]
buffer_load_dwordx4 v61,s[32:35],s56 offen lds
v_mfma_f32_16x16x32_bf16 v[180:183],a[156:159],a[212:215],v[180:183]
s_cmp_eq_u32 s86,s87
s_addc_u32 s79,s79,0
ds_read_b64_tr_b16 a[138:139],v22 offset:2056
v_mfma_f32_16x16x32_bf16 v[188:191],a[156:159],a[220:223],v[188:191]
ds_read_b64_tr_b16 a[140:141],v22 offset:16392
v_mfma_f32_16x16x32_bf16 v[188:191],a[152:155],a[216:219],v[188:191]
s_addk_i32 m0,1024
ds_read_b64_tr_b16 a[142:143],v22 offset:18440
v_mfma_f32_16x16x32_bf16 v[192:195],a[160:163],a[192:195],v[192:195]
s_cmp_eq_u32 s86,s87
s_addc_u32 s79,s79,0
ds_read_b64_tr_b16 a[144:145],v22 offset:256
v_mfma_f32_16x16x32_bf16 v[192:195],a[164:167],a[196:199],v[192:195]
ds_read_b64_tr_b16 a[146:147],v22 offset:2304
v_mfma_f32_16x16x32_bf16 v[200:203],a[164:167],a[204:207],v[200:203]
buffer_load_dwordx4 v62,s[32:35],s56 offen lds
v_mfma_f32_16x16x32_bf16 v[200:203],a[160:163],a[200:203],v[200:203]
s_cmp_eq_u32 s86,s87
s_addc_u32 s79,s79,0
ds_read_b64_tr_b16 a[148:149],v22 offset:16640
v_mfma_f32_16x16x32_bf16 v[208:211],a[160:163],a[208:211],v[208:211]
s_addk_i32 m0,1024
ds_read_b64_tr_b16 a[150:151],v22 offset:18688
v_mfma_f32_16x16x32_bf16 v[208:211],a[164:167],a[212:215],v[208:211]
ds_read_b64_tr_b16 a[152:153],v22 offset:264
v_mfma_f32_16x16x32_bf16 v[216:219],a[164:167],a[220:223],v[216:219]
s_cmp_eq_u32 s86,s87
s_addc_u32 s79,s79,0
ds_read_b64_tr_b16 a[154:155],v22 offset:2312
v_mfma_f32_16x16x32_bf16 v[216:219],a[160:163],a[216:219],v[216:219]
buffer_load_dwordx4 v63,s[32:35],s56 offen lds
v_mfma_f32_16x16x32_bf16 v[196:199],a[168:171],a[192:195],v[196:199]
s_add_u32 s56,s56,524288
ds_read_b64_tr_b16 a[156:157],v22 offset:16648
v_mfma_f32_16x16x32_bf16 v[196:199],a[172:175],a[196:199],v[196:199]
s_cmp_eq_u32 s86,s87
s_addc_u32 s79,s79,0
ds_read_b64_tr_b16 a[158:159],v22 offset:18696
v_mfma_f32_16x16x32_bf16 v[204:207],a[172:175],a[204:207],v[204:207]
s_add_u32 m0,s59,0
s_xor_b32 s59,s59,131072
ds_read_b64_tr_b16 a[160:161],v22 offset:512
v_mfma_f32_16x16x32_bf16 v[204:207],a[168:171],a[200:203],v[204:207]
ds_read_b64_tr_b16 a[162:163],v22 offset:2560
v_mfma_f32_16x16x32_bf16 v[212:215],a[168:171],a[208:211],v[212:215]
s_cmp_eq_u32 s86,s87
s_addc_u32 s79,s79,0
ds_read_b64_tr_b16 a[164:165],v22 offset:16896
v_mfma_f32_16x16x32_bf16 v[212:215],a[172:175],a[212:215],v[212:215]
buffer_load_dwordx4 v48,s[28:31],s57 offen lds
v_mfma_f32_16x16x32_bf16 v[220:223],a[172:175],a[220:223],v[220:223]
ds_read_b64_tr_b16 a[166:167],v22 offset:18944
v_mfma_f32_16x16x32_bf16 v[220:223],a[168:171],a[216:219],v[220:223]
s_cmp_eq_u32 s86,s87
s_addc_u32 s79,s79,0
ds_read_b64_tr_b16 a[168:169],v22 offset:520
v_mfma_f32_16x16x32_bf16 v[224:227],a[176:179],a[192:195],v[224:227]
ds_read_b64_tr_b16 a[170:171],v22 offset:2568
v_mfma_f32_16x16x32_bf16 v[224:227],a[180:183],a[196:199],v[224:227]
ds_read_b64_tr_b16 a[172:173],v22 offset:16904
v_mfma_f32_16x16x32_bf16 v[232:235],a[180:183],a[204:207],v[232:235]
s_cmp_eq_u32 s86,s87
s_addc_u32 s79,s79,0
ds_read_b64_tr_b16 a[174:175],v22 offset:18952
v_mfma_f32_16x16x32_bf16 v[232:235],a[176:179],a[200:203],v[232:235]
buffer_load_dwordx4 v49,s[28:31],s57 offen offset:1024 lds
v_mfma_f32_16x16x32_bf16 v[240:243],a[176:179],a[208:211],v[240:243]
v_mfma_f32_16x16x32_bf16 v[240:243],a[180:183],a[212:215],v[240:243]
s_cmp_eq_u32 s86,s87
s_addc_u32 s79,s79,0
v_mfma_f32_16x16x32_bf16 v[248:251],a[180:183],a[220:223],v[248:251]
ds_read_b64_tr_b16 a[180:181],v22 offset:17152
v_mfma_f32_16x16x32_bf16 v[248:251],a[176:179],a[216:219],v[248:251]
s_waitcnt lgkmcnt(4)
ds_read_b64_tr_b16 a[176:177],v22 offset:768
v_mfma_f32_16x16x32_bf16 v[228:231],a[184:187],a[192:195],v[228:231]
s_cmp_eq_u32 s86,s87
s_addc_u32 s79,s79,0
ds_read_b64_tr_b16 a[178:179],v22 offset:2816
v_mfma_f32_16x16x32_bf16 v[228:231],a[188:191],a[196:199],v[228:231]
buffer_load_dwordx4 v50,s[28:31],s57 offen offset:2048 lds
v_mfma_f32_16x16x32_bf16 v[236:239],a[188:191],a[204:207],v[236:239]
ds_read_b64_tr_b16 a[182:183],v22 offset:19200
v_mfma_f32_16x16x32_bf16 v[236:239],a[184:187],a[200:203],v[236:239]
s_cmp_eq_u32 s86,s87
s_addc_u32 s79,s79,0
v_mfma_f32_16x16x32_bf16 v[244:247],a[184:187],a[208:211],v[244:247]
v_mfma_f32_16x16x32_bf16 v[244:247],a[188:191],a[212:215],v[244:247]
buffer_load_dwordx4 v51,s[28:31],s57 offen offset:3072 lds
v_mfma_f32_16x16x32_bf16 v[252:255],a[188:191],a[220:223],v[252:255]
ds_read_b64_tr_b16 a[188:189],v22 offset:17160
v_mfma_f32_16x16x32_bf16 v[252:255],a[184:187],a[216:219],v[252:255]
ds_read_b64_tr_b16 a[184:185],v22 offset:776
v_mfma_f32_16x16x32_bf16 a[0:3],a[128:131],a[192:195],a[0:3]
s_addk_i32 m0,4096
ds_read_b64_tr_b16 a[186:187],v22 offset:2824
v_mfma_f32_16x16x32_bf16 a[0:3],a[132:135],a[196:199],a[0:3]
ds_read_b64_tr_b16 a[190:191],v22 offset:19208
v_add_u32 v22,s60,v23
v_mfma_f32_16x16x32_bf16 a[32:35],a[132:135],a[204:207],a[32:35]
s_add_u32 s60,s60,32768
s_cmp_eq_u32 s60,131072
s_cselect_b32 s60,32768,s60
v_mfma_f32_16x16x32_bf16 a[32:35],a[128:131],a[200:203],a[32:35]
buffer_load_dwordx4 v52,s[28:31],s57 offen lds
v_mfma_f32_16x16x32_bf16 a[64:67],a[128:131],a[208:211],a[64:67]
v_mfma_f32_16x16x32_bf16 a[64:67],a[132:135],a[212:215],a[64:67]
v_mfma_f32_16x16x32_bf16 a[96:99],a[132:135],a[220:223],a[96:99]
v_mfma_f32_16x16x32_bf16 a[96:99],a[128:131],a[216:219],a[96:99]
v_mfma_f32_16x16x32_bf16 a[4:7],a[136:139],a[192:195],a[4:7]
buffer_load_dwordx4 v53,s[28:31],s57 offen offset:1024 lds
v_mfma_f32_16x16x32_bf16 a[4:7],a[140:143],a[196:199],a[4:7]
v_mfma_f32_16x16x32_bf16 a[36:39],a[140:143],a[204:207],a[36:39]
v_mfma_f32_16x16x32_bf16 a[36:39],a[136:139],a[200:203],a[36:39]
v_mfma_f32_16x16x32_bf16 a[68:71],a[136:139],a[208:211],a[68:71]
v_mfma_f32_16x16x32_bf16 a[68:71],a[140:143],a[212:215],a[68:71]
v_mfma_f32_16x16x32_bf16 a[100:103],a[140:143],a[220:223],a[100:103]
buffer_load_dwordx4 v54,s[28:31],s57 offen offset:2048 lds
v_mfma_f32_16x16x32_bf16 a[100:103],a[136:139],a[216:219],a[100:103]
v_mfma_f32_16x16x32_bf16 a[8:11],a[144:147],a[192:195],a[8:11]
v_mfma_f32_16x16x32_bf16 a[8:11],a[148:151],a[196:199],a[8:11]
v_mfma_f32_16x16x32_bf16 a[40:43],a[148:151],a[204:207],a[40:43]
)A"
R"A(v_mfma_f32_16x16x32_bf16 a[40:43],a[144:147],a[200:203],a[40:43]
buffer_load_dwordx4 v55,s[28:31],s57 offen offset:3072 lds
v_mfma_f32_16x16x32_bf16 a[72:75],a[144:147],a[208:211],a[72:75]
s_addk_i32 s57,128
v_mfma_f32_16x16x32_bf16 a[72:75],a[148:151],a[212:215],a[72:75]
v_mfma_f32_16x16x32_bf16 a[104:107],a[148:151],a[220:223],a[104:107]
v_mfma_f32_16x16x32_bf16 a[104:107],a[144:147],a[216:219],a[104:107]
v_mfma_f32_16x16x32_bf16 a[12:15],a[152:155],a[192:195],a[12:15]
v_mfma_f32_16x16x32_bf16 a[12:15],a[156:159],a[196:199],a[12:15]
v_mfma_f32_16x16x32_bf16 a[44:47],a[156:159],a[204:207],a[44:47]
buffer_load_dword v16,v15,s[72:75],s83 offen
v_mfma_f32_16x16x32_bf16 a[44:47],a[152:155],a[200:203],a[44:47]
s_addk_i32 s83,896
v_mfma_f32_16x16x32_bf16 a[76:79],a[152:155],a[208:211],a[76:79]
v_mfma_f32_16x16x32_bf16 a[76:79],a[156:159],a[212:215],a[76:79]
v_mfma_f32_16x16x32_bf16 a[108:111],a[156:159],a[220:223],a[108:111]
v_mfma_f32_16x16x32_bf16 a[108:111],a[152:155],a[216:219],a[108:111]
s_waitcnt vmcnt(17) lgkmcnt(0)
v_mfma_f32_16x16x32_bf16 a[16:19],a[160:163],a[192:195],a[16:19]
s_barrier
ds_read_b128 a[224:227],v20
v_mfma_f32_16x16x32_bf16 a[16:19],a[164:167],a[196:199],a[16:19]
ds_read_b64_tr_b16 a[128:129],v22 offset:1024
v_mfma_f32_16x16x32_bf16 a[48:51],a[164:167],a[204:207],a[48:51]
ds_read_b64_tr_b16 a[130:131],v22 offset:3072
v_mfma_f32_16x16x32_bf16 a[48:51],a[160:163],a[200:203],a[48:51]
ds_read_b128 a[228:231],v21
v_mfma_f32_16x16x32_bf16 a[80:83],a[160:163],a[208:211],a[80:83]
ds_read_b64_tr_b16 a[132:133],v22 offset:17408
v_mfma_f32_16x16x32_bf16 a[80:83],a[164:167],a[212:215],a[80:83]
ds_read_b64_tr_b16 a[134:135],v22 offset:19456
v_mfma_f32_16x16x32_bf16 a[112:115],a[164:167],a[220:223],a[112:115]
ds_read_b128 a[232:235],v20 offset:2048
v_mfma_f32_16x16x32_bf16 a[112:115],a[160:163],a[216:219],a[112:115]
ds_read_b128 a[236:239],v21 offset:2048
v_mfma_f32_16x16x32_bf16 a[20:23],a[168:171],a[192:195],a[20:23]
ds_read_b128 a[240:243],v20 offset:4096
v_mfma_f32_16x16x32_bf16 a[20:23],a[172:175],a[196:199],a[20:23]
ds_read_b128 a[244:247],v21 offset:4096
v_mfma_f32_16x16x32_bf16 a[52:55],a[172:175],a[204:207],a[52:55]
ds_read_b128 a[248:251],v20 offset:6144
v_xor_b32 v20,131072,v20
v_mfma_f32_16x16x32_bf16 a[52:55],a[168:171],a[200:203],a[52:55]
ds_read_b128 a[252:255],v21 offset:6144
v_xor_b32 v21,131072,v21
v_mfma_f32_16x16x32_bf16 a[84:87],a[168:171],a[208:211],a[84:87]
ds_read_b64_tr_b16 a[136:137],v22 offset:1032
v_mfma_f32_16x16x32_bf16 a[84:87],a[172:175],a[212:215],a[84:87]
ds_read_b64_tr_b16 a[138:139],v22 offset:3080
v_mfma_f32_16x16x32_bf16 a[116:119],a[172:175],a[220:223],a[116:119]
ds_read_b64_tr_b16 a[140:141],v22 offset:17416
v_mfma_f32_16x16x32_bf16 a[116:119],a[168:171],a[216:219],a[116:119]
ds_read_b64_tr_b16 a[142:143],v22 offset:19464
v_mfma_f32_16x16x32_bf16 a[24:27],a[176:179],a[192:195],a[24:27]
ds_read_b64_tr_b16 a[144:145],v22 offset:1280
v_mfma_f32_16x16x32_bf16 a[24:27],a[180:183],a[196:199],a[24:27]
ds_read_b64_tr_b16 a[146:147],v22 offset:3328
v_mfma_f32_16x16x32_bf16 a[56:59],a[180:183],a[204:207],a[56:59]
ds_read_b64_tr_b16 a[148:149],v22 offset:17664
v_mfma_f32_16x16x32_bf16 a[56:59],a[176:179],a[200:203],a[56:59]
ds_read_b64_tr_b16 a[150:151],v22 offset:19712
v_mfma_f32_16x16x32_bf16 a[88:91],a[176:179],a[208:211],a[88:91]
ds_read_b64_tr_b16 a[152:153],v22 offset:1288
v_mfma_f32_16x16x32_bf16 a[88:91],a[180:183],a[212:215],a[88:91]
ds_read_b64_tr_b16 a[154:155],v22 offset:3336
v_mfma_f32_16x16x32_bf16 a[120:123],a[180:183],a[220:223],a[120:123]
ds_read_b64_tr_b16 a[156:157],v22 offset:17672
v_mfma_f32_16x16x32_bf16 a[120:123],a[176:179],a[216:219],a[120:123]
ds_read_b64_tr_b16 a[158:159],v22 offset:19720
v_mfma_f32_16x16x32_bf16 a[28:31],a[184:187],a[192:195],a[28:31]
ds_read_b64_tr_b16 a[160:161],v22 offset:1536
v_mfma_f32_16x16x32_bf16 a[28:31],a[188:191],a[196:199],a[28:31]
ds_read_b64_tr_b16 a[162:163],v22 offset:3584
v_mfma_f32_16x16x32_bf16 a[60:63],a[188:191],a[204:207],a[60:63]
ds_read_b64_tr_b16 a[164:165],v22 offset:17920
v_mfma_f32_16x16x32_bf16 a[60:63],a[184:187],a[200:203],a[60:63]
ds_read_b64_tr_b16 a[166:167],v22 offset:19968
v_mfma_f32_16x16x32_bf16 a[92:95],a[184:187],a[208:211],a[92:95]
ds_read_b64_tr_b16 a[168:169],v22 offset:1544
v_mfma_f32_16x16x32_bf16 a[92:95],a[188:191],a[212:215],a[92:95]
ds_read_b64_tr_b16 a[170:171],v22 offset:3592
v_mfma_f32_16x16x32_bf16 a[124:127],a[188:191],a[220:223],a[124:127]
s_add_u32 m0,s58,0
s_add_u32 s58,s58,32768
ds_read_b64_tr_b16 a[172:173],v22 offset:17928
v_mfma_f32_16x16x32_bf16 a[124:127],a[184:187],a[216:219],a[124:127]
s_cmp_ge_u32 s58,131072
s_cselect_b32 s62,98304,0
ds_read_b64_tr_b16 a[174:175],v22 offset:19976
s_waitcnt lgkmcnt(8)
v_mfma_f32_16x16x32_bf16 v[128:131],a[128:131],a[224:227],v[128:131]
s_mov_b32 s86,s87
s_sub_u32 s58,s58,s62
ds_read_b64_tr_b16 a[176:177],v22 offset:1792
v_mfma_f32_16x16x32_bf16 v[128:131],a[132:135],a[228:231],v[128:131]
buffer_load_dwordx4 v56,s[32:35],s56 offen lds
v_mfma_f32_16x16x32_bf16 v[136:139],a[132:135],a[236:239],v[136:139]
ds_read_b64_tr_b16 a[178:179],v22 offset:3840
v_mfma_f32_16x16x32_bf16 v[136:139],a[128:131],a[232:235],v[136:139]
s_load_dword s86,s[84:85],0x2000 glc
s_addk_i32 m0,1024
ds_read_b64_tr_b16 a[180:181],v22 offset:18176
v_mfma_f32_16x16x32_bf16 v[144:147],a[128:131],a[240:243],v[144:147]
s_addk_i32 s84,256
ds_read_b64_tr_b16 a[182:183],v22 offset:20224
v_mfma_f32_16x16x32_bf16 v[144:147],a[132:135],a[244:247],v[144:147]
ds_read_b64_tr_b16 a[184:185],v22 offset:1800
v_mfma_f32_16x16x32_bf16 v[152:155],a[132:135],a[252:255],v[152:155]
buffer_load_dwordx4 v57,s[32:35],s56 offen lds
v_mfma_f32_16x16x32_bf16 v[152:155],a[128:131],a[248:251],v[152:155]
ds_read_b64_tr_b16 a[186:187],v22 offset:3848
v_mfma_f32_16x16x32_bf16 v[132:135],a[136:139],a[224:227],v[132:135]
ds_read_b64_tr_b16 a[188:189],v22 offset:18184
v_mfma_f32_16x16x32_bf16 v[132:135],a[140:143],a[228:231],v[132:135]
s_addk_i32 m0,1024
ds_read_b64_tr_b16 a[190:191],v22 offset:20232
v_mfma_f32_16x16x32_bf16 v[140:143],a[140:143],a[236:239],v[140:143]
ds_read_b64_tr_b16 a[128:129],v22
v_mfma_f32_16x16x32_bf16 v[140:143],a[136:139],a[232:235],v[140:143]
ds_read_b64_tr_b16 a[130:131],v22 offset:2048
v_mfma_f32_16x16x32_bf16 v[148:151],a[136:139],a[240:243],v[148:151]
buffer_load_dwordx4 v58,s[32:35],s56 offen lds
v_mfma_f32_16x16x32_bf16 v[148:151],a[140:143],a[244:247],v[148:151]
ds_read_b64_tr_b16 a[132:133],v22 offset:16384
v_mfma_f32_16x16x32_bf16 v[156:159],a[140:143],a[252:255],v[156:159]
s_cmp_eq_u32 s86,s87
s_subb_u32 s79,s79,0
s_addk_i32 m0,1024
v_mfma_f32_16x16x32_bf16 v[156:159],a[136:139],a[248:251],v[156:159]
ds_read_b64_tr_b16 a[134:135],v22 offset:18432
v_mfma_f32_16x16x32_bf16 v[160:163],a[144:147],a[224:227],v[160:163]
ds_read_b64_tr_b16 a[136:137],v22 offset:8
v_mfma_f32_16x16x32_bf16 v[160:163],a[148:151],a[228:231],v[160:163]
s_cmp_eq_u32 s86,s87
s_subb_u32 s79,s79,0
ds_read_b64_tr_b16 a[138:139],v22 offset:2056
v_mfma_f32_16x16x32_bf16 v[168:171],a[148:151],a[236:239],v[168:171]
buffer_load_dwordx4 v59,s[32:35],s56 offen lds
v_mfma_f32_16x16x32_bf16 v[168:171],a[144:147],a[232:235],v[168:171]
ds_read_b64_tr_b16 a[140:141],v22 offset:16392
v_mfma_f32_16x16x32_bf16 v[176:179],a[144:147],a[240:243],v[176:179]
s_cmp_eq_u32 s86,s87
s_subb_u32 s79,s79,0
s_addk_i32 m0,1024
v_mfma_f32_16x16x32_bf16 v[176:179],a[148:151],a[244:247],v[176:179]
ds_read_b64_tr_b16 a[142:143],v22 offset:18440
v_mfma_f32_16x16x32_bf16 v[184:187],a[148:151],a[252:255],v[184:187]
ds_read_b64_tr_b16 a[148:149],v22 offset:16640
v_mfma_f32_16x16x32_bf16 v[184:187],a[144:147],a[248:251],v[184:187]
s_cmp_eq_u32 s86,s87
s_subb_u32 s79,s79,0
ds_read_b64_tr_b16 a[144:145],v22 offset:256
v_mfma_f32_16x16x32_bf16 v[164:167],a[152:155],a[224:227],v[164:167]
buffer_load_dwordx4 v60,s[32:35],s56 offen lds
v_mfma_f32_16x16x32_bf16 v[164:167],a[156:159],a[228:231],v[164:167]
s_addk_i32 m0,1024
ds_read_b64_tr_b16 a[146:147],v22 offset:2304
v_mfma_f32_16x16x32_bf16 v[172:175],a[156:159],a[236:239],v[172:175]
s_cmp_eq_u32 s86,s87
s_subb_u32 s79,s79,0
ds_read_b64_tr_b16 a[150:151],v22 offset:18688
v_mfma_f32_16x16x32_bf16 v[172:175],a[152:155],a[232:235],v[172:175]
v_mfma_f32_16x16x32_bf16 v[180:183],a[152:155],a[240:243],v[180:183]
buffer_load_dwordx4 v61,s[32:35],s56 offen lds
v_mfma_f32_16x16x32_bf16 v[180:183],a[156:159],a[244:247],v[180:183]
s_cmp_eq_u32 s86,s87
s_subb_u32 s79,s79,0
v_mfma_f32_16x16x32_bf16 v[188:191],a[156:159],a[252:255],v[188:191]
ds_read_b64_tr_b16 a[156:157],v22 offset:16648
v_mfma_f32_16x16x32_bf16 v[188:191],a[152:155],a[248:251],v[188:191]
s_addk_i32 m0,1024
ds_read_b64_tr_b16 a[152:153],v22 offset:264
s_waitcnt lgkmcnt(4)
v_mfma_f32_16x16x32_bf16 v[192:195],a[160:163],a[224:227],v[192:195]
s_cmp_eq_u32 s86,s87
s_subb_u32 s79,s79,0
ds_read_b64_tr_b16 a[154:155],v22 offset:2312
v_mfma_f32_16x16x32_bf16 v[192:195],a[164:167],a[228:231],v[192:195]
ds_read_b64_tr_b16 a[158:159],v22 offset:18696
v_mfma_f32_16x16x32_bf16 v[200:203],a[164:167],a[236:239],v[200:203]
buffer_load_dwordx4 v62,s[32:35],s56 offen lds
v_mfma_f32_16x16x32_bf16 v[200:203],a[160:163],a[232:235],v[200:203]
s_cmp_eq_u32 s86,s87
s_subb_u32 s79,s79,0
v_mfma_f32_16x16x32_bf16 v[208:211],a[160:163],a[240:243],v[208:211]
s_addk_i32 m0,1024
v_mfma_f32_16x16x32_bf16 v[208:211],a[164:167],a[244:247],v[208:211]
v_mfma_f32_16x16x32_bf16 v[216:219],a[164:167],a[252:255],v[216:219]
s_cmp_eq_u32 s86,s87
s_subb_u32 s79,s79,0
ds_read_b64_tr_b16 a[164:165],v22 offset:16896
v_mfma_f32_16x16x32_bf16 v[216:219],a[160:163],a[248:251],v[216:219]
buffer_load_dwordx4 v63,s[32:35],s56 offen lds
v_mfma_f32_16x16x32_bf16 v[196:199],a[168:171],a[224:227],v[196:199]
s_add_u32 s56,s56,524288
ds_read_b64_tr_b16 a[160:161],v22 offset:512
v_mfma_f32_16x16x32_bf16 v[196:199],a[172:175],a[228:231],v[196:199]
s_cmp_eq_u32 s86,s87
s_subb_u32 s79,s79,0
ds_read_b64_tr_b16 a[162:163],v22 offset:2560
v_mfma_f32_16x16x32_bf16 v[204:207],a[172:175],a[236:239],v[204:207]
s_add_u32 m0,s59,0
s_xor_b32 s59,s59,131072
ds_read_b64_tr_b16 a[166:167],v22 offset:18944
v_mfma_f32_16x16x32_bf16 v[204:207],a[168:171],a[232:235],v[204:207]
v_mfma_f32_16x16x32_bf16 v[212:215],a[168:171],a[240:243],v[212:215]
s_cmp_eq_u32 s86,s87
s_subb_u32 s79,s79,0
v_mfma_f32_16x16x32_bf16 v[212:215],a[172:175],a[244:247],v[212:215]
buffer_load_dwordx4 v48,s[28:31],s57 offen lds
v_mfma_f32_16x16x32_bf16 v[220:223],a[172:175],a[252:255],v[220:223]
ds_read_b64_tr_b16 a[172:173],v22 offset:16904
v_mfma_f32_16x16x32_bf16 v[220:223],a[168:171],a[248:251],v[220:223]
s_cmp_eq_u32 s86,s87
s_subb_u32 s79,s79,0
ds_read_b64_tr_b16 a[168:169],v22 offset:520
v_mfma_f32_16x16x32_bf16 v[224:227],a[176:179],a[224:227],v[224:227]
ds_read_b64_tr_b16 a[170:171],v22 offset:2568
v_mfma_f32_16x16x32_bf16 v[224:227],a[180:183],a[228:231],v[224:227]
ds_read_b64_tr_b16 a[174:175],v22 offset:18952
v_mfma_f32_16x16x32_bf16 v[232:235],a[180:183],a[236:239],v[232:235]
s_cmp_eq_u32 s86,s87
s_subb_u32 s79,s79,0
v_mfma_f32_16x16x32_bf16 v[232:235],a[176:179],a[232:235],v[232:235]
buffer_load_dwordx4 v49,s[28:31],s57 offen offset:1024 lds
v_mfma_f32_16x16x32_bf16 v[240:243],a[176:179],a[240:243],v[240:243]
v_mfma_f32_16x16x32_bf16 v[240:243],a[180:183],a[244:247],v[240:243]
s_cmp_eq_u32 s86,s87
s_subb_u32 s79,s79,0
v_mfma_f32_16x16x32_bf16 v[248:251],a[180:183],a[252:255],v[248:251]
ds_read_b64_tr_b16 a[180:181],v22 offset:17152
v_mfma_f32_16x16x32_bf16 v[248:251],a[176:179],a[248:251],v[248:251]
ds_read_b64_tr_b16 a[176:177],v22 offset:768
v_mfma_f32_16x16x32_bf16 v[228:231],a[184:187],a[224:227],v[228:231]
s_cmp_eq_u32 s86,s87
s_subb_u32 s79,s79,0
ds_read_b64_tr_b16 a[178:179],v22 offset:2816
v_mfma_f32_16x16x32_bf16 v[228:231],a[188:191],a[228:231],v[228:231]
buffer_load_dwordx4 v50,s[28:31],s57 offen offset:2048 lds
v_mfma_f32_16x16x32_bf16 v[236:239],a[188:191],a[236:239],v[236:239]
ds_read_b64_tr_b16 a[182:183],v22 offset:19200
v_mfma_f32_16x16x32_bf16 v[236:239],a[184:187],a[232:235],v[236:239]
)A"
R"A(s_cmp_eq_u32 s86,s87
s_subb_u32 s79,s79,0
v_mfma_f32_16x16x32_bf16 v[244:247],a[184:187],a[240:243],v[244:247]
v_mfma_f32_16x16x32_bf16 v[244:247],a[188:191],a[244:247],v[244:247]
buffer_load_dwordx4 v51,s[28:31],s57 offen offset:3072 lds
v_mfma_f32_16x16x32_bf16 v[252:255],a[188:191],a[252:255],v[252:255]
ds_read_b64_tr_b16 a[188:189],v22 offset:17160
v_mfma_f32_16x16x32_bf16 v[252:255],a[184:187],a[248:251],v[252:255]
ds_read_b64_tr_b16 a[184:185],v22 offset:776
v_mfma_f32_16x16x32_bf16 a[0:3],a[128:131],a[224:227],a[0:3]
s_addk_i32 m0,4096
ds_read_b64_tr_b16 a[186:187],v22 offset:2824
v_mfma_f32_16x16x32_bf16 a[0:3],a[132:135],a[228:231],a[0:3]
ds_read_b64_tr_b16 a[190:191],v22 offset:19208
v_add_u32 v22,s60,v23
v_mfma_f32_16x16x32_bf16 a[32:35],a[132:135],a[236:239],a[32:35]
v_mfma_f32_16x16x32_bf16 a[32:35],a[128:131],a[232:235],a[32:35]
buffer_load_dwordx4 v52,s[28:31],s57 offen lds
v_mfma_f32_16x16x32_bf16 a[64:67],a[128:131],a[240:243],a[64:67]
v_mfma_f32_16x16x32_bf16 a[64:67],a[132:135],a[244:247],a[64:67]
v_mfma_f32_16x16x32_bf16 a[96:99],a[132:135],a[252:255],a[96:99]
v_mfma_f32_16x16x32_bf16 a[96:99],a[128:131],a[248:251],a[96:99]
v_mfma_f32_16x16x32_bf16 a[4:7],a[136:139],a[224:227],a[4:7]
buffer_load_dwordx4 v53,s[28:31],s57 offen offset:1024 lds
v_mfma_f32_16x16x32_bf16 a[4:7],a[140:143],a[228:231],a[4:7]
v_mfma_f32_16x16x32_bf16 a[36:39],a[140:143],a[236:239],a[36:39]
v_mfma_f32_16x16x32_bf16 a[36:39],a[136:139],a[232:235],a[36:39]
v_mfma_f32_16x16x32_bf16 a[68:71],a[136:139],a[240:243],a[68:71]
v_mfma_f32_16x16x32_bf16 a[68:71],a[140:143],a[244:247],a[68:71]
v_mfma_f32_16x16x32_bf16 a[100:103],a[140:143],a[252:255],a[100:103]
buffer_load_dwordx4 v54,s[28:31],s57 offen offset:2048 lds
v_mfma_f32_16x16x32_bf16 a[100:103],a[136:139],a[248:251],a[100:103]
s_waitcnt lgkmcnt(0)
v_mfma_f32_16x16x32_bf16 a[8:11],a[144:147],a[224:227],a[8:11]
v_mfma_f32_16x16x32_bf16 a[8:11],a[148:151],a[228:231],a[8:11]
v_mfma_f32_16x16x32_bf16 a[40:43],a[148:151],a[236:239],a[40:43]
v_mfma_f32_16x16x32_bf16 a[40:43],a[144:147],a[232:235],a[40:43]
buffer_load_dwordx4 v55,s[28:31],s57 offen offset:3072 lds
v_mfma_f32_16x16x32_bf16 a[72:75],a[144:147],a[240:243],a[72:75]
s_addk_i32 s57,128
v_mfma_f32_16x16x32_bf16 a[72:75],a[148:151],a[244:247],a[72:75]
v_mfma_f32_16x16x32_bf16 a[104:107],a[148:151],a[252:255],a[104:107]
v_mfma_f32_16x16x32_bf16 a[104:107],a[144:147],a[248:251],a[104:107]
v_mfma_f32_16x16x32_bf16 a[12:15],a[152:155],a[224:227],a[12:15]
v_mfma_f32_16x16x32_bf16 a[12:15],a[156:159],a[228:231],a[12:15]
v_mfma_f32_16x16x32_bf16 a[44:47],a[156:159],a[236:239],a[44:47]
buffer_load_dword v16,v15,s[72:75],s83 offen
v_mfma_f32_16x16x32_bf16 a[44:47],a[152:155],a[232:235],a[44:47]
s_addk_i32 s83,896
v_mfma_f32_16x16x32_bf16 a[76:79],a[152:155],a[240:243],a[76:79]
v_mfma_f32_16x16x32_bf16 a[76:79],a[156:159],a[244:247],a[76:79]
v_mfma_f32_16x16x32_bf16 a[108:111],a[156:159],a[252:255],a[108:111]
v_mfma_f32_16x16x32_bf16 a[108:111],a[152:155],a[248:251],a[108:111]
s_waitcnt vmcnt(17) lgkmcnt(0)
v_mfma_f32_16x16x32_bf16 a[16:19],a[160:163],a[224:227],a[16:19]
s_barrier
ds_read_b128 a[192:195],v20
v_mfma_f32_16x16x32_bf16 a[16:19],a[164:167],a[228:231],a[16:19]
ds_read_b128 a[196:199],v21
v_mfma_f32_16x16x32_bf16 a[48:51],a[164:167],a[236:239],a[48:51]
ds_read_b128 a[200:203],v20 offset:2048
v_mfma_f32_16x16x32_bf16 a[48:51],a[160:163],a[232:235],a[48:51]
ds_read_b128 a[204:207],v21 offset:2048
v_mfma_f32_16x16x32_bf16 a[80:83],a[160:163],a[240:243],a[80:83]
ds_read_b128 a[208:211],v20 offset:4096
v_mfma_f32_16x16x32_bf16 a[80:83],a[164:167],a[244:247],a[80:83]
ds_read_b128 a[212:215],v21 offset:4096
v_mfma_f32_16x16x32_bf16 a[112:115],a[164:167],a[252:255],a[112:115]
buffer_load_dword v26,v24,s[88:91],s93 offen
v_mfma_f32_16x16x32_bf16 a[112:115],a[160:163],a[248:251],a[112:115]
ds_read_b128 a[216:219],v20 offset:6144
v_mfma_f32_16x16x32_bf16 a[20:23],a[168:171],a[224:227],a[20:23]
ds_read_b128 a[220:223],v21 offset:6144
v_mfma_f32_16x16x32_bf16 a[20:23],a[172:175],a[228:231],a[20:23]
ds_read_b64_tr_b16 a[128:129],v22 offset:1024
v_mfma_f32_16x16x32_bf16 a[52:55],a[172:175],a[236:239],a[52:55]
ds_read_b64_tr_b16 a[130:131],v22 offset:3072
v_mfma_f32_16x16x32_bf16 a[52:55],a[168:171],a[232:235],a[52:55]
ds_read_b64_tr_b16 a[132:133],v22 offset:17408
v_mfma_f32_16x16x32_bf16 a[84:87],a[168:171],a[240:243],a[84:87]
ds_read_b64_tr_b16 a[134:135],v22 offset:19456
v_mfma_f32_16x16x32_bf16 a[84:87],a[172:175],a[244:247],a[84:87]
ds_read_b64_tr_b16 a[136:137],v22 offset:1032
v_mfma_f32_16x16x32_bf16 a[116:119],a[172:175],a[252:255],a[116:119]
ds_read_b64_tr_b16 a[138:139],v22 offset:3080
v_mfma_f32_16x16x32_bf16 a[116:119],a[168:171],a[248:251],a[116:119]
ds_read_b64_tr_b16 a[140:141],v22 offset:17416
v_mfma_f32_16x16x32_bf16 a[24:27],a[176:179],a[224:227],a[24:27]
ds_read_b64_tr_b16 a[142:143],v22 offset:19464
v_mfma_f32_16x16x32_bf16 a[24:27],a[180:183],a[228:231],a[24:27]
ds_read_b64_tr_b16 a[144:145],v22 offset:1280
v_mfma_f32_16x16x32_bf16 a[56:59],a[180:183],a[236:239],a[56:59]
ds_read_b64_tr_b16 a[146:147],v22 offset:3328
v_mfma_f32_16x16x32_bf16 a[56:59],a[176:179],a[232:235],a[56:59]
ds_read_b64_tr_b16 a[148:149],v22 offset:17664
v_mfma_f32_16x16x32_bf16 a[88:91],a[176:179],a[240:243],a[88:91]
ds_read_b64_tr_b16 a[150:151],v22 offset:19712
v_mfma_f32_16x16x32_bf16 a[88:91],a[180:183],a[244:247],a[88:91]
ds_read_b64_tr_b16 a[152:153],v22 offset:1288
v_mfma_f32_16x16x32_bf16 a[120:123],a[180:183],a[252:255],a[120:123]
ds_read_b64_tr_b16 a[154:155],v22 offset:3336
v_mfma_f32_16x16x32_bf16 a[120:123],a[176:179],a[248:251],a[120:123]
ds_read_b64_tr_b16 a[156:157],v22 offset:17672
v_mfma_f32_16x16x32_bf16 a[28:31],a[184:187],a[224:227],a[28:31]
ds_read_b64_tr_b16 a[158:159],v22 offset:19720
v_mfma_f32_16x16x32_bf16 a[28:31],a[188:191],a[228:231],a[28:31]
v_mfma_f32_16x16x32_bf16 a[60:63],a[188:191],a[236:239],a[60:63]
v_mfma_f32_16x16x32_bf16 a[60:63],a[184:187],a[232:235],a[60:63]
v_mfma_f32_16x16x32_bf16 a[92:95],a[184:187],a[240:243],a[92:95]
v_mfma_f32_16x16x32_bf16 a[92:95],a[188:191],a[244:247],a[92:95]
v_mfma_f32_16x16x32_bf16 a[124:127],a[188:191],a[252:255],a[124:127]
s_cmp_lg_u32 s53,0
v_mfma_f32_16x16x32_bf16 a[124:127],a[184:187],a[248:251],a[124:127]
s_cbranch_scc1 L_qp_%=
L_qpx_%=:
L_tailseg_%=:
s_waitcnt lgkmcnt(1)
v_mfma_f32_16x16x32_bf16 v[128:131],a[128:131],a[192:195],v[128:131]
s_add_u32 m0,98304,s51
ds_read_b64_tr_b16 a[160:161],v22 offset:1536
v_xor_b32 v20,131072,v20
v_mfma_f32_16x16x32_bf16 v[128:131],a[132:135],a[196:199],v[128:131]
s_addk_i32 s54,16384
buffer_load_dwordx4 v4,s[20:23],s47 offen nt lds
v_mfma_f32_16x16x32_bf16 v[136:139],a[132:135],a[204:207],v[136:139]
s_and_b32 s46,s54,0xfffc0000
ds_read_b64_tr_b16 a[162:163],v22 offset:3584
v_xor_b32 v21,131072,v21
v_mfma_f32_16x16x32_bf16 v[136:139],a[128:131],a[200:203],v[136:139]
s_addk_i32 s54,16384
ds_read_b64_tr_b16 a[164:165],v22 offset:17920
v_and_b32 v18,48,v6
v_mfma_f32_16x16x32_bf16 v[144:147],a[128:131],a[208:211],v[144:147]
s_mov_b32 s48,-98304
ds_read_b64_tr_b16 a[166:167],v22 offset:19968
v_lshrrev_b32 v19,4,v6
v_mfma_f32_16x16x32_bf16 v[144:147],a[132:135],a[212:215],v[144:147]
s_movk_i32 s60,141
ds_read_b64_tr_b16 a[168:169],v22 offset:1544
v_lshlrev_b32 v18,2,v18
v_mfma_f32_16x16x32_bf16 v[152:155],a[132:135],a[220:223],v[152:155]
s_mul_i32 s83,s41,5
ds_read_b64_tr_b16 a[170:171],v22 offset:3592
v_and_b32 v19,48,v19
v_mfma_f32_16x16x32_bf16 v[152:155],a[128:131],a[216:219],v[152:155]
s_add_u32 s83,s83,s42
ds_read_b64_tr_b16 a[172:173],v22 offset:17928
v_add_u32 v18,v18,v19
v_mfma_f32_16x16x32_bf16 v[132:135],a[136:139],a[192:195],v[132:135]
s_add_u32 s83,s83,15
ds_read_b64_tr_b16 a[174:175],v22 offset:19976
v_add_u32 v18,98304,v18
v_mfma_f32_16x16x32_bf16 v[132:135],a[140:143],a[196:199],v[132:135]
s_cmp_gt_i32 s79,0
s_cselect_b32 s86,8192,0
ds_read_b64_tr_b16 a[176:177],v22 offset:1792
v_mfma_f32_16x16x32_bf16 v[140:143],a[140:143],a[204:207],v[140:143]
s_lshr_b32 s79,%1,3
v_readfirstlane_b32 s68,v13
s_nop 1
s_add_u32 s68,s68,1
v_mfma_f32_16x16x32_bf16 v[140:143],a[136:139],a[200:203],v[140:143]
s_mul_i32 s79,s79,4
s_lshr_b32 s79,s79,5
ds_read_b64_tr_b16 a[178:179],v22 offset:3840
v_mfma_f32_16x16x32_bf16 v[148:151],a[136:139],a[208:211],v[148:151]
s_sub_u32 s60,s60,s79
s_lshl_b32 s94,s60,2
ds_read_b64_tr_b16 a[180:181],v22 offset:18176
v_mfma_f32_16x16x32_bf16 v[148:151],a[140:143],a[212:215],v[148:151]
s_sub_u32 s94,s94,3
buffer_load_dwordx4 v4,s[20:23],s47 offen offset:1024 nt lds
v_mfma_f32_16x16x32_bf16 v[156:159],a[140:143],a[220:223],v[156:159]
s_lshl_b32 s43,s83,14
s_lshl_b32 s83,s83,12
ds_read_b64_tr_b16 a[182:183],v22 offset:20224
v_mfma_f32_16x16x32_bf16 v[156:159],a[136:139],a[216:219],v[156:159]
s_lshl_b32 s44,s40,4
s_and_b32 s43,s43,0x20000
ds_read_b64_tr_b16 a[184:185],v22 offset:1800
v_mfma_f32_16x16x32_bf16 v[160:163],a[144:147],a[192:195],v[160:163]
s_and_b32 s83,s83,0x7000
s_xor_b32 s69,s68,0x9e3779b9
ds_read_b64_tr_b16 a[186:187],v22 offset:3848
v_mfma_f32_16x16x32_bf16 v[160:163],a[148:151],a[196:199],v[160:163]
s_lshl_b32 s80,s40,10
s_and_b32 s44,s44,32
ds_read_b64_tr_b16 a[188:189],v22 offset:18184
v_mfma_f32_16x16x32_bf16 v[168:171],a[148:151],a[204:207],v[168:171]
s_or_b32 s83,s83,s43
s_and_b32 s69,s69,-2
ds_read_b64_tr_b16 a[190:191],v22 offset:20232
v_mfma_f32_16x16x32_bf16 v[168:171],a[144:147],a[200:203],v[168:171]
s_add_u32 s55,s55,s86
s_add_u32 s93,s92,L_att0x_%=-L_pc_%=
ds_read_b64_tr_b16 a[128:129],v22
v_mfma_f32_16x16x32_bf16 v[176:179],a[144:147],a[208:211],v[176:179]
s_mov_b32 s50,32768
s_mov_b32 s49,98304
ds_read_b64_tr_b16 a[130:131],v22 offset:2048
v_mfma_f32_16x16x32_bf16 v[176:179],a[148:151],a[212:215],v[176:179]
s_mov_b32 s45,0x1249
ds_read_b64_tr_b16 a[132:133],v22 offset:16384
v_xor_b32 v25,s44,v4
v_mfma_f32_16x16x32_bf16 v[184:187],a[148:151],a[220:223],v[184:187]
s_add_u32 s80,s80,131072
s_or_b32 s83,s83,0x40000
ds_read_b64_tr_b16 a[134:135],v22 offset:18432
v_mfma_f32_16x16x32_bf16 v[184:187],a[144:147],a[216:219],v[184:187]
ds_read_b64_tr_b16 a[144:145],v22 offset:8
v_mfma_f32_16x16x32_bf16 v[164:167],a[152:155],a[192:195],v[164:167]
s_waitcnt lgkmcnt(7)
ds_read_b64_tr_b16 a[146:147],v22 offset:2056
v_mfma_f32_16x16x32_bf16 v[164:167],a[156:159],a[196:199],v[164:167]
buffer_load_dwordx4 v5,s[20:23],s47 offen offset:2048 nt lds
v_mfma_f32_16x16x32_bf16 v[172:175],a[156:159],a[204:207],v[172:175]
s_waitcnt vmcnt(5)
ds_read_b128 a[224:227],v20
v_mfma_f32_16x16x32_bf16 v[172:175],a[152:155],a[200:203],v[172:175]
ds_read_b64_tr_b16 a[148:149],v22 offset:16392
v_mfma_f32_16x16x32_bf16 v[180:183],a[152:155],a[208:211],v[180:183]
ds_read_b64_tr_b16 a[150:151],v22 offset:18440
v_mfma_f32_16x16x32_bf16 v[180:183],a[156:159],a[212:215],v[180:183]
v_mfma_f32_16x16x32_bf16 v[188:191],a[156:159],a[220:223],v[188:191]
ds_read_b128 a[228:231],v21
v_mfma_f32_16x16x32_bf16 v[188:191],a[152:155],a[216:219],v[188:191]
v_mfma_f32_16x16x32_bf16 v[192:195],a[160:163],a[192:195],v[192:195]
ds_read_b128 a[232:235],v20 offset:2048
v_mfma_f32_16x16x32_bf16 v[192:195],a[164:167],a[196:199],v[192:195]
v_mfma_f32_16x16x32_bf16 v[200:203],a[164:167],a[204:207],v[200:203]
ds_read_b128 a[236:239],v21 offset:2048
v_mfma_f32_16x16x32_bf16 v[200:203],a[160:163],a[200:203],v[200:203]
v_mfma_f32_16x16x32_bf16 v[208:211],a[160:163],a[208:211],v[208:211]
ds_read_b128 a[240:243],v20 offset:4096
v_mfma_f32_16x16x32_bf16 v[208:211],a[164:167],a[212:215],v[208:211]
buffer_load_dwordx4 v5,s[20:23],s47 offen offset:3072 nt lds
v_mfma_f32_16x16x32_bf16 v[216:219],a[164:167],a[220:223],v[216:219]
ds_read_b128 a[244:247],v21 offset:4096
v_mfma_f32_16x16x32_bf16 v[216:219],a[160:163],a[216:219],v[216:219]
ds_read_b64_tr_b16 a[160:161],v22 offset:256
v_mfma_f32_16x16x32_bf16 v[196:199],a[168:171],a[192:195],v[196:199]
ds_read_b128 a[248:251],v20 offset:6144
ds_read_b128 a[252:255],v21 offset:6144
v_mfma_f32_16x16x32_bf16 v[196:199],a[172:175],a[196:199],v[196:199]
v_mfma_f32_16x16x32_bf16 v[204:207],a[172:175],a[204:207],v[204:207]
)A"
R"A(v_mfma_f32_16x16x32_bf16 v[204:207],a[168:171],a[200:203],v[204:207]
v_mfma_f32_16x16x32_bf16 v[212:215],a[168:171],a[208:211],v[212:215]
ds_read_b64_tr_b16 a[162:163],v22 offset:2304
v_mfma_f32_16x16x32_bf16 v[212:215],a[172:175],a[212:215],v[212:215]
ds_read_b64_tr_b16 a[164:165],v22 offset:16640
v_mfma_f32_16x16x32_bf16 v[220:223],a[172:175],a[220:223],v[220:223]
s_waitcnt vmcnt(5) lgkmcnt(2)
s_addk_i32 m0,16384
ds_read_b64_tr_b16 a[166:167],v22 offset:18688
v_mfma_f32_16x16x32_bf16 v[220:223],a[168:171],a[216:219],v[220:223]
s_barrier
ds_read_b64_tr_b16 a[136:137],v22 offset:32768
v_mfma_f32_16x16x32_bf16 v[224:227],a[176:179],a[192:195],v[224:227]
buffer_load_dwordx4 v4,s[24:27],s47 offen nt lds
v_mfma_f32_16x16x32_bf16 v[224:227],a[180:183],a[196:199],v[224:227]
ds_read_b64_tr_b16 a[138:139],v22 offset:34816
v_mfma_f32_16x16x32_bf16 v[232:235],a[180:183],a[204:207],v[232:235]
ds_read_b64_tr_b16 a[140:141],v22 offset:49152
v_mfma_f32_16x16x32_bf16 v[232:235],a[176:179],a[200:203],v[232:235]
ds_read_b64_tr_b16 a[142:143],v22 offset:51200
v_mfma_f32_16x16x32_bf16 v[240:243],a[176:179],a[208:211],v[240:243]
ds_read_b64_tr_b16 a[152:153],v22 offset:32776
v_mfma_f32_16x16x32_bf16 v[240:243],a[180:183],a[212:215],v[240:243]
ds_read_b64_tr_b16 a[154:155],v22 offset:34824
v_mfma_f32_16x16x32_bf16 v[248:251],a[180:183],a[220:223],v[248:251]
ds_read_b64_tr_b16 a[156:157],v22 offset:49160
v_mfma_f32_16x16x32_bf16 v[248:251],a[176:179],a[216:219],v[248:251]
ds_read_b64_tr_b16 a[158:159],v22 offset:51208
v_mfma_f32_16x16x32_bf16 v[228:231],a[184:187],a[192:195],v[228:231]
ds_read_b64_tr_b16 a[168:169],v22 offset:33024
v_mfma_f32_16x16x32_bf16 v[228:231],a[188:191],a[196:199],v[228:231]
ds_read_b64_tr_b16 a[170:171],v22 offset:35072
v_mfma_f32_16x16x32_bf16 v[236:239],a[188:191],a[204:207],v[236:239]
ds_read_b64_tr_b16 a[172:173],v22 offset:49408
v_mfma_f32_16x16x32_bf16 v[236:239],a[184:187],a[200:203],v[236:239]
ds_read_b64_tr_b16 a[174:175],v22 offset:51456
v_mfma_f32_16x16x32_bf16 v[244:247],a[184:187],a[208:211],v[244:247]
buffer_load_dwordx4 v5,s[24:27],s47 offen offset:1024 nt lds
v_mfma_f32_16x16x32_bf16 v[244:247],a[188:191],a[212:215],v[244:247]
ds_read_b64_tr_b16 a[176:177],v22 offset:264
v_mfma_f32_16x16x32_bf16 v[252:255],a[188:191],a[220:223],v[252:255]
ds_read_b64_tr_b16 a[178:179],v22 offset:2312
v_mfma_f32_16x16x32_bf16 v[252:255],a[184:187],a[216:219],v[252:255]
ds_read_b64_tr_b16 a[180:181],v22 offset:16648
v_mfma_f32_16x16x32_bf16 a[0:3],a[128:131],a[192:195],a[0:3]
ds_read_b64_tr_b16 a[182:183],v22 offset:18696
v_mfma_f32_16x16x32_bf16 a[0:3],a[132:135],a[196:199],a[0:3]
s_waitcnt lgkmcnt(6)
ds_read_b64_tr_b16 a[184:185],v22 offset:33032
v_mfma_f32_16x16x32_bf16 a[0:3],a[136:139],a[224:227],a[0:3]
ds_read_b64_tr_b16 a[186:187],v22 offset:35080
v_mfma_f32_16x16x32_bf16 a[0:3],a[140:143],a[228:231],a[0:3]
ds_read_b64_tr_b16 a[188:189],v22 offset:49416
v_mfma_f32_16x16x32_bf16 a[32:35],a[140:143],a[236:239],a[32:35]
ds_read_b64_tr_b16 a[190:191],v22 offset:51464
v_mfma_f32_16x16x32_bf16 a[32:35],a[136:139],a[232:235],a[32:35]
v_mfma_f32_16x16x32_bf16 a[32:35],a[132:135],a[204:207],a[32:35]
v_mfma_f32_16x16x32_bf16 a[32:35],a[128:131],a[200:203],a[32:35]
v_mfma_f32_16x16x32_bf16 a[64:67],a[128:131],a[208:211],a[64:67]
buffer_load_dwordx4 v4,s[24:27],s47 offen offset:2048 nt lds
v_mfma_f32_16x16x32_bf16 a[64:67],a[132:135],a[212:215],a[64:67]
v_accvgpr_read_b32 v64,a0
v_accvgpr_read_b32 v65,a1
v_mfma_f32_16x16x32_bf16 a[64:67],a[136:139],a[240:243],a[64:67]
v_mul_f32 v64,s61,v64
v_mul_f32 v65,s61,v65
v_mfma_f32_16x16x32_bf16 a[64:67],a[140:143],a[244:247],a[64:67]
v_accvgpr_read_b32 v66,a2
v_accvgpr_read_b32 v67,a3
v_mfma_f32_16x16x32_bf16 a[96:99],a[140:143],a[252:255],a[96:99]
ds_read_b64_tr_b16 a[140:141],v22 offset:49664
v_cvt_pk_bf16_f32 v64,v64,v65
v_mfma_f32_16x16x32_bf16 a[96:99],a[136:139],a[248:251],a[96:99]
ds_read_b64_tr_b16 a[136:137],v22 offset:33280
v_accvgpr_read_b32 v80,a32
v_mfma_f32_16x16x32_bf16 a[96:99],a[132:135],a[220:223],a[96:99]
ds_read_b64_tr_b16 a[132:133],v22 offset:16896
v_accvgpr_read_b32 v81,a33
v_mfma_f32_16x16x32_bf16 a[96:99],a[128:131],a[216:219],a[96:99]
ds_read_b64_tr_b16 a[128:129],v22 offset:512
v_mul_f32 v80,s61,v80
v_mfma_f32_16x16x32_bf16 a[4:7],a[144:147],a[192:195],a[4:7]
ds_read_b64_tr_b16 a[130:131],v22 offset:2560
v_mul_f32 v81,s61,v81
v_mfma_f32_16x16x32_bf16 a[4:7],a[148:151],a[196:199],a[4:7]
ds_read_b64_tr_b16 a[134:135],v22 offset:18944
v_accvgpr_read_b32 v82,a34
v_mfma_f32_16x16x32_bf16 a[4:7],a[152:155],a[224:227],a[4:7]
ds_read_b64_tr_b16 a[138:139],v22 offset:35328
v_accvgpr_read_b32 v83,a35
v_mfma_f32_16x16x32_bf16 a[4:7],a[156:159],a[228:231],a[4:7]
ds_read_b64_tr_b16 a[142:143],v22 offset:51712
v_mul_f32 v66,s61,v66
v_mfma_f32_16x16x32_bf16 a[36:39],a[156:159],a[236:239],a[36:39]
buffer_load_dwordx4 v5,s[24:27],s47 offen offset:3072 nt lds
v_mfma_f32_16x16x32_bf16 a[36:39],a[152:155],a[232:235],a[36:39]
s_or_b32 s47,s47,0x18000
s_addk_i32 s47,4096
s_and_b32 s47,s47,0x27000
v_mfma_f32_16x16x32_bf16 a[36:39],a[148:151],a[204:207],a[36:39]
s_or_b32 s47,s47,s46
s_and_b32 s46,s54,0xfffc0000
v_mul_f32 v67,s61,v67
v_mfma_f32_16x16x32_bf16 a[36:39],a[144:147],a[200:203],a[36:39]
s_addk_i32 s54,16384
v_cvt_pk_bf16_f32 v80,v80,v81
v_mul_f32 v82,s61,v82
v_mfma_f32_16x16x32_bf16 a[68:71],a[144:147],a[208:211],a[68:71]
v_mul_f32 v83,s61,v83
v_cvt_pk_bf16_f32 v65,v66,v67
v_mfma_f32_16x16x32_bf16 a[68:71],a[148:151],a[212:215],a[68:71]
v_cvt_pk_bf16_f32 v81,v82,v83
v_accvgpr_read_b32 v112,a96
v_mfma_f32_16x16x32_bf16 a[68:71],a[152:155],a[240:243],a[68:71]
v_accvgpr_read_b32 v113,a97
v_accvgpr_read_b32 v66,a4
v_mfma_f32_16x16x32_bf16 a[68:71],a[156:159],a[244:247],a[68:71]
v_accvgpr_read_b32 v67,a5
v_mul_f32 v112,s61,v112
v_mfma_f32_16x16x32_bf16 a[100:103],a[156:159],a[252:255],a[100:103]
ds_read_b64_tr_b16 a[156:157],v22 offset:49672
v_mul_f32 v113,s61,v113
v_mfma_f32_16x16x32_bf16 a[100:103],a[152:155],a[248:251],a[100:103]
ds_read_b64_tr_b16 a[152:153],v22 offset:33288
v_accvgpr_read_b32 v82,a36
v_mfma_f32_16x16x32_bf16 a[100:103],a[148:151],a[220:223],a[100:103]
ds_read_b64_tr_b16 a[148:149],v22 offset:16904
v_accvgpr_read_b32 v83,a37
v_mfma_f32_16x16x32_bf16 a[100:103],a[144:147],a[216:219],a[100:103]
ds_read_b64_tr_b16 a[144:145],v22 offset:520
v_accvgpr_read_b32 v114,a98
v_mfma_f32_16x16x32_bf16 a[8:11],a[160:163],a[192:195],a[8:11]
s_add_u32 m0,0,s51
ds_read_b64_tr_b16 a[146:147],v22 offset:2568
v_accvgpr_read_b32 v115,a99
v_mfma_f32_16x16x32_bf16 a[8:11],a[164:167],a[196:199],a[8:11]
ds_read_b64_tr_b16 a[150:151],v22 offset:18952
v_accvgpr_read_b32 v12,a6
v_mfma_f32_16x16x32_bf16 a[8:11],a[168:171],a[224:227],a[8:11]
s_waitcnt lgkmcnt(6)
ds_read_b64_tr_b16 a[154:155],v22 offset:35336
v_accvgpr_read_b32 v13,a7
v_mfma_f32_16x16x32_bf16 a[8:11],a[172:175],a[228:231],a[8:11]
buffer_load_dwordx4 v4,s[20:23],s47 offen nt lds
v_mfma_f32_16x16x32_bf16 a[40:43],a[172:175],a[236:239],a[40:43]
ds_read_b64_tr_b16 a[158:159],v22 offset:51720
v_mul_f32 v66,s61,v66
v_mfma_f32_16x16x32_bf16 a[40:43],a[168:171],a[232:235],a[40:43]
v_mul_f32 v67,s61,v67
v_accvgpr_read_b32 v14,a38
v_mfma_f32_16x16x32_bf16 a[40:43],a[164:167],a[204:207],a[40:43]
v_accvgpr_read_b32 v15,a39
v_mul_f32 v82,s61,v82
v_mfma_f32_16x16x32_bf16 a[40:43],a[160:163],a[200:203],a[40:43]
v_mul_f32 v83,s61,v83
v_cvt_pk_bf16_f32 v112,v112,v113
v_mfma_f32_16x16x32_bf16 a[72:75],a[160:163],a[208:211],a[72:75]
v_mul_f32 v114,s61,v114
v_mul_f32 v115,s61,v115
v_mfma_f32_16x16x32_bf16 a[72:75],a[164:167],a[212:215],a[72:75]
v_accvgpr_read_b32 v68,a8
v_accvgpr_read_b32 v69,a9
v_mfma_f32_16x16x32_bf16 a[72:75],a[168:171],a[240:243],a[72:75]
v_mul_f32 v68,s61,v68
v_mul_f32 v69,s61,v69
v_mfma_f32_16x16x32_bf16 a[72:75],a[172:175],a[244:247],a[72:75]
v_accvgpr_read_b32 v70,a10
v_accvgpr_read_b32 v71,a11
v_mfma_f32_16x16x32_bf16 a[104:107],a[172:175],a[252:255],a[104:107]
ds_read_b64_tr_b16 a[172:173],v22 offset:49920
v_cvt_pk_bf16_f32 v68,v68,v69
v_mfma_f32_16x16x32_bf16 a[104:107],a[168:171],a[248:251],a[104:107]
ds_read_b64_tr_b16 a[168:169],v22 offset:33536
v_mul_f32 v70,s61,v70
v_mfma_f32_16x16x32_bf16 a[104:107],a[164:167],a[220:223],a[104:107]
ds_read_b64_tr_b16 a[164:165],v22 offset:17152
v_mul_f32 v71,s61,v71
v_mfma_f32_16x16x32_bf16 a[104:107],a[160:163],a[216:219],a[104:107]
ds_read_b64_tr_b16 a[160:161],v22 offset:768
v_mul_f32 v12,s61,v12
v_mfma_f32_16x16x32_bf16 a[12:15],a[176:179],a[192:195],a[12:15]
ds_read_b64_tr_b16 a[162:163],v22 offset:2816
v_mul_f32 v13,s61,v13
v_mfma_f32_16x16x32_bf16 a[12:15],a[180:183],a[196:199],a[12:15]
buffer_load_dwordx4 v4,s[20:23],s47 offen offset:1024 nt lds
v_mfma_f32_16x16x32_bf16 a[12:15],a[184:187],a[224:227],a[12:15]
ds_read_b64_tr_b16 a[166:167],v22 offset:19200
v_cvt_pk_bf16_f32 v66,v66,v67
v_mfma_f32_16x16x32_bf16 a[12:15],a[188:191],a[228:231],a[12:15]
ds_read_b64_tr_b16 a[170:171],v22 offset:35584
v_mul_f32 v14,s61,v14
v_mfma_f32_16x16x32_bf16 a[44:47],a[188:191],a[236:239],a[44:47]
ds_read_b64_tr_b16 a[174:175],v22 offset:51968
v_mul_f32 v15,s61,v15
v_mfma_f32_16x16x32_bf16 a[44:47],a[184:187],a[232:235],a[44:47]
v_cvt_pk_bf16_f32 v82,v82,v83
v_cvt_pk_bf16_f32 v113,v114,v115
v_mfma_f32_16x16x32_bf16 a[44:47],a[180:183],a[204:207],a[44:47]
v_cvt_pk_bf16_f32 v69,v70,v71
v_accvgpr_read_b32 v100,a72
v_mfma_f32_16x16x32_bf16 a[44:47],a[176:179],a[200:203],a[44:47]
v_accvgpr_read_b32 v101,a73
v_accvgpr_read_b32 v116,a104
v_mfma_f32_16x16x32_bf16 a[76:79],a[176:179],a[208:211],a[76:79]
v_accvgpr_read_b32 v117,a105
v_cvt_pk_bf16_f32 v67,v12,v13
v_mfma_f32_16x16x32_bf16 a[76:79],a[180:183],a[212:215],a[76:79]
v_cvt_pk_bf16_f32 v83,v14,v15
v_accvgpr_read_b32 v114,a100
v_mfma_f32_16x16x32_bf16 a[76:79],a[184:187],a[240:243],a[76:79]
v_accvgpr_read_b32 v115,a101
v_accvgpr_read_b32 v70,a12
v_mfma_f32_16x16x32_bf16 a[76:79],a[188:191],a[244:247],a[76:79]
v_accvgpr_read_b32 v71,a13
v_mul_f32 v100,s61,v100
v_mfma_f32_16x16x32_bf16 a[108:111],a[188:191],a[252:255],a[108:111]
ds_read_b64_tr_b16 a[188:189],v22 offset:49928
v_mul_f32 v101,s61,v101
v_mfma_f32_16x16x32_bf16 a[108:111],a[184:187],a[248:251],a[108:111]
ds_read_b64_tr_b16 a[184:185],v22 offset:33544
v_accvgpr_read_b32 v102,a74
v_mfma_f32_16x16x32_bf16 a[108:111],a[180:183],a[220:223],a[108:111]
ds_read_b64_tr_b16 a[180:181],v22 offset:17160
v_accvgpr_read_b32 v103,a75
v_mfma_f32_16x16x32_bf16 a[108:111],a[176:179],a[216:219],a[108:111]
buffer_load_dwordx4 v5,s[20:23],s47 offen offset:2048 nt lds
v_mfma_f32_16x16x32_bf16 a[16:19],a[128:131],a[192:195],a[16:19]
ds_read_b64_tr_b16 a[176:177],v22 offset:776
v_mul_f32 v116,s61,v116
v_mfma_f32_16x16x32_bf16 a[16:19],a[132:135],a[196:199],a[16:19]
ds_read_b64_tr_b16 a[178:179],v22 offset:2824
v_mul_f32 v117,s61,v117
v_mfma_f32_16x16x32_bf16 a[16:19],a[136:139],a[224:227],a[16:19]
ds_read_b64_tr_b16 a[182:183],v22 offset:19208
v_accvgpr_read_b32 v118,a106
v_mfma_f32_16x16x32_bf16 a[16:19],a[140:143],a[228:231],a[16:19]
ds_read_b64_tr_b16 a[186:187],v22 offset:35592
v_accvgpr_read_b32 v119,a107
v_mfma_f32_16x16x32_bf16 a[48:51],a[140:143],a[236:239],a[48:51]
ds_read_b64_tr_b16 a[190:191],v22 offset:51976
v_accvgpr_read_b32 v12,a102
v_mfma_f32_16x16x32_bf16 a[48:51],a[136:139],a[232:235],a[48:51]
v_accvgpr_read_b32 v13,a103
v_mul_f32 v114,s61,v114
v_mfma_f32_16x16x32_bf16 a[48:51],a[132:135],a[204:207],a[48:51]
v_mul_f32 v115,s61,v115
v_accvgpr_read_b32 v14,a14
v_mfma_f32_16x16x32_bf16 a[48:51],a[128:131],a[200:203],a[48:51]
v_accvgpr_read_b32 v15,a15
v_mul_f32 v70,s61,v70
v_mfma_f32_16x16x32_bf16 a[80:83],a[128:131],a[208:211],a[80:83]
v_mul_f32 v71,s61,v71
v_cvt_pk_bf16_f32 v100,v100,v101
v_mfma_f32_16x16x32_bf16 a[80:83],a[132:135],a[212:215],a[80:83]
v_mul_f32 v102,s61,v102
v_mul_f32 v103,s61,v103
v_mfma_f32_16x16x32_bf16 a[80:83],a[136:139],a[240:243],a[80:83]
v_cvt_pk_bf16_f32 v116,v116,v117
v_mul_f32 v118,s61,v118
v_mfma_f32_16x16x32_bf16 a[80:83],a[140:143],a[244:247],a[80:83]
v_mul_f32 v119,s61,v119
v_mul_f32 v12,s61,v12
v_mfma_f32_16x16x32_bf16 a[112:115],a[140:143],a[252:255],a[112:115]
)A"
R"A(ds_read_b64_tr_b16 a[140:141],v22 offset:50184
v_mul_f32 v13,s61,v13
v_mfma_f32_16x16x32_bf16 a[112:115],a[136:139],a[248:251],a[112:115]
buffer_load_dwordx4 v5,s[20:23],s47 offen offset:3072 nt lds
v_mfma_f32_16x16x32_bf16 a[112:115],a[132:135],a[220:223],a[112:115]
ds_read_b64_tr_b16 a[132:133],v22 offset:50176
v_cvt_pk_bf16_f32 v114,v114,v115
v_mfma_f32_16x16x32_bf16 a[112:115],a[128:131],a[216:219],a[112:115]
s_waitcnt lgkmcnt(2)
ds_read_b64_tr_b16 a[128:129],v22 offset:33792
v_mul_f32 v14,s61,v14
v_mfma_f32_16x16x32_bf16 a[20:23],a[144:147],a[192:195],a[20:23]
ds_read_b64_tr_b16 a[130:131],v22 offset:35840
v_mul_f32 v15,s61,v15
v_mfma_f32_16x16x32_bf16 a[20:23],a[148:151],a[196:199],a[20:23]
ds_read_b64_tr_b16 a[134:135],v22 offset:52224
v_cvt_pk_bf16_f32 v70,v70,v71
v_mfma_f32_16x16x32_bf16 a[20:23],a[152:155],a[224:227],a[20:23]
ds_read_b64_tr_b16 a[136:137],v22 offset:33800
v_cvt_pk_bf16_f32 v101,v102,v103
v_mfma_f32_16x16x32_bf16 a[20:23],a[156:159],a[228:231],a[20:23]
ds_read_b64_tr_b16 a[138:139],v22 offset:35848
v_cvt_pk_bf16_f32 v117,v118,v119
v_mfma_f32_16x16x32_bf16 a[52:55],a[156:159],a[236:239],a[52:55]
ds_read_b64_tr_b16 a[142:143],v22 offset:52232
v_cvt_pk_bf16_f32 v115,v12,v13
v_mfma_f32_16x16x32_bf16 a[52:55],a[152:155],a[232:235],a[52:55]
v_cvt_pk_bf16_f32 v71,v14,v15
v_accvgpr_read_b32 v102,a76
v_mfma_f32_16x16x32_bf16 a[52:55],a[148:151],a[204:207],a[52:55]
v_accvgpr_read_b32 v103,a77
v_accvgpr_read_b32 v118,a108
v_mfma_f32_16x16x32_bf16 a[52:55],a[144:147],a[200:203],a[52:55]
v_accvgpr_read_b32 v119,a109
v_accvgpr_read_b32 v12,a78
v_mfma_f32_16x16x32_bf16 a[84:87],a[144:147],a[208:211],a[84:87]
s_addk_i32 m0,16384
v_accvgpr_read_b32 v13,a79
v_mul_f32 v102,s61,v102
v_mfma_f32_16x16x32_bf16 a[84:87],a[148:151],a[212:215],a[84:87]
v_mul_f32 v103,s61,v103
v_accvgpr_read_b32 v14,a110
v_mfma_f32_16x16x32_bf16 a[84:87],a[152:155],a[240:243],a[84:87]
v_accvgpr_read_b32 v15,a111
v_mul_f32 v118,s61,v118
v_mfma_f32_16x16x32_bf16 a[84:87],a[156:159],a[244:247],a[84:87]
buffer_load_dwordx4 v4,s[24:27],s47 offen nt lds
v_mfma_f32_16x16x32_bf16 a[116:119],a[156:159],a[252:255],a[116:119]
ds_read_b64_tr_b16 a[156:157],v22 offset:50440
v_mul_f32 v119,s61,v119
v_mfma_f32_16x16x32_bf16 a[116:119],a[152:155],a[248:251],a[116:119]
ds_read_b64_tr_b16 a[152:153],v22 offset:34056
v_mul_f32 v12,s61,v12
v_mfma_f32_16x16x32_bf16 a[116:119],a[148:151],a[220:223],a[116:119]
ds_read_b64_tr_b16 a[148:149],v22 offset:50432
v_mul_f32 v13,s61,v13
v_mfma_f32_16x16x32_bf16 a[116:119],a[144:147],a[216:219],a[116:119]
ds_read_b64_tr_b16 a[144:145],v22 offset:34048
v_cvt_pk_bf16_f32 v102,v102,v103
v_mfma_f32_16x16x32_bf16 a[24:27],a[160:163],a[192:195],a[24:27]
ds_read_b64_tr_b16 a[146:147],v22 offset:36096
v_mul_f32 v14,s61,v14
v_mfma_f32_16x16x32_bf16 a[24:27],a[164:167],a[196:199],a[24:27]
ds_read_b64_tr_b16 a[150:151],v22 offset:52480
v_mul_f32 v15,s61,v15
v_mfma_f32_16x16x32_bf16 a[24:27],a[168:171],a[224:227],a[24:27]
ds_read_b64_tr_b16 a[154:155],v22 offset:36104
v_cvt_pk_bf16_f32 v118,v118,v119
v_mfma_f32_16x16x32_bf16 a[24:27],a[172:175],a[228:231],a[24:27]
ds_read_b64_tr_b16 a[158:159],v22 offset:52488
v_cvt_pk_bf16_f32 v103,v12,v13
v_mfma_f32_16x16x32_bf16 a[56:59],a[172:175],a[236:239],a[56:59]
v_cvt_pk_bf16_f32 v119,v14,v15
v_accvgpr_read_b32 v96,a64
v_mfma_f32_16x16x32_bf16 a[56:59],a[168:171],a[232:235],a[56:59]
.p2align 3
v_accvgpr_read_b32 v72,a16
v_accvgpr_read_b32 v73,a17
v_accvgpr_read_b32 v74,a18
v_accvgpr_read_b32 v75,a19
v_accvgpr_read_b32 v12,a20
v_accvgpr_read_b32 v13,a21
v_accvgpr_read_b32 v14,a22
v_accvgpr_read_b32 v15,a23
v_pk_mul_f32 v[72:73],s[60:61],v[72:73] op_sel:[1,0] op_sel_hi:[1,1]
v_pk_mul_f32 v[74:75],s[60:61],v[74:75] op_sel:[1,0] op_sel_hi:[1,1]
v_pk_mul_f32 v[12:13],s[60:61],v[12:13] op_sel:[1,0] op_sel_hi:[1,1]
v_pk_mul_f32 v[14:15],s[60:61],v[14:15] op_sel:[1,0] op_sel_hi:[1,1]
v_cvt_pk_bf16_f32 v72,v72,v73
v_cvt_pk_bf16_f32 v73,v74,v75
v_cvt_pk_bf16_f32 v74,v12,v13
v_cvt_pk_bf16_f32 v75,v14,v15
v_mfma_f32_16x16x32_bf16 a[56:59],a[164:167],a[204:207],a[56:59]
v_accvgpr_read_b32 v97,a65
v_mul_f32 v96,s61,v96
v_mfma_f32_16x16x32_bf16 a[56:59],a[160:163],a[200:203],a[56:59]
.p2align 3
v_accvgpr_read_b32 v88,a48
v_accvgpr_read_b32 v89,a49
v_accvgpr_read_b32 v90,a50
v_accvgpr_read_b32 v91,a51
v_accvgpr_read_b32 v12,a52
v_accvgpr_read_b32 v13,a53
v_accvgpr_read_b32 v14,a54
v_accvgpr_read_b32 v15,a55
v_pk_mul_f32 v[88:89],s[60:61],v[88:89] op_sel:[1,0] op_sel_hi:[1,1]
v_pk_mul_f32 v[90:91],s[60:61],v[90:91] op_sel:[1,0] op_sel_hi:[1,1]
v_pk_mul_f32 v[12:13],s[60:61],v[12:13] op_sel:[1,0] op_sel_hi:[1,1]
v_pk_mul_f32 v[14:15],s[60:61],v[14:15] op_sel:[1,0] op_sel_hi:[1,1]
v_cvt_pk_bf16_f32 v88,v88,v89
v_cvt_pk_bf16_f32 v89,v90,v91
v_cvt_pk_bf16_f32 v90,v12,v13
v_cvt_pk_bf16_f32 v91,v14,v15
v_mfma_f32_16x16x32_bf16 a[88:91],a[160:163],a[208:211],a[88:91]
v_mul_f32 v97,s61,v97
v_accvgpr_read_b32 v98,a66
v_mfma_f32_16x16x32_bf16 a[88:91],a[164:167],a[212:215],a[88:91]
buffer_load_dwordx4 v5,s[24:27],s47 offen offset:1024 nt lds
v_mfma_f32_16x16x32_bf16 a[88:91],a[168:171],a[240:243],a[88:91]
v_accvgpr_read_b32 v99,a67
v_cvt_pk_bf16_f32 v96,v96,v97
v_mfma_f32_16x16x32_bf16 a[88:91],a[172:175],a[244:247],a[88:91]
v_mul_f32 v98,s61,v98
v_mul_f32 v99,s61,v99
v_mfma_f32_16x16x32_bf16 a[120:123],a[172:175],a[252:255],a[120:123]
ds_read_b64_tr_b16 a[172:173],v22 offset:50696
v_cvt_pk_bf16_f32 v97,v98,v99
v_mfma_f32_16x16x32_bf16 a[120:123],a[168:171],a[248:251],a[120:123]
ds_read_b64_tr_b16 a[168:169],v22 offset:34312
v_accvgpr_read_b32 v84,a40
v_mfma_f32_16x16x32_bf16 a[120:123],a[164:167],a[220:223],a[120:123]
ds_read_b64_tr_b16 a[164:165],v22 offset:50688
v_accvgpr_read_b32 v85,a41
v_mfma_f32_16x16x32_bf16 a[120:123],a[160:163],a[216:219],a[120:123]
ds_read_b64_tr_b16 a[160:161],v22 offset:34304
v_accvgpr_read_b32 v98,a68
v_mfma_f32_16x16x32_bf16 a[28:31],a[176:179],a[192:195],a[28:31]
ds_read_b64_tr_b16 a[162:163],v22 offset:36352
v_accvgpr_read_b32 v99,a69
v_mfma_f32_16x16x32_bf16 a[28:31],a[180:183],a[196:199],a[28:31]
ds_read_b64_tr_b16 a[166:167],v22 offset:52736
v_mul_f32 v84,s61,v84
v_mfma_f32_16x16x32_bf16 a[28:31],a[184:187],a[224:227],a[28:31]
ds_read_b64_tr_b16 a[170:171],v22 offset:36360
v_mul_f32 v85,s61,v85
v_mfma_f32_16x16x32_bf16 a[28:31],a[188:191],a[228:231],a[28:31]
ds_read_b64_tr_b16 a[174:175],v22 offset:52744
v_accvgpr_read_b32 v86,a42
v_mfma_f32_16x16x32_bf16 a[60:63],a[188:191],a[236:239],a[60:63]
.p2align 3
v_accvgpr_read_b32 v104,a80
v_accvgpr_read_b32 v105,a81
v_accvgpr_read_b32 v106,a82
v_accvgpr_read_b32 v107,a83
v_accvgpr_read_b32 v12,a84
v_accvgpr_read_b32 v13,a85
v_accvgpr_read_b32 v14,a86
v_accvgpr_read_b32 v15,a87
v_pk_mul_f32 v[104:105],s[60:61],v[104:105] op_sel:[1,0] op_sel_hi:[1,1]
v_pk_mul_f32 v[106:107],s[60:61],v[106:107] op_sel:[1,0] op_sel_hi:[1,1]
v_pk_mul_f32 v[12:13],s[60:61],v[12:13] op_sel:[1,0] op_sel_hi:[1,1]
v_pk_mul_f32 v[14:15],s[60:61],v[14:15] op_sel:[1,0] op_sel_hi:[1,1]
v_cvt_pk_bf16_f32 v104,v104,v105
v_cvt_pk_bf16_f32 v105,v106,v107
v_cvt_pk_bf16_f32 v106,v12,v13
v_cvt_pk_bf16_f32 v107,v14,v15
v_mfma_f32_16x16x32_bf16 a[60:63],a[184:187],a[232:235],a[60:63]
v_accvgpr_read_b32 v87,a43
v_accvgpr_read_b32 v16,a70
v_mfma_f32_16x16x32_bf16 a[60:63],a[180:183],a[204:207],a[60:63]
.p2align 3
v_accvgpr_read_b32 v120,a112
v_accvgpr_read_b32 v121,a113
v_accvgpr_read_b32 v122,a114
v_accvgpr_read_b32 v123,a115
v_accvgpr_read_b32 v12,a116
v_accvgpr_read_b32 v13,a117
v_accvgpr_read_b32 v14,a118
v_accvgpr_read_b32 v15,a119
v_pk_mul_f32 v[120:121],s[60:61],v[120:121] op_sel:[1,0] op_sel_hi:[1,1]
v_pk_mul_f32 v[122:123],s[60:61],v[122:123] op_sel:[1,0] op_sel_hi:[1,1]
v_pk_mul_f32 v[12:13],s[60:61],v[12:13] op_sel:[1,0] op_sel_hi:[1,1]
v_pk_mul_f32 v[14:15],s[60:61],v[14:15] op_sel:[1,0] op_sel_hi:[1,1]
v_cvt_pk_bf16_f32 v120,v120,v121
v_cvt_pk_bf16_f32 v121,v122,v123
v_cvt_pk_bf16_f32 v122,v12,v13
v_cvt_pk_bf16_f32 v123,v14,v15
v_mfma_f32_16x16x32_bf16 a[60:63],a[176:179],a[200:203],a[60:63]
buffer_load_dwordx4 v4,s[24:27],s47 offen offset:2048 nt lds
v_mfma_f32_16x16x32_bf16 a[92:95],a[176:179],a[208:211],a[92:95]
v_accvgpr_read_b32 v17,a71
v_mul_f32 v98,s61,v98
v_mfma_f32_16x16x32_bf16 a[92:95],a[180:183],a[212:215],a[92:95]
v_mul_f32 v99,s61,v99
v_cvt_pk_bf16_f32 v84,v84,v85
v_mfma_f32_16x16x32_bf16 a[92:95],a[184:187],a[240:243],a[92:95]
v_mul_f32 v86,s61,v86
v_mul_f32 v87,s61,v87
v_mfma_f32_16x16x32_bf16 a[92:95],a[188:191],a[244:247],a[92:95]
v_mul_f32 v16,s61,v16
v_mul_f32 v17,s61,v17
v_mfma_f32_16x16x32_bf16 a[124:127],a[188:191],a[252:255],a[124:127]
ds_read_b64_tr_b16 a[188:189],v22 offset:50952
v_cvt_pk_bf16_f32 v98,v98,v99
v_mfma_f32_16x16x32_bf16 a[124:127],a[184:187],a[248:251],a[124:127]
ds_read_b64_tr_b16 a[184:185],v22 offset:34568
v_cvt_pk_bf16_f32 v85,v86,v87
v_mfma_f32_16x16x32_bf16 a[124:127],a[180:183],a[220:223],a[124:127]
ds_read_b64_tr_b16 a[180:181],v22 offset:50944
v_cvt_pk_bf16_f32 v99,v16,v17
v_mfma_f32_16x16x32_bf16 a[124:127],a[176:179],a[216:219],a[124:127]
s_waitcnt lgkmcnt(3)
ds_read_b64_tr_b16 a[176:177],v22 offset:34560
v_accvgpr_read_b32 v86,a44
v_mfma_f32_16x16x32_bf16 v[128:131],a[128:131],a[224:227],v[128:131]
ds_read_b64_tr_b16 a[178:179],v22 offset:36608
v_accvgpr_read_b32 v87,a45
v_mfma_f32_16x16x32_bf16 v[128:131],a[132:135],a[228:231],v[128:131]
ds_read_b64_tr_b16 a[182:183],v22 offset:52992
v_accvgpr_read_b32 v16,a46
v_mfma_f32_16x16x32_bf16 v[136:139],a[132:135],a[236:239],v[136:139]
ds_read_b64_tr_b16 a[186:187],v22 offset:36616
v_accvgpr_read_b32 v17,a47
v_mfma_f32_16x16x32_bf16 v[136:139],a[128:131],a[232:235],v[136:139]
ds_read_b64_tr_b16 a[190:191],v22 offset:53000
v_mul_f32 v86,s61,v86
v_mfma_f32_16x16x32_bf16 v[144:147],a[128:131],a[240:243],v[144:147]
.p2align 3
v_accvgpr_read_b32 v76,a24
v_accvgpr_read_b32 v77,a25
v_accvgpr_read_b32 v78,a26
v_accvgpr_read_b32 v79,a27
v_accvgpr_read_b32 v12,a28
v_accvgpr_read_b32 v13,a29
v_accvgpr_read_b32 v14,a30
v_accvgpr_read_b32 v15,a31
v_pk_mul_f32 v[76:77],s[60:61],v[76:77] op_sel:[1,0] op_sel_hi:[1,1]
v_pk_mul_f32 v[78:79],s[60:61],v[78:79] op_sel:[1,0] op_sel_hi:[1,1]
v_pk_mul_f32 v[12:13],s[60:61],v[12:13] op_sel:[1,0] op_sel_hi:[1,1]
v_pk_mul_f32 v[14:15],s[60:61],v[14:15] op_sel:[1,0] op_sel_hi:[1,1]
v_cvt_pk_bf16_f32 v76,v76,v77
v_cvt_pk_bf16_f32 v77,v78,v79
v_cvt_pk_bf16_f32 v78,v12,v13
v_cvt_pk_bf16_f32 v79,v14,v15
v_mfma_f32_16x16x32_bf16 v[144:147],a[132:135],a[244:247],v[144:147]
buffer_load_dwordx4 v5,s[24:27],s47 offen offset:3072 nt lds
v_mfma_f32_16x16x32_bf16 v[152:155],a[132:135],a[252:255],v[152:155]
s_or_b32 s47,s47,0x18000
s_addk_i32 s47,4096
s_and_b32 s47,s47,0x27000
v_mfma_f32_16x16x32_bf16 v[152:155],a[128:131],a[248:251],v[152:155]
s_or_b32 s47,s47,s46
v_mul_f32 v87,s61,v87
v_mul_f32 v16,s61,v16
v_mfma_f32_16x16x32_bf16 v[132:135],a[136:139],a[224:227],v[132:135]
s_and_b32 s46,s54,0xfffc0000
v_mul_f32 v17,s61,v17
v_cvt_pk_bf16_f32 v86,v86,v87
v_mfma_f32_16x16x32_bf16 v[132:135],a[140:143],a[228:231],v[132:135]
s_addk_i32 s54,16384
v_cvt_pk_bf16_f32 v87,v16,v17
v_mfma_f32_16x16x32_bf16 v[140:143],a[140:143],a[236:239],v[140:143]
.p2align 3
v_accvgpr_read_b32 v92,a56
v_accvgpr_read_b32 v93,a57
v_accvgpr_read_b32 v94,a58
v_accvgpr_read_b32 v95,a59
v_accvgpr_read_b32 v12,a60
v_accvgpr_read_b32 v13,a61
v_accvgpr_read_b32 v14,a62
v_accvgpr_read_b32 v15,a63
v_pk_mul_f32 v[92:93],s[60:61],v[92:93] op_sel:[1,0] op_sel_hi:[1,1]
v_pk_mul_f32 v[94:95],s[60:61],v[94:95] op_sel:[1,0] op_sel_hi:[1,1]
v_pk_mul_f32 v[12:13],s[60:61],v[12:13] op_sel:[1,0] op_sel_hi:[1,1]
v_pk_mul_f32 v[14:15],s[60:61],v[14:15] op_sel:[1,0] op_sel_hi:[1,1]
v_cvt_pk_bf16_f32 v92,v92,v93
v_cvt_pk_bf16_f32 v93,v94,v95
v_cvt_pk_bf16_f32 v94,v12,v13
v_cvt_pk_bf16_f32 v95,v14,v15
v_mfma_f32_16x16x32_bf16 v[140:143],a[136:139],a[232:235],v[140:143]
.p2align 3
v_accvgpr_read_b32 v108,a88
v_accvgpr_read_b32 v109,a89
v_accvgpr_read_b32 v110,a90
v_accvgpr_read_b32 v111,a91
v_accvgpr_read_b32 v12,a92
v_accvgpr_read_b32 v13,a93
v_accvgpr_read_b32 v14,a94
)A"
R"A(v_accvgpr_read_b32 v15,a95
v_pk_mul_f32 v[108:109],s[60:61],v[108:109] op_sel:[1,0] op_sel_hi:[1,1]
v_pk_mul_f32 v[110:111],s[60:61],v[110:111] op_sel:[1,0] op_sel_hi:[1,1]
v_pk_mul_f32 v[12:13],s[60:61],v[12:13] op_sel:[1,0] op_sel_hi:[1,1]
v_pk_mul_f32 v[14:15],s[60:61],v[14:15] op_sel:[1,0] op_sel_hi:[1,1]
v_cvt_pk_bf16_f32 v108,v108,v109
v_cvt_pk_bf16_f32 v109,v110,v111
v_cvt_pk_bf16_f32 v110,v12,v13
v_cvt_pk_bf16_f32 v111,v14,v15
v_mfma_f32_16x16x32_bf16 v[148:151],a[136:139],a[240:243],v[148:151]
.p2align 3
v_accvgpr_read_b32 v124,a120
v_accvgpr_read_b32 v125,a121
v_accvgpr_read_b32 v126,a122
v_accvgpr_read_b32 v127,a123
v_accvgpr_read_b32 v12,a124
v_accvgpr_read_b32 v13,a125
v_accvgpr_read_b32 v14,a126
v_accvgpr_read_b32 v15,a127
v_pk_mul_f32 v[124:125],s[60:61],v[124:125] op_sel:[1,0] op_sel_hi:[1,1]
v_pk_mul_f32 v[126:127],s[60:61],v[126:127] op_sel:[1,0] op_sel_hi:[1,1]
v_pk_mul_f32 v[12:13],s[60:61],v[12:13] op_sel:[1,0] op_sel_hi:[1,1]
v_pk_mul_f32 v[14:15],s[60:61],v[14:15] op_sel:[1,0] op_sel_hi:[1,1]
v_cvt_pk_bf16_f32 v124,v124,v125
v_cvt_pk_bf16_f32 v125,v126,v127
v_cvt_pk_bf16_f32 v126,v12,v13
v_cvt_pk_bf16_f32 v127,v14,v15
v_mfma_f32_16x16x32_bf16 v[148:151],a[140:143],a[244:247],v[148:151]
v_mfma_f32_16x16x32_bf16 v[156:159],a[140:143],a[252:255],v[156:159]
s_waitcnt vmcnt(8) lgkmcnt(0)
v_mfma_f32_16x16x32_bf16 v[156:159],a[136:139],a[248:251],v[156:159]
s_barrier
ds_read_b128 v[48:51],v18
v_mfma_f32_16x16x32_bf16 v[160:163],a[144:147],a[224:227],v[160:163]
ds_read_b64_tr_b16 a[192:193],v2
v_mfma_f32_16x16x32_bf16 v[160:163],a[148:151],a[228:231],v[160:163]
ds_read_b64_tr_b16 a[194:195],v2 offset:4096
v_mfma_f32_16x16x32_bf16 v[168:171],a[148:151],a[236:239],v[168:171]
ds_read_b64_tr_b16 a[196:197],v2 offset:8192
v_mfma_f32_16x16x32_bf16 v[168:171],a[144:147],a[232:235],v[168:171]
ds_read_b128 v[52:55],v18 offset:256
v_mfma_f32_16x16x32_bf16 v[176:179],a[144:147],a[240:243],v[176:179]
v_mul_f32 v128,s61,v128
v_mul_f32 v129,s61,v129
v_mul_f32 v130,s61,v130
v_mul_f32 v131,s61,v131
.p2align 3
v_cvt_pk_bf16_f32 v128,v128,v129
v_cvt_pk_bf16_f32 v129,v130,v131
v_pk_mul_f32 v[132:133],s[60:61],v[132:133] op_sel:[1,0] op_sel_hi:[1,1]
v_pk_mul_f32 v[134:135],s[60:61],v[134:135] op_sel:[1,0] op_sel_hi:[1,1]
v_pk_mul_f32 v[136:137],s[60:61],v[136:137] op_sel:[1,0] op_sel_hi:[1,1]
v_pk_mul_f32 v[138:139],s[60:61],v[138:139] op_sel:[1,0] op_sel_hi:[1,1]
v_pk_mul_f32 v[140:141],s[60:61],v[140:141] op_sel:[1,0] op_sel_hi:[1,1]
v_pk_mul_f32 v[142:143],s[60:61],v[142:143] op_sel:[1,0] op_sel_hi:[1,1]
v_pk_mul_f32 v[144:145],s[60:61],v[144:145] op_sel:[1,0] op_sel_hi:[1,1]
v_pk_mul_f32 v[146:147],s[60:61],v[146:147] op_sel:[1,0] op_sel_hi:[1,1]
v_pk_mul_f32 v[148:149],s[60:61],v[148:149] op_sel:[1,0] op_sel_hi:[1,1]
v_pk_mul_f32 v[150:151],s[60:61],v[150:151] op_sel:[1,0] op_sel_hi:[1,1]
v_pk_mul_f32 v[152:153],s[60:61],v[152:153] op_sel:[1,0] op_sel_hi:[1,1]
v_pk_mul_f32 v[154:155],s[60:61],v[154:155] op_sel:[1,0] op_sel_hi:[1,1]
v_pk_mul_f32 v[156:157],s[60:61],v[156:157] op_sel:[1,0] op_sel_hi:[1,1]
v_pk_mul_f32 v[158:159],s[60:61],v[158:159] op_sel:[1,0] op_sel_hi:[1,1]
v_mfma_f32_16x16x32_bf16 v[176:179],a[148:151],a[244:247],v[176:179]
ds_read_b128 v[56:59],v18 offset:512
v_cvt_pk_bf16_f32 v130,v132,v133
v_mfma_f32_16x16x32_bf16 v[184:187],a[148:151],a[252:255],v[184:187]
v_cvt_pk_bf16_f32 v132,v136,v137
v_cvt_pk_bf16_f32 v133,v138,v139
v_mfma_f32_16x16x32_bf16 v[184:187],a[144:147],a[248:251],v[184:187]
ds_read_b128 v[60:63],v18 offset:768
v_cvt_pk_bf16_f32 v131,v134,v135
v_mfma_f32_16x16x32_bf16 v[164:167],a[152:155],a[224:227],v[164:167]
v_cvt_pk_bf16_f32 v134,v140,v141
v_cvt_pk_bf16_f32 v135,v142,v143
v_mfma_f32_16x16x32_bf16 v[164:167],a[156:159],a[228:231],v[164:167]
ds_read_b128 v[20:23],v18 offset:16384
v_cvt_pk_bf16_f32 v144,v144,v145
v_mfma_f32_16x16x32_bf16 v[172:175],a[156:159],a[236:239],v[172:175]
v_cvt_pk_bf16_f32 v145,v146,v147
v_cvt_pk_bf16_f32 v146,v148,v149
v_mfma_f32_16x16x32_bf16 v[172:175],a[152:155],a[232:235],v[172:175]
ds_read_b128 a[128:131],v1
v_cvt_pk_bf16_f32 v148,v152,v153
v_mfma_f32_16x16x32_bf16 v[180:183],a[152:155],a[240:243],v[180:183]
v_cvt_pk_bf16_f32 v149,v154,v155
v_cvt_pk_bf16_f32 v147,v150,v151
v_mfma_f32_16x16x32_bf16 v[180:183],a[156:159],a[244:247],v[180:183]
ds_read_b128 a[132:135],v1 offset:256
v_cvt_pk_bf16_f32 v150,v156,v157
v_mfma_f32_16x16x32_bf16 v[188:191],a[156:159],a[252:255],v[188:191]
ds_read_b64_tr_b16 a[198:199],v2 offset:12288
v_cvt_pk_bf16_f32 v151,v158,v159
v_mfma_f32_16x16x32_bf16 v[188:191],a[152:155],a[248:251],v[188:191]
ds_read_b64_tr_b16 a[204:205],v3 offset:8192
v_mfma_f32_16x16x32_bf16 v[192:195],a[160:163],a[224:227],v[192:195]
ds_read_b64_tr_b16 a[206:207],v3 offset:12288
v_mfma_f32_16x16x32_bf16 v[192:195],a[164:167],a[228:231],v[192:195]
s_add_u32 m0,32768,s51
ds_read_b128 a[136:139],v1 offset:512
v_mfma_f32_16x16x32_bf16 v[200:203],a[164:167],a[236:239],v[200:203]
s_waitcnt lgkmcnt(6)
v_and_b32 v20,0x7fff7fff,v20
v_and_b32 v21,0x7fff7fff,v21
v_mfma_f32_16x16x32_bf16 v[200:203],a[160:163],a[232:235],v[200:203]
v_sad_u16 v20,v20,0,0
v_and_b32 v22,0x7fff7fff,v22
v_mfma_f32_16x16x32_bf16 v[208:211],a[160:163],a[240:243],v[208:211]
buffer_load_dwordx4 v4,s[20:23],s47 offen nt lds
v_mfma_f32_16x16x32_bf16 v[208:211],a[164:167],a[244:247],v[208:211]
v_sad_u16 v20,v21,0,v20
v_and_b32 v23,0x7fff7fff,v23
v_mfma_f32_16x16x32_bf16 v[216:219],a[164:167],a[252:255],v[216:219]
v_sad_u16 v20,v22,0,v20
v_sad_u16 v20,v23,0,v20
v_mfma_f32_16x16x32_bf16 v[216:219],a[160:163],a[248:251],v[216:219]
s_nop 1
v_add_u32_dpp v20,v20,v20 quad_perm:[1,0,3,2] row_mask:0xf bank_mask:0xf
v_mfma_f32_16x16x32_bf16 v[196:199],a[168:171],a[224:227],v[196:199]
s_nop 1
v_add_u32_dpp v20,v20,v20 quad_perm:[2,3,0,1] row_mask:0xf bank_mask:0xf
v_mfma_f32_16x16x32_bf16 v[196:199],a[172:175],a[228:231],v[196:199]
v_cvt_f32_u32 v20,v20
v_mov_b32 v21,0xb9800000
v_mfma_f32_16x16x32_bf16 v[204:207],a[172:175],a[236:239],v[204:207]
ds_read_b128 a[140:143],v1 offset:768
v_fmaak_f32 v20,v20,v21,0x42e8c937
v_mfma_f32_16x16x32_bf16 v[204:207],a[168:171],a[232:235],v[204:207]
s_nop 0
v_readfirstlane_b32 s52,v20
v_mfma_f32_16x16x32_bf16 v[212:215],a[168:171],a[240:243],v[212:215]
s_nop 1
s_min_u32 s52,s52,0xc2200000
v_mfma_f32_16x16x32_bf16 v[212:215],a[172:175],a[244:247],v[212:215]
s_min_i32 s52,s52,0x41a00000
ds_read_b128 a[156:159],v1 offset:4864
v_mfma_f32_16x16x32_bf16 v[220:223],a[172:175],a[252:255],v[220:223]
v_mul_f32 v160,s61,v160
v_mul_f32 v161,s61,v161
v_mul_f32 v162,s61,v162
v_mul_f32 v163,s61,v163
.p2align 3
v_cvt_pk_bf16_f32 v160,v160,v161
v_cvt_pk_bf16_f32 v161,v162,v163
v_pk_mul_f32 v[164:165],s[60:61],v[164:165] op_sel:[1,0] op_sel_hi:[1,1]
v_pk_mul_f32 v[166:167],s[60:61],v[166:167] op_sel:[1,0] op_sel_hi:[1,1]
v_pk_mul_f32 v[168:169],s[60:61],v[168:169] op_sel:[1,0] op_sel_hi:[1,1]
v_pk_mul_f32 v[170:171],s[60:61],v[170:171] op_sel:[1,0] op_sel_hi:[1,1]
v_pk_mul_f32 v[172:173],s[60:61],v[172:173] op_sel:[1,0] op_sel_hi:[1,1]
v_pk_mul_f32 v[174:175],s[60:61],v[174:175] op_sel:[1,0] op_sel_hi:[1,1]
v_pk_mul_f32 v[176:177],s[60:61],v[176:177] op_sel:[1,0] op_sel_hi:[1,1]
v_pk_mul_f32 v[178:179],s[60:61],v[178:179] op_sel:[1,0] op_sel_hi:[1,1]
v_pk_mul_f32 v[180:181],s[60:61],v[180:181] op_sel:[1,0] op_sel_hi:[1,1]
v_pk_mul_f32 v[182:183],s[60:61],v[182:183] op_sel:[1,0] op_sel_hi:[1,1]
v_pk_mul_f32 v[184:185],s[60:61],v[184:185] op_sel:[1,0] op_sel_hi:[1,1]
v_pk_mul_f32 v[186:187],s[60:61],v[186:187] op_sel:[1,0] op_sel_hi:[1,1]
v_pk_mul_f32 v[188:189],s[60:61],v[188:189] op_sel:[1,0] op_sel_hi:[1,1]
v_pk_mul_f32 v[190:191],s[60:61],v[190:191] op_sel:[1,0] op_sel_hi:[1,1]
v_mfma_f32_16x16x32_bf16 v[220:223],a[168:171],a[248:251],v[220:223]
ds_read_b128 a[152:155],v1 offset:4608
v_cvt_pk_bf16_f32 v162,v164,v165
v_mfma_f32_16x16x32_bf16 v[224:227],a[176:179],a[224:227],v[224:227]
v_cvt_pk_bf16_f32 v164,v168,v169
v_cvt_pk_bf16_f32 v165,v170,v171
v_mfma_f32_16x16x32_bf16 v[224:227],a[180:183],a[228:231],v[224:227]
buffer_load_dwordx4 v4,s[20:23],s47 offen offset:1024 nt lds
v_mfma_f32_16x16x32_bf16 v[232:235],a[180:183],a[236:239],v[232:235]
ds_read_b128 a[148:151],v1 offset:4352
v_cvt_pk_bf16_f32 v163,v166,v167
v_mfma_f32_16x16x32_bf16 v[232:235],a[176:179],a[232:235],v[232:235]
v_cvt_pk_bf16_f32 v166,v172,v173
v_cvt_pk_bf16_f32 v167,v174,v175
v_mfma_f32_16x16x32_bf16 v[240:243],a[176:179],a[240:243],v[240:243]
ds_read_b128 a[144:147],v1 offset:4096
v_cvt_pk_bf16_f32 v176,v176,v177
v_mfma_f32_16x16x32_bf16 v[240:243],a[180:183],a[244:247],v[240:243]
v_cvt_pk_bf16_f32 v177,v178,v179
v_cvt_pk_bf16_f32 v178,v180,v181
v_mfma_f32_16x16x32_bf16 v[248:251],a[180:183],a[252:255],v[248:251]
ds_read_b128 a[160:163],v1 offset:8192
v_cvt_pk_bf16_f32 v180,v184,v185
v_mfma_f32_16x16x32_bf16 v[248:251],a[176:179],a[248:251],v[248:251]
v_cvt_pk_bf16_f32 v181,v186,v187
v_cvt_pk_bf16_f32 v179,v182,v183
v_mfma_f32_16x16x32_bf16 v[228:231],a[184:187],a[224:227],v[228:231]
ds_read_b128 a[164:167],v1 offset:8448
v_cvt_pk_bf16_f32 v182,v188,v189
v_mfma_f32_16x16x32_bf16 v[228:231],a[188:191],a[228:231],v[228:231]
ds_read_b64_tr_b16 a[200:201],v3
v_cvt_pk_bf16_f32 v183,v190,v191
v_mfma_f32_16x16x32_bf16 v[236:239],a[188:191],a[236:239],v[236:239]
v_mul_f32 v192,s61,v192
v_mul_f32 v193,s61,v193
v_mul_f32 v194,s61,v194
v_mul_f32 v195,s61,v195
.p2align 3
v_cvt_pk_bf16_f32 v192,v192,v193
v_cvt_pk_bf16_f32 v193,v194,v195
v_pk_mul_f32 v[196:197],s[60:61],v[196:197] op_sel:[1,0] op_sel_hi:[1,1]
v_pk_mul_f32 v[198:199],s[60:61],v[198:199] op_sel:[1,0] op_sel_hi:[1,1]
v_pk_mul_f32 v[200:201],s[60:61],v[200:201] op_sel:[1,0] op_sel_hi:[1,1]
v_pk_mul_f32 v[202:203],s[60:61],v[202:203] op_sel:[1,0] op_sel_hi:[1,1]
v_pk_mul_f32 v[204:205],s[60:61],v[204:205] op_sel:[1,0] op_sel_hi:[1,1]
v_pk_mul_f32 v[206:207],s[60:61],v[206:207] op_sel:[1,0] op_sel_hi:[1,1]
v_pk_mul_f32 v[208:209],s[60:61],v[208:209] op_sel:[1,0] op_sel_hi:[1,1]
v_pk_mul_f32 v[210:211],s[60:61],v[210:211] op_sel:[1,0] op_sel_hi:[1,1]
v_pk_mul_f32 v[212:213],s[60:61],v[212:213] op_sel:[1,0] op_sel_hi:[1,1]
v_pk_mul_f32 v[214:215],s[60:61],v[214:215] op_sel:[1,0] op_sel_hi:[1,1]
v_pk_mul_f32 v[216:217],s[60:61],v[216:217] op_sel:[1,0] op_sel_hi:[1,1]
v_pk_mul_f32 v[218:219],s[60:61],v[218:219] op_sel:[1,0] op_sel_hi:[1,1]
v_pk_mul_f32 v[220:221],s[60:61],v[220:221] op_sel:[1,0] op_sel_hi:[1,1]
v_pk_mul_f32 v[222:223],s[60:61],v[222:223] op_sel:[1,0] op_sel_hi:[1,1]
v_mfma_f32_16x16x32_bf16 v[236:239],a[184:187],a[232:235],v[236:239]
ds_read_b128 a[168:171],v1 offset:8704
v_cvt_pk_bf16_f32 v194,v196,v197
v_mfma_f32_16x16x32_bf16 v[244:247],a[184:187],a[240:243],v[244:247]
v_cvt_pk_bf16_f32 v196,v200,v201
v_cvt_pk_bf16_f32 v197,v202,v203
v_mfma_f32_16x16x32_bf16 v[244:247],a[188:191],a[244:247],v[244:247]
ds_read_b128 a[172:175],v1 offset:8960
v_cvt_pk_bf16_f32 v195,v198,v199
v_mfma_f32_16x16x32_bf16 v[252:255],a[188:191],a[252:255],v[252:255]
buffer_load_dwordx4 v5,s[20:23],s47 offen offset:2048 nt lds
v_mfma_f32_16x16x32_bf16 v[252:255],a[184:187],a[248:251],v[252:255]
ds_read_b128 a[188:191],v1 offset:13056
v_cvt_pk_bf16_f32 v198,v204,v205
v_mfma_f32_16x16x32_bf16 v[32:35],v[48:51],v[64:67],0
v_cvt_pk_bf16_f32 v199,v206,v207
v_cvt_pk_bf16_f32 v208,v208,v209
v_mfma_f32_16x16x32_bf16 v[32:35],v[52:55],v[68:71],v[32:35]
ds_read_b128 a[184:187],v1 offset:12800
v_cvt_pk_bf16_f32 v209,v210,v211
v_mfma_f32_16x16x32_bf16 v[32:35],v[56:59],v[72:75],v[32:35]
v_cvt_pk_bf16_f32 v210,v212,v213
v_cvt_pk_bf16_f32 v212,v216,v217
v_mfma_f32_16x16x32_bf16 v[32:35],v[60:63],v[76:79],v[32:35]
ds_read_b128 a[180:183],v1 offset:12544
v_cvt_pk_bf16_f32 v213,v218,v219
v_mfma_f32_16x16x32_bf16 v[36:39],v[48:51],v[80:83],0
ds_read_b64_tr_b16 a[202:203],v3 offset:4096
v_cvt_pk_bf16_f32 v211,v214,v215
v_mfma_f32_16x16x32_bf16 v[36:39],v[52:55],v[84:87],v[36:39]
v_cvt_pk_bf16_f32 v214,v220,v221
v_cvt_pk_bf16_f32 v215,v222,v223
v_mfma_f32_16x16x32_bf16 v[36:39],v[56:59],v[88:91],v[36:39]
ds_read_b128 a[176:179],v1 offset:12288
v_add_u32 v1,s48,v1
)A"
R"A(v_mfma_f32_16x16x32_bf16 v[36:39],v[60:63],v[92:95],v[36:39]
ds_read_b64_tr_b16 a[208:209],v2 offset:256
v_mfma_f32_16x16x32_bf16 v[40:43],v[48:51],v[96:99],0
ds_read_b64_tr_b16 a[210:211],v2 offset:4352
v_mfma_f32_16x16x32_bf16 v[40:43],v[52:55],v[100:103],v[40:43]
v_max3_f32 v32,v32,v33,v34
v_max3_f32 v32,v32,v35,s18
v_mfma_f32_16x16x32_bf16 v[40:43],v[56:59],v[104:107],v[40:43]
v_sub_f32 v35,s52,v32
v_sub_f32 v34,s52,v32
v_mfma_f32_16x16x32_bf16 v[40:43],v[60:63],v[108:111],v[40:43]
v_sub_f32 v33,s52,v32
v_sub_f32 v32,s52,v32
v_mfma_f32_16x16x32_bf16 v[44:47],v[48:51],v[112:115],0
buffer_load_dwordx4 v5,s[20:23],s47 offen offset:3072 nt lds
v_mfma_f32_16x16x32_bf16 v[44:47],v[52:55],v[116:119],v[44:47]
v_max3_f32 v36,v36,v37,v38
v_max3_f32 v36,v36,v39,s19
v_mfma_f32_16x16x32_bf16 v[44:47],v[56:59],v[120:123],v[44:47]
v_sub_f32 v39,s52,v36
v_sub_f32 v38,s52,v36
v_mfma_f32_16x16x32_bf16 v[44:47],v[60:63],v[124:127],v[44:47]
s_waitcnt lgkmcnt(1)
v_sub_f32 v37,s52,v36
v_sub_f32 v36,s52,v36
v_mfma_f32_16x16x32_bf16 v[136:139],a[128:131],v[64:67],v[32:35]
ds_read_b64_tr_b16 a[212:213],v2 offset:8448
ds_read_b64_tr_b16 a[214:215],v2 offset:12544
v_mfma_f32_16x16x32_bf16 v[136:139],a[132:135],v[68:71],v[136:139]
v_max3_f32 v40,v40,v41,v42
v_max3_f32 v40,v40,v43,s100
v_mfma_f32_16x16x32_bf16 v[136:139],a[136:139],v[72:75],v[136:139]
v_sub_f32 v43,s52,v40
v_sub_f32 v42,s52,v40
v_mfma_f32_16x16x32_bf16 v[136:139],a[140:143],v[76:79],v[136:139]
v_sub_f32 v41,s52,v40
v_sub_f32 v40,s52,v40
v_mfma_f32_16x16x32_bf16 v[140:143],a[156:159],v[76:79],v[32:35]
ds_read_b64_tr_b16 a[220:221],v3 offset:8448
ds_read_b64_tr_b16 a[222:223],v3 offset:12544
v_mfma_f32_16x16x32_bf16 v[140:143],a[152:155],v[72:75],v[140:143]
s_addk_i32 m0,16384
ds_read_b64_tr_b16 a[216:217],v3 offset:256
v_max3_f32 v44,v44,v45,v46
v_mfma_f32_16x16x32_bf16 v[140:143],a[148:151],v[68:71],v[140:143]
ds_read_b64_tr_b16 a[218:219],v3 offset:4352
v_max3_f32 v44,v44,v47,s101
v_mfma_f32_16x16x32_bf16 v[140:143],a[144:147],v[64:67],v[140:143]
ds_read_b64_tr_b16 a[224:225],v2 offset:512
v_mov_b32 v48,0
v_mfma_f32_16x16x32_bf16 v[152:155],a[160:163],v[64:67],v[32:35]
buffer_load_dwordx4 v4,s[24:27],s47 offen nt lds
v_mfma_f32_16x16x32_bf16 v[152:155],a[164:167],v[68:71],v[152:155]
v_exp_f32 v136,v136
v_mfma_f32_16x16x32_bf16 v[152:155],a[168:171],v[72:75],v[152:155]
v_exp_f32 v137,v137
v_mfma_f32_16x16x32_bf16 v[152:155],a[172:175],v[76:79],v[152:155]
v_exp_f32 v138,v138
v_mfma_f32_16x16x32_bf16 v[156:159],a[188:191],v[76:79],v[32:35]
v_exp_f32 v139,v139
v_mfma_f32_16x16x32_bf16 v[156:159],a[184:187],v[72:75],v[156:159]
v_cvt_pk_bf16_f32 v136,v136,v137
v_cvt_pk_bf16_f32 v137,v138,v139
v_mfma_f32_16x16x32_bf16 v[156:159],a[180:183],v[68:71],v[156:159]
v_exp_f32 v140,v140
v_mfma_f32_16x16x32_bf16 v[156:159],a[176:179],v[64:67],v[156:159]
v_exp_f32 v141,v141
v_mfma_f32_16x16x32_bf16 v[168:171],a[128:131],v[80:83],v[36:39]
v_exp_f32 v142,v142
v_mfma_f32_16x16x32_bf16 v[168:171],a[132:135],v[84:87],v[168:171]
v_exp_f32 v143,v143
v_mfma_f32_16x16x32_bf16 v[168:171],a[136:139],v[88:91],v[168:171]
v_cvt_pk_bf16_f32 v138,v140,v141
v_cvt_pk_bf16_f32 v139,v142,v143
v_mfma_f32_16x16x32_bf16 v[168:171],a[140:143],v[92:95],v[168:171]
v_exp_f32 v152,v152
v_mfma_f32_16x16x32_bf16 v[172:175],a[156:159],v[92:95],v[36:39]
v_exp_f32 v153,v153
v_mfma_f32_16x16x32_bf16 v[172:175],a[152:155],v[88:91],v[172:175]
buffer_load_dwordx4 v5,s[24:27],s47 offen offset:1024 nt lds
v_mfma_f32_16x16x32_bf16 v[172:175],a[148:151],v[84:87],v[172:175]
v_exp_f32 v154,v154
v_mfma_f32_16x16x32_bf16 v[172:175],a[144:147],v[80:83],v[172:175]
v_exp_f32 v155,v155
v_mfma_f32_16x16x32_bf16 v[184:187],a[160:163],v[80:83],v[36:39]
v_exp_f32 v156,v156
v_mfma_f32_16x16x32_bf16 v[184:187],a[164:167],v[84:87],v[184:187]
v_exp_f32 v157,v157
v_mfma_f32_16x16x32_bf16 v[184:187],a[168:171],v[88:91],v[184:187]
v_exp_f32 v158,v158
v_mfma_f32_16x16x32_bf16 v[184:187],a[172:175],v[92:95],v[184:187]
v_exp_f32 v159,v159
v_mfma_f32_16x16x32_bf16 v[188:191],a[188:191],v[92:95],v[36:39]
v_cvt_pk_bf16_f32 v140,v152,v153
v_cvt_pk_bf16_f32 v141,v154,v155
v_mfma_f32_16x16x32_bf16 v[188:191],a[184:187],v[88:91],v[188:191]
v_cvt_pk_bf16_f32 v142,v156,v157
v_cvt_pk_bf16_f32 v143,v158,v159
v_mfma_f32_16x16x32_bf16 v[188:191],a[180:183],v[84:87],v[188:191]
ds_read_b64_tr_b16 a[226:227],v2 offset:4608
ds_read_b64_tr_b16 a[228:229],v2 offset:8704
v_mfma_f32_16x16x32_bf16 v[188:191],a[176:179],v[80:83],v[188:191]
ds_read_b64_tr_b16 a[230:231],v2 offset:12800
v_mov_b32 v49,0
v_mfma_f32_16x16x32_bf16 v[200:203],a[128:131],v[96:99],v[40:43]
ds_read_b64_tr_b16 a[236:237],v3 offset:8704
v_mov_b32 v50,0
v_mfma_f32_16x16x32_bf16 v[200:203],a[132:135],v[100:103],v[200:203]
ds_read_b64_tr_b16 a[238:239],v3 offset:12800
v_mov_b32 v51,0
v_mfma_f32_16x16x32_bf16 v[200:203],a[136:139],v[104:107],v[200:203]
buffer_load_dwordx4 v4,s[24:27],s47 offen offset:2048 nt lds
v_mfma_f32_16x16x32_bf16 v[200:203],a[140:143],v[108:111],v[200:203]
ds_read_b64_tr_b16 a[232:233],v3 offset:512
ds_read_b64_tr_b16 a[234:235],v3 offset:4608
v_mfma_f32_16x16x32_bf16 v[204:207],a[156:159],v[108:111],v[40:43]
ds_read_b64_tr_b16 a[240:241],v2 offset:768
v_sub_f32 v47,s52,v44
v_mfma_f32_16x16x32_bf16 v[204:207],a[152:155],v[104:107],v[204:207]
ds_read_b64_tr_b16 a[242:243],v2 offset:4864
v_sub_f32 v46,s52,v44
v_mfma_f32_16x16x32_bf16 v[204:207],a[148:151],v[100:103],v[204:207]
ds_read_b64_tr_b16 a[244:245],v2 offset:8960
v_sub_f32 v45,s52,v44
v_mfma_f32_16x16x32_bf16 v[204:207],a[144:147],v[96:99],v[204:207]
ds_read_b64_tr_b16 a[246:247],v2 offset:13056
ds_read_b64_tr_b16 a[252:253],v3 offset:8960
v_mfma_f32_16x16x32_bf16 v[216:219],a[160:163],v[96:99],v[40:43]
ds_read_b64_tr_b16 a[254:255],v3 offset:13056
v_sub_f32 v44,s52,v44
v_mfma_f32_16x16x32_bf16 v[216:219],a[164:167],v[100:103],v[216:219]
ds_read_b64_tr_b16 a[248:249],v3 offset:768
v_mov_b32 v52,0
v_mfma_f32_16x16x32_bf16 v[216:219],a[168:171],v[104:107],v[216:219]
ds_read_b64_tr_b16 a[250:251],v3 offset:4864
v_mov_b32 v53,0
v_mfma_f32_16x16x32_bf16 v[216:219],a[172:175],v[108:111],v[216:219]
v_mul_f32 v224,s61,v224
v_mul_f32 v225,s61,v225
v_mul_f32 v226,s61,v226
v_mul_f32 v227,s61,v227
.p2align 3
v_cvt_pk_bf16_f32 v224,v224,v225
v_cvt_pk_bf16_f32 v225,v226,v227
v_pk_mul_f32 v[228:229],s[60:61],v[228:229] op_sel:[1,0] op_sel_hi:[1,1]
v_pk_mul_f32 v[230:231],s[60:61],v[230:231] op_sel:[1,0] op_sel_hi:[1,1]
v_pk_mul_f32 v[232:233],s[60:61],v[232:233] op_sel:[1,0] op_sel_hi:[1,1]
v_pk_mul_f32 v[234:235],s[60:61],v[234:235] op_sel:[1,0] op_sel_hi:[1,1]
v_pk_mul_f32 v[236:237],s[60:61],v[236:237] op_sel:[1,0] op_sel_hi:[1,1]
v_pk_mul_f32 v[238:239],s[60:61],v[238:239] op_sel:[1,0] op_sel_hi:[1,1]
v_pk_mul_f32 v[240:241],s[60:61],v[240:241] op_sel:[1,0] op_sel_hi:[1,1]
v_pk_mul_f32 v[242:243],s[60:61],v[242:243] op_sel:[1,0] op_sel_hi:[1,1]
v_pk_mul_f32 v[244:245],s[60:61],v[244:245] op_sel:[1,0] op_sel_hi:[1,1]
v_pk_mul_f32 v[246:247],s[60:61],v[246:247] op_sel:[1,0] op_sel_hi:[1,1]
v_pk_mul_f32 v[248:249],s[60:61],v[248:249] op_sel:[1,0] op_sel_hi:[1,1]
v_pk_mul_f32 v[250:251],s[60:61],v[250:251] op_sel:[1,0] op_sel_hi:[1,1]
v_pk_mul_f32 v[252:253],s[60:61],v[252:253] op_sel:[1,0] op_sel_hi:[1,1]
v_pk_mul_f32 v[254:255],s[60:61],v[254:255] op_sel:[1,0] op_sel_hi:[1,1]
v_mfma_f32_16x16x32_bf16 v[220:223],a[188:191],v[108:111],v[40:43]
v_cvt_pk_bf16_f32 v226,v228,v229
v_cvt_pk_bf16_f32 v228,v232,v233
v_mfma_f32_16x16x32_bf16 v[220:223],a[184:187],v[104:107],v[220:223]
v_cvt_pk_bf16_f32 v229,v234,v235
v_cvt_pk_bf16_f32 v227,v230,v231
v_mfma_f32_16x16x32_bf16 v[220:223],a[180:183],v[100:103],v[220:223]
v_cvt_pk_bf16_f32 v230,v236,v237
v_cvt_pk_bf16_f32 v231,v238,v239
v_mfma_f32_16x16x32_bf16 v[220:223],a[176:179],v[96:99],v[220:223]
buffer_load_dwordx4 v5,s[24:27],s47 offen offset:3072 nt lds
v_mfma_f32_16x16x32_bf16 a[0:3],a[192:195],v[136:139],0.15915494
s_or_b32 s47,s47,0x18000
v_cvt_pk_bf16_f32 v240,v240,v241
v_cvt_pk_bf16_f32 v241,v242,v243
v_mfma_f32_16x16x32_bf16 a[0:3],a[196:199],v[140:143],a[0:3]
s_addk_i32 s47,4096
v_cvt_pk_bf16_f32 v242,v244,v245
v_cvt_pk_bf16_f32 v244,v248,v249
v_mfma_f32_16x16x32_bf16 a[4:7],a[204:207],v[140:143],0.15915494
s_and_b32 s47,s47,0x27000
s_or_b32 s47,s47,s46
v_cvt_pk_bf16_f32 v245,v250,v251
v_mfma_f32_16x16x32_bf16 a[4:7],a[200:203],v[136:139],a[4:7]
s_memrealtime s[76:77]
v_cvt_pk_bf16_f32 v243,v246,v247
v_cvt_pk_bf16_f32 v246,v252,v253
s_waitcnt lgkmcnt(0)
v_mfma_f32_16x16x32_bf16 a[8:11],a[208:211],v[136:139],0.15915494
s_and_b32 s46,s54,0xfffc0000
v_cvt_pk_bf16_f32 v247,v254,v255
v_mov_b32 v54,0
v_mfma_f32_16x16x32_bf16 a[8:11],a[212:215],v[140:143],a[8:11]
v_exp_f32 v168,v168
v_mfma_f32_16x16x32_bf16 a[12:15],a[220:223],v[140:143],0.15915494
v_exp_f32 v169,v169
v_mfma_f32_16x16x32_bf16 a[12:15],a[216:219],v[136:139],a[12:15]
v_exp_f32 v170,v170
v_mfma_f32_16x16x32_bf16 a[16:19],a[224:227],v[136:139],0.15915494
v_exp_f32 v171,v171
v_mfma_f32_16x16x32_bf16 a[16:19],a[228:231],v[140:143],a[16:19]
v_cvt_pk_bf16_f32 v168,v168,v169
v_cvt_pk_bf16_f32 v169,v170,v171
v_mfma_f32_16x16x32_bf16 a[20:23],a[236:239],v[140:143],0.15915494
s_waitcnt lgkmcnt(0)
v_exp_f32 v172,v172
v_mfma_f32_16x16x32_bf16 a[20:23],a[232:235],v[136:139],a[20:23]
s_add_u32 s78,s76,s60
v_exp_f32 v173,v173
v_mfma_f32_16x16x32_bf16 a[24:27],a[240:243],v[136:139],0.15915494
s_lshr_b32 s60,s94,2
v_exp_f32 v174,v174
v_mfma_f32_16x16x32_bf16 a[24:27],a[244:247],v[140:143],a[24:27]
s_max_i32 s60,s60,124
v_exp_f32 v175,v175
v_mfma_f32_16x16x32_bf16 a[28:31],a[252:255],v[140:143],0.15915494
v_cvt_pk_bf16_f32 v170,v172,v173
v_cvt_pk_bf16_f32 v171,v174,v175
v_mfma_f32_16x16x32_bf16 a[28:31],a[248:251],v[136:139],a[28:31]
v_exp_f32 v184,v184
v_smfmac_f32_16x16x64_bf16 v[48:51],v[28:31],v[136:143],v27
v_mov_b32 v55,0
v_mfma_f32_16x16x32_bf16 v[232:235],a[128:131],v[112:115],v[44:47]
s_waitcnt vmcnt(8)
v_exp_f32 v185,v185
v_mfma_f32_16x16x32_bf16 v[232:235],a[132:135],v[116:119],v[232:235]
s_barrier
v_exp_f32 v186,v186
v_mfma_f32_16x16x32_bf16 v[232:235],a[136:139],v[120:123],v[232:235]
v_exp_f32 v187,v187
v_mfma_f32_16x16x32_bf16 v[232:235],a[140:143],v[124:127],v[232:235]
s_add_u32 m0,65536,s51
v_exp_f32 v188,v188
v_mfma_f32_16x16x32_bf16 v[236:239],a[156:159],v[124:127],v[44:47]
v_exp_f32 v189,v189
v_mfma_f32_16x16x32_bf16 v[236:239],a[152:155],v[120:123],v[236:239]
v_exp_f32 v190,v190
v_mfma_f32_16x16x32_bf16 v[236:239],a[148:151],v[116:119],v[236:239]
buffer_load_dwordx4 v4,s[20:23],s47 offen nt lds
v_mfma_f32_16x16x32_bf16 v[236:239],a[144:147],v[112:115],v[236:239]
v_exp_f32 v191,v191
v_mfma_f32_16x16x32_bf16 v[248:251],a[160:163],v[112:115],v[44:47]
v_cvt_pk_bf16_f32 v172,v184,v185
v_cvt_pk_bf16_f32 v173,v186,v187
v_mfma_f32_16x16x32_bf16 v[248:251],a[164:167],v[116:119],v[248:251]
v_cvt_pk_bf16_f32 v174,v188,v189
v_cvt_pk_bf16_f32 v175,v190,v191
v_mfma_f32_16x16x32_bf16 v[248:251],a[168:171],v[120:123],v[248:251]
ds_read_b128 a[128:131],v1
v_mov_b32 v56,0
v_mfma_f32_16x16x32_bf16 v[248:251],a[172:175],v[124:127],v[248:251]
v_exp_f32 v200,v200
v_mfma_f32_16x16x32_bf16 v[252:255],a[188:191],v[124:127],v[44:47]
ds_read_b128 a[132:135],v1 offset:256
v_mov_b32 v57,0
v_mfma_f32_16x16x32_bf16 v[252:255],a[184:187],v[120:123],v[252:255]
v_exp_f32 v201,v201
v_mfma_f32_16x16x32_bf16 v[252:255],a[180:183],v[116:119],v[252:255]
ds_read_b128 a[136:139],v1 offset:512
v_cvt_pk_bf16_f32 v200,v200,v201
v_mfma_f32_16x16x32_bf16 v[252:255],a[176:179],v[112:115],v[252:255]
v_exp_f32 v202,v202
v_mfma_f32_16x16x32_bf16 a[32:35],a[192:195],v[168:171],0.15915494
ds_read_b128 a[140:143],v1 offset:768
v_mov_b32 v58,0
v_mfma_f32_16x16x32_bf16 a[32:35],a[196:199],v[172:175],a[32:35]
v_exp_f32 v203,v203
v_mfma_f32_16x16x32_bf16 a[36:39],a[204:207],v[172:175],0.15915494
ds_read_b128 a[156:159],v1 offset:4864
v_cvt_pk_bf16_f32 v201,v202,v203
v_mfma_f32_16x16x32_bf16 a[36:39],a[200:203],v[168:171],a[36:39]
v_exp_f32 v204,v204
v_mfma_f32_16x16x32_bf16 a[40:43],a[208:211],v[168:171],0.15915494
buffer_load_dwordx4 v4,s[20:23],s47 offen offset:1024 nt lds
)A"
R"A(v_mfma_f32_16x16x32_bf16 a[40:43],a[212:215],v[172:175],a[40:43]
ds_read_b128 a[152:155],v1 offset:4608
v_mov_b32 v59,0
v_mfma_f32_16x16x32_bf16 a[44:47],a[220:223],v[172:175],0.15915494
v_exp_f32 v205,v205
v_mfma_f32_16x16x32_bf16 a[44:47],a[216:219],v[168:171],a[44:47]
ds_read_b128 a[148:151],v1 offset:4352
v_cvt_pk_bf16_f32 v202,v204,v205
v_mfma_f32_16x16x32_bf16 a[48:51],a[224:227],v[168:171],0.15915494
v_exp_f32 v206,v206
v_mfma_f32_16x16x32_bf16 a[48:51],a[228:231],v[172:175],a[48:51]
ds_read_b128 a[144:147],v1 offset:4096
v_mov_b32 v60,0
v_mfma_f32_16x16x32_bf16 a[52:55],a[236:239],v[172:175],0.15915494
v_exp_f32 v207,v207
v_mfma_f32_16x16x32_bf16 a[52:55],a[232:235],v[168:171],a[52:55]
ds_read_b128 a[160:163],v1 offset:8192
v_cvt_pk_bf16_f32 v203,v206,v207
v_mfma_f32_16x16x32_bf16 a[56:59],a[240:243],v[168:171],0.15915494
v_exp_f32 v216,v216
v_mfma_f32_16x16x32_bf16 a[56:59],a[244:247],v[172:175],a[56:59]
ds_read_b128 a[164:167],v1 offset:8448
v_mov_b32 v61,0
v_mfma_f32_16x16x32_bf16 a[60:63],a[252:255],v[172:175],0.15915494
v_exp_f32 v217,v217
v_mfma_f32_16x16x32_bf16 a[60:63],a[248:251],v[168:171],a[60:63]
ds_read_b128 a[168:171],v1 offset:8704
v_cvt_pk_bf16_f32 v204,v216,v217
v_smfmac_f32_16x16x64_bf16 v[52:55],v[28:31],v[168:175],v27
s_waitcnt lgkmcnt(4)
v_mov_b32 v62,0
v_mfma_f32_16x16x32_bf16 v[136:139],a[128:131],v[64:67],v[32:35]
ds_read_b128 a[172:175],v1 offset:8960
v_mov_b32 v63,0
v_mfma_f32_16x16x32_bf16 v[136:139],a[132:135],v[68:71],v[136:139]
buffer_load_dwordx4 v5,s[20:23],s47 offen offset:2048 nt lds
v_mfma_f32_16x16x32_bf16 v[136:139],a[136:139],v[72:75],v[136:139]
ds_read_b128 a[188:191],v1 offset:13056
v_add_u32 v2,s48,v2
v_mfma_f32_16x16x32_bf16 v[136:139],a[140:143],v[76:79],v[136:139]
v_exp_f32 v218,v218
v_mfma_f32_16x16x32_bf16 v[140:143],a[156:159],v[76:79],v[32:35]
ds_read_b128 a[184:187],v1 offset:12800
v_add_u32 v3,s48,v3
v_mfma_f32_16x16x32_bf16 v[140:143],a[152:155],v[72:75],v[140:143]
ds_read_b128 a[180:183],v1 offset:12544
v_mfma_f32_16x16x32_bf16 v[140:143],a[148:151],v[68:71],v[140:143]
s_waitcnt lgkmcnt(4)
ds_read_b128 a[176:179],v1 offset:12288
v_mfma_f32_16x16x32_bf16 v[140:143],a[144:147],v[64:67],v[140:143]
v_mfma_f32_16x16x32_bf16 v[152:155],a[160:163],v[64:67],v[32:35]
s_mov_b32 s48,32768
v_exp_f32 v219,v219
v_mfma_f32_16x16x32_bf16 v[152:155],a[164:167],v[68:71],v[152:155]
v_exp_f32 v220,v220
v_mfma_f32_16x16x32_bf16 v[152:155],a[168:171],v[72:75],v[152:155]
s_waitcnt lgkmcnt(3)
v_exp_f32 v221,v221
v_mfma_f32_16x16x32_bf16 v[152:155],a[172:175],v[76:79],v[152:155]
v_exp_f32 v222,v222
v_mfma_f32_16x16x32_bf16 v[156:159],a[188:191],v[76:79],v[32:35]
s_waitcnt lgkmcnt(2)
v_exp_f32 v223,v223
v_mfma_f32_16x16x32_bf16 v[156:159],a[184:187],v[72:75],v[156:159]
s_waitcnt lgkmcnt(1)
v_cvt_pk_bf16_f32 v205,v218,v219
v_cvt_pk_bf16_f32 v206,v220,v221
v_mfma_f32_16x16x32_bf16 v[156:159],a[180:183],v[68:71],v[156:159]
s_waitcnt lgkmcnt(0)
v_cvt_pk_bf16_f32 v207,v222,v223
v_add_u32 v1,s48,v1
v_mfma_f32_16x16x32_bf16 v[156:159],a[176:179],v[64:67],v[156:159]
buffer_load_dwordx4 v5,s[20:23],s47 offen offset:3072 nt lds
v_mfma_f32_16x16x32_bf16 a[64:67],a[192:195],v[200:203],0.15915494
v_exp_f32 v232,v232
v_mfma_f32_16x16x32_bf16 a[64:67],a[196:199],v[204:207],a[64:67]
v_exp_f32 v233,v233
v_mfma_f32_16x16x32_bf16 a[68:71],a[204:207],v[204:207],0.15915494
v_exp_f32 v234,v234
v_mfma_f32_16x16x32_bf16 a[68:71],a[200:203],v[200:203],a[68:71]
v_exp_f32 v235,v235
v_mfma_f32_16x16x32_bf16 a[72:75],a[208:211],v[200:203],0.15915494
v_exp_f32 v236,v236
v_mfma_f32_16x16x32_bf16 a[72:75],a[212:215],v[204:207],a[72:75]
v_exp_f32 v237,v237
v_mfma_f32_16x16x32_bf16 a[76:79],a[220:223],v[204:207],0.15915494
v_exp_f32 v238,v238
v_mfma_f32_16x16x32_bf16 a[76:79],a[216:219],v[200:203],a[76:79]
v_exp_f32 v239,v239
v_mfma_f32_16x16x32_bf16 a[80:83],a[224:227],v[200:203],0.15915494
v_exp_f32 v248,v248
v_mfma_f32_16x16x32_bf16 a[80:83],a[228:231],v[204:207],a[80:83]
v_exp_f32 v249,v249
v_mfma_f32_16x16x32_bf16 a[84:87],a[236:239],v[204:207],0.15915494
s_addk_i32 m0,16384
v_exp_f32 v250,v250
v_mfma_f32_16x16x32_bf16 a[84:87],a[232:235],v[200:203],a[84:87]
v_exp_f32 v251,v251
v_mfma_f32_16x16x32_bf16 a[88:91],a[240:243],v[200:203],0.15915494
v_cvt_pk_bf16_f32 v232,v232,v233
v_cvt_pk_bf16_f32 v233,v234,v235
v_mfma_f32_16x16x32_bf16 a[88:91],a[244:247],v[204:207],a[88:91]
buffer_load_dwordx4 v4,s[24:27],s47 offen nt lds
v_mfma_f32_16x16x32_bf16 a[92:95],a[252:255],v[204:207],0.15915494
v_cvt_pk_bf16_f32 v234,v236,v237
v_cvt_pk_bf16_f32 v235,v238,v239
v_mfma_f32_16x16x32_bf16 a[92:95],a[248:251],v[200:203],a[92:95]
v_exp_f32 v252,v252
v_smfmac_f32_16x16x64_bf16 v[56:59],v[28:31],v[200:207],v27
v_cvt_pk_bf16_f32 v236,v248,v249
v_mfma_f32_16x16x32_bf16 v[168:171],a[128:131],v[80:83],v[36:39]
v_exp_f32 v253,v253
v_mfma_f32_16x16x32_bf16 v[168:171],a[132:135],v[84:87],v[168:171]
v_exp_f32 v254,v254
v_mfma_f32_16x16x32_bf16 v[168:171],a[136:139],v[88:91],v[168:171]
v_exp_f32 v255,v255
v_mfma_f32_16x16x32_bf16 v[168:171],a[140:143],v[92:95],v[168:171]
v_cvt_pk_bf16_f32 v237,v250,v251
v_cvt_pk_bf16_f32 v238,v252,v253
v_mfma_f32_16x16x32_bf16 v[172:175],a[156:159],v[92:95],v[36:39]
v_cvt_pk_bf16_f32 v239,v254,v255
v_mfma_f32_16x16x32_bf16 v[172:175],a[152:155],v[88:91],v[172:175]
v_exp_f32 v136,v136
v_mfma_f32_16x16x32_bf16 v[172:175],a[148:151],v[84:87],v[172:175]
v_exp_f32 v137,v137
v_mfma_f32_16x16x32_bf16 v[172:175],a[144:147],v[80:83],v[172:175]
v_exp_f32 v138,v138
v_mfma_f32_16x16x32_bf16 v[184:187],a[160:163],v[80:83],v[36:39]
v_exp_f32 v139,v139
v_mfma_f32_16x16x32_bf16 v[184:187],a[164:167],v[84:87],v[184:187]
v_exp_f32 v140,v140
v_mfma_f32_16x16x32_bf16 v[184:187],a[168:171],v[88:91],v[184:187]
buffer_load_dwordx4 v5,s[24:27],s47 offen offset:1024 nt lds
v_mfma_f32_16x16x32_bf16 v[184:187],a[172:175],v[92:95],v[184:187]
v_exp_f32 v141,v141
v_mfma_f32_16x16x32_bf16 v[188:191],a[188:191],v[92:95],v[36:39]
v_exp_f32 v142,v142
v_mfma_f32_16x16x32_bf16 v[188:191],a[184:187],v[88:91],v[188:191]
v_exp_f32 v143,v143
v_mfma_f32_16x16x32_bf16 v[188:191],a[180:183],v[84:87],v[188:191]
v_exp_f32 v152,v152
v_mfma_f32_16x16x32_bf16 v[188:191],a[176:179],v[80:83],v[188:191]
v_exp_f32 v153,v153
v_mfma_f32_16x16x32_bf16 a[96:99],a[192:195],v[232:235],0.15915494
ds_read_b64_tr_b16 a[192:193],v2
ds_read_b64_tr_b16 a[194:195],v2 offset:4096
v_mfma_f32_16x16x32_bf16 a[96:99],a[196:199],v[236:239],a[96:99]
ds_read_b64_tr_b16 a[196:197],v2 offset:8192
v_cvt_pk_bf16_f32 v136,v136,v137
v_mfma_f32_16x16x32_bf16 a[100:103],a[204:207],v[236:239],0.15915494
ds_read_b64_tr_b16 a[198:199],v2 offset:12288
v_cvt_pk_bf16_f32 v137,v138,v139
v_mfma_f32_16x16x32_bf16 a[100:103],a[200:203],v[232:235],a[100:103]
ds_read_b64_tr_b16 a[204:205],v3 offset:8192
v_cvt_pk_bf16_f32 v138,v140,v141
v_mfma_f32_16x16x32_bf16 a[104:107],a[208:211],v[232:235],0.15915494
ds_read_b64_tr_b16 a[206:207],v3 offset:12288
ds_read_b64_tr_b16 a[200:201],v3
v_mfma_f32_16x16x32_bf16 a[104:107],a[212:215],v[236:239],a[104:107]
ds_read_b64_tr_b16 a[202:203],v3 offset:4096
v_cvt_pk_bf16_f32 v139,v142,v143
v_mfma_f32_16x16x32_bf16 a[108:111],a[220:223],v[236:239],0.15915494
ds_read_b64_tr_b16 a[208:209],v2 offset:256
v_cvt_pk_bf16_f32 v140,v152,v153
v_mfma_f32_16x16x32_bf16 a[108:111],a[216:219],v[232:235],a[108:111]
v_exp_f32 v154,v154
v_mfma_f32_16x16x32_bf16 a[112:115],a[224:227],v[232:235],0.15915494
buffer_load_dwordx4 v4,s[24:27],s47 offen offset:2048 nt lds
v_mfma_f32_16x16x32_bf16 a[112:115],a[228:231],v[236:239],a[112:115]
ds_read_b64_tr_b16 a[210:211],v2 offset:4352
ds_read_b64_tr_b16 a[212:213],v2 offset:8448
v_mfma_f32_16x16x32_bf16 a[116:119],a[236:239],v[236:239],0.15915494
v_exp_f32 v155,v155
v_mfma_f32_16x16x32_bf16 a[116:119],a[232:235],v[232:235],a[116:119]
ds_read_b64_tr_b16 a[214:215],v2 offset:12544
ds_read_b64_tr_b16 a[220:221],v3 offset:8448
v_mfma_f32_16x16x32_bf16 a[120:123],a[240:243],v[232:235],0.15915494
v_exp_f32 v156,v156
v_mfma_f32_16x16x32_bf16 a[120:123],a[244:247],v[236:239],a[120:123]
v_exp_f32 v157,v157
v_mfma_f32_16x16x32_bf16 a[124:127],a[252:255],v[236:239],0.15915494
v_exp_f32 v158,v158
v_mfma_f32_16x16x32_bf16 a[124:127],a[248:251],v[232:235],a[124:127]
v_exp_f32 v159,v159
v_smfmac_f32_16x16x64_bf16 v[60:63],v[28:31],v[232:239],v27
ds_read_b64_tr_b16 a[222:223],v3 offset:12544
v_mfma_f32_16x16x32_bf16 v[200:203],a[128:131],v[96:99],v[40:43]
v_cvt_pk_bf16_f32 v141,v154,v155
v_cvt_pk_bf16_f32 v142,v156,v157
v_mfma_f32_16x16x32_bf16 v[200:203],a[132:135],v[100:103],v[200:203]
ds_read_b64_tr_b16 a[216:217],v3 offset:256
v_cvt_pk_bf16_f32 v143,v158,v159
v_mfma_f32_16x16x32_bf16 v[200:203],a[136:139],v[104:107],v[200:203]
ds_read_b64_tr_b16 a[218:219],v3 offset:4352
ds_read_b64_tr_b16 a[224:225],v2 offset:512
v_mfma_f32_16x16x32_bf16 v[200:203],a[140:143],v[108:111],v[200:203]
ds_read_b64_tr_b16 a[226:227],v2 offset:4608
v_mfma_f32_16x16x32_bf16 v[204:207],a[156:159],v[108:111],v[40:43]
ds_read_b64_tr_b16 a[228:229],v2 offset:8704
v_mfma_f32_16x16x32_bf16 v[204:207],a[152:155],v[104:107],v[204:207]
buffer_load_dwordx4 v5,s[24:27],s47 offen offset:3072 nt lds
v_mfma_f32_16x16x32_bf16 v[204:207],a[148:151],v[100:103],v[204:207]
s_or_b32 s47,s47,0x18000
ds_read_b64_tr_b16 a[230:231],v2 offset:12800
ds_read_b64_tr_b16 a[236:237],v3 offset:8704
v_mfma_f32_16x16x32_bf16 v[204:207],a[144:147],v[96:99],v[204:207]
s_addk_i32 s47,4096
s_and_b32 s47,s47,0x27000
ds_read_b64_tr_b16 a[238:239],v3 offset:12800
v_mfma_f32_16x16x32_bf16 v[216:219],a[160:163],v[96:99],v[40:43]
s_or_b32 s47,s47,s46
ds_read_b64_tr_b16 a[232:233],v3 offset:512
v_mfma_f32_16x16x32_bf16 v[216:219],a[164:167],v[100:103],v[216:219]
ds_read_b64_tr_b16 a[234:235],v3 offset:4608
v_mfma_f32_16x16x32_bf16 v[216:219],a[168:171],v[104:107],v[216:219]
ds_read_b64_tr_b16 a[240:241],v2 offset:768
ds_read_b64_tr_b16 a[242:243],v2 offset:4864
v_mfma_f32_16x16x32_bf16 v[216:219],a[172:175],v[108:111],v[216:219]
ds_read_b64_tr_b16 a[244:245],v2 offset:8960
v_mfma_f32_16x16x32_bf16 v[220:223],a[188:191],v[108:111],v[40:43]
ds_read_b64_tr_b16 a[246:247],v2 offset:13056
v_mfma_f32_16x16x32_bf16 v[220:223],a[184:187],v[104:107],v[220:223]
ds_read_b64_tr_b16 a[252:253],v3 offset:8960
v_mfma_f32_16x16x32_bf16 v[220:223],a[180:183],v[100:103],v[220:223]
ds_read_b64_tr_b16 a[254:255],v3 offset:13056
ds_read_b64_tr_b16 a[248:249],v3 offset:768
v_mfma_f32_16x16x32_bf16 v[220:223],a[176:179],v[96:99],v[220:223]
s_waitcnt lgkmcnt(9)
ds_read_b64_tr_b16 a[250:251],v3 offset:4864
v_mfma_f32_16x16x32_bf16 a[0:3],a[192:195],v[136:139],a[0:3]
v_exp_f32 v168,v168
v_mfma_f32_16x16x32_bf16 a[0:3],a[196:199],v[140:143],a[0:3]
v_exp_f32 v169,v169
v_mfma_f32_16x16x32_bf16 a[4:7],a[204:207],v[140:143],a[4:7]
v_exp_f32 v170,v170
v_mfma_f32_16x16x32_bf16 a[4:7],a[200:203],v[136:139],a[4:7]
s_memrealtime s[76:77]
v_exp_f32 v171,v171
v_mfma_f32_16x16x32_bf16 a[8:11],a[208:211],v[136:139],a[8:11]
v_cvt_pk_bf16_f32 v168,v168,v169
v_cvt_pk_bf16_f32 v169,v170,v171
v_mfma_f32_16x16x32_bf16 a[8:11],a[212:215],v[140:143],a[8:11]
v_exp_f32 v172,v172
v_mfma_f32_16x16x32_bf16 a[12:15],a[220:223],v[140:143],a[12:15]
v_exp_f32 v173,v173
v_mfma_f32_16x16x32_bf16 a[12:15],a[216:219],v[136:139],a[12:15]
v_exp_f32 v174,v174
v_mfma_f32_16x16x32_bf16 a[16:19],a[224:227],v[136:139],a[16:19]
v_exp_f32 v175,v175
v_mfma_f32_16x16x32_bf16 a[16:19],a[228:231],v[140:143],a[16:19]
v_exp_f32 v184,v184
v_mfma_f32_16x16x32_bf16 a[20:23],a[236:239],v[140:143],a[20:23]
s_waitcnt lgkmcnt(0)
v_exp_f32 v185,v185
v_mfma_f32_16x16x32_bf16 a[20:23],a[232:235],v[136:139],a[20:23]
s_sub_u32 s79,s78,s76
v_exp_f32 v186,v186
v_mfma_f32_16x16x32_bf16 a[24:27],a[240:243],v[136:139],a[24:27]
s_add_u32 s78,s76,s60
v_exp_f32 v187,v187
v_mfma_f32_16x16x32_bf16 a[24:27],a[244:247],v[140:143],a[24:27]
s_cmp_gt_i32 s79,s95
s_cbranch_scc1 L_ps2_%=
L_pr2_%=:
v_cvt_pk_bf16_f32 v170,v172,v173
v_mfma_f32_16x16x32_bf16 a[28:31],a[252:255],v[140:143],a[28:31]
v_exp_f32 v188,v188
v_exp_f32 v189,v189
v_exp_f32 v190,v190
v_exp_f32 v191,v191
)A"
R"A(v_mfma_f32_16x16x32_bf16 a[28:31],a[248:251],v[136:139],a[28:31]
.p2align 3
v_cvt_pk_bf16_f32 v171,v174,v175
v_cvt_pk_bf16_f32 v172,v184,v185
v_cvt_pk_bf16_f32 v173,v186,v187
v_cvt_pk_bf16_f32 v174,v188,v189
v_cvt_pk_bf16_f32 v175,v190,v191
v_smfmac_f32_16x16x64_bf16 v[48:51],v[28:31],v[136:143],v27
s_mov_b32 s53,13
s_cmp_eq_u32 s40,3
s_cbranch_scc0 L_att0_%=
s_cmp_gt_i32 s18,0
s_cbranch_scc1 L_att0v0_%=
s_cmp_gt_i32 s19,0
s_cbranch_scc1 L_att0v1_%=
s_cmp_gt_i32 s100,0
s_cbranch_scc1 L_att0v2_%=
.p2align 5
L_att0v3_%=:
s_waitcnt vmcnt(8) lgkmcnt(0)
v_exp_f32 v200,v200
s_barrier
ds_read_b128 a[128:131],v1
v_add_u32 v2,s48,v2
s_mov_b32 s90,L_endseg_%=-L_pc_%=+128
v_exp_f32 v201,v201
s_add_u32 m0,s49,s51
ds_read_b128 a[132:135],v1 offset:256
v_cvt_pk_bf16_f32 v200,v200,v201
s_lshr_b32 s45,s45,1
v_exp_f32 v202,v202
ds_read_b128 a[136:139],v1 offset:512
v_add_u32 v3,s48,v3
buffer_load_dwordx4 v4,s[20:23],s47 offen nt lds
v_exp_f32 v203,v203
s_add_u32 s62,s50,32768
ds_read_b128 a[140:143],v1 offset:768
v_cvt_pk_bf16_f32 v201,v202,v203
s_and_b32 s62,s62,131071
v_exp_f32 v204,v204
s_addk_i32 s54,16384
v_exp_f32 v205,v205
s_sub_u32 s48,s62,s50
ds_read_b128 a[156:159],v1 offset:4864
v_cvt_pk_bf16_f32 v202,v204,v205
s_sub_u32 s53,s53,1
v_exp_f32 v206,v206
s_and_b32 s46,s54,0xfffc0000
v_exp_f32 v207,v207
s_mov_b32 s50,s62
ds_read_b128 a[152:155],v1 offset:4608
v_cvt_pk_bf16_f32 v203,v206,v207
s_xor_b32 s49,s50,65536
v_exp_f32 v216,v216
v_mfma_f32_16x16x32_bf16 a[32:35],a[192:195],v[168:171],a[32:35]
ds_read_b128 a[148:151],v1 offset:4352
v_mfma_f32_16x16x32_bf16 a[32:35],a[196:199],v[172:175],a[32:35]
v_exp_f32 v217,v217
v_mfma_f32_16x16x32_bf16 a[36:39],a[204:207],v[172:175],a[36:39]
ds_read_b128 a[144:147],v1 offset:4096
v_cvt_pk_bf16_f32 v204,v216,v217
v_mfma_f32_16x16x32_bf16 a[36:39],a[200:203],v[168:171],a[36:39]
v_exp_f32 v218,v218
v_mfma_f32_16x16x32_bf16 a[40:43],a[208:211],v[168:171],a[40:43]
buffer_load_dwordx4 v4,s[20:23],s47 offen offset:1024 nt lds
v_mfma_f32_16x16x32_bf16 a[40:43],a[212:215],v[172:175],a[40:43]
ds_read_b128 a[160:163],v1 offset:8192
v_mfma_f32_16x16x32_bf16 a[44:47],a[220:223],v[172:175],a[44:47]
v_exp_f32 v219,v219
v_mfma_f32_16x16x32_bf16 a[44:47],a[216:219],v[168:171],a[44:47]
ds_read_b128 a[164:167],v1 offset:8448
v_cvt_pk_bf16_f32 v205,v218,v219
v_mfma_f32_16x16x32_bf16 a[48:51],a[224:227],v[168:171],a[48:51]
v_exp_f32 v220,v220
v_mfma_f32_16x16x32_bf16 a[48:51],a[228:231],v[172:175],a[48:51]
ds_read_b128 a[168:171],v1 offset:8704
v_mfma_f32_16x16x32_bf16 a[52:55],a[236:239],v[172:175],a[52:55]
v_exp_f32 v221,v221
v_mfma_f32_16x16x32_bf16 a[52:55],a[232:235],v[168:171],a[52:55]
ds_read_b128 a[172:175],v1 offset:8960
v_cvt_pk_bf16_f32 v206,v220,v221
v_mfma_f32_16x16x32_bf16 a[56:59],a[240:243],v[168:171],a[56:59]
v_exp_f32 v222,v222
v_mfma_f32_16x16x32_bf16 a[56:59],a[244:247],v[172:175],a[56:59]
ds_read_b128 a[188:191],v1 offset:13056
v_mfma_f32_16x16x32_bf16 a[60:63],a[252:255],v[172:175],a[60:63]
v_exp_f32 v223,v223
v_mfma_f32_16x16x32_bf16 a[60:63],a[248:251],v[168:171],a[60:63]
ds_read_b128 a[184:187],v1 offset:12800
v_cvt_pk_bf16_f32 v207,v222,v223
v_smfmac_f32_16x16x64_bf16 v[52:55],v[28:31],v[168:175],v27
s_waitcnt lgkmcnt(4)
v_mfma_f32_16x16x32_bf16 v[136:139],a[128:131],v[64:67],v[32:35]
ds_read_b128 a[180:183],v1 offset:12544
v_mfma_f32_16x16x32_bf16 v[136:139],a[132:135],v[68:71],v[136:139]
buffer_load_dwordx4 v5,s[20:23],s47 offen offset:2048 nt lds
v_mfma_f32_16x16x32_bf16 v[136:139],a[136:139],v[72:75],v[136:139]
ds_read_b128 a[176:179],v1 offset:12288
v_add_u32 v1,s48,v1
v_mfma_f32_16x16x32_bf16 v[136:139],a[140:143],v[76:79],v[136:139]
v_mfma_f32_16x16x32_bf16 v[140:143],a[156:159],v[76:79],v[32:35]
v_mfma_f32_16x16x32_bf16 v[140:143],a[152:155],v[72:75],v[140:143]
v_mfma_f32_16x16x32_bf16 v[140:143],a[148:151],v[68:71],v[140:143]
v_mfma_f32_16x16x32_bf16 v[140:143],a[144:147],v[64:67],v[140:143]
v_mfma_f32_16x16x32_bf16 v[152:155],a[160:163],v[64:67],v[32:35]
v_mfma_f32_16x16x32_bf16 v[152:155],a[164:167],v[68:71],v[152:155]
s_waitcnt lgkmcnt(1)
v_mfma_f32_16x16x32_bf16 v[152:155],a[168:171],v[72:75],v[152:155]
v_mfma_f32_16x16x32_bf16 v[152:155],a[172:175],v[76:79],v[152:155]
v_mfma_f32_16x16x32_bf16 v[156:159],a[188:191],v[76:79],v[32:35]
v_mfma_f32_16x16x32_bf16 v[156:159],a[184:187],v[72:75],v[156:159]
v_mfma_f32_16x16x32_bf16 v[156:159],a[180:183],v[68:71],v[156:159]
s_waitcnt lgkmcnt(0)
v_mfma_f32_16x16x32_bf16 v[156:159],a[176:179],v[64:67],v[156:159]
buffer_load_dwordx4 v5,s[20:23],s47 offen offset:3072 nt lds
v_mfma_f32_16x16x32_bf16 a[64:67],a[192:195],v[200:203],a[64:67]
v_mfma_f32_16x16x32_bf16 a[64:67],a[196:199],v[204:207],a[64:67]
v_mfma_f32_16x16x32_bf16 a[68:71],a[204:207],v[204:207],a[68:71]
v_mfma_f32_16x16x32_bf16 a[68:71],a[200:203],v[200:203],a[68:71]
v_mfma_f32_16x16x32_bf16 a[72:75],a[208:211],v[200:203],a[72:75]
v_exp_f32 v136,v136
v_mfma_f32_16x16x32_bf16 a[72:75],a[212:215],v[204:207],a[72:75]
v_exp_f32 v137,v137
v_mfma_f32_16x16x32_bf16 a[76:79],a[220:223],v[204:207],a[76:79]
v_exp_f32 v138,v138
v_mfma_f32_16x16x32_bf16 a[76:79],a[216:219],v[200:203],a[76:79]
v_exp_f32 v139,v139
v_mfma_f32_16x16x32_bf16 a[80:83],a[224:227],v[200:203],a[80:83]
v_exp_f32 v140,v140
v_mfma_f32_16x16x32_bf16 a[80:83],a[228:231],v[204:207],a[80:83]
v_exp_f32 v141,v141
v_mfma_f32_16x16x32_bf16 a[84:87],a[236:239],v[204:207],a[84:87]
s_addk_i32 m0,16384
v_exp_f32 v142,v142
v_mfma_f32_16x16x32_bf16 a[84:87],a[232:235],v[200:203],a[84:87]
v_exp_f32 v143,v143
v_mfma_f32_16x16x32_bf16 a[88:91],a[240:243],v[200:203],a[88:91]
v_exp_f32 v152,v152
v_mfma_f32_16x16x32_bf16 a[88:91],a[244:247],v[204:207],a[88:91]
buffer_load_dwordx4 v4,s[24:27],s47 offen nt lds
v_mfma_f32_16x16x32_bf16 a[92:95],a[252:255],v[204:207],a[92:95]
v_mfma_f32_16x16x32_bf16 a[92:95],a[248:251],v[200:203],a[92:95]
v_smfmac_f32_16x16x64_bf16 v[56:59],v[28:31],v[200:207],v27
v_mfma_f32_16x16x32_bf16 v[168:171],a[128:131],v[80:83],v[36:39]
v_mfma_f32_16x16x32_bf16 v[168:171],a[132:135],v[84:87],v[168:171]
v_mfma_f32_16x16x32_bf16 v[168:171],a[136:139],v[88:91],v[168:171]
v_exp_f32 v153,v153
v_mfma_f32_16x16x32_bf16 v[168:171],a[140:143],v[92:95],v[168:171]
v_exp_f32 v154,v154
v_mfma_f32_16x16x32_bf16 v[172:175],a[156:159],v[92:95],v[36:39]
v_exp_f32 v155,v155
v_mfma_f32_16x16x32_bf16 v[172:175],a[152:155],v[88:91],v[172:175]
v_exp_f32 v156,v156
v_mfma_f32_16x16x32_bf16 v[172:175],a[148:151],v[84:87],v[172:175]
v_exp_f32 v157,v157
v_mfma_f32_16x16x32_bf16 v[172:175],a[144:147],v[80:83],v[172:175]
v_exp_f32 v158,v158
v_mfma_f32_16x16x32_bf16 v[184:187],a[160:163],v[80:83],v[36:39]
v_exp_f32 v159,v159
v_mfma_f32_16x16x32_bf16 v[184:187],a[164:167],v[84:87],v[184:187]
v_exp_f32 v168,v168
v_mfma_f32_16x16x32_bf16 v[184:187],a[168:171],v[88:91],v[184:187]
buffer_load_dwordx4 v5,s[24:27],s47 offen offset:1024 nt lds
v_mfma_f32_16x16x32_bf16 v[184:187],a[172:175],v[92:95],v[184:187]
v_exp_f32 v169,v169
v_mfma_f32_16x16x32_bf16 v[188:191],a[188:191],v[92:95],v[36:39]
v_exp_f32 v170,v170
v_mfma_f32_16x16x32_bf16 v[188:191],a[184:187],v[88:91],v[188:191]
v_exp_f32 v171,v171
v_mfma_f32_16x16x32_bf16 v[188:191],a[180:183],v[84:87],v[188:191]
v_exp_f32 v172,v172
v_mfma_f32_16x16x32_bf16 v[188:191],a[176:179],v[80:83],v[188:191]
v_exp_f32 v173,v173
ds_read_b64_tr_b16 a[192:193],v2
v_cvt_pk_bf16_f32 v168,v168,v169
ds_read_b64_tr_b16 a[194:195],v2 offset:4096
v_cvt_pk_bf16_f32 v169,v170,v171
ds_read_b64_tr_b16 a[196:197],v2 offset:8192
v_cvt_pk_bf16_f32 v170,v172,v173
v_exp_f32 v174,v174
v_exp_f32 v175,v175
ds_read_b64_tr_b16 a[198:199],v2 offset:12288
v_cvt_pk_bf16_f32 v171,v174,v175
v_exp_f32 v184,v184
v_exp_f32 v185,v185
buffer_load_dwordx4 v4,s[24:27],s47 offen offset:2048 nt lds
ds_read_b64_tr_b16 a[204:205],v3 offset:8192
v_cvt_pk_bf16_f32 v172,v184,v185
ds_read_b64_tr_b16 a[206:207],v3 offset:12288
v_cvt_pk_bf16_f32 v136,v136,v137
v_exp_f32 v186,v186
ds_read_b64_tr_b16 a[200:201],v3
ds_read_b64_tr_b16 a[202:203],v3 offset:4096
v_cvt_pk_bf16_f32 v137,v138,v139
ds_read_b64_tr_b16 a[208:209],v2 offset:256
ds_read_b64_tr_b16 a[210:211],v2 offset:4352
ds_read_b64_tr_b16 a[212:213],v2 offset:8448
v_mfma_f32_16x16x32_bf16 v[200:203],a[128:131],v[96:99],v[40:43]
ds_read_b64_tr_b16 a[214:215],v2 offset:12544
v_cvt_pk_bf16_f32 v138,v140,v141
v_mfma_f32_16x16x32_bf16 v[200:203],a[132:135],v[100:103],v[200:203]
ds_read_b64_tr_b16 a[220:221],v3 offset:8448
v_mfma_f32_16x16x32_bf16 v[200:203],a[136:139],v[104:107],v[200:203]
ds_read_b64_tr_b16 a[222:223],v3 offset:12544
v_cvt_pk_bf16_f32 v139,v142,v143
v_mfma_f32_16x16x32_bf16 v[200:203],a[140:143],v[108:111],v[200:203]
ds_read_b64_tr_b16 a[216:217],v3 offset:256
v_mfma_f32_16x16x32_bf16 v[204:207],a[156:159],v[108:111],v[40:43]
ds_read_b64_tr_b16 a[218:219],v3 offset:4352
v_mfma_f32_16x16x32_bf16 v[204:207],a[152:155],v[104:107],v[204:207]
buffer_load_dwordx4 v5,s[24:27],s47 offen offset:3072 nt lds
v_mfma_f32_16x16x32_bf16 v[204:207],a[148:151],v[100:103],v[204:207]
s_or_b32 s47,s47,0x18000
ds_read_b64_tr_b16 a[224:225],v2 offset:512
v_cvt_pk_bf16_f32 v140,v152,v153
v_mfma_f32_16x16x32_bf16 v[204:207],a[144:147],v[96:99],v[204:207]
s_addk_i32 s47,4096
s_and_b32 s47,s47,0x27000
ds_read_b64_tr_b16 a[226:227],v2 offset:4608
v_mfma_f32_16x16x32_bf16 v[216:219],a[160:163],v[96:99],v[40:43]
s_or_b32 s47,s47,s46
ds_read_b64_tr_b16 a[228:229],v2 offset:8704
v_cvt_pk_bf16_f32 v141,v154,v155
v_mfma_f32_16x16x32_bf16 v[216:219],a[164:167],v[100:103],v[216:219]
ds_read_b64_tr_b16 a[230:231],v2 offset:12800
v_mfma_f32_16x16x32_bf16 v[216:219],a[168:171],v[104:107],v[216:219]
ds_read_b64_tr_b16 a[236:237],v3 offset:8704
v_mfma_f32_16x16x32_bf16 v[216:219],a[172:175],v[108:111],v[216:219]
ds_read_b64_tr_b16 a[238:239],v3 offset:12800
v_cvt_pk_bf16_f32 v142,v156,v157
v_mfma_f32_16x16x32_bf16 v[220:223],a[188:191],v[108:111],v[40:43]
ds_read_b64_tr_b16 a[232:233],v3 offset:512
v_mfma_f32_16x16x32_bf16 v[220:223],a[184:187],v[104:107],v[220:223]
ds_read_b64_tr_b16 a[234:235],v3 offset:4608
v_mfma_f32_16x16x32_bf16 v[220:223],a[180:183],v[100:103],v[220:223]
ds_read_b64_tr_b16 a[240:241],v2 offset:768
v_cvt_pk_bf16_f32 v143,v158,v159
v_mfma_f32_16x16x32_bf16 v[220:223],a[176:179],v[96:99],v[220:223]
s_waitcnt lgkmcnt(7)
buffer_load_dword v26,v24,s[88:91],s93 offen
v_mfma_f32_16x16x32_bf16 a[0:3],a[192:195],v[136:139],a[0:3]
ds_read_b64_tr_b16 a[242:243],v2 offset:4864
v_mfma_f32_16x16x32_bf16 a[0:3],a[196:199],v[140:143],a[0:3]
ds_read_b64_tr_b16 a[244:245],v2 offset:8960
v_mfma_f32_16x16x32_bf16 a[4:7],a[204:207],v[140:143],a[4:7]
ds_read_b64_tr_b16 a[246:247],v2 offset:13056
v_mfma_f32_16x16x32_bf16 a[4:7],a[200:203],v[136:139],a[4:7]
ds_read_b64_tr_b16 a[252:253],v3 offset:8960
v_mfma_f32_16x16x32_bf16 a[8:11],a[208:211],v[136:139],a[8:11]
ds_read_b64_tr_b16 a[254:255],v3 offset:13056
v_mfma_f32_16x16x32_bf16 a[8:11],a[212:215],v[140:143],a[8:11]
ds_read_b64_tr_b16 a[248:249],v3 offset:768
v_mfma_f32_16x16x32_bf16 a[12:15],a[220:223],v[140:143],a[12:15]
ds_read_b64_tr_b16 a[250:251],v3 offset:4864
v_mfma_f32_16x16x32_bf16 a[12:15],a[216:219],v[136:139],a[12:15]
s_bitcmp1_b32 s45,0
s_cbranch_scc0 L_t31v30_%=
s_mov_b32 m0,s80
s_addk_i32 s80,4096
buffer_load_dwordx4 v25,s[20:23],s83 offen nt lds
s_addk_i32 s83,1024
L_t31v30_%=:
v_mfma_f32_16x16x32_bf16 a[16:19],a[224:227],v[136:139],a[16:19]
s_waitcnt lgkmcnt(6)
v_exp_f32 v187,v187
v_mfma_f32_16x16x32_bf16 a[16:19],a[228:231],v[140:143],a[16:19]
v_exp_f32 v188,v188
v_mfma_f32_16x16x32_bf16 a[20:23],a[236:239],v[140:143],a[20:23]
v_exp_f32 v189,v189
v_mfma_f32_16x16x32_bf16 a[20:23],a[232:235],v[136:139],a[20:23]
v_exp_f32 v190,v190
v_mfma_f32_16x16x32_bf16 a[24:27],a[240:243],v[136:139],a[24:27]
s_waitcnt lgkmcnt(2)
v_exp_f32 v191,v191
v_mfma_f32_16x16x32_bf16 a[24:27],a[244:247],v[140:143],a[24:27]
v_cvt_pk_bf16_f32 v173,v186,v187
v_mfma_f32_16x16x32_bf16 a[28:31],a[252:255],v[140:143],a[28:31]
s_waitcnt lgkmcnt(0)
v_cvt_pk_bf16_f32 v174,v188,v189
v_cvt_pk_bf16_f32 v175,v190,v191
v_mfma_f32_16x16x32_bf16 a[28:31],a[248:251],v[136:139],a[28:31]
)A"
R"A(s_cmp_lg_u32 s53,0
v_smfmac_f32_16x16x64_bf16 v[48:51],v[28:31],v[136:143],v27
s_cbranch_scc1 L_att0v3_%=
s_waitcnt lgkmcnt(0)
s_nop 7
s_branch L_att0x_%=
.p2align 5
L_att0v0_%=:
v_mfma_f32_16x16x32_bf16 v[232:235],a[128:131],v[112:115],v[44:47]
s_waitcnt vmcnt(8) lgkmcnt(0)
v_exp_f32 v200,v200
v_mfma_f32_16x16x32_bf16 v[232:235],a[132:135],v[116:119],v[232:235]
s_barrier
ds_read_b128 a[128:131],v1
v_add_u32 v2,s48,v2
v_mfma_f32_16x16x32_bf16 v[232:235],a[136:139],v[120:123],v[232:235]
s_mov_b32 s90,L_endseg_%=-L_pc_%=+128
v_exp_f32 v201,v201
v_mfma_f32_16x16x32_bf16 v[232:235],a[140:143],v[124:127],v[232:235]
s_add_u32 m0,s49,s51
ds_read_b128 a[132:135],v1 offset:256
v_cvt_pk_bf16_f32 v200,v200,v201
v_mfma_f32_16x16x32_bf16 v[236:239],a[156:159],v[124:127],v[44:47]
s_lshr_b32 s45,s45,1
v_exp_f32 v202,v202
v_mfma_f32_16x16x32_bf16 v[236:239],a[152:155],v[120:123],v[236:239]
ds_read_b128 a[136:139],v1 offset:512
v_add_u32 v3,s48,v3
v_mfma_f32_16x16x32_bf16 v[236:239],a[148:151],v[116:119],v[236:239]
buffer_load_dwordx4 v4,s[20:23],s47 offen nt lds
v_mfma_f32_16x16x32_bf16 v[236:239],a[144:147],v[112:115],v[236:239]
v_exp_f32 v203,v203
v_mfma_f32_16x16x32_bf16 v[248:251],a[160:163],v[112:115],v[44:47]
s_add_u32 s62,s50,32768
ds_read_b128 a[140:143],v1 offset:768
v_cvt_pk_bf16_f32 v201,v202,v203
v_mfma_f32_16x16x32_bf16 v[248:251],a[164:167],v[116:119],v[248:251]
s_and_b32 s62,s62,131071
v_exp_f32 v204,v204
v_mfma_f32_16x16x32_bf16 v[248:251],a[168:171],v[120:123],v[248:251]
s_addk_i32 s54,16384
v_exp_f32 v205,v205
v_mfma_f32_16x16x32_bf16 v[248:251],a[172:175],v[124:127],v[248:251]
s_sub_u32 s48,s62,s50
ds_read_b128 a[156:159],v1 offset:4864
v_cvt_pk_bf16_f32 v202,v204,v205
v_mfma_f32_16x16x32_bf16 v[252:255],a[188:191],v[124:127],v[44:47]
s_sub_u32 s53,s53,1
v_exp_f32 v206,v206
v_mfma_f32_16x16x32_bf16 v[252:255],a[184:187],v[120:123],v[252:255]
s_and_b32 s46,s54,0xfffc0000
v_exp_f32 v207,v207
v_mfma_f32_16x16x32_bf16 v[252:255],a[180:183],v[116:119],v[252:255]
s_mov_b32 s50,s62
ds_read_b128 a[152:155],v1 offset:4608
v_cvt_pk_bf16_f32 v203,v206,v207
v_mfma_f32_16x16x32_bf16 v[252:255],a[176:179],v[112:115],v[252:255]
s_xor_b32 s49,s50,65536
v_exp_f32 v216,v216
v_mfma_f32_16x16x32_bf16 a[32:35],a[192:195],v[168:171],a[32:35]
ds_read_b128 a[148:151],v1 offset:4352
v_mfma_f32_16x16x32_bf16 a[32:35],a[196:199],v[172:175],a[32:35]
v_exp_f32 v217,v217
v_mfma_f32_16x16x32_bf16 a[36:39],a[204:207],v[172:175],a[36:39]
ds_read_b128 a[144:147],v1 offset:4096
v_cvt_pk_bf16_f32 v204,v216,v217
v_mfma_f32_16x16x32_bf16 a[36:39],a[200:203],v[168:171],a[36:39]
v_exp_f32 v218,v218
v_mfma_f32_16x16x32_bf16 a[40:43],a[208:211],v[168:171],a[40:43]
buffer_load_dwordx4 v4,s[20:23],s47 offen offset:1024 nt lds
v_mfma_f32_16x16x32_bf16 a[40:43],a[212:215],v[172:175],a[40:43]
ds_read_b128 a[160:163],v1 offset:8192
v_mfma_f32_16x16x32_bf16 a[44:47],a[220:223],v[172:175],a[44:47]
v_exp_f32 v219,v219
v_mfma_f32_16x16x32_bf16 a[44:47],a[216:219],v[168:171],a[44:47]
ds_read_b128 a[164:167],v1 offset:8448
v_cvt_pk_bf16_f32 v205,v218,v219
v_mfma_f32_16x16x32_bf16 a[48:51],a[224:227],v[168:171],a[48:51]
v_exp_f32 v220,v220
v_mfma_f32_16x16x32_bf16 a[48:51],a[228:231],v[172:175],a[48:51]
ds_read_b128 a[168:171],v1 offset:8704
v_mfma_f32_16x16x32_bf16 a[52:55],a[236:239],v[172:175],a[52:55]
v_exp_f32 v221,v221
v_mfma_f32_16x16x32_bf16 a[52:55],a[232:235],v[168:171],a[52:55]
ds_read_b128 a[172:175],v1 offset:8960
v_cvt_pk_bf16_f32 v206,v220,v221
v_mfma_f32_16x16x32_bf16 a[56:59],a[240:243],v[168:171],a[56:59]
v_exp_f32 v222,v222
v_mfma_f32_16x16x32_bf16 a[56:59],a[244:247],v[172:175],a[56:59]
ds_read_b128 a[188:191],v1 offset:13056
v_mfma_f32_16x16x32_bf16 a[60:63],a[252:255],v[172:175],a[60:63]
v_exp_f32 v223,v223
v_mfma_f32_16x16x32_bf16 a[60:63],a[248:251],v[168:171],a[60:63]
ds_read_b128 a[184:187],v1 offset:12800
v_cvt_pk_bf16_f32 v207,v222,v223
v_smfmac_f32_16x16x64_bf16 v[52:55],v[28:31],v[168:175],v27
ds_read_b128 a[180:183],v1 offset:12544
buffer_load_dwordx4 v5,s[20:23],s47 offen offset:2048 nt lds
ds_read_b128 a[176:179],v1 offset:12288
v_add_u32 v1,s48,v1
v_exp_f32 v232,v232
v_exp_f32 v233,v233
v_exp_f32 v234,v234
v_exp_f32 v235,v235
v_exp_f32 v236,v236
v_exp_f32 v237,v237
v_exp_f32 v238,v238
v_exp_f32 v239,v239
v_exp_f32 v248,v248
v_exp_f32 v249,v249
v_exp_f32 v250,v250
v_exp_f32 v251,v251
buffer_load_dwordx4 v5,s[20:23],s47 offen offset:3072 nt lds
v_mfma_f32_16x16x32_bf16 a[64:67],a[192:195],v[200:203],a[64:67]
v_exp_f32 v252,v252
v_mfma_f32_16x16x32_bf16 a[64:67],a[196:199],v[204:207],a[64:67]
v_exp_f32 v253,v253
v_mfma_f32_16x16x32_bf16 a[68:71],a[204:207],v[204:207],a[68:71]
v_exp_f32 v254,v254
v_mfma_f32_16x16x32_bf16 a[68:71],a[200:203],v[200:203],a[68:71]
v_exp_f32 v255,v255
v_mfma_f32_16x16x32_bf16 a[72:75],a[208:211],v[200:203],a[72:75]
v_mfma_f32_16x16x32_bf16 a[72:75],a[212:215],v[204:207],a[72:75]
v_mfma_f32_16x16x32_bf16 a[76:79],a[220:223],v[204:207],a[76:79]
v_mfma_f32_16x16x32_bf16 a[76:79],a[216:219],v[200:203],a[76:79]
v_mfma_f32_16x16x32_bf16 a[80:83],a[224:227],v[200:203],a[80:83]
v_mfma_f32_16x16x32_bf16 a[80:83],a[228:231],v[204:207],a[80:83]
v_mfma_f32_16x16x32_bf16 a[84:87],a[236:239],v[204:207],a[84:87]
s_addk_i32 m0,16384
v_mfma_f32_16x16x32_bf16 a[84:87],a[232:235],v[200:203],a[84:87]
v_mfma_f32_16x16x32_bf16 a[88:91],a[240:243],v[200:203],a[88:91]
v_mfma_f32_16x16x32_bf16 a[88:91],a[244:247],v[204:207],a[88:91]
buffer_load_dwordx4 v4,s[24:27],s47 offen nt lds
v_mfma_f32_16x16x32_bf16 a[92:95],a[252:255],v[204:207],a[92:95]
v_cvt_pk_bf16_f32 v232,v232,v233
v_cvt_pk_bf16_f32 v233,v234,v235
v_mfma_f32_16x16x32_bf16 a[92:95],a[248:251],v[200:203],a[92:95]
v_cvt_pk_bf16_f32 v234,v236,v237
v_cvt_pk_bf16_f32 v235,v238,v239
v_smfmac_f32_16x16x64_bf16 v[56:59],v[28:31],v[200:207],v27
s_waitcnt lgkmcnt(0)
v_cvt_pk_bf16_f32 v236,v248,v249
v_mfma_f32_16x16x32_bf16 v[168:171],a[128:131],v[80:83],v[36:39]
v_cvt_pk_bf16_f32 v237,v250,v251
v_cvt_pk_bf16_f32 v238,v252,v253
v_mfma_f32_16x16x32_bf16 v[168:171],a[132:135],v[84:87],v[168:171]
v_cvt_pk_bf16_f32 v239,v254,v255
v_mfma_f32_16x16x32_bf16 v[168:171],a[136:139],v[88:91],v[168:171]
v_mfma_f32_16x16x32_bf16 v[168:171],a[140:143],v[92:95],v[168:171]
v_mfma_f32_16x16x32_bf16 v[172:175],a[156:159],v[92:95],v[36:39]
v_mfma_f32_16x16x32_bf16 v[172:175],a[152:155],v[88:91],v[172:175]
v_mfma_f32_16x16x32_bf16 v[172:175],a[148:151],v[84:87],v[172:175]
v_mfma_f32_16x16x32_bf16 v[172:175],a[144:147],v[80:83],v[172:175]
v_mfma_f32_16x16x32_bf16 v[184:187],a[160:163],v[80:83],v[36:39]
v_mfma_f32_16x16x32_bf16 v[184:187],a[164:167],v[84:87],v[184:187]
s_nop 1
v_exp_f32 v168,v168
v_mfma_f32_16x16x32_bf16 v[184:187],a[168:171],v[88:91],v[184:187]
buffer_load_dwordx4 v5,s[24:27],s47 offen offset:1024 nt lds
v_mfma_f32_16x16x32_bf16 v[184:187],a[172:175],v[92:95],v[184:187]
v_exp_f32 v169,v169
v_mfma_f32_16x16x32_bf16 v[188:191],a[188:191],v[92:95],v[36:39]
v_exp_f32 v170,v170
v_mfma_f32_16x16x32_bf16 v[188:191],a[184:187],v[88:91],v[188:191]
v_exp_f32 v171,v171
v_mfma_f32_16x16x32_bf16 v[188:191],a[180:183],v[84:87],v[188:191]
v_exp_f32 v172,v172
v_mfma_f32_16x16x32_bf16 v[188:191],a[176:179],v[80:83],v[188:191]
v_exp_f32 v173,v173
v_mfma_f32_16x16x32_bf16 a[96:99],a[192:195],v[232:235],a[96:99]
ds_read_b64_tr_b16 a[192:193],v2
v_cvt_pk_bf16_f32 v168,v168,v169
v_mfma_f32_16x16x32_bf16 a[96:99],a[196:199],v[236:239],a[96:99]
ds_read_b64_tr_b16 a[194:195],v2 offset:4096
v_cvt_pk_bf16_f32 v169,v170,v171
v_mfma_f32_16x16x32_bf16 a[100:103],a[204:207],v[236:239],a[100:103]
ds_read_b64_tr_b16 a[196:197],v2 offset:8192
v_cvt_pk_bf16_f32 v170,v172,v173
v_mfma_f32_16x16x32_bf16 a[100:103],a[200:203],v[232:235],a[100:103]
v_exp_f32 v174,v174
v_mfma_f32_16x16x32_bf16 a[104:107],a[208:211],v[232:235],a[104:107]
v_exp_f32 v175,v175
v_mfma_f32_16x16x32_bf16 a[104:107],a[212:215],v[236:239],a[104:107]
ds_read_b64_tr_b16 a[198:199],v2 offset:12288
v_cvt_pk_bf16_f32 v171,v174,v175
v_mfma_f32_16x16x32_bf16 a[108:111],a[220:223],v[236:239],a[108:111]
v_exp_f32 v184,v184
v_mfma_f32_16x16x32_bf16 a[108:111],a[216:219],v[232:235],a[108:111]
v_exp_f32 v185,v185
v_mfma_f32_16x16x32_bf16 a[112:115],a[224:227],v[232:235],a[112:115]
buffer_load_dwordx4 v4,s[24:27],s47 offen offset:2048 nt lds
v_mfma_f32_16x16x32_bf16 a[112:115],a[228:231],v[236:239],a[112:115]
ds_read_b64_tr_b16 a[204:205],v3 offset:8192
v_cvt_pk_bf16_f32 v172,v184,v185
v_mfma_f32_16x16x32_bf16 a[116:119],a[236:239],v[236:239],a[116:119]
ds_read_b64_tr_b16 a[206:207],v3 offset:12288
v_mfma_f32_16x16x32_bf16 a[116:119],a[232:235],v[232:235],a[116:119]
v_exp_f32 v186,v186
v_mfma_f32_16x16x32_bf16 a[120:123],a[240:243],v[232:235],a[120:123]
ds_read_b64_tr_b16 a[200:201],v3
v_mfma_f32_16x16x32_bf16 a[120:123],a[244:247],v[236:239],a[120:123]
ds_read_b64_tr_b16 a[202:203],v3 offset:4096
v_mfma_f32_16x16x32_bf16 a[124:127],a[252:255],v[236:239],a[124:127]
ds_read_b64_tr_b16 a[208:209],v2 offset:256
v_mfma_f32_16x16x32_bf16 a[124:127],a[248:251],v[232:235],a[124:127]
ds_read_b64_tr_b16 a[210:211],v2 offset:4352
v_smfmac_f32_16x16x64_bf16 v[60:63],v[28:31],v[232:239],v27
ds_read_b64_tr_b16 a[212:213],v2 offset:8448
v_mfma_f32_16x16x32_bf16 v[200:203],a[128:131],v[96:99],v[40:43]
ds_read_b64_tr_b16 a[214:215],v2 offset:12544
v_mfma_f32_16x16x32_bf16 v[200:203],a[132:135],v[100:103],v[200:203]
ds_read_b64_tr_b16 a[220:221],v3 offset:8448
v_mfma_f32_16x16x32_bf16 v[200:203],a[136:139],v[104:107],v[200:203]
ds_read_b64_tr_b16 a[222:223],v3 offset:12544
v_mfma_f32_16x16x32_bf16 v[200:203],a[140:143],v[108:111],v[200:203]
ds_read_b64_tr_b16 a[216:217],v3 offset:256
v_mfma_f32_16x16x32_bf16 v[204:207],a[156:159],v[108:111],v[40:43]
ds_read_b64_tr_b16 a[218:219],v3 offset:4352
v_mfma_f32_16x16x32_bf16 v[204:207],a[152:155],v[104:107],v[204:207]
buffer_load_dwordx4 v5,s[24:27],s47 offen offset:3072 nt lds
v_mfma_f32_16x16x32_bf16 v[204:207],a[148:151],v[100:103],v[204:207]
s_or_b32 s47,s47,0x18000
ds_read_b64_tr_b16 a[224:225],v2 offset:512
v_mfma_f32_16x16x32_bf16 v[204:207],a[144:147],v[96:99],v[204:207]
s_addk_i32 s47,4096
s_and_b32 s47,s47,0x27000
ds_read_b64_tr_b16 a[226:227],v2 offset:4608
v_mfma_f32_16x16x32_bf16 v[216:219],a[160:163],v[96:99],v[40:43]
s_or_b32 s47,s47,s46
ds_read_b64_tr_b16 a[228:229],v2 offset:8704
v_mfma_f32_16x16x32_bf16 v[216:219],a[164:167],v[100:103],v[216:219]
ds_read_b64_tr_b16 a[230:231],v2 offset:12800
v_mfma_f32_16x16x32_bf16 v[216:219],a[168:171],v[104:107],v[216:219]
ds_read_b64_tr_b16 a[236:237],v3 offset:8704
v_mfma_f32_16x16x32_bf16 v[216:219],a[172:175],v[108:111],v[216:219]
ds_read_b64_tr_b16 a[238:239],v3 offset:12800
v_mfma_f32_16x16x32_bf16 v[220:223],a[188:191],v[108:111],v[40:43]
ds_read_b64_tr_b16 a[232:233],v3 offset:512
v_mfma_f32_16x16x32_bf16 v[220:223],a[184:187],v[104:107],v[220:223]
ds_read_b64_tr_b16 a[234:235],v3 offset:4608
v_mfma_f32_16x16x32_bf16 v[220:223],a[180:183],v[100:103],v[220:223]
ds_read_b64_tr_b16 a[240:241],v2 offset:768
v_mfma_f32_16x16x32_bf16 v[220:223],a[176:179],v[96:99],v[220:223]
buffer_load_dword v26,v24,s[88:91],s93 offen
ds_read_b64_tr_b16 a[242:243],v2 offset:4864
ds_read_b64_tr_b16 a[244:245],v2 offset:8960
ds_read_b64_tr_b16 a[246:247],v2 offset:13056
ds_read_b64_tr_b16 a[252:253],v3 offset:8960
ds_read_b64_tr_b16 a[254:255],v3 offset:13056
ds_read_b64_tr_b16 a[248:249],v3 offset:768
ds_read_b64_tr_b16 a[250:251],v3 offset:4864
s_bitcmp1_b32 s45,0
s_cbranch_scc0 L_t31v00_%=
s_mov_b32 m0,s80
s_addk_i32 s80,4096
buffer_load_dwordx4 v25,s[20:23],s83 offen nt lds
s_addk_i32 s83,1024
L_t31v00_%=:
v_exp_f32 v187,v187
v_exp_f32 v188,v188
v_exp_f32 v189,v189
v_exp_f32 v190,v190
v_exp_f32 v191,v191
.p2align 3
v_cvt_pk_bf16_f32 v173,v186,v187
v_cvt_pk_bf16_f32 v174,v188,v189
v_cvt_pk_bf16_f32 v175,v190,v191
s_cmp_lg_u32 s53,0
s_cbranch_scc1 L_att0v0_%=
s_waitcnt lgkmcnt(0)
s_nop 7
s_branch L_att0x_%=
.p2align 5
L_att0v1_%=:
v_mfma_f32_16x16x32_bf16 v[232:235],a[128:131],v[112:115],v[44:47]
s_waitcnt vmcnt(8) lgkmcnt(0)
v_exp_f32 v200,v200
v_mfma_f32_16x16x32_bf16 v[232:235],a[132:135],v[116:119],v[232:235]
)A"
R"A(s_barrier
ds_read_b128 a[128:131],v1
v_add_u32 v2,s48,v2
v_mfma_f32_16x16x32_bf16 v[232:235],a[136:139],v[120:123],v[232:235]
s_mov_b32 s90,L_endseg_%=-L_pc_%=+128
v_exp_f32 v201,v201
v_mfma_f32_16x16x32_bf16 v[232:235],a[140:143],v[124:127],v[232:235]
s_add_u32 m0,s49,s51
ds_read_b128 a[132:135],v1 offset:256
v_cvt_pk_bf16_f32 v200,v200,v201
v_mfma_f32_16x16x32_bf16 v[236:239],a[156:159],v[124:127],v[44:47]
s_lshr_b32 s45,s45,1
v_exp_f32 v202,v202
v_mfma_f32_16x16x32_bf16 v[236:239],a[152:155],v[120:123],v[236:239]
ds_read_b128 a[136:139],v1 offset:512
v_add_u32 v3,s48,v3
v_mfma_f32_16x16x32_bf16 v[236:239],a[148:151],v[116:119],v[236:239]
buffer_load_dwordx4 v4,s[20:23],s47 offen nt lds
v_mfma_f32_16x16x32_bf16 v[236:239],a[144:147],v[112:115],v[236:239]
v_exp_f32 v203,v203
v_mfma_f32_16x16x32_bf16 v[248:251],a[160:163],v[112:115],v[44:47]
s_add_u32 s62,s50,32768
ds_read_b128 a[140:143],v1 offset:768
v_cvt_pk_bf16_f32 v201,v202,v203
v_mfma_f32_16x16x32_bf16 v[248:251],a[164:167],v[116:119],v[248:251]
s_and_b32 s62,s62,131071
v_exp_f32 v204,v204
v_mfma_f32_16x16x32_bf16 v[248:251],a[168:171],v[120:123],v[248:251]
s_addk_i32 s54,16384
v_exp_f32 v205,v205
v_mfma_f32_16x16x32_bf16 v[248:251],a[172:175],v[124:127],v[248:251]
s_sub_u32 s48,s62,s50
ds_read_b128 a[156:159],v1 offset:4864
v_cvt_pk_bf16_f32 v202,v204,v205
v_mfma_f32_16x16x32_bf16 v[252:255],a[188:191],v[124:127],v[44:47]
s_sub_u32 s53,s53,1
v_exp_f32 v206,v206
v_mfma_f32_16x16x32_bf16 v[252:255],a[184:187],v[120:123],v[252:255]
s_and_b32 s46,s54,0xfffc0000
v_exp_f32 v207,v207
v_mfma_f32_16x16x32_bf16 v[252:255],a[180:183],v[116:119],v[252:255]
s_mov_b32 s50,s62
ds_read_b128 a[152:155],v1 offset:4608
v_cvt_pk_bf16_f32 v203,v206,v207
v_mfma_f32_16x16x32_bf16 v[252:255],a[176:179],v[112:115],v[252:255]
s_xor_b32 s49,s50,65536
v_exp_f32 v216,v216
ds_read_b128 a[148:151],v1 offset:4352
v_exp_f32 v217,v217
ds_read_b128 a[144:147],v1 offset:4096
v_cvt_pk_bf16_f32 v204,v216,v217
v_exp_f32 v218,v218
buffer_load_dwordx4 v4,s[20:23],s47 offen offset:1024 nt lds
ds_read_b128 a[160:163],v1 offset:8192
v_exp_f32 v219,v219
ds_read_b128 a[164:167],v1 offset:8448
v_cvt_pk_bf16_f32 v205,v218,v219
v_exp_f32 v220,v220
ds_read_b128 a[168:171],v1 offset:8704
v_exp_f32 v221,v221
ds_read_b128 a[172:175],v1 offset:8960
v_cvt_pk_bf16_f32 v206,v220,v221
v_exp_f32 v222,v222
ds_read_b128 a[188:191],v1 offset:13056
v_exp_f32 v223,v223
ds_read_b128 a[184:187],v1 offset:12800
v_cvt_pk_bf16_f32 v207,v222,v223
s_waitcnt lgkmcnt(11)
v_mfma_f32_16x16x32_bf16 v[136:139],a[128:131],v[64:67],v[32:35]
ds_read_b128 a[180:183],v1 offset:12544
v_mfma_f32_16x16x32_bf16 v[136:139],a[132:135],v[68:71],v[136:139]
buffer_load_dwordx4 v5,s[20:23],s47 offen offset:2048 nt lds
v_mfma_f32_16x16x32_bf16 v[136:139],a[136:139],v[72:75],v[136:139]
s_waitcnt lgkmcnt(11)
ds_read_b128 a[176:179],v1 offset:12288
v_add_u32 v1,s48,v1
v_mfma_f32_16x16x32_bf16 v[136:139],a[140:143],v[76:79],v[136:139]
s_waitcnt lgkmcnt(11)
v_exp_f32 v232,v232
v_mfma_f32_16x16x32_bf16 v[140:143],a[156:159],v[76:79],v[32:35]
s_waitcnt lgkmcnt(10)
v_exp_f32 v233,v233
v_mfma_f32_16x16x32_bf16 v[140:143],a[152:155],v[72:75],v[140:143]
s_waitcnt lgkmcnt(9)
v_exp_f32 v234,v234
v_mfma_f32_16x16x32_bf16 v[140:143],a[148:151],v[68:71],v[140:143]
s_waitcnt lgkmcnt(8)
v_exp_f32 v235,v235
v_mfma_f32_16x16x32_bf16 v[140:143],a[144:147],v[64:67],v[140:143]
s_waitcnt lgkmcnt(2)
v_exp_f32 v236,v236
v_mfma_f32_16x16x32_bf16 v[152:155],a[160:163],v[64:67],v[32:35]
v_exp_f32 v237,v237
v_mfma_f32_16x16x32_bf16 v[152:155],a[164:167],v[68:71],v[152:155]
v_exp_f32 v238,v238
v_mfma_f32_16x16x32_bf16 v[152:155],a[168:171],v[72:75],v[152:155]
v_exp_f32 v239,v239
v_mfma_f32_16x16x32_bf16 v[152:155],a[172:175],v[76:79],v[152:155]
v_exp_f32 v248,v248
v_mfma_f32_16x16x32_bf16 v[156:159],a[188:191],v[76:79],v[32:35]
v_exp_f32 v249,v249
v_mfma_f32_16x16x32_bf16 v[156:159],a[184:187],v[72:75],v[156:159]
s_waitcnt lgkmcnt(0)
v_exp_f32 v250,v250
v_mfma_f32_16x16x32_bf16 v[156:159],a[180:183],v[68:71],v[156:159]
v_exp_f32 v251,v251
v_mfma_f32_16x16x32_bf16 v[156:159],a[176:179],v[64:67],v[156:159]
buffer_load_dwordx4 v5,s[20:23],s47 offen offset:3072 nt lds
v_mfma_f32_16x16x32_bf16 a[64:67],a[192:195],v[200:203],a[64:67]
v_exp_f32 v252,v252
v_mfma_f32_16x16x32_bf16 a[64:67],a[196:199],v[204:207],a[64:67]
v_exp_f32 v253,v253
v_mfma_f32_16x16x32_bf16 a[68:71],a[204:207],v[204:207],a[68:71]
v_exp_f32 v254,v254
v_mfma_f32_16x16x32_bf16 a[68:71],a[200:203],v[200:203],a[68:71]
v_exp_f32 v255,v255
v_mfma_f32_16x16x32_bf16 a[72:75],a[208:211],v[200:203],a[72:75]
v_exp_f32 v136,v136
v_mfma_f32_16x16x32_bf16 a[72:75],a[212:215],v[204:207],a[72:75]
v_exp_f32 v137,v137
v_mfma_f32_16x16x32_bf16 a[76:79],a[220:223],v[204:207],a[76:79]
v_exp_f32 v138,v138
v_mfma_f32_16x16x32_bf16 a[76:79],a[216:219],v[200:203],a[76:79]
v_exp_f32 v139,v139
v_mfma_f32_16x16x32_bf16 a[80:83],a[224:227],v[200:203],a[80:83]
v_exp_f32 v140,v140
v_mfma_f32_16x16x32_bf16 a[80:83],a[228:231],v[204:207],a[80:83]
v_exp_f32 v141,v141
v_mfma_f32_16x16x32_bf16 a[84:87],a[236:239],v[204:207],a[84:87]
s_addk_i32 m0,16384
v_exp_f32 v142,v142
v_mfma_f32_16x16x32_bf16 a[84:87],a[232:235],v[200:203],a[84:87]
v_exp_f32 v143,v143
v_mfma_f32_16x16x32_bf16 a[88:91],a[240:243],v[200:203],a[88:91]
v_exp_f32 v152,v152
v_mfma_f32_16x16x32_bf16 a[88:91],a[244:247],v[204:207],a[88:91]
buffer_load_dwordx4 v4,s[24:27],s47 offen nt lds
v_mfma_f32_16x16x32_bf16 a[92:95],a[252:255],v[204:207],a[92:95]
v_cvt_pk_bf16_f32 v232,v232,v233
v_cvt_pk_bf16_f32 v233,v234,v235
v_mfma_f32_16x16x32_bf16 a[92:95],a[248:251],v[200:203],a[92:95]
v_cvt_pk_bf16_f32 v234,v236,v237
v_cvt_pk_bf16_f32 v235,v238,v239
v_smfmac_f32_16x16x64_bf16 v[56:59],v[28:31],v[200:207],v27
.p2align 3
v_cvt_pk_bf16_f32 v236,v248,v249
v_cvt_pk_bf16_f32 v237,v250,v251
v_cvt_pk_bf16_f32 v238,v252,v253
v_cvt_pk_bf16_f32 v239,v254,v255
v_exp_f32 v153,v153
v_exp_f32 v154,v154
v_exp_f32 v155,v155
v_exp_f32 v156,v156
v_exp_f32 v157,v157
v_exp_f32 v158,v158
v_exp_f32 v159,v159
buffer_load_dwordx4 v5,s[24:27],s47 offen offset:1024 nt lds
v_mfma_f32_16x16x32_bf16 a[96:99],a[192:195],v[232:235],a[96:99]
ds_read_b64_tr_b16 a[192:193],v2
v_mfma_f32_16x16x32_bf16 a[96:99],a[196:199],v[236:239],a[96:99]
ds_read_b64_tr_b16 a[194:195],v2 offset:4096
v_mfma_f32_16x16x32_bf16 a[100:103],a[204:207],v[236:239],a[100:103]
ds_read_b64_tr_b16 a[196:197],v2 offset:8192
v_mfma_f32_16x16x32_bf16 a[100:103],a[200:203],v[232:235],a[100:103]
v_mfma_f32_16x16x32_bf16 a[104:107],a[208:211],v[232:235],a[104:107]
v_mfma_f32_16x16x32_bf16 a[104:107],a[212:215],v[236:239],a[104:107]
ds_read_b64_tr_b16 a[198:199],v2 offset:12288
v_mfma_f32_16x16x32_bf16 a[108:111],a[220:223],v[236:239],a[108:111]
v_mfma_f32_16x16x32_bf16 a[108:111],a[216:219],v[232:235],a[108:111]
v_mfma_f32_16x16x32_bf16 a[112:115],a[224:227],v[232:235],a[112:115]
buffer_load_dwordx4 v4,s[24:27],s47 offen offset:2048 nt lds
v_mfma_f32_16x16x32_bf16 a[112:115],a[228:231],v[236:239],a[112:115]
ds_read_b64_tr_b16 a[204:205],v3 offset:8192
v_mfma_f32_16x16x32_bf16 a[116:119],a[236:239],v[236:239],a[116:119]
ds_read_b64_tr_b16 a[206:207],v3 offset:12288
v_cvt_pk_bf16_f32 v136,v136,v137
v_mfma_f32_16x16x32_bf16 a[116:119],a[232:235],v[232:235],a[116:119]
v_mfma_f32_16x16x32_bf16 a[120:123],a[240:243],v[232:235],a[120:123]
ds_read_b64_tr_b16 a[200:201],v3
v_mfma_f32_16x16x32_bf16 a[120:123],a[244:247],v[236:239],a[120:123]
ds_read_b64_tr_b16 a[202:203],v3 offset:4096
v_cvt_pk_bf16_f32 v137,v138,v139
v_mfma_f32_16x16x32_bf16 a[124:127],a[252:255],v[236:239],a[124:127]
ds_read_b64_tr_b16 a[208:209],v2 offset:256
v_mfma_f32_16x16x32_bf16 a[124:127],a[248:251],v[232:235],a[124:127]
ds_read_b64_tr_b16 a[210:211],v2 offset:4352
v_smfmac_f32_16x16x64_bf16 v[60:63],v[28:31],v[232:239],v27
ds_read_b64_tr_b16 a[212:213],v2 offset:8448
v_mfma_f32_16x16x32_bf16 v[200:203],a[128:131],v[96:99],v[40:43]
ds_read_b64_tr_b16 a[214:215],v2 offset:12544
v_cvt_pk_bf16_f32 v138,v140,v141
v_mfma_f32_16x16x32_bf16 v[200:203],a[132:135],v[100:103],v[200:203]
ds_read_b64_tr_b16 a[220:221],v3 offset:8448
v_mfma_f32_16x16x32_bf16 v[200:203],a[136:139],v[104:107],v[200:203]
ds_read_b64_tr_b16 a[222:223],v3 offset:12544
v_cvt_pk_bf16_f32 v139,v142,v143
v_mfma_f32_16x16x32_bf16 v[200:203],a[140:143],v[108:111],v[200:203]
ds_read_b64_tr_b16 a[216:217],v3 offset:256
v_mfma_f32_16x16x32_bf16 v[204:207],a[156:159],v[108:111],v[40:43]
ds_read_b64_tr_b16 a[218:219],v3 offset:4352
v_mfma_f32_16x16x32_bf16 v[204:207],a[152:155],v[104:107],v[204:207]
buffer_load_dwordx4 v5,s[24:27],s47 offen offset:3072 nt lds
v_mfma_f32_16x16x32_bf16 v[204:207],a[148:151],v[100:103],v[204:207]
s_or_b32 s47,s47,0x18000
ds_read_b64_tr_b16 a[224:225],v2 offset:512
v_cvt_pk_bf16_f32 v140,v152,v153
v_mfma_f32_16x16x32_bf16 v[204:207],a[144:147],v[96:99],v[204:207]
s_addk_i32 s47,4096
s_and_b32 s47,s47,0x27000
ds_read_b64_tr_b16 a[226:227],v2 offset:4608
v_mfma_f32_16x16x32_bf16 v[216:219],a[160:163],v[96:99],v[40:43]
s_or_b32 s47,s47,s46
ds_read_b64_tr_b16 a[228:229],v2 offset:8704
v_cvt_pk_bf16_f32 v141,v154,v155
v_mfma_f32_16x16x32_bf16 v[216:219],a[164:167],v[100:103],v[216:219]
ds_read_b64_tr_b16 a[230:231],v2 offset:12800
v_mfma_f32_16x16x32_bf16 v[216:219],a[168:171],v[104:107],v[216:219]
ds_read_b64_tr_b16 a[236:237],v3 offset:8704
v_mfma_f32_16x16x32_bf16 v[216:219],a[172:175],v[108:111],v[216:219]
ds_read_b64_tr_b16 a[238:239],v3 offset:12800
v_cvt_pk_bf16_f32 v142,v156,v157
v_mfma_f32_16x16x32_bf16 v[220:223],a[188:191],v[108:111],v[40:43]
ds_read_b64_tr_b16 a[232:233],v3 offset:512
v_mfma_f32_16x16x32_bf16 v[220:223],a[184:187],v[104:107],v[220:223]
ds_read_b64_tr_b16 a[234:235],v3 offset:4608
v_mfma_f32_16x16x32_bf16 v[220:223],a[180:183],v[100:103],v[220:223]
ds_read_b64_tr_b16 a[240:241],v2 offset:768
v_cvt_pk_bf16_f32 v143,v158,v159
v_mfma_f32_16x16x32_bf16 v[220:223],a[176:179],v[96:99],v[220:223]
s_waitcnt lgkmcnt(7)
buffer_load_dword v26,v24,s[88:91],s93 offen
v_mfma_f32_16x16x32_bf16 a[0:3],a[192:195],v[136:139],a[0:3]
ds_read_b64_tr_b16 a[242:243],v2 offset:4864
v_mfma_f32_16x16x32_bf16 a[0:3],a[196:199],v[140:143],a[0:3]
ds_read_b64_tr_b16 a[244:245],v2 offset:8960
v_mfma_f32_16x16x32_bf16 a[4:7],a[204:207],v[140:143],a[4:7]
ds_read_b64_tr_b16 a[246:247],v2 offset:13056
v_mfma_f32_16x16x32_bf16 a[4:7],a[200:203],v[136:139],a[4:7]
ds_read_b64_tr_b16 a[252:253],v3 offset:8960
v_mfma_f32_16x16x32_bf16 a[8:11],a[208:211],v[136:139],a[8:11]
ds_read_b64_tr_b16 a[254:255],v3 offset:13056
v_mfma_f32_16x16x32_bf16 a[8:11],a[212:215],v[140:143],a[8:11]
ds_read_b64_tr_b16 a[248:249],v3 offset:768
v_mfma_f32_16x16x32_bf16 a[12:15],a[220:223],v[140:143],a[12:15]
ds_read_b64_tr_b16 a[250:251],v3 offset:4864
v_mfma_f32_16x16x32_bf16 a[12:15],a[216:219],v[136:139],a[12:15]
s_bitcmp1_b32 s45,0
s_cbranch_scc0 L_t31v10_%=
s_mov_b32 m0,s80
s_addk_i32 s80,4096
buffer_load_dwordx4 v25,s[20:23],s83 offen nt lds
s_addk_i32 s83,1024
L_t31v10_%=:
v_mfma_f32_16x16x32_bf16 a[16:19],a[224:227],v[136:139],a[16:19]
s_waitcnt lgkmcnt(6)
v_mfma_f32_16x16x32_bf16 a[16:19],a[228:231],v[140:143],a[16:19]
v_mfma_f32_16x16x32_bf16 a[20:23],a[236:239],v[140:143],a[20:23]
v_mfma_f32_16x16x32_bf16 a[20:23],a[232:235],v[136:139],a[20:23]
v_mfma_f32_16x16x32_bf16 a[24:27],a[240:243],v[136:139],a[24:27]
s_waitcnt lgkmcnt(2)
v_mfma_f32_16x16x32_bf16 a[24:27],a[244:247],v[140:143],a[24:27]
v_mfma_f32_16x16x32_bf16 a[28:31],a[252:255],v[140:143],a[28:31]
s_waitcnt lgkmcnt(0)
v_mfma_f32_16x16x32_bf16 a[28:31],a[248:251],v[136:139],a[28:31]
s_cmp_lg_u32 s53,0
v_smfmac_f32_16x16x64_bf16 v[48:51],v[28:31],v[136:143],v27
s_cbranch_scc1 L_att0v1_%=
s_waitcnt lgkmcnt(0)
s_nop 7
s_branch L_att0x_%=
.p2align 5
L_att0v2_%=:
v_mfma_f32_16x16x32_bf16 v[232:235],a[128:131],v[112:115],v[44:47]
s_waitcnt vmcnt(8) lgkmcnt(0)
v_mfma_f32_16x16x32_bf16 v[232:235],a[132:135],v[116:119],v[232:235]
s_barrier
ds_read_b128 a[128:131],v1
v_add_u32 v2,s48,v2
v_mfma_f32_16x16x32_bf16 v[232:235],a[136:139],v[120:123],v[232:235]
)A"
R"A(s_mov_b32 s90,L_endseg_%=-L_pc_%=+128
v_mfma_f32_16x16x32_bf16 v[232:235],a[140:143],v[124:127],v[232:235]
s_add_u32 m0,s49,s51
ds_read_b128 a[132:135],v1 offset:256
v_mfma_f32_16x16x32_bf16 v[236:239],a[156:159],v[124:127],v[44:47]
s_lshr_b32 s45,s45,1
v_mfma_f32_16x16x32_bf16 v[236:239],a[152:155],v[120:123],v[236:239]
ds_read_b128 a[136:139],v1 offset:512
v_add_u32 v3,s48,v3
v_mfma_f32_16x16x32_bf16 v[236:239],a[148:151],v[116:119],v[236:239]
buffer_load_dwordx4 v4,s[20:23],s47 offen nt lds
v_mfma_f32_16x16x32_bf16 v[236:239],a[144:147],v[112:115],v[236:239]
v_mfma_f32_16x16x32_bf16 v[248:251],a[160:163],v[112:115],v[44:47]
s_add_u32 s62,s50,32768
ds_read_b128 a[140:143],v1 offset:768
v_mfma_f32_16x16x32_bf16 v[248:251],a[164:167],v[116:119],v[248:251]
s_and_b32 s62,s62,131071
v_mfma_f32_16x16x32_bf16 v[248:251],a[168:171],v[120:123],v[248:251]
s_addk_i32 s54,16384
v_mfma_f32_16x16x32_bf16 v[248:251],a[172:175],v[124:127],v[248:251]
s_sub_u32 s48,s62,s50
ds_read_b128 a[156:159],v1 offset:4864
v_mfma_f32_16x16x32_bf16 v[252:255],a[188:191],v[124:127],v[44:47]
s_sub_u32 s53,s53,1
v_mfma_f32_16x16x32_bf16 v[252:255],a[184:187],v[120:123],v[252:255]
s_and_b32 s46,s54,0xfffc0000
v_mfma_f32_16x16x32_bf16 v[252:255],a[180:183],v[116:119],v[252:255]
s_mov_b32 s50,s62
ds_read_b128 a[152:155],v1 offset:4608
v_mfma_f32_16x16x32_bf16 v[252:255],a[176:179],v[112:115],v[252:255]
s_xor_b32 s49,s50,65536
v_mfma_f32_16x16x32_bf16 a[32:35],a[192:195],v[168:171],a[32:35]
ds_read_b128 a[148:151],v1 offset:4352
v_mfma_f32_16x16x32_bf16 a[32:35],a[196:199],v[172:175],a[32:35]
v_mfma_f32_16x16x32_bf16 a[36:39],a[204:207],v[172:175],a[36:39]
ds_read_b128 a[144:147],v1 offset:4096
v_mfma_f32_16x16x32_bf16 a[36:39],a[200:203],v[168:171],a[36:39]
v_mfma_f32_16x16x32_bf16 a[40:43],a[208:211],v[168:171],a[40:43]
buffer_load_dwordx4 v4,s[20:23],s47 offen offset:1024 nt lds
v_mfma_f32_16x16x32_bf16 a[40:43],a[212:215],v[172:175],a[40:43]
ds_read_b128 a[160:163],v1 offset:8192
v_mfma_f32_16x16x32_bf16 a[44:47],a[220:223],v[172:175],a[44:47]
v_mfma_f32_16x16x32_bf16 a[44:47],a[216:219],v[168:171],a[44:47]
ds_read_b128 a[164:167],v1 offset:8448
v_mfma_f32_16x16x32_bf16 a[48:51],a[224:227],v[168:171],a[48:51]
v_mfma_f32_16x16x32_bf16 a[48:51],a[228:231],v[172:175],a[48:51]
ds_read_b128 a[168:171],v1 offset:8704
v_mfma_f32_16x16x32_bf16 a[52:55],a[236:239],v[172:175],a[52:55]
v_mfma_f32_16x16x32_bf16 a[52:55],a[232:235],v[168:171],a[52:55]
ds_read_b128 a[172:175],v1 offset:8960
v_mfma_f32_16x16x32_bf16 a[56:59],a[240:243],v[168:171],a[56:59]
v_mfma_f32_16x16x32_bf16 a[56:59],a[244:247],v[172:175],a[56:59]
ds_read_b128 a[188:191],v1 offset:13056
v_mfma_f32_16x16x32_bf16 a[60:63],a[252:255],v[172:175],a[60:63]
v_mfma_f32_16x16x32_bf16 a[60:63],a[248:251],v[168:171],a[60:63]
ds_read_b128 a[184:187],v1 offset:12800
v_smfmac_f32_16x16x64_bf16 v[52:55],v[28:31],v[168:175],v27
s_waitcnt lgkmcnt(4)
v_mfma_f32_16x16x32_bf16 v[136:139],a[128:131],v[64:67],v[32:35]
ds_read_b128 a[180:183],v1 offset:12544
v_mfma_f32_16x16x32_bf16 v[136:139],a[132:135],v[68:71],v[136:139]
buffer_load_dwordx4 v5,s[20:23],s47 offen offset:2048 nt lds
v_mfma_f32_16x16x32_bf16 v[136:139],a[136:139],v[72:75],v[136:139]
ds_read_b128 a[176:179],v1 offset:12288
v_add_u32 v1,s48,v1
v_mfma_f32_16x16x32_bf16 v[136:139],a[140:143],v[76:79],v[136:139]
v_exp_f32 v232,v232
v_mfma_f32_16x16x32_bf16 v[140:143],a[156:159],v[76:79],v[32:35]
v_exp_f32 v233,v233
v_mfma_f32_16x16x32_bf16 v[140:143],a[152:155],v[72:75],v[140:143]
v_exp_f32 v234,v234
v_mfma_f32_16x16x32_bf16 v[140:143],a[148:151],v[68:71],v[140:143]
v_exp_f32 v235,v235
v_mfma_f32_16x16x32_bf16 v[140:143],a[144:147],v[64:67],v[140:143]
v_exp_f32 v236,v236
v_mfma_f32_16x16x32_bf16 v[152:155],a[160:163],v[64:67],v[32:35]
v_exp_f32 v237,v237
v_mfma_f32_16x16x32_bf16 v[152:155],a[164:167],v[68:71],v[152:155]
s_waitcnt lgkmcnt(1)
v_exp_f32 v238,v238
v_mfma_f32_16x16x32_bf16 v[152:155],a[168:171],v[72:75],v[152:155]
v_exp_f32 v239,v239
v_mfma_f32_16x16x32_bf16 v[152:155],a[172:175],v[76:79],v[152:155]
v_exp_f32 v248,v248
v_mfma_f32_16x16x32_bf16 v[156:159],a[188:191],v[76:79],v[32:35]
v_exp_f32 v249,v249
v_mfma_f32_16x16x32_bf16 v[156:159],a[184:187],v[72:75],v[156:159]
v_exp_f32 v250,v250
v_mfma_f32_16x16x32_bf16 v[156:159],a[180:183],v[68:71],v[156:159]
s_waitcnt lgkmcnt(0)
v_exp_f32 v251,v251
v_mfma_f32_16x16x32_bf16 v[156:159],a[176:179],v[64:67],v[156:159]
buffer_load_dwordx4 v5,s[20:23],s47 offen offset:3072 nt lds
v_exp_f32 v252,v252
v_exp_f32 v253,v253
v_exp_f32 v254,v254
v_exp_f32 v255,v255
v_exp_f32 v136,v136
v_exp_f32 v137,v137
v_exp_f32 v138,v138
v_exp_f32 v139,v139
v_exp_f32 v140,v140
v_exp_f32 v141,v141
s_addk_i32 m0,16384
v_exp_f32 v142,v142
v_exp_f32 v143,v143
v_exp_f32 v152,v152
buffer_load_dwordx4 v4,s[24:27],s47 offen nt lds
.p2align 3
v_cvt_pk_bf16_f32 v232,v232,v233
v_cvt_pk_bf16_f32 v233,v234,v235
v_cvt_pk_bf16_f32 v234,v236,v237
v_cvt_pk_bf16_f32 v235,v238,v239
v_cvt_pk_bf16_f32 v236,v248,v249
v_mfma_f32_16x16x32_bf16 v[168:171],a[128:131],v[80:83],v[36:39]
v_cvt_pk_bf16_f32 v237,v250,v251
v_cvt_pk_bf16_f32 v238,v252,v253
v_mfma_f32_16x16x32_bf16 v[168:171],a[132:135],v[84:87],v[168:171]
v_cvt_pk_bf16_f32 v239,v254,v255
v_mfma_f32_16x16x32_bf16 v[168:171],a[136:139],v[88:91],v[168:171]
v_exp_f32 v153,v153
v_mfma_f32_16x16x32_bf16 v[168:171],a[140:143],v[92:95],v[168:171]
v_exp_f32 v154,v154
v_mfma_f32_16x16x32_bf16 v[172:175],a[156:159],v[92:95],v[36:39]
v_exp_f32 v155,v155
v_mfma_f32_16x16x32_bf16 v[172:175],a[152:155],v[88:91],v[172:175]
v_exp_f32 v156,v156
v_mfma_f32_16x16x32_bf16 v[172:175],a[148:151],v[84:87],v[172:175]
v_exp_f32 v157,v157
v_mfma_f32_16x16x32_bf16 v[172:175],a[144:147],v[80:83],v[172:175]
v_exp_f32 v158,v158
v_mfma_f32_16x16x32_bf16 v[184:187],a[160:163],v[80:83],v[36:39]
v_exp_f32 v159,v159
v_mfma_f32_16x16x32_bf16 v[184:187],a[164:167],v[84:87],v[184:187]
v_exp_f32 v168,v168
v_mfma_f32_16x16x32_bf16 v[184:187],a[168:171],v[88:91],v[184:187]
buffer_load_dwordx4 v5,s[24:27],s47 offen offset:1024 nt lds
v_mfma_f32_16x16x32_bf16 v[184:187],a[172:175],v[92:95],v[184:187]
v_exp_f32 v169,v169
v_mfma_f32_16x16x32_bf16 v[188:191],a[188:191],v[92:95],v[36:39]
v_exp_f32 v170,v170
v_mfma_f32_16x16x32_bf16 v[188:191],a[184:187],v[88:91],v[188:191]
v_exp_f32 v171,v171
v_mfma_f32_16x16x32_bf16 v[188:191],a[180:183],v[84:87],v[188:191]
v_exp_f32 v172,v172
v_mfma_f32_16x16x32_bf16 v[188:191],a[176:179],v[80:83],v[188:191]
v_exp_f32 v173,v173
v_mfma_f32_16x16x32_bf16 a[96:99],a[192:195],v[232:235],a[96:99]
ds_read_b64_tr_b16 a[192:193],v2
v_cvt_pk_bf16_f32 v168,v168,v169
v_mfma_f32_16x16x32_bf16 a[96:99],a[196:199],v[236:239],a[96:99]
ds_read_b64_tr_b16 a[194:195],v2 offset:4096
v_cvt_pk_bf16_f32 v169,v170,v171
v_mfma_f32_16x16x32_bf16 a[100:103],a[204:207],v[236:239],a[100:103]
ds_read_b64_tr_b16 a[196:197],v2 offset:8192
v_cvt_pk_bf16_f32 v170,v172,v173
v_mfma_f32_16x16x32_bf16 a[100:103],a[200:203],v[232:235],a[100:103]
v_exp_f32 v174,v174
v_mfma_f32_16x16x32_bf16 a[104:107],a[208:211],v[232:235],a[104:107]
v_exp_f32 v175,v175
v_mfma_f32_16x16x32_bf16 a[104:107],a[212:215],v[236:239],a[104:107]
ds_read_b64_tr_b16 a[198:199],v2 offset:12288
v_cvt_pk_bf16_f32 v171,v174,v175
v_mfma_f32_16x16x32_bf16 a[108:111],a[220:223],v[236:239],a[108:111]
v_exp_f32 v184,v184
v_mfma_f32_16x16x32_bf16 a[108:111],a[216:219],v[232:235],a[108:111]
v_exp_f32 v185,v185
v_mfma_f32_16x16x32_bf16 a[112:115],a[224:227],v[232:235],a[112:115]
buffer_load_dwordx4 v4,s[24:27],s47 offen offset:2048 nt lds
v_mfma_f32_16x16x32_bf16 a[112:115],a[228:231],v[236:239],a[112:115]
ds_read_b64_tr_b16 a[204:205],v3 offset:8192
v_cvt_pk_bf16_f32 v172,v184,v185
v_mfma_f32_16x16x32_bf16 a[116:119],a[236:239],v[236:239],a[116:119]
ds_read_b64_tr_b16 a[206:207],v3 offset:12288
v_cvt_pk_bf16_f32 v136,v136,v137
v_mfma_f32_16x16x32_bf16 a[116:119],a[232:235],v[232:235],a[116:119]
v_exp_f32 v186,v186
v_mfma_f32_16x16x32_bf16 a[120:123],a[240:243],v[232:235],a[120:123]
ds_read_b64_tr_b16 a[200:201],v3
v_mfma_f32_16x16x32_bf16 a[120:123],a[244:247],v[236:239],a[120:123]
ds_read_b64_tr_b16 a[202:203],v3 offset:4096
v_cvt_pk_bf16_f32 v137,v138,v139
v_mfma_f32_16x16x32_bf16 a[124:127],a[252:255],v[236:239],a[124:127]
ds_read_b64_tr_b16 a[208:209],v2 offset:256
v_mfma_f32_16x16x32_bf16 a[124:127],a[248:251],v[232:235],a[124:127]
ds_read_b64_tr_b16 a[210:211],v2 offset:4352
v_smfmac_f32_16x16x64_bf16 v[60:63],v[28:31],v[232:239],v27
ds_read_b64_tr_b16 a[212:213],v2 offset:8448
ds_read_b64_tr_b16 a[214:215],v2 offset:12544
v_cvt_pk_bf16_f32 v138,v140,v141
ds_read_b64_tr_b16 a[220:221],v3 offset:8448
ds_read_b64_tr_b16 a[222:223],v3 offset:12544
v_cvt_pk_bf16_f32 v139,v142,v143
ds_read_b64_tr_b16 a[216:217],v3 offset:256
ds_read_b64_tr_b16 a[218:219],v3 offset:4352
buffer_load_dwordx4 v5,s[24:27],s47 offen offset:3072 nt lds
s_or_b32 s47,s47,0x18000
ds_read_b64_tr_b16 a[224:225],v2 offset:512
v_cvt_pk_bf16_f32 v140,v152,v153
s_addk_i32 s47,4096
s_and_b32 s47,s47,0x27000
ds_read_b64_tr_b16 a[226:227],v2 offset:4608
s_or_b32 s47,s47,s46
ds_read_b64_tr_b16 a[228:229],v2 offset:8704
v_cvt_pk_bf16_f32 v141,v154,v155
ds_read_b64_tr_b16 a[230:231],v2 offset:12800
ds_read_b64_tr_b16 a[236:237],v3 offset:8704
ds_read_b64_tr_b16 a[238:239],v3 offset:12800
v_cvt_pk_bf16_f32 v142,v156,v157
ds_read_b64_tr_b16 a[232:233],v3 offset:512
ds_read_b64_tr_b16 a[234:235],v3 offset:4608
ds_read_b64_tr_b16 a[240:241],v2 offset:768
v_cvt_pk_bf16_f32 v143,v158,v159
buffer_load_dword v26,v24,s[88:91],s93 offen
s_waitcnt lgkmcnt(15)
v_mfma_f32_16x16x32_bf16 a[0:3],a[192:195],v[136:139],a[0:3]
ds_read_b64_tr_b16 a[242:243],v2 offset:4864
v_mfma_f32_16x16x32_bf16 a[0:3],a[196:199],v[140:143],a[0:3]
ds_read_b64_tr_b16 a[244:245],v2 offset:8960
v_mfma_f32_16x16x32_bf16 a[4:7],a[204:207],v[140:143],a[4:7]
ds_read_b64_tr_b16 a[246:247],v2 offset:13056
v_mfma_f32_16x16x32_bf16 a[4:7],a[200:203],v[136:139],a[4:7]
ds_read_b64_tr_b16 a[252:253],v3 offset:8960
v_mfma_f32_16x16x32_bf16 a[8:11],a[208:211],v[136:139],a[8:11]
s_waitcnt lgkmcnt(15)
ds_read_b64_tr_b16 a[254:255],v3 offset:13056
v_mfma_f32_16x16x32_bf16 a[8:11],a[212:215],v[140:143],a[8:11]
ds_read_b64_tr_b16 a[248:249],v3 offset:768
v_mfma_f32_16x16x32_bf16 a[12:15],a[220:223],v[140:143],a[12:15]
s_waitcnt lgkmcnt(15)
ds_read_b64_tr_b16 a[250:251],v3 offset:4864
v_mfma_f32_16x16x32_bf16 a[12:15],a[216:219],v[136:139],a[12:15]
s_bitcmp1_b32 s45,0
s_cbranch_scc0 L_t31v20_%=
s_mov_b32 m0,s80
s_addk_i32 s80,4096
buffer_load_dwordx4 v25,s[20:23],s83 offen nt lds
s_addk_i32 s83,1024
L_t31v20_%=:
s_waitcnt lgkmcnt(7)
v_mfma_f32_16x16x32_bf16 a[16:19],a[224:227],v[136:139],a[16:19]
v_exp_f32 v187,v187
v_mfma_f32_16x16x32_bf16 a[16:19],a[228:231],v[140:143],a[16:19]
v_exp_f32 v188,v188
v_mfma_f32_16x16x32_bf16 a[20:23],a[236:239],v[140:143],a[20:23]
v_exp_f32 v189,v189
v_mfma_f32_16x16x32_bf16 a[20:23],a[232:235],v[136:139],a[20:23]
s_waitcnt lgkmcnt(3)
v_exp_f32 v190,v190
v_mfma_f32_16x16x32_bf16 a[24:27],a[240:243],v[136:139],a[24:27]
v_exp_f32 v191,v191
v_mfma_f32_16x16x32_bf16 a[24:27],a[244:247],v[140:143],a[24:27]
s_waitcnt lgkmcnt(1)
v_cvt_pk_bf16_f32 v173,v186,v187
v_mfma_f32_16x16x32_bf16 a[28:31],a[252:255],v[140:143],a[28:31]
s_waitcnt lgkmcnt(0)
v_cvt_pk_bf16_f32 v174,v188,v189
v_cvt_pk_bf16_f32 v175,v190,v191
v_mfma_f32_16x16x32_bf16 a[28:31],a[248:251],v[136:139],a[28:31]
s_cmp_lg_u32 s53,0
v_smfmac_f32_16x16x64_bf16 v[48:51],v[28:31],v[136:143],v27
s_cbranch_scc1 L_att0v2_%=
s_waitcnt lgkmcnt(0)
s_nop 7
s_branch L_att0x_%=
.p2align 5
L_att0_%=:
v_mfma_f32_16x16x32_bf16 v[232:235],a[128:131],v[112:115],v[44:47]
s_waitcnt vmcnt(8) lgkmcnt(0)
v_exp_f32 v200,v200
v_mfma_f32_16x16x32_bf16 v[232:235],a[132:135],v[116:119],v[232:235]
s_barrier
ds_read_b128 a[128:131],v1
v_add_u32 v2,s48,v2
v_mfma_f32_16x16x32_bf16 v[232:235],a[136:139],v[120:123],v[232:235]
s_mov_b32 s90,L_endseg_%=-L_pc_%=+128
v_exp_f32 v201,v201
v_mfma_f32_16x16x32_bf16 v[232:235],a[140:143],v[124:127],v[232:235]
s_add_u32 m0,s49,s51
)A"
R"A(ds_read_b128 a[132:135],v1 offset:256
v_cvt_pk_bf16_f32 v200,v200,v201
v_mfma_f32_16x16x32_bf16 v[236:239],a[156:159],v[124:127],v[44:47]
s_lshr_b32 s45,s45,1
v_exp_f32 v202,v202
v_mfma_f32_16x16x32_bf16 v[236:239],a[152:155],v[120:123],v[236:239]
s_sub_u32 s94,s94,3
ds_read_b128 a[136:139],v1 offset:512
v_add_u32 v3,s48,v3
v_mfma_f32_16x16x32_bf16 v[236:239],a[148:151],v[116:119],v[236:239]
s_lshr_b32 s60,s94,2
buffer_load_dwordx4 v4,s[20:23],s47 offen nt lds
v_mfma_f32_16x16x32_bf16 v[236:239],a[144:147],v[112:115],v[236:239]
s_max_i32 s60,s60,124
v_exp_f32 v203,v203
v_mfma_f32_16x16x32_bf16 v[248:251],a[160:163],v[112:115],v[44:47]
s_add_u32 s62,s50,32768
ds_read_b128 a[140:143],v1 offset:768
v_cvt_pk_bf16_f32 v201,v202,v203
v_mfma_f32_16x16x32_bf16 v[248:251],a[164:167],v[116:119],v[248:251]
s_and_b32 s62,s62,131071
v_exp_f32 v204,v204
v_mfma_f32_16x16x32_bf16 v[248:251],a[168:171],v[120:123],v[248:251]
s_addk_i32 s54,16384
v_exp_f32 v205,v205
v_mfma_f32_16x16x32_bf16 v[248:251],a[172:175],v[124:127],v[248:251]
s_sub_u32 s48,s62,s50
ds_read_b128 a[156:159],v1 offset:4864
v_cvt_pk_bf16_f32 v202,v204,v205
v_mfma_f32_16x16x32_bf16 v[252:255],a[188:191],v[124:127],v[44:47]
s_sub_u32 s53,s53,1
v_exp_f32 v206,v206
v_mfma_f32_16x16x32_bf16 v[252:255],a[184:187],v[120:123],v[252:255]
s_and_b32 s46,s54,0xfffc0000
v_exp_f32 v207,v207
v_mfma_f32_16x16x32_bf16 v[252:255],a[180:183],v[116:119],v[252:255]
s_mov_b32 s50,s62
ds_read_b128 a[152:155],v1 offset:4608
v_cvt_pk_bf16_f32 v203,v206,v207
v_mfma_f32_16x16x32_bf16 v[252:255],a[176:179],v[112:115],v[252:255]
s_xor_b32 s49,s50,65536
v_exp_f32 v216,v216
v_mfma_f32_16x16x32_bf16 a[32:35],a[192:195],v[168:171],a[32:35]
ds_read_b128 a[148:151],v1 offset:4352
v_mfma_f32_16x16x32_bf16 a[32:35],a[196:199],v[172:175],a[32:35]
v_exp_f32 v217,v217
v_mfma_f32_16x16x32_bf16 a[36:39],a[204:207],v[172:175],a[36:39]
ds_read_b128 a[144:147],v1 offset:4096
v_cvt_pk_bf16_f32 v204,v216,v217
v_mfma_f32_16x16x32_bf16 a[36:39],a[200:203],v[168:171],a[36:39]
v_exp_f32 v218,v218
v_mfma_f32_16x16x32_bf16 a[40:43],a[208:211],v[168:171],a[40:43]
buffer_load_dwordx4 v4,s[20:23],s47 offen offset:1024 nt lds
v_mfma_f32_16x16x32_bf16 a[40:43],a[212:215],v[172:175],a[40:43]
ds_read_b128 a[160:163],v1 offset:8192
v_mfma_f32_16x16x32_bf16 a[44:47],a[220:223],v[172:175],a[44:47]
v_exp_f32 v219,v219
v_mfma_f32_16x16x32_bf16 a[44:47],a[216:219],v[168:171],a[44:47]
ds_read_b128 a[164:167],v1 offset:8448
v_cvt_pk_bf16_f32 v205,v218,v219
v_mfma_f32_16x16x32_bf16 a[48:51],a[224:227],v[168:171],a[48:51]
v_exp_f32 v220,v220
v_mfma_f32_16x16x32_bf16 a[48:51],a[228:231],v[172:175],a[48:51]
ds_read_b128 a[168:171],v1 offset:8704
v_mfma_f32_16x16x32_bf16 a[52:55],a[236:239],v[172:175],a[52:55]
v_exp_f32 v221,v221
v_mfma_f32_16x16x32_bf16 a[52:55],a[232:235],v[168:171],a[52:55]
ds_read_b128 a[172:175],v1 offset:8960
v_cvt_pk_bf16_f32 v206,v220,v221
v_mfma_f32_16x16x32_bf16 a[56:59],a[240:243],v[168:171],a[56:59]
v_exp_f32 v222,v222
v_mfma_f32_16x16x32_bf16 a[56:59],a[244:247],v[172:175],a[56:59]
ds_read_b128 a[188:191],v1 offset:13056
v_mfma_f32_16x16x32_bf16 a[60:63],a[252:255],v[172:175],a[60:63]
v_exp_f32 v223,v223
v_mfma_f32_16x16x32_bf16 a[60:63],a[248:251],v[168:171],a[60:63]
ds_read_b128 a[184:187],v1 offset:12800
v_cvt_pk_bf16_f32 v207,v222,v223
v_smfmac_f32_16x16x64_bf16 v[52:55],v[28:31],v[168:175],v27
s_waitcnt lgkmcnt(4)
v_mfma_f32_16x16x32_bf16 v[136:139],a[128:131],v[64:67],v[32:35]
ds_read_b128 a[180:183],v1 offset:12544
v_mfma_f32_16x16x32_bf16 v[136:139],a[132:135],v[68:71],v[136:139]
buffer_load_dwordx4 v5,s[20:23],s47 offen offset:2048 nt lds
v_mfma_f32_16x16x32_bf16 v[136:139],a[136:139],v[72:75],v[136:139]
ds_read_b128 a[176:179],v1 offset:12288
v_add_u32 v1,s48,v1
v_mfma_f32_16x16x32_bf16 v[136:139],a[140:143],v[76:79],v[136:139]
v_exp_f32 v232,v232
v_mfma_f32_16x16x32_bf16 v[140:143],a[156:159],v[76:79],v[32:35]
v_exp_f32 v233,v233
v_mfma_f32_16x16x32_bf16 v[140:143],a[152:155],v[72:75],v[140:143]
v_exp_f32 v234,v234
v_mfma_f32_16x16x32_bf16 v[140:143],a[148:151],v[68:71],v[140:143]
v_exp_f32 v235,v235
v_mfma_f32_16x16x32_bf16 v[140:143],a[144:147],v[64:67],v[140:143]
v_exp_f32 v236,v236
v_mfma_f32_16x16x32_bf16 v[152:155],a[160:163],v[64:67],v[32:35]
v_exp_f32 v237,v237
v_mfma_f32_16x16x32_bf16 v[152:155],a[164:167],v[68:71],v[152:155]
s_waitcnt lgkmcnt(1)
v_exp_f32 v238,v238
v_mfma_f32_16x16x32_bf16 v[152:155],a[168:171],v[72:75],v[152:155]
v_exp_f32 v239,v239
v_mfma_f32_16x16x32_bf16 v[152:155],a[172:175],v[76:79],v[152:155]
v_exp_f32 v248,v248
v_mfma_f32_16x16x32_bf16 v[156:159],a[188:191],v[76:79],v[32:35]
v_exp_f32 v249,v249
v_mfma_f32_16x16x32_bf16 v[156:159],a[184:187],v[72:75],v[156:159]
v_exp_f32 v250,v250
v_mfma_f32_16x16x32_bf16 v[156:159],a[180:183],v[68:71],v[156:159]
s_waitcnt lgkmcnt(0)
v_exp_f32 v251,v251
v_mfma_f32_16x16x32_bf16 v[156:159],a[176:179],v[64:67],v[156:159]
buffer_load_dwordx4 v5,s[20:23],s47 offen offset:3072 nt lds
v_mfma_f32_16x16x32_bf16 a[64:67],a[192:195],v[200:203],a[64:67]
v_exp_f32 v252,v252
v_mfma_f32_16x16x32_bf16 a[64:67],a[196:199],v[204:207],a[64:67]
v_exp_f32 v253,v253
v_mfma_f32_16x16x32_bf16 a[68:71],a[204:207],v[204:207],a[68:71]
v_exp_f32 v254,v254
v_mfma_f32_16x16x32_bf16 a[68:71],a[200:203],v[200:203],a[68:71]
v_exp_f32 v255,v255
v_mfma_f32_16x16x32_bf16 a[72:75],a[208:211],v[200:203],a[72:75]
v_exp_f32 v136,v136
v_mfma_f32_16x16x32_bf16 a[72:75],a[212:215],v[204:207],a[72:75]
v_exp_f32 v137,v137
v_mfma_f32_16x16x32_bf16 a[76:79],a[220:223],v[204:207],a[76:79]
v_exp_f32 v138,v138
v_mfma_f32_16x16x32_bf16 a[76:79],a[216:219],v[200:203],a[76:79]
v_exp_f32 v139,v139
v_mfma_f32_16x16x32_bf16 a[80:83],a[224:227],v[200:203],a[80:83]
v_exp_f32 v140,v140
v_mfma_f32_16x16x32_bf16 a[80:83],a[228:231],v[204:207],a[80:83]
v_exp_f32 v141,v141
v_mfma_f32_16x16x32_bf16 a[84:87],a[236:239],v[204:207],a[84:87]
s_addk_i32 m0,16384
v_exp_f32 v142,v142
v_mfma_f32_16x16x32_bf16 a[84:87],a[232:235],v[200:203],a[84:87]
v_exp_f32 v143,v143
v_mfma_f32_16x16x32_bf16 a[88:91],a[240:243],v[200:203],a[88:91]
v_exp_f32 v152,v152
v_mfma_f32_16x16x32_bf16 a[88:91],a[244:247],v[204:207],a[88:91]
buffer_load_dwordx4 v4,s[24:27],s47 offen nt lds
v_mfma_f32_16x16x32_bf16 a[92:95],a[252:255],v[204:207],a[92:95]
v_cvt_pk_bf16_f32 v232,v232,v233
v_cvt_pk_bf16_f32 v233,v234,v235
v_mfma_f32_16x16x32_bf16 a[92:95],a[248:251],v[200:203],a[92:95]
v_cvt_pk_bf16_f32 v234,v236,v237
v_cvt_pk_bf16_f32 v235,v238,v239
v_smfmac_f32_16x16x64_bf16 v[56:59],v[28:31],v[200:207],v27
v_cvt_pk_bf16_f32 v236,v248,v249
v_mfma_f32_16x16x32_bf16 v[168:171],a[128:131],v[80:83],v[36:39]
v_cvt_pk_bf16_f32 v237,v250,v251
v_cvt_pk_bf16_f32 v238,v252,v253
v_mfma_f32_16x16x32_bf16 v[168:171],a[132:135],v[84:87],v[168:171]
v_cvt_pk_bf16_f32 v239,v254,v255
v_mfma_f32_16x16x32_bf16 v[168:171],a[136:139],v[88:91],v[168:171]
v_exp_f32 v153,v153
v_mfma_f32_16x16x32_bf16 v[168:171],a[140:143],v[92:95],v[168:171]
v_exp_f32 v154,v154
v_mfma_f32_16x16x32_bf16 v[172:175],a[156:159],v[92:95],v[36:39]
v_exp_f32 v155,v155
v_mfma_f32_16x16x32_bf16 v[172:175],a[152:155],v[88:91],v[172:175]
v_exp_f32 v156,v156
v_mfma_f32_16x16x32_bf16 v[172:175],a[148:151],v[84:87],v[172:175]
v_exp_f32 v157,v157
v_mfma_f32_16x16x32_bf16 v[172:175],a[144:147],v[80:83],v[172:175]
v_exp_f32 v158,v158
v_mfma_f32_16x16x32_bf16 v[184:187],a[160:163],v[80:83],v[36:39]
v_exp_f32 v159,v159
v_mfma_f32_16x16x32_bf16 v[184:187],a[164:167],v[84:87],v[184:187]
v_exp_f32 v168,v168
v_mfma_f32_16x16x32_bf16 v[184:187],a[168:171],v[88:91],v[184:187]
buffer_load_dwordx4 v5,s[24:27],s47 offen offset:1024 nt lds
v_mfma_f32_16x16x32_bf16 v[184:187],a[172:175],v[92:95],v[184:187]
v_exp_f32 v169,v169
v_mfma_f32_16x16x32_bf16 v[188:191],a[188:191],v[92:95],v[36:39]
v_exp_f32 v170,v170
v_mfma_f32_16x16x32_bf16 v[188:191],a[184:187],v[88:91],v[188:191]
v_exp_f32 v171,v171
v_mfma_f32_16x16x32_bf16 v[188:191],a[180:183],v[84:87],v[188:191]
v_exp_f32 v172,v172
v_mfma_f32_16x16x32_bf16 v[188:191],a[176:179],v[80:83],v[188:191]
v_exp_f32 v173,v173
v_mfma_f32_16x16x32_bf16 a[96:99],a[192:195],v[232:235],a[96:99]
ds_read_b64_tr_b16 a[192:193],v2
v_cvt_pk_bf16_f32 v168,v168,v169
v_mfma_f32_16x16x32_bf16 a[96:99],a[196:199],v[236:239],a[96:99]
ds_read_b64_tr_b16 a[194:195],v2 offset:4096
v_cvt_pk_bf16_f32 v169,v170,v171
v_mfma_f32_16x16x32_bf16 a[100:103],a[204:207],v[236:239],a[100:103]
ds_read_b64_tr_b16 a[196:197],v2 offset:8192
v_cvt_pk_bf16_f32 v170,v172,v173
v_mfma_f32_16x16x32_bf16 a[100:103],a[200:203],v[232:235],a[100:103]
v_exp_f32 v174,v174
v_mfma_f32_16x16x32_bf16 a[104:107],a[208:211],v[232:235],a[104:107]
v_exp_f32 v175,v175
v_mfma_f32_16x16x32_bf16 a[104:107],a[212:215],v[236:239],a[104:107]
ds_read_b64_tr_b16 a[198:199],v2 offset:12288
v_cvt_pk_bf16_f32 v171,v174,v175
v_mfma_f32_16x16x32_bf16 a[108:111],a[220:223],v[236:239],a[108:111]
v_exp_f32 v184,v184
v_mfma_f32_16x16x32_bf16 a[108:111],a[216:219],v[232:235],a[108:111]
v_exp_f32 v185,v185
v_mfma_f32_16x16x32_bf16 a[112:115],a[224:227],v[232:235],a[112:115]
buffer_load_dwordx4 v4,s[24:27],s47 offen offset:2048 nt lds
v_mfma_f32_16x16x32_bf16 a[112:115],a[228:231],v[236:239],a[112:115]
ds_read_b64_tr_b16 a[204:205],v3 offset:8192
v_cvt_pk_bf16_f32 v172,v184,v185
v_mfma_f32_16x16x32_bf16 a[116:119],a[236:239],v[236:239],a[116:119]
ds_read_b64_tr_b16 a[206:207],v3 offset:12288
v_cvt_pk_bf16_f32 v136,v136,v137
v_mfma_f32_16x16x32_bf16 a[116:119],a[232:235],v[232:235],a[116:119]
v_exp_f32 v186,v186
v_mfma_f32_16x16x32_bf16 a[120:123],a[240:243],v[232:235],a[120:123]
ds_read_b64_tr_b16 a[200:201],v3
v_mfma_f32_16x16x32_bf16 a[120:123],a[244:247],v[236:239],a[120:123]
ds_read_b64_tr_b16 a[202:203],v3 offset:4096
v_cvt_pk_bf16_f32 v137,v138,v139
v_mfma_f32_16x16x32_bf16 a[124:127],a[252:255],v[236:239],a[124:127]
ds_read_b64_tr_b16 a[208:209],v2 offset:256
v_mfma_f32_16x16x32_bf16 a[124:127],a[248:251],v[232:235],a[124:127]
ds_read_b64_tr_b16 a[210:211],v2 offset:4352
v_smfmac_f32_16x16x64_bf16 v[60:63],v[28:31],v[232:239],v27
ds_read_b64_tr_b16 a[212:213],v2 offset:8448
v_mfma_f32_16x16x32_bf16 v[200:203],a[128:131],v[96:99],v[40:43]
ds_read_b64_tr_b16 a[214:215],v2 offset:12544
v_cvt_pk_bf16_f32 v138,v140,v141
v_mfma_f32_16x16x32_bf16 v[200:203],a[132:135],v[100:103],v[200:203]
ds_read_b64_tr_b16 a[220:221],v3 offset:8448
v_mfma_f32_16x16x32_bf16 v[200:203],a[136:139],v[104:107],v[200:203]
ds_read_b64_tr_b16 a[222:223],v3 offset:12544
v_cvt_pk_bf16_f32 v139,v142,v143
v_mfma_f32_16x16x32_bf16 v[200:203],a[140:143],v[108:111],v[200:203]
ds_read_b64_tr_b16 a[216:217],v3 offset:256
v_mfma_f32_16x16x32_bf16 v[204:207],a[156:159],v[108:111],v[40:43]
ds_read_b64_tr_b16 a[218:219],v3 offset:4352
v_mfma_f32_16x16x32_bf16 v[204:207],a[152:155],v[104:107],v[204:207]
buffer_load_dwordx4 v5,s[24:27],s47 offen offset:3072 nt lds
v_mfma_f32_16x16x32_bf16 v[204:207],a[148:151],v[100:103],v[204:207]
s_or_b32 s47,s47,0x18000
ds_read_b64_tr_b16 a[224:225],v2 offset:512
v_cvt_pk_bf16_f32 v140,v152,v153
v_mfma_f32_16x16x32_bf16 v[204:207],a[144:147],v[96:99],v[204:207]
s_addk_i32 s47,4096
s_and_b32 s47,s47,0x27000
ds_read_b64_tr_b16 a[226:227],v2 offset:4608
v_mfma_f32_16x16x32_bf16 v[216:219],a[160:163],v[96:99],v[40:43]
s_or_b32 s47,s47,s46
ds_read_b64_tr_b16 a[228:229],v2 offset:8704
v_cvt_pk_bf16_f32 v141,v154,v155
v_mfma_f32_16x16x32_bf16 v[216:219],a[164:167],v[100:103],v[216:219]
ds_read_b64_tr_b16 a[230:231],v2 offset:12800
v_mfma_f32_16x16x32_bf16 v[216:219],a[168:171],v[104:107],v[216:219]
ds_read_b64_tr_b16 a[236:237],v3 offset:8704
v_mfma_f32_16x16x32_bf16 v[216:219],a[172:175],v[108:111],v[216:219]
ds_read_b64_tr_b16 a[238:239],v3 offset:12800
v_cvt_pk_bf16_f32 v142,v156,v157
v_mfma_f32_16x16x32_bf16 v[220:223],a[188:191],v[108:111],v[40:43]
ds_read_b64_tr_b16 a[232:233],v3 offset:512
v_mfma_f32_16x16x32_bf16 v[220:223],a[184:187],v[104:107],v[220:223]
ds_read_b64_tr_b16 a[234:235],v3 offset:4608
v_mfma_f32_16x16x32_bf16 v[220:223],a[180:183],v[100:103],v[220:223]
ds_read_b64_tr_b16 a[240:241],v2 offset:768
)A"
R"A(v_cvt_pk_bf16_f32 v143,v158,v159
v_mfma_f32_16x16x32_bf16 v[220:223],a[176:179],v[96:99],v[220:223]
s_waitcnt lgkmcnt(7)
buffer_load_dword v26,v24,s[88:91],s93 offen
v_mfma_f32_16x16x32_bf16 a[0:3],a[192:195],v[136:139],a[0:3]
ds_read_b64_tr_b16 a[242:243],v2 offset:4864
v_mfma_f32_16x16x32_bf16 a[0:3],a[196:199],v[140:143],a[0:3]
ds_read_b64_tr_b16 a[244:245],v2 offset:8960
v_mfma_f32_16x16x32_bf16 a[4:7],a[204:207],v[140:143],a[4:7]
ds_read_b64_tr_b16 a[246:247],v2 offset:13056
v_mfma_f32_16x16x32_bf16 a[4:7],a[200:203],v[136:139],a[4:7]
s_memrealtime s[76:77]
ds_read_b64_tr_b16 a[252:253],v3 offset:8960
v_mfma_f32_16x16x32_bf16 a[8:11],a[208:211],v[136:139],a[8:11]
ds_read_b64_tr_b16 a[254:255],v3 offset:13056
v_mfma_f32_16x16x32_bf16 a[8:11],a[212:215],v[140:143],a[8:11]
ds_read_b64_tr_b16 a[248:249],v3 offset:768
v_mfma_f32_16x16x32_bf16 a[12:15],a[220:223],v[140:143],a[12:15]
ds_read_b64_tr_b16 a[250:251],v3 offset:4864
v_mfma_f32_16x16x32_bf16 a[12:15],a[216:219],v[136:139],a[12:15]
s_bitcmp1_b32 s45,0
s_cbranch_scc0 L_t310_%=
s_mov_b32 m0,s80
s_addk_i32 s80,4096
buffer_load_dwordx4 v25,s[20:23],s83 offen nt lds
s_addk_i32 s83,1024
L_t310_%=:
v_mfma_f32_16x16x32_bf16 a[16:19],a[224:227],v[136:139],a[16:19]
s_waitcnt lgkmcnt(6)
v_exp_f32 v187,v187
v_mfma_f32_16x16x32_bf16 a[16:19],a[228:231],v[140:143],a[16:19]
v_exp_f32 v188,v188
v_mfma_f32_16x16x32_bf16 a[20:23],a[236:239],v[140:143],a[20:23]
s_waitcnt lgkmcnt(0)
v_exp_f32 v189,v189
v_mfma_f32_16x16x32_bf16 a[20:23],a[232:235],v[136:139],a[20:23]
s_sub_u32 s79,s78,s76
v_exp_f32 v190,v190
v_mfma_f32_16x16x32_bf16 a[24:27],a[240:243],v[136:139],a[24:27]
s_add_u32 s78,s76,s60
v_exp_f32 v191,v191
v_mfma_f32_16x16x32_bf16 a[24:27],a[244:247],v[140:143],a[24:27]
s_cmp_gt_i32 s79,s95
s_cbranch_scc1 L_ps3_%=
L_pr3_%=:
v_cvt_pk_bf16_f32 v173,v186,v187
v_mfma_f32_16x16x32_bf16 a[28:31],a[252:255],v[140:143],a[28:31]
v_cvt_pk_bf16_f32 v174,v188,v189
v_cvt_pk_bf16_f32 v175,v190,v191
v_mfma_f32_16x16x32_bf16 a[28:31],a[248:251],v[136:139],a[28:31]
s_cmp_lg_u32 s53,0
v_smfmac_f32_16x16x64_bf16 v[48:51],v[28:31],v[136:143],v27
s_cbranch_scc1 L_att0_%=
L_att0x_%=:
v_mfma_f32_16x16x32_bf16 v[232:235],a[128:131],v[112:115],v[44:47]
s_waitcnt vmcnt(10)
s_sub_u32 s94,s94,3
v_exp_f32 v200,v200
v_mfma_f32_16x16x32_bf16 v[232:235],a[132:135],v[116:119],v[232:235]
s_barrier
ds_read_b128 a[128:131],v1
v_and_b32 v18,48,v6
v_mfma_f32_16x16x32_bf16 v[232:235],a[136:139],v[120:123],v[232:235]
s_lshr_b32 s60,s94,2
v_exp_f32 v201,v201
v_mfma_f32_16x16x32_bf16 v[232:235],a[140:143],v[124:127],v[232:235]
s_add_u32 m0,0,s51
ds_read_b128 a[132:135],v1 offset:256
v_cvt_pk_bf16_f32 v200,v200,v201
v_mfma_f32_16x16x32_bf16 v[236:239],a[156:159],v[124:127],v[44:47]
s_max_i32 s60,s60,124
v_exp_f32 v202,v202
v_mfma_f32_16x16x32_bf16 v[236:239],a[152:155],v[120:123],v[236:239]
s_addk_i32 s54,16384
ds_read_b128 a[136:139],v1 offset:512
v_lshrrev_b32 v19,4,v6
v_mfma_f32_16x16x32_bf16 v[236:239],a[148:151],v[116:119],v[236:239]
s_and_b32 s46,s54,0xfffc0000
buffer_load_dwordx4 v4,s[20:23],s47 offen nt lds
v_mfma_f32_16x16x32_bf16 v[236:239],a[144:147],v[112:115],v[236:239]
s_sub_u32 s94,s94,3
ds_read_b128 a[140:143],v1 offset:768
v_lshlrev_b32 v18,2,v18
v_mfma_f32_16x16x32_bf16 v[248:251],a[160:163],v[112:115],v[44:47]
s_addk_i32 s54,16384
v_exp_f32 v203,v203
v_mfma_f32_16x16x32_bf16 v[248:251],a[164:167],v[116:119],v[248:251]
s_mul_i32 s83,s41,5
ds_read_b128 a[156:159],v1 offset:4864
v_cvt_pk_bf16_f32 v201,v202,v203
v_mfma_f32_16x16x32_bf16 v[248:251],a[168:171],v[120:123],v[248:251]
s_add_u32 s83,s83,s42
v_exp_f32 v204,v204
v_mfma_f32_16x16x32_bf16 v[248:251],a[172:175],v[124:127],v[248:251]
s_add_u32 s83,s83,15
ds_read_b128 a[152:155],v1 offset:4608
v_and_b32 v19,48,v19
v_mfma_f32_16x16x32_bf16 v[252:255],a[188:191],v[124:127],v[44:47]
s_lshl_b32 s43,s83,14
v_exp_f32 v205,v205
v_mfma_f32_16x16x32_bf16 v[252:255],a[184:187],v[120:123],v[252:255]
s_add_u32 s84,s55,0
ds_read_b128 a[148:151],v1 offset:4352
v_cvt_pk_bf16_f32 v202,v204,v205
v_mfma_f32_16x16x32_bf16 v[252:255],a[180:183],v[116:119],v[252:255]
s_add_u32 s85,s55,4096
v_exp_f32 v206,v206
v_mfma_f32_16x16x32_bf16 v[252:255],a[176:179],v[112:115],v[252:255]
s_add_u32 s86,s55,16384
ds_read_b128 a[144:147],v1 offset:4096
v_add_u32 v18,v18,v19
v_mfma_f32_16x16x32_bf16 a[32:35],a[192:195],v[168:171],a[32:35]
s_add_u32 s87,s55,20480
v_exp_f32 v207,v207
v_mfma_f32_16x16x32_bf16 a[32:35],a[196:199],v[172:175],a[32:35]
s_lshl_b32 s83,s83,12
ds_read_b128 a[160:163],v1 offset:8192
v_cvt_pk_bf16_f32 v203,v206,v207
v_mfma_f32_16x16x32_bf16 a[36:39],a[204:207],v[172:175],a[36:39]
s_max_i32 s84,s84,s18
v_exp_f32 v216,v216
v_mfma_f32_16x16x32_bf16 a[36:39],a[200:203],v[168:171],a[36:39]
s_max_i32 s85,s85,s19
ds_read_b128 a[164:167],v1 offset:8448
v_add_u32 v18,98304,v18
v_mfma_f32_16x16x32_bf16 a[40:43],a[208:211],v[168:171],a[40:43]
s_max_i32 s86,s86,s100
buffer_load_dwordx4 v4,s[20:23],s47 offen offset:1024 nt lds
v_mfma_f32_16x16x32_bf16 a[40:43],a[212:215],v[172:175],a[40:43]
s_max_i32 s87,s87,s101
ds_read_b128 a[168:171],v1 offset:8704
v_add_u32 v2,s48,v2
v_mfma_f32_16x16x32_bf16 a[44:47],a[220:223],v[172:175],a[44:47]
s_lshl_b32 s44,s40,5
v_exp_f32 v217,v217
v_mfma_f32_16x16x32_bf16 a[44:47],a[216:219],v[168:171],a[44:47]
s_and_b32 s43,s43,0x20000
ds_read_b128 a[172:175],v1 offset:8960
v_cvt_pk_bf16_f32 v204,v216,v217
v_mfma_f32_16x16x32_bf16 a[48:51],a[224:227],v[168:171],a[48:51]
s_and_b32 s83,s83,0x7000
v_exp_f32 v218,v218
v_mfma_f32_16x16x32_bf16 a[48:51],a[228:231],v[172:175],a[48:51]
s_lshl_b32 s80,s40,10
ds_read_b128 a[188:191],v1 offset:13056
v_add_u32 v3,s48,v3
v_mfma_f32_16x16x32_bf16 a[52:55],a[236:239],v[172:175],a[52:55]
s_mov_b32 s48,32768
v_exp_f32 v219,v219
v_mfma_f32_16x16x32_bf16 a[52:55],a[232:235],v[168:171],a[52:55]
s_and_b32 s44,s44,32
ds_read_b128 a[184:187],v1 offset:12800
v_cvt_pk_bf16_f32 v205,v218,v219
v_mfma_f32_16x16x32_bf16 a[56:59],a[240:243],v[168:171],a[56:59]
s_or_b32 s83,s83,s43
v_exp_f32 v220,v220
v_mfma_f32_16x16x32_bf16 a[56:59],a[244:247],v[172:175],a[56:59]
s_mov_b32 s50,32768
s_mov_b32 s49,98304
ds_read_b128 a[180:183],v1 offset:12544
v_mfma_f32_16x16x32_bf16 a[60:63],a[252:255],v[172:175],a[60:63]
s_mov_b32 s45,0x2a8
v_exp_f32 v221,v221
v_mfma_f32_16x16x32_bf16 a[60:63],a[248:251],v[168:171],a[60:63]
s_add_u32 s80,s80,147456
ds_read_b128 a[176:179],v1 offset:12288
v_cvt_pk_bf16_f32 v206,v220,v221
v_smfmac_f32_16x16x64_bf16 v[52:55],v[28:31],v[168:175],v27
s_or_b32 s83,s83,0x40000
v_add_u32 v1,s48,v1
s_waitcnt lgkmcnt(4)
v_mfma_f32_16x16x32_bf16 v[136:139],a[128:131],v[64:67],v[32:35]
s_add_u32 s93,s92,L_endseg_%=-L_pc_%=
v_exp_f32 v222,v222
v_mfma_f32_16x16x32_bf16 v[136:139],a[132:135],v[68:71],v[136:139]
buffer_load_dwordx4 v5,s[20:23],s47 offen offset:2048 nt lds
v_mfma_f32_16x16x32_bf16 v[136:139],a[136:139],v[72:75],v[136:139]
v_exp_f32 v223,v223
v_mfma_f32_16x16x32_bf16 v[136:139],a[140:143],v[76:79],v[136:139]
v_cvt_pk_bf16_f32 v207,v222,v223
v_xor_b32 v25,s44,v4
v_mfma_f32_16x16x32_bf16 v[140:143],a[156:159],v[76:79],v[32:35]
v_exp_f32 v232,v232
v_mfma_f32_16x16x32_bf16 v[140:143],a[152:155],v[72:75],v[140:143]
v_exp_f32 v233,v233
v_mfma_f32_16x16x32_bf16 v[140:143],a[148:151],v[68:71],v[140:143]
v_exp_f32 v234,v234
v_mfma_f32_16x16x32_bf16 v[140:143],a[144:147],v[64:67],v[140:143]
v_exp_f32 v235,v235
v_mfma_f32_16x16x32_bf16 v[152:155],a[160:163],v[64:67],v[32:35]
v_exp_f32 v236,v236
v_mfma_f32_16x16x32_bf16 v[152:155],a[164:167],v[68:71],v[152:155]
v_exp_f32 v237,v237
v_mfma_f32_16x16x32_bf16 v[152:155],a[168:171],v[72:75],v[152:155]
v_exp_f32 v238,v238
v_mfma_f32_16x16x32_bf16 v[152:155],a[172:175],v[76:79],v[152:155]
s_waitcnt lgkmcnt(0)
v_exp_f32 v239,v239
v_mfma_f32_16x16x32_bf16 v[156:159],a[188:191],v[76:79],v[32:35]
v_exp_f32 v248,v248
v_mfma_f32_16x16x32_bf16 v[156:159],a[184:187],v[72:75],v[156:159]
v_exp_f32 v249,v249
v_mfma_f32_16x16x32_bf16 v[156:159],a[180:183],v[68:71],v[156:159]
v_exp_f32 v250,v250
v_mfma_f32_16x16x32_bf16 v[156:159],a[176:179],v[64:67],v[156:159]
buffer_load_dwordx4 v5,s[20:23],s47 offen offset:3072 nt lds
v_mfma_f32_16x16x32_bf16 a[64:67],a[192:195],v[200:203],a[64:67]
v_exp_f32 v251,v251
v_mfma_f32_16x16x32_bf16 a[64:67],a[196:199],v[204:207],a[64:67]
v_exp_f32 v252,v252
v_mfma_f32_16x16x32_bf16 a[68:71],a[204:207],v[204:207],a[68:71]
v_exp_f32 v253,v253
v_mfma_f32_16x16x32_bf16 a[68:71],a[200:203],v[200:203],a[68:71]
v_exp_f32 v254,v254
v_mfma_f32_16x16x32_bf16 a[72:75],a[208:211],v[200:203],a[72:75]
v_exp_f32 v255,v255
v_mfma_f32_16x16x32_bf16 a[72:75],a[212:215],v[204:207],a[72:75]
v_exp_f32 v136,v136
v_mfma_f32_16x16x32_bf16 a[76:79],a[220:223],v[204:207],a[76:79]
v_exp_f32 v137,v137
v_mfma_f32_16x16x32_bf16 a[76:79],a[216:219],v[200:203],a[76:79]
v_cvt_pk_bf16_f32 v232,v232,v233
v_cvt_pk_bf16_f32 v233,v234,v235
v_mfma_f32_16x16x32_bf16 a[80:83],a[224:227],v[200:203],a[80:83]
v_cvt_pk_bf16_f32 v234,v236,v237
v_cvt_pk_bf16_f32 v235,v238,v239
v_mfma_f32_16x16x32_bf16 a[80:83],a[228:231],v[204:207],a[80:83]
v_cvt_pk_bf16_f32 v236,v248,v249
v_cvt_pk_bf16_f32 v237,v250,v251
v_mfma_f32_16x16x32_bf16 a[84:87],a[236:239],v[204:207],a[84:87]
s_addk_i32 m0,16384
v_cvt_pk_bf16_f32 v238,v252,v253
v_cvt_pk_bf16_f32 v239,v254,v255
v_mfma_f32_16x16x32_bf16 a[84:87],a[232:235],v[200:203],a[84:87]
v_exp_f32 v138,v138
v_mfma_f32_16x16x32_bf16 a[88:91],a[240:243],v[200:203],a[88:91]
v_exp_f32 v139,v139
v_mfma_f32_16x16x32_bf16 a[88:91],a[244:247],v[204:207],a[88:91]
buffer_load_dwordx4 v4,s[24:27],s47 offen nt lds
v_mfma_f32_16x16x32_bf16 a[92:95],a[252:255],v[204:207],a[92:95]
v_exp_f32 v140,v140
v_mfma_f32_16x16x32_bf16 a[92:95],a[248:251],v[200:203],a[92:95]
v_exp_f32 v141,v141
v_smfmac_f32_16x16x64_bf16 v[56:59],v[28:31],v[200:207],v27
v_cvt_pk_bf16_f32 v136,v136,v137
v_mfma_f32_16x16x32_bf16 v[168:171],a[128:131],v[80:83],v[36:39]
v_exp_f32 v142,v142
v_mfma_f32_16x16x32_bf16 v[168:171],a[132:135],v[84:87],v[168:171]
v_exp_f32 v143,v143
v_mfma_f32_16x16x32_bf16 v[168:171],a[136:139],v[88:91],v[168:171]
v_exp_f32 v152,v152
v_mfma_f32_16x16x32_bf16 v[168:171],a[140:143],v[92:95],v[168:171]
v_exp_f32 v153,v153
v_mfma_f32_16x16x32_bf16 v[172:175],a[156:159],v[92:95],v[36:39]
v_exp_f32 v154,v154
v_mfma_f32_16x16x32_bf16 v[172:175],a[152:155],v[88:91],v[172:175]
v_exp_f32 v155,v155
v_mfma_f32_16x16x32_bf16 v[172:175],a[148:151],v[84:87],v[172:175]
v_exp_f32 v156,v156
v_mfma_f32_16x16x32_bf16 v[172:175],a[144:147],v[80:83],v[172:175]
v_exp_f32 v157,v157
v_mfma_f32_16x16x32_bf16 v[184:187],a[160:163],v[80:83],v[36:39]
v_exp_f32 v158,v158
v_mfma_f32_16x16x32_bf16 v[184:187],a[164:167],v[84:87],v[184:187]
v_exp_f32 v159,v159
v_mfma_f32_16x16x32_bf16 v[184:187],a[168:171],v[88:91],v[184:187]
buffer_load_dwordx4 v5,s[24:27],s47 offen offset:1024 nt lds
v_mfma_f32_16x16x32_bf16 v[184:187],a[172:175],v[92:95],v[184:187]
v_exp_f32 v168,v168
v_mfma_f32_16x16x32_bf16 v[188:191],a[188:191],v[92:95],v[36:39]
v_exp_f32 v169,v169
v_mfma_f32_16x16x32_bf16 v[188:191],a[184:187],v[88:91],v[188:191]
v_cvt_pk_bf16_f32 v137,v138,v139
v_cvt_pk_bf16_f32 v138,v140,v141
v_mfma_f32_16x16x32_bf16 v[188:191],a[180:183],v[84:87],v[188:191]
s_waitcnt vmcnt(10)
v_cvt_pk_bf16_f32 v139,v142,v143
v_cvt_pk_bf16_f32 v140,v152,v153
v_mfma_f32_16x16x32_bf16 v[188:191],a[176:179],v[80:83],v[188:191]
s_barrier
ds_read_b128 v[80:83],v18
v_cvt_pk_bf16_f32 v141,v154,v155
v_mfma_f32_16x16x32_bf16 a[96:99],a[192:195],v[232:235],a[96:99]
ds_read_b64_tr_b16 a[192:193],v2
v_cvt_pk_bf16_f32 v142,v156,v157
v_mfma_f32_16x16x32_bf16 a[96:99],a[196:199],v[236:239],a[96:99]
ds_read_b64_tr_b16 a[194:195],v2 offset:4096
v_cvt_pk_bf16_f32 v143,v158,v159
v_mfma_f32_16x16x32_bf16 a[100:103],a[204:207],v[236:239],a[100:103]
ds_read_b64_tr_b16 a[196:197],v2 offset:8192
v_cvt_pk_bf16_f32 v168,v168,v169
v_mfma_f32_16x16x32_bf16 a[100:103],a[200:203],v[232:235],a[100:103]
v_exp_f32 v170,v170
v_mfma_f32_16x16x32_bf16 a[104:107],a[208:211],v[232:235],a[104:107]
v_exp_f32 v171,v171
v_mfma_f32_16x16x32_bf16 a[104:107],a[212:215],v[236:239],a[104:107]
)A"
R"A(ds_read_b128 v[84:87],v18 offset:256
v_cvt_pk_bf16_f32 v169,v170,v171
v_mfma_f32_16x16x32_bf16 a[108:111],a[220:223],v[236:239],a[108:111]
v_exp_f32 v172,v172
v_mfma_f32_16x16x32_bf16 a[108:111],a[216:219],v[232:235],a[108:111]
buffer_load_dwordx4 v4,s[24:27],s47 offen offset:2048 nt lds
v_mfma_f32_16x16x32_bf16 a[112:115],a[224:227],v[232:235],a[112:115]
v_exp_f32 v173,v173
v_mfma_f32_16x16x32_bf16 a[112:115],a[228:231],v[236:239],a[112:115]
ds_read_b128 v[88:91],v18 offset:512
v_cvt_pk_bf16_f32 v170,v172,v173
v_mfma_f32_16x16x32_bf16 a[116:119],a[236:239],v[236:239],a[116:119]
v_exp_f32 v174,v174
v_mfma_f32_16x16x32_bf16 a[116:119],a[232:235],v[232:235],a[116:119]
ds_read_b128 v[92:95],v18 offset:768
v_mfma_f32_16x16x32_bf16 a[120:123],a[240:243],v[232:235],a[120:123]
v_exp_f32 v175,v175
v_mfma_f32_16x16x32_bf16 a[120:123],a[244:247],v[236:239],a[120:123]
ds_read_b64_tr_b16 a[198:199],v2 offset:12288
ds_read_b64_tr_b16 a[204:205],v3 offset:8192
v_mfma_f32_16x16x32_bf16 a[124:127],a[252:255],v[236:239],a[124:127]
ds_read_b64_tr_b16 a[206:207],v3 offset:12288
v_cvt_pk_bf16_f32 v171,v174,v175
v_mfma_f32_16x16x32_bf16 a[124:127],a[248:251],v[232:235],a[124:127]
v_exp_f32 v184,v184
v_smfmac_f32_16x16x64_bf16 v[60:63],v[28:31],v[232:239],v27
ds_read_b64_tr_b16 a[200:201],v3
v_mfma_f32_16x16x32_bf16 v[200:203],a[128:131],v[96:99],v[40:43]
ds_read_b64_tr_b16 a[202:203],v3 offset:4096
ds_read_b64_tr_b16 a[208:209],v2 offset:256
v_mfma_f32_16x16x32_bf16 v[200:203],a[132:135],v[100:103],v[200:203]
v_exp_f32 v185,v185
v_mfma_f32_16x16x32_bf16 v[200:203],a[136:139],v[104:107],v[200:203]
ds_read_b64_tr_b16 a[210:211],v2 offset:4352
ds_read_b64_tr_b16 a[212:213],v2 offset:8448
v_mfma_f32_16x16x32_bf16 v[200:203],a[140:143],v[108:111],v[200:203]
ds_read_b64_tr_b16 a[214:215],v2 offset:12544
v_cvt_pk_bf16_f32 v172,v184,v185
v_mfma_f32_16x16x32_bf16 v[204:207],a[156:159],v[108:111],v[40:43]
buffer_load_dwordx4 v5,s[24:27],s47 offen offset:3072 nt lds
v_mfma_f32_16x16x32_bf16 v[204:207],a[152:155],v[104:107],v[204:207]
s_or_b32 s47,s47,0x18000
ds_read_b64_tr_b16 a[220:221],v3 offset:8448
ds_read_b64_tr_b16 a[222:223],v3 offset:12544
v_mfma_f32_16x16x32_bf16 v[204:207],a[148:151],v[100:103],v[204:207]
s_addk_i32 s47,4096
v_exp_f32 v186,v186
v_mfma_f32_16x16x32_bf16 v[204:207],a[144:147],v[96:99],v[204:207]
s_and_b32 s47,s47,0x27000
ds_read_b64_tr_b16 a[216:217],v3 offset:256
ds_read_b64_tr_b16 a[218:219],v3 offset:4352
v_mfma_f32_16x16x32_bf16 v[216:219],a[160:163],v[96:99],v[40:43]
s_or_b32 s47,s47,s46
v_exp_f32 v187,v187
v_mfma_f32_16x16x32_bf16 v[216:219],a[164:167],v[100:103],v[216:219]
s_and_b32 s46,s54,0xfffc0000
ds_read_b64_tr_b16 a[224:225],v2 offset:512
ds_read_b64_tr_b16 a[226:227],v2 offset:4608
v_mfma_f32_16x16x32_bf16 v[216:219],a[168:171],v[104:107],v[216:219]
s_addk_i32 s54,16384
ds_read_b64_tr_b16 a[228:229],v2 offset:8704
v_cvt_pk_bf16_f32 v173,v186,v187
v_mfma_f32_16x16x32_bf16 v[216:219],a[172:175],v[108:111],v[216:219]
ds_read_b64_tr_b16 a[230:231],v2 offset:12800
v_mfma_f32_16x16x32_bf16 v[220:223],a[188:191],v[108:111],v[40:43]
ds_read_b64_tr_b16 a[236:237],v3 offset:8704
v_mfma_f32_16x16x32_bf16 v[220:223],a[184:187],v[104:107],v[220:223]
ds_read_b64_tr_b16 a[238:239],v3 offset:12800
ds_read_b64_tr_b16 a[232:233],v3 offset:512
v_mfma_f32_16x16x32_bf16 v[220:223],a[180:183],v[100:103],v[220:223]
ds_read_b64_tr_b16 a[234:235],v3 offset:4608
v_mfma_f32_16x16x32_bf16 v[220:223],a[176:179],v[96:99],v[220:223]
s_waitcnt lgkmcnt(8)
ds_read_b64_tr_b16 a[240:241],v2 offset:768
v_mfma_f32_16x16x32_bf16 v[32:35],v[80:83],v[128:131],0
ds_read_b64_tr_b16 a[242:243],v2 offset:4864
v_mfma_f32_16x16x32_bf16 v[32:35],v[84:87],v[160:163],v[32:35]
ds_read_b64_tr_b16 a[244:245],v2 offset:8960
ds_read_b64_tr_b16 a[246:247],v2 offset:13056
v_mfma_f32_16x16x32_bf16 v[32:35],v[88:91],v[192:195],v[32:35]
ds_read_b64_tr_b16 a[252:253],v3 offset:8960
v_add_u32 v2,s48,v2
v_mfma_f32_16x16x32_bf16 v[32:35],v[92:95],v[224:227],v[32:35]
ds_read_b64_tr_b16 a[254:255],v3 offset:13056
v_mfma_f32_16x16x32_bf16 v[36:39],v[80:83],v[132:135],0
ds_read_b64_tr_b16 a[248:249],v3 offset:768
v_mfma_f32_16x16x32_bf16 v[36:39],v[84:87],v[164:167],v[36:39]
ds_read_b64_tr_b16 a[250:251],v3 offset:4864
v_add_u32 v3,s48,v3
v_mfma_f32_16x16x32_bf16 v[36:39],v[88:91],v[196:199],v[36:39]
s_mov_b32 s48,-98304
v_exp_f32 v188,v188
v_mfma_f32_16x16x32_bf16 v[36:39],v[92:95],v[228:231],v[36:39]
v_exp_f32 v189,v189
v_mfma_f32_16x16x32_bf16 v[40:43],v[80:83],v[144:147],0
ds_read_b128 v[20:23],v18 offset:16384
v_cvt_pk_bf16_f32 v174,v188,v189
v_mfma_f32_16x16x32_bf16 v[40:43],v[84:87],v[176:179],v[40:43]
v_exp_f32 v190,v190
v_mfma_f32_16x16x32_bf16 v[40:43],v[88:91],v[208:211],v[40:43]
v_exp_f32 v191,v191
v_mfma_f32_16x16x32_bf16 v[40:43],v[92:95],v[240:243],v[40:43]
v_cvt_pk_bf16_f32 v175,v190,v191
v_max3_f32 v32,v32,v33,v34
v_mfma_f32_16x16x32_bf16 a[0:3],a[192:195],v[136:139],a[0:3]
v_exp_f32 v200,v200
v_mfma_f32_16x16x32_bf16 a[0:3],a[196:199],v[140:143],a[0:3]
v_max3_f32 v32,v32,v35,s18
v_max3_f32 v36,v36,v37,v38
v_mfma_f32_16x16x32_bf16 a[4:7],a[204:207],v[140:143],a[4:7]
v_exp_f32 v201,v201
v_mfma_f32_16x16x32_bf16 a[4:7],a[200:203],v[136:139],a[4:7]
s_memrealtime s[76:77]
v_exp_f32 v202,v202
v_mfma_f32_16x16x32_bf16 a[8:11],a[208:211],v[136:139],a[8:11]
v_exp_f32 v203,v203
v_mfma_f32_16x16x32_bf16 a[8:11],a[212:215],v[140:143],a[8:11]
s_waitcnt lgkmcnt(0)
v_and_b32 v20,0x7fff7fff,v20
v_and_b32 v21,0x7fff7fff,v21
v_mfma_f32_16x16x32_bf16 a[12:15],a[220:223],v[140:143],a[12:15]
v_sad_u16 v20,v20,0,0
v_and_b32 v22,0x7fff7fff,v22
v_mfma_f32_16x16x32_bf16 a[12:15],a[216:219],v[136:139],a[12:15]
v_sad_u16 v20,v21,0,v20
v_and_b32 v23,0x7fff7fff,v23
v_mfma_f32_16x16x32_bf16 a[16:19],a[224:227],v[136:139],a[16:19]
v_sad_u16 v20,v22,0,v20
v_sad_u16 v20,v23,0,v20
v_mfma_f32_16x16x32_bf16 a[16:19],a[228:231],v[140:143],a[16:19]
s_nop 1
v_add_u32_dpp v20,v20,v20 quad_perm:[1,0,3,2] row_mask:0xf bank_mask:0xf
v_mfma_f32_16x16x32_bf16 a[20:23],a[236:239],v[140:143],a[20:23]
s_waitcnt lgkmcnt(0)
s_nop 1
v_add_u32_dpp v20,v20,v20 quad_perm:[2,3,0,1] row_mask:0xf bank_mask:0xf
v_mfma_f32_16x16x32_bf16 a[20:23],a[232:235],v[136:139],a[20:23]
s_sub_u32 s79,s78,s76
v_cvt_f32_u32 v20,v20
v_mov_b32 v21,0xb9800000
v_mfma_f32_16x16x32_bf16 a[24:27],a[240:243],v[136:139],a[24:27]
s_add_u32 s78,s76,s60
v_fmaak_f32 v20,v20,v21,0x42e8c937
v_cvt_pk_bf16_f32 v200,v200,v201
v_mfma_f32_16x16x32_bf16 a[24:27],a[244:247],v[140:143],a[24:27]
s_cmp_gt_i32 s79,s95
s_cbranch_scc1 L_ps4_%=
L_pr4_%=:
v_cvt_pk_bf16_f32 v201,v202,v203
v_mfma_f32_16x16x32_bf16 a[28:31],a[252:255],v[140:143],a[28:31]
s_lshr_b32 s60,s94,2
s_nop 0
v_readfirstlane_b32 s52,v20
v_mfma_f32_16x16x32_bf16 a[28:31],a[248:251],v[136:139],a[28:31]
s_nop 1
s_min_u32 s52,s52,0xc2200000
v_smfmac_f32_16x16x64_bf16 v[48:51],v[28:31],v[136:143],v27
s_min_i32 s52,s52,0x41a00000
v_sub_f32 v35,s52,v32
v_mfma_f32_16x16x32_bf16 v[232:235],a[128:131],v[112:115],v[44:47]
s_waitcnt vmcnt(9)
s_max_i32 s60,s60,124
v_sub_f32 v34,s52,v32
v_sub_f32 v33,s52,v32
v_mfma_f32_16x16x32_bf16 v[232:235],a[132:135],v[116:119],v[232:235]
s_barrier
ds_read_b128 a[128:131],v1
v_sub_f32 v32,s52,v32
v_mfma_f32_16x16x32_bf16 v[232:235],a[136:139],v[120:123],v[232:235]
s_sub_u32 s94,s94,3
v_exp_f32 v204,v204
v_mfma_f32_16x16x32_bf16 v[232:235],a[140:143],v[124:127],v[232:235]
s_add_u32 m0,32768,s51
ds_read_b128 a[132:135],v1 offset:256
v_max3_f32 v36,v36,v39,s19
v_mfma_f32_16x16x32_bf16 v[236:239],a[156:159],v[124:127],v[44:47]
v_exp_f32 v205,v205
v_mfma_f32_16x16x32_bf16 v[236:239],a[152:155],v[120:123],v[236:239]
ds_read_b128 a[136:139],v1 offset:512
v_cvt_pk_bf16_f32 v202,v204,v205
v_mfma_f32_16x16x32_bf16 v[236:239],a[148:151],v[116:119],v[236:239]
buffer_load_dwordx4 v4,s[20:23],s47 offen nt lds
v_mfma_f32_16x16x32_bf16 v[236:239],a[144:147],v[112:115],v[236:239]
ds_read_b128 a[140:143],v1 offset:768
v_sub_f32 v39,s52,v36
v_mfma_f32_16x16x32_bf16 v[248:251],a[160:163],v[112:115],v[44:47]
v_exp_f32 v206,v206
v_mfma_f32_16x16x32_bf16 v[248:251],a[164:167],v[116:119],v[248:251]
ds_read_b128 a[156:159],v1 offset:4864
v_sub_f32 v38,s52,v36
v_mfma_f32_16x16x32_bf16 v[248:251],a[168:171],v[120:123],v[248:251]
v_exp_f32 v207,v207
v_mfma_f32_16x16x32_bf16 v[248:251],a[172:175],v[124:127],v[248:251]
ds_read_b128 a[152:155],v1 offset:4608
v_cvt_pk_bf16_f32 v203,v206,v207
v_mfma_f32_16x16x32_bf16 v[252:255],a[188:191],v[124:127],v[44:47]
v_exp_f32 v216,v216
v_mfma_f32_16x16x32_bf16 v[252:255],a[184:187],v[120:123],v[252:255]
ds_read_b128 a[148:151],v1 offset:4352
v_sub_f32 v37,s52,v36
v_mfma_f32_16x16x32_bf16 v[252:255],a[180:183],v[116:119],v[252:255]
v_exp_f32 v217,v217
v_mfma_f32_16x16x32_bf16 v[252:255],a[176:179],v[112:115],v[252:255]
ds_read_b128 a[144:147],v1 offset:4096
v_cvt_pk_bf16_f32 v204,v216,v217
v_mfma_f32_16x16x32_bf16 v[44:47],v[80:83],v[148:151],0
v_exp_f32 v218,v218
v_mfma_f32_16x16x32_bf16 v[44:47],v[84:87],v[180:183],v[44:47]
ds_read_b128 a[160:163],v1 offset:8192
v_sub_f32 v36,s52,v36
v_mfma_f32_16x16x32_bf16 v[44:47],v[88:91],v[212:215],v[44:47]
v_exp_f32 v219,v219
v_mfma_f32_16x16x32_bf16 v[44:47],v[92:95],v[244:247],v[44:47]
ds_read_b128 a[164:167],v1 offset:8448
v_cvt_pk_bf16_f32 v205,v218,v219
v_mfma_f32_16x16x32_bf16 a[32:35],a[192:195],v[168:171],a[32:35]
buffer_load_dwordx4 v4,s[20:23],s47 offen offset:1024 nt lds
v_mfma_f32_16x16x32_bf16 a[32:35],a[196:199],v[172:175],a[32:35]
ds_read_b128 a[168:171],v1 offset:8704
v_max3_f32 v40,v40,v41,v42
v_mfma_f32_16x16x32_bf16 a[36:39],a[204:207],v[172:175],a[36:39]
v_exp_f32 v220,v220
v_mfma_f32_16x16x32_bf16 a[36:39],a[200:203],v[168:171],a[36:39]
ds_read_b128 a[172:175],v1 offset:8960
v_max3_f32 v40,v40,v43,s100
v_mfma_f32_16x16x32_bf16 a[40:43],a[208:211],v[168:171],a[40:43]
v_exp_f32 v221,v221
v_mfma_f32_16x16x32_bf16 a[40:43],a[212:215],v[172:175],a[40:43]
ds_read_b128 a[188:191],v1 offset:13056
v_cvt_pk_bf16_f32 v206,v220,v221
v_mfma_f32_16x16x32_bf16 a[44:47],a[220:223],v[172:175],a[44:47]
v_exp_f32 v222,v222
v_mfma_f32_16x16x32_bf16 a[44:47],a[216:219],v[168:171],a[44:47]
ds_read_b128 a[184:187],v1 offset:12800
v_sub_f32 v43,s52,v40
v_mfma_f32_16x16x32_bf16 a[48:51],a[224:227],v[168:171],a[48:51]
v_exp_f32 v223,v223
v_mfma_f32_16x16x32_bf16 a[48:51],a[228:231],v[172:175],a[48:51]
ds_read_b128 a[180:183],v1 offset:12544
v_cvt_pk_bf16_f32 v207,v222,v223
v_mfma_f32_16x16x32_bf16 a[52:55],a[236:239],v[172:175],a[52:55]
v_exp_f32 v232,v232
v_mfma_f32_16x16x32_bf16 a[52:55],a[232:235],v[168:171],a[52:55]
ds_read_b128 a[176:179],v1 offset:12288
v_sub_f32 v42,s52,v40
v_mfma_f32_16x16x32_bf16 a[56:59],a[240:243],v[168:171],a[56:59]
v_exp_f32 v233,v233
v_mfma_f32_16x16x32_bf16 a[56:59],a[244:247],v[172:175],a[56:59]
v_exp_f32 v234,v234
v_mfma_f32_16x16x32_bf16 a[60:63],a[252:255],v[172:175],a[60:63]
buffer_load_dwordx4 v5,s[20:23],s47 offen offset:2048 nt lds
v_mfma_f32_16x16x32_bf16 a[60:63],a[248:251],v[168:171],a[60:63]
v_exp_f32 v235,v235
v_smfmac_f32_16x16x64_bf16 v[52:55],v[28:31],v[168:175],v27
s_waitcnt lgkmcnt(2)
v_cvt_pk_bf16_f32 v232,v232,v233
v_mfma_f32_16x16x32_bf16 v[136:139],a[128:131],v[128:131],v[32:35]
v_exp_f32 v236,v236
v_mfma_f32_16x16x32_bf16 v[136:139],a[132:135],v[160:163],v[136:139]
v_exp_f32 v237,v237
v_mfma_f32_16x16x32_bf16 v[136:139],a[136:139],v[192:195],v[136:139]
v_exp_f32 v238,v238
v_mfma_f32_16x16x32_bf16 v[136:139],a[140:143],v[224:227],v[136:139]
v_exp_f32 v239,v239
v_mfma_f32_16x16x32_bf16 v[140:143],a[156:159],v[224:227],v[32:35]
v_cvt_pk_bf16_f32 v233,v234,v235
v_cvt_pk_bf16_f32 v234,v236,v237
v_mfma_f32_16x16x32_bf16 v[140:143],a[152:155],v[192:195],v[140:143]
v_cvt_pk_bf16_f32 v235,v238,v239
v_sub_f32 v41,s52,v40
v_mfma_f32_16x16x32_bf16 v[140:143],a[148:151],v[160:163],v[140:143]
v_exp_f32 v248,v248
v_mfma_f32_16x16x32_bf16 v[140:143],a[144:147],v[128:131],v[140:143]
v_exp_f32 v249,v249
v_mfma_f32_16x16x32_bf16 v[152:155],a[160:163],v[128:131],v[32:35]
v_exp_f32 v250,v250
v_mfma_f32_16x16x32_bf16 v[152:155],a[164:167],v[160:163],v[152:155]
)A"
R"A(v_exp_f32 v251,v251
v_mfma_f32_16x16x32_bf16 v[152:155],a[168:171],v[192:195],v[152:155]
v_exp_f32 v252,v252
v_mfma_f32_16x16x32_bf16 v[152:155],a[172:175],v[224:227],v[152:155]
buffer_load_dwordx4 v5,s[20:23],s47 offen offset:3072 nt lds
v_mfma_f32_16x16x32_bf16 v[156:159],a[188:191],v[224:227],v[32:35]
v_exp_f32 v253,v253
v_mfma_f32_16x16x32_bf16 v[156:159],a[184:187],v[192:195],v[156:159]
s_waitcnt lgkmcnt(0)
v_exp_f32 v254,v254
v_mfma_f32_16x16x32_bf16 v[156:159],a[180:183],v[160:163],v[156:159]
v_exp_f32 v255,v255
v_mfma_f32_16x16x32_bf16 v[156:159],a[176:179],v[128:131],v[156:159]
v_cvt_pk_bf16_f32 v236,v248,v249
v_cvt_pk_bf16_f32 v237,v250,v251
v_mfma_f32_16x16x32_bf16 a[64:67],a[192:195],v[200:203],a[64:67]
v_cvt_pk_bf16_f32 v238,v252,v253
v_cvt_pk_bf16_f32 v239,v254,v255
v_mfma_f32_16x16x32_bf16 a[64:67],a[196:199],v[204:207],a[64:67]
v_sub_f32 v40,s52,v40
v_add_f32 v48,v49,v48
v_mfma_f32_16x16x32_bf16 a[68:71],a[204:207],v[204:207],a[68:71]
v_exp_f32 v136,v136
v_mfma_f32_16x16x32_bf16 a[68:71],a[200:203],v[200:203],a[68:71]
v_exp_f32 v137,v137
v_mfma_f32_16x16x32_bf16 a[72:75],a[208:211],v[200:203],a[72:75]
v_exp_f32 v138,v138
v_mfma_f32_16x16x32_bf16 a[72:75],a[212:215],v[204:207],a[72:75]
v_exp_f32 v139,v139
v_mfma_f32_16x16x32_bf16 a[76:79],a[220:223],v[204:207],a[76:79]
s_addk_i32 m0,16384
v_max_f32 v20,0x00800000,v48
v_cvt_pk_bf16_f32 v136,v136,v137
v_mfma_f32_16x16x32_bf16 a[76:79],a[216:219],v[200:203],a[76:79]
v_exp_f32 v140,v140
v_mfma_f32_16x16x32_bf16 a[80:83],a[224:227],v[200:203],a[80:83]
v_exp_f32 v141,v141
v_mfma_f32_16x16x32_bf16 a[80:83],a[228:231],v[204:207],a[80:83]
buffer_load_dwordx4 v4,s[24:27],s47 offen nt lds
v_mfma_f32_16x16x32_bf16 a[84:87],a[236:239],v[204:207],a[84:87]
v_exp_f32 v142,v142
v_mfma_f32_16x16x32_bf16 a[84:87],a[232:235],v[200:203],a[84:87]
v_exp_f32 v143,v143
v_mfma_f32_16x16x32_bf16 a[88:91],a[240:243],v[200:203],a[88:91]
v_cvt_pk_bf16_f32 v137,v138,v139
v_cvt_pk_bf16_f32 v138,v140,v141
v_mfma_f32_16x16x32_bf16 a[88:91],a[244:247],v[204:207],a[88:91]
v_rcp_f32 v20,v20
v_mfma_f32_16x16x32_bf16 a[92:95],a[252:255],v[204:207],a[92:95]
v_cvt_pk_bf16_f32 v139,v142,v143
v_mul_f32 v21,0xbe22f983,v20
v_mfma_f32_16x16x32_bf16 a[92:95],a[248:251],v[200:203],a[92:95]
v_exp_f32 v152,v152
v_smfmac_f32_16x16x64_bf16 v[56:59],v[28:31],v[200:207],v27
v_cmp_nlt_f32 s[56:57],v48,s63
v_mfma_f32_16x16x32_bf16 v[168:171],a[128:131],v[132:135],v[36:39]
v_exp_f32 v153,v153
v_mfma_f32_16x16x32_bf16 v[168:171],a[132:135],v[164:167],v[168:171]
v_exp_f32 v154,v154
v_mfma_f32_16x16x32_bf16 v[168:171],a[136:139],v[196:199],v[168:171]
v_exp_f32 v155,v155
v_mfma_f32_16x16x32_bf16 v[168:171],a[140:143],v[228:231],v[168:171]
v_exp_f32 v156,v156
v_mfma_f32_16x16x32_bf16 v[172:175],a[156:159],v[228:231],v[36:39]
v_exp_f32 v157,v157
v_mfma_f32_16x16x32_bf16 v[172:175],a[152:155],v[196:199],v[172:175]
v_exp_f32 v158,v158
v_mfma_f32_16x16x32_bf16 v[172:175],a[148:151],v[164:167],v[172:175]
buffer_load_dwordx4 v5,s[24:27],s47 offen offset:1024 nt lds
v_mfma_f32_16x16x32_bf16 v[172:175],a[144:147],v[132:135],v[172:175]
v_exp_f32 v159,v159
v_mfma_f32_16x16x32_bf16 v[184:187],a[160:163],v[132:135],v[36:39]
v_cvt_pk_bf16_f32 v140,v152,v153
v_cvt_pk_bf16_f32 v141,v154,v155
v_mfma_f32_16x16x32_bf16 v[184:187],a[164:167],v[164:167],v[184:187]
v_cvt_pk_bf16_f32 v142,v156,v157
v_cvt_pk_bf16_f32 v143,v158,v159
v_mfma_f32_16x16x32_bf16 v[184:187],a[168:171],v[196:199],v[184:187]
.p2align 3
v_accvgpr_read_b32 v64,a0
v_accvgpr_read_b32 v65,a1
v_accvgpr_read_b32 v66,a2
v_accvgpr_read_b32 v67,a3
v_accvgpr_read_b32 v12,a4
v_accvgpr_read_b32 v13,a5
v_accvgpr_read_b32 v14,a6
v_accvgpr_read_b32 v15,a7
v_pk_fma_f32 v[64:65],v[20:21],v[64:65],v[20:21] op_sel:[0,0,1] op_sel_hi:[0,1,1]
v_pk_fma_f32 v[66:67],v[20:21],v[66:67],v[20:21] op_sel:[0,0,1] op_sel_hi:[0,1,1]
v_pk_fma_f32 v[12:13],v[20:21],v[12:13],v[20:21] op_sel:[0,0,1] op_sel_hi:[0,1,1]
v_pk_fma_f32 v[14:15],v[20:21],v[14:15],v[20:21] op_sel:[0,0,1] op_sel_hi:[0,1,1]
v_cvt_pk_bf16_f32 v64,v64,v65
v_cvt_pk_bf16_f32 v65,v66,v67
v_cvt_pk_bf16_f32 v66,v12,v13
v_cvt_pk_bf16_f32 v67,v14,v15
v_mfma_f32_16x16x32_bf16 v[184:187],a[172:175],v[228:231],v[184:187]
.p2align 3
v_accvgpr_read_b32 v68,a8
v_accvgpr_read_b32 v69,a9
v_accvgpr_read_b32 v70,a10
v_accvgpr_read_b32 v71,a11
v_accvgpr_read_b32 v12,a12
v_accvgpr_read_b32 v13,a13
v_accvgpr_read_b32 v14,a14
v_accvgpr_read_b32 v15,a15
v_pk_fma_f32 v[68:69],v[20:21],v[68:69],v[20:21] op_sel:[0,0,1] op_sel_hi:[0,1,1]
v_pk_fma_f32 v[70:71],v[20:21],v[70:71],v[20:21] op_sel:[0,0,1] op_sel_hi:[0,1,1]
v_pk_fma_f32 v[12:13],v[20:21],v[12:13],v[20:21] op_sel:[0,0,1] op_sel_hi:[0,1,1]
v_pk_fma_f32 v[14:15],v[20:21],v[14:15],v[20:21] op_sel:[0,0,1] op_sel_hi:[0,1,1]
v_cvt_pk_bf16_f32 v68,v68,v69
v_cvt_pk_bf16_f32 v69,v70,v71
v_cvt_pk_bf16_f32 v70,v12,v13
v_cvt_pk_bf16_f32 v71,v14,v15
v_mfma_f32_16x16x32_bf16 v[188:191],a[188:191],v[228:231],v[36:39]
.p2align 3
v_accvgpr_read_b32 v72,a16
v_accvgpr_read_b32 v73,a17
v_accvgpr_read_b32 v74,a18
v_accvgpr_read_b32 v75,a19
v_accvgpr_read_b32 v12,a20
v_accvgpr_read_b32 v13,a21
v_accvgpr_read_b32 v14,a22
v_accvgpr_read_b32 v15,a23
v_pk_fma_f32 v[72:73],v[20:21],v[72:73],v[20:21] op_sel:[0,0,1] op_sel_hi:[0,1,1]
v_pk_fma_f32 v[74:75],v[20:21],v[74:75],v[20:21] op_sel:[0,0,1] op_sel_hi:[0,1,1]
v_pk_fma_f32 v[12:13],v[20:21],v[12:13],v[20:21] op_sel:[0,0,1] op_sel_hi:[0,1,1]
v_pk_fma_f32 v[14:15],v[20:21],v[14:15],v[20:21] op_sel:[0,0,1] op_sel_hi:[0,1,1]
v_cvt_pk_bf16_f32 v72,v72,v73
v_cvt_pk_bf16_f32 v73,v74,v75
v_cvt_pk_bf16_f32 v74,v12,v13
v_cvt_pk_bf16_f32 v75,v14,v15
v_mfma_f32_16x16x32_bf16 v[188:191],a[184:187],v[196:199],v[188:191]
.p2align 3
v_accvgpr_read_b32 v76,a24
v_accvgpr_read_b32 v77,a25
v_accvgpr_read_b32 v78,a26
v_accvgpr_read_b32 v79,a27
v_accvgpr_read_b32 v12,a28
v_accvgpr_read_b32 v13,a29
v_accvgpr_read_b32 v14,a30
v_accvgpr_read_b32 v15,a31
v_pk_fma_f32 v[76:77],v[20:21],v[76:77],v[20:21] op_sel:[0,0,1] op_sel_hi:[0,1,1]
v_pk_fma_f32 v[78:79],v[20:21],v[78:79],v[20:21] op_sel:[0,0,1] op_sel_hi:[0,1,1]
v_pk_fma_f32 v[12:13],v[20:21],v[12:13],v[20:21] op_sel:[0,0,1] op_sel_hi:[0,1,1]
v_pk_fma_f32 v[14:15],v[20:21],v[14:15],v[20:21] op_sel:[0,0,1] op_sel_hi:[0,1,1]
v_cvt_pk_bf16_f32 v76,v76,v77
v_cvt_pk_bf16_f32 v77,v78,v79
v_cvt_pk_bf16_f32 v78,v12,v13
v_cvt_pk_bf16_f32 v79,v14,v15
v_mfma_f32_16x16x32_bf16 v[188:191],a[180:183],v[164:167],v[188:191]
v_max3_f32 v44,v44,v45,v46
v_max3_f32 v44,v44,v47,s101
v_mfma_f32_16x16x32_bf16 v[188:191],a[176:179],v[132:135],v[188:191]
v_mov_b32 v48,0
v_mov_b32 v49,0
v_mfma_f32_16x16x32_bf16 a[96:99],a[192:195],v[232:235],a[96:99]
ds_read_b64_tr_b16 a[192:193],v2
ds_read_b64_tr_b16 a[194:195],v2 offset:4096
v_mfma_f32_16x16x32_bf16 a[96:99],a[196:199],v[236:239],a[96:99]
ds_read_b64_tr_b16 a[196:197],v2 offset:8192
v_mov_b32 v50,0
v_mfma_f32_16x16x32_bf16 a[100:103],a[204:207],v[236:239],a[100:103]
ds_read_b64_tr_b16 a[198:199],v2 offset:12288
v_mov_b32 v51,0
v_mfma_f32_16x16x32_bf16 a[100:103],a[200:203],v[232:235],a[100:103]
ds_read_b64_tr_b16 a[204:205],v3 offset:8192
v_sub_f32 v47,s52,v44
v_mfma_f32_16x16x32_bf16 a[104:107],a[208:211],v[232:235],a[104:107]
buffer_load_dwordx4 v4,s[24:27],s47 offen offset:2048 nt lds
v_mfma_f32_16x16x32_bf16 a[104:107],a[212:215],v[236:239],a[104:107]
ds_read_b64_tr_b16 a[206:207],v3 offset:12288
ds_read_b64_tr_b16 a[200:201],v3
v_mfma_f32_16x16x32_bf16 a[108:111],a[220:223],v[236:239],a[108:111]
ds_read_b64_tr_b16 a[202:203],v3 offset:4096
v_sub_f32 v46,s52,v44
v_mfma_f32_16x16x32_bf16 a[108:111],a[216:219],v[232:235],a[108:111]
ds_read_b64_tr_b16 a[208:209],v2 offset:256
v_sub_f32 v45,s52,v44
v_mfma_f32_16x16x32_bf16 a[112:115],a[224:227],v[232:235],a[112:115]
ds_read_b64_tr_b16 a[210:211],v2 offset:4352
v_sub_f32 v44,s52,v44
v_mfma_f32_16x16x32_bf16 a[112:115],a[228:231],v[236:239],a[112:115]
ds_read_b64_tr_b16 a[212:213],v2 offset:8448
ds_read_b64_tr_b16 a[214:215],v2 offset:12544
v_mfma_f32_16x16x32_bf16 a[116:119],a[236:239],v[236:239],a[116:119]
ds_read_b64_tr_b16 a[220:221],v3 offset:8448
v_add_f32 v52,v53,v52
v_mfma_f32_16x16x32_bf16 a[116:119],a[232:235],v[232:235],a[116:119]
ds_read_b64_tr_b16 a[222:223],v3 offset:12544
v_max_f32 v22,0x00800000,v52
v_mfma_f32_16x16x32_bf16 a[120:123],a[240:243],v[232:235],a[120:123]
ds_read_b64_tr_b16 a[216:217],v3 offset:256
v_add_u32 v1,s48,v1
v_mfma_f32_16x16x32_bf16 a[120:123],a[244:247],v[236:239],a[120:123]
ds_read_b64_tr_b16 a[218:219],v3 offset:4352
ds_read_b64_tr_b16 a[224:225],v2 offset:512
v_mfma_f32_16x16x32_bf16 a[124:127],a[252:255],v[236:239],a[124:127]
ds_read_b64_tr_b16 a[226:227],v2 offset:4608
v_mov_b32 v53,0
v_mfma_f32_16x16x32_bf16 a[124:127],a[248:251],v[232:235],a[124:127]
ds_read_b64_tr_b16 a[228:229],v2 offset:8704
v_mov_b32 v54,0
v_smfmac_f32_16x16x64_bf16 v[60:63],v[28:31],v[232:239],v27
ds_read_b64_tr_b16 a[230:231],v2 offset:12800
v_mfma_f32_16x16x32_bf16 v[200:203],a[128:131],v[144:147],v[40:43]
ds_read_b64_tr_b16 a[236:237],v3 offset:8704
ds_read_b64_tr_b16 a[238:239],v3 offset:12800
v_mfma_f32_16x16x32_bf16 v[200:203],a[132:135],v[176:179],v[200:203]
buffer_load_dwordx4 v5,s[24:27],s47 offen offset:3072 nt lds
v_mfma_f32_16x16x32_bf16 v[200:203],a[136:139],v[208:211],v[200:203]
s_or_b32 s47,s47,0x18000
ds_read_b64_tr_b16 a[232:233],v3 offset:512
ds_read_b64_tr_b16 a[234:235],v3 offset:4608
v_mfma_f32_16x16x32_bf16 v[200:203],a[140:143],v[240:243],v[200:203]
s_addk_i32 s47,4096
s_and_b32 s47,s47,0x27000
ds_read_b64_tr_b16 a[240:241],v2 offset:768
v_mfma_f32_16x16x32_bf16 v[204:207],a[156:159],v[240:243],v[40:43]
s_or_b32 s47,s47,s46
ds_read_b64_tr_b16 a[242:243],v2 offset:4864
v_mov_b32 v55,0
v_mfma_f32_16x16x32_bf16 v[204:207],a[152:155],v[208:211],v[204:207]
s_and_b32 s46,s54,0xfffc0000
ds_read_b64_tr_b16 a[244:245],v2 offset:8960
v_add_f32 v56,v57,v56
v_mfma_f32_16x16x32_bf16 v[204:207],a[148:151],v[176:179],v[204:207]
ds_read_b64_tr_b16 a[246:247],v2 offset:13056
ds_read_b64_tr_b16 a[252:253],v3 offset:8960
v_mfma_f32_16x16x32_bf16 v[204:207],a[144:147],v[144:147],v[204:207]
ds_read_b64_tr_b16 a[254:255],v3 offset:13056
v_max_f32 v20,0x00800000,v56
v_mfma_f32_16x16x32_bf16 v[216:219],a[160:163],v[144:147],v[40:43]
ds_read_b64_tr_b16 a[248:249],v3 offset:768
v_max3_f32 v18,|v64|,|v65|,|v66|
v_mfma_f32_16x16x32_bf16 v[216:219],a[164:167],v[176:179],v[216:219]
ds_read_b64_tr_b16 a[250:251],v3 offset:4864
v_max3_f32 v18,v18,|v67|,s18
v_mfma_f32_16x16x32_bf16 v[216:219],a[168:171],v[208:211],v[216:219]
v_exp_f32 v168,v168
v_mfma_f32_16x16x32_bf16 v[216:219],a[172:175],v[240:243],v[216:219]
v_exp_f32 v169,v169
v_mfma_f32_16x16x32_bf16 v[220:223],a[188:191],v[240:243],v[40:43]
v_exp_f32 v170,v170
v_mfma_f32_16x16x32_bf16 v[220:223],a[184:187],v[208:211],v[220:223]
v_exp_f32 v171,v171
v_mfma_f32_16x16x32_bf16 v[220:223],a[180:183],v[176:179],v[220:223]
v_cvt_pk_bf16_f32 v168,v168,v169
v_cvt_pk_bf16_f32 v169,v170,v171
v_mfma_f32_16x16x32_bf16 v[220:223],a[176:179],v[144:147],v[220:223]
s_waitcnt lgkmcnt(2)
v_exp_f32 v172,v172
v_mfma_f32_16x16x32_bf16 a[0:3],a[192:195],v[136:139],0.15915494
v_exp_f32 v173,v173
v_mfma_f32_16x16x32_bf16 a[0:3],a[196:199],v[140:143],a[0:3]
v_exp_f32 v174,v174
v_mfma_f32_16x16x32_bf16 a[4:7],a[204:207],v[140:143],0.15915494
v_exp_f32 v175,v175
v_mfma_f32_16x16x32_bf16 a[4:7],a[200:203],v[136:139],a[4:7]
s_memrealtime s[76:77]
v_rcp_f32 v22,v22
v_mfma_f32_16x16x32_bf16 a[8:11],a[208:211],v[136:139],0.15915494
v_cvt_pk_bf16_f32 v170,v172,v173
v_cvt_pk_bf16_f32 v171,v174,v175
v_mfma_f32_16x16x32_bf16 a[8:11],a[212:215],v[140:143],a[8:11]
v_exp_f32 v184,v184
v_mfma_f32_16x16x32_bf16 a[12:15],a[220:223],v[140:143],0.15915494
v_exp_f32 v185,v185
v_mfma_f32_16x16x32_bf16 a[12:15],a[216:219],v[136:139],a[12:15]
v_exp_f32 v186,v186
v_mfma_f32_16x16x32_bf16 a[16:19],a[224:227],v[136:139],0.15915494
v_exp_f32 v187,v187
v_mfma_f32_16x16x32_bf16 a[16:19],a[228:231],v[140:143],a[16:19]
v_exp_f32 v188,v188
v_mfma_f32_16x16x32_bf16 a[20:23],a[236:239],v[140:143],0.15915494
s_waitcnt lgkmcnt(0)
)A"
R"A(v_exp_f32 v189,v189
v_mfma_f32_16x16x32_bf16 a[20:23],a[232:235],v[136:139],a[20:23]
s_sub_u32 s79,s78,s76
v_exp_f32 v190,v190
v_mfma_f32_16x16x32_bf16 a[24:27],a[240:243],v[136:139],0.15915494
s_add_u32 s78,s76,s60
v_exp_f32 v191,v191
v_mfma_f32_16x16x32_bf16 a[24:27],a[244:247],v[140:143],a[24:27]
s_cmp_gt_i32 s79,s95
s_cbranch_scc1 L_ps5_%=
L_pr5_%=:
v_mul_f32 v23,0xbe22f983,v22
v_mfma_f32_16x16x32_bf16 a[28:31],a[252:255],v[140:143],0.15915494
s_lshr_b32 s60,s94,2
v_cvt_pk_bf16_f32 v172,v184,v185
v_cvt_pk_bf16_f32 v173,v186,v187
v_mfma_f32_16x16x32_bf16 a[28:31],a[248:251],v[136:139],a[28:31]
s_max_i32 s60,s60,124
v_cvt_pk_bf16_f32 v174,v188,v189
v_cvt_pk_bf16_f32 v175,v190,v191
v_smfmac_f32_16x16x64_bf16 v[48:51],v[28:31],v[136:143],v27
v_fmac_f32 v18,0x39800000,v21
v_mfma_f32_16x16x32_bf16 v[232:235],a[128:131],v[148:151],v[44:47]
s_waitcnt vmcnt(8)
.p2align 3
v_accvgpr_read_b32 v80,a32
v_accvgpr_read_b32 v81,a33
v_accvgpr_read_b32 v82,a34
v_accvgpr_read_b32 v83,a35
v_accvgpr_read_b32 v12,a36
v_accvgpr_read_b32 v13,a37
v_accvgpr_read_b32 v14,a38
v_accvgpr_read_b32 v15,a39
v_pk_fma_f32 v[80:81],v[22:23],v[80:81],v[22:23] op_sel:[0,0,1] op_sel_hi:[0,1,1]
v_pk_fma_f32 v[82:83],v[22:23],v[82:83],v[22:23] op_sel:[0,0,1] op_sel_hi:[0,1,1]
v_pk_fma_f32 v[12:13],v[22:23],v[12:13],v[22:23] op_sel:[0,0,1] op_sel_hi:[0,1,1]
v_pk_fma_f32 v[14:15],v[22:23],v[14:15],v[22:23] op_sel:[0,0,1] op_sel_hi:[0,1,1]
v_cvt_pk_bf16_f32 v80,v80,v81
v_cvt_pk_bf16_f32 v81,v82,v83
v_cvt_pk_bf16_f32 v82,v12,v13
v_cvt_pk_bf16_f32 v83,v14,v15
v_mfma_f32_16x16x32_bf16 v[232:235],a[132:135],v[180:183],v[232:235]
s_barrier
.p2align 3
v_accvgpr_read_b32 v84,a40
v_accvgpr_read_b32 v85,a41
v_accvgpr_read_b32 v86,a42
v_accvgpr_read_b32 v87,a43
v_accvgpr_read_b32 v12,a44
v_accvgpr_read_b32 v13,a45
v_accvgpr_read_b32 v14,a46
v_accvgpr_read_b32 v15,a47
v_pk_fma_f32 v[84:85],v[22:23],v[84:85],v[22:23] op_sel:[0,0,1] op_sel_hi:[0,1,1]
v_pk_fma_f32 v[86:87],v[22:23],v[86:87],v[22:23] op_sel:[0,0,1] op_sel_hi:[0,1,1]
v_pk_fma_f32 v[12:13],v[22:23],v[12:13],v[22:23] op_sel:[0,0,1] op_sel_hi:[0,1,1]
v_pk_fma_f32 v[14:15],v[22:23],v[14:15],v[22:23] op_sel:[0,0,1] op_sel_hi:[0,1,1]
v_cvt_pk_bf16_f32 v84,v84,v85
v_cvt_pk_bf16_f32 v85,v86,v87
v_cvt_pk_bf16_f32 v86,v12,v13
v_cvt_pk_bf16_f32 v87,v14,v15
v_mfma_f32_16x16x32_bf16 v[232:235],a[136:139],v[212:215],v[232:235]
.p2align 3
v_accvgpr_read_b32 v88,a48
v_accvgpr_read_b32 v89,a49
v_accvgpr_read_b32 v90,a50
v_accvgpr_read_b32 v91,a51
v_accvgpr_read_b32 v12,a52
v_accvgpr_read_b32 v13,a53
v_accvgpr_read_b32 v14,a54
v_accvgpr_read_b32 v15,a55
v_pk_fma_f32 v[88:89],v[22:23],v[88:89],v[22:23] op_sel:[0,0,1] op_sel_hi:[0,1,1]
v_pk_fma_f32 v[90:91],v[22:23],v[90:91],v[22:23] op_sel:[0,0,1] op_sel_hi:[0,1,1]
v_pk_fma_f32 v[12:13],v[22:23],v[12:13],v[22:23] op_sel:[0,0,1] op_sel_hi:[0,1,1]
v_pk_fma_f32 v[14:15],v[22:23],v[14:15],v[22:23] op_sel:[0,0,1] op_sel_hi:[0,1,1]
v_cvt_pk_bf16_f32 v88,v88,v89
v_cvt_pk_bf16_f32 v89,v90,v91
v_cvt_pk_bf16_f32 v90,v12,v13
v_cvt_pk_bf16_f32 v91,v14,v15
v_mfma_f32_16x16x32_bf16 v[232:235],a[140:143],v[244:247],v[232:235]
s_add_u32 m0,65536,s51
ds_read_b128 a[128:131],v1
v_mov_b32 v57,0
v_mfma_f32_16x16x32_bf16 v[236:239],a[156:159],v[244:247],v[44:47]
.p2align 3
v_accvgpr_read_b32 v92,a56
v_accvgpr_read_b32 v93,a57
v_accvgpr_read_b32 v94,a58
v_accvgpr_read_b32 v95,a59
v_accvgpr_read_b32 v12,a60
v_accvgpr_read_b32 v13,a61
v_accvgpr_read_b32 v14,a62
v_accvgpr_read_b32 v15,a63
v_pk_fma_f32 v[92:93],v[22:23],v[92:93],v[22:23] op_sel:[0,0,1] op_sel_hi:[0,1,1]
v_pk_fma_f32 v[94:95],v[22:23],v[94:95],v[22:23] op_sel:[0,0,1] op_sel_hi:[0,1,1]
v_pk_fma_f32 v[12:13],v[22:23],v[12:13],v[22:23] op_sel:[0,0,1] op_sel_hi:[0,1,1]
v_pk_fma_f32 v[14:15],v[22:23],v[14:15],v[22:23] op_sel:[0,0,1] op_sel_hi:[0,1,1]
v_cvt_pk_bf16_f32 v92,v92,v93
v_cvt_pk_bf16_f32 v93,v94,v95
v_cvt_pk_bf16_f32 v94,v12,v13
v_cvt_pk_bf16_f32 v95,v14,v15
v_mfma_f32_16x16x32_bf16 v[236:239],a[152:155],v[212:215],v[236:239]
ds_read_b128 a[132:135],v1 offset:256
v_mov_b32 v58,0
v_mfma_f32_16x16x32_bf16 v[236:239],a[148:151],v[180:183],v[236:239]
buffer_load_dwordx4 v4,s[20:23],s47 offen nt lds
v_mfma_f32_16x16x32_bf16 v[236:239],a[144:147],v[148:151],v[236:239]
ds_read_b128 a[136:139],v1 offset:512
v_mov_b32 v59,0
v_mfma_f32_16x16x32_bf16 v[248:251],a[160:163],v[148:151],v[44:47]
v_cmp_nlt_f32 s[58:59],v52,s63
s_nop 1
s_or_b64 s[56:57],s[56:57],s[58:59]
v_mfma_f32_16x16x32_bf16 v[248:251],a[164:167],v[180:183],v[248:251]
ds_read_b128 a[140:143],v1 offset:768
v_mov_b32 v52,0
v_mfma_f32_16x16x32_bf16 v[248:251],a[168:171],v[212:215],v[248:251]
v_exp_f32 v200,v200
v_mfma_f32_16x16x32_bf16 v[248:251],a[172:175],v[244:247],v[248:251]
ds_read_b128 a[156:159],v1 offset:4864
v_add_f32 v60,v61,v60
v_mfma_f32_16x16x32_bf16 v[252:255],a[188:191],v[244:247],v[44:47]
v_exp_f32 v201,v201
v_mfma_f32_16x16x32_bf16 v[252:255],a[184:187],v[212:215],v[252:255]
ds_read_b128 a[152:155],v1 offset:4608
v_cvt_pk_bf16_f32 v200,v200,v201
v_mfma_f32_16x16x32_bf16 v[252:255],a[180:183],v[180:183],v[252:255]
v_exp_f32 v202,v202
v_mfma_f32_16x16x32_bf16 v[252:255],a[176:179],v[148:151],v[252:255]
ds_read_b128 a[148:151],v1 offset:4352
v_max_f32 v22,0x00800000,v60
v_mfma_f32_16x16x32_bf16 a[32:35],a[192:195],v[168:171],0.15915494
v_exp_f32 v203,v203
v_mfma_f32_16x16x32_bf16 a[32:35],a[196:199],v[172:175],a[32:35]
ds_read_b128 a[144:147],v1 offset:4096
v_cvt_pk_bf16_f32 v201,v202,v203
v_mfma_f32_16x16x32_bf16 a[36:39],a[204:207],v[172:175],0.15915494
v_exp_f32 v204,v204
v_mfma_f32_16x16x32_bf16 a[36:39],a[200:203],v[168:171],a[36:39]
ds_read_b128 a[160:163],v1 offset:8192
v_max3_f32 v19,|v80|,|v81|,|v82|
v_mfma_f32_16x16x32_bf16 a[40:43],a[208:211],v[168:171],0.15915494
buffer_load_dwordx4 v4,s[20:23],s47 offen offset:1024 nt lds
v_mfma_f32_16x16x32_bf16 a[40:43],a[212:215],v[172:175],a[40:43]
ds_read_b128 a[164:167],v1 offset:8448
v_max3_f32 v19,v19,|v83|,s19
v_mfma_f32_16x16x32_bf16 a[44:47],a[220:223],v[172:175],0.15915494
v_exp_f32 v205,v205
v_mfma_f32_16x16x32_bf16 a[44:47],a[216:219],v[168:171],a[44:47]
ds_read_b128 a[168:171],v1 offset:8704
v_cvt_pk_bf16_f32 v202,v204,v205
v_mfma_f32_16x16x32_bf16 a[48:51],a[224:227],v[168:171],0.15915494
v_exp_f32 v206,v206
v_mfma_f32_16x16x32_bf16 a[48:51],a[228:231],v[172:175],a[48:51]
ds_read_b128 a[172:175],v1 offset:8960
v_fmac_f32 v19,0x39800000,v23
v_mfma_f32_16x16x32_bf16 a[52:55],a[236:239],v[172:175],0.15915494
v_exp_f32 v207,v207
v_mfma_f32_16x16x32_bf16 a[52:55],a[232:235],v[168:171],a[52:55]
ds_read_b128 a[188:191],v1 offset:13056
v_cvt_pk_bf16_f32 v203,v206,v207
v_mfma_f32_16x16x32_bf16 a[56:59],a[240:243],v[168:171],0.15915494
v_rcp_f32 v20,v20
v_mfma_f32_16x16x32_bf16 a[56:59],a[244:247],v[172:175],a[56:59]
ds_read_b128 a[184:187],v1 offset:12800
v_mul_f32 v21,0xbe22f983,v20
v_mfma_f32_16x16x32_bf16 a[60:63],a[252:255],v[172:175],0.15915494
v_exp_f32 v216,v216
v_mfma_f32_16x16x32_bf16 a[60:63],a[248:251],v[168:171],a[60:63]
ds_read_b128 a[180:183],v1 offset:12544
v_mov_b32 v61,0
v_smfmac_f32_16x16x64_bf16 v[52:55],v[28:31],v[168:175],v27
s_waitcnt lgkmcnt(4)
v_mov_b32 v62,0
v_mfma_f32_16x16x32_bf16 v[136:139],a[128:131],v[128:131],v[32:35]
ds_read_b128 a[176:179],v1 offset:12288
v_mov_b32 v63,0
v_mfma_f32_16x16x32_bf16 v[136:139],a[132:135],v[160:163],v[136:139]
buffer_load_dwordx4 v5,s[20:23],s47 offen offset:2048 nt lds
v_mfma_f32_16x16x32_bf16 v[136:139],a[136:139],v[192:195],v[136:139]
v_exp_f32 v217,v217
v_mfma_f32_16x16x32_bf16 v[136:139],a[140:143],v[224:227],v[136:139]
v_exp_f32 v218,v218
v_mfma_f32_16x16x32_bf16 v[140:143],a[156:159],v[224:227],v[32:35]
v_exp_f32 v219,v219
v_mfma_f32_16x16x32_bf16 v[140:143],a[152:155],v[192:195],v[140:143]
v_exp_f32 v220,v220
v_mfma_f32_16x16x32_bf16 v[140:143],a[148:151],v[160:163],v[140:143]
v_exp_f32 v221,v221
v_mfma_f32_16x16x32_bf16 v[140:143],a[144:147],v[128:131],v[140:143]
v_exp_f32 v222,v222
v_mfma_f32_16x16x32_bf16 v[152:155],a[160:163],v[128:131],v[32:35]
v_exp_f32 v223,v223
v_mfma_f32_16x16x32_bf16 v[152:155],a[164:167],v[160:163],v[152:155]
v_cvt_pk_bf16_f32 v204,v216,v217
v_cvt_pk_bf16_f32 v205,v218,v219
v_mfma_f32_16x16x32_bf16 v[152:155],a[168:171],v[192:195],v[152:155]
s_waitcnt lgkmcnt(0)
v_cvt_pk_bf16_f32 v206,v220,v221
v_cvt_pk_bf16_f32 v207,v222,v223
v_mfma_f32_16x16x32_bf16 v[152:155],a[172:175],v[224:227],v[152:155]
.p2align 3
v_accvgpr_read_b32 v96,a64
v_accvgpr_read_b32 v97,a65
v_accvgpr_read_b32 v98,a66
v_accvgpr_read_b32 v99,a67
v_accvgpr_read_b32 v12,a68
v_accvgpr_read_b32 v13,a69
v_accvgpr_read_b32 v14,a70
v_accvgpr_read_b32 v15,a71
v_pk_fma_f32 v[96:97],v[20:21],v[96:97],v[20:21] op_sel:[0,0,1] op_sel_hi:[0,1,1]
v_pk_fma_f32 v[98:99],v[20:21],v[98:99],v[20:21] op_sel:[0,0,1] op_sel_hi:[0,1,1]
v_pk_fma_f32 v[12:13],v[20:21],v[12:13],v[20:21] op_sel:[0,0,1] op_sel_hi:[0,1,1]
v_pk_fma_f32 v[14:15],v[20:21],v[14:15],v[20:21] op_sel:[0,0,1] op_sel_hi:[0,1,1]
v_cvt_pk_bf16_f32 v96,v96,v97
v_cvt_pk_bf16_f32 v97,v98,v99
v_cvt_pk_bf16_f32 v98,v12,v13
v_cvt_pk_bf16_f32 v99,v14,v15
v_mfma_f32_16x16x32_bf16 v[156:159],a[188:191],v[224:227],v[32:35]
.p2align 3
v_accvgpr_read_b32 v100,a72
v_accvgpr_read_b32 v101,a73
v_accvgpr_read_b32 v102,a74
v_accvgpr_read_b32 v103,a75
v_accvgpr_read_b32 v12,a76
v_accvgpr_read_b32 v13,a77
v_accvgpr_read_b32 v14,a78
v_accvgpr_read_b32 v15,a79
v_pk_fma_f32 v[100:101],v[20:21],v[100:101],v[20:21] op_sel:[0,0,1] op_sel_hi:[0,1,1]
v_pk_fma_f32 v[102:103],v[20:21],v[102:103],v[20:21] op_sel:[0,0,1] op_sel_hi:[0,1,1]
v_pk_fma_f32 v[12:13],v[20:21],v[12:13],v[20:21] op_sel:[0,0,1] op_sel_hi:[0,1,1]
v_pk_fma_f32 v[14:15],v[20:21],v[14:15],v[20:21] op_sel:[0,0,1] op_sel_hi:[0,1,1]
v_cvt_pk_bf16_f32 v100,v100,v101
v_cvt_pk_bf16_f32 v101,v102,v103
v_cvt_pk_bf16_f32 v102,v12,v13
v_cvt_pk_bf16_f32 v103,v14,v15
v_mfma_f32_16x16x32_bf16 v[156:159],a[184:187],v[192:195],v[156:159]
.p2align 3
v_accvgpr_read_b32 v104,a80
v_accvgpr_read_b32 v105,a81
v_accvgpr_read_b32 v106,a82
v_accvgpr_read_b32 v107,a83
v_accvgpr_read_b32 v12,a84
v_accvgpr_read_b32 v13,a85
v_accvgpr_read_b32 v14,a86
v_accvgpr_read_b32 v15,a87
v_pk_fma_f32 v[104:105],v[20:21],v[104:105],v[20:21] op_sel:[0,0,1] op_sel_hi:[0,1,1]
v_pk_fma_f32 v[106:107],v[20:21],v[106:107],v[20:21] op_sel:[0,0,1] op_sel_hi:[0,1,1]
v_pk_fma_f32 v[12:13],v[20:21],v[12:13],v[20:21] op_sel:[0,0,1] op_sel_hi:[0,1,1]
v_pk_fma_f32 v[14:15],v[20:21],v[14:15],v[20:21] op_sel:[0,0,1] op_sel_hi:[0,1,1]
v_cvt_pk_bf16_f32 v104,v104,v105
v_cvt_pk_bf16_f32 v105,v106,v107
v_cvt_pk_bf16_f32 v106,v12,v13
v_cvt_pk_bf16_f32 v107,v14,v15
v_mfma_f32_16x16x32_bf16 v[156:159],a[180:183],v[160:163],v[156:159]
.p2align 3
v_accvgpr_read_b32 v108,a88
v_accvgpr_read_b32 v109,a89
v_accvgpr_read_b32 v110,a90
v_accvgpr_read_b32 v111,a91
v_accvgpr_read_b32 v12,a92
v_accvgpr_read_b32 v13,a93
v_accvgpr_read_b32 v14,a94
v_accvgpr_read_b32 v15,a95
v_pk_fma_f32 v[108:109],v[20:21],v[108:109],v[20:21] op_sel:[0,0,1] op_sel_hi:[0,1,1]
v_pk_fma_f32 v[110:111],v[20:21],v[110:111],v[20:21] op_sel:[0,0,1] op_sel_hi:[0,1,1]
v_pk_fma_f32 v[12:13],v[20:21],v[12:13],v[20:21] op_sel:[0,0,1] op_sel_hi:[0,1,1]
v_pk_fma_f32 v[14:15],v[20:21],v[14:15],v[20:21] op_sel:[0,0,1] op_sel_hi:[0,1,1]
v_cvt_pk_bf16_f32 v108,v108,v109
v_cvt_pk_bf16_f32 v109,v110,v111
v_cvt_pk_bf16_f32 v110,v12,v13
v_cvt_pk_bf16_f32 v111,v14,v15
v_mfma_f32_16x16x32_bf16 v[156:159],a[176:179],v[128:131],v[156:159]
buffer_load_dwordx4 v5,s[20:23],s47 offen offset:3072 nt lds
v_mfma_f32_16x16x32_bf16 a[64:67],a[192:195],v[200:203],0.15915494
v_cmp_nlt_f32 s[58:59],v56,s63
s_nop 1
s_or_b64 s[56:57],s[56:57],s[58:59]
v_mfma_f32_16x16x32_bf16 a[64:67],a[196:199],v[204:207],a[64:67]
v_mov_b32 v56,0
v_add_u32 v2,s48,v2
v_mfma_f32_16x16x32_bf16 a[68:71],a[204:207],v[204:207],0.15915494
v_exp_f32 v232,v232
v_mfma_f32_16x16x32_bf16 a[68:71],a[200:203],v[200:203],a[68:71]
v_exp_f32 v233,v233
v_mfma_f32_16x16x32_bf16 a[72:75],a[208:211],v[200:203],0.15915494
v_exp_f32 v234,v234
v_mfma_f32_16x16x32_bf16 a[72:75],a[212:215],v[204:207],a[72:75]
)A"
R"A(v_exp_f32 v235,v235
v_mfma_f32_16x16x32_bf16 a[76:79],a[220:223],v[204:207],0.15915494
v_cvt_pk_bf16_f32 v232,v232,v233
v_cvt_pk_bf16_f32 v233,v234,v235
v_mfma_f32_16x16x32_bf16 a[76:79],a[216:219],v[200:203],a[76:79]
v_exp_f32 v236,v236
v_mfma_f32_16x16x32_bf16 a[80:83],a[224:227],v[200:203],0.15915494
v_exp_f32 v237,v237
v_mfma_f32_16x16x32_bf16 a[80:83],a[228:231],v[204:207],a[80:83]
v_exp_f32 v238,v238
v_mfma_f32_16x16x32_bf16 a[84:87],a[236:239],v[204:207],0.15915494
s_addk_i32 m0,16384
v_exp_f32 v239,v239
v_mfma_f32_16x16x32_bf16 a[84:87],a[232:235],v[200:203],a[84:87]
v_rcp_f32 v22,v22
v_mfma_f32_16x16x32_bf16 a[88:91],a[240:243],v[200:203],0.15915494
v_cvt_pk_bf16_f32 v234,v236,v237
v_cvt_pk_bf16_f32 v235,v238,v239
v_mfma_f32_16x16x32_bf16 a[88:91],a[244:247],v[204:207],a[88:91]
buffer_load_dwordx4 v4,s[24:27],s47 offen nt lds
v_mfma_f32_16x16x32_bf16 a[92:95],a[252:255],v[204:207],0.15915494
v_exp_f32 v248,v248
v_mfma_f32_16x16x32_bf16 a[92:95],a[248:251],v[200:203],a[92:95]
v_exp_f32 v249,v249
v_smfmac_f32_16x16x64_bf16 v[56:59],v[28:31],v[200:207],v27
v_mul_f32 v23,0xbe22f983,v22
v_mfma_f32_16x16x32_bf16 v[168:171],a[128:131],v[132:135],v[36:39]
v_exp_f32 v250,v250
v_mfma_f32_16x16x32_bf16 v[168:171],a[132:135],v[164:167],v[168:171]
v_exp_f32 v251,v251
v_mfma_f32_16x16x32_bf16 v[168:171],a[136:139],v[196:199],v[168:171]
v_exp_f32 v252,v252
v_mfma_f32_16x16x32_bf16 v[168:171],a[140:143],v[228:231],v[168:171]
v_exp_f32 v253,v253
v_mfma_f32_16x16x32_bf16 v[172:175],a[156:159],v[228:231],v[36:39]
v_exp_f32 v254,v254
v_mfma_f32_16x16x32_bf16 v[172:175],a[152:155],v[196:199],v[172:175]
v_exp_f32 v255,v255
v_mfma_f32_16x16x32_bf16 v[172:175],a[148:151],v[164:167],v[172:175]
v_cvt_pk_bf16_f32 v236,v248,v249
v_cvt_pk_bf16_f32 v237,v250,v251
v_mfma_f32_16x16x32_bf16 v[172:175],a[144:147],v[132:135],v[172:175]
v_cvt_pk_bf16_f32 v238,v252,v253
v_cvt_pk_bf16_f32 v239,v254,v255
v_mfma_f32_16x16x32_bf16 v[184:187],a[160:163],v[132:135],v[36:39]
.p2align 3
v_accvgpr_read_b32 v112,a96
v_accvgpr_read_b32 v113,a97
v_accvgpr_read_b32 v114,a98
v_accvgpr_read_b32 v115,a99
v_accvgpr_read_b32 v12,a100
v_accvgpr_read_b32 v13,a101
v_accvgpr_read_b32 v14,a102
v_accvgpr_read_b32 v15,a103
v_pk_fma_f32 v[112:113],v[22:23],v[112:113],v[22:23] op_sel:[0,0,1] op_sel_hi:[0,1,1]
v_pk_fma_f32 v[114:115],v[22:23],v[114:115],v[22:23] op_sel:[0,0,1] op_sel_hi:[0,1,1]
v_pk_fma_f32 v[12:13],v[22:23],v[12:13],v[22:23] op_sel:[0,0,1] op_sel_hi:[0,1,1]
v_pk_fma_f32 v[14:15],v[22:23],v[14:15],v[22:23] op_sel:[0,0,1] op_sel_hi:[0,1,1]
v_cvt_pk_bf16_f32 v112,v112,v113
v_cvt_pk_bf16_f32 v113,v114,v115
v_cvt_pk_bf16_f32 v114,v12,v13
v_cvt_pk_bf16_f32 v115,v14,v15
v_mfma_f32_16x16x32_bf16 v[184:187],a[164:167],v[164:167],v[184:187]
.p2align 3
v_accvgpr_read_b32 v116,a104
v_accvgpr_read_b32 v117,a105
v_accvgpr_read_b32 v118,a106
v_accvgpr_read_b32 v119,a107
v_accvgpr_read_b32 v12,a108
v_accvgpr_read_b32 v13,a109
v_accvgpr_read_b32 v14,a110
v_accvgpr_read_b32 v15,a111
v_pk_fma_f32 v[116:117],v[22:23],v[116:117],v[22:23] op_sel:[0,0,1] op_sel_hi:[0,1,1]
v_pk_fma_f32 v[118:119],v[22:23],v[118:119],v[22:23] op_sel:[0,0,1] op_sel_hi:[0,1,1]
v_pk_fma_f32 v[12:13],v[22:23],v[12:13],v[22:23] op_sel:[0,0,1] op_sel_hi:[0,1,1]
v_pk_fma_f32 v[14:15],v[22:23],v[14:15],v[22:23] op_sel:[0,0,1] op_sel_hi:[0,1,1]
v_cvt_pk_bf16_f32 v116,v116,v117
v_cvt_pk_bf16_f32 v117,v118,v119
v_cvt_pk_bf16_f32 v118,v12,v13
v_cvt_pk_bf16_f32 v119,v14,v15
v_mfma_f32_16x16x32_bf16 v[184:187],a[168:171],v[196:199],v[184:187]
buffer_load_dwordx4 v5,s[24:27],s47 offen offset:1024 nt lds
v_mfma_f32_16x16x32_bf16 v[184:187],a[172:175],v[228:231],v[184:187]
.p2align 3
v_accvgpr_read_b32 v120,a112
v_accvgpr_read_b32 v121,a113
v_accvgpr_read_b32 v122,a114
v_accvgpr_read_b32 v123,a115
v_accvgpr_read_b32 v12,a116
v_accvgpr_read_b32 v13,a117
v_accvgpr_read_b32 v14,a118
v_accvgpr_read_b32 v15,a119
v_pk_fma_f32 v[120:121],v[22:23],v[120:121],v[22:23] op_sel:[0,0,1] op_sel_hi:[0,1,1]
v_pk_fma_f32 v[122:123],v[22:23],v[122:123],v[22:23] op_sel:[0,0,1] op_sel_hi:[0,1,1]
v_pk_fma_f32 v[12:13],v[22:23],v[12:13],v[22:23] op_sel:[0,0,1] op_sel_hi:[0,1,1]
v_pk_fma_f32 v[14:15],v[22:23],v[14:15],v[22:23] op_sel:[0,0,1] op_sel_hi:[0,1,1]
v_cvt_pk_bf16_f32 v120,v120,v121
v_cvt_pk_bf16_f32 v121,v122,v123
v_cvt_pk_bf16_f32 v122,v12,v13
v_cvt_pk_bf16_f32 v123,v14,v15
v_mfma_f32_16x16x32_bf16 v[188:191],a[188:191],v[228:231],v[36:39]
.p2align 3
v_accvgpr_read_b32 v124,a120
v_accvgpr_read_b32 v125,a121
v_accvgpr_read_b32 v126,a122
v_accvgpr_read_b32 v127,a123
v_accvgpr_read_b32 v12,a124
v_accvgpr_read_b32 v13,a125
v_accvgpr_read_b32 v14,a126
v_accvgpr_read_b32 v15,a127
v_pk_fma_f32 v[124:125],v[22:23],v[124:125],v[22:23] op_sel:[0,0,1] op_sel_hi:[0,1,1]
v_pk_fma_f32 v[126:127],v[22:23],v[126:127],v[22:23] op_sel:[0,0,1] op_sel_hi:[0,1,1]
v_pk_fma_f32 v[12:13],v[22:23],v[12:13],v[22:23] op_sel:[0,0,1] op_sel_hi:[0,1,1]
v_pk_fma_f32 v[14:15],v[22:23],v[14:15],v[22:23] op_sel:[0,0,1] op_sel_hi:[0,1,1]
v_cvt_pk_bf16_f32 v124,v124,v125
v_cvt_pk_bf16_f32 v125,v126,v127
v_cvt_pk_bf16_f32 v126,v12,v13
v_cvt_pk_bf16_f32 v127,v14,v15
v_mfma_f32_16x16x32_bf16 v[188:191],a[184:187],v[196:199],v[188:191]
v_cmp_nlt_f32 s[58:59],v60,s63
s_nop 1
s_or_b64 s[56:57],s[56:57],s[58:59]
v_mfma_f32_16x16x32_bf16 v[188:191],a[180:183],v[164:167],v[188:191]
v_mov_b32 v60,0
v_add_u32 v3,s48,v3
v_mfma_f32_16x16x32_bf16 v[188:191],a[176:179],v[132:135],v[188:191]
s_mov_b32 s48,32768
v_exp_f32 v136,v136
v_mfma_f32_16x16x32_bf16 a[96:99],a[192:195],v[232:235],0.15915494
ds_read_b64_tr_b16 a[192:193],v2
ds_read_b64_tr_b16 a[194:195],v2 offset:4096
v_mfma_f32_16x16x32_bf16 a[96:99],a[196:199],v[236:239],a[96:99]
ds_read_b64_tr_b16 a[196:197],v2 offset:8192
v_min_f32 v18,v18,v19
v_mfma_f32_16x16x32_bf16 a[100:103],a[204:207],v[236:239],0.15915494
ds_read_b64_tr_b16 a[198:199],v2 offset:12288
v_max3_f32 v19,|v96|,|v97|,|v98|
v_mfma_f32_16x16x32_bf16 a[100:103],a[200:203],v[232:235],a[100:103]
ds_read_b64_tr_b16 a[204:205],v3 offset:8192
v_max3_f32 v19,v19,|v99|,s100
v_mfma_f32_16x16x32_bf16 a[104:107],a[208:211],v[232:235],0.15915494
ds_read_b64_tr_b16 a[206:207],v3 offset:12288
ds_read_b64_tr_b16 a[200:201],v3
v_mfma_f32_16x16x32_bf16 a[104:107],a[212:215],v[236:239],a[104:107]
ds_read_b64_tr_b16 a[202:203],v3 offset:4096
v_fmac_f32 v19,0x39800000,v21
v_mfma_f32_16x16x32_bf16 a[108:111],a[220:223],v[236:239],0.15915494
v_exp_f32 v137,v137
v_mfma_f32_16x16x32_bf16 a[108:111],a[216:219],v[232:235],a[108:111]
ds_read_b64_tr_b16 a[208:209],v2 offset:256
ds_read_b64_tr_b16 a[210:211],v2 offset:4352
v_mfma_f32_16x16x32_bf16 a[112:115],a[224:227],v[232:235],0.15915494
buffer_load_dwordx4 v4,s[24:27],s47 offen offset:2048 nt lds
v_mfma_f32_16x16x32_bf16 a[112:115],a[228:231],v[236:239],a[112:115]
v_exp_f32 v138,v138
v_mfma_f32_16x16x32_bf16 a[116:119],a[236:239],v[236:239],0.15915494
v_exp_f32 v139,v139
v_mfma_f32_16x16x32_bf16 a[116:119],a[232:235],v[232:235],a[116:119]
ds_read_b64_tr_b16 a[212:213],v2 offset:8448
v_cvt_pk_bf16_f32 v136,v136,v137
v_mfma_f32_16x16x32_bf16 a[120:123],a[240:243],v[232:235],0.15915494
v_exp_f32 v140,v140
v_mfma_f32_16x16x32_bf16 a[120:123],a[244:247],v[236:239],a[120:123]
v_exp_f32 v141,v141
v_mfma_f32_16x16x32_bf16 a[124:127],a[252:255],v[236:239],0.15915494
v_exp_f32 v142,v142
v_mfma_f32_16x16x32_bf16 a[124:127],a[248:251],v[232:235],a[124:127]
v_exp_f32 v143,v143
v_smfmac_f32_16x16x64_bf16 v[60:63],v[28:31],v[232:239],v27
ds_read_b64_tr_b16 a[214:215],v2 offset:12544
v_mfma_f32_16x16x32_bf16 v[200:203],a[128:131],v[144:147],v[40:43]
v_cvt_pk_bf16_f32 v137,v138,v139
v_cvt_pk_bf16_f32 v138,v140,v141
v_mfma_f32_16x16x32_bf16 v[200:203],a[132:135],v[176:179],v[200:203]
ds_read_b64_tr_b16 a[220:221],v3 offset:8448
v_cvt_pk_bf16_f32 v139,v142,v143
v_mfma_f32_16x16x32_bf16 v[200:203],a[136:139],v[208:211],v[200:203]
v_exp_f32 v152,v152
v_mfma_f32_16x16x32_bf16 v[200:203],a[140:143],v[240:243],v[200:203]
v_exp_f32 v153,v153
v_mfma_f32_16x16x32_bf16 v[204:207],a[156:159],v[240:243],v[40:43]
v_exp_f32 v154,v154
v_mfma_f32_16x16x32_bf16 v[204:207],a[152:155],v[208:211],v[204:207]
buffer_load_dwordx4 v5,s[24:27],s47 offen offset:3072 nt lds
v_mfma_f32_16x16x32_bf16 v[204:207],a[148:151],v[176:179],v[204:207]
s_or_b32 s47,s47,0x18000
v_exp_f32 v155,v155
v_mfma_f32_16x16x32_bf16 v[204:207],a[144:147],v[144:147],v[204:207]
s_addk_i32 s47,4096
v_exp_f32 v156,v156
v_mfma_f32_16x16x32_bf16 v[216:219],a[160:163],v[144:147],v[40:43]
s_and_b32 s47,s47,0x27000
v_exp_f32 v157,v157
v_mfma_f32_16x16x32_bf16 v[216:219],a[164:167],v[176:179],v[216:219]
s_or_b32 s47,s47,s46
v_exp_f32 v158,v158
v_mfma_f32_16x16x32_bf16 v[216:219],a[168:171],v[208:211],v[216:219]
v_exp_f32 v159,v159
v_mfma_f32_16x16x32_bf16 v[216:219],a[172:175],v[240:243],v[216:219]
ds_read_b64_tr_b16 a[222:223],v3 offset:12544
v_cvt_pk_bf16_f32 v140,v152,v153
v_mfma_f32_16x16x32_bf16 v[220:223],a[188:191],v[240:243],v[40:43]
v_cvt_pk_bf16_f32 v141,v154,v155
v_cvt_pk_bf16_f32 v142,v156,v157
v_mfma_f32_16x16x32_bf16 v[220:223],a[184:187],v[208:211],v[220:223]
ds_read_b64_tr_b16 a[216:217],v3 offset:256
v_cvt_pk_bf16_f32 v143,v158,v159
v_mfma_f32_16x16x32_bf16 v[220:223],a[180:183],v[176:179],v[220:223]
ds_read_b64_tr_b16 a[218:219],v3 offset:4352
ds_read_b64_tr_b16 a[224:225],v2 offset:512
v_mfma_f32_16x16x32_bf16 v[220:223],a[176:179],v[144:147],v[220:223]
s_waitcnt lgkmcnt(4)
ds_read_b64_tr_b16 a[226:227],v2 offset:4608
v_min_f32 v18,v18,v19
v_mfma_f32_16x16x32_bf16 a[0:3],a[192:195],v[136:139],a[0:3]
ds_read_b64_tr_b16 a[228:229],v2 offset:8704
ds_read_b64_tr_b16 a[230:231],v2 offset:12800
v_mfma_f32_16x16x32_bf16 a[0:3],a[196:199],v[140:143],a[0:3]
ds_read_b64_tr_b16 a[236:237],v3 offset:8704
ds_read_b64_tr_b16 a[238:239],v3 offset:12800
v_mfma_f32_16x16x32_bf16 a[4:7],a[204:207],v[140:143],a[4:7]
ds_read_b64_tr_b16 a[232:233],v3 offset:512
ds_read_b64_tr_b16 a[234:235],v3 offset:4608
v_mfma_f32_16x16x32_bf16 a[4:7],a[200:203],v[136:139],a[4:7]
ds_read_b64_tr_b16 a[240:241],v2 offset:768
ds_read_b64_tr_b16 a[242:243],v2 offset:4864
v_mfma_f32_16x16x32_bf16 a[8:11],a[208:211],v[136:139],a[8:11]
s_memrealtime s[76:77]
ds_read_b64_tr_b16 a[244:245],v2 offset:8960
ds_read_b64_tr_b16 a[246:247],v2 offset:13056
v_mfma_f32_16x16x32_bf16 a[8:11],a[212:215],v[140:143],a[8:11]
s_waitcnt lgkmcnt(13)
ds_read_b64_tr_b16 a[252:253],v3 offset:8960
ds_read_b64_tr_b16 a[254:255],v3 offset:13056
v_mfma_f32_16x16x32_bf16 a[12:15],a[220:223],v[140:143],a[12:15]
s_waitcnt lgkmcnt(13)
ds_read_b64_tr_b16 a[248:249],v3 offset:768
ds_read_b64_tr_b16 a[250:251],v3 offset:4864
v_mfma_f32_16x16x32_bf16 a[12:15],a[216:219],v[136:139],a[12:15]
s_waitcnt lgkmcnt(14)
v_mfma_f32_16x16x32_bf16 a[16:19],a[224:227],v[136:139],a[16:19]
s_waitcnt lgkmcnt(12)
v_mfma_f32_16x16x32_bf16 a[16:19],a[228:231],v[140:143],a[16:19]
s_waitcnt lgkmcnt(10)
v_mfma_f32_16x16x32_bf16 a[20:23],a[236:239],v[140:143],a[20:23]
s_waitcnt lgkmcnt(0)
v_max3_f32 v19,|v112|,|v113|,|v114|
v_max3_f32 v19,v19,|v115|,s101
v_mfma_f32_16x16x32_bf16 a[20:23],a[232:235],v[136:139],a[20:23]
s_sub_u32 s79,s78,s76
v_fmac_f32 v19,0x39800000,v23
v_min_f32 v18,v18,v19
v_mfma_f32_16x16x32_bf16 a[24:27],a[240:243],v[136:139],a[24:27]
s_add_u32 s78,s76,s60
v_exp_f32 v168,v168
v_mfma_f32_16x16x32_bf16 a[24:27],a[244:247],v[140:143],a[24:27]
s_cmp_gt_i32 s79,s95
s_cbranch_scc1 L_ps6_%=
L_pr6_%=:
v_exp_f32 v169,v169
v_exp_f32 v170,v170
v_exp_f32 v171,v171
v_mfma_f32_16x16x32_bf16 a[28:31],a[252:255],v[140:143],a[28:31]
v_cvt_pk_bf16_f32 v168,v168,v169
v_exp_f32 v172,v172
v_exp_f32 v173,v173
v_exp_f32 v174,v174
v_exp_f32 v175,v175
v_cmp_gt_f32 s[58:59],0,v18
s_nop 1
s_or_b64 s[56:57],s[56:57],s[58:59]
v_cvt_pk_bf16_f32 v169,v170,v171
v_exp_f32 v184,v184
v_exp_f32 v185,v185
v_exp_f32 v186,v186
v_exp_f32 v187,v187
v_exp_f32 v188,v188
v_exp_f32 v189,v189
v_exp_f32 v190,v190
v_exp_f32 v191,v191
v_mfma_f32_16x16x32_bf16 a[28:31],a[248:251],v[136:139],a[28:31]
s_cmp_lg_u64 s[56:57],0
s_cbranch_scc1 L_slow0_%=
L_sret0_%=:
v_cvt_pk_bf16_f32 v170,v172,v173
)A"
R"A(v_cvt_pk_bf16_f32 v171,v174,v175
v_add_u32 v1,s48,v1
.p2align 3
v_cvt_pk_bf16_f32 v172,v184,v185
v_cvt_pk_bf16_f32 v173,v186,v187
v_cvt_pk_bf16_f32 v174,v188,v189
v_cvt_pk_bf16_f32 v175,v190,v191
v_smfmac_f32_16x16x64_bf16 v[48:51],v[28:31],v[136:143],v27
s_mov_b32 s53,11
s_cmp_eq_u32 s40,3
s_cbranch_scc0 L_att1_%=
s_cmp_gt_i32 s18,0
s_cbranch_scc1 L_att1v0_%=
s_cmp_gt_i32 s19,0
s_cbranch_scc1 L_att1v1_%=
s_cmp_gt_i32 s100,0
s_cbranch_scc1 L_att1v2_%=
.p2align 5
L_att1v3_%=:
s_waitcnt vmcnt(8) lgkmcnt(0)
v_exp_f32 v200,v200
s_barrier
ds_read_b128 a[128:131],v1
v_add_u32 v2,s48,v2
s_mov_b32 s90,L_out_end_%=-L_pc_%=+128
v_exp_f32 v201,v201
s_add_u32 m0,s49,s51
ds_read_b128 a[132:135],v1 offset:256
v_cvt_pk_bf16_f32 v200,v200,v201
s_lshr_b32 s45,s45,1
v_exp_f32 v202,v202
ds_read_b128 a[136:139],v1 offset:512
v_add_u32 v3,s48,v3
buffer_load_dwordx4 v4,s[20:23],s47 offen nt lds
v_exp_f32 v203,v203
s_add_u32 s62,s50,32768
ds_read_b128 a[140:143],v1 offset:768
v_cvt_pk_bf16_f32 v201,v202,v203
s_and_b32 s62,s62,131071
v_exp_f32 v204,v204
s_addk_i32 s54,16384
v_exp_f32 v205,v205
s_sub_u32 s48,s62,s50
ds_read_b128 a[156:159],v1 offset:4864
v_cvt_pk_bf16_f32 v202,v204,v205
s_sub_u32 s53,s53,1
v_exp_f32 v206,v206
s_and_b32 s46,s54,0xfffc0000
v_exp_f32 v207,v207
s_mov_b32 s50,s62
ds_read_b128 a[152:155],v1 offset:4608
v_cvt_pk_bf16_f32 v203,v206,v207
s_xor_b32 s49,s50,65536
v_exp_f32 v216,v216
v_mfma_f32_16x16x32_bf16 a[32:35],a[192:195],v[168:171],a[32:35]
ds_read_b128 a[148:151],v1 offset:4352
v_mfma_f32_16x16x32_bf16 a[32:35],a[196:199],v[172:175],a[32:35]
v_exp_f32 v217,v217
v_mfma_f32_16x16x32_bf16 a[36:39],a[204:207],v[172:175],a[36:39]
ds_read_b128 a[144:147],v1 offset:4096
v_cvt_pk_bf16_f32 v204,v216,v217
v_mfma_f32_16x16x32_bf16 a[36:39],a[200:203],v[168:171],a[36:39]
v_exp_f32 v218,v218
v_mfma_f32_16x16x32_bf16 a[40:43],a[208:211],v[168:171],a[40:43]
buffer_load_dwordx4 v4,s[20:23],s47 offen offset:1024 nt lds
v_mfma_f32_16x16x32_bf16 a[40:43],a[212:215],v[172:175],a[40:43]
ds_read_b128 a[160:163],v1 offset:8192
v_mfma_f32_16x16x32_bf16 a[44:47],a[220:223],v[172:175],a[44:47]
v_exp_f32 v219,v219
v_mfma_f32_16x16x32_bf16 a[44:47],a[216:219],v[168:171],a[44:47]
ds_read_b128 a[164:167],v1 offset:8448
v_cvt_pk_bf16_f32 v205,v218,v219
v_mfma_f32_16x16x32_bf16 a[48:51],a[224:227],v[168:171],a[48:51]
v_exp_f32 v220,v220
v_mfma_f32_16x16x32_bf16 a[48:51],a[228:231],v[172:175],a[48:51]
ds_read_b128 a[168:171],v1 offset:8704
v_mfma_f32_16x16x32_bf16 a[52:55],a[236:239],v[172:175],a[52:55]
v_exp_f32 v221,v221
v_mfma_f32_16x16x32_bf16 a[52:55],a[232:235],v[168:171],a[52:55]
ds_read_b128 a[172:175],v1 offset:8960
v_cvt_pk_bf16_f32 v206,v220,v221
v_mfma_f32_16x16x32_bf16 a[56:59],a[240:243],v[168:171],a[56:59]
v_exp_f32 v222,v222
v_mfma_f32_16x16x32_bf16 a[56:59],a[244:247],v[172:175],a[56:59]
ds_read_b128 a[188:191],v1 offset:13056
v_mfma_f32_16x16x32_bf16 a[60:63],a[252:255],v[172:175],a[60:63]
v_exp_f32 v223,v223
v_mfma_f32_16x16x32_bf16 a[60:63],a[248:251],v[168:171],a[60:63]
ds_read_b128 a[184:187],v1 offset:12800
v_cvt_pk_bf16_f32 v207,v222,v223
v_smfmac_f32_16x16x64_bf16 v[52:55],v[28:31],v[168:175],v27
s_waitcnt lgkmcnt(4)
v_mfma_f32_16x16x32_bf16 v[136:139],a[128:131],v[128:131],v[32:35]
ds_read_b128 a[180:183],v1 offset:12544
v_mfma_f32_16x16x32_bf16 v[136:139],a[132:135],v[160:163],v[136:139]
buffer_load_dwordx4 v5,s[20:23],s47 offen offset:2048 nt lds
v_mfma_f32_16x16x32_bf16 v[136:139],a[136:139],v[192:195],v[136:139]
ds_read_b128 a[176:179],v1 offset:12288
v_add_u32 v1,s48,v1
v_mfma_f32_16x16x32_bf16 v[136:139],a[140:143],v[224:227],v[136:139]
v_mfma_f32_16x16x32_bf16 v[140:143],a[156:159],v[224:227],v[32:35]
v_mfma_f32_16x16x32_bf16 v[140:143],a[152:155],v[192:195],v[140:143]
v_mfma_f32_16x16x32_bf16 v[140:143],a[148:151],v[160:163],v[140:143]
v_mfma_f32_16x16x32_bf16 v[140:143],a[144:147],v[128:131],v[140:143]
v_mfma_f32_16x16x32_bf16 v[152:155],a[160:163],v[128:131],v[32:35]
v_mfma_f32_16x16x32_bf16 v[152:155],a[164:167],v[160:163],v[152:155]
s_waitcnt lgkmcnt(1)
v_mfma_f32_16x16x32_bf16 v[152:155],a[168:171],v[192:195],v[152:155]
v_mfma_f32_16x16x32_bf16 v[152:155],a[172:175],v[224:227],v[152:155]
v_mfma_f32_16x16x32_bf16 v[156:159],a[188:191],v[224:227],v[32:35]
v_mfma_f32_16x16x32_bf16 v[156:159],a[184:187],v[192:195],v[156:159]
v_mfma_f32_16x16x32_bf16 v[156:159],a[180:183],v[160:163],v[156:159]
s_waitcnt lgkmcnt(0)
v_mfma_f32_16x16x32_bf16 v[156:159],a[176:179],v[128:131],v[156:159]
buffer_load_dwordx4 v5,s[20:23],s47 offen offset:3072 nt lds
v_mfma_f32_16x16x32_bf16 a[64:67],a[192:195],v[200:203],a[64:67]
v_mfma_f32_16x16x32_bf16 a[64:67],a[196:199],v[204:207],a[64:67]
v_mfma_f32_16x16x32_bf16 a[68:71],a[204:207],v[204:207],a[68:71]
v_mfma_f32_16x16x32_bf16 a[68:71],a[200:203],v[200:203],a[68:71]
v_mfma_f32_16x16x32_bf16 a[72:75],a[208:211],v[200:203],a[72:75]
v_exp_f32 v136,v136
v_mfma_f32_16x16x32_bf16 a[72:75],a[212:215],v[204:207],a[72:75]
v_exp_f32 v137,v137
v_mfma_f32_16x16x32_bf16 a[76:79],a[220:223],v[204:207],a[76:79]
v_exp_f32 v138,v138
v_mfma_f32_16x16x32_bf16 a[76:79],a[216:219],v[200:203],a[76:79]
v_exp_f32 v139,v139
v_mfma_f32_16x16x32_bf16 a[80:83],a[224:227],v[200:203],a[80:83]
v_exp_f32 v140,v140
v_mfma_f32_16x16x32_bf16 a[80:83],a[228:231],v[204:207],a[80:83]
v_exp_f32 v141,v141
v_mfma_f32_16x16x32_bf16 a[84:87],a[236:239],v[204:207],a[84:87]
s_addk_i32 m0,16384
v_exp_f32 v142,v142
v_mfma_f32_16x16x32_bf16 a[84:87],a[232:235],v[200:203],a[84:87]
v_exp_f32 v143,v143
v_mfma_f32_16x16x32_bf16 a[88:91],a[240:243],v[200:203],a[88:91]
v_exp_f32 v152,v152
v_mfma_f32_16x16x32_bf16 a[88:91],a[244:247],v[204:207],a[88:91]
buffer_load_dwordx4 v4,s[24:27],s47 offen nt lds
v_mfma_f32_16x16x32_bf16 a[92:95],a[252:255],v[204:207],a[92:95]
v_mfma_f32_16x16x32_bf16 a[92:95],a[248:251],v[200:203],a[92:95]
v_smfmac_f32_16x16x64_bf16 v[56:59],v[28:31],v[200:207],v27
v_mfma_f32_16x16x32_bf16 v[168:171],a[128:131],v[132:135],v[36:39]
v_mfma_f32_16x16x32_bf16 v[168:171],a[132:135],v[164:167],v[168:171]
v_mfma_f32_16x16x32_bf16 v[168:171],a[136:139],v[196:199],v[168:171]
v_exp_f32 v153,v153
v_mfma_f32_16x16x32_bf16 v[168:171],a[140:143],v[228:231],v[168:171]
v_exp_f32 v154,v154
v_mfma_f32_16x16x32_bf16 v[172:175],a[156:159],v[228:231],v[36:39]
v_exp_f32 v155,v155
v_mfma_f32_16x16x32_bf16 v[172:175],a[152:155],v[196:199],v[172:175]
v_exp_f32 v156,v156
v_mfma_f32_16x16x32_bf16 v[172:175],a[148:151],v[164:167],v[172:175]
v_exp_f32 v157,v157
v_mfma_f32_16x16x32_bf16 v[172:175],a[144:147],v[132:135],v[172:175]
v_exp_f32 v158,v158
v_mfma_f32_16x16x32_bf16 v[184:187],a[160:163],v[132:135],v[36:39]
v_exp_f32 v159,v159
v_mfma_f32_16x16x32_bf16 v[184:187],a[164:167],v[164:167],v[184:187]
v_exp_f32 v168,v168
v_mfma_f32_16x16x32_bf16 v[184:187],a[168:171],v[196:199],v[184:187]
buffer_load_dwordx4 v5,s[24:27],s47 offen offset:1024 nt lds
v_mfma_f32_16x16x32_bf16 v[184:187],a[172:175],v[228:231],v[184:187]
v_exp_f32 v169,v169
v_mfma_f32_16x16x32_bf16 v[188:191],a[188:191],v[228:231],v[36:39]
v_exp_f32 v170,v170
v_mfma_f32_16x16x32_bf16 v[188:191],a[184:187],v[196:199],v[188:191]
v_exp_f32 v171,v171
v_mfma_f32_16x16x32_bf16 v[188:191],a[180:183],v[164:167],v[188:191]
v_exp_f32 v172,v172
v_mfma_f32_16x16x32_bf16 v[188:191],a[176:179],v[132:135],v[188:191]
v_exp_f32 v173,v173
ds_read_b64_tr_b16 a[192:193],v2
v_cvt_pk_bf16_f32 v168,v168,v169
ds_read_b64_tr_b16 a[194:195],v2 offset:4096
v_cvt_pk_bf16_f32 v169,v170,v171
ds_read_b64_tr_b16 a[196:197],v2 offset:8192
v_cvt_pk_bf16_f32 v170,v172,v173
v_exp_f32 v174,v174
v_exp_f32 v175,v175
ds_read_b64_tr_b16 a[198:199],v2 offset:12288
v_cvt_pk_bf16_f32 v171,v174,v175
v_exp_f32 v184,v184
v_exp_f32 v185,v185
buffer_load_dwordx4 v4,s[24:27],s47 offen offset:2048 nt lds
ds_read_b64_tr_b16 a[204:205],v3 offset:8192
v_cvt_pk_bf16_f32 v172,v184,v185
ds_read_b64_tr_b16 a[206:207],v3 offset:12288
v_cvt_pk_bf16_f32 v136,v136,v137
v_exp_f32 v186,v186
ds_read_b64_tr_b16 a[200:201],v3
ds_read_b64_tr_b16 a[202:203],v3 offset:4096
v_cvt_pk_bf16_f32 v137,v138,v139
ds_read_b64_tr_b16 a[208:209],v2 offset:256
ds_read_b64_tr_b16 a[210:211],v2 offset:4352
ds_read_b64_tr_b16 a[212:213],v2 offset:8448
v_mfma_f32_16x16x32_bf16 v[200:203],a[128:131],v[144:147],v[40:43]
ds_read_b64_tr_b16 a[214:215],v2 offset:12544
v_cvt_pk_bf16_f32 v138,v140,v141
v_mfma_f32_16x16x32_bf16 v[200:203],a[132:135],v[176:179],v[200:203]
ds_read_b64_tr_b16 a[220:221],v3 offset:8448
v_mfma_f32_16x16x32_bf16 v[200:203],a[136:139],v[208:211],v[200:203]
ds_read_b64_tr_b16 a[222:223],v3 offset:12544
v_cvt_pk_bf16_f32 v139,v142,v143
v_mfma_f32_16x16x32_bf16 v[200:203],a[140:143],v[240:243],v[200:203]
ds_read_b64_tr_b16 a[216:217],v3 offset:256
v_mfma_f32_16x16x32_bf16 v[204:207],a[156:159],v[240:243],v[40:43]
ds_read_b64_tr_b16 a[218:219],v3 offset:4352
v_mfma_f32_16x16x32_bf16 v[204:207],a[152:155],v[208:211],v[204:207]
buffer_load_dwordx4 v5,s[24:27],s47 offen offset:3072 nt lds
v_mfma_f32_16x16x32_bf16 v[204:207],a[148:151],v[176:179],v[204:207]
s_or_b32 s47,s47,0x18000
ds_read_b64_tr_b16 a[224:225],v2 offset:512
v_cvt_pk_bf16_f32 v140,v152,v153
v_mfma_f32_16x16x32_bf16 v[204:207],a[144:147],v[144:147],v[204:207]
s_addk_i32 s47,4096
s_and_b32 s47,s47,0x27000
ds_read_b64_tr_b16 a[226:227],v2 offset:4608
v_mfma_f32_16x16x32_bf16 v[216:219],a[160:163],v[144:147],v[40:43]
s_or_b32 s47,s47,s46
ds_read_b64_tr_b16 a[228:229],v2 offset:8704
v_cvt_pk_bf16_f32 v141,v154,v155
v_mfma_f32_16x16x32_bf16 v[216:219],a[164:167],v[176:179],v[216:219]
ds_read_b64_tr_b16 a[230:231],v2 offset:12800
v_mfma_f32_16x16x32_bf16 v[216:219],a[168:171],v[208:211],v[216:219]
ds_read_b64_tr_b16 a[236:237],v3 offset:8704
v_mfma_f32_16x16x32_bf16 v[216:219],a[172:175],v[240:243],v[216:219]
ds_read_b64_tr_b16 a[238:239],v3 offset:12800
v_cvt_pk_bf16_f32 v142,v156,v157
v_mfma_f32_16x16x32_bf16 v[220:223],a[188:191],v[240:243],v[40:43]
ds_read_b64_tr_b16 a[232:233],v3 offset:512
v_mfma_f32_16x16x32_bf16 v[220:223],a[184:187],v[208:211],v[220:223]
ds_read_b64_tr_b16 a[234:235],v3 offset:4608
v_mfma_f32_16x16x32_bf16 v[220:223],a[180:183],v[176:179],v[220:223]
ds_read_b64_tr_b16 a[240:241],v2 offset:768
v_cvt_pk_bf16_f32 v143,v158,v159
v_mfma_f32_16x16x32_bf16 v[220:223],a[176:179],v[144:147],v[220:223]
s_waitcnt lgkmcnt(7)
buffer_load_dword v26,v24,s[88:91],s93 offen
v_mfma_f32_16x16x32_bf16 a[0:3],a[192:195],v[136:139],a[0:3]
ds_read_b64_tr_b16 a[242:243],v2 offset:4864
v_mfma_f32_16x16x32_bf16 a[0:3],a[196:199],v[140:143],a[0:3]
ds_read_b64_tr_b16 a[244:245],v2 offset:8960
v_mfma_f32_16x16x32_bf16 a[4:7],a[204:207],v[140:143],a[4:7]
ds_read_b64_tr_b16 a[246:247],v2 offset:13056
v_mfma_f32_16x16x32_bf16 a[4:7],a[200:203],v[136:139],a[4:7]
ds_read_b64_tr_b16 a[252:253],v3 offset:8960
v_mfma_f32_16x16x32_bf16 a[8:11],a[208:211],v[136:139],a[8:11]
ds_read_b64_tr_b16 a[254:255],v3 offset:13056
v_mfma_f32_16x16x32_bf16 a[8:11],a[212:215],v[140:143],a[8:11]
ds_read_b64_tr_b16 a[248:249],v3 offset:768
v_mfma_f32_16x16x32_bf16 a[12:15],a[220:223],v[140:143],a[12:15]
ds_read_b64_tr_b16 a[250:251],v3 offset:4864
v_mfma_f32_16x16x32_bf16 a[12:15],a[216:219],v[136:139],a[12:15]
s_bitcmp1_b32 s45,0
s_cbranch_scc0 L_t31v31_%=
s_mov_b32 m0,s80
s_addk_i32 s80,4096
buffer_load_dwordx4 v25,s[24:27],s83 offen nt lds
s_addk_i32 s83,1024
L_t31v31_%=:
v_mfma_f32_16x16x32_bf16 a[16:19],a[224:227],v[136:139],a[16:19]
s_waitcnt lgkmcnt(6)
v_exp_f32 v187,v187
v_mfma_f32_16x16x32_bf16 a[16:19],a[228:231],v[140:143],a[16:19]
v_exp_f32 v188,v188
v_mfma_f32_16x16x32_bf16 a[20:23],a[236:239],v[140:143],a[20:23]
v_exp_f32 v189,v189
v_mfma_f32_16x16x32_bf16 a[20:23],a[232:235],v[136:139],a[20:23]
v_exp_f32 v190,v190
v_mfma_f32_16x16x32_bf16 a[24:27],a[240:243],v[136:139],a[24:27]
s_waitcnt lgkmcnt(2)
v_exp_f32 v191,v191
v_mfma_f32_16x16x32_bf16 a[24:27],a[244:247],v[140:143],a[24:27]
v_cvt_pk_bf16_f32 v173,v186,v187
v_mfma_f32_16x16x32_bf16 a[28:31],a[252:255],v[140:143],a[28:31]
s_waitcnt lgkmcnt(0)
v_cvt_pk_bf16_f32 v174,v188,v189
v_cvt_pk_bf16_f32 v175,v190,v191
)A"
R"A(v_mfma_f32_16x16x32_bf16 a[28:31],a[248:251],v[136:139],a[28:31]
s_cmp_lg_u32 s53,0
v_smfmac_f32_16x16x64_bf16 v[48:51],v[28:31],v[136:143],v27
s_cbranch_scc1 L_att1v3_%=
s_waitcnt lgkmcnt(0)
s_nop 7
s_branch L_att1x_%=
.p2align 5
L_att1v0_%=:
v_mfma_f32_16x16x32_bf16 v[232:235],a[128:131],v[148:151],v[44:47]
s_waitcnt vmcnt(8) lgkmcnt(0)
v_exp_f32 v200,v200
v_mfma_f32_16x16x32_bf16 v[232:235],a[132:135],v[180:183],v[232:235]
s_barrier
ds_read_b128 a[128:131],v1
v_add_u32 v2,s48,v2
v_mfma_f32_16x16x32_bf16 v[232:235],a[136:139],v[212:215],v[232:235]
s_mov_b32 s90,L_out_end_%=-L_pc_%=+128
v_exp_f32 v201,v201
v_mfma_f32_16x16x32_bf16 v[232:235],a[140:143],v[244:247],v[232:235]
s_add_u32 m0,s49,s51
ds_read_b128 a[132:135],v1 offset:256
v_cvt_pk_bf16_f32 v200,v200,v201
v_mfma_f32_16x16x32_bf16 v[236:239],a[156:159],v[244:247],v[44:47]
s_lshr_b32 s45,s45,1
v_exp_f32 v202,v202
v_mfma_f32_16x16x32_bf16 v[236:239],a[152:155],v[212:215],v[236:239]
ds_read_b128 a[136:139],v1 offset:512
v_add_u32 v3,s48,v3
v_mfma_f32_16x16x32_bf16 v[236:239],a[148:151],v[180:183],v[236:239]
buffer_load_dwordx4 v4,s[20:23],s47 offen nt lds
v_mfma_f32_16x16x32_bf16 v[236:239],a[144:147],v[148:151],v[236:239]
v_exp_f32 v203,v203
v_mfma_f32_16x16x32_bf16 v[248:251],a[160:163],v[148:151],v[44:47]
s_add_u32 s62,s50,32768
ds_read_b128 a[140:143],v1 offset:768
v_cvt_pk_bf16_f32 v201,v202,v203
v_mfma_f32_16x16x32_bf16 v[248:251],a[164:167],v[180:183],v[248:251]
s_and_b32 s62,s62,131071
v_exp_f32 v204,v204
v_mfma_f32_16x16x32_bf16 v[248:251],a[168:171],v[212:215],v[248:251]
s_addk_i32 s54,16384
v_exp_f32 v205,v205
v_mfma_f32_16x16x32_bf16 v[248:251],a[172:175],v[244:247],v[248:251]
s_sub_u32 s48,s62,s50
ds_read_b128 a[156:159],v1 offset:4864
v_cvt_pk_bf16_f32 v202,v204,v205
v_mfma_f32_16x16x32_bf16 v[252:255],a[188:191],v[244:247],v[44:47]
s_sub_u32 s53,s53,1
v_exp_f32 v206,v206
v_mfma_f32_16x16x32_bf16 v[252:255],a[184:187],v[212:215],v[252:255]
s_and_b32 s46,s54,0xfffc0000
v_exp_f32 v207,v207
v_mfma_f32_16x16x32_bf16 v[252:255],a[180:183],v[180:183],v[252:255]
s_mov_b32 s50,s62
ds_read_b128 a[152:155],v1 offset:4608
v_cvt_pk_bf16_f32 v203,v206,v207
v_mfma_f32_16x16x32_bf16 v[252:255],a[176:179],v[148:151],v[252:255]
s_xor_b32 s49,s50,65536
v_exp_f32 v216,v216
v_mfma_f32_16x16x32_bf16 a[32:35],a[192:195],v[168:171],a[32:35]
ds_read_b128 a[148:151],v1 offset:4352
v_mfma_f32_16x16x32_bf16 a[32:35],a[196:199],v[172:175],a[32:35]
v_exp_f32 v217,v217
v_mfma_f32_16x16x32_bf16 a[36:39],a[204:207],v[172:175],a[36:39]
ds_read_b128 a[144:147],v1 offset:4096
v_cvt_pk_bf16_f32 v204,v216,v217
v_mfma_f32_16x16x32_bf16 a[36:39],a[200:203],v[168:171],a[36:39]
v_exp_f32 v218,v218
v_mfma_f32_16x16x32_bf16 a[40:43],a[208:211],v[168:171],a[40:43]
buffer_load_dwordx4 v4,s[20:23],s47 offen offset:1024 nt lds
v_mfma_f32_16x16x32_bf16 a[40:43],a[212:215],v[172:175],a[40:43]
ds_read_b128 a[160:163],v1 offset:8192
v_mfma_f32_16x16x32_bf16 a[44:47],a[220:223],v[172:175],a[44:47]
v_exp_f32 v219,v219
v_mfma_f32_16x16x32_bf16 a[44:47],a[216:219],v[168:171],a[44:47]
ds_read_b128 a[164:167],v1 offset:8448
v_cvt_pk_bf16_f32 v205,v218,v219
v_mfma_f32_16x16x32_bf16 a[48:51],a[224:227],v[168:171],a[48:51]
v_exp_f32 v220,v220
v_mfma_f32_16x16x32_bf16 a[48:51],a[228:231],v[172:175],a[48:51]
ds_read_b128 a[168:171],v1 offset:8704
v_mfma_f32_16x16x32_bf16 a[52:55],a[236:239],v[172:175],a[52:55]
v_exp_f32 v221,v221
v_mfma_f32_16x16x32_bf16 a[52:55],a[232:235],v[168:171],a[52:55]
ds_read_b128 a[172:175],v1 offset:8960
v_cvt_pk_bf16_f32 v206,v220,v221
v_mfma_f32_16x16x32_bf16 a[56:59],a[240:243],v[168:171],a[56:59]
v_exp_f32 v222,v222
v_mfma_f32_16x16x32_bf16 a[56:59],a[244:247],v[172:175],a[56:59]
ds_read_b128 a[188:191],v1 offset:13056
v_mfma_f32_16x16x32_bf16 a[60:63],a[252:255],v[172:175],a[60:63]
v_exp_f32 v223,v223
v_mfma_f32_16x16x32_bf16 a[60:63],a[248:251],v[168:171],a[60:63]
ds_read_b128 a[184:187],v1 offset:12800
v_cvt_pk_bf16_f32 v207,v222,v223
v_smfmac_f32_16x16x64_bf16 v[52:55],v[28:31],v[168:175],v27
ds_read_b128 a[180:183],v1 offset:12544
buffer_load_dwordx4 v5,s[20:23],s47 offen offset:2048 nt lds
ds_read_b128 a[176:179],v1 offset:12288
v_add_u32 v1,s48,v1
v_exp_f32 v232,v232
v_exp_f32 v233,v233
v_exp_f32 v234,v234
v_exp_f32 v235,v235
v_exp_f32 v236,v236
v_exp_f32 v237,v237
v_exp_f32 v238,v238
v_exp_f32 v239,v239
v_exp_f32 v248,v248
v_exp_f32 v249,v249
v_exp_f32 v250,v250
v_exp_f32 v251,v251
buffer_load_dwordx4 v5,s[20:23],s47 offen offset:3072 nt lds
v_mfma_f32_16x16x32_bf16 a[64:67],a[192:195],v[200:203],a[64:67]
v_exp_f32 v252,v252
v_mfma_f32_16x16x32_bf16 a[64:67],a[196:199],v[204:207],a[64:67]
v_exp_f32 v253,v253
v_mfma_f32_16x16x32_bf16 a[68:71],a[204:207],v[204:207],a[68:71]
v_exp_f32 v254,v254
v_mfma_f32_16x16x32_bf16 a[68:71],a[200:203],v[200:203],a[68:71]
v_exp_f32 v255,v255
v_mfma_f32_16x16x32_bf16 a[72:75],a[208:211],v[200:203],a[72:75]
v_mfma_f32_16x16x32_bf16 a[72:75],a[212:215],v[204:207],a[72:75]
v_mfma_f32_16x16x32_bf16 a[76:79],a[220:223],v[204:207],a[76:79]
v_mfma_f32_16x16x32_bf16 a[76:79],a[216:219],v[200:203],a[76:79]
v_mfma_f32_16x16x32_bf16 a[80:83],a[224:227],v[200:203],a[80:83]
v_mfma_f32_16x16x32_bf16 a[80:83],a[228:231],v[204:207],a[80:83]
v_mfma_f32_16x16x32_bf16 a[84:87],a[236:239],v[204:207],a[84:87]
s_addk_i32 m0,16384
v_mfma_f32_16x16x32_bf16 a[84:87],a[232:235],v[200:203],a[84:87]
v_mfma_f32_16x16x32_bf16 a[88:91],a[240:243],v[200:203],a[88:91]
v_mfma_f32_16x16x32_bf16 a[88:91],a[244:247],v[204:207],a[88:91]
buffer_load_dwordx4 v4,s[24:27],s47 offen nt lds
v_mfma_f32_16x16x32_bf16 a[92:95],a[252:255],v[204:207],a[92:95]
v_cvt_pk_bf16_f32 v232,v232,v233
v_cvt_pk_bf16_f32 v233,v234,v235
v_mfma_f32_16x16x32_bf16 a[92:95],a[248:251],v[200:203],a[92:95]
v_cvt_pk_bf16_f32 v234,v236,v237
v_cvt_pk_bf16_f32 v235,v238,v239
v_smfmac_f32_16x16x64_bf16 v[56:59],v[28:31],v[200:207],v27
s_waitcnt lgkmcnt(0)
v_cvt_pk_bf16_f32 v236,v248,v249
v_mfma_f32_16x16x32_bf16 v[168:171],a[128:131],v[132:135],v[36:39]
v_cvt_pk_bf16_f32 v237,v250,v251
v_cvt_pk_bf16_f32 v238,v252,v253
v_mfma_f32_16x16x32_bf16 v[168:171],a[132:135],v[164:167],v[168:171]
v_cvt_pk_bf16_f32 v239,v254,v255
v_mfma_f32_16x16x32_bf16 v[168:171],a[136:139],v[196:199],v[168:171]
v_mfma_f32_16x16x32_bf16 v[168:171],a[140:143],v[228:231],v[168:171]
v_mfma_f32_16x16x32_bf16 v[172:175],a[156:159],v[228:231],v[36:39]
v_mfma_f32_16x16x32_bf16 v[172:175],a[152:155],v[196:199],v[172:175]
v_mfma_f32_16x16x32_bf16 v[172:175],a[148:151],v[164:167],v[172:175]
v_mfma_f32_16x16x32_bf16 v[172:175],a[144:147],v[132:135],v[172:175]
v_mfma_f32_16x16x32_bf16 v[184:187],a[160:163],v[132:135],v[36:39]
v_mfma_f32_16x16x32_bf16 v[184:187],a[164:167],v[164:167],v[184:187]
s_nop 1
v_exp_f32 v168,v168
v_mfma_f32_16x16x32_bf16 v[184:187],a[168:171],v[196:199],v[184:187]
buffer_load_dwordx4 v5,s[24:27],s47 offen offset:1024 nt lds
v_mfma_f32_16x16x32_bf16 v[184:187],a[172:175],v[228:231],v[184:187]
v_exp_f32 v169,v169
v_mfma_f32_16x16x32_bf16 v[188:191],a[188:191],v[228:231],v[36:39]
v_exp_f32 v170,v170
v_mfma_f32_16x16x32_bf16 v[188:191],a[184:187],v[196:199],v[188:191]
v_exp_f32 v171,v171
v_mfma_f32_16x16x32_bf16 v[188:191],a[180:183],v[164:167],v[188:191]
v_exp_f32 v172,v172
v_mfma_f32_16x16x32_bf16 v[188:191],a[176:179],v[132:135],v[188:191]
v_exp_f32 v173,v173
v_mfma_f32_16x16x32_bf16 a[96:99],a[192:195],v[232:235],a[96:99]
ds_read_b64_tr_b16 a[192:193],v2
v_cvt_pk_bf16_f32 v168,v168,v169
v_mfma_f32_16x16x32_bf16 a[96:99],a[196:199],v[236:239],a[96:99]
ds_read_b64_tr_b16 a[194:195],v2 offset:4096
v_cvt_pk_bf16_f32 v169,v170,v171
v_mfma_f32_16x16x32_bf16 a[100:103],a[204:207],v[236:239],a[100:103]
ds_read_b64_tr_b16 a[196:197],v2 offset:8192
v_cvt_pk_bf16_f32 v170,v172,v173
v_mfma_f32_16x16x32_bf16 a[100:103],a[200:203],v[232:235],a[100:103]
v_exp_f32 v174,v174
v_mfma_f32_16x16x32_bf16 a[104:107],a[208:211],v[232:235],a[104:107]
v_exp_f32 v175,v175
v_mfma_f32_16x16x32_bf16 a[104:107],a[212:215],v[236:239],a[104:107]
ds_read_b64_tr_b16 a[198:199],v2 offset:12288
v_cvt_pk_bf16_f32 v171,v174,v175
v_mfma_f32_16x16x32_bf16 a[108:111],a[220:223],v[236:239],a[108:111]
v_exp_f32 v184,v184
v_mfma_f32_16x16x32_bf16 a[108:111],a[216:219],v[232:235],a[108:111]
v_exp_f32 v185,v185
v_mfma_f32_16x16x32_bf16 a[112:115],a[224:227],v[232:235],a[112:115]
buffer_load_dwordx4 v4,s[24:27],s47 offen offset:2048 nt lds
v_mfma_f32_16x16x32_bf16 a[112:115],a[228:231],v[236:239],a[112:115]
ds_read_b64_tr_b16 a[204:205],v3 offset:8192
v_cvt_pk_bf16_f32 v172,v184,v185
v_mfma_f32_16x16x32_bf16 a[116:119],a[236:239],v[236:239],a[116:119]
ds_read_b64_tr_b16 a[206:207],v3 offset:12288
v_mfma_f32_16x16x32_bf16 a[116:119],a[232:235],v[232:235],a[116:119]
v_exp_f32 v186,v186
v_mfma_f32_16x16x32_bf16 a[120:123],a[240:243],v[232:235],a[120:123]
ds_read_b64_tr_b16 a[200:201],v3
v_mfma_f32_16x16x32_bf16 a[120:123],a[244:247],v[236:239],a[120:123]
ds_read_b64_tr_b16 a[202:203],v3 offset:4096
v_mfma_f32_16x16x32_bf16 a[124:127],a[252:255],v[236:239],a[124:127]
ds_read_b64_tr_b16 a[208:209],v2 offset:256
v_mfma_f32_16x16x32_bf16 a[124:127],a[248:251],v[232:235],a[124:127]
ds_read_b64_tr_b16 a[210:211],v2 offset:4352
v_smfmac_f32_16x16x64_bf16 v[60:63],v[28:31],v[232:239],v27
ds_read_b64_tr_b16 a[212:213],v2 offset:8448
v_mfma_f32_16x16x32_bf16 v[200:203],a[128:131],v[144:147],v[40:43]
ds_read_b64_tr_b16 a[214:215],v2 offset:12544
v_mfma_f32_16x16x32_bf16 v[200:203],a[132:135],v[176:179],v[200:203]
ds_read_b64_tr_b16 a[220:221],v3 offset:8448
v_mfma_f32_16x16x32_bf16 v[200:203],a[136:139],v[208:211],v[200:203]
ds_read_b64_tr_b16 a[222:223],v3 offset:12544
v_mfma_f32_16x16x32_bf16 v[200:203],a[140:143],v[240:243],v[200:203]
ds_read_b64_tr_b16 a[216:217],v3 offset:256
v_mfma_f32_16x16x32_bf16 v[204:207],a[156:159],v[240:243],v[40:43]
ds_read_b64_tr_b16 a[218:219],v3 offset:4352
v_mfma_f32_16x16x32_bf16 v[204:207],a[152:155],v[208:211],v[204:207]
buffer_load_dwordx4 v5,s[24:27],s47 offen offset:3072 nt lds
v_mfma_f32_16x16x32_bf16 v[204:207],a[148:151],v[176:179],v[204:207]
s_or_b32 s47,s47,0x18000
ds_read_b64_tr_b16 a[224:225],v2 offset:512
v_mfma_f32_16x16x32_bf16 v[204:207],a[144:147],v[144:147],v[204:207]
s_addk_i32 s47,4096
s_and_b32 s47,s47,0x27000
ds_read_b64_tr_b16 a[226:227],v2 offset:4608
v_mfma_f32_16x16x32_bf16 v[216:219],a[160:163],v[144:147],v[40:43]
s_or_b32 s47,s47,s46
ds_read_b64_tr_b16 a[228:229],v2 offset:8704
v_mfma_f32_16x16x32_bf16 v[216:219],a[164:167],v[176:179],v[216:219]
ds_read_b64_tr_b16 a[230:231],v2 offset:12800
v_mfma_f32_16x16x32_bf16 v[216:219],a[168:171],v[208:211],v[216:219]
ds_read_b64_tr_b16 a[236:237],v3 offset:8704
v_mfma_f32_16x16x32_bf16 v[216:219],a[172:175],v[240:243],v[216:219]
ds_read_b64_tr_b16 a[238:239],v3 offset:12800
v_mfma_f32_16x16x32_bf16 v[220:223],a[188:191],v[240:243],v[40:43]
ds_read_b64_tr_b16 a[232:233],v3 offset:512
v_mfma_f32_16x16x32_bf16 v[220:223],a[184:187],v[208:211],v[220:223]
ds_read_b64_tr_b16 a[234:235],v3 offset:4608
v_mfma_f32_16x16x32_bf16 v[220:223],a[180:183],v[176:179],v[220:223]
ds_read_b64_tr_b16 a[240:241],v2 offset:768
v_mfma_f32_16x16x32_bf16 v[220:223],a[176:179],v[144:147],v[220:223]
buffer_load_dword v26,v24,s[88:91],s93 offen
ds_read_b64_tr_b16 a[242:243],v2 offset:4864
ds_read_b64_tr_b16 a[244:245],v2 offset:8960
ds_read_b64_tr_b16 a[246:247],v2 offset:13056
ds_read_b64_tr_b16 a[252:253],v3 offset:8960
ds_read_b64_tr_b16 a[254:255],v3 offset:13056
ds_read_b64_tr_b16 a[248:249],v3 offset:768
ds_read_b64_tr_b16 a[250:251],v3 offset:4864
s_bitcmp1_b32 s45,0
s_cbranch_scc0 L_t31v01_%=
s_mov_b32 m0,s80
s_addk_i32 s80,4096
buffer_load_dwordx4 v25,s[24:27],s83 offen nt lds
s_addk_i32 s83,1024
L_t31v01_%=:
v_exp_f32 v187,v187
v_exp_f32 v188,v188
v_exp_f32 v189,v189
v_exp_f32 v190,v190
v_exp_f32 v191,v191
.p2align 3
v_cvt_pk_bf16_f32 v173,v186,v187
v_cvt_pk_bf16_f32 v174,v188,v189
v_cvt_pk_bf16_f32 v175,v190,v191
s_cmp_lg_u32 s53,0
s_cbranch_scc1 L_att1v0_%=
s_waitcnt lgkmcnt(0)
s_nop 7
s_branch L_att1x_%=
.p2align 5
L_att1v1_%=:
v_mfma_f32_16x16x32_bf16 v[232:235],a[128:131],v[148:151],v[44:47]
)A"
R"A(s_waitcnt vmcnt(8) lgkmcnt(0)
v_exp_f32 v200,v200
v_mfma_f32_16x16x32_bf16 v[232:235],a[132:135],v[180:183],v[232:235]
s_barrier
ds_read_b128 a[128:131],v1
v_add_u32 v2,s48,v2
v_mfma_f32_16x16x32_bf16 v[232:235],a[136:139],v[212:215],v[232:235]
s_mov_b32 s90,L_out_end_%=-L_pc_%=+128
v_exp_f32 v201,v201
v_mfma_f32_16x16x32_bf16 v[232:235],a[140:143],v[244:247],v[232:235]
s_add_u32 m0,s49,s51
ds_read_b128 a[132:135],v1 offset:256
v_cvt_pk_bf16_f32 v200,v200,v201
v_mfma_f32_16x16x32_bf16 v[236:239],a[156:159],v[244:247],v[44:47]
s_lshr_b32 s45,s45,1
v_exp_f32 v202,v202
v_mfma_f32_16x16x32_bf16 v[236:239],a[152:155],v[212:215],v[236:239]
ds_read_b128 a[136:139],v1 offset:512
v_add_u32 v3,s48,v3
v_mfma_f32_16x16x32_bf16 v[236:239],a[148:151],v[180:183],v[236:239]
buffer_load_dwordx4 v4,s[20:23],s47 offen nt lds
v_mfma_f32_16x16x32_bf16 v[236:239],a[144:147],v[148:151],v[236:239]
v_exp_f32 v203,v203
v_mfma_f32_16x16x32_bf16 v[248:251],a[160:163],v[148:151],v[44:47]
s_add_u32 s62,s50,32768
ds_read_b128 a[140:143],v1 offset:768
v_cvt_pk_bf16_f32 v201,v202,v203
v_mfma_f32_16x16x32_bf16 v[248:251],a[164:167],v[180:183],v[248:251]
s_and_b32 s62,s62,131071
v_exp_f32 v204,v204
v_mfma_f32_16x16x32_bf16 v[248:251],a[168:171],v[212:215],v[248:251]
s_addk_i32 s54,16384
v_exp_f32 v205,v205
v_mfma_f32_16x16x32_bf16 v[248:251],a[172:175],v[244:247],v[248:251]
s_sub_u32 s48,s62,s50
ds_read_b128 a[156:159],v1 offset:4864
v_cvt_pk_bf16_f32 v202,v204,v205
v_mfma_f32_16x16x32_bf16 v[252:255],a[188:191],v[244:247],v[44:47]
s_sub_u32 s53,s53,1
v_exp_f32 v206,v206
v_mfma_f32_16x16x32_bf16 v[252:255],a[184:187],v[212:215],v[252:255]
s_and_b32 s46,s54,0xfffc0000
v_exp_f32 v207,v207
v_mfma_f32_16x16x32_bf16 v[252:255],a[180:183],v[180:183],v[252:255]
s_mov_b32 s50,s62
ds_read_b128 a[152:155],v1 offset:4608
v_cvt_pk_bf16_f32 v203,v206,v207
v_mfma_f32_16x16x32_bf16 v[252:255],a[176:179],v[148:151],v[252:255]
s_xor_b32 s49,s50,65536
v_exp_f32 v216,v216
ds_read_b128 a[148:151],v1 offset:4352
v_exp_f32 v217,v217
ds_read_b128 a[144:147],v1 offset:4096
v_cvt_pk_bf16_f32 v204,v216,v217
v_exp_f32 v218,v218
buffer_load_dwordx4 v4,s[20:23],s47 offen offset:1024 nt lds
ds_read_b128 a[160:163],v1 offset:8192
v_exp_f32 v219,v219
ds_read_b128 a[164:167],v1 offset:8448
v_cvt_pk_bf16_f32 v205,v218,v219
v_exp_f32 v220,v220
ds_read_b128 a[168:171],v1 offset:8704
v_exp_f32 v221,v221
ds_read_b128 a[172:175],v1 offset:8960
v_cvt_pk_bf16_f32 v206,v220,v221
v_exp_f32 v222,v222
ds_read_b128 a[188:191],v1 offset:13056
v_exp_f32 v223,v223
ds_read_b128 a[184:187],v1 offset:12800
v_cvt_pk_bf16_f32 v207,v222,v223
s_waitcnt lgkmcnt(11)
v_mfma_f32_16x16x32_bf16 v[136:139],a[128:131],v[128:131],v[32:35]
ds_read_b128 a[180:183],v1 offset:12544
v_mfma_f32_16x16x32_bf16 v[136:139],a[132:135],v[160:163],v[136:139]
buffer_load_dwordx4 v5,s[20:23],s47 offen offset:2048 nt lds
v_mfma_f32_16x16x32_bf16 v[136:139],a[136:139],v[192:195],v[136:139]
s_waitcnt lgkmcnt(11)
ds_read_b128 a[176:179],v1 offset:12288
v_add_u32 v1,s48,v1
v_mfma_f32_16x16x32_bf16 v[136:139],a[140:143],v[224:227],v[136:139]
s_waitcnt lgkmcnt(11)
v_exp_f32 v232,v232
v_mfma_f32_16x16x32_bf16 v[140:143],a[156:159],v[224:227],v[32:35]
s_waitcnt lgkmcnt(10)
v_exp_f32 v233,v233
v_mfma_f32_16x16x32_bf16 v[140:143],a[152:155],v[192:195],v[140:143]
s_waitcnt lgkmcnt(9)
v_exp_f32 v234,v234
v_mfma_f32_16x16x32_bf16 v[140:143],a[148:151],v[160:163],v[140:143]
s_waitcnt lgkmcnt(8)
v_exp_f32 v235,v235
v_mfma_f32_16x16x32_bf16 v[140:143],a[144:147],v[128:131],v[140:143]
s_waitcnt lgkmcnt(2)
v_exp_f32 v236,v236
v_mfma_f32_16x16x32_bf16 v[152:155],a[160:163],v[128:131],v[32:35]
v_exp_f32 v237,v237
v_mfma_f32_16x16x32_bf16 v[152:155],a[164:167],v[160:163],v[152:155]
v_exp_f32 v238,v238
v_mfma_f32_16x16x32_bf16 v[152:155],a[168:171],v[192:195],v[152:155]
v_exp_f32 v239,v239
v_mfma_f32_16x16x32_bf16 v[152:155],a[172:175],v[224:227],v[152:155]
v_exp_f32 v248,v248
v_mfma_f32_16x16x32_bf16 v[156:159],a[188:191],v[224:227],v[32:35]
v_exp_f32 v249,v249
v_mfma_f32_16x16x32_bf16 v[156:159],a[184:187],v[192:195],v[156:159]
s_waitcnt lgkmcnt(0)
v_exp_f32 v250,v250
v_mfma_f32_16x16x32_bf16 v[156:159],a[180:183],v[160:163],v[156:159]
v_exp_f32 v251,v251
v_mfma_f32_16x16x32_bf16 v[156:159],a[176:179],v[128:131],v[156:159]
buffer_load_dwordx4 v5,s[20:23],s47 offen offset:3072 nt lds
v_mfma_f32_16x16x32_bf16 a[64:67],a[192:195],v[200:203],a[64:67]
v_exp_f32 v252,v252
v_mfma_f32_16x16x32_bf16 a[64:67],a[196:199],v[204:207],a[64:67]
v_exp_f32 v253,v253
v_mfma_f32_16x16x32_bf16 a[68:71],a[204:207],v[204:207],a[68:71]
v_exp_f32 v254,v254
v_mfma_f32_16x16x32_bf16 a[68:71],a[200:203],v[200:203],a[68:71]
v_exp_f32 v255,v255
v_mfma_f32_16x16x32_bf16 a[72:75],a[208:211],v[200:203],a[72:75]
v_exp_f32 v136,v136
v_mfma_f32_16x16x32_bf16 a[72:75],a[212:215],v[204:207],a[72:75]
v_exp_f32 v137,v137
v_mfma_f32_16x16x32_bf16 a[76:79],a[220:223],v[204:207],a[76:79]
v_exp_f32 v138,v138
v_mfma_f32_16x16x32_bf16 a[76:79],a[216:219],v[200:203],a[76:79]
v_exp_f32 v139,v139
v_mfma_f32_16x16x32_bf16 a[80:83],a[224:227],v[200:203],a[80:83]
v_exp_f32 v140,v140
v_mfma_f32_16x16x32_bf16 a[80:83],a[228:231],v[204:207],a[80:83]
v_exp_f32 v141,v141
v_mfma_f32_16x16x32_bf16 a[84:87],a[236:239],v[204:207],a[84:87]
s_addk_i32 m0,16384
v_exp_f32 v142,v142
v_mfma_f32_16x16x32_bf16 a[84:87],a[232:235],v[200:203],a[84:87]
v_exp_f32 v143,v143
v_mfma_f32_16x16x32_bf16 a[88:91],a[240:243],v[200:203],a[88:91]
v_exp_f32 v152,v152
v_mfma_f32_16x16x32_bf16 a[88:91],a[244:247],v[204:207],a[88:91]
buffer_load_dwordx4 v4,s[24:27],s47 offen nt lds
v_mfma_f32_16x16x32_bf16 a[92:95],a[252:255],v[204:207],a[92:95]
v_cvt_pk_bf16_f32 v232,v232,v233
v_cvt_pk_bf16_f32 v233,v234,v235
v_mfma_f32_16x16x32_bf16 a[92:95],a[248:251],v[200:203],a[92:95]
v_cvt_pk_bf16_f32 v234,v236,v237
v_cvt_pk_bf16_f32 v235,v238,v239
v_smfmac_f32_16x16x64_bf16 v[56:59],v[28:31],v[200:207],v27
.p2align 3
v_cvt_pk_bf16_f32 v236,v248,v249
v_cvt_pk_bf16_f32 v237,v250,v251
v_cvt_pk_bf16_f32 v238,v252,v253
v_cvt_pk_bf16_f32 v239,v254,v255
v_exp_f32 v153,v153
v_exp_f32 v154,v154
v_exp_f32 v155,v155
v_exp_f32 v156,v156
v_exp_f32 v157,v157
v_exp_f32 v158,v158
v_exp_f32 v159,v159
buffer_load_dwordx4 v5,s[24:27],s47 offen offset:1024 nt lds
v_mfma_f32_16x16x32_bf16 a[96:99],a[192:195],v[232:235],a[96:99]
ds_read_b64_tr_b16 a[192:193],v2
v_mfma_f32_16x16x32_bf16 a[96:99],a[196:199],v[236:239],a[96:99]
ds_read_b64_tr_b16 a[194:195],v2 offset:4096
v_mfma_f32_16x16x32_bf16 a[100:103],a[204:207],v[236:239],a[100:103]
ds_read_b64_tr_b16 a[196:197],v2 offset:8192
v_mfma_f32_16x16x32_bf16 a[100:103],a[200:203],v[232:235],a[100:103]
v_mfma_f32_16x16x32_bf16 a[104:107],a[208:211],v[232:235],a[104:107]
v_mfma_f32_16x16x32_bf16 a[104:107],a[212:215],v[236:239],a[104:107]
ds_read_b64_tr_b16 a[198:199],v2 offset:12288
v_mfma_f32_16x16x32_bf16 a[108:111],a[220:223],v[236:239],a[108:111]
v_mfma_f32_16x16x32_bf16 a[108:111],a[216:219],v[232:235],a[108:111]
v_mfma_f32_16x16x32_bf16 a[112:115],a[224:227],v[232:235],a[112:115]
buffer_load_dwordx4 v4,s[24:27],s47 offen offset:2048 nt lds
v_mfma_f32_16x16x32_bf16 a[112:115],a[228:231],v[236:239],a[112:115]
ds_read_b64_tr_b16 a[204:205],v3 offset:8192
v_mfma_f32_16x16x32_bf16 a[116:119],a[236:239],v[236:239],a[116:119]
ds_read_b64_tr_b16 a[206:207],v3 offset:12288
v_cvt_pk_bf16_f32 v136,v136,v137
v_mfma_f32_16x16x32_bf16 a[116:119],a[232:235],v[232:235],a[116:119]
v_mfma_f32_16x16x32_bf16 a[120:123],a[240:243],v[232:235],a[120:123]
ds_read_b64_tr_b16 a[200:201],v3
v_mfma_f32_16x16x32_bf16 a[120:123],a[244:247],v[236:239],a[120:123]
ds_read_b64_tr_b16 a[202:203],v3 offset:4096
v_cvt_pk_bf16_f32 v137,v138,v139
v_mfma_f32_16x16x32_bf16 a[124:127],a[252:255],v[236:239],a[124:127]
ds_read_b64_tr_b16 a[208:209],v2 offset:256
v_mfma_f32_16x16x32_bf16 a[124:127],a[248:251],v[232:235],a[124:127]
ds_read_b64_tr_b16 a[210:211],v2 offset:4352
v_smfmac_f32_16x16x64_bf16 v[60:63],v[28:31],v[232:239],v27
ds_read_b64_tr_b16 a[212:213],v2 offset:8448
v_mfma_f32_16x16x32_bf16 v[200:203],a[128:131],v[144:147],v[40:43]
ds_read_b64_tr_b16 a[214:215],v2 offset:12544
v_cvt_pk_bf16_f32 v138,v140,v141
v_mfma_f32_16x16x32_bf16 v[200:203],a[132:135],v[176:179],v[200:203]
ds_read_b64_tr_b16 a[220:221],v3 offset:8448
v_mfma_f32_16x16x32_bf16 v[200:203],a[136:139],v[208:211],v[200:203]
ds_read_b64_tr_b16 a[222:223],v3 offset:12544
v_cvt_pk_bf16_f32 v139,v142,v143
v_mfma_f32_16x16x32_bf16 v[200:203],a[140:143],v[240:243],v[200:203]
ds_read_b64_tr_b16 a[216:217],v3 offset:256
v_mfma_f32_16x16x32_bf16 v[204:207],a[156:159],v[240:243],v[40:43]
ds_read_b64_tr_b16 a[218:219],v3 offset:4352
v_mfma_f32_16x16x32_bf16 v[204:207],a[152:155],v[208:211],v[204:207]
buffer_load_dwordx4 v5,s[24:27],s47 offen offset:3072 nt lds
v_mfma_f32_16x16x32_bf16 v[204:207],a[148:151],v[176:179],v[204:207]
s_or_b32 s47,s47,0x18000
ds_read_b64_tr_b16 a[224:225],v2 offset:512
v_cvt_pk_bf16_f32 v140,v152,v153
v_mfma_f32_16x16x32_bf16 v[204:207],a[144:147],v[144:147],v[204:207]
s_addk_i32 s47,4096
s_and_b32 s47,s47,0x27000
ds_read_b64_tr_b16 a[226:227],v2 offset:4608
v_mfma_f32_16x16x32_bf16 v[216:219],a[160:163],v[144:147],v[40:43]
s_or_b32 s47,s47,s46
ds_read_b64_tr_b16 a[228:229],v2 offset:8704
v_cvt_pk_bf16_f32 v141,v154,v155
v_mfma_f32_16x16x32_bf16 v[216:219],a[164:167],v[176:179],v[216:219]
ds_read_b64_tr_b16 a[230:231],v2 offset:12800
v_mfma_f32_16x16x32_bf16 v[216:219],a[168:171],v[208:211],v[216:219]
ds_read_b64_tr_b16 a[236:237],v3 offset:8704
v_mfma_f32_16x16x32_bf16 v[216:219],a[172:175],v[240:243],v[216:219]
ds_read_b64_tr_b16 a[238:239],v3 offset:12800
v_cvt_pk_bf16_f32 v142,v156,v157
v_mfma_f32_16x16x32_bf16 v[220:223],a[188:191],v[240:243],v[40:43]
ds_read_b64_tr_b16 a[232:233],v3 offset:512
v_mfma_f32_16x16x32_bf16 v[220:223],a[184:187],v[208:211],v[220:223]
ds_read_b64_tr_b16 a[234:235],v3 offset:4608
v_mfma_f32_16x16x32_bf16 v[220:223],a[180:183],v[176:179],v[220:223]
ds_read_b64_tr_b16 a[240:241],v2 offset:768
v_cvt_pk_bf16_f32 v143,v158,v159
v_mfma_f32_16x16x32_bf16 v[220:223],a[176:179],v[144:147],v[220:223]
s_waitcnt lgkmcnt(7)
buffer_load_dword v26,v24,s[88:91],s93 offen
v_mfma_f32_16x16x32_bf16 a[0:3],a[192:195],v[136:139],a[0:3]
ds_read_b64_tr_b16 a[242:243],v2 offset:4864
v_mfma_f32_16x16x32_bf16 a[0:3],a[196:199],v[140:143],a[0:3]
ds_read_b64_tr_b16 a[244:245],v2 offset:8960
v_mfma_f32_16x16x32_bf16 a[4:7],a[204:207],v[140:143],a[4:7]
ds_read_b64_tr_b16 a[246:247],v2 offset:13056
v_mfma_f32_16x16x32_bf16 a[4:7],a[200:203],v[136:139],a[4:7]
ds_read_b64_tr_b16 a[252:253],v3 offset:8960
v_mfma_f32_16x16x32_bf16 a[8:11],a[208:211],v[136:139],a[8:11]
ds_read_b64_tr_b16 a[254:255],v3 offset:13056
v_mfma_f32_16x16x32_bf16 a[8:11],a[212:215],v[140:143],a[8:11]
ds_read_b64_tr_b16 a[248:249],v3 offset:768
v_mfma_f32_16x16x32_bf16 a[12:15],a[220:223],v[140:143],a[12:15]
ds_read_b64_tr_b16 a[250:251],v3 offset:4864
v_mfma_f32_16x16x32_bf16 a[12:15],a[216:219],v[136:139],a[12:15]
s_bitcmp1_b32 s45,0
s_cbranch_scc0 L_t31v11_%=
s_mov_b32 m0,s80
s_addk_i32 s80,4096
buffer_load_dwordx4 v25,s[24:27],s83 offen nt lds
s_addk_i32 s83,1024
L_t31v11_%=:
v_mfma_f32_16x16x32_bf16 a[16:19],a[224:227],v[136:139],a[16:19]
s_waitcnt lgkmcnt(6)
v_mfma_f32_16x16x32_bf16 a[16:19],a[228:231],v[140:143],a[16:19]
v_mfma_f32_16x16x32_bf16 a[20:23],a[236:239],v[140:143],a[20:23]
v_mfma_f32_16x16x32_bf16 a[20:23],a[232:235],v[136:139],a[20:23]
v_mfma_f32_16x16x32_bf16 a[24:27],a[240:243],v[136:139],a[24:27]
s_waitcnt lgkmcnt(2)
v_mfma_f32_16x16x32_bf16 a[24:27],a[244:247],v[140:143],a[24:27]
v_mfma_f32_16x16x32_bf16 a[28:31],a[252:255],v[140:143],a[28:31]
s_waitcnt lgkmcnt(0)
v_mfma_f32_16x16x32_bf16 a[28:31],a[248:251],v[136:139],a[28:31]
s_cmp_lg_u32 s53,0
v_smfmac_f32_16x16x64_bf16 v[48:51],v[28:31],v[136:143],v27
s_cbranch_scc1 L_att1v1_%=
s_waitcnt lgkmcnt(0)
s_nop 7
s_branch L_att1x_%=
.p2align 5
L_att1v2_%=:
v_mfma_f32_16x16x32_bf16 v[232:235],a[128:131],v[148:151],v[44:47]
s_waitcnt vmcnt(8) lgkmcnt(0)
v_mfma_f32_16x16x32_bf16 v[232:235],a[132:135],v[180:183],v[232:235]
)A"
R"A(s_barrier
ds_read_b128 a[128:131],v1
v_add_u32 v2,s48,v2
v_mfma_f32_16x16x32_bf16 v[232:235],a[136:139],v[212:215],v[232:235]
s_mov_b32 s90,L_out_end_%=-L_pc_%=+128
v_mfma_f32_16x16x32_bf16 v[232:235],a[140:143],v[244:247],v[232:235]
s_add_u32 m0,s49,s51
ds_read_b128 a[132:135],v1 offset:256
v_mfma_f32_16x16x32_bf16 v[236:239],a[156:159],v[244:247],v[44:47]
s_lshr_b32 s45,s45,1
v_mfma_f32_16x16x32_bf16 v[236:239],a[152:155],v[212:215],v[236:239]
ds_read_b128 a[136:139],v1 offset:512
v_add_u32 v3,s48,v3
v_mfma_f32_16x16x32_bf16 v[236:239],a[148:151],v[180:183],v[236:239]
buffer_load_dwordx4 v4,s[20:23],s47 offen nt lds
v_mfma_f32_16x16x32_bf16 v[236:239],a[144:147],v[148:151],v[236:239]
v_mfma_f32_16x16x32_bf16 v[248:251],a[160:163],v[148:151],v[44:47]
s_add_u32 s62,s50,32768
ds_read_b128 a[140:143],v1 offset:768
v_mfma_f32_16x16x32_bf16 v[248:251],a[164:167],v[180:183],v[248:251]
s_and_b32 s62,s62,131071
v_mfma_f32_16x16x32_bf16 v[248:251],a[168:171],v[212:215],v[248:251]
s_addk_i32 s54,16384
v_mfma_f32_16x16x32_bf16 v[248:251],a[172:175],v[244:247],v[248:251]
s_sub_u32 s48,s62,s50
ds_read_b128 a[156:159],v1 offset:4864
v_mfma_f32_16x16x32_bf16 v[252:255],a[188:191],v[244:247],v[44:47]
s_sub_u32 s53,s53,1
v_mfma_f32_16x16x32_bf16 v[252:255],a[184:187],v[212:215],v[252:255]
s_and_b32 s46,s54,0xfffc0000
v_mfma_f32_16x16x32_bf16 v[252:255],a[180:183],v[180:183],v[252:255]
s_mov_b32 s50,s62
ds_read_b128 a[152:155],v1 offset:4608
v_mfma_f32_16x16x32_bf16 v[252:255],a[176:179],v[148:151],v[252:255]
s_xor_b32 s49,s50,65536
v_mfma_f32_16x16x32_bf16 a[32:35],a[192:195],v[168:171],a[32:35]
ds_read_b128 a[148:151],v1 offset:4352
v_mfma_f32_16x16x32_bf16 a[32:35],a[196:199],v[172:175],a[32:35]
v_mfma_f32_16x16x32_bf16 a[36:39],a[204:207],v[172:175],a[36:39]
ds_read_b128 a[144:147],v1 offset:4096
v_mfma_f32_16x16x32_bf16 a[36:39],a[200:203],v[168:171],a[36:39]
v_mfma_f32_16x16x32_bf16 a[40:43],a[208:211],v[168:171],a[40:43]
buffer_load_dwordx4 v4,s[20:23],s47 offen offset:1024 nt lds
v_mfma_f32_16x16x32_bf16 a[40:43],a[212:215],v[172:175],a[40:43]
ds_read_b128 a[160:163],v1 offset:8192
v_mfma_f32_16x16x32_bf16 a[44:47],a[220:223],v[172:175],a[44:47]
v_mfma_f32_16x16x32_bf16 a[44:47],a[216:219],v[168:171],a[44:47]
ds_read_b128 a[164:167],v1 offset:8448
v_mfma_f32_16x16x32_bf16 a[48:51],a[224:227],v[168:171],a[48:51]
v_mfma_f32_16x16x32_bf16 a[48:51],a[228:231],v[172:175],a[48:51]
ds_read_b128 a[168:171],v1 offset:8704
v_mfma_f32_16x16x32_bf16 a[52:55],a[236:239],v[172:175],a[52:55]
v_mfma_f32_16x16x32_bf16 a[52:55],a[232:235],v[168:171],a[52:55]
ds_read_b128 a[172:175],v1 offset:8960
v_mfma_f32_16x16x32_bf16 a[56:59],a[240:243],v[168:171],a[56:59]
v_mfma_f32_16x16x32_bf16 a[56:59],a[244:247],v[172:175],a[56:59]
ds_read_b128 a[188:191],v1 offset:13056
v_mfma_f32_16x16x32_bf16 a[60:63],a[252:255],v[172:175],a[60:63]
v_mfma_f32_16x16x32_bf16 a[60:63],a[248:251],v[168:171],a[60:63]
ds_read_b128 a[184:187],v1 offset:12800
v_smfmac_f32_16x16x64_bf16 v[52:55],v[28:31],v[168:175],v27
s_waitcnt lgkmcnt(4)
v_mfma_f32_16x16x32_bf16 v[136:139],a[128:131],v[128:131],v[32:35]
ds_read_b128 a[180:183],v1 offset:12544
v_mfma_f32_16x16x32_bf16 v[136:139],a[132:135],v[160:163],v[136:139]
buffer_load_dwordx4 v5,s[20:23],s47 offen offset:2048 nt lds
v_mfma_f32_16x16x32_bf16 v[136:139],a[136:139],v[192:195],v[136:139]
ds_read_b128 a[176:179],v1 offset:12288
v_add_u32 v1,s48,v1
v_mfma_f32_16x16x32_bf16 v[136:139],a[140:143],v[224:227],v[136:139]
v_exp_f32 v232,v232
v_mfma_f32_16x16x32_bf16 v[140:143],a[156:159],v[224:227],v[32:35]
v_exp_f32 v233,v233
v_mfma_f32_16x16x32_bf16 v[140:143],a[152:155],v[192:195],v[140:143]
v_exp_f32 v234,v234
v_mfma_f32_16x16x32_bf16 v[140:143],a[148:151],v[160:163],v[140:143]
v_exp_f32 v235,v235
v_mfma_f32_16x16x32_bf16 v[140:143],a[144:147],v[128:131],v[140:143]
v_exp_f32 v236,v236
v_mfma_f32_16x16x32_bf16 v[152:155],a[160:163],v[128:131],v[32:35]
v_exp_f32 v237,v237
v_mfma_f32_16x16x32_bf16 v[152:155],a[164:167],v[160:163],v[152:155]
s_waitcnt lgkmcnt(1)
v_exp_f32 v238,v238
v_mfma_f32_16x16x32_bf16 v[152:155],a[168:171],v[192:195],v[152:155]
v_exp_f32 v239,v239
v_mfma_f32_16x16x32_bf16 v[152:155],a[172:175],v[224:227],v[152:155]
v_exp_f32 v248,v248
v_mfma_f32_16x16x32_bf16 v[156:159],a[188:191],v[224:227],v[32:35]
v_exp_f32 v249,v249
v_mfma_f32_16x16x32_bf16 v[156:159],a[184:187],v[192:195],v[156:159]
v_exp_f32 v250,v250
v_mfma_f32_16x16x32_bf16 v[156:159],a[180:183],v[160:163],v[156:159]
s_waitcnt lgkmcnt(0)
v_exp_f32 v251,v251
v_mfma_f32_16x16x32_bf16 v[156:159],a[176:179],v[128:131],v[156:159]
buffer_load_dwordx4 v5,s[20:23],s47 offen offset:3072 nt lds
v_exp_f32 v252,v252
v_exp_f32 v253,v253
v_exp_f32 v254,v254
v_exp_f32 v255,v255
v_exp_f32 v136,v136
v_exp_f32 v137,v137
v_exp_f32 v138,v138
v_exp_f32 v139,v139
v_exp_f32 v140,v140
v_exp_f32 v141,v141
s_addk_i32 m0,16384
v_exp_f32 v142,v142
v_exp_f32 v143,v143
v_exp_f32 v152,v152
buffer_load_dwordx4 v4,s[24:27],s47 offen nt lds
.p2align 3
v_cvt_pk_bf16_f32 v232,v232,v233
v_cvt_pk_bf16_f32 v233,v234,v235
v_cvt_pk_bf16_f32 v234,v236,v237
v_cvt_pk_bf16_f32 v235,v238,v239
v_cvt_pk_bf16_f32 v236,v248,v249
v_mfma_f32_16x16x32_bf16 v[168:171],a[128:131],v[132:135],v[36:39]
v_cvt_pk_bf16_f32 v237,v250,v251
v_cvt_pk_bf16_f32 v238,v252,v253
v_mfma_f32_16x16x32_bf16 v[168:171],a[132:135],v[164:167],v[168:171]
v_cvt_pk_bf16_f32 v239,v254,v255
v_mfma_f32_16x16x32_bf16 v[168:171],a[136:139],v[196:199],v[168:171]
v_exp_f32 v153,v153
v_mfma_f32_16x16x32_bf16 v[168:171],a[140:143],v[228:231],v[168:171]
v_exp_f32 v154,v154
v_mfma_f32_16x16x32_bf16 v[172:175],a[156:159],v[228:231],v[36:39]
v_exp_f32 v155,v155
v_mfma_f32_16x16x32_bf16 v[172:175],a[152:155],v[196:199],v[172:175]
v_exp_f32 v156,v156
v_mfma_f32_16x16x32_bf16 v[172:175],a[148:151],v[164:167],v[172:175]
v_exp_f32 v157,v157
v_mfma_f32_16x16x32_bf16 v[172:175],a[144:147],v[132:135],v[172:175]
v_exp_f32 v158,v158
v_mfma_f32_16x16x32_bf16 v[184:187],a[160:163],v[132:135],v[36:39]
v_exp_f32 v159,v159
v_mfma_f32_16x16x32_bf16 v[184:187],a[164:167],v[164:167],v[184:187]
v_exp_f32 v168,v168
v_mfma_f32_16x16x32_bf16 v[184:187],a[168:171],v[196:199],v[184:187]
buffer_load_dwordx4 v5,s[24:27],s47 offen offset:1024 nt lds
v_mfma_f32_16x16x32_bf16 v[184:187],a[172:175],v[228:231],v[184:187]
v_exp_f32 v169,v169
v_mfma_f32_16x16x32_bf16 v[188:191],a[188:191],v[228:231],v[36:39]
v_exp_f32 v170,v170
v_mfma_f32_16x16x32_bf16 v[188:191],a[184:187],v[196:199],v[188:191]
v_exp_f32 v171,v171
v_mfma_f32_16x16x32_bf16 v[188:191],a[180:183],v[164:167],v[188:191]
v_exp_f32 v172,v172
v_mfma_f32_16x16x32_bf16 v[188:191],a[176:179],v[132:135],v[188:191]
v_exp_f32 v173,v173
v_mfma_f32_16x16x32_bf16 a[96:99],a[192:195],v[232:235],a[96:99]
ds_read_b64_tr_b16 a[192:193],v2
v_cvt_pk_bf16_f32 v168,v168,v169
v_mfma_f32_16x16x32_bf16 a[96:99],a[196:199],v[236:239],a[96:99]
ds_read_b64_tr_b16 a[194:195],v2 offset:4096
v_cvt_pk_bf16_f32 v169,v170,v171
v_mfma_f32_16x16x32_bf16 a[100:103],a[204:207],v[236:239],a[100:103]
ds_read_b64_tr_b16 a[196:197],v2 offset:8192
v_cvt_pk_bf16_f32 v170,v172,v173
v_mfma_f32_16x16x32_bf16 a[100:103],a[200:203],v[232:235],a[100:103]
v_exp_f32 v174,v174
v_mfma_f32_16x16x32_bf16 a[104:107],a[208:211],v[232:235],a[104:107]
v_exp_f32 v175,v175
v_mfma_f32_16x16x32_bf16 a[104:107],a[212:215],v[236:239],a[104:107]
ds_read_b64_tr_b16 a[198:199],v2 offset:12288
v_cvt_pk_bf16_f32 v171,v174,v175
v_mfma_f32_16x16x32_bf16 a[108:111],a[220:223],v[236:239],a[108:111]
v_exp_f32 v184,v184
v_mfma_f32_16x16x32_bf16 a[108:111],a[216:219],v[232:235],a[108:111]
v_exp_f32 v185,v185
v_mfma_f32_16x16x32_bf16 a[112:115],a[224:227],v[232:235],a[112:115]
buffer_load_dwordx4 v4,s[24:27],s47 offen offset:2048 nt lds
v_mfma_f32_16x16x32_bf16 a[112:115],a[228:231],v[236:239],a[112:115]
ds_read_b64_tr_b16 a[204:205],v3 offset:8192
v_cvt_pk_bf16_f32 v172,v184,v185
v_mfma_f32_16x16x32_bf16 a[116:119],a[236:239],v[236:239],a[116:119]
ds_read_b64_tr_b16 a[206:207],v3 offset:12288
v_cvt_pk_bf16_f32 v136,v136,v137
v_mfma_f32_16x16x32_bf16 a[116:119],a[232:235],v[232:235],a[116:119]
v_exp_f32 v186,v186
v_mfma_f32_16x16x32_bf16 a[120:123],a[240:243],v[232:235],a[120:123]
ds_read_b64_tr_b16 a[200:201],v3
v_mfma_f32_16x16x32_bf16 a[120:123],a[244:247],v[236:239],a[120:123]
ds_read_b64_tr_b16 a[202:203],v3 offset:4096
v_cvt_pk_bf16_f32 v137,v138,v139
v_mfma_f32_16x16x32_bf16 a[124:127],a[252:255],v[236:239],a[124:127]
ds_read_b64_tr_b16 a[208:209],v2 offset:256
v_mfma_f32_16x16x32_bf16 a[124:127],a[248:251],v[232:235],a[124:127]
ds_read_b64_tr_b16 a[210:211],v2 offset:4352
v_smfmac_f32_16x16x64_bf16 v[60:63],v[28:31],v[232:239],v27
ds_read_b64_tr_b16 a[212:213],v2 offset:8448
ds_read_b64_tr_b16 a[214:215],v2 offset:12544
v_cvt_pk_bf16_f32 v138,v140,v141
ds_read_b64_tr_b16 a[220:221],v3 offset:8448
ds_read_b64_tr_b16 a[222:223],v3 offset:12544
v_cvt_pk_bf16_f32 v139,v142,v143
ds_read_b64_tr_b16 a[216:217],v3 offset:256
ds_read_b64_tr_b16 a[218:219],v3 offset:4352
buffer_load_dwordx4 v5,s[24:27],s47 offen offset:3072 nt lds
s_or_b32 s47,s47,0x18000
ds_read_b64_tr_b16 a[224:225],v2 offset:512
v_cvt_pk_bf16_f32 v140,v152,v153
s_addk_i32 s47,4096
s_and_b32 s47,s47,0x27000
ds_read_b64_tr_b16 a[226:227],v2 offset:4608
s_or_b32 s47,s47,s46
ds_read_b64_tr_b16 a[228:229],v2 offset:8704
v_cvt_pk_bf16_f32 v141,v154,v155
ds_read_b64_tr_b16 a[230:231],v2 offset:12800
ds_read_b64_tr_b16 a[236:237],v3 offset:8704
ds_read_b64_tr_b16 a[238:239],v3 offset:12800
v_cvt_pk_bf16_f32 v142,v156,v157
ds_read_b64_tr_b16 a[232:233],v3 offset:512
ds_read_b64_tr_b16 a[234:235],v3 offset:4608
ds_read_b64_tr_b16 a[240:241],v2 offset:768
v_cvt_pk_bf16_f32 v143,v158,v159
buffer_load_dword v26,v24,s[88:91],s93 offen
s_waitcnt lgkmcnt(15)
v_mfma_f32_16x16x32_bf16 a[0:3],a[192:195],v[136:139],a[0:3]
ds_read_b64_tr_b16 a[242:243],v2 offset:4864
v_mfma_f32_16x16x32_bf16 a[0:3],a[196:199],v[140:143],a[0:3]
ds_read_b64_tr_b16 a[244:245],v2 offset:8960
v_mfma_f32_16x16x32_bf16 a[4:7],a[204:207],v[140:143],a[4:7]
ds_read_b64_tr_b16 a[246:247],v2 offset:13056
v_mfma_f32_16x16x32_bf16 a[4:7],a[200:203],v[136:139],a[4:7]
ds_read_b64_tr_b16 a[252:253],v3 offset:8960
v_mfma_f32_16x16x32_bf16 a[8:11],a[208:211],v[136:139],a[8:11]
s_waitcnt lgkmcnt(15)
ds_read_b64_tr_b16 a[254:255],v3 offset:13056
v_mfma_f32_16x16x32_bf16 a[8:11],a[212:215],v[140:143],a[8:11]
ds_read_b64_tr_b16 a[248:249],v3 offset:768
v_mfma_f32_16x16x32_bf16 a[12:15],a[220:223],v[140:143],a[12:15]
s_waitcnt lgkmcnt(15)
ds_read_b64_tr_b16 a[250:251],v3 offset:4864
v_mfma_f32_16x16x32_bf16 a[12:15],a[216:219],v[136:139],a[12:15]
s_bitcmp1_b32 s45,0
s_cbranch_scc0 L_t31v21_%=
s_mov_b32 m0,s80
s_addk_i32 s80,4096
buffer_load_dwordx4 v25,s[24:27],s83 offen nt lds
s_addk_i32 s83,1024
L_t31v21_%=:
s_waitcnt lgkmcnt(7)
v_mfma_f32_16x16x32_bf16 a[16:19],a[224:227],v[136:139],a[16:19]
v_exp_f32 v187,v187
v_mfma_f32_16x16x32_bf16 a[16:19],a[228:231],v[140:143],a[16:19]
v_exp_f32 v188,v188
v_mfma_f32_16x16x32_bf16 a[20:23],a[236:239],v[140:143],a[20:23]
v_exp_f32 v189,v189
v_mfma_f32_16x16x32_bf16 a[20:23],a[232:235],v[136:139],a[20:23]
s_waitcnt lgkmcnt(3)
v_exp_f32 v190,v190
v_mfma_f32_16x16x32_bf16 a[24:27],a[240:243],v[136:139],a[24:27]
v_exp_f32 v191,v191
v_mfma_f32_16x16x32_bf16 a[24:27],a[244:247],v[140:143],a[24:27]
s_waitcnt lgkmcnt(1)
v_cvt_pk_bf16_f32 v173,v186,v187
v_mfma_f32_16x16x32_bf16 a[28:31],a[252:255],v[140:143],a[28:31]
s_waitcnt lgkmcnt(0)
v_cvt_pk_bf16_f32 v174,v188,v189
v_cvt_pk_bf16_f32 v175,v190,v191
v_mfma_f32_16x16x32_bf16 a[28:31],a[248:251],v[136:139],a[28:31]
s_cmp_lg_u32 s53,0
v_smfmac_f32_16x16x64_bf16 v[48:51],v[28:31],v[136:143],v27
s_cbranch_scc1 L_att1v2_%=
s_waitcnt lgkmcnt(0)
s_nop 7
s_branch L_att1x_%=
.p2align 5
L_att1_%=:
v_mfma_f32_16x16x32_bf16 v[232:235],a[128:131],v[148:151],v[44:47]
s_waitcnt vmcnt(8) lgkmcnt(0)
v_exp_f32 v200,v200
v_mfma_f32_16x16x32_bf16 v[232:235],a[132:135],v[180:183],v[232:235]
s_barrier
ds_read_b128 a[128:131],v1
v_add_u32 v2,s48,v2
v_mfma_f32_16x16x32_bf16 v[232:235],a[136:139],v[212:215],v[232:235]
)A"
R"A(s_mov_b32 s90,L_out_end_%=-L_pc_%=+128
v_exp_f32 v201,v201
v_mfma_f32_16x16x32_bf16 v[232:235],a[140:143],v[244:247],v[232:235]
s_add_u32 m0,s49,s51
ds_read_b128 a[132:135],v1 offset:256
v_cvt_pk_bf16_f32 v200,v200,v201
v_mfma_f32_16x16x32_bf16 v[236:239],a[156:159],v[244:247],v[44:47]
s_lshr_b32 s45,s45,1
v_exp_f32 v202,v202
v_mfma_f32_16x16x32_bf16 v[236:239],a[152:155],v[212:215],v[236:239]
s_sub_u32 s94,s94,3
ds_read_b128 a[136:139],v1 offset:512
v_add_u32 v3,s48,v3
v_mfma_f32_16x16x32_bf16 v[236:239],a[148:151],v[180:183],v[236:239]
s_lshr_b32 s60,s94,2
buffer_load_dwordx4 v4,s[20:23],s47 offen nt lds
v_mfma_f32_16x16x32_bf16 v[236:239],a[144:147],v[148:151],v[236:239]
s_max_i32 s60,s60,124
v_exp_f32 v203,v203
v_mfma_f32_16x16x32_bf16 v[248:251],a[160:163],v[148:151],v[44:47]
s_add_u32 s62,s50,32768
ds_read_b128 a[140:143],v1 offset:768
v_cvt_pk_bf16_f32 v201,v202,v203
v_mfma_f32_16x16x32_bf16 v[248:251],a[164:167],v[180:183],v[248:251]
s_and_b32 s62,s62,131071
v_exp_f32 v204,v204
v_mfma_f32_16x16x32_bf16 v[248:251],a[168:171],v[212:215],v[248:251]
s_addk_i32 s54,16384
v_exp_f32 v205,v205
v_mfma_f32_16x16x32_bf16 v[248:251],a[172:175],v[244:247],v[248:251]
s_sub_u32 s48,s62,s50
ds_read_b128 a[156:159],v1 offset:4864
v_cvt_pk_bf16_f32 v202,v204,v205
v_mfma_f32_16x16x32_bf16 v[252:255],a[188:191],v[244:247],v[44:47]
s_sub_u32 s53,s53,1
v_exp_f32 v206,v206
v_mfma_f32_16x16x32_bf16 v[252:255],a[184:187],v[212:215],v[252:255]
s_and_b32 s46,s54,0xfffc0000
v_exp_f32 v207,v207
v_mfma_f32_16x16x32_bf16 v[252:255],a[180:183],v[180:183],v[252:255]
s_mov_b32 s50,s62
ds_read_b128 a[152:155],v1 offset:4608
v_cvt_pk_bf16_f32 v203,v206,v207
v_mfma_f32_16x16x32_bf16 v[252:255],a[176:179],v[148:151],v[252:255]
s_xor_b32 s49,s50,65536
v_exp_f32 v216,v216
v_mfma_f32_16x16x32_bf16 a[32:35],a[192:195],v[168:171],a[32:35]
ds_read_b128 a[148:151],v1 offset:4352
v_mfma_f32_16x16x32_bf16 a[32:35],a[196:199],v[172:175],a[32:35]
v_exp_f32 v217,v217
v_mfma_f32_16x16x32_bf16 a[36:39],a[204:207],v[172:175],a[36:39]
ds_read_b128 a[144:147],v1 offset:4096
v_cvt_pk_bf16_f32 v204,v216,v217
v_mfma_f32_16x16x32_bf16 a[36:39],a[200:203],v[168:171],a[36:39]
v_exp_f32 v218,v218
v_mfma_f32_16x16x32_bf16 a[40:43],a[208:211],v[168:171],a[40:43]
buffer_load_dwordx4 v4,s[20:23],s47 offen offset:1024 nt lds
v_mfma_f32_16x16x32_bf16 a[40:43],a[212:215],v[172:175],a[40:43]
ds_read_b128 a[160:163],v1 offset:8192
v_mfma_f32_16x16x32_bf16 a[44:47],a[220:223],v[172:175],a[44:47]
v_exp_f32 v219,v219
v_mfma_f32_16x16x32_bf16 a[44:47],a[216:219],v[168:171],a[44:47]
ds_read_b128 a[164:167],v1 offset:8448
v_cvt_pk_bf16_f32 v205,v218,v219
v_mfma_f32_16x16x32_bf16 a[48:51],a[224:227],v[168:171],a[48:51]
v_exp_f32 v220,v220
v_mfma_f32_16x16x32_bf16 a[48:51],a[228:231],v[172:175],a[48:51]
ds_read_b128 a[168:171],v1 offset:8704
v_mfma_f32_16x16x32_bf16 a[52:55],a[236:239],v[172:175],a[52:55]
v_exp_f32 v221,v221
v_mfma_f32_16x16x32_bf16 a[52:55],a[232:235],v[168:171],a[52:55]
ds_read_b128 a[172:175],v1 offset:8960
v_cvt_pk_bf16_f32 v206,v220,v221
v_mfma_f32_16x16x32_bf16 a[56:59],a[240:243],v[168:171],a[56:59]
v_exp_f32 v222,v222
v_mfma_f32_16x16x32_bf16 a[56:59],a[244:247],v[172:175],a[56:59]
ds_read_b128 a[188:191],v1 offset:13056
v_mfma_f32_16x16x32_bf16 a[60:63],a[252:255],v[172:175],a[60:63]
v_exp_f32 v223,v223
v_mfma_f32_16x16x32_bf16 a[60:63],a[248:251],v[168:171],a[60:63]
ds_read_b128 a[184:187],v1 offset:12800
v_cvt_pk_bf16_f32 v207,v222,v223
v_smfmac_f32_16x16x64_bf16 v[52:55],v[28:31],v[168:175],v27
s_waitcnt lgkmcnt(4)
v_mfma_f32_16x16x32_bf16 v[136:139],a[128:131],v[128:131],v[32:35]
ds_read_b128 a[180:183],v1 offset:12544
v_mfma_f32_16x16x32_bf16 v[136:139],a[132:135],v[160:163],v[136:139]
buffer_load_dwordx4 v5,s[20:23],s47 offen offset:2048 nt lds
v_mfma_f32_16x16x32_bf16 v[136:139],a[136:139],v[192:195],v[136:139]
ds_read_b128 a[176:179],v1 offset:12288
v_add_u32 v1,s48,v1
v_mfma_f32_16x16x32_bf16 v[136:139],a[140:143],v[224:227],v[136:139]
v_exp_f32 v232,v232
v_mfma_f32_16x16x32_bf16 v[140:143],a[156:159],v[224:227],v[32:35]
v_exp_f32 v233,v233
v_mfma_f32_16x16x32_bf16 v[140:143],a[152:155],v[192:195],v[140:143]
v_exp_f32 v234,v234
v_mfma_f32_16x16x32_bf16 v[140:143],a[148:151],v[160:163],v[140:143]
v_exp_f32 v235,v235
v_mfma_f32_16x16x32_bf16 v[140:143],a[144:147],v[128:131],v[140:143]
v_exp_f32 v236,v236
v_mfma_f32_16x16x32_bf16 v[152:155],a[160:163],v[128:131],v[32:35]
v_exp_f32 v237,v237
v_mfma_f32_16x16x32_bf16 v[152:155],a[164:167],v[160:163],v[152:155]
s_waitcnt lgkmcnt(1)
v_exp_f32 v238,v238
v_mfma_f32_16x16x32_bf16 v[152:155],a[168:171],v[192:195],v[152:155]
v_exp_f32 v239,v239
v_mfma_f32_16x16x32_bf16 v[152:155],a[172:175],v[224:227],v[152:155]
v_exp_f32 v248,v248
v_mfma_f32_16x16x32_bf16 v[156:159],a[188:191],v[224:227],v[32:35]
v_exp_f32 v249,v249
v_mfma_f32_16x16x32_bf16 v[156:159],a[184:187],v[192:195],v[156:159]
v_exp_f32 v250,v250
v_mfma_f32_16x16x32_bf16 v[156:159],a[180:183],v[160:163],v[156:159]
s_waitcnt lgkmcnt(0)
v_exp_f32 v251,v251
v_mfma_f32_16x16x32_bf16 v[156:159],a[176:179],v[128:131],v[156:159]
buffer_load_dwordx4 v5,s[20:23],s47 offen offset:3072 nt lds
v_mfma_f32_16x16x32_bf16 a[64:67],a[192:195],v[200:203],a[64:67]
v_exp_f32 v252,v252
v_mfma_f32_16x16x32_bf16 a[64:67],a[196:199],v[204:207],a[64:67]
v_exp_f32 v253,v253
v_mfma_f32_16x16x32_bf16 a[68:71],a[204:207],v[204:207],a[68:71]
v_exp_f32 v254,v254
v_mfma_f32_16x16x32_bf16 a[68:71],a[200:203],v[200:203],a[68:71]
v_exp_f32 v255,v255
v_mfma_f32_16x16x32_bf16 a[72:75],a[208:211],v[200:203],a[72:75]
v_exp_f32 v136,v136
v_mfma_f32_16x16x32_bf16 a[72:75],a[212:215],v[204:207],a[72:75]
v_exp_f32 v137,v137
v_mfma_f32_16x16x32_bf16 a[76:79],a[220:223],v[204:207],a[76:79]
v_exp_f32 v138,v138
v_mfma_f32_16x16x32_bf16 a[76:79],a[216:219],v[200:203],a[76:79]
v_exp_f32 v139,v139
v_mfma_f32_16x16x32_bf16 a[80:83],a[224:227],v[200:203],a[80:83]
v_exp_f32 v140,v140
v_mfma_f32_16x16x32_bf16 a[80:83],a[228:231],v[204:207],a[80:83]
v_exp_f32 v141,v141
v_mfma_f32_16x16x32_bf16 a[84:87],a[236:239],v[204:207],a[84:87]
s_addk_i32 m0,16384
v_exp_f32 v142,v142
v_mfma_f32_16x16x32_bf16 a[84:87],a[232:235],v[200:203],a[84:87]
v_exp_f32 v143,v143
v_mfma_f32_16x16x32_bf16 a[88:91],a[240:243],v[200:203],a[88:91]
v_exp_f32 v152,v152
v_mfma_f32_16x16x32_bf16 a[88:91],a[244:247],v[204:207],a[88:91]
buffer_load_dwordx4 v4,s[24:27],s47 offen nt lds
v_mfma_f32_16x16x32_bf16 a[92:95],a[252:255],v[204:207],a[92:95]
v_cvt_pk_bf16_f32 v232,v232,v233
v_cvt_pk_bf16_f32 v233,v234,v235
v_mfma_f32_16x16x32_bf16 a[92:95],a[248:251],v[200:203],a[92:95]
v_cvt_pk_bf16_f32 v234,v236,v237
v_cvt_pk_bf16_f32 v235,v238,v239
v_smfmac_f32_16x16x64_bf16 v[56:59],v[28:31],v[200:207],v27
v_cvt_pk_bf16_f32 v236,v248,v249
v_mfma_f32_16x16x32_bf16 v[168:171],a[128:131],v[132:135],v[36:39]
v_cvt_pk_bf16_f32 v237,v250,v251
v_cvt_pk_bf16_f32 v238,v252,v253
v_mfma_f32_16x16x32_bf16 v[168:171],a[132:135],v[164:167],v[168:171]
v_cvt_pk_bf16_f32 v239,v254,v255
v_mfma_f32_16x16x32_bf16 v[168:171],a[136:139],v[196:199],v[168:171]
v_exp_f32 v153,v153
v_mfma_f32_16x16x32_bf16 v[168:171],a[140:143],v[228:231],v[168:171]
v_exp_f32 v154,v154
v_mfma_f32_16x16x32_bf16 v[172:175],a[156:159],v[228:231],v[36:39]
v_exp_f32 v155,v155
v_mfma_f32_16x16x32_bf16 v[172:175],a[152:155],v[196:199],v[172:175]
v_exp_f32 v156,v156
v_mfma_f32_16x16x32_bf16 v[172:175],a[148:151],v[164:167],v[172:175]
v_exp_f32 v157,v157
v_mfma_f32_16x16x32_bf16 v[172:175],a[144:147],v[132:135],v[172:175]
v_exp_f32 v158,v158
v_mfma_f32_16x16x32_bf16 v[184:187],a[160:163],v[132:135],v[36:39]
v_exp_f32 v159,v159
v_mfma_f32_16x16x32_bf16 v[184:187],a[164:167],v[164:167],v[184:187]
v_exp_f32 v168,v168
v_mfma_f32_16x16x32_bf16 v[184:187],a[168:171],v[196:199],v[184:187]
buffer_load_dwordx4 v5,s[24:27],s47 offen offset:1024 nt lds
v_mfma_f32_16x16x32_bf16 v[184:187],a[172:175],v[228:231],v[184:187]
v_exp_f32 v169,v169
v_mfma_f32_16x16x32_bf16 v[188:191],a[188:191],v[228:231],v[36:39]
v_exp_f32 v170,v170
v_mfma_f32_16x16x32_bf16 v[188:191],a[184:187],v[196:199],v[188:191]
v_exp_f32 v171,v171
v_mfma_f32_16x16x32_bf16 v[188:191],a[180:183],v[164:167],v[188:191]
v_exp_f32 v172,v172
v_mfma_f32_16x16x32_bf16 v[188:191],a[176:179],v[132:135],v[188:191]
v_exp_f32 v173,v173
v_mfma_f32_16x16x32_bf16 a[96:99],a[192:195],v[232:235],a[96:99]
ds_read_b64_tr_b16 a[192:193],v2
v_cvt_pk_bf16_f32 v168,v168,v169
v_mfma_f32_16x16x32_bf16 a[96:99],a[196:199],v[236:239],a[96:99]
ds_read_b64_tr_b16 a[194:195],v2 offset:4096
v_cvt_pk_bf16_f32 v169,v170,v171
v_mfma_f32_16x16x32_bf16 a[100:103],a[204:207],v[236:239],a[100:103]
ds_read_b64_tr_b16 a[196:197],v2 offset:8192
v_cvt_pk_bf16_f32 v170,v172,v173
v_mfma_f32_16x16x32_bf16 a[100:103],a[200:203],v[232:235],a[100:103]
v_exp_f32 v174,v174
v_mfma_f32_16x16x32_bf16 a[104:107],a[208:211],v[232:235],a[104:107]
v_exp_f32 v175,v175
v_mfma_f32_16x16x32_bf16 a[104:107],a[212:215],v[236:239],a[104:107]
ds_read_b64_tr_b16 a[198:199],v2 offset:12288
v_cvt_pk_bf16_f32 v171,v174,v175
v_mfma_f32_16x16x32_bf16 a[108:111],a[220:223],v[236:239],a[108:111]
v_exp_f32 v184,v184
v_mfma_f32_16x16x32_bf16 a[108:111],a[216:219],v[232:235],a[108:111]
v_exp_f32 v185,v185
v_mfma_f32_16x16x32_bf16 a[112:115],a[224:227],v[232:235],a[112:115]
buffer_load_dwordx4 v4,s[24:27],s47 offen offset:2048 nt lds
v_mfma_f32_16x16x32_bf16 a[112:115],a[228:231],v[236:239],a[112:115]
ds_read_b64_tr_b16 a[204:205],v3 offset:8192
v_cvt_pk_bf16_f32 v172,v184,v185
v_mfma_f32_16x16x32_bf16 a[116:119],a[236:239],v[236:239],a[116:119]
ds_read_b64_tr_b16 a[206:207],v3 offset:12288
v_cvt_pk_bf16_f32 v136,v136,v137
v_mfma_f32_16x16x32_bf16 a[116:119],a[232:235],v[232:235],a[116:119]
v_exp_f32 v186,v186
v_mfma_f32_16x16x32_bf16 a[120:123],a[240:243],v[232:235],a[120:123]
ds_read_b64_tr_b16 a[200:201],v3
v_mfma_f32_16x16x32_bf16 a[120:123],a[244:247],v[236:239],a[120:123]
ds_read_b64_tr_b16 a[202:203],v3 offset:4096
v_cvt_pk_bf16_f32 v137,v138,v139
v_mfma_f32_16x16x32_bf16 a[124:127],a[252:255],v[236:239],a[124:127]
ds_read_b64_tr_b16 a[208:209],v2 offset:256
v_mfma_f32_16x16x32_bf16 a[124:127],a[248:251],v[232:235],a[124:127]
ds_read_b64_tr_b16 a[210:211],v2 offset:4352
v_smfmac_f32_16x16x64_bf16 v[60:63],v[28:31],v[232:239],v27
ds_read_b64_tr_b16 a[212:213],v2 offset:8448
v_mfma_f32_16x16x32_bf16 v[200:203],a[128:131],v[144:147],v[40:43]
ds_read_b64_tr_b16 a[214:215],v2 offset:12544
v_cvt_pk_bf16_f32 v138,v140,v141
v_mfma_f32_16x16x32_bf16 v[200:203],a[132:135],v[176:179],v[200:203]
ds_read_b64_tr_b16 a[220:221],v3 offset:8448
v_mfma_f32_16x16x32_bf16 v[200:203],a[136:139],v[208:211],v[200:203]
ds_read_b64_tr_b16 a[222:223],v3 offset:12544
v_cvt_pk_bf16_f32 v139,v142,v143
v_mfma_f32_16x16x32_bf16 v[200:203],a[140:143],v[240:243],v[200:203]
ds_read_b64_tr_b16 a[216:217],v3 offset:256
v_mfma_f32_16x16x32_bf16 v[204:207],a[156:159],v[240:243],v[40:43]
ds_read_b64_tr_b16 a[218:219],v3 offset:4352
v_mfma_f32_16x16x32_bf16 v[204:207],a[152:155],v[208:211],v[204:207]
buffer_load_dwordx4 v5,s[24:27],s47 offen offset:3072 nt lds
v_mfma_f32_16x16x32_bf16 v[204:207],a[148:151],v[176:179],v[204:207]
s_or_b32 s47,s47,0x18000
ds_read_b64_tr_b16 a[224:225],v2 offset:512
v_cvt_pk_bf16_f32 v140,v152,v153
v_mfma_f32_16x16x32_bf16 v[204:207],a[144:147],v[144:147],v[204:207]
s_addk_i32 s47,4096
s_and_b32 s47,s47,0x27000
ds_read_b64_tr_b16 a[226:227],v2 offset:4608
v_mfma_f32_16x16x32_bf16 v[216:219],a[160:163],v[144:147],v[40:43]
s_or_b32 s47,s47,s46
ds_read_b64_tr_b16 a[228:229],v2 offset:8704
v_cvt_pk_bf16_f32 v141,v154,v155
v_mfma_f32_16x16x32_bf16 v[216:219],a[164:167],v[176:179],v[216:219]
ds_read_b64_tr_b16 a[230:231],v2 offset:12800
v_mfma_f32_16x16x32_bf16 v[216:219],a[168:171],v[208:211],v[216:219]
ds_read_b64_tr_b16 a[236:237],v3 offset:8704
v_mfma_f32_16x16x32_bf16 v[216:219],a[172:175],v[240:243],v[216:219]
ds_read_b64_tr_b16 a[238:239],v3 offset:12800
v_cvt_pk_bf16_f32 v142,v156,v157
v_mfma_f32_16x16x32_bf16 v[220:223],a[188:191],v[240:243],v[40:43]
ds_read_b64_tr_b16 a[232:233],v3 offset:512
)A"
R"A(v_mfma_f32_16x16x32_bf16 v[220:223],a[184:187],v[208:211],v[220:223]
ds_read_b64_tr_b16 a[234:235],v3 offset:4608
v_mfma_f32_16x16x32_bf16 v[220:223],a[180:183],v[176:179],v[220:223]
ds_read_b64_tr_b16 a[240:241],v2 offset:768
v_cvt_pk_bf16_f32 v143,v158,v159
v_mfma_f32_16x16x32_bf16 v[220:223],a[176:179],v[144:147],v[220:223]
s_waitcnt lgkmcnt(7)
buffer_load_dword v26,v24,s[88:91],s93 offen
v_mfma_f32_16x16x32_bf16 a[0:3],a[192:195],v[136:139],a[0:3]
ds_read_b64_tr_b16 a[242:243],v2 offset:4864
v_mfma_f32_16x16x32_bf16 a[0:3],a[196:199],v[140:143],a[0:3]
ds_read_b64_tr_b16 a[244:245],v2 offset:8960
v_mfma_f32_16x16x32_bf16 a[4:7],a[204:207],v[140:143],a[4:7]
ds_read_b64_tr_b16 a[246:247],v2 offset:13056
v_mfma_f32_16x16x32_bf16 a[4:7],a[200:203],v[136:139],a[4:7]
s_memrealtime s[76:77]
ds_read_b64_tr_b16 a[252:253],v3 offset:8960
v_mfma_f32_16x16x32_bf16 a[8:11],a[208:211],v[136:139],a[8:11]
ds_read_b64_tr_b16 a[254:255],v3 offset:13056
v_mfma_f32_16x16x32_bf16 a[8:11],a[212:215],v[140:143],a[8:11]
ds_read_b64_tr_b16 a[248:249],v3 offset:768
v_mfma_f32_16x16x32_bf16 a[12:15],a[220:223],v[140:143],a[12:15]
ds_read_b64_tr_b16 a[250:251],v3 offset:4864
v_mfma_f32_16x16x32_bf16 a[12:15],a[216:219],v[136:139],a[12:15]
s_bitcmp1_b32 s45,0
s_cbranch_scc0 L_t311_%=
s_mov_b32 m0,s80
s_addk_i32 s80,4096
buffer_load_dwordx4 v25,s[24:27],s83 offen nt lds
s_addk_i32 s83,1024
L_t311_%=:
v_mfma_f32_16x16x32_bf16 a[16:19],a[224:227],v[136:139],a[16:19]
s_waitcnt lgkmcnt(6)
v_exp_f32 v187,v187
v_mfma_f32_16x16x32_bf16 a[16:19],a[228:231],v[140:143],a[16:19]
v_exp_f32 v188,v188
v_mfma_f32_16x16x32_bf16 a[20:23],a[236:239],v[140:143],a[20:23]
s_waitcnt lgkmcnt(0)
v_exp_f32 v189,v189
v_mfma_f32_16x16x32_bf16 a[20:23],a[232:235],v[136:139],a[20:23]
s_sub_u32 s79,s78,s76
v_exp_f32 v190,v190
v_mfma_f32_16x16x32_bf16 a[24:27],a[240:243],v[136:139],a[24:27]
s_add_u32 s78,s76,s60
v_exp_f32 v191,v191
v_mfma_f32_16x16x32_bf16 a[24:27],a[244:247],v[140:143],a[24:27]
s_cmp_gt_i32 s79,s95
s_cbranch_scc1 L_ps7_%=
L_pr7_%=:
v_cvt_pk_bf16_f32 v173,v186,v187
v_mfma_f32_16x16x32_bf16 a[28:31],a[252:255],v[140:143],a[28:31]
v_cvt_pk_bf16_f32 v174,v188,v189
v_cvt_pk_bf16_f32 v175,v190,v191
v_mfma_f32_16x16x32_bf16 a[28:31],a[248:251],v[136:139],a[28:31]
s_cmp_lg_u32 s53,0
v_smfmac_f32_16x16x64_bf16 v[48:51],v[28:31],v[136:143],v27
s_cbranch_scc1 L_att1_%=
L_att1x_%=:
L_endseg_%=:
v_mfma_f32_16x16x32_bf16 v[232:235],a[128:131],v[148:151],v[44:47]
s_waitcnt vmcnt(10)
s_lshl_b32 s43,s41,2
v_exp_f32 v200,v200
v_mfma_f32_16x16x32_bf16 v[232:235],a[132:135],v[180:183],v[232:235]
s_barrier
ds_read_b128 a[128:131],v1
v_add_u32 v2,s48,v2
v_mfma_f32_16x16x32_bf16 v[232:235],a[136:139],v[212:215],v[232:235]
s_add_u32 s43,s43,s70
v_exp_f32 v201,v201
v_mfma_f32_16x16x32_bf16 v[232:235],a[140:143],v[244:247],v[232:235]
s_lshl_b32 s43,s43,6
ds_read_b128 a[132:135],v1 offset:256
v_cvt_pk_bf16_f32 v200,v200,v201
v_mfma_f32_16x16x32_bf16 v[236:239],a[156:159],v[244:247],v[44:47]
s_add_u32 s43,s43,s40
v_exp_f32 v202,v202
v_mfma_f32_16x16x32_bf16 v[236:239],a[152:155],v[212:215],v[236:239]
s_lshl_b32 s43,s43,15
ds_read_b128 a[136:139],v1 offset:512
v_add_u32 v3,s48,v3
v_mfma_f32_16x16x32_bf16 v[236:239],a[148:151],v[180:183],v[236:239]
s_mov_b32 s48,32768
v_exp_f32 v203,v203
v_mfma_f32_16x16x32_bf16 v[236:239],a[144:147],v[148:151],v[236:239]
s_mov_b64 s[28:29],s[12:13]
ds_read_b128 a[140:143],v1 offset:768
v_cvt_pk_bf16_f32 v201,v202,v203
v_mfma_f32_16x16x32_bf16 v[248:251],a[160:163],v[148:151],v[44:47]
s_mov_b32 s30,950272
v_exp_f32 v204,v204
v_mfma_f32_16x16x32_bf16 v[248:251],a[164:167],v[180:183],v[248:251]
s_mov_b32 s31,0x20000
ds_read_b128 a[156:159],v1 offset:4864
v_lshrrev_b32 v25,6,v6
v_mfma_f32_16x16x32_bf16 v[248:251],a[168:171],v[212:215],v[248:251]
v_exp_f32 v205,v205
v_mfma_f32_16x16x32_bf16 v[248:251],a[172:175],v[244:247],v[248:251]
ds_read_b128 a[152:155],v1 offset:4608
v_cvt_pk_bf16_f32 v202,v204,v205
v_mfma_f32_16x16x32_bf16 v[252:255],a[188:191],v[244:247],v[44:47]
v_exp_f32 v206,v206
v_mfma_f32_16x16x32_bf16 v[252:255],a[184:187],v[212:215],v[252:255]
ds_read_b128 a[148:151],v1 offset:4352
v_mul_u32_u24 v25,320,v25
v_mfma_f32_16x16x32_bf16 v[252:255],a[180:183],v[180:183],v[252:255]
v_exp_f32 v207,v207
v_mfma_f32_16x16x32_bf16 v[252:255],a[176:179],v[148:151],v[252:255]
ds_read_b128 a[144:147],v1 offset:4096
v_cvt_pk_bf16_f32 v203,v206,v207
v_mfma_f32_16x16x32_bf16 a[32:35],a[192:195],v[168:171],a[32:35]
v_exp_f32 v216,v216
v_mfma_f32_16x16x32_bf16 a[32:35],a[196:199],v[172:175],a[32:35]
s_add_u32 s28,s28,s43
s_addc_u32 s29,s29,0
ds_read_b128 a[160:163],v1 offset:8192
v_mfma_f32_16x16x32_bf16 a[36:39],a[204:207],v[172:175],a[36:39]
s_and_b32 s29,s29,0xffff
v_exp_f32 v217,v217
v_mfma_f32_16x16x32_bf16 a[36:39],a[200:203],v[168:171],a[36:39]
ds_read_b128 a[164:167],v1 offset:8448
v_cvt_pk_bf16_f32 v204,v216,v217
v_mfma_f32_16x16x32_bf16 a[40:43],a[208:211],v[168:171],a[40:43]
v_exp_f32 v218,v218
v_mfma_f32_16x16x32_bf16 a[40:43],a[212:215],v[172:175],a[40:43]
ds_read_b128 a[168:171],v1 offset:8704
v_mfma_f32_16x16x32_bf16 a[44:47],a[220:223],v[172:175],a[44:47]
v_exp_f32 v219,v219
v_mfma_f32_16x16x32_bf16 a[44:47],a[216:219],v[168:171],a[44:47]
ds_read_b128 a[172:175],v1 offset:8960
v_cvt_pk_bf16_f32 v205,v218,v219
v_mfma_f32_16x16x32_bf16 a[48:51],a[224:227],v[168:171],a[48:51]
v_exp_f32 v220,v220
v_mfma_f32_16x16x32_bf16 a[48:51],a[228:231],v[172:175],a[48:51]
ds_read_b128 a[188:191],v1 offset:13056
v_mfma_f32_16x16x32_bf16 a[52:55],a[236:239],v[172:175],a[52:55]
v_exp_f32 v221,v221
v_mfma_f32_16x16x32_bf16 a[52:55],a[232:235],v[168:171],a[52:55]
ds_read_b128 a[184:187],v1 offset:12800
v_cvt_pk_bf16_f32 v206,v220,v221
v_mfma_f32_16x16x32_bf16 a[56:59],a[240:243],v[168:171],a[56:59]
v_exp_f32 v222,v222
v_mfma_f32_16x16x32_bf16 a[56:59],a[244:247],v[172:175],a[56:59]
ds_read_b128 a[180:183],v1 offset:12544
v_mfma_f32_16x16x32_bf16 a[60:63],a[252:255],v[172:175],a[60:63]
v_exp_f32 v223,v223
v_mfma_f32_16x16x32_bf16 a[60:63],a[248:251],v[168:171],a[60:63]
ds_read_b128 a[176:179],v1 offset:12288
v_cvt_pk_bf16_f32 v207,v222,v223
v_smfmac_f32_16x16x64_bf16 v[52:55],v[28:31],v[168:175],v27
s_waitcnt lgkmcnt(4)
v_add_u32 v1,s48,v1
v_mfma_f32_16x16x32_bf16 v[136:139],a[128:131],v[128:131],v[32:35]
v_exp_f32 v232,v232
v_mfma_f32_16x16x32_bf16 v[136:139],a[132:135],v[160:163],v[136:139]
v_exp_f32 v233,v233
v_mfma_f32_16x16x32_bf16 v[136:139],a[136:139],v[192:195],v[136:139]
v_exp_f32 v234,v234
v_mfma_f32_16x16x32_bf16 v[136:139],a[140:143],v[224:227],v[136:139]
v_exp_f32 v235,v235
v_mfma_f32_16x16x32_bf16 v[140:143],a[156:159],v[224:227],v[32:35]
v_exp_f32 v236,v236
v_mfma_f32_16x16x32_bf16 v[140:143],a[152:155],v[192:195],v[140:143]
s_add_u32 s80,s71,0
s_and_b32 s80,s80,3
s_lshl_b32 s80,s80,1
v_mfma_f32_16x16x32_bf16 v[140:143],a[148:151],v[160:163],v[140:143]
s_mul_i32 s80,s80,163840
s_add_u32 s80,s80,s81
v_cvt_pk_bf16_f32 v232,v232,v233
v_mfma_f32_16x16x32_bf16 v[140:143],a[144:147],v[128:131],v[140:143]
s_add_u32 s83,s80,20480
v_exp_f32 v237,v237
v_mfma_f32_16x16x32_bf16 v[152:155],a[160:163],v[128:131],v[32:35]
v_exp_f32 v238,v238
v_mfma_f32_16x16x32_bf16 v[152:155],a[164:167],v[160:163],v[152:155]
buffer_store_dwordx4 v[64:67],v6,s[36:39],s84 offen sc1
v_mfma_f32_16x16x32_bf16 v[152:155],a[168:171],v[192:195],v[152:155]
v_exp_f32 v239,v239
v_mfma_f32_16x16x32_bf16 v[152:155],a[172:175],v[224:227],v[152:155]
s_waitcnt lgkmcnt(0)
v_exp_f32 v248,v248
v_mfma_f32_16x16x32_bf16 v[156:159],a[188:191],v[224:227],v[32:35]
v_exp_f32 v249,v249
v_mfma_f32_16x16x32_bf16 v[156:159],a[184:187],v[192:195],v[156:159]
v_exp_f32 v250,v250
v_mfma_f32_16x16x32_bf16 v[156:159],a[180:183],v[160:163],v[156:159]
v_exp_f32 v251,v251
v_mfma_f32_16x16x32_bf16 v[156:159],a[176:179],v[128:131],v[156:159]
v_exp_f32 v252,v252
v_mfma_f32_16x16x32_bf16 a[64:67],a[192:195],v[200:203],a[64:67]
v_exp_f32 v253,v253
v_mfma_f32_16x16x32_bf16 a[64:67],a[196:199],v[204:207],a[64:67]
v_exp_f32 v254,v254
v_mfma_f32_16x16x32_bf16 a[68:71],a[204:207],v[204:207],a[68:71]
v_exp_f32 v255,v255
v_mfma_f32_16x16x32_bf16 a[68:71],a[200:203],v[200:203],a[68:71]
v_exp_f32 v136,v136
v_mfma_f32_16x16x32_bf16 a[72:75],a[208:211],v[200:203],a[72:75]
v_exp_f32 v137,v137
v_mfma_f32_16x16x32_bf16 a[72:75],a[212:215],v[204:207],a[72:75]
v_cvt_pk_bf16_f32 v233,v234,v235
v_cvt_pk_bf16_f32 v234,v236,v237
v_mfma_f32_16x16x32_bf16 a[76:79],a[220:223],v[204:207],a[76:79]
v_cvt_pk_bf16_f32 v235,v238,v239
v_cvt_pk_bf16_f32 v236,v248,v249
v_mfma_f32_16x16x32_bf16 a[76:79],a[216:219],v[200:203],a[76:79]
v_cvt_pk_bf16_f32 v237,v250,v251
v_cvt_pk_bf16_f32 v238,v252,v253
v_mfma_f32_16x16x32_bf16 a[80:83],a[224:227],v[200:203],a[80:83]
v_cvt_pk_bf16_f32 v239,v254,v255
v_cvt_pk_bf16_f32 v136,v136,v137
v_mfma_f32_16x16x32_bf16 a[80:83],a[228:231],v[204:207],a[80:83]
buffer_store_dwordx4 v[68:71],v6,s[36:39],s84 offen offset:1024 sc1
v_mfma_f32_16x16x32_bf16 a[84:87],a[236:239],v[204:207],a[84:87]
s_add_u32 m0,s82,81920
v_exp_f32 v138,v138
v_mfma_f32_16x16x32_bf16 a[84:87],a[232:235],v[200:203],a[84:87]
v_exp_f32 v139,v139
v_mfma_f32_16x16x32_bf16 a[88:91],a[240:243],v[200:203],a[88:91]
v_exp_f32 v140,v140
v_mfma_f32_16x16x32_bf16 a[88:91],a[244:247],v[204:207],a[88:91]
buffer_load_dwordx4 v7,s[72:75],s80 offen lds
v_mfma_f32_16x16x32_bf16 a[92:95],a[252:255],v[204:207],a[92:95]
v_exp_f32 v141,v141
v_mfma_f32_16x16x32_bf16 a[92:95],a[248:251],v[200:203],a[92:95]
v_exp_f32 v142,v142
v_smfmac_f32_16x16x64_bf16 v[56:59],v[28:31],v[200:207],v27
v_cvt_pk_bf16_f32 v137,v138,v139
v_mfma_f32_16x16x32_bf16 v[168:171],a[128:131],v[132:135],v[36:39]
v_exp_f32 v143,v143
v_mfma_f32_16x16x32_bf16 v[168:171],a[132:135],v[164:167],v[168:171]
v_cvt_pk_bf16_f32 v138,v140,v141
v_cvt_pk_bf16_f32 v139,v142,v143
v_mfma_f32_16x16x32_bf16 v[168:171],a[136:139],v[196:199],v[168:171]
v_exp_f32 v152,v152
v_mfma_f32_16x16x32_bf16 v[168:171],a[140:143],v[228:231],v[168:171]
v_exp_f32 v153,v153
v_mfma_f32_16x16x32_bf16 v[172:175],a[156:159],v[228:231],v[36:39]
v_exp_f32 v154,v154
v_mfma_f32_16x16x32_bf16 v[172:175],a[152:155],v[196:199],v[172:175]
v_exp_f32 v155,v155
v_mfma_f32_16x16x32_bf16 v[172:175],a[148:151],v[164:167],v[172:175]
v_exp_f32 v156,v156
v_mfma_f32_16x16x32_bf16 v[172:175],a[144:147],v[132:135],v[172:175]
v_exp_f32 v157,v157
v_mfma_f32_16x16x32_bf16 v[184:187],a[160:163],v[132:135],v[36:39]
buffer_store_dwordx4 v[72:75],v6,s[36:39],s84 offen offset:2048 sc1
v_mfma_f32_16x16x32_bf16 v[184:187],a[164:167],v[164:167],v[184:187]
v_exp_f32 v158,v158
v_mfma_f32_16x16x32_bf16 v[184:187],a[168:171],v[196:199],v[184:187]
v_exp_f32 v159,v159
v_mfma_f32_16x16x32_bf16 v[184:187],a[172:175],v[228:231],v[184:187]
v_cvt_pk_bf16_f32 v140,v152,v153
v_cvt_pk_bf16_f32 v141,v154,v155
v_mfma_f32_16x16x32_bf16 v[188:191],a[188:191],v[228:231],v[36:39]
v_cvt_pk_bf16_f32 v142,v156,v157
v_cvt_pk_bf16_f32 v143,v158,v159
v_mfma_f32_16x16x32_bf16 v[188:191],a[184:187],v[196:199],v[188:191]
v_exp_f32 v168,v168
v_mfma_f32_16x16x32_bf16 v[188:191],a[180:183],v[164:167],v[188:191]
v_exp_f32 v169,v169
v_mfma_f32_16x16x32_bf16 v[188:191],a[176:179],v[132:135],v[188:191]
v_exp_f32 v170,v170
v_mfma_f32_16x16x32_bf16 a[96:99],a[192:195],v[232:235],a[96:99]
buffer_load_dwordx4 v8,s[72:75],s80 offen offset:1024 lds
v_mfma_f32_16x16x32_bf16 a[96:99],a[196:199],v[236:239],a[96:99]
ds_read_b64_tr_b16 a[192:193],v2
ds_read_b64_tr_b16 a[194:195],v2 offset:4096
v_mfma_f32_16x16x32_bf16 a[100:103],a[204:207],v[236:239],a[100:103]
ds_read_b64_tr_b16 a[196:197],v2 offset:8192
v_cvt_pk_bf16_f32 v168,v168,v169
v_mfma_f32_16x16x32_bf16 a[100:103],a[200:203],v[232:235],a[100:103]
v_exp_f32 v171,v171
v_mfma_f32_16x16x32_bf16 a[104:107],a[208:211],v[232:235],a[104:107]
ds_read_b64_tr_b16 a[198:199],v2 offset:12288
ds_read_b64_tr_b16 a[204:205],v3 offset:8192
v_mfma_f32_16x16x32_bf16 a[104:107],a[212:215],v[236:239],a[104:107]
ds_read_b64_tr_b16 a[206:207],v3 offset:12288
v_cvt_pk_bf16_f32 v169,v170,v171
v_mfma_f32_16x16x32_bf16 a[108:111],a[220:223],v[236:239],a[108:111]
)A"
R"A(v_exp_f32 v172,v172
v_mfma_f32_16x16x32_bf16 a[108:111],a[216:219],v[232:235],a[108:111]
ds_read_b64_tr_b16 a[200:201],v3
ds_read_b64_tr_b16 a[202:203],v3 offset:4096
v_mfma_f32_16x16x32_bf16 a[112:115],a[224:227],v[232:235],a[112:115]
buffer_store_dwordx4 v[76:79],v6,s[36:39],s84 offen offset:3072 sc1
v_mfma_f32_16x16x32_bf16 a[112:115],a[228:231],v[236:239],a[112:115]
s_add_u32 s84,s55,131072
ds_read_b64_tr_b16 a[208:209],v2 offset:256
ds_read_b64_tr_b16 a[210:211],v2 offset:4352
v_mfma_f32_16x16x32_bf16 a[116:119],a[236:239],v[236:239],a[116:119]
s_max_i32 s84,s84,s18
v_exp_f32 v173,v173
v_mfma_f32_16x16x32_bf16 a[116:119],a[232:235],v[232:235],a[116:119]
ds_read_b64_tr_b16 a[212:213],v2 offset:8448
ds_read_b64_tr_b16 a[214:215],v2 offset:12544
v_mfma_f32_16x16x32_bf16 a[120:123],a[240:243],v[232:235],a[120:123]
ds_read_b64_tr_b16 a[220:221],v3 offset:8448
v_cvt_pk_bf16_f32 v170,v172,v173
v_mfma_f32_16x16x32_bf16 a[120:123],a[244:247],v[236:239],a[120:123]
ds_read_b64_tr_b16 a[222:223],v3 offset:12544
v_mfma_f32_16x16x32_bf16 a[124:127],a[252:255],v[236:239],a[124:127]
ds_read_b64_tr_b16 a[216:217],v3 offset:256
v_mfma_f32_16x16x32_bf16 a[124:127],a[248:251],v[232:235],a[124:127]
ds_read_b64_tr_b16 a[218:219],v3 offset:4352
ds_read_b64_tr_b16 a[224:225],v2 offset:512
v_smfmac_f32_16x16x64_bf16 v[60:63],v[28:31],v[232:239],v27
ds_read_b64_tr_b16 a[226:227],v2 offset:4608
v_mfma_f32_16x16x32_bf16 v[200:203],a[128:131],v[144:147],v[40:43]
ds_read_b64_tr_b16 a[228:229],v2 offset:8704
v_mfma_f32_16x16x32_bf16 v[200:203],a[132:135],v[176:179],v[200:203]
ds_read_b64_tr_b16 a[230:231],v2 offset:12800
v_mfma_f32_16x16x32_bf16 v[200:203],a[136:139],v[208:211],v[200:203]
ds_read_b64_tr_b16 a[236:237],v3 offset:8704
ds_read_b64_tr_b16 a[238:239],v3 offset:12800
v_mfma_f32_16x16x32_bf16 v[200:203],a[140:143],v[240:243],v[200:203]
buffer_load_dwordx4 v9,s[72:75],s80 offen offset:2048 lds
v_mfma_f32_16x16x32_bf16 v[204:207],a[156:159],v[240:243],v[40:43]
ds_read_b64_tr_b16 a[232:233],v3 offset:512
ds_read_b64_tr_b16 a[234:235],v3 offset:4608
v_mfma_f32_16x16x32_bf16 v[204:207],a[152:155],v[208:211],v[204:207]
ds_read_b64_tr_b16 a[240:241],v2 offset:768
v_mfma_f32_16x16x32_bf16 v[204:207],a[148:151],v[176:179],v[204:207]
ds_read_b64_tr_b16 a[242:243],v2 offset:4864
v_mfma_f32_16x16x32_bf16 v[204:207],a[144:147],v[144:147],v[204:207]
buffer_store_dwordx4 v[80:83],v6,s[36:39],s85 offen sc1
v_mfma_f32_16x16x32_bf16 v[216:219],a[160:163],v[144:147],v[40:43]
ds_read_b64_tr_b16 a[244:245],v2 offset:8960
ds_read_b64_tr_b16 a[246:247],v2 offset:13056
v_mfma_f32_16x16x32_bf16 v[216:219],a[164:167],v[176:179],v[216:219]
ds_read_b64_tr_b16 a[252:253],v3 offset:8960
v_add_u32 v2,s48,v2
v_mfma_f32_16x16x32_bf16 v[216:219],a[168:171],v[208:211],v[216:219]
ds_read_b64_tr_b16 a[254:255],v3 offset:13056
v_mfma_f32_16x16x32_bf16 v[216:219],a[172:175],v[240:243],v[216:219]
ds_read_b64_tr_b16 a[248:249],v3 offset:768
v_mfma_f32_16x16x32_bf16 v[220:223],a[188:191],v[240:243],v[40:43]
ds_read_b64_tr_b16 a[250:251],v3 offset:4864
v_add_u32 v3,s48,v3
v_mfma_f32_16x16x32_bf16 v[220:223],a[184:187],v[208:211],v[220:223]
s_mov_b32 s48,98304
v_exp_f32 v174,v174
v_mfma_f32_16x16x32_bf16 v[220:223],a[180:183],v[176:179],v[220:223]
v_exp_f32 v175,v175
v_mfma_f32_16x16x32_bf16 v[220:223],a[176:179],v[144:147],v[220:223]
s_waitcnt lgkmcnt(6)
v_exp_f32 v184,v184
v_mfma_f32_16x16x32_bf16 a[0:3],a[192:195],v[136:139],a[0:3]
v_exp_f32 v185,v185
v_mfma_f32_16x16x32_bf16 a[0:3],a[196:199],v[140:143],a[0:3]
v_cvt_pk_bf16_f32 v171,v174,v175
v_cvt_pk_bf16_f32 v172,v184,v185
v_mfma_f32_16x16x32_bf16 a[4:7],a[204:207],v[140:143],a[4:7]
v_exp_f32 v186,v186
v_mfma_f32_16x16x32_bf16 a[4:7],a[200:203],v[136:139],a[4:7]
v_exp_f32 v187,v187
v_mfma_f32_16x16x32_bf16 a[8:11],a[208:211],v[136:139],a[8:11]
v_exp_f32 v188,v188
v_mfma_f32_16x16x32_bf16 a[8:11],a[212:215],v[140:143],a[8:11]
v_exp_f32 v189,v189
v_mfma_f32_16x16x32_bf16 a[12:15],a[220:223],v[140:143],a[12:15]
v_exp_f32 v190,v190
v_mfma_f32_16x16x32_bf16 a[12:15],a[216:219],v[136:139],a[12:15]
buffer_store_dwordx4 v[84:87],v6,s[36:39],s85 offen offset:1024 sc1
v_mfma_f32_16x16x32_bf16 a[16:19],a[224:227],v[136:139],a[16:19]
v_exp_f32 v191,v191
v_mfma_f32_16x16x32_bf16 a[16:19],a[228:231],v[140:143],a[16:19]
v_cvt_pk_bf16_f32 v173,v186,v187
v_cvt_pk_bf16_f32 v174,v188,v189
v_mfma_f32_16x16x32_bf16 a[20:23],a[236:239],v[140:143],a[20:23]
v_exp_f32 v200,v200
v_mfma_f32_16x16x32_bf16 a[20:23],a[232:235],v[136:139],a[20:23]
buffer_load_dwordx4 v10,s[72:75],s80 offen offset:3072 lds
v_mfma_f32_16x16x32_bf16 a[24:27],a[240:243],v[136:139],a[24:27]
s_waitcnt lgkmcnt(0)
v_exp_f32 v201,v201
v_mfma_f32_16x16x32_bf16 a[24:27],a[244:247],v[140:143],a[24:27]
v_cvt_pk_bf16_f32 v175,v190,v191
v_cvt_pk_bf16_f32 v200,v200,v201
v_mfma_f32_16x16x32_bf16 a[28:31],a[252:255],v[140:143],a[28:31]
v_exp_f32 v202,v202
v_mfma_f32_16x16x32_bf16 a[28:31],a[248:251],v[136:139],a[28:31]
v_exp_f32 v203,v203
v_smfmac_f32_16x16x64_bf16 v[48:51],v[28:31],v[136:143],v27
v_cvt_pk_bf16_f32 v201,v202,v203
v_mfma_f32_16x16x32_bf16 v[232:235],a[128:131],v[148:151],v[44:47]
s_waitcnt vmcnt(11)
v_exp_f32 v204,v204
v_mfma_f32_16x16x32_bf16 v[232:235],a[132:135],v[180:183],v[232:235]
s_barrier
v_exp_f32 v205,v205
v_mfma_f32_16x16x32_bf16 v[232:235],a[136:139],v[212:215],v[232:235]
ds_read_b128 a[128:131],v1
v_cvt_pk_bf16_f32 v202,v204,v205
v_mfma_f32_16x16x32_bf16 v[232:235],a[140:143],v[244:247],v[232:235]
v_exp_f32 v206,v206
v_mfma_f32_16x16x32_bf16 v[236:239],a[156:159],v[244:247],v[44:47]
v_exp_f32 v207,v207
v_mfma_f32_16x16x32_bf16 v[236:239],a[152:155],v[212:215],v[236:239]
buffer_store_dwordx4 v[88:91],v6,s[36:39],s85 offen offset:2048 sc1
v_mfma_f32_16x16x32_bf16 v[236:239],a[148:151],v[180:183],v[236:239]
s_addk_i32 m0,4096
ds_read_b128 a[132:135],v1 offset:256
v_cvt_pk_bf16_f32 v203,v206,v207
v_mfma_f32_16x16x32_bf16 v[236:239],a[144:147],v[148:151],v[236:239]
v_exp_f32 v216,v216
v_mfma_f32_16x16x32_bf16 v[248:251],a[160:163],v[148:151],v[44:47]
v_exp_f32 v217,v217
v_mfma_f32_16x16x32_bf16 v[248:251],a[164:167],v[180:183],v[248:251]
buffer_load_dwordx4 v11,s[72:75],s80 offen lds
v_mfma_f32_16x16x32_bf16 v[248:251],a[168:171],v[212:215],v[248:251]
ds_read_b128 a[136:139],v1 offset:512
v_cvt_pk_bf16_f32 v204,v216,v217
v_mfma_f32_16x16x32_bf16 v[248:251],a[172:175],v[244:247],v[248:251]
v_exp_f32 v218,v218
v_mfma_f32_16x16x32_bf16 v[252:255],a[188:191],v[244:247],v[44:47]
ds_read_b128 a[140:143],v1 offset:768
v_mfma_f32_16x16x32_bf16 v[252:255],a[184:187],v[212:215],v[252:255]
v_exp_f32 v219,v219
v_mfma_f32_16x16x32_bf16 v[252:255],a[180:183],v[180:183],v[252:255]
ds_read_b128 a[156:159],v1 offset:4864
v_cvt_pk_bf16_f32 v205,v218,v219
v_mfma_f32_16x16x32_bf16 v[252:255],a[176:179],v[148:151],v[252:255]
v_exp_f32 v220,v220
v_mfma_f32_16x16x32_bf16 a[32:35],a[192:195],v[168:171],a[32:35]
ds_read_b128 a[152:155],v1 offset:4608
v_mfma_f32_16x16x32_bf16 a[32:35],a[196:199],v[172:175],a[32:35]
v_exp_f32 v221,v221
v_mfma_f32_16x16x32_bf16 a[36:39],a[204:207],v[172:175],a[36:39]
ds_read_b128 a[148:151],v1 offset:4352
v_cvt_pk_bf16_f32 v206,v220,v221
v_mfma_f32_16x16x32_bf16 a[36:39],a[200:203],v[168:171],a[36:39]
v_exp_f32 v222,v222
v_mfma_f32_16x16x32_bf16 a[40:43],a[208:211],v[168:171],a[40:43]
ds_read_b128 a[144:147],v1 offset:4096
v_mfma_f32_16x16x32_bf16 a[40:43],a[212:215],v[172:175],a[40:43]
buffer_store_dwordx4 v[92:95],v6,s[36:39],s85 offen offset:3072 sc1
v_mfma_f32_16x16x32_bf16 a[44:47],a[220:223],v[172:175],a[44:47]
s_add_u32 s85,s55,135168
s_max_i32 s85,s85,s19
ds_read_b128 a[160:163],v1 offset:8192
v_mfma_f32_16x16x32_bf16 a[44:47],a[216:219],v[168:171],a[44:47]
v_exp_f32 v223,v223
v_mfma_f32_16x16x32_bf16 a[48:51],a[224:227],v[168:171],a[48:51]
ds_read_b128 a[164:167],v1 offset:8448
v_cvt_pk_bf16_f32 v207,v222,v223
v_mfma_f32_16x16x32_bf16 a[48:51],a[228:231],v[172:175],a[48:51]
v_exp_f32 v232,v232
v_mfma_f32_16x16x32_bf16 a[52:55],a[236:239],v[172:175],a[52:55]
s_addk_i32 m0,1024
ds_read_b128 a[168:171],v1 offset:8704
v_mfma_f32_16x16x32_bf16 a[52:55],a[232:235],v[168:171],a[52:55]
v_exp_f32 v233,v233
v_mfma_f32_16x16x32_bf16 a[56:59],a[240:243],v[168:171],a[56:59]
ds_read_b128 a[172:175],v1 offset:8960
v_cvt_pk_bf16_f32 v232,v232,v233
v_mfma_f32_16x16x32_bf16 a[56:59],a[244:247],v[172:175],a[56:59]
buffer_load_dwordx4 v7,s[72:75],s83 offen lds
v_mfma_f32_16x16x32_bf16 a[60:63],a[252:255],v[172:175],a[60:63]
ds_read_b128 a[188:191],v1 offset:13056
v_mfma_f32_16x16x32_bf16 a[60:63],a[248:251],v[168:171],a[60:63]
v_exp_f32 v234,v234
v_smfmac_f32_16x16x64_bf16 v[52:55],v[28:31],v[168:175],v27
s_waitcnt lgkmcnt(3)
ds_read_b128 a[184:187],v1 offset:12800
v_mfma_f32_16x16x32_bf16 v[136:139],a[128:131],v[128:131],v[32:35]
v_exp_f32 v235,v235
v_mfma_f32_16x16x32_bf16 v[136:139],a[132:135],v[160:163],v[136:139]
ds_read_b128 a[180:183],v1 offset:12544
v_cvt_pk_bf16_f32 v233,v234,v235
v_mfma_f32_16x16x32_bf16 v[136:139],a[136:139],v[192:195],v[136:139]
v_exp_f32 v236,v236
v_mfma_f32_16x16x32_bf16 v[136:139],a[140:143],v[224:227],v[136:139]
ds_read_b128 a[176:179],v1 offset:12288
v_add_u32 v1,s48,v1
v_mfma_f32_16x16x32_bf16 v[140:143],a[156:159],v[224:227],v[32:35]
buffer_store_dwordx4 v[96:99],v6,s[36:39],s86 offen sc1
v_mfma_f32_16x16x32_bf16 v[140:143],a[152:155],v[192:195],v[140:143]
v_exp_f32 v237,v237
v_mfma_f32_16x16x32_bf16 v[140:143],a[148:151],v[160:163],v[140:143]
v_exp_f32 v238,v238
v_mfma_f32_16x16x32_bf16 v[140:143],a[144:147],v[128:131],v[140:143]
v_exp_f32 v239,v239
v_mfma_f32_16x16x32_bf16 v[152:155],a[160:163],v[128:131],v[32:35]
v_cvt_pk_bf16_f32 v234,v236,v237
v_cvt_pk_bf16_f32 v235,v238,v239
v_mfma_f32_16x16x32_bf16 v[152:155],a[164:167],v[160:163],v[152:155]
s_waitcnt lgkmcnt(1)
v_exp_f32 v248,v248
v_mfma_f32_16x16x32_bf16 v[152:155],a[168:171],v[192:195],v[152:155]
v_exp_f32 v249,v249
v_mfma_f32_16x16x32_bf16 v[152:155],a[172:175],v[224:227],v[152:155]
v_exp_f32 v250,v250
v_mfma_f32_16x16x32_bf16 v[156:159],a[188:191],v[224:227],v[32:35]
v_exp_f32 v251,v251
v_mfma_f32_16x16x32_bf16 v[156:159],a[184:187],v[192:195],v[156:159]
v_exp_f32 v252,v252
v_mfma_f32_16x16x32_bf16 v[156:159],a[180:183],v[160:163],v[156:159]
s_waitcnt lgkmcnt(0)
v_exp_f32 v253,v253
v_mfma_f32_16x16x32_bf16 v[156:159],a[176:179],v[128:131],v[156:159]
v_exp_f32 v254,v254
v_mfma_f32_16x16x32_bf16 a[64:67],a[192:195],v[200:203],a[64:67]
buffer_load_dwordx4 v8,s[72:75],s83 offen offset:1024 lds
v_mfma_f32_16x16x32_bf16 a[64:67],a[196:199],v[204:207],a[64:67]
v_exp_f32 v255,v255
v_mfma_f32_16x16x32_bf16 a[68:71],a[204:207],v[204:207],a[68:71]
v_cvt_pk_bf16_f32 v236,v248,v249
v_cvt_pk_bf16_f32 v237,v250,v251
v_mfma_f32_16x16x32_bf16 a[68:71],a[200:203],v[200:203],a[68:71]
v_cvt_pk_bf16_f32 v238,v252,v253
v_cvt_pk_bf16_f32 v239,v254,v255
v_mfma_f32_16x16x32_bf16 a[72:75],a[208:211],v[200:203],a[72:75]
buffer_store_dwordx4 v[100:103],v6,s[36:39],s86 offen offset:1024 sc1
v_mfma_f32_16x16x32_bf16 a[72:75],a[212:215],v[204:207],a[72:75]
v_exp_f32 v136,v136
v_mfma_f32_16x16x32_bf16 a[76:79],a[220:223],v[204:207],a[76:79]
v_exp_f32 v137,v137
v_mfma_f32_16x16x32_bf16 a[76:79],a[216:219],v[200:203],a[76:79]
v_exp_f32 v138,v138
v_mfma_f32_16x16x32_bf16 a[80:83],a[224:227],v[200:203],a[80:83]
v_exp_f32 v139,v139
v_mfma_f32_16x16x32_bf16 a[80:83],a[228:231],v[204:207],a[80:83]
v_exp_f32 v140,v140
v_mfma_f32_16x16x32_bf16 a[84:87],a[236:239],v[204:207],a[84:87]
v_exp_f32 v141,v141
v_mfma_f32_16x16x32_bf16 a[84:87],a[232:235],v[200:203],a[84:87]
v_exp_f32 v142,v142
v_mfma_f32_16x16x32_bf16 a[88:91],a[240:243],v[200:203],a[88:91]
v_exp_f32 v143,v143
v_mfma_f32_16x16x32_bf16 a[88:91],a[244:247],v[204:207],a[88:91]
v_exp_f32 v152,v152
v_mfma_f32_16x16x32_bf16 a[92:95],a[252:255],v[204:207],a[92:95]
v_exp_f32 v153,v153
v_mfma_f32_16x16x32_bf16 a[92:95],a[248:251],v[200:203],a[92:95]
v_exp_f32 v154,v154
v_smfmac_f32_16x16x64_bf16 v[56:59],v[28:31],v[200:207],v27
v_cvt_pk_bf16_f32 v136,v136,v137
v_mfma_f32_16x16x32_bf16 v[168:171],a[128:131],v[132:135],v[36:39]
)A"
R"A(v_exp_f32 v155,v155
v_mfma_f32_16x16x32_bf16 v[168:171],a[132:135],v[164:167],v[168:171]
v_exp_f32 v156,v156
v_mfma_f32_16x16x32_bf16 v[168:171],a[136:139],v[196:199],v[168:171]
v_exp_f32 v157,v157
v_mfma_f32_16x16x32_bf16 v[168:171],a[140:143],v[228:231],v[168:171]
buffer_store_dwordx4 v[104:107],v6,s[36:39],s86 offen offset:2048 sc1
v_mfma_f32_16x16x32_bf16 v[172:175],a[156:159],v[228:231],v[36:39]
v_exp_f32 v158,v158
v_mfma_f32_16x16x32_bf16 v[172:175],a[152:155],v[196:199],v[172:175]
v_exp_f32 v159,v159
v_mfma_f32_16x16x32_bf16 v[172:175],a[148:151],v[164:167],v[172:175]
v_cvt_pk_bf16_f32 v137,v138,v139
v_cvt_pk_bf16_f32 v138,v140,v141
v_mfma_f32_16x16x32_bf16 v[172:175],a[144:147],v[132:135],v[172:175]
buffer_load_dwordx4 v9,s[72:75],s83 offen offset:2048 lds
v_mfma_f32_16x16x32_bf16 v[184:187],a[160:163],v[132:135],v[36:39]
v_cvt_pk_bf16_f32 v139,v142,v143
v_cvt_pk_bf16_f32 v140,v152,v153
v_mfma_f32_16x16x32_bf16 v[184:187],a[164:167],v[164:167],v[184:187]
v_cvt_pk_bf16_f32 v141,v154,v155
v_cvt_pk_bf16_f32 v142,v156,v157
v_mfma_f32_16x16x32_bf16 v[184:187],a[168:171],v[196:199],v[184:187]
v_exp_f32 v168,v168
v_mfma_f32_16x16x32_bf16 v[184:187],a[172:175],v[228:231],v[184:187]
v_exp_f32 v169,v169
v_mfma_f32_16x16x32_bf16 v[188:191],a[188:191],v[228:231],v[36:39]
v_cvt_pk_bf16_f32 v143,v158,v159
v_cvt_pk_bf16_f32 v168,v168,v169
v_mfma_f32_16x16x32_bf16 v[188:191],a[184:187],v[196:199],v[188:191]
v_exp_f32 v170,v170
v_mfma_f32_16x16x32_bf16 v[188:191],a[180:183],v[164:167],v[188:191]
v_exp_f32 v171,v171
v_mfma_f32_16x16x32_bf16 v[188:191],a[176:179],v[132:135],v[188:191]
v_exp_f32 v172,v172
v_mfma_f32_16x16x32_bf16 a[96:99],a[192:195],v[232:235],a[96:99]
ds_read_b64_tr_b16 a[192:193],v2
ds_read_b64_tr_b16 a[194:195],v2 offset:4096
v_mfma_f32_16x16x32_bf16 a[96:99],a[196:199],v[236:239],a[96:99]
ds_read_b64_tr_b16 a[196:197],v2 offset:8192
v_cvt_pk_bf16_f32 v169,v170,v171
v_mfma_f32_16x16x32_bf16 a[100:103],a[204:207],v[236:239],a[100:103]
v_exp_f32 v173,v173
v_mfma_f32_16x16x32_bf16 a[100:103],a[200:203],v[232:235],a[100:103]
buffer_store_dwordx4 v[108:111],v6,s[36:39],s86 offen offset:3072 sc1
v_mfma_f32_16x16x32_bf16 a[104:107],a[208:211],v[232:235],a[104:107]
s_add_u32 s86,s55,147456
ds_read_b64_tr_b16 a[198:199],v2 offset:12288
ds_read_b64_tr_b16 a[204:205],v3 offset:8192
v_mfma_f32_16x16x32_bf16 a[104:107],a[212:215],v[236:239],a[104:107]
s_max_i32 s86,s86,s100
ds_read_b64_tr_b16 a[206:207],v3 offset:12288
v_cvt_pk_bf16_f32 v170,v172,v173
v_mfma_f32_16x16x32_bf16 a[108:111],a[220:223],v[236:239],a[108:111]
v_exp_f32 v174,v174
v_mfma_f32_16x16x32_bf16 a[108:111],a[216:219],v[232:235],a[108:111]
buffer_load_dwordx4 v10,s[72:75],s83 offen offset:3072 lds
v_mfma_f32_16x16x32_bf16 a[112:115],a[224:227],v[232:235],a[112:115]
ds_read_b64_tr_b16 a[200:201],v3
ds_read_b64_tr_b16 a[202:203],v3 offset:4096
v_mfma_f32_16x16x32_bf16 a[112:115],a[228:231],v[236:239],a[112:115]
v_exp_f32 v175,v175
v_mfma_f32_16x16x32_bf16 a[116:119],a[236:239],v[236:239],a[116:119]
ds_read_b64_tr_b16 a[208:209],v2 offset:256
ds_read_b64_tr_b16 a[210:211],v2 offset:4352
v_mfma_f32_16x16x32_bf16 a[116:119],a[232:235],v[232:235],a[116:119]
ds_read_b64_tr_b16 a[212:213],v2 offset:8448
v_cvt_pk_bf16_f32 v171,v174,v175
v_mfma_f32_16x16x32_bf16 a[120:123],a[240:243],v[232:235],a[120:123]
ds_read_b64_tr_b16 a[214:215],v2 offset:12544
v_mfma_f32_16x16x32_bf16 a[120:123],a[244:247],v[236:239],a[120:123]
ds_read_b64_tr_b16 a[220:221],v3 offset:8448
v_mfma_f32_16x16x32_bf16 a[124:127],a[252:255],v[236:239],a[124:127]
ds_read_b64_tr_b16 a[222:223],v3 offset:12544
ds_read_b64_tr_b16 a[216:217],v3 offset:256
v_mfma_f32_16x16x32_bf16 a[124:127],a[248:251],v[232:235],a[124:127]
ds_read_b64_tr_b16 a[218:219],v3 offset:4352
v_smfmac_f32_16x16x64_bf16 v[60:63],v[28:31],v[232:239],v27
ds_read_b64_tr_b16 a[224:225],v2 offset:512
v_mfma_f32_16x16x32_bf16 v[200:203],a[128:131],v[144:147],v[40:43]
ds_read_b64_tr_b16 a[226:227],v2 offset:4608
v_mfma_f32_16x16x32_bf16 v[200:203],a[132:135],v[176:179],v[200:203]
ds_read_b64_tr_b16 a[228:229],v2 offset:8704
ds_read_b64_tr_b16 a[230:231],v2 offset:12800
v_mfma_f32_16x16x32_bf16 v[200:203],a[136:139],v[208:211],v[200:203]
buffer_store_dwordx4 v[112:115],v6,s[36:39],s87 offen sc1
v_mfma_f32_16x16x32_bf16 v[200:203],a[140:143],v[240:243],v[200:203]
ds_read_b64_tr_b16 a[236:237],v3 offset:8704
ds_read_b64_tr_b16 a[238:239],v3 offset:12800
v_mfma_f32_16x16x32_bf16 v[204:207],a[156:159],v[240:243],v[40:43]
ds_read_b64_tr_b16 a[232:233],v3 offset:512
v_mfma_f32_16x16x32_bf16 v[204:207],a[152:155],v[208:211],v[204:207]
ds_read_b64_tr_b16 a[234:235],v3 offset:4608
v_mfma_f32_16x16x32_bf16 v[204:207],a[148:151],v[176:179],v[204:207]
ds_read_b64_tr_b16 a[240:241],v2 offset:768
v_mfma_f32_16x16x32_bf16 v[204:207],a[144:147],v[144:147],v[204:207]
s_addk_i32 m0,4096
ds_read_b64_tr_b16 a[242:243],v2 offset:4864
ds_read_b64_tr_b16 a[244:245],v2 offset:8960
v_mfma_f32_16x16x32_bf16 v[216:219],a[160:163],v[144:147],v[40:43]
ds_read_b64_tr_b16 a[246:247],v2 offset:13056
v_add_u32 v2,s48,v2
v_mfma_f32_16x16x32_bf16 v[216:219],a[164:167],v[176:179],v[216:219]
ds_read_b64_tr_b16 a[252:253],v3 offset:8960
v_mfma_f32_16x16x32_bf16 v[216:219],a[168:171],v[208:211],v[216:219]
buffer_load_dwordx4 v11,s[72:75],s83 offen lds
v_mfma_f32_16x16x32_bf16 v[216:219],a[172:175],v[240:243],v[216:219]
ds_read_b64_tr_b16 a[254:255],v3 offset:13056
ds_read_b64_tr_b16 a[248:249],v3 offset:768
v_mfma_f32_16x16x32_bf16 v[220:223],a[188:191],v[240:243],v[40:43]
ds_read_b64_tr_b16 a[250:251],v3 offset:4864
v_add_u32 v3,s48,v3
v_mfma_f32_16x16x32_bf16 v[220:223],a[184:187],v[208:211],v[220:223]
v_exp_f32 v184,v184
v_mfma_f32_16x16x32_bf16 v[220:223],a[180:183],v[176:179],v[220:223]
v_exp_f32 v185,v185
v_mfma_f32_16x16x32_bf16 v[220:223],a[176:179],v[144:147],v[220:223]
s_waitcnt lgkmcnt(5)
v_exp_f32 v186,v186
v_mfma_f32_16x16x32_bf16 a[0:3],a[192:195],v[136:139],a[0:3]
v_exp_f32 v187,v187
v_mfma_f32_16x16x32_bf16 a[0:3],a[196:199],v[140:143],a[0:3]
s_add_u32 s80,s71,0
s_and_b32 s80,s80,3
s_lshl_b32 s80,s80,1
v_mfma_f32_16x16x32_bf16 a[4:7],a[204:207],v[140:143],a[4:7]
s_or_b32 s80,s80,1
buffer_store_dwordx4 v[116:119],v6,s[36:39],s87 offen offset:1024 sc1
v_mfma_f32_16x16x32_bf16 a[4:7],a[200:203],v[136:139],a[4:7]
s_mul_i32 s80,s80,163840
s_add_u32 s80,s80,s81
v_cvt_pk_bf16_f32 v172,v184,v185
v_mfma_f32_16x16x32_bf16 a[8:11],a[208:211],v[136:139],a[8:11]
s_add_u32 s83,s80,20480
v_exp_f32 v188,v188
v_mfma_f32_16x16x32_bf16 a[8:11],a[212:215],v[140:143],a[8:11]
v_exp_f32 v189,v189
v_mfma_f32_16x16x32_bf16 a[12:15],a[220:223],v[140:143],a[12:15]
v_exp_f32 v190,v190
v_mfma_f32_16x16x32_bf16 a[12:15],a[216:219],v[136:139],a[12:15]
v_exp_f32 v191,v191
v_mfma_f32_16x16x32_bf16 a[16:19],a[224:227],v[136:139],a[16:19]
v_cvt_pk_bf16_f32 v173,v186,v187
v_cvt_pk_bf16_f32 v174,v188,v189
v_mfma_f32_16x16x32_bf16 a[16:19],a[228:231],v[140:143],a[16:19]
v_exp_f32 v200,v200
v_mfma_f32_16x16x32_bf16 a[20:23],a[236:239],v[140:143],a[20:23]
v_exp_f32 v201,v201
v_mfma_f32_16x16x32_bf16 a[20:23],a[232:235],v[136:139],a[20:23]
v_cvt_pk_bf16_f32 v175,v190,v191
v_cvt_pk_bf16_f32 v200,v200,v201
v_mfma_f32_16x16x32_bf16 a[24:27],a[240:243],v[136:139],a[24:27]
s_waitcnt lgkmcnt(0)
v_exp_f32 v202,v202
v_mfma_f32_16x16x32_bf16 a[24:27],a[244:247],v[140:143],a[24:27]
v_exp_f32 v203,v203
v_mfma_f32_16x16x32_bf16 a[28:31],a[252:255],v[140:143],a[28:31]
v_exp_f32 v204,v204
v_mfma_f32_16x16x32_bf16 a[28:31],a[248:251],v[136:139],a[28:31]
v_exp_f32 v205,v205
v_smfmac_f32_16x16x64_bf16 v[48:51],v[28:31],v[136:143],v27
v_cvt_pk_bf16_f32 v201,v202,v203
v_mfma_f32_16x16x32_bf16 v[232:235],a[128:131],v[148:151],v[44:47]
v_exp_f32 v206,v206
v_mfma_f32_16x16x32_bf16 v[232:235],a[132:135],v[180:183],v[232:235]
s_barrier
buffer_store_dwordx4 v[120:123],v6,s[36:39],s87 offen offset:2048 sc1
v_mfma_f32_16x16x32_bf16 v[232:235],a[136:139],v[212:215],v[232:235]
ds_read_b128 a[128:131],v1
v_cvt_pk_bf16_f32 v202,v204,v205
v_mfma_f32_16x16x32_bf16 v[232:235],a[140:143],v[244:247],v[232:235]
v_exp_f32 v207,v207
v_mfma_f32_16x16x32_bf16 v[236:239],a[156:159],v[244:247],v[44:47]
s_add_u32 m0,s82,0
ds_read_b128 a[132:135],v1 offset:256
v_cvt_pk_bf16_f32 v203,v206,v207
v_mfma_f32_16x16x32_bf16 v[236:239],a[152:155],v[212:215],v[236:239]
v_exp_f32 v216,v216
v_mfma_f32_16x16x32_bf16 v[236:239],a[148:151],v[180:183],v[236:239]
v_exp_f32 v217,v217
v_mfma_f32_16x16x32_bf16 v[236:239],a[144:147],v[148:151],v[236:239]
buffer_load_dwordx4 v7,s[72:75],s80 offen lds
v_mfma_f32_16x16x32_bf16 v[248:251],a[160:163],v[148:151],v[44:47]
ds_read_b128 a[136:139],v1 offset:512
v_cvt_pk_bf16_f32 v204,v216,v217
v_mfma_f32_16x16x32_bf16 v[248:251],a[164:167],v[180:183],v[248:251]
v_exp_f32 v218,v218
v_mfma_f32_16x16x32_bf16 v[248:251],a[168:171],v[212:215],v[248:251]
ds_read_b128 a[140:143],v1 offset:768
v_mfma_f32_16x16x32_bf16 v[248:251],a[172:175],v[244:247],v[248:251]
v_exp_f32 v219,v219
v_mfma_f32_16x16x32_bf16 v[252:255],a[188:191],v[244:247],v[44:47]
ds_read_b128 a[156:159],v1 offset:4864
v_cvt_pk_bf16_f32 v205,v218,v219
v_mfma_f32_16x16x32_bf16 v[252:255],a[184:187],v[212:215],v[252:255]
v_exp_f32 v220,v220
v_mfma_f32_16x16x32_bf16 v[252:255],a[180:183],v[180:183],v[252:255]
ds_read_b128 a[152:155],v1 offset:4608
v_mfma_f32_16x16x32_bf16 v[252:255],a[176:179],v[148:151],v[252:255]
v_exp_f32 v221,v221
v_mfma_f32_16x16x32_bf16 a[32:35],a[192:195],v[168:171],a[32:35]
buffer_store_dwordx4 v[124:127],v6,s[36:39],s87 offen offset:3072 sc1
v_mfma_f32_16x16x32_bf16 a[32:35],a[196:199],v[172:175],a[32:35]
s_add_u32 s87,s55,151552
ds_read_b128 a[148:151],v1 offset:4352
v_cvt_pk_bf16_f32 v206,v220,v221
v_mfma_f32_16x16x32_bf16 a[36:39],a[204:207],v[172:175],a[36:39]
s_max_i32 s87,s87,s101
v_exp_f32 v222,v222
v_mfma_f32_16x16x32_bf16 a[36:39],a[200:203],v[168:171],a[36:39]
ds_read_b128 a[144:147],v1 offset:4096
v_mfma_f32_16x16x32_bf16 a[40:43],a[208:211],v[168:171],a[40:43]
buffer_load_dwordx4 v8,s[72:75],s80 offen offset:1024 lds
v_mfma_f32_16x16x32_bf16 a[40:43],a[212:215],v[172:175],a[40:43]
ds_read_b128 a[160:163],v1 offset:8192
v_mfma_f32_16x16x32_bf16 a[44:47],a[220:223],v[172:175],a[44:47]
v_exp_f32 v223,v223
v_mfma_f32_16x16x32_bf16 a[44:47],a[216:219],v[168:171],a[44:47]
ds_read_b128 a[164:167],v1 offset:8448
v_cvt_pk_bf16_f32 v207,v222,v223
v_mfma_f32_16x16x32_bf16 a[48:51],a[224:227],v[168:171],a[48:51]
v_exp_f32 v232,v232
v_mfma_f32_16x16x32_bf16 a[48:51],a[228:231],v[172:175],a[48:51]
ds_read_b128 a[168:171],v1 offset:8704
v_mfma_f32_16x16x32_bf16 a[52:55],a[236:239],v[172:175],a[52:55]
v_exp_f32 v233,v233
v_mfma_f32_16x16x32_bf16 a[52:55],a[232:235],v[168:171],a[52:55]
ds_read_b128 a[172:175],v1 offset:8960
v_cvt_pk_bf16_f32 v232,v232,v233
v_mfma_f32_16x16x32_bf16 a[56:59],a[240:243],v[168:171],a[56:59]
v_exp_f32 v234,v234
v_mfma_f32_16x16x32_bf16 a[56:59],a[244:247],v[172:175],a[56:59]
buffer_load_dwordx4 v9,s[72:75],s80 offen offset:2048 lds
v_mfma_f32_16x16x32_bf16 a[60:63],a[252:255],v[172:175],a[60:63]
ds_read_b128 a[188:191],v1 offset:13056
v_mfma_f32_16x16x32_bf16 a[60:63],a[248:251],v[168:171],a[60:63]
v_exp_f32 v235,v235
v_smfmac_f32_16x16x64_bf16 v[52:55],v[28:31],v[168:175],v27
s_waitcnt lgkmcnt(3)
ds_read_b128 a[184:187],v1 offset:12800
v_mfma_f32_16x16x32_bf16 v[136:139],a[128:131],v[128:131],v[32:35]
v_exp_f32 v236,v236
v_mfma_f32_16x16x32_bf16 v[136:139],a[132:135],v[160:163],v[136:139]
ds_read_b128 a[180:183],v1 offset:12544
v_cvt_pk_bf16_f32 v233,v234,v235
v_mfma_f32_16x16x32_bf16 v[136:139],a[136:139],v[192:195],v[136:139]
v_exp_f32 v237,v237
v_mfma_f32_16x16x32_bf16 v[136:139],a[140:143],v[224:227],v[136:139]
ds_read_b128 a[176:179],v1 offset:12288
v_cvt_pk_bf16_f32 v234,v236,v237
v_mfma_f32_16x16x32_bf16 v[140:143],a[156:159],v[224:227],v[32:35]
v_exp_f32 v238,v238
v_mfma_f32_16x16x32_bf16 v[140:143],a[152:155],v[192:195],v[140:143]
v_exp_f32 v239,v239
v_mfma_f32_16x16x32_bf16 v[140:143],a[148:151],v[160:163],v[140:143]
v_exp_f32 v248,v248
v_mfma_f32_16x16x32_bf16 v[140:143],a[144:147],v[128:131],v[140:143]
)A"
R"A(buffer_load_dwordx4 v10,s[72:75],s80 offen offset:3072 lds
v_mfma_f32_16x16x32_bf16 v[152:155],a[160:163],v[128:131],v[32:35]
v_exp_f32 v249,v249
v_mfma_f32_16x16x32_bf16 v[152:155],a[164:167],v[160:163],v[152:155]
s_waitcnt lgkmcnt(1)
v_cvt_pk_bf16_f32 v235,v238,v239
v_cvt_pk_bf16_f32 v236,v248,v249
v_mfma_f32_16x16x32_bf16 v[152:155],a[168:171],v[192:195],v[152:155]
v_exp_f32 v250,v250
v_mfma_f32_16x16x32_bf16 v[152:155],a[172:175],v[224:227],v[152:155]
v_exp_f32 v251,v251
v_mfma_f32_16x16x32_bf16 v[156:159],a[188:191],v[224:227],v[32:35]
v_exp_f32 v252,v252
v_mfma_f32_16x16x32_bf16 v[156:159],a[184:187],v[192:195],v[156:159]
v_exp_f32 v253,v253
v_mfma_f32_16x16x32_bf16 v[156:159],a[180:183],v[160:163],v[156:159]
s_waitcnt lgkmcnt(0)
v_exp_f32 v254,v254
v_mfma_f32_16x16x32_bf16 v[156:159],a[176:179],v[128:131],v[156:159]
s_addk_i32 m0,4096
v_exp_f32 v255,v255
v_mfma_f32_16x16x32_bf16 a[64:67],a[192:195],v[200:203],a[64:67]
v_cvt_pk_bf16_f32 v237,v250,v251
v_cvt_pk_bf16_f32 v238,v252,v253
v_mfma_f32_16x16x32_bf16 a[64:67],a[196:199],v[204:207],a[64:67]
v_cvt_pk_bf16_f32 v239,v254,v255
v_bfe_u32 v32,v6,4,2
v_mfma_f32_16x16x32_bf16 a[68:71],a[204:207],v[204:207],a[68:71]
buffer_load_dwordx4 v11,s[72:75],s80 offen lds
v_mfma_f32_16x16x32_bf16 a[68:71],a[200:203],v[200:203],a[68:71]
v_exp_f32 v136,v136
v_mfma_f32_16x16x32_bf16 a[72:75],a[208:211],v[200:203],a[72:75]
v_exp_f32 v137,v137
v_mfma_f32_16x16x32_bf16 a[72:75],a[212:215],v[204:207],a[72:75]
v_exp_f32 v138,v138
v_mfma_f32_16x16x32_bf16 a[76:79],a[220:223],v[204:207],a[76:79]
v_exp_f32 v139,v139
v_mfma_f32_16x16x32_bf16 a[76:79],a[216:219],v[200:203],a[76:79]
v_cvt_pk_bf16_f32 v136,v136,v137
v_cvt_pk_bf16_f32 v137,v138,v139
v_mfma_f32_16x16x32_bf16 a[80:83],a[224:227],v[200:203],a[80:83]
v_exp_f32 v140,v140
v_mfma_f32_16x16x32_bf16 a[80:83],a[228:231],v[204:207],a[80:83]
v_exp_f32 v141,v141
v_mfma_f32_16x16x32_bf16 a[84:87],a[236:239],v[204:207],a[84:87]
s_addk_i32 m0,1024
v_exp_f32 v142,v142
v_mfma_f32_16x16x32_bf16 a[84:87],a[232:235],v[200:203],a[84:87]
v_exp_f32 v143,v143
v_mfma_f32_16x16x32_bf16 a[88:91],a[240:243],v[200:203],a[88:91]
v_cvt_pk_bf16_f32 v138,v140,v141
v_cvt_pk_bf16_f32 v139,v142,v143
v_mfma_f32_16x16x32_bf16 a[88:91],a[244:247],v[204:207],a[88:91]
buffer_load_dwordx4 v7,s[72:75],s83 offen lds
v_mfma_f32_16x16x32_bf16 a[92:95],a[252:255],v[204:207],a[92:95]
v_exp_f32 v152,v152
v_mfma_f32_16x16x32_bf16 a[92:95],a[248:251],v[200:203],a[92:95]
v_exp_f32 v153,v153
v_smfmac_f32_16x16x64_bf16 v[56:59],v[28:31],v[200:207],v27
v_cvt_pk_bf16_f32 v140,v152,v153
v_mfma_f32_16x16x32_bf16 v[168:171],a[128:131],v[132:135],v[36:39]
s_lshl_b32 s95,s42,2
s_add_u32 s95,s95,s40
v_bfe_u32 v19,s55,13,1
v_mfma_f32_16x16x32_bf16 v[168:171],a[132:135],v[164:167],v[168:171]
s_lshl_b32 s95,s95,4
v_mov_b32 v18,s68
v_or_b32 v19,s69,v19
v_mfma_f32_16x16x32_bf16 v[168:171],a[136:139],v[196:199],v[168:171]
v_exp_f32 v154,v154
v_mfma_f32_16x16x32_bf16 v[168:171],a[140:143],v[228:231],v[168:171]
v_exp_f32 v155,v155
v_mfma_f32_16x16x32_bf16 v[172:175],a[156:159],v[228:231],v[36:39]
v_exp_f32 v156,v156
v_mfma_f32_16x16x32_bf16 v[172:175],a[152:155],v[196:199],v[172:175]
v_exp_f32 v157,v157
v_mfma_f32_16x16x32_bf16 v[172:175],a[148:151],v[164:167],v[172:175]
v_exp_f32 v158,v158
v_mfma_f32_16x16x32_bf16 v[172:175],a[144:147],v[132:135],v[172:175]
buffer_load_dwordx4 v8,s[72:75],s83 offen offset:1024 lds
v_mfma_f32_16x16x32_bf16 v[184:187],a[160:163],v[132:135],v[36:39]
v_exp_f32 v159,v159
v_mfma_f32_16x16x32_bf16 v[184:187],a[164:167],v[164:167],v[184:187]
v_cvt_pk_bf16_f32 v141,v154,v155
v_cvt_pk_bf16_f32 v142,v156,v157
v_mfma_f32_16x16x32_bf16 v[184:187],a[168:171],v[196:199],v[184:187]
v_cvt_pk_bf16_f32 v143,v158,v159
v_lshl_add_u32 v25,v32,3,v25
v_mfma_f32_16x16x32_bf16 v[184:187],a[172:175],v[228:231],v[184:187]
s_waitcnt vmcnt(6)
s_mov_b64 exec,1
buffer_store_dwordx2 v[18:19],v6,s[64:67],s95 offen sc1
s_mov_b64 exec,-1
v_mfma_f32_16x16x32_bf16 v[188:191],a[188:191],v[228:231],v[36:39]
v_exp_f32 v168,v168
v_mfma_f32_16x16x32_bf16 v[188:191],a[184:187],v[196:199],v[188:191]
v_exp_f32 v169,v169
v_mfma_f32_16x16x32_bf16 v[188:191],a[180:183],v[164:167],v[188:191]
v_exp_f32 v170,v170
v_mfma_f32_16x16x32_bf16 v[188:191],a[176:179],v[132:135],v[188:191]
v_exp_f32 v171,v171
v_mfma_f32_16x16x32_bf16 a[96:99],a[192:195],v[232:235],a[96:99]
ds_read_b64_tr_b16 a[192:193],v2
ds_read_b64_tr_b16 a[194:195],v2 offset:4096
v_mfma_f32_16x16x32_bf16 a[96:99],a[196:199],v[236:239],a[96:99]
ds_read_b64_tr_b16 a[196:197],v2 offset:8192
v_cvt_pk_bf16_f32 v168,v168,v169
v_mfma_f32_16x16x32_bf16 a[100:103],a[204:207],v[236:239],a[100:103]
buffer_load_dwordx4 v9,s[72:75],s83 offen offset:2048 lds
v_mfma_f32_16x16x32_bf16 a[100:103],a[200:203],v[232:235],a[100:103]
ds_read_b64_tr_b16 a[198:199],v2 offset:12288
ds_read_b64_tr_b16 a[204:205],v3 offset:8192
v_mfma_f32_16x16x32_bf16 a[104:107],a[208:211],v[232:235],a[104:107]
ds_read_b64_tr_b16 a[206:207],v3 offset:12288
v_cvt_pk_bf16_f32 v169,v170,v171
v_mfma_f32_16x16x32_bf16 a[104:107],a[212:215],v[236:239],a[104:107]
ds_read_b64_tr_b16 a[200:201],v3
v_bfe_u32 v32,v6,8,1
v_mfma_f32_16x16x32_bf16 a[108:111],a[220:223],v[236:239],a[108:111]
ds_read_b64_tr_b16 a[202:203],v3 offset:4096
v_lshlrev_b32 v32,5,v32
v_mfma_f32_16x16x32_bf16 a[108:111],a[216:219],v[232:235],a[108:111]
ds_read_b64_tr_b16 a[208:209],v2 offset:256
ds_read_b64_tr_b16 a[210:211],v2 offset:4352
v_mfma_f32_16x16x32_bf16 a[112:115],a[224:227],v[232:235],a[112:115]
ds_read_b64_tr_b16 a[212:213],v2 offset:8448
v_add_u32 v1,v25,v32
v_mfma_f32_16x16x32_bf16 a[112:115],a[228:231],v[236:239],a[112:115]
ds_read_b64_tr_b16 a[214:215],v2 offset:12544
v_add_u32 v1,81920,v1
v_mfma_f32_16x16x32_bf16 a[116:119],a[236:239],v[236:239],a[116:119]
v_exp_f32 v172,v172
v_mfma_f32_16x16x32_bf16 a[116:119],a[232:235],v[232:235],a[116:119]
ds_read_b64_tr_b16 a[220:221],v3 offset:8448
ds_read_b64_tr_b16 a[222:223],v3 offset:12544
v_mfma_f32_16x16x32_bf16 a[120:123],a[240:243],v[232:235],a[120:123]
s_cmp_ge_u32 0,s71
s_cselect_b32 s80,2,0
ds_read_b64_tr_b16 a[216:217],v3 offset:256
v_mfma_f32_16x16x32_bf16 a[120:123],a[244:247],v[236:239],a[120:123]
s_mul_i32 s80,s80,163840
buffer_load_dwordx4 v10,s[72:75],s83 offen offset:3072 lds
v_mfma_f32_16x16x32_bf16 a[124:127],a[252:255],v[236:239],a[124:127]
s_add_u32 s80,s80,s81
ds_read_b64_tr_b16 a[218:219],v3 offset:4352
ds_read_b64_tr_b16 a[224:225],v2 offset:512
v_mfma_f32_16x16x32_bf16 a[124:127],a[248:251],v[232:235],a[124:127]
ds_read_b64_tr_b16 a[226:227],v2 offset:4608
v_smfmac_f32_16x16x64_bf16 v[60:63],v[28:31],v[232:239],v27
ds_read_b64_tr_b16 a[228:229],v2 offset:8704
v_mfma_f32_16x16x32_bf16 v[200:203],a[128:131],v[144:147],v[40:43]
ds_read_b64_tr_b16 a[230:231],v2 offset:12800
v_mfma_f32_16x16x32_bf16 v[200:203],a[132:135],v[176:179],v[200:203]
ds_read_b64_tr_b16 a[236:237],v3 offset:8704
ds_read_b64_tr_b16 a[238:239],v3 offset:12800
v_mfma_f32_16x16x32_bf16 v[200:203],a[136:139],v[208:211],v[200:203]
ds_read_b64_tr_b16 a[232:233],v3 offset:512
v_mfma_f32_16x16x32_bf16 v[200:203],a[140:143],v[240:243],v[200:203]
ds_read_b64_tr_b16 a[234:235],v3 offset:4608
v_mfma_f32_16x16x32_bf16 v[204:207],a[156:159],v[240:243],v[40:43]
s_addk_i32 m0,4096
ds_read_b64_tr_b16 a[240:241],v2 offset:768
v_mfma_f32_16x16x32_bf16 v[204:207],a[152:155],v[208:211],v[204:207]
ds_read_b64_tr_b16 a[242:243],v2 offset:4864
ds_read_b64_tr_b16 a[244:245],v2 offset:8960
v_mfma_f32_16x16x32_bf16 v[204:207],a[148:151],v[176:179],v[204:207]
ds_read_b64_tr_b16 a[246:247],v2 offset:13056
v_sub_u32 v2,v25,v32
v_mfma_f32_16x16x32_bf16 v[204:207],a[144:147],v[144:147],v[204:207]
buffer_load_dwordx4 v11,s[72:75],s83 offen lds
v_mfma_f32_16x16x32_bf16 v[216:219],a[160:163],v[144:147],v[40:43]
s_add_u32 s83,s80,20480
ds_read_b64_tr_b16 a[252:253],v3 offset:8960
ds_read_b64_tr_b16 a[254:255],v3 offset:13056
v_mfma_f32_16x16x32_bf16 v[216:219],a[164:167],v[176:179],v[216:219]
ds_read_b64_tr_b16 a[248:249],v3 offset:768
v_add_u32 v2,81920,v2
v_mfma_f32_16x16x32_bf16 v[216:219],a[168:171],v[208:211],v[216:219]
ds_read_b64_tr_b16 a[250:251],v3 offset:4864
v_mfma_f32_16x16x32_bf16 v[216:219],a[172:175],v[240:243],v[216:219]
v_exp_f32 v173,v173
v_mfma_f32_16x16x32_bf16 v[220:223],a[188:191],v[240:243],v[40:43]
v_exp_f32 v174,v174
v_mfma_f32_16x16x32_bf16 v[220:223],a[184:187],v[208:211],v[220:223]
v_exp_f32 v175,v175
v_mfma_f32_16x16x32_bf16 v[220:223],a[180:183],v[176:179],v[220:223]
v_cvt_pk_bf16_f32 v170,v172,v173
v_cvt_pk_bf16_f32 v171,v174,v175
v_mfma_f32_16x16x32_bf16 v[220:223],a[176:179],v[144:147],v[220:223]
s_add_u32 m0,s82,40960
v_exp_f32 v184,v184
s_waitcnt lgkmcnt(4)
v_mfma_f32_16x16x32_bf16 a[0:3],a[192:195],v[136:139],a[0:3]
v_exp_f32 v185,v185
v_mfma_f32_16x16x32_bf16 a[0:3],a[196:199],v[140:143],a[0:3]
v_exp_f32 v186,v186
v_mfma_f32_16x16x32_bf16 a[4:7],a[204:207],v[140:143],a[4:7]
buffer_load_dwordx4 v7,s[72:75],s80 offen lds
v_mfma_f32_16x16x32_bf16 a[4:7],a[200:203],v[136:139],a[4:7]
v_exp_f32 v187,v187
v_mfma_f32_16x16x32_bf16 a[8:11],a[208:211],v[136:139],a[8:11]
v_exp_f32 v188,v188
v_mfma_f32_16x16x32_bf16 a[8:11],a[212:215],v[140:143],a[8:11]
v_exp_f32 v189,v189
v_mfma_f32_16x16x32_bf16 a[12:15],a[220:223],v[140:143],a[12:15]
v_exp_f32 v190,v190
v_mfma_f32_16x16x32_bf16 a[12:15],a[216:219],v[136:139],a[12:15]
v_exp_f32 v191,v191
v_mfma_f32_16x16x32_bf16 a[16:19],a[224:227],v[136:139],a[16:19]
v_cvt_pk_bf16_f32 v172,v184,v185
v_cvt_pk_bf16_f32 v173,v186,v187
v_mfma_f32_16x16x32_bf16 a[16:19],a[228:231],v[140:143],a[16:19]
v_cvt_pk_bf16_f32 v174,v188,v189
v_cvt_pk_bf16_f32 v175,v190,v191
v_mfma_f32_16x16x32_bf16 a[20:23],a[236:239],v[140:143],a[20:23]
v_exp_f32 v200,v200
v_mfma_f32_16x16x32_bf16 a[20:23],a[232:235],v[136:139],a[20:23]
v_exp_f32 v201,v201
v_mfma_f32_16x16x32_bf16 a[24:27],a[240:243],v[136:139],a[24:27]
v_exp_f32 v202,v202
v_mfma_f32_16x16x32_bf16 a[24:27],a[244:247],v[140:143],a[24:27]
s_waitcnt lgkmcnt(0)
buffer_load_dwordx4 v8,s[72:75],s80 offen offset:1024 lds
v_mfma_f32_16x16x32_bf16 a[28:31],a[252:255],v[140:143],a[28:31]
v_exp_f32 v203,v203
v_mfma_f32_16x16x32_bf16 a[28:31],a[248:251],v[136:139],a[28:31]
v_cvt_pk_bf16_f32 v200,v200,v201
v_cvt_pk_bf16_f32 v201,v202,v203
v_smfmac_f32_16x16x64_bf16 v[48:51],v[28:31],v[136:143],v27
s_barrier
v_exp_f32 v204,v204
v_mfma_f32_16x16x32_bf16 v[232:235],a[128:131],v[148:151],v[44:47]
v_exp_f32 v205,v205
v_mfma_f32_16x16x32_bf16 v[232:235],a[132:135],v[180:183],v[232:235]
v_exp_f32 v206,v206
v_mfma_f32_16x16x32_bf16 v[232:235],a[136:139],v[212:215],v[232:235]
v_exp_f32 v207,v207
v_mfma_f32_16x16x32_bf16 v[232:235],a[140:143],v[244:247],v[232:235]
v_cvt_pk_bf16_f32 v202,v204,v205
v_cvt_pk_bf16_f32 v203,v206,v207
v_mfma_f32_16x16x32_bf16 v[236:239],a[156:159],v[244:247],v[44:47]
v_exp_f32 v216,v216
v_mfma_f32_16x16x32_bf16 v[236:239],a[152:155],v[212:215],v[236:239]
v_exp_f32 v217,v217
v_mfma_f32_16x16x32_bf16 v[236:239],a[148:151],v[180:183],v[236:239]
buffer_load_dwordx4 v9,s[72:75],s80 offen offset:2048 lds
v_mfma_f32_16x16x32_bf16 v[236:239],a[144:147],v[148:151],v[236:239]
v_exp_f32 v218,v218
v_mfma_f32_16x16x32_bf16 v[248:251],a[160:163],v[148:151],v[44:47]
v_exp_f32 v219,v219
v_mfma_f32_16x16x32_bf16 v[248:251],a[164:167],v[180:183],v[248:251]
v_exp_f32 v220,v220
v_mfma_f32_16x16x32_bf16 v[248:251],a[168:171],v[212:215],v[248:251]
v_exp_f32 v221,v221
v_mfma_f32_16x16x32_bf16 v[248:251],a[172:175],v[244:247],v[248:251]
v_exp_f32 v222,v222
v_mfma_f32_16x16x32_bf16 v[252:255],a[188:191],v[244:247],v[44:47]
v_exp_f32 v223,v223
v_mfma_f32_16x16x32_bf16 v[252:255],a[184:187],v[212:215],v[252:255]
v_cvt_pk_bf16_f32 v204,v216,v217
v_cvt_pk_bf16_f32 v205,v218,v219
v_mfma_f32_16x16x32_bf16 v[252:255],a[180:183],v[180:183],v[252:255]
v_cvt_pk_bf16_f32 v206,v220,v221
v_cvt_pk_bf16_f32 v207,v222,v223
v_mfma_f32_16x16x32_bf16 v[252:255],a[176:179],v[148:151],v[252:255]
v_exp_f32 v232,v232
v_mfma_f32_16x16x32_bf16 a[32:35],a[192:195],v[168:171],a[32:35]
v_exp_f32 v233,v233
v_mfma_f32_16x16x32_bf16 a[32:35],a[196:199],v[172:175],a[32:35]
)A"
R"A(buffer_load_dwordx4 v10,s[72:75],s80 offen offset:3072 lds
v_mfma_f32_16x16x32_bf16 a[36:39],a[204:207],v[172:175],a[36:39]
v_add_f32 v48,v49,v48
v_max_f32 v20,0x00800000,v48
v_mfma_f32_16x16x32_bf16 a[36:39],a[200:203],v[168:171],a[36:39]
v_exp_f32 v234,v234
v_mfma_f32_16x16x32_bf16 a[40:43],a[208:211],v[168:171],a[40:43]
v_exp_f32 v235,v235
v_mfma_f32_16x16x32_bf16 a[40:43],a[212:215],v[172:175],a[40:43]
v_cvt_pk_bf16_f32 v232,v232,v233
v_cvt_pk_bf16_f32 v233,v234,v235
v_mfma_f32_16x16x32_bf16 a[44:47],a[220:223],v[172:175],a[44:47]
v_exp_f32 v236,v236
v_mfma_f32_16x16x32_bf16 a[44:47],a[216:219],v[168:171],a[44:47]
v_exp_f32 v237,v237
v_mfma_f32_16x16x32_bf16 a[48:51],a[224:227],v[168:171],a[48:51]
v_exp_f32 v238,v238
v_mfma_f32_16x16x32_bf16 a[48:51],a[228:231],v[172:175],a[48:51]
s_addk_i32 m0,4096
v_exp_f32 v239,v239
v_mfma_f32_16x16x32_bf16 a[52:55],a[236:239],v[172:175],a[52:55]
v_rcp_f32 v20,v20
v_mfma_f32_16x16x32_bf16 a[52:55],a[232:235],v[168:171],a[52:55]
v_cvt_pk_bf16_f32 v234,v236,v237
v_cvt_pk_bf16_f32 v235,v238,v239
v_mfma_f32_16x16x32_bf16 a[56:59],a[240:243],v[168:171],a[56:59]
buffer_load_dwordx4 v11,s[72:75],s80 offen lds
v_mfma_f32_16x16x32_bf16 a[56:59],a[244:247],v[172:175],a[56:59]
v_exp_f32 v248,v248
v_mfma_f32_16x16x32_bf16 a[60:63],a[252:255],v[172:175],a[60:63]
v_exp_f32 v249,v249
v_mfma_f32_16x16x32_bf16 a[60:63],a[248:251],v[168:171],a[60:63]
v_exp_f32 v250,v250
v_smfmac_f32_16x16x64_bf16 v[52:55],v[28:31],v[168:175],v27
v_mul_f32 v21,0xbe22f983,v20
v_mfma_f32_16x16x32_bf16 a[64:67],a[192:195],v[200:203],a[64:67]
v_exp_f32 v251,v251
v_mfma_f32_16x16x32_bf16 a[64:67],a[196:199],v[204:207],a[64:67]
v_exp_f32 v252,v252
v_mfma_f32_16x16x32_bf16 a[68:71],a[204:207],v[204:207],a[68:71]
v_exp_f32 v253,v253
v_mfma_f32_16x16x32_bf16 a[68:71],a[200:203],v[200:203],a[68:71]
s_addk_i32 m0,1024
v_exp_f32 v254,v254
v_mfma_f32_16x16x32_bf16 a[72:75],a[208:211],v[200:203],a[72:75]
v_exp_f32 v255,v255
v_mfma_f32_16x16x32_bf16 a[72:75],a[212:215],v[204:207],a[72:75]
v_cvt_pk_bf16_f32 v236,v248,v249
v_cvt_pk_bf16_f32 v237,v250,v251
v_mfma_f32_16x16x32_bf16 a[76:79],a[220:223],v[204:207],a[76:79]
buffer_load_dwordx4 v7,s[72:75],s83 offen lds
v_mfma_f32_16x16x32_bf16 a[76:79],a[216:219],v[200:203],a[76:79]
v_cvt_pk_bf16_f32 v238,v252,v253
v_cvt_pk_bf16_f32 v239,v254,v255
v_mfma_f32_16x16x32_bf16 a[80:83],a[224:227],v[200:203],a[80:83]
.p2align 3
v_accvgpr_read_b32 v136,a0
v_accvgpr_read_b32 v137,a1
v_accvgpr_read_b32 v138,a2
v_accvgpr_read_b32 v139,a3
v_accvgpr_read_b32 v12,a4
v_accvgpr_read_b32 v13,a5
v_accvgpr_read_b32 v14,a6
v_accvgpr_read_b32 v15,a7
v_pk_fma_f32 v[136:137],v[20:21],v[136:137],v[20:21] op_sel:[0,0,1] op_sel_hi:[0,1,1]
v_pk_fma_f32 v[138:139],v[20:21],v[138:139],v[20:21] op_sel:[0,0,1] op_sel_hi:[0,1,1]
v_pk_fma_f32 v[12:13],v[20:21],v[12:13],v[20:21] op_sel:[0,0,1] op_sel_hi:[0,1,1]
v_pk_fma_f32 v[14:15],v[20:21],v[14:15],v[20:21] op_sel:[0,0,1] op_sel_hi:[0,1,1]
v_cvt_pk_bf16_f32 v136,v136,v137
v_cvt_pk_bf16_f32 v137,v138,v139
v_cvt_pk_bf16_f32 v138,v12,v13
v_cvt_pk_bf16_f32 v139,v14,v15
v_mfma_f32_16x16x32_bf16 a[80:83],a[228:231],v[204:207],a[80:83]
ds_read_b64_tr_b16 a[128:129],v1
ds_read_b64_tr_b16 a[130:131],v1 offset:5120
v_mfma_f32_16x16x32_bf16 a[84:87],a[236:239],v[204:207],a[84:87]
ds_read_b64_tr_b16 a[132:133],v1 offset:10240
v_accvgpr_read_b32 v152,a16
v_mfma_f32_16x16x32_bf16 a[84:87],a[232:235],v[200:203],a[84:87]
ds_read_b64_tr_b16 a[134:135],v1 offset:15360
v_accvgpr_read_b32 v153,a17
v_mfma_f32_16x16x32_bf16 a[88:91],a[240:243],v[200:203],a[88:91]
ds_read_b64_tr_b16 a[136:137],v1 offset:20480
v_fma_f32 v152,v20,v152,v21
v_mfma_f32_16x16x32_bf16 a[88:91],a[244:247],v[204:207],a[88:91]
ds_read_b64_tr_b16 a[138:139],v1 offset:25600
ds_read_b64_tr_b16 a[140:141],v1 offset:30720
v_mfma_f32_16x16x32_bf16 a[92:95],a[252:255],v[204:207],a[92:95]
.p2align 3
v_accvgpr_read_b32 v140,a8
v_accvgpr_read_b32 v141,a9
v_accvgpr_read_b32 v142,a10
v_accvgpr_read_b32 v143,a11
v_accvgpr_read_b32 v12,a12
v_accvgpr_read_b32 v13,a13
v_accvgpr_read_b32 v14,a14
v_accvgpr_read_b32 v15,a15
v_pk_fma_f32 v[140:141],v[20:21],v[140:141],v[20:21] op_sel:[0,0,1] op_sel_hi:[0,1,1]
v_pk_fma_f32 v[142:143],v[20:21],v[142:143],v[20:21] op_sel:[0,0,1] op_sel_hi:[0,1,1]
v_pk_fma_f32 v[12:13],v[20:21],v[12:13],v[20:21] op_sel:[0,0,1] op_sel_hi:[0,1,1]
v_pk_fma_f32 v[14:15],v[20:21],v[14:15],v[20:21] op_sel:[0,0,1] op_sel_hi:[0,1,1]
v_cvt_pk_bf16_f32 v140,v140,v141
v_cvt_pk_bf16_f32 v141,v142,v143
v_cvt_pk_bf16_f32 v142,v12,v13
v_cvt_pk_bf16_f32 v143,v14,v15
v_mfma_f32_16x16x32_bf16 a[92:95],a[248:251],v[200:203],a[92:95]
ds_read_b64_tr_b16 a[142:143],v1 offset:35840
v_fma_f32 v153,v20,v153,v21
v_smfmac_f32_16x16x64_bf16 v[56:59],v[28:31],v[200:207],v27
v_accvgpr_read_b32 v154,a18
v_mfma_f32_16x16x32_bf16 a[96:99],a[192:195],v[232:235],a[96:99]
buffer_store_dwordx4 v[136:139],v6,s[36:39],s84 offen sc1
v_mfma_f32_16x16x32_bf16 a[96:99],a[196:199],v[236:239],a[96:99]
v_accvgpr_read_b32 v155,a19
v_cvt_pk_bf16_f32 v152,v152,v153
v_mfma_f32_16x16x32_bf16 a[100:103],a[204:207],v[236:239],a[100:103]
v_fma_f32 v154,v20,v154,v21
v_fma_f32 v155,v20,v155,v21
v_mfma_f32_16x16x32_bf16 a[100:103],a[200:203],v[232:235],a[100:103]
v_cvt_pk_bf16_f32 v153,v154,v155
v_accvgpr_read_b32 v154,a20
v_mfma_f32_16x16x32_bf16 a[104:107],a[208:211],v[232:235],a[104:107]
buffer_load_dwordx4 v8,s[72:75],s83 offen offset:1024 lds
v_mfma_f32_16x16x32_bf16 a[104:107],a[212:215],v[236:239],a[104:107]
v_accvgpr_read_b32 v155,a21
v_accvgpr_read_b32 v16,a22
v_mfma_f32_16x16x32_bf16 a[108:111],a[220:223],v[236:239],a[108:111]
v_accvgpr_read_b32 v17,a23
v_fma_f32 v154,v20,v154,v21
v_mfma_f32_16x16x32_bf16 a[108:111],a[216:219],v[232:235],a[108:111]
v_fma_f32 v155,v20,v155,v21
v_fma_f32 v16,v20,v16,v21
v_mfma_f32_16x16x32_bf16 a[112:115],a[224:227],v[232:235],a[112:115]
v_fma_f32 v17,v20,v17,v21
v_cvt_pk_bf16_f32 v154,v154,v155
v_mfma_f32_16x16x32_bf16 a[112:115],a[228:231],v[236:239],a[112:115]
ds_read_b64_tr_b16 a[144:145],v2 offset:32
v_cvt_pk_bf16_f32 v155,v16,v17
v_mfma_f32_16x16x32_bf16 a[116:119],a[236:239],v[236:239],a[116:119]
ds_read_b64_tr_b16 a[146:147],v2 offset:5152
ds_read_b64_tr_b16 a[148:149],v2 offset:10272
v_mfma_f32_16x16x32_bf16 a[116:119],a[232:235],v[232:235],a[116:119]
buffer_load_dwordx4 v9,s[72:75],s83 offen offset:2048 lds
v_mfma_f32_16x16x32_bf16 a[120:123],a[240:243],v[232:235],a[120:123]
ds_read_b64_tr_b16 a[150:151],v2 offset:15392
ds_read_b64_tr_b16 a[152:153],v2 offset:20512
v_mfma_f32_16x16x32_bf16 a[120:123],a[244:247],v[236:239],a[120:123]
ds_read_b64_tr_b16 a[154:155],v2 offset:25632
v_accvgpr_read_b32 v156,a24
v_mfma_f32_16x16x32_bf16 a[124:127],a[252:255],v[236:239],a[124:127]
ds_read_b64_tr_b16 a[156:157],v2 offset:30752
v_accvgpr_read_b32 v157,a25
v_mfma_f32_16x16x32_bf16 a[124:127],a[248:251],v[232:235],a[124:127]
ds_read_b64_tr_b16 a[158:159],v2 offset:35872
v_fma_f32 v156,v20,v156,v21
v_smfmac_f32_16x16x64_bf16 v[60:63],v[28:31],v[232:239],v27
s_waitcnt lgkmcnt(8)
v_fma_f32 v157,v20,v157,v21
v_mfma_f32_16x16x32_bf16 a[192:195],a[128:131],v[64:67],0
s_lshl_b32 s94,s70,4
v_accvgpr_read_b32 v158,a26
v_accvgpr_read_b32 v159,a27
v_mfma_f32_16x16x32_bf16 a[192:195],a[132:135],v[68:71],a[192:195]
s_add_u32 s94,s94,s40
v_cvt_pk_bf16_f32 v156,v156,v157
v_fma_f32 v158,v20,v158,v21
v_mfma_f32_16x16x32_bf16 a[192:195],a[136:139],v[72:75],a[192:195]
s_lshl_b32 s94,s94,4
buffer_store_dwordx4 v[140:143],v6,s[36:39],s84 offen offset:1024 sc1
v_mfma_f32_16x16x32_bf16 a[192:195],a[140:143],v[76:79],a[192:195]
v_fma_f32 v159,v20,v159,v21
v_cvt_pk_bf16_f32 v157,v158,v159
v_mfma_f32_16x16x32_bf16 a[196:199],a[140:143],v[92:95],0
v_accvgpr_read_b32 v158,a28
v_accvgpr_read_b32 v159,a29
v_mfma_f32_16x16x32_bf16 a[196:199],a[136:139],v[88:91],a[196:199]
v_accvgpr_read_b32 v12,a30
v_accvgpr_read_b32 v13,a31
v_mfma_f32_16x16x32_bf16 a[196:199],a[132:135],v[84:87],a[196:199]
buffer_load_dwordx4 v10,s[72:75],s83 offen offset:3072 lds
v_mfma_f32_16x16x32_bf16 a[196:199],a[128:131],v[80:83],a[196:199]
v_fma_f32 v158,v20,v158,v21
v_fma_f32 v159,v20,v159,v21
v_mfma_f32_16x16x32_bf16 a[200:203],a[128:131],v[96:99],0
v_fma_f32 v12,v20,v12,v21
v_fma_f32 v13,v20,v13,v21
v_mfma_f32_16x16x32_bf16 a[200:203],a[132:135],v[100:103],a[200:203]
v_cvt_pk_bf16_f32 v158,v158,v159
v_cvt_pk_bf16_f32 v159,v12,v13
v_mfma_f32_16x16x32_bf16 a[200:203],a[136:139],v[104:107],a[200:203]
ds_read_b64_tr_b16 a[160:161],v1 offset:64
ds_read_b64_tr_b16 a[162:163],v1 offset:5184
v_mfma_f32_16x16x32_bf16 a[200:203],a[140:143],v[108:111],a[200:203]
ds_read_b64_tr_b16 a[164:165],v1 offset:10304
v_add_f32 v52,v53,v52
v_mfma_f32_16x16x32_bf16 a[204:207],a[140:143],v[124:127],0
ds_read_b64_tr_b16 a[166:167],v1 offset:15424
v_max_f32 v22,0x00800000,v52
v_mfma_f32_16x16x32_bf16 a[204:207],a[136:139],v[120:123],a[204:207]
s_addk_i32 m0,4096
ds_read_b64_tr_b16 a[168:169],v1 offset:20544
v_bfe_u32 v24,v6,4,2
v_mfma_f32_16x16x32_bf16 a[204:207],a[132:135],v[116:119],a[204:207]
ds_read_b64_tr_b16 a[170:171],v1 offset:25664
ds_read_b64_tr_b16 a[172:173],v1 offset:30784
v_mfma_f32_16x16x32_bf16 a[204:207],a[128:131],v[112:115],a[204:207]
s_waitcnt lgkmcnt(7)
ds_read_b64_tr_b16 a[174:175],v1 offset:35904
v_min_u32 v24,2,v24
v_mfma_f32_16x16x32_bf16 a[208:211],a[144:147],v[64:67],0
buffer_load_dwordx4 v11,s[72:75],s83 offen lds
v_mfma_f32_16x16x32_bf16 a[208:211],a[148:151],v[68:71],a[208:211]
v_rcp_f32 v22,v22
v_mfma_f32_16x16x32_bf16 a[208:211],a[152:155],v[72:75],a[208:211]
v_mul_f32 v23,0xbe22f983,v22
v_add_u32 v24,s71,v24
v_mfma_f32_16x16x32_bf16 a[208:211],a[156:159],v[76:79],a[208:211]
.p2align 3
v_accvgpr_read_b32 v168,a32
v_accvgpr_read_b32 v169,a33
v_accvgpr_read_b32 v170,a34
v_accvgpr_read_b32 v171,a35
v_accvgpr_read_b32 v12,a36
v_accvgpr_read_b32 v13,a37
v_accvgpr_read_b32 v14,a38
v_accvgpr_read_b32 v15,a39
v_pk_fma_f32 v[168:169],v[22:23],v[168:169],v[22:23] op_sel:[0,0,1] op_sel_hi:[0,1,1]
v_pk_fma_f32 v[170:171],v[22:23],v[170:171],v[22:23] op_sel:[0,0,1] op_sel_hi:[0,1,1]
v_pk_fma_f32 v[12:13],v[22:23],v[12:13],v[22:23] op_sel:[0,0,1] op_sel_hi:[0,1,1]
v_pk_fma_f32 v[14:15],v[22:23],v[14:15],v[22:23] op_sel:[0,0,1] op_sel_hi:[0,1,1]
v_cvt_pk_bf16_f32 v168,v168,v169
v_cvt_pk_bf16_f32 v169,v170,v171
v_cvt_pk_bf16_f32 v170,v12,v13
v_cvt_pk_bf16_f32 v171,v14,v15
v_mfma_f32_16x16x32_bf16 a[212:215],a[156:159],v[92:95],0
s_cmp_ge_u32 1,s71
s_cselect_b32 s80,4,2
v_add_u32 v24,1,v24
v_mfma_f32_16x16x32_bf16 a[212:215],a[152:155],v[88:91],a[212:215]
s_mul_i32 s80,s80,163840
s_add_u32 s80,s80,s81
v_and_b32 v24,3,v24
v_mfma_f32_16x16x32_bf16 a[212:215],a[148:151],v[84:87],a[212:215]
s_add_u32 s83,s80,20480
buffer_store_dwordx4 v[152:155],v6,s[36:39],s84 offen offset:2048 sc1
v_mfma_f32_16x16x32_bf16 a[212:215],a[144:147],v[80:83],a[212:215]
ds_read_b64_tr_b16 a[176:177],v2 offset:96
v_lshlrev_b32 v24,6,v24
v_mfma_f32_16x16x32_bf16 a[216:219],a[144:147],v[96:99],0
ds_read_b64_tr_b16 a[178:179],v2 offset:5216
ds_read_b64_tr_b16 a[180:181],v2 offset:10336
v_mfma_f32_16x16x32_bf16 a[216:219],a[148:151],v[100:103],a[216:219]
ds_read_b64_tr_b16 a[182:183],v2 offset:15456
v_accvgpr_read_b32 v184,a48
v_mfma_f32_16x16x32_bf16 a[216:219],a[152:155],v[104:107],a[216:219]
ds_read_b64_tr_b16 a[184:185],v2 offset:20576
v_accvgpr_read_b32 v185,a49
v_mfma_f32_16x16x32_bf16 a[216:219],a[156:159],v[108:111],a[216:219]
ds_read_b64_tr_b16 a[186:187],v2 offset:25696
v_fma_f32 v184,v22,v184,v23
v_mfma_f32_16x16x32_bf16 a[220:223],a[156:159],v[124:127],0
ds_read_b64_tr_b16 a[188:189],v2 offset:30816
ds_read_b64_tr_b16 a[190:191],v2 offset:35936
v_mfma_f32_16x16x32_bf16 a[220:223],a[152:155],v[120:123],a[220:223]
.p2align 3
v_accvgpr_read_b32 v172,a40
v_accvgpr_read_b32 v173,a41
v_accvgpr_read_b32 v174,a42
v_accvgpr_read_b32 v175,a43
v_accvgpr_read_b32 v12,a44
v_accvgpr_read_b32 v13,a45
v_accvgpr_read_b32 v14,a46
v_accvgpr_read_b32 v15,a47
v_pk_fma_f32 v[172:173],v[22:23],v[172:173],v[22:23] op_sel:[0,0,1] op_sel_hi:[0,1,1]
v_pk_fma_f32 v[174:175],v[22:23],v[174:175],v[22:23] op_sel:[0,0,1] op_sel_hi:[0,1,1]
)A"
R"A(v_pk_fma_f32 v[12:13],v[22:23],v[12:13],v[22:23] op_sel:[0,0,1] op_sel_hi:[0,1,1]
v_pk_fma_f32 v[14:15],v[22:23],v[14:15],v[22:23] op_sel:[0,0,1] op_sel_hi:[0,1,1]
v_cvt_pk_bf16_f32 v172,v172,v173
v_cvt_pk_bf16_f32 v173,v174,v175
v_cvt_pk_bf16_f32 v174,v12,v13
v_cvt_pk_bf16_f32 v175,v14,v15
v_mfma_f32_16x16x32_bf16 a[220:223],a[148:151],v[116:119],a[220:223]
s_add_u32 m0,s82,122880
v_fma_f32 v185,v22,v185,v23
v_accvgpr_read_b32 v186,a50
v_mfma_f32_16x16x32_bf16 a[220:223],a[144:147],v[112:115],a[220:223]
s_waitcnt lgkmcnt(7)
v_accvgpr_read_b32 v187,a51
v_cvt_pk_bf16_f32 v184,v184,v185
v_mfma_f32_16x16x32_bf16 a[224:227],a[160:163],v[64:67],0
v_fma_f32 v186,v22,v186,v23
v_fma_f32 v187,v22,v187,v23
v_mfma_f32_16x16x32_bf16 a[224:227],a[164:167],v[68:71],a[224:227]
buffer_load_dwordx4 v7,s[72:75],s80 offen lds
v_mfma_f32_16x16x32_bf16 a[224:227],a[168:171],v[72:75],a[224:227]
v_cvt_pk_bf16_f32 v185,v186,v187
v_accvgpr_read_b32 v186,a52
v_mfma_f32_16x16x32_bf16 a[224:227],a[172:175],v[76:79],a[224:227]
v_accvgpr_read_b32 v187,a53
v_accvgpr_read_b32 v14,a54
v_mfma_f32_16x16x32_bf16 a[228:231],a[172:175],v[92:95],0
v_accvgpr_read_b32 v15,a55
v_fma_f32 v186,v22,v186,v23
v_mfma_f32_16x16x32_bf16 a[228:231],a[168:171],v[88:91],a[228:231]
v_fma_f32 v187,v22,v187,v23
v_fma_f32 v14,v22,v14,v23
v_mfma_f32_16x16x32_bf16 a[228:231],a[164:167],v[84:87],a[228:231]
v_fma_f32 v15,v22,v15,v23
v_cvt_pk_bf16_f32 v186,v186,v187
v_mfma_f32_16x16x32_bf16 a[228:231],a[160:163],v[80:83],a[228:231]
ds_read_b64_tr_b16 a[128:129],v1 offset:128
v_cvt_pk_bf16_f32 v187,v14,v15
v_mfma_f32_16x16x32_bf16 a[232:235],a[160:163],v[96:99],0
buffer_load_dwordx2 v[4:5],v24,s[64:67],s94 offen sc1
v_mfma_f32_16x16x32_bf16 a[232:235],a[164:167],v[100:103],a[232:235]
ds_read_b64_tr_b16 a[130:131],v1 offset:5248
ds_read_b64_tr_b16 a[132:133],v1 offset:10368
v_mfma_f32_16x16x32_bf16 a[232:235],a[168:171],v[104:107],a[232:235]
ds_read_b64_tr_b16 a[134:135],v1 offset:15488
v_accvgpr_read_b32 v188,a56
v_mfma_f32_16x16x32_bf16 a[232:235],a[172:175],v[108:111],a[232:235]
ds_read_b64_tr_b16 a[136:137],v1 offset:20608
v_accvgpr_read_b32 v189,a57
v_mfma_f32_16x16x32_bf16 a[236:239],a[172:175],v[124:127],0
buffer_store_dwordx4 v[156:159],v6,s[36:39],s84 offen offset:3072 sc1
v_mfma_f32_16x16x32_bf16 a[236:239],a[168:171],v[120:123],a[236:239]
ds_read_b64_tr_b16 a[138:139],v1 offset:25728
ds_read_b64_tr_b16 a[140:141],v1 offset:30848
v_mfma_f32_16x16x32_bf16 a[236:239],a[164:167],v[116:119],a[236:239]
ds_read_b64_tr_b16 a[142:143],v1 offset:35968
v_fma_f32 v188,v22,v188,v23
v_mfma_f32_16x16x32_bf16 a[236:239],a[160:163],v[112:115],a[236:239]
s_waitcnt lgkmcnt(7)
v_fma_f32 v189,v22,v189,v23
v_accvgpr_read_b32 v190,a58
v_mfma_f32_16x16x32_bf16 a[240:243],a[176:179],v[64:67],0
buffer_load_dwordx4 v8,s[72:75],s80 offen offset:1024 lds
v_mfma_f32_16x16x32_bf16 a[240:243],a[180:183],v[68:71],a[240:243]
v_accvgpr_read_b32 v191,a59
v_cvt_pk_bf16_f32 v188,v188,v189
v_mfma_f32_16x16x32_bf16 a[240:243],a[184:187],v[72:75],a[240:243]
v_fma_f32 v190,v22,v190,v23
v_fma_f32 v191,v22,v191,v23
v_mfma_f32_16x16x32_bf16 a[240:243],a[188:191],v[76:79],a[240:243]
v_cvt_pk_bf16_f32 v189,v190,v191
v_accvgpr_read_b32 v190,a60
v_mfma_f32_16x16x32_bf16 a[244:247],a[188:191],v[92:95],0
v_accvgpr_read_b32 v191,a61
v_accvgpr_read_b32 v16,a62
v_mfma_f32_16x16x32_bf16 a[244:247],a[184:187],v[88:91],a[244:247]
v_accvgpr_read_b32 v17,a63
v_fma_f32 v190,v22,v190,v23
v_mfma_f32_16x16x32_bf16 a[244:247],a[180:183],v[84:87],a[244:247]
v_fma_f32 v191,v22,v191,v23
v_fma_f32 v16,v22,v16,v23
v_mfma_f32_16x16x32_bf16 a[244:247],a[176:179],v[80:83],a[244:247]
v_fma_f32 v17,v22,v17,v23
v_cvt_pk_bf16_f32 v190,v190,v191
v_mfma_f32_16x16x32_bf16 a[248:251],a[176:179],v[96:99],0
ds_read_b64_tr_b16 a[144:145],v2 offset:160
v_cvt_pk_bf16_f32 v191,v16,v17
v_mfma_f32_16x16x32_bf16 a[248:251],a[180:183],v[100:103],a[248:251]
buffer_load_dwordx4 v9,s[72:75],s80 offen offset:2048 lds
v_mfma_f32_16x16x32_bf16 a[248:251],a[184:187],v[104:107],a[248:251]
ds_read_b64_tr_b16 a[146:147],v2 offset:5280
ds_read_b64_tr_b16 a[148:149],v2 offset:10400
v_mfma_f32_16x16x32_bf16 a[248:251],a[188:191],v[108:111],a[248:251]
ds_read_b64_tr_b16 a[150:151],v2 offset:15520
v_add_f32 v56,v57,v56
v_mfma_f32_16x16x32_bf16 a[252:255],a[188:191],v[124:127],0
ds_read_b64_tr_b16 a[152:153],v2 offset:20640
v_max_f32 v20,0x00800000,v56
v_mfma_f32_16x16x32_bf16 a[252:255],a[184:187],v[120:123],a[252:255]
ds_read_b64_tr_b16 a[154:155],v2 offset:25760
v_max3_f32 v18,|v136|,|v137|,|v138|
v_mfma_f32_16x16x32_bf16 a[252:255],a[180:183],v[116:119],a[252:255]
s_waitcnt vmcnt(18)
ds_read_b64_tr_b16 a[156:157],v2 offset:30880
ds_read_b64_tr_b16 a[158:159],v2 offset:36000
v_mfma_f32_16x16x32_bf16 a[252:255],a[176:179],v[112:115],a[252:255]
s_barrier
buffer_store_dwordx4 v[168:171],v6,s[36:39],s85 offen sc1
s_waitcnt lgkmcnt(8)
v_mfma_f32_16x16x32_bf16 a[0:3],a[128:131],v[64:67],0
v_max3_f32 v18,v18,|v139|,s18
v_fmac_f32 v18,0x39800000,v21
v_mfma_f32_16x16x32_bf16 a[0:3],a[132:135],v[68:71],a[0:3]
v_rcp_f32 v20,v20
v_mfma_f32_16x16x32_bf16 a[0:3],a[136:139],v[72:75],a[0:3]
ds_read_b64_tr_b16 a[160:161],v1 offset:192
v_mul_f32 v21,0xbe22f983,v20
v_mfma_f32_16x16x32_bf16 a[0:3],a[140:143],v[76:79],a[0:3]
.p2align 3
v_accvgpr_read_b32 v200,a64
v_accvgpr_read_b32 v201,a65
v_accvgpr_read_b32 v202,a66
v_accvgpr_read_b32 v203,a67
v_accvgpr_read_b32 v12,a68
v_accvgpr_read_b32 v13,a69
v_accvgpr_read_b32 v14,a70
v_accvgpr_read_b32 v15,a71
v_pk_fma_f32 v[200:201],v[20:21],v[200:201],v[20:21] op_sel:[0,0,1] op_sel_hi:[0,1,1]
v_pk_fma_f32 v[202:203],v[20:21],v[202:203],v[20:21] op_sel:[0,0,1] op_sel_hi:[0,1,1]
v_pk_fma_f32 v[12:13],v[20:21],v[12:13],v[20:21] op_sel:[0,0,1] op_sel_hi:[0,1,1]
v_pk_fma_f32 v[14:15],v[20:21],v[14:15],v[20:21] op_sel:[0,0,1] op_sel_hi:[0,1,1]
v_cvt_pk_bf16_f32 v200,v200,v201
v_cvt_pk_bf16_f32 v201,v202,v203
v_cvt_pk_bf16_f32 v202,v12,v13
v_cvt_pk_bf16_f32 v203,v14,v15
v_mfma_f32_16x16x32_bf16 a[4:7],a[140:143],v[92:95],0
buffer_load_dwordx4 v10,s[72:75],s80 offen offset:3072 lds
v_mfma_f32_16x16x32_bf16 a[4:7],a[136:139],v[88:91],a[4:7]
ds_read_b64_tr_b16 a[162:163],v1 offset:5312
ds_read_b64_tr_b16 a[164:165],v1 offset:10432
v_mfma_f32_16x16x32_bf16 a[4:7],a[132:135],v[84:87],a[4:7]
ds_read_b64_tr_b16 a[166:167],v1 offset:15552
v_accvgpr_read_b32 v216,a80
v_mfma_f32_16x16x32_bf16 a[4:7],a[128:131],v[80:83],a[4:7]
ds_read_b64_tr_b16 a[168:169],v1 offset:20672
v_accvgpr_read_b32 v217,a81
v_mfma_f32_16x16x32_bf16 a[8:11],a[128:131],v[96:99],0
ds_read_b64_tr_b16 a[170:171],v1 offset:25792
v_fma_f32 v216,v20,v216,v21
v_mfma_f32_16x16x32_bf16 a[8:11],a[132:135],v[100:103],a[8:11]
.p2align 3
v_accvgpr_read_b32 v204,a72
v_accvgpr_read_b32 v205,a73
v_accvgpr_read_b32 v206,a74
v_accvgpr_read_b32 v207,a75
v_accvgpr_read_b32 v12,a76
v_accvgpr_read_b32 v13,a77
v_accvgpr_read_b32 v14,a78
v_accvgpr_read_b32 v15,a79
v_pk_fma_f32 v[204:205],v[20:21],v[204:205],v[20:21] op_sel:[0,0,1] op_sel_hi:[0,1,1]
v_pk_fma_f32 v[206:207],v[20:21],v[206:207],v[20:21] op_sel:[0,0,1] op_sel_hi:[0,1,1]
v_pk_fma_f32 v[12:13],v[20:21],v[12:13],v[20:21] op_sel:[0,0,1] op_sel_hi:[0,1,1]
v_pk_fma_f32 v[14:15],v[20:21],v[14:15],v[20:21] op_sel:[0,0,1] op_sel_hi:[0,1,1]
v_cvt_pk_bf16_f32 v204,v204,v205
v_cvt_pk_bf16_f32 v205,v206,v207
v_cvt_pk_bf16_f32 v206,v12,v13
v_cvt_pk_bf16_f32 v207,v14,v15
v_mfma_f32_16x16x32_bf16 a[8:11],a[136:139],v[104:107],a[8:11]
ds_read_b64_tr_b16 a[172:173],v1 offset:30912
ds_read_b64_tr_b16 a[174:175],v1 offset:36032
v_mfma_f32_16x16x32_bf16 a[8:11],a[140:143],v[108:111],a[8:11]
s_waitcnt vmcnt(5)
v_cmp_ne_u32 s[90:91],s68,v4
v_and_b32 v4,-2,v5
v_cmp_ne_u32 s[92:93],s69,v4
s_nop 0
s_or_b64 s[90:91],s[90:91],s[92:93]
s_cbranch_scc1 L_fr8_%=
L_fo8_%=:
v_mfma_f32_16x16x32_bf16 a[12:15],a[140:143],v[124:127],0
s_nop 0
v_readlane_b32 s76,v5,0
v_readlane_b32 s77,v5,1
v_readlane_b32 s78,v5,2
v_mfma_f32_16x16x32_bf16 a[12:15],a[136:139],v[120:123],a[12:15]
s_addk_i32 m0,4096
s_and_b32 s77,s77,1
s_lshl_b32 s77,s77,2
v_mfma_f32_16x16x32_bf16 a[12:15],a[132:135],v[116:119],a[12:15]
s_nop 1
s_and_b32 s54,s76,1
v_mfma_f32_16x16x32_bf16 a[12:15],a[128:131],v[112:115],a[12:15]
s_and_b32 s78,s78,1
s_or_b32 s54,s54,s77
s_lshl_b32 s78,s78,4
s_waitcnt lgkmcnt(3)
v_mfma_f32_16x16x32_bf16 a[16:19],a[144:147],v[64:67],0
s_or_b32 s54,s54,s78
buffer_load_dwordx4 v11,s[72:75],s80 offen lds
v_mfma_f32_16x16x32_bf16 a[16:19],a[148:151],v[68:71],a[16:19]
s_mulk_i32 s54,3084
s_lshl_b32 s76,s71,1
s_lshl_b32 s54,s54,s76
v_mfma_f32_16x16x32_bf16 a[16:19],a[152:155],v[72:75],a[16:19]
s_lshr_b32 s54,s54,8
s_cmp_ge_u32 0,s71
s_cselect_b32 s76,2,0
v_mfma_f32_16x16x32_bf16 a[16:19],a[156:159],v[76:79],a[16:19]
s_bitcmp1_b32 s54,s76
s_cselect_b32 s77,8192,0
s_lshl_b32 s76,s76,17
v_mfma_f32_16x16x32_bf16 a[20:23],a[156:159],v[92:95],0
s_add_u32 s76,s76,s77
buffer_store_dwordx4 v[172:175],v6,s[36:39],s85 offen offset:1024 sc1
v_mfma_f32_16x16x32_bf16 a[20:23],a[152:155],v[88:91],a[20:23]
s_add_u32 s77,s76,4096
s_add_u32 s78,s76,16384
s_add_u32 s79,s76,20480
v_mfma_f32_16x16x32_bf16 a[20:23],a[148:151],v[84:87],a[20:23]
s_max_i32 s76,s76,s18
v_fma_f32 v217,v20,v217,v21
v_accvgpr_read_b32 v218,a82
v_mfma_f32_16x16x32_bf16 a[20:23],a[144:147],v[80:83],a[20:23]
s_max_i32 s77,s77,s19
v_accvgpr_read_b32 v219,a83
v_cvt_pk_bf16_f32 v216,v216,v217
v_mfma_f32_16x16x32_bf16 a[24:27],a[144:147],v[96:99],0
s_max_i32 s78,s78,s100
v_fma_f32 v218,v20,v218,v21
v_fma_f32 v219,v20,v219,v21
v_mfma_f32_16x16x32_bf16 a[24:27],a[148:151],v[100:103],a[24:27]
s_addk_i32 m0,1024
v_cvt_pk_bf16_f32 v217,v218,v219
v_accvgpr_read_b32 v218,a84
v_mfma_f32_16x16x32_bf16 a[24:27],a[152:155],v[104:107],a[24:27]
s_max_i32 s79,s79,s101
v_accvgpr_read_b32 v219,a85
v_accvgpr_read_b32 v12,a86
v_mfma_f32_16x16x32_bf16 a[24:27],a[156:159],v[108:111],a[24:27]
v_accvgpr_read_b32 v13,a87
v_fma_f32 v218,v20,v218,v21
v_mfma_f32_16x16x32_bf16 a[28:31],a[156:159],v[124:127],0
buffer_load_dwordx4 v7,s[72:75],s83 offen lds
v_mfma_f32_16x16x32_bf16 a[28:31],a[152:155],v[120:123],a[28:31]
v_fma_f32 v219,v20,v219,v21
v_fma_f32 v12,v20,v12,v21
v_mfma_f32_16x16x32_bf16 a[28:31],a[148:151],v[116:119],a[28:31]
v_fma_f32 v13,v20,v13,v21
v_cvt_pk_bf16_f32 v218,v218,v219
v_mfma_f32_16x16x32_bf16 a[28:31],a[144:147],v[112:115],a[28:31]
ds_read_b64_tr_b16 a[176:177],v2 offset:224
v_cvt_pk_bf16_f32 v219,v12,v13
v_mfma_f32_16x16x32_bf16 a[32:35],a[160:163],v[64:67],0
buffer_load_dwordx4 v[128:131],v6,s[28:31],s76 offen sc1
v_mfma_f32_16x16x32_bf16 a[32:35],a[164:167],v[68:71],a[32:35]
s_waitcnt lgkmcnt(1)
ds_read_b64_tr_b16 a[178:179],v2 offset:5344
ds_read_b64_tr_b16 a[180:181],v2 offset:10464
v_mfma_f32_16x16x32_bf16 a[32:35],a[168:171],v[72:75],a[32:35]
ds_read_b64_tr_b16 a[182:183],v2 offset:15584
v_accvgpr_read_b32 v220,a88
v_mfma_f32_16x16x32_bf16 a[32:35],a[172:175],v[76:79],a[32:35]
ds_read_b64_tr_b16 a[184:185],v2 offset:20704
v_accvgpr_read_b32 v221,a89
v_mfma_f32_16x16x32_bf16 a[36:39],a[172:175],v[92:95],0
ds_read_b64_tr_b16 a[186:187],v2 offset:25824
v_fma_f32 v220,v20,v220,v21
v_mfma_f32_16x16x32_bf16 a[36:39],a[168:171],v[88:91],a[36:39]
ds_read_b64_tr_b16 a[188:189],v2 offset:30944
ds_read_b64_tr_b16 a[190:191],v2 offset:36064
v_mfma_f32_16x16x32_bf16 a[36:39],a[164:167],v[84:87],a[36:39]
buffer_store_dwordx4 v[184:187],v6,s[36:39],s85 offen offset:2048 sc1
v_mfma_f32_16x16x32_bf16 a[36:39],a[160:163],v[80:83],a[36:39]
v_fma_f32 v221,v20,v221,v21
v_accvgpr_read_b32 v222,a90
v_mfma_f32_16x16x32_bf16 a[40:43],a[160:163],v[96:99],0
v_accvgpr_read_b32 v223,a91
v_cvt_pk_bf16_f32 v220,v220,v221
v_mfma_f32_16x16x32_bf16 a[40:43],a[164:167],v[100:103],a[40:43]
v_fma_f32 v222,v20,v222,v21
v_fma_f32 v223,v20,v223,v21
v_mfma_f32_16x16x32_bf16 a[40:43],a[168:171],v[104:107],a[40:43]
buffer_load_dwordx4 v8,s[72:75],s83 offen offset:1024 lds
v_mfma_f32_16x16x32_bf16 a[40:43],a[172:175],v[108:111],a[40:43]
v_cvt_pk_bf16_f32 v221,v222,v223
v_accvgpr_read_b32 v222,a92
v_mfma_f32_16x16x32_bf16 a[44:47],a[172:175],v[124:127],0
)A"
R"A(v_accvgpr_read_b32 v223,a93
v_accvgpr_read_b32 v14,a94
v_mfma_f32_16x16x32_bf16 a[44:47],a[168:171],v[120:123],a[44:47]
v_accvgpr_read_b32 v15,a95
v_fma_f32 v222,v20,v222,v21
v_mfma_f32_16x16x32_bf16 a[44:47],a[164:167],v[116:119],a[44:47]
buffer_load_dwordx4 v[160:163],v6,s[28:31],s76 offen offset:1024 sc1
v_mfma_f32_16x16x32_bf16 a[44:47],a[160:163],v[112:115],a[44:47]
s_waitcnt lgkmcnt(0)
v_fma_f32 v223,v20,v223,v21
v_fma_f32 v14,v20,v14,v21
v_mfma_f32_16x16x32_bf16 a[48:51],a[176:179],v[64:67],0
v_fma_f32 v15,v20,v15,v21
v_cvt_pk_bf16_f32 v222,v222,v223
v_mfma_f32_16x16x32_bf16 a[48:51],a[180:183],v[68:71],a[48:51]
ds_read_b64_tr_b16 a[128:129],v1 offset:256
v_cvt_pk_bf16_f32 v223,v14,v15
v_mfma_f32_16x16x32_bf16 a[48:51],a[184:187],v[72:75],a[48:51]
ds_read_b64_tr_b16 a[130:131],v1 offset:5376
ds_read_b64_tr_b16 a[132:133],v1 offset:10496
v_mfma_f32_16x16x32_bf16 a[48:51],a[188:191],v[76:79],a[48:51]
buffer_load_dwordx4 v[192:195],v6,s[28:31],s76 offen offset:2048 sc1
v_mfma_f32_16x16x32_bf16 a[52:55],a[188:191],v[92:95],0
ds_read_b64_tr_b16 a[134:135],v1 offset:15616
ds_read_b64_tr_b16 a[136:137],v1 offset:20736
v_mfma_f32_16x16x32_bf16 a[52:55],a[184:187],v[88:91],a[52:55]
ds_read_b64_tr_b16 a[138:139],v1 offset:25856
v_add_f32 v60,v61,v60
v_mfma_f32_16x16x32_bf16 a[52:55],a[180:183],v[84:87],a[52:55]
ds_read_b64_tr_b16 a[140:141],v1 offset:30976
v_max_f32 v22,0x00800000,v60
v_mfma_f32_16x16x32_bf16 a[52:55],a[176:179],v[80:83],a[52:55]
buffer_load_dwordx4 v9,s[72:75],s83 offen offset:2048 lds
v_mfma_f32_16x16x32_bf16 a[56:59],a[176:179],v[96:99],0
ds_read_b64_tr_b16 a[142:143],v1 offset:36096
v_max3_f32 v19,|v168|,|v169|,|v170|
v_mfma_f32_16x16x32_bf16 a[56:59],a[180:183],v[100:103],a[56:59]
v_max3_f32 v19,v19,|v171|,s19
v_fmac_f32 v19,0x39800000,v23
v_mfma_f32_16x16x32_bf16 a[56:59],a[184:187],v[104:107],a[56:59]
v_rcp_f32 v22,v22
v_mfma_f32_16x16x32_bf16 a[56:59],a[188:191],v[108:111],a[56:59]
buffer_store_dwordx4 v[188:191],v6,s[36:39],s85 offen offset:3072 sc1
v_mfma_f32_16x16x32_bf16 a[60:63],a[188:191],v[124:127],0
ds_read_b64_tr_b16 a[144:145],v2 offset:288
v_mul_f32 v23,0xbe22f983,v22
v_mfma_f32_16x16x32_bf16 a[60:63],a[184:187],v[120:123],a[60:63]
.p2align 3
v_accvgpr_read_b32 v232,a96
v_accvgpr_read_b32 v233,a97
v_accvgpr_read_b32 v234,a98
v_accvgpr_read_b32 v235,a99
v_accvgpr_read_b32 v12,a100
v_accvgpr_read_b32 v13,a101
v_accvgpr_read_b32 v14,a102
v_accvgpr_read_b32 v15,a103
v_pk_fma_f32 v[232:233],v[22:23],v[232:233],v[22:23] op_sel:[0,0,1] op_sel_hi:[0,1,1]
v_pk_fma_f32 v[234:235],v[22:23],v[234:235],v[22:23] op_sel:[0,0,1] op_sel_hi:[0,1,1]
v_pk_fma_f32 v[12:13],v[22:23],v[12:13],v[22:23] op_sel:[0,0,1] op_sel_hi:[0,1,1]
v_pk_fma_f32 v[14:15],v[22:23],v[14:15],v[22:23] op_sel:[0,0,1] op_sel_hi:[0,1,1]
v_cvt_pk_bf16_f32 v232,v232,v233
v_cvt_pk_bf16_f32 v233,v234,v235
v_cvt_pk_bf16_f32 v234,v12,v13
v_cvt_pk_bf16_f32 v235,v14,v15
v_mfma_f32_16x16x32_bf16 a[60:63],a[180:183],v[116:119],a[60:63]
ds_read_b64_tr_b16 a[146:147],v2 offset:5408
ds_read_b64_tr_b16 a[148:149],v2 offset:10528
v_mfma_f32_16x16x32_bf16 a[60:63],a[176:179],v[112:115],a[60:63]
s_waitcnt lgkmcnt(4)
buffer_load_dwordx4 v[224:227],v6,s[28:31],s76 offen offset:3072 sc1
v_mfma_f32_16x16x32_bf16 a[64:67],a[128:131],v[64:67],0
ds_read_b64_tr_b16 a[150:151],v2 offset:15648
ds_read_b64_tr_b16 a[152:153],v2 offset:20768
v_mfma_f32_16x16x32_bf16 a[64:67],a[132:135],v[68:71],a[64:67]
ds_read_b64_tr_b16 a[154:155],v2 offset:25888
v_accvgpr_read_b32 v248,a112
v_mfma_f32_16x16x32_bf16 a[64:67],a[136:139],v[72:75],a[64:67]
.p2align 3
v_accvgpr_read_b32 v236,a104
v_accvgpr_read_b32 v237,a105
v_accvgpr_read_b32 v238,a106
v_accvgpr_read_b32 v239,a107
v_accvgpr_read_b32 v12,a108
v_accvgpr_read_b32 v13,a109
v_accvgpr_read_b32 v14,a110
v_accvgpr_read_b32 v15,a111
v_pk_fma_f32 v[236:237],v[22:23],v[236:237],v[22:23] op_sel:[0,0,1] op_sel_hi:[0,1,1]
v_pk_fma_f32 v[238:239],v[22:23],v[238:239],v[22:23] op_sel:[0,0,1] op_sel_hi:[0,1,1]
v_pk_fma_f32 v[12:13],v[22:23],v[12:13],v[22:23] op_sel:[0,0,1] op_sel_hi:[0,1,1]
v_pk_fma_f32 v[14:15],v[22:23],v[14:15],v[22:23] op_sel:[0,0,1] op_sel_hi:[0,1,1]
v_cvt_pk_bf16_f32 v236,v236,v237
v_cvt_pk_bf16_f32 v237,v238,v239
v_cvt_pk_bf16_f32 v238,v12,v13
v_cvt_pk_bf16_f32 v239,v14,v15
s_waitcnt lgkmcnt(6)
v_mfma_f32_16x16x32_bf16 a[64:67],a[140:143],v[76:79],a[64:67]
buffer_load_dwordx4 v10,s[72:75],s83 offen offset:3072 lds
v_mfma_f32_16x16x32_bf16 a[68:71],a[140:143],v[92:95],0
ds_read_b64_tr_b16 a[156:157],v2 offset:31008
ds_read_b64_tr_b16 a[158:159],v2 offset:36128
v_mfma_f32_16x16x32_bf16 a[68:71],a[136:139],v[88:91],a[68:71]
v_accvgpr_read_b32 v249,a113
v_fma_f32 v248,v22,v248,v23
v_mfma_f32_16x16x32_bf16 a[68:71],a[132:135],v[84:87],a[68:71]
v_fma_f32 v249,v22,v249,v23
v_accvgpr_read_b32 v250,a114
v_mfma_f32_16x16x32_bf16 a[68:71],a[128:131],v[80:83],a[68:71]
buffer_load_dwordx4 v[132:135],v6,s[28:31],s77 offen sc1
v_mfma_f32_16x16x32_bf16 a[72:75],a[128:131],v[96:99],0
v_accvgpr_read_b32 v251,a115
v_cvt_pk_bf16_f32 v248,v248,v249
v_mfma_f32_16x16x32_bf16 a[72:75],a[132:135],v[100:103],a[72:75]
s_addk_i32 m0,4096
v_fma_f32 v250,v22,v250,v23
v_fma_f32 v251,v22,v251,v23
v_mfma_f32_16x16x32_bf16 a[72:75],a[136:139],v[104:107],a[72:75]
v_cvt_pk_bf16_f32 v249,v250,v251
v_accvgpr_read_b32 v250,a116
v_mfma_f32_16x16x32_bf16 a[72:75],a[140:143],v[108:111],a[72:75]
v_accvgpr_read_b32 v251,a117
v_accvgpr_read_b32 v16,a118
v_mfma_f32_16x16x32_bf16 a[76:79],a[140:143],v[124:127],0
buffer_load_dwordx4 v11,s[72:75],s83 offen lds
v_mfma_f32_16x16x32_bf16 a[76:79],a[136:139],v[120:123],a[76:79]
v_accvgpr_read_b32 v17,a119
v_fma_f32 v250,v22,v250,v23
v_mfma_f32_16x16x32_bf16 a[76:79],a[132:135],v[116:119],a[76:79]
v_fma_f32 v251,v22,v251,v23
v_fma_f32 v16,v22,v16,v23
v_mfma_f32_16x16x32_bf16 a[76:79],a[128:131],v[112:115],a[76:79]
s_waitcnt lgkmcnt(0)
v_fma_f32 v17,v22,v17,v23
v_cvt_pk_bf16_f32 v250,v250,v251
v_mfma_f32_16x16x32_bf16 a[80:83],a[144:147],v[64:67],0
v_cvt_pk_bf16_f32 v251,v16,v17
v_subrev_u32 v1,81920,v1
v_mfma_f32_16x16x32_bf16 a[80:83],a[148:151],v[68:71],a[80:83]
buffer_load_dwordx4 v[164:167],v6,s[28:31],s77 offen offset:1024 sc1
v_mfma_f32_16x16x32_bf16 a[80:83],a[152:155],v[72:75],a[80:83]
ds_read_b64_tr_b16 a[160:161],v1
ds_read_b64_tr_b16 a[162:163],v1 offset:5120
v_mfma_f32_16x16x32_bf16 a[80:83],a[156:159],v[76:79],a[80:83]
ds_read_b64_tr_b16 a[164:165],v1 offset:10240
v_min_f32 v18,v18,v19
v_mfma_f32_16x16x32_bf16 a[84:87],a[156:159],v[92:95],0
ds_read_b64_tr_b16 a[166:167],v1 offset:15360
v_max3_f32 v19,|v200|,|v201|,|v202|
v_mfma_f32_16x16x32_bf16 a[84:87],a[152:155],v[88:91],a[84:87]
ds_read_b64_tr_b16 a[168:169],v1 offset:20480
v_cmp_nlt_f32 s[56:57],v48,s63
v_mfma_f32_16x16x32_bf16 a[84:87],a[148:151],v[84:87],a[84:87]
ds_read_b64_tr_b16 a[170:171],v1 offset:25600
v_max3_f32 v19,v19,|v203|,s100
v_mfma_f32_16x16x32_bf16 a[84:87],a[144:147],v[80:83],a[84:87]
ds_read_b64_tr_b16 a[172:173],v1 offset:30720
ds_read_b64_tr_b16 a[174:175],v1 offset:35840
v_mfma_f32_16x16x32_bf16 a[88:91],a[144:147],v[96:99],0
v_fmac_f32 v19,0x39800000,v21
v_accvgpr_read_b32 v252,a120
v_mfma_f32_16x16x32_bf16 a[88:91],a[148:151],v[100:103],a[88:91]
v_accvgpr_read_b32 v253,a121
v_min_f32 v18,v18,v19
v_mfma_f32_16x16x32_bf16 a[88:91],a[152:155],v[104:107],a[88:91]
v_cmp_nlt_f32 s[58:59],v52,s63
s_nop 1
s_or_b64 s[56:57],s[56:57],s[58:59]
v_mfma_f32_16x16x32_bf16 a[88:91],a[156:159],v[108:111],a[88:91]
.p2align 3
v_fma_f32 v252,v22,v252,v23
v_fma_f32 v253,v22,v253,v23
v_accvgpr_read_b32 v254,a122
v_accvgpr_read_b32 v255,a123
v_mfma_f32_16x16x32_bf16 a[92:95],a[156:159],v[124:127],0
v_max3_f32 v19,|v232|,|v233|,|v234|
.p2align 3
v_cvt_pk_bf16_f32 v252,v252,v253
v_fma_f32 v254,v22,v254,v23
v_fma_f32 v255,v22,v255,v23
v_cmp_nlt_f32 s[58:59],v56,s63
s_nop 1
s_or_b64 s[56:57],s[56:57],s[58:59]
v_max3_f32 v19,v19,|v235|,s101
v_cvt_pk_bf16_f32 v253,v254,v255
v_mfma_f32_16x16x32_bf16 a[92:95],a[152:155],v[120:123],a[92:95]
.p2align 3
v_fmac_f32 v19,0x39800000,v23
v_accvgpr_read_b32 v254,a124
v_accvgpr_read_b32 v255,a125
v_cmp_nlt_f32 s[58:59],v60,s63
s_nop 1
s_or_b64 s[56:57],s[56:57],s[58:59]
v_min_f32 v18,v18,v19
.p2align 3
v_accvgpr_read_b32 v12,a126
v_accvgpr_read_b32 v13,a127
v_fma_f32 v254,v22,v254,v23
v_fma_f32 v255,v22,v255,v23
v_mfma_f32_16x16x32_bf16 a[92:95],a[148:151],v[116:119],a[92:95]
buffer_load_dwordx4 v[196:199],v6,s[28:31],s77 offen offset:2048 sc1
.p2align 3
v_fma_f32 v12,v22,v12,v23
v_fma_f32 v13,v22,v13,v23
v_cvt_pk_bf16_f32 v254,v254,v255
v_cmp_gt_f32 s[58:59],0,v18
s_nop 1
s_or_b64 s[56:57],s[56:57],s[58:59]
v_cvt_pk_bf16_f32 v255,v12,v13
v_mfma_f32_16x16x32_bf16 a[92:95],a[144:147],v[112:115],a[92:95]
s_cmp_lg_u64 s[56:57],0
s_cbranch_scc1 L_slow1_%=
L_sret1_%=:
v_subrev_u32 v2,81920,v2
s_waitcnt lgkmcnt(0)
v_mfma_f32_16x16x32_bf16 a[192:195],a[160:163],v[136:139],a[192:195]
ds_read_b64_tr_b16 a[176:177],v2 offset:32
ds_read_b64_tr_b16 a[178:179],v2 offset:5152
v_mfma_f32_16x16x32_bf16 a[192:195],a[164:167],v[140:143],a[192:195]
s_cmp_ge_u32 2,s71
s_cselect_b32 s80,6,4
ds_read_b64_tr_b16 a[180:181],v2 offset:10272
v_mfma_f32_16x16x32_bf16 a[192:195],a[168:171],v[152:155],a[192:195]
s_mul_i32 s80,s80,163840
buffer_store_dwordx4 v[200:203],v6,s[36:39],s86 offen sc1
v_mfma_f32_16x16x32_bf16 a[192:195],a[172:175],v[156:159],a[192:195]
s_add_u32 s80,s80,s81
ds_read_b64_tr_b16 a[182:183],v2 offset:15392
ds_read_b64_tr_b16 a[184:185],v2 offset:20512
v_mfma_f32_16x16x32_bf16 a[196:199],a[172:175],v[188:191],a[196:199]
s_add_u32 s83,s80,20480
ds_read_b64_tr_b16 a[186:187],v2 offset:25632
v_mfma_f32_16x16x32_bf16 a[196:199],a[168:171],v[184:187],a[196:199]
ds_read_b64_tr_b16 a[188:189],v2 offset:30752
v_mfma_f32_16x16x32_bf16 a[196:199],a[164:167],v[172:175],a[196:199]
buffer_store_dwordx4 v[204:207],v6,s[36:39],s86 offen offset:1024 sc1
v_mfma_f32_16x16x32_bf16 a[196:199],a[160:163],v[168:171],a[196:199]
ds_read_b64_tr_b16 a[190:191],v2 offset:35872
ds_read_b64_tr_b16 a[128:129],v1 offset:64
v_mfma_f32_16x16x32_bf16 a[200:203],a[160:163],v[200:203],a[200:203]
ds_read_b64_tr_b16 a[130:131],v1 offset:5184
v_mfma_f32_16x16x32_bf16 a[200:203],a[164:167],v[204:207],a[200:203]
ds_read_b64_tr_b16 a[132:133],v1 offset:10304
v_mfma_f32_16x16x32_bf16 a[200:203],a[168:171],v[216:219],a[200:203]
buffer_load_dwordx4 v[228:231],v6,s[28:31],s77 offen offset:3072 sc1
v_mfma_f32_16x16x32_bf16 a[200:203],a[172:175],v[220:223],a[200:203]
ds_read_b64_tr_b16 a[134:135],v1 offset:15424
ds_read_b64_tr_b16 a[136:137],v1 offset:20544
v_mfma_f32_16x16x32_bf16 a[204:207],a[172:175],v[252:255],a[204:207]
ds_read_b64_tr_b16 a[138:139],v1 offset:25664
v_mfma_f32_16x16x32_bf16 a[204:207],a[168:171],v[248:251],a[204:207]
ds_read_b64_tr_b16 a[140:141],v1 offset:30784
v_mfma_f32_16x16x32_bf16 a[204:207],a[164:167],v[236:239],a[204:207]
ds_read_b64_tr_b16 a[142:143],v1 offset:35904
v_mfma_f32_16x16x32_bf16 a[204:207],a[160:163],v[232:235],a[204:207]
s_barrier
buffer_load_dwordx4 v[144:147],v6,s[28:31],s78 offen sc1
s_waitcnt lgkmcnt(7)
v_mfma_f32_16x16x32_bf16 a[208:211],a[176:179],v[136:139],a[208:211]
ds_read_b64_tr_b16 a[144:145],v2 offset:96
ds_read_b64_tr_b16 a[146:147],v2 offset:5216
v_mfma_f32_16x16x32_bf16 a[208:211],a[180:183],v[140:143],a[208:211]
ds_read_b64_tr_b16 a[148:149],v2 offset:10336
v_mfma_f32_16x16x32_bf16 a[208:211],a[184:187],v[152:155],a[208:211]
s_add_u32 m0,s82,81920
ds_read_b64_tr_b16 a[150:151],v2 offset:15456
v_mfma_f32_16x16x32_bf16 a[208:211],a[188:191],v[156:159],a[208:211]
ds_read_b64_tr_b16 a[152:153],v2 offset:20576
v_mfma_f32_16x16x32_bf16 a[212:215],a[188:191],v[188:191],a[212:215]
ds_read_b64_tr_b16 a[154:155],v2 offset:25696
ds_read_b64_tr_b16 a[156:157],v2 offset:30816
v_mfma_f32_16x16x32_bf16 a[212:215],a[184:187],v[184:187],a[212:215]
buffer_store_dwordx4 v[216:219],v6,s[36:39],s86 offen offset:2048 sc1
v_mfma_f32_16x16x32_bf16 a[212:215],a[180:183],v[172:175],a[212:215]
ds_read_b64_tr_b16 a[158:159],v2 offset:35936
ds_read_b64_tr_b16 a[160:161],v1 offset:128
v_mfma_f32_16x16x32_bf16 a[212:215],a[176:179],v[168:171],a[212:215]
)A"
R"A(ds_read_b64_tr_b16 a[162:163],v1 offset:5248
v_mfma_f32_16x16x32_bf16 a[216:219],a[176:179],v[200:203],a[216:219]
ds_read_b64_tr_b16 a[164:165],v1 offset:10368
v_mfma_f32_16x16x32_bf16 a[216:219],a[180:183],v[204:207],a[216:219]
buffer_load_dwordx4 v7,s[72:75],s80 offen lds
v_mfma_f32_16x16x32_bf16 a[216:219],a[184:187],v[216:219],a[216:219]
ds_read_b64_tr_b16 a[166:167],v1 offset:15488
ds_read_b64_tr_b16 a[168:169],v1 offset:20608
v_mfma_f32_16x16x32_bf16 a[216:219],a[188:191],v[220:223],a[216:219]
ds_read_b64_tr_b16 a[170:171],v1 offset:25728
v_mfma_f32_16x16x32_bf16 a[220:223],a[188:191],v[252:255],a[220:223]
ds_read_b64_tr_b16 a[172:173],v1 offset:30848
v_mfma_f32_16x16x32_bf16 a[220:223],a[184:187],v[248:251],a[220:223]
buffer_load_dwordx4 v[176:179],v6,s[28:31],s78 offen offset:1024 sc1
v_mfma_f32_16x16x32_bf16 a[220:223],a[180:183],v[236:239],a[220:223]
ds_read_b64_tr_b16 a[174:175],v1 offset:35968
ds_read_b64_tr_b16 a[180:181],v2 offset:10400
v_mfma_f32_16x16x32_bf16 a[220:223],a[176:179],v[232:235],a[220:223]
s_waitcnt lgkmcnt(7)
ds_read_b64_tr_b16 a[176:177],v2 offset:160
v_mfma_f32_16x16x32_bf16 a[224:227],a[128:131],v[136:139],a[224:227]
ds_read_b64_tr_b16 a[178:179],v2 offset:5280
v_mfma_f32_16x16x32_bf16 a[224:227],a[132:135],v[140:143],a[224:227]
ds_read_b64_tr_b16 a[182:183],v2 offset:15520
v_mfma_f32_16x16x32_bf16 a[224:227],a[136:139],v[152:155],a[224:227]
buffer_load_dwordx4 v[208:211],v6,s[28:31],s78 offen offset:2048 sc1
v_mfma_f32_16x16x32_bf16 a[224:227],a[140:143],v[156:159],a[224:227]
ds_read_b64_tr_b16 a[184:185],v2 offset:20640
ds_read_b64_tr_b16 a[186:187],v2 offset:25760
v_mfma_f32_16x16x32_bf16 a[228:231],a[140:143],v[188:191],a[228:231]
ds_read_b64_tr_b16 a[188:189],v2 offset:30880
v_mfma_f32_16x16x32_bf16 a[228:231],a[136:139],v[184:187],a[228:231]
ds_read_b64_tr_b16 a[190:191],v2 offset:36000
v_mfma_f32_16x16x32_bf16 a[228:231],a[132:135],v[172:175],a[228:231]
buffer_load_dwordx4 v8,s[72:75],s80 offen offset:1024 lds
v_mfma_f32_16x16x32_bf16 a[228:231],a[128:131],v[168:171],a[228:231]
v_mfma_f32_16x16x32_bf16 a[232:235],a[128:131],v[200:203],a[232:235]
v_mfma_f32_16x16x32_bf16 a[232:235],a[132:135],v[204:207],a[232:235]
v_mfma_f32_16x16x32_bf16 a[232:235],a[136:139],v[216:219],a[232:235]
buffer_store_dwordx4 v[220:223],v6,s[36:39],s86 offen offset:3072 sc1
v_mfma_f32_16x16x32_bf16 a[232:235],a[140:143],v[220:223],a[232:235]
v_mfma_f32_16x16x32_bf16 a[236:239],a[140:143],v[252:255],a[236:239]
ds_read_b64_tr_b16 a[140:141],v1 offset:30912
ds_read_b64_tr_b16 a[142:143],v1 offset:36032
v_mfma_f32_16x16x32_bf16 a[236:239],a[136:139],v[248:251],a[236:239]
ds_read_b64_tr_b16 a[136:137],v1 offset:20672
v_mfma_f32_16x16x32_bf16 a[236:239],a[132:135],v[236:239],a[236:239]
buffer_load_dwordx4 v[240:243],v6,s[28:31],s78 offen offset:3072 sc1
v_mfma_f32_16x16x32_bf16 a[236:239],a[128:131],v[232:235],a[236:239]
ds_read_b64_tr_b16 a[128:129],v1 offset:192
ds_read_b64_tr_b16 a[130:131],v1 offset:5312
v_mfma_f32_16x16x32_bf16 a[240:243],a[144:147],v[136:139],a[240:243]
ds_read_b64_tr_b16 a[132:133],v1 offset:10432
v_mfma_f32_16x16x32_bf16 a[240:243],a[148:151],v[140:143],a[240:243]
ds_read_b64_tr_b16 a[134:135],v1 offset:15552
v_mfma_f32_16x16x32_bf16 a[240:243],a[152:155],v[152:155],a[240:243]
ds_read_b64_tr_b16 a[138:139],v1 offset:25792
v_mfma_f32_16x16x32_bf16 a[240:243],a[156:159],v[156:159],a[240:243]
buffer_load_dwordx4 v9,s[72:75],s80 offen offset:2048 lds
v_mfma_f32_16x16x32_bf16 a[244:247],a[156:159],v[188:191],a[244:247]
v_mfma_f32_16x16x32_bf16 a[244:247],a[152:155],v[184:187],a[244:247]
v_mfma_f32_16x16x32_bf16 a[244:247],a[148:151],v[172:175],a[244:247]
v_mfma_f32_16x16x32_bf16 a[244:247],a[144:147],v[168:171],a[244:247]
buffer_load_dwordx4 v[148:151],v6,s[28:31],s79 offen sc1
v_mfma_f32_16x16x32_bf16 a[248:251],a[144:147],v[200:203],a[248:251]
v_mfma_f32_16x16x32_bf16 a[248:251],a[148:151],v[204:207],a[248:251]
v_mfma_f32_16x16x32_bf16 a[248:251],a[152:155],v[216:219],a[248:251]
v_mfma_f32_16x16x32_bf16 a[248:251],a[156:159],v[220:223],a[248:251]
v_mfma_f32_16x16x32_bf16 a[252:255],a[156:159],v[252:255],a[252:255]
ds_read_b64_tr_b16 a[156:157],v2 offset:30944
ds_read_b64_tr_b16 a[158:159],v2 offset:36064
v_mfma_f32_16x16x32_bf16 a[252:255],a[152:155],v[248:251],a[252:255]
buffer_store_dwordx4 v[232:235],v6,s[36:39],s87 offen sc1
v_mfma_f32_16x16x32_bf16 a[252:255],a[148:151],v[236:239],a[252:255]
ds_read_b64_tr_b16 a[148:149],v2 offset:10464
ds_read_b64_tr_b16 a[150:151],v2 offset:15584
v_mfma_f32_16x16x32_bf16 a[252:255],a[144:147],v[232:235],a[252:255]
ds_read_b64_tr_b16 a[144:145],v2 offset:224
v_mfma_f32_16x16x32_bf16 a[0:3],a[160:163],v[136:139],a[0:3]
s_waitcnt lgkmcnt(5)
ds_read_b64_tr_b16 a[146:147],v2 offset:5344
v_mfma_f32_16x16x32_bf16 a[0:3],a[164:167],v[140:143],a[0:3]
buffer_load_dwordx4 v[180:183],v6,s[28:31],s79 offen offset:1024 sc1
v_mfma_f32_16x16x32_bf16 a[0:3],a[168:171],v[152:155],a[0:3]
ds_read_b64_tr_b16 a[152:153],v2 offset:20704
ds_read_b64_tr_b16 a[154:155],v2 offset:25824
v_mfma_f32_16x16x32_bf16 a[0:3],a[172:175],v[156:159],a[0:3]
v_mfma_f32_16x16x32_bf16 a[4:7],a[172:175],v[188:191],a[4:7]
v_mfma_f32_16x16x32_bf16 a[4:7],a[168:171],v[184:187],a[4:7]
buffer_load_dwordx4 v10,s[72:75],s80 offen offset:3072 lds
v_mfma_f32_16x16x32_bf16 a[4:7],a[164:167],v[172:175],a[4:7]
v_mfma_f32_16x16x32_bf16 a[4:7],a[160:163],v[168:171],a[4:7]
v_mfma_f32_16x16x32_bf16 a[8:11],a[160:163],v[200:203],a[8:11]
v_mfma_f32_16x16x32_bf16 a[8:11],a[164:167],v[204:207],a[8:11]
v_mfma_f32_16x16x32_bf16 a[8:11],a[168:171],v[216:219],a[8:11]
buffer_load_dwordx4 v[212:215],v6,s[28:31],s79 offen offset:2048 sc1
v_mfma_f32_16x16x32_bf16 a[8:11],a[172:175],v[220:223],a[8:11]
v_mfma_f32_16x16x32_bf16 a[12:15],a[172:175],v[252:255],a[12:15]
ds_read_b64_tr_b16 a[172:173],v1 offset:30976
ds_read_b64_tr_b16 a[174:175],v1 offset:36096
v_mfma_f32_16x16x32_bf16 a[12:15],a[168:171],v[248:251],a[12:15]
ds_read_b64_tr_b16 a[168:169],v1 offset:20736
v_mfma_f32_16x16x32_bf16 a[12:15],a[164:167],v[236:239],a[12:15]
s_addk_i32 m0,4096
ds_read_b64_tr_b16 a[164:165],v1 offset:10496
v_mfma_f32_16x16x32_bf16 a[12:15],a[160:163],v[232:235],a[12:15]
ds_read_b64_tr_b16 a[160:161],v1 offset:256
v_mfma_f32_16x16x32_bf16 a[16:19],a[176:179],v[136:139],a[16:19]
ds_read_b64_tr_b16 a[162:163],v1 offset:5376
ds_read_b64_tr_b16 a[166:167],v1 offset:15616
v_mfma_f32_16x16x32_bf16 a[16:19],a[180:183],v[140:143],a[16:19]
buffer_store_dwordx4 v[236:239],v6,s[36:39],s87 offen offset:1024 sc1
v_mfma_f32_16x16x32_bf16 a[16:19],a[184:187],v[152:155],a[16:19]
ds_read_b64_tr_b16 a[170:171],v1 offset:25856
v_add_u32 v1,40960,v1
v_mfma_f32_16x16x32_bf16 a[16:19],a[188:191],v[156:159],a[16:19]
ds_read_b64_tr_b16 a[96:97],v1 offset:128
ds_read_b64_tr_b16 a[98:99],v1 offset:5248
v_mfma_f32_16x16x32_bf16 a[20:23],a[188:191],v[188:191],a[20:23]
ds_read_b64_tr_b16 a[100:101],v1 offset:10368
v_mfma_f32_16x16x32_bf16 a[20:23],a[184:187],v[184:187],a[20:23]
buffer_load_dwordx4 v11,s[72:75],s80 offen lds
v_mfma_f32_16x16x32_bf16 a[20:23],a[180:183],v[172:175],a[20:23]
ds_read_b64_tr_b16 a[102:103],v1 offset:15488
ds_read_b64_tr_b16 a[104:105],v1 offset:20608
v_mfma_f32_16x16x32_bf16 a[20:23],a[176:179],v[168:171],a[20:23]
ds_read_b64_tr_b16 a[106:107],v1 offset:25728
v_mfma_f32_16x16x32_bf16 a[24:27],a[176:179],v[200:203],a[24:27]
ds_read_b64_tr_b16 a[108:109],v1 offset:30848
v_mfma_f32_16x16x32_bf16 a[24:27],a[180:183],v[204:207],a[24:27]
buffer_load_dwordx4 v[244:247],v6,s[28:31],s79 offen offset:3072 sc1
v_mfma_f32_16x16x32_bf16 a[24:27],a[184:187],v[216:219],a[24:27]
ds_read_b64_tr_b16 a[110:111],v1 offset:35968
v_mfma_f32_16x16x32_bf16 a[24:27],a[188:191],v[220:223],a[24:27]
v_mfma_f32_16x16x32_bf16 a[28:31],a[188:191],v[252:255],a[28:31]
ds_read_b64_tr_b16 a[188:189],v2 offset:31008
ds_read_b64_tr_b16 a[190:191],v2 offset:36128
v_mfma_f32_16x16x32_bf16 a[28:31],a[184:187],v[248:251],a[28:31]
s_addk_i32 m0,1024
ds_read_b64_tr_b16 a[184:185],v2 offset:20768
v_mfma_f32_16x16x32_bf16 a[28:31],a[180:183],v[236:239],a[28:31]
ds_read_b64_tr_b16 a[180:181],v2 offset:10528
v_mfma_f32_16x16x32_bf16 a[28:31],a[176:179],v[232:235],a[28:31]
ds_read_b64_tr_b16 a[176:177],v2 offset:288
v_mfma_f32_16x16x32_bf16 a[32:35],a[128:131],v[136:139],a[32:35]
buffer_load_dwordx4 v7,s[72:75],s83 offen lds
v_mfma_f32_16x16x32_bf16 a[32:35],a[132:135],v[140:143],a[32:35]
ds_read_b64_tr_b16 a[178:179],v2 offset:5408
ds_read_b64_tr_b16 a[182:183],v2 offset:15648
v_mfma_f32_16x16x32_bf16 a[32:35],a[136:139],v[152:155],a[32:35]
ds_read_b64_tr_b16 a[186:187],v2 offset:25888
v_add_u32 v2,40960,v2
v_mfma_f32_16x16x32_bf16 a[32:35],a[140:143],v[156:159],a[32:35]
ds_read_b64_tr_b16 a[112:113],v2 offset:160
v_mfma_f32_16x16x32_bf16 a[36:39],a[140:143],v[188:191],a[36:39]
ds_read_b64_tr_b16 a[114:115],v2 offset:5280
v_mfma_f32_16x16x32_bf16 a[36:39],a[136:139],v[184:187],a[36:39]
buffer_store_dwordx4 v[248:251],v6,s[36:39],s87 offen offset:2048 sc1
v_mfma_f32_16x16x32_bf16 a[36:39],a[132:135],v[172:175],a[36:39]
ds_read_b64_tr_b16 a[116:117],v2 offset:10400
ds_read_b64_tr_b16 a[118:119],v2 offset:15520
v_mfma_f32_16x16x32_bf16 a[36:39],a[128:131],v[168:171],a[36:39]
ds_read_b64_tr_b16 a[120:121],v2 offset:20640
v_mfma_f32_16x16x32_bf16 a[40:43],a[128:131],v[200:203],a[40:43]
ds_read_b64_tr_b16 a[122:123],v2 offset:25760
v_mfma_f32_16x16x32_bf16 a[40:43],a[132:135],v[204:207],a[40:43]
ds_read_b64_tr_b16 a[124:125],v2 offset:30880
v_mfma_f32_16x16x32_bf16 a[40:43],a[136:139],v[216:219],a[40:43]
ds_read_b64_tr_b16 a[126:127],v2 offset:36000
v_mfma_f32_16x16x32_bf16 a[40:43],a[140:143],v[220:223],a[40:43]
v_mfma_f32_16x16x32_bf16 a[44:47],a[140:143],v[252:255],a[44:47]
ds_read_b64_tr_b16 a[140:141],v1 offset:30720
ds_read_b64_tr_b16 a[142:143],v1 offset:35840
v_mfma_f32_16x16x32_bf16 a[44:47],a[136:139],v[248:251],a[44:47]
ds_read_b64_tr_b16 a[136:137],v1 offset:20480
v_mfma_f32_16x16x32_bf16 a[44:47],a[132:135],v[236:239],a[44:47]
ds_read_b64_tr_b16 a[132:133],v1 offset:10240
v_mfma_f32_16x16x32_bf16 a[44:47],a[128:131],v[232:235],a[44:47]
s_waitcnt lgkmcnt(7)
buffer_load_dwordx4 v8,s[72:75],s83 offen offset:1024 lds
v_mfma_f32_16x16x32_bf16 a[48:51],a[144:147],v[136:139],a[48:51]
ds_read_b64_tr_b16 a[128:129],v1
ds_read_b64_tr_b16 a[130:131],v1 offset:5120
v_mfma_f32_16x16x32_bf16 a[48:51],a[148:151],v[140:143],a[48:51]
ds_read_b64_tr_b16 a[134:135],v1 offset:15360
v_mfma_f32_16x16x32_bf16 a[48:51],a[152:155],v[152:155],a[48:51]
ds_read_b64_tr_b16 a[138:139],v1 offset:25600
v_mfma_f32_16x16x32_bf16 a[48:51],a[156:159],v[156:159],a[48:51]
s_cmp_ge_u32 1,s71
s_cselect_b32 s76,4,2
v_mfma_f32_16x16x32_bf16 a[52:55],a[156:159],v[188:191],a[52:55]
s_bitcmp1_b32 s54,s76
s_cselect_b32 s77,8192,0
s_lshl_b32 s76,s76,17
v_mfma_f32_16x16x32_bf16 a[52:55],a[152:155],v[184:187],a[52:55]
s_add_u32 s76,s76,s77
s_add_u32 s77,s76,4096
s_add_u32 s78,s76,16384
v_mfma_f32_16x16x32_bf16 a[52:55],a[148:151],v[172:175],a[52:55]
s_add_u32 s79,s76,20480
s_max_i32 s76,s76,s18
s_max_i32 s77,s77,s19
v_mfma_f32_16x16x32_bf16 a[52:55],a[144:147],v[168:171],a[52:55]
s_max_i32 s78,s78,s100
s_max_i32 s79,s79,s101
v_mfma_f32_16x16x32_bf16 a[56:59],a[144:147],v[200:203],a[56:59]
v_mfma_f32_16x16x32_bf16 a[56:59],a[148:151],v[204:207],a[56:59]
buffer_store_dwordx4 v[252:255],v6,s[36:39],s87 offen offset:3072 sc1
v_mfma_f32_16x16x32_bf16 a[56:59],a[152:155],v[216:219],a[56:59]
v_mfma_f32_16x16x32_bf16 a[56:59],a[156:159],v[220:223],a[56:59]
v_mfma_f32_16x16x32_bf16 a[60:63],a[156:159],v[252:255],a[60:63]
ds_read_b64_tr_b16 a[156:157],v2 offset:30752
ds_read_b64_tr_b16 a[158:159],v2 offset:35872
v_mfma_f32_16x16x32_bf16 a[60:63],a[152:155],v[248:251],a[60:63]
ds_read_b64_tr_b16 a[152:153],v2 offset:20512
v_mfma_f32_16x16x32_bf16 a[60:63],a[148:151],v[236:239],a[60:63]
buffer_load_dwordx4 v9,s[72:75],s83 offen offset:2048 lds
v_mfma_f32_16x16x32_bf16 a[60:63],a[144:147],v[232:235],a[60:63]
ds_read_b64_tr_b16 a[144:145],v2 offset:32
ds_read_b64_tr_b16 a[146:147],v2 offset:5152
)A"
R"A(v_mfma_f32_16x16x32_bf16 a[64:67],a[160:163],v[136:139],a[64:67]
ds_read_b64_tr_b16 a[148:149],v2 offset:10272
v_mfma_f32_16x16x32_bf16 a[64:67],a[164:167],v[140:143],a[64:67]
ds_read_b64_tr_b16 a[150:151],v2 offset:15392
v_mfma_f32_16x16x32_bf16 a[64:67],a[168:171],v[152:155],a[64:67]
ds_read_b64_tr_b16 a[154:155],v2 offset:25632
v_mfma_f32_16x16x32_bf16 a[64:67],a[172:175],v[156:159],a[64:67]
v_mfma_f32_16x16x32_bf16 a[68:71],a[172:175],v[188:191],a[68:71]
v_mfma_f32_16x16x32_bf16 a[68:71],a[168:171],v[184:187],a[68:71]
v_mfma_f32_16x16x32_bf16 a[68:71],a[164:167],v[172:175],a[68:71]
v_mfma_f32_16x16x32_bf16 a[68:71],a[160:163],v[168:171],a[68:71]
v_mfma_f32_16x16x32_bf16 a[72:75],a[160:163],v[200:203],a[72:75]
v_mfma_f32_16x16x32_bf16 a[72:75],a[164:167],v[204:207],a[72:75]
v_mfma_f32_16x16x32_bf16 a[72:75],a[168:171],v[216:219],a[72:75]
v_mfma_f32_16x16x32_bf16 a[72:75],a[172:175],v[220:223],a[72:75]
buffer_load_dwordx4 v[64:67],v6,s[28:31],s76 offen sc1
v_mfma_f32_16x16x32_bf16 a[76:79],a[172:175],v[252:255],a[76:79]
ds_read_b64_tr_b16 a[172:173],v1 offset:30784
ds_read_b64_tr_b16 a[174:175],v1 offset:35904
v_mfma_f32_16x16x32_bf16 a[76:79],a[168:171],v[248:251],a[76:79]
ds_read_b64_tr_b16 a[168:169],v1 offset:20544
v_mfma_f32_16x16x32_bf16 a[76:79],a[164:167],v[236:239],a[76:79]
ds_read_b64_tr_b16 a[164:165],v1 offset:10304
v_mfma_f32_16x16x32_bf16 a[76:79],a[160:163],v[232:235],a[76:79]
buffer_load_dwordx4 v10,s[72:75],s83 offen offset:3072 lds
v_mfma_f32_16x16x32_bf16 a[80:83],a[176:179],v[136:139],a[80:83]
ds_read_b64_tr_b16 a[160:161],v1 offset:64
ds_read_b64_tr_b16 a[162:163],v1 offset:5184
v_mfma_f32_16x16x32_bf16 a[80:83],a[180:183],v[140:143],a[80:83]
ds_read_b64_tr_b16 a[166:167],v1 offset:15424
v_mfma_f32_16x16x32_bf16 a[80:83],a[184:187],v[152:155],a[80:83]
ds_read_b64_tr_b16 a[170:171],v1 offset:25664
v_mfma_f32_16x16x32_bf16 a[80:83],a[188:191],v[156:159],a[80:83]
buffer_load_dwordx4 v[68:71],v6,s[28:31],s76 offen offset:1024 sc1
v_mfma_f32_16x16x32_bf16 a[84:87],a[188:191],v[188:191],a[84:87]
v_mfma_f32_16x16x32_bf16 a[84:87],a[184:187],v[184:187],a[84:87]
v_mfma_f32_16x16x32_bf16 a[84:87],a[180:183],v[172:175],a[84:87]
v_mfma_f32_16x16x32_bf16 a[84:87],a[176:179],v[168:171],a[84:87]
v_mfma_f32_16x16x32_bf16 a[88:91],a[176:179],v[200:203],a[88:91]
v_mfma_f32_16x16x32_bf16 a[88:91],a[180:183],v[204:207],a[88:91]
s_addk_i32 m0,4096
v_mfma_f32_16x16x32_bf16 a[88:91],a[184:187],v[216:219],a[88:91]
v_mfma_f32_16x16x32_bf16 a[88:91],a[188:191],v[220:223],a[88:91]
buffer_load_dwordx4 v[72:75],v6,s[28:31],s76 offen offset:2048 sc1
v_mfma_f32_16x16x32_bf16 a[92:95],a[188:191],v[252:255],a[92:95]
ds_read_b64_tr_b16 a[188:189],v2 offset:30816
ds_read_b64_tr_b16 a[190:191],v2 offset:35936
v_mfma_f32_16x16x32_bf16 a[92:95],a[184:187],v[248:251],a[92:95]
ds_read_b64_tr_b16 a[184:185],v2 offset:20576
v_mfma_f32_16x16x32_bf16 a[92:95],a[180:183],v[236:239],a[92:95]
s_cmp_ge_u32 0,s71
s_cselect_b32 s80,3,1
s_mul_i32 s80,s80,163840
v_mfma_f32_16x16x32_bf16 a[92:95],a[176:179],v[232:235],a[92:95]
s_add_u32 s80,s80,s81
buffer_load_dwordx4 v11,s[72:75],s83 offen lds
s_waitcnt vmcnt(10) lgkmcnt(3)
v_mfma_f32_16x16x32_bf16 a[192:195],a[128:131],v[128:131],a[192:195]
s_add_u32 s83,s80,20480
ds_read_b64_tr_b16 a[176:177],v2 offset:96
ds_read_b64_tr_b16 a[178:179],v2 offset:5216
v_mfma_f32_16x16x32_bf16 a[192:195],a[132:135],v[160:163],a[192:195]
ds_read_b64_tr_b16 a[180:181],v2 offset:10336
v_mfma_f32_16x16x32_bf16 a[192:195],a[136:139],v[192:195],a[192:195]
ds_read_b64_tr_b16 a[182:183],v2 offset:15456
v_mfma_f32_16x16x32_bf16 a[192:195],a[140:143],v[224:227],a[192:195]
buffer_load_dwordx4 v[76:79],v6,s[28:31],s76 offen offset:3072 sc1
v_mfma_f32_16x16x32_bf16 a[196:199],a[140:143],v[228:231],a[196:199]
ds_read_b64_tr_b16 a[186:187],v2 offset:25696
v_mfma_f32_16x16x32_bf16 a[196:199],a[136:139],v[196:199],a[196:199]
v_mfma_f32_16x16x32_bf16 a[196:199],a[132:135],v[164:167],a[196:199]
v_mfma_f32_16x16x32_bf16 a[196:199],a[128:131],v[132:135],a[196:199]
v_mfma_f32_16x16x32_bf16 a[200:203],a[128:131],v[144:147],a[200:203]
v_mfma_f32_16x16x32_bf16 a[200:203],a[132:135],v[176:179],a[200:203]
v_mfma_f32_16x16x32_bf16 a[200:203],a[136:139],v[208:211],a[200:203]
v_mfma_f32_16x16x32_bf16 a[200:203],a[140:143],v[240:243],a[200:203]
buffer_load_dwordx4 v[80:83],v6,s[28:31],s77 offen sc1
v_mfma_f32_16x16x32_bf16 a[204:207],a[140:143],v[244:247],a[204:207]
v_bfe_u32 v19,s55,13,1
v_mov_b32 v18,s68
v_mfma_f32_16x16x32_bf16 a[204:207],a[136:139],v[212:215],a[204:207]
ds_read_b64_tr_b16 a[136:137],v1 offset:20672
v_or_b32 v19,s69,v19
v_mfma_f32_16x16x32_bf16 a[204:207],a[132:135],v[180:183],a[204:207]
ds_read_b64_tr_b16 a[132:133],v1 offset:10432
ds_read_b64_tr_b16 a[134:135],v1 offset:15552
v_mfma_f32_16x16x32_bf16 a[204:207],a[128:131],v[148:151],a[204:207]
s_barrier
ds_read_b64_tr_b16 a[128:129],v1 offset:192
ds_read_b64_tr_b16 a[130:131],v1 offset:5312
v_mfma_f32_16x16x32_bf16 a[208:211],a[144:147],v[128:131],a[208:211]
ds_read_b64_tr_b16 a[138:139],v1 offset:25792
v_mfma_f32_16x16x32_bf16 a[208:211],a[148:151],v[160:163],a[208:211]
ds_read_b64_tr_b16 a[140:141],v1 offset:30912
v_mfma_f32_16x16x32_bf16 a[208:211],a[152:155],v[192:195],a[208:211]
s_add_u32 m0,s82,0
s_waitcnt vmcnt(8)
s_mov_b64 exec,1
buffer_store_dwordx2 v[18:19],v6,s[64:67],s95 offen offset:1024 sc1
s_mov_b64 exec,-1
v_mfma_f32_16x16x32_bf16 a[208:211],a[156:159],v[224:227],a[208:211]
ds_read_b64_tr_b16 a[142:143],v1 offset:36032
v_mfma_f32_16x16x32_bf16 a[212:215],a[156:159],v[228:231],a[212:215]
v_mfma_f32_16x16x32_bf16 a[212:215],a[152:155],v[196:199],a[212:215]
v_mfma_f32_16x16x32_bf16 a[212:215],a[148:151],v[164:167],a[212:215]
buffer_load_dwordx4 v[84:87],v6,s[28:31],s77 offen offset:1024 sc1
v_mfma_f32_16x16x32_bf16 a[212:215],a[144:147],v[132:135],a[212:215]
v_mfma_f32_16x16x32_bf16 a[216:219],a[144:147],v[144:147],a[216:219]
v_mfma_f32_16x16x32_bf16 a[216:219],a[148:151],v[176:179],a[216:219]
v_mfma_f32_16x16x32_bf16 a[216:219],a[152:155],v[208:211],a[216:219]
buffer_load_dwordx4 v7,s[72:75],s80 offen lds
v_mfma_f32_16x16x32_bf16 a[216:219],a[156:159],v[240:243],a[216:219]
v_mfma_f32_16x16x32_bf16 a[220:223],a[156:159],v[244:247],a[220:223]
ds_read_b64_tr_b16 a[156:157],v2 offset:30944
ds_read_b64_tr_b16 a[158:159],v2 offset:36064
v_mfma_f32_16x16x32_bf16 a[220:223],a[152:155],v[212:215],a[220:223]
ds_read_b64_tr_b16 a[152:153],v2 offset:20704
v_mfma_f32_16x16x32_bf16 a[220:223],a[148:151],v[180:183],a[220:223]
buffer_load_dwordx4 v[88:91],v6,s[28:31],s77 offen offset:2048 sc1
v_mfma_f32_16x16x32_bf16 a[220:223],a[144:147],v[148:151],a[220:223]
ds_read_b64_tr_b16 a[144:145],v2 offset:224
ds_read_b64_tr_b16 a[146:147],v2 offset:5344
v_mfma_f32_16x16x32_bf16 a[224:227],a[160:163],v[128:131],a[224:227]
ds_read_b64_tr_b16 a[148:149],v2 offset:10464
v_mfma_f32_16x16x32_bf16 a[224:227],a[164:167],v[160:163],a[224:227]
ds_read_b64_tr_b16 a[150:151],v2 offset:15584
v_mfma_f32_16x16x32_bf16 a[224:227],a[168:171],v[192:195],a[224:227]
buffer_load_dwordx4 v[92:95],v6,s[28:31],s77 offen offset:3072 sc1
v_mfma_f32_16x16x32_bf16 a[224:227],a[172:175],v[224:227],a[224:227]
ds_read_b64_tr_b16 a[154:155],v2 offset:25824
v_mfma_f32_16x16x32_bf16 a[228:231],a[172:175],v[228:231],a[228:231]
v_mfma_f32_16x16x32_bf16 a[228:231],a[168:171],v[196:199],a[228:231]
v_mfma_f32_16x16x32_bf16 a[228:231],a[164:167],v[164:167],a[228:231]
buffer_load_dwordx4 v8,s[72:75],s80 offen offset:1024 lds
v_mfma_f32_16x16x32_bf16 a[228:231],a[160:163],v[132:135],a[228:231]
v_mfma_f32_16x16x32_bf16 a[232:235],a[160:163],v[144:147],a[232:235]
v_mfma_f32_16x16x32_bf16 a[232:235],a[164:167],v[176:179],a[232:235]
v_mfma_f32_16x16x32_bf16 a[232:235],a[168:171],v[208:211],a[232:235]
buffer_load_dwordx4 v[96:99],v6,s[28:31],s78 offen sc1
v_mfma_f32_16x16x32_bf16 a[232:235],a[172:175],v[240:243],a[232:235]
v_mfma_f32_16x16x32_bf16 a[236:239],a[172:175],v[244:247],a[236:239]
ds_read_b64_tr_b16 a[172:173],v1 offset:30976
ds_read_b64_tr_b16 a[174:175],v1 offset:36096
v_mfma_f32_16x16x32_bf16 a[236:239],a[168:171],v[212:215],a[236:239]
ds_read_b64_tr_b16 a[168:169],v1 offset:20736
v_mfma_f32_16x16x32_bf16 a[236:239],a[164:167],v[180:183],a[236:239]
ds_read_b64_tr_b16 a[164:165],v1 offset:10496
v_mfma_f32_16x16x32_bf16 a[236:239],a[160:163],v[148:151],a[236:239]
s_waitcnt lgkmcnt(4)
ds_read_b64_tr_b16 a[160:161],v1 offset:256
v_mfma_f32_16x16x32_bf16 a[240:243],a[176:179],v[128:131],a[240:243]
ds_read_b64_tr_b16 a[162:163],v1 offset:5376
ds_read_b64_tr_b16 a[166:167],v1 offset:15616
v_mfma_f32_16x16x32_bf16 a[240:243],a[180:183],v[160:163],a[240:243]
ds_read_b64_tr_b16 a[170:171],v1 offset:25856
v_add_u32 v1,81920,v1
v_mfma_f32_16x16x32_bf16 a[240:243],a[184:187],v[192:195],a[240:243]
buffer_load_dwordx4 v[100:103],v6,s[28:31],s78 offen offset:1024 sc1
v_mfma_f32_16x16x32_bf16 a[240:243],a[188:191],v[224:227],a[240:243]
v_mfma_f32_16x16x32_bf16 a[244:247],a[188:191],v[228:231],a[244:247]
v_mfma_f32_16x16x32_bf16 a[244:247],a[184:187],v[196:199],a[244:247]
v_mfma_f32_16x16x32_bf16 a[244:247],a[180:183],v[164:167],a[244:247]
buffer_load_dwordx4 v9,s[72:75],s80 offen offset:2048 lds
v_mfma_f32_16x16x32_bf16 a[244:247],a[176:179],v[132:135],a[244:247]
v_mfma_f32_16x16x32_bf16 a[248:251],a[176:179],v[144:147],a[248:251]
v_mfma_f32_16x16x32_bf16 a[248:251],a[180:183],v[176:179],a[248:251]
v_mfma_f32_16x16x32_bf16 a[248:251],a[184:187],v[208:211],a[248:251]
buffer_load_dwordx4 v[104:107],v6,s[28:31],s78 offen offset:2048 sc1
v_mfma_f32_16x16x32_bf16 a[248:251],a[188:191],v[240:243],a[248:251]
v_mfma_f32_16x16x32_bf16 a[252:255],a[188:191],v[244:247],a[252:255]
ds_read_b64_tr_b16 a[188:189],v2 offset:31008
ds_read_b64_tr_b16 a[190:191],v2 offset:36128
v_mfma_f32_16x16x32_bf16 a[252:255],a[184:187],v[212:215],a[252:255]
ds_read_b64_tr_b16 a[184:185],v2 offset:20768
v_mfma_f32_16x16x32_bf16 a[252:255],a[180:183],v[180:183],a[252:255]
ds_read_b64_tr_b16 a[180:181],v2 offset:10528
v_mfma_f32_16x16x32_bf16 a[252:255],a[176:179],v[148:151],a[252:255]
ds_read_b64_tr_b16 a[176:177],v2 offset:288
v_mfma_f32_16x16x32_bf16 a[0:3],a[96:99],v[128:131],a[0:3]
ds_read_b64_tr_b16 a[178:179],v2 offset:5408
ds_read_b64_tr_b16 a[182:183],v2 offset:15648
v_mfma_f32_16x16x32_bf16 a[0:3],a[100:103],v[160:163],a[0:3]
ds_read_b64_tr_b16 a[186:187],v2 offset:25888
v_add_u32 v2,81920,v2
v_mfma_f32_16x16x32_bf16 a[0:3],a[104:107],v[192:195],a[0:3]
buffer_load_dwordx4 v[108:111],v6,s[28:31],s78 offen offset:3072 sc1
v_mfma_f32_16x16x32_bf16 a[0:3],a[108:111],v[224:227],a[0:3]
v_mfma_f32_16x16x32_bf16 a[4:7],a[108:111],v[228:231],a[4:7]
v_mfma_f32_16x16x32_bf16 a[4:7],a[104:107],v[196:199],a[4:7]
v_mfma_f32_16x16x32_bf16 a[4:7],a[100:103],v[164:167],a[4:7]
buffer_load_dwordx4 v10,s[72:75],s80 offen offset:3072 lds
v_mfma_f32_16x16x32_bf16 a[4:7],a[96:99],v[132:135],a[4:7]
v_mfma_f32_16x16x32_bf16 a[8:11],a[96:99],v[144:147],a[8:11]
v_mfma_f32_16x16x32_bf16 a[8:11],a[100:103],v[176:179],a[8:11]
v_mfma_f32_16x16x32_bf16 a[8:11],a[104:107],v[208:211],a[8:11]
buffer_load_dwordx4 v[112:115],v6,s[28:31],s79 offen sc1
v_mfma_f32_16x16x32_bf16 a[8:11],a[108:111],v[240:243],a[8:11]
v_mfma_f32_16x16x32_bf16 a[12:15],a[108:111],v[244:247],a[12:15]
ds_read_b64_tr_b16 a[108:109],v1 offset:30720
ds_read_b64_tr_b16 a[110:111],v1 offset:35840
v_mfma_f32_16x16x32_bf16 a[12:15],a[104:107],v[212:215],a[12:15]
ds_read_b64_tr_b16 a[104:105],v1 offset:20480
v_mfma_f32_16x16x32_bf16 a[12:15],a[100:103],v[180:183],a[12:15]
s_addk_i32 m0,4096
ds_read_b64_tr_b16 a[100:101],v1 offset:10240
v_mfma_f32_16x16x32_bf16 a[12:15],a[96:99],v[148:151],a[12:15]
ds_read_b64_tr_b16 a[96:97],v1
v_mfma_f32_16x16x32_bf16 a[16:19],a[112:115],v[128:131],a[16:19]
ds_read_b64_tr_b16 a[98:99],v1 offset:5120
ds_read_b64_tr_b16 a[102:103],v1 offset:15360
v_mfma_f32_16x16x32_bf16 a[16:19],a[116:119],v[160:163],a[16:19]
buffer_load_dwordx4 v11,s[72:75],s80 offen lds
v_mfma_f32_16x16x32_bf16 a[16:19],a[120:123],v[192:195],a[16:19]
ds_read_b64_tr_b16 a[106:107],v1 offset:25600
)A"
R"A(v_mfma_f32_16x16x32_bf16 a[16:19],a[124:127],v[224:227],a[16:19]
v_mfma_f32_16x16x32_bf16 a[20:23],a[124:127],v[228:231],a[20:23]
v_mfma_f32_16x16x32_bf16 a[20:23],a[120:123],v[196:199],a[20:23]
buffer_load_dwordx4 v[116:119],v6,s[28:31],s79 offen offset:1024 sc1
v_mfma_f32_16x16x32_bf16 a[20:23],a[116:119],v[164:167],a[20:23]
v_mfma_f32_16x16x32_bf16 a[20:23],a[112:115],v[132:135],a[20:23]
v_mfma_f32_16x16x32_bf16 a[24:27],a[112:115],v[144:147],a[24:27]
v_mfma_f32_16x16x32_bf16 a[24:27],a[116:119],v[176:179],a[24:27]
v_mfma_f32_16x16x32_bf16 a[24:27],a[120:123],v[208:211],a[24:27]
buffer_load_dwordx4 v[120:123],v6,s[28:31],s79 offen offset:2048 sc1
v_mfma_f32_16x16x32_bf16 a[24:27],a[124:127],v[240:243],a[24:27]
v_mfma_f32_16x16x32_bf16 a[28:31],a[124:127],v[244:247],a[28:31]
ds_read_b64_tr_b16 a[124:125],v2 offset:30752
ds_read_b64_tr_b16 a[126:127],v2 offset:35872
v_mfma_f32_16x16x32_bf16 a[28:31],a[120:123],v[212:215],a[28:31]
s_addk_i32 m0,1024
ds_read_b64_tr_b16 a[120:121],v2 offset:20512
v_mfma_f32_16x16x32_bf16 a[28:31],a[116:119],v[180:183],a[28:31]
ds_read_b64_tr_b16 a[116:117],v2 offset:10272
v_mfma_f32_16x16x32_bf16 a[28:31],a[112:115],v[148:151],a[28:31]
s_cmp_ge_u32 2,s71
s_cselect_b32 s76,6,4
ds_read_b64_tr_b16 a[112:113],v2 offset:32
v_mfma_f32_16x16x32_bf16 a[32:35],a[128:131],v[128:131],a[32:35]
buffer_load_dwordx4 v7,s[72:75],s83 offen lds
v_mfma_f32_16x16x32_bf16 a[32:35],a[132:135],v[160:163],a[32:35]
s_bitcmp1_b32 s54,s76
s_cselect_b32 s77,8192,0
s_lshl_b32 s76,s76,17
v_mfma_f32_16x16x32_bf16 a[32:35],a[136:139],v[192:195],a[32:35]
s_add_u32 s76,s76,s77
s_add_u32 s77,s76,4096
s_add_u32 s78,s76,16384
v_mfma_f32_16x16x32_bf16 a[32:35],a[140:143],v[224:227],a[32:35]
s_max_i32 s77,s77,s19
s_max_i32 s78,s78,s100
ds_read_b64_tr_b16 a[114:115],v2 offset:5152
v_mfma_f32_16x16x32_bf16 a[36:39],a[140:143],v[228:231],a[36:39]
buffer_load_dwordx4 v[124:127],v6,s[28:31],s79 offen offset:3072 sc1
v_mfma_f32_16x16x32_bf16 a[36:39],a[136:139],v[196:199],a[36:39]
s_add_u32 s79,s76,20480
s_max_i32 s76,s76,s18
ds_read_b64_tr_b16 a[118:119],v2 offset:15392
v_mfma_f32_16x16x32_bf16 a[36:39],a[132:135],v[164:167],a[36:39]
s_max_i32 s79,s79,s101
ds_read_b64_tr_b16 a[122:123],v2 offset:25632
v_mfma_f32_16x16x32_bf16 a[36:39],a[128:131],v[132:135],a[36:39]
v_mfma_f32_16x16x32_bf16 a[40:43],a[128:131],v[144:147],a[40:43]
v_mfma_f32_16x16x32_bf16 a[40:43],a[132:135],v[176:179],a[40:43]
v_mfma_f32_16x16x32_bf16 a[40:43],a[136:139],v[208:211],a[40:43]
v_mfma_f32_16x16x32_bf16 a[40:43],a[140:143],v[240:243],a[40:43]
v_mfma_f32_16x16x32_bf16 a[44:47],a[140:143],v[244:247],a[44:47]
ds_read_b64_tr_b16 a[140:141],v1 offset:30784
ds_read_b64_tr_b16 a[142:143],v1 offset:35904
v_mfma_f32_16x16x32_bf16 a[44:47],a[136:139],v[212:215],a[44:47]
ds_read_b64_tr_b16 a[136:137],v1 offset:20544
v_mfma_f32_16x16x32_bf16 a[44:47],a[132:135],v[180:183],a[44:47]
ds_read_b64_tr_b16 a[132:133],v1 offset:10304
v_mfma_f32_16x16x32_bf16 a[44:47],a[128:131],v[148:151],a[44:47]
buffer_load_dwordx4 v8,s[72:75],s83 offen offset:1024 lds
v_mfma_f32_16x16x32_bf16 a[48:51],a[144:147],v[128:131],a[48:51]
ds_read_b64_tr_b16 a[128:129],v1 offset:64
ds_read_b64_tr_b16 a[130:131],v1 offset:5184
v_mfma_f32_16x16x32_bf16 a[48:51],a[148:151],v[160:163],a[48:51]
ds_read_b64_tr_b16 a[134:135],v1 offset:15424
v_mfma_f32_16x16x32_bf16 a[48:51],a[152:155],v[192:195],a[48:51]
ds_read_b64_tr_b16 a[138:139],v1 offset:25664
v_mfma_f32_16x16x32_bf16 a[48:51],a[156:159],v[224:227],a[48:51]
v_mfma_f32_16x16x32_bf16 a[52:55],a[156:159],v[228:231],a[52:55]
v_mfma_f32_16x16x32_bf16 a[52:55],a[152:155],v[196:199],a[52:55]
v_mfma_f32_16x16x32_bf16 a[52:55],a[148:151],v[164:167],a[52:55]
v_mfma_f32_16x16x32_bf16 a[52:55],a[144:147],v[132:135],a[52:55]
buffer_load_dwordx4 v[136:139],v6,s[28:31],s76 offen sc1
v_mfma_f32_16x16x32_bf16 a[56:59],a[144:147],v[144:147],a[56:59]
v_mfma_f32_16x16x32_bf16 a[56:59],a[148:151],v[176:179],a[56:59]
v_mfma_f32_16x16x32_bf16 a[56:59],a[152:155],v[208:211],a[56:59]
v_mfma_f32_16x16x32_bf16 a[56:59],a[156:159],v[240:243],a[56:59]
v_mfma_f32_16x16x32_bf16 a[60:63],a[156:159],v[244:247],a[60:63]
ds_read_b64_tr_b16 a[156:157],v2 offset:30816
ds_read_b64_tr_b16 a[158:159],v2 offset:35936
v_mfma_f32_16x16x32_bf16 a[60:63],a[152:155],v[212:215],a[60:63]
buffer_load_dwordx4 v[140:143],v6,s[28:31],s76 offen offset:1024 sc1
v_mfma_f32_16x16x32_bf16 a[60:63],a[148:151],v[180:183],a[60:63]
ds_read_b64_tr_b16 a[148:149],v2 offset:10336
ds_read_b64_tr_b16 a[150:151],v2 offset:15456
v_mfma_f32_16x16x32_bf16 a[60:63],a[144:147],v[148:151],a[60:63]
s_waitcnt lgkmcnt(4)
ds_read_b64_tr_b16 a[144:145],v2 offset:96
v_mfma_f32_16x16x32_bf16 a[64:67],a[160:163],v[128:131],a[64:67]
ds_read_b64_tr_b16 a[146:147],v2 offset:5216
v_mfma_f32_16x16x32_bf16 a[64:67],a[164:167],v[160:163],a[64:67]
buffer_load_dwordx4 v9,s[72:75],s83 offen offset:2048 lds
v_mfma_f32_16x16x32_bf16 a[64:67],a[168:171],v[192:195],a[64:67]
ds_read_b64_tr_b16 a[152:153],v2 offset:20576
ds_read_b64_tr_b16 a[154:155],v2 offset:25696
v_mfma_f32_16x16x32_bf16 a[64:67],a[172:175],v[224:227],a[64:67]
v_mfma_f32_16x16x32_bf16 a[68:71],a[172:175],v[228:231],a[68:71]
v_mfma_f32_16x16x32_bf16 a[68:71],a[168:171],v[196:199],a[68:71]
buffer_load_dwordx4 v[152:155],v6,s[28:31],s76 offen offset:2048 sc1
v_mfma_f32_16x16x32_bf16 a[68:71],a[164:167],v[164:167],a[68:71]
v_mfma_f32_16x16x32_bf16 a[68:71],a[160:163],v[132:135],a[68:71]
v_mfma_f32_16x16x32_bf16 a[72:75],a[160:163],v[144:147],a[72:75]
v_mfma_f32_16x16x32_bf16 a[72:75],a[164:167],v[176:179],a[72:75]
buffer_load_dwordx4 v[156:159],v6,s[28:31],s76 offen offset:3072 sc1
v_mfma_f32_16x16x32_bf16 a[72:75],a[168:171],v[208:211],a[72:75]
v_mfma_f32_16x16x32_bf16 a[72:75],a[172:175],v[240:243],a[72:75]
v_mfma_f32_16x16x32_bf16 a[76:79],a[172:175],v[244:247],a[76:79]
ds_read_b64_tr_b16 a[172:173],v1 offset:30848
ds_read_b64_tr_b16 a[174:175],v1 offset:35968
v_mfma_f32_16x16x32_bf16 a[76:79],a[168:171],v[212:215],a[76:79]
buffer_load_dwordx4 v10,s[72:75],s83 offen offset:3072 lds
v_mfma_f32_16x16x32_bf16 a[76:79],a[164:167],v[180:183],a[76:79]
ds_read_b64_tr_b16 a[164:165],v1 offset:10368
ds_read_b64_tr_b16 a[166:167],v1 offset:15488
v_mfma_f32_16x16x32_bf16 a[76:79],a[160:163],v[148:151],a[76:79]
ds_read_b64_tr_b16 a[160:161],v1 offset:128
v_mfma_f32_16x16x32_bf16 a[80:83],a[176:179],v[128:131],a[80:83]
ds_read_b64_tr_b16 a[162:163],v1 offset:5248
v_mfma_f32_16x16x32_bf16 a[80:83],a[180:183],v[160:163],a[80:83]
buffer_load_dwordx4 v[168:171],v6,s[28:31],s77 offen sc1
v_mfma_f32_16x16x32_bf16 a[80:83],a[184:187],v[192:195],a[80:83]
ds_read_b64_tr_b16 a[168:169],v1 offset:20608
ds_read_b64_tr_b16 a[170:171],v1 offset:25728
v_mfma_f32_16x16x32_bf16 a[80:83],a[188:191],v[224:227],a[80:83]
v_mfma_f32_16x16x32_bf16 a[84:87],a[188:191],v[228:231],a[84:87]
v_mfma_f32_16x16x32_bf16 a[84:87],a[184:187],v[196:199],a[84:87]
buffer_load_dwordx4 v[172:175],v6,s[28:31],s77 offen offset:1024 sc1
v_mfma_f32_16x16x32_bf16 a[84:87],a[180:183],v[164:167],a[84:87]
v_mfma_f32_16x16x32_bf16 a[84:87],a[176:179],v[132:135],a[84:87]
v_mfma_f32_16x16x32_bf16 a[88:91],a[176:179],v[144:147],a[88:91]
v_mfma_f32_16x16x32_bf16 a[88:91],a[180:183],v[176:179],a[88:91]
s_addk_i32 m0,4096
v_mfma_f32_16x16x32_bf16 a[88:91],a[184:187],v[208:211],a[88:91]
v_mfma_f32_16x16x32_bf16 a[88:91],a[188:191],v[240:243],a[88:91]
buffer_load_dwordx2 v[4:5],v24,s[64:67],s94 offen offset:1024 sc1
v_mfma_f32_16x16x32_bf16 a[92:95],a[188:191],v[244:247],a[92:95]
ds_read_b64_tr_b16 a[188:189],v2 offset:30880
ds_read_b64_tr_b16 a[190:191],v2 offset:36000
v_mfma_f32_16x16x32_bf16 a[92:95],a[184:187],v[212:215],a[92:95]
ds_read_b64_tr_b16 a[184:185],v2 offset:20640
v_mfma_f32_16x16x32_bf16 a[92:95],a[180:183],v[180:183],a[92:95]
ds_read_b64_tr_b16 a[180:181],v2 offset:10400
v_mfma_f32_16x16x32_bf16 a[92:95],a[176:179],v[148:151],a[92:95]
s_waitcnt vmcnt(13)
buffer_load_dwordx4 v[184:187],v6,s[28:31],s77 offen offset:2048 sc1
v_mfma_f32_16x16x32_bf16 a[192:195],a[96:99],v[64:67],a[192:195]
s_lshr_b32 s44,s71,1
ds_read_b64_tr_b16 a[176:177],v2 offset:160
ds_read_b64_tr_b16 a[178:179],v2 offset:5280
v_mfma_f32_16x16x32_bf16 a[192:195],a[100:103],v[68:71],a[192:195]
s_lshl_b32 s43,s41,9
s_mul_i32 s44,s44,22
ds_read_b64_tr_b16 a[182:183],v2 offset:15520
v_mfma_f32_16x16x32_bf16 a[192:195],a[104:107],v[72:75],a[192:195]
s_add_u32 s43,s43,s44
s_add_u32 s43,s43,s71
ds_read_b64_tr_b16 a[186:187],v2 offset:25760
v_mfma_f32_16x16x32_bf16 a[192:195],a[108:111],v[76:79],a[192:195]
s_add_u32 s43,s43,s40
buffer_load_dwordx4 v11,s[72:75],s83 offen lds
v_mfma_f32_16x16x32_bf16 a[196:199],a[108:111],v[92:95],a[196:199]
s_add_u32 s43,s43,s40
s_add_u32 s43,s43,64
s_lshl_b32 s43,s43,14
v_mfma_f32_16x16x32_bf16 a[196:199],a[104:107],v[88:91],a[196:199]
s_mov_b64 s[32:33],s[12:13]
s_and_b32 s87,s55,8192
s_lshl_b32 s84,s70,21
v_mfma_f32_16x16x32_bf16 a[196:199],a[100:103],v[84:87],a[196:199]
s_add_u32 s87,s87,s84
s_add_u32 s32,s32,s43
s_addc_u32 s33,s33,0
v_mfma_f32_16x16x32_bf16 a[196:199],a[96:99],v[80:83],a[196:199]
s_add_u32 s84,s87,0
buffer_load_dwordx4 v[188:191],v6,s[28:31],s77 offen offset:3072 sc1
v_mfma_f32_16x16x32_bf16 a[200:203],a[96:99],v[96:99],a[200:203]
s_add_u32 s85,s87,131072
s_add_u32 s86,s87,262144
s_and_b32 s33,s33,0xffff
v_mfma_f32_16x16x32_bf16 a[200:203],a[100:103],v[100:103],a[200:203]
s_mov_b32 s34,6569984
s_mov_b32 s35,0x20000
s_max_i32 s84,s84,s18
v_mfma_f32_16x16x32_bf16 a[200:203],a[104:107],v[104:107],a[200:203]
s_max_i32 s85,s85,s19
s_max_i32 s86,s86,s100
v_bfe_u32 v18,v6,6,2
v_mfma_f32_16x16x32_bf16 a[200:203],a[108:111],v[108:111],a[200:203]
s_mul_i32 s44,s41,307200
buffer_load_dwordx4 v[200:203],v6,s[28:31],s78 offen sc1
s_waitcnt vmcnt(14)
v_mfma_f32_16x16x32_bf16 a[204:207],a[108:111],v[124:127],a[204:207]
s_mov_b64 s[20:21],s[16:17]
ds_read_b64_tr_b16 a[108:109],v1 offset:30912
ds_read_b64_tr_b16 a[110:111],v1 offset:36032
v_mfma_f32_16x16x32_bf16 a[204:207],a[104:107],v[120:123],a[204:207]
s_lshl_b32 s43,s40,2
ds_read_b64_tr_b16 a[104:105],v1 offset:20672
v_lshrrev_b32 v19,8,v6
v_mfma_f32_16x16x32_bf16 a[204:207],a[100:103],v[116:119],a[204:207]
s_add_u32 s43,s43,s70
ds_read_b64_tr_b16 a[100:101],v1 offset:10432
v_xor_b32 v18,v18,v19
v_mfma_f32_16x16x32_bf16 a[204:207],a[96:99],v[112:115],a[204:207]
s_barrier
buffer_load_dwordx4 v[204:207],v6,s[28:31],s78 offen offset:1024 sc1
v_mfma_f32_16x16x32_bf16 a[208:211],a[112:115],v[64:67],a[208:211]
s_mulk_i32 s43,20480
ds_read_b64_tr_b16 a[96:97],v1 offset:192
ds_read_b64_tr_b16 a[98:99],v1 offset:5312
v_mfma_f32_16x16x32_bf16 a[208:211],a[116:119],v[68:71],a[208:211]
s_mov_b32 s22,307200
ds_read_b64_tr_b16 a[102:103],v1 offset:15552
v_lshlrev_b32 v18,2,v18
v_mfma_f32_16x16x32_bf16 a[208:211],a[120:123],v[72:75],a[208:211]
s_mov_b32 s23,0x20000
ds_read_b64_tr_b16 a[106:107],v1 offset:25792
v_bfe_u32 v19,v6,4,2
v_mfma_f32_16x16x32_bf16 a[208:211],a[124:127],v[76:79],a[208:211]
buffer_load_dwordx4 v[216:219],v6,s[28:31],s78 offen offset:2048 sc1
v_mfma_f32_16x16x32_bf16 a[212:215],a[124:127],v[92:95],a[212:215]
v_or_b32 v18,v18,v19
v_and_b32 v19,16,v6
v_mfma_f32_16x16x32_bf16 a[212:215],a[120:123],v[88:91],a[212:215]
v_mov_b32 v34,0x3f80
v_lshlrev_b32 v19,v19,v34
v_mfma_f32_16x16x32_bf16 a[212:215],a[116:119],v[84:87],a[212:215]
v_lshrrev_b32 v18,1,v18
v_add_u32 v25,4096,v6
v_mfma_f32_16x16x32_bf16 a[212:215],a[112:115],v[80:83],a[212:215]
s_cmp_ge_u32 0,s71
s_cselect_b32 s76,3,1
v_mov_b32 v36,0
v_mfma_f32_16x16x32_bf16 a[216:219],a[112:115],v[96:99],a[216:219]
buffer_load_dwordx4 v[220:223],v6,s[28:31],s78 offen offset:3072 sc1
v_mfma_f32_16x16x32_bf16 a[216:219],a[116:119],v[100:103],a[216:219]
s_bitcmp1_b32 s54,s76
s_cselect_b32 s77,8192,0
s_lshl_b32 s76,s76,17
v_mfma_f32_16x16x32_bf16 a[216:219],a[120:123],v[104:107],a[216:219]
s_add_u32 s76,s76,s77
s_add_u32 s77,s76,4096
s_add_u32 s78,s76,16384
v_mfma_f32_16x16x32_bf16 a[216:219],a[124:127],v[108:111],a[216:219]
s_max_i32 s77,s77,s19
s_max_i32 s78,s78,s100
v_mov_b32 v37,0
)A"
R"A(v_mfma_f32_16x16x32_bf16 a[220:223],a[124:127],v[124:127],a[220:223]
ds_read_b64_tr_b16 a[124:125],v2 offset:30944
ds_read_b64_tr_b16 a[126:127],v2 offset:36064
v_mfma_f32_16x16x32_bf16 a[220:223],a[120:123],v[120:123],a[220:223]
ds_read_b64_tr_b16 a[120:121],v2 offset:20704
v_mov_b32 v32,0
v_mfma_f32_16x16x32_bf16 a[220:223],a[116:119],v[116:119],a[220:223]
buffer_load_dwordx4 v[232:235],v6,s[28:31],s79 offen sc1
v_mfma_f32_16x16x32_bf16 a[220:223],a[112:115],v[112:115],a[220:223]
ds_read_b64_tr_b16 a[112:113],v2 offset:224
ds_read_b64_tr_b16 a[114:115],v2 offset:5344
v_mfma_f32_16x16x32_bf16 a[224:227],a[128:131],v[64:67],a[224:227]
ds_read_b64_tr_b16 a[116:117],v2 offset:10464
v_mov_b32 v33,0
v_mfma_f32_16x16x32_bf16 a[224:227],a[132:135],v[68:71],a[224:227]
ds_read_b64_tr_b16 a[118:119],v2 offset:15584
v_bfe_u32 v40,v6,4,4
v_mfma_f32_16x16x32_bf16 a[224:227],a[136:139],v[72:75],a[224:227]
ds_read_b64_tr_b16 a[122:123],v2 offset:25824
v_mul_u32_u24 v40,1280,v40
v_mfma_f32_16x16x32_bf16 a[224:227],a[140:143],v[76:79],a[224:227]
v_cmp_eq_u32 vcc,0,v18
s_nop 1
v_cndmask_b32 v34,0,v19,vcc
v_mfma_f32_16x16x32_bf16 a[228:231],a[140:143],v[92:95],a[228:231]
buffer_load_dwordx4 v[236:239],v6,s[28:31],s79 offen offset:1024 sc1
v_mfma_f32_16x16x32_bf16 a[228:231],a[136:139],v[88:91],a[228:231]
v_cmp_eq_u32 vcc,1,v18
s_nop 1
v_cndmask_b32 v35,0,v19,vcc
v_mfma_f32_16x16x32_bf16 a[228:231],a[132:135],v[84:87],a[228:231]
s_add_u32 s20,s20,s44
s_addc_u32 s21,s21,0
v_bfe_u32 v41,v6,8,1
v_mfma_f32_16x16x32_bf16 a[228:231],a[128:131],v[80:83],a[228:231]
s_waitcnt vmcnt(9)
v_cmp_ne_u32 s[90:91],s68,v4
v_and_b32 v4,-2,v5
v_cmp_ne_u32 s[92:93],s69,v4
s_nop 0
s_or_b64 s[90:91],s[90:91],s[92:93]
s_cbranch_scc1 L_fr9_%=
L_fo9_%=:
v_mfma_f32_16x16x32_bf16 a[232:235],a[128:131],v[96:99],a[232:235]
s_mul_i32 s44,s71,320
v_lshl_add_u32 v40,v41,5,v40
v_lshrrev_b32 v41,9,v6
v_mfma_f32_16x16x32_bf16 a[232:235],a[132:135],v[100:103],a[232:235]
s_and_b32 s21,s21,0xffff
s_add_u32 s43,s43,s44
v_lshl_add_u32 v40,v41,4,v40
v_mfma_f32_16x16x32_bf16 a[232:235],a[136:139],v[104:107],a[232:235]
buffer_load_dwordx4 v[248:251],v6,s[28:31],s79 offen offset:2048 sc1
v_mfma_f32_16x16x32_bf16 a[232:235],a[140:143],v[108:111],a[232:235]
v_mfma_f32_16x16x32_bf16 a[236:239],a[140:143],v[124:127],a[236:239]
ds_read_b64_tr_b16 a[140:141],v1 offset:30976
ds_read_b64_tr_b16 a[142:143],v1 offset:36096
v_mfma_f32_16x16x32_bf16 a[236:239],a[136:139],v[120:123],a[236:239]
ds_read_b64_tr_b16 a[136:137],v1 offset:20736
v_mfma_f32_16x16x32_bf16 a[236:239],a[132:135],v[116:119],a[236:239]
ds_read_b64_tr_b16 a[132:133],v1 offset:10496
v_mfma_f32_16x16x32_bf16 a[236:239],a[128:131],v[112:115],a[236:239]
s_waitcnt lgkmcnt(4)
ds_read_b64_tr_b16 a[128:129],v1 offset:256
v_mfma_f32_16x16x32_bf16 a[240:243],a[144:147],v[64:67],a[240:243]
buffer_load_dwordx4 v[252:255],v6,s[28:31],s79 offen offset:3072 sc1
v_mfma_f32_16x16x32_bf16 a[240:243],a[148:151],v[68:71],a[240:243]
s_add_u32 s79,s76,20480
s_max_i32 s76,s76,s18
ds_read_b64_tr_b16 a[130:131],v1 offset:5376
v_mfma_f32_16x16x32_bf16 a[240:243],a[152:155],v[72:75],a[240:243]
s_max_i32 s79,s79,s101
ds_read_b64_tr_b16 a[134:135],v1 offset:15616
ds_read_b64_tr_b16 a[138:139],v1 offset:25856
v_mfma_f32_16x16x32_bf16 a[240:243],a[156:159],v[76:79],a[240:243]
v_subrev_u32 v1,40960,v1
v_mfma_f32_16x16x32_bf16 a[244:247],a[156:159],v[92:95],a[244:247]
buffer_load_dwordx4 v[128:131],v6,s[28:31],s76 offen sc1
v_mfma_f32_16x16x32_bf16 a[244:247],a[152:155],v[88:91],a[244:247]
v_mfma_f32_16x16x32_bf16 a[244:247],a[148:151],v[84:87],a[244:247]
v_mfma_f32_16x16x32_bf16 a[244:247],a[144:147],v[80:83],a[244:247]
v_mfma_f32_16x16x32_bf16 a[248:251],a[144:147],v[96:99],a[248:251]
buffer_load_dwordx4 v[160:163],v6,s[28:31],s76 offen offset:1024 sc1
v_mfma_f32_16x16x32_bf16 a[248:251],a[148:151],v[100:103],a[248:251]
v_mfma_f32_16x16x32_bf16 a[248:251],a[152:155],v[104:107],a[248:251]
v_mfma_f32_16x16x32_bf16 a[248:251],a[156:159],v[108:111],a[248:251]
v_mfma_f32_16x16x32_bf16 a[252:255],a[156:159],v[124:127],a[252:255]
buffer_load_dwordx4 v[192:195],v6,s[28:31],s76 offen offset:2048 sc1
v_mfma_f32_16x16x32_bf16 a[252:255],a[152:155],v[120:123],a[252:255]
ds_read_b64_tr_b16 a[152:153],v2 offset:20768
ds_read_b64_tr_b16 a[154:155],v2 offset:25888
v_mfma_f32_16x16x32_bf16 a[252:255],a[148:151],v[116:119],a[252:255]
ds_read_b64_tr_b16 a[148:149],v2 offset:10528
v_mfma_f32_16x16x32_bf16 a[252:255],a[144:147],v[112:115],a[252:255]
ds_read_b64_tr_b16 a[144:145],v2 offset:288
v_mfma_f32_16x16x32_bf16 a[0:3],a[160:163],v[64:67],a[0:3]
ds_read_b64_tr_b16 a[146:147],v2 offset:5408
v_mfma_f32_16x16x32_bf16 a[0:3],a[164:167],v[68:71],a[0:3]
buffer_load_dwordx4 v[224:227],v6,s[28:31],s76 offen offset:3072 sc1
v_mfma_f32_16x16x32_bf16 a[0:3],a[168:171],v[72:75],a[0:3]
ds_read_b64_tr_b16 a[150:151],v2 offset:15648
ds_read_b64_tr_b16 a[156:157],v2 offset:31008
v_mfma_f32_16x16x32_bf16 a[0:3],a[172:175],v[76:79],a[0:3]
ds_read_b64_tr_b16 a[158:159],v2 offset:36128
v_subrev_u32 v2,40960,v2
v_mfma_f32_16x16x32_bf16 a[4:7],a[172:175],v[92:95],a[4:7]
v_mfma_f32_16x16x32_bf16 a[4:7],a[168:171],v[88:91],a[4:7]
v_mfma_f32_16x16x32_bf16 a[4:7],a[164:167],v[84:87],a[4:7]
v_mfma_f32_16x16x32_bf16 a[4:7],a[160:163],v[80:83],a[4:7]
buffer_load_dwordx4 v[132:135],v6,s[28:31],s77 offen sc1
v_mfma_f32_16x16x32_bf16 a[8:11],a[160:163],v[96:99],a[8:11]
v_mfma_f32_16x16x32_bf16 a[8:11],a[164:167],v[100:103],a[8:11]
v_mfma_f32_16x16x32_bf16 a[8:11],a[168:171],v[104:107],a[8:11]
v_mfma_f32_16x16x32_bf16 a[8:11],a[172:175],v[108:111],a[8:11]
v_mfma_f32_16x16x32_bf16 a[12:15],a[172:175],v[124:127],a[12:15]
ds_read_b64_tr_b16 a[172:173],v1 offset:30720
ds_read_b64_tr_b16 a[174:175],v1 offset:35840
v_mfma_f32_16x16x32_bf16 a[12:15],a[168:171],v[120:123],a[12:15]
buffer_load_dwordx4 v[164:167],v6,s[28:31],s77 offen offset:1024 sc1
v_mfma_f32_16x16x32_bf16 a[12:15],a[164:167],v[116:119],a[12:15]
ds_read_b64_tr_b16 a[164:165],v1 offset:10240
ds_read_b64_tr_b16 a[166:167],v1 offset:15360
v_mfma_f32_16x16x32_bf16 a[12:15],a[160:163],v[112:115],a[12:15]
ds_read_b64_tr_b16 a[160:161],v1
v_mfma_f32_16x16x32_bf16 a[16:19],a[176:179],v[64:67],a[16:19]
ds_read_b64_tr_b16 a[162:163],v1 offset:5120
v_mfma_f32_16x16x32_bf16 a[16:19],a[180:183],v[68:71],a[16:19]
ds_read_b64_tr_b16 a[168:169],v1 offset:20480
v_mfma_f32_16x16x32_bf16 a[16:19],a[184:187],v[72:75],a[16:19]
ds_read_b64_tr_b16 a[170:171],v1 offset:25600
v_mfma_f32_16x16x32_bf16 a[16:19],a[188:191],v[76:79],a[16:19]
buffer_load_dwordx4 v[196:199],v6,s[28:31],s77 offen offset:2048 sc1
v_mfma_f32_16x16x32_bf16 a[20:23],a[188:191],v[92:95],a[20:23]
v_mfma_f32_16x16x32_bf16 a[20:23],a[184:187],v[88:91],a[20:23]
v_mfma_f32_16x16x32_bf16 a[20:23],a[180:183],v[84:87],a[20:23]
v_mfma_f32_16x16x32_bf16 a[20:23],a[176:179],v[80:83],a[20:23]
v_mfma_f32_16x16x32_bf16 a[24:27],a[176:179],v[96:99],a[24:27]
v_mfma_f32_16x16x32_bf16 a[24:27],a[180:183],v[100:103],a[24:27]
buffer_load_dwordx4 v[228:231],v6,s[28:31],s77 offen offset:3072 sc1
v_mfma_f32_16x16x32_bf16 a[24:27],a[184:187],v[104:107],a[24:27]
v_mfma_f32_16x16x32_bf16 a[24:27],a[188:191],v[108:111],a[24:27]
v_mfma_f32_16x16x32_bf16 a[28:31],a[188:191],v[124:127],a[28:31]
ds_read_b64_tr_b16 a[188:189],v2 offset:30752
ds_read_b64_tr_b16 a[190:191],v2 offset:35872
v_mfma_f32_16x16x32_bf16 a[28:31],a[184:187],v[120:123],a[28:31]
ds_read_b64_tr_b16 a[184:185],v2 offset:20512
v_mfma_f32_16x16x32_bf16 a[28:31],a[180:183],v[116:119],a[28:31]
ds_read_b64_tr_b16 a[180:181],v2 offset:10272
v_mfma_f32_16x16x32_bf16 a[28:31],a[176:179],v[112:115],a[28:31]
buffer_load_dwordx4 v[144:147],v6,s[28:31],s78 offen sc1
v_mfma_f32_16x16x32_bf16 a[32:35],a[96:99],v[64:67],a[32:35]
ds_read_b64_tr_b16 a[176:177],v2 offset:32
ds_read_b64_tr_b16 a[178:179],v2 offset:5152
v_mfma_f32_16x16x32_bf16 a[32:35],a[100:103],v[68:71],a[32:35]
ds_read_b64_tr_b16 a[182:183],v2 offset:15392
v_mfma_f32_16x16x32_bf16 a[32:35],a[104:107],v[72:75],a[32:35]
ds_read_b64_tr_b16 a[186:187],v2 offset:25632
v_mfma_f32_16x16x32_bf16 a[32:35],a[108:111],v[76:79],a[32:35]
v_mfma_f32_16x16x32_bf16 a[36:39],a[108:111],v[92:95],a[36:39]
v_mfma_f32_16x16x32_bf16 a[36:39],a[104:107],v[88:91],a[36:39]
buffer_load_dwordx4 v[176:179],v6,s[28:31],s78 offen offset:1024 sc1
v_mfma_f32_16x16x32_bf16 a[36:39],a[100:103],v[84:87],a[36:39]
v_mfma_f32_16x16x32_bf16 a[36:39],a[96:99],v[80:83],a[36:39]
v_mfma_f32_16x16x32_bf16 a[40:43],a[96:99],v[96:99],a[40:43]
v_mfma_f32_16x16x32_bf16 a[40:43],a[100:103],v[100:103],a[40:43]
v_mfma_f32_16x16x32_bf16 a[40:43],a[104:107],v[104:107],a[40:43]
v_mfma_f32_16x16x32_bf16 a[40:43],a[108:111],v[108:111],a[40:43]
buffer_load_dwordx4 v[208:211],v6,s[28:31],s78 offen offset:2048 sc1
v_mfma_f32_16x16x32_bf16 a[44:47],a[108:111],v[124:127],a[44:47]
ds_read_b64_tr_b16 a[108:109],v1 offset:30784
ds_read_b64_tr_b16 a[110:111],v1 offset:35904
v_mfma_f32_16x16x32_bf16 a[44:47],a[104:107],v[120:123],a[44:47]
ds_read_b64_tr_b16 a[104:105],v1 offset:20544
v_mfma_f32_16x16x32_bf16 a[44:47],a[100:103],v[116:119],a[44:47]
ds_read_b64_tr_b16 a[100:101],v1 offset:10304
v_mfma_f32_16x16x32_bf16 a[44:47],a[96:99],v[112:115],a[44:47]
ds_read_b64_tr_b16 a[96:97],v1 offset:64
v_mfma_f32_16x16x32_bf16 a[48:51],a[112:115],v[64:67],a[48:51]
ds_read_b64_tr_b16 a[98:99],v1 offset:5184
ds_read_b64_tr_b16 a[102:103],v1 offset:15424
v_mfma_f32_16x16x32_bf16 a[48:51],a[116:119],v[68:71],a[48:51]
buffer_load_dwordx4 v[240:243],v6,s[28:31],s78 offen offset:3072 sc1
v_mfma_f32_16x16x32_bf16 a[48:51],a[120:123],v[72:75],a[48:51]
ds_read_b64_tr_b16 a[106:107],v1 offset:25664
v_mfma_f32_16x16x32_bf16 a[48:51],a[124:127],v[76:79],a[48:51]
v_mfma_f32_16x16x32_bf16 a[52:55],a[124:127],v[92:95],a[52:55]
v_mfma_f32_16x16x32_bf16 a[52:55],a[120:123],v[88:91],a[52:55]
v_mfma_f32_16x16x32_bf16 a[52:55],a[116:119],v[84:87],a[52:55]
v_mfma_f32_16x16x32_bf16 a[52:55],a[112:115],v[80:83],a[52:55]
buffer_load_dwordx4 v[148:151],v6,s[28:31],s79 offen sc1
v_mfma_f32_16x16x32_bf16 a[56:59],a[112:115],v[96:99],a[56:59]
v_mfma_f32_16x16x32_bf16 a[56:59],a[116:119],v[100:103],a[56:59]
s_cmp_ge_u32 1,s71
s_cselect_b32 s80,5,3
s_mul_i32 s80,s80,163840
v_mfma_f32_16x16x32_bf16 a[56:59],a[120:123],v[104:107],a[56:59]
s_add_u32 s80,s80,s81
s_add_u32 s83,s80,20480
v_mfma_f32_16x16x32_bf16 a[56:59],a[124:127],v[108:111],a[56:59]
v_mfma_f32_16x16x32_bf16 a[60:63],a[124:127],v[124:127],a[60:63]
ds_read_b64_tr_b16 a[124:125],v2 offset:30816
ds_read_b64_tr_b16 a[126:127],v2 offset:35936
v_mfma_f32_16x16x32_bf16 a[60:63],a[120:123],v[120:123],a[60:63]
buffer_load_dwordx4 v[180:183],v6,s[28:31],s79 offen offset:1024 sc1
v_mfma_f32_16x16x32_bf16 a[60:63],a[116:119],v[116:119],a[60:63]
ds_read_b64_tr_b16 a[116:117],v2 offset:10336
ds_read_b64_tr_b16 a[118:119],v2 offset:15456
v_mfma_f32_16x16x32_bf16 a[60:63],a[112:115],v[112:115],a[60:63]
s_waitcnt lgkmcnt(4)
ds_read_b64_tr_b16 a[112:113],v2 offset:96
v_mfma_f32_16x16x32_bf16 a[64:67],a[128:131],v[64:67],a[64:67]
ds_read_b64_tr_b16 a[114:115],v2 offset:5216
v_mfma_f32_16x16x32_bf16 a[64:67],a[132:135],v[68:71],a[64:67]
ds_read_b64_tr_b16 a[120:121],v2 offset:20576
v_mfma_f32_16x16x32_bf16 a[64:67],a[136:139],v[72:75],a[64:67]
ds_read_b64_tr_b16 a[122:123],v2 offset:25696
v_mfma_f32_16x16x32_bf16 a[64:67],a[140:143],v[76:79],a[64:67]
buffer_load_dwordx4 v[212:215],v6,s[28:31],s79 offen offset:2048 sc1
v_mfma_f32_16x16x32_bf16 a[68:71],a[140:143],v[92:95],a[68:71]
v_mfma_f32_16x16x32_bf16 a[68:71],a[136:139],v[88:91],a[68:71]
v_mfma_f32_16x16x32_bf16 a[68:71],a[132:135],v[84:87],a[68:71]
v_mfma_f32_16x16x32_bf16 a[68:71],a[128:131],v[80:83],a[68:71]
v_mfma_f32_16x16x32_bf16 a[72:75],a[128:131],v[96:99],a[72:75]
v_mfma_f32_16x16x32_bf16 a[72:75],a[132:135],v[100:103],a[72:75]
buffer_load_dwordx4 v[244:247],v6,s[28:31],s79 offen offset:3072 sc1
v_mfma_f32_16x16x32_bf16 a[72:75],a[136:139],v[104:107],a[72:75]
v_mfma_f32_16x16x32_bf16 a[72:75],a[140:143],v[108:111],a[72:75]
v_mfma_f32_16x16x32_bf16 a[76:79],a[140:143],v[124:127],a[76:79]
)A"
R"A(ds_read_b64_tr_b16 a[140:141],v1 offset:30848
ds_read_b64_tr_b16 a[142:143],v1 offset:35968
v_mfma_f32_16x16x32_bf16 a[76:79],a[136:139],v[120:123],a[76:79]
ds_read_b64_tr_b16 a[136:137],v1 offset:20608
v_mfma_f32_16x16x32_bf16 a[76:79],a[132:135],v[116:119],a[76:79]
s_add_u32 m0,s82,40960
ds_read_b64_tr_b16 a[132:133],v1 offset:10368
v_mfma_f32_16x16x32_bf16 a[76:79],a[128:131],v[112:115],a[76:79]
ds_read_b64_tr_b16 a[128:129],v1 offset:128
v_mfma_f32_16x16x32_bf16 a[80:83],a[144:147],v[64:67],a[80:83]
ds_read_b64_tr_b16 a[130:131],v1 offset:5248
ds_read_b64_tr_b16 a[134:135],v1 offset:15488
v_mfma_f32_16x16x32_bf16 a[80:83],a[148:151],v[68:71],a[80:83]
buffer_load_dwordx4 v7,s[72:75],s80 offen lds
v_mfma_f32_16x16x32_bf16 a[80:83],a[152:155],v[72:75],a[80:83]
ds_read_b64_tr_b16 a[138:139],v1 offset:25728
v_mfma_f32_16x16x32_bf16 a[80:83],a[156:159],v[76:79],a[80:83]
v_mfma_f32_16x16x32_bf16 a[84:87],a[156:159],v[92:95],a[84:87]
v_mfma_f32_16x16x32_bf16 a[84:87],a[152:155],v[88:91],a[84:87]
v_mfma_f32_16x16x32_bf16 a[84:87],a[148:151],v[84:87],a[84:87]
buffer_load_dwordx4 v8,s[72:75],s80 offen offset:1024 lds
v_mfma_f32_16x16x32_bf16 a[84:87],a[144:147],v[80:83],a[84:87]
v_mfma_f32_16x16x32_bf16 a[88:91],a[144:147],v[96:99],a[88:91]
v_mfma_f32_16x16x32_bf16 a[88:91],a[148:151],v[100:103],a[88:91]
v_mfma_f32_16x16x32_bf16 a[88:91],a[152:155],v[104:107],a[88:91]
v_mfma_f32_16x16x32_bf16 a[88:91],a[156:159],v[108:111],a[88:91]
buffer_load_dwordx4 v9,s[72:75],s80 offen offset:2048 lds
v_mfma_f32_16x16x32_bf16 a[92:95],a[156:159],v[124:127],a[92:95]
ds_read_b64_tr_b16 a[156:157],v2 offset:30880
ds_read_b64_tr_b16 a[158:159],v2 offset:36000
v_mfma_f32_16x16x32_bf16 a[92:95],a[152:155],v[120:123],a[92:95]
ds_read_b64_tr_b16 a[152:153],v2 offset:20640
v_mfma_f32_16x16x32_bf16 a[92:95],a[148:151],v[116:119],a[92:95]
ds_read_b64_tr_b16 a[148:149],v2 offset:10400
v_mfma_f32_16x16x32_bf16 a[92:95],a[144:147],v[112:115],a[92:95]
ds_read_b64_tr_b16 a[144:145],v2 offset:160
v_mfma_f32_16x16x32_bf16 a[192:195],a[160:163],v[136:139],a[192:195]
buffer_load_dwordx4 v10,s[72:75],s80 offen offset:3072 lds
v_mfma_f32_16x16x32_bf16 a[192:195],a[164:167],v[140:143],a[192:195]
ds_read_b64_tr_b16 a[146:147],v2 offset:5280
ds_read_b64_tr_b16 a[150:151],v2 offset:15520
v_mfma_f32_16x16x32_bf16 a[192:195],a[168:171],v[152:155],a[192:195]
s_addk_i32 m0,4096
ds_read_b64_tr_b16 a[154:155],v2 offset:25760
v_mfma_f32_16x16x32_bf16 a[192:195],a[172:175],v[156:159],a[192:195]
s_waitcnt vmcnt(12)
v_mfma_f32_16x16x32_bf16 a[196:199],a[172:175],v[188:191],a[196:199]
v_mfma_f32_16x16x32_bf16 a[196:199],a[168:171],v[184:187],a[196:199]
buffer_load_dwordx4 v11,s[72:75],s80 offen lds
v_mfma_f32_16x16x32_bf16 a[196:199],a[164:167],v[172:175],a[196:199]
v_mfma_f32_16x16x32_bf16 a[196:199],a[160:163],v[168:171],a[196:199]
s_addk_i32 m0,1024
v_mfma_f32_16x16x32_bf16 a[200:203],a[160:163],v[200:203],a[200:203]
v_mfma_f32_16x16x32_bf16 a[200:203],a[164:167],v[204:207],a[200:203]
v_mfma_f32_16x16x32_bf16 a[200:203],a[168:171],v[216:219],a[200:203]
buffer_load_dwordx4 v7,s[72:75],s83 offen lds
v_mfma_f32_16x16x32_bf16 a[200:203],a[172:175],v[220:223],a[200:203]
v_mfma_f32_16x16x32_bf16 a[204:207],a[172:175],v[252:255],a[204:207]
ds_read_b64_tr_b16 a[172:173],v1 offset:30912
ds_read_b64_tr_b16 a[174:175],v1 offset:36032
v_mfma_f32_16x16x32_bf16 a[204:207],a[168:171],v[248:251],a[204:207]
ds_read_b64_tr_b16 a[168:169],v1 offset:20672
v_mfma_f32_16x16x32_bf16 a[204:207],a[164:167],v[236:239],a[204:207]
ds_read_b64_tr_b16 a[164:165],v1 offset:10432
v_mfma_f32_16x16x32_bf16 a[204:207],a[160:163],v[232:235],a[204:207]
s_barrier
buffer_load_dwordx4 v8,s[72:75],s83 offen offset:1024 lds
v_mfma_f32_16x16x32_bf16 a[208:211],a[176:179],v[136:139],a[208:211]
s_cmp_ge_u32 1,s71
s_cselect_b32 s76,5,3
ds_read_b64_tr_b16 a[160:161],v1 offset:192
v_mfma_f32_16x16x32_bf16 a[208:211],a[180:183],v[140:143],a[208:211]
s_bitcmp1_b32 s54,s76
s_cselect_b32 s77,8192,0
s_lshl_b32 s76,s76,17
v_mfma_f32_16x16x32_bf16 a[208:211],a[184:187],v[152:155],a[208:211]
s_add_u32 s76,s76,s77
s_add_u32 s77,s76,4096
s_add_u32 s78,s76,16384
v_mfma_f32_16x16x32_bf16 a[208:211],a[188:191],v[156:159],a[208:211]
s_add_u32 s79,s76,20480
buffer_load_dwordx4 v9,s[72:75],s83 offen offset:2048 lds
v_mfma_f32_16x16x32_bf16 a[212:215],a[188:191],v[188:191],a[212:215]
s_max_i32 s76,s76,s18
s_max_i32 s77,s77,s19
s_max_i32 s78,s78,s100
v_mfma_f32_16x16x32_bf16 a[212:215],a[184:187],v[184:187],a[212:215]
s_max_i32 s79,s79,s101
ds_read_b64_tr_b16 a[162:163],v1 offset:5312
ds_read_b64_tr_b16 a[166:167],v1 offset:15552
v_mfma_f32_16x16x32_bf16 a[212:215],a[180:183],v[172:175],a[212:215]
ds_read_b64_tr_b16 a[170:171],v1 offset:25792
v_mfma_f32_16x16x32_bf16 a[212:215],a[176:179],v[168:171],a[212:215]
v_mfma_f32_16x16x32_bf16 a[216:219],a[176:179],v[200:203],a[216:219]
buffer_load_dwordx4 v10,s[72:75],s83 offen offset:3072 lds
v_mfma_f32_16x16x32_bf16 a[216:219],a[180:183],v[204:207],a[216:219]
v_mfma_f32_16x16x32_bf16 a[216:219],a[184:187],v[216:219],a[216:219]
s_addk_i32 m0,4096
v_mfma_f32_16x16x32_bf16 a[216:219],a[188:191],v[220:223],a[216:219]
v_mfma_f32_16x16x32_bf16 a[220:223],a[188:191],v[252:255],a[220:223]
ds_read_b64_tr_b16 a[188:189],v2 offset:30944
ds_read_b64_tr_b16 a[190:191],v2 offset:36064
v_mfma_f32_16x16x32_bf16 a[220:223],a[184:187],v[248:251],a[220:223]
buffer_load_dwordx4 v11,s[72:75],s83 offen lds
v_mfma_f32_16x16x32_bf16 a[220:223],a[180:183],v[236:239],a[220:223]
ds_read_b64_tr_b16 a[180:181],v2 offset:10464
ds_read_b64_tr_b16 a[182:183],v2 offset:15584
v_mfma_f32_16x16x32_bf16 a[220:223],a[176:179],v[232:235],a[220:223]
ds_read_b64_tr_b16 a[176:177],v2 offset:224
v_mfma_f32_16x16x32_bf16 a[224:227],a[96:99],v[136:139],a[224:227]
ds_read_b64_tr_b16 a[178:179],v2 offset:5344
v_mfma_f32_16x16x32_bf16 a[224:227],a[100:103],v[140:143],a[224:227]
ds_read_b64_tr_b16 a[184:185],v2 offset:20704
v_mfma_f32_16x16x32_bf16 a[224:227],a[104:107],v[152:155],a[224:227]
ds_read_b64_tr_b16 a[186:187],v2 offset:25824
v_mfma_f32_16x16x32_bf16 a[224:227],a[108:111],v[156:159],a[224:227]
v_mfma_f32_16x16x32_bf16 a[228:231],a[108:111],v[188:191],a[228:231]
v_mfma_f32_16x16x32_bf16 a[228:231],a[104:107],v[184:187],a[228:231]
v_mfma_f32_16x16x32_bf16 a[228:231],a[100:103],v[172:175],a[228:231]
v_mfma_f32_16x16x32_bf16 a[228:231],a[96:99],v[168:171],a[228:231]
buffer_load_dwordx4 v[64:67],v6,s[28:31],s76 offen sc1
v_mfma_f32_16x16x32_bf16 a[232:235],a[96:99],v[200:203],a[232:235]
v_mfma_f32_16x16x32_bf16 a[232:235],a[100:103],v[204:207],a[232:235]
v_mfma_f32_16x16x32_bf16 a[232:235],a[104:107],v[216:219],a[232:235]
v_mfma_f32_16x16x32_bf16 a[232:235],a[108:111],v[220:223],a[232:235]
v_mfma_f32_16x16x32_bf16 a[236:239],a[108:111],v[252:255],a[236:239]
buffer_load_dwordx4 v[68:71],v6,s[28:31],s76 offen offset:1024 sc1
v_mfma_f32_16x16x32_bf16 a[236:239],a[104:107],v[248:251],a[236:239]
ds_read_b64_tr_b16 a[104:105],v1 offset:20736
ds_read_b64_tr_b16 a[106:107],v1 offset:25856
v_mfma_f32_16x16x32_bf16 a[236:239],a[100:103],v[236:239],a[236:239]
ds_read_b64_tr_b16 a[100:101],v1 offset:10496
v_mfma_f32_16x16x32_bf16 a[236:239],a[96:99],v[232:235],a[236:239]
s_waitcnt lgkmcnt(3)
ds_read_b64_tr_b16 a[96:97],v1 offset:256
v_mfma_f32_16x16x32_bf16 a[240:243],a[112:115],v[136:139],a[240:243]
ds_read_b64_tr_b16 a[98:99],v1 offset:5376
v_mfma_f32_16x16x32_bf16 a[240:243],a[116:119],v[140:143],a[240:243]
buffer_load_dwordx4 v[72:75],v6,s[28:31],s76 offen offset:2048 sc1
v_mfma_f32_16x16x32_bf16 a[240:243],a[120:123],v[152:155],a[240:243]
ds_read_b64_tr_b16 a[102:103],v1 offset:15616
ds_read_b64_tr_b16 a[108:109],v1 offset:30976
v_mfma_f32_16x16x32_bf16 a[240:243],a[124:127],v[156:159],a[240:243]
ds_read_b64_tr_b16 a[110:111],v1 offset:36096
v_subrev_u32 v1,81920,v1
v_mfma_f32_16x16x32_bf16 a[244:247],a[124:127],v[188:191],a[244:247]
v_add_u32 v42,40960,v1
v_add_u32 v44,122880,v1
v_mfma_f32_16x16x32_bf16 a[244:247],a[120:123],v[184:187],a[244:247]
v_mfma_f32_16x16x32_bf16 a[244:247],a[116:119],v[172:175],a[244:247]
buffer_load_dwordx4 v[76:79],v6,s[28:31],s76 offen offset:3072 sc1
v_mfma_f32_16x16x32_bf16 a[244:247],a[112:115],v[168:171],a[244:247]
v_mfma_f32_16x16x32_bf16 a[248:251],a[112:115],v[200:203],a[248:251]
v_mfma_f32_16x16x32_bf16 a[248:251],a[116:119],v[204:207],a[248:251]
v_mfma_f32_16x16x32_bf16 a[248:251],a[120:123],v[216:219],a[248:251]
v_mfma_f32_16x16x32_bf16 a[248:251],a[124:127],v[220:223],a[248:251]
buffer_load_dwordx4 v[80:83],v6,s[28:31],s77 offen sc1
v_mfma_f32_16x16x32_bf16 a[252:255],a[124:127],v[252:255],a[252:255]
ds_read_b64_tr_b16 a[124:125],v2 offset:31008
ds_read_b64_tr_b16 a[126:127],v2 offset:36128
v_mfma_f32_16x16x32_bf16 a[252:255],a[120:123],v[248:251],a[252:255]
ds_read_b64_tr_b16 a[120:121],v2 offset:20768
v_mfma_f32_16x16x32_bf16 a[252:255],a[116:119],v[236:239],a[252:255]
ds_read_b64_tr_b16 a[116:117],v2 offset:10528
v_mfma_f32_16x16x32_bf16 a[252:255],a[112:115],v[232:235],a[252:255]
ds_read_b64_tr_b16 a[112:113],v2 offset:288
v_mfma_f32_16x16x32_bf16 a[0:3],a[128:131],v[136:139],a[0:3]
buffer_load_dwordx4 v[84:87],v6,s[28:31],s77 offen offset:1024 sc1
v_mfma_f32_16x16x32_bf16 a[0:3],a[132:135],v[140:143],a[0:3]
ds_read_b64_tr_b16 a[114:115],v2 offset:5408
ds_read_b64_tr_b16 a[118:119],v2 offset:15648
v_mfma_f32_16x16x32_bf16 a[0:3],a[136:139],v[152:155],a[0:3]
ds_read_b64_tr_b16 a[122:123],v2 offset:25888
v_subrev_u32 v2,81920,v2
v_mfma_f32_16x16x32_bf16 a[0:3],a[140:143],v[156:159],a[0:3]
v_add_u32 v43,40960,v2
v_add_u32 v45,122880,v2
v_mfma_f32_16x16x32_bf16 a[4:7],a[140:143],v[188:191],a[4:7]
v_mfma_f32_16x16x32_bf16 a[4:7],a[136:139],v[184:187],a[4:7]
buffer_load_dwordx4 v[88:91],v6,s[28:31],s77 offen offset:2048 sc1
v_mfma_f32_16x16x32_bf16 a[4:7],a[132:135],v[172:175],a[4:7]
v_mfma_f32_16x16x32_bf16 a[4:7],a[128:131],v[168:171],a[4:7]
v_mfma_f32_16x16x32_bf16 a[8:11],a[128:131],v[200:203],a[8:11]
v_mfma_f32_16x16x32_bf16 a[8:11],a[132:135],v[204:207],a[8:11]
v_mfma_f32_16x16x32_bf16 a[8:11],a[136:139],v[216:219],a[8:11]
buffer_load_dwordx4 v[92:95],v6,s[28:31],s77 offen offset:3072 sc1
v_mfma_f32_16x16x32_bf16 a[8:11],a[140:143],v[220:223],a[8:11]
v_mfma_f32_16x16x32_bf16 a[12:15],a[140:143],v[252:255],a[12:15]
ds_read_b64_tr_b16 a[140:141],v1 offset:30720
ds_read_b64_tr_b16 a[142:143],v1 offset:35840
v_mfma_f32_16x16x32_bf16 a[12:15],a[136:139],v[248:251],a[12:15]
ds_read_b64_tr_b16 a[136:137],v1 offset:20480
v_mfma_f32_16x16x32_bf16 a[12:15],a[132:135],v[236:239],a[12:15]
ds_read_b64_tr_b16 a[132:133],v1 offset:10240
v_mfma_f32_16x16x32_bf16 a[12:15],a[128:131],v[232:235],a[12:15]
buffer_load_dwordx4 v[96:99],v6,s[28:31],s78 offen sc1
v_mfma_f32_16x16x32_bf16 a[16:19],a[144:147],v[136:139],a[16:19]
ds_read_b64_tr_b16 a[128:129],v1
ds_read_b64_tr_b16 a[130:131],v1 offset:5120
v_mfma_f32_16x16x32_bf16 a[16:19],a[148:151],v[140:143],a[16:19]
ds_read_b64_tr_b16 a[134:135],v1 offset:15360
v_mfma_f32_16x16x32_bf16 a[16:19],a[152:155],v[152:155],a[16:19]
ds_read_b64_tr_b16 a[138:139],v1 offset:25600
v_mfma_f32_16x16x32_bf16 a[16:19],a[156:159],v[156:159],a[16:19]
v_mfma_f32_16x16x32_bf16 a[20:23],a[156:159],v[188:191],a[20:23]
buffer_load_dwordx4 v[100:103],v6,s[28:31],s78 offen offset:1024 sc1
v_mfma_f32_16x16x32_bf16 a[20:23],a[152:155],v[184:187],a[20:23]
v_mfma_f32_16x16x32_bf16 a[20:23],a[148:151],v[172:175],a[20:23]
v_mfma_f32_16x16x32_bf16 a[20:23],a[144:147],v[168:171],a[20:23]
v_mfma_f32_16x16x32_bf16 a[24:27],a[144:147],v[200:203],a[24:27]
v_mfma_f32_16x16x32_bf16 a[24:27],a[148:151],v[204:207],a[24:27]
buffer_load_dwordx4 v[104:107],v6,s[28:31],s78 offen offset:2048 sc1
v_mfma_f32_16x16x32_bf16 a[24:27],a[152:155],v[216:219],a[24:27]
v_mfma_f32_16x16x32_bf16 a[24:27],a[156:159],v[220:223],a[24:27]
v_mfma_f32_16x16x32_bf16 a[28:31],a[156:159],v[252:255],a[28:31]
ds_read_b64_tr_b16 a[156:157],v2 offset:30752
ds_read_b64_tr_b16 a[158:159],v2 offset:35872
v_mfma_f32_16x16x32_bf16 a[28:31],a[152:155],v[248:251],a[28:31]
ds_read_b64_tr_b16 a[152:153],v2 offset:20512
)A"
R"A(v_mfma_f32_16x16x32_bf16 a[28:31],a[148:151],v[236:239],a[28:31]
buffer_load_dwordx4 v[108:111],v6,s[28:31],s78 offen offset:3072 sc1
v_mfma_f32_16x16x32_bf16 a[28:31],a[144:147],v[232:235],a[28:31]
s_cmp_ge_u32 2,s71
s_cselect_b32 s80,7,5
s_mul_i32 s80,s80,163840
v_mfma_f32_16x16x32_bf16 a[32:35],a[160:163],v[136:139],a[32:35]
s_add_u32 s80,s80,s81
s_add_u32 s83,s80,20480
ds_read_b64_tr_b16 a[144:145],v2 offset:32
v_mfma_f32_16x16x32_bf16 a[32:35],a[164:167],v[140:143],a[32:35]
ds_read_b64_tr_b16 a[146:147],v2 offset:5152
ds_read_b64_tr_b16 a[148:149],v2 offset:10272
v_mfma_f32_16x16x32_bf16 a[32:35],a[168:171],v[152:155],a[32:35]
ds_read_b64_tr_b16 a[150:151],v2 offset:15392
v_mfma_f32_16x16x32_bf16 a[32:35],a[172:175],v[156:159],a[32:35]
buffer_load_dwordx4 v[112:115],v6,s[28:31],s79 offen sc1
v_mfma_f32_16x16x32_bf16 a[36:39],a[172:175],v[188:191],a[36:39]
ds_read_b64_tr_b16 a[154:155],v2 offset:25632
v_mfma_f32_16x16x32_bf16 a[36:39],a[168:171],v[184:187],a[36:39]
v_mfma_f32_16x16x32_bf16 a[36:39],a[164:167],v[172:175],a[36:39]
v_mfma_f32_16x16x32_bf16 a[36:39],a[160:163],v[168:171],a[36:39]
v_mfma_f32_16x16x32_bf16 a[40:43],a[160:163],v[200:203],a[40:43]
buffer_load_dwordx4 v[116:119],v6,s[28:31],s79 offen offset:1024 sc1
v_mfma_f32_16x16x32_bf16 a[40:43],a[164:167],v[204:207],a[40:43]
v_mfma_f32_16x16x32_bf16 a[40:43],a[168:171],v[216:219],a[40:43]
v_mfma_f32_16x16x32_bf16 a[40:43],a[172:175],v[220:223],a[40:43]
v_mfma_f32_16x16x32_bf16 a[44:47],a[172:175],v[252:255],a[44:47]
ds_read_b64_tr_b16 a[172:173],v1 offset:30784
ds_read_b64_tr_b16 a[174:175],v1 offset:35904
v_mfma_f32_16x16x32_bf16 a[44:47],a[168:171],v[248:251],a[44:47]
buffer_load_dwordx4 v[120:123],v6,s[28:31],s79 offen offset:2048 sc1
v_mfma_f32_16x16x32_bf16 a[44:47],a[164:167],v[236:239],a[44:47]
ds_read_b64_tr_b16 a[164:165],v1 offset:10304
ds_read_b64_tr_b16 a[166:167],v1 offset:15424
v_mfma_f32_16x16x32_bf16 a[44:47],a[160:163],v[232:235],a[44:47]
ds_read_b64_tr_b16 a[160:161],v1 offset:64
v_mfma_f32_16x16x32_bf16 a[48:51],a[176:179],v[136:139],a[48:51]
ds_read_b64_tr_b16 a[162:163],v1 offset:5184
v_mfma_f32_16x16x32_bf16 a[48:51],a[180:183],v[140:143],a[48:51]
ds_read_b64_tr_b16 a[168:169],v1 offset:20544
v_mfma_f32_16x16x32_bf16 a[48:51],a[184:187],v[152:155],a[48:51]
buffer_load_dwordx4 v[124:127],v6,s[28:31],s79 offen offset:3072 sc1
v_mfma_f32_16x16x32_bf16 a[48:51],a[188:191],v[156:159],a[48:51]
ds_read_b64_tr_b16 a[170:171],v1 offset:25664
v_mfma_f32_16x16x32_bf16 a[52:55],a[188:191],v[188:191],a[52:55]
s_add_u32 m0,s82,122880
v_mfma_f32_16x16x32_bf16 a[52:55],a[184:187],v[184:187],a[52:55]
v_mfma_f32_16x16x32_bf16 a[52:55],a[180:183],v[172:175],a[52:55]
v_mfma_f32_16x16x32_bf16 a[52:55],a[176:179],v[168:171],a[52:55]
buffer_load_dwordx4 v7,s[72:75],s80 offen lds
v_mfma_f32_16x16x32_bf16 a[56:59],a[176:179],v[200:203],a[56:59]
v_mfma_f32_16x16x32_bf16 a[56:59],a[180:183],v[204:207],a[56:59]
v_mfma_f32_16x16x32_bf16 a[56:59],a[184:187],v[216:219],a[56:59]
v_mfma_f32_16x16x32_bf16 a[56:59],a[188:191],v[220:223],a[56:59]
v_mfma_f32_16x16x32_bf16 a[60:63],a[188:191],v[252:255],a[60:63]
buffer_load_dwordx4 v8,s[72:75],s80 offen offset:1024 lds
v_mfma_f32_16x16x32_bf16 a[60:63],a[184:187],v[248:251],a[60:63]
ds_read_b64_tr_b16 a[184:185],v2 offset:20576
ds_read_b64_tr_b16 a[186:187],v2 offset:25696
v_mfma_f32_16x16x32_bf16 a[60:63],a[180:183],v[236:239],a[60:63]
ds_read_b64_tr_b16 a[180:181],v2 offset:10336
v_mfma_f32_16x16x32_bf16 a[60:63],a[176:179],v[232:235],a[60:63]
s_waitcnt lgkmcnt(3)
ds_read_b64_tr_b16 a[176:177],v2 offset:96
v_mfma_f32_16x16x32_bf16 a[64:67],a[96:99],v[136:139],a[64:67]
ds_read_b64_tr_b16 a[178:179],v2 offset:5216
v_mfma_f32_16x16x32_bf16 a[64:67],a[100:103],v[140:143],a[64:67]
buffer_load_dwordx4 v9,s[72:75],s80 offen offset:2048 lds
v_mfma_f32_16x16x32_bf16 a[64:67],a[104:107],v[152:155],a[64:67]
ds_read_b64_tr_b16 a[182:183],v2 offset:15456
ds_read_b64_tr_b16 a[188:189],v2 offset:30816
v_mfma_f32_16x16x32_bf16 a[64:67],a[108:111],v[156:159],a[64:67]
ds_read_b64_tr_b16 a[190:191],v2 offset:35936
v_mfma_f32_16x16x32_bf16 a[68:71],a[108:111],v[188:191],a[68:71]
v_mfma_f32_16x16x32_bf16 a[68:71],a[104:107],v[184:187],a[68:71]
v_mfma_f32_16x16x32_bf16 a[68:71],a[100:103],v[172:175],a[68:71]
buffer_load_dwordx4 v10,s[72:75],s80 offen offset:3072 lds
v_mfma_f32_16x16x32_bf16 a[68:71],a[96:99],v[168:171],a[68:71]
v_mfma_f32_16x16x32_bf16 a[72:75],a[96:99],v[200:203],a[72:75]
s_addk_i32 m0,4096
v_mfma_f32_16x16x32_bf16 a[72:75],a[100:103],v[204:207],a[72:75]
v_mfma_f32_16x16x32_bf16 a[72:75],a[104:107],v[216:219],a[72:75]
s_cmp_ge_u32 2,s71
s_cselect_b32 s76,7,5
v_mfma_f32_16x16x32_bf16 a[72:75],a[108:111],v[220:223],a[72:75]
buffer_load_dwordx4 v11,s[72:75],s80 offen lds
v_mfma_f32_16x16x32_bf16 a[76:79],a[108:111],v[252:255],a[76:79]
s_bitcmp1_b32 s54,s76
s_cselect_b32 s77,8192,0
s_lshl_b32 s76,s76,17
v_mfma_f32_16x16x32_bf16 a[76:79],a[104:107],v[248:251],a[76:79]
s_addk_i32 m0,1024
s_add_u32 s76,s76,s77
s_add_u32 s77,s76,4096
v_mfma_f32_16x16x32_bf16 a[76:79],a[100:103],v[236:239],a[76:79]
s_add_u32 s78,s76,16384
s_add_u32 s79,s76,20480
s_max_i32 s76,s76,s18
v_mfma_f32_16x16x32_bf16 a[76:79],a[96:99],v[232:235],a[76:79]
s_max_i32 s77,s77,s19
ds_read_b64_tr_b16 a[96:97],v1 offset:128
ds_read_b64_tr_b16 a[98:99],v1 offset:5248
v_mfma_f32_16x16x32_bf16 a[80:83],a[112:115],v[136:139],a[80:83]
s_max_i32 s78,s78,s100
buffer_load_dwordx4 v7,s[72:75],s83 offen lds
v_mfma_f32_16x16x32_bf16 a[80:83],a[116:119],v[140:143],a[80:83]
s_max_i32 s79,s79,s101
ds_read_b64_tr_b16 a[100:101],v1 offset:10368
ds_read_b64_tr_b16 a[102:103],v1 offset:15488
v_mfma_f32_16x16x32_bf16 a[80:83],a[120:123],v[152:155],a[80:83]
ds_read_b64_tr_b16 a[104:105],v1 offset:20608
v_mfma_f32_16x16x32_bf16 a[80:83],a[124:127],v[156:159],a[80:83]
ds_read_b64_tr_b16 a[106:107],v1 offset:25728
v_mfma_f32_16x16x32_bf16 a[84:87],a[124:127],v[188:191],a[84:87]
ds_read_b64_tr_b16 a[108:109],v1 offset:30848
v_mfma_f32_16x16x32_bf16 a[84:87],a[120:123],v[184:187],a[84:87]
buffer_load_dwordx4 v8,s[72:75],s83 offen offset:1024 lds
v_mfma_f32_16x16x32_bf16 a[84:87],a[116:119],v[172:175],a[84:87]
ds_read_b64_tr_b16 a[110:111],v1 offset:35968
v_mfma_f32_16x16x32_bf16 a[84:87],a[112:115],v[168:171],a[84:87]
v_mfma_f32_16x16x32_bf16 a[88:91],a[112:115],v[200:203],a[88:91]
v_mfma_f32_16x16x32_bf16 a[88:91],a[116:119],v[204:207],a[88:91]
v_mfma_f32_16x16x32_bf16 a[88:91],a[120:123],v[216:219],a[88:91]
buffer_load_dwordx4 v9,s[72:75],s83 offen offset:2048 lds
v_mfma_f32_16x16x32_bf16 a[88:91],a[124:127],v[220:223],a[88:91]
v_mfma_f32_16x16x32_bf16 a[92:95],a[124:127],v[252:255],a[92:95]
ds_read_b64_tr_b16 a[124:125],v2 offset:30880
ds_read_b64_tr_b16 a[126:127],v2 offset:36000
v_mfma_f32_16x16x32_bf16 a[92:95],a[120:123],v[248:251],a[92:95]
ds_read_b64_tr_b16 a[120:121],v2 offset:20640
v_mfma_f32_16x16x32_bf16 a[92:95],a[116:119],v[236:239],a[92:95]
ds_read_b64_tr_b16 a[116:117],v2 offset:10400
v_mfma_f32_16x16x32_bf16 a[92:95],a[112:115],v[232:235],a[92:95]
buffer_load_dwordx4 v10,s[72:75],s83 offen offset:3072 lds
v_mfma_f32_16x16x32_bf16 a[192:195],a[128:131],v[128:131],a[192:195]
ds_read_b64_tr_b16 a[112:113],v2 offset:160
ds_read_b64_tr_b16 a[114:115],v2 offset:5280
v_mfma_f32_16x16x32_bf16 a[192:195],a[132:135],v[160:163],a[192:195]
s_addk_i32 m0,4096
ds_read_b64_tr_b16 a[118:119],v2 offset:15520
v_mfma_f32_16x16x32_bf16 a[192:195],a[136:139],v[192:195],a[192:195]
ds_read_b64_tr_b16 a[122:123],v2 offset:25760
v_mfma_f32_16x16x32_bf16 a[192:195],a[140:143],v[224:227],a[192:195]
buffer_load_dwordx4 v[136:139],v6,s[28:31],s76 offen sc1
v_mfma_f32_16x16x32_bf16 a[196:199],a[140:143],v[228:231],a[196:199]
v_mfma_f32_16x16x32_bf16 a[196:199],a[136:139],v[196:199],a[196:199]
v_mfma_f32_16x16x32_bf16 a[196:199],a[132:135],v[164:167],a[196:199]
v_mfma_f32_16x16x32_bf16 a[196:199],a[128:131],v[132:135],a[196:199]
s_waitcnt vmcnt(14)
buffer_load_dwordx4 v11,s[72:75],s83 offen lds
v_mfma_f32_16x16x32_bf16 a[200:203],a[128:131],v[144:147],a[200:203]
ds_read_b64_tr_b16 a[128:129],v1 offset:192
ds_read_b64_tr_b16 a[130:131],v1 offset:5312
v_mfma_f32_16x16x32_bf16 a[200:203],a[132:135],v[176:179],a[200:203]
ds_read_b64_tr_b16 a[132:133],v1 offset:10432
v_mfma_f32_16x16x32_bf16 a[200:203],a[136:139],v[208:211],a[200:203]
ds_read_b64_tr_b16 a[134:135],v1 offset:15552
v_mfma_f32_16x16x32_bf16 a[200:203],a[140:143],v[240:243],a[200:203]
buffer_load_dwordx4 v[140:143],v6,s[28:31],s76 offen offset:1024 sc1
v_mfma_f32_16x16x32_bf16 a[208:211],a[144:147],v[128:131],a[208:211]
ds_read_b64_tr_b16 a[136:137],v1 offset:20672
ds_read_b64_tr_b16 a[138:139],v1 offset:25792
v_mfma_f32_16x16x32_bf16 a[208:211],a[148:151],v[160:163],a[208:211]
ds_read_b64_tr_b16 a[140:141],v1 offset:30912
v_mfma_f32_16x16x32_bf16 a[208:211],a[152:155],v[192:195],a[208:211]
ds_read_b64_tr_b16 a[142:143],v1 offset:36032
v_mfma_f32_16x16x32_bf16 a[208:211],a[156:159],v[224:227],a[208:211]
buffer_load_dwordx4 v[152:155],v6,s[28:31],s76 offen offset:2048 sc1
v_mfma_f32_16x16x32_bf16 a[212:215],a[156:159],v[228:231],a[212:215]
v_mfma_f32_16x16x32_bf16 a[212:215],a[152:155],v[196:199],a[212:215]
v_mfma_f32_16x16x32_bf16 a[212:215],a[148:151],v[164:167],a[212:215]
v_mfma_f32_16x16x32_bf16 a[212:215],a[144:147],v[132:135],a[212:215]
buffer_load_dwordx4 v[156:159],v6,s[28:31],s76 offen offset:3072 sc1
v_mfma_f32_16x16x32_bf16 a[216:219],a[144:147],v[144:147],a[216:219]
s_add_u32 s76,s70,3
ds_read_b64_tr_b16 a[144:145],v2 offset:224
ds_read_b64_tr_b16 a[146:147],v2 offset:5344
v_mfma_f32_16x16x32_bf16 a[216:219],a[148:151],v[176:179],a[216:219]
s_and_b32 s76,s76,3
s_lshl_b32 s76,s76,21
ds_read_b64_tr_b16 a[148:149],v2 offset:10464
v_mfma_f32_16x16x32_bf16 a[216:219],a[152:155],v[208:211],a[216:219]
s_add_u32 s76,s76,0
s_max_i32 s76,s76,s101
ds_read_b64_tr_b16 a[150:151],v2 offset:15584
v_mfma_f32_16x16x32_bf16 a[216:219],a[156:159],v[240:243],a[216:219]
buffer_load_dwordx4 v[168:171],v6,s[28:31],s77 offen sc1
v_mfma_f32_16x16x32_bf16 a[224:227],a[160:163],v[128:131],a[224:227]
ds_read_b64_tr_b16 a[152:153],v2 offset:20704
ds_read_b64_tr_b16 a[154:155],v2 offset:25824
v_mfma_f32_16x16x32_bf16 a[224:227],a[164:167],v[160:163],a[224:227]
ds_read_b64_tr_b16 a[156:157],v2 offset:30944
v_mfma_f32_16x16x32_bf16 a[224:227],a[168:171],v[192:195],a[224:227]
ds_read_b64_tr_b16 a[158:159],v2 offset:36064
v_mfma_f32_16x16x32_bf16 a[224:227],a[172:175],v[224:227],a[224:227]
buffer_load_dwordx4 v[172:175],v6,s[28:31],s77 offen offset:1024 sc1
v_mfma_f32_16x16x32_bf16 a[228:231],a[172:175],v[228:231],a[228:231]
v_mfma_f32_16x16x32_bf16 a[228:231],a[168:171],v[196:199],a[228:231]
v_mfma_f32_16x16x32_bf16 a[228:231],a[164:167],v[164:167],a[228:231]
v_mfma_f32_16x16x32_bf16 a[228:231],a[160:163],v[132:135],a[228:231]
v_mfma_f32_16x16x32_bf16 a[232:235],a[160:163],v[144:147],a[232:235]
buffer_load_dwordx4 v[184:187],v6,s[28:31],s77 offen offset:2048 sc1
v_mfma_f32_16x16x32_bf16 a[232:235],a[164:167],v[176:179],a[232:235]
ds_read_b64_tr_b16 a[160:161],v1 offset:256
ds_read_b64_tr_b16 a[162:163],v1 offset:5376
v_mfma_f32_16x16x32_bf16 a[232:235],a[168:171],v[208:211],a[232:235]
ds_read_b64_tr_b16 a[164:165],v1 offset:10496
v_mfma_f32_16x16x32_bf16 a[232:235],a[172:175],v[240:243],a[232:235]
s_waitcnt lgkmcnt(3)
ds_read_b64_tr_b16 a[166:167],v1 offset:15616
v_mfma_f32_16x16x32_bf16 a[240:243],a[176:179],v[128:131],a[240:243]
ds_read_b64_tr_b16 a[168:169],v1 offset:20736
v_mfma_f32_16x16x32_bf16 a[240:243],a[180:183],v[160:163],a[240:243]
buffer_load_dwordx4 v[188:191],v6,s[28:31],s77 offen offset:3072 sc1
v_mfma_f32_16x16x32_bf16 a[240:243],a[184:187],v[192:195],a[240:243]
s_add_u32 s77,s70,2
ds_read_b64_tr_b16 a[170:171],v1 offset:25856
ds_read_b64_tr_b16 a[172:173],v1 offset:30976
v_mfma_f32_16x16x32_bf16 a[240:243],a[188:191],v[224:227],a[240:243]
s_and_b32 s77,s77,3
s_lshl_b32 s77,s77,21
ds_read_b64_tr_b16 a[174:175],v1 offset:36096
v_mfma_f32_16x16x32_bf16 a[244:247],a[188:191],v[228:231],a[244:247]
s_add_u32 s77,s77,131072
)A"
R"A(s_max_i32 s77,s77,s101
v_mfma_f32_16x16x32_bf16 a[244:247],a[184:187],v[196:199],a[244:247]
v_mfma_f32_16x16x32_bf16 a[244:247],a[180:183],v[164:167],a[244:247]
buffer_load_dwordx4 v[200:203],v6,s[28:31],s78 offen sc1
v_mfma_f32_16x16x32_bf16 a[244:247],a[176:179],v[132:135],a[244:247]
v_mfma_f32_16x16x32_bf16 a[248:251],a[176:179],v[144:147],a[248:251]
ds_read_b64_tr_b16 a[176:177],v2 offset:288
ds_read_b64_tr_b16 a[178:179],v2 offset:5408
v_mfma_f32_16x16x32_bf16 a[248:251],a[180:183],v[176:179],a[248:251]
ds_read_b64_tr_b16 a[180:181],v2 offset:10528
v_mfma_f32_16x16x32_bf16 a[248:251],a[184:187],v[208:211],a[248:251]
ds_read_b64_tr_b16 a[182:183],v2 offset:15648
v_mfma_f32_16x16x32_bf16 a[248:251],a[188:191],v[240:243],a[248:251]
buffer_load_dwordx4 v[204:207],v6,s[28:31],s78 offen offset:1024 sc1
v_mfma_f32_16x16x32_bf16 a[0:3],a[96:99],v[128:131],a[0:3]
ds_read_b64_tr_b16 a[184:185],v2 offset:20768
ds_read_b64_tr_b16 a[186:187],v2 offset:25888
v_mfma_f32_16x16x32_bf16 a[0:3],a[100:103],v[160:163],a[0:3]
ds_read_b64_tr_b16 a[188:189],v2 offset:31008
v_mfma_f32_16x16x32_bf16 a[0:3],a[104:107],v[192:195],a[0:3]
ds_read_b64_tr_b16 a[190:191],v2 offset:36128
v_mfma_f32_16x16x32_bf16 a[0:3],a[108:111],v[224:227],a[0:3]
v_mfma_f32_16x16x32_bf16 a[4:7],a[108:111],v[228:231],a[4:7]
buffer_load_dwordx4 v[216:219],v6,s[28:31],s78 offen offset:2048 sc1
v_mfma_f32_16x16x32_bf16 a[4:7],a[104:107],v[196:199],a[4:7]
v_mfma_f32_16x16x32_bf16 a[4:7],a[100:103],v[164:167],a[4:7]
v_mfma_f32_16x16x32_bf16 a[4:7],a[96:99],v[132:135],a[4:7]
v_mfma_f32_16x16x32_bf16 a[8:11],a[96:99],v[144:147],a[8:11]
v_mfma_f32_16x16x32_bf16 a[8:11],a[100:103],v[176:179],a[8:11]
buffer_load_dwordx4 v[220:223],v6,s[28:31],s78 offen offset:3072 sc1
v_mfma_f32_16x16x32_bf16 a[8:11],a[104:107],v[208:211],a[8:11]
s_add_u32 s78,s70,1
s_and_b32 s78,s78,3
s_lshl_b32 s78,s78,21
v_mfma_f32_16x16x32_bf16 a[8:11],a[108:111],v[240:243],a[8:11]
s_add_u32 s78,s78,262144
s_max_i32 s78,s78,s101
v_mfma_f32_16x16x32_bf16 a[16:19],a[112:115],v[128:131],a[16:19]
v_mfma_f32_16x16x32_bf16 a[16:19],a[116:119],v[160:163],a[16:19]
v_mfma_f32_16x16x32_bf16 a[16:19],a[120:123],v[192:195],a[16:19]
buffer_load_dwordx4 v[232:235],v6,s[28:31],s79 offen sc1
v_mfma_f32_16x16x32_bf16 a[16:19],a[124:127],v[224:227],a[16:19]
v_mfma_f32_16x16x32_bf16 a[20:23],a[124:127],v[228:231],a[20:23]
v_mfma_f32_16x16x32_bf16 a[20:23],a[120:123],v[196:199],a[20:23]
v_mfma_f32_16x16x32_bf16 a[20:23],a[116:119],v[164:167],a[20:23]
v_mfma_f32_16x16x32_bf16 a[20:23],a[112:115],v[132:135],a[20:23]
buffer_load_dwordx4 v[236:239],v6,s[28:31],s79 offen offset:1024 sc1
v_mfma_f32_16x16x32_bf16 a[24:27],a[112:115],v[144:147],a[24:27]
v_mfma_f32_16x16x32_bf16 a[24:27],a[116:119],v[176:179],a[24:27]
v_mfma_f32_16x16x32_bf16 a[24:27],a[120:123],v[208:211],a[24:27]
v_mfma_f32_16x16x32_bf16 a[24:27],a[124:127],v[240:243],a[24:27]
v_mfma_f32_16x16x32_bf16 a[32:35],a[128:131],v[128:131],a[32:35]
buffer_load_dwordx4 v[248:251],v6,s[28:31],s79 offen offset:2048 sc1
v_mfma_f32_16x16x32_bf16 a[32:35],a[132:135],v[160:163],a[32:35]
v_mfma_f32_16x16x32_bf16 a[32:35],a[136:139],v[192:195],a[32:35]
v_mfma_f32_16x16x32_bf16 a[32:35],a[140:143],v[224:227],a[32:35]
v_mfma_f32_16x16x32_bf16 a[36:39],a[140:143],v[228:231],a[36:39]
v_mfma_f32_16x16x32_bf16 a[36:39],a[136:139],v[196:199],a[36:39]
buffer_load_dwordx4 v[252:255],v6,s[28:31],s79 offen offset:3072 sc1
v_mfma_f32_16x16x32_bf16 a[36:39],a[132:135],v[164:167],a[36:39]
v_mfma_f32_16x16x32_bf16 a[36:39],a[128:131],v[132:135],a[36:39]
v_mfma_f32_16x16x32_bf16 a[40:43],a[128:131],v[144:147],a[40:43]
v_mfma_f32_16x16x32_bf16 a[40:43],a[132:135],v[176:179],a[40:43]
v_mfma_f32_16x16x32_bf16 a[40:43],a[136:139],v[208:211],a[40:43]
s_waitcnt vmcnt(15)
v_mfma_f32_16x16x32_bf16 a[40:43],a[140:143],v[240:243],a[40:43]
s_barrier
ds_read_b64_tr_b16 a[96:97],v42
ds_read_b64_tr_b16 a[98:99],v42 offset:5120
v_mfma_f32_16x16x32_bf16 a[48:51],a[144:147],v[128:131],a[48:51]
ds_read_b64_tr_b16 a[100:101],v42 offset:10240
v_mfma_f32_16x16x32_bf16 a[48:51],a[148:151],v[160:163],a[48:51]
ds_read_b64_tr_b16 a[102:103],v42 offset:15360
v_mfma_f32_16x16x32_bf16 a[48:51],a[152:155],v[192:195],a[48:51]
ds_read_b64_tr_b16 a[104:105],v42 offset:20480
v_mfma_f32_16x16x32_bf16 a[48:51],a[156:159],v[224:227],a[48:51]
ds_read_b64_tr_b16 a[106:107],v42 offset:25600
ds_read_b64_tr_b16 a[108:109],v42 offset:30720
v_mfma_f32_16x16x32_bf16 a[52:55],a[156:159],v[228:231],a[52:55]
ds_read_b64_tr_b16 a[110:111],v42 offset:35840
v_mfma_f32_16x16x32_bf16 a[52:55],a[152:155],v[196:199],a[52:55]
ds_read_b64_tr_b16 a[112:113],v44
v_mfma_f32_16x16x32_bf16 a[52:55],a[148:151],v[164:167],a[52:55]
ds_read_b64_tr_b16 a[114:115],v44 offset:5120
v_mfma_f32_16x16x32_bf16 a[52:55],a[144:147],v[132:135],a[52:55]
ds_read_b64_tr_b16 a[116:117],v44 offset:10240
ds_read_b64_tr_b16 a[118:119],v44 offset:15360
v_mfma_f32_16x16x32_bf16 a[56:59],a[144:147],v[144:147],a[56:59]
ds_read_b64_tr_b16 a[120:121],v44 offset:20480
v_mfma_f32_16x16x32_bf16 a[56:59],a[148:151],v[176:179],a[56:59]
ds_read_b64_tr_b16 a[122:123],v44 offset:25600
v_mfma_f32_16x16x32_bf16 a[56:59],a[152:155],v[208:211],a[56:59]
ds_read_b64_tr_b16 a[124:125],v44 offset:30720
v_mfma_f32_16x16x32_bf16 a[56:59],a[156:159],v[240:243],a[56:59]
s_waitcnt lgkmcnt(8)
ds_read_b64_tr_b16 a[126:127],v44 offset:35840
ds_read_b64_tr_b16 a[128:129],v43 offset:32
v_mfma_f32_16x16x32_bf16 a[64:67],a[160:163],v[128:131],a[64:67]
ds_read_b64_tr_b16 a[130:131],v43 offset:5152
v_mfma_f32_16x16x32_bf16 a[64:67],a[164:167],v[160:163],a[64:67]
ds_read_b64_tr_b16 a[132:133],v43 offset:10272
v_mfma_f32_16x16x32_bf16 a[64:67],a[168:171],v[192:195],a[64:67]
ds_read_b64_tr_b16 a[134:135],v43 offset:15392
v_mfma_f32_16x16x32_bf16 a[64:67],a[172:175],v[224:227],a[64:67]
ds_read_b64_tr_b16 a[136:137],v43 offset:20512
ds_read_b64_tr_b16 a[138:139],v43 offset:25632
v_mfma_f32_16x16x32_bf16 a[68:71],a[172:175],v[228:231],a[68:71]
ds_read_b64_tr_b16 a[140:141],v43 offset:30752
v_mfma_f32_16x16x32_bf16 a[68:71],a[168:171],v[196:199],a[68:71]
ds_read_b64_tr_b16 a[142:143],v43 offset:35872
v_mfma_f32_16x16x32_bf16 a[68:71],a[164:167],v[164:167],a[68:71]
ds_read_b64_tr_b16 a[144:145],v45 offset:32
v_mfma_f32_16x16x32_bf16 a[68:71],a[160:163],v[132:135],a[68:71]
ds_read_b64_tr_b16 a[146:147],v45 offset:5152
ds_read_b64_tr_b16 a[148:149],v45 offset:10272
v_mfma_f32_16x16x32_bf16 a[72:75],a[160:163],v[144:147],a[72:75]
ds_read_b64_tr_b16 a[150:151],v45 offset:15392
v_mfma_f32_16x16x32_bf16 a[72:75],a[164:167],v[176:179],a[72:75]
ds_read_b64_tr_b16 a[152:153],v45 offset:20512
v_mfma_f32_16x16x32_bf16 a[72:75],a[168:171],v[208:211],a[72:75]
ds_read_b64_tr_b16 a[154:155],v45 offset:25632
v_mfma_f32_16x16x32_bf16 a[72:75],a[172:175],v[240:243],a[72:75]
ds_read_b64_tr_b16 a[156:157],v45 offset:30752
ds_read_b64_tr_b16 a[158:159],v45 offset:35872
v_mfma_f32_16x16x32_bf16 a[80:83],a[176:179],v[128:131],a[80:83]
ds_read_b64_tr_b16 a[160:161],v42 offset:64
v_mfma_f32_16x16x32_bf16 a[80:83],a[180:183],v[160:163],a[80:83]
ds_read_b64_tr_b16 a[162:163],v42 offset:5184
v_mfma_f32_16x16x32_bf16 a[80:83],a[184:187],v[192:195],a[80:83]
ds_read_b64_tr_b16 a[164:165],v42 offset:10304
v_mfma_f32_16x16x32_bf16 a[80:83],a[188:191],v[224:227],a[80:83]
ds_read_b64_tr_b16 a[166:167],v42 offset:15424
ds_read_b64_tr_b16 a[168:169],v42 offset:20544
v_mfma_f32_16x16x32_bf16 a[84:87],a[188:191],v[228:231],a[84:87]
ds_read_b64_tr_b16 a[170:171],v42 offset:25664
v_mfma_f32_16x16x32_bf16 a[84:87],a[184:187],v[196:199],a[84:87]
ds_read_b64_tr_b16 a[172:173],v42 offset:30784
v_mfma_f32_16x16x32_bf16 a[84:87],a[180:183],v[164:167],a[84:87]
ds_read_b64_tr_b16 a[174:175],v42 offset:35904
v_mfma_f32_16x16x32_bf16 a[84:87],a[176:179],v[132:135],a[84:87]
v_mfma_f32_16x16x32_bf16 a[88:91],a[176:179],v[144:147],a[88:91]
ds_read_b64_tr_b16 a[176:177],v44 offset:64
ds_read_b64_tr_b16 a[178:179],v44 offset:5184
v_mfma_f32_16x16x32_bf16 a[88:91],a[180:183],v[176:179],a[88:91]
ds_read_b64_tr_b16 a[180:181],v44 offset:10304
v_mfma_f32_16x16x32_bf16 a[88:91],a[184:187],v[208:211],a[88:91]
ds_read_b64_tr_b16 a[182:183],v44 offset:15424
v_mfma_f32_16x16x32_bf16 a[88:91],a[188:191],v[240:243],a[88:91]
ds_read_b64_tr_b16 a[184:185],v44 offset:20544
v_mfma_f32_16x16x32_bf16 a[192:195],a[96:99],v[64:67],a[192:195]
ds_read_b64_tr_b16 a[186:187],v44 offset:25664
ds_read_b64_tr_b16 a[188:189],v44 offset:30784
v_mfma_f32_16x16x32_bf16 a[192:195],a[100:103],v[68:71],a[192:195]
ds_read_b64_tr_b16 a[190:191],v44 offset:35904
v_mfma_f32_16x16x32_bf16 a[192:195],a[104:107],v[72:75],a[192:195]
s_waitcnt lgkmcnt(8)
v_mfma_f32_16x16x32_bf16 a[192:195],a[108:111],v[76:79],a[192:195]
v_mfma_f32_16x16x32_bf16 a[196:199],a[108:111],v[92:95],a[196:199]
v_mfma_f32_16x16x32_bf16 a[196:199],a[104:107],v[88:91],a[196:199]
v_mfma_f32_16x16x32_bf16 a[196:199],a[100:103],v[84:87],a[196:199]
v_mfma_f32_16x16x32_bf16 a[196:199],a[96:99],v[80:83],a[196:199]
v_mfma_f32_16x16x32_bf16 a[200:203],a[96:99],v[96:99],a[200:203]
ds_read_b64_tr_b16 a[96:97],v43 offset:96
ds_read_b64_tr_b16 a[98:99],v43 offset:5216
v_mfma_f32_16x16x32_bf16 a[200:203],a[100:103],v[100:103],a[200:203]
ds_read_b64_tr_b16 a[100:101],v43 offset:10336
v_mfma_f32_16x16x32_bf16 a[200:203],a[104:107],v[104:107],a[200:203]
ds_read_b64_tr_b16 a[102:103],v43 offset:15456
v_mfma_f32_16x16x32_bf16 a[200:203],a[108:111],v[108:111],a[200:203]
ds_read_b64_tr_b16 a[104:105],v43 offset:20576
v_mfma_f32_16x16x32_bf16 a[192:195],a[112:115],v[136:139],a[192:195]
s_waitcnt vmcnt(3)
ds_read_b64_tr_b16 a[106:107],v43 offset:25696
ds_read_b64_tr_b16 a[108:109],v43 offset:30816
v_mfma_f32_16x16x32_bf16 a[192:195],a[116:119],v[140:143],a[192:195]
ds_read_b64_tr_b16 a[110:111],v43 offset:35936
v_mfma_f32_16x16x32_bf16 a[192:195],a[120:123],v[152:155],a[192:195]
v_mfma_f32_16x16x32_bf16 a[192:195],a[124:127],v[156:159],a[192:195]
v_mfma_f32_16x16x32_bf16 a[196:199],a[124:127],v[188:191],a[196:199]
v_mfma_f32_16x16x32_bf16 a[196:199],a[120:123],v[184:187],a[196:199]
v_mfma_f32_16x16x32_bf16 a[196:199],a[116:119],v[172:175],a[196:199]
v_mfma_f32_16x16x32_bf16 a[196:199],a[112:115],v[168:171],a[196:199]
v_mfma_f32_16x16x32_bf16 a[200:203],a[112:115],v[200:203],a[200:203]
ds_read_b64_tr_b16 a[112:113],v45 offset:96
ds_read_b64_tr_b16 a[114:115],v45 offset:5216
v_mfma_f32_16x16x32_bf16 a[200:203],a[116:119],v[204:207],a[200:203]
v_accvgpr_read_b32 v128,a192
v_accvgpr_read_b32 v129,a193
v_mfma_f32_16x16x32_bf16 a[200:203],a[120:123],v[216:219],a[200:203]
v_cvt_pk_bf16_f32 v128,v128,v129
v_accvgpr_read_b32 v130,a194
v_mfma_f32_16x16x32_bf16 a[200:203],a[124:127],v[220:223],a[200:203]
v_accvgpr_read_b32 v131,a195
v_cvt_pk_bf16_f32 v129,v130,v131
v_mfma_f32_16x16x32_bf16 a[208:211],a[128:131],v[64:67],a[208:211]
ds_read_b64_tr_b16 a[116:117],v45 offset:10336
ds_read_b64_tr_b16 a[118:119],v45 offset:15456
v_mfma_f32_16x16x32_bf16 a[208:211],a[132:135],v[68:71],a[208:211]
v_accvgpr_read_b32 v132,a196
v_accvgpr_read_b32 v133,a197
v_mfma_f32_16x16x32_bf16 a[208:211],a[136:139],v[72:75],a[208:211]
v_cvt_pk_bf16_f32 v132,v132,v133
v_accvgpr_read_b32 v134,a198
v_mfma_f32_16x16x32_bf16 a[208:211],a[140:143],v[76:79],a[208:211]
v_accvgpr_read_b32 v135,a199
v_cvt_pk_bf16_f32 v133,v134,v135
v_mfma_f32_16x16x32_bf16 a[212:215],a[140:143],v[92:95],a[212:215]
ds_read_b64_tr_b16 a[120:121],v45 offset:20576
ds_read_b64_tr_b16 a[122:123],v45 offset:25696
v_mfma_f32_16x16x32_bf16 a[212:215],a[136:139],v[88:91],a[212:215]
ds_read_b64_tr_b16 a[124:125],v45 offset:30816
v_accvgpr_read_b32 v144,a200
v_mfma_f32_16x16x32_bf16 a[212:215],a[132:135],v[84:87],a[212:215]
ds_read_b64_tr_b16 a[126:127],v45 offset:35936
v_accvgpr_read_b32 v145,a201
v_mfma_f32_16x16x32_bf16 a[212:215],a[128:131],v[80:83],a[212:215]
v_cvt_pk_bf16_f32 v144,v144,v145
v_accvgpr_read_b32 v146,a202
v_mfma_f32_16x16x32_bf16 a[216:219],a[128:131],v[96:99],a[216:219]
v_accvgpr_read_b32 v147,a203
v_cvt_pk_bf16_f32 v145,v146,v147
v_mfma_f32_16x16x32_bf16 a[216:219],a[132:135],v[100:103],a[216:219]
)A"
R"A(ds_read_b64_tr_b16 a[128:129],v42 offset:128
ds_read_b64_tr_b16 a[130:131],v42 offset:5248
v_mfma_f32_16x16x32_bf16 a[216:219],a[136:139],v[104:107],a[216:219]
ds_read_b64_tr_b16 a[132:133],v42 offset:10368
v_mfma_f32_16x16x32_bf16 a[216:219],a[140:143],v[108:111],a[216:219]
ds_read_b64_tr_b16 a[134:135],v42 offset:15488
v_mfma_f32_16x16x32_bf16 a[208:211],a[144:147],v[136:139],a[208:211]
ds_read_b64_tr_b16 a[136:137],v42 offset:20608
v_mfma_f32_16x16x32_bf16 a[208:211],a[148:151],v[140:143],a[208:211]
ds_read_b64_tr_b16 a[138:139],v42 offset:25728
ds_read_b64_tr_b16 a[140:141],v42 offset:30848
v_mfma_f32_16x16x32_bf16 a[208:211],a[152:155],v[152:155],a[208:211]
ds_read_b64_tr_b16 a[142:143],v42 offset:35968
v_mfma_f32_16x16x32_bf16 a[208:211],a[156:159],v[156:159],a[208:211]
v_mfma_f32_16x16x32_bf16 a[212:215],a[156:159],v[188:191],a[212:215]
v_mfma_f32_16x16x32_bf16 a[212:215],a[152:155],v[184:187],a[212:215]
v_mfma_f32_16x16x32_bf16 a[212:215],a[148:151],v[172:175],a[212:215]
v_mfma_f32_16x16x32_bf16 a[212:215],a[144:147],v[168:171],a[212:215]
v_mfma_f32_16x16x32_bf16 a[216:219],a[144:147],v[200:203],a[216:219]
ds_read_b64_tr_b16 a[144:145],v44 offset:128
ds_read_b64_tr_b16 a[146:147],v44 offset:5248
v_mfma_f32_16x16x32_bf16 a[216:219],a[148:151],v[204:207],a[216:219]
v_accvgpr_read_b32 v130,a208
v_accvgpr_read_b32 v131,a209
v_mfma_f32_16x16x32_bf16 a[216:219],a[152:155],v[216:219],a[216:219]
v_accvgpr_read_b32 v14,a210
v_accvgpr_read_b32 v15,a211
v_mfma_f32_16x16x32_bf16 a[216:219],a[156:159],v[220:223],a[216:219]
v_cvt_pk_bf16_f32 v130,v130,v131
v_cvt_pk_bf16_f32 v131,v14,v15
v_mfma_f32_16x16x32_bf16 a[224:227],a[160:163],v[64:67],a[224:227]
buffer_store_dwordx4 v[128:131],v6,s[32:35],s84 offen sc1
v_mfma_f32_16x16x32_bf16 a[224:227],a[164:167],v[68:71],a[224:227]
v_accvgpr_read_b32 v134,a212
v_accvgpr_read_b32 v135,a213
v_mfma_f32_16x16x32_bf16 a[224:227],a[168:171],v[72:75],a[224:227]
v_accvgpr_read_b32 v16,a214
v_accvgpr_read_b32 v17,a215
v_mfma_f32_16x16x32_bf16 a[224:227],a[172:175],v[76:79],a[224:227]
v_cvt_pk_bf16_f32 v134,v134,v135
v_cvt_pk_bf16_f32 v135,v16,v17
v_mfma_f32_16x16x32_bf16 a[228:231],a[172:175],v[92:95],a[228:231]
buffer_store_dwordx4 v[132:135],v6,s[32:35],s85 offen sc1
v_mfma_f32_16x16x32_bf16 a[228:231],a[168:171],v[88:91],a[228:231]
v_accvgpr_read_b32 v146,a216
v_accvgpr_read_b32 v147,a217
v_mfma_f32_16x16x32_bf16 a[228:231],a[164:167],v[84:87],a[228:231]
v_accvgpr_read_b32 v12,a218
v_accvgpr_read_b32 v13,a219
v_mfma_f32_16x16x32_bf16 a[228:231],a[160:163],v[80:83],a[228:231]
v_cvt_pk_bf16_f32 v146,v146,v147
v_cvt_pk_bf16_f32 v147,v12,v13
v_mfma_f32_16x16x32_bf16 a[232:235],a[160:163],v[96:99],a[232:235]
buffer_store_dwordx4 v[144:147],v6,s[32:35],s86 offen sc1
v_mfma_f32_16x16x32_bf16 a[232:235],a[164:167],v[100:103],a[232:235]
ds_read_b64_tr_b16 a[148:149],v44 offset:10368
ds_read_b64_tr_b16 a[150:151],v44 offset:15488
v_mfma_f32_16x16x32_bf16 a[232:235],a[168:171],v[104:107],a[232:235]
ds_read_b64_tr_b16 a[152:153],v44 offset:20608
v_mfma_f32_16x16x32_bf16 a[232:235],a[172:175],v[108:111],a[232:235]
s_waitcnt lgkmcnt(3)
ds_read_b64_tr_b16 a[154:155],v44 offset:25728
v_mfma_f32_16x16x32_bf16 a[224:227],a[176:179],v[136:139],a[224:227]
ds_read_b64_tr_b16 a[156:157],v44 offset:30848
v_mfma_f32_16x16x32_bf16 a[224:227],a[180:183],v[140:143],a[224:227]
ds_read_b64_tr_b16 a[158:159],v44 offset:35968
ds_read_b64_tr_b16 a[160:161],v43 offset:160
v_mfma_f32_16x16x32_bf16 a[224:227],a[184:187],v[152:155],a[224:227]
ds_read_b64_tr_b16 a[162:163],v43 offset:5280
v_mfma_f32_16x16x32_bf16 a[224:227],a[188:191],v[156:159],a[224:227]
ds_read_b64_tr_b16 a[164:165],v43 offset:10400
v_mfma_f32_16x16x32_bf16 a[228:231],a[188:191],v[188:191],a[228:231]
ds_read_b64_tr_b16 a[166:167],v43 offset:15520
v_mfma_f32_16x16x32_bf16 a[228:231],a[184:187],v[184:187],a[228:231]
ds_read_b64_tr_b16 a[168:169],v43 offset:20640
ds_read_b64_tr_b16 a[170:171],v43 offset:25760
v_mfma_f32_16x16x32_bf16 a[228:231],a[180:183],v[172:175],a[228:231]
ds_read_b64_tr_b16 a[172:173],v43 offset:30880
v_mfma_f32_16x16x32_bf16 a[228:231],a[176:179],v[168:171],a[228:231]
ds_read_b64_tr_b16 a[174:175],v43 offset:36000
v_mfma_f32_16x16x32_bf16 a[232:235],a[176:179],v[200:203],a[232:235]
ds_read_b64_tr_b16 a[176:177],v45 offset:160
v_mfma_f32_16x16x32_bf16 a[232:235],a[180:183],v[204:207],a[232:235]
v_accvgpr_read_b32 v160,a224
v_accvgpr_read_b32 v161,a225
v_mfma_f32_16x16x32_bf16 a[232:235],a[184:187],v[216:219],a[232:235]
v_cvt_pk_bf16_f32 v160,v160,v161
v_accvgpr_read_b32 v162,a226
v_mfma_f32_16x16x32_bf16 a[232:235],a[188:191],v[220:223],a[232:235]
v_accvgpr_read_b32 v163,a227
v_cvt_pk_bf16_f32 v161,v162,v163
v_mfma_f32_16x16x32_bf16 a[240:243],a[96:99],v[64:67],a[240:243]
ds_read_b64_tr_b16 a[178:179],v45 offset:5280
ds_read_b64_tr_b16 a[180:181],v45 offset:10400
v_mfma_f32_16x16x32_bf16 a[240:243],a[100:103],v[68:71],a[240:243]
v_accvgpr_read_b32 v164,a228
v_accvgpr_read_b32 v165,a229
v_mfma_f32_16x16x32_bf16 a[240:243],a[104:107],v[72:75],a[240:243]
v_cvt_pk_bf16_f32 v164,v164,v165
v_accvgpr_read_b32 v166,a230
v_mfma_f32_16x16x32_bf16 a[240:243],a[108:111],v[76:79],a[240:243]
v_accvgpr_read_b32 v167,a231
v_cvt_pk_bf16_f32 v165,v166,v167
v_mfma_f32_16x16x32_bf16 a[244:247],a[108:111],v[92:95],a[244:247]
ds_read_b64_tr_b16 a[182:183],v45 offset:15520
ds_read_b64_tr_b16 a[184:185],v45 offset:20640
v_mfma_f32_16x16x32_bf16 a[244:247],a[104:107],v[88:91],a[244:247]
v_accvgpr_read_b32 v176,a232
v_accvgpr_read_b32 v177,a233
v_mfma_f32_16x16x32_bf16 a[244:247],a[100:103],v[84:87],a[244:247]
v_cvt_pk_bf16_f32 v176,v176,v177
v_accvgpr_read_b32 v178,a234
v_mfma_f32_16x16x32_bf16 a[244:247],a[96:99],v[80:83],a[244:247]
v_accvgpr_read_b32 v179,a235
v_cvt_pk_bf16_f32 v177,v178,v179
v_mfma_f32_16x16x32_bf16 a[248:251],a[96:99],v[96:99],a[248:251]
ds_read_b64_tr_b16 a[186:187],v45 offset:25760
ds_read_b64_tr_b16 a[188:189],v45 offset:30880
v_mfma_f32_16x16x32_bf16 a[248:251],a[100:103],v[100:103],a[248:251]
ds_read_b64_tr_b16 a[190:191],v45 offset:36000
v_mfma_f32_16x16x32_bf16 a[248:251],a[104:107],v[104:107],a[248:251]
ds_read_b64_tr_b16 a[96:97],v42 offset:192
v_mfma_f32_16x16x32_bf16 a[248:251],a[108:111],v[108:111],a[248:251]
ds_read_b64_tr_b16 a[98:99],v42 offset:5312
v_mfma_f32_16x16x32_bf16 a[240:243],a[112:115],v[136:139],a[240:243]
ds_read_b64_tr_b16 a[100:101],v42 offset:10432
ds_read_b64_tr_b16 a[102:103],v42 offset:15552
v_mfma_f32_16x16x32_bf16 a[240:243],a[116:119],v[140:143],a[240:243]
ds_read_b64_tr_b16 a[104:105],v42 offset:20672
v_mfma_f32_16x16x32_bf16 a[240:243],a[120:123],v[152:155],a[240:243]
ds_read_b64_tr_b16 a[106:107],v42 offset:25792
v_mfma_f32_16x16x32_bf16 a[240:243],a[124:127],v[156:159],a[240:243]
ds_read_b64_tr_b16 a[108:109],v42 offset:30912
v_mfma_f32_16x16x32_bf16 a[244:247],a[124:127],v[188:191],a[244:247]
ds_read_b64_tr_b16 a[110:111],v42 offset:36032
v_mfma_f32_16x16x32_bf16 a[244:247],a[120:123],v[184:187],a[244:247]
v_mfma_f32_16x16x32_bf16 a[244:247],a[116:119],v[172:175],a[244:247]
v_mfma_f32_16x16x32_bf16 a[244:247],a[112:115],v[168:171],a[244:247]
v_mfma_f32_16x16x32_bf16 a[248:251],a[112:115],v[200:203],a[248:251]
ds_read_b64_tr_b16 a[112:113],v44 offset:192
ds_read_b64_tr_b16 a[114:115],v44 offset:5312
v_mfma_f32_16x16x32_bf16 a[248:251],a[116:119],v[204:207],a[248:251]
v_accvgpr_read_b32 v162,a240
v_accvgpr_read_b32 v163,a241
v_mfma_f32_16x16x32_bf16 a[248:251],a[120:123],v[216:219],a[248:251]
v_accvgpr_read_b32 v14,a242
v_accvgpr_read_b32 v15,a243
v_mfma_f32_16x16x32_bf16 a[248:251],a[124:127],v[220:223],a[248:251]
v_cvt_pk_bf16_f32 v162,v162,v163
v_cvt_pk_bf16_f32 v163,v14,v15
v_mfma_f32_16x16x32_bf16 a[0:3],a[128:131],v[64:67],a[0:3]
buffer_store_dwordx4 v[160:163],v6,s[32:35],s84 offen offset:1024 sc1
v_mfma_f32_16x16x32_bf16 a[0:3],a[132:135],v[68:71],a[0:3]
v_accvgpr_read_b32 v166,a244
v_accvgpr_read_b32 v167,a245
v_mfma_f32_16x16x32_bf16 a[0:3],a[136:139],v[72:75],a[0:3]
v_accvgpr_read_b32 v16,a246
v_accvgpr_read_b32 v17,a247
v_mfma_f32_16x16x32_bf16 a[0:3],a[140:143],v[76:79],a[0:3]
v_cvt_pk_bf16_f32 v166,v166,v167
v_cvt_pk_bf16_f32 v167,v16,v17
v_mfma_f32_16x16x32_bf16 a[4:7],a[140:143],v[92:95],a[4:7]
buffer_store_dwordx4 v[164:167],v6,s[32:35],s85 offen offset:1024 sc1
v_mfma_f32_16x16x32_bf16 a[4:7],a[136:139],v[88:91],a[4:7]
v_accvgpr_read_b32 v178,a248
v_accvgpr_read_b32 v179,a249
v_mfma_f32_16x16x32_bf16 a[4:7],a[132:135],v[84:87],a[4:7]
.p2align 3
v_accvgpr_read_b32 v12,a250
v_accvgpr_read_b32 v13,a251
v_cvt_pk_bf16_f32 v178,v178,v179
v_cvt_pk_bf16_f32 v179,v12,v13
v_mfma_f32_16x16x32_bf16 a[4:7],a[128:131],v[80:83],a[4:7]
ds_read_b64_tr_b16 a[116:117],v44 offset:10432
ds_read_b64_tr_b16 a[118:119],v44 offset:15552
v_mfma_f32_16x16x32_bf16 a[8:11],a[128:131],v[96:99],a[8:11]
buffer_store_dwordx4 v[176:179],v6,s[32:35],s86 offen offset:1024 sc1
v_mfma_f32_16x16x32_bf16 a[8:11],a[132:135],v[100:103],a[8:11]
ds_read_b64_tr_b16 a[120:121],v44 offset:20672
ds_read_b64_tr_b16 a[122:123],v44 offset:25792
v_mfma_f32_16x16x32_bf16 a[8:11],a[136:139],v[104:107],a[8:11]
ds_read_b64_tr_b16 a[124:125],v44 offset:30912
v_mfma_f32_16x16x32_bf16 a[8:11],a[140:143],v[108:111],a[8:11]
ds_read_b64_tr_b16 a[126:127],v44 offset:36032
v_mfma_f32_16x16x32_bf16 a[0:3],a[144:147],v[136:139],a[0:3]
s_waitcnt lgkmcnt(6)
ds_read_b64_tr_b16 a[128:129],v43 offset:224
v_mfma_f32_16x16x32_bf16 a[0:3],a[148:151],v[140:143],a[0:3]
ds_read_b64_tr_b16 a[130:131],v43 offset:5344
ds_read_b64_tr_b16 a[132:133],v43 offset:10464
v_mfma_f32_16x16x32_bf16 a[0:3],a[152:155],v[152:155],a[0:3]
ds_read_b64_tr_b16 a[134:135],v43 offset:15584
v_mfma_f32_16x16x32_bf16 a[0:3],a[156:159],v[156:159],a[0:3]
ds_read_b64_tr_b16 a[136:137],v43 offset:20704
v_mfma_f32_16x16x32_bf16 a[4:7],a[156:159],v[188:191],a[4:7]
ds_read_b64_tr_b16 a[138:139],v43 offset:25824
v_mfma_f32_16x16x32_bf16 a[4:7],a[152:155],v[184:187],a[4:7]
ds_read_b64_tr_b16 a[140:141],v43 offset:30944
ds_read_b64_tr_b16 a[142:143],v43 offset:36064
v_mfma_f32_16x16x32_bf16 a[4:7],a[148:151],v[172:175],a[4:7]
v_mfma_f32_16x16x32_bf16 a[4:7],a[144:147],v[168:171],a[4:7]
v_mfma_f32_16x16x32_bf16 a[8:11],a[144:147],v[200:203],a[8:11]
ds_read_b64_tr_b16 a[144:145],v45 offset:224
ds_read_b64_tr_b16 a[146:147],v45 offset:5344
v_mfma_f32_16x16x32_bf16 a[8:11],a[148:151],v[204:207],a[8:11]
v_accvgpr_read_b32 v192,a0
v_accvgpr_read_b32 v193,a1
v_mfma_f32_16x16x32_bf16 a[8:11],a[152:155],v[216:219],a[8:11]
v_cvt_pk_bf16_f32 v192,v192,v193
v_accvgpr_read_b32 v194,a2
v_mfma_f32_16x16x32_bf16 a[8:11],a[156:159],v[220:223],a[8:11]
v_accvgpr_read_b32 v195,a3
v_cvt_pk_bf16_f32 v193,v194,v195
v_mfma_f32_16x16x32_bf16 a[16:19],a[160:163],v[64:67],a[16:19]
ds_read_b64_tr_b16 a[148:149],v45 offset:10464
ds_read_b64_tr_b16 a[150:151],v45 offset:15584
v_mfma_f32_16x16x32_bf16 a[16:19],a[164:167],v[68:71],a[16:19]
v_accvgpr_read_b32 v196,a4
v_accvgpr_read_b32 v197,a5
v_mfma_f32_16x16x32_bf16 a[16:19],a[168:171],v[72:75],a[16:19]
v_cvt_pk_bf16_f32 v196,v196,v197
v_accvgpr_read_b32 v198,a6
v_mfma_f32_16x16x32_bf16 a[16:19],a[172:175],v[76:79],a[16:19]
v_accvgpr_read_b32 v199,a7
v_cvt_pk_bf16_f32 v197,v198,v199
v_mfma_f32_16x16x32_bf16 a[20:23],a[172:175],v[92:95],a[20:23]
ds_read_b64_tr_b16 a[152:153],v45 offset:20704
ds_read_b64_tr_b16 a[154:155],v45 offset:25824
v_mfma_f32_16x16x32_bf16 a[20:23],a[168:171],v[88:91],a[20:23]
ds_read_b64_tr_b16 a[156:157],v45 offset:30944
v_accvgpr_read_b32 v208,a8
v_mfma_f32_16x16x32_bf16 a[20:23],a[164:167],v[84:87],a[20:23]
ds_read_b64_tr_b16 a[158:159],v45 offset:36064
v_accvgpr_read_b32 v209,a9
v_mfma_f32_16x16x32_bf16 a[20:23],a[160:163],v[80:83],a[20:23]
v_cvt_pk_bf16_f32 v208,v208,v209
v_accvgpr_read_b32 v210,a10
v_mfma_f32_16x16x32_bf16 a[24:27],a[160:163],v[96:99],a[24:27]
v_accvgpr_read_b32 v211,a11
v_cvt_pk_bf16_f32 v209,v210,v211
v_mfma_f32_16x16x32_bf16 a[24:27],a[164:167],v[100:103],a[24:27]
ds_read_b64_tr_b16 a[160:161],v42 offset:256
ds_read_b64_tr_b16 a[162:163],v42 offset:5376
v_mfma_f32_16x16x32_bf16 a[24:27],a[168:171],v[104:107],a[24:27]
)A"
R"A(ds_read_b64_tr_b16 a[164:165],v42 offset:10496
v_mfma_f32_16x16x32_bf16 a[24:27],a[172:175],v[108:111],a[24:27]
ds_read_b64_tr_b16 a[166:167],v42 offset:15616
v_mfma_f32_16x16x32_bf16 a[16:19],a[176:179],v[136:139],a[16:19]
ds_read_b64_tr_b16 a[168:169],v42 offset:20736
v_mfma_f32_16x16x32_bf16 a[16:19],a[180:183],v[140:143],a[16:19]
ds_read_b64_tr_b16 a[170:171],v42 offset:25856
ds_read_b64_tr_b16 a[172:173],v42 offset:30976
v_mfma_f32_16x16x32_bf16 a[16:19],a[184:187],v[152:155],a[16:19]
ds_read_b64_tr_b16 a[174:175],v42 offset:36096
v_mfma_f32_16x16x32_bf16 a[16:19],a[188:191],v[156:159],a[16:19]
v_mfma_f32_16x16x32_bf16 a[20:23],a[188:191],v[188:191],a[20:23]
v_mfma_f32_16x16x32_bf16 a[20:23],a[184:187],v[184:187],a[20:23]
v_mfma_f32_16x16x32_bf16 a[20:23],a[180:183],v[172:175],a[20:23]
v_mfma_f32_16x16x32_bf16 a[20:23],a[176:179],v[168:171],a[20:23]
v_mfma_f32_16x16x32_bf16 a[24:27],a[176:179],v[200:203],a[24:27]
ds_read_b64_tr_b16 a[176:177],v44 offset:256
ds_read_b64_tr_b16 a[178:179],v44 offset:5376
v_mfma_f32_16x16x32_bf16 a[24:27],a[180:183],v[204:207],a[24:27]
s_addk_i32 s95,5120
v_bfe_u32 v19,s55,13,1
v_mov_b32 v18,s68
v_mfma_f32_16x16x32_bf16 a[24:27],a[184:187],v[216:219],a[24:27]
v_or_b32 v19,s69,v19
v_accvgpr_read_b32 v194,a16
v_mfma_f32_16x16x32_bf16 a[24:27],a[188:191],v[220:223],a[24:27]
s_waitcnt vmcnt(0)
s_mov_b64 exec,1
buffer_store_dwordx2 v[18:19],v6,s[64:67],s95 offen sc1
s_mov_b64 exec,-1
v_mfma_f32_16x16x32_bf16 a[32:35],a[96:99],v[64:67],a[32:35]
v_accvgpr_read_b32 v195,a17
v_accvgpr_read_b32 v14,a18
v_mfma_f32_16x16x32_bf16 a[32:35],a[100:103],v[68:71],a[32:35]
v_accvgpr_read_b32 v15,a19
v_cvt_pk_bf16_f32 v194,v194,v195
v_mfma_f32_16x16x32_bf16 a[32:35],a[104:107],v[72:75],a[32:35]
v_cvt_pk_bf16_f32 v195,v14,v15
v_accvgpr_read_b32 v198,a20
v_mfma_f32_16x16x32_bf16 a[32:35],a[108:111],v[76:79],a[32:35]
buffer_store_dwordx4 v[192:195],v6,s[32:35],s84 offen offset:2048 sc1
v_mfma_f32_16x16x32_bf16 a[36:39],a[108:111],v[92:95],a[36:39]
v_accvgpr_read_b32 v199,a21
v_accvgpr_read_b32 v16,a22
v_mfma_f32_16x16x32_bf16 a[36:39],a[104:107],v[88:91],a[36:39]
v_accvgpr_read_b32 v17,a23
v_cvt_pk_bf16_f32 v198,v198,v199
v_mfma_f32_16x16x32_bf16 a[36:39],a[100:103],v[84:87],a[36:39]
v_cvt_pk_bf16_f32 v199,v16,v17
v_accvgpr_read_b32 v210,a24
v_mfma_f32_16x16x32_bf16 a[36:39],a[96:99],v[80:83],a[36:39]
buffer_store_dwordx4 v[196:199],v6,s[32:35],s85 offen offset:2048 sc1
v_mfma_f32_16x16x32_bf16 a[40:43],a[96:99],v[96:99],a[40:43]
v_accvgpr_read_b32 v211,a25
v_accvgpr_read_b32 v12,a26
v_mfma_f32_16x16x32_bf16 a[40:43],a[100:103],v[100:103],a[40:43]
v_accvgpr_read_b32 v13,a27
v_cvt_pk_bf16_f32 v210,v210,v211
v_mfma_f32_16x16x32_bf16 a[40:43],a[104:107],v[104:107],a[40:43]
ds_read_b64_tr_b16 a[180:181],v44 offset:10496
v_cvt_pk_bf16_f32 v211,v12,v13
v_mfma_f32_16x16x32_bf16 a[40:43],a[108:111],v[108:111],a[40:43]
buffer_store_dwordx4 v[208:211],v6,s[32:35],s86 offen offset:2048 sc1
v_mfma_f32_16x16x32_bf16 a[32:35],a[112:115],v[136:139],a[32:35]
s_waitcnt lgkmcnt(1)
ds_read_b64_tr_b16 a[182:183],v44 offset:15616
ds_read_b64_tr_b16 a[184:185],v44 offset:20736
v_mfma_f32_16x16x32_bf16 a[32:35],a[116:119],v[140:143],a[32:35]
ds_read_b64_tr_b16 a[186:187],v44 offset:25856
v_mfma_f32_16x16x32_bf16 a[32:35],a[120:123],v[152:155],a[32:35]
ds_read_b64_tr_b16 a[188:189],v44 offset:30976
v_mfma_f32_16x16x32_bf16 a[32:35],a[124:127],v[156:159],a[32:35]
ds_read_b64_tr_b16 a[190:191],v44 offset:36096
v_mfma_f32_16x16x32_bf16 a[36:39],a[124:127],v[188:191],a[36:39]
ds_read_b64_tr_b16 a[96:97],v43 offset:288
ds_read_b64_tr_b16 a[98:99],v43 offset:5408
v_mfma_f32_16x16x32_bf16 a[36:39],a[120:123],v[184:187],a[36:39]
ds_read_b64_tr_b16 a[100:101],v43 offset:10528
v_mfma_f32_16x16x32_bf16 a[36:39],a[116:119],v[172:175],a[36:39]
ds_read_b64_tr_b16 a[102:103],v43 offset:15648
v_mfma_f32_16x16x32_bf16 a[36:39],a[112:115],v[168:171],a[36:39]
ds_read_b64_tr_b16 a[104:105],v43 offset:20768
v_mfma_f32_16x16x32_bf16 a[40:43],a[112:115],v[200:203],a[40:43]
ds_read_b64_tr_b16 a[106:107],v43 offset:25888
ds_read_b64_tr_b16 a[108:109],v43 offset:31008
v_mfma_f32_16x16x32_bf16 a[40:43],a[116:119],v[204:207],a[40:43]
v_accvgpr_read_b32 v224,a32
v_accvgpr_read_b32 v225,a33
v_mfma_f32_16x16x32_bf16 a[40:43],a[120:123],v[216:219],a[40:43]
s_lshl_b32 s94,s71,2
v_cvt_pk_bf16_f32 v224,v224,v225
v_accvgpr_read_b32 v226,a34
v_mfma_f32_16x16x32_bf16 a[40:43],a[124:127],v[220:223],a[40:43]
s_add_u32 s94,s94,s40
v_accvgpr_read_b32 v227,a35
v_cvt_pk_bf16_f32 v225,v226,v227
v_mfma_f32_16x16x32_bf16 a[48:51],a[128:131],v[64:67],a[48:51]
s_lshl_b32 s94,s94,4
v_bfe_u32 v24,v6,4,2
v_min_u32 v24,2,v24
v_mfma_f32_16x16x32_bf16 a[48:51],a[132:135],v[68:71],a[48:51]
s_addk_i32 s94,5120
v_accvgpr_read_b32 v228,a36
v_accvgpr_read_b32 v229,a37
v_mfma_f32_16x16x32_bf16 a[48:51],a[136:139],v[72:75],a[48:51]
v_cvt_pk_bf16_f32 v228,v228,v229
v_accvgpr_read_b32 v230,a38
v_mfma_f32_16x16x32_bf16 a[48:51],a[140:143],v[76:79],a[48:51]
v_accvgpr_read_b32 v231,a39
v_cvt_pk_bf16_f32 v229,v230,v231
v_mfma_f32_16x16x32_bf16 a[52:55],a[140:143],v[92:95],a[52:55]
v_add_u32 v24,s70,v24
v_add_u32 v24,1,v24
v_mfma_f32_16x16x32_bf16 a[52:55],a[136:139],v[88:91],a[52:55]
v_accvgpr_read_b32 v240,a40
v_accvgpr_read_b32 v241,a41
v_mfma_f32_16x16x32_bf16 a[52:55],a[132:135],v[84:87],a[52:55]
v_cvt_pk_bf16_f32 v240,v240,v241
v_accvgpr_read_b32 v242,a42
v_mfma_f32_16x16x32_bf16 a[52:55],a[128:131],v[80:83],a[52:55]
v_accvgpr_read_b32 v243,a43
v_cvt_pk_bf16_f32 v241,v242,v243
v_mfma_f32_16x16x32_bf16 a[56:59],a[128:131],v[96:99],a[56:59]
v_and_b32 v24,3,v24
v_lshlrev_b32 v24,8,v24
v_mfma_f32_16x16x32_bf16 a[56:59],a[132:135],v[100:103],a[56:59]
ds_read_b64_tr_b16 a[110:111],v43 offset:36128
ds_read_b64_tr_b16 a[112:113],v45 offset:288
v_mfma_f32_16x16x32_bf16 a[56:59],a[136:139],v[104:107],a[56:59]
ds_read_b64_tr_b16 a[114:115],v45 offset:5408
v_mfma_f32_16x16x32_bf16 a[56:59],a[140:143],v[108:111],a[56:59]
ds_read_b64_tr_b16 a[116:117],v45 offset:10528
v_mfma_f32_16x16x32_bf16 a[48:51],a[144:147],v[136:139],a[48:51]
ds_read_b64_tr_b16 a[118:119],v45 offset:15648
v_mfma_f32_16x16x32_bf16 a[48:51],a[148:151],v[140:143],a[48:51]
ds_read_b64_tr_b16 a[120:121],v45 offset:20768
ds_read_b64_tr_b16 a[122:123],v45 offset:25888
v_mfma_f32_16x16x32_bf16 a[48:51],a[152:155],v[152:155],a[48:51]
ds_read_b64_tr_b16 a[124:125],v45 offset:31008
v_mfma_f32_16x16x32_bf16 a[48:51],a[156:159],v[156:159],a[48:51]
ds_read_b64_tr_b16 a[126:127],v45 offset:36128
v_mfma_f32_16x16x32_bf16 a[52:55],a[156:159],v[188:191],a[52:55]
ds_read_b64_tr_b16 a[128:129],v1
v_mfma_f32_16x16x32_bf16 a[52:55],a[152:155],v[184:187],a[52:55]
ds_read_b64_tr_b16 a[130:131],v1 offset:5120
ds_read_b64_tr_b16 a[132:133],v1 offset:10240
v_mfma_f32_16x16x32_bf16 a[52:55],a[148:151],v[172:175],a[52:55]
ds_read_b64_tr_b16 a[134:135],v1 offset:15360
v_mfma_f32_16x16x32_bf16 a[52:55],a[144:147],v[168:171],a[52:55]
ds_read_b64_tr_b16 a[136:137],v1 offset:20480
v_mfma_f32_16x16x32_bf16 a[56:59],a[144:147],v[200:203],a[56:59]
ds_read_b64_tr_b16 a[138:139],v1 offset:25600
v_mfma_f32_16x16x32_bf16 a[56:59],a[148:151],v[204:207],a[56:59]
v_accvgpr_read_b32 v226,a48
v_accvgpr_read_b32 v227,a49
v_mfma_f32_16x16x32_bf16 a[56:59],a[152:155],v[216:219],a[56:59]
v_accvgpr_read_b32 v14,a50
v_accvgpr_read_b32 v15,a51
v_mfma_f32_16x16x32_bf16 a[56:59],a[156:159],v[220:223],a[56:59]
v_cvt_pk_bf16_f32 v226,v226,v227
v_cvt_pk_bf16_f32 v227,v14,v15
v_mfma_f32_16x16x32_bf16 a[64:67],a[160:163],v[64:67],a[64:67]
buffer_store_dwordx4 v[224:227],v6,s[32:35],s84 offen offset:3072 sc1
v_mfma_f32_16x16x32_bf16 a[64:67],a[164:167],v[68:71],a[64:67]
v_accvgpr_read_b32 v230,a52
v_accvgpr_read_b32 v231,a53
v_mfma_f32_16x16x32_bf16 a[64:67],a[168:171],v[72:75],a[64:67]
v_accvgpr_read_b32 v16,a54
v_accvgpr_read_b32 v17,a55
v_mfma_f32_16x16x32_bf16 a[64:67],a[172:175],v[76:79],a[64:67]
v_cvt_pk_bf16_f32 v230,v230,v231
v_cvt_pk_bf16_f32 v231,v16,v17
v_mfma_f32_16x16x32_bf16 a[68:71],a[172:175],v[92:95],a[68:71]
buffer_store_dwordx4 v[228:231],v6,s[32:35],s85 offen offset:3072 sc1
v_mfma_f32_16x16x32_bf16 a[68:71],a[168:171],v[88:91],a[68:71]
v_accvgpr_read_b32 v242,a56
v_accvgpr_read_b32 v243,a57
v_mfma_f32_16x16x32_bf16 a[68:71],a[164:167],v[84:87],a[68:71]
v_accvgpr_read_b32 v12,a58
v_accvgpr_read_b32 v13,a59
v_mfma_f32_16x16x32_bf16 a[68:71],a[160:163],v[80:83],a[68:71]
v_cvt_pk_bf16_f32 v242,v242,v243
v_cvt_pk_bf16_f32 v243,v12,v13
v_mfma_f32_16x16x32_bf16 a[72:75],a[160:163],v[96:99],a[72:75]
buffer_store_dwordx4 v[240:243],v6,s[32:35],s86 offen offset:3072 sc1
v_mfma_f32_16x16x32_bf16 a[72:75],a[164:167],v[100:103],a[72:75]
ds_read_b64_tr_b16 a[140:141],v1 offset:30720
ds_read_b64_tr_b16 a[142:143],v1 offset:35840
v_mfma_f32_16x16x32_bf16 a[72:75],a[168:171],v[104:107],a[72:75]
ds_read_b64_tr_b16 a[144:145],v42
v_mfma_f32_16x16x32_bf16 a[72:75],a[172:175],v[108:111],a[72:75]
ds_read_b64_tr_b16 a[146:147],v42 offset:5120
v_mfma_f32_16x16x32_bf16 a[64:67],a[176:179],v[136:139],a[64:67]
s_waitcnt lgkmcnt(4)
ds_read_b64_tr_b16 a[148:149],v42 offset:10240
v_mfma_f32_16x16x32_bf16 a[64:67],a[180:183],v[140:143],a[64:67]
ds_read_b64_tr_b16 a[150:151],v42 offset:15360
ds_read_b64_tr_b16 a[152:153],v42 offset:20480
v_mfma_f32_16x16x32_bf16 a[64:67],a[184:187],v[152:155],a[64:67]
buffer_load_dwordx2 v[4:5],v24,s[64:67],s94 offen sc1
v_mfma_f32_16x16x32_bf16 a[64:67],a[188:191],v[156:159],a[64:67]
ds_read_b64_tr_b16 a[154:155],v42 offset:25600
ds_read_b64_tr_b16 a[156:157],v42 offset:30720
v_mfma_f32_16x16x32_bf16 a[68:71],a[188:191],v[188:191],a[68:71]
ds_read_b64_tr_b16 a[158:159],v42 offset:35840
v_mfma_f32_16x16x32_bf16 a[68:71],a[184:187],v[184:187],a[68:71]
ds_read_b64_tr_b16 a[160:161],v44
v_mfma_f32_16x16x32_bf16 a[68:71],a[180:183],v[172:175],a[68:71]
ds_read_b64_tr_b16 a[162:163],v44 offset:5120
v_mfma_f32_16x16x32_bf16 a[68:71],a[176:179],v[168:171],a[68:71]
ds_read_b64_tr_b16 a[164:165],v44 offset:10240
ds_read_b64_tr_b16 a[166:167],v44 offset:15360
v_mfma_f32_16x16x32_bf16 a[72:75],a[176:179],v[200:203],a[72:75]
ds_read_b64_tr_b16 a[168:169],v44 offset:20480
v_mfma_f32_16x16x32_bf16 a[72:75],a[180:183],v[204:207],a[72:75]
v_accvgpr_read_b32 v128,a64
v_accvgpr_read_b32 v129,a65
v_mfma_f32_16x16x32_bf16 a[72:75],a[184:187],v[216:219],a[72:75]
v_cvt_pk_bf16_f32 v128,v128,v129
v_accvgpr_read_b32 v130,a66
v_mfma_f32_16x16x32_bf16 a[72:75],a[188:191],v[220:223],a[72:75]
v_accvgpr_read_b32 v131,a67
v_cvt_pk_bf16_f32 v129,v130,v131
v_mfma_f32_16x16x32_bf16 a[80:83],a[96:99],v[64:67],a[80:83]
ds_read_b64_tr_b16 a[170:171],v44 offset:25600
ds_read_b64_tr_b16 a[172:173],v44 offset:30720
v_mfma_f32_16x16x32_bf16 a[80:83],a[100:103],v[68:71],a[80:83]
ds_read_b64_tr_b16 a[174:175],v44 offset:35840
v_accvgpr_read_b32 v132,a68
v_mfma_f32_16x16x32_bf16 a[80:83],a[104:107],v[72:75],a[80:83]
ds_read_b64_tr_b16 a[176:177],v2 offset:32
v_accvgpr_read_b32 v133,a69
v_mfma_f32_16x16x32_bf16 a[80:83],a[108:111],v[76:79],a[80:83]
ds_read_b64_tr_b16 a[178:179],v2 offset:5152
v_cvt_pk_bf16_f32 v132,v132,v133
v_mfma_f32_16x16x32_bf16 a[84:87],a[108:111],v[92:95],a[84:87]
v_accvgpr_read_b32 v134,a70
v_accvgpr_read_b32 v135,a71
v_mfma_f32_16x16x32_bf16 a[84:87],a[104:107],v[88:91],a[84:87]
v_accvgpr_read_b32 v144,a72
v_accvgpr_read_b32 v145,a73
v_mfma_f32_16x16x32_bf16 a[84:87],a[100:103],v[84:87],a[84:87]
v_cvt_pk_bf16_f32 v144,v144,v145
v_accvgpr_read_b32 v146,a74
v_mfma_f32_16x16x32_bf16 a[84:87],a[96:99],v[80:83],a[84:87]
ds_read_b64_tr_b16 a[180:181],v2 offset:10272
v_accvgpr_read_b32 v147,a75
v_mfma_f32_16x16x32_bf16 a[88:91],a[96:99],v[96:99],a[88:91]
ds_read_b64_tr_b16 a[182:183],v2 offset:15392
v_cvt_pk_bf16_f32 v133,v134,v135
v_mfma_f32_16x16x32_bf16 a[88:91],a[100:103],v[100:103],a[88:91]
ds_read_b64_tr_b16 a[184:185],v2 offset:20512
v_cvt_pk_bf16_f32 v145,v146,v147
v_mfma_f32_16x16x32_bf16 a[88:91],a[104:107],v[104:107],a[88:91]
ds_read_b64_tr_b16 a[186:187],v2 offset:25632
ds_read_b64_tr_b16 a[188:189],v2 offset:30752
v_mfma_f32_16x16x32_bf16 a[88:91],a[108:111],v[108:111],a[88:91]
)A"
R"A(ds_read_b64_tr_b16 a[190:191],v2 offset:35872
v_mfma_f32_16x16x32_bf16 a[80:83],a[112:115],v[136:139],a[80:83]
ds_read_b64_tr_b16 a[96:97],v43 offset:32
v_mfma_f32_16x16x32_bf16 a[80:83],a[116:119],v[140:143],a[80:83]
ds_read_b64_tr_b16 a[98:99],v43 offset:5152
v_mfma_f32_16x16x32_bf16 a[80:83],a[120:123],v[152:155],a[80:83]
ds_read_b64_tr_b16 a[100:101],v43 offset:10272
ds_read_b64_tr_b16 a[102:103],v43 offset:15392
v_mfma_f32_16x16x32_bf16 a[80:83],a[124:127],v[156:159],a[80:83]
ds_read_b64_tr_b16 a[104:105],v43 offset:20512
v_mfma_f32_16x16x32_bf16 a[84:87],a[124:127],v[188:191],a[84:87]
ds_read_b64_tr_b16 a[106:107],v43 offset:25632
v_mfma_f32_16x16x32_bf16 a[84:87],a[120:123],v[184:187],a[84:87]
ds_read_b64_tr_b16 a[108:109],v43 offset:30752
v_mfma_f32_16x16x32_bf16 a[84:87],a[116:119],v[172:175],a[84:87]
ds_read_b64_tr_b16 a[110:111],v43 offset:35872
v_mfma_f32_16x16x32_bf16 a[84:87],a[112:115],v[168:171],a[84:87]
v_mfma_f32_16x16x32_bf16 a[88:91],a[112:115],v[200:203],a[88:91]
ds_read_b64_tr_b16 a[112:113],v45 offset:32
ds_read_b64_tr_b16 a[114:115],v45 offset:5152
v_mfma_f32_16x16x32_bf16 a[88:91],a[116:119],v[204:207],a[88:91]
v_accvgpr_read_b32 v130,a80
v_accvgpr_read_b32 v131,a81
v_mfma_f32_16x16x32_bf16 a[88:91],a[120:123],v[216:219],a[88:91]
v_accvgpr_read_b32 v14,a82
v_accvgpr_read_b32 v15,a83
v_mfma_f32_16x16x32_bf16 a[88:91],a[124:127],v[220:223],a[88:91]
v_cvt_pk_bf16_f32 v130,v130,v131
v_cvt_pk_bf16_f32 v131,v14,v15
v_mfma_f32_16x16x32_bf16 a[204:207],a[128:131],v[148:151],a[204:207]
buffer_store_dwordx4 v[128:131],v25,s[32:35],s84 offen sc1
v_mfma_f32_16x16x32_bf16 a[204:207],a[132:135],v[180:183],a[204:207]
v_accvgpr_read_b32 v134,a84
v_accvgpr_read_b32 v135,a85
v_mfma_f32_16x16x32_bf16 a[204:207],a[136:139],v[212:215],a[204:207]
s_waitcnt vmcnt(1)
v_cmp_ne_u32 s[90:91],s68,v4
v_and_b32 v4,-2,v5
v_cmp_ne_u32 s[92:93],s69,v4
s_nop 0
s_or_b64 s[90:91],s[90:91],s[92:93]
s_cbranch_scc1 L_fr10_%=
L_fo10_%=:
v_lshlrev_b32 v19,13,v5
v_and_b32 v19,8192,v19
s_nop 0
v_readlane_b32 s90,v19,2
v_readlane_b32 s91,v19,1
v_readlane_b32 s92,v19,0
s_nop 1
s_or_b32 s76,s76,s90
s_or_b32 s77,s77,s91
s_or_b32 s78,s78,s92
s_waitcnt lgkmcnt(2)
v_mfma_f32_16x16x32_bf16 a[204:207],a[140:143],v[244:247],a[204:207]
s_sub_u32 s95,s95,5120
v_bfe_u32 v19,s55,13,1
v_mov_b32 v18,s68
v_or_b32 v19,s69,v19
v_mfma_f32_16x16x32_bf16 a[204:207],a[144:147],v[112:115],a[204:207]
s_sub_u32 s94,s94,5120
s_mov_b64 exec,1
buffer_store_dwordx2 v[18:19],v6,s[64:67],s95 offen offset:2048 sc1
s_mov_b64 exec,-1
v_mfma_f32_16x16x32_bf16 a[204:207],a[148:151],v[116:119],a[204:207]
v_accvgpr_read_b32 v146,a88
v_accvgpr_read_b32 v147,a89
v_mfma_f32_16x16x32_bf16 a[204:207],a[152:155],v[120:123],a[204:207]
.p2align 3
v_accvgpr_read_b32 v16,a86
v_accvgpr_read_b32 v17,a87
v_cvt_pk_bf16_f32 v134,v134,v135
v_accvgpr_read_b32 v12,a90
v_accvgpr_read_b32 v13,a91
v_cvt_pk_bf16_f32 v146,v146,v147
v_cvt_pk_bf16_f32 v135,v16,v17
v_cvt_pk_bf16_f32 v147,v12,v13
v_mfma_f32_16x16x32_bf16 a[204:207],a[156:159],v[124:127],a[204:207]
ds_read_b64_tr_b16 a[116:117],v45 offset:10272
ds_read_b64_tr_b16 a[118:119],v45 offset:15392
v_mfma_f32_16x16x32_bf16 a[204:207],a[160:163],v[232:235],a[204:207]
buffer_store_dwordx4 v[132:135],v25,s[32:35],s85 offen sc1
v_mfma_f32_16x16x32_bf16 a[204:207],a[164:167],v[236:239],a[204:207]
ds_read_b64_tr_b16 a[120:121],v45 offset:20512
ds_read_b64_tr_b16 a[122:123],v45 offset:25632
v_mfma_f32_16x16x32_bf16 a[204:207],a[168:171],v[248:251],a[204:207]
ds_read_b64_tr_b16 a[124:125],v45 offset:30752
ds_read_b64_tr_b16 a[126:127],v45 offset:35872
v_mfma_f32_16x16x32_bf16 a[204:207],a[172:175],v[252:255],a[204:207]
ds_read_b64_tr_b16 a[128:129],v1 offset:64
ds_read_b64_tr_b16 a[130:131],v1 offset:5184
v_mfma_f32_16x16x32_bf16 a[220:223],a[176:179],v[148:151],a[220:223]
buffer_store_dwordx4 v[144:147],v25,s[32:35],s86 offen sc1
v_mfma_f32_16x16x32_bf16 a[220:223],a[180:183],v[180:183],a[220:223]
ds_read_b64_tr_b16 a[132:133],v1 offset:10304
ds_read_b64_tr_b16 a[134:135],v1 offset:15424
v_mfma_f32_16x16x32_bf16 a[220:223],a[184:187],v[212:215],a[220:223]
ds_read_b64_tr_b16 a[136:137],v1 offset:20544
ds_read_b64_tr_b16 a[138:139],v1 offset:25664
v_mfma_f32_16x16x32_bf16 a[220:223],a[188:191],v[244:247],a[220:223]
ds_read_b64_tr_b16 a[140:141],v1 offset:30784
ds_read_b64_tr_b16 a[142:143],v1 offset:35904
v_mfma_f32_16x16x32_bf16 a[220:223],a[96:99],v[112:115],a[220:223]
buffer_load_dwordx4 a[192:195],v6,s[32:35],s76 offen sc1
v_mfma_f32_16x16x32_bf16 a[220:223],a[100:103],v[116:119],a[220:223]
ds_read_b64_tr_b16 a[144:145],v42 offset:64
ds_read_b64_tr_b16 a[146:147],v42 offset:5184
v_mfma_f32_16x16x32_bf16 a[220:223],a[104:107],v[120:123],a[220:223]
ds_read_b64_tr_b16 a[148:149],v42 offset:10304
ds_read_b64_tr_b16 a[150:151],v42 offset:15424
v_mfma_f32_16x16x32_bf16 a[220:223],a[108:111],v[124:127],a[220:223]
s_waitcnt lgkmcnt(10)
ds_read_b64_tr_b16 a[152:153],v42 offset:20544
ds_read_b64_tr_b16 a[154:155],v42 offset:25664
v_mfma_f32_16x16x32_bf16 a[220:223],a[112:115],v[232:235],a[220:223]
buffer_load_dwordx4 a[196:199],v6,s[32:35],s77 offen sc1
v_mfma_f32_16x16x32_bf16 a[220:223],a[116:119],v[236:239],a[220:223]
ds_read_b64_tr_b16 a[156:157],v42 offset:30784
ds_read_b64_tr_b16 a[158:159],v42 offset:35904
v_mfma_f32_16x16x32_bf16 a[220:223],a[120:123],v[248:251],a[220:223]
ds_read_b64_tr_b16 a[160:161],v44 offset:64
ds_read_b64_tr_b16 a[162:163],v44 offset:5184
v_mfma_f32_16x16x32_bf16 a[220:223],a[124:127],v[252:255],a[220:223]
ds_read_b64_tr_b16 a[164:165],v44 offset:10304
ds_read_b64_tr_b16 a[166:167],v44 offset:15424
v_mfma_f32_16x16x32_bf16 a[236:239],a[128:131],v[148:151],a[236:239]
s_waitcnt lgkmcnt(12)
buffer_load_dwordx4 a[200:203],v6,s[32:35],s78 offen sc1
v_mfma_f32_16x16x32_bf16 a[236:239],a[132:135],v[180:183],a[236:239]
ds_read_b64_tr_b16 a[168:169],v44 offset:20544
ds_read_b64_tr_b16 a[170:171],v44 offset:25664
v_mfma_f32_16x16x32_bf16 a[236:239],a[136:139],v[212:215],a[236:239]
ds_read_b64_tr_b16 a[172:173],v44 offset:30784
ds_read_b64_tr_b16 a[174:175],v44 offset:35904
v_mfma_f32_16x16x32_bf16 a[236:239],a[140:143],v[244:247],a[236:239]
s_waitcnt lgkmcnt(10)
ds_read_b64_tr_b16 a[176:177],v2 offset:96
ds_read_b64_tr_b16 a[178:179],v2 offset:5216
v_mfma_f32_16x16x32_bf16 a[236:239],a[144:147],v[112:115],a[236:239]
buffer_load_dwordx4 a[224:227],v6,s[32:35],s76 offen offset:1024 sc1
ds_read_b64_tr_b16 a[180:181],v2 offset:10336
ds_read_b64_tr_b16 a[182:183],v2 offset:15456
v_mfma_f32_16x16x32_bf16 a[236:239],a[148:151],v[116:119],a[236:239]
ds_read_b64_tr_b16 a[184:185],v2 offset:20576
ds_read_b64_tr_b16 a[186:187],v2 offset:25696
v_mfma_f32_16x16x32_bf16 a[236:239],a[152:155],v[120:123],a[236:239]
s_waitcnt lgkmcnt(12)
ds_read_b64_tr_b16 a[188:189],v2 offset:30816
ds_read_b64_tr_b16 a[190:191],v2 offset:35936
v_mfma_f32_16x16x32_bf16 a[236:239],a[156:159],v[124:127],a[236:239]
ds_read_b64_tr_b16 a[96:97],v43 offset:96
ds_read_b64_tr_b16 a[98:99],v43 offset:5216
v_mfma_f32_16x16x32_bf16 a[236:239],a[160:163],v[232:235],a[236:239]
s_waitcnt lgkmcnt(14)
buffer_load_dwordx4 a[228:231],v6,s[32:35],s77 offen offset:1024 sc1
ds_read_b64_tr_b16 a[100:101],v43 offset:10336
ds_read_b64_tr_b16 a[102:103],v43 offset:15456
v_mfma_f32_16x16x32_bf16 a[236:239],a[164:167],v[236:239],a[236:239]
s_waitcnt lgkmcnt(14)
ds_read_b64_tr_b16 a[104:105],v43 offset:20576
ds_read_b64_tr_b16 a[106:107],v43 offset:25696
v_mfma_f32_16x16x32_bf16 a[236:239],a[168:171],v[248:251],a[236:239]
s_waitcnt lgkmcnt(14)
ds_read_b64_tr_b16 a[108:109],v43 offset:30816
ds_read_b64_tr_b16 a[110:111],v43 offset:35936
v_mfma_f32_16x16x32_bf16 a[236:239],a[172:175],v[252:255],a[236:239]
s_waitcnt lgkmcnt(14)
ds_read_b64_tr_b16 a[112:113],v45 offset:96
ds_read_b64_tr_b16 a[114:115],v45 offset:5216
v_mfma_f32_16x16x32_bf16 a[252:255],a[176:179],v[148:151],a[252:255]
s_waitcnt lgkmcnt(14)
buffer_load_dwordx4 a[232:235],v6,s[32:35],s78 offen offset:1024 sc1
ds_read_b64_tr_b16 a[116:117],v45 offset:10336
ds_read_b64_tr_b16 a[118:119],v45 offset:15456
v_mfma_f32_16x16x32_bf16 a[252:255],a[180:183],v[180:183],a[252:255]
s_waitcnt lgkmcnt(14)
ds_read_b64_tr_b16 a[120:121],v45 offset:20576
ds_read_b64_tr_b16 a[122:123],v45 offset:25696
v_mfma_f32_16x16x32_bf16 a[252:255],a[184:187],v[212:215],a[252:255]
s_waitcnt lgkmcnt(14)
ds_read_b64_tr_b16 a[124:125],v45 offset:30816
ds_read_b64_tr_b16 a[126:127],v45 offset:35936
v_mfma_f32_16x16x32_bf16 a[252:255],a[188:191],v[244:247],a[252:255]
s_waitcnt lgkmcnt(14)
ds_read_b64_tr_b16 a[128:129],v1 offset:128
ds_read_b64_tr_b16 a[130:131],v1 offset:5248
v_mfma_f32_16x16x32_bf16 a[252:255],a[96:99],v[112:115],a[252:255]
s_waitcnt lgkmcnt(14)
ds_read_b64_tr_b16 a[132:133],v1 offset:10368
ds_read_b64_tr_b16 a[134:135],v1 offset:15488
v_mfma_f32_16x16x32_bf16 a[252:255],a[100:103],v[116:119],a[252:255]
s_waitcnt lgkmcnt(14)
ds_read_b64_tr_b16 a[136:137],v1 offset:20608
ds_read_b64_tr_b16 a[138:139],v1 offset:25728
v_mfma_f32_16x16x32_bf16 a[252:255],a[104:107],v[120:123],a[252:255]
s_waitcnt lgkmcnt(14)
buffer_load_dwordx2 v[4:5],v24,s[64:67],s94 offen offset:2048 sc1
ds_read_b64_tr_b16 a[140:141],v1 offset:30848
ds_read_b64_tr_b16 a[142:143],v1 offset:35968
v_mfma_f32_16x16x32_bf16 a[252:255],a[108:111],v[124:127],a[252:255]
s_waitcnt lgkmcnt(14)
ds_read_b64_tr_b16 a[144:145],v42 offset:128
ds_read_b64_tr_b16 a[146:147],v42 offset:5248
v_mfma_f32_16x16x32_bf16 a[252:255],a[112:115],v[232:235],a[252:255]
s_waitcnt lgkmcnt(14)
ds_read_b64_tr_b16 a[148:149],v42 offset:10368
ds_read_b64_tr_b16 a[150:151],v42 offset:15488
v_mfma_f32_16x16x32_bf16 a[252:255],a[116:119],v[236:239],a[252:255]
s_waitcnt lgkmcnt(14)
ds_read_b64_tr_b16 a[152:153],v42 offset:20608
ds_read_b64_tr_b16 a[154:155],v42 offset:25728
v_mfma_f32_16x16x32_bf16 a[252:255],a[120:123],v[248:251],a[252:255]
s_waitcnt lgkmcnt(14)
ds_read_b64_tr_b16 a[156:157],v42 offset:30848
ds_read_b64_tr_b16 a[158:159],v42 offset:35968
v_bfe_u32 v19,s55,13,1
v_mov_b32 v18,s68
v_or_b32 v19,s69,v19
v_mfma_f32_16x16x32_bf16 a[252:255],a[124:127],v[252:255],a[252:255]
s_waitcnt vmcnt(7)
s_mov_b64 exec,1
buffer_store_dwordx2 v[18:19],v6,s[64:67],s95 offen offset:3072 sc1
s_mov_b64 exec,-1
ds_read_b64_tr_b16 a[160:161],v44 offset:128
ds_read_b64_tr_b16 a[162:163],v44 offset:5248
s_waitcnt lgkmcnt(15)
v_mfma_f32_16x16x32_bf16 a[12:15],a[128:131],v[148:151],a[12:15]
s_waitcnt lgkmcnt(14)
ds_read_b64_tr_b16 a[164:165],v44 offset:10368
ds_read_b64_tr_b16 a[166:167],v44 offset:15488
v_mfma_f32_16x16x32_bf16 a[12:15],a[132:135],v[180:183],a[12:15]
s_waitcnt lgkmcnt(14)
ds_read_b64_tr_b16 a[168:169],v44 offset:20608
ds_read_b64_tr_b16 a[170:171],v44 offset:25728
v_mfma_f32_16x16x32_bf16 a[12:15],a[136:139],v[212:215],a[12:15]
s_waitcnt lgkmcnt(14)
ds_read_b64_tr_b16 a[172:173],v44 offset:30848
ds_read_b64_tr_b16 a[174:175],v44 offset:35968
v_mfma_f32_16x16x32_bf16 a[12:15],a[140:143],v[244:247],a[12:15]
s_waitcnt lgkmcnt(14)
ds_read_b64_tr_b16 a[176:177],v2 offset:160
ds_read_b64_tr_b16 a[178:179],v2 offset:5280
v_mfma_f32_16x16x32_bf16 a[12:15],a[144:147],v[112:115],a[12:15]
s_waitcnt lgkmcnt(14)
ds_read_b64_tr_b16 a[180:181],v2 offset:10400
ds_read_b64_tr_b16 a[182:183],v2 offset:15520
v_mfma_f32_16x16x32_bf16 a[12:15],a[148:151],v[116:119],a[12:15]
s_waitcnt lgkmcnt(14)
ds_read_b64_tr_b16 a[184:185],v2 offset:20640
ds_read_b64_tr_b16 a[186:187],v2 offset:25760
v_mfma_f32_16x16x32_bf16 a[12:15],a[152:155],v[120:123],a[12:15]
s_waitcnt lgkmcnt(14)
ds_read_b64_tr_b16 a[188:189],v2 offset:30880
ds_read_b64_tr_b16 a[190:191],v2 offset:36000
v_mfma_f32_16x16x32_bf16 a[12:15],a[156:159],v[124:127],a[12:15]
s_waitcnt lgkmcnt(14)
ds_read_b64_tr_b16 a[96:97],v43 offset:160
ds_read_b64_tr_b16 a[98:99],v43 offset:5280
v_mfma_f32_16x16x32_bf16 a[12:15],a[160:163],v[232:235],a[12:15]
s_waitcnt lgkmcnt(14)
ds_read_b64_tr_b16 a[100:101],v43 offset:10400
ds_read_b64_tr_b16 a[102:103],v43 offset:15520
v_mfma_f32_16x16x32_bf16 a[12:15],a[164:167],v[236:239],a[12:15]
s_waitcnt lgkmcnt(14)
ds_read_b64_tr_b16 a[104:105],v43 offset:20640
)A"
R"A(ds_read_b64_tr_b16 a[106:107],v43 offset:25760
v_mfma_f32_16x16x32_bf16 a[12:15],a[168:171],v[248:251],a[12:15]
s_waitcnt lgkmcnt(14)
ds_read_b64_tr_b16 a[108:109],v43 offset:30880
ds_read_b64_tr_b16 a[110:111],v43 offset:36000
v_mfma_f32_16x16x32_bf16 a[12:15],a[172:175],v[252:255],a[12:15]
s_waitcnt lgkmcnt(14)
ds_read_b64_tr_b16 a[112:113],v45 offset:160
ds_read_b64_tr_b16 a[114:115],v45 offset:5280
v_mfma_f32_16x16x32_bf16 a[28:31],a[176:179],v[148:151],a[28:31]
s_waitcnt lgkmcnt(14)
ds_read_b64_tr_b16 a[116:117],v45 offset:10400
ds_read_b64_tr_b16 a[118:119],v45 offset:15520
v_mfma_f32_16x16x32_bf16 a[28:31],a[180:183],v[180:183],a[28:31]
s_waitcnt lgkmcnt(14)
ds_read_b64_tr_b16 a[120:121],v45 offset:20640
ds_read_b64_tr_b16 a[122:123],v45 offset:25760
v_mfma_f32_16x16x32_bf16 a[28:31],a[184:187],v[212:215],a[28:31]
s_waitcnt lgkmcnt(14)
ds_read_b64_tr_b16 a[124:125],v45 offset:30880
ds_read_b64_tr_b16 a[126:127],v45 offset:36000
v_mfma_f32_16x16x32_bf16 a[28:31],a[188:191],v[244:247],a[28:31]
s_waitcnt lgkmcnt(14)
ds_read_b64_tr_b16 a[128:129],v1 offset:192
ds_read_b64_tr_b16 a[130:131],v1 offset:5312
v_mfma_f32_16x16x32_bf16 a[28:31],a[96:99],v[112:115],a[28:31]
s_waitcnt lgkmcnt(14)
ds_read_b64_tr_b16 a[132:133],v1 offset:10432
ds_read_b64_tr_b16 a[134:135],v1 offset:15552
v_mfma_f32_16x16x32_bf16 a[28:31],a[100:103],v[116:119],a[28:31]
s_waitcnt lgkmcnt(14)
ds_read_b64_tr_b16 a[136:137],v1 offset:20672
ds_read_b64_tr_b16 a[138:139],v1 offset:25792
v_mfma_f32_16x16x32_bf16 a[28:31],a[104:107],v[120:123],a[28:31]
s_waitcnt lgkmcnt(14)
ds_read_b64_tr_b16 a[140:141],v1 offset:30912
ds_read_b64_tr_b16 a[142:143],v1 offset:36032
v_mfma_f32_16x16x32_bf16 a[28:31],a[108:111],v[124:127],a[28:31]
s_waitcnt lgkmcnt(14)
ds_read_b64_tr_b16 a[144:145],v42 offset:192
ds_read_b64_tr_b16 a[146:147],v42 offset:5312
v_mfma_f32_16x16x32_bf16 a[28:31],a[112:115],v[232:235],a[28:31]
s_waitcnt lgkmcnt(14)
ds_read_b64_tr_b16 a[148:149],v42 offset:10432
ds_read_b64_tr_b16 a[150:151],v42 offset:15552
v_mfma_f32_16x16x32_bf16 a[28:31],a[116:119],v[236:239],a[28:31]
s_waitcnt lgkmcnt(14)
ds_read_b64_tr_b16 a[152:153],v42 offset:20672
ds_read_b64_tr_b16 a[154:155],v42 offset:25792
v_mfma_f32_16x16x32_bf16 a[28:31],a[120:123],v[248:251],a[28:31]
s_waitcnt lgkmcnt(14)
ds_read_b64_tr_b16 a[156:157],v42 offset:30912
ds_read_b64_tr_b16 a[158:159],v42 offset:36032
v_mfma_f32_16x16x32_bf16 a[28:31],a[124:127],v[252:255],a[28:31]
s_waitcnt lgkmcnt(14)
ds_read_b64_tr_b16 a[160:161],v44 offset:192
ds_read_b64_tr_b16 a[162:163],v44 offset:5312
v_mfma_f32_16x16x32_bf16 a[44:47],a[128:131],v[148:151],a[44:47]
s_waitcnt lgkmcnt(14)
ds_read_b64_tr_b16 a[164:165],v44 offset:10432
ds_read_b64_tr_b16 a[166:167],v44 offset:15552
v_mfma_f32_16x16x32_bf16 a[44:47],a[132:135],v[180:183],a[44:47]
s_waitcnt lgkmcnt(14)
ds_read_b64_tr_b16 a[168:169],v44 offset:20672
ds_read_b64_tr_b16 a[170:171],v44 offset:25792
v_mfma_f32_16x16x32_bf16 a[44:47],a[136:139],v[212:215],a[44:47]
ds_read_b64_tr_b16 a[172:173],v44 offset:30912
ds_read_b64_tr_b16 a[174:175],v44 offset:36032
s_waitcnt vmcnt(1)
v_cmp_ne_u32 s[90:91],s68,v4
v_and_b32 v4,-2,v5
v_cmp_ne_u32 s[92:93],s69,v4
s_nop 0
s_or_b64 s[90:91],s[90:91],s[92:93]
s_cbranch_scc1 L_fr11_%=
L_fo11_%=:
s_waitcnt lgkmcnt(15)
v_mfma_f32_16x16x32_bf16 a[44:47],a[140:143],v[244:247],a[44:47]
s_waitcnt lgkmcnt(14)
buffer_load_dwordx4 v[72:75],v6,s[32:35],s76 offen offset:2048 sc1
ds_read_b64_tr_b16 a[176:177],v2 offset:224
ds_read_b64_tr_b16 a[178:179],v2 offset:5344
v_mfma_f32_16x16x32_bf16 a[44:47],a[144:147],v[112:115],a[44:47]
s_waitcnt lgkmcnt(14)
ds_read_b64_tr_b16 a[180:181],v2 offset:10464
ds_read_b64_tr_b16 a[182:183],v2 offset:15584
v_mfma_f32_16x16x32_bf16 a[44:47],a[148:151],v[116:119],a[44:47]
s_waitcnt lgkmcnt(14)
ds_read_b64_tr_b16 a[184:185],v2 offset:20704
ds_read_b64_tr_b16 a[186:187],v2 offset:25824
v_mfma_f32_16x16x32_bf16 a[44:47],a[152:155],v[120:123],a[44:47]
s_waitcnt lgkmcnt(14)
ds_read_b64_tr_b16 a[188:189],v2 offset:30944
ds_read_b64_tr_b16 a[190:191],v2 offset:36064
v_mfma_f32_16x16x32_bf16 a[44:47],a[156:159],v[124:127],a[44:47]
s_waitcnt lgkmcnt(14)
buffer_load_dwordx4 v[88:91],v6,s[32:35],s77 offen offset:2048 sc1
ds_read_b64_tr_b16 a[96:97],v43 offset:224
ds_read_b64_tr_b16 a[98:99],v43 offset:5344
v_mfma_f32_16x16x32_bf16 a[44:47],a[160:163],v[232:235],a[44:47]
s_waitcnt lgkmcnt(14)
ds_read_b64_tr_b16 a[100:101],v43 offset:10464
ds_read_b64_tr_b16 a[102:103],v43 offset:15584
v_mfma_f32_16x16x32_bf16 a[44:47],a[164:167],v[236:239],a[44:47]
s_waitcnt lgkmcnt(14)
ds_read_b64_tr_b16 a[104:105],v43 offset:20704
ds_read_b64_tr_b16 a[106:107],v43 offset:25824
v_mfma_f32_16x16x32_bf16 a[44:47],a[168:171],v[248:251],a[44:47]
s_waitcnt lgkmcnt(14)
ds_read_b64_tr_b16 a[108:109],v43 offset:30944
ds_read_b64_tr_b16 a[110:111],v43 offset:36064
v_mfma_f32_16x16x32_bf16 a[44:47],a[172:175],v[252:255],a[44:47]
s_waitcnt lgkmcnt(14)
buffer_load_dwordx4 v[104:107],v6,s[32:35],s78 offen offset:2048 sc1
ds_read_b64_tr_b16 a[112:113],v45 offset:224
ds_read_b64_tr_b16 a[114:115],v45 offset:5344
v_mfma_f32_16x16x32_bf16 a[60:63],a[176:179],v[148:151],a[60:63]
s_waitcnt lgkmcnt(14)
ds_read_b64_tr_b16 a[116:117],v45 offset:10464
ds_read_b64_tr_b16 a[118:119],v45 offset:15584
v_mfma_f32_16x16x32_bf16 a[60:63],a[180:183],v[180:183],a[60:63]
s_waitcnt lgkmcnt(14)
ds_read_b64_tr_b16 a[120:121],v45 offset:20704
ds_read_b64_tr_b16 a[122:123],v45 offset:25824
v_mfma_f32_16x16x32_bf16 a[60:63],a[184:187],v[212:215],a[60:63]
s_waitcnt lgkmcnt(14)
ds_read_b64_tr_b16 a[124:125],v45 offset:30944
ds_read_b64_tr_b16 a[126:127],v45 offset:36064
v_mfma_f32_16x16x32_bf16 a[60:63],a[188:191],v[244:247],a[60:63]
s_waitcnt lgkmcnt(14)
buffer_load_dwordx4 v[76:79],v6,s[32:35],s76 offen offset:3072 sc1
v_mfma_f32_16x16x32_bf16 a[60:63],a[96:99],v[112:115],a[60:63]
s_waitcnt lgkmcnt(12)
ds_read_b64_tr_b16 a[128:129],v1 offset:256
ds_read_b64_tr_b16 a[130:131],v1 offset:5376
v_mfma_f32_16x16x32_bf16 a[60:63],a[100:103],v[116:119],a[60:63]
s_waitcnt lgkmcnt(12)
ds_read_b64_tr_b16 a[132:133],v1 offset:10496
ds_read_b64_tr_b16 a[134:135],v1 offset:15616
v_mfma_f32_16x16x32_bf16 a[60:63],a[104:107],v[120:123],a[60:63]
s_waitcnt lgkmcnt(12)
ds_read_b64_tr_b16 a[136:137],v1 offset:20736
ds_read_b64_tr_b16 a[138:139],v1 offset:25856
v_mfma_f32_16x16x32_bf16 a[60:63],a[108:111],v[124:127],a[60:63]
s_waitcnt lgkmcnt(12)
buffer_load_dwordx4 v[92:95],v6,s[32:35],s77 offen offset:3072 sc1
v_mfma_f32_16x16x32_bf16 a[60:63],a[112:115],v[232:235],a[60:63]
s_waitcnt lgkmcnt(10)
ds_read_b64_tr_b16 a[140:141],v1 offset:30976
ds_read_b64_tr_b16 a[142:143],v1 offset:36096
v_mfma_f32_16x16x32_bf16 a[60:63],a[116:119],v[236:239],a[60:63]
s_waitcnt lgkmcnt(10)
ds_read_b64_tr_b16 a[144:145],v42 offset:256
ds_read_b64_tr_b16 a[146:147],v42 offset:5376
v_mfma_f32_16x16x32_bf16 a[60:63],a[120:123],v[248:251],a[60:63]
s_waitcnt lgkmcnt(10)
ds_read_b64_tr_b16 a[148:149],v42 offset:10496
ds_read_b64_tr_b16 a[150:151],v42 offset:15616
v_mfma_f32_16x16x32_bf16 a[60:63],a[124:127],v[252:255],a[60:63]
buffer_load_dwordx4 v[108:111],v6,s[32:35],s78 offen offset:3072 sc1
v_mfma_f32_16x16x32_bf16 a[204:207],v[34:37],a[192:195],a[204:207]
ds_read_b64_tr_b16 a[152:153],v42 offset:20736
ds_read_b64_tr_b16 a[154:155],v42 offset:25856
v_mfma_f32_16x16x32_bf16 a[204:207],v[34:37],a[196:199],a[204:207]
ds_read_b64_tr_b16 a[156:157],v42 offset:30976
ds_read_b64_tr_b16 a[158:159],v42 offset:36096
v_mfma_f32_16x16x32_bf16 a[204:207],v[34:37],a[200:203],a[204:207]
ds_read_b64_tr_b16 a[160:161],v44 offset:256
ds_read_b64_tr_b16 a[162:163],v44 offset:5376
v_mfma_f32_16x16x32_bf16 a[220:223],v[32:35],a[192:195],a[220:223]
ds_read_b64_tr_b16 a[164:165],v44 offset:10496
ds_read_b64_tr_b16 a[166:167],v44 offset:15616
v_mfma_f32_16x16x32_bf16 a[220:223],v[32:35],a[196:199],a[220:223]
ds_read_b64_tr_b16 a[168:169],v44 offset:20736
ds_read_b64_tr_b16 a[170:171],v44 offset:25856
v_mfma_f32_16x16x32_bf16 a[220:223],v[32:35],a[200:203],a[220:223]
s_waitcnt lgkmcnt(12)
ds_read_b64_tr_b16 a[172:173],v44 offset:30976
ds_read_b64_tr_b16 a[174:175],v44 offset:36096
v_mfma_f32_16x16x32_bf16 a[76:79],a[128:131],v[148:151],a[76:79]
ds_read_b64_tr_b16 a[176:177],v2 offset:288
ds_read_b64_tr_b16 a[178:179],v2 offset:5408
v_mfma_f32_16x16x32_bf16 a[76:79],a[132:135],v[180:183],a[76:79]
ds_read_b64_tr_b16 a[180:181],v2 offset:10528
ds_read_b64_tr_b16 a[182:183],v2 offset:15648
v_mfma_f32_16x16x32_bf16 a[76:79],a[136:139],v[212:215],a[76:79]
v_accvgpr_read_b32 v160,a204
v_accvgpr_read_b32 v161,a205
v_mfma_f32_16x16x32_bf16 a[76:79],a[140:143],v[244:247],a[76:79]
v_cvt_pk_bf16_f32 v160,v160,v161
v_accvgpr_read_b32 v162,a206
v_mfma_f32_16x16x32_bf16 a[76:79],a[144:147],v[112:115],a[76:79]
s_waitcnt lgkmcnt(10)
v_accvgpr_read_b32 v163,a207
v_cvt_pk_bf16_f32 v161,v162,v163
v_mfma_f32_16x16x32_bf16 a[76:79],a[148:151],v[116:119],a[76:79]
v_accvgpr_read_b32 v162,a220
v_accvgpr_read_b32 v163,a221
v_mfma_f32_16x16x32_bf16 a[76:79],a[152:155],v[120:123],a[76:79]
v_accvgpr_read_b32 v14,a222
v_accvgpr_read_b32 v15,a223
v_mfma_f32_16x16x32_bf16 a[76:79],a[156:159],v[124:127],a[76:79]
v_cvt_pk_bf16_f32 v162,v162,v163
v_cvt_pk_bf16_f32 v163,v14,v15
v_mfma_f32_16x16x32_bf16 a[76:79],a[160:163],v[232:235],a[76:79]
s_waitcnt lgkmcnt(2)
ds_read_b64_tr_b16 a[184:185],v2 offset:20768
ds_read_b64_tr_b16 a[186:187],v2 offset:25888
v_mfma_f32_16x16x32_bf16 a[76:79],a[164:167],v[236:239],a[76:79]
v_permlane16_swap_b32 v160,v162
v_mfma_f32_16x16x32_bf16 a[76:79],a[168:171],v[248:251],a[76:79]
v_permlane16_swap_b32 v161,v163
v_mfma_f32_16x16x32_bf16 a[76:79],a[172:175],v[252:255],a[76:79]
buffer_load_dwordx2 v[4:5],v24,s[64:67],s94 offen offset:3072 sc1
v_mfma_f32_16x16x32_bf16 a[236:239],v[34:37],a[224:227],a[236:239]
ds_read_b64_tr_b16 a[188:189],v2 offset:31008
ds_read_b64_tr_b16 a[190:191],v2 offset:36128
v_mfma_f32_16x16x32_bf16 a[236:239],v[34:37],a[228:231],a[236:239]
ds_read_b64_tr_b16 a[96:97],v43 offset:288
ds_read_b64_tr_b16 a[98:99],v43 offset:5408
v_mfma_f32_16x16x32_bf16 a[236:239],v[34:37],a[232:235],a[236:239]
ds_read_b64_tr_b16 a[100:101],v43 offset:10528
ds_read_b64_tr_b16 a[102:103],v43 offset:15648
v_mfma_f32_16x16x32_bf16 a[252:255],v[32:35],a[224:227],a[252:255]
ds_read_b64_tr_b16 a[104:105],v43 offset:20768
ds_read_b64_tr_b16 a[106:107],v43 offset:25888
v_mfma_f32_16x16x32_bf16 a[252:255],v[32:35],a[228:231],a[252:255]
ds_read_b64_tr_b16 a[108:109],v43 offset:31008
ds_read_b64_tr_b16 a[110:111],v43 offset:36128
v_mfma_f32_16x16x32_bf16 a[252:255],v[32:35],a[232:235],a[252:255]
ds_read_b64_tr_b16 a[112:113],v45 offset:288
ds_read_b64_tr_b16 a[114:115],v45 offset:5408
v_mfma_f32_16x16x32_bf16 a[92:95],a[176:179],v[148:151],a[92:95]
s_waitcnt lgkmcnt(12)
ds_read_b64_tr_b16 a[116:117],v45 offset:10528
ds_read_b64_tr_b16 a[118:119],v45 offset:15648
v_mfma_f32_16x16x32_bf16 a[92:95],a[180:183],v[180:183],a[92:95]
ds_read_b64_tr_b16 a[120:121],v45 offset:20768
ds_read_b64_tr_b16 a[122:123],v45 offset:25888
v_mfma_f32_16x16x32_bf16 a[92:95],a[184:187],v[212:215],a[92:95]
s_waitcnt lgkmcnt(14)
ds_read_b64_tr_b16 a[124:125],v45 offset:31008
ds_read_b64_tr_b16 a[126:127],v45 offset:36128
v_mfma_f32_16x16x32_bf16 a[92:95],a[188:191],v[244:247],a[92:95]
s_cmp_lg_u32 %2,0
s_cbranch_scc1 L_ea0_%=
buffer_store_dwordx4 v[160:163],v40,s[20:23],s43 offen
s_branch L_eb0_%=
L_ea0_%=:
buffer_store_dwordx4 v[160:163],v40,s[20:23],s43 offen sc1
L_eb0_%=:
s_waitcnt lgkmcnt(14)
v_mfma_f32_16x16x32_bf16 a[92:95],a[96:99],v[112:115],a[92:95]
s_waitcnt lgkmcnt(12)
v_accvgpr_read_b32 v164,a236
v_accvgpr_read_b32 v165,a237
v_mfma_f32_16x16x32_bf16 a[92:95],a[100:103],v[116:119],a[92:95]
s_waitcnt lgkmcnt(10)
v_cvt_pk_bf16_f32 v164,v164,v165
v_accvgpr_read_b32 v166,a238
v_mfma_f32_16x16x32_bf16 a[92:95],a[104:107],v[120:123],a[92:95]
s_waitcnt lgkmcnt(8)
v_accvgpr_read_b32 v167,a239
v_cvt_pk_bf16_f32 v165,v166,v167
v_mfma_f32_16x16x32_bf16 a[92:95],a[108:111],v[124:127],a[92:95]
s_waitcnt lgkmcnt(6)
v_accvgpr_read_b32 v166,a252
)A"
R"A(v_accvgpr_read_b32 v167,a253
v_mfma_f32_16x16x32_bf16 a[92:95],a[112:115],v[232:235],a[92:95]
s_waitcnt lgkmcnt(4)
v_accvgpr_read_b32 v16,a254
v_accvgpr_read_b32 v17,a255
v_mfma_f32_16x16x32_bf16 a[92:95],a[116:119],v[236:239],a[92:95]
s_waitcnt lgkmcnt(2)
v_cvt_pk_bf16_f32 v166,v166,v167
v_cvt_pk_bf16_f32 v167,v16,v17
v_mfma_f32_16x16x32_bf16 a[92:95],a[120:123],v[248:251],a[92:95]
s_waitcnt lgkmcnt(0)
v_mfma_f32_16x16x32_bf16 a[92:95],a[124:127],v[252:255],a[92:95]
v_permlane16_swap_b32 v164,v166
v_permlane16_swap_b32 v165,v167
s_nop 3
s_cmp_lg_u32 %2,0
s_cbranch_scc1 L_ea1_%=
buffer_store_dwordx4 v[164:167],v40,s[20:23],s43 offen offset:64
s_branch L_eb1_%=
L_ea1_%=:
buffer_store_dwordx4 v[164:167],v40,s[20:23],s43 offen offset:64 sc1
L_eb1_%=:
s_waitcnt vmcnt(2)
v_cmp_ne_u32 s[90:91],s68,v4
v_and_b32 v4,-2,v5
v_cmp_ne_u32 s[92:93],s69,v4
s_nop 0
s_or_b64 s[90:91],s[90:91],s[92:93]
s_cbranch_scc1 L_fr12_%=
L_fo12_%=:
buffer_load_dwordx4 v[136:139],v25,s[32:35],s76 offen sc1
buffer_load_dwordx4 v[168:171],v25,s[32:35],s77 offen sc1
buffer_load_dwordx4 v[200:203],v25,s[32:35],s78 offen sc1
s_cmp_lg_u32 %2,0
s_cbranch_scc1 L_dual_%=
v_mfma_f32_16x16x32_bf16 a[12:15],v[34:37],v[72:75],a[12:15]
v_mfma_f32_16x16x32_bf16 a[12:15],v[34:37],v[88:91],a[12:15]
v_mfma_f32_16x16x32_bf16 a[12:15],v[34:37],v[104:107],a[12:15]
v_mfma_f32_16x16x32_bf16 a[28:31],v[32:35],v[72:75],a[28:31]
v_mfma_f32_16x16x32_bf16 a[28:31],v[32:35],v[88:91],a[28:31]
v_mfma_f32_16x16x32_bf16 a[28:31],v[32:35],v[104:107],a[28:31]
v_mfma_f32_16x16x32_bf16 a[44:47],v[34:37],v[76:79],a[44:47]
v_mfma_f32_16x16x32_bf16 a[44:47],v[34:37],v[92:95],a[44:47]
v_mfma_f32_16x16x32_bf16 a[44:47],v[34:37],v[108:111],a[44:47]
s_nop 1
.p2align 3
v_accvgpr_read_b32 v176,a12
v_accvgpr_read_b32 v177,a13
v_cvt_pk_bf16_f32 v176,v176,v177
v_accvgpr_read_b32 v178,a14
v_accvgpr_read_b32 v179,a15
v_cvt_pk_bf16_f32 v177,v178,v179
v_mfma_f32_16x16x32_bf16 a[60:63],v[32:35],v[76:79],a[60:63]
v_mfma_f32_16x16x32_bf16 a[60:63],v[32:35],v[92:95],a[60:63]
v_mfma_f32_16x16x32_bf16 a[60:63],v[32:35],v[108:111],a[60:63]
.p2align 3
v_accvgpr_read_b32 v178,a28
v_accvgpr_read_b32 v179,a29
v_accvgpr_read_b32 v12,a30
v_accvgpr_read_b32 v13,a31
v_cvt_pk_bf16_f32 v178,v178,v179
v_cvt_pk_bf16_f32 v179,v12,v13
s_nop 3
v_permlane16_swap_b32 v176,v178
v_permlane16_swap_b32 v177,v179
buffer_store_dwordx4 v[176:179],v40,s[20:23],s43 offen offset:128
.p2align 3
v_accvgpr_read_b32 v192,a44
v_accvgpr_read_b32 v193,a45
v_cvt_pk_bf16_f32 v192,v192,v193
v_accvgpr_read_b32 v194,a46
v_accvgpr_read_b32 v195,a47
v_cvt_pk_bf16_f32 v193,v194,v195
v_accvgpr_read_b32 v194,a60
v_accvgpr_read_b32 v195,a61
v_accvgpr_read_b32 v14,a62
v_accvgpr_read_b32 v15,a63
v_cvt_pk_bf16_f32 v194,v194,v195
s_nop 1
v_permlane16_swap_b32 v192,v194
v_cvt_pk_bf16_f32 v195,v14,v15
s_nop 1
v_permlane16_swap_b32 v193,v195
buffer_store_dwordx4 v[192:195],v40,s[20:23],s43 offen offset:192
s_waitcnt vmcnt(4)
v_mfma_f32_16x16x32_bf16 a[76:79],v[34:37],v[136:139],a[76:79]
s_waitcnt vmcnt(3)
v_mfma_f32_16x16x32_bf16 a[76:79],v[34:37],v[168:171],a[76:79]
s_waitcnt vmcnt(2)
v_mfma_f32_16x16x32_bf16 a[76:79],v[34:37],v[200:203],a[76:79]
v_mfma_f32_16x16x32_bf16 a[92:95],v[32:35],v[136:139],a[92:95]
v_mfma_f32_16x16x32_bf16 a[92:95],v[32:35],v[168:171],a[92:95]
v_mfma_f32_16x16x32_bf16 a[92:95],v[32:35],v[200:203],a[92:95]
s_nop 3
s_nop 0
.p2align 3
v_accvgpr_read_b32 v196,a76
v_accvgpr_read_b32 v197,a77
v_cvt_pk_bf16_f32 v196,v196,v197
v_accvgpr_read_b32 v198,a78
v_accvgpr_read_b32 v199,a79
v_cvt_pk_bf16_f32 v197,v198,v199
v_accvgpr_read_b32 v198,a92
v_accvgpr_read_b32 v199,a93
v_accvgpr_read_b32 v16,a94
v_accvgpr_read_b32 v17,a95
v_cvt_pk_bf16_f32 v198,v198,v199
s_nop 1
v_permlane16_swap_b32 v196,v198
v_cvt_pk_bf16_f32 v199,v16,v17
s_nop 1
v_permlane16_swap_b32 v197,v199
buffer_store_dwordx4 v[196:199],v40,s[20:23],s43 offen offset:256
s_or_b32 s62,s42,s40
s_cmp_lg_u32 s62,0
s_cbranch_scc1 L_noep_%=
v_mov_b32 v12,s68
s_mov_b64 exec,1
buffer_store_dword v12,v25,s[64:67],0 offen sc1
s_mov_b64 exec,-1
L_noep_%=:
s_endpgm
L_dual_%=:
v_mfma_f32_16x16x32_bf16 a[12:15],v[34:37],v[72:75],a[12:15]
v_mfma_f32_16x16x32_bf16 a[12:15],v[34:37],v[88:91],a[12:15]
v_mfma_f32_16x16x32_bf16 a[12:15],v[34:37],v[104:107],a[12:15]
v_mfma_f32_16x16x32_bf16 a[28:31],v[32:35],v[72:75],a[28:31]
v_mfma_f32_16x16x32_bf16 a[28:31],v[32:35],v[88:91],a[28:31]
v_mfma_f32_16x16x32_bf16 a[28:31],v[32:35],v[104:107],a[28:31]
v_mfma_f32_16x16x32_bf16 a[44:47],v[34:37],v[76:79],a[44:47]
v_mfma_f32_16x16x32_bf16 a[44:47],v[34:37],v[92:95],a[44:47]
v_mfma_f32_16x16x32_bf16 a[44:47],v[34:37],v[108:111],a[44:47]
s_nop 1
.p2align 3
v_accvgpr_read_b32 v176,a12
v_accvgpr_read_b32 v177,a13
v_cvt_pk_bf16_f32 v176,v176,v177
v_accvgpr_read_b32 v178,a14
v_accvgpr_read_b32 v179,a15
v_cvt_pk_bf16_f32 v177,v178,v179
v_mfma_f32_16x16x32_bf16 a[60:63],v[32:35],v[76:79],a[60:63]
v_mfma_f32_16x16x32_bf16 a[60:63],v[32:35],v[92:95],a[60:63]
v_mfma_f32_16x16x32_bf16 a[60:63],v[32:35],v[108:111],a[60:63]
.p2align 3
v_accvgpr_read_b32 v178,a28
v_accvgpr_read_b32 v179,a29
v_accvgpr_read_b32 v12,a30
v_accvgpr_read_b32 v13,a31
v_cvt_pk_bf16_f32 v178,v178,v179
v_cvt_pk_bf16_f32 v179,v12,v13
s_nop 3
v_permlane16_swap_b32 v176,v178
v_permlane16_swap_b32 v177,v179
buffer_store_dwordx4 v[176:179],v40,s[20:23],s43 offen offset:128 sc1
.p2align 3
v_accvgpr_read_b32 v192,a44
v_accvgpr_read_b32 v193,a45
v_cvt_pk_bf16_f32 v192,v192,v193
v_accvgpr_read_b32 v194,a46
v_accvgpr_read_b32 v195,a47
v_cvt_pk_bf16_f32 v193,v194,v195
v_accvgpr_read_b32 v194,a60
v_accvgpr_read_b32 v195,a61
v_accvgpr_read_b32 v14,a62
v_accvgpr_read_b32 v15,a63
v_cvt_pk_bf16_f32 v194,v194,v195
s_nop 1
v_permlane16_swap_b32 v192,v194
v_cvt_pk_bf16_f32 v195,v14,v15
s_nop 1
v_permlane16_swap_b32 v193,v195
buffer_store_dwordx4 v[192:195],v40,s[20:23],s43 offen offset:192 sc1
s_waitcnt vmcnt(4)
v_mfma_f32_16x16x32_bf16 a[76:79],v[34:37],v[136:139],a[76:79]
s_waitcnt vmcnt(3)
v_mfma_f32_16x16x32_bf16 a[76:79],v[34:37],v[168:171],a[76:79]
s_waitcnt vmcnt(2)
v_mfma_f32_16x16x32_bf16 a[76:79],v[34:37],v[200:203],a[76:79]
v_mfma_f32_16x16x32_bf16 a[92:95],v[32:35],v[136:139],a[92:95]
v_mfma_f32_16x16x32_bf16 a[92:95],v[32:35],v[168:171],a[92:95]
v_mfma_f32_16x16x32_bf16 a[92:95],v[32:35],v[200:203],a[92:95]
s_nop 3
s_nop 0
.p2align 3
v_accvgpr_read_b32 v196,a76
v_accvgpr_read_b32 v197,a77
v_cvt_pk_bf16_f32 v196,v196,v197
v_accvgpr_read_b32 v198,a78
v_accvgpr_read_b32 v199,a79
v_cvt_pk_bf16_f32 v197,v198,v199
v_accvgpr_read_b32 v198,a92
v_accvgpr_read_b32 v199,a93
v_accvgpr_read_b32 v16,a94
v_accvgpr_read_b32 v17,a95
v_cvt_pk_bf16_f32 v198,v198,v199
s_nop 1
v_permlane16_swap_b32 v196,v198
v_cvt_pk_bf16_f32 v199,v16,v17
s_nop 1
v_permlane16_swap_b32 v197,v199
buffer_store_dwordx4 v[196:199],v40,s[20:23],s43 offen offset:256 sc1
s_or_b32 s62,s42,s40
s_cmp_lg_u32 s62,0
s_cbranch_scc1 L_noep2_%=
v_mov_b32 v12,s68
s_mov_b64 exec,1
buffer_store_dword v12,v25,s[64:67],0 offen sc1
s_mov_b64 exec,-1
L_noep2_%=:
s_endpgm
L_ps2_%=:
s_min_i32 s79,s79,100
s_add_u32 s78,s78,s79
L_pl2_%=:
s_sleep 1
s_sub_u32 s79,s79,4
s_cmp_gt_i32 s79,1
s_cbranch_scc1 L_pl2_%=
s_branch L_pr2_%=
L_ps3_%=:
s_min_i32 s79,s79,100
s_add_u32 s78,s78,s79
L_pl3_%=:
s_sleep 1
s_sub_u32 s79,s79,4
s_cmp_gt_i32 s79,1
s_cbranch_scc1 L_pl3_%=
s_branch L_pr3_%=
L_ps4_%=:
s_min_i32 s79,s79,100
s_add_u32 s78,s78,s79
L_pl4_%=:
s_sleep 1
s_sub_u32 s79,s79,4
s_cmp_gt_i32 s79,1
s_cbranch_scc1 L_pl4_%=
s_branch L_pr4_%=
L_ps5_%=:
s_min_i32 s79,s79,100
s_add_u32 s78,s78,s79
L_pl5_%=:
s_sleep 1
s_sub_u32 s79,s79,4
s_cmp_gt_i32 s79,1
s_cbranch_scc1 L_pl5_%=
s_branch L_pr5_%=
L_ps6_%=:
s_min_i32 s79,s79,100
s_add_u32 s78,s78,s79
L_pl6_%=:
s_sleep 1
s_sub_u32 s79,s79,4
s_cmp_gt_i32 s79,1
s_cbranch_scc1 L_pl6_%=
s_branch L_pr6_%=
L_ps7_%=:
s_min_i32 s79,s79,100
s_add_u32 s78,s78,s79
L_pl7_%=:
s_sleep 1
s_sub_u32 s79,s79,4
s_cmp_gt_i32 s79,1
s_cbranch_scc1 L_pl7_%=
s_branch L_pr7_%=
L_fr8_%=:
s_mov_b32 s88,262144
L_fl8_%=:
buffer_load_dwordx2 v[4:5],v24,s[64:67],s94 offen sc1
s_waitcnt vmcnt(0)
s_sub_u32 s88,s88,1
s_cmp_eq_u32 s88,0
s_cbranch_scc1 L_fo8_%=
v_cmp_ne_u32 s[90:91],s68,v4
v_and_b32 v4,-2,v5
v_cmp_ne_u32 s[92:93],s69,v4
s_nop 0
s_or_b64 s[90:91],s[90:91],s[92:93]
s_cbranch_scc1 L_fl8_%=
s_branch L_fo8_%=
L_fr9_%=:
s_mov_b32 s88,262144
L_fl9_%=:
buffer_load_dwordx2 v[4:5],v24,s[64:67],s94 offen offset:1024 sc1
s_waitcnt vmcnt(0)
s_sub_u32 s88,s88,1
s_cmp_eq_u32 s88,0
s_cbranch_scc1 L_fo9_%=
v_cmp_ne_u32 s[90:91],s68,v4
v_and_b32 v4,-2,v5
v_cmp_ne_u32 s[92:93],s69,v4
s_nop 0
s_or_b64 s[90:91],s[90:91],s[92:93]
s_cbranch_scc1 L_fl9_%=
s_branch L_fo9_%=
L_fr10_%=:
s_mov_b32 s88,262144
L_fl10_%=:
buffer_load_dwordx2 v[4:5],v24,s[64:67],s94 offen sc1
s_waitcnt vmcnt(0)
s_sub_u32 s88,s88,1
s_cmp_eq_u32 s88,0
s_cbranch_scc1 L_fo10_%=
v_cmp_ne_u32 s[90:91],s68,v4
v_and_b32 v4,-2,v5
v_cmp_ne_u32 s[92:93],s69,v4
s_nop 0
s_or_b64 s[90:91],s[90:91],s[92:93]
s_cbranch_scc1 L_fl10_%=
s_branch L_fo10_%=
L_fr11_%=:
s_mov_b32 s88,262144
L_fl11_%=:
buffer_load_dwordx2 v[4:5],v24,s[64:67],s94 offen offset:2048 sc1
s_waitcnt vmcnt(0)
s_sub_u32 s88,s88,1
s_cmp_eq_u32 s88,0
s_cbranch_scc1 L_fo11_%=
v_cmp_ne_u32 s[90:91],s68,v4
v_and_b32 v4,-2,v5
v_cmp_ne_u32 s[92:93],s69,v4
s_nop 0
s_or_b64 s[90:91],s[90:91],s[92:93]
s_cbranch_scc1 L_fl11_%=
s_branch L_fo11_%=
L_fr12_%=:
s_mov_b32 s88,262144
L_fl12_%=:
buffer_load_dwordx2 v[4:5],v24,s[64:67],s94 offen offset:3072 sc1
s_waitcnt vmcnt(0)
s_sub_u32 s88,s88,1
s_cmp_eq_u32 s88,0
s_cbranch_scc1 L_fo12_%=
v_cmp_ne_u32 s[90:91],s68,v4
v_and_b32 v4,-2,v5
v_cmp_ne_u32 s[92:93],s69,v4
s_nop 0
s_or_b64 s[90:91],s[90:91],s[92:93]
s_cbranch_scc1 L_fl12_%=
s_branch L_fo12_%=
L_out_end_%=:
L_fast_end_%=:
L_slow0_%=:
s_mov_b32 s98,0
s_branch L_slowc_%=
L_slow1_%=:
s_mov_b32 s98,1
L_slowc_%=:
s_nop 7
s_nop 7
s_waitcnt vmcnt(0) lgkmcnt(0)
s_lshl_b32 s96,%1,2
s_add_u32 s96,s96,s40
s_lshl_b32 s96,s96,15
s_add_u32 s96,s96,167772160
s_mov_b64 s[56:57],s[12:13]
s_add_u32 s56,s56,s96
s_addc_u32 s57,s57,0
s_and_b32 s57,s57,0xffff
s_mov_b32 s58,32768
s_mov_b32 s59,0x20000
s_mov_b32 s96,0
buffer_store_dwordx4 v[64:67],v6,s[56:59],s96 offen
buffer_store_dwordx4 v[68:71],v6,s[56:59],s96 offen offset:1024
buffer_store_dwordx4 v[72:75],v6,s[56:59],s96 offen offset:2048
buffer_store_dwordx4 v[76:79],v6,s[56:59],s96 offen offset:3072
s_movk_i32 s96,4096
buffer_store_dwordx4 v[80:83],v6,s[56:59],s96 offen
buffer_store_dwordx4 v[84:87],v6,s[56:59],s96 offen offset:1024
buffer_store_dwordx4 v[88:91],v6,s[56:59],s96 offen offset:2048
buffer_store_dwordx4 v[92:95],v6,s[56:59],s96 offen offset:3072
s_movk_i32 s96,8192
buffer_store_dwordx4 v[96:99],v6,s[56:59],s96 offen
buffer_store_dwordx4 v[100:103],v6,s[56:59],s96 offen offset:1024
buffer_store_dwordx4 v[104:107],v6,s[56:59],s96 offen offset:2048
buffer_store_dwordx4 v[108:111],v6,s[56:59],s96 offen offset:3072
s_movk_i32 s96,12288
buffer_store_dwordx4 v[112:115],v6,s[56:59],s96 offen
buffer_store_dwordx4 v[116:119],v6,s[56:59],s96 offen offset:1024
buffer_store_dwordx4 v[120:123],v6,s[56:59],s96 offen offset:2048
buffer_store_dwordx4 v[124:127],v6,s[56:59],s96 offen offset:3072
s_movk_i32 s96,16384
buffer_store_dwordx4 v[128:131],v6,s[56:59],s96 offen
buffer_store_dwordx4 v[132:135],v6,s[56:59],s96 offen offset:1024
buffer_store_dwordx4 v[136:139],v6,s[56:59],s96 offen offset:2048
buffer_store_dwordx4 v[140:143],v6,s[56:59],s96 offen offset:3072
s_movk_i32 s96,20480
buffer_store_dwordx4 v[144:147],v6,s[56:59],s96 offen
buffer_store_dwordx4 v[148:151],v6,s[56:59],s96 offen offset:1024
buffer_store_dwordx4 v[152:155],v6,s[56:59],s96 offen offset:2048
buffer_store_dwordx4 v[156:159],v6,s[56:59],s96 offen offset:3072
s_movk_i32 s96,24576
buffer_store_dwordx4 v[160:163],v6,s[56:59],s96 offen
buffer_store_dwordx4 v[164:167],v6,s[56:59],s96 offen offset:1024
buffer_store_dwordx4 v[168:171],v6,s[56:59],s96 offen offset:2048
buffer_store_dwordx4 v[172:175],v6,s[56:59],s96 offen offset:3072
s_movk_i32 s96,28672
buffer_store_dwordx4 v[176:179],v6,s[56:59],s96 offen
)A"
R"A(buffer_store_dwordx4 v[180:183],v6,s[56:59],s96 offen offset:1024
buffer_store_dwordx4 v[184:187],v6,s[56:59],s96 offen offset:2048
buffer_store_dwordx4 v[188:191],v6,s[56:59],s96 offen offset:3072
s_waitcnt vmcnt(0)
v_writelane_b32 v173,s88,0
v_writelane_b32 v173,s89,1
v_writelane_b32 v173,s90,2
v_writelane_b32 v173,s91,3
v_writelane_b32 v173,s92,4
v_writelane_b32 v173,s93,5
v_writelane_b32 v173,s94,6
v_writelane_b32 v173,s95,7
s_mov_b32 s88,s98
s_mul_i32 s90,s41,307200
s_mov_b64 s[92:93],s[4:5]
s_add_u32 s92,s92,s90
s_addc_u32 s93,s93,0
s_and_b32 s93,s93,0xffff
s_mov_b32 s94,307200
s_mov_b32 s95,0x20000
s_mov_b64 s[96:97],s[10:11]
s_and_b32 s97,s97,0xffff
s_mov_b32 s98,5242880
s_mov_b32 s99,0x20000
v_bfe_u32 v171,v6,4,4
v_lshrrev_b32 v172,8,v6
v_lshlrev_b32 v164,8,v171
v_lshl_add_u32 v164,v172,4,v164
v_lshlrev_b32 v165,10,v172
v_lshl_add_u32 v165,v171,1,v165
v_add_u32 v166,4096,v165
v_lshlrev_b32 v168,16,v172
v_lshrrev_b32 v170,2,v171
v_lshl_add_u32 v168,v170,4,v168
v_and_b32 v170,3,v171
v_lshl_add_u32 v168,v170,1,v168
s_lshl_b32 s90,s42,1
s_add_u32 s90,s90,s88
s_lshl_b32 s90,s90,8
v_add_u32 v168,s90,v168
s_mov_b32 s89,0
L_sjb_%=:
s_add_u32 s90,s89,s70
s_add_u32 s90,s90,1
s_and_b32 s90,s90,3
s_lshl_b32 s91,s40,2
s_add_u32 s90,s90,s91
s_lshl_b32 s90,s90,4
v_bfe_u32 v171,v6,4,4
v_lshrrev_b32 v172,8,v6
v_add_u32 v167,s90,v171
v_mul_u32_u24 v167,1280,v167
v_lshl_add_u32 v167,v172,4,v167
s_lshl_b32 s90,s88,5
s_add_u32 s90,s90,s89
s_and_b32 s91,s89,2
s_add_u32 s90,s90,s91
s_lshl_b32 s90,s90,12
v_add_u32 v169,s90,v6
v_mov_b32 v64,0
v_mov_b32 v65,0
v_mov_b32 v66,0
v_mov_b32 v67,0
v_mov_b32 v68,0
v_mov_b32 v69,0
v_mov_b32 v70,0
v_mov_b32 v71,0
v_mov_b32 v72,0
v_mov_b32 v73,0
v_mov_b32 v74,0
v_mov_b32 v75,0
v_mov_b32 v76,0
v_mov_b32 v77,0
v_mov_b32 v78,0
v_mov_b32 v79,0
v_mov_b32 v80,0
v_mov_b32 v81,0
v_mov_b32 v82,0
v_mov_b32 v83,0
v_mov_b32 v84,0
v_mov_b32 v85,0
v_mov_b32 v86,0
v_mov_b32 v87,0
v_mov_b32 v88,0
v_mov_b32 v89,0
v_mov_b32 v90,0
v_mov_b32 v91,0
v_mov_b32 v92,0
v_mov_b32 v93,0
v_mov_b32 v94,0
v_mov_b32 v95,0
s_mov_b32 s90,0
L_sq_%=:
s_lshl_b32 s91,s90,6
buffer_load_dwordx4 v[160:163],v167,s[92:95],s91 offen
s_lshl_b32 s91,s90,18
buffer_load_ushort v112,v168,s[96:99],s91 offen
buffer_load_ushort v120,v168,s[96:99],s91 offen offset:8
buffer_load_ushort v128,v168,s[96:99],s91 offen offset:64
buffer_load_ushort v136,v168,s[96:99],s91 offen offset:72
s_addk_i32 s91,8192
buffer_load_ushort v113,v168,s[96:99],s91 offen
buffer_load_ushort v121,v168,s[96:99],s91 offen offset:8
buffer_load_ushort v129,v168,s[96:99],s91 offen offset:64
buffer_load_ushort v137,v168,s[96:99],s91 offen offset:72
s_addk_i32 s91,8192
buffer_load_ushort v114,v168,s[96:99],s91 offen
buffer_load_ushort v122,v168,s[96:99],s91 offen offset:8
buffer_load_ushort v130,v168,s[96:99],s91 offen offset:64
buffer_load_ushort v138,v168,s[96:99],s91 offen offset:72
s_addk_i32 s91,8192
buffer_load_ushort v115,v168,s[96:99],s91 offen
buffer_load_ushort v123,v168,s[96:99],s91 offen offset:8
buffer_load_ushort v131,v168,s[96:99],s91 offen offset:64
buffer_load_ushort v139,v168,s[96:99],s91 offen offset:72
s_addk_i32 s91,8192
buffer_load_ushort v116,v168,s[96:99],s91 offen
buffer_load_ushort v124,v168,s[96:99],s91 offen offset:8
buffer_load_ushort v132,v168,s[96:99],s91 offen offset:64
buffer_load_ushort v140,v168,s[96:99],s91 offen offset:72
s_addk_i32 s91,8192
buffer_load_ushort v117,v168,s[96:99],s91 offen
buffer_load_ushort v125,v168,s[96:99],s91 offen offset:8
buffer_load_ushort v133,v168,s[96:99],s91 offen offset:64
buffer_load_ushort v141,v168,s[96:99],s91 offen offset:72
s_addk_i32 s91,8192
buffer_load_ushort v118,v168,s[96:99],s91 offen
buffer_load_ushort v126,v168,s[96:99],s91 offen offset:8
buffer_load_ushort v134,v168,s[96:99],s91 offen offset:64
buffer_load_ushort v142,v168,s[96:99],s91 offen offset:72
s_addk_i32 s91,8192
buffer_load_ushort v119,v168,s[96:99],s91 offen
buffer_load_ushort v127,v168,s[96:99],s91 offen offset:8
buffer_load_ushort v135,v168,s[96:99],s91 offen offset:64
buffer_load_ushort v143,v168,s[96:99],s91 offen offset:72
s_addk_i32 s91,8192
s_waitcnt vmcnt(0)
v_lshl_or_b32 v112,v113,16,v112
v_lshl_or_b32 v113,v115,16,v114
v_lshl_or_b32 v114,v117,16,v116
v_lshl_or_b32 v115,v119,16,v118
v_lshl_or_b32 v120,v121,16,v120
v_lshl_or_b32 v121,v123,16,v122
v_lshl_or_b32 v122,v125,16,v124
v_lshl_or_b32 v123,v127,16,v126
v_lshl_or_b32 v128,v129,16,v128
v_lshl_or_b32 v129,v131,16,v130
v_lshl_or_b32 v130,v133,16,v132
v_lshl_or_b32 v131,v135,16,v134
v_lshl_or_b32 v136,v137,16,v136
v_lshl_or_b32 v137,v139,16,v138
v_lshl_or_b32 v138,v141,16,v140
v_lshl_or_b32 v139,v143,16,v142
s_nop 1
v_mfma_f32_16x16x32_bf16 v[64:67],v[112:115],v[160:163],v[64:67]
v_mfma_f32_16x16x32_bf16 v[68:71],v[120:123],v[160:163],v[68:71]
v_mfma_f32_16x16x32_bf16 v[72:75],v[128:131],v[160:163],v[72:75]
v_mfma_f32_16x16x32_bf16 v[76:79],v[136:139],v[160:163],v[76:79]
s_nop 7
s_lshl_b32 s91,s90,18
buffer_load_ushort v112,v168,s[96:99],s91 offen offset:128
buffer_load_ushort v120,v168,s[96:99],s91 offen offset:136
buffer_load_ushort v128,v168,s[96:99],s91 offen offset:192
buffer_load_ushort v136,v168,s[96:99],s91 offen offset:200
s_addk_i32 s91,8192
buffer_load_ushort v113,v168,s[96:99],s91 offen offset:128
buffer_load_ushort v121,v168,s[96:99],s91 offen offset:136
buffer_load_ushort v129,v168,s[96:99],s91 offen offset:192
buffer_load_ushort v137,v168,s[96:99],s91 offen offset:200
s_addk_i32 s91,8192
buffer_load_ushort v114,v168,s[96:99],s91 offen offset:128
buffer_load_ushort v122,v168,s[96:99],s91 offen offset:136
buffer_load_ushort v130,v168,s[96:99],s91 offen offset:192
buffer_load_ushort v138,v168,s[96:99],s91 offen offset:200
s_addk_i32 s91,8192
buffer_load_ushort v115,v168,s[96:99],s91 offen offset:128
buffer_load_ushort v123,v168,s[96:99],s91 offen offset:136
buffer_load_ushort v131,v168,s[96:99],s91 offen offset:192
buffer_load_ushort v139,v168,s[96:99],s91 offen offset:200
s_addk_i32 s91,8192
buffer_load_ushort v116,v168,s[96:99],s91 offen offset:128
buffer_load_ushort v124,v168,s[96:99],s91 offen offset:136
buffer_load_ushort v132,v168,s[96:99],s91 offen offset:192
buffer_load_ushort v140,v168,s[96:99],s91 offen offset:200
s_addk_i32 s91,8192
buffer_load_ushort v117,v168,s[96:99],s91 offen offset:128
buffer_load_ushort v125,v168,s[96:99],s91 offen offset:136
buffer_load_ushort v133,v168,s[96:99],s91 offen offset:192
buffer_load_ushort v141,v168,s[96:99],s91 offen offset:200
s_addk_i32 s91,8192
buffer_load_ushort v118,v168,s[96:99],s91 offen offset:128
buffer_load_ushort v126,v168,s[96:99],s91 offen offset:136
buffer_load_ushort v134,v168,s[96:99],s91 offen offset:192
buffer_load_ushort v142,v168,s[96:99],s91 offen offset:200
s_addk_i32 s91,8192
buffer_load_ushort v119,v168,s[96:99],s91 offen offset:128
buffer_load_ushort v127,v168,s[96:99],s91 offen offset:136
buffer_load_ushort v135,v168,s[96:99],s91 offen offset:192
buffer_load_ushort v143,v168,s[96:99],s91 offen offset:200
s_addk_i32 s91,8192
s_waitcnt vmcnt(0)
v_lshl_or_b32 v112,v113,16,v112
v_lshl_or_b32 v113,v115,16,v114
v_lshl_or_b32 v114,v117,16,v116
v_lshl_or_b32 v115,v119,16,v118
v_lshl_or_b32 v120,v121,16,v120
v_lshl_or_b32 v121,v123,16,v122
v_lshl_or_b32 v122,v125,16,v124
v_lshl_or_b32 v123,v127,16,v126
v_lshl_or_b32 v128,v129,16,v128
v_lshl_or_b32 v129,v131,16,v130
v_lshl_or_b32 v130,v133,16,v132
v_lshl_or_b32 v131,v135,16,v134
v_lshl_or_b32 v136,v137,16,v136
v_lshl_or_b32 v137,v139,16,v138
v_lshl_or_b32 v138,v141,16,v140
v_lshl_or_b32 v139,v143,16,v142
s_nop 1
v_mfma_f32_16x16x32_bf16 v[80:83],v[112:115],v[160:163],v[80:83]
v_mfma_f32_16x16x32_bf16 v[84:87],v[120:123],v[160:163],v[84:87]
v_mfma_f32_16x16x32_bf16 v[88:91],v[128:131],v[160:163],v[88:91]
v_mfma_f32_16x16x32_bf16 v[92:95],v[136:139],v[160:163],v[92:95]
s_nop 7
s_add_u32 s90,s90,1
s_cmp_lg_u32 s90,20
s_cbranch_scc1 L_sq_%=
s_nop 7
v_mul_f32 v64,s61,v64
v_mul_f32 v65,s61,v65
v_mul_f32 v66,s61,v66
v_mul_f32 v67,s61,v67
v_mul_f32 v68,s61,v68
v_mul_f32 v69,s61,v69
v_mul_f32 v70,s61,v70
v_mul_f32 v71,s61,v71
v_mul_f32 v72,s61,v72
v_mul_f32 v73,s61,v73
v_mul_f32 v74,s61,v74
v_mul_f32 v75,s61,v75
v_mul_f32 v76,s61,v76
v_mul_f32 v77,s61,v77
v_mul_f32 v78,s61,v78
v_mul_f32 v79,s61,v79
v_mul_f32 v80,s61,v80
v_mul_f32 v81,s61,v81
v_mul_f32 v82,s61,v82
v_mul_f32 v83,s61,v83
v_mul_f32 v84,s61,v84
v_mul_f32 v85,s61,v85
v_mul_f32 v86,s61,v86
v_mul_f32 v87,s61,v87
v_mul_f32 v88,s61,v88
v_mul_f32 v89,s61,v89
v_mul_f32 v90,s61,v90
v_mul_f32 v91,s61,v91
v_mul_f32 v92,s61,v92
v_mul_f32 v93,s61,v93
v_mul_f32 v94,s61,v94
v_mul_f32 v95,s61,v95
.p2align 3
v_cvt_pk_bf16_f32 v96,v64,v65
v_cvt_pk_bf16_f32 v97,v66,v67
v_cvt_pk_bf16_f32 v98,v68,v69
v_cvt_pk_bf16_f32 v99,v70,v71
v_cvt_pk_bf16_f32 v100,v72,v73
v_cvt_pk_bf16_f32 v101,v74,v75
v_cvt_pk_bf16_f32 v102,v76,v77
v_cvt_pk_bf16_f32 v103,v78,v79
v_cvt_pk_bf16_f32 v104,v80,v81
v_cvt_pk_bf16_f32 v105,v82,v83
v_cvt_pk_bf16_f32 v106,v84,v85
v_cvt_pk_bf16_f32 v107,v86,v87
v_cvt_pk_bf16_f32 v108,v88,v89
v_cvt_pk_bf16_f32 v109,v90,v91
v_cvt_pk_bf16_f32 v110,v92,v93
v_cvt_pk_bf16_f32 v111,v94,v95
v_mov_b32 v170,0xff800000
s_lshl_b32 s91,s88,18
s_mov_b32 s90,64
L_s1_%=:
buffer_load_dwordx4 v[112:115],v164,s[20:23],s91 offen
buffer_load_dwordx4 v[116:119],v164,s[20:23],s91 offen offset:64
buffer_load_dwordx4 v[120:123],v164,s[20:23],s91 offen offset:128
buffer_load_dwordx4 v[124:127],v164,s[20:23],s91 offen offset:192
s_waitcnt vmcnt(0)
v_mfma_f32_16x16x32_bf16 v[144:147],v[112:115],v[96:99],0
v_mfma_f32_16x16x32_bf16 v[144:147],v[116:119],v[100:103],v[144:147]
v_mfma_f32_16x16x32_bf16 v[144:147],v[120:123],v[104:107],v[144:147]
v_mfma_f32_16x16x32_bf16 v[144:147],v[124:127],v[108:111],v[144:147]
s_nop 7
v_max3_f32 v170,v170,v144,v145
v_max3_f32 v170,v170,v146,v147
s_addk_i32 s91,4096
s_sub_u32 s90,s90,1
s_cmp_lg_u32 s90,0
s_cbranch_scc1 L_s1_%=
v_mov_b32 v171,v170
s_nop 1
v_permlane32_swap_b32 v170,v171
v_max_f32 v170,v170,v171
v_mov_b32 v171,v170
s_nop 1
v_permlane16_swap_b32 v170,v171
v_max_f32 v170,v170,v171
v_sub_f32 v152,0,v170
v_mov_b32 v156,0
v_sub_f32 v153,0,v170
v_mov_b32 v157,0
v_sub_f32 v154,0,v170
v_mov_b32 v158,0
v_sub_f32 v155,0,v170
v_mov_b32 v159,0
v_mov_b32 v64,0
v_mov_b32 v65,0
v_mov_b32 v66,0
v_mov_b32 v67,0
v_mov_b32 v68,0
v_mov_b32 v69,0
v_mov_b32 v70,0
v_mov_b32 v71,0
v_mov_b32 v72,0
v_mov_b32 v73,0
v_mov_b32 v74,0
v_mov_b32 v75,0
v_mov_b32 v76,0
v_mov_b32 v77,0
v_mov_b32 v78,0
v_mov_b32 v79,0
v_mov_b32 v80,0
v_mov_b32 v81,0
v_mov_b32 v82,0
v_mov_b32 v83,0
v_mov_b32 v84,0
v_mov_b32 v85,0
v_mov_b32 v86,0
v_mov_b32 v87,0
v_mov_b32 v88,0
v_mov_b32 v89,0
v_mov_b32 v90,0
v_mov_b32 v91,0
v_mov_b32 v92,0
v_mov_b32 v93,0
v_mov_b32 v94,0
v_mov_b32 v95,0
s_lshl_b32 s91,s88,18
s_mov_b32 s90,32
v_add_u32 v172,4096,v164
L_s2_%=:
buffer_load_dwordx4 v[112:115],v164,s[20:23],s91 offen
buffer_load_dwordx4 v[116:119],v164,s[20:23],s91 offen offset:64
buffer_load_dwordx4 v[120:123],v164,s[20:23],s91 offen offset:128
buffer_load_dwordx4 v[124:127],v164,s[20:23],s91 offen offset:192
buffer_load_dwordx4 v[128:131],v172,s[20:23],s91 offen
buffer_load_dwordx4 v[132:135],v172,s[20:23],s91 offen offset:64
buffer_load_dwordx4 v[136:139],v172,s[20:23],s91 offen offset:128
buffer_load_dwordx4 v[140:143],v172,s[20:23],s91 offen offset:192
s_waitcnt vmcnt(0)
v_mfma_f32_16x16x32_bf16 v[144:147],v[112:115],v[96:99],v[152:155]
v_mfma_f32_16x16x32_bf16 v[144:147],v[116:119],v[100:103],v[144:147]
v_mfma_f32_16x16x32_bf16 v[144:147],v[120:123],v[104:107],v[144:147]
v_mfma_f32_16x16x32_bf16 v[144:147],v[124:127],v[108:111],v[144:147]
v_mfma_f32_16x16x32_bf16 v[148:151],v[128:131],v[96:99],v[152:155]
v_mfma_f32_16x16x32_bf16 v[148:151],v[132:135],v[100:103],v[148:151]
v_mfma_f32_16x16x32_bf16 v[148:151],v[136:139],v[104:107],v[148:151]
v_mfma_f32_16x16x32_bf16 v[148:151],v[140:143],v[108:111],v[148:151]
s_nop 7
v_exp_f32 v144,v144
v_exp_f32 v145,v145
v_exp_f32 v146,v146
v_exp_f32 v147,v147
v_exp_f32 v148,v148
v_exp_f32 v149,v149
v_exp_f32 v150,v150
v_exp_f32 v151,v151
s_nop 0
.p2align 3
v_cvt_pk_bf16_f32 v144,v144,v145
)A"
R"A(v_cvt_pk_bf16_f32 v145,v146,v147
v_cvt_pk_bf16_f32 v146,v148,v149
v_cvt_pk_bf16_f32 v147,v150,v151
buffer_load_ushort v112,v165,s[24:27],s91 offen
buffer_load_ushort v113,v165,s[24:27],s91 offen offset:256
buffer_load_ushort v114,v165,s[24:27],s91 offen offset:512
buffer_load_ushort v115,v165,s[24:27],s91 offen offset:768
buffer_load_ushort v116,v166,s[24:27],s91 offen
buffer_load_ushort v117,v166,s[24:27],s91 offen offset:256
buffer_load_ushort v118,v166,s[24:27],s91 offen offset:512
buffer_load_ushort v119,v166,s[24:27],s91 offen offset:768
buffer_load_ushort v120,v165,s[24:27],s91 offen offset:32
buffer_load_ushort v121,v165,s[24:27],s91 offen offset:288
buffer_load_ushort v122,v165,s[24:27],s91 offen offset:544
buffer_load_ushort v123,v165,s[24:27],s91 offen offset:800
buffer_load_ushort v124,v166,s[24:27],s91 offen offset:32
buffer_load_ushort v125,v166,s[24:27],s91 offen offset:288
buffer_load_ushort v126,v166,s[24:27],s91 offen offset:544
buffer_load_ushort v127,v166,s[24:27],s91 offen offset:800
buffer_load_ushort v128,v165,s[24:27],s91 offen offset:64
buffer_load_ushort v129,v165,s[24:27],s91 offen offset:320
buffer_load_ushort v130,v165,s[24:27],s91 offen offset:576
buffer_load_ushort v131,v165,s[24:27],s91 offen offset:832
buffer_load_ushort v132,v166,s[24:27],s91 offen offset:64
buffer_load_ushort v133,v166,s[24:27],s91 offen offset:320
buffer_load_ushort v134,v166,s[24:27],s91 offen offset:576
buffer_load_ushort v135,v166,s[24:27],s91 offen offset:832
buffer_load_ushort v136,v165,s[24:27],s91 offen offset:96
buffer_load_ushort v137,v165,s[24:27],s91 offen offset:352
buffer_load_ushort v138,v165,s[24:27],s91 offen offset:608
buffer_load_ushort v139,v165,s[24:27],s91 offen offset:864
buffer_load_ushort v140,v166,s[24:27],s91 offen offset:96
buffer_load_ushort v141,v166,s[24:27],s91 offen offset:352
buffer_load_ushort v142,v166,s[24:27],s91 offen offset:608
buffer_load_ushort v143,v166,s[24:27],s91 offen offset:864
s_waitcnt vmcnt(0)
v_lshl_or_b32 v112,v113,16,v112
v_lshl_or_b32 v113,v115,16,v114
v_lshl_or_b32 v114,v117,16,v116
v_lshl_or_b32 v115,v119,16,v118
v_lshl_or_b32 v120,v121,16,v120
v_lshl_or_b32 v121,v123,16,v122
v_lshl_or_b32 v122,v125,16,v124
v_lshl_or_b32 v123,v127,16,v126
v_lshl_or_b32 v128,v129,16,v128
v_lshl_or_b32 v129,v131,16,v130
v_lshl_or_b32 v130,v133,16,v132
v_lshl_or_b32 v131,v135,16,v134
v_lshl_or_b32 v136,v137,16,v136
v_lshl_or_b32 v137,v139,16,v138
v_lshl_or_b32 v138,v141,16,v140
v_lshl_or_b32 v139,v143,16,v142
s_nop 1
v_mfma_f32_16x16x32_bf16 v[64:67],v[112:115],v[144:147],v[64:67]
v_mfma_f32_16x16x32_bf16 v[68:71],v[120:123],v[144:147],v[68:71]
v_mfma_f32_16x16x32_bf16 v[72:75],v[128:131],v[144:147],v[72:75]
v_mfma_f32_16x16x32_bf16 v[76:79],v[136:139],v[144:147],v[76:79]
s_nop 7
buffer_load_ushort v112,v165,s[24:27],s91 offen offset:128
buffer_load_ushort v113,v165,s[24:27],s91 offen offset:384
buffer_load_ushort v114,v165,s[24:27],s91 offen offset:640
buffer_load_ushort v115,v165,s[24:27],s91 offen offset:896
buffer_load_ushort v116,v166,s[24:27],s91 offen offset:128
buffer_load_ushort v117,v166,s[24:27],s91 offen offset:384
buffer_load_ushort v118,v166,s[24:27],s91 offen offset:640
buffer_load_ushort v119,v166,s[24:27],s91 offen offset:896
buffer_load_ushort v120,v165,s[24:27],s91 offen offset:160
buffer_load_ushort v121,v165,s[24:27],s91 offen offset:416
buffer_load_ushort v122,v165,s[24:27],s91 offen offset:672
buffer_load_ushort v123,v165,s[24:27],s91 offen offset:928
buffer_load_ushort v124,v166,s[24:27],s91 offen offset:160
buffer_load_ushort v125,v166,s[24:27],s91 offen offset:416
buffer_load_ushort v126,v166,s[24:27],s91 offen offset:672
buffer_load_ushort v127,v166,s[24:27],s91 offen offset:928
buffer_load_ushort v128,v165,s[24:27],s91 offen offset:192
buffer_load_ushort v129,v165,s[24:27],s91 offen offset:448
buffer_load_ushort v130,v165,s[24:27],s91 offen offset:704
buffer_load_ushort v131,v165,s[24:27],s91 offen offset:960
buffer_load_ushort v132,v166,s[24:27],s91 offen offset:192
buffer_load_ushort v133,v166,s[24:27],s91 offen offset:448
buffer_load_ushort v134,v166,s[24:27],s91 offen offset:704
buffer_load_ushort v135,v166,s[24:27],s91 offen offset:960
buffer_load_ushort v136,v165,s[24:27],s91 offen offset:224
buffer_load_ushort v137,v165,s[24:27],s91 offen offset:480
buffer_load_ushort v138,v165,s[24:27],s91 offen offset:736
buffer_load_ushort v139,v165,s[24:27],s91 offen offset:992
buffer_load_ushort v140,v166,s[24:27],s91 offen offset:224
buffer_load_ushort v141,v166,s[24:27],s91 offen offset:480
buffer_load_ushort v142,v166,s[24:27],s91 offen offset:736
buffer_load_ushort v143,v166,s[24:27],s91 offen offset:992
s_waitcnt vmcnt(0)
v_lshl_or_b32 v112,v113,16,v112
v_lshl_or_b32 v113,v115,16,v114
v_lshl_or_b32 v114,v117,16,v116
v_lshl_or_b32 v115,v119,16,v118
v_lshl_or_b32 v120,v121,16,v120
v_lshl_or_b32 v121,v123,16,v122
v_lshl_or_b32 v122,v125,16,v124
v_lshl_or_b32 v123,v127,16,v126
v_lshl_or_b32 v128,v129,16,v128
v_lshl_or_b32 v129,v131,16,v130
v_lshl_or_b32 v130,v133,16,v132
v_lshl_or_b32 v131,v135,16,v134
v_lshl_or_b32 v136,v137,16,v136
v_lshl_or_b32 v137,v139,16,v138
v_lshl_or_b32 v138,v141,16,v140
v_lshl_or_b32 v139,v143,16,v142
s_nop 1
v_mfma_f32_16x16x32_bf16 v[80:83],v[112:115],v[144:147],v[80:83]
v_mfma_f32_16x16x32_bf16 v[84:87],v[120:123],v[144:147],v[84:87]
v_mfma_f32_16x16x32_bf16 v[88:91],v[128:131],v[144:147],v[88:91]
v_mfma_f32_16x16x32_bf16 v[92:95],v[136:139],v[144:147],v[92:95]
s_nop 7
v_mfma_f32_16x16x32_bf16 v[156:159],v[28:31],v[144:147],v[156:159]
s_nop 7
s_addk_i32 s91,8192
s_sub_u32 s90,s90,1
s_cmp_lg_u32 s90,0
s_cbranch_scc1 L_s2_%=
v_rcp_f32 v170,v156
s_nop 0
v_mul_f32 v64,v170,v64
v_mul_f32 v65,v170,v65
v_mul_f32 v66,v170,v66
v_mul_f32 v67,v170,v67
v_mul_f32 v68,v170,v68
v_mul_f32 v69,v170,v69
v_mul_f32 v70,v170,v70
v_mul_f32 v71,v170,v71
v_mul_f32 v72,v170,v72
v_mul_f32 v73,v170,v73
v_mul_f32 v74,v170,v74
v_mul_f32 v75,v170,v75
v_mul_f32 v76,v170,v76
v_mul_f32 v77,v170,v77
v_mul_f32 v78,v170,v78
v_mul_f32 v79,v170,v79
v_mul_f32 v80,v170,v80
v_mul_f32 v81,v170,v81
v_mul_f32 v82,v170,v82
v_mul_f32 v83,v170,v83
v_mul_f32 v84,v170,v84
v_mul_f32 v85,v170,v85
v_mul_f32 v86,v170,v86
v_mul_f32 v87,v170,v87
v_mul_f32 v88,v170,v88
v_mul_f32 v89,v170,v89
v_mul_f32 v90,v170,v90
v_mul_f32 v91,v170,v91
v_mul_f32 v92,v170,v92
v_mul_f32 v93,v170,v93
v_mul_f32 v94,v170,v94
v_mul_f32 v95,v170,v95
.p2align 3
v_cvt_pk_bf16_f32 v96,v64,v65
v_cvt_pk_bf16_f32 v97,v66,v67
v_cvt_pk_bf16_f32 v98,v68,v69
v_cvt_pk_bf16_f32 v99,v70,v71
v_cvt_pk_bf16_f32 v100,v72,v73
v_cvt_pk_bf16_f32 v101,v74,v75
v_cvt_pk_bf16_f32 v102,v76,v77
v_cvt_pk_bf16_f32 v103,v78,v79
v_cvt_pk_bf16_f32 v104,v80,v81
v_cvt_pk_bf16_f32 v105,v82,v83
v_cvt_pk_bf16_f32 v106,v84,v85
v_cvt_pk_bf16_f32 v107,v86,v87
v_cvt_pk_bf16_f32 v108,v88,v89
v_cvt_pk_bf16_f32 v109,v90,v91
v_cvt_pk_bf16_f32 v110,v92,v93
v_cvt_pk_bf16_f32 v111,v94,v95
buffer_store_dwordx4 v[96:99],v169,s[36:39],s55 offen sc1
buffer_store_dwordx4 v[100:103],v169,s[36:39],s55 offen offset:1024 sc1
buffer_store_dwordx4 v[104:107],v169,s[36:39],s55 offen offset:2048 sc1
buffer_store_dwordx4 v[108:111],v169,s[36:39],s55 offen offset:3072 sc1
s_waitcnt vmcnt(0)
s_add_u32 s89,s89,1
s_cmp_lg_u32 s89,4
s_cbranch_scc1 L_sjb_%=
s_mov_b32 s98,s88
v_readlane_b32 s88,v173,0
v_readlane_b32 s89,v173,1
v_readlane_b32 s90,v173,2
v_readlane_b32 s91,v173,3
v_readlane_b32 s92,v173,4
v_readlane_b32 s93,v173,5
v_readlane_b32 s94,v173,6
v_readlane_b32 s95,v173,7
s_nop 4
s_mov_b32 s96,0
buffer_load_dwordx4 v[64:67],v6,s[56:59],s96 offen sc1
buffer_load_dwordx4 v[68:71],v6,s[56:59],s96 offen offset:1024 sc1
buffer_load_dwordx4 v[72:75],v6,s[56:59],s96 offen offset:2048 sc1
buffer_load_dwordx4 v[76:79],v6,s[56:59],s96 offen offset:3072 sc1
s_movk_i32 s96,4096
buffer_load_dwordx4 v[80:83],v6,s[56:59],s96 offen sc1
buffer_load_dwordx4 v[84:87],v6,s[56:59],s96 offen offset:1024 sc1
buffer_load_dwordx4 v[88:91],v6,s[56:59],s96 offen offset:2048 sc1
buffer_load_dwordx4 v[92:95],v6,s[56:59],s96 offen offset:3072 sc1
s_movk_i32 s96,8192
buffer_load_dwordx4 v[96:99],v6,s[56:59],s96 offen sc1
buffer_load_dwordx4 v[100:103],v6,s[56:59],s96 offen offset:1024 sc1
buffer_load_dwordx4 v[104:107],v6,s[56:59],s96 offen offset:2048 sc1
buffer_load_dwordx4 v[108:111],v6,s[56:59],s96 offen offset:3072 sc1
s_movk_i32 s96,12288
buffer_load_dwordx4 v[112:115],v6,s[56:59],s96 offen sc1
buffer_load_dwordx4 v[116:119],v6,s[56:59],s96 offen offset:1024 sc1
buffer_load_dwordx4 v[120:123],v6,s[56:59],s96 offen offset:2048 sc1
buffer_load_dwordx4 v[124:127],v6,s[56:59],s96 offen offset:3072 sc1
s_movk_i32 s96,16384
buffer_load_dwordx4 v[128:131],v6,s[56:59],s96 offen sc1
buffer_load_dwordx4 v[132:135],v6,s[56:59],s96 offen offset:1024 sc1
buffer_load_dwordx4 v[136:139],v6,s[56:59],s96 offen offset:2048 sc1
buffer_load_dwordx4 v[140:143],v6,s[56:59],s96 offen offset:3072 sc1
s_movk_i32 s96,20480
buffer_load_dwordx4 v[144:147],v6,s[56:59],s96 offen sc1
buffer_load_dwordx4 v[148:151],v6,s[56:59],s96 offen offset:1024 sc1
buffer_load_dwordx4 v[152:155],v6,s[56:59],s96 offen offset:2048 sc1
buffer_load_dwordx4 v[156:159],v6,s[56:59],s96 offen offset:3072 sc1
s_movk_i32 s96,24576
buffer_load_dwordx4 v[160:163],v6,s[56:59],s96 offen sc1
buffer_load_dwordx4 v[164:167],v6,s[56:59],s96 offen offset:1024 sc1
buffer_load_dwordx4 v[168:171],v6,s[56:59],s96 offen offset:2048 sc1
buffer_load_dwordx4 v[172:175],v6,s[56:59],s96 offen offset:3072 sc1
s_movk_i32 s96,28672
buffer_load_dwordx4 v[176:179],v6,s[56:59],s96 offen sc1
buffer_load_dwordx4 v[180:183],v6,s[56:59],s96 offen offset:1024 sc1
buffer_load_dwordx4 v[184:187],v6,s[56:59],s96 offen offset:2048 sc1
buffer_load_dwordx4 v[188:191],v6,s[56:59],s96 offen offset:3072 sc1
s_waitcnt vmcnt(0)
s_cmp_lg_u32 s98,0
s_cbranch_scc1 L_sr1_%=
buffer_load_dwordx4 v[64:67],v6,s[36:39],s84 offen sc1
buffer_load_dwordx4 v[68:71],v6,s[36:39],s84 offen offset:1024 sc1
buffer_load_dwordx4 v[72:75],v6,s[36:39],s84 offen offset:2048 sc1
buffer_load_dwordx4 v[76:79],v6,s[36:39],s84 offen offset:3072 sc1
buffer_load_dwordx4 v[80:83],v6,s[36:39],s85 offen sc1
buffer_load_dwordx4 v[84:87],v6,s[36:39],s85 offen offset:1024 sc1
buffer_load_dwordx4 v[88:91],v6,s[36:39],s85 offen offset:2048 sc1
buffer_load_dwordx4 v[92:95],v6,s[36:39],s85 offen offset:3072 sc1
buffer_load_dwordx4 v[96:99],v6,s[36:39],s86 offen sc1
buffer_load_dwordx4 v[100:103],v6,s[36:39],s86 offen offset:1024 sc1
buffer_load_dwordx4 v[104:107],v6,s[36:39],s86 offen offset:2048 sc1
buffer_load_dwordx4 v[108:111],v6,s[36:39],s86 offen offset:3072 sc1
buffer_load_dwordx4 v[112:115],v6,s[36:39],s87 offen sc1
buffer_load_dwordx4 v[116:119],v6,s[36:39],s87 offen offset:1024 sc1
buffer_load_dwordx4 v[120:123],v6,s[36:39],s87 offen offset:2048 sc1
buffer_load_dwordx4 v[124:127],v6,s[36:39],s87 offen offset:3072 sc1
s_waitcnt vmcnt(0)
s_nop 7
s_branch L_sret0_%=
L_sr1_%=:
buffer_load_dwordx4 v[136:139],v6,s[36:39],s84 offen sc1
buffer_load_dwordx4 v[140:143],v6,s[36:39],s84 offen offset:1024 sc1
buffer_load_dwordx4 v[152:155],v6,s[36:39],s84 offen offset:2048 sc1
buffer_load_dwordx4 v[156:159],v6,s[36:39],s84 offen offset:3072 sc1
buffer_load_dwordx4 v[168:171],v6,s[36:39],s85 offen sc1
buffer_load_dwordx4 v[172:175],v6,s[36:39],s85 offen offset:1024 sc1
buffer_load_dwordx4 v[184:187],v6,s[36:39],s85 offen offset:2048 sc1
buffer_load_dwordx4 v[188:191],v6,s[36:39],s85 offen offset:3072 sc1
buffer_load_dwordx4 v[200:203],v6,s[36:39],s86 offen sc1
buffer_load_dwordx4 v[204:207],v6,s[36:39],s86 offen offset:1024 sc1
buffer_load_dwordx4 v[216:219],v6,s[36:39],s86 offen offset:2048 sc1
buffer_load_dwordx4 v[220:223],v6,s[36:39],s86 offen offset:3072 sc1
buffer_load_dwordx4 v[232:235],v6,s[36:39],s87 offen sc1
buffer_load_dwordx4 v[236:239],v6,s[36:39],s87 offen offset:1024 sc1
buffer_load_dwordx4 v[248:251],v6,s[36:39],s87 offen offset:2048 sc1
buffer_load_dwordx4 v[252:255],v6,s[36:39],s87 offen offset:3072 sc1
s_waitcnt vmcnt(0)
s_nop 7
s_branch L_sret1_%=
L_end_%=:
)A"
    :: "s"(karg), "s"(blk), "s"(wid)
    : "v0", "v1", "v2", "v3", "v4", "v5", "v6", "v7", "v8", "v9", "v10", "v11", "v12", "v13", "v14", "v15", "v16", "v17", "v18", "v19", "v20", "v21", "v22", "v23", "v24", "v25", "v26", "v27", "v28", "v29", "v30", "v31", "v32", "v33", "v34", "v35", "v36", "v37", "v38", "v39", "v40", "v41", "v42", "v43", "v44", "v45", "v46", "v47", "v48", "v49", "v50", "v51", "v52", "v53", "v54", "v55", "v56", "v57", "v58", "v59", "v60", "v61", "v62", "v63", "v64", "v65", "v66", "v67", "v68", "v69", "v70", "v71", "v72", "v73", "v74", "v75", "v76", "v77", "v78", "v79", "v80", "v81", "v82", "v83", "v84", "v85", "v86", "v87", "v88", "v89", "v90", "v91", "v92", "v93", "v94", "v95", "v96", "v97", "v98", "v99", "v100", "v101", "v102", "v103", "v104", "v105", "v106", "v107", "v108", "v109", "v110", "v111", "v112", "v113", "v114", "v115", "v116", "v117", "v118", "v119", "v120", "v121", "v122", "v123", "v124", "v125", "v126", "v127", "v128", "v129", "v130", "v131", "v132", "v133", "v134", "v135", "v136", "v137", "v138", "v139", "v140", "v141", "v142", "v143", "v144", "v145", "v146", "v147", "v148", "v149", "v150", "v151", "v152", "v153", "v154", "v155", "v156", "v157", "v158", "v159", "v160", "v161", "v162", "v163", "v164", "v165", "v166", "v167", "v168", "v169", "v170", "v171", "v172", "v173", "v174", "v175", "v176", "v177", "v178", "v179", "v180", "v181", "v182", "v183", "v184", "v185", "v186", "v187", "v188", "v189", "v190", "v191", "v192", "v193", "v194", "v195", "v196", "v197", "v198", "v199", "v200", "v201", "v202", "v203", "v204", "v205", "v206", "v207", "v208", "v209", "v210", "v211", "v212", "v213", "v214", "v215", "v216", "v217", "v218", "v219", "v220", "v221", "v222", "v223", "v224", "v225", "v226", "v227", "v228", "v229", "v230", "v231", "v232", "v233", "v234", "v235", "v236", "v237", "v238", "v239", "v240", "v241", "v242", "v243", "v244", "v245", "v246", "v247", "v248", "v249", "v250", "v251", "v252", "v253", "v254", "v255", "a0", "a1", "a2", "a3", "a4", "a5", "a6", "a7", "a8", "a9", "a10", "a11", "a12", "a13", "a14", "a15", "a16", "a17", "a18", "a19", "a20", "a21", "a22", "a23", "a24", "a25", "a26", "a27", "a28", "a29", "a30", "a31", "a32", "a33", "a34", "a35", "a36", "a37", "a38", "a39", "a40", "a41", "a42", "a43", "a44", "a45", "a46", "a47", "a48", "a49", "a50", "a51", "a52", "a53", "a54", "a55", "a56", "a57", "a58", "a59", "a60", "a61", "a62", "a63", "a64", "a65", "a66", "a67", "a68", "a69", "a70", "a71", "a72", "a73", "a74", "a75", "a76", "a77", "a78", "a79", "a80", "a81", "a82", "a83", "a84", "a85", "a86", "a87", "a88", "a89", "a90", "a91", "a92", "a93", "a94", "a95", "a96", "a97", "a98", "a99", "a100", "a101", "a102", "a103", "a104", "a105", "a106", "a107", "a108", "a109", "a110", "a111", "a112", "a113", "a114", "a115", "a116", "a117", "a118", "a119", "a120", "a121", "a122", "a123", "a124", "a125", "a126", "a127", "a128", "a129", "a130", "a131", "a132", "a133", "a134", "a135", "a136", "a137", "a138", "a139", "a140", "a141", "a142", "a143", "a144", "a145", "a146", "a147", "a148", "a149", "a150", "a151", "a152", "a153", "a154", "a155", "a156", "a157", "a158", "a159", "a160", "a161", "a162", "a163", "a164", "a165", "a166", "a167", "a168", "a169", "a170", "a171", "a172", "a173", "a174", "a175", "a176", "a177", "a178", "a179", "a180", "a181", "a182", "a183", "a184", "a185", "a186", "a187", "a188", "a189", "a190", "a191", "a192", "a193", "a194", "a195", "a196", "a197", "a198", "a199", "a200", "a201", "a202", "a203", "a204", "a205", "a206", "a207", "a208", "a209", "a210", "a211", "a212", "a213", "a214", "a215", "a216", "a217", "a218", "a219", "a220", "a221", "a222", "a223", "a224", "a225", "a226", "a227", "a228", "a229", "a230", "a231", "a232", "a233", "a234", "a235", "a236", "a237", "a238", "a239", "a240", "a241", "a242", "a243", "a244", "a245", "a246", "a247", "a248", "a249", "a250", "a251", "a252", "a253", "a254", "a255", "s4", "s5", "s6", "s7", "s8", "s9", "s10", "s11", "s12", "s13", "s14", "s15", "s16", "s17", "s18", "s19", "s20", "s21", "s22", "s23", "s24", "s25", "s26", "s27", "s28", "s29", "s30", "s31", "s32", "s33", "s34", "s35", "s36", "s37", "s38", "s39", "s40", "s41", "s42", "s43", "s44", "s45", "s46", "s47", "s48", "s49", "s50", "s51", "s52", "s53", "s54", "s55", "s56", "s57", "s58", "s59", "s60", "s61", "s62", "s63", "s64", "s65", "s66", "s67", "s68", "s69", "s70", "s71", "s72", "s73", "s74", "s75", "s76", "s77", "s78", "s79", "s80", "s81", "s82", "s83", "s84", "s85", "s86", "s87", "s88", "s89", "s90", "s91", "s92", "s93", "s94", "s95", "s96", "s97", "s98", "s99", "s100", "s101", "vcc", "scc", "memory");
#endif
}

extern "C" void kernel_launch(void* const* d_in, const int* in_sizes, int n_in,
                              void* d_out, int out_size, void* d_ws, size_t ws_size,
                              hipStream_t stream) {
  (void)hipFuncSetAttribute((const void*)qattn_kernel, hipFuncAttributeMaxDynamicSharedMemorySize, LDS_ATT);
  qattn_kernel<<<256, 256, LDS_ATT, stream>>>(d_in[0], d_in[1], d_in[2], d_in[3], d_ws, d_in[4], d_out);
}
```
